```python
import torch

def _softmax(x, axis=-1):
    return torch.softmax(x, dim=axis)
def _gelu(x):
    return torch.nn.functional.gelu(x, approximate="tanh")
def _silu(x):
    return torch.nn.functional.silu(x)
def _sigmoid(x):
    return torch.sigmoid(x)
def _layernorm(x, axis=-1, eps=1e-5):
    m = x.mean(dim=axis, keepdim=True)
    v = x.var(dim=axis, keepdim=True, unbiased=False)
    return (x - m) / torch.sqrt(v + eps)
def _scatter_add(tgt, idx, src, axis=0):
    return tgt.clone().index_add_(axis, idx, src)
def _segment_sum(src, seg, n):
    out = torch.zeros((n,) + src.shape[1:], dtype=src.dtype)
    return out.index_add_(0, seg, src)
def _pad(x, pw):
    flat = tuple(v for pair in reversed(pw) for v in pair)
    return torch.nn.functional.pad(x, flat)

kernel_name = 'standalone__winograd__vision'

def setup_inputs():

    _w = (lambda a: (lambda t: t.to(torch.bfloat16) if t.is_floating_point() else t)(torch.as_tensor(a)))
    H = W = 512 // 2 * 2
    C = 192
    return {
        "x": _w((lambda *s: torch.randn(*s, dtype=torch.bfloat16) * 0.05)(1, H, W, C)),
        "w": _w((lambda *s: torch.randn(*s, dtype=torch.bfloat16) * 0.05)(3, 3, C, C)),
    }

def reference(x, w):
    N, H, W, C = x.shape
    K = w.shape[-1]
    G  = torch.as_tensor([[1.0,0.0,0.0],[0.5,0.5,0.5],[0.5,-0.5,0.5],[0.0,0.0,1.0]], dtype=x.dtype)
    Bt = torch.as_tensor([[1.0,0.0,-1.0,0.0],[0.0,1.0,1.0,0.0],[0.0,-1.0,1.0,0.0],[0.0,1.0,0.0,-1.0]], dtype=x.dtype)
    At = torch.as_tensor([[1.0,1.0,1.0,0.0],[0.0,1.0,-1.0,-1.0]], dtype=x.dtype)
    U = torch.einsum("ar,rsck,bs->abck", G, w, G)
    xp = _pad(x, ((0,0),(1,1),(1,1),(0,0)))
    nH, nW = H // 2, W // 2
    patches = []
    for i in range(4):
        for j in range(4):
            patches.append(xp[:, i:i + 2 * nH:2, j:j + 2 * nW:2, :])
    d = torch.stack(patches).reshape(4, 4, N * nH * nW, C)
    V = torch.einsum("ai,ijpc,bj->abpc", Bt, d, Bt)
    M = torch.einsum("abck,abpc->abpk", U, V)
    Y = torch.einsum("ma,abpk,nb->pmnk", At, M, At)
    Y = Y.reshape(N, nH, nW, 2, 2, K)
    Y = (lambda x, *d: x.permute(*d))(Y, 0, 1, 3, 2, 4, 5).reshape(N, H, W, K)
    return Y
```

```cpp
#include <hip/hip_runtime.h>
#include <hip/hip_bf16.h>
#include <stdint.h>

#define IMG_W 512
#define CH 192
#define PXB 384
#define ROWB (IMG_W * PXB)
#define U_BYTES 1327104

__device__ const __attribute__((aligned(1024))) unsigned lane_tab[5888] = {
  0xa800,0xa820,0x0,0x0,0xa808,0xa828,0x40,0x0,0xa810,0xa830,0x90,0x0,0xa818,0xa838,0xd0,0x0,
  0xa920,0xa900,0x2400,0x0,0xa928,0xa908,0x2440,0x0,0xa930,0xa910,0x2490,0x0,0xa938,0xa918,0x24d0,0x0,
  0xa840,0xa860,0x200,0x0,0xa848,0xa868,0x240,0x0,0xa850,0xa870,0x290,0x0,0xa858,0xa878,0x2d0,0x0,
  0xa960,0xa940,0x200,0x0,0xa968,0xa948,0x240,0x0,0xa970,0xa950,0x290,0x0,0xa978,0xa958,0x2d0,0x0,
  0xea80,0xeaa0,0x10,0x0,0xea88,0xeaa8,0x50,0x0,0xea90,0xeab0,0x80,0x0,0xea98,0xeab8,0xc0,0x0,
  0xa9a0,0xa980,0x2410,0x0,0xa9a8,0xa988,0x2450,0x0,0xa9b0,0xa990,0x2480,0x0,0xa9b8,0xa998,0x24c0,0x0,
  0xc0c0,0xc0e0,0x210,0x0,0xc0c8,0xc0e8,0x250,0x0,0xc0d0,0xc0f0,0x280,0x0,0xc0d8,0xc0f8,0x2c0,0x0,
  0xc1e0,0xc1c0,0x210,0x0,0xc1e8,0xc1c8,0x250,0x0,0xc1f0,0xc1d0,0x280,0x0,0xc1f8,0xc1d8,0x2c0,0x0,
  0xae00,0xae20,0x20,0x0,0xae08,0xae28,0x60,0x0,0xae10,0xae30,0xb0,0x0,0xae18,0xae38,0xf0,0x0,
  0xeb20,0xeb00,0x2420,0x0,0xeb28,0xeb08,0x2460,0x0,0xeb30,0xeb10,0x24b0,0x0,0xeb38,0xeb18,0x24f0,0x0,
  0xc040,0xc060,0x220,0x0,0xc048,0xc068,0x260,0x0,0xc050,0xc070,0x2b0,0x0,0xc058,0xc078,0x2f0,0x0,
  0xc160,0xc140,0x220,0x0,0xc168,0xc148,0x260,0x0,0xc170,0xc150,0x2b0,0x0,0xc178,0xc158,0x2f0,0x0,
  0xa880,0xa8a0,0x30,0x0,0xa888,0xa8a8,0x70,0x0,0xa890,0xa8b0,0xa0,0x0,0xa898,0xa8b8,0xe0,0x0,
  0xa8c0,0xa8e0,0x2430,0x0,0xa8c8,0xa8e8,0x2470,0x0,0xa8d0,0xa8f0,0x24a0,0x0,0xa8d8,0xa8f8,0x24e0,0x0,
  0xa9e0,0xa9c0,0x230,0x0,0xa9e8,0xa9c8,0x270,0x0,0xa9f0,0xa9d0,0x2a0,0x0,0xa9f8,0xa9d8,0x2e0,0x0,
  0xae00,0xae20,0x230,0x0,0xae08,0xae28,0x270,0x0,0xae10,0xae30,0x2a0,0x0,0xae18,0xae38,0x2e0,0x0,
  0x0,0xc042,0x0,0x0,0x0,0xc0420000,0x0,0x0,0x0,0x0,0x0,0xc042,0x0,0x0,0x0,0xc0420000,
  0x3f903f90,0x0,0x0,0x0,0x3f900000,0x0,0x3f90,0x0,0x0,0x0,0x3f903f90,0x0,0x0,0x0,0x3f900000,0x0,
  0x0,0x0,0x0,0x0,0x0,0x0,0x0,0x0,0x0,0x0,0x0,0x0,0x0,0x0,0x0,0x0,
  0x0,0x0,0x0,0x0,0x0,0x0,0x0,0x0,0x0,0x0,0x0,0x0,0x0,0x0,0x0,0x0,
  0x0,0x0,0x3f90,0x0,0x0,0x0,0x0,0x0,0x0,0x0,0x0,0x0,0x0,0x0,0x0,0x0,
  0x0,0x0,0x0,0x0,0xc0420000,0x0,0x0,0x0,0x0,0x0,0x0,0x0,0x0,0x0,0xc0420000,0x0,
  0x0,0x3f90,0x0,0x0,0x0,0x3f903f90,0x0,0x0,0x0,0x3f900000,0x0,0x3f90,0x0,0x0,0x0,0x3f903f90,
  0x0,0x0,0x0,0x0,0x0,0x0,0x0,0x0,0x0,0x0,0x0,0x0,0x0,0x0,0x0,0x0,
  0x0,0x0,0x0,0x0,0x0,0x0,0x0,0x0,0x0,0x0,0x0,0x0,0x0,0x0,0x0,0x0,
  0x0,0x0,0x0,0x0,0x0,0x0,0x0,0x0,0x0,0x0,0x0,0x0,0x0,0x0,0x3f90,0x0,
  0x0,0xc042,0x3f900000,0x0,0x0,0xc0420000,0x0,0x0,0x0,0x0,0x0,0xc042,0x0,0x0,0x0,0xc0420000,
  0x0,0x0,0x0,0x0,0x0,0x0,0x0,0x0,0x0,0x0,0x0,0x0,0x0,0x0,0x0,0x0,
  0x3f900000,0x0,0x0,0x0,0x3f900000,0x3f90,0x0,0x0,0x0,0x3f90,0x3f900000,0x0,0x0,0x0,0x3f900000,0x3f90,
  0xc042,0x0,0x0,0x0,0x0,0x0,0x0,0x0,0x0,0x0,0xc042,0x0,0x0,0x0,0x0,0x0,
  0x0,0x0,0x0,0x0,0x0,0x0,0x0,0x0,0x0,0x0,0x0,0x0,0x0,0x0,0x0,0x0,
  0x0,0x0,0x0,0x0,0x0,0x0,0x0,0x0,0x0,0x0,0x0,0x0,0x0,0x0,0x0,0x0,
  0x0,0x0,0xa800,0x0,0x40,0x40,0xa808,0x10,0x90,0x90,0xa810,0x20,0xd0,0xd0,0xa818,0x30,
  0x120,0x120,0xa840,0x180,0x160,0x160,0xa848,0x190,0x1b0,0x1b0,0xa850,0x1a0,0x1f0,0x1f0,0xa858,0x1b0,
  0x1000,0x2408,0xa880,0x300,0x1040,0x2448,0xa888,0x310,0x1090,0x2498,0xa890,0x320,0x10d0,0x24d8,0xa898,0x330,
  0x1120,0x2528,0xa8c0,0x480,0x1160,0x2568,0xa8c8,0x490,0x11b0,0x25b8,0xa8d0,0x4a0,0x11f0,0x25f8,0xa8d8,0x4b0,
  0x10,0x10,0xa920,0x620,0x50,0x50,0xa928,0x630,0x80,0x80,0xa930,0x600,0xc0,0xc0,0xa938,0x610,
  0x130,0x130,0xa960,0x7a0,0x170,0x170,0xa968,0x7b0,0x1a0,0x1a0,0xa970,0x780,0x1e0,0x1e0,0xa978,0x790,
  0x1010,0x2418,0xa9a0,0x920,0x1050,0x2458,0xa9a8,0x930,0x1080,0x2488,0xa9b0,0x900,0x10c0,0x24c8,0xa9b8,0x910,
  0x1130,0x2538,0xa9e0,0xaa0,0x1170,0x2578,0xa9e8,0xab0,0x11a0,0x25a8,0xa9f0,0xa80,0x11e0,0x25e8,0xa9f8,0xa90,
  0x20,0x20,0xae00,0x40,0x60,0x60,0xae08,0x50,0xb0,0xb0,0xae10,0x60,0xf0,0xf0,0xae18,0x70,
  0x100,0x100,0xae40,0x1c0,0x140,0x140,0xae48,0x1d0,0x190,0x190,0xae50,0x1e0,0x1d0,0x1d0,0xae58,0x1f0,
  0x1020,0x2428,0xae80,0x340,0x1060,0x2468,0xae88,0x350,0x10b0,0x24b8,0xae90,0x360,0x10f0,0x24f8,0xae98,0x370,
  0x1100,0x2508,0xaec0,0x4c0,0x1140,0x2548,0xaec8,0x4d0,0x1190,0x2598,0xaed0,0x4e0,0x11d0,0x25d8,0xaed8,0x4f0,
  0x30,0x30,0xaf20,0x660,0x70,0x70,0xaf28,0x670,0xa0,0xa0,0xaf30,0x640,0xe0,0xe0,0xaf38,0x650,
  0x110,0x110,0xaf60,0x7e0,0x150,0x150,0xaf68,0x7f0,0x180,0x180,0xaf70,0x7c0,0x1c0,0x1c0,0xaf78,0x7d0,
  0x1030,0x2438,0xafa0,0x960,0x1070,0x2478,0xafa8,0x970,0x10a0,0x24a8,0xafb0,0x940,0x10e0,0x24e8,0xafb8,0x950,
  0x1110,0x2518,0xafe0,0xae0,0x1150,0x2558,0xafe8,0xaf0,0x1180,0x2588,0xaff0,0xac0,0x11c0,0x25c8,0xaff8,0xad0,
  0xb400,0xb420,0x120,0x0,0xb408,0xb428,0x160,0x0,0xb410,0xb430,0x1b0,0x0,0xb418,0xb438,0x1f0,0x0,
  0xb440,0xb460,0x2520,0x0,0xb448,0xb468,0x2560,0x0,0xb450,0xb470,0x25b0,0x0,0xb458,0xb478,0x25f0,0x0,
  0xcc40,0xcc60,0x320,0x0,0xcc48,0xcc68,0x360,0x0,0xcc50,0xcc70,0x3b0,0x0,0xcc58,0xcc78,0x3f0,0x0,
  0xcd60,0xcd40,0x320,0x0,0xcd68,0xcd48,0x360,0x0,0xcd70,0xcd50,0x3b0,0x0,0xcd78,0xcd58,0x3f0,0x0,
  0xb5a0,0xb580,0x130,0x0,0xb5a8,0xb588,0x170,0x0,0xb5b0,0xb590,0x1a0,0x0,0xb5b8,0xb598,0x1e0,0x0,
  0xccc0,0xcce0,0x2530,0x0,0xccc8,0xcce8,0x2570,0x0,0xccd0,0xccf0,0x25a0,0x0,0xccd8,0xccf8,0x25e0,0x0,
  0xa9e0,0xa9c0,0x330,0x0,0xa9e8,0xa9c8,0x370,0x0,0xa9f0,0xa9d0,0x3a0,0x0,0xa9f8,0xa9d8,0x3e0,0x0,
  0xc1e0,0xc1c0,0x330,0x0,0xc1e8,0xc1c8,0x370,0x0,0xc1f0,0xc1d0,0x3a0,0x0,0xc1f8,0xc1d8,0x3e0,0x0,
  0xb520,0xb500,0x100,0x0,0xb528,0xb508,0x140,0x0,0xb530,0xb510,0x190,0x0,0xb538,0xb518,0x1d0,0x0,
  0xea40,0xea60,0x2500,0x0,0xea48,0xea68,0x2540,0x0,0xea50,0xea70,0x2590,0x0,0xea58,0xea78,0x25d0,0x0,
  0xb560,0xb540,0x300,0x0,0xb568,0xb548,0x340,0x0,0xb570,0xb550,0x390,0x0,0xb578,0xb558,0x3d0,0x0,
  0xb480,0xb4a0,0x300,0x0,0xb488,0xb4a8,0x340,0x0,0xb490,0xb4b0,0x390,0x0,0xb498,0xb4b8,0x3d0,0x0,
  0xb4c0,0xb4e0,0x110,0x0,0xb4c8,0xb4e8,0x150,0x0,0xb4d0,0xb4f0,0x180,0x0,0xb4d8,0xb4f8,0x1c0,0x0,
  0xb5e0,0xb5c0,0x2510,0x0,0xb5e8,0xb5c8,0x2550,0x0,0xb5f0,0xb5d0,0x2580,0x0,0xb5f8,0xb5d8,0x25c0,0x0,
  0xcde0,0xcdc0,0x310,0x0,0xcde8,0xcdc8,0x350,0x0,0xcdf0,0xcdd0,0x380,0x0,0xcdf8,0xcdd8,0x3c0,0x0,
  0xb520,0xb500,0x310,0x0,0xb528,0xb508,0x350,0x0,0xb530,0xb510,0x380,0x0,0xb538,0xb518,0x3c0,0x0,
  0xc0420000,0x0,0x0,0x0,0x0,0x0,0x0,0x0,0x0,0x0,0xc0420000,0x0,0x0,0x0,0x0,0x0,
  0x3f90,0x0,0x0,0x0,0x0,0x0,0x3f90,0x0,0x0,0x0,0x3f90,0x0,0x0,0x0,0x0,0x0,
  0x0,0xc042,0x0,0x0,0x0,0xc0420000,0x0,0x0,0x0,0x0,0x0,0xc042,0x0,0x0,0x0,0xc0420000,
  0x0,0x0,0x0,0x0,0x0,0x0,0x0,0x0,0x0,0x0,0x0,0x0,0x0,0x0,0x0,0x0,
  0x0,0x0,0x0,0x3f90,0x0,0x0,0x0,0x0,0x0,0x0,0x0,0x0,0x0,0x0,0x0,0x0,
  0x0,0x0,0x0,0x0,0xc042,0x0,0x0,0x0,0x0,0x0,0x0,0x0,0x0,0x0,0xc042,0x0,
  0x3f900000,0x0,0x0,0x3f900000,0x3f900000,0x0,0x0,0x0,0x0,0x0,0x3f900000,0x0,0x0,0x0,0x3f900000,0x0,
  0x0,0x0,0x0,0x0,0x0,0x0,0x0,0x0,0x0,0x0,0x0,0x0,0x0,0x0,0x0,0x0,
  0x0,0x0,0x0,0x0,0x0,0xc042,0x0,0x0,0x0,0x0,0x0,0x0,0x0,0x0,0x0,0xc042,
  0x3f90,0xc0420000,0x0,0x0,0x3f90,0x0,0x0,0x0,0x0,0x0,0x3f90,0xc0420000,0x0,0x0,0x3f903f90,0x0,
  0x0,0x0,0x0,0x0,0x0,0x0,0x0,0x0,0x0,0x0,0x0,0x0,0x0,0x0,0x0,0x0,
  0x0,0x0,0x0,0x0,0x0,0x0,0x0,0x0,0x0,0x0,0x0,0x0,0x0,0x0,0x0,0x0,
  0x3f90,0x0,0x0,0x0,0x3f903f90,0x0,0x0,0x0,0x3f900000,0x0,0x3f90,0x0,0x0,0x0,0x3f903f90,0x0,
  0x0,0x0,0x0,0x0,0x0,0x0,0x0,0x0,0x0,0x0,0x0,0x0,0x0,0x0,0x0,0x0,
  0x0,0x0,0x0,0x0,0x0,0x3f90,0x0,0x0,0x0,0x3f90,0x0,0x0,0x0,0x0,0x0,0x3f90,
  0x0,0x0,0x0,0x0,0x0,0x0,0x0,0x0,0x0,0x0,0x0,0x0,0x0,0x0,0x0,0x0,
  0x0,0x8,0xa820,0xc00,0x40,0x48,0xa828,0xc10,0x90,0x98,0xa830,0xc20,0xd0,0xd8,0xa838,0xc30,
  0x120,0x128,0xa860,0xd80,0x160,0x168,0xa868,0xd90,0x1b0,0x1b8,0xa870,0xda0,0x1f0,0x1f8,0xa878,0xdb0,
  0x1000,0x2400,0xa8a0,0xf00,0x1040,0x2440,0xa8a8,0xf10,0x1090,0x2490,0xa8b0,0xf20,0x10d0,0x24d0,0xa8b8,0xf30,
  0x1120,0x2520,0xa8e0,0x1080,0x1160,0x2560,0xa8e8,0x1090,0x11b0,0x25b0,0xa8f0,0x10a0,0x11f0,0x25f0,0xa8f8,0x10b0,
  0x10,0x18,0xa900,0x1220,0x50,0x58,0xa908,0x1230,0x80,0x88,0xa910,0x1200,0xc0,0xc8,0xa918,0x1210,
  0x130,0x138,0xa940,0x13a0,0x170,0x178,0xa948,0x13b0,0x1a0,0x1a8,0xa950,0x1380,0x1e0,0x1e8,0xa958,0x1390,
  0x1010,0x2410,0xa980,0x1520,0x1050,0x2450,0xa988,0x1530,0x1080,0x2480,0xa990,0x1500,0x10c0,0x24c0,0xa998,0x1510,
  0x1130,0x2530,0xa9c0,0x16a0,0x1170,0x2570,0xa9c8,0x16b0,0x11a0,0x25a0,0xa9d0,0x1680,0x11e0,0x25e0,0xa9d8,0x1690,
  0x20,0x28,0xae20,0xc40,0x60,0x68,0xae28,0xc50,0xb0,0xb8,0xae30,0xc60,0xf0,0xf8,0xae38,0xc70,
  0x100,0x108,0xae60,0xdc0,0x140,0x148,0xae68,0xdd0,0x190,0x198,0xae70,0xde0,0x1d0,0x1d8,0xae78,0xdf0,
  0x1020,0x2420,0xaea0,0xf40,0x1060,0x2460,0xaea8,0xf50,0x10b0,0x24b0,0xaeb0,0xf60,0x10f0,0x24f0,0xaeb8,0xf70,
  0x1100,0x2500,0xaee0,0x10c0,0x1140,0x2540,0xaee8,0x10d0,0x1190,0x2590,0xaef0,0x10e0,0x11d0,0x25d0,0xaef8,0x10f0,
  0x30,0x38,0xaf00,0x1260,0x70,0x78,0xaf08,0x1270,0xa0,0xa8,0xaf10,0x1240,0xe0,0xe8,0xaf18,0x1250,
  0x110,0x118,0xaf40,0x13e0,0x150,0x158,0xaf48,0x13f0,0x180,0x188,0xaf50,0x13c0,0x1c0,0x1c8,0xaf58,0x13d0,
  0x1030,0x2430,0xaf80,0x1560,0x1070,0x2470,0xaf88,0x1570,0x10a0,0x24a0,0xaf90,0x1540,0x10e0,0x24e0,0xaf98,0x1550,
  0x1110,0x2510,0xafc0,0x16e0,0x1150,0x2550,0xafc8,0x16f0,0x1180,0x2580,0xafd0,0x16c0,0x11c0,0x25c0,0xafd8,0x16d0,
  0xc000,0xc020,0x2600,0x0,0xc008,0xc028,0x2640,0x0,0xc010,0xc030,0x2690,0x0,0xc018,0xc038,0x26d0,0x0,
  0xd800,0xd820,0x400,0x0,0xd808,0xd828,0x440,0x0,0xd810,0xd830,0x490,0x0,0xd818,0xd838,0x4d0,0x0,
  0xc120,0xc100,0x2800,0x0,0xc128,0xc108,0x2840,0x0,0xc130,0xc110,0x2890,0x0,0xc138,0xc118,0x28d0,0x0,
  0xd840,0xd860,0x2800,0x0,0xd848,0xd868,0x2840,0x0,0xd850,0xd870,0x2890,0x0,0xd858,0xd878,0x28d0,0x0,
  0xc080,0xc0a0,0x2610,0x0,0xc088,0xc0a8,0x2650,0x0,0xc090,0xc0b0,0x2680,0x0,0xc098,0xc0b8,0x26c0,0x0,
  0xc1a0,0xc180,0x410,0x0,0xc1a8,0xc188,0x450,0x0,0xc1b0,0xc190,0x480,0x0,0xc1b8,0xc198,0x4c0,0x0,
  0xd9a0,0xd980,0x2810,0x0,0xd9a8,0xd988,0x2850,0x0,0xd9b0,0xd990,0x2880,0x0,0xd9b8,0xd998,0x28c0,0x0,
  0xd9e0,0xd9c0,0x2810,0x0,0xd9e8,0xd9c8,0x2850,0x0,0xd9f0,0xd9d0,0x2880,0x0,0xd9f8,0xd9d8,0x28c0,0x0,
  0xc600,0xc620,0x2620,0x0,0xc608,0xc628,0x2660,0x0,0xc610,0xc630,0x26b0,0x0,0xc618,0xc638,0x26f0,0x0,
  0xde00,0xde20,0x420,0x0,0xde08,0xde28,0x460,0x0,0xde10,0xde30,0x4b0,0x0,0xde18,0xde38,0x4f0,0x0,
  0xd920,0xd900,0x2820,0x0,0xd928,0xd908,0x2860,0x0,0xd930,0xd910,0x28b0,0x0,0xd938,0xd918,0x28f0,0x0,
  0xd960,0xd940,0x2820,0x0,0xd968,0xd948,0x2860,0x0,0xd970,0xd950,0x28b0,0x0,0xd978,0xd958,0x28f0,0x0,
  0xd880,0xd8a0,0x2630,0x0,0xd888,0xd8a8,0x2670,0x0,0xd890,0xd8b0,0x26a0,0x0,0xd898,0xd8b8,0x26e0,0x0,
  0xeba0,0xeb80,0x430,0x0,0xeba8,0xeb88,0x470,0x0,0xebb0,0xeb90,0x4a0,0x0,0xebb8,0xeb98,0x4e0,0x0,
  0xd8c0,0xd8e0,0x2830,0x0,0xd8c8,0xd8e8,0x2870,0x0,0xd8d0,0xd8f0,0x28a0,0x0,0xd8d8,0xd8f8,0x28e0,0x0,
  0xc600,0xc620,0x2830,0x0,0xc608,0xc628,0x2870,0x0,0xc610,0xc630,0x28a0,0x0,0xc618,0xc638,0x28e0,0x0,
  0x3f90,0x3f90,0x0,0x0,0x0,0x3f90,0x3f90,0x0,0x0,0x0,0x3f90,0x3f90,0x0,0x0,0x0,0x3f90,
  0x0,0xc0420000,0x0,0x0,0x0,0x0,0x0,0x0,0x0,0x0,0x0,0xc0420000,0x0,0x0,0x0,0x0,
  0x3f900000,0x0,0x0,0x0,0x0,0x0,0x3f900000,0x0,0x0,0x0,0x3f900000,0x0,0x0,0x0,0x0,0x0,
  0x0,0x0,0x0,0x0,0x0,0x0,0x0,0x0,0x0,0x0,0x0,0x0,0x0,0x0,0x0,0x0,
  0xc042,0x0,0x0,0x0,0xc0420000,0x0,0x0,0x0,0x0,0x0,0xc042,0x0,0x0,0x0,0xc0420000,0x0,
  0x0,0x0,0x0,0x0,0x0,0x3f900000,0x0,0x0,0x0,0x3f900000,0x0,0x0,0x0,0x0,0x0,0x3f900000,
  0x0,0x0,0x0,0x0,0x0,0xc042,0x0,0x0,0x0,0x0,0x0,0x0,0x0,0x0,0x0,0xc042,
  0x0,0x0,0x0,0x0,0x0,0x0,0x0,0x0,0x0,0x0,0x0,0x0,0x0,0x0,0x0,0x0,
  0x0,0x0,0x0,0x0,0x0,0x0,0x0,0x0,0x0,0x0,0x0,0x0,0x0,0x0,0x3f90,0x0,
  0x0,0x0,0x0,0x0,0x0,0xc0420000,0x0,0x0,0x0,0x0,0x0,0x0,0x0,0x0,0x0,0xc0420000,
  0x0,0x3f90,0x0,0x0,0x0,0x3f90,0x0,0x0,0x0,0x0,0x0,0x3f90,0x0,0x0,0x3f900000,0x3f90,
  0x0,0x0,0x0,0x0,0x0,0x0,0x0,0x0,0x0,0x0,0x0,0x0,0x0,0x0,0x0,0x0,
  0x0,0x0,0x0,0x0,0x0,0x0,0x0,0x0,0x0,0x0,0x0,0x0,0x0,0x0,0x0,0x0,
  0x0,0x3f90,0x3f900000,0x0,0x0,0x3f90,0x0,0x0,0x0,0x0,0x0,0x3f90,0x0,0x0,0x0,0x3f90,
  0xc042,0x0,0x0,0x0,0x0,0x0,0x0,0x0,0x0,0x0,0xc042,0x0,0x0,0x0,0x0,0x0,
  0x0,0x0,0x0,0x0,0x0,0x0,0x0,0x0,0x0,0x0,0x0,0x0,0x0,0x0,0x0,0x0,
  0x0,0x1200,0x19200,0x1800,0x40,0x1240,0x19208,0x1810,0x90,0x1290,0x19210,0x1820,0xd0,0x12d0,0x19218,0x1830,
  0x120,0x1320,0x19240,0x1980,0x160,0x1360,0x19248,0x1990,0x1b0,0x13b0,0x19250,0x19a0,0x1f0,0x13f0,0x19258,0x19b0,
  0x1000,0x3608,0x19280,0x1b00,0x1040,0x3648,0x19288,0x1b10,0x1090,0x3698,0x19290,0x1b20,0x10d0,0x36d8,0x19298,0x1b30,
  0x1120,0x3728,0x192c0,0x1c80,0x1160,0x3768,0x192c8,0x1c90,0x11b0,0x37b8,0x192d0,0x1ca0,0x11f0,0x37f8,0x192d8,0x1cb0,
  0x10,0x1210,0x19320,0x1e20,0x50,0x1250,0x19328,0x1e30,0x80,0x1280,0x19330,0x1e00,0xc0,0x12c0,0x19338,0x1e10,
  0x130,0x1330,0x19360,0x1fa0,0x170,0x1370,0x19368,0x1fb0,0x1a0,0x13a0,0x19370,0x1f80,0x1e0,0x13e0,0x19378,0x1f90,
  0x1010,0x3618,0x193a0,0x2120,0x1050,0x3658,0x193a8,0x2130,0x1080,0x3688,0x193b0,0x2100,0x10c0,0x36c8,0x193b8,0x2110,
  0x1130,0x3738,0x193e0,0x22a0,0x1170,0x3778,0x193e8,0x22b0,0x11a0,0x37a8,0x193f0,0x2280,0x11e0,0x37e8,0x193f8,0x2290,
  0x20,0x1220,0x19800,0x1840,0x60,0x1260,0x19808,0x1850,0xb0,0x12b0,0x19810,0x1860,0xf0,0x12f0,0x19818,0x1870,
  0x100,0x1300,0x19840,0x19c0,0x140,0x1340,0x19848,0x19d0,0x190,0x1390,0x19850,0x19e0,0x1d0,0x13d0,0x19858,0x19f0,
  0x1020,0x3628,0x19880,0x1b40,0x1060,0x3668,0x19888,0x1b50,0x10b0,0x36b8,0x19890,0x1b60,0x10f0,0x36f8,0x19898,0x1b70,
  0x1100,0x3708,0x198c0,0x1cc0,0x1140,0x3748,0x198c8,0x1cd0,0x1190,0x3798,0x198d0,0x1ce0,0x11d0,0x37d8,0x198d8,0x1cf0,
  0x30,0x1230,0x19920,0x1e60,0x70,0x1270,0x19928,0x1e70,0xa0,0x12a0,0x19930,0x1e40,0xe0,0x12e0,0x19938,0x1e50,
  0x110,0x1310,0x19960,0x1fe0,0x150,0x1350,0x19968,0x1ff0,0x180,0x1380,0x19970,0x1fc0,0x1c0,0x13c0,0x19978,0x1fd0,
  0x1030,0x3638,0x199a0,0x2160,0x1070,0x3678,0x199a8,0x2170,0x10a0,0x36a8,0x199b0,0x2140,0x10e0,0x36e8,0x199b8,0x2150,
  0x1110,0x3718,0x199e0,0x22e0,0x1150,0x3758,0x199e8,0x22f0,0x1180,0x3788,0x199f0,0x22c0,0x11c0,0x37c8,0x199f8,0x22d0,
  0xcc00,0xcc20,0x2720,0x0,0xcc08,0xcc28,0x2760,0x0,0xcc10,0xcc30,0x27b0,0x0,0xcc18,0xcc38,0x27f0,0x0,
  0xcd20,0xcd00,0x520,0x0,0xcd28,0xcd08,0x560,0x0,0xcd30,0xcd10,0x5b0,0x0,0xcd38,0xcd18,0x5f0,0x0,
  0xe440,0xe460,0x2920,0x0,0xe448,0xe468,0x2960,0x0,0xe450,0xe470,0x29b0,0x0,0xe458,0xe478,0x29f0,0x0,
  0xeb60,0xeb40,0x2920,0x0,0xeb68,0xeb48,0x2960,0x0,0xeb70,0xeb50,0x29b0,0x0,0xeb78,0xeb58,0x29f0,0x0,
  0xe480,0xe4a0,0x2730,0x0,0xe488,0xe4a8,0x2770,0x0,0xe490,0xe4b0,0x27a0,0x0,0xe498,0xe4b8,0x27e0,0x0,
  0xe5a0,0xe580,0x530,0x0,0xe5a8,0xe588,0x570,0x0,0xe5b0,0xe590,0x5a0,0x0,0xe5b8,0xe598,0x5e0,0x0,
  0xe4c0,0xe4e0,0x2930,0x0,0xe4c8,0xe4e8,0x2970,0x0,0xe4d0,0xe4f0,0x29a0,0x0,0xe4d8,0xe4f8,0x29e0,0x0,
  0xd9e0,0xd9c0,0x2930,0x0,0xd9e8,0xd9c8,0x2970,0x0,0xd9f0,0xd9d0,0x29a0,0x0,0xd9f8,0xd9d8,0x29e0,0x0,
  0xe400,0xe420,0x2700,0x0,0xe408,0xe428,0x2740,0x0,0xe410,0xe430,0x2790,0x0,0xe418,0xe438,0x27d0,0x0,
  0xe520,0xe500,0x500,0x0,0xe528,0xe508,0x540,0x0,0xe530,0xe510,0x590,0x0,0xe538,0xe518,0x5d0,0x0,
  0xe560,0xe540,0x2900,0x0,0xe568,0xe548,0x2940,0x0,0xe570,0xe550,0x2990,0x0,0xe578,0xe558,0x29d0,0x0,
  0xcc80,0xcca0,0x2900,0x0,0xcc88,0xcca8,0x2940,0x0,0xcc90,0xccb0,0x2990,0x0,0xcc98,0xccb8,0x29d0,0x0,
  0xcda0,0xcd80,0x2710,0x0,0xcda8,0xcd88,0x2750,0x0,0xcdb0,0xcd90,0x2780,0x0,0xcdb8,0xcd98,0x27c0,0x0,
  0xeac0,0xeae0,0x510,0x0,0xeac8,0xeae8,0x550,0x0,0xead0,0xeaf0,0x580,0x0,0xead8,0xeaf8,0x5c0,0x0,
  0xe5e0,0xe5c0,0x2910,0x0,0xe5e8,0xe5c8,0x2950,0x0,0xe5f0,0xe5d0,0x2980,0x0,0xe5f8,0xe5d8,0x29c0,0x0,
  0xe400,0xe420,0x2910,0x0,0xe408,0xe428,0x2950,0x0,0xe410,0xe430,0x2980,0x0,0xe418,0xe438,0x29c0,0x0,
  0x3f903f90,0x0,0x0,0x0,0x3f900000,0x0,0x3f90,0x0,0x0,0x0,0x3f903f90,0x0,0x0,0x0,0x3f900000,0x0,
  0x0,0xc042,0x0,0x0,0x0,0x0,0x0,0x0,0x0,0x0,0x0,0xc042,0x0,0x0,0x0,0x0,
  0x0,0x0,0x0,0x0,0x0,0x0,0x0,0x0,0x0,0x0,0x0,0x0,0x0,0x0,0x0,0x3f900000,
  0x0,0x0,0x0,0x0,0x0,0x0,0x0,0x0,0x0,0x0,0x0,0x0,0x0,0x0,0x0,0x0,
  0x0,0x0,0x0,0x0,0x0,0x0,0x0,0x0,0x0,0x0,0x0,0x0,0x0,0x0,0x0,0x0,
  0x0,0x3f90,0x0,0x3f900000,0x0,0x3f90,0x0,0x0,0x0,0x0,0x0,0x3f90,0x0,0x0,0x0,0x3f90,
  0xc042,0x0,0x0,0x0,0xc0420000,0x0,0x0,0x0,0x0,0x0,0xc042,0x0,0x0,0x0,0xc0420000,0x0,
  0x0,0x0,0x0,0x0,0x0,0x0,0x0,0x0,0x0,0x0,0x0,0x0,0x0,0x0,0x0,0x0,
  0x0,0xc0420000,0x0,0x0,0x0,0x0,0x0,0x0,0x0,0x0,0x0,0xc0420000,0x0,0x0,0x0,0x0,
  0x0,0x0,0x0,0x0,0x0,0xc042,0x0,0x0,0x0,0x0,0x0,0x0,0x0,0x0,0x0,0xc042,
  0x3f903f90,0x0,0x0,0x0,0x3f900000,0x0,0x3f90,0x0,0x0,0x0,0x3f903f90,0x0,0x0,0x0,0x3f900000,0x0,
  0x0,0x0,0x0,0x0,0x0,0x0,0x0,0x0,0x0,0x0,0x0,0x0,0x0,0x0,0x0,0x0,
  0x0,0x0,0x0,0x0,0xc042,0x0,0x0,0x0,0x0,0x0,0x0,0x0,0x0,0x0,0x3f90c042,0x0,
  0x0,0x0,0x0,0x0,0x0,0x3f90,0x0,0x0,0x0,0x3f90,0x0,0x0,0x0,0x0,0x0,0x3f90,
  0x0,0x0,0x0,0x0,0x0,0x0,0x0,0x0,0x0,0x0,0x0,0x0,0x0,0x0,0x0,0x0,
  0x0,0x0,0x0,0x0,0x0,0x0,0x0,0x0,0x0,0x0,0x0,0x0,0x0,0x0,0x0,0x0,
  0x0,0x1208,0x19220,0x2400,0x40,0x1248,0x19228,0x2410,0x90,0x1298,0x19230,0x2420,0xd0,0x12d8,0x19238,0x2430,
  0x120,0x1328,0x19260,0x2580,0x160,0x1368,0x19268,0x2590,0x1b0,0x13b8,0x19270,0x25a0,0x1f0,0x13f8,0x19278,0x25b0,
  0x1000,0x3600,0x192a0,0x2700,0x1040,0x3640,0x192a8,0x2710,0x1090,0x3690,0x192b0,0x2720,0x10d0,0x36d0,0x192b8,0x2730,
  0x1120,0x3720,0x192e0,0x2880,0x1160,0x3760,0x192e8,0x2890,0x11b0,0x37b0,0x192f0,0x28a0,0x11f0,0x37f0,0x192f8,0x28b0,
  0x10,0x1218,0x19300,0x2a20,0x50,0x1258,0x19308,0x2a30,0x80,0x1288,0x19310,0x2a00,0xc0,0x12c8,0x19318,0x2a10,
  0x130,0x1338,0x19340,0x2ba0,0x170,0x1378,0x19348,0x2bb0,0x1a0,0x13a8,0x19350,0x2b80,0x1e0,0x13e8,0x19358,0x2b90,
  0x1010,0x3610,0x19380,0x2d20,0x1050,0x3650,0x19388,0x2d30,0x1080,0x3680,0x19390,0x2d00,0x10c0,0x36c0,0x19398,0x2d10,
  0x1130,0x3730,0x193c0,0x2ea0,0x1170,0x3770,0x193c8,0x2eb0,0x11a0,0x37a0,0x193d0,0x2e80,0x11e0,0x37e0,0x193d8,0x2e90,
  0x20,0x1228,0x19820,0x2440,0x60,0x1268,0x19828,0x2450,0xb0,0x12b8,0x19830,0x2460,0xf0,0x12f8,0x19838,0x2470,
  0x100,0x1308,0x19860,0x25c0,0x140,0x1348,0x19868,0x25d0,0x190,0x1398,0x19870,0x25e0,0x1d0,0x13d8,0x19878,0x25f0,
  0x1020,0x3620,0x198a0,0x2740,0x1060,0x3660,0x198a8,0x2750,0x10b0,0x36b0,0x198b0,0x2760,0x10f0,0x36f0,0x198b8,0x2770,
  0x1100,0x3700,0x198e0,0x28c0,0x1140,0x3740,0x198e8,0x28d0,0x1190,0x3790,0x198f0,0x28e0,0x11d0,0x37d0,0x198f8,0x28f0,
  0x30,0x1238,0x19900,0x2a60,0x70,0x1278,0x19908,0x2a70,0xa0,0x12a8,0x19910,0x2a40,0xe0,0x12e8,0x19918,0x2a50,
  0x110,0x1318,0x19940,0x2be0,0x150,0x1358,0x19948,0x2bf0,0x180,0x1388,0x19950,0x2bc0,0x1c0,0x13c8,0x19958,0x2bd0,
  0x1030,0x3630,0x19980,0x2d60,0x1070,0x3670,0x19988,0x2d70,0x10a0,0x36a0,0x19990,0x2d40,0x10e0,0x36e0,0x19998,0x2d50,
  0x1110,0x3710,0x199c0,0x2ee0,0x1150,0x3750,0x199c8,0x2ef0,0x1180,0x3780,0x199d0,0x2ec0,0x11c0,0x37c0,0x199d8,0x2ed0,
  0x10000000,0x0,0x0,0x800,0x10000000,0x10,0x90,0x890,0x10000000,0x20,0x120,0x920,0x10000000,0x30,0x1b0,0x9b0,
  0x20003180,0x40,0x240,0xa40,0x20003190,0x50,0x2d0,0xad0,0x200031a0,0x60,0x360,0xb60,0x200031b0,0x70,0x3f0,0xbf0,
  0x40000000,0x610,0xc00,0x1400,0x40000010,0x600,0xc90,0x1490,0x40000020,0x630,0xd20,0x1520,0x40000030,0x620,0xdb0,0x15b0,
  0x20033180,0x650,0xe40,0x1640,0x20033190,0x640,0xed0,0x16d0,0x200331a0,0x670,0xf60,0x1760,0x200331b0,0x660,0xff0,0x17f0,
  0x40030020,0xc20,0x10,0x808,0x40030030,0xc30,0x80,0x898,0x40030000,0xc00,0x130,0x928,0x40030010,0xc10,0x1a0,0x9b8,
  0x200631a0,0xc60,0x250,0xa48,0x200631b0,0xc70,0x2c0,0xad8,0x20063180,0xc40,0x370,0xb68,0x20063190,0xc50,0x3e0,0xbf8,
  0x40060020,0x1230,0xc10,0x1408,0x40060030,0x1220,0xc80,0x1498,0x40060000,0x1210,0xd30,0x1528,0x40060010,0x1200,0xda0,0x15b8,
  0x10000000,0x1270,0xe50,0x1648,0x10000000,0x1260,0xec0,0x16d8,0x10000000,0x1250,0xf70,0x1768,0x10000000,0x1240,0xfe0,0x17f8,
  0x10000000,0x1840,0x20,0x810,0x10000000,0x1850,0xb0,0x880,0x10000000,0x1860,0x100,0x930,0x10000000,0x1870,0x190,0x9a0,
  0x200031c0,0x1800,0x260,0xa50,0x200031d0,0x1810,0x2f0,0xac0,0x200031e0,0x1820,0x340,0xb70,0x200031f0,0x1830,0x3d0,0xbe0,
  0x40000040,0x1e50,0xc20,0x1410,0x40000050,0x1e40,0xcb0,0x1480,0x40000060,0x1e70,0xd00,0x1530,0x40000070,0x1e60,0xd90,0x15a0,
  0x200331c0,0x1e10,0xe60,0x1650,0x200331d0,0x1e00,0xef0,0x16c0,0x200331e0,0x1e30,0xf40,0x1770,0x200331f0,0x1e20,0xfd0,0x17e0,
  0x40030060,0x2460,0x30,0x818,0x40030070,0x2470,0xa0,0x888,0x40030040,0x2440,0x110,0x938,0x40030050,0x2450,0x180,0x9a8,
  0x200631e0,0x2420,0x270,0xa58,0x200631f0,0x2430,0x2e0,0xac8,0x200631c0,0x2400,0x350,0xb78,0x200631d0,0x2410,0x3c0,0xbe8,
  0x40060060,0x2a70,0xc30,0x1418,0x40060070,0x2a60,0xca0,0x1488,0x40060040,0x2a50,0xd10,0x1538,0x40060050,0x2a40,0xd80,0x15a8,
  0x10000000,0x2a30,0xe70,0x1658,0x10000000,0x2a20,0xee0,0x16c8,0x10000000,0x2a10,0xf50,0x1778,0x10000000,0x2a00,0xfc0,0x17e8,
  0xbfd8bf90,0x3f403f00,0x0,0x0,0x0,0x0,0x0,0x0,0x0,0x0,0x0,0x0,0x0,0x0,0x0,0x0,
  0x0,0x0,0xbfd8bf90,0x3f403f00,0x0,0x0,0x0,0x0,0x0,0x0,0x0,0x0,0x0,0x0,0x0,0x0,
  0xbfd83f90,0x3f40bf00,0x0,0x0,0x0,0x0,0x0,0x0,0x0,0x0,0x0,0x0,0x0,0x0,0x0,0x0,
  0x0,0x0,0xbfd83f90,0x3f40bf00,0x0,0x0,0x0,0x0,0x0,0x0,0x0,0x0,0x0,0x0,0x0,0x0,
  0x0,0x0,0x0,0x0,0xbfd8bf90,0x3f403f00,0x0,0x0,0x0,0x0,0x0,0x0,0x0,0x0,0x0,0x0,
  0x0,0x0,0x0,0x0,0x0,0x0,0xbfd8bf90,0x3f403f00,0x0,0x0,0x0,0x0,0x0,0x0,0x0,0x0,
  0x0,0x0,0x0,0x0,0xbfd83f90,0x3f40bf00,0x0,0x0,0x0,0x0,0x0,0x0,0x0,0x0,0x0,0x0,
  0x0,0x0,0x0,0x0,0x0,0x0,0xbfd83f90,0x3f40bf00,0x0,0x0,0x0,0x0,0x0,0x0,0x0,0x0,
  0x0,0x0,0x0,0x0,0x0,0x0,0x0,0x0,0xbfd8bf90,0x3f403f00,0x0,0x0,0x0,0x0,0x0,0x0,
  0x0,0x0,0x0,0x0,0x0,0x0,0x0,0x0,0x0,0x0,0xbfd8bf90,0x3f403f00,0x0,0x0,0x0,0x0,
  0x0,0x0,0x0,0x0,0x0,0x0,0x0,0x0,0xbfd83f90,0x3f40bf00,0x0,0x0,0x0,0x0,0x0,0x0,
  0x0,0x0,0x0,0x0,0x0,0x0,0x0,0x0,0x0,0x0,0xbfd83f90,0x3f40bf00,0x0,0x0,0x0,0x0,
  0x0,0x0,0x0,0x0,0x0,0x0,0x0,0x0,0x0,0x0,0x0,0x0,0xbfd8bf90,0x3f403f00,0x0,0x0,
  0x0,0x0,0x0,0x0,0x0,0x0,0x0,0x0,0x0,0x0,0x0,0x0,0x0,0x0,0xbfd8bf90,0x3f403f00,
  0x0,0x0,0x0,0x0,0x0,0x0,0x0,0x0,0x0,0x0,0x0,0x0,0xbfd83f90,0x3f40bf00,0x0,0x0,
  0x0,0x0,0x0,0x0,0x0,0x0,0x0,0x0,0x0,0x0,0x0,0x0,0x0,0x0,0xbfd83f90,0x3f40bf00,
  0x0,0x0,0x0,0x0,0x0,0x0,0x0,0x0,0x0,0x0,0x0,0x0,0x0,0x0,0x0,0x0,
  0x0,0x0,0x0,0x0,0x0,0x0,0x0,0x0,0x0,0x0,0x0,0x0,0x0,0x0,0x0,0x0,
  0x0,0x0,0x0,0x0,0x0,0x0,0x0,0x0,0x0,0x0,0x0,0x0,0x0,0x0,0x0,0x0,
  0x0,0x0,0x0,0x0,0x0,0x0,0x0,0x0,0x0,0x0,0x0,0x0,0x0,0x0,0x0,0x0,
  0x0,0x0,0x0,0x0,0x0,0x0,0x0,0x0,0x0,0x0,0x0,0x0,0x0,0x0,0x0,0x0,
  0x0,0x0,0x0,0x0,0x0,0x0,0x0,0x0,0x0,0x0,0x0,0x0,0x0,0x0,0x0,0x0,
  0x0,0x0,0x0,0x0,0x0,0x0,0x0,0x0,0x0,0x0,0x0,0x0,0x0,0x0,0x0,0x0,
  0x0,0x0,0x0,0x0,0x0,0x0,0x0,0x0,0x0,0x0,0x0,0x0,0x0,0x0,0x0,0x0,
  0x0,0x0,0x0,0x0,0x0,0x0,0x0,0x0,0x0,0x0,0x0,0x0,0x0,0x0,0x0,0x0,
  0x0,0x0,0x0,0x0,0x0,0x0,0x0,0x0,0x0,0x0,0x0,0x0,0x0,0x0,0x0,0x0,
  0x0,0x0,0x0,0x0,0x0,0x0,0x0,0x0,0x0,0x0,0x0,0x0,0x0,0x0,0x0,0x0,
  0x0,0x0,0x0,0x0,0x0,0x0,0x0,0x0,0x0,0x0,0x0,0x0,0x0,0x0,0x0,0x0,
  0x0,0x0,0x0,0x0,0x0,0x0,0x0,0x0,0x0,0x0,0x0,0x0,0x0,0x0,0x0,0x0,
  0x0,0x0,0x0,0x0,0x0,0x0,0x0,0x0,0x0,0x0,0x0,0x0,0x0,0x0,0x0,0x0,
  0x0,0x0,0x0,0x0,0x0,0x0,0x0,0x0,0x0,0x0,0x0,0x0,0x0,0x0,0x0,0x0,
  0x0,0x0,0x0,0x0,0x0,0x0,0x0,0x0,0x0,0x0,0x0,0x0,0x0,0x0,0x0,0x0,
  0x10000000,0x180000,0x40,0x820,0x10000000,0x180010,0xd0,0x8b0,0x10000000,0x180020,0x160,0x900,0x10000000,0x180030,0x1f0,0x990,
  0x20093180,0x180040,0x200,0xa60,0x20093190,0x180050,0x290,0xaf0,0x200931a0,0x180060,0x320,0xb40,0x200931b0,0x180070,0x3b0,0xbd0,
  0x40090000,0x180610,0xc40,0x1420,0x40090010,0x180600,0xcd0,0x14b0,0x40090020,0x180630,0xd60,0x1500,0x40090030,0x180620,0xdf0,0x1590,
  0x200c3180,0x180650,0xe00,0x1660,0x200c3190,0x180640,0xe90,0x16f0,0x200c31a0,0x180670,0xf20,0x1740,0x200c31b0,0x180660,0xfb0,0x17d0,
  0x400c0020,0x180c20,0x50,0x828,0x400c0030,0x180c30,0xc0,0x8b8,0x400c0000,0x180c00,0x170,0x908,0x400c0010,0x180c10,0x1e0,0x998,
  0x200f31a0,0x180c60,0x210,0xa68,0x200f31b0,0x180c70,0x280,0xaf8,0x200f3180,0x180c40,0x330,0xb48,0x200f3190,0x180c50,0x3a0,0xbd8,
  0x400f0020,0x181230,0xc50,0x1428,0x400f0030,0x181220,0xcc0,0x14b8,0x400f0000,0x181210,0xd70,0x1508,0x400f0010,0x181200,0xde0,0x1598,
  0x10000000,0x181270,0xe10,0x1668,0x10000000,0x181260,0xe80,0x16f8,0x10000000,0x181250,0xf30,0x1748,0x10000000,0x181240,0xfa0,0x17d8,
  0x10000000,0x181840,0x60,0x830,0x10000000,0x181850,0xf0,0x8a0,0x10000000,0x181860,0x140,0x910,0x10000000,0x181870,0x1d0,0x980,
  0x200931c0,0x181800,0x220,0xa70,0x200931d0,0x181810,0x2b0,0xae0,0x200931e0,0x181820,0x300,0xb50,0x200931f0,0x181830,0x390,0xbc0,
  0x40090040,0x181e50,0xc60,0x1430,0x40090050,0x181e40,0xcf0,0x14a0,0x40090060,0x181e70,0xd40,0x1510,0x40090070,0x181e60,0xdd0,0x1580,
  0x200c31c0,0x181e10,0xe20,0x1670,0x200c31d0,0x181e00,0xeb0,0x16e0,0x200c31e0,0x181e30,0xf00,0x1750,0x200c31f0,0x181e20,0xf90,0x17c0,
  0x400c0060,0x182460,0x70,0x838,0x400c0070,0x182470,0xe0,0x8a8,0x400c0040,0x182440,0x150,0x918,0x400c0050,0x182450,0x1c0,0x988,
  0x200f31e0,0x182420,0x230,0xa78,0x200f31f0,0x182430,0x2a0,0xae8,0x200f31c0,0x182400,0x310,0xb58,0x200f31d0,0x182410,0x380,0xbc8,
  0x400f0060,0x182a70,0xc70,0x1438,0x400f0070,0x182a60,0xce0,0x14a8,0x400f0040,0x182a50,0xd50,0x1518,0x400f0050,0x182a40,0xdc0,0x1588,
  0x10000000,0x182a30,0xe30,0x1678,0x10000000,0x182a20,0xea0,0x16e8,0x10000000,0x182a10,0xf10,0x1758,0x10000000,0x182a00,0xf80,0x17c8,
  0xbfd8bf90,0x3f403f00,0x0,0x0,0x0,0x0,0x0,0x0,0x0,0x0,0x0,0x0,0x0,0x0,0x0,0x0,
  0x0,0x0,0xbfd8bf90,0x3f403f00,0x0,0x0,0x0,0x0,0x0,0x0,0x0,0x0,0x0,0x0,0x0,0x0,
  0xbfd83f90,0x3f40bf00,0x0,0x0,0x0,0x0,0x0,0x0,0x0,0x0,0x0,0x0,0x0,0x0,0x0,0x0,
  0x0,0x0,0xbfd83f90,0x3f40bf00,0x0,0x0,0x0,0x0,0x0,0x0,0x0,0x0,0x0,0x0,0x0,0x0,
  0x0,0x0,0x0,0x0,0xbfd8bf90,0x3f403f00,0x0,0x0,0x0,0x0,0x0,0x0,0x0,0x0,0x0,0x0,
  0x0,0x0,0x0,0x0,0x0,0x0,0xbfd8bf90,0x3f403f00,0x0,0x0,0x0,0x0,0x0,0x0,0x0,0x0,
  0x0,0x0,0x0,0x0,0xbfd83f90,0x3f40bf00,0x0,0x0,0x0,0x0,0x0,0x0,0x0,0x0,0x0,0x0,
  0x0,0x0,0x0,0x0,0x0,0x0,0xbfd83f90,0x3f40bf00,0x0,0x0,0x0,0x0,0x0,0x0,0x0,0x0,
  0x0,0x0,0x0,0x0,0x0,0x0,0x0,0x0,0xbfd8bf90,0x3f403f00,0x0,0x0,0x0,0x0,0x0,0x0,
  0x0,0x0,0x0,0x0,0x0,0x0,0x0,0x0,0x0,0x0,0xbfd8bf90,0x3f403f00,0x0,0x0,0x0,0x0,
  0x0,0x0,0x0,0x0,0x0,0x0,0x0,0x0,0xbfd83f90,0x3f40bf00,0x0,0x0,0x0,0x0,0x0,0x0,
  0x0,0x0,0x0,0x0,0x0,0x0,0x0,0x0,0x0,0x0,0xbfd83f90,0x3f40bf00,0x0,0x0,0x0,0x0,
  0x0,0x0,0x0,0x0,0x0,0x0,0x0,0x0,0x0,0x0,0x0,0x0,0xbfd8bf90,0x3f403f00,0x0,0x0,
  0x0,0x0,0x0,0x0,0x0,0x0,0x0,0x0,0x0,0x0,0x0,0x0,0x0,0x0,0xbfd8bf90,0x3f403f00,
  0x0,0x0,0x0,0x0,0x0,0x0,0x0,0x0,0x0,0x0,0x0,0x0,0xbfd83f90,0x3f40bf00,0x0,0x0,
  0x0,0x0,0x0,0x0,0x0,0x0,0x0,0x0,0x0,0x0,0x0,0x0,0x0,0x0,0xbfd83f90,0x3f40bf00,
  0x0,0x0,0x0,0x0,0x0,0x0,0x0,0x0,0x0,0x0,0x0,0x0,0x0,0x0,0x0,0x0,
  0x0,0x0,0x0,0x0,0x0,0x0,0x0,0x0,0x0,0x0,0x0,0x0,0x0,0x0,0x0,0x0,
  0x0,0x0,0x0,0x0,0x0,0x0,0x0,0x0,0x0,0x0,0x0,0x0,0x0,0x0,0x0,0x0,
  0x0,0x0,0x0,0x0,0x0,0x0,0x0,0x0,0x0,0x0,0x0,0x0,0x0,0x0,0x0,0x0,
  0x0,0x0,0x0,0x0,0x0,0x0,0x0,0x0,0x0,0x0,0x0,0x0,0x0,0x0,0x0,0x0,
  0x0,0x0,0x0,0x0,0x0,0x0,0x0,0x0,0x0,0x0,0x0,0x0,0x0,0x0,0x0,0x0,
  0x0,0x0,0x0,0x0,0x0,0x0,0x0,0x0,0x0,0x0,0x0,0x0,0x0,0x0,0x0,0x0,
  0x0,0x0,0x0,0x0,0x0,0x0,0x0,0x0,0x0,0x0,0x0,0x0,0x0,0x0,0x0,0x0,
  0x0,0x0,0x0,0x0,0x0,0x0,0x0,0x0,0x0,0x0,0x0,0x0,0x0,0x0,0x0,0x0,
  0x0,0x0,0x0,0x0,0x0,0x0,0x0,0x0,0x0,0x0,0x0,0x0,0x0,0x0,0x0,0x0,
  0x0,0x0,0x0,0x0,0x0,0x0,0x0,0x0,0x0,0x0,0x0,0x0,0x0,0x0,0x0,0x0,
  0x0,0x0,0x0,0x0,0x0,0x0,0x0,0x0,0x0,0x0,0x0,0x0,0x0,0x0,0x0,0x0,
  0x0,0x0,0x0,0x0,0x0,0x0,0x0,0x0,0x0,0x0,0x0,0x0,0x0,0x0,0x0,0x0,
  0x0,0x0,0x0,0x0,0x0,0x0,0x0,0x0,0x0,0x0,0x0,0x0,0x0,0x0,0x0,0x0,
  0x0,0x0,0x0,0x0,0x0,0x0,0x0,0x0,0x0,0x0,0x0,0x0,0x0,0x0,0x0,0x0,
  0x0,0x0,0x0,0x0,0x0,0x0,0x0,0x0,0x0,0x0,0x0,0x0,0x0,0x0,0x0,0x0,
  0x10000000,0x0,0x400,0x840,0x10000000,0x10,0x490,0x8d0,0x10000000,0x20,0x520,0x960,0x10000000,0x30,0x5b0,0x9f0,
  0x20123180,0x40,0x640,0xa00,0x20123190,0x50,0x6d0,0xa90,0x201231a0,0x60,0x760,0xb20,0x201231b0,0x70,0x7f0,0xbb0,
  0x40120000,0x610,0x1000,0x1440,0x40120010,0x600,0x1090,0x14d0,0x40120020,0x630,0x1120,0x1560,0x40120030,0x620,0x11b0,0x15f0,
  0x20153180,0x650,0x1240,0x1600,0x20153190,0x640,0x12d0,0x1690,0x201531a0,0x670,0x1360,0x1720,0x201531b0,0x660,0x13f0,0x17b0,
  0x40150020,0xc20,0x410,0x848,0x40150030,0xc30,0x480,0x8d8,0x40150000,0xc00,0x530,0x968,0x40150010,0xc10,0x5a0,0x9f8,
  0x201831a0,0xc60,0x650,0xa08,0x201831b0,0xc70,0x6c0,0xa98,0x20183180,0xc40,0x770,0xb28,0x20183190,0xc50,0x7e0,0xbb8,
  0x40180020,0x1230,0x1010,0x1448,0x40180030,0x1220,0x1080,0x14d8,0x40180000,0x1210,0x1130,0x1568,0x40180010,0x1200,0x11a0,0x15f8,
  0x10000000,0x1270,0x1250,0x1608,0x10000000,0x1260,0x12c0,0x1698,0x10000000,0x1250,0x1370,0x1728,0x10000000,0x1240,0x13e0,0x17b8,
  0x10000000,0x1840,0x420,0x850,0x10000000,0x1850,0x4b0,0x8c0,0x10000000,0x1860,0x500,0x970,0x10000000,0x1870,0x590,0x9e0,
  0x201231c0,0x1800,0x660,0xa10,0x201231d0,0x1810,0x6f0,0xa80,0x201231e0,0x1820,0x740,0xb30,0x201231f0,0x1830,0x7d0,0xba0,
  0x40120040,0x1e50,0x1020,0x1450,0x40120050,0x1e40,0x10b0,0x14c0,0x40120060,0x1e70,0x1100,0x1570,0x40120070,0x1e60,0x1190,0x15e0,
  0x201531c0,0x1e10,0x1260,0x1610,0x201531d0,0x1e00,0x12f0,0x1680,0x201531e0,0x1e30,0x1340,0x1730,0x201531f0,0x1e20,0x13d0,0x17a0,
  0x40150060,0x2460,0x430,0x858,0x40150070,0x2470,0x4a0,0x8c8,0x40150040,0x2440,0x510,0x978,0x40150050,0x2450,0x580,0x9e8,
  0x201831e0,0x2420,0x670,0xa18,0x201831f0,0x2430,0x6e0,0xa88,0x201831c0,0x2400,0x750,0xb38,0x201831d0,0x2410,0x7c0,0xba8,
  0x40180060,0x2a70,0x1030,0x1458,0x40180070,0x2a60,0x10a0,0x14c8,0x40180040,0x2a50,0x1110,0x1578,0x40180050,0x2a40,0x1180,0x15e8,
  0x10000000,0x2a30,0x1270,0x1618,0x10000000,0x2a20,0x12e0,0x1688,0x10000000,0x2a10,0x1350,0x1738,0x10000000,0x2a00,0x13c0,0x17a8,
  0xbfd8bf90,0x3f403f00,0x0,0x0,0x0,0x0,0x0,0x0,0x0,0x0,0x0,0x0,0x0,0x0,0x0,0x0,
  0x0,0x0,0xbfd8bf90,0x3f403f00,0x0,0x0,0x0,0x0,0x0,0x0,0x0,0x0,0x0,0x0,0x0,0x0,
  0xbfd83f90,0x3f40bf00,0x0,0x0,0x0,0x0,0x0,0x0,0x0,0x0,0x0,0x0,0x0,0x0,0x0,0x0,
  0x0,0x0,0xbfd83f90,0x3f40bf00,0x0,0x0,0x0,0x0,0x0,0x0,0x0,0x0,0x0,0x0,0x0,0x0,
  0x0,0x0,0x0,0x0,0xbfd8bf90,0x3f403f00,0x0,0x0,0x0,0x0,0x0,0x0,0x0,0x0,0x0,0x0,
  0x0,0x0,0x0,0x0,0x0,0x0,0xbfd8bf90,0x3f403f00,0x0,0x0,0x0,0x0,0x0,0x0,0x0,0x0,
  0x0,0x0,0x0,0x0,0xbfd83f90,0x3f40bf00,0x0,0x0,0x0,0x0,0x0,0x0,0x0,0x0,0x0,0x0,
  0x0,0x0,0x0,0x0,0x0,0x0,0xbfd83f90,0x3f40bf00,0x0,0x0,0x0,0x0,0x0,0x0,0x0,0x0,
  0x0,0x0,0x0,0x0,0x0,0x0,0x0,0x0,0xbfd8bf90,0x3f403f00,0x0,0x0,0x0,0x0,0x0,0x0,
  0x0,0x0,0x0,0x0,0x0,0x0,0x0,0x0,0x0,0x0,0xbfd8bf90,0x3f403f00,0x0,0x0,0x0,0x0,
  0x0,0x0,0x0,0x0,0x0,0x0,0x0,0x0,0xbfd83f90,0x3f40bf00,0x0,0x0,0x0,0x0,0x0,0x0,
  0x0,0x0,0x0,0x0,0x0,0x0,0x0,0x0,0x0,0x0,0xbfd83f90,0x3f40bf00,0x0,0x0,0x0,0x0,
  0x0,0x0,0x0,0x0,0x0,0x0,0x0,0x0,0x0,0x0,0x0,0x0,0xbfd8bf90,0x3f403f00,0x0,0x0,
  0x0,0x0,0x0,0x0,0x0,0x0,0x0,0x0,0x0,0x0,0x0,0x0,0x0,0x0,0xbfd8bf90,0x3f403f00,
  0x0,0x0,0x0,0x0,0x0,0x0,0x0,0x0,0x0,0x0,0x0,0x0,0xbfd83f90,0x3f40bf00,0x0,0x0,
  0x0,0x0,0x0,0x0,0x0,0x0,0x0,0x0,0x0,0x0,0x0,0x0,0x0,0x0,0xbfd83f90,0x3f40bf00,
  0x0,0x0,0x0,0x0,0x0,0x0,0x0,0x0,0x0,0x0,0x0,0x0,0x0,0x0,0x0,0x0,
  0x0,0x0,0x0,0x0,0x0,0x0,0x0,0x0,0x0,0x0,0x0,0x0,0x0,0x0,0x0,0x0,
  0x0,0x0,0x0,0x0,0x0,0x0,0x0,0x0,0x0,0x0,0x0,0x0,0x0,0x0,0x0,0x0,
  0x0,0x0,0x0,0x0,0x0,0x0,0x0,0x0,0x0,0x0,0x0,0x0,0x0,0x0,0x0,0x0,
  0x0,0x0,0x0,0x0,0x0,0x0,0x0,0x0,0x0,0x0,0x0,0x0,0x0,0x0,0x0,0x0,
  0x0,0x0,0x0,0x0,0x0,0x0,0x0,0x0,0x0,0x0,0x0,0x0,0x0,0x0,0x0,0x0,
  0x0,0x0,0x0,0x0,0x0,0x0,0x0,0x0,0x0,0x0,0x0,0x0,0x0,0x0,0x0,0x0,
  0x0,0x0,0x0,0x0,0x0,0x0,0x0,0x0,0x0,0x0,0x0,0x0,0x0,0x0,0x0,0x0,
  0x0,0x0,0x0,0x0,0x0,0x0,0x0,0x0,0x0,0x0,0x0,0x0,0x0,0x0,0x0,0x0,
  0x0,0x0,0x0,0x0,0x0,0x0,0x0,0x0,0x0,0x0,0x0,0x0,0x0,0x0,0x0,0x0,
  0x0,0x0,0x0,0x0,0x0,0x0,0x0,0x0,0x0,0x0,0x0,0x0,0x0,0x0,0x0,0x0,
  0x0,0x0,0x0,0x0,0x0,0x0,0x0,0x0,0x0,0x0,0x0,0x0,0x0,0x0,0x0,0x0,
  0x0,0x0,0x0,0x0,0x0,0x0,0x0,0x0,0x0,0x0,0x0,0x0,0x0,0x0,0x0,0x0,
  0x0,0x0,0x0,0x0,0x0,0x0,0x0,0x0,0x0,0x0,0x0,0x0,0x0,0x0,0x0,0x0,
  0x0,0x0,0x0,0x0,0x0,0x0,0x0,0x0,0x0,0x0,0x0,0x0,0x0,0x0,0x0,0x0,
  0x0,0x0,0x0,0x0,0x0,0x0,0x0,0x0,0x0,0x0,0x0,0x0,0x0,0x0,0x0,0x0,
  0x10000000,0x180000,0x440,0x860,0x10000000,0x180010,0x4d0,0x8f0,0x10000000,0x180020,0x560,0x940,0x10000000,0x180030,0x5f0,0x9d0,
  0x201b3180,0x180040,0x600,0xa20,0x201b3190,0x180050,0x690,0xab0,0x201b31a0,0x180060,0x720,0xb00,0x201b31b0,0x180070,0x7b0,0xb90,
  0x401b0000,0x180610,0x1040,0x1460,0x401b0010,0x180600,0x10d0,0x14f0,0x401b0020,0x180630,0x1160,0x1540,0x401b0030,0x180620,0x11f0,0x15d0,
  0x201e3180,0x180650,0x1200,0x1620,0x201e3190,0x180640,0x1290,0x16b0,0x201e31a0,0x180670,0x1320,0x1700,0x201e31b0,0x180660,0x13b0,0x1790,
  0x401e0020,0x180c20,0x450,0x868,0x401e0030,0x180c30,0x4c0,0x8f8,0x401e0000,0x180c00,0x570,0x948,0x401e0010,0x180c10,0x5e0,0x9d8,
  0x202131a0,0x180c60,0x610,0xa28,0x202131b0,0x180c70,0x680,0xab8,0x20213180,0x180c40,0x730,0xb08,0x20213190,0x180c50,0x7a0,0xb98,
  0x40210020,0x181230,0x1050,0x1468,0x40210030,0x181220,0x10c0,0x14f8,0x40210000,0x181210,0x1170,0x1548,0x40210010,0x181200,0x11e0,0x15d8,
  0x10000000,0x181270,0x1210,0x1628,0x10000000,0x181260,0x1280,0x16b8,0x10000000,0x181250,0x1330,0x1708,0x10000000,0x181240,0x13a0,0x1798,
  0x10000000,0x181840,0x460,0x870,0x10000000,0x181850,0x4f0,0x8e0,0x10000000,0x181860,0x540,0x950,0x10000000,0x181870,0x5d0,0x9c0,
  0x201b31c0,0x181800,0x620,0xa30,0x201b31d0,0x181810,0x6b0,0xaa0,0x201b31e0,0x181820,0x700,0xb10,0x201b31f0,0x181830,0x790,0xb80,
  0x401b0040,0x181e50,0x1060,0x1470,0x401b0050,0x181e40,0x10f0,0x14e0,0x401b0060,0x181e70,0x1140,0x1550,0x401b0070,0x181e60,0x11d0,0x15c0,
  0x201e31c0,0x181e10,0x1220,0x1630,0x201e31d0,0x181e00,0x12b0,0x16a0,0x201e31e0,0x181e30,0x1300,0x1710,0x201e31f0,0x181e20,0x1390,0x1780,
  0x401e0060,0x182460,0x470,0x878,0x401e0070,0x182470,0x4e0,0x8e8,0x401e0040,0x182440,0x550,0x958,0x401e0050,0x182450,0x5c0,0x9c8,
  0x202131e0,0x182420,0x630,0xa38,0x202131f0,0x182430,0x6a0,0xaa8,0x202131c0,0x182400,0x710,0xb18,0x202131d0,0x182410,0x780,0xb88,
  0x40210060,0x182a70,0x1070,0x1478,0x40210070,0x182a60,0x10e0,0x14e8,0x40210040,0x182a50,0x1150,0x1558,0x40210050,0x182a40,0x11c0,0x15c8,
  0x10000000,0x182a30,0x1230,0x1638,0x10000000,0x182a20,0x12a0,0x16a8,0x10000000,0x182a10,0x1310,0x1718,0x10000000,0x182a00,0x1380,0x1788,
  0xbfd8bf90,0x3f403f00,0x0,0x0,0x0,0x0,0x0,0x0,0x0,0x0,0x0,0x0,0x0,0x0,0x0,0x0,
  0x0,0x0,0xbfd8bf90,0x3f403f00,0x0,0x0,0x0,0x0,0x0,0x0,0x0,0x0,0x0,0x0,0x0,0x0,
  0xbfd83f90,0x3f40bf00,0x0,0x0,0x0,0x0,0x0,0x0,0x0,0x0,0x0,0x0,0x0,0x0,0x0,0x0,
  0x0,0x0,0xbfd83f90,0x3f40bf00,0x0,0x0,0x0,0x0,0x0,0x0,0x0,0x0,0x0,0x0,0x0,0x0,
  0x0,0x0,0x0,0x0,0xbfd8bf90,0x3f403f00,0x0,0x0,0x0,0x0,0x0,0x0,0x0,0x0,0x0,0x0,
  0x0,0x0,0x0,0x0,0x0,0x0,0xbfd8bf90,0x3f403f00,0x0,0x0,0x0,0x0,0x0,0x0,0x0,0x0,
  0x0,0x0,0x0,0x0,0xbfd83f90,0x3f40bf00,0x0,0x0,0x0,0x0,0x0,0x0,0x0,0x0,0x0,0x0,
  0x0,0x0,0x0,0x0,0x0,0x0,0xbfd83f90,0x3f40bf00,0x0,0x0,0x0,0x0,0x0,0x0,0x0,0x0,
  0x0,0x0,0x0,0x0,0x0,0x0,0x0,0x0,0xbfd8bf90,0x3f403f00,0x0,0x0,0x0,0x0,0x0,0x0,
  0x0,0x0,0x0,0x0,0x0,0x0,0x0,0x0,0x0,0x0,0xbfd8bf90,0x3f403f00,0x0,0x0,0x0,0x0,
  0x0,0x0,0x0,0x0,0x0,0x0,0x0,0x0,0xbfd83f90,0x3f40bf00,0x0,0x0,0x0,0x0,0x0,0x0,
  0x0,0x0,0x0,0x0,0x0,0x0,0x0,0x0,0x0,0x0,0xbfd83f90,0x3f40bf00,0x0,0x0,0x0,0x0,
  0x0,0x0,0x0,0x0,0x0,0x0,0x0,0x0,0x0,0x0,0x0,0x0,0xbfd8bf90,0x3f403f00,0x0,0x0,
  0x0,0x0,0x0,0x0,0x0,0x0,0x0,0x0,0x0,0x0,0x0,0x0,0x0,0x0,0xbfd8bf90,0x3f403f00,
  0x0,0x0,0x0,0x0,0x0,0x0,0x0,0x0,0x0,0x0,0x0,0x0,0xbfd83f90,0x3f40bf00,0x0,0x0,
  0x0,0x0,0x0,0x0,0x0,0x0,0x0,0x0,0x0,0x0,0x0,0x0,0x0,0x0,0xbfd83f90,0x3f40bf00,
};

__global__ __launch_bounds__(256, 1) void wino_main_kernel(const char* __restrict__ x, char* __restrict__ Uq, char* __restrict__ out, const char* __restrict__ wt) {
  __shared__ __attribute__((aligned(1024))) char smem[163840];
  asm volatile("s_getpc_b64 s[40:41]\n\tLwino_pc:\n\ts_bfe_u32 s42, %0, 0x50003\n\ts_lshl_b32 s42, s42, 12\n\tv_lshl_add_u32 v203, %1, 4, s42\n\t"
               "v_min_u32 v203, (Lwino_end-Lwino_pc-16)&-16, v203\n\tglobal_load_dwordx4 v[196:199], v203, s[40:41]"
               :: "s"(blockIdx.x), "v"(threadIdx.x) : "s40", "s41", "s42", "v196", "v197", "v198", "v199", "v203", "scc", "memory");
  const int lane = threadIdx.x & 63;
  const int w = __builtin_amdgcn_readfirstlane(threadIdx.x >> 6);
  const int sub = blockIdx.x >> 3, bx = sub & 15, by = 4 * (blockIdx.x & 7) + (sub >> 4);
  const unsigned long long px = (unsigned long long)x, pu = (unsigned long long)Uq, py = (unsigned long long)out, pw = (unsigned long long)wt;
  const int xbase = ((16 * by - 1) * IMG_W + 32 * bx) * PXB;
  const int ybase = (16 * by * IMG_W + 32 * bx) * PXB;
  const unsigned medge = (bx == 0 ? 0xffffffffu : 0xbfffffffu) & (bx == 15 ? 0xffffffffu : 0xdfffffffu);
  unsigned fso, fuo, fk0, fk5, fs8;
  {
    const unsigned p = blockIdx.x - 1, hb = p & 1, q = (p >> 1) & 127, r = q / 3, kb = q - 3 * r, wv = r & 3, chunk = r >> 2;
    fs8 = 1 - (kb >> 1);
    fso = p < 144 ? chunk * 12288 + hb * 3072 + (w >> 1) * 1536 + (kb < 2 ? kb * 8 : 256) + ((2 * wv + (w & 1)) << (4 + fs8)) : 0x40000000u;
    fuo = p < 144 ? (chunk * 18 + kb) * 12288 + wv * 3072 + hb * 512 + w * 128 : 0x40000000u;
    fk0 = chunk < 2 ? 0x3ea80a81u : 0x3f638e39u; fk5 = chunk < 2 ? 0xbebd0bd1u : 0x3f638e39u;
  }
  const unsigned fwo = (((lane & 3) + 4 * (lane & 4)) * CH + 4 * (lane >> 5) + ((lane >> 3) & 3)) * 2;
  const unsigned flw = 18432 + w * 3072 + lane * 2, fst = lane < 48 ? (lane >> 3) * 36864 + (lane & 7) * 16 : 0x40000000u;
  unsigned dummy = 0;
  asm volatile(
"s_mov_b32 s52, %15\n\t"
"s_and_b32 s53, %16, 0xffff\n\t"
"s_mov_b32 s54, 663552\n\t"
"s_mov_b32 s55, 0x20000\n\t"
"s_mov_b32 s56, %17\n\t"
"s_mov_b32 s57, %18\n\t"
"s_mov_b32 s58, %19\n\t"
"s_mov_b32 s59, %20\n\t"
"s_mov_b32 s74, %21\n\t"
"s_mul_i32 s66, %11, 4\n\t"
"s_mov_b32 s73, %9\n\t"
"s_or_b32 s72, %11, %9\n\t"
"v_mov_b32 v32, 0\n\t"
"v_mov_b32 v33, 0\n\t"
"v_mov_b32 v34, 0\n\t"
"v_mov_b32 v35, 0\n\t"
"v_mov_b32 v36, 0\n\t"
"v_mov_b32 v37, 0\n\t"
"v_mov_b32 v38, 0\n\t"
"v_mov_b32 v39, 0\n\t"
"v_mov_b32 v40, 0\n\t"
"v_mov_b32 v118, %22\n\t"
"v_mov_b32 v42, %23\n\t"
"v_add_u32 v43, 18432, v252\n\t"
"v_mov_b32 v44, %24\n\t"
"v_mov_b32 v119, %25\n\t"
"s_mov_b32 s8, %1\n\t"
"s_and_b32 s9, %2, 0xffff\n\t"
"s_mov_b32 s10, 0x200000\n\t"
"s_mov_b32 s11, 0x20000\n\t"
"s_mov_b32 s12, %3\n\t"
"s_and_b32 s13, %4, 0xffff\n\t"
"s_mov_b32 s14, 100663296\n\t"
"s_mov_b32 s15, 0x20000\n\t"
"s_mov_b32 s16, %5\n\t"
"s_and_b32 s17, %6, 0xffff\n\t"
"s_mov_b32 s18, 100663296\n\t"
"s_mov_b32 s19, 0x20000\n\t"
"s_mov_b32 s21, %7\n\t"
"s_lshr_b32 s60, %9, 1\n\t"
"s_mul_i32 s61, s60, 384\n\t"
"s_add_u32 s22, %8, s61\n\t"
"s_mul_i32 s61, s60, 196608\n\t"
"s_add_u32 s23, %8, s61\n\t"
"s_mul_i32 s24, %9, 1536\n\t"
"s_add_u32 s24, s24, 43008\n\t"
"s_mul_i32 s25, %9, 19968\n\t"
"s_add_u32 s25, s25, 61440\n\t"
"s_mul_i32 s26, s60, 19968\n\t"
"s_add_u32 s26, s26, 141312\n\t"
"s_sub_u32 s61, s60, %9\n\t"
"s_add_u32 s61, s61, 4\n\t"
"s_mul_i32 s27, s61, 589824\n\t"
"s_bitcmp1_b32 %9, 0\n\t"
"s_mov_b32 s36, 0x0fff0000\n\t"
"s_cselect_b32 s36, 0xfff0, s36\n\t"
"s_mov_b32 s37, s36\n\t"
"s_mov_b32 s44, 0x0fff0fff\n\t"
"s_mov_b32 s45, 0x0fff0fff\n\t"
"s_mov_b64 s[46:47], %12\n\t"
"s_add_u32 s48, s46, 12288\n\t"
"s_addc_u32 s49, s47, 0\n\t"
"s_mul_i32 s50, %9, 122880\n\t"
"s_lshl_b32 s51, s60, 30\n\t"
"s_add_u32 s51, s51, s50\n\t"
"s_mov_b32 s40, %14\n\t"
"s_sub_u32 s42, %7, 384\n\t"
"s_mov_b32 s38, 0x3f2aaaab\n\t"
"s_mov_b32 s39, 0x3fc00000\n\t"
"s_mov_b32 s41, 0x3ee38e39\n\t"
"s_mov_b32 s43, 0x40100000\n\t"
"s_nop 4\n\t"
"s_branch Lpro_%=\n\t"
"Lfb_%=:\n\t"
"s_mov_b32 s69, 0\n\t"
"Lfb1_%=:\n\t"
"s_lshr_b32 s71, s69, 2\n\t"
"s_and_b32 s70, s69, 3\n\t"
"s_and_b32 s60, s71, 1\n\t"
"s_lshr_b32 s61, s71, 1\n\t"
"s_mul_i32 s62, s61, 171\n\t"
"s_lshr_b32 s62, s62, 9\n\t"
"s_mul_i32 s63, s62, 3\n\t"
"s_sub_u32 s61, s61, s63\n\t"
"s_and_b32 s63, s62, 3\n\t"
"s_lshr_b32 s62, s62, 2\n\t"
"s_mul_i32 s56, s62, 12288\n\t"
"s_mul_i32 s64, s60, 3072\n\t"
"s_add_u32 s56, s56, s64\n\t"
"s_lshr_b32 s64, s70, 1\n\t"
"s_mul_i32 s64, s64, 1536\n\t"
"s_add_u32 s56, s56, s64\n\t"
"s_and_b32 s64, s70, 1\n\t"
"s_lshl1_add_u32 s64, s63, s64\n\t"
"s_lshl_b32 s65, s61, 3\n\t"
"s_cmp_lt_u32 s61, 2\n\t"
"s_cselect_b32 s74, 1, 0\n\t"
"s_cselect_b32 s65, s65, 0x100\n\t"
"s_add_u32 s56, s56, s65\n\t"
"s_add_u32 s65, s74, 4\n\t"
"s_lshl_b32 s64, s64, s65\n\t"
"s_add_u32 s56, s56, s64\n\t"
"s_mul_i32 s57, s62, 18\n\t"
"s_add_u32 s57, s57, s61\n\t"
"s_mul_i32 s57, s57, 12288\n\t"
"s_mul_i32 s64, s63, 3072\n\t"
"s_add_u32 s57, s57, s64\n\t"
"s_lshl_b32 s64, s60, 9\n\t"
"s_add_u32 s57, s57, s64\n\t"
"s_lshl_b32 s64, s70, 7\n\t"
"s_add_u32 s57, s57, s64\n\t"
"s_mov_b32 s64, 0x3f638e39\n\t"
"s_cmp_lt_u32 s62, 2\n\t"
"s_cselect_b32 s58, 0x3ea80a81, s64\n\t"
"s_cselect_b32 s59, 0xbebd0bd1, s64\n\t"
"v_mad_u32_u24 v41, v119, s74, v118\n\t"
"s_add_u32 s60, s56, 0\n\t"
"buffer_load_short_d16_hi v32, v41, s[52:55], s60 offen\n\t"
"s_add_u32 s60, s56, 73728\n\t"
"buffer_load_short_d16_hi v33, v41, s[52:55], s60 offen\n\t"
"s_add_u32 s60, s56, 147456\n\t"
"buffer_load_short_d16_hi v34, v41, s[52:55], s60 offen\n\t"
"s_add_u32 s60, s56, 221184\n\t"
"buffer_load_short_d16_hi v35, v41, s[52:55], s60 offen\n\t"
"s_add_u32 s60, s56, 294912\n\t"
"buffer_load_short_d16_hi v36, v41, s[52:55], s60 offen\n\t"
"s_add_u32 s60, s56, 368640\n\t"
"buffer_load_short_d16_hi v37, v41, s[52:55], s60 offen\n\t"
"s_add_u32 s60, s56, 442368\n\t"
"buffer_load_short_d16_hi v38, v41, s[52:55], s60 offen\n\t"
"s_add_u32 s60, s56, 516096\n\t"
"buffer_load_short_d16_hi v39, v41, s[52:55], s60 offen\n\t"
"s_add_u32 s60, s56, 589824\n\t"
"buffer_load_short_d16_hi v40, v41, s[52:55], s60 offen\n\t"
"s_waitcnt vmcnt(6)\n\t"
"v_fma_f32 v48, v34, s41, v32\n\t"
"v_fma_f32 v51, v34, s43, v32\n\t"
"s_waitcnt vmcnt(3)\n\t"
"v_fma_f32 v49, v37, s41, v35\n\t"
"v_fma_f32 v52, v37, s43, v35\n\t"
"s_waitcnt vmcnt(0)\n\t"
"v_fma_f32 v50, v40, s41, v38\n\t"
"v_fma_f32 v53, v40, s43, v38\n\t"
"v_fma_f32 v54, v33, s38, v48\n\t"
"v_fma_f32 v57, -v33, s38, v48\n\t"
"v_fma_f32 v60, v33, s39, v51\n\t"
"v_fma_f32 v63, -v33, s39, v51\n\t"
"v_fma_f32 v55, v36, s38, v49\n\t"
"v_fma_f32 v58, -v36, s38, v49\n\t"
"v_fma_f32 v61, v36, s39, v52\n\t"
"v_fma_f32 v64, -v36, s39, v52\n\t"
"v_fma_f32 v56, v39, s38, v50\n\t"
"v_fma_f32 v59, -v39, s38, v50\n\t"
"v_fma_f32 v62, v39, s39, v53\n\t"
"v_fma_f32 v65, -v39, s39, v53\n\t"
"v_mul_f32 v70, s58, v32\n\t"
"v_mul_f32 v71, s58, v35\n\t"
"v_mul_f32 v72, s58, v38\n\t"
"v_mul_f32 v73, 0xbf54ad4b, v54\n\t"
"v_mul_f32 v74, 0xbf54ad4b, v55\n\t"
"v_mul_f32 v75, 0xbf54ad4b, v56\n\t"
"v_mul_f32 v76, 0xbefc0fc1, v57\n\t"
"v_mul_f32 v77, 0xbefc0fc1, v58\n\t"
"v_mul_f32 v78, 0xbefc0fc1, v59\n\t"
"v_mul_f32 v79, 0xbe7c0fc1, v60\n\t"
"v_mul_f32 v80, 0xbe7c0fc1, v61\n\t"
"v_mul_f32 v81, 0xbe7c0fc1, v62\n\t"
"v_mul_f32 v82, 0x3ea80a81, v63\n\t"
"v_mul_f32 v83, 0x3ea80a81, v64\n\t"
"v_mul_f32 v84, 0x3ea80a81, v65\n\t"
"v_mul_f32 v85, s59, v34\n\t"
"v_mul_f32 v86, s59, v37\n\t"
"v_mul_f32 v87, s59, v40\n\t"
"v_cvt_pk_bf16_f32 v88, v70, v71\n\t"
"v_cvt_pk_bf16_f32 v89, v72, v73\n\t"
"v_cvt_pk_bf16_f32 v90, v74, v75\n\t"
"v_cvt_pk_bf16_f32 v91, v76, v77\n\t"
"v_cvt_pk_bf16_f32 v92, v78, v79\n\t"
"v_cvt_pk_bf16_f32 v93, v80, v81\n\t"
"v_cvt_pk_bf16_f32 v94, v82, v83\n\t"
"v_cvt_pk_bf16_f32 v95, v84, v85\n\t"
"v_cvt_pk_bf16_f32 v96, v86, v87\n\t"
"ds_write_b16 v42, v88 offset:0\n\t"
"ds_write_b16_d16_hi v42, v88 offset:1024\n\t"
"ds_write_b16 v42, v89 offset:2048\n\t"
"ds_write_b16_d16_hi v42, v89 offset:128\n\t"
"ds_write_b16 v42, v90 offset:1152\n\t"
"ds_write_b16_d16_hi v42, v90 offset:2176\n\t"
"ds_write_b16 v45, v91 offset:256\n\t"
"ds_write_b16_d16_hi v45, v91 offset:1280\n\t"
"ds_write_b16 v45, v92 offset:2304\n\t"
"ds_write_b16_d16_hi v42, v92 offset:384\n\t"
"ds_write_b16 v42, v93 offset:1408\n\t"
"ds_write_b16_d16_hi v42, v93 offset:2432\n\t"
"ds_write_b16 v45, v94 offset:512\n\t"
"ds_write_b16_d16_hi v45, v94 offset:1536\n\t"
"ds_write_b16 v45, v95 offset:2560\n\t"
"ds_write_b16_d16_hi v42, v95 offset:640\n\t"
"ds_write_b16 v42, v96 offset:1664\n\t"
"ds_write_b16_d16_hi v42, v96 offset:2688\n\t"
"ds_read_b128 v[48:51], v43 offset:0\n\t"
"ds_read_b128 v[52:55], v43 offset:1024\n\t"
"ds_read_b128 v[56:59], v43 offset:2048\n\t"
"s_waitcnt lgkmcnt(2)\n\t"
"buffer_store_dwordx4 v[48:51], v44, s[8:11], s57 offen offset:0 sc1\n\t"
"s_waitcnt lgkmcnt(1)\n\t"
"buffer_store_dwordx4 v[52:55], v44, s[8:11], s57 offen offset:1024 sc1\n\t"
"s_waitcnt lgkmcnt(0)\n\t"
"buffer_store_dwordx4 v[56:59], v44, s[8:11], s57 offen offset:2048 sc1\n\t"
"s_waitcnt vmcnt(0) lgkmcnt(0)\n\t"
"s_add_u32 s69, s69, 1\n\t"
"s_cmp_lt_u32 s69, 576\n\t"
"s_cbranch_scc1 Lfb1_%=\n\t"
"s_branch Lgo_%=\n\t"
"Lpro_%=:\n\t"
"v_mad_u32_u24 v41, v119, s74, v118\n\t"
"s_add_u32 s60, s56, 0\n\t"
"buffer_load_short_d16_hi v32, v41, s[52:55], s60 offen\n\t"
"s_add_u32 s60, s56, 73728\n\t"
"buffer_load_short_d16_hi v33, v41, s[52:55], s60 offen\n\t"
"s_add_u32 s60, s56, 147456\n\t"
"buffer_load_short_d16_hi v34, v41, s[52:55], s60 offen\n\t"
"s_add_u32 s60, s56, 221184\n\t"
"buffer_load_short_d16_hi v35, v41, s[52:55], s60 offen\n\t"
"s_add_u32 s60, s56, 294912\n\t"
"buffer_load_short_d16_hi v36, v41, s[52:55], s60 offen\n\t"
"s_add_u32 s60, s56, 368640\n\t"
"buffer_load_short_d16_hi v37, v41, s[52:55], s60 offen\n\t"
"s_add_u32 s60, s56, 442368\n\t"
"buffer_load_short_d16_hi v38, v41, s[52:55], s60 offen\n\t"
"s_add_u32 s60, s56, 516096\n\t"
"buffer_load_short_d16_hi v39, v41, s[52:55], s60 offen\n\t"
"s_add_u32 s60, s56, 589824\n\t"
"buffer_load_short_d16_hi v40, v41, s[52:55], s60 offen\n\t"
"s_add_u32 s60, s66, 1867776\n\t"
"buffer_load_dword v46, off, s[8:11], s60 sc1\n\t"
"s_mul_i32 s70, s66, 1\n\t"
"s_add_u32 s70, s70, 1900544\n\t"
"s_mov_b32 s60, s70\n\t"
"buffer_load_dword v47, off, s[8:11], s60 sc1\n\t"
"s_cmp_lg_u32 s72, 0\n\t"
"s_cbranch_scc1 Lnl1_%=\n\t"
"v_mul_u32_u24 v116, 1, v252\n\t"
"v_lshrrev_b32 v116, 2, v116\n\t"
"v_mul_u32_u24 v117, 1, v116\n\t"
"s_mov_b32 s60, 1900544\n\t"
"buffer_load_dword v104, v117, s[8:11], s60 offen sc1\n\t"
"s_mov_b32 s60, 1900800\n\t"
"buffer_load_dword v105, v117, s[8:11], s60 offen sc1\n\t"
"s_mov_b32 s60, 1901056\n\t"
"buffer_load_dword v106, v117, s[8:11], s60 offen sc1\n\t"
"s_mov_b32 s60, 1901312\n\t"
"buffer_load_dword v107, v117, s[8:11], s60 offen sc1\n\t"
"s_waitcnt vmcnt(0)\n\t"
"v_add_u32 v104, 0x10000, v104\n\t"
"v_add_u32 v105, 0x10000, v105\n\t"
"v_add_u32 v106, 0x10000, v106\n\t"
"v_add_u32 v107, 0x10000, v107\n\t"
"v_and_b32 v104, 0xffff0000, v104\n\t"
"v_and_b32 v105, 0xffff0000, v105\n\t"
"v_and_b32 v106, 0xffff0000, v106\n\t"
"v_and_b32 v107, 0xffff0000, v107\n\t"
"s_mov_b32 s60, 1835008\n\t"
"buffer_store_dword v104, v116, s[8:11], s60 offen sc1\n\t"
"s_mov_b32 s60, 1835264\n\t"
"buffer_store_dword v105, v116, s[8:11], s60 offen sc1\n\t"
"s_mov_b32 s60, 1835520\n\t"
"buffer_store_dword v106, v116, s[8:11], s60 offen sc1\n\t"
"s_mov_b32 s60, 1835776\n\t"
"buffer_store_dword v107, v116, s[8:11], s60 offen sc1\n\t"
"Lnl1_%=:\n\t"
"global_load_dwordx4 v[242:245], v252, s[46:47] offset:2048\n\t"
"global_load_dwordx4 v[246:249], v252, s[48:49] offset:0\n\t"
"global_load_dwordx4 v[238:241], v252, s[46:47] offset:0\n\t"
"s_waitcnt vmcnt(14)\n\t"
"global_load_dwordx4 v[194:197], v252, s[46:47] offset:1024\n\t"
"global_load_dwordx4 v[218:221], v252, s[48:49] offset:1024\n\t"
"v_xor_b32 v45, 8, v42\n\t"
"s_waitcnt vmcnt(13)\n\t"
"v_fma_f32 v48, v34, s41, v32\n\t"
"v_fma_f32 v51, v34, s43, v32\n\t"
"s_waitcnt vmcnt(10)\n\t"
"v_fma_f32 v49, v37, s41, v35\n\t"
"v_fma_f32 v52, v37, s43, v35\n\t"
"s_waitcnt vmcnt(7)\n\t"
"v_fma_f32 v50, v40, s41, v38\n\t"
"v_fma_f32 v53, v40, s43, v38\n\t"
"v_fma_f32 v54, v33, s38, v48\n\t"
"v_fma_f32 v57, -v33, s38, v48\n\t"
"v_fma_f32 v60, v33, s39, v51\n\t"
"v_fma_f32 v63, -v33, s39, v51\n\t"
"v_fma_f32 v55, v36, s38, v49\n\t"
"v_fma_f32 v58, -v36, s38, v49\n\t"
"v_fma_f32 v61, v36, s39, v52\n\t"
"v_fma_f32 v64, -v36, s39, v52\n\t"
"v_fma_f32 v56, v39, s38, v50\n\t"
"v_fma_f32 v59, -v39, s38, v50\n\t"
"v_fma_f32 v62, v39, s39, v53\n\t"
"v_fma_f32 v65, -v39, s39, v53\n\t"
"v_mul_f32 v70, s58, v32\n\t"
"v_mul_f32 v71, s58, v35\n\t"
"v_mul_f32 v72, s58, v38\n\t"
"v_mul_f32 v73, 0xbf54ad4b, v54\n\t"
"v_mul_f32 v74, 0xbf54ad4b, v55\n\t"
"v_mul_f32 v75, 0xbf54ad4b, v56\n\t"
"v_mul_f32 v76, 0xbefc0fc1, v57\n\t"
"v_mul_f32 v77, 0xbefc0fc1, v58\n\t"
"v_mul_f32 v78, 0xbefc0fc1, v59\n\t"
"v_mul_f32 v79, 0xbe7c0fc1, v60\n\t"
"v_mul_f32 v80, 0xbe7c0fc1, v61\n\t"
"v_mul_f32 v81, 0xbe7c0fc1, v62\n\t"
"v_mul_f32 v82, 0x3ea80a81, v63\n\t"
"v_mul_f32 v83, 0x3ea80a81, v64\n\t"
"v_mul_f32 v84, 0x3ea80a81, v65\n\t"
"v_mul_f32 v85, s59, v34\n\t"
"v_mul_f32 v86, s59, v37\n\t"
"v_mul_f32 v87, s59, v40\n\t"
"v_cvt_pk_bf16_f32 v88, v70, v71\n\t"
"v_cvt_pk_bf16_f32 v89, v72, v73\n\t"
"v_cvt_pk_bf16_f32 v90, v74, v75\n\t"
"v_cvt_pk_bf16_f32 v91, v76, v77\n\t"
"v_cvt_pk_bf16_f32 v92, v78, v79\n\t"
"v_cvt_pk_bf16_f32 v93, v80, v81\n\t"
"v_cvt_pk_bf16_f32 v94, v82, v83\n\t"
"v_cvt_pk_bf16_f32 v95, v84, v85\n\t"
"v_cvt_pk_bf16_f32 v96, v86, v87\n\t"
"ds_write_b16 v42, v88 offset:0\n\t"
"ds_write_b16_d16_hi v42, v88 offset:1024\n\t"
"ds_write_b16 v42, v89 offset:2048\n\t"
"ds_write_b16_d16_hi v42, v89 offset:128\n\t"
"ds_write_b16 v42, v90 offset:1152\n\t"
"ds_write_b16_d16_hi v42, v90 offset:2176\n\t"
"ds_write_b16 v45, v91 offset:256\n\t"
"ds_write_b16_d16_hi v45, v91 offset:1280\n\t"
"ds_write_b16 v45, v92 offset:2304\n\t"
"ds_write_b16_d16_hi v42, v92 offset:384\n\t"
"ds_write_b16 v42, v93 offset:1408\n\t"
"ds_write_b16_d16_hi v42, v93 offset:2432\n\t"
"ds_write_b16 v45, v94 offset:512\n\t"
"ds_write_b16_d16_hi v45, v94 offset:1536\n\t"
"ds_write_b16 v45, v95 offset:2560\n\t"
"ds_write_b16_d16_hi v42, v95 offset:640\n\t"
"ds_write_b16 v42, v96 offset:1664\n\t"
"ds_write_b16_d16_hi v42, v96 offset:2688\n\t"
"ds_read_b128 v[48:51], v43 offset:0\n\t"
"ds_read_b128 v[52:55], v43 offset:1024\n\t"
"ds_read_b128 v[56:59], v43 offset:2048\n\t"
"s_waitcnt lgkmcnt(2)\n\t"
"buffer_store_dwordx4 v[48:51], v44, s[8:11], s57 offen offset:0 sc1\n\t"
"s_waitcnt lgkmcnt(1)\n\t"
"buffer_store_dwordx4 v[52:55], v44, s[8:11], s57 offen offset:1024 sc1\n\t"
"s_waitcnt lgkmcnt(0)\n\t"
"buffer_store_dwordx4 v[56:59], v44, s[8:11], s57 offen offset:2048 sc1\n\t"
"s_waitcnt vmcnt(6)\n\t"
"v_and_b32 v246, s40, v246\n\t"
"v_add_u32 v246, s42, v246\n\t"
"s_waitcnt vmcnt(0)\n\t"
"s_barrier\n\t"
"v_readfirstlane_b32 s67, v46\n\t"
"v_readfirstlane_b32 s71, v47\n\t"
"s_add_u32 s67, s67, 1\n\t"
"s_and_b32 s67, s67, 0xffff\n\t"
"s_add_u32 s71, s71, 0x10000\n\t"
"s_and_b32 s71, s71, 0xffff0000\n\t"
"s_or_b32 s71, s71, s67\n\t"
"s_mov_b32 s68, 3000\n\t"
"s_cmp_lg_u32 s73, 0\n\t"
"s_cbranch_scc1 Lnl2_%=\n\t"
"v_mov_b32 v47, s71\n\t"
"s_mov_b64 exec, 1\n\t"
"buffer_store_dword v47, off, s[8:11], s70 sc1\n\t"
"s_mov_b64 exec, -1\n\t"
"Lnl2_%=:\n\t"
"s_add_u32 m0, s25, 0\n\t"
"s_nop 0\n\t"
"buffer_load_dwordx4 v246, s[12:15], 0 offen offset:0 nt lds\n\t"
"s_add_u32 m0, s26, 0\n\t"
"s_mov_b64 exec, s[36:37]\n\t"
"buffer_load_dwordx4 v246, s[12:15], s27 offen offset:0 nt lds\n\t"
"s_mov_b64 exec, -1\n\t"
"s_add_u32 m0, s24, 0\n\t"
"s_add_u32 s28, s21, 0\n\t"
"buffer_load_dwordx4 v245, s[12:15], s28 offen offset:0 nt lds\n\t"
"s_add_u32 m0, s24, 6144\n\t"
"s_add_u32 s28, s21, 196608\n\t"
"buffer_load_dwordx4 v245, s[12:15], s28 offen offset:0 nt lds\n\t"
"s_add_u32 m0, s24, 12288\n\t"
"s_add_u32 s28, s21, 393216\n\t"
"buffer_load_dwordx4 v245, s[12:15], s28 offen offset:0 nt lds\n\t"
"s_add_u32 m0, s24, 19968\n\t"
"s_add_u32 s28, s21, 589824\n\t"
"buffer_load_dwordx4 v245, s[12:15], s28 offen offset:0 nt lds\n\t"
"s_add_u32 m0, s24, 26112\n\t"
"s_add_u32 s28, s21, 786432\n\t"
"buffer_load_dwordx4 v245, s[12:15], s28 offen offset:0 nt lds\n\t"
"s_add_u32 m0, s24, 32256\n\t"
"s_add_u32 s28, s21, 983040\n\t"
"buffer_load_dwordx4 v245, s[12:15], s28 offen offset:0 nt lds\n\t"
"s_add_u32 m0, s24, 39936\n\t"
"s_add_u32 s28, s21, 1179648\n\t"
"buffer_load_dwordx4 v245, s[12:15], s28 offen offset:0 nt lds\n\t"
"s_add_u32 m0, s24, 46080\n\t"
"s_add_u32 s28, s21, 1376256\n\t"
"buffer_load_dwordx4 v245, s[12:15], s28 offen offset:0 nt lds\n\t"
"s_add_u32 m0, s24, 52224\n\t"
"s_add_u32 s28, s21, 1572864\n\t"
"buffer_load_dwordx4 v245, s[12:15], s28 offen offset:0 nt lds\n\t"
"s_add_u32 m0, s24, 59904\n\t"
"s_add_u32 s28, s21, 1769472\n\t"
"buffer_load_dwordx4 v245, s[12:15], s28 offen offset:0 nt lds\n\t"
"s_cmp_lg_u32 s66, 0\n\t"
"s_cbranch_scc1 Lld4_%=\n\t"
"s_cmp_lg_u32 s73, 0\n\t"
"s_cbranch_scc1 Lld2_%=\n\t"
"Lld1_%=:\n\t"
"s_mov_b32 s60, 1900544\n\t"
"buffer_load_dword v108, v117, s[8:11], s60 offen sc1\n\t"
"s_mov_b32 s60, 1900800\n\t"
"buffer_load_dword v109, v117, s[8:11], s60 offen sc1\n\t"
"s_mov_b32 s60, 1901056\n\t"
"buffer_load_dword v110, v117, s[8:11], s60 offen sc1\n\t"
"s_mov_b32 s60, 1901312\n\t"
"buffer_load_dword v111, v117, s[8:11], s60 offen sc1\n\t"
"s_waitcnt vmcnt(0)\n\t"
"v_xor_b32 v112, v108, v104\n\t"
"v_xor_b32 v113, v109, v105\n\t"
"v_xor_b32 v114, v110, v106\n\t"
"v_xor_b32 v115, v111, v107\n\t"
"s_nop 1\n\t"
"v_writelane_b32 v112, 0, 0\n\t"
"s_nop 1\n\t"
"v_or3_b32 v112, v112, v113, v114\n\t"
"v_or_b32 v112, v112, v115\n\t"
"v_cmp_lt_u32 vcc, 0xffff, v112\n\t"
"s_nop 4\n\t"
"s_cmp_eq_u64 vcc, 0\n\t"
"s_cbranch_scc1 Lld3_%=\n\t"
"s_sub_u32 s68, s68, 1\n\t"
"s_cmp_lg_u32 s68, 0\n\t"
"s_cbranch_scc1 Lld1_%=\n\t"
"s_branch Lld2_%=\n\t"
"Lld3_%=:\n\t"
"s_mov_b32 s60, 1867776\n\t"
"buffer_store_dword v108, v116, s[8:11], s60 offen sc1\n\t"
"s_mov_b32 s60, 1868032\n\t"
"buffer_store_dword v109, v116, s[8:11], s60 offen sc1\n\t"
"s_mov_b32 s60, 1868288\n\t"
"buffer_store_dword v110, v116, s[8:11], s60 offen sc1\n\t"
"s_mov_b32 s60, 1868544\n\t"
"buffer_store_dword v111, v116, s[8:11], s60 offen sc1\n\t"
"s_mov_b32 s68, -1\n\t"
"Lld2_%=:\n\t"
"s_barrier\n\t"
"Lld4_%=:\n\t"
"s_add_u32 m0, s24, 66048\n\t"
"s_add_u32 s28, s21, 1966080\n\t"
"buffer_load_dwordx4 v245, s[12:15], s28 offen offset:0 nt lds\n\t"
"s_add_u32 m0, s24, 72192\n\t"
"s_add_u32 s28, s21, 2162688\n\t"
"buffer_load_dwordx4 v245, s[12:15], s28 offen offset:0 nt lds\n\t"
"s_add_u32 m0, s24, 79872\n\t"
"s_add_u32 s28, s21, 2359296\n\t"
"buffer_load_dwordx4 v245, s[12:15], s28 offen offset:0 nt lds\n\t"
"s_add_u32 m0, s24, 86016\n\t"
"s_add_u32 s28, s21, 2555904\n\t"
"buffer_load_dwordx4 v245, s[12:15], s28 offen offset:0 nt lds\n\t"
"s_add_u32 m0, s24, 92160\n\t"
"s_add_u32 s28, s21, 2752512\n\t"
"buffer_load_dwordx4 v245, s[12:15], s28 offen offset:0 nt lds\n\t"
"s_add_u32 m0, s24, 99840\n\t"
"s_add_u32 s28, s21, 2949120\n\t"
"buffer_load_dwordx4 v245, s[12:15], s28 offen offset:0 nt lds\n\t"
"s_add_u32 m0, s24, 105984\n\t"
"s_add_u32 s28, s21, 3145728\n\t"
"buffer_load_dwordx4 v245, s[12:15], s28 offen offset:0 nt lds\n\t"
"s_add_u32 m0, s24, 112128\n\t"
"s_add_u32 s28, s21, 3342336\n\t"
"buffer_load_dwordx4 v245, s[12:15], s28 offen offset:0 nt lds\n\t"
"s_add_u32 s60, s66, 1867776\n\t"
"buffer_load_dword v46, off, s[8:11], s60 sc1\n\t"
"s_waitcnt vmcnt(0)\n\t"
"s_barrier\n\t"
"ds_read_b64_tr_b16 v[120:121], v238 offset:0\n\t"
"ds_read_b64_tr_b16 v[122:123], v238 offset:1536\n\t"
"ds_read_b64_tr_b16 v[124:125], v239 offset:0\n\t"
"ds_read_b64_tr_b16 v[126:127], v239 offset:1536\n\t"
"ds_read_b64_tr_b16 v[128:129], v238 offset:19968\n\t"
"ds_read_b64_tr_b16 v[130:131], v238 offset:21504\n\t"
"ds_read_b64_tr_b16 v[132:133], v239 offset:19968\n\t"
"ds_read_b64_tr_b16 v[134:135], v239 offset:21504\n\t"
"ds_read_b64_tr_b16 v[136:137], v238 offset:39936\n\t"
"ds_read_b64_tr_b16 v[138:139], v238 offset:41472\n\t"
"ds_read_b64_tr_b16 v[140:141], v239 offset:39936\n\t"
"ds_read_b64_tr_b16 v[142:143], v239 offset:41472\n\t"
"v_add_u32 v238, 59904, v238\n\t"
"v_add_u32 v239, 59904, v239\n\t"
"ds_read_b64_tr_b16 v[144:145], v238 offset:0\n\t"
"ds_read_b64_tr_b16 v[146:147], v238 offset:1536\n\t"
"ds_read_b64_tr_b16 v[148:149], v239 offset:0\n\t"
"ds_read_b64_tr_b16 v[150:151], v239 offset:1536\n\t"
"ds_read_b64_tr_b16 v[152:153], v238 offset:19968\n\t"
"ds_read_b64_tr_b16 v[154:155], v238 offset:21504\n\t"
"ds_read_b64_tr_b16 v[156:157], v239 offset:19968\n\t"
"ds_read_b64_tr_b16 v[158:159], v239 offset:21504\n\t"
"ds_read_b64_tr_b16 v[160:161], v238 offset:39936\n\t"
"ds_read_b64_tr_b16 v[162:163], v238 offset:41472\n\t"
"ds_read_b64_tr_b16 v[164:165], v239 offset:39936\n\t"
"ds_read_b64_tr_b16 v[166:167], v239 offset:41472\n\t"
"s_waitcnt lgkmcnt(15)\n\t"
"v_mfma_f32_16x16x32_bf16 v[120:123], v[120:123], v[194:197], 0\n\t"
"v_mfma_f32_16x16x32_bf16 v[124:127], v[124:127], v[194:197], 0\n\t"
"v_mfma_f32_16x16x32_bf16 v[128:131], v[128:131], v[194:197], 0\n\t"
"v_mfma_f32_16x16x32_bf16 v[132:135], v[132:135], v[194:197], 0\n\t"
"s_nop 4\n\t"
"v_cvt_pk_bf16_f32 v120, v120, v121\n\t"
"v_cvt_pk_bf16_f32 v121, v122, v123\n\t"
"v_cvt_pk_bf16_f32 v122, v124, v125\n\t"
"v_cvt_pk_bf16_f32 v123, v126, v127\n\t"
"s_mov_b64 exec, s[44:45]\n\t"
"ds_write_b128 v240, v[120:123] offset:0\n\t"
"s_mov_b64 exec, -1\n\t"
"v_cvt_pk_bf16_f32 v128, v128, v129\n\t"
"v_cvt_pk_bf16_f32 v129, v130, v131\n\t"
"v_cvt_pk_bf16_f32 v130, v132, v133\n\t"
"v_cvt_pk_bf16_f32 v131, v134, v135\n\t"
"s_mov_b64 exec, s[44:45]\n\t"
"ds_write_b128 v240, v[128:131] offset:1536\n\t"
"s_mov_b64 exec, -1\n\t"
"s_waitcnt lgkmcnt(15)\n\t"
"v_mfma_f32_16x16x32_bf16 v[136:139], v[136:139], v[194:197], 0\n\t"
"s_waitcnt lgkmcnt(14)\n\t"
"v_mfma_f32_16x16x32_bf16 v[140:143], v[140:143], v[194:197], 0\n\t"
"s_waitcnt lgkmcnt(12)\n\t"
"v_mfma_f32_16x16x32_bf16 v[144:147], v[144:147], v[194:197], 0\n\t"
"s_waitcnt lgkmcnt(10)\n\t"
"v_mfma_f32_16x16x32_bf16 v[148:151], v[148:151], v[194:197], 0\n\t"
"s_nop 1\n\t"
"v_cvt_pk_bf16_f32 v136, v136, v137\n\t"
"v_cvt_pk_bf16_f32 v137, v138, v139\n\t"
"v_cvt_pk_bf16_f32 v138, v140, v141\n\t"
"v_cvt_pk_bf16_f32 v139, v142, v143\n\t"
"s_mov_b64 exec, s[44:45]\n\t"
"ds_write_b128 v240, v[136:139] offset:3072\n\t"
"s_mov_b64 exec, -1\n\t"
"v_cvt_pk_bf16_f32 v144, v144, v145\n\t"
"v_cvt_pk_bf16_f32 v145, v146, v147\n\t"
"v_cvt_pk_bf16_f32 v146, v148, v149\n\t"
"v_cvt_pk_bf16_f32 v147, v150, v151\n\t"
"s_mov_b64 exec, s[44:45]\n\t"
"ds_write_b128 v240, v[144:147] offset:4608\n\t"
"s_mov_b64 exec, -1\n\t"
"s_waitcnt lgkmcnt(10)\n\t"
"v_mfma_f32_16x16x32_bf16 v[152:155], v[152:155], v[194:197], 0\n\t"
"s_waitcnt lgkmcnt(8)\n\t"
"v_mfma_f32_16x16x32_bf16 v[156:159], v[156:159], v[194:197], 0\n\t"
"s_waitcnt lgkmcnt(6)\n\t"
"v_mfma_f32_16x16x32_bf16 v[160:163], v[160:163], v[194:197], 0\n\t"
"s_waitcnt lgkmcnt(4)\n\t"
"v_mfma_f32_16x16x32_bf16 v[164:167], v[164:167], v[194:197], 0\n\t"
"s_nop 1\n\t"
"v_cvt_pk_bf16_f32 v152, v152, v153\n\t"
"v_cvt_pk_bf16_f32 v153, v154, v155\n\t"
"v_cvt_pk_bf16_f32 v154, v156, v157\n\t"
"v_cvt_pk_bf16_f32 v155, v158, v159\n\t"
"s_mov_b64 exec, s[44:45]\n\t"
"ds_write_b128 v240, v[152:155] offset:6144\n\t"
"s_mov_b64 exec, -1\n\t"
"v_cvt_pk_bf16_f32 v160, v160, v161\n\t"
"v_cvt_pk_bf16_f32 v161, v162, v163\n\t"
"v_cvt_pk_bf16_f32 v162, v164, v165\n\t"
"v_cvt_pk_bf16_f32 v163, v166, v167\n\t"
"s_mov_b64 exec, s[44:45]\n\t"
"ds_write_b128 v240, v[160:163] offset:7680\n\t"
"s_mov_b64 exec, -1\n\t"
"s_waitcnt lgkmcnt(0)\n\t"
"s_cmp_lg_u32 s73, 0\n\t"
"s_cbranch_scc1 Lgo_%=\n\t"
"s_cmp_lg_u32 s66, 0\n\t"
"s_cbranch_scc1 Lchk_%=\n\t"
"s_cmp_eq_u32 s68, -1\n\t"
"s_cbranch_scc1 Lgo_%=\n\t"
"s_branch Lfb_%=\n\t"
"Lchk_%=:\n\t"
"s_waitcnt vmcnt(0)\n\t"
"v_readfirstlane_b32 s60, v46\n\t"
"s_and_b32 s60, s60, 0xffff\n\t"
"s_cmp_eq_u32 s60, s67\n\t"
"s_cbranch_scc1 Lgo_%=\n\t"
"s_sub_u32 s68, s68, 1\n\t"
"s_cmp_eq_u32 s68, 0\n\t"
"s_cbranch_scc1 Lfb_%=\n\t"
"s_add_u32 s60, s66, 1835008\n\t"
"buffer_load_dword v47, off, s[8:11], s60 sc1\n\t"
"s_add_u32 s60, s66, 1867776\n\t"
"buffer_load_dword v46, off, s[8:11], s60 sc1\n\t"
"s_waitcnt vmcnt(1)\n\t"
"v_readfirstlane_b32 s60, v47\n\t"
"s_and_b32 s60, s60, 0xffff0000\n\t"
"s_or_b32 s60, s60, s67\n\t"
"s_cmp_eq_u32 s60, s71\n\t"
"s_cbranch_scc1 Lchk_%=\n\t"
"s_mov_b32 s71, s60\n\t"
"v_mov_b32 v47, s71\n\t"
"s_mov_b64 exec, 1\n\t"
"buffer_store_dword v47, off, s[8:11], s70 sc1\n\t"
"s_mov_b64 exec, -1\n\t"
"s_branch Lchk_%=\n\t"
"Lgo_%=:\n\t"
"s_barrier\n\t"
"s_mov_b32 s20, 0\n\t"
"buffer_load_dwordx4 a[208:211], v252, s[8:11], s20 offen offset:0 sc1\n\t"
"buffer_load_dwordx4 a[212:215], v252, s[8:11], s20 offen offset:1024 sc1\n\t"
"buffer_load_dwordx4 a[216:219], v252, s[8:11], s20 offen offset:2048 sc1\n\t"
"ds_read_b128 a[168:171], v242 offset:0\n\t"
"ds_read_b128 a[172:175], v242 offset:512\n\t"
"ds_read_b128 a[176:179], v242 offset:1024\n\t"
"ds_read_b128 a[180:183], v242 offset:1536\n\t"
"ds_read_b128 a[184:187], v242 offset:2048\n\t"
"ds_read_b128 a[188:191], v242 offset:2560\n\t"
"ds_read_b128 a[192:195], v242 offset:3072\n\t"
"ds_read_b128 a[196:199], v242 offset:3584\n\t"
"ds_read_b128 a[200:203], v242 offset:4096\n\t"
"ds_read_b128 a[204:207], v242 offset:4608\n\t"
"s_mov_b32 s52, 0x3fd80000\n\t"
"s_mov_b32 s53, s52\n\t"
"s_mov_b32 s54, 0x3e97b426\n\t"
"s_mov_b32 s55, s54\n\t"
"s_mov_b32 s56, 0x3f400000\n\t"
"s_mov_b32 s57, s56\n\t"
"s_mov_b32 s58, 0x3f2aaaab\n\t"
"s_mov_b32 s59, s58\n\t"
"s_mov_b32 s60, 0x3ee38e39\n\t"
"s_mov_b32 s61, s60\n\t"
"s_mov_b32 s62, 0x40100000\n\t"
"s_mov_b32 s63, s62\n\t"
"s_mov_b32 s64, 0x3faaaaab\n\t"
"s_mov_b32 s65, s64\n\t"
"s_mov_b32 s66, 0x3f17b426\n\t"
"s_mov_b32 s67, s66\n\t"
"s_mov_b32 s20, 12288\n\t"
"buffer_load_dwordx4 a[220:223], v252, s[8:11], s20 offen offset:0 sc1\n\t"
"buffer_load_dwordx4 a[224:227], v252, s[8:11], s20 offen offset:1024 sc1\n\t"
"buffer_load_dwordx4 a[228:231], v252, s[8:11], s20 offen offset:2048 sc1\n\t"
"s_mov_b32 s20, 24576\n\t"
"buffer_load_dwordx4 a[232:235], v252, s[8:11], s20 offen offset:0 sc1\n\t"
"buffer_load_dwordx4 a[236:239], v252, s[8:11], s20 offen offset:1024 sc1\n\t"
"buffer_load_dwordx4 a[240:243], v252, s[8:11], s20 offen offset:2048 sc1\n\t"
"s_waitcnt vmcnt(6) lgkmcnt(7)\n\t"
"v_mfma_f32_16x16x32_bf16 v[2:5], a[208:211], a[168:171], 0\n\t"
"v_mfma_f32_16x16x32_bf16 v[2:5], a[212:215], a[172:175], v[2:5]\n\t"
"v_mfma_f32_16x16x32_bf16 v[2:5], a[216:219], a[176:179], v[2:5]\n\t"
"s_mov_b32 s20, 184320\n\t"
"buffer_load_dwordx4 v[202:205], v252, s[8:11], s20 offen offset:0 sc1\n\t"
"s_waitcnt lgkmcnt(6)\n\t"
"v_mfma_f32_16x16x32_bf16 v[6:9], a[208:211], a[172:175], 0\n\t"
"v_mfma_f32_16x16x32_bf16 v[6:9], a[212:215], a[176:179], v[6:9]\n\t"
"v_mfma_f32_16x16x32_bf16 v[6:9], a[216:219], a[180:183], v[6:9]\n\t"
"buffer_load_dwordx4 v[210:213], v252, s[8:11], s20 offen offset:1024 sc1\n\t"
"s_waitcnt lgkmcnt(5)\n\t"
"v_mfma_f32_16x16x32_bf16 v[10:13], a[208:211], a[176:179], 0\n\t"
"v_mfma_f32_16x16x32_bf16 v[10:13], a[212:215], a[180:183], v[10:13]\n\t"
"v_mfma_f32_16x16x32_bf16 v[10:13], a[216:219], a[184:187], v[10:13]\n\t"
"buffer_load_dwordx4 a[0:3], v252, s[8:11], s20 offen offset:2048 sc1\n\t"
"s_waitcnt lgkmcnt(4)\n\t"
"v_mfma_f32_16x16x32_bf16 v[14:17], a[208:211], a[180:183], 0\n\t"
"v_mfma_f32_16x16x32_bf16 v[14:17], a[212:215], a[184:187], v[14:17]\n\t"
"v_mfma_f32_16x16x32_bf16 v[14:17], a[216:219], a[188:191], v[14:17]\n\t"
"s_mov_b32 s20, 196608\n\t"
"buffer_load_dwordx4 a[8:11], v252, s[8:11], s20 offen offset:0 sc1\n\t"
"s_waitcnt lgkmcnt(3)\n\t"
"v_mfma_f32_16x16x32_bf16 v[18:21], a[208:211], a[184:187], 0\n\t"
"v_mfma_f32_16x16x32_bf16 v[18:21], a[212:215], a[188:191], v[18:21]\n\t"
"v_mfma_f32_16x16x32_bf16 v[18:21], a[216:219], a[192:195], v[18:21]\n\t"
"buffer_load_dwordx4 a[16:19], v252, s[8:11], s20 offen offset:1024 sc1\n\t"
"s_waitcnt lgkmcnt(2)\n\t"
"v_mfma_f32_16x16x32_bf16 v[22:25], a[208:211], a[188:191], 0\n\t"
"v_mfma_f32_16x16x32_bf16 v[22:25], a[212:215], a[192:195], v[22:25]\n\t"
"v_mfma_f32_16x16x32_bf16 v[22:25], a[216:219], a[196:199], v[22:25]\n\t"
"buffer_load_dwordx4 a[24:27], v252, s[8:11], s20 offen offset:2048 sc1\n\t"
"s_waitcnt lgkmcnt(1)\n\t"
"v_mfma_f32_16x16x32_bf16 v[26:29], a[208:211], a[192:195], 0\n\t"
"v_mfma_f32_16x16x32_bf16 v[26:29], a[212:215], a[196:199], v[26:29]\n\t"
"v_mfma_f32_16x16x32_bf16 v[26:29], a[216:219], a[200:203], v[26:29]\n\t"
"s_mov_b32 s20, 208896\n\t"
"buffer_load_dwordx4 a[32:35], v252, s[8:11], s20 offen offset:0 sc1\n\t"
"s_waitcnt lgkmcnt(0)\n\t"
"v_mfma_f32_16x16x32_bf16 v[30:33], a[208:211], a[196:199], 0\n\t"
"v_mfma_f32_16x16x32_bf16 v[30:33], a[212:215], a[200:203], v[30:33]\n\t"
"v_mfma_f32_16x16x32_bf16 v[30:33], a[216:219], a[204:207], v[30:33]\n\t"
"buffer_load_dwordx4 a[40:43], v252, s[8:11], s20 offen offset:1024 sc1\n\t"
"s_waitcnt vmcnt(11)\n\t"
"v_mfma_f32_16x16x32_bf16 v[34:37], a[220:223], a[168:171], 0\n\t"
"v_mfma_f32_16x16x32_bf16 v[34:37], a[224:227], a[172:175], v[34:37]\n\t"
"v_mfma_f32_16x16x32_bf16 v[34:37], a[228:231], a[176:179], v[34:37]\n\t"
"buffer_load_dwordx4 a[48:51], v252, s[8:11], s20 offen offset:2048 sc1\n\t"
"v_mfma_f32_16x16x32_bf16 v[38:41], a[220:223], a[172:175], 0\n\t"
"ds_read_b64_tr_b16 v[222:223], v238 offset:512\n\t"
"v_mfma_f32_16x16x32_bf16 v[38:41], a[224:227], a[176:179], v[38:41]\n\t"
"ds_read_b64_tr_b16 v[224:225], v238 offset:2048\n\t"
"v_mfma_f32_16x16x32_bf16 v[38:41], a[228:231], a[180:183], v[38:41]\n\t"
"s_mov_b32 s20, 221184\n\t"
"buffer_load_dwordx4 a[56:59], v252, s[8:11], s20 offen offset:0 sc1\n\t"
"v_mfma_f32_16x16x32_bf16 v[42:45], a[220:223], a[176:179], 0\n\t"
"ds_read_b64_tr_b16 v[226:227], v239 offset:512\n\t"
"v_mfma_f32_16x16x32_bf16 v[42:45], a[224:227], a[180:183], v[42:45]\n\t"
"ds_read_b64_tr_b16 v[228:229], v239 offset:2048\n\t"
"v_mfma_f32_16x16x32_bf16 v[42:45], a[228:231], a[184:187], v[42:45]\n\t"
"v_mfma_f32_16x16x32_bf16 v[46:49], a[220:223], a[180:183], 0\n\t"
"v_mfma_f32_16x16x32_bf16 v[46:49], a[224:227], a[184:187], v[46:49]\n\t"
"v_mfma_f32_16x16x32_bf16 v[46:49], a[228:231], a[188:191], v[46:49]\n\t"
"buffer_load_dwordx4 a[64:67], v252, s[8:11], s20 offen offset:1024 sc1\n\t"
"v_mfma_f32_16x16x32_bf16 v[50:53], a[220:223], a[184:187], 0\n\t"
"v_mfma_f32_16x16x32_bf16 v[50:53], a[224:227], a[188:191], v[50:53]\n\t"
"v_mfma_f32_16x16x32_bf16 v[50:53], a[228:231], a[192:195], v[50:53]\n\t"
"buffer_load_dwordx4 a[72:75], v252, s[8:11], s20 offen offset:2048 sc1\n\t"
"v_mfma_f32_16x16x32_bf16 v[54:57], a[220:223], a[188:191], 0\n\t"
"v_mfma_f32_16x16x32_bf16 v[54:57], a[224:227], a[192:195], v[54:57]\n\t"
"ds_read_b64_tr_b16 v[230:231], v238 offset:20480\n\t"
"v_mfma_f32_16x16x32_bf16 v[54:57], a[228:231], a[196:199], v[54:57]\n\t"
"s_mov_b32 s20, 233472\n\t"
"buffer_load_dwordx4 a[80:83], v252, s[8:11], s20 offen offset:0 sc1\n\t"
"s_waitcnt lgkmcnt(3)\n\t"
"v_mfma_f32_16x16x32_bf16 v[222:225], v[222:225], v[194:197], 0\n\t"
"buffer_load_dwordx4 a[88:91], v252, s[8:11], s20 offen offset:1024 sc1\n\t"
"v_mfma_f32_16x16x32_bf16 v[58:61], a[220:223], a[192:195], 0\n\t"
"ds_read_b64_tr_b16 v[232:233], v238 offset:22016\n\t"
"v_mfma_f32_16x16x32_bf16 v[58:61], a[224:227], a[196:199], v[58:61]\n\t"
"ds_read_b64_tr_b16 v[234:235], v239 offset:20480\n\t"
"v_mfma_f32_16x16x32_bf16 v[58:61], a[228:231], a[200:203], v[58:61]\n\t"
"buffer_load_dwordx4 a[96:99], v252, s[8:11], s20 offen offset:2048 sc1\n\t"
"s_waitcnt lgkmcnt(3)\n\t"
"v_mfma_f32_16x16x32_bf16 v[226:229], v[226:229], v[194:197], 0\n\t"
"s_mov_b32 s20, 245760\n\t"
"buffer_load_dwordx4 a[104:107], v252, s[8:11], s20 offen offset:0 sc1\n\t"
"v_mfma_f32_16x16x32_bf16 v[62:65], a[220:223], a[196:199], 0\n\t"
"v_cvt_pk_bf16_f32 v222, v222, v223\n\t"
"v_mfma_f32_16x16x32_bf16 v[62:65], a[224:227], a[200:203], v[62:65]\n\t"
"v_cvt_pk_bf16_f32 v223, v224, v225\n\t"
"ds_read_b64_tr_b16 v[236:237], v239 offset:22016\n\t"
"v_mfma_f32_16x16x32_bf16 v[62:65], a[228:231], a[204:207], v[62:65]\n\t"
"s_waitcnt vmcnt(16)\n\t"
"v_mfma_f32_16x16x32_bf16 v[66:69], a[232:235], a[168:171], 0\n\t"
"ds_read_b128 a[168:171], v242 offset:9216\n\t"
"v_mfma_f32_16x16x32_bf16 v[66:69], a[236:239], a[172:175], v[66:69]\n\t"
"v_cvt_pk_bf16_f32 v224, v226, v227\n\t"
"v_cvt_pk_bf16_f32 v225, v228, v229\n\t"
"v_mfma_f32_16x16x32_bf16 v[66:69], a[240:243], a[176:179], v[66:69]\n\t"
"buffer_load_dwordx4 a[112:115], v252, s[8:11], s20 offen offset:1024 sc1\n\t"
"v_mfma_f32_16x16x32_bf16 v[70:73], a[232:235], a[172:175], 0\n\t"
"ds_read_b128 a[172:175], v242 offset:9728\n\t"
"v_mfma_f32_16x16x32_bf16 v[70:73], a[236:239], a[176:179], v[70:73]\n\t"
"v_mfma_f32_16x16x32_bf16 v[70:73], a[240:243], a[180:183], v[70:73]\n\t"
"buffer_load_dwordx4 a[120:123], v252, s[8:11], s20 offen offset:2048 sc1\n\t"
"s_waitcnt lgkmcnt(4)\n\t"
"v_mfma_f32_16x16x32_bf16 v[230:233], v[230:233], v[194:197], 0\n\t"
"s_mov_b32 s20, 405504\n\t"
"buffer_load_dwordx4 a[128:131], v252, s[8:11], s20 offen offset:0 sc1\n\t"
"v_mfma_f32_16x16x32_bf16 v[74:77], a[232:235], a[176:179], 0\n\t"
"ds_read_b128 a[176:179], v242 offset:10240\n\t"
"v_mfma_f32_16x16x32_bf16 v[74:77], a[236:239], a[180:183], v[74:77]\n\t"
"v_mfma_f32_16x16x32_bf16 v[74:77], a[240:243], a[184:187], v[74:77]\n\t"
"buffer_load_dwordx4 a[136:139], v252, s[8:11], s20 offen offset:1024 sc1\n\t"
"s_waitcnt lgkmcnt(3)\n\t"
"v_mfma_f32_16x16x32_bf16 v[234:237], v[234:237], v[194:197], 0\n\t"
"v_mfma_f32_16x16x32_bf16 v[78:81], a[232:235], a[180:183], 0\n\t"
"v_cvt_pk_bf16_f32 v230, v230, v231\n\t"
"v_cvt_pk_bf16_f32 v231, v232, v233\n\t"
"v_mfma_f32_16x16x32_bf16 v[78:81], a[236:239], a[184:187], v[78:81]\n\t"
"ds_read_b128 a[180:183], v242 offset:10752\n\t"
"v_mfma_f32_16x16x32_bf16 v[78:81], a[240:243], a[188:191], v[78:81]\n\t"
"buffer_load_dwordx4 a[144:147], v252, s[8:11], s20 offen offset:2048 sc1\n\t"
"v_mfma_f32_16x16x32_bf16 v[82:85], a[232:235], a[184:187], 0\n\t"
"ds_read_b128 a[184:187], v242 offset:11264\n\t"
"v_mfma_f32_16x16x32_bf16 v[82:85], a[236:239], a[188:191], v[82:85]\n\t"
"v_cvt_pk_bf16_f32 v232, v234, v235\n\t"
"v_cvt_pk_bf16_f32 v233, v236, v237\n\t"
"v_mfma_f32_16x16x32_bf16 v[82:85], a[240:243], a[192:195], v[82:85]\n\t"
"s_mov_b32 s20, 417792\n\t"
"buffer_load_dwordx4 a[152:155], v252, s[8:11], s20 offen offset:0 sc1\n\t"
"v_mfma_f32_16x16x32_bf16 v[86:89], a[232:235], a[188:191], 0\n\t"
"ds_read_b128 a[188:191], v242 offset:11776\n\t"
"v_mfma_f32_16x16x32_bf16 v[86:89], a[236:239], a[192:195], v[86:89]\n\t"
"v_mfma_f32_16x16x32_bf16 v[86:89], a[240:243], a[196:199], v[86:89]\n\t"
"buffer_load_dwordx4 a[160:163], v252, s[8:11], s20 offen offset:1024 sc1\n\t"
"v_mfma_f32_16x16x32_bf16 v[90:93], a[232:235], a[192:195], 0\n\t"
"ds_read_b128 a[192:195], v242 offset:12288\n\t"
"v_mfma_f32_16x16x32_bf16 v[90:93], a[236:239], a[196:199], v[90:93]\n\t"
"v_mfma_f32_16x16x32_bf16 v[90:93], a[240:243], a[200:203], v[90:93]\n\t"
"buffer_load_dwordx4 v[198:201], v252, s[8:11], s20 offen offset:2048 sc1\n\t"
"v_mfma_f32_16x16x32_bf16 v[94:97], a[232:235], a[196:199], 0\n\t"
"ds_read_b128 a[196:199], v242 offset:12800\n\t"
"v_mfma_f32_16x16x32_bf16 v[94:97], a[236:239], a[200:203], v[94:97]\n\t"
"ds_read_b128 a[200:203], v242 offset:13312\n\t"
"v_mfma_f32_16x16x32_bf16 v[94:97], a[240:243], a[204:207], v[94:97]\n\t"
"s_mov_b32 s20, 430080\n\t"
"buffer_load_dwordx4 v[206:209], v252, s[8:11], s20 offen offset:0 sc1\n\t"
"s_waitcnt vmcnt(22) lgkmcnt(6)\n\t"
"v_mfma_f32_16x16x32_bf16 v[98:101], v[202:205], a[168:171], 0\n\t"
"ds_read_b128 a[204:207], v242 offset:13824\n\t"
"v_mfma_f32_16x16x32_bf16 v[98:101], v[210:213], a[172:175], v[98:101]\n\t"
"v_mfma_f32_16x16x32_bf16 v[98:101], a[0:3], a[176:179], v[98:101]\n\t"
"buffer_load_dwordx4 v[214:217], v252, s[8:11], s20 offen offset:1024 sc1\n\t"
"s_waitcnt lgkmcnt(6)\n\t"
"v_mfma_f32_16x16x32_bf16 v[102:105], v[202:205], a[172:175], 0\n\t"
"v_mfma_f32_16x16x32_bf16 v[102:105], v[210:213], a[176:179], v[102:105]\n\t"
"v_mfma_f32_16x16x32_bf16 v[102:105], a[0:3], a[180:183], v[102:105]\n\t"
"buffer_load_dwordx4 a[4:7], v252, s[8:11], s20 offen offset:2048 sc1\n\t"
"s_waitcnt lgkmcnt(0)\n\t"
"s_barrier\n\t"
"v_mfma_f32_16x16x32_bf16 v[106:109], v[202:205], a[176:179], 0\n\t"
"v_mfma_f32_16x16x32_bf16 v[106:109], v[210:213], a[180:183], v[106:109]\n\t"
"v_mfma_f32_16x16x32_bf16 v[106:109], a[0:3], a[184:187], v[106:109]\n\t"
"s_mov_b64 exec, s[44:45]\n\t"
"ds_write_b128 v240, v[222:225] offset:4608\n\t"
"s_mov_b64 exec, -1\n\t"
"s_mov_b32 s20, 110592\n\t"
"buffer_load_dwordx4 a[12:15], v252, s[8:11], s20 offen offset:0 sc1\n\t"
"v_mfma_f32_16x16x32_bf16 v[110:113], v[202:205], a[180:183], 0\n\t"
"ds_read_b64_tr_b16 v[222:223], v238 offset:40448\n\t"
"v_mfma_f32_16x16x32_bf16 v[110:113], v[210:213], a[184:187], v[110:113]\n\t"
"ds_read_b64_tr_b16 v[224:225], v238 offset:41984\n\t"
"v_mfma_f32_16x16x32_bf16 v[110:113], a[0:3], a[188:191], v[110:113]\n\t"
"s_mov_b64 exec, s[44:45]\n\t"
"ds_write_b128 v240, v[230:233] offset:6144\n\t"
"s_mov_b64 exec, -1\n\t"
"ds_read_b64_tr_b16 v[226:227], v239 offset:40448\n\t"
"ds_read_b64_tr_b16 v[228:229], v239 offset:41984\n\t"
"v_mfma_f32_16x16x32_bf16 v[114:117], v[202:205], a[184:187], 0\n\t"
"v_subrev_u32 v238, 59904, v238\n\t"
"v_subrev_u32 v239, 59904, v239\n\t"
"v_mfma_f32_16x16x32_bf16 v[114:117], v[210:213], a[188:191], v[114:117]\n\t"
"ds_read_b64_tr_b16 v[230:231], v238 offset:512\n\t"
"v_mfma_f32_16x16x32_bf16 v[114:117], a[0:3], a[192:195], v[114:117]\n\t"
"buffer_load_dwordx4 a[20:23], v252, s[8:11], s20 offen offset:1024 sc1\n\t"
"v_mfma_f32_16x16x32_bf16 v[118:121], v[202:205], a[188:191], 0\n\t"
"ds_read_b64_tr_b16 v[232:233], v238 offset:2048\n\t"
"v_mfma_f32_16x16x32_bf16 v[118:121], v[210:213], a[192:195], v[118:121]\n\t"
"ds_read_b64_tr_b16 v[234:235], v239 offset:512\n\t"
"v_mfma_f32_16x16x32_bf16 v[118:121], a[0:3], a[196:199], v[118:121]\n\t"
"buffer_load_dwordx4 a[28:31], v252, s[8:11], s20 offen offset:2048 sc1\n\t"
"s_waitcnt lgkmcnt(6)\n\t"
"v_mfma_f32_16x16x32_bf16 v[222:225], v[222:225], v[194:197], 0\n\t"
"s_mov_b32 s20, 122880\n\t"
"buffer_load_dwordx4 a[36:39], v252, s[8:11], s20 offen offset:0 sc1\n\t"
"s_waitcnt lgkmcnt(3)\n\t"
"v_mfma_f32_16x16x32_bf16 v[226:229], v[226:229], v[194:197], 0\n\t"
"ds_read_b64_tr_b16 v[236:237], v239 offset:2048\n\t"
"v_mfma_f32_16x16x32_bf16 v[122:125], v[202:205], a[192:195], 0\n\t"
"v_mfma_f32_16x16x32_bf16 v[122:125], v[210:213], a[196:199], v[122:125]\n\t"
"v_mfma_f32_16x16x32_bf16 v[122:125], a[0:3], a[200:203], v[122:125]\n\t"
"buffer_load_dwordx4 a[44:47], v252, s[8:11], s20 offen offset:1024 sc1\n\t"
"v_mfma_f32_16x16x32_bf16 v[126:129], v[202:205], a[196:199], 0\n\t"
"v_cvt_pk_bf16_f32 v222, v222, v223\n\t"
"v_cvt_pk_bf16_f32 v223, v224, v225\n\t"
"v_mfma_f32_16x16x32_bf16 v[126:129], v[210:213], a[200:203], v[126:129]\n\t"
"v_cvt_pk_bf16_f32 v224, v226, v227\n\t"
"v_cvt_pk_bf16_f32 v225, v228, v229\n\t"
"v_mfma_f32_16x16x32_bf16 v[126:129], a[0:3], a[204:207], v[126:129]\n\t"
"buffer_load_dwordx4 a[52:55], v252, s[8:11], s20 offen offset:2048 sc1\n\t"
"s_waitcnt lgkmcnt(2)\n\t"
"v_mfma_f32_16x16x32_bf16 v[230:233], v[230:233], v[194:197], 0\n\t"
"s_mov_b64 exec, s[44:45]\n\t"
"ds_write_b128 v240, v[222:225] offset:7680\n\t"
"s_mov_b64 exec, -1\n\t"
"s_waitcnt lgkmcnt(1)\n\t"
"v_mfma_f32_16x16x32_bf16 v[234:237], v[234:237], v[194:197], 0\n\t"
"s_mov_b32 s20, 135168\n\t"
"buffer_load_dwordx4 a[76:79], v252, s[8:11], s20 offen offset:0 sc1\n\t"
"s_waitcnt vmcnt(28)\n\t"
"v_mfma_f32_16x16x32_bf16 v[130:133], a[8:11], a[168:171], 0\n\t"
"ds_read_b64_tr_b16 v[222:223], v238 offset:20480\n\t"
"v_mfma_f32_16x16x32_bf16 v[130:133], a[16:19], a[172:175], v[130:133]\n\t"
"ds_read_b64_tr_b16 v[224:225], v238 offset:22016\n\t"
"v_mfma_f32_16x16x32_bf16 v[130:133], a[24:27], a[176:179], v[130:133]\n\t"
"ds_read_b64_tr_b16 v[226:227], v239 offset:20480\n\t"
"ds_read_b64_tr_b16 v[228:229], v239 offset:22016\n\t"
"v_mfma_f32_16x16x32_bf16 v[134:137], a[8:11], a[172:175], 0\n\t"
"v_cvt_pk_bf16_f32 v230, v230, v231\n\t"
"v_cvt_pk_bf16_f32 v231, v232, v233\n\t"
"v_mfma_f32_16x16x32_bf16 v[134:137], a[16:19], a[176:179], v[134:137]\n\t"
"v_cvt_pk_bf16_f32 v232, v234, v235\n\t"
"v_cvt_pk_bf16_f32 v233, v236, v237\n\t"
"v_mfma_f32_16x16x32_bf16 v[134:137], a[24:27], a[180:183], v[134:137]\n\t"
"buffer_load_dwordx4 a[84:87], v252, s[8:11], s20 offen offset:1024 sc1\n\t"
"v_mfma_f32_16x16x32_bf16 v[138:141], a[8:11], a[176:179], 0\n\t"
"v_mfma_f32_16x16x32_bf16 v[138:141], a[16:19], a[180:183], v[138:141]\n\t"
"v_mfma_f32_16x16x32_bf16 v[138:141], a[24:27], a[184:187], v[138:141]\n\t"
"buffer_load_dwordx4 a[92:95], v252, s[8:11], s20 offen offset:2048 sc1\n\t"
"v_mfma_f32_16x16x32_bf16 v[142:145], a[8:11], a[180:183], 0\n\t"
"v_mfma_f32_16x16x32_bf16 v[142:145], a[16:19], a[184:187], v[142:145]\n\t"
"v_mfma_f32_16x16x32_bf16 v[142:145], a[24:27], a[188:191], v[142:145]\n\t"
"s_mov_b64 exec, s[44:45]\n\t"
"ds_write_b128 v240, v[230:233] offset:0\n\t"
"s_mov_b64 exec, -1\n\t"
"s_waitcnt lgkmcnt(3)\n\t"
"v_mfma_f32_16x16x32_bf16 v[222:225], v[222:225], v[194:197], 0\n\t"
"ds_read_b64_tr_b16 v[230:231], v238 offset:40448\n\t"
"ds_read_b64_tr_b16 v[232:233], v238 offset:41984\n\t"
"s_waitcnt lgkmcnt(3)\n\t"
"v_mfma_f32_16x16x32_bf16 v[226:229], v[226:229], v[194:197], 0\n\t"
"ds_read_b64_tr_b16 v[234:235], v239 offset:40448\n\t"
"ds_read_b64_tr_b16 v[236:237], v239 offset:41984\n\t"
"v_mfma_f32_16x16x32_bf16 v[146:149], a[8:11], a[184:187], 0\n\t"
"v_mfma_f32_16x16x32_bf16 v[146:149], a[16:19], a[188:191], v[146:149]\n\t"
"v_mfma_f32_16x16x32_bf16 v[146:149], a[24:27], a[192:195], v[146:149]\n\t"
"s_mov_b32 s20, 147456\n\t"
"buffer_load_dwordx4 a[140:143], v252, s[8:11], s20 offen offset:0 sc1\n\t"
"v_mfma_f32_16x16x32_bf16 v[150:153], a[8:11], a[188:191], 0\n\t"
"v_cvt_pk_bf16_f32 v222, v222, v223\n\t"
"v_cvt_pk_bf16_f32 v223, v224, v225\n\t"
"v_mfma_f32_16x16x32_bf16 v[150:153], a[16:19], a[192:195], v[150:153]\n\t"
"v_cvt_pk_bf16_f32 v224, v226, v227\n\t"
"v_cvt_pk_bf16_f32 v225, v228, v229\n\t"
"v_mfma_f32_16x16x32_bf16 v[150:153], a[24:27], a[196:199], v[150:153]\n\t"
"buffer_load_dwordx4 a[148:151], v252, s[8:11], s20 offen offset:1024 sc1\n\t"
"s_waitcnt lgkmcnt(2)\n\t"
"v_mfma_f32_16x16x32_bf16 v[230:233], v[230:233], v[194:197], 0\n\t"
"buffer_load_dwordx4 a[156:159], v252, s[8:11], s20 offen offset:2048 sc1\n\t"
"s_waitcnt lgkmcnt(0)\n\t"
"v_mfma_f32_16x16x32_bf16 v[234:237], v[234:237], v[194:197], 0\n\t"
"s_mov_b64 exec, s[44:45]\n\t"
"ds_write_b128 v240, v[222:225] offset:1536\n\t"
"s_mov_b64 exec, -1\n\t"
"v_mfma_f32_16x16x32_bf16 v[154:157], a[8:11], a[192:195], 0\n\t"
"v_mfma_f32_16x16x32_bf16 v[154:157], a[16:19], a[196:199], v[154:157]\n\t"
"v_mfma_f32_16x16x32_bf16 v[154:157], a[24:27], a[200:203], v[154:157]\n\t"
"s_mov_b32 s20, 159744\n\t"
"buffer_load_dwordx4 a[208:211], v252, s[8:11], s20 offen offset:0 sc1\n\t"
"v_mfma_f32_16x16x32_bf16 v[158:161], a[8:11], a[196:199], 0\n\t"
"v_cvt_pk_bf16_f32 v230, v230, v231\n\t"
"v_cvt_pk_bf16_f32 v231, v232, v233\n\t"
"v_mfma_f32_16x16x32_bf16 v[158:161], a[16:19], a[200:203], v[158:161]\n\t"
"v_cvt_pk_bf16_f32 v232, v234, v235\n\t"
"v_cvt_pk_bf16_f32 v233, v236, v237\n\t"
"v_mfma_f32_16x16x32_bf16 v[158:161], a[24:27], a[204:207], v[158:161]\n\t"
"buffer_load_dwordx4 a[212:215], v252, s[8:11], s20 offen offset:1024 sc1\n\t"
"s_waitcnt vmcnt(32)\n\t"
"v_mfma_f32_16x16x32_bf16 v[162:165], a[32:35], a[168:171], 0\n\t"
"v_mfma_f32_16x16x32_bf16 v[162:165], a[40:43], a[172:175], v[162:165]\n\t"
"v_mfma_f32_16x16x32_bf16 v[162:165], a[48:51], a[176:179], v[162:165]\n\t"
"s_mov_b64 exec, s[44:45]\n\t"
"ds_write_b128 v240, v[230:233] offset:3072\n\t"
"s_mov_b64 exec, -1\n\t"
"v_mfma_f32_16x16x32_bf16 v[166:169], a[32:35], a[172:175], 0\n\t"
"v_mfma_f32_16x16x32_bf16 v[166:169], a[40:43], a[176:179], v[166:169]\n\t"
"v_mfma_f32_16x16x32_bf16 v[166:169], a[48:51], a[180:183], v[166:169]\n\t"
"buffer_load_dwordx4 a[216:219], v252, s[8:11], s20 offen offset:2048 sc1\n\t"
"v_mfma_f32_16x16x32_bf16 v[170:173], a[32:35], a[176:179], 0\n\t"
"v_mfma_f32_16x16x32_bf16 v[170:173], a[40:43], a[180:183], v[170:173]\n\t"
"v_mfma_f32_16x16x32_bf16 v[170:173], a[48:51], a[184:187], v[170:173]\n\t"
"s_mov_b32 s20, 172032\n\t"
"buffer_load_dwordx4 a[220:223], v252, s[8:11], s20 offen offset:0 sc1\n\t"
"v_mfma_f32_16x16x32_bf16 v[174:177], a[32:35], a[180:183], 0\n\t"
"v_mfma_f32_16x16x32_bf16 v[174:177], a[40:43], a[184:187], v[174:177]\n\t"
"v_mfma_f32_16x16x32_bf16 v[174:177], a[48:51], a[188:191], v[174:177]\n\t"
"buffer_load_dwordx4 a[224:227], v252, s[8:11], s20 offen offset:1024 sc1\n\t"
"s_waitcnt lgkmcnt(0)\n\t"
"s_barrier\n\t"
"v_mfma_f32_16x16x32_bf16 v[178:181], a[32:35], a[184:187], 0\n\t"
"ds_read_b128 a[168:171], v242 offset:0\n\t"
"v_mfma_f32_16x16x32_bf16 v[178:181], a[40:43], a[188:191], v[178:181]\n\t"
"ds_read_b128 a[172:175], v242 offset:512\n\t"
"v_mfma_f32_16x16x32_bf16 v[178:181], a[48:51], a[192:195], v[178:181]\n\t"
"ds_read_b128 a[176:179], v242 offset:1024\n\t"
"ds_read_b128 a[180:183], v242 offset:1536\n\t"
"v_mfma_f32_16x16x32_bf16 v[182:185], a[32:35], a[188:191], 0\n\t"
"ds_read_b128 a[184:187], v242 offset:2048\n\t"
"v_mfma_f32_16x16x32_bf16 v[182:185], a[40:43], a[192:195], v[182:185]\n\t"
"ds_read_b128 a[188:191], v242 offset:2560\n\t"
"v_mfma_f32_16x16x32_bf16 v[182:185], a[48:51], a[196:199], v[182:185]\n\t"
"buffer_load_dwordx4 a[228:231], v252, s[8:11], s20 offen offset:2048 sc1\n\t"
"v_mfma_f32_16x16x32_bf16 v[186:189], a[32:35], a[192:195], 0\n\t"
"ds_read_b128 a[192:195], v242 offset:3072\n\t"
"v_mfma_f32_16x16x32_bf16 v[186:189], a[40:43], a[196:199], v[186:189]\n\t"
"v_mfma_f32_16x16x32_bf16 v[186:189], a[48:51], a[200:203], v[186:189]\n\t"
"s_mov_b32 s20, 36864\n\t"
"buffer_load_dwordx4 a[232:235], v252, s[8:11], s20 offen offset:0 sc1\n\t"
"v_mfma_f32_16x16x32_bf16 v[190:193], a[32:35], a[196:199], 0\n\t"
"ds_read_b128 a[196:199], v242 offset:3584\n\t"
"v_mfma_f32_16x16x32_bf16 v[190:193], a[40:43], a[200:203], v[190:193]\n\t"
"ds_read_b128 a[200:203], v242 offset:4096\n\t"
"v_mfma_f32_16x16x32_bf16 v[190:193], a[48:51], a[204:207], v[190:193]\n\t"
"buffer_load_dwordx4 a[236:239], v252, s[8:11], s20 offen offset:1024 sc1\n\t"
"s_waitcnt vmcnt(35) lgkmcnt(6)\n\t"
"v_mfma_f32_16x16x32_bf16 v[2:5], a[56:59], a[168:171], v[2:5]\n\t"
"ds_read_b128 a[204:207], v242 offset:4608\n\t"
"v_mfma_f32_16x16x32_bf16 v[2:5], a[64:67], a[172:175], v[2:5]\n\t"
"v_mfma_f32_16x16x32_bf16 v[2:5], a[72:75], a[176:179], v[2:5]\n\t"
"buffer_load_dwordx4 a[240:243], v252, s[8:11], s20 offen offset:2048 sc1\n\t"
"s_waitcnt lgkmcnt(6)\n\t"
"v_mfma_f32_16x16x32_bf16 v[6:9], a[56:59], a[172:175], v[6:9]\n\t"
"v_mfma_f32_16x16x32_bf16 v[6:9], a[64:67], a[176:179], v[6:9]\n\t"
"v_mfma_f32_16x16x32_bf16 v[6:9], a[72:75], a[180:183], v[6:9]\n\t"
"s_mov_b32 s20, 49152\n\t"
"buffer_load_dwordx4 a[244:247], v252, s[8:11], s20 offen offset:0 sc1\n\t"
"s_waitcnt lgkmcnt(5)\n\t"
"v_mfma_f32_16x16x32_bf16 v[10:13], a[56:59], a[176:179], v[10:13]\n\t"
"v_mfma_f32_16x16x32_bf16 v[10:13], a[64:67], a[180:183], v[10:13]\n\t"
"v_mfma_f32_16x16x32_bf16 v[10:13], a[72:75], a[184:187], v[10:13]\n\t"
"buffer_load_dwordx4 a[248:251], v252, s[8:11], s20 offen offset:1024 sc1\n\t"
"s_waitcnt lgkmcnt(4)\n\t"
"v_mfma_f32_16x16x32_bf16 v[14:17], a[56:59], a[180:183], v[14:17]\n\t"
"v_mfma_f32_16x16x32_bf16 v[14:17], a[64:67], a[184:187], v[14:17]\n\t"
"v_mfma_f32_16x16x32_bf16 v[14:17], a[72:75], a[188:191], v[14:17]\n\t"
"buffer_load_dwordx4 a[252:255], v252, s[8:11], s20 offen offset:2048 sc1\n\t"
"s_waitcnt lgkmcnt(3)\n\t"
"v_mfma_f32_16x16x32_bf16 v[18:21], a[56:59], a[184:187], v[18:21]\n\t"
"v_xor_b32 v244, 0xc0, v244\n\t"
"v_mfma_f32_16x16x32_bf16 v[18:21], a[64:67], a[188:191], v[18:21]\n\t"
"v_mfma_f32_16x16x32_bf16 v[18:21], a[72:75], a[192:195], v[18:21]\n\t"
"s_waitcnt lgkmcnt(2)\n\t"
"v_mfma_f32_16x16x32_bf16 v[22:25], a[56:59], a[188:191], v[22:25]\n\t"
"v_mfma_f32_16x16x32_bf16 v[22:25], a[64:67], a[192:195], v[22:25]\n\t"
"v_mfma_f32_16x16x32_bf16 v[22:25], a[72:75], a[196:199], v[22:25]\n\t"
"s_waitcnt lgkmcnt(1)\n\t"
"v_mfma_f32_16x16x32_bf16 v[26:29], a[56:59], a[192:195], v[26:29]\n\t"
"v_mfma_f32_16x16x32_bf16 v[26:29], a[64:67], a[196:199], v[26:29]\n\t"
"v_mfma_f32_16x16x32_bf16 v[26:29], a[72:75], a[200:203], v[26:29]\n\t"
"s_waitcnt lgkmcnt(0)\n\t"
"v_mfma_f32_16x16x32_bf16 v[30:33], a[56:59], a[196:199], v[30:33]\n\t"
"v_mfma_f32_16x16x32_bf16 v[30:33], a[64:67], a[200:203], v[30:33]\n\t"
"v_mfma_f32_16x16x32_bf16 v[30:33], a[72:75], a[204:207], v[30:33]\n\t"
"s_waitcnt vmcnt(36)\n\t"
"v_mfma_f32_16x16x32_bf16 v[34:37], a[80:83], a[168:171], v[34:37]\n\t"
"v_mfma_f32_16x16x32_bf16 v[34:37], a[88:91], a[172:175], v[34:37]\n\t"
"v_mfma_f32_16x16x32_bf16 v[34:37], a[96:99], a[176:179], v[34:37]\n\t"
"ds_read_b64_tr_b16 v[222:223], v244 offset:0\n\t"
"ds_read_b64_tr_b16 v[224:225], v244 offset:3072\n\t"
"v_mfma_f32_16x16x32_bf16 v[38:41], a[80:83], a[172:175], v[38:41]\n\t"
"v_mfma_f32_16x16x32_bf16 v[38:41], a[88:91], a[176:179], v[38:41]\n\t"
"v_mfma_f32_16x16x32_bf16 v[38:41], a[96:99], a[180:183], v[38:41]\n\t"
"v_mfma_f32_16x16x32_bf16 v[42:45], a[80:83], a[176:179], v[42:45]\n\t"
"v_mfma_f32_16x16x32_bf16 v[42:45], a[88:91], a[180:183], v[42:45]\n\t"
"v_mfma_f32_16x16x32_bf16 v[42:45], a[96:99], a[184:187], v[42:45]\n\t"
"v_mfma_f32_16x16x32_bf16 v[46:49], a[80:83], a[180:183], v[46:49]\n\t"
"ds_read_b64_tr_b16 v[226:227], v244 offset:6144\n\t"
"v_mfma_f32_16x16x32_bf16 v[46:49], a[88:91], a[184:187], v[46:49]\n\t"
"ds_read_b64_tr_b16 v[228:229], v244 offset:9216\n\t"
"v_mfma_f32_16x16x32_bf16 v[46:49], a[96:99], a[188:191], v[46:49]\n\t"
"v_mfma_f32_16x16x32_bf16 v[50:53], a[80:83], a[184:187], v[50:53]\n\t"
"v_mfma_f32_16x16x32_bf16 v[50:53], a[88:91], a[188:191], v[50:53]\n\t"
"v_mfma_f32_16x16x32_bf16 v[50:53], a[96:99], a[192:195], v[50:53]\n\t"
"v_mfma_f32_16x16x32_bf16 v[54:57], a[80:83], a[188:191], v[54:57]\n\t"
"v_mfma_f32_16x16x32_bf16 v[54:57], a[88:91], a[192:195], v[54:57]\n\t"
"v_mfma_f32_16x16x32_bf16 v[54:57], a[96:99], a[196:199], v[54:57]\n\t"
"s_waitcnt lgkmcnt(2)\n\t"
"v_mfma_f32_16x16x32_bf16 v[222:225], v[222:225], v[218:221], 0\n\t"
"v_mfma_f32_16x16x32_bf16 v[58:61], a[80:83], a[192:195], v[58:61]\n\t"
"ds_read_b64_tr_b16 v[230:231], v244 offset:12288\n\t"
"v_mfma_f32_16x16x32_bf16 v[58:61], a[88:91], a[196:199], v[58:61]\n\t"
"ds_read_b64_tr_b16 v[232:233], v244 offset:15360\n\t"
"v_mfma_f32_16x16x32_bf16 v[58:61], a[96:99], a[200:203], v[58:61]\n\t"
"v_mfma_f32_16x16x32_bf16 v[62:65], a[80:83], a[196:199], v[62:65]\n\t"
"v_mfma_f32_16x16x32_bf16 v[62:65], a[88:91], a[200:203], v[62:65]\n\t"
"s_nop 0\n\t"
"v_cvt_pk_bf16_f32 v222, v222, v223\n\t"
"v_mfma_f32_16x16x32_bf16 v[62:65], a[96:99], a[204:207], v[62:65]\n\t"
"v_cvt_pk_bf16_f32 v223, v224, v225\n\t"
"s_waitcnt lgkmcnt(2)\n\t"
"v_mfma_f32_16x16x32_bf16 v[226:229], v[226:229], v[218:221], 0\n\t"
"s_waitcnt vmcnt(33)\n\t"
"v_mfma_f32_16x16x32_bf16 v[66:69], a[104:107], a[168:171], v[66:69]\n\t"
"ds_read_b64_tr_b16 v[234:235], v244 offset:19968\n\t"
"v_mfma_f32_16x16x32_bf16 v[66:69], a[112:115], a[172:175], v[66:69]\n\t"
"ds_read_b64_tr_b16 v[236:237], v244 offset:23040\n\t"
"v_mfma_f32_16x16x32_bf16 v[66:69], a[120:123], a[176:179], v[66:69]\n\t"
"v_mfma_f32_16x16x32_bf16 v[70:73], a[104:107], a[172:175], v[70:73]\n\t"
"ds_read_b128 a[168:171], v242 offset:9216\n\t"
"v_mfma_f32_16x16x32_bf16 v[70:73], a[112:115], a[176:179], v[70:73]\n\t"
"v_cvt_pk_bf16_f32 v226, v226, v227\n\t"
"v_mfma_f32_16x16x32_bf16 v[70:73], a[120:123], a[180:183], v[70:73]\n\t"
"v_cvt_pk_bf16_f32 v227, v228, v229\n\t"
"v_mfma_f32_16x16x32_bf16 v[74:77], a[104:107], a[176:179], v[74:77]\n\t"
"ds_read_b128 a[172:175], v242 offset:9728\n\t"
"v_mfma_f32_16x16x32_bf16 v[74:77], a[112:115], a[180:183], v[74:77]\n\t"
"ds_read_b128 a[176:179], v242 offset:10240\n\t"
"v_mfma_f32_16x16x32_bf16 v[74:77], a[120:123], a[184:187], v[74:77]\n\t"
"s_waitcnt lgkmcnt(5)\n\t"
"v_mfma_f32_16x16x32_bf16 v[230:233], v[230:233], v[218:221], 0\n\t"
"v_mfma_f32_16x16x32_bf16 v[78:81], a[104:107], a[180:183], v[78:81]\n\t"
"ds_read_b128 a[180:183], v242 offset:10752\n\t"
"v_mfma_f32_16x16x32_bf16 v[78:81], a[112:115], a[184:187], v[78:81]\n\t"
"v_mfma_f32_16x16x32_bf16 v[78:81], a[120:123], a[188:191], v[78:81]\n\t"
"v_mfma_f32_16x16x32_bf16 v[82:85], a[104:107], a[184:187], v[82:85]\n\t"
"ds_read_b128 a[184:187], v242 offset:11264\n\t"
"v_mfma_f32_16x16x32_bf16 v[82:85], a[112:115], a[188:191], v[82:85]\n\t"
"s_nop 0\n\t"
"v_cvt_pk_bf16_f32 v230, v230, v231\n\t"
"v_mfma_f32_16x16x32_bf16 v[82:85], a[120:123], a[192:195], v[82:85]\n\t"
"v_cvt_pk_bf16_f32 v231, v232, v233\n\t"
"s_waitcnt lgkmcnt(5)\n\t"
"v_mfma_f32_16x16x32_bf16 v[234:237], v[234:237], v[218:221], 0\n\t"
"v_mfma_f32_16x16x32_bf16 v[86:89], a[104:107], a[188:191], v[86:89]\n\t"
"ds_read_b128 a[188:191], v242 offset:11776\n\t"
"v_mfma_f32_16x16x32_bf16 v[86:89], a[112:115], a[192:195], v[86:89]\n\t"
"v_mfma_f32_16x16x32_bf16 v[86:89], a[120:123], a[196:199], v[86:89]\n\t"
"v_mfma_f32_16x16x32_bf16 v[90:93], a[104:107], a[192:195], v[90:93]\n\t"
"ds_read_b128 a[192:195], v242 offset:12288\n\t"
"v_mfma_f32_16x16x32_bf16 v[90:93], a[112:115], a[196:199], v[90:93]\n\t"
"s_nop 0\n\t"
"v_cvt_pk_bf16_f32 v234, v234, v235\n\t"
"v_mfma_f32_16x16x32_bf16 v[90:93], a[120:123], a[200:203], v[90:93]\n\t"
"v_cvt_pk_bf16_f32 v235, v236, v237\n\t"
"v_mfma_f32_16x16x32_bf16 v[94:97], a[104:107], a[196:199], v[94:97]\n\t"
"ds_read_b128 a[196:199], v242 offset:12800\n\t"
"v_mfma_f32_16x16x32_bf16 v[94:97], a[112:115], a[200:203], v[94:97]\n\t"
"ds_read_b128 a[200:203], v242 offset:13312\n\t"
"v_mfma_f32_16x16x32_bf16 v[94:97], a[120:123], a[204:207], v[94:97]\n\t"
"ds_read_b128 a[204:207], v242 offset:13824\n\t"
"s_waitcnt vmcnt(30) lgkmcnt(7)\n\t"
"v_mfma_f32_16x16x32_bf16 v[98:101], a[128:131], a[168:171], v[98:101]\n\t"
"v_mfma_f32_16x16x32_bf16 v[98:101], a[136:139], a[172:175], v[98:101]\n\t"
"v_mfma_f32_16x16x32_bf16 v[98:101], a[144:147], a[176:179], v[98:101]\n\t"
"s_waitcnt lgkmcnt(6)\n\t"
"v_mfma_f32_16x16x32_bf16 v[102:105], a[128:131], a[172:175], v[102:105]\n\t"
"v_mfma_f32_16x16x32_bf16 v[102:105], a[136:139], a[176:179], v[102:105]\n\t"
"v_mfma_f32_16x16x32_bf16 v[102:105], a[144:147], a[180:183], v[102:105]\n\t"
"s_waitcnt lgkmcnt(0)\n\t"
"s_barrier\n\t"
"v_mfma_f32_16x16x32_bf16 v[106:109], a[128:131], a[176:179], v[106:109]\n\t"
"s_nop 0\n\t"
"v_fma_f32 v194, -v2, s56, -v98\n\t"
"v_fma_f32 v195, -v3, s56, -v99\n\t"
"v_mfma_f32_16x16x32_bf16 v[106:109], a[136:139], a[180:183], v[106:109]\n\t"
"v_fma_f32 v196, -v4, s56, -v100\n\t"
"v_fma_f32 v197, -v5, s56, -v101\n\t"
"v_mfma_f32_16x16x32_bf16 v[106:109], a[144:147], a[184:187], v[106:109]\n\t"
"ds_write_b64 v243, v[222:223] offset:0\n\t"
"v_mfma_f32_16x16x32_bf16 v[110:113], a[128:131], a[180:183], v[110:113]\n\t"
"ds_read_b64_tr_b16 v[222:223], v244 offset:26112\n\t"
"v_fma_f32 v202, -v6, s56, -v102\n\t"
"v_mfma_f32_16x16x32_bf16 v[110:113], a[136:139], a[184:187], v[110:113]\n\t"
"ds_read_b64_tr_b16 v[224:225], v244 offset:29184\n\t"
"v_fma_f32 v203, -v7, s56, -v103\n\t"
"v_mfma_f32_16x16x32_bf16 v[110:113], a[144:147], a[188:191], v[110:113]\n\t"
"ds_write_b64 v243, v[226:227] offset:512\n\t"
"v_mfma_f32_16x16x32_bf16 v[114:117], a[128:131], a[184:187], v[114:117]\n\t"
"ds_read_b64_tr_b16 v[226:227], v244 offset:32256\n\t"
"v_fma_f32 v204, -v8, s56, -v104\n\t"
"v_mfma_f32_16x16x32_bf16 v[114:117], a[136:139], a[188:191], v[114:117]\n\t"
"ds_read_b64_tr_b16 v[228:229], v244 offset:35328\n\t"
"v_mfma_f32_16x16x32_bf16 v[114:117], a[144:147], a[192:195], v[114:117]\n\t"
"ds_write_b64 v243, v[230:231] offset:1024\n\t"
"ds_write_b64 v243, v[234:235] offset:1536\n\t"
"v_mfma_f32_16x16x32_bf16 v[118:121], a[128:131], a[188:191], v[118:121]\n\t"
"v_mfma_f32_16x16x32_bf16 v[118:121], a[136:139], a[192:195], v[118:121]\n\t"
"v_mfma_f32_16x16x32_bf16 v[118:121], a[144:147], a[196:199], v[118:121]\n\t"
"s_waitcnt lgkmcnt(5)\n\t"
"v_mfma_f32_16x16x32_bf16 v[222:225], v[222:225], v[218:221], 0\n\t"
"ds_read_b64_tr_b16 v[230:231], v244 offset:39936\n\t"
"ds_read_b64_tr_b16 v[232:233], v244 offset:43008\n\t"
"v_mfma_f32_16x16x32_bf16 v[122:125], a[128:131], a[192:195], v[122:125]\n\t"
"v_mfma_f32_16x16x32_bf16 v[122:125], a[136:139], a[196:199], v[122:125]\n\t"
"ds_read_b64_tr_b16 v[234:235], v244 offset:46080\n\t"
"v_mfma_f32_16x16x32_bf16 v[122:125], a[144:147], a[200:203], v[122:125]\n\t"
"ds_read_b64_tr_b16 v[236:237], v244 offset:49152\n\t"
"s_waitcnt lgkmcnt(6)\n\t"
"v_mfma_f32_16x16x32_bf16 v[226:229], v[226:229], v[218:221], 0\n\t"
"v_fma_f32 v205, -v9, s56, -v105\n\t"
"v_mfma_f32_16x16x32_bf16 v[126:129], a[128:131], a[196:199], v[126:129]\n\t"
"v_cvt_pk_bf16_f32 v222, v222, v223\n\t"
"v_cvt_pk_bf16_f32 v223, v224, v225\n\t"
"v_mfma_f32_16x16x32_bf16 v[126:129], a[136:139], a[200:203], v[126:129]\n\t"
"v_fma_f32 v210, -v10, s56, -v106\n\t"
"v_fma_f32 v211, -v11, s56, -v107\n\t"
"v_mfma_f32_16x16x32_bf16 v[126:129], a[144:147], a[204:207], v[126:129]\n\t"
"ds_write_b64 v243, v[222:223] offset:2048\n\t"
"s_waitcnt lgkmcnt(3)\n\t"
"v_mfma_f32_16x16x32_bf16 v[230:233], v[230:233], v[218:221], 0\n\t"
"ds_read_b64_tr_b16 v[222:223], v244 offset:52224\n\t"
"ds_read_b64_tr_b16 v[224:225], v244 offset:55296\n\t"
"s_waitcnt vmcnt(27)\n\t"
"v_mfma_f32_16x16x32_bf16 v[130:133], a[152:155], a[168:171], v[130:133]\n\t"
"v_cvt_pk_bf16_f32 v226, v226, v227\n\t"
"v_cvt_pk_bf16_f32 v227, v228, v229\n\t"
"v_mfma_f32_16x16x32_bf16 v[130:133], a[160:163], a[172:175], v[130:133]\n\t"
"v_fma_f32 v212, -v12, s56, -v108\n\t"
"v_fma_f32 v213, -v13, s56, -v109\n\t"
"v_mfma_f32_16x16x32_bf16 v[130:133], v[198:201], a[176:179], v[130:133]\n\t"
"ds_write_b64 v243, v[226:227] offset:2560\n\t"
"s_waitcnt lgkmcnt(4)\n\t"
"v_mfma_f32_16x16x32_bf16 v[234:237], v[234:237], v[218:221], 0\n\t"
"v_fma_f32 v250, -v14, s56, -v110\n\t"
"v_fma_f32 v251, -v15, s56, -v111\n\t"
"v_mfma_f32_16x16x32_bf16 v[134:137], a[152:155], a[172:175], v[134:137]\n\t"
"v_cvt_pk_bf16_f32 v230, v230, v231\n\t"
"v_cvt_pk_bf16_f32 v231, v232, v233\n\t"
"v_mfma_f32_16x16x32_bf16 v[134:137], a[160:163], a[176:179], v[134:137]\n\t"
"v_accvgpr_write_b32 a0, v250\n\t"
"v_fma_f32 v250, -v16, s56, -v112\n\t"
"v_mfma_f32_16x16x32_bf16 v[134:137], v[198:201], a[180:183], v[134:137]\n\t"
"ds_write_b64 v243, v[230:231] offset:3072\n\t"
"v_mfma_f32_16x16x32_bf16 v[138:141], a[152:155], a[176:179], v[138:141]\n\t"
"v_accvgpr_write_b32 a1, v251\n\t"
"v_fma_f32 v251, -v17, s56, -v113\n\t"
"v_mfma_f32_16x16x32_bf16 v[138:141], a[160:163], a[180:183], v[138:141]\n\t"
"v_cvt_pk_bf16_f32 v234, v234, v235\n\t"
"v_cvt_pk_bf16_f32 v235, v236, v237\n\t"
"v_mfma_f32_16x16x32_bf16 v[138:141], v[198:201], a[184:187], v[138:141]\n\t"
"ds_write_b64 v243, v[234:235] offset:3584\n\t"
"v_mfma_f32_16x16x32_bf16 v[142:145], a[152:155], a[180:183], v[142:145]\n\t"
"v_accvgpr_write_b32 a2, v250\n\t"
"v_accvgpr_write_b32 a3, v251\n\t"
"v_mfma_f32_16x16x32_bf16 v[142:145], a[160:163], a[184:187], v[142:145]\n\t"
"v_fma_f32 v250, -v18, s56, -v114\n\t"
"v_fma_f32 v251, -v19, s56, -v115\n\t"
"v_mfma_f32_16x16x32_bf16 v[142:145], v[198:201], a[188:191], v[142:145]\n\t"
"v_accvgpr_write_b32 a8, v250\n\t"
"v_fma_f32 v250, -v20, s56, -v116\n\t"
"s_waitcnt lgkmcnt(3)\n\t"
"v_mfma_f32_16x16x32_bf16 v[222:225], v[222:225], v[218:221], 0\n\t"
"v_accvgpr_write_b32 a9, v251\n\t"
"v_fma_f32 v251, -v21, s56, -v117\n\t"
"v_mfma_f32_16x16x32_bf16 v[146:149], a[152:155], a[184:187], v[146:149]\n\t"
"v_accvgpr_write_b32 a10, v250\n\t"
"v_accvgpr_write_b32 a11, v251\n\t"
"v_mfma_f32_16x16x32_bf16 v[146:149], a[160:163], a[188:191], v[146:149]\n\t"
"v_fma_f32 v250, -v22, s56, -v118\n\t"
"v_fma_f32 v251, -v23, s56, -v119\n\t"
"v_mfma_f32_16x16x32_bf16 v[146:149], v[198:201], a[192:195], v[146:149]\n\t"
"v_accvgpr_write_b32 a16, v250\n\t"
"v_fma_f32 v250, -v24, s56, -v120\n\t"
"v_mfma_f32_16x16x32_bf16 v[150:153], a[152:155], a[188:191], v[150:153]\n\t"
"v_accvgpr_write_b32 a17, v251\n\t"
"v_fma_f32 v251, -v25, s56, -v121\n\t"
"v_mfma_f32_16x16x32_bf16 v[150:153], a[160:163], a[192:195], v[150:153]\n\t"
"v_cvt_pk_bf16_f32 v222, v222, v223\n\t"
"v_cvt_pk_bf16_f32 v223, v224, v225\n\t"
"v_mfma_f32_16x16x32_bf16 v[150:153], v[198:201], a[196:199], v[150:153]\n\t"
"ds_write_b64 v243, v[222:223] offset:4096\n\t"
"v_accvgpr_write_b32 a18, v250\n\t"
"v_accvgpr_write_b32 a19, v251\n\t"
"v_mfma_f32_16x16x32_bf16 v[154:157], a[152:155], a[192:195], v[154:157]\n\t"
"v_fma_f32 v250, -v26, s56, -v122\n\t"
"v_fma_f32 v251, -v27, s56, -v123\n\t"
"v_mfma_f32_16x16x32_bf16 v[154:157], a[160:163], a[196:199], v[154:157]\n\t"
"v_accvgpr_write_b32 a24, v250\n\t"
"v_fma_f32 v250, -v28, s56, -v124\n\t"
"v_mfma_f32_16x16x32_bf16 v[154:157], v[198:201], a[200:203], v[154:157]\n\t"
"v_accvgpr_write_b32 a25, v251\n\t"
"v_fma_f32 v251, -v29, s56, -v125\n\t"
"v_accvgpr_write_b32 a26, v250\n\t"
"v_accvgpr_write_b32 a27, v251\n\t"
"v_mfma_f32_16x16x32_bf16 v[158:161], a[152:155], a[196:199], v[158:161]\n\t"
"v_fma_f32 v250, -v30, s56, -v126\n\t"
"v_fma_f32 v251, -v31, s56, -v127\n\t"
"v_mfma_f32_16x16x32_bf16 v[158:161], a[160:163], a[200:203], v[158:161]\n\t"
"v_accvgpr_write_b32 a32, v250\n\t"
"v_fma_f32 v250, -v32, s56, -v128\n\t"
"v_mfma_f32_16x16x32_bf16 v[158:161], v[198:201], a[204:207], v[158:161]\n\t"
"v_accvgpr_write_b32 a33, v251\n\t"
"v_fma_f32 v251, -v33, s56, -v129\n\t"
"v_accvgpr_write_b32 a34, v250\n\t"
"v_accvgpr_write_b32 a35, v251\n\t"
"s_waitcnt vmcnt(24)\n\t"
"v_mfma_f32_16x16x32_bf16 v[162:165], v[206:209], a[168:171], v[162:165]\n\t"
"v_fma_f32 v250, -v34, s56, -v130\n\t"
"v_fma_f32 v251, -v35, s56, -v131\n\t"
"v_mfma_f32_16x16x32_bf16 v[162:165], v[214:217], a[172:175], v[162:165]\n\t"
"v_accvgpr_write_b32 a40, v250\n\t"
"v_fma_f32 v250, -v36, s56, -v132\n\t"
"v_mfma_f32_16x16x32_bf16 v[162:165], a[4:7], a[176:179], v[162:165]\n\t"
"v_accvgpr_write_b32 a41, v251\n\t"
"v_fma_f32 v251, -v37, s56, -v133\n\t"
"v_accvgpr_write_b32 a42, v250\n\t"
"v_accvgpr_write_b32 a43, v251\n\t"
"s_waitcnt lgkmcnt(0)\n\t"
"s_barrier\n\t"
"v_mfma_f32_16x16x32_bf16 v[166:169], v[206:209], a[172:175], v[166:169]\n\t"
"ds_read_b128 a[168:171], v242 offset:0\n\t"
"v_fma_f32 v250, -v38, s56, -v134\n\t"
"v_mfma_f32_16x16x32_bf16 v[166:169], v[214:217], a[176:179], v[166:169]\n\t"
"ds_read_b128 a[172:175], v242 offset:512\n\t"
"v_fma_f32 v251, -v39, s56, -v135\n\t"
"v_mfma_f32_16x16x32_bf16 v[166:169], a[4:7], a[180:183], v[166:169]\n\t"
"v_accvgpr_write_b32 a48, v250\n\t"
"v_fma_f32 v250, -v40, s56, -v136\n\t"
"v_accvgpr_write_b32 a49, v251\n\t"
"v_fma_f32 v251, -v41, s56, -v137\n\t"
"v_mfma_f32_16x16x32_bf16 v[170:173], v[206:209], a[176:179], v[170:173]\n\t"
"ds_read_b128 a[176:179], v242 offset:1024\n\t"
"v_accvgpr_write_b32 a50, v250\n\t"
"v_mfma_f32_16x16x32_bf16 v[170:173], v[214:217], a[180:183], v[170:173]\n\t"
"v_accvgpr_write_b32 a51, v251\n\t"
"v_fma_f32 v250, -v42, s56, -v138\n\t"
"v_mfma_f32_16x16x32_bf16 v[170:173], a[4:7], a[184:187], v[170:173]\n\t"
"v_fma_f32 v251, -v43, s56, -v139\n\t"
"v_accvgpr_write_b32 a56, v250\n\t"
"v_fma_f32 v250, -v44, s56, -v140\n\t"
"v_accvgpr_write_b32 a57, v251\n\t"
"v_mfma_f32_16x16x32_bf16 v[174:177], v[206:209], a[180:183], v[174:177]\n\t"
"ds_read_b128 a[180:183], v242 offset:1536\n\t"
"v_fma_f32 v251, -v45, s56, -v141\n\t"
"v_mfma_f32_16x16x32_bf16 v[174:177], v[214:217], a[184:187], v[174:177]\n\t"
"v_accvgpr_write_b32 a58, v250\n\t"
"v_accvgpr_write_b32 a59, v251\n\t"
"v_mfma_f32_16x16x32_bf16 v[174:177], a[4:7], a[188:191], v[174:177]\n\t"
"v_fma_f32 v250, -v46, s56, -v142\n\t"
"v_fma_f32 v251, -v47, s56, -v143\n\t"
"v_accvgpr_write_b32 a64, v250\n\t"
"v_fma_f32 v250, -v48, s56, -v144\n\t"
"v_mfma_f32_16x16x32_bf16 v[178:181], v[206:209], a[184:187], v[178:181]\n\t"
"ds_read_b128 a[184:187], v242 offset:2048\n\t"
"v_accvgpr_write_b32 a65, v251\n\t"
"v_mfma_f32_16x16x32_bf16 v[178:181], v[214:217], a[188:191], v[178:181]\n\t"
"v_fma_f32 v251, -v49, s56, -v145\n\t"
"v_accvgpr_write_b32 a66, v250\n\t"
"v_mfma_f32_16x16x32_bf16 v[178:181], a[4:7], a[192:195], v[178:181]\n\t"
"v_accvgpr_write_b32 a67, v251\n\t"
"v_fma_f32 v250, -v50, s56, -v146\n\t"
"v_fma_f32 v251, -v51, s56, -v147\n\t"
"v_accvgpr_write_b32 a72, v250\n\t"
"v_mfma_f32_16x16x32_bf16 v[182:185], v[206:209], a[188:191], v[182:185]\n\t"
"ds_read_b128 a[188:191], v242 offset:2560\n\t"
"v_fma_f32 v250, -v52, s56, -v148\n\t"
"v_mfma_f32_16x16x32_bf16 v[182:185], v[214:217], a[192:195], v[182:185]\n\t"
"v_accvgpr_write_b32 a73, v251\n\t"
"v_fma_f32 v251, -v53, s56, -v149\n\t"
"v_mfma_f32_16x16x32_bf16 v[182:185], a[4:7], a[196:199], v[182:185]\n\t"
"v_accvgpr_write_b32 a74, v250\n\t"
"v_accvgpr_write_b32 a75, v251\n\t"
"v_fma_f32 v250, -v54, s56, -v150\n\t"
"v_fma_f32 v251, -v55, s56, -v151\n\t"
"v_mfma_f32_16x16x32_bf16 v[186:189], v[206:209], a[192:195], v[186:189]\n\t"
"ds_read_b128 a[192:195], v242 offset:3072\n\t"
"v_accvgpr_write_b32 a80, v250\n\t"
"v_mfma_f32_16x16x32_bf16 v[186:189], v[214:217], a[196:199], v[186:189]\n\t"
"v_fma_f32 v250, -v56, s56, -v152\n\t"
"v_accvgpr_write_b32 a81, v251\n\t"
"v_mfma_f32_16x16x32_bf16 v[186:189], a[4:7], a[200:203], v[186:189]\n\t"
"v_fma_f32 v251, -v57, s56, -v153\n\t"
"v_accvgpr_write_b32 a82, v250\n\t"
"v_accvgpr_write_b32 a83, v251\n\t"
"v_fma_f32 v250, -v58, s56, -v154\n\t"
"v_mfma_f32_16x16x32_bf16 v[190:193], v[206:209], a[196:199], v[190:193]\n\t"
"ds_read_b128 a[196:199], v242 offset:3584\n\t"
"v_fma_f32 v251, -v59, s56, -v155\n\t"
"v_mfma_f32_16x16x32_bf16 v[190:193], v[214:217], a[200:203], v[190:193]\n\t"
"ds_read_b128 a[200:203], v242 offset:4096\n\t"
"v_accvgpr_write_b32 a88, v250\n\t"
"v_mfma_f32_16x16x32_bf16 v[190:193], a[4:7], a[204:207], v[190:193]\n\t"
"ds_read_b128 a[204:207], v242 offset:4608\n\t"
"v_fma_f32 v250, -v60, s56, -v156\n\t"
"v_accvgpr_write_b32 a89, v251\n\t"
"v_fma_f32 v251, -v61, s56, -v157\n\t"
"s_waitcnt vmcnt(21) lgkmcnt(7)\n\t"
"v_mfma_f32_16x16x32_bf16 v[194:197], a[12:15], a[168:171], v[194:197]\n\t"
"v_accvgpr_write_b32 a90, v250\n\t"
"v_accvgpr_write_b32 a91, v251\n\t"
"v_mfma_f32_16x16x32_bf16 v[194:197], a[20:23], a[172:175], v[194:197]\n\t"
"v_fma_f32 v250, -v62, s56, -v158\n\t"
"v_fma_f32 v251, -v63, s56, -v159\n\t"
"v_mfma_f32_16x16x32_bf16 v[194:197], a[28:31], a[176:179], v[194:197]\n\t"
"v_accvgpr_write_b32 a96, v250\n\t"
"v_fma_f32 v250, -v64, s56, -v160\n\t"
"v_accvgpr_write_b32 a97, v251\n\t"
"v_fma_f32 v251, -v65, s56, -v161\n\t"
"s_waitcnt lgkmcnt(6)\n\t"
"v_mfma_f32_16x16x32_bf16 v[202:205], a[12:15], a[172:175], v[202:205]\n\t"
"v_accvgpr_write_b32 a98, v250\n\t"
"v_accvgpr_write_b32 a99, v251\n\t"
"v_mfma_f32_16x16x32_bf16 v[202:205], a[20:23], a[176:179], v[202:205]\n\t"
"v_fma_f32 v250, -v66, s56, -v162\n\t"
"v_fma_f32 v251, -v67, s56, -v163\n\t"
"v_mfma_f32_16x16x32_bf16 v[202:205], a[28:31], a[180:183], v[202:205]\n\t"
"v_accvgpr_write_b32 a104, v250\n\t"
"v_fma_f32 v250, -v68, s56, -v164\n\t"
"v_accvgpr_write_b32 a105, v251\n\t"
"v_fma_f32 v251, -v69, s56, -v165\n\t"
"s_waitcnt lgkmcnt(5)\n\t"
"v_mfma_f32_16x16x32_bf16 v[210:213], a[12:15], a[176:179], v[210:213]\n\t"
"v_accvgpr_write_b32 a106, v250\n\t"
"v_accvgpr_write_b32 a107, v251\n\t"
"v_mfma_f32_16x16x32_bf16 v[210:213], a[20:23], a[180:183], v[210:213]\n\t"
"v_fma_f32 v250, -v70, s56, -v166\n\t"
"v_fma_f32 v251, -v71, s56, -v167\n\t"
"v_mfma_f32_16x16x32_bf16 v[210:213], a[28:31], a[184:187], v[210:213]\n\t"
"v_accvgpr_write_b32 a112, v250\n\t"
"v_fma_f32 v250, -v72, s56, -v168\n\t"
"v_accvgpr_write_b32 a113, v251\n\t"
"v_fma_f32 v251, -v73, s56, -v169\n\t"
"s_waitcnt lgkmcnt(4)\n\t"
"v_mfma_f32_16x16x32_bf16 a[0:3], a[12:15], a[180:183], a[0:3]\n\t"
"v_accvgpr_write_b32 a114, v250\n\t"
"v_accvgpr_write_b32 a115, v251\n\t"
"v_mfma_f32_16x16x32_bf16 a[0:3], a[20:23], a[184:187], a[0:3]\n\t"
"v_fma_f32 v250, -v74, s56, -v170\n\t"
"v_fma_f32 v251, -v75, s56, -v171\n\t"
"v_mfma_f32_16x16x32_bf16 a[0:3], a[28:31], a[188:191], a[0:3]\n\t"
"v_accvgpr_write_b32 a120, v250\n\t"
"v_fma_f32 v250, -v76, s56, -v172\n\t"
"v_accvgpr_write_b32 a121, v251\n\t"
"v_fma_f32 v251, -v77, s56, -v173\n\t"
"s_waitcnt lgkmcnt(3)\n\t"
"v_mfma_f32_16x16x32_bf16 a[8:11], a[12:15], a[184:187], a[8:11]\n\t"
"v_xor_b32 v244, 0xc0, v244\n\t"
"v_accvgpr_write_b32 a122, v250\n\t"
"v_mfma_f32_16x16x32_bf16 a[8:11], a[20:23], a[188:191], a[8:11]\n\t"
"v_accvgpr_write_b32 a123, v251\n\t"
"v_fma_f32 v250, -v78, s56, -v174\n\t"
"v_mfma_f32_16x16x32_bf16 a[8:11], a[28:31], a[192:195], a[8:11]\n\t"
"v_fma_f32 v251, -v79, s56, -v175\n\t"
"v_accvgpr_write_b32 a128, v250\n\t"
"v_fma_f32 v250, -v80, s56, -v176\n\t"
"v_accvgpr_write_b32 a129, v251\n\t"
"s_waitcnt lgkmcnt(2)\n\t"
"v_mfma_f32_16x16x32_bf16 a[16:19], a[12:15], a[188:191], a[16:19]\n\t"
"v_fma_f32 v251, -v81, s56, -v177\n\t"
"v_accvgpr_write_b32 a130, v250\n\t"
"v_mfma_f32_16x16x32_bf16 a[16:19], a[20:23], a[192:195], a[16:19]\n\t"
"v_accvgpr_write_b32 a131, v251\n\t"
"v_fma_f32 v250, -v82, s56, -v178\n\t"
"v_mfma_f32_16x16x32_bf16 a[16:19], a[28:31], a[196:199], a[16:19]\n\t"
"v_fma_f32 v251, -v83, s56, -v179\n\t"
"v_accvgpr_write_b32 a136, v250\n\t"
"v_fma_f32 v250, -v84, s56, -v180\n\t"
"v_accvgpr_write_b32 a137, v251\n\t"
"s_waitcnt lgkmcnt(1)\n\t"
"v_mfma_f32_16x16x32_bf16 a[24:27], a[12:15], a[192:195], a[24:27]\n\t"
"v_fma_f32 v251, -v85, s56, -v181\n\t"
"v_accvgpr_write_b32 a138, v250\n\t"
"v_mfma_f32_16x16x32_bf16 a[24:27], a[20:23], a[196:199], a[24:27]\n\t"
"v_accvgpr_write_b32 a139, v251\n\t"
"v_fma_f32 v250, -v86, s56, -v182\n\t"
"v_mfma_f32_16x16x32_bf16 a[24:27], a[28:31], a[200:203], a[24:27]\n\t"
"v_fma_f32 v251, -v87, s56, -v183\n\t"
"v_accvgpr_write_b32 a144, v250\n\t"
"v_fma_f32 v250, -v88, s56, -v184\n\t"
"v_accvgpr_write_b32 a145, v251\n\t"
"s_waitcnt lgkmcnt(0)\n\t"
"v_mfma_f32_16x16x32_bf16 a[32:35], a[12:15], a[196:199], a[32:35]\n\t"
"v_fma_f32 v251, -v89, s56, -v185\n\t"
"v_accvgpr_write_b32 a146, v250\n\t"
"v_mfma_f32_16x16x32_bf16 a[32:35], a[20:23], a[200:203], a[32:35]\n\t"
"v_accvgpr_write_b32 a147, v251\n\t"
"v_fma_f32 v250, -v90, s56, -v186\n\t"
"v_mfma_f32_16x16x32_bf16 a[32:35], a[28:31], a[204:207], a[32:35]\n\t"
"v_fma_f32 v251, -v91, s56, -v187\n\t"
"v_accvgpr_write_b32 a152, v250\n\t"
"v_fma_f32 v250, -v92, s56, -v188\n\t"
"v_accvgpr_write_b32 a153, v251\n\t"
"s_waitcnt vmcnt(18)\n\t"
"v_mfma_f32_16x16x32_bf16 a[40:43], a[36:39], a[168:171], a[40:43]\n\t"
"v_fma_f32 v251, -v93, s56, -v189\n\t"
"v_accvgpr_write_b32 a154, v250\n\t"
"v_mfma_f32_16x16x32_bf16 a[40:43], a[44:47], a[172:175], a[40:43]\n\t"
"v_accvgpr_write_b32 a155, v251\n\t"
"v_fma_f32 v250, -v94, s56, -v190\n\t"
"v_mfma_f32_16x16x32_bf16 a[40:43], a[52:55], a[176:179], a[40:43]\n\t"
"ds_read_b64_tr_b16 v[226:227], v244 offset:0\n\t"
"ds_read_b64_tr_b16 v[228:229], v244 offset:3072\n\t"
"v_fma_f32 v251, -v95, s56, -v191\n\t"
"v_accvgpr_write_b32 a160, v250\n\t"
"v_mfma_f32_16x16x32_bf16 a[48:51], a[36:39], a[172:175], a[48:51]\n\t"
"v_fma_f32 v250, -v96, s56, -v192\n\t"
"v_accvgpr_write_b32 a161, v251\n\t"
"v_mfma_f32_16x16x32_bf16 a[48:51], a[44:47], a[176:179], a[48:51]\n\t"
"v_fma_f32 v251, -v97, s56, -v193\n\t"
"v_accvgpr_write_b32 a162, v250\n\t"
"v_mfma_f32_16x16x32_bf16 a[48:51], a[52:55], a[180:183], a[48:51]\n\t"
"v_accvgpr_write_b32 a163, v251\n\t"
"v_fma_f32 v198, v98, s64, -v2\n\t"
"v_fma_f32 v199, v99, s64, -v3\n\t"
"v_fma_f32 v200, v100, s64, -v4\n\t"
"v_mfma_f32_16x16x32_bf16 a[56:59], a[36:39], a[176:179], a[56:59]\n\t"
"v_fma_f32 v201, v101, s64, -v5\n\t"
"v_fma_f32 v206, v102, s64, -v6\n\t"
"v_mfma_f32_16x16x32_bf16 a[56:59], a[44:47], a[180:183], a[56:59]\n\t"
"v_fma_f32 v207, v103, s64, -v7\n\t"
"v_fma_f32 v208, v104, s64, -v8\n\t"
"v_mfma_f32_16x16x32_bf16 a[56:59], a[52:55], a[184:187], a[56:59]\n\t"
"v_fma_f32 v209, v105, s64, -v9\n\t"
"v_fma_f32 v214, v106, s64, -v10\n\t"
"v_fma_f32 v215, v107, s64, -v11\n\t"
"v_fma_f32 v216, v108, s64, -v12\n\t"
"v_mfma_f32_16x16x32_bf16 a[64:67], a[36:39], a[180:183], a[64:67]\n\t"
"ds_read_b64_tr_b16 v[230:231], v244 offset:6144\n\t"
"v_fma_f32 v217, v109, s64, -v13\n\t"
"v_mfma_f32_16x16x32_bf16 a[64:67], a[44:47], a[184:187], a[64:67]\n\t"
"ds_read_b64_tr_b16 v[232:233], v244 offset:9216\n\t"
"v_fma_f32 v250, v110, s64, -v14\n\t"
"v_mfma_f32_16x16x32_bf16 a[64:67], a[52:55], a[188:191], a[64:67]\n\t"
"v_fma_f32 v251, v111, s64, -v15\n\t"
"v_accvgpr_write_b32 a4, v250\n\t"
"v_fma_f32 v250, v112, s64, -v16\n\t"
"v_accvgpr_write_b32 a5, v251\n\t"
"v_mfma_f32_16x16x32_bf16 a[72:75], a[36:39], a[184:187], a[72:75]\n\t"
"v_fma_f32 v251, v113, s64, -v17\n\t"
"v_accvgpr_write_b32 a6, v250\n\t"
"v_mfma_f32_16x16x32_bf16 a[72:75], a[44:47], a[188:191], a[72:75]\n\t"
"v_accvgpr_write_b32 a7, v251\n\t"
"v_fma_f32 v250, v114, s64, -v18\n\t"
"v_mfma_f32_16x16x32_bf16 a[72:75], a[52:55], a[192:195], a[72:75]\n\t"
"v_fma_f32 v251, v115, s64, -v19\n\t"
"v_accvgpr_write_b32 a12, v250\n\t"
"v_fma_f32 v250, v116, s64, -v20\n\t"
"v_accvgpr_write_b32 a13, v251\n\t"
"v_mfma_f32_16x16x32_bf16 a[80:83], a[36:39], a[188:191], a[80:83]\n\t"
"v_fma_f32 v251, v117, s64, -v21\n\t"
"v_accvgpr_write_b32 a14, v250\n\t"
"v_mfma_f32_16x16x32_bf16 a[80:83], a[44:47], a[192:195], a[80:83]\n\t"
"v_accvgpr_write_b32 a15, v251\n\t"
"v_fma_f32 v250, v118, s64, -v22\n\t"
"v_mfma_f32_16x16x32_bf16 a[80:83], a[52:55], a[196:199], a[80:83]\n\t"
"v_fma_f32 v251, v119, s64, -v23\n\t"
"v_accvgpr_write_b32 a20, v250\n\t"
"v_fma_f32 v250, v120, s64, -v24\n\t"
"v_accvgpr_write_b32 a21, v251\n\t"
"s_waitcnt lgkmcnt(2)\n\t"
"v_mfma_f32_16x16x32_bf16 v[226:229], v[226:229], v[218:221], 0\n\t"
"v_fma_f32 v251, v121, s64, -v25\n\t"
"v_accvgpr_write_b32 a22, v250\n\t"
"v_accvgpr_write_b32 a23, v251\n\t"
"v_fma_f32 v250, v122, s64, -v26\n\t"
"v_mfma_f32_16x16x32_bf16 a[88:91], a[36:39], a[192:195], a[88:91]\n\t"
"ds_read_b64_tr_b16 v[234:235], v244 offset:12288\n\t"
"v_fma_f32 v251, v123, s64, -v27\n\t"
"v_mfma_f32_16x16x32_bf16 a[88:91], a[44:47], a[196:199], a[88:91]\n\t"
"ds_read_b64_tr_b16 v[236:237], v244 offset:15360\n\t"
"v_accvgpr_write_b32 a28, v250\n\t"
"v_mfma_f32_16x16x32_bf16 a[88:91], a[52:55], a[200:203], a[88:91]\n\t"
"v_fma_f32 v250, v124, s64, -v28\n\t"
"v_accvgpr_write_b32 a29, v251\n\t"
"v_fma_f32 v251, v125, s64, -v29\n\t"
"v_accvgpr_write_b32 a30, v250\n\t"
"v_mfma_f32_16x16x32_bf16 a[96:99], a[36:39], a[196:199], a[96:99]\n\t"
"v_accvgpr_write_b32 a31, v251\n\t"
"v_fma_f32 v250, v126, s64, -v30\n\t"
"v_mfma_f32_16x16x32_bf16 a[96:99], a[44:47], a[200:203], a[96:99]\n\t"
"v_cvt_pk_bf16_f32 v226, v226, v227\n\t"
"v_cvt_pk_bf16_f32 v227, v228, v229\n\t"
"v_mfma_f32_16x16x32_bf16 a[96:99], a[52:55], a[204:207], a[96:99]\n\t"
"v_fma_f32 v251, v127, s64, -v31\n\t"
"v_accvgpr_write_b32 a36, v250\n\t"
"v_fma_f32 v250, v128, s64, -v32\n\t"
"v_accvgpr_write_b32 a37, v251\n\t"
"s_waitcnt lgkmcnt(2)\n\t"
"v_mfma_f32_16x16x32_bf16 v[230:233], v[230:233], v[218:221], 0\n\t"
"v_fma_f32 v251, v129, s64, -v33\n\t"
"v_accvgpr_write_b32 a38, v250\n\t"
"v_accvgpr_write_b32 a39, v251\n\t"
"v_fma_f32 v250, v130, s64, -v34\n\t"
"s_waitcnt vmcnt(15)\n\t"
"v_mfma_f32_16x16x32_bf16 a[104:107], a[76:79], a[168:171], a[104:107]\n\t"
"ds_read_b64_tr_b16 v[222:223], v244 offset:19968\n\t"
"v_fma_f32 v251, v131, s64, -v35\n\t"
"v_mfma_f32_16x16x32_bf16 a[104:107], a[84:87], a[172:175], a[104:107]\n\t"
"ds_read_b64_tr_b16 v[224:225], v244 offset:23040\n\t"
"v_accvgpr_write_b32 a44, v250\n\t"
"v_mfma_f32_16x16x32_bf16 a[104:107], a[92:95], a[176:179], a[104:107]\n\t"
"ds_read_b128 a[168:171], v242 offset:9216\n\t"
"v_fma_f32 v250, v132, s64, -v36\n\t"
"v_accvgpr_write_b32 a45, v251\n\t"
"v_fma_f32 v251, v133, s64, -v37\n\t"
"v_mfma_f32_16x16x32_bf16 a[112:115], a[76:79], a[172:175], a[112:115]\n\t"
"ds_read_b128 a[172:175], v242 offset:9728\n\t"
"v_accvgpr_write_b32 a46, v250\n\t"
"v_mfma_f32_16x16x32_bf16 a[112:115], a[84:87], a[176:179], a[112:115]\n\t"
"v_cvt_pk_bf16_f32 v230, v230, v231\n\t"
"v_cvt_pk_bf16_f32 v231, v232, v233\n\t"
"v_mfma_f32_16x16x32_bf16 a[112:115], a[92:95], a[180:183], a[112:115]\n\t"
"v_accvgpr_write_b32 a47, v251\n\t"
"v_fma_f32 v250, v134, s64, -v38\n\t"
"v_fma_f32 v251, v135, s64, -v39\n\t"
"v_accvgpr_write_b32 a52, v250\n\t"
"v_mfma_f32_16x16x32_bf16 a[120:123], a[76:79], a[176:179], a[120:123]\n\t"
"ds_read_b128 a[176:179], v242 offset:10240\n\t"
"v_fma_f32 v250, v136, s64, -v40\n\t"
"v_mfma_f32_16x16x32_bf16 a[120:123], a[84:87], a[180:183], a[120:123]\n\t"
"v_accvgpr_write_b32 a53, v251\n\t"
"v_fma_f32 v251, v137, s64, -v41\n\t"
"v_mfma_f32_16x16x32_bf16 a[120:123], a[92:95], a[184:187], a[120:123]\n\t"
"v_accvgpr_write_b32 a54, v250\n\t"
"v_accvgpr_write_b32 a55, v251\n\t"
"v_fma_f32 v250, v138, s64, -v42\n\t"
"v_fma_f32 v251, v139, s64, -v43\n\t"
"s_waitcnt lgkmcnt(5)\n\t"
"v_mfma_f32_16x16x32_bf16 v[234:237], v[234:237], v[218:221], 0\n\t"
"v_accvgpr_write_b32 a60, v250\n\t"
"v_fma_f32 v250, v140, s64, -v44\n\t"
"v_accvgpr_write_b32 a61, v251\n\t"
"v_fma_f32 v251, v141, s64, -v45\n\t"
"v_mfma_f32_16x16x32_bf16 a[128:131], a[76:79], a[180:183], a[128:131]\n\t"
"ds_read_b128 a[180:183], v242 offset:10752\n\t"
"v_accvgpr_write_b32 a62, v250\n\t"
"v_mfma_f32_16x16x32_bf16 a[128:131], a[84:87], a[184:187], a[128:131]\n\t"
"v_accvgpr_write_b32 a63, v251\n\t"
"v_fma_f32 v250, v142, s64, -v46\n\t"
"v_mfma_f32_16x16x32_bf16 a[128:131], a[92:95], a[188:191], a[128:131]\n\t"
"v_fma_f32 v251, v143, s64, -v47\n\t"
"v_accvgpr_write_b32 a68, v250\n\t"
"v_fma_f32 v250, v144, s64, -v48\n\t"
"v_accvgpr_write_b32 a69, v251\n\t"
"v_mfma_f32_16x16x32_bf16 a[136:139], a[76:79], a[184:187], a[136:139]\n\t"
"ds_read_b128 a[184:187], v242 offset:11264\n\t"
"v_fma_f32 v251, v145, s64, -v49\n\t"
"v_mfma_f32_16x16x32_bf16 a[136:139], a[84:87], a[188:191], a[136:139]\n\t"
"v_cvt_pk_bf16_f32 v234, v234, v235\n\t"
"v_cvt_pk_bf16_f32 v235, v236, v237\n\t"
"v_mfma_f32_16x16x32_bf16 a[136:139], a[92:95], a[192:195], a[136:139]\n\t"
"v_accvgpr_write_b32 a70, v250\n\t"
"v_accvgpr_write_b32 a71, v251\n\t"
"v_fma_f32 v0, v2, s52, v98\n\t"
"v_fma_f32 v1, v3, s52, v99\n\t"
"s_waitcnt lgkmcnt(5)\n\t"
"v_mfma_f32_16x16x32_bf16 v[222:225], v[222:225], v[218:221], 0\n\t"
"v_fma_f32 v98, -v98, s66, v2\n\t"
"v_fma_f32 v2, v4, s52, v100\n\t"
"v_fma_f32 v99, -v99, s66, v3\n\t"
"v_fma_f32 v3, v5, s52, v101\n\t"
"v_mfma_f32_16x16x32_bf16 a[144:147], a[76:79], a[188:191], a[144:147]\n\t"
"ds_read_b128 a[188:191], v242 offset:11776\n\t"
"v_fma_f32 v100, -v100, s66, v4\n\t"
"v_mfma_f32_16x16x32_bf16 a[144:147], a[84:87], a[192:195], a[144:147]\n\t"
"v_fma_f32 v101, -v101, s66, v5\n\t"
"v_fma_f32 v4, v6, s52, v102\n\t"
"v_mfma_f32_16x16x32_bf16 a[144:147], a[92:95], a[196:199], a[144:147]\n\t"
"v_fma_f32 v5, v7, s52, v103\n\t"
"v_fma_f32 v102, -v102, s66, v6\n\t"
"v_fma_f32 v6, v8, s52, v104\n\t"
"v_fma_f32 v103, -v103, s66, v7\n\t"
"v_mfma_f32_16x16x32_bf16 a[152:155], a[76:79], a[192:195], a[152:155]\n\t"
"ds_read_b128 a[192:195], v242 offset:12288\n\t"
"v_fma_f32 v7, v9, s52, v105\n\t"
"v_mfma_f32_16x16x32_bf16 a[152:155], a[84:87], a[196:199], a[152:155]\n\t"
"v_cvt_pk_bf16_f32 v222, v222, v223\n\t"
"v_cvt_pk_bf16_f32 v223, v224, v225\n\t"
"v_mfma_f32_16x16x32_bf16 a[152:155], a[92:95], a[200:203], a[152:155]\n\t"
"v_fma_f32 v104, -v104, s66, v8\n\t"
"v_fma_f32 v105, -v105, s66, v9\n\t"
"v_fma_f32 v8, v10, s52, v106\n\t"
"v_fma_f32 v9, v11, s52, v107\n\t"
"v_mfma_f32_16x16x32_bf16 a[160:163], a[76:79], a[196:199], a[160:163]\n\t"
"ds_read_b128 a[196:199], v242 offset:12800\n\t"
"v_fma_f32 v250, v146, s64, -v50\n\t"
"v_mfma_f32_16x16x32_bf16 a[160:163], a[84:87], a[200:203], a[160:163]\n\t"
"ds_read_b128 a[200:203], v242 offset:13312\n\t"
"v_fma_f32 v251, v147, s64, -v51\n\t"
"v_mfma_f32_16x16x32_bf16 a[160:163], a[92:95], a[204:207], a[160:163]\n\t"
"ds_read_b128 a[204:207], v242 offset:13824\n\t"
"v_accvgpr_write_b32 a76, v250\n\t"
"v_fma_f32 v250, v148, s64, -v52\n\t"
"v_accvgpr_write_b32 a77, v251\n\t"
"s_waitcnt vmcnt(12) lgkmcnt(7)\n\t"
"v_mfma_f32_16x16x32_bf16 v[198:201], a[140:143], a[168:171], v[198:201]\n\t"
"v_fma_f32 v251, v149, s64, -v53\n\t"
"v_accvgpr_write_b32 a78, v250\n\t"
"v_mfma_f32_16x16x32_bf16 v[198:201], a[148:151], a[172:175], v[198:201]\n\t"
"v_accvgpr_write_b32 a79, v251\n\t"
"v_fma_f32 v250, v150, s64, -v54\n\t"
"v_mfma_f32_16x16x32_bf16 v[198:201], a[156:159], a[176:179], v[198:201]\n\t"
"v_fma_f32 v251, v151, s64, -v55\n\t"
"v_accvgpr_write_b32 a84, v250\n\t"
"v_fma_f32 v250, v152, s64, -v56\n\t"
"v_accvgpr_write_b32 a85, v251\n\t"
"s_waitcnt lgkmcnt(6)\n\t"
"v_mfma_f32_16x16x32_bf16 v[206:209], a[140:143], a[172:175], v[206:209]\n\t"
"v_fma_f32 v251, v153, s64, -v57\n\t"
"v_accvgpr_write_b32 a86, v250\n\t"
"v_mfma_f32_16x16x32_bf16 v[206:209], a[148:151], a[176:179], v[206:209]\n\t"
"v_accvgpr_write_b32 a87, v251\n\t"
"v_fma_f32 v250, v154, s64, -v58\n\t"
"v_mfma_f32_16x16x32_bf16 v[206:209], a[156:159], a[180:183], v[206:209]\n\t"
"v_fma_f32 v251, v155, s64, -v59\n\t"
"v_accvgpr_write_b32 a92, v250\n\t"
"v_fma_f32 v250, v156, s64, -v60\n\t"
"v_accvgpr_write_b32 a93, v251\n\t"
"s_waitcnt lgkmcnt(0)\n\t"
"s_barrier\n\t"
"v_mfma_f32_16x16x32_bf16 v[214:217], a[140:143], a[176:179], v[214:217]\n\t"
"v_fma_f32 v251, v157, s64, -v61\n\t"
"v_accvgpr_write_b32 a94, v250\n\t"
"v_mfma_f32_16x16x32_bf16 v[214:217], a[148:151], a[180:183], v[214:217]\n\t"
"v_accvgpr_write_b32 a95, v251\n\t"
"v_fma_f32 v250, v158, s64, -v62\n\t"
"v_mfma_f32_16x16x32_bf16 v[214:217], a[156:159], a[184:187], v[214:217]\n\t"
"ds_write_b64 v243, v[226:227] offset:0\n\t"
"ds_write_b64 v243, v[230:231] offset:512\n\t"
"v_mfma_f32_16x16x32_bf16 a[4:7], a[140:143], a[180:183], a[4:7]\n\t"
"ds_read_b64_tr_b16 v[226:227], v244 offset:26112\n\t"
"v_fma_f32 v251, v159, s64, -v63\n\t"
"v_mfma_f32_16x16x32_bf16 a[4:7], a[148:151], a[184:187], a[4:7]\n\t"
"ds_read_b64_tr_b16 v[228:229], v244 offset:29184\n\t"
"v_accvgpr_write_b32 a100, v250\n\t"
"v_mfma_f32_16x16x32_bf16 a[4:7], a[156:159], a[188:191], a[4:7]\n\t"
"ds_write_b64 v243, v[234:235] offset:1024\n\t"
"ds_write_b64 v243, v[222:223] offset:1536\n\t"
"v_mfma_f32_16x16x32_bf16 a[12:15], a[140:143], a[184:187], a[12:15]\n\t"
"ds_read_b64_tr_b16 v[230:231], v244 offset:32256\n\t"
"v_fma_f32 v250, v160, s64, -v64\n\t"
"v_mfma_f32_16x16x32_bf16 a[12:15], a[148:151], a[188:191], a[12:15]\n\t"
"ds_read_b64_tr_b16 v[232:233], v244 offset:35328\n\t"
"v_accvgpr_write_b32 a101, v251\n\t"
"v_mfma_f32_16x16x32_bf16 a[12:15], a[156:159], a[192:195], a[12:15]\n\t"
"ds_read_b64_tr_b16 v[234:235], v244 offset:39936\n\t"
"ds_read_b64_tr_b16 v[236:237], v244 offset:43008\n\t"
"ds_read_b64_tr_b16 v[222:223], v244 offset:46080\n\t"
"v_fma_f32 v251, v161, s64, -v65\n\t"
"v_mfma_f32_16x16x32_bf16 a[20:23], a[140:143], a[188:191], a[20:23]\n\t"
"ds_read_b64_tr_b16 v[224:225], v244 offset:49152\n\t"
"v_accvgpr_write_b32 a102, v250\n\t"
"v_mfma_f32_16x16x32_bf16 a[20:23], a[148:151], a[192:195], a[20:23]\n\t"
"v_accvgpr_write_b32 a103, v251\n\t"
"v_fma_f32 v250, v162, s64, -v66\n\t"
"v_mfma_f32_16x16x32_bf16 a[20:23], a[156:159], a[196:199], a[20:23]\n\t"
"v_fma_f32 v251, v163, s64, -v67\n\t"
"v_accvgpr_write_b32 a108, v250\n\t"
"v_fma_f32 v250, v164, s64, -v68\n\t"
"v_accvgpr_write_b32 a109, v251\n\t"
"s_waitcnt lgkmcnt(8)\n\t"
"v_mfma_f32_16x16x32_bf16 v[226:229], v[226:229], v[218:221], 0\n\t"
"v_fma_f32 v251, v165, s64, -v69\n\t"
"v_accvgpr_write_b32 a110, v250\n\t"
"v_accvgpr_write_b32 a111, v251\n\t"
"v_fma_f32 v250, v166, s64, -v70\n\t"
"v_mfma_f32_16x16x32_bf16 a[28:31], a[140:143], a[192:195], a[28:31]\n\t"
"v_fma_f32 v251, v167, s64, -v71\n\t"
"v_accvgpr_write_b32 a116, v250\n\t"
"v_mfma_f32_16x16x32_bf16 a[28:31], a[148:151], a[196:199], a[28:31]\n\t"
"v_fma_f32 v250, v168, s64, -v72\n\t"
"v_accvgpr_write_b32 a117, v251\n\t"
"v_mfma_f32_16x16x32_bf16 a[28:31], a[156:159], a[200:203], a[28:31]\n\t"
"v_fma_f32 v251, v169, s64, -v73\n\t"
"v_accvgpr_write_b32 a118, v250\n\t"
"v_accvgpr_write_b32 a119, v251\n\t"
"v_fma_f32 v250, v170, s64, -v74\n\t"
"s_waitcnt lgkmcnt(4)\n\t"
"v_mfma_f32_16x16x32_bf16 v[230:233], v[230:233], v[218:221], 0\n\t"
"v_fma_f32 v251, v171, s64, -v75\n\t"
"v_accvgpr_write_b32 a124, v250\n\t"
"v_fma_f32 v250, v172, s64, -v76\n\t"
"v_accvgpr_write_b32 a125, v251\n\t"
"v_mfma_f32_16x16x32_bf16 a[36:39], a[140:143], a[196:199], a[36:39]\n\t"
"v_cvt_pk_bf16_f32 v226, v226, v227\n\t"
"v_cvt_pk_bf16_f32 v227, v228, v229\n\t"
"v_mfma_f32_16x16x32_bf16 a[36:39], a[148:151], a[200:203], a[36:39]\n\t"
"v_fma_f32 v251, v173, s64, -v77\n\t"
"v_accvgpr_write_b32 a126, v250\n\t"
"v_mfma_f32_16x16x32_bf16 a[36:39], a[156:159], a[204:207], a[36:39]\n\t"
"ds_write_b64 v243, v[226:227] offset:2048\n\t"
"ds_read_b64_tr_b16 v[226:227], v244 offset:52224\n\t"
"ds_read_b64_tr_b16 v[228:229], v244 offset:55296\n\t"
"s_waitcnt lgkmcnt(5)\n\t"
"v_mfma_f32_16x16x32_bf16 v[234:237], v[234:237], v[218:221], 0\n\t"
"v_accvgpr_write_b32 a127, v251\n\t"
"v_fma_f32 v250, v174, s64, -v78\n\t"
"v_fma_f32 v251, v175, s64, -v79\n\t"
"v_accvgpr_write_b32 a132, v250\n\t"
"s_waitcnt vmcnt(9)\n\t"
"v_mfma_f32_16x16x32_bf16 a[44:47], a[208:211], a[168:171], a[44:47]\n\t"
"v_cvt_pk_bf16_f32 v230, v230, v231\n\t"
"v_cvt_pk_bf16_f32 v231, v232, v233\n\t"
"v_mfma_f32_16x16x32_bf16 a[44:47], a[212:215], a[172:175], a[44:47]\n\t"
"v_fma_f32 v250, v176, s64, -v80\n\t"
"v_accvgpr_write_b32 a133, v251\n\t"
"v_mfma_f32_16x16x32_bf16 a[44:47], a[216:219], a[176:179], a[44:47]\n\t"
"ds_write_b64 v243, v[230:231] offset:2560\n\t"
"v_fma_f32 v251, v177, s64, -v81\n\t"
"v_accvgpr_write_b32 a134, v250\n\t"
"s_waitcnt lgkmcnt(4)\n\t"
"v_mfma_f32_16x16x32_bf16 v[222:225], v[222:225], v[218:221], 0\n\t"
"v_accvgpr_write_b32 a135, v251\n\t"
"v_fma_f32 v250, v178, s64, -v82\n\t"
"v_fma_f32 v251, v179, s64, -v83\n\t"
"v_accvgpr_write_b32 a140, v250\n\t"
"v_mfma_f32_16x16x32_bf16 a[52:55], a[208:211], a[172:175], a[52:55]\n\t"
"v_cvt_pk_bf16_f32 v234, v234, v235\n\t"
"v_cvt_pk_bf16_f32 v235, v236, v237\n\t"
"v_mfma_f32_16x16x32_bf16 a[52:55], a[212:215], a[176:179], a[52:55]\n\t"
"v_fma_f32 v250, v180, s64, -v84\n\t"
"v_accvgpr_write_b32 a141, v251\n\t"
"v_mfma_f32_16x16x32_bf16 a[52:55], a[216:219], a[180:183], a[52:55]\n\t"
"ds_write_b64 v243, v[234:235] offset:3072\n\t"
"v_fma_f32 v251, v181, s64, -v85\n\t"
"v_accvgpr_write_b32 a142, v250\n\t"
"v_mfma_f32_16x16x32_bf16 a[60:63], a[208:211], a[176:179], a[60:63]\n\t"
"v_accvgpr_write_b32 a143, v251\n\t"
"v_fma_f32 v250, v182, s64, -v86\n\t"
"v_mfma_f32_16x16x32_bf16 a[60:63], a[212:215], a[180:183], a[60:63]\n\t"
"v_cvt_pk_bf16_f32 v222, v222, v223\n\t"
"v_cvt_pk_bf16_f32 v223, v224, v225\n\t"
"v_mfma_f32_16x16x32_bf16 a[60:63], a[216:219], a[184:187], a[60:63]\n\t"
"ds_write_b64 v243, v[222:223] offset:3584\n\t"
"v_fma_f32 v251, v183, s64, -v87\n\t"
"v_accvgpr_write_b32 a148, v250\n\t"
"v_mfma_f32_16x16x32_bf16 a[68:71], a[208:211], a[180:183], a[68:71]\n\t"
"v_fma_f32 v250, v184, s64, -v88\n\t"
"v_accvgpr_write_b32 a149, v251\n\t"
"v_mfma_f32_16x16x32_bf16 a[68:71], a[212:215], a[184:187], a[68:71]\n\t"
"v_fma_f32 v251, v185, s64, -v89\n\t"
"v_accvgpr_write_b32 a150, v250\n\t"
"v_mfma_f32_16x16x32_bf16 a[68:71], a[216:219], a[188:191], a[68:71]\n\t"
"v_accvgpr_write_b32 a151, v251\n\t"
"v_fma_f32 v250, v186, s64, -v90\n\t"
"v_fma_f32 v251, v187, s64, -v91\n\t"
"v_accvgpr_write_b32 a156, v250\n\t"
"s_waitcnt lgkmcnt(3)\n\t"
"v_mfma_f32_16x16x32_bf16 v[226:229], v[226:229], v[218:221], 0\n\t"
"v_fma_f32 v250, v188, s64, -v92\n\t"
"v_accvgpr_write_b32 a157, v251\n\t"
"v_fma_f32 v251, v189, s64, -v93\n\t"
"v_accvgpr_write_b32 a158, v250\n\t"
"v_mfma_f32_16x16x32_bf16 a[76:79], a[208:211], a[184:187], a[76:79]\n\t"
"v_accvgpr_write_b32 a159, v251\n\t"
"v_fma_f32 v250, v190, s64, -v94\n\t"
"v_mfma_f32_16x16x32_bf16 a[76:79], a[212:215], a[188:191], a[76:79]\n\t"
"v_fma_f32 v251, v191, s64, -v95\n\t"
"v_accvgpr_write_b32 a164, v250\n\t"
"v_mfma_f32_16x16x32_bf16 a[76:79], a[216:219], a[192:195], a[76:79]\n\t"
"v_fma_f32 v250, v192, s64, -v96\n\t"
"v_accvgpr_write_b32 a165, v251\n\t"
"v_fma_f32 v251, v193, s64, -v97\n\t"
"v_accvgpr_write_b32 a166, v250\n\t"
"v_mfma_f32_16x16x32_bf16 a[84:87], a[208:211], a[188:191], a[84:87]\n\t"
"v_accvgpr_write_b32 a167, v251\n\t"
"v_fma_f32 v106, -v106, s66, v10\n\t"
"v_mfma_f32_16x16x32_bf16 a[84:87], a[212:215], a[192:195], a[84:87]\n\t"
"v_cvt_pk_bf16_f32 v226, v226, v227\n\t"
"v_cvt_pk_bf16_f32 v227, v228, v229\n\t"
"v_mfma_f32_16x16x32_bf16 a[84:87], a[216:219], a[196:199], a[84:87]\n\t"
"ds_write_b64 v243, v[226:227] offset:4096\n\t"
"v_fma_f32 v10, v12, s52, v108\n\t"
"v_fma_f32 v107, -v107, s66, v11\n\t"
"v_mfma_f32_16x16x32_bf16 a[92:95], a[208:211], a[192:195], a[92:95]\n\t"
"v_fma_f32 v11, v13, s52, v109\n\t"
"v_fma_f32 v108, -v108, s66, v12\n\t"
"v_mfma_f32_16x16x32_bf16 a[92:95], a[212:215], a[196:199], a[92:95]\n\t"
"v_fma_f32 v109, -v109, s66, v13\n\t"
"v_fma_f32 v12, v14, s52, v110\n\t"
"v_mfma_f32_16x16x32_bf16 a[92:95], a[216:219], a[200:203], a[92:95]\n\t"
"v_fma_f32 v13, v15, s52, v111\n\t"
"v_fma_f32 v110, -v110, s66, v14\n\t"
"v_fma_f32 v14, v16, s52, v112\n\t"
"v_fma_f32 v111, -v111, s66, v15\n\t"
"v_mfma_f32_16x16x32_bf16 a[100:103], a[208:211], a[196:199], a[100:103]\n\t"
"v_fma_f32 v15, v17, s52, v113\n\t"
"v_fma_f32 v112, -v112, s66, v16\n\t"
"v_mfma_f32_16x16x32_bf16 a[100:103], a[212:215], a[200:203], a[100:103]\n\t"
"v_fma_f32 v113, -v113, s66, v17\n\t"
"v_fma_f32 v16, v18, s52, v114\n\t"
"v_mfma_f32_16x16x32_bf16 a[100:103], a[216:219], a[204:207], a[100:103]\n\t"
"s_mov_b32 s20, 61440\n\t"
"buffer_load_dwordx4 a[208:211], v252, s[8:11], s20 offen offset:0 sc1\n\t"
"v_fma_f32 v17, v19, s52, v115\n\t"
"v_fma_f32 v114, -v114, s66, v18\n\t"
"s_waitcnt vmcnt(7)\n\t"
"v_mfma_f32_16x16x32_bf16 a[108:111], a[220:223], a[168:171], a[108:111]\n\t"
"v_fma_f32 v18, v20, s52, v116\n\t"
"v_fma_f32 v115, -v115, s66, v19\n\t"
"v_mfma_f32_16x16x32_bf16 a[108:111], a[224:227], a[172:175], a[108:111]\n\t"
"v_fma_f32 v19, v21, s52, v117\n\t"
"v_fma_f32 v116, -v116, s66, v20\n\t"
"v_mfma_f32_16x16x32_bf16 a[108:111], a[228:231], a[176:179], a[108:111]\n\t"
"buffer_load_dwordx4 a[212:215], v252, s[8:11], s20 offen offset:1024 sc1\n\t"
"v_fma_f32 v117, -v117, s66, v21\n\t"
"v_fma_f32 v20, v22, s52, v118\n\t"
"s_waitcnt lgkmcnt(0)\n\t"
"s_barrier\n\t"
"v_mfma_f32_16x16x32_bf16 a[116:119], a[220:223], a[172:175], a[116:119]\n\t"
"ds_read_b128 a[168:171], v242 offset:0\n\t"
"v_fma_f32 v21, v23, s52, v119\n\t"
"v_mfma_f32_16x16x32_bf16 a[116:119], a[224:227], a[176:179], a[116:119]\n\t"
"ds_read_b128 a[172:175], v242 offset:512\n\t"
"v_fma_f32 v118, -v118, s66, v22\n\t"
"v_mfma_f32_16x16x32_bf16 a[116:119], a[228:231], a[180:183], a[116:119]\n\t"
"s_add_u32 m0, s25, 896\n\t"
"s_nop 0\n\t"
"buffer_load_dwordx4 v246, s[12:15], 0 offen offset:128 nt lds\n\t"
"s_add_u32 m0, s26, 896\n\t"
"s_mov_b64 exec, s[36:37]\n\t"
"buffer_load_dwordx4 v246, s[12:15], s27 offen offset:128 nt lds\n\t"
"s_mov_b64 exec, -1\n\t"
"v_mfma_f32_16x16x32_bf16 a[124:127], a[220:223], a[176:179], a[124:127]\n\t"
"ds_read_b128 a[176:179], v242 offset:1024\n\t"
"v_fma_f32 v22, v24, s52, v120\n\t"
"v_mfma_f32_16x16x32_bf16 a[124:127], a[224:227], a[180:183], a[124:127]\n\t"
"v_fma_f32 v119, -v119, s66, v23\n\t"
"v_fma_f32 v23, v25, s52, v121\n\t"
"v_mfma_f32_16x16x32_bf16 a[124:127], a[228:231], a[184:187], a[124:127]\n\t"
"s_add_u32 m0, s24, 896\n\t"
"s_add_u32 s28, s21, 0\n\t"
"buffer_load_dwordx4 v245, s[12:15], s28 offen offset:128 nt lds\n\t"
"v_fma_f32 v120, -v120, s66, v24\n\t"
"v_fma_f32 v121, -v121, s66, v25\n\t"
"v_mfma_f32_16x16x32_bf16 a[132:135], a[220:223], a[180:183], a[132:135]\n\t"
"ds_read_b128 a[180:183], v242 offset:1536\n\t"
"v_fma_f32 v24, v26, s52, v122\n\t"
"v_mfma_f32_16x16x32_bf16 a[132:135], a[224:227], a[184:187], a[132:135]\n\t"
"v_fma_f32 v25, v27, s52, v123\n\t"
"v_fma_f32 v122, -v122, s66, v26\n\t"
"v_mfma_f32_16x16x32_bf16 a[132:135], a[228:231], a[188:191], a[132:135]\n\t"
"s_add_u32 m0, s24, 7040\n\t"
"s_add_u32 s28, s21, 196608\n\t"
"buffer_load_dwordx4 v245, s[12:15], s28 offen offset:128 nt lds\n\t"
"s_add_u32 m0, s24, 13184\n\t"
"s_add_u32 s28, s21, 393216\n\t"
"buffer_load_dwordx4 v245, s[12:15], s28 offen offset:128 nt lds\n\t"
"v_mfma_f32_16x16x32_bf16 a[140:143], a[220:223], a[184:187], a[140:143]\n\t"
"ds_read_b128 a[184:187], v242 offset:2048\n\t"
"v_fma_f32 v26, v28, s52, v124\n\t"
"v_mfma_f32_16x16x32_bf16 a[140:143], a[224:227], a[188:191], a[140:143]\n\t"
"v_fma_f32 v123, -v123, s66, v27\n\t"
"v_fma_f32 v27, v29, s52, v125\n\t"
"v_mfma_f32_16x16x32_bf16 a[140:143], a[228:231], a[192:195], a[140:143]\n\t"
"s_add_u32 m0, s24, 20864\n\t"
"s_add_u32 s28, s21, 589824\n\t"
"buffer_load_dwordx4 v245, s[12:15], s28 offen offset:128 nt lds\n\t"
"v_fma_f32 v124, -v124, s66, v28\n\t"
"v_fma_f32 v125, -v125, s66, v29\n\t"
"v_mfma_f32_16x16x32_bf16 a[148:151], a[220:223], a[188:191], a[148:151]\n\t"
"ds_read_b128 a[188:191], v242 offset:2560\n\t"
"v_fma_f32 v28, v30, s52, v126\n\t"
"v_mfma_f32_16x16x32_bf16 a[148:151], a[224:227], a[192:195], a[148:151]\n\t"
"v_fma_f32 v29, v31, s52, v127\n\t"
"v_fma_f32 v126, -v126, s66, v30\n\t"
"v_mfma_f32_16x16x32_bf16 a[148:151], a[228:231], a[196:199], a[148:151]\n\t"
"s_add_u32 m0, s24, 27008\n\t"
"s_add_u32 s28, s21, 786432\n\t"
"buffer_load_dwordx4 v245, s[12:15], s28 offen offset:128 nt lds\n\t"
"v_fma_f32 v30, v32, s52, v128\n\t"
"v_fma_f32 v127, -v127, s66, v31\n\t"
"v_mfma_f32_16x16x32_bf16 a[156:159], a[220:223], a[192:195], a[156:159]\n\t"
"ds_read_b128 a[192:195], v242 offset:3072\n\t"
"v_fma_f32 v31, v33, s52, v129\n\t"
"v_mfma_f32_16x16x32_bf16 a[156:159], a[224:227], a[196:199], a[156:159]\n\t"
"v_fma_f32 v128, -v128, s66, v32\n\t"
"v_fma_f32 v129, -v129, s66, v33\n\t"
"v_mfma_f32_16x16x32_bf16 a[156:159], a[228:231], a[200:203], a[156:159]\n\t"
"v_fma_f32 v32, v34, s52, v130\n\t"
"v_fma_f32 v33, v35, s52, v131\n\t"
"v_fma_f32 v130, -v130, s66, v34\n\t"
"v_fma_f32 v34, v36, s52, v132\n\t"
"v_mfma_f32_16x16x32_bf16 a[164:167], a[220:223], a[196:199], a[164:167]\n\t"
"ds_read_b128 a[196:199], v242 offset:3584\n\t"
"v_fma_f32 v131, -v131, s66, v35\n\t"
"v_mfma_f32_16x16x32_bf16 a[164:167], a[224:227], a[200:203], a[164:167]\n\t"
"ds_read_b128 a[200:203], v242 offset:4096\n\t"
"v_fma_f32 v35, v37, s52, v133\n\t"
"v_mfma_f32_16x16x32_bf16 a[164:167], a[228:231], a[204:207], a[164:167]\n\t"
"ds_read_b128 a[204:207], v242 offset:4608\n\t"
"v_fma_f32 v132, -v132, s66, v36\n\t"
"v_fma_f32 v133, -v133, s66, v37\n\t"
"v_fma_f32 v36, v38, s52, v134\n\t"
"s_waitcnt vmcnt(12) lgkmcnt(7)\n\t"
"v_mfma_f32_16x16x32_bf16 v[0:3], a[232:235], a[168:171], v[0:3]\n\t"
"v_fma_f32 v37, v39, s52, v135\n\t"
"v_fma_f32 v134, -v134, s66, v38\n\t"
"v_mfma_f32_16x16x32_bf16 v[0:3], a[236:239], a[172:175], v[0:3]\n\t"
"v_fma_f32 v38, v40, s52, v136\n\t"
"v_fma_f32 v135, -v135, s66, v39\n\t"
"v_mfma_f32_16x16x32_bf16 v[0:3], a[240:243], a[176:179], v[0:3]\n\t"
"v_fma_f32 v39, v41, s52, v137\n\t"
"v_fma_f32 v136, -v136, s66, v40\n\t"
"v_fma_f32 v137, -v137, s66, v41\n\t"
"v_fma_f32 v40, v42, s52, v138\n\t"
"s_waitcnt lgkmcnt(6)\n\t"
"v_mfma_f32_16x16x32_bf16 v[4:7], a[232:235], a[172:175], v[4:7]\n\t"
"v_fma_f32 v41, v43, s52, v139\n\t"
"v_fma_f32 v138, -v138, s66, v42\n\t"
"v_mfma_f32_16x16x32_bf16 v[4:7], a[236:239], a[176:179], v[4:7]\n\t"
"v_fma_f32 v42, v44, s52, v140\n\t"
"v_fma_f32 v139, -v139, s66, v43\n\t"
"v_mfma_f32_16x16x32_bf16 v[4:7], a[240:243], a[180:183], v[4:7]\n\t"
"v_fma_f32 v43, v45, s52, v141\n\t"
"v_fma_f32 v140, -v140, s66, v44\n\t"
"v_fma_f32 v141, -v141, s66, v45\n\t"
"v_fma_f32 v44, v46, s52, v142\n\t"
"s_waitcnt lgkmcnt(5)\n\t"
"v_mfma_f32_16x16x32_bf16 v[8:11], a[232:235], a[176:179], v[8:11]\n\t"
"v_fma_f32 v45, v47, s52, v143\n\t"
"v_fma_f32 v142, -v142, s66, v46\n\t"
"v_mfma_f32_16x16x32_bf16 v[8:11], a[236:239], a[180:183], v[8:11]\n\t"
"v_fma_f32 v46, v48, s52, v144\n\t"
"v_fma_f32 v143, -v143, s66, v47\n\t"
"v_mfma_f32_16x16x32_bf16 v[8:11], a[240:243], a[184:187], v[8:11]\n\t"
"v_fma_f32 v47, v49, s52, v145\n\t"
"v_fma_f32 v144, -v144, s66, v48\n\t"
"v_fma_f32 v145, -v145, s66, v49\n\t"
"v_fma_f32 v48, v50, s52, v146\n\t"
"s_waitcnt lgkmcnt(4)\n\t"
"v_mfma_f32_16x16x32_bf16 v[12:15], a[232:235], a[180:183], v[12:15]\n\t"
"v_fma_f32 v49, v51, s52, v147\n\t"
"v_fma_f32 v146, -v146, s66, v50\n\t"
"v_mfma_f32_16x16x32_bf16 v[12:15], a[236:239], a[184:187], v[12:15]\n\t"
"v_fma_f32 v50, v52, s52, v148\n\t"
"v_fma_f32 v147, -v147, s66, v51\n\t"
"v_mfma_f32_16x16x32_bf16 v[12:15], a[240:243], a[188:191], v[12:15]\n\t"
"buffer_load_dwordx4 a[216:219], v252, s[8:11], s20 offen offset:2048 sc1\n\t"
"v_fma_f32 v51, v53, s52, v149\n\t"
"v_fma_f32 v148, -v148, s66, v52\n\t"
"s_waitcnt lgkmcnt(3)\n\t"
"v_mfma_f32_16x16x32_bf16 v[16:19], a[232:235], a[184:187], v[16:19]\n\t"
"v_fma_f32 v149, -v149, s66, v53\n\t"
"v_fma_f32 v52, v54, s52, v150\n\t"
"v_mfma_f32_16x16x32_bf16 v[16:19], a[236:239], a[188:191], v[16:19]\n\t"
"v_fma_f32 v53, v55, s52, v151\n\t"
"v_fma_f32 v150, -v150, s66, v54\n\t"
"v_mfma_f32_16x16x32_bf16 v[16:19], a[240:243], a[192:195], v[16:19]\n\t"
"v_fma_f32 v54, v56, s52, v152\n\t"
"v_fma_f32 v151, -v151, s66, v55\n\t"
"v_fma_f32 v55, v57, s52, v153\n\t"
"v_fma_f32 v152, -v152, s66, v56\n\t"
"s_waitcnt lgkmcnt(2)\n\t"
"v_mfma_f32_16x16x32_bf16 v[20:23], a[232:235], a[188:191], v[20:23]\n\t"
"v_fma_f32 v153, -v153, s66, v57\n\t"
"v_fma_f32 v56, v58, s52, v154\n\t"
"v_mfma_f32_16x16x32_bf16 v[20:23], a[236:239], a[192:195], v[20:23]\n\t"
"v_fma_f32 v57, v59, s52, v155\n\t"
"v_fma_f32 v154, -v154, s66, v58\n\t"
"v_mfma_f32_16x16x32_bf16 v[20:23], a[240:243], a[196:199], v[20:23]\n\t"
"v_fma_f32 v58, v60, s52, v156\n\t"
"v_fma_f32 v155, -v155, s66, v59\n\t"
"v_fma_f32 v59, v61, s52, v157\n\t"
"v_fma_f32 v156, -v156, s66, v60\n\t"
"s_waitcnt lgkmcnt(1)\n\t"
"v_mfma_f32_16x16x32_bf16 v[24:27], a[232:235], a[192:195], v[24:27]\n\t"
"v_fma_f32 v157, -v157, s66, v61\n\t"
"v_fma_f32 v60, v62, s52, v158\n\t"
"v_mfma_f32_16x16x32_bf16 v[24:27], a[236:239], a[196:199], v[24:27]\n\t"
"v_fma_f32 v61, v63, s52, v159\n\t"
"v_fma_f32 v158, -v158, s66, v62\n\t"
"v_mfma_f32_16x16x32_bf16 v[24:27], a[240:243], a[200:203], v[24:27]\n\t"
"v_fma_f32 v62, v64, s52, v160\n\t"
"v_fma_f32 v159, -v159, s66, v63\n\t"
"v_fma_f32 v63, v65, s52, v161\n\t"
"v_fma_f32 v160, -v160, s66, v64\n\t"
"s_waitcnt lgkmcnt(0)\n\t"
"v_mfma_f32_16x16x32_bf16 v[28:31], a[232:235], a[196:199], v[28:31]\n\t"
"v_fma_f32 v161, -v161, s66, v65\n\t"
"v_fma_f32 v64, v66, s52, v162\n\t"
"v_mfma_f32_16x16x32_bf16 v[28:31], a[236:239], a[200:203], v[28:31]\n\t"
"v_fma_f32 v65, v67, s52, v163\n\t"
"v_fma_f32 v162, -v162, s66, v66\n\t"
"v_mfma_f32_16x16x32_bf16 v[28:31], a[240:243], a[204:207], v[28:31]\n\t"
"v_fma_f32 v66, v68, s52, v164\n\t"
"v_fma_f32 v163, -v163, s66, v67\n\t"
"v_fma_f32 v67, v69, s52, v165\n\t"
"v_fma_f32 v164, -v164, s66, v68\n\t"
"s_waitcnt vmcnt(10)\n\t"
"v_mfma_f32_16x16x32_bf16 v[32:35], a[244:247], a[168:171], v[32:35]\n\t"
"v_fma_f32 v165, -v165, s66, v69\n\t"
"v_fma_f32 v68, v70, s52, v166\n\t"
"v_mfma_f32_16x16x32_bf16 v[32:35], a[248:251], a[172:175], v[32:35]\n\t"
"v_fma_f32 v69, v71, s52, v167\n\t"
"v_fma_f32 v166, -v166, s66, v70\n\t"
"v_mfma_f32_16x16x32_bf16 v[32:35], a[252:255], a[176:179], v[32:35]\n\t"
"ds_read_b64_tr_b16 v[230:231], v244 offset:512\n\t"
"ds_read_b64_tr_b16 v[232:233], v244 offset:3584\n\t"
"v_fma_f32 v70, v72, s52, v168\n\t"
"v_fma_f32 v167, -v167, s66, v71\n\t"
"v_mfma_f32_16x16x32_bf16 v[36:39], a[244:247], a[172:175], v[36:39]\n\t"
"v_fma_f32 v71, v73, s52, v169\n\t"
"v_fma_f32 v168, -v168, s66, v72\n\t"
"v_mfma_f32_16x16x32_bf16 v[36:39], a[248:251], a[176:179], v[36:39]\n\t"
"v_fma_f32 v169, -v169, s66, v73\n\t"
"v_fma_f32 v72, v74, s52, v170\n\t"
"v_mfma_f32_16x16x32_bf16 v[36:39], a[252:255], a[180:183], v[36:39]\n\t"
"v_fma_f32 v73, v75, s52, v171\n\t"
"v_fma_f32 v170, -v170, s66, v74\n\t"
"v_fma_f32 v74, v76, s52, v172\n\t"
"v_fma_f32 v171, -v171, s66, v75\n\t"
"v_mfma_f32_16x16x32_bf16 v[40:43], a[244:247], a[176:179], v[40:43]\n\t"
"v_fma_f32 v75, v77, s52, v173\n\t"
"v_fma_f32 v172, -v172, s66, v76\n\t"
"v_mfma_f32_16x16x32_bf16 v[40:43], a[248:251], a[180:183], v[40:43]\n\t"
"v_fma_f32 v173, -v173, s66, v77\n\t"
"v_fma_f32 v76, v78, s52, v174\n\t"
"v_mfma_f32_16x16x32_bf16 v[40:43], a[252:255], a[184:187], v[40:43]\n\t"
"v_fma_f32 v77, v79, s52, v175\n\t"
"v_fma_f32 v174, -v174, s66, v78\n\t"
"v_fma_f32 v78, v80, s52, v176\n\t"
"v_fma_f32 v175, -v175, s66, v79\n\t"
"v_mfma_f32_16x16x32_bf16 v[44:47], a[244:247], a[180:183], v[44:47]\n\t"
"ds_read_b64_tr_b16 v[234:235], v244 offset:6656\n\t"
"v_fma_f32 v79, v81, s52, v177\n\t"
"v_mfma_f32_16x16x32_bf16 v[44:47], a[248:251], a[184:187], v[44:47]\n\t"
"ds_read_b64_tr_b16 v[236:237], v244 offset:9728\n\t"
"v_fma_f32 v176, -v176, s66, v80\n\t"
"v_mfma_f32_16x16x32_bf16 v[44:47], a[252:255], a[188:191], v[44:47]\n\t"
"v_fma_f32 v177, -v177, s66, v81\n\t"
"v_fma_f32 v80, v82, s52, v178\n\t"
"v_fma_f32 v81, v83, s52, v179\n\t"
"v_fma_f32 v178, -v178, s66, v82\n\t"
"v_mfma_f32_16x16x32_bf16 v[48:51], a[244:247], a[184:187], v[48:51]\n\t"
"v_fma_f32 v82, v84, s52, v180\n\t"
"v_fma_f32 v179, -v179, s66, v83\n\t"
"v_mfma_f32_16x16x32_bf16 v[48:51], a[248:251], a[188:191], v[48:51]\n\t"
"v_fma_f32 v83, v85, s52, v181\n\t"
"v_fma_f32 v180, -v180, s66, v84\n\t"
"v_mfma_f32_16x16x32_bf16 v[48:51], a[252:255], a[192:195], v[48:51]\n\t"
"s_mov_b32 s20, 73728\n\t"
"buffer_load_dwordx4 a[220:223], v252, s[8:11], s20 offen offset:0 sc1\n\t"
"v_fma_f32 v181, -v181, s66, v85\n\t"
"v_fma_f32 v84, v86, s52, v182\n\t"
"v_mfma_f32_16x16x32_bf16 v[52:55], a[244:247], a[188:191], v[52:55]\n\t"
"v_fma_f32 v85, v87, s52, v183\n\t"
"v_fma_f32 v182, -v182, s66, v86\n\t"
"v_mfma_f32_16x16x32_bf16 v[52:55], a[248:251], a[192:195], v[52:55]\n\t"
"v_fma_f32 v86, v88, s52, v184\n\t"
"v_fma_f32 v183, -v183, s66, v87\n\t"
"v_mfma_f32_16x16x32_bf16 v[52:55], a[252:255], a[196:199], v[52:55]\n\t"
"buffer_load_dwordx4 a[224:227], v252, s[8:11], s20 offen offset:1024 sc1\n\t"
"v_fma_f32 v87, v89, s52, v185\n\t"
"v_fma_f32 v184, -v184, s66, v88\n\t"
"s_waitcnt lgkmcnt(2)\n\t"
"v_mfma_f32_16x16x32_bf16 v[230:233], v[230:233], v[218:221], 0\n\t"
"buffer_load_dwordx4 a[228:231], v252, s[8:11], s20 offen offset:2048 sc1\n\t"
"v_fma_f32 v185, -v185, s66, v89\n\t"
"v_fma_f32 v88, v90, s52, v186\n\t"
"v_mfma_f32_16x16x32_bf16 v[56:59], a[244:247], a[192:195], v[56:59]\n\t"
"ds_read_b64_tr_b16 v[222:223], v244 offset:12800\n\t"
"v_fma_f32 v89, v91, s52, v187\n\t"
"v_mfma_f32_16x16x32_bf16 v[56:59], a[248:251], a[196:199], v[56:59]\n\t"
"ds_read_b64_tr_b16 v[224:225], v244 offset:15872\n\t"
"v_fma_f32 v186, -v186, s66, v90\n\t"
"v_mfma_f32_16x16x32_bf16 v[56:59], a[252:255], a[200:203], v[56:59]\n\t"
"s_mov_b32 s20, 86016\n\t"
"buffer_load_dwordx4 a[232:235], v252, s[8:11], s20 offen offset:0 sc1\n\t"
"v_fma_f32 v90, v92, s52, v188\n\t"
"v_fma_f32 v187, -v187, s66, v91\n\t"
"v_mfma_f32_16x16x32_bf16 v[60:63], a[244:247], a[196:199], v[60:63]\n\t"
"v_fma_f32 v91, v93, s52, v189\n\t"
"v_fma_f32 v188, -v188, s66, v92\n\t"
"v_mfma_f32_16x16x32_bf16 v[60:63], a[248:251], a[200:203], v[60:63]\n\t"
"v_cvt_pk_bf16_f32 v230, v230, v231\n\t"
"v_cvt_pk_bf16_f32 v231, v232, v233\n\t"
"v_mfma_f32_16x16x32_bf16 v[60:63], a[252:255], a[204:207], v[60:63]\n\t"
"buffer_load_dwordx4 a[236:239], v252, s[8:11], s20 offen offset:1024 sc1\n\t"
"s_waitcnt lgkmcnt(2)\n\t"
"v_mfma_f32_16x16x32_bf16 v[234:237], v[234:237], v[218:221], 0\n\t"
"v_fma_f32 v189, -v189, s66, v93\n\t"
"v_fma_f32 v92, v94, s52, v190\n\t"
"s_waitcnt vmcnt(5)\n\t"
"v_mfma_f32_16x16x32_bf16 v[64:67], a[208:211], a[168:171], v[64:67]\n\t"
"ds_read_b64_tr_b16 v[226:227], v244 offset:20480\n\t"
"v_fma_f32 v93, v95, s52, v191\n\t"
"v_mfma_f32_16x16x32_bf16 v[64:67], a[212:215], a[172:175], v[64:67]\n\t"
"ds_read_b64_tr_b16 v[228:229], v244 offset:23552\n\t"
"v_fma_f32 v190, -v190, s66, v94\n\t"
"v_mfma_f32_16x16x32_bf16 v[64:67], a[216:219], a[176:179], v[64:67]\n\t"
"buffer_load_dwordx4 a[240:243], v252, s[8:11], s20 offen offset:2048 sc1\n\t"
"v_mfma_f32_16x16x32_bf16 v[68:71], a[208:211], a[172:175], v[68:71]\n\t"
"ds_read_b128 a[168:171], v242 offset:9216\n\t"
"v_fma_f32 v94, v96, s52, v192\n\t"
"v_mfma_f32_16x16x32_bf16 v[68:71], a[212:215], a[176:179], v[68:71]\n\t"
"v_cvt_pk_bf16_f32 v234, v234, v235\n\t"
"v_cvt_pk_bf16_f32 v235, v236, v237\n\t"
"v_mfma_f32_16x16x32_bf16 v[68:71], a[216:219], a[180:183], v[68:71]\n\t"
"s_mov_b32 s20, 98304\n\t"
"buffer_load_dwordx4 a[244:247], v252, s[8:11], s20 offen offset:0 sc1\n\t"
"v_mfma_f32_16x16x32_bf16 v[72:75], a[208:211], a[176:179], v[72:75]\n\t"
"ds_read_b128 a[172:175], v242 offset:9728\n\t"
"v_fma_f32 v191, -v191, s66, v95\n\t"
"v_mfma_f32_16x16x32_bf16 v[72:75], a[212:215], a[180:183], v[72:75]\n\t"
"ds_read_b128 a[176:179], v242 offset:10240\n\t"
"v_fma_f32 v95, v97, s52, v193\n\t"
"v_mfma_f32_16x16x32_bf16 v[72:75], a[216:219], a[184:187], v[72:75]\n\t"
"buffer_load_dwordx4 a[248:251], v252, s[8:11], s20 offen offset:1024 sc1\n\t"
"s_waitcnt lgkmcnt(5)\n\t"
"v_mfma_f32_16x16x32_bf16 v[222:225], v[222:225], v[218:221], 0\n\t"
"v_fma_f32 v192, -v192, s66, v96\n\t"
"v_mfma_f32_16x16x32_bf16 v[76:79], a[208:211], a[180:183], v[76:79]\n\t"
"ds_read_b128 a[180:183], v242 offset:10752\n\t"
"v_fma_f32 v193, -v193, s66, v97\n\t"
"v_mfma_f32_16x16x32_bf16 v[76:79], a[212:215], a[184:187], v[76:79]\n\t"
"v_mfma_f32_16x16x32_bf16 v[76:79], a[216:219], a[188:191], v[76:79]\n\t"
"buffer_load_dwordx4 a[252:255], v252, s[8:11], s20 offen offset:2048 sc1\n\t"
"v_mfma_f32_16x16x32_bf16 v[80:83], a[208:211], a[184:187], v[80:83]\n\t"
"ds_read_b128 a[184:187], v242 offset:11264\n\t"
"v_mfma_f32_16x16x32_bf16 v[80:83], a[212:215], a[188:191], v[80:83]\n\t"
"v_cvt_pk_bf16_f32 v222, v222, v223\n\t"
"v_mfma_f32_16x16x32_bf16 v[80:83], a[216:219], a[192:195], v[80:83]\n\t"
"v_cvt_pk_bf16_f32 v223, v224, v225\n\t"
"s_waitcnt lgkmcnt(5)\n\t"
"v_mfma_f32_16x16x32_bf16 v[226:229], v[226:229], v[218:221], 0\n\t"
"v_mfma_f32_16x16x32_bf16 v[84:87], a[208:211], a[188:191], v[84:87]\n\t"
"ds_read_b128 a[188:191], v242 offset:11776\n\t"
"v_mfma_f32_16x16x32_bf16 v[84:87], a[212:215], a[192:195], v[84:87]\n\t"
"v_mfma_f32_16x16x32_bf16 v[84:87], a[216:219], a[196:199], v[84:87]\n\t"
"v_mfma_f32_16x16x32_bf16 v[88:91], a[208:211], a[192:195], v[88:91]\n\t"
"ds_read_b128 a[192:195], v242 offset:12288\n\t"
"v_mfma_f32_16x16x32_bf16 v[88:91], a[212:215], a[196:199], v[88:91]\n\t"
"s_nop 0\n\t"
"v_cvt_pk_bf16_f32 v226, v226, v227\n\t"
"v_mfma_f32_16x16x32_bf16 v[88:91], a[216:219], a[200:203], v[88:91]\n\t"
"v_cvt_pk_bf16_f32 v227, v228, v229\n\t"
"v_mfma_f32_16x16x32_bf16 v[92:95], a[208:211], a[196:199], v[92:95]\n\t"
"ds_read_b128 a[196:199], v242 offset:12800\n\t"
"v_mfma_f32_16x16x32_bf16 v[92:95], a[212:215], a[200:203], v[92:95]\n\t"
"ds_read_b128 a[200:203], v242 offset:13312\n\t"
"v_mfma_f32_16x16x32_bf16 v[92:95], a[216:219], a[204:207], v[92:95]\n\t"
"s_mov_b32 s20, 258048\n\t"
"buffer_load_dwordx4 a[208:211], v252, s[8:11], s20 offen offset:0 sc1\n\t"
"s_waitcnt vmcnt(7) lgkmcnt(6)\n\t"
"v_mfma_f32_16x16x32_bf16 v[98:101], a[220:223], a[168:171], v[98:101]\n\t"
"ds_read_b128 a[204:207], v242 offset:13824\n\t"
"v_mfma_f32_16x16x32_bf16 v[98:101], a[224:227], a[172:175], v[98:101]\n\t"
"v_mfma_f32_16x16x32_bf16 v[98:101], a[228:231], a[176:179], v[98:101]\n\t"
"buffer_load_dwordx4 a[212:215], v252, s[8:11], s20 offen offset:1024 sc1\n\t"
"s_waitcnt lgkmcnt(6)\n\t"
"v_mfma_f32_16x16x32_bf16 v[102:105], a[220:223], a[172:175], v[102:105]\n\t"
"v_mfma_f32_16x16x32_bf16 v[102:105], a[224:227], a[176:179], v[102:105]\n\t"
"v_mfma_f32_16x16x32_bf16 v[102:105], a[228:231], a[180:183], v[102:105]\n\t"
"buffer_load_dwordx4 a[216:219], v252, s[8:11], s20 offen offset:2048 sc1\n\t"
"s_waitcnt lgkmcnt(0)\n\t"
"s_barrier\n\t"
"v_mfma_f32_16x16x32_bf16 v[106:109], a[220:223], a[176:179], v[106:109]\n\t"
"v_mfma_f32_16x16x32_bf16 v[106:109], a[224:227], a[180:183], v[106:109]\n\t"
"v_mfma_f32_16x16x32_bf16 v[106:109], a[228:231], a[184:187], v[106:109]\n\t"
"ds_write_b64 v243, v[230:231] offset:0\n\t"
"ds_write_b64 v243, v[234:235] offset:512\n\t"
"v_mfma_f32_16x16x32_bf16 v[110:113], a[220:223], a[180:183], v[110:113]\n\t"
"ds_read_b64_tr_b16 v[230:231], v244 offset:26624\n\t"
"v_mfma_f32_16x16x32_bf16 v[110:113], a[224:227], a[184:187], v[110:113]\n\t"
"ds_read_b64_tr_b16 v[232:233], v244 offset:29696\n\t"
"v_mfma_f32_16x16x32_bf16 v[110:113], a[228:231], a[188:191], v[110:113]\n\t"
"ds_write_b64 v243, v[222:223] offset:1024\n\t"
"v_mfma_f32_16x16x32_bf16 v[114:117], a[220:223], a[184:187], v[114:117]\n\t"
"ds_read_b64_tr_b16 v[234:235], v244 offset:32768\n\t"
"v_mfma_f32_16x16x32_bf16 v[114:117], a[224:227], a[188:191], v[114:117]\n\t"
"ds_read_b64_tr_b16 v[236:237], v244 offset:35840\n\t"
"v_mfma_f32_16x16x32_bf16 v[114:117], a[228:231], a[192:195], v[114:117]\n\t"
"ds_write_b64 v243, v[226:227] offset:1536\n\t"
"v_mfma_f32_16x16x32_bf16 v[118:121], a[220:223], a[188:191], v[118:121]\n\t"
"ds_read_b64_tr_b16 v[222:223], v244 offset:40448\n\t"
"v_mfma_f32_16x16x32_bf16 v[118:121], a[224:227], a[192:195], v[118:121]\n\t"
"ds_read_b64_tr_b16 v[224:225], v244 offset:43520\n\t"
"v_mfma_f32_16x16x32_bf16 v[118:121], a[228:231], a[196:199], v[118:121]\n\t"
"ds_read_b64_tr_b16 v[226:227], v244 offset:46592\n\t"
"ds_read_b64_tr_b16 v[228:229], v244 offset:49664\n\t"
"s_waitcnt lgkmcnt(8)\n\t"
"v_mfma_f32_16x16x32_bf16 v[230:233], v[230:233], v[218:221], 0\n\t"
"s_add_u32 m0, s24, 33152\n\t"
"s_add_u32 s28, s21, 983040\n\t"
"buffer_load_dwordx4 v245, s[12:15], s28 offen offset:128 nt lds\n\t"
"v_mfma_f32_16x16x32_bf16 v[122:125], a[220:223], a[192:195], v[122:125]\n\t"
"v_mfma_f32_16x16x32_bf16 v[122:125], a[224:227], a[196:199], v[122:125]\n\t"
"v_mfma_f32_16x16x32_bf16 v[122:125], a[228:231], a[200:203], v[122:125]\n\t"
"s_add_u32 m0, s24, 40832\n\t"
"s_add_u32 s28, s21, 1179648\n\t"
"buffer_load_dwordx4 v245, s[12:15], s28 offen offset:128 nt lds\n\t"
"s_waitcnt lgkmcnt(5)\n\t"
"v_mfma_f32_16x16x32_bf16 v[234:237], v[234:237], v[218:221], 0\n\t"
"s_add_u32 m0, s24, 46976\n\t"
"s_add_u32 s28, s21, 1376256\n\t"
"buffer_load_dwordx4 v245, s[12:15], s28 offen offset:128 nt lds\n\t"
"v_mfma_f32_16x16x32_bf16 v[126:129], a[220:223], a[196:199], v[126:129]\n\t"
"v_cvt_pk_bf16_f32 v230, v230, v231\n\t"
"v_cvt_pk_bf16_f32 v231, v232, v233\n\t"
"v_mfma_f32_16x16x32_bf16 v[126:129], a[224:227], a[200:203], v[126:129]\n\t"
"v_mfma_f32_16x16x32_bf16 v[126:129], a[228:231], a[204:207], v[126:129]\n\t"
"s_add_u32 m0, s24, 53120\n\t"
"s_add_u32 s28, s21, 1572864\n\t"
"buffer_load_dwordx4 v245, s[12:15], s28 offen offset:128 nt lds\n\t"
"s_waitcnt lgkmcnt(2)\n\t"
"v_mfma_f32_16x16x32_bf16 v[222:225], v[222:225], v[218:221], 0\n\t"
"s_add_u32 m0, s24, 60800\n\t"
"s_add_u32 s28, s21, 1769472\n\t"
"buffer_load_dwordx4 v245, s[12:15], s28 offen offset:128 nt lds\n\t"
"s_waitcnt vmcnt(11)\n\t"
"v_mfma_f32_16x16x32_bf16 v[130:133], a[232:235], a[168:171], v[130:133]\n\t"
"v_cvt_pk_bf16_f32 v234, v234, v235\n\t"
"v_cvt_pk_bf16_f32 v235, v236, v237\n\t"
"v_mfma_f32_16x16x32_bf16 v[130:133], a[236:239], a[172:175], v[130:133]\n\t"
"v_mfma_f32_16x16x32_bf16 v[130:133], a[240:243], a[176:179], v[130:133]\n\t"
"s_add_u32 m0, s24, 66944\n\t"
"s_add_u32 s28, s21, 1966080\n\t"
"buffer_load_dwordx4 v245, s[12:15], s28 offen offset:128 nt lds\n\t"
"s_waitcnt lgkmcnt(0)\n\t"
"v_mfma_f32_16x16x32_bf16 v[226:229], v[226:229], v[218:221], 0\n\t"
"ds_write_b64 v243, v[230:231] offset:2048\n\t"
"s_add_u32 m0, s24, 73088\n\t"
"s_add_u32 s28, s21, 2162688\n\t"
"buffer_load_dwordx4 v245, s[12:15], s28 offen offset:128 nt lds\n\t"
"v_mfma_f32_16x16x32_bf16 v[134:137], a[232:235], a[172:175], v[134:137]\n\t"
"ds_read_b64_tr_b16 v[230:231], v244 offset:52736\n\t"
"v_cvt_pk_bf16_f32 v222, v222, v223\n\t"
"v_mfma_f32_16x16x32_bf16 v[134:137], a[236:239], a[176:179], v[134:137]\n\t"
"ds_read_b64_tr_b16 v[232:233], v244 offset:55808\n\t"
"v_cvt_pk_bf16_f32 v223, v224, v225\n\t"
"v_mfma_f32_16x16x32_bf16 v[134:137], a[240:243], a[180:183], v[134:137]\n\t"
"ds_write_b64 v243, v[234:235] offset:2560\n\t"
"v_mfma_f32_16x16x32_bf16 v[138:141], a[232:235], a[176:179], v[138:141]\n\t"
"v_mfma_f32_16x16x32_bf16 v[138:141], a[236:239], a[180:183], v[138:141]\n\t"
"v_cvt_pk_bf16_f32 v226, v226, v227\n\t"
"v_cvt_pk_bf16_f32 v227, v228, v229\n\t"
"v_mfma_f32_16x16x32_bf16 v[138:141], a[240:243], a[184:187], v[138:141]\n\t"
"ds_write_b64 v243, v[222:223] offset:3072\n\t"
"v_mfma_f32_16x16x32_bf16 v[142:145], a[232:235], a[180:183], v[142:145]\n\t"
"v_mfma_f32_16x16x32_bf16 v[142:145], a[236:239], a[184:187], v[142:145]\n\t"
"v_mfma_f32_16x16x32_bf16 v[142:145], a[240:243], a[188:191], v[142:145]\n\t"
"ds_write_b64 v243, v[226:227] offset:3584\n\t"
"s_waitcnt lgkmcnt(3)\n\t"
"v_mfma_f32_16x16x32_bf16 v[230:233], v[230:233], v[218:221], 0\n\t"
"v_mfma_f32_16x16x32_bf16 v[146:149], a[232:235], a[184:187], v[146:149]\n\t"
"v_mfma_f32_16x16x32_bf16 v[146:149], a[236:239], a[188:191], v[146:149]\n\t"
"v_mfma_f32_16x16x32_bf16 v[146:149], a[240:243], a[192:195], v[146:149]\n\t"
"v_mfma_f32_16x16x32_bf16 v[150:153], a[232:235], a[188:191], v[150:153]\n\t"
"v_mfma_f32_16x16x32_bf16 v[150:153], a[236:239], a[192:195], v[150:153]\n\t"
"s_nop 2\n\t"
"v_cvt_pk_bf16_f32 v230, v230, v231\n\t"
"v_cvt_pk_bf16_f32 v231, v232, v233\n\t"
"v_mfma_f32_16x16x32_bf16 v[150:153], a[240:243], a[196:199], v[150:153]\n\t"
"v_mfma_f32_16x16x32_bf16 v[154:157], a[232:235], a[192:195], v[154:157]\n\t"
"v_mfma_f32_16x16x32_bf16 v[154:157], a[236:239], a[196:199], v[154:157]\n\t"
"v_mfma_f32_16x16x32_bf16 v[154:157], a[240:243], a[200:203], v[154:157]\n\t"
"ds_write_b64 v243, v[230:231] offset:4096\n\t"
"v_mfma_f32_16x16x32_bf16 v[158:161], a[232:235], a[196:199], v[158:161]\n\t"
"v_mfma_f32_16x16x32_bf16 v[158:161], a[236:239], a[200:203], v[158:161]\n\t"
"v_mfma_f32_16x16x32_bf16 v[158:161], a[240:243], a[204:207], v[158:161]\n\t"
"s_waitcnt vmcnt(10)\n\t"
"v_mfma_f32_16x16x32_bf16 v[162:165], a[244:247], a[168:171], v[162:165]\n\t"
"v_mfma_f32_16x16x32_bf16 v[162:165], a[248:251], a[172:175], v[162:165]\n\t"
"v_mfma_f32_16x16x32_bf16 v[162:165], a[252:255], a[176:179], v[162:165]\n\t"
"s_waitcnt lgkmcnt(0)\n\t"
"s_barrier\n\t"
"v_mfma_f32_16x16x32_bf16 v[166:169], a[244:247], a[172:175], v[166:169]\n\t"
"ds_read_b128 a[168:171], v242 offset:0\n\t"
"v_mfma_f32_16x16x32_bf16 v[166:169], a[248:251], a[176:179], v[166:169]\n\t"
"ds_read_b128 a[172:175], v242 offset:512\n\t"
"v_mfma_f32_16x16x32_bf16 v[166:169], a[252:255], a[180:183], v[166:169]\n\t"
"v_mfma_f32_16x16x32_bf16 v[170:173], a[244:247], a[176:179], v[170:173]\n\t"
"ds_read_b128 a[176:179], v242 offset:1024\n\t"
"v_mfma_f32_16x16x32_bf16 v[170:173], a[248:251], a[180:183], v[170:173]\n\t"
"v_mfma_f32_16x16x32_bf16 v[170:173], a[252:255], a[184:187], v[170:173]\n\t"
"s_mov_b32 s20, 270336\n\t"
"buffer_load_dwordx4 a[220:223], v252, s[8:11], s20 offen offset:0 sc1\n\t"
"v_mfma_f32_16x16x32_bf16 v[174:177], a[244:247], a[180:183], v[174:177]\n\t"
"ds_read_b128 a[180:183], v242 offset:1536\n\t"
"v_mfma_f32_16x16x32_bf16 v[174:177], a[248:251], a[184:187], v[174:177]\n\t"
"v_mfma_f32_16x16x32_bf16 v[174:177], a[252:255], a[188:191], v[174:177]\n\t"
"buffer_load_dwordx4 a[224:227], v252, s[8:11], s20 offen offset:1024 sc1\n\t"
"v_mfma_f32_16x16x32_bf16 v[178:181], a[244:247], a[184:187], v[178:181]\n\t"
"ds_read_b128 a[184:187], v242 offset:2048\n\t"
"v_mfma_f32_16x16x32_bf16 v[178:181], a[248:251], a[188:191], v[178:181]\n\t"
"v_mfma_f32_16x16x32_bf16 v[178:181], a[252:255], a[192:195], v[178:181]\n\t"
"buffer_load_dwordx4 a[228:231], v252, s[8:11], s20 offen offset:2048 sc1\n\t"
"v_mfma_f32_16x16x32_bf16 v[182:185], a[244:247], a[188:191], v[182:185]\n\t"
"ds_read_b128 a[188:191], v242 offset:2560\n\t"
"v_mfma_f32_16x16x32_bf16 v[182:185], a[248:251], a[192:195], v[182:185]\n\t"
"v_mfma_f32_16x16x32_bf16 v[182:185], a[252:255], a[196:199], v[182:185]\n\t"
"s_mov_b32 s20, 282624\n\t"
"buffer_load_dwordx4 a[232:235], v252, s[8:11], s20 offen offset:0 sc1\n\t"
"v_mfma_f32_16x16x32_bf16 v[186:189], a[244:247], a[192:195], v[186:189]\n\t"
"ds_read_b128 a[192:195], v242 offset:3072\n\t"
"v_mfma_f32_16x16x32_bf16 v[186:189], a[248:251], a[196:199], v[186:189]\n\t"
"v_mfma_f32_16x16x32_bf16 v[186:189], a[252:255], a[200:203], v[186:189]\n\t"
"v_mfma_f32_16x16x32_bf16 v[190:193], a[244:247], a[196:199], v[190:193]\n\t"
"ds_read_b128 a[196:199], v242 offset:3584\n\t"
"v_mfma_f32_16x16x32_bf16 v[190:193], a[248:251], a[200:203], v[190:193]\n\t"
"ds_read_b128 a[200:203], v242 offset:4096\n\t"
"v_mfma_f32_16x16x32_bf16 v[190:193], a[252:255], a[204:207], v[190:193]\n\t"
"buffer_load_dwordx4 a[236:239], v252, s[8:11], s20 offen offset:1024 sc1\n\t"
"s_waitcnt vmcnt(12) lgkmcnt(6)\n\t"
"v_mfma_f32_16x16x32_bf16 v[0:3], a[208:211], a[168:171], v[0:3]\n\t"
"ds_read_b128 a[204:207], v242 offset:4608\n\t"
"v_mfma_f32_16x16x32_bf16 v[0:3], a[212:215], a[172:175], v[0:3]\n\t"
"v_mfma_f32_16x16x32_bf16 v[0:3], a[216:219], a[176:179], v[0:3]\n\t"
"buffer_load_dwordx4 a[240:243], v252, s[8:11], s20 offen offset:2048 sc1\n\t"
"s_waitcnt lgkmcnt(6)\n\t"
"v_mfma_f32_16x16x32_bf16 v[4:7], a[208:211], a[172:175], v[4:7]\n\t"
"v_mfma_f32_16x16x32_bf16 v[4:7], a[212:215], a[176:179], v[4:7]\n\t"
"v_mfma_f32_16x16x32_bf16 v[4:7], a[216:219], a[180:183], v[4:7]\n\t"
"s_mov_b32 s20, 294912\n\t"
"buffer_load_dwordx4 a[244:247], v252, s[8:11], s20 offen offset:0 sc1\n\t"
"s_waitcnt lgkmcnt(5)\n\t"
"v_mfma_f32_16x16x32_bf16 v[8:11], a[208:211], a[176:179], v[8:11]\n\t"
"v_mfma_f32_16x16x32_bf16 v[8:11], a[212:215], a[180:183], v[8:11]\n\t"
"v_mfma_f32_16x16x32_bf16 v[8:11], a[216:219], a[184:187], v[8:11]\n\t"
"s_waitcnt lgkmcnt(4)\n\t"
"v_mfma_f32_16x16x32_bf16 v[12:15], a[208:211], a[180:183], v[12:15]\n\t"
"v_mfma_f32_16x16x32_bf16 v[12:15], a[212:215], a[184:187], v[12:15]\n\t"
"v_mfma_f32_16x16x32_bf16 v[12:15], a[216:219], a[188:191], v[12:15]\n\t"
"buffer_load_dwordx4 a[248:251], v252, s[8:11], s20 offen offset:1024 sc1\n\t"
"s_waitcnt lgkmcnt(3)\n\t"
"v_mfma_f32_16x16x32_bf16 v[16:19], a[208:211], a[184:187], v[16:19]\n\t"
"v_xor_b32 v244, 0xc0, v244\n\t"
"v_mfma_f32_16x16x32_bf16 v[16:19], a[212:215], a[188:191], v[16:19]\n\t"
"v_mfma_f32_16x16x32_bf16 v[16:19], a[216:219], a[192:195], v[16:19]\n\t"
"buffer_load_dwordx4 a[252:255], v252, s[8:11], s20 offen offset:2048 sc1\n\t"
"s_waitcnt lgkmcnt(2)\n\t"
"v_mfma_f32_16x16x32_bf16 v[20:23], a[208:211], a[188:191], v[20:23]\n\t"
"v_mfma_f32_16x16x32_bf16 v[20:23], a[212:215], a[192:195], v[20:23]\n\t"
"v_mfma_f32_16x16x32_bf16 v[20:23], a[216:219], a[196:199], v[20:23]\n\t"
"s_waitcnt lgkmcnt(1)\n\t"
"v_mfma_f32_16x16x32_bf16 v[24:27], a[208:211], a[192:195], v[24:27]\n\t"
"v_mfma_f32_16x16x32_bf16 v[24:27], a[212:215], a[196:199], v[24:27]\n\t"
"v_mfma_f32_16x16x32_bf16 v[24:27], a[216:219], a[200:203], v[24:27]\n\t"
"s_waitcnt lgkmcnt(0)\n\t"
"v_mfma_f32_16x16x32_bf16 v[28:31], a[208:211], a[196:199], v[28:31]\n\t"
"v_mfma_f32_16x16x32_bf16 v[28:31], a[212:215], a[200:203], v[28:31]\n\t"
"v_mfma_f32_16x16x32_bf16 v[28:31], a[216:219], a[204:207], v[28:31]\n\t"
"s_mov_b32 s20, 307200\n\t"
"buffer_load_dwordx4 a[208:211], v252, s[8:11], s20 offen offset:0 sc1\n\t"
"s_waitcnt vmcnt(7)\n\t"
"v_mfma_f32_16x16x32_bf16 v[32:35], a[220:223], a[168:171], v[32:35]\n\t"
"v_mfma_f32_16x16x32_bf16 v[32:35], a[224:227], a[172:175], v[32:35]\n\t"
"v_mfma_f32_16x16x32_bf16 v[32:35], a[228:231], a[176:179], v[32:35]\n\t"
"buffer_load_dwordx4 a[212:215], v252, s[8:11], s20 offen offset:1024 sc1\n\t"
"v_mfma_f32_16x16x32_bf16 v[36:39], a[220:223], a[172:175], v[36:39]\n\t"
"ds_read_b64_tr_b16 v[234:235], v244 offset:512\n\t"
"v_mfma_f32_16x16x32_bf16 v[36:39], a[224:227], a[176:179], v[36:39]\n\t"
"ds_read_b64_tr_b16 v[236:237], v244 offset:3584\n\t"
"v_mfma_f32_16x16x32_bf16 v[36:39], a[228:231], a[180:183], v[36:39]\n\t"
"buffer_load_dwordx4 a[216:219], v252, s[8:11], s20 offen offset:2048 sc1\n\t"
"v_mfma_f32_16x16x32_bf16 v[40:43], a[220:223], a[176:179], v[40:43]\n\t"
"v_mfma_f32_16x16x32_bf16 v[40:43], a[224:227], a[180:183], v[40:43]\n\t"
"v_mfma_f32_16x16x32_bf16 v[40:43], a[228:231], a[184:187], v[40:43]\n\t"
"v_mfma_f32_16x16x32_bf16 v[44:47], a[220:223], a[180:183], v[44:47]\n\t"
"ds_read_b64_tr_b16 v[222:223], v244 offset:6656\n\t"
"v_mfma_f32_16x16x32_bf16 v[44:47], a[224:227], a[184:187], v[44:47]\n\t"
"ds_read_b64_tr_b16 v[224:225], v244 offset:9728\n\t"
"v_mfma_f32_16x16x32_bf16 v[44:47], a[228:231], a[188:191], v[44:47]\n\t"
"v_mfma_f32_16x16x32_bf16 v[48:51], a[220:223], a[184:187], v[48:51]\n\t"
"v_mfma_f32_16x16x32_bf16 v[48:51], a[224:227], a[188:191], v[48:51]\n\t"
"v_mfma_f32_16x16x32_bf16 v[48:51], a[228:231], a[192:195], v[48:51]\n\t"
"v_mfma_f32_16x16x32_bf16 v[52:55], a[220:223], a[188:191], v[52:55]\n\t"
"v_mfma_f32_16x16x32_bf16 v[52:55], a[224:227], a[192:195], v[52:55]\n\t"
"v_mfma_f32_16x16x32_bf16 v[52:55], a[228:231], a[196:199], v[52:55]\n\t"
"s_waitcnt lgkmcnt(2)\n\t"
"v_mfma_f32_16x16x32_bf16 v[234:237], v[234:237], v[218:221], 0\n\t"
"v_mfma_f32_16x16x32_bf16 v[56:59], a[220:223], a[192:195], v[56:59]\n\t"
"ds_read_b64_tr_b16 v[226:227], v244 offset:12800\n\t"
"v_mfma_f32_16x16x32_bf16 v[56:59], a[224:227], a[196:199], v[56:59]\n\t"
"ds_read_b64_tr_b16 v[228:229], v244 offset:15872\n\t"
"v_mfma_f32_16x16x32_bf16 v[56:59], a[228:231], a[200:203], v[56:59]\n\t"
"v_mfma_f32_16x16x32_bf16 v[60:63], a[220:223], a[196:199], v[60:63]\n\t"
"v_mfma_f32_16x16x32_bf16 v[60:63], a[224:227], a[200:203], v[60:63]\n\t"
"s_nop 0\n\t"
"v_cvt_pk_bf16_f32 v234, v234, v235\n\t"
"v_cvt_pk_bf16_f32 v235, v236, v237\n\t"
"v_mfma_f32_16x16x32_bf16 v[60:63], a[228:231], a[204:207], v[60:63]\n\t"
"s_waitcnt lgkmcnt(2)\n\t"
"v_mfma_f32_16x16x32_bf16 v[222:225], v[222:225], v[218:221], 0\n\t"
"s_mov_b32 s20, 319488\n\t"
"buffer_load_dwordx4 a[220:223], v252, s[8:11], s20 offen offset:0 sc1\n\t"
"s_waitcnt vmcnt(7)\n\t"
"v_mfma_f32_16x16x32_bf16 v[64:67], a[232:235], a[168:171], v[64:67]\n\t"
"ds_read_b64_tr_b16 v[230:231], v244 offset:20480\n\t"
"v_mfma_f32_16x16x32_bf16 v[64:67], a[236:239], a[172:175], v[64:67]\n\t"
"ds_read_b64_tr_b16 v[232:233], v244 offset:23552\n\t"
"v_mfma_f32_16x16x32_bf16 v[64:67], a[240:243], a[176:179], v[64:67]\n\t"
"buffer_load_dwordx4 a[224:227], v252, s[8:11], s20 offen offset:1024 sc1\n\t"
"v_mfma_f32_16x16x32_bf16 v[68:71], a[232:235], a[172:175], v[68:71]\n\t"
"ds_read_b128 a[168:171], v242 offset:9216\n\t"
"v_mfma_f32_16x16x32_bf16 v[68:71], a[236:239], a[176:179], v[68:71]\n\t"
"v_cvt_pk_bf16_f32 v222, v222, v223\n\t"
"v_cvt_pk_bf16_f32 v223, v224, v225\n\t"
"v_mfma_f32_16x16x32_bf16 v[68:71], a[240:243], a[180:183], v[68:71]\n\t"
"buffer_load_dwordx4 a[228:231], v252, s[8:11], s20 offen offset:2048 sc1\n\t"
"v_mfma_f32_16x16x32_bf16 v[72:75], a[232:235], a[176:179], v[72:75]\n\t"
"ds_read_b128 a[172:175], v242 offset:9728\n\t"
"v_mfma_f32_16x16x32_bf16 v[72:75], a[236:239], a[180:183], v[72:75]\n\t"
"ds_read_b128 a[176:179], v242 offset:10240\n\t"
"v_mfma_f32_16x16x32_bf16 v[72:75], a[240:243], a[184:187], v[72:75]\n\t"
"s_add_u32 m0, s24, 80768\n\t"
"s_add_u32 s28, s21, 2359296\n\t"
"buffer_load_dwordx4 v245, s[12:15], s28 offen offset:128 nt lds\n\t"
"s_add_u32 m0, s24, 86912\n\t"
"s_add_u32 s28, s21, 2555904\n\t"
"buffer_load_dwordx4 v245, s[12:15], s28 offen offset:128 nt lds\n\t"
"s_waitcnt lgkmcnt(5)\n\t"
"v_mfma_f32_16x16x32_bf16 v[226:229], v[226:229], v[218:221], 0\n\t"
"v_mfma_f32_16x16x32_bf16 v[76:79], a[232:235], a[180:183], v[76:79]\n\t"
"ds_read_b128 a[180:183], v242 offset:10752\n\t"
"v_mfma_f32_16x16x32_bf16 v[76:79], a[236:239], a[184:187], v[76:79]\n\t"
"v_mfma_f32_16x16x32_bf16 v[76:79], a[240:243], a[188:191], v[76:79]\n\t"
"s_add_u32 m0, s24, 93056\n\t"
"s_add_u32 s28, s21, 2752512\n\t"
"buffer_load_dwordx4 v245, s[12:15], s28 offen offset:128 nt lds\n\t"
"s_add_u32 m0, s24, 100736\n\t"
"s_add_u32 s28, s21, 2949120\n\t"
"buffer_load_dwordx4 v245, s[12:15], s28 offen offset:128 nt lds\n\t"
"v_mfma_f32_16x16x32_bf16 v[80:83], a[232:235], a[184:187], v[80:83]\n\t"
"ds_read_b128 a[184:187], v242 offset:11264\n\t"
"v_mfma_f32_16x16x32_bf16 v[80:83], a[236:239], a[188:191], v[80:83]\n\t"
"v_cvt_pk_bf16_f32 v226, v226, v227\n\t"
"v_cvt_pk_bf16_f32 v227, v228, v229\n\t"
"v_mfma_f32_16x16x32_bf16 v[80:83], a[240:243], a[192:195], v[80:83]\n\t"
"s_add_u32 m0, s24, 106880\n\t"
"s_add_u32 s28, s21, 3145728\n\t"
"buffer_load_dwordx4 v245, s[12:15], s28 offen offset:128 nt lds\n\t"
"s_waitcnt lgkmcnt(5)\n\t"
"v_mfma_f32_16x16x32_bf16 v[230:233], v[230:233], v[218:221], 0\n\t"
"s_add_u32 m0, s24, 113024\n\t"
"s_add_u32 s28, s21, 3342336\n\t"
"buffer_load_dwordx4 v245, s[12:15], s28 offen offset:128 nt lds\n\t"
"v_mfma_f32_16x16x32_bf16 v[84:87], a[232:235], a[188:191], v[84:87]\n\t"
"ds_read_b128 a[188:191], v242 offset:11776\n\t"
"v_mfma_f32_16x16x32_bf16 v[84:87], a[236:239], a[192:195], v[84:87]\n\t"
"v_mfma_f32_16x16x32_bf16 v[84:87], a[240:243], a[196:199], v[84:87]\n\t"
"v_mfma_f32_16x16x32_bf16 v[88:91], a[232:235], a[192:195], v[88:91]\n\t"
"ds_read_b128 a[192:195], v242 offset:12288\n\t"
"v_mfma_f32_16x16x32_bf16 v[88:91], a[236:239], a[196:199], v[88:91]\n\t"
"v_cvt_pk_bf16_f32 v230, v230, v231\n\t"
"v_mfma_f32_16x16x32_bf16 v[88:91], a[240:243], a[200:203], v[88:91]\n\t"
"v_cvt_pk_bf16_f32 v231, v232, v233\n\t"
"v_mfma_f32_16x16x32_bf16 v[92:95], a[232:235], a[196:199], v[92:95]\n\t"
"ds_read_b128 a[196:199], v242 offset:12800\n\t"
"v_mfma_f32_16x16x32_bf16 v[92:95], a[236:239], a[200:203], v[92:95]\n\t"
"ds_read_b128 a[200:203], v242 offset:13312\n\t"
"v_mfma_f32_16x16x32_bf16 v[92:95], a[240:243], a[204:207], v[92:95]\n\t"
"ds_read_b128 a[204:207], v242 offset:13824\n\t"
"s_waitcnt vmcnt(12) lgkmcnt(7)\n\t"
"v_mfma_f32_16x16x32_bf16 v[98:101], a[244:247], a[168:171], v[98:101]\n\t"
"v_mfma_f32_16x16x32_bf16 v[98:101], a[248:251], a[172:175], v[98:101]\n\t"
"v_mfma_f32_16x16x32_bf16 v[98:101], a[252:255], a[176:179], v[98:101]\n\t"
"s_waitcnt lgkmcnt(6)\n\t"
"v_mfma_f32_16x16x32_bf16 v[102:105], a[244:247], a[172:175], v[102:105]\n\t"
"v_mfma_f32_16x16x32_bf16 v[102:105], a[248:251], a[176:179], v[102:105]\n\t"
"v_mfma_f32_16x16x32_bf16 v[102:105], a[252:255], a[180:183], v[102:105]\n\t"
"s_waitcnt lgkmcnt(0)\n\t"
"s_barrier\n\t"
"v_mfma_f32_16x16x32_bf16 v[106:109], a[244:247], a[176:179], v[106:109]\n\t"
"v_mfma_f32_16x16x32_bf16 v[106:109], a[248:251], a[180:183], v[106:109]\n\t"
"v_mfma_f32_16x16x32_bf16 v[106:109], a[252:255], a[184:187], v[106:109]\n\t"
"ds_write_b64 v243, v[234:235] offset:0\n\t"
"ds_write_b64 v243, v[222:223] offset:512\n\t"
"v_mfma_f32_16x16x32_bf16 v[110:113], a[244:247], a[180:183], v[110:113]\n\t"
"ds_read_b64_tr_b16 v[234:235], v244 offset:26624\n\t"
"v_mfma_f32_16x16x32_bf16 v[110:113], a[248:251], a[184:187], v[110:113]\n\t"
"ds_read_b64_tr_b16 v[236:237], v244 offset:29696\n\t"
"v_mfma_f32_16x16x32_bf16 v[110:113], a[252:255], a[188:191], v[110:113]\n\t"
"ds_write_b64 v243, v[226:227] offset:1024\n\t"
"v_mfma_f32_16x16x32_bf16 v[114:117], a[244:247], a[184:187], v[114:117]\n\t"
"ds_read_b64_tr_b16 v[222:223], v244 offset:32768\n\t"
"v_mfma_f32_16x16x32_bf16 v[114:117], a[248:251], a[188:191], v[114:117]\n\t"
"ds_read_b64_tr_b16 v[224:225], v244 offset:35840\n\t"
"v_mfma_f32_16x16x32_bf16 v[114:117], a[252:255], a[192:195], v[114:117]\n\t"
"ds_write_b64 v243, v[230:231] offset:1536\n\t"
"v_mfma_f32_16x16x32_bf16 v[118:121], a[244:247], a[188:191], v[118:121]\n\t"
"ds_read_b64_tr_b16 v[226:227], v244 offset:40448\n\t"
"v_mfma_f32_16x16x32_bf16 v[118:121], a[248:251], a[192:195], v[118:121]\n\t"
"ds_read_b64_tr_b16 v[228:229], v244 offset:43520\n\t"
"v_mfma_f32_16x16x32_bf16 v[118:121], a[252:255], a[196:199], v[118:121]\n\t"
"s_mov_b32 s20, 331776\n\t"
"buffer_load_dwordx4 a[232:235], v252, s[8:11], s20 offen offset:0 sc1\n\t"
"s_waitcnt lgkmcnt(6)\n\t"
"v_mfma_f32_16x16x32_bf16 v[234:237], v[234:237], v[218:221], 0\n\t"
"ds_read_b64_tr_b16 v[230:231], v244 offset:46592\n\t"
"ds_read_b64_tr_b16 v[232:233], v244 offset:49664\n\t"
"v_mfma_f32_16x16x32_bf16 v[122:125], a[244:247], a[192:195], v[122:125]\n\t"
"v_mfma_f32_16x16x32_bf16 v[122:125], a[248:251], a[196:199], v[122:125]\n\t"
"v_mfma_f32_16x16x32_bf16 v[122:125], a[252:255], a[200:203], v[122:125]\n\t"
"s_waitcnt lgkmcnt(5)\n\t"
"v_mfma_f32_16x16x32_bf16 v[222:225], v[222:225], v[218:221], 0\n\t"
"v_mfma_f32_16x16x32_bf16 v[126:129], a[244:247], a[196:199], v[126:129]\n\t"
"v_cvt_pk_bf16_f32 v234, v234, v235\n\t"
"v_cvt_pk_bf16_f32 v235, v236, v237\n\t"
"v_mfma_f32_16x16x32_bf16 v[126:129], a[248:251], a[200:203], v[126:129]\n\t"
"v_mfma_f32_16x16x32_bf16 v[126:129], a[252:255], a[204:207], v[126:129]\n\t"
"ds_write_b64 v243, v[234:235] offset:2048\n\t"
"s_waitcnt lgkmcnt(3)\n\t"
"v_mfma_f32_16x16x32_bf16 v[226:229], v[226:229], v[218:221], 0\n\t"
"ds_read_b64_tr_b16 v[234:235], v244 offset:52736\n\t"
"s_waitcnt vmcnt(10)\n\t"
"v_mfma_f32_16x16x32_bf16 v[130:133], a[208:211], a[168:171], v[130:133]\n\t"
"v_cvt_pk_bf16_f32 v222, v222, v223\n\t"
"v_cvt_pk_bf16_f32 v223, v224, v225\n\t"
"v_mfma_f32_16x16x32_bf16 v[130:133], a[212:215], a[172:175], v[130:133]\n\t"
"ds_read_b64_tr_b16 v[236:237], v244 offset:55808\n\t"
"v_mfma_f32_16x16x32_bf16 v[130:133], a[216:219], a[176:179], v[130:133]\n\t"
"s_waitcnt lgkmcnt(3)\n\t"
"v_mfma_f32_16x16x32_bf16 v[230:233], v[230:233], v[218:221], 0\n\t"
"ds_write_b64 v243, v[222:223] offset:2560\n\t"
"v_mfma_f32_16x16x32_bf16 v[134:137], a[208:211], a[172:175], v[134:137]\n\t"
"v_cvt_pk_bf16_f32 v226, v226, v227\n\t"
"v_cvt_pk_bf16_f32 v227, v228, v229\n\t"
"v_mfma_f32_16x16x32_bf16 v[134:137], a[212:215], a[176:179], v[134:137]\n\t"
"v_mfma_f32_16x16x32_bf16 v[134:137], a[216:219], a[180:183], v[134:137]\n\t"
"ds_write_b64 v243, v[226:227] offset:3072\n\t"
"v_mfma_f32_16x16x32_bf16 v[138:141], a[208:211], a[176:179], v[138:141]\n\t"
"v_mfma_f32_16x16x32_bf16 v[138:141], a[212:215], a[180:183], v[138:141]\n\t"
"v_cvt_pk_bf16_f32 v230, v230, v231\n\t"
"v_cvt_pk_bf16_f32 v231, v232, v233\n\t"
"v_mfma_f32_16x16x32_bf16 v[138:141], a[216:219], a[184:187], v[138:141]\n\t"
"v_mfma_f32_16x16x32_bf16 v[142:145], a[208:211], a[180:183], v[142:145]\n\t"
"v_mfma_f32_16x16x32_bf16 v[142:145], a[212:215], a[184:187], v[142:145]\n\t"
"v_mfma_f32_16x16x32_bf16 v[142:145], a[216:219], a[188:191], v[142:145]\n\t"
"buffer_load_dwordx4 a[236:239], v252, s[8:11], s20 offen offset:1024 sc1\n\t"
"s_waitcnt lgkmcnt(2)\n\t"
"v_mfma_f32_16x16x32_bf16 v[234:237], v[234:237], v[218:221], 0\n\t"
"ds_write_b64 v243, v[230:231] offset:3584\n\t"
"v_mfma_f32_16x16x32_bf16 v[146:149], a[208:211], a[184:187], v[146:149]\n\t"
"v_mfma_f32_16x16x32_bf16 v[146:149], a[212:215], a[188:191], v[146:149]\n\t"
"v_mfma_f32_16x16x32_bf16 v[146:149], a[216:219], a[192:195], v[146:149]\n\t"
"buffer_load_dwordx4 a[240:243], v252, s[8:11], s20 offen offset:2048 sc1\n\t"
"v_mfma_f32_16x16x32_bf16 v[150:153], a[208:211], a[188:191], v[150:153]\n\t"
"v_mfma_f32_16x16x32_bf16 v[150:153], a[212:215], a[192:195], v[150:153]\n\t"
"s_nop 0\n\t"
"v_cvt_pk_bf16_f32 v234, v234, v235\n\t"
"v_cvt_pk_bf16_f32 v235, v236, v237\n\t"
"v_mfma_f32_16x16x32_bf16 v[150:153], a[216:219], a[196:199], v[150:153]\n\t"
"s_mov_b32 s20, 344064\n\t"
"buffer_load_dwordx4 a[244:247], v252, s[8:11], s20 offen offset:0 sc1\n\t"
"v_mfma_f32_16x16x32_bf16 v[154:157], a[208:211], a[192:195], v[154:157]\n\t"
"v_mfma_f32_16x16x32_bf16 v[154:157], a[212:215], a[196:199], v[154:157]\n\t"
"v_mfma_f32_16x16x32_bf16 v[154:157], a[216:219], a[200:203], v[154:157]\n\t"
"ds_write_b64 v243, v[234:235] offset:4096\n\t"
"v_mfma_f32_16x16x32_bf16 v[158:161], a[208:211], a[196:199], v[158:161]\n\t"
"v_mfma_f32_16x16x32_bf16 v[158:161], a[212:215], a[200:203], v[158:161]\n\t"
"v_mfma_f32_16x16x32_bf16 v[158:161], a[216:219], a[204:207], v[158:161]\n\t"
"buffer_load_dwordx4 a[248:251], v252, s[8:11], s20 offen offset:1024 sc1\n\t"
"s_waitcnt vmcnt(11)\n\t"
"v_mfma_f32_16x16x32_bf16 v[162:165], a[220:223], a[168:171], v[162:165]\n\t"
"v_mfma_f32_16x16x32_bf16 v[162:165], a[224:227], a[172:175], v[162:165]\n\t"
"v_mfma_f32_16x16x32_bf16 v[162:165], a[228:231], a[176:179], v[162:165]\n\t"
"buffer_load_dwordx4 a[208:211], v252, s[8:11], s20 offen offset:2048 sc1\n\t"
"s_waitcnt lgkmcnt(0)\n\t"
"s_barrier\n\t"
"v_mfma_f32_16x16x32_bf16 v[166:169], a[220:223], a[172:175], v[166:169]\n\t"
"ds_read_b128 a[168:171], v242 offset:0\n\t"
"v_mfma_f32_16x16x32_bf16 v[166:169], a[224:227], a[176:179], v[166:169]\n\t"
"ds_read_b128 a[172:175], v242 offset:512\n\t"
"v_mfma_f32_16x16x32_bf16 v[166:169], a[228:231], a[180:183], v[166:169]\n\t"
"s_mov_b32 s20, 356352\n\t"
"buffer_load_dwordx4 a[212:215], v252, s[8:11], s20 offen offset:0 sc1\n\t"
"v_mfma_f32_16x16x32_bf16 v[170:173], a[220:223], a[176:179], v[170:173]\n\t"
"ds_read_b128 a[176:179], v242 offset:1024\n\t"
"v_mfma_f32_16x16x32_bf16 v[170:173], a[224:227], a[180:183], v[170:173]\n\t"
"v_mfma_f32_16x16x32_bf16 v[170:173], a[228:231], a[184:187], v[170:173]\n\t"
"buffer_load_dwordx4 a[216:219], v252, s[8:11], s20 offen offset:1024 sc1\n\t"
"v_mfma_f32_16x16x32_bf16 v[174:177], a[220:223], a[180:183], v[174:177]\n\t"
"ds_read_b128 a[180:183], v242 offset:1536\n\t"
"v_mfma_f32_16x16x32_bf16 v[174:177], a[224:227], a[184:187], v[174:177]\n\t"
"v_mfma_f32_16x16x32_bf16 v[174:177], a[228:231], a[188:191], v[174:177]\n\t"
"buffer_load_dwordx4 a[252:255], v252, s[8:11], s20 offen offset:2048 sc1\n\t"
"v_mfma_f32_16x16x32_bf16 v[178:181], a[220:223], a[184:187], v[178:181]\n\t"
"ds_read_b128 a[184:187], v242 offset:2048\n\t"
"v_mfma_f32_16x16x32_bf16 v[178:181], a[224:227], a[188:191], v[178:181]\n\t"
"v_mfma_f32_16x16x32_bf16 v[178:181], a[228:231], a[192:195], v[178:181]\n\t"
"v_mfma_f32_16x16x32_bf16 v[182:185], a[220:223], a[188:191], v[182:185]\n\t"
"ds_read_b128 a[188:191], v242 offset:2560\n\t"
"v_mfma_f32_16x16x32_bf16 v[182:185], a[224:227], a[192:195], v[182:185]\n\t"
"v_mfma_f32_16x16x32_bf16 v[182:185], a[228:231], a[196:199], v[182:185]\n\t"
"v_mfma_f32_16x16x32_bf16 v[186:189], a[220:223], a[192:195], v[186:189]\n\t"
"ds_read_b128 a[192:195], v242 offset:3072\n\t"
"v_mfma_f32_16x16x32_bf16 v[186:189], a[224:227], a[196:199], v[186:189]\n\t"
"v_mfma_f32_16x16x32_bf16 v[186:189], a[228:231], a[200:203], v[186:189]\n\t"
"v_mfma_f32_16x16x32_bf16 v[190:193], a[220:223], a[196:199], v[190:193]\n\t"
"ds_read_b128 a[196:199], v242 offset:3584\n\t"
"v_mfma_f32_16x16x32_bf16 v[190:193], a[224:227], a[200:203], v[190:193]\n\t"
"ds_read_b128 a[200:203], v242 offset:4096\n\t"
"v_mfma_f32_16x16x32_bf16 v[190:193], a[228:231], a[204:207], v[190:193]\n\t"
"s_mov_b32 s20, 368640\n\t"
"buffer_load_dwordx4 a[220:223], v252, s[8:11], s20 offen offset:0 sc1\n\t"
"s_waitcnt vmcnt(7) lgkmcnt(6)\n\t"
"v_mfma_f32_16x16x32_bf16 v[194:197], a[232:235], a[168:171], v[194:197]\n\t"
"ds_read_b128 a[204:207], v242 offset:4608\n\t"
"v_mfma_f32_16x16x32_bf16 v[194:197], a[236:239], a[172:175], v[194:197]\n\t"
"v_mfma_f32_16x16x32_bf16 v[194:197], a[240:243], a[176:179], v[194:197]\n\t"
"buffer_load_dwordx4 a[224:227], v252, s[8:11], s20 offen offset:1024 sc1\n\t"
"s_waitcnt lgkmcnt(6)\n\t"
"v_mfma_f32_16x16x32_bf16 v[202:205], a[232:235], a[172:175], v[202:205]\n\t"
"v_mfma_f32_16x16x32_bf16 v[202:205], a[236:239], a[176:179], v[202:205]\n\t"
"v_mfma_f32_16x16x32_bf16 v[202:205], a[240:243], a[180:183], v[202:205]\n\t"
"buffer_load_dwordx4 a[228:231], v252, s[8:11], s20 offen offset:2048 sc1\n\t"
"s_waitcnt lgkmcnt(5)\n\t"
"v_mfma_f32_16x16x32_bf16 v[210:213], a[232:235], a[176:179], v[210:213]\n\t"
"v_mfma_f32_16x16x32_bf16 v[210:213], a[236:239], a[180:183], v[210:213]\n\t"
"v_mfma_f32_16x16x32_bf16 v[210:213], a[240:243], a[184:187], v[210:213]\n\t"
"s_waitcnt lgkmcnt(4)\n\t"
"v_mfma_f32_16x16x32_bf16 a[0:3], a[232:235], a[180:183], a[0:3]\n\t"
"v_mfma_f32_16x16x32_bf16 a[0:3], a[236:239], a[184:187], a[0:3]\n\t"
"v_mfma_f32_16x16x32_bf16 a[0:3], a[240:243], a[188:191], a[0:3]\n\t"
"s_waitcnt lgkmcnt(3)\n\t"
"v_mfma_f32_16x16x32_bf16 a[8:11], a[232:235], a[184:187], a[8:11]\n\t"
"v_mfma_f32_16x16x32_bf16 a[8:11], a[236:239], a[188:191], a[8:11]\n\t"
"v_mfma_f32_16x16x32_bf16 a[8:11], a[240:243], a[192:195], a[8:11]\n\t"
"s_waitcnt lgkmcnt(2)\n\t"
"v_mfma_f32_16x16x32_bf16 a[16:19], a[232:235], a[188:191], a[16:19]\n\t"
"v_mfma_f32_16x16x32_bf16 a[16:19], a[236:239], a[192:195], a[16:19]\n\t"
"v_mfma_f32_16x16x32_bf16 a[16:19], a[240:243], a[196:199], a[16:19]\n\t"
"s_waitcnt lgkmcnt(1)\n\t"
"v_mfma_f32_16x16x32_bf16 a[24:27], a[232:235], a[192:195], a[24:27]\n\t"
"v_mfma_f32_16x16x32_bf16 a[24:27], a[236:239], a[196:199], a[24:27]\n\t"
"v_mfma_f32_16x16x32_bf16 a[24:27], a[240:243], a[200:203], a[24:27]\n\t"
"s_waitcnt lgkmcnt(0)\n\t"
"v_mfma_f32_16x16x32_bf16 a[32:35], a[232:235], a[196:199], a[32:35]\n\t"
"v_mfma_f32_16x16x32_bf16 a[32:35], a[236:239], a[200:203], a[32:35]\n\t"
"v_mfma_f32_16x16x32_bf16 a[32:35], a[240:243], a[204:207], a[32:35]\n\t"
"s_mov_b32 s20, 380928\n\t"
"buffer_load_dwordx4 a[232:235], v252, s[8:11], s20 offen offset:0 sc1\n\t"
"s_waitcnt vmcnt(7)\n\t"
"v_mfma_f32_16x16x32_bf16 a[40:43], a[244:247], a[168:171], a[40:43]\n\t"
"v_mfma_f32_16x16x32_bf16 a[40:43], a[248:251], a[172:175], a[40:43]\n\t"
"v_mfma_f32_16x16x32_bf16 a[40:43], a[208:211], a[176:179], a[40:43]\n\t"
"buffer_load_dwordx4 a[236:239], v252, s[8:11], s20 offen offset:1024 sc1\n\t"
"v_mfma_f32_16x16x32_bf16 a[48:51], a[244:247], a[172:175], a[48:51]\n\t"
"v_mfma_f32_16x16x32_bf16 a[48:51], a[248:251], a[176:179], a[48:51]\n\t"
"v_mfma_f32_16x16x32_bf16 a[48:51], a[208:211], a[180:183], a[48:51]\n\t"
"buffer_load_dwordx4 a[240:243], v252, s[8:11], s20 offen offset:2048 sc1\n\t"
"v_mfma_f32_16x16x32_bf16 a[56:59], a[244:247], a[176:179], a[56:59]\n\t"
"v_mfma_f32_16x16x32_bf16 a[56:59], a[248:251], a[180:183], a[56:59]\n\t"
"v_mfma_f32_16x16x32_bf16 a[56:59], a[208:211], a[184:187], a[56:59]\n\t"
"v_mfma_f32_16x16x32_bf16 a[64:67], a[244:247], a[180:183], a[64:67]\n\t"
"v_mfma_f32_16x16x32_bf16 a[64:67], a[248:251], a[184:187], a[64:67]\n\t"
"v_mfma_f32_16x16x32_bf16 a[64:67], a[208:211], a[188:191], a[64:67]\n\t"
"s_barrier\n\t"
"v_mfma_f32_16x16x32_bf16 a[72:75], a[244:247], a[184:187], a[72:75]\n\t"
"ds_read_b64_tr_b16 v[222:223], v244 offset:1024\n\t"
"v_mfma_f32_16x16x32_bf16 a[72:75], a[248:251], a[188:191], a[72:75]\n\t"
"ds_read_b64_tr_b16 v[224:225], v244 offset:4096\n\t"
"v_mfma_f32_16x16x32_bf16 a[72:75], a[208:211], a[192:195], a[72:75]\n\t"
"ds_read_b64_tr_b16 v[226:227], v244 offset:7168\n\t"
"ds_read_b64_tr_b16 v[228:229], v244 offset:10240\n\t"
"v_mfma_f32_16x16x32_bf16 a[80:83], a[244:247], a[188:191], a[80:83]\n\t"
"v_mfma_f32_16x16x32_bf16 a[80:83], a[248:251], a[192:195], a[80:83]\n\t"
"v_mfma_f32_16x16x32_bf16 a[80:83], a[208:211], a[196:199], a[80:83]\n\t"
"v_mfma_f32_16x16x32_bf16 a[88:91], a[244:247], a[192:195], a[88:91]\n\t"
"v_mfma_f32_16x16x32_bf16 a[88:91], a[248:251], a[196:199], a[88:91]\n\t"
"ds_read_b64_tr_b16 v[230:231], v244 offset:13312\n\t"
"v_mfma_f32_16x16x32_bf16 a[88:91], a[208:211], a[200:203], a[88:91]\n\t"
"ds_read_b64_tr_b16 v[232:233], v244 offset:16384\n\t"
"s_waitcnt lgkmcnt(4)\n\t"
"v_mfma_f32_16x16x32_bf16 v[222:225], v[222:225], v[218:221], 0\n\t"
"v_mfma_f32_16x16x32_bf16 a[96:99], a[244:247], a[196:199], a[96:99]\n\t"
"v_mfma_f32_16x16x32_bf16 a[96:99], a[248:251], a[200:203], a[96:99]\n\t"
"v_mfma_f32_16x16x32_bf16 a[96:99], a[208:211], a[204:207], a[96:99]\n\t"
"s_mov_b32 s20, 393216\n\t"
"buffer_load_dwordx4 a[244:247], v252, s[8:11], s20 offen offset:0 sc1\n\t"
"s_waitcnt vmcnt(7)\n\t"
"v_mfma_f32_16x16x32_bf16 a[104:107], a[212:215], a[168:171], a[104:107]\n\t"
"ds_read_b128 a[168:171], v242 offset:9216\n\t"
"v_mfma_f32_16x16x32_bf16 a[104:107], a[216:219], a[172:175], a[104:107]\n\t"
"v_cvt_pk_bf16_f32 v222, v222, v223\n\t"
"v_cvt_pk_bf16_f32 v223, v224, v225\n\t"
"v_mfma_f32_16x16x32_bf16 a[104:107], a[252:255], a[176:179], a[104:107]\n\t"
"buffer_load_dwordx4 a[248:251], v252, s[8:11], s20 offen offset:1024 sc1\n\t"
"s_waitcnt lgkmcnt(3)\n\t"
"v_mfma_f32_16x16x32_bf16 v[226:229], v[226:229], v[218:221], 0\n\t"
"ds_read_b64_tr_b16 v[234:235], v244 offset:20992\n\t"
"v_mfma_f32_16x16x32_bf16 a[112:115], a[212:215], a[172:175], a[112:115]\n\t"
"ds_read_b64_tr_b16 v[236:237], v244 offset:24064\n\t"
"v_mfma_f32_16x16x32_bf16 a[112:115], a[216:219], a[176:179], a[112:115]\n\t"
"v_mfma_f32_16x16x32_bf16 a[112:115], a[252:255], a[180:183], a[112:115]\n\t"
"ds_read_b128 a[172:175], v242 offset:9728\n\t"
"v_mfma_f32_16x16x32_bf16 a[120:123], a[212:215], a[176:179], a[120:123]\n\t"
"v_mfma_f32_16x16x32_bf16 a[120:123], a[216:219], a[180:183], a[120:123]\n\t"
"v_cvt_pk_bf16_f32 v226, v226, v227\n\t"
"v_mfma_f32_16x16x32_bf16 a[120:123], a[252:255], a[184:187], a[120:123]\n\t"
"v_cvt_pk_bf16_f32 v227, v228, v229\n\t"
"s_waitcnt lgkmcnt(4)\n\t"
"v_mfma_f32_16x16x32_bf16 v[230:233], v[230:233], v[218:221], 0\n\t"
"ds_read_b128 a[176:179], v242 offset:10240\n\t"
"v_mfma_f32_16x16x32_bf16 a[128:131], a[212:215], a[180:183], a[128:131]\n\t"
"ds_read_b128 a[180:183], v242 offset:10752\n\t"
"v_mfma_f32_16x16x32_bf16 a[128:131], a[216:219], a[184:187], a[128:131]\n\t"
"v_mfma_f32_16x16x32_bf16 a[128:131], a[252:255], a[188:191], a[128:131]\n\t"
"v_mfma_f32_16x16x32_bf16 a[136:139], a[212:215], a[184:187], a[136:139]\n\t"
"ds_read_b128 a[184:187], v242 offset:11264\n\t"
"v_mfma_f32_16x16x32_bf16 a[136:139], a[216:219], a[188:191], a[136:139]\n\t"
"v_cvt_pk_bf16_f32 v230, v230, v231\n\t"
"v_mfma_f32_16x16x32_bf16 a[136:139], a[252:255], a[192:195], a[136:139]\n\t"
"v_cvt_pk_bf16_f32 v231, v232, v233\n\t"
"s_waitcnt lgkmcnt(4)\n\t"
"v_mfma_f32_16x16x32_bf16 v[234:237], v[234:237], v[218:221], 0\n\t"
"v_mfma_f32_16x16x32_bf16 a[144:147], a[212:215], a[188:191], a[144:147]\n\t"
"ds_read_b128 a[188:191], v242 offset:11776\n\t"
"v_mfma_f32_16x16x32_bf16 a[144:147], a[216:219], a[192:195], a[144:147]\n\t"
"v_mfma_f32_16x16x32_bf16 a[144:147], a[252:255], a[196:199], a[144:147]\n\t"
"v_mfma_f32_16x16x32_bf16 a[152:155], a[212:215], a[192:195], a[152:155]\n\t"
"ds_read_b128 a[192:195], v242 offset:12288\n\t"
"v_mfma_f32_16x16x32_bf16 a[152:155], a[216:219], a[196:199], a[152:155]\n\t"
"s_nop 0\n\t"
"v_cvt_pk_bf16_f32 v234, v234, v235\n\t"
"v_mfma_f32_16x16x32_bf16 a[152:155], a[252:255], a[200:203], a[152:155]\n\t"
"v_cvt_pk_bf16_f32 v235, v236, v237\n\t"
"v_mfma_f32_16x16x32_bf16 a[160:163], a[212:215], a[196:199], a[160:163]\n\t"
"ds_read_b128 a[196:199], v242 offset:12800\n\t"
"v_mfma_f32_16x16x32_bf16 a[160:163], a[216:219], a[200:203], a[160:163]\n\t"
"ds_read_b128 a[200:203], v242 offset:13312\n\t"
"v_mfma_f32_16x16x32_bf16 a[160:163], a[252:255], a[204:207], a[160:163]\n\t"
"buffer_load_dwordx4 a[212:215], v252, s[8:11], s20 offen offset:2048 sc1\n\t"
"s_waitcnt vmcnt(6) lgkmcnt(6)\n\t"
"v_mfma_f32_16x16x32_bf16 v[198:201], a[220:223], a[168:171], v[198:201]\n\t"
"ds_read_b128 a[204:207], v242 offset:13824\n\t"
"v_mfma_f32_16x16x32_bf16 v[198:201], a[224:227], a[172:175], v[198:201]\n\t"
"v_mfma_f32_16x16x32_bf16 v[198:201], a[228:231], a[176:179], v[198:201]\n\t"
"s_mov_b32 s20, 552960\n\t"
"buffer_load_dwordx4 a[216:219], v252, s[8:11], s20 offen offset:0 sc1\n\t"
"s_waitcnt lgkmcnt(6)\n\t"
"v_mfma_f32_16x16x32_bf16 v[206:209], a[220:223], a[172:175], v[206:209]\n\t"
"v_mfma_f32_16x16x32_bf16 v[206:209], a[224:227], a[176:179], v[206:209]\n\t"
"v_mfma_f32_16x16x32_bf16 v[206:209], a[228:231], a[180:183], v[206:209]\n\t"
"buffer_load_dwordx4 a[252:255], v252, s[8:11], s20 offen offset:1024 sc1\n\t"
"s_waitcnt lgkmcnt(0)\n\t"
"s_barrier\n\t"
"v_mfma_f32_16x16x32_bf16 v[214:217], a[220:223], a[176:179], v[214:217]\n\t"
"v_mfma_f32_16x16x32_bf16 v[214:217], a[224:227], a[180:183], v[214:217]\n\t"
"v_mfma_f32_16x16x32_bf16 v[214:217], a[228:231], a[184:187], v[214:217]\n\t"
"ds_write_b64 v243, v[222:223] offset:0\n\t"
"ds_write_b64 v243, v[226:227] offset:512\n\t"
"v_mfma_f32_16x16x32_bf16 a[4:7], a[220:223], a[180:183], a[4:7]\n\t"
"ds_read_b64_tr_b16 v[222:223], v244 offset:27136\n\t"
"v_mfma_f32_16x16x32_bf16 a[4:7], a[224:227], a[184:187], a[4:7]\n\t"
"ds_read_b64_tr_b16 v[224:225], v244 offset:30208\n\t"
"v_mfma_f32_16x16x32_bf16 a[4:7], a[228:231], a[188:191], a[4:7]\n\t"
"ds_write_b64 v243, v[230:231] offset:1024\n\t"
"v_mfma_f32_16x16x32_bf16 a[12:15], a[220:223], a[184:187], a[12:15]\n\t"
"ds_read_b64_tr_b16 v[226:227], v244 offset:33280\n\t"
"v_mfma_f32_16x16x32_bf16 a[12:15], a[224:227], a[188:191], a[12:15]\n\t"
"ds_read_b64_tr_b16 v[228:229], v244 offset:36352\n\t"
"v_mfma_f32_16x16x32_bf16 a[12:15], a[228:231], a[192:195], a[12:15]\n\t"
"ds_write_b64 v243, v[234:235] offset:1536\n\t"
"v_mfma_f32_16x16x32_bf16 a[20:23], a[220:223], a[188:191], a[20:23]\n\t"
"ds_read_b64_tr_b16 v[230:231], v244 offset:40960\n\t"
"v_mfma_f32_16x16x32_bf16 a[20:23], a[224:227], a[192:195], a[20:23]\n\t"
"ds_read_b64_tr_b16 v[232:233], v244 offset:44032\n\t"
"v_mfma_f32_16x16x32_bf16 a[20:23], a[228:231], a[196:199], a[20:23]\n\t"
"global_load_dwordx4 v[238:241], v252, s[46:47] offset:0\n\t"
"s_waitcnt lgkmcnt(6)\n\t"
"v_mfma_f32_16x16x32_bf16 v[222:225], v[222:225], v[218:221], 0\n\t"
"ds_read_b64_tr_b16 v[234:235], v244 offset:47104\n\t"
"ds_read_b64_tr_b16 v[236:237], v244 offset:50176\n\t"
"v_mfma_f32_16x16x32_bf16 a[28:31], a[220:223], a[192:195], a[28:31]\n\t"
"v_mfma_f32_16x16x32_bf16 a[28:31], a[224:227], a[196:199], a[28:31]\n\t"
"v_mfma_f32_16x16x32_bf16 a[28:31], a[228:231], a[200:203], a[28:31]\n\t"
"global_load_dwordx4 a[208:211], v252, s[46:47] offset:1024\n\t"
"s_waitcnt lgkmcnt(5)\n\t"
"v_mfma_f32_16x16x32_bf16 v[226:229], v[226:229], v[218:221], 0\n\t"
"v_mfma_f32_16x16x32_bf16 a[36:39], a[220:223], a[196:199], a[36:39]\n\t"
"v_cvt_pk_bf16_f32 v222, v222, v223\n\t"
"v_cvt_pk_bf16_f32 v223, v224, v225\n\t"
"v_mfma_f32_16x16x32_bf16 a[36:39], a[224:227], a[200:203], a[36:39]\n\t"
"v_mfma_f32_16x16x32_bf16 a[36:39], a[228:231], a[204:207], a[36:39]\n\t"
"buffer_load_dwordx4 a[220:223], v252, s[8:11], s20 offen offset:2048 sc1\n\t"
"s_waitcnt lgkmcnt(2)\n\t"
"v_mfma_f32_16x16x32_bf16 v[230:233], v[230:233], v[218:221], 0\n\t"
"s_mov_b32 s20, 565248\n\t"
"buffer_load_dwordx4 a[224:227], v252, s[8:11], s20 offen offset:0 sc1\n\t"
"s_waitcnt vmcnt(9)\n\t"
"v_mfma_f32_16x16x32_bf16 a[44:47], a[232:235], a[168:171], a[44:47]\n\t"
"v_cvt_pk_bf16_f32 v226, v226, v227\n\t"
"v_cvt_pk_bf16_f32 v227, v228, v229\n\t"
"v_mfma_f32_16x16x32_bf16 a[44:47], a[236:239], a[172:175], a[44:47]\n\t"
"v_mfma_f32_16x16x32_bf16 a[44:47], a[240:243], a[176:179], a[44:47]\n\t"
"ds_write_b64 v243, v[222:223] offset:2048\n\t"
"s_waitcnt lgkmcnt(1)\n\t"
"v_mfma_f32_16x16x32_bf16 v[234:237], v[234:237], v[218:221], 0\n\t"
"ds_read_b64_tr_b16 v[222:223], v244 offset:53248\n\t"
"ds_read_b64_tr_b16 v[224:225], v244 offset:56320\n\t"
"v_mfma_f32_16x16x32_bf16 a[52:55], a[232:235], a[172:175], a[52:55]\n\t"
"v_cvt_pk_bf16_f32 v230, v230, v231\n\t"
"v_cvt_pk_bf16_f32 v231, v232, v233\n\t"
"v_mfma_f32_16x16x32_bf16 a[52:55], a[236:239], a[176:179], a[52:55]\n\t"
"v_mfma_f32_16x16x32_bf16 a[52:55], a[240:243], a[180:183], a[52:55]\n\t"
"buffer_load_dwordx4 a[228:231], v252, s[8:11], s20 offen offset:1024 sc1\n\t"
"v_mfma_f32_16x16x32_bf16 a[60:63], a[232:235], a[176:179], a[60:63]\n\t"
"v_mfma_f32_16x16x32_bf16 a[60:63], a[236:239], a[180:183], a[60:63]\n\t"
"v_cvt_pk_bf16_f32 v234, v234, v235\n\t"
"v_cvt_pk_bf16_f32 v235, v236, v237\n\t"
"v_mfma_f32_16x16x32_bf16 a[60:63], a[240:243], a[184:187], a[60:63]\n\t"
"ds_write_b64 v243, v[226:227] offset:2560\n\t"
"v_mfma_f32_16x16x32_bf16 a[68:71], a[232:235], a[180:183], a[68:71]\n\t"
"v_mfma_f32_16x16x32_bf16 a[68:71], a[236:239], a[184:187], a[68:71]\n\t"
"v_mfma_f32_16x16x32_bf16 a[68:71], a[240:243], a[188:191], a[68:71]\n\t"
"ds_write_b64 v243, v[230:231] offset:3072\n\t"
"s_waitcnt lgkmcnt(2)\n\t"
"v_mfma_f32_16x16x32_bf16 v[222:225], v[222:225], v[218:221], 0\n\t"
"ds_write_b64 v243, v[234:235] offset:3584\n\t"
"v_mfma_f32_16x16x32_bf16 a[76:79], a[232:235], a[184:187], a[76:79]\n\t"
"v_mfma_f32_16x16x32_bf16 a[76:79], a[236:239], a[188:191], a[76:79]\n\t"
"v_mfma_f32_16x16x32_bf16 a[76:79], a[240:243], a[192:195], a[76:79]\n\t"
"v_mfma_f32_16x16x32_bf16 a[84:87], a[232:235], a[188:191], a[84:87]\n\t"
"v_mfma_f32_16x16x32_bf16 a[84:87], a[236:239], a[192:195], a[84:87]\n\t"
"s_nop 1\n\t"
"v_cvt_pk_bf16_f32 v222, v222, v223\n\t"
"v_cvt_pk_bf16_f32 v223, v224, v225\n\t"
"v_mfma_f32_16x16x32_bf16 a[84:87], a[240:243], a[196:199], a[84:87]\n\t"
"v_mfma_f32_16x16x32_bf16 a[92:95], a[232:235], a[192:195], a[92:95]\n\t"
"v_mfma_f32_16x16x32_bf16 a[92:95], a[236:239], a[196:199], a[92:95]\n\t"
"v_mfma_f32_16x16x32_bf16 a[92:95], a[240:243], a[200:203], a[92:95]\n\t"
"ds_write_b64 v243, v[222:223] offset:4096\n\t"
"v_mfma_f32_16x16x32_bf16 a[100:103], a[232:235], a[196:199], a[100:103]\n\t"
"s_waitcnt vmcnt(4)\n\t"
"ds_read_b64_tr_b16 v[230:231], v238 offset:1024\n\t"
"v_mfma_f32_16x16x32_bf16 a[100:103], a[236:239], a[200:203], a[100:103]\n\t"
"ds_read_b64_tr_b16 v[232:233], v238 offset:2560\n\t"
"v_mfma_f32_16x16x32_bf16 a[100:103], a[240:243], a[204:207], a[100:103]\n\t"
"buffer_load_dwordx4 a[232:235], v252, s[8:11], s20 offen offset:2048 sc1\n\t"
"v_mfma_f32_16x16x32_bf16 a[108:111], a[244:247], a[168:171], a[108:111]\n\t"
"ds_read_b64_tr_b16 v[234:235], v239 offset:1024\n\t"
"v_mfma_f32_16x16x32_bf16 a[108:111], a[248:251], a[172:175], a[108:111]\n\t"
"ds_read_b64_tr_b16 v[236:237], v239 offset:2560\n\t"
"v_mfma_f32_16x16x32_bf16 a[108:111], a[212:215], a[176:179], a[108:111]\n\t"
"s_mov_b32 s20, 577536\n\t"
"buffer_load_dwordx4 a[236:239], v252, s[8:11], s20 offen offset:0 sc1\n\t"
"s_waitcnt lgkmcnt(4)\n\t"
"s_barrier\n\t"
"v_mfma_f32_16x16x32_bf16 a[116:119], a[244:247], a[172:175], a[116:119]\n\t"
"ds_read_b128 a[168:171], v242 offset:0\n\t"
"v_mfma_f32_16x16x32_bf16 a[116:119], a[248:251], a[176:179], a[116:119]\n\t"
"ds_read_b64_tr_b16 v[222:223], v238 offset:20992\n\t"
"v_mfma_f32_16x16x32_bf16 a[116:119], a[212:215], a[180:183], a[116:119]\n\t"
"buffer_load_dwordx4 a[240:243], v252, s[8:11], s20 offen offset:1024 sc1\n\t"
"v_mfma_f32_16x16x32_bf16 a[124:127], a[244:247], a[176:179], a[124:127]\n\t"
"ds_read_b64_tr_b16 v[224:225], v238 offset:22528\n\t"
"v_mfma_f32_16x16x32_bf16 a[124:127], a[248:251], a[180:183], a[124:127]\n\t"
"ds_read_b64_tr_b16 v[226:227], v239 offset:20992\n\t"
"v_mfma_f32_16x16x32_bf16 a[124:127], a[212:215], a[184:187], a[124:127]\n\t"
"ds_read_b64_tr_b16 v[228:229], v239 offset:22528\n\t"
"v_mfma_f32_16x16x32_bf16 a[132:135], a[244:247], a[180:183], a[132:135]\n\t"
"ds_read_b128 a[172:175], v242 offset:512\n\t"
"v_mfma_f32_16x16x32_bf16 a[132:135], a[248:251], a[184:187], a[132:135]\n\t"
"v_mfma_f32_16x16x32_bf16 a[132:135], a[212:215], a[188:191], a[132:135]\n\t"
"ds_read_b128 a[176:179], v242 offset:1024\n\t"
"s_waitcnt vmcnt(6) lgkmcnt(9)\n\t"
"v_mfma_f32_16x16x32_bf16 v[230:233], v[230:233], a[208:211], 0\n\t"
"ds_read_b128 a[180:183], v242 offset:1536\n\t"
"s_waitcnt lgkmcnt(8)\n\t"
"v_mfma_f32_16x16x32_bf16 v[234:237], v[234:237], a[208:211], 0\n\t"
"v_mfma_f32_16x16x32_bf16 a[140:143], a[244:247], a[184:187], a[140:143]\n\t"
"ds_read_b128 a[184:187], v242 offset:2048\n\t"
"v_mfma_f32_16x16x32_bf16 a[140:143], a[248:251], a[188:191], a[140:143]\n\t"
"v_mfma_f32_16x16x32_bf16 a[140:143], a[212:215], a[192:195], a[140:143]\n\t"
"v_mfma_f32_16x16x32_bf16 a[148:151], a[244:247], a[188:191], a[148:151]\n\t"
"v_cvt_pk_bf16_f32 v230, v230, v231\n\t"
"v_cvt_pk_bf16_f32 v231, v232, v233\n\t"
"v_mfma_f32_16x16x32_bf16 a[148:151], a[248:251], a[192:195], a[148:151]\n\t"
"v_cvt_pk_bf16_f32 v232, v234, v235\n\t"
"v_mfma_f32_16x16x32_bf16 a[148:151], a[212:215], a[196:199], a[148:151]\n\t"
"v_cvt_pk_bf16_f32 v233, v236, v237\n\t"
"ds_read_b128 a[188:191], v242 offset:2560\n\t"
"s_waitcnt lgkmcnt(7)\n\t"
"v_mfma_f32_16x16x32_bf16 v[222:225], v[222:225], a[208:211], 0\n\t"
"s_waitcnt lgkmcnt(5)\n\t"
"v_mfma_f32_16x16x32_bf16 v[226:229], v[226:229], a[208:211], 0\n\t"
"s_mov_b64 exec, s[44:45]\n\t"
"ds_write_b128 v240, v[230:233] offset:18432\n\t"
"s_mov_b64 exec, -1\n\t"
"v_mfma_f32_16x16x32_bf16 a[156:159], a[244:247], a[192:195], a[156:159]\n\t"
"ds_read_b128 a[192:195], v242 offset:3072\n\t"
"v_mfma_f32_16x16x32_bf16 a[156:159], a[248:251], a[196:199], a[156:159]\n\t"
"ds_read_b64_tr_b16 v[230:231], v238 offset:40960\n\t"
"v_mfma_f32_16x16x32_bf16 a[156:159], a[212:215], a[200:203], a[156:159]\n\t"
"ds_read_b64_tr_b16 v[232:233], v238 offset:42496\n\t"
"v_mfma_f32_16x16x32_bf16 a[164:167], a[244:247], a[196:199], a[164:167]\n\t"
"v_cvt_pk_bf16_f32 v222, v222, v223\n\t"
"v_cvt_pk_bf16_f32 v223, v224, v225\n\t"
"v_mfma_f32_16x16x32_bf16 a[164:167], a[248:251], a[200:203], a[164:167]\n\t"
"v_cvt_pk_bf16_f32 v224, v226, v227\n\t"
"v_cvt_pk_bf16_f32 v225, v228, v229\n\t"
"v_mfma_f32_16x16x32_bf16 a[164:167], a[212:215], a[204:207], a[164:167]\n\t"
"buffer_load_dwordx4 a[244:247], v252, s[8:11], s20 offen offset:2048 sc1\n\t"
"s_waitcnt vmcnt(6) lgkmcnt(7)\n\t"
"v_mfma_f32_16x16x32_bf16 v[194:197], a[216:219], a[168:171], v[194:197]\n\t"
"ds_read_b64_tr_b16 v[234:235], v239 offset:40960\n\t"
"v_mfma_f32_16x16x32_bf16 v[194:197], a[252:255], a[172:175], v[194:197]\n\t"
"ds_read_b64_tr_b16 v[236:237], v239 offset:42496\n\t"
"v_add_u32 v238, 59904, v238\n\t"
"v_mfma_f32_16x16x32_bf16 v[194:197], a[220:223], a[176:179], v[194:197]\n\t"
"s_mov_b32 s20, 589824\n\t"
"buffer_load_dwordx4 a[212:215], v252, s[8:11], s20 offen offset:0 sc1\n\t"
"s_waitcnt lgkmcnt(8)\n\t"
"v_mfma_f32_16x16x32_bf16 v[202:205], a[216:219], a[172:175], v[202:205]\n\t"
"ds_read_b128 a[196:199], v242 offset:3584\n\t"
"v_add_u32 v239, 59904, v239\n\t"
"v_mfma_f32_16x16x32_bf16 v[202:205], a[252:255], a[176:179], v[202:205]\n\t"
"ds_read_b128 a[200:203], v242 offset:4096\n\t"
"v_mfma_f32_16x16x32_bf16 v[202:205], a[220:223], a[180:183], v[202:205]\n\t"
"s_mov_b64 exec, s[44:45]\n\t"
"ds_write_b128 v240, v[222:225] offset:19968\n\t"
"s_mov_b64 exec, -1\n\t"
"s_waitcnt lgkmcnt(10)\n\t"
"v_mfma_f32_16x16x32_bf16 v[210:213], a[216:219], a[176:179], v[210:213]\n\t"
"ds_read_b64_tr_b16 v[222:223], v238 offset:1024\n\t"
"v_mfma_f32_16x16x32_bf16 v[210:213], a[252:255], a[180:183], v[210:213]\n\t"
"ds_read_b64_tr_b16 v[224:225], v238 offset:2560\n\t"
"v_mfma_f32_16x16x32_bf16 v[210:213], a[220:223], a[184:187], v[210:213]\n\t"
"buffer_load_dwordx4 a[248:251], v252, s[8:11], s20 offen offset:1024 sc1\n\t"
"s_waitcnt lgkmcnt(7)\n\t"
"v_mfma_f32_16x16x32_bf16 v[230:233], v[230:233], a[208:211], 0\n\t"
"ds_read_b64_tr_b16 v[226:227], v239 offset:1024\n\t"
"ds_read_b64_tr_b16 v[228:229], v239 offset:2560\n\t"
"s_waitcnt lgkmcnt(7)\n\t"
"v_mfma_f32_16x16x32_bf16 v[234:237], v[234:237], a[208:211], 0\n\t"
"ds_read_b128 a[204:207], v242 offset:4608\n\t"
"v_mfma_f32_16x16x32_bf16 a[0:3], a[216:219], a[180:183], a[0:3]\n\t"
"v_mfma_f32_16x16x32_bf16 a[0:3], a[252:255], a[184:187], a[0:3]\n\t"
"v_mfma_f32_16x16x32_bf16 a[0:3], a[220:223], a[188:191], a[0:3]\n\t"
"v_mfma_f32_16x16x32_bf16 a[8:11], a[216:219], a[184:187], a[8:11]\n\t"
"v_cvt_pk_bf16_f32 v230, v230, v231\n\t"
"v_cvt_pk_bf16_f32 v231, v232, v233\n\t"
"v_mfma_f32_16x16x32_bf16 a[8:11], a[252:255], a[188:191], a[8:11]\n\t"
"v_cvt_pk_bf16_f32 v232, v234, v235\n\t"
"v_mfma_f32_16x16x32_bf16 a[8:11], a[220:223], a[192:195], a[8:11]\n\t"
"v_cvt_pk_bf16_f32 v233, v236, v237\n\t"
"s_waitcnt lgkmcnt(3)\n\t"
"v_mfma_f32_16x16x32_bf16 v[222:225], v[222:225], a[208:211], 0\n\t"
"s_mov_b64 exec, s[44:45]\n\t"
"ds_write_b128 v240, v[230:233] offset:21504\n\t"
"s_mov_b64 exec, -1\n\t"
"s_waitcnt lgkmcnt(2)\n\t"
"v_mfma_f32_16x16x32_bf16 v[226:229], v[226:229], a[208:211], 0\n\t"
"ds_read_b64_tr_b16 v[230:231], v238 offset:20992\n\t"
"v_mfma_f32_16x16x32_bf16 a[16:19], a[216:219], a[188:191], a[16:19]\n\t"
"v_mfma_f32_16x16x32_bf16 a[16:19], a[252:255], a[192:195], a[16:19]\n\t"
"ds_read_b64_tr_b16 v[232:233], v238 offset:22528\n\t"
"v_mfma_f32_16x16x32_bf16 a[16:19], a[220:223], a[196:199], a[16:19]\n\t"
"ds_read_b64_tr_b16 v[234:235], v239 offset:20992\n\t"
"v_mfma_f32_16x16x32_bf16 a[24:27], a[216:219], a[192:195], a[24:27]\n\t"
"v_cvt_pk_bf16_f32 v222, v222, v223\n\t"
"v_mfma_f32_16x16x32_bf16 a[24:27], a[252:255], a[196:199], a[24:27]\n\t"
"v_cvt_pk_bf16_f32 v223, v224, v225\n\t"
"v_cvt_pk_bf16_f32 v224, v226, v227\n\t"
"v_mfma_f32_16x16x32_bf16 a[24:27], a[220:223], a[200:203], a[24:27]\n\t"
"v_cvt_pk_bf16_f32 v225, v228, v229\n\t"
"ds_read_b64_tr_b16 v[236:237], v239 offset:22528\n\t"
"s_waitcnt lgkmcnt(5)\n\t"
"v_mfma_f32_16x16x32_bf16 a[32:35], a[216:219], a[196:199], a[32:35]\n\t"
"v_mfma_f32_16x16x32_bf16 a[32:35], a[252:255], a[200:203], a[32:35]\n\t"
"v_mfma_f32_16x16x32_bf16 a[32:35], a[220:223], a[204:207], a[32:35]\n\t"
"buffer_load_dwordx4 a[216:219], v252, s[8:11], s20 offen offset:2048 sc1\n\t"
"s_waitcnt vmcnt(6)\n\t"
"v_mfma_f32_16x16x32_bf16 a[40:43], a[224:227], a[168:171], a[40:43]\n\t"
"v_mfma_f32_16x16x32_bf16 a[40:43], a[228:231], a[172:175], a[40:43]\n\t"
"v_mfma_f32_16x16x32_bf16 a[40:43], a[232:235], a[176:179], a[40:43]\n\t"
"s_mov_b64 exec, s[44:45]\n\t"
"ds_write_b128 v240, v[222:225] offset:23040\n\t"
"s_mov_b64 exec, -1\n\t"
"v_mfma_f32_16x16x32_bf16 a[48:51], a[224:227], a[172:175], a[48:51]\n\t"
"ds_read_b64_tr_b16 v[222:223], v238 offset:40960\n\t"
"v_mfma_f32_16x16x32_bf16 a[48:51], a[228:231], a[176:179], a[48:51]\n\t"
"ds_read_b64_tr_b16 v[224:225], v238 offset:42496\n\t"
"v_mfma_f32_16x16x32_bf16 a[48:51], a[232:235], a[180:183], a[48:51]\n\t"
"s_mov_b32 s20, 602112\n\t"
"buffer_load_dwordx4 a[220:223], v252, s[8:11], s20 offen offset:0 sc1\n\t"
"s_waitcnt lgkmcnt(5)\n\t"
"v_mfma_f32_16x16x32_bf16 v[230:233], v[230:233], a[208:211], 0\n\t"
"buffer_load_dwordx4 a[252:255], v252, s[8:11], s20 offen offset:1024 sc1\n\t"
"s_waitcnt lgkmcnt(3)\n\t"
"v_mfma_f32_16x16x32_bf16 v[234:237], v[234:237], a[208:211], 0\n\t"
"ds_read_b64_tr_b16 v[226:227], v239 offset:40960\n\t"
"ds_read_b64_tr_b16 v[228:229], v239 offset:42496\n\t"
"v_mfma_f32_16x16x32_bf16 a[56:59], a[224:227], a[176:179], a[56:59]\n\t"
"v_xor_b32 v244, 0xc0, v244\n\t"
"v_mfma_f32_16x16x32_bf16 a[56:59], a[228:231], a[180:183], a[56:59]\n\t"
"v_mfma_f32_16x16x32_bf16 a[56:59], a[232:235], a[184:187], a[56:59]\n\t"
"v_mfma_f32_16x16x32_bf16 a[64:67], a[224:227], a[180:183], a[64:67]\n\t"
"v_cvt_pk_bf16_f32 v230, v230, v231\n\t"
"v_mfma_f32_16x16x32_bf16 a[64:67], a[228:231], a[184:187], a[64:67]\n\t"
"v_cvt_pk_bf16_f32 v231, v232, v233\n\t"
"v_cvt_pk_bf16_f32 v232, v234, v235\n\t"
"v_mfma_f32_16x16x32_bf16 a[64:67], a[232:235], a[188:191], a[64:67]\n\t"
"v_cvt_pk_bf16_f32 v233, v236, v237\n\t"
"s_waitcnt lgkmcnt(2)\n\t"
"v_mfma_f32_16x16x32_bf16 v[222:225], v[222:225], a[208:211], 0\n\t"
"s_waitcnt lgkmcnt(0)\n\t"
"v_mfma_f32_16x16x32_bf16 v[226:229], v[226:229], a[208:211], 0\n\t"
"s_mov_b64 exec, s[44:45]\n\t"
"ds_write_b128 v240, v[230:233] offset:24576\n\t"
"s_mov_b64 exec, -1\n\t"
"v_mfma_f32_16x16x32_bf16 a[72:75], a[224:227], a[184:187], a[72:75]\n\t"
"v_mfma_f32_16x16x32_bf16 a[72:75], a[228:231], a[188:191], a[72:75]\n\t"
"v_mfma_f32_16x16x32_bf16 a[72:75], a[232:235], a[192:195], a[72:75]\n\t"
"buffer_load_dwordx4 a[208:211], v252, s[8:11], s20 offen offset:2048 sc1\n\t"
"v_mfma_f32_16x16x32_bf16 a[80:83], a[224:227], a[188:191], a[80:83]\n\t"
"v_cvt_pk_bf16_f32 v222, v222, v223\n\t"
"v_cvt_pk_bf16_f32 v223, v224, v225\n\t"
"v_mfma_f32_16x16x32_bf16 a[80:83], a[228:231], a[192:195], a[80:83]\n\t"
"v_cvt_pk_bf16_f32 v224, v226, v227\n\t"
"v_mfma_f32_16x16x32_bf16 a[80:83], a[232:235], a[196:199], a[80:83]\n\t"
"v_cvt_pk_bf16_f32 v225, v228, v229\n\t"
"ds_read_b64_tr_b16 v[230:231], v244 offset:1024\n\t"
"v_mfma_f32_16x16x32_bf16 a[88:91], a[224:227], a[192:195], a[88:91]\n\t"
"ds_read_b64_tr_b16 v[232:233], v244 offset:4096\n\t"
"v_mfma_f32_16x16x32_bf16 a[88:91], a[228:231], a[196:199], a[88:91]\n\t"
"v_mfma_f32_16x16x32_bf16 a[88:91], a[232:235], a[200:203], a[88:91]\n\t"
"s_mov_b64 exec, s[44:45]\n\t"
"ds_write_b128 v240, v[222:225] offset:26112\n\t"
"s_mov_b64 exec, -1\n\t"
"v_mfma_f32_16x16x32_bf16 a[96:99], a[224:227], a[196:199], a[96:99]\n\t"
"ds_read_b64_tr_b16 v[234:235], v244 offset:7168\n\t"
"v_mfma_f32_16x16x32_bf16 a[96:99], a[228:231], a[200:203], a[96:99]\n\t"
"ds_read_b64_tr_b16 v[236:237], v244 offset:10240\n\t"
"v_mfma_f32_16x16x32_bf16 a[96:99], a[232:235], a[204:207], a[96:99]\n\t"
"s_waitcnt vmcnt(6)\n\t"
"v_mfma_f32_16x16x32_bf16 a[104:107], a[236:239], a[168:171], a[104:107]\n\t"
"ds_read_b128 a[168:171], v242 offset:9216\n\t"
"v_mfma_f32_16x16x32_bf16 a[104:107], a[240:243], a[172:175], a[104:107]\n\t"
"ds_read_b64_tr_b16 v[222:223], v244 offset:13312\n\t"
"v_mfma_f32_16x16x32_bf16 a[104:107], a[244:247], a[176:179], a[104:107]\n\t"
"s_mov_b32 s20, 614400\n\t"
"buffer_load_dwordx4 a[224:227], v252, s[8:11], s20 offen offset:0 sc1\n\t"
"v_mfma_f32_16x16x32_bf16 a[112:115], a[236:239], a[172:175], a[112:115]\n\t"
"ds_read_b64_tr_b16 v[224:225], v244 offset:16384\n\t"
"v_mfma_f32_16x16x32_bf16 a[112:115], a[240:243], a[176:179], a[112:115]\n\t"
"ds_read_b128 a[172:175], v242 offset:9728\n\t"
"v_mfma_f32_16x16x32_bf16 a[112:115], a[244:247], a[180:183], a[112:115]\n\t"
"buffer_load_dwordx4 a[228:231], v252, s[8:11], s20 offen offset:1024 sc1\n\t"
"v_mfma_f32_16x16x32_bf16 a[120:123], a[236:239], a[176:179], a[120:123]\n\t"
"ds_read_b64_tr_b16 v[226:227], v244 offset:20992\n\t"
"v_mfma_f32_16x16x32_bf16 a[120:123], a[240:243], a[180:183], a[120:123]\n\t"
"ds_read_b64_tr_b16 v[228:229], v244 offset:24064\n\t"
"v_mfma_f32_16x16x32_bf16 a[120:123], a[244:247], a[184:187], a[120:123]\n\t"
"buffer_load_dwordx4 a[232:235], v252, s[8:11], s20 offen offset:2048 sc1\n\t"
"s_waitcnt lgkmcnt(9)\n\t"
"v_mfma_f32_16x16x32_bf16 v[230:233], v[230:233], v[218:221], 0\n\t"
"v_mfma_f32_16x16x32_bf16 a[128:131], a[236:239], a[180:183], a[128:131]\n\t"
"ds_read_b128 a[176:179], v242 offset:10240\n\t"
"v_mfma_f32_16x16x32_bf16 a[128:131], a[240:243], a[184:187], a[128:131]\n\t"
"ds_read_b128 a[180:183], v242 offset:10752\n\t"
"v_mfma_f32_16x16x32_bf16 a[128:131], a[244:247], a[188:191], a[128:131]\n\t"
"s_waitcnt lgkmcnt(8)\n\t"
"v_mfma_f32_16x16x32_bf16 v[234:237], v[234:237], v[218:221], 0\n\t"
"v_mfma_f32_16x16x32_bf16 a[136:139], a[236:239], a[184:187], a[136:139]\n\t"
"v_cvt_pk_bf16_f32 v230, v230, v231\n\t"
"v_cvt_pk_bf16_f32 v231, v232, v233\n\t"
"v_mfma_f32_16x16x32_bf16 a[136:139], a[240:243], a[188:191], a[136:139]\n\t"
"ds_read_b128 a[184:187], v242 offset:11264\n\t"
"v_mfma_f32_16x16x32_bf16 a[136:139], a[244:247], a[192:195], a[136:139]\n\t"
"s_waitcnt lgkmcnt(6)\n\t"
"v_mfma_f32_16x16x32_bf16 v[222:225], v[222:225], v[218:221], 0\n\t"
"v_mfma_f32_16x16x32_bf16 a[144:147], a[236:239], a[188:191], a[144:147]\n\t"
"v_cvt_pk_bf16_f32 v234, v234, v235\n\t"
"v_cvt_pk_bf16_f32 v235, v236, v237\n\t"
"v_mfma_f32_16x16x32_bf16 a[144:147], a[240:243], a[192:195], a[144:147]\n\t"
"ds_read_b128 a[188:191], v242 offset:11776\n\t"
"v_mfma_f32_16x16x32_bf16 a[144:147], a[244:247], a[196:199], a[144:147]\n\t"
"s_waitcnt lgkmcnt(4)\n\t"
"v_mfma_f32_16x16x32_bf16 v[226:229], v[226:229], v[218:221], 0\n\t"
"v_mfma_f32_16x16x32_bf16 a[152:155], a[236:239], a[192:195], a[152:155]\n\t"
"v_cvt_pk_bf16_f32 v222, v222, v223\n\t"
"v_cvt_pk_bf16_f32 v223, v224, v225\n\t"
"v_mfma_f32_16x16x32_bf16 a[152:155], a[240:243], a[196:199], a[152:155]\n\t"
"ds_read_b128 a[192:195], v242 offset:12288\n\t"
"v_mfma_f32_16x16x32_bf16 a[152:155], a[244:247], a[200:203], a[152:155]\n\t"
"v_mfma_f32_16x16x32_bf16 a[160:163], a[236:239], a[196:199], a[160:163]\n\t"
"ds_read_b128 a[196:199], v242 offset:12800\n\t"
"v_mfma_f32_16x16x32_bf16 a[160:163], a[240:243], a[200:203], a[160:163]\n\t"
"ds_read_b128 a[200:203], v242 offset:13312\n\t"
"v_cvt_pk_bf16_f32 v226, v226, v227\n\t"
"v_mfma_f32_16x16x32_bf16 a[160:163], a[244:247], a[204:207], a[160:163]\n\t"
"s_mov_b32 s20, 479232\n\t"
"buffer_load_dwordx4 a[236:239], v252, s[8:11], s20 offen offset:0 sc1\n\t"
"s_waitcnt vmcnt(7) lgkmcnt(6)\n\t"
"v_mfma_f32_16x16x32_bf16 v[198:201], a[212:215], a[168:171], v[198:201]\n\t"
"ds_read_b128 a[204:207], v242 offset:13824\n\t"
"v_cvt_pk_bf16_f32 v227, v228, v229\n\t"
"v_mfma_f32_16x16x32_bf16 v[198:201], a[248:251], a[172:175], v[198:201]\n\t"
"v_mfma_f32_16x16x32_bf16 v[198:201], a[216:219], a[176:179], v[198:201]\n\t"
"buffer_load_dwordx4 a[240:243], v252, s[8:11], s20 offen offset:1024 sc1\n\t"
"s_waitcnt lgkmcnt(6)\n\t"
"v_mfma_f32_16x16x32_bf16 v[206:209], a[212:215], a[172:175], v[206:209]\n\t"
"v_mfma_f32_16x16x32_bf16 v[206:209], a[248:251], a[176:179], v[206:209]\n\t"
"v_mfma_f32_16x16x32_bf16 v[206:209], a[216:219], a[180:183], v[206:209]\n\t"
"buffer_load_dwordx4 a[244:247], v252, s[8:11], s20 offen offset:2048 sc1\n\t"
"s_waitcnt lgkmcnt(0)\n\t"
"s_barrier\n\t"
"v_mfma_f32_16x16x32_bf16 v[214:217], a[212:215], a[176:179], v[214:217]\n\t"
"v_mfma_f32_16x16x32_bf16 v[214:217], a[248:251], a[180:183], v[214:217]\n\t"
"v_mfma_f32_16x16x32_bf16 v[214:217], a[216:219], a[184:187], v[214:217]\n\t"
"ds_write_b64 v243, v[230:231] offset:0\n\t"
"ds_write_b64 v243, v[234:235] offset:512\n\t"
"v_mfma_f32_16x16x32_bf16 a[4:7], a[212:215], a[180:183], a[4:7]\n\t"
"ds_read_b64_tr_b16 v[230:231], v244 offset:27136\n\t"
"v_mfma_f32_16x16x32_bf16 a[4:7], a[248:251], a[184:187], a[4:7]\n\t"
"ds_read_b64_tr_b16 v[232:233], v244 offset:30208\n\t"
"v_mfma_f32_16x16x32_bf16 a[4:7], a[216:219], a[188:191], a[4:7]\n\t"
"ds_write_b64 v243, v[222:223] offset:1024\n\t"
"v_mfma_f32_16x16x32_bf16 a[12:15], a[212:215], a[184:187], a[12:15]\n\t"
"ds_read_b64_tr_b16 v[234:235], v244 offset:33280\n\t"
"v_mfma_f32_16x16x32_bf16 a[12:15], a[248:251], a[188:191], a[12:15]\n\t"
"ds_read_b64_tr_b16 v[236:237], v244 offset:36352\n\t"
"v_mfma_f32_16x16x32_bf16 a[12:15], a[216:219], a[192:195], a[12:15]\n\t"
"ds_write_b64 v243, v[226:227] offset:1536\n\t"
"v_mfma_f32_16x16x32_bf16 a[20:23], a[212:215], a[188:191], a[20:23]\n\t"
"ds_read_b64_tr_b16 v[222:223], v244 offset:40960\n\t"
"v_mfma_f32_16x16x32_bf16 a[20:23], a[248:251], a[192:195], a[20:23]\n\t"
"ds_read_b64_tr_b16 v[224:225], v244 offset:44032\n\t"
"v_mfma_f32_16x16x32_bf16 a[20:23], a[216:219], a[196:199], a[20:23]\n\t"
"ds_read_b64_tr_b16 v[226:227], v244 offset:47104\n\t"
"ds_read_b64_tr_b16 v[228:229], v244 offset:50176\n\t"
"s_waitcnt lgkmcnt(8)\n\t"
"v_mfma_f32_16x16x32_bf16 v[230:233], v[230:233], v[218:221], 0\n\t"
"v_mfma_f32_16x16x32_bf16 a[28:31], a[212:215], a[192:195], a[28:31]\n\t"
"v_mfma_f32_16x16x32_bf16 a[28:31], a[248:251], a[196:199], a[28:31]\n\t"
"v_mfma_f32_16x16x32_bf16 a[28:31], a[216:219], a[200:203], a[28:31]\n\t"
"s_waitcnt lgkmcnt(5)\n\t"
"v_mfma_f32_16x16x32_bf16 v[234:237], v[234:237], v[218:221], 0\n\t"
"v_mfma_f32_16x16x32_bf16 a[36:39], a[212:215], a[196:199], a[36:39]\n\t"
"s_nop 1\n\t"
"v_cvt_pk_bf16_f32 v230, v230, v231\n\t"
"v_cvt_pk_bf16_f32 v231, v232, v233\n\t"
"v_mfma_f32_16x16x32_bf16 a[36:39], a[248:251], a[200:203], a[36:39]\n\t"
"v_mfma_f32_16x16x32_bf16 a[36:39], a[216:219], a[204:207], a[36:39]\n\t"
"s_mov_b32 s20, 491520\n\t"
"buffer_load_dwordx4 a[212:215], v252, s[8:11], s20 offen offset:0 sc1\n\t"
"s_waitcnt lgkmcnt(2)\n\t"
"v_mfma_f32_16x16x32_bf16 v[222:225], v[222:225], v[218:221], 0\n\t"
"buffer_load_dwordx4 a[216:219], v252, s[8:11], s20 offen offset:1024 sc1\n\t"
"s_waitcnt vmcnt(8)\n\t"
"v_mfma_f32_16x16x32_bf16 a[44:47], a[220:223], a[168:171], a[44:47]\n\t"
"v_cvt_pk_bf16_f32 v234, v234, v235\n\t"
"v_cvt_pk_bf16_f32 v235, v236, v237\n\t"
"v_mfma_f32_16x16x32_bf16 a[44:47], a[252:255], a[172:175], a[44:47]\n\t"
"v_mfma_f32_16x16x32_bf16 a[44:47], a[208:211], a[176:179], a[44:47]\n\t"
"ds_write_b64 v243, v[230:231] offset:2048\n\t"
"s_waitcnt lgkmcnt(1)\n\t"
"v_mfma_f32_16x16x32_bf16 v[226:229], v[226:229], v[218:221], 0\n\t"
"ds_read_b64_tr_b16 v[230:231], v244 offset:53248\n\t"
"ds_read_b64_tr_b16 v[232:233], v244 offset:56320\n\t"
"v_mfma_f32_16x16x32_bf16 a[52:55], a[220:223], a[172:175], a[52:55]\n\t"
"v_cvt_pk_bf16_f32 v222, v222, v223\n\t"
"v_cvt_pk_bf16_f32 v223, v224, v225\n\t"
"v_mfma_f32_16x16x32_bf16 a[52:55], a[252:255], a[176:179], a[52:55]\n\t"
"v_mfma_f32_16x16x32_bf16 a[52:55], a[208:211], a[180:183], a[52:55]\n\t"
"buffer_load_dwordx4 a[248:251], v252, s[8:11], s20 offen offset:2048 sc1\n\t"
"v_mfma_f32_16x16x32_bf16 a[60:63], a[220:223], a[176:179], a[60:63]\n\t"
"v_mfma_f32_16x16x32_bf16 a[60:63], a[252:255], a[180:183], a[60:63]\n\t"
"v_cvt_pk_bf16_f32 v226, v226, v227\n\t"
"v_cvt_pk_bf16_f32 v227, v228, v229\n\t"
"v_mfma_f32_16x16x32_bf16 a[60:63], a[208:211], a[184:187], a[60:63]\n\t"
"ds_write_b64 v243, v[234:235] offset:2560\n\t"
"v_mfma_f32_16x16x32_bf16 a[68:71], a[220:223], a[180:183], a[68:71]\n\t"
"v_mfma_f32_16x16x32_bf16 a[68:71], a[252:255], a[184:187], a[68:71]\n\t"
"v_mfma_f32_16x16x32_bf16 a[68:71], a[208:211], a[188:191], a[68:71]\n\t"
"ds_write_b64 v243, v[222:223] offset:3072\n\t"
"s_waitcnt lgkmcnt(2)\n\t"
"v_mfma_f32_16x16x32_bf16 v[230:233], v[230:233], v[218:221], 0\n\t"
"ds_write_b64 v243, v[226:227] offset:3584\n\t"
"v_mfma_f32_16x16x32_bf16 a[76:79], a[220:223], a[184:187], a[76:79]\n\t"
"v_mfma_f32_16x16x32_bf16 a[76:79], a[252:255], a[188:191], a[76:79]\n\t"
"v_mfma_f32_16x16x32_bf16 a[76:79], a[208:211], a[192:195], a[76:79]\n\t"
"v_mfma_f32_16x16x32_bf16 a[84:87], a[220:223], a[188:191], a[84:87]\n\t"
"v_mfma_f32_16x16x32_bf16 a[84:87], a[252:255], a[192:195], a[84:87]\n\t"
"s_nop 1\n\t"
"v_cvt_pk_bf16_f32 v230, v230, v231\n\t"
"v_cvt_pk_bf16_f32 v231, v232, v233\n\t"
"v_mfma_f32_16x16x32_bf16 a[84:87], a[208:211], a[196:199], a[84:87]\n\t"
"v_mfma_f32_16x16x32_bf16 a[92:95], a[220:223], a[192:195], a[92:95]\n\t"
"v_mfma_f32_16x16x32_bf16 a[92:95], a[252:255], a[196:199], a[92:95]\n\t"
"v_mfma_f32_16x16x32_bf16 a[92:95], a[208:211], a[200:203], a[92:95]\n\t"
"ds_write_b64 v243, v[230:231] offset:4096\n\t"
"v_mfma_f32_16x16x32_bf16 a[100:103], a[220:223], a[196:199], a[100:103]\n\t"
"v_mfma_f32_16x16x32_bf16 a[100:103], a[252:255], a[200:203], a[100:103]\n\t"
"v_mfma_f32_16x16x32_bf16 a[100:103], a[208:211], a[204:207], a[100:103]\n\t"
"s_mov_b32 s20, 503808\n\t"
"buffer_load_dwordx4 a[220:223], v252, s[8:11], s20 offen offset:0 sc1\n\t"
"s_waitcnt vmcnt(7)\n\t"
"v_mfma_f32_16x16x32_bf16 a[108:111], a[224:227], a[168:171], a[108:111]\n\t"
"v_mfma_f32_16x16x32_bf16 a[108:111], a[228:231], a[172:175], a[108:111]\n\t"
"v_mfma_f32_16x16x32_bf16 a[108:111], a[232:235], a[176:179], a[108:111]\n\t"
"buffer_load_dwordx4 a[208:211], v252, s[8:11], s20 offen offset:1024 sc1\n\t"
"s_waitcnt lgkmcnt(0)\n\t"
"s_barrier\n\t"
"v_mfma_f32_16x16x32_bf16 a[116:119], a[224:227], a[172:175], a[116:119]\n\t"
"ds_read_b128 a[168:171], v242 offset:0\n\t"
"v_mfma_f32_16x16x32_bf16 a[116:119], a[228:231], a[176:179], a[116:119]\n\t"
"ds_read_b128 a[172:175], v242 offset:512\n\t"
"v_mfma_f32_16x16x32_bf16 a[116:119], a[232:235], a[180:183], a[116:119]\n\t"
"s_add_u32 m0, s25, 256\n\t"
"s_nop 0\n\t"
"buffer_load_dwordx4 v246, s[12:15], 0 offen offset:256 nt lds\n\t"
"s_add_u32 m0, s26, 256\n\t"
"s_mov_b64 exec, s[36:37]\n\t"
"buffer_load_dwordx4 v246, s[12:15], s27 offen offset:256 nt lds\n\t"
"s_mov_b64 exec, -1\n\t"
"v_mfma_f32_16x16x32_bf16 a[124:127], a[224:227], a[176:179], a[124:127]\n\t"
"ds_read_b128 a[176:179], v242 offset:1024\n\t"
"v_mfma_f32_16x16x32_bf16 a[124:127], a[228:231], a[180:183], a[124:127]\n\t"
"v_mfma_f32_16x16x32_bf16 a[124:127], a[232:235], a[184:187], a[124:127]\n\t"
"s_add_u32 m0, s24, 256\n\t"
"s_add_u32 s28, s21, 0\n\t"
"buffer_load_dwordx4 v245, s[12:15], s28 offen offset:256 nt lds\n\t"
"v_mfma_f32_16x16x32_bf16 a[132:135], a[224:227], a[180:183], a[132:135]\n\t"
"ds_read_b128 a[180:183], v242 offset:1536\n\t"
"v_mfma_f32_16x16x32_bf16 a[132:135], a[228:231], a[184:187], a[132:135]\n\t"
"v_mfma_f32_16x16x32_bf16 a[132:135], a[232:235], a[188:191], a[132:135]\n\t"
"s_add_u32 m0, s24, 6400\n\t"
"s_add_u32 s28, s21, 196608\n\t"
"buffer_load_dwordx4 v245, s[12:15], s28 offen offset:256 nt lds\n\t"
"s_add_u32 m0, s24, 12544\n\t"
"s_add_u32 s28, s21, 393216\n\t"
"buffer_load_dwordx4 v245, s[12:15], s28 offen offset:256 nt lds\n\t"
"v_mfma_f32_16x16x32_bf16 a[140:143], a[224:227], a[184:187], a[140:143]\n\t"
"ds_read_b128 a[184:187], v242 offset:2048\n\t"
"v_mfma_f32_16x16x32_bf16 a[140:143], a[228:231], a[188:191], a[140:143]\n\t"
"v_mfma_f32_16x16x32_bf16 a[140:143], a[232:235], a[192:195], a[140:143]\n\t"
"s_add_u32 m0, s24, 20224\n\t"
"s_add_u32 s28, s21, 589824\n\t"
"buffer_load_dwordx4 v245, s[12:15], s28 offen offset:256 nt lds\n\t"
"v_mfma_f32_16x16x32_bf16 a[148:151], a[224:227], a[188:191], a[148:151]\n\t"
"ds_read_b128 a[188:191], v242 offset:2560\n\t"
"v_mfma_f32_16x16x32_bf16 a[148:151], a[228:231], a[192:195], a[148:151]\n\t"
"v_mfma_f32_16x16x32_bf16 a[148:151], a[232:235], a[196:199], a[148:151]\n\t"
"s_add_u32 m0, s24, 26368\n\t"
"s_add_u32 s28, s21, 786432\n\t"
"buffer_load_dwordx4 v245, s[12:15], s28 offen offset:256 nt lds\n\t"
"v_mfma_f32_16x16x32_bf16 a[156:159], a[224:227], a[192:195], a[156:159]\n\t"
"ds_read_b128 a[192:195], v242 offset:3072\n\t"
"v_mfma_f32_16x16x32_bf16 a[156:159], a[228:231], a[196:199], a[156:159]\n\t"
"v_mfma_f32_16x16x32_bf16 a[156:159], a[232:235], a[200:203], a[156:159]\n\t"
"v_mfma_f32_16x16x32_bf16 a[164:167], a[224:227], a[196:199], a[164:167]\n\t"
"ds_read_b128 a[196:199], v242 offset:3584\n\t"
"v_mfma_f32_16x16x32_bf16 a[164:167], a[228:231], a[200:203], a[164:167]\n\t"
"ds_read_b128 a[200:203], v242 offset:4096\n\t"
"v_mfma_f32_16x16x32_bf16 a[164:167], a[232:235], a[204:207], a[164:167]\n\t"
"s_waitcnt vmcnt(12) lgkmcnt(6)\n\t"
"v_mfma_f32_16x16x32_bf16 v[0:3], a[236:239], a[168:171], v[0:3]\n\t"
"ds_read_b128 a[204:207], v242 offset:4608\n\t"
"v_mfma_f32_16x16x32_bf16 v[0:3], a[240:243], a[172:175], v[0:3]\n\t"
"v_mfma_f32_16x16x32_bf16 v[0:3], a[244:247], a[176:179], v[0:3]\n\t"
"s_waitcnt lgkmcnt(6)\n\t"
"v_mfma_f32_16x16x32_bf16 v[4:7], a[236:239], a[172:175], v[4:7]\n\t"
"v_mfma_f32_16x16x32_bf16 v[4:7], a[240:243], a[176:179], v[4:7]\n\t"
"v_mfma_f32_16x16x32_bf16 v[4:7], a[244:247], a[180:183], v[4:7]\n\t"
"s_waitcnt lgkmcnt(5)\n\t"
"v_mfma_f32_16x16x32_bf16 v[8:11], a[236:239], a[176:179], v[8:11]\n\t"
"v_mfma_f32_16x16x32_bf16 v[8:11], a[240:243], a[180:183], v[8:11]\n\t"
"v_mfma_f32_16x16x32_bf16 v[8:11], a[244:247], a[184:187], v[8:11]\n\t"
"s_waitcnt lgkmcnt(4)\n\t"
"v_mfma_f32_16x16x32_bf16 v[12:15], a[236:239], a[180:183], v[12:15]\n\t"
"v_mfma_f32_16x16x32_bf16 v[12:15], a[240:243], a[184:187], v[12:15]\n\t"
"v_mfma_f32_16x16x32_bf16 v[12:15], a[244:247], a[188:191], v[12:15]\n\t"
"buffer_load_dwordx4 a[224:227], v252, s[8:11], s20 offen offset:2048 sc1\n\t"
"s_waitcnt lgkmcnt(3)\n\t"
"v_mfma_f32_16x16x32_bf16 v[16:19], a[236:239], a[184:187], v[16:19]\n\t"
"v_mfma_f32_16x16x32_bf16 v[16:19], a[240:243], a[188:191], v[16:19]\n\t"
"v_mfma_f32_16x16x32_bf16 v[16:19], a[244:247], a[192:195], v[16:19]\n\t"
"s_waitcnt lgkmcnt(2)\n\t"
"v_mfma_f32_16x16x32_bf16 v[20:23], a[236:239], a[188:191], v[20:23]\n\t"
"v_mfma_f32_16x16x32_bf16 v[20:23], a[240:243], a[192:195], v[20:23]\n\t"
"v_mfma_f32_16x16x32_bf16 v[20:23], a[244:247], a[196:199], v[20:23]\n\t"
"s_waitcnt lgkmcnt(1)\n\t"
"v_mfma_f32_16x16x32_bf16 v[24:27], a[236:239], a[192:195], v[24:27]\n\t"
"v_mfma_f32_16x16x32_bf16 v[24:27], a[240:243], a[196:199], v[24:27]\n\t"
"v_mfma_f32_16x16x32_bf16 v[24:27], a[244:247], a[200:203], v[24:27]\n\t"
"s_waitcnt lgkmcnt(0)\n\t"
"v_mfma_f32_16x16x32_bf16 v[28:31], a[236:239], a[196:199], v[28:31]\n\t"
"v_mfma_f32_16x16x32_bf16 v[28:31], a[240:243], a[200:203], v[28:31]\n\t"
"v_mfma_f32_16x16x32_bf16 v[28:31], a[244:247], a[204:207], v[28:31]\n\t"
"s_waitcnt vmcnt(10)\n\t"
"v_mfma_f32_16x16x32_bf16 v[32:35], a[212:215], a[168:171], v[32:35]\n\t"
"v_mfma_f32_16x16x32_bf16 v[32:35], a[216:219], a[172:175], v[32:35]\n\t"
"v_mfma_f32_16x16x32_bf16 v[32:35], a[248:251], a[176:179], v[32:35]\n\t"
"ds_read_b64_tr_b16 v[234:235], v244 offset:1536\n\t"
"ds_read_b64_tr_b16 v[236:237], v244 offset:4608\n\t"
"v_mfma_f32_16x16x32_bf16 v[36:39], a[212:215], a[172:175], v[36:39]\n\t"
"v_mfma_f32_16x16x32_bf16 v[36:39], a[216:219], a[176:179], v[36:39]\n\t"
"v_mfma_f32_16x16x32_bf16 v[36:39], a[248:251], a[180:183], v[36:39]\n\t"
"v_mfma_f32_16x16x32_bf16 v[40:43], a[212:215], a[176:179], v[40:43]\n\t"
"v_mfma_f32_16x16x32_bf16 v[40:43], a[216:219], a[180:183], v[40:43]\n\t"
"v_mfma_f32_16x16x32_bf16 v[40:43], a[248:251], a[184:187], v[40:43]\n\t"
"v_mfma_f32_16x16x32_bf16 v[44:47], a[212:215], a[180:183], v[44:47]\n\t"
"ds_read_b64_tr_b16 v[222:223], v244 offset:7680\n\t"
"v_mfma_f32_16x16x32_bf16 v[44:47], a[216:219], a[184:187], v[44:47]\n\t"
"ds_read_b64_tr_b16 v[224:225], v244 offset:10752\n\t"
"v_mfma_f32_16x16x32_bf16 v[44:47], a[248:251], a[188:191], v[44:47]\n\t"
"v_mfma_f32_16x16x32_bf16 v[48:51], a[212:215], a[184:187], v[48:51]\n\t"
"v_mfma_f32_16x16x32_bf16 v[48:51], a[216:219], a[188:191], v[48:51]\n\t"
"v_mfma_f32_16x16x32_bf16 v[48:51], a[248:251], a[192:195], v[48:51]\n\t"
"s_mov_b32 s20, 516096\n\t"
"buffer_load_dwordx4 a[228:231], v252, s[8:11], s20 offen offset:0 sc1\n\t"
"v_mfma_f32_16x16x32_bf16 v[52:55], a[212:215], a[188:191], v[52:55]\n\t"
"v_mfma_f32_16x16x32_bf16 v[52:55], a[216:219], a[192:195], v[52:55]\n\t"
"v_mfma_f32_16x16x32_bf16 v[52:55], a[248:251], a[196:199], v[52:55]\n\t"
"buffer_load_dwordx4 a[232:235], v252, s[8:11], s20 offen offset:1024 sc1\n\t"
"s_waitcnt lgkmcnt(2)\n\t"
"v_mfma_f32_16x16x32_bf16 v[234:237], v[234:237], v[218:221], 0\n\t"
"buffer_load_dwordx4 a[236:239], v252, s[8:11], s20 offen offset:2048 sc1\n\t"
"v_mfma_f32_16x16x32_bf16 v[56:59], a[212:215], a[192:195], v[56:59]\n\t"
"ds_read_b64_tr_b16 v[226:227], v244 offset:13824\n\t"
"v_mfma_f32_16x16x32_bf16 v[56:59], a[216:219], a[196:199], v[56:59]\n\t"
"v_mfma_f32_16x16x32_bf16 v[56:59], a[248:251], a[200:203], v[56:59]\n\t"
"ds_read_b64_tr_b16 v[228:229], v244 offset:16896\n\t"
"v_mfma_f32_16x16x32_bf16 v[60:63], a[212:215], a[196:199], v[60:63]\n\t"
"v_mfma_f32_16x16x32_bf16 v[60:63], a[216:219], a[200:203], v[60:63]\n\t"
"v_cvt_pk_bf16_f32 v234, v234, v235\n\t"
"v_cvt_pk_bf16_f32 v235, v236, v237\n\t"
"v_mfma_f32_16x16x32_bf16 v[60:63], a[248:251], a[204:207], v[60:63]\n\t"
"s_waitcnt lgkmcnt(2)\n\t"
"v_mfma_f32_16x16x32_bf16 v[222:225], v[222:225], v[218:221], 0\n\t"
"s_mov_b32 s20, 528384\n\t"
"buffer_load_dwordx4 a[212:215], v252, s[8:11], s20 offen offset:0 sc1\n\t"
"s_waitcnt vmcnt(4)\n\t"
"v_mfma_f32_16x16x32_bf16 v[64:67], a[220:223], a[168:171], v[64:67]\n\t"
"ds_read_b64_tr_b16 v[230:231], v244 offset:21504\n\t"
"v_mfma_f32_16x16x32_bf16 v[64:67], a[208:211], a[172:175], v[64:67]\n\t"
"ds_read_b64_tr_b16 v[232:233], v244 offset:24576\n\t"
"v_mfma_f32_16x16x32_bf16 v[64:67], a[224:227], a[176:179], v[64:67]\n\t"
"buffer_load_dwordx4 a[216:219], v252, s[8:11], s20 offen offset:1024 sc1\n\t"
"v_mfma_f32_16x16x32_bf16 v[68:71], a[220:223], a[172:175], v[68:71]\n\t"
"ds_read_b128 a[168:171], v242 offset:9216\n\t"
"v_mfma_f32_16x16x32_bf16 v[68:71], a[208:211], a[176:179], v[68:71]\n\t"
"v_cvt_pk_bf16_f32 v222, v222, v223\n\t"
"v_cvt_pk_bf16_f32 v223, v224, v225\n\t"
"v_mfma_f32_16x16x32_bf16 v[68:71], a[224:227], a[180:183], v[68:71]\n\t"
"ds_read_b128 a[172:175], v242 offset:9728\n\t"
"v_mfma_f32_16x16x32_bf16 v[72:75], a[220:223], a[176:179], v[72:75]\n\t"
"ds_read_b128 a[176:179], v242 offset:10240\n\t"
"v_mfma_f32_16x16x32_bf16 v[72:75], a[208:211], a[180:183], v[72:75]\n\t"
"v_mfma_f32_16x16x32_bf16 v[72:75], a[224:227], a[184:187], v[72:75]\n\t"
"buffer_load_dwordx4 a[240:243], v252, s[8:11], s20 offen offset:2048 sc1\n\t"
"s_waitcnt lgkmcnt(5)\n\t"
"v_mfma_f32_16x16x32_bf16 v[226:229], v[226:229], v[218:221], 0\n\t"
"s_mov_b32 s20, 540672\n\t"
"buffer_load_dwordx4 a[244:247], v252, s[8:11], s20 offen offset:0 sc1\n\t"
"v_mfma_f32_16x16x32_bf16 v[76:79], a[220:223], a[180:183], v[76:79]\n\t"
"ds_read_b128 a[180:183], v242 offset:10752\n\t"
"v_mfma_f32_16x16x32_bf16 v[76:79], a[208:211], a[184:187], v[76:79]\n\t"
"v_mfma_f32_16x16x32_bf16 v[76:79], a[224:227], a[188:191], v[76:79]\n\t"
"buffer_load_dwordx4 a[248:251], v252, s[8:11], s20 offen offset:1024 sc1\n\t"
"v_mfma_f32_16x16x32_bf16 v[80:83], a[220:223], a[184:187], v[80:83]\n\t"
"ds_read_b128 a[184:187], v242 offset:11264\n\t"
"v_mfma_f32_16x16x32_bf16 v[80:83], a[208:211], a[188:191], v[80:83]\n\t"
"v_cvt_pk_bf16_f32 v226, v226, v227\n\t"
"v_cvt_pk_bf16_f32 v227, v228, v229\n\t"
"v_mfma_f32_16x16x32_bf16 v[80:83], a[224:227], a[192:195], v[80:83]\n\t"
"buffer_load_dwordx4 a[252:255], v252, s[8:11], s20 offen offset:2048 sc1\n\t"
"s_waitcnt lgkmcnt(5)\n\t"
"v_mfma_f32_16x16x32_bf16 v[230:233], v[230:233], v[218:221], 0\n\t"
"v_mfma_f32_16x16x32_bf16 v[84:87], a[220:223], a[188:191], v[84:87]\n\t"
"ds_read_b128 a[188:191], v242 offset:11776\n\t"
"v_mfma_f32_16x16x32_bf16 v[84:87], a[208:211], a[192:195], v[84:87]\n\t"
"v_mfma_f32_16x16x32_bf16 v[84:87], a[224:227], a[196:199], v[84:87]\n\t"
"v_mfma_f32_16x16x32_bf16 v[88:91], a[220:223], a[192:195], v[88:91]\n\t"
"ds_read_b128 a[192:195], v242 offset:12288\n\t"
"v_mfma_f32_16x16x32_bf16 v[88:91], a[208:211], a[196:199], v[88:91]\n\t"
"s_nop 0\n\t"
"v_cvt_pk_bf16_f32 v230, v230, v231\n\t"
"v_mfma_f32_16x16x32_bf16 v[88:91], a[224:227], a[200:203], v[88:91]\n\t"
"v_cvt_pk_bf16_f32 v231, v232, v233\n\t"
"v_mfma_f32_16x16x32_bf16 v[92:95], a[220:223], a[196:199], v[92:95]\n\t"
"ds_read_b128 a[196:199], v242 offset:12800\n\t"
"v_mfma_f32_16x16x32_bf16 v[92:95], a[208:211], a[200:203], v[92:95]\n\t"
"ds_read_b128 a[200:203], v242 offset:13312\n\t"
"v_mfma_f32_16x16x32_bf16 v[92:95], a[224:227], a[204:207], v[92:95]\n\t"
"s_mov_b32 s20, 700416\n\t"
"buffer_load_dwordx4 a[220:223], v252, s[8:11], s20 offen offset:0 sc1\n\t"
"s_waitcnt vmcnt(7) lgkmcnt(6)\n\t"
"v_mfma_f32_16x16x32_bf16 v[98:101], a[228:231], a[168:171], v[98:101]\n\t"
"ds_read_b128 a[204:207], v242 offset:13824\n\t"
"v_mfma_f32_16x16x32_bf16 v[98:101], a[232:235], a[172:175], v[98:101]\n\t"
"v_mfma_f32_16x16x32_bf16 v[98:101], a[236:239], a[176:179], v[98:101]\n\t"
"buffer_load_dwordx4 a[208:211], v252, s[8:11], s20 offen offset:1024 sc1\n\t"
"s_waitcnt lgkmcnt(6)\n\t"
"v_mfma_f32_16x16x32_bf16 v[102:105], a[228:231], a[172:175], v[102:105]\n\t"
"v_mfma_f32_16x16x32_bf16 v[102:105], a[232:235], a[176:179], v[102:105]\n\t"
"v_mfma_f32_16x16x32_bf16 v[102:105], a[236:239], a[180:183], v[102:105]\n\t"
"buffer_load_dwordx4 a[224:227], v252, s[8:11], s20 offen offset:2048 sc1\n\t"
"s_waitcnt lgkmcnt(0)\n\t"
"s_barrier\n\t"
"v_mfma_f32_16x16x32_bf16 v[106:109], a[228:231], a[176:179], v[106:109]\n\t"
"v_mfma_f32_16x16x32_bf16 v[106:109], a[232:235], a[180:183], v[106:109]\n\t"
"v_mfma_f32_16x16x32_bf16 v[106:109], a[236:239], a[184:187], v[106:109]\n\t"
"ds_write_b64 v243, v[234:235] offset:0\n\t"
"ds_write_b64 v243, v[222:223] offset:512\n\t"
"v_mfma_f32_16x16x32_bf16 v[110:113], a[228:231], a[180:183], v[110:113]\n\t"
"ds_read_b64_tr_b16 v[234:235], v244 offset:27648\n\t"
"v_mfma_f32_16x16x32_bf16 v[110:113], a[232:235], a[184:187], v[110:113]\n\t"
"ds_read_b64_tr_b16 v[236:237], v244 offset:30720\n\t"
"v_mfma_f32_16x16x32_bf16 v[110:113], a[236:239], a[188:191], v[110:113]\n\t"
"ds_write_b64 v243, v[226:227] offset:1024\n\t"
"v_mfma_f32_16x16x32_bf16 v[114:117], a[228:231], a[184:187], v[114:117]\n\t"
"ds_read_b64_tr_b16 v[222:223], v244 offset:33792\n\t"
"v_mfma_f32_16x16x32_bf16 v[114:117], a[232:235], a[188:191], v[114:117]\n\t"
"ds_read_b64_tr_b16 v[224:225], v244 offset:36864\n\t"
"v_mfma_f32_16x16x32_bf16 v[114:117], a[236:239], a[192:195], v[114:117]\n\t"
"ds_write_b64 v243, v[230:231] offset:1536\n\t"
"v_mfma_f32_16x16x32_bf16 v[118:121], a[228:231], a[188:191], v[118:121]\n\t"
"ds_read_b64_tr_b16 v[226:227], v244 offset:41472\n\t"
"v_mfma_f32_16x16x32_bf16 v[118:121], a[232:235], a[192:195], v[118:121]\n\t"
"ds_read_b64_tr_b16 v[228:229], v244 offset:44544\n\t"
"v_mfma_f32_16x16x32_bf16 v[118:121], a[236:239], a[196:199], v[118:121]\n\t"
"ds_read_b64_tr_b16 v[230:231], v244 offset:47616\n\t"
"ds_read_b64_tr_b16 v[232:233], v244 offset:50688\n\t"
"s_waitcnt lgkmcnt(8)\n\t"
"v_mfma_f32_16x16x32_bf16 v[234:237], v[234:237], v[218:221], 0\n\t"
"s_add_u32 m0, s24, 32512\n\t"
"s_add_u32 s28, s21, 983040\n\t"
"buffer_load_dwordx4 v245, s[12:15], s28 offen offset:256 nt lds\n\t"
"v_mfma_f32_16x16x32_bf16 v[122:125], a[228:231], a[192:195], v[122:125]\n\t"
"v_mfma_f32_16x16x32_bf16 v[122:125], a[232:235], a[196:199], v[122:125]\n\t"
"v_mfma_f32_16x16x32_bf16 v[122:125], a[236:239], a[200:203], v[122:125]\n\t"
"s_add_u32 m0, s24, 40192\n\t"
"s_add_u32 s28, s21, 1179648\n\t"
"buffer_load_dwordx4 v245, s[12:15], s28 offen offset:256 nt lds\n\t"
"s_waitcnt lgkmcnt(5)\n\t"
"v_mfma_f32_16x16x32_bf16 v[222:225], v[222:225], v[218:221], 0\n\t"
"s_add_u32 m0, s24, 46336\n\t"
"s_add_u32 s28, s21, 1376256\n\t"
"buffer_load_dwordx4 v245, s[12:15], s28 offen offset:256 nt lds\n\t"
"v_mfma_f32_16x16x32_bf16 v[126:129], a[228:231], a[196:199], v[126:129]\n\t"
"v_cvt_pk_bf16_f32 v234, v234, v235\n\t"
"v_cvt_pk_bf16_f32 v235, v236, v237\n\t"
"v_mfma_f32_16x16x32_bf16 v[126:129], a[232:235], a[200:203], v[126:129]\n\t"
"v_mfma_f32_16x16x32_bf16 v[126:129], a[236:239], a[204:207], v[126:129]\n\t"
"s_add_u32 m0, s24, 52480\n\t"
"s_add_u32 s28, s21, 1572864\n\t"
"buffer_load_dwordx4 v245, s[12:15], s28 offen offset:256 nt lds\n\t"
"s_waitcnt lgkmcnt(2)\n\t"
"v_mfma_f32_16x16x32_bf16 v[226:229], v[226:229], v[218:221], 0\n\t"
"s_add_u32 m0, s24, 60160\n\t"
"s_add_u32 s28, s21, 1769472\n\t"
"buffer_load_dwordx4 v245, s[12:15], s28 offen offset:256 nt lds\n\t"
"s_waitcnt vmcnt(11)\n\t"
"v_mfma_f32_16x16x32_bf16 v[130:133], a[212:215], a[168:171], v[130:133]\n\t"
"v_cvt_pk_bf16_f32 v222, v222, v223\n\t"
"v_cvt_pk_bf16_f32 v223, v224, v225\n\t"
"v_mfma_f32_16x16x32_bf16 v[130:133], a[216:219], a[172:175], v[130:133]\n\t"
"v_mfma_f32_16x16x32_bf16 v[130:133], a[240:243], a[176:179], v[130:133]\n\t"
"s_add_u32 m0, s24, 66304\n\t"
"s_add_u32 s28, s21, 1966080\n\t"
"buffer_load_dwordx4 v245, s[12:15], s28 offen offset:256 nt lds\n\t"
"s_waitcnt lgkmcnt(0)\n\t"
"v_mfma_f32_16x16x32_bf16 v[230:233], v[230:233], v[218:221], 0\n\t"
"ds_write_b64 v243, v[234:235] offset:2048\n\t"
"s_add_u32 m0, s24, 72448\n\t"
"s_add_u32 s28, s21, 2162688\n\t"
"buffer_load_dwordx4 v245, s[12:15], s28 offen offset:256 nt lds\n\t"
"v_mfma_f32_16x16x32_bf16 v[134:137], a[212:215], a[172:175], v[134:137]\n\t"
"ds_read_b64_tr_b16 v[234:235], v244 offset:53760\n\t"
"v_cvt_pk_bf16_f32 v226, v226, v227\n\t"
"v_mfma_f32_16x16x32_bf16 v[134:137], a[216:219], a[176:179], v[134:137]\n\t"
"ds_read_b64_tr_b16 v[236:237], v244 offset:56832\n\t"
"v_cvt_pk_bf16_f32 v227, v228, v229\n\t"
"v_mfma_f32_16x16x32_bf16 v[134:137], a[240:243], a[180:183], v[134:137]\n\t"
"ds_write_b64 v243, v[222:223] offset:2560\n\t"
"v_mfma_f32_16x16x32_bf16 v[138:141], a[212:215], a[176:179], v[138:141]\n\t"
"v_mfma_f32_16x16x32_bf16 v[138:141], a[216:219], a[180:183], v[138:141]\n\t"
"v_cvt_pk_bf16_f32 v230, v230, v231\n\t"
"v_cvt_pk_bf16_f32 v231, v232, v233\n\t"
"v_mfma_f32_16x16x32_bf16 v[138:141], a[240:243], a[184:187], v[138:141]\n\t"
"ds_write_b64 v243, v[226:227] offset:3072\n\t"
"v_mfma_f32_16x16x32_bf16 v[142:145], a[212:215], a[180:183], v[142:145]\n\t"
"v_mfma_f32_16x16x32_bf16 v[142:145], a[216:219], a[184:187], v[142:145]\n\t"
"v_mfma_f32_16x16x32_bf16 v[142:145], a[240:243], a[188:191], v[142:145]\n\t"
"ds_write_b64 v243, v[230:231] offset:3584\n\t"
"s_waitcnt lgkmcnt(3)\n\t"
"v_mfma_f32_16x16x32_bf16 v[234:237], v[234:237], v[218:221], 0\n\t"
"v_mfma_f32_16x16x32_bf16 v[146:149], a[212:215], a[184:187], v[146:149]\n\t"
"v_mfma_f32_16x16x32_bf16 v[146:149], a[216:219], a[188:191], v[146:149]\n\t"
"v_mfma_f32_16x16x32_bf16 v[146:149], a[240:243], a[192:195], v[146:149]\n\t"
"v_mfma_f32_16x16x32_bf16 v[150:153], a[212:215], a[188:191], v[150:153]\n\t"
"v_mfma_f32_16x16x32_bf16 v[150:153], a[216:219], a[192:195], v[150:153]\n\t"
"s_nop 2\n\t"
"v_cvt_pk_bf16_f32 v234, v234, v235\n\t"
"v_cvt_pk_bf16_f32 v235, v236, v237\n\t"
"v_mfma_f32_16x16x32_bf16 v[150:153], a[240:243], a[196:199], v[150:153]\n\t"
"v_mfma_f32_16x16x32_bf16 v[154:157], a[212:215], a[192:195], v[154:157]\n\t"
"v_mfma_f32_16x16x32_bf16 v[154:157], a[216:219], a[196:199], v[154:157]\n\t"
"v_mfma_f32_16x16x32_bf16 v[154:157], a[240:243], a[200:203], v[154:157]\n\t"
"ds_write_b64 v243, v[234:235] offset:4096\n\t"
"v_mfma_f32_16x16x32_bf16 v[158:161], a[212:215], a[196:199], v[158:161]\n\t"
"v_mfma_f32_16x16x32_bf16 v[158:161], a[216:219], a[200:203], v[158:161]\n\t"
"v_mfma_f32_16x16x32_bf16 v[158:161], a[240:243], a[204:207], v[158:161]\n\t"
"s_waitcnt vmcnt(10)\n\t"
"v_mfma_f32_16x16x32_bf16 v[162:165], a[244:247], a[168:171], v[162:165]\n\t"
"v_mfma_f32_16x16x32_bf16 v[162:165], a[248:251], a[172:175], v[162:165]\n\t"
"v_mfma_f32_16x16x32_bf16 v[162:165], a[252:255], a[176:179], v[162:165]\n\t"
"s_waitcnt lgkmcnt(0)\n\t"
"s_barrier\n\t"
"v_mfma_f32_16x16x32_bf16 v[166:169], a[244:247], a[172:175], v[166:169]\n\t"
"ds_read_b128 a[168:171], v242 offset:0\n\t"
"v_mfma_f32_16x16x32_bf16 v[166:169], a[248:251], a[176:179], v[166:169]\n\t"
"ds_read_b128 a[172:175], v242 offset:512\n\t"
"v_mfma_f32_16x16x32_bf16 v[166:169], a[252:255], a[180:183], v[166:169]\n\t"
"v_mfma_f32_16x16x32_bf16 v[170:173], a[244:247], a[176:179], v[170:173]\n\t"
"ds_read_b128 a[176:179], v242 offset:1024\n\t"
"v_mfma_f32_16x16x32_bf16 v[170:173], a[248:251], a[180:183], v[170:173]\n\t"
"v_mfma_f32_16x16x32_bf16 v[170:173], a[252:255], a[184:187], v[170:173]\n\t"
"s_mov_b32 s20, 712704\n\t"
"buffer_load_dwordx4 a[212:215], v252, s[8:11], s20 offen offset:0 sc1\n\t"
"v_mfma_f32_16x16x32_bf16 v[174:177], a[244:247], a[180:183], v[174:177]\n\t"
"ds_read_b128 a[180:183], v242 offset:1536\n\t"
"v_mfma_f32_16x16x32_bf16 v[174:177], a[248:251], a[184:187], v[174:177]\n\t"
"v_mfma_f32_16x16x32_bf16 v[174:177], a[252:255], a[188:191], v[174:177]\n\t"
"buffer_load_dwordx4 a[216:219], v252, s[8:11], s20 offen offset:1024 sc1\n\t"
"v_mfma_f32_16x16x32_bf16 v[178:181], a[244:247], a[184:187], v[178:181]\n\t"
"ds_read_b128 a[184:187], v242 offset:2048\n\t"
"v_mfma_f32_16x16x32_bf16 v[178:181], a[248:251], a[188:191], v[178:181]\n\t"
"v_mfma_f32_16x16x32_bf16 v[178:181], a[252:255], a[192:195], v[178:181]\n\t"
"buffer_load_dwordx4 a[228:231], v252, s[8:11], s20 offen offset:2048 sc1\n\t"
"v_mfma_f32_16x16x32_bf16 v[182:185], a[244:247], a[188:191], v[182:185]\n\t"
"ds_read_b128 a[188:191], v242 offset:2560\n\t"
"v_mfma_f32_16x16x32_bf16 v[182:185], a[248:251], a[192:195], v[182:185]\n\t"
"v_mfma_f32_16x16x32_bf16 v[182:185], a[252:255], a[196:199], v[182:185]\n\t"
"s_mov_b32 s20, 724992\n\t"
"buffer_load_dwordx4 a[232:235], v252, s[8:11], s20 offen offset:0 sc1\n\t"
"v_mfma_f32_16x16x32_bf16 v[186:189], a[244:247], a[192:195], v[186:189]\n\t"
"v_mfma_f32_16x16x32_bf16 v[186:189], a[248:251], a[196:199], v[186:189]\n\t"
"ds_read_b128 a[192:195], v242 offset:3072\n\t"
"v_mfma_f32_16x16x32_bf16 v[186:189], a[252:255], a[200:203], v[186:189]\n\t"
"v_mfma_f32_16x16x32_bf16 v[190:193], a[244:247], a[196:199], v[190:193]\n\t"
"ds_read_b128 a[196:199], v242 offset:3584\n\t"
"v_mfma_f32_16x16x32_bf16 v[190:193], a[248:251], a[200:203], v[190:193]\n\t"
"ds_read_b128 a[200:203], v242 offset:4096\n\t"
"v_mfma_f32_16x16x32_bf16 v[190:193], a[252:255], a[204:207], v[190:193]\n\t"
"buffer_load_dwordx4 a[236:239], v252, s[8:11], s20 offen offset:1024 sc1\n\t"
"s_waitcnt vmcnt(12) lgkmcnt(6)\n\t"
"v_mfma_f32_16x16x32_bf16 v[0:3], a[220:223], a[168:171], v[0:3]\n\t"
"ds_read_b128 a[204:207], v242 offset:4608\n\t"
"v_mfma_f32_16x16x32_bf16 v[0:3], a[208:211], a[172:175], v[0:3]\n\t"
"v_mfma_f32_16x16x32_bf16 v[0:3], a[224:227], a[176:179], v[0:3]\n\t"
"buffer_load_dwordx4 a[240:243], v252, s[8:11], s20 offen offset:2048 sc1\n\t"
"s_waitcnt lgkmcnt(6)\n\t"
"v_mfma_f32_16x16x32_bf16 v[4:7], a[220:223], a[172:175], v[4:7]\n\t"
"v_mfma_f32_16x16x32_bf16 v[4:7], a[208:211], a[176:179], v[4:7]\n\t"
"v_mfma_f32_16x16x32_bf16 v[4:7], a[224:227], a[180:183], v[4:7]\n\t"
"s_mov_b32 s20, 737280\n\t"
"buffer_load_dwordx4 a[244:247], v252, s[8:11], s20 offen offset:0 sc1\n\t"
"s_waitcnt lgkmcnt(5)\n\t"
"v_mfma_f32_16x16x32_bf16 v[8:11], a[220:223], a[176:179], v[8:11]\n\t"
"v_mfma_f32_16x16x32_bf16 v[8:11], a[208:211], a[180:183], v[8:11]\n\t"
"v_mfma_f32_16x16x32_bf16 v[8:11], a[224:227], a[184:187], v[8:11]\n\t"
"s_waitcnt lgkmcnt(4)\n\t"
"v_mfma_f32_16x16x32_bf16 v[12:15], a[220:223], a[180:183], v[12:15]\n\t"
"v_mfma_f32_16x16x32_bf16 v[12:15], a[208:211], a[184:187], v[12:15]\n\t"
"v_mfma_f32_16x16x32_bf16 v[12:15], a[224:227], a[188:191], v[12:15]\n\t"
"buffer_load_dwordx4 a[248:251], v252, s[8:11], s20 offen offset:1024 sc1\n\t"
"s_waitcnt lgkmcnt(3)\n\t"
"v_mfma_f32_16x16x32_bf16 v[16:19], a[220:223], a[184:187], v[16:19]\n\t"
"v_xor_b32 v244, 0xc0, v244\n\t"
"v_mfma_f32_16x16x32_bf16 v[16:19], a[208:211], a[188:191], v[16:19]\n\t"
"v_mfma_f32_16x16x32_bf16 v[16:19], a[224:227], a[192:195], v[16:19]\n\t"
"buffer_load_dwordx4 a[252:255], v252, s[8:11], s20 offen offset:2048 sc1\n\t"
"s_waitcnt lgkmcnt(2)\n\t"
"v_mfma_f32_16x16x32_bf16 v[20:23], a[220:223], a[188:191], v[20:23]\n\t"
"v_mfma_f32_16x16x32_bf16 v[20:23], a[208:211], a[192:195], v[20:23]\n\t"
"v_mfma_f32_16x16x32_bf16 v[20:23], a[224:227], a[196:199], v[20:23]\n\t"
"s_waitcnt lgkmcnt(1)\n\t"
"v_mfma_f32_16x16x32_bf16 v[24:27], a[220:223], a[192:195], v[24:27]\n\t"
"v_mfma_f32_16x16x32_bf16 v[24:27], a[208:211], a[196:199], v[24:27]\n\t"
"v_mfma_f32_16x16x32_bf16 v[24:27], a[224:227], a[200:203], v[24:27]\n\t"
"s_waitcnt lgkmcnt(0)\n\t"
"v_mfma_f32_16x16x32_bf16 v[28:31], a[220:223], a[196:199], v[28:31]\n\t"
"v_mfma_f32_16x16x32_bf16 v[28:31], a[208:211], a[200:203], v[28:31]\n\t"
"v_mfma_f32_16x16x32_bf16 v[28:31], a[224:227], a[204:207], v[28:31]\n\t"
"s_mov_b32 s20, 749568\n\t"
"buffer_load_dwordx4 a[220:223], v252, s[8:11], s20 offen offset:0 sc1\n\t"
"s_waitcnt vmcnt(7)\n\t"
"v_mfma_f32_16x16x32_bf16 v[32:35], a[212:215], a[168:171], v[32:35]\n\t"
"v_mfma_f32_16x16x32_bf16 v[32:35], a[216:219], a[172:175], v[32:35]\n\t"
"v_mfma_f32_16x16x32_bf16 v[32:35], a[228:231], a[176:179], v[32:35]\n\t"
"buffer_load_dwordx4 a[208:211], v252, s[8:11], s20 offen offset:1024 sc1\n\t"
"v_mfma_f32_16x16x32_bf16 v[36:39], a[212:215], a[172:175], v[36:39]\n\t"
"ds_read_b64_tr_b16 v[222:223], v244 offset:1536\n\t"
"v_mfma_f32_16x16x32_bf16 v[36:39], a[216:219], a[176:179], v[36:39]\n\t"
"ds_read_b64_tr_b16 v[224:225], v244 offset:4608\n\t"
"v_mfma_f32_16x16x32_bf16 v[36:39], a[228:231], a[180:183], v[36:39]\n\t"
"buffer_load_dwordx4 a[224:227], v252, s[8:11], s20 offen offset:2048 sc1\n\t"
"v_mfma_f32_16x16x32_bf16 v[40:43], a[212:215], a[176:179], v[40:43]\n\t"
"v_mfma_f32_16x16x32_bf16 v[40:43], a[216:219], a[180:183], v[40:43]\n\t"
"v_mfma_f32_16x16x32_bf16 v[40:43], a[228:231], a[184:187], v[40:43]\n\t"
"v_mfma_f32_16x16x32_bf16 v[44:47], a[212:215], a[180:183], v[44:47]\n\t"
"ds_read_b64_tr_b16 v[226:227], v244 offset:7680\n\t"
"v_mfma_f32_16x16x32_bf16 v[44:47], a[216:219], a[184:187], v[44:47]\n\t"
"ds_read_b64_tr_b16 v[228:229], v244 offset:10752\n\t"
"v_mfma_f32_16x16x32_bf16 v[44:47], a[228:231], a[188:191], v[44:47]\n\t"
"v_mfma_f32_16x16x32_bf16 v[48:51], a[212:215], a[184:187], v[48:51]\n\t"
"v_mfma_f32_16x16x32_bf16 v[48:51], a[216:219], a[188:191], v[48:51]\n\t"
"v_mfma_f32_16x16x32_bf16 v[48:51], a[228:231], a[192:195], v[48:51]\n\t"
"v_mfma_f32_16x16x32_bf16 v[52:55], a[212:215], a[188:191], v[52:55]\n\t"
"v_mfma_f32_16x16x32_bf16 v[52:55], a[216:219], a[192:195], v[52:55]\n\t"
"v_mfma_f32_16x16x32_bf16 v[52:55], a[228:231], a[196:199], v[52:55]\n\t"
"s_waitcnt lgkmcnt(2)\n\t"
"v_mfma_f32_16x16x32_bf16 v[222:225], v[222:225], v[218:221], 0\n\t"
"v_mfma_f32_16x16x32_bf16 v[56:59], a[212:215], a[192:195], v[56:59]\n\t"
"ds_read_b64_tr_b16 v[230:231], v244 offset:13824\n\t"
"v_mfma_f32_16x16x32_bf16 v[56:59], a[216:219], a[196:199], v[56:59]\n\t"
"ds_read_b64_tr_b16 v[232:233], v244 offset:16896\n\t"
"v_mfma_f32_16x16x32_bf16 v[56:59], a[228:231], a[200:203], v[56:59]\n\t"
"v_mfma_f32_16x16x32_bf16 v[60:63], a[212:215], a[196:199], v[60:63]\n\t"
"v_mfma_f32_16x16x32_bf16 v[60:63], a[216:219], a[200:203], v[60:63]\n\t"
"s_nop 0\n\t"
"v_cvt_pk_bf16_f32 v222, v222, v223\n\t"
"v_cvt_pk_bf16_f32 v223, v224, v225\n\t"
"v_mfma_f32_16x16x32_bf16 v[60:63], a[228:231], a[204:207], v[60:63]\n\t"
"s_waitcnt lgkmcnt(2)\n\t"
"v_mfma_f32_16x16x32_bf16 v[226:229], v[226:229], v[218:221], 0\n\t"
"s_mov_b32 s20, 761856\n\t"
"buffer_load_dwordx4 a[212:215], v252, s[8:11], s20 offen offset:0 sc1\n\t"
"s_waitcnt vmcnt(7)\n\t"
"v_mfma_f32_16x16x32_bf16 v[64:67], a[232:235], a[168:171], v[64:67]\n\t"
"ds_read_b64_tr_b16 v[234:235], v244 offset:21504\n\t"
"v_mfma_f32_16x16x32_bf16 v[64:67], a[236:239], a[172:175], v[64:67]\n\t"
"ds_read_b64_tr_b16 v[236:237], v244 offset:24576\n\t"
"v_mfma_f32_16x16x32_bf16 v[64:67], a[240:243], a[176:179], v[64:67]\n\t"
"buffer_load_dwordx4 a[216:219], v252, s[8:11], s20 offen offset:1024 sc1\n\t"
"v_mfma_f32_16x16x32_bf16 v[68:71], a[232:235], a[172:175], v[68:71]\n\t"
"ds_read_b128 a[168:171], v242 offset:9216\n\t"
"v_mfma_f32_16x16x32_bf16 v[68:71], a[236:239], a[176:179], v[68:71]\n\t"
"v_cvt_pk_bf16_f32 v226, v226, v227\n\t"
"v_cvt_pk_bf16_f32 v227, v228, v229\n\t"
"v_mfma_f32_16x16x32_bf16 v[68:71], a[240:243], a[180:183], v[68:71]\n\t"
"buffer_load_dwordx4 a[228:231], v252, s[8:11], s20 offen offset:2048 sc1\n\t"
"v_mfma_f32_16x16x32_bf16 v[72:75], a[232:235], a[176:179], v[72:75]\n\t"
"ds_read_b128 a[172:175], v242 offset:9728\n\t"
"v_mfma_f32_16x16x32_bf16 v[72:75], a[236:239], a[180:183], v[72:75]\n\t"
"ds_read_b128 a[176:179], v242 offset:10240\n\t"
"v_mfma_f32_16x16x32_bf16 v[72:75], a[240:243], a[184:187], v[72:75]\n\t"
"s_add_u32 m0, s24, 80128\n\t"
"s_add_u32 s28, s21, 2359296\n\t"
"buffer_load_dwordx4 v245, s[12:15], s28 offen offset:256 nt lds\n\t"
"s_add_u32 m0, s24, 86272\n\t"
"s_add_u32 s28, s21, 2555904\n\t"
"buffer_load_dwordx4 v245, s[12:15], s28 offen offset:256 nt lds\n\t"
"s_waitcnt lgkmcnt(5)\n\t"
"v_mfma_f32_16x16x32_bf16 v[230:233], v[230:233], v[218:221], 0\n\t"
"v_mfma_f32_16x16x32_bf16 v[76:79], a[232:235], a[180:183], v[76:79]\n\t"
"ds_read_b128 a[180:183], v242 offset:10752\n\t"
"v_mfma_f32_16x16x32_bf16 v[76:79], a[236:239], a[184:187], v[76:79]\n\t"
"v_mfma_f32_16x16x32_bf16 v[76:79], a[240:243], a[188:191], v[76:79]\n\t"
"s_add_u32 m0, s24, 92416\n\t"
"s_add_u32 s28, s21, 2752512\n\t"
"buffer_load_dwordx4 v245, s[12:15], s28 offen offset:256 nt lds\n\t"
"s_add_u32 m0, s24, 100096\n\t"
"s_add_u32 s28, s21, 2949120\n\t"
"buffer_load_dwordx4 v245, s[12:15], s28 offen offset:256 nt lds\n\t"
"v_mfma_f32_16x16x32_bf16 v[80:83], a[232:235], a[184:187], v[80:83]\n\t"
"ds_read_b128 a[184:187], v242 offset:11264\n\t"
"v_mfma_f32_16x16x32_bf16 v[80:83], a[236:239], a[188:191], v[80:83]\n\t"
"v_cvt_pk_bf16_f32 v230, v230, v231\n\t"
"v_cvt_pk_bf16_f32 v231, v232, v233\n\t"
"v_mfma_f32_16x16x32_bf16 v[80:83], a[240:243], a[192:195], v[80:83]\n\t"
"s_add_u32 m0, s24, 106240\n\t"
"s_add_u32 s28, s21, 3145728\n\t"
"buffer_load_dwordx4 v245, s[12:15], s28 offen offset:256 nt lds\n\t"
"s_waitcnt lgkmcnt(5)\n\t"
"v_mfma_f32_16x16x32_bf16 v[234:237], v[234:237], v[218:221], 0\n\t"
"s_add_u32 m0, s24, 112384\n\t"
"s_add_u32 s28, s21, 3342336\n\t"
"buffer_load_dwordx4 v245, s[12:15], s28 offen offset:256 nt lds\n\t"
"v_mfma_f32_16x16x32_bf16 v[84:87], a[232:235], a[188:191], v[84:87]\n\t"
"ds_read_b128 a[188:191], v242 offset:11776\n\t"
"v_mfma_f32_16x16x32_bf16 v[84:87], a[236:239], a[192:195], v[84:87]\n\t"
"v_mfma_f32_16x16x32_bf16 v[84:87], a[240:243], a[196:199], v[84:87]\n\t"
"v_mfma_f32_16x16x32_bf16 v[88:91], a[232:235], a[192:195], v[88:91]\n\t"
"ds_read_b128 a[192:195], v242 offset:12288\n\t"
"v_mfma_f32_16x16x32_bf16 v[88:91], a[236:239], a[196:199], v[88:91]\n\t"
"v_cvt_pk_bf16_f32 v234, v234, v235\n\t"
"v_mfma_f32_16x16x32_bf16 v[88:91], a[240:243], a[200:203], v[88:91]\n\t"
"v_cvt_pk_bf16_f32 v235, v236, v237\n\t"
"v_mfma_f32_16x16x32_bf16 v[92:95], a[232:235], a[196:199], v[92:95]\n\t"
"ds_read_b128 a[196:199], v242 offset:12800\n\t"
"v_mfma_f32_16x16x32_bf16 v[92:95], a[236:239], a[200:203], v[92:95]\n\t"
"ds_read_b128 a[200:203], v242 offset:13312\n\t"
"v_mfma_f32_16x16x32_bf16 v[92:95], a[240:243], a[204:207], v[92:95]\n\t"
"ds_read_b128 a[204:207], v242 offset:13824\n\t"
"s_waitcnt vmcnt(12) lgkmcnt(7)\n\t"
"v_mfma_f32_16x16x32_bf16 v[98:101], a[244:247], a[168:171], v[98:101]\n\t"
"v_mfma_f32_16x16x32_bf16 v[98:101], a[248:251], a[172:175], v[98:101]\n\t"
"v_mfma_f32_16x16x32_bf16 v[98:101], a[252:255], a[176:179], v[98:101]\n\t"
"s_waitcnt lgkmcnt(6)\n\t"
"v_mfma_f32_16x16x32_bf16 v[102:105], a[244:247], a[172:175], v[102:105]\n\t"
"v_mfma_f32_16x16x32_bf16 v[102:105], a[248:251], a[176:179], v[102:105]\n\t"
"v_mfma_f32_16x16x32_bf16 v[102:105], a[252:255], a[180:183], v[102:105]\n\t"
"s_waitcnt lgkmcnt(0)\n\t"
"s_barrier\n\t"
"v_mfma_f32_16x16x32_bf16 v[106:109], a[244:247], a[176:179], v[106:109]\n\t"
"v_mfma_f32_16x16x32_bf16 v[106:109], a[248:251], a[180:183], v[106:109]\n\t"
"v_mfma_f32_16x16x32_bf16 v[106:109], a[252:255], a[184:187], v[106:109]\n\t"
"ds_write_b64 v243, v[222:223] offset:0\n\t"
"ds_write_b64 v243, v[226:227] offset:512\n\t"
"v_mfma_f32_16x16x32_bf16 v[110:113], a[244:247], a[180:183], v[110:113]\n\t"
"ds_read_b64_tr_b16 v[222:223], v244 offset:27648\n\t"
"v_mfma_f32_16x16x32_bf16 v[110:113], a[248:251], a[184:187], v[110:113]\n\t"
"ds_read_b64_tr_b16 v[224:225], v244 offset:30720\n\t"
"v_mfma_f32_16x16x32_bf16 v[110:113], a[252:255], a[188:191], v[110:113]\n\t"
"ds_write_b64 v243, v[230:231] offset:1024\n\t"
"v_mfma_f32_16x16x32_bf16 v[114:117], a[244:247], a[184:187], v[114:117]\n\t"
"ds_read_b64_tr_b16 v[226:227], v244 offset:33792\n\t"
"v_mfma_f32_16x16x32_bf16 v[114:117], a[248:251], a[188:191], v[114:117]\n\t"
"ds_read_b64_tr_b16 v[228:229], v244 offset:36864\n\t"
"v_mfma_f32_16x16x32_bf16 v[114:117], a[252:255], a[192:195], v[114:117]\n\t"
"ds_write_b64 v243, v[234:235] offset:1536\n\t"
"v_mfma_f32_16x16x32_bf16 v[118:121], a[244:247], a[188:191], v[118:121]\n\t"
"ds_read_b64_tr_b16 v[230:231], v244 offset:41472\n\t"
"v_mfma_f32_16x16x32_bf16 v[118:121], a[248:251], a[192:195], v[118:121]\n\t"
"ds_read_b64_tr_b16 v[232:233], v244 offset:44544\n\t"
"v_mfma_f32_16x16x32_bf16 v[118:121], a[252:255], a[196:199], v[118:121]\n\t"
"s_mov_b32 s20, 774144\n\t"
"buffer_load_dwordx4 a[232:235], v252, s[8:11], s20 offen offset:0 sc1\n\t"
"s_waitcnt lgkmcnt(6)\n\t"
"v_mfma_f32_16x16x32_bf16 v[222:225], v[222:225], v[218:221], 0\n\t"
"ds_read_b64_tr_b16 v[234:235], v244 offset:47616\n\t"
"ds_read_b64_tr_b16 v[236:237], v244 offset:50688\n\t"
"v_mfma_f32_16x16x32_bf16 v[122:125], a[244:247], a[192:195], v[122:125]\n\t"
"v_mfma_f32_16x16x32_bf16 v[122:125], a[248:251], a[196:199], v[122:125]\n\t"
"v_mfma_f32_16x16x32_bf16 v[122:125], a[252:255], a[200:203], v[122:125]\n\t"
"s_waitcnt lgkmcnt(5)\n\t"
"v_mfma_f32_16x16x32_bf16 v[226:229], v[226:229], v[218:221], 0\n\t"
"v_mfma_f32_16x16x32_bf16 v[126:129], a[244:247], a[196:199], v[126:129]\n\t"
"v_cvt_pk_bf16_f32 v222, v222, v223\n\t"
"v_cvt_pk_bf16_f32 v223, v224, v225\n\t"
"v_mfma_f32_16x16x32_bf16 v[126:129], a[248:251], a[200:203], v[126:129]\n\t"
"v_mfma_f32_16x16x32_bf16 v[126:129], a[252:255], a[204:207], v[126:129]\n\t"
"ds_write_b64 v243, v[222:223] offset:2048\n\t"
"s_waitcnt lgkmcnt(3)\n\t"
"v_mfma_f32_16x16x32_bf16 v[230:233], v[230:233], v[218:221], 0\n\t"
"ds_read_b64_tr_b16 v[222:223], v244 offset:53760\n\t"
"s_waitcnt vmcnt(10)\n\t"
"v_mfma_f32_16x16x32_bf16 v[130:133], a[220:223], a[168:171], v[130:133]\n\t"
"v_cvt_pk_bf16_f32 v226, v226, v227\n\t"
"v_cvt_pk_bf16_f32 v227, v228, v229\n\t"
"v_mfma_f32_16x16x32_bf16 v[130:133], a[208:211], a[172:175], v[130:133]\n\t"
"ds_read_b64_tr_b16 v[224:225], v244 offset:56832\n\t"
"v_mfma_f32_16x16x32_bf16 v[130:133], a[224:227], a[176:179], v[130:133]\n\t"
"s_waitcnt lgkmcnt(3)\n\t"
"v_mfma_f32_16x16x32_bf16 v[234:237], v[234:237], v[218:221], 0\n\t"
"ds_write_b64 v243, v[226:227] offset:2560\n\t"
"v_mfma_f32_16x16x32_bf16 v[134:137], a[220:223], a[172:175], v[134:137]\n\t"
"v_cvt_pk_bf16_f32 v230, v230, v231\n\t"
"v_cvt_pk_bf16_f32 v231, v232, v233\n\t"
"v_mfma_f32_16x16x32_bf16 v[134:137], a[208:211], a[176:179], v[134:137]\n\t"
"v_mfma_f32_16x16x32_bf16 v[134:137], a[224:227], a[180:183], v[134:137]\n\t"
"ds_write_b64 v243, v[230:231] offset:3072\n\t"
"v_mfma_f32_16x16x32_bf16 v[138:141], a[220:223], a[176:179], v[138:141]\n\t"
"v_mfma_f32_16x16x32_bf16 v[138:141], a[208:211], a[180:183], v[138:141]\n\t"
"v_cvt_pk_bf16_f32 v234, v234, v235\n\t"
"v_cvt_pk_bf16_f32 v235, v236, v237\n\t"
"v_mfma_f32_16x16x32_bf16 v[138:141], a[224:227], a[184:187], v[138:141]\n\t"
"v_mfma_f32_16x16x32_bf16 v[142:145], a[220:223], a[180:183], v[142:145]\n\t"
"v_mfma_f32_16x16x32_bf16 v[142:145], a[208:211], a[184:187], v[142:145]\n\t"
"v_mfma_f32_16x16x32_bf16 v[142:145], a[224:227], a[188:191], v[142:145]\n\t"
"buffer_load_dwordx4 a[236:239], v252, s[8:11], s20 offen offset:1024 sc1\n\t"
"s_waitcnt lgkmcnt(2)\n\t"
"v_mfma_f32_16x16x32_bf16 v[222:225], v[222:225], v[218:221], 0\n\t"
"ds_write_b64 v243, v[234:235] offset:3584\n\t"
"v_mfma_f32_16x16x32_bf16 v[146:149], a[220:223], a[184:187], v[146:149]\n\t"
"v_mfma_f32_16x16x32_bf16 v[146:149], a[208:211], a[188:191], v[146:149]\n\t"
"v_mfma_f32_16x16x32_bf16 v[146:149], a[224:227], a[192:195], v[146:149]\n\t"
"buffer_load_dwordx4 a[240:243], v252, s[8:11], s20 offen offset:2048 sc1\n\t"
"v_mfma_f32_16x16x32_bf16 v[150:153], a[220:223], a[188:191], v[150:153]\n\t"
"v_mfma_f32_16x16x32_bf16 v[150:153], a[208:211], a[192:195], v[150:153]\n\t"
"s_nop 0\n\t"
"v_cvt_pk_bf16_f32 v222, v222, v223\n\t"
"v_cvt_pk_bf16_f32 v223, v224, v225\n\t"
"v_mfma_f32_16x16x32_bf16 v[150:153], a[224:227], a[196:199], v[150:153]\n\t"
"s_mov_b32 s20, 786432\n\t"
"buffer_load_dwordx4 a[244:247], v252, s[8:11], s20 offen offset:0 sc1\n\t"
"v_mfma_f32_16x16x32_bf16 v[154:157], a[220:223], a[192:195], v[154:157]\n\t"
"v_mfma_f32_16x16x32_bf16 v[154:157], a[208:211], a[196:199], v[154:157]\n\t"
"v_mfma_f32_16x16x32_bf16 v[154:157], a[224:227], a[200:203], v[154:157]\n\t"
"ds_write_b64 v243, v[222:223] offset:4096\n\t"
"v_mfma_f32_16x16x32_bf16 v[158:161], a[220:223], a[196:199], v[158:161]\n\t"
"v_mfma_f32_16x16x32_bf16 v[158:161], a[208:211], a[200:203], v[158:161]\n\t"
"v_mfma_f32_16x16x32_bf16 v[158:161], a[224:227], a[204:207], v[158:161]\n\t"
"buffer_load_dwordx4 a[248:251], v252, s[8:11], s20 offen offset:1024 sc1\n\t"
"s_waitcnt vmcnt(11)\n\t"
"v_mfma_f32_16x16x32_bf16 v[162:165], a[212:215], a[168:171], v[162:165]\n\t"
"v_mfma_f32_16x16x32_bf16 v[162:165], a[216:219], a[172:175], v[162:165]\n\t"
"v_mfma_f32_16x16x32_bf16 v[162:165], a[228:231], a[176:179], v[162:165]\n\t"
"buffer_load_dwordx4 a[208:211], v252, s[8:11], s20 offen offset:2048 sc1\n\t"
"s_waitcnt lgkmcnt(0)\n\t"
"s_barrier\n\t"
"v_mfma_f32_16x16x32_bf16 v[166:169], a[212:215], a[172:175], v[166:169]\n\t"
"ds_read_b128 a[168:171], v242 offset:0\n\t"
"v_mfma_f32_16x16x32_bf16 v[166:169], a[216:219], a[176:179], v[166:169]\n\t"
"ds_read_b128 a[172:175], v242 offset:512\n\t"
"v_mfma_f32_16x16x32_bf16 v[166:169], a[228:231], a[180:183], v[166:169]\n\t"
"s_mov_b32 s20, 798720\n\t"
"buffer_load_dwordx4 a[220:223], v252, s[8:11], s20 offen offset:0 sc1\n\t"
"v_mfma_f32_16x16x32_bf16 v[170:173], a[212:215], a[176:179], v[170:173]\n\t"
"ds_read_b128 a[176:179], v242 offset:1024\n\t"
"v_mfma_f32_16x16x32_bf16 v[170:173], a[216:219], a[180:183], v[170:173]\n\t"
"v_mfma_f32_16x16x32_bf16 v[170:173], a[228:231], a[184:187], v[170:173]\n\t"
"buffer_load_dwordx4 a[224:227], v252, s[8:11], s20 offen offset:1024 sc1\n\t"
"v_mfma_f32_16x16x32_bf16 v[174:177], a[212:215], a[180:183], v[174:177]\n\t"
"ds_read_b128 a[180:183], v242 offset:1536\n\t"
"v_mfma_f32_16x16x32_bf16 v[174:177], a[216:219], a[184:187], v[174:177]\n\t"
"v_mfma_f32_16x16x32_bf16 v[174:177], a[228:231], a[188:191], v[174:177]\n\t"
"buffer_load_dwordx4 a[252:255], v252, s[8:11], s20 offen offset:2048 sc1\n\t"
"v_mfma_f32_16x16x32_bf16 v[178:181], a[212:215], a[184:187], v[178:181]\n\t"
"ds_read_b128 a[184:187], v242 offset:2048\n\t"
"v_mfma_f32_16x16x32_bf16 v[178:181], a[216:219], a[188:191], v[178:181]\n\t"
"v_mfma_f32_16x16x32_bf16 v[178:181], a[228:231], a[192:195], v[178:181]\n\t"
"v_mfma_f32_16x16x32_bf16 v[182:185], a[212:215], a[188:191], v[182:185]\n\t"
"ds_read_b128 a[188:191], v242 offset:2560\n\t"
"v_mfma_f32_16x16x32_bf16 v[182:185], a[216:219], a[192:195], v[182:185]\n\t"
"v_mfma_f32_16x16x32_bf16 v[182:185], a[228:231], a[196:199], v[182:185]\n\t"
"v_mfma_f32_16x16x32_bf16 v[186:189], a[212:215], a[192:195], v[186:189]\n\t"
"ds_read_b128 a[192:195], v242 offset:3072\n\t"
"v_mfma_f32_16x16x32_bf16 v[186:189], a[216:219], a[196:199], v[186:189]\n\t"
"v_mfma_f32_16x16x32_bf16 v[186:189], a[228:231], a[200:203], v[186:189]\n\t"
"v_mfma_f32_16x16x32_bf16 v[190:193], a[212:215], a[196:199], v[190:193]\n\t"
"ds_read_b128 a[196:199], v242 offset:3584\n\t"
"v_mfma_f32_16x16x32_bf16 v[190:193], a[216:219], a[200:203], v[190:193]\n\t"
"ds_read_b128 a[200:203], v242 offset:4096\n\t"
"v_mfma_f32_16x16x32_bf16 v[190:193], a[228:231], a[204:207], v[190:193]\n\t"
"s_mov_b32 s20, 811008\n\t"
"buffer_load_dwordx4 a[212:215], v252, s[8:11], s20 offen offset:0 sc1\n\t"
"s_waitcnt vmcnt(7) lgkmcnt(6)\n\t"
"v_mfma_f32_16x16x32_bf16 v[194:197], a[232:235], a[168:171], v[194:197]\n\t"
"ds_read_b128 a[204:207], v242 offset:4608\n\t"
"v_mfma_f32_16x16x32_bf16 v[194:197], a[236:239], a[172:175], v[194:197]\n\t"
"v_mfma_f32_16x16x32_bf16 v[194:197], a[240:243], a[176:179], v[194:197]\n\t"
"buffer_load_dwordx4 a[216:219], v252, s[8:11], s20 offen offset:1024 sc1\n\t"
"s_waitcnt lgkmcnt(6)\n\t"
"v_mfma_f32_16x16x32_bf16 v[202:205], a[232:235], a[172:175], v[202:205]\n\t"
"v_mfma_f32_16x16x32_bf16 v[202:205], a[236:239], a[176:179], v[202:205]\n\t"
"v_mfma_f32_16x16x32_bf16 v[202:205], a[240:243], a[180:183], v[202:205]\n\t"
"buffer_load_dwordx4 a[228:231], v252, s[8:11], s20 offen offset:2048 sc1\n\t"
"s_waitcnt lgkmcnt(5)\n\t"
"v_mfma_f32_16x16x32_bf16 v[210:213], a[232:235], a[176:179], v[210:213]\n\t"
"v_mfma_f32_16x16x32_bf16 v[210:213], a[236:239], a[180:183], v[210:213]\n\t"
"v_mfma_f32_16x16x32_bf16 v[210:213], a[240:243], a[184:187], v[210:213]\n\t"
"s_waitcnt lgkmcnt(4)\n\t"
"v_mfma_f32_16x16x32_bf16 a[0:3], a[232:235], a[180:183], a[0:3]\n\t"
"v_mfma_f32_16x16x32_bf16 a[0:3], a[236:239], a[184:187], a[0:3]\n\t"
"v_mfma_f32_16x16x32_bf16 a[0:3], a[240:243], a[188:191], a[0:3]\n\t"
"s_waitcnt lgkmcnt(3)\n\t"
"v_mfma_f32_16x16x32_bf16 a[8:11], a[232:235], a[184:187], a[8:11]\n\t"
"v_mfma_f32_16x16x32_bf16 a[8:11], a[236:239], a[188:191], a[8:11]\n\t"
"v_mfma_f32_16x16x32_bf16 a[8:11], a[240:243], a[192:195], a[8:11]\n\t"
"s_waitcnt lgkmcnt(2)\n\t"
"v_mfma_f32_16x16x32_bf16 a[16:19], a[232:235], a[188:191], a[16:19]\n\t"
"v_mfma_f32_16x16x32_bf16 a[16:19], a[236:239], a[192:195], a[16:19]\n\t"
"v_mfma_f32_16x16x32_bf16 a[16:19], a[240:243], a[196:199], a[16:19]\n\t"
"s_waitcnt lgkmcnt(1)\n\t"
"v_mfma_f32_16x16x32_bf16 a[24:27], a[232:235], a[192:195], a[24:27]\n\t"
"v_mfma_f32_16x16x32_bf16 a[24:27], a[236:239], a[196:199], a[24:27]\n\t"
"v_mfma_f32_16x16x32_bf16 a[24:27], a[240:243], a[200:203], a[24:27]\n\t"
"s_waitcnt lgkmcnt(0)\n\t"
"v_mfma_f32_16x16x32_bf16 a[32:35], a[232:235], a[196:199], a[32:35]\n\t"
"v_mfma_f32_16x16x32_bf16 a[32:35], a[236:239], a[200:203], a[32:35]\n\t"
"v_mfma_f32_16x16x32_bf16 a[32:35], a[240:243], a[204:207], a[32:35]\n\t"
"s_mov_b32 s20, 823296\n\t"
"buffer_load_dwordx4 a[232:235], v252, s[8:11], s20 offen offset:0 sc1\n\t"
"s_waitcnt vmcnt(7)\n\t"
"v_mfma_f32_16x16x32_bf16 a[40:43], a[244:247], a[168:171], a[40:43]\n\t"
"v_mfma_f32_16x16x32_bf16 a[40:43], a[248:251], a[172:175], a[40:43]\n\t"
"v_mfma_f32_16x16x32_bf16 a[40:43], a[208:211], a[176:179], a[40:43]\n\t"
"buffer_load_dwordx4 a[236:239], v252, s[8:11], s20 offen offset:1024 sc1\n\t"
"v_mfma_f32_16x16x32_bf16 a[48:51], a[244:247], a[172:175], a[48:51]\n\t"
"v_mfma_f32_16x16x32_bf16 a[48:51], a[248:251], a[176:179], a[48:51]\n\t"
"v_mfma_f32_16x16x32_bf16 a[48:51], a[208:211], a[180:183], a[48:51]\n\t"
"buffer_load_dwordx4 a[240:243], v252, s[8:11], s20 offen offset:2048 sc1\n\t"
"v_mfma_f32_16x16x32_bf16 a[56:59], a[244:247], a[176:179], a[56:59]\n\t"
"v_mfma_f32_16x16x32_bf16 a[56:59], a[248:251], a[180:183], a[56:59]\n\t"
"v_mfma_f32_16x16x32_bf16 a[56:59], a[208:211], a[184:187], a[56:59]\n\t"
"v_mfma_f32_16x16x32_bf16 a[64:67], a[244:247], a[180:183], a[64:67]\n\t"
"v_mfma_f32_16x16x32_bf16 a[64:67], a[248:251], a[184:187], a[64:67]\n\t"
"v_mfma_f32_16x16x32_bf16 a[64:67], a[208:211], a[188:191], a[64:67]\n\t"
"s_barrier\n\t"
"v_mfma_f32_16x16x32_bf16 a[72:75], a[244:247], a[184:187], a[72:75]\n\t"
"ds_read_b64_tr_b16 v[226:227], v244 offset:512\n\t"
"v_mfma_f32_16x16x32_bf16 a[72:75], a[248:251], a[188:191], a[72:75]\n\t"
"ds_read_b64_tr_b16 v[228:229], v244 offset:3584\n\t"
"v_mfma_f32_16x16x32_bf16 a[72:75], a[208:211], a[192:195], a[72:75]\n\t"
"ds_read_b64_tr_b16 v[230:231], v244 offset:6656\n\t"
"ds_read_b64_tr_b16 v[232:233], v244 offset:9728\n\t"
"v_mfma_f32_16x16x32_bf16 a[80:83], a[244:247], a[188:191], a[80:83]\n\t"
"v_mfma_f32_16x16x32_bf16 a[80:83], a[248:251], a[192:195], a[80:83]\n\t"
"v_mfma_f32_16x16x32_bf16 a[80:83], a[208:211], a[196:199], a[80:83]\n\t"
"v_mfma_f32_16x16x32_bf16 a[88:91], a[244:247], a[192:195], a[88:91]\n\t"
"v_mfma_f32_16x16x32_bf16 a[88:91], a[248:251], a[196:199], a[88:91]\n\t"
"ds_read_b64_tr_b16 v[234:235], v244 offset:12800\n\t"
"v_mfma_f32_16x16x32_bf16 a[88:91], a[208:211], a[200:203], a[88:91]\n\t"
"ds_read_b64_tr_b16 v[236:237], v244 offset:15872\n\t"
"s_waitcnt lgkmcnt(4)\n\t"
"v_mfma_f32_16x16x32_bf16 v[226:229], v[226:229], v[218:221], 0\n\t"
"v_mfma_f32_16x16x32_bf16 a[96:99], a[244:247], a[196:199], a[96:99]\n\t"
"v_mfma_f32_16x16x32_bf16 a[96:99], a[248:251], a[200:203], a[96:99]\n\t"
"v_mfma_f32_16x16x32_bf16 a[96:99], a[208:211], a[204:207], a[96:99]\n\t"
"s_mov_b32 s20, 835584\n\t"
"buffer_load_dwordx4 a[244:247], v252, s[8:11], s20 offen offset:0 sc1\n\t"
"s_waitcnt vmcnt(7)\n\t"
"v_mfma_f32_16x16x32_bf16 a[104:107], a[220:223], a[168:171], a[104:107]\n\t"
"ds_read_b128 a[168:171], v242 offset:9216\n\t"
"v_mfma_f32_16x16x32_bf16 a[104:107], a[224:227], a[172:175], a[104:107]\n\t"
"v_cvt_pk_bf16_f32 v226, v226, v227\n\t"
"v_cvt_pk_bf16_f32 v227, v228, v229\n\t"
"v_mfma_f32_16x16x32_bf16 a[104:107], a[252:255], a[176:179], a[104:107]\n\t"
"buffer_load_dwordx4 a[208:211], v252, s[8:11], s20 offen offset:1024 sc1\n\t"
"s_waitcnt lgkmcnt(3)\n\t"
"v_mfma_f32_16x16x32_bf16 v[230:233], v[230:233], v[218:221], 0\n\t"
"ds_read_b64_tr_b16 v[222:223], v244 offset:20480\n\t"
"v_mfma_f32_16x16x32_bf16 a[112:115], a[220:223], a[172:175], a[112:115]\n\t"
"ds_read_b64_tr_b16 v[224:225], v244 offset:23552\n\t"
"v_mfma_f32_16x16x32_bf16 a[112:115], a[224:227], a[176:179], a[112:115]\n\t"
"ds_read_b128 a[172:175], v242 offset:9728\n\t"
"v_mfma_f32_16x16x32_bf16 a[112:115], a[252:255], a[180:183], a[112:115]\n\t"
"buffer_load_dwordx4 a[248:251], v252, s[8:11], s20 offen offset:2048 sc1\n\t"
"v_mfma_f32_16x16x32_bf16 a[120:123], a[220:223], a[176:179], a[120:123]\n\t"
"v_mfma_f32_16x16x32_bf16 a[120:123], a[224:227], a[180:183], a[120:123]\n\t"
"v_cvt_pk_bf16_f32 v230, v230, v231\n\t"
"v_mfma_f32_16x16x32_bf16 a[120:123], a[252:255], a[184:187], a[120:123]\n\t"
"v_cvt_pk_bf16_f32 v231, v232, v233\n\t"
"s_waitcnt lgkmcnt(4)\n\t"
"v_mfma_f32_16x16x32_bf16 v[234:237], v[234:237], v[218:221], 0\n\t"
"ds_read_b128 a[176:179], v242 offset:10240\n\t"
"v_mfma_f32_16x16x32_bf16 a[128:131], a[220:223], a[180:183], a[128:131]\n\t"
"ds_read_b128 a[180:183], v242 offset:10752\n\t"
"v_mfma_f32_16x16x32_bf16 a[128:131], a[224:227], a[184:187], a[128:131]\n\t"
"v_mfma_f32_16x16x32_bf16 a[128:131], a[252:255], a[188:191], a[128:131]\n\t"
"v_mfma_f32_16x16x32_bf16 a[136:139], a[220:223], a[184:187], a[136:139]\n\t"
"ds_read_b128 a[184:187], v242 offset:11264\n\t"
"v_mfma_f32_16x16x32_bf16 a[136:139], a[224:227], a[188:191], a[136:139]\n\t"
"v_cvt_pk_bf16_f32 v234, v234, v235\n\t"
"v_mfma_f32_16x16x32_bf16 a[136:139], a[252:255], a[192:195], a[136:139]\n\t"
"v_cvt_pk_bf16_f32 v235, v236, v237\n\t"
"s_waitcnt lgkmcnt(4)\n\t"
"v_mfma_f32_16x16x32_bf16 v[222:225], v[222:225], v[218:221], 0\n\t"
"v_mfma_f32_16x16x32_bf16 a[144:147], a[220:223], a[188:191], a[144:147]\n\t"
"ds_read_b128 a[188:191], v242 offset:11776\n\t"
"v_mfma_f32_16x16x32_bf16 a[144:147], a[224:227], a[192:195], a[144:147]\n\t"
"v_mfma_f32_16x16x32_bf16 a[144:147], a[252:255], a[196:199], a[144:147]\n\t"
"v_mfma_f32_16x16x32_bf16 a[152:155], a[220:223], a[192:195], a[152:155]\n\t"
"ds_read_b128 a[192:195], v242 offset:12288\n\t"
"v_mfma_f32_16x16x32_bf16 a[152:155], a[224:227], a[196:199], a[152:155]\n\t"
"s_nop 0\n\t"
"v_cvt_pk_bf16_f32 v222, v222, v223\n\t"
"v_mfma_f32_16x16x32_bf16 a[152:155], a[252:255], a[200:203], a[152:155]\n\t"
"v_cvt_pk_bf16_f32 v223, v224, v225\n\t"
"v_mfma_f32_16x16x32_bf16 a[160:163], a[220:223], a[196:199], a[160:163]\n\t"
"ds_read_b128 a[196:199], v242 offset:12800\n\t"
"v_mfma_f32_16x16x32_bf16 a[160:163], a[224:227], a[200:203], a[160:163]\n\t"
"ds_read_b128 a[200:203], v242 offset:13312\n\t"
"v_mfma_f32_16x16x32_bf16 a[160:163], a[252:255], a[204:207], a[160:163]\n\t"
"s_mov_b32 s20, 995328\n\t"
"buffer_load_dwordx4 a[220:223], v252, s[8:11], s20 offen offset:0 sc1\n\t"
"s_waitcnt vmcnt(7) lgkmcnt(6)\n\t"
"v_mfma_f32_16x16x32_bf16 v[198:201], a[212:215], a[168:171], v[198:201]\n\t"
"ds_read_b128 a[204:207], v242 offset:13824\n\t"
"v_mfma_f32_16x16x32_bf16 v[198:201], a[216:219], a[172:175], v[198:201]\n\t"
"v_mfma_f32_16x16x32_bf16 v[198:201], a[228:231], a[176:179], v[198:201]\n\t"
"buffer_load_dwordx4 a[224:227], v252, s[8:11], s20 offen offset:1024 sc1\n\t"
"s_waitcnt lgkmcnt(6)\n\t"
"v_mfma_f32_16x16x32_bf16 v[206:209], a[212:215], a[172:175], v[206:209]\n\t"
"v_mfma_f32_16x16x32_bf16 v[206:209], a[216:219], a[176:179], v[206:209]\n\t"
"v_mfma_f32_16x16x32_bf16 v[206:209], a[228:231], a[180:183], v[206:209]\n\t"
"buffer_load_dwordx4 a[252:255], v252, s[8:11], s20 offen offset:2048 sc1\n\t"
"s_waitcnt lgkmcnt(0)\n\t"
"s_barrier\n\t"
"v_mfma_f32_16x16x32_bf16 v[214:217], a[212:215], a[176:179], v[214:217]\n\t"
"v_mfma_f32_16x16x32_bf16 v[214:217], a[216:219], a[180:183], v[214:217]\n\t"
"v_mfma_f32_16x16x32_bf16 v[214:217], a[228:231], a[184:187], v[214:217]\n\t"
"ds_write_b64 v243, v[226:227] offset:0\n\t"
"ds_write_b64 v243, v[230:231] offset:512\n\t"
"v_mfma_f32_16x16x32_bf16 a[4:7], a[212:215], a[180:183], a[4:7]\n\t"
"ds_read_b64_tr_b16 v[226:227], v244 offset:26624\n\t"
"v_mfma_f32_16x16x32_bf16 a[4:7], a[216:219], a[184:187], a[4:7]\n\t"
"ds_read_b64_tr_b16 v[228:229], v244 offset:29696\n\t"
"v_mfma_f32_16x16x32_bf16 a[4:7], a[228:231], a[188:191], a[4:7]\n\t"
"ds_write_b64 v243, v[234:235] offset:1024\n\t"
"v_mfma_f32_16x16x32_bf16 a[12:15], a[212:215], a[184:187], a[12:15]\n\t"
"ds_read_b64_tr_b16 v[230:231], v244 offset:32768\n\t"
"v_mfma_f32_16x16x32_bf16 a[12:15], a[216:219], a[188:191], a[12:15]\n\t"
"ds_read_b64_tr_b16 v[232:233], v244 offset:35840\n\t"
"v_mfma_f32_16x16x32_bf16 a[12:15], a[228:231], a[192:195], a[12:15]\n\t"
"ds_write_b64 v243, v[222:223] offset:1536\n\t"
"v_mad_u32_u24 v255, v252, 24, s50\n\t"
"s_add_u32 s28, s21, 6291072\n\t"
"s_mov_b32 exec_hi, 3\n\t"
"buffer_load_dword v255, v255, s[12:15], s28 offen offset:0\n\t"
"s_mov_b32 exec_hi, -1\n\t"
"v_mfma_f32_16x16x32_bf16 a[20:23], a[212:215], a[188:191], a[20:23]\n\t"
"ds_read_b64_tr_b16 v[234:235], v244 offset:40448\n\t"
"v_mfma_f32_16x16x32_bf16 a[20:23], a[216:219], a[192:195], a[20:23]\n\t"
"ds_read_b64_tr_b16 v[236:237], v244 offset:43520\n\t"
"v_mfma_f32_16x16x32_bf16 a[20:23], a[228:231], a[196:199], a[20:23]\n\t"
"ds_read_b64_tr_b16 v[222:223], v244 offset:46592\n\t"
"ds_read_b64_tr_b16 v[224:225], v244 offset:49664\n\t"
"s_waitcnt lgkmcnt(8)\n\t"
"v_mfma_f32_16x16x32_bf16 v[226:229], v[226:229], v[218:221], 0\n\t"
"v_mfma_f32_16x16x32_bf16 a[28:31], a[212:215], a[192:195], a[28:31]\n\t"
"v_mfma_f32_16x16x32_bf16 a[28:31], a[216:219], a[196:199], a[28:31]\n\t"
"v_mfma_f32_16x16x32_bf16 a[28:31], a[228:231], a[200:203], a[28:31]\n\t"
"s_waitcnt lgkmcnt(5)\n\t"
"v_mfma_f32_16x16x32_bf16 v[230:233], v[230:233], v[218:221], 0\n\t"
"v_mfma_f32_16x16x32_bf16 a[36:39], a[212:215], a[196:199], a[36:39]\n\t"
"s_nop 1\n\t"
"v_cvt_pk_bf16_f32 v226, v226, v227\n\t"
"v_cvt_pk_bf16_f32 v227, v228, v229\n\t"
"v_mfma_f32_16x16x32_bf16 a[36:39], a[216:219], a[200:203], a[36:39]\n\t"
"v_mfma_f32_16x16x32_bf16 a[36:39], a[228:231], a[204:207], a[36:39]\n\t"
"s_mov_b32 s20, 1007616\n\t"
"buffer_load_dwordx4 a[212:215], v252, s[8:11], s20 offen offset:0 sc1\n\t"
"s_waitcnt lgkmcnt(2)\n\t"
"v_mfma_f32_16x16x32_bf16 v[234:237], v[234:237], v[218:221], 0\n\t"
"ds_write_b64 v243, v[226:227] offset:2048\n\t"
"s_waitcnt vmcnt(8)\n\t"
"v_mfma_f32_16x16x32_bf16 a[44:47], a[232:235], a[168:171], a[44:47]\n\t"
"v_cvt_pk_bf16_f32 v230, v230, v231\n\t"
"v_cvt_pk_bf16_f32 v231, v232, v233\n\t"
"v_mfma_f32_16x16x32_bf16 a[44:47], a[236:239], a[172:175], a[44:47]\n\t"
"ds_read_b64_tr_b16 v[226:227], v244 offset:52736\n\t"
"v_mfma_f32_16x16x32_bf16 a[44:47], a[240:243], a[176:179], a[44:47]\n\t"
"ds_read_b64_tr_b16 v[228:229], v244 offset:55808\n\t"
"s_waitcnt lgkmcnt(3)\n\t"
"v_mfma_f32_16x16x32_bf16 v[222:225], v[222:225], v[218:221], 0\n\t"
"ds_write_b64 v243, v[230:231] offset:2560\n\t"
"v_mfma_f32_16x16x32_bf16 a[52:55], a[232:235], a[172:175], a[52:55]\n\t"
"v_cvt_pk_bf16_f32 v234, v234, v235\n\t"
"v_cvt_pk_bf16_f32 v235, v236, v237\n\t"
"v_mfma_f32_16x16x32_bf16 a[52:55], a[236:239], a[176:179], a[52:55]\n\t"
"v_mfma_f32_16x16x32_bf16 a[52:55], a[240:243], a[180:183], a[52:55]\n\t"
"ds_write_b64 v243, v[234:235] offset:3072\n\t"
"v_mfma_f32_16x16x32_bf16 a[60:63], a[232:235], a[176:179], a[60:63]\n\t"
"v_mfma_f32_16x16x32_bf16 a[60:63], a[236:239], a[180:183], a[60:63]\n\t"
"v_cvt_pk_bf16_f32 v222, v222, v223\n\t"
"v_cvt_pk_bf16_f32 v223, v224, v225\n\t"
"v_mfma_f32_16x16x32_bf16 a[60:63], a[240:243], a[184:187], a[60:63]\n\t"
"v_mfma_f32_16x16x32_bf16 a[68:71], a[232:235], a[180:183], a[68:71]\n\t"
"v_mfma_f32_16x16x32_bf16 a[68:71], a[236:239], a[184:187], a[68:71]\n\t"
"v_mfma_f32_16x16x32_bf16 a[68:71], a[240:243], a[188:191], a[68:71]\n\t"
"ds_write_b64 v243, v[222:223] offset:3584\n\t"
"s_waitcnt lgkmcnt(3)\n\t"
"v_mfma_f32_16x16x32_bf16 v[226:229], v[226:229], v[218:221], 0\n\t"
"v_mfma_f32_16x16x32_bf16 a[76:79], a[232:235], a[184:187], a[76:79]\n\t"
"v_mfma_f32_16x16x32_bf16 a[76:79], a[236:239], a[188:191], a[76:79]\n\t"
"v_mfma_f32_16x16x32_bf16 a[76:79], a[240:243], a[192:195], a[76:79]\n\t"
"v_mfma_f32_16x16x32_bf16 a[84:87], a[232:235], a[188:191], a[84:87]\n\t"
"v_mfma_f32_16x16x32_bf16 a[84:87], a[236:239], a[192:195], a[84:87]\n\t"
"s_nop 2\n\t"
"v_cvt_pk_bf16_f32 v226, v226, v227\n\t"
"v_cvt_pk_bf16_f32 v227, v228, v229\n\t"
"v_mfma_f32_16x16x32_bf16 a[84:87], a[240:243], a[196:199], a[84:87]\n\t"
"v_mfma_f32_16x16x32_bf16 a[92:95], a[232:235], a[192:195], a[92:95]\n\t"
"v_mfma_f32_16x16x32_bf16 a[92:95], a[236:239], a[196:199], a[92:95]\n\t"
"v_mfma_f32_16x16x32_bf16 a[92:95], a[240:243], a[200:203], a[92:95]\n\t"
"ds_write_b64 v243, v[226:227] offset:4096\n\t"
"v_mfma_f32_16x16x32_bf16 a[100:103], a[232:235], a[196:199], a[100:103]\n\t"
"v_mfma_f32_16x16x32_bf16 a[100:103], a[236:239], a[200:203], a[100:103]\n\t"
"v_mfma_f32_16x16x32_bf16 a[100:103], a[240:243], a[204:207], a[100:103]\n\t"
"s_waitcnt vmcnt(5)\n\t"
"v_mfma_f32_16x16x32_bf16 a[108:111], a[244:247], a[168:171], a[108:111]\n\t"
"v_mfma_f32_16x16x32_bf16 a[108:111], a[208:211], a[172:175], a[108:111]\n\t"
"v_mfma_f32_16x16x32_bf16 a[108:111], a[248:251], a[176:179], a[108:111]\n\t"
"s_waitcnt lgkmcnt(0)\n\t"
"s_barrier\n\t"
"v_mfma_f32_16x16x32_bf16 a[116:119], a[244:247], a[172:175], a[116:119]\n\t"
"ds_read_b128 a[168:171], v242 offset:0\n\t"
"v_mfma_f32_16x16x32_bf16 a[116:119], a[208:211], a[176:179], a[116:119]\n\t"
"ds_read_b128 a[172:175], v242 offset:512\n\t"
"v_mfma_f32_16x16x32_bf16 a[116:119], a[248:251], a[180:183], a[116:119]\n\t"
"v_mfma_f32_16x16x32_bf16 a[124:127], a[244:247], a[176:179], a[124:127]\n\t"
"ds_read_b128 a[176:179], v242 offset:1024\n\t"
"v_mfma_f32_16x16x32_bf16 a[124:127], a[208:211], a[180:183], a[124:127]\n\t"
"v_mfma_f32_16x16x32_bf16 a[124:127], a[248:251], a[184:187], a[124:127]\n\t"
"buffer_load_dwordx4 a[216:219], v252, s[8:11], s20 offen offset:1024 sc1\n\t"
"v_mfma_f32_16x16x32_bf16 a[132:135], a[244:247], a[180:183], a[132:135]\n\t"
"ds_read_b128 a[180:183], v242 offset:1536\n\t"
"v_mfma_f32_16x16x32_bf16 a[132:135], a[208:211], a[184:187], a[132:135]\n\t"
"v_mfma_f32_16x16x32_bf16 a[132:135], a[248:251], a[188:191], a[132:135]\n\t"
"buffer_load_dwordx4 a[228:231], v252, s[8:11], s20 offen offset:2048 sc1\n\t"
"v_mfma_f32_16x16x32_bf16 a[140:143], a[244:247], a[184:187], a[140:143]\n\t"
"ds_read_b128 a[184:187], v242 offset:2048\n\t"
"v_mfma_f32_16x16x32_bf16 a[140:143], a[208:211], a[188:191], a[140:143]\n\t"
"v_mfma_f32_16x16x32_bf16 a[140:143], a[248:251], a[192:195], a[140:143]\n\t"
"s_mov_b32 s20, 1019904\n\t"
"buffer_load_dwordx4 a[232:235], v252, s[8:11], s20 offen offset:0 sc1\n\t"
"v_mfma_f32_16x16x32_bf16 a[148:151], a[244:247], a[188:191], a[148:151]\n\t"
"ds_read_b128 a[188:191], v242 offset:2560\n\t"
"v_mfma_f32_16x16x32_bf16 a[148:151], a[208:211], a[192:195], a[148:151]\n\t"
"v_mfma_f32_16x16x32_bf16 a[148:151], a[248:251], a[196:199], a[148:151]\n\t"
"buffer_load_dwordx4 a[236:239], v252, s[8:11], s20 offen offset:1024 sc1\n\t"
"v_mfma_f32_16x16x32_bf16 a[156:159], a[244:247], a[192:195], a[156:159]\n\t"
"ds_read_b128 a[192:195], v242 offset:3072\n\t"
"v_mfma_f32_16x16x32_bf16 a[156:159], a[208:211], a[196:199], a[156:159]\n\t"
"v_mfma_f32_16x16x32_bf16 a[156:159], a[248:251], a[200:203], a[156:159]\n\t"
"buffer_load_dwordx4 a[240:243], v252, s[8:11], s20 offen offset:2048 sc1\n\t"
"v_mfma_f32_16x16x32_bf16 a[164:167], a[244:247], a[196:199], a[164:167]\n\t"
"v_mfma_f32_16x16x32_bf16 a[164:167], a[208:211], a[200:203], a[164:167]\n\t"
"ds_read_b128 a[196:199], v242 offset:3584\n\t"
"v_mfma_f32_16x16x32_bf16 a[164:167], a[248:251], a[204:207], a[164:167]\n\t"
"ds_read_b128 a[200:203], v242 offset:4096\n\t"
"s_waitcnt vmcnt(7) lgkmcnt(6)\n\t"
"v_mfma_f32_16x16x32_bf16 v[194:197], a[220:223], a[168:171], v[194:197]\n\t"
"ds_read_b128 a[204:207], v242 offset:4608\n\t"
"v_mfma_f32_16x16x32_bf16 v[194:197], a[224:227], a[172:175], v[194:197]\n\t"
"v_mfma_f32_16x16x32_bf16 v[194:197], a[252:255], a[176:179], v[194:197]\n\t"
"s_mov_b32 s20, 1032192\n\t"
"buffer_load_dwordx4 a[208:211], v252, s[8:11], s20 offen offset:0 sc1\n\t"
"s_waitcnt lgkmcnt(6)\n\t"
"v_mfma_f32_16x16x32_bf16 v[202:205], a[220:223], a[172:175], v[202:205]\n\t"
"v_mfma_f32_16x16x32_bf16 v[202:205], a[224:227], a[176:179], v[202:205]\n\t"
"v_mfma_f32_16x16x32_bf16 v[202:205], a[252:255], a[180:183], v[202:205]\n\t"
"buffer_load_dwordx4 a[244:247], v252, s[8:11], s20 offen offset:1024 sc1\n\t"
"s_waitcnt lgkmcnt(5)\n\t"
"v_mfma_f32_16x16x32_bf16 v[210:213], a[220:223], a[176:179], v[210:213]\n\t"
"v_mfma_f32_16x16x32_bf16 v[210:213], a[224:227], a[180:183], v[210:213]\n\t"
"v_mfma_f32_16x16x32_bf16 v[210:213], a[252:255], a[184:187], v[210:213]\n\t"
"buffer_load_dwordx4 a[248:251], v252, s[8:11], s20 offen offset:2048 sc1\n\t"
"s_waitcnt lgkmcnt(4)\n\t"
"v_mfma_f32_16x16x32_bf16 a[0:3], a[220:223], a[180:183], a[0:3]\n\t"
"v_mfma_f32_16x16x32_bf16 a[0:3], a[224:227], a[184:187], a[0:3]\n\t"
"v_mfma_f32_16x16x32_bf16 a[0:3], a[252:255], a[188:191], a[0:3]\n\t"
"s_waitcnt lgkmcnt(3)\n\t"
"v_mfma_f32_16x16x32_bf16 a[8:11], a[220:223], a[184:187], a[8:11]\n\t"
"v_xor_b32 v244, 0xc0, v244\n\t"
"v_mfma_f32_16x16x32_bf16 a[8:11], a[224:227], a[188:191], a[8:11]\n\t"
"v_mfma_f32_16x16x32_bf16 a[8:11], a[252:255], a[192:195], a[8:11]\n\t"
"s_waitcnt lgkmcnt(2)\n\t"
"v_mfma_f32_16x16x32_bf16 a[16:19], a[220:223], a[188:191], a[16:19]\n\t"
"v_mfma_f32_16x16x32_bf16 a[16:19], a[224:227], a[192:195], a[16:19]\n\t"
"v_mfma_f32_16x16x32_bf16 a[16:19], a[252:255], a[196:199], a[16:19]\n\t"
"s_waitcnt lgkmcnt(1)\n\t"
"v_mfma_f32_16x16x32_bf16 a[24:27], a[220:223], a[192:195], a[24:27]\n\t"
"v_mfma_f32_16x16x32_bf16 a[24:27], a[224:227], a[196:199], a[24:27]\n\t"
"v_mfma_f32_16x16x32_bf16 a[24:27], a[252:255], a[200:203], a[24:27]\n\t"
"s_waitcnt lgkmcnt(0)\n\t"
"v_mfma_f32_16x16x32_bf16 a[32:35], a[220:223], a[196:199], a[32:35]\n\t"
"v_mfma_f32_16x16x32_bf16 a[32:35], a[224:227], a[200:203], a[32:35]\n\t"
"v_mfma_f32_16x16x32_bf16 a[32:35], a[252:255], a[204:207], a[32:35]\n\t"
"s_mov_b32 s20, 1044480\n\t"
"buffer_load_dwordx4 a[220:223], v252, s[8:11], s20 offen offset:0 sc1\n\t"
"s_waitcnt vmcnt(7)\n\t"
"v_mfma_f32_16x16x32_bf16 a[40:43], a[212:215], a[168:171], a[40:43]\n\t"
"v_mfma_f32_16x16x32_bf16 a[40:43], a[216:219], a[172:175], a[40:43]\n\t"
"v_mfma_f32_16x16x32_bf16 a[40:43], a[228:231], a[176:179], a[40:43]\n\t"
"buffer_load_dwordx4 a[224:227], v252, s[8:11], s20 offen offset:1024 sc1\n\t"
"v_mfma_f32_16x16x32_bf16 a[48:51], a[212:215], a[172:175], a[48:51]\n\t"
"ds_read_b64_tr_b16 v[230:231], v244 offset:512\n\t"
"v_mfma_f32_16x16x32_bf16 a[48:51], a[216:219], a[176:179], a[48:51]\n\t"
"ds_read_b64_tr_b16 v[232:233], v244 offset:3584\n\t"
"v_mfma_f32_16x16x32_bf16 a[48:51], a[228:231], a[180:183], a[48:51]\n\t"
"buffer_load_dwordx4 a[252:255], v252, s[8:11], s20 offen offset:2048 sc1\n\t"
"v_mfma_f32_16x16x32_bf16 a[56:59], a[212:215], a[176:179], a[56:59]\n\t"
"v_mfma_f32_16x16x32_bf16 a[56:59], a[216:219], a[180:183], a[56:59]\n\t"
"v_mfma_f32_16x16x32_bf16 a[56:59], a[228:231], a[184:187], a[56:59]\n\t"
"v_mfma_f32_16x16x32_bf16 a[64:67], a[212:215], a[180:183], a[64:67]\n\t"
"ds_read_b64_tr_b16 v[234:235], v244 offset:6656\n\t"
"v_mfma_f32_16x16x32_bf16 a[64:67], a[216:219], a[184:187], a[64:67]\n\t"
"ds_read_b64_tr_b16 v[236:237], v244 offset:9728\n\t"
"v_mfma_f32_16x16x32_bf16 a[64:67], a[228:231], a[188:191], a[64:67]\n\t"
"v_mfma_f32_16x16x32_bf16 a[72:75], a[212:215], a[184:187], a[72:75]\n\t"
"v_mfma_f32_16x16x32_bf16 a[72:75], a[216:219], a[188:191], a[72:75]\n\t"
"v_mfma_f32_16x16x32_bf16 a[72:75], a[228:231], a[192:195], a[72:75]\n\t"
"v_mfma_f32_16x16x32_bf16 a[80:83], a[212:215], a[188:191], a[80:83]\n\t"
"v_mfma_f32_16x16x32_bf16 a[80:83], a[216:219], a[192:195], a[80:83]\n\t"
"v_mfma_f32_16x16x32_bf16 a[80:83], a[228:231], a[196:199], a[80:83]\n\t"
"s_waitcnt lgkmcnt(2)\n\t"
"v_mfma_f32_16x16x32_bf16 v[230:233], v[230:233], v[218:221], 0\n\t"
"v_mfma_f32_16x16x32_bf16 a[88:91], a[212:215], a[192:195], a[88:91]\n\t"
"ds_read_b64_tr_b16 v[222:223], v244 offset:12800\n\t"
"v_mfma_f32_16x16x32_bf16 a[88:91], a[216:219], a[196:199], a[88:91]\n\t"
"ds_read_b64_tr_b16 v[224:225], v244 offset:15872\n\t"
"v_mfma_f32_16x16x32_bf16 a[88:91], a[228:231], a[200:203], a[88:91]\n\t"
"v_mfma_f32_16x16x32_bf16 a[96:99], a[212:215], a[196:199], a[96:99]\n\t"
"v_mfma_f32_16x16x32_bf16 a[96:99], a[216:219], a[200:203], a[96:99]\n\t"
"s_nop 0\n\t"
"v_cvt_pk_bf16_f32 v230, v230, v231\n\t"
"v_cvt_pk_bf16_f32 v231, v232, v233\n\t"
"v_mfma_f32_16x16x32_bf16 a[96:99], a[228:231], a[204:207], a[96:99]\n\t"
"s_waitcnt lgkmcnt(2)\n\t"
"v_mfma_f32_16x16x32_bf16 v[234:237], v[234:237], v[218:221], 0\n\t"
"s_mov_b32 s20, 1056768\n\t"
"buffer_load_dwordx4 a[212:215], v252, s[8:11], s20 offen offset:0 sc1\n\t"
"s_waitcnt vmcnt(7)\n\t"
"v_mfma_f32_16x16x32_bf16 a[104:107], a[232:235], a[168:171], a[104:107]\n\t"
"ds_read_b64_tr_b16 v[226:227], v244 offset:20480\n\t"
"v_mfma_f32_16x16x32_bf16 a[104:107], a[236:239], a[172:175], a[104:107]\n\t"
"ds_read_b64_tr_b16 v[228:229], v244 offset:23552\n\t"
"v_mfma_f32_16x16x32_bf16 a[104:107], a[240:243], a[176:179], a[104:107]\n\t"
"buffer_load_dwordx4 a[216:219], v252, s[8:11], s20 offen offset:1024 sc1\n\t"
"v_mfma_f32_16x16x32_bf16 a[112:115], a[232:235], a[172:175], a[112:115]\n\t"
"ds_read_b128 a[168:171], v242 offset:9216\n\t"
"v_mfma_f32_16x16x32_bf16 a[112:115], a[236:239], a[176:179], a[112:115]\n\t"
"v_cvt_pk_bf16_f32 v234, v234, v235\n\t"
"v_cvt_pk_bf16_f32 v235, v236, v237\n\t"
"v_mfma_f32_16x16x32_bf16 a[112:115], a[240:243], a[180:183], a[112:115]\n\t"
"buffer_load_dwordx4 a[228:231], v252, s[8:11], s20 offen offset:2048 sc1\n\t"
"v_mfma_f32_16x16x32_bf16 a[120:123], a[232:235], a[176:179], a[120:123]\n\t"
"ds_read_b128 a[172:175], v242 offset:9728\n\t"
"v_mfma_f32_16x16x32_bf16 a[120:123], a[236:239], a[180:183], a[120:123]\n\t"
"ds_read_b128 a[176:179], v242 offset:10240\n\t"
"v_mfma_f32_16x16x32_bf16 a[120:123], a[240:243], a[184:187], a[120:123]\n\t"
"s_waitcnt lgkmcnt(5)\n\t"
"v_mfma_f32_16x16x32_bf16 v[222:225], v[222:225], v[218:221], 0\n\t"
"v_mfma_f32_16x16x32_bf16 a[128:131], a[232:235], a[180:183], a[128:131]\n\t"
"ds_read_b128 a[180:183], v242 offset:10752\n\t"
"v_mfma_f32_16x16x32_bf16 a[128:131], a[236:239], a[184:187], a[128:131]\n\t"
"v_mfma_f32_16x16x32_bf16 a[128:131], a[240:243], a[188:191], a[128:131]\n\t"
"v_mad_u32_u24 v255, v252, 24, s50\n\t"
"s_add_u32 s28, s21, 7077504\n\t"
"s_mov_b32 exec_hi, 3\n\t"
"buffer_load_dword v255, v255, s[12:15], s28 offen offset:0\n\t"
"s_mov_b32 exec_hi, -1\n\t"
"v_mfma_f32_16x16x32_bf16 a[136:139], a[232:235], a[184:187], a[136:139]\n\t"
"ds_read_b128 a[184:187], v242 offset:11264\n\t"
"v_mfma_f32_16x16x32_bf16 a[136:139], a[236:239], a[188:191], a[136:139]\n\t"
"v_cvt_pk_bf16_f32 v222, v222, v223\n\t"
"v_mfma_f32_16x16x32_bf16 a[136:139], a[240:243], a[192:195], a[136:139]\n\t"
"v_cvt_pk_bf16_f32 v223, v224, v225\n\t"
"s_waitcnt lgkmcnt(5)\n\t"
"v_mfma_f32_16x16x32_bf16 v[226:229], v[226:229], v[218:221], 0\n\t"
"v_mfma_f32_16x16x32_bf16 a[144:147], a[232:235], a[188:191], a[144:147]\n\t"
"ds_read_b128 a[188:191], v242 offset:11776\n\t"
"v_mfma_f32_16x16x32_bf16 a[144:147], a[236:239], a[192:195], a[144:147]\n\t"
"v_mfma_f32_16x16x32_bf16 a[144:147], a[240:243], a[196:199], a[144:147]\n\t"
"v_mfma_f32_16x16x32_bf16 a[152:155], a[232:235], a[192:195], a[152:155]\n\t"
"ds_read_b128 a[192:195], v242 offset:12288\n\t"
"v_mfma_f32_16x16x32_bf16 a[152:155], a[236:239], a[196:199], a[152:155]\n\t"
"s_nop 0\n\t"
"v_cvt_pk_bf16_f32 v226, v226, v227\n\t"
"v_mfma_f32_16x16x32_bf16 a[152:155], a[240:243], a[200:203], a[152:155]\n\t"
"v_cvt_pk_bf16_f32 v227, v228, v229\n\t"
"v_mfma_f32_16x16x32_bf16 a[160:163], a[232:235], a[196:199], a[160:163]\n\t"
"ds_read_b128 a[196:199], v242 offset:12800\n\t"
"v_mfma_f32_16x16x32_bf16 a[160:163], a[236:239], a[200:203], a[160:163]\n\t"
"ds_read_b128 a[200:203], v242 offset:13312\n\t"
"v_mfma_f32_16x16x32_bf16 a[160:163], a[240:243], a[204:207], a[160:163]\n\t"
"s_mov_b32 s20, 921600\n\t"
"buffer_load_dwordx4 a[232:235], v252, s[8:11], s20 offen offset:0 sc1\n\t"
"s_waitcnt vmcnt(8) lgkmcnt(6)\n\t"
"v_mfma_f32_16x16x32_bf16 v[198:201], a[208:211], a[168:171], v[198:201]\n\t"
"ds_read_b128 a[204:207], v242 offset:13824\n\t"
"v_mfma_f32_16x16x32_bf16 v[198:201], a[244:247], a[172:175], v[198:201]\n\t"
"v_mfma_f32_16x16x32_bf16 v[198:201], a[248:251], a[176:179], v[198:201]\n\t"
"s_waitcnt lgkmcnt(6)\n\t"
"v_mfma_f32_16x16x32_bf16 v[206:209], a[208:211], a[172:175], v[206:209]\n\t"
"v_mfma_f32_16x16x32_bf16 v[206:209], a[244:247], a[176:179], v[206:209]\n\t"
"v_mfma_f32_16x16x32_bf16 v[206:209], a[248:251], a[180:183], v[206:209]\n\t"
"s_waitcnt lgkmcnt(0)\n\t"
"s_barrier\n\t"
"v_mfma_f32_16x16x32_bf16 v[214:217], a[208:211], a[176:179], v[214:217]\n\t"
"v_mfma_f32_16x16x32_bf16 v[214:217], a[244:247], a[180:183], v[214:217]\n\t"
"v_mfma_f32_16x16x32_bf16 v[214:217], a[248:251], a[184:187], v[214:217]\n\t"
"ds_write_b64 v243, v[230:231] offset:0\n\t"
"ds_write_b64 v243, v[234:235] offset:512\n\t"
"v_mfma_f32_16x16x32_bf16 a[4:7], a[208:211], a[180:183], a[4:7]\n\t"
"ds_read_b64_tr_b16 v[230:231], v244 offset:26624\n\t"
"v_mfma_f32_16x16x32_bf16 a[4:7], a[244:247], a[184:187], a[4:7]\n\t"
"ds_read_b64_tr_b16 v[232:233], v244 offset:29696\n\t"
"v_mfma_f32_16x16x32_bf16 a[4:7], a[248:251], a[188:191], a[4:7]\n\t"
"ds_write_b64 v243, v[222:223] offset:1024\n\t"
"v_mfma_f32_16x16x32_bf16 a[12:15], a[208:211], a[184:187], a[12:15]\n\t"
"ds_read_b64_tr_b16 v[234:235], v244 offset:32768\n\t"
"v_mfma_f32_16x16x32_bf16 a[12:15], a[244:247], a[188:191], a[12:15]\n\t"
"ds_read_b64_tr_b16 v[236:237], v244 offset:35840\n\t"
"v_mfma_f32_16x16x32_bf16 a[12:15], a[248:251], a[192:195], a[12:15]\n\t"
"ds_write_b64 v243, v[226:227] offset:1536\n\t"
"v_mfma_f32_16x16x32_bf16 a[20:23], a[208:211], a[188:191], a[20:23]\n\t"
"ds_read_b64_tr_b16 v[222:223], v244 offset:40448\n\t"
"v_mfma_f32_16x16x32_bf16 a[20:23], a[244:247], a[192:195], a[20:23]\n\t"
"ds_read_b64_tr_b16 v[224:225], v244 offset:43520\n\t"
"v_mfma_f32_16x16x32_bf16 a[20:23], a[248:251], a[196:199], a[20:23]\n\t"
"ds_read_b64_tr_b16 v[226:227], v244 offset:46592\n\t"
"ds_read_b64_tr_b16 v[228:229], v244 offset:49664\n\t"
"s_waitcnt lgkmcnt(8)\n\t"
"v_mfma_f32_16x16x32_bf16 v[230:233], v[230:233], v[218:221], 0\n\t"
"v_mfma_f32_16x16x32_bf16 a[28:31], a[208:211], a[192:195], a[28:31]\n\t"
"v_mfma_f32_16x16x32_bf16 a[28:31], a[244:247], a[196:199], a[28:31]\n\t"
"v_mfma_f32_16x16x32_bf16 a[28:31], a[248:251], a[200:203], a[28:31]\n\t"
"s_waitcnt lgkmcnt(5)\n\t"
"v_mfma_f32_16x16x32_bf16 v[234:237], v[234:237], v[218:221], 0\n\t"
"v_mfma_f32_16x16x32_bf16 a[36:39], a[208:211], a[196:199], a[36:39]\n\t"
"s_nop 1\n\t"
"v_cvt_pk_bf16_f32 v230, v230, v231\n\t"
"v_cvt_pk_bf16_f32 v231, v232, v233\n\t"
"v_mfma_f32_16x16x32_bf16 a[36:39], a[244:247], a[200:203], a[36:39]\n\t"
"v_mfma_f32_16x16x32_bf16 a[36:39], a[248:251], a[204:207], a[36:39]\n\t"
"ds_write_b64 v243, v[230:231] offset:2048\n\t"
"s_waitcnt lgkmcnt(3)\n\t"
"v_mfma_f32_16x16x32_bf16 v[222:225], v[222:225], v[218:221], 0\n\t"
"ds_read_b64_tr_b16 v[230:231], v244 offset:52736\n\t"
"s_waitcnt vmcnt(5)\n\t"
"v_mfma_f32_16x16x32_bf16 a[44:47], a[220:223], a[168:171], a[44:47]\n\t"
"v_cvt_pk_bf16_f32 v234, v234, v235\n\t"
"v_cvt_pk_bf16_f32 v235, v236, v237\n\t"
"v_mfma_f32_16x16x32_bf16 a[44:47], a[224:227], a[172:175], a[44:47]\n\t"
"ds_read_b64_tr_b16 v[232:233], v244 offset:55808\n\t"
"v_mfma_f32_16x16x32_bf16 a[44:47], a[252:255], a[176:179], a[44:47]\n\t"
"buffer_load_dwordx4 a[208:211], v252, s[8:11], s20 offen offset:1024 sc1\n\t"
"s_waitcnt lgkmcnt(3)\n\t"
"v_mfma_f32_16x16x32_bf16 v[226:229], v[226:229], v[218:221], 0\n\t"
"ds_write_b64 v243, v[234:235] offset:2560\n\t"
"v_mfma_f32_16x16x32_bf16 a[52:55], a[220:223], a[172:175], a[52:55]\n\t"
"v_cvt_pk_bf16_f32 v222, v222, v223\n\t"
"v_cvt_pk_bf16_f32 v223, v224, v225\n\t"
"v_mfma_f32_16x16x32_bf16 a[52:55], a[224:227], a[176:179], a[52:55]\n\t"
"v_mfma_f32_16x16x32_bf16 a[52:55], a[252:255], a[180:183], a[52:55]\n\t"
"buffer_load_dwordx4 a[236:239], v252, s[8:11], s20 offen offset:2048 sc1\n\t"
"v_mfma_f32_16x16x32_bf16 a[60:63], a[220:223], a[176:179], a[60:63]\n\t"
"v_mfma_f32_16x16x32_bf16 a[60:63], a[224:227], a[180:183], a[60:63]\n\t"
"v_cvt_pk_bf16_f32 v226, v226, v227\n\t"
"v_cvt_pk_bf16_f32 v227, v228, v229\n\t"
"v_mfma_f32_16x16x32_bf16 a[60:63], a[252:255], a[184:187], a[60:63]\n\t"
"ds_write_b64 v243, v[222:223] offset:3072\n\t"
"v_mfma_f32_16x16x32_bf16 a[68:71], a[220:223], a[180:183], a[68:71]\n\t"
"v_mfma_f32_16x16x32_bf16 a[68:71], a[224:227], a[184:187], a[68:71]\n\t"
"v_mfma_f32_16x16x32_bf16 a[68:71], a[252:255], a[188:191], a[68:71]\n\t"
"s_mov_b32 s20, 933888\n\t"
"buffer_load_dwordx4 a[240:243], v252, s[8:11], s20 offen offset:0 sc1\n\t"
"s_waitcnt lgkmcnt(2)\n\t"
"v_mfma_f32_16x16x32_bf16 v[230:233], v[230:233], v[218:221], 0\n\t"
"ds_write_b64 v243, v[226:227] offset:3584\n\t"
"v_mfma_f32_16x16x32_bf16 a[76:79], a[220:223], a[184:187], a[76:79]\n\t"
"v_mfma_f32_16x16x32_bf16 a[76:79], a[224:227], a[188:191], a[76:79]\n\t"
"v_mfma_f32_16x16x32_bf16 a[76:79], a[252:255], a[192:195], a[76:79]\n\t"
"buffer_load_dwordx4 a[244:247], v252, s[8:11], s20 offen offset:1024 sc1\n\t"
"v_mfma_f32_16x16x32_bf16 a[84:87], a[220:223], a[188:191], a[84:87]\n\t"
"v_mfma_f32_16x16x32_bf16 a[84:87], a[224:227], a[192:195], a[84:87]\n\t"
"s_nop 0\n\t"
"v_cvt_pk_bf16_f32 v230, v230, v231\n\t"
"v_cvt_pk_bf16_f32 v231, v232, v233\n\t"
"v_mfma_f32_16x16x32_bf16 a[84:87], a[252:255], a[196:199], a[84:87]\n\t"
"buffer_load_dwordx4 a[248:251], v252, s[8:11], s20 offen offset:2048 sc1\n\t"
"v_mfma_f32_16x16x32_bf16 a[92:95], a[220:223], a[192:195], a[92:95]\n\t"
"v_mfma_f32_16x16x32_bf16 a[92:95], a[224:227], a[196:199], a[92:95]\n\t"
"v_mfma_f32_16x16x32_bf16 a[92:95], a[252:255], a[200:203], a[92:95]\n\t"
"ds_write_b64 v243, v[230:231] offset:4096\n\t"
"v_mfma_f32_16x16x32_bf16 a[100:103], a[220:223], a[196:199], a[100:103]\n\t"
"v_mfma_f32_16x16x32_bf16 a[100:103], a[224:227], a[200:203], a[100:103]\n\t"
"v_mfma_f32_16x16x32_bf16 a[100:103], a[252:255], a[204:207], a[100:103]\n\t"
"s_mov_b32 s20, 946176\n\t"
"buffer_load_dwordx4 a[220:223], v252, s[8:11], s20 offen offset:0 sc1\n\t"
"s_waitcnt vmcnt(8)\n\t"
"v_mfma_f32_16x16x32_bf16 a[108:111], a[212:215], a[168:171], a[108:111]\n\t"
"v_mfma_f32_16x16x32_bf16 a[108:111], a[216:219], a[172:175], a[108:111]\n\t"
"v_mfma_f32_16x16x32_bf16 a[108:111], a[228:231], a[176:179], a[108:111]\n\t"
"buffer_load_dwordx4 a[224:227], v252, s[8:11], s20 offen offset:1024 sc1\n\t"
"s_waitcnt lgkmcnt(0)\n\t"
"s_barrier\n\t"
"v_mfma_f32_16x16x32_bf16 a[116:119], a[212:215], a[172:175], a[116:119]\n\t"
"ds_read_b128 a[168:171], v242 offset:0\n\t"
"v_mfma_f32_16x16x32_bf16 a[116:119], a[216:219], a[176:179], a[116:119]\n\t"
"ds_read_b128 a[172:175], v242 offset:512\n\t"
"v_mfma_f32_16x16x32_bf16 a[116:119], a[228:231], a[180:183], a[116:119]\n\t"
"buffer_load_dwordx4 a[252:255], v252, s[8:11], s20 offen offset:2048 sc1\n\t"
"v_mfma_f32_16x16x32_bf16 a[124:127], a[212:215], a[176:179], a[124:127]\n\t"
"ds_read_b128 a[176:179], v242 offset:1024\n\t"
"v_mfma_f32_16x16x32_bf16 a[124:127], a[216:219], a[180:183], a[124:127]\n\t"
"v_mfma_f32_16x16x32_bf16 a[124:127], a[228:231], a[184:187], a[124:127]\n\t"
"v_mfma_f32_16x16x32_bf16 a[132:135], a[212:215], a[180:183], a[132:135]\n\t"
"ds_read_b128 a[180:183], v242 offset:1536\n\t"
"v_mfma_f32_16x16x32_bf16 a[132:135], a[216:219], a[184:187], a[132:135]\n\t"
"v_mfma_f32_16x16x32_bf16 a[132:135], a[228:231], a[188:191], a[132:135]\n\t"
"v_mfma_f32_16x16x32_bf16 a[140:143], a[212:215], a[184:187], a[140:143]\n\t"
"ds_read_b128 a[184:187], v242 offset:2048\n\t"
"v_mfma_f32_16x16x32_bf16 a[140:143], a[216:219], a[188:191], a[140:143]\n\t"
"v_mfma_f32_16x16x32_bf16 a[140:143], a[228:231], a[192:195], a[140:143]\n\t"
"v_mfma_f32_16x16x32_bf16 a[148:151], a[212:215], a[188:191], a[148:151]\n\t"
"ds_read_b128 a[188:191], v242 offset:2560\n\t"
"v_mfma_f32_16x16x32_bf16 a[148:151], a[216:219], a[192:195], a[148:151]\n\t"
"v_mfma_f32_16x16x32_bf16 a[148:151], a[228:231], a[196:199], a[148:151]\n\t"
"v_mfma_f32_16x16x32_bf16 a[156:159], a[212:215], a[192:195], a[156:159]\n\t"
"ds_read_b128 a[192:195], v242 offset:3072\n\t"
"v_mfma_f32_16x16x32_bf16 a[156:159], a[216:219], a[196:199], a[156:159]\n\t"
"v_mfma_f32_16x16x32_bf16 a[156:159], a[228:231], a[200:203], a[156:159]\n\t"
"v_mfma_f32_16x16x32_bf16 a[164:167], a[212:215], a[196:199], a[164:167]\n\t"
"ds_read_b128 a[196:199], v242 offset:3584\n\t"
"v_mfma_f32_16x16x32_bf16 a[164:167], a[216:219], a[200:203], a[164:167]\n\t"
"ds_read_b128 a[200:203], v242 offset:4096\n\t"
"v_mfma_f32_16x16x32_bf16 a[164:167], a[228:231], a[204:207], a[164:167]\n\t"
"s_mov_b32 s20, 958464\n\t"
"buffer_load_dwordx4 a[212:215], v252, s[8:11], s20 offen offset:0 sc1\n\t"
"s_waitcnt vmcnt(7) lgkmcnt(6)\n\t"
"v_mfma_f32_16x16x32_bf16 v[0:3], a[232:235], a[168:171], v[0:3]\n\t"
"ds_read_b128 a[204:207], v242 offset:4608\n\t"
"v_mfma_f32_16x16x32_bf16 v[0:3], a[208:211], a[172:175], v[0:3]\n\t"
"v_mfma_f32_16x16x32_bf16 v[0:3], a[236:239], a[176:179], v[0:3]\n\t"
"buffer_load_dwordx4 a[216:219], v252, s[8:11], s20 offen offset:1024 sc1\n\t"
"s_waitcnt lgkmcnt(6)\n\t"
"v_mfma_f32_16x16x32_bf16 v[4:7], a[232:235], a[172:175], v[4:7]\n\t"
"v_mfma_f32_16x16x32_bf16 v[4:7], a[208:211], a[176:179], v[4:7]\n\t"
"v_mfma_f32_16x16x32_bf16 v[4:7], a[236:239], a[180:183], v[4:7]\n\t"
"buffer_load_dwordx4 a[228:231], v252, s[8:11], s20 offen offset:2048 sc1\n\t"
"s_waitcnt lgkmcnt(5)\n\t"
"v_mfma_f32_16x16x32_bf16 v[8:11], a[232:235], a[176:179], v[8:11]\n\t"
"v_mfma_f32_16x16x32_bf16 v[8:11], a[208:211], a[180:183], v[8:11]\n\t"
"v_mfma_f32_16x16x32_bf16 v[8:11], a[236:239], a[184:187], v[8:11]\n\t"
"s_waitcnt lgkmcnt(4)\n\t"
"v_mfma_f32_16x16x32_bf16 v[12:15], a[232:235], a[180:183], v[12:15]\n\t"
"v_mfma_f32_16x16x32_bf16 v[12:15], a[208:211], a[184:187], v[12:15]\n\t"
"v_mfma_f32_16x16x32_bf16 v[12:15], a[236:239], a[188:191], v[12:15]\n\t"
"s_waitcnt lgkmcnt(3)\n\t"
"v_mfma_f32_16x16x32_bf16 v[16:19], a[232:235], a[184:187], v[16:19]\n\t"
"v_mfma_f32_16x16x32_bf16 v[16:19], a[208:211], a[188:191], v[16:19]\n\t"
"v_mfma_f32_16x16x32_bf16 v[16:19], a[236:239], a[192:195], v[16:19]\n\t"
"s_waitcnt lgkmcnt(2)\n\t"
"v_mfma_f32_16x16x32_bf16 v[20:23], a[232:235], a[188:191], v[20:23]\n\t"
"v_mfma_f32_16x16x32_bf16 v[20:23], a[208:211], a[192:195], v[20:23]\n\t"
"v_mfma_f32_16x16x32_bf16 v[20:23], a[236:239], a[196:199], v[20:23]\n\t"
"s_waitcnt lgkmcnt(1)\n\t"
"v_mfma_f32_16x16x32_bf16 v[24:27], a[232:235], a[192:195], v[24:27]\n\t"
"v_mfma_f32_16x16x32_bf16 v[24:27], a[208:211], a[196:199], v[24:27]\n\t"
"v_mfma_f32_16x16x32_bf16 v[24:27], a[236:239], a[200:203], v[24:27]\n\t"
"s_waitcnt lgkmcnt(0)\n\t"
"v_mfma_f32_16x16x32_bf16 v[28:31], a[232:235], a[196:199], v[28:31]\n\t"
"v_mfma_f32_16x16x32_bf16 v[28:31], a[208:211], a[200:203], v[28:31]\n\t"
"v_mfma_f32_16x16x32_bf16 v[28:31], a[236:239], a[204:207], v[28:31]\n\t"
"s_mov_b32 s20, 970752\n\t"
"buffer_load_dwordx4 a[232:235], v252, s[8:11], s20 offen offset:0 sc1\n\t"
"s_waitcnt vmcnt(7)\n\t"
"v_mfma_f32_16x16x32_bf16 v[32:35], a[240:243], a[168:171], v[32:35]\n\t"
"v_mfma_f32_16x16x32_bf16 v[32:35], a[244:247], a[172:175], v[32:35]\n\t"
"v_mfma_f32_16x16x32_bf16 v[32:35], a[248:251], a[176:179], v[32:35]\n\t"
"buffer_load_dwordx4 a[208:211], v252, s[8:11], s20 offen offset:1024 sc1\n\t"
"v_mfma_f32_16x16x32_bf16 v[36:39], a[240:243], a[172:175], v[36:39]\n\t"
"ds_read_b64_tr_b16 v[234:235], v244 offset:1024\n\t"
"v_mfma_f32_16x16x32_bf16 v[36:39], a[244:247], a[176:179], v[36:39]\n\t"
"ds_read_b64_tr_b16 v[236:237], v244 offset:4096\n\t"
"v_mfma_f32_16x16x32_bf16 v[36:39], a[248:251], a[180:183], v[36:39]\n\t"
"v_mad_u32_u24 v255, v252, 24, s50\n\t"
"s_add_u32 s28, s21, 7863936\n\t"
"s_mov_b32 exec_hi, 3\n\t"
"buffer_load_dword v255, v255, s[12:15], s28 offen offset:0\n\t"
"s_mov_b32 exec_hi, -1\n\t"
"v_mfma_f32_16x16x32_bf16 v[40:43], a[240:243], a[176:179], v[40:43]\n\t"
"v_mfma_f32_16x16x32_bf16 v[40:43], a[244:247], a[180:183], v[40:43]\n\t"
"v_mfma_f32_16x16x32_bf16 v[40:43], a[248:251], a[184:187], v[40:43]\n\t"
"buffer_load_dwordx4 a[236:239], v252, s[8:11], s20 offen offset:2048 sc1\n\t"
"v_mfma_f32_16x16x32_bf16 v[44:47], a[240:243], a[180:183], v[44:47]\n\t"
"ds_read_b64_tr_b16 v[222:223], v244 offset:7168\n\t"
"v_mfma_f32_16x16x32_bf16 v[44:47], a[244:247], a[184:187], v[44:47]\n\t"
"ds_read_b64_tr_b16 v[224:225], v244 offset:10240\n\t"
"v_mfma_f32_16x16x32_bf16 v[44:47], a[248:251], a[188:191], v[44:47]\n\t"
"v_mfma_f32_16x16x32_bf16 v[48:51], a[240:243], a[184:187], v[48:51]\n\t"
"v_mfma_f32_16x16x32_bf16 v[48:51], a[244:247], a[188:191], v[48:51]\n\t"
"v_mfma_f32_16x16x32_bf16 v[48:51], a[248:251], a[192:195], v[48:51]\n\t"
"v_mfma_f32_16x16x32_bf16 v[52:55], a[240:243], a[188:191], v[52:55]\n\t"
"v_mfma_f32_16x16x32_bf16 v[52:55], a[244:247], a[192:195], v[52:55]\n\t"
"v_mfma_f32_16x16x32_bf16 v[52:55], a[248:251], a[196:199], v[52:55]\n\t"
"s_waitcnt lgkmcnt(2)\n\t"
"v_mfma_f32_16x16x32_bf16 v[234:237], v[234:237], v[218:221], 0\n\t"
"v_mfma_f32_16x16x32_bf16 v[56:59], a[240:243], a[192:195], v[56:59]\n\t"
"ds_read_b64_tr_b16 v[226:227], v244 offset:13312\n\t"
"v_mfma_f32_16x16x32_bf16 v[56:59], a[244:247], a[196:199], v[56:59]\n\t"
"ds_read_b64_tr_b16 v[228:229], v244 offset:16384\n\t"
"v_mfma_f32_16x16x32_bf16 v[56:59], a[248:251], a[200:203], v[56:59]\n\t"
"v_mfma_f32_16x16x32_bf16 v[60:63], a[240:243], a[196:199], v[60:63]\n\t"
"v_mfma_f32_16x16x32_bf16 v[60:63], a[244:247], a[200:203], v[60:63]\n\t"
"s_nop 0\n\t"
"v_cvt_pk_bf16_f32 v234, v234, v235\n\t"
"v_cvt_pk_bf16_f32 v235, v236, v237\n\t"
"v_mfma_f32_16x16x32_bf16 v[60:63], a[248:251], a[204:207], v[60:63]\n\t"
"s_waitcnt lgkmcnt(2)\n\t"
"v_mfma_f32_16x16x32_bf16 v[222:225], v[222:225], v[218:221], 0\n\t"
"s_mov_b32 s20, 983040\n\t"
"buffer_load_dwordx4 a[240:243], v252, s[8:11], s20 offen offset:0 sc1\n\t"
"s_waitcnt vmcnt(8)\n\t"
"v_mfma_f32_16x16x32_bf16 v[64:67], a[220:223], a[168:171], v[64:67]\n\t"
"ds_read_b64_tr_b16 v[230:231], v244 offset:20992\n\t"
"v_mfma_f32_16x16x32_bf16 v[64:67], a[224:227], a[172:175], v[64:67]\n\t"
"ds_read_b64_tr_b16 v[232:233], v244 offset:24064\n\t"
"v_mfma_f32_16x16x32_bf16 v[64:67], a[252:255], a[176:179], v[64:67]\n\t"
"v_mfma_f32_16x16x32_bf16 v[68:71], a[220:223], a[172:175], v[68:71]\n\t"
"ds_read_b128 a[168:171], v242 offset:9216\n\t"
"v_mfma_f32_16x16x32_bf16 v[68:71], a[224:227], a[176:179], v[68:71]\n\t"
"v_cvt_pk_bf16_f32 v222, v222, v223\n\t"
"v_mfma_f32_16x16x32_bf16 v[68:71], a[252:255], a[180:183], v[68:71]\n\t"
"v_cvt_pk_bf16_f32 v223, v224, v225\n\t"
"v_mfma_f32_16x16x32_bf16 v[72:75], a[220:223], a[176:179], v[72:75]\n\t"
"ds_read_b128 a[172:175], v242 offset:9728\n\t"
"v_mfma_f32_16x16x32_bf16 v[72:75], a[224:227], a[180:183], v[72:75]\n\t"
"ds_read_b128 a[176:179], v242 offset:10240\n\t"
"v_mfma_f32_16x16x32_bf16 v[72:75], a[252:255], a[184:187], v[72:75]\n\t"
"s_waitcnt lgkmcnt(5)\n\t"
"v_mfma_f32_16x16x32_bf16 v[226:229], v[226:229], v[218:221], 0\n\t"
"v_mfma_f32_16x16x32_bf16 v[76:79], a[220:223], a[180:183], v[76:79]\n\t"
"ds_read_b128 a[180:183], v242 offset:10752\n\t"
"v_mfma_f32_16x16x32_bf16 v[76:79], a[224:227], a[184:187], v[76:79]\n\t"
"v_mfma_f32_16x16x32_bf16 v[76:79], a[252:255], a[188:191], v[76:79]\n\t"
"v_mfma_f32_16x16x32_bf16 v[80:83], a[220:223], a[184:187], v[80:83]\n\t"
"ds_read_b128 a[184:187], v242 offset:11264\n\t"
"v_mfma_f32_16x16x32_bf16 v[80:83], a[224:227], a[188:191], v[80:83]\n\t"
"s_nop 0\n\t"
"v_cvt_pk_bf16_f32 v226, v226, v227\n\t"
"v_mfma_f32_16x16x32_bf16 v[80:83], a[252:255], a[192:195], v[80:83]\n\t"
"v_cvt_pk_bf16_f32 v227, v228, v229\n\t"
"s_waitcnt lgkmcnt(5)\n\t"
"v_mfma_f32_16x16x32_bf16 v[230:233], v[230:233], v[218:221], 0\n\t"
"v_mfma_f32_16x16x32_bf16 v[84:87], a[220:223], a[188:191], v[84:87]\n\t"
"ds_read_b128 a[188:191], v242 offset:11776\n\t"
"v_mfma_f32_16x16x32_bf16 v[84:87], a[224:227], a[192:195], v[84:87]\n\t"
"v_mfma_f32_16x16x32_bf16 v[84:87], a[252:255], a[196:199], v[84:87]\n\t"
"v_mfma_f32_16x16x32_bf16 v[88:91], a[220:223], a[192:195], v[88:91]\n\t"
"ds_read_b128 a[192:195], v242 offset:12288\n\t"
"v_mfma_f32_16x16x32_bf16 v[88:91], a[224:227], a[196:199], v[88:91]\n\t"
"s_nop 0\n\t"
"v_cvt_pk_bf16_f32 v230, v230, v231\n\t"
"v_mfma_f32_16x16x32_bf16 v[88:91], a[252:255], a[200:203], v[88:91]\n\t"
"v_cvt_pk_bf16_f32 v231, v232, v233\n\t"
"v_mfma_f32_16x16x32_bf16 v[92:95], a[220:223], a[196:199], v[92:95]\n\t"
"ds_read_b128 a[196:199], v242 offset:12800\n\t"
"v_mfma_f32_16x16x32_bf16 v[92:95], a[224:227], a[200:203], v[92:95]\n\t"
"ds_read_b128 a[200:203], v242 offset:13312\n\t"
"v_mfma_f32_16x16x32_bf16 v[92:95], a[252:255], a[204:207], v[92:95]\n\t"
"ds_read_b128 a[204:207], v242 offset:13824\n\t"
"s_waitcnt vmcnt(5) lgkmcnt(7)\n\t"
"v_mfma_f32_16x16x32_bf16 v[98:101], a[212:215], a[168:171], v[98:101]\n\t"
"v_mfma_f32_16x16x32_bf16 v[98:101], a[216:219], a[172:175], v[98:101]\n\t"
"v_mfma_f32_16x16x32_bf16 v[98:101], a[228:231], a[176:179], v[98:101]\n\t"
"s_waitcnt lgkmcnt(6)\n\t"
"v_mfma_f32_16x16x32_bf16 v[102:105], a[212:215], a[172:175], v[102:105]\n\t"
"v_mfma_f32_16x16x32_bf16 v[102:105], a[216:219], a[176:179], v[102:105]\n\t"
"v_mfma_f32_16x16x32_bf16 v[102:105], a[228:231], a[180:183], v[102:105]\n\t"
"buffer_load_dwordx4 a[220:223], v252, s[8:11], s20 offen offset:1024 sc1\n\t"
"s_waitcnt lgkmcnt(0)\n\t"
"s_barrier\n\t"
"v_mfma_f32_16x16x32_bf16 v[106:109], a[212:215], a[176:179], v[106:109]\n\t"
"v_mfma_f32_16x16x32_bf16 v[106:109], a[216:219], a[180:183], v[106:109]\n\t"
"v_mfma_f32_16x16x32_bf16 v[106:109], a[228:231], a[184:187], v[106:109]\n\t"
"ds_write_b64 v243, v[234:235] offset:0\n\t"
"buffer_load_dwordx4 a[224:227], v252, s[8:11], s20 offen offset:2048 sc1\n\t"
"v_mfma_f32_16x16x32_bf16 v[110:113], a[212:215], a[180:183], v[110:113]\n\t"
"ds_read_b64_tr_b16 v[234:235], v244 offset:27136\n\t"
"v_mfma_f32_16x16x32_bf16 v[110:113], a[216:219], a[184:187], v[110:113]\n\t"
"ds_read_b64_tr_b16 v[236:237], v244 offset:30208\n\t"
"v_mfma_f32_16x16x32_bf16 v[110:113], a[228:231], a[188:191], v[110:113]\n\t"
"ds_write_b64 v243, v[222:223] offset:512\n\t"
"ds_write_b64 v243, v[226:227] offset:1024\n\t"
"v_mfma_f32_16x16x32_bf16 v[114:117], a[212:215], a[184:187], v[114:117]\n\t"
"ds_read_b64_tr_b16 v[222:223], v244 offset:33280\n\t"
"v_mfma_f32_16x16x32_bf16 v[114:117], a[216:219], a[188:191], v[114:117]\n\t"
"ds_read_b64_tr_b16 v[224:225], v244 offset:36352\n\t"
"v_mfma_f32_16x16x32_bf16 v[114:117], a[228:231], a[192:195], v[114:117]\n\t"
"s_mov_b32 s20, 1142784\n\t"
"buffer_load_dwordx4 a[244:247], v252, s[8:11], s20 offen offset:0 sc1\n\t"
"ds_write_b64 v243, v[230:231] offset:1536\n\t"
"v_mfma_f32_16x16x32_bf16 v[118:121], a[212:215], a[188:191], v[118:121]\n\t"
"ds_read_b64_tr_b16 v[226:227], v244 offset:40960\n\t"
"v_mfma_f32_16x16x32_bf16 v[118:121], a[216:219], a[192:195], v[118:121]\n\t"
"ds_read_b64_tr_b16 v[228:229], v244 offset:44032\n\t"
"v_mfma_f32_16x16x32_bf16 v[118:121], a[228:231], a[196:199], v[118:121]\n\t"
"buffer_load_dwordx4 a[248:251], v252, s[8:11], s20 offen offset:1024 sc1\n\t"
"s_waitcnt lgkmcnt(7)\n\t"
"v_mfma_f32_16x16x32_bf16 v[234:237], v[234:237], v[218:221], 0\n\t"
"buffer_load_dwordx4 a[252:255], v252, s[8:11], s20 offen offset:2048 sc1\n\t"
"v_mfma_f32_16x16x32_bf16 v[122:125], a[212:215], a[192:195], v[122:125]\n\t"
"ds_read_b64_tr_b16 v[230:231], v244 offset:47104\n\t"
"v_mfma_f32_16x16x32_bf16 v[122:125], a[216:219], a[196:199], v[122:125]\n\t"
"ds_read_b64_tr_b16 v[232:233], v244 offset:50176\n\t"
"v_mfma_f32_16x16x32_bf16 v[122:125], a[228:231], a[200:203], v[122:125]\n\t"
"s_waitcnt lgkmcnt(5)\n\t"
"v_mfma_f32_16x16x32_bf16 v[222:225], v[222:225], v[218:221], 0\n\t"
"v_mfma_f32_16x16x32_bf16 v[126:129], a[212:215], a[196:199], v[126:129]\n\t"
"v_cvt_pk_bf16_f32 v234, v234, v235\n\t"
"v_cvt_pk_bf16_f32 v235, v236, v237\n\t"
"v_mfma_f32_16x16x32_bf16 v[126:129], a[216:219], a[200:203], v[126:129]\n\t"
"v_mfma_f32_16x16x32_bf16 v[126:129], a[228:231], a[204:207], v[126:129]\n\t"
"s_mov_b32 s20, 1155072\n\t"
"buffer_load_dwordx4 a[212:215], v252, s[8:11], s20 offen offset:0 sc1\n\t"
"s_waitcnt lgkmcnt(2)\n\t"
"v_mfma_f32_16x16x32_bf16 v[226:229], v[226:229], v[218:221], 0\n\t"
"buffer_load_dwordx4 a[216:219], v252, s[8:11], s20 offen offset:1024 sc1\n\t"
"s_waitcnt vmcnt(8)\n\t"
"v_mfma_f32_16x16x32_bf16 v[130:133], a[232:235], a[168:171], v[130:133]\n\t"
"v_cvt_pk_bf16_f32 v222, v222, v223\n\t"
"v_cvt_pk_bf16_f32 v223, v224, v225\n\t"
"v_mfma_f32_16x16x32_bf16 v[130:133], a[208:211], a[172:175], v[130:133]\n\t"
"v_mfma_f32_16x16x32_bf16 v[130:133], a[236:239], a[176:179], v[130:133]\n\t"
"ds_write_b64 v243, v[234:235] offset:2048\n\t"
"s_waitcnt lgkmcnt(1)\n\t"
"v_mfma_f32_16x16x32_bf16 v[230:233], v[230:233], v[218:221], 0\n\t"
"ds_read_b64_tr_b16 v[234:235], v244 offset:53248\n\t"
"ds_read_b64_tr_b16 v[236:237], v244 offset:56320\n\t"
"v_mfma_f32_16x16x32_bf16 v[134:137], a[232:235], a[172:175], v[134:137]\n\t"
"v_cvt_pk_bf16_f32 v226, v226, v227\n\t"
"v_cvt_pk_bf16_f32 v227, v228, v229\n\t"
"v_mfma_f32_16x16x32_bf16 v[134:137], a[208:211], a[176:179], v[134:137]\n\t"
"v_mfma_f32_16x16x32_bf16 v[134:137], a[236:239], a[180:183], v[134:137]\n\t"
"buffer_load_dwordx4 a[228:231], v252, s[8:11], s20 offen offset:2048 sc1\n\t"
"v_mfma_f32_16x16x32_bf16 v[138:141], a[232:235], a[176:179], v[138:141]\n\t"
"v_mfma_f32_16x16x32_bf16 v[138:141], a[208:211], a[180:183], v[138:141]\n\t"
"v_cvt_pk_bf16_f32 v230, v230, v231\n\t"
"v_cvt_pk_bf16_f32 v231, v232, v233\n\t"
"v_mfma_f32_16x16x32_bf16 v[138:141], a[236:239], a[184:187], v[138:141]\n\t"
"ds_write_b64 v243, v[222:223] offset:2560\n\t"
"v_mfma_f32_16x16x32_bf16 v[142:145], a[232:235], a[180:183], v[142:145]\n\t"
"v_mfma_f32_16x16x32_bf16 v[142:145], a[208:211], a[184:187], v[142:145]\n\t"
"v_mfma_f32_16x16x32_bf16 v[142:145], a[236:239], a[188:191], v[142:145]\n\t"
"ds_write_b64 v243, v[226:227] offset:3072\n\t"
"s_waitcnt lgkmcnt(2)\n\t"
"v_mfma_f32_16x16x32_bf16 v[234:237], v[234:237], v[218:221], 0\n\t"
"ds_write_b64 v243, v[230:231] offset:3584\n\t"
"v_mfma_f32_16x16x32_bf16 v[146:149], a[232:235], a[184:187], v[146:149]\n\t"
"v_mfma_f32_16x16x32_bf16 v[146:149], a[208:211], a[188:191], v[146:149]\n\t"
"v_mfma_f32_16x16x32_bf16 v[146:149], a[236:239], a[192:195], v[146:149]\n\t"
"v_mfma_f32_16x16x32_bf16 v[150:153], a[232:235], a[188:191], v[150:153]\n\t"
"v_mfma_f32_16x16x32_bf16 v[150:153], a[208:211], a[192:195], v[150:153]\n\t"
"s_nop 1\n\t"
"v_cvt_pk_bf16_f32 v234, v234, v235\n\t"
"v_cvt_pk_bf16_f32 v235, v236, v237\n\t"
"v_mfma_f32_16x16x32_bf16 v[150:153], a[236:239], a[196:199], v[150:153]\n\t"
"v_mfma_f32_16x16x32_bf16 v[154:157], a[232:235], a[192:195], v[154:157]\n\t"
"v_mfma_f32_16x16x32_bf16 v[154:157], a[208:211], a[196:199], v[154:157]\n\t"
"v_mfma_f32_16x16x32_bf16 v[154:157], a[236:239], a[200:203], v[154:157]\n\t"
"ds_write_b64 v243, v[234:235] offset:4096\n\t"
"v_mfma_f32_16x16x32_bf16 v[158:161], a[232:235], a[196:199], v[158:161]\n\t"
"v_mfma_f32_16x16x32_bf16 v[158:161], a[208:211], a[200:203], v[158:161]\n\t"
"v_mfma_f32_16x16x32_bf16 v[158:161], a[236:239], a[204:207], v[158:161]\n\t"
"s_mov_b32 s20, 1167360\n\t"
"buffer_load_dwordx4 a[232:235], v252, s[8:11], s20 offen offset:0 sc1\n\t"
"s_waitcnt vmcnt(7)\n\t"
"v_mfma_f32_16x16x32_bf16 v[162:165], a[240:243], a[168:171], v[162:165]\n\t"
"v_mfma_f32_16x16x32_bf16 v[162:165], a[220:223], a[172:175], v[162:165]\n\t"
"v_mfma_f32_16x16x32_bf16 v[162:165], a[224:227], a[176:179], v[162:165]\n\t"
"buffer_load_dwordx4 a[208:211], v252, s[8:11], s20 offen offset:1024 sc1\n\t"
"s_waitcnt lgkmcnt(0)\n\t"
"s_barrier\n\t"
"v_mfma_f32_16x16x32_bf16 v[166:169], a[240:243], a[172:175], v[166:169]\n\t"
"ds_read_b128 a[168:171], v242 offset:0\n\t"
"v_mfma_f32_16x16x32_bf16 v[166:169], a[220:223], a[176:179], v[166:169]\n\t"
"ds_read_b128 a[172:175], v242 offset:512\n\t"
"v_mfma_f32_16x16x32_bf16 v[166:169], a[224:227], a[180:183], v[166:169]\n\t"
"buffer_load_dwordx4 a[236:239], v252, s[8:11], s20 offen offset:2048 sc1\n\t"
"v_mfma_f32_16x16x32_bf16 v[170:173], a[240:243], a[176:179], v[170:173]\n\t"
"ds_read_b128 a[176:179], v242 offset:1024\n\t"
"v_mfma_f32_16x16x32_bf16 v[170:173], a[220:223], a[180:183], v[170:173]\n\t"
"v_mfma_f32_16x16x32_bf16 v[170:173], a[224:227], a[184:187], v[170:173]\n\t"
"v_mfma_f32_16x16x32_bf16 v[174:177], a[240:243], a[180:183], v[174:177]\n\t"
"ds_read_b128 a[180:183], v242 offset:1536\n\t"
"v_mfma_f32_16x16x32_bf16 v[174:177], a[220:223], a[184:187], v[174:177]\n\t"
"v_mfma_f32_16x16x32_bf16 v[174:177], a[224:227], a[188:191], v[174:177]\n\t"
"v_mfma_f32_16x16x32_bf16 v[178:181], a[240:243], a[184:187], v[178:181]\n\t"
"ds_read_b128 a[184:187], v242 offset:2048\n\t"
"v_mfma_f32_16x16x32_bf16 v[178:181], a[220:223], a[188:191], v[178:181]\n\t"
"v_mfma_f32_16x16x32_bf16 v[178:181], a[224:227], a[192:195], v[178:181]\n\t"
"v_mfma_f32_16x16x32_bf16 v[182:185], a[240:243], a[188:191], v[182:185]\n\t"
"ds_read_b128 a[188:191], v242 offset:2560\n\t"
"v_mfma_f32_16x16x32_bf16 v[182:185], a[220:223], a[192:195], v[182:185]\n\t"
"v_mfma_f32_16x16x32_bf16 v[182:185], a[224:227], a[196:199], v[182:185]\n\t"
"v_mfma_f32_16x16x32_bf16 v[186:189], a[240:243], a[192:195], v[186:189]\n\t"
"ds_read_b128 a[192:195], v242 offset:3072\n\t"
"v_mfma_f32_16x16x32_bf16 v[186:189], a[220:223], a[196:199], v[186:189]\n\t"
"v_mfma_f32_16x16x32_bf16 v[186:189], a[224:227], a[200:203], v[186:189]\n\t"
"v_mfma_f32_16x16x32_bf16 v[190:193], a[240:243], a[196:199], v[190:193]\n\t"
"ds_read_b128 a[196:199], v242 offset:3584\n\t"
"v_mfma_f32_16x16x32_bf16 v[190:193], a[220:223], a[200:203], v[190:193]\n\t"
"ds_read_b128 a[200:203], v242 offset:4096\n\t"
"v_mfma_f32_16x16x32_bf16 v[190:193], a[224:227], a[204:207], v[190:193]\n\t"
"s_mov_b32 s20, 1179648\n\t"
"buffer_load_dwordx4 a[240:243], v252, s[8:11], s20 offen offset:0 sc1\n\t"
"s_waitcnt vmcnt(7) lgkmcnt(6)\n\t"
"v_mfma_f32_16x16x32_bf16 v[0:3], a[244:247], a[168:171], v[0:3]\n\t"
"ds_read_b128 a[204:207], v242 offset:4608\n\t"
"v_mfma_f32_16x16x32_bf16 v[0:3], a[248:251], a[172:175], v[0:3]\n\t"
"v_mfma_f32_16x16x32_bf16 v[0:3], a[252:255], a[176:179], v[0:3]\n\t"
"v_mad_u32_u24 v255, v252, 24, s50\n\t"
"s_add_u32 s28, s21, 8650368\n\t"
"s_mov_b32 exec_hi, 3\n\t"
"buffer_load_dword v255, v255, s[12:15], s28 offen offset:0\n\t"
"s_mov_b32 exec_hi, -1\n\t"
"s_waitcnt lgkmcnt(6)\n\t"
"v_mfma_f32_16x16x32_bf16 v[4:7], a[244:247], a[172:175], v[4:7]\n\t"
"v_mfma_f32_16x16x32_bf16 v[4:7], a[248:251], a[176:179], v[4:7]\n\t"
"v_mfma_f32_16x16x32_bf16 v[4:7], a[252:255], a[180:183], v[4:7]\n\t"
"s_waitcnt lgkmcnt(5)\n\t"
"v_mfma_f32_16x16x32_bf16 v[8:11], a[244:247], a[176:179], v[8:11]\n\t"
"v_mfma_f32_16x16x32_bf16 v[8:11], a[248:251], a[180:183], v[8:11]\n\t"
"v_mfma_f32_16x16x32_bf16 v[8:11], a[252:255], a[184:187], v[8:11]\n\t"
"s_waitcnt lgkmcnt(4)\n\t"
"v_mfma_f32_16x16x32_bf16 v[12:15], a[244:247], a[180:183], v[12:15]\n\t"
"v_mfma_f32_16x16x32_bf16 v[12:15], a[248:251], a[184:187], v[12:15]\n\t"
"v_mfma_f32_16x16x32_bf16 v[12:15], a[252:255], a[188:191], v[12:15]\n\t"
"s_waitcnt lgkmcnt(3)\n\t"
"v_mfma_f32_16x16x32_bf16 v[16:19], a[244:247], a[184:187], v[16:19]\n\t"
"v_xor_b32 v244, 0xc0, v244\n\t"
"v_mfma_f32_16x16x32_bf16 v[16:19], a[248:251], a[188:191], v[16:19]\n\t"
"v_mfma_f32_16x16x32_bf16 v[16:19], a[252:255], a[192:195], v[16:19]\n\t"
"buffer_load_dwordx4 a[220:223], v252, s[8:11], s20 offen offset:1024 sc1\n\t"
"s_waitcnt lgkmcnt(2)\n\t"
"v_mfma_f32_16x16x32_bf16 v[20:23], a[244:247], a[188:191], v[20:23]\n\t"
"v_mfma_f32_16x16x32_bf16 v[20:23], a[248:251], a[192:195], v[20:23]\n\t"
"v_mfma_f32_16x16x32_bf16 v[20:23], a[252:255], a[196:199], v[20:23]\n\t"
"s_waitcnt lgkmcnt(1)\n\t"
"v_mfma_f32_16x16x32_bf16 v[24:27], a[244:247], a[192:195], v[24:27]\n\t"
"v_mfma_f32_16x16x32_bf16 v[24:27], a[248:251], a[196:199], v[24:27]\n\t"
"v_mfma_f32_16x16x32_bf16 v[24:27], a[252:255], a[200:203], v[24:27]\n\t"
"s_waitcnt lgkmcnt(0)\n\t"
"v_mfma_f32_16x16x32_bf16 v[28:31], a[244:247], a[196:199], v[28:31]\n\t"
"v_mfma_f32_16x16x32_bf16 v[28:31], a[248:251], a[200:203], v[28:31]\n\t"
"v_mfma_f32_16x16x32_bf16 v[28:31], a[252:255], a[204:207], v[28:31]\n\t"
"s_waitcnt vmcnt(6)\n\t"
"v_mfma_f32_16x16x32_bf16 v[32:35], a[212:215], a[168:171], v[32:35]\n\t"
"v_mfma_f32_16x16x32_bf16 v[32:35], a[216:219], a[172:175], v[32:35]\n\t"
"v_mfma_f32_16x16x32_bf16 v[32:35], a[228:231], a[176:179], v[32:35]\n\t"
"ds_read_b64_tr_b16 v[222:223], v244 offset:1024\n\t"
"ds_read_b64_tr_b16 v[224:225], v244 offset:4096\n\t"
"v_mfma_f32_16x16x32_bf16 v[36:39], a[212:215], a[172:175], v[36:39]\n\t"
"v_mfma_f32_16x16x32_bf16 v[36:39], a[216:219], a[176:179], v[36:39]\n\t"
"v_mfma_f32_16x16x32_bf16 v[36:39], a[228:231], a[180:183], v[36:39]\n\t"
"v_mfma_f32_16x16x32_bf16 v[40:43], a[212:215], a[176:179], v[40:43]\n\t"
"v_mfma_f32_16x16x32_bf16 v[40:43], a[216:219], a[180:183], v[40:43]\n\t"
"v_mfma_f32_16x16x32_bf16 v[40:43], a[228:231], a[184:187], v[40:43]\n\t"
"v_mfma_f32_16x16x32_bf16 v[44:47], a[212:215], a[180:183], v[44:47]\n\t"
"ds_read_b64_tr_b16 v[226:227], v244 offset:7168\n\t"
"v_mfma_f32_16x16x32_bf16 v[44:47], a[216:219], a[184:187], v[44:47]\n\t"
"ds_read_b64_tr_b16 v[228:229], v244 offset:10240\n\t"
"v_mfma_f32_16x16x32_bf16 v[44:47], a[228:231], a[188:191], v[44:47]\n\t"
"v_mfma_f32_16x16x32_bf16 v[48:51], a[212:215], a[184:187], v[48:51]\n\t"
"v_mfma_f32_16x16x32_bf16 v[48:51], a[216:219], a[188:191], v[48:51]\n\t"
"v_mfma_f32_16x16x32_bf16 v[48:51], a[228:231], a[192:195], v[48:51]\n\t"
"buffer_load_dwordx4 a[224:227], v252, s[8:11], s20 offen offset:2048 sc1\n\t"
"v_mfma_f32_16x16x32_bf16 v[52:55], a[212:215], a[188:191], v[52:55]\n\t"
"v_mfma_f32_16x16x32_bf16 v[52:55], a[216:219], a[192:195], v[52:55]\n\t"
"v_mfma_f32_16x16x32_bf16 v[52:55], a[228:231], a[196:199], v[52:55]\n\t"
"s_mov_b32 s20, 1191936\n\t"
"buffer_load_dwordx4 a[244:247], v252, s[8:11], s20 offen offset:0 sc1\n\t"
"s_waitcnt lgkmcnt(2)\n\t"
"v_mfma_f32_16x16x32_bf16 v[222:225], v[222:225], v[218:221], 0\n\t"
"v_mfma_f32_16x16x32_bf16 v[56:59], a[212:215], a[192:195], v[56:59]\n\t"
"ds_read_b64_tr_b16 v[230:231], v244 offset:13312\n\t"
"v_mfma_f32_16x16x32_bf16 v[56:59], a[216:219], a[196:199], v[56:59]\n\t"
"ds_read_b64_tr_b16 v[232:233], v244 offset:16384\n\t"
"v_mfma_f32_16x16x32_bf16 v[56:59], a[228:231], a[200:203], v[56:59]\n\t"
"v_mfma_f32_16x16x32_bf16 v[60:63], a[212:215], a[196:199], v[60:63]\n\t"
"v_mfma_f32_16x16x32_bf16 v[60:63], a[216:219], a[200:203], v[60:63]\n\t"
"s_nop 0\n\t"
"v_cvt_pk_bf16_f32 v222, v222, v223\n\t"
"v_cvt_pk_bf16_f32 v223, v224, v225\n\t"
"v_mfma_f32_16x16x32_bf16 v[60:63], a[228:231], a[204:207], v[60:63]\n\t"
"buffer_load_dwordx4 a[248:251], v252, s[8:11], s20 offen offset:1024 sc1\n\t"
"s_waitcnt lgkmcnt(2)\n\t"
"v_mfma_f32_16x16x32_bf16 v[226:229], v[226:229], v[218:221], 0\n\t"
"buffer_load_dwordx4 a[212:215], v252, s[8:11], s20 offen offset:2048 sc1\n\t"
"s_waitcnt vmcnt(7)\n\t"
"v_mfma_f32_16x16x32_bf16 v[64:67], a[232:235], a[168:171], v[64:67]\n\t"
"ds_read_b64_tr_b16 v[234:235], v244 offset:20992\n\t"
"v_mfma_f32_16x16x32_bf16 v[64:67], a[208:211], a[172:175], v[64:67]\n\t"
"ds_read_b64_tr_b16 v[236:237], v244 offset:24064\n\t"
"v_mfma_f32_16x16x32_bf16 v[64:67], a[236:239], a[176:179], v[64:67]\n\t"
"ds_read_b128 a[168:171], v242 offset:9216\n\t"
"v_mfma_f32_16x16x32_bf16 v[68:71], a[232:235], a[172:175], v[68:71]\n\t"
"ds_read_b128 a[172:175], v242 offset:9728\n\t"
"v_mfma_f32_16x16x32_bf16 v[68:71], a[208:211], a[176:179], v[68:71]\n\t"
"v_cvt_pk_bf16_f32 v226, v226, v227\n\t"
"v_mfma_f32_16x16x32_bf16 v[68:71], a[236:239], a[180:183], v[68:71]\n\t"
"s_mov_b32 s20, 1204224\n\t"
"buffer_load_dwordx4 a[216:219], v252, s[8:11], s20 offen offset:0 sc1\n\t"
"v_mfma_f32_16x16x32_bf16 v[72:75], a[232:235], a[176:179], v[72:75]\n\t"
"v_cvt_pk_bf16_f32 v227, v228, v229\n\t"
"v_mfma_f32_16x16x32_bf16 v[72:75], a[208:211], a[180:183], v[72:75]\n\t"
"ds_read_b128 a[176:179], v242 offset:10240\n\t"
"v_mfma_f32_16x16x32_bf16 v[72:75], a[236:239], a[184:187], v[72:75]\n\t"
"buffer_load_dwordx4 a[228:231], v252, s[8:11], s20 offen offset:1024 sc1\n\t"
"s_waitcnt lgkmcnt(5)\n\t"
"v_mfma_f32_16x16x32_bf16 v[230:233], v[230:233], v[218:221], 0\n\t"
"v_mfma_f32_16x16x32_bf16 v[76:79], a[232:235], a[180:183], v[76:79]\n\t"
"ds_read_b128 a[180:183], v242 offset:10752\n\t"
"v_mfma_f32_16x16x32_bf16 v[76:79], a[208:211], a[184:187], v[76:79]\n\t"
"v_mfma_f32_16x16x32_bf16 v[76:79], a[236:239], a[188:191], v[76:79]\n\t"
"buffer_load_dwordx4 a[252:255], v252, s[8:11], s20 offen offset:2048 sc1\n\t"
"v_mfma_f32_16x16x32_bf16 v[80:83], a[232:235], a[184:187], v[80:83]\n\t"
"ds_read_b128 a[184:187], v242 offset:11264\n\t"
"v_mfma_f32_16x16x32_bf16 v[80:83], a[208:211], a[188:191], v[80:83]\n\t"
"v_cvt_pk_bf16_f32 v230, v230, v231\n\t"
"v_mfma_f32_16x16x32_bf16 v[80:83], a[236:239], a[192:195], v[80:83]\n\t"
"v_cvt_pk_bf16_f32 v231, v232, v233\n\t"
"s_waitcnt lgkmcnt(5)\n\t"
"v_mfma_f32_16x16x32_bf16 v[234:237], v[234:237], v[218:221], 0\n\t"
"v_mfma_f32_16x16x32_bf16 v[84:87], a[232:235], a[188:191], v[84:87]\n\t"
"ds_read_b128 a[188:191], v242 offset:11776\n\t"
"v_mfma_f32_16x16x32_bf16 v[84:87], a[208:211], a[192:195], v[84:87]\n\t"
"v_mfma_f32_16x16x32_bf16 v[84:87], a[236:239], a[196:199], v[84:87]\n\t"
"v_mfma_f32_16x16x32_bf16 v[88:91], a[232:235], a[192:195], v[88:91]\n\t"
"ds_read_b128 a[192:195], v242 offset:12288\n\t"
"v_mfma_f32_16x16x32_bf16 v[88:91], a[208:211], a[196:199], v[88:91]\n\t"
"s_nop 0\n\t"
"v_cvt_pk_bf16_f32 v234, v234, v235\n\t"
"v_mfma_f32_16x16x32_bf16 v[88:91], a[236:239], a[200:203], v[88:91]\n\t"
"v_cvt_pk_bf16_f32 v235, v236, v237\n\t"
"v_mfma_f32_16x16x32_bf16 v[92:95], a[232:235], a[196:199], v[92:95]\n\t"
"ds_read_b128 a[196:199], v242 offset:12800\n\t"
"v_mfma_f32_16x16x32_bf16 v[92:95], a[208:211], a[200:203], v[92:95]\n\t"
"ds_read_b128 a[200:203], v242 offset:13312\n\t"
"v_mfma_f32_16x16x32_bf16 v[92:95], a[236:239], a[204:207], v[92:95]\n\t"
"s_mov_b32 s20, 1216512\n\t"
"buffer_load_dwordx4 a[232:235], v252, s[8:11], s20 offen offset:0 sc1\n\t"
"s_waitcnt vmcnt(7) lgkmcnt(6)\n\t"
"v_mfma_f32_16x16x32_bf16 v[98:101], a[240:243], a[168:171], v[98:101]\n\t"
"ds_read_b128 a[204:207], v242 offset:13824\n\t"
"v_mfma_f32_16x16x32_bf16 v[98:101], a[220:223], a[172:175], v[98:101]\n\t"
"v_mfma_f32_16x16x32_bf16 v[98:101], a[224:227], a[176:179], v[98:101]\n\t"
"buffer_load_dwordx4 a[208:211], v252, s[8:11], s20 offen offset:1024 sc1\n\t"
"s_waitcnt lgkmcnt(6)\n\t"
"v_mfma_f32_16x16x32_bf16 v[102:105], a[240:243], a[172:175], v[102:105]\n\t"
"v_mfma_f32_16x16x32_bf16 v[102:105], a[220:223], a[176:179], v[102:105]\n\t"
"v_mfma_f32_16x16x32_bf16 v[102:105], a[224:227], a[180:183], v[102:105]\n\t"
"buffer_load_dwordx4 a[236:239], v252, s[8:11], s20 offen offset:2048 sc1\n\t"
"s_waitcnt lgkmcnt(0)\n\t"
"s_barrier\n\t"
"v_mfma_f32_16x16x32_bf16 v[106:109], a[240:243], a[176:179], v[106:109]\n\t"
"v_fma_f32 v0, v98, s52, v0\n\t"
"v_fma_f32 v1, v99, s52, v1\n\t"
"v_mfma_f32_16x16x32_bf16 v[106:109], a[220:223], a[180:183], v[106:109]\n\t"
"v_fma_f32 v2, v100, s52, v2\n\t"
"v_fma_f32 v3, v101, s52, v3\n\t"
"v_mfma_f32_16x16x32_bf16 v[106:109], a[224:227], a[184:187], v[106:109]\n\t"
"ds_write_b64 v243, v[222:223] offset:0\n\t"
"v_mfma_f32_16x16x32_bf16 v[110:113], a[240:243], a[180:183], v[110:113]\n\t"
"ds_read_b64_tr_b16 v[222:223], v244 offset:27136\n\t"
"v_fma_f32 v98, v0, s54, -v98\n\t"
"v_mfma_f32_16x16x32_bf16 v[110:113], a[220:223], a[184:187], v[110:113]\n\t"
"ds_read_b64_tr_b16 v[224:225], v244 offset:30208\n\t"
"v_fma_f32 v99, v1, s54, -v99\n\t"
"v_mfma_f32_16x16x32_bf16 v[110:113], a[224:227], a[188:191], v[110:113]\n\t"
"ds_write_b64 v243, v[226:227] offset:512\n\t"
"v_mfma_f32_16x16x32_bf16 v[114:117], a[240:243], a[184:187], v[114:117]\n\t"
"ds_read_b64_tr_b16 v[226:227], v244 offset:33280\n\t"
"v_fma_f32 v100, v2, s54, -v100\n\t"
"v_mfma_f32_16x16x32_bf16 v[114:117], a[220:223], a[188:191], v[114:117]\n\t"
"ds_read_b64_tr_b16 v[228:229], v244 offset:36352\n\t"
"v_fma_f32 v101, v3, s54, -v101\n\t"
"v_mfma_f32_16x16x32_bf16 v[114:117], a[224:227], a[192:195], v[114:117]\n\t"
"ds_write_b64 v243, v[230:231] offset:1024\n\t"
"ds_write_b64 v243, v[234:235] offset:1536\n\t"
"v_mfma_f32_16x16x32_bf16 v[118:121], a[240:243], a[188:191], v[118:121]\n\t"
"v_mfma_f32_16x16x32_bf16 v[118:121], a[220:223], a[192:195], v[118:121]\n\t"
"v_mfma_f32_16x16x32_bf16 v[118:121], a[224:227], a[196:199], v[118:121]\n\t"
"s_waitcnt lgkmcnt(5)\n\t"
"v_mfma_f32_16x16x32_bf16 v[222:225], v[222:225], v[218:221], 0\n\t"
"ds_read_b64_tr_b16 v[230:231], v244 offset:40960\n\t"
"ds_read_b64_tr_b16 v[232:233], v244 offset:44032\n\t"
"v_mfma_f32_16x16x32_bf16 v[122:125], a[240:243], a[192:195], v[122:125]\n\t"
"v_mfma_f32_16x16x32_bf16 v[122:125], a[220:223], a[196:199], v[122:125]\n\t"
"v_mfma_f32_16x16x32_bf16 v[122:125], a[224:227], a[200:203], v[122:125]\n\t"
"ds_read_b64_tr_b16 v[234:235], v244 offset:47104\n\t"
"s_waitcnt lgkmcnt(5)\n\t"
"v_mfma_f32_16x16x32_bf16 v[226:229], v[226:229], v[218:221], 0\n\t"
"ds_read_b64_tr_b16 v[236:237], v244 offset:50176\n\t"
"v_mfma_f32_16x16x32_bf16 v[126:129], a[240:243], a[196:199], v[126:129]\n\t"
"v_cvt_pk_bf16_f32 v222, v222, v223\n\t"
"v_cvt_pk_bf16_f32 v223, v224, v225\n\t"
"v_mfma_f32_16x16x32_bf16 v[126:129], a[220:223], a[200:203], v[126:129]\n\t"
"v_fma_f32 v4, v102, s52, v4\n\t"
"v_fma_f32 v5, v103, s52, v5\n\t"
"v_mfma_f32_16x16x32_bf16 v[126:129], a[224:227], a[204:207], v[126:129]\n\t"
"s_mov_b32 s20, 1228800\n\t"
"buffer_load_dwordx4 a[240:243], v252, s[8:11], s20 offen offset:0 sc1\n\t"
"s_waitcnt lgkmcnt(2)\n\t"
"v_mfma_f32_16x16x32_bf16 v[230:233], v[230:233], v[218:221], 0\n\t"
"buffer_load_dwordx4 a[220:223], v252, s[8:11], s20 offen offset:1024 sc1\n\t"
"s_waitcnt vmcnt(8)\n\t"
"v_mfma_f32_16x16x32_bf16 v[130:133], a[244:247], a[168:171], v[130:133]\n\t"
"v_cvt_pk_bf16_f32 v226, v226, v227\n\t"
"v_cvt_pk_bf16_f32 v227, v228, v229\n\t"
"v_mfma_f32_16x16x32_bf16 v[130:133], a[248:251], a[172:175], v[130:133]\n\t"
"v_fma_f32 v6, v104, s52, v6\n\t"
"v_fma_f32 v7, v105, s52, v7\n\t"
"v_mfma_f32_16x16x32_bf16 v[130:133], a[212:215], a[176:179], v[130:133]\n\t"
"ds_write_b64 v243, v[222:223] offset:2048\n\t"
"s_waitcnt lgkmcnt(1)\n\t"
"v_mfma_f32_16x16x32_bf16 v[234:237], v[234:237], v[218:221], 0\n\t"
"ds_read_b64_tr_b16 v[222:223], v244 offset:53248\n\t"
"ds_read_b64_tr_b16 v[224:225], v244 offset:56320\n\t"
"v_mfma_f32_16x16x32_bf16 v[134:137], a[244:247], a[172:175], v[134:137]\n\t"
"v_cvt_pk_bf16_f32 v230, v230, v231\n\t"
"v_cvt_pk_bf16_f32 v231, v232, v233\n\t"
"v_mfma_f32_16x16x32_bf16 v[134:137], a[248:251], a[176:179], v[134:137]\n\t"
"v_fma_f32 v102, v4, s54, -v102\n\t"
"v_fma_f32 v103, v5, s54, -v103\n\t"
"v_mfma_f32_16x16x32_bf16 v[134:137], a[212:215], a[180:183], v[134:137]\n\t"
"buffer_load_dwordx4 a[224:227], v252, s[8:11], s20 offen offset:2048 sc1\n\t"
"v_mfma_f32_16x16x32_bf16 v[138:141], a[244:247], a[176:179], v[138:141]\n\t"
"v_fma_f32 v104, v6, s54, -v104\n\t"
"v_fma_f32 v105, v7, s54, -v105\n\t"
"v_mfma_f32_16x16x32_bf16 v[138:141], a[248:251], a[180:183], v[138:141]\n\t"
"v_cvt_pk_bf16_f32 v234, v234, v235\n\t"
"v_cvt_pk_bf16_f32 v235, v236, v237\n\t"
"v_mfma_f32_16x16x32_bf16 v[138:141], a[212:215], a[184:187], v[138:141]\n\t"
"ds_write_b64 v243, v[226:227] offset:2560\n\t"
"v_mfma_f32_16x16x32_bf16 v[142:145], a[244:247], a[180:183], v[142:145]\n\t"
"v_fma_f32 v8, v106, s52, v8\n\t"
"v_fma_f32 v9, v107, s52, v9\n\t"
"v_mfma_f32_16x16x32_bf16 v[142:145], a[248:251], a[184:187], v[142:145]\n\t"
"v_fma_f32 v10, v108, s52, v10\n\t"
"v_fma_f32 v11, v109, s52, v11\n\t"
"v_mfma_f32_16x16x32_bf16 v[142:145], a[212:215], a[188:191], v[142:145]\n\t"
"ds_write_b64 v243, v[230:231] offset:3072\n\t"
"s_waitcnt lgkmcnt(2)\n\t"
"v_mfma_f32_16x16x32_bf16 v[222:225], v[222:225], v[218:221], 0\n\t"
"ds_write_b64 v243, v[234:235] offset:3584\n\t"
"v_mfma_f32_16x16x32_bf16 v[146:149], a[244:247], a[184:187], v[146:149]\n\t"
"v_fma_f32 v106, v8, s54, -v106\n\t"
"v_fma_f32 v107, v9, s54, -v107\n\t"
"v_mfma_f32_16x16x32_bf16 v[146:149], a[248:251], a[188:191], v[146:149]\n\t"
"v_fma_f32 v108, v10, s54, -v108\n\t"
"v_fma_f32 v109, v11, s54, -v109\n\t"
"v_mfma_f32_16x16x32_bf16 v[146:149], a[212:215], a[192:195], v[146:149]\n\t"
"v_fma_f32 v12, v110, s52, v12\n\t"
"v_fma_f32 v13, v111, s52, v13\n\t"
"v_mfma_f32_16x16x32_bf16 v[150:153], a[244:247], a[188:191], v[150:153]\n\t"
"v_fma_f32 v14, v112, s52, v14\n\t"
"v_fma_f32 v15, v113, s52, v15\n\t"
"v_mfma_f32_16x16x32_bf16 v[150:153], a[248:251], a[192:195], v[150:153]\n\t"
"v_cvt_pk_bf16_f32 v222, v222, v223\n\t"
"v_cvt_pk_bf16_f32 v223, v224, v225\n\t"
"v_mfma_f32_16x16x32_bf16 v[150:153], a[212:215], a[196:199], v[150:153]\n\t"
"ds_write_b64 v243, v[222:223] offset:4096\n\t"
"v_mfma_f32_16x16x32_bf16 v[154:157], a[244:247], a[192:195], v[154:157]\n\t"
"v_fma_f32 v110, v12, s54, -v110\n\t"
"v_fma_f32 v111, v13, s54, -v111\n\t"
"v_mfma_f32_16x16x32_bf16 v[154:157], a[248:251], a[196:199], v[154:157]\n\t"
"v_fma_f32 v112, v14, s54, -v112\n\t"
"v_fma_f32 v113, v15, s54, -v113\n\t"
"v_mfma_f32_16x16x32_bf16 v[154:157], a[212:215], a[200:203], v[154:157]\n\t"
"v_mad_u32_u24 v255, v252, 24, s51\n\t"
"s_add_u32 s28, s21, 9436800\n\t"
"s_mov_b32 exec_hi, 3\n\t"
"buffer_load_dword v255, v255, s[12:15], s28 offen offset:0\n\t"
"s_mov_b32 exec_hi, -1\n\t"
"v_fma_f32 v16, v114, s52, v16\n\t"
"v_mfma_f32_16x16x32_bf16 v[158:161], a[244:247], a[196:199], v[158:161]\n\t"
"v_fma_f32 v17, v115, s52, v17\n\t"
"v_fma_f32 v18, v116, s52, v18\n\t"
"v_mfma_f32_16x16x32_bf16 v[158:161], a[248:251], a[200:203], v[158:161]\n\t"
"v_fma_f32 v19, v117, s52, v19\n\t"
"v_fma_f32 v114, v16, s54, -v114\n\t"
"v_mfma_f32_16x16x32_bf16 v[158:161], a[212:215], a[204:207], v[158:161]\n\t"
"v_fma_f32 v115, v17, s54, -v115\n\t"
"v_fma_f32 v116, v18, s54, -v116\n\t"
"s_waitcnt vmcnt(7)\n\t"
"v_mfma_f32_16x16x32_bf16 v[162:165], a[216:219], a[168:171], v[162:165]\n\t"
"v_fma_f32 v117, v19, s54, -v117\n\t"
"v_fma_f32 v20, v118, s52, v20\n\t"
"v_mfma_f32_16x16x32_bf16 v[162:165], a[228:231], a[172:175], v[162:165]\n\t"
"v_fma_f32 v21, v119, s52, v21\n\t"
"v_fma_f32 v22, v120, s52, v22\n\t"
"v_mfma_f32_16x16x32_bf16 v[162:165], a[252:255], a[176:179], v[162:165]\n\t"
"v_fma_f32 v23, v121, s52, v23\n\t"
"v_fma_f32 v118, v20, s54, -v118\n\t"
"s_waitcnt lgkmcnt(0)\n\t"
"s_barrier\n\t"
"v_mfma_f32_16x16x32_bf16 v[166:169], a[216:219], a[172:175], v[166:169]\n\t"
"ds_read_b128 a[168:171], v242 offset:0\n\t"
"v_fma_f32 v119, v21, s54, -v119\n\t"
"v_mfma_f32_16x16x32_bf16 v[166:169], a[228:231], a[176:179], v[166:169]\n\t"
"ds_read_b128 a[172:175], v242 offset:512\n\t"
"v_fma_f32 v120, v22, s54, -v120\n\t"
"v_mfma_f32_16x16x32_bf16 v[166:169], a[252:255], a[180:183], v[166:169]\n\t"
"s_mov_b32 s20, 1241088\n\t"
"buffer_load_dwordx4 a[212:215], v252, s[8:11], s20 offen offset:0 sc1\n\t"
"v_mfma_f32_16x16x32_bf16 v[170:173], a[216:219], a[176:179], v[170:173]\n\t"
"ds_read_b128 a[176:179], v242 offset:1024\n\t"
"v_fma_f32 v121, v23, s54, -v121\n\t"
"v_mfma_f32_16x16x32_bf16 v[170:173], a[228:231], a[180:183], v[170:173]\n\t"
"v_fma_f32 v24, v122, s52, v24\n\t"
"v_fma_f32 v25, v123, s52, v25\n\t"
"v_mfma_f32_16x16x32_bf16 v[170:173], a[252:255], a[184:187], v[170:173]\n\t"
"v_fma_f32 v26, v124, s52, v26\n\t"
"v_fma_f32 v27, v125, s52, v27\n\t"
"v_mfma_f32_16x16x32_bf16 v[174:177], a[216:219], a[180:183], v[174:177]\n\t"
"ds_read_b128 a[180:183], v242 offset:1536\n\t"
"v_fma_f32 v122, v24, s54, -v122\n\t"
"v_mfma_f32_16x16x32_bf16 v[174:177], a[228:231], a[184:187], v[174:177]\n\t"
"v_fma_f32 v123, v25, s54, -v123\n\t"
"v_fma_f32 v124, v26, s54, -v124\n\t"
"v_mfma_f32_16x16x32_bf16 v[174:177], a[252:255], a[188:191], v[174:177]\n\t"
"v_fma_f32 v125, v27, s54, -v125\n\t"
"v_fma_f32 v28, v126, s52, v28\n\t"
"v_fma_f32 v29, v127, s52, v29\n\t"
"v_fma_f32 v30, v128, s52, v30\n\t"
"v_mfma_f32_16x16x32_bf16 v[178:181], a[216:219], a[184:187], v[178:181]\n\t"
"ds_read_b128 a[184:187], v242 offset:2048\n\t"
"v_fma_f32 v31, v129, s52, v31\n\t"
"v_mfma_f32_16x16x32_bf16 v[178:181], a[228:231], a[188:191], v[178:181]\n\t"
"v_fma_f32 v126, v28, s54, -v126\n\t"
"v_fma_f32 v127, v29, s54, -v127\n\t"
"v_mfma_f32_16x16x32_bf16 v[178:181], a[252:255], a[192:195], v[178:181]\n\t"
"v_fma_f32 v128, v30, s54, -v128\n\t"
"v_fma_f32 v129, v31, s54, -v129\n\t"
"v_fma_f32 v32, v130, s52, v32\n\t"
"v_fma_f32 v33, v131, s52, v33\n\t"
"v_mfma_f32_16x16x32_bf16 v[182:185], a[216:219], a[188:191], v[182:185]\n\t"
"ds_read_b128 a[188:191], v242 offset:2560\n\t"
"v_fma_f32 v34, v132, s52, v34\n\t"
"v_mfma_f32_16x16x32_bf16 v[182:185], a[228:231], a[192:195], v[182:185]\n\t"
"v_fma_f32 v35, v133, s52, v35\n\t"
"v_fma_f32 v130, v32, s54, -v130\n\t"
"v_mfma_f32_16x16x32_bf16 v[182:185], a[252:255], a[196:199], v[182:185]\n\t"
"v_fma_f32 v131, v33, s54, -v131\n\t"
"v_fma_f32 v132, v34, s54, -v132\n\t"
"v_fma_f32 v133, v35, s54, -v133\n\t"
"v_fma_f32 v36, v134, s52, v36\n\t"
"v_mfma_f32_16x16x32_bf16 v[186:189], a[216:219], a[192:195], v[186:189]\n\t"
"ds_read_b128 a[192:195], v242 offset:3072\n\t"
"v_fma_f32 v37, v135, s52, v37\n\t"
"v_mfma_f32_16x16x32_bf16 v[186:189], a[228:231], a[196:199], v[186:189]\n\t"
"v_fma_f32 v38, v136, s52, v38\n\t"
"v_fma_f32 v39, v137, s52, v39\n\t"
"v_mfma_f32_16x16x32_bf16 v[186:189], a[252:255], a[200:203], v[186:189]\n\t"
"v_fma_f32 v134, v36, s54, -v134\n\t"
"v_fma_f32 v135, v37, s54, -v135\n\t"
"v_fma_f32 v136, v38, s54, -v136\n\t"
"v_fma_f32 v137, v39, s54, -v137\n\t"
"v_mfma_f32_16x16x32_bf16 v[190:193], a[216:219], a[196:199], v[190:193]\n\t"
"ds_read_b128 a[196:199], v242 offset:3584\n\t"
"v_fma_f32 v40, v138, s52, v40\n\t"
"v_mfma_f32_16x16x32_bf16 v[190:193], a[228:231], a[200:203], v[190:193]\n\t"
"ds_read_b128 a[200:203], v242 offset:4096\n\t"
"v_fma_f32 v41, v139, s52, v41\n\t"
"v_mfma_f32_16x16x32_bf16 v[190:193], a[252:255], a[204:207], v[190:193]\n\t"
"ds_read_b128 a[204:207], v242 offset:4608\n\t"
"v_fma_f32 v42, v140, s52, v42\n\t"
"v_fma_f32 v43, v141, s52, v43\n\t"
"v_fma_f32 v138, v40, s54, -v138\n\t"
"s_waitcnt vmcnt(5) lgkmcnt(7)\n\t"
"v_mfma_f32_16x16x32_bf16 v[194:197], a[232:235], a[168:171], v[194:197]\n\t"
"v_fma_f32 v139, v41, s54, -v139\n\t"
"v_fma_f32 v140, v42, s54, -v140\n\t"
"v_mfma_f32_16x16x32_bf16 v[194:197], a[208:211], a[172:175], v[194:197]\n\t"
"v_fma_f32 v141, v43, s54, -v141\n\t"
"v_fma_f32 v44, v142, s52, v44\n\t"
"v_mfma_f32_16x16x32_bf16 v[194:197], a[236:239], a[176:179], v[194:197]\n\t"
"v_fma_f32 v45, v143, s52, v45\n\t"
"v_fma_f32 v46, v144, s52, v46\n\t"
"v_fma_f32 v47, v145, s52, v47\n\t"
"v_fma_f32 v142, v44, s54, -v142\n\t"
"s_waitcnt lgkmcnt(6)\n\t"
"v_mfma_f32_16x16x32_bf16 v[202:205], a[232:235], a[172:175], v[202:205]\n\t"
"v_fma_f32 v143, v45, s54, -v143\n\t"
"v_fma_f32 v144, v46, s54, -v144\n\t"
"v_mfma_f32_16x16x32_bf16 v[202:205], a[208:211], a[176:179], v[202:205]\n\t"
"v_fma_f32 v145, v47, s54, -v145\n\t"
"v_fma_f32 v48, v146, s52, v48\n\t"
"v_mfma_f32_16x16x32_bf16 v[202:205], a[236:239], a[180:183], v[202:205]\n\t"
"v_fma_f32 v49, v147, s52, v49\n\t"
"v_fma_f32 v50, v148, s52, v50\n\t"
"v_fma_f32 v51, v149, s52, v51\n\t"
"v_fma_f32 v146, v48, s54, -v146\n\t"
"s_waitcnt lgkmcnt(5)\n\t"
"v_mfma_f32_16x16x32_bf16 v[210:213], a[232:235], a[176:179], v[210:213]\n\t"
"v_fma_f32 v147, v49, s54, -v147\n\t"
"v_fma_f32 v148, v50, s54, -v148\n\t"
"v_mfma_f32_16x16x32_bf16 v[210:213], a[208:211], a[180:183], v[210:213]\n\t"
"v_fma_f32 v149, v51, s54, -v149\n\t"
"v_fma_f32 v52, v150, s52, v52\n\t"
"v_mfma_f32_16x16x32_bf16 v[210:213], a[236:239], a[184:187], v[210:213]\n\t"
"buffer_load_dwordx4 a[216:219], v252, s[8:11], s20 offen offset:1024 sc1\n\t"
"v_fma_f32 v53, v151, s52, v53\n\t"
"v_fma_f32 v54, v152, s52, v54\n\t"
"s_waitcnt lgkmcnt(4)\n\t"
"v_mfma_f32_16x16x32_bf16 a[0:3], a[232:235], a[180:183], a[0:3]\n\t"
"v_fma_f32 v55, v153, s52, v55\n\t"
"v_fma_f32 v150, v52, s54, -v150\n\t"
"v_mfma_f32_16x16x32_bf16 a[0:3], a[208:211], a[184:187], a[0:3]\n\t"
"v_fma_f32 v151, v53, s54, -v151\n\t"
"v_fma_f32 v152, v54, s54, -v152\n\t"
"v_mfma_f32_16x16x32_bf16 a[0:3], a[236:239], a[188:191], a[0:3]\n\t"
"buffer_load_dwordx4 a[228:231], v252, s[8:11], s20 offen offset:2048 sc1\n\t"
"v_fma_f32 v153, v55, s54, -v153\n\t"
"v_fma_f32 v56, v154, s52, v56\n\t"
"s_waitcnt lgkmcnt(3)\n\t"
"v_mfma_f32_16x16x32_bf16 a[8:11], a[232:235], a[184:187], a[8:11]\n\t"
"v_fma_f32 v57, v155, s52, v57\n\t"
"v_fma_f32 v58, v156, s52, v58\n\t"
"v_mfma_f32_16x16x32_bf16 a[8:11], a[208:211], a[188:191], a[8:11]\n\t"
"v_fma_f32 v59, v157, s52, v59\n\t"
"v_fma_f32 v154, v56, s54, -v154\n\t"
"v_mfma_f32_16x16x32_bf16 a[8:11], a[236:239], a[192:195], a[8:11]\n\t"
"v_fma_f32 v155, v57, s54, -v155\n\t"
"v_fma_f32 v156, v58, s54, -v156\n\t"
"v_fma_f32 v157, v59, s54, -v157\n\t"
"v_fma_f32 v60, v158, s52, v60\n\t"
"s_waitcnt lgkmcnt(2)\n\t"
"v_mfma_f32_16x16x32_bf16 a[16:19], a[232:235], a[188:191], a[16:19]\n\t"
"v_fma_f32 v61, v159, s52, v61\n\t"
"v_fma_f32 v62, v160, s52, v62\n\t"
"v_mfma_f32_16x16x32_bf16 a[16:19], a[208:211], a[192:195], a[16:19]\n\t"
"v_fma_f32 v63, v161, s52, v63\n\t"
"v_fma_f32 v158, v60, s54, -v158\n\t"
"v_mfma_f32_16x16x32_bf16 a[16:19], a[236:239], a[196:199], a[16:19]\n\t"
"s_mov_b32 s20, 1253376\n\t"
"buffer_load_dwordx4 a[244:247], v252, s[8:11], s20 offen offset:0 sc1\n\t"
"v_fma_f32 v159, v61, s54, -v159\n\t"
"v_fma_f32 v160, v62, s54, -v160\n\t"
"s_waitcnt lgkmcnt(1)\n\t"
"v_mfma_f32_16x16x32_bf16 a[24:27], a[232:235], a[192:195], a[24:27]\n\t"
"v_fma_f32 v161, v63, s54, -v161\n\t"
"v_fma_f32 v64, v162, s52, v64\n\t"
"v_mfma_f32_16x16x32_bf16 a[24:27], a[208:211], a[196:199], a[24:27]\n\t"
"v_fma_f32 v65, v163, s52, v65\n\t"
"v_fma_f32 v66, v164, s52, v66\n\t"
"v_mfma_f32_16x16x32_bf16 a[24:27], a[236:239], a[200:203], a[24:27]\n\t"
"buffer_load_dwordx4 a[248:251], v252, s[8:11], s20 offen offset:1024 sc1\n\t"
"v_fma_f32 v67, v165, s52, v67\n\t"
"v_fma_f32 v162, v64, s54, -v162\n\t"
"s_waitcnt lgkmcnt(0)\n\t"
"v_mfma_f32_16x16x32_bf16 a[32:35], a[232:235], a[196:199], a[32:35]\n\t"
"v_fma_f32 v163, v65, s54, -v163\n\t"
"v_fma_f32 v164, v66, s54, -v164\n\t"
"v_mfma_f32_16x16x32_bf16 a[32:35], a[208:211], a[200:203], a[32:35]\n\t"
"v_fma_f32 v165, v67, s54, -v165\n\t"
"v_fma_f32 v68, v166, s52, v68\n\t"
"v_mfma_f32_16x16x32_bf16 a[32:35], a[236:239], a[204:207], a[32:35]\n\t"
"buffer_load_dwordx4 a[208:211], v252, s[8:11], s20 offen offset:2048 sc1\n\t"
"v_fma_f32 v69, v167, s52, v69\n\t"
"v_fma_f32 v70, v168, s52, v70\n\t"
"s_waitcnt vmcnt(7)\n\t"
"v_mfma_f32_16x16x32_bf16 a[40:43], a[240:243], a[168:171], a[40:43]\n\t"
"v_fma_f32 v71, v169, s52, v71\n\t"
"v_fma_f32 v166, v68, s54, -v166\n\t"
"v_mfma_f32_16x16x32_bf16 a[40:43], a[220:223], a[172:175], a[40:43]\n\t"
"v_fma_f32 v167, v69, s54, -v167\n\t"
"v_fma_f32 v168, v70, s54, -v168\n\t"
"v_mfma_f32_16x16x32_bf16 a[40:43], a[224:227], a[176:179], a[40:43]\n\t"
"v_fma_f32 v169, v71, s54, -v169\n\t"
"v_fma_f32 v72, v170, s52, v72\n\t"
"v_mfma_f32_16x16x32_bf16 a[48:51], a[240:243], a[172:175], a[48:51]\n\t"
"v_fma_f32 v73, v171, s52, v73\n\t"
"v_fma_f32 v74, v172, s52, v74\n\t"
"v_mfma_f32_16x16x32_bf16 a[48:51], a[220:223], a[176:179], a[48:51]\n\t"
"v_fma_f32 v75, v173, s52, v75\n\t"
"v_fma_f32 v170, v72, s54, -v170\n\t"
"v_mfma_f32_16x16x32_bf16 a[48:51], a[224:227], a[180:183], a[48:51]\n\t"
"s_mov_b32 s20, 1265664\n\t"
"buffer_load_dwordx4 a[232:235], v252, s[8:11], s20 offen offset:0 sc1\n\t"
"v_mfma_f32_16x16x32_bf16 a[56:59], a[240:243], a[176:179], a[56:59]\n\t"
"v_fma_f32 v171, v73, s54, -v171\n\t"
"v_fma_f32 v172, v74, s54, -v172\n\t"
"v_mfma_f32_16x16x32_bf16 a[56:59], a[220:223], a[180:183], a[56:59]\n\t"
"v_fma_f32 v173, v75, s54, -v173\n\t"
"v_fma_f32 v76, v174, s52, v76\n\t"
"v_mfma_f32_16x16x32_bf16 a[56:59], a[224:227], a[184:187], a[56:59]\n\t"
"buffer_load_dwordx4 a[236:239], v252, s[8:11], s20 offen offset:1024 sc1\n\t"
"v_fma_f32 v77, v175, s52, v77\n\t"
"v_fma_f32 v78, v176, s52, v78\n\t"
"v_mfma_f32_16x16x32_bf16 a[64:67], a[240:243], a[180:183], a[64:67]\n\t"
"v_fma_f32 v79, v177, s52, v79\n\t"
"v_fma_f32 v174, v76, s54, -v174\n\t"
"v_mfma_f32_16x16x32_bf16 a[64:67], a[220:223], a[184:187], a[64:67]\n\t"
"v_fma_f32 v175, v77, s54, -v175\n\t"
"v_fma_f32 v176, v78, s54, -v176\n\t"
"v_mfma_f32_16x16x32_bf16 a[64:67], a[224:227], a[188:191], a[64:67]\n\t"
"buffer_load_dwordx4 a[252:255], v252, s[8:11], s20 offen offset:2048 sc1\n\t"
"v_mfma_f32_16x16x32_bf16 a[72:75], a[240:243], a[184:187], a[72:75]\n\t"
"v_fma_f32 v177, v79, s54, -v177\n\t"
"v_fma_f32 v80, v178, s52, v80\n\t"
"v_mfma_f32_16x16x32_bf16 a[72:75], a[220:223], a[188:191], a[72:75]\n\t"
"v_fma_f32 v81, v179, s52, v81\n\t"
"v_fma_f32 v82, v180, s52, v82\n\t"
"v_mfma_f32_16x16x32_bf16 a[72:75], a[224:227], a[192:195], a[72:75]\n\t"
"v_fma_f32 v83, v181, s52, v83\n\t"
"v_fma_f32 v178, v80, s54, -v178\n\t"
"v_mfma_f32_16x16x32_bf16 a[80:83], a[240:243], a[188:191], a[80:83]\n\t"
"v_fma_f32 v179, v81, s54, -v179\n\t"
"v_fma_f32 v180, v82, s54, -v180\n\t"
"v_mfma_f32_16x16x32_bf16 a[80:83], a[220:223], a[192:195], a[80:83]\n\t"
"v_fma_f32 v181, v83, s54, -v181\n\t"
"v_fma_f32 v84, v182, s52, v84\n\t"
"v_mfma_f32_16x16x32_bf16 a[80:83], a[224:227], a[196:199], a[80:83]\n\t"
"v_fma_f32 v85, v183, s52, v85\n\t"
"v_fma_f32 v86, v184, s52, v86\n\t"
"v_mfma_f32_16x16x32_bf16 a[88:91], a[240:243], a[192:195], a[88:91]\n\t"
"v_fma_f32 v87, v185, s52, v87\n\t"
"v_fma_f32 v182, v84, s54, -v182\n\t"
"v_mfma_f32_16x16x32_bf16 a[88:91], a[220:223], a[196:199], a[88:91]\n\t"
"v_fma_f32 v183, v85, s54, -v183\n\t"
"v_fma_f32 v184, v86, s54, -v184\n\t"
"v_mfma_f32_16x16x32_bf16 a[88:91], a[224:227], a[200:203], a[88:91]\n\t"
"v_fma_f32 v185, v87, s54, -v185\n\t"
"v_mfma_f32_16x16x32_bf16 a[96:99], a[240:243], a[196:199], a[96:99]\n\t"
"v_fma_f32 v88, v186, s52, v88\n\t"
"v_fma_f32 v89, v187, s52, v89\n\t"
"v_mfma_f32_16x16x32_bf16 a[96:99], a[220:223], a[200:203], a[96:99]\n\t"
"v_fma_f32 v90, v188, s52, v90\n\t"
"v_mfma_f32_16x16x32_bf16 a[96:99], a[224:227], a[204:207], a[96:99]\n\t"
"s_mov_b32 s20, 1277952\n\t"
"buffer_load_dwordx4 a[240:243], v252, s[8:11], s20 offen offset:0 sc1\n\t"
"s_waitcnt vmcnt(7)\n\t"
"v_mfma_f32_16x16x32_bf16 a[104:107], a[212:215], a[168:171], a[104:107]\n\t"
"ds_read_b128 a[168:171], v242 offset:9216\n\t"
"v_fma_f32 v91, v189, s52, v91\n\t"
"v_mfma_f32_16x16x32_bf16 a[104:107], a[216:219], a[172:175], a[104:107]\n\t"
"v_fma_f32 v186, v88, s54, -v186\n\t"
"v_mfma_f32_16x16x32_bf16 a[104:107], a[228:231], a[176:179], a[104:107]\n\t"
"buffer_load_dwordx4 a[220:223], v252, s[8:11], s20 offen offset:1024 sc1\n\t"
"v_mfma_f32_16x16x32_bf16 a[112:115], a[212:215], a[172:175], a[112:115]\n\t"
"ds_read_b128 a[172:175], v242 offset:9728\n\t"
"v_fma_f32 v187, v89, s54, -v187\n\t"
"v_mfma_f32_16x16x32_bf16 a[112:115], a[216:219], a[176:179], a[112:115]\n\t"
"v_fma_f32 v188, v90, s54, -v188\n\t"
"v_fma_f32 v189, v91, s54, -v189\n\t"
"v_mfma_f32_16x16x32_bf16 a[112:115], a[228:231], a[180:183], a[112:115]\n\t"
"v_fma_f32 v92, v190, s52, v92\n\t"
"v_mfma_f32_16x16x32_bf16 a[120:123], a[212:215], a[176:179], a[120:123]\n\t"
"ds_read_b128 a[176:179], v242 offset:10240\n\t"
"v_fma_f32 v93, v191, s52, v93\n\t"
"v_mfma_f32_16x16x32_bf16 a[120:123], a[216:219], a[180:183], a[120:123]\n\t"
"v_fma_f32 v94, v192, s52, v94\n\t"
"v_mfma_f32_16x16x32_bf16 a[120:123], a[228:231], a[184:187], a[120:123]\n\t"
"buffer_load_dwordx4 a[224:227], v252, s[8:11], s20 offen offset:2048 sc1\n\t"
"v_mfma_f32_16x16x32_bf16 a[128:131], a[212:215], a[180:183], a[128:131]\n\t"
"ds_read_b128 a[180:183], v242 offset:10752\n\t"
"v_mfma_f32_16x16x32_bf16 a[128:131], a[216:219], a[184:187], a[128:131]\n\t"
"v_fma_f32 v95, v193, s52, v95\n\t"
"v_mfma_f32_16x16x32_bf16 a[128:131], a[228:231], a[188:191], a[128:131]\n\t"
"v_fma_f32 v190, v92, s54, -v190\n\t"
"v_mfma_f32_16x16x32_bf16 a[136:139], a[212:215], a[184:187], a[136:139]\n\t"
"ds_read_b128 a[184:187], v242 offset:11264\n\t"
"v_mfma_f32_16x16x32_bf16 a[136:139], a[216:219], a[188:191], a[136:139]\n\t"
"v_mfma_f32_16x16x32_bf16 a[136:139], a[228:231], a[192:195], a[136:139]\n\t"
"v_fma_f32 v191, v93, s54, -v191\n\t"
"v_mfma_f32_16x16x32_bf16 a[144:147], a[212:215], a[188:191], a[144:147]\n\t"
"ds_read_b128 a[188:191], v242 offset:11776\n\t"
"v_mfma_f32_16x16x32_bf16 a[144:147], a[216:219], a[192:195], a[144:147]\n\t"
"v_fma_f32 v192, v94, s54, -v192\n\t"
"v_mfma_f32_16x16x32_bf16 a[144:147], a[228:231], a[196:199], a[144:147]\n\t"
"v_mfma_f32_16x16x32_bf16 a[152:155], a[212:215], a[192:195], a[152:155]\n\t"
"ds_read_b128 a[192:195], v242 offset:12288\n\t"
"v_mfma_f32_16x16x32_bf16 a[152:155], a[216:219], a[196:199], a[152:155]\n\t"
"v_fma_f32 v193, v95, s54, -v193\n\t"
"v_mfma_f32_16x16x32_bf16 a[152:155], a[228:231], a[200:203], a[152:155]\n\t"
"v_mfma_f32_16x16x32_bf16 a[160:163], a[212:215], a[196:199], a[160:163]\n\t"
"ds_read_b128 a[196:199], v242 offset:12800\n\t"
"v_mfma_f32_16x16x32_bf16 a[160:163], a[216:219], a[200:203], a[160:163]\n\t"
"ds_read_b128 a[200:203], v242 offset:13312\n\t"
"v_mfma_f32_16x16x32_bf16 a[160:163], a[228:231], a[204:207], a[160:163]\n\t"
"s_mov_b32 s20, 442368\n\t"
"buffer_load_dwordx4 a[212:215], v252, s[8:11], s20 offen offset:0 sc1\n\t"
"s_waitcnt vmcnt(7) lgkmcnt(6)\n\t"
"v_mfma_f32_16x16x32_bf16 v[198:201], a[244:247], a[168:171], v[198:201]\n\t"
"ds_read_b128 a[204:207], v242 offset:13824\n\t"
"v_mfma_f32_16x16x32_bf16 v[198:201], a[248:251], a[172:175], v[198:201]\n\t"
"v_mfma_f32_16x16x32_bf16 v[198:201], a[208:211], a[176:179], v[198:201]\n\t"
"buffer_load_dwordx4 a[216:219], v252, s[8:11], s20 offen offset:1024 sc1\n\t"
"s_waitcnt lgkmcnt(6)\n\t"
"v_mfma_f32_16x16x32_bf16 v[206:209], a[244:247], a[172:175], v[206:209]\n\t"
"v_mfma_f32_16x16x32_bf16 v[206:209], a[248:251], a[176:179], v[206:209]\n\t"
"v_mfma_f32_16x16x32_bf16 v[206:209], a[208:211], a[180:183], v[206:209]\n\t"
"buffer_load_dwordx4 a[228:231], v252, s[8:11], s20 offen offset:2048 sc1\n\t"
"s_waitcnt lgkmcnt(0)\n\t"
"s_barrier\n\t"
"v_mfma_f32_16x16x32_bf16 v[214:217], a[244:247], a[176:179], v[214:217]\n\t"
"v_mfma_f32_16x16x32_bf16 v[214:217], a[248:251], a[180:183], v[214:217]\n\t"
"v_mfma_f32_16x16x32_bf16 v[214:217], a[208:211], a[184:187], v[214:217]\n\t"
"v_pk_fma_f32 v[194:195], v[198:199], s[56:57], v[194:195]\n\t"
"v_pk_fma_f32 v[198:199], v[194:195], s[58:59], v[198:199] neg_lo:[0,0,1] neg_hi:[0,0,1]\n\t"
"v_pk_fma_f32 v[226:227], v[0:1], s[60:61], v[194:195]\n\t"
"v_pk_fma_f32 v[0:1], v[194:195], s[60:61], v[0:1]\n\t"
"v_pk_fma_f32 v[194:195], v[98:99], s[62:63], v[198:199]\n\t"
"v_pk_fma_f32 v[98:99], v[198:199], s[62:63], v[98:99]\n\t"
"v_pk_fma_f32 v[196:197], v[200:201], s[56:57], v[196:197]\n\t"
"v_pk_fma_f32 v[200:201], v[196:197], s[58:59], v[200:201] neg_lo:[0,0,1] neg_hi:[0,0,1]\n\t"
"v_pk_fma_f32 v[232:233], v[2:3], s[60:61], v[196:197]\n\t"
"v_pk_fma_f32 v[2:3], v[196:197], s[60:61], v[2:3]\n\t"
"v_pk_fma_f32 v[196:197], v[100:101], s[62:63], v[200:201]\n\t"
"v_pk_fma_f32 v[100:101], v[200:201], s[62:63], v[100:101]\n\t"
"v_cvt_pk_bf16_f32 v194, v194, v195\n\t"
"v_cvt_pk_bf16_f32 v198, v226, v227\n\t"
"v_mfma_f32_16x16x32_bf16 a[4:7], a[244:247], a[180:183], a[4:7]\n\t"
"v_cvt_pk_bf16_f32 v195, v196, v197\n\t"
"v_mfma_f32_16x16x32_bf16 a[4:7], a[248:251], a[184:187], a[4:7]\n\t"
"v_cvt_pk_bf16_f32 v199, v232, v233\n\t"
"v_mfma_f32_16x16x32_bf16 a[4:7], a[208:211], a[188:191], a[4:7]\n\t"
"v_pk_fma_f32 v[202:203], v[206:207], s[56:57], v[202:203]\n\t"
"v_pk_fma_f32 v[206:207], v[202:203], s[58:59], v[206:207] neg_lo:[0,0,1] neg_hi:[0,0,1]\n\t"
"v_pk_fma_f32 v[250:251], v[4:5], s[60:61], v[202:203]\n\t"
"v_pk_fma_f32 v[4:5], v[202:203], s[60:61], v[4:5]\n\t"
"v_pk_fma_f32 v[202:203], v[102:103], s[62:63], v[206:207]\n\t"
"v_pk_fma_f32 v[102:103], v[206:207], s[62:63], v[102:103]\n\t"
"v_cvt_pk_bf16_f32 v202, v202, v203\n\t"
"v_mfma_f32_16x16x32_bf16 a[12:15], a[244:247], a[184:187], a[12:15]\n\t"
"v_cvt_pk_bf16_f32 v206, v250, v251\n\t"
"v_mfma_f32_16x16x32_bf16 a[12:15], a[248:251], a[188:191], a[12:15]\n\t"
"v_mfma_f32_16x16x32_bf16 a[12:15], a[208:211], a[192:195], a[12:15]\n\t"
"v_pk_fma_f32 v[204:205], v[208:209], s[56:57], v[204:205]\n\t"
"v_pk_fma_f32 v[208:209], v[204:205], s[58:59], v[208:209] neg_lo:[0,0,1] neg_hi:[0,0,1]\n\t"
"v_pk_fma_f32 v[226:227], v[6:7], s[60:61], v[204:205]\n\t"
"v_pk_fma_f32 v[6:7], v[204:205], s[60:61], v[6:7]\n\t"
"v_pk_fma_f32 v[204:205], v[104:105], s[62:63], v[208:209]\n\t"
"v_pk_fma_f32 v[104:105], v[208:209], s[62:63], v[104:105]\n\t"
"v_cvt_pk_bf16_f32 v203, v204, v205\n\t"
"v_mfma_f32_16x16x32_bf16 a[20:23], a[244:247], a[188:191], a[20:23]\n\t"
"v_cvt_pk_bf16_f32 v207, v226, v227\n\t"
"v_mfma_f32_16x16x32_bf16 a[20:23], a[248:251], a[192:195], a[20:23]\n\t"
"v_accvgpr_read_b32 v222, a0\n\t"
"v_mfma_f32_16x16x32_bf16 a[20:23], a[208:211], a[196:199], a[20:23]\n\t"
"v_pk_fma_f32 v[210:211], v[214:215], s[56:57], v[210:211]\n\t"
"v_pk_fma_f32 v[214:215], v[210:211], s[58:59], v[214:215] neg_lo:[0,0,1] neg_hi:[0,0,1]\n\t"
"v_pk_fma_f32 v[232:233], v[8:9], s[60:61], v[210:211]\n\t"
"v_pk_fma_f32 v[8:9], v[210:211], s[60:61], v[8:9]\n\t"
"v_pk_fma_f32 v[210:211], v[106:107], s[62:63], v[214:215]\n\t"
"v_pk_fma_f32 v[106:107], v[214:215], s[62:63], v[106:107]\n\t"
"v_cvt_pk_bf16_f32 v210, v210, v211\n\t"
"v_cvt_pk_bf16_f32 v214, v232, v233\n\t"
"v_mfma_f32_16x16x32_bf16 a[28:31], a[244:247], a[192:195], a[28:31]\n\t"
"v_accvgpr_read_b32 v224, a4\n\t"
"v_mfma_f32_16x16x32_bf16 a[28:31], a[248:251], a[196:199], a[28:31]\n\t"
"v_accvgpr_read_b32 v223, a1\n\t"
"v_accvgpr_read_b32 v225, a5\n\t"
"v_mfma_f32_16x16x32_bf16 a[28:31], a[208:211], a[200:203], a[28:31]\n\t"
"v_pk_fma_f32 v[212:213], v[216:217], s[56:57], v[212:213]\n\t"
"v_pk_fma_f32 v[216:217], v[212:213], s[58:59], v[216:217] neg_lo:[0,0,1] neg_hi:[0,0,1]\n\t"
"v_pk_fma_f32 v[250:251], v[10:11], s[60:61], v[212:213]\n\t"
"v_pk_fma_f32 v[10:11], v[212:213], s[60:61], v[10:11]\n\t"
"v_pk_fma_f32 v[212:213], v[108:109], s[62:63], v[216:217]\n\t"
"v_pk_fma_f32 v[108:109], v[216:217], s[62:63], v[108:109]\n\t"
"v_cvt_pk_bf16_f32 v211, v212, v213\n\t"
"v_mfma_f32_16x16x32_bf16 a[36:39], a[244:247], a[196:199], a[36:39]\n\t"
"v_cvt_pk_bf16_f32 v215, v250, v251\n\t"
"v_accvgpr_read_b32 v228, a2\n\t"
"v_mfma_f32_16x16x32_bf16 a[36:39], a[248:251], a[200:203], a[36:39]\n\t"
"v_accvgpr_read_b32 v230, a6\n\t"
"v_accvgpr_read_b32 v229, a3\n\t"
"v_mfma_f32_16x16x32_bf16 a[36:39], a[208:211], a[204:207], a[36:39]\n\t"
"v_pk_fma_f32 v[222:223], v[224:225], s[56:57], v[222:223]\n\t"
"v_pk_fma_f32 v[224:225], v[222:223], s[58:59], v[224:225] neg_lo:[0,0,1] neg_hi:[0,0,1]\n\t"
"v_pk_fma_f32 v[226:227], v[12:13], s[60:61], v[222:223]\n\t"
"v_pk_fma_f32 v[12:13], v[222:223], s[60:61], v[12:13]\n\t"
"v_pk_fma_f32 v[222:223], v[110:111], s[62:63], v[224:225]\n\t"
"v_pk_fma_f32 v[110:111], v[224:225], s[62:63], v[110:111]\n\t"
"s_mov_b32 s20, 454656\n\t"
"buffer_load_dwordx4 a[244:247], v252, s[8:11], s20 offen offset:0 sc1\n\t"
"s_waitcnt vmcnt(7)\n\t"
"v_mfma_f32_16x16x32_bf16 a[44:47], a[232:235], a[168:171], a[44:47]\n\t"
"v_accvgpr_read_b32 v231, a7\n\t"
"v_cvt_pk_bf16_f32 v222, v222, v223\n\t"
"v_mfma_f32_16x16x32_bf16 a[44:47], a[236:239], a[172:175], a[44:47]\n\t"
"v_cvt_pk_bf16_f32 v226, v226, v227\n\t"
"v_accvgpr_write_b32 a0, v222\n\t"
"v_mfma_f32_16x16x32_bf16 a[44:47], a[252:255], a[176:179], a[44:47]\n\t"
"v_pk_fma_f32 v[228:229], v[230:231], s[56:57], v[228:229]\n\t"
"v_pk_fma_f32 v[230:231], v[228:229], s[58:59], v[230:231] neg_lo:[0,0,1] neg_hi:[0,0,1]\n\t"
"v_pk_fma_f32 v[232:233], v[14:15], s[60:61], v[228:229]\n\t"
"v_pk_fma_f32 v[14:15], v[228:229], s[60:61], v[14:15]\n\t"
"v_pk_fma_f32 v[228:229], v[112:113], s[62:63], v[230:231]\n\t"
"v_pk_fma_f32 v[112:113], v[230:231], s[62:63], v[112:113]\n\t"
"buffer_load_dwordx4 a[248:251], v252, s[8:11], s20 offen offset:1024 sc1\n\t"
"v_accvgpr_write_b32 a4, v226\n\t"
"v_accvgpr_read_b32 v234, a8\n\t"
"v_mfma_f32_16x16x32_bf16 a[52:55], a[232:235], a[172:175], a[52:55]\n\t"
"v_accvgpr_read_b32 v236, a12\n\t"
"v_accvgpr_read_b32 v235, a9\n\t"
"v_mfma_f32_16x16x32_bf16 a[52:55], a[236:239], a[176:179], a[52:55]\n\t"
"v_accvgpr_read_b32 v237, a13\n\t"
"v_cvt_pk_bf16_f32 v228, v228, v229\n\t"
"v_mfma_f32_16x16x32_bf16 a[52:55], a[252:255], a[180:183], a[52:55]\n\t"
"v_cvt_pk_bf16_f32 v232, v232, v233\n\t"
"v_pk_fma_f32 v[234:235], v[236:237], s[56:57], v[234:235]\n\t"
"v_pk_fma_f32 v[236:237], v[234:235], s[58:59], v[236:237] neg_lo:[0,0,1] neg_hi:[0,0,1]\n\t"
"v_pk_fma_f32 v[250:251], v[16:17], s[60:61], v[234:235]\n\t"
"v_pk_fma_f32 v[16:17], v[234:235], s[60:61], v[16:17]\n\t"
"v_pk_fma_f32 v[234:235], v[114:115], s[62:63], v[236:237]\n\t"
"v_pk_fma_f32 v[114:115], v[236:237], s[62:63], v[114:115]\n\t"
"v_accvgpr_write_b32 a1, v228\n\t"
"v_accvgpr_write_b32 a5, v232\n\t"
"v_accvgpr_read_b32 v222, a10\n\t"
"v_mfma_f32_16x16x32_bf16 a[60:63], a[232:235], a[176:179], a[60:63]\n\t"
"v_accvgpr_read_b32 v224, a14\n\t"
"v_accvgpr_read_b32 v223, a11\n\t"
"v_mfma_f32_16x16x32_bf16 a[60:63], a[236:239], a[180:183], a[60:63]\n\t"
"v_accvgpr_read_b32 v225, a15\n\t"
"v_accvgpr_read_b32 v228, a16\n\t"
"v_mfma_f32_16x16x32_bf16 a[60:63], a[252:255], a[184:187], a[60:63]\n\t"
"v_accvgpr_read_b32 v230, a20\n\t"
"v_accvgpr_read_b32 v229, a17\n\t"
"v_accvgpr_read_b32 v231, a21\n\t"
"v_cvt_pk_bf16_f32 v234, v234, v235\n\t"
"v_cvt_pk_bf16_f32 v250, v250, v251\n\t"
"v_pk_fma_f32 v[222:223], v[224:225], s[56:57], v[222:223]\n\t"
"v_pk_fma_f32 v[224:225], v[222:223], s[58:59], v[224:225] neg_lo:[0,0,1] neg_hi:[0,0,1]\n\t"
"v_pk_fma_f32 v[226:227], v[18:19], s[60:61], v[222:223]\n\t"
"v_pk_fma_f32 v[18:19], v[222:223], s[60:61], v[18:19]\n\t"
"v_pk_fma_f32 v[222:223], v[116:117], s[62:63], v[224:225]\n\t"
"v_pk_fma_f32 v[116:117], v[224:225], s[62:63], v[116:117]\n\t"
"v_pk_fma_f32 v[228:229], v[230:231], s[56:57], v[228:229]\n\t"
"v_pk_fma_f32 v[230:231], v[228:229], s[58:59], v[230:231] neg_lo:[0,0,1] neg_hi:[0,0,1]\n\t"
"v_pk_fma_f32 v[232:233], v[20:21], s[60:61], v[228:229]\n\t"
"v_pk_fma_f32 v[20:21], v[228:229], s[60:61], v[20:21]\n\t"
"v_pk_fma_f32 v[228:229], v[118:119], s[62:63], v[230:231]\n\t"
"v_pk_fma_f32 v[118:119], v[230:231], s[62:63], v[118:119]\n\t"
"v_mfma_f32_16x16x32_bf16 a[68:71], a[232:235], a[180:183], a[68:71]\n\t"
"v_accvgpr_write_b32 a8, v234\n\t"
"v_accvgpr_write_b32 a12, v250\n\t"
"v_mfma_f32_16x16x32_bf16 a[68:71], a[236:239], a[184:187], a[68:71]\n\t"
"v_accvgpr_read_b32 v234, a18\n\t"
"v_accvgpr_read_b32 v236, a22\n\t"
"v_mfma_f32_16x16x32_bf16 a[68:71], a[252:255], a[188:191], a[68:71]\n\t"
"v_accvgpr_read_b32 v235, a19\n\t"
"v_accvgpr_read_b32 v237, a23\n\t"
"v_cvt_pk_bf16_f32 v222, v222, v223\n\t"
"v_cvt_pk_bf16_f32 v226, v226, v227\n\t"
"v_cvt_pk_bf16_f32 v228, v228, v229\n\t"
"v_cvt_pk_bf16_f32 v232, v232, v233\n\t"
"v_pk_fma_f32 v[234:235], v[236:237], s[56:57], v[234:235]\n\t"
"v_pk_fma_f32 v[236:237], v[234:235], s[58:59], v[236:237] neg_lo:[0,0,1] neg_hi:[0,0,1]\n\t"
"v_pk_fma_f32 v[250:251], v[22:23], s[60:61], v[234:235]\n\t"
"v_pk_fma_f32 v[22:23], v[234:235], s[60:61], v[22:23]\n\t"
"v_pk_fma_f32 v[234:235], v[120:121], s[62:63], v[236:237]\n\t"
"v_pk_fma_f32 v[120:121], v[236:237], s[62:63], v[120:121]\n\t"
"v_mfma_f32_16x16x32_bf16 a[76:79], a[232:235], a[184:187], a[76:79]\n\t"
"v_accvgpr_write_b32 a9, v222\n\t"
"v_accvgpr_write_b32 a13, v226\n\t"
"v_mfma_f32_16x16x32_bf16 a[76:79], a[236:239], a[188:191], a[76:79]\n\t"
"v_accvgpr_write_b32 a16, v228\n\t"
"v_accvgpr_write_b32 a20, v232\n\t"
"v_mfma_f32_16x16x32_bf16 a[76:79], a[252:255], a[192:195], a[76:79]\n\t"
"v_accvgpr_read_b32 v222, a24\n\t"
"v_accvgpr_read_b32 v224, a28\n\t"
"v_accvgpr_read_b32 v223, a25\n\t"
"v_accvgpr_read_b32 v225, a29\n\t"
"v_cvt_pk_bf16_f32 v234, v234, v235\n\t"
"v_cvt_pk_bf16_f32 v250, v250, v251\n\t"
"v_pk_fma_f32 v[222:223], v[224:225], s[56:57], v[222:223]\n\t"
"v_pk_fma_f32 v[224:225], v[222:223], s[58:59], v[224:225] neg_lo:[0,0,1] neg_hi:[0,0,1]\n\t"
"v_pk_fma_f32 v[226:227], v[24:25], s[60:61], v[222:223]\n\t"
"v_pk_fma_f32 v[24:25], v[222:223], s[60:61], v[24:25]\n\t"
"v_pk_fma_f32 v[222:223], v[122:123], s[62:63], v[224:225]\n\t"
"v_pk_fma_f32 v[122:123], v[224:225], s[62:63], v[122:123]\n\t"
"v_mfma_f32_16x16x32_bf16 a[84:87], a[232:235], a[188:191], a[84:87]\n\t"
"v_accvgpr_write_b32 a17, v234\n\t"
"v_accvgpr_write_b32 a21, v250\n\t"
"v_mfma_f32_16x16x32_bf16 a[84:87], a[236:239], a[192:195], a[84:87]\n\t"
"v_accvgpr_read_b32 v228, a26\n\t"
"v_accvgpr_read_b32 v230, a30\n\t"
"v_mfma_f32_16x16x32_bf16 a[84:87], a[252:255], a[196:199], a[84:87]\n\t"
"v_accvgpr_read_b32 v229, a27\n\t"
"v_accvgpr_read_b32 v231, a31\n\t"
"v_cvt_pk_bf16_f32 v222, v222, v223\n\t"
"v_cvt_pk_bf16_f32 v226, v226, v227\n\t"
"v_pk_fma_f32 v[228:229], v[230:231], s[56:57], v[228:229]\n\t"
"v_pk_fma_f32 v[230:231], v[228:229], s[58:59], v[230:231] neg_lo:[0,0,1] neg_hi:[0,0,1]\n\t"
"v_pk_fma_f32 v[232:233], v[26:27], s[60:61], v[228:229]\n\t"
"v_pk_fma_f32 v[26:27], v[228:229], s[60:61], v[26:27]\n\t"
"v_pk_fma_f32 v[228:229], v[124:125], s[62:63], v[230:231]\n\t"
"v_pk_fma_f32 v[124:125], v[230:231], s[62:63], v[124:125]\n\t"
"v_mfma_f32_16x16x32_bf16 a[92:95], a[232:235], a[192:195], a[92:95]\n\t"
"v_accvgpr_write_b32 a24, v222\n\t"
"v_accvgpr_write_b32 a28, v226\n\t"
"v_mfma_f32_16x16x32_bf16 a[92:95], a[236:239], a[196:199], a[92:95]\n\t"
"v_accvgpr_read_b32 v234, a32\n\t"
"v_accvgpr_read_b32 v236, a36\n\t"
"v_mfma_f32_16x16x32_bf16 a[92:95], a[252:255], a[200:203], a[92:95]\n\t"
"v_accvgpr_read_b32 v235, a33\n\t"
"v_accvgpr_read_b32 v237, a37\n\t"
"v_cvt_pk_bf16_f32 v228, v228, v229\n\t"
"v_cvt_pk_bf16_f32 v232, v232, v233\n\t"
"v_pk_fma_f32 v[234:235], v[236:237], s[56:57], v[234:235]\n\t"
"v_pk_fma_f32 v[236:237], v[234:235], s[58:59], v[236:237] neg_lo:[0,0,1] neg_hi:[0,0,1]\n\t"
"v_pk_fma_f32 v[250:251], v[28:29], s[60:61], v[234:235]\n\t"
"v_pk_fma_f32 v[28:29], v[234:235], s[60:61], v[28:29]\n\t"
"v_pk_fma_f32 v[234:235], v[126:127], s[62:63], v[236:237]\n\t"
"v_pk_fma_f32 v[126:127], v[236:237], s[62:63], v[126:127]\n\t"
"v_mfma_f32_16x16x32_bf16 a[100:103], a[232:235], a[196:199], a[100:103]\n\t"
"v_accvgpr_write_b32 a25, v228\n\t"
"v_accvgpr_write_b32 a29, v232\n\t"
"v_mfma_f32_16x16x32_bf16 a[100:103], a[236:239], a[200:203], a[100:103]\n\t"
"v_accvgpr_read_b32 v222, a34\n\t"
"v_accvgpr_read_b32 v224, a38\n\t"
"v_mfma_f32_16x16x32_bf16 a[100:103], a[252:255], a[204:207], a[100:103]\n\t"
"v_accvgpr_read_b32 v223, a35\n\t"
"v_accvgpr_read_b32 v225, a39\n\t"
"v_cvt_pk_bf16_f32 v234, v234, v235\n\t"
"v_cvt_pk_bf16_f32 v250, v250, v251\n\t"
"v_pk_fma_f32 v[222:223], v[224:225], s[56:57], v[222:223]\n\t"
"v_pk_fma_f32 v[224:225], v[222:223], s[58:59], v[224:225] neg_lo:[0,0,1] neg_hi:[0,0,1]\n\t"
"v_pk_fma_f32 v[226:227], v[30:31], s[60:61], v[222:223]\n\t"
"v_pk_fma_f32 v[30:31], v[222:223], s[60:61], v[30:31]\n\t"
"v_pk_fma_f32 v[222:223], v[128:129], s[62:63], v[224:225]\n\t"
"v_pk_fma_f32 v[128:129], v[224:225], s[62:63], v[128:129]\n\t"
"s_waitcnt vmcnt(5)\n\t"
"v_mfma_f32_16x16x32_bf16 a[108:111], a[240:243], a[168:171], a[108:111]\n\t"
"v_accvgpr_write_b32 a32, v234\n\t"
"v_accvgpr_write_b32 a36, v250\n\t"
"v_mfma_f32_16x16x32_bf16 a[108:111], a[220:223], a[172:175], a[108:111]\n\t"
"v_accvgpr_read_b32 v228, a40\n\t"
"v_accvgpr_read_b32 v230, a44\n\t"
"v_mfma_f32_16x16x32_bf16 a[108:111], a[224:227], a[176:179], a[108:111]\n\t"
"buffer_load_dwordx4 a[232:235], v252, s[8:11], s20 offen offset:2048 sc1\n\t"
"v_accvgpr_read_b32 v229, a41\n\t"
"v_accvgpr_read_b32 v231, a45\n\t"
"v_cvt_pk_bf16_f32 v222, v222, v223\n\t"
"v_cvt_pk_bf16_f32 v226, v226, v227\n\t"
"v_pk_fma_f32 v[228:229], v[230:231], s[56:57], v[228:229]\n\t"
"v_pk_fma_f32 v[230:231], v[228:229], s[58:59], v[230:231] neg_lo:[0,0,1] neg_hi:[0,0,1]\n\t"
"v_pk_fma_f32 v[232:233], v[32:33], s[60:61], v[228:229]\n\t"
"v_pk_fma_f32 v[32:33], v[228:229], s[60:61], v[32:33]\n\t"
"v_pk_fma_f32 v[228:229], v[130:131], s[62:63], v[230:231]\n\t"
"v_pk_fma_f32 v[130:131], v[230:231], s[62:63], v[130:131]\n\t"
"v_mfma_f32_16x16x32_bf16 a[116:119], a[240:243], a[172:175], a[116:119]\n\t"
"v_accvgpr_write_b32 a33, v222\n\t"
"v_accvgpr_write_b32 a37, v226\n\t"
"v_mfma_f32_16x16x32_bf16 a[116:119], a[220:223], a[176:179], a[116:119]\n\t"
"v_accvgpr_read_b32 v234, a42\n\t"
"v_accvgpr_read_b32 v236, a46\n\t"
"v_mfma_f32_16x16x32_bf16 a[116:119], a[224:227], a[180:183], a[116:119]\n\t"
"s_mov_b32 s20, 466944\n\t"
"buffer_load_dwordx4 a[236:239], v252, s[8:11], s20 offen offset:0 sc1\n\t"
"v_accvgpr_read_b32 v235, a43\n\t"
"v_accvgpr_read_b32 v237, a47\n\t"
"v_pk_fma_f32 v[234:235], v[236:237], s[56:57], v[234:235]\n\t"
"v_pk_fma_f32 v[236:237], v[234:235], s[58:59], v[236:237] neg_lo:[0,0,1] neg_hi:[0,0,1]\n\t"
"v_pk_fma_f32 v[250:251], v[34:35], s[60:61], v[234:235]\n\t"
"v_pk_fma_f32 v[34:35], v[234:235], s[60:61], v[34:35]\n\t"
"v_pk_fma_f32 v[234:235], v[132:133], s[62:63], v[236:237]\n\t"
"v_pk_fma_f32 v[132:133], v[236:237], s[62:63], v[132:133]\n\t"
"v_mfma_f32_16x16x32_bf16 a[124:127], a[240:243], a[176:179], a[124:127]\n\t"
"v_cvt_pk_bf16_f32 v196, v228, v229\n\t"
"v_cvt_pk_bf16_f32 v200, v232, v233\n\t"
"v_mfma_f32_16x16x32_bf16 a[124:127], a[220:223], a[180:183], a[124:127]\n\t"
"v_accvgpr_read_b32 v222, a48\n\t"
"v_accvgpr_read_b32 v224, a52\n\t"
"v_mfma_f32_16x16x32_bf16 a[124:127], a[224:227], a[184:187], a[124:127]\n\t"
"buffer_load_dwordx4 a[252:255], v252, s[8:11], s20 offen offset:1024 sc1\n\t"
"v_accvgpr_read_b32 v223, a49\n\t"
"v_accvgpr_read_b32 v225, a53\n\t"
"v_accvgpr_read_b32 v228, a50\n\t"
"v_accvgpr_read_b32 v230, a54\n\t"
"v_accvgpr_read_b32 v229, a51\n\t"
"v_accvgpr_read_b32 v231, a55\n\t"
"v_pk_fma_f32 v[222:223], v[224:225], s[56:57], v[222:223]\n\t"
"v_pk_fma_f32 v[224:225], v[222:223], s[58:59], v[224:225] neg_lo:[0,0,1] neg_hi:[0,0,1]\n\t"
"v_pk_fma_f32 v[226:227], v[36:37], s[60:61], v[222:223]\n\t"
"v_pk_fma_f32 v[36:37], v[222:223], s[60:61], v[36:37]\n\t"
"v_pk_fma_f32 v[222:223], v[134:135], s[62:63], v[224:225]\n\t"
"v_pk_fma_f32 v[134:135], v[224:225], s[62:63], v[134:135]\n\t"
"v_pk_fma_f32 v[228:229], v[230:231], s[56:57], v[228:229]\n\t"
"v_pk_fma_f32 v[230:231], v[228:229], s[58:59], v[230:231] neg_lo:[0,0,1] neg_hi:[0,0,1]\n\t"
"v_pk_fma_f32 v[232:233], v[38:39], s[60:61], v[228:229]\n\t"
"v_pk_fma_f32 v[38:39], v[228:229], s[60:61], v[38:39]\n\t"
"v_pk_fma_f32 v[228:229], v[136:137], s[62:63], v[230:231]\n\t"
"v_pk_fma_f32 v[136:137], v[230:231], s[62:63], v[136:137]\n\t"
"v_mfma_f32_16x16x32_bf16 a[132:135], a[240:243], a[180:183], a[132:135]\n\t"
"v_cvt_pk_bf16_f32 v197, v234, v235\n\t"
"v_cvt_pk_bf16_f32 v201, v250, v251\n\t"
"v_mfma_f32_16x16x32_bf16 a[132:135], a[220:223], a[184:187], a[132:135]\n\t"
"ds_read_b128 a[168:171], v242 offset:18432\n\t"
"v_accvgpr_read_b32 v234, a56\n\t"
"v_mfma_f32_16x16x32_bf16 a[132:135], a[224:227], a[188:191], a[132:135]\n\t"
"ds_read_b128 a[172:175], v242 offset:18944\n\t"
"ds_read_b128 a[176:179], v242 offset:19456\n\t"
"v_accvgpr_read_b32 v236, a60\n\t"
"v_accvgpr_read_b32 v235, a57\n\t"
"v_accvgpr_read_b32 v237, a61\n\t"
"buffer_load_dwordx4 a[40:43], v252, s[8:11], s20 offen offset:2048 sc1\n\t"
"v_pk_fma_f32 v[234:235], v[236:237], s[56:57], v[234:235]\n\t"
"v_pk_fma_f32 v[236:237], v[234:235], s[58:59], v[236:237] neg_lo:[0,0,1] neg_hi:[0,0,1]\n\t"
"v_pk_fma_f32 v[250:251], v[40:41], s[60:61], v[234:235]\n\t"
"v_pk_fma_f32 v[40:41], v[234:235], s[60:61], v[40:41]\n\t"
"v_pk_fma_f32 v[234:235], v[138:139], s[62:63], v[236:237]\n\t"
"v_pk_fma_f32 v[138:139], v[236:237], s[62:63], v[138:139]\n\t"
"v_mfma_f32_16x16x32_bf16 a[140:143], a[240:243], a[184:187], a[140:143]\n\t"
"v_cvt_pk_bf16_f32 v204, v222, v223\n\t"
"v_cvt_pk_bf16_f32 v208, v226, v227\n\t"
"v_mfma_f32_16x16x32_bf16 a[140:143], a[220:223], a[188:191], a[140:143]\n\t"
"v_cvt_pk_bf16_f32 v205, v228, v229\n\t"
"v_cvt_pk_bf16_f32 v209, v232, v233\n\t"
"v_mfma_f32_16x16x32_bf16 a[140:143], a[224:227], a[192:195], a[140:143]\n\t"
"ds_read_b128 a[180:183], v242 offset:19968\n\t"
"v_accvgpr_read_b32 v222, a58\n\t"
"v_accvgpr_read_b32 v224, a62\n\t"
"v_accvgpr_read_b32 v223, a59\n\t"
"v_accvgpr_read_b32 v225, a63\n\t"
"s_mov_b32 s20, 626688\n\t"
"buffer_load_dwordx4 a[48:51], v252, s[8:11], s20 offen offset:0 sc1\n\t"
"v_pk_fma_f32 v[222:223], v[224:225], s[56:57], v[222:223]\n\t"
"v_pk_fma_f32 v[224:225], v[222:223], s[58:59], v[224:225] neg_lo:[0,0,1] neg_hi:[0,0,1]\n\t"
"v_pk_fma_f32 v[226:227], v[42:43], s[60:61], v[222:223]\n\t"
"v_pk_fma_f32 v[42:43], v[222:223], s[60:61], v[42:43]\n\t"
"v_pk_fma_f32 v[222:223], v[140:141], s[62:63], v[224:225]\n\t"
"v_pk_fma_f32 v[140:141], v[224:225], s[62:63], v[140:141]\n\t"
"v_mfma_f32_16x16x32_bf16 a[148:151], a[240:243], a[188:191], a[148:151]\n\t"
"v_cvt_pk_bf16_f32 v212, v234, v235\n\t"
"v_cvt_pk_bf16_f32 v216, v250, v251\n\t"
"v_mfma_f32_16x16x32_bf16 a[148:151], a[220:223], a[192:195], a[148:151]\n\t"
"ds_read_b128 a[184:187], v242 offset:20480\n\t"
"v_accvgpr_read_b32 v228, a64\n\t"
"v_mfma_f32_16x16x32_bf16 a[148:151], a[224:227], a[196:199], a[148:151]\n\t"
"v_accvgpr_read_b32 v230, a68\n\t"
"v_accvgpr_read_b32 v229, a65\n\t"
"v_accvgpr_read_b32 v231, a69\n\t"
"buffer_load_dwordx4 a[52:55], v252, s[8:11], s20 offen offset:1024 sc1\n\t"
"v_pk_fma_f32 v[228:229], v[230:231], s[56:57], v[228:229]\n\t"
"v_pk_fma_f32 v[230:231], v[228:229], s[58:59], v[230:231] neg_lo:[0,0,1] neg_hi:[0,0,1]\n\t"
"v_pk_fma_f32 v[232:233], v[44:45], s[60:61], v[228:229]\n\t"
"v_pk_fma_f32 v[44:45], v[228:229], s[60:61], v[44:45]\n\t"
"v_pk_fma_f32 v[228:229], v[142:143], s[62:63], v[230:231]\n\t"
"v_pk_fma_f32 v[142:143], v[230:231], s[62:63], v[142:143]\n\t"
"v_mfma_f32_16x16x32_bf16 a[156:159], a[240:243], a[192:195], a[156:159]\n\t"
"v_cvt_pk_bf16_f32 v213, v222, v223\n\t"
"v_cvt_pk_bf16_f32 v217, v226, v227\n\t"
"v_mfma_f32_16x16x32_bf16 a[156:159], a[220:223], a[196:199], a[156:159]\n\t"
"ds_read_b128 a[188:191], v242 offset:20992\n\t"
"v_accvgpr_read_b32 v234, a66\n\t"
"v_mfma_f32_16x16x32_bf16 a[156:159], a[224:227], a[200:203], a[156:159]\n\t"
"v_accvgpr_read_b32 v236, a70\n\t"
"v_accvgpr_read_b32 v235, a67\n\t"
"v_accvgpr_read_b32 v237, a71\n\t"
"buffer_load_dwordx4 a[56:59], v252, s[8:11], s20 offen offset:2048 sc1\n\t"
"v_cvt_pk_bf16_f32 v228, v228, v229\n\t"
"v_cvt_pk_bf16_f32 v232, v232, v233\n\t"
"v_pk_fma_f32 v[234:235], v[236:237], s[56:57], v[234:235]\n\t"
"v_pk_fma_f32 v[236:237], v[234:235], s[58:59], v[236:237] neg_lo:[0,0,1] neg_hi:[0,0,1]\n\t"
"v_pk_fma_f32 v[250:251], v[46:47], s[60:61], v[234:235]\n\t"
"v_pk_fma_f32 v[46:47], v[234:235], s[60:61], v[46:47]\n\t"
"v_pk_fma_f32 v[234:235], v[144:145], s[62:63], v[236:237]\n\t"
"v_pk_fma_f32 v[144:145], v[236:237], s[62:63], v[144:145]\n\t"
"v_mfma_f32_16x16x32_bf16 a[164:167], a[240:243], a[196:199], a[164:167]\n\t"
"ds_read_b128 a[192:195], v242 offset:21504\n\t"
"v_accvgpr_write_b32 a2, v228\n\t"
"v_mfma_f32_16x16x32_bf16 a[164:167], a[220:223], a[200:203], a[164:167]\n\t"
"v_accvgpr_write_b32 a6, v232\n\t"
"v_accvgpr_read_b32 v222, a72\n\t"
"v_mfma_f32_16x16x32_bf16 a[164:167], a[224:227], a[204:207], a[164:167]\n\t"
"v_accvgpr_read_b32 v224, a76\n\t"
"v_accvgpr_read_b32 v223, a73\n\t"
"v_accvgpr_read_b32 v225, a77\n\t"
"s_mov_b32 s20, 638976\n\t"
"buffer_load_dwordx4 a[44:47], v252, s[8:11], s20 offen offset:0 sc1\n\t"
"v_cvt_pk_bf16_f32 v234, v234, v235\n\t"
"v_cvt_pk_bf16_f32 v250, v250, v251\n\t"
"v_pk_fma_f32 v[222:223], v[224:225], s[56:57], v[222:223]\n\t"
"v_pk_fma_f32 v[224:225], v[222:223], s[58:59], v[224:225] neg_lo:[0,0,1] neg_hi:[0,0,1]\n\t"
"v_pk_fma_f32 v[226:227], v[48:49], s[60:61], v[222:223]\n\t"
"v_pk_fma_f32 v[48:49], v[222:223], s[60:61], v[48:49]\n\t"
"v_pk_fma_f32 v[222:223], v[146:147], s[62:63], v[224:225]\n\t"
"v_pk_fma_f32 v[146:147], v[224:225], s[62:63], v[146:147]\n\t"
"s_waitcnt vmcnt(10) lgkmcnt(4)\n\t"
"v_mfma_f32_16x16x32_bf16 v[0:3], a[212:215], a[168:171], v[0:3]\n\t"
"ds_read_b128 a[196:199], v242 offset:22016\n\t"
"v_accvgpr_write_b32 a3, v234\n\t"
"v_mfma_f32_16x16x32_bf16 v[0:3], a[216:219], a[172:175], v[0:3]\n\t"
"v_accvgpr_write_b32 a7, v250\n\t"
"v_accvgpr_read_b32 v228, a74\n\t"
"v_mfma_f32_16x16x32_bf16 v[0:3], a[228:231], a[176:179], v[0:3]\n\t"
"v_accvgpr_read_b32 v230, a78\n\t"
"v_accvgpr_read_b32 v229, a75\n\t"
"v_accvgpr_read_b32 v231, a79\n\t"
"buffer_load_dwordx4 a[64:67], v252, s[8:11], s20 offen offset:1024 sc1\n\t"
"v_cvt_pk_bf16_f32 v222, v222, v223\n\t"
"v_cvt_pk_bf16_f32 v226, v226, v227\n\t"
"v_pk_fma_f32 v[228:229], v[230:231], s[56:57], v[228:229]\n\t"
"v_pk_fma_f32 v[230:231], v[228:229], s[58:59], v[230:231] neg_lo:[0,0,1] neg_hi:[0,0,1]\n\t"
"v_pk_fma_f32 v[232:233], v[50:51], s[60:61], v[228:229]\n\t"
"v_pk_fma_f32 v[50:51], v[228:229], s[60:61], v[50:51]\n\t"
"v_pk_fma_f32 v[228:229], v[148:149], s[62:63], v[230:231]\n\t"
"v_pk_fma_f32 v[148:149], v[230:231], s[62:63], v[148:149]\n\t"
"s_waitcnt lgkmcnt(4)\n\t"
"v_mfma_f32_16x16x32_bf16 v[4:7], a[212:215], a[172:175], v[4:7]\n\t"
"ds_read_b128 a[200:203], v242 offset:22528\n\t"
"v_accvgpr_write_b32 a10, v222\n\t"
"v_mfma_f32_16x16x32_bf16 v[4:7], a[216:219], a[176:179], v[4:7]\n\t"
"v_accvgpr_write_b32 a14, v226\n\t"
"v_accvgpr_read_b32 v234, a80\n\t"
"v_mfma_f32_16x16x32_bf16 v[4:7], a[228:231], a[180:183], v[4:7]\n\t"
"v_accvgpr_read_b32 v236, a84\n\t"
"v_accvgpr_read_b32 v235, a81\n\t"
"v_accvgpr_read_b32 v237, a85\n\t"
"buffer_load_dwordx4 a[60:63], v252, s[8:11], s20 offen offset:2048 sc1\n\t"
"v_cvt_pk_bf16_f32 v228, v228, v229\n\t"
"v_cvt_pk_bf16_f32 v232, v232, v233\n\t"
"v_pk_fma_f32 v[234:235], v[236:237], s[56:57], v[234:235]\n\t"
"v_pk_fma_f32 v[236:237], v[234:235], s[58:59], v[236:237] neg_lo:[0,0,1] neg_hi:[0,0,1]\n\t"
"v_pk_fma_f32 v[250:251], v[52:53], s[60:61], v[234:235]\n\t"
"v_pk_fma_f32 v[52:53], v[234:235], s[60:61], v[52:53]\n\t"
"v_pk_fma_f32 v[234:235], v[150:151], s[62:63], v[236:237]\n\t"
"v_pk_fma_f32 v[150:151], v[236:237], s[62:63], v[150:151]\n\t"
"s_waitcnt lgkmcnt(4)\n\t"
"v_mfma_f32_16x16x32_bf16 v[8:11], a[212:215], a[176:179], v[8:11]\n\t"
"ds_read_b128 a[204:207], v242 offset:23040\n\t"
"v_accvgpr_write_b32 a11, v228\n\t"
"v_mfma_f32_16x16x32_bf16 v[8:11], a[216:219], a[180:183], v[8:11]\n\t"
"v_accvgpr_write_b32 a15, v232\n\t"
"v_accvgpr_read_b32 v222, a82\n\t"
"v_mfma_f32_16x16x32_bf16 v[8:11], a[228:231], a[184:187], v[8:11]\n\t"
"v_accvgpr_read_b32 v224, a86\n\t"
"v_accvgpr_read_b32 v223, a83\n\t"
"v_accvgpr_read_b32 v225, a87\n\t"
"s_mov_b32 s20, 651264\n\t"
"buffer_load_dwordx4 a[72:75], v252, s[8:11], s20 offen offset:0 sc1\n\t"
"v_cvt_pk_bf16_f32 v234, v234, v235\n\t"
"v_cvt_pk_bf16_f32 v250, v250, v251\n\t"
"v_pk_fma_f32 v[222:223], v[224:225], s[56:57], v[222:223]\n\t"
"v_pk_fma_f32 v[224:225], v[222:223], s[58:59], v[224:225] neg_lo:[0,0,1] neg_hi:[0,0,1]\n\t"
"v_pk_fma_f32 v[226:227], v[54:55], s[60:61], v[222:223]\n\t"
"v_pk_fma_f32 v[54:55], v[222:223], s[60:61], v[54:55]\n\t"
"v_pk_fma_f32 v[222:223], v[152:153], s[62:63], v[224:225]\n\t"
"v_pk_fma_f32 v[152:153], v[224:225], s[62:63], v[152:153]\n\t"
"s_waitcnt lgkmcnt(4)\n\t"
"v_mfma_f32_16x16x32_bf16 v[12:15], a[212:215], a[180:183], v[12:15]\n\t"
"v_accvgpr_write_b32 a18, v234\n\t"
"v_accvgpr_write_b32 a22, v250\n\t"
"v_mfma_f32_16x16x32_bf16 v[12:15], a[216:219], a[184:187], v[12:15]\n\t"
"v_accvgpr_read_b32 v228, a88\n\t"
"v_accvgpr_read_b32 v230, a92\n\t"
"v_mfma_f32_16x16x32_bf16 v[12:15], a[228:231], a[188:191], v[12:15]\n\t"
"v_accvgpr_read_b32 v229, a89\n\t"
"v_accvgpr_read_b32 v231, a93\n\t"
"v_accvgpr_read_b32 v234, a90\n\t"
"v_accvgpr_read_b32 v236, a94\n\t"
"v_accvgpr_read_b32 v235, a91\n\t"
"v_accvgpr_read_b32 v237, a95\n\t"
"buffer_load_dwordx4 a[68:71], v252, s[8:11], s20 offen offset:1024 sc1\n\t"
"v_cvt_pk_bf16_f32 v222, v222, v223\n\t"
"v_cvt_pk_bf16_f32 v226, v226, v227\n\t"
"v_pk_fma_f32 v[228:229], v[230:231], s[56:57], v[228:229]\n\t"
"v_pk_fma_f32 v[230:231], v[228:229], s[58:59], v[230:231] neg_lo:[0,0,1] neg_hi:[0,0,1]\n\t"
"v_pk_fma_f32 v[232:233], v[56:57], s[60:61], v[228:229]\n\t"
"v_pk_fma_f32 v[56:57], v[228:229], s[60:61], v[56:57]\n\t"
"v_pk_fma_f32 v[228:229], v[154:155], s[62:63], v[230:231]\n\t"
"v_pk_fma_f32 v[154:155], v[230:231], s[62:63], v[154:155]\n\t"
"v_pk_fma_f32 v[234:235], v[236:237], s[56:57], v[234:235]\n\t"
"v_pk_fma_f32 v[236:237], v[234:235], s[58:59], v[236:237] neg_lo:[0,0,1] neg_hi:[0,0,1]\n\t"
"v_pk_fma_f32 v[250:251], v[58:59], s[60:61], v[234:235]\n\t"
"v_pk_fma_f32 v[58:59], v[234:235], s[60:61], v[58:59]\n\t"
"v_pk_fma_f32 v[234:235], v[156:157], s[62:63], v[236:237]\n\t"
"v_pk_fma_f32 v[156:157], v[236:237], s[62:63], v[156:157]\n\t"
"s_waitcnt lgkmcnt(3)\n\t"
"v_mfma_f32_16x16x32_bf16 v[16:19], a[212:215], a[184:187], v[16:19]\n\t"
"v_accvgpr_write_b32 a19, v222\n\t"
"v_accvgpr_write_b32 a23, v226\n\t"
"v_mfma_f32_16x16x32_bf16 v[16:19], a[216:219], a[188:191], v[16:19]\n\t"
"v_accvgpr_read_b32 v222, a96\n\t"
"v_accvgpr_read_b32 v224, a100\n\t"
"v_mfma_f32_16x16x32_bf16 v[16:19], a[228:231], a[192:195], v[16:19]\n\t"
"v_accvgpr_read_b32 v223, a97\n\t"
"v_accvgpr_read_b32 v225, a101\n\t"
"buffer_load_dwordx4 a[80:83], v252, s[8:11], s20 offen offset:2048 sc1\n\t"
"v_cvt_pk_bf16_f32 v228, v228, v229\n\t"
"v_cvt_pk_bf16_f32 v232, v232, v233\n\t"
"v_pk_fma_f32 v[222:223], v[224:225], s[56:57], v[222:223]\n\t"
"v_pk_fma_f32 v[224:225], v[222:223], s[58:59], v[224:225] neg_lo:[0,0,1] neg_hi:[0,0,1]\n\t"
"v_pk_fma_f32 v[226:227], v[60:61], s[60:61], v[222:223]\n\t"
"v_pk_fma_f32 v[60:61], v[222:223], s[60:61], v[60:61]\n\t"
"v_pk_fma_f32 v[222:223], v[158:159], s[62:63], v[224:225]\n\t"
"v_pk_fma_f32 v[158:159], v[224:225], s[62:63], v[158:159]\n\t"
"s_waitcnt lgkmcnt(2)\n\t"
"v_mfma_f32_16x16x32_bf16 v[20:23], a[212:215], a[188:191], v[20:23]\n\t"
"v_accvgpr_write_b32 a26, v228\n\t"
"v_cvt_pk_bf16_f32 v234, v234, v235\n\t"
"v_mfma_f32_16x16x32_bf16 v[20:23], a[216:219], a[192:195], v[20:23]\n\t"
"v_accvgpr_write_b32 a30, v232\n\t"
"v_cvt_pk_bf16_f32 v250, v250, v251\n\t"
"v_mfma_f32_16x16x32_bf16 v[20:23], a[228:231], a[196:199], v[20:23]\n\t"
"v_accvgpr_write_b32 a27, v234\n\t"
"v_accvgpr_write_b32 a31, v250\n\t"
"v_accvgpr_read_b32 v228, a98\n\t"
"v_accvgpr_read_b32 v230, a102\n\t"
"v_accvgpr_read_b32 v229, a99\n\t"
"v_accvgpr_read_b32 v231, a103\n\t"
"s_mov_b32 s20, 663552\n\t"
"buffer_load_dwordx4 a[92:95], v252, s[8:11], s20 offen offset:0 sc1\n\t"
"v_cvt_pk_bf16_f32 v222, v222, v223\n\t"
"v_cvt_pk_bf16_f32 v226, v226, v227\n\t"
"v_pk_fma_f32 v[228:229], v[230:231], s[56:57], v[228:229]\n\t"
"v_pk_fma_f32 v[230:231], v[228:229], s[58:59], v[230:231] neg_lo:[0,0,1] neg_hi:[0,0,1]\n\t"
"v_pk_fma_f32 v[232:233], v[62:63], s[60:61], v[228:229]\n\t"
"v_pk_fma_f32 v[62:63], v[228:229], s[60:61], v[62:63]\n\t"
"v_pk_fma_f32 v[228:229], v[160:161], s[62:63], v[230:231]\n\t"
"v_pk_fma_f32 v[160:161], v[230:231], s[62:63], v[160:161]\n\t"
"s_waitcnt lgkmcnt(1)\n\t"
"v_mfma_f32_16x16x32_bf16 v[24:27], a[212:215], a[192:195], v[24:27]\n\t"
"v_accvgpr_write_b32 a34, v222\n\t"
"v_accvgpr_write_b32 a38, v226\n\t"
"v_mfma_f32_16x16x32_bf16 v[24:27], a[216:219], a[196:199], v[24:27]\n\t"
"v_accvgpr_read_b32 v234, a104\n\t"
"v_accvgpr_read_b32 v236, a108\n\t"
"v_mfma_f32_16x16x32_bf16 v[24:27], a[228:231], a[200:203], v[24:27]\n\t"
"v_accvgpr_read_b32 v235, a105\n\t"
"v_accvgpr_read_b32 v237, a109\n\t"
"buffer_load_dwordx4 a[88:91], v252, s[8:11], s20 offen offset:1024 sc1\n\t"
"v_cvt_pk_bf16_f32 v228, v228, v229\n\t"
"v_cvt_pk_bf16_f32 v232, v232, v233\n\t"
"v_pk_fma_f32 v[234:235], v[236:237], s[56:57], v[234:235]\n\t"
"v_pk_fma_f32 v[236:237], v[234:235], s[58:59], v[236:237] neg_lo:[0,0,1] neg_hi:[0,0,1]\n\t"
"v_pk_fma_f32 v[250:251], v[64:65], s[60:61], v[234:235]\n\t"
"v_pk_fma_f32 v[64:65], v[234:235], s[60:61], v[64:65]\n\t"
"v_pk_fma_f32 v[234:235], v[162:163], s[62:63], v[236:237]\n\t"
"v_pk_fma_f32 v[162:163], v[236:237], s[62:63], v[162:163]\n\t"
"s_waitcnt lgkmcnt(0)\n\t"
"v_mfma_f32_16x16x32_bf16 v[28:31], a[212:215], a[196:199], v[28:31]\n\t"
"v_accvgpr_write_b32 a35, v228\n\t"
"v_accvgpr_write_b32 a39, v232\n\t"
"v_mfma_f32_16x16x32_bf16 v[28:31], a[216:219], a[200:203], v[28:31]\n\t"
"v_accvgpr_read_b32 v222, a106\n\t"
"v_accvgpr_read_b32 v224, a110\n\t"
"v_mfma_f32_16x16x32_bf16 v[28:31], a[228:231], a[204:207], v[28:31]\n\t"
"v_accvgpr_read_b32 v223, a107\n\t"
"v_accvgpr_read_b32 v225, a111\n\t"
"buffer_load_dwordx4 a[96:99], v252, s[8:11], s20 offen offset:2048 sc1\n\t"
"v_cvt_pk_bf16_f32 v234, v234, v235\n\t"
"v_cvt_pk_bf16_f32 v250, v250, v251\n\t"
"v_pk_fma_f32 v[222:223], v[224:225], s[56:57], v[222:223]\n\t"
"v_pk_fma_f32 v[224:225], v[222:223], s[58:59], v[224:225] neg_lo:[0,0,1] neg_hi:[0,0,1]\n\t"
"v_pk_fma_f32 v[226:227], v[66:67], s[60:61], v[222:223]\n\t"
"v_pk_fma_f32 v[66:67], v[222:223], s[60:61], v[66:67]\n\t"
"v_pk_fma_f32 v[222:223], v[164:165], s[62:63], v[224:225]\n\t"
"v_pk_fma_f32 v[164:165], v[224:225], s[62:63], v[164:165]\n\t"
"s_waitcnt vmcnt(15)\n\t"
"v_mfma_f32_16x16x32_bf16 v[32:35], a[244:247], a[168:171], v[32:35]\n\t"
"v_accvgpr_write_b32 a104, v234\n\t"
"v_accvgpr_write_b32 a106, v250\n\t"
"v_mfma_f32_16x16x32_bf16 v[32:35], a[248:251], a[172:175], v[32:35]\n\t"
"v_accvgpr_read_b32 v228, a112\n\t"
"v_accvgpr_read_b32 v230, a116\n\t"
"v_mfma_f32_16x16x32_bf16 v[32:35], a[232:235], a[176:179], v[32:35]\n\t"
"v_accvgpr_read_b32 v229, a113\n\t"
"v_accvgpr_read_b32 v231, a117\n\t"
"s_mov_b32 s20, 675840\n\t"
"buffer_load_dwordx4 a[76:79], v252, s[8:11], s20 offen offset:0 sc1\n\t"
"v_cvt_pk_bf16_f32 v222, v222, v223\n\t"
"v_cvt_pk_bf16_f32 v226, v226, v227\n\t"
"v_pk_fma_f32 v[228:229], v[230:231], s[56:57], v[228:229]\n\t"
"v_pk_fma_f32 v[230:231], v[228:229], s[58:59], v[230:231] neg_lo:[0,0,1] neg_hi:[0,0,1]\n\t"
"v_pk_fma_f32 v[232:233], v[68:69], s[60:61], v[228:229]\n\t"
"v_pk_fma_f32 v[68:69], v[228:229], s[60:61], v[68:69]\n\t"
"v_pk_fma_f32 v[228:229], v[166:167], s[62:63], v[230:231]\n\t"
"v_pk_fma_f32 v[166:167], v[230:231], s[62:63], v[166:167]\n\t"
"v_mfma_f32_16x16x32_bf16 v[36:39], a[244:247], a[172:175], v[36:39]\n\t"
"v_accvgpr_write_b32 a105, v222\n\t"
"v_accvgpr_write_b32 a107, v226\n\t"
"v_mfma_f32_16x16x32_bf16 v[36:39], a[248:251], a[176:179], v[36:39]\n\t"
"v_accvgpr_read_b32 v234, a114\n\t"
"v_accvgpr_read_b32 v236, a118\n\t"
"v_mfma_f32_16x16x32_bf16 v[36:39], a[232:235], a[180:183], v[36:39]\n\t"
"v_accvgpr_read_b32 v235, a115\n\t"
"v_accvgpr_read_b32 v237, a119\n\t"
"buffer_load_dwordx4 a[108:111], v252, s[8:11], s20 offen offset:1024 sc1\n\t"
"v_cvt_pk_bf16_f32 v228, v228, v229\n\t"
"v_cvt_pk_bf16_f32 v232, v232, v233\n\t"
"v_pk_fma_f32 v[234:235], v[236:237], s[56:57], v[234:235]\n\t"
"v_pk_fma_f32 v[236:237], v[234:235], s[58:59], v[236:237] neg_lo:[0,0,1] neg_hi:[0,0,1]\n\t"
"v_pk_fma_f32 v[250:251], v[70:71], s[60:61], v[234:235]\n\t"
"v_pk_fma_f32 v[70:71], v[234:235], s[60:61], v[70:71]\n\t"
"v_pk_fma_f32 v[234:235], v[168:169], s[62:63], v[236:237]\n\t"
"v_pk_fma_f32 v[168:169], v[236:237], s[62:63], v[168:169]\n\t"
"v_mfma_f32_16x16x32_bf16 v[40:43], a[244:247], a[176:179], v[40:43]\n\t"
"v_accvgpr_write_b32 a112, v228\n\t"
"v_accvgpr_write_b32 a114, v232\n\t"
"v_mfma_f32_16x16x32_bf16 v[40:43], a[248:251], a[180:183], v[40:43]\n\t"
"v_accvgpr_read_b32 v222, a120\n\t"
"v_accvgpr_read_b32 v224, a124\n\t"
"v_mfma_f32_16x16x32_bf16 v[40:43], a[232:235], a[184:187], v[40:43]\n\t"
"v_accvgpr_read_b32 v223, a121\n\t"
"v_accvgpr_read_b32 v225, a125\n\t"
"buffer_load_dwordx4 a[84:87], v252, s[8:11], s20 offen offset:2048 sc1\n\t"
"v_cvt_pk_bf16_f32 v234, v234, v235\n\t"
"v_cvt_pk_bf16_f32 v250, v250, v251\n\t"
"v_pk_fma_f32 v[222:223], v[224:225], s[56:57], v[222:223]\n\t"
"v_pk_fma_f32 v[224:225], v[222:223], s[58:59], v[224:225] neg_lo:[0,0,1] neg_hi:[0,0,1]\n\t"
"v_pk_fma_f32 v[226:227], v[72:73], s[60:61], v[222:223]\n\t"
"v_pk_fma_f32 v[72:73], v[222:223], s[60:61], v[72:73]\n\t"
"v_pk_fma_f32 v[222:223], v[170:171], s[62:63], v[224:225]\n\t"
"v_pk_fma_f32 v[170:171], v[224:225], s[62:63], v[170:171]\n\t"
"v_mfma_f32_16x16x32_bf16 v[44:47], a[244:247], a[180:183], v[44:47]\n\t"
"v_accvgpr_write_b32 a113, v234\n\t"
"v_accvgpr_write_b32 a115, v250\n\t"
"v_mfma_f32_16x16x32_bf16 v[44:47], a[248:251], a[184:187], v[44:47]\n\t"
"v_accvgpr_read_b32 v228, a122\n\t"
"v_accvgpr_read_b32 v230, a126\n\t"
"v_mfma_f32_16x16x32_bf16 v[44:47], a[232:235], a[188:191], v[44:47]\n\t"
"v_accvgpr_read_b32 v229, a123\n\t"
"v_accvgpr_read_b32 v231, a127\n\t"
"v_accvgpr_read_b32 v234, a128\n\t"
"v_accvgpr_read_b32 v236, a132\n\t"
"v_accvgpr_read_b32 v235, a129\n\t"
"v_accvgpr_read_b32 v237, a133\n\t"
"s_mov_b32 s20, 688128\n\t"
"buffer_load_dwordx4 a[116:119], v252, s[8:11], s20 offen offset:0 sc1\n\t"
"v_cvt_pk_bf16_f32 v222, v222, v223\n\t"
"v_cvt_pk_bf16_f32 v226, v226, v227\n\t"
"v_pk_fma_f32 v[228:229], v[230:231], s[56:57], v[228:229]\n\t"
"v_pk_fma_f32 v[230:231], v[228:229], s[58:59], v[230:231] neg_lo:[0,0,1] neg_hi:[0,0,1]\n\t"
"v_pk_fma_f32 v[232:233], v[74:75], s[60:61], v[228:229]\n\t"
"v_pk_fma_f32 v[74:75], v[228:229], s[60:61], v[74:75]\n\t"
"v_pk_fma_f32 v[228:229], v[172:173], s[62:63], v[230:231]\n\t"
"v_pk_fma_f32 v[172:173], v[230:231], s[62:63], v[172:173]\n\t"
"v_pk_fma_f32 v[234:235], v[236:237], s[56:57], v[234:235]\n\t"
"v_pk_fma_f32 v[236:237], v[234:235], s[58:59], v[236:237] neg_lo:[0,0,1] neg_hi:[0,0,1]\n\t"
"v_pk_fma_f32 v[250:251], v[76:77], s[60:61], v[234:235]\n\t"
"v_pk_fma_f32 v[76:77], v[234:235], s[60:61], v[76:77]\n\t"
"v_pk_fma_f32 v[234:235], v[174:175], s[62:63], v[236:237]\n\t"
"v_pk_fma_f32 v[174:175], v[236:237], s[62:63], v[174:175]\n\t"
"global_load_dwordx4 a[208:211], v252, s[46:47] offset:1024\n\t"
"v_mfma_f32_16x16x32_bf16 v[48:51], a[244:247], a[184:187], v[48:51]\n\t"
"v_accvgpr_write_b32 a120, v222\n\t"
"v_accvgpr_write_b32 a122, v226\n\t"
"v_mfma_f32_16x16x32_bf16 v[48:51], a[248:251], a[188:191], v[48:51]\n\t"
"v_accvgpr_read_b32 v222, a130\n\t"
"v_accvgpr_read_b32 v224, a134\n\t"
"v_mfma_f32_16x16x32_bf16 v[48:51], a[232:235], a[192:195], v[48:51]\n\t"
"v_accvgpr_read_b32 v223, a131\n\t"
"v_accvgpr_read_b32 v225, a135\n\t"
"buffer_load_dwordx4 a[100:103], v252, s[8:11], s20 offen offset:1024 sc1\n\t"
"v_cvt_pk_bf16_f32 v228, v228, v229\n\t"
"v_cvt_pk_bf16_f32 v232, v232, v233\n\t"
"v_cvt_pk_bf16_f32 v234, v234, v235\n\t"
"v_cvt_pk_bf16_f32 v250, v250, v251\n\t"
"v_pk_fma_f32 v[222:223], v[224:225], s[56:57], v[222:223]\n\t"
"v_pk_fma_f32 v[224:225], v[222:223], s[58:59], v[224:225] neg_lo:[0,0,1] neg_hi:[0,0,1]\n\t"
"v_pk_fma_f32 v[226:227], v[78:79], s[60:61], v[222:223]\n\t"
"v_pk_fma_f32 v[78:79], v[222:223], s[60:61], v[78:79]\n\t"
"v_pk_fma_f32 v[222:223], v[176:177], s[62:63], v[224:225]\n\t"
"v_pk_fma_f32 v[176:177], v[224:225], s[62:63], v[176:177]\n\t"
"v_mfma_f32_16x16x32_bf16 v[52:55], a[244:247], a[188:191], v[52:55]\n\t"
"v_accvgpr_write_b32 a121, v228\n\t"
"v_accvgpr_write_b32 a123, v232\n\t"
"v_mfma_f32_16x16x32_bf16 v[52:55], a[248:251], a[192:195], v[52:55]\n\t"
"v_accvgpr_write_b32 a128, v234\n\t"
"v_accvgpr_write_b32 a130, v250\n\t"
"v_mfma_f32_16x16x32_bf16 v[52:55], a[232:235], a[196:199], v[52:55]\n\t"
"v_accvgpr_read_b32 v228, a136\n\t"
"v_accvgpr_read_b32 v230, a140\n\t"
"v_accvgpr_read_b32 v229, a137\n\t"
"v_accvgpr_read_b32 v231, a141\n\t"
"buffer_load_dwordx4 a[124:127], v252, s[8:11], s20 offen offset:2048 sc1\n\t"
"v_cvt_pk_bf16_f32 v222, v222, v223\n\t"
"v_cvt_pk_bf16_f32 v226, v226, v227\n\t"
"v_pk_fma_f32 v[228:229], v[230:231], s[56:57], v[228:229]\n\t"
"v_pk_fma_f32 v[230:231], v[228:229], s[58:59], v[230:231] neg_lo:[0,0,1] neg_hi:[0,0,1]\n\t"
"v_pk_fma_f32 v[232:233], v[80:81], s[60:61], v[228:229]\n\t"
"v_pk_fma_f32 v[80:81], v[228:229], s[60:61], v[80:81]\n\t"
"v_pk_fma_f32 v[228:229], v[178:179], s[62:63], v[230:231]\n\t"
"v_pk_fma_f32 v[178:179], v[230:231], s[62:63], v[178:179]\n\t"
"v_mfma_f32_16x16x32_bf16 v[56:59], a[244:247], a[192:195], v[56:59]\n\t"
"v_accvgpr_write_b32 a129, v222\n\t"
"v_accvgpr_write_b32 a131, v226\n\t"
"v_mfma_f32_16x16x32_bf16 v[56:59], a[248:251], a[196:199], v[56:59]\n\t"
"v_accvgpr_read_b32 v234, a138\n\t"
"v_accvgpr_read_b32 v236, a142\n\t"
"v_mfma_f32_16x16x32_bf16 v[56:59], a[232:235], a[200:203], v[56:59]\n\t"
"v_accvgpr_read_b32 v235, a139\n\t"
"v_accvgpr_read_b32 v237, a143\n\t"
"s_mov_b32 s20, 847872\n\t"
"buffer_load_dwordx4 a[132:135], v252, s[8:11], s20 offen offset:0 sc1\n\t"
"v_cvt_pk_bf16_f32 v228, v228, v229\n\t"
"v_cvt_pk_bf16_f32 v232, v232, v233\n\t"
"v_pk_fma_f32 v[234:235], v[236:237], s[56:57], v[234:235]\n\t"
"v_pk_fma_f32 v[236:237], v[234:235], s[58:59], v[236:237] neg_lo:[0,0,1] neg_hi:[0,0,1]\n\t"
"v_pk_fma_f32 v[250:251], v[82:83], s[60:61], v[234:235]\n\t"
"v_pk_fma_f32 v[82:83], v[234:235], s[60:61], v[82:83]\n\t"
"v_pk_fma_f32 v[234:235], v[180:181], s[62:63], v[236:237]\n\t"
"v_pk_fma_f32 v[180:181], v[236:237], s[62:63], v[180:181]\n\t"
"v_mfma_f32_16x16x32_bf16 v[60:63], a[244:247], a[196:199], v[60:63]\n\t"
"v_accvgpr_write_b32 a136, v228\n\t"
"v_accvgpr_write_b32 a138, v232\n\t"
"v_mfma_f32_16x16x32_bf16 v[60:63], a[248:251], a[200:203], v[60:63]\n\t"
"v_accvgpr_read_b32 v222, a144\n\t"
"v_accvgpr_read_b32 v224, a148\n\t"
"v_mfma_f32_16x16x32_bf16 v[60:63], a[232:235], a[204:207], v[60:63]\n\t"
"v_accvgpr_read_b32 v223, a145\n\t"
"v_accvgpr_read_b32 v225, a149\n\t"
"buffer_load_dwordx4 a[212:215], v252, s[8:11], s20 offen offset:1024 sc1\n\t"
"v_cvt_pk_bf16_f32 v234, v234, v235\n\t"
"v_cvt_pk_bf16_f32 v250, v250, v251\n\t"
"v_pk_fma_f32 v[222:223], v[224:225], s[56:57], v[222:223]\n\t"
"v_pk_fma_f32 v[224:225], v[222:223], s[58:59], v[224:225] neg_lo:[0,0,1] neg_hi:[0,0,1]\n\t"
"v_pk_fma_f32 v[226:227], v[84:85], s[60:61], v[222:223]\n\t"
"v_pk_fma_f32 v[84:85], v[222:223], s[60:61], v[84:85]\n\t"
"v_pk_fma_f32 v[222:223], v[182:183], s[62:63], v[224:225]\n\t"
"v_pk_fma_f32 v[182:183], v[224:225], s[62:63], v[182:183]\n\t"
"s_waitcnt vmcnt(21)\n\t"
"v_mfma_f32_16x16x32_bf16 v[64:67], a[236:239], a[168:171], v[64:67]\n\t"
"v_accvgpr_write_b32 a137, v234\n\t"
"v_accvgpr_write_b32 a139, v250\n\t"
"v_mfma_f32_16x16x32_bf16 v[64:67], a[252:255], a[172:175], v[64:67]\n\t"
"v_accvgpr_read_b32 v228, a146\n\t"
"v_accvgpr_read_b32 v230, a150\n\t"
"v_mfma_f32_16x16x32_bf16 v[64:67], a[40:43], a[176:179], v[64:67]\n\t"
"v_accvgpr_read_b32 v229, a147\n\t"
"v_accvgpr_read_b32 v231, a151\n\t"
"buffer_load_dwordx4 a[140:143], v252, s[8:11], s20 offen offset:2048 sc1\n\t"
"v_cvt_pk_bf16_f32 v222, v222, v223\n\t"
"v_cvt_pk_bf16_f32 v226, v226, v227\n\t"
"v_pk_fma_f32 v[228:229], v[230:231], s[56:57], v[228:229]\n\t"
"v_pk_fma_f32 v[230:231], v[228:229], s[58:59], v[230:231] neg_lo:[0,0,1] neg_hi:[0,0,1]\n\t"
"v_pk_fma_f32 v[232:233], v[86:87], s[60:61], v[228:229]\n\t"
"v_pk_fma_f32 v[86:87], v[228:229], s[60:61], v[86:87]\n\t"
"v_pk_fma_f32 v[228:229], v[184:185], s[62:63], v[230:231]\n\t"
"v_pk_fma_f32 v[184:185], v[230:231], s[62:63], v[184:185]\n\t"
"v_mfma_f32_16x16x32_bf16 v[68:71], a[236:239], a[172:175], v[68:71]\n\t"
"v_accvgpr_write_b32 a144, v222\n\t"
"v_accvgpr_write_b32 a146, v226\n\t"
"v_mfma_f32_16x16x32_bf16 v[68:71], a[252:255], a[176:179], v[68:71]\n\t"
"v_accvgpr_read_b32 v234, a152\n\t"
"v_accvgpr_read_b32 v236, a156\n\t"
"v_mfma_f32_16x16x32_bf16 v[68:71], a[40:43], a[180:183], v[68:71]\n\t"
"v_accvgpr_read_b32 v235, a153\n\t"
"v_accvgpr_read_b32 v237, a157\n\t"
"s_mov_b32 s20, 860160\n\t"
"buffer_load_dwordx4 a[216:219], v252, s[8:11], s20 offen offset:0 sc1\n\t"
"v_cvt_pk_bf16_f32 v228, v228, v229\n\t"
"v_cvt_pk_bf16_f32 v232, v232, v233\n\t"
"v_pk_fma_f32 v[234:235], v[236:237], s[56:57], v[234:235]\n\t"
"v_pk_fma_f32 v[236:237], v[234:235], s[58:59], v[236:237] neg_lo:[0,0,1] neg_hi:[0,0,1]\n\t"
"v_pk_fma_f32 v[250:251], v[88:89], s[60:61], v[234:235]\n\t"
"v_pk_fma_f32 v[88:89], v[234:235], s[60:61], v[88:89]\n\t"
"v_pk_fma_f32 v[234:235], v[186:187], s[62:63], v[236:237]\n\t"
"v_pk_fma_f32 v[186:187], v[236:237], s[62:63], v[186:187]\n\t"
"v_mfma_f32_16x16x32_bf16 v[72:75], a[236:239], a[176:179], v[72:75]\n\t"
"v_accvgpr_write_b32 a145, v228\n\t"
"v_accvgpr_write_b32 a147, v232\n\t"
"v_mfma_f32_16x16x32_bf16 v[72:75], a[252:255], a[180:183], v[72:75]\n\t"
"v_accvgpr_read_b32 v222, a154\n\t"
"v_accvgpr_read_b32 v224, a158\n\t"
"v_mfma_f32_16x16x32_bf16 v[72:75], a[40:43], a[184:187], v[72:75]\n\t"
"v_accvgpr_read_b32 v223, a155\n\t"
"v_accvgpr_read_b32 v225, a159\n\t"
"buffer_load_dwordx4 a[148:151], v252, s[8:11], s20 offen offset:1024 sc1\n\t"
"v_cvt_pk_bf16_f32 v234, v234, v235\n\t"
"v_cvt_pk_bf16_f32 v250, v250, v251\n\t"
"v_pk_fma_f32 v[222:223], v[224:225], s[56:57], v[222:223]\n\t"
"v_pk_fma_f32 v[224:225], v[222:223], s[58:59], v[224:225] neg_lo:[0,0,1] neg_hi:[0,0,1]\n\t"
"v_pk_fma_f32 v[226:227], v[90:91], s[60:61], v[222:223]\n\t"
"v_pk_fma_f32 v[90:91], v[222:223], s[60:61], v[90:91]\n\t"
"v_pk_fma_f32 v[222:223], v[188:189], s[62:63], v[224:225]\n\t"
"v_pk_fma_f32 v[188:189], v[224:225], s[62:63], v[188:189]\n\t"
"v_mfma_f32_16x16x32_bf16 v[76:79], a[236:239], a[180:183], v[76:79]\n\t"
"ds_read_b128 a[168:171], v242 offset:27648\n\t"
"v_accvgpr_write_b32 a152, v234\n\t"
"v_mfma_f32_16x16x32_bf16 v[76:79], a[252:255], a[184:187], v[76:79]\n\t"
"ds_read_b128 a[172:175], v242 offset:28160\n\t"
"v_accvgpr_write_b32 a154, v250\n\t"
"v_mfma_f32_16x16x32_bf16 v[76:79], a[40:43], a[188:191], v[76:79]\n\t"
"ds_read_b128 a[176:179], v242 offset:28672\n\t"
"v_accvgpr_read_b32 v228, a160\n\t"
"v_accvgpr_read_b32 v230, a164\n\t"
"v_accvgpr_read_b32 v229, a161\n\t"
"v_accvgpr_read_b32 v231, a165\n\t"
"buffer_load_dwordx4 a[220:223], v252, s[8:11], s20 offen offset:2048 sc1\n\t"
"v_cvt_pk_bf16_f32 v222, v222, v223\n\t"
"v_cvt_pk_bf16_f32 v226, v226, v227\n\t"
"v_pk_fma_f32 v[228:229], v[230:231], s[56:57], v[228:229]\n\t"
"v_pk_fma_f32 v[230:231], v[228:229], s[58:59], v[230:231] neg_lo:[0,0,1] neg_hi:[0,0,1]\n\t"
"v_pk_fma_f32 v[232:233], v[92:93], s[60:61], v[228:229]\n\t"
"v_pk_fma_f32 v[92:93], v[228:229], s[60:61], v[92:93]\n\t"
"v_pk_fma_f32 v[228:229], v[190:191], s[62:63], v[230:231]\n\t"
"v_pk_fma_f32 v[190:191], v[230:231], s[62:63], v[190:191]\n\t"
"v_mfma_f32_16x16x32_bf16 v[80:83], a[236:239], a[184:187], v[80:83]\n\t"
"v_accvgpr_write_b32 a153, v222\n\t"
"v_accvgpr_write_b32 a155, v226\n\t"
"v_mfma_f32_16x16x32_bf16 v[80:83], a[252:255], a[188:191], v[80:83]\n\t"
"v_accvgpr_read_b32 v234, a162\n\t"
"v_accvgpr_read_b32 v236, a166\n\t"
"v_mfma_f32_16x16x32_bf16 v[80:83], a[40:43], a[192:195], v[80:83]\n\t"
"v_accvgpr_read_b32 v235, a163\n\t"
"v_accvgpr_read_b32 v237, a167\n\t"
"s_mov_b32 s20, 872448\n\t"
"buffer_load_dwordx4 a[156:159], v252, s[8:11], s20 offen offset:0 sc1\n\t"
"v_cvt_pk_bf16_f32 v228, v228, v229\n\t"
"v_cvt_pk_bf16_f32 v232, v232, v233\n\t"
"v_pk_fma_f32 v[234:235], v[236:237], s[56:57], v[234:235]\n\t"
"v_pk_fma_f32 v[236:237], v[234:235], s[58:59], v[236:237] neg_lo:[0,0,1] neg_hi:[0,0,1]\n\t"
"v_pk_fma_f32 v[250:251], v[94:95], s[60:61], v[234:235]\n\t"
"v_pk_fma_f32 v[94:95], v[234:235], s[60:61], v[94:95]\n\t"
"v_pk_fma_f32 v[234:235], v[192:193], s[62:63], v[236:237]\n\t"
"v_pk_fma_f32 v[192:193], v[236:237], s[62:63], v[192:193]\n\t"
"v_mfma_f32_16x16x32_bf16 v[84:87], a[236:239], a[188:191], v[84:87]\n\t"
"ds_read_b128 a[180:183], v242 offset:29184\n\t"
"v_accvgpr_write_b32 a160, v228\n\t"
"v_mfma_f32_16x16x32_bf16 v[84:87], a[252:255], a[192:195], v[84:87]\n\t"
"v_accvgpr_write_b32 a162, v232\n\t"
"v_cvt_pk_bf16_f32 v234, v234, v235\n\t"
"v_mfma_f32_16x16x32_bf16 v[84:87], a[40:43], a[196:199], v[84:87]\n\t"
"buffer_load_dwordx4 a[224:227], v252, s[8:11], s20 offen offset:1024 sc1\n\t"
"v_cvt_pk_bf16_f32 v250, v250, v251\n\t"
"ds_read_b128 a[184:187], v242 offset:29696\n\t"
"v_mfma_f32_16x16x32_bf16 v[88:91], a[236:239], a[192:195], v[88:91]\n\t"
"v_accvgpr_write_b32 a161, v234\n\t"
"v_accvgpr_write_b32 a163, v250\n\t"
"v_mfma_f32_16x16x32_bf16 v[88:91], a[252:255], a[196:199], v[88:91]\n\t"
"ds_read_b64_tr_b16 v[230:231], v238 offset:1536\n\t"
"v_mfma_f32_16x16x32_bf16 v[88:91], a[40:43], a[200:203], v[88:91]\n\t"
"ds_read_b64_tr_b16 v[232:233], v238 offset:3072\n\t"
"buffer_load_dwordx4 a[164:167], v252, s[8:11], s20 offen offset:2048 sc1\n\t"
"ds_read_b64_tr_b16 v[234:235], v239 offset:1536\n\t"
"ds_read_b64_tr_b16 v[236:237], v239 offset:3072\n\t"
"v_mfma_f32_16x16x32_bf16 v[92:95], a[236:239], a[196:199], v[92:95]\n\t"
"ds_read_b128 a[188:191], v242 offset:30208\n\t"
"v_mfma_f32_16x16x32_bf16 v[92:95], a[252:255], a[200:203], v[92:95]\n\t"
"ds_read_b128 a[192:195], v242 offset:30720\n\t"
"v_mfma_f32_16x16x32_bf16 v[92:95], a[40:43], a[204:207], v[92:95]\n\t"
"s_mov_b32 s20, 884736\n\t"
"buffer_load_dwordx4 a[228:231], v252, s[8:11], s20 offen offset:0 sc1\n\t"
"s_waitcnt vmcnt(26) lgkmcnt(8)\n\t"
"v_mfma_f32_16x16x32_bf16 v[98:101], a[48:51], a[168:171], v[98:101]\n\t"
"ds_read_b128 a[196:199], v242 offset:31232\n\t"
"v_mfma_f32_16x16x32_bf16 v[98:101], a[52:55], a[172:175], v[98:101]\n\t"
"ds_read_b128 a[200:203], v242 offset:31744\n\t"
"v_mfma_f32_16x16x32_bf16 v[98:101], a[56:59], a[176:179], v[98:101]\n\t"
"buffer_load_dwordx4 a[232:235], v252, s[8:11], s20 offen offset:1024 sc1\n\t"
"ds_read_b128 a[204:207], v242 offset:32256\n\t"
"ds_read_b64_tr_b16 v[222:223], v238 offset:21504\n\t"
"s_waitcnt vmcnt(13) lgkmcnt(8)\n\t"
"v_mfma_f32_16x16x32_bf16 v[230:233], v[230:233], a[208:211], 0\n\t"
"ds_read_b64_tr_b16 v[224:225], v238 offset:23040\n\t"
"ds_read_b64_tr_b16 v[226:227], v239 offset:21504\n\t"
"s_waitcnt lgkmcnt(8)\n\t"
"v_mfma_f32_16x16x32_bf16 v[234:237], v[234:237], a[208:211], 0\n\t"
"ds_read_b64_tr_b16 v[228:229], v239 offset:23040\n\t"
"buffer_load_dwordx4 a[236:239], v252, s[8:11], s20 offen offset:2048 sc1\n\t"
"v_mfma_f32_16x16x32_bf16 v[102:105], a[48:51], a[172:175], v[102:105]\n\t"
"v_mfma_f32_16x16x32_bf16 v[102:105], a[52:55], a[176:179], v[102:105]\n\t"
"v_mfma_f32_16x16x32_bf16 v[102:105], a[56:59], a[180:183], v[102:105]\n\t"
"s_mov_b32 s20, 897024\n\t"
"buffer_load_dwordx4 a[240:243], v252, s[8:11], s20 offen offset:0 sc1\n\t"
"s_waitcnt lgkmcnt(4)\n\t"
"s_barrier\n\t"
"v_mfma_f32_16x16x32_bf16 v[106:109], a[48:51], a[176:179], v[106:109]\n\t"
"v_cvt_pk_bf16_f32 v230, v230, v231\n\t"
"v_cvt_pk_bf16_f32 v231, v232, v233\n\t"
"v_mfma_f32_16x16x32_bf16 v[106:109], a[52:55], a[180:183], v[106:109]\n\t"
"v_cvt_pk_bf16_f32 v232, v234, v235\n\t"
"v_cvt_pk_bf16_f32 v233, v236, v237\n\t"
"v_mfma_f32_16x16x32_bf16 v[106:109], a[56:59], a[184:187], v[106:109]\n\t"
"buffer_load_dwordx4 a[244:247], v252, s[8:11], s20 offen offset:1024 sc1\n\t"
"s_waitcnt lgkmcnt(2)\n\t"
"v_mfma_f32_16x16x32_bf16 v[222:225], v[222:225], a[208:211], 0\n\t"
"buffer_load_dwordx4 a[248:251], v252, s[8:11], s20 offen offset:2048 sc1\n\t"
"s_waitcnt lgkmcnt(0)\n\t"
"v_mfma_f32_16x16x32_bf16 v[226:229], v[226:229], a[208:211], 0\n\t"
"s_mov_b64 exec, s[44:45]\n\t"
"ds_write_b128 v240, v[230:233] offset:4608\n\t"
"s_mov_b64 exec, -1\n\t"
"v_mfma_f32_16x16x32_bf16 v[110:113], a[48:51], a[180:183], v[110:113]\n\t"
"ds_read_b64_tr_b16 v[230:231], v238 offset:41472\n\t"
"v_mfma_f32_16x16x32_bf16 v[110:113], a[52:55], a[184:187], v[110:113]\n\t"
"ds_read_b64_tr_b16 v[232:233], v238 offset:43008\n\t"
"v_mfma_f32_16x16x32_bf16 v[110:113], a[56:59], a[188:191], v[110:113]\n\t"
"s_mov_b32 s20, 909312\n\t"
"buffer_load_dwordx4 a[252:255], v252, s[8:11], s20 offen offset:0 sc1\n\t"
"v_mfma_f32_16x16x32_bf16 v[114:117], a[48:51], a[184:187], v[114:117]\n\t"
"ds_read_b64_tr_b16 v[234:235], v239 offset:41472\n\t"
"v_cvt_pk_bf16_f32 v222, v222, v223\n\t"
"v_mfma_f32_16x16x32_bf16 v[114:117], a[52:55], a[188:191], v[114:117]\n\t"
"ds_read_b64_tr_b16 v[236:237], v239 offset:43008\n\t"
"v_cvt_pk_bf16_f32 v223, v224, v225\n\t"
"v_mfma_f32_16x16x32_bf16 v[114:117], a[56:59], a[192:195], v[114:117]\n\t"
"v_cvt_pk_bf16_f32 v224, v226, v227\n\t"
"v_cvt_pk_bf16_f32 v225, v228, v229\n\t"
"v_mfma_f32_16x16x32_bf16 v[118:121], a[48:51], a[188:191], v[118:121]\n\t"
"v_subrev_u32 v238, 59904, v238\n\t"
"v_subrev_u32 v239, 59904, v239\n\t"
"v_mfma_f32_16x16x32_bf16 v[118:121], a[52:55], a[192:195], v[118:121]\n\t"
"v_mfma_f32_16x16x32_bf16 v[118:121], a[56:59], a[196:199], v[118:121]\n\t"
"ds_write_b128 v248, v[194:197] offset:18432\n\t"
"s_mov_b64 exec, s[44:45]\n\t"
"ds_write_b128 v240, v[222:225] offset:6144\n\t"
"s_mov_b64 exec, -1\n\t"
"v_mfma_f32_16x16x32_bf16 v[122:125], a[48:51], a[192:195], v[122:125]\n\t"
"ds_read_b64_tr_b16 v[222:223], v238 offset:1536\n\t"
"v_mfma_f32_16x16x32_bf16 v[122:125], a[52:55], a[196:199], v[122:125]\n\t"
"ds_read_b64_tr_b16 v[224:225], v238 offset:3072\n\t"
"v_mfma_f32_16x16x32_bf16 v[122:125], a[56:59], a[200:203], v[122:125]\n\t"
"ds_read_b64_tr_b16 v[226:227], v239 offset:1536\n\t"
"ds_read_b64_tr_b16 v[228:229], v239 offset:3072\n\t"
"s_waitcnt lgkmcnt(8)\n\t"
"v_mfma_f32_16x16x32_bf16 v[230:233], v[230:233], a[208:211], 0\n\t"
"ds_write_b64 v249, a[104:105] offset:18432\n\t"
"s_waitcnt lgkmcnt(7)\n\t"
"v_mfma_f32_16x16x32_bf16 v[234:237], v[234:237], a[208:211], 0\n\t"
"ds_write_b128 v248, v[198:201] offset:24576\n\t"
"v_mfma_f32_16x16x32_bf16 v[126:129], a[48:51], a[196:199], v[126:129]\n\t"
"v_mfma_f32_16x16x32_bf16 v[126:129], a[52:55], a[200:203], v[126:129]\n\t"
"v_mfma_f32_16x16x32_bf16 v[126:129], a[56:59], a[204:207], v[126:129]\n\t"
"v_mfma_f32_16x16x32_bf16 v[130:133], a[44:47], a[168:171], v[130:133]\n\t"
"v_cvt_pk_bf16_f32 v230, v230, v231\n\t"
"v_mfma_f32_16x16x32_bf16 v[130:133], a[64:67], a[172:175], v[130:133]\n\t"
"v_cvt_pk_bf16_f32 v231, v232, v233\n\t"
"v_cvt_pk_bf16_f32 v232, v234, v235\n\t"
"v_mfma_f32_16x16x32_bf16 v[130:133], a[60:63], a[176:179], v[130:133]\n\t"
"v_cvt_pk_bf16_f32 v233, v236, v237\n\t"
"s_waitcnt lgkmcnt(4)\n\t"
"v_mfma_f32_16x16x32_bf16 v[222:225], v[222:225], a[208:211], 0\n\t"
"s_mov_b64 exec, s[44:45]\n\t"
"ds_write_b128 v240, v[230:233] offset:7680\n\t"
"s_mov_b64 exec, -1\n\t"
"s_waitcnt lgkmcnt(3)\n\t"
"v_mfma_f32_16x16x32_bf16 v[226:229], v[226:229], a[208:211], 0\n\t"
"ds_read_b64_tr_b16 v[230:231], v238 offset:21504\n\t"
"ds_read_b64_tr_b16 v[232:233], v238 offset:23040\n\t"
"v_mfma_f32_16x16x32_bf16 v[134:137], a[44:47], a[172:175], v[134:137]\n\t"
"ds_read_b64_tr_b16 v[234:235], v239 offset:21504\n\t"
"v_mfma_f32_16x16x32_bf16 v[134:137], a[64:67], a[176:179], v[134:137]\n\t"
"v_mfma_f32_16x16x32_bf16 v[134:137], a[60:63], a[180:183], v[134:137]\n\t"
"ds_read_b64_tr_b16 v[236:237], v239 offset:23040\n\t"
"v_mfma_f32_16x16x32_bf16 v[138:141], a[44:47], a[176:179], v[138:141]\n\t"
"v_cvt_pk_bf16_f32 v222, v222, v223\n\t"
"v_cvt_pk_bf16_f32 v223, v224, v225\n\t"
"v_mfma_f32_16x16x32_bf16 v[138:141], a[64:67], a[180:183], v[138:141]\n\t"
"v_cvt_pk_bf16_f32 v224, v226, v227\n\t"
"v_cvt_pk_bf16_f32 v225, v228, v229\n\t"
"v_mfma_f32_16x16x32_bf16 v[138:141], a[60:63], a[184:187], v[138:141]\n\t"
"s_mov_b64 exec, s[44:45]\n\t"
"ds_write_b128 v240, v[222:225] offset:0\n\t"
"s_mov_b64 exec, -1\n\t"
"v_mfma_f32_16x16x32_bf16 v[142:145], a[44:47], a[180:183], v[142:145]\n\t"
"ds_read_b64_tr_b16 v[222:223], v238 offset:41472\n\t"
"v_mfma_f32_16x16x32_bf16 v[142:145], a[64:67], a[184:187], v[142:145]\n\t"
"ds_read_b64_tr_b16 v[224:225], v238 offset:43008\n\t"
"v_mfma_f32_16x16x32_bf16 v[142:145], a[60:63], a[188:191], v[142:145]\n\t"
"ds_read_b64_tr_b16 v[226:227], v239 offset:41472\n\t"
"ds_read_b64_tr_b16 v[228:229], v239 offset:43008\n\t"
"v_mfma_f32_16x16x32_bf16 v[146:149], a[44:47], a[184:187], v[146:149]\n\t"
"v_mfma_f32_16x16x32_bf16 v[146:149], a[64:67], a[188:191], v[146:149]\n\t"
"v_mfma_f32_16x16x32_bf16 v[146:149], a[60:63], a[192:195], v[146:149]\n\t"
"ds_write_b64 v249, a[106:107] offset:24576\n\t"
"s_waitcnt lgkmcnt(8)\n\t"
"v_mfma_f32_16x16x32_bf16 v[230:233], v[230:233], a[208:211], 0\n\t"
"s_waitcnt lgkmcnt(6)\n\t"
"v_mfma_f32_16x16x32_bf16 v[234:237], v[234:237], a[208:211], 0\n\t"
"v_mfma_f32_16x16x32_bf16 v[150:153], a[44:47], a[188:191], v[150:153]\n\t"
"v_mfma_f32_16x16x32_bf16 v[150:153], a[64:67], a[192:195], v[150:153]\n\t"
"v_mfma_f32_16x16x32_bf16 v[150:153], a[60:63], a[196:199], v[150:153]\n\t"
"v_mfma_f32_16x16x32_bf16 v[154:157], a[44:47], a[192:195], v[154:157]\n\t"
"s_nop 1\n\t"
"v_cvt_pk_bf16_f32 v230, v230, v231\n\t"
"v_cvt_pk_bf16_f32 v231, v232, v233\n\t"
"v_mfma_f32_16x16x32_bf16 v[154:157], a[64:67], a[196:199], v[154:157]\n\t"
"v_cvt_pk_bf16_f32 v232, v234, v235\n\t"
"v_mfma_f32_16x16x32_bf16 v[154:157], a[60:63], a[200:203], v[154:157]\n\t"
"v_cvt_pk_bf16_f32 v233, v236, v237\n\t"
"s_waitcnt lgkmcnt(3)\n\t"
"v_mfma_f32_16x16x32_bf16 v[222:225], v[222:225], a[208:211], 0\n\t"
"s_waitcnt lgkmcnt(1)\n\t"
"v_mfma_f32_16x16x32_bf16 v[226:229], v[226:229], a[208:211], 0\n\t"
"s_mov_b64 exec, s[44:45]\n\t"
"ds_write_b128 v240, v[230:233] offset:1536\n\t"
"s_mov_b64 exec, -1\n\t"
"v_mfma_f32_16x16x32_bf16 v[158:161], a[44:47], a[196:199], v[158:161]\n\t"
"v_mfma_f32_16x16x32_bf16 v[158:161], a[64:67], a[200:203], v[158:161]\n\t"
"v_mfma_f32_16x16x32_bf16 v[158:161], a[60:63], a[204:207], v[158:161]\n\t"
"buffer_load_dwordx4 a[44:47], v252, s[8:11], s20 offen offset:1024 sc1\n\t"
"v_mfma_f32_16x16x32_bf16 v[162:165], a[72:75], a[168:171], v[162:165]\n\t"
"v_cvt_pk_bf16_f32 v222, v222, v223\n\t"
"v_cvt_pk_bf16_f32 v223, v224, v225\n\t"
"v_mfma_f32_16x16x32_bf16 v[162:165], a[68:71], a[172:175], v[162:165]\n\t"
"v_cvt_pk_bf16_f32 v224, v226, v227\n\t"
"v_cvt_pk_bf16_f32 v225, v228, v229\n\t"
"v_mfma_f32_16x16x32_bf16 v[162:165], a[80:83], a[176:179], v[162:165]\n\t"
"buffer_load_dwordx4 a[64:67], v252, s[8:11], s20 offen offset:2048 sc1\n\t"
"v_mfma_f32_16x16x32_bf16 v[166:169], a[72:75], a[172:175], v[166:169]\n\t"
"v_mfma_f32_16x16x32_bf16 v[166:169], a[68:71], a[176:179], v[166:169]\n\t"
"v_mfma_f32_16x16x32_bf16 v[166:169], a[80:83], a[180:183], v[166:169]\n\t"
"s_mov_b32 s20, 1069056\n\t"
"buffer_load_dwordx4 a[60:63], v252, s[8:11], s20 offen offset:0 sc1\n\t"
"v_mfma_f32_16x16x32_bf16 v[170:173], a[72:75], a[176:179], v[170:173]\n\t"
"v_mfma_f32_16x16x32_bf16 v[170:173], a[68:71], a[180:183], v[170:173]\n\t"
"v_mfma_f32_16x16x32_bf16 v[170:173], a[80:83], a[184:187], v[170:173]\n\t"
"s_mov_b64 exec, s[44:45]\n\t"
"ds_write_b128 v240, v[222:225] offset:3072\n\t"
"s_mov_b64 exec, -1\n\t"
"v_mfma_f32_16x16x32_bf16 v[174:177], a[72:75], a[180:183], v[174:177]\n\t"
"v_mfma_f32_16x16x32_bf16 v[174:177], a[68:71], a[184:187], v[174:177]\n\t"
"v_mfma_f32_16x16x32_bf16 v[174:177], a[80:83], a[188:191], v[174:177]\n\t"
"v_mfma_f32_16x16x32_bf16 v[178:181], a[72:75], a[184:187], v[178:181]\n\t"
"v_mfma_f32_16x16x32_bf16 v[178:181], a[68:71], a[188:191], v[178:181]\n\t"
"v_mfma_f32_16x16x32_bf16 v[178:181], a[80:83], a[192:195], v[178:181]\n\t"
"v_mfma_f32_16x16x32_bf16 v[182:185], a[72:75], a[188:191], v[182:185]\n\t"
"v_mfma_f32_16x16x32_bf16 v[182:185], a[68:71], a[192:195], v[182:185]\n\t"
"v_mfma_f32_16x16x32_bf16 v[182:185], a[80:83], a[196:199], v[182:185]\n\t"
"s_waitcnt lgkmcnt(0)\n\t"
"s_barrier\n\t"
"v_mfma_f32_16x16x32_bf16 v[186:189], a[72:75], a[192:195], v[186:189]\n\t"
"ds_read_b128 a[168:171], v242 offset:0\n\t"
"v_mfma_f32_16x16x32_bf16 v[186:189], a[68:71], a[196:199], v[186:189]\n\t"
"ds_read_b128 a[172:175], v242 offset:512\n\t"
"v_mfma_f32_16x16x32_bf16 v[186:189], a[80:83], a[200:203], v[186:189]\n\t"
"ds_read_b128 a[176:179], v242 offset:1024\n\t"
"ds_read_b128 a[180:183], v242 offset:1536\n\t"
"ds_read_b128 a[184:187], v242 offset:2048\n\t"
"ds_read_b128 a[188:191], v242 offset:2560\n\t"
"ds_read_b128 a[40:43], v252 offset:18432\n\t"
"v_mfma_f32_16x16x32_bf16 v[190:193], a[72:75], a[196:199], v[190:193]\n\t"
"ds_read_b128 a[192:195], v242 offset:3072\n\t"
"v_mfma_f32_16x16x32_bf16 v[190:193], a[68:71], a[200:203], v[190:193]\n\t"
"ds_read_b128 a[48:51], v252 offset:19456\n\t"
"v_mfma_f32_16x16x32_bf16 v[190:193], a[80:83], a[204:207], v[190:193]\n\t"
"buffer_load_dwordx4 a[104:107], v252, s[8:11], s20 offen offset:1024 sc1\n\t"
"ds_read_b128 a[196:199], v242 offset:3584\n\t"
"ds_read_b128 a[200:203], v242 offset:4096\n\t"
"s_waitcnt lgkmcnt(8)\n\t"
"v_mfma_f32_16x16x32_bf16 v[0:3], a[92:95], a[168:171], v[0:3]\n\t"
"ds_read_b128 a[204:207], v242 offset:4608\n\t"
"v_mfma_f32_16x16x32_bf16 v[0:3], a[88:91], a[172:175], v[0:3]\n\t"
"ds_read_b128 a[56:59], v252 offset:20480\n\t"
"v_mfma_f32_16x16x32_bf16 v[0:3], a[96:99], a[176:179], v[0:3]\n\t"
"buffer_load_dwordx4 a[72:75], v252, s[8:11], s20 offen offset:2048 sc1\n\t"
"ds_write_b128 v248, v[202:205] offset:30720\n\t"
"s_waitcnt lgkmcnt(10)\n\t"
"v_mfma_f32_16x16x32_bf16 v[4:7], a[92:95], a[172:175], v[4:7]\n\t"
"v_mfma_f32_16x16x32_bf16 v[4:7], a[88:91], a[176:179], v[4:7]\n\t"
"v_mfma_f32_16x16x32_bf16 v[4:7], a[96:99], a[180:183], v[4:7]\n\t"
"s_mov_b32 s20, 1081344\n\t"
"buffer_load_dwordx4 a[80:83], v252, s[8:11], s20 offen offset:0 sc1\n\t"
"buffer_load_dwordx4 a[68:71], v252, s[8:11], s20 offen offset:1024 sc1\n\t"
"s_waitcnt lgkmcnt(9)\n\t"
"v_mfma_f32_16x16x32_bf16 v[8:11], a[92:95], a[176:179], v[8:11]\n\t"
"v_mfma_f32_16x16x32_bf16 v[8:11], a[88:91], a[180:183], v[8:11]\n\t"
"v_mfma_f32_16x16x32_bf16 v[8:11], a[96:99], a[184:187], v[8:11]\n\t"
"s_add_u32 s29, s22, 384\n\t"
"s_waitcnt lgkmcnt(7)\n\t"
"buffer_store_dwordx4 a[40:43], v247, s[16:19], s29 offen offset:0 nt\n\t"
"buffer_load_dwordx4 v[198:201], v252, s[8:11], s20 offen offset:2048 sc1\n\t"
"v_mfma_f32_16x16x32_bf16 v[12:15], a[92:95], a[180:183], v[12:15]\n\t"
"v_mfma_f32_16x16x32_bf16 v[12:15], a[88:91], a[184:187], v[12:15]\n\t"
"v_mfma_f32_16x16x32_bf16 v[12:15], a[96:99], a[188:191], v[12:15]\n\t"
"ds_write_b64 v249, a[112:113] offset:30720\n\t"
"s_add_u32 s29, s22, 384\n\t"
"s_waitcnt lgkmcnt(6)\n\t"
"buffer_store_dwordx4 a[48:51], v247, s[16:19], s29 offen offset:128 nt\n\t"
"v_mfma_f32_16x16x32_bf16 v[16:19], a[92:95], a[184:187], v[16:19]\n\t"
"v_mfma_f32_16x16x32_bf16 v[16:19], a[88:91], a[188:191], v[16:19]\n\t"
"v_mfma_f32_16x16x32_bf16 v[16:19], a[96:99], a[192:195], v[16:19]\n\t"
"ds_write_b128 v248, v[206:209] offset:36864\n\t"
"s_waitcnt lgkmcnt(6)\n\t"
"v_mfma_f32_16x16x32_bf16 v[20:23], a[92:95], a[188:191], v[20:23]\n\t"
"v_mfma_f32_16x16x32_bf16 v[20:23], a[88:91], a[192:195], v[20:23]\n\t"
"v_mfma_f32_16x16x32_bf16 v[20:23], a[96:99], a[196:199], v[20:23]\n\t"
"s_add_u32 s29, s22, 384\n\t"
"s_waitcnt lgkmcnt(3)\n\t"
"buffer_store_dwordx4 a[56:59], v247, s[16:19], s29 offen offset:256 nt\n\t"
"v_mfma_f32_16x16x32_bf16 v[24:27], a[92:95], a[192:195], v[24:27]\n\t"
"v_mfma_f32_16x16x32_bf16 v[24:27], a[88:91], a[196:199], v[24:27]\n\t"
"v_mfma_f32_16x16x32_bf16 v[24:27], a[96:99], a[200:203], v[24:27]\n\t"
"ds_write_b64 v249, a[114:115] offset:36864\n\t"
"v_mfma_f32_16x16x32_bf16 v[28:31], a[92:95], a[196:199], v[28:31]\n\t"
"v_mfma_f32_16x16x32_bf16 v[28:31], a[88:91], a[200:203], v[28:31]\n\t"
"v_mfma_f32_16x16x32_bf16 v[28:31], a[96:99], a[204:207], v[28:31]\n\t"
"s_mov_b32 s20, 1093632\n\t"
"buffer_load_dwordx4 a[88:91], v252, s[8:11], s20 offen offset:0 sc1\n\t"
"v_mfma_f32_16x16x32_bf16 v[32:35], a[76:79], a[168:171], v[32:35]\n\t"
"v_mfma_f32_16x16x32_bf16 v[32:35], a[108:111], a[172:175], v[32:35]\n\t"
"v_mfma_f32_16x16x32_bf16 v[32:35], a[84:87], a[176:179], v[32:35]\n\t"
"buffer_load_dwordx4 a[96:99], v252, s[8:11], s20 offen offset:1024 sc1\n\t"
"v_mfma_f32_16x16x32_bf16 v[36:39], a[76:79], a[172:175], v[36:39]\n\t"
"ds_read_b64_tr_b16 v[230:231], v238 offset:512\n\t"
"v_mfma_f32_16x16x32_bf16 v[36:39], a[108:111], a[176:179], v[36:39]\n\t"
"ds_read_b64_tr_b16 v[232:233], v238 offset:2048\n\t"
"v_mfma_f32_16x16x32_bf16 v[36:39], a[84:87], a[180:183], v[36:39]\n\t"
"v_mfma_f32_16x16x32_bf16 v[40:43], a[76:79], a[176:179], v[40:43]\n\t"
"ds_read_b64_tr_b16 v[234:235], v239 offset:512\n\t"
"v_mfma_f32_16x16x32_bf16 v[40:43], a[108:111], a[180:183], v[40:43]\n\t"
"v_mfma_f32_16x16x32_bf16 v[40:43], a[84:87], a[184:187], v[40:43]\n\t"
"ds_read_b64_tr_b16 v[236:237], v239 offset:2048\n\t"
"v_mfma_f32_16x16x32_bf16 v[44:47], a[76:79], a[180:183], v[44:47]\n\t"
"v_mfma_f32_16x16x32_bf16 v[44:47], a[108:111], a[184:187], v[44:47]\n\t"
"v_mfma_f32_16x16x32_bf16 v[44:47], a[84:87], a[188:191], v[44:47]\n\t"
"s_waitcnt lgkmcnt(4)\n\t"
"s_barrier\n\t"
"v_mfma_f32_16x16x32_bf16 v[48:51], a[76:79], a[184:187], v[48:51]\n\t"
"ds_read_b128 a[40:43], v252 offset:30720\n\t"
"v_mfma_f32_16x16x32_bf16 v[48:51], a[108:111], a[188:191], v[48:51]\n\t"
"ds_read_b128 a[48:51], v252 offset:31744\n\t"
"v_mfma_f32_16x16x32_bf16 v[48:51], a[84:87], a[192:195], v[48:51]\n\t"
"buffer_load_dwordx4 a[112:115], v252, s[8:11], s20 offen offset:2048 sc1\n\t"
"v_mfma_f32_16x16x32_bf16 v[52:55], a[76:79], a[188:191], v[52:55]\n\t"
"v_mfma_f32_16x16x32_bf16 v[52:55], a[108:111], a[192:195], v[52:55]\n\t"
"ds_read_b64_tr_b16 v[222:223], v238 offset:20480\n\t"
"v_mfma_f32_16x16x32_bf16 v[52:55], a[84:87], a[196:199], v[52:55]\n\t"
"s_mov_b32 s20, 1105920\n\t"
"buffer_load_dwordx4 v[206:209], v252, s[8:11], s20 offen offset:0 sc1\n\t"
"s_waitcnt lgkmcnt(5)\n\t"
"v_mfma_f32_16x16x32_bf16 v[230:233], v[230:233], a[208:211], 0\n\t"
"ds_write_b128 v248, v[210:213] offset:18432\n\t"
"v_mfma_f32_16x16x32_bf16 v[56:59], a[76:79], a[192:195], v[56:59]\n\t"
"ds_read_b64_tr_b16 v[224:225], v238 offset:22016\n\t"
"v_mfma_f32_16x16x32_bf16 v[56:59], a[108:111], a[196:199], v[56:59]\n\t"
"ds_read_b64_tr_b16 v[226:227], v239 offset:20480\n\t"
"v_mfma_f32_16x16x32_bf16 v[56:59], a[84:87], a[200:203], v[56:59]\n\t"
"s_add_u32 s29, s22, 196992\n\t"
"s_waitcnt lgkmcnt(5)\n\t"
"buffer_store_dwordx4 a[40:43], v247, s[16:19], s29 offen offset:0 nt\n\t"
"v_mfma_f32_16x16x32_bf16 v[234:237], v[234:237], a[208:211], 0\n\t"
"ds_read_b64_tr_b16 v[228:229], v239 offset:22016\n\t"
"ds_read_b128 a[56:59], v252 offset:32768\n\t"
"v_mfma_f32_16x16x32_bf16 v[60:63], a[76:79], a[196:199], v[60:63]\n\t"
"v_cvt_pk_bf16_f32 v230, v230, v231\n\t"
"v_cvt_pk_bf16_f32 v231, v232, v233\n\t"
"v_mfma_f32_16x16x32_bf16 v[60:63], a[108:111], a[200:203], v[60:63]\n\t"
"v_mfma_f32_16x16x32_bf16 v[60:63], a[84:87], a[204:207], v[60:63]\n\t"
"buffer_load_dwordx4 a[76:79], v252, s[8:11], s20 offen offset:1024 sc1\n\t"
"s_waitcnt vmcnt(33)\n\t"
"v_mfma_f32_16x16x32_bf16 v[64:67], a[116:119], a[168:171], v[64:67]\n\t"
"ds_read_b128 a[168:171], v242 offset:9216\n\t"
"v_mfma_f32_16x16x32_bf16 v[64:67], a[100:103], a[172:175], v[64:67]\n\t"
"v_cvt_pk_bf16_f32 v232, v234, v235\n\t"
"v_cvt_pk_bf16_f32 v233, v236, v237\n\t"
"v_mfma_f32_16x16x32_bf16 v[64:67], a[124:127], a[176:179], v[64:67]\n\t"
"buffer_load_dwordx4 a[84:87], v252, s[8:11], s20 offen offset:2048 sc1\n\t"
"v_mfma_f32_16x16x32_bf16 v[68:71], a[116:119], a[172:175], v[68:71]\n\t"
"ds_read_b128 a[172:175], v242 offset:9728\n\t"
"v_mfma_f32_16x16x32_bf16 v[68:71], a[100:103], a[176:179], v[68:71]\n\t"
"v_mfma_f32_16x16x32_bf16 v[68:71], a[124:127], a[180:183], v[68:71]\n\t"
"s_add_u32 s29, s22, 196992\n\t"
"s_waitcnt lgkmcnt(8)\n\t"
"buffer_store_dwordx4 a[48:51], v247, s[16:19], s29 offen offset:128 nt\n\t"
"ds_write_b64 v249, a[120:121] offset:18432\n\t"
"s_waitcnt lgkmcnt(6)\n\t"
"v_mfma_f32_16x16x32_bf16 v[222:225], v[222:225], a[208:211], 0\n\t"
"ds_write_b128 v248, v[214:217] offset:24576\n\t"
"v_mfma_f32_16x16x32_bf16 v[72:75], a[116:119], a[176:179], v[72:75]\n\t"
"ds_read_b128 a[176:179], v242 offset:10240\n\t"
"v_mfma_f32_16x16x32_bf16 v[72:75], a[100:103], a[180:183], v[72:75]\n\t"
"v_mfma_f32_16x16x32_bf16 v[72:75], a[124:127], a[184:187], v[72:75]\n\t"
"s_waitcnt lgkmcnt(6)\n\t"
"v_mfma_f32_16x16x32_bf16 v[226:229], v[226:229], a[208:211], 0\n\t"
"ds_write_b64 v249, a[122:123] offset:24576\n\t"
"v_mfma_f32_16x16x32_bf16 v[76:79], a[116:119], a[180:183], v[76:79]\n\t"
"v_cvt_pk_bf16_f32 v222, v222, v223\n\t"
"v_mfma_f32_16x16x32_bf16 v[76:79], a[100:103], a[184:187], v[76:79]\n\t"
"v_cvt_pk_bf16_f32 v223, v224, v225\n\t"
"v_mfma_f32_16x16x32_bf16 v[76:79], a[124:127], a[188:191], v[76:79]\n\t"
"ds_read_b128 a[180:183], v242 offset:10752\n\t"
"v_mfma_f32_16x16x32_bf16 v[80:83], a[116:119], a[184:187], v[80:83]\n\t"
"ds_read_b128 a[184:187], v242 offset:11264\n\t"
"v_mfma_f32_16x16x32_bf16 v[80:83], a[100:103], a[188:191], v[80:83]\n\t"
"v_cvt_pk_bf16_f32 v224, v226, v227\n\t"
"v_cvt_pk_bf16_f32 v225, v228, v229\n\t"
"v_mfma_f32_16x16x32_bf16 v[80:83], a[124:127], a[192:195], v[80:83]\n\t"
"v_mfma_f32_16x16x32_bf16 v[84:87], a[116:119], a[188:191], v[84:87]\n\t"
"ds_read_b128 a[188:191], v242 offset:11776\n\t"
"v_mfma_f32_16x16x32_bf16 v[84:87], a[100:103], a[192:195], v[84:87]\n\t"
"v_mfma_f32_16x16x32_bf16 v[84:87], a[124:127], a[196:199], v[84:87]\n\t"
"s_add_u32 s29, s22, 196992\n\t"
"s_waitcnt lgkmcnt(9)\n\t"
"buffer_store_dwordx4 a[56:59], v247, s[16:19], s29 offen offset:256 nt\n\t"
"v_mfma_f32_16x16x32_bf16 v[88:91], a[116:119], a[192:195], v[88:91]\n\t"
"ds_read_b128 a[192:195], v242 offset:12288\n\t"
"v_mfma_f32_16x16x32_bf16 v[88:91], a[100:103], a[196:199], v[88:91]\n\t"
"v_mfma_f32_16x16x32_bf16 v[88:91], a[124:127], a[200:203], v[88:91]\n\t"
"v_mfma_f32_16x16x32_bf16 v[92:95], a[116:119], a[196:199], v[92:95]\n\t"
"ds_read_b128 a[196:199], v242 offset:12800\n\t"
"v_mfma_f32_16x16x32_bf16 v[92:95], a[100:103], a[200:203], v[92:95]\n\t"
"ds_read_b128 a[200:203], v242 offset:13312\n\t"
"v_mfma_f32_16x16x32_bf16 v[92:95], a[124:127], a[204:207], v[92:95]\n\t"
"s_mov_b32 s20, 1118208\n\t"
"buffer_load_dwordx4 a[116:119], v252, s[8:11], s20 offen offset:0 sc1\n\t"
"s_waitcnt vmcnt(34) lgkmcnt(7)\n\t"
"v_mfma_f32_16x16x32_bf16 v[98:101], a[132:135], a[168:171], v[98:101]\n\t"
"ds_read_b128 a[204:207], v242 offset:13824\n\t"
"v_mfma_f32_16x16x32_bf16 v[98:101], a[212:215], a[172:175], v[98:101]\n\t"
"v_mfma_f32_16x16x32_bf16 v[98:101], a[140:143], a[176:179], v[98:101]\n\t"
"buffer_load_dwordx4 a[124:127], v252, s[8:11], s20 offen offset:1024 sc1\n\t"
"s_waitcnt lgkmcnt(6)\n\t"
"v_mfma_f32_16x16x32_bf16 v[102:105], a[132:135], a[172:175], v[102:105]\n\t"
"v_mfma_f32_16x16x32_bf16 v[102:105], a[212:215], a[176:179], v[102:105]\n\t"
"v_mfma_f32_16x16x32_bf16 v[102:105], a[140:143], a[180:183], v[102:105]\n\t"
"buffer_load_dwordx4 a[100:103], v252, s[8:11], s20 offen offset:2048 sc1\n\t"
"s_waitcnt lgkmcnt(0)\n\t"
"s_barrier\n\t"
"v_mfma_f32_16x16x32_bf16 v[106:109], a[132:135], a[176:179], v[106:109]\n\t"
"ds_read_b128 a[40:43], v252 offset:18432\n\t"
"v_mfma_f32_16x16x32_bf16 v[106:109], a[212:215], a[180:183], v[106:109]\n\t"
"ds_read_b128 a[48:51], v252 offset:19456\n\t"
"v_mfma_f32_16x16x32_bf16 v[106:109], a[140:143], a[184:187], v[106:109]\n\t"
"s_mov_b64 exec, s[44:45]\n\t"
"ds_write_b128 v240, v[230:233] offset:0\n\t"
"s_mov_b64 exec, -1\n\t"
"s_mov_b64 exec, s[44:45]\n\t"
"ds_write_b128 v240, v[222:225] offset:1536\n\t"
"s_mov_b64 exec, -1\n\t"
"v_mfma_f32_16x16x32_bf16 v[110:113], a[132:135], a[180:183], v[110:113]\n\t"
"ds_read_b64_tr_b16 v[230:231], v238 offset:40448\n\t"
"s_add_u32 s21, s21, 6291456\n\t"
"v_add_u32 v246, 6291456, v246\n\t"
"v_mfma_f32_16x16x32_bf16 v[110:113], a[212:215], a[184:187], v[110:113]\n\t"
"ds_read_b64_tr_b16 v[232:233], v238 offset:41984\n\t"
"v_mfma_f32_16x16x32_bf16 v[110:113], a[140:143], a[188:191], v[110:113]\n\t"
"s_mov_b32 s20, 1130496\n\t"
"buffer_load_dwordx4 v[214:217], v252, s[8:11], s20 offen offset:0 sc1\n\t"
"ds_read_b64_tr_b16 v[234:235], v239 offset:40448\n\t"
"ds_read_b64_tr_b16 v[236:237], v239 offset:41984\n\t"
"v_mfma_f32_16x16x32_bf16 v[114:117], a[132:135], a[184:187], v[114:117]\n\t"
"v_add_u32 v238, 59904, v238\n\t"
"v_add_u32 v239, 59904, v239\n\t"
"v_mfma_f32_16x16x32_bf16 v[114:117], a[212:215], a[188:191], v[114:117]\n\t"
"ds_read_b64_tr_b16 v[222:223], v238 offset:512\n\t"
"v_mfma_f32_16x16x32_bf16 v[114:117], a[140:143], a[192:195], v[114:117]\n\t"
"buffer_load_dwordx4 a[120:123], v252, s[8:11], s20 offen offset:1024 sc1\n\t"
"v_mfma_f32_16x16x32_bf16 v[118:121], a[132:135], a[188:191], v[118:121]\n\t"
"ds_read_b64_tr_b16 v[224:225], v238 offset:2048\n\t"
"v_mfma_f32_16x16x32_bf16 v[118:121], a[212:215], a[192:195], v[118:121]\n\t"
"ds_read_b64_tr_b16 v[226:227], v239 offset:512\n\t"
"v_mfma_f32_16x16x32_bf16 v[118:121], a[140:143], a[196:199], v[118:121]\n\t"
"ds_read_b64_tr_b16 v[228:229], v239 offset:2048\n\t"
"ds_read_b128 a[56:59], v252 offset:20480\n\t"
"s_waitcnt lgkmcnt(7)\n\t"
"v_mfma_f32_16x16x32_bf16 v[230:233], v[230:233], a[208:211], 0\n\t"
"ds_write_b128 v248, a[0:3] offset:30720\n\t"
"s_waitcnt lgkmcnt(6)\n\t"
"v_mfma_f32_16x16x32_bf16 v[234:237], v[234:237], a[208:211], 0\n\t"
"s_add_u32 s29, s22, 393600\n\t"
"buffer_store_dwordx4 a[40:43], v247, s[16:19], s29 offen offset:0 nt\n\t"
"v_mfma_f32_16x16x32_bf16 v[122:125], a[132:135], a[192:195], v[122:125]\n\t"
"v_mfma_f32_16x16x32_bf16 v[122:125], a[212:215], a[196:199], v[122:125]\n\t"
"v_mfma_f32_16x16x32_bf16 v[122:125], a[140:143], a[200:203], v[122:125]\n\t"
"s_add_u32 s29, s22, 393600\n\t"
"buffer_store_dwordx4 a[48:51], v247, s[16:19], s29 offen offset:128 nt\n\t"
"v_mfma_f32_16x16x32_bf16 v[126:129], a[132:135], a[196:199], v[126:129]\n\t"
"v_cvt_pk_bf16_f32 v230, v230, v231\n\t"
"v_cvt_pk_bf16_f32 v231, v232, v233\n\t"
"v_mfma_f32_16x16x32_bf16 v[126:129], a[212:215], a[200:203], v[126:129]\n\t"
"v_cvt_pk_bf16_f32 v232, v234, v235\n\t"
"v_cvt_pk_bf16_f32 v233, v236, v237\n\t"
"v_mfma_f32_16x16x32_bf16 v[126:129], a[140:143], a[204:207], v[126:129]\n\t"
"buffer_load_dwordx4 a[212:215], v252, s[8:11], s20 offen offset:2048 sc1\n\t"
"s_waitcnt lgkmcnt(4)\n\t"
"v_mfma_f32_16x16x32_bf16 v[222:225], v[222:225], a[208:211], 0\n\t"
"s_mov_b64 exec, s[44:45]\n\t"
"ds_write_b128 v240, v[230:233] offset:3072\n\t"
"s_mov_b64 exec, -1\n\t"
"s_waitcnt lgkmcnt(3)\n\t"
"v_mfma_f32_16x16x32_bf16 v[226:229], v[226:229], a[208:211], 0\n\t"
"ds_read_b64_tr_b16 v[230:231], v238 offset:20480\n\t"
"ds_read_b64_tr_b16 v[232:233], v238 offset:22016\n\t"
"s_waitcnt vmcnt(38)\n\t"
"v_mfma_f32_16x16x32_bf16 v[130:133], a[216:219], a[168:171], v[130:133]\n\t"
"ds_read_b64_tr_b16 v[234:235], v239 offset:20480\n\t"
"v_mfma_f32_16x16x32_bf16 v[130:133], a[148:151], a[172:175], v[130:133]\n\t"
"ds_read_b64_tr_b16 v[236:237], v239 offset:22016\n\t"
"v_mfma_f32_16x16x32_bf16 v[130:133], a[220:223], a[176:179], v[130:133]\n\t"
"ds_write_b64 v249, a[128:129] offset:30720\n\t"
"v_mfma_f32_16x16x32_bf16 v[134:137], a[216:219], a[172:175], v[134:137]\n\t"
"v_cvt_pk_bf16_f32 v222, v222, v223\n\t"
"v_cvt_pk_bf16_f32 v223, v224, v225\n\t"
"v_mfma_f32_16x16x32_bf16 v[134:137], a[148:151], a[176:179], v[134:137]\n\t"
"v_cvt_pk_bf16_f32 v224, v226, v227\n\t"
"v_cvt_pk_bf16_f32 v225, v228, v229\n\t"
"v_mfma_f32_16x16x32_bf16 v[134:137], a[220:223], a[180:183], v[134:137]\n\t"
"s_mov_b64 exec, s[44:45]\n\t"
"ds_write_b128 v240, v[222:225] offset:4608\n\t"
"s_mov_b64 exec, -1\n\t"
"v_mfma_f32_16x16x32_bf16 v[138:141], a[216:219], a[176:179], v[138:141]\n\t"
"ds_read_b64_tr_b16 v[222:223], v238 offset:40448\n\t"
"v_mfma_f32_16x16x32_bf16 v[138:141], a[148:151], a[180:183], v[138:141]\n\t"
"ds_read_b64_tr_b16 v[224:225], v238 offset:41984\n\t"
"v_mfma_f32_16x16x32_bf16 v[138:141], a[220:223], a[184:187], v[138:141]\n\t"
"s_add_u32 s29, s22, 393600\n\t"
"s_waitcnt lgkmcnt(10)\n\t"
"buffer_store_dwordx4 a[56:59], v247, s[16:19], s29 offen offset:256 nt\n\t"
"v_mfma_f32_16x16x32_bf16 v[142:145], a[216:219], a[180:183], v[142:145]\n\t"
"ds_read_b64_tr_b16 v[226:227], v239 offset:40448\n\t"
"v_mfma_f32_16x16x32_bf16 v[142:145], a[148:151], a[184:187], v[142:145]\n\t"
"ds_read_b64_tr_b16 v[228:229], v239 offset:41984\n\t"
"v_mfma_f32_16x16x32_bf16 v[142:145], a[220:223], a[188:191], v[142:145]\n\t"
"ds_write_b128 v248, a[4:7] offset:36864\n\t"
"s_waitcnt lgkmcnt(9)\n\t"
"v_mfma_f32_16x16x32_bf16 v[230:233], v[230:233], a[208:211], 0\n\t"
"ds_write_b64 v249, a[130:131] offset:36864\n\t"
"s_waitcnt lgkmcnt(8)\n\t"
"v_mfma_f32_16x16x32_bf16 v[234:237], v[234:237], a[208:211], 0\n\t"
"v_mfma_f32_16x16x32_bf16 v[146:149], a[216:219], a[184:187], v[146:149]\n\t"
"v_mfma_f32_16x16x32_bf16 v[146:149], a[148:151], a[188:191], v[146:149]\n\t"
"v_mfma_f32_16x16x32_bf16 v[146:149], a[220:223], a[192:195], v[146:149]\n\t"
"v_mfma_f32_16x16x32_bf16 v[150:153], a[216:219], a[188:191], v[150:153]\n\t"
"s_nop 0\n\t"
"v_cvt_pk_bf16_f32 v230, v230, v231\n\t"
"v_cvt_pk_bf16_f32 v231, v232, v233\n\t"
"v_mfma_f32_16x16x32_bf16 v[150:153], a[148:151], a[192:195], v[150:153]\n\t"
"v_cvt_pk_bf16_f32 v232, v234, v235\n\t"
"v_mfma_f32_16x16x32_bf16 v[150:153], a[220:223], a[196:199], v[150:153]\n\t"
"v_cvt_pk_bf16_f32 v233, v236, v237\n\t"
"s_waitcnt lgkmcnt(4)\n\t"
"v_mfma_f32_16x16x32_bf16 v[222:225], v[222:225], a[208:211], 0\n\t"
"s_waitcnt lgkmcnt(2)\n\t"
"v_mfma_f32_16x16x32_bf16 v[226:229], v[226:229], a[208:211], 0\n\t"
"s_mov_b64 exec, s[44:45]\n\t"
"ds_write_b128 v240, v[230:233] offset:6144\n\t"
"s_mov_b64 exec, -1\n\t"
"v_mfma_f32_16x16x32_bf16 v[154:157], a[216:219], a[192:195], v[154:157]\n\t"
"v_mfma_f32_16x16x32_bf16 v[154:157], a[148:151], a[196:199], v[154:157]\n\t"
"v_mfma_f32_16x16x32_bf16 v[154:157], a[220:223], a[200:203], v[154:157]\n\t"
"v_mfma_f32_16x16x32_bf16 v[158:161], a[216:219], a[196:199], v[158:161]\n\t"
"v_cvt_pk_bf16_f32 v222, v222, v223\n\t"
"v_cvt_pk_bf16_f32 v223, v224, v225\n\t"
"v_mfma_f32_16x16x32_bf16 v[158:161], a[148:151], a[200:203], v[158:161]\n\t"
"v_cvt_pk_bf16_f32 v224, v226, v227\n\t"
"v_cvt_pk_bf16_f32 v225, v228, v229\n\t"
"v_mfma_f32_16x16x32_bf16 v[158:161], a[220:223], a[204:207], v[158:161]\n\t"
"s_mov_b32 s20, 1290240\n\t"
"buffer_load_dwordx4 a[216:219], v252, s[8:11], s20 offen offset:0 sc1\n\t"
"s_waitcnt vmcnt(37)\n\t"
"v_mfma_f32_16x16x32_bf16 v[162:165], a[156:159], a[168:171], v[162:165]\n\t"
"v_mfma_f32_16x16x32_bf16 v[162:165], a[224:227], a[172:175], v[162:165]\n\t"
"v_mfma_f32_16x16x32_bf16 v[162:165], a[164:167], a[176:179], v[162:165]\n\t"
"buffer_load_dwordx4 a[220:223], v252, s[8:11], s20 offen offset:1024 sc1\n\t"
"v_mfma_f32_16x16x32_bf16 v[166:169], a[156:159], a[172:175], v[166:169]\n\t"
"v_mfma_f32_16x16x32_bf16 v[166:169], a[224:227], a[176:179], v[166:169]\n\t"
"v_mfma_f32_16x16x32_bf16 v[166:169], a[164:167], a[180:183], v[166:169]\n\t"
"s_mov_b64 exec, s[44:45]\n\t"
"ds_write_b128 v240, v[222:225] offset:7680\n\t"
"s_mov_b64 exec, -1\n\t"
"v_mfma_f32_16x16x32_bf16 v[170:173], a[156:159], a[176:179], v[170:173]\n\t"
"v_mfma_f32_16x16x32_bf16 v[170:173], a[224:227], a[180:183], v[170:173]\n\t"
"v_mfma_f32_16x16x32_bf16 v[170:173], a[164:167], a[184:187], v[170:173]\n\t"
"v_mfma_f32_16x16x32_bf16 v[174:177], a[156:159], a[180:183], v[174:177]\n\t"
"v_mfma_f32_16x16x32_bf16 v[174:177], a[224:227], a[184:187], v[174:177]\n\t"
"v_mfma_f32_16x16x32_bf16 v[174:177], a[164:167], a[188:191], v[174:177]\n\t"
"s_waitcnt lgkmcnt(0)\n\t"
"s_barrier\n\t"
"v_mfma_f32_16x16x32_bf16 v[178:181], a[156:159], a[184:187], v[178:181]\n\t"
"ds_read_b128 a[168:171], v242 offset:0\n\t"
"v_mfma_f32_16x16x32_bf16 v[178:181], a[224:227], a[188:191], v[178:181]\n\t"
"ds_read_b128 a[172:175], v242 offset:512\n\t"
"v_mfma_f32_16x16x32_bf16 v[178:181], a[164:167], a[192:195], v[178:181]\n\t"
"ds_read_b128 a[176:179], v242 offset:1024\n\t"
"s_add_u32 m0, s25, 0\n\t"
"s_nop 0\n\t"
"buffer_load_dwordx4 v246, s[12:15], 0 offen offset:0 nt lds\n\t"
"ds_read_b128 a[180:183], v242 offset:1536\n\t"
"ds_read_b128 a[40:43], v252 offset:30720\n\t"
"s_add_u32 m0, s26, 0\n\t"
"s_mov_b64 exec, s[36:37]\n\t"
"buffer_load_dwordx4 v246, s[12:15], s27 offen offset:0 nt lds\n\t"
"s_mov_b64 exec, -1\n\t"
"v_mfma_f32_16x16x32_bf16 v[182:185], a[156:159], a[188:191], v[182:185]\n\t"
"ds_read_b128 a[184:187], v242 offset:2048\n\t"
"v_mfma_f32_16x16x32_bf16 v[182:185], a[224:227], a[192:195], v[182:185]\n\t"
"ds_read_b128 a[188:191], v242 offset:2560\n\t"
"v_mfma_f32_16x16x32_bf16 v[182:185], a[164:167], a[196:199], v[182:185]\n\t"
"s_add_u32 m0, s24, 0\n\t"
"s_add_u32 s28, s21, 0\n\t"
"buffer_load_dwordx4 v245, s[12:15], s28 offen offset:0 nt lds\n\t"
"v_mfma_f32_16x16x32_bf16 v[186:189], a[156:159], a[192:195], v[186:189]\n\t"
"ds_read_b128 a[192:195], v242 offset:3072\n\t"
"v_mfma_f32_16x16x32_bf16 v[186:189], a[224:227], a[196:199], v[186:189]\n\t"
"v_mfma_f32_16x16x32_bf16 v[186:189], a[164:167], a[200:203], v[186:189]\n\t"
"s_add_u32 m0, s24, 6144\n\t"
"s_add_u32 s28, s21, 196608\n\t"
"buffer_load_dwordx4 v245, s[12:15], s28 offen offset:0 nt lds\n\t"
"s_add_u32 m0, s24, 12288\n\t"
"s_add_u32 s28, s21, 393216\n\t"
"buffer_load_dwordx4 v245, s[12:15], s28 offen offset:0 nt lds\n\t"
"v_mfma_f32_16x16x32_bf16 v[190:193], a[156:159], a[196:199], v[190:193]\n\t"
"ds_read_b128 a[196:199], v242 offset:3584\n\t"
"v_mfma_f32_16x16x32_bf16 v[190:193], a[224:227], a[200:203], v[190:193]\n\t"
"ds_read_b128 a[200:203], v242 offset:4096\n\t"
"v_mfma_f32_16x16x32_bf16 v[190:193], a[164:167], a[204:207], v[190:193]\n\t"
"ds_write_b128 v248, a[8:11] offset:18432\n\t"
"s_add_u32 m0, s24, 19968\n\t"
"s_add_u32 s28, s21, 589824\n\t"
"buffer_load_dwordx4 v245, s[12:15], s28 offen offset:0 nt lds\n\t"
"s_waitcnt vmcnt(41) lgkmcnt(8)\n\t"
"v_mfma_f32_16x16x32_bf16 v[0:3], a[228:231], a[168:171], v[0:3]\n\t"
"ds_read_b128 a[204:207], v242 offset:4608\n\t"
"v_mfma_f32_16x16x32_bf16 v[0:3], a[232:235], a[172:175], v[0:3]\n\t"
"v_mfma_f32_16x16x32_bf16 v[0:3], a[236:239], a[176:179], v[0:3]\n\t"
"s_add_u32 m0, s24, 26112\n\t"
"s_add_u32 s28, s21, 786432\n\t"
"buffer_load_dwordx4 v245, s[12:15], s28 offen offset:0 nt lds\n\t"
"s_add_u32 m0, s24, 32256\n\t"
"s_add_u32 s28, s21, 983040\n\t"
"buffer_load_dwordx4 v245, s[12:15], s28 offen offset:0 nt lds\n\t"
"s_waitcnt lgkmcnt(8)\n\t"
"v_mfma_f32_16x16x32_bf16 v[4:7], a[228:231], a[172:175], v[4:7]\n\t"
"v_mfma_f32_16x16x32_bf16 v[4:7], a[232:235], a[176:179], v[4:7]\n\t"
"v_mfma_f32_16x16x32_bf16 v[4:7], a[236:239], a[180:183], v[4:7]\n\t"
"s_add_u32 s29, s22, 590208\n\t"
"s_waitcnt lgkmcnt(7)\n\t"
"buffer_store_dwordx4 a[40:43], v247, s[16:19], s29 offen offset:0 nt\n\t"
"s_add_u32 m0, s24, 39936\n\t"
"s_add_u32 s28, s21, 1179648\n\t"
"buffer_load_dwordx4 v245, s[12:15], s28 offen offset:0 nt lds\n\t"
"s_waitcnt lgkmcnt(6)\n\t"
"v_mfma_f32_16x16x32_bf16 v[8:11], a[228:231], a[176:179], v[8:11]\n\t"
"v_mfma_f32_16x16x32_bf16 v[8:11], a[232:235], a[180:183], v[8:11]\n\t"
"v_mfma_f32_16x16x32_bf16 v[8:11], a[236:239], a[184:187], v[8:11]\n\t"
"s_add_u32 m0, s24, 46080\n\t"
"s_add_u32 s28, s21, 1376256\n\t"
"buffer_load_dwordx4 v245, s[12:15], s28 offen offset:0 nt lds\n\t"
"ds_write_b64 v249, a[136:137] offset:18432\n\t"
"s_add_u32 m0, s24, 52224\n\t"
"s_add_u32 s28, s21, 1572864\n\t"
"buffer_load_dwordx4 v245, s[12:15], s28 offen offset:0 nt lds\n\t"
"s_waitcnt lgkmcnt(6)\n\t"
"v_mfma_f32_16x16x32_bf16 v[12:15], a[228:231], a[180:183], v[12:15]\n\t"
"v_mfma_f32_16x16x32_bf16 v[12:15], a[232:235], a[184:187], v[12:15]\n\t"
"v_mfma_f32_16x16x32_bf16 v[12:15], a[236:239], a[188:191], v[12:15]\n\t"
"s_add_u32 m0, s24, 59904\n\t"
"s_add_u32 s28, s21, 1769472\n\t"
"buffer_load_dwordx4 v245, s[12:15], s28 offen offset:0 nt lds\n\t"
"s_waitcnt lgkmcnt(5)\n\t"
"v_mfma_f32_16x16x32_bf16 v[16:19], a[228:231], a[184:187], v[16:19]\n\t"
"v_mfma_f32_16x16x32_bf16 v[16:19], a[232:235], a[188:191], v[16:19]\n\t"
"v_mfma_f32_16x16x32_bf16 v[16:19], a[236:239], a[192:195], v[16:19]\n\t"
"s_add_u32 m0, s24, 66048\n\t"
"s_add_u32 s28, s21, 1966080\n\t"
"buffer_load_dwordx4 v245, s[12:15], s28 offen offset:0 nt lds\n\t"
"s_add_u32 m0, s24, 72192\n\t"
"s_add_u32 s28, s21, 2162688\n\t"
"buffer_load_dwordx4 v245, s[12:15], s28 offen offset:0 nt lds\n\t"
"s_waitcnt lgkmcnt(4)\n\t"
"v_mfma_f32_16x16x32_bf16 v[20:23], a[228:231], a[188:191], v[20:23]\n\t"
"v_mfma_f32_16x16x32_bf16 v[20:23], a[232:235], a[192:195], v[20:23]\n\t"
"ds_read_b128 a[108:111], v252 offset:31744\n\t"
"v_mfma_f32_16x16x32_bf16 v[20:23], a[236:239], a[196:199], v[20:23]\n\t"
"s_add_u32 m0, s24, 79872\n\t"
"s_add_u32 s28, s21, 2359296\n\t"
"buffer_load_dwordx4 v245, s[12:15], s28 offen offset:0 nt lds\n\t"
"s_waitcnt lgkmcnt(4)\n\t"
"v_mfma_f32_16x16x32_bf16 v[24:27], a[228:231], a[192:195], v[24:27]\n\t"
"ds_read_b128 a[140:143], v252 offset:32768\n\t"
"v_mfma_f32_16x16x32_bf16 v[24:27], a[232:235], a[196:199], v[24:27]\n\t"
"v_mfma_f32_16x16x32_bf16 v[24:27], a[236:239], a[200:203], v[24:27]\n\t"
"ds_write_b128 v248, a[12:15] offset:24576\n\t"
"s_add_u32 m0, s24, 86016\n\t"
"s_add_u32 s28, s21, 2555904\n\t"
"buffer_load_dwordx4 v245, s[12:15], s28 offen offset:0 nt lds\n\t"
"s_add_u32 m0, s24, 92160\n\t"
"s_add_u32 s28, s21, 2752512\n\t"
"buffer_load_dwordx4 v245, s[12:15], s28 offen offset:0 nt lds\n\t"
"s_waitcnt lgkmcnt(4)\n\t"
"v_mfma_f32_16x16x32_bf16 v[28:31], a[228:231], a[196:199], v[28:31]\n\t"
"v_mfma_f32_16x16x32_bf16 v[28:31], a[232:235], a[200:203], v[28:31]\n\t"
"v_mfma_f32_16x16x32_bf16 v[28:31], a[236:239], a[204:207], v[28:31]\n\t"
"s_add_u32 m0, s24, 99840\n\t"
"s_add_u32 s28, s21, 2949120\n\t"
"buffer_load_dwordx4 v245, s[12:15], s28 offen offset:0 nt lds\n\t"
"s_waitcnt vmcnt(51)\n\t"
"v_mfma_f32_16x16x32_bf16 v[32:35], a[240:243], a[168:171], v[32:35]\n\t"
"v_mfma_f32_16x16x32_bf16 v[32:35], a[244:247], a[172:175], v[32:35]\n\t"
"v_mfma_f32_16x16x32_bf16 v[32:35], a[248:251], a[176:179], v[32:35]\n\t"
"s_add_u32 m0, s24, 105984\n\t"
"s_add_u32 s28, s21, 3145728\n\t"
"buffer_load_dwordx4 v245, s[12:15], s28 offen offset:0 nt lds\n\t"
"s_add_u32 m0, s24, 112128\n\t"
"s_add_u32 s28, s21, 3342336\n\t"
"buffer_load_dwordx4 v245, s[12:15], s28 offen offset:0 nt lds\n\t"
"v_mfma_f32_16x16x32_bf16 v[36:39], a[240:243], a[172:175], v[36:39]\n\t"
"ds_read_b64_tr_b16 v[230:231], v238 offset:1024\n\t"
"v_mfma_f32_16x16x32_bf16 v[36:39], a[244:247], a[176:179], v[36:39]\n\t"
"ds_read_b64_tr_b16 v[232:233], v238 offset:2560\n\t"
"v_mfma_f32_16x16x32_bf16 v[36:39], a[248:251], a[180:183], v[36:39]\n\t"
"ds_write_b64 v249, a[138:139] offset:24576\n\t"
"v_mfma_f32_16x16x32_bf16 v[40:43], a[240:243], a[176:179], v[40:43]\n\t"
"ds_read_b64_tr_b16 v[234:235], v239 offset:1024\n\t"
"v_mfma_f32_16x16x32_bf16 v[40:43], a[244:247], a[180:183], v[40:43]\n\t"
"v_mfma_f32_16x16x32_bf16 v[40:43], a[248:251], a[184:187], v[40:43]\n\t"
"ds_read_b64_tr_b16 v[236:237], v239 offset:2560\n\t"
"v_mfma_f32_16x16x32_bf16 v[44:47], a[240:243], a[180:183], v[44:47]\n\t"
"v_mfma_f32_16x16x32_bf16 v[44:47], a[244:247], a[184:187], v[44:47]\n\t"
"v_mfma_f32_16x16x32_bf16 v[44:47], a[248:251], a[188:191], v[44:47]\n\t"
"s_waitcnt lgkmcnt(2)\n\t"
"s_barrier\n\t"
"v_mfma_f32_16x16x32_bf16 v[48:51], a[240:243], a[184:187], v[48:51]\n\t"
"v_mfma_f32_16x16x32_bf16 v[48:51], a[244:247], a[188:191], v[48:51]\n\t"
"v_mfma_f32_16x16x32_bf16 v[48:51], a[248:251], a[192:195], v[48:51]\n\t"
"ds_write_b128 v248, a[16:19] offset:30720\n\t"
"v_mfma_f32_16x16x32_bf16 v[52:55], a[240:243], a[188:191], v[52:55]\n\t"
"v_mfma_f32_16x16x32_bf16 v[52:55], a[244:247], a[192:195], v[52:55]\n\t"
"ds_read_b64_tr_b16 v[222:223], v238 offset:20992\n\t"
"v_mfma_f32_16x16x32_bf16 v[52:55], a[248:251], a[196:199], v[52:55]\n\t"
"ds_read_b64_tr_b16 v[224:225], v238 offset:22528\n\t"
"v_mfma_f32_16x16x32_bf16 v[230:233], v[230:233], a[208:211], 0\n\t"
"v_mfma_f32_16x16x32_bf16 v[56:59], a[240:243], a[192:195], v[56:59]\n\t"
"v_mfma_f32_16x16x32_bf16 v[56:59], a[244:247], a[196:199], v[56:59]\n\t"
"ds_read_b64_tr_b16 v[226:227], v239 offset:20992\n\t"
"v_mfma_f32_16x16x32_bf16 v[56:59], a[248:251], a[200:203], v[56:59]\n\t"
"ds_read_b64_tr_b16 v[228:229], v239 offset:22528\n\t"
"s_waitcnt lgkmcnt(5)\n\t"
"v_mfma_f32_16x16x32_bf16 v[234:237], v[234:237], a[208:211], 0\n\t"
"buffer_load_dwordx4 a[128:131], v252, s[8:11], s20 offen offset:2048 sc1\n\t"
"v_mfma_f32_16x16x32_bf16 v[60:63], a[240:243], a[196:199], v[60:63]\n\t"
"v_cvt_pk_bf16_f32 v230, v230, v231\n\t"
"v_cvt_pk_bf16_f32 v231, v232, v233\n\t"
"v_mfma_f32_16x16x32_bf16 v[60:63], a[244:247], a[200:203], v[60:63]\n\t"
"v_mfma_f32_16x16x32_bf16 v[60:63], a[248:251], a[204:207], v[60:63]\n\t"
"ds_write_b64 v249, a[144:145] offset:30720\n\t"
"s_waitcnt vmcnt(51)\n\t"
"v_mfma_f32_16x16x32_bf16 v[64:67], a[252:255], a[168:171], v[64:67]\n\t"
"ds_read_b128 a[168:171], v242 offset:9216\n\t"
"v_mfma_f32_16x16x32_bf16 v[64:67], a[44:47], a[172:175], v[64:67]\n\t"
"v_cvt_pk_bf16_f32 v232, v234, v235\n\t"
"v_cvt_pk_bf16_f32 v233, v236, v237\n\t"
"v_mfma_f32_16x16x32_bf16 v[64:67], a[64:67], a[176:179], v[64:67]\n\t"
"s_mov_b32 s20, 1302528\n\t"
"buffer_load_dwordx4 a[136:139], v252, s[8:11], s20 offen offset:0 sc1\n\t"
"v_mfma_f32_16x16x32_bf16 v[68:71], a[252:255], a[172:175], v[68:71]\n\t"
"ds_read_b128 a[172:175], v242 offset:9728\n\t"
"v_mfma_f32_16x16x32_bf16 v[68:71], a[44:47], a[176:179], v[68:71]\n\t"
"v_mfma_f32_16x16x32_bf16 v[68:71], a[64:67], a[180:183], v[68:71]\n\t"
"buffer_load_dwordx4 a[4:7], v252, s[8:11], s20 offen offset:1024 sc1\n\t"
"s_waitcnt lgkmcnt(5)\n\t"
"v_mfma_f32_16x16x32_bf16 v[222:225], v[222:225], a[208:211], 0\n\t"
"ds_write_b128 v248, a[20:23] offset:36864\n\t"
"v_mfma_f32_16x16x32_bf16 v[72:75], a[252:255], a[176:179], v[72:75]\n\t"
"ds_read_b128 a[176:179], v242 offset:10240\n\t"
"v_mfma_f32_16x16x32_bf16 v[72:75], a[44:47], a[180:183], v[72:75]\n\t"
"v_mfma_f32_16x16x32_bf16 v[72:75], a[64:67], a[184:187], v[72:75]\n\t"
"buffer_load_dwordx4 a[12:15], v252, s[8:11], s20 offen offset:2048 sc1\n\t"
"s_waitcnt lgkmcnt(5)\n\t"
"v_mfma_f32_16x16x32_bf16 v[226:229], v[226:229], a[208:211], 0\n\t"
"ds_read_b128 a[52:55], v252 offset:18432\n\t"
"v_mfma_f32_16x16x32_bf16 v[76:79], a[252:255], a[180:183], v[76:79]\n\t"
"v_cvt_pk_bf16_f32 v222, v222, v223\n\t"
"v_cvt_pk_bf16_f32 v223, v224, v225\n\t"
"v_mfma_f32_16x16x32_bf16 v[76:79], a[44:47], a[184:187], v[76:79]\n\t"
"ds_read_b128 a[180:183], v242 offset:10752\n\t"
"v_mfma_f32_16x16x32_bf16 v[76:79], a[64:67], a[188:191], v[76:79]\n\t"
"s_mov_b32 s20, 1314816\n\t"
"buffer_load_dwordx4 a[224:227], v252, s[8:11], s20 offen offset:0 sc1\n\t"
"v_mfma_f32_16x16x32_bf16 v[80:83], a[252:255], a[184:187], v[80:83]\n\t"
"ds_read_b128 a[184:187], v242 offset:11264\n\t"
"v_mfma_f32_16x16x32_bf16 v[80:83], a[44:47], a[188:191], v[80:83]\n\t"
"v_cvt_pk_bf16_f32 v224, v226, v227\n\t"
"v_cvt_pk_bf16_f32 v225, v228, v229\n\t"
"v_mfma_f32_16x16x32_bf16 v[80:83], a[64:67], a[192:195], v[80:83]\n\t"
"buffer_load_dwordx4 a[228:231], v252, s[8:11], s20 offen offset:1024 sc1\n\t"
"v_mfma_f32_16x16x32_bf16 v[84:87], a[252:255], a[188:191], v[84:87]\n\t"
"ds_read_b128 a[188:191], v242 offset:11776\n\t"
"v_mfma_f32_16x16x32_bf16 v[84:87], a[44:47], a[192:195], v[84:87]\n\t"
"ds_read_b128 a[92:95], v252 offset:19456\n\t"
"v_mfma_f32_16x16x32_bf16 v[84:87], a[64:67], a[196:199], v[84:87]\n\t"
"buffer_load_dwordx4 a[232:235], v252, s[8:11], s20 offen offset:2048 sc1\n\t"
"v_mfma_f32_16x16x32_bf16 v[88:91], a[252:255], a[192:195], v[88:91]\n\t"
"ds_read_b128 a[192:195], v242 offset:12288\n\t"
"v_mfma_f32_16x16x32_bf16 v[88:91], a[44:47], a[196:199], v[88:91]\n\t"
"ds_read_b128 v[194:197], v252 offset:20480\n\t"
"v_mfma_f32_16x16x32_bf16 v[88:91], a[64:67], a[200:203], v[88:91]\n\t"
"ds_write_b64 v249, a[146:147] offset:36864\n\t"
"v_mfma_f32_16x16x32_bf16 v[92:95], a[252:255], a[196:199], v[92:95]\n\t"
"ds_read_b128 a[196:199], v242 offset:12800\n\t"
"v_mfma_f32_16x16x32_bf16 v[92:95], a[44:47], a[200:203], v[92:95]\n\t"
"ds_read_b128 a[200:203], v242 offset:13312\n\t"
"v_mfma_f32_16x16x32_bf16 v[92:95], a[64:67], a[204:207], v[92:95]\n\t"
"s_mov_b32 s20, 0\n\t"
"buffer_load_dwordx4 a[236:239], v252, s[8:11], s20 offen offset:0 sc1\n\t"
"s_waitcnt vmcnt(55) lgkmcnt(10)\n\t"
"v_mfma_f32_16x16x32_bf16 v[98:101], a[60:63], a[168:171], v[98:101]\n\t"
"ds_read_b128 a[204:207], v242 offset:13824\n\t"
"v_mfma_f32_16x16x32_bf16 v[98:101], a[104:107], a[172:175], v[98:101]\n\t"
"v_mfma_f32_16x16x32_bf16 v[98:101], a[72:75], a[176:179], v[98:101]\n\t"
"buffer_load_dwordx4 a[240:243], v252, s[8:11], s20 offen offset:1024 sc1\n\t"
"s_waitcnt lgkmcnt(9)\n\t"
"v_mfma_f32_16x16x32_bf16 v[102:105], a[60:63], a[172:175], v[102:105]\n\t"
"v_mfma_f32_16x16x32_bf16 v[102:105], a[104:107], a[176:179], v[102:105]\n\t"
"v_mfma_f32_16x16x32_bf16 v[102:105], a[72:75], a[180:183], v[102:105]\n\t"
"buffer_load_dwordx4 a[244:247], v252, s[8:11], s20 offen offset:2048 sc1\n\t"
"s_waitcnt lgkmcnt(0)\n\t"
"s_barrier\n\t"
"v_mfma_f32_16x16x32_bf16 v[106:109], a[60:63], a[176:179], v[106:109]\n\t"
"v_mfma_f32_16x16x32_bf16 v[106:109], a[104:107], a[180:183], v[106:109]\n\t"
"v_mfma_f32_16x16x32_bf16 v[106:109], a[72:75], a[184:187], v[106:109]\n\t"
"s_mov_b64 exec, s[44:45]\n\t"
"ds_write_b128 v240, v[230:233] offset:4608\n\t"
"s_mov_b64 exec, -1\n\t"
"s_mov_b64 exec, s[44:45]\n\t"
"ds_write_b128 v240, v[222:225] offset:6144\n\t"
"s_mov_b64 exec, -1\n\t"
"v_mfma_f32_16x16x32_bf16 v[110:113], a[60:63], a[180:183], v[110:113]\n\t"
"ds_read_b64_tr_b16 v[230:231], v238 offset:40960\n\t"
"v_mfma_f32_16x16x32_bf16 v[110:113], a[104:107], a[184:187], v[110:113]\n\t"
"ds_read_b64_tr_b16 v[232:233], v238 offset:42496\n\t"
"v_mfma_f32_16x16x32_bf16 v[110:113], a[72:75], a[188:191], v[110:113]\n\t"
"ds_read_b64_tr_b16 v[234:235], v239 offset:40960\n\t"
"ds_read_b64_tr_b16 v[236:237], v239 offset:42496\n\t"
"s_mov_b32 s20, 12288\n\t"
"buffer_load_dwordx4 a[144:147], v252, s[8:11], s20 offen offset:0 sc1\n\t"
"v_mfma_f32_16x16x32_bf16 v[114:117], a[60:63], a[184:187], v[114:117]\n\t"
"v_subrev_u32 v238, 59904, v238\n\t"
"v_subrev_u32 v239, 59904, v239\n\t"
"v_mfma_f32_16x16x32_bf16 v[114:117], a[104:107], a[188:191], v[114:117]\n\t"
"ds_read_b64_tr_b16 v[222:223], v238 offset:1024\n\t"
"v_mfma_f32_16x16x32_bf16 v[114:117], a[72:75], a[192:195], v[114:117]\n\t"
"buffer_load_dwordx4 a[20:23], v252, s[8:11], s20 offen offset:1024 sc1\n\t"
"v_mfma_f32_16x16x32_bf16 v[118:121], a[60:63], a[188:191], v[118:121]\n\t"
"ds_read_b64_tr_b16 v[224:225], v238 offset:2560\n\t"
"v_mfma_f32_16x16x32_bf16 v[118:121], a[104:107], a[192:195], v[118:121]\n\t"
"ds_read_b64_tr_b16 v[226:227], v239 offset:1024\n\t"
"v_mfma_f32_16x16x32_bf16 v[118:121], a[72:75], a[196:199], v[118:121]\n\t"
"buffer_load_dwordx4 a[248:251], v252, s[8:11], s20 offen offset:2048 sc1\n\t"
"s_waitcnt lgkmcnt(5)\n\t"
"v_mfma_f32_16x16x32_bf16 v[230:233], v[230:233], a[208:211], 0\n\t"
"ds_read_b64_tr_b16 v[228:229], v239 offset:2560\n\t"
"s_waitcnt lgkmcnt(4)\n\t"
"v_mfma_f32_16x16x32_bf16 v[234:237], v[234:237], a[208:211], 0\n\t"
"ds_write_b128 v248, a[24:27] offset:18432\n\t"
"v_mfma_f32_16x16x32_bf16 v[122:125], a[60:63], a[192:195], v[122:125]\n\t"
"v_mfma_f32_16x16x32_bf16 v[122:125], a[104:107], a[196:199], v[122:125]\n\t"
"v_mfma_f32_16x16x32_bf16 v[122:125], a[72:75], a[200:203], v[122:125]\n\t"
"s_mov_b32 s20, 24576\n\t"
"buffer_load_dwordx4 a[252:255], v252, s[8:11], s20 offen offset:0 sc1\n\t"
"v_mfma_f32_16x16x32_bf16 v[126:129], a[60:63], a[196:199], v[126:129]\n\t"
"v_cvt_pk_bf16_f32 v230, v230, v231\n\t"
"v_cvt_pk_bf16_f32 v231, v232, v233\n\t"
"v_mfma_f32_16x16x32_bf16 v[126:129], a[104:107], a[200:203], v[126:129]\n\t"
"v_cvt_pk_bf16_f32 v232, v234, v235\n\t"
"v_cvt_pk_bf16_f32 v233, v236, v237\n\t"
"v_mfma_f32_16x16x32_bf16 v[126:129], a[72:75], a[204:207], v[126:129]\n\t"
"s_mov_b64 exec, s[44:45]\n\t"
"ds_write_b128 v240, v[230:233] offset:7680\n\t"
"s_mov_b64 exec, -1\n\t"
"s_waitcnt lgkmcnt(4)\n\t"
"v_mfma_f32_16x16x32_bf16 v[222:225], v[222:225], a[208:211], 0\n\t"
"ds_read_b64_tr_b16 v[230:231], v238 offset:20992\n\t"
"ds_read_b64_tr_b16 v[232:233], v238 offset:22528\n\t"
"s_waitcnt lgkmcnt(4)\n\t"
"v_mfma_f32_16x16x32_bf16 v[226:229], v[226:229], a[208:211], 0\n\t"
"ds_read_b64_tr_b16 v[234:235], v239 offset:20992\n\t"
"ds_read_b64_tr_b16 v[236:237], v239 offset:22528\n\t"
"s_waitcnt vmcnt(57)\n\t"
"v_mfma_f32_16x16x32_bf16 v[130:133], a[80:83], a[168:171], v[130:133]\n\t"
"v_mfma_f32_16x16x32_bf16 v[130:133], a[68:71], a[172:175], v[130:133]\n\t"
"v_mfma_f32_16x16x32_bf16 v[130:133], v[198:201], a[176:179], v[130:133]\n\t"
"ds_write_b64 v249, a[152:153] offset:18432\n\t"
"v_mfma_f32_16x16x32_bf16 v[134:137], a[80:83], a[172:175], v[134:137]\n\t"
"v_cvt_pk_bf16_f32 v222, v222, v223\n\t"
"v_cvt_pk_bf16_f32 v223, v224, v225\n\t"
"v_mfma_f32_16x16x32_bf16 v[134:137], a[68:71], a[176:179], v[134:137]\n\t"
"v_cvt_pk_bf16_f32 v224, v226, v227\n\t"
"v_cvt_pk_bf16_f32 v225, v228, v229\n\t"
"v_mfma_f32_16x16x32_bf16 v[134:137], v[198:201], a[180:183], v[134:137]\n\t"
"s_mov_b64 exec, s[44:45]\n\t"
"ds_write_b128 v240, v[222:225] offset:0\n\t"
"s_mov_b64 exec, -1\n\t"
"v_mfma_f32_16x16x32_bf16 v[138:141], a[80:83], a[176:179], v[138:141]\n\t"
"ds_read_b64_tr_b16 v[222:223], v238 offset:40960\n\t"
"v_mfma_f32_16x16x32_bf16 v[138:141], a[68:71], a[180:183], v[138:141]\n\t"
"ds_read_b64_tr_b16 v[224:225], v238 offset:42496\n\t"
"v_mfma_f32_16x16x32_bf16 v[138:141], v[198:201], a[184:187], v[138:141]\n\t"
"ds_read_b64_tr_b16 v[226:227], v239 offset:40960\n\t"
"ds_read_b64_tr_b16 v[228:229], v239 offset:42496\n\t"
"v_mfma_f32_16x16x32_bf16 v[142:145], a[80:83], a[180:183], v[142:145]\n\t"
"v_mfma_f32_16x16x32_bf16 v[142:145], a[68:71], a[184:187], v[142:145]\n\t"
"v_mfma_f32_16x16x32_bf16 v[142:145], v[198:201], a[188:191], v[142:145]\n\t"
"ds_write_b128 v248, a[28:31] offset:24576\n\t"
"s_waitcnt lgkmcnt(9)\n\t"
"v_mfma_f32_16x16x32_bf16 v[230:233], v[230:233], a[208:211], 0\n\t"
"ds_write_b64 v249, a[154:155] offset:24576\n\t"
"s_waitcnt lgkmcnt(8)\n\t"
"v_mfma_f32_16x16x32_bf16 v[234:237], v[234:237], a[208:211], 0\n\t"
"v_mfma_f32_16x16x32_bf16 v[146:149], a[80:83], a[184:187], v[146:149]\n\t"
"v_mfma_f32_16x16x32_bf16 v[146:149], a[68:71], a[188:191], v[146:149]\n\t"
"v_mfma_f32_16x16x32_bf16 v[146:149], v[198:201], a[192:195], v[146:149]\n\t"
"v_mfma_f32_16x16x32_bf16 v[150:153], a[80:83], a[188:191], v[150:153]\n\t"
"s_nop 0\n\t"
"v_cvt_pk_bf16_f32 v230, v230, v231\n\t"
"v_cvt_pk_bf16_f32 v231, v232, v233\n\t"
"v_mfma_f32_16x16x32_bf16 v[150:153], a[68:71], a[192:195], v[150:153]\n\t"
"v_cvt_pk_bf16_f32 v232, v234, v235\n\t"
"v_mfma_f32_16x16x32_bf16 v[150:153], v[198:201], a[196:199], v[150:153]\n\t"
"v_cvt_pk_bf16_f32 v233, v236, v237\n\t"
"s_waitcnt lgkmcnt(4)\n\t"
"v_mfma_f32_16x16x32_bf16 v[222:225], v[222:225], a[208:211], 0\n\t"
"s_mov_b64 exec, s[44:45]\n\t"
"ds_write_b128 v240, v[230:233] offset:1536\n\t"
"s_mov_b64 exec, -1\n\t"
"s_waitcnt lgkmcnt(3)\n\t"
"v_mfma_f32_16x16x32_bf16 v[226:229], v[226:229], a[208:211], 0\n\t"
"ds_read_b128 v[202:205], v252 offset:30720\n\t"
"v_mfma_f32_16x16x32_bf16 v[154:157], a[80:83], a[192:195], v[154:157]\n\t"
"v_mfma_f32_16x16x32_bf16 v[154:157], a[68:71], a[196:199], v[154:157]\n\t"
"ds_read_b128 v[210:213], v252 offset:31744\n\t"
"v_mfma_f32_16x16x32_bf16 v[154:157], v[198:201], a[200:203], v[154:157]\n\t"
"v_mfma_f32_16x16x32_bf16 v[158:161], a[80:83], a[196:199], v[158:161]\n\t"
"v_cvt_pk_bf16_f32 v222, v222, v223\n\t"
"v_cvt_pk_bf16_f32 v223, v224, v225\n\t"
"v_mfma_f32_16x16x32_bf16 v[158:161], a[68:71], a[200:203], v[158:161]\n\t"
"v_cvt_pk_bf16_f32 v224, v226, v227\n\t"
"v_mfma_f32_16x16x32_bf16 v[158:161], v[198:201], a[204:207], v[158:161]\n\t"
"v_cvt_pk_bf16_f32 v225, v228, v229\n\t"
"ds_read_b128 a[0:3], v252 offset:32768\n\t"
"s_waitcnt vmcnt(52)\n\t"
"v_mfma_f32_16x16x32_bf16 v[162:165], a[88:91], a[168:171], v[162:165]\n\t"
"v_mfma_f32_16x16x32_bf16 v[162:165], a[96:99], a[172:175], v[162:165]\n\t"
"v_mfma_f32_16x16x32_bf16 v[162:165], a[112:115], a[176:179], v[162:165]\n\t"
"buffer_load_dwordx4 v[198:201], v252, s[8:11], s20 offen offset:1024 sc1\n\t"
"v_mfma_f32_16x16x32_bf16 v[166:169], a[88:91], a[172:175], v[166:169]\n\t"
"v_mfma_f32_16x16x32_bf16 v[166:169], a[96:99], a[176:179], v[166:169]\n\t"
"v_mfma_f32_16x16x32_bf16 v[166:169], a[112:115], a[180:183], v[166:169]\n\t"
"s_mov_b64 exec, s[44:45]\n\t"
"ds_write_b128 v240, v[222:225] offset:3072\n\t"
"s_mov_b64 exec, -1\n\t"
"v_mfma_f32_16x16x32_bf16 v[170:173], a[88:91], a[176:179], v[170:173]\n\t"
"v_mfma_f32_16x16x32_bf16 v[170:173], a[96:99], a[180:183], v[170:173]\n\t"
"v_mfma_f32_16x16x32_bf16 v[170:173], a[112:115], a[184:187], v[170:173]\n\t"
"v_mfma_f32_16x16x32_bf16 v[174:177], a[88:91], a[180:183], v[174:177]\n\t"
"v_mfma_f32_16x16x32_bf16 v[174:177], a[96:99], a[184:187], v[174:177]\n\t"
"v_mfma_f32_16x16x32_bf16 v[174:177], a[112:115], a[188:191], v[174:177]\n\t"
"s_waitcnt lgkmcnt(0)\n\t"
"s_barrier\n\t"
"v_mfma_f32_16x16x32_bf16 v[178:181], a[88:91], a[184:187], v[178:181]\n\t"
"ds_read_b128 a[168:171], v242 offset:0\n\t"
"v_mfma_f32_16x16x32_bf16 v[178:181], a[96:99], a[188:191], v[178:181]\n\t"
"ds_read_b128 a[172:175], v242 offset:512\n\t"
"v_mfma_f32_16x16x32_bf16 v[178:181], a[112:115], a[192:195], v[178:181]\n\t"
"ds_read_b128 a[176:179], v242 offset:1024\n\t"
"ds_read_b128 a[180:183], v242 offset:1536\n\t"
"v_mfma_f32_16x16x32_bf16 v[182:185], a[88:91], a[188:191], v[182:185]\n\t"
"ds_read_b128 a[184:187], v242 offset:2048\n\t"
"v_mfma_f32_16x16x32_bf16 v[182:185], a[96:99], a[192:195], v[182:185]\n\t"
"ds_read_b128 a[188:191], v242 offset:2560\n\t"
"v_mfma_f32_16x16x32_bf16 v[182:185], a[112:115], a[196:199], v[182:185]\n\t"
"buffer_load_dwordx4 a[152:155], v252, s[8:11], s20 offen offset:2048 sc1\n\t"
"v_mfma_f32_16x16x32_bf16 v[186:189], a[88:91], a[192:195], v[186:189]\n\t"
"ds_read_b128 a[192:195], v242 offset:3072\n\t"
"v_mfma_f32_16x16x32_bf16 v[186:189], a[96:99], a[196:199], v[186:189]\n\t"
"v_mfma_f32_16x16x32_bf16 v[186:189], a[112:115], a[200:203], v[186:189]\n\t"
"s_mov_b32 s20, 184320\n\t"
"buffer_load_dwordx4 a[28:31], v252, s[8:11], s20 offen offset:0 sc1\n\t"
"v_mfma_f32_16x16x32_bf16 v[190:193], a[88:91], a[196:199], v[190:193]\n\t"
"ds_read_b128 a[196:199], v242 offset:3584\n\t"
"v_mfma_f32_16x16x32_bf16 v[190:193], a[96:99], a[200:203], v[190:193]\n\t"
"ds_read_b128 a[200:203], v242 offset:4096\n\t"
"v_mfma_f32_16x16x32_bf16 v[190:193], a[112:115], a[204:207], v[190:193]\n\t"
"ds_write_b128 v248, a[32:35] offset:30720\n\t"
"s_waitcnt vmcnt(51) lgkmcnt(7)\n\t"
"v_mfma_f32_16x16x32_bf16 v[0:3], v[206:209], a[168:171], v[0:3]\n\t"
"ds_read_b128 a[204:207], v242 offset:4608\n\t"
"v_mfma_f32_16x16x32_bf16 v[0:3], a[76:79], a[172:175], v[0:3]\n\t"
"v_mfma_f32_16x16x32_bf16 v[0:3], a[84:87], a[176:179], v[0:3]\n\t"
"ds_write_b64 v249, a[160:161] offset:30720\n\t"
"s_waitcnt lgkmcnt(8)\n\t"
"v_mfma_f32_16x16x32_bf16 v[4:7], v[206:209], a[172:175], v[4:7]\n\t"
"v_mfma_f32_16x16x32_bf16 v[4:7], a[76:79], a[176:179], v[4:7]\n\t"
"v_mfma_f32_16x16x32_bf16 v[4:7], a[84:87], a[180:183], v[4:7]\n\t"
"ds_write_b128 v248, a[36:39] offset:36864\n\t"
"s_waitcnt lgkmcnt(8)\n\t"
"v_mfma_f32_16x16x32_bf16 v[8:11], v[206:209], a[176:179], v[8:11]\n\t"
"v_cvt_pk_bf16_f32 v0, v0, v1\n\t"
"v_cvt_pk_bf16_f32 v2, v2, v3\n\t"
"v_mfma_f32_16x16x32_bf16 v[8:11], a[76:79], a[180:183], v[8:11]\n\t"
"v_mfma_f32_16x16x32_bf16 v[8:11], a[84:87], a[184:187], v[8:11]\n\t"
"v_accvgpr_write_b32 a40, v0\n\t"
"s_waitcnt lgkmcnt(7)\n\t"
"v_mfma_f32_16x16x32_bf16 v[12:15], v[206:209], a[180:183], v[12:15]\n\t"
"v_mfma_f32_16x16x32_bf16 v[12:15], a[76:79], a[184:187], v[12:15]\n\t"
"v_cvt_pk_bf16_f32 v4, v4, v5\n\t"
"v_mfma_f32_16x16x32_bf16 v[12:15], a[84:87], a[188:191], v[12:15]\n\t"
"s_waitcnt lgkmcnt(6)\n\t"
"v_mfma_f32_16x16x32_bf16 v[16:19], v[206:209], a[184:187], v[16:19]\n\t"
"v_cvt_pk_bf16_f32 v6, v6, v7\n\t"
"v_mfma_f32_16x16x32_bf16 v[16:19], a[76:79], a[188:191], v[16:19]\n\t"
"v_accvgpr_write_b32 a41, v2\n\t"
"v_mfma_f32_16x16x32_bf16 v[16:19], a[84:87], a[192:195], v[16:19]\n\t"
"s_waitcnt lgkmcnt(5)\n\t"
"v_mfma_f32_16x16x32_bf16 v[20:23], v[206:209], a[188:191], v[20:23]\n\t"
"ds_read_b128 a[8:11], v252 offset:18432\n\t"
"v_cvt_pk_bf16_f32 v8, v8, v9\n\t"
"v_mfma_f32_16x16x32_bf16 v[20:23], a[76:79], a[192:195], v[20:23]\n\t"
"ds_read_b128 a[16:19], v252 offset:19456\n\t"
"v_mfma_f32_16x16x32_bf16 v[20:23], a[84:87], a[196:199], v[20:23]\n\t"
"v_cvt_pk_bf16_f32 v10, v10, v11\n\t"
"s_waitcnt lgkmcnt(6)\n\t"
"v_mfma_f32_16x16x32_bf16 v[24:27], v[206:209], a[192:195], v[24:27]\n\t"
"ds_read_b128 a[24:27], v252 offset:20480\n\t"
"v_mfma_f32_16x16x32_bf16 v[24:27], a[76:79], a[196:199], v[24:27]\n\t"
"v_accvgpr_write_b32 a48, v4\n\t"
"v_accvgpr_write_b32 a49, v6\n\t"
"v_mfma_f32_16x16x32_bf16 v[24:27], a[84:87], a[200:203], v[24:27]\n\t"
"v_cvt_pk_bf16_f32 v12, v12, v13\n\t"
"s_waitcnt lgkmcnt(5)\n\t"
"v_mfma_f32_16x16x32_bf16 v[28:31], v[206:209], a[196:199], v[28:31]\n\t"
"v_cvt_pk_bf16_f32 v14, v14, v15\n\t"
"v_accvgpr_write_b32 a56, v8\n\t"
"v_mfma_f32_16x16x32_bf16 v[28:31], a[76:79], a[200:203], v[28:31]\n\t"
"v_accvgpr_write_b32 a57, v10\n\t"
"v_cvt_pk_bf16_f32 v16, v16, v17\n\t"
"v_mfma_f32_16x16x32_bf16 v[28:31], a[84:87], a[204:207], v[28:31]\n\t"
"buffer_load_dwordx4 v[206:209], v252, s[8:11], s20 offen offset:1024 sc1\n\t"
"s_waitcnt vmcnt(47)\n\t"
"v_mfma_f32_16x16x32_bf16 v[32:35], a[116:119], a[168:171], v[32:35]\n\t"
"v_cvt_pk_bf16_f32 v18, v18, v19\n\t"
"v_accvgpr_write_b32 a64, v12\n\t"
"v_mfma_f32_16x16x32_bf16 v[32:35], a[124:127], a[172:175], v[32:35]\n\t"
"v_accvgpr_write_b32 a65, v14\n\t"
"v_cvt_pk_bf16_f32 v20, v20, v21\n\t"
"v_mfma_f32_16x16x32_bf16 v[32:35], a[100:103], a[176:179], v[32:35]\n\t"
"ds_write_b64 v249, a[162:163] offset:36864\n\t"
"v_mfma_f32_16x16x32_bf16 v[36:39], a[116:119], a[172:175], v[36:39]\n\t"
"v_cvt_pk_bf16_f32 v22, v22, v23\n\t"
"v_mfma_f32_16x16x32_bf16 v[36:39], a[124:127], a[176:179], v[36:39]\n\t"
"v_accvgpr_write_b32 a72, v16\n\t"
"v_accvgpr_write_b32 a73, v18\n\t"
"v_mfma_f32_16x16x32_bf16 v[36:39], a[100:103], a[180:183], v[36:39]\n\t"
"v_cvt_pk_bf16_f32 v24, v24, v25\n\t"
"v_mfma_f32_16x16x32_bf16 v[40:43], a[116:119], a[176:179], v[40:43]\n\t"
"v_cvt_pk_bf16_f32 v26, v26, v27\n\t"
"v_mfma_f32_16x16x32_bf16 v[40:43], a[124:127], a[180:183], v[40:43]\n\t"
"v_accvgpr_write_b32 a80, v20\n\t"
"v_accvgpr_write_b32 a81, v22\n\t"
"v_mfma_f32_16x16x32_bf16 v[40:43], a[100:103], a[184:187], v[40:43]\n\t"
"v_cvt_pk_bf16_f32 v28, v28, v29\n\t"
"v_mfma_f32_16x16x32_bf16 v[44:47], a[116:119], a[180:183], v[44:47]\n\t"
"v_cvt_pk_bf16_f32 v30, v30, v31\n\t"
"v_mfma_f32_16x16x32_bf16 v[44:47], a[124:127], a[184:187], v[44:47]\n\t"
"v_accvgpr_write_b32 a88, v24\n\t"
"v_accvgpr_write_b32 a89, v26\n\t"
"v_mfma_f32_16x16x32_bf16 v[44:47], a[100:103], a[188:191], v[44:47]\n\t"
"v_cvt_pk_bf16_f32 v32, v32, v33\n\t"
"s_waitcnt lgkmcnt(0)\n\t"
"s_waitcnt vmcnt(18)\n\t"
"s_barrier\n\t"
"v_mfma_f32_16x16x32_bf16 v[48:51], a[116:119], a[184:187], v[48:51]\n\t"
"ds_read_b64_tr_b16 v[230:231], v238 offset:0\n\t"
"v_cvt_pk_bf16_f32 v34, v34, v35\n\t"
"v_mfma_f32_16x16x32_bf16 v[48:51], a[124:127], a[188:191], v[48:51]\n\t"
"ds_read_b64_tr_b16 v[232:233], v238 offset:1536\n\t"
"v_accvgpr_write_b32 a96, v28\n\t"
"v_mfma_f32_16x16x32_bf16 v[48:51], a[100:103], a[192:195], v[48:51]\n\t"
"buffer_load_dwordx4 a[36:39], v252, s[8:11], s20 offen offset:2048 sc1\n\t"
"v_mfma_f32_16x16x32_bf16 v[52:55], a[116:119], a[188:191], v[52:55]\n\t"
"ds_read_b64_tr_b16 v[234:235], v239 offset:0\n\t"
"v_accvgpr_write_b32 a97, v30\n\t"
"v_mfma_f32_16x16x32_bf16 v[52:55], a[124:127], a[192:195], v[52:55]\n\t"
"ds_read_b64_tr_b16 v[236:237], v239 offset:1536\n\t"
"v_cvt_pk_bf16_f32 v36, v36, v37\n\t"
"v_mfma_f32_16x16x32_bf16 v[52:55], a[100:103], a[196:199], v[52:55]\n\t"
"s_mov_b32 s20, 196608\n\t"
"buffer_load_dwordx4 a[160:163], v252, s[8:11], s20 offen offset:0 sc1\n\t"
"v_mfma_f32_16x16x32_bf16 v[56:59], a[116:119], a[192:195], v[56:59]\n\t"
"v_cvt_pk_bf16_f32 v38, v38, v39\n\t"
"v_accvgpr_write_b32 a42, v32\n\t"
"v_mfma_f32_16x16x32_bf16 v[56:59], a[124:127], a[196:199], v[56:59]\n\t"
"v_accvgpr_write_b32 a43, v34\n\t"
"ds_read_b64_tr_b16 v[222:223], v238 offset:19968\n\t"
"v_mfma_f32_16x16x32_bf16 v[56:59], a[100:103], a[200:203], v[56:59]\n\t"
"ds_read_b64_tr_b16 v[224:225], v238 offset:21504\n\t"
"v_cvt_pk_bf16_f32 v40, v40, v41\n\t"
"s_waitcnt lgkmcnt(4)\n\t"
"v_mfma_f32_16x16x32_bf16 v[230:233], v[230:233], a[208:211], 0\n\t"
"s_add_u32 s29, s22, 590208\n\t"
"buffer_store_dwordx4 a[108:111], v247, s[16:19], s29 offen offset:128 nt\n\t"
"v_mfma_f32_16x16x32_bf16 v[60:63], a[116:119], a[196:199], v[60:63]\n\t"
"v_cvt_pk_bf16_f32 v42, v42, v43\n\t"
"v_accvgpr_write_b32 a50, v36\n\t"
"v_mfma_f32_16x16x32_bf16 v[60:63], a[124:127], a[200:203], v[60:63]\n\t"
"v_accvgpr_write_b32 a51, v38\n\t"
"ds_read_b64_tr_b16 v[226:227], v239 offset:19968\n\t"
"v_mfma_f32_16x16x32_bf16 v[60:63], a[100:103], a[204:207], v[60:63]\n\t"
"ds_read_b64_tr_b16 v[228:229], v239 offset:21504\n\t"
"v_cvt_pk_bf16_f32 v44, v44, v45\n\t"
"s_waitcnt lgkmcnt(4)\n\t"
"v_mfma_f32_16x16x32_bf16 v[234:237], v[234:237], a[208:211], 0\n\t"
"v_cvt_pk_bf16_f32 v46, v46, v47\n\t"
"v_accvgpr_write_b32 a58, v40\n\t"
"v_mfma_f32_16x16x32_bf16 v[64:67], v[214:217], a[168:171], v[64:67]\n\t"
"v_cvt_pk_bf16_f32 v230, v230, v231\n\t"
"v_cvt_pk_bf16_f32 v231, v232, v233\n\t"
"v_mfma_f32_16x16x32_bf16 v[64:67], a[120:123], a[172:175], v[64:67]\n\t"
"v_accvgpr_write_b32 a59, v42\n\t"
"v_cvt_pk_bf16_f32 v48, v48, v49\n\t"
"v_mfma_f32_16x16x32_bf16 v[64:67], a[212:215], a[176:179], v[64:67]\n\t"
"v_cvt_pk_bf16_f32 v50, v50, v51\n\t"
"v_accvgpr_write_b32 a66, v44\n\t"
"v_mfma_f32_16x16x32_bf16 v[68:71], v[214:217], a[172:175], v[68:71]\n\t"
"v_accvgpr_write_b32 a67, v46\n\t"
"v_cvt_pk_bf16_f32 v52, v52, v53\n\t"
"v_mfma_f32_16x16x32_bf16 v[68:71], a[120:123], a[176:179], v[68:71]\n\t"
"v_cvt_pk_bf16_f32 v232, v234, v235\n\t"
"v_cvt_pk_bf16_f32 v233, v236, v237\n\t"
"v_mfma_f32_16x16x32_bf16 v[68:71], a[212:215], a[180:183], v[68:71]\n\t"
"v_cvt_pk_bf16_f32 v54, v54, v55\n\t"
"ds_read_b128 a[168:171], v242 offset:9216\n\t"
"v_mfma_f32_16x16x32_bf16 v[72:75], v[214:217], a[176:179], v[72:75]\n\t"
"ds_read_b128 a[172:175], v242 offset:9728\n\t"
"v_accvgpr_write_b32 a74, v48\n\t"
"v_mfma_f32_16x16x32_bf16 v[72:75], a[120:123], a[180:183], v[72:75]\n\t"
"ds_read_b128 a[176:179], v242 offset:10240\n\t"
"v_accvgpr_write_b32 a75, v50\n\t"
"v_mfma_f32_16x16x32_bf16 v[72:75], a[212:215], a[184:187], v[72:75]\n\t"
"v_cvt_pk_bf16_f32 v56, v56, v57\n\t"
"v_cvt_pk_bf16_f32 v58, v58, v59\n\t"
"s_waitcnt lgkmcnt(5)\n\t"
"v_mfma_f32_16x16x32_bf16 v[222:225], v[222:225], a[208:211], 0\n\t"
"v_accvgpr_write_b32 a82, v52\n\t"
"v_accvgpr_write_b32 a83, v54\n\t"
"v_mfma_f32_16x16x32_bf16 v[76:79], v[214:217], a[180:183], v[76:79]\n\t"
"ds_read_b128 a[180:183], v242 offset:10752\n\t"
"v_accvgpr_write_b32 a90, v56\n\t"
"v_mfma_f32_16x16x32_bf16 v[76:79], a[120:123], a[184:187], v[76:79]\n\t"
"v_accvgpr_write_b32 a91, v58\n\t"
"v_cvt_pk_bf16_f32 v60, v60, v61\n\t"
"v_mfma_f32_16x16x32_bf16 v[76:79], a[212:215], a[188:191], v[76:79]\n\t"
"s_add_u32 s29, s22, 590208\n\t"
"buffer_store_dwordx4 a[140:143], v247, s[16:19], s29 offen offset:256 nt\n\t"
"s_waitcnt lgkmcnt(4)\n\t"
"v_mfma_f32_16x16x32_bf16 v[226:229], v[226:229], a[208:211], 0\n\t"
"v_cvt_pk_bf16_f32 v62, v62, v63\n\t"
"ds_read_b128 a[32:35], v252 offset:30720\n\t"
"v_mfma_f32_16x16x32_bf16 v[80:83], v[214:217], a[184:187], v[80:83]\n\t"
"v_cvt_pk_bf16_f32 v222, v222, v223\n\t"
"v_cvt_pk_bf16_f32 v223, v224, v225\n\t"
"v_mfma_f32_16x16x32_bf16 v[80:83], a[120:123], a[188:191], v[80:83]\n\t"
"ds_read_b128 a[184:187], v242 offset:11264\n\t"
"v_accvgpr_write_b32 a98, v60\n\t"
"v_mfma_f32_16x16x32_bf16 v[80:83], a[212:215], a[192:195], v[80:83]\n\t"
"v_accvgpr_write_b32 a99, v62\n\t"
"v_cvt_pk_bf16_f32 v64, v64, v65\n\t"
"v_mfma_f32_16x16x32_bf16 v[84:87], v[214:217], a[188:191], v[84:87]\n\t"
"ds_read_b128 a[188:191], v242 offset:11776\n\t"
"v_cvt_pk_bf16_f32 v66, v66, v67\n\t"
"v_mfma_f32_16x16x32_bf16 v[84:87], a[120:123], a[192:195], v[84:87]\n\t"
"v_cvt_pk_bf16_f32 v224, v226, v227\n\t"
"v_cvt_pk_bf16_f32 v225, v228, v229\n\t"
"v_mfma_f32_16x16x32_bf16 v[84:87], a[212:215], a[196:199], v[84:87]\n\t"
"ds_read_b128 a[104:107], v252 offset:31744\n\t"
"ds_read_b128 a[112:115], v252 offset:32768\n\t"
"v_mfma_f32_16x16x32_bf16 v[88:91], v[214:217], a[192:195], v[88:91]\n\t"
"ds_read_b128 a[192:195], v242 offset:12288\n\t"
"v_cvt_pk_bf16_f32 v68, v68, v69\n\t"
"v_mfma_f32_16x16x32_bf16 v[88:91], a[120:123], a[196:199], v[88:91]\n\t"
"v_cvt_pk_bf16_f32 v70, v70, v71\n\t"
"v_accvgpr_write_b32 a108, v64\n\t"
"v_mfma_f32_16x16x32_bf16 v[88:91], a[212:215], a[200:203], v[88:91]\n\t"
"v_accvgpr_write_b32 a109, v66\n\t"
"v_cvt_pk_bf16_f32 v72, v72, v73\n\t"
"v_mfma_f32_16x16x32_bf16 v[92:95], v[214:217], a[196:199], v[92:95]\n\t"
"ds_read_b128 a[196:199], v242 offset:12800\n\t"
"v_cvt_pk_bf16_f32 v74, v74, v75\n\t"
"v_mfma_f32_16x16x32_bf16 v[92:95], a[120:123], a[200:203], v[92:95]\n\t"
"ds_read_b128 a[200:203], v242 offset:13312\n\t"
"v_accvgpr_write_b32 a116, v68\n\t"
"v_mfma_f32_16x16x32_bf16 v[92:95], a[212:215], a[204:207], v[92:95]\n\t"
"buffer_load_dwordx4 v[214:217], v252, s[8:11], s20 offen offset:1024 sc1\n\t"
"ds_read_b128 a[204:207], v242 offset:13824\n\t"
"v_accvgpr_write_b32 a117, v70\n\t"
"s_waitcnt vmcnt(22) lgkmcnt(10)\n\t"
"v_mfma_f32_16x16x32_bf16 v[98:101], a[216:219], a[168:171], v[98:101]\n\t"
"v_accvgpr_write_b32 a124, v72\n\t"
"v_accvgpr_write_b32 a125, v74\n\t"
"v_mfma_f32_16x16x32_bf16 v[98:101], a[220:223], a[172:175], v[98:101]\n\t"
"v_cvt_pk_bf16_f32 v76, v76, v77\n\t"
"v_cvt_pk_bf16_f32 v78, v78, v79\n\t"
"v_mfma_f32_16x16x32_bf16 v[98:101], a[128:131], a[176:179], v[98:101]\n\t"
"buffer_load_dwordx4 a[120:123], v252, s[8:11], s20 offen offset:2048 sc1\n\t"
"s_add_u32 s29, s22, 786816\n\t"
"buffer_store_dwordx4 a[52:55], v247, s[16:19], s29 offen offset:0 nt\n\t"
"s_waitcnt lgkmcnt(9)\n\t"
"v_mfma_f32_16x16x32_bf16 v[102:105], a[216:219], a[172:175], v[102:105]\n\t"
"v_accvgpr_write_b32 a132, v76\n\t"
"v_accvgpr_write_b32 a133, v78\n\t"
"v_mfma_f32_16x16x32_bf16 v[102:105], a[220:223], a[176:179], v[102:105]\n\t"
"v_cvt_pk_bf16_f32 v80, v80, v81\n\t"
"v_cvt_pk_bf16_f32 v82, v82, v83\n\t"
"v_mfma_f32_16x16x32_bf16 v[102:105], a[128:131], a[180:183], v[102:105]\n\t"
"s_mov_b32 s20, 208896\n\t"
"buffer_load_dwordx4 a[212:215], v252, s[8:11], s20 offen offset:0 sc1\n\t"
"v_cvt_pk_bf16_f32 v84, v84, v85\n\t"
"v_cvt_pk_bf16_f32 v86, v86, v87\n\t"
"s_waitcnt lgkmcnt(0)\n\t"
"s_barrier\n\t"
"v_mfma_f32_16x16x32_bf16 v[106:109], a[216:219], a[176:179], v[106:109]\n\t"
"v_accvgpr_write_b32 a140, v80\n\t"
"v_accvgpr_write_b32 a141, v82\n\t"
"v_mfma_f32_16x16x32_bf16 v[106:109], a[220:223], a[180:183], v[106:109]\n\t"
"v_cvt_pk_bf16_f32 v88, v88, v89\n\t"
"v_cvt_pk_bf16_f32 v90, v90, v91\n\t"
"v_mfma_f32_16x16x32_bf16 v[106:109], a[128:131], a[184:187], v[106:109]\n\t"
"s_mov_b64 exec, s[44:45]\n\t"
"ds_write_b128 v240, v[230:233] offset:0\n\t"
"s_mov_b64 exec, -1\n\t"
"s_mov_b64 exec, s[44:45]\n\t"
"ds_write_b128 v240, v[222:225] offset:1536\n\t"
"s_mov_b64 exec, -1\n\t"
"v_mfma_f32_16x16x32_bf16 v[110:113], a[216:219], a[180:183], v[110:113]\n\t"
"ds_read_b64_tr_b16 v[230:231], v238 offset:39936\n\t"
"v_accvgpr_write_b32 a148, v84\n\t"
"v_mfma_f32_16x16x32_bf16 v[110:113], a[220:223], a[184:187], v[110:113]\n\t"
"ds_read_b64_tr_b16 v[232:233], v238 offset:41472\n\t"
"v_accvgpr_write_b32 a149, v86\n\t"
"v_mfma_f32_16x16x32_bf16 v[110:113], a[128:131], a[188:191], v[110:113]\n\t"
"ds_read_b64_tr_b16 v[234:235], v239 offset:39936\n\t"
"ds_read_b64_tr_b16 v[236:237], v239 offset:41472\n\t"
"v_add_u32 v238, 59904, v238\n\t"
"v_add_u32 v239, 59904, v239\n\t"
"v_mfma_f32_16x16x32_bf16 v[114:117], a[216:219], a[184:187], v[114:117]\n\t"
"ds_read_b64_tr_b16 v[222:223], v238 offset:0\n\t"
"v_cvt_pk_bf16_f32 v92, v92, v93\n\t"
"v_mfma_f32_16x16x32_bf16 v[114:117], a[220:223], a[188:191], v[114:117]\n\t"
"ds_read_b64_tr_b16 v[224:225], v238 offset:1536\n\t"
"v_cvt_pk_bf16_f32 v94, v94, v95\n\t"
"v_mfma_f32_16x16x32_bf16 v[114:117], a[128:131], a[192:195], v[114:117]\n\t"
"ds_read_b64_tr_b16 v[226:227], v239 offset:0\n\t"
"ds_read_b64_tr_b16 v[228:229], v239 offset:1536\n\t"
"ds_write_b128 v248, a[40:43] offset:18432\n\t"
"v_mfma_f32_16x16x32_bf16 v[118:121], a[216:219], a[188:191], v[118:121]\n\t"
"v_accvgpr_write_b32 a156, v88\n\t"
"v_accvgpr_write_b32 a157, v90\n\t"
"v_mfma_f32_16x16x32_bf16 v[118:121], a[220:223], a[192:195], v[118:121]\n\t"
"v_cvt_pk_bf16_f32 v98, v98, v99\n\t"
"v_cvt_pk_bf16_f32 v100, v100, v101\n\t"
"v_mfma_f32_16x16x32_bf16 v[118:121], a[128:131], a[196:199], v[118:121]\n\t"
"s_add_u32 s29, s22, 786816\n\t"
"buffer_store_dwordx4 a[92:95], v247, s[16:19], s29 offen offset:128 nt\n\t"
"v_accvgpr_write_b32 a164, v92\n\t"
"v_accvgpr_write_b32 a165, v94\n\t"
"s_waitcnt lgkmcnt(7)\n\t"
"v_mfma_f32_16x16x32_bf16 v[230:233], v[230:233], a[208:211], 0\n\t"
"ds_write_b128 v248, a[56:59] offset:30720\n\t"
"v_cvt_pk_bf16_f32 v102, v102, v103\n\t"
"v_cvt_pk_bf16_f32 v104, v104, v105\n\t"
"s_waitcnt lgkmcnt(6)\n\t"
"v_mfma_f32_16x16x32_bf16 v[234:237], v[234:237], a[208:211], 0\n\t"
"v_accvgpr_write_b32 a44, v98\n\t"
"v_accvgpr_write_b32 a45, v100\n\t"
"v_cvt_pk_bf16_f32 v106, v106, v107\n\t"
"v_cvt_pk_bf16_f32 v108, v108, v109\n\t"
"v_mfma_f32_16x16x32_bf16 v[122:125], a[216:219], a[192:195], v[122:125]\n\t"
"v_accvgpr_write_b32 a52, v102\n\t"
"v_accvgpr_write_b32 a53, v104\n\t"
"v_mfma_f32_16x16x32_bf16 v[122:125], a[220:223], a[196:199], v[122:125]\n\t"
"v_cvt_pk_bf16_f32 v110, v110, v111\n\t"
"v_cvt_pk_bf16_f32 v112, v112, v113\n\t"
"v_mfma_f32_16x16x32_bf16 v[122:125], a[128:131], a[200:203], v[122:125]\n\t"
"v_accvgpr_write_b32 a60, v106\n\t"
"v_accvgpr_write_b32 a61, v108\n\t"
"v_mfma_f32_16x16x32_bf16 v[126:129], a[216:219], a[196:199], v[126:129]\n\t"
"v_cvt_pk_bf16_f32 v230, v230, v231\n\t"
"v_cvt_pk_bf16_f32 v231, v232, v233\n\t"
"v_mfma_f32_16x16x32_bf16 v[126:129], a[220:223], a[200:203], v[126:129]\n\t"
"v_cvt_pk_bf16_f32 v232, v234, v235\n\t"
"v_cvt_pk_bf16_f32 v233, v236, v237\n\t"
"v_mfma_f32_16x16x32_bf16 v[126:129], a[128:131], a[204:207], v[126:129]\n\t"
"buffer_load_dwordx4 a[216:219], v252, s[8:11], s20 offen offset:1024 sc1\n\t"
"s_mov_b64 exec, s[44:45]\n\t"
"ds_write_b128 v240, v[230:233] offset:3072\n\t"
"s_mov_b64 exec, -1\n\t"
"s_waitcnt lgkmcnt(5)\n\t"
"v_mfma_f32_16x16x32_bf16 v[222:225], v[222:225], a[208:211], 0\n\t"
"buffer_load_dwordx4 a[128:131], v252, s[8:11], s20 offen offset:2048 sc1\n\t"
"ds_read_b64_tr_b16 v[230:231], v238 offset:19968\n\t"
"ds_read_b64_tr_b16 v[232:233], v238 offset:21504\n\t"
"s_waitcnt lgkmcnt(5)\n\t"
"v_mfma_f32_16x16x32_bf16 v[226:229], v[226:229], a[208:211], 0\n\t"
"ds_read_b64_tr_b16 v[234:235], v239 offset:19968\n\t"
"ds_read_b64_tr_b16 v[236:237], v239 offset:21504\n\t"
"ds_write_b64 v249, a[108:109] offset:18432\n\t"
"s_waitcnt vmcnt(25)\n\t"
"v_mfma_f32_16x16x32_bf16 v[130:133], a[136:139], a[168:171], v[130:133]\n\t"
"v_cvt_pk_bf16_f32 v114, v114, v115\n\t"
"v_cvt_pk_bf16_f32 v116, v116, v117\n\t"
"v_mfma_f32_16x16x32_bf16 v[130:133], a[4:7], a[172:175], v[130:133]\n\t"
"v_accvgpr_write_b32 a68, v110\n\t"
"v_accvgpr_write_b32 a69, v112\n\t"
"v_mfma_f32_16x16x32_bf16 v[130:133], a[12:15], a[176:179], v[130:133]\n\t"
"s_mov_b32 s20, 221184\n\t"
"buffer_load_dwordx4 a[220:223], v252, s[8:11], s20 offen offset:0 sc1\n\t"
"ds_write_b64 v249, a[124:125] offset:30720\n\t"
"v_mfma_f32_16x16x32_bf16 v[134:137], a[136:139], a[172:175], v[134:137]\n\t"
"v_cvt_pk_bf16_f32 v222, v222, v223\n\t"
"v_cvt_pk_bf16_f32 v223, v224, v225\n\t"
"v_mfma_f32_16x16x32_bf16 v[134:137], a[4:7], a[176:179], v[134:137]\n\t"
"v_cvt_pk_bf16_f32 v224, v226, v227\n\t"
"v_cvt_pk_bf16_f32 v225, v228, v229\n\t"
"v_mfma_f32_16x16x32_bf16 v[134:137], a[12:15], a[180:183], v[134:137]\n\t"
"v_cvt_pk_bf16_f32 v118, v118, v119\n\t"
"v_cvt_pk_bf16_f32 v120, v120, v121\n\t"
"s_add_u32 s29, s22, 786816\n\t"
"buffer_store_dwordx4 v[194:197], v247, s[16:19], s29 offen offset:256 nt\n\t"
"v_mfma_f32_16x16x32_bf16 v[138:141], a[136:139], a[176:179], v[138:141]\n\t"
"v_accvgpr_write_b32 a76, v114\n\t"
"v_accvgpr_write_b32 a77, v116\n\t"
"v_mfma_f32_16x16x32_bf16 v[138:141], a[4:7], a[180:183], v[138:141]\n\t"
"v_accvgpr_write_b32 a84, v118\n\t"
"v_accvgpr_write_b32 a85, v120\n\t"
"v_mfma_f32_16x16x32_bf16 v[138:141], a[12:15], a[184:187], v[138:141]\n\t"
"s_mov_b64 exec, s[44:45]\n\t"
"ds_write_b128 v240, v[222:225] offset:4608\n\t"
"s_mov_b64 exec, -1\n\t"
"ds_read_b64_tr_b16 v[222:223], v238 offset:39936\n\t"
"ds_read_b64_tr_b16 v[224:225], v238 offset:41472\n\t"
"v_mfma_f32_16x16x32_bf16 v[142:145], a[136:139], a[180:183], v[142:145]\n\t"
"ds_read_b64_tr_b16 v[226:227], v239 offset:39936\n\t"
"v_cvt_pk_bf16_f32 v122, v122, v123\n\t"
"v_mfma_f32_16x16x32_bf16 v[142:145], a[4:7], a[184:187], v[142:145]\n\t"
"ds_read_b64_tr_b16 v[228:229], v239 offset:41472\n\t"
"v_cvt_pk_bf16_f32 v124, v124, v125\n\t"
"v_mfma_f32_16x16x32_bf16 v[142:145], a[12:15], a[188:191], v[142:145]\n\t"
"v_cvt_pk_bf16_f32 v126, v126, v127\n\t"
"v_cvt_pk_bf16_f32 v128, v128, v129\n\t"
"v_accvgpr_write_b32 a92, v122\n\t"
"v_accvgpr_write_b32 a93, v124\n\t"
"s_waitcnt lgkmcnt(9)\n\t"
"v_mfma_f32_16x16x32_bf16 v[230:233], v[230:233], a[208:211], 0\n\t"
"ds_write_b128 v248, a[48:51] offset:24576\n\t"
"ds_write_b128 v248, a[64:67] offset:36864\n\t"
"s_waitcnt lgkmcnt(9)\n\t"
"v_mfma_f32_16x16x32_bf16 v[234:237], v[234:237], a[208:211], 0\n\t"
"v_accvgpr_write_b32 a100, v126\n\t"
"v_accvgpr_write_b32 a101, v128\n\t"
"v_mfma_f32_16x16x32_bf16 v[146:149], a[136:139], a[184:187], v[146:149]\n\t"
"v_cvt_pk_bf16_f32 v130, v130, v131\n\t"
"v_cvt_pk_bf16_f32 v132, v132, v133\n\t"
"v_mfma_f32_16x16x32_bf16 v[146:149], a[4:7], a[188:191], v[146:149]\n\t"
"v_cvt_pk_bf16_f32 v134, v134, v135\n\t"
"v_cvt_pk_bf16_f32 v136, v136, v137\n\t"
"v_mfma_f32_16x16x32_bf16 v[146:149], a[12:15], a[192:195], v[146:149]\n\t"
"v_accvgpr_write_b32 a46, v130\n\t"
"v_accvgpr_write_b32 a47, v132\n\t"
"v_mfma_f32_16x16x32_bf16 v[150:153], a[136:139], a[188:191], v[150:153]\n\t"
"v_cvt_pk_bf16_f32 v230, v230, v231\n\t"
"v_cvt_pk_bf16_f32 v231, v232, v233\n\t"
"v_mfma_f32_16x16x32_bf16 v[150:153], a[4:7], a[192:195], v[150:153]\n\t"
"v_cvt_pk_bf16_f32 v232, v234, v235\n\t"
"v_cvt_pk_bf16_f32 v233, v236, v237\n\t"
"v_mfma_f32_16x16x32_bf16 v[150:153], a[12:15], a[196:199], v[150:153]\n\t"
"s_add_u32 s29, s22, 983424\n\t"
"buffer_store_dwordx4 v[202:205], v247, s[16:19], s29 offen offset:0 nt\n\t"
"s_waitcnt lgkmcnt(4)\n\t"
"v_mfma_f32_16x16x32_bf16 v[222:225], v[222:225], a[208:211], 0\n\t"
"s_mov_b64 exec, s[44:45]\n\t"
"ds_write_b128 v240, v[230:233] offset:6144\n\t"
"s_mov_b64 exec, -1\n\t"
"s_waitcnt lgkmcnt(3)\n\t"
"v_mfma_f32_16x16x32_bf16 v[226:229], v[226:229], a[208:211], 0\n\t"
"v_cvt_pk_bf16_f32 v138, v138, v139\n\t"
"v_cvt_pk_bf16_f32 v140, v140, v141\n\t"
"v_mfma_f32_16x16x32_bf16 v[154:157], a[136:139], a[192:195], v[154:157]\n\t"
"v_accvgpr_write_b32 a54, v134\n\t"
"v_accvgpr_write_b32 a55, v136\n\t"
"v_mfma_f32_16x16x32_bf16 v[154:157], a[4:7], a[196:199], v[154:157]\n\t"
"v_cvt_pk_bf16_f32 v142, v142, v143\n\t"
"v_cvt_pk_bf16_f32 v144, v144, v145\n\t"
"v_mfma_f32_16x16x32_bf16 v[154:157], a[12:15], a[200:203], v[154:157]\n\t"
"v_accvgpr_write_b32 a62, v138\n\t"
"v_accvgpr_write_b32 a63, v140\n\t"
"v_mfma_f32_16x16x32_bf16 v[158:161], a[136:139], a[196:199], v[158:161]\n\t"
"v_cvt_pk_bf16_f32 v222, v222, v223\n\t"
"v_cvt_pk_bf16_f32 v223, v224, v225\n\t"
"v_mfma_f32_16x16x32_bf16 v[158:161], a[4:7], a[200:203], v[158:161]\n\t"
"v_cvt_pk_bf16_f32 v224, v226, v227\n\t"
"v_cvt_pk_bf16_f32 v225, v228, v229\n\t"
"v_mfma_f32_16x16x32_bf16 v[158:161], a[12:15], a[204:207], v[158:161]\n\t"
"buffer_load_dwordx4 a[136:139], v252, s[8:11], s20 offen offset:1024 sc1\n\t"
"ds_write_b64 v249, a[116:117] offset:24576\n\t"
"s_waitcnt vmcnt(26)\n\t"
"v_mfma_f32_16x16x32_bf16 v[162:165], a[224:227], a[168:171], v[162:165]\n\t"
"v_accvgpr_write_b32 a70, v142\n\t"
"v_accvgpr_write_b32 a71, v144\n\t"
"v_mfma_f32_16x16x32_bf16 v[162:165], a[228:231], a[172:175], v[162:165]\n\t"
"v_cvt_pk_bf16_f32 v146, v146, v147\n\t"
"v_cvt_pk_bf16_f32 v148, v148, v149\n\t"
"v_mfma_f32_16x16x32_bf16 v[162:165], a[232:235], a[176:179], v[162:165]\n\t"
"buffer_load_dwordx4 a[4:7], v252, s[8:11], s20 offen offset:2048 sc1\n\t"
"ds_write_b64 v249, a[132:133] offset:36864\n\t"
"v_mfma_f32_16x16x32_bf16 v[166:169], a[224:227], a[172:175], v[166:169]\n\t"
"v_cvt_pk_bf16_f32 v150, v150, v151\n\t"
"v_cvt_pk_bf16_f32 v152, v152, v153\n\t"
"v_mfma_f32_16x16x32_bf16 v[166:169], a[228:231], a[176:179], v[166:169]\n\t"
"v_accvgpr_write_b32 a78, v146\n\t"
"v_accvgpr_write_b32 a79, v148\n\t"
"v_mfma_f32_16x16x32_bf16 v[166:169], a[232:235], a[180:183], v[166:169]\n\t"
"s_mov_b64 exec, s[44:45]\n\t"
"ds_write_b128 v240, v[222:225] offset:7680\n\t"
"s_mov_b64 exec, -1\n\t"
"v_mfma_f32_16x16x32_bf16 v[170:173], a[224:227], a[176:179], v[170:173]\n\t"
"v_accvgpr_write_b32 a86, v150\n\t"
"v_accvgpr_write_b32 a87, v152\n\t"
"v_mfma_f32_16x16x32_bf16 v[170:173], a[228:231], a[180:183], v[170:173]\n\t"
"v_cvt_pk_bf16_f32 v154, v154, v155\n\t"
"v_cvt_pk_bf16_f32 v156, v156, v157\n\t"
"v_mfma_f32_16x16x32_bf16 v[170:173], a[232:235], a[184:187], v[170:173]\n\t"
"s_mov_b32 s20, 233472\n\t"
"buffer_load_dwordx4 a[12:15], v252, s[8:11], s20 offen offset:0 sc1\n\t"
"v_mfma_f32_16x16x32_bf16 v[174:177], a[224:227], a[180:183], v[174:177]\n\t"
"v_cvt_pk_bf16_f32 v158, v158, v159\n\t"
"v_cvt_pk_bf16_f32 v160, v160, v161\n\t"
"v_mfma_f32_16x16x32_bf16 v[174:177], a[228:231], a[184:187], v[174:177]\n\t"
"v_accvgpr_write_b32 a94, v154\n\t"
"v_accvgpr_write_b32 a95, v156\n\t"
"v_mfma_f32_16x16x32_bf16 v[174:177], a[232:235], a[188:191], v[174:177]\n\t"
"s_add_u32 s29, s22, 983424\n\t"
"buffer_store_dwordx4 v[210:213], v247, s[16:19], s29 offen offset:128 nt\n\t"
"s_waitcnt lgkmcnt(0)\n\t"
"s_barrier\n\t"
"v_mfma_f32_16x16x32_bf16 v[178:181], a[224:227], a[184:187], v[178:181]\n\t"
"ds_read_b128 a[168:171], v242 offset:0\n\t"
"v_cvt_pk_bf16_f32 v162, v162, v163\n\t"
"v_mfma_f32_16x16x32_bf16 v[178:181], a[228:231], a[188:191], v[178:181]\n\t"
"ds_read_b128 a[172:175], v242 offset:512\n\t"
"v_cvt_pk_bf16_f32 v164, v164, v165\n\t"
"v_mfma_f32_16x16x32_bf16 v[178:181], a[232:235], a[192:195], v[178:181]\n\t"
"ds_read_b128 a[176:179], v242 offset:1024\n\t"
"ds_read_b128 a[180:183], v242 offset:1536\n\t"
"v_mfma_f32_16x16x32_bf16 v[182:185], a[224:227], a[188:191], v[182:185]\n\t"
"ds_read_b128 a[184:187], v242 offset:2048\n\t"
"v_accvgpr_write_b32 a102, v158\n\t"
"v_mfma_f32_16x16x32_bf16 v[182:185], a[228:231], a[192:195], v[182:185]\n\t"
"ds_read_b128 a[188:191], v242 offset:2560\n\t"
"v_accvgpr_write_b32 a103, v160\n\t"
"v_mfma_f32_16x16x32_bf16 v[182:185], a[232:235], a[196:199], v[182:185]\n\t"
"buffer_load_dwordx4 a[64:67], v252, s[8:11], s20 offen offset:1024 sc1\n\t"
"v_mfma_f32_16x16x32_bf16 v[186:189], a[224:227], a[192:195], v[186:189]\n\t"
"ds_read_b128 a[192:195], v242 offset:3072\n\t"
"v_cvt_pk_bf16_f32 v166, v166, v167\n\t"
"v_mfma_f32_16x16x32_bf16 v[186:189], a[228:231], a[196:199], v[186:189]\n\t"
"v_cvt_pk_bf16_f32 v168, v168, v169\n\t"
"v_accvgpr_write_b32 a110, v162\n\t"
"v_mfma_f32_16x16x32_bf16 v[186:189], a[232:235], a[200:203], v[186:189]\n\t"
"v_accvgpr_write_b32 a111, v164\n\t"
"v_cvt_pk_bf16_f32 v170, v170, v171\n\t"
"v_mfma_f32_16x16x32_bf16 v[190:193], a[224:227], a[196:199], v[190:193]\n\t"
"ds_read_b128 a[196:199], v242 offset:3584\n\t"
"v_cvt_pk_bf16_f32 v172, v172, v173\n\t"
"v_mfma_f32_16x16x32_bf16 v[190:193], a[228:231], a[200:203], v[190:193]\n\t"
"ds_read_b128 a[200:203], v242 offset:4096\n\t"
"v_accvgpr_write_b32 a118, v166\n\t"
"v_mfma_f32_16x16x32_bf16 v[190:193], a[232:235], a[204:207], v[190:193]\n\t"
"buffer_load_dwordx4 a[224:227], v252, s[8:11], s20 offen offset:2048 sc1\n\t"
"s_waitcnt vmcnt(28) lgkmcnt(6)\n\t"
"v_mfma_f32_16x16x32_bf16 v[2:5], a[236:239], a[168:171], 0\n\t"
"v_accvgpr_write_b32 a119, v168\n\t"
"ds_read_b128 a[204:207], v242 offset:4608\n\t"
"v_mfma_f32_16x16x32_bf16 v[2:5], a[240:243], a[172:175], v[2:5]\n\t"
"v_cvt_pk_bf16_f32 v174, v174, v175\n\t"
"v_cvt_pk_bf16_f32 v176, v176, v177\n\t"
"v_mfma_f32_16x16x32_bf16 v[2:5], a[244:247], a[176:179], v[2:5]\n\t"
"s_add_u32 s29, s22, 983424\n\t"
"buffer_store_dwordx4 a[0:3], v247, s[16:19], s29 offen offset:256 nt\n\t"
"s_mov_b32 s20, 245760\n\t"
"buffer_load_dwordx4 a[228:231], v252, s[8:11], s20 offen offset:0 sc1\n\t"
"s_waitcnt lgkmcnt(6)\n\t"
"v_mfma_f32_16x16x32_bf16 v[6:9], a[236:239], a[172:175], 0\n\t"
"v_accvgpr_write_b32 a126, v170\n\t"
"v_accvgpr_write_b32 a127, v172\n\t"
"v_mfma_f32_16x16x32_bf16 v[6:9], a[240:243], a[176:179], v[6:9]\n\t"
"v_cvt_pk_bf16_f32 v178, v178, v179\n\t"
"v_cvt_pk_bf16_f32 v180, v180, v181\n\t"
"v_mfma_f32_16x16x32_bf16 v[6:9], a[244:247], a[180:183], v[6:9]\n\t"
"buffer_load_dwordx4 a[232:235], v252, s[8:11], s20 offen offset:1024 sc1\n\t"
"v_accvgpr_write_b32 a134, v174\n\t"
"v_accvgpr_write_b32 a135, v176\n\t"
"s_waitcnt lgkmcnt(5)\n\t"
"v_mfma_f32_16x16x32_bf16 v[10:13], a[236:239], a[176:179], 0\n\t"
"v_cvt_pk_bf16_f32 v182, v182, v183\n\t"
"v_cvt_pk_bf16_f32 v184, v184, v185\n\t"
"v_mfma_f32_16x16x32_bf16 v[10:13], a[240:243], a[180:183], v[10:13]\n\t"
"v_accvgpr_write_b32 a142, v178\n\t"
"v_accvgpr_write_b32 a143, v180\n\t"
"v_mfma_f32_16x16x32_bf16 v[10:13], a[244:247], a[184:187], v[10:13]\n\t"
"v_cvt_pk_bf16_f32 v186, v186, v187\n\t"
"v_cvt_pk_bf16_f32 v188, v188, v189\n\t"
"s_waitcnt lgkmcnt(4)\n\t"
"v_mfma_f32_16x16x32_bf16 v[14:17], a[236:239], a[180:183], 0\n\t"
"v_accvgpr_write_b32 a150, v182\n\t"
"v_accvgpr_write_b32 a151, v184\n\t"
"v_mfma_f32_16x16x32_bf16 v[14:17], a[240:243], a[184:187], v[14:17]\n\t"
"v_cvt_pk_bf16_f32 v190, v190, v191\n\t"
"v_cvt_pk_bf16_f32 v192, v192, v193\n\t"
"v_mfma_f32_16x16x32_bf16 v[14:17], a[244:247], a[188:191], v[14:17]\n\t"
"v_accvgpr_write_b32 a158, v186\n\t"
"s_waitcnt lgkmcnt(3)\n\t"
"v_mfma_f32_16x16x32_bf16 v[18:21], a[236:239], a[184:187], 0\n\t"
"v_accvgpr_write_b32 a159, v188\n\t"
"v_mfma_f32_16x16x32_bf16 v[18:21], a[240:243], a[188:191], v[18:21]\n\t"
"v_accvgpr_write_b32 a166, v190\n\t"
"v_mfma_f32_16x16x32_bf16 v[18:21], a[244:247], a[192:195], v[18:21]\n\t"
"v_accvgpr_write_b32 a167, v192\n\t"
"s_waitcnt lgkmcnt(2)\n\t"
"v_mfma_f32_16x16x32_bf16 v[22:25], a[236:239], a[188:191], 0\n\t"
"ds_read_b128 v[194:197], v252 offset:18432\n\t"
"v_mfma_f32_16x16x32_bf16 v[22:25], a[240:243], a[192:195], v[22:25]\n\t"
"ds_read_b128 a[0:3], v252 offset:30720\n\t"
"v_mfma_f32_16x16x32_bf16 v[22:25], a[244:247], a[196:199], v[22:25]\n\t"
"ds_read_b128 v[202:205], v252 offset:19456\n\t"
"s_waitcnt lgkmcnt(4)\n\t"
"v_mfma_f32_16x16x32_bf16 v[26:29], a[236:239], a[192:195], 0\n\t"
"ds_read_b128 a[40:43], v252 offset:31744\n\t"
"v_mfma_f32_16x16x32_bf16 v[26:29], a[240:243], a[196:199], v[26:29]\n\t"
"ds_read_b128 v[210:213], v252 offset:20480\n\t"
"v_mfma_f32_16x16x32_bf16 v[26:29], a[244:247], a[200:203], v[26:29]\n\t"
"s_add_u32 s29, s22, 1180032\n\t"
"buffer_store_dwordx4 a[8:11], v247, s[16:19], s29 offen offset:0 nt\n\t"
"s_waitcnt lgkmcnt(5)\n\t"
"v_mfma_f32_16x16x32_bf16 v[30:33], a[236:239], a[196:199], 0\n\t"
"v_mfma_f32_16x16x32_bf16 v[30:33], a[240:243], a[200:203], v[30:33]\n\t"
"ds_read_b128 a[48:51], v252 offset:32768\n\t"
"v_mfma_f32_16x16x32_bf16 v[30:33], a[244:247], a[204:207], v[30:33]\n\t"
"s_waitcnt vmcnt(29)\n\t"
"v_mfma_f32_16x16x32_bf16 v[34:37], a[144:147], a[168:171], 0\n\t"
"v_mfma_f32_16x16x32_bf16 v[34:37], a[20:23], a[172:175], v[34:37]\n\t"
"v_mfma_f32_16x16x32_bf16 v[34:37], a[248:251], a[176:179], v[34:37]\n\t"
"buffer_load_dwordx4 a[236:239], v252, s[8:11], s20 offen offset:2048 sc1\n\t"
"v_mfma_f32_16x16x32_bf16 v[38:41], a[144:147], a[172:175], 0\n\t"
"ds_read_b64_tr_b16 v[230:231], v238 offset:512\n\t"
"v_mfma_f32_16x16x32_bf16 v[38:41], a[20:23], a[176:179], v[38:41]\n\t"
"ds_read_b64_tr_b16 v[232:233], v238 offset:2048\n\t"
"v_mfma_f32_16x16x32_bf16 v[38:41], a[248:251], a[180:183], v[38:41]\n\t"
"s_mov_b32 s20, 405504\n\t"
"buffer_load_dwordx4 a[240:243], v252, s[8:11], s20 offen offset:0 sc1\n\t"
"v_mfma_f32_16x16x32_bf16 v[42:45], a[144:147], a[176:179], 0\n\t"
"ds_read_b64_tr_b16 v[234:235], v239 offset:512\n\t"
"v_mfma_f32_16x16x32_bf16 v[42:45], a[20:23], a[180:183], v[42:45]\n\t"
"ds_read_b64_tr_b16 v[236:237], v239 offset:2048\n\t"
"v_mfma_f32_16x16x32_bf16 v[42:45], a[248:251], a[184:187], v[42:45]\n\t"
"buffer_load_dwordx4 a[244:247], v252, s[8:11], s20 offen offset:1024 sc1\n\t"
"v_mfma_f32_16x16x32_bf16 v[46:49], a[144:147], a[180:183], 0\n\t"
"v_mfma_f32_16x16x32_bf16 v[46:49], a[20:23], a[184:187], v[46:49]\n\t"
"v_mfma_f32_16x16x32_bf16 v[46:49], a[248:251], a[188:191], v[46:49]\n\t"
"s_add_u32 s29, s22, 1180032\n\t"
"buffer_store_dwordx4 a[16:19], v247, s[16:19], s29 offen offset:128 nt\n\t"
"s_waitcnt lgkmcnt(4)\n\t"
"s_barrier\n\t"
"v_mfma_f32_16x16x32_bf16 v[50:53], a[144:147], a[184:187], 0\n\t"
"v_mfma_f32_16x16x32_bf16 v[50:53], a[20:23], a[188:191], v[50:53]\n\t"
"v_mfma_f32_16x16x32_bf16 v[50:53], a[248:251], a[192:195], v[50:53]\n\t"
"ds_write_b128 v248, a[72:75] offset:18432\n\t"
"v_mfma_f32_16x16x32_bf16 v[54:57], a[144:147], a[188:191], 0\n\t"
"v_mfma_f32_16x16x32_bf16 v[54:57], a[20:23], a[192:195], v[54:57]\n\t"
"ds_read_b64_tr_b16 v[222:223], v238 offset:20480\n\t"
"v_mfma_f32_16x16x32_bf16 v[54:57], a[248:251], a[196:199], v[54:57]\n\t"
"ds_write_b128 v248, a[88:91] offset:30720\n\t"
"s_waitcnt lgkmcnt(5)\n\t"
"v_mfma_f32_16x16x32_bf16 v[230:233], v[230:233], a[208:211], 0\n\t"
"ds_read_b64_tr_b16 v[224:225], v238 offset:22016\n\t"
"v_mfma_f32_16x16x32_bf16 v[58:61], a[144:147], a[192:195], 0\n\t"
"v_mfma_f32_16x16x32_bf16 v[58:61], a[20:23], a[196:199], v[58:61]\n\t"
"ds_read_b64_tr_b16 v[226:227], v239 offset:20480\n\t"
"v_mfma_f32_16x16x32_bf16 v[58:61], a[248:251], a[200:203], v[58:61]\n\t"
"s_add_u32 s29, s22, 1180032\n\t"
"buffer_store_dwordx4 a[24:27], v247, s[16:19], s29 offen offset:256 nt\n\t"
"s_waitcnt lgkmcnt(5)\n\t"
"v_mfma_f32_16x16x32_bf16 v[234:237], v[234:237], a[208:211], 0\n\t"
"ds_read_b64_tr_b16 v[228:229], v239 offset:22016\n\t"
"v_mfma_f32_16x16x32_bf16 v[62:65], a[144:147], a[196:199], 0\n\t"
"v_cvt_pk_bf16_f32 v230, v230, v231\n\t"
"v_cvt_pk_bf16_f32 v231, v232, v233\n\t"
"v_mfma_f32_16x16x32_bf16 v[62:65], a[20:23], a[200:203], v[62:65]\n\t"
"v_mfma_f32_16x16x32_bf16 v[62:65], a[248:251], a[204:207], v[62:65]\n\t"
"buffer_load_dwordx4 a[20:23], v252, s[8:11], s20 offen offset:2048 sc1\n\t"
"s_waitcnt vmcnt(32)\n\t"
"v_mfma_f32_16x16x32_bf16 v[66:69], a[252:255], a[168:171], 0\n\t"
"ds_read_b128 a[168:171], v242 offset:9216\n\t"
"v_mfma_f32_16x16x32_bf16 v[66:69], v[198:201], a[172:175], v[66:69]\n\t"
"v_cvt_pk_bf16_f32 v232, v234, v235\n\t"
"v_cvt_pk_bf16_f32 v233, v236, v237\n\t"
"v_mfma_f32_16x16x32_bf16 v[66:69], a[152:155], a[176:179], v[66:69]\n\t"
"s_mov_b32 s20, 417792\n\t"
"buffer_load_dwordx4 a[248:251], v252, s[8:11], s20 offen offset:0 sc1\n\t"
"ds_write_b64 v249, a[140:141] offset:18432\n\t"
"v_mfma_f32_16x16x32_bf16 v[70:73], a[252:255], a[172:175], 0\n\t"
"ds_read_b128 a[172:175], v242 offset:9728\n\t"
"v_mfma_f32_16x16x32_bf16 v[70:73], v[198:201], a[176:179], v[70:73]\n\t"
"v_mfma_f32_16x16x32_bf16 v[70:73], a[152:155], a[180:183], v[70:73]\n\t"
"ds_write_b64 v249, a[156:157] offset:30720\n\t"
"s_waitcnt lgkmcnt(6)\n\t"
"v_mfma_f32_16x16x32_bf16 v[222:225], v[222:225], a[208:211], 0\n\t"
"ds_write_b128 v248, a[80:83] offset:24576\n\t"
"v_mfma_f32_16x16x32_bf16 v[74:77], a[252:255], a[176:179], 0\n\t"
"ds_read_b128 a[176:179], v242 offset:10240\n\t"
"v_mfma_f32_16x16x32_bf16 v[74:77], v[198:201], a[180:183], v[74:77]\n\t"
"v_mfma_f32_16x16x32_bf16 v[74:77], a[152:155], a[184:187], v[74:77]\n\t"
"ds_write_b128 v248, a[96:99] offset:36864\n\t"
"s_waitcnt lgkmcnt(7)\n\t"
"v_mfma_f32_16x16x32_bf16 v[226:229], v[226:229], a[208:211], 0\n\t"
"s_add_u32 s29, s22, 1376640\n\t"
"buffer_store_dwordx4 a[32:35], v247, s[16:19], s29 offen offset:0 nt\n\t"
"v_mfma_f32_16x16x32_bf16 v[78:81], a[252:255], a[180:183], 0\n\t"
"v_cvt_pk_bf16_f32 v222, v222, v223\n\t"
"v_cvt_pk_bf16_f32 v223, v224, v225\n\t"
"v_mfma_f32_16x16x32_bf16 v[78:81], v[198:201], a[184:187], v[78:81]\n\t"
"ds_read_b128 a[180:183], v242 offset:10752\n\t"
"v_mfma_f32_16x16x32_bf16 v[78:81], a[152:155], a[188:191], v[78:81]\n\t"
"ds_write_b64 v249, a[148:149] offset:24576\n\t"
"v_mfma_f32_16x16x32_bf16 v[82:85], a[252:255], a[184:187], 0\n\t"
"ds_read_b128 a[184:187], v242 offset:11264\n\t"
"v_mfma_f32_16x16x32_bf16 v[82:85], v[198:201], a[188:191], v[82:85]\n\t"
"v_cvt_pk_bf16_f32 v224, v226, v227\n\t"
"v_cvt_pk_bf16_f32 v225, v228, v229\n\t"
"v_mfma_f32_16x16x32_bf16 v[82:85], a[152:155], a[192:195], v[82:85]\n\t"
"v_mfma_f32_16x16x32_bf16 v[86:89], a[252:255], a[188:191], 0\n\t"
"ds_read_b128 a[188:191], v242 offset:11776\n\t"
"v_mfma_f32_16x16x32_bf16 v[86:89], v[198:201], a[192:195], v[86:89]\n\t"
"v_mfma_f32_16x16x32_bf16 v[86:89], a[152:155], a[196:199], v[86:89]\n\t"
"ds_write_b64 v249, a[164:165] offset:36864\n\t"
"v_mfma_f32_16x16x32_bf16 v[90:93], a[252:255], a[192:195], 0\n\t"
"ds_read_b128 a[192:195], v242 offset:12288\n\t"
"v_mfma_f32_16x16x32_bf16 v[90:93], v[198:201], a[196:199], v[90:93]\n\t"
"v_mfma_f32_16x16x32_bf16 v[90:93], a[152:155], a[200:203], v[90:93]\n\t"
"v_mfma_f32_16x16x32_bf16 v[94:97], a[252:255], a[196:199], 0\n\t"
"ds_read_b128 a[196:199], v242 offset:12800\n\t"
"v_mfma_f32_16x16x32_bf16 v[94:97], v[198:201], a[200:203], v[94:97]\n\t"
"ds_read_b128 a[200:203], v242 offset:13312\n\t"
"v_mfma_f32_16x16x32_bf16 v[94:97], a[152:155], a[204:207], v[94:97]\n\t"
"buffer_load_dwordx4 a[252:255], v252, s[8:11], s20 offen offset:1024 sc1\n\t"
"s_waitcnt vmcnt(32) lgkmcnt(9)\n\t"
"v_mfma_f32_16x16x32_bf16 v[98:101], a[28:31], a[168:171], 0\n\t"
"ds_read_b128 a[204:207], v242 offset:13824\n\t"
"v_mfma_f32_16x16x32_bf16 v[98:101], v[206:209], a[172:175], v[98:101]\n\t"
"v_mfma_f32_16x16x32_bf16 v[98:101], a[36:39], a[176:179], v[98:101]\n\t"
"buffer_load_dwordx4 a[152:155], v252, s[8:11], s20 offen offset:2048 sc1\n\t"
"s_waitcnt lgkmcnt(8)\n\t"
"v_mfma_f32_16x16x32_bf16 v[102:105], a[28:31], a[172:175], 0\n\t"
"v_mfma_f32_16x16x32_bf16 v[102:105], v[206:209], a[176:179], v[102:105]\n\t"
"v_mfma_f32_16x16x32_bf16 v[102:105], a[36:39], a[180:183], v[102:105]\n\t"
"s_add_u32 s29, s22, 1376640\n\t"
"buffer_store_dwordx4 a[104:107], v247, s[16:19], s29 offen offset:128 nt\n\t"
"s_waitcnt lgkmcnt(0)\n\t"
"s_barrier\n\t"
"v_mfma_f32_16x16x32_bf16 v[106:109], a[28:31], a[176:179], 0\n\t"
"v_mfma_f32_16x16x32_bf16 v[106:109], v[206:209], a[180:183], v[106:109]\n\t"
"v_mfma_f32_16x16x32_bf16 v[106:109], a[36:39], a[184:187], v[106:109]\n\t"
"s_mov_b32 s20, 430080\n\t"
"buffer_load_dwordx4 v[198:201], v252, s[8:11], s20 offen offset:0 sc1\n\t"
"s_mov_b64 exec, s[44:45]\n\t"
"ds_write_b128 v240, v[230:233] offset:4608\n\t"
"s_mov_b64 exec, -1\n\t"
"v_mfma_f32_16x16x32_bf16 v[110:113], a[28:31], a[180:183], 0\n\t"
"ds_read_b64_tr_b16 v[230:231], v238 offset:40448\n\t"
"v_mfma_f32_16x16x32_bf16 v[110:113], v[206:209], a[184:187], v[110:113]\n\t"
"ds_read_b64_tr_b16 v[232:233], v238 offset:41984\n\t"
"v_mfma_f32_16x16x32_bf16 v[110:113], a[36:39], a[188:191], v[110:113]\n\t"
"s_mov_b64 exec, s[44:45]\n\t"
"ds_write_b128 v240, v[222:225] offset:6144\n\t"
"s_mov_b64 exec, -1\n\t"
"buffer_load_dwordx4 a[88:91], v252, s[8:11], s20 offen offset:1024 sc1\n\t"
"ds_read_b64_tr_b16 v[234:235], v239 offset:40448\n\t"
"ds_read_b64_tr_b16 v[236:237], v239 offset:41984\n\t"
"v_mfma_f32_16x16x32_bf16 v[114:117], a[28:31], a[184:187], 0\n\t"
"v_subrev_u32 v238, 59904, v238\n\t"
"v_subrev_u32 v239, 59904, v239\n\t"
"v_mfma_f32_16x16x32_bf16 v[114:117], v[206:209], a[188:191], v[114:117]\n\t"
"ds_read_b64_tr_b16 v[222:223], v238 offset:512\n\t"
"v_mfma_f32_16x16x32_bf16 v[114:117], a[36:39], a[192:195], v[114:117]\n\t"
"buffer_load_dwordx4 a[96:99], v252, s[8:11], s20 offen offset:2048 sc1\n\t"
"v_mfma_f32_16x16x32_bf16 v[118:121], a[28:31], a[188:191], 0\n\t"
"ds_read_b64_tr_b16 v[224:225], v238 offset:2048\n\t"
"v_mfma_f32_16x16x32_bf16 v[118:121], v[206:209], a[192:195], v[118:121]\n\t"
"ds_read_b64_tr_b16 v[226:227], v239 offset:512\n\t"
"v_mfma_f32_16x16x32_bf16 v[118:121], a[36:39], a[196:199], v[118:121]\n\t"
"ds_read_b64_tr_b16 v[228:229], v239 offset:2048\n\t"
"s_waitcnt lgkmcnt(7)\n\t"
"v_mfma_f32_16x16x32_bf16 v[230:233], v[230:233], a[208:211], 0\n\t"
"s_add_u32 s29, s22, 1376640\n\t"
"buffer_store_dwordx4 a[112:115], v247, s[16:19], s29 offen offset:256 nt\n\t"
"s_waitcnt lgkmcnt(4)\n\t"
"v_mfma_f32_16x16x32_bf16 v[234:237], v[234:237], a[208:211], 0\n\t"
"v_mfma_f32_16x16x32_bf16 v[122:125], a[28:31], a[192:195], 0\n\t"
"v_mfma_f32_16x16x32_bf16 v[122:125], v[206:209], a[196:199], v[122:125]\n\t"
"v_mfma_f32_16x16x32_bf16 v[122:125], a[36:39], a[200:203], v[122:125]\n\t"
"v_mfma_f32_16x16x32_bf16 v[126:129], a[28:31], a[196:199], 0\n\t"
"v_cvt_pk_bf16_f32 v230, v230, v231\n\t"
"v_cvt_pk_bf16_f32 v231, v232, v233\n\t"
"v_mfma_f32_16x16x32_bf16 v[126:129], v[206:209], a[200:203], v[126:129]\n\t"
"s_nop 0\n\t"
"v_cvt_pk_bf16_f32 v232, v234, v235\n\t"
"v_cvt_pk_bf16_f32 v233, v236, v237\n\t"
"v_mfma_f32_16x16x32_bf16 v[126:129], a[36:39], a[204:207], v[126:129]\n\t"
"s_mov_b32 s20, 110592\n\t"
"buffer_load_dwordx4 a[28:31], v252, s[8:11], s20 offen offset:0 sc1\n\t"
"s_waitcnt lgkmcnt(2)\n\t"
"v_mfma_f32_16x16x32_bf16 v[222:225], v[222:225], a[208:211], 0\n\t"
"buffer_load_dwordx4 v[206:209], v252, s[8:11], s20 offen offset:1024 sc1\n\t"
"s_waitcnt lgkmcnt(0)\n\t"
"v_mfma_f32_16x16x32_bf16 v[226:229], v[226:229], a[208:211], 0\n\t"
"s_mov_b64 exec, s[44:45]\n\t"
"ds_write_b128 v240, v[230:233] offset:7680\n\t"
"s_mov_b64 exec, -1\n\t"
"s_waitcnt vmcnt(35)\n\t"
"v_mfma_f32_16x16x32_bf16 v[130:133], a[160:163], a[168:171], 0\n\t"
"ds_read_b64_tr_b16 v[230:231], v238 offset:20480\n\t"
"v_mfma_f32_16x16x32_bf16 v[130:133], v[214:217], a[172:175], v[130:133]\n\t"
"ds_read_b64_tr_b16 v[232:233], v238 offset:22016\n\t"
"v_mfma_f32_16x16x32_bf16 v[130:133], a[120:123], a[176:179], v[130:133]\n\t"
"ds_read_b64_tr_b16 v[234:235], v239 offset:20480\n\t"
"ds_read_b64_tr_b16 v[236:237], v239 offset:22016\n\t"
"v_mfma_f32_16x16x32_bf16 v[134:137], a[160:163], a[172:175], 0\n\t"
"v_cvt_pk_bf16_f32 v222, v222, v223\n\t"
"v_cvt_pk_bf16_f32 v223, v224, v225\n\t"
"v_mfma_f32_16x16x32_bf16 v[134:137], v[214:217], a[176:179], v[134:137]\n\t"
"v_cvt_pk_bf16_f32 v224, v226, v227\n\t"
"v_cvt_pk_bf16_f32 v225, v228, v229\n\t"
"v_mfma_f32_16x16x32_bf16 v[134:137], a[120:123], a[180:183], v[134:137]\n\t"
"s_add_u32 s29, s23, 0\n\t"
"buffer_store_dwordx4 v[194:197], v247, s[16:19], s29 offen offset:0 nt\n\t"
"v_mfma_f32_16x16x32_bf16 v[138:141], a[160:163], a[176:179], 0\n\t"
"v_mfma_f32_16x16x32_bf16 v[138:141], v[214:217], a[180:183], v[138:141]\n\t"
"v_mfma_f32_16x16x32_bf16 v[138:141], a[120:123], a[184:187], v[138:141]\n\t"
"s_mov_b64 exec, s[44:45]\n\t"
"ds_write_b128 v240, v[222:225] offset:0\n\t"
"s_mov_b64 exec, -1\n\t"
"v_mfma_f32_16x16x32_bf16 v[142:145], a[160:163], a[180:183], 0\n\t"
"ds_read_b64_tr_b16 v[222:223], v238 offset:40448\n\t"
"v_mfma_f32_16x16x32_bf16 v[142:145], v[214:217], a[184:187], v[142:145]\n\t"
"ds_read_b64_tr_b16 v[224:225], v238 offset:41984\n\t"
"v_mfma_f32_16x16x32_bf16 v[142:145], a[120:123], a[188:191], v[142:145]\n\t"
"ds_read_b64_tr_b16 v[226:227], v239 offset:40448\n\t"
"ds_read_b64_tr_b16 v[228:229], v239 offset:41984\n\t"
"s_waitcnt lgkmcnt(7)\n\t"
"v_mfma_f32_16x16x32_bf16 v[230:233], v[230:233], a[208:211], 0\n\t"
"s_waitcnt lgkmcnt(5)\n\t"
"v_mfma_f32_16x16x32_bf16 v[234:237], v[234:237], a[208:211], 0\n\t"
"v_mfma_f32_16x16x32_bf16 v[146:149], a[160:163], a[184:187], 0\n\t"
"v_mfma_f32_16x16x32_bf16 v[146:149], v[214:217], a[188:191], v[146:149]\n\t"
"v_mfma_f32_16x16x32_bf16 v[146:149], a[120:123], a[192:195], v[146:149]\n\t"
"v_mfma_f32_16x16x32_bf16 v[150:153], a[160:163], a[188:191], 0\n\t"
"s_nop 1\n\t"
"v_cvt_pk_bf16_f32 v230, v230, v231\n\t"
"v_cvt_pk_bf16_f32 v231, v232, v233\n\t"
"v_mfma_f32_16x16x32_bf16 v[150:153], v[214:217], a[192:195], v[150:153]\n\t"
"v_cvt_pk_bf16_f32 v232, v234, v235\n\t"
"v_cvt_pk_bf16_f32 v233, v236, v237\n\t"
"v_mfma_f32_16x16x32_bf16 v[150:153], a[120:123], a[196:199], v[150:153]\n\t"
"s_add_u32 s29, s23, 0\n\t"
"buffer_store_dwordx4 v[202:205], v247, s[16:19], s29 offen offset:128 nt\n\t"
"s_waitcnt lgkmcnt(2)\n\t"
"v_mfma_f32_16x16x32_bf16 v[222:225], v[222:225], a[208:211], 0\n\t"
"s_mov_b64 exec, s[44:45]\n\t"
"ds_write_b128 v240, v[230:233] offset:1536\n\t"
"s_mov_b64 exec, -1\n\t"
"s_waitcnt lgkmcnt(1)\n\t"
"v_mfma_f32_16x16x32_bf16 v[226:229], v[226:229], a[208:211], 0\n\t"
"ds_read_b128 v[194:197], v252 offset:18432\n\t"
"v_mfma_f32_16x16x32_bf16 v[154:157], a[160:163], a[192:195], 0\n\t"
"ds_read_b128 a[24:27], v252 offset:30720\n\t"
"v_mfma_f32_16x16x32_bf16 v[154:157], v[214:217], a[196:199], v[154:157]\n\t"
"ds_read_b128 a[8:11], v252 offset:19456\n\t"
"v_mfma_f32_16x16x32_bf16 v[154:157], a[120:123], a[200:203], v[154:157]\n\t"
"buffer_load_dwordx4 a[208:211], v252, s[8:11], s20 offen offset:2048 sc1\n\t"
"v_mfma_f32_16x16x32_bf16 v[158:161], a[160:163], a[196:199], 0\n\t"
"v_cvt_pk_bf16_f32 v222, v222, v223\n\t"
"v_cvt_pk_bf16_f32 v223, v224, v225\n\t"
"v_mfma_f32_16x16x32_bf16 v[158:161], v[214:217], a[200:203], v[158:161]\n\t"
"v_cvt_pk_bf16_f32 v224, v226, v227\n\t"
"v_cvt_pk_bf16_f32 v225, v228, v229\n\t"
"v_mfma_f32_16x16x32_bf16 v[158:161], a[120:123], a[204:207], v[158:161]\n\t"
"ds_read_b128 a[32:35], v252 offset:31744\n\t"
"ds_read_b128 a[16:19], v252 offset:20480\n\t"
"s_waitcnt vmcnt(33)\n\t"
"v_mfma_f32_16x16x32_bf16 v[162:165], a[212:215], a[168:171], 0\n\t"
"ds_read_b128 a[56:59], v252 offset:32768\n\t"
"v_mfma_f32_16x16x32_bf16 v[162:165], a[216:219], a[172:175], v[162:165]\n\t"
"v_mfma_f32_16x16x32_bf16 v[162:165], a[128:131], a[176:179], v[162:165]\n\t"
"s_mov_b64 exec, s[44:45]\n\t"
"ds_write_b128 v240, v[222:225] offset:3072\n\t"
"s_mov_b64 exec, -1\n\t"
"v_mfma_f32_16x16x32_bf16 v[166:169], a[212:215], a[172:175], 0\n\t"
"v_mfma_f32_16x16x32_bf16 v[166:169], a[216:219], a[176:179], v[166:169]\n\t"
"v_mfma_f32_16x16x32_bf16 v[166:169], a[128:131], a[180:183], v[166:169]\n\t"
"s_add_u32 s29, s23, 0\n\t"
"buffer_store_dwordx4 v[210:213], v247, s[16:19], s29 offen offset:256 nt\n\t"
"v_mfma_f32_16x16x32_bf16 v[170:173], a[212:215], a[176:179], 0\n\t"
"v_mfma_f32_16x16x32_bf16 v[170:173], a[216:219], a[180:183], v[170:173]\n\t"
"v_mfma_f32_16x16x32_bf16 v[170:173], a[128:131], a[184:187], v[170:173]\n\t"
"v_mfma_f32_16x16x32_bf16 v[174:177], a[212:215], a[180:183], 0\n\t"
"v_mfma_f32_16x16x32_bf16 v[174:177], a[216:219], a[184:187], v[174:177]\n\t"
"v_mfma_f32_16x16x32_bf16 v[174:177], a[128:131], a[188:191], v[174:177]\n\t"
"s_waitcnt lgkmcnt(0)\n\t"
"s_barrier\n\t"
"v_mfma_f32_16x16x32_bf16 v[178:181], a[212:215], a[184:187], 0\n\t"
"ds_read_b128 a[168:171], v242 offset:0\n\t"
"v_mfma_f32_16x16x32_bf16 v[178:181], a[216:219], a[188:191], v[178:181]\n\t"
"ds_read_b128 a[172:175], v242 offset:512\n\t"
"v_mfma_f32_16x16x32_bf16 v[178:181], a[128:131], a[192:195], v[178:181]\n\t"
"ds_read_b128 a[176:179], v242 offset:1024\n\t"
"ds_read_b128 a[180:183], v242 offset:1536\n\t"
"v_mfma_f32_16x16x32_bf16 v[182:185], a[212:215], a[188:191], 0\n\t"
"ds_read_b128 a[184:187], v242 offset:2048\n\t"
"v_mfma_f32_16x16x32_bf16 v[182:185], a[216:219], a[192:195], v[182:185]\n\t"
"ds_read_b128 a[188:191], v242 offset:2560\n\t"
"v_mfma_f32_16x16x32_bf16 v[182:185], a[128:131], a[196:199], v[182:185]\n\t"
"ds_write_b128 v248, a[44:47] offset:18432\n\t"
"v_mfma_f32_16x16x32_bf16 v[186:189], a[212:215], a[192:195], 0\n\t"
"ds_read_b128 a[192:195], v242 offset:3072\n\t"
"v_mfma_f32_16x16x32_bf16 v[186:189], a[216:219], a[196:199], v[186:189]\n\t"
"v_mfma_f32_16x16x32_bf16 v[186:189], a[128:131], a[200:203], v[186:189]\n\t"
"ds_write_b128 v248, a[60:63] offset:30720\n\t"
"v_mfma_f32_16x16x32_bf16 v[190:193], a[212:215], a[196:199], 0\n\t"
"ds_read_b128 a[196:199], v242 offset:3584\n\t"
"v_mfma_f32_16x16x32_bf16 v[190:193], a[216:219], a[200:203], v[190:193]\n\t"
"ds_read_b128 a[200:203], v242 offset:4096\n\t"
"v_mfma_f32_16x16x32_bf16 v[190:193], a[128:131], a[204:207], v[190:193]\n\t"
"s_mov_b32 s20, 122880\n\t"
"buffer_load_dwordx4 a[212:215], v252, s[8:11], s20 offen offset:0 sc1\n\t"
"s_add_u32 s29, s23, 393216\n\t"
"buffer_store_dwordx4 a[0:3], v247, s[16:19], s29 offen offset:0 nt\n\t"
"s_waitcnt vmcnt(31) lgkmcnt(8)\n\t"
"v_mfma_f32_16x16x32_bf16 v[2:5], a[220:223], a[168:171], v[2:5]\n\t"
"ds_read_b128 a[204:207], v242 offset:4608\n\t"
"v_mfma_f32_16x16x32_bf16 v[2:5], a[136:139], a[172:175], v[2:5]\n\t"
"v_mfma_f32_16x16x32_bf16 v[2:5], a[4:7], a[176:179], v[2:5]\n\t"
"buffer_load_dwordx4 a[216:219], v252, s[8:11], s20 offen offset:1024 sc1\n\t"
"ds_write_b64 v249, a[110:111] offset:18432\n\t"
"s_waitcnt lgkmcnt(9)\n\t"
"v_mfma_f32_16x16x32_bf16 v[6:9], a[220:223], a[172:175], v[6:9]\n\t"
"v_mfma_f32_16x16x32_bf16 v[6:9], a[136:139], a[176:179], v[6:9]\n\t"
"v_mfma_f32_16x16x32_bf16 v[6:9], a[4:7], a[180:183], v[6:9]\n\t"
"ds_write_b64 v249, a[126:127] offset:30720\n\t"
"s_waitcnt lgkmcnt(9)\n\t"
"v_mfma_f32_16x16x32_bf16 v[10:13], a[220:223], a[176:179], v[10:13]\n\t"
"v_mfma_f32_16x16x32_bf16 v[10:13], a[136:139], a[180:183], v[10:13]\n\t"
"v_mfma_f32_16x16x32_bf16 v[10:13], a[4:7], a[184:187], v[10:13]\n\t"
"ds_write_b128 v248, a[52:55] offset:24576\n\t"
"s_waitcnt lgkmcnt(9)\n\t"
"v_mfma_f32_16x16x32_bf16 v[14:17], a[220:223], a[180:183], v[14:17]\n\t"
"v_mfma_f32_16x16x32_bf16 v[14:17], a[136:139], a[184:187], v[14:17]\n\t"
"v_mfma_f32_16x16x32_bf16 v[14:17], a[4:7], a[188:191], v[14:17]\n\t"
"ds_write_b128 v248, a[68:71] offset:36864\n\t"
"s_waitcnt lgkmcnt(8)\n\t"
"v_mfma_f32_16x16x32_bf16 v[18:21], a[220:223], a[184:187], v[18:21]\n\t"
"v_mfma_f32_16x16x32_bf16 v[18:21], a[136:139], a[188:191], v[18:21]\n\t"
"v_mfma_f32_16x16x32_bf16 v[18:21], a[4:7], a[192:195], v[18:21]\n\t"
"s_add_u32 s29, s23, 393216\n\t"
"buffer_store_dwordx4 a[40:43], v247, s[16:19], s29 offen offset:128 nt\n\t"
"s_waitcnt lgkmcnt(6)\n\t"
"v_mfma_f32_16x16x32_bf16 v[22:25], a[220:223], a[188:191], v[22:25]\n\t"
"v_mfma_f32_16x16x32_bf16 v[22:25], a[136:139], a[192:195], v[22:25]\n\t"
"v_mfma_f32_16x16x32_bf16 v[22:25], a[4:7], a[196:199], v[22:25]\n\t"
"ds_write_b64 v249, a[118:119] offset:24576\n\t"
"s_waitcnt lgkmcnt(6)\n\t"
"v_mfma_f32_16x16x32_bf16 v[26:29], a[220:223], a[192:195], v[26:29]\n\t"
"v_mfma_f32_16x16x32_bf16 v[26:29], a[136:139], a[196:199], v[26:29]\n\t"
"v_mfma_f32_16x16x32_bf16 v[26:29], a[4:7], a[200:203], v[26:29]\n\t"
"ds_write_b64 v249, a[134:135] offset:36864\n\t"
"s_waitcnt lgkmcnt(6)\n\t"
"v_mfma_f32_16x16x32_bf16 v[30:33], a[220:223], a[196:199], v[30:33]\n\t"
"v_mfma_f32_16x16x32_bf16 v[30:33], a[136:139], a[200:203], v[30:33]\n\t"
"v_mfma_f32_16x16x32_bf16 v[30:33], a[4:7], a[204:207], v[30:33]\n\t"
"buffer_load_dwordx4 a[220:223], v252, s[8:11], s20 offen offset:2048 sc1\n\t"
"s_waitcnt vmcnt(30)\n\t"
"v_mfma_f32_16x16x32_bf16 v[34:37], a[12:15], a[168:171], v[34:37]\n\t"
"v_mfma_f32_16x16x32_bf16 v[34:37], a[64:67], a[172:175], v[34:37]\n\t"
"v_mfma_f32_16x16x32_bf16 v[34:37], a[224:227], a[176:179], v[34:37]\n\t"
"ds_read_b64_tr_b16 v[230:231], v244 offset:0\n\t"
"ds_read_b64_tr_b16 v[232:233], v244 offset:3072\n\t"
"v_mfma_f32_16x16x32_bf16 v[38:41], a[12:15], a[172:175], v[38:41]\n\t"
"v_mfma_f32_16x16x32_bf16 v[38:41], a[64:67], a[176:179], v[38:41]\n\t"
"v_mfma_f32_16x16x32_bf16 v[38:41], a[224:227], a[180:183], v[38:41]\n\t"
"v_mfma_f32_16x16x32_bf16 v[42:45], a[12:15], a[176:179], v[42:45]\n\t"
"v_mfma_f32_16x16x32_bf16 v[42:45], a[64:67], a[180:183], v[42:45]\n\t"
"v_mfma_f32_16x16x32_bf16 v[42:45], a[224:227], a[184:187], v[42:45]\n\t"
"s_add_u32 s29, s23, 393216\n\t"
"buffer_store_dwordx4 a[48:51], v247, s[16:19], s29 offen offset:256 nt\n\t"
"v_mfma_f32_16x16x32_bf16 v[46:49], a[12:15], a[180:183], v[46:49]\n\t"
"ds_read_b64_tr_b16 v[234:235], v244 offset:6144\n\t"
"v_mfma_f32_16x16x32_bf16 v[46:49], a[64:67], a[184:187], v[46:49]\n\t"
"ds_read_b64_tr_b16 v[236:237], v244 offset:9216\n\t"
"v_mfma_f32_16x16x32_bf16 v[46:49], a[224:227], a[188:191], v[46:49]\n\t"
"s_waitcnt lgkmcnt(4)\n\t"
"s_barrier\n\t"
"v_mfma_f32_16x16x32_bf16 v[50:53], a[12:15], a[184:187], v[50:53]\n\t"
"v_mfma_f32_16x16x32_bf16 v[50:53], a[64:67], a[188:191], v[50:53]\n\t"
"v_mfma_f32_16x16x32_bf16 v[50:53], a[224:227], a[192:195], v[50:53]\n\t"
"s_mov_b32 s20, 135168\n\t"
"buffer_load_dwordx4 a[108:111], v252, s[8:11], s20 offen offset:0 sc1\n\t"
"v_mfma_f32_16x16x32_bf16 v[54:57], a[12:15], a[188:191], v[54:57]\n\t"
"v_mfma_f32_16x16x32_bf16 v[54:57], a[64:67], a[192:195], v[54:57]\n\t"
"v_mfma_f32_16x16x32_bf16 v[54:57], a[224:227], a[196:199], v[54:57]\n\t"
"buffer_load_dwordx4 a[116:119], v252, s[8:11], s20 offen offset:1024 sc1\n\t"
"s_waitcnt lgkmcnt(2)\n\t"
"v_mfma_f32_16x16x32_bf16 v[230:233], v[230:233], v[218:221], 0\n\t"
"v_mfma_f32_16x16x32_bf16 v[58:61], a[12:15], a[192:195], v[58:61]\n\t"
"ds_read_b64_tr_b16 v[222:223], v244 offset:12288\n\t"
"v_mfma_f32_16x16x32_bf16 v[58:61], a[64:67], a[196:199], v[58:61]\n\t"
"ds_read_b64_tr_b16 v[224:225], v244 offset:15360\n\t"
"v_mfma_f32_16x16x32_bf16 v[58:61], a[224:227], a[200:203], v[58:61]\n\t"
"buffer_load_dwordx4 a[124:127], v252, s[8:11], s20 offen offset:2048 sc1\n\t"
"v_mfma_f32_16x16x32_bf16 v[62:65], a[12:15], a[196:199], v[62:65]\n\t"
"v_mfma_f32_16x16x32_bf16 v[62:65], a[64:67], a[200:203], v[62:65]\n\t"
"v_cvt_pk_bf16_f32 v230, v230, v231\n\t"
"v_cvt_pk_bf16_f32 v231, v232, v233\n\t"
"v_mfma_f32_16x16x32_bf16 v[62:65], a[224:227], a[204:207], v[62:65]\n\t"
"s_add_u32 s29, s23, 786432\n\t"
"buffer_store_dwordx4 v[194:197], v247, s[16:19], s29 offen offset:0 nt\n\t"
"s_waitcnt lgkmcnt(2)\n\t"
"v_mfma_f32_16x16x32_bf16 v[234:237], v[234:237], v[218:221], 0\n\t"
"s_mov_b32 s20, 147456\n\t"
"buffer_load_dwordx4 a[224:227], v252, s[8:11], s20 offen offset:0 sc1\n\t"
"s_waitcnt vmcnt(31)\n\t"
"v_mfma_f32_16x16x32_bf16 v[66:69], a[228:231], a[168:171], v[66:69]\n\t"
"ds_read_b64_tr_b16 v[226:227], v244 offset:19968\n\t"
"v_mfma_f32_16x16x32_bf16 v[66:69], a[232:235], a[172:175], v[66:69]\n\t"
"ds_read_b64_tr_b16 v[228:229], v244 offset:23040\n\t"
"v_mfma_f32_16x16x32_bf16 v[66:69], a[236:239], a[176:179], v[66:69]\n\t"
"v_mfma_f32_16x16x32_bf16 v[70:73], a[228:231], a[172:175], v[70:73]\n\t"
"ds_read_b128 a[168:171], v242 offset:9216\n\t"
"v_mfma_f32_16x16x32_bf16 v[70:73], a[232:235], a[176:179], v[70:73]\n\t"
"v_cvt_pk_bf16_f32 v234, v234, v235\n\t"
"v_mfma_f32_16x16x32_bf16 v[70:73], a[236:239], a[180:183], v[70:73]\n\t"
"v_cvt_pk_bf16_f32 v235, v236, v237\n\t"
"v_mfma_f32_16x16x32_bf16 v[74:77], a[228:231], a[176:179], v[74:77]\n\t"
"ds_read_b128 a[172:175], v242 offset:9728\n\t"
"v_mfma_f32_16x16x32_bf16 v[74:77], a[232:235], a[180:183], v[74:77]\n\t"
"ds_read_b128 a[176:179], v242 offset:10240\n\t"
"v_mfma_f32_16x16x32_bf16 v[74:77], a[236:239], a[184:187], v[74:77]\n\t"
"s_waitcnt lgkmcnt(5)\n\t"
"v_mfma_f32_16x16x32_bf16 v[222:225], v[222:225], v[218:221], 0\n\t"
"v_mfma_f32_16x16x32_bf16 v[78:81], a[228:231], a[180:183], v[78:81]\n\t"
"ds_read_b128 a[180:183], v242 offset:10752\n\t"
"v_mfma_f32_16x16x32_bf16 v[78:81], a[232:235], a[184:187], v[78:81]\n\t"
"ds_read_b128 a[72:75], v252 offset:18432\n\t"
"v_mfma_f32_16x16x32_bf16 v[78:81], a[236:239], a[188:191], v[78:81]\n\t"
"s_add_u32 s29, s23, 786432\n\t"
"buffer_store_dwordx4 a[8:11], v247, s[16:19], s29 offen offset:128 nt\n\t"
"v_mfma_f32_16x16x32_bf16 v[82:85], a[228:231], a[184:187], v[82:85]\n\t"
"ds_read_b128 a[184:187], v242 offset:11264\n\t"
"v_mfma_f32_16x16x32_bf16 v[82:85], a[232:235], a[188:191], v[82:85]\n\t"
"v_cvt_pk_bf16_f32 v222, v222, v223\n\t"
"v_cvt_pk_bf16_f32 v223, v224, v225\n\t"
"v_mfma_f32_16x16x32_bf16 v[82:85], a[236:239], a[192:195], v[82:85]\n\t"
"ds_read_b128 v[214:217], v252 offset:30720\n\t"
"s_waitcnt lgkmcnt(7)\n\t"
"v_mfma_f32_16x16x32_bf16 v[226:229], v[226:229], v[218:221], 0\n\t"
"ds_read_b128 a[104:107], v252 offset:19456\n\t"
"v_mfma_f32_16x16x32_bf16 v[86:89], a[228:231], a[188:191], v[86:89]\n\t"
"ds_read_b128 a[188:191], v242 offset:11776\n\t"
"v_mfma_f32_16x16x32_bf16 v[86:89], a[232:235], a[192:195], v[86:89]\n\t"
"ds_read_b128 a[36:39], v252 offset:31744\n\t"
"v_mfma_f32_16x16x32_bf16 v[86:89], a[236:239], a[196:199], v[86:89]\n\t"
"ds_read_b128 a[144:147], v252 offset:20480\n\t"
"v_mfma_f32_16x16x32_bf16 v[90:93], a[228:231], a[192:195], v[90:93]\n\t"
"ds_read_b128 a[192:195], v242 offset:12288\n\t"
"v_mfma_f32_16x16x32_bf16 v[90:93], a[232:235], a[196:199], v[90:93]\n\t"
"v_cvt_pk_bf16_f32 v226, v226, v227\n\t"
"v_cvt_pk_bf16_f32 v227, v228, v229\n\t"
"v_mfma_f32_16x16x32_bf16 v[90:93], a[236:239], a[200:203], v[90:93]\n\t"
"ds_read_b128 a[68:71], v252 offset:32768\n\t"
"v_mfma_f32_16x16x32_bf16 v[94:97], a[228:231], a[196:199], v[94:97]\n\t"
"ds_read_b128 a[196:199], v242 offset:12800\n\t"
"v_mfma_f32_16x16x32_bf16 v[94:97], a[232:235], a[200:203], v[94:97]\n\t"
"ds_read_b128 a[200:203], v242 offset:13312\n\t"
"v_mfma_f32_16x16x32_bf16 v[94:97], a[236:239], a[204:207], v[94:97]\n\t"
"buffer_load_dwordx4 a[228:231], v252, s[8:11], s20 offen offset:1024 sc1\n\t"
"s_waitcnt vmcnt(28) lgkmcnt(12)\n\t"
"v_mfma_f32_16x16x32_bf16 v[98:101], a[240:243], a[168:171], v[98:101]\n\t"
"ds_read_b128 a[204:207], v242 offset:13824\n\t"
"v_mfma_f32_16x16x32_bf16 v[98:101], a[244:247], a[172:175], v[98:101]\n\t"
"v_mfma_f32_16x16x32_bf16 v[98:101], a[20:23], a[176:179], v[98:101]\n\t"
"buffer_load_dwordx4 a[232:235], v252, s[8:11], s20 offen offset:2048 sc1\n\t"
"s_waitcnt lgkmcnt(12)\n\t"
"v_mfma_f32_16x16x32_bf16 v[102:105], a[240:243], a[172:175], v[102:105]\n\t"
"v_mfma_f32_16x16x32_bf16 v[102:105], a[244:247], a[176:179], v[102:105]\n\t"
"v_mfma_f32_16x16x32_bf16 v[102:105], a[20:23], a[180:183], v[102:105]\n\t"
"s_add_u32 s29, s23, 786432\n\t"
"buffer_store_dwordx4 a[16:19], v247, s[16:19], s29 offen offset:256 nt\n\t"
"s_waitcnt lgkmcnt(0)\n\t"
"s_barrier\n\t"
"v_mfma_f32_16x16x32_bf16 v[106:109], a[240:243], a[176:179], v[106:109]\n\t"
"v_fma_f32 v194, -v2, s56, -v98\n\t"
"v_fma_f32 v195, -v3, s56, -v99\n\t"
"v_mfma_f32_16x16x32_bf16 v[106:109], a[244:247], a[180:183], v[106:109]\n\t"
"v_fma_f32 v196, -v4, s56, -v100\n\t"
"v_fma_f32 v197, -v5, s56, -v101\n\t"
"v_mfma_f32_16x16x32_bf16 v[106:109], a[20:23], a[184:187], v[106:109]\n\t"
"s_mov_b32 s20, 159744\n\t"
"buffer_load_dwordx4 a[236:239], v252, s[8:11], s20 offen offset:0 sc1\n\t"
"ds_write_b64 v243, v[230:231] offset:0\n\t"
"v_mfma_f32_16x16x32_bf16 v[110:113], a[240:243], a[180:183], v[110:113]\n\t"
"ds_read_b64_tr_b16 v[230:231], v244 offset:26112\n\t"
"v_fma_f32 v202, -v6, s56, -v102\n\t"
"v_mfma_f32_16x16x32_bf16 v[110:113], a[244:247], a[184:187], v[110:113]\n\t"
"ds_read_b64_tr_b16 v[232:233], v244 offset:29184\n\t"
"v_fma_f32 v203, -v7, s56, -v103\n\t"
"v_mfma_f32_16x16x32_bf16 v[110:113], a[20:23], a[188:191], v[110:113]\n\t"
"ds_write_b64 v243, v[234:235] offset:512\n\t"
"ds_write_b64 v243, v[222:223] offset:1024\n\t"
"v_mfma_f32_16x16x32_bf16 v[114:117], a[240:243], a[184:187], v[114:117]\n\t"
"ds_read_b64_tr_b16 v[234:235], v244 offset:32256\n\t"
"v_fma_f32 v204, -v8, s56, -v104\n\t"
"v_mfma_f32_16x16x32_bf16 v[114:117], a[244:247], a[188:191], v[114:117]\n\t"
"ds_read_b64_tr_b16 v[236:237], v244 offset:35328\n\t"
"v_fma_f32 v205, -v9, s56, -v105\n\t"
"v_mfma_f32_16x16x32_bf16 v[114:117], a[20:23], a[192:195], v[114:117]\n\t"
"ds_write_b64 v243, v[226:227] offset:1536\n\t"
"v_mfma_f32_16x16x32_bf16 v[118:121], a[240:243], a[188:191], v[118:121]\n\t"
"ds_read_b64_tr_b16 v[222:223], v244 offset:39936\n\t"
"v_fma_f32 v210, -v10, s56, -v106\n\t"
"v_mfma_f32_16x16x32_bf16 v[118:121], a[244:247], a[192:195], v[118:121]\n\t"
"ds_read_b64_tr_b16 v[224:225], v244 offset:43008\n\t"
"v_fma_f32 v211, -v11, s56, -v107\n\t"
"v_mfma_f32_16x16x32_bf16 v[118:121], a[20:23], a[196:199], v[118:121]\n\t"
"ds_write_b128 v248, a[76:79] offset:18432\n\t"
"ds_write_b128 v248, a[92:95] offset:30720\n\t"
"s_waitcnt lgkmcnt(9)\n\t"
"v_mfma_f32_16x16x32_bf16 v[230:233], v[230:233], v[218:221], 0\n\t"
"s_add_u32 s29, s23, 1179648\n\t"
"buffer_store_dwordx4 a[24:27], v247, s[16:19], s29 offen offset:0 nt\n\t"
"v_mfma_f32_16x16x32_bf16 v[122:125], a[240:243], a[192:195], v[122:125]\n\t"
"ds_read_b64_tr_b16 v[226:227], v244 offset:46080\n\t"
"v_fma_f32 v212, -v12, s56, -v108\n\t"
"v_mfma_f32_16x16x32_bf16 v[122:125], a[244:247], a[196:199], v[122:125]\n\t"
"ds_read_b64_tr_b16 v[228:229], v244 offset:49152\n\t"
"v_fma_f32 v213, -v13, s56, -v109\n\t"
"v_mfma_f32_16x16x32_bf16 v[122:125], a[20:23], a[200:203], v[122:125]\n\t"
"ds_write_b64 v249, a[142:143] offset:18432\n\t"
"s_waitcnt lgkmcnt(8)\n\t"
"v_mfma_f32_16x16x32_bf16 v[234:237], v[234:237], v[218:221], 0\n\t"
"ds_write_b64 v249, a[158:159] offset:30720\n\t"
"v_mfma_f32_16x16x32_bf16 v[126:129], a[240:243], a[196:199], v[126:129]\n\t"
"v_cvt_pk_bf16_f32 v230, v230, v231\n\t"
"v_cvt_pk_bf16_f32 v231, v232, v233\n\t"
"v_mfma_f32_16x16x32_bf16 v[126:129], a[244:247], a[200:203], v[126:129]\n\t"
"v_fma_f32 v250, -v14, s56, -v110\n\t"
"v_fma_f32 v251, -v15, s56, -v111\n\t"
"v_mfma_f32_16x16x32_bf16 v[126:129], a[20:23], a[204:207], v[126:129]\n\t"
"buffer_load_dwordx4 a[240:243], v252, s[8:11], s20 offen offset:1024 sc1\n\t"
"s_waitcnt lgkmcnt(6)\n\t"
"v_mfma_f32_16x16x32_bf16 v[222:225], v[222:225], v[218:221], 0\n\t"
"buffer_load_dwordx4 a[244:247], v252, s[8:11], s20 offen offset:2048 sc1\n\t"
"s_waitcnt vmcnt(30)\n\t"
"v_mfma_f32_16x16x32_bf16 v[130:133], a[248:251], a[168:171], v[130:133]\n\t"
"v_cvt_pk_bf16_f32 v234, v234, v235\n\t"
"v_cvt_pk_bf16_f32 v235, v236, v237\n\t"
"v_mfma_f32_16x16x32_bf16 v[130:133], a[252:255], a[172:175], v[130:133]\n\t"
"v_accvgpr_write_b32 a0, v250\n\t"
"v_fma_f32 v250, -v16, s56, -v112\n\t"
"v_mfma_f32_16x16x32_bf16 v[130:133], a[152:155], a[176:179], v[130:133]\n\t"
"ds_write_b64 v243, v[230:231] offset:2048\n\t"
"ds_read_b64_tr_b16 v[230:231], v244 offset:52224\n\t"
"ds_read_b64_tr_b16 v[232:233], v244 offset:55296\n\t"
"s_waitcnt lgkmcnt(5)\n\t"
"v_mfma_f32_16x16x32_bf16 v[226:229], v[226:229], v[218:221], 0\n\t"
"ds_write_b64 v243, v[234:235] offset:2560\n\t"
"s_add_u32 s29, s23, 1179648\n\t"
"buffer_store_dwordx4 a[32:35], v247, s[16:19], s29 offen offset:128 nt\n\t"
"v_mfma_f32_16x16x32_bf16 v[134:137], a[248:251], a[172:175], v[134:137]\n\t"
"v_cvt_pk_bf16_f32 v222, v222, v223\n\t"
"v_cvt_pk_bf16_f32 v223, v224, v225\n\t"
"v_mfma_f32_16x16x32_bf16 v[134:137], a[252:255], a[176:179], v[134:137]\n\t"
"v_accvgpr_write_b32 a1, v251\n\t"
"v_fma_f32 v251, -v17, s56, -v113\n\t"
"v_mfma_f32_16x16x32_bf16 v[134:137], a[152:155], a[180:183], v[134:137]\n\t"
"ds_write_b128 v248, a[84:87] offset:24576\n\t"
"ds_write_b128 v248, a[100:103] offset:36864\n\t"
"v_mfma_f32_16x16x32_bf16 v[138:141], a[248:251], a[176:179], v[138:141]\n\t"
"v_accvgpr_write_b32 a2, v250\n\t"
"v_accvgpr_write_b32 a3, v251\n\t"
"v_mfma_f32_16x16x32_bf16 v[138:141], a[252:255], a[180:183], v[138:141]\n\t"
"v_cvt_pk_bf16_f32 v226, v226, v227\n\t"
"v_cvt_pk_bf16_f32 v227, v228, v229\n\t"
"v_mfma_f32_16x16x32_bf16 v[138:141], a[152:155], a[184:187], v[138:141]\n\t"
"ds_write_b64 v243, v[222:223] offset:3072\n\t"
"ds_write_b64 v243, v[226:227] offset:3584\n\t"
"v_mfma_f32_16x16x32_bf16 v[142:145], a[248:251], a[180:183], v[142:145]\n\t"
"v_fma_f32 v250, -v18, s56, -v114\n\t"
"v_fma_f32 v251, -v19, s56, -v115\n\t"
"v_mfma_f32_16x16x32_bf16 v[142:145], a[252:255], a[184:187], v[142:145]\n\t"
"v_accvgpr_write_b32 a8, v250\n\t"
"v_fma_f32 v250, -v20, s56, -v116\n\t"
"v_mfma_f32_16x16x32_bf16 v[142:145], a[152:155], a[188:191], v[142:145]\n\t"
"ds_write_b64 v249, a[150:151] offset:24576\n\t"
"ds_write_b64 v249, a[166:167] offset:36864\n\t"
"s_waitcnt lgkmcnt(7)\n\t"
"v_mfma_f32_16x16x32_bf16 v[230:233], v[230:233], v[218:221], 0\n\t"
"v_accvgpr_write_b32 a9, v251\n\t"
"v_fma_f32 v251, -v21, s56, -v117\n\t"
"v_mfma_f32_16x16x32_bf16 v[146:149], a[248:251], a[184:187], v[146:149]\n\t"
"v_accvgpr_write_b32 a10, v250\n\t"
"v_accvgpr_write_b32 a11, v251\n\t"
"v_mfma_f32_16x16x32_bf16 v[146:149], a[252:255], a[188:191], v[146:149]\n\t"
"v_fma_f32 v250, -v22, s56, -v118\n\t"
"v_fma_f32 v251, -v23, s56, -v119\n\t"
"v_mfma_f32_16x16x32_bf16 v[146:149], a[152:155], a[192:195], v[146:149]\n\t"
"v_accvgpr_write_b32 a16, v250\n\t"
"v_fma_f32 v250, -v24, s56, -v120\n\t"
"v_accvgpr_write_b32 a17, v251\n\t"
"v_fma_f32 v251, -v25, s56, -v121\n\t"
"v_mfma_f32_16x16x32_bf16 v[150:153], a[248:251], a[188:191], v[150:153]\n\t"
"v_accvgpr_write_b32 a18, v250\n\t"
"v_accvgpr_write_b32 a19, v251\n\t"
"v_mfma_f32_16x16x32_bf16 v[150:153], a[252:255], a[192:195], v[150:153]\n\t"
"v_cvt_pk_bf16_f32 v230, v230, v231\n\t"
"v_cvt_pk_bf16_f32 v231, v232, v233\n\t"
"v_mfma_f32_16x16x32_bf16 v[150:153], a[152:155], a[196:199], v[150:153]\n\t"
"ds_write_b64 v243, v[230:231] offset:4096\n\t"
"s_add_u32 s29, s23, 1179648\n\t"
"buffer_store_dwordx4 a[56:59], v247, s[16:19], s29 offen offset:256 nt\n\t"
"v_mfma_f32_16x16x32_bf16 v[154:157], a[248:251], a[192:195], v[154:157]\n\t"
"v_fma_f32 v250, -v26, s56, -v122\n\t"
"v_fma_f32 v251, -v27, s56, -v123\n\t"
"v_mfma_f32_16x16x32_bf16 v[154:157], a[252:255], a[196:199], v[154:157]\n\t"
"v_accvgpr_write_b32 a24, v250\n\t"
"v_fma_f32 v250, -v28, s56, -v124\n\t"
"v_mfma_f32_16x16x32_bf16 v[154:157], a[152:155], a[200:203], v[154:157]\n\t"
"v_accvgpr_write_b32 a25, v251\n\t"
"v_fma_f32 v251, -v29, s56, -v125\n\t"
"v_accvgpr_write_b32 a26, v250\n\t"
"v_accvgpr_write_b32 a27, v251\n\t"
"v_mfma_f32_16x16x32_bf16 v[158:161], a[248:251], a[196:199], v[158:161]\n\t"
"v_fma_f32 v250, -v30, s56, -v126\n\t"
"v_fma_f32 v251, -v31, s56, -v127\n\t"
"v_mfma_f32_16x16x32_bf16 v[158:161], a[252:255], a[200:203], v[158:161]\n\t"
"v_accvgpr_write_b32 a32, v250\n\t"
"v_fma_f32 v250, -v32, s56, -v128\n\t"
"v_mfma_f32_16x16x32_bf16 v[158:161], a[152:155], a[204:207], v[158:161]\n\t"
"s_mov_b32 s20, 172032\n\t"
"buffer_load_dwordx4 a[248:251], v252, s[8:11], s20 offen offset:0 sc1\n\t"
"v_accvgpr_write_b32 a33, v251\n\t"
"v_fma_f32 v251, -v33, s56, -v129\n\t"
"s_waitcnt vmcnt(29)\n\t"
"v_mfma_f32_16x16x32_bf16 v[162:165], v[198:201], a[168:171], v[162:165]\n\t"
"v_accvgpr_write_b32 a34, v250\n\t"
"v_accvgpr_write_b32 a35, v251\n\t"
"v_mfma_f32_16x16x32_bf16 v[162:165], a[88:91], a[172:175], v[162:165]\n\t"
"v_fma_f32 v250, -v34, s56, -v130\n\t"
"v_fma_f32 v251, -v35, s56, -v131\n\t"
"v_mfma_f32_16x16x32_bf16 v[162:165], a[96:99], a[176:179], v[162:165]\n\t"
"buffer_load_dwordx4 a[252:255], v252, s[8:11], s20 offen offset:1024 sc1\n\t"
"v_accvgpr_write_b32 a40, v250\n\t"
"v_fma_f32 v250, -v36, s56, -v132\n\t"
"s_waitcnt lgkmcnt(0)\n\t"
"s_barrier\n\t"
"v_mfma_f32_16x16x32_bf16 v[166:169], v[198:201], a[172:175], v[166:169]\n\t"
"ds_read_b128 a[168:171], v242 offset:0\n\t"
"v_accvgpr_write_b32 a41, v251\n\t"
"v_mfma_f32_16x16x32_bf16 v[166:169], a[88:91], a[176:179], v[166:169]\n\t"
"ds_read_b128 a[172:175], v242 offset:512\n\t"
"v_fma_f32 v251, -v37, s56, -v133\n\t"
"v_mfma_f32_16x16x32_bf16 v[166:169], a[96:99], a[180:183], v[166:169]\n\t"
"v_accvgpr_write_b32 a42, v250\n\t"
"v_accvgpr_write_b32 a43, v251\n\t"
"v_fma_f32 v250, -v38, s56, -v134\n\t"
"v_fma_f32 v251, -v39, s56, -v135\n\t"
"v_mfma_f32_16x16x32_bf16 v[170:173], v[198:201], a[176:179], v[170:173]\n\t"
"ds_read_b128 a[176:179], v242 offset:1024\n\t"
"v_accvgpr_write_b32 a48, v250\n\t"
"v_mfma_f32_16x16x32_bf16 v[170:173], a[88:91], a[180:183], v[170:173]\n\t"
"v_fma_f32 v250, -v40, s56, -v136\n\t"
"v_accvgpr_write_b32 a49, v251\n\t"
"v_mfma_f32_16x16x32_bf16 v[170:173], a[96:99], a[184:187], v[170:173]\n\t"
"s_add_u32 s29, s23, 1152\n\t"
"buffer_store_dwordx4 a[72:75], v247, s[16:19], s29 offen offset:0 nt\n\t"
"v_fma_f32 v251, -v41, s56, -v137\n\t"
"v_accvgpr_write_b32 a50, v250\n\t"
"v_mfma_f32_16x16x32_bf16 v[174:177], v[198:201], a[180:183], v[174:177]\n\t"
"ds_read_b128 a[180:183], v242 offset:1536\n\t"
"v_accvgpr_write_b32 a51, v251\n\t"
"v_mfma_f32_16x16x32_bf16 v[174:177], a[88:91], a[184:187], v[174:177]\n\t"
"v_fma_f32 v250, -v42, s56, -v138\n\t"
"v_fma_f32 v251, -v43, s56, -v139\n\t"
"v_mfma_f32_16x16x32_bf16 v[174:177], a[96:99], a[188:191], v[174:177]\n\t"
"v_accvgpr_write_b32 a56, v250\n\t"
"v_fma_f32 v250, -v44, s56, -v140\n\t"
"v_accvgpr_write_b32 a57, v251\n\t"
"v_fma_f32 v251, -v45, s56, -v141\n\t"
"v_mfma_f32_16x16x32_bf16 v[178:181], v[198:201], a[184:187], v[178:181]\n\t"
"ds_read_b128 a[184:187], v242 offset:2048\n\t"
"v_accvgpr_write_b32 a58, v250\n\t"
"v_mfma_f32_16x16x32_bf16 v[178:181], a[88:91], a[188:191], v[178:181]\n\t"
"v_accvgpr_write_b32 a59, v251\n\t"
"v_fma_f32 v250, -v46, s56, -v142\n\t"
"v_mfma_f32_16x16x32_bf16 v[178:181], a[96:99], a[192:195], v[178:181]\n\t"
"v_fma_f32 v251, -v47, s56, -v143\n\t"
"v_accvgpr_write_b32 a64, v250\n\t"
"v_fma_f32 v250, -v48, s56, -v144\n\t"
"v_accvgpr_write_b32 a65, v251\n\t"
"v_mfma_f32_16x16x32_bf16 v[182:185], v[198:201], a[188:191], v[182:185]\n\t"
"ds_read_b128 a[188:191], v242 offset:2560\n\t"
"v_fma_f32 v251, -v49, s56, -v145\n\t"
"v_mfma_f32_16x16x32_bf16 v[182:185], a[88:91], a[192:195], v[182:185]\n\t"
"v_accvgpr_write_b32 a66, v250\n\t"
"v_accvgpr_write_b32 a67, v251\n\t"
"v_mfma_f32_16x16x32_bf16 v[182:185], a[96:99], a[196:199], v[182:185]\n\t"
"v_fma_f32 v250, -v50, s56, -v146\n\t"
"v_fma_f32 v251, -v51, s56, -v147\n\t"
"v_accvgpr_write_b32 a72, v250\n\t"
"v_fma_f32 v250, -v52, s56, -v148\n\t"
"v_mfma_f32_16x16x32_bf16 v[186:189], v[198:201], a[192:195], v[186:189]\n\t"
"ds_read_b128 a[192:195], v242 offset:3072\n\t"
"v_accvgpr_write_b32 a73, v251\n\t"
"v_mfma_f32_16x16x32_bf16 v[186:189], a[88:91], a[196:199], v[186:189]\n\t"
"v_fma_f32 v251, -v53, s56, -v149\n\t"
"v_accvgpr_write_b32 a74, v250\n\t"
"v_mfma_f32_16x16x32_bf16 v[186:189], a[96:99], a[200:203], v[186:189]\n\t"
"v_accvgpr_write_b32 a75, v251\n\t"
"v_fma_f32 v250, -v54, s56, -v150\n\t"
"v_fma_f32 v251, -v55, s56, -v151\n\t"
"v_accvgpr_write_b32 a80, v250\n\t"
"v_mfma_f32_16x16x32_bf16 v[190:193], v[198:201], a[196:199], v[190:193]\n\t"
"ds_read_b128 a[196:199], v242 offset:3584\n\t"
"v_fma_f32 v250, -v56, s56, -v152\n\t"
"v_mfma_f32_16x16x32_bf16 v[190:193], a[88:91], a[200:203], v[190:193]\n\t"
"ds_read_b128 a[200:203], v242 offset:4096\n\t"
"v_accvgpr_write_b32 a81, v251\n\t"
"v_mfma_f32_16x16x32_bf16 v[190:193], a[96:99], a[204:207], v[190:193]\n\t"
"s_add_u32 s29, s23, 1152\n\t"
"buffer_store_dwordx4 a[104:107], v247, s[16:19], s29 offen offset:128 nt\n\t"
"ds_read_b128 a[204:207], v242 offset:4608\n\t"
"v_fma_f32 v251, -v57, s56, -v153\n\t"
"s_waitcnt vmcnt(26) lgkmcnt(7)\n\t"
"v_mfma_f32_16x16x32_bf16 v[194:197], a[28:31], a[168:171], v[194:197]\n\t"
"v_accvgpr_write_b32 a82, v250\n\t"
"v_accvgpr_write_b32 a83, v251\n\t"
"v_mfma_f32_16x16x32_bf16 v[194:197], v[206:209], a[172:175], v[194:197]\n\t"
"v_fma_f32 v250, -v58, s56, -v154\n\t"
"v_fma_f32 v251, -v59, s56, -v155\n\t"
"v_mfma_f32_16x16x32_bf16 v[194:197], a[208:211], a[176:179], v[194:197]\n\t"
"v_accvgpr_write_b32 a88, v250\n\t"
"v_fma_f32 v250, -v60, s56, -v156\n\t"
"v_accvgpr_write_b32 a89, v251\n\t"
"v_fma_f32 v251, -v61, s56, -v157\n\t"
"s_waitcnt lgkmcnt(6)\n\t"
"v_mfma_f32_16x16x32_bf16 v[202:205], a[28:31], a[172:175], v[202:205]\n\t"
"v_accvgpr_write_b32 a90, v250\n\t"
"v_accvgpr_write_b32 a91, v251\n\t"
"v_mfma_f32_16x16x32_bf16 v[202:205], v[206:209], a[176:179], v[202:205]\n\t"
"v_fma_f32 v250, -v62, s56, -v158\n\t"
"v_fma_f32 v251, -v63, s56, -v159\n\t"
"v_mfma_f32_16x16x32_bf16 v[202:205], a[208:211], a[180:183], v[202:205]\n\t"
"v_accvgpr_write_b32 a96, v250\n\t"
"v_fma_f32 v250, -v64, s56, -v160\n\t"
"v_accvgpr_write_b32 a97, v251\n\t"
"v_fma_f32 v251, -v65, s56, -v161\n\t"
"s_waitcnt lgkmcnt(5)\n\t"
"v_mfma_f32_16x16x32_bf16 v[210:213], a[28:31], a[176:179], v[210:213]\n\t"
"v_accvgpr_write_b32 a98, v250\n\t"
"v_accvgpr_write_b32 a99, v251\n\t"
"v_mfma_f32_16x16x32_bf16 v[210:213], v[206:209], a[180:183], v[210:213]\n\t"
"v_fma_f32 v250, -v66, s56, -v162\n\t"
"v_fma_f32 v251, -v67, s56, -v163\n\t"
"v_mfma_f32_16x16x32_bf16 v[210:213], a[208:211], a[184:187], v[210:213]\n\t"
"v_accvgpr_write_b32 a104, v250\n\t"
"v_fma_f32 v250, -v68, s56, -v164\n\t"
"v_accvgpr_write_b32 a105, v251\n\t"
"v_fma_f32 v251, -v69, s56, -v165\n\t"
"s_waitcnt lgkmcnt(4)\n\t"
"v_mfma_f32_16x16x32_bf16 a[0:3], a[28:31], a[180:183], a[0:3]\n\t"
"v_accvgpr_write_b32 a106, v250\n\t"
"v_accvgpr_write_b32 a107, v251\n\t"
"v_mfma_f32_16x16x32_bf16 a[0:3], v[206:209], a[184:187], a[0:3]\n\t"
"v_fma_f32 v250, -v70, s56, -v166\n\t"
"v_fma_f32 v251, -v71, s56, -v167\n\t"
"v_mfma_f32_16x16x32_bf16 a[0:3], a[208:211], a[188:191], a[0:3]\n\t"
"v_accvgpr_write_b32 a112, v250\n\t"
"v_fma_f32 v250, -v72, s56, -v168\n\t"
"v_accvgpr_write_b32 a113, v251\n\t"
"v_fma_f32 v251, -v73, s56, -v169\n\t"
"s_waitcnt lgkmcnt(3)\n\t"
"v_mfma_f32_16x16x32_bf16 a[8:11], a[28:31], a[184:187], a[8:11]\n\t"
"v_xor_b32 v244, 0xc0, v244\n\t"
"v_accvgpr_write_b32 a114, v250\n\t"
"v_mfma_f32_16x16x32_bf16 a[8:11], v[206:209], a[188:191], a[8:11]\n\t"
"v_accvgpr_write_b32 a115, v251\n\t"
"v_fma_f32 v250, -v74, s56, -v170\n\t"
"v_mfma_f32_16x16x32_bf16 a[8:11], a[208:211], a[192:195], a[8:11]\n\t"
"s_add_u32 s29, s23, 1152\n\t"
"buffer_store_dwordx4 a[144:147], v247, s[16:19], s29 offen offset:256 nt\n\t"
"v_fma_f32 v251, -v75, s56, -v171\n\t"
"v_accvgpr_write_b32 a120, v250\n\t"
"s_waitcnt lgkmcnt(2)\n\t"
"v_mfma_f32_16x16x32_bf16 a[16:19], a[28:31], a[188:191], a[16:19]\n\t"
"v_fma_f32 v250, -v76, s56, -v172\n\t"
"v_accvgpr_write_b32 a121, v251\n\t"
"v_mfma_f32_16x16x32_bf16 a[16:19], v[206:209], a[192:195], a[16:19]\n\t"
"v_fma_f32 v251, -v77, s56, -v173\n\t"
"v_accvgpr_write_b32 a122, v250\n\t"
"v_mfma_f32_16x16x32_bf16 a[16:19], a[208:211], a[196:199], a[16:19]\n\t"
"v_accvgpr_write_b32 a123, v251\n\t"
"v_fma_f32 v250, -v78, s56, -v174\n\t"
"v_fma_f32 v251, -v79, s56, -v175\n\t"
"v_accvgpr_write_b32 a128, v250\n\t"
"s_waitcnt lgkmcnt(1)\n\t"
"v_mfma_f32_16x16x32_bf16 a[24:27], a[28:31], a[192:195], a[24:27]\n\t"
"v_fma_f32 v250, -v80, s56, -v176\n\t"
"v_accvgpr_write_b32 a129, v251\n\t"
"v_mfma_f32_16x16x32_bf16 a[24:27], v[206:209], a[196:199], a[24:27]\n\t"
"v_fma_f32 v251, -v81, s56, -v177\n\t"
"v_accvgpr_write_b32 a130, v250\n\t"
"v_mfma_f32_16x16x32_bf16 a[24:27], a[208:211], a[200:203], a[24:27]\n\t"
"v_accvgpr_write_b32 a131, v251\n\t"
"v_fma_f32 v250, -v82, s56, -v178\n\t"
"v_fma_f32 v251, -v83, s56, -v179\n\t"
"v_accvgpr_write_b32 a136, v250\n\t"
"s_waitcnt lgkmcnt(0)\n\t"
"v_mfma_f32_16x16x32_bf16 a[32:35], a[28:31], a[196:199], a[32:35]\n\t"
"v_fma_f32 v250, -v84, s56, -v180\n\t"
"v_accvgpr_write_b32 a137, v251\n\t"
"v_mfma_f32_16x16x32_bf16 a[32:35], v[206:209], a[200:203], a[32:35]\n\t"
"v_fma_f32 v251, -v85, s56, -v181\n\t"
"v_accvgpr_write_b32 a138, v250\n\t"
"v_mfma_f32_16x16x32_bf16 a[32:35], a[208:211], a[204:207], a[32:35]\n\t"
"buffer_load_dwordx4 a[208:211], v252, s[8:11], s20 offen offset:2048 sc1\n\t"
"v_accvgpr_write_b32 a139, v251\n\t"
"v_fma_f32 v250, -v86, s56, -v182\n\t"
"s_waitcnt vmcnt(22)\n\t"
"v_mfma_f32_16x16x32_bf16 a[40:43], a[212:215], a[168:171], a[40:43]\n\t"
"v_fma_f32 v251, -v87, s56, -v183\n\t"
"v_accvgpr_write_b32 a144, v250\n\t"
"v_mfma_f32_16x16x32_bf16 a[40:43], a[216:219], a[172:175], a[40:43]\n\t"
"v_fma_f32 v250, -v88, s56, -v184\n\t"
"v_accvgpr_write_b32 a145, v251\n\t"
"v_mfma_f32_16x16x32_bf16 a[40:43], a[220:223], a[176:179], a[40:43]\n\t"
"ds_read_b64_tr_b16 v[234:235], v244 offset:0\n\t"
"ds_read_b64_tr_b16 v[236:237], v244 offset:3072\n\t"
"v_fma_f32 v251, -v89, s56, -v185\n\t"
"v_accvgpr_write_b32 a146, v250\n\t"
"v_mfma_f32_16x16x32_bf16 a[48:51], a[212:215], a[172:175], a[48:51]\n\t"
"v_accvgpr_write_b32 a147, v251\n\t"
"v_fma_f32 v250, -v90, s56, -v186\n\t"
"v_mfma_f32_16x16x32_bf16 a[48:51], a[216:219], a[176:179], a[48:51]\n\t"
"v_fma_f32 v251, -v91, s56, -v187\n\t"
"v_accvgpr_write_b32 a152, v250\n\t"
"v_mfma_f32_16x16x32_bf16 a[48:51], a[220:223], a[180:183], a[48:51]\n\t"
"s_add_u32 s29, s23, 394368\n\t"
"buffer_store_dwordx4 v[214:217], v247, s[16:19], s29 offen offset:0 nt\n\t"
"v_fma_f32 v250, -v92, s56, -v188\n\t"
"v_accvgpr_write_b32 a153, v251\n\t"
"v_mfma_f32_16x16x32_bf16 a[56:59], a[212:215], a[176:179], a[56:59]\n\t"
"v_fma_f32 v251, -v93, s56, -v189\n\t"
"v_accvgpr_write_b32 a154, v250\n\t"
"v_mfma_f32_16x16x32_bf16 a[56:59], a[216:219], a[180:183], a[56:59]\n\t"
"v_accvgpr_write_b32 a155, v251\n\t"
"v_fma_f32 v250, -v94, s56, -v190\n\t"
"v_mfma_f32_16x16x32_bf16 a[56:59], a[220:223], a[184:187], a[56:59]\n\t"
"v_fma_f32 v251, -v95, s56, -v191\n\t"
"v_accvgpr_write_b32 a160, v250\n\t"
"v_fma_f32 v250, -v96, s56, -v192\n\t"
"v_accvgpr_write_b32 a161, v251\n\t"
"v_mfma_f32_16x16x32_bf16 a[64:67], a[212:215], a[180:183], a[64:67]\n\t"
"ds_read_b64_tr_b16 v[222:223], v244 offset:6144\n\t"
"v_fma_f32 v251, -v97, s56, -v193\n\t"
"v_mfma_f32_16x16x32_bf16 a[64:67], a[216:219], a[184:187], a[64:67]\n\t"
"ds_read_b64_tr_b16 v[224:225], v244 offset:9216\n\t"
"v_accvgpr_write_b32 a162, v250\n\t"
"v_mfma_f32_16x16x32_bf16 a[64:67], a[220:223], a[188:191], a[64:67]\n\t"
"v_accvgpr_write_b32 a163, v251\n\t"
"v_fma_f32 v198, v98, s64, -v2\n\t"
"v_fma_f32 v199, v99, s64, -v3\n\t"
"v_fma_f32 v200, v100, s64, -v4\n\t"
"s_barrier\n\t"
"v_mfma_f32_16x16x32_bf16 a[72:75], a[212:215], a[184:187], a[72:75]\n\t"
"v_fma_f32 v201, v101, s64, -v5\n\t"
"v_fma_f32 v206, v102, s64, -v6\n\t"
"v_mfma_f32_16x16x32_bf16 a[72:75], a[216:219], a[188:191], a[72:75]\n\t"
"v_fma_f32 v207, v103, s64, -v7\n\t"
"v_fma_f32 v208, v104, s64, -v8\n\t"
"v_mfma_f32_16x16x32_bf16 a[72:75], a[220:223], a[192:195], a[72:75]\n\t"
"v_fma_f32 v209, v105, s64, -v9\n\t"
"v_fma_f32 v214, v106, s64, -v10\n\t"
"v_fma_f32 v215, v107, s64, -v11\n\t"
"v_fma_f32 v216, v108, s64, -v12\n\t"
"v_mfma_f32_16x16x32_bf16 a[80:83], a[212:215], a[188:191], a[80:83]\n\t"
"v_fma_f32 v217, v109, s64, -v13\n\t"
"v_fma_f32 v250, v110, s64, -v14\n\t"
"v_mfma_f32_16x16x32_bf16 a[80:83], a[216:219], a[192:195], a[80:83]\n\t"
"v_fma_f32 v251, v111, s64, -v15\n\t"
"v_accvgpr_write_b32 a4, v250\n\t"
"v_mfma_f32_16x16x32_bf16 a[80:83], a[220:223], a[196:199], a[80:83]\n\t"
"v_fma_f32 v250, v112, s64, -v16\n\t"
"v_accvgpr_write_b32 a5, v251\n\t"
"v_fma_f32 v251, v113, s64, -v17\n\t"
"v_accvgpr_write_b32 a6, v250\n\t"
"s_waitcnt lgkmcnt(2)\n\t"
"v_mfma_f32_16x16x32_bf16 v[234:237], v[234:237], v[218:221], 0\n\t"
"v_accvgpr_write_b32 a7, v251\n\t"
"v_fma_f32 v250, v114, s64, -v18\n\t"
"v_fma_f32 v251, v115, s64, -v19\n\t"
"v_accvgpr_write_b32 a12, v250\n\t"
"v_mfma_f32_16x16x32_bf16 a[88:91], a[212:215], a[192:195], a[88:91]\n\t"
"ds_read_b64_tr_b16 v[226:227], v244 offset:12288\n\t"
"v_fma_f32 v250, v116, s64, -v20\n\t"
"v_mfma_f32_16x16x32_bf16 a[88:91], a[216:219], a[196:199], a[88:91]\n\t"
"ds_read_b64_tr_b16 v[228:229], v244 offset:15360\n\t"
"v_accvgpr_write_b32 a13, v251\n\t"
"v_mfma_f32_16x16x32_bf16 a[88:91], a[220:223], a[200:203], a[88:91]\n\t"
"s_add_u32 s29, s23, 394368\n\t"
"buffer_store_dwordx4 a[36:39], v247, s[16:19], s29 offen offset:128 nt\n\t"
"v_fma_f32 v251, v117, s64, -v21\n\t"
"v_accvgpr_write_b32 a14, v250\n\t"
"v_mfma_f32_16x16x32_bf16 a[96:99], a[212:215], a[196:199], a[96:99]\n\t"
"v_accvgpr_write_b32 a15, v251\n\t"
"v_fma_f32 v250, v118, s64, -v22\n\t"
"v_mfma_f32_16x16x32_bf16 a[96:99], a[216:219], a[200:203], a[96:99]\n\t"
"v_cvt_pk_bf16_f32 v234, v234, v235\n\t"
"v_cvt_pk_bf16_f32 v235, v236, v237\n\t"
"v_mfma_f32_16x16x32_bf16 a[96:99], a[220:223], a[204:207], a[96:99]\n\t"
"s_mov_b32 s20, 36864\n\t"
"buffer_load_dwordx4 a[212:215], v252, s[8:11], s20 offen offset:0 sc1\n\t"
"v_fma_f32 v251, v119, s64, -v23\n\t"
"v_accvgpr_write_b32 a20, v250\n\t"
"s_waitcnt lgkmcnt(2)\n\t"
"v_mfma_f32_16x16x32_bf16 v[222:225], v[222:225], v[218:221], 0\n\t"
"buffer_load_dwordx4 a[216:219], v252, s[8:11], s20 offen offset:1024 sc1\n\t"
"v_fma_f32 v250, v120, s64, -v24\n\t"
"v_accvgpr_write_b32 a21, v251\n\t"
"s_waitcnt vmcnt(22)\n\t"
"v_mfma_f32_16x16x32_bf16 a[104:107], a[108:111], a[168:171], a[104:107]\n\t"
"ds_read_b64_tr_b16 v[230:231], v244 offset:19968\n\t"
"v_fma_f32 v251, v121, s64, -v25\n\t"
"v_mfma_f32_16x16x32_bf16 a[104:107], a[116:119], a[172:175], a[104:107]\n\t"
"ds_read_b64_tr_b16 v[232:233], v244 offset:23040\n\t"
"v_accvgpr_write_b32 a22, v250\n\t"
"v_mfma_f32_16x16x32_bf16 a[104:107], a[124:127], a[176:179], a[104:107]\n\t"
"buffer_load_dwordx4 a[220:223], v252, s[8:11], s20 offen offset:2048 sc1\n\t"
"ds_read_b128 a[168:171], v242 offset:9216\n\t"
"v_accvgpr_write_b32 a23, v251\n\t"
"v_mfma_f32_16x16x32_bf16 a[112:115], a[108:111], a[172:175], a[112:115]\n\t"
"ds_read_b128 a[172:175], v242 offset:9728\n\t"
"v_fma_f32 v250, v122, s64, -v26\n\t"
"v_mfma_f32_16x16x32_bf16 a[112:115], a[116:119], a[176:179], a[112:115]\n\t"
"v_cvt_pk_bf16_f32 v222, v222, v223\n\t"
"v_cvt_pk_bf16_f32 v223, v224, v225\n\t"
"v_mfma_f32_16x16x32_bf16 a[112:115], a[124:127], a[180:183], a[112:115]\n\t"
"v_fma_f32 v251, v123, s64, -v27\n\t"
"v_accvgpr_write_b32 a28, v250\n\t"
"v_fma_f32 v250, v124, s64, -v28\n\t"
"v_accvgpr_write_b32 a29, v251\n\t"
"v_mfma_f32_16x16x32_bf16 a[120:123], a[108:111], a[176:179], a[120:123]\n\t"
"ds_read_b128 a[176:179], v242 offset:10240\n\t"
"v_fma_f32 v251, v125, s64, -v29\n\t"
"v_mfma_f32_16x16x32_bf16 a[120:123], a[116:119], a[180:183], a[120:123]\n\t"
"v_accvgpr_write_b32 a30, v250\n\t"
"v_accvgpr_write_b32 a31, v251\n\t"
"v_mfma_f32_16x16x32_bf16 a[120:123], a[124:127], a[184:187], a[120:123]\n\t"
"v_fma_f32 v250, v126, s64, -v30\n\t"
"v_fma_f32 v251, v127, s64, -v31\n\t"
"v_accvgpr_write_b32 a36, v250\n\t"
"v_fma_f32 v250, v128, s64, -v32\n\t"
"s_waitcnt lgkmcnt(5)\n\t"
"v_mfma_f32_16x16x32_bf16 v[226:229], v[226:229], v[218:221], 0\n\t"
"v_accvgpr_write_b32 a37, v251\n\t"
"v_fma_f32 v251, v129, s64, -v33\n\t"
"v_accvgpr_write_b32 a38, v250\n\t"
"v_accvgpr_write_b32 a39, v251\n\t"
"v_mfma_f32_16x16x32_bf16 a[128:131], a[108:111], a[180:183], a[128:131]\n\t"
"ds_read_b128 a[180:183], v242 offset:10752\n\t"
"v_fma_f32 v250, v130, s64, -v34\n\t"
"v_mfma_f32_16x16x32_bf16 a[128:131], a[116:119], a[184:187], a[128:131]\n\t"
"v_fma_f32 v251, v131, s64, -v35\n\t"
"v_accvgpr_write_b32 a44, v250\n\t"
"v_mfma_f32_16x16x32_bf16 a[128:131], a[124:127], a[188:191], a[128:131]\n\t"
"s_add_u32 s29, s23, 394368\n\t"
"buffer_store_dwordx4 a[68:71], v247, s[16:19], s29 offen offset:256 nt\n\t"
"v_fma_f32 v250, v132, s64, -v36\n\t"
"v_accvgpr_write_b32 a45, v251\n\t"
"v_mfma_f32_16x16x32_bf16 a[136:139], a[108:111], a[184:187], a[136:139]\n\t"
"ds_read_b128 a[184:187], v242 offset:11264\n\t"
"v_fma_f32 v251, v133, s64, -v37\n\t"
"v_mfma_f32_16x16x32_bf16 a[136:139], a[116:119], a[188:191], a[136:139]\n\t"
"v_cvt_pk_bf16_f32 v226, v226, v227\n\t"
"v_cvt_pk_bf16_f32 v227, v228, v229\n\t"
"v_mfma_f32_16x16x32_bf16 a[136:139], a[124:127], a[192:195], a[136:139]\n\t"
"v_accvgpr_write_b32 a46, v250\n\t"
"v_accvgpr_write_b32 a47, v251\n\t"
"v_fma_f32 v250, v134, s64, -v38\n\t"
"v_fma_f32 v251, v135, s64, -v39\n\t"
"s_waitcnt lgkmcnt(5)\n\t"
"v_mfma_f32_16x16x32_bf16 v[230:233], v[230:233], v[218:221], 0\n\t"
"v_accvgpr_write_b32 a52, v250\n\t"
"v_fma_f32 v250, v136, s64, -v40\n\t"
"v_accvgpr_write_b32 a53, v251\n\t"
"v_fma_f32 v251, v137, s64, -v41\n\t"
"v_mfma_f32_16x16x32_bf16 a[144:147], a[108:111], a[188:191], a[144:147]\n\t"
"ds_read_b128 a[188:191], v242 offset:11776\n\t"
"v_accvgpr_write_b32 a54, v250\n\t"
"v_mfma_f32_16x16x32_bf16 a[144:147], a[116:119], a[192:195], a[144:147]\n\t"
"v_accvgpr_write_b32 a55, v251\n\t"
"v_fma_f32 v250, v138, s64, -v42\n\t"
"v_mfma_f32_16x16x32_bf16 a[144:147], a[124:127], a[196:199], a[144:147]\n\t"
"v_fma_f32 v251, v139, s64, -v43\n\t"
"v_accvgpr_write_b32 a60, v250\n\t"
"v_fma_f32 v250, v140, s64, -v44\n\t"
"v_accvgpr_write_b32 a61, v251\n\t"
"v_mfma_f32_16x16x32_bf16 a[152:155], a[108:111], a[192:195], a[152:155]\n\t"
"ds_read_b128 a[192:195], v242 offset:12288\n\t"
"v_fma_f32 v251, v141, s64, -v45\n\t"
"v_mfma_f32_16x16x32_bf16 a[152:155], a[116:119], a[196:199], a[152:155]\n\t"
"v_cvt_pk_bf16_f32 v230, v230, v231\n\t"
"v_cvt_pk_bf16_f32 v231, v232, v233\n\t"
"v_mfma_f32_16x16x32_bf16 a[152:155], a[124:127], a[200:203], a[152:155]\n\t"
"v_accvgpr_write_b32 a62, v250\n\t"
"v_accvgpr_write_b32 a63, v251\n\t"
"v_fma_f32 v250, v142, s64, -v46\n\t"
"v_fma_f32 v251, v143, s64, -v47\n\t"
"v_mfma_f32_16x16x32_bf16 a[160:163], a[108:111], a[196:199], a[160:163]\n\t"
"ds_read_b128 a[196:199], v242 offset:12800\n\t"
"v_accvgpr_write_b32 a68, v250\n\t"
"v_mfma_f32_16x16x32_bf16 a[160:163], a[116:119], a[200:203], a[160:163]\n\t"
"ds_read_b128 a[200:203], v242 offset:13312\n\t"
"v_fma_f32 v250, v144, s64, -v48\n\t"
"v_mfma_f32_16x16x32_bf16 a[160:163], a[124:127], a[204:207], a[160:163]\n\t"
"ds_read_b128 a[204:207], v242 offset:13824\n\t"
"v_accvgpr_write_b32 a69, v251\n\t"
"v_fma_f32 v251, v145, s64, -v49\n\t"
"v_accvgpr_write_b32 a70, v250\n\t"
"s_waitcnt vmcnt(19) lgkmcnt(7)\n\t"
"v_mfma_f32_16x16x32_bf16 v[198:201], a[224:227], a[168:171], v[198:201]\n\t"
"v_accvgpr_write_b32 a71, v251\n\t"
"v_fma_f32 v250, v146, s64, -v50\n\t"
"v_mfma_f32_16x16x32_bf16 v[198:201], a[228:231], a[172:175], v[198:201]\n\t"
"v_fma_f32 v251, v147, s64, -v51\n\t"
"v_accvgpr_write_b32 a76, v250\n\t"
"v_mfma_f32_16x16x32_bf16 v[198:201], a[232:235], a[176:179], v[198:201]\n\t"
"v_fma_f32 v250, v148, s64, -v52\n\t"
"v_accvgpr_write_b32 a77, v251\n\t"
"v_fma_f32 v251, v149, s64, -v53\n\t"
"v_accvgpr_write_b32 a78, v250\n\t"
"s_waitcnt lgkmcnt(6)\n\t"
"v_mfma_f32_16x16x32_bf16 v[206:209], a[224:227], a[172:175], v[206:209]\n\t"
"v_accvgpr_write_b32 a79, v251\n\t"
"v_fma_f32 v250, v150, s64, -v54\n\t"
"v_mfma_f32_16x16x32_bf16 v[206:209], a[228:231], a[176:179], v[206:209]\n\t"
"v_fma_f32 v251, v151, s64, -v55\n\t"
"v_accvgpr_write_b32 a84, v250\n\t"
"v_mfma_f32_16x16x32_bf16 v[206:209], a[232:235], a[180:183], v[206:209]\n\t"
"v_fma_f32 v250, v152, s64, -v56\n\t"
"v_accvgpr_write_b32 a85, v251\n\t"
"v_fma_f32 v251, v153, s64, -v57\n\t"
"v_accvgpr_write_b32 a86, v250\n\t"
"s_waitcnt lgkmcnt(0)\n\t"
"s_barrier\n\t"
"v_mfma_f32_16x16x32_bf16 v[214:217], a[224:227], a[176:179], v[214:217]\n\t"
"v_accvgpr_write_b32 a87, v251\n\t"
"v_fma_f32 v250, v154, s64, -v58\n\t"
"v_mfma_f32_16x16x32_bf16 v[214:217], a[228:231], a[180:183], v[214:217]\n\t"
"v_fma_f32 v251, v155, s64, -v59\n\t"
"v_accvgpr_write_b32 a92, v250\n\t"
"v_mfma_f32_16x16x32_bf16 v[214:217], a[232:235], a[184:187], v[214:217]\n\t"
"ds_write_b64 v243, v[234:235] offset:0\n\t"
"ds_write_b64 v243, v[222:223] offset:512\n\t"
"v_mfma_f32_16x16x32_bf16 a[4:7], a[224:227], a[180:183], a[4:7]\n\t"
"ds_read_b64_tr_b16 v[234:235], v244 offset:26112\n\t"
"v_fma_f32 v250, v156, s64, -v60\n\t"
"v_mfma_f32_16x16x32_bf16 a[4:7], a[228:231], a[184:187], a[4:7]\n\t"
"ds_read_b64_tr_b16 v[236:237], v244 offset:29184\n\t"
"v_accvgpr_write_b32 a93, v251\n\t"
"v_mfma_f32_16x16x32_bf16 a[4:7], a[232:235], a[188:191], a[4:7]\n\t"
"ds_write_b64 v243, v[226:227] offset:1024\n\t"
"ds_write_b64 v243, v[230:231] offset:1536\n\t"
"v_mfma_f32_16x16x32_bf16 a[12:15], a[224:227], a[184:187], a[12:15]\n\t"
"ds_read_b64_tr_b16 v[222:223], v244 offset:32256\n\t"
"v_fma_f32 v251, v157, s64, -v61\n\t"
"v_mfma_f32_16x16x32_bf16 a[12:15], a[228:231], a[188:191], a[12:15]\n\t"
"ds_read_b64_tr_b16 v[224:225], v244 offset:35328\n\t"
"v_accvgpr_write_b32 a94, v250\n\t"
"v_mfma_f32_16x16x32_bf16 a[12:15], a[232:235], a[192:195], a[12:15]\n\t"
"ds_read_b64_tr_b16 v[226:227], v244 offset:39936\n\t"
"ds_read_b64_tr_b16 v[228:229], v244 offset:43008\n\t"
"ds_read_b64_tr_b16 v[230:231], v244 offset:46080\n\t"
"v_accvgpr_write_b32 a95, v251\n\t"
"v_mfma_f32_16x16x32_bf16 a[20:23], a[224:227], a[188:191], a[20:23]\n\t"
"ds_read_b64_tr_b16 v[232:233], v244 offset:49152\n\t"
"v_fma_f32 v250, v158, s64, -v62\n\t"
"v_mfma_f32_16x16x32_bf16 a[20:23], a[228:231], a[192:195], a[20:23]\n\t"
"v_fma_f32 v251, v159, s64, -v63\n\t"
"v_accvgpr_write_b32 a100, v250\n\t"
"v_mfma_f32_16x16x32_bf16 a[20:23], a[232:235], a[196:199], a[20:23]\n\t"
"v_fma_f32 v250, v160, s64, -v64\n\t"
"v_accvgpr_write_b32 a101, v251\n\t"
"v_fma_f32 v251, v161, s64, -v65\n\t"
"v_accvgpr_write_b32 a102, v250\n\t"
"s_waitcnt lgkmcnt(8)\n\t"
"v_mfma_f32_16x16x32_bf16 v[234:237], v[234:237], v[218:221], 0\n\t"
"v_accvgpr_write_b32 a103, v251\n\t"
"v_fma_f32 v250, v162, s64, -v66\n\t"
"v_fma_f32 v251, v163, s64, -v67\n\t"
"v_accvgpr_write_b32 a108, v250\n\t"
"v_mfma_f32_16x16x32_bf16 a[28:31], a[224:227], a[192:195], a[28:31]\n\t"
"v_fma_f32 v250, v164, s64, -v68\n\t"
"v_accvgpr_write_b32 a109, v251\n\t"
"v_mfma_f32_16x16x32_bf16 a[28:31], a[228:231], a[196:199], a[28:31]\n\t"
"v_fma_f32 v251, v165, s64, -v69\n\t"
"v_accvgpr_write_b32 a110, v250\n\t"
"v_mfma_f32_16x16x32_bf16 a[28:31], a[232:235], a[200:203], a[28:31]\n\t"
"v_accvgpr_write_b32 a111, v251\n\t"
"v_fma_f32 v250, v166, s64, -v70\n\t"
"v_fma_f32 v251, v167, s64, -v71\n\t"
"v_accvgpr_write_b32 a116, v250\n\t"
"s_waitcnt lgkmcnt(4)\n\t"
"v_mfma_f32_16x16x32_bf16 v[222:225], v[222:225], v[218:221], 0\n\t"
"v_fma_f32 v250, v168, s64, -v72\n\t"
"v_accvgpr_write_b32 a117, v251\n\t"
"v_fma_f32 v251, v169, s64, -v73\n\t"
"v_accvgpr_write_b32 a118, v250\n\t"
"v_mfma_f32_16x16x32_bf16 a[36:39], a[224:227], a[196:199], a[36:39]\n\t"
"v_cvt_pk_bf16_f32 v234, v234, v235\n\t"
"v_cvt_pk_bf16_f32 v235, v236, v237\n\t"
"v_mfma_f32_16x16x32_bf16 a[36:39], a[228:231], a[200:203], a[36:39]\n\t"
"v_accvgpr_write_b32 a119, v251\n\t"
"v_fma_f32 v250, v170, s64, -v74\n\t"
"v_mfma_f32_16x16x32_bf16 a[36:39], a[232:235], a[204:207], a[36:39]\n\t"
"s_mov_b32 s20, 49152\n\t"
"buffer_load_dwordx4 a[224:227], v252, s[8:11], s20 offen offset:0 sc1\n\t"
"ds_write_b64 v243, v[234:235] offset:2048\n\t"
"s_waitcnt lgkmcnt(3)\n\t"
"v_mfma_f32_16x16x32_bf16 v[226:229], v[226:229], v[218:221], 0\n\t"
"buffer_load_dwordx4 a[228:231], v252, s[8:11], s20 offen offset:1024 sc1\n\t"
"ds_read_b64_tr_b16 v[234:235], v244 offset:52224\n\t"
"ds_read_b64_tr_b16 v[236:237], v244 offset:55296\n\t"
"s_waitcnt vmcnt(16)\n\t"
"v_mfma_f32_16x16x32_bf16 a[44:47], a[236:239], a[168:171], a[44:47]\n\t"
"v_cvt_pk_bf16_f32 v222, v222, v223\n\t"
"v_cvt_pk_bf16_f32 v223, v224, v225\n\t"
"v_mfma_f32_16x16x32_bf16 a[44:47], a[240:243], a[172:175], a[44:47]\n\t"
"v_fma_f32 v251, v171, s64, -v75\n\t"
"v_accvgpr_write_b32 a124, v250\n\t"
"v_mfma_f32_16x16x32_bf16 a[44:47], a[244:247], a[176:179], a[44:47]\n\t"
"buffer_load_dwordx4 a[232:235], v252, s[8:11], s20 offen offset:2048 sc1\n\t"
"ds_write_b64 v243, v[222:223] offset:2560\n\t"
"s_waitcnt lgkmcnt(4)\n\t"
"v_mfma_f32_16x16x32_bf16 v[230:233], v[230:233], v[218:221], 0\n\t"
"v_fma_f32 v250, v172, s64, -v76\n\t"
"v_accvgpr_write_b32 a125, v251\n\t"
"v_fma_f32 v251, v173, s64, -v77\n\t"
"v_accvgpr_write_b32 a126, v250\n\t"
"v_mfma_f32_16x16x32_bf16 a[52:55], a[236:239], a[172:175], a[52:55]\n\t"
"v_cvt_pk_bf16_f32 v226, v226, v227\n\t"
"v_cvt_pk_bf16_f32 v227, v228, v229\n\t"
"v_mfma_f32_16x16x32_bf16 a[52:55], a[240:243], a[176:179], a[52:55]\n\t"
"v_accvgpr_write_b32 a127, v251\n\t"
"v_fma_f32 v250, v174, s64, -v78\n\t"
"v_mfma_f32_16x16x32_bf16 a[52:55], a[244:247], a[180:183], a[52:55]\n\t"
"ds_write_b64 v243, v[226:227] offset:3072\n\t"
"v_fma_f32 v251, v175, s64, -v79\n\t"
"v_accvgpr_write_b32 a132, v250\n\t"
"v_mfma_f32_16x16x32_bf16 a[60:63], a[236:239], a[176:179], a[60:63]\n\t"
"v_fma_f32 v250, v176, s64, -v80\n\t"
"v_accvgpr_write_b32 a133, v251\n\t"
"v_mfma_f32_16x16x32_bf16 a[60:63], a[240:243], a[180:183], a[60:63]\n\t"
"v_cvt_pk_bf16_f32 v230, v230, v231\n\t"
"v_cvt_pk_bf16_f32 v231, v232, v233\n\t"
"v_mfma_f32_16x16x32_bf16 a[60:63], a[244:247], a[184:187], a[60:63]\n\t"
"ds_write_b64 v243, v[230:231] offset:3584\n\t"
"v_fma_f32 v251, v177, s64, -v81\n\t"
"v_accvgpr_write_b32 a134, v250\n\t"
"v_mfma_f32_16x16x32_bf16 a[68:71], a[236:239], a[180:183], a[68:71]\n\t"
"v_accvgpr_write_b32 a135, v251\n\t"
"v_fma_f32 v250, v178, s64, -v82\n\t"
"v_mfma_f32_16x16x32_bf16 a[68:71], a[240:243], a[184:187], a[68:71]\n\t"
"v_fma_f32 v251, v179, s64, -v83\n\t"
"v_accvgpr_write_b32 a140, v250\n\t"
"v_mfma_f32_16x16x32_bf16 a[68:71], a[244:247], a[188:191], a[68:71]\n\t"
"v_fma_f32 v250, v180, s64, -v84\n\t"
"v_accvgpr_write_b32 a141, v251\n\t"
"v_fma_f32 v251, v181, s64, -v85\n\t"
"v_accvgpr_write_b32 a142, v250\n\t"
"s_waitcnt lgkmcnt(3)\n\t"
"v_mfma_f32_16x16x32_bf16 v[234:237], v[234:237], v[218:221], 0\n\t"
"v_accvgpr_write_b32 a143, v251\n\t"
"v_fma_f32 v250, v182, s64, -v86\n\t"
"v_fma_f32 v251, v183, s64, -v87\n\t"
"v_accvgpr_write_b32 a148, v250\n\t"
"v_mfma_f32_16x16x32_bf16 a[76:79], a[236:239], a[184:187], a[76:79]\n\t"
"v_fma_f32 v250, v184, s64, -v88\n\t"
"v_accvgpr_write_b32 a149, v251\n\t"
"v_mfma_f32_16x16x32_bf16 a[76:79], a[240:243], a[188:191], a[76:79]\n\t"
"v_fma_f32 v251, v185, s64, -v89\n\t"
"v_accvgpr_write_b32 a150, v250\n\t"
"v_mfma_f32_16x16x32_bf16 a[76:79], a[244:247], a[192:195], a[76:79]\n\t"
"v_accvgpr_write_b32 a151, v251\n\t"
"v_fma_f32 v250, v186, s64, -v90\n\t"
"v_fma_f32 v251, v187, s64, -v91\n\t"
"v_accvgpr_write_b32 a156, v250\n\t"
"v_mfma_f32_16x16x32_bf16 a[84:87], a[236:239], a[188:191], a[84:87]\n\t"
"v_fma_f32 v250, v188, s64, -v92\n\t"
"v_accvgpr_write_b32 a157, v251\n\t"
"v_mfma_f32_16x16x32_bf16 a[84:87], a[240:243], a[192:195], a[84:87]\n\t"
"v_cvt_pk_bf16_f32 v234, v234, v235\n\t"
"v_cvt_pk_bf16_f32 v235, v236, v237\n\t"
"v_mfma_f32_16x16x32_bf16 a[84:87], a[244:247], a[196:199], a[84:87]\n\t"
"ds_write_b64 v243, v[234:235] offset:4096\n\t"
"v_fma_f32 v251, v189, s64, -v93\n\t"
"v_accvgpr_write_b32 a158, v250\n\t"
"v_mfma_f32_16x16x32_bf16 a[92:95], a[236:239], a[192:195], a[92:95]\n\t"
"v_accvgpr_write_b32 a159, v251\n\t"
"v_fma_f32 v250, v190, s64, -v94\n\t"
"v_mfma_f32_16x16x32_bf16 a[92:95], a[240:243], a[196:199], a[92:95]\n\t"
"v_fma_f32 v251, v191, s64, -v95\n\t"
"v_accvgpr_write_b32 a164, v250\n\t"
"v_mfma_f32_16x16x32_bf16 a[92:95], a[244:247], a[200:203], a[92:95]\n\t"
"v_fma_f32 v250, v192, s64, -v96\n\t"
"v_accvgpr_write_b32 a165, v251\n\t"
"v_fma_f32 v251, v193, s64, -v97\n\t"
"v_accvgpr_write_b32 a166, v250\n\t"
"v_mfma_f32_16x16x32_bf16 a[100:103], a[236:239], a[196:199], a[100:103]\n\t"
"v_accvgpr_write_b32 a167, v251\n\t"
"v_fma_f32 v0, v2, s52, v98\n\t"
"v_mfma_f32_16x16x32_bf16 a[100:103], a[240:243], a[200:203], a[100:103]\n\t"
"v_fma_f32 v1, v3, s52, v99\n\t"
"v_fma_f32 v98, -v98, s66, v2\n\t"
"v_mfma_f32_16x16x32_bf16 a[100:103], a[244:247], a[204:207], a[100:103]\n\t"
"s_mov_b32 s20, 61440\n\t"
"buffer_load_dwordx4 a[236:239], v252, s[8:11], s20 offen offset:0 sc1\n\t"
"v_fma_f32 v2, v4, s52, v100\n\t"
"v_fma_f32 v99, -v99, s66, v3\n\t"
"s_waitcnt vmcnt(10)\n\t"
"v_mfma_f32_16x16x32_bf16 a[108:111], a[248:251], a[168:171], a[108:111]\n\t"
"v_fma_f32 v3, v5, s52, v101\n\t"
"v_fma_f32 v100, -v100, s66, v4\n\t"
"v_mfma_f32_16x16x32_bf16 a[108:111], a[252:255], a[172:175], a[108:111]\n\t"
"v_fma_f32 v101, -v101, s66, v5\n\t"
"v_fma_f32 v4, v6, s52, v102\n\t"
"v_mfma_f32_16x16x32_bf16 a[108:111], a[208:211], a[176:179], a[108:111]\n\t"
"buffer_load_dwordx4 a[240:243], v252, s[8:11], s20 offen offset:1024 sc1\n\t"
"v_fma_f32 v5, v7, s52, v103\n\t"
"v_fma_f32 v102, -v102, s66, v6\n\t"
"s_waitcnt lgkmcnt(0)\n\t"
"s_barrier\n\t"
"v_mfma_f32_16x16x32_bf16 a[116:119], a[248:251], a[172:175], a[116:119]\n\t"
"ds_read_b128 a[168:171], v242 offset:0\n\t"
"v_fma_f32 v6, v8, s52, v104\n\t"
"v_mfma_f32_16x16x32_bf16 a[116:119], a[252:255], a[176:179], a[116:119]\n\t"
"ds_read_b128 a[172:175], v242 offset:512\n\t"
"v_fma_f32 v103, -v103, s66, v7\n\t"
"v_mfma_f32_16x16x32_bf16 a[116:119], a[208:211], a[180:183], a[116:119]\n\t"
"s_add_u32 m0, s25, 896\n\t"
"s_nop 0\n\t"
"buffer_load_dwordx4 v246, s[12:15], 0 offen offset:128 nt lds\n\t"
"s_add_u32 m0, s26, 896\n\t"
"s_mov_b64 exec, s[36:37]\n\t"
"buffer_load_dwordx4 v246, s[12:15], s27 offen offset:128 nt lds\n\t"
"s_mov_b64 exec, -1\n\t"
"v_mfma_f32_16x16x32_bf16 a[124:127], a[248:251], a[176:179], a[124:127]\n\t"
"ds_read_b128 a[176:179], v242 offset:1024\n\t"
"v_fma_f32 v7, v9, s52, v105\n\t"
"v_mfma_f32_16x16x32_bf16 a[124:127], a[252:255], a[180:183], a[124:127]\n\t"
"v_fma_f32 v104, -v104, s66, v8\n\t"
"v_fma_f32 v105, -v105, s66, v9\n\t"
"v_mfma_f32_16x16x32_bf16 a[124:127], a[208:211], a[184:187], a[124:127]\n\t"
"s_add_u32 m0, s24, 896\n\t"
"s_add_u32 s28, s21, 0\n\t"
"buffer_load_dwordx4 v245, s[12:15], s28 offen offset:128 nt lds\n\t"
"v_fma_f32 v8, v10, s52, v106\n\t"
"v_fma_f32 v9, v11, s52, v107\n\t"
"v_mfma_f32_16x16x32_bf16 a[132:135], a[248:251], a[180:183], a[132:135]\n\t"
"ds_read_b128 a[180:183], v242 offset:1536\n\t"
"v_fma_f32 v106, -v106, s66, v10\n\t"
"v_mfma_f32_16x16x32_bf16 a[132:135], a[252:255], a[184:187], a[132:135]\n\t"
"v_fma_f32 v10, v12, s52, v108\n\t"
"v_fma_f32 v107, -v107, s66, v11\n\t"
"v_mfma_f32_16x16x32_bf16 a[132:135], a[208:211], a[188:191], a[132:135]\n\t"
"s_add_u32 m0, s24, 7040\n\t"
"s_add_u32 s28, s21, 196608\n\t"
"buffer_load_dwordx4 v245, s[12:15], s28 offen offset:128 nt lds\n\t"
"s_add_u32 m0, s24, 13184\n\t"
"s_add_u32 s28, s21, 393216\n\t"
"buffer_load_dwordx4 v245, s[12:15], s28 offen offset:128 nt lds\n\t"
"v_mfma_f32_16x16x32_bf16 a[140:143], a[248:251], a[184:187], a[140:143]\n\t"
"ds_read_b128 a[184:187], v242 offset:2048\n\t"
"v_fma_f32 v11, v13, s52, v109\n\t"
"v_mfma_f32_16x16x32_bf16 a[140:143], a[252:255], a[188:191], a[140:143]\n\t"
"v_fma_f32 v108, -v108, s66, v12\n\t"
"v_fma_f32 v109, -v109, s66, v13\n\t"
"v_mfma_f32_16x16x32_bf16 a[140:143], a[208:211], a[192:195], a[140:143]\n\t"
"s_add_u32 m0, s24, 20864\n\t"
"s_add_u32 s28, s21, 589824\n\t"
"buffer_load_dwordx4 v245, s[12:15], s28 offen offset:128 nt lds\n\t"
"v_fma_f32 v12, v14, s52, v110\n\t"
"v_fma_f32 v13, v15, s52, v111\n\t"
"v_mfma_f32_16x16x32_bf16 a[148:151], a[248:251], a[188:191], a[148:151]\n\t"
"ds_read_b128 a[188:191], v242 offset:2560\n\t"
"v_fma_f32 v110, -v110, s66, v14\n\t"
"v_mfma_f32_16x16x32_bf16 a[148:151], a[252:255], a[192:195], a[148:151]\n\t"
"v_fma_f32 v14, v16, s52, v112\n\t"
"v_fma_f32 v111, -v111, s66, v15\n\t"
"v_mfma_f32_16x16x32_bf16 a[148:151], a[208:211], a[196:199], a[148:151]\n\t"
"s_add_u32 m0, s24, 27008\n\t"
"s_add_u32 s28, s21, 786432\n\t"
"buffer_load_dwordx4 v245, s[12:15], s28 offen offset:128 nt lds\n\t"
"v_fma_f32 v15, v17, s52, v113\n\t"
"v_fma_f32 v112, -v112, s66, v16\n\t"
"v_mfma_f32_16x16x32_bf16 a[156:159], a[248:251], a[192:195], a[156:159]\n\t"
"ds_read_b128 a[192:195], v242 offset:3072\n\t"
"v_fma_f32 v113, -v113, s66, v17\n\t"
"v_mfma_f32_16x16x32_bf16 a[156:159], a[252:255], a[196:199], a[156:159]\n\t"
"v_fma_f32 v16, v18, s52, v114\n\t"
"v_fma_f32 v17, v19, s52, v115\n\t"
"v_mfma_f32_16x16x32_bf16 a[156:159], a[208:211], a[200:203], a[156:159]\n\t"
"v_fma_f32 v114, -v114, s66, v18\n\t"
"v_fma_f32 v18, v20, s52, v116\n\t"
"v_fma_f32 v115, -v115, s66, v19\n\t"
"v_fma_f32 v19, v21, s52, v117\n\t"
"v_mfma_f32_16x16x32_bf16 a[164:167], a[248:251], a[196:199], a[164:167]\n\t"
"ds_read_b128 a[196:199], v242 offset:3584\n\t"
"v_fma_f32 v116, -v116, s66, v20\n\t"
"v_mfma_f32_16x16x32_bf16 a[164:167], a[252:255], a[200:203], a[164:167]\n\t"
"ds_read_b128 a[200:203], v242 offset:4096\n\t"
"v_fma_f32 v117, -v117, s66, v21\n\t"
"v_mfma_f32_16x16x32_bf16 a[164:167], a[208:211], a[204:207], a[164:167]\n\t"
"ds_read_b128 a[204:207], v242 offset:4608\n\t"
"v_fma_f32 v20, v22, s52, v118\n\t"
"v_fma_f32 v21, v23, s52, v119\n\t"
"v_fma_f32 v118, -v118, s66, v22\n\t"
"s_waitcnt vmcnt(13) lgkmcnt(7)\n\t"
"v_mfma_f32_16x16x32_bf16 v[0:3], a[212:215], a[168:171], v[0:3]\n\t"
"v_fma_f32 v22, v24, s52, v120\n\t"
"v_fma_f32 v119, -v119, s66, v23\n\t"
"v_mfma_f32_16x16x32_bf16 v[0:3], a[216:219], a[172:175], v[0:3]\n\t"
"v_fma_f32 v23, v25, s52, v121\n\t"
"v_fma_f32 v120, -v120, s66, v24\n\t"
"v_mfma_f32_16x16x32_bf16 v[0:3], a[220:223], a[176:179], v[0:3]\n\t"
"v_fma_f32 v121, -v121, s66, v25\n\t"
"v_fma_f32 v24, v26, s52, v122\n\t"
"v_fma_f32 v25, v27, s52, v123\n\t"
"v_fma_f32 v122, -v122, s66, v26\n\t"
"s_waitcnt lgkmcnt(6)\n\t"
"v_mfma_f32_16x16x32_bf16 v[4:7], a[212:215], a[172:175], v[4:7]\n\t"
"v_fma_f32 v26, v28, s52, v124\n\t"
"v_fma_f32 v123, -v123, s66, v27\n\t"
"v_mfma_f32_16x16x32_bf16 v[4:7], a[216:219], a[176:179], v[4:7]\n\t"
"v_fma_f32 v27, v29, s52, v125\n\t"
"v_fma_f32 v124, -v124, s66, v28\n\t"
"v_mfma_f32_16x16x32_bf16 v[4:7], a[220:223], a[180:183], v[4:7]\n\t"
"v_fma_f32 v125, -v125, s66, v29\n\t"
"v_fma_f32 v28, v30, s52, v126\n\t"
"v_fma_f32 v29, v31, s52, v127\n\t"
"v_fma_f32 v126, -v126, s66, v30\n\t"
"s_waitcnt lgkmcnt(5)\n\t"
"v_mfma_f32_16x16x32_bf16 v[8:11], a[212:215], a[176:179], v[8:11]\n\t"
"v_fma_f32 v30, v32, s52, v128\n\t"
"v_fma_f32 v127, -v127, s66, v31\n\t"
"v_mfma_f32_16x16x32_bf16 v[8:11], a[216:219], a[180:183], v[8:11]\n\t"
"v_fma_f32 v31, v33, s52, v129\n\t"
"v_fma_f32 v128, -v128, s66, v32\n\t"
"v_mfma_f32_16x16x32_bf16 v[8:11], a[220:223], a[184:187], v[8:11]\n\t"
"v_fma_f32 v129, -v129, s66, v33\n\t"
"v_fma_f32 v32, v34, s52, v130\n\t"
"v_fma_f32 v33, v35, s52, v131\n\t"
"v_fma_f32 v130, -v130, s66, v34\n\t"
"s_waitcnt lgkmcnt(4)\n\t"
"v_mfma_f32_16x16x32_bf16 v[12:15], a[212:215], a[180:183], v[12:15]\n\t"
"v_fma_f32 v34, v36, s52, v132\n\t"
"v_fma_f32 v131, -v131, s66, v35\n\t"
"v_mfma_f32_16x16x32_bf16 v[12:15], a[216:219], a[184:187], v[12:15]\n\t"
"v_fma_f32 v35, v37, s52, v133\n\t"
"v_fma_f32 v132, -v132, s66, v36\n\t"
"v_mfma_f32_16x16x32_bf16 v[12:15], a[220:223], a[188:191], v[12:15]\n\t"
"buffer_load_dwordx4 a[208:211], v252, s[8:11], s20 offen offset:2048 sc1\n\t"
"v_fma_f32 v133, -v133, s66, v37\n\t"
"v_fma_f32 v36, v38, s52, v134\n\t"
"s_waitcnt lgkmcnt(3)\n\t"
"v_mfma_f32_16x16x32_bf16 v[16:19], a[212:215], a[184:187], v[16:19]\n\t"
"v_fma_f32 v37, v39, s52, v135\n\t"
"v_fma_f32 v134, -v134, s66, v38\n\t"
"v_mfma_f32_16x16x32_bf16 v[16:19], a[216:219], a[188:191], v[16:19]\n\t"
"v_fma_f32 v38, v40, s52, v136\n\t"
"v_fma_f32 v135, -v135, s66, v39\n\t"
"v_mfma_f32_16x16x32_bf16 v[16:19], a[220:223], a[192:195], v[16:19]\n\t"
"v_fma_f32 v39, v41, s52, v137\n\t"
"v_fma_f32 v136, -v136, s66, v40\n\t"
"v_fma_f32 v137, -v137, s66, v41\n\t"
"v_fma_f32 v40, v42, s52, v138\n\t"
"s_waitcnt lgkmcnt(2)\n\t"
"v_mfma_f32_16x16x32_bf16 v[20:23], a[212:215], a[188:191], v[20:23]\n\t"
"v_fma_f32 v41, v43, s52, v139\n\t"
"v_fma_f32 v138, -v138, s66, v42\n\t"
"v_mfma_f32_16x16x32_bf16 v[20:23], a[216:219], a[192:195], v[20:23]\n\t"
"v_fma_f32 v42, v44, s52, v140\n\t"
"v_fma_f32 v139, -v139, s66, v43\n\t"
"v_mfma_f32_16x16x32_bf16 v[20:23], a[220:223], a[196:199], v[20:23]\n\t"
"v_fma_f32 v43, v45, s52, v141\n\t"
"v_fma_f32 v140, -v140, s66, v44\n\t"
"v_fma_f32 v141, -v141, s66, v45\n\t"
"v_fma_f32 v44, v46, s52, v142\n\t"
"s_waitcnt lgkmcnt(1)\n\t"
"v_mfma_f32_16x16x32_bf16 v[24:27], a[212:215], a[192:195], v[24:27]\n\t"
"v_fma_f32 v45, v47, s52, v143\n\t"
"v_fma_f32 v142, -v142, s66, v46\n\t"
"v_mfma_f32_16x16x32_bf16 v[24:27], a[216:219], a[196:199], v[24:27]\n\t"
"v_fma_f32 v46, v48, s52, v144\n\t"
"v_fma_f32 v143, -v143, s66, v47\n\t"
"v_mfma_f32_16x16x32_bf16 v[24:27], a[220:223], a[200:203], v[24:27]\n\t"
"v_fma_f32 v47, v49, s52, v145\n\t"
"v_fma_f32 v144, -v144, s66, v48\n\t"
"v_fma_f32 v145, -v145, s66, v49\n\t"
"v_fma_f32 v48, v50, s52, v146\n\t"
"s_waitcnt lgkmcnt(0)\n\t"
"v_mfma_f32_16x16x32_bf16 v[28:31], a[212:215], a[196:199], v[28:31]\n\t"
"v_fma_f32 v49, v51, s52, v147\n\t"
"v_fma_f32 v146, -v146, s66, v50\n\t"
"v_mfma_f32_16x16x32_bf16 v[28:31], a[216:219], a[200:203], v[28:31]\n\t"
"v_fma_f32 v50, v52, s52, v148\n\t"
"v_fma_f32 v147, -v147, s66, v51\n\t"
"v_mfma_f32_16x16x32_bf16 v[28:31], a[220:223], a[204:207], v[28:31]\n\t"
"v_fma_f32 v51, v53, s52, v149\n\t"
"v_fma_f32 v148, -v148, s66, v52\n\t"
"v_fma_f32 v149, -v149, s66, v53\n\t"
"v_fma_f32 v52, v54, s52, v150\n\t"
"s_waitcnt vmcnt(10)\n\t"
"v_mfma_f32_16x16x32_bf16 v[32:35], a[224:227], a[168:171], v[32:35]\n\t"
"v_fma_f32 v53, v55, s52, v151\n\t"
"v_fma_f32 v150, -v150, s66, v54\n\t"
"v_mfma_f32_16x16x32_bf16 v[32:35], a[228:231], a[172:175], v[32:35]\n\t"
"v_fma_f32 v54, v56, s52, v152\n\t"
"v_fma_f32 v151, -v151, s66, v55\n\t"
"v_mfma_f32_16x16x32_bf16 v[32:35], a[232:235], a[176:179], v[32:35]\n\t"
"ds_read_b64_tr_b16 v[222:223], v244 offset:512\n\t"
"ds_read_b64_tr_b16 v[224:225], v244 offset:3584\n\t"
"v_fma_f32 v55, v57, s52, v153\n\t"
"v_fma_f32 v152, -v152, s66, v56\n\t"
"v_mfma_f32_16x16x32_bf16 v[36:39], a[224:227], a[172:175], v[36:39]\n\t"
"v_fma_f32 v153, -v153, s66, v57\n\t"
"v_fma_f32 v56, v58, s52, v154\n\t"
"v_mfma_f32_16x16x32_bf16 v[36:39], a[228:231], a[176:179], v[36:39]\n\t"
"v_fma_f32 v57, v59, s52, v155\n\t"
"v_fma_f32 v154, -v154, s66, v58\n\t"
"v_mfma_f32_16x16x32_bf16 v[36:39], a[232:235], a[180:183], v[36:39]\n\t"
"v_fma_f32 v58, v60, s52, v156\n\t"
"v_fma_f32 v155, -v155, s66, v59\n\t"
"v_fma_f32 v59, v61, s52, v157\n\t"
"v_fma_f32 v156, -v156, s66, v60\n\t"
"v_mfma_f32_16x16x32_bf16 v[40:43], a[224:227], a[176:179], v[40:43]\n\t"
"v_fma_f32 v157, -v157, s66, v61\n\t"
"v_fma_f32 v60, v62, s52, v158\n\t"
"v_mfma_f32_16x16x32_bf16 v[40:43], a[228:231], a[180:183], v[40:43]\n\t"
"v_fma_f32 v61, v63, s52, v159\n\t"
"v_fma_f32 v158, -v158, s66, v62\n\t"
"v_mfma_f32_16x16x32_bf16 v[40:43], a[232:235], a[184:187], v[40:43]\n\t"
"v_fma_f32 v62, v64, s52, v160\n\t"
"v_fma_f32 v159, -v159, s66, v63\n\t"
"v_fma_f32 v63, v65, s52, v161\n\t"
"v_fma_f32 v160, -v160, s66, v64\n\t"
"v_mfma_f32_16x16x32_bf16 v[44:47], a[224:227], a[180:183], v[44:47]\n\t"
"ds_read_b64_tr_b16 v[226:227], v244 offset:6656\n\t"
"v_fma_f32 v161, -v161, s66, v65\n\t"
"v_mfma_f32_16x16x32_bf16 v[44:47], a[228:231], a[184:187], v[44:47]\n\t"
"ds_read_b64_tr_b16 v[228:229], v244 offset:9728\n\t"
"v_fma_f32 v64, v66, s52, v162\n\t"
"v_mfma_f32_16x16x32_bf16 v[44:47], a[232:235], a[188:191], v[44:47]\n\t"
"v_fma_f32 v65, v67, s52, v163\n\t"
"v_fma_f32 v162, -v162, s66, v66\n\t"
"v_fma_f32 v66, v68, s52, v164\n\t"
"v_fma_f32 v163, -v163, s66, v67\n\t"
"v_mfma_f32_16x16x32_bf16 v[48:51], a[224:227], a[184:187], v[48:51]\n\t"
"v_fma_f32 v67, v69, s52, v165\n\t"
"v_fma_f32 v164, -v164, s66, v68\n\t"
"v_mfma_f32_16x16x32_bf16 v[48:51], a[228:231], a[188:191], v[48:51]\n\t"
"v_fma_f32 v165, -v165, s66, v69\n\t"
"v_fma_f32 v68, v70, s52, v166\n\t"
"v_mfma_f32_16x16x32_bf16 v[48:51], a[232:235], a[192:195], v[48:51]\n\t"
"s_mov_b32 s20, 73728\n\t"
"buffer_load_dwordx4 a[212:215], v252, s[8:11], s20 offen offset:0 sc1\n\t"
"v_fma_f32 v69, v71, s52, v167\n\t"
"v_fma_f32 v166, -v166, s66, v70\n\t"
"v_mfma_f32_16x16x32_bf16 v[52:55], a[224:227], a[188:191], v[52:55]\n\t"
"v_fma_f32 v70, v72, s52, v168\n\t"
"v_fma_f32 v167, -v167, s66, v71\n\t"
"v_mfma_f32_16x16x32_bf16 v[52:55], a[228:231], a[192:195], v[52:55]\n\t"
"v_fma_f32 v71, v73, s52, v169\n\t"
"v_fma_f32 v168, -v168, s66, v72\n\t"
"v_mfma_f32_16x16x32_bf16 v[52:55], a[232:235], a[196:199], v[52:55]\n\t"
"buffer_load_dwordx4 a[216:219], v252, s[8:11], s20 offen offset:1024 sc1\n\t"
"v_fma_f32 v169, -v169, s66, v73\n\t"
"v_fma_f32 v72, v74, s52, v170\n\t"
"s_waitcnt lgkmcnt(2)\n\t"
"v_mfma_f32_16x16x32_bf16 v[222:225], v[222:225], v[218:221], 0\n\t"
"buffer_load_dwordx4 a[220:223], v252, s[8:11], s20 offen offset:2048 sc1\n\t"
"v_fma_f32 v73, v75, s52, v171\n\t"
"v_fma_f32 v170, -v170, s66, v74\n\t"
"v_mfma_f32_16x16x32_bf16 v[56:59], a[224:227], a[192:195], v[56:59]\n\t"
"ds_read_b64_tr_b16 v[230:231], v244 offset:12800\n\t"
"v_fma_f32 v74, v76, s52, v172\n\t"
"v_mfma_f32_16x16x32_bf16 v[56:59], a[228:231], a[196:199], v[56:59]\n\t"
"ds_read_b64_tr_b16 v[232:233], v244 offset:15872\n\t"
"v_fma_f32 v171, -v171, s66, v75\n\t"
"v_mfma_f32_16x16x32_bf16 v[56:59], a[232:235], a[200:203], v[56:59]\n\t"
"s_mov_b32 s20, 86016\n\t"
"buffer_load_dwordx4 a[244:247], v252, s[8:11], s20 offen offset:0 sc1\n\t"
"v_fma_f32 v75, v77, s52, v173\n\t"
"v_fma_f32 v172, -v172, s66, v76\n\t"
"v_mfma_f32_16x16x32_bf16 v[60:63], a[224:227], a[196:199], v[60:63]\n\t"
"v_fma_f32 v173, -v173, s66, v77\n\t"
"v_fma_f32 v76, v78, s52, v174\n\t"
"v_mfma_f32_16x16x32_bf16 v[60:63], a[228:231], a[200:203], v[60:63]\n\t"
"v_cvt_pk_bf16_f32 v222, v222, v223\n\t"
"v_cvt_pk_bf16_f32 v223, v224, v225\n\t"
"v_mfma_f32_16x16x32_bf16 v[60:63], a[232:235], a[204:207], v[60:63]\n\t"
"buffer_load_dwordx4 a[224:227], v252, s[8:11], s20 offen offset:1024 sc1\n\t"
"v_fma_f32 v77, v79, s52, v175\n\t"
"v_fma_f32 v174, -v174, s66, v78\n\t"
"s_waitcnt lgkmcnt(2)\n\t"
"v_mfma_f32_16x16x32_bf16 v[226:229], v[226:229], v[218:221], 0\n\t"
"v_fma_f32 v78, v80, s52, v176\n\t"
"v_fma_f32 v175, -v175, s66, v79\n\t"
"v_fma_f32 v79, v81, s52, v177\n\t"
"v_fma_f32 v176, -v176, s66, v80\n\t"
"s_waitcnt vmcnt(5)\n\t"
"v_mfma_f32_16x16x32_bf16 v[64:67], a[236:239], a[168:171], v[64:67]\n\t"
"v_fma_f32 v177, -v177, s66, v81\n\t"
"ds_read_b64_tr_b16 v[234:235], v244 offset:20480\n\t"
"v_mfma_f32_16x16x32_bf16 v[64:67], a[240:243], a[172:175], v[64:67]\n\t"
"ds_read_b64_tr_b16 v[236:237], v244 offset:23552\n\t"
"v_fma_f32 v80, v82, s52, v178\n\t"
"v_mfma_f32_16x16x32_bf16 v[64:67], a[208:211], a[176:179], v[64:67]\n\t"
"buffer_load_dwordx4 a[228:231], v252, s[8:11], s20 offen offset:2048 sc1\n\t"
"v_fma_f32 v81, v83, s52, v179\n\t"
"v_fma_f32 v178, -v178, s66, v82\n\t"
"v_mfma_f32_16x16x32_bf16 v[68:71], a[236:239], a[172:175], v[68:71]\n\t"
"v_fma_f32 v82, v84, s52, v180\n\t"
"v_fma_f32 v179, -v179, s66, v83\n\t"
"v_mfma_f32_16x16x32_bf16 v[68:71], a[240:243], a[176:179], v[68:71]\n\t"
"v_cvt_pk_bf16_f32 v226, v226, v227\n\t"
"v_cvt_pk_bf16_f32 v227, v228, v229\n\t"
"v_mfma_f32_16x16x32_bf16 v[68:71], a[208:211], a[180:183], v[68:71]\n\t"
"s_mov_b32 s20, 98304\n\t"
"buffer_load_dwordx4 a[232:235], v252, s[8:11], s20 offen offset:0 sc1\n\t"
"v_fma_f32 v83, v85, s52, v181\n\t"
"v_fma_f32 v180, -v180, s66, v84\n\t"
"v_mfma_f32_16x16x32_bf16 v[72:75], a[236:239], a[176:179], v[72:75]\n\t"
"v_fma_f32 v181, -v181, s66, v85\n\t"
"ds_read_b128 a[168:171], v242 offset:9216\n\t"
"v_mfma_f32_16x16x32_bf16 v[72:75], a[240:243], a[180:183], v[72:75]\n\t"
"ds_read_b128 a[172:175], v242 offset:9728\n\t"
"v_fma_f32 v84, v86, s52, v182\n\t"
"v_mfma_f32_16x16x32_bf16 v[72:75], a[208:211], a[184:187], v[72:75]\n\t"
"buffer_load_dwordx4 a[248:251], v252, s[8:11], s20 offen offset:1024 sc1\n\t"
"ds_read_b128 a[176:179], v242 offset:10240\n\t"
"v_fma_f32 v85, v87, s52, v183\n\t"
"s_waitcnt lgkmcnt(5)\n\t"
"v_mfma_f32_16x16x32_bf16 v[230:233], v[230:233], v[218:221], 0\n\t"
"v_fma_f32 v182, -v182, s66, v86\n\t"
"v_fma_f32 v86, v88, s52, v184\n\t"
"v_fma_f32 v183, -v183, s66, v87\n\t"
"v_fma_f32 v87, v89, s52, v185\n\t"
"v_mfma_f32_16x16x32_bf16 v[76:79], a[236:239], a[180:183], v[76:79]\n\t"
"v_fma_f32 v184, -v184, s66, v88\n\t"
"v_fma_f32 v185, -v185, s66, v89\n\t"
"v_mfma_f32_16x16x32_bf16 v[76:79], a[240:243], a[184:187], v[76:79]\n\t"
"ds_read_b128 a[180:183], v242 offset:10752\n\t"
"v_fma_f32 v88, v90, s52, v186\n\t"
"v_mfma_f32_16x16x32_bf16 v[76:79], a[208:211], a[188:191], v[76:79]\n\t"
"buffer_load_dwordx4 a[252:255], v252, s[8:11], s20 offen offset:2048 sc1\n\t"
"v_fma_f32 v89, v91, s52, v187\n\t"
"v_fma_f32 v186, -v186, s66, v90\n\t"
"v_mfma_f32_16x16x32_bf16 v[80:83], a[236:239], a[184:187], v[80:83]\n\t"
"v_fma_f32 v90, v92, s52, v188\n\t"
"v_fma_f32 v187, -v187, s66, v91\n\t"
"v_mfma_f32_16x16x32_bf16 v[80:83], a[240:243], a[188:191], v[80:83]\n\t"
"v_fma_f32 v91, v93, s52, v189\n\t"
"v_fma_f32 v188, -v188, s66, v92\n\t"
"v_mfma_f32_16x16x32_bf16 v[80:83], a[208:211], a[192:195], v[80:83]\n\t"
"v_fma_f32 v189, -v189, s66, v93\n\t"
"v_cvt_pk_bf16_f32 v230, v230, v231\n\t"
"v_cvt_pk_bf16_f32 v231, v232, v233\n\t"
"ds_read_b128 a[184:187], v242 offset:11264\n\t"
"s_waitcnt lgkmcnt(5)\n\t"
"v_mfma_f32_16x16x32_bf16 v[234:237], v[234:237], v[218:221], 0\n\t"
"v_fma_f32 v92, v94, s52, v190\n\t"
"v_fma_f32 v93, v95, s52, v191\n\t"
"v_fma_f32 v190, -v190, s66, v94\n\t"
"v_fma_f32 v94, v96, s52, v192\n\t"
"v_mfma_f32_16x16x32_bf16 v[84:87], a[236:239], a[188:191], v[84:87]\n\t"
"v_fma_f32 v191, -v191, s66, v95\n\t"
"v_fma_f32 v95, v97, s52, v193\n\t"
"v_mfma_f32_16x16x32_bf16 v[84:87], a[240:243], a[192:195], v[84:87]\n\t"
"v_fma_f32 v192, -v192, s66, v96\n\t"
"v_fma_f32 v193, -v193, s66, v97\n\t"
"v_mfma_f32_16x16x32_bf16 v[84:87], a[208:211], a[196:199], v[84:87]\n\t"
"ds_read_b128 a[188:191], v242 offset:11776\n\t"
"v_mfma_f32_16x16x32_bf16 v[88:91], a[236:239], a[192:195], v[88:91]\n\t"
"ds_read_b128 a[192:195], v242 offset:12288\n\t"
"v_mfma_f32_16x16x32_bf16 v[88:91], a[240:243], a[196:199], v[88:91]\n\t"
"v_cvt_pk_bf16_f32 v234, v234, v235\n\t"
"v_mfma_f32_16x16x32_bf16 v[88:91], a[208:211], a[200:203], v[88:91]\n\t"
"v_cvt_pk_bf16_f32 v235, v236, v237\n\t"
"v_mfma_f32_16x16x32_bf16 v[92:95], a[236:239], a[196:199], v[92:95]\n\t"
"ds_read_b128 a[196:199], v242 offset:12800\n\t"
"v_mfma_f32_16x16x32_bf16 v[92:95], a[240:243], a[200:203], v[92:95]\n\t"
"ds_read_b128 a[200:203], v242 offset:13312\n\t"
"v_mfma_f32_16x16x32_bf16 v[92:95], a[208:211], a[204:207], v[92:95]\n\t"
"s_mov_b32 s20, 258048\n\t"
"buffer_load_dwordx4 a[236:239], v252, s[8:11], s20 offen offset:0 sc1\n\t"
"s_waitcnt vmcnt(7) lgkmcnt(6)\n\t"
"v_mfma_f32_16x16x32_bf16 v[98:101], a[212:215], a[168:171], v[98:101]\n\t"
"ds_read_b128 a[204:207], v242 offset:13824\n\t"
"v_mfma_f32_16x16x32_bf16 v[98:101], a[216:219], a[172:175], v[98:101]\n\t"
"v_mfma_f32_16x16x32_bf16 v[98:101], a[220:223], a[176:179], v[98:101]\n\t"
"buffer_load_dwordx4 a[208:211], v252, s[8:11], s20 offen offset:1024 sc1\n\t"
"s_waitcnt lgkmcnt(6)\n\t"
"v_mfma_f32_16x16x32_bf16 v[102:105], a[212:215], a[172:175], v[102:105]\n\t"
"v_mfma_f32_16x16x32_bf16 v[102:105], a[216:219], a[176:179], v[102:105]\n\t"
"v_mfma_f32_16x16x32_bf16 v[102:105], a[220:223], a[180:183], v[102:105]\n\t"
"buffer_load_dwordx4 a[240:243], v252, s[8:11], s20 offen offset:2048 sc1\n\t"
"s_waitcnt lgkmcnt(0)\n\t"
"s_barrier\n\t"
"v_mfma_f32_16x16x32_bf16 v[106:109], a[212:215], a[176:179], v[106:109]\n\t"
"v_mfma_f32_16x16x32_bf16 v[106:109], a[216:219], a[180:183], v[106:109]\n\t"
"v_mfma_f32_16x16x32_bf16 v[106:109], a[220:223], a[184:187], v[106:109]\n\t"
"ds_write_b64 v243, v[222:223] offset:0\n\t"
"ds_write_b64 v243, v[226:227] offset:512\n\t"
"v_mfma_f32_16x16x32_bf16 v[110:113], a[212:215], a[180:183], v[110:113]\n\t"
"ds_read_b64_tr_b16 v[222:223], v244 offset:26624\n\t"
"v_mfma_f32_16x16x32_bf16 v[110:113], a[216:219], a[184:187], v[110:113]\n\t"
"ds_read_b64_tr_b16 v[224:225], v244 offset:29696\n\t"
"v_mfma_f32_16x16x32_bf16 v[110:113], a[220:223], a[188:191], v[110:113]\n\t"
"ds_write_b64 v243, v[230:231] offset:1024\n\t"
"v_mfma_f32_16x16x32_bf16 v[114:117], a[212:215], a[184:187], v[114:117]\n\t"
"ds_read_b64_tr_b16 v[226:227], v244 offset:32768\n\t"
"v_mfma_f32_16x16x32_bf16 v[114:117], a[216:219], a[188:191], v[114:117]\n\t"
"ds_read_b64_tr_b16 v[228:229], v244 offset:35840\n\t"
"v_mfma_f32_16x16x32_bf16 v[114:117], a[220:223], a[192:195], v[114:117]\n\t"
"ds_write_b64 v243, v[234:235] offset:1536\n\t"
"v_mfma_f32_16x16x32_bf16 v[118:121], a[212:215], a[188:191], v[118:121]\n\t"
"ds_read_b64_tr_b16 v[230:231], v244 offset:40448\n\t"
"v_mfma_f32_16x16x32_bf16 v[118:121], a[216:219], a[192:195], v[118:121]\n\t"
"ds_read_b64_tr_b16 v[232:233], v244 offset:43520\n\t"
"v_mfma_f32_16x16x32_bf16 v[118:121], a[220:223], a[196:199], v[118:121]\n\t"
"ds_read_b64_tr_b16 v[234:235], v244 offset:46592\n\t"
"ds_read_b64_tr_b16 v[236:237], v244 offset:49664\n\t"
"s_waitcnt lgkmcnt(8)\n\t"
"v_mfma_f32_16x16x32_bf16 v[222:225], v[222:225], v[218:221], 0\n\t"
"s_add_u32 m0, s24, 33152\n\t"
"s_add_u32 s28, s21, 983040\n\t"
"buffer_load_dwordx4 v245, s[12:15], s28 offen offset:128 nt lds\n\t"
"v_mfma_f32_16x16x32_bf16 v[122:125], a[212:215], a[192:195], v[122:125]\n\t"
"v_mfma_f32_16x16x32_bf16 v[122:125], a[216:219], a[196:199], v[122:125]\n\t"
"v_mfma_f32_16x16x32_bf16 v[122:125], a[220:223], a[200:203], v[122:125]\n\t"
"s_add_u32 m0, s24, 40832\n\t"
"s_add_u32 s28, s21, 1179648\n\t"
"buffer_load_dwordx4 v245, s[12:15], s28 offen offset:128 nt lds\n\t"
"s_waitcnt lgkmcnt(5)\n\t"
"v_mfma_f32_16x16x32_bf16 v[226:229], v[226:229], v[218:221], 0\n\t"
"s_add_u32 m0, s24, 46976\n\t"
"s_add_u32 s28, s21, 1376256\n\t"
"buffer_load_dwordx4 v245, s[12:15], s28 offen offset:128 nt lds\n\t"
"v_mfma_f32_16x16x32_bf16 v[126:129], a[212:215], a[196:199], v[126:129]\n\t"
"v_cvt_pk_bf16_f32 v222, v222, v223\n\t"
"v_cvt_pk_bf16_f32 v223, v224, v225\n\t"
"v_mfma_f32_16x16x32_bf16 v[126:129], a[216:219], a[200:203], v[126:129]\n\t"
"v_mfma_f32_16x16x32_bf16 v[126:129], a[220:223], a[204:207], v[126:129]\n\t"
"s_add_u32 m0, s24, 53120\n\t"
"s_add_u32 s28, s21, 1572864\n\t"
"buffer_load_dwordx4 v245, s[12:15], s28 offen offset:128 nt lds\n\t"
"s_waitcnt lgkmcnt(2)\n\t"
"v_mfma_f32_16x16x32_bf16 v[230:233], v[230:233], v[218:221], 0\n\t"
"s_add_u32 m0, s24, 60800\n\t"
"s_add_u32 s28, s21, 1769472\n\t"
"buffer_load_dwordx4 v245, s[12:15], s28 offen offset:128 nt lds\n\t"
"s_waitcnt vmcnt(11)\n\t"
"v_mfma_f32_16x16x32_bf16 v[130:133], a[244:247], a[168:171], v[130:133]\n\t"
"v_cvt_pk_bf16_f32 v226, v226, v227\n\t"
"v_cvt_pk_bf16_f32 v227, v228, v229\n\t"
"v_mfma_f32_16x16x32_bf16 v[130:133], a[224:227], a[172:175], v[130:133]\n\t"
"v_mfma_f32_16x16x32_bf16 v[130:133], a[228:231], a[176:179], v[130:133]\n\t"
"s_add_u32 m0, s24, 66944\n\t"
"s_add_u32 s28, s21, 1966080\n\t"
"buffer_load_dwordx4 v245, s[12:15], s28 offen offset:128 nt lds\n\t"
"s_waitcnt lgkmcnt(0)\n\t"
"v_mfma_f32_16x16x32_bf16 v[234:237], v[234:237], v[218:221], 0\n\t"
"ds_write_b64 v243, v[222:223] offset:2048\n\t"
"s_add_u32 m0, s24, 73088\n\t"
"s_add_u32 s28, s21, 2162688\n\t"
"buffer_load_dwordx4 v245, s[12:15], s28 offen offset:128 nt lds\n\t"
"v_mfma_f32_16x16x32_bf16 v[134:137], a[244:247], a[172:175], v[134:137]\n\t"
"ds_read_b64_tr_b16 v[222:223], v244 offset:52736\n\t"
"v_cvt_pk_bf16_f32 v230, v230, v231\n\t"
"v_mfma_f32_16x16x32_bf16 v[134:137], a[224:227], a[176:179], v[134:137]\n\t"
"ds_read_b64_tr_b16 v[224:225], v244 offset:55808\n\t"
"v_cvt_pk_bf16_f32 v231, v232, v233\n\t"
"v_mfma_f32_16x16x32_bf16 v[134:137], a[228:231], a[180:183], v[134:137]\n\t"
"ds_write_b64 v243, v[226:227] offset:2560\n\t"
"v_mfma_f32_16x16x32_bf16 v[138:141], a[244:247], a[176:179], v[138:141]\n\t"
"v_mfma_f32_16x16x32_bf16 v[138:141], a[224:227], a[180:183], v[138:141]\n\t"
"v_cvt_pk_bf16_f32 v234, v234, v235\n\t"
"v_cvt_pk_bf16_f32 v235, v236, v237\n\t"
"v_mfma_f32_16x16x32_bf16 v[138:141], a[228:231], a[184:187], v[138:141]\n\t"
"ds_write_b64 v243, v[230:231] offset:3072\n\t"
"v_mfma_f32_16x16x32_bf16 v[142:145], a[244:247], a[180:183], v[142:145]\n\t"
"v_mfma_f32_16x16x32_bf16 v[142:145], a[224:227], a[184:187], v[142:145]\n\t"
"v_mfma_f32_16x16x32_bf16 v[142:145], a[228:231], a[188:191], v[142:145]\n\t"
"ds_write_b64 v243, v[234:235] offset:3584\n\t"
"s_waitcnt lgkmcnt(3)\n\t"
"v_mfma_f32_16x16x32_bf16 v[222:225], v[222:225], v[218:221], 0\n\t"
"v_mfma_f32_16x16x32_bf16 v[146:149], a[244:247], a[184:187], v[146:149]\n\t"
"v_mfma_f32_16x16x32_bf16 v[146:149], a[224:227], a[188:191], v[146:149]\n\t"
"v_mfma_f32_16x16x32_bf16 v[146:149], a[228:231], a[192:195], v[146:149]\n\t"
"v_mfma_f32_16x16x32_bf16 v[150:153], a[244:247], a[188:191], v[150:153]\n\t"
"v_mfma_f32_16x16x32_bf16 v[150:153], a[224:227], a[192:195], v[150:153]\n\t"
"s_nop 2\n\t"
"v_cvt_pk_bf16_f32 v222, v222, v223\n\t"
"v_cvt_pk_bf16_f32 v223, v224, v225\n\t"
"v_mfma_f32_16x16x32_bf16 v[150:153], a[228:231], a[196:199], v[150:153]\n\t"
"v_mfma_f32_16x16x32_bf16 v[154:157], a[244:247], a[192:195], v[154:157]\n\t"
"v_mfma_f32_16x16x32_bf16 v[154:157], a[224:227], a[196:199], v[154:157]\n\t"
"v_mfma_f32_16x16x32_bf16 v[154:157], a[228:231], a[200:203], v[154:157]\n\t"
"ds_write_b64 v243, v[222:223] offset:4096\n\t"
"v_mfma_f32_16x16x32_bf16 v[158:161], a[244:247], a[196:199], v[158:161]\n\t"
"v_mfma_f32_16x16x32_bf16 v[158:161], a[224:227], a[200:203], v[158:161]\n\t"
"v_mfma_f32_16x16x32_bf16 v[158:161], a[228:231], a[204:207], v[158:161]\n\t"
"s_waitcnt vmcnt(10)\n\t"
"v_mfma_f32_16x16x32_bf16 v[162:165], a[232:235], a[168:171], v[162:165]\n\t"
"v_mfma_f32_16x16x32_bf16 v[162:165], a[248:251], a[172:175], v[162:165]\n\t"
"v_mfma_f32_16x16x32_bf16 v[162:165], a[252:255], a[176:179], v[162:165]\n\t"
"s_waitcnt lgkmcnt(0)\n\t"
"s_barrier\n\t"
"v_mfma_f32_16x16x32_bf16 v[166:169], a[232:235], a[172:175], v[166:169]\n\t"
"ds_read_b128 a[168:171], v242 offset:0\n\t"
"v_mfma_f32_16x16x32_bf16 v[166:169], a[248:251], a[176:179], v[166:169]\n\t"
"ds_read_b128 a[172:175], v242 offset:512\n\t"
"v_mfma_f32_16x16x32_bf16 v[166:169], a[252:255], a[180:183], v[166:169]\n\t"
"v_mfma_f32_16x16x32_bf16 v[170:173], a[232:235], a[176:179], v[170:173]\n\t"
"ds_read_b128 a[176:179], v242 offset:1024\n\t"
"v_mfma_f32_16x16x32_bf16 v[170:173], a[248:251], a[180:183], v[170:173]\n\t"
"v_mfma_f32_16x16x32_bf16 v[170:173], a[252:255], a[184:187], v[170:173]\n\t"
"s_mov_b32 s20, 270336\n\t"
"buffer_load_dwordx4 a[212:215], v252, s[8:11], s20 offen offset:0 sc1\n\t"
"v_mfma_f32_16x16x32_bf16 v[174:177], a[232:235], a[180:183], v[174:177]\n\t"
"ds_read_b128 a[180:183], v242 offset:1536\n\t"
"v_mfma_f32_16x16x32_bf16 v[174:177], a[248:251], a[184:187], v[174:177]\n\t"
"v_mfma_f32_16x16x32_bf16 v[174:177], a[252:255], a[188:191], v[174:177]\n\t"
"buffer_load_dwordx4 a[216:219], v252, s[8:11], s20 offen offset:1024 sc1\n\t"
"v_mfma_f32_16x16x32_bf16 v[178:181], a[232:235], a[184:187], v[178:181]\n\t"
"ds_read_b128 a[184:187], v242 offset:2048\n\t"
"v_mfma_f32_16x16x32_bf16 v[178:181], a[248:251], a[188:191], v[178:181]\n\t"
"v_mfma_f32_16x16x32_bf16 v[178:181], a[252:255], a[192:195], v[178:181]\n\t"
"buffer_load_dwordx4 a[220:223], v252, s[8:11], s20 offen offset:2048 sc1\n\t"
"v_mfma_f32_16x16x32_bf16 v[182:185], a[232:235], a[188:191], v[182:185]\n\t"
"ds_read_b128 a[188:191], v242 offset:2560\n\t"
"v_mfma_f32_16x16x32_bf16 v[182:185], a[248:251], a[192:195], v[182:185]\n\t"
"v_mfma_f32_16x16x32_bf16 v[182:185], a[252:255], a[196:199], v[182:185]\n\t"
"s_mov_b32 s20, 282624\n\t"
"buffer_load_dwordx4 a[224:227], v252, s[8:11], s20 offen offset:0 sc1\n\t"
"v_mfma_f32_16x16x32_bf16 v[186:189], a[232:235], a[192:195], v[186:189]\n\t"
"ds_read_b128 a[192:195], v242 offset:3072\n\t"
"v_mfma_f32_16x16x32_bf16 v[186:189], a[248:251], a[196:199], v[186:189]\n\t"
"v_mfma_f32_16x16x32_bf16 v[186:189], a[252:255], a[200:203], v[186:189]\n\t"
"v_mfma_f32_16x16x32_bf16 v[190:193], a[232:235], a[196:199], v[190:193]\n\t"
"ds_read_b128 a[196:199], v242 offset:3584\n\t"
"v_mfma_f32_16x16x32_bf16 v[190:193], a[248:251], a[200:203], v[190:193]\n\t"
"ds_read_b128 a[200:203], v242 offset:4096\n\t"
"v_mfma_f32_16x16x32_bf16 v[190:193], a[252:255], a[204:207], v[190:193]\n\t"
"buffer_load_dwordx4 a[228:231], v252, s[8:11], s20 offen offset:1024 sc1\n\t"
"s_waitcnt vmcnt(12) lgkmcnt(6)\n\t"
"v_mfma_f32_16x16x32_bf16 v[0:3], a[236:239], a[168:171], v[0:3]\n\t"
"ds_read_b128 a[204:207], v242 offset:4608\n\t"
"v_mfma_f32_16x16x32_bf16 v[0:3], a[208:211], a[172:175], v[0:3]\n\t"
"v_mfma_f32_16x16x32_bf16 v[0:3], a[240:243], a[176:179], v[0:3]\n\t"
"buffer_load_dwordx4 a[232:235], v252, s[8:11], s20 offen offset:2048 sc1\n\t"
"s_waitcnt lgkmcnt(6)\n\t"
"v_mfma_f32_16x16x32_bf16 v[4:7], a[236:239], a[172:175], v[4:7]\n\t"
"v_mfma_f32_16x16x32_bf16 v[4:7], a[208:211], a[176:179], v[4:7]\n\t"
"v_mfma_f32_16x16x32_bf16 v[4:7], a[240:243], a[180:183], v[4:7]\n\t"
"s_mov_b32 s20, 294912\n\t"
"buffer_load_dwordx4 a[244:247], v252, s[8:11], s20 offen offset:0 sc1\n\t"
"s_waitcnt lgkmcnt(5)\n\t"
"v_mfma_f32_16x16x32_bf16 v[8:11], a[236:239], a[176:179], v[8:11]\n\t"
"v_mfma_f32_16x16x32_bf16 v[8:11], a[208:211], a[180:183], v[8:11]\n\t"
"v_mfma_f32_16x16x32_bf16 v[8:11], a[240:243], a[184:187], v[8:11]\n\t"
"s_waitcnt lgkmcnt(4)\n\t"
"v_mfma_f32_16x16x32_bf16 v[12:15], a[236:239], a[180:183], v[12:15]\n\t"
"v_mfma_f32_16x16x32_bf16 v[12:15], a[208:211], a[184:187], v[12:15]\n\t"
"v_mfma_f32_16x16x32_bf16 v[12:15], a[240:243], a[188:191], v[12:15]\n\t"
"buffer_load_dwordx4 a[248:251], v252, s[8:11], s20 offen offset:1024 sc1\n\t"
"s_waitcnt lgkmcnt(3)\n\t"
"v_mfma_f32_16x16x32_bf16 v[16:19], a[236:239], a[184:187], v[16:19]\n\t"
"v_xor_b32 v244, 0xc0, v244\n\t"
"v_mfma_f32_16x16x32_bf16 v[16:19], a[208:211], a[188:191], v[16:19]\n\t"
"v_mfma_f32_16x16x32_bf16 v[16:19], a[240:243], a[192:195], v[16:19]\n\t"
"buffer_load_dwordx4 a[252:255], v252, s[8:11], s20 offen offset:2048 sc1\n\t"
"s_waitcnt lgkmcnt(2)\n\t"
"v_mfma_f32_16x16x32_bf16 v[20:23], a[236:239], a[188:191], v[20:23]\n\t"
"v_mfma_f32_16x16x32_bf16 v[20:23], a[208:211], a[192:195], v[20:23]\n\t"
"v_mfma_f32_16x16x32_bf16 v[20:23], a[240:243], a[196:199], v[20:23]\n\t"
"s_waitcnt lgkmcnt(1)\n\t"
"v_mfma_f32_16x16x32_bf16 v[24:27], a[236:239], a[192:195], v[24:27]\n\t"
"v_mfma_f32_16x16x32_bf16 v[24:27], a[208:211], a[196:199], v[24:27]\n\t"
"v_mfma_f32_16x16x32_bf16 v[24:27], a[240:243], a[200:203], v[24:27]\n\t"
"s_waitcnt lgkmcnt(0)\n\t"
"v_mfma_f32_16x16x32_bf16 v[28:31], a[236:239], a[196:199], v[28:31]\n\t"
"v_mfma_f32_16x16x32_bf16 v[28:31], a[208:211], a[200:203], v[28:31]\n\t"
"v_mfma_f32_16x16x32_bf16 v[28:31], a[240:243], a[204:207], v[28:31]\n\t"
"s_mov_b32 s20, 307200\n\t"
"buffer_load_dwordx4 a[236:239], v252, s[8:11], s20 offen offset:0 sc1\n\t"
"s_waitcnt vmcnt(7)\n\t"
"v_mfma_f32_16x16x32_bf16 v[32:35], a[212:215], a[168:171], v[32:35]\n\t"
"v_mfma_f32_16x16x32_bf16 v[32:35], a[216:219], a[172:175], v[32:35]\n\t"
"v_mfma_f32_16x16x32_bf16 v[32:35], a[220:223], a[176:179], v[32:35]\n\t"
"buffer_load_dwordx4 a[208:211], v252, s[8:11], s20 offen offset:1024 sc1\n\t"
"v_mfma_f32_16x16x32_bf16 v[36:39], a[212:215], a[172:175], v[36:39]\n\t"
"ds_read_b64_tr_b16 v[226:227], v244 offset:512\n\t"
"v_mfma_f32_16x16x32_bf16 v[36:39], a[216:219], a[176:179], v[36:39]\n\t"
"ds_read_b64_tr_b16 v[228:229], v244 offset:3584\n\t"
"v_mfma_f32_16x16x32_bf16 v[36:39], a[220:223], a[180:183], v[36:39]\n\t"
"buffer_load_dwordx4 a[240:243], v252, s[8:11], s20 offen offset:2048 sc1\n\t"
"v_mfma_f32_16x16x32_bf16 v[40:43], a[212:215], a[176:179], v[40:43]\n\t"
"v_mfma_f32_16x16x32_bf16 v[40:43], a[216:219], a[180:183], v[40:43]\n\t"
"v_mfma_f32_16x16x32_bf16 v[40:43], a[220:223], a[184:187], v[40:43]\n\t"
"v_mfma_f32_16x16x32_bf16 v[44:47], a[212:215], a[180:183], v[44:47]\n\t"
"ds_read_b64_tr_b16 v[230:231], v244 offset:6656\n\t"
"v_mfma_f32_16x16x32_bf16 v[44:47], a[216:219], a[184:187], v[44:47]\n\t"
"ds_read_b64_tr_b16 v[232:233], v244 offset:9728\n\t"
"v_mfma_f32_16x16x32_bf16 v[44:47], a[220:223], a[188:191], v[44:47]\n\t"
"v_mfma_f32_16x16x32_bf16 v[48:51], a[212:215], a[184:187], v[48:51]\n\t"
"v_mfma_f32_16x16x32_bf16 v[48:51], a[216:219], a[188:191], v[48:51]\n\t"
"v_mfma_f32_16x16x32_bf16 v[48:51], a[220:223], a[192:195], v[48:51]\n\t"
"v_mfma_f32_16x16x32_bf16 v[52:55], a[212:215], a[188:191], v[52:55]\n\t"
"v_mfma_f32_16x16x32_bf16 v[52:55], a[216:219], a[192:195], v[52:55]\n\t"
"v_mfma_f32_16x16x32_bf16 v[52:55], a[220:223], a[196:199], v[52:55]\n\t"
"s_waitcnt lgkmcnt(2)\n\t"
"v_mfma_f32_16x16x32_bf16 v[226:229], v[226:229], v[218:221], 0\n\t"
"v_mfma_f32_16x16x32_bf16 v[56:59], a[212:215], a[192:195], v[56:59]\n\t"
"ds_read_b64_tr_b16 v[234:235], v244 offset:12800\n\t"
"v_mfma_f32_16x16x32_bf16 v[56:59], a[216:219], a[196:199], v[56:59]\n\t"
"ds_read_b64_tr_b16 v[236:237], v244 offset:15872\n\t"
"v_mfma_f32_16x16x32_bf16 v[56:59], a[220:223], a[200:203], v[56:59]\n\t"
"v_mfma_f32_16x16x32_bf16 v[60:63], a[212:215], a[196:199], v[60:63]\n\t"
"v_mfma_f32_16x16x32_bf16 v[60:63], a[216:219], a[200:203], v[60:63]\n\t"
"s_nop 0\n\t"
"v_cvt_pk_bf16_f32 v226, v226, v227\n\t"
"v_cvt_pk_bf16_f32 v227, v228, v229\n\t"
"v_mfma_f32_16x16x32_bf16 v[60:63], a[220:223], a[204:207], v[60:63]\n\t"
"s_waitcnt lgkmcnt(2)\n\t"
"v_mfma_f32_16x16x32_bf16 v[230:233], v[230:233], v[218:221], 0\n\t"
"s_mov_b32 s20, 319488\n\t"
"buffer_load_dwordx4 a[212:215], v252, s[8:11], s20 offen offset:0 sc1\n\t"
"s_waitcnt vmcnt(7)\n\t"
"v_mfma_f32_16x16x32_bf16 v[64:67], a[224:227], a[168:171], v[64:67]\n\t"
"ds_read_b64_tr_b16 v[222:223], v244 offset:20480\n\t"
"v_mfma_f32_16x16x32_bf16 v[64:67], a[228:231], a[172:175], v[64:67]\n\t"
"ds_read_b64_tr_b16 v[224:225], v244 offset:23552\n\t"
"v_mfma_f32_16x16x32_bf16 v[64:67], a[232:235], a[176:179], v[64:67]\n\t"
"buffer_load_dwordx4 a[216:219], v252, s[8:11], s20 offen offset:1024 sc1\n\t"
"v_mfma_f32_16x16x32_bf16 v[68:71], a[224:227], a[172:175], v[68:71]\n\t"
"ds_read_b128 a[168:171], v242 offset:9216\n\t"
"v_mfma_f32_16x16x32_bf16 v[68:71], a[228:231], a[176:179], v[68:71]\n\t"
"v_cvt_pk_bf16_f32 v230, v230, v231\n\t"
"v_cvt_pk_bf16_f32 v231, v232, v233\n\t"
"v_mfma_f32_16x16x32_bf16 v[68:71], a[232:235], a[180:183], v[68:71]\n\t"
"buffer_load_dwordx4 a[220:223], v252, s[8:11], s20 offen offset:2048 sc1\n\t"
"v_mfma_f32_16x16x32_bf16 v[72:75], a[224:227], a[176:179], v[72:75]\n\t"
"ds_read_b128 a[172:175], v242 offset:9728\n\t"
"v_mfma_f32_16x16x32_bf16 v[72:75], a[228:231], a[180:183], v[72:75]\n\t"
"ds_read_b128 a[176:179], v242 offset:10240\n\t"
"v_mfma_f32_16x16x32_bf16 v[72:75], a[232:235], a[184:187], v[72:75]\n\t"
"s_add_u32 m0, s24, 80768\n\t"
"s_add_u32 s28, s21, 2359296\n\t"
"buffer_load_dwordx4 v245, s[12:15], s28 offen offset:128 nt lds\n\t"
"s_add_u32 m0, s24, 86912\n\t"
"s_add_u32 s28, s21, 2555904\n\t"
"buffer_load_dwordx4 v245, s[12:15], s28 offen offset:128 nt lds\n\t"
"s_waitcnt lgkmcnt(5)\n\t"
"v_mfma_f32_16x16x32_bf16 v[234:237], v[234:237], v[218:221], 0\n\t"
"v_mfma_f32_16x16x32_bf16 v[76:79], a[224:227], a[180:183], v[76:79]\n\t"
"ds_read_b128 a[180:183], v242 offset:10752\n\t"
"v_mfma_f32_16x16x32_bf16 v[76:79], a[228:231], a[184:187], v[76:79]\n\t"
"v_mfma_f32_16x16x32_bf16 v[76:79], a[232:235], a[188:191], v[76:79]\n\t"
"s_add_u32 m0, s24, 93056\n\t"
"s_add_u32 s28, s21, 2752512\n\t"
"buffer_load_dwordx4 v245, s[12:15], s28 offen offset:128 nt lds\n\t"
"s_add_u32 m0, s24, 100736\n\t"
"s_add_u32 s28, s21, 2949120\n\t"
"buffer_load_dwordx4 v245, s[12:15], s28 offen offset:128 nt lds\n\t"
"v_mfma_f32_16x16x32_bf16 v[80:83], a[224:227], a[184:187], v[80:83]\n\t"
"ds_read_b128 a[184:187], v242 offset:11264\n\t"
"v_mfma_f32_16x16x32_bf16 v[80:83], a[228:231], a[188:191], v[80:83]\n\t"
"v_cvt_pk_bf16_f32 v234, v234, v235\n\t"
"v_cvt_pk_bf16_f32 v235, v236, v237\n\t"
"v_mfma_f32_16x16x32_bf16 v[80:83], a[232:235], a[192:195], v[80:83]\n\t"
"s_add_u32 m0, s24, 106880\n\t"
"s_add_u32 s28, s21, 3145728\n\t"
"buffer_load_dwordx4 v245, s[12:15], s28 offen offset:128 nt lds\n\t"
"s_waitcnt lgkmcnt(5)\n\t"
"v_mfma_f32_16x16x32_bf16 v[222:225], v[222:225], v[218:221], 0\n\t"
"s_add_u32 m0, s24, 113024\n\t"
"s_add_u32 s28, s21, 3342336\n\t"
"buffer_load_dwordx4 v245, s[12:15], s28 offen offset:128 nt lds\n\t"
"v_mfma_f32_16x16x32_bf16 v[84:87], a[224:227], a[188:191], v[84:87]\n\t"
"ds_read_b128 a[188:191], v242 offset:11776\n\t"
"v_mfma_f32_16x16x32_bf16 v[84:87], a[228:231], a[192:195], v[84:87]\n\t"
"v_mfma_f32_16x16x32_bf16 v[84:87], a[232:235], a[196:199], v[84:87]\n\t"
"v_mfma_f32_16x16x32_bf16 v[88:91], a[224:227], a[192:195], v[88:91]\n\t"
"ds_read_b128 a[192:195], v242 offset:12288\n\t"
"v_mfma_f32_16x16x32_bf16 v[88:91], a[228:231], a[196:199], v[88:91]\n\t"
"v_cvt_pk_bf16_f32 v222, v222, v223\n\t"
"v_mfma_f32_16x16x32_bf16 v[88:91], a[232:235], a[200:203], v[88:91]\n\t"
"v_cvt_pk_bf16_f32 v223, v224, v225\n\t"
"v_mfma_f32_16x16x32_bf16 v[92:95], a[224:227], a[196:199], v[92:95]\n\t"
"ds_read_b128 a[196:199], v242 offset:12800\n\t"
"v_mfma_f32_16x16x32_bf16 v[92:95], a[228:231], a[200:203], v[92:95]\n\t"
"ds_read_b128 a[200:203], v242 offset:13312\n\t"
"v_mfma_f32_16x16x32_bf16 v[92:95], a[232:235], a[204:207], v[92:95]\n\t"
"ds_read_b128 a[204:207], v242 offset:13824\n\t"
"s_waitcnt vmcnt(12) lgkmcnt(7)\n\t"
"v_mfma_f32_16x16x32_bf16 v[98:101], a[244:247], a[168:171], v[98:101]\n\t"
"v_mfma_f32_16x16x32_bf16 v[98:101], a[248:251], a[172:175], v[98:101]\n\t"
"v_mfma_f32_16x16x32_bf16 v[98:101], a[252:255], a[176:179], v[98:101]\n\t"
"s_waitcnt lgkmcnt(6)\n\t"
"v_mfma_f32_16x16x32_bf16 v[102:105], a[244:247], a[172:175], v[102:105]\n\t"
"v_mfma_f32_16x16x32_bf16 v[102:105], a[248:251], a[176:179], v[102:105]\n\t"
"v_mfma_f32_16x16x32_bf16 v[102:105], a[252:255], a[180:183], v[102:105]\n\t"
"s_waitcnt lgkmcnt(0)\n\t"
"s_barrier\n\t"
"v_mfma_f32_16x16x32_bf16 v[106:109], a[244:247], a[176:179], v[106:109]\n\t"
"v_mfma_f32_16x16x32_bf16 v[106:109], a[248:251], a[180:183], v[106:109]\n\t"
"v_mfma_f32_16x16x32_bf16 v[106:109], a[252:255], a[184:187], v[106:109]\n\t"
"ds_write_b64 v243, v[226:227] offset:0\n\t"
"ds_write_b64 v243, v[230:231] offset:512\n\t"
"v_mfma_f32_16x16x32_bf16 v[110:113], a[244:247], a[180:183], v[110:113]\n\t"
"ds_read_b64_tr_b16 v[226:227], v244 offset:26624\n\t"
"v_mfma_f32_16x16x32_bf16 v[110:113], a[248:251], a[184:187], v[110:113]\n\t"
"ds_read_b64_tr_b16 v[228:229], v244 offset:29696\n\t"
"v_mfma_f32_16x16x32_bf16 v[110:113], a[252:255], a[188:191], v[110:113]\n\t"
"ds_write_b64 v243, v[234:235] offset:1024\n\t"
"v_mfma_f32_16x16x32_bf16 v[114:117], a[244:247], a[184:187], v[114:117]\n\t"
"ds_read_b64_tr_b16 v[230:231], v244 offset:32768\n\t"
"v_mfma_f32_16x16x32_bf16 v[114:117], a[248:251], a[188:191], v[114:117]\n\t"
"ds_read_b64_tr_b16 v[232:233], v244 offset:35840\n\t"
"v_mfma_f32_16x16x32_bf16 v[114:117], a[252:255], a[192:195], v[114:117]\n\t"
"ds_write_b64 v243, v[222:223] offset:1536\n\t"
"v_mfma_f32_16x16x32_bf16 v[118:121], a[244:247], a[188:191], v[118:121]\n\t"
"ds_read_b64_tr_b16 v[234:235], v244 offset:40448\n\t"
"v_mfma_f32_16x16x32_bf16 v[118:121], a[248:251], a[192:195], v[118:121]\n\t"
"ds_read_b64_tr_b16 v[236:237], v244 offset:43520\n\t"
"v_mfma_f32_16x16x32_bf16 v[118:121], a[252:255], a[196:199], v[118:121]\n\t"
"s_mov_b32 s20, 331776\n\t"
"buffer_load_dwordx4 a[224:227], v252, s[8:11], s20 offen offset:0 sc1\n\t"
"s_waitcnt lgkmcnt(6)\n\t"
"v_mfma_f32_16x16x32_bf16 v[226:229], v[226:229], v[218:221], 0\n\t"
"ds_read_b64_tr_b16 v[222:223], v244 offset:46592\n\t"
"ds_read_b64_tr_b16 v[224:225], v244 offset:49664\n\t"
"v_mfma_f32_16x16x32_bf16 v[122:125], a[244:247], a[192:195], v[122:125]\n\t"
"v_mfma_f32_16x16x32_bf16 v[122:125], a[248:251], a[196:199], v[122:125]\n\t"
"v_mfma_f32_16x16x32_bf16 v[122:125], a[252:255], a[200:203], v[122:125]\n\t"
"s_waitcnt lgkmcnt(5)\n\t"
"v_mfma_f32_16x16x32_bf16 v[230:233], v[230:233], v[218:221], 0\n\t"
"v_mfma_f32_16x16x32_bf16 v[126:129], a[244:247], a[196:199], v[126:129]\n\t"
"v_cvt_pk_bf16_f32 v226, v226, v227\n\t"
"v_cvt_pk_bf16_f32 v227, v228, v229\n\t"
"v_mfma_f32_16x16x32_bf16 v[126:129], a[248:251], a[200:203], v[126:129]\n\t"
"ds_read_b128 v[238:241], v252 offset:18432\n\t"
"v_mfma_f32_16x16x32_bf16 v[126:129], a[252:255], a[204:207], v[126:129]\n\t"
"ds_write_b64 v243, v[226:227] offset:2048\n\t"
"s_waitcnt lgkmcnt(4)\n\t"
"v_mfma_f32_16x16x32_bf16 v[234:237], v[234:237], v[218:221], 0\n\t"
"ds_read_b64_tr_b16 v[226:227], v244 offset:52736\n\t"
"s_waitcnt vmcnt(10)\n\t"
"v_mfma_f32_16x16x32_bf16 v[130:133], a[236:239], a[168:171], v[130:133]\n\t"
"v_cvt_pk_bf16_f32 v230, v230, v231\n\t"
"v_cvt_pk_bf16_f32 v231, v232, v233\n\t"
"v_mfma_f32_16x16x32_bf16 v[130:133], a[208:211], a[172:175], v[130:133]\n\t"
"ds_read_b64_tr_b16 v[228:229], v244 offset:55808\n\t"
"v_mfma_f32_16x16x32_bf16 v[130:133], a[240:243], a[176:179], v[130:133]\n\t"
"s_waitcnt lgkmcnt(4)\n\t"
"v_mfma_f32_16x16x32_bf16 v[222:225], v[222:225], v[218:221], 0\n\t"
"ds_write_b64 v243, v[230:231] offset:2560\n\t"
"v_mfma_f32_16x16x32_bf16 v[134:137], a[236:239], a[172:175], v[134:137]\n\t"
"v_cvt_pk_bf16_f32 v234, v234, v235\n\t"
"v_cvt_pk_bf16_f32 v235, v236, v237\n\t"
"v_mfma_f32_16x16x32_bf16 v[134:137], a[208:211], a[176:179], v[134:137]\n\t"
"v_mfma_f32_16x16x32_bf16 v[134:137], a[240:243], a[180:183], v[134:137]\n\t"
"ds_write_b64 v243, v[234:235] offset:3072\n\t"
"v_mfma_f32_16x16x32_bf16 v[138:141], a[236:239], a[176:179], v[138:141]\n\t"
"v_mfma_f32_16x16x32_bf16 v[138:141], a[208:211], a[180:183], v[138:141]\n\t"
"v_cvt_pk_bf16_f32 v222, v222, v223\n\t"
"v_cvt_pk_bf16_f32 v223, v224, v225\n\t"
"v_mfma_f32_16x16x32_bf16 v[138:141], a[240:243], a[184:187], v[138:141]\n\t"
"v_mfma_f32_16x16x32_bf16 v[142:145], a[236:239], a[180:183], v[142:145]\n\t"
"v_mfma_f32_16x16x32_bf16 v[142:145], a[208:211], a[184:187], v[142:145]\n\t"
"v_mfma_f32_16x16x32_bf16 v[142:145], a[240:243], a[188:191], v[142:145]\n\t"
"buffer_load_dwordx4 a[228:231], v252, s[8:11], s20 offen offset:1024 sc1\n\t"
"s_waitcnt lgkmcnt(2)\n\t"
"v_mfma_f32_16x16x32_bf16 v[226:229], v[226:229], v[218:221], 0\n\t"
"s_add_u32 s29, s23, 787584\n\t"
"buffer_store_dwordx4 v[238:241], v247, s[16:19], s29 offen offset:0 nt\n\t"
"v_mfma_f32_16x16x32_bf16 v[146:149], a[236:239], a[184:187], v[146:149]\n\t"
"v_mfma_f32_16x16x32_bf16 v[146:149], a[208:211], a[188:191], v[146:149]\n\t"
"v_mfma_f32_16x16x32_bf16 v[146:149], a[240:243], a[192:195], v[146:149]\n\t"
"ds_write_b64 v243, v[222:223] offset:3584\n\t"
"buffer_load_dwordx4 a[232:235], v252, s[8:11], s20 offen offset:2048 sc1\n\t"
"v_mfma_f32_16x16x32_bf16 v[150:153], a[236:239], a[188:191], v[150:153]\n\t"
"v_mfma_f32_16x16x32_bf16 v[150:153], a[208:211], a[192:195], v[150:153]\n\t"
"v_cvt_pk_bf16_f32 v226, v226, v227\n\t"
"v_cvt_pk_bf16_f32 v227, v228, v229\n\t"
"v_mfma_f32_16x16x32_bf16 v[150:153], a[240:243], a[196:199], v[150:153]\n\t"
"s_mov_b32 s20, 344064\n\t"
"buffer_load_dwordx4 a[244:247], v252, s[8:11], s20 offen offset:0 sc1\n\t"
"v_mfma_f32_16x16x32_bf16 v[154:157], a[236:239], a[192:195], v[154:157]\n\t"
"ds_read_b128 v[238:241], v252 offset:19456\n\t"
"v_mfma_f32_16x16x32_bf16 v[154:157], a[208:211], a[196:199], v[154:157]\n\t"
"v_mfma_f32_16x16x32_bf16 v[154:157], a[240:243], a[200:203], v[154:157]\n\t"
"ds_write_b64 v243, v[226:227] offset:4096\n\t"
"v_mfma_f32_16x16x32_bf16 v[158:161], a[236:239], a[196:199], v[158:161]\n\t"
"v_mfma_f32_16x16x32_bf16 v[158:161], a[208:211], a[200:203], v[158:161]\n\t"
"v_mfma_f32_16x16x32_bf16 v[158:161], a[240:243], a[204:207], v[158:161]\n\t"
"buffer_load_dwordx4 a[248:251], v252, s[8:11], s20 offen offset:1024 sc1\n\t"
"s_waitcnt vmcnt(12)\n\t"
"v_mfma_f32_16x16x32_bf16 v[162:165], a[212:215], a[168:171], v[162:165]\n\t"
"v_mfma_f32_16x16x32_bf16 v[162:165], a[216:219], a[172:175], v[162:165]\n\t"
"v_mfma_f32_16x16x32_bf16 v[162:165], a[220:223], a[176:179], v[162:165]\n\t"
"buffer_load_dwordx4 a[208:211], v252, s[8:11], s20 offen offset:2048 sc1\n\t"
"s_waitcnt lgkmcnt(0)\n\t"
"s_barrier\n\t"
"v_mfma_f32_16x16x32_bf16 v[166:169], a[212:215], a[172:175], v[166:169]\n\t"
"ds_read_b128 a[168:171], v242 offset:0\n\t"
"v_mfma_f32_16x16x32_bf16 v[166:169], a[216:219], a[176:179], v[166:169]\n\t"
"ds_read_b128 a[172:175], v242 offset:512\n\t"
"v_mfma_f32_16x16x32_bf16 v[166:169], a[220:223], a[180:183], v[166:169]\n\t"
"s_mov_b32 s20, 356352\n\t"
"buffer_load_dwordx4 a[236:239], v252, s[8:11], s20 offen offset:0 sc1\n\t"
"v_mfma_f32_16x16x32_bf16 v[170:173], a[212:215], a[176:179], v[170:173]\n\t"
"ds_read_b128 a[176:179], v242 offset:1024\n\t"
"v_mfma_f32_16x16x32_bf16 v[170:173], a[216:219], a[180:183], v[170:173]\n\t"
"v_mfma_f32_16x16x32_bf16 v[170:173], a[220:223], a[184:187], v[170:173]\n\t"
"s_add_u32 s29, s23, 787584\n\t"
"buffer_store_dwordx4 v[238:241], v247, s[16:19], s29 offen offset:128 nt\n\t"
"v_mfma_f32_16x16x32_bf16 v[174:177], a[212:215], a[180:183], v[174:177]\n\t"
"ds_read_b128 a[180:183], v242 offset:1536\n\t"
"v_mfma_f32_16x16x32_bf16 v[174:177], a[216:219], a[184:187], v[174:177]\n\t"
"v_mfma_f32_16x16x32_bf16 v[174:177], a[220:223], a[188:191], v[174:177]\n\t"
"buffer_load_dwordx4 a[240:243], v252, s[8:11], s20 offen offset:1024 sc1\n\t"
"v_mfma_f32_16x16x32_bf16 v[178:181], a[212:215], a[184:187], v[178:181]\n\t"
"ds_read_b128 a[184:187], v242 offset:2048\n\t"
"v_mfma_f32_16x16x32_bf16 v[178:181], a[216:219], a[188:191], v[178:181]\n\t"
"v_mfma_f32_16x16x32_bf16 v[178:181], a[220:223], a[192:195], v[178:181]\n\t"
"buffer_load_dwordx4 a[252:255], v252, s[8:11], s20 offen offset:2048 sc1\n\t"
"v_mfma_f32_16x16x32_bf16 v[182:185], a[212:215], a[188:191], v[182:185]\n\t"
"ds_read_b128 v[238:241], v252 offset:20480\n\t"
"v_mfma_f32_16x16x32_bf16 v[182:185], a[216:219], a[192:195], v[182:185]\n\t"
"ds_read_b128 a[188:191], v242 offset:2560\n\t"
"v_mfma_f32_16x16x32_bf16 v[182:185], a[220:223], a[196:199], v[182:185]\n\t"
"v_mfma_f32_16x16x32_bf16 v[186:189], a[212:215], a[192:195], v[186:189]\n\t"
"ds_read_b128 a[192:195], v242 offset:3072\n\t"
"v_mfma_f32_16x16x32_bf16 v[186:189], a[216:219], a[196:199], v[186:189]\n\t"
"v_mfma_f32_16x16x32_bf16 v[186:189], a[220:223], a[200:203], v[186:189]\n\t"
"v_mfma_f32_16x16x32_bf16 v[190:193], a[212:215], a[196:199], v[190:193]\n\t"
"ds_read_b128 a[196:199], v242 offset:3584\n\t"
"v_mfma_f32_16x16x32_bf16 v[190:193], a[216:219], a[200:203], v[190:193]\n\t"
"ds_read_b128 a[200:203], v242 offset:4096\n\t"
"v_mfma_f32_16x16x32_bf16 v[190:193], a[220:223], a[204:207], v[190:193]\n\t"
"s_mov_b32 s20, 368640\n\t"
"buffer_load_dwordx4 a[212:215], v252, s[8:11], s20 offen offset:0 sc1\n\t"
"s_waitcnt vmcnt(8) lgkmcnt(7)\n\t"
"v_mfma_f32_16x16x32_bf16 v[194:197], a[224:227], a[168:171], v[194:197]\n\t"
"ds_read_b128 a[204:207], v242 offset:4608\n\t"
"v_mfma_f32_16x16x32_bf16 v[194:197], a[228:231], a[172:175], v[194:197]\n\t"
"v_mfma_f32_16x16x32_bf16 v[194:197], a[232:235], a[176:179], v[194:197]\n\t"
"buffer_load_dwordx4 a[216:219], v252, s[8:11], s20 offen offset:1024 sc1\n\t"
"s_waitcnt lgkmcnt(7)\n\t"
"v_mfma_f32_16x16x32_bf16 v[202:205], a[224:227], a[172:175], v[202:205]\n\t"
"v_mfma_f32_16x16x32_bf16 v[202:205], a[228:231], a[176:179], v[202:205]\n\t"
"v_mfma_f32_16x16x32_bf16 v[202:205], a[232:235], a[180:183], v[202:205]\n\t"
"buffer_load_dwordx4 a[220:223], v252, s[8:11], s20 offen offset:2048 sc1\n\t"
"s_waitcnt lgkmcnt(6)\n\t"
"v_mfma_f32_16x16x32_bf16 v[210:213], a[224:227], a[176:179], v[210:213]\n\t"
"v_mfma_f32_16x16x32_bf16 v[210:213], a[228:231], a[180:183], v[210:213]\n\t"
"v_mfma_f32_16x16x32_bf16 v[210:213], a[232:235], a[184:187], v[210:213]\n\t"
"s_add_u32 s29, s23, 787584\n\t"
"s_waitcnt lgkmcnt(5)\n\t"
"buffer_store_dwordx4 v[238:241], v247, s[16:19], s29 offen offset:256 nt\n\t"
"s_waitcnt lgkmcnt(4)\n\t"
"v_mfma_f32_16x16x32_bf16 a[0:3], a[224:227], a[180:183], a[0:3]\n\t"
"v_mfma_f32_16x16x32_bf16 a[0:3], a[228:231], a[184:187], a[0:3]\n\t"
"v_mfma_f32_16x16x32_bf16 a[0:3], a[232:235], a[188:191], a[0:3]\n\t"
"s_waitcnt lgkmcnt(3)\n\t"
"v_mfma_f32_16x16x32_bf16 a[8:11], a[224:227], a[184:187], a[8:11]\n\t"
"ds_read_b128 v[238:241], v252 offset:30720\n\t"
"v_mfma_f32_16x16x32_bf16 a[8:11], a[228:231], a[188:191], a[8:11]\n\t"
"v_mfma_f32_16x16x32_bf16 a[8:11], a[232:235], a[192:195], a[8:11]\n\t"
"s_waitcnt lgkmcnt(3)\n\t"
"v_mfma_f32_16x16x32_bf16 a[16:19], a[224:227], a[188:191], a[16:19]\n\t"
"v_mfma_f32_16x16x32_bf16 a[16:19], a[228:231], a[192:195], a[16:19]\n\t"
"v_mfma_f32_16x16x32_bf16 a[16:19], a[232:235], a[196:199], a[16:19]\n\t"
"s_waitcnt lgkmcnt(2)\n\t"
"v_mfma_f32_16x16x32_bf16 a[24:27], a[224:227], a[192:195], a[24:27]\n\t"
"v_mfma_f32_16x16x32_bf16 a[24:27], a[228:231], a[196:199], a[24:27]\n\t"
"v_mfma_f32_16x16x32_bf16 a[24:27], a[232:235], a[200:203], a[24:27]\n\t"
"s_waitcnt lgkmcnt(1)\n\t"
"v_mfma_f32_16x16x32_bf16 a[32:35], a[224:227], a[196:199], a[32:35]\n\t"
"v_mfma_f32_16x16x32_bf16 a[32:35], a[228:231], a[200:203], a[32:35]\n\t"
"v_mfma_f32_16x16x32_bf16 a[32:35], a[232:235], a[204:207], a[32:35]\n\t"
"s_mov_b32 s20, 380928\n\t"
"buffer_load_dwordx4 a[224:227], v252, s[8:11], s20 offen offset:0 sc1\n\t"
"s_waitcnt vmcnt(9)\n\t"
"v_mfma_f32_16x16x32_bf16 a[40:43], a[244:247], a[168:171], a[40:43]\n\t"
"v_mfma_f32_16x16x32_bf16 a[40:43], a[248:251], a[172:175], a[40:43]\n\t"
"v_mfma_f32_16x16x32_bf16 a[40:43], a[208:211], a[176:179], a[40:43]\n\t"
"buffer_load_dwordx4 a[228:231], v252, s[8:11], s20 offen offset:1024 sc1\n\t"
"v_mfma_f32_16x16x32_bf16 a[48:51], a[244:247], a[172:175], a[48:51]\n\t"
"v_mfma_f32_16x16x32_bf16 a[48:51], a[248:251], a[176:179], a[48:51]\n\t"
"v_mfma_f32_16x16x32_bf16 a[48:51], a[208:211], a[180:183], a[48:51]\n\t"
"s_add_u32 s29, s23, 1180800\n\t"
"s_waitcnt lgkmcnt(0)\n\t"
"buffer_store_dwordx4 v[238:241], v247, s[16:19], s29 offen offset:0 nt\n\t"
"v_mfma_f32_16x16x32_bf16 a[56:59], a[244:247], a[176:179], a[56:59]\n\t"
"v_mfma_f32_16x16x32_bf16 a[56:59], a[248:251], a[180:183], a[56:59]\n\t"
"v_mfma_f32_16x16x32_bf16 a[56:59], a[208:211], a[184:187], a[56:59]\n\t"
"buffer_load_dwordx4 a[232:235], v252, s[8:11], s20 offen offset:2048 sc1\n\t"
"v_mfma_f32_16x16x32_bf16 a[64:67], a[244:247], a[180:183], a[64:67]\n\t"
"v_mfma_f32_16x16x32_bf16 a[64:67], a[248:251], a[184:187], a[64:67]\n\t"
"ds_read_b128 v[238:241], v252 offset:31744\n\t"
"v_mfma_f32_16x16x32_bf16 a[64:67], a[208:211], a[188:191], a[64:67]\n\t"
"s_barrier\n\t"
"v_mfma_f32_16x16x32_bf16 a[72:75], a[244:247], a[184:187], a[72:75]\n\t"
"ds_read_b64_tr_b16 v[230:231], v244 offset:1024\n\t"
"v_mfma_f32_16x16x32_bf16 a[72:75], a[248:251], a[188:191], a[72:75]\n\t"
"ds_read_b64_tr_b16 v[232:233], v244 offset:4096\n\t"
"v_mfma_f32_16x16x32_bf16 a[72:75], a[208:211], a[192:195], a[72:75]\n\t"
"ds_read_b64_tr_b16 v[234:235], v244 offset:7168\n\t"
"ds_read_b64_tr_b16 v[236:237], v244 offset:10240\n\t"
"v_mfma_f32_16x16x32_bf16 a[80:83], a[244:247], a[188:191], a[80:83]\n\t"
"v_mfma_f32_16x16x32_bf16 a[80:83], a[248:251], a[192:195], a[80:83]\n\t"
"v_mfma_f32_16x16x32_bf16 a[80:83], a[208:211], a[196:199], a[80:83]\n\t"
"v_mfma_f32_16x16x32_bf16 a[88:91], a[244:247], a[192:195], a[88:91]\n\t"
"v_mfma_f32_16x16x32_bf16 a[88:91], a[248:251], a[196:199], a[88:91]\n\t"
"ds_read_b64_tr_b16 v[222:223], v244 offset:13312\n\t"
"v_mfma_f32_16x16x32_bf16 a[88:91], a[208:211], a[200:203], a[88:91]\n\t"
"ds_read_b64_tr_b16 v[224:225], v244 offset:16384\n\t"
"s_waitcnt lgkmcnt(4)\n\t"
"v_mfma_f32_16x16x32_bf16 v[230:233], v[230:233], v[218:221], 0\n\t"
"v_mfma_f32_16x16x32_bf16 a[96:99], a[244:247], a[196:199], a[96:99]\n\t"
"v_mfma_f32_16x16x32_bf16 a[96:99], a[248:251], a[200:203], a[96:99]\n\t"
"v_mfma_f32_16x16x32_bf16 a[96:99], a[208:211], a[204:207], a[96:99]\n\t"
"s_mov_b32 s20, 393216\n\t"
"buffer_load_dwordx4 a[244:247], v252, s[8:11], s20 offen offset:0 sc1\n\t"
"s_waitcnt vmcnt(9)\n\t"
"v_mfma_f32_16x16x32_bf16 a[104:107], a[236:239], a[168:171], a[104:107]\n\t"
"ds_read_b128 a[168:171], v242 offset:9216\n\t"
"v_mfma_f32_16x16x32_bf16 a[104:107], a[240:243], a[172:175], a[104:107]\n\t"
"v_cvt_pk_bf16_f32 v230, v230, v231\n\t"
"v_cvt_pk_bf16_f32 v231, v232, v233\n\t"
"v_mfma_f32_16x16x32_bf16 a[104:107], a[252:255], a[176:179], a[104:107]\n\t"
"s_add_u32 s29, s23, 1180800\n\t"
"buffer_store_dwordx4 v[238:241], v247, s[16:19], s29 offen offset:128 nt\n\t"
"s_waitcnt lgkmcnt(3)\n\t"
"v_mfma_f32_16x16x32_bf16 v[234:237], v[234:237], v[218:221], 0\n\t"
"buffer_load_dwordx4 a[248:251], v252, s[8:11], s20 offen offset:1024 sc1\n\t"
"v_mfma_f32_16x16x32_bf16 a[112:115], a[236:239], a[172:175], a[112:115]\n\t"
"ds_read_b64_tr_b16 v[226:227], v244 offset:20992\n\t"
"v_mfma_f32_16x16x32_bf16 a[112:115], a[240:243], a[176:179], a[112:115]\n\t"
"ds_read_b64_tr_b16 v[228:229], v244 offset:24064\n\t"
"v_mfma_f32_16x16x32_bf16 a[112:115], a[252:255], a[180:183], a[112:115]\n\t"
"ds_read_b128 a[172:175], v242 offset:9728\n\t"
"v_mfma_f32_16x16x32_bf16 a[120:123], a[236:239], a[176:179], a[120:123]\n\t"
"v_mfma_f32_16x16x32_bf16 a[120:123], a[240:243], a[180:183], a[120:123]\n\t"
"v_cvt_pk_bf16_f32 v234, v234, v235\n\t"
"v_cvt_pk_bf16_f32 v235, v236, v237\n\t"
"v_mfma_f32_16x16x32_bf16 a[120:123], a[252:255], a[184:187], a[120:123]\n\t"
"ds_read_b128 v[238:241], v252 offset:32768\n\t"
"s_waitcnt lgkmcnt(5)\n\t"
"v_mfma_f32_16x16x32_bf16 v[222:225], v[222:225], v[218:221], 0\n\t"
"ds_read_b128 a[176:179], v242 offset:10240\n\t"
"v_mfma_f32_16x16x32_bf16 a[128:131], a[236:239], a[180:183], a[128:131]\n\t"
"ds_read_b128 a[180:183], v242 offset:10752\n\t"
"v_mfma_f32_16x16x32_bf16 a[128:131], a[240:243], a[184:187], a[128:131]\n\t"
"v_mfma_f32_16x16x32_bf16 a[128:131], a[252:255], a[188:191], a[128:131]\n\t"
"v_mfma_f32_16x16x32_bf16 a[136:139], a[236:239], a[184:187], a[136:139]\n\t"
"ds_read_b128 a[184:187], v242 offset:11264\n\t"
"v_mfma_f32_16x16x32_bf16 a[136:139], a[240:243], a[188:191], a[136:139]\n\t"
"v_cvt_pk_bf16_f32 v222, v222, v223\n\t"
"v_mfma_f32_16x16x32_bf16 a[136:139], a[252:255], a[192:195], a[136:139]\n\t"
"v_cvt_pk_bf16_f32 v223, v224, v225\n\t"
"s_waitcnt lgkmcnt(5)\n\t"
"v_mfma_f32_16x16x32_bf16 v[226:229], v[226:229], v[218:221], 0\n\t"
"v_mfma_f32_16x16x32_bf16 a[144:147], a[236:239], a[188:191], a[144:147]\n\t"
"ds_read_b128 a[188:191], v242 offset:11776\n\t"
"v_mfma_f32_16x16x32_bf16 a[144:147], a[240:243], a[192:195], a[144:147]\n\t"
"v_mfma_f32_16x16x32_bf16 a[144:147], a[252:255], a[196:199], a[144:147]\n\t"
"s_add_u32 s29, s23, 1180800\n\t"
"s_waitcnt lgkmcnt(4)\n\t"
"buffer_store_dwordx4 v[238:241], v247, s[16:19], s29 offen offset:256 nt\n\t"
"v_mfma_f32_16x16x32_bf16 a[152:155], a[236:239], a[192:195], a[152:155]\n\t"
"v_mfma_f32_16x16x32_bf16 a[152:155], a[240:243], a[196:199], a[152:155]\n\t"
"ds_read_b128 a[192:195], v242 offset:12288\n\t"
"v_cvt_pk_bf16_f32 v226, v226, v227\n\t"
"v_mfma_f32_16x16x32_bf16 a[152:155], a[252:255], a[200:203], a[152:155]\n\t"
"v_cvt_pk_bf16_f32 v227, v228, v229\n\t"
"v_mfma_f32_16x16x32_bf16 a[160:163], a[236:239], a[196:199], a[160:163]\n\t"
"ds_read_b128 a[196:199], v242 offset:12800\n\t"
"v_mfma_f32_16x16x32_bf16 a[160:163], a[240:243], a[200:203], a[160:163]\n\t"
"ds_read_b128 a[200:203], v242 offset:13312\n\t"
"v_mfma_f32_16x16x32_bf16 a[160:163], a[252:255], a[204:207], a[160:163]\n\t"
"buffer_load_dwordx4 a[236:239], v252, s[8:11], s20 offen offset:2048 sc1\n\t"
"s_waitcnt vmcnt(10) lgkmcnt(6)\n\t"
"v_mfma_f32_16x16x32_bf16 v[198:201], a[212:215], a[168:171], v[198:201]\n\t"
"ds_read_b128 a[204:207], v242 offset:13824\n\t"
"v_mfma_f32_16x16x32_bf16 v[198:201], a[216:219], a[172:175], v[198:201]\n\t"
"v_mfma_f32_16x16x32_bf16 v[198:201], a[220:223], a[176:179], v[198:201]\n\t"
"s_mov_b32 s20, 552960\n\t"
"buffer_load_dwordx4 a[240:243], v252, s[8:11], s20 offen offset:0 sc1\n\t"
"s_waitcnt lgkmcnt(6)\n\t"
"v_mfma_f32_16x16x32_bf16 v[206:209], a[212:215], a[172:175], v[206:209]\n\t"
"v_mfma_f32_16x16x32_bf16 v[206:209], a[216:219], a[176:179], v[206:209]\n\t"
"v_mfma_f32_16x16x32_bf16 v[206:209], a[220:223], a[180:183], v[206:209]\n\t"
"buffer_load_dwordx4 a[252:255], v252, s[8:11], s20 offen offset:1024 sc1\n\t"
"s_waitcnt lgkmcnt(0)\n\t"
"s_barrier\n\t"
"v_mfma_f32_16x16x32_bf16 v[214:217], a[212:215], a[176:179], v[214:217]\n\t"
"v_mfma_f32_16x16x32_bf16 v[214:217], a[216:219], a[180:183], v[214:217]\n\t"
"v_mfma_f32_16x16x32_bf16 v[214:217], a[220:223], a[184:187], v[214:217]\n\t"
"ds_write_b64 v243, v[230:231] offset:0\n\t"
"ds_write_b64 v243, v[234:235] offset:512\n\t"
"v_mfma_f32_16x16x32_bf16 a[4:7], a[212:215], a[180:183], a[4:7]\n\t"
"ds_read_b64_tr_b16 v[230:231], v244 offset:27136\n\t"
"v_mfma_f32_16x16x32_bf16 a[4:7], a[216:219], a[184:187], a[4:7]\n\t"
"ds_read_b64_tr_b16 v[232:233], v244 offset:30208\n\t"
"v_mfma_f32_16x16x32_bf16 a[4:7], a[220:223], a[188:191], a[4:7]\n\t"
"ds_write_b64 v243, v[222:223] offset:1024\n\t"
"v_mfma_f32_16x16x32_bf16 a[12:15], a[212:215], a[184:187], a[12:15]\n\t"
"ds_read_b64_tr_b16 v[234:235], v244 offset:33280\n\t"
"v_mfma_f32_16x16x32_bf16 a[12:15], a[216:219], a[188:191], a[12:15]\n\t"
"ds_read_b64_tr_b16 v[236:237], v244 offset:36352\n\t"
"v_mfma_f32_16x16x32_bf16 a[12:15], a[220:223], a[192:195], a[12:15]\n\t"
"ds_write_b64 v243, v[226:227] offset:1536\n\t"
"v_mfma_f32_16x16x32_bf16 a[20:23], a[212:215], a[188:191], a[20:23]\n\t"
"ds_read_b64_tr_b16 v[222:223], v244 offset:40960\n\t"
"v_mfma_f32_16x16x32_bf16 a[20:23], a[216:219], a[192:195], a[20:23]\n\t"
"ds_read_b64_tr_b16 v[224:225], v244 offset:44032\n\t"
"v_mfma_f32_16x16x32_bf16 a[20:23], a[220:223], a[196:199], a[20:23]\n\t"
"global_load_dwordx4 v[238:241], v252, s[46:47] offset:0\n\t"
"s_waitcnt lgkmcnt(6)\n\t"
"v_mfma_f32_16x16x32_bf16 v[230:233], v[230:233], v[218:221], 0\n\t"
"ds_read_b64_tr_b16 v[226:227], v244 offset:47104\n\t"
"ds_read_b64_tr_b16 v[228:229], v244 offset:50176\n\t"
"v_mfma_f32_16x16x32_bf16 a[28:31], a[212:215], a[192:195], a[28:31]\n\t"
"v_mfma_f32_16x16x32_bf16 a[28:31], a[216:219], a[196:199], a[28:31]\n\t"
"v_mfma_f32_16x16x32_bf16 a[28:31], a[220:223], a[200:203], a[28:31]\n\t"
"global_load_dwordx4 a[208:211], v252, s[46:47] offset:1024\n\t"
"s_waitcnt lgkmcnt(5)\n\t"
"v_mfma_f32_16x16x32_bf16 v[234:237], v[234:237], v[218:221], 0\n\t"
"v_mfma_f32_16x16x32_bf16 a[36:39], a[212:215], a[196:199], a[36:39]\n\t"
"v_cvt_pk_bf16_f32 v230, v230, v231\n\t"
"v_cvt_pk_bf16_f32 v231, v232, v233\n\t"
"v_mfma_f32_16x16x32_bf16 a[36:39], a[216:219], a[200:203], a[36:39]\n\t"
"v_mfma_f32_16x16x32_bf16 a[36:39], a[220:223], a[204:207], a[36:39]\n\t"
"buffer_load_dwordx4 a[212:215], v252, s[8:11], s20 offen offset:2048 sc1\n\t"
"s_waitcnt lgkmcnt(2)\n\t"
"v_mfma_f32_16x16x32_bf16 v[222:225], v[222:225], v[218:221], 0\n\t"
"s_mov_b32 s20, 565248\n\t"
"buffer_load_dwordx4 a[216:219], v252, s[8:11], s20 offen offset:0 sc1\n\t"
"s_waitcnt vmcnt(11)\n\t"
"v_mfma_f32_16x16x32_bf16 a[44:47], a[224:227], a[168:171], a[44:47]\n\t"
"v_cvt_pk_bf16_f32 v234, v234, v235\n\t"
"v_cvt_pk_bf16_f32 v235, v236, v237\n\t"
"v_mfma_f32_16x16x32_bf16 a[44:47], a[228:231], a[172:175], a[44:47]\n\t"
"v_mfma_f32_16x16x32_bf16 a[44:47], a[232:235], a[176:179], a[44:47]\n\t"
"ds_write_b64 v243, v[230:231] offset:2048\n\t"
"s_waitcnt lgkmcnt(1)\n\t"
"v_mfma_f32_16x16x32_bf16 v[226:229], v[226:229], v[218:221], 0\n\t"
"ds_read_b64_tr_b16 v[230:231], v244 offset:53248\n\t"
"ds_read_b64_tr_b16 v[232:233], v244 offset:56320\n\t"
"v_mfma_f32_16x16x32_bf16 a[52:55], a[224:227], a[172:175], a[52:55]\n\t"
"v_cvt_pk_bf16_f32 v222, v222, v223\n\t"
"v_cvt_pk_bf16_f32 v223, v224, v225\n\t"
"v_mfma_f32_16x16x32_bf16 a[52:55], a[228:231], a[176:179], a[52:55]\n\t"
"v_mfma_f32_16x16x32_bf16 a[52:55], a[232:235], a[180:183], a[52:55]\n\t"
"buffer_load_dwordx4 a[220:223], v252, s[8:11], s20 offen offset:1024 sc1\n\t"
"v_mfma_f32_16x16x32_bf16 a[60:63], a[224:227], a[176:179], a[60:63]\n\t"
"v_mfma_f32_16x16x32_bf16 a[60:63], a[228:231], a[180:183], a[60:63]\n\t"
"v_cvt_pk_bf16_f32 v226, v226, v227\n\t"
"v_cvt_pk_bf16_f32 v227, v228, v229\n\t"
"v_mfma_f32_16x16x32_bf16 a[60:63], a[232:235], a[184:187], a[60:63]\n\t"
"ds_write_b64 v243, v[234:235] offset:2560\n\t"
"v_mfma_f32_16x16x32_bf16 a[68:71], a[224:227], a[180:183], a[68:71]\n\t"
"v_mfma_f32_16x16x32_bf16 a[68:71], a[228:231], a[184:187], a[68:71]\n\t"
"v_mfma_f32_16x16x32_bf16 a[68:71], a[232:235], a[188:191], a[68:71]\n\t"
"ds_write_b64 v243, v[222:223] offset:3072\n\t"
"s_waitcnt lgkmcnt(2)\n\t"
"v_mfma_f32_16x16x32_bf16 v[230:233], v[230:233], v[218:221], 0\n\t"
"ds_write_b64 v243, v[226:227] offset:3584\n\t"
"v_mfma_f32_16x16x32_bf16 a[76:79], a[224:227], a[184:187], a[76:79]\n\t"
"v_mfma_f32_16x16x32_bf16 a[76:79], a[228:231], a[188:191], a[76:79]\n\t"
"v_mfma_f32_16x16x32_bf16 a[76:79], a[232:235], a[192:195], a[76:79]\n\t"
"v_mfma_f32_16x16x32_bf16 a[84:87], a[224:227], a[188:191], a[84:87]\n\t"
"v_mfma_f32_16x16x32_bf16 a[84:87], a[228:231], a[192:195], a[84:87]\n\t"
"s_nop 1\n\t"
"v_cvt_pk_bf16_f32 v230, v230, v231\n\t"
"v_cvt_pk_bf16_f32 v231, v232, v233\n\t"
"v_mfma_f32_16x16x32_bf16 a[84:87], a[232:235], a[196:199], a[84:87]\n\t"
"v_mfma_f32_16x16x32_bf16 a[92:95], a[224:227], a[192:195], a[92:95]\n\t"
"v_mfma_f32_16x16x32_bf16 a[92:95], a[228:231], a[196:199], a[92:95]\n\t"
"v_mfma_f32_16x16x32_bf16 a[92:95], a[232:235], a[200:203], a[92:95]\n\t"
"ds_write_b64 v243, v[230:231] offset:4096\n\t"
"v_mfma_f32_16x16x32_bf16 a[100:103], a[224:227], a[196:199], a[100:103]\n\t"
"s_waitcnt vmcnt(4)\n\t"
"ds_read_b64_tr_b16 v[222:223], v238 offset:1024\n\t"
"v_mfma_f32_16x16x32_bf16 a[100:103], a[228:231], a[200:203], a[100:103]\n\t"
"ds_read_b64_tr_b16 v[224:225], v238 offset:2560\n\t"
"v_mfma_f32_16x16x32_bf16 a[100:103], a[232:235], a[204:207], a[100:103]\n\t"
"buffer_load_dwordx4 a[224:227], v252, s[8:11], s20 offen offset:2048 sc1\n\t"
"v_mfma_f32_16x16x32_bf16 a[108:111], a[244:247], a[168:171], a[108:111]\n\t"
"ds_read_b64_tr_b16 v[226:227], v239 offset:1024\n\t"
"v_mfma_f32_16x16x32_bf16 a[108:111], a[248:251], a[172:175], a[108:111]\n\t"
"ds_read_b64_tr_b16 v[228:229], v239 offset:2560\n\t"
"v_mfma_f32_16x16x32_bf16 a[108:111], a[236:239], a[176:179], a[108:111]\n\t"
"s_mov_b32 s20, 577536\n\t"
"buffer_load_dwordx4 a[228:231], v252, s[8:11], s20 offen offset:0 sc1\n\t"
"s_waitcnt lgkmcnt(4)\n\t"
"s_barrier\n\t"
"v_mfma_f32_16x16x32_bf16 a[116:119], a[244:247], a[172:175], a[116:119]\n\t"
"ds_read_b128 a[168:171], v242 offset:0\n\t"
"v_mfma_f32_16x16x32_bf16 a[116:119], a[248:251], a[176:179], a[116:119]\n\t"
"ds_read_b64_tr_b16 v[230:231], v238 offset:20992\n\t"
"v_mfma_f32_16x16x32_bf16 a[116:119], a[236:239], a[180:183], a[116:119]\n\t"
"buffer_load_dwordx4 a[232:235], v252, s[8:11], s20 offen offset:1024 sc1\n\t"
"v_mfma_f32_16x16x32_bf16 a[124:127], a[244:247], a[176:179], a[124:127]\n\t"
"ds_read_b64_tr_b16 v[232:233], v238 offset:22528\n\t"
"v_mfma_f32_16x16x32_bf16 a[124:127], a[248:251], a[180:183], a[124:127]\n\t"
"ds_read_b64_tr_b16 v[234:235], v239 offset:20992\n\t"
"v_mfma_f32_16x16x32_bf16 a[124:127], a[236:239], a[184:187], a[124:127]\n\t"
"ds_read_b64_tr_b16 v[236:237], v239 offset:22528\n\t"
"v_mfma_f32_16x16x32_bf16 a[132:135], a[244:247], a[180:183], a[132:135]\n\t"
"ds_read_b128 a[172:175], v242 offset:512\n\t"
"v_mfma_f32_16x16x32_bf16 a[132:135], a[248:251], a[184:187], a[132:135]\n\t"
"ds_read_b128 a[176:179], v242 offset:1024\n\t"
"v_mfma_f32_16x16x32_bf16 a[132:135], a[236:239], a[188:191], a[132:135]\n\t"
"s_waitcnt vmcnt(6) lgkmcnt(9)\n\t"
"v_mfma_f32_16x16x32_bf16 v[222:225], v[222:225], a[208:211], 0\n\t"
"ds_read_b128 a[180:183], v242 offset:1536\n\t"
"s_waitcnt lgkmcnt(8)\n\t"
"v_mfma_f32_16x16x32_bf16 v[226:229], v[226:229], a[208:211], 0\n\t"
"v_mfma_f32_16x16x32_bf16 a[140:143], a[244:247], a[184:187], a[140:143]\n\t"
"ds_read_b128 a[184:187], v242 offset:2048\n\t"
"v_mfma_f32_16x16x32_bf16 a[140:143], a[248:251], a[188:191], a[140:143]\n\t"
"v_mfma_f32_16x16x32_bf16 a[140:143], a[236:239], a[192:195], a[140:143]\n\t"
"v_mfma_f32_16x16x32_bf16 a[148:151], a[244:247], a[188:191], a[148:151]\n\t"
"v_cvt_pk_bf16_f32 v222, v222, v223\n\t"
"v_cvt_pk_bf16_f32 v223, v224, v225\n\t"
"v_mfma_f32_16x16x32_bf16 a[148:151], a[248:251], a[192:195], a[148:151]\n\t"
"v_cvt_pk_bf16_f32 v224, v226, v227\n\t"
"v_mfma_f32_16x16x32_bf16 a[148:151], a[236:239], a[196:199], a[148:151]\n\t"
"v_cvt_pk_bf16_f32 v225, v228, v229\n\t"
"ds_read_b128 a[188:191], v242 offset:2560\n\t"
"s_waitcnt lgkmcnt(7)\n\t"
"v_mfma_f32_16x16x32_bf16 v[230:233], v[230:233], a[208:211], 0\n\t"
"s_waitcnt lgkmcnt(5)\n\t"
"v_mfma_f32_16x16x32_bf16 v[234:237], v[234:237], a[208:211], 0\n\t"
"s_mov_b64 exec, s[44:45]\n\t"
"ds_write_b128 v240, v[222:225] offset:18432\n\t"
"s_mov_b64 exec, -1\n\t"
"v_mfma_f32_16x16x32_bf16 a[156:159], a[244:247], a[192:195], a[156:159]\n\t"
"ds_read_b128 a[192:195], v242 offset:3072\n\t"
"v_mfma_f32_16x16x32_bf16 a[156:159], a[248:251], a[196:199], a[156:159]\n\t"
"ds_read_b64_tr_b16 v[222:223], v238 offset:40960\n\t"
"v_mfma_f32_16x16x32_bf16 a[156:159], a[236:239], a[200:203], a[156:159]\n\t"
"ds_read_b64_tr_b16 v[224:225], v238 offset:42496\n\t"
"v_mfma_f32_16x16x32_bf16 a[164:167], a[244:247], a[196:199], a[164:167]\n\t"
"v_cvt_pk_bf16_f32 v230, v230, v231\n\t"
"v_cvt_pk_bf16_f32 v231, v232, v233\n\t"
"v_mfma_f32_16x16x32_bf16 a[164:167], a[248:251], a[200:203], a[164:167]\n\t"
"v_cvt_pk_bf16_f32 v232, v234, v235\n\t"
"v_cvt_pk_bf16_f32 v233, v236, v237\n\t"
"v_mfma_f32_16x16x32_bf16 a[164:167], a[236:239], a[204:207], a[164:167]\n\t"
"buffer_load_dwordx4 a[244:247], v252, s[8:11], s20 offen offset:2048 sc1\n\t"
"s_waitcnt vmcnt(6) lgkmcnt(7)\n\t"
"v_mfma_f32_16x16x32_bf16 v[194:197], a[240:243], a[168:171], v[194:197]\n\t"
"ds_read_b64_tr_b16 v[226:227], v239 offset:40960\n\t"
"v_mfma_f32_16x16x32_bf16 v[194:197], a[252:255], a[172:175], v[194:197]\n\t"
"ds_read_b64_tr_b16 v[228:229], v239 offset:42496\n\t"
"v_add_u32 v238, 59904, v238\n\t"
"v_mfma_f32_16x16x32_bf16 v[194:197], a[212:215], a[176:179], v[194:197]\n\t"
"s_mov_b32 s20, 589824\n\t"
"buffer_load_dwordx4 a[236:239], v252, s[8:11], s20 offen offset:0 sc1\n\t"
"s_waitcnt lgkmcnt(8)\n\t"
"v_mfma_f32_16x16x32_bf16 v[202:205], a[240:243], a[172:175], v[202:205]\n\t"
"ds_read_b128 a[196:199], v242 offset:3584\n\t"
"v_add_u32 v239, 59904, v239\n\t"
"v_mfma_f32_16x16x32_bf16 v[202:205], a[252:255], a[176:179], v[202:205]\n\t"
"ds_read_b128 a[200:203], v242 offset:4096\n\t"
"v_mfma_f32_16x16x32_bf16 v[202:205], a[212:215], a[180:183], v[202:205]\n\t"
"s_mov_b64 exec, s[44:45]\n\t"
"ds_write_b128 v240, v[230:233] offset:19968\n\t"
"s_mov_b64 exec, -1\n\t"
"s_waitcnt lgkmcnt(10)\n\t"
"v_mfma_f32_16x16x32_bf16 v[210:213], a[240:243], a[176:179], v[210:213]\n\t"
"ds_read_b64_tr_b16 v[230:231], v238 offset:1024\n\t"
"v_mfma_f32_16x16x32_bf16 v[210:213], a[252:255], a[180:183], v[210:213]\n\t"
"ds_read_b64_tr_b16 v[232:233], v238 offset:2560\n\t"
"v_mfma_f32_16x16x32_bf16 v[210:213], a[212:215], a[184:187], v[210:213]\n\t"
"buffer_load_dwordx4 a[248:251], v252, s[8:11], s20 offen offset:1024 sc1\n\t"
"s_waitcnt lgkmcnt(7)\n\t"
"v_mfma_f32_16x16x32_bf16 v[222:225], v[222:225], a[208:211], 0\n\t"
"ds_read_b64_tr_b16 v[234:235], v239 offset:1024\n\t"
"ds_read_b64_tr_b16 v[236:237], v239 offset:2560\n\t"
"s_waitcnt lgkmcnt(7)\n\t"
"v_mfma_f32_16x16x32_bf16 v[226:229], v[226:229], a[208:211], 0\n\t"
"ds_read_b128 a[204:207], v242 offset:4608\n\t"
"v_mfma_f32_16x16x32_bf16 a[0:3], a[240:243], a[180:183], a[0:3]\n\t"
"v_mfma_f32_16x16x32_bf16 a[0:3], a[252:255], a[184:187], a[0:3]\n\t"
"v_mfma_f32_16x16x32_bf16 a[0:3], a[212:215], a[188:191], a[0:3]\n\t"
"v_mfma_f32_16x16x32_bf16 a[8:11], a[240:243], a[184:187], a[8:11]\n\t"
"v_cvt_pk_bf16_f32 v222, v222, v223\n\t"
"v_cvt_pk_bf16_f32 v223, v224, v225\n\t"
"v_mfma_f32_16x16x32_bf16 a[8:11], a[252:255], a[188:191], a[8:11]\n\t"
"v_cvt_pk_bf16_f32 v224, v226, v227\n\t"
"v_mfma_f32_16x16x32_bf16 a[8:11], a[212:215], a[192:195], a[8:11]\n\t"
"v_cvt_pk_bf16_f32 v225, v228, v229\n\t"
"s_waitcnt lgkmcnt(3)\n\t"
"v_mfma_f32_16x16x32_bf16 v[230:233], v[230:233], a[208:211], 0\n\t"
"s_mov_b64 exec, s[44:45]\n\t"
"ds_write_b128 v240, v[222:225] offset:21504\n\t"
"s_mov_b64 exec, -1\n\t"
"s_waitcnt lgkmcnt(2)\n\t"
"v_mfma_f32_16x16x32_bf16 v[234:237], v[234:237], a[208:211], 0\n\t"
"ds_read_b64_tr_b16 v[222:223], v238 offset:20992\n\t"
"v_mfma_f32_16x16x32_bf16 a[16:19], a[240:243], a[188:191], a[16:19]\n\t"
"ds_read_b64_tr_b16 v[224:225], v238 offset:22528\n\t"
"v_mfma_f32_16x16x32_bf16 a[16:19], a[252:255], a[192:195], a[16:19]\n\t"
"v_mfma_f32_16x16x32_bf16 a[16:19], a[212:215], a[196:199], a[16:19]\n\t"
"ds_read_b64_tr_b16 v[226:227], v239 offset:20992\n\t"
"v_mfma_f32_16x16x32_bf16 a[24:27], a[240:243], a[192:195], a[24:27]\n\t"
"v_cvt_pk_bf16_f32 v230, v230, v231\n\t"
"v_mfma_f32_16x16x32_bf16 a[24:27], a[252:255], a[196:199], a[24:27]\n\t"
"v_cvt_pk_bf16_f32 v231, v232, v233\n\t"
"v_cvt_pk_bf16_f32 v232, v234, v235\n\t"
"v_mfma_f32_16x16x32_bf16 a[24:27], a[212:215], a[200:203], a[24:27]\n\t"
"v_cvt_pk_bf16_f32 v233, v236, v237\n\t"
"ds_read_b64_tr_b16 v[228:229], v239 offset:22528\n\t"
"s_waitcnt lgkmcnt(5)\n\t"
"v_mfma_f32_16x16x32_bf16 a[32:35], a[240:243], a[196:199], a[32:35]\n\t"
"v_mfma_f32_16x16x32_bf16 a[32:35], a[252:255], a[200:203], a[32:35]\n\t"
"v_mfma_f32_16x16x32_bf16 a[32:35], a[212:215], a[204:207], a[32:35]\n\t"
"buffer_load_dwordx4 a[240:243], v252, s[8:11], s20 offen offset:2048 sc1\n\t"
"s_waitcnt vmcnt(6)\n\t"
"v_mfma_f32_16x16x32_bf16 a[40:43], a[216:219], a[168:171], a[40:43]\n\t"
"v_mfma_f32_16x16x32_bf16 a[40:43], a[220:223], a[172:175], a[40:43]\n\t"
"v_mfma_f32_16x16x32_bf16 a[40:43], a[224:227], a[176:179], a[40:43]\n\t"
"s_mov_b64 exec, s[44:45]\n\t"
"ds_write_b128 v240, v[230:233] offset:23040\n\t"
"s_mov_b64 exec, -1\n\t"
"v_mfma_f32_16x16x32_bf16 a[48:51], a[216:219], a[172:175], a[48:51]\n\t"
"ds_read_b64_tr_b16 v[230:231], v238 offset:40960\n\t"
"v_mfma_f32_16x16x32_bf16 a[48:51], a[220:223], a[176:179], a[48:51]\n\t"
"ds_read_b64_tr_b16 v[232:233], v238 offset:42496\n\t"
"v_mfma_f32_16x16x32_bf16 a[48:51], a[224:227], a[180:183], a[48:51]\n\t"
"s_mov_b32 s20, 602112\n\t"
"buffer_load_dwordx4 a[212:215], v252, s[8:11], s20 offen offset:0 sc1\n\t"
"s_waitcnt lgkmcnt(5)\n\t"
"v_mfma_f32_16x16x32_bf16 v[222:225], v[222:225], a[208:211], 0\n\t"
"buffer_load_dwordx4 a[252:255], v252, s[8:11], s20 offen offset:1024 sc1\n\t"
"s_waitcnt lgkmcnt(3)\n\t"
"v_mfma_f32_16x16x32_bf16 v[226:229], v[226:229], a[208:211], 0\n\t"
"ds_read_b64_tr_b16 v[234:235], v239 offset:40960\n\t"
"ds_read_b64_tr_b16 v[236:237], v239 offset:42496\n\t"
"v_mfma_f32_16x16x32_bf16 a[56:59], a[216:219], a[176:179], a[56:59]\n\t"
"v_xor_b32 v244, 0xc0, v244\n\t"
"v_mfma_f32_16x16x32_bf16 a[56:59], a[220:223], a[180:183], a[56:59]\n\t"
"v_mfma_f32_16x16x32_bf16 a[56:59], a[224:227], a[184:187], a[56:59]\n\t"
"v_mfma_f32_16x16x32_bf16 a[64:67], a[216:219], a[180:183], a[64:67]\n\t"
"v_cvt_pk_bf16_f32 v222, v222, v223\n\t"
"v_mfma_f32_16x16x32_bf16 a[64:67], a[220:223], a[184:187], a[64:67]\n\t"
"v_cvt_pk_bf16_f32 v223, v224, v225\n\t"
"v_cvt_pk_bf16_f32 v224, v226, v227\n\t"
"v_mfma_f32_16x16x32_bf16 a[64:67], a[224:227], a[188:191], a[64:67]\n\t"
"v_cvt_pk_bf16_f32 v225, v228, v229\n\t"
"s_waitcnt lgkmcnt(2)\n\t"
"v_mfma_f32_16x16x32_bf16 v[230:233], v[230:233], a[208:211], 0\n\t"
"s_waitcnt lgkmcnt(0)\n\t"
"v_mfma_f32_16x16x32_bf16 v[234:237], v[234:237], a[208:211], 0\n\t"
"s_mov_b64 exec, s[44:45]\n\t"
"ds_write_b128 v240, v[222:225] offset:24576\n\t"
"s_mov_b64 exec, -1\n\t"
"v_mfma_f32_16x16x32_bf16 a[72:75], a[216:219], a[184:187], a[72:75]\n\t"
"v_mfma_f32_16x16x32_bf16 a[72:75], a[220:223], a[188:191], a[72:75]\n\t"
"v_mfma_f32_16x16x32_bf16 a[72:75], a[224:227], a[192:195], a[72:75]\n\t"
"buffer_load_dwordx4 a[208:211], v252, s[8:11], s20 offen offset:2048 sc1\n\t"
"v_mfma_f32_16x16x32_bf16 a[80:83], a[216:219], a[188:191], a[80:83]\n\t"
"v_cvt_pk_bf16_f32 v230, v230, v231\n\t"
"v_cvt_pk_bf16_f32 v231, v232, v233\n\t"
"v_mfma_f32_16x16x32_bf16 a[80:83], a[220:223], a[192:195], a[80:83]\n\t"
"v_cvt_pk_bf16_f32 v232, v234, v235\n\t"
"v_mfma_f32_16x16x32_bf16 a[80:83], a[224:227], a[196:199], a[80:83]\n\t"
"v_cvt_pk_bf16_f32 v233, v236, v237\n\t"
"ds_read_b64_tr_b16 v[222:223], v244 offset:1024\n\t"
"v_mfma_f32_16x16x32_bf16 a[88:91], a[216:219], a[192:195], a[88:91]\n\t"
"ds_read_b64_tr_b16 v[224:225], v244 offset:4096\n\t"
"v_mfma_f32_16x16x32_bf16 a[88:91], a[220:223], a[196:199], a[88:91]\n\t"
"v_mfma_f32_16x16x32_bf16 a[88:91], a[224:227], a[200:203], a[88:91]\n\t"
"s_mov_b64 exec, s[44:45]\n\t"
"ds_write_b128 v240, v[230:233] offset:26112\n\t"
"s_mov_b64 exec, -1\n\t"
"v_mfma_f32_16x16x32_bf16 a[96:99], a[216:219], a[196:199], a[96:99]\n\t"
"ds_read_b64_tr_b16 v[226:227], v244 offset:7168\n\t"
"v_mfma_f32_16x16x32_bf16 a[96:99], a[220:223], a[200:203], a[96:99]\n\t"
"ds_read_b64_tr_b16 v[228:229], v244 offset:10240\n\t"
"v_mfma_f32_16x16x32_bf16 a[96:99], a[224:227], a[204:207], a[96:99]\n\t"
"s_waitcnt vmcnt(6)\n\t"
"v_mfma_f32_16x16x32_bf16 a[104:107], a[228:231], a[168:171], a[104:107]\n\t"
"ds_read_b128 a[168:171], v242 offset:9216\n\t"
"v_mfma_f32_16x16x32_bf16 a[104:107], a[232:235], a[172:175], a[104:107]\n\t"
"ds_read_b64_tr_b16 v[230:231], v244 offset:13312\n\t"
"v_mfma_f32_16x16x32_bf16 a[104:107], a[244:247], a[176:179], a[104:107]\n\t"
"s_mov_b32 s20, 614400\n\t"
"buffer_load_dwordx4 a[216:219], v252, s[8:11], s20 offen offset:0 sc1\n\t"
"v_mfma_f32_16x16x32_bf16 a[112:115], a[228:231], a[172:175], a[112:115]\n\t"
"ds_read_b64_tr_b16 v[232:233], v244 offset:16384\n\t"
"v_mfma_f32_16x16x32_bf16 a[112:115], a[232:235], a[176:179], a[112:115]\n\t"
"ds_read_b128 a[172:175], v242 offset:9728\n\t"
"v_mfma_f32_16x16x32_bf16 a[112:115], a[244:247], a[180:183], a[112:115]\n\t"
"buffer_load_dwordx4 a[220:223], v252, s[8:11], s20 offen offset:1024 sc1\n\t"
"v_mfma_f32_16x16x32_bf16 a[120:123], a[228:231], a[176:179], a[120:123]\n\t"
"ds_read_b64_tr_b16 v[234:235], v244 offset:20992\n\t"
"v_mfma_f32_16x16x32_bf16 a[120:123], a[232:235], a[180:183], a[120:123]\n\t"
"ds_read_b64_tr_b16 v[236:237], v244 offset:24064\n\t"
"v_mfma_f32_16x16x32_bf16 a[120:123], a[244:247], a[184:187], a[120:123]\n\t"
"buffer_load_dwordx4 a[224:227], v252, s[8:11], s20 offen offset:2048 sc1\n\t"
"s_waitcnt lgkmcnt(9)\n\t"
"v_mfma_f32_16x16x32_bf16 v[222:225], v[222:225], v[218:221], 0\n\t"
"v_mfma_f32_16x16x32_bf16 a[128:131], a[228:231], a[180:183], a[128:131]\n\t"
"ds_read_b128 a[176:179], v242 offset:10240\n\t"
"v_mfma_f32_16x16x32_bf16 a[128:131], a[232:235], a[184:187], a[128:131]\n\t"
"ds_read_b128 a[180:183], v242 offset:10752\n\t"
"v_mfma_f32_16x16x32_bf16 a[128:131], a[244:247], a[188:191], a[128:131]\n\t"
"s_waitcnt lgkmcnt(8)\n\t"
"v_mfma_f32_16x16x32_bf16 v[226:229], v[226:229], v[218:221], 0\n\t"
"v_mfma_f32_16x16x32_bf16 a[136:139], a[228:231], a[184:187], a[136:139]\n\t"
"v_cvt_pk_bf16_f32 v222, v222, v223\n\t"
"v_cvt_pk_bf16_f32 v223, v224, v225\n\t"
"v_mfma_f32_16x16x32_bf16 a[136:139], a[232:235], a[188:191], a[136:139]\n\t"
"ds_read_b128 a[184:187], v242 offset:11264\n\t"
"v_mfma_f32_16x16x32_bf16 a[136:139], a[244:247], a[192:195], a[136:139]\n\t"
"s_waitcnt lgkmcnt(6)\n\t"
"v_mfma_f32_16x16x32_bf16 v[230:233], v[230:233], v[218:221], 0\n\t"
"v_mfma_f32_16x16x32_bf16 a[144:147], a[228:231], a[188:191], a[144:147]\n\t"
"v_cvt_pk_bf16_f32 v226, v226, v227\n\t"
"v_cvt_pk_bf16_f32 v227, v228, v229\n\t"
"v_mfma_f32_16x16x32_bf16 a[144:147], a[232:235], a[192:195], a[144:147]\n\t"
"ds_read_b128 a[188:191], v242 offset:11776\n\t"
"v_mfma_f32_16x16x32_bf16 a[144:147], a[244:247], a[196:199], a[144:147]\n\t"
"s_waitcnt lgkmcnt(4)\n\t"
"v_mfma_f32_16x16x32_bf16 v[234:237], v[234:237], v[218:221], 0\n\t"
"v_mfma_f32_16x16x32_bf16 a[152:155], a[228:231], a[192:195], a[152:155]\n\t"
"v_cvt_pk_bf16_f32 v230, v230, v231\n\t"
"v_cvt_pk_bf16_f32 v231, v232, v233\n\t"
"v_mfma_f32_16x16x32_bf16 a[152:155], a[232:235], a[196:199], a[152:155]\n\t"
"ds_read_b128 a[192:195], v242 offset:12288\n\t"
"v_mfma_f32_16x16x32_bf16 a[152:155], a[244:247], a[200:203], a[152:155]\n\t"
"v_mfma_f32_16x16x32_bf16 a[160:163], a[228:231], a[196:199], a[160:163]\n\t"
"ds_read_b128 a[196:199], v242 offset:12800\n\t"
"v_mfma_f32_16x16x32_bf16 a[160:163], a[232:235], a[200:203], a[160:163]\n\t"
"ds_read_b128 a[200:203], v242 offset:13312\n\t"
"v_cvt_pk_bf16_f32 v234, v234, v235\n\t"
"v_mfma_f32_16x16x32_bf16 a[160:163], a[244:247], a[204:207], a[160:163]\n\t"
"s_mov_b32 s20, 479232\n\t"
"buffer_load_dwordx4 a[228:231], v252, s[8:11], s20 offen offset:0 sc1\n\t"
"s_waitcnt vmcnt(7) lgkmcnt(6)\n\t"
"v_mfma_f32_16x16x32_bf16 v[198:201], a[236:239], a[168:171], v[198:201]\n\t"
"ds_read_b128 a[204:207], v242 offset:13824\n\t"
"v_cvt_pk_bf16_f32 v235, v236, v237\n\t"
"v_mfma_f32_16x16x32_bf16 v[198:201], a[248:251], a[172:175], v[198:201]\n\t"
"v_mfma_f32_16x16x32_bf16 v[198:201], a[240:243], a[176:179], v[198:201]\n\t"
"buffer_load_dwordx4 a[232:235], v252, s[8:11], s20 offen offset:1024 sc1\n\t"
"s_waitcnt lgkmcnt(6)\n\t"
"v_mfma_f32_16x16x32_bf16 v[206:209], a[236:239], a[172:175], v[206:209]\n\t"
"v_mfma_f32_16x16x32_bf16 v[206:209], a[248:251], a[176:179], v[206:209]\n\t"
"v_mfma_f32_16x16x32_bf16 v[206:209], a[240:243], a[180:183], v[206:209]\n\t"
"buffer_load_dwordx4 a[244:247], v252, s[8:11], s20 offen offset:2048 sc1\n\t"
"s_waitcnt lgkmcnt(0)\n\t"
"s_barrier\n\t"
"v_mfma_f32_16x16x32_bf16 v[214:217], a[236:239], a[176:179], v[214:217]\n\t"
"v_mfma_f32_16x16x32_bf16 v[214:217], a[248:251], a[180:183], v[214:217]\n\t"
"v_mfma_f32_16x16x32_bf16 v[214:217], a[240:243], a[184:187], v[214:217]\n\t"
"ds_write_b64 v243, v[222:223] offset:0\n\t"
"ds_write_b64 v243, v[226:227] offset:512\n\t"
"v_mfma_f32_16x16x32_bf16 a[4:7], a[236:239], a[180:183], a[4:7]\n\t"
"ds_read_b64_tr_b16 v[222:223], v244 offset:27136\n\t"
"v_mfma_f32_16x16x32_bf16 a[4:7], a[248:251], a[184:187], a[4:7]\n\t"
"ds_read_b64_tr_b16 v[224:225], v244 offset:30208\n\t"
"v_mfma_f32_16x16x32_bf16 a[4:7], a[240:243], a[188:191], a[4:7]\n\t"
"ds_write_b64 v243, v[230:231] offset:1024\n\t"
"v_mfma_f32_16x16x32_bf16 a[12:15], a[236:239], a[184:187], a[12:15]\n\t"
"ds_read_b64_tr_b16 v[226:227], v244 offset:33280\n\t"
"v_mfma_f32_16x16x32_bf16 a[12:15], a[248:251], a[188:191], a[12:15]\n\t"
"ds_read_b64_tr_b16 v[228:229], v244 offset:36352\n\t"
"v_mfma_f32_16x16x32_bf16 a[12:15], a[240:243], a[192:195], a[12:15]\n\t"
"ds_write_b64 v243, v[234:235] offset:1536\n\t"
"v_mfma_f32_16x16x32_bf16 a[20:23], a[236:239], a[188:191], a[20:23]\n\t"
"ds_read_b64_tr_b16 v[230:231], v244 offset:40960\n\t"
"v_mfma_f32_16x16x32_bf16 a[20:23], a[248:251], a[192:195], a[20:23]\n\t"
"ds_read_b64_tr_b16 v[232:233], v244 offset:44032\n\t"
"v_mfma_f32_16x16x32_bf16 a[20:23], a[240:243], a[196:199], a[20:23]\n\t"
"ds_read_b64_tr_b16 v[234:235], v244 offset:47104\n\t"
"ds_read_b64_tr_b16 v[236:237], v244 offset:50176\n\t"
"s_waitcnt lgkmcnt(8)\n\t"
"v_mfma_f32_16x16x32_bf16 v[222:225], v[222:225], v[218:221], 0\n\t"
"v_mfma_f32_16x16x32_bf16 a[28:31], a[236:239], a[192:195], a[28:31]\n\t"
"v_mfma_f32_16x16x32_bf16 a[28:31], a[248:251], a[196:199], a[28:31]\n\t"
"v_mfma_f32_16x16x32_bf16 a[28:31], a[240:243], a[200:203], a[28:31]\n\t"
"s_waitcnt lgkmcnt(5)\n\t"
"v_mfma_f32_16x16x32_bf16 v[226:229], v[226:229], v[218:221], 0\n\t"
"v_mfma_f32_16x16x32_bf16 a[36:39], a[236:239], a[196:199], a[36:39]\n\t"
"s_nop 1\n\t"
"v_cvt_pk_bf16_f32 v222, v222, v223\n\t"
"v_cvt_pk_bf16_f32 v223, v224, v225\n\t"
"v_mfma_f32_16x16x32_bf16 a[36:39], a[248:251], a[200:203], a[36:39]\n\t"
"v_mfma_f32_16x16x32_bf16 a[36:39], a[240:243], a[204:207], a[36:39]\n\t"
"s_mov_b32 s20, 491520\n\t"
"buffer_load_dwordx4 a[236:239], v252, s[8:11], s20 offen offset:0 sc1\n\t"
"s_waitcnt lgkmcnt(2)\n\t"
"v_mfma_f32_16x16x32_bf16 v[230:233], v[230:233], v[218:221], 0\n\t"
"buffer_load_dwordx4 a[240:243], v252, s[8:11], s20 offen offset:1024 sc1\n\t"
"s_waitcnt vmcnt(8)\n\t"
"v_mfma_f32_16x16x32_bf16 a[44:47], a[212:215], a[168:171], a[44:47]\n\t"
"v_cvt_pk_bf16_f32 v226, v226, v227\n\t"
"v_cvt_pk_bf16_f32 v227, v228, v229\n\t"
"v_mfma_f32_16x16x32_bf16 a[44:47], a[252:255], a[172:175], a[44:47]\n\t"
"v_mfma_f32_16x16x32_bf16 a[44:47], a[208:211], a[176:179], a[44:47]\n\t"
"ds_write_b64 v243, v[222:223] offset:2048\n\t"
"s_waitcnt lgkmcnt(1)\n\t"
"v_mfma_f32_16x16x32_bf16 v[234:237], v[234:237], v[218:221], 0\n\t"
"ds_read_b64_tr_b16 v[222:223], v244 offset:53248\n\t"
"ds_read_b64_tr_b16 v[224:225], v244 offset:56320\n\t"
"v_mfma_f32_16x16x32_bf16 a[52:55], a[212:215], a[172:175], a[52:55]\n\t"
"v_cvt_pk_bf16_f32 v230, v230, v231\n\t"
"v_cvt_pk_bf16_f32 v231, v232, v233\n\t"
"v_mfma_f32_16x16x32_bf16 a[52:55], a[252:255], a[176:179], a[52:55]\n\t"
"v_mfma_f32_16x16x32_bf16 a[52:55], a[208:211], a[180:183], a[52:55]\n\t"
"buffer_load_dwordx4 a[248:251], v252, s[8:11], s20 offen offset:2048 sc1\n\t"
"v_mfma_f32_16x16x32_bf16 a[60:63], a[212:215], a[176:179], a[60:63]\n\t"
"v_mfma_f32_16x16x32_bf16 a[60:63], a[252:255], a[180:183], a[60:63]\n\t"
"v_cvt_pk_bf16_f32 v234, v234, v235\n\t"
"v_cvt_pk_bf16_f32 v235, v236, v237\n\t"
"v_mfma_f32_16x16x32_bf16 a[60:63], a[208:211], a[184:187], a[60:63]\n\t"
"ds_write_b64 v243, v[226:227] offset:2560\n\t"
"v_mfma_f32_16x16x32_bf16 a[68:71], a[212:215], a[180:183], a[68:71]\n\t"
"v_mfma_f32_16x16x32_bf16 a[68:71], a[252:255], a[184:187], a[68:71]\n\t"
"v_mfma_f32_16x16x32_bf16 a[68:71], a[208:211], a[188:191], a[68:71]\n\t"
"ds_write_b64 v243, v[230:231] offset:3072\n\t"
"s_waitcnt lgkmcnt(2)\n\t"
"v_mfma_f32_16x16x32_bf16 v[222:225], v[222:225], v[218:221], 0\n\t"
"ds_write_b64 v243, v[234:235] offset:3584\n\t"
"v_mfma_f32_16x16x32_bf16 a[76:79], a[212:215], a[184:187], a[76:79]\n\t"
"v_mfma_f32_16x16x32_bf16 a[76:79], a[252:255], a[188:191], a[76:79]\n\t"
"v_mfma_f32_16x16x32_bf16 a[76:79], a[208:211], a[192:195], a[76:79]\n\t"
"v_mfma_f32_16x16x32_bf16 a[84:87], a[212:215], a[188:191], a[84:87]\n\t"
"v_mfma_f32_16x16x32_bf16 a[84:87], a[252:255], a[192:195], a[84:87]\n\t"
"s_nop 1\n\t"
"v_cvt_pk_bf16_f32 v222, v222, v223\n\t"
"v_cvt_pk_bf16_f32 v223, v224, v225\n\t"
"v_mfma_f32_16x16x32_bf16 a[84:87], a[208:211], a[196:199], a[84:87]\n\t"
"v_mfma_f32_16x16x32_bf16 a[92:95], a[212:215], a[192:195], a[92:95]\n\t"
"v_mfma_f32_16x16x32_bf16 a[92:95], a[252:255], a[196:199], a[92:95]\n\t"
"v_mfma_f32_16x16x32_bf16 a[92:95], a[208:211], a[200:203], a[92:95]\n\t"
"ds_write_b64 v243, v[222:223] offset:4096\n\t"
"v_mfma_f32_16x16x32_bf16 a[100:103], a[212:215], a[196:199], a[100:103]\n\t"
"v_mfma_f32_16x16x32_bf16 a[100:103], a[252:255], a[200:203], a[100:103]\n\t"
"v_mfma_f32_16x16x32_bf16 a[100:103], a[208:211], a[204:207], a[100:103]\n\t"
"s_mov_b32 s20, 503808\n\t"
"buffer_load_dwordx4 a[212:215], v252, s[8:11], s20 offen offset:0 sc1\n\t"
"s_waitcnt vmcnt(7)\n\t"
"v_mfma_f32_16x16x32_bf16 a[108:111], a[216:219], a[168:171], a[108:111]\n\t"
"v_mfma_f32_16x16x32_bf16 a[108:111], a[220:223], a[172:175], a[108:111]\n\t"
"v_mfma_f32_16x16x32_bf16 a[108:111], a[224:227], a[176:179], a[108:111]\n\t"
"buffer_load_dwordx4 a[208:211], v252, s[8:11], s20 offen offset:1024 sc1\n\t"
"s_waitcnt lgkmcnt(0)\n\t"
"s_barrier\n\t"
"v_mfma_f32_16x16x32_bf16 a[116:119], a[216:219], a[172:175], a[116:119]\n\t"
"ds_read_b128 a[168:171], v242 offset:0\n\t"
"v_mfma_f32_16x16x32_bf16 a[116:119], a[220:223], a[176:179], a[116:119]\n\t"
"ds_read_b128 a[172:175], v242 offset:512\n\t"
"v_mfma_f32_16x16x32_bf16 a[116:119], a[224:227], a[180:183], a[116:119]\n\t"
"s_add_u32 m0, s25, 256\n\t"
"s_nop 0\n\t"
"buffer_load_dwordx4 v246, s[12:15], 0 offen offset:256 nt lds\n\t"
"s_add_u32 m0, s26, 256\n\t"
"s_mov_b64 exec, s[36:37]\n\t"
"buffer_load_dwordx4 v246, s[12:15], s27 offen offset:256 nt lds\n\t"
"s_mov_b64 exec, -1\n\t"
"v_mfma_f32_16x16x32_bf16 a[124:127], a[216:219], a[176:179], a[124:127]\n\t"
"ds_read_b128 a[176:179], v242 offset:1024\n\t"
"v_mfma_f32_16x16x32_bf16 a[124:127], a[220:223], a[180:183], a[124:127]\n\t"
"v_mfma_f32_16x16x32_bf16 a[124:127], a[224:227], a[184:187], a[124:127]\n\t"
"s_add_u32 m0, s24, 256\n\t"
"s_add_u32 s28, s21, 0\n\t"
"buffer_load_dwordx4 v245, s[12:15], s28 offen offset:256 nt lds\n\t"
"v_mfma_f32_16x16x32_bf16 a[132:135], a[216:219], a[180:183], a[132:135]\n\t"
"ds_read_b128 a[180:183], v242 offset:1536\n\t"
"v_mfma_f32_16x16x32_bf16 a[132:135], a[220:223], a[184:187], a[132:135]\n\t"
"v_mfma_f32_16x16x32_bf16 a[132:135], a[224:227], a[188:191], a[132:135]\n\t"
"s_add_u32 m0, s24, 6400\n\t"
"s_add_u32 s28, s21, 196608\n\t"
"buffer_load_dwordx4 v245, s[12:15], s28 offen offset:256 nt lds\n\t"
"s_add_u32 m0, s24, 12544\n\t"
"s_add_u32 s28, s21, 393216\n\t"
"buffer_load_dwordx4 v245, s[12:15], s28 offen offset:256 nt lds\n\t"
"v_mfma_f32_16x16x32_bf16 a[140:143], a[216:219], a[184:187], a[140:143]\n\t"
"ds_read_b128 a[184:187], v242 offset:2048\n\t"
"v_mfma_f32_16x16x32_bf16 a[140:143], a[220:223], a[188:191], a[140:143]\n\t"
"v_mfma_f32_16x16x32_bf16 a[140:143], a[224:227], a[192:195], a[140:143]\n\t"
"s_add_u32 m0, s24, 20224\n\t"
"s_add_u32 s28, s21, 589824\n\t"
"buffer_load_dwordx4 v245, s[12:15], s28 offen offset:256 nt lds\n\t"
"v_mfma_f32_16x16x32_bf16 a[148:151], a[216:219], a[188:191], a[148:151]\n\t"
"ds_read_b128 a[188:191], v242 offset:2560\n\t"
"v_mfma_f32_16x16x32_bf16 a[148:151], a[220:223], a[192:195], a[148:151]\n\t"
"v_mfma_f32_16x16x32_bf16 a[148:151], a[224:227], a[196:199], a[148:151]\n\t"
"s_add_u32 m0, s24, 26368\n\t"
"s_add_u32 s28, s21, 786432\n\t"
"buffer_load_dwordx4 v245, s[12:15], s28 offen offset:256 nt lds\n\t"
"v_mfma_f32_16x16x32_bf16 a[156:159], a[216:219], a[192:195], a[156:159]\n\t"
"ds_read_b128 a[192:195], v242 offset:3072\n\t"
"v_mfma_f32_16x16x32_bf16 a[156:159], a[220:223], a[196:199], a[156:159]\n\t"
"v_mfma_f32_16x16x32_bf16 a[156:159], a[224:227], a[200:203], a[156:159]\n\t"
"v_mfma_f32_16x16x32_bf16 a[164:167], a[216:219], a[196:199], a[164:167]\n\t"
"ds_read_b128 a[196:199], v242 offset:3584\n\t"
"v_mfma_f32_16x16x32_bf16 a[164:167], a[220:223], a[200:203], a[164:167]\n\t"
"ds_read_b128 a[200:203], v242 offset:4096\n\t"
"v_mfma_f32_16x16x32_bf16 a[164:167], a[224:227], a[204:207], a[164:167]\n\t"
"s_waitcnt vmcnt(12) lgkmcnt(6)\n\t"
"v_mfma_f32_16x16x32_bf16 v[0:3], a[228:231], a[168:171], v[0:3]\n\t"
"ds_read_b128 a[204:207], v242 offset:4608\n\t"
"v_mfma_f32_16x16x32_bf16 v[0:3], a[232:235], a[172:175], v[0:3]\n\t"
"v_mfma_f32_16x16x32_bf16 v[0:3], a[244:247], a[176:179], v[0:3]\n\t"
"s_waitcnt lgkmcnt(6)\n\t"
"v_mfma_f32_16x16x32_bf16 v[4:7], a[228:231], a[172:175], v[4:7]\n\t"
"v_mfma_f32_16x16x32_bf16 v[4:7], a[232:235], a[176:179], v[4:7]\n\t"
"v_mfma_f32_16x16x32_bf16 v[4:7], a[244:247], a[180:183], v[4:7]\n\t"
"s_waitcnt lgkmcnt(5)\n\t"
"v_mfma_f32_16x16x32_bf16 v[8:11], a[228:231], a[176:179], v[8:11]\n\t"
"v_mfma_f32_16x16x32_bf16 v[8:11], a[232:235], a[180:183], v[8:11]\n\t"
"v_mfma_f32_16x16x32_bf16 v[8:11], a[244:247], a[184:187], v[8:11]\n\t"
"s_waitcnt lgkmcnt(4)\n\t"
"v_mfma_f32_16x16x32_bf16 v[12:15], a[228:231], a[180:183], v[12:15]\n\t"
"v_mfma_f32_16x16x32_bf16 v[12:15], a[232:235], a[184:187], v[12:15]\n\t"
"v_mfma_f32_16x16x32_bf16 v[12:15], a[244:247], a[188:191], v[12:15]\n\t"
"buffer_load_dwordx4 a[216:219], v252, s[8:11], s20 offen offset:2048 sc1\n\t"
"s_waitcnt lgkmcnt(3)\n\t"
"v_mfma_f32_16x16x32_bf16 v[16:19], a[228:231], a[184:187], v[16:19]\n\t"
"v_mfma_f32_16x16x32_bf16 v[16:19], a[232:235], a[188:191], v[16:19]\n\t"
"v_mfma_f32_16x16x32_bf16 v[16:19], a[244:247], a[192:195], v[16:19]\n\t"
"s_waitcnt lgkmcnt(2)\n\t"
"v_mfma_f32_16x16x32_bf16 v[20:23], a[228:231], a[188:191], v[20:23]\n\t"
"v_mfma_f32_16x16x32_bf16 v[20:23], a[232:235], a[192:195], v[20:23]\n\t"
"v_mfma_f32_16x16x32_bf16 v[20:23], a[244:247], a[196:199], v[20:23]\n\t"
"s_waitcnt lgkmcnt(1)\n\t"
"v_mfma_f32_16x16x32_bf16 v[24:27], a[228:231], a[192:195], v[24:27]\n\t"
"v_mfma_f32_16x16x32_bf16 v[24:27], a[232:235], a[196:199], v[24:27]\n\t"
"v_mfma_f32_16x16x32_bf16 v[24:27], a[244:247], a[200:203], v[24:27]\n\t"
"s_waitcnt lgkmcnt(0)\n\t"
"v_mfma_f32_16x16x32_bf16 v[28:31], a[228:231], a[196:199], v[28:31]\n\t"
"v_mfma_f32_16x16x32_bf16 v[28:31], a[232:235], a[200:203], v[28:31]\n\t"
"v_mfma_f32_16x16x32_bf16 v[28:31], a[244:247], a[204:207], v[28:31]\n\t"
"s_waitcnt vmcnt(10)\n\t"
"v_mfma_f32_16x16x32_bf16 v[32:35], a[236:239], a[168:171], v[32:35]\n\t"
"v_mfma_f32_16x16x32_bf16 v[32:35], a[240:243], a[172:175], v[32:35]\n\t"
"v_mfma_f32_16x16x32_bf16 v[32:35], a[248:251], a[176:179], v[32:35]\n\t"
"ds_read_b64_tr_b16 v[226:227], v244 offset:1536\n\t"
"ds_read_b64_tr_b16 v[228:229], v244 offset:4608\n\t"
"v_mfma_f32_16x16x32_bf16 v[36:39], a[236:239], a[172:175], v[36:39]\n\t"
"v_mfma_f32_16x16x32_bf16 v[36:39], a[240:243], a[176:179], v[36:39]\n\t"
"v_mfma_f32_16x16x32_bf16 v[36:39], a[248:251], a[180:183], v[36:39]\n\t"
"v_mfma_f32_16x16x32_bf16 v[40:43], a[236:239], a[176:179], v[40:43]\n\t"
"v_mfma_f32_16x16x32_bf16 v[40:43], a[240:243], a[180:183], v[40:43]\n\t"
"v_mfma_f32_16x16x32_bf16 v[40:43], a[248:251], a[184:187], v[40:43]\n\t"
"v_mfma_f32_16x16x32_bf16 v[44:47], a[236:239], a[180:183], v[44:47]\n\t"
"ds_read_b64_tr_b16 v[230:231], v244 offset:7680\n\t"
"v_mfma_f32_16x16x32_bf16 v[44:47], a[240:243], a[184:187], v[44:47]\n\t"
"ds_read_b64_tr_b16 v[232:233], v244 offset:10752\n\t"
"v_mfma_f32_16x16x32_bf16 v[44:47], a[248:251], a[188:191], v[44:47]\n\t"
"v_mfma_f32_16x16x32_bf16 v[48:51], a[236:239], a[184:187], v[48:51]\n\t"
"v_mfma_f32_16x16x32_bf16 v[48:51], a[240:243], a[188:191], v[48:51]\n\t"
"v_mfma_f32_16x16x32_bf16 v[48:51], a[248:251], a[192:195], v[48:51]\n\t"
"s_mov_b32 s20, 516096\n\t"
"buffer_load_dwordx4 a[220:223], v252, s[8:11], s20 offen offset:0 sc1\n\t"
"v_mfma_f32_16x16x32_bf16 v[52:55], a[236:239], a[188:191], v[52:55]\n\t"
"v_mfma_f32_16x16x32_bf16 v[52:55], a[240:243], a[192:195], v[52:55]\n\t"
"v_mfma_f32_16x16x32_bf16 v[52:55], a[248:251], a[196:199], v[52:55]\n\t"
"buffer_load_dwordx4 a[224:227], v252, s[8:11], s20 offen offset:1024 sc1\n\t"
"s_waitcnt lgkmcnt(2)\n\t"
"v_mfma_f32_16x16x32_bf16 v[226:229], v[226:229], v[218:221], 0\n\t"
"buffer_load_dwordx4 a[228:231], v252, s[8:11], s20 offen offset:2048 sc1\n\t"
"v_mfma_f32_16x16x32_bf16 v[56:59], a[236:239], a[192:195], v[56:59]\n\t"
"ds_read_b64_tr_b16 v[234:235], v244 offset:13824\n\t"
"v_mfma_f32_16x16x32_bf16 v[56:59], a[240:243], a[196:199], v[56:59]\n\t"
"v_mfma_f32_16x16x32_bf16 v[56:59], a[248:251], a[200:203], v[56:59]\n\t"
"ds_read_b64_tr_b16 v[236:237], v244 offset:16896\n\t"
"v_mfma_f32_16x16x32_bf16 v[60:63], a[236:239], a[196:199], v[60:63]\n\t"
"v_mfma_f32_16x16x32_bf16 v[60:63], a[240:243], a[200:203], v[60:63]\n\t"
"v_cvt_pk_bf16_f32 v226, v226, v227\n\t"
"v_cvt_pk_bf16_f32 v227, v228, v229\n\t"
"v_mfma_f32_16x16x32_bf16 v[60:63], a[248:251], a[204:207], v[60:63]\n\t"
"s_waitcnt lgkmcnt(2)\n\t"
"v_mfma_f32_16x16x32_bf16 v[230:233], v[230:233], v[218:221], 0\n\t"
"s_mov_b32 s20, 528384\n\t"
"buffer_load_dwordx4 a[232:235], v252, s[8:11], s20 offen offset:0 sc1\n\t"
"s_waitcnt vmcnt(4)\n\t"
"v_mfma_f32_16x16x32_bf16 v[64:67], a[212:215], a[168:171], v[64:67]\n\t"
"ds_read_b64_tr_b16 v[222:223], v244 offset:21504\n\t"
"v_mfma_f32_16x16x32_bf16 v[64:67], a[208:211], a[172:175], v[64:67]\n\t"
"ds_read_b64_tr_b16 v[224:225], v244 offset:24576\n\t"
"v_mfma_f32_16x16x32_bf16 v[64:67], a[216:219], a[176:179], v[64:67]\n\t"
"buffer_load_dwordx4 a[236:239], v252, s[8:11], s20 offen offset:1024 sc1\n\t"
"v_mfma_f32_16x16x32_bf16 v[68:71], a[212:215], a[172:175], v[68:71]\n\t"
"ds_read_b128 a[168:171], v242 offset:9216\n\t"
"v_mfma_f32_16x16x32_bf16 v[68:71], a[208:211], a[176:179], v[68:71]\n\t"
"v_cvt_pk_bf16_f32 v230, v230, v231\n\t"
"v_cvt_pk_bf16_f32 v231, v232, v233\n\t"
"v_mfma_f32_16x16x32_bf16 v[68:71], a[216:219], a[180:183], v[68:71]\n\t"
"ds_read_b128 a[172:175], v242 offset:9728\n\t"
"v_mfma_f32_16x16x32_bf16 v[72:75], a[212:215], a[176:179], v[72:75]\n\t"
"ds_read_b128 a[176:179], v242 offset:10240\n\t"
"v_mfma_f32_16x16x32_bf16 v[72:75], a[208:211], a[180:183], v[72:75]\n\t"
"v_mfma_f32_16x16x32_bf16 v[72:75], a[216:219], a[184:187], v[72:75]\n\t"
"buffer_load_dwordx4 a[240:243], v252, s[8:11], s20 offen offset:2048 sc1\n\t"
"s_waitcnt lgkmcnt(5)\n\t"
"v_mfma_f32_16x16x32_bf16 v[234:237], v[234:237], v[218:221], 0\n\t"
"s_mov_b32 s20, 540672\n\t"
"buffer_load_dwordx4 a[244:247], v252, s[8:11], s20 offen offset:0 sc1\n\t"
"v_mfma_f32_16x16x32_bf16 v[76:79], a[212:215], a[180:183], v[76:79]\n\t"
"ds_read_b128 a[180:183], v242 offset:10752\n\t"
"v_mfma_f32_16x16x32_bf16 v[76:79], a[208:211], a[184:187], v[76:79]\n\t"
"v_mfma_f32_16x16x32_bf16 v[76:79], a[216:219], a[188:191], v[76:79]\n\t"
"buffer_load_dwordx4 a[248:251], v252, s[8:11], s20 offen offset:1024 sc1\n\t"
"v_mfma_f32_16x16x32_bf16 v[80:83], a[212:215], a[184:187], v[80:83]\n\t"
"ds_read_b128 a[184:187], v242 offset:11264\n\t"
"v_mfma_f32_16x16x32_bf16 v[80:83], a[208:211], a[188:191], v[80:83]\n\t"
"v_cvt_pk_bf16_f32 v234, v234, v235\n\t"
"v_cvt_pk_bf16_f32 v235, v236, v237\n\t"
"v_mfma_f32_16x16x32_bf16 v[80:83], a[216:219], a[192:195], v[80:83]\n\t"
"buffer_load_dwordx4 a[252:255], v252, s[8:11], s20 offen offset:2048 sc1\n\t"
"s_waitcnt lgkmcnt(5)\n\t"
"v_mfma_f32_16x16x32_bf16 v[222:225], v[222:225], v[218:221], 0\n\t"
"v_mfma_f32_16x16x32_bf16 v[84:87], a[212:215], a[188:191], v[84:87]\n\t"
"ds_read_b128 a[188:191], v242 offset:11776\n\t"
"v_mfma_f32_16x16x32_bf16 v[84:87], a[208:211], a[192:195], v[84:87]\n\t"
"v_mfma_f32_16x16x32_bf16 v[84:87], a[216:219], a[196:199], v[84:87]\n\t"
"v_mfma_f32_16x16x32_bf16 v[88:91], a[212:215], a[192:195], v[88:91]\n\t"
"ds_read_b128 a[192:195], v242 offset:12288\n\t"
"v_mfma_f32_16x16x32_bf16 v[88:91], a[208:211], a[196:199], v[88:91]\n\t"
"s_nop 0\n\t"
"v_cvt_pk_bf16_f32 v222, v222, v223\n\t"
"v_mfma_f32_16x16x32_bf16 v[88:91], a[216:219], a[200:203], v[88:91]\n\t"
"v_cvt_pk_bf16_f32 v223, v224, v225\n\t"
"v_mfma_f32_16x16x32_bf16 v[92:95], a[212:215], a[196:199], v[92:95]\n\t"
"ds_read_b128 a[196:199], v242 offset:12800\n\t"
"v_mfma_f32_16x16x32_bf16 v[92:95], a[208:211], a[200:203], v[92:95]\n\t"
"ds_read_b128 a[200:203], v242 offset:13312\n\t"
"v_mfma_f32_16x16x32_bf16 v[92:95], a[216:219], a[204:207], v[92:95]\n\t"
"s_mov_b32 s20, 700416\n\t"
"buffer_load_dwordx4 a[212:215], v252, s[8:11], s20 offen offset:0 sc1\n\t"
"s_waitcnt vmcnt(7) lgkmcnt(6)\n\t"
"v_mfma_f32_16x16x32_bf16 v[98:101], a[220:223], a[168:171], v[98:101]\n\t"
"ds_read_b128 a[204:207], v242 offset:13824\n\t"
"v_mfma_f32_16x16x32_bf16 v[98:101], a[224:227], a[172:175], v[98:101]\n\t"
"v_mfma_f32_16x16x32_bf16 v[98:101], a[228:231], a[176:179], v[98:101]\n\t"
"buffer_load_dwordx4 a[208:211], v252, s[8:11], s20 offen offset:1024 sc1\n\t"
"s_waitcnt lgkmcnt(6)\n\t"
"v_mfma_f32_16x16x32_bf16 v[102:105], a[220:223], a[172:175], v[102:105]\n\t"
"v_mfma_f32_16x16x32_bf16 v[102:105], a[224:227], a[176:179], v[102:105]\n\t"
"v_mfma_f32_16x16x32_bf16 v[102:105], a[228:231], a[180:183], v[102:105]\n\t"
"buffer_load_dwordx4 a[216:219], v252, s[8:11], s20 offen offset:2048 sc1\n\t"
"s_waitcnt lgkmcnt(0)\n\t"
"s_barrier\n\t"
"v_mfma_f32_16x16x32_bf16 v[106:109], a[220:223], a[176:179], v[106:109]\n\t"
"v_mfma_f32_16x16x32_bf16 v[106:109], a[224:227], a[180:183], v[106:109]\n\t"
"v_mfma_f32_16x16x32_bf16 v[106:109], a[228:231], a[184:187], v[106:109]\n\t"
"ds_write_b64 v243, v[226:227] offset:0\n\t"
"ds_write_b64 v243, v[230:231] offset:512\n\t"
"v_mfma_f32_16x16x32_bf16 v[110:113], a[220:223], a[180:183], v[110:113]\n\t"
"ds_read_b64_tr_b16 v[226:227], v244 offset:27648\n\t"
"v_mfma_f32_16x16x32_bf16 v[110:113], a[224:227], a[184:187], v[110:113]\n\t"
"ds_read_b64_tr_b16 v[228:229], v244 offset:30720\n\t"
"v_mfma_f32_16x16x32_bf16 v[110:113], a[228:231], a[188:191], v[110:113]\n\t"
"ds_write_b64 v243, v[234:235] offset:1024\n\t"
"v_mfma_f32_16x16x32_bf16 v[114:117], a[220:223], a[184:187], v[114:117]\n\t"
"ds_read_b64_tr_b16 v[230:231], v244 offset:33792\n\t"
"v_mfma_f32_16x16x32_bf16 v[114:117], a[224:227], a[188:191], v[114:117]\n\t"
"ds_read_b64_tr_b16 v[232:233], v244 offset:36864\n\t"
"v_mfma_f32_16x16x32_bf16 v[114:117], a[228:231], a[192:195], v[114:117]\n\t"
"ds_write_b64 v243, v[222:223] offset:1536\n\t"
"v_mfma_f32_16x16x32_bf16 v[118:121], a[220:223], a[188:191], v[118:121]\n\t"
"ds_read_b64_tr_b16 v[234:235], v244 offset:41472\n\t"
"v_mfma_f32_16x16x32_bf16 v[118:121], a[224:227], a[192:195], v[118:121]\n\t"
"ds_read_b64_tr_b16 v[236:237], v244 offset:44544\n\t"
"v_mfma_f32_16x16x32_bf16 v[118:121], a[228:231], a[196:199], v[118:121]\n\t"
"ds_read_b64_tr_b16 v[222:223], v244 offset:47616\n\t"
"ds_read_b64_tr_b16 v[224:225], v244 offset:50688\n\t"
"s_waitcnt lgkmcnt(8)\n\t"
"v_mfma_f32_16x16x32_bf16 v[226:229], v[226:229], v[218:221], 0\n\t"
"s_add_u32 m0, s24, 32512\n\t"
"s_add_u32 s28, s21, 983040\n\t"
"buffer_load_dwordx4 v245, s[12:15], s28 offen offset:256 nt lds\n\t"
"v_mfma_f32_16x16x32_bf16 v[122:125], a[220:223], a[192:195], v[122:125]\n\t"
"v_mfma_f32_16x16x32_bf16 v[122:125], a[224:227], a[196:199], v[122:125]\n\t"
"v_mfma_f32_16x16x32_bf16 v[122:125], a[228:231], a[200:203], v[122:125]\n\t"
"s_add_u32 m0, s24, 40192\n\t"
"s_add_u32 s28, s21, 1179648\n\t"
"buffer_load_dwordx4 v245, s[12:15], s28 offen offset:256 nt lds\n\t"
"s_waitcnt lgkmcnt(5)\n\t"
"v_mfma_f32_16x16x32_bf16 v[230:233], v[230:233], v[218:221], 0\n\t"
"s_add_u32 m0, s24, 46336\n\t"
"s_add_u32 s28, s21, 1376256\n\t"
"buffer_load_dwordx4 v245, s[12:15], s28 offen offset:256 nt lds\n\t"
"v_mfma_f32_16x16x32_bf16 v[126:129], a[220:223], a[196:199], v[126:129]\n\t"
"v_cvt_pk_bf16_f32 v226, v226, v227\n\t"
"v_cvt_pk_bf16_f32 v227, v228, v229\n\t"
"v_mfma_f32_16x16x32_bf16 v[126:129], a[224:227], a[200:203], v[126:129]\n\t"
"v_mfma_f32_16x16x32_bf16 v[126:129], a[228:231], a[204:207], v[126:129]\n\t"
"s_add_u32 m0, s24, 52480\n\t"
"s_add_u32 s28, s21, 1572864\n\t"
"buffer_load_dwordx4 v245, s[12:15], s28 offen offset:256 nt lds\n\t"
"s_waitcnt lgkmcnt(2)\n\t"
"v_mfma_f32_16x16x32_bf16 v[234:237], v[234:237], v[218:221], 0\n\t"
"s_add_u32 m0, s24, 60160\n\t"
"s_add_u32 s28, s21, 1769472\n\t"
"buffer_load_dwordx4 v245, s[12:15], s28 offen offset:256 nt lds\n\t"
"s_waitcnt vmcnt(11)\n\t"
"v_mfma_f32_16x16x32_bf16 v[130:133], a[232:235], a[168:171], v[130:133]\n\t"
"v_cvt_pk_bf16_f32 v230, v230, v231\n\t"
"v_cvt_pk_bf16_f32 v231, v232, v233\n\t"
"v_mfma_f32_16x16x32_bf16 v[130:133], a[236:239], a[172:175], v[130:133]\n\t"
"v_mfma_f32_16x16x32_bf16 v[130:133], a[240:243], a[176:179], v[130:133]\n\t"
"s_add_u32 m0, s24, 66304\n\t"
"s_add_u32 s28, s21, 1966080\n\t"
"buffer_load_dwordx4 v245, s[12:15], s28 offen offset:256 nt lds\n\t"
"s_waitcnt lgkmcnt(0)\n\t"
"v_mfma_f32_16x16x32_bf16 v[222:225], v[222:225], v[218:221], 0\n\t"
"ds_write_b64 v243, v[226:227] offset:2048\n\t"
"s_add_u32 m0, s24, 72448\n\t"
"s_add_u32 s28, s21, 2162688\n\t"
"buffer_load_dwordx4 v245, s[12:15], s28 offen offset:256 nt lds\n\t"
"v_mfma_f32_16x16x32_bf16 v[134:137], a[232:235], a[172:175], v[134:137]\n\t"
"ds_read_b64_tr_b16 v[226:227], v244 offset:53760\n\t"
"v_cvt_pk_bf16_f32 v234, v234, v235\n\t"
"v_mfma_f32_16x16x32_bf16 v[134:137], a[236:239], a[176:179], v[134:137]\n\t"
"ds_read_b64_tr_b16 v[228:229], v244 offset:56832\n\t"
"v_cvt_pk_bf16_f32 v235, v236, v237\n\t"
"v_mfma_f32_16x16x32_bf16 v[134:137], a[240:243], a[180:183], v[134:137]\n\t"
"ds_write_b64 v243, v[230:231] offset:2560\n\t"
"v_mfma_f32_16x16x32_bf16 v[138:141], a[232:235], a[176:179], v[138:141]\n\t"
"v_mfma_f32_16x16x32_bf16 v[138:141], a[236:239], a[180:183], v[138:141]\n\t"
"v_cvt_pk_bf16_f32 v222, v222, v223\n\t"
"v_cvt_pk_bf16_f32 v223, v224, v225\n\t"
"v_mfma_f32_16x16x32_bf16 v[138:141], a[240:243], a[184:187], v[138:141]\n\t"
"ds_write_b64 v243, v[234:235] offset:3072\n\t"
"v_mfma_f32_16x16x32_bf16 v[142:145], a[232:235], a[180:183], v[142:145]\n\t"
"v_mfma_f32_16x16x32_bf16 v[142:145], a[236:239], a[184:187], v[142:145]\n\t"
"v_mfma_f32_16x16x32_bf16 v[142:145], a[240:243], a[188:191], v[142:145]\n\t"
"ds_write_b64 v243, v[222:223] offset:3584\n\t"
"s_waitcnt lgkmcnt(3)\n\t"
"v_mfma_f32_16x16x32_bf16 v[226:229], v[226:229], v[218:221], 0\n\t"
"v_mfma_f32_16x16x32_bf16 v[146:149], a[232:235], a[184:187], v[146:149]\n\t"
"v_mfma_f32_16x16x32_bf16 v[146:149], a[236:239], a[188:191], v[146:149]\n\t"
"v_mfma_f32_16x16x32_bf16 v[146:149], a[240:243], a[192:195], v[146:149]\n\t"
"v_mfma_f32_16x16x32_bf16 v[150:153], a[232:235], a[188:191], v[150:153]\n\t"
"v_mfma_f32_16x16x32_bf16 v[150:153], a[236:239], a[192:195], v[150:153]\n\t"
"s_nop 2\n\t"
"v_cvt_pk_bf16_f32 v226, v226, v227\n\t"
"v_cvt_pk_bf16_f32 v227, v228, v229\n\t"
"v_mfma_f32_16x16x32_bf16 v[150:153], a[240:243], a[196:199], v[150:153]\n\t"
"v_mfma_f32_16x16x32_bf16 v[154:157], a[232:235], a[192:195], v[154:157]\n\t"
"v_mfma_f32_16x16x32_bf16 v[154:157], a[236:239], a[196:199], v[154:157]\n\t"
"v_mfma_f32_16x16x32_bf16 v[154:157], a[240:243], a[200:203], v[154:157]\n\t"
"ds_write_b64 v243, v[226:227] offset:4096\n\t"
"v_mfma_f32_16x16x32_bf16 v[158:161], a[232:235], a[196:199], v[158:161]\n\t"
"v_mfma_f32_16x16x32_bf16 v[158:161], a[236:239], a[200:203], v[158:161]\n\t"
"v_mfma_f32_16x16x32_bf16 v[158:161], a[240:243], a[204:207], v[158:161]\n\t"
"s_waitcnt vmcnt(10)\n\t"
"v_mfma_f32_16x16x32_bf16 v[162:165], a[244:247], a[168:171], v[162:165]\n\t"
"v_mfma_f32_16x16x32_bf16 v[162:165], a[248:251], a[172:175], v[162:165]\n\t"
"v_mfma_f32_16x16x32_bf16 v[162:165], a[252:255], a[176:179], v[162:165]\n\t"
"s_waitcnt lgkmcnt(0)\n\t"
"s_barrier\n\t"
"v_mfma_f32_16x16x32_bf16 v[166:169], a[244:247], a[172:175], v[166:169]\n\t"
"ds_read_b128 a[168:171], v242 offset:0\n\t"
"v_mfma_f32_16x16x32_bf16 v[166:169], a[248:251], a[176:179], v[166:169]\n\t"
"ds_read_b128 a[172:175], v242 offset:512\n\t"
"v_mfma_f32_16x16x32_bf16 v[166:169], a[252:255], a[180:183], v[166:169]\n\t"
"v_mfma_f32_16x16x32_bf16 v[170:173], a[244:247], a[176:179], v[170:173]\n\t"
"ds_read_b128 a[176:179], v242 offset:1024\n\t"
"v_mfma_f32_16x16x32_bf16 v[170:173], a[248:251], a[180:183], v[170:173]\n\t"
"v_mfma_f32_16x16x32_bf16 v[170:173], a[252:255], a[184:187], v[170:173]\n\t"
"s_mov_b32 s20, 712704\n\t"
"buffer_load_dwordx4 a[220:223], v252, s[8:11], s20 offen offset:0 sc1\n\t"
"v_mfma_f32_16x16x32_bf16 v[174:177], a[244:247], a[180:183], v[174:177]\n\t"
"ds_read_b128 a[180:183], v242 offset:1536\n\t"
"v_mfma_f32_16x16x32_bf16 v[174:177], a[248:251], a[184:187], v[174:177]\n\t"
"v_mfma_f32_16x16x32_bf16 v[174:177], a[252:255], a[188:191], v[174:177]\n\t"
"buffer_load_dwordx4 a[224:227], v252, s[8:11], s20 offen offset:1024 sc1\n\t"
"v_mfma_f32_16x16x32_bf16 v[178:181], a[244:247], a[184:187], v[178:181]\n\t"
"ds_read_b128 a[184:187], v242 offset:2048\n\t"
"v_mfma_f32_16x16x32_bf16 v[178:181], a[248:251], a[188:191], v[178:181]\n\t"
"v_mfma_f32_16x16x32_bf16 v[178:181], a[252:255], a[192:195], v[178:181]\n\t"
"buffer_load_dwordx4 a[228:231], v252, s[8:11], s20 offen offset:2048 sc1\n\t"
"v_mfma_f32_16x16x32_bf16 v[182:185], a[244:247], a[188:191], v[182:185]\n\t"
"ds_read_b128 a[188:191], v242 offset:2560\n\t"
"v_mfma_f32_16x16x32_bf16 v[182:185], a[248:251], a[192:195], v[182:185]\n\t"
"v_mfma_f32_16x16x32_bf16 v[182:185], a[252:255], a[196:199], v[182:185]\n\t"
"s_mov_b32 s20, 724992\n\t"
"buffer_load_dwordx4 a[232:235], v252, s[8:11], s20 offen offset:0 sc1\n\t"
"v_mfma_f32_16x16x32_bf16 v[186:189], a[244:247], a[192:195], v[186:189]\n\t"
"ds_read_b128 a[192:195], v242 offset:3072\n\t"
"v_mfma_f32_16x16x32_bf16 v[186:189], a[248:251], a[196:199], v[186:189]\n\t"
"v_mfma_f32_16x16x32_bf16 v[186:189], a[252:255], a[200:203], v[186:189]\n\t"
"v_mfma_f32_16x16x32_bf16 v[190:193], a[244:247], a[196:199], v[190:193]\n\t"
"ds_read_b128 a[196:199], v242 offset:3584\n\t"
"v_mfma_f32_16x16x32_bf16 v[190:193], a[248:251], a[200:203], v[190:193]\n\t"
"ds_read_b128 a[200:203], v242 offset:4096\n\t"
"v_mfma_f32_16x16x32_bf16 v[190:193], a[252:255], a[204:207], v[190:193]\n\t"
"buffer_load_dwordx4 a[236:239], v252, s[8:11], s20 offen offset:1024 sc1\n\t"
"s_waitcnt vmcnt(12) lgkmcnt(6)\n\t"
"v_mfma_f32_16x16x32_bf16 v[0:3], a[212:215], a[168:171], v[0:3]\n\t"
"ds_read_b128 a[204:207], v242 offset:4608\n\t"
"v_mfma_f32_16x16x32_bf16 v[0:3], a[208:211], a[172:175], v[0:3]\n\t"
"v_mfma_f32_16x16x32_bf16 v[0:3], a[216:219], a[176:179], v[0:3]\n\t"
"s_add_u32 s23, s23, 6291456\n\t"
"s_add_u32 s22, s22, 6291456\n\t"
"buffer_load_dwordx4 a[240:243], v252, s[8:11], s20 offen offset:2048 sc1\n\t"
"s_waitcnt lgkmcnt(6)\n\t"
"v_mfma_f32_16x16x32_bf16 v[4:7], a[212:215], a[172:175], v[4:7]\n\t"
"v_mfma_f32_16x16x32_bf16 v[4:7], a[208:211], a[176:179], v[4:7]\n\t"
"v_mfma_f32_16x16x32_bf16 v[4:7], a[216:219], a[180:183], v[4:7]\n\t"
"s_mov_b32 s20, 737280\n\t"
"buffer_load_dwordx4 a[244:247], v252, s[8:11], s20 offen offset:0 sc1\n\t"
"s_waitcnt lgkmcnt(5)\n\t"
"v_mfma_f32_16x16x32_bf16 v[8:11], a[212:215], a[176:179], v[8:11]\n\t"
"v_mfma_f32_16x16x32_bf16 v[8:11], a[208:211], a[180:183], v[8:11]\n\t"
"v_mfma_f32_16x16x32_bf16 v[8:11], a[216:219], a[184:187], v[8:11]\n\t"
"s_waitcnt lgkmcnt(4)\n\t"
"v_mfma_f32_16x16x32_bf16 v[12:15], a[212:215], a[180:183], v[12:15]\n\t"
"v_mfma_f32_16x16x32_bf16 v[12:15], a[208:211], a[184:187], v[12:15]\n\t"
"v_mfma_f32_16x16x32_bf16 v[12:15], a[216:219], a[188:191], v[12:15]\n\t"
"buffer_load_dwordx4 a[248:251], v252, s[8:11], s20 offen offset:1024 sc1\n\t"
"s_waitcnt lgkmcnt(3)\n\t"
"v_mfma_f32_16x16x32_bf16 v[16:19], a[212:215], a[184:187], v[16:19]\n\t"
"v_xor_b32 v244, 0xc0, v244\n\t"
"v_mfma_f32_16x16x32_bf16 v[16:19], a[208:211], a[188:191], v[16:19]\n\t"
"v_mfma_f32_16x16x32_bf16 v[16:19], a[216:219], a[192:195], v[16:19]\n\t"
"buffer_load_dwordx4 a[252:255], v252, s[8:11], s20 offen offset:2048 sc1\n\t"
"s_waitcnt lgkmcnt(2)\n\t"
"v_mfma_f32_16x16x32_bf16 v[20:23], a[212:215], a[188:191], v[20:23]\n\t"
"v_mfma_f32_16x16x32_bf16 v[20:23], a[208:211], a[192:195], v[20:23]\n\t"
"v_mfma_f32_16x16x32_bf16 v[20:23], a[216:219], a[196:199], v[20:23]\n\t"
"s_waitcnt lgkmcnt(1)\n\t"
"v_mfma_f32_16x16x32_bf16 v[24:27], a[212:215], a[192:195], v[24:27]\n\t"
"v_mfma_f32_16x16x32_bf16 v[24:27], a[208:211], a[196:199], v[24:27]\n\t"
"v_mfma_f32_16x16x32_bf16 v[24:27], a[216:219], a[200:203], v[24:27]\n\t"
"s_waitcnt lgkmcnt(0)\n\t"
"v_mfma_f32_16x16x32_bf16 v[28:31], a[212:215], a[196:199], v[28:31]\n\t"
"v_mfma_f32_16x16x32_bf16 v[28:31], a[208:211], a[200:203], v[28:31]\n\t"
"v_mfma_f32_16x16x32_bf16 v[28:31], a[216:219], a[204:207], v[28:31]\n\t"
"s_mov_b32 s20, 749568\n\t"
"buffer_load_dwordx4 a[212:215], v252, s[8:11], s20 offen offset:0 sc1\n\t"
"s_waitcnt vmcnt(7)\n\t"
"v_mfma_f32_16x16x32_bf16 v[32:35], a[220:223], a[168:171], v[32:35]\n\t"
"v_mfma_f32_16x16x32_bf16 v[32:35], a[224:227], a[172:175], v[32:35]\n\t"
"v_mfma_f32_16x16x32_bf16 v[32:35], a[228:231], a[176:179], v[32:35]\n\t"
"buffer_load_dwordx4 a[208:211], v252, s[8:11], s20 offen offset:1024 sc1\n\t"
"v_mfma_f32_16x16x32_bf16 v[36:39], a[220:223], a[172:175], v[36:39]\n\t"
"ds_read_b64_tr_b16 v[230:231], v244 offset:1536\n\t"
"v_mfma_f32_16x16x32_bf16 v[36:39], a[224:227], a[176:179], v[36:39]\n\t"
"ds_read_b64_tr_b16 v[232:233], v244 offset:4608\n\t"
"v_mfma_f32_16x16x32_bf16 v[36:39], a[228:231], a[180:183], v[36:39]\n\t"
"buffer_load_dwordx4 a[216:219], v252, s[8:11], s20 offen offset:2048 sc1\n\t"
"v_mfma_f32_16x16x32_bf16 v[40:43], a[220:223], a[176:179], v[40:43]\n\t"
"v_mfma_f32_16x16x32_bf16 v[40:43], a[224:227], a[180:183], v[40:43]\n\t"
"v_mfma_f32_16x16x32_bf16 v[40:43], a[228:231], a[184:187], v[40:43]\n\t"
"v_mfma_f32_16x16x32_bf16 v[44:47], a[220:223], a[180:183], v[44:47]\n\t"
"ds_read_b64_tr_b16 v[234:235], v244 offset:7680\n\t"
"v_mfma_f32_16x16x32_bf16 v[44:47], a[224:227], a[184:187], v[44:47]\n\t"
"ds_read_b64_tr_b16 v[236:237], v244 offset:10752\n\t"
"v_mfma_f32_16x16x32_bf16 v[44:47], a[228:231], a[188:191], v[44:47]\n\t"
"v_mfma_f32_16x16x32_bf16 v[48:51], a[220:223], a[184:187], v[48:51]\n\t"
"v_mfma_f32_16x16x32_bf16 v[48:51], a[224:227], a[188:191], v[48:51]\n\t"
"v_mfma_f32_16x16x32_bf16 v[48:51], a[228:231], a[192:195], v[48:51]\n\t"
"v_mfma_f32_16x16x32_bf16 v[52:55], a[220:223], a[188:191], v[52:55]\n\t"
"v_mfma_f32_16x16x32_bf16 v[52:55], a[224:227], a[192:195], v[52:55]\n\t"
"v_mfma_f32_16x16x32_bf16 v[52:55], a[228:231], a[196:199], v[52:55]\n\t"
"s_waitcnt lgkmcnt(2)\n\t"
"v_mfma_f32_16x16x32_bf16 v[230:233], v[230:233], v[218:221], 0\n\t"
"v_mfma_f32_16x16x32_bf16 v[56:59], a[220:223], a[192:195], v[56:59]\n\t"
"ds_read_b64_tr_b16 v[222:223], v244 offset:13824\n\t"
"v_mfma_f32_16x16x32_bf16 v[56:59], a[224:227], a[196:199], v[56:59]\n\t"
"ds_read_b64_tr_b16 v[224:225], v244 offset:16896\n\t"
"v_mfma_f32_16x16x32_bf16 v[56:59], a[228:231], a[200:203], v[56:59]\n\t"
"v_mfma_f32_16x16x32_bf16 v[60:63], a[220:223], a[196:199], v[60:63]\n\t"
"v_mfma_f32_16x16x32_bf16 v[60:63], a[224:227], a[200:203], v[60:63]\n\t"
"s_nop 0\n\t"
"v_cvt_pk_bf16_f32 v230, v230, v231\n\t"
"v_cvt_pk_bf16_f32 v231, v232, v233\n\t"
"v_mfma_f32_16x16x32_bf16 v[60:63], a[228:231], a[204:207], v[60:63]\n\t"
"s_waitcnt lgkmcnt(2)\n\t"
"v_mfma_f32_16x16x32_bf16 v[234:237], v[234:237], v[218:221], 0\n\t"
"s_mov_b32 s20, 761856\n\t"
"buffer_load_dwordx4 a[220:223], v252, s[8:11], s20 offen offset:0 sc1\n\t"
"s_waitcnt vmcnt(7)\n\t"
"v_mfma_f32_16x16x32_bf16 v[64:67], a[232:235], a[168:171], v[64:67]\n\t"
"ds_read_b64_tr_b16 v[226:227], v244 offset:21504\n\t"
"v_mfma_f32_16x16x32_bf16 v[64:67], a[236:239], a[172:175], v[64:67]\n\t"
"ds_read_b64_tr_b16 v[228:229], v244 offset:24576\n\t"
"v_mfma_f32_16x16x32_bf16 v[64:67], a[240:243], a[176:179], v[64:67]\n\t"
"buffer_load_dwordx4 a[224:227], v252, s[8:11], s20 offen offset:1024 sc1\n\t"
"v_mfma_f32_16x16x32_bf16 v[68:71], a[232:235], a[172:175], v[68:71]\n\t"
"ds_read_b128 a[168:171], v242 offset:9216\n\t"
"v_mfma_f32_16x16x32_bf16 v[68:71], a[236:239], a[176:179], v[68:71]\n\t"
"v_cvt_pk_bf16_f32 v234, v234, v235\n\t"
"v_cvt_pk_bf16_f32 v235, v236, v237\n\t"
"v_mfma_f32_16x16x32_bf16 v[68:71], a[240:243], a[180:183], v[68:71]\n\t"
"buffer_load_dwordx4 a[228:231], v252, s[8:11], s20 offen offset:2048 sc1\n\t"
"v_mfma_f32_16x16x32_bf16 v[72:75], a[232:235], a[176:179], v[72:75]\n\t"
"ds_read_b128 a[172:175], v242 offset:9728\n\t"
"v_mfma_f32_16x16x32_bf16 v[72:75], a[236:239], a[180:183], v[72:75]\n\t"
"ds_read_b128 a[176:179], v242 offset:10240\n\t"
"v_mfma_f32_16x16x32_bf16 v[72:75], a[240:243], a[184:187], v[72:75]\n\t"
"s_add_u32 m0, s24, 80128\n\t"
"s_add_u32 s28, s21, 2359296\n\t"
"buffer_load_dwordx4 v245, s[12:15], s28 offen offset:256 nt lds\n\t"
"s_add_u32 m0, s24, 86272\n\t"
"s_add_u32 s28, s21, 2555904\n\t"
"buffer_load_dwordx4 v245, s[12:15], s28 offen offset:256 nt lds\n\t"
"s_waitcnt lgkmcnt(5)\n\t"
"v_mfma_f32_16x16x32_bf16 v[222:225], v[222:225], v[218:221], 0\n\t"
"v_mfma_f32_16x16x32_bf16 v[76:79], a[232:235], a[180:183], v[76:79]\n\t"
"ds_read_b128 a[180:183], v242 offset:10752\n\t"
"v_mfma_f32_16x16x32_bf16 v[76:79], a[236:239], a[184:187], v[76:79]\n\t"
"v_mfma_f32_16x16x32_bf16 v[76:79], a[240:243], a[188:191], v[76:79]\n\t"
"s_add_u32 m0, s24, 92416\n\t"
"s_add_u32 s28, s21, 2752512\n\t"
"buffer_load_dwordx4 v245, s[12:15], s28 offen offset:256 nt lds\n\t"
"s_add_u32 m0, s24, 100096\n\t"
"s_add_u32 s28, s21, 2949120\n\t"
"buffer_load_dwordx4 v245, s[12:15], s28 offen offset:256 nt lds\n\t"
"v_mfma_f32_16x16x32_bf16 v[80:83], a[232:235], a[184:187], v[80:83]\n\t"
"ds_read_b128 a[184:187], v242 offset:11264\n\t"
"v_mfma_f32_16x16x32_bf16 v[80:83], a[236:239], a[188:191], v[80:83]\n\t"
"v_cvt_pk_bf16_f32 v222, v222, v223\n\t"
"v_cvt_pk_bf16_f32 v223, v224, v225\n\t"
"v_mfma_f32_16x16x32_bf16 v[80:83], a[240:243], a[192:195], v[80:83]\n\t"
"s_add_u32 m0, s24, 106240\n\t"
"s_add_u32 s28, s21, 3145728\n\t"
"buffer_load_dwordx4 v245, s[12:15], s28 offen offset:256 nt lds\n\t"
"s_waitcnt lgkmcnt(5)\n\t"
"v_mfma_f32_16x16x32_bf16 v[226:229], v[226:229], v[218:221], 0\n\t"
"s_add_u32 m0, s24, 112384\n\t"
"s_add_u32 s28, s21, 3342336\n\t"
"buffer_load_dwordx4 v245, s[12:15], s28 offen offset:256 nt lds\n\t"
"v_mfma_f32_16x16x32_bf16 v[84:87], a[232:235], a[188:191], v[84:87]\n\t"
"ds_read_b128 a[188:191], v242 offset:11776\n\t"
"v_mfma_f32_16x16x32_bf16 v[84:87], a[236:239], a[192:195], v[84:87]\n\t"
"v_mfma_f32_16x16x32_bf16 v[84:87], a[240:243], a[196:199], v[84:87]\n\t"
"v_mfma_f32_16x16x32_bf16 v[88:91], a[232:235], a[192:195], v[88:91]\n\t"
"ds_read_b128 a[192:195], v242 offset:12288\n\t"
"v_mfma_f32_16x16x32_bf16 v[88:91], a[236:239], a[196:199], v[88:91]\n\t"
"v_cvt_pk_bf16_f32 v226, v226, v227\n\t"
"v_mfma_f32_16x16x32_bf16 v[88:91], a[240:243], a[200:203], v[88:91]\n\t"
"v_cvt_pk_bf16_f32 v227, v228, v229\n\t"
"v_mfma_f32_16x16x32_bf16 v[92:95], a[232:235], a[196:199], v[92:95]\n\t"
"ds_read_b128 a[196:199], v242 offset:12800\n\t"
"v_mfma_f32_16x16x32_bf16 v[92:95], a[236:239], a[200:203], v[92:95]\n\t"
"ds_read_b128 a[200:203], v242 offset:13312\n\t"
"v_mfma_f32_16x16x32_bf16 v[92:95], a[240:243], a[204:207], v[92:95]\n\t"
"ds_read_b128 a[204:207], v242 offset:13824\n\t"
"s_waitcnt vmcnt(12) lgkmcnt(7)\n\t"
"v_mfma_f32_16x16x32_bf16 v[98:101], a[244:247], a[168:171], v[98:101]\n\t"
"v_mfma_f32_16x16x32_bf16 v[98:101], a[248:251], a[172:175], v[98:101]\n\t"
"v_mfma_f32_16x16x32_bf16 v[98:101], a[252:255], a[176:179], v[98:101]\n\t"
"s_waitcnt lgkmcnt(6)\n\t"
"v_mfma_f32_16x16x32_bf16 v[102:105], a[244:247], a[172:175], v[102:105]\n\t"
"v_mfma_f32_16x16x32_bf16 v[102:105], a[248:251], a[176:179], v[102:105]\n\t"
"v_mfma_f32_16x16x32_bf16 v[102:105], a[252:255], a[180:183], v[102:105]\n\t"
"s_waitcnt lgkmcnt(0)\n\t"
"s_barrier\n\t"
"v_mfma_f32_16x16x32_bf16 v[106:109], a[244:247], a[176:179], v[106:109]\n\t"
"v_mfma_f32_16x16x32_bf16 v[106:109], a[248:251], a[180:183], v[106:109]\n\t"
"v_mfma_f32_16x16x32_bf16 v[106:109], a[252:255], a[184:187], v[106:109]\n\t"
"ds_write_b64 v243, v[230:231] offset:0\n\t"
"ds_write_b64 v243, v[234:235] offset:512\n\t"
"v_mfma_f32_16x16x32_bf16 v[110:113], a[244:247], a[180:183], v[110:113]\n\t"
"ds_read_b64_tr_b16 v[230:231], v244 offset:27648\n\t"
"v_mfma_f32_16x16x32_bf16 v[110:113], a[248:251], a[184:187], v[110:113]\n\t"
"ds_read_b64_tr_b16 v[232:233], v244 offset:30720\n\t"
"v_mfma_f32_16x16x32_bf16 v[110:113], a[252:255], a[188:191], v[110:113]\n\t"
"ds_write_b64 v243, v[222:223] offset:1024\n\t"
"v_mfma_f32_16x16x32_bf16 v[114:117], a[244:247], a[184:187], v[114:117]\n\t"
"ds_read_b64_tr_b16 v[234:235], v244 offset:33792\n\t"
"v_mfma_f32_16x16x32_bf16 v[114:117], a[248:251], a[188:191], v[114:117]\n\t"
"ds_read_b64_tr_b16 v[236:237], v244 offset:36864\n\t"
"v_mfma_f32_16x16x32_bf16 v[114:117], a[252:255], a[192:195], v[114:117]\n\t"
"ds_write_b64 v243, v[226:227] offset:1536\n\t"
"v_mfma_f32_16x16x32_bf16 v[118:121], a[244:247], a[188:191], v[118:121]\n\t"
"ds_read_b64_tr_b16 v[222:223], v244 offset:41472\n\t"
"v_mfma_f32_16x16x32_bf16 v[118:121], a[248:251], a[192:195], v[118:121]\n\t"
"ds_read_b64_tr_b16 v[224:225], v244 offset:44544\n\t"
"v_mfma_f32_16x16x32_bf16 v[118:121], a[252:255], a[196:199], v[118:121]\n\t"
"s_mov_b32 s20, 774144\n\t"
"buffer_load_dwordx4 a[232:235], v252, s[8:11], s20 offen offset:0 sc1\n\t"
"s_waitcnt lgkmcnt(6)\n\t"
"v_mfma_f32_16x16x32_bf16 v[230:233], v[230:233], v[218:221], 0\n\t"
"ds_read_b64_tr_b16 v[226:227], v244 offset:47616\n\t"
"ds_read_b64_tr_b16 v[228:229], v244 offset:50688\n\t"
"v_mfma_f32_16x16x32_bf16 v[122:125], a[244:247], a[192:195], v[122:125]\n\t"
"v_mfma_f32_16x16x32_bf16 v[122:125], a[248:251], a[196:199], v[122:125]\n\t"
"v_mfma_f32_16x16x32_bf16 v[122:125], a[252:255], a[200:203], v[122:125]\n\t"
"s_waitcnt lgkmcnt(5)\n\t"
"v_mfma_f32_16x16x32_bf16 v[234:237], v[234:237], v[218:221], 0\n\t"
"v_mfma_f32_16x16x32_bf16 v[126:129], a[244:247], a[196:199], v[126:129]\n\t"
"v_cvt_pk_bf16_f32 v230, v230, v231\n\t"
"v_cvt_pk_bf16_f32 v231, v232, v233\n\t"
"v_mfma_f32_16x16x32_bf16 v[126:129], a[248:251], a[200:203], v[126:129]\n\t"
"v_mfma_f32_16x16x32_bf16 v[126:129], a[252:255], a[204:207], v[126:129]\n\t"
"ds_write_b64 v243, v[230:231] offset:2048\n\t"
"s_waitcnt lgkmcnt(3)\n\t"
"v_mfma_f32_16x16x32_bf16 v[222:225], v[222:225], v[218:221], 0\n\t"
"ds_read_b64_tr_b16 v[230:231], v244 offset:53760\n\t"
"ds_read_b64_tr_b16 v[232:233], v244 offset:56832\n\t"
"s_waitcnt vmcnt(10)\n\t"
"v_mfma_f32_16x16x32_bf16 v[130:133], a[212:215], a[168:171], v[130:133]\n\t"
"v_cvt_pk_bf16_f32 v234, v234, v235\n\t"
"v_mfma_f32_16x16x32_bf16 v[130:133], a[208:211], a[172:175], v[130:133]\n\t"
"v_cvt_pk_bf16_f32 v235, v236, v237\n\t"
"v_mfma_f32_16x16x32_bf16 v[130:133], a[216:219], a[176:179], v[130:133]\n\t"
"s_waitcnt lgkmcnt(3)\n\t"
"v_mfma_f32_16x16x32_bf16 v[226:229], v[226:229], v[218:221], 0\n\t"
"ds_write_b64 v243, v[234:235] offset:2560\n\t"
"v_mfma_f32_16x16x32_bf16 v[134:137], a[212:215], a[172:175], v[134:137]\n\t"
"v_cvt_pk_bf16_f32 v222, v222, v223\n\t"
"v_cvt_pk_bf16_f32 v223, v224, v225\n\t"
"v_mfma_f32_16x16x32_bf16 v[134:137], a[208:211], a[176:179], v[134:137]\n\t"
"v_mfma_f32_16x16x32_bf16 v[134:137], a[216:219], a[180:183], v[134:137]\n\t"
"ds_write_b64 v243, v[222:223] offset:3072\n\t"
"v_mfma_f32_16x16x32_bf16 v[138:141], a[212:215], a[176:179], v[138:141]\n\t"
"v_mfma_f32_16x16x32_bf16 v[138:141], a[208:211], a[180:183], v[138:141]\n\t"
"v_cvt_pk_bf16_f32 v226, v226, v227\n\t"
"v_cvt_pk_bf16_f32 v227, v228, v229\n\t"
"v_mfma_f32_16x16x32_bf16 v[138:141], a[216:219], a[184:187], v[138:141]\n\t"
"v_mfma_f32_16x16x32_bf16 v[142:145], a[212:215], a[180:183], v[142:145]\n\t"
"v_mfma_f32_16x16x32_bf16 v[142:145], a[208:211], a[184:187], v[142:145]\n\t"
"v_mfma_f32_16x16x32_bf16 v[142:145], a[216:219], a[188:191], v[142:145]\n\t"
"buffer_load_dwordx4 a[236:239], v252, s[8:11], s20 offen offset:1024 sc1\n\t"
"s_waitcnt lgkmcnt(2)\n\t"
"v_mfma_f32_16x16x32_bf16 v[230:233], v[230:233], v[218:221], 0\n\t"
"ds_write_b64 v243, v[226:227] offset:3584\n\t"
"v_mfma_f32_16x16x32_bf16 v[146:149], a[212:215], a[184:187], v[146:149]\n\t"
"v_mfma_f32_16x16x32_bf16 v[146:149], a[208:211], a[188:191], v[146:149]\n\t"
"v_mfma_f32_16x16x32_bf16 v[146:149], a[216:219], a[192:195], v[146:149]\n\t"
"buffer_load_dwordx4 a[240:243], v252, s[8:11], s20 offen offset:2048 sc1\n\t"
"v_mfma_f32_16x16x32_bf16 v[150:153], a[212:215], a[188:191], v[150:153]\n\t"
"v_mfma_f32_16x16x32_bf16 v[150:153], a[208:211], a[192:195], v[150:153]\n\t"
"s_nop 0\n\t"
"v_cvt_pk_bf16_f32 v230, v230, v231\n\t"
"v_cvt_pk_bf16_f32 v231, v232, v233\n\t"
"v_mfma_f32_16x16x32_bf16 v[150:153], a[216:219], a[196:199], v[150:153]\n\t"
"s_mov_b32 s20, 786432\n\t"
"buffer_load_dwordx4 a[244:247], v252, s[8:11], s20 offen offset:0 sc1\n\t"
"v_mfma_f32_16x16x32_bf16 v[154:157], a[212:215], a[192:195], v[154:157]\n\t"
"v_mfma_f32_16x16x32_bf16 v[154:157], a[208:211], a[196:199], v[154:157]\n\t"
"v_mfma_f32_16x16x32_bf16 v[154:157], a[216:219], a[200:203], v[154:157]\n\t"
"ds_write_b64 v243, v[230:231] offset:4096\n\t"
"v_mfma_f32_16x16x32_bf16 v[158:161], a[212:215], a[196:199], v[158:161]\n\t"
"v_mfma_f32_16x16x32_bf16 v[158:161], a[208:211], a[200:203], v[158:161]\n\t"
"v_mfma_f32_16x16x32_bf16 v[158:161], a[216:219], a[204:207], v[158:161]\n\t"
"buffer_load_dwordx4 a[248:251], v252, s[8:11], s20 offen offset:1024 sc1\n\t"
"s_waitcnt vmcnt(11)\n\t"
"v_mfma_f32_16x16x32_bf16 v[162:165], a[220:223], a[168:171], v[162:165]\n\t"
"v_mfma_f32_16x16x32_bf16 v[162:165], a[224:227], a[172:175], v[162:165]\n\t"
"v_mfma_f32_16x16x32_bf16 v[162:165], a[228:231], a[176:179], v[162:165]\n\t"
"buffer_load_dwordx4 a[208:211], v252, s[8:11], s20 offen offset:2048 sc1\n\t"
"s_waitcnt lgkmcnt(0)\n\t"
"s_barrier\n\t"
"v_mfma_f32_16x16x32_bf16 v[166:169], a[220:223], a[172:175], v[166:169]\n\t"
"ds_read_b128 a[168:171], v242 offset:0\n\t"
"v_mfma_f32_16x16x32_bf16 v[166:169], a[224:227], a[176:179], v[166:169]\n\t"
"ds_read_b128 a[172:175], v242 offset:512\n\t"
"v_mfma_f32_16x16x32_bf16 v[166:169], a[228:231], a[180:183], v[166:169]\n\t"
"s_mov_b32 s20, 798720\n\t"
"buffer_load_dwordx4 a[212:215], v252, s[8:11], s20 offen offset:0 sc1\n\t"
"v_mfma_f32_16x16x32_bf16 v[170:173], a[220:223], a[176:179], v[170:173]\n\t"
"ds_read_b128 a[176:179], v242 offset:1024\n\t"
"v_mfma_f32_16x16x32_bf16 v[170:173], a[224:227], a[180:183], v[170:173]\n\t"
"v_mfma_f32_16x16x32_bf16 v[170:173], a[228:231], a[184:187], v[170:173]\n\t"
"buffer_load_dwordx4 a[216:219], v252, s[8:11], s20 offen offset:1024 sc1\n\t"
"v_mfma_f32_16x16x32_bf16 v[174:177], a[220:223], a[180:183], v[174:177]\n\t"
"ds_read_b128 a[180:183], v242 offset:1536\n\t"
"v_mfma_f32_16x16x32_bf16 v[174:177], a[224:227], a[184:187], v[174:177]\n\t"
"v_mfma_f32_16x16x32_bf16 v[174:177], a[228:231], a[188:191], v[174:177]\n\t"
"buffer_load_dwordx4 a[252:255], v252, s[8:11], s20 offen offset:2048 sc1\n\t"
"v_mfma_f32_16x16x32_bf16 v[178:181], a[220:223], a[184:187], v[178:181]\n\t"
"ds_read_b128 a[184:187], v242 offset:2048\n\t"
"v_mfma_f32_16x16x32_bf16 v[178:181], a[224:227], a[188:191], v[178:181]\n\t"
"v_mfma_f32_16x16x32_bf16 v[178:181], a[228:231], a[192:195], v[178:181]\n\t"
"v_mfma_f32_16x16x32_bf16 v[182:185], a[220:223], a[188:191], v[182:185]\n\t"
"ds_read_b128 a[188:191], v242 offset:2560\n\t"
"v_mfma_f32_16x16x32_bf16 v[182:185], a[224:227], a[192:195], v[182:185]\n\t"
"v_mfma_f32_16x16x32_bf16 v[182:185], a[228:231], a[196:199], v[182:185]\n\t"
"v_mfma_f32_16x16x32_bf16 v[186:189], a[220:223], a[192:195], v[186:189]\n\t"
"ds_read_b128 a[192:195], v242 offset:3072\n\t"
"v_mfma_f32_16x16x32_bf16 v[186:189], a[224:227], a[196:199], v[186:189]\n\t"
"v_mfma_f32_16x16x32_bf16 v[186:189], a[228:231], a[200:203], v[186:189]\n\t"
"v_mfma_f32_16x16x32_bf16 v[190:193], a[220:223], a[196:199], v[190:193]\n\t"
"ds_read_b128 a[196:199], v242 offset:3584\n\t"
"v_mfma_f32_16x16x32_bf16 v[190:193], a[224:227], a[200:203], v[190:193]\n\t"
"ds_read_b128 a[200:203], v242 offset:4096\n\t"
"v_mfma_f32_16x16x32_bf16 v[190:193], a[228:231], a[204:207], v[190:193]\n\t"
"s_mov_b32 s20, 811008\n\t"
"buffer_load_dwordx4 a[220:223], v252, s[8:11], s20 offen offset:0 sc1\n\t"
"s_waitcnt vmcnt(7) lgkmcnt(6)\n\t"
"v_mfma_f32_16x16x32_bf16 v[194:197], a[232:235], a[168:171], v[194:197]\n\t"
"ds_read_b128 a[204:207], v242 offset:4608\n\t"
"v_mfma_f32_16x16x32_bf16 v[194:197], a[236:239], a[172:175], v[194:197]\n\t"
"v_mfma_f32_16x16x32_bf16 v[194:197], a[240:243], a[176:179], v[194:197]\n\t"
"buffer_load_dwordx4 a[224:227], v252, s[8:11], s20 offen offset:1024 sc1\n\t"
"s_waitcnt lgkmcnt(6)\n\t"
"v_mfma_f32_16x16x32_bf16 v[202:205], a[232:235], a[172:175], v[202:205]\n\t"
"v_mfma_f32_16x16x32_bf16 v[202:205], a[236:239], a[176:179], v[202:205]\n\t"
"v_mfma_f32_16x16x32_bf16 v[202:205], a[240:243], a[180:183], v[202:205]\n\t"
"buffer_load_dwordx4 a[228:231], v252, s[8:11], s20 offen offset:2048 sc1\n\t"
"s_waitcnt lgkmcnt(5)\n\t"
"v_mfma_f32_16x16x32_bf16 v[210:213], a[232:235], a[176:179], v[210:213]\n\t"
"v_mfma_f32_16x16x32_bf16 v[210:213], a[236:239], a[180:183], v[210:213]\n\t"
"v_mfma_f32_16x16x32_bf16 v[210:213], a[240:243], a[184:187], v[210:213]\n\t"
"s_waitcnt lgkmcnt(4)\n\t"
"v_mfma_f32_16x16x32_bf16 a[0:3], a[232:235], a[180:183], a[0:3]\n\t"
"v_mfma_f32_16x16x32_bf16 a[0:3], a[236:239], a[184:187], a[0:3]\n\t"
"v_mfma_f32_16x16x32_bf16 a[0:3], a[240:243], a[188:191], a[0:3]\n\t"
"s_waitcnt lgkmcnt(3)\n\t"
"v_mfma_f32_16x16x32_bf16 a[8:11], a[232:235], a[184:187], a[8:11]\n\t"
"v_mfma_f32_16x16x32_bf16 a[8:11], a[236:239], a[188:191], a[8:11]\n\t"
"v_mfma_f32_16x16x32_bf16 a[8:11], a[240:243], a[192:195], a[8:11]\n\t"
"s_waitcnt lgkmcnt(2)\n\t"
"v_mfma_f32_16x16x32_bf16 a[16:19], a[232:235], a[188:191], a[16:19]\n\t"
"v_mfma_f32_16x16x32_bf16 a[16:19], a[236:239], a[192:195], a[16:19]\n\t"
"v_mfma_f32_16x16x32_bf16 a[16:19], a[240:243], a[196:199], a[16:19]\n\t"
"s_waitcnt lgkmcnt(1)\n\t"
"v_mfma_f32_16x16x32_bf16 a[24:27], a[232:235], a[192:195], a[24:27]\n\t"
"v_mfma_f32_16x16x32_bf16 a[24:27], a[236:239], a[196:199], a[24:27]\n\t"
"v_mfma_f32_16x16x32_bf16 a[24:27], a[240:243], a[200:203], a[24:27]\n\t"
"s_waitcnt lgkmcnt(0)\n\t"
"v_mfma_f32_16x16x32_bf16 a[32:35], a[232:235], a[196:199], a[32:35]\n\t"
"v_mfma_f32_16x16x32_bf16 a[32:35], a[236:239], a[200:203], a[32:35]\n\t"
"v_mfma_f32_16x16x32_bf16 a[32:35], a[240:243], a[204:207], a[32:35]\n\t"
"s_mov_b32 s20, 823296\n\t"
"buffer_load_dwordx4 a[232:235], v252, s[8:11], s20 offen offset:0 sc1\n\t"
"s_waitcnt vmcnt(7)\n\t"
"v_mfma_f32_16x16x32_bf16 a[40:43], a[244:247], a[168:171], a[40:43]\n\t"
"v_mfma_f32_16x16x32_bf16 a[40:43], a[248:251], a[172:175], a[40:43]\n\t"
"v_mfma_f32_16x16x32_bf16 a[40:43], a[208:211], a[176:179], a[40:43]\n\t"
"buffer_load_dwordx4 a[236:239], v252, s[8:11], s20 offen offset:1024 sc1\n\t"
"v_mfma_f32_16x16x32_bf16 a[48:51], a[244:247], a[172:175], a[48:51]\n\t"
"v_mfma_f32_16x16x32_bf16 a[48:51], a[248:251], a[176:179], a[48:51]\n\t"
"v_mfma_f32_16x16x32_bf16 a[48:51], a[208:211], a[180:183], a[48:51]\n\t"
"buffer_load_dwordx4 a[240:243], v252, s[8:11], s20 offen offset:2048 sc1\n\t"
"v_mfma_f32_16x16x32_bf16 a[56:59], a[244:247], a[176:179], a[56:59]\n\t"
"v_mfma_f32_16x16x32_bf16 a[56:59], a[248:251], a[180:183], a[56:59]\n\t"
"v_mfma_f32_16x16x32_bf16 a[56:59], a[208:211], a[184:187], a[56:59]\n\t"
"v_mfma_f32_16x16x32_bf16 a[64:67], a[244:247], a[180:183], a[64:67]\n\t"
"v_mfma_f32_16x16x32_bf16 a[64:67], a[248:251], a[184:187], a[64:67]\n\t"
"v_mfma_f32_16x16x32_bf16 a[64:67], a[208:211], a[188:191], a[64:67]\n\t"
"s_barrier\n\t"
"v_mfma_f32_16x16x32_bf16 a[72:75], a[244:247], a[184:187], a[72:75]\n\t"
"ds_read_b64_tr_b16 v[234:235], v244 offset:512\n\t"
"v_mfma_f32_16x16x32_bf16 a[72:75], a[248:251], a[188:191], a[72:75]\n\t"
"ds_read_b64_tr_b16 v[236:237], v244 offset:3584\n\t"
"v_mfma_f32_16x16x32_bf16 a[72:75], a[208:211], a[192:195], a[72:75]\n\t"
"ds_read_b64_tr_b16 v[222:223], v244 offset:6656\n\t"
"ds_read_b64_tr_b16 v[224:225], v244 offset:9728\n\t"
"v_mfma_f32_16x16x32_bf16 a[80:83], a[244:247], a[188:191], a[80:83]\n\t"
"v_mfma_f32_16x16x32_bf16 a[80:83], a[248:251], a[192:195], a[80:83]\n\t"
"v_mfma_f32_16x16x32_bf16 a[80:83], a[208:211], a[196:199], a[80:83]\n\t"
"v_mfma_f32_16x16x32_bf16 a[88:91], a[244:247], a[192:195], a[88:91]\n\t"
"v_mfma_f32_16x16x32_bf16 a[88:91], a[248:251], a[196:199], a[88:91]\n\t"
"ds_read_b64_tr_b16 v[226:227], v244 offset:12800\n\t"
"v_mfma_f32_16x16x32_bf16 a[88:91], a[208:211], a[200:203], a[88:91]\n\t"
"ds_read_b64_tr_b16 v[228:229], v244 offset:15872\n\t"
"s_waitcnt lgkmcnt(4)\n\t"
"v_mfma_f32_16x16x32_bf16 v[234:237], v[234:237], v[218:221], 0\n\t"
"v_mfma_f32_16x16x32_bf16 a[96:99], a[244:247], a[196:199], a[96:99]\n\t"
"v_mfma_f32_16x16x32_bf16 a[96:99], a[248:251], a[200:203], a[96:99]\n\t"
"v_mfma_f32_16x16x32_bf16 a[96:99], a[208:211], a[204:207], a[96:99]\n\t"
"s_mov_b32 s20, 835584\n\t"
"buffer_load_dwordx4 a[244:247], v252, s[8:11], s20 offen offset:0 sc1\n\t"
"s_waitcnt vmcnt(7)\n\t"
"v_mfma_f32_16x16x32_bf16 a[104:107], a[212:215], a[168:171], a[104:107]\n\t"
"ds_read_b128 a[168:171], v242 offset:9216\n\t"
"v_mfma_f32_16x16x32_bf16 a[104:107], a[216:219], a[172:175], a[104:107]\n\t"
"v_cvt_pk_bf16_f32 v234, v234, v235\n\t"
"v_cvt_pk_bf16_f32 v235, v236, v237\n\t"
"v_mfma_f32_16x16x32_bf16 a[104:107], a[252:255], a[176:179], a[104:107]\n\t"
"buffer_load_dwordx4 a[208:211], v252, s[8:11], s20 offen offset:1024 sc1\n\t"
"s_waitcnt lgkmcnt(3)\n\t"
"v_mfma_f32_16x16x32_bf16 v[222:225], v[222:225], v[218:221], 0\n\t"
"ds_read_b64_tr_b16 v[230:231], v244 offset:20480\n\t"
"v_mfma_f32_16x16x32_bf16 a[112:115], a[212:215], a[172:175], a[112:115]\n\t"
"ds_read_b64_tr_b16 v[232:233], v244 offset:23552\n\t"
"v_mfma_f32_16x16x32_bf16 a[112:115], a[216:219], a[176:179], a[112:115]\n\t"
"ds_read_b128 a[172:175], v242 offset:9728\n\t"
"v_mfma_f32_16x16x32_bf16 a[112:115], a[252:255], a[180:183], a[112:115]\n\t"
"buffer_load_dwordx4 a[248:251], v252, s[8:11], s20 offen offset:2048 sc1\n\t"
"v_mfma_f32_16x16x32_bf16 a[120:123], a[212:215], a[176:179], a[120:123]\n\t"
"v_mfma_f32_16x16x32_bf16 a[120:123], a[216:219], a[180:183], a[120:123]\n\t"
"v_cvt_pk_bf16_f32 v222, v222, v223\n\t"
"v_mfma_f32_16x16x32_bf16 a[120:123], a[252:255], a[184:187], a[120:123]\n\t"
"v_cvt_pk_bf16_f32 v223, v224, v225\n\t"
"s_waitcnt lgkmcnt(4)\n\t"
"v_mfma_f32_16x16x32_bf16 v[226:229], v[226:229], v[218:221], 0\n\t"
"ds_read_b128 a[176:179], v242 offset:10240\n\t"
"v_mfma_f32_16x16x32_bf16 a[128:131], a[212:215], a[180:183], a[128:131]\n\t"
"ds_read_b128 a[180:183], v242 offset:10752\n\t"
"v_mfma_f32_16x16x32_bf16 a[128:131], a[216:219], a[184:187], a[128:131]\n\t"
"v_mfma_f32_16x16x32_bf16 a[128:131], a[252:255], a[188:191], a[128:131]\n\t"
"v_mfma_f32_16x16x32_bf16 a[136:139], a[212:215], a[184:187], a[136:139]\n\t"
"ds_read_b128 a[184:187], v242 offset:11264\n\t"
"v_mfma_f32_16x16x32_bf16 a[136:139], a[216:219], a[188:191], a[136:139]\n\t"
"v_cvt_pk_bf16_f32 v226, v226, v227\n\t"
"v_mfma_f32_16x16x32_bf16 a[136:139], a[252:255], a[192:195], a[136:139]\n\t"
"v_cvt_pk_bf16_f32 v227, v228, v229\n\t"
"s_waitcnt lgkmcnt(4)\n\t"
"v_mfma_f32_16x16x32_bf16 v[230:233], v[230:233], v[218:221], 0\n\t"
"v_mfma_f32_16x16x32_bf16 a[144:147], a[212:215], a[188:191], a[144:147]\n\t"
"ds_read_b128 a[188:191], v242 offset:11776\n\t"
"v_mfma_f32_16x16x32_bf16 a[144:147], a[216:219], a[192:195], a[144:147]\n\t"
"v_mfma_f32_16x16x32_bf16 a[144:147], a[252:255], a[196:199], a[144:147]\n\t"
"v_mfma_f32_16x16x32_bf16 a[152:155], a[212:215], a[192:195], a[152:155]\n\t"
"ds_read_b128 a[192:195], v242 offset:12288\n\t"
"v_mfma_f32_16x16x32_bf16 a[152:155], a[216:219], a[196:199], a[152:155]\n\t"
"s_nop 0\n\t"
"v_cvt_pk_bf16_f32 v230, v230, v231\n\t"
"v_mfma_f32_16x16x32_bf16 a[152:155], a[252:255], a[200:203], a[152:155]\n\t"
"v_cvt_pk_bf16_f32 v231, v232, v233\n\t"
"v_mfma_f32_16x16x32_bf16 a[160:163], a[212:215], a[196:199], a[160:163]\n\t"
"ds_read_b128 a[196:199], v242 offset:12800\n\t"
"v_mfma_f32_16x16x32_bf16 a[160:163], a[216:219], a[200:203], a[160:163]\n\t"
"ds_read_b128 a[200:203], v242 offset:13312\n\t"
"v_mfma_f32_16x16x32_bf16 a[160:163], a[252:255], a[204:207], a[160:163]\n\t"
"s_mov_b32 s20, 995328\n\t"
"buffer_load_dwordx4 a[212:215], v252, s[8:11], s20 offen offset:0 sc1\n\t"
"s_waitcnt vmcnt(7) lgkmcnt(6)\n\t"
"v_mfma_f32_16x16x32_bf16 v[198:201], a[220:223], a[168:171], v[198:201]\n\t"
"ds_read_b128 a[204:207], v242 offset:13824\n\t"
"v_mfma_f32_16x16x32_bf16 v[198:201], a[224:227], a[172:175], v[198:201]\n\t"
"v_mfma_f32_16x16x32_bf16 v[198:201], a[228:231], a[176:179], v[198:201]\n\t"
"buffer_load_dwordx4 a[216:219], v252, s[8:11], s20 offen offset:1024 sc1\n\t"
"s_waitcnt lgkmcnt(6)\n\t"
"v_mfma_f32_16x16x32_bf16 v[206:209], a[220:223], a[172:175], v[206:209]\n\t"
"v_mfma_f32_16x16x32_bf16 v[206:209], a[224:227], a[176:179], v[206:209]\n\t"
"v_mfma_f32_16x16x32_bf16 v[206:209], a[228:231], a[180:183], v[206:209]\n\t"
"buffer_load_dwordx4 a[252:255], v252, s[8:11], s20 offen offset:2048 sc1\n\t"
"s_waitcnt lgkmcnt(0)\n\t"
"s_barrier\n\t"
"v_mfma_f32_16x16x32_bf16 v[214:217], a[220:223], a[176:179], v[214:217]\n\t"
"v_mfma_f32_16x16x32_bf16 v[214:217], a[224:227], a[180:183], v[214:217]\n\t"
"v_mfma_f32_16x16x32_bf16 v[214:217], a[228:231], a[184:187], v[214:217]\n\t"
"ds_write_b64 v243, v[234:235] offset:0\n\t"
"ds_write_b64 v243, v[222:223] offset:512\n\t"
"v_mfma_f32_16x16x32_bf16 a[4:7], a[220:223], a[180:183], a[4:7]\n\t"
"ds_read_b64_tr_b16 v[234:235], v244 offset:26624\n\t"
"v_mfma_f32_16x16x32_bf16 a[4:7], a[224:227], a[184:187], a[4:7]\n\t"
"ds_read_b64_tr_b16 v[236:237], v244 offset:29696\n\t"
"v_mfma_f32_16x16x32_bf16 a[4:7], a[228:231], a[188:191], a[4:7]\n\t"
"ds_write_b64 v243, v[226:227] offset:1024\n\t"
"v_mfma_f32_16x16x32_bf16 a[12:15], a[220:223], a[184:187], a[12:15]\n\t"
"ds_read_b64_tr_b16 v[222:223], v244 offset:32768\n\t"
"v_mfma_f32_16x16x32_bf16 a[12:15], a[224:227], a[188:191], a[12:15]\n\t"
"ds_read_b64_tr_b16 v[224:225], v244 offset:35840\n\t"
"v_mfma_f32_16x16x32_bf16 a[12:15], a[228:231], a[192:195], a[12:15]\n\t"
"ds_write_b64 v243, v[230:231] offset:1536\n\t"
"v_mfma_f32_16x16x32_bf16 a[20:23], a[220:223], a[188:191], a[20:23]\n\t"
"ds_read_b64_tr_b16 v[226:227], v244 offset:40448\n\t"
"v_mfma_f32_16x16x32_bf16 a[20:23], a[224:227], a[192:195], a[20:23]\n\t"
"ds_read_b64_tr_b16 v[228:229], v244 offset:43520\n\t"
"v_mfma_f32_16x16x32_bf16 a[20:23], a[228:231], a[196:199], a[20:23]\n\t"
"ds_read_b64_tr_b16 v[230:231], v244 offset:46592\n\t"
"ds_read_b64_tr_b16 v[232:233], v244 offset:49664\n\t"
"s_waitcnt lgkmcnt(8)\n\t"
"v_mfma_f32_16x16x32_bf16 v[234:237], v[234:237], v[218:221], 0\n\t"
"v_mfma_f32_16x16x32_bf16 a[28:31], a[220:223], a[192:195], a[28:31]\n\t"
"v_mfma_f32_16x16x32_bf16 a[28:31], a[224:227], a[196:199], a[28:31]\n\t"
"v_mfma_f32_16x16x32_bf16 a[28:31], a[228:231], a[200:203], a[28:31]\n\t"
"s_waitcnt lgkmcnt(5)\n\t"
"v_mfma_f32_16x16x32_bf16 v[222:225], v[222:225], v[218:221], 0\n\t"
"v_mfma_f32_16x16x32_bf16 a[36:39], a[220:223], a[196:199], a[36:39]\n\t"
"s_nop 1\n\t"
"v_cvt_pk_bf16_f32 v234, v234, v235\n\t"
"v_cvt_pk_bf16_f32 v235, v236, v237\n\t"
"v_mfma_f32_16x16x32_bf16 a[36:39], a[224:227], a[200:203], a[36:39]\n\t"
"v_mfma_f32_16x16x32_bf16 a[36:39], a[228:231], a[204:207], a[36:39]\n\t"
"s_mov_b32 s20, 1007616\n\t"
"buffer_load_dwordx4 a[220:223], v252, s[8:11], s20 offen offset:0 sc1\n\t"
"s_waitcnt lgkmcnt(2)\n\t"
"v_mfma_f32_16x16x32_bf16 v[226:229], v[226:229], v[218:221], 0\n\t"
"buffer_load_dwordx4 a[224:227], v252, s[8:11], s20 offen offset:1024 sc1\n\t"
"s_waitcnt vmcnt(8)\n\t"
"v_mfma_f32_16x16x32_bf16 a[44:47], a[232:235], a[168:171], a[44:47]\n\t"
"v_cvt_pk_bf16_f32 v222, v222, v223\n\t"
"v_cvt_pk_bf16_f32 v223, v224, v225\n\t"
"v_mfma_f32_16x16x32_bf16 a[44:47], a[236:239], a[172:175], a[44:47]\n\t"
"v_mfma_f32_16x16x32_bf16 a[44:47], a[240:243], a[176:179], a[44:47]\n\t"
"ds_write_b64 v243, v[234:235] offset:2048\n\t"
"s_waitcnt lgkmcnt(1)\n\t"
"v_mfma_f32_16x16x32_bf16 v[230:233], v[230:233], v[218:221], 0\n\t"
"ds_read_b64_tr_b16 v[234:235], v244 offset:52736\n\t"
"ds_read_b64_tr_b16 v[236:237], v244 offset:55808\n\t"
"v_mfma_f32_16x16x32_bf16 a[52:55], a[232:235], a[172:175], a[52:55]\n\t"
"v_cvt_pk_bf16_f32 v226, v226, v227\n\t"
"v_cvt_pk_bf16_f32 v227, v228, v229\n\t"
"v_mfma_f32_16x16x32_bf16 a[52:55], a[236:239], a[176:179], a[52:55]\n\t"
"v_mfma_f32_16x16x32_bf16 a[52:55], a[240:243], a[180:183], a[52:55]\n\t"
"buffer_load_dwordx4 a[228:231], v252, s[8:11], s20 offen offset:2048 sc1\n\t"
"v_mfma_f32_16x16x32_bf16 a[60:63], a[232:235], a[176:179], a[60:63]\n\t"
"v_mfma_f32_16x16x32_bf16 a[60:63], a[236:239], a[180:183], a[60:63]\n\t"
"v_cvt_pk_bf16_f32 v230, v230, v231\n\t"
"v_cvt_pk_bf16_f32 v231, v232, v233\n\t"
"v_mfma_f32_16x16x32_bf16 a[60:63], a[240:243], a[184:187], a[60:63]\n\t"
"ds_write_b64 v243, v[222:223] offset:2560\n\t"
"v_mfma_f32_16x16x32_bf16 a[68:71], a[232:235], a[180:183], a[68:71]\n\t"
"v_mfma_f32_16x16x32_bf16 a[68:71], a[236:239], a[184:187], a[68:71]\n\t"
"v_mfma_f32_16x16x32_bf16 a[68:71], a[240:243], a[188:191], a[68:71]\n\t"
"ds_write_b64 v243, v[226:227] offset:3072\n\t"
"s_waitcnt lgkmcnt(2)\n\t"
"v_mfma_f32_16x16x32_bf16 v[234:237], v[234:237], v[218:221], 0\n\t"
"ds_write_b64 v243, v[230:231] offset:3584\n\t"
"v_mfma_f32_16x16x32_bf16 a[76:79], a[232:235], a[184:187], a[76:79]\n\t"
"v_mfma_f32_16x16x32_bf16 a[76:79], a[236:239], a[188:191], a[76:79]\n\t"
"v_mfma_f32_16x16x32_bf16 a[76:79], a[240:243], a[192:195], a[76:79]\n\t"
"v_mfma_f32_16x16x32_bf16 a[84:87], a[232:235], a[188:191], a[84:87]\n\t"
"v_mfma_f32_16x16x32_bf16 a[84:87], a[236:239], a[192:195], a[84:87]\n\t"
"s_nop 1\n\t"
"v_cvt_pk_bf16_f32 v234, v234, v235\n\t"
"v_cvt_pk_bf16_f32 v235, v236, v237\n\t"
"v_mfma_f32_16x16x32_bf16 a[84:87], a[240:243], a[196:199], a[84:87]\n\t"
"v_mfma_f32_16x16x32_bf16 a[92:95], a[232:235], a[192:195], a[92:95]\n\t"
"v_mfma_f32_16x16x32_bf16 a[92:95], a[236:239], a[196:199], a[92:95]\n\t"
"v_mfma_f32_16x16x32_bf16 a[92:95], a[240:243], a[200:203], a[92:95]\n\t"
"ds_write_b64 v243, v[234:235] offset:4096\n\t"
"v_mfma_f32_16x16x32_bf16 a[100:103], a[232:235], a[196:199], a[100:103]\n\t"
"v_mfma_f32_16x16x32_bf16 a[100:103], a[236:239], a[200:203], a[100:103]\n\t"
"v_mfma_f32_16x16x32_bf16 a[100:103], a[240:243], a[204:207], a[100:103]\n\t"
"s_mov_b32 s20, 1019904\n\t"
"buffer_load_dwordx4 a[232:235], v252, s[8:11], s20 offen offset:0 sc1\n\t"
"s_waitcnt vmcnt(7)\n\t"
"v_mfma_f32_16x16x32_bf16 a[108:111], a[244:247], a[168:171], a[108:111]\n\t"
"v_mfma_f32_16x16x32_bf16 a[108:111], a[208:211], a[172:175], a[108:111]\n\t"
"v_mfma_f32_16x16x32_bf16 a[108:111], a[248:251], a[176:179], a[108:111]\n\t"
"buffer_load_dwordx4 a[236:239], v252, s[8:11], s20 offen offset:1024 sc1\n\t"
"s_waitcnt lgkmcnt(0)\n\t"
"s_barrier\n\t"
"v_mfma_f32_16x16x32_bf16 a[116:119], a[244:247], a[172:175], a[116:119]\n\t"
"ds_read_b128 a[168:171], v242 offset:0\n\t"
"v_mfma_f32_16x16x32_bf16 a[116:119], a[208:211], a[176:179], a[116:119]\n\t"
"ds_read_b128 a[172:175], v242 offset:512\n\t"
"v_mfma_f32_16x16x32_bf16 a[116:119], a[248:251], a[180:183], a[116:119]\n\t"
"buffer_load_dwordx4 a[240:243], v252, s[8:11], s20 offen offset:2048 sc1\n\t"
"v_mfma_f32_16x16x32_bf16 a[124:127], a[244:247], a[176:179], a[124:127]\n\t"
"ds_read_b128 a[176:179], v242 offset:1024\n\t"
"v_mfma_f32_16x16x32_bf16 a[124:127], a[208:211], a[180:183], a[124:127]\n\t"
"v_mfma_f32_16x16x32_bf16 a[124:127], a[248:251], a[184:187], a[124:127]\n\t"
"v_mfma_f32_16x16x32_bf16 a[132:135], a[244:247], a[180:183], a[132:135]\n\t"
"ds_read_b128 a[180:183], v242 offset:1536\n\t"
"v_mfma_f32_16x16x32_bf16 a[132:135], a[208:211], a[184:187], a[132:135]\n\t"
"v_mfma_f32_16x16x32_bf16 a[132:135], a[248:251], a[188:191], a[132:135]\n\t"
"v_mfma_f32_16x16x32_bf16 a[140:143], a[244:247], a[184:187], a[140:143]\n\t"
"ds_read_b128 a[184:187], v242 offset:2048\n\t"
"v_mfma_f32_16x16x32_bf16 a[140:143], a[208:211], a[188:191], a[140:143]\n\t"
"v_mfma_f32_16x16x32_bf16 a[140:143], a[248:251], a[192:195], a[140:143]\n\t"
"v_mfma_f32_16x16x32_bf16 a[148:151], a[244:247], a[188:191], a[148:151]\n\t"
"ds_read_b128 a[188:191], v242 offset:2560\n\t"
"v_mfma_f32_16x16x32_bf16 a[148:151], a[208:211], a[192:195], a[148:151]\n\t"
"v_mfma_f32_16x16x32_bf16 a[148:151], a[248:251], a[196:199], a[148:151]\n\t"
"v_mfma_f32_16x16x32_bf16 a[156:159], a[244:247], a[192:195], a[156:159]\n\t"
"ds_read_b128 a[192:195], v242 offset:3072\n\t"
"v_mfma_f32_16x16x32_bf16 a[156:159], a[208:211], a[196:199], a[156:159]\n\t"
"v_mfma_f32_16x16x32_bf16 a[156:159], a[248:251], a[200:203], a[156:159]\n\t"
"v_mfma_f32_16x16x32_bf16 a[164:167], a[244:247], a[196:199], a[164:167]\n\t"
"ds_read_b128 a[196:199], v242 offset:3584\n\t"
"v_mfma_f32_16x16x32_bf16 a[164:167], a[208:211], a[200:203], a[164:167]\n\t"
"ds_read_b128 a[200:203], v242 offset:4096\n\t"
"v_mfma_f32_16x16x32_bf16 a[164:167], a[248:251], a[204:207], a[164:167]\n\t"
"s_mov_b32 s20, 1032192\n\t"
"buffer_load_dwordx4 a[244:247], v252, s[8:11], s20 offen offset:0 sc1\n\t"
"s_waitcnt vmcnt(7) lgkmcnt(6)\n\t"
"v_mfma_f32_16x16x32_bf16 v[194:197], a[212:215], a[168:171], v[194:197]\n\t"
"ds_read_b128 a[204:207], v242 offset:4608\n\t"
"v_mfma_f32_16x16x32_bf16 v[194:197], a[216:219], a[172:175], v[194:197]\n\t"
"v_mfma_f32_16x16x32_bf16 v[194:197], a[252:255], a[176:179], v[194:197]\n\t"
"buffer_load_dwordx4 a[208:211], v252, s[8:11], s20 offen offset:1024 sc1\n\t"
"s_waitcnt lgkmcnt(6)\n\t"
"v_mfma_f32_16x16x32_bf16 v[202:205], a[212:215], a[172:175], v[202:205]\n\t"
"v_mfma_f32_16x16x32_bf16 v[202:205], a[216:219], a[176:179], v[202:205]\n\t"
"v_mfma_f32_16x16x32_bf16 v[202:205], a[252:255], a[180:183], v[202:205]\n\t"
"buffer_load_dwordx4 a[248:251], v252, s[8:11], s20 offen offset:2048 sc1\n\t"
"s_waitcnt lgkmcnt(5)\n\t"
"v_mfma_f32_16x16x32_bf16 v[210:213], a[212:215], a[176:179], v[210:213]\n\t"
"v_mfma_f32_16x16x32_bf16 v[210:213], a[216:219], a[180:183], v[210:213]\n\t"
"v_mfma_f32_16x16x32_bf16 v[210:213], a[252:255], a[184:187], v[210:213]\n\t"
"s_waitcnt lgkmcnt(4)\n\t"
"v_mfma_f32_16x16x32_bf16 a[0:3], a[212:215], a[180:183], a[0:3]\n\t"
"v_mfma_f32_16x16x32_bf16 a[0:3], a[216:219], a[184:187], a[0:3]\n\t"
"v_mfma_f32_16x16x32_bf16 a[0:3], a[252:255], a[188:191], a[0:3]\n\t"
"s_waitcnt lgkmcnt(3)\n\t"
"v_mfma_f32_16x16x32_bf16 a[8:11], a[212:215], a[184:187], a[8:11]\n\t"
"v_xor_b32 v244, 0xc0, v244\n\t"
"v_mfma_f32_16x16x32_bf16 a[8:11], a[216:219], a[188:191], a[8:11]\n\t"
"v_mfma_f32_16x16x32_bf16 a[8:11], a[252:255], a[192:195], a[8:11]\n\t"
"s_waitcnt lgkmcnt(2)\n\t"
"v_mfma_f32_16x16x32_bf16 a[16:19], a[212:215], a[188:191], a[16:19]\n\t"
"v_mfma_f32_16x16x32_bf16 a[16:19], a[216:219], a[192:195], a[16:19]\n\t"
"v_mfma_f32_16x16x32_bf16 a[16:19], a[252:255], a[196:199], a[16:19]\n\t"
"s_waitcnt lgkmcnt(1)\n\t"
"v_mfma_f32_16x16x32_bf16 a[24:27], a[212:215], a[192:195], a[24:27]\n\t"
"v_mfma_f32_16x16x32_bf16 a[24:27], a[216:219], a[196:199], a[24:27]\n\t"
"v_mfma_f32_16x16x32_bf16 a[24:27], a[252:255], a[200:203], a[24:27]\n\t"
"s_waitcnt lgkmcnt(0)\n\t"
"v_mfma_f32_16x16x32_bf16 a[32:35], a[212:215], a[196:199], a[32:35]\n\t"
"v_mfma_f32_16x16x32_bf16 a[32:35], a[216:219], a[200:203], a[32:35]\n\t"
"v_mfma_f32_16x16x32_bf16 a[32:35], a[252:255], a[204:207], a[32:35]\n\t"
"s_mov_b32 s20, 1044480\n\t"
"buffer_load_dwordx4 a[212:215], v252, s[8:11], s20 offen offset:0 sc1\n\t"
"s_waitcnt vmcnt(7)\n\t"
"v_mfma_f32_16x16x32_bf16 a[40:43], a[220:223], a[168:171], a[40:43]\n\t"
"v_mfma_f32_16x16x32_bf16 a[40:43], a[224:227], a[172:175], a[40:43]\n\t"
"v_mfma_f32_16x16x32_bf16 a[40:43], a[228:231], a[176:179], a[40:43]\n\t"
"buffer_load_dwordx4 a[216:219], v252, s[8:11], s20 offen offset:1024 sc1\n\t"
"v_mfma_f32_16x16x32_bf16 a[48:51], a[220:223], a[172:175], a[48:51]\n\t"
"ds_read_b64_tr_b16 v[222:223], v244 offset:512\n\t"
"v_mfma_f32_16x16x32_bf16 a[48:51], a[224:227], a[176:179], a[48:51]\n\t"
"ds_read_b64_tr_b16 v[224:225], v244 offset:3584\n\t"
"v_mfma_f32_16x16x32_bf16 a[48:51], a[228:231], a[180:183], a[48:51]\n\t"
"buffer_load_dwordx4 a[252:255], v252, s[8:11], s20 offen offset:2048 sc1\n\t"
"v_mfma_f32_16x16x32_bf16 a[56:59], a[220:223], a[176:179], a[56:59]\n\t"
"v_mfma_f32_16x16x32_bf16 a[56:59], a[224:227], a[180:183], a[56:59]\n\t"
"v_mfma_f32_16x16x32_bf16 a[56:59], a[228:231], a[184:187], a[56:59]\n\t"
"v_mfma_f32_16x16x32_bf16 a[64:67], a[220:223], a[180:183], a[64:67]\n\t"
"ds_read_b64_tr_b16 v[226:227], v244 offset:6656\n\t"
"v_mfma_f32_16x16x32_bf16 a[64:67], a[224:227], a[184:187], a[64:67]\n\t"
"ds_read_b64_tr_b16 v[228:229], v244 offset:9728\n\t"
"v_mfma_f32_16x16x32_bf16 a[64:67], a[228:231], a[188:191], a[64:67]\n\t"
"v_mfma_f32_16x16x32_bf16 a[72:75], a[220:223], a[184:187], a[72:75]\n\t"
"v_mfma_f32_16x16x32_bf16 a[72:75], a[224:227], a[188:191], a[72:75]\n\t"
"v_mfma_f32_16x16x32_bf16 a[72:75], a[228:231], a[192:195], a[72:75]\n\t"
"v_mfma_f32_16x16x32_bf16 a[80:83], a[220:223], a[188:191], a[80:83]\n\t"
"v_mfma_f32_16x16x32_bf16 a[80:83], a[224:227], a[192:195], a[80:83]\n\t"
"v_mfma_f32_16x16x32_bf16 a[80:83], a[228:231], a[196:199], a[80:83]\n\t"
"s_waitcnt lgkmcnt(2)\n\t"
"v_mfma_f32_16x16x32_bf16 v[222:225], v[222:225], v[218:221], 0\n\t"
"v_mfma_f32_16x16x32_bf16 a[88:91], a[220:223], a[192:195], a[88:91]\n\t"
"ds_read_b64_tr_b16 v[230:231], v244 offset:12800\n\t"
"v_mfma_f32_16x16x32_bf16 a[88:91], a[224:227], a[196:199], a[88:91]\n\t"
"ds_read_b64_tr_b16 v[232:233], v244 offset:15872\n\t"
"v_mfma_f32_16x16x32_bf16 a[88:91], a[228:231], a[200:203], a[88:91]\n\t"
"v_mfma_f32_16x16x32_bf16 a[96:99], a[220:223], a[196:199], a[96:99]\n\t"
"v_mfma_f32_16x16x32_bf16 a[96:99], a[224:227], a[200:203], a[96:99]\n\t"
"s_nop 0\n\t"
"v_cvt_pk_bf16_f32 v222, v222, v223\n\t"
"v_cvt_pk_bf16_f32 v223, v224, v225\n\t"
"v_mfma_f32_16x16x32_bf16 a[96:99], a[228:231], a[204:207], a[96:99]\n\t"
"s_waitcnt lgkmcnt(2)\n\t"
"v_mfma_f32_16x16x32_bf16 v[226:229], v[226:229], v[218:221], 0\n\t"
"s_mov_b32 s20, 1056768\n\t"
"buffer_load_dwordx4 a[220:223], v252, s[8:11], s20 offen offset:0 sc1\n\t"
"s_waitcnt vmcnt(7)\n\t"
"v_mfma_f32_16x16x32_bf16 a[104:107], a[232:235], a[168:171], a[104:107]\n\t"
"ds_read_b64_tr_b16 v[234:235], v244 offset:20480\n\t"
"v_mfma_f32_16x16x32_bf16 a[104:107], a[236:239], a[172:175], a[104:107]\n\t"
"ds_read_b64_tr_b16 v[236:237], v244 offset:23552\n\t"
"v_mfma_f32_16x16x32_bf16 a[104:107], a[240:243], a[176:179], a[104:107]\n\t"
"buffer_load_dwordx4 a[224:227], v252, s[8:11], s20 offen offset:1024 sc1\n\t"
"v_mfma_f32_16x16x32_bf16 a[112:115], a[232:235], a[172:175], a[112:115]\n\t"
"ds_read_b128 a[168:171], v242 offset:9216\n\t"
"v_mfma_f32_16x16x32_bf16 a[112:115], a[236:239], a[176:179], a[112:115]\n\t"
"v_cvt_pk_bf16_f32 v226, v226, v227\n\t"
"v_cvt_pk_bf16_f32 v227, v228, v229\n\t"
"v_mfma_f32_16x16x32_bf16 a[112:115], a[240:243], a[180:183], a[112:115]\n\t"
"buffer_load_dwordx4 a[228:231], v252, s[8:11], s20 offen offset:2048 sc1\n\t"
"v_mfma_f32_16x16x32_bf16 a[120:123], a[232:235], a[176:179], a[120:123]\n\t"
"ds_read_b128 a[172:175], v242 offset:9728\n\t"
"v_mfma_f32_16x16x32_bf16 a[120:123], a[236:239], a[180:183], a[120:123]\n\t"
"ds_read_b128 a[176:179], v242 offset:10240\n\t"
"v_mfma_f32_16x16x32_bf16 a[120:123], a[240:243], a[184:187], a[120:123]\n\t"
"s_waitcnt lgkmcnt(5)\n\t"
"v_mfma_f32_16x16x32_bf16 v[230:233], v[230:233], v[218:221], 0\n\t"
"v_mfma_f32_16x16x32_bf16 a[128:131], a[232:235], a[180:183], a[128:131]\n\t"
"ds_read_b128 a[180:183], v242 offset:10752\n\t"
"v_mfma_f32_16x16x32_bf16 a[128:131], a[236:239], a[184:187], a[128:131]\n\t"
"v_mfma_f32_16x16x32_bf16 a[128:131], a[240:243], a[188:191], a[128:131]\n\t"
"v_mfma_f32_16x16x32_bf16 a[136:139], a[232:235], a[184:187], a[136:139]\n\t"
"ds_read_b128 a[184:187], v242 offset:11264\n\t"
"v_mfma_f32_16x16x32_bf16 a[136:139], a[236:239], a[188:191], a[136:139]\n\t"
"s_nop 0\n\t"
"v_cvt_pk_bf16_f32 v230, v230, v231\n\t"
"v_mfma_f32_16x16x32_bf16 a[136:139], a[240:243], a[192:195], a[136:139]\n\t"
"v_cvt_pk_bf16_f32 v231, v232, v233\n\t"
"s_waitcnt lgkmcnt(5)\n\t"
"v_mfma_f32_16x16x32_bf16 v[234:237], v[234:237], v[218:221], 0\n\t"
"v_mfma_f32_16x16x32_bf16 a[144:147], a[232:235], a[188:191], a[144:147]\n\t"
"ds_read_b128 a[188:191], v242 offset:11776\n\t"
"v_mfma_f32_16x16x32_bf16 a[144:147], a[236:239], a[192:195], a[144:147]\n\t"
"v_mfma_f32_16x16x32_bf16 a[144:147], a[240:243], a[196:199], a[144:147]\n\t"
"v_mfma_f32_16x16x32_bf16 a[152:155], a[232:235], a[192:195], a[152:155]\n\t"
"ds_read_b128 a[192:195], v242 offset:12288\n\t"
"v_mfma_f32_16x16x32_bf16 a[152:155], a[236:239], a[196:199], a[152:155]\n\t"
"s_nop 0\n\t"
"v_cvt_pk_bf16_f32 v234, v234, v235\n\t"
"v_mfma_f32_16x16x32_bf16 a[152:155], a[240:243], a[200:203], a[152:155]\n\t"
"v_cvt_pk_bf16_f32 v235, v236, v237\n\t"
"v_mfma_f32_16x16x32_bf16 a[160:163], a[232:235], a[196:199], a[160:163]\n\t"
"ds_read_b128 a[196:199], v242 offset:12800\n\t"
"v_mfma_f32_16x16x32_bf16 a[160:163], a[236:239], a[200:203], a[160:163]\n\t"
"ds_read_b128 a[200:203], v242 offset:13312\n\t"
"v_mfma_f32_16x16x32_bf16 a[160:163], a[240:243], a[204:207], a[160:163]\n\t"
"s_mov_b32 s20, 921600\n\t"
"buffer_load_dwordx4 a[232:235], v252, s[8:11], s20 offen offset:0 sc1\n\t"
"s_waitcnt vmcnt(7) lgkmcnt(6)\n\t"
"v_mfma_f32_16x16x32_bf16 v[198:201], a[244:247], a[168:171], v[198:201]\n\t"
"ds_read_b128 a[204:207], v242 offset:13824\n\t"
"v_mfma_f32_16x16x32_bf16 v[198:201], a[208:211], a[172:175], v[198:201]\n\t"
"v_mfma_f32_16x16x32_bf16 v[198:201], a[248:251], a[176:179], v[198:201]\n\t"
"buffer_load_dwordx4 a[236:239], v252, s[8:11], s20 offen offset:1024 sc1\n\t"
"s_waitcnt lgkmcnt(6)\n\t"
"v_mfma_f32_16x16x32_bf16 v[206:209], a[244:247], a[172:175], v[206:209]\n\t"
"v_mfma_f32_16x16x32_bf16 v[206:209], a[208:211], a[176:179], v[206:209]\n\t"
"v_mfma_f32_16x16x32_bf16 v[206:209], a[248:251], a[180:183], v[206:209]\n\t"
"buffer_load_dwordx4 a[240:243], v252, s[8:11], s20 offen offset:2048 sc1\n\t"
"s_waitcnt lgkmcnt(0)\n\t"
"s_barrier\n\t"
"v_mfma_f32_16x16x32_bf16 v[214:217], a[244:247], a[176:179], v[214:217]\n\t"
"v_mfma_f32_16x16x32_bf16 v[214:217], a[208:211], a[180:183], v[214:217]\n\t"
"v_mfma_f32_16x16x32_bf16 v[214:217], a[248:251], a[184:187], v[214:217]\n\t"
"ds_write_b64 v243, v[222:223] offset:0\n\t"
"ds_write_b64 v243, v[226:227] offset:512\n\t"
"v_mfma_f32_16x16x32_bf16 a[4:7], a[244:247], a[180:183], a[4:7]\n\t"
"ds_read_b64_tr_b16 v[222:223], v244 offset:26624\n\t"
"v_mfma_f32_16x16x32_bf16 a[4:7], a[208:211], a[184:187], a[4:7]\n\t"
"ds_read_b64_tr_b16 v[224:225], v244 offset:29696\n\t"
"v_mfma_f32_16x16x32_bf16 a[4:7], a[248:251], a[188:191], a[4:7]\n\t"
"ds_write_b64 v243, v[230:231] offset:1024\n\t"
"v_mfma_f32_16x16x32_bf16 a[12:15], a[244:247], a[184:187], a[12:15]\n\t"
"ds_read_b64_tr_b16 v[226:227], v244 offset:32768\n\t"
"v_mfma_f32_16x16x32_bf16 a[12:15], a[208:211], a[188:191], a[12:15]\n\t"
"ds_read_b64_tr_b16 v[228:229], v244 offset:35840\n\t"
"v_mfma_f32_16x16x32_bf16 a[12:15], a[248:251], a[192:195], a[12:15]\n\t"
"ds_write_b64 v243, v[234:235] offset:1536\n\t"
"v_mfma_f32_16x16x32_bf16 a[20:23], a[244:247], a[188:191], a[20:23]\n\t"
"ds_read_b64_tr_b16 v[230:231], v244 offset:40448\n\t"
"v_mfma_f32_16x16x32_bf16 a[20:23], a[208:211], a[192:195], a[20:23]\n\t"
"ds_read_b64_tr_b16 v[232:233], v244 offset:43520\n\t"
"v_mfma_f32_16x16x32_bf16 a[20:23], a[248:251], a[196:199], a[20:23]\n\t"
"ds_read_b64_tr_b16 v[234:235], v244 offset:46592\n\t"
"ds_read_b64_tr_b16 v[236:237], v244 offset:49664\n\t"
"s_waitcnt lgkmcnt(8)\n\t"
"v_mfma_f32_16x16x32_bf16 v[222:225], v[222:225], v[218:221], 0\n\t"
"v_mfma_f32_16x16x32_bf16 a[28:31], a[244:247], a[192:195], a[28:31]\n\t"
"v_mfma_f32_16x16x32_bf16 a[28:31], a[208:211], a[196:199], a[28:31]\n\t"
"v_mfma_f32_16x16x32_bf16 a[28:31], a[248:251], a[200:203], a[28:31]\n\t"
"s_waitcnt lgkmcnt(5)\n\t"
"v_mfma_f32_16x16x32_bf16 v[226:229], v[226:229], v[218:221], 0\n\t"
"v_mfma_f32_16x16x32_bf16 a[36:39], a[244:247], a[196:199], a[36:39]\n\t"
"s_nop 1\n\t"
"v_cvt_pk_bf16_f32 v222, v222, v223\n\t"
"v_cvt_pk_bf16_f32 v223, v224, v225\n\t"
"v_mfma_f32_16x16x32_bf16 a[36:39], a[208:211], a[200:203], a[36:39]\n\t"
"v_mfma_f32_16x16x32_bf16 a[36:39], a[248:251], a[204:207], a[36:39]\n\t"
"s_mov_b32 s20, 933888\n\t"
"buffer_load_dwordx4 a[244:247], v252, s[8:11], s20 offen offset:0 sc1\n\t"
"s_waitcnt lgkmcnt(2)\n\t"
"v_mfma_f32_16x16x32_bf16 v[230:233], v[230:233], v[218:221], 0\n\t"
"buffer_load_dwordx4 a[208:211], v252, s[8:11], s20 offen offset:1024 sc1\n\t"
"s_waitcnt vmcnt(8)\n\t"
"v_mfma_f32_16x16x32_bf16 a[44:47], a[212:215], a[168:171], a[44:47]\n\t"
"v_cvt_pk_bf16_f32 v226, v226, v227\n\t"
"v_cvt_pk_bf16_f32 v227, v228, v229\n\t"
"v_mfma_f32_16x16x32_bf16 a[44:47], a[216:219], a[172:175], a[44:47]\n\t"
"v_mfma_f32_16x16x32_bf16 a[44:47], a[252:255], a[176:179], a[44:47]\n\t"
"ds_write_b64 v243, v[222:223] offset:2048\n\t"
"s_waitcnt lgkmcnt(1)\n\t"
"v_mfma_f32_16x16x32_bf16 v[234:237], v[234:237], v[218:221], 0\n\t"
"ds_read_b64_tr_b16 v[222:223], v244 offset:52736\n\t"
"ds_read_b64_tr_b16 v[224:225], v244 offset:55808\n\t"
"v_mfma_f32_16x16x32_bf16 a[52:55], a[212:215], a[172:175], a[52:55]\n\t"
"v_cvt_pk_bf16_f32 v230, v230, v231\n\t"
"v_cvt_pk_bf16_f32 v231, v232, v233\n\t"
"v_mfma_f32_16x16x32_bf16 a[52:55], a[216:219], a[176:179], a[52:55]\n\t"
"v_mfma_f32_16x16x32_bf16 a[52:55], a[252:255], a[180:183], a[52:55]\n\t"
"buffer_load_dwordx4 a[248:251], v252, s[8:11], s20 offen offset:2048 sc1\n\t"
"v_mfma_f32_16x16x32_bf16 a[60:63], a[212:215], a[176:179], a[60:63]\n\t"
"v_mfma_f32_16x16x32_bf16 a[60:63], a[216:219], a[180:183], a[60:63]\n\t"
"v_cvt_pk_bf16_f32 v234, v234, v235\n\t"
"v_cvt_pk_bf16_f32 v235, v236, v237\n\t"
"v_mfma_f32_16x16x32_bf16 a[60:63], a[252:255], a[184:187], a[60:63]\n\t"
"ds_write_b64 v243, v[226:227] offset:2560\n\t"
"v_mfma_f32_16x16x32_bf16 a[68:71], a[212:215], a[180:183], a[68:71]\n\t"
"v_mfma_f32_16x16x32_bf16 a[68:71], a[216:219], a[184:187], a[68:71]\n\t"
"v_mfma_f32_16x16x32_bf16 a[68:71], a[252:255], a[188:191], a[68:71]\n\t"
"ds_write_b64 v243, v[230:231] offset:3072\n\t"
"s_waitcnt lgkmcnt(2)\n\t"
"v_mfma_f32_16x16x32_bf16 v[222:225], v[222:225], v[218:221], 0\n\t"
"ds_write_b64 v243, v[234:235] offset:3584\n\t"
"v_mfma_f32_16x16x32_bf16 a[76:79], a[212:215], a[184:187], a[76:79]\n\t"
"v_mfma_f32_16x16x32_bf16 a[76:79], a[216:219], a[188:191], a[76:79]\n\t"
"v_mfma_f32_16x16x32_bf16 a[76:79], a[252:255], a[192:195], a[76:79]\n\t"
"v_mfma_f32_16x16x32_bf16 a[84:87], a[212:215], a[188:191], a[84:87]\n\t"
"v_mfma_f32_16x16x32_bf16 a[84:87], a[216:219], a[192:195], a[84:87]\n\t"
"s_nop 1\n\t"
"v_cvt_pk_bf16_f32 v222, v222, v223\n\t"
"v_cvt_pk_bf16_f32 v223, v224, v225\n\t"
"v_mfma_f32_16x16x32_bf16 a[84:87], a[252:255], a[196:199], a[84:87]\n\t"
"v_mfma_f32_16x16x32_bf16 a[92:95], a[212:215], a[192:195], a[92:95]\n\t"
"v_mfma_f32_16x16x32_bf16 a[92:95], a[216:219], a[196:199], a[92:95]\n\t"
"v_mfma_f32_16x16x32_bf16 a[92:95], a[252:255], a[200:203], a[92:95]\n\t"
"ds_write_b64 v243, v[222:223] offset:4096\n\t"
"v_mfma_f32_16x16x32_bf16 a[100:103], a[212:215], a[196:199], a[100:103]\n\t"
"v_mfma_f32_16x16x32_bf16 a[100:103], a[216:219], a[200:203], a[100:103]\n\t"
"v_mfma_f32_16x16x32_bf16 a[100:103], a[252:255], a[204:207], a[100:103]\n\t"
"s_mov_b32 s20, 946176\n\t"
"buffer_load_dwordx4 a[212:215], v252, s[8:11], s20 offen offset:0 sc1\n\t"
"s_waitcnt vmcnt(7)\n\t"
"v_mfma_f32_16x16x32_bf16 a[108:111], a[220:223], a[168:171], a[108:111]\n\t"
"v_mfma_f32_16x16x32_bf16 a[108:111], a[224:227], a[172:175], a[108:111]\n\t"
"v_mfma_f32_16x16x32_bf16 a[108:111], a[228:231], a[176:179], a[108:111]\n\t"
"buffer_load_dwordx4 a[216:219], v252, s[8:11], s20 offen offset:1024 sc1\n\t"
"s_waitcnt lgkmcnt(0)\n\t"
"s_barrier\n\t"
"v_mfma_f32_16x16x32_bf16 a[116:119], a[220:223], a[172:175], a[116:119]\n\t"
"ds_read_b128 a[168:171], v242 offset:0\n\t"
"v_mfma_f32_16x16x32_bf16 a[116:119], a[224:227], a[176:179], a[116:119]\n\t"
"ds_read_b128 a[172:175], v242 offset:512\n\t"
"v_mfma_f32_16x16x32_bf16 a[116:119], a[228:231], a[180:183], a[116:119]\n\t"
"buffer_load_dwordx4 a[252:255], v252, s[8:11], s20 offen offset:2048 sc1\n\t"
"v_mfma_f32_16x16x32_bf16 a[124:127], a[220:223], a[176:179], a[124:127]\n\t"
"ds_read_b128 a[176:179], v242 offset:1024\n\t"
"v_mfma_f32_16x16x32_bf16 a[124:127], a[224:227], a[180:183], a[124:127]\n\t"
"v_mfma_f32_16x16x32_bf16 a[124:127], a[228:231], a[184:187], a[124:127]\n\t"
"v_mfma_f32_16x16x32_bf16 a[132:135], a[220:223], a[180:183], a[132:135]\n\t"
"ds_read_b128 a[180:183], v242 offset:1536\n\t"
"v_mfma_f32_16x16x32_bf16 a[132:135], a[224:227], a[184:187], a[132:135]\n\t"
"v_mfma_f32_16x16x32_bf16 a[132:135], a[228:231], a[188:191], a[132:135]\n\t"
"v_mfma_f32_16x16x32_bf16 a[140:143], a[220:223], a[184:187], a[140:143]\n\t"
"ds_read_b128 a[184:187], v242 offset:2048\n\t"
"v_mfma_f32_16x16x32_bf16 a[140:143], a[224:227], a[188:191], a[140:143]\n\t"
"v_mfma_f32_16x16x32_bf16 a[140:143], a[228:231], a[192:195], a[140:143]\n\t"
"v_mfma_f32_16x16x32_bf16 a[148:151], a[220:223], a[188:191], a[148:151]\n\t"
"ds_read_b128 a[188:191], v242 offset:2560\n\t"
"v_mfma_f32_16x16x32_bf16 a[148:151], a[224:227], a[192:195], a[148:151]\n\t"
"v_mfma_f32_16x16x32_bf16 a[148:151], a[228:231], a[196:199], a[148:151]\n\t"
"v_mfma_f32_16x16x32_bf16 a[156:159], a[220:223], a[192:195], a[156:159]\n\t"
"ds_read_b128 a[192:195], v242 offset:3072\n\t"
"v_mfma_f32_16x16x32_bf16 a[156:159], a[224:227], a[196:199], a[156:159]\n\t"
"v_mfma_f32_16x16x32_bf16 a[156:159], a[228:231], a[200:203], a[156:159]\n\t"
"v_mfma_f32_16x16x32_bf16 a[164:167], a[220:223], a[196:199], a[164:167]\n\t"
"ds_read_b128 a[196:199], v242 offset:3584\n\t"
"v_mfma_f32_16x16x32_bf16 a[164:167], a[224:227], a[200:203], a[164:167]\n\t"
"ds_read_b128 a[200:203], v242 offset:4096\n\t"
"v_mfma_f32_16x16x32_bf16 a[164:167], a[228:231], a[204:207], a[164:167]\n\t"
"s_mov_b32 s20, 958464\n\t"
"buffer_load_dwordx4 a[220:223], v252, s[8:11], s20 offen offset:0 sc1\n\t"
"s_waitcnt vmcnt(7) lgkmcnt(6)\n\t"
"v_mfma_f32_16x16x32_bf16 v[0:3], a[232:235], a[168:171], v[0:3]\n\t"
"ds_read_b128 a[204:207], v242 offset:4608\n\t"
"v_mfma_f32_16x16x32_bf16 v[0:3], a[236:239], a[172:175], v[0:3]\n\t"
"v_mfma_f32_16x16x32_bf16 v[0:3], a[240:243], a[176:179], v[0:3]\n\t"
"buffer_load_dwordx4 a[224:227], v252, s[8:11], s20 offen offset:1024 sc1\n\t"
"s_waitcnt lgkmcnt(6)\n\t"
"v_mfma_f32_16x16x32_bf16 v[4:7], a[232:235], a[172:175], v[4:7]\n\t"
"v_mfma_f32_16x16x32_bf16 v[4:7], a[236:239], a[176:179], v[4:7]\n\t"
"v_mfma_f32_16x16x32_bf16 v[4:7], a[240:243], a[180:183], v[4:7]\n\t"
"buffer_load_dwordx4 a[228:231], v252, s[8:11], s20 offen offset:2048 sc1\n\t"
"s_waitcnt lgkmcnt(5)\n\t"
"v_mfma_f32_16x16x32_bf16 v[8:11], a[232:235], a[176:179], v[8:11]\n\t"
"v_mfma_f32_16x16x32_bf16 v[8:11], a[236:239], a[180:183], v[8:11]\n\t"
"v_mfma_f32_16x16x32_bf16 v[8:11], a[240:243], a[184:187], v[8:11]\n\t"
"s_waitcnt lgkmcnt(4)\n\t"
"v_mfma_f32_16x16x32_bf16 v[12:15], a[232:235], a[180:183], v[12:15]\n\t"
"v_mfma_f32_16x16x32_bf16 v[12:15], a[236:239], a[184:187], v[12:15]\n\t"
"v_mfma_f32_16x16x32_bf16 v[12:15], a[240:243], a[188:191], v[12:15]\n\t"
"s_waitcnt lgkmcnt(3)\n\t"
"v_mfma_f32_16x16x32_bf16 v[16:19], a[232:235], a[184:187], v[16:19]\n\t"
"v_mfma_f32_16x16x32_bf16 v[16:19], a[236:239], a[188:191], v[16:19]\n\t"
"v_mfma_f32_16x16x32_bf16 v[16:19], a[240:243], a[192:195], v[16:19]\n\t"
"s_waitcnt lgkmcnt(2)\n\t"
"v_mfma_f32_16x16x32_bf16 v[20:23], a[232:235], a[188:191], v[20:23]\n\t"
"v_mfma_f32_16x16x32_bf16 v[20:23], a[236:239], a[192:195], v[20:23]\n\t"
"v_mfma_f32_16x16x32_bf16 v[20:23], a[240:243], a[196:199], v[20:23]\n\t"
"s_waitcnt lgkmcnt(1)\n\t"
"v_mfma_f32_16x16x32_bf16 v[24:27], a[232:235], a[192:195], v[24:27]\n\t"
"v_mfma_f32_16x16x32_bf16 v[24:27], a[236:239], a[196:199], v[24:27]\n\t"
"v_mfma_f32_16x16x32_bf16 v[24:27], a[240:243], a[200:203], v[24:27]\n\t"
"s_waitcnt lgkmcnt(0)\n\t"
"v_mfma_f32_16x16x32_bf16 v[28:31], a[232:235], a[196:199], v[28:31]\n\t"
"v_mfma_f32_16x16x32_bf16 v[28:31], a[236:239], a[200:203], v[28:31]\n\t"
"v_mfma_f32_16x16x32_bf16 v[28:31], a[240:243], a[204:207], v[28:31]\n\t"
"s_mov_b32 s20, 970752\n\t"
"buffer_load_dwordx4 a[232:235], v252, s[8:11], s20 offen offset:0 sc1\n\t"
"s_waitcnt vmcnt(7)\n\t"
"v_mfma_f32_16x16x32_bf16 v[32:35], a[244:247], a[168:171], v[32:35]\n\t"
"v_mfma_f32_16x16x32_bf16 v[32:35], a[208:211], a[172:175], v[32:35]\n\t"
"v_mfma_f32_16x16x32_bf16 v[32:35], a[248:251], a[176:179], v[32:35]\n\t"
"buffer_load_dwordx4 a[236:239], v252, s[8:11], s20 offen offset:1024 sc1\n\t"
"v_mfma_f32_16x16x32_bf16 v[36:39], a[244:247], a[172:175], v[36:39]\n\t"
"ds_read_b64_tr_b16 v[226:227], v244 offset:1024\n\t"
"v_mfma_f32_16x16x32_bf16 v[36:39], a[208:211], a[176:179], v[36:39]\n\t"
"ds_read_b64_tr_b16 v[228:229], v244 offset:4096\n\t"
"v_mfma_f32_16x16x32_bf16 v[36:39], a[248:251], a[180:183], v[36:39]\n\t"
"buffer_load_dwordx4 a[240:243], v252, s[8:11], s20 offen offset:2048 sc1\n\t"
"v_mfma_f32_16x16x32_bf16 v[40:43], a[244:247], a[176:179], v[40:43]\n\t"
"v_mfma_f32_16x16x32_bf16 v[40:43], a[208:211], a[180:183], v[40:43]\n\t"
"v_mfma_f32_16x16x32_bf16 v[40:43], a[248:251], a[184:187], v[40:43]\n\t"
"v_mfma_f32_16x16x32_bf16 v[44:47], a[244:247], a[180:183], v[44:47]\n\t"
"ds_read_b64_tr_b16 v[230:231], v244 offset:7168\n\t"
"v_mfma_f32_16x16x32_bf16 v[44:47], a[208:211], a[184:187], v[44:47]\n\t"
"ds_read_b64_tr_b16 v[232:233], v244 offset:10240\n\t"
"v_mfma_f32_16x16x32_bf16 v[44:47], a[248:251], a[188:191], v[44:47]\n\t"
"v_mfma_f32_16x16x32_bf16 v[48:51], a[244:247], a[184:187], v[48:51]\n\t"
"v_mfma_f32_16x16x32_bf16 v[48:51], a[208:211], a[188:191], v[48:51]\n\t"
"v_mfma_f32_16x16x32_bf16 v[48:51], a[248:251], a[192:195], v[48:51]\n\t"
"v_mfma_f32_16x16x32_bf16 v[52:55], a[244:247], a[188:191], v[52:55]\n\t"
"v_mfma_f32_16x16x32_bf16 v[52:55], a[208:211], a[192:195], v[52:55]\n\t"
"v_mfma_f32_16x16x32_bf16 v[52:55], a[248:251], a[196:199], v[52:55]\n\t"
"s_waitcnt lgkmcnt(2)\n\t"
"v_mfma_f32_16x16x32_bf16 v[226:229], v[226:229], v[218:221], 0\n\t"
"v_mfma_f32_16x16x32_bf16 v[56:59], a[244:247], a[192:195], v[56:59]\n\t"
"ds_read_b64_tr_b16 v[234:235], v244 offset:13312\n\t"
"v_mfma_f32_16x16x32_bf16 v[56:59], a[208:211], a[196:199], v[56:59]\n\t"
"ds_read_b64_tr_b16 v[236:237], v244 offset:16384\n\t"
"v_mfma_f32_16x16x32_bf16 v[56:59], a[248:251], a[200:203], v[56:59]\n\t"
"v_mfma_f32_16x16x32_bf16 v[60:63], a[244:247], a[196:199], v[60:63]\n\t"
"v_mfma_f32_16x16x32_bf16 v[60:63], a[208:211], a[200:203], v[60:63]\n\t"
"s_nop 0\n\t"
"v_cvt_pk_bf16_f32 v226, v226, v227\n\t"
"v_cvt_pk_bf16_f32 v227, v228, v229\n\t"
"v_mfma_f32_16x16x32_bf16 v[60:63], a[248:251], a[204:207], v[60:63]\n\t"
"s_waitcnt lgkmcnt(2)\n\t"
"v_mfma_f32_16x16x32_bf16 v[230:233], v[230:233], v[218:221], 0\n\t"
"s_mov_b32 s20, 983040\n\t"
"buffer_load_dwordx4 a[244:247], v252, s[8:11], s20 offen offset:0 sc1\n\t"
"s_waitcnt vmcnt(7)\n\t"
"v_mfma_f32_16x16x32_bf16 v[64:67], a[212:215], a[168:171], v[64:67]\n\t"
"ds_read_b64_tr_b16 v[222:223], v244 offset:20992\n\t"
"v_mfma_f32_16x16x32_bf16 v[64:67], a[216:219], a[172:175], v[64:67]\n\t"
"ds_read_b64_tr_b16 v[224:225], v244 offset:24064\n\t"
"v_mfma_f32_16x16x32_bf16 v[64:67], a[252:255], a[176:179], v[64:67]\n\t"
"buffer_load_dwordx4 a[208:211], v252, s[8:11], s20 offen offset:1024 sc1\n\t"
"v_mfma_f32_16x16x32_bf16 v[68:71], a[212:215], a[172:175], v[68:71]\n\t"
"ds_read_b128 a[168:171], v242 offset:9216\n\t"
"v_mfma_f32_16x16x32_bf16 v[68:71], a[216:219], a[176:179], v[68:71]\n\t"
"v_cvt_pk_bf16_f32 v230, v230, v231\n\t"
"v_cvt_pk_bf16_f32 v231, v232, v233\n\t"
"v_mfma_f32_16x16x32_bf16 v[68:71], a[252:255], a[180:183], v[68:71]\n\t"
"buffer_load_dwordx4 a[248:251], v252, s[8:11], s20 offen offset:2048 sc1\n\t"
"v_mfma_f32_16x16x32_bf16 v[72:75], a[212:215], a[176:179], v[72:75]\n\t"
"ds_read_b128 a[172:175], v242 offset:9728\n\t"
"v_mfma_f32_16x16x32_bf16 v[72:75], a[216:219], a[180:183], v[72:75]\n\t"
"ds_read_b128 a[176:179], v242 offset:10240\n\t"
"v_mfma_f32_16x16x32_bf16 v[72:75], a[252:255], a[184:187], v[72:75]\n\t"
"s_waitcnt lgkmcnt(5)\n\t"
"v_mfma_f32_16x16x32_bf16 v[234:237], v[234:237], v[218:221], 0\n\t"
"v_mfma_f32_16x16x32_bf16 v[76:79], a[212:215], a[180:183], v[76:79]\n\t"
"ds_read_b128 a[180:183], v242 offset:10752\n\t"
"v_mfma_f32_16x16x32_bf16 v[76:79], a[216:219], a[184:187], v[76:79]\n\t"
"v_mfma_f32_16x16x32_bf16 v[76:79], a[252:255], a[188:191], v[76:79]\n\t"
"v_mfma_f32_16x16x32_bf16 v[80:83], a[212:215], a[184:187], v[80:83]\n\t"
"ds_read_b128 a[184:187], v242 offset:11264\n\t"
"v_mfma_f32_16x16x32_bf16 v[80:83], a[216:219], a[188:191], v[80:83]\n\t"
"s_nop 0\n\t"
"v_cvt_pk_bf16_f32 v234, v234, v235\n\t"
"v_mfma_f32_16x16x32_bf16 v[80:83], a[252:255], a[192:195], v[80:83]\n\t"
"v_cvt_pk_bf16_f32 v235, v236, v237\n\t"
"s_waitcnt lgkmcnt(5)\n\t"
"v_mfma_f32_16x16x32_bf16 v[222:225], v[222:225], v[218:221], 0\n\t"
"v_mfma_f32_16x16x32_bf16 v[84:87], a[212:215], a[188:191], v[84:87]\n\t"
"ds_read_b128 a[188:191], v242 offset:11776\n\t"
"v_mfma_f32_16x16x32_bf16 v[84:87], a[216:219], a[192:195], v[84:87]\n\t"
"v_mfma_f32_16x16x32_bf16 v[84:87], a[252:255], a[196:199], v[84:87]\n\t"
"v_mfma_f32_16x16x32_bf16 v[88:91], a[212:215], a[192:195], v[88:91]\n\t"
"ds_read_b128 a[192:195], v242 offset:12288\n\t"
"v_mfma_f32_16x16x32_bf16 v[88:91], a[216:219], a[196:199], v[88:91]\n\t"
"s_nop 0\n\t"
"v_cvt_pk_bf16_f32 v222, v222, v223\n\t"
"v_mfma_f32_16x16x32_bf16 v[88:91], a[252:255], a[200:203], v[88:91]\n\t"
"v_cvt_pk_bf16_f32 v223, v224, v225\n\t"
"v_mfma_f32_16x16x32_bf16 v[92:95], a[212:215], a[196:199], v[92:95]\n\t"
"ds_read_b128 a[196:199], v242 offset:12800\n\t"
"v_mfma_f32_16x16x32_bf16 v[92:95], a[216:219], a[200:203], v[92:95]\n\t"
"ds_read_b128 a[200:203], v242 offset:13312\n\t"
"v_mfma_f32_16x16x32_bf16 v[92:95], a[252:255], a[204:207], v[92:95]\n\t"
"s_mov_b32 s20, 1142784\n\t"
"buffer_load_dwordx4 a[212:215], v252, s[8:11], s20 offen offset:0 sc1\n\t"
"s_waitcnt vmcnt(7) lgkmcnt(6)\n\t"
"v_mfma_f32_16x16x32_bf16 v[98:101], a[220:223], a[168:171], v[98:101]\n\t"
"ds_read_b128 a[204:207], v242 offset:13824\n\t"
"v_mfma_f32_16x16x32_bf16 v[98:101], a[224:227], a[172:175], v[98:101]\n\t"
"v_mfma_f32_16x16x32_bf16 v[98:101], a[228:231], a[176:179], v[98:101]\n\t"
"buffer_load_dwordx4 a[216:219], v252, s[8:11], s20 offen offset:1024 sc1\n\t"
"s_waitcnt lgkmcnt(6)\n\t"
"v_mfma_f32_16x16x32_bf16 v[102:105], a[220:223], a[172:175], v[102:105]\n\t"
"v_mfma_f32_16x16x32_bf16 v[102:105], a[224:227], a[176:179], v[102:105]\n\t"
"v_mfma_f32_16x16x32_bf16 v[102:105], a[228:231], a[180:183], v[102:105]\n\t"
"buffer_load_dwordx4 a[252:255], v252, s[8:11], s20 offen offset:2048 sc1\n\t"
"s_waitcnt lgkmcnt(0)\n\t"
"s_barrier\n\t"
"v_mfma_f32_16x16x32_bf16 v[106:109], a[220:223], a[176:179], v[106:109]\n\t"
"v_mfma_f32_16x16x32_bf16 v[106:109], a[224:227], a[180:183], v[106:109]\n\t"
"v_mfma_f32_16x16x32_bf16 v[106:109], a[228:231], a[184:187], v[106:109]\n\t"
"ds_write_b64 v243, v[226:227] offset:0\n\t"
"ds_write_b64 v243, v[230:231] offset:512\n\t"
"v_mfma_f32_16x16x32_bf16 v[110:113], a[220:223], a[180:183], v[110:113]\n\t"
"ds_read_b64_tr_b16 v[226:227], v244 offset:27136\n\t"
"v_mfma_f32_16x16x32_bf16 v[110:113], a[224:227], a[184:187], v[110:113]\n\t"
"ds_read_b64_tr_b16 v[228:229], v244 offset:30208\n\t"
"v_mfma_f32_16x16x32_bf16 v[110:113], a[228:231], a[188:191], v[110:113]\n\t"
"ds_write_b64 v243, v[234:235] offset:1024\n\t"
"v_mfma_f32_16x16x32_bf16 v[114:117], a[220:223], a[184:187], v[114:117]\n\t"
"ds_read_b64_tr_b16 v[230:231], v244 offset:33280\n\t"
"v_mfma_f32_16x16x32_bf16 v[114:117], a[224:227], a[188:191], v[114:117]\n\t"
"ds_read_b64_tr_b16 v[232:233], v244 offset:36352\n\t"
"v_mfma_f32_16x16x32_bf16 v[114:117], a[228:231], a[192:195], v[114:117]\n\t"
"ds_write_b64 v243, v[222:223] offset:1536\n\t"
"v_mfma_f32_16x16x32_bf16 v[118:121], a[220:223], a[188:191], v[118:121]\n\t"
"ds_read_b64_tr_b16 v[234:235], v244 offset:40960\n\t"
"v_mfma_f32_16x16x32_bf16 v[118:121], a[224:227], a[192:195], v[118:121]\n\t"
"ds_read_b64_tr_b16 v[236:237], v244 offset:44032\n\t"
"v_mfma_f32_16x16x32_bf16 v[118:121], a[228:231], a[196:199], v[118:121]\n\t"
"ds_read_b64_tr_b16 v[222:223], v244 offset:47104\n\t"
"ds_read_b64_tr_b16 v[224:225], v244 offset:50176\n\t"
"s_waitcnt lgkmcnt(8)\n\t"
"v_mfma_f32_16x16x32_bf16 v[226:229], v[226:229], v[218:221], 0\n\t"
"v_mfma_f32_16x16x32_bf16 v[122:125], a[220:223], a[192:195], v[122:125]\n\t"
"v_mfma_f32_16x16x32_bf16 v[122:125], a[224:227], a[196:199], v[122:125]\n\t"
"v_mfma_f32_16x16x32_bf16 v[122:125], a[228:231], a[200:203], v[122:125]\n\t"
"s_waitcnt lgkmcnt(5)\n\t"
"v_mfma_f32_16x16x32_bf16 v[230:233], v[230:233], v[218:221], 0\n\t"
"v_mfma_f32_16x16x32_bf16 v[126:129], a[220:223], a[196:199], v[126:129]\n\t"
"s_nop 1\n\t"
"v_cvt_pk_bf16_f32 v226, v226, v227\n\t"
"v_cvt_pk_bf16_f32 v227, v228, v229\n\t"
"v_mfma_f32_16x16x32_bf16 v[126:129], a[224:227], a[200:203], v[126:129]\n\t"
"v_mfma_f32_16x16x32_bf16 v[126:129], a[228:231], a[204:207], v[126:129]\n\t"
"s_mov_b32 s20, 1155072\n\t"
"buffer_load_dwordx4 a[220:223], v252, s[8:11], s20 offen offset:0 sc1\n\t"
"s_waitcnt lgkmcnt(2)\n\t"
"v_mfma_f32_16x16x32_bf16 v[234:237], v[234:237], v[218:221], 0\n\t"
"buffer_load_dwordx4 a[224:227], v252, s[8:11], s20 offen offset:1024 sc1\n\t"
"s_waitcnt vmcnt(8)\n\t"
"v_mfma_f32_16x16x32_bf16 v[130:133], a[232:235], a[168:171], v[130:133]\n\t"
"v_cvt_pk_bf16_f32 v230, v230, v231\n\t"
"v_cvt_pk_bf16_f32 v231, v232, v233\n\t"
"v_mfma_f32_16x16x32_bf16 v[130:133], a[236:239], a[172:175], v[130:133]\n\t"
"v_mfma_f32_16x16x32_bf16 v[130:133], a[240:243], a[176:179], v[130:133]\n\t"
"ds_write_b64 v243, v[226:227] offset:2048\n\t"
"s_waitcnt lgkmcnt(1)\n\t"
"v_mfma_f32_16x16x32_bf16 v[222:225], v[222:225], v[218:221], 0\n\t"
"ds_read_b64_tr_b16 v[226:227], v244 offset:53248\n\t"
"ds_read_b64_tr_b16 v[228:229], v244 offset:56320\n\t"
"v_mfma_f32_16x16x32_bf16 v[134:137], a[232:235], a[172:175], v[134:137]\n\t"
"v_cvt_pk_bf16_f32 v234, v234, v235\n\t"
"v_cvt_pk_bf16_f32 v235, v236, v237\n\t"
"v_mfma_f32_16x16x32_bf16 v[134:137], a[236:239], a[176:179], v[134:137]\n\t"
"v_mfma_f32_16x16x32_bf16 v[134:137], a[240:243], a[180:183], v[134:137]\n\t"
"buffer_load_dwordx4 a[228:231], v252, s[8:11], s20 offen offset:2048 sc1\n\t"
"v_mfma_f32_16x16x32_bf16 v[138:141], a[232:235], a[176:179], v[138:141]\n\t"
"v_mfma_f32_16x16x32_bf16 v[138:141], a[236:239], a[180:183], v[138:141]\n\t"
"v_cvt_pk_bf16_f32 v222, v222, v223\n\t"
"v_cvt_pk_bf16_f32 v223, v224, v225\n\t"
"v_mfma_f32_16x16x32_bf16 v[138:141], a[240:243], a[184:187], v[138:141]\n\t"
"ds_write_b64 v243, v[230:231] offset:2560\n\t"
"v_mfma_f32_16x16x32_bf16 v[142:145], a[232:235], a[180:183], v[142:145]\n\t"
"v_mfma_f32_16x16x32_bf16 v[142:145], a[236:239], a[184:187], v[142:145]\n\t"
"v_mfma_f32_16x16x32_bf16 v[142:145], a[240:243], a[188:191], v[142:145]\n\t"
"ds_write_b64 v243, v[234:235] offset:3072\n\t"
"s_waitcnt lgkmcnt(2)\n\t"
"v_mfma_f32_16x16x32_bf16 v[226:229], v[226:229], v[218:221], 0\n\t"
"ds_write_b64 v243, v[222:223] offset:3584\n\t"
"v_mfma_f32_16x16x32_bf16 v[146:149], a[232:235], a[184:187], v[146:149]\n\t"
"v_mfma_f32_16x16x32_bf16 v[146:149], a[236:239], a[188:191], v[146:149]\n\t"
"v_mfma_f32_16x16x32_bf16 v[146:149], a[240:243], a[192:195], v[146:149]\n\t"
"v_mfma_f32_16x16x32_bf16 v[150:153], a[232:235], a[188:191], v[150:153]\n\t"
"v_mfma_f32_16x16x32_bf16 v[150:153], a[236:239], a[192:195], v[150:153]\n\t"
"s_nop 1\n\t"
"v_cvt_pk_bf16_f32 v226, v226, v227\n\t"
"v_cvt_pk_bf16_f32 v227, v228, v229\n\t"
"v_mfma_f32_16x16x32_bf16 v[150:153], a[240:243], a[196:199], v[150:153]\n\t"
"v_mfma_f32_16x16x32_bf16 v[154:157], a[232:235], a[192:195], v[154:157]\n\t"
"v_mfma_f32_16x16x32_bf16 v[154:157], a[236:239], a[196:199], v[154:157]\n\t"
"v_mfma_f32_16x16x32_bf16 v[154:157], a[240:243], a[200:203], v[154:157]\n\t"
"ds_write_b64 v243, v[226:227] offset:4096\n\t"
"v_mfma_f32_16x16x32_bf16 v[158:161], a[232:235], a[196:199], v[158:161]\n\t"
"v_mfma_f32_16x16x32_bf16 v[158:161], a[236:239], a[200:203], v[158:161]\n\t"
"v_mfma_f32_16x16x32_bf16 v[158:161], a[240:243], a[204:207], v[158:161]\n\t"
"s_mov_b32 s20, 1167360\n\t"
"buffer_load_dwordx4 a[232:235], v252, s[8:11], s20 offen offset:0 sc1\n\t"
"s_waitcnt vmcnt(7)\n\t"
"v_mfma_f32_16x16x32_bf16 v[162:165], a[244:247], a[168:171], v[162:165]\n\t"
"v_mfma_f32_16x16x32_bf16 v[162:165], a[208:211], a[172:175], v[162:165]\n\t"
"v_mfma_f32_16x16x32_bf16 v[162:165], a[248:251], a[176:179], v[162:165]\n\t"
"buffer_load_dwordx4 a[236:239], v252, s[8:11], s20 offen offset:1024 sc1\n\t"
"s_waitcnt lgkmcnt(0)\n\t"
"s_barrier\n\t"
"v_mfma_f32_16x16x32_bf16 v[166:169], a[244:247], a[172:175], v[166:169]\n\t"
"ds_read_b128 a[168:171], v242 offset:0\n\t"
"v_mfma_f32_16x16x32_bf16 v[166:169], a[208:211], a[176:179], v[166:169]\n\t"
"ds_read_b128 a[172:175], v242 offset:512\n\t"
"v_mfma_f32_16x16x32_bf16 v[166:169], a[248:251], a[180:183], v[166:169]\n\t"
"buffer_load_dwordx4 a[240:243], v252, s[8:11], s20 offen offset:2048 sc1\n\t"
"v_mfma_f32_16x16x32_bf16 v[170:173], a[244:247], a[176:179], v[170:173]\n\t"
"ds_read_b128 a[176:179], v242 offset:1024\n\t"
"v_mfma_f32_16x16x32_bf16 v[170:173], a[208:211], a[180:183], v[170:173]\n\t"
"v_mfma_f32_16x16x32_bf16 v[170:173], a[248:251], a[184:187], v[170:173]\n\t"
"v_mfma_f32_16x16x32_bf16 v[174:177], a[244:247], a[180:183], v[174:177]\n\t"
"ds_read_b128 a[180:183], v242 offset:1536\n\t"
"v_mfma_f32_16x16x32_bf16 v[174:177], a[208:211], a[184:187], v[174:177]\n\t"
"v_mfma_f32_16x16x32_bf16 v[174:177], a[248:251], a[188:191], v[174:177]\n\t"
"v_mfma_f32_16x16x32_bf16 v[178:181], a[244:247], a[184:187], v[178:181]\n\t"
"ds_read_b128 a[184:187], v242 offset:2048\n\t"
"v_mfma_f32_16x16x32_bf16 v[178:181], a[208:211], a[188:191], v[178:181]\n\t"
"v_mfma_f32_16x16x32_bf16 v[178:181], a[248:251], a[192:195], v[178:181]\n\t"
"v_mfma_f32_16x16x32_bf16 v[182:185], a[244:247], a[188:191], v[182:185]\n\t"
"ds_read_b128 a[188:191], v242 offset:2560\n\t"
"v_mfma_f32_16x16x32_bf16 v[182:185], a[208:211], a[192:195], v[182:185]\n\t"
"v_mfma_f32_16x16x32_bf16 v[182:185], a[248:251], a[196:199], v[182:185]\n\t"
"v_mfma_f32_16x16x32_bf16 v[186:189], a[244:247], a[192:195], v[186:189]\n\t"
"ds_read_b128 a[192:195], v242 offset:3072\n\t"
"v_mfma_f32_16x16x32_bf16 v[186:189], a[208:211], a[196:199], v[186:189]\n\t"
"v_mfma_f32_16x16x32_bf16 v[186:189], a[248:251], a[200:203], v[186:189]\n\t"
"v_mfma_f32_16x16x32_bf16 v[190:193], a[244:247], a[196:199], v[190:193]\n\t"
"ds_read_b128 a[196:199], v242 offset:3584\n\t"
"v_mfma_f32_16x16x32_bf16 v[190:193], a[208:211], a[200:203], v[190:193]\n\t"
"ds_read_b128 a[200:203], v242 offset:4096\n\t"
"v_mfma_f32_16x16x32_bf16 v[190:193], a[248:251], a[204:207], v[190:193]\n\t"
"s_mov_b32 s20, 1179648\n\t"
"buffer_load_dwordx4 a[244:247], v252, s[8:11], s20 offen offset:0 sc1\n\t"
"s_waitcnt vmcnt(7) lgkmcnt(6)\n\t"
"v_mfma_f32_16x16x32_bf16 v[0:3], a[212:215], a[168:171], v[0:3]\n\t"
"ds_read_b128 a[204:207], v242 offset:4608\n\t"
"v_mfma_f32_16x16x32_bf16 v[0:3], a[216:219], a[172:175], v[0:3]\n\t"
"v_mfma_f32_16x16x32_bf16 v[0:3], a[252:255], a[176:179], v[0:3]\n\t"
"buffer_load_dwordx4 a[208:211], v252, s[8:11], s20 offen offset:1024 sc1\n\t"
"s_waitcnt lgkmcnt(6)\n\t"
"v_mfma_f32_16x16x32_bf16 v[4:7], a[212:215], a[172:175], v[4:7]\n\t"
"v_mfma_f32_16x16x32_bf16 v[4:7], a[216:219], a[176:179], v[4:7]\n\t"
"v_mfma_f32_16x16x32_bf16 v[4:7], a[252:255], a[180:183], v[4:7]\n\t"
"buffer_load_dwordx4 a[248:251], v252, s[8:11], s20 offen offset:2048 sc1\n\t"
"s_waitcnt lgkmcnt(5)\n\t"
"v_mfma_f32_16x16x32_bf16 v[8:11], a[212:215], a[176:179], v[8:11]\n\t"
"v_mfma_f32_16x16x32_bf16 v[8:11], a[216:219], a[180:183], v[8:11]\n\t"
"v_mfma_f32_16x16x32_bf16 v[8:11], a[252:255], a[184:187], v[8:11]\n\t"
"s_waitcnt lgkmcnt(4)\n\t"
"v_mfma_f32_16x16x32_bf16 v[12:15], a[212:215], a[180:183], v[12:15]\n\t"
"v_mfma_f32_16x16x32_bf16 v[12:15], a[216:219], a[184:187], v[12:15]\n\t"
"v_mfma_f32_16x16x32_bf16 v[12:15], a[252:255], a[188:191], v[12:15]\n\t"
"s_waitcnt lgkmcnt(3)\n\t"
"v_mfma_f32_16x16x32_bf16 v[16:19], a[212:215], a[184:187], v[16:19]\n\t"
"v_xor_b32 v244, 0xc0, v244\n\t"
"v_mfma_f32_16x16x32_bf16 v[16:19], a[216:219], a[188:191], v[16:19]\n\t"
"v_mfma_f32_16x16x32_bf16 v[16:19], a[252:255], a[192:195], v[16:19]\n\t"
"s_waitcnt lgkmcnt(2)\n\t"
"v_mfma_f32_16x16x32_bf16 v[20:23], a[212:215], a[188:191], v[20:23]\n\t"
"v_mfma_f32_16x16x32_bf16 v[20:23], a[216:219], a[192:195], v[20:23]\n\t"
"v_mfma_f32_16x16x32_bf16 v[20:23], a[252:255], a[196:199], v[20:23]\n\t"
"s_waitcnt lgkmcnt(1)\n\t"
"v_mfma_f32_16x16x32_bf16 v[24:27], a[212:215], a[192:195], v[24:27]\n\t"
"v_mfma_f32_16x16x32_bf16 v[24:27], a[216:219], a[196:199], v[24:27]\n\t"
"v_mfma_f32_16x16x32_bf16 v[24:27], a[252:255], a[200:203], v[24:27]\n\t"
"s_waitcnt lgkmcnt(0)\n\t"
"v_mfma_f32_16x16x32_bf16 v[28:31], a[212:215], a[196:199], v[28:31]\n\t"
"v_mfma_f32_16x16x32_bf16 v[28:31], a[216:219], a[200:203], v[28:31]\n\t"
"v_mfma_f32_16x16x32_bf16 v[28:31], a[252:255], a[204:207], v[28:31]\n\t"
"s_mov_b32 s20, 1191936\n\t"
"buffer_load_dwordx4 a[212:215], v252, s[8:11], s20 offen offset:0 sc1\n\t"
"s_waitcnt vmcnt(7)\n\t"
"v_mfma_f32_16x16x32_bf16 v[32:35], a[220:223], a[168:171], v[32:35]\n\t"
"v_mfma_f32_16x16x32_bf16 v[32:35], a[224:227], a[172:175], v[32:35]\n\t"
"v_mfma_f32_16x16x32_bf16 v[32:35], a[228:231], a[176:179], v[32:35]\n\t"
"buffer_load_dwordx4 a[216:219], v252, s[8:11], s20 offen offset:1024 sc1\n\t"
"v_mfma_f32_16x16x32_bf16 v[36:39], a[220:223], a[172:175], v[36:39]\n\t"
"ds_read_b64_tr_b16 v[230:231], v244 offset:1024\n\t"
"v_mfma_f32_16x16x32_bf16 v[36:39], a[224:227], a[176:179], v[36:39]\n\t"
"ds_read_b64_tr_b16 v[232:233], v244 offset:4096\n\t"
"v_mfma_f32_16x16x32_bf16 v[36:39], a[228:231], a[180:183], v[36:39]\n\t"
"buffer_load_dwordx4 a[252:255], v252, s[8:11], s20 offen offset:2048 sc1\n\t"
"v_mfma_f32_16x16x32_bf16 v[40:43], a[220:223], a[176:179], v[40:43]\n\t"
"v_mfma_f32_16x16x32_bf16 v[40:43], a[224:227], a[180:183], v[40:43]\n\t"
"v_mfma_f32_16x16x32_bf16 v[40:43], a[228:231], a[184:187], v[40:43]\n\t"
"v_mfma_f32_16x16x32_bf16 v[44:47], a[220:223], a[180:183], v[44:47]\n\t"
"ds_read_b64_tr_b16 v[234:235], v244 offset:7168\n\t"
"v_mfma_f32_16x16x32_bf16 v[44:47], a[224:227], a[184:187], v[44:47]\n\t"
"ds_read_b64_tr_b16 v[236:237], v244 offset:10240\n\t"
"v_mfma_f32_16x16x32_bf16 v[44:47], a[228:231], a[188:191], v[44:47]\n\t"
"v_mfma_f32_16x16x32_bf16 v[48:51], a[220:223], a[184:187], v[48:51]\n\t"
"v_mfma_f32_16x16x32_bf16 v[48:51], a[224:227], a[188:191], v[48:51]\n\t"
"v_mfma_f32_16x16x32_bf16 v[48:51], a[228:231], a[192:195], v[48:51]\n\t"
"v_mfma_f32_16x16x32_bf16 v[52:55], a[220:223], a[188:191], v[52:55]\n\t"
"v_mfma_f32_16x16x32_bf16 v[52:55], a[224:227], a[192:195], v[52:55]\n\t"
"v_mfma_f32_16x16x32_bf16 v[52:55], a[228:231], a[196:199], v[52:55]\n\t"
"s_waitcnt lgkmcnt(2)\n\t"
"v_mfma_f32_16x16x32_bf16 v[230:233], v[230:233], v[218:221], 0\n\t"
"v_mfma_f32_16x16x32_bf16 v[56:59], a[220:223], a[192:195], v[56:59]\n\t"
"ds_read_b64_tr_b16 v[222:223], v244 offset:13312\n\t"
"v_mfma_f32_16x16x32_bf16 v[56:59], a[224:227], a[196:199], v[56:59]\n\t"
"ds_read_b64_tr_b16 v[224:225], v244 offset:16384\n\t"
"v_mfma_f32_16x16x32_bf16 v[56:59], a[228:231], a[200:203], v[56:59]\n\t"
"v_mfma_f32_16x16x32_bf16 v[60:63], a[220:223], a[196:199], v[60:63]\n\t"
"v_mfma_f32_16x16x32_bf16 v[60:63], a[224:227], a[200:203], v[60:63]\n\t"
"s_nop 0\n\t"
"v_cvt_pk_bf16_f32 v230, v230, v231\n\t"
"v_cvt_pk_bf16_f32 v231, v232, v233\n\t"
"v_mfma_f32_16x16x32_bf16 v[60:63], a[228:231], a[204:207], v[60:63]\n\t"
"s_waitcnt lgkmcnt(2)\n\t"
"v_mfma_f32_16x16x32_bf16 v[234:237], v[234:237], v[218:221], 0\n\t"
"s_mov_b32 s20, 1204224\n\t"
"buffer_load_dwordx4 a[220:223], v252, s[8:11], s20 offen offset:0 sc1\n\t"
"s_waitcnt vmcnt(7)\n\t"
"v_mfma_f32_16x16x32_bf16 v[64:67], a[232:235], a[168:171], v[64:67]\n\t"
"ds_read_b64_tr_b16 v[226:227], v244 offset:20992\n\t"
"v_mfma_f32_16x16x32_bf16 v[64:67], a[236:239], a[172:175], v[64:67]\n\t"
"ds_read_b64_tr_b16 v[228:229], v244 offset:24064\n\t"
"v_mfma_f32_16x16x32_bf16 v[64:67], a[240:243], a[176:179], v[64:67]\n\t"
"buffer_load_dwordx4 a[224:227], v252, s[8:11], s20 offen offset:1024 sc1\n\t"
"v_mfma_f32_16x16x32_bf16 v[68:71], a[232:235], a[172:175], v[68:71]\n\t"
"ds_read_b128 a[168:171], v242 offset:9216\n\t"
"v_mfma_f32_16x16x32_bf16 v[68:71], a[236:239], a[176:179], v[68:71]\n\t"
"v_cvt_pk_bf16_f32 v234, v234, v235\n\t"
"v_cvt_pk_bf16_f32 v235, v236, v237\n\t"
"v_mfma_f32_16x16x32_bf16 v[68:71], a[240:243], a[180:183], v[68:71]\n\t"
"buffer_load_dwordx4 a[228:231], v252, s[8:11], s20 offen offset:2048 sc1\n\t"
"v_mfma_f32_16x16x32_bf16 v[72:75], a[232:235], a[176:179], v[72:75]\n\t"
"ds_read_b128 a[172:175], v242 offset:9728\n\t"
"v_mfma_f32_16x16x32_bf16 v[72:75], a[236:239], a[180:183], v[72:75]\n\t"
"ds_read_b128 a[176:179], v242 offset:10240\n\t"
"v_mfma_f32_16x16x32_bf16 v[72:75], a[240:243], a[184:187], v[72:75]\n\t"
"s_waitcnt lgkmcnt(5)\n\t"
"v_mfma_f32_16x16x32_bf16 v[222:225], v[222:225], v[218:221], 0\n\t"
"v_mfma_f32_16x16x32_bf16 v[76:79], a[232:235], a[180:183], v[76:79]\n\t"
"ds_read_b128 a[180:183], v242 offset:10752\n\t"
"v_mfma_f32_16x16x32_bf16 v[76:79], a[236:239], a[184:187], v[76:79]\n\t"
"v_mfma_f32_16x16x32_bf16 v[76:79], a[240:243], a[188:191], v[76:79]\n\t"
"v_mfma_f32_16x16x32_bf16 v[80:83], a[232:235], a[184:187], v[80:83]\n\t"
"ds_read_b128 a[184:187], v242 offset:11264\n\t"
"v_mfma_f32_16x16x32_bf16 v[80:83], a[236:239], a[188:191], v[80:83]\n\t"
"s_nop 0\n\t"
"v_cvt_pk_bf16_f32 v222, v222, v223\n\t"
"v_mfma_f32_16x16x32_bf16 v[80:83], a[240:243], a[192:195], v[80:83]\n\t"
"v_cvt_pk_bf16_f32 v223, v224, v225\n\t"
"s_waitcnt lgkmcnt(5)\n\t"
"v_mfma_f32_16x16x32_bf16 v[226:229], v[226:229], v[218:221], 0\n\t"
"v_mfma_f32_16x16x32_bf16 v[84:87], a[232:235], a[188:191], v[84:87]\n\t"
"ds_read_b128 a[188:191], v242 offset:11776\n\t"
"v_mfma_f32_16x16x32_bf16 v[84:87], a[236:239], a[192:195], v[84:87]\n\t"
"v_mfma_f32_16x16x32_bf16 v[84:87], a[240:243], a[196:199], v[84:87]\n\t"
"v_mfma_f32_16x16x32_bf16 v[88:91], a[232:235], a[192:195], v[88:91]\n\t"
"ds_read_b128 a[192:195], v242 offset:12288\n\t"
"v_mfma_f32_16x16x32_bf16 v[88:91], a[236:239], a[196:199], v[88:91]\n\t"
"s_nop 0\n\t"
"v_cvt_pk_bf16_f32 v226, v226, v227\n\t"
"v_mfma_f32_16x16x32_bf16 v[88:91], a[240:243], a[200:203], v[88:91]\n\t"
"v_cvt_pk_bf16_f32 v227, v228, v229\n\t"
"v_mfma_f32_16x16x32_bf16 v[92:95], a[232:235], a[196:199], v[92:95]\n\t"
"ds_read_b128 a[196:199], v242 offset:12800\n\t"
"v_mfma_f32_16x16x32_bf16 v[92:95], a[236:239], a[200:203], v[92:95]\n\t"
"ds_read_b128 a[200:203], v242 offset:13312\n\t"
"v_mfma_f32_16x16x32_bf16 v[92:95], a[240:243], a[204:207], v[92:95]\n\t"
"s_mov_b32 s20, 1216512\n\t"
"buffer_load_dwordx4 a[232:235], v252, s[8:11], s20 offen offset:0 sc1\n\t"
"s_waitcnt vmcnt(7) lgkmcnt(6)\n\t"
"v_mfma_f32_16x16x32_bf16 v[98:101], a[244:247], a[168:171], v[98:101]\n\t"
"ds_read_b128 a[204:207], v242 offset:13824\n\t"
"v_mfma_f32_16x16x32_bf16 v[98:101], a[208:211], a[172:175], v[98:101]\n\t"
"v_mfma_f32_16x16x32_bf16 v[98:101], a[248:251], a[176:179], v[98:101]\n\t"
"buffer_load_dwordx4 a[236:239], v252, s[8:11], s20 offen offset:1024 sc1\n\t"
"s_waitcnt lgkmcnt(6)\n\t"
"v_mfma_f32_16x16x32_bf16 v[102:105], a[244:247], a[172:175], v[102:105]\n\t"
"v_mfma_f32_16x16x32_bf16 v[102:105], a[208:211], a[176:179], v[102:105]\n\t"
"v_mfma_f32_16x16x32_bf16 v[102:105], a[248:251], a[180:183], v[102:105]\n\t"
"buffer_load_dwordx4 a[240:243], v252, s[8:11], s20 offen offset:2048 sc1\n\t"
"s_waitcnt lgkmcnt(0)\n\t"
"s_barrier\n\t"
"v_mfma_f32_16x16x32_bf16 v[106:109], a[244:247], a[176:179], v[106:109]\n\t"
"v_fma_f32 v0, v98, s52, v0\n\t"
"v_fma_f32 v1, v99, s52, v1\n\t"
"v_mfma_f32_16x16x32_bf16 v[106:109], a[208:211], a[180:183], v[106:109]\n\t"
"v_fma_f32 v2, v100, s52, v2\n\t"
"v_fma_f32 v3, v101, s52, v3\n\t"
"v_mfma_f32_16x16x32_bf16 v[106:109], a[248:251], a[184:187], v[106:109]\n\t"
"ds_write_b64 v243, v[230:231] offset:0\n\t"
"v_mfma_f32_16x16x32_bf16 v[110:113], a[244:247], a[180:183], v[110:113]\n\t"
"ds_read_b64_tr_b16 v[230:231], v244 offset:27136\n\t"
"v_fma_f32 v98, v0, s54, -v98\n\t"
"v_mfma_f32_16x16x32_bf16 v[110:113], a[208:211], a[184:187], v[110:113]\n\t"
"ds_read_b64_tr_b16 v[232:233], v244 offset:30208\n\t"
"v_fma_f32 v99, v1, s54, -v99\n\t"
"v_mfma_f32_16x16x32_bf16 v[110:113], a[248:251], a[188:191], v[110:113]\n\t"
"ds_write_b64 v243, v[234:235] offset:512\n\t"
"v_mfma_f32_16x16x32_bf16 v[114:117], a[244:247], a[184:187], v[114:117]\n\t"
"ds_read_b64_tr_b16 v[234:235], v244 offset:33280\n\t"
"v_fma_f32 v100, v2, s54, -v100\n\t"
"v_mfma_f32_16x16x32_bf16 v[114:117], a[208:211], a[188:191], v[114:117]\n\t"
"ds_read_b64_tr_b16 v[236:237], v244 offset:36352\n\t"
"v_fma_f32 v101, v3, s54, -v101\n\t"
"v_mfma_f32_16x16x32_bf16 v[114:117], a[248:251], a[192:195], v[114:117]\n\t"
"ds_write_b64 v243, v[222:223] offset:1024\n\t"
"ds_write_b64 v243, v[226:227] offset:1536\n\t"
"v_mfma_f32_16x16x32_bf16 v[118:121], a[244:247], a[188:191], v[118:121]\n\t"
"v_mfma_f32_16x16x32_bf16 v[118:121], a[208:211], a[192:195], v[118:121]\n\t"
"v_mfma_f32_16x16x32_bf16 v[118:121], a[248:251], a[196:199], v[118:121]\n\t"
"s_waitcnt lgkmcnt(5)\n\t"
"v_mfma_f32_16x16x32_bf16 v[230:233], v[230:233], v[218:221], 0\n\t"
"ds_read_b64_tr_b16 v[222:223], v244 offset:40960\n\t"
"ds_read_b64_tr_b16 v[224:225], v244 offset:44032\n\t"
"v_mfma_f32_16x16x32_bf16 v[122:125], a[244:247], a[192:195], v[122:125]\n\t"
"v_mfma_f32_16x16x32_bf16 v[122:125], a[208:211], a[196:199], v[122:125]\n\t"
"v_mfma_f32_16x16x32_bf16 v[122:125], a[248:251], a[200:203], v[122:125]\n\t"
"ds_read_b64_tr_b16 v[226:227], v244 offset:47104\n\t"
"s_waitcnt lgkmcnt(5)\n\t"
"v_mfma_f32_16x16x32_bf16 v[234:237], v[234:237], v[218:221], 0\n\t"
"ds_read_b64_tr_b16 v[228:229], v244 offset:50176\n\t"
"v_mfma_f32_16x16x32_bf16 v[126:129], a[244:247], a[196:199], v[126:129]\n\t"
"v_cvt_pk_bf16_f32 v230, v230, v231\n\t"
"v_cvt_pk_bf16_f32 v231, v232, v233\n\t"
"v_mfma_f32_16x16x32_bf16 v[126:129], a[208:211], a[200:203], v[126:129]\n\t"
"v_fma_f32 v4, v102, s52, v4\n\t"
"v_fma_f32 v5, v103, s52, v5\n\t"
"v_mfma_f32_16x16x32_bf16 v[126:129], a[248:251], a[204:207], v[126:129]\n\t"
"s_mov_b32 s20, 1228800\n\t"
"buffer_load_dwordx4 a[244:247], v252, s[8:11], s20 offen offset:0 sc1\n\t"
"s_waitcnt lgkmcnt(2)\n\t"
"v_mfma_f32_16x16x32_bf16 v[222:225], v[222:225], v[218:221], 0\n\t"
"buffer_load_dwordx4 a[208:211], v252, s[8:11], s20 offen offset:1024 sc1\n\t"
"s_waitcnt vmcnt(8)\n\t"
"v_mfma_f32_16x16x32_bf16 v[130:133], a[212:215], a[168:171], v[130:133]\n\t"
"v_cvt_pk_bf16_f32 v234, v234, v235\n\t"
"v_cvt_pk_bf16_f32 v235, v236, v237\n\t"
"v_mfma_f32_16x16x32_bf16 v[130:133], a[216:219], a[172:175], v[130:133]\n\t"
"v_fma_f32 v6, v104, s52, v6\n\t"
"v_fma_f32 v7, v105, s52, v7\n\t"
"v_mfma_f32_16x16x32_bf16 v[130:133], a[252:255], a[176:179], v[130:133]\n\t"
"ds_write_b64 v243, v[230:231] offset:2048\n\t"
"s_waitcnt lgkmcnt(1)\n\t"
"v_mfma_f32_16x16x32_bf16 v[226:229], v[226:229], v[218:221], 0\n\t"
"ds_read_b64_tr_b16 v[230:231], v244 offset:53248\n\t"
"ds_read_b64_tr_b16 v[232:233], v244 offset:56320\n\t"
"v_mfma_f32_16x16x32_bf16 v[134:137], a[212:215], a[172:175], v[134:137]\n\t"
"v_cvt_pk_bf16_f32 v222, v222, v223\n\t"
"v_cvt_pk_bf16_f32 v223, v224, v225\n\t"
"v_mfma_f32_16x16x32_bf16 v[134:137], a[216:219], a[176:179], v[134:137]\n\t"
"v_fma_f32 v102, v4, s54, -v102\n\t"
"v_fma_f32 v103, v5, s54, -v103\n\t"
"v_mfma_f32_16x16x32_bf16 v[134:137], a[252:255], a[180:183], v[134:137]\n\t"
"buffer_load_dwordx4 a[248:251], v252, s[8:11], s20 offen offset:2048 sc1\n\t"
"v_mfma_f32_16x16x32_bf16 v[138:141], a[212:215], a[176:179], v[138:141]\n\t"
"v_fma_f32 v104, v6, s54, -v104\n\t"
"v_fma_f32 v105, v7, s54, -v105\n\t"
"v_mfma_f32_16x16x32_bf16 v[138:141], a[216:219], a[180:183], v[138:141]\n\t"
"v_cvt_pk_bf16_f32 v226, v226, v227\n\t"
"v_cvt_pk_bf16_f32 v227, v228, v229\n\t"
"v_mfma_f32_16x16x32_bf16 v[138:141], a[252:255], a[184:187], v[138:141]\n\t"
"ds_write_b64 v243, v[234:235] offset:2560\n\t"
"v_mfma_f32_16x16x32_bf16 v[142:145], a[212:215], a[180:183], v[142:145]\n\t"
"v_fma_f32 v8, v106, s52, v8\n\t"
"v_fma_f32 v9, v107, s52, v9\n\t"
"v_mfma_f32_16x16x32_bf16 v[142:145], a[216:219], a[184:187], v[142:145]\n\t"
"v_fma_f32 v10, v108, s52, v10\n\t"
"v_fma_f32 v11, v109, s52, v11\n\t"
"v_mfma_f32_16x16x32_bf16 v[142:145], a[252:255], a[188:191], v[142:145]\n\t"
"ds_write_b64 v243, v[222:223] offset:3072\n\t"
"s_waitcnt lgkmcnt(2)\n\t"
"v_mfma_f32_16x16x32_bf16 v[230:233], v[230:233], v[218:221], 0\n\t"
"ds_write_b64 v243, v[226:227] offset:3584\n\t"
"v_mfma_f32_16x16x32_bf16 v[146:149], a[212:215], a[184:187], v[146:149]\n\t"
"v_fma_f32 v106, v8, s54, -v106\n\t"
"v_fma_f32 v107, v9, s54, -v107\n\t"
"v_mfma_f32_16x16x32_bf16 v[146:149], a[216:219], a[188:191], v[146:149]\n\t"
"v_fma_f32 v108, v10, s54, -v108\n\t"
"v_fma_f32 v109, v11, s54, -v109\n\t"
"v_mfma_f32_16x16x32_bf16 v[146:149], a[252:255], a[192:195], v[146:149]\n\t"
"v_fma_f32 v12, v110, s52, v12\n\t"
"v_fma_f32 v13, v111, s52, v13\n\t"
"v_mfma_f32_16x16x32_bf16 v[150:153], a[212:215], a[188:191], v[150:153]\n\t"
"v_fma_f32 v14, v112, s52, v14\n\t"
"v_fma_f32 v15, v113, s52, v15\n\t"
"v_mfma_f32_16x16x32_bf16 v[150:153], a[216:219], a[192:195], v[150:153]\n\t"
"v_cvt_pk_bf16_f32 v230, v230, v231\n\t"
"v_cvt_pk_bf16_f32 v231, v232, v233\n\t"
"v_mfma_f32_16x16x32_bf16 v[150:153], a[252:255], a[196:199], v[150:153]\n\t"
"ds_write_b64 v243, v[230:231] offset:4096\n\t"
"v_mfma_f32_16x16x32_bf16 v[154:157], a[212:215], a[192:195], v[154:157]\n\t"
"v_fma_f32 v110, v12, s54, -v110\n\t"
"v_fma_f32 v111, v13, s54, -v111\n\t"
"v_mfma_f32_16x16x32_bf16 v[154:157], a[216:219], a[196:199], v[154:157]\n\t"
"v_fma_f32 v112, v14, s54, -v112\n\t"
"v_fma_f32 v113, v15, s54, -v113\n\t"
"v_mfma_f32_16x16x32_bf16 v[154:157], a[252:255], a[200:203], v[154:157]\n\t"
"v_fma_f32 v16, v114, s52, v16\n\t"
"v_fma_f32 v17, v115, s52, v17\n\t"
"v_mfma_f32_16x16x32_bf16 v[158:161], a[212:215], a[196:199], v[158:161]\n\t"
"v_fma_f32 v18, v116, s52, v18\n\t"
"v_fma_f32 v19, v117, s52, v19\n\t"
"v_mfma_f32_16x16x32_bf16 v[158:161], a[216:219], a[200:203], v[158:161]\n\t"
"v_fma_f32 v114, v16, s54, -v114\n\t"
"v_fma_f32 v115, v17, s54, -v115\n\t"
"v_mfma_f32_16x16x32_bf16 v[158:161], a[252:255], a[204:207], v[158:161]\n\t"
"s_mov_b32 s20, 1241088\n\t"
"buffer_load_dwordx4 a[212:215], v252, s[8:11], s20 offen offset:0 sc1\n\t"
"s_waitcnt vmcnt(7)\n\t"
"v_mfma_f32_16x16x32_bf16 v[162:165], a[220:223], a[168:171], v[162:165]\n\t"
"v_fma_f32 v116, v18, s54, -v116\n\t"
"v_fma_f32 v117, v19, s54, -v117\n\t"
"v_mfma_f32_16x16x32_bf16 v[162:165], a[224:227], a[172:175], v[162:165]\n\t"
"v_fma_f32 v20, v118, s52, v20\n\t"
"v_fma_f32 v21, v119, s52, v21\n\t"
"v_mfma_f32_16x16x32_bf16 v[162:165], a[228:231], a[176:179], v[162:165]\n\t"
"buffer_load_dwordx4 a[216:219], v252, s[8:11], s20 offen offset:1024 sc1\n\t"
"s_waitcnt lgkmcnt(0)\n\t"
"s_barrier\n\t"
"v_mfma_f32_16x16x32_bf16 v[166:169], a[220:223], a[172:175], v[166:169]\n\t"
"ds_read_b128 a[168:171], v242 offset:0\n\t"
"v_fma_f32 v22, v120, s52, v22\n\t"
"v_mfma_f32_16x16x32_bf16 v[166:169], a[224:227], a[176:179], v[166:169]\n\t"
"ds_read_b128 a[172:175], v242 offset:512\n\t"
"v_fma_f32 v23, v121, s52, v23\n\t"
"v_mfma_f32_16x16x32_bf16 v[166:169], a[228:231], a[180:183], v[166:169]\n\t"
"buffer_load_dwordx4 a[252:255], v252, s[8:11], s20 offen offset:2048 sc1\n\t"
"v_fma_f32 v118, v20, s54, -v118\n\t"
"v_fma_f32 v119, v21, s54, -v119\n\t"
"v_mfma_f32_16x16x32_bf16 v[170:173], a[220:223], a[176:179], v[170:173]\n\t"
"ds_read_b128 a[176:179], v242 offset:1024\n\t"
"v_fma_f32 v120, v22, s54, -v120\n\t"
"v_mfma_f32_16x16x32_bf16 v[170:173], a[224:227], a[180:183], v[170:173]\n\t"
"v_fma_f32 v121, v23, s54, -v121\n\t"
"v_fma_f32 v24, v122, s52, v24\n\t"
"v_mfma_f32_16x16x32_bf16 v[170:173], a[228:231], a[184:187], v[170:173]\n\t"
"v_fma_f32 v25, v123, s52, v25\n\t"
"v_fma_f32 v26, v124, s52, v26\n\t"
"v_fma_f32 v27, v125, s52, v27\n\t"
"v_fma_f32 v122, v24, s54, -v122\n\t"
"v_mfma_f32_16x16x32_bf16 v[174:177], a[220:223], a[180:183], v[174:177]\n\t"
"ds_read_b128 a[180:183], v242 offset:1536\n\t"
"v_fma_f32 v123, v25, s54, -v123\n\t"
"v_mfma_f32_16x16x32_bf16 v[174:177], a[224:227], a[184:187], v[174:177]\n\t"
"v_fma_f32 v124, v26, s54, -v124\n\t"
"v_fma_f32 v125, v27, s54, -v125\n\t"
"v_mfma_f32_16x16x32_bf16 v[174:177], a[228:231], a[188:191], v[174:177]\n\t"
"v_fma_f32 v28, v126, s52, v28\n\t"
"v_fma_f32 v29, v127, s52, v29\n\t"
"v_fma_f32 v30, v128, s52, v30\n\t"
"v_fma_f32 v31, v129, s52, v31\n\t"
"v_mfma_f32_16x16x32_bf16 v[178:181], a[220:223], a[184:187], v[178:181]\n\t"
"ds_read_b128 a[184:187], v242 offset:2048\n\t"
"v_fma_f32 v126, v28, s54, -v126\n\t"
"v_mfma_f32_16x16x32_bf16 v[178:181], a[224:227], a[188:191], v[178:181]\n\t"
"v_fma_f32 v127, v29, s54, -v127\n\t"
"v_fma_f32 v128, v30, s54, -v128\n\t"
"v_mfma_f32_16x16x32_bf16 v[178:181], a[228:231], a[192:195], v[178:181]\n\t"
"v_fma_f32 v129, v31, s54, -v129\n\t"
"v_fma_f32 v32, v130, s52, v32\n\t"
"v_mfma_f32_16x16x32_bf16 v[182:185], a[220:223], a[188:191], v[182:185]\n\t"
"ds_read_b128 a[188:191], v242 offset:2560\n\t"
"v_fma_f32 v33, v131, s52, v33\n\t"
"v_mfma_f32_16x16x32_bf16 v[182:185], a[224:227], a[192:195], v[182:185]\n\t"
"v_fma_f32 v34, v132, s52, v34\n\t"
"v_fma_f32 v35, v133, s52, v35\n\t"
"v_mfma_f32_16x16x32_bf16 v[182:185], a[228:231], a[196:199], v[182:185]\n\t"
"v_fma_f32 v130, v32, s54, -v130\n\t"
"v_fma_f32 v131, v33, s54, -v131\n\t"
"v_fma_f32 v132, v34, s54, -v132\n\t"
"v_fma_f32 v133, v35, s54, -v133\n\t"
"v_mfma_f32_16x16x32_bf16 v[186:189], a[220:223], a[192:195], v[186:189]\n\t"
"ds_read_b128 a[192:195], v242 offset:3072\n\t"
"v_fma_f32 v36, v134, s52, v36\n\t"
"v_mfma_f32_16x16x32_bf16 v[186:189], a[224:227], a[196:199], v[186:189]\n\t"
"v_fma_f32 v37, v135, s52, v37\n\t"
"v_fma_f32 v38, v136, s52, v38\n\t"
"v_mfma_f32_16x16x32_bf16 v[186:189], a[228:231], a[200:203], v[186:189]\n\t"
"v_fma_f32 v39, v137, s52, v39\n\t"
"v_fma_f32 v134, v36, s54, -v134\n\t"
"v_fma_f32 v135, v37, s54, -v135\n\t"
"v_fma_f32 v136, v38, s54, -v136\n\t"
"v_mfma_f32_16x16x32_bf16 v[190:193], a[220:223], a[196:199], v[190:193]\n\t"
"ds_read_b128 a[196:199], v242 offset:3584\n\t"
"v_fma_f32 v137, v39, s54, -v137\n\t"
"v_mfma_f32_16x16x32_bf16 v[190:193], a[224:227], a[200:203], v[190:193]\n\t"
"ds_read_b128 a[200:203], v242 offset:4096\n\t"
"v_fma_f32 v40, v138, s52, v40\n\t"
"v_mfma_f32_16x16x32_bf16 v[190:193], a[228:231], a[204:207], v[190:193]\n\t"
"s_mov_b32 s20, 1253376\n\t"
"buffer_load_dwordx4 a[220:223], v252, s[8:11], s20 offen offset:0 sc1\n\t"
"ds_read_b128 a[204:207], v242 offset:4608\n\t"
"v_fma_f32 v41, v139, s52, v41\n\t"
"s_waitcnt vmcnt(7) lgkmcnt(7)\n\t"
"v_mfma_f32_16x16x32_bf16 v[194:197], a[232:235], a[168:171], v[194:197]\n\t"
"v_fma_f32 v42, v140, s52, v42\n\t"
"v_fma_f32 v43, v141, s52, v43\n\t"
"v_mfma_f32_16x16x32_bf16 v[194:197], a[236:239], a[172:175], v[194:197]\n\t"
"v_fma_f32 v138, v40, s54, -v138\n\t"
"v_fma_f32 v139, v41, s54, -v139\n\t"
"v_mfma_f32_16x16x32_bf16 v[194:197], a[240:243], a[176:179], v[194:197]\n\t"
"buffer_load_dwordx4 a[224:227], v252, s[8:11], s20 offen offset:1024 sc1\n\t"
"v_fma_f32 v140, v42, s54, -v140\n\t"
"v_fma_f32 v141, v43, s54, -v141\n\t"
"s_waitcnt lgkmcnt(6)\n\t"
"v_mfma_f32_16x16x32_bf16 v[202:205], a[232:235], a[172:175], v[202:205]\n\t"
"v_fma_f32 v44, v142, s52, v44\n\t"
"v_fma_f32 v45, v143, s52, v45\n\t"
"v_mfma_f32_16x16x32_bf16 v[202:205], a[236:239], a[176:179], v[202:205]\n\t"
"v_fma_f32 v46, v144, s52, v46\n\t"
"v_fma_f32 v47, v145, s52, v47\n\t"
"v_mfma_f32_16x16x32_bf16 v[202:205], a[240:243], a[180:183], v[202:205]\n\t"
"buffer_load_dwordx4 a[228:231], v252, s[8:11], s20 offen offset:2048 sc1\n\t"
"v_fma_f32 v142, v44, s54, -v142\n\t"
"v_fma_f32 v143, v45, s54, -v143\n\t"
"s_waitcnt lgkmcnt(5)\n\t"
"v_mfma_f32_16x16x32_bf16 v[210:213], a[232:235], a[176:179], v[210:213]\n\t"
"v_fma_f32 v144, v46, s54, -v144\n\t"
"v_fma_f32 v145, v47, s54, -v145\n\t"
"v_mfma_f32_16x16x32_bf16 v[210:213], a[236:239], a[180:183], v[210:213]\n\t"
"v_fma_f32 v48, v146, s52, v48\n\t"
"v_fma_f32 v49, v147, s52, v49\n\t"
"v_mfma_f32_16x16x32_bf16 v[210:213], a[240:243], a[184:187], v[210:213]\n\t"
"v_fma_f32 v50, v148, s52, v50\n\t"
"v_fma_f32 v51, v149, s52, v51\n\t"
"v_fma_f32 v146, v48, s54, -v146\n\t"
"v_fma_f32 v147, v49, s54, -v147\n\t"
"s_waitcnt lgkmcnt(4)\n\t"
"v_mfma_f32_16x16x32_bf16 a[0:3], a[232:235], a[180:183], a[0:3]\n\t"
"v_fma_f32 v148, v50, s54, -v148\n\t"
"v_fma_f32 v149, v51, s54, -v149\n\t"
"v_mfma_f32_16x16x32_bf16 a[0:3], a[236:239], a[184:187], a[0:3]\n\t"
"v_fma_f32 v52, v150, s52, v52\n\t"
"v_fma_f32 v53, v151, s52, v53\n\t"
"v_mfma_f32_16x16x32_bf16 a[0:3], a[240:243], a[188:191], a[0:3]\n\t"
"v_fma_f32 v54, v152, s52, v54\n\t"
"v_fma_f32 v55, v153, s52, v55\n\t"
"v_fma_f32 v150, v52, s54, -v150\n\t"
"v_fma_f32 v151, v53, s54, -v151\n\t"
"s_waitcnt lgkmcnt(3)\n\t"
"v_mfma_f32_16x16x32_bf16 a[8:11], a[232:235], a[184:187], a[8:11]\n\t"
"v_fma_f32 v152, v54, s54, -v152\n\t"
"v_fma_f32 v153, v55, s54, -v153\n\t"
"v_mfma_f32_16x16x32_bf16 a[8:11], a[236:239], a[188:191], a[8:11]\n\t"
"v_fma_f32 v56, v154, s52, v56\n\t"
"v_fma_f32 v57, v155, s52, v57\n\t"
"v_mfma_f32_16x16x32_bf16 a[8:11], a[240:243], a[192:195], a[8:11]\n\t"
"v_fma_f32 v58, v156, s52, v58\n\t"
"v_fma_f32 v59, v157, s52, v59\n\t"
"v_fma_f32 v154, v56, s54, -v154\n\t"
"v_fma_f32 v155, v57, s54, -v155\n\t"
"s_waitcnt lgkmcnt(2)\n\t"
"v_mfma_f32_16x16x32_bf16 a[16:19], a[232:235], a[188:191], a[16:19]\n\t"
"v_fma_f32 v156, v58, s54, -v156\n\t"
"v_fma_f32 v157, v59, s54, -v157\n\t"
"v_mfma_f32_16x16x32_bf16 a[16:19], a[236:239], a[192:195], a[16:19]\n\t"
"v_fma_f32 v60, v158, s52, v60\n\t"
"v_fma_f32 v61, v159, s52, v61\n\t"
"v_mfma_f32_16x16x32_bf16 a[16:19], a[240:243], a[196:199], a[16:19]\n\t"
"v_fma_f32 v62, v160, s52, v62\n\t"
"v_fma_f32 v63, v161, s52, v63\n\t"
"v_fma_f32 v158, v60, s54, -v158\n\t"
"v_fma_f32 v159, v61, s54, -v159\n\t"
"s_waitcnt lgkmcnt(1)\n\t"
"v_mfma_f32_16x16x32_bf16 a[24:27], a[232:235], a[192:195], a[24:27]\n\t"
"v_fma_f32 v160, v62, s54, -v160\n\t"
"v_fma_f32 v161, v63, s54, -v161\n\t"
"v_mfma_f32_16x16x32_bf16 a[24:27], a[236:239], a[196:199], a[24:27]\n\t"
"v_fma_f32 v64, v162, s52, v64\n\t"
"v_fma_f32 v65, v163, s52, v65\n\t"
"v_mfma_f32_16x16x32_bf16 a[24:27], a[240:243], a[200:203], a[24:27]\n\t"
"v_fma_f32 v66, v164, s52, v66\n\t"
"v_fma_f32 v67, v165, s52, v67\n\t"
"v_fma_f32 v162, v64, s54, -v162\n\t"
"v_fma_f32 v163, v65, s54, -v163\n\t"
"s_waitcnt lgkmcnt(0)\n\t"
"v_mfma_f32_16x16x32_bf16 a[32:35], a[232:235], a[196:199], a[32:35]\n\t"
"v_fma_f32 v164, v66, s54, -v164\n\t"
"v_fma_f32 v165, v67, s54, -v165\n\t"
"v_mfma_f32_16x16x32_bf16 a[32:35], a[236:239], a[200:203], a[32:35]\n\t"
"v_fma_f32 v68, v166, s52, v68\n\t"
"v_fma_f32 v69, v167, s52, v69\n\t"
"v_mfma_f32_16x16x32_bf16 a[32:35], a[240:243], a[204:207], a[32:35]\n\t"
"s_mov_b32 s20, 1265664\n\t"
"buffer_load_dwordx4 a[232:235], v252, s[8:11], s20 offen offset:0 sc1\n\t"
"v_fma_f32 v70, v168, s52, v70\n\t"
"v_fma_f32 v71, v169, s52, v71\n\t"
"s_waitcnt vmcnt(7)\n\t"
"v_mfma_f32_16x16x32_bf16 a[40:43], a[244:247], a[168:171], a[40:43]\n\t"
"v_fma_f32 v166, v68, s54, -v166\n\t"
"v_fma_f32 v167, v69, s54, -v167\n\t"
"v_mfma_f32_16x16x32_bf16 a[40:43], a[208:211], a[172:175], a[40:43]\n\t"
"v_fma_f32 v168, v70, s54, -v168\n\t"
"v_fma_f32 v169, v71, s54, -v169\n\t"
"v_mfma_f32_16x16x32_bf16 a[40:43], a[248:251], a[176:179], a[40:43]\n\t"
"buffer_load_dwordx4 a[236:239], v252, s[8:11], s20 offen offset:1024 sc1\n\t"
"v_mfma_f32_16x16x32_bf16 a[48:51], a[244:247], a[172:175], a[48:51]\n\t"
"v_fma_f32 v72, v170, s52, v72\n\t"
"v_fma_f32 v73, v171, s52, v73\n\t"
"v_mfma_f32_16x16x32_bf16 a[48:51], a[208:211], a[176:179], a[48:51]\n\t"
"v_fma_f32 v74, v172, s52, v74\n\t"
"v_fma_f32 v75, v173, s52, v75\n\t"
"v_mfma_f32_16x16x32_bf16 a[48:51], a[248:251], a[180:183], a[48:51]\n\t"
"buffer_load_dwordx4 a[240:243], v252, s[8:11], s20 offen offset:2048 sc1\n\t"
"v_fma_f32 v170, v72, s54, -v170\n\t"
"v_fma_f32 v171, v73, s54, -v171\n\t"
"v_mfma_f32_16x16x32_bf16 a[56:59], a[244:247], a[176:179], a[56:59]\n\t"
"v_fma_f32 v172, v74, s54, -v172\n\t"
"v_fma_f32 v173, v75, s54, -v173\n\t"
"v_mfma_f32_16x16x32_bf16 a[56:59], a[208:211], a[180:183], a[56:59]\n\t"
"v_fma_f32 v76, v174, s52, v76\n\t"
"v_fma_f32 v77, v175, s52, v77\n\t"
"v_mfma_f32_16x16x32_bf16 a[56:59], a[248:251], a[184:187], a[56:59]\n\t"
"v_fma_f32 v78, v176, s52, v78\n\t"
"v_fma_f32 v79, v177, s52, v79\n\t"
"v_mfma_f32_16x16x32_bf16 a[64:67], a[244:247], a[180:183], a[64:67]\n\t"
"v_fma_f32 v174, v76, s54, -v174\n\t"
"v_fma_f32 v175, v77, s54, -v175\n\t"
"v_mfma_f32_16x16x32_bf16 a[64:67], a[208:211], a[184:187], a[64:67]\n\t"
"v_fma_f32 v176, v78, s54, -v176\n\t"
"v_fma_f32 v177, v79, s54, -v177\n\t"
"v_mfma_f32_16x16x32_bf16 a[64:67], a[248:251], a[188:191], a[64:67]\n\t"
"v_fma_f32 v80, v178, s52, v80\n\t"
"v_fma_f32 v81, v179, s52, v81\n\t"
"v_mfma_f32_16x16x32_bf16 a[72:75], a[244:247], a[184:187], a[72:75]\n\t"
"v_fma_f32 v82, v180, s52, v82\n\t"
"v_fma_f32 v83, v181, s52, v83\n\t"
"v_mfma_f32_16x16x32_bf16 a[72:75], a[208:211], a[188:191], a[72:75]\n\t"
"v_fma_f32 v178, v80, s54, -v178\n\t"
"v_fma_f32 v179, v81, s54, -v179\n\t"
"v_mfma_f32_16x16x32_bf16 a[72:75], a[248:251], a[192:195], a[72:75]\n\t"
"v_fma_f32 v180, v82, s54, -v180\n\t"
"v_fma_f32 v181, v83, s54, -v181\n\t"
"v_mfma_f32_16x16x32_bf16 a[80:83], a[244:247], a[188:191], a[80:83]\n\t"
"v_fma_f32 v84, v182, s52, v84\n\t"
"v_fma_f32 v85, v183, s52, v85\n\t"
"v_mfma_f32_16x16x32_bf16 a[80:83], a[208:211], a[192:195], a[80:83]\n\t"
"v_fma_f32 v86, v184, s52, v86\n\t"
"v_mfma_f32_16x16x32_bf16 a[80:83], a[248:251], a[196:199], a[80:83]\n\t"
"v_fma_f32 v87, v185, s52, v87\n\t"
"v_mfma_f32_16x16x32_bf16 a[88:91], a[244:247], a[192:195], a[88:91]\n\t"
"v_fma_f32 v182, v84, s54, -v182\n\t"
"v_fma_f32 v183, v85, s54, -v183\n\t"
"v_mfma_f32_16x16x32_bf16 a[88:91], a[208:211], a[196:199], a[88:91]\n\t"
"v_fma_f32 v184, v86, s54, -v184\n\t"
"v_mfma_f32_16x16x32_bf16 a[88:91], a[248:251], a[200:203], a[88:91]\n\t"
"v_fma_f32 v185, v87, s54, -v185\n\t"
"v_mfma_f32_16x16x32_bf16 a[96:99], a[244:247], a[196:199], a[96:99]\n\t"
"v_fma_f32 v88, v186, s52, v88\n\t"
"v_fma_f32 v89, v187, s52, v89\n\t"
"v_mfma_f32_16x16x32_bf16 a[96:99], a[208:211], a[200:203], a[96:99]\n\t"
"v_fma_f32 v90, v188, s52, v90\n\t"
"v_mfma_f32_16x16x32_bf16 a[96:99], a[248:251], a[204:207], a[96:99]\n\t"
"s_mov_b32 s20, 1277952\n\t"
"buffer_load_dwordx4 a[244:247], v252, s[8:11], s20 offen offset:0 sc1\n\t"
"s_waitcnt vmcnt(7)\n\t"
"v_mfma_f32_16x16x32_bf16 a[104:107], a[212:215], a[168:171], a[104:107]\n\t"
"ds_read_b128 a[168:171], v242 offset:9216\n\t"
"v_fma_f32 v91, v189, s52, v91\n\t"
"v_mfma_f32_16x16x32_bf16 a[104:107], a[216:219], a[172:175], a[104:107]\n\t"
"v_fma_f32 v186, v88, s54, -v186\n\t"
"v_mfma_f32_16x16x32_bf16 a[104:107], a[252:255], a[176:179], a[104:107]\n\t"
"buffer_load_dwordx4 a[208:211], v252, s[8:11], s20 offen offset:1024 sc1\n\t"
"v_mfma_f32_16x16x32_bf16 a[112:115], a[212:215], a[172:175], a[112:115]\n\t"
"ds_read_b128 a[172:175], v242 offset:9728\n\t"
"v_fma_f32 v187, v89, s54, -v187\n\t"
"v_mfma_f32_16x16x32_bf16 a[112:115], a[216:219], a[176:179], a[112:115]\n\t"
"v_fma_f32 v188, v90, s54, -v188\n\t"
"v_fma_f32 v189, v91, s54, -v189\n\t"
"v_mfma_f32_16x16x32_bf16 a[112:115], a[252:255], a[180:183], a[112:115]\n\t"
"v_fma_f32 v92, v190, s52, v92\n\t"
"v_mfma_f32_16x16x32_bf16 a[120:123], a[212:215], a[176:179], a[120:123]\n\t"
"ds_read_b128 a[176:179], v242 offset:10240\n\t"
"v_fma_f32 v93, v191, s52, v93\n\t"
"v_mfma_f32_16x16x32_bf16 a[120:123], a[216:219], a[180:183], a[120:123]\n\t"
"v_fma_f32 v94, v192, s52, v94\n\t"
"v_mfma_f32_16x16x32_bf16 a[120:123], a[252:255], a[184:187], a[120:123]\n\t"
"buffer_load_dwordx4 a[248:251], v252, s[8:11], s20 offen offset:2048 sc1\n\t"
"v_mfma_f32_16x16x32_bf16 a[128:131], a[212:215], a[180:183], a[128:131]\n\t"
"ds_read_b128 a[180:183], v242 offset:10752\n\t"
"v_mfma_f32_16x16x32_bf16 a[128:131], a[216:219], a[184:187], a[128:131]\n\t"
"v_fma_f32 v95, v193, s52, v95\n\t"
"v_mfma_f32_16x16x32_bf16 a[128:131], a[252:255], a[188:191], a[128:131]\n\t"
"v_fma_f32 v190, v92, s54, -v190\n\t"
"v_mfma_f32_16x16x32_bf16 a[136:139], a[212:215], a[184:187], a[136:139]\n\t"
"ds_read_b128 a[184:187], v242 offset:11264\n\t"
"v_mfma_f32_16x16x32_bf16 a[136:139], a[216:219], a[188:191], a[136:139]\n\t"
"v_mfma_f32_16x16x32_bf16 a[136:139], a[252:255], a[192:195], a[136:139]\n\t"
"v_fma_f32 v191, v93, s54, -v191\n\t"
"v_mfma_f32_16x16x32_bf16 a[144:147], a[212:215], a[188:191], a[144:147]\n\t"
"ds_read_b128 a[188:191], v242 offset:11776\n\t"
"v_mfma_f32_16x16x32_bf16 a[144:147], a[216:219], a[192:195], a[144:147]\n\t"
"v_fma_f32 v192, v94, s54, -v192\n\t"
"v_mfma_f32_16x16x32_bf16 a[144:147], a[252:255], a[196:199], a[144:147]\n\t"
"v_mfma_f32_16x16x32_bf16 a[152:155], a[212:215], a[192:195], a[152:155]\n\t"
"ds_read_b128 a[192:195], v242 offset:12288\n\t"
"v_mfma_f32_16x16x32_bf16 a[152:155], a[216:219], a[196:199], a[152:155]\n\t"
"v_fma_f32 v193, v95, s54, -v193\n\t"
"v_mfma_f32_16x16x32_bf16 a[152:155], a[252:255], a[200:203], a[152:155]\n\t"
"v_mfma_f32_16x16x32_bf16 a[160:163], a[212:215], a[196:199], a[160:163]\n\t"
"ds_read_b128 a[196:199], v242 offset:12800\n\t"
"v_mfma_f32_16x16x32_bf16 a[160:163], a[216:219], a[200:203], a[160:163]\n\t"
"ds_read_b128 a[200:203], v242 offset:13312\n\t"
"v_mfma_f32_16x16x32_bf16 a[160:163], a[252:255], a[204:207], a[160:163]\n\t"
"s_mov_b32 s20, 442368\n\t"
"buffer_load_dwordx4 a[212:215], v252, s[8:11], s20 offen offset:0 sc1\n\t"
"s_waitcnt vmcnt(7) lgkmcnt(6)\n\t"
"v_mfma_f32_16x16x32_bf16 v[198:201], a[220:223], a[168:171], v[198:201]\n\t"
"ds_read_b128 a[204:207], v242 offset:13824\n\t"
"v_mfma_f32_16x16x32_bf16 v[198:201], a[224:227], a[172:175], v[198:201]\n\t"
"v_mfma_f32_16x16x32_bf16 v[198:201], a[228:231], a[176:179], v[198:201]\n\t"
"buffer_load_dwordx4 a[216:219], v252, s[8:11], s20 offen offset:1024 sc1\n\t"
"s_waitcnt lgkmcnt(6)\n\t"
"v_mfma_f32_16x16x32_bf16 v[206:209], a[220:223], a[172:175], v[206:209]\n\t"
"v_mfma_f32_16x16x32_bf16 v[206:209], a[224:227], a[176:179], v[206:209]\n\t"
"v_mfma_f32_16x16x32_bf16 v[206:209], a[228:231], a[180:183], v[206:209]\n\t"
"buffer_load_dwordx4 a[252:255], v252, s[8:11], s20 offen offset:2048 sc1\n\t"
"s_waitcnt lgkmcnt(0)\n\t"
"s_barrier\n\t"
"v_mfma_f32_16x16x32_bf16 v[214:217], a[220:223], a[176:179], v[214:217]\n\t"
"v_mfma_f32_16x16x32_bf16 v[214:217], a[224:227], a[180:183], v[214:217]\n\t"
"v_mfma_f32_16x16x32_bf16 v[214:217], a[228:231], a[184:187], v[214:217]\n\t"
"v_pk_fma_f32 v[194:195], v[198:199], s[56:57], v[194:195]\n\t"
"v_pk_fma_f32 v[198:199], v[194:195], s[58:59], v[198:199] neg_lo:[0,0,1] neg_hi:[0,0,1]\n\t"
"v_pk_fma_f32 v[226:227], v[0:1], s[60:61], v[194:195]\n\t"
"v_pk_fma_f32 v[0:1], v[194:195], s[60:61], v[0:1]\n\t"
"v_pk_fma_f32 v[194:195], v[98:99], s[62:63], v[198:199]\n\t"
"v_pk_fma_f32 v[98:99], v[198:199], s[62:63], v[98:99]\n\t"
"v_pk_fma_f32 v[196:197], v[200:201], s[56:57], v[196:197]\n\t"
"v_pk_fma_f32 v[200:201], v[196:197], s[58:59], v[200:201] neg_lo:[0,0,1] neg_hi:[0,0,1]\n\t"
"v_pk_fma_f32 v[232:233], v[2:3], s[60:61], v[196:197]\n\t"
"v_pk_fma_f32 v[2:3], v[196:197], s[60:61], v[2:3]\n\t"
"v_pk_fma_f32 v[196:197], v[100:101], s[62:63], v[200:201]\n\t"
"v_pk_fma_f32 v[100:101], v[200:201], s[62:63], v[100:101]\n\t"
"v_cvt_pk_bf16_f32 v194, v194, v195\n\t"
"v_cvt_pk_bf16_f32 v198, v226, v227\n\t"
"v_mfma_f32_16x16x32_bf16 a[4:7], a[220:223], a[180:183], a[4:7]\n\t"
"v_cvt_pk_bf16_f32 v195, v196, v197\n\t"
"v_mfma_f32_16x16x32_bf16 a[4:7], a[224:227], a[184:187], a[4:7]\n\t"
"v_cvt_pk_bf16_f32 v199, v232, v233\n\t"
"v_mfma_f32_16x16x32_bf16 a[4:7], a[228:231], a[188:191], a[4:7]\n\t"
"v_pk_fma_f32 v[202:203], v[206:207], s[56:57], v[202:203]\n\t"
"v_pk_fma_f32 v[206:207], v[202:203], s[58:59], v[206:207] neg_lo:[0,0,1] neg_hi:[0,0,1]\n\t"
"v_pk_fma_f32 v[250:251], v[4:5], s[60:61], v[202:203]\n\t"
"v_pk_fma_f32 v[4:5], v[202:203], s[60:61], v[4:5]\n\t"
"v_pk_fma_f32 v[202:203], v[102:103], s[62:63], v[206:207]\n\t"
"v_pk_fma_f32 v[102:103], v[206:207], s[62:63], v[102:103]\n\t"
"v_cvt_pk_bf16_f32 v202, v202, v203\n\t"
"v_mfma_f32_16x16x32_bf16 a[12:15], a[220:223], a[184:187], a[12:15]\n\t"
"v_cvt_pk_bf16_f32 v206, v250, v251\n\t"
"v_mfma_f32_16x16x32_bf16 a[12:15], a[224:227], a[188:191], a[12:15]\n\t"
"v_mfma_f32_16x16x32_bf16 a[12:15], a[228:231], a[192:195], a[12:15]\n\t"
"v_pk_fma_f32 v[204:205], v[208:209], s[56:57], v[204:205]\n\t"
"v_pk_fma_f32 v[208:209], v[204:205], s[58:59], v[208:209] neg_lo:[0,0,1] neg_hi:[0,0,1]\n\t"
"v_pk_fma_f32 v[226:227], v[6:7], s[60:61], v[204:205]\n\t"
"v_pk_fma_f32 v[6:7], v[204:205], s[60:61], v[6:7]\n\t"
"v_pk_fma_f32 v[204:205], v[104:105], s[62:63], v[208:209]\n\t"
"v_pk_fma_f32 v[104:105], v[208:209], s[62:63], v[104:105]\n\t"
"v_cvt_pk_bf16_f32 v203, v204, v205\n\t"
"v_cvt_pk_bf16_f32 v207, v226, v227\n\t"
"v_mfma_f32_16x16x32_bf16 a[20:23], a[220:223], a[188:191], a[20:23]\n\t"
"v_mfma_f32_16x16x32_bf16 a[20:23], a[224:227], a[192:195], a[20:23]\n\t"
"v_accvgpr_read_b32 v222, a0\n\t"
"v_mfma_f32_16x16x32_bf16 a[20:23], a[228:231], a[196:199], a[20:23]\n\t"
"v_pk_fma_f32 v[210:211], v[214:215], s[56:57], v[210:211]\n\t"
"v_pk_fma_f32 v[214:215], v[210:211], s[58:59], v[214:215] neg_lo:[0,0,1] neg_hi:[0,0,1]\n\t"
"v_pk_fma_f32 v[232:233], v[8:9], s[60:61], v[210:211]\n\t"
"v_pk_fma_f32 v[8:9], v[210:211], s[60:61], v[8:9]\n\t"
"v_pk_fma_f32 v[210:211], v[106:107], s[62:63], v[214:215]\n\t"
"v_pk_fma_f32 v[106:107], v[214:215], s[62:63], v[106:107]\n\t"
"v_cvt_pk_bf16_f32 v210, v210, v211\n\t"
"v_cvt_pk_bf16_f32 v214, v232, v233\n\t"
"v_mfma_f32_16x16x32_bf16 a[28:31], a[220:223], a[192:195], a[28:31]\n\t"
"v_accvgpr_read_b32 v224, a4\n\t"
"v_mfma_f32_16x16x32_bf16 a[28:31], a[224:227], a[196:199], a[28:31]\n\t"
"v_accvgpr_read_b32 v223, a1\n\t"
"v_accvgpr_read_b32 v225, a5\n\t"
"v_mfma_f32_16x16x32_bf16 a[28:31], a[228:231], a[200:203], a[28:31]\n\t"
"v_pk_fma_f32 v[212:213], v[216:217], s[56:57], v[212:213]\n\t"
"v_pk_fma_f32 v[216:217], v[212:213], s[58:59], v[216:217] neg_lo:[0,0,1] neg_hi:[0,0,1]\n\t"
"v_pk_fma_f32 v[250:251], v[10:11], s[60:61], v[212:213]\n\t"
"v_pk_fma_f32 v[10:11], v[212:213], s[60:61], v[10:11]\n\t"
"v_pk_fma_f32 v[212:213], v[108:109], s[62:63], v[216:217]\n\t"
"v_pk_fma_f32 v[108:109], v[216:217], s[62:63], v[108:109]\n\t"
"v_cvt_pk_bf16_f32 v211, v212, v213\n\t"
"v_cvt_pk_bf16_f32 v215, v250, v251\n\t"
"v_mfma_f32_16x16x32_bf16 a[36:39], a[220:223], a[196:199], a[36:39]\n\t"
"v_accvgpr_read_b32 v228, a2\n\t"
"v_accvgpr_read_b32 v230, a6\n\t"
"v_mfma_f32_16x16x32_bf16 a[36:39], a[224:227], a[200:203], a[36:39]\n\t"
"v_accvgpr_read_b32 v229, a3\n\t"
"v_accvgpr_read_b32 v231, a7\n\t"
"v_mfma_f32_16x16x32_bf16 a[36:39], a[228:231], a[204:207], a[36:39]\n\t"
"v_pk_fma_f32 v[222:223], v[224:225], s[56:57], v[222:223]\n\t"
"v_pk_fma_f32 v[224:225], v[222:223], s[58:59], v[224:225] neg_lo:[0,0,1] neg_hi:[0,0,1]\n\t"
"v_pk_fma_f32 v[226:227], v[12:13], s[60:61], v[222:223]\n\t"
"v_pk_fma_f32 v[12:13], v[222:223], s[60:61], v[12:13]\n\t"
"v_pk_fma_f32 v[222:223], v[110:111], s[62:63], v[224:225]\n\t"
"v_pk_fma_f32 v[110:111], v[224:225], s[62:63], v[110:111]\n\t"
"s_mov_b32 s20, 454656\n\t"
"buffer_load_dwordx4 a[220:223], v252, s[8:11], s20 offen offset:0 sc1\n\t"
"s_waitcnt vmcnt(7)\n\t"
"v_mfma_f32_16x16x32_bf16 a[44:47], a[232:235], a[168:171], a[44:47]\n\t"
"v_cvt_pk_bf16_f32 v222, v222, v223\n\t"
"v_cvt_pk_bf16_f32 v226, v226, v227\n\t"
"v_mfma_f32_16x16x32_bf16 a[44:47], a[236:239], a[172:175], a[44:47]\n\t"
"v_accvgpr_write_b32 a0, v222\n\t"
"v_accvgpr_write_b32 a4, v226\n\t"
"v_mfma_f32_16x16x32_bf16 a[44:47], a[240:243], a[176:179], a[44:47]\n\t"
"v_pk_fma_f32 v[228:229], v[230:231], s[56:57], v[228:229]\n\t"
"v_pk_fma_f32 v[230:231], v[228:229], s[58:59], v[230:231] neg_lo:[0,0,1] neg_hi:[0,0,1]\n\t"
"v_pk_fma_f32 v[232:233], v[14:15], s[60:61], v[228:229]\n\t"
"v_pk_fma_f32 v[14:15], v[228:229], s[60:61], v[14:15]\n\t"
"v_pk_fma_f32 v[228:229], v[112:113], s[62:63], v[230:231]\n\t"
"v_pk_fma_f32 v[112:113], v[230:231], s[62:63], v[112:113]\n\t"
"buffer_load_dwordx4 a[224:227], v252, s[8:11], s20 offen offset:1024 sc1\n\t"
"v_accvgpr_read_b32 v234, a8\n\t"
"v_accvgpr_read_b32 v236, a12\n\t"
"v_mfma_f32_16x16x32_bf16 a[52:55], a[232:235], a[172:175], a[52:55]\n\t"
"v_accvgpr_read_b32 v235, a9\n\t"
"v_accvgpr_read_b32 v237, a13\n\t"
"v_mfma_f32_16x16x32_bf16 a[52:55], a[236:239], a[176:179], a[52:55]\n\t"
"v_cvt_pk_bf16_f32 v228, v228, v229\n\t"
"v_cvt_pk_bf16_f32 v232, v232, v233\n\t"
"v_mfma_f32_16x16x32_bf16 a[52:55], a[240:243], a[180:183], a[52:55]\n\t"
"v_pk_fma_f32 v[234:235], v[236:237], s[56:57], v[234:235]\n\t"
"v_pk_fma_f32 v[236:237], v[234:235], s[58:59], v[236:237] neg_lo:[0,0,1] neg_hi:[0,0,1]\n\t"
"v_pk_fma_f32 v[250:251], v[16:17], s[60:61], v[234:235]\n\t"
"v_pk_fma_f32 v[16:17], v[234:235], s[60:61], v[16:17]\n\t"
"v_pk_fma_f32 v[234:235], v[114:115], s[62:63], v[236:237]\n\t"
"v_pk_fma_f32 v[114:115], v[236:237], s[62:63], v[114:115]\n\t"
"buffer_load_dwordx4 a[228:231], v252, s[8:11], s20 offen offset:2048 sc1\n\t"
"v_accvgpr_write_b32 a1, v228\n\t"
"v_accvgpr_write_b32 a5, v232\n\t"
"v_mfma_f32_16x16x32_bf16 a[60:63], a[232:235], a[176:179], a[60:63]\n\t"
"v_accvgpr_read_b32 v222, a10\n\t"
"v_accvgpr_read_b32 v224, a14\n\t"
"v_mfma_f32_16x16x32_bf16 a[60:63], a[236:239], a[180:183], a[60:63]\n\t"
"v_accvgpr_read_b32 v223, a11\n\t"
"v_accvgpr_read_b32 v225, a15\n\t"
"v_mfma_f32_16x16x32_bf16 a[60:63], a[240:243], a[184:187], a[60:63]\n\t"
"v_accvgpr_read_b32 v228, a16\n\t"
"v_accvgpr_read_b32 v230, a20\n\t"
"v_accvgpr_read_b32 v229, a17\n\t"
"v_accvgpr_read_b32 v231, a21\n\t"
"v_cvt_pk_bf16_f32 v234, v234, v235\n\t"
"v_cvt_pk_bf16_f32 v250, v250, v251\n\t"
"v_pk_fma_f32 v[222:223], v[224:225], s[56:57], v[222:223]\n\t"
"v_pk_fma_f32 v[224:225], v[222:223], s[58:59], v[224:225] neg_lo:[0,0,1] neg_hi:[0,0,1]\n\t"
"v_pk_fma_f32 v[226:227], v[18:19], s[60:61], v[222:223]\n\t"
"v_pk_fma_f32 v[18:19], v[222:223], s[60:61], v[18:19]\n\t"
"v_pk_fma_f32 v[222:223], v[116:117], s[62:63], v[224:225]\n\t"
"v_pk_fma_f32 v[116:117], v[224:225], s[62:63], v[116:117]\n\t"
"v_pk_fma_f32 v[228:229], v[230:231], s[56:57], v[228:229]\n\t"
"v_pk_fma_f32 v[230:231], v[228:229], s[58:59], v[230:231] neg_lo:[0,0,1] neg_hi:[0,0,1]\n\t"
"v_pk_fma_f32 v[232:233], v[20:21], s[60:61], v[228:229]\n\t"
"v_pk_fma_f32 v[20:21], v[228:229], s[60:61], v[20:21]\n\t"
"v_pk_fma_f32 v[228:229], v[118:119], s[62:63], v[230:231]\n\t"
"v_pk_fma_f32 v[118:119], v[230:231], s[62:63], v[118:119]\n\t"
"v_mfma_f32_16x16x32_bf16 a[68:71], a[232:235], a[180:183], a[68:71]\n\t"
"v_accvgpr_write_b32 a8, v234\n\t"
"v_accvgpr_write_b32 a12, v250\n\t"
"v_mfma_f32_16x16x32_bf16 a[68:71], a[236:239], a[184:187], a[68:71]\n\t"
"v_accvgpr_read_b32 v234, a18\n\t"
"v_accvgpr_read_b32 v236, a22\n\t"
"v_mfma_f32_16x16x32_bf16 a[68:71], a[240:243], a[188:191], a[68:71]\n\t"
"v_accvgpr_read_b32 v235, a19\n\t"
"v_accvgpr_read_b32 v237, a23\n\t"
"v_cvt_pk_bf16_f32 v222, v222, v223\n\t"
"v_cvt_pk_bf16_f32 v226, v226, v227\n\t"
"v_cvt_pk_bf16_f32 v228, v228, v229\n\t"
"v_cvt_pk_bf16_f32 v232, v232, v233\n\t"
"v_pk_fma_f32 v[234:235], v[236:237], s[56:57], v[234:235]\n\t"
"v_pk_fma_f32 v[236:237], v[234:235], s[58:59], v[236:237] neg_lo:[0,0,1] neg_hi:[0,0,1]\n\t"
"v_pk_fma_f32 v[250:251], v[22:23], s[60:61], v[234:235]\n\t"
"v_pk_fma_f32 v[22:23], v[234:235], s[60:61], v[22:23]\n\t"
"v_pk_fma_f32 v[234:235], v[120:121], s[62:63], v[236:237]\n\t"
"v_pk_fma_f32 v[120:121], v[236:237], s[62:63], v[120:121]\n\t"
"v_mfma_f32_16x16x32_bf16 a[76:79], a[232:235], a[184:187], a[76:79]\n\t"
"v_accvgpr_write_b32 a9, v222\n\t"
"v_accvgpr_write_b32 a13, v226\n\t"
"v_mfma_f32_16x16x32_bf16 a[76:79], a[236:239], a[188:191], a[76:79]\n\t"
"v_accvgpr_write_b32 a16, v228\n\t"
"v_accvgpr_write_b32 a20, v232\n\t"
"v_mfma_f32_16x16x32_bf16 a[76:79], a[240:243], a[192:195], a[76:79]\n\t"
"v_accvgpr_read_b32 v222, a24\n\t"
"v_accvgpr_read_b32 v224, a28\n\t"
"v_accvgpr_read_b32 v223, a25\n\t"
"v_accvgpr_read_b32 v225, a29\n\t"
"v_cvt_pk_bf16_f32 v234, v234, v235\n\t"
"v_cvt_pk_bf16_f32 v250, v250, v251\n\t"
"v_pk_fma_f32 v[222:223], v[224:225], s[56:57], v[222:223]\n\t"
"v_pk_fma_f32 v[224:225], v[222:223], s[58:59], v[224:225] neg_lo:[0,0,1] neg_hi:[0,0,1]\n\t"
"v_pk_fma_f32 v[226:227], v[24:25], s[60:61], v[222:223]\n\t"
"v_pk_fma_f32 v[24:25], v[222:223], s[60:61], v[24:25]\n\t"
"v_pk_fma_f32 v[222:223], v[122:123], s[62:63], v[224:225]\n\t"
"v_pk_fma_f32 v[122:123], v[224:225], s[62:63], v[122:123]\n\t"
"v_mfma_f32_16x16x32_bf16 a[84:87], a[232:235], a[188:191], a[84:87]\n\t"
"v_accvgpr_write_b32 a17, v234\n\t"
"v_accvgpr_write_b32 a21, v250\n\t"
"v_mfma_f32_16x16x32_bf16 a[84:87], a[236:239], a[192:195], a[84:87]\n\t"
"v_accvgpr_read_b32 v228, a26\n\t"
"v_accvgpr_read_b32 v230, a30\n\t"
"v_mfma_f32_16x16x32_bf16 a[84:87], a[240:243], a[196:199], a[84:87]\n\t"
"v_accvgpr_read_b32 v229, a27\n\t"
"v_accvgpr_read_b32 v231, a31\n\t"
"v_cvt_pk_bf16_f32 v222, v222, v223\n\t"
"v_cvt_pk_bf16_f32 v226, v226, v227\n\t"
"v_pk_fma_f32 v[228:229], v[230:231], s[56:57], v[228:229]\n\t"
"v_pk_fma_f32 v[230:231], v[228:229], s[58:59], v[230:231] neg_lo:[0,0,1] neg_hi:[0,0,1]\n\t"
"v_pk_fma_f32 v[232:233], v[26:27], s[60:61], v[228:229]\n\t"
"v_pk_fma_f32 v[26:27], v[228:229], s[60:61], v[26:27]\n\t"
"v_pk_fma_f32 v[228:229], v[124:125], s[62:63], v[230:231]\n\t"
"v_pk_fma_f32 v[124:125], v[230:231], s[62:63], v[124:125]\n\t"
"v_mfma_f32_16x16x32_bf16 a[92:95], a[232:235], a[192:195], a[92:95]\n\t"
"v_accvgpr_write_b32 a24, v222\n\t"
"v_accvgpr_write_b32 a28, v226\n\t"
"v_mfma_f32_16x16x32_bf16 a[92:95], a[236:239], a[196:199], a[92:95]\n\t"
"v_accvgpr_read_b32 v234, a32\n\t"
"v_accvgpr_read_b32 v236, a36\n\t"
"v_mfma_f32_16x16x32_bf16 a[92:95], a[240:243], a[200:203], a[92:95]\n\t"
"v_accvgpr_read_b32 v235, a33\n\t"
"v_accvgpr_read_b32 v237, a37\n\t"
"v_cvt_pk_bf16_f32 v228, v228, v229\n\t"
"v_cvt_pk_bf16_f32 v232, v232, v233\n\t"
"v_pk_fma_f32 v[234:235], v[236:237], s[56:57], v[234:235]\n\t"
"v_pk_fma_f32 v[236:237], v[234:235], s[58:59], v[236:237] neg_lo:[0,0,1] neg_hi:[0,0,1]\n\t"
"v_pk_fma_f32 v[250:251], v[28:29], s[60:61], v[234:235]\n\t"
"v_pk_fma_f32 v[28:29], v[234:235], s[60:61], v[28:29]\n\t"
"v_pk_fma_f32 v[234:235], v[126:127], s[62:63], v[236:237]\n\t"
"v_pk_fma_f32 v[126:127], v[236:237], s[62:63], v[126:127]\n\t"
"v_mfma_f32_16x16x32_bf16 a[100:103], a[232:235], a[196:199], a[100:103]\n\t"
"v_accvgpr_write_b32 a25, v228\n\t"
"v_accvgpr_write_b32 a29, v232\n\t"
"v_mfma_f32_16x16x32_bf16 a[100:103], a[236:239], a[200:203], a[100:103]\n\t"
"v_accvgpr_read_b32 v222, a34\n\t"
"v_accvgpr_read_b32 v224, a38\n\t"
"v_mfma_f32_16x16x32_bf16 a[100:103], a[240:243], a[204:207], a[100:103]\n\t"
"v_accvgpr_read_b32 v223, a35\n\t"
"v_accvgpr_read_b32 v225, a39\n\t"
"v_cvt_pk_bf16_f32 v234, v234, v235\n\t"
"v_cvt_pk_bf16_f32 v250, v250, v251\n\t"
"v_pk_fma_f32 v[222:223], v[224:225], s[56:57], v[222:223]\n\t"
"v_pk_fma_f32 v[224:225], v[222:223], s[58:59], v[224:225] neg_lo:[0,0,1] neg_hi:[0,0,1]\n\t"
"v_pk_fma_f32 v[226:227], v[30:31], s[60:61], v[222:223]\n\t"
"v_pk_fma_f32 v[30:31], v[222:223], s[60:61], v[30:31]\n\t"
"v_pk_fma_f32 v[222:223], v[128:129], s[62:63], v[224:225]\n\t"
"v_pk_fma_f32 v[128:129], v[224:225], s[62:63], v[128:129]\n\t"
"s_waitcnt vmcnt(6)\n\t"
"v_mfma_f32_16x16x32_bf16 a[108:111], a[244:247], a[168:171], a[108:111]\n\t"
"v_accvgpr_write_b32 a32, v234\n\t"
"v_accvgpr_write_b32 a36, v250\n\t"
"v_mfma_f32_16x16x32_bf16 a[108:111], a[208:211], a[172:175], a[108:111]\n\t"
"v_accvgpr_read_b32 v228, a40\n\t"
"v_accvgpr_read_b32 v230, a44\n\t"
"v_mfma_f32_16x16x32_bf16 a[108:111], a[248:251], a[176:179], a[108:111]\n\t"
"s_mov_b32 s20, 466944\n\t"
"buffer_load_dwordx4 a[232:235], v252, s[8:11], s20 offen offset:0 sc1\n\t"
"v_accvgpr_read_b32 v229, a41\n\t"
"v_accvgpr_read_b32 v231, a45\n\t"
"v_cvt_pk_bf16_f32 v222, v222, v223\n\t"
"v_cvt_pk_bf16_f32 v226, v226, v227\n\t"
"v_pk_fma_f32 v[228:229], v[230:231], s[56:57], v[228:229]\n\t"
"v_pk_fma_f32 v[230:231], v[228:229], s[58:59], v[230:231] neg_lo:[0,0,1] neg_hi:[0,0,1]\n\t"
"v_pk_fma_f32 v[232:233], v[32:33], s[60:61], v[228:229]\n\t"
"v_pk_fma_f32 v[32:33], v[228:229], s[60:61], v[32:33]\n\t"
"v_pk_fma_f32 v[228:229], v[130:131], s[62:63], v[230:231]\n\t"
"v_pk_fma_f32 v[130:131], v[230:231], s[62:63], v[130:131]\n\t"
"v_mfma_f32_16x16x32_bf16 a[116:119], a[244:247], a[172:175], a[116:119]\n\t"
"v_accvgpr_write_b32 a33, v222\n\t"
"v_accvgpr_write_b32 a37, v226\n\t"
"v_mfma_f32_16x16x32_bf16 a[116:119], a[208:211], a[176:179], a[116:119]\n\t"
"v_accvgpr_read_b32 v234, a42\n\t"
"v_accvgpr_read_b32 v236, a46\n\t"
"v_mfma_f32_16x16x32_bf16 a[116:119], a[248:251], a[180:183], a[116:119]\n\t"
"buffer_load_dwordx4 a[236:239], v252, s[8:11], s20 offen offset:1024 sc1\n\t"
"v_accvgpr_read_b32 v235, a43\n\t"
"v_accvgpr_read_b32 v237, a47\n\t"
"v_pk_fma_f32 v[234:235], v[236:237], s[56:57], v[234:235]\n\t"
"v_pk_fma_f32 v[236:237], v[234:235], s[58:59], v[236:237] neg_lo:[0,0,1] neg_hi:[0,0,1]\n\t"
"v_pk_fma_f32 v[250:251], v[34:35], s[60:61], v[234:235]\n\t"
"v_pk_fma_f32 v[34:35], v[234:235], s[60:61], v[34:35]\n\t"
"v_pk_fma_f32 v[234:235], v[132:133], s[62:63], v[236:237]\n\t"
"v_pk_fma_f32 v[132:133], v[236:237], s[62:63], v[132:133]\n\t"
"v_mfma_f32_16x16x32_bf16 a[124:127], a[244:247], a[176:179], a[124:127]\n\t"
"v_cvt_pk_bf16_f32 v196, v228, v229\n\t"
"v_cvt_pk_bf16_f32 v200, v232, v233\n\t"
"v_mfma_f32_16x16x32_bf16 a[124:127], a[208:211], a[180:183], a[124:127]\n\t"
"v_accvgpr_read_b32 v222, a48\n\t"
"v_accvgpr_read_b32 v224, a52\n\t"
"v_mfma_f32_16x16x32_bf16 a[124:127], a[248:251], a[184:187], a[124:127]\n\t"
"buffer_load_dwordx4 a[240:243], v252, s[8:11], s20 offen offset:2048 sc1\n\t"
"v_accvgpr_read_b32 v223, a49\n\t"
"v_accvgpr_read_b32 v225, a53\n\t"
"v_accvgpr_read_b32 v228, a50\n\t"
"v_accvgpr_read_b32 v230, a54\n\t"
"v_accvgpr_read_b32 v229, a51\n\t"
"v_accvgpr_read_b32 v231, a55\n\t"
"v_pk_fma_f32 v[222:223], v[224:225], s[56:57], v[222:223]\n\t"
"v_pk_fma_f32 v[224:225], v[222:223], s[58:59], v[224:225] neg_lo:[0,0,1] neg_hi:[0,0,1]\n\t"
"v_pk_fma_f32 v[226:227], v[36:37], s[60:61], v[222:223]\n\t"
"v_pk_fma_f32 v[36:37], v[222:223], s[60:61], v[36:37]\n\t"
"v_pk_fma_f32 v[222:223], v[134:135], s[62:63], v[224:225]\n\t"
"v_pk_fma_f32 v[134:135], v[224:225], s[62:63], v[134:135]\n\t"
"v_pk_fma_f32 v[228:229], v[230:231], s[56:57], v[228:229]\n\t"
"v_pk_fma_f32 v[230:231], v[228:229], s[58:59], v[230:231] neg_lo:[0,0,1] neg_hi:[0,0,1]\n\t"
"v_pk_fma_f32 v[232:233], v[38:39], s[60:61], v[228:229]\n\t"
"v_pk_fma_f32 v[38:39], v[228:229], s[60:61], v[38:39]\n\t"
"v_pk_fma_f32 v[228:229], v[136:137], s[62:63], v[230:231]\n\t"
"v_pk_fma_f32 v[136:137], v[230:231], s[62:63], v[136:137]\n\t"
"v_mfma_f32_16x16x32_bf16 a[132:135], a[244:247], a[180:183], a[132:135]\n\t"
"v_cvt_pk_bf16_f32 v197, v234, v235\n\t"
"v_cvt_pk_bf16_f32 v201, v250, v251\n\t"
"v_mfma_f32_16x16x32_bf16 a[132:135], a[208:211], a[184:187], a[132:135]\n\t"
"ds_read_b128 a[168:171], v242 offset:18432\n\t"
"v_accvgpr_read_b32 v234, a56\n\t"
"v_mfma_f32_16x16x32_bf16 a[132:135], a[248:251], a[188:191], a[132:135]\n\t"
"ds_read_b128 a[172:175], v242 offset:18944\n\t"
"ds_read_b128 a[176:179], v242 offset:19456\n\t"
"v_accvgpr_read_b32 v236, a60\n\t"
"v_accvgpr_read_b32 v235, a57\n\t"
"v_accvgpr_read_b32 v237, a61\n\t"
"s_mov_b32 s20, 626688\n\t"
"buffer_load_dwordx4 a[40:43], v252, s[8:11], s20 offen offset:0 sc1\n\t"
"v_pk_fma_f32 v[234:235], v[236:237], s[56:57], v[234:235]\n\t"
"v_pk_fma_f32 v[236:237], v[234:235], s[58:59], v[236:237] neg_lo:[0,0,1] neg_hi:[0,0,1]\n\t"
"v_pk_fma_f32 v[250:251], v[40:41], s[60:61], v[234:235]\n\t"
"v_pk_fma_f32 v[40:41], v[234:235], s[60:61], v[40:41]\n\t"
"v_pk_fma_f32 v[234:235], v[138:139], s[62:63], v[236:237]\n\t"
"v_pk_fma_f32 v[138:139], v[236:237], s[62:63], v[138:139]\n\t"
"v_mfma_f32_16x16x32_bf16 a[140:143], a[244:247], a[184:187], a[140:143]\n\t"
"v_cvt_pk_bf16_f32 v204, v222, v223\n\t"
"v_cvt_pk_bf16_f32 v208, v226, v227\n\t"
"v_mfma_f32_16x16x32_bf16 a[140:143], a[208:211], a[188:191], a[140:143]\n\t"
"v_cvt_pk_bf16_f32 v205, v228, v229\n\t"
"v_cvt_pk_bf16_f32 v209, v232, v233\n\t"
"v_mfma_f32_16x16x32_bf16 a[140:143], a[248:251], a[192:195], a[140:143]\n\t"
"ds_read_b128 a[180:183], v242 offset:19968\n\t"
"v_accvgpr_read_b32 v222, a58\n\t"
"v_accvgpr_read_b32 v224, a62\n\t"
"v_accvgpr_read_b32 v223, a59\n\t"
"v_accvgpr_read_b32 v225, a63\n\t"
"buffer_load_dwordx4 a[44:47], v252, s[8:11], s20 offen offset:1024 sc1\n\t"
"v_pk_fma_f32 v[222:223], v[224:225], s[56:57], v[222:223]\n\t"
"v_pk_fma_f32 v[224:225], v[222:223], s[58:59], v[224:225] neg_lo:[0,0,1] neg_hi:[0,0,1]\n\t"
"v_pk_fma_f32 v[226:227], v[42:43], s[60:61], v[222:223]\n\t"
"v_pk_fma_f32 v[42:43], v[222:223], s[60:61], v[42:43]\n\t"
"v_pk_fma_f32 v[222:223], v[140:141], s[62:63], v[224:225]\n\t"
"v_pk_fma_f32 v[140:141], v[224:225], s[62:63], v[140:141]\n\t"
"v_mfma_f32_16x16x32_bf16 a[148:151], a[244:247], a[188:191], a[148:151]\n\t"
"v_cvt_pk_bf16_f32 v212, v234, v235\n\t"
"v_cvt_pk_bf16_f32 v216, v250, v251\n\t"
"v_mfma_f32_16x16x32_bf16 a[148:151], a[208:211], a[192:195], a[148:151]\n\t"
"ds_read_b128 a[184:187], v242 offset:20480\n\t"
"v_accvgpr_read_b32 v228, a64\n\t"
"v_mfma_f32_16x16x32_bf16 a[148:151], a[248:251], a[196:199], a[148:151]\n\t"
"v_accvgpr_read_b32 v230, a68\n\t"
"v_accvgpr_read_b32 v229, a65\n\t"
"v_accvgpr_read_b32 v231, a69\n\t"
"buffer_load_dwordx4 a[48:51], v252, s[8:11], s20 offen offset:2048 sc1\n\t"
"v_pk_fma_f32 v[228:229], v[230:231], s[56:57], v[228:229]\n\t"
"v_pk_fma_f32 v[230:231], v[228:229], s[58:59], v[230:231] neg_lo:[0,0,1] neg_hi:[0,0,1]\n\t"
"v_pk_fma_f32 v[232:233], v[44:45], s[60:61], v[228:229]\n\t"
"v_pk_fma_f32 v[44:45], v[228:229], s[60:61], v[44:45]\n\t"
"v_pk_fma_f32 v[228:229], v[142:143], s[62:63], v[230:231]\n\t"
"v_pk_fma_f32 v[142:143], v[230:231], s[62:63], v[142:143]\n\t"
"v_mfma_f32_16x16x32_bf16 a[156:159], a[244:247], a[192:195], a[156:159]\n\t"
"v_cvt_pk_bf16_f32 v213, v222, v223\n\t"
"v_cvt_pk_bf16_f32 v217, v226, v227\n\t"
"v_mfma_f32_16x16x32_bf16 a[156:159], a[208:211], a[196:199], a[156:159]\n\t"
"ds_read_b128 a[188:191], v242 offset:20992\n\t"
"v_accvgpr_read_b32 v234, a66\n\t"
"v_mfma_f32_16x16x32_bf16 a[156:159], a[248:251], a[200:203], a[156:159]\n\t"
"v_accvgpr_read_b32 v236, a70\n\t"
"v_accvgpr_read_b32 v235, a67\n\t"
"v_accvgpr_read_b32 v237, a71\n\t"
"s_mov_b32 s20, 638976\n\t"
"buffer_load_dwordx4 a[56:59], v252, s[8:11], s20 offen offset:0 sc1\n\t"
"v_cvt_pk_bf16_f32 v228, v228, v229\n\t"
"v_cvt_pk_bf16_f32 v232, v232, v233\n\t"
"v_pk_fma_f32 v[234:235], v[236:237], s[56:57], v[234:235]\n\t"
"v_pk_fma_f32 v[236:237], v[234:235], s[58:59], v[236:237] neg_lo:[0,0,1] neg_hi:[0,0,1]\n\t"
"v_pk_fma_f32 v[250:251], v[46:47], s[60:61], v[234:235]\n\t"
"v_pk_fma_f32 v[46:47], v[234:235], s[60:61], v[46:47]\n\t"
"v_pk_fma_f32 v[234:235], v[144:145], s[62:63], v[236:237]\n\t"
"v_pk_fma_f32 v[144:145], v[236:237], s[62:63], v[144:145]\n\t"
"v_mfma_f32_16x16x32_bf16 a[164:167], a[244:247], a[196:199], a[164:167]\n\t"
"ds_read_b128 a[192:195], v242 offset:21504\n\t"
"v_accvgpr_write_b32 a2, v228\n\t"
"v_mfma_f32_16x16x32_bf16 a[164:167], a[208:211], a[200:203], a[164:167]\n\t"
"v_accvgpr_write_b32 a6, v232\n\t"
"v_accvgpr_read_b32 v222, a72\n\t"
"v_mfma_f32_16x16x32_bf16 a[164:167], a[248:251], a[204:207], a[164:167]\n\t"
"v_accvgpr_read_b32 v224, a76\n\t"
"v_accvgpr_read_b32 v223, a73\n\t"
"v_accvgpr_read_b32 v225, a77\n\t"
"buffer_load_dwordx4 a[60:63], v252, s[8:11], s20 offen offset:1024 sc1\n\t"
"v_cvt_pk_bf16_f32 v234, v234, v235\n\t"
"v_cvt_pk_bf16_f32 v250, v250, v251\n\t"
"v_pk_fma_f32 v[222:223], v[224:225], s[56:57], v[222:223]\n\t"
"v_pk_fma_f32 v[224:225], v[222:223], s[58:59], v[224:225] neg_lo:[0,0,1] neg_hi:[0,0,1]\n\t"
"v_pk_fma_f32 v[226:227], v[48:49], s[60:61], v[222:223]\n\t"
"v_pk_fma_f32 v[48:49], v[222:223], s[60:61], v[48:49]\n\t"
"v_pk_fma_f32 v[222:223], v[146:147], s[62:63], v[224:225]\n\t"
"v_pk_fma_f32 v[146:147], v[224:225], s[62:63], v[146:147]\n\t"
"s_waitcnt vmcnt(11) lgkmcnt(4)\n\t"
"v_mfma_f32_16x16x32_bf16 v[0:3], a[212:215], a[168:171], v[0:3]\n\t"
"ds_read_b128 a[196:199], v242 offset:22016\n\t"
"v_accvgpr_write_b32 a3, v234\n\t"
"v_mfma_f32_16x16x32_bf16 v[0:3], a[216:219], a[172:175], v[0:3]\n\t"
"v_accvgpr_write_b32 a7, v250\n\t"
"v_accvgpr_read_b32 v228, a74\n\t"
"v_mfma_f32_16x16x32_bf16 v[0:3], a[252:255], a[176:179], v[0:3]\n\t"
"v_accvgpr_read_b32 v230, a78\n\t"
"v_accvgpr_read_b32 v229, a75\n\t"
"v_accvgpr_read_b32 v231, a79\n\t"
"buffer_load_dwordx4 a[208:211], v252, s[8:11], s20 offen offset:2048 sc1\n\t"
"v_cvt_pk_bf16_f32 v222, v222, v223\n\t"
"v_cvt_pk_bf16_f32 v226, v226, v227\n\t"
"v_pk_fma_f32 v[228:229], v[230:231], s[56:57], v[228:229]\n\t"
"v_pk_fma_f32 v[230:231], v[228:229], s[58:59], v[230:231] neg_lo:[0,0,1] neg_hi:[0,0,1]\n\t"
"v_pk_fma_f32 v[232:233], v[50:51], s[60:61], v[228:229]\n\t"
"v_pk_fma_f32 v[50:51], v[228:229], s[60:61], v[50:51]\n\t"
"v_pk_fma_f32 v[228:229], v[148:149], s[62:63], v[230:231]\n\t"
"v_pk_fma_f32 v[148:149], v[230:231], s[62:63], v[148:149]\n\t"
"s_waitcnt lgkmcnt(4)\n\t"
"v_mfma_f32_16x16x32_bf16 v[4:7], a[212:215], a[172:175], v[4:7]\n\t"
"ds_read_b128 a[200:203], v242 offset:22528\n\t"
"v_accvgpr_write_b32 a10, v222\n\t"
"v_mfma_f32_16x16x32_bf16 v[4:7], a[216:219], a[176:179], v[4:7]\n\t"
"v_accvgpr_write_b32 a14, v226\n\t"
"v_accvgpr_read_b32 v234, a80\n\t"
"v_mfma_f32_16x16x32_bf16 v[4:7], a[252:255], a[180:183], v[4:7]\n\t"
"v_accvgpr_read_b32 v236, a84\n\t"
"v_accvgpr_read_b32 v235, a81\n\t"
"v_accvgpr_read_b32 v237, a85\n\t"
"s_mov_b32 s20, 651264\n\t"
"buffer_load_dwordx4 a[244:247], v252, s[8:11], s20 offen offset:0 sc1\n\t"
"v_cvt_pk_bf16_f32 v228, v228, v229\n\t"
"v_cvt_pk_bf16_f32 v232, v232, v233\n\t"
"v_pk_fma_f32 v[234:235], v[236:237], s[56:57], v[234:235]\n\t"
"v_pk_fma_f32 v[236:237], v[234:235], s[58:59], v[236:237] neg_lo:[0,0,1] neg_hi:[0,0,1]\n\t"
"v_pk_fma_f32 v[250:251], v[52:53], s[60:61], v[234:235]\n\t"
"v_pk_fma_f32 v[52:53], v[234:235], s[60:61], v[52:53]\n\t"
"v_pk_fma_f32 v[234:235], v[150:151], s[62:63], v[236:237]\n\t"
"v_pk_fma_f32 v[150:151], v[236:237], s[62:63], v[150:151]\n\t"
"s_waitcnt lgkmcnt(4)\n\t"
"v_mfma_f32_16x16x32_bf16 v[8:11], a[212:215], a[176:179], v[8:11]\n\t"
"ds_read_b128 a[204:207], v242 offset:23040\n\t"
"v_accvgpr_write_b32 a11, v228\n\t"
"v_mfma_f32_16x16x32_bf16 v[8:11], a[216:219], a[180:183], v[8:11]\n\t"
"v_accvgpr_write_b32 a15, v232\n\t"
"v_accvgpr_read_b32 v222, a82\n\t"
"v_mfma_f32_16x16x32_bf16 v[8:11], a[252:255], a[184:187], v[8:11]\n\t"
"v_accvgpr_read_b32 v224, a86\n\t"
"v_accvgpr_read_b32 v223, a83\n\t"
"v_accvgpr_read_b32 v225, a87\n\t"
"buffer_load_dwordx4 a[248:251], v252, s[8:11], s20 offen offset:1024 sc1\n\t"
"v_cvt_pk_bf16_f32 v234, v234, v235\n\t"
"v_cvt_pk_bf16_f32 v250, v250, v251\n\t"
"v_pk_fma_f32 v[222:223], v[224:225], s[56:57], v[222:223]\n\t"
"v_pk_fma_f32 v[224:225], v[222:223], s[58:59], v[224:225] neg_lo:[0,0,1] neg_hi:[0,0,1]\n\t"
"v_pk_fma_f32 v[226:227], v[54:55], s[60:61], v[222:223]\n\t"
"v_pk_fma_f32 v[54:55], v[222:223], s[60:61], v[54:55]\n\t"
"v_pk_fma_f32 v[222:223], v[152:153], s[62:63], v[224:225]\n\t"
"v_pk_fma_f32 v[152:153], v[224:225], s[62:63], v[152:153]\n\t"
"s_waitcnt lgkmcnt(4)\n\t"
"v_mfma_f32_16x16x32_bf16 v[12:15], a[212:215], a[180:183], v[12:15]\n\t"
"v_accvgpr_write_b32 a18, v234\n\t"
"v_accvgpr_write_b32 a22, v250\n\t"
"v_mfma_f32_16x16x32_bf16 v[12:15], a[216:219], a[184:187], v[12:15]\n\t"
"v_accvgpr_read_b32 v228, a88\n\t"
"v_accvgpr_read_b32 v230, a92\n\t"
"v_mfma_f32_16x16x32_bf16 v[12:15], a[252:255], a[188:191], v[12:15]\n\t"
"v_accvgpr_read_b32 v229, a89\n\t"
"v_accvgpr_read_b32 v231, a93\n\t"
"v_accvgpr_read_b32 v234, a90\n\t"
"v_accvgpr_read_b32 v236, a94\n\t"
"v_accvgpr_read_b32 v235, a91\n\t"
"v_accvgpr_read_b32 v237, a95\n\t"
"buffer_load_dwordx4 a[64:67], v252, s[8:11], s20 offen offset:2048 sc1\n\t"
"v_cvt_pk_bf16_f32 v222, v222, v223\n\t"
"v_cvt_pk_bf16_f32 v226, v226, v227\n\t"
"v_pk_fma_f32 v[228:229], v[230:231], s[56:57], v[228:229]\n\t"
"v_pk_fma_f32 v[230:231], v[228:229], s[58:59], v[230:231] neg_lo:[0,0,1] neg_hi:[0,0,1]\n\t"
"v_pk_fma_f32 v[232:233], v[56:57], s[60:61], v[228:229]\n\t"
"v_pk_fma_f32 v[56:57], v[228:229], s[60:61], v[56:57]\n\t"
"v_pk_fma_f32 v[228:229], v[154:155], s[62:63], v[230:231]\n\t"
"v_pk_fma_f32 v[154:155], v[230:231], s[62:63], v[154:155]\n\t"
"v_pk_fma_f32 v[234:235], v[236:237], s[56:57], v[234:235]\n\t"
"v_pk_fma_f32 v[236:237], v[234:235], s[58:59], v[236:237] neg_lo:[0,0,1] neg_hi:[0,0,1]\n\t"
"v_pk_fma_f32 v[250:251], v[58:59], s[60:61], v[234:235]\n\t"
"v_pk_fma_f32 v[58:59], v[234:235], s[60:61], v[58:59]\n\t"
"v_pk_fma_f32 v[234:235], v[156:157], s[62:63], v[236:237]\n\t"
"v_pk_fma_f32 v[156:157], v[236:237], s[62:63], v[156:157]\n\t"
"s_waitcnt lgkmcnt(3)\n\t"
"v_mfma_f32_16x16x32_bf16 v[16:19], a[212:215], a[184:187], v[16:19]\n\t"
"v_accvgpr_write_b32 a19, v222\n\t"
"v_accvgpr_write_b32 a23, v226\n\t"
"v_mfma_f32_16x16x32_bf16 v[16:19], a[216:219], a[188:191], v[16:19]\n\t"
"v_accvgpr_read_b32 v222, a96\n\t"
"v_accvgpr_read_b32 v224, a100\n\t"
"v_mfma_f32_16x16x32_bf16 v[16:19], a[252:255], a[192:195], v[16:19]\n\t"
"v_accvgpr_read_b32 v223, a97\n\t"
"v_accvgpr_read_b32 v225, a101\n\t"
"s_mov_b32 s20, 663552\n\t"
"buffer_load_dwordx4 a[68:71], v252, s[8:11], s20 offen offset:0 sc1\n\t"
"v_cvt_pk_bf16_f32 v228, v228, v229\n\t"
"v_cvt_pk_bf16_f32 v232, v232, v233\n\t"
"v_pk_fma_f32 v[222:223], v[224:225], s[56:57], v[222:223]\n\t"
"v_pk_fma_f32 v[224:225], v[222:223], s[58:59], v[224:225] neg_lo:[0,0,1] neg_hi:[0,0,1]\n\t"
"v_pk_fma_f32 v[226:227], v[60:61], s[60:61], v[222:223]\n\t"
"v_pk_fma_f32 v[60:61], v[222:223], s[60:61], v[60:61]\n\t"
"v_pk_fma_f32 v[222:223], v[158:159], s[62:63], v[224:225]\n\t"
"v_pk_fma_f32 v[158:159], v[224:225], s[62:63], v[158:159]\n\t"
"s_waitcnt lgkmcnt(2)\n\t"
"v_mfma_f32_16x16x32_bf16 v[20:23], a[212:215], a[188:191], v[20:23]\n\t"
"v_accvgpr_write_b32 a26, v228\n\t"
"v_cvt_pk_bf16_f32 v234, v234, v235\n\t"
"v_mfma_f32_16x16x32_bf16 v[20:23], a[216:219], a[192:195], v[20:23]\n\t"
"v_accvgpr_write_b32 a30, v232\n\t"
"v_cvt_pk_bf16_f32 v250, v250, v251\n\t"
"v_mfma_f32_16x16x32_bf16 v[20:23], a[252:255], a[196:199], v[20:23]\n\t"
"v_accvgpr_write_b32 a27, v234\n\t"
"v_accvgpr_write_b32 a31, v250\n\t"
"v_accvgpr_read_b32 v228, a98\n\t"
"v_accvgpr_read_b32 v230, a102\n\t"
"v_accvgpr_read_b32 v229, a99\n\t"
"v_accvgpr_read_b32 v231, a103\n\t"
"buffer_load_dwordx4 a[72:75], v252, s[8:11], s20 offen offset:1024 sc1\n\t"
"v_cvt_pk_bf16_f32 v222, v222, v223\n\t"
"v_cvt_pk_bf16_f32 v226, v226, v227\n\t"
"v_pk_fma_f32 v[228:229], v[230:231], s[56:57], v[228:229]\n\t"
"v_pk_fma_f32 v[230:231], v[228:229], s[58:59], v[230:231] neg_lo:[0,0,1] neg_hi:[0,0,1]\n\t"
"v_pk_fma_f32 v[232:233], v[62:63], s[60:61], v[228:229]\n\t"
"v_pk_fma_f32 v[62:63], v[228:229], s[60:61], v[62:63]\n\t"
"v_pk_fma_f32 v[228:229], v[160:161], s[62:63], v[230:231]\n\t"
"v_pk_fma_f32 v[160:161], v[230:231], s[62:63], v[160:161]\n\t"
"s_waitcnt lgkmcnt(1)\n\t"
"v_mfma_f32_16x16x32_bf16 v[24:27], a[212:215], a[192:195], v[24:27]\n\t"
"v_accvgpr_write_b32 a34, v222\n\t"
"v_accvgpr_write_b32 a38, v226\n\t"
"v_mfma_f32_16x16x32_bf16 v[24:27], a[216:219], a[196:199], v[24:27]\n\t"
"v_accvgpr_read_b32 v234, a104\n\t"
"v_accvgpr_read_b32 v236, a108\n\t"
"v_mfma_f32_16x16x32_bf16 v[24:27], a[252:255], a[200:203], v[24:27]\n\t"
"v_accvgpr_read_b32 v235, a105\n\t"
"v_accvgpr_read_b32 v237, a109\n\t"
"buffer_load_dwordx4 a[76:79], v252, s[8:11], s20 offen offset:2048 sc1\n\t"
"v_cvt_pk_bf16_f32 v228, v228, v229\n\t"
"v_cvt_pk_bf16_f32 v232, v232, v233\n\t"
"v_pk_fma_f32 v[234:235], v[236:237], s[56:57], v[234:235]\n\t"
"v_pk_fma_f32 v[236:237], v[234:235], s[58:59], v[236:237] neg_lo:[0,0,1] neg_hi:[0,0,1]\n\t"
"v_pk_fma_f32 v[250:251], v[64:65], s[60:61], v[234:235]\n\t"
"v_pk_fma_f32 v[64:65], v[234:235], s[60:61], v[64:65]\n\t"
"v_pk_fma_f32 v[234:235], v[162:163], s[62:63], v[236:237]\n\t"
"v_pk_fma_f32 v[162:163], v[236:237], s[62:63], v[162:163]\n\t"
"s_waitcnt lgkmcnt(0)\n\t"
"v_mfma_f32_16x16x32_bf16 v[28:31], a[212:215], a[196:199], v[28:31]\n\t"
"v_accvgpr_write_b32 a35, v228\n\t"
"v_accvgpr_write_b32 a39, v232\n\t"
"v_mfma_f32_16x16x32_bf16 v[28:31], a[216:219], a[200:203], v[28:31]\n\t"
"v_accvgpr_read_b32 v222, a106\n\t"
"v_accvgpr_read_b32 v224, a110\n\t"
"v_mfma_f32_16x16x32_bf16 v[28:31], a[252:255], a[204:207], v[28:31]\n\t"
"v_accvgpr_read_b32 v223, a107\n\t"
"v_accvgpr_read_b32 v225, a111\n\t"
"s_mov_b32 s20, 675840\n\t"
"buffer_load_dwordx4 a[80:83], v252, s[8:11], s20 offen offset:0 sc1\n\t"
"v_cvt_pk_bf16_f32 v234, v234, v235\n\t"
"v_cvt_pk_bf16_f32 v250, v250, v251\n\t"
"v_pk_fma_f32 v[222:223], v[224:225], s[56:57], v[222:223]\n\t"
"v_pk_fma_f32 v[224:225], v[222:223], s[58:59], v[224:225] neg_lo:[0,0,1] neg_hi:[0,0,1]\n\t"
"v_pk_fma_f32 v[226:227], v[66:67], s[60:61], v[222:223]\n\t"
"v_pk_fma_f32 v[66:67], v[222:223], s[60:61], v[66:67]\n\t"
"v_pk_fma_f32 v[222:223], v[164:165], s[62:63], v[224:225]\n\t"
"v_pk_fma_f32 v[164:165], v[224:225], s[62:63], v[164:165]\n\t"
"s_waitcnt vmcnt(16)\n\t"
"v_mfma_f32_16x16x32_bf16 v[32:35], a[220:223], a[168:171], v[32:35]\n\t"
"v_accvgpr_write_b32 a104, v234\n\t"
"v_accvgpr_write_b32 a106, v250\n\t"
"v_mfma_f32_16x16x32_bf16 v[32:35], a[224:227], a[172:175], v[32:35]\n\t"
"v_accvgpr_read_b32 v228, a112\n\t"
"v_accvgpr_read_b32 v230, a116\n\t"
"v_mfma_f32_16x16x32_bf16 v[32:35], a[228:231], a[176:179], v[32:35]\n\t"
"v_accvgpr_read_b32 v229, a113\n\t"
"v_accvgpr_read_b32 v231, a117\n\t"
"buffer_load_dwordx4 a[212:215], v252, s[8:11], s20 offen offset:1024 sc1\n\t"
"v_cvt_pk_bf16_f32 v222, v222, v223\n\t"
"v_cvt_pk_bf16_f32 v226, v226, v227\n\t"
"v_pk_fma_f32 v[228:229], v[230:231], s[56:57], v[228:229]\n\t"
"v_pk_fma_f32 v[230:231], v[228:229], s[58:59], v[230:231] neg_lo:[0,0,1] neg_hi:[0,0,1]\n\t"
"v_pk_fma_f32 v[232:233], v[68:69], s[60:61], v[228:229]\n\t"
"v_pk_fma_f32 v[68:69], v[228:229], s[60:61], v[68:69]\n\t"
"v_pk_fma_f32 v[228:229], v[166:167], s[62:63], v[230:231]\n\t"
"v_pk_fma_f32 v[166:167], v[230:231], s[62:63], v[166:167]\n\t"
"v_mfma_f32_16x16x32_bf16 v[36:39], a[220:223], a[172:175], v[36:39]\n\t"
"v_accvgpr_write_b32 a105, v222\n\t"
"v_accvgpr_write_b32 a107, v226\n\t"
"v_mfma_f32_16x16x32_bf16 v[36:39], a[224:227], a[176:179], v[36:39]\n\t"
"v_accvgpr_read_b32 v234, a114\n\t"
"v_accvgpr_read_b32 v236, a118\n\t"
"v_mfma_f32_16x16x32_bf16 v[36:39], a[228:231], a[180:183], v[36:39]\n\t"
"v_accvgpr_read_b32 v235, a115\n\t"
"v_accvgpr_read_b32 v237, a119\n\t"
"buffer_load_dwordx4 a[216:219], v252, s[8:11], s20 offen offset:2048 sc1\n\t"
"v_cvt_pk_bf16_f32 v228, v228, v229\n\t"
"v_cvt_pk_bf16_f32 v232, v232, v233\n\t"
"v_pk_fma_f32 v[234:235], v[236:237], s[56:57], v[234:235]\n\t"
"v_pk_fma_f32 v[236:237], v[234:235], s[58:59], v[236:237] neg_lo:[0,0,1] neg_hi:[0,0,1]\n\t"
"v_pk_fma_f32 v[250:251], v[70:71], s[60:61], v[234:235]\n\t"
"v_pk_fma_f32 v[70:71], v[234:235], s[60:61], v[70:71]\n\t"
"v_pk_fma_f32 v[234:235], v[168:169], s[62:63], v[236:237]\n\t"
"v_pk_fma_f32 v[168:169], v[236:237], s[62:63], v[168:169]\n\t"
"v_mfma_f32_16x16x32_bf16 v[40:43], a[220:223], a[176:179], v[40:43]\n\t"
"v_accvgpr_write_b32 a112, v228\n\t"
"v_accvgpr_write_b32 a114, v232\n\t"
"v_mfma_f32_16x16x32_bf16 v[40:43], a[224:227], a[180:183], v[40:43]\n\t"
"v_accvgpr_read_b32 v222, a120\n\t"
"v_accvgpr_read_b32 v224, a124\n\t"
"v_mfma_f32_16x16x32_bf16 v[40:43], a[228:231], a[184:187], v[40:43]\n\t"
"v_accvgpr_read_b32 v223, a121\n\t"
"v_accvgpr_read_b32 v225, a125\n\t"
"s_mov_b32 s20, 688128\n\t"
"buffer_load_dwordx4 a[252:255], v252, s[8:11], s20 offen offset:0 sc1\n\t"
"v_cvt_pk_bf16_f32 v234, v234, v235\n\t"
"v_cvt_pk_bf16_f32 v250, v250, v251\n\t"
"v_pk_fma_f32 v[222:223], v[224:225], s[56:57], v[222:223]\n\t"
"v_pk_fma_f32 v[224:225], v[222:223], s[58:59], v[224:225] neg_lo:[0,0,1] neg_hi:[0,0,1]\n\t"
"v_pk_fma_f32 v[226:227], v[72:73], s[60:61], v[222:223]\n\t"
"v_pk_fma_f32 v[72:73], v[222:223], s[60:61], v[72:73]\n\t"
"v_pk_fma_f32 v[222:223], v[170:171], s[62:63], v[224:225]\n\t"
"v_pk_fma_f32 v[170:171], v[224:225], s[62:63], v[170:171]\n\t"
"v_mfma_f32_16x16x32_bf16 v[44:47], a[220:223], a[180:183], v[44:47]\n\t"
"v_accvgpr_write_b32 a113, v234\n\t"
"v_accvgpr_write_b32 a115, v250\n\t"
"v_mfma_f32_16x16x32_bf16 v[44:47], a[224:227], a[184:187], v[44:47]\n\t"
"v_accvgpr_read_b32 v228, a122\n\t"
"v_accvgpr_read_b32 v230, a126\n\t"
"v_mfma_f32_16x16x32_bf16 v[44:47], a[228:231], a[188:191], v[44:47]\n\t"
"v_accvgpr_read_b32 v229, a123\n\t"
"v_accvgpr_read_b32 v231, a127\n\t"
"v_accvgpr_read_b32 v234, a128\n\t"
"v_accvgpr_read_b32 v236, a132\n\t"
"v_accvgpr_read_b32 v235, a129\n\t"
"v_accvgpr_read_b32 v237, a133\n\t"
"buffer_load_dwordx4 a[84:87], v252, s[8:11], s20 offen offset:1024 sc1\n\t"
"v_cvt_pk_bf16_f32 v222, v222, v223\n\t"
"v_cvt_pk_bf16_f32 v226, v226, v227\n\t"
"v_pk_fma_f32 v[228:229], v[230:231], s[56:57], v[228:229]\n\t"
"v_pk_fma_f32 v[230:231], v[228:229], s[58:59], v[230:231] neg_lo:[0,0,1] neg_hi:[0,0,1]\n\t"
"v_pk_fma_f32 v[232:233], v[74:75], s[60:61], v[228:229]\n\t"
"v_pk_fma_f32 v[74:75], v[228:229], s[60:61], v[74:75]\n\t"
"v_pk_fma_f32 v[228:229], v[172:173], s[62:63], v[230:231]\n\t"
"v_pk_fma_f32 v[172:173], v[230:231], s[62:63], v[172:173]\n\t"
"v_pk_fma_f32 v[234:235], v[236:237], s[56:57], v[234:235]\n\t"
"v_pk_fma_f32 v[236:237], v[234:235], s[58:59], v[236:237] neg_lo:[0,0,1] neg_hi:[0,0,1]\n\t"
"v_pk_fma_f32 v[250:251], v[76:77], s[60:61], v[234:235]\n\t"
"v_pk_fma_f32 v[76:77], v[234:235], s[60:61], v[76:77]\n\t"
"v_pk_fma_f32 v[234:235], v[174:175], s[62:63], v[236:237]\n\t"
"v_pk_fma_f32 v[174:175], v[236:237], s[62:63], v[174:175]\n\t"
"global_load_dwordx4 a[52:55], v252, s[46:47] offset:1024\n\t"
"v_mfma_f32_16x16x32_bf16 v[48:51], a[220:223], a[184:187], v[48:51]\n\t"
"v_accvgpr_write_b32 a120, v222\n\t"
"v_accvgpr_write_b32 a122, v226\n\t"
"v_mfma_f32_16x16x32_bf16 v[48:51], a[224:227], a[188:191], v[48:51]\n\t"
"v_accvgpr_read_b32 v222, a130\n\t"
"v_accvgpr_read_b32 v224, a134\n\t"
"v_mfma_f32_16x16x32_bf16 v[48:51], a[228:231], a[192:195], v[48:51]\n\t"
"v_accvgpr_read_b32 v223, a131\n\t"
"v_accvgpr_read_b32 v225, a135\n\t"
"buffer_load_dwordx4 a[88:91], v252, s[8:11], s20 offen offset:2048 sc1\n\t"
"v_cvt_pk_bf16_f32 v228, v228, v229\n\t"
"v_cvt_pk_bf16_f32 v232, v232, v233\n\t"
"v_cvt_pk_bf16_f32 v234, v234, v235\n\t"
"v_cvt_pk_bf16_f32 v250, v250, v251\n\t"
"v_pk_fma_f32 v[222:223], v[224:225], s[56:57], v[222:223]\n\t"
"v_pk_fma_f32 v[224:225], v[222:223], s[58:59], v[224:225] neg_lo:[0,0,1] neg_hi:[0,0,1]\n\t"
"v_pk_fma_f32 v[226:227], v[78:79], s[60:61], v[222:223]\n\t"
"v_pk_fma_f32 v[78:79], v[222:223], s[60:61], v[78:79]\n\t"
"v_pk_fma_f32 v[222:223], v[176:177], s[62:63], v[224:225]\n\t"
"v_pk_fma_f32 v[176:177], v[224:225], s[62:63], v[176:177]\n\t"
"v_mfma_f32_16x16x32_bf16 v[52:55], a[220:223], a[188:191], v[52:55]\n\t"
"v_accvgpr_write_b32 a121, v228\n\t"
"v_accvgpr_write_b32 a123, v232\n\t"
"v_mfma_f32_16x16x32_bf16 v[52:55], a[224:227], a[192:195], v[52:55]\n\t"
"v_accvgpr_write_b32 a128, v234\n\t"
"v_accvgpr_write_b32 a130, v250\n\t"
"v_mfma_f32_16x16x32_bf16 v[52:55], a[228:231], a[196:199], v[52:55]\n\t"
"v_accvgpr_read_b32 v228, a136\n\t"
"v_accvgpr_read_b32 v230, a140\n\t"
"v_accvgpr_read_b32 v229, a137\n\t"
"v_accvgpr_read_b32 v231, a141\n\t"
"s_mov_b32 s20, 847872\n\t"
"buffer_load_dwordx4 a[92:95], v252, s[8:11], s20 offen offset:0 sc1\n\t"
"v_cvt_pk_bf16_f32 v222, v222, v223\n\t"
"v_cvt_pk_bf16_f32 v226, v226, v227\n\t"
"v_pk_fma_f32 v[228:229], v[230:231], s[56:57], v[228:229]\n\t"
"v_pk_fma_f32 v[230:231], v[228:229], s[58:59], v[230:231] neg_lo:[0,0,1] neg_hi:[0,0,1]\n\t"
"v_pk_fma_f32 v[232:233], v[80:81], s[60:61], v[228:229]\n\t"
"v_pk_fma_f32 v[80:81], v[228:229], s[60:61], v[80:81]\n\t"
"v_pk_fma_f32 v[228:229], v[178:179], s[62:63], v[230:231]\n\t"
"v_pk_fma_f32 v[178:179], v[230:231], s[62:63], v[178:179]\n\t"
"v_mfma_f32_16x16x32_bf16 v[56:59], a[220:223], a[192:195], v[56:59]\n\t"
"v_accvgpr_write_b32 a129, v222\n\t"
"v_accvgpr_write_b32 a131, v226\n\t"
"v_mfma_f32_16x16x32_bf16 v[56:59], a[224:227], a[196:199], v[56:59]\n\t"
"v_accvgpr_read_b32 v234, a138\n\t"
"v_accvgpr_read_b32 v236, a142\n\t"
"v_mfma_f32_16x16x32_bf16 v[56:59], a[228:231], a[200:203], v[56:59]\n\t"
"v_accvgpr_read_b32 v235, a139\n\t"
"v_accvgpr_read_b32 v237, a143\n\t"
"buffer_load_dwordx4 a[96:99], v252, s[8:11], s20 offen offset:1024 sc1\n\t"
"v_cvt_pk_bf16_f32 v228, v228, v229\n\t"
"v_cvt_pk_bf16_f32 v232, v232, v233\n\t"
"v_pk_fma_f32 v[234:235], v[236:237], s[56:57], v[234:235]\n\t"
"v_pk_fma_f32 v[236:237], v[234:235], s[58:59], v[236:237] neg_lo:[0,0,1] neg_hi:[0,0,1]\n\t"
"v_pk_fma_f32 v[250:251], v[82:83], s[60:61], v[234:235]\n\t"
"v_pk_fma_f32 v[82:83], v[234:235], s[60:61], v[82:83]\n\t"
"v_pk_fma_f32 v[234:235], v[180:181], s[62:63], v[236:237]\n\t"
"v_pk_fma_f32 v[180:181], v[236:237], s[62:63], v[180:181]\n\t"
"v_mfma_f32_16x16x32_bf16 v[60:63], a[220:223], a[196:199], v[60:63]\n\t"
"v_accvgpr_write_b32 a136, v228\n\t"
"v_accvgpr_write_b32 a138, v232\n\t"
"v_mfma_f32_16x16x32_bf16 v[60:63], a[224:227], a[200:203], v[60:63]\n\t"
"v_accvgpr_read_b32 v222, a144\n\t"
"v_accvgpr_read_b32 v224, a148\n\t"
"v_mfma_f32_16x16x32_bf16 v[60:63], a[228:231], a[204:207], v[60:63]\n\t"
"v_accvgpr_read_b32 v223, a145\n\t"
"v_accvgpr_read_b32 v225, a149\n\t"
"buffer_load_dwordx4 a[100:103], v252, s[8:11], s20 offen offset:2048 sc1\n\t"
"v_cvt_pk_bf16_f32 v234, v234, v235\n\t"
"v_cvt_pk_bf16_f32 v250, v250, v251\n\t"
"v_pk_fma_f32 v[222:223], v[224:225], s[56:57], v[222:223]\n\t"
"v_pk_fma_f32 v[224:225], v[222:223], s[58:59], v[224:225] neg_lo:[0,0,1] neg_hi:[0,0,1]\n\t"
"v_pk_fma_f32 v[226:227], v[84:85], s[60:61], v[222:223]\n\t"
"v_pk_fma_f32 v[84:85], v[222:223], s[60:61], v[84:85]\n\t"
"v_pk_fma_f32 v[222:223], v[182:183], s[62:63], v[224:225]\n\t"
"v_pk_fma_f32 v[182:183], v[224:225], s[62:63], v[182:183]\n\t"
"s_waitcnt vmcnt(22)\n\t"
"v_mfma_f32_16x16x32_bf16 v[64:67], a[232:235], a[168:171], v[64:67]\n\t"
"v_accvgpr_write_b32 a137, v234\n\t"
"v_accvgpr_write_b32 a139, v250\n\t"
"v_mfma_f32_16x16x32_bf16 v[64:67], a[236:239], a[172:175], v[64:67]\n\t"
"v_accvgpr_read_b32 v228, a146\n\t"
"v_accvgpr_read_b32 v230, a150\n\t"
"v_mfma_f32_16x16x32_bf16 v[64:67], a[240:243], a[176:179], v[64:67]\n\t"
"v_accvgpr_read_b32 v229, a147\n\t"
"v_accvgpr_read_b32 v231, a151\n\t"
"s_mov_b32 s20, 860160\n\t"
"buffer_load_dwordx4 a[220:223], v252, s[8:11], s20 offen offset:0 sc1\n\t"
"v_cvt_pk_bf16_f32 v222, v222, v223\n\t"
"v_cvt_pk_bf16_f32 v226, v226, v227\n\t"
"v_pk_fma_f32 v[228:229], v[230:231], s[56:57], v[228:229]\n\t"
"v_pk_fma_f32 v[230:231], v[228:229], s[58:59], v[230:231] neg_lo:[0,0,1] neg_hi:[0,0,1]\n\t"
"v_pk_fma_f32 v[232:233], v[86:87], s[60:61], v[228:229]\n\t"
"v_pk_fma_f32 v[86:87], v[228:229], s[60:61], v[86:87]\n\t"
"v_pk_fma_f32 v[228:229], v[184:185], s[62:63], v[230:231]\n\t"
"v_pk_fma_f32 v[184:185], v[230:231], s[62:63], v[184:185]\n\t"
"v_mfma_f32_16x16x32_bf16 v[68:71], a[232:235], a[172:175], v[68:71]\n\t"
"v_accvgpr_write_b32 a144, v222\n\t"
"v_accvgpr_write_b32 a146, v226\n\t"
"v_mfma_f32_16x16x32_bf16 v[68:71], a[236:239], a[176:179], v[68:71]\n\t"
"v_accvgpr_read_b32 v234, a152\n\t"
"v_accvgpr_read_b32 v236, a156\n\t"
"v_mfma_f32_16x16x32_bf16 v[68:71], a[240:243], a[180:183], v[68:71]\n\t"
"v_accvgpr_read_b32 v235, a153\n\t"
"v_accvgpr_read_b32 v237, a157\n\t"
"buffer_load_dwordx4 a[224:227], v252, s[8:11], s20 offen offset:1024 sc1\n\t"
"v_cvt_pk_bf16_f32 v228, v228, v229\n\t"
"v_cvt_pk_bf16_f32 v232, v232, v233\n\t"
"v_pk_fma_f32 v[234:235], v[236:237], s[56:57], v[234:235]\n\t"
"v_pk_fma_f32 v[236:237], v[234:235], s[58:59], v[236:237] neg_lo:[0,0,1] neg_hi:[0,0,1]\n\t"
"v_pk_fma_f32 v[250:251], v[88:89], s[60:61], v[234:235]\n\t"
"v_pk_fma_f32 v[88:89], v[234:235], s[60:61], v[88:89]\n\t"
"v_pk_fma_f32 v[234:235], v[186:187], s[62:63], v[236:237]\n\t"
"v_pk_fma_f32 v[186:187], v[236:237], s[62:63], v[186:187]\n\t"
"v_mfma_f32_16x16x32_bf16 v[72:75], a[232:235], a[176:179], v[72:75]\n\t"
"v_accvgpr_write_b32 a145, v228\n\t"
"v_accvgpr_write_b32 a147, v232\n\t"
"v_mfma_f32_16x16x32_bf16 v[72:75], a[236:239], a[180:183], v[72:75]\n\t"
"v_accvgpr_read_b32 v222, a154\n\t"
"v_accvgpr_read_b32 v224, a158\n\t"
"v_mfma_f32_16x16x32_bf16 v[72:75], a[240:243], a[184:187], v[72:75]\n\t"
"v_accvgpr_read_b32 v223, a155\n\t"
"v_accvgpr_read_b32 v225, a159\n\t"
"buffer_load_dwordx4 a[228:231], v252, s[8:11], s20 offen offset:2048 sc1\n\t"
"v_cvt_pk_bf16_f32 v234, v234, v235\n\t"
"v_cvt_pk_bf16_f32 v250, v250, v251\n\t"
"v_pk_fma_f32 v[222:223], v[224:225], s[56:57], v[222:223]\n\t"
"v_pk_fma_f32 v[224:225], v[222:223], s[58:59], v[224:225] neg_lo:[0,0,1] neg_hi:[0,0,1]\n\t"
"v_pk_fma_f32 v[226:227], v[90:91], s[60:61], v[222:223]\n\t"
"v_pk_fma_f32 v[90:91], v[222:223], s[60:61], v[90:91]\n\t"
"v_pk_fma_f32 v[222:223], v[188:189], s[62:63], v[224:225]\n\t"
"v_pk_fma_f32 v[188:189], v[224:225], s[62:63], v[188:189]\n\t"
"v_mfma_f32_16x16x32_bf16 v[76:79], a[232:235], a[180:183], v[76:79]\n\t"
"ds_read_b128 a[168:171], v242 offset:27648\n\t"
"v_accvgpr_write_b32 a152, v234\n\t"
"v_mfma_f32_16x16x32_bf16 v[76:79], a[236:239], a[184:187], v[76:79]\n\t"
"ds_read_b128 a[172:175], v242 offset:28160\n\t"
"v_accvgpr_write_b32 a154, v250\n\t"
"v_mfma_f32_16x16x32_bf16 v[76:79], a[240:243], a[188:191], v[76:79]\n\t"
"ds_read_b128 a[176:179], v242 offset:28672\n\t"
"v_accvgpr_read_b32 v228, a160\n\t"
"v_accvgpr_read_b32 v230, a164\n\t"
"v_accvgpr_read_b32 v229, a161\n\t"
"v_accvgpr_read_b32 v231, a165\n\t"
"s_mov_b32 s20, 872448\n\t"
"buffer_load_dwordx4 a[108:111], v252, s[8:11], s20 offen offset:0 sc1\n\t"
"v_cvt_pk_bf16_f32 v222, v222, v223\n\t"
"v_cvt_pk_bf16_f32 v226, v226, v227\n\t"
"v_pk_fma_f32 v[228:229], v[230:231], s[56:57], v[228:229]\n\t"
"v_pk_fma_f32 v[230:231], v[228:229], s[58:59], v[230:231] neg_lo:[0,0,1] neg_hi:[0,0,1]\n\t"
"v_pk_fma_f32 v[232:233], v[92:93], s[60:61], v[228:229]\n\t"
"v_pk_fma_f32 v[92:93], v[228:229], s[60:61], v[92:93]\n\t"
"v_pk_fma_f32 v[228:229], v[190:191], s[62:63], v[230:231]\n\t"
"v_pk_fma_f32 v[190:191], v[230:231], s[62:63], v[190:191]\n\t"
"v_mfma_f32_16x16x32_bf16 v[80:83], a[232:235], a[184:187], v[80:83]\n\t"
"v_accvgpr_write_b32 a153, v222\n\t"
"v_accvgpr_write_b32 a155, v226\n\t"
"v_mfma_f32_16x16x32_bf16 v[80:83], a[236:239], a[188:191], v[80:83]\n\t"
"v_accvgpr_read_b32 v234, a162\n\t"
"v_accvgpr_read_b32 v236, a166\n\t"
"v_mfma_f32_16x16x32_bf16 v[80:83], a[240:243], a[192:195], v[80:83]\n\t"
"v_accvgpr_read_b32 v235, a163\n\t"
"v_accvgpr_read_b32 v237, a167\n\t"
"buffer_load_dwordx4 a[116:119], v252, s[8:11], s20 offen offset:1024 sc1\n\t"
"v_cvt_pk_bf16_f32 v228, v228, v229\n\t"
"v_cvt_pk_bf16_f32 v232, v232, v233\n\t"
"v_pk_fma_f32 v[234:235], v[236:237], s[56:57], v[234:235]\n\t"
"v_pk_fma_f32 v[236:237], v[234:235], s[58:59], v[236:237] neg_lo:[0,0,1] neg_hi:[0,0,1]\n\t"
"v_pk_fma_f32 v[250:251], v[94:95], s[60:61], v[234:235]\n\t"
"v_pk_fma_f32 v[94:95], v[234:235], s[60:61], v[94:95]\n\t"
"v_pk_fma_f32 v[234:235], v[192:193], s[62:63], v[236:237]\n\t"
"v_pk_fma_f32 v[192:193], v[236:237], s[62:63], v[192:193]\n\t"
"v_mfma_f32_16x16x32_bf16 v[84:87], a[232:235], a[188:191], v[84:87]\n\t"
"ds_read_b128 a[180:183], v242 offset:29184\n\t"
"v_accvgpr_write_b32 a160, v228\n\t"
"v_mfma_f32_16x16x32_bf16 v[84:87], a[236:239], a[192:195], v[84:87]\n\t"
"v_accvgpr_write_b32 a162, v232\n\t"
"v_cvt_pk_bf16_f32 v234, v234, v235\n\t"
"v_mfma_f32_16x16x32_bf16 v[84:87], a[240:243], a[196:199], v[84:87]\n\t"
"buffer_load_dwordx4 a[124:127], v252, s[8:11], s20 offen offset:2048 sc1\n\t"
"v_cvt_pk_bf16_f32 v250, v250, v251\n\t"
"ds_read_b128 a[184:187], v242 offset:29696\n\t"
"v_mfma_f32_16x16x32_bf16 v[88:91], a[232:235], a[192:195], v[88:91]\n\t"
"v_accvgpr_write_b32 a161, v234\n\t"
"v_accvgpr_write_b32 a163, v250\n\t"
"v_mfma_f32_16x16x32_bf16 v[88:91], a[236:239], a[196:199], v[88:91]\n\t"
"ds_read_b64_tr_b16 v[222:223], v238 offset:1536\n\t"
"v_mfma_f32_16x16x32_bf16 v[88:91], a[240:243], a[200:203], v[88:91]\n\t"
"ds_read_b64_tr_b16 v[224:225], v238 offset:3072\n\t"
"s_mov_b32 s20, 884736\n\t"
"buffer_load_dwordx4 a[132:135], v252, s[8:11], s20 offen offset:0 sc1\n\t"
"ds_read_b64_tr_b16 v[226:227], v239 offset:1536\n\t"
"ds_read_b64_tr_b16 v[228:229], v239 offset:3072\n\t"
"v_mfma_f32_16x16x32_bf16 v[92:95], a[232:235], a[196:199], v[92:95]\n\t"
"ds_read_b128 a[188:191], v242 offset:30208\n\t"
"v_mfma_f32_16x16x32_bf16 v[92:95], a[236:239], a[200:203], v[92:95]\n\t"
"ds_read_b128 a[192:195], v242 offset:30720\n\t"
"v_mfma_f32_16x16x32_bf16 v[92:95], a[240:243], a[204:207], v[92:95]\n\t"
"buffer_load_dwordx4 a[140:143], v252, s[8:11], s20 offen offset:1024 sc1\n\t"
"s_waitcnt vmcnt(27) lgkmcnt(8)\n\t"
"v_mfma_f32_16x16x32_bf16 v[98:101], a[40:43], a[168:171], v[98:101]\n\t"
"ds_read_b128 a[196:199], v242 offset:31232\n\t"
"v_mfma_f32_16x16x32_bf16 v[98:101], a[44:47], a[172:175], v[98:101]\n\t"
"ds_read_b128 a[200:203], v242 offset:31744\n\t"
"v_mfma_f32_16x16x32_bf16 v[98:101], a[48:51], a[176:179], v[98:101]\n\t"
"buffer_load_dwordx4 a[232:235], v252, s[8:11], s20 offen offset:2048 sc1\n\t"
"ds_read_b128 a[204:207], v242 offset:32256\n\t"
"ds_read_b64_tr_b16 v[230:231], v238 offset:21504\n\t"
"s_waitcnt vmcnt(13) lgkmcnt(8)\n\t"
"v_mfma_f32_16x16x32_bf16 v[222:225], v[222:225], a[52:55], 0\n\t"
"ds_read_b64_tr_b16 v[232:233], v238 offset:23040\n\t"
"ds_read_b64_tr_b16 v[234:235], v239 offset:21504\n\t"
"s_waitcnt lgkmcnt(8)\n\t"
"v_mfma_f32_16x16x32_bf16 v[226:229], v[226:229], a[52:55], 0\n\t"
"ds_read_b64_tr_b16 v[236:237], v239 offset:23040\n\t"
"s_mov_b32 s20, 897024\n\t"
"buffer_load_dwordx4 a[236:239], v252, s[8:11], s20 offen offset:0 sc1\n\t"
"v_mfma_f32_16x16x32_bf16 v[102:105], a[40:43], a[172:175], v[102:105]\n\t"
"v_mfma_f32_16x16x32_bf16 v[102:105], a[44:47], a[176:179], v[102:105]\n\t"
"v_mfma_f32_16x16x32_bf16 v[102:105], a[48:51], a[180:183], v[102:105]\n\t"
"buffer_load_dwordx4 a[240:243], v252, s[8:11], s20 offen offset:1024 sc1\n\t"
"s_waitcnt lgkmcnt(4)\n\t"
"s_barrier\n\t"
"v_mfma_f32_16x16x32_bf16 v[106:109], a[40:43], a[176:179], v[106:109]\n\t"
"v_cvt_pk_bf16_f32 v222, v222, v223\n\t"
"v_cvt_pk_bf16_f32 v223, v224, v225\n\t"
"v_mfma_f32_16x16x32_bf16 v[106:109], a[44:47], a[180:183], v[106:109]\n\t"
"v_cvt_pk_bf16_f32 v224, v226, v227\n\t"
"v_cvt_pk_bf16_f32 v225, v228, v229\n\t"
"v_mfma_f32_16x16x32_bf16 v[106:109], a[48:51], a[184:187], v[106:109]\n\t"
"buffer_load_dwordx4 a[148:151], v252, s[8:11], s20 offen offset:2048 sc1\n\t"
"s_waitcnt lgkmcnt(2)\n\t"
"v_mfma_f32_16x16x32_bf16 v[230:233], v[230:233], a[52:55], 0\n\t"
"s_mov_b32 s20, 909312\n\t"
"buffer_load_dwordx4 a[156:159], v252, s[8:11], s20 offen offset:0 sc1\n\t"
"s_waitcnt lgkmcnt(0)\n\t"
"v_mfma_f32_16x16x32_bf16 v[234:237], v[234:237], a[52:55], 0\n\t"
"s_mov_b64 exec, s[44:45]\n\t"
"ds_write_b128 v240, v[222:225] offset:4608\n\t"
"s_mov_b64 exec, -1\n\t"
"v_mfma_f32_16x16x32_bf16 v[110:113], a[40:43], a[180:183], v[110:113]\n\t"
"ds_read_b64_tr_b16 v[222:223], v238 offset:41472\n\t"
"v_mfma_f32_16x16x32_bf16 v[110:113], a[44:47], a[184:187], v[110:113]\n\t"
"ds_read_b64_tr_b16 v[224:225], v238 offset:43008\n\t"
"v_mfma_f32_16x16x32_bf16 v[110:113], a[48:51], a[188:191], v[110:113]\n\t"
"buffer_load_dwordx4 a[164:167], v252, s[8:11], s20 offen offset:1024 sc1\n\t"
"v_mfma_f32_16x16x32_bf16 v[114:117], a[40:43], a[184:187], v[114:117]\n\t"
"ds_read_b64_tr_b16 v[226:227], v239 offset:41472\n\t"
"v_cvt_pk_bf16_f32 v230, v230, v231\n\t"
"v_mfma_f32_16x16x32_bf16 v[114:117], a[44:47], a[188:191], v[114:117]\n\t"
"ds_read_b64_tr_b16 v[228:229], v239 offset:43008\n\t"
"v_cvt_pk_bf16_f32 v231, v232, v233\n\t"
"v_mfma_f32_16x16x32_bf16 v[114:117], a[48:51], a[192:195], v[114:117]\n\t"
"v_cvt_pk_bf16_f32 v232, v234, v235\n\t"
"v_cvt_pk_bf16_f32 v233, v236, v237\n\t"
"v_mfma_f32_16x16x32_bf16 v[118:121], a[40:43], a[188:191], v[118:121]\n\t"
"v_subrev_u32 v238, 59904, v238\n\t"
"v_subrev_u32 v239, 59904, v239\n\t"
"v_mfma_f32_16x16x32_bf16 v[118:121], a[44:47], a[192:195], v[118:121]\n\t"
"v_mfma_f32_16x16x32_bf16 v[118:121], a[48:51], a[196:199], v[118:121]\n\t"
"ds_write_b128 v248, v[194:197] offset:18432\n\t"
"s_mov_b64 exec, s[44:45]\n\t"
"ds_write_b128 v240, v[230:233] offset:6144\n\t"
"s_mov_b64 exec, -1\n\t"
"v_mfma_f32_16x16x32_bf16 v[122:125], a[40:43], a[192:195], v[122:125]\n\t"
"ds_read_b64_tr_b16 v[230:231], v238 offset:1536\n\t"
"v_mfma_f32_16x16x32_bf16 v[122:125], a[44:47], a[196:199], v[122:125]\n\t"
"ds_read_b64_tr_b16 v[232:233], v238 offset:3072\n\t"
"v_mfma_f32_16x16x32_bf16 v[122:125], a[48:51], a[200:203], v[122:125]\n\t"
"ds_read_b64_tr_b16 v[234:235], v239 offset:1536\n\t"
"ds_read_b64_tr_b16 v[236:237], v239 offset:3072\n\t"
"s_waitcnt lgkmcnt(8)\n\t"
"v_mfma_f32_16x16x32_bf16 v[222:225], v[222:225], a[52:55], 0\n\t"
"ds_write_b64 v249, a[104:105] offset:18432\n\t"
"s_waitcnt lgkmcnt(7)\n\t"
"v_mfma_f32_16x16x32_bf16 v[226:229], v[226:229], a[52:55], 0\n\t"
"ds_write_b128 v248, v[198:201] offset:24576\n\t"
"v_mfma_f32_16x16x32_bf16 v[126:129], a[40:43], a[196:199], v[126:129]\n\t"
"v_mfma_f32_16x16x32_bf16 v[126:129], a[44:47], a[200:203], v[126:129]\n\t"
"v_mfma_f32_16x16x32_bf16 v[126:129], a[48:51], a[204:207], v[126:129]\n\t"
"v_mfma_f32_16x16x32_bf16 v[130:133], a[56:59], a[168:171], v[130:133]\n\t"
"v_cvt_pk_bf16_f32 v222, v222, v223\n\t"
"v_mfma_f32_16x16x32_bf16 v[130:133], a[60:63], a[172:175], v[130:133]\n\t"
"v_cvt_pk_bf16_f32 v223, v224, v225\n\t"
"v_cvt_pk_bf16_f32 v224, v226, v227\n\t"
"v_mfma_f32_16x16x32_bf16 v[130:133], a[208:211], a[176:179], v[130:133]\n\t"
"v_cvt_pk_bf16_f32 v225, v228, v229\n\t"
"s_waitcnt lgkmcnt(4)\n\t"
"v_mfma_f32_16x16x32_bf16 v[230:233], v[230:233], a[52:55], 0\n\t"
"s_mov_b64 exec, s[44:45]\n\t"
"ds_write_b128 v240, v[222:225] offset:7680\n\t"
"s_mov_b64 exec, -1\n\t"
"s_waitcnt lgkmcnt(3)\n\t"
"v_mfma_f32_16x16x32_bf16 v[234:237], v[234:237], a[52:55], 0\n\t"
"ds_read_b64_tr_b16 v[222:223], v238 offset:21504\n\t"
"ds_read_b64_tr_b16 v[224:225], v238 offset:23040\n\t"
"v_mfma_f32_16x16x32_bf16 v[134:137], a[56:59], a[172:175], v[134:137]\n\t"
"ds_read_b64_tr_b16 v[226:227], v239 offset:21504\n\t"
"v_mfma_f32_16x16x32_bf16 v[134:137], a[60:63], a[176:179], v[134:137]\n\t"
"v_mfma_f32_16x16x32_bf16 v[134:137], a[208:211], a[180:183], v[134:137]\n\t"
"ds_read_b64_tr_b16 v[228:229], v239 offset:23040\n\t"
"v_mfma_f32_16x16x32_bf16 v[138:141], a[56:59], a[176:179], v[138:141]\n\t"
"v_cvt_pk_bf16_f32 v230, v230, v231\n\t"
"v_cvt_pk_bf16_f32 v231, v232, v233\n\t"
"v_mfma_f32_16x16x32_bf16 v[138:141], a[60:63], a[180:183], v[138:141]\n\t"
"v_cvt_pk_bf16_f32 v232, v234, v235\n\t"
"v_cvt_pk_bf16_f32 v233, v236, v237\n\t"
"v_mfma_f32_16x16x32_bf16 v[138:141], a[208:211], a[184:187], v[138:141]\n\t"
"s_mov_b64 exec, s[44:45]\n\t"
"ds_write_b128 v240, v[230:233] offset:0\n\t"
"s_mov_b64 exec, -1\n\t"
"v_mfma_f32_16x16x32_bf16 v[142:145], a[56:59], a[180:183], v[142:145]\n\t"
"ds_read_b64_tr_b16 v[230:231], v238 offset:41472\n\t"
"v_mfma_f32_16x16x32_bf16 v[142:145], a[60:63], a[184:187], v[142:145]\n\t"
"ds_read_b64_tr_b16 v[232:233], v238 offset:43008\n\t"
"v_mfma_f32_16x16x32_bf16 v[142:145], a[208:211], a[188:191], v[142:145]\n\t"
"ds_read_b64_tr_b16 v[234:235], v239 offset:41472\n\t"
"ds_read_b64_tr_b16 v[236:237], v239 offset:43008\n\t"
"v_mfma_f32_16x16x32_bf16 v[146:149], a[56:59], a[184:187], v[146:149]\n\t"
"v_mfma_f32_16x16x32_bf16 v[146:149], a[60:63], a[188:191], v[146:149]\n\t"
"v_mfma_f32_16x16x32_bf16 v[146:149], a[208:211], a[192:195], v[146:149]\n\t"
"ds_write_b64 v249, a[106:107] offset:24576\n\t"
"s_waitcnt lgkmcnt(8)\n\t"
"v_mfma_f32_16x16x32_bf16 v[222:225], v[222:225], a[52:55], 0\n\t"
"s_waitcnt lgkmcnt(6)\n\t"
"v_mfma_f32_16x16x32_bf16 v[226:229], v[226:229], a[52:55], 0\n\t"
"v_mfma_f32_16x16x32_bf16 v[150:153], a[56:59], a[188:191], v[150:153]\n\t"
"v_mfma_f32_16x16x32_bf16 v[150:153], a[60:63], a[192:195], v[150:153]\n\t"
"v_mfma_f32_16x16x32_bf16 v[150:153], a[208:211], a[196:199], v[150:153]\n\t"
"v_mfma_f32_16x16x32_bf16 v[154:157], a[56:59], a[192:195], v[154:157]\n\t"
"s_nop 1\n\t"
"v_cvt_pk_bf16_f32 v222, v222, v223\n\t"
"v_cvt_pk_bf16_f32 v223, v224, v225\n\t"
"v_mfma_f32_16x16x32_bf16 v[154:157], a[60:63], a[196:199], v[154:157]\n\t"
"v_cvt_pk_bf16_f32 v224, v226, v227\n\t"
"v_mfma_f32_16x16x32_bf16 v[154:157], a[208:211], a[200:203], v[154:157]\n\t"
"v_cvt_pk_bf16_f32 v225, v228, v229\n\t"
"s_waitcnt lgkmcnt(3)\n\t"
"v_mfma_f32_16x16x32_bf16 v[230:233], v[230:233], a[52:55], 0\n\t"
"s_waitcnt lgkmcnt(1)\n\t"
"v_mfma_f32_16x16x32_bf16 v[234:237], v[234:237], a[52:55], 0\n\t"
"s_mov_b64 exec, s[44:45]\n\t"
"ds_write_b128 v240, v[222:225] offset:1536\n\t"
"s_mov_b64 exec, -1\n\t"
"v_mfma_f32_16x16x32_bf16 v[158:161], a[56:59], a[196:199], v[158:161]\n\t"
"v_mfma_f32_16x16x32_bf16 v[158:161], a[60:63], a[200:203], v[158:161]\n\t"
"v_mfma_f32_16x16x32_bf16 v[158:161], a[208:211], a[204:207], v[158:161]\n\t"
"buffer_load_dwordx4 a[56:59], v252, s[8:11], s20 offen offset:2048 sc1\n\t"
"v_mfma_f32_16x16x32_bf16 v[162:165], a[244:247], a[168:171], v[162:165]\n\t"
"v_cvt_pk_bf16_f32 v230, v230, v231\n\t"
"v_cvt_pk_bf16_f32 v231, v232, v233\n\t"
"v_mfma_f32_16x16x32_bf16 v[162:165], a[248:251], a[172:175], v[162:165]\n\t"
"v_cvt_pk_bf16_f32 v232, v234, v235\n\t"
"v_cvt_pk_bf16_f32 v233, v236, v237\n\t"
"v_mfma_f32_16x16x32_bf16 v[162:165], a[64:67], a[176:179], v[162:165]\n\t"
"s_mov_b32 s20, 1069056\n\t"
"buffer_load_dwordx4 a[208:211], v252, s[8:11], s20 offen offset:0 sc1\n\t"
"v_mfma_f32_16x16x32_bf16 v[166:169], a[244:247], a[172:175], v[166:169]\n\t"
"v_mfma_f32_16x16x32_bf16 v[166:169], a[248:251], a[176:179], v[166:169]\n\t"
"v_mfma_f32_16x16x32_bf16 v[166:169], a[64:67], a[180:183], v[166:169]\n\t"
"buffer_load_dwordx4 a[60:63], v252, s[8:11], s20 offen offset:1024 sc1\n\t"
"v_mfma_f32_16x16x32_bf16 v[170:173], a[244:247], a[176:179], v[170:173]\n\t"
"v_mfma_f32_16x16x32_bf16 v[170:173], a[248:251], a[180:183], v[170:173]\n\t"
"v_mfma_f32_16x16x32_bf16 v[170:173], a[64:67], a[184:187], v[170:173]\n\t"
"s_mov_b64 exec, s[44:45]\n\t"
"ds_write_b128 v240, v[230:233] offset:3072\n\t"
"s_mov_b64 exec, -1\n\t"
"v_mfma_f32_16x16x32_bf16 v[174:177], a[244:247], a[180:183], v[174:177]\n\t"
"v_mfma_f32_16x16x32_bf16 v[174:177], a[248:251], a[184:187], v[174:177]\n\t"
"v_mfma_f32_16x16x32_bf16 v[174:177], a[64:67], a[188:191], v[174:177]\n\t"
"v_mfma_f32_16x16x32_bf16 v[178:181], a[244:247], a[184:187], v[178:181]\n\t"
"v_mfma_f32_16x16x32_bf16 v[178:181], a[248:251], a[188:191], v[178:181]\n\t"
"v_mfma_f32_16x16x32_bf16 v[178:181], a[64:67], a[192:195], v[178:181]\n\t"
"v_mfma_f32_16x16x32_bf16 v[182:185], a[244:247], a[188:191], v[182:185]\n\t"
"v_mfma_f32_16x16x32_bf16 v[182:185], a[248:251], a[192:195], v[182:185]\n\t"
"v_mfma_f32_16x16x32_bf16 v[182:185], a[64:67], a[196:199], v[182:185]\n\t"
"s_waitcnt lgkmcnt(0)\n\t"
"s_barrier\n\t"
"v_mfma_f32_16x16x32_bf16 v[186:189], a[244:247], a[192:195], v[186:189]\n\t"
"ds_read_b128 a[168:171], v242 offset:0\n\t"
"v_mfma_f32_16x16x32_bf16 v[186:189], a[248:251], a[196:199], v[186:189]\n\t"
"ds_read_b128 a[172:175], v242 offset:512\n\t"
"v_mfma_f32_16x16x32_bf16 v[186:189], a[64:67], a[200:203], v[186:189]\n\t"
"ds_read_b128 a[176:179], v242 offset:1024\n\t"
"ds_read_b128 a[180:183], v242 offset:1536\n\t"
"ds_read_b128 a[184:187], v242 offset:2048\n\t"
"ds_read_b128 a[188:191], v242 offset:2560\n\t"
"ds_read_b128 a[40:43], v252 offset:18432\n\t"
"v_mfma_f32_16x16x32_bf16 v[190:193], a[244:247], a[196:199], v[190:193]\n\t"
"ds_read_b128 a[192:195], v242 offset:3072\n\t"
"v_mfma_f32_16x16x32_bf16 v[190:193], a[248:251], a[200:203], v[190:193]\n\t"
"ds_read_b128 a[44:47], v252 offset:19456\n\t"
"v_mfma_f32_16x16x32_bf16 v[190:193], a[64:67], a[204:207], v[190:193]\n\t"
"buffer_load_dwordx4 a[104:107], v252, s[8:11], s20 offen offset:2048 sc1\n\t"
"ds_read_b128 a[196:199], v242 offset:3584\n\t"
"ds_read_b128 a[48:51], v252 offset:20480\n\t"
"s_waitcnt lgkmcnt(8)\n\t"
"v_mfma_f32_16x16x32_bf16 v[0:3], a[68:71], a[168:171], v[0:3]\n\t"
"ds_read_b128 a[200:203], v242 offset:4096\n\t"
"v_mfma_f32_16x16x32_bf16 v[0:3], a[72:75], a[172:175], v[0:3]\n\t"
"ds_read_b128 a[204:207], v242 offset:4608\n\t"
"v_mfma_f32_16x16x32_bf16 v[0:3], a[76:79], a[176:179], v[0:3]\n\t"
"s_mov_b32 s20, 1081344\n\t"
"buffer_load_dwordx4 a[244:247], v252, s[8:11], s20 offen offset:0 sc1\n\t"
"ds_write_b128 v248, v[202:205] offset:30720\n\t"
"s_waitcnt lgkmcnt(10)\n\t"
"v_mfma_f32_16x16x32_bf16 v[4:7], a[68:71], a[172:175], v[4:7]\n\t"
"v_mfma_f32_16x16x32_bf16 v[4:7], a[72:75], a[176:179], v[4:7]\n\t"
"v_mfma_f32_16x16x32_bf16 v[4:7], a[76:79], a[180:183], v[4:7]\n\t"
"buffer_load_dwordx4 a[248:251], v252, s[8:11], s20 offen offset:1024 sc1\n\t"
"buffer_load_dwordx4 a[64:67], v252, s[8:11], s20 offen offset:2048 sc1\n\t"
"s_waitcnt lgkmcnt(9)\n\t"
"v_mfma_f32_16x16x32_bf16 v[8:11], a[68:71], a[176:179], v[8:11]\n\t"
"v_mfma_f32_16x16x32_bf16 v[8:11], a[72:75], a[180:183], v[8:11]\n\t"
"v_mfma_f32_16x16x32_bf16 v[8:11], a[76:79], a[184:187], v[8:11]\n\t"
"s_add_u32 s29, s22, 384\n\t"
"s_waitcnt lgkmcnt(7)\n\t"
"buffer_store_dwordx4 a[40:43], v247, s[16:19], s29 offen offset:0 nt\n\t"
"ds_write_b64 v249, a[112:113] offset:30720\n\t"
"v_mfma_f32_16x16x32_bf16 v[12:15], a[68:71], a[180:183], v[12:15]\n\t"
"v_mfma_f32_16x16x32_bf16 v[12:15], a[72:75], a[184:187], v[12:15]\n\t"
"v_mfma_f32_16x16x32_bf16 v[12:15], a[76:79], a[188:191], v[12:15]\n\t"
"s_add_u32 s29, s22, 384\n\t"
"s_waitcnt lgkmcnt(6)\n\t"
"buffer_store_dwordx4 a[44:47], v247, s[16:19], s29 offen offset:128 nt\n\t"
"v_mfma_f32_16x16x32_bf16 v[16:19], a[68:71], a[184:187], v[16:19]\n\t"
"v_mfma_f32_16x16x32_bf16 v[16:19], a[72:75], a[188:191], v[16:19]\n\t"
"v_mfma_f32_16x16x32_bf16 v[16:19], a[76:79], a[192:195], v[16:19]\n\t"
"s_add_u32 s29, s22, 384\n\t"
"s_waitcnt lgkmcnt(4)\n\t"
"buffer_store_dwordx4 a[48:51], v247, s[16:19], s29 offen offset:256 nt\n\t"
"v_mfma_f32_16x16x32_bf16 v[20:23], a[68:71], a[188:191], v[20:23]\n\t"
"v_mfma_f32_16x16x32_bf16 v[20:23], a[72:75], a[192:195], v[20:23]\n\t"
"v_mfma_f32_16x16x32_bf16 v[20:23], a[76:79], a[196:199], v[20:23]\n\t"
"s_mov_b32 s20, 1093632\n\t"
"buffer_load_dwordx4 a[44:47], v252, s[8:11], s20 offen offset:0 sc1\n\t"
"s_waitcnt lgkmcnt(3)\n\t"
"v_mfma_f32_16x16x32_bf16 v[24:27], a[68:71], a[192:195], v[24:27]\n\t"
"v_mfma_f32_16x16x32_bf16 v[24:27], a[72:75], a[196:199], v[24:27]\n\t"
"v_mfma_f32_16x16x32_bf16 v[24:27], a[76:79], a[200:203], v[24:27]\n\t"
"ds_write_b128 v248, v[206:209] offset:36864\n\t"
"s_waitcnt lgkmcnt(3)\n\t"
"v_mfma_f32_16x16x32_bf16 v[28:31], a[68:71], a[196:199], v[28:31]\n\t"
"v_mfma_f32_16x16x32_bf16 v[28:31], a[72:75], a[200:203], v[28:31]\n\t"
"v_mfma_f32_16x16x32_bf16 v[28:31], a[76:79], a[204:207], v[28:31]\n\t"
"buffer_load_dwordx4 a[48:51], v252, s[8:11], s20 offen offset:1024 sc1\n\t"
"v_mfma_f32_16x16x32_bf16 v[32:35], a[80:83], a[168:171], v[32:35]\n\t"
"v_mfma_f32_16x16x32_bf16 v[32:35], a[212:215], a[172:175], v[32:35]\n\t"
"v_mfma_f32_16x16x32_bf16 v[32:35], a[216:219], a[176:179], v[32:35]\n\t"
"buffer_load_dwordx4 a[68:71], v252, s[8:11], s20 offen offset:2048 sc1\n\t"
"v_mfma_f32_16x16x32_bf16 v[36:39], a[80:83], a[172:175], v[36:39]\n\t"
"ds_read_b64_tr_b16 v[222:223], v238 offset:512\n\t"
"v_mfma_f32_16x16x32_bf16 v[36:39], a[212:215], a[176:179], v[36:39]\n\t"
"ds_read_b64_tr_b16 v[224:225], v238 offset:2048\n\t"
"v_mfma_f32_16x16x32_bf16 v[36:39], a[216:219], a[180:183], v[36:39]\n\t"
"s_mov_b32 s20, 1105920\n\t"
"buffer_load_dwordx4 a[72:75], v252, s[8:11], s20 offen offset:0 sc1\n\t"
"ds_write_b64 v249, a[114:115] offset:36864\n\t"
"v_mfma_f32_16x16x32_bf16 v[40:43], a[80:83], a[176:179], v[40:43]\n\t"
"ds_read_b64_tr_b16 v[226:227], v239 offset:512\n\t"
"v_mfma_f32_16x16x32_bf16 v[40:43], a[212:215], a[180:183], v[40:43]\n\t"
"ds_read_b64_tr_b16 v[228:229], v239 offset:2048\n\t"
"v_mfma_f32_16x16x32_bf16 v[40:43], a[216:219], a[184:187], v[40:43]\n\t"
"buffer_load_dwordx4 a[76:79], v252, s[8:11], s20 offen offset:1024 sc1\n\t"
"v_mfma_f32_16x16x32_bf16 v[44:47], a[80:83], a[180:183], v[44:47]\n\t"
"v_mfma_f32_16x16x32_bf16 v[44:47], a[212:215], a[184:187], v[44:47]\n\t"
"v_mfma_f32_16x16x32_bf16 v[44:47], a[216:219], a[188:191], v[44:47]\n\t"
"s_waitcnt lgkmcnt(2)\n\t"
"s_barrier\n\t"
"v_mfma_f32_16x16x32_bf16 v[48:51], a[80:83], a[184:187], v[48:51]\n\t"
"ds_read_b128 v[194:197], v252 offset:30720\n\t"
"v_mfma_f32_16x16x32_bf16 v[48:51], a[212:215], a[188:191], v[48:51]\n\t"
"ds_read_b128 v[198:201], v252 offset:31744\n\t"
"v_mfma_f32_16x16x32_bf16 v[48:51], a[216:219], a[192:195], v[48:51]\n\t"
"buffer_load_dwordx4 v[206:209], v252, s[8:11], s20 offen offset:2048 sc1\n\t"
"v_mfma_f32_16x16x32_bf16 v[52:55], a[80:83], a[188:191], v[52:55]\n\t"
"ds_read_b128 a[40:43], v252 offset:32768\n\t"
"v_mfma_f32_16x16x32_bf16 v[52:55], a[212:215], a[192:195], v[52:55]\n\t"
"ds_read_b64_tr_b16 v[230:231], v238 offset:20480\n\t"
"v_mfma_f32_16x16x32_bf16 v[52:55], a[216:219], a[196:199], v[52:55]\n\t"
"s_mov_b32 s20, 1118208\n\t"
"buffer_load_dwordx4 a[112:115], v252, s[8:11], s20 offen offset:0 sc1\n\t"
"v_mfma_f32_16x16x32_bf16 v[222:225], v[222:225], a[52:55], 0\n\t"
"ds_write_b128 v248, v[210:213] offset:18432\n\t"
"v_mfma_f32_16x16x32_bf16 v[56:59], a[80:83], a[192:195], v[56:59]\n\t"
"ds_read_b64_tr_b16 v[232:233], v238 offset:22016\n\t"
"v_mfma_f32_16x16x32_bf16 v[56:59], a[212:215], a[196:199], v[56:59]\n\t"
"ds_read_b64_tr_b16 v[234:235], v239 offset:20480\n\t"
"v_mfma_f32_16x16x32_bf16 v[56:59], a[216:219], a[200:203], v[56:59]\n\t"
"s_add_u32 s29, s22, 196992\n\t"
"s_waitcnt lgkmcnt(6)\n\t"
"buffer_store_dwordx4 v[194:197], v247, s[16:19], s29 offen offset:0 nt\n\t"
"v_mfma_f32_16x16x32_bf16 v[226:229], v[226:229], a[52:55], 0\n\t"
"ds_read_b64_tr_b16 v[236:237], v239 offset:22016\n\t"
"v_mfma_f32_16x16x32_bf16 v[60:63], a[80:83], a[196:199], v[60:63]\n\t"
"v_cvt_pk_bf16_f32 v222, v222, v223\n\t"
"v_cvt_pk_bf16_f32 v223, v224, v225\n\t"
"v_mfma_f32_16x16x32_bf16 v[60:63], a[212:215], a[200:203], v[60:63]\n\t"
"v_mfma_f32_16x16x32_bf16 v[60:63], a[216:219], a[204:207], v[60:63]\n\t"
"buffer_load_dwordx4 a[80:83], v252, s[8:11], s20 offen offset:1024 sc1\n\t"
"s_waitcnt vmcnt(36)\n\t"
"v_mfma_f32_16x16x32_bf16 v[64:67], a[252:255], a[168:171], v[64:67]\n\t"
"ds_read_b128 a[168:171], v242 offset:9216\n\t"
"v_mfma_f32_16x16x32_bf16 v[64:67], a[84:87], a[172:175], v[64:67]\n\t"
"v_cvt_pk_bf16_f32 v224, v226, v227\n\t"
"v_cvt_pk_bf16_f32 v225, v228, v229\n\t"
"v_mfma_f32_16x16x32_bf16 v[64:67], a[88:91], a[176:179], v[64:67]\n\t"
"buffer_load_dwordx4 a[212:215], v252, s[8:11], s20 offen offset:2048 sc1\n\t"
"v_mfma_f32_16x16x32_bf16 v[68:71], a[252:255], a[172:175], v[68:71]\n\t"
"ds_read_b128 a[172:175], v242 offset:9728\n\t"
"v_mfma_f32_16x16x32_bf16 v[68:71], a[84:87], a[176:179], v[68:71]\n\t"
"v_mfma_f32_16x16x32_bf16 v[68:71], a[88:91], a[180:183], v[68:71]\n\t"
"s_add_u32 s29, s22, 196992\n\t"
"s_waitcnt lgkmcnt(8)\n\t"
"buffer_store_dwordx4 v[198:201], v247, s[16:19], s29 offen offset:128 nt\n\t"
"ds_write_b64 v249, a[120:121] offset:18432\n\t"
"s_waitcnt lgkmcnt(5)\n\t"
"v_mfma_f32_16x16x32_bf16 v[230:233], v[230:233], a[52:55], 0\n\t"
"s_mov_b32 s20, 1130496\n\t"
"buffer_load_dwordx4 a[216:219], v252, s[8:11], s20 offen offset:0 sc1\n\t"
"v_mfma_f32_16x16x32_bf16 v[72:75], a[252:255], a[176:179], v[72:75]\n\t"
"ds_read_b128 a[176:179], v242 offset:10240\n\t"
"v_mfma_f32_16x16x32_bf16 v[72:75], a[84:87], a[180:183], v[72:75]\n\t"
"v_mfma_f32_16x16x32_bf16 v[72:75], a[88:91], a[184:187], v[72:75]\n\t"
"s_add_u32 s29, s22, 196992\n\t"
"buffer_store_dwordx4 a[40:43], v247, s[16:19], s29 offen offset:256 nt\n\t"
"s_waitcnt lgkmcnt(4)\n\t"
"v_mfma_f32_16x16x32_bf16 v[234:237], v[234:237], a[52:55], 0\n\t"
"buffer_load_dwordx4 a[40:43], v252, s[8:11], s20 offen offset:1024 sc1\n\t"
"v_mfma_f32_16x16x32_bf16 v[76:79], a[252:255], a[180:183], v[76:79]\n\t"
"v_cvt_pk_bf16_f32 v230, v230, v231\n\t"
"v_cvt_pk_bf16_f32 v231, v232, v233\n\t"
"v_mfma_f32_16x16x32_bf16 v[76:79], a[84:87], a[184:187], v[76:79]\n\t"
"ds_read_b128 a[180:183], v242 offset:10752\n\t"
"v_mfma_f32_16x16x32_bf16 v[76:79], a[88:91], a[188:191], v[76:79]\n\t"
"ds_write_b128 v248, v[214:217] offset:24576\n\t"
"v_mfma_f32_16x16x32_bf16 v[80:83], a[252:255], a[184:187], v[80:83]\n\t"
"ds_read_b128 a[184:187], v242 offset:11264\n\t"
"v_mfma_f32_16x16x32_bf16 v[80:83], a[84:87], a[188:191], v[80:83]\n\t"
"v_cvt_pk_bf16_f32 v232, v234, v235\n\t"
"v_cvt_pk_bf16_f32 v233, v236, v237\n\t"
"v_mfma_f32_16x16x32_bf16 v[80:83], a[88:91], a[192:195], v[80:83]\n\t"
"ds_write_b64 v249, a[122:123] offset:24576\n\t"
"v_mfma_f32_16x16x32_bf16 v[84:87], a[252:255], a[188:191], v[84:87]\n\t"
"ds_read_b128 a[188:191], v242 offset:11776\n\t"
"v_mfma_f32_16x16x32_bf16 v[84:87], a[84:87], a[192:195], v[84:87]\n\t"
"v_mfma_f32_16x16x32_bf16 v[84:87], a[88:91], a[196:199], v[84:87]\n\t"
"v_mfma_f32_16x16x32_bf16 v[88:91], a[252:255], a[192:195], v[88:91]\n\t"
"ds_read_b128 a[192:195], v242 offset:12288\n\t"
"v_mfma_f32_16x16x32_bf16 v[88:91], a[84:87], a[196:199], v[88:91]\n\t"
"v_mfma_f32_16x16x32_bf16 v[88:91], a[88:91], a[200:203], v[88:91]\n\t"
"v_mfma_f32_16x16x32_bf16 v[92:95], a[252:255], a[196:199], v[92:95]\n\t"
"ds_read_b128 a[196:199], v242 offset:12800\n\t"
"v_mfma_f32_16x16x32_bf16 v[92:95], a[84:87], a[200:203], v[92:95]\n\t"
"ds_read_b128 a[200:203], v242 offset:13312\n\t"
"v_mfma_f32_16x16x32_bf16 v[92:95], a[88:91], a[204:207], v[92:95]\n\t"
"buffer_load_dwordx4 a[252:255], v252, s[8:11], s20 offen offset:2048 sc1\n\t"
"s_waitcnt vmcnt(39) lgkmcnt(8)\n\t"
"v_mfma_f32_16x16x32_bf16 v[98:101], a[92:95], a[168:171], v[98:101]\n\t"
"ds_read_b128 a[204:207], v242 offset:13824\n\t"
"v_mfma_f32_16x16x32_bf16 v[98:101], a[96:99], a[172:175], v[98:101]\n\t"
"v_mfma_f32_16x16x32_bf16 v[98:101], a[100:103], a[176:179], v[98:101]\n\t"
"s_mov_b32 s20, 1290240\n\t"
"buffer_load_dwordx4 a[84:87], v252, s[8:11], s20 offen offset:0 sc1\n\t"
"s_waitcnt lgkmcnt(8)\n\t"
"v_mfma_f32_16x16x32_bf16 v[102:105], a[92:95], a[172:175], v[102:105]\n\t"
"v_mfma_f32_16x16x32_bf16 v[102:105], a[96:99], a[176:179], v[102:105]\n\t"
"v_mfma_f32_16x16x32_bf16 v[102:105], a[100:103], a[180:183], v[102:105]\n\t"
"buffer_load_dwordx4 a[88:91], v252, s[8:11], s20 offen offset:1024 sc1\n\t"
"s_waitcnt lgkmcnt(0)\n\t"
"s_barrier\n\t"
"v_mfma_f32_16x16x32_bf16 v[106:109], a[92:95], a[176:179], v[106:109]\n\t"
"ds_read_b128 v[194:197], v252 offset:18432\n\t"
"v_mfma_f32_16x16x32_bf16 v[106:109], a[96:99], a[180:183], v[106:109]\n\t"
"ds_read_b128 v[198:201], v252 offset:19456\n\t"
"v_mfma_f32_16x16x32_bf16 v[106:109], a[100:103], a[184:187], v[106:109]\n\t"
"s_mov_b64 exec, s[44:45]\n\t"
"ds_write_b128 v240, v[222:225] offset:0\n\t"
"s_mov_b64 exec, -1\n\t"
"s_mov_b64 exec, s[44:45]\n\t"
"ds_write_b128 v240, v[230:233] offset:1536\n\t"
"s_mov_b64 exec, -1\n\t"
"v_mfma_f32_16x16x32_bf16 v[110:113], a[92:95], a[180:183], v[110:113]\n\t"
"ds_read_b64_tr_b16 v[222:223], v238 offset:40448\n\t"
"v_mfma_f32_16x16x32_bf16 v[110:113], a[96:99], a[184:187], v[110:113]\n\t"
"ds_read_b64_tr_b16 v[224:225], v238 offset:41984\n\t"
"v_mfma_f32_16x16x32_bf16 v[110:113], a[100:103], a[188:191], v[110:113]\n\t"
"buffer_load_dwordx4 v[210:213], v252, s[8:11], s20 offen offset:2048 sc1\n\t"
"ds_read_b64_tr_b16 v[226:227], v239 offset:40448\n\t"
"ds_read_b64_tr_b16 v[228:229], v239 offset:41984\n\t"
"v_mfma_f32_16x16x32_bf16 v[114:117], a[92:95], a[184:187], v[114:117]\n\t"
"ds_read_b128 v[202:205], v252 offset:20480\n\t"
"v_add_u32 v238, 59904, v238\n\t"
"v_mfma_f32_16x16x32_bf16 v[114:117], a[96:99], a[188:191], v[114:117]\n\t"
"v_add_u32 v239, 59904, v239\n\t"
"ds_read_b64_tr_b16 v[230:231], v238 offset:512\n\t"
"v_mfma_f32_16x16x32_bf16 v[114:117], a[100:103], a[192:195], v[114:117]\n\t"
"s_mov_b32 s20, 1302528\n\t"
"buffer_load_dwordx4 v[214:217], v252, s[8:11], s20 offen offset:0 sc1\n\t"
"buffer_load_dwordx4 a[120:123], v252, s[8:11], s20 offen offset:1024 sc1\n\t"
"v_mfma_f32_16x16x32_bf16 v[118:121], a[92:95], a[188:191], v[118:121]\n\t"
"ds_read_b64_tr_b16 v[232:233], v238 offset:2048\n\t"
"v_mfma_f32_16x16x32_bf16 v[118:121], a[96:99], a[192:195], v[118:121]\n\t"
"ds_read_b64_tr_b16 v[234:235], v239 offset:512\n\t"
"v_mfma_f32_16x16x32_bf16 v[118:121], a[100:103], a[196:199], v[118:121]\n\t"
"ds_read_b64_tr_b16 v[236:237], v239 offset:2048\n\t"
"s_waitcnt lgkmcnt(7)\n\t"
"v_mfma_f32_16x16x32_bf16 v[222:225], v[222:225], a[52:55], 0\n\t"
"ds_write_b128 v248, a[0:3] offset:30720\n\t"
"s_waitcnt lgkmcnt(6)\n\t"
"v_mfma_f32_16x16x32_bf16 v[226:229], v[226:229], a[52:55], 0\n\t"
"s_add_u32 s29, s22, 393600\n\t"
"buffer_store_dwordx4 v[194:197], v247, s[16:19], s29 offen offset:0 nt\n\t"
"v_mfma_f32_16x16x32_bf16 v[122:125], a[92:95], a[192:195], v[122:125]\n\t"
"v_mfma_f32_16x16x32_bf16 v[122:125], a[96:99], a[196:199], v[122:125]\n\t"
"v_mfma_f32_16x16x32_bf16 v[122:125], a[100:103], a[200:203], v[122:125]\n\t"
"s_add_u32 s29, s22, 393600\n\t"
"buffer_store_dwordx4 v[198:201], v247, s[16:19], s29 offen offset:128 nt\n\t"
"v_mfma_f32_16x16x32_bf16 v[126:129], a[92:95], a[196:199], v[126:129]\n\t"
"v_cvt_pk_bf16_f32 v222, v222, v223\n\t"
"v_cvt_pk_bf16_f32 v223, v224, v225\n\t"
"v_mfma_f32_16x16x32_bf16 v[126:129], a[96:99], a[200:203], v[126:129]\n\t"
"v_cvt_pk_bf16_f32 v224, v226, v227\n\t"
"v_cvt_pk_bf16_f32 v225, v228, v229\n\t"
"v_mfma_f32_16x16x32_bf16 v[126:129], a[100:103], a[204:207], v[126:129]\n\t"
"buffer_load_dwordx4 a[92:95], v252, s[8:11], s20 offen offset:2048 sc1\n\t"
"s_waitcnt lgkmcnt(3)\n\t"
"v_mfma_f32_16x16x32_bf16 v[230:233], v[230:233], a[52:55], 0\n\t"
"s_add_u32 s29, s22, 393600\n\t"
"buffer_store_dwordx4 v[202:205], v247, s[16:19], s29 offen offset:256 nt\n\t"
"s_mov_b32 s20, 1314816\n\t"
"buffer_load_dwordx4 a[96:99], v252, s[8:11], s20 offen offset:0 sc1\n\t"
"s_waitcnt lgkmcnt(1)\n\t"
"v_mfma_f32_16x16x32_bf16 v[234:237], v[234:237], a[52:55], 0\n\t"
"s_mov_b64 exec, s[44:45]\n\t"
"ds_write_b128 v240, v[222:225] offset:3072\n\t"
"s_mov_b64 exec, -1\n\t"
"s_waitcnt vmcnt(46)\n\t"
"v_mfma_f32_16x16x32_bf16 v[130:133], a[220:223], a[168:171], v[130:133]\n\t"
"ds_read_b64_tr_b16 v[222:223], v238 offset:20480\n\t"
"v_mfma_f32_16x16x32_bf16 v[130:133], a[224:227], a[172:175], v[130:133]\n\t"
"ds_read_b64_tr_b16 v[224:225], v238 offset:22016\n\t"
"v_mfma_f32_16x16x32_bf16 v[130:133], a[228:231], a[176:179], v[130:133]\n\t"
"buffer_load_dwordx4 a[100:103], v252, s[8:11], s20 offen offset:1024 sc1\n\t"
"v_mfma_f32_16x16x32_bf16 v[134:137], a[220:223], a[172:175], v[134:137]\n\t"
"ds_read_b64_tr_b16 v[226:227], v239 offset:20480\n\t"
"v_cvt_pk_bf16_f32 v230, v230, v231\n\t"
"v_mfma_f32_16x16x32_bf16 v[134:137], a[224:227], a[176:179], v[134:137]\n\t"
"ds_read_b64_tr_b16 v[228:229], v239 offset:22016\n\t"
"v_cvt_pk_bf16_f32 v231, v232, v233\n\t"
"v_mfma_f32_16x16x32_bf16 v[134:137], a[228:231], a[180:183], v[134:137]\n\t"
"ds_write_b64 v249, a[128:129] offset:30720\n\t"
"v_cvt_pk_bf16_f32 v232, v234, v235\n\t"
"v_cvt_pk_bf16_f32 v233, v236, v237\n\t"
"v_mfma_f32_16x16x32_bf16 v[138:141], a[220:223], a[176:179], v[138:141]\n\t"
"v_mfma_f32_16x16x32_bf16 v[138:141], a[224:227], a[180:183], v[138:141]\n\t"
"v_mfma_f32_16x16x32_bf16 v[138:141], a[228:231], a[184:187], v[138:141]\n\t"
"s_mov_b64 exec, s[44:45]\n\t"
"ds_write_b128 v240, v[230:233] offset:4608\n\t"
"s_mov_b64 exec, -1\n\t"
"v_mfma_f32_16x16x32_bf16 v[142:145], a[220:223], a[180:183], v[142:145]\n\t"
"ds_read_b64_tr_b16 v[230:231], v238 offset:40448\n\t"
"v_mfma_f32_16x16x32_bf16 v[142:145], a[224:227], a[184:187], v[142:145]\n\t"
"ds_read_b64_tr_b16 v[232:233], v238 offset:41984\n\t"
"v_mfma_f32_16x16x32_bf16 v[142:145], a[228:231], a[188:191], v[142:145]\n\t"
"ds_read_b64_tr_b16 v[234:235], v239 offset:40448\n\t"
"ds_read_b64_tr_b16 v[236:237], v239 offset:41984\n\t"
"s_waitcnt lgkmcnt(8)\n\t"
"v_mfma_f32_16x16x32_bf16 v[222:225], v[222:225], a[52:55], 0\n\t"
"ds_write_b128 v248, a[4:7] offset:36864\n\t"
"s_waitcnt lgkmcnt(7)\n\t"
"v_mfma_f32_16x16x32_bf16 v[226:229], v[226:229], a[52:55], 0\n\t"
"ds_write_b64 v249, a[130:131] offset:36864\n\t"
"v_mfma_f32_16x16x32_bf16 v[146:149], a[220:223], a[184:187], v[146:149]\n\t"
"v_mfma_f32_16x16x32_bf16 v[146:149], a[224:227], a[188:191], v[146:149]\n\t"
"v_mfma_f32_16x16x32_bf16 v[146:149], a[228:231], a[192:195], v[146:149]\n\t"
"v_mfma_f32_16x16x32_bf16 v[150:153], a[220:223], a[188:191], v[150:153]\n\t"
"v_cvt_pk_bf16_f32 v222, v222, v223\n\t"
"v_cvt_pk_bf16_f32 v223, v224, v225\n\t"
"v_mfma_f32_16x16x32_bf16 v[150:153], a[224:227], a[192:195], v[150:153]\n\t"
"v_cvt_pk_bf16_f32 v224, v226, v227\n\t"
"v_mfma_f32_16x16x32_bf16 v[150:153], a[228:231], a[196:199], v[150:153]\n\t"
"v_cvt_pk_bf16_f32 v225, v228, v229\n\t"
"s_waitcnt lgkmcnt(4)\n\t"
"v_mfma_f32_16x16x32_bf16 v[230:233], v[230:233], a[52:55], 0\n\t"
"s_waitcnt lgkmcnt(2)\n\t"
"v_mfma_f32_16x16x32_bf16 v[234:237], v[234:237], a[52:55], 0\n\t"
"s_mov_b64 exec, s[44:45]\n\t"
"ds_write_b128 v240, v[222:225] offset:6144\n\t"
"s_mov_b64 exec, -1\n\t"
"v_mfma_f32_16x16x32_bf16 v[154:157], a[220:223], a[192:195], v[154:157]\n\t"
"v_mfma_f32_16x16x32_bf16 v[154:157], a[224:227], a[196:199], v[154:157]\n\t"
"v_mfma_f32_16x16x32_bf16 v[154:157], a[228:231], a[200:203], v[154:157]\n\t"
"v_mfma_f32_16x16x32_bf16 v[158:161], a[220:223], a[196:199], v[158:161]\n\t"
"v_cvt_pk_bf16_f32 v230, v230, v231\n\t"
"v_cvt_pk_bf16_f32 v231, v232, v233\n\t"
"v_mfma_f32_16x16x32_bf16 v[158:161], a[224:227], a[200:203], v[158:161]\n\t"
"v_cvt_pk_bf16_f32 v232, v234, v235\n\t"
"v_cvt_pk_bf16_f32 v233, v236, v237\n\t"
"v_mfma_f32_16x16x32_bf16 v[158:161], a[228:231], a[204:207], v[158:161]\n\t"
"buffer_load_dwordx4 a[220:223], v252, s[8:11], s20 offen offset:2048 sc1\n\t"
"s_waitcnt vmcnt(45)\n\t"
"v_mfma_f32_16x16x32_bf16 v[162:165], a[108:111], a[168:171], v[162:165]\n\t"
"v_mfma_f32_16x16x32_bf16 v[162:165], a[116:119], a[172:175], v[162:165]\n\t"
"v_mfma_f32_16x16x32_bf16 v[162:165], a[124:127], a[176:179], v[162:165]\n\t"
"s_mov_b64 exec, s[44:45]\n\t"
"ds_write_b128 v240, v[230:233] offset:7680\n\t"
"s_mov_b64 exec, -1\n\t"
"v_mfma_f32_16x16x32_bf16 v[166:169], a[108:111], a[172:175], v[166:169]\n\t"
"v_mfma_f32_16x16x32_bf16 v[166:169], a[116:119], a[176:179], v[166:169]\n\t"
"v_mfma_f32_16x16x32_bf16 v[166:169], a[124:127], a[180:183], v[166:169]\n\t"
"v_mfma_f32_16x16x32_bf16 v[170:173], a[108:111], a[176:179], v[170:173]\n\t"
"v_mfma_f32_16x16x32_bf16 v[170:173], a[116:119], a[180:183], v[170:173]\n\t"
"v_mfma_f32_16x16x32_bf16 v[170:173], a[124:127], a[184:187], v[170:173]\n\t"
"v_mfma_f32_16x16x32_bf16 v[174:177], a[108:111], a[180:183], v[174:177]\n\t"
"v_mfma_f32_16x16x32_bf16 v[174:177], a[116:119], a[184:187], v[174:177]\n\t"
"v_mfma_f32_16x16x32_bf16 v[174:177], a[124:127], a[188:191], v[174:177]\n\t"
"s_waitcnt lgkmcnt(0)\n\t"
"s_barrier\n\t"
"v_mfma_f32_16x16x32_bf16 v[178:181], a[108:111], a[184:187], v[178:181]\n\t"
"ds_read_b128 a[168:171], v242 offset:0\n\t"
"v_mfma_f32_16x16x32_bf16 v[178:181], a[116:119], a[188:191], v[178:181]\n\t"
"ds_read_b128 a[172:175], v242 offset:512\n\t"
"v_mfma_f32_16x16x32_bf16 v[178:181], a[124:127], a[192:195], v[178:181]\n\t"
"ds_read_b128 a[176:179], v242 offset:1024\n\t"
"ds_read_b128 a[180:183], v242 offset:1536\n\t"
"ds_read_b128 v[194:197], v252 offset:30720\n\t"
"v_mfma_f32_16x16x32_bf16 v[182:185], a[108:111], a[188:191], v[182:185]\n\t"
"ds_read_b128 a[184:187], v242 offset:2048\n\t"
"v_mfma_f32_16x16x32_bf16 v[182:185], a[116:119], a[192:195], v[182:185]\n\t"
"ds_read_b128 v[198:201], v252 offset:31744\n\t"
"v_mfma_f32_16x16x32_bf16 v[182:185], a[124:127], a[196:199], v[182:185]\n\t"
"ds_read_b128 a[188:191], v242 offset:2560\n\t"
"ds_read_b128 v[202:205], v252 offset:32768\n\t"
"v_mfma_f32_16x16x32_bf16 v[186:189], a[108:111], a[192:195], v[186:189]\n\t"
"ds_read_b128 a[192:195], v242 offset:3072\n\t"
"v_mfma_f32_16x16x32_bf16 v[186:189], a[116:119], a[196:199], v[186:189]\n\t"
"v_mfma_f32_16x16x32_bf16 v[186:189], a[124:127], a[200:203], v[186:189]\n\t"
"ds_write_b128 v248, a[8:11] offset:18432\n\t"
"v_mfma_f32_16x16x32_bf16 v[190:193], a[108:111], a[196:199], v[190:193]\n\t"
"ds_read_b128 a[196:199], v242 offset:3584\n\t"
"v_mfma_f32_16x16x32_bf16 v[190:193], a[116:119], a[200:203], v[190:193]\n\t"
"ds_read_b128 a[200:203], v242 offset:4096\n\t"
"v_mfma_f32_16x16x32_bf16 v[190:193], a[124:127], a[204:207], v[190:193]\n\t"
"s_add_u32 s29, s22, 590208\n\t"
"s_waitcnt lgkmcnt(8)\n\t"
"buffer_store_dwordx4 v[194:197], v247, s[16:19], s29 offen offset:0 nt\n\t"
"s_waitcnt vmcnt(43)\n\t"
"v_mfma_f32_16x16x32_bf16 v[0:3], a[132:135], a[168:171], v[0:3]\n\t"
"ds_read_b128 a[204:207], v242 offset:4608\n\t"
"v_mfma_f32_16x16x32_bf16 v[0:3], a[140:143], a[172:175], v[0:3]\n\t"
"v_mfma_f32_16x16x32_bf16 v[0:3], a[232:235], a[176:179], v[0:3]\n\t"
"s_add_u32 s29, s22, 590208\n\t"
"s_waitcnt lgkmcnt(7)\n\t"
"buffer_store_dwordx4 v[198:201], v247, s[16:19], s29 offen offset:128 nt\n\t"
"v_mfma_f32_16x16x32_bf16 v[4:7], a[132:135], a[172:175], v[4:7]\n\t"
"v_mfma_f32_16x16x32_bf16 v[4:7], a[140:143], a[176:179], v[4:7]\n\t"
"v_mfma_f32_16x16x32_bf16 v[4:7], a[232:235], a[180:183], v[4:7]\n\t"
"ds_write_b64 v249, a[136:137] offset:18432\n\t"
"v_mfma_f32_16x16x32_bf16 v[8:11], a[132:135], a[176:179], v[8:11]\n\t"
"v_mfma_f32_16x16x32_bf16 v[8:11], a[140:143], a[180:183], v[8:11]\n\t"
"v_mfma_f32_16x16x32_bf16 v[8:11], a[232:235], a[184:187], v[8:11]\n\t"
"s_add_u32 s29, s22, 590208\n\t"
"s_waitcnt lgkmcnt(6)\n\t"
"buffer_store_dwordx4 v[202:205], v247, s[16:19], s29 offen offset:256 nt\n\t"
"v_mfma_f32_16x16x32_bf16 v[12:15], a[132:135], a[180:183], v[12:15]\n\t"
"v_mfma_f32_16x16x32_bf16 v[12:15], a[140:143], a[184:187], v[12:15]\n\t"
"v_mfma_f32_16x16x32_bf16 v[12:15], a[232:235], a[188:191], v[12:15]\n\t"
"ds_write_b128 v248, a[12:15] offset:24576\n\t"
"s_waitcnt lgkmcnt(6)\n\t"
"v_mfma_f32_16x16x32_bf16 v[16:19], a[132:135], a[184:187], v[16:19]\n\t"
"v_mfma_f32_16x16x32_bf16 v[16:19], a[140:143], a[188:191], v[16:19]\n\t"
"v_mfma_f32_16x16x32_bf16 v[16:19], a[232:235], a[192:195], v[16:19]\n\t"
"ds_write_b64 v249, a[138:139] offset:24576\n\t"
"s_waitcnt lgkmcnt(5)\n\t"
"v_mfma_f32_16x16x32_bf16 v[20:23], a[132:135], a[188:191], v[20:23]\n\t"
"v_mfma_f32_16x16x32_bf16 v[20:23], a[140:143], a[192:195], v[20:23]\n\t"
"v_mfma_f32_16x16x32_bf16 v[20:23], a[232:235], a[196:199], v[20:23]\n\t"
"s_waitcnt lgkmcnt(4)\n\t"
"v_mfma_f32_16x16x32_bf16 v[24:27], a[132:135], a[192:195], v[24:27]\n\t"
"v_mfma_f32_16x16x32_bf16 v[24:27], a[140:143], a[196:199], v[24:27]\n\t"
"v_mfma_f32_16x16x32_bf16 v[24:27], a[232:235], a[200:203], v[24:27]\n\t"
"s_waitcnt lgkmcnt(3)\n\t"
"v_mfma_f32_16x16x32_bf16 v[28:31], a[132:135], a[196:199], v[28:31]\n\t"
"v_mfma_f32_16x16x32_bf16 v[28:31], a[140:143], a[200:203], v[28:31]\n\t"
"v_mfma_f32_16x16x32_bf16 v[28:31], a[232:235], a[204:207], v[28:31]\n\t"
"s_waitcnt vmcnt(42)\n\t"
"v_mfma_f32_16x16x32_bf16 v[32:35], a[236:239], a[168:171], v[32:35]\n\t"
"v_mfma_f32_16x16x32_bf16 v[32:35], a[240:243], a[172:175], v[32:35]\n\t"
"v_mfma_f32_16x16x32_bf16 v[32:35], a[148:151], a[176:179], v[32:35]\n\t"
"ds_read_b64_tr_b16 v[222:223], v238 offset:1024\n\t"
"ds_read_b64_tr_b16 v[224:225], v238 offset:2560\n\t"
"v_mfma_f32_16x16x32_bf16 v[36:39], a[236:239], a[172:175], v[36:39]\n\t"
"v_mfma_f32_16x16x32_bf16 v[36:39], a[240:243], a[176:179], v[36:39]\n\t"
"v_mfma_f32_16x16x32_bf16 v[36:39], a[148:151], a[180:183], v[36:39]\n\t"
"v_mfma_f32_16x16x32_bf16 v[40:43], a[236:239], a[176:179], v[40:43]\n\t"
"ds_read_b64_tr_b16 v[226:227], v239 offset:1024\n\t"
"v_mfma_f32_16x16x32_bf16 v[40:43], a[240:243], a[180:183], v[40:43]\n\t"
"v_mfma_f32_16x16x32_bf16 v[40:43], a[148:151], a[184:187], v[40:43]\n\t"
"ds_read_b64_tr_b16 v[228:229], v239 offset:2560\n\t"
"v_mfma_f32_16x16x32_bf16 v[44:47], a[236:239], a[180:183], v[44:47]\n\t"
"v_mfma_f32_16x16x32_bf16 v[44:47], a[240:243], a[184:187], v[44:47]\n\t"
"v_mfma_f32_16x16x32_bf16 v[44:47], a[148:151], a[188:191], v[44:47]\n\t"
"s_waitcnt lgkmcnt(4)\n\t"
"s_barrier\n\t"
"v_mfma_f32_16x16x32_bf16 v[48:51], a[236:239], a[184:187], v[48:51]\n\t"
"ds_read_b128 v[194:197], v252 offset:18432\n\t"
"v_mfma_f32_16x16x32_bf16 v[48:51], a[240:243], a[188:191], v[48:51]\n\t"
"ds_read_b128 v[198:201], v252 offset:19456\n\t"
"v_mfma_f32_16x16x32_bf16 v[48:51], a[148:151], a[192:195], v[48:51]\n\t"
"ds_read_b128 v[202:205], v252 offset:20480\n\t"
"v_mfma_f32_16x16x32_bf16 v[52:55], a[236:239], a[188:191], v[52:55]\n\t"
"v_mfma_f32_16x16x32_bf16 v[52:55], a[240:243], a[192:195], v[52:55]\n\t"
"ds_read_b64_tr_b16 v[230:231], v238 offset:20992\n\t"
"v_mfma_f32_16x16x32_bf16 v[52:55], a[148:151], a[196:199], v[52:55]\n\t"
"ds_write_b128 v248, a[16:19] offset:30720\n\t"
"s_waitcnt lgkmcnt(7)\n\t"
"v_mfma_f32_16x16x32_bf16 v[222:225], v[222:225], a[52:55], 0\n\t"
"ds_read_b64_tr_b16 v[232:233], v238 offset:22528\n\t"
"v_mfma_f32_16x16x32_bf16 v[56:59], a[236:239], a[192:195], v[56:59]\n\t"
"v_mfma_f32_16x16x32_bf16 v[56:59], a[240:243], a[196:199], v[56:59]\n\t"
"ds_read_b64_tr_b16 v[234:235], v239 offset:20992\n\t"
"v_mfma_f32_16x16x32_bf16 v[56:59], a[148:151], a[200:203], v[56:59]\n\t"
"s_add_u32 s29, s22, 786816\n\t"
"s_waitcnt lgkmcnt(6)\n\t"
"buffer_store_dwordx4 v[194:197], v247, s[16:19], s29 offen offset:0 nt\n\t"
"v_mfma_f32_16x16x32_bf16 v[226:229], v[226:229], a[52:55], 0\n\t"
"ds_read_b64_tr_b16 v[236:237], v239 offset:22528\n\t"
"v_mfma_f32_16x16x32_bf16 v[60:63], a[236:239], a[196:199], v[60:63]\n\t"
"v_cvt_pk_bf16_f32 v222, v222, v223\n\t"
"v_cvt_pk_bf16_f32 v223, v224, v225\n\t"
"v_mfma_f32_16x16x32_bf16 v[60:63], a[240:243], a[200:203], v[60:63]\n\t"
"v_mfma_f32_16x16x32_bf16 v[60:63], a[148:151], a[204:207], v[60:63]\n\t"
"s_add_u32 s29, s22, 786816\n\t"
"s_waitcnt lgkmcnt(6)\n\t"
"buffer_store_dwordx4 v[198:201], v247, s[16:19], s29 offen offset:128 nt\n\t"
"s_waitcnt vmcnt(41)\n\t"
"v_mfma_f32_16x16x32_bf16 v[64:67], a[156:159], a[168:171], v[64:67]\n\t"
"ds_read_b128 a[168:171], v242 offset:9216\n\t"
"v_mfma_f32_16x16x32_bf16 v[64:67], a[164:167], a[172:175], v[64:67]\n\t"
"v_cvt_pk_bf16_f32 v224, v226, v227\n\t"
"v_cvt_pk_bf16_f32 v225, v228, v229\n\t"
"v_mfma_f32_16x16x32_bf16 v[64:67], a[56:59], a[176:179], v[64:67]\n\t"
"ds_write_b64 v249, a[144:145] offset:30720\n\t"
"v_mfma_f32_16x16x32_bf16 v[68:71], a[156:159], a[172:175], v[68:71]\n\t"
"ds_read_b128 a[172:175], v242 offset:9728\n\t"
"v_mfma_f32_16x16x32_bf16 v[68:71], a[164:167], a[176:179], v[68:71]\n\t"
"v_mfma_f32_16x16x32_bf16 v[68:71], a[56:59], a[180:183], v[68:71]\n\t"
"s_add_u32 s29, s22, 786816\n\t"
"s_waitcnt lgkmcnt(8)\n\t"
"buffer_store_dwordx4 v[202:205], v247, s[16:19], s29 offen offset:256 nt\n\t"
"s_waitcnt lgkmcnt(5)\n\t"
"v_mfma_f32_16x16x32_bf16 v[230:233], v[230:233], a[52:55], 0\n\t"
"ds_write_b128 v248, a[20:23] offset:36864\n\t"
"v_mfma_f32_16x16x32_bf16 v[72:75], a[156:159], a[176:179], v[72:75]\n\t"
"ds_read_b128 a[176:179], v242 offset:10240\n\t"
"v_mfma_f32_16x16x32_bf16 v[72:75], a[164:167], a[180:183], v[72:75]\n\t"
"v_mfma_f32_16x16x32_bf16 v[72:75], a[56:59], a[184:187], v[72:75]\n\t"
"s_waitcnt lgkmcnt(5)\n\t"
"v_mfma_f32_16x16x32_bf16 v[234:237], v[234:237], a[52:55], 0\n\t"
"ds_write_b64 v249, a[146:147] offset:36864\n\t"
"v_mfma_f32_16x16x32_bf16 v[76:79], a[156:159], a[180:183], v[76:79]\n\t"
"v_cvt_pk_bf16_f32 v230, v230, v231\n\t"
"v_mfma_f32_16x16x32_bf16 v[76:79], a[164:167], a[184:187], v[76:79]\n\t"
"v_cvt_pk_bf16_f32 v231, v232, v233\n\t"
"v_mfma_f32_16x16x32_bf16 v[76:79], a[56:59], a[188:191], v[76:79]\n\t"
"ds_read_b128 a[180:183], v242 offset:10752\n\t"
"v_mfma_f32_16x16x32_bf16 v[80:83], a[156:159], a[184:187], v[80:83]\n\t"
"ds_read_b128 a[184:187], v242 offset:11264\n\t"
"v_mfma_f32_16x16x32_bf16 v[80:83], a[164:167], a[188:191], v[80:83]\n\t"
"v_cvt_pk_bf16_f32 v232, v234, v235\n\t"
"v_mfma_f32_16x16x32_bf16 v[80:83], a[56:59], a[192:195], v[80:83]\n\t"
"v_cvt_pk_bf16_f32 v233, v236, v237\n\t"
"v_mfma_f32_16x16x32_bf16 v[84:87], a[156:159], a[188:191], v[84:87]\n\t"
"ds_read_b128 a[188:191], v242 offset:11776\n\t"
"v_mfma_f32_16x16x32_bf16 v[84:87], a[164:167], a[192:195], v[84:87]\n\t"
"v_mfma_f32_16x16x32_bf16 v[84:87], a[56:59], a[196:199], v[84:87]\n\t"
"v_mfma_f32_16x16x32_bf16 v[88:91], a[156:159], a[192:195], v[88:91]\n\t"
"ds_read_b128 a[192:195], v242 offset:12288\n\t"
"v_mfma_f32_16x16x32_bf16 v[88:91], a[164:167], a[196:199], v[88:91]\n\t"
"v_mfma_f32_16x16x32_bf16 v[88:91], a[56:59], a[200:203], v[88:91]\n\t"
"v_mfma_f32_16x16x32_bf16 v[92:95], a[156:159], a[196:199], v[92:95]\n\t"
"ds_read_b128 a[196:199], v242 offset:12800\n\t"
"v_mfma_f32_16x16x32_bf16 v[92:95], a[164:167], a[200:203], v[92:95]\n\t"
"ds_read_b128 a[200:203], v242 offset:13312\n\t"
"v_mfma_f32_16x16x32_bf16 v[92:95], a[56:59], a[204:207], v[92:95]\n\t"
"ds_read_b128 a[204:207], v242 offset:13824\n\t"
"s_waitcnt vmcnt(39) lgkmcnt(8)\n\t"
"v_mfma_f32_16x16x32_bf16 v[98:101], a[208:211], a[168:171], v[98:101]\n\t"
"v_mfma_f32_16x16x32_bf16 v[98:101], a[60:63], a[172:175], v[98:101]\n\t"
"v_mfma_f32_16x16x32_bf16 v[98:101], a[104:107], a[176:179], v[98:101]\n\t"
"s_waitcnt lgkmcnt(6)\n\t"
"v_mfma_f32_16x16x32_bf16 v[102:105], a[208:211], a[172:175], v[102:105]\n\t"
"v_mfma_f32_16x16x32_bf16 v[102:105], a[60:63], a[176:179], v[102:105]\n\t"
"v_mfma_f32_16x16x32_bf16 v[102:105], a[104:107], a[180:183], v[102:105]\n\t"
"s_waitcnt lgkmcnt(0)\n\t"
"s_barrier\n\t"
"v_mfma_f32_16x16x32_bf16 v[106:109], a[208:211], a[176:179], v[106:109]\n\t"
"ds_read_b128 v[194:197], v252 offset:30720\n\t"
"v_mfma_f32_16x16x32_bf16 v[106:109], a[60:63], a[180:183], v[106:109]\n\t"
"ds_read_b128 v[198:201], v252 offset:31744\n\t"
"v_mfma_f32_16x16x32_bf16 v[106:109], a[104:107], a[184:187], v[106:109]\n\t"
"s_mov_b64 exec, s[44:45]\n\t"
"ds_write_b128 v240, v[222:225] offset:4608\n\t"
"s_mov_b64 exec, -1\n\t"
"s_mov_b64 exec, s[44:45]\n\t"
"ds_write_b128 v240, v[230:233] offset:6144\n\t"
"s_mov_b64 exec, -1\n\t"
"v_mfma_f32_16x16x32_bf16 v[110:113], a[208:211], a[180:183], v[110:113]\n\t"
"ds_read_b64_tr_b16 v[222:223], v238 offset:40960\n\t"
"v_mfma_f32_16x16x32_bf16 v[110:113], a[60:63], a[184:187], v[110:113]\n\t"
"ds_read_b64_tr_b16 v[224:225], v238 offset:42496\n\t"
"v_mfma_f32_16x16x32_bf16 v[110:113], a[104:107], a[188:191], v[110:113]\n\t"
"ds_read_b64_tr_b16 v[226:227], v239 offset:40960\n\t"
"ds_read_b64_tr_b16 v[228:229], v239 offset:42496\n\t"
"ds_read_b128 v[202:205], v252 offset:32768\n\t"
"v_subrev_u32 v238, 59904, v238\n\t"
"v_mfma_f32_16x16x32_bf16 v[114:117], a[208:211], a[184:187], v[114:117]\n\t"
"v_subrev_u32 v239, 59904, v239\n\t"
"ds_read_b64_tr_b16 v[230:231], v238 offset:1024\n\t"
"v_mfma_f32_16x16x32_bf16 v[114:117], a[60:63], a[188:191], v[114:117]\n\t"
"ds_read_b64_tr_b16 v[232:233], v238 offset:2560\n\t"
"v_mfma_f32_16x16x32_bf16 v[114:117], a[104:107], a[192:195], v[114:117]\n\t"
"ds_read_b64_tr_b16 v[234:235], v239 offset:1024\n\t"
"ds_read_b64_tr_b16 v[236:237], v239 offset:2560\n\t"
"v_mfma_f32_16x16x32_bf16 v[118:121], a[208:211], a[188:191], v[118:121]\n\t"
"v_mfma_f32_16x16x32_bf16 v[118:121], a[60:63], a[192:195], v[118:121]\n\t"
"v_mfma_f32_16x16x32_bf16 v[118:121], a[104:107], a[196:199], v[118:121]\n\t"
"ds_write_b128 v248, a[24:27] offset:18432\n\t"
"s_waitcnt lgkmcnt(8)\n\t"
"v_mfma_f32_16x16x32_bf16 v[222:225], v[222:225], a[52:55], 0\n\t"
"s_add_u32 s29, s22, 983424\n\t"
"buffer_store_dwordx4 v[194:197], v247, s[16:19], s29 offen offset:0 nt\n\t"
"s_waitcnt lgkmcnt(6)\n\t"
"v_mfma_f32_16x16x32_bf16 v[226:229], v[226:229], a[52:55], 0\n\t"
"s_add_u32 s29, s22, 983424\n\t"
"buffer_store_dwordx4 v[198:201], v247, s[16:19], s29 offen offset:128 nt\n\t"
"v_mfma_f32_16x16x32_bf16 v[122:125], a[208:211], a[192:195], v[122:125]\n\t"
"v_mfma_f32_16x16x32_bf16 v[122:125], a[60:63], a[196:199], v[122:125]\n\t"
"v_mfma_f32_16x16x32_bf16 v[122:125], a[104:107], a[200:203], v[122:125]\n\t"
"s_add_u32 s29, s22, 983424\n\t"
"s_waitcnt lgkmcnt(5)\n\t"
"buffer_store_dwordx4 v[202:205], v247, s[16:19], s29 offen offset:256 nt\n\t"
"v_mfma_f32_16x16x32_bf16 v[126:129], a[208:211], a[196:199], v[126:129]\n\t"
"v_cvt_pk_bf16_f32 v222, v222, v223\n\t"
"v_cvt_pk_bf16_f32 v223, v224, v225\n\t"
"v_mfma_f32_16x16x32_bf16 v[126:129], a[60:63], a[200:203], v[126:129]\n\t"
"v_cvt_pk_bf16_f32 v224, v226, v227\n\t"
"v_cvt_pk_bf16_f32 v225, v228, v229\n\t"
"v_mfma_f32_16x16x32_bf16 v[126:129], a[104:107], a[204:207], v[126:129]\n\t"
"s_mov_b64 exec, s[44:45]\n\t"
"ds_write_b128 v240, v[222:225] offset:7680\n\t"
"s_mov_b64 exec, -1\n\t"
"s_waitcnt lgkmcnt(4)\n\t"
"v_mfma_f32_16x16x32_bf16 v[230:233], v[230:233], a[52:55], 0\n\t"
"ds_read_b64_tr_b16 v[222:223], v238 offset:20992\n\t"
"ds_read_b64_tr_b16 v[224:225], v238 offset:22528\n\t"
"s_waitcnt lgkmcnt(4)\n\t"
"v_mfma_f32_16x16x32_bf16 v[234:237], v[234:237], a[52:55], 0\n\t"
"ds_read_b64_tr_b16 v[226:227], v239 offset:20992\n\t"
"ds_read_b64_tr_b16 v[228:229], v239 offset:22528\n\t"
"s_waitcnt vmcnt(39)\n\t"
"v_mfma_f32_16x16x32_bf16 v[130:133], a[244:247], a[168:171], v[130:133]\n\t"
"v_mfma_f32_16x16x32_bf16 v[130:133], a[248:251], a[172:175], v[130:133]\n\t"
"v_mfma_f32_16x16x32_bf16 v[130:133], a[64:67], a[176:179], v[130:133]\n\t"
"ds_write_b64 v249, a[152:153] offset:18432\n\t"
"v_mfma_f32_16x16x32_bf16 v[134:137], a[244:247], a[172:175], v[134:137]\n\t"
"v_cvt_pk_bf16_f32 v230, v230, v231\n\t"
"v_cvt_pk_bf16_f32 v231, v232, v233\n\t"
"v_mfma_f32_16x16x32_bf16 v[134:137], a[248:251], a[176:179], v[134:137]\n\t"
"v_cvt_pk_bf16_f32 v232, v234, v235\n\t"
"v_cvt_pk_bf16_f32 v233, v236, v237\n\t"
"v_mfma_f32_16x16x32_bf16 v[134:137], a[64:67], a[180:183], v[134:137]\n\t"
"s_mov_b64 exec, s[44:45]\n\t"
"ds_write_b128 v240, v[230:233] offset:0\n\t"
"s_mov_b64 exec, -1\n\t"
"v_mfma_f32_16x16x32_bf16 v[138:141], a[244:247], a[176:179], v[138:141]\n\t"
"ds_read_b64_tr_b16 v[230:231], v238 offset:40960\n\t"
"v_mfma_f32_16x16x32_bf16 v[138:141], a[248:251], a[180:183], v[138:141]\n\t"
"ds_read_b64_tr_b16 v[232:233], v238 offset:42496\n\t"
"v_mfma_f32_16x16x32_bf16 v[138:141], a[64:67], a[184:187], v[138:141]\n\t"
"ds_read_b64_tr_b16 v[234:235], v239 offset:40960\n\t"
"ds_read_b64_tr_b16 v[236:237], v239 offset:42496\n\t"
"v_mfma_f32_16x16x32_bf16 v[142:145], a[244:247], a[180:183], v[142:145]\n\t"
"v_mfma_f32_16x16x32_bf16 v[142:145], a[248:251], a[184:187], v[142:145]\n\t"
"v_mfma_f32_16x16x32_bf16 v[142:145], a[64:67], a[188:191], v[142:145]\n\t"
"ds_write_b128 v248, a[28:31] offset:24576\n\t"
"s_waitcnt lgkmcnt(9)\n\t"
"v_mfma_f32_16x16x32_bf16 v[222:225], v[222:225], a[52:55], 0\n\t"
"ds_write_b64 v249, a[154:155] offset:24576\n\t"
"s_waitcnt lgkmcnt(8)\n\t"
"v_mfma_f32_16x16x32_bf16 v[226:229], v[226:229], a[52:55], 0\n\t"
"v_mfma_f32_16x16x32_bf16 v[146:149], a[244:247], a[184:187], v[146:149]\n\t"
"v_mfma_f32_16x16x32_bf16 v[146:149], a[248:251], a[188:191], v[146:149]\n\t"
"v_mfma_f32_16x16x32_bf16 v[146:149], a[64:67], a[192:195], v[146:149]\n\t"
"v_mfma_f32_16x16x32_bf16 v[150:153], a[244:247], a[188:191], v[150:153]\n\t"
"s_nop 0\n\t"
"v_cvt_pk_bf16_f32 v222, v222, v223\n\t"
"v_cvt_pk_bf16_f32 v223, v224, v225\n\t"
"v_mfma_f32_16x16x32_bf16 v[150:153], a[248:251], a[192:195], v[150:153]\n\t"
"v_cvt_pk_bf16_f32 v224, v226, v227\n\t"
"v_mfma_f32_16x16x32_bf16 v[150:153], a[64:67], a[196:199], v[150:153]\n\t"
"v_cvt_pk_bf16_f32 v225, v228, v229\n\t"
"s_waitcnt lgkmcnt(4)\n\t"
"v_mfma_f32_16x16x32_bf16 v[230:233], v[230:233], a[52:55], 0\n\t"
"s_waitcnt lgkmcnt(2)\n\t"
"v_mfma_f32_16x16x32_bf16 v[234:237], v[234:237], a[52:55], 0\n\t"
"s_mov_b64 exec, s[44:45]\n\t"
"ds_write_b128 v240, v[222:225] offset:1536\n\t"
"s_mov_b64 exec, -1\n\t"
"v_mfma_f32_16x16x32_bf16 v[154:157], a[244:247], a[192:195], v[154:157]\n\t"
"v_mfma_f32_16x16x32_bf16 v[154:157], a[248:251], a[196:199], v[154:157]\n\t"
"v_mfma_f32_16x16x32_bf16 v[154:157], a[64:67], a[200:203], v[154:157]\n\t"
"v_mfma_f32_16x16x32_bf16 v[158:161], a[244:247], a[196:199], v[158:161]\n\t"
"v_cvt_pk_bf16_f32 v230, v230, v231\n\t"
"v_cvt_pk_bf16_f32 v231, v232, v233\n\t"
"v_mfma_f32_16x16x32_bf16 v[158:161], a[248:251], a[200:203], v[158:161]\n\t"
"v_cvt_pk_bf16_f32 v232, v234, v235\n\t"
"v_mfma_f32_16x16x32_bf16 v[158:161], a[64:67], a[204:207], v[158:161]\n\t"
"v_cvt_pk_bf16_f32 v233, v236, v237\n\t"
"s_waitcnt vmcnt(33)\n\t"
"v_mfma_f32_16x16x32_bf16 v[162:165], a[44:47], a[168:171], v[162:165]\n\t"
"v_mfma_f32_16x16x32_bf16 v[162:165], a[48:51], a[172:175], v[162:165]\n\t"
"v_mfma_f32_16x16x32_bf16 v[162:165], a[68:71], a[176:179], v[162:165]\n\t"
"s_mov_b64 exec, s[44:45]\n\t"
"ds_write_b128 v240, v[230:233] offset:3072\n\t"
"s_mov_b64 exec, -1\n\t"
"v_mfma_f32_16x16x32_bf16 v[166:169], a[44:47], a[172:175], v[166:169]\n\t"
"v_mfma_f32_16x16x32_bf16 v[166:169], a[48:51], a[176:179], v[166:169]\n\t"
"v_mfma_f32_16x16x32_bf16 v[166:169], a[68:71], a[180:183], v[166:169]\n\t"
"v_mfma_f32_16x16x32_bf16 v[170:173], a[44:47], a[176:179], v[170:173]\n\t"
"v_mfma_f32_16x16x32_bf16 v[170:173], a[48:51], a[180:183], v[170:173]\n\t"
"v_mfma_f32_16x16x32_bf16 v[170:173], a[68:71], a[184:187], v[170:173]\n\t"
"v_mfma_f32_16x16x32_bf16 v[174:177], a[44:47], a[180:183], v[174:177]\n\t"
"v_mfma_f32_16x16x32_bf16 v[174:177], a[48:51], a[184:187], v[174:177]\n\t"
"v_mfma_f32_16x16x32_bf16 v[174:177], a[68:71], a[188:191], v[174:177]\n\t"
"s_waitcnt lgkmcnt(0)\n\t"
"s_barrier\n\t"
"v_mfma_f32_16x16x32_bf16 v[178:181], a[44:47], a[184:187], v[178:181]\n\t"
"ds_read_b128 a[168:171], v242 offset:0\n\t"
"v_add_u32 v222, 67584, v248\n\t"
"v_mfma_f32_16x16x32_bf16 v[178:181], a[48:51], a[188:191], v[178:181]\n\t"
"ds_read_b128 a[172:175], v242 offset:512\n\t"
"v_add_u32 v223, 67584, v249\n\t"
"v_mfma_f32_16x16x32_bf16 v[178:181], a[68:71], a[192:195], v[178:181]\n\t"
"ds_read_b128 a[176:179], v242 offset:1024\n\t"
"ds_read_b128 a[180:183], v242 offset:1536\n\t"
"ds_read_b128 v[194:197], v252 offset:18432\n\t"
"v_mfma_f32_16x16x32_bf16 v[182:185], a[44:47], a[188:191], v[182:185]\n\t"
"v_add_u32 v224, 67584, v252\n\t"
"ds_read_b128 a[184:187], v242 offset:2048\n\t"
"v_mfma_f32_16x16x32_bf16 v[182:185], a[48:51], a[192:195], v[182:185]\n\t"
"ds_read_b128 v[198:201], v252 offset:19456\n\t"
"v_mfma_f32_16x16x32_bf16 v[182:185], a[68:71], a[196:199], v[182:185]\n\t"
"ds_read_b128 a[188:191], v242 offset:2560\n\t"
"ds_read_b128 v[202:205], v252 offset:20480\n\t"
"v_mfma_f32_16x16x32_bf16 v[186:189], a[44:47], a[192:195], v[186:189]\n\t"
"ds_read_b128 a[192:195], v242 offset:3072\n\t"
"v_mfma_f32_16x16x32_bf16 v[186:189], a[48:51], a[196:199], v[186:189]\n\t"
"v_mfma_f32_16x16x32_bf16 v[186:189], a[68:71], a[200:203], v[186:189]\n\t"
"ds_write_b128 v248, a[32:35] offset:30720\n\t"
"v_mfma_f32_16x16x32_bf16 v[190:193], a[44:47], a[196:199], v[190:193]\n\t"
"ds_read_b128 a[196:199], v242 offset:3584\n\t"
"v_mfma_f32_16x16x32_bf16 v[190:193], a[48:51], a[200:203], v[190:193]\n\t"
"ds_read_b128 a[200:203], v242 offset:4096\n\t"
"v_mfma_f32_16x16x32_bf16 v[190:193], a[68:71], a[204:207], v[190:193]\n\t"
"s_add_u32 s29, s22, 1180032\n\t"
"s_waitcnt lgkmcnt(8)\n\t"
"buffer_store_dwordx4 v[194:197], v247, s[16:19], s29 offen offset:0 nt\n\t"
"s_waitcnt vmcnt(31)\n\t"
"v_mfma_f32_16x16x32_bf16 v[0:3], a[72:75], a[168:171], v[0:3]\n\t"
"ds_read_b128 a[204:207], v242 offset:4608\n\t"
"v_mfma_f32_16x16x32_bf16 v[0:3], a[76:79], a[172:175], v[0:3]\n\t"
"v_mfma_f32_16x16x32_bf16 v[0:3], v[206:209], a[176:179], v[0:3]\n\t"
"s_add_u32 s29, s22, 1180032\n\t"
"s_waitcnt lgkmcnt(7)\n\t"
"buffer_store_dwordx4 v[198:201], v247, s[16:19], s29 offen offset:128 nt\n\t"
"v_mfma_f32_16x16x32_bf16 v[4:7], a[72:75], a[172:175], v[4:7]\n\t"
"v_mfma_f32_16x16x32_bf16 v[4:7], a[76:79], a[176:179], v[4:7]\n\t"
"v_mfma_f32_16x16x32_bf16 v[4:7], v[206:209], a[180:183], v[4:7]\n\t"
"ds_write_b64 v249, a[160:161] offset:30720\n\t"
"v_mfma_f32_16x16x32_bf16 v[8:11], a[72:75], a[176:179], v[8:11]\n\t"
"v_cvt_pk_bf16_f32 v0, v0, v1\n\t"
"v_cvt_pk_bf16_f32 v1, v2, v3\n\t"
"v_mfma_f32_16x16x32_bf16 v[8:11], a[76:79], a[180:183], v[8:11]\n\t"
"v_mfma_f32_16x16x32_bf16 v[8:11], v[206:209], a[184:187], v[8:11]\n\t"
"s_add_u32 s29, s22, 1180032\n\t"
"s_waitcnt lgkmcnt(6)\n\t"
"buffer_store_dwordx4 v[202:205], v247, s[16:19], s29 offen offset:256 nt\n\t"
"v_mfma_f32_16x16x32_bf16 v[12:15], a[72:75], a[180:183], v[12:15]\n\t"
"v_cvt_pk_bf16_f32 v4, v4, v5\n\t"
"v_cvt_pk_bf16_f32 v5, v6, v7\n\t"
"v_mfma_f32_16x16x32_bf16 v[12:15], a[76:79], a[184:187], v[12:15]\n\t"
"v_mfma_f32_16x16x32_bf16 v[12:15], v[206:209], a[188:191], v[12:15]\n\t"
"s_waitcnt lgkmcnt(5)\n\t"
"v_mfma_f32_16x16x32_bf16 v[16:19], a[72:75], a[184:187], v[16:19]\n\t"
"v_cvt_pk_bf16_f32 v8, v8, v9\n\t"
"v_cvt_pk_bf16_f32 v9, v10, v11\n\t"
"v_mfma_f32_16x16x32_bf16 v[16:19], a[76:79], a[188:191], v[16:19]\n\t"
"v_mfma_f32_16x16x32_bf16 v[16:19], v[206:209], a[192:195], v[16:19]\n\t"
"s_waitcnt lgkmcnt(3)\n\t"
"v_mfma_f32_16x16x32_bf16 v[20:23], a[72:75], a[188:191], v[20:23]\n\t"
"v_cvt_pk_bf16_f32 v12, v12, v13\n\t"
"v_mfma_f32_16x16x32_bf16 v[20:23], a[76:79], a[192:195], v[20:23]\n\t"
"v_cvt_pk_bf16_f32 v13, v14, v15\n\t"
"v_mfma_f32_16x16x32_bf16 v[20:23], v[206:209], a[196:199], v[20:23]\n\t"
"s_waitcnt lgkmcnt(2)\n\t"
"v_mfma_f32_16x16x32_bf16 v[24:27], a[72:75], a[192:195], v[24:27]\n\t"
"v_cvt_pk_bf16_f32 v16, v16, v17\n\t"
"v_mfma_f32_16x16x32_bf16 v[24:27], a[76:79], a[196:199], v[24:27]\n\t"
"v_cvt_pk_bf16_f32 v17, v18, v19\n\t"
"v_mfma_f32_16x16x32_bf16 v[24:27], v[206:209], a[200:203], v[24:27]\n\t"
"ds_write_b128 v248, a[36:39] offset:36864\n\t"
"s_waitcnt lgkmcnt(2)\n\t"
"v_mfma_f32_16x16x32_bf16 v[28:31], a[72:75], a[196:199], v[28:31]\n\t"
"v_mfma_f32_16x16x32_bf16 v[28:31], a[76:79], a[200:203], v[28:31]\n\t"
"v_mfma_f32_16x16x32_bf16 v[28:31], v[206:209], a[204:207], v[28:31]\n\t"
"v_cvt_pk_bf16_f32 v20, v20, v21\n\t"
"s_waitcnt vmcnt(29)\n\t"
"v_mfma_f32_16x16x32_bf16 v[32:35], a[112:115], a[168:171], v[32:35]\n\t"
"v_mfma_f32_16x16x32_bf16 v[32:35], a[80:83], a[172:175], v[32:35]\n\t"
"v_cvt_pk_bf16_f32 v21, v22, v23\n\t"
"v_mfma_f32_16x16x32_bf16 v[32:35], a[212:215], a[176:179], v[32:35]\n\t"
"v_cvt_pk_bf16_f32 v24, v24, v25\n\t"
"v_mfma_f32_16x16x32_bf16 v[36:39], a[112:115], a[172:175], v[36:39]\n\t"
"v_mfma_f32_16x16x32_bf16 v[36:39], a[80:83], a[176:179], v[36:39]\n\t"
"v_cvt_pk_bf16_f32 v25, v26, v27\n\t"
"v_mfma_f32_16x16x32_bf16 v[36:39], a[212:215], a[180:183], v[36:39]\n\t"
"ds_write_b64 v249, a[162:163] offset:36864\n\t"
"v_mfma_f32_16x16x32_bf16 v[40:43], a[112:115], a[176:179], v[40:43]\n\t"
"v_mfma_f32_16x16x32_bf16 v[40:43], a[80:83], a[180:183], v[40:43]\n\t"
"v_mfma_f32_16x16x32_bf16 v[40:43], a[212:215], a[184:187], v[40:43]\n\t"
"v_cvt_pk_bf16_f32 v28, v28, v29\n\t"
"v_mfma_f32_16x16x32_bf16 v[44:47], a[112:115], a[180:183], v[44:47]\n\t"
"v_cvt_pk_bf16_f32 v29, v30, v31\n\t"
"v_mfma_f32_16x16x32_bf16 v[44:47], a[80:83], a[184:187], v[44:47]\n\t"
"v_mfma_f32_16x16x32_bf16 v[44:47], a[212:215], a[188:191], v[44:47]\n\t"
"v_cvt_pk_bf16_f32 v2, v32, v33\n\t"
"s_waitcnt lgkmcnt(0)\n\t"
"s_barrier\n\t"
"v_mfma_f32_16x16x32_bf16 v[48:51], a[112:115], a[184:187], v[48:51]\n\t"
"ds_read_b128 v[194:197], v252 offset:30720\n\t"
"v_cvt_pk_bf16_f32 v3, v34, v35\n\t"
"v_mfma_f32_16x16x32_bf16 v[48:51], a[80:83], a[188:191], v[48:51]\n\t"
"ds_read_b128 v[198:201], v252 offset:31744\n\t"
"v_mfma_f32_16x16x32_bf16 v[48:51], a[212:215], a[192:195], v[48:51]\n\t"
"ds_read_b128 v[202:205], v252 offset:32768\n\t"
"v_cvt_pk_bf16_f32 v6, v36, v37\n\t"
"v_mfma_f32_16x16x32_bf16 v[52:55], a[112:115], a[188:191], v[52:55]\n\t"
"v_cvt_pk_bf16_f32 v7, v38, v39\n\t"
"v_mfma_f32_16x16x32_bf16 v[52:55], a[80:83], a[192:195], v[52:55]\n\t"
"v_cvt_pk_bf16_f32 v10, v40, v41\n\t"
"v_mfma_f32_16x16x32_bf16 v[52:55], a[212:215], a[196:199], v[52:55]\n\t"
"v_cvt_pk_bf16_f32 v11, v42, v43\n\t"
"v_mfma_f32_16x16x32_bf16 v[56:59], a[112:115], a[192:195], v[56:59]\n\t"
"v_cvt_pk_bf16_f32 v14, v44, v45\n\t"
"v_mfma_f32_16x16x32_bf16 v[56:59], a[80:83], a[196:199], v[56:59]\n\t"
"v_cvt_pk_bf16_f32 v15, v46, v47\n\t"
"v_mfma_f32_16x16x32_bf16 v[56:59], a[212:215], a[200:203], v[56:59]\n\t"
"v_cvt_pk_bf16_f32 v18, v48, v49\n\t"
"v_mfma_f32_16x16x32_bf16 v[60:63], a[112:115], a[196:199], v[60:63]\n\t"
"v_cvt_pk_bf16_f32 v19, v50, v51\n\t"
"v_mfma_f32_16x16x32_bf16 v[60:63], a[80:83], a[200:203], v[60:63]\n\t"
"v_cvt_pk_bf16_f32 v22, v52, v53\n\t"
"v_mfma_f32_16x16x32_bf16 v[60:63], a[212:215], a[204:207], v[60:63]\n\t"
"s_add_u32 s29, s22, 1376640\n\t"
"s_waitcnt lgkmcnt(2)\n\t"
"buffer_store_dwordx4 v[194:197], v247, s[16:19], s29 offen offset:0 nt\n\t"
"s_waitcnt vmcnt(25)\n\t"
"v_mfma_f32_16x16x32_bf16 v[64:67], a[216:219], a[168:171], v[64:67]\n\t"
"ds_read_b128 a[168:171], v242 offset:9216\n\t"
"v_mfma_f32_16x16x32_bf16 v[64:67], a[40:43], a[172:175], v[64:67]\n\t"
"v_cvt_pk_bf16_f32 v23, v54, v55\n\t"
"v_cvt_pk_bf16_f32 v26, v56, v57\n\t"
"v_mfma_f32_16x16x32_bf16 v[64:67], a[252:255], a[176:179], v[64:67]\n\t"
"v_cvt_pk_bf16_f32 v27, v58, v59\n\t"
"v_mfma_f32_16x16x32_bf16 v[68:71], a[216:219], a[172:175], v[68:71]\n\t"
"ds_read_b128 a[172:175], v242 offset:9728\n\t"
"v_cvt_pk_bf16_f32 v30, v60, v61\n\t"
"v_mfma_f32_16x16x32_bf16 v[68:71], a[40:43], a[176:179], v[68:71]\n\t"
"v_cvt_pk_bf16_f32 v31, v62, v63\n\t"
"v_mfma_f32_16x16x32_bf16 v[68:71], a[252:255], a[180:183], v[68:71]\n\t"
"s_add_u32 s29, s22, 1376640\n\t"
"s_waitcnt lgkmcnt(3)\n\t"
"buffer_store_dwordx4 v[198:201], v247, s[16:19], s29 offen offset:128 nt\n\t"
"v_mfma_f32_16x16x32_bf16 v[72:75], a[216:219], a[176:179], v[72:75]\n\t"
"v_cvt_pk_bf16_f32 v64, v64, v65\n\t"
"v_cvt_pk_bf16_f32 v65, v66, v67\n\t"
"v_mfma_f32_16x16x32_bf16 v[72:75], a[40:43], a[180:183], v[72:75]\n\t"
"ds_read_b128 a[176:179], v242 offset:10240\n\t"
"v_mfma_f32_16x16x32_bf16 v[72:75], a[252:255], a[184:187], v[72:75]\n\t"
"s_add_u32 s29, s22, 1376640\n\t"
"s_waitcnt lgkmcnt(3)\n\t"
"buffer_store_dwordx4 v[202:205], v247, s[16:19], s29 offen offset:256 nt\n\t"
"v_mfma_f32_16x16x32_bf16 v[76:79], a[216:219], a[180:183], v[76:79]\n\t"
"v_cvt_pk_bf16_f32 v68, v68, v69\n\t"
"v_cvt_pk_bf16_f32 v69, v70, v71\n\t"
"v_mfma_f32_16x16x32_bf16 v[76:79], a[40:43], a[184:187], v[76:79]\n\t"
"ds_read_b128 a[180:183], v242 offset:10752\n\t"
"v_mfma_f32_16x16x32_bf16 v[76:79], a[252:255], a[188:191], v[76:79]\n\t"
"ds_write_b128 v248, v[0:3] offset:18432\n\t"
"v_mfma_f32_16x16x32_bf16 v[80:83], a[216:219], a[184:187], v[80:83]\n\t"
"v_cvt_pk_bf16_f32 v72, v72, v73\n\t"
"v_cvt_pk_bf16_f32 v73, v74, v75\n\t"
"v_mfma_f32_16x16x32_bf16 v[80:83], a[40:43], a[188:191], v[80:83]\n\t"
"ds_read_b128 a[184:187], v242 offset:11264\n\t"
"v_mfma_f32_16x16x32_bf16 v[80:83], a[252:255], a[192:195], v[80:83]\n\t"
"ds_write_b64 v249, v[64:65] offset:18432\n\t"
"v_mfma_f32_16x16x32_bf16 v[84:87], a[216:219], a[188:191], v[84:87]\n\t"
"v_cvt_pk_bf16_f32 v76, v76, v77\n\t"
"v_cvt_pk_bf16_f32 v77, v78, v79\n\t"
"v_mfma_f32_16x16x32_bf16 v[84:87], a[40:43], a[192:195], v[84:87]\n\t"
"ds_read_b128 a[188:191], v242 offset:11776\n\t"
"v_mfma_f32_16x16x32_bf16 v[84:87], a[252:255], a[196:199], v[84:87]\n\t"
"ds_write_b128 v248, v[4:7] offset:24576\n\t"
"v_mfma_f32_16x16x32_bf16 v[88:91], a[216:219], a[192:195], v[88:91]\n\t"
"v_cvt_pk_bf16_f32 v80, v80, v81\n\t"
"v_cvt_pk_bf16_f32 v81, v82, v83\n\t"
"v_mfma_f32_16x16x32_bf16 v[88:91], a[40:43], a[196:199], v[88:91]\n\t"
"ds_read_b128 a[192:195], v242 offset:12288\n\t"
"v_mfma_f32_16x16x32_bf16 v[88:91], a[252:255], a[200:203], v[88:91]\n\t"
"ds_write_b64 v249, v[68:69] offset:24576\n\t"
"v_mfma_f32_16x16x32_bf16 v[92:95], a[216:219], a[196:199], v[92:95]\n\t"
"v_cvt_pk_bf16_f32 v84, v84, v85\n\t"
"v_cvt_pk_bf16_f32 v85, v86, v87\n\t"
"v_mfma_f32_16x16x32_bf16 v[92:95], a[40:43], a[200:203], v[92:95]\n\t"
"ds_read_b128 a[196:199], v242 offset:12800\n\t"
"v_mfma_f32_16x16x32_bf16 v[92:95], a[252:255], a[204:207], v[92:95]\n\t"
"ds_read_b128 a[200:203], v242 offset:13312\n\t"
"ds_read_b128 a[204:207], v242 offset:13824\n\t"
"s_waitcnt vmcnt(24) lgkmcnt(11)\n\t"
"v_mfma_f32_16x16x32_bf16 v[98:101], a[84:87], a[168:171], v[98:101]\n\t"
"v_cvt_pk_bf16_f32 v88, v88, v89\n\t"
"v_mfma_f32_16x16x32_bf16 v[98:101], a[88:91], a[172:175], v[98:101]\n\t"
"v_cvt_pk_bf16_f32 v89, v90, v91\n\t"
"v_mfma_f32_16x16x32_bf16 v[98:101], v[210:213], a[176:179], v[98:101]\n\t"
"s_waitcnt lgkmcnt(10)\n\t"
"v_mfma_f32_16x16x32_bf16 v[102:105], a[84:87], a[172:175], v[102:105]\n\t"
"v_cvt_pk_bf16_f32 v92, v92, v93\n\t"
"v_cvt_pk_bf16_f32 v93, v94, v95\n\t"
"v_mfma_f32_16x16x32_bf16 v[102:105], a[88:91], a[176:179], v[102:105]\n\t"
"v_mfma_f32_16x16x32_bf16 v[102:105], v[210:213], a[180:183], v[102:105]\n\t"
"s_waitcnt lgkmcnt(0)\n\t"
"s_barrier\n\t"
"v_mfma_f32_16x16x32_bf16 v[106:109], a[84:87], a[176:179], v[106:109]\n\t"
"ds_read_b128 v[194:197], v252 offset:18432\n\t"
"v_cvt_pk_bf16_f32 v98, v98, v99\n\t"
"v_mfma_f32_16x16x32_bf16 v[106:109], a[88:91], a[180:183], v[106:109]\n\t"
"ds_read_b128 v[198:201], v252 offset:19456\n\t"
"v_cvt_pk_bf16_f32 v99, v100, v101\n\t"
"v_mfma_f32_16x16x32_bf16 v[106:109], v[210:213], a[184:187], v[106:109]\n\t"
"ds_read_b128 v[202:205], v252 offset:20480\n\t"
"v_mfma_f32_16x16x32_bf16 v[110:113], a[84:87], a[180:183], v[110:113]\n\t"
"v_cvt_pk_bf16_f32 v102, v102, v103\n\t"
"v_cvt_pk_bf16_f32 v103, v104, v105\n\t"
"v_mfma_f32_16x16x32_bf16 v[110:113], a[88:91], a[184:187], v[110:113]\n\t"
"v_mfma_f32_16x16x32_bf16 v[110:113], v[210:213], a[188:191], v[110:113]\n\t"
"ds_write_b128 v248, v[8:11] offset:30720\n\t"
"v_mfma_f32_16x16x32_bf16 v[114:117], a[84:87], a[184:187], v[114:117]\n\t"
"v_cvt_pk_bf16_f32 v106, v106, v107\n\t"
"v_cvt_pk_bf16_f32 v107, v108, v109\n\t"
"v_mfma_f32_16x16x32_bf16 v[114:117], a[88:91], a[188:191], v[114:117]\n\t"
"v_mfma_f32_16x16x32_bf16 v[114:117], v[210:213], a[192:195], v[114:117]\n\t"
"ds_write_b128 v248, v[16:19] offset:43008\n\t"
"v_mfma_f32_16x16x32_bf16 v[118:121], a[84:87], a[188:191], v[118:121]\n\t"
"v_cvt_pk_bf16_f32 v110, v110, v111\n\t"
"v_cvt_pk_bf16_f32 v111, v112, v113\n\t"
"v_mfma_f32_16x16x32_bf16 v[118:121], a[88:91], a[192:195], v[118:121]\n\t"
"v_mfma_f32_16x16x32_bf16 v[118:121], v[210:213], a[196:199], v[118:121]\n\t"
"ds_write_b128 v248, v[24:27] offset:55296\n\t"
"v_mfma_f32_16x16x32_bf16 v[122:125], a[84:87], a[192:195], v[122:125]\n\t"
"v_cvt_pk_bf16_f32 v114, v114, v115\n\t"
"v_cvt_pk_bf16_f32 v115, v116, v117\n\t"
"v_mfma_f32_16x16x32_bf16 v[122:125], a[88:91], a[196:199], v[122:125]\n\t"
"v_mfma_f32_16x16x32_bf16 v[122:125], v[210:213], a[200:203], v[122:125]\n\t"
"s_add_u32 s29, s23, 0\n\t"
"s_waitcnt lgkmcnt(5)\n\t"
"buffer_store_dwordx4 v[194:197], v247, s[16:19], s29 offen offset:0 nt\n\t"
"v_mfma_f32_16x16x32_bf16 v[126:129], a[84:87], a[196:199], v[126:129]\n\t"
"v_cvt_pk_bf16_f32 v118, v118, v119\n\t"
"v_cvt_pk_bf16_f32 v119, v120, v121\n\t"
"v_mfma_f32_16x16x32_bf16 v[126:129], a[88:91], a[200:203], v[126:129]\n\t"
"v_mfma_f32_16x16x32_bf16 v[126:129], v[210:213], a[204:207], v[126:129]\n\t"
"s_add_u32 s29, s23, 0\n\t"
"s_waitcnt lgkmcnt(4)\n\t"
"buffer_store_dwordx4 v[198:201], v247, s[16:19], s29 offen offset:128 nt\n\t"
"ds_write_b64 v249, v[72:73] offset:30720\n\t"
"s_waitcnt vmcnt(21)\n\t"
"v_mfma_f32_16x16x32_bf16 v[130:133], v[214:217], a[168:171], v[130:133]\n\t"
"v_cvt_pk_bf16_f32 v122, v122, v123\n\t"
"v_cvt_pk_bf16_f32 v123, v124, v125\n\t"
"v_mfma_f32_16x16x32_bf16 v[130:133], a[120:123], a[172:175], v[130:133]\n\t"
"v_mfma_f32_16x16x32_bf16 v[130:133], a[92:95], a[176:179], v[130:133]\n\t"
"s_add_u32 s29, s23, 0\n\t"
"s_waitcnt lgkmcnt(4)\n\t"
"buffer_store_dwordx4 v[202:205], v247, s[16:19], s29 offen offset:256 nt\n\t"
"ds_write_b64 v249, v[80:81] offset:43008\n\t"
"v_mfma_f32_16x16x32_bf16 v[134:137], v[214:217], a[172:175], v[134:137]\n\t"
"v_cvt_pk_bf16_f32 v126, v126, v127\n\t"
"v_cvt_pk_bf16_f32 v127, v128, v129\n\t"
"v_mfma_f32_16x16x32_bf16 v[134:137], a[120:123], a[176:179], v[134:137]\n\t"
"v_mfma_f32_16x16x32_bf16 v[134:137], a[92:95], a[180:183], v[134:137]\n\t"
"ds_write_b64 v249, v[88:89] offset:55296\n\t"
"v_mfma_f32_16x16x32_bf16 v[138:141], v[214:217], a[176:179], v[138:141]\n\t"
"v_cvt_pk_bf16_f32 v100, v130, v131\n\t"
"v_cvt_pk_bf16_f32 v101, v132, v133\n\t"
"v_mfma_f32_16x16x32_bf16 v[138:141], a[120:123], a[180:183], v[138:141]\n\t"
"v_mfma_f32_16x16x32_bf16 v[138:141], a[92:95], a[184:187], v[138:141]\n\t"
"ds_write_b128 v248, v[12:15] offset:36864\n\t"
"v_mfma_f32_16x16x32_bf16 v[142:145], v[214:217], a[180:183], v[142:145]\n\t"
"v_cvt_pk_bf16_f32 v104, v134, v135\n\t"
"v_cvt_pk_bf16_f32 v105, v136, v137\n\t"
"v_mfma_f32_16x16x32_bf16 v[142:145], a[120:123], a[184:187], v[142:145]\n\t"
"v_mfma_f32_16x16x32_bf16 v[142:145], a[92:95], a[188:191], v[142:145]\n\t"
"ds_write_b128 v248, v[20:23] offset:49152\n\t"
"v_mfma_f32_16x16x32_bf16 v[146:149], v[214:217], a[184:187], v[146:149]\n\t"
"v_cvt_pk_bf16_f32 v108, v138, v139\n\t"
"v_cvt_pk_bf16_f32 v109, v140, v141\n\t"
"v_mfma_f32_16x16x32_bf16 v[146:149], a[120:123], a[188:191], v[146:149]\n\t"
"v_mfma_f32_16x16x32_bf16 v[146:149], a[92:95], a[192:195], v[146:149]\n\t"
"ds_write_b128 v248, v[28:31] offset:61440\n\t"
"v_mfma_f32_16x16x32_bf16 v[150:153], v[214:217], a[188:191], v[150:153]\n\t"
"v_cvt_pk_bf16_f32 v112, v142, v143\n\t"
"v_cvt_pk_bf16_f32 v113, v144, v145\n\t"
"v_mfma_f32_16x16x32_bf16 v[150:153], a[120:123], a[192:195], v[150:153]\n\t"
"v_mfma_f32_16x16x32_bf16 v[150:153], a[92:95], a[196:199], v[150:153]\n\t"
"v_mfma_f32_16x16x32_bf16 v[154:157], v[214:217], a[192:195], v[154:157]\n\t"
"s_nop 0\n\t"
"v_cvt_pk_bf16_f32 v116, v146, v147\n\t"
"v_cvt_pk_bf16_f32 v117, v148, v149\n\t"
"v_mfma_f32_16x16x32_bf16 v[154:157], a[120:123], a[196:199], v[154:157]\n\t"
"v_mfma_f32_16x16x32_bf16 v[154:157], a[92:95], a[200:203], v[154:157]\n\t"
"ds_write_b64 v249, v[76:77] offset:36864\n\t"
"v_mfma_f32_16x16x32_bf16 v[158:161], v[214:217], a[196:199], v[158:161]\n\t"
"v_cvt_pk_bf16_f32 v120, v150, v151\n\t"
"v_mfma_f32_16x16x32_bf16 v[158:161], a[120:123], a[200:203], v[158:161]\n\t"
"v_mfma_f32_16x16x32_bf16 v[158:161], a[92:95], a[204:207], v[158:161]\n\t"
"v_cvt_pk_bf16_f32 v121, v152, v153\n\t"
"s_waitcnt vmcnt(18)\n\t"
"v_mfma_f32_16x16x32_bf16 v[162:165], a[96:99], a[168:171], v[162:165]\n\t"
"v_cvt_pk_bf16_f32 v124, v154, v155\n\t"
"v_mfma_f32_16x16x32_bf16 v[162:165], a[100:103], a[172:175], v[162:165]\n\t"
"v_cvt_pk_bf16_f32 v125, v156, v157\n\t"
"v_mfma_f32_16x16x32_bf16 v[162:165], a[220:223], a[176:179], v[162:165]\n\t"
"v_mfma_f32_16x16x32_bf16 v[166:169], a[96:99], a[172:175], v[166:169]\n\t"
"v_cvt_pk_bf16_f32 v128, v158, v159\n\t"
"v_mfma_f32_16x16x32_bf16 v[166:169], a[100:103], a[176:179], v[166:169]\n\t"
"v_cvt_pk_bf16_f32 v129, v160, v161\n\t"
"v_mfma_f32_16x16x32_bf16 v[166:169], a[220:223], a[180:183], v[166:169]\n\t"
"ds_write_b64 v249, v[84:85] offset:49152\n\t"
"ds_write_b64 v249, v[92:93] offset:61440\n\t"
"v_mfma_f32_16x16x32_bf16 v[170:173], a[96:99], a[176:179], v[170:173]\n\t"
"v_mfma_f32_16x16x32_bf16 v[170:173], a[100:103], a[180:183], v[170:173]\n\t"
"v_mfma_f32_16x16x32_bf16 v[170:173], a[220:223], a[184:187], v[170:173]\n\t"
"v_cvt_pk_bf16_f32 v162, v162, v163\n\t"
"v_cvt_pk_bf16_f32 v163, v164, v165\n\t"
"v_mfma_f32_16x16x32_bf16 v[174:177], a[96:99], a[180:183], v[174:177]\n\t"
"v_mfma_f32_16x16x32_bf16 v[174:177], a[100:103], a[184:187], v[174:177]\n\t"
"v_cvt_pk_bf16_f32 v166, v166, v167\n\t"
"v_mfma_f32_16x16x32_bf16 v[174:177], a[220:223], a[188:191], v[174:177]\n\t"
"v_cvt_pk_bf16_f32 v167, v168, v169\n\t"
"s_waitcnt lgkmcnt(0)\n\t"
"s_barrier\n\t"
"v_mfma_f32_16x16x32_bf16 v[178:181], a[96:99], a[184:187], v[178:181]\n\t"
"ds_read_b128 v[194:197], v252 offset:30720\n\t"
"v_cvt_pk_bf16_f32 v170, v170, v171\n\t"
"v_mfma_f32_16x16x32_bf16 v[178:181], a[100:103], a[188:191], v[178:181]\n\t"
"ds_read_b128 v[206:209], v252 offset:43008\n\t"
"v_cvt_pk_bf16_f32 v171, v172, v173\n\t"
"v_mfma_f32_16x16x32_bf16 v[178:181], a[220:223], a[192:195], v[178:181]\n\t"
"ds_read_b128 a[0:3], v252 offset:55296\n\t"
"ds_read_b128 v[198:201], v252 offset:31744\n\t"
"ds_read_b128 v[210:213], v252 offset:44032\n\t"
"ds_read_b128 a[4:7], v252 offset:56320\n\t"
"v_mfma_f32_16x16x32_bf16 v[182:185], a[96:99], a[188:191], v[182:185]\n\t"
"ds_read_b128 v[202:205], v252 offset:32768\n\t"
"v_cvt_pk_bf16_f32 v174, v174, v175\n\t"
"v_mfma_f32_16x16x32_bf16 v[182:185], a[100:103], a[192:195], v[182:185]\n\t"
"ds_read_b128 v[214:217], v252 offset:45056\n\t"
"v_cvt_pk_bf16_f32 v175, v176, v177\n\t"
"v_mfma_f32_16x16x32_bf16 v[182:185], a[220:223], a[196:199], v[182:185]\n\t"
"ds_write_b128 v248, v[98:101] offset:18432\n\t"
"ds_write_b64 v249, v[162:163] offset:18432\n\t"
"ds_write_b128 v248, v[102:105] offset:24576\n\t"
"ds_write_b64 v249, v[166:167] offset:24576\n\t"
"ds_read_b128 a[8:11], v252 offset:57344\n\t"
"v_mfma_f32_16x16x32_bf16 v[186:189], a[96:99], a[192:195], v[186:189]\n\t"
"v_cvt_pk_bf16_f32 v178, v178, v179\n\t"
"v_cvt_pk_bf16_f32 v179, v180, v181\n\t"
"v_mfma_f32_16x16x32_bf16 v[186:189], a[100:103], a[196:199], v[186:189]\n\t"
"v_mfma_f32_16x16x32_bf16 v[186:189], a[220:223], a[200:203], v[186:189]\n\t"
"ds_write_b128 v222, v[106:109] offset:0\n\t"
"ds_write_b64 v223, v[170:171] offset:0\n\t"
"v_mfma_f32_16x16x32_bf16 v[190:193], a[96:99], a[196:199], v[190:193]\n\t"
"v_cvt_pk_bf16_f32 v182, v182, v183\n\t"
"v_cvt_pk_bf16_f32 v183, v184, v185\n\t"
"v_mfma_f32_16x16x32_bf16 v[190:193], a[100:103], a[200:203], v[190:193]\n\t"
"v_mfma_f32_16x16x32_bf16 v[190:193], a[220:223], a[204:207], v[190:193]\n\t"
"ds_write_b128 v222, v[110:113] offset:6144\n\t"
"ds_write_b64 v223, v[174:175] offset:6144\n\t"
"ds_write_b128 v222, v[114:117] offset:12288\n\t"
"ds_write_b64 v223, v[178:179] offset:12288\n\t"
"ds_write_b128 v222, v[118:121] offset:18432\n\t"
"ds_write_b64 v223, v[182:183] offset:18432\n\t"
"ds_write_b128 v222, v[122:125] offset:24576\n\t"
"v_cvt_pk_bf16_f32 v186, v186, v187\n\t"
"v_cvt_pk_bf16_f32 v187, v188, v189\n\t"
"ds_write_b64 v223, v[186:187] offset:24576\n\t"
"ds_write_b128 v222, v[126:129] offset:30720\n\t"
"v_cvt_pk_bf16_f32 v190, v190, v191\n\t"
"v_cvt_pk_bf16_f32 v191, v192, v193\n\t"
"ds_write_b64 v223, v[190:191] offset:30720\n\t"
"s_waitcnt lgkmcnt(0)\n\t"
"s_barrier\n\t"
"ds_read_b128 a[12:15], v252 offset:18432\n\t"
"ds_read_b128 a[16:19], v252 offset:19456\n\t"
"ds_read_b128 a[20:23], v252 offset:20480\n\t"
"ds_read_b128 a[24:27], v224 offset:0\n\t"
"ds_read_b128 a[28:31], v224 offset:1024\n\t"
"ds_read_b128 a[32:35], v224 offset:2048\n\t"
"ds_read_b128 a[36:39], v224 offset:12288\n\t"
"ds_read_b128 a[40:43], v224 offset:13312\n\t"
"ds_read_b128 a[44:47], v224 offset:14336\n\t"
"ds_read_b128 a[48:51], v224 offset:24576\n\t"
"ds_read_b128 a[52:55], v224 offset:25600\n\t"
"ds_read_b128 a[56:59], v224 offset:26624\n\t"
"s_add_u32 s29, s23, 393216\n\t"
"buffer_store_dwordx4 v[194:197], v247, s[16:19], s29 offen offset:0 nt\n\t"
"s_add_u32 s29, s23, 393216\n\t"
"buffer_store_dwordx4 v[198:201], v247, s[16:19], s29 offen offset:128 nt\n\t"
"s_add_u32 s29, s23, 393216\n\t"
"buffer_store_dwordx4 v[202:205], v247, s[16:19], s29 offen offset:256 nt\n\t"
"s_add_u32 s29, s23, 786432\n\t"
"buffer_store_dwordx4 v[206:209], v247, s[16:19], s29 offen offset:0 nt\n\t"
"s_add_u32 s29, s23, 786432\n\t"
"buffer_store_dwordx4 v[210:213], v247, s[16:19], s29 offen offset:128 nt\n\t"
"s_add_u32 s29, s23, 786432\n\t"
"buffer_store_dwordx4 v[214:217], v247, s[16:19], s29 offen offset:256 nt\n\t"
"s_add_u32 s29, s23, 1179648\n\t"
"buffer_store_dwordx4 a[0:3], v247, s[16:19], s29 offen offset:0 nt\n\t"
"s_add_u32 s29, s23, 1179648\n\t"
"buffer_store_dwordx4 a[4:7], v247, s[16:19], s29 offen offset:128 nt\n\t"
"s_add_u32 s29, s23, 1179648\n\t"
"buffer_store_dwordx4 a[8:11], v247, s[16:19], s29 offen offset:256 nt\n\t"
"s_add_u32 s29, s23, 1152\n\t"
"s_waitcnt lgkmcnt(11)\n\t"
"buffer_store_dwordx4 a[12:15], v247, s[16:19], s29 offen offset:0 nt\n\t"
"s_add_u32 s29, s23, 1152\n\t"
"s_waitcnt lgkmcnt(10)\n\t"
"buffer_store_dwordx4 a[16:19], v247, s[16:19], s29 offen offset:128 nt\n\t"
"s_add_u32 s29, s23, 1152\n\t"
"s_waitcnt lgkmcnt(9)\n\t"
"buffer_store_dwordx4 a[20:23], v247, s[16:19], s29 offen offset:256 nt\n\t"
"s_add_u32 s29, s23, 394368\n\t"
"s_waitcnt lgkmcnt(8)\n\t"
"buffer_store_dwordx4 a[24:27], v247, s[16:19], s29 offen offset:0 nt\n\t"
"s_add_u32 s29, s23, 394368\n\t"
"s_waitcnt lgkmcnt(7)\n\t"
"buffer_store_dwordx4 a[28:31], v247, s[16:19], s29 offen offset:128 nt\n\t"
"s_add_u32 s29, s23, 394368\n\t"
"s_waitcnt lgkmcnt(6)\n\t"
"buffer_store_dwordx4 a[32:35], v247, s[16:19], s29 offen offset:256 nt\n\t"
"s_add_u32 s29, s23, 787584\n\t"
"s_waitcnt lgkmcnt(5)\n\t"
"buffer_store_dwordx4 a[36:39], v247, s[16:19], s29 offen offset:0 nt\n\t"
"s_add_u32 s29, s23, 787584\n\t"
"s_waitcnt lgkmcnt(4)\n\t"
"buffer_store_dwordx4 a[40:43], v247, s[16:19], s29 offen offset:128 nt\n\t"
"s_add_u32 s29, s23, 787584\n\t"
"s_waitcnt lgkmcnt(3)\n\t"
"buffer_store_dwordx4 a[44:47], v247, s[16:19], s29 offen offset:256 nt\n\t"
"s_add_u32 s29, s23, 1180800\n\t"
"s_waitcnt lgkmcnt(2)\n\t"
"buffer_store_dwordx4 a[48:51], v247, s[16:19], s29 offen offset:0 nt\n\t"
"s_add_u32 s29, s23, 1180800\n\t"
"s_waitcnt lgkmcnt(1)\n\t"
"buffer_store_dwordx4 a[52:55], v247, s[16:19], s29 offen offset:128 nt\n\t"
"s_add_u32 s29, s23, 1180800\n\t"
"s_waitcnt lgkmcnt(0)\n\t"
"buffer_store_dwordx4 a[56:59], v247, s[16:19], s29 offen offset:256 nt\n\t"
"s_waitcnt vmcnt(0)\n\t"
"Lwino_end:\n\t"
  : "+{v205}"(dummy)
  : "s"((unsigned)pu), "s"((unsigned)(pu >> 32)), "s"((unsigned)px), "s"((unsigned)(px >> 32)), "s"((unsigned)py), "s"((unsigned)(py >> 32)),
    "s"(xbase), "s"(ybase), "s"(w), "s"((unsigned)(size_t)(__attribute__((address_space(3))) char*)smem), "s"(blockIdx.x), "s"((unsigned long long)lane_tab),
    "{v252}"(w * 3072 + lane * 16), "s"(medge), "s"((unsigned)pw), "s"((unsigned)(pw >> 32)), "s"(fso), "s"(fuo), "s"(fk0), "s"(fk5), "s"(fs8),
    "{v206}"(fwo), "{v207}"(flw), "{v208}"(fst), "{v209}"((lane >> 5) * 8)
  : "v0", "v1", "v2", "v3", "v4", "v5", "v6", "v7", "v8", "v9", "v10", "v11", "v12", "v13", "v14", "v15", "v16", "v17", "v18", "v19", "v20", "v21", "v22", "v23", "v24", "v25", "v26", "v27", "v28", "v29", "v30", "v31", "v32", "v33", "v34", "v35", "v36", "v37", "v38", "v39", "v40", "v41", "v42", "v43", "v44", "v45", "v46", "v47", "v48", "v49", "v50", "v51", "v52", "v53", "v54", "v55", "v56", "v57", "v58", "v59", "v60", "v61", "v62", "v63", "v64", "v65", "v66", "v67", "v68", "v69", "v70", "v71", "v72", "v73", "v74", "v75", "v76", "v77", "v78", "v79", "v80", "v81", "v82", "v83", "v84", "v85", "v86", "v87", "v88", "v89", "v90", "v91", "v92", "v93", "v94", "v95", "v96", "v97", "v98", "v99", "v100", "v101", "v102", "v103", "v104", "v105", "v106", "v107", "v108", "v109", "v110", "v111", "v112", "v113", "v114", "v115", "v116", "v117", "v118", "v119", "v120", "v121", "v122", "v123", "v124", "v125", "v126", "v127", "v128", "v129", "v130", "v131", "v132", "v133", "v134", "v135", "v136", "v137", "v138", "v139", "v140", "v141", "v142", "v143", "v144", "v145", "v146", "v147", "v148", "v149", "v150", "v151", "v152", "v153", "v154", "v155", "v156", "v157", "v158", "v159", "v160", "v161", "v162", "v163", "v164", "v165", "v166", "v167", "v168", "v169", "v170", "v171", "v172", "v173", "v174", "v175", "v176", "v177", "v178", "v179", "v180", "v181", "v182", "v183", "v184", "v185", "v186", "v187", "v188", "v189", "v190", "v191", "v192", "v193", "v194", "v195", "v196", "v197", "v198", "v199", "v200", "v201", "v202", "v203", "v204", "v210", "v211", "v212", "v213", "v214", "v215", "v216", "v217", "v218", "v219", "v220", "v221", "v222", "v223", "v224", "v225", "v226", "v227", "v228", "v229", "v230", "v231", "v232", "v233", "v234", "v235", "v236", "v237", "v238", "v239", "v240", "v241", "v242", "v243", "v244", "v245", "v246", "v247", "v248", "v249", "v250", "v251", "v253", "v254", "v255", "a0", "a1", "a2", "a3", "a4", "a5", "a6", "a7", "a8", "a9", "a10", "a11", "a12", "a13", "a14", "a15", "a16", "a17", "a18", "a19", "a20", "a21", "a22", "a23", "a24", "a25", "a26", "a27", "a28", "a29", "a30", "a31", "a32", "a33", "a34", "a35", "a36", "a37", "a38", "a39", "a40", "a41", "a42", "a43", "a44", "a45", "a46", "a47", "a48", "a49", "a50", "a51", "a52", "a53", "a54", "a55", "a56", "a57", "a58", "a59", "a60", "a61", "a62", "a63", "a64", "a65", "a66", "a67", "a68", "a69", "a70", "a71", "a72", "a73", "a74", "a75", "a76", "a77", "a78", "a79", "a80", "a81", "a82", "a83", "a84", "a85", "a86", "a87", "a88", "a89", "a90", "a91", "a92", "a93", "a94", "a95", "a96", "a97", "a98", "a99", "a100", "a101", "a102", "a103", "a104", "a105", "a106", "a107", "a108", "a109", "a110", "a111", "a112", "a113", "a114", "a115", "a116", "a117", "a118", "a119", "a120", "a121", "a122", "a123", "a124", "a125", "a126", "a127", "a128", "a129", "a130", "a131", "a132", "a133", "a134", "a135", "a136", "a137", "a138", "a139", "a140", "a141", "a142", "a143", "a144", "a145", "a146", "a147", "a148", "a149", "a150", "a151", "a152", "a153", "a154", "a155", "a156", "a157", "a158", "a159", "a160", "a161", "a162", "a163", "a164", "a165", "a166", "a167", "a168", "a169", "a170", "a171", "a172", "a173", "a174", "a175", "a176", "a177", "a178", "a179", "a180", "a181", "a182", "a183", "a184", "a185", "a186", "a187", "a188", "a189", "a190", "a191", "a192", "a193", "a194", "a195", "a196", "a197", "a198", "a199", "a200", "a201", "a202", "a203", "a204", "a205", "a206", "a207", "a208", "a209", "a210", "a211", "a212", "a213", "a214", "a215", "a216", "a217", "a218", "a219", "a220", "a221", "a222", "a223", "a224", "a225", "a226", "a227", "a228", "a229", "a230", "a231", "a232", "a233", "a234", "a235", "a236", "a237", "a238", "a239", "a240", "a241", "a242", "a243", "a244", "a245", "a246", "a247", "a248", "a249", "a250", "a251", "a252", "a253", "a254", "a255", "s8", "s9", "s10", "s11", "s12", "s13", "s14", "s15", "s16", "s17", "s18", "s19", "s20", "s21", "s22", "s23", "s24", "s25", "s26", "s27", "s28", "s29", "s30", "s31", "s32", "s33", "s34", "s35", "s36", "s37", "s38", "s39", "s40", "s41", "s42", "s43", "s44", "s45", "s46", "s47", "s48", "s49", "s50", "s51", "s52", "s53", "s54", "s55", "s56", "s57", "s58", "s59", "s60", "s61", "s62", "s63", "s64", "s65", "s66", "s67", "s68", "s69", "s70", "s71", "s72", "s73", "s74", "scc", "vcc", "memory");
}

extern "C" void kernel_launch(void* const* d_in, const int* in_sizes, int n_in, void* d_out, int out_size, void* d_ws,
                              size_t ws_size, hipStream_t stream) {
  wino_main_kernel<<<256, 256, 0, stream>>>((const char*)d_in[0], (char*)d_ws, (char*)d_out, (const char*)d_in[1]);
}
```

```cpp
#include <hip/hip_runtime.h>
#include <hip/hip_bf16.h>
#include <stdint.h>

#define IMG_W 512
#define CH 192
#define PXB 384
#define ROWB (IMG_W * PXB)
#define U_BYTES 1327104

__device__ const __attribute__((aligned(1024))) unsigned lane_tab[5888] = {
  0xa800,0xa820,0x0,0x3ea80a81,0xa808,0xa828,0x40,0x3ea80a81,0xa810,0xa830,0x90,0x3ea80a81,0xa818,0xa838,0xd0,0x3ea80a81,
  0xa920,0xa900,0x2400,0xbebd0bd1,0xa928,0xa908,0x2440,0xbebd0bd1,0xa930,0xa910,0x2490,0xbebd0bd1,0xa938,0xa918,0x24d0,0xbebd0bd1,
  0xa840,0xa860,0x200,0x3ea80a81,0xa848,0xa868,0x240,0x3ea80a81,0xa850,0xa870,0x290,0x3ea80a81,0xa858,0xa878,0x2d0,0x3ea80a81,
  0xa960,0xa940,0x200,0x3ea80a81,0xa968,0xa948,0x240,0x3ea80a81,0xa970,0xa950,0x290,0x3ea80a81,0xa978,0xa958,0x2d0,0x3ea80a81,
  0xea80,0xeaa0,0x10,0x3ea80a81,0xea88,0xeaa8,0x50,0x3ea80a81,0xea90,0xeab0,0x80,0x3ea80a81,0xea98,0xeab8,0xc0,0x3ea80a81,
  0xa9a0,0xa980,0x2410,0xbebd0bd1,0xa9a8,0xa988,0x2450,0xbebd0bd1,0xa9b0,0xa990,0x2480,0xbebd0bd1,0xa9b8,0xa998,0x24c0,0xbebd0bd1,
  0xc0c0,0xc0e0,0x210,0x3ea80a81,0xc0c8,0xc0e8,0x250,0x3ea80a81,0xc0d0,0xc0f0,0x280,0x3ea80a81,0xc0d8,0xc0f8,0x2c0,0x3ea80a81,
  0xc1e0,0xc1c0,0x210,0x3ea80a81,0xc1e8,0xc1c8,0x250,0x3ea80a81,0xc1f0,0xc1d0,0x280,0x3ea80a81,0xc1f8,0xc1d8,0x2c0,0x3ea80a81,
  0xae00,0xae20,0x20,0x3ea80a81,0xae08,0xae28,0x60,0x3ea80a81,0xae10,0xae30,0xb0,0x3ea80a81,0xae18,0xae38,0xf0,0x3ea80a81,
  0xeb20,0xeb00,0x2420,0xbebd0bd1,0xeb28,0xeb08,0x2460,0xbebd0bd1,0xeb30,0xeb10,0x24b0,0xbebd0bd1,0xeb38,0xeb18,0x24f0,0xbebd0bd1,
  0xc040,0xc060,0x220,0x3ea80a81,0xc048,0xc068,0x260,0x3ea80a81,0xc050,0xc070,0x2b0,0x3ea80a81,0xc058,0xc078,0x2f0,0x3ea80a81,
  0xc160,0xc140,0x220,0x3ea80a81,0xc168,0xc148,0x260,0x3ea80a81,0xc170,0xc150,0x2b0,0x3ea80a81,0xc178,0xc158,0x2f0,0x3ea80a81,
  0xa880,0xa8a0,0x30,0x3ea80a81,0xa888,0xa8a8,0x70,0x3ea80a81,0xa890,0xa8b0,0xa0,0x3ea80a81,0xa898,0xa8b8,0xe0,0x3ea80a81,
  0xa8c0,0xa8e0,0x2430,0xbebd0bd1,0xa8c8,0xa8e8,0x2470,0xbebd0bd1,0xa8d0,0xa8f0,0x24a0,0xbebd0bd1,0xa8d8,0xa8f8,0x24e0,0xbebd0bd1,
  0xa9e0,0xa9c0,0x230,0x3ea80a81,0xa9e8,0xa9c8,0x270,0x3ea80a81,0xa9f0,0xa9d0,0x2a0,0x3ea80a81,0xa9f8,0xa9d8,0x2e0,0x3ea80a81,
  0xae00,0xae20,0x230,0x3ea80a81,0xae08,0xae28,0x270,0x3ea80a81,0xae10,0xae30,0x2a0,0x3ea80a81,0xae18,0xae38,0x2e0,0x3ea80a81,
  0x0,0xc042,0x0,0x0,0x0,0xc0420000,0x0,0x0,0x0,0x0,0x0,0xc042,0x0,0x0,0x0,0xc0420000,
  0x3f903f90,0x0,0x0,0x0,0x3f900000,0x0,0x3f90,0x0,0x0,0x0,0x3f903f90,0x0,0x0,0x0,0x3f900000,0x0,
  0x0,0x0,0x0,0x0,0x0,0x0,0x0,0x0,0x0,0x0,0x0,0x0,0x0,0x0,0x0,0x0,
  0x0,0x0,0x0,0x0,0x0,0x0,0x0,0x0,0x0,0x0,0x0,0x0,0x0,0x0,0x0,0x0,
  0x0,0x0,0x3f90,0x0,0x0,0x0,0x0,0x0,0x0,0x0,0x0,0x0,0x0,0x0,0x0,0x0,
  0x0,0x0,0x0,0x0,0xc0420000,0x0,0x0,0x0,0x0,0x0,0x0,0x0,0x0,0x0,0xc0420000,0x0,
  0x0,0x3f90,0x0,0x0,0x0,0x3f903f90,0x0,0x0,0x0,0x3f900000,0x0,0x3f90,0x0,0x0,0x0,0x3f903f90,
  0x0,0x0,0x0,0x0,0x0,0x0,0x0,0x0,0x0,0x0,0x0,0x0,0x0,0x0,0x0,0x0,
  0x0,0x0,0x0,0x0,0x0,0x0,0x0,0x0,0x0,0x0,0x0,0x0,0x0,0x0,0x0,0x0,
  0x0,0x0,0x0,0x0,0x0,0x0,0x0,0x0,0x0,0x0,0x0,0x0,0x0,0x0,0x3f90,0x0,
  0x0,0xc042,0x3f900000,0x0,0x0,0xc0420000,0x0,0x0,0x0,0x0,0x0,0xc042,0x0,0x0,0x0,0xc0420000,
  0x0,0x0,0x0,0x0,0x0,0x0,0x0,0x0,0x0,0x0,0x0,0x0,0x0,0x0,0x0,0x0,
  0x3f900000,0x0,0x0,0x0,0x3f900000,0x3f90,0x0,0x0,0x0,0x3f90,0x3f900000,0x0,0x0,0x0,0x3f900000,0x3f90,
  0xc042,0x0,0x0,0x0,0x0,0x0,0x0,0x0,0x0,0x0,0xc042,0x0,0x0,0x0,0x0,0x0,
  0x0,0x0,0x0,0x0,0x0,0x0,0x0,0x0,0x0,0x0,0x0,0x0,0x0,0x0,0x0,0x0,
  0x0,0x0,0x0,0x0,0x0,0x0,0x0,0x0,0x0,0x0,0x0,0x0,0x0,0x0,0x0,0x0,
  0x0,0x0,0xa800,0x0,0x40,0x40,0xa808,0x10,0x90,0x90,0xa810,0x20,0xd0,0xd0,0xa818,0x30,
  0x120,0x120,0xa840,0x180,0x160,0x160,0xa848,0x190,0x1b0,0x1b0,0xa850,0x1a0,0x1f0,0x1f0,0xa858,0x1b0,
  0x1000,0x2408,0xa880,0x300,0x1040,0x2448,0xa888,0x310,0x1090,0x2498,0xa890,0x320,0x10d0,0x24d8,0xa898,0x330,
  0x1120,0x2528,0xa8c0,0x480,0x1160,0x2568,0xa8c8,0x490,0x11b0,0x25b8,0xa8d0,0x4a0,0x11f0,0x25f8,0xa8d8,0x4b0,
  0x10,0x10,0xa920,0x620,0x50,0x50,0xa928,0x630,0x80,0x80,0xa930,0x600,0xc0,0xc0,0xa938,0x610,
  0x130,0x130,0xa960,0x7a0,0x170,0x170,0xa968,0x7b0,0x1a0,0x1a0,0xa970,0x780,0x1e0,0x1e0,0xa978,0x790,
  0x1010,0x2418,0xa9a0,0x920,0x1050,0x2458,0xa9a8,0x930,0x1080,0x2488,0xa9b0,0x900,0x10c0,0x24c8,0xa9b8,0x910,
  0x1130,0x2538,0xa9e0,0xaa0,0x1170,0x2578,0xa9e8,0xab0,0x11a0,0x25a8,0xa9f0,0xa80,0x11e0,0x25e8,0xa9f8,0xa90,
  0x20,0x20,0xae00,0x40,0x60,0x60,0xae08,0x50,0xb0,0xb0,0xae10,0x60,0xf0,0xf0,0xae18,0x70,
  0x100,0x100,0xae40,0x1c0,0x140,0x140,0xae48,0x1d0,0x190,0x190,0xae50,0x1e0,0x1d0,0x1d0,0xae58,0x1f0,
  0x1020,0x2428,0xae80,0x340,0x1060,0x2468,0xae88,0x350,0x10b0,0x24b8,0xae90,0x360,0x10f0,0x24f8,0xae98,0x370,
  0x1100,0x2508,0xaec0,0x4c0,0x1140,0x2548,0xaec8,0x4d0,0x1190,0x2598,0xaed0,0x4e0,0x11d0,0x25d8,0xaed8,0x4f0,
  0x30,0x30,0xaf20,0x660,0x70,0x70,0xaf28,0x670,0xa0,0xa0,0xaf30,0x640,0xe0,0xe0,0xaf38,0x650,
  0x110,0x110,0xaf60,0x7e0,0x150,0x150,0xaf68,0x7f0,0x180,0x180,0xaf70,0x7c0,0x1c0,0x1c0,0xaf78,0x7d0,
  0x1030,0x2438,0xafa0,0x960,0x1070,0x2478,0xafa8,0x970,0x10a0,0x24a8,0xafb0,0x940,0x10e0,0x24e8,0xafb8,0x950,
  0x1110,0x2518,0xafe0,0xae0,0x1150,0x2558,0xafe8,0xaf0,0x1180,0x2588,0xaff0,0xac0,0x11c0,0x25c8,0xaff8,0xad0,
  0xb400,0xb420,0x120,0x3ea80a81,0xb408,0xb428,0x160,0x3ea80a81,0xb410,0xb430,0x1b0,0x3ea80a81,0xb418,0xb438,0x1f0,0x3ea80a81,
  0xb440,0xb460,0x2520,0xbebd0bd1,0xb448,0xb468,0x2560,0xbebd0bd1,0xb450,0xb470,0x25b0,0xbebd0bd1,0xb458,0xb478,0x25f0,0xbebd0bd1,
  0xcc40,0xcc60,0x320,0x3ea80a81,0xcc48,0xcc68,0x360,0x3ea80a81,0xcc50,0xcc70,0x3b0,0x3ea80a81,0xcc58,0xcc78,0x3f0,0x3ea80a81,
  0xcd60,0xcd40,0x320,0x3ea80a81,0xcd68,0xcd48,0x360,0x3ea80a81,0xcd70,0xcd50,0x3b0,0x3ea80a81,0xcd78,0xcd58,0x3f0,0x3ea80a81,
  0xb5a0,0xb580,0x130,0x3ea80a81,0xb5a8,0xb588,0x170,0x3ea80a81,0xb5b0,0xb590,0x1a0,0x3ea80a81,0xb5b8,0xb598,0x1e0,0x3ea80a81,
  0xccc0,0xcce0,0x2530,0xbebd0bd1,0xccc8,0xcce8,0x2570,0xbebd0bd1,0xccd0,0xccf0,0x25a0,0xbebd0bd1,0xccd8,0xccf8,0x25e0,0xbebd0bd1,
  0xa9e0,0xa9c0,0x330,0x3ea80a81,0xa9e8,0xa9c8,0x370,0x3ea80a81,0xa9f0,0xa9d0,0x3a0,0x3ea80a81,0xa9f8,0xa9d8,0x3e0,0x3ea80a81,
  0xc1e0,0xc1c0,0x330,0x3ea80a81,0xc1e8,0xc1c8,0x370,0x3ea80a81,0xc1f0,0xc1d0,0x3a0,0x3ea80a81,0xc1f8,0xc1d8,0x3e0,0x3ea80a81,
  0xb520,0xb500,0x100,0x3ea80a81,0xb528,0xb508,0x140,0x3ea80a81,0xb530,0xb510,0x190,0x3ea80a81,0xb538,0xb518,0x1d0,0x3ea80a81,
  0xea40,0xea60,0x2500,0xbebd0bd1,0xea48,0xea68,0x2540,0xbebd0bd1,0xea50,0xea70,0x2590,0xbebd0bd1,0xea58,0xea78,0x25d0,0xbebd0bd1,
  0xb560,0xb540,0x300,0x3ea80a81,0xb568,0xb548,0x340,0x3ea80a81,0xb570,0xb550,0x390,0x3ea80a81,0xb578,0xb558,0x3d0,0x3ea80a81,
  0xb480,0xb4a0,0x300,0x3ea80a81,0xb488,0xb4a8,0x340,0x3ea80a81,0xb490,0xb4b0,0x390,0x3ea80a81,0xb498,0xb4b8,0x3d0,0x3ea80a81,
  0xb4c0,0xb4e0,0x110,0x3ea80a81,0xb4c8,0xb4e8,0x150,0x3ea80a81,0xb4d0,0xb4f0,0x180,0x3ea80a81,0xb4d8,0xb4f8,0x1c0,0x3ea80a81,
  0xb5e0,0xb5c0,0x2510,0xbebd0bd1,0xb5e8,0xb5c8,0x2550,0xbebd0bd1,0xb5f0,0xb5d0,0x2580,0xbebd0bd1,0xb5f8,0xb5d8,0x25c0,0xbebd0bd1,
  0xcde0,0xcdc0,0x310,0x3ea80a81,0xcde8,0xcdc8,0x350,0x3ea80a81,0xcdf0,0xcdd0,0x380,0x3ea80a81,0xcdf8,0xcdd8,0x3c0,0x3ea80a81,
  0xb520,0xb500,0x310,0x3ea80a81,0xb528,0xb508,0x350,0x3ea80a81,0xb530,0xb510,0x380,0x3ea80a81,0xb538,0xb518,0x3c0,0x3ea80a81,
  0xc0420000,0x0,0x0,0x0,0x0,0x0,0x0,0x0,0x0,0x0,0xc0420000,0x0,0x0,0x0,0x0,0x0,
  0x3f90,0x0,0x0,0x0,0x0,0x0,0x3f90,0x0,0x0,0x0,0x3f90,0x0,0x0,0x0,0x0,0x0,
  0x0,0xc042,0x0,0x0,0x0,0xc0420000,0x0,0x0,0x0,0x0,0x0,0xc042,0x0,0x0,0x0,0xc0420000,
  0x0,0x0,0x0,0x0,0x0,0x0,0x0,0x0,0x0,0x0,0x0,0x0,0x0,0x0,0x0,0x0,
  0x0,0x0,0x0,0x3f90,0x0,0x0,0x0,0x0,0x0,0x0,0x0,0x0,0x0,0x0,0x0,0x0,
  0x0,0x0,0x0,0x0,0xc042,0x0,0x0,0x0,0x0,0x0,0x0,0x0,0x0,0x0,0xc042,0x0,
  0x3f900000,0x0,0x0,0x3f900000,0x3f900000,0x0,0x0,0x0,0x0,0x0,0x3f900000,0x0,0x0,0x0,0x3f900000,0x0,
  0x0,0x0,0x0,0x0,0x0,0x0,0x0,0x0,0x0,0x0,0x0,0x0,0x0,0x0,0x0,0x0,
  0x0,0x0,0x0,0x0,0x0,0xc042,0x0,0x0,0x0,0x0,0x0,0x0,0x0,0x0,0x0,0xc042,
  0x3f90,0xc0420000,0x0,0x0,0x3f90,0x0,0x0,0x0,0x0,0x0,0x3f90,0xc0420000,0x0,0x0,0x3f903f90,0x0,
  0x0,0x0,0x0,0x0,0x0,0x0,0x0,0x0,0x0,0x0,0x0,0x0,0x0,0x0,0x0,0x0,
  0x0,0x0,0x0,0x0,0x0,0x0,0x0,0x0,0x0,0x0,0x0,0x0,0x0,0x0,0x0,0x0,
  0x3f90,0x0,0x0,0x0,0x3f903f90,0x0,0x0,0x0,0x3f900000,0x0,0x3f90,0x0,0x0,0x0,0x3f903f90,0x0,
  0x0,0x0,0x0,0x0,0x0,0x0,0x0,0x0,0x0,0x0,0x0,0x0,0x0,0x0,0x0,0x0,
  0x0,0x0,0x0,0x0,0x0,0x3f90,0x0,0x0,0x0,0x3f90,0x0,0x0,0x0,0x0,0x0,0x3f90,
  0x0,0x0,0x0,0x0,0x0,0x0,0x0,0x0,0x0,0x0,0x0,0x0,0x0,0x0,0x0,0x0,
  0x0,0x8,0xa820,0xc00,0x40,0x48,0xa828,0xc10,0x90,0x98,0xa830,0xc20,0xd0,0xd8,0xa838,0xc30,
  0x120,0x128,0xa860,0xd80,0x160,0x168,0xa868,0xd90,0x1b0,0x1b8,0xa870,0xda0,0x1f0,0x1f8,0xa878,0xdb0,
  0x1000,0x2400,0xa8a0,0xf00,0x1040,0x2440,0xa8a8,0xf10,0x1090,0x2490,0xa8b0,0xf20,0x10d0,0x24d0,0xa8b8,0xf30,
  0x1120,0x2520,0xa8e0,0x1080,0x1160,0x2560,0xa8e8,0x1090,0x11b0,0x25b0,0xa8f0,0x10a0,0x11f0,0x25f0,0xa8f8,0x10b0,
  0x10,0x18,0xa900,0x1220,0x50,0x58,0xa908,0x1230,0x80,0x88,0xa910,0x1200,0xc0,0xc8,0xa918,0x1210,
  0x130,0x138,0xa940,0x13a0,0x170,0x178,0xa948,0x13b0,0x1a0,0x1a8,0xa950,0x1380,0x1e0,0x1e8,0xa958,0x1390,
  0x1010,0x2410,0xa980,0x1520,0x1050,0x2450,0xa988,0x1530,0x1080,0x2480,0xa990,0x1500,0x10c0,0x24c0,0xa998,0x1510,
  0x1130,0x2530,0xa9c0,0x16a0,0x1170,0x2570,0xa9c8,0x16b0,0x11a0,0x25a0,0xa9d0,0x1680,0x11e0,0x25e0,0xa9d8,0x1690,
  0x20,0x28,0xae20,0xc40,0x60,0x68,0xae28,0xc50,0xb0,0xb8,0xae30,0xc60,0xf0,0xf8,0xae38,0xc70,
  0x100,0x108,0xae60,0xdc0,0x140,0x148,0xae68,0xdd0,0x190,0x198,0xae70,0xde0,0x1d0,0x1d8,0xae78,0xdf0,
  0x1020,0x2420,0xaea0,0xf40,0x1060,0x2460,0xaea8,0xf50,0x10b0,0x24b0,0xaeb0,0xf60,0x10f0,0x24f0,0xaeb8,0xf70,
  0x1100,0x2500,0xaee0,0x10c0,0x1140,0x2540,0xaee8,0x10d0,0x1190,0x2590,0xaef0,0x10e0,0x11d0,0x25d0,0xaef8,0x10f0,
  0x30,0x38,0xaf00,0x1260,0x70,0x78,0xaf08,0x1270,0xa0,0xa8,0xaf10,0x1240,0xe0,0xe8,0xaf18,0x1250,
  0x110,0x118,0xaf40,0x13e0,0x150,0x158,0xaf48,0x13f0,0x180,0x188,0xaf50,0x13c0,0x1c0,0x1c8,0xaf58,0x13d0,
  0x1030,0x2430,0xaf80,0x1560,0x1070,0x2470,0xaf88,0x1570,0x10a0,0x24a0,0xaf90,0x1540,0x10e0,0x24e0,0xaf98,0x1550,
  0x1110,0x2510,0xafc0,0x16e0,0x1150,0x2550,0xafc8,0x16f0,0x1180,0x2580,0xafd0,0x16c0,0x11c0,0x25c0,0xafd8,0x16d0,
  0xc000,0xc020,0x2600,0xbebd0bd1,0xc008,0xc028,0x2640,0xbebd0bd1,0xc010,0xc030,0x2690,0xbebd0bd1,0xc018,0xc038,0x26d0,0xbebd0bd1,
  0xd800,0xd820,0x400,0x3ea80a81,0xd808,0xd828,0x440,0x3ea80a81,0xd810,0xd830,0x490,0x3ea80a81,0xd818,0xd838,0x4d0,0x3ea80a81,
  0xc120,0xc100,0x2800,0xbebd0bd1,0xc128,0xc108,0x2840,0xbebd0bd1,0xc130,0xc110,0x2890,0xbebd0bd1,0xc138,0xc118,0x28d0,0xbebd0bd1,
  0xd840,0xd860,0x2800,0xbebd0bd1,0xd848,0xd868,0x2840,0xbebd0bd1,0xd850,0xd870,0x2890,0xbebd0bd1,0xd858,0xd878,0x28d0,0xbebd0bd1,
  0xc080,0xc0a0,0x2610,0xbebd0bd1,0xc088,0xc0a8,0x2650,0xbebd0bd1,0xc090,0xc0b0,0x2680,0xbebd0bd1,0xc098,0xc0b8,0x26c0,0xbebd0bd1,
  0xc1a0,0xc180,0x410,0x3ea80a81,0xc1a8,0xc188,0x450,0x3ea80a81,0xc1b0,0xc190,0x480,0x3ea80a81,0xc1b8,0xc198,0x4c0,0x3ea80a81,
  0xd9a0,0xd980,0x2810,0xbebd0bd1,0xd9a8,0xd988,0x2850,0xbebd0bd1,0xd9b0,0xd990,0x2880,0xbebd0bd1,0xd9b8,0xd998,0x28c0,0xbebd0bd1,
  0xd9e0,0xd9c0,0x2810,0xbebd0bd1,0xd9e8,0xd9c8,0x2850,0xbebd0bd1,0xd9f0,0xd9d0,0x2880,0xbebd0bd1,0xd9f8,0xd9d8,0x28c0,0xbebd0bd1,
  0xc600,0xc620,0x2620,0xbebd0bd1,0xc608,0xc628,0x2660,0xbebd0bd1,0xc610,0xc630,0x26b0,0xbebd0bd1,0xc618,0xc638,0x26f0,0xbebd0bd1,
  0xde00,0xde20,0x420,0x3ea80a81,0xde08,0xde28,0x460,0x3ea80a81,0xde10,0xde30,0x4b0,0x3ea80a81,0xde18,0xde38,0x4f0,0x3ea80a81,
  0xd920,0xd900,0x2820,0xbebd0bd1,0xd928,0xd908,0x2860,0xbebd0bd1,0xd930,0xd910,0x28b0,0xbebd0bd1,0xd938,0xd918,0x28f0,0xbebd0bd1,
  0xd960,0xd940,0x2820,0xbebd0bd1,0xd968,0xd948,0x2860,0xbebd0bd1,0xd970,0xd950,0x28b0,0xbebd0bd1,0xd978,0xd958,0x28f0,0xbebd0bd1,
  0xd880,0xd8a0,0x2630,0xbebd0bd1,0xd888,0xd8a8,0x2670,0xbebd0bd1,0xd890,0xd8b0,0x26a0,0xbebd0bd1,0xd898,0xd8b8,0x26e0,0xbebd0bd1,
  0xeba0,0xeb80,0x430,0x3ea80a81,0xeba8,0xeb88,0x470,0x3ea80a81,0xebb0,0xeb90,0x4a0,0x3ea80a81,0xebb8,0xeb98,0x4e0,0x3ea80a81,
  0xd8c0,0xd8e0,0x2830,0xbebd0bd1,0xd8c8,0xd8e8,0x2870,0xbebd0bd1,0xd8d0,0xd8f0,0x28a0,0xbebd0bd1,0xd8d8,0xd8f8,0x28e0,0xbebd0bd1,
  0xc600,0xc620,0x2830,0xbebd0bd1,0xc608,0xc628,0x2870,0xbebd0bd1,0xc610,0xc630,0x28a0,0xbebd0bd1,0xc618,0xc638,0x28e0,0xbebd0bd1,
  0x3f90,0x3f90,0x0,0x0,0x0,0x3f90,0x3f90,0x0,0x0,0x0,0x3f90,0x3f90,0x0,0x0,0x0,0x3f90,
  0x0,0xc0420000,0x0,0x0,0x0,0x0,0x0,0x0,0x0,0x0,0x0,0xc0420000,0x0,0x0,0x0,0x0,
  0x3f900000,0x0,0x0,0x0,0x0,0x0,0x3f900000,0x0,0x0,0x0,0x3f900000,0x0,0x0,0x0,0x0,0x0,
  0x0,0x0,0x0,0x0,0x0,0x0,0x0,0x0,0x0,0x0,0x0,0x0,0x0,0x0,0x0,0x0,
  0xc042,0x0,0x0,0x0,0xc0420000,0x0,0x0,0x0,0x0,0x0,0xc042,0x0,0x0,0x0,0xc0420000,0x0,
  0x0,0x0,0x0,0x0,0x0,0x3f900000,0x0,0x0,0x0,0x3f900000,0x0,0x0,0x0,0x0,0x0,0x3f900000,
  0x0,0x0,0x0,0x0,0x0,0xc042,0x0,0x0,0x0,0x0,0x0,0x0,0x0,0x0,0x0,0xc042,
  0x0,0x0,0x0,0x0,0x0,0x0,0x0,0x0,0x0,0x0,0x0,0x0,0x0,0x0,0x0,0x0,
  0x0,0x0,0x0,0x0,0x0,0x0,0x0,0x0,0x0,0x0,0x0,0x0,0x0,0x0,0x3f90,0x0,
  0x0,0x0,0x0,0x0,0x0,0xc0420000,0x0,0x0,0x0,0x0,0x0,0x0,0x0,0x0,0x0,0xc0420000,
  0x0,0x3f90,0x0,0x0,0x0,0x3f90,0x0,0x0,0x0,0x0,0x0,0x3f90,0x0,0x0,0x3f900000,0x3f90,
  0x0,0x0,0x0,0x0,0x0,0x0,0x0,0x0,0x0,0x0,0x0,0x0,0x0,0x0,0x0,0x0,
  0x0,0x0,0x0,0x0,0x0,0x0,0x0,0x0,0x0,0x0,0x0,0x0,0x0,0x0,0x0,0x0,
  0x0,0x3f90,0x3f900000,0x0,0x0,0x3f90,0x0,0x0,0x0,0x0,0x0,0x3f90,0x0,0x0,0x0,0x3f90,
  0xc042,0x0,0x0,0x0,0x0,0x0,0x0,0x0,0x0,0x0,0xc042,0x0,0x0,0x0,0x0,0x0,
  0x0,0x0,0x0,0x0,0x0,0x0,0x0,0x0,0x0,0x0,0x0,0x0,0x0,0x0,0x0,0x0,
  0x0,0x1200,0x19200,0x1800,0x40,0x1240,0x19208,0x1810,0x90,0x1290,0x19210,0x1820,0xd0,0x12d0,0x19218,0x1830,
  0x120,0x1320,0x19240,0x1980,0x160,0x1360,0x19248,0x1990,0x1b0,0x13b0,0x19250,0x19a0,0x1f0,0x13f0,0x19258,0x19b0,
  0x1000,0x3608,0x19280,0x1b00,0x1040,0x3648,0x19288,0x1b10,0x1090,0x3698,0x19290,0x1b20,0x10d0,0x36d8,0x19298,0x1b30,
  0x1120,0x3728,0x192c0,0x1c80,0x1160,0x3768,0x192c8,0x1c90,0x11b0,0x37b8,0x192d0,0x1ca0,0x11f0,0x37f8,0x192d8,0x1cb0,
  0x10,0x1210,0x19320,0x1e20,0x50,0x1250,0x19328,0x1e30,0x80,0x1280,0x19330,0x1e00,0xc0,0x12c0,0x19338,0x1e10,
  0x130,0x1330,0x19360,0x1fa0,0x170,0x1370,0x19368,0x1fb0,0x1a0,0x13a0,0x19370,0x1f80,0x1e0,0x13e0,0x19378,0x1f90,
  0x1010,0x3618,0x193a0,0x2120,0x1050,0x3658,0x193a8,0x2130,0x1080,0x3688,0x193b0,0x2100,0x10c0,0x36c8,0x193b8,0x2110,
  0x1130,0x3738,0x193e0,0x22a0,0x1170,0x3778,0x193e8,0x22b0,0x11a0,0x37a8,0x193f0,0x2280,0x11e0,0x37e8,0x193f8,0x2290,
  0x20,0x1220,0x19800,0x1840,0x60,0x1260,0x19808,0x1850,0xb0,0x12b0,0x19810,0x1860,0xf0,0x12f0,0x19818,0x1870,
  0x100,0x1300,0x19840,0x19c0,0x140,0x1340,0x19848,0x19d0,0x190,0x1390,0x19850,0x19e0,0x1d0,0x13d0,0x19858,0x19f0,
  0x1020,0x3628,0x19880,0x1b40,0x1060,0x3668,0x19888,0x1b50,0x10b0,0x36b8,0x19890,0x1b60,0x10f0,0x36f8,0x19898,0x1b70,
  0x1100,0x3708,0x198c0,0x1cc0,0x1140,0x3748,0x198c8,0x1cd0,0x1190,0x3798,0x198d0,0x1ce0,0x11d0,0x37d8,0x198d8,0x1cf0,
  0x30,0x1230,0x19920,0x1e60,0x70,0x1270,0x19928,0x1e70,0xa0,0x12a0,0x19930,0x1e40,0xe0,0x12e0,0x19938,0x1e50,
  0x110,0x1310,0x19960,0x1fe0,0x150,0x1350,0x19968,0x1ff0,0x180,0x1380,0x19970,0x1fc0,0x1c0,0x13c0,0x19978,0x1fd0,
  0x1030,0x3638,0x199a0,0x2160,0x1070,0x3678,0x199a8,0x2170,0x10a0,0x36a8,0x199b0,0x2140,0x10e0,0x36e8,0x199b8,0x2150,
  0x1110,0x3718,0x199e0,0x22e0,0x1150,0x3758,0x199e8,0x22f0,0x1180,0x3788,0x199f0,0x22c0,0x11c0,0x37c8,0x199f8,0x22d0,
  0xcc00,0xcc20,0x2720,0xbebd0bd1,0xcc08,0xcc28,0x2760,0xbebd0bd1,0xcc10,0xcc30,0x27b0,0xbebd0bd1,0xcc18,0xcc38,0x27f0,0xbebd0bd1,
  0xcd20,0xcd00,0x520,0x3ea80a81,0xcd28,0xcd08,0x560,0x3ea80a81,0xcd30,0xcd10,0x5b0,0x3ea80a81,0xcd38,0xcd18,0x5f0,0x3ea80a81,
  0xe440,0xe460,0x2920,0xbebd0bd1,0xe448,0xe468,0x2960,0xbebd0bd1,0xe450,0xe470,0x29b0,0xbebd0bd1,0xe458,0xe478,0x29f0,0xbebd0bd1,
  0xeb60,0xeb40,0x2920,0xbebd0bd1,0xeb68,0xeb48,0x2960,0xbebd0bd1,0xeb70,0xeb50,0x29b0,0xbebd0bd1,0xeb78,0xeb58,0x29f0,0xbebd0bd1,
  0xe480,0xe4a0,0x2730,0xbebd0bd1,0xe488,0xe4a8,0x2770,0xbebd0bd1,0xe490,0xe4b0,0x27a0,0xbebd0bd1,0xe498,0xe4b8,0x27e0,0xbebd0bd1,
  0xe5a0,0xe580,0x530,0x3ea80a81,0xe5a8,0xe588,0x570,0x3ea80a81,0xe5b0,0xe590,0x5a0,0x3ea80a81,0xe5b8,0xe598,0x5e0,0x3ea80a81,
  0xe4c0,0xe4e0,0x2930,0xbebd0bd1,0xe4c8,0xe4e8,0x2970,0xbebd0bd1,0xe4d0,0xe4f0,0x29a0,0xbebd0bd1,0xe4d8,0xe4f8,0x29e0,0xbebd0bd1,
  0xd9e0,0xd9c0,0x2930,0xbebd0bd1,0xd9e8,0xd9c8,0x2970,0xbebd0bd1,0xd9f0,0xd9d0,0x29a0,0xbebd0bd1,0xd9f8,0xd9d8,0x29e0,0xbebd0bd1,
  0xe400,0xe420,0x2700,0xbebd0bd1,0xe408,0xe428,0x2740,0xbebd0bd1,0xe410,0xe430,0x2790,0xbebd0bd1,0xe418,0xe438,0x27d0,0xbebd0bd1,
  0xe520,0xe500,0x500,0x3ea80a81,0xe528,0xe508,0x540,0x3ea80a81,0xe530,0xe510,0x590,0x3ea80a81,0xe538,0xe518,0x5d0,0x3ea80a81,
  0xe560,0xe540,0x2900,0xbebd0bd1,0xe568,0xe548,0x2940,0xbebd0bd1,0xe570,0xe550,0x2990,0xbebd0bd1,0xe578,0xe558,0x29d0,0xbebd0bd1,
  0xcc80,0xcca0,0x2900,0xbebd0bd1,0xcc88,0xcca8,0x2940,0xbebd0bd1,0xcc90,0xccb0,0x2990,0xbebd0bd1,0xcc98,0xccb8,0x29d0,0xbebd0bd1,
  0xcda0,0xcd80,0x2710,0xbebd0bd1,0xcda8,0xcd88,0x2750,0xbebd0bd1,0xcdb0,0xcd90,0x2780,0xbebd0bd1,0xcdb8,0xcd98,0x27c0,0xbebd0bd1,
  0xeac0,0xeae0,0x510,0x3ea80a81,0xeac8,0xeae8,0x550,0x3ea80a81,0xead0,0xeaf0,0x580,0x3ea80a81,0xead8,0xeaf8,0x5c0,0x3ea80a81,
  0xe5e0,0xe5c0,0x2910,0xbebd0bd1,0xe5e8,0xe5c8,0x2950,0xbebd0bd1,0xe5f0,0xe5d0,0x2980,0xbebd0bd1,0xe5f8,0xe5d8,0x29c0,0xbebd0bd1,
  0xe400,0xe420,0x2910,0xbebd0bd1,0xe408,0xe428,0x2950,0xbebd0bd1,0xe410,0xe430,0x2980,0xbebd0bd1,0xe418,0xe438,0x29c0,0xbebd0bd1,
  0x3f903f90,0x0,0x0,0x0,0x3f900000,0x0,0x3f90,0x0,0x0,0x0,0x3f903f90,0x0,0x0,0x0,0x3f900000,0x0,
  0x0,0xc042,0x0,0x0,0x0,0x0,0x0,0x0,0x0,0x0,0x0,0xc042,0x0,0x0,0x0,0x0,
  0x0,0x0,0x0,0x0,0x0,0x0,0x0,0x0,0x0,0x0,0x0,0x0,0x0,0x0,0x0,0x3f900000,
  0x0,0x0,0x0,0x0,0x0,0x0,0x0,0x0,0x0,0x0,0x0,0x0,0x0,0x0,0x0,0x0,
  0x0,0x0,0x0,0x0,0x0,0x0,0x0,0x0,0x0,0x0,0x0,0x0,0x0,0x0,0x0,0x0,
  0x0,0x3f90,0x0,0x3f900000,0x0,0x3f90,0x0,0x0,0x0,0x0,0x0,0x3f90,0x0,0x0,0x0,0x3f90,
  0xc042,0x0,0x0,0x0,0xc0420000,0x0,0x0,0x0,0x0,0x0,0xc042,0x0,0x0,0x0,0xc0420000,0x0,
  0x0,0x0,0x0,0x0,0x0,0x0,0x0,0x0,0x0,0x0,0x0,0x0,0x0,0x0,0x0,0x0,
  0x0,0xc0420000,0x0,0x0,0x0,0x0,0x0,0x0,0x0,0x0,0x0,0xc0420000,0x0,0x0,0x0,0x0,
  0x0,0x0,0x0,0x0,0x0,0xc042,0x0,0x0,0x0,0x0,0x0,0x0,0x0,0x0,0x0,0xc042,
  0x3f903f90,0x0,0x0,0x0,0x3f900000,0x0,0x3f90,0x0,0x0,0x0,0x3f903f90,0x0,0x0,0x0,0x3f900000,0x0,
  0x0,0x0,0x0,0x0,0x0,0x0,0x0,0x0,0x0,0x0,0x0,0x0,0x0,0x0,0x0,0x0,
  0x0,0x0,0x0,0x0,0xc042,0x0,0x0,0x0,0x0,0x0,0x0,0x0,0x0,0x0,0x3f90c042,0x0,
  0x0,0x0,0x0,0x0,0x0,0x3f90,0x0,0x0,0x0,0x3f90,0x0,0x0,0x0,0x0,0x0,0x3f90,
  0x0,0x0,0x0,0x0,0x0,0x0,0x0,0x0,0x0,0x0,0x0,0x0,0x0,0x0,0x0,0x0,
  0x0,0x0,0x0,0x0,0x0,0x0,0x0,0x0,0x0,0x0,0x0,0x0,0x0,0x0,0x0,0x0,
  0x0,0x1208,0x19220,0x2400,0x40,0x1248,0x19228,0x2410,0x90,0x1298,0x19230,0x2420,0xd0,0x12d8,0x19238,0x2430,
  0x120,0x1328,0x19260,0x2580,0x160,0x1368,0x19268,0x2590,0x1b0,0x13b8,0x19270,0x25a0,0x1f0,0x13f8,0x19278,0x25b0,
  0x1000,0x3600,0x192a0,0x2700,0x1040,0x3640,0x192a8,0x2710,0x1090,0x3690,0x192b0,0x2720,0x10d0,0x36d0,0x192b8,0x2730,
  0x1120,0x3720,0x192e0,0x2880,0x1160,0x3760,0x192e8,0x2890,0x11b0,0x37b0,0x192f0,0x28a0,0x11f0,0x37f0,0x192f8,0x28b0,
  0x10,0x1218,0x19300,0x2a20,0x50,0x1258,0x19308,0x2a30,0x80,0x1288,0x19310,0x2a00,0xc0,0x12c8,0x19318,0x2a10,
  0x130,0x1338,0x19340,0x2ba0,0x170,0x1378,0x19348,0x2bb0,0x1a0,0x13a8,0x19350,0x2b80,0x1e0,0x13e8,0x19358,0x2b90,
  0x1010,0x3610,0x19380,0x2d20,0x1050,0x3650,0x19388,0x2d30,0x1080,0x3680,0x19390,0x2d00,0x10c0,0x36c0,0x19398,0x2d10,
  0x1130,0x3730,0x193c0,0x2ea0,0x1170,0x3770,0x193c8,0x2eb0,0x11a0,0x37a0,0x193d0,0x2e80,0x11e0,0x37e0,0x193d8,0x2e90,
  0x20,0x1228,0x19820,0x2440,0x60,0x1268,0x19828,0x2450,0xb0,0x12b8,0x19830,0x2460,0xf0,0x12f8,0x19838,0x2470,
  0x100,0x1308,0x19860,0x25c0,0x140,0x1348,0x19868,0x25d0,0x190,0x1398,0x19870,0x25e0,0x1d0,0x13d8,0x19878,0x25f0,
  0x1020,0x3620,0x198a0,0x2740,0x1060,0x3660,0x198a8,0x2750,0x10b0,0x36b0,0x198b0,0x2760,0x10f0,0x36f0,0x198b8,0x2770,
  0x1100,0x3700,0x198e0,0x28c0,0x1140,0x3740,0x198e8,0x28d0,0x1190,0x3790,0x198f0,0x28e0,0x11d0,0x37d0,0x198f8,0x28f0,
  0x30,0x1238,0x19900,0x2a60,0x70,0x1278,0x19908,0x2a70,0xa0,0x12a8,0x19910,0x2a40,0xe0,0x12e8,0x19918,0x2a50,
  0x110,0x1318,0x19940,0x2be0,0x150,0x1358,0x19948,0x2bf0,0x180,0x1388,0x19950,0x2bc0,0x1c0,0x13c8,0x19958,0x2bd0,
  0x1030,0x3630,0x19980,0x2d60,0x1070,0x3670,0x19988,0x2d70,0x10a0,0x36a0,0x19990,0x2d40,0x10e0,0x36e0,0x19998,0x2d50,
  0x1110,0x3710,0x199c0,0x2ee0,0x1150,0x3750,0x199c8,0x2ef0,0x1180,0x3780,0x199d0,0x2ec0,0x11c0,0x37c0,0x199d8,0x2ed0,
  0x0,0x0,0x800,0x10000000,0x10,0x90,0x890,0x10000000,0x20,0x120,0x920,0x10000000,0x30,0x1b0,0x9b0,0x10000000,
  0x40,0x240,0xa40,0x20003180,0x50,0x2d0,0xad0,0x20003190,0x60,0x360,0xb60,0x200031a0,0x70,0x3f0,0xbf0,0x200031b0,
  0x610,0xc00,0x1400,0x40000000,0x600,0xc90,0x1490,0x40000010,0x630,0xd20,0x1520,0x40000020,0x620,0xdb0,0x15b0,0x40000030,
  0x650,0xe40,0x1640,0x20033180,0x640,0xed0,0x16d0,0x20033190,0x670,0xf60,0x1760,0x200331a0,0x660,0xff0,0x17f0,0x200331b0,
  0xc20,0x10,0x808,0x40030020,0xc30,0x80,0x898,0x40030030,0xc00,0x130,0x928,0x40030000,0xc10,0x1a0,0x9b8,0x40030010,
  0xc60,0x250,0xa48,0x200631a0,0xc70,0x2c0,0xad8,0x200631b0,0xc40,0x370,0xb68,0x20063180,0xc50,0x3e0,0xbf8,0x20063190,
  0x1230,0xc10,0x1408,0x40060020,0x1220,0xc80,0x1498,0x40060030,0x1210,0xd30,0x1528,0x40060000,0x1200,0xda0,0x15b8,0x40060010,
  0x1270,0xe50,0x1648,0x10000000,0x1260,0xec0,0x16d8,0x10000000,0x1250,0xf70,0x1768,0x10000000,0x1240,0xfe0,0x17f8,0x10000000,
  0x1840,0x20,0x810,0x10000000,0x1850,0xb0,0x880,0x10000000,0x1860,0x100,0x930,0x10000000,0x1870,0x190,0x9a0,0x10000000,
  0x1800,0x260,0xa50,0x200031c0,0x1810,0x2f0,0xac0,0x200031d0,0x1820,0x340,0xb70,0x200031e0,0x1830,0x3d0,0xbe0,0x200031f0,
  0x1e50,0xc20,0x1410,0x40000040,0x1e40,0xcb0,0x1480,0x40000050,0x1e70,0xd00,0x1530,0x40000060,0x1e60,0xd90,0x15a0,0x40000070,
  0x1e10,0xe60,0x1650,0x200331c0,0x1e00,0xef0,0x16c0,0x200331d0,0x1e30,0xf40,0x1770,0x200331e0,0x1e20,0xfd0,0x17e0,0x200331f0,
  0x2460,0x30,0x818,0x40030060,0x2470,0xa0,0x888,0x40030070,0x2440,0x110,0x938,0x40030040,0x2450,0x180,0x9a8,0x40030050,
  0x2420,0x270,0xa58,0x200631e0,0x2430,0x2e0,0xac8,0x200631f0,0x2400,0x350,0xb78,0x200631c0,0x2410,0x3c0,0xbe8,0x200631d0,
  0x2a70,0xc30,0x1418,0x40060060,0x2a60,0xca0,0x1488,0x40060070,0x2a50,0xd10,0x1538,0x40060040,0x2a40,0xd80,0x15a8,0x40060050,
  0x2a30,0xe70,0x1658,0x10000000,0x2a20,0xee0,0x16c8,0x10000000,0x2a10,0xf50,0x1778,0x10000000,0x2a00,0xfc0,0x17e8,0x10000000,
  0xbfd8bf90,0x3f403f00,0x0,0x0,0x0,0x0,0x0,0x0,0x0,0x0,0x0,0x0,0x0,0x0,0x0,0x0,
  0x0,0x0,0xbfd8bf90,0x3f403f00,0x0,0x0,0x0,0x0,0x0,0x0,0x0,0x0,0x0,0x0,0x0,0x0,
  0xbfd83f90,0x3f40bf00,0x0,0x0,0x0,0x0,0x0,0x0,0x0,0x0,0x0,0x0,0x0,0x0,0x0,0x0,
  0x0,0x0,0xbfd83f90,0x3f40bf00,0x0,0x0,0x0,0x0,0x0,0x0,0x0,0x0,0x0,0x0,0x0,0x0,
  0x0,0x0,0x0,0x0,0xbfd8bf90,0x3f403f00,0x0,0x0,0x0,0x0,0x0,0x0,0x0,0x0,0x0,0x0,
  0x0,0x0,0x0,0x0,0x0,0x0,0xbfd8bf90,0x3f403f00,0x0,0x0,0x0,0x0,0x0,0x0,0x0,0x0,
  0x0,0x0,0x0,0x0,0xbfd83f90,0x3f40bf00,0x0,0x0,0x0,0x0,0x0,0x0,0x0,0x0,0x0,0x0,
  0x0,0x0,0x0,0x0,0x0,0x0,0xbfd83f90,0x3f40bf00,0x0,0x0,0x0,0x0,0x0,0x0,0x0,0x0,
  0x0,0x0,0x0,0x0,0x0,0x0,0x0,0x0,0xbfd8bf90,0x3f403f00,0x0,0x0,0x0,0x0,0x0,0x0,
  0x0,0x0,0x0,0x0,0x0,0x0,0x0,0x0,0x0,0x0,0xbfd8bf90,0x3f403f00,0x0,0x0,0x0,0x0,
  0x0,0x0,0x0,0x0,0x0,0x0,0x0,0x0,0xbfd83f90,0x3f40bf00,0x0,0x0,0x0,0x0,0x0,0x0,
  0x0,0x0,0x0,0x0,0x0,0x0,0x0,0x0,0x0,0x0,0xbfd83f90,0x3f40bf00,0x0,0x0,0x0,0x0,
  0x0,0x0,0x0,0x0,0x0,0x0,0x0,0x0,0x0,0x0,0x0,0x0,0xbfd8bf90,0x3f403f00,0x0,0x0,
  0x0,0x0,0x0,0x0,0x0,0x0,0x0,0x0,0x0,0x0,0x0,0x0,0x0,0x0,0xbfd8bf90,0x3f403f00,
  0x0,0x0,0x0,0x0,0x0,0x0,0x0,0x0,0x0,0x0,0x0,0x0,0xbfd83f90,0x3f40bf00,0x0,0x0,
  0x0,0x0,0x0,0x0,0x0,0x0,0x0,0x0,0x0,0x0,0x0,0x0,0x0,0x0,0xbfd83f90,0x3f40bf00,
  0x0,0x9000,0x0,0x0,0x10,0x9002,0x180,0x0,0x20,0x9004,0x300,0x0,0x30,0x9006,0x480,0x0,
  0x40,0x9008,0x1800,0x0,0x50,0x900a,0x1980,0x0,0x60,0x900c,0x1b00,0x0,0x70,0x900e,0x1c80,0x0,
  0x9000,0x9010,0x2,0x0,0x9010,0x9012,0x182,0x0,0x9020,0x9014,0x302,0x0,0x9030,0x9016,0x482,0x0,
  0x9040,0x9018,0x1802,0x0,0x9050,0x901a,0x1982,0x0,0x9060,0x901c,0x1b02,0x0,0x9070,0x901e,0x1c82,0x0,
  0x12000,0x9020,0x4,0x0,0x12010,0x9022,0x184,0x0,0x12020,0x9024,0x304,0x0,0x12030,0x9026,0x484,0x0,
  0x12040,0x9028,0x1804,0x0,0x12050,0x902a,0x1984,0x0,0x12060,0x902c,0x1b04,0x0,0x12070,0x902e,0x1c84,0x0,
  0x1b000,0x9030,0x6,0x0,0x1b010,0x9032,0x186,0x0,0x1b020,0x9034,0x306,0x0,0x1b030,0x9036,0x486,0x0,
  0x1b040,0x9038,0x1806,0x0,0x1b050,0x903a,0x1986,0x0,0x1b060,0x903c,0x1b06,0x0,0x1b070,0x903e,0x1c86,0x0,
  0x24000,0x9040,0x8,0x8,0x24010,0x9042,0x188,0x8,0x24020,0x9044,0x308,0x8,0x24030,0x9046,0x488,0x8,
  0x24040,0x9048,0x1808,0x8,0x24050,0x904a,0x1988,0x8,0x24060,0x904c,0x1b08,0x8,0x24070,0x904e,0x1c88,0x8,
  0x2d000,0x9050,0xa,0x8,0x2d010,0x9052,0x18a,0x8,0x2d020,0x9054,0x30a,0x8,0x2d030,0x9056,0x48a,0x8,
  0x2d040,0x9058,0x180a,0x8,0x2d050,0x905a,0x198a,0x8,0x2d060,0x905c,0x1b0a,0x8,0x2d070,0x905e,0x1c8a,0x8,
  0x40000000,0x9060,0xc,0x8,0x40000000,0x9062,0x18c,0x8,0x40000000,0x9064,0x30c,0x8,0x40000000,0x9066,0x48c,0x8,
  0x40000000,0x9068,0x180c,0x8,0x40000000,0x906a,0x198c,0x8,0x40000000,0x906c,0x1b0c,0x8,0x40000000,0x906e,0x1c8c,0x8,
  0x40000000,0x9070,0xe,0x8,0x40000000,0x9072,0x18e,0x8,0x40000000,0x9074,0x30e,0x8,0x40000000,0x9076,0x48e,0x8,
  0x40000000,0x9078,0x180e,0x8,0x40000000,0x907a,0x198e,0x8,0x40000000,0x907c,0x1b0e,0x8,0x40000000,0x907e,0x1c8e,0x8,
  0x180000,0x40,0x820,0x10000000,0x180010,0xd0,0x8b0,0x10000000,0x180020,0x160,0x900,0x10000000,0x180030,0x1f0,0x990,0x10000000,
  0x180040,0x200,0xa60,0x20093180,0x180050,0x290,0xaf0,0x20093190,0x180060,0x320,0xb40,0x200931a0,0x180070,0x3b0,0xbd0,0x200931b0,
  0x180610,0xc40,0x1420,0x40090000,0x180600,0xcd0,0x14b0,0x40090010,0x180630,0xd60,0x1500,0x40090020,0x180620,0xdf0,0x1590,0x40090030,
  0x180650,0xe00,0x1660,0x200c3180,0x180640,0xe90,0x16f0,0x200c3190,0x180670,0xf20,0x1740,0x200c31a0,0x180660,0xfb0,0x17d0,0x200c31b0,
  0x180c20,0x50,0x828,0x400c0020,0x180c30,0xc0,0x8b8,0x400c0030,0x180c00,0x170,0x908,0x400c0000,0x180c10,0x1e0,0x998,0x400c0010,
  0x180c60,0x210,0xa68,0x200f31a0,0x180c70,0x280,0xaf8,0x200f31b0,0x180c40,0x330,0xb48,0x200f3180,0x180c50,0x3a0,0xbd8,0x200f3190,
  0x181230,0xc50,0x1428,0x400f0020,0x181220,0xcc0,0x14b8,0x400f0030,0x181210,0xd70,0x1508,0x400f0000,0x181200,0xde0,0x1598,0x400f0010,
  0x181270,0xe10,0x1668,0x10000000,0x181260,0xe80,0x16f8,0x10000000,0x181250,0xf30,0x1748,0x10000000,0x181240,0xfa0,0x17d8,0x10000000,
  0x181840,0x60,0x830,0x10000000,0x181850,0xf0,0x8a0,0x10000000,0x181860,0x140,0x910,0x10000000,0x181870,0x1d0,0x980,0x10000000,
  0x181800,0x220,0xa70,0x200931c0,0x181810,0x2b0,0xae0,0x200931d0,0x181820,0x300,0xb50,0x200931e0,0x181830,0x390,0xbc0,0x200931f0,
  0x181e50,0xc60,0x1430,0x40090040,0x181e40,0xcf0,0x14a0,0x40090050,0x181e70,0xd40,0x1510,0x40090060,0x181e60,0xdd0,0x1580,0x40090070,
  0x181e10,0xe20,0x1670,0x200c31c0,0x181e00,0xeb0,0x16e0,0x200c31d0,0x181e30,0xf00,0x1750,0x200c31e0,0x181e20,0xf90,0x17c0,0x200c31f0,
  0x182460,0x70,0x838,0x400c0060,0x182470,0xe0,0x8a8,0x400c0070,0x182440,0x150,0x918,0x400c0040,0x182450,0x1c0,0x988,0x400c0050,
  0x182420,0x230,0xa78,0x200f31e0,0x182430,0x2a0,0xae8,0x200f31f0,0x182400,0x310,0xb58,0x200f31c0,0x182410,0x380,0xbc8,0x200f31d0,
  0x182a70,0xc70,0x1438,0x400f0060,0x182a60,0xce0,0x14a8,0x400f0070,0x182a50,0xd50,0x1518,0x400f0040,0x182a40,0xdc0,0x1588,0x400f0050,
  0x182a30,0xe30,0x1678,0x10000000,0x182a20,0xea0,0x16e8,0x10000000,0x182a10,0xf10,0x1758,0x10000000,0x182a00,0xf80,0x17c8,0x10000000,
  0xbfd8bf90,0x3f403f00,0x0,0x0,0x0,0x0,0x0,0x0,0x0,0x0,0x0,0x0,0x0,0x0,0x0,0x0,
  0x0,0x0,0xbfd8bf90,0x3f403f00,0x0,0x0,0x0,0x0,0x0,0x0,0x0,0x0,0x0,0x0,0x0,0x0,
  0xbfd83f90,0x3f40bf00,0x0,0x0,0x0,0x0,0x0,0x0,0x0,0x0,0x0,0x0,0x0,0x0,0x0,0x0,
  0x0,0x0,0xbfd83f90,0x3f40bf00,0x0,0x0,0x0,0x0,0x0,0x0,0x0,0x0,0x0,0x0,0x0,0x0,
  0x0,0x0,0x0,0x0,0xbfd8bf90,0x3f403f00,0x0,0x0,0x0,0x0,0x0,0x0,0x0,0x0,0x0,0x0,
  0x0,0x0,0x0,0x0,0x0,0x0,0xbfd8bf90,0x3f403f00,0x0,0x0,0x0,0x0,0x0,0x0,0x0,0x0,
  0x0,0x0,0x0,0x0,0xbfd83f90,0x3f40bf00,0x0,0x0,0x0,0x0,0x0,0x0,0x0,0x0,0x0,0x0,
  0x0,0x0,0x0,0x0,0x0,0x0,0xbfd83f90,0x3f40bf00,0x0,0x0,0x0,0x0,0x0,0x0,0x0,0x0,
  0x0,0x0,0x0,0x0,0x0,0x0,0x0,0x0,0xbfd8bf90,0x3f403f00,0x0,0x0,0x0,0x0,0x0,0x0,
  0x0,0x0,0x0,0x0,0x0,0x0,0x0,0x0,0x0,0x0,0xbfd8bf90,0x3f403f00,0x0,0x0,0x0,0x0,
  0x0,0x0,0x0,0x0,0x0,0x0,0x0,0x0,0xbfd83f90,0x3f40bf00,0x0,0x0,0x0,0x0,0x0,0x0,
  0x0,0x0,0x0,0x0,0x0,0x0,0x0,0x0,0x0,0x0,0xbfd83f90,0x3f40bf00,0x0,0x0,0x0,0x0,
  0x0,0x0,0x0,0x0,0x0,0x0,0x0,0x0,0x0,0x0,0x0,0x0,0xbfd8bf90,0x3f403f00,0x0,0x0,
  0x0,0x0,0x0,0x0,0x0,0x0,0x0,0x0,0x0,0x0,0x0,0x0,0x0,0x0,0xbfd8bf90,0x3f403f00,
  0x0,0x0,0x0,0x0,0x0,0x0,0x0,0x0,0x0,0x0,0x0,0x0,0xbfd83f90,0x3f40bf00,0x0,0x0,
  0x0,0x0,0x0,0x0,0x0,0x0,0x0,0x0,0x0,0x0,0x0,0x0,0x0,0x0,0xbfd83f90,0x3f40bf00,
  0x0,0x0,0x0,0x0,0x0,0x0,0x0,0x0,0x0,0x0,0x0,0x0,0x0,0x0,0x0,0x0,
  0x0,0x0,0x0,0x0,0x0,0x0,0x0,0x0,0x0,0x0,0x0,0x0,0x0,0x0,0x0,0x0,
  0x0,0x0,0x0,0x0,0x0,0x0,0x0,0x0,0x0,0x0,0x0,0x0,0x0,0x0,0x0,0x0,
  0x0,0x0,0x0,0x0,0x0,0x0,0x0,0x0,0x0,0x0,0x0,0x0,0x0,0x0,0x0,0x0,
  0x0,0x0,0x0,0x0,0x0,0x0,0x0,0x0,0x0,0x0,0x0,0x0,0x0,0x0,0x0,0x0,
  0x0,0x0,0x0,0x0,0x0,0x0,0x0,0x0,0x0,0x0,0x0,0x0,0x0,0x0,0x0,0x0,
  0x0,0x0,0x0,0x0,0x0,0x0,0x0,0x0,0x0,0x0,0x0,0x0,0x0,0x0,0x0,0x0,
  0x0,0x0,0x0,0x0,0x0,0x0,0x0,0x0,0x0,0x0,0x0,0x0,0x0,0x0,0x0,0x0,
  0x0,0x0,0x0,0x0,0x0,0x0,0x0,0x0,0x0,0x0,0x0,0x0,0x0,0x0,0x0,0x0,
  0x0,0x0,0x0,0x0,0x0,0x0,0x0,0x0,0x0,0x0,0x0,0x0,0x0,0x0,0x0,0x0,
  0x0,0x0,0x0,0x0,0x0,0x0,0x0,0x0,0x0,0x0,0x0,0x0,0x0,0x0,0x0,0x0,
  0x0,0x0,0x0,0x0,0x0,0x0,0x0,0x0,0x0,0x0,0x0,0x0,0x0,0x0,0x0,0x0,
  0x0,0x0,0x0,0x0,0x0,0x0,0x0,0x0,0x0,0x0,0x0,0x0,0x0,0x0,0x0,0x0,
  0x0,0x0,0x0,0x0,0x0,0x0,0x0,0x0,0x0,0x0,0x0,0x0,0x0,0x0,0x0,0x0,
  0x0,0x0,0x0,0x0,0x0,0x0,0x0,0x0,0x0,0x0,0x0,0x0,0x0,0x0,0x0,0x0,
  0x0,0x0,0x0,0x0,0x0,0x0,0x0,0x0,0x0,0x0,0x0,0x0,0x0,0x0,0x0,0x0,
  0x0,0x400,0x840,0x10000000,0x10,0x490,0x8d0,0x10000000,0x20,0x520,0x960,0x10000000,0x30,0x5b0,0x9f0,0x10000000,
  0x40,0x640,0xa00,0x20123180,0x50,0x6d0,0xa90,0x20123190,0x60,0x760,0xb20,0x201231a0,0x70,0x7f0,0xbb0,0x201231b0,
  0x610,0x1000,0x1440,0x40120000,0x600,0x1090,0x14d0,0x40120010,0x630,0x1120,0x1560,0x40120020,0x620,0x11b0,0x15f0,0x40120030,
  0x650,0x1240,0x1600,0x20153180,0x640,0x12d0,0x1690,0x20153190,0x670,0x1360,0x1720,0x201531a0,0x660,0x13f0,0x17b0,0x201531b0,
  0xc20,0x410,0x848,0x40150020,0xc30,0x480,0x8d8,0x40150030,0xc00,0x530,0x968,0x40150000,0xc10,0x5a0,0x9f8,0x40150010,
  0xc60,0x650,0xa08,0x201831a0,0xc70,0x6c0,0xa98,0x201831b0,0xc40,0x770,0xb28,0x20183180,0xc50,0x7e0,0xbb8,0x20183190,
  0x1230,0x1010,0x1448,0x40180020,0x1220,0x1080,0x14d8,0x40180030,0x1210,0x1130,0x1568,0x40180000,0x1200,0x11a0,0x15f8,0x40180010,
  0x1270,0x1250,0x1608,0x10000000,0x1260,0x12c0,0x1698,0x10000000,0x1250,0x1370,0x1728,0x10000000,0x1240,0x13e0,0x17b8,0x10000000,
  0x1840,0x420,0x850,0x10000000,0x1850,0x4b0,0x8c0,0x10000000,0x1860,0x500,0x970,0x10000000,0x1870,0x590,0x9e0,0x10000000,
  0x1800,0x660,0xa10,0x201231c0,0x1810,0x6f0,0xa80,0x201231d0,0x1820,0x740,0xb30,0x201231e0,0x1830,0x7d0,0xba0,0x201231f0,
  0x1e50,0x1020,0x1450,0x40120040,0x1e40,0x10b0,0x14c0,0x40120050,0x1e70,0x1100,0x1570,0x40120060,0x1e60,0x1190,0x15e0,0x40120070,
  0x1e10,0x1260,0x1610,0x201531c0,0x1e00,0x12f0,0x1680,0x201531d0,0x1e30,0x1340,0x1730,0x201531e0,0x1e20,0x13d0,0x17a0,0x201531f0,
  0x2460,0x430,0x858,0x40150060,0x2470,0x4a0,0x8c8,0x40150070,0x2440,0x510,0x978,0x40150040,0x2450,0x580,0x9e8,0x40150050,
  0x2420,0x670,0xa18,0x201831e0,0x2430,0x6e0,0xa88,0x201831f0,0x2400,0x750,0xb38,0x201831c0,0x2410,0x7c0,0xba8,0x201831d0,
  0x2a70,0x1030,0x1458,0x40180060,0x2a60,0x10a0,0x14c8,0x40180070,0x2a50,0x1110,0x1578,0x40180040,0x2a40,0x1180,0x15e8,0x40180050,
  0x2a30,0x1270,0x1618,0x10000000,0x2a20,0x12e0,0x1688,0x10000000,0x2a10,0x1350,0x1738,0x10000000,0x2a00,0x13c0,0x17a8,0x10000000,
  0xbfd8bf90,0x3f403f00,0x0,0x0,0x0,0x0,0x0,0x0,0x0,0x0,0x0,0x0,0x0,0x0,0x0,0x0,
  0x0,0x0,0xbfd8bf90,0x3f403f00,0x0,0x0,0x0,0x0,0x0,0x0,0x0,0x0,0x0,0x0,0x0,0x0,
  0xbfd83f90,0x3f40bf00,0x0,0x0,0x0,0x0,0x0,0x0,0x0,0x0,0x0,0x0,0x0,0x0,0x0,0x0,
  0x0,0x0,0xbfd83f90,0x3f40bf00,0x0,0x0,0x0,0x0,0x0,0x0,0x0,0x0,0x0,0x0,0x0,0x0,
  0x0,0x0,0x0,0x0,0xbfd8bf90,0x3f403f00,0x0,0x0,0x0,0x0,0x0,0x0,0x0,0x0,0x0,0x0,
  0x0,0x0,0x0,0x0,0x0,0x0,0xbfd8bf90,0x3f403f00,0x0,0x0,0x0,0x0,0x0,0x0,0x0,0x0,
  0x0,0x0,0x0,0x0,0xbfd83f90,0x3f40bf00,0x0,0x0,0x0,0x0,0x0,0x0,0x0,0x0,0x0,0x0,
  0x0,0x0,0x0,0x0,0x0,0x0,0xbfd83f90,0x3f40bf00,0x0,0x0,0x0,0x0,0x0,0x0,0x0,0x0,
  0x0,0x0,0x0,0x0,0x0,0x0,0x0,0x0,0xbfd8bf90,0x3f403f00,0x0,0x0,0x0,0x0,0x0,0x0,
  0x0,0x0,0x0,0x0,0x0,0x0,0x0,0x0,0x0,0x0,0xbfd8bf90,0x3f403f00,0x0,0x0,0x0,0x0,
  0x0,0x0,0x0,0x0,0x0,0x0,0x0,0x0,0xbfd83f90,0x3f40bf00,0x0,0x0,0x0,0x0,0x0,0x0,
  0x0,0x0,0x0,0x0,0x0,0x0,0x0,0x0,0x0,0x0,0xbfd83f90,0x3f40bf00,0x0,0x0,0x0,0x0,
  0x0,0x0,0x0,0x0,0x0,0x0,0x0,0x0,0x0,0x0,0x0,0x0,0xbfd8bf90,0x3f403f00,0x0,0x0,
  0x0,0x0,0x0,0x0,0x0,0x0,0x0,0x0,0x0,0x0,0x0,0x0,0x0,0x0,0xbfd8bf90,0x3f403f00,
  0x0,0x0,0x0,0x0,0x0,0x0,0x0,0x0,0x0,0x0,0x0,0x0,0xbfd83f90,0x3f40bf00,0x0,0x0,
  0x0,0x0,0x0,0x0,0x0,0x0,0x0,0x0,0x0,0x0,0x0,0x0,0x0,0x0,0xbfd83f90,0x3f40bf00,
  0x0,0x0,0x0,0x0,0x0,0x0,0x0,0x0,0x0,0x0,0x0,0x0,0x0,0x0,0x0,0x0,
  0x0,0x0,0x0,0x0,0x0,0x0,0x0,0x0,0x0,0x0,0x0,0x0,0x0,0x0,0x0,0x0,
  0x0,0x0,0x0,0x0,0x0,0x0,0x0,0x0,0x0,0x0,0x0,0x0,0x0,0x0,0x0,0x0,
  0x0,0x0,0x0,0x0,0x0,0x0,0x0,0x0,0x0,0x0,0x0,0x0,0x0,0x0,0x0,0x0,
  0x0,0x0,0x0,0x0,0x0,0x0,0x0,0x0,0x0,0x0,0x0,0x0,0x0,0x0,0x0,0x0,
  0x0,0x0,0x0,0x0,0x0,0x0,0x0,0x0,0x0,0x0,0x0,0x0,0x0,0x0,0x0,0x0,
  0x0,0x0,0x0,0x0,0x0,0x0,0x0,0x0,0x0,0x0,0x0,0x0,0x0,0x0,0x0,0x0,
  0x0,0x0,0x0,0x0,0x0,0x0,0x0,0x0,0x0,0x0,0x0,0x0,0x0,0x0,0x0,0x0,
  0x0,0x0,0x0,0x0,0x0,0x0,0x0,0x0,0x0,0x0,0x0,0x0,0x0,0x0,0x0,0x0,
  0x0,0x0,0x0,0x0,0x0,0x0,0x0,0x0,0x0,0x0,0x0,0x0,0x0,0x0,0x0,0x0,
  0x0,0x0,0x0,0x0,0x0,0x0,0x0,0x0,0x0,0x0,0x0,0x0,0x0,0x0,0x0,0x0,
  0x0,0x0,0x0,0x0,0x0,0x0,0x0,0x0,0x0,0x0,0x0,0x0,0x0,0x0,0x0,0x0,
  0x0,0x0,0x0,0x0,0x0,0x0,0x0,0x0,0x0,0x0,0x0,0x0,0x0,0x0,0x0,0x0,
  0x0,0x0,0x0,0x0,0x0,0x0,0x0,0x0,0x0,0x0,0x0,0x0,0x0,0x0,0x0,0x0,
  0x0,0x0,0x0,0x0,0x0,0x0,0x0,0x0,0x0,0x0,0x0,0x0,0x0,0x0,0x0,0x0,
  0x0,0x0,0x0,0x0,0x0,0x0,0x0,0x0,0x0,0x0,0x0,0x0,0x0,0x0,0x0,0x0,
  0x180000,0x440,0x860,0x10000000,0x180010,0x4d0,0x8f0,0x10000000,0x180020,0x560,0x940,0x10000000,0x180030,0x5f0,0x9d0,0x10000000,
  0x180040,0x600,0xa20,0x201b3180,0x180050,0x690,0xab0,0x201b3190,0x180060,0x720,0xb00,0x201b31a0,0x180070,0x7b0,0xb90,0x201b31b0,
  0x180610,0x1040,0x1460,0x401b0000,0x180600,0x10d0,0x14f0,0x401b0010,0x180630,0x1160,0x1540,0x401b0020,0x180620,0x11f0,0x15d0,0x401b0030,
  0x180650,0x1200,0x1620,0x201e3180,0x180640,0x1290,0x16b0,0x201e3190,0x180670,0x1320,0x1700,0x201e31a0,0x180660,0x13b0,0x1790,0x201e31b0,
  0x180c20,0x450,0x868,0x401e0020,0x180c30,0x4c0,0x8f8,0x401e0030,0x180c00,0x570,0x948,0x401e0000,0x180c10,0x5e0,0x9d8,0x401e0010,
  0x180c60,0x610,0xa28,0x202131a0,0x180c70,0x680,0xab8,0x202131b0,0x180c40,0x730,0xb08,0x20213180,0x180c50,0x7a0,0xb98,0x20213190,
  0x181230,0x1050,0x1468,0x40210020,0x181220,0x10c0,0x14f8,0x40210030,0x181210,0x1170,0x1548,0x40210000,0x181200,0x11e0,0x15d8,0x40210010,
  0x181270,0x1210,0x1628,0x10000000,0x181260,0x1280,0x16b8,0x10000000,0x181250,0x1330,0x1708,0x10000000,0x181240,0x13a0,0x1798,0x10000000,
  0x181840,0x460,0x870,0x10000000,0x181850,0x4f0,0x8e0,0x10000000,0x181860,0x540,0x950,0x10000000,0x181870,0x5d0,0x9c0,0x10000000,
  0x181800,0x620,0xa30,0x201b31c0,0x181810,0x6b0,0xaa0,0x201b31d0,0x181820,0x700,0xb10,0x201b31e0,0x181830,0x790,0xb80,0x201b31f0,
  0x181e50,0x1060,0x1470,0x401b0040,0x181e40,0x10f0,0x14e0,0x401b0050,0x181e70,0x1140,0x1550,0x401b0060,0x181e60,0x11d0,0x15c0,0x401b0070,
  0x181e10,0x1220,0x1630,0x201e31c0,0x181e00,0x12b0,0x16a0,0x201e31d0,0x181e30,0x1300,0x1710,0x201e31e0,0x181e20,0x1390,0x1780,0x201e31f0,
  0x182460,0x470,0x878,0x401e0060,0x182470,0x4e0,0x8e8,0x401e0070,0x182440,0x550,0x958,0x401e0040,0x182450,0x5c0,0x9c8,0x401e0050,
  0x182420,0x630,0xa38,0x202131e0,0x182430,0x6a0,0xaa8,0x202131f0,0x182400,0x710,0xb18,0x202131c0,0x182410,0x780,0xb88,0x202131d0,
  0x182a70,0x1070,0x1478,0x40210060,0x182a60,0x10e0,0x14e8,0x40210070,0x182a50,0x1150,0x1558,0x40210040,0x182a40,0x11c0,0x15c8,0x40210050,
  0x182a30,0x1230,0x1638,0x10000000,0x182a20,0x12a0,0x16a8,0x10000000,0x182a10,0x1310,0x1718,0x10000000,0x182a00,0x1380,0x1788,0x10000000,
  0xbfd8bf90,0x3f403f00,0x0,0x0,0x0,0x0,0x0,0x0,0x0,0x0,0x0,0x0,0x0,0x0,0x0,0x0,
  0x0,0x0,0xbfd8bf90,0x3f403f00,0x0,0x0,0x0,0x0,0x0,0x0,0x0,0x0,0x0,0x0,0x0,0x0,
  0xbfd83f90,0x3f40bf00,0x0,0x0,0x0,0x0,0x0,0x0,0x0,0x0,0x0,0x0,0x0,0x0,0x0,0x0,
  0x0,0x0,0xbfd83f90,0x3f40bf00,0x0,0x0,0x0,0x0,0x0,0x0,0x0,0x0,0x0,0x0,0x0,0x0,
  0x0,0x0,0x0,0x0,0xbfd8bf90,0x3f403f00,0x0,0x0,0x0,0x0,0x0,0x0,0x0,0x0,0x0,0x0,
  0x0,0x0,0x0,0x0,0x0,0x0,0xbfd8bf90,0x3f403f00,0x0,0x0,0x0,0x0,0x0,0x0,0x0,0x0,
  0x0,0x0,0x0,0x0,0xbfd83f90,0x3f40bf00,0x0,0x0,0x0,0x0,0x0,0x0,0x0,0x0,0x0,0x0,
  0x0,0x0,0x0,0x0,0x0,0x0,0xbfd83f90,0x3f40bf00,0x0,0x0,0x0,0x0,0x0,0x0,0x0,0x0,
  0x0,0x0,0x0,0x0,0x0,0x0,0x0,0x0,0xbfd8bf90,0x3f403f00,0x0,0x0,0x0,0x0,0x0,0x0,
  0x0,0x0,0x0,0x0,0x0,0x0,0x0,0x0,0x0,0x0,0xbfd8bf90,0x3f403f00,0x0,0x0,0x0,0x0,
  0x0,0x0,0x0,0x0,0x0,0x0,0x0,0x0,0xbfd83f90,0x3f40bf00,0x0,0x0,0x0,0x0,0x0,0x0,
  0x0,0x0,0x0,0x0,0x0,0x0,0x0,0x0,0x0,0x0,0xbfd83f90,0x3f40bf00,0x0,0x0,0x0,0x0,
  0x0,0x0,0x0,0x0,0x0,0x0,0x0,0x0,0x0,0x0,0x0,0x0,0xbfd8bf90,0x3f403f00,0x0,0x0,
  0x0,0x0,0x0,0x0,0x0,0x0,0x0,0x0,0x0,0x0,0x0,0x0,0x0,0x0,0xbfd8bf90,0x3f403f00,
  0x0,0x0,0x0,0x0,0x0,0x0,0x0,0x0,0x0,0x0,0x0,0x0,0xbfd83f90,0x3f40bf00,0x0,0x0,
  0x0,0x0,0x0,0x0,0x0,0x0,0x0,0x0,0x0,0x0,0x0,0x0,0x0,0x0,0xbfd83f90,0x3f40bf00,
};

__global__ __launch_bounds__(256, 1) void wino_main_kernel(const char* __restrict__ x, char* __restrict__ Uq, char* __restrict__ out, const char* __restrict__ wt) {
  __shared__ __attribute__((aligned(1024))) char smem[163840];
  asm volatile("s_getpc_b64 s[40:41]\n\tLwino_pc:\n\ts_bfe_u32 s42, %0, 0x50003\n\ts_lshl_b32 s42, s42, 12\n\tv_lshl_add_u32 v203, %1, 4, s42\n\t"
               "v_min_u32 v203, (Lwino_end-Lwino_pc-16)&-16, v203\n\tglobal_load_dwordx4 v[100:103], v203, s[40:41]"
               :: "s"(blockIdx.x), "v"(threadIdx.x) : "s40", "s41", "s42", "v100", "v101", "v102", "v103", "v203", "scc", "memory");
  const int lane = threadIdx.x & 63;
  const int w = __builtin_amdgcn_readfirstlane(threadIdx.x >> 6);
  const int sub = blockIdx.x >> 3, bx = sub & 15, by = 4 * (blockIdx.x & 7) + (sub >> 4);
  const unsigned long long px = (unsigned long long)x, pu = (unsigned long long)Uq, py = (unsigned long long)out, pw = (unsigned long long)wt;
  const int xbase = ((16 * by - 1) * IMG_W + 32 * bx) * PXB;
  const int ybase = (16 * by * IMG_W + 32 * bx) * PXB;
  const unsigned medge = (bx == 0 ? 0xffffffffu : 0xbfffffffu) & (bx == 15 ? 0xffffffffu : 0xdfffffffu);
  unsigned fso, fuo, fk0, fk5, fs8;
  {
    const unsigned p = blockIdx.x - 1, hb = p & 1, q = (p >> 1) & 127, r = q / 3, kb = q - 3 * r, wv = r & 3, chunk = r >> 2;
    fs8 = 1 - (kb >> 1);
    fso = p < 144 ? chunk * 12288 + hb * 3072 + (w >> 1) * 1536 + (kb < 2 ? kb * 8 : 256) + ((2 * wv + (w & 1)) << (4 + fs8)) : 0x40000000u;
    fuo = p < 144 ? (chunk * 18 + kb) * 12288 + wv * 3072 + hb * 512 + w * 128 : 0x40000000u;
    fk0 = chunk < 2 ? 0x3ea80a81u : 0x3f638e39u; fk5 = chunk < 2 ? 0xbebd0bd1u : 0x3f638e39u;
  }
  const unsigned fwo = (((lane & 3) + 4 * (lane & 4)) * CH + 4 * (lane >> 5) + ((lane >> 3) & 3)) * 2;
  const unsigned flw = 18432 + w * 3072 + lane * 2, fst = lane < 48 ? (lane >> 3) * 36864 + (lane & 7) * 16 : 0x40000000u;
  unsigned dummy = 0;
  asm volatile(
"s_mov_b32 s52, %15\n\t"
"s_and_b32 s53, %16, 0xffff\n\t"
"s_mov_b32 s54, 663552\n\t"
"s_mov_b32 s55, 0x20000\n\t"
"s_mov_b32 s56, %17\n\t"
"s_mov_b32 s57, %18\n\t"
"s_mov_b32 s58, %19\n\t"
"s_mov_b32 s59, %20\n\t"
"s_mov_b32 s74, %21\n\t"
"s_mul_i32 s66, %11, 4\n\t"
"s_mov_b32 s73, %9\n\t"
"s_or_b32 s72, %11, %9\n\t"
"v_mov_b32 v32, 0\n\t"
"v_mov_b32 v33, 0\n\t"
"v_mov_b32 v34, 0\n\t"
"v_mov_b32 v35, 0\n\t"
"v_mov_b32 v36, 0\n\t"
"v_mov_b32 v37, 0\n\t"
"v_mov_b32 v38, 0\n\t"
"v_mov_b32 v39, 0\n\t"
"v_mov_b32 v40, 0\n\t"
"v_mov_b32 v118, %22\n\t"
"v_mov_b32 v42, %23\n\t"
"v_add_u32 v43, 18432, v252\n\t"
"v_mov_b32 v44, %24\n\t"
"v_mov_b32 v119, %25\n\t"
"s_mov_b32 s8, %1\n\t"
"s_and_b32 s9, %2, 0xffff\n\t"
"s_mov_b32 s10, 0x200000\n\t"
"s_mov_b32 s11, 0x20000\n\t"
"s_mov_b32 s12, %3\n\t"
"s_and_b32 s13, %4, 0xffff\n\t"
"s_mov_b32 s14, 100663296\n\t"
"s_mov_b32 s15, 0x20000\n\t"
"s_mov_b32 s16, %5\n\t"
"s_and_b32 s17, %6, 0xffff\n\t"
"s_mov_b32 s18, 100663296\n\t"
"s_mov_b32 s19, 0x20000\n\t"
"s_mov_b32 s21, %7\n\t"
"s_lshr_b32 s60, %9, 1\n\t"
"s_mul_i32 s61, s60, 384\n\t"
"s_add_u32 s22, %8, s61\n\t"
"s_mul_i32 s61, s60, 196608\n\t"
"s_add_u32 s23, %8, s61\n\t"
"s_mul_i32 s24, %9, 1536\n\t"
"s_add_u32 s24, s24, 43008\n\t"
"s_mul_i32 s25, %9, 19968\n\t"
"s_add_u32 s25, s25, 61440\n\t"
"s_mul_i32 s26, s60, 19968\n\t"
"s_add_u32 s26, s26, 141312\n\t"
"s_sub_u32 s61, s60, %9\n\t"
"s_add_u32 s61, s61, 4\n\t"
"s_mul_i32 s27, s61, 589824\n\t"
"s_bitcmp1_b32 %9, 0\n\t"
"s_mov_b32 s36, 0x0fff0000\n\t"
"s_cselect_b32 s36, 0xfff0, s36\n\t"
"s_mov_b32 s37, s36\n\t"
"s_mov_b32 s44, 0x0fff0fff\n\t"
"s_mov_b32 s45, 0x0fff0fff\n\t"
"s_mov_b64 s[46:47], %12\n\t"
"s_add_u32 s48, s46, 12288\n\t"
"s_addc_u32 s49, s47, 0\n\t"
"s_mul_i32 s50, %9, 122880\n\t"
"s_lshl_b32 s51, s60, 30\n\t"
"s_add_u32 s51, s51, s50\n\t"
"s_mov_b32 s40, %14\n\t"
"s_sub_u32 s42, %7, 384\n\t"
"s_mov_b32 s38, 0x3f2aaaab\n\t"
"s_mov_b32 s39, 0x3fc00000\n\t"
"s_mov_b32 s41, 0x3ee38e39\n\t"
"s_mov_b32 s43, 0x40100000\n\t"
"s_nop 4\n\t"
"s_branch Lpro_%=\n\t"
"Lfb_%=:\n\t"
"s_mov_b32 s69, 0\n\t"
"Lfb1_%=:\n\t"
"s_lshr_b32 s71, s69, 2\n\t"
"s_and_b32 s70, s69, 3\n\t"
"s_and_b32 s60, s71, 1\n\t"
"s_lshr_b32 s61, s71, 1\n\t"
"s_mul_i32 s62, s61, 171\n\t"
"s_lshr_b32 s62, s62, 9\n\t"
"s_mul_i32 s63, s62, 3\n\t"
"s_sub_u32 s61, s61, s63\n\t"
"s_and_b32 s63, s62, 3\n\t"
"s_lshr_b32 s62, s62, 2\n\t"
"s_mul_i32 s56, s62, 12288\n\t"
"s_mul_i32 s64, s60, 3072\n\t"
"s_add_u32 s56, s56, s64\n\t"
"s_lshr_b32 s64, s70, 1\n\t"
"s_mul_i32 s64, s64, 1536\n\t"
"s_add_u32 s56, s56, s64\n\t"
"s_and_b32 s64, s70, 1\n\t"
"s_lshl1_add_u32 s64, s63, s64\n\t"
"s_lshl_b32 s65, s61, 3\n\t"
"s_cmp_lt_u32 s61, 2\n\t"
"s_cselect_b32 s74, 1, 0\n\t"
"s_cselect_b32 s65, s65, 0x100\n\t"
"s_add_u32 s56, s56, s65\n\t"
"s_add_u32 s65, s74, 4\n\t"
"s_lshl_b32 s64, s64, s65\n\t"
"s_add_u32 s56, s56, s64\n\t"
"s_mul_i32 s57, s62, 18\n\t"
"s_add_u32 s57, s57, s61\n\t"
"s_mul_i32 s57, s57, 12288\n\t"
"s_mul_i32 s64, s63, 3072\n\t"
"s_add_u32 s57, s57, s64\n\t"
"s_lshl_b32 s64, s60, 9\n\t"
"s_add_u32 s57, s57, s64\n\t"
"s_lshl_b32 s64, s70, 7\n\t"
"s_add_u32 s57, s57, s64\n\t"
"s_mov_b32 s64, 0x3f638e39\n\t"
"s_cmp_lt_u32 s62, 2\n\t"
"s_cselect_b32 s58, 0x3ea80a81, s64\n\t"
"s_cselect_b32 s59, 0xbebd0bd1, s64\n\t"
"v_mad_u32_u24 v41, v119, s74, v118\n\t"
"s_add_u32 s60, s56, 0\n\t"
"buffer_load_short_d16_hi v32, v41, s[52:55], s60 offen\n\t"
"s_add_u32 s60, s56, 73728\n\t"
"buffer_load_short_d16_hi v33, v41, s[52:55], s60 offen\n\t"
"s_add_u32 s60, s56, 147456\n\t"
"buffer_load_short_d16_hi v34, v41, s[52:55], s60 offen\n\t"
"s_add_u32 s60, s56, 221184\n\t"
"buffer_load_short_d16_hi v35, v41, s[52:55], s60 offen\n\t"
"s_add_u32 s60, s56, 294912\n\t"
"buffer_load_short_d16_hi v36, v41, s[52:55], s60 offen\n\t"
"s_add_u32 s60, s56, 368640\n\t"
"buffer_load_short_d16_hi v37, v41, s[52:55], s60 offen\n\t"
"s_add_u32 s60, s56, 442368\n\t"
"buffer_load_short_d16_hi v38, v41, s[52:55], s60 offen\n\t"
"s_add_u32 s60, s56, 516096\n\t"
"buffer_load_short_d16_hi v39, v41, s[52:55], s60 offen\n\t"
"s_add_u32 s60, s56, 589824\n\t"
"buffer_load_short_d16_hi v40, v41, s[52:55], s60 offen\n\t"
"s_waitcnt vmcnt(6)\n\t"
"v_fma_f32 v48, v34, s41, v32\n\t"
"v_fma_f32 v51, v34, s43, v32\n\t"
"s_waitcnt vmcnt(3)\n\t"
"v_fma_f32 v49, v37, s41, v35\n\t"
"v_fma_f32 v52, v37, s43, v35\n\t"
"s_waitcnt vmcnt(0)\n\t"
"v_fma_f32 v50, v40, s41, v38\n\t"
"v_fma_f32 v53, v40, s43, v38\n\t"
"v_fma_f32 v54, v33, s38, v48\n\t"
"v_fma_f32 v57, -v33, s38, v48\n\t"
"v_fma_f32 v60, v33, s39, v51\n\t"
"v_fma_f32 v63, -v33, s39, v51\n\t"
"v_fma_f32 v55, v36, s38, v49\n\t"
"v_fma_f32 v58, -v36, s38, v49\n\t"
"v_fma_f32 v61, v36, s39, v52\n\t"
"v_fma_f32 v64, -v36, s39, v52\n\t"
"v_fma_f32 v56, v39, s38, v50\n\t"
"v_fma_f32 v59, -v39, s38, v50\n\t"
"v_fma_f32 v62, v39, s39, v53\n\t"
"v_fma_f32 v65, -v39, s39, v53\n\t"
"v_mul_f32 v70, s58, v32\n\t"
"v_mul_f32 v71, s58, v35\n\t"
"v_mul_f32 v72, s58, v38\n\t"
"v_mul_f32 v73, 0xbf54ad4b, v54\n\t"
"v_mul_f32 v74, 0xbf54ad4b, v55\n\t"
"v_mul_f32 v75, 0xbf54ad4b, v56\n\t"
"v_mul_f32 v76, 0xbefc0fc1, v57\n\t"
"v_mul_f32 v77, 0xbefc0fc1, v58\n\t"
"v_mul_f32 v78, 0xbefc0fc1, v59\n\t"
"v_mul_f32 v79, 0xbe7c0fc1, v60\n\t"
"v_mul_f32 v80, 0xbe7c0fc1, v61\n\t"
"v_mul_f32 v81, 0xbe7c0fc1, v62\n\t"
"v_mul_f32 v82, 0x3ea80a81, v63\n\t"
"v_mul_f32 v83, 0x3ea80a81, v64\n\t"
"v_mul_f32 v84, 0x3ea80a81, v65\n\t"
"v_mul_f32 v85, s59, v34\n\t"
"v_mul_f32 v86, s59, v37\n\t"
"v_mul_f32 v87, s59, v40\n\t"
"v_cvt_pk_bf16_f32 v88, v70, v71\n\t"
"v_cvt_pk_bf16_f32 v89, v72, v73\n\t"
"v_cvt_pk_bf16_f32 v90, v74, v75\n\t"
"v_cvt_pk_bf16_f32 v91, v76, v77\n\t"
"v_cvt_pk_bf16_f32 v92, v78, v79\n\t"
"v_cvt_pk_bf16_f32 v93, v80, v81\n\t"
"v_cvt_pk_bf16_f32 v94, v82, v83\n\t"
"v_cvt_pk_bf16_f32 v95, v84, v85\n\t"
"v_cvt_pk_bf16_f32 v96, v86, v87\n\t"
"ds_write_b16 v42, v88 offset:0\n\t"
"ds_write_b16_d16_hi v42, v88 offset:1024\n\t"
"ds_write_b16 v42, v89 offset:2048\n\t"
"ds_write_b16_d16_hi v42, v89 offset:128\n\t"
"ds_write_b16 v42, v90 offset:1152\n\t"
"ds_write_b16_d16_hi v42, v90 offset:2176\n\t"
"ds_write_b16 v45, v91 offset:256\n\t"
"ds_write_b16_d16_hi v45, v91 offset:1280\n\t"
"ds_write_b16 v45, v92 offset:2304\n\t"
"ds_write_b16_d16_hi v42, v92 offset:384\n\t"
"ds_write_b16 v42, v93 offset:1408\n\t"
"ds_write_b16_d16_hi v42, v93 offset:2432\n\t"
"ds_write_b16 v45, v94 offset:512\n\t"
"ds_write_b16_d16_hi v45, v94 offset:1536\n\t"
"ds_write_b16 v45, v95 offset:2560\n\t"
"ds_write_b16_d16_hi v42, v95 offset:640\n\t"
"ds_write_b16 v42, v96 offset:1664\n\t"
"ds_write_b16_d16_hi v42, v96 offset:2688\n\t"
"ds_read_b128 v[48:51], v43 offset:0\n\t"
"ds_read_b128 v[52:55], v43 offset:1024\n\t"
"ds_read_b128 v[56:59], v43 offset:2048\n\t"
"s_waitcnt lgkmcnt(2)\n\t"
"buffer_store_dwordx4 v[48:51], v44, s[8:11], s57 offen offset:0 sc1\n\t"
"s_waitcnt lgkmcnt(1)\n\t"
"buffer_store_dwordx4 v[52:55], v44, s[8:11], s57 offen offset:1024 sc1\n\t"
"s_waitcnt lgkmcnt(0)\n\t"
"buffer_store_dwordx4 v[56:59], v44, s[8:11], s57 offen offset:2048 sc1\n\t"
"s_waitcnt vmcnt(0) lgkmcnt(0)\n\t"
"s_add_u32 s69, s69, 1\n\t"
"s_cmp_lt_u32 s69, 576\n\t"
"s_cbranch_scc1 Lfb1_%=\n\t"
"s_branch Lgo_%=\n\t"
"Lpro_%=:\n\t"
"v_mad_u32_u24 v41, v119, s74, v118\n\t"
"s_add_u32 s60, s56, 0\n\t"
"buffer_load_short_d16_hi v32, v41, s[52:55], s60 offen\n\t"
"s_add_u32 s60, s56, 73728\n\t"
"buffer_load_short_d16_hi v33, v41, s[52:55], s60 offen\n\t"
"s_add_u32 s60, s56, 147456\n\t"
"buffer_load_short_d16_hi v34, v41, s[52:55], s60 offen\n\t"
"s_add_u32 s60, s56, 221184\n\t"
"buffer_load_short_d16_hi v35, v41, s[52:55], s60 offen\n\t"
"s_add_u32 s60, s56, 294912\n\t"
"buffer_load_short_d16_hi v36, v41, s[52:55], s60 offen\n\t"
"s_add_u32 s60, s56, 368640\n\t"
"buffer_load_short_d16_hi v37, v41, s[52:55], s60 offen\n\t"
"s_add_u32 s60, s56, 442368\n\t"
"buffer_load_short_d16_hi v38, v41, s[52:55], s60 offen\n\t"
"s_add_u32 s60, s56, 516096\n\t"
"buffer_load_short_d16_hi v39, v41, s[52:55], s60 offen\n\t"
"s_add_u32 s60, s56, 589824\n\t"
"buffer_load_short_d16_hi v40, v41, s[52:55], s60 offen\n\t"
"s_add_u32 s60, s66, 1867776\n\t"
"buffer_load_dword v46, off, s[8:11], s60 sc1\n\t"
"s_mul_i32 s70, s66, 1\n\t"
"s_add_u32 s70, s70, 1900544\n\t"
"s_mov_b32 s60, s70\n\t"
"buffer_load_dword v47, off, s[8:11], s60 sc1\n\t"
"global_load_dwordx4 v[244:247], v252, s[46:47] offset:2048\n\t"
"global_load_dwordx4 v[248:251], v252, s[48:49] offset:0\n\t"
"global_load_dwordx4 v[240:243], v252, s[46:47] offset:0\n\t"
"global_load_dwordx4 v[196:199], v252, s[46:47] offset:1024\n\t"
"global_load_dwordx4 v[220:223], v252, s[48:49] offset:1024\n\t"
"s_cmp_lg_u32 s72, 0\n\t"
"s_cbranch_scc1 Lnl1_%=\n\t"
"v_mul_u32_u24 v116, 1, v252\n\t"
"v_lshrrev_b32 v116, 2, v116\n\t"
"v_mul_u32_u24 v117, 1, v116\n\t"
"s_mov_b32 s60, 1900544\n\t"
"buffer_load_dword v104, v117, s[8:11], s60 offen sc1\n\t"
"s_mov_b32 s60, 1900800\n\t"
"buffer_load_dword v105, v117, s[8:11], s60 offen sc1\n\t"
"s_mov_b32 s60, 1901056\n\t"
"buffer_load_dword v106, v117, s[8:11], s60 offen sc1\n\t"
"s_mov_b32 s60, 1901312\n\t"
"buffer_load_dword v107, v117, s[8:11], s60 offen sc1\n\t"
"s_waitcnt vmcnt(0)\n\t"
"v_add_u32 v104, 0x10000, v104\n\t"
"v_add_u32 v105, 0x10000, v105\n\t"
"v_add_u32 v106, 0x10000, v106\n\t"
"v_add_u32 v107, 0x10000, v107\n\t"
"v_and_b32 v104, 0xffff0000, v104\n\t"
"v_and_b32 v105, 0xffff0000, v105\n\t"
"v_and_b32 v106, 0xffff0000, v106\n\t"
"v_and_b32 v107, 0xffff0000, v107\n\t"
"s_mov_b32 s60, 1835008\n\t"
"buffer_store_dword v104, v116, s[8:11], s60 offen sc1\n\t"
"s_mov_b32 s60, 1835264\n\t"
"buffer_store_dword v105, v116, s[8:11], s60 offen sc1\n\t"
"s_mov_b32 s60, 1835520\n\t"
"buffer_store_dword v106, v116, s[8:11], s60 offen sc1\n\t"
"s_mov_b32 s60, 1835776\n\t"
"buffer_store_dword v107, v116, s[8:11], s60 offen sc1\n\t"
"Lnl1_%=:\n\t"
"v_xor_b32 v45, 8, v42\n\t"
"s_waitcnt vmcnt(13)\n\t"
"v_fma_f32 v48, v34, s41, v32\n\t"
"v_fma_f32 v51, v34, s43, v32\n\t"
"s_waitcnt vmcnt(10)\n\t"
"v_fma_f32 v49, v37, s41, v35\n\t"
"v_fma_f32 v52, v37, s43, v35\n\t"
"s_waitcnt vmcnt(7)\n\t"
"v_fma_f32 v50, v40, s41, v38\n\t"
"v_fma_f32 v53, v40, s43, v38\n\t"
"v_fma_f32 v54, v33, s38, v48\n\t"
"v_fma_f32 v57, -v33, s38, v48\n\t"
"v_fma_f32 v60, v33, s39, v51\n\t"
"v_fma_f32 v63, -v33, s39, v51\n\t"
"v_fma_f32 v55, v36, s38, v49\n\t"
"v_fma_f32 v58, -v36, s38, v49\n\t"
"v_fma_f32 v61, v36, s39, v52\n\t"
"v_fma_f32 v64, -v36, s39, v52\n\t"
"v_fma_f32 v56, v39, s38, v50\n\t"
"v_fma_f32 v59, -v39, s38, v50\n\t"
"v_fma_f32 v62, v39, s39, v53\n\t"
"v_fma_f32 v65, -v39, s39, v53\n\t"
"v_mul_f32 v70, s58, v32\n\t"
"v_mul_f32 v71, s58, v35\n\t"
"v_mul_f32 v72, s58, v38\n\t"
"v_mul_f32 v73, 0xbf54ad4b, v54\n\t"
"v_mul_f32 v74, 0xbf54ad4b, v55\n\t"
"v_mul_f32 v75, 0xbf54ad4b, v56\n\t"
"v_mul_f32 v76, 0xbefc0fc1, v57\n\t"
"v_mul_f32 v77, 0xbefc0fc1, v58\n\t"
"v_mul_f32 v78, 0xbefc0fc1, v59\n\t"
"v_mul_f32 v79, 0xbe7c0fc1, v60\n\t"
"v_mul_f32 v80, 0xbe7c0fc1, v61\n\t"
"v_mul_f32 v81, 0xbe7c0fc1, v62\n\t"
"v_mul_f32 v82, 0x3ea80a81, v63\n\t"
"v_mul_f32 v83, 0x3ea80a81, v64\n\t"
"v_mul_f32 v84, 0x3ea80a81, v65\n\t"
"v_mul_f32 v85, s59, v34\n\t"
"v_mul_f32 v86, s59, v37\n\t"
"v_mul_f32 v87, s59, v40\n\t"
"v_cvt_pk_bf16_f32 v88, v70, v71\n\t"
"v_cvt_pk_bf16_f32 v89, v72, v73\n\t"
"v_cvt_pk_bf16_f32 v90, v74, v75\n\t"
"v_cvt_pk_bf16_f32 v91, v76, v77\n\t"
"v_cvt_pk_bf16_f32 v92, v78, v79\n\t"
"v_cvt_pk_bf16_f32 v93, v80, v81\n\t"
"v_cvt_pk_bf16_f32 v94, v82, v83\n\t"
"v_cvt_pk_bf16_f32 v95, v84, v85\n\t"
"v_cvt_pk_bf16_f32 v96, v86, v87\n\t"
"ds_write_b16 v42, v88 offset:0\n\t"
"ds_write_b16_d16_hi v42, v88 offset:1024\n\t"
"ds_write_b16 v42, v89 offset:2048\n\t"
"ds_write_b16_d16_hi v42, v89 offset:128\n\t"
"ds_write_b16 v42, v90 offset:1152\n\t"
"ds_write_b16_d16_hi v42, v90 offset:2176\n\t"
"ds_write_b16 v45, v91 offset:256\n\t"
"ds_write_b16_d16_hi v45, v91 offset:1280\n\t"
"ds_write_b16 v45, v92 offset:2304\n\t"
"ds_write_b16_d16_hi v42, v92 offset:384\n\t"
"ds_write_b16 v42, v93 offset:1408\n\t"
"ds_write_b16_d16_hi v42, v93 offset:2432\n\t"
"ds_write_b16 v45, v94 offset:512\n\t"
"ds_write_b16_d16_hi v45, v94 offset:1536\n\t"
"ds_write_b16 v45, v95 offset:2560\n\t"
"ds_write_b16_d16_hi v42, v95 offset:640\n\t"
"ds_write_b16 v42, v96 offset:1664\n\t"
"ds_write_b16_d16_hi v42, v96 offset:2688\n\t"
"ds_read_b128 v[48:51], v43 offset:0\n\t"
"ds_read_b128 v[52:55], v43 offset:1024\n\t"
"ds_read_b128 v[56:59], v43 offset:2048\n\t"
"s_waitcnt lgkmcnt(2)\n\t"
"buffer_store_dwordx4 v[48:51], v44, s[8:11], s57 offen offset:0 sc1\n\t"
"s_waitcnt lgkmcnt(1)\n\t"
"buffer_store_dwordx4 v[52:55], v44, s[8:11], s57 offen offset:1024 sc1\n\t"
"s_waitcnt lgkmcnt(0)\n\t"
"buffer_store_dwordx4 v[56:59], v44, s[8:11], s57 offen offset:2048 sc1\n\t"
"s_waitcnt vmcnt(6)\n\t"
"v_and_b32 v254, s40, v251\n\t"
"v_add_u32 v254, s42, v254\n\t"
"s_lshr_b32 s40, s73, 1\n\t"
"s_mul_i32 s40, s40, 768\n\t"
"s_add_u32 s40, s40, s22\n\t"
"s_waitcnt vmcnt(0)\n\t"
"s_barrier\n\t"
"v_readfirstlane_b32 s67, v46\n\t"
"v_readfirstlane_b32 s71, v47\n\t"
"s_add_u32 s67, s67, 1\n\t"
"s_and_b32 s67, s67, 0xffff\n\t"
"s_add_u32 s71, s71, 0x10000\n\t"
"s_and_b32 s71, s71, 0xffff0000\n\t"
"s_or_b32 s71, s71, s67\n\t"
"s_mov_b32 s68, 3000\n\t"
"s_cmp_lg_u32 s73, 0\n\t"
"s_cbranch_scc1 Lnl2_%=\n\t"
"v_mov_b32 v47, s71\n\t"
"s_mov_b64 exec, 1\n\t"
"buffer_store_dword v47, off, s[8:11], s70 sc1\n\t"
"s_mov_b64 exec, -1\n\t"
"Lnl2_%=:\n\t"
"s_add_u32 m0, s25, 0\n\t"
"s_nop 0\n\t"
"buffer_load_dwordx4 v254, s[12:15], 0 offen offset:0 nt lds\n\t"
"s_add_u32 m0, s26, 0\n\t"
"s_mov_b64 exec, s[36:37]\n\t"
"buffer_load_dwordx4 v254, s[12:15], s27 offen offset:0 nt lds\n\t"
"s_mov_b64 exec, -1\n\t"
"s_add_u32 m0, s24, 0\n\t"
"s_add_u32 s28, s21, 0\n\t"
"buffer_load_dwordx4 v247, s[12:15], s28 offen offset:0 nt lds\n\t"
"s_add_u32 m0, s24, 6144\n\t"
"s_add_u32 s28, s21, 196608\n\t"
"buffer_load_dwordx4 v247, s[12:15], s28 offen offset:0 nt lds\n\t"
"s_add_u32 m0, s24, 12288\n\t"
"s_add_u32 s28, s21, 393216\n\t"
"buffer_load_dwordx4 v247, s[12:15], s28 offen offset:0 nt lds\n\t"
"s_add_u32 m0, s24, 19968\n\t"
"s_add_u32 s28, s21, 589824\n\t"
"buffer_load_dwordx4 v247, s[12:15], s28 offen offset:0 nt lds\n\t"
"s_add_u32 m0, s24, 26112\n\t"
"s_add_u32 s28, s21, 786432\n\t"
"buffer_load_dwordx4 v247, s[12:15], s28 offen offset:0 nt lds\n\t"
"s_add_u32 m0, s24, 32256\n\t"
"s_add_u32 s28, s21, 983040\n\t"
"buffer_load_dwordx4 v247, s[12:15], s28 offen offset:0 nt lds\n\t"
"s_add_u32 m0, s24, 39936\n\t"
"s_add_u32 s28, s21, 1179648\n\t"
"buffer_load_dwordx4 v247, s[12:15], s28 offen offset:0 nt lds\n\t"
"s_add_u32 m0, s24, 46080\n\t"
"s_add_u32 s28, s21, 1376256\n\t"
"buffer_load_dwordx4 v247, s[12:15], s28 offen offset:0 nt lds\n\t"
"s_add_u32 m0, s24, 52224\n\t"
"s_add_u32 s28, s21, 1572864\n\t"
"buffer_load_dwordx4 v247, s[12:15], s28 offen offset:0 nt lds\n\t"
"s_add_u32 m0, s24, 59904\n\t"
"s_add_u32 s28, s21, 1769472\n\t"
"buffer_load_dwordx4 v247, s[12:15], s28 offen offset:0 nt lds\n\t"
"s_cmp_lg_u32 s66, 0\n\t"
"s_cbranch_scc1 Lld4_%=\n\t"
"s_cmp_lg_u32 s73, 0\n\t"
"s_cbranch_scc1 Lld2_%=\n\t"
"Lld1_%=:\n\t"
"s_mov_b32 s60, 1900544\n\t"
"buffer_load_dword v108, v117, s[8:11], s60 offen sc1\n\t"
"s_mov_b32 s60, 1900800\n\t"
"buffer_load_dword v109, v117, s[8:11], s60 offen sc1\n\t"
"s_mov_b32 s60, 1901056\n\t"
"buffer_load_dword v110, v117, s[8:11], s60 offen sc1\n\t"
"s_mov_b32 s60, 1901312\n\t"
"buffer_load_dword v111, v117, s[8:11], s60 offen sc1\n\t"
"s_waitcnt vmcnt(0)\n\t"
"v_xor_b32 v112, v108, v104\n\t"
"v_xor_b32 v113, v109, v105\n\t"
"v_xor_b32 v114, v110, v106\n\t"
"v_xor_b32 v115, v111, v107\n\t"
"s_nop 1\n\t"
"v_writelane_b32 v112, 0, 0\n\t"
"s_nop 1\n\t"
"v_or3_b32 v112, v112, v113, v114\n\t"
"v_or_b32 v112, v112, v115\n\t"
"v_cmp_lt_u32 vcc, 0xffff, v112\n\t"
"s_nop 4\n\t"
"s_cmp_eq_u64 vcc, 0\n\t"
"s_cbranch_scc1 Lld3_%=\n\t"
"s_sub_u32 s68, s68, 1\n\t"
"s_cmp_lg_u32 s68, 0\n\t"
"s_cbranch_scc1 Lld1_%=\n\t"
"s_branch Lld2_%=\n\t"
"Lld3_%=:\n\t"
"s_mov_b32 s60, 1867776\n\t"
"buffer_store_dword v108, v116, s[8:11], s60 offen sc1\n\t"
"s_mov_b32 s60, 1868032\n\t"
"buffer_store_dword v109, v116, s[8:11], s60 offen sc1\n\t"
"s_mov_b32 s60, 1868288\n\t"
"buffer_store_dword v110, v116, s[8:11], s60 offen sc1\n\t"
"s_mov_b32 s60, 1868544\n\t"
"buffer_store_dword v111, v116, s[8:11], s60 offen sc1\n\t"
"s_mov_b32 s68, -1\n\t"
"Lld2_%=:\n\t"
"s_barrier\n\t"
"Lld4_%=:\n\t"
"s_add_u32 m0, s24, 66048\n\t"
"s_add_u32 s28, s21, 1966080\n\t"
"buffer_load_dwordx4 v247, s[12:15], s28 offen offset:0 nt lds\n\t"
"s_add_u32 m0, s24, 72192\n\t"
"s_add_u32 s28, s21, 2162688\n\t"
"buffer_load_dwordx4 v247, s[12:15], s28 offen offset:0 nt lds\n\t"
"s_add_u32 m0, s24, 79872\n\t"
"s_add_u32 s28, s21, 2359296\n\t"
"buffer_load_dwordx4 v247, s[12:15], s28 offen offset:0 nt lds\n\t"
"s_add_u32 m0, s24, 86016\n\t"
"s_add_u32 s28, s21, 2555904\n\t"
"buffer_load_dwordx4 v247, s[12:15], s28 offen offset:0 nt lds\n\t"
"s_add_u32 m0, s24, 92160\n\t"
"s_add_u32 s28, s21, 2752512\n\t"
"buffer_load_dwordx4 v247, s[12:15], s28 offen offset:0 nt lds\n\t"
"s_add_u32 m0, s24, 99840\n\t"
"s_add_u32 s28, s21, 2949120\n\t"
"buffer_load_dwordx4 v247, s[12:15], s28 offen offset:0 nt lds\n\t"
"s_add_u32 m0, s24, 105984\n\t"
"s_add_u32 s28, s21, 3145728\n\t"
"buffer_load_dwordx4 v247, s[12:15], s28 offen offset:0 nt lds\n\t"
"s_add_u32 m0, s24, 112128\n\t"
"s_add_u32 s28, s21, 3342336\n\t"
"buffer_load_dwordx4 v247, s[12:15], s28 offen offset:0 nt lds\n\t"
"s_add_u32 s60, s66, 1867776\n\t"
"buffer_load_dword v46, off, s[8:11], s60 sc1\n\t"
"s_waitcnt vmcnt(0)\n\t"
"s_barrier\n\t"
"ds_read_b64_tr_b16 v[120:121], v240 offset:0\n\t"
"ds_read_b64_tr_b16 v[122:123], v240 offset:1536\n\t"
"ds_read_b64_tr_b16 v[124:125], v241 offset:0\n\t"
"ds_read_b64_tr_b16 v[126:127], v241 offset:1536\n\t"
"ds_read_b64_tr_b16 v[128:129], v240 offset:19968\n\t"
"ds_read_b64_tr_b16 v[130:131], v240 offset:21504\n\t"
"ds_read_b64_tr_b16 v[132:133], v241 offset:19968\n\t"
"ds_read_b64_tr_b16 v[134:135], v241 offset:21504\n\t"
"ds_read_b64_tr_b16 v[136:137], v240 offset:39936\n\t"
"ds_read_b64_tr_b16 v[138:139], v240 offset:41472\n\t"
"ds_read_b64_tr_b16 v[140:141], v241 offset:39936\n\t"
"ds_read_b64_tr_b16 v[142:143], v241 offset:41472\n\t"
"v_add_u32 v240, 59904, v240\n\t"
"v_add_u32 v241, 59904, v241\n\t"
"ds_read_b64_tr_b16 v[144:145], v240 offset:0\n\t"
"ds_read_b64_tr_b16 v[146:147], v240 offset:1536\n\t"
"ds_read_b64_tr_b16 v[148:149], v241 offset:0\n\t"
"ds_read_b64_tr_b16 v[150:151], v241 offset:1536\n\t"
"ds_read_b64_tr_b16 v[152:153], v240 offset:19968\n\t"
"ds_read_b64_tr_b16 v[154:155], v240 offset:21504\n\t"
"ds_read_b64_tr_b16 v[156:157], v241 offset:19968\n\t"
"ds_read_b64_tr_b16 v[158:159], v241 offset:21504\n\t"
"ds_read_b64_tr_b16 v[160:161], v240 offset:39936\n\t"
"ds_read_b64_tr_b16 v[162:163], v240 offset:41472\n\t"
"ds_read_b64_tr_b16 v[164:165], v241 offset:39936\n\t"
"ds_read_b64_tr_b16 v[166:167], v241 offset:41472\n\t"
"s_waitcnt lgkmcnt(15)\n\t"
"v_mfma_f32_16x16x32_bf16 v[120:123], v[120:123], v[196:199], 0\n\t"
"v_mfma_f32_16x16x32_bf16 v[124:127], v[124:127], v[196:199], 0\n\t"
"v_mfma_f32_16x16x32_bf16 v[128:131], v[128:131], v[196:199], 0\n\t"
"v_mfma_f32_16x16x32_bf16 v[132:135], v[132:135], v[196:199], 0\n\t"
"s_waitcnt lgkmcnt(14)\n\t"
"v_mfma_f32_16x16x32_bf16 v[136:139], v[136:139], v[196:199], 0\n\t"
"s_waitcnt lgkmcnt(12)\n\t"
"v_mfma_f32_16x16x32_bf16 v[140:143], v[140:143], v[196:199], 0\n\t"
"s_waitcnt lgkmcnt(10)\n\t"
"v_mfma_f32_16x16x32_bf16 v[144:147], v[144:147], v[196:199], 0\n\t"
"s_waitcnt lgkmcnt(8)\n\t"
"v_mfma_f32_16x16x32_bf16 v[148:151], v[148:151], v[196:199], 0\n\t"
"v_cvt_pk_bf16_f32 v120, v120, v121\n\t"
"v_cvt_pk_bf16_f32 v121, v122, v123\n\t"
"v_cvt_pk_bf16_f32 v122, v124, v125\n\t"
"v_cvt_pk_bf16_f32 v123, v126, v127\n\t"
"s_mov_b64 exec, s[44:45]\n\t"
"ds_write_b128 v242, v[120:123] offset:0\n\t"
"s_mov_b64 exec, -1\n\t"
"v_cvt_pk_bf16_f32 v128, v128, v129\n\t"
"v_cvt_pk_bf16_f32 v129, v130, v131\n\t"
"v_cvt_pk_bf16_f32 v130, v132, v133\n\t"
"v_cvt_pk_bf16_f32 v131, v134, v135\n\t"
"s_mov_b64 exec, s[44:45]\n\t"
"ds_write_b128 v242, v[128:131] offset:1536\n\t"
"s_mov_b64 exec, -1\n\t"
"s_waitcnt lgkmcnt(8)\n\t"
"v_mfma_f32_16x16x32_bf16 v[152:155], v[152:155], v[196:199], 0\n\t"
"s_waitcnt lgkmcnt(6)\n\t"
"v_mfma_f32_16x16x32_bf16 v[156:159], v[156:159], v[196:199], 0\n\t"
"s_waitcnt lgkmcnt(4)\n\t"
"v_mfma_f32_16x16x32_bf16 v[160:163], v[160:163], v[196:199], 0\n\t"
"s_waitcnt lgkmcnt(2)\n\t"
"v_mfma_f32_16x16x32_bf16 v[164:167], v[164:167], v[196:199], 0\n\t"
"v_cvt_pk_bf16_f32 v136, v136, v137\n\t"
"v_cvt_pk_bf16_f32 v137, v138, v139\n\t"
"v_cvt_pk_bf16_f32 v138, v140, v141\n\t"
"v_cvt_pk_bf16_f32 v139, v142, v143\n\t"
"s_mov_b64 exec, s[44:45]\n\t"
"ds_write_b128 v242, v[136:139] offset:3072\n\t"
"s_mov_b64 exec, -1\n\t"
"v_cvt_pk_bf16_f32 v144, v144, v145\n\t"
"v_cvt_pk_bf16_f32 v145, v146, v147\n\t"
"v_cvt_pk_bf16_f32 v146, v148, v149\n\t"
"v_cvt_pk_bf16_f32 v147, v150, v151\n\t"
"s_mov_b64 exec, s[44:45]\n\t"
"ds_write_b128 v242, v[144:147] offset:4608\n\t"
"s_mov_b64 exec, -1\n\t"
"v_cvt_pk_bf16_f32 v152, v152, v153\n\t"
"v_cvt_pk_bf16_f32 v153, v154, v155\n\t"
"v_cvt_pk_bf16_f32 v154, v156, v157\n\t"
"v_cvt_pk_bf16_f32 v155, v158, v159\n\t"
"s_mov_b64 exec, s[44:45]\n\t"
"ds_write_b128 v242, v[152:155] offset:6144\n\t"
"s_mov_b64 exec, -1\n\t"
"v_cvt_pk_bf16_f32 v160, v160, v161\n\t"
"v_cvt_pk_bf16_f32 v161, v162, v163\n\t"
"v_cvt_pk_bf16_f32 v162, v164, v165\n\t"
"v_cvt_pk_bf16_f32 v163, v166, v167\n\t"
"s_mov_b64 exec, s[44:45]\n\t"
"ds_write_b128 v242, v[160:163] offset:7680\n\t"
"s_mov_b64 exec, -1\n\t"
"s_waitcnt lgkmcnt(0)\n\t"
"s_cmp_lg_u32 s73, 0\n\t"
"s_cbranch_scc1 Lgo_%=\n\t"
"s_cmp_lg_u32 s66, 0\n\t"
"s_cbranch_scc1 Lchk_%=\n\t"
"s_cmp_eq_u32 s68, -1\n\t"
"s_cbranch_scc1 Lgo_%=\n\t"
"s_branch Lfb_%=\n\t"
"Lchk_%=:\n\t"
"s_waitcnt vmcnt(0)\n\t"
"v_readfirstlane_b32 s60, v46\n\t"
"s_and_b32 s60, s60, 0xffff\n\t"
"s_cmp_eq_u32 s60, s67\n\t"
"s_cbranch_scc1 Lgo_%=\n\t"
"s_sub_u32 s68, s68, 1\n\t"
"s_cmp_eq_u32 s68, 0\n\t"
"s_cbranch_scc1 Lfb_%=\n\t"
"s_add_u32 s60, s66, 1835008\n\t"
"buffer_load_dword v47, off, s[8:11], s60 sc1\n\t"
"s_add_u32 s60, s66, 1867776\n\t"
"buffer_load_dword v46, off, s[8:11], s60 sc1\n\t"
"s_waitcnt vmcnt(1)\n\t"
"v_readfirstlane_b32 s60, v47\n\t"
"s_and_b32 s60, s60, 0xffff0000\n\t"
"s_or_b32 s60, s60, s67\n\t"
"s_cmp_eq_u32 s60, s71\n\t"
"s_cbranch_scc1 Lchk_%=\n\t"
"s_mov_b32 s71, s60\n\t"
"v_mov_b32 v47, s71\n\t"
"s_mov_b64 exec, 1\n\t"
"buffer_store_dword v47, off, s[8:11], s70 sc1\n\t"
"s_mov_b64 exec, -1\n\t"
"s_branch Lchk_%=\n\t"
"Lgo_%=:\n\t"
"s_barrier\n\t"
"s_mov_b32 s20, 0\n\t"
"buffer_load_dwordx4 a[208:211], v252, s[8:11], s20 offen offset:0 sc1\n\t"
"buffer_load_dwordx4 a[212:215], v252, s[8:11], s20 offen offset:1024 sc1\n\t"
"buffer_load_dwordx4 a[216:219], v252, s[8:11], s20 offen offset:2048 sc1\n\t"
"ds_read_b128 a[168:171], v244 offset:0\n\t"
"ds_read_b128 a[172:175], v244 offset:512\n\t"
"ds_read_b128 a[176:179], v244 offset:1024\n\t"
"ds_read_b128 a[180:183], v244 offset:1536\n\t"
"ds_read_b128 a[184:187], v244 offset:2048\n\t"
"ds_read_b128 a[188:191], v244 offset:2560\n\t"
"ds_read_b128 a[192:195], v244 offset:3072\n\t"
"ds_read_b128 a[196:199], v244 offset:3584\n\t"
"ds_read_b128 a[200:203], v244 offset:4096\n\t"
"ds_read_b128 a[204:207], v244 offset:4608\n\t"
"s_mov_b32 s52, 0x3fd80000\n\t"
"s_mov_b32 s53, s52\n\t"
"s_mov_b32 s54, 0x3e97b426\n\t"
"s_mov_b32 s55, s54\n\t"
"s_mov_b32 s56, 0x3f400000\n\t"
"s_mov_b32 s57, s56\n\t"
"s_mov_b32 s58, 0x3f2aaaab\n\t"
"s_mov_b32 s59, s58\n\t"
"s_mov_b32 s60, 0x3ee38e39\n\t"
"s_mov_b32 s61, s60\n\t"
"s_mov_b32 s62, 0x40100000\n\t"
"s_mov_b32 s63, s62\n\t"
"s_mov_b32 s64, 0x3faaaaab\n\t"
"s_mov_b32 s65, s64\n\t"
"s_mov_b32 s66, 0x3f17b426\n\t"
"s_mov_b32 s67, s66\n\t"
"s_mov_b32 s20, 12288\n\t"
"buffer_load_dwordx4 a[220:223], v252, s[8:11], s20 offen offset:0 sc1\n\t"
"buffer_load_dwordx4 a[224:227], v252, s[8:11], s20 offen offset:1024 sc1\n\t"
"buffer_load_dwordx4 a[228:231], v252, s[8:11], s20 offen offset:2048 sc1\n\t"
"s_mov_b32 s20, 24576\n\t"
"buffer_load_dwordx4 a[232:235], v252, s[8:11], s20 offen offset:0 sc1\n\t"
"buffer_load_dwordx4 a[236:239], v252, s[8:11], s20 offen offset:1024 sc1\n\t"
"buffer_load_dwordx4 a[240:243], v252, s[8:11], s20 offen offset:2048 sc1\n\t"
"s_waitcnt vmcnt(6) lgkmcnt(7)\n\t"
"v_mfma_f32_16x16x32_bf16 v[2:5], a[208:211], a[168:171], 0\n\t"
"v_mfma_f32_16x16x32_bf16 v[2:5], a[212:215], a[172:175], v[2:5]\n\t"
"v_mfma_f32_16x16x32_bf16 v[2:5], a[216:219], a[176:179], v[2:5]\n\t"
"s_mov_b32 s20, 184320\n\t"
"buffer_load_dwordx4 v[204:207], v252, s[8:11], s20 offen offset:0 sc1\n\t"
"s_waitcnt lgkmcnt(6)\n\t"
"v_mfma_f32_16x16x32_bf16 v[6:9], a[208:211], a[172:175], 0\n\t"
"v_mfma_f32_16x16x32_bf16 v[6:9], a[212:215], a[176:179], v[6:9]\n\t"
"v_mfma_f32_16x16x32_bf16 v[6:9], a[216:219], a[180:183], v[6:9]\n\t"
"buffer_load_dwordx4 v[212:215], v252, s[8:11], s20 offen offset:1024 sc1\n\t"
"s_waitcnt lgkmcnt(5)\n\t"
"v_mfma_f32_16x16x32_bf16 v[10:13], a[208:211], a[176:179], 0\n\t"
"v_mfma_f32_16x16x32_bf16 v[10:13], a[212:215], a[180:183], v[10:13]\n\t"
"v_mfma_f32_16x16x32_bf16 v[10:13], a[216:219], a[184:187], v[10:13]\n\t"
"buffer_load_dwordx4 a[0:3], v252, s[8:11], s20 offen offset:2048 sc1\n\t"
"s_waitcnt lgkmcnt(4)\n\t"
"v_mfma_f32_16x16x32_bf16 v[14:17], a[208:211], a[180:183], 0\n\t"
"v_mfma_f32_16x16x32_bf16 v[14:17], a[212:215], a[184:187], v[14:17]\n\t"
"v_mfma_f32_16x16x32_bf16 v[14:17], a[216:219], a[188:191], v[14:17]\n\t"
"s_mov_b32 s20, 196608\n\t"
"buffer_load_dwordx4 a[8:11], v252, s[8:11], s20 offen offset:0 sc1\n\t"
"s_waitcnt lgkmcnt(3)\n\t"
"v_mfma_f32_16x16x32_bf16 v[18:21], a[208:211], a[184:187], 0\n\t"
"v_mfma_f32_16x16x32_bf16 v[18:21], a[212:215], a[188:191], v[18:21]\n\t"
"v_mfma_f32_16x16x32_bf16 v[18:21], a[216:219], a[192:195], v[18:21]\n\t"
"buffer_load_dwordx4 a[16:19], v252, s[8:11], s20 offen offset:1024 sc1\n\t"
"s_waitcnt lgkmcnt(2)\n\t"
"v_mfma_f32_16x16x32_bf16 v[22:25], a[208:211], a[188:191], 0\n\t"
"v_mfma_f32_16x16x32_bf16 v[22:25], a[212:215], a[192:195], v[22:25]\n\t"
"v_mfma_f32_16x16x32_bf16 v[22:25], a[216:219], a[196:199], v[22:25]\n\t"
"buffer_load_dwordx4 a[24:27], v252, s[8:11], s20 offen offset:2048 sc1\n\t"
"s_waitcnt lgkmcnt(1)\n\t"
"v_mfma_f32_16x16x32_bf16 v[26:29], a[208:211], a[192:195], 0\n\t"
"v_mfma_f32_16x16x32_bf16 v[26:29], a[212:215], a[196:199], v[26:29]\n\t"
"v_mfma_f32_16x16x32_bf16 v[26:29], a[216:219], a[200:203], v[26:29]\n\t"
"s_mov_b32 s20, 208896\n\t"
"buffer_load_dwordx4 a[32:35], v252, s[8:11], s20 offen offset:0 sc1\n\t"
"s_waitcnt lgkmcnt(0)\n\t"
"v_mfma_f32_16x16x32_bf16 v[30:33], a[208:211], a[196:199], 0\n\t"
"v_mfma_f32_16x16x32_bf16 v[30:33], a[212:215], a[200:203], v[30:33]\n\t"
"v_mfma_f32_16x16x32_bf16 v[30:33], a[216:219], a[204:207], v[30:33]\n\t"
"buffer_load_dwordx4 a[40:43], v252, s[8:11], s20 offen offset:1024 sc1\n\t"
"s_waitcnt vmcnt(11)\n\t"
"v_mfma_f32_16x16x32_bf16 v[34:37], a[220:223], a[168:171], 0\n\t"
"v_mfma_f32_16x16x32_bf16 v[34:37], a[224:227], a[172:175], v[34:37]\n\t"
"v_mfma_f32_16x16x32_bf16 v[34:37], a[228:231], a[176:179], v[34:37]\n\t"
"buffer_load_dwordx4 a[48:51], v252, s[8:11], s20 offen offset:2048 sc1\n\t"
"v_mfma_f32_16x16x32_bf16 v[38:41], a[220:223], a[172:175], 0\n\t"
"ds_read_b64_tr_b16 v[224:225], v240 offset:512\n\t"
"v_mfma_f32_16x16x32_bf16 v[38:41], a[224:227], a[176:179], v[38:41]\n\t"
"ds_read_b64_tr_b16 v[226:227], v240 offset:2048\n\t"
"v_mfma_f32_16x16x32_bf16 v[38:41], a[228:231], a[180:183], v[38:41]\n\t"
"s_mov_b32 s20, 221184\n\t"
"buffer_load_dwordx4 a[56:59], v252, s[8:11], s20 offen offset:0 sc1\n\t"
"v_mfma_f32_16x16x32_bf16 v[42:45], a[220:223], a[176:179], 0\n\t"
"ds_read_b64_tr_b16 v[228:229], v241 offset:512\n\t"
"v_mfma_f32_16x16x32_bf16 v[42:45], a[224:227], a[180:183], v[42:45]\n\t"
"ds_read_b64_tr_b16 v[230:231], v241 offset:2048\n\t"
"v_mfma_f32_16x16x32_bf16 v[42:45], a[228:231], a[184:187], v[42:45]\n\t"
"v_mfma_f32_16x16x32_bf16 v[46:49], a[220:223], a[180:183], 0\n\t"
"v_mfma_f32_16x16x32_bf16 v[46:49], a[224:227], a[184:187], v[46:49]\n\t"
"v_mfma_f32_16x16x32_bf16 v[46:49], a[228:231], a[188:191], v[46:49]\n\t"
"buffer_load_dwordx4 a[64:67], v252, s[8:11], s20 offen offset:1024 sc1\n\t"
"v_mfma_f32_16x16x32_bf16 v[50:53], a[220:223], a[184:187], 0\n\t"
"v_mfma_f32_16x16x32_bf16 v[50:53], a[224:227], a[188:191], v[50:53]\n\t"
"v_mfma_f32_16x16x32_bf16 v[50:53], a[228:231], a[192:195], v[50:53]\n\t"
"buffer_load_dwordx4 a[72:75], v252, s[8:11], s20 offen offset:2048 sc1\n\t"
"v_mfma_f32_16x16x32_bf16 v[54:57], a[220:223], a[188:191], 0\n\t"
"v_mfma_f32_16x16x32_bf16 v[54:57], a[224:227], a[192:195], v[54:57]\n\t"
"ds_read_b64_tr_b16 v[232:233], v240 offset:20480\n\t"
"v_mfma_f32_16x16x32_bf16 v[54:57], a[228:231], a[196:199], v[54:57]\n\t"
"s_mov_b32 s20, 233472\n\t"
"buffer_load_dwordx4 a[80:83], v252, s[8:11], s20 offen offset:0 sc1\n\t"
"s_waitcnt lgkmcnt(3)\n\t"
"v_mfma_f32_16x16x32_bf16 v[224:227], v[224:227], v[196:199], 0\n\t"
"buffer_load_dwordx4 a[88:91], v252, s[8:11], s20 offen offset:1024 sc1\n\t"
"v_mfma_f32_16x16x32_bf16 v[58:61], a[220:223], a[192:195], 0\n\t"
"ds_read_b64_tr_b16 v[234:235], v240 offset:22016\n\t"
"v_mfma_f32_16x16x32_bf16 v[58:61], a[224:227], a[196:199], v[58:61]\n\t"
"ds_read_b64_tr_b16 v[236:237], v241 offset:20480\n\t"
"v_mfma_f32_16x16x32_bf16 v[58:61], a[228:231], a[200:203], v[58:61]\n\t"
"buffer_load_dwordx4 a[96:99], v252, s[8:11], s20 offen offset:2048 sc1\n\t"
"s_waitcnt lgkmcnt(3)\n\t"
"v_mfma_f32_16x16x32_bf16 v[228:231], v[228:231], v[196:199], 0\n\t"
"s_mov_b32 s20, 245760\n\t"
"buffer_load_dwordx4 a[104:107], v252, s[8:11], s20 offen offset:0 sc1\n\t"
"v_mfma_f32_16x16x32_bf16 v[62:65], a[220:223], a[196:199], 0\n\t"
"v_cvt_pk_bf16_f32 v224, v224, v225\n\t"
"v_mfma_f32_16x16x32_bf16 v[62:65], a[224:227], a[200:203], v[62:65]\n\t"
"v_cvt_pk_bf16_f32 v225, v226, v227\n\t"
"ds_read_b64_tr_b16 v[238:239], v241 offset:22016\n\t"
"v_mfma_f32_16x16x32_bf16 v[62:65], a[228:231], a[204:207], v[62:65]\n\t"
"s_waitcnt vmcnt(16)\n\t"
"v_mfma_f32_16x16x32_bf16 v[66:69], a[232:235], a[168:171], 0\n\t"
"ds_read_b128 a[168:171], v244 offset:9216\n\t"
"v_mfma_f32_16x16x32_bf16 v[66:69], a[236:239], a[172:175], v[66:69]\n\t"
"v_cvt_pk_bf16_f32 v226, v228, v229\n\t"
"v_cvt_pk_bf16_f32 v227, v230, v231\n\t"
"v_mfma_f32_16x16x32_bf16 v[66:69], a[240:243], a[176:179], v[66:69]\n\t"
"buffer_load_dwordx4 a[112:115], v252, s[8:11], s20 offen offset:1024 sc1\n\t"
"v_mfma_f32_16x16x32_bf16 v[70:73], a[232:235], a[172:175], 0\n\t"
"ds_read_b128 a[172:175], v244 offset:9728\n\t"
"v_mfma_f32_16x16x32_bf16 v[70:73], a[236:239], a[176:179], v[70:73]\n\t"
"v_mfma_f32_16x16x32_bf16 v[70:73], a[240:243], a[180:183], v[70:73]\n\t"
"buffer_load_dwordx4 a[120:123], v252, s[8:11], s20 offen offset:2048 sc1\n\t"
"s_waitcnt lgkmcnt(4)\n\t"
"v_mfma_f32_16x16x32_bf16 v[232:235], v[232:235], v[196:199], 0\n\t"
"s_mov_b32 s20, 405504\n\t"
"buffer_load_dwordx4 a[128:131], v252, s[8:11], s20 offen offset:0 sc1\n\t"
"v_mfma_f32_16x16x32_bf16 v[74:77], a[232:235], a[176:179], 0\n\t"
"ds_read_b128 a[176:179], v244 offset:10240\n\t"
"v_mfma_f32_16x16x32_bf16 v[74:77], a[236:239], a[180:183], v[74:77]\n\t"
"v_mfma_f32_16x16x32_bf16 v[74:77], a[240:243], a[184:187], v[74:77]\n\t"
"buffer_load_dwordx4 a[136:139], v252, s[8:11], s20 offen offset:1024 sc1\n\t"
"s_waitcnt lgkmcnt(3)\n\t"
"v_mfma_f32_16x16x32_bf16 v[236:239], v[236:239], v[196:199], 0\n\t"
"v_mfma_f32_16x16x32_bf16 v[78:81], a[232:235], a[180:183], 0\n\t"
"v_cvt_pk_bf16_f32 v232, v232, v233\n\t"
"v_cvt_pk_bf16_f32 v233, v234, v235\n\t"
"v_mfma_f32_16x16x32_bf16 v[78:81], a[236:239], a[184:187], v[78:81]\n\t"
"ds_read_b128 a[180:183], v244 offset:10752\n\t"
"v_mfma_f32_16x16x32_bf16 v[78:81], a[240:243], a[188:191], v[78:81]\n\t"
"buffer_load_dwordx4 a[144:147], v252, s[8:11], s20 offen offset:2048 sc1\n\t"
"v_mfma_f32_16x16x32_bf16 v[82:85], a[232:235], a[184:187], 0\n\t"
"ds_read_b128 a[184:187], v244 offset:11264\n\t"
"v_mfma_f32_16x16x32_bf16 v[82:85], a[236:239], a[188:191], v[82:85]\n\t"
"v_cvt_pk_bf16_f32 v234, v236, v237\n\t"
"v_cvt_pk_bf16_f32 v235, v238, v239\n\t"
"v_mfma_f32_16x16x32_bf16 v[82:85], a[240:243], a[192:195], v[82:85]\n\t"
"s_mov_b32 s20, 417792\n\t"
"buffer_load_dwordx4 a[152:155], v252, s[8:11], s20 offen offset:0 sc1\n\t"
"v_mfma_f32_16x16x32_bf16 v[86:89], a[232:235], a[188:191], 0\n\t"
"ds_read_b128 a[188:191], v244 offset:11776\n\t"
"v_mfma_f32_16x16x32_bf16 v[86:89], a[236:239], a[192:195], v[86:89]\n\t"
"v_mfma_f32_16x16x32_bf16 v[86:89], a[240:243], a[196:199], v[86:89]\n\t"
"buffer_load_dwordx4 a[160:163], v252, s[8:11], s20 offen offset:1024 sc1\n\t"
"v_mfma_f32_16x16x32_bf16 v[90:93], a[232:235], a[192:195], 0\n\t"
"ds_read_b128 a[192:195], v244 offset:12288\n\t"
"v_mfma_f32_16x16x32_bf16 v[90:93], a[236:239], a[196:199], v[90:93]\n\t"
"v_mfma_f32_16x16x32_bf16 v[90:93], a[240:243], a[200:203], v[90:93]\n\t"
"buffer_load_dwordx4 v[200:203], v252, s[8:11], s20 offen offset:2048 sc1\n\t"
"v_mfma_f32_16x16x32_bf16 v[94:97], a[232:235], a[196:199], 0\n\t"
"ds_read_b128 a[196:199], v244 offset:12800\n\t"
"v_mfma_f32_16x16x32_bf16 v[94:97], a[236:239], a[200:203], v[94:97]\n\t"
"ds_read_b128 a[200:203], v244 offset:13312\n\t"
"v_mfma_f32_16x16x32_bf16 v[94:97], a[240:243], a[204:207], v[94:97]\n\t"
"s_mov_b32 s20, 430080\n\t"
"buffer_load_dwordx4 v[208:211], v252, s[8:11], s20 offen offset:0 sc1\n\t"
"s_waitcnt vmcnt(22) lgkmcnt(6)\n\t"
"v_mfma_f32_16x16x32_bf16 v[100:103], v[204:207], a[168:171], 0\n\t"
"ds_read_b128 a[204:207], v244 offset:13824\n\t"
"v_mfma_f32_16x16x32_bf16 v[100:103], v[212:215], a[172:175], v[100:103]\n\t"
"v_mfma_f32_16x16x32_bf16 v[100:103], a[0:3], a[176:179], v[100:103]\n\t"
"buffer_load_dwordx4 v[216:219], v252, s[8:11], s20 offen offset:1024 sc1\n\t"
"s_waitcnt lgkmcnt(6)\n\t"
"v_mfma_f32_16x16x32_bf16 v[104:107], v[204:207], a[172:175], 0\n\t"
"v_mfma_f32_16x16x32_bf16 v[104:107], v[212:215], a[176:179], v[104:107]\n\t"
"v_mfma_f32_16x16x32_bf16 v[104:107], a[0:3], a[180:183], v[104:107]\n\t"
"buffer_load_dwordx4 a[4:7], v252, s[8:11], s20 offen offset:2048 sc1\n\t"
"s_waitcnt lgkmcnt(0)\n\t"
"s_barrier\n\t"
"v_mfma_f32_16x16x32_bf16 v[108:111], v[204:207], a[176:179], 0\n\t"
"ds_read_b64_tr_b16 v[228:229], v240 offset:40448\n\t"
"v_mfma_f32_16x16x32_bf16 v[108:111], v[212:215], a[180:183], v[108:111]\n\t"
"ds_read_b64_tr_b16 v[230:231], v240 offset:41984\n\t"
"v_mfma_f32_16x16x32_bf16 v[108:111], a[0:3], a[184:187], v[108:111]\n\t"
"s_mov_b64 exec, s[44:45]\n\t"
"ds_write_b128 v242, v[224:227] offset:4608\n\t"
"s_mov_b64 exec, -1\n\t"
"v_mfma_f32_16x16x32_bf16 v[112:115], v[204:207], a[180:183], 0\n\t"
"ds_read_b64_tr_b16 v[236:237], v241 offset:40448\n\t"
"v_mfma_f32_16x16x32_bf16 v[112:115], v[212:215], a[184:187], v[112:115]\n\t"
"ds_read_b64_tr_b16 v[238:239], v241 offset:41984\n\t"
"v_subrev_u32 v240, 59904, v240\n\t"
"v_mfma_f32_16x16x32_bf16 v[112:115], a[0:3], a[188:191], v[112:115]\n\t"
"s_mov_b32 s20, 110592\n\t"
"buffer_load_dwordx4 a[12:15], v252, s[8:11], s20 offen offset:0 sc1\n\t"
"s_mov_b64 exec, s[44:45]\n\t"
"ds_write_b128 v242, v[232:235] offset:6144\n\t"
"s_mov_b64 exec, -1\n\t"
"v_mfma_f32_16x16x32_bf16 v[116:119], v[204:207], a[184:187], 0\n\t"
"v_subrev_u32 v241, 59904, v241\n\t"
"ds_read_b64_tr_b16 v[224:225], v240 offset:512\n\t"
"v_mfma_f32_16x16x32_bf16 v[116:119], v[212:215], a[188:191], v[116:119]\n\t"
"ds_read_b64_tr_b16 v[226:227], v240 offset:2048\n\t"
"v_mfma_f32_16x16x32_bf16 v[116:119], a[0:3], a[192:195], v[116:119]\n\t"
"buffer_load_dwordx4 a[20:23], v252, s[8:11], s20 offen offset:1024 sc1\n\t"
"v_mfma_f32_16x16x32_bf16 v[120:123], v[204:207], a[188:191], 0\n\t"
"ds_read_b64_tr_b16 v[232:233], v241 offset:512\n\t"
"v_mfma_f32_16x16x32_bf16 v[120:123], v[212:215], a[192:195], v[120:123]\n\t"
"ds_read_b64_tr_b16 v[234:235], v241 offset:2048\n\t"
"v_mfma_f32_16x16x32_bf16 v[120:123], a[0:3], a[196:199], v[120:123]\n\t"
"buffer_load_dwordx4 a[28:31], v252, s[8:11], s20 offen offset:2048 sc1\n\t"
"s_waitcnt lgkmcnt(8)\n\t"
"v_mfma_f32_16x16x32_bf16 v[228:231], v[228:231], v[196:199], 0\n\t"
"s_mov_b32 s20, 122880\n\t"
"buffer_load_dwordx4 a[36:39], v252, s[8:11], s20 offen offset:0 sc1\n\t"
"s_waitcnt lgkmcnt(5)\n\t"
"v_mfma_f32_16x16x32_bf16 v[236:239], v[236:239], v[196:199], 0\n\t"
"buffer_load_dwordx4 a[44:47], v252, s[8:11], s20 offen offset:1024 sc1\n\t"
"v_mfma_f32_16x16x32_bf16 v[124:127], v[204:207], a[192:195], 0\n\t"
"v_mfma_f32_16x16x32_bf16 v[124:127], v[212:215], a[196:199], v[124:127]\n\t"
"v_mfma_f32_16x16x32_bf16 v[124:127], a[0:3], a[200:203], v[124:127]\n\t"
"v_mfma_f32_16x16x32_bf16 v[128:131], v[204:207], a[196:199], 0\n\t"
"v_cvt_pk_bf16_f32 v228, v228, v229\n\t"
"v_cvt_pk_bf16_f32 v229, v230, v231\n\t"
"v_mfma_f32_16x16x32_bf16 v[128:131], v[212:215], a[200:203], v[128:131]\n\t"
"v_cvt_pk_bf16_f32 v230, v236, v237\n\t"
"v_cvt_pk_bf16_f32 v231, v238, v239\n\t"
"v_mfma_f32_16x16x32_bf16 v[128:131], a[0:3], a[204:207], v[128:131]\n\t"
"buffer_load_dwordx4 a[52:55], v252, s[8:11], s20 offen offset:2048 sc1\n\t"
"s_waitcnt lgkmcnt(2)\n\t"
"v_mfma_f32_16x16x32_bf16 v[224:227], v[224:227], v[196:199], 0\n\t"
"s_mov_b64 exec, s[44:45]\n\t"
"ds_write_b128 v242, v[228:231] offset:7680\n\t"
"s_mov_b64 exec, -1\n\t"
"s_waitcnt lgkmcnt(1)\n\t"
"v_mfma_f32_16x16x32_bf16 v[232:235], v[232:235], v[196:199], 0\n\t"
"s_mov_b32 s20, 135168\n\t"
"buffer_load_dwordx4 a[76:79], v252, s[8:11], s20 offen offset:0 sc1\n\t"
"s_waitcnt vmcnt(28)\n\t"
"v_mfma_f32_16x16x32_bf16 v[132:135], a[8:11], a[168:171], 0\n\t"
"ds_read_b64_tr_b16 v[236:237], v240 offset:20480\n\t"
"v_mfma_f32_16x16x32_bf16 v[132:135], a[16:19], a[172:175], v[132:135]\n\t"
"ds_read_b64_tr_b16 v[238:239], v240 offset:22016\n\t"
"v_mfma_f32_16x16x32_bf16 v[132:135], a[24:27], a[176:179], v[132:135]\n\t"
"ds_read_b64_tr_b16 v[228:229], v241 offset:20480\n\t"
"ds_read_b64_tr_b16 v[230:231], v241 offset:22016\n\t"
"v_mfma_f32_16x16x32_bf16 v[136:139], a[8:11], a[172:175], 0\n\t"
"v_cvt_pk_bf16_f32 v224, v224, v225\n\t"
"v_cvt_pk_bf16_f32 v225, v226, v227\n\t"
"v_mfma_f32_16x16x32_bf16 v[136:139], a[16:19], a[176:179], v[136:139]\n\t"
"v_cvt_pk_bf16_f32 v226, v232, v233\n\t"
"v_cvt_pk_bf16_f32 v227, v234, v235\n\t"
"v_mfma_f32_16x16x32_bf16 v[136:139], a[24:27], a[180:183], v[136:139]\n\t"
"buffer_load_dwordx4 a[84:87], v252, s[8:11], s20 offen offset:1024 sc1\n\t"
"v_mfma_f32_16x16x32_bf16 v[140:143], a[8:11], a[176:179], 0\n\t"
"ds_read_b64_tr_b16 v[232:233], v240 offset:40448\n\t"
"v_mfma_f32_16x16x32_bf16 v[140:143], a[16:19], a[180:183], v[140:143]\n\t"
"ds_read_b64_tr_b16 v[234:235], v240 offset:41984\n\t"
"v_mfma_f32_16x16x32_bf16 v[140:143], a[24:27], a[184:187], v[140:143]\n\t"
"buffer_load_dwordx4 a[92:95], v252, s[8:11], s20 offen offset:2048 sc1\n\t"
"v_mfma_f32_16x16x32_bf16 v[144:147], a[8:11], a[180:183], 0\n\t"
"v_mfma_f32_16x16x32_bf16 v[144:147], a[16:19], a[184:187], v[144:147]\n\t"
"v_mfma_f32_16x16x32_bf16 v[144:147], a[24:27], a[188:191], v[144:147]\n\t"
"s_mov_b64 exec, s[44:45]\n\t"
"ds_write_b128 v242, v[224:227] offset:0\n\t"
"s_mov_b64 exec, -1\n\t"
"s_waitcnt lgkmcnt(5)\n\t"
"v_mfma_f32_16x16x32_bf16 v[236:239], v[236:239], v[196:199], 0\n\t"
"ds_read_b64_tr_b16 v[224:225], v241 offset:40448\n\t"
"ds_read_b64_tr_b16 v[226:227], v241 offset:41984\n\t"
"s_waitcnt lgkmcnt(5)\n\t"
"v_mfma_f32_16x16x32_bf16 v[228:231], v[228:231], v[196:199], 0\n\t"
"s_mov_b32 s20, 147456\n\t"
"buffer_load_dwordx4 a[148:151], v252, s[8:11], s20 offen offset:0 sc1\n\t"
"v_mfma_f32_16x16x32_bf16 v[148:151], a[8:11], a[184:187], 0\n\t"
"v_mfma_f32_16x16x32_bf16 v[148:151], a[16:19], a[188:191], v[148:151]\n\t"
"v_mfma_f32_16x16x32_bf16 v[148:151], a[24:27], a[192:195], v[148:151]\n\t"
"buffer_load_dwordx4 a[156:159], v252, s[8:11], s20 offen offset:1024 sc1\n\t"
"v_mfma_f32_16x16x32_bf16 v[152:155], a[8:11], a[188:191], 0\n\t"
"v_cvt_pk_bf16_f32 v236, v236, v237\n\t"
"v_cvt_pk_bf16_f32 v237, v238, v239\n\t"
"v_mfma_f32_16x16x32_bf16 v[152:155], a[16:19], a[192:195], v[152:155]\n\t"
"v_cvt_pk_bf16_f32 v238, v228, v229\n\t"
"v_mfma_f32_16x16x32_bf16 v[152:155], a[24:27], a[196:199], v[152:155]\n\t"
"v_cvt_pk_bf16_f32 v239, v230, v231\n\t"
"s_waitcnt lgkmcnt(3)\n\t"
"v_mfma_f32_16x16x32_bf16 v[232:235], v[232:235], v[196:199], 0\n\t"
"buffer_load_dwordx4 a[164:167], v252, s[8:11], s20 offen offset:2048 sc1\n\t"
"s_waitcnt lgkmcnt(0)\n\t"
"v_mfma_f32_16x16x32_bf16 v[224:227], v[224:227], v[196:199], 0\n\t"
"s_mov_b64 exec, s[44:45]\n\t"
"ds_write_b128 v242, v[236:239] offset:1536\n\t"
"s_mov_b64 exec, -1\n\t"
"v_mfma_f32_16x16x32_bf16 v[156:159], a[8:11], a[192:195], 0\n\t"
"v_mfma_f32_16x16x32_bf16 v[156:159], a[16:19], a[196:199], v[156:159]\n\t"
"v_mfma_f32_16x16x32_bf16 v[156:159], a[24:27], a[200:203], v[156:159]\n\t"
"s_mov_b32 s20, 159744\n\t"
"buffer_load_dwordx4 a[208:211], v252, s[8:11], s20 offen offset:0 sc1\n\t"
"v_mfma_f32_16x16x32_bf16 v[160:163], a[8:11], a[196:199], 0\n\t"
"v_cvt_pk_bf16_f32 v232, v232, v233\n\t"
"v_cvt_pk_bf16_f32 v233, v234, v235\n\t"
"v_mfma_f32_16x16x32_bf16 v[160:163], a[16:19], a[200:203], v[160:163]\n\t"
"v_cvt_pk_bf16_f32 v234, v224, v225\n\t"
"v_cvt_pk_bf16_f32 v235, v226, v227\n\t"
"v_mfma_f32_16x16x32_bf16 v[160:163], a[24:27], a[204:207], v[160:163]\n\t"
"buffer_load_dwordx4 v[248:251], v252, s[8:11], s20 offen offset:1024 sc1\n\t"
"s_waitcnt vmcnt(32)\n\t"
"v_mfma_f32_16x16x32_bf16 v[164:167], a[32:35], a[168:171], 0\n\t"
"v_mfma_f32_16x16x32_bf16 v[164:167], a[40:43], a[172:175], v[164:167]\n\t"
"v_mfma_f32_16x16x32_bf16 v[164:167], a[48:51], a[176:179], v[164:167]\n\t"
"s_mov_b64 exec, s[44:45]\n\t"
"ds_write_b128 v242, v[232:235] offset:3072\n\t"
"s_mov_b64 exec, -1\n\t"
"v_mfma_f32_16x16x32_bf16 v[168:171], a[32:35], a[172:175], 0\n\t"
"v_mfma_f32_16x16x32_bf16 v[168:171], a[40:43], a[176:179], v[168:171]\n\t"
"v_mfma_f32_16x16x32_bf16 v[168:171], a[48:51], a[180:183], v[168:171]\n\t"
"buffer_load_dwordx4 a[212:215], v252, s[8:11], s20 offen offset:2048 sc1\n\t"
"v_mfma_f32_16x16x32_bf16 v[172:175], a[32:35], a[176:179], 0\n\t"
"v_mfma_f32_16x16x32_bf16 v[172:175], a[40:43], a[180:183], v[172:175]\n\t"
"v_mfma_f32_16x16x32_bf16 v[172:175], a[48:51], a[184:187], v[172:175]\n\t"
"s_mov_b32 s20, 172032\n\t"
"buffer_load_dwordx4 a[216:219], v252, s[8:11], s20 offen offset:0 sc1\n\t"
"v_mfma_f32_16x16x32_bf16 v[176:179], a[32:35], a[180:183], 0\n\t"
"v_mfma_f32_16x16x32_bf16 v[176:179], a[40:43], a[184:187], v[176:179]\n\t"
"v_mfma_f32_16x16x32_bf16 v[176:179], a[48:51], a[188:191], v[176:179]\n\t"
"buffer_load_dwordx4 a[220:223], v252, s[8:11], s20 offen offset:1024 sc1\n\t"
"s_waitcnt lgkmcnt(0)\n\t"
"s_barrier\n\t"
"v_mfma_f32_16x16x32_bf16 v[180:183], a[32:35], a[184:187], 0\n\t"
"ds_read_b128 a[168:171], v244 offset:0\n\t"
"v_mfma_f32_16x16x32_bf16 v[180:183], a[40:43], a[188:191], v[180:183]\n\t"
"ds_read_b128 a[172:175], v244 offset:512\n\t"
"v_mfma_f32_16x16x32_bf16 v[180:183], a[48:51], a[192:195], v[180:183]\n\t"
"ds_read_b128 a[176:179], v244 offset:1024\n\t"
"ds_read_b128 a[180:183], v244 offset:1536\n\t"
"v_mfma_f32_16x16x32_bf16 v[184:187], a[32:35], a[188:191], 0\n\t"
"ds_read_b128 a[184:187], v244 offset:2048\n\t"
"v_mfma_f32_16x16x32_bf16 v[184:187], a[40:43], a[192:195], v[184:187]\n\t"
"ds_read_b128 a[188:191], v244 offset:2560\n\t"
"v_mfma_f32_16x16x32_bf16 v[184:187], a[48:51], a[196:199], v[184:187]\n\t"
"buffer_load_dwordx4 v[240:243], v252, s[8:11], s20 offen offset:2048 sc1\n\t"
"v_mfma_f32_16x16x32_bf16 v[188:191], a[32:35], a[192:195], 0\n\t"
"ds_read_b128 a[192:195], v244 offset:3072\n\t"
"v_mfma_f32_16x16x32_bf16 v[188:191], a[40:43], a[196:199], v[188:191]\n\t"
"v_mfma_f32_16x16x32_bf16 v[188:191], a[48:51], a[200:203], v[188:191]\n\t"
"s_mov_b32 s20, 36864\n\t"
"buffer_load_dwordx4 a[224:227], v252, s[8:11], s20 offen offset:0 sc1\n\t"
"v_mfma_f32_16x16x32_bf16 v[192:195], a[32:35], a[196:199], 0\n\t"
"ds_read_b128 a[196:199], v244 offset:3584\n\t"
"v_mfma_f32_16x16x32_bf16 v[192:195], a[40:43], a[200:203], v[192:195]\n\t"
"ds_read_b128 a[200:203], v244 offset:4096\n\t"
"v_mfma_f32_16x16x32_bf16 v[192:195], a[48:51], a[204:207], v[192:195]\n\t"
"buffer_load_dwordx4 a[228:231], v252, s[8:11], s20 offen offset:1024 sc1\n\t"
"Lst1_%=:\n\t"
"s_waitcnt vmcnt(35) lgkmcnt(6)\n\t"
"v_mfma_f32_16x16x32_bf16 v[2:5], a[56:59], a[168:171], v[2:5]\n\t"
"ds_read_b128 a[204:207], v244 offset:4608\n\t"
"v_mfma_f32_16x16x32_bf16 v[2:5], a[64:67], a[172:175], v[2:5]\n\t"
"v_mfma_f32_16x16x32_bf16 v[2:5], a[72:75], a[176:179], v[2:5]\n\t"
"buffer_load_dwordx4 a[232:235], v252, s[8:11], s20 offen offset:2048 sc1\n\t"
"s_waitcnt lgkmcnt(6)\n\t"
"v_mfma_f32_16x16x32_bf16 v[6:9], a[56:59], a[172:175], v[6:9]\n\t"
"v_mfma_f32_16x16x32_bf16 v[6:9], a[64:67], a[176:179], v[6:9]\n\t"
"v_mfma_f32_16x16x32_bf16 v[6:9], a[72:75], a[180:183], v[6:9]\n\t"
"s_mov_b32 s20, 49152\n\t"
"buffer_load_dwordx4 a[236:239], v252, s[8:11], s20 offen offset:0 sc1\n\t"
"s_waitcnt lgkmcnt(5)\n\t"
"v_mfma_f32_16x16x32_bf16 v[10:13], a[56:59], a[176:179], v[10:13]\n\t"
"v_mfma_f32_16x16x32_bf16 v[10:13], a[64:67], a[180:183], v[10:13]\n\t"
"v_mfma_f32_16x16x32_bf16 v[10:13], a[72:75], a[184:187], v[10:13]\n\t"
"buffer_load_dwordx4 a[240:243], v252, s[8:11], s20 offen offset:1024 sc1\n\t"
"s_waitcnt lgkmcnt(4)\n\t"
"v_mfma_f32_16x16x32_bf16 v[14:17], a[56:59], a[180:183], v[14:17]\n\t"
"v_mfma_f32_16x16x32_bf16 v[14:17], a[64:67], a[184:187], v[14:17]\n\t"
"v_mfma_f32_16x16x32_bf16 v[14:17], a[72:75], a[188:191], v[14:17]\n\t"
"buffer_load_dwordx4 a[244:247], v252, s[8:11], s20 offen offset:2048 sc1\n\t"
"s_waitcnt lgkmcnt(3)\n\t"
"v_mfma_f32_16x16x32_bf16 v[18:21], a[56:59], a[184:187], v[18:21]\n\t"
"v_xor_b32 v246, 0xc0, v246\n\t"
"v_mfma_f32_16x16x32_bf16 v[18:21], a[64:67], a[188:191], v[18:21]\n\t"
"v_mfma_f32_16x16x32_bf16 v[18:21], a[72:75], a[192:195], v[18:21]\n\t"
"s_waitcnt lgkmcnt(2)\n\t"
"v_mfma_f32_16x16x32_bf16 v[22:25], a[56:59], a[188:191], v[22:25]\n\t"
"v_mfma_f32_16x16x32_bf16 v[22:25], a[64:67], a[192:195], v[22:25]\n\t"
"v_mfma_f32_16x16x32_bf16 v[22:25], a[72:75], a[196:199], v[22:25]\n\t"
"s_mov_b32 s20, 61440\n\t"
"buffer_load_dwordx4 a[248:251], v252, s[8:11], s20 offen offset:0 sc1\n\t"
"s_waitcnt lgkmcnt(1)\n\t"
"v_mfma_f32_16x16x32_bf16 v[26:29], a[56:59], a[192:195], v[26:29]\n\t"
"v_mfma_f32_16x16x32_bf16 v[26:29], a[64:67], a[196:199], v[26:29]\n\t"
"v_mfma_f32_16x16x32_bf16 v[26:29], a[72:75], a[200:203], v[26:29]\n\t"
"buffer_load_dwordx4 a[252:255], v252, s[8:11], s20 offen offset:1024 sc1\n\t"
"s_waitcnt lgkmcnt(0)\n\t"
"v_mfma_f32_16x16x32_bf16 v[30:33], a[56:59], a[196:199], v[30:33]\n\t"
"v_mfma_f32_16x16x32_bf16 v[30:33], a[64:67], a[200:203], v[30:33]\n\t"
"v_mfma_f32_16x16x32_bf16 v[30:33], a[72:75], a[204:207], v[30:33]\n\t"
"s_waitcnt vmcnt(38)\n\t"
"v_mfma_f32_16x16x32_bf16 v[34:37], a[80:83], a[168:171], v[34:37]\n\t"
"v_mfma_f32_16x16x32_bf16 v[34:37], a[88:91], a[172:175], v[34:37]\n\t"
"v_mfma_f32_16x16x32_bf16 v[34:37], a[96:99], a[176:179], v[34:37]\n\t"
"ds_read_b64_tr_b16 v[228:229], v246 offset:0\n\t"
"ds_read_b64_tr_b16 v[230:231], v246 offset:3072\n\t"
"v_mfma_f32_16x16x32_bf16 v[38:41], a[80:83], a[172:175], v[38:41]\n\t"
"v_mfma_f32_16x16x32_bf16 v[38:41], a[88:91], a[176:179], v[38:41]\n\t"
"v_mfma_f32_16x16x32_bf16 v[38:41], a[96:99], a[180:183], v[38:41]\n\t"
"v_mfma_f32_16x16x32_bf16 v[42:45], a[80:83], a[176:179], v[42:45]\n\t"
"v_mfma_f32_16x16x32_bf16 v[42:45], a[88:91], a[180:183], v[42:45]\n\t"
"v_mfma_f32_16x16x32_bf16 v[42:45], a[96:99], a[184:187], v[42:45]\n\t"
"v_mfma_f32_16x16x32_bf16 v[46:49], a[80:83], a[180:183], v[46:49]\n\t"
"ds_read_b64_tr_b16 v[236:237], v246 offset:6144\n\t"
"v_mfma_f32_16x16x32_bf16 v[46:49], a[88:91], a[184:187], v[46:49]\n\t"
"ds_read_b64_tr_b16 v[238:239], v246 offset:9216\n\t"
"v_mfma_f32_16x16x32_bf16 v[46:49], a[96:99], a[188:191], v[46:49]\n\t"
"v_mfma_f32_16x16x32_bf16 v[50:53], a[80:83], a[184:187], v[50:53]\n\t"
"v_mfma_f32_16x16x32_bf16 v[50:53], a[88:91], a[188:191], v[50:53]\n\t"
"v_mfma_f32_16x16x32_bf16 v[50:53], a[96:99], a[192:195], v[50:53]\n\t"
"v_mfma_f32_16x16x32_bf16 v[54:57], a[80:83], a[188:191], v[54:57]\n\t"
"v_mfma_f32_16x16x32_bf16 v[54:57], a[88:91], a[192:195], v[54:57]\n\t"
"v_mfma_f32_16x16x32_bf16 v[54:57], a[96:99], a[196:199], v[54:57]\n\t"
"s_waitcnt lgkmcnt(2)\n\t"
"v_mfma_f32_16x16x32_bf16 v[228:231], v[228:231], v[220:223], 0\n\t"
"v_mfma_f32_16x16x32_bf16 v[58:61], a[80:83], a[192:195], v[58:61]\n\t"
"ds_read_b64_tr_b16 v[224:225], v246 offset:12288\n\t"
"v_mfma_f32_16x16x32_bf16 v[58:61], a[88:91], a[196:199], v[58:61]\n\t"
"ds_read_b64_tr_b16 v[226:227], v246 offset:15360\n\t"
"v_mfma_f32_16x16x32_bf16 v[58:61], a[96:99], a[200:203], v[58:61]\n\t"
"v_mfma_f32_16x16x32_bf16 v[62:65], a[80:83], a[196:199], v[62:65]\n\t"
"v_mfma_f32_16x16x32_bf16 v[62:65], a[88:91], a[200:203], v[62:65]\n\t"
"s_nop 0\n\t"
"v_cvt_pk_bf16_f32 v228, v228, v229\n\t"
"v_mfma_f32_16x16x32_bf16 v[62:65], a[96:99], a[204:207], v[62:65]\n\t"
"v_cvt_pk_bf16_f32 v229, v230, v231\n\t"
"s_waitcnt lgkmcnt(2)\n\t"
"v_mfma_f32_16x16x32_bf16 v[236:239], v[236:239], v[220:223], 0\n\t"
"s_waitcnt vmcnt(35)\n\t"
"v_mfma_f32_16x16x32_bf16 v[66:69], a[104:107], a[168:171], v[66:69]\n\t"
"ds_read_b64_tr_b16 v[232:233], v246 offset:19968\n\t"
"v_mfma_f32_16x16x32_bf16 v[66:69], a[112:115], a[172:175], v[66:69]\n\t"
"ds_read_b64_tr_b16 v[234:235], v246 offset:23040\n\t"
"v_mfma_f32_16x16x32_bf16 v[66:69], a[120:123], a[176:179], v[66:69]\n\t"
"v_mfma_f32_16x16x32_bf16 v[70:73], a[104:107], a[172:175], v[70:73]\n\t"
"ds_read_b128 a[168:171], v244 offset:9216\n\t"
"v_mfma_f32_16x16x32_bf16 v[70:73], a[112:115], a[176:179], v[70:73]\n\t"
"v_cvt_pk_bf16_f32 v236, v236, v237\n\t"
"v_mfma_f32_16x16x32_bf16 v[70:73], a[120:123], a[180:183], v[70:73]\n\t"
"v_cvt_pk_bf16_f32 v237, v238, v239\n\t"
"v_mfma_f32_16x16x32_bf16 v[74:77], a[104:107], a[176:179], v[74:77]\n\t"
"ds_read_b128 a[172:175], v244 offset:9728\n\t"
"v_mfma_f32_16x16x32_bf16 v[74:77], a[112:115], a[180:183], v[74:77]\n\t"
"ds_read_b128 a[176:179], v244 offset:10240\n\t"
"v_mfma_f32_16x16x32_bf16 v[74:77], a[120:123], a[184:187], v[74:77]\n\t"
"s_waitcnt lgkmcnt(5)\n\t"
"v_mfma_f32_16x16x32_bf16 v[224:227], v[224:227], v[220:223], 0\n\t"
"v_mfma_f32_16x16x32_bf16 v[78:81], a[104:107], a[180:183], v[78:81]\n\t"
"ds_read_b128 a[180:183], v244 offset:10752\n\t"
"v_mfma_f32_16x16x32_bf16 v[78:81], a[112:115], a[184:187], v[78:81]\n\t"
"v_mfma_f32_16x16x32_bf16 v[78:81], a[120:123], a[188:191], v[78:81]\n\t"
"v_mfma_f32_16x16x32_bf16 v[82:85], a[104:107], a[184:187], v[82:85]\n\t"
"ds_read_b128 a[184:187], v244 offset:11264\n\t"
"v_mfma_f32_16x16x32_bf16 v[82:85], a[112:115], a[188:191], v[82:85]\n\t"
"s_nop 0\n\t"
"v_cvt_pk_bf16_f32 v224, v224, v225\n\t"
"v_mfma_f32_16x16x32_bf16 v[82:85], a[120:123], a[192:195], v[82:85]\n\t"
"v_cvt_pk_bf16_f32 v225, v226, v227\n\t"
"s_waitcnt lgkmcnt(5)\n\t"
"v_mfma_f32_16x16x32_bf16 v[232:235], v[232:235], v[220:223], 0\n\t"
"v_mfma_f32_16x16x32_bf16 v[86:89], a[104:107], a[188:191], v[86:89]\n\t"
"ds_read_b128 a[188:191], v244 offset:11776\n\t"
"v_mfma_f32_16x16x32_bf16 v[86:89], a[112:115], a[192:195], v[86:89]\n\t"
"v_mfma_f32_16x16x32_bf16 v[86:89], a[120:123], a[196:199], v[86:89]\n\t"
"v_mfma_f32_16x16x32_bf16 v[90:93], a[104:107], a[192:195], v[90:93]\n\t"
"ds_read_b128 a[192:195], v244 offset:12288\n\t"
"v_mfma_f32_16x16x32_bf16 v[90:93], a[112:115], a[196:199], v[90:93]\n\t"
"s_nop 0\n\t"
"v_cvt_pk_bf16_f32 v232, v232, v233\n\t"
"v_mfma_f32_16x16x32_bf16 v[90:93], a[120:123], a[200:203], v[90:93]\n\t"
"v_cvt_pk_bf16_f32 v233, v234, v235\n\t"
"v_mfma_f32_16x16x32_bf16 v[94:97], a[104:107], a[196:199], v[94:97]\n\t"
"ds_read_b128 a[196:199], v244 offset:12800\n\t"
"v_mfma_f32_16x16x32_bf16 v[94:97], a[112:115], a[200:203], v[94:97]\n\t"
"ds_read_b128 a[200:203], v244 offset:13312\n\t"
"v_mfma_f32_16x16x32_bf16 v[94:97], a[120:123], a[204:207], v[94:97]\n\t"
"ds_read_b128 a[204:207], v244 offset:13824\n\t"
"s_waitcnt vmcnt(32) lgkmcnt(7)\n\t"
"v_mfma_f32_16x16x32_bf16 v[100:103], a[128:131], a[168:171], v[100:103]\n\t"
"v_mfma_f32_16x16x32_bf16 v[100:103], a[136:139], a[172:175], v[100:103]\n\t"
"v_mfma_f32_16x16x32_bf16 v[100:103], a[144:147], a[176:179], v[100:103]\n\t"
"s_waitcnt lgkmcnt(6)\n\t"
"v_mfma_f32_16x16x32_bf16 v[104:107], a[128:131], a[172:175], v[104:107]\n\t"
"v_mfma_f32_16x16x32_bf16 v[104:107], a[136:139], a[176:179], v[104:107]\n\t"
"v_mfma_f32_16x16x32_bf16 v[104:107], a[144:147], a[180:183], v[104:107]\n\t"
"s_waitcnt lgkmcnt(0)\n\t"
"s_barrier\n\t"
"v_mfma_f32_16x16x32_bf16 v[108:111], a[128:131], a[176:179], v[108:111]\n\t"
"s_nop 0\n\t"
"v_fma_f32 v196, -v2, s56, -v100\n\t"
"v_fma_f32 v197, -v3, s56, -v101\n\t"
"v_mfma_f32_16x16x32_bf16 v[108:111], a[136:139], a[180:183], v[108:111]\n\t"
"v_fma_f32 v198, -v4, s56, -v102\n\t"
"v_fma_f32 v199, -v5, s56, -v103\n\t"
"v_mfma_f32_16x16x32_bf16 v[108:111], a[144:147], a[184:187], v[108:111]\n\t"
"ds_write_b64 v245, v[228:229] offset:0\n\t"
"v_mfma_f32_16x16x32_bf16 v[112:115], a[128:131], a[180:183], v[112:115]\n\t"
"ds_read_b64_tr_b16 v[228:229], v246 offset:26112\n\t"
"v_fma_f32 v204, -v6, s56, -v104\n\t"
"v_mfma_f32_16x16x32_bf16 v[112:115], a[136:139], a[184:187], v[112:115]\n\t"
"ds_read_b64_tr_b16 v[230:231], v246 offset:29184\n\t"
"v_fma_f32 v205, -v7, s56, -v105\n\t"
"v_mfma_f32_16x16x32_bf16 v[112:115], a[144:147], a[188:191], v[112:115]\n\t"
"ds_write_b64 v245, v[236:237] offset:512\n\t"
"v_mfma_f32_16x16x32_bf16 v[116:119], a[128:131], a[184:187], v[116:119]\n\t"
"ds_read_b64_tr_b16 v[236:237], v246 offset:32256\n\t"
"v_fma_f32 v206, -v8, s56, -v106\n\t"
"v_mfma_f32_16x16x32_bf16 v[116:119], a[136:139], a[188:191], v[116:119]\n\t"
"ds_read_b64_tr_b16 v[238:239], v246 offset:35328\n\t"
"v_mfma_f32_16x16x32_bf16 v[116:119], a[144:147], a[192:195], v[116:119]\n\t"
"ds_write_b64 v245, v[224:225] offset:1024\n\t"
"ds_write_b64 v245, v[232:233] offset:1536\n\t"
"v_mfma_f32_16x16x32_bf16 v[120:123], a[128:131], a[188:191], v[120:123]\n\t"
"v_mfma_f32_16x16x32_bf16 v[120:123], a[136:139], a[192:195], v[120:123]\n\t"
"v_mfma_f32_16x16x32_bf16 v[120:123], a[144:147], a[196:199], v[120:123]\n\t"
"s_waitcnt lgkmcnt(5)\n\t"
"v_mfma_f32_16x16x32_bf16 v[228:231], v[228:231], v[220:223], 0\n\t"
"ds_read_b64_tr_b16 v[224:225], v246 offset:39936\n\t"
"ds_read_b64_tr_b16 v[226:227], v246 offset:43008\n\t"
"v_mfma_f32_16x16x32_bf16 v[124:127], a[128:131], a[192:195], v[124:127]\n\t"
"v_mfma_f32_16x16x32_bf16 v[124:127], a[136:139], a[196:199], v[124:127]\n\t"
"ds_read_b64_tr_b16 v[232:233], v246 offset:46080\n\t"
"v_mfma_f32_16x16x32_bf16 v[124:127], a[144:147], a[200:203], v[124:127]\n\t"
"ds_read_b64_tr_b16 v[234:235], v246 offset:49152\n\t"
"s_waitcnt lgkmcnt(6)\n\t"
"v_mfma_f32_16x16x32_bf16 v[236:239], v[236:239], v[220:223], 0\n\t"
"v_fma_f32 v207, -v9, s56, -v107\n\t"
"v_mfma_f32_16x16x32_bf16 v[128:131], a[128:131], a[196:199], v[128:131]\n\t"
"v_cvt_pk_bf16_f32 v228, v228, v229\n\t"
"v_cvt_pk_bf16_f32 v229, v230, v231\n\t"
"v_mfma_f32_16x16x32_bf16 v[128:131], a[136:139], a[200:203], v[128:131]\n\t"
"v_fma_f32 v212, -v10, s56, -v108\n\t"
"v_fma_f32 v213, -v11, s56, -v109\n\t"
"v_mfma_f32_16x16x32_bf16 v[128:131], a[144:147], a[204:207], v[128:131]\n\t"
"ds_write_b64 v245, v[228:229] offset:2048\n\t"
"s_waitcnt lgkmcnt(3)\n\t"
"v_mfma_f32_16x16x32_bf16 v[224:227], v[224:227], v[220:223], 0\n\t"
"ds_read_b64_tr_b16 v[228:229], v246 offset:52224\n\t"
"ds_read_b64_tr_b16 v[230:231], v246 offset:55296\n\t"
"s_waitcnt vmcnt(29)\n\t"
"v_mfma_f32_16x16x32_bf16 v[132:135], a[152:155], a[168:171], v[132:135]\n\t"
"v_cvt_pk_bf16_f32 v236, v236, v237\n\t"
"v_cvt_pk_bf16_f32 v237, v238, v239\n\t"
"v_mfma_f32_16x16x32_bf16 v[132:135], a[160:163], a[172:175], v[132:135]\n\t"
"v_fma_f32 v214, -v12, s56, -v110\n\t"
"v_fma_f32 v215, -v13, s56, -v111\n\t"
"v_mfma_f32_16x16x32_bf16 v[132:135], v[200:203], a[176:179], v[132:135]\n\t"
"ds_write_b64 v245, v[236:237] offset:2560\n\t"
"s_waitcnt lgkmcnt(4)\n\t"
"v_mfma_f32_16x16x32_bf16 v[232:235], v[232:235], v[220:223], 0\n\t"
"v_fma_f32 v98, -v14, s56, -v112\n\t"
"v_fma_f32 v99, -v15, s56, -v113\n\t"
"v_mfma_f32_16x16x32_bf16 v[136:139], a[152:155], a[172:175], v[136:139]\n\t"
"v_cvt_pk_bf16_f32 v224, v224, v225\n\t"
"v_cvt_pk_bf16_f32 v225, v226, v227\n\t"
"v_mfma_f32_16x16x32_bf16 v[136:139], a[160:163], a[176:179], v[136:139]\n\t"
"v_accvgpr_write_b32 a0, v98\n\t"
"v_fma_f32 v98, -v16, s56, -v114\n\t"
"v_mfma_f32_16x16x32_bf16 v[136:139], v[200:203], a[180:183], v[136:139]\n\t"
"ds_write_b64 v245, v[224:225] offset:3072\n\t"
"v_mfma_f32_16x16x32_bf16 v[140:143], a[152:155], a[176:179], v[140:143]\n\t"
"v_accvgpr_write_b32 a1, v99\n\t"
"v_fma_f32 v99, -v17, s56, -v115\n\t"
"v_mfma_f32_16x16x32_bf16 v[140:143], a[160:163], a[180:183], v[140:143]\n\t"
"v_cvt_pk_bf16_f32 v232, v232, v233\n\t"
"v_cvt_pk_bf16_f32 v233, v234, v235\n\t"
"v_mfma_f32_16x16x32_bf16 v[140:143], v[200:203], a[184:187], v[140:143]\n\t"
"buffer_load_dwordx4 v[236:239], v252, s[8:11], s20 offen offset:2048 sc1\n\t"
"v_mfma_f32_16x16x32_bf16 v[144:147], a[152:155], a[180:183], v[144:147]\n\t"
"v_accvgpr_write_b32 a2, v98\n\t"
"v_accvgpr_write_b32 a3, v99\n\t"
"v_mfma_f32_16x16x32_bf16 v[144:147], a[160:163], a[184:187], v[144:147]\n\t"
"v_fma_f32 v98, -v18, s56, -v116\n\t"
"v_fma_f32 v99, -v19, s56, -v117\n\t"
"v_mfma_f32_16x16x32_bf16 v[144:147], v[200:203], a[188:191], v[144:147]\n\t"
"ds_write_b64 v245, v[232:233] offset:3584\n\t"
"v_accvgpr_write_b32 a8, v98\n\t"
"v_fma_f32 v98, -v20, s56, -v118\n\t"
"s_waitcnt lgkmcnt(3)\n\t"
"v_mfma_f32_16x16x32_bf16 v[228:231], v[228:231], v[220:223], 0\n\t"
"v_accvgpr_write_b32 a9, v99\n\t"
"v_fma_f32 v99, -v21, s56, -v119\n\t"
"v_mfma_f32_16x16x32_bf16 v[148:151], a[152:155], a[184:187], v[148:151]\n\t"
"v_accvgpr_write_b32 a10, v98\n\t"
"v_accvgpr_write_b32 a11, v99\n\t"
"v_mfma_f32_16x16x32_bf16 v[148:151], a[160:163], a[188:191], v[148:151]\n\t"
"v_fma_f32 v98, -v22, s56, -v120\n\t"
"v_fma_f32 v99, -v23, s56, -v121\n\t"
"v_mfma_f32_16x16x32_bf16 v[148:151], v[200:203], a[192:195], v[148:151]\n\t"
"v_accvgpr_write_b32 a16, v98\n\t"
"v_fma_f32 v98, -v24, s56, -v122\n\t"
"v_mfma_f32_16x16x32_bf16 v[152:155], a[152:155], a[188:191], v[152:155]\n\t"
"v_accvgpr_write_b32 a17, v99\n\t"
"v_fma_f32 v99, -v25, s56, -v123\n\t"
"v_mfma_f32_16x16x32_bf16 v[152:155], a[160:163], a[192:195], v[152:155]\n\t"
"v_cvt_pk_bf16_f32 v228, v228, v229\n\t"
"v_cvt_pk_bf16_f32 v229, v230, v231\n\t"
"v_mfma_f32_16x16x32_bf16 v[152:155], v[200:203], a[196:199], v[152:155]\n\t"
"ds_write_b64 v245, v[228:229] offset:4096\n\t"
"v_accvgpr_write_b32 a18, v98\n\t"
"v_accvgpr_write_b32 a19, v99\n\t"
"v_mfma_f32_16x16x32_bf16 v[156:159], a[152:155], a[192:195], v[156:159]\n\t"
"v_fma_f32 v98, -v26, s56, -v124\n\t"
"v_fma_f32 v99, -v27, s56, -v125\n\t"
"v_mfma_f32_16x16x32_bf16 v[156:159], a[160:163], a[196:199], v[156:159]\n\t"
"v_accvgpr_write_b32 a24, v98\n\t"
"v_fma_f32 v98, -v28, s56, -v126\n\t"
"v_mfma_f32_16x16x32_bf16 v[156:159], v[200:203], a[200:203], v[156:159]\n\t"
"v_accvgpr_write_b32 a25, v99\n\t"
"v_fma_f32 v99, -v29, s56, -v127\n\t"
"v_accvgpr_write_b32 a26, v98\n\t"
"v_accvgpr_write_b32 a27, v99\n\t"
"v_mfma_f32_16x16x32_bf16 v[160:163], a[152:155], a[196:199], v[160:163]\n\t"
"v_fma_f32 v98, -v30, s56, -v128\n\t"
"v_fma_f32 v99, -v31, s56, -v129\n\t"
"v_mfma_f32_16x16x32_bf16 v[160:163], a[160:163], a[200:203], v[160:163]\n\t"
"v_accvgpr_write_b32 a32, v98\n\t"
"v_fma_f32 v98, -v32, s56, -v130\n\t"
"v_mfma_f32_16x16x32_bf16 v[160:163], v[200:203], a[204:207], v[160:163]\n\t"
"v_accvgpr_write_b32 a33, v99\n\t"
"v_fma_f32 v99, -v33, s56, -v131\n\t"
"v_accvgpr_write_b32 a34, v98\n\t"
"v_accvgpr_write_b32 a35, v99\n\t"
"s_waitcnt vmcnt(27)\n\t"
"v_mfma_f32_16x16x32_bf16 v[164:167], v[208:211], a[168:171], v[164:167]\n\t"
"v_fma_f32 v98, -v34, s56, -v132\n\t"
"v_fma_f32 v99, -v35, s56, -v133\n\t"
"v_mfma_f32_16x16x32_bf16 v[164:167], v[216:219], a[172:175], v[164:167]\n\t"
"v_accvgpr_write_b32 a40, v98\n\t"
"v_fma_f32 v98, -v36, s56, -v134\n\t"
"v_mfma_f32_16x16x32_bf16 v[164:167], a[4:7], a[176:179], v[164:167]\n\t"
"v_accvgpr_write_b32 a41, v99\n\t"
"v_fma_f32 v99, -v37, s56, -v135\n\t"
"v_accvgpr_write_b32 a42, v98\n\t"
"v_accvgpr_write_b32 a43, v99\n\t"
"s_waitcnt lgkmcnt(0)\n\t"
"s_barrier\n\t"
"v_mfma_f32_16x16x32_bf16 v[168:171], v[208:211], a[172:175], v[168:171]\n\t"
"ds_read_b128 a[168:171], v244 offset:0\n\t"
"v_fma_f32 v98, -v38, s56, -v136\n\t"
"v_mfma_f32_16x16x32_bf16 v[168:171], v[216:219], a[176:179], v[168:171]\n\t"
"ds_read_b128 a[172:175], v244 offset:512\n\t"
"v_fma_f32 v99, -v39, s56, -v137\n\t"
"v_mfma_f32_16x16x32_bf16 v[168:171], a[4:7], a[180:183], v[168:171]\n\t"
"v_accvgpr_write_b32 a48, v98\n\t"
"v_fma_f32 v98, -v40, s56, -v138\n\t"
"v_accvgpr_write_b32 a49, v99\n\t"
"v_fma_f32 v99, -v41, s56, -v139\n\t"
"v_mfma_f32_16x16x32_bf16 v[172:175], v[208:211], a[176:179], v[172:175]\n\t"
"ds_read_b128 a[176:179], v244 offset:1024\n\t"
"v_accvgpr_write_b32 a50, v98\n\t"
"v_mfma_f32_16x16x32_bf16 v[172:175], v[216:219], a[180:183], v[172:175]\n\t"
"v_accvgpr_write_b32 a51, v99\n\t"
"v_fma_f32 v98, -v42, s56, -v140\n\t"
"v_mfma_f32_16x16x32_bf16 v[172:175], a[4:7], a[184:187], v[172:175]\n\t"
"v_fma_f32 v99, -v43, s56, -v141\n\t"
"v_accvgpr_write_b32 a56, v98\n\t"
"v_fma_f32 v98, -v44, s56, -v142\n\t"
"v_accvgpr_write_b32 a57, v99\n\t"
"v_mfma_f32_16x16x32_bf16 v[176:179], v[208:211], a[180:183], v[176:179]\n\t"
"ds_read_b128 a[180:183], v244 offset:1536\n\t"
"v_fma_f32 v99, -v45, s56, -v143\n\t"
"v_mfma_f32_16x16x32_bf16 v[176:179], v[216:219], a[184:187], v[176:179]\n\t"
"v_accvgpr_write_b32 a58, v98\n\t"
"v_accvgpr_write_b32 a59, v99\n\t"
"v_mfma_f32_16x16x32_bf16 v[176:179], a[4:7], a[188:191], v[176:179]\n\t"
"v_fma_f32 v98, -v46, s56, -v144\n\t"
"v_fma_f32 v99, -v47, s56, -v145\n\t"
"v_accvgpr_write_b32 a64, v98\n\t"
"v_fma_f32 v98, -v48, s56, -v146\n\t"
"v_mfma_f32_16x16x32_bf16 v[180:183], v[208:211], a[184:187], v[180:183]\n\t"
"ds_read_b128 a[184:187], v244 offset:2048\n\t"
"v_accvgpr_write_b32 a65, v99\n\t"
"v_mfma_f32_16x16x32_bf16 v[180:183], v[216:219], a[188:191], v[180:183]\n\t"
"v_fma_f32 v99, -v49, s56, -v147\n\t"
"v_accvgpr_write_b32 a66, v98\n\t"
"v_mfma_f32_16x16x32_bf16 v[180:183], a[4:7], a[192:195], v[180:183]\n\t"
"v_accvgpr_write_b32 a67, v99\n\t"
"v_fma_f32 v98, -v50, s56, -v148\n\t"
"v_fma_f32 v99, -v51, s56, -v149\n\t"
"v_accvgpr_write_b32 a72, v98\n\t"
"v_mfma_f32_16x16x32_bf16 v[184:187], v[208:211], a[188:191], v[184:187]\n\t"
"ds_read_b128 a[188:191], v244 offset:2560\n\t"
"v_fma_f32 v98, -v52, s56, -v150\n\t"
"v_mfma_f32_16x16x32_bf16 v[184:187], v[216:219], a[192:195], v[184:187]\n\t"
"v_accvgpr_write_b32 a73, v99\n\t"
"v_fma_f32 v99, -v53, s56, -v151\n\t"
"v_mfma_f32_16x16x32_bf16 v[184:187], a[4:7], a[196:199], v[184:187]\n\t"
"v_accvgpr_write_b32 a74, v98\n\t"
"v_accvgpr_write_b32 a75, v99\n\t"
"v_fma_f32 v98, -v54, s56, -v152\n\t"
"v_fma_f32 v99, -v55, s56, -v153\n\t"
"v_mfma_f32_16x16x32_bf16 v[188:191], v[208:211], a[192:195], v[188:191]\n\t"
"ds_read_b128 a[192:195], v244 offset:3072\n\t"
"v_accvgpr_write_b32 a80, v98\n\t"
"v_mfma_f32_16x16x32_bf16 v[188:191], v[216:219], a[196:199], v[188:191]\n\t"
"v_fma_f32 v98, -v56, s56, -v154\n\t"
"v_accvgpr_write_b32 a81, v99\n\t"
"v_mfma_f32_16x16x32_bf16 v[188:191], a[4:7], a[200:203], v[188:191]\n\t"
"v_fma_f32 v99, -v57, s56, -v155\n\t"
"v_accvgpr_write_b32 a82, v98\n\t"
"v_accvgpr_write_b32 a83, v99\n\t"
"v_fma_f32 v98, -v58, s56, -v156\n\t"
"v_mfma_f32_16x16x32_bf16 v[192:195], v[208:211], a[196:199], v[192:195]\n\t"
"ds_read_b128 a[196:199], v244 offset:3584\n\t"
"v_fma_f32 v99, -v59, s56, -v157\n\t"
"v_mfma_f32_16x16x32_bf16 v[192:195], v[216:219], a[200:203], v[192:195]\n\t"
"ds_read_b128 a[200:203], v244 offset:4096\n\t"
"v_accvgpr_write_b32 a88, v98\n\t"
"v_mfma_f32_16x16x32_bf16 v[192:195], a[4:7], a[204:207], v[192:195]\n\t"
"ds_read_b128 a[204:207], v244 offset:4608\n\t"
"v_fma_f32 v98, -v60, s56, -v158\n\t"
"v_accvgpr_write_b32 a89, v99\n\t"
"v_fma_f32 v99, -v61, s56, -v159\n\t"
"Lst2_%=:\n\t"
"s_waitcnt vmcnt(24) lgkmcnt(7)\n\t"
"v_mfma_f32_16x16x32_bf16 v[196:199], a[12:15], a[168:171], v[196:199]\n\t"
"v_xor_b32 v246, 0xc0, v246\n\t"
"v_accvgpr_write_b32 a90, v98\n\t"
"v_mfma_f32_16x16x32_bf16 v[196:199], a[20:23], a[172:175], v[196:199]\n\t"
"v_accvgpr_write_b32 a91, v99\n\t"
"v_fma_f32 v98, -v62, s56, -v160\n\t"
"v_mfma_f32_16x16x32_bf16 v[196:199], a[28:31], a[176:179], v[196:199]\n\t"
"v_fma_f32 v99, -v63, s56, -v161\n\t"
"v_accvgpr_write_b32 a96, v98\n\t"
"v_fma_f32 v98, -v64, s56, -v162\n\t"
"v_accvgpr_write_b32 a97, v99\n\t"
"s_waitcnt lgkmcnt(6)\n\t"
"v_mfma_f32_16x16x32_bf16 v[204:207], a[12:15], a[172:175], v[204:207]\n\t"
"v_fma_f32 v99, -v65, s56, -v163\n\t"
"v_accvgpr_write_b32 a98, v98\n\t"
"v_mfma_f32_16x16x32_bf16 v[204:207], a[20:23], a[176:179], v[204:207]\n\t"
"v_accvgpr_write_b32 a99, v99\n\t"
"v_fma_f32 v98, -v66, s56, -v164\n\t"
"v_mfma_f32_16x16x32_bf16 v[204:207], a[28:31], a[180:183], v[204:207]\n\t"
"v_fma_f32 v99, -v67, s56, -v165\n\t"
"v_accvgpr_write_b32 a104, v98\n\t"
"v_fma_f32 v98, -v68, s56, -v166\n\t"
"v_accvgpr_write_b32 a105, v99\n\t"
"s_waitcnt lgkmcnt(5)\n\t"
"v_mfma_f32_16x16x32_bf16 v[212:215], a[12:15], a[176:179], v[212:215]\n\t"
"v_fma_f32 v99, -v69, s56, -v167\n\t"
"v_accvgpr_write_b32 a106, v98\n\t"
"v_mfma_f32_16x16x32_bf16 v[212:215], a[20:23], a[180:183], v[212:215]\n\t"
"v_accvgpr_write_b32 a107, v99\n\t"
"v_fma_f32 v98, -v70, s56, -v168\n\t"
"v_mfma_f32_16x16x32_bf16 v[212:215], a[28:31], a[184:187], v[212:215]\n\t"
"v_fma_f32 v99, -v71, s56, -v169\n\t"
"v_accvgpr_write_b32 a112, v98\n\t"
"v_fma_f32 v98, -v72, s56, -v170\n\t"
"v_accvgpr_write_b32 a113, v99\n\t"
"s_waitcnt lgkmcnt(4)\n\t"
"v_mfma_f32_16x16x32_bf16 a[0:3], a[12:15], a[180:183], a[0:3]\n\t"
"v_fma_f32 v99, -v73, s56, -v171\n\t"
"v_accvgpr_write_b32 a114, v98\n\t"
"v_mfma_f32_16x16x32_bf16 a[0:3], a[20:23], a[184:187], a[0:3]\n\t"
"v_accvgpr_write_b32 a115, v99\n\t"
"v_fma_f32 v98, -v74, s56, -v172\n\t"
"v_mfma_f32_16x16x32_bf16 a[0:3], a[28:31], a[188:191], a[0:3]\n\t"
"v_fma_f32 v99, -v75, s56, -v173\n\t"
"v_accvgpr_write_b32 a120, v98\n\t"
"v_fma_f32 v98, -v76, s56, -v174\n\t"
"v_accvgpr_write_b32 a121, v99\n\t"
"s_waitcnt lgkmcnt(3)\n\t"
"v_mfma_f32_16x16x32_bf16 a[8:11], a[12:15], a[184:187], a[8:11]\n\t"
"v_fma_f32 v99, -v77, s56, -v175\n\t"
"v_accvgpr_write_b32 a122, v98\n\t"
"v_mfma_f32_16x16x32_bf16 a[8:11], a[20:23], a[188:191], a[8:11]\n\t"
"v_accvgpr_write_b32 a123, v99\n\t"
"v_fma_f32 v98, -v78, s56, -v176\n\t"
"v_mfma_f32_16x16x32_bf16 a[8:11], a[28:31], a[192:195], a[8:11]\n\t"
"ds_read_b64_tr_b16 v[224:225], v246 offset:0\n\t"
"ds_read_b64_tr_b16 v[226:227], v246 offset:3072\n\t"
"v_fma_f32 v99, -v79, s56, -v177\n\t"
"v_accvgpr_write_b32 a128, v98\n\t"
"s_waitcnt lgkmcnt(4)\n\t"
"v_mfma_f32_16x16x32_bf16 a[16:19], a[12:15], a[188:191], a[16:19]\n\t"
"v_fma_f32 v98, -v80, s56, -v178\n\t"
"v_accvgpr_write_b32 a129, v99\n\t"
"v_mfma_f32_16x16x32_bf16 a[16:19], a[20:23], a[192:195], a[16:19]\n\t"
"v_fma_f32 v99, -v81, s56, -v179\n\t"
"v_accvgpr_write_b32 a130, v98\n\t"
"v_mfma_f32_16x16x32_bf16 a[16:19], a[28:31], a[196:199], a[16:19]\n\t"
"v_accvgpr_write_b32 a131, v99\n\t"
"v_fma_f32 v98, -v82, s56, -v180\n\t"
"v_fma_f32 v99, -v83, s56, -v181\n\t"
"v_accvgpr_write_b32 a136, v98\n\t"
"s_waitcnt lgkmcnt(3)\n\t"
"v_mfma_f32_16x16x32_bf16 a[24:27], a[12:15], a[192:195], a[24:27]\n\t"
"v_fma_f32 v98, -v84, s56, -v182\n\t"
"v_accvgpr_write_b32 a137, v99\n\t"
"v_mfma_f32_16x16x32_bf16 a[24:27], a[20:23], a[196:199], a[24:27]\n\t"
"v_fma_f32 v99, -v85, s56, -v183\n\t"
"v_accvgpr_write_b32 a138, v98\n\t"
"v_mfma_f32_16x16x32_bf16 a[24:27], a[28:31], a[200:203], a[24:27]\n\t"
"v_accvgpr_write_b32 a139, v99\n\t"
"v_fma_f32 v98, -v86, s56, -v184\n\t"
"v_fma_f32 v99, -v87, s56, -v185\n\t"
"v_accvgpr_write_b32 a144, v98\n\t"
"s_waitcnt lgkmcnt(2)\n\t"
"v_mfma_f32_16x16x32_bf16 a[32:35], a[12:15], a[196:199], a[32:35]\n\t"
"ds_read_b64_tr_b16 v[232:233], v246 offset:6144\n\t"
"v_fma_f32 v98, -v88, s56, -v186\n\t"
"v_mfma_f32_16x16x32_bf16 a[32:35], a[20:23], a[200:203], a[32:35]\n\t"
"ds_read_b64_tr_b16 v[234:235], v246 offset:9216\n\t"
"v_accvgpr_write_b32 a145, v99\n\t"
"v_mfma_f32_16x16x32_bf16 a[32:35], a[28:31], a[204:207], a[32:35]\n\t"
"v_fma_f32 v99, -v89, s56, -v187\n\t"
"v_accvgpr_write_b32 a146, v98\n\t"
"v_accvgpr_write_b32 a147, v99\n\t"
"v_fma_f32 v98, -v90, s56, -v188\n\t"
"s_waitcnt vmcnt(21)\n\t"
"v_mfma_f32_16x16x32_bf16 a[40:43], a[36:39], a[168:171], a[40:43]\n\t"
"v_fma_f32 v99, -v91, s56, -v189\n\t"
"v_accvgpr_write_b32 a152, v98\n\t"
"v_mfma_f32_16x16x32_bf16 a[40:43], a[44:47], a[172:175], a[40:43]\n\t"
"v_fma_f32 v98, -v92, s56, -v190\n\t"
"v_accvgpr_write_b32 a153, v99\n\t"
"v_mfma_f32_16x16x32_bf16 a[40:43], a[52:55], a[176:179], a[40:43]\n\t"
"v_fma_f32 v99, -v93, s56, -v191\n\t"
"v_accvgpr_write_b32 a154, v98\n\t"
"v_accvgpr_write_b32 a155, v99\n\t"
"v_fma_f32 v98, -v94, s56, -v192\n\t"
"v_mfma_f32_16x16x32_bf16 a[48:51], a[36:39], a[172:175], a[48:51]\n\t"
"v_fma_f32 v99, -v95, s56, -v193\n\t"
"v_accvgpr_write_b32 a160, v98\n\t"
"v_mfma_f32_16x16x32_bf16 a[48:51], a[44:47], a[176:179], a[48:51]\n\t"
"v_fma_f32 v98, -v96, s56, -v194\n\t"
"v_accvgpr_write_b32 a161, v99\n\t"
"v_mfma_f32_16x16x32_bf16 a[48:51], a[52:55], a[180:183], a[48:51]\n\t"
"v_fma_f32 v99, -v97, s56, -v195\n\t"
"v_accvgpr_write_b32 a162, v98\n\t"
"v_accvgpr_write_b32 a163, v99\n\t"
"v_fma_f32 v200, v100, s64, -v2\n\t"
"s_waitcnt lgkmcnt(2)\n\t"
"v_mfma_f32_16x16x32_bf16 v[224:227], v[224:227], v[220:223], 0\n\t"
"v_fma_f32 v201, v101, s64, -v3\n\t"
"v_fma_f32 v202, v102, s64, -v4\n\t"
"v_fma_f32 v203, v103, s64, -v5\n\t"
"v_fma_f32 v208, v104, s64, -v6\n\t"
"v_mfma_f32_16x16x32_bf16 a[56:59], a[36:39], a[176:179], a[56:59]\n\t"
"ds_read_b64_tr_b16 v[228:229], v246 offset:12288\n\t"
"v_fma_f32 v209, v105, s64, -v7\n\t"
"v_mfma_f32_16x16x32_bf16 a[56:59], a[44:47], a[180:183], a[56:59]\n\t"
"ds_read_b64_tr_b16 v[230:231], v246 offset:15360\n\t"
"v_fma_f32 v210, v106, s64, -v8\n\t"
"v_mfma_f32_16x16x32_bf16 a[56:59], a[52:55], a[184:187], a[56:59]\n\t"
"v_fma_f32 v211, v107, s64, -v9\n\t"
"v_fma_f32 v216, v108, s64, -v10\n\t"
"v_fma_f32 v217, v109, s64, -v11\n\t"
"v_fma_f32 v218, v110, s64, -v12\n\t"
"v_mfma_f32_16x16x32_bf16 a[64:67], a[36:39], a[180:183], a[64:67]\n\t"
"v_fma_f32 v219, v111, s64, -v13\n\t"
"v_fma_f32 v98, v112, s64, -v14\n\t"
"v_mfma_f32_16x16x32_bf16 a[64:67], a[44:47], a[184:187], a[64:67]\n\t"
"v_cvt_pk_bf16_f32 v224, v224, v225\n\t"
"v_cvt_pk_bf16_f32 v225, v226, v227\n\t"
"v_mfma_f32_16x16x32_bf16 a[64:67], a[52:55], a[188:191], a[64:67]\n\t"
"v_fma_f32 v99, v113, s64, -v15\n\t"
"v_accvgpr_write_b32 a4, v98\n\t"
"v_fma_f32 v98, v114, s64, -v16\n\t"
"v_accvgpr_write_b32 a5, v99\n\t"
"s_waitcnt lgkmcnt(2)\n\t"
"v_mfma_f32_16x16x32_bf16 v[232:235], v[232:235], v[220:223], 0\n\t"
"v_fma_f32 v99, v115, s64, -v17\n\t"
"v_accvgpr_write_b32 a6, v98\n\t"
"v_accvgpr_write_b32 a7, v99\n\t"
"v_fma_f32 v98, v116, s64, -v18\n\t"
"v_mfma_f32_16x16x32_bf16 a[72:75], a[36:39], a[184:187], a[72:75]\n\t"
"v_fma_f32 v99, v117, s64, -v19\n\t"
"v_accvgpr_write_b32 a12, v98\n\t"
"v_mfma_f32_16x16x32_bf16 a[72:75], a[44:47], a[188:191], a[72:75]\n\t"
"v_fma_f32 v98, v118, s64, -v20\n\t"
"v_accvgpr_write_b32 a13, v99\n\t"
"v_mfma_f32_16x16x32_bf16 a[72:75], a[52:55], a[192:195], a[72:75]\n\t"
"v_fma_f32 v99, v119, s64, -v21\n\t"
"v_accvgpr_write_b32 a14, v98\n\t"
"v_accvgpr_write_b32 a15, v99\n\t"
"v_fma_f32 v98, v120, s64, -v22\n\t"
"v_mfma_f32_16x16x32_bf16 a[80:83], a[36:39], a[188:191], a[80:83]\n\t"
"v_fma_f32 v99, v121, s64, -v23\n\t"
"v_accvgpr_write_b32 a20, v98\n\t"
"v_mfma_f32_16x16x32_bf16 a[80:83], a[44:47], a[192:195], a[80:83]\n\t"
"v_cvt_pk_bf16_f32 v226, v232, v233\n\t"
"v_cvt_pk_bf16_f32 v227, v234, v235\n\t"
"v_mfma_f32_16x16x32_bf16 a[80:83], a[52:55], a[196:199], a[80:83]\n\t"
"ds_read_b64_tr_b16 v[232:233], v246 offset:19968\n\t"
"ds_read_b64_tr_b16 v[234:235], v246 offset:23040\n\t"
"v_fma_f32 v98, v122, s64, -v24\n\t"
"v_accvgpr_write_b32 a21, v99\n\t"
"v_mfma_f32_16x16x32_bf16 a[88:91], a[36:39], a[192:195], a[88:91]\n\t"
"v_fma_f32 v99, v123, s64, -v25\n\t"
"v_accvgpr_write_b32 a22, v98\n\t"
"v_mfma_f32_16x16x32_bf16 a[88:91], a[44:47], a[196:199], a[88:91]\n\t"
"v_accvgpr_write_b32 a23, v99\n\t"
"v_fma_f32 v98, v124, s64, -v26\n\t"
"v_mfma_f32_16x16x32_bf16 a[88:91], a[52:55], a[200:203], a[88:91]\n\t"
"v_fma_f32 v99, v125, s64, -v27\n\t"
"v_accvgpr_write_b32 a28, v98\n\t"
"v_fma_f32 v98, v126, s64, -v28\n\t"
"v_accvgpr_write_b32 a29, v99\n\t"
"s_waitcnt lgkmcnt(2)\n\t"
"v_mfma_f32_16x16x32_bf16 v[228:231], v[228:231], v[220:223], 0\n\t"
"v_fma_f32 v99, v127, s64, -v29\n\t"
"v_accvgpr_write_b32 a30, v98\n\t"
"v_accvgpr_write_b32 a31, v99\n\t"
"v_fma_f32 v0, v2, s52, v100\n\t"
"v_mfma_f32_16x16x32_bf16 a[96:99], a[36:39], a[196:199], a[96:99]\n\t"
"v_fma_f32 v98, v128, s64, -v30\n\t"
"v_fma_f32 v99, v129, s64, -v31\n\t"
"v_mfma_f32_16x16x32_bf16 a[96:99], a[44:47], a[200:203], a[96:99]\n\t"
"v_accvgpr_write_b32 a36, v98\n\t"
"v_fma_f32 v98, v130, s64, -v32\n\t"
"v_mfma_f32_16x16x32_bf16 a[96:99], a[52:55], a[204:207], a[96:99]\n\t"
"v_accvgpr_write_b32 a37, v99\n\t"
"v_fma_f32 v99, v131, s64, -v33\n\t"
"v_accvgpr_write_b32 a38, v98\n\t"
"v_accvgpr_write_b32 a39, v99\n\t"
"s_waitcnt vmcnt(18)\n\t"
"v_mfma_f32_16x16x32_bf16 a[104:107], a[76:79], a[168:171], a[104:107]\n\t"
"ds_read_b128 a[168:171], v244 offset:9216\n\t"
"v_fma_f32 v98, v132, s64, -v34\n\t"
"v_mfma_f32_16x16x32_bf16 a[104:107], a[84:87], a[172:175], a[104:107]\n\t"
"v_cvt_pk_bf16_f32 v228, v228, v229\n\t"
"v_cvt_pk_bf16_f32 v229, v230, v231\n\t"
"v_mfma_f32_16x16x32_bf16 a[104:107], a[92:95], a[176:179], a[104:107]\n\t"
"v_fma_f32 v99, v133, s64, -v35\n\t"
"v_accvgpr_write_b32 a44, v98\n\t"
"v_fma_f32 v98, v134, s64, -v36\n\t"
"v_accvgpr_write_b32 a45, v99\n\t"
"v_mfma_f32_16x16x32_bf16 a[112:115], a[76:79], a[172:175], a[112:115]\n\t"
"ds_read_b128 a[172:175], v244 offset:9728\n\t"
"v_fma_f32 v99, v135, s64, -v37\n\t"
"v_mfma_f32_16x16x32_bf16 a[112:115], a[84:87], a[176:179], a[112:115]\n\t"
"v_accvgpr_write_b32 a46, v98\n\t"
"v_accvgpr_write_b32 a47, v99\n\t"
"v_mfma_f32_16x16x32_bf16 a[112:115], a[92:95], a[180:183], a[112:115]\n\t"
"v_fma_f32 v98, v136, s64, -v38\n\t"
"v_fma_f32 v99, v137, s64, -v39\n\t"
"v_accvgpr_write_b32 a52, v98\n\t"
"v_fma_f32 v98, v138, s64, -v40\n\t"
"s_waitcnt lgkmcnt(2)\n\t"
"v_mfma_f32_16x16x32_bf16 v[232:235], v[232:235], v[220:223], 0\n\t"
"v_accvgpr_write_b32 a53, v99\n\t"
"v_fma_f32 v99, v139, s64, -v41\n\t"
"v_accvgpr_write_b32 a54, v98\n\t"
"v_accvgpr_write_b32 a55, v99\n\t"
"v_mfma_f32_16x16x32_bf16 a[120:123], a[76:79], a[176:179], a[120:123]\n\t"
"ds_read_b128 a[176:179], v244 offset:10240\n\t"
"v_fma_f32 v98, v140, s64, -v42\n\t"
"v_mfma_f32_16x16x32_bf16 a[120:123], a[84:87], a[180:183], a[120:123]\n\t"
"v_fma_f32 v99, v141, s64, -v43\n\t"
"v_accvgpr_write_b32 a60, v98\n\t"
"v_mfma_f32_16x16x32_bf16 a[120:123], a[92:95], a[184:187], a[120:123]\n\t"
"v_fma_f32 v98, v142, s64, -v44\n\t"
"v_accvgpr_write_b32 a61, v99\n\t"
"v_fma_f32 v99, v143, s64, -v45\n\t"
"v_accvgpr_write_b32 a62, v98\n\t"
"v_mfma_f32_16x16x32_bf16 a[128:131], a[76:79], a[180:183], a[128:131]\n\t"
"ds_read_b128 a[180:183], v244 offset:10752\n\t"
"v_accvgpr_write_b32 a63, v99\n\t"
"v_mfma_f32_16x16x32_bf16 a[128:131], a[84:87], a[184:187], a[128:131]\n\t"
"v_cvt_pk_bf16_f32 v230, v232, v233\n\t"
"v_cvt_pk_bf16_f32 v231, v234, v235\n\t"
"v_mfma_f32_16x16x32_bf16 a[128:131], a[92:95], a[188:191], a[128:131]\n\t"
"ds_read_b64_tr_b16 v[232:233], v246 offset:26112\n\t"
"ds_read_b64_tr_b16 v[234:235], v246 offset:29184\n\t"
"v_fma_f32 v98, v144, s64, -v46\n\t"
"v_fma_f32 v99, v145, s64, -v47\n\t"
"v_mfma_f32_16x16x32_bf16 a[136:139], a[76:79], a[184:187], a[136:139]\n\t"
"ds_read_b128 a[184:187], v244 offset:11264\n\t"
"v_accvgpr_write_b32 a68, v98\n\t"
"v_mfma_f32_16x16x32_bf16 a[136:139], a[84:87], a[188:191], a[136:139]\n\t"
"v_fma_f32 v98, v146, s64, -v48\n\t"
"v_accvgpr_write_b32 a69, v99\n\t"
"v_mfma_f32_16x16x32_bf16 a[136:139], a[92:95], a[192:195], a[136:139]\n\t"
"v_fma_f32 v99, v147, s64, -v49\n\t"
"v_accvgpr_write_b32 a70, v98\n\t"
"v_accvgpr_write_b32 a71, v99\n\t"
"v_fma_f32 v1, v3, s52, v101\n\t"
"v_mfma_f32_16x16x32_bf16 a[144:147], a[76:79], a[188:191], a[144:147]\n\t"
"ds_read_b128 a[188:191], v244 offset:11776\n\t"
"v_fma_f32 v100, -v100, s66, v2\n\t"
"v_mfma_f32_16x16x32_bf16 a[144:147], a[84:87], a[192:195], a[144:147]\n\t"
"v_fma_f32 v2, v4, s52, v102\n\t"
"v_fma_f32 v101, -v101, s66, v3\n\t"
"v_mfma_f32_16x16x32_bf16 a[144:147], a[92:95], a[196:199], a[144:147]\n\t"
"v_fma_f32 v3, v5, s52, v103\n\t"
"v_fma_f32 v102, -v102, s66, v4\n\t"
"v_fma_f32 v103, -v103, s66, v5\n\t"
"v_fma_f32 v4, v6, s52, v104\n\t"
"v_mfma_f32_16x16x32_bf16 a[152:155], a[76:79], a[192:195], a[152:155]\n\t"
"ds_read_b128 a[192:195], v244 offset:12288\n\t"
"v_fma_f32 v5, v7, s52, v105\n\t"
"v_mfma_f32_16x16x32_bf16 a[152:155], a[84:87], a[196:199], a[152:155]\n\t"
"v_fma_f32 v104, -v104, s66, v6\n\t"
"v_fma_f32 v6, v8, s52, v106\n\t"
"v_mfma_f32_16x16x32_bf16 a[152:155], a[92:95], a[200:203], a[152:155]\n\t"
"v_fma_f32 v105, -v105, s66, v7\n\t"
"v_fma_f32 v7, v9, s52, v107\n\t"
"v_fma_f32 v106, -v106, s66, v8\n\t"
"v_fma_f32 v107, -v107, s66, v9\n\t"
"v_mfma_f32_16x16x32_bf16 a[160:163], a[76:79], a[196:199], a[160:163]\n\t"
"ds_read_b128 a[196:199], v244 offset:12800\n\t"
"v_fma_f32 v98, v148, s64, -v50\n\t"
"v_mfma_f32_16x16x32_bf16 a[160:163], a[84:87], a[200:203], a[160:163]\n\t"
"ds_read_b128 a[200:203], v244 offset:13312\n\t"
"v_fma_f32 v99, v149, s64, -v51\n\t"
"v_mfma_f32_16x16x32_bf16 a[160:163], a[92:95], a[204:207], a[160:163]\n\t"
"ds_read_b128 a[204:207], v244 offset:13824\n\t"
"v_accvgpr_write_b32 a76, v98\n\t"
"v_fma_f32 v98, v150, s64, -v52\n\t"
"v_accvgpr_write_b32 a77, v99\n\t"
"s_waitcnt lgkmcnt(6)\n\t"
"v_mfma_f32_16x16x32_bf16 v[232:235], v[232:235], v[220:223], 0\n\t"
"v_fma_f32 v99, v151, s64, -v53\n\t"
"v_accvgpr_write_b32 a78, v98\n\t"
"v_accvgpr_write_b32 a79, v99\n\t"
"v_fma_f32 v98, v152, s64, -v54\n\t"
"s_waitcnt vmcnt(15)\n\t"
"v_mfma_f32_16x16x32_bf16 v[200:203], a[148:151], a[168:171], v[200:203]\n\t"
"v_fma_f32 v99, v153, s64, -v55\n\t"
"v_accvgpr_write_b32 a84, v98\n\t"
"v_mfma_f32_16x16x32_bf16 v[200:203], a[156:159], a[172:175], v[200:203]\n\t"
"v_fma_f32 v98, v154, s64, -v56\n\t"
"v_accvgpr_write_b32 a85, v99\n\t"
"v_mfma_f32_16x16x32_bf16 v[200:203], a[164:167], a[176:179], v[200:203]\n\t"
"v_fma_f32 v99, v155, s64, -v57\n\t"
"v_accvgpr_write_b32 a86, v98\n\t"
"v_accvgpr_write_b32 a87, v99\n\t"
"v_fma_f32 v98, v156, s64, -v58\n\t"
"v_mfma_f32_16x16x32_bf16 v[208:211], a[148:151], a[172:175], v[208:211]\n\t"
"v_fma_f32 v99, v157, s64, -v59\n\t"
"v_accvgpr_write_b32 a92, v98\n\t"
"v_mfma_f32_16x16x32_bf16 v[208:211], a[156:159], a[176:179], v[208:211]\n\t"
"v_cvt_pk_bf16_f32 v232, v232, v233\n\t"
"v_cvt_pk_bf16_f32 v233, v234, v235\n\t"
"v_mfma_f32_16x16x32_bf16 v[208:211], a[164:167], a[180:183], v[208:211]\n\t"
"v_fma_f32 v98, v158, s64, -v60\n\t"
"v_accvgpr_write_b32 a93, v99\n\t"
"v_fma_f32 v99, v159, s64, -v61\n\t"
"v_accvgpr_write_b32 a94, v98\n\t"
"s_waitcnt lgkmcnt(0)\n\t"
"s_barrier\n\t"
"v_mfma_f32_16x16x32_bf16 v[216:219], a[148:151], a[176:179], v[216:219]\n\t"
"v_accvgpr_write_b32 a95, v99\n\t"
"v_fma_f32 v98, v160, s64, -v62\n\t"
"v_mfma_f32_16x16x32_bf16 v[216:219], a[156:159], a[180:183], v[216:219]\n\t"
"v_fma_f32 v99, v161, s64, -v63\n\t"
"v_accvgpr_write_b32 a100, v98\n\t"
"v_mfma_f32_16x16x32_bf16 v[216:219], a[164:167], a[184:187], v[216:219]\n\t"
"s_add_u32 m0, s24, 896\n\t"
"s_add_u32 s28, s21, 0\n\t"
"buffer_load_dwordx4 v247, s[12:15], s28 offen offset:128 nt lds\n\t"
"ds_write2st64_b64 v245, v[224:225], v[226:227] offset0:0 offset1:1\n\t"
"s_add_u32 m0, s24, 7040\n\t"
"s_add_u32 s28, s21, 196608\n\t"
"buffer_load_dwordx4 v247, s[12:15], s28 offen offset:128 nt lds\n\t"
"v_mfma_f32_16x16x32_bf16 a[4:7], a[148:151], a[180:183], a[4:7]\n\t"
"ds_read_b64_tr_b16 v[224:225], v246 offset:32256\n\t"
"v_fma_f32 v98, v162, s64, -v64\n\t"
"v_mfma_f32_16x16x32_bf16 a[4:7], a[156:159], a[184:187], a[4:7]\n\t"
"ds_read_b64_tr_b16 v[226:227], v246 offset:35328\n\t"
"v_accvgpr_write_b32 a101, v99\n\t"
"v_mfma_f32_16x16x32_bf16 a[4:7], a[164:167], a[188:191], a[4:7]\n\t"
"ds_write2st64_b64 v245, v[228:229], v[230:231] offset0:2 offset1:3\n\t"
"s_add_u32 m0, s24, 13184\n\t"
"s_add_u32 s28, s21, 393216\n\t"
"buffer_load_dwordx4 v247, s[12:15], s28 offen offset:128 nt lds\n\t"
"v_mfma_f32_16x16x32_bf16 a[12:15], a[148:151], a[184:187], a[12:15]\n\t"
"ds_read_b64_tr_b16 v[228:229], v246 offset:39936\n\t"
"v_fma_f32 v99, v163, s64, -v65\n\t"
"v_mfma_f32_16x16x32_bf16 a[12:15], a[156:159], a[188:191], a[12:15]\n\t"
"ds_read_b64_tr_b16 v[230:231], v246 offset:43008\n\t"
"v_accvgpr_write_b32 a102, v98\n\t"
"v_mfma_f32_16x16x32_bf16 a[12:15], a[164:167], a[192:195], a[12:15]\n\t"
"ds_write_b64 v245, v[232:233] offset:2048\n\t"
"s_add_u32 m0, s24, 20864\n\t"
"s_add_u32 s28, s21, 589824\n\t"
"buffer_load_dwordx4 v247, s[12:15], s28 offen offset:128 nt lds\n\t"
"s_add_u32 m0, s24, 60800\n\t"
"s_add_u32 s28, s21, 1769472\n\t"
"buffer_load_dwordx4 v247, s[12:15], s28 offen offset:128 nt lds\n\t"
"v_mfma_f32_16x16x32_bf16 a[20:23], a[148:151], a[188:191], a[20:23]\n\t"
"ds_read_b64_tr_b16 v[232:233], v246 offset:46080\n\t"
"v_accvgpr_write_b32 a103, v99\n\t"
"v_mfma_f32_16x16x32_bf16 a[20:23], a[156:159], a[192:195], a[20:23]\n\t"
"ds_read_b64_tr_b16 v[234:235], v246 offset:49152\n\t"
"v_fma_f32 v98, v164, s64, -v66\n\t"
"v_mfma_f32_16x16x32_bf16 a[20:23], a[164:167], a[196:199], a[20:23]\n\t"
"s_add_u32 m0, s24, 66944\n\t"
"s_add_u32 s28, s21, 1966080\n\t"
"buffer_load_dwordx4 v247, s[12:15], s28 offen offset:128 nt lds\n\t"
"v_fma_f32 v99, v165, s64, -v67\n\t"
"v_accvgpr_write_b32 a108, v98\n\t"
"s_waitcnt lgkmcnt(6)\n\t"
"v_mfma_f32_16x16x32_bf16 v[224:227], v[224:227], v[220:223], 0\n\t"
"v_fma_f32 v98, v166, s64, -v68\n\t"
"v_accvgpr_write_b32 a109, v99\n\t"
"v_fma_f32 v99, v167, s64, -v69\n\t"
"v_accvgpr_write_b32 a110, v98\n\t"
"v_mfma_f32_16x16x32_bf16 a[28:31], a[148:151], a[192:195], a[28:31]\n\t"
"v_accvgpr_write_b32 a111, v99\n\t"
"v_fma_f32 v98, v168, s64, -v70\n\t"
"v_mfma_f32_16x16x32_bf16 a[28:31], a[156:159], a[196:199], a[28:31]\n\t"
"v_fma_f32 v99, v169, s64, -v71\n\t"
"v_accvgpr_write_b32 a116, v98\n\t"
"v_mfma_f32_16x16x32_bf16 a[28:31], a[164:167], a[200:203], a[28:31]\n\t"
"v_fma_f32 v98, v170, s64, -v72\n\t"
"v_accvgpr_write_b32 a117, v99\n\t"
"v_fma_f32 v99, v171, s64, -v73\n\t"
"v_accvgpr_write_b32 a118, v98\n\t"
"s_waitcnt lgkmcnt(3)\n\t"
"v_mfma_f32_16x16x32_bf16 v[228:231], v[228:231], v[220:223], 0\n\t"
"v_accvgpr_write_b32 a119, v99\n\t"
"v_fma_f32 v98, v172, s64, -v74\n\t"
"v_fma_f32 v99, v173, s64, -v75\n\t"
"v_accvgpr_write_b32 a124, v98\n\t"
"v_mfma_f32_16x16x32_bf16 a[36:39], a[148:151], a[196:199], a[36:39]\n\t"
"v_cvt_pk_bf16_f32 v224, v224, v225\n\t"
"v_cvt_pk_bf16_f32 v225, v226, v227\n\t"
"v_mfma_f32_16x16x32_bf16 a[36:39], a[156:159], a[200:203], a[36:39]\n\t"
"v_fma_f32 v98, v174, s64, -v76\n\t"
"v_accvgpr_write_b32 a125, v99\n\t"
"v_mfma_f32_16x16x32_bf16 a[36:39], a[164:167], a[204:207], a[36:39]\n\t"
"v_fma_f32 v99, v175, s64, -v77\n\t"
"v_accvgpr_write_b32 a126, v98\n\t"
"v_accvgpr_write_b32 a127, v99\n\t"
"v_fma_f32 v98, v176, s64, -v78\n\t"
"s_waitcnt lgkmcnt(0)\n\t"
"v_mfma_f32_16x16x32_bf16 v[232:235], v[232:235], v[220:223], 0\n\t"
"v_fma_f32 v99, v177, s64, -v79\n\t"
"v_accvgpr_write_b32 a132, v98\n\t"
"v_fma_f32 v98, v178, s64, -v80\n\t"
"v_accvgpr_write_b32 a133, v99\n\t"
"s_waitcnt vmcnt(18)\n\t"
"v_mfma_f32_16x16x32_bf16 a[44:47], a[208:211], a[168:171], a[44:47]\n\t"
"v_cvt_pk_bf16_f32 v226, v228, v229\n\t"
"v_cvt_pk_bf16_f32 v227, v230, v231\n\t"
"v_mfma_f32_16x16x32_bf16 a[44:47], v[248:251], a[172:175], a[44:47]\n\t"
"ds_read_b64_tr_b16 v[228:229], v246 offset:52224\n\t"
"v_fma_f32 v99, v179, s64, -v81\n\t"
"v_mfma_f32_16x16x32_bf16 a[44:47], a[212:215], a[176:179], a[44:47]\n\t"
"ds_write2st64_b64 v245, v[224:225], v[226:227] offset0:5 offset1:6\n\t"
"ds_read_b64_tr_b16 v[230:231], v246 offset:55296\n\t"
"v_accvgpr_write_b32 a134, v98\n\t"
"v_mfma_f32_16x16x32_bf16 a[52:55], a[208:211], a[172:175], a[52:55]\n\t"
"v_accvgpr_write_b32 a135, v99\n\t"
"v_fma_f32 v98, v180, s64, -v82\n\t"
"v_mfma_f32_16x16x32_bf16 a[52:55], v[248:251], a[176:179], a[52:55]\n\t"
"v_cvt_pk_bf16_f32 v232, v232, v233\n\t"
"v_cvt_pk_bf16_f32 v233, v234, v235\n\t"
"v_mfma_f32_16x16x32_bf16 a[52:55], a[212:215], a[180:183], a[52:55]\n\t"
"v_fma_f32 v99, v181, s64, -v83\n\t"
"v_accvgpr_write_b32 a140, v98\n\t"
"v_fma_f32 v98, v182, s64, -v84\n\t"
"v_accvgpr_write_b32 a141, v99\n\t"
"v_mfma_f32_16x16x32_bf16 a[60:63], a[208:211], a[176:179], a[60:63]\n\t"
"v_fma_f32 v99, v183, s64, -v85\n\t"
"v_accvgpr_write_b32 a142, v98\n\t"
"v_mfma_f32_16x16x32_bf16 a[60:63], v[248:251], a[180:183], a[60:63]\n\t"
"v_accvgpr_write_b32 a143, v99\n\t"
"v_fma_f32 v98, v184, s64, -v86\n\t"
"v_mfma_f32_16x16x32_bf16 a[60:63], a[212:215], a[184:187], a[60:63]\n\t"
"v_fma_f32 v99, v185, s64, -v87\n\t"
"v_accvgpr_write_b32 a148, v98\n\t"
"v_fma_f32 v98, v186, s64, -v88\n\t"
"v_accvgpr_write_b32 a149, v99\n\t"
"v_mfma_f32_16x16x32_bf16 a[68:71], a[208:211], a[180:183], a[68:71]\n\t"
"v_fma_f32 v99, v187, s64, -v89\n\t"
"v_accvgpr_write_b32 a150, v98\n\t"
"v_mfma_f32_16x16x32_bf16 a[68:71], v[248:251], a[184:187], a[68:71]\n\t"
"v_accvgpr_write_b32 a151, v99\n\t"
"v_fma_f32 v98, v188, s64, -v90\n\t"
"v_mfma_f32_16x16x32_bf16 a[68:71], a[212:215], a[188:191], a[68:71]\n\t"
"v_fma_f32 v99, v189, s64, -v91\n\t"
"v_accvgpr_write_b32 a156, v98\n\t"
"v_fma_f32 v98, v190, s64, -v92\n\t"
"v_accvgpr_write_b32 a157, v99\n\t"
"v_mfma_f32_16x16x32_bf16 a[76:79], a[208:211], a[184:187], a[76:79]\n\t"
"v_fma_f32 v99, v191, s64, -v93\n\t"
"v_accvgpr_write_b32 a158, v98\n\t"
"v_mfma_f32_16x16x32_bf16 a[76:79], v[248:251], a[188:191], a[76:79]\n\t"
"v_accvgpr_write_b32 a159, v99\n\t"
"v_fma_f32 v98, v192, s64, -v94\n\t"
"v_mfma_f32_16x16x32_bf16 a[76:79], a[212:215], a[192:195], a[76:79]\n\t"
"v_fma_f32 v99, v193, s64, -v95\n\t"
"v_accvgpr_write_b32 a164, v98\n\t"
"v_fma_f32 v98, v194, s64, -v96\n\t"
"v_accvgpr_write_b32 a165, v99\n\t"
"s_waitcnt lgkmcnt(0)\n\t"
"v_mfma_f32_16x16x32_bf16 v[228:231], v[228:231], v[220:223], 0\n\t"
"v_fma_f32 v99, v195, s64, -v97\n\t"
"v_accvgpr_write_b32 a166, v98\n\t"
"v_accvgpr_write_b32 a167, v99\n\t"
"v_fma_f32 v8, v10, s52, v108\n\t"
"v_mfma_f32_16x16x32_bf16 a[84:87], a[208:211], a[188:191], a[84:87]\n\t"
"v_fma_f32 v9, v11, s52, v109\n\t"
"v_fma_f32 v108, -v108, s66, v10\n\t"
"v_mfma_f32_16x16x32_bf16 a[84:87], v[248:251], a[192:195], a[84:87]\n\t"
"v_fma_f32 v10, v12, s52, v110\n\t"
"v_fma_f32 v109, -v109, s66, v11\n\t"
"v_mfma_f32_16x16x32_bf16 a[84:87], a[212:215], a[196:199], a[84:87]\n\t"
"v_fma_f32 v11, v13, s52, v111\n\t"
"v_fma_f32 v110, -v110, s66, v12\n\t"
"v_fma_f32 v111, -v111, s66, v13\n\t"
"v_fma_f32 v12, v14, s52, v112\n\t"
"v_mfma_f32_16x16x32_bf16 a[92:95], a[208:211], a[192:195], a[92:95]\n\t"
"v_fma_f32 v13, v15, s52, v113\n\t"
"v_fma_f32 v112, -v112, s66, v14\n\t"
"v_mfma_f32_16x16x32_bf16 a[92:95], v[248:251], a[196:199], a[92:95]\n\t"
"v_cvt_pk_bf16_f32 v234, v228, v229\n\t"
"v_cvt_pk_bf16_f32 v235, v230, v231\n\t"
"v_mfma_f32_16x16x32_bf16 a[92:95], a[212:215], a[200:203], a[92:95]\n\t"
"ds_write2st64_b64 v245, v[232:233], v[234:235] offset0:7 offset1:8\n\t"
"v_fma_f32 v14, v16, s52, v114\n\t"
"v_fma_f32 v113, -v113, s66, v15\n\t"
"v_mfma_f32_16x16x32_bf16 a[100:103], a[208:211], a[196:199], a[100:103]\n\t"
"v_fma_f32 v15, v17, s52, v115\n\t"
"v_fma_f32 v114, -v114, s66, v16\n\t"
"v_mfma_f32_16x16x32_bf16 a[100:103], v[248:251], a[200:203], a[100:103]\n\t"
"v_fma_f32 v115, -v115, s66, v17\n\t"
"v_fma_f32 v16, v18, s52, v116\n\t"
"v_mfma_f32_16x16x32_bf16 a[100:103], a[212:215], a[204:207], a[100:103]\n\t"
"s_mov_b32 s20, 73728\n\t"
"buffer_load_dwordx4 a[208:211], v252, s[8:11], s20 offen offset:0 sc1\n\t"
"v_fma_f32 v17, v19, s52, v117\n\t"
"v_fma_f32 v116, -v116, s66, v18\n\t"
"s_waitcnt vmcnt(16)\n\t"
"v_mfma_f32_16x16x32_bf16 a[108:111], a[216:219], a[168:171], a[108:111]\n\t"
"v_fma_f32 v18, v20, s52, v118\n\t"
"v_fma_f32 v117, -v117, s66, v19\n\t"
"v_mfma_f32_16x16x32_bf16 a[108:111], a[220:223], a[172:175], a[108:111]\n\t"
"v_fma_f32 v19, v21, s52, v119\n\t"
"v_fma_f32 v118, -v118, s66, v20\n\t"
"v_mfma_f32_16x16x32_bf16 a[108:111], v[240:243], a[176:179], a[108:111]\n\t"
"v_fma_f32 v119, -v119, s66, v21\n\t"
"v_fma_f32 v20, v22, s52, v120\n\t"
"v_fma_f32 v21, v23, s52, v121\n\t"
"v_fma_f32 v120, -v120, s66, v22\n\t"
"s_waitcnt lgkmcnt(0)\n\t"
"s_barrier\n\t"
"v_mfma_f32_16x16x32_bf16 a[116:119], a[216:219], a[172:175], a[116:119]\n\t"
"ds_read_b128 a[168:171], v244 offset:0\n\t"
"v_fma_f32 v22, v24, s52, v122\n\t"
"v_mfma_f32_16x16x32_bf16 a[116:119], a[220:223], a[176:179], a[116:119]\n\t"
"ds_read_b128 a[172:175], v244 offset:512\n\t"
"v_fma_f32 v121, -v121, s66, v23\n\t"
"v_mfma_f32_16x16x32_bf16 a[116:119], v[240:243], a[180:183], a[116:119]\n\t"
"buffer_load_dwordx4 v[248:251], v252, s[8:11], s20 offen offset:1024 sc1\n\t"
"v_fma_f32 v23, v25, s52, v123\n\t"
"v_fma_f32 v122, -v122, s66, v24\n\t"
"v_mfma_f32_16x16x32_bf16 a[124:127], a[216:219], a[176:179], a[124:127]\n\t"
"ds_read_b128 a[176:179], v244 offset:1024\n\t"
"v_fma_f32 v123, -v123, s66, v25\n\t"
"v_mfma_f32_16x16x32_bf16 a[124:127], a[220:223], a[180:183], a[124:127]\n\t"
"v_fma_f32 v24, v26, s52, v124\n\t"
"v_fma_f32 v25, v27, s52, v125\n\t"
"v_mfma_f32_16x16x32_bf16 a[124:127], v[240:243], a[184:187], a[124:127]\n\t"
"buffer_load_dwordx4 a[212:215], v252, s[8:11], s20 offen offset:2048 sc1\n\t"
"v_fma_f32 v124, -v124, s66, v26\n\t"
"v_fma_f32 v26, v28, s52, v126\n\t"
"v_mfma_f32_16x16x32_bf16 a[132:135], a[216:219], a[180:183], a[132:135]\n\t"
"ds_read_b128 a[180:183], v244 offset:1536\n\t"
"v_fma_f32 v125, -v125, s66, v27\n\t"
"v_mfma_f32_16x16x32_bf16 a[132:135], a[220:223], a[184:187], a[132:135]\n\t"
"v_fma_f32 v27, v29, s52, v127\n\t"
"v_fma_f32 v126, -v126, s66, v28\n\t"
"v_mfma_f32_16x16x32_bf16 a[132:135], v[240:243], a[188:191], a[132:135]\n\t"
"v_fma_f32 v127, -v127, s66, v29\n\t"
"v_fma_f32 v28, v30, s52, v128\n\t"
"v_fma_f32 v29, v31, s52, v129\n\t"
"v_fma_f32 v128, -v128, s66, v30\n\t"
"v_mfma_f32_16x16x32_bf16 a[140:143], a[216:219], a[184:187], a[140:143]\n\t"
"ds_read_b128 a[184:187], v244 offset:2048\n\t"
"v_fma_f32 v30, v32, s52, v130\n\t"
"v_mfma_f32_16x16x32_bf16 a[140:143], a[220:223], a[188:191], a[140:143]\n\t"
"v_fma_f32 v129, -v129, s66, v31\n\t"
"v_fma_f32 v31, v33, s52, v131\n\t"
"v_mfma_f32_16x16x32_bf16 a[140:143], v[240:243], a[192:195], a[140:143]\n\t"
"v_fma_f32 v130, -v130, s66, v32\n\t"
"v_fma_f32 v131, -v131, s66, v33\n\t"
"v_fma_f32 v32, v34, s52, v132\n\t"
"v_fma_f32 v33, v35, s52, v133\n\t"
"v_mfma_f32_16x16x32_bf16 a[148:151], a[216:219], a[188:191], a[148:151]\n\t"
"ds_read_b128 a[188:191], v244 offset:2560\n\t"
"v_fma_f32 v132, -v132, s66, v34\n\t"
"v_mfma_f32_16x16x32_bf16 a[148:151], a[220:223], a[192:195], a[148:151]\n\t"
"v_fma_f32 v34, v36, s52, v134\n\t"
"v_fma_f32 v133, -v133, s66, v35\n\t"
"v_mfma_f32_16x16x32_bf16 a[148:151], v[240:243], a[196:199], a[148:151]\n\t"
"v_fma_f32 v35, v37, s52, v135\n\t"
"v_fma_f32 v134, -v134, s66, v36\n\t"
"v_fma_f32 v135, -v135, s66, v37\n\t"
"v_fma_f32 v36, v38, s52, v136\n\t"
"v_mfma_f32_16x16x32_bf16 a[156:159], a[216:219], a[192:195], a[156:159]\n\t"
"ds_read_b128 a[192:195], v244 offset:3072\n\t"
"v_fma_f32 v37, v39, s52, v137\n\t"
"v_mfma_f32_16x16x32_bf16 a[156:159], a[220:223], a[196:199], a[156:159]\n\t"
"v_fma_f32 v136, -v136, s66, v38\n\t"
"v_fma_f32 v38, v40, s52, v138\n\t"
"v_mfma_f32_16x16x32_bf16 a[156:159], v[240:243], a[200:203], a[156:159]\n\t"
"v_fma_f32 v137, -v137, s66, v39\n\t"
"v_fma_f32 v39, v41, s52, v139\n\t"
"v_fma_f32 v138, -v138, s66, v40\n\t"
"v_fma_f32 v139, -v139, s66, v41\n\t"
"v_mfma_f32_16x16x32_bf16 a[164:167], a[216:219], a[196:199], a[164:167]\n\t"
"ds_read_b128 a[196:199], v244 offset:3584\n\t"
"v_fma_f32 v40, v42, s52, v140\n\t"
"v_mfma_f32_16x16x32_bf16 a[164:167], a[220:223], a[200:203], a[164:167]\n\t"
"ds_read_b128 a[200:203], v244 offset:4096\n\t"
"v_fma_f32 v41, v43, s52, v141\n\t"
"v_mfma_f32_16x16x32_bf16 a[164:167], v[240:243], a[204:207], a[164:167]\n\t"
"s_mov_b32 s20, 86016\n\t"
"buffer_load_dwordx4 a[216:219], v252, s[8:11], s20 offen offset:0 sc1\n\t"
"ds_read_b128 a[204:207], v244 offset:4608\n\t"
"v_fma_f32 v140, -v140, s66, v42\n\t"
"Lst3_%=:\n\t"
"s_waitcnt vmcnt(16) lgkmcnt(7)\n\t"
"v_mfma_f32_16x16x32_bf16 v[0:3], a[224:227], a[168:171], v[0:3]\n\t"
"v_fma_f32 v42, v44, s52, v142\n\t"
"v_fma_f32 v141, -v141, s66, v43\n\t"
"v_mfma_f32_16x16x32_bf16 v[0:3], a[228:231], a[172:175], v[0:3]\n\t"
"v_fma_f32 v43, v45, s52, v143\n\t"
"v_fma_f32 v142, -v142, s66, v44\n\t"
"v_mfma_f32_16x16x32_bf16 v[0:3], a[232:235], a[176:179], v[0:3]\n\t"
"buffer_load_dwordx4 v[240:243], v252, s[8:11], s20 offen offset:1024 sc1\n\t"
"v_fma_f32 v143, -v143, s66, v45\n\t"
"v_fma_f32 v44, v46, s52, v144\n\t"
"s_waitcnt lgkmcnt(6)\n\t"
"v_mfma_f32_16x16x32_bf16 v[4:7], a[224:227], a[172:175], v[4:7]\n\t"
"v_fma_f32 v45, v47, s52, v145\n\t"
"v_fma_f32 v144, -v144, s66, v46\n\t"
"v_mfma_f32_16x16x32_bf16 v[4:7], a[228:231], a[176:179], v[4:7]\n\t"
"v_fma_f32 v46, v48, s52, v146\n\t"
"v_fma_f32 v145, -v145, s66, v47\n\t"
"v_mfma_f32_16x16x32_bf16 v[4:7], a[232:235], a[180:183], v[4:7]\n\t"
"buffer_load_dwordx4 a[220:223], v252, s[8:11], s20 offen offset:2048 sc1\n\t"
"v_fma_f32 v47, v49, s52, v147\n\t"
"v_fma_f32 v146, -v146, s66, v48\n\t"
"s_waitcnt lgkmcnt(5)\n\t"
"v_mfma_f32_16x16x32_bf16 v[8:11], a[224:227], a[176:179], v[8:11]\n\t"
"v_fma_f32 v147, -v147, s66, v49\n\t"
"v_fma_f32 v48, v50, s52, v148\n\t"
"v_mfma_f32_16x16x32_bf16 v[8:11], a[228:231], a[180:183], v[8:11]\n\t"
"v_fma_f32 v49, v51, s52, v149\n\t"
"v_fma_f32 v148, -v148, s66, v50\n\t"
"v_mfma_f32_16x16x32_bf16 v[8:11], a[232:235], a[184:187], v[8:11]\n\t"
"v_fma_f32 v50, v52, s52, v150\n\t"
"v_fma_f32 v149, -v149, s66, v51\n\t"
"v_fma_f32 v51, v53, s52, v151\n\t"
"v_fma_f32 v150, -v150, s66, v52\n\t"
"s_waitcnt lgkmcnt(4)\n\t"
"v_mfma_f32_16x16x32_bf16 v[12:15], a[224:227], a[180:183], v[12:15]\n\t"
"v_fma_f32 v151, -v151, s66, v53\n\t"
"v_fma_f32 v52, v54, s52, v152\n\t"
"v_mfma_f32_16x16x32_bf16 v[12:15], a[228:231], a[184:187], v[12:15]\n\t"
"v_fma_f32 v53, v55, s52, v153\n\t"
"v_fma_f32 v152, -v152, s66, v54\n\t"
"v_mfma_f32_16x16x32_bf16 v[12:15], a[232:235], a[188:191], v[12:15]\n\t"
"v_fma_f32 v54, v56, s52, v154\n\t"
"v_fma_f32 v153, -v153, s66, v55\n\t"
"v_fma_f32 v55, v57, s52, v155\n\t"
"v_fma_f32 v154, -v154, s66, v56\n\t"
"s_waitcnt lgkmcnt(3)\n\t"
"v_mfma_f32_16x16x32_bf16 v[16:19], a[224:227], a[184:187], v[16:19]\n\t"
"v_fma_f32 v155, -v155, s66, v57\n\t"
"v_fma_f32 v56, v58, s52, v156\n\t"
"v_mfma_f32_16x16x32_bf16 v[16:19], a[228:231], a[188:191], v[16:19]\n\t"
"v_fma_f32 v57, v59, s52, v157\n\t"
"v_fma_f32 v156, -v156, s66, v58\n\t"
"v_mfma_f32_16x16x32_bf16 v[16:19], a[232:235], a[192:195], v[16:19]\n\t"
"ds_read_b64_tr_b16 v[224:225], v246 offset:512\n\t"
"ds_read_b64_tr_b16 v[226:227], v246 offset:3584\n\t"
"v_fma_f32 v58, v60, s52, v158\n\t"
"v_fma_f32 v157, -v157, s66, v59\n\t"
"s_waitcnt lgkmcnt(4)\n\t"
"v_mfma_f32_16x16x32_bf16 v[20:23], a[224:227], a[188:191], v[20:23]\n\t"
"v_fma_f32 v59, v61, s52, v159\n\t"
"v_fma_f32 v158, -v158, s66, v60\n\t"
"v_mfma_f32_16x16x32_bf16 v[20:23], a[228:231], a[192:195], v[20:23]\n\t"
"v_fma_f32 v159, -v159, s66, v61\n\t"
"v_fma_f32 v60, v62, s52, v160\n\t"
"v_mfma_f32_16x16x32_bf16 v[20:23], a[232:235], a[196:199], v[20:23]\n\t"
"v_fma_f32 v61, v63, s52, v161\n\t"
"v_fma_f32 v160, -v160, s66, v62\n\t"
"v_fma_f32 v62, v64, s52, v162\n\t"
"v_fma_f32 v161, -v161, s66, v63\n\t"
"s_waitcnt lgkmcnt(3)\n\t"
"v_mfma_f32_16x16x32_bf16 v[24:27], a[224:227], a[192:195], v[24:27]\n\t"
"v_fma_f32 v63, v65, s52, v163\n\t"
"v_fma_f32 v162, -v162, s66, v64\n\t"
"v_mfma_f32_16x16x32_bf16 v[24:27], a[228:231], a[196:199], v[24:27]\n\t"
"v_fma_f32 v163, -v163, s66, v65\n\t"
"v_fma_f32 v64, v66, s52, v164\n\t"
"v_mfma_f32_16x16x32_bf16 v[24:27], a[232:235], a[200:203], v[24:27]\n\t"
"v_fma_f32 v65, v67, s52, v165\n\t"
"v_fma_f32 v164, -v164, s66, v66\n\t"
"v_fma_f32 v66, v68, s52, v166\n\t"
"v_fma_f32 v165, -v165, s66, v67\n\t"
"s_waitcnt lgkmcnt(2)\n\t"
"v_mfma_f32_16x16x32_bf16 v[28:31], a[224:227], a[196:199], v[28:31]\n\t"
"ds_read_b64_tr_b16 v[228:229], v246 offset:6656\n\t"
"v_fma_f32 v67, v69, s52, v167\n\t"
"v_mfma_f32_16x16x32_bf16 v[28:31], a[228:231], a[200:203], v[28:31]\n\t"
"ds_read_b64_tr_b16 v[230:231], v246 offset:9728\n\t"
"v_fma_f32 v166, -v166, s66, v68\n\t"
"v_mfma_f32_16x16x32_bf16 v[28:31], a[232:235], a[204:207], v[28:31]\n\t"
"s_mov_b32 s20, 98304\n\t"
"buffer_load_dwordx4 a[224:227], v252, s[8:11], s20 offen offset:0 sc1\n\t"
"v_fma_f32 v167, -v167, s66, v69\n\t"
"v_fma_f32 v68, v70, s52, v168\n\t"
"s_waitcnt vmcnt(16)\n\t"
"v_mfma_f32_16x16x32_bf16 v[32:35], a[236:239], a[168:171], v[32:35]\n\t"
"v_fma_f32 v69, v71, s52, v169\n\t"
"v_fma_f32 v168, -v168, s66, v70\n\t"
"v_mfma_f32_16x16x32_bf16 v[32:35], a[240:243], a[172:175], v[32:35]\n\t"
"v_fma_f32 v70, v72, s52, v170\n\t"
"v_fma_f32 v169, -v169, s66, v71\n\t"
"v_mfma_f32_16x16x32_bf16 v[32:35], a[244:247], a[176:179], v[32:35]\n\t"
"buffer_load_dwordx4 a[228:231], v252, s[8:11], s20 offen offset:1024 sc1\n\t"
"v_fma_f32 v71, v73, s52, v171\n\t"
"v_fma_f32 v170, -v170, s66, v72\n\t"
"v_mfma_f32_16x16x32_bf16 v[36:39], a[236:239], a[172:175], v[36:39]\n\t"
"v_fma_f32 v171, -v171, s66, v73\n\t"
"v_fma_f32 v72, v74, s52, v172\n\t"
"v_mfma_f32_16x16x32_bf16 v[36:39], a[240:243], a[176:179], v[36:39]\n\t"
"v_fma_f32 v73, v75, s52, v173\n\t"
"v_fma_f32 v172, -v172, s66, v74\n\t"
"v_mfma_f32_16x16x32_bf16 v[36:39], a[244:247], a[180:183], v[36:39]\n\t"
"buffer_load_dwordx4 a[232:235], v252, s[8:11], s20 offen offset:2048 sc1\n\t"
"v_fma_f32 v74, v76, s52, v174\n\t"
"v_fma_f32 v173, -v173, s66, v75\n\t"
"s_waitcnt lgkmcnt(2)\n\t"
"v_mfma_f32_16x16x32_bf16 v[224:227], v[224:227], v[220:223], 0\n\t"
"v_fma_f32 v75, v77, s52, v175\n\t"
"v_fma_f32 v174, -v174, s66, v76\n\t"
"v_fma_f32 v175, -v175, s66, v77\n\t"
"v_fma_f32 v76, v78, s52, v176\n\t"
"v_mfma_f32_16x16x32_bf16 v[40:43], a[236:239], a[176:179], v[40:43]\n\t"
"ds_read_b64_tr_b16 v[232:233], v246 offset:12800\n\t"
"v_fma_f32 v77, v79, s52, v177\n\t"
"v_mfma_f32_16x16x32_bf16 v[40:43], a[240:243], a[180:183], v[40:43]\n\t"
"ds_read_b64_tr_b16 v[234:235], v246 offset:15872\n\t"
"v_fma_f32 v176, -v176, s66, v78\n\t"
"v_mfma_f32_16x16x32_bf16 v[40:43], a[244:247], a[184:187], v[40:43]\n\t"
"v_fma_f32 v78, v80, s52, v178\n\t"
"v_fma_f32 v177, -v177, s66, v79\n\t"
"v_fma_f32 v79, v81, s52, v179\n\t"
"v_fma_f32 v178, -v178, s66, v80\n\t"
"v_mfma_f32_16x16x32_bf16 v[44:47], a[236:239], a[180:183], v[44:47]\n\t"
"v_fma_f32 v179, -v179, s66, v81\n\t"
"v_fma_f32 v80, v82, s52, v180\n\t"
"v_mfma_f32_16x16x32_bf16 v[44:47], a[240:243], a[184:187], v[44:47]\n\t"
"v_cvt_pk_bf16_f32 v224, v224, v225\n\t"
"v_cvt_pk_bf16_f32 v225, v226, v227\n\t"
"v_mfma_f32_16x16x32_bf16 v[44:47], a[244:247], a[188:191], v[44:47]\n\t"
"v_fma_f32 v81, v83, s52, v181\n\t"
"v_fma_f32 v180, -v180, s66, v82\n\t"
"v_fma_f32 v82, v84, s52, v182\n\t"
"v_fma_f32 v181, -v181, s66, v83\n\t"
"s_waitcnt lgkmcnt(2)\n\t"
"v_mfma_f32_16x16x32_bf16 v[228:231], v[228:231], v[220:223], 0\n\t"
"v_fma_f32 v83, v85, s52, v183\n\t"
"v_fma_f32 v182, -v182, s66, v84\n\t"
"v_fma_f32 v183, -v183, s66, v85\n\t"
"v_fma_f32 v84, v86, s52, v184\n\t"
"v_mfma_f32_16x16x32_bf16 v[48:51], a[236:239], a[184:187], v[48:51]\n\t"
"v_fma_f32 v85, v87, s52, v185\n\t"
"v_fma_f32 v184, -v184, s66, v86\n\t"
"v_mfma_f32_16x16x32_bf16 v[48:51], a[240:243], a[188:191], v[48:51]\n\t"
"v_fma_f32 v86, v88, s52, v186\n\t"
"v_fma_f32 v185, -v185, s66, v87\n\t"
"v_mfma_f32_16x16x32_bf16 v[48:51], a[244:247], a[192:195], v[48:51]\n\t"
"v_fma_f32 v87, v89, s52, v187\n\t"
"v_fma_f32 v186, -v186, s66, v88\n\t"
"v_fma_f32 v187, -v187, s66, v89\n\t"
"v_fma_f32 v88, v90, s52, v188\n\t"
"v_mfma_f32_16x16x32_bf16 v[52:55], a[236:239], a[188:191], v[52:55]\n\t"
"v_fma_f32 v89, v91, s52, v189\n\t"
"v_fma_f32 v188, -v188, s66, v90\n\t"
"v_mfma_f32_16x16x32_bf16 v[52:55], a[240:243], a[192:195], v[52:55]\n\t"
"v_cvt_pk_bf16_f32 v226, v228, v229\n\t"
"v_cvt_pk_bf16_f32 v227, v230, v231\n\t"
"v_mfma_f32_16x16x32_bf16 v[52:55], a[244:247], a[196:199], v[52:55]\n\t"
"ds_read_b64_tr_b16 v[228:229], v246 offset:20480\n\t"
"ds_read_b64_tr_b16 v[230:231], v246 offset:23552\n\t"
"v_fma_f32 v90, v92, s52, v190\n\t"
"v_fma_f32 v189, -v189, s66, v91\n\t"
"v_mfma_f32_16x16x32_bf16 v[56:59], a[236:239], a[192:195], v[56:59]\n\t"
"v_fma_f32 v91, v93, s52, v191\n\t"
"v_fma_f32 v190, -v190, s66, v92\n\t"
"v_mfma_f32_16x16x32_bf16 v[56:59], a[240:243], a[196:199], v[56:59]\n\t"
"v_fma_f32 v191, -v191, s66, v93\n\t"
"v_fma_f32 v92, v94, s52, v192\n\t"
"v_mfma_f32_16x16x32_bf16 v[56:59], a[244:247], a[200:203], v[56:59]\n\t"
"v_fma_f32 v93, v95, s52, v193\n\t"
"v_fma_f32 v192, -v192, s66, v94\n\t"
"s_waitcnt lgkmcnt(2)\n\t"
"v_mfma_f32_16x16x32_bf16 v[232:235], v[232:235], v[220:223], 0\n\t"
"v_fma_f32 v94, v96, s52, v194\n\t"
"v_fma_f32 v193, -v193, s66, v95\n\t"
"v_mfma_f32_16x16x32_bf16 v[60:63], a[236:239], a[196:199], v[60:63]\n\t"
"v_fma_f32 v95, v97, s52, v195\n\t"
"v_fma_f32 v194, -v194, s66, v96\n\t"
"v_mfma_f32_16x16x32_bf16 v[60:63], a[240:243], a[200:203], v[60:63]\n\t"
"v_fma_f32 v195, -v195, s66, v97\n\t"
"v_mfma_f32_16x16x32_bf16 v[60:63], a[244:247], a[204:207], v[60:63]\n\t"
"s_waitcnt vmcnt(15)\n\t"
"v_mfma_f32_16x16x32_bf16 v[64:67], a[248:251], a[168:171], v[64:67]\n\t"
"ds_read_b128 a[168:171], v244 offset:9216\n\t"
"v_mfma_f32_16x16x32_bf16 v[64:67], a[252:255], a[172:175], v[64:67]\n\t"
"v_cvt_pk_bf16_f32 v232, v232, v233\n\t"
"v_cvt_pk_bf16_f32 v233, v234, v235\n\t"
"v_mfma_f32_16x16x32_bf16 v[64:67], v[236:239], a[176:179], v[64:67]\n\t"
"s_mov_b32 s20, 258048\n\t"
"buffer_load_dwordx4 a[236:239], v252, s[8:11], s20 offen offset:0 sc1\n\t"
"v_mfma_f32_16x16x32_bf16 v[68:71], a[248:251], a[172:175], v[68:71]\n\t"
"ds_read_b128 a[172:175], v244 offset:9728\n\t"
"v_mfma_f32_16x16x32_bf16 v[68:71], a[252:255], a[176:179], v[68:71]\n\t"
"v_mfma_f32_16x16x32_bf16 v[68:71], v[236:239], a[180:183], v[68:71]\n\t"
"buffer_load_dwordx4 a[240:243], v252, s[8:11], s20 offen offset:1024 sc1\n\t"
"s_waitcnt lgkmcnt(2)\n\t"
"v_mfma_f32_16x16x32_bf16 v[228:231], v[228:231], v[220:223], 0\n\t"
"buffer_load_dwordx4 a[244:247], v252, s[8:11], s20 offen offset:2048 sc1\n\t"
"v_mfma_f32_16x16x32_bf16 v[72:75], a[248:251], a[176:179], v[72:75]\n\t"
"v_mfma_f32_16x16x32_bf16 v[72:75], a[252:255], a[180:183], v[72:75]\n\t"
"v_mfma_f32_16x16x32_bf16 v[72:75], v[236:239], a[184:187], v[72:75]\n\t"
"ds_read_b128 a[176:179], v244 offset:10240\n\t"
"v_mfma_f32_16x16x32_bf16 v[76:79], a[248:251], a[180:183], v[76:79]\n\t"
"ds_read_b128 a[180:183], v244 offset:10752\n\t"
"v_mfma_f32_16x16x32_bf16 v[76:79], a[252:255], a[184:187], v[76:79]\n\t"
"v_cvt_pk_bf16_f32 v234, v228, v229\n\t"
"v_mfma_f32_16x16x32_bf16 v[76:79], v[236:239], a[188:191], v[76:79]\n\t"
"v_cvt_pk_bf16_f32 v235, v230, v231\n\t"
"ds_read_b64_tr_b16 v[228:229], v246 offset:26624\n\t"
"v_mfma_f32_16x16x32_bf16 v[80:83], a[248:251], a[184:187], v[80:83]\n\t"
"ds_read_b64_tr_b16 v[230:231], v246 offset:29696\n\t"
"v_mfma_f32_16x16x32_bf16 v[80:83], a[252:255], a[188:191], v[80:83]\n\t"
"ds_read_b128 a[184:187], v244 offset:11264\n\t"
"v_mfma_f32_16x16x32_bf16 v[80:83], v[236:239], a[192:195], v[80:83]\n\t"
"v_mfma_f32_16x16x32_bf16 v[84:87], a[248:251], a[188:191], v[84:87]\n\t"
"ds_read_b128 a[188:191], v244 offset:11776\n\t"
"v_mfma_f32_16x16x32_bf16 v[84:87], a[252:255], a[192:195], v[84:87]\n\t"
"v_mfma_f32_16x16x32_bf16 v[84:87], v[236:239], a[196:199], v[84:87]\n\t"
"v_mfma_f32_16x16x32_bf16 v[88:91], a[248:251], a[192:195], v[88:91]\n\t"
"ds_read_b128 a[192:195], v244 offset:12288\n\t"
"v_mfma_f32_16x16x32_bf16 v[88:91], a[252:255], a[196:199], v[88:91]\n\t"
"v_mfma_f32_16x16x32_bf16 v[88:91], v[236:239], a[200:203], v[88:91]\n\t"
"s_mov_b32 s20, 270336\n\t"
"buffer_load_dwordx4 v[96:99], v252, s[8:11], s20 offen offset:0 sc1\n\t"
"v_mfma_f32_16x16x32_bf16 v[92:95], a[248:251], a[196:199], v[92:95]\n\t"
"ds_read_b128 a[196:199], v244 offset:12800\n\t"
"v_mfma_f32_16x16x32_bf16 v[92:95], a[252:255], a[200:203], v[92:95]\n\t"
"ds_read_b128 a[200:203], v244 offset:13312\n\t"
"v_mfma_f32_16x16x32_bf16 v[92:95], v[236:239], a[204:207], v[92:95]\n\t"
"buffer_load_dwordx4 a[248:251], v252, s[8:11], s20 offen offset:1024 sc1\n\t"
"s_waitcnt lgkmcnt(5)\n\t"
"v_mfma_f32_16x16x32_bf16 v[228:231], v[228:231], v[220:223], 0\n\t"
"ds_read_b128 a[204:207], v244 offset:13824\n\t"
"s_waitcnt vmcnt(11)\n\t"
"v_mfma_f32_16x16x32_bf16 v[100:103], a[208:211], a[168:171], v[100:103]\n\t"
"v_mfma_f32_16x16x32_bf16 v[100:103], v[248:251], a[172:175], v[100:103]\n\t"
"v_mfma_f32_16x16x32_bf16 v[100:103], a[212:215], a[176:179], v[100:103]\n\t"
"buffer_load_dwordx4 v[236:239], v252, s[8:11], s20 offen offset:2048 sc1\n\t"
"v_mfma_f32_16x16x32_bf16 v[104:107], a[208:211], a[172:175], v[104:107]\n\t"
"v_mfma_f32_16x16x32_bf16 v[104:107], v[248:251], a[176:179], v[104:107]\n\t"
"v_cvt_pk_bf16_f32 v228, v228, v229\n\t"
"v_cvt_pk_bf16_f32 v229, v230, v231\n\t"
"v_mfma_f32_16x16x32_bf16 v[104:107], a[212:215], a[180:183], v[104:107]\n\t"
"s_mov_b32 s20, 282624\n\t"
"buffer_load_dwordx4 a[252:255], v252, s[8:11], s20 offen offset:0 sc1\n\t"
"s_waitcnt lgkmcnt(0)\n\t"
"s_barrier\n\t"
"v_mfma_f32_16x16x32_bf16 v[108:111], a[208:211], a[176:179], v[108:111]\n\t"
"v_mfma_f32_16x16x32_bf16 v[108:111], v[248:251], a[180:183], v[108:111]\n\t"
"v_mfma_f32_16x16x32_bf16 v[108:111], a[212:215], a[184:187], v[108:111]\n\t"
"ds_write2st64_b64 v245, v[224:225], v[226:227] offset0:0 offset1:1\n\t"
"v_mfma_f32_16x16x32_bf16 v[112:115], a[208:211], a[180:183], v[112:115]\n\t"
"ds_read_b64_tr_b16 v[224:225], v246 offset:32768\n\t"
"v_mfma_f32_16x16x32_bf16 v[112:115], v[248:251], a[184:187], v[112:115]\n\t"
"ds_read_b64_tr_b16 v[226:227], v246 offset:35840\n\t"
"v_mfma_f32_16x16x32_bf16 v[112:115], a[212:215], a[188:191], v[112:115]\n\t"
"ds_write2st64_b64 v245, v[232:233], v[234:235] offset0:2 offset1:3\n\t"
"v_mfma_f32_16x16x32_bf16 v[116:119], a[208:211], a[184:187], v[116:119]\n\t"
"ds_read_b64_tr_b16 v[232:233], v246 offset:40448\n\t"
"v_mfma_f32_16x16x32_bf16 v[116:119], v[248:251], a[188:191], v[116:119]\n\t"
"ds_read_b64_tr_b16 v[234:235], v246 offset:43520\n\t"
"v_mfma_f32_16x16x32_bf16 v[116:119], a[212:215], a[192:195], v[116:119]\n\t"
"ds_write_b64 v245, v[228:229] offset:2048\n\t"
"v_mfma_f32_16x16x32_bf16 v[120:123], a[208:211], a[188:191], v[120:123]\n\t"
"ds_read_b64_tr_b16 v[228:229], v246 offset:46592\n\t"
"v_mfma_f32_16x16x32_bf16 v[120:123], v[248:251], a[192:195], v[120:123]\n\t"
"ds_read_b64_tr_b16 v[230:231], v246 offset:49664\n\t"
"v_mfma_f32_16x16x32_bf16 v[120:123], a[212:215], a[196:199], v[120:123]\n\t"
"s_waitcnt lgkmcnt(6)\n\t"
"v_mfma_f32_16x16x32_bf16 v[224:227], v[224:227], v[220:223], 0\n\t"
"s_add_u32 m0, s24, 73088\n\t"
"s_add_u32 s28, s21, 2162688\n\t"
"buffer_load_dwordx4 v247, s[12:15], s28 offen offset:128 nt lds\n\t"
"v_mfma_f32_16x16x32_bf16 v[124:127], a[208:211], a[192:195], v[124:127]\n\t"
"v_mfma_f32_16x16x32_bf16 v[124:127], v[248:251], a[196:199], v[124:127]\n\t"
"v_mfma_f32_16x16x32_bf16 v[124:127], a[212:215], a[200:203], v[124:127]\n\t"
"s_add_u32 m0, s24, 80768\n\t"
"s_add_u32 s28, s21, 2359296\n\t"
"buffer_load_dwordx4 v247, s[12:15], s28 offen offset:128 nt lds\n\t"
"s_waitcnt lgkmcnt(3)\n\t"
"v_mfma_f32_16x16x32_bf16 v[232:235], v[232:235], v[220:223], 0\n\t"
"s_add_u32 m0, s24, 27008\n\t"
"s_add_u32 s28, s21, 786432\n\t"
"buffer_load_dwordx4 v247, s[12:15], s28 offen offset:128 nt lds\n\t"
"v_mfma_f32_16x16x32_bf16 v[128:131], a[208:211], a[196:199], v[128:131]\n\t"
"v_cvt_pk_bf16_f32 v224, v224, v225\n\t"
"v_cvt_pk_bf16_f32 v225, v226, v227\n\t"
"v_mfma_f32_16x16x32_bf16 v[128:131], v[248:251], a[200:203], v[128:131]\n\t"
"v_mfma_f32_16x16x32_bf16 v[128:131], a[212:215], a[204:207], v[128:131]\n\t"
"s_add_u32 m0, s24, 33152\n\t"
"s_add_u32 s28, s21, 983040\n\t"
"buffer_load_dwordx4 v247, s[12:15], s28 offen offset:128 nt lds\n\t"
"s_waitcnt lgkmcnt(0)\n\t"
"v_mfma_f32_16x16x32_bf16 v[228:231], v[228:231], v[220:223], 0\n\t"
"s_add_u32 m0, s24, 40832\n\t"
"s_add_u32 s28, s21, 1179648\n\t"
"buffer_load_dwordx4 v247, s[12:15], s28 offen offset:128 nt lds\n\t"
"s_waitcnt vmcnt(15)\n\t"
"v_mfma_f32_16x16x32_bf16 v[132:135], a[216:219], a[168:171], v[132:135]\n\t"
"v_cvt_pk_bf16_f32 v226, v232, v233\n\t"
"v_cvt_pk_bf16_f32 v227, v234, v235\n\t"
"v_mfma_f32_16x16x32_bf16 v[132:135], v[240:243], a[172:175], v[132:135]\n\t"
"ds_read_b64_tr_b16 v[232:233], v246 offset:52736\n\t"
"v_mfma_f32_16x16x32_bf16 v[132:135], a[220:223], a[176:179], v[132:135]\n\t"
"s_add_u32 m0, s24, 46976\n\t"
"s_add_u32 s28, s21, 1376256\n\t"
"buffer_load_dwordx4 v247, s[12:15], s28 offen offset:128 nt lds\n\t"
"v_mfma_f32_16x16x32_bf16 v[136:139], a[216:219], a[172:175], v[136:139]\n\t"
"ds_read_b64_tr_b16 v[234:235], v246 offset:55808\n\t"
"v_mfma_f32_16x16x32_bf16 v[136:139], v[240:243], a[176:179], v[136:139]\n\t"
"v_cvt_pk_bf16_f32 v228, v228, v229\n\t"
"v_cvt_pk_bf16_f32 v229, v230, v231\n\t"
"v_mfma_f32_16x16x32_bf16 v[136:139], a[220:223], a[180:183], v[136:139]\n\t"
"ds_write2st64_b64 v245, v[224:225], v[226:227] offset0:5 offset1:6\n\t"
"v_mfma_f32_16x16x32_bf16 v[140:143], a[216:219], a[176:179], v[140:143]\n\t"
"v_mfma_f32_16x16x32_bf16 v[140:143], v[240:243], a[180:183], v[140:143]\n\t"
"v_mfma_f32_16x16x32_bf16 v[140:143], a[220:223], a[184:187], v[140:143]\n\t"
"v_mfma_f32_16x16x32_bf16 v[144:147], a[216:219], a[180:183], v[144:147]\n\t"
"v_mfma_f32_16x16x32_bf16 v[144:147], v[240:243], a[184:187], v[144:147]\n\t"
"v_mfma_f32_16x16x32_bf16 v[144:147], a[220:223], a[188:191], v[144:147]\n\t"
"v_mfma_f32_16x16x32_bf16 v[148:151], a[216:219], a[184:187], v[148:151]\n\t"
"v_mfma_f32_16x16x32_bf16 v[148:151], v[240:243], a[188:191], v[148:151]\n\t"
"v_mfma_f32_16x16x32_bf16 v[148:151], a[220:223], a[192:195], v[148:151]\n\t"
"s_waitcnt lgkmcnt(1)\n\t"
"v_mfma_f32_16x16x32_bf16 v[232:235], v[232:235], v[220:223], 0\n\t"
"v_mfma_f32_16x16x32_bf16 v[152:155], a[216:219], a[188:191], v[152:155]\n\t"
"v_mfma_f32_16x16x32_bf16 v[152:155], v[240:243], a[192:195], v[152:155]\n\t"
"v_mfma_f32_16x16x32_bf16 v[152:155], a[220:223], a[196:199], v[152:155]\n\t"
"v_mfma_f32_16x16x32_bf16 v[156:159], a[216:219], a[192:195], v[156:159]\n\t"
"v_mfma_f32_16x16x32_bf16 v[156:159], v[240:243], a[196:199], v[156:159]\n\t"
"s_nop 2\n\t"
"v_cvt_pk_bf16_f32 v230, v232, v233\n\t"
"v_cvt_pk_bf16_f32 v231, v234, v235\n\t"
"v_mfma_f32_16x16x32_bf16 v[156:159], a[220:223], a[200:203], v[156:159]\n\t"
"ds_write2st64_b64 v245, v[228:229], v[230:231] offset0:7 offset1:8\n\t"
"v_mfma_f32_16x16x32_bf16 v[160:163], a[216:219], a[196:199], v[160:163]\n\t"
"v_mfma_f32_16x16x32_bf16 v[160:163], v[240:243], a[200:203], v[160:163]\n\t"
"v_mfma_f32_16x16x32_bf16 v[160:163], a[220:223], a[204:207], v[160:163]\n\t"
"s_waitcnt vmcnt(13)\n\t"
"v_mfma_f32_16x16x32_bf16 v[164:167], a[224:227], a[168:171], v[164:167]\n\t"
"v_mfma_f32_16x16x32_bf16 v[164:167], a[228:231], a[172:175], v[164:167]\n\t"
"v_mfma_f32_16x16x32_bf16 v[164:167], a[232:235], a[176:179], v[164:167]\n\t"
"buffer_load_dwordx4 v[240:243], v252, s[8:11], s20 offen offset:1024 sc1\n\t"
"s_waitcnt lgkmcnt(0)\n\t"
"s_barrier\n\t"
"v_mfma_f32_16x16x32_bf16 v[168:171], a[224:227], a[172:175], v[168:171]\n\t"
"ds_read_b128 a[168:171], v244 offset:0\n\t"
"v_mfma_f32_16x16x32_bf16 v[168:171], a[228:231], a[176:179], v[168:171]\n\t"
"ds_read_b128 a[172:175], v244 offset:512\n\t"
"v_mfma_f32_16x16x32_bf16 v[168:171], a[232:235], a[180:183], v[168:171]\n\t"
"v_mfma_f32_16x16x32_bf16 v[172:175], a[224:227], a[176:179], v[172:175]\n\t"
"ds_read_b128 a[176:179], v244 offset:1024\n\t"
"v_mfma_f32_16x16x32_bf16 v[172:175], a[228:231], a[180:183], v[172:175]\n\t"
"v_mfma_f32_16x16x32_bf16 v[172:175], a[232:235], a[184:187], v[172:175]\n\t"
"v_mfma_f32_16x16x32_bf16 v[176:179], a[224:227], a[180:183], v[176:179]\n\t"
"ds_read_b128 a[180:183], v244 offset:1536\n\t"
"v_mfma_f32_16x16x32_bf16 v[176:179], a[228:231], a[184:187], v[176:179]\n\t"
"v_mfma_f32_16x16x32_bf16 v[176:179], a[232:235], a[188:191], v[176:179]\n\t"
"v_mfma_f32_16x16x32_bf16 v[180:183], a[224:227], a[184:187], v[180:183]\n\t"
"ds_read_b128 a[184:187], v244 offset:2048\n\t"
"v_mfma_f32_16x16x32_bf16 v[180:183], a[228:231], a[188:191], v[180:183]\n\t"
"v_mfma_f32_16x16x32_bf16 v[180:183], a[232:235], a[192:195], v[180:183]\n\t"
"v_mfma_f32_16x16x32_bf16 v[184:187], a[224:227], a[188:191], v[184:187]\n\t"
"ds_read_b128 a[188:191], v244 offset:2560\n\t"
"v_mfma_f32_16x16x32_bf16 v[184:187], a[228:231], a[192:195], v[184:187]\n\t"
"v_mfma_f32_16x16x32_bf16 v[184:187], a[232:235], a[196:199], v[184:187]\n\t"
"v_mfma_f32_16x16x32_bf16 v[188:191], a[224:227], a[192:195], v[188:191]\n\t"
"ds_read_b128 a[192:195], v244 offset:3072\n\t"
"v_mfma_f32_16x16x32_bf16 v[188:191], a[228:231], a[196:199], v[188:191]\n\t"
"v_mfma_f32_16x16x32_bf16 v[188:191], a[232:235], a[200:203], v[188:191]\n\t"
"v_mfma_f32_16x16x32_bf16 v[192:195], a[224:227], a[196:199], v[192:195]\n\t"
"ds_read_b128 a[196:199], v244 offset:3584\n\t"
"v_mfma_f32_16x16x32_bf16 v[192:195], a[228:231], a[200:203], v[192:195]\n\t"
"ds_read_b128 a[200:203], v244 offset:4096\n\t"
"v_mfma_f32_16x16x32_bf16 v[192:195], a[232:235], a[204:207], v[192:195]\n\t"
"Lst4_%=:\n\t"
"s_waitcnt vmcnt(11) lgkmcnt(6)\n\t"
"v_mfma_f32_16x16x32_bf16 v[0:3], a[236:239], a[168:171], v[0:3]\n\t"
"ds_read_b128 a[204:207], v244 offset:4608\n\t"
"v_mfma_f32_16x16x32_bf16 v[0:3], a[240:243], a[172:175], v[0:3]\n\t"
"v_xor_b32 v246, 0xc0, v246\n\t"
"v_mfma_f32_16x16x32_bf16 v[0:3], a[244:247], a[176:179], v[0:3]\n\t"
"s_waitcnt lgkmcnt(6)\n\t"
"v_mfma_f32_16x16x32_bf16 v[4:7], a[236:239], a[172:175], v[4:7]\n\t"
"v_mfma_f32_16x16x32_bf16 v[4:7], a[240:243], a[176:179], v[4:7]\n\t"
"v_mfma_f32_16x16x32_bf16 v[4:7], a[244:247], a[180:183], v[4:7]\n\t"
"s_waitcnt lgkmcnt(5)\n\t"
"v_mfma_f32_16x16x32_bf16 v[8:11], a[236:239], a[176:179], v[8:11]\n\t"
"v_mfma_f32_16x16x32_bf16 v[8:11], a[240:243], a[180:183], v[8:11]\n\t"
"v_mfma_f32_16x16x32_bf16 v[8:11], a[244:247], a[184:187], v[8:11]\n\t"
"s_waitcnt lgkmcnt(4)\n\t"
"v_mfma_f32_16x16x32_bf16 v[12:15], a[236:239], a[180:183], v[12:15]\n\t"
"v_mfma_f32_16x16x32_bf16 v[12:15], a[240:243], a[184:187], v[12:15]\n\t"
"v_mfma_f32_16x16x32_bf16 v[12:15], a[244:247], a[188:191], v[12:15]\n\t"
"buffer_load_dwordx4 a[208:211], v252, s[8:11], s20 offen offset:2048 sc1\n\t"
"s_waitcnt lgkmcnt(3)\n\t"
"v_mfma_f32_16x16x32_bf16 v[16:19], a[236:239], a[184:187], v[16:19]\n\t"
"v_mfma_f32_16x16x32_bf16 v[16:19], a[240:243], a[188:191], v[16:19]\n\t"
"v_mfma_f32_16x16x32_bf16 v[16:19], a[244:247], a[192:195], v[16:19]\n\t"
"ds_read_b64_tr_b16 v[224:225], v246 offset:512\n\t"
"ds_read_b64_tr_b16 v[226:227], v246 offset:3584\n\t"
"s_waitcnt lgkmcnt(4)\n\t"
"v_mfma_f32_16x16x32_bf16 v[20:23], a[236:239], a[188:191], v[20:23]\n\t"
"v_mfma_f32_16x16x32_bf16 v[20:23], a[240:243], a[192:195], v[20:23]\n\t"
"v_mfma_f32_16x16x32_bf16 v[20:23], a[244:247], a[196:199], v[20:23]\n\t"
"s_mov_b32 s20, 294912\n\t"
"buffer_load_dwordx4 v[248:251], v252, s[8:11], s20 offen offset:0 sc1\n\t"
"s_waitcnt lgkmcnt(3)\n\t"
"v_mfma_f32_16x16x32_bf16 v[24:27], a[236:239], a[192:195], v[24:27]\n\t"
"v_mfma_f32_16x16x32_bf16 v[24:27], a[240:243], a[196:199], v[24:27]\n\t"
"v_mfma_f32_16x16x32_bf16 v[24:27], a[244:247], a[200:203], v[24:27]\n\t"
"buffer_load_dwordx4 a[212:215], v252, s[8:11], s20 offen offset:1024 sc1\n\t"
"s_waitcnt lgkmcnt(2)\n\t"
"v_mfma_f32_16x16x32_bf16 v[28:31], a[236:239], a[196:199], v[28:31]\n\t"
"ds_read_b64_tr_b16 v[232:233], v246 offset:6656\n\t"
"v_mfma_f32_16x16x32_bf16 v[28:31], a[240:243], a[200:203], v[28:31]\n\t"
"ds_read_b64_tr_b16 v[234:235], v246 offset:9728\n\t"
"v_mfma_f32_16x16x32_bf16 v[28:31], a[244:247], a[204:207], v[28:31]\n\t"
"s_waitcnt vmcnt(11)\n\t"
"v_mfma_f32_16x16x32_bf16 v[32:35], v[96:99], a[168:171], v[32:35]\n\t"
"v_mfma_f32_16x16x32_bf16 v[32:35], a[248:251], a[172:175], v[32:35]\n\t"
"v_mfma_f32_16x16x32_bf16 v[32:35], v[236:239], a[176:179], v[32:35]\n\t"
"buffer_load_dwordx4 a[216:219], v252, s[8:11], s20 offen offset:2048 sc1\n\t"
"v_mfma_f32_16x16x32_bf16 v[36:39], v[96:99], a[172:175], v[36:39]\n\t"
"v_mfma_f32_16x16x32_bf16 v[36:39], a[248:251], a[176:179], v[36:39]\n\t"
"v_mfma_f32_16x16x32_bf16 v[36:39], v[236:239], a[180:183], v[36:39]\n\t"
"s_mov_b32 s20, 307200\n\t"
"buffer_load_dwordx4 a[220:223], v252, s[8:11], s20 offen offset:0 sc1\n\t"
"s_waitcnt lgkmcnt(2)\n\t"
"v_mfma_f32_16x16x32_bf16 v[224:227], v[224:227], v[220:223], 0\n\t"
"buffer_load_dwordx4 a[224:227], v252, s[8:11], s20 offen offset:1024 sc1\n\t"
"v_mfma_f32_16x16x32_bf16 v[40:43], v[96:99], a[176:179], v[40:43]\n\t"
"ds_read_b64_tr_b16 v[228:229], v246 offset:12800\n\t"
"v_mfma_f32_16x16x32_bf16 v[40:43], a[248:251], a[180:183], v[40:43]\n\t"
"v_mfma_f32_16x16x32_bf16 v[40:43], v[236:239], a[184:187], v[40:43]\n\t"
"ds_read_b64_tr_b16 v[230:231], v246 offset:15872\n\t"
"v_mfma_f32_16x16x32_bf16 v[44:47], v[96:99], a[180:183], v[44:47]\n\t"
"v_mfma_f32_16x16x32_bf16 v[44:47], a[248:251], a[184:187], v[44:47]\n\t"
"v_cvt_pk_bf16_f32 v224, v224, v225\n\t"
"v_cvt_pk_bf16_f32 v225, v226, v227\n\t"
"v_mfma_f32_16x16x32_bf16 v[44:47], v[236:239], a[188:191], v[44:47]\n\t"
"s_waitcnt lgkmcnt(2)\n\t"
"v_mfma_f32_16x16x32_bf16 v[232:235], v[232:235], v[220:223], 0\n\t"
"buffer_load_dwordx4 a[228:231], v252, s[8:11], s20 offen offset:2048 sc1\n\t"
"v_mfma_f32_16x16x32_bf16 v[48:51], v[96:99], a[184:187], v[48:51]\n\t"
"v_mfma_f32_16x16x32_bf16 v[48:51], a[248:251], a[188:191], v[48:51]\n\t"
"v_mfma_f32_16x16x32_bf16 v[48:51], v[236:239], a[192:195], v[48:51]\n\t"
"s_mov_b32 s20, 319488\n\t"
"buffer_load_dwordx4 a[232:235], v252, s[8:11], s20 offen offset:0 sc1\n\t"
"v_mfma_f32_16x16x32_bf16 v[52:55], v[96:99], a[188:191], v[52:55]\n\t"
"v_mfma_f32_16x16x32_bf16 v[52:55], a[248:251], a[192:195], v[52:55]\n\t"
"v_cvt_pk_bf16_f32 v226, v232, v233\n\t"
"v_cvt_pk_bf16_f32 v227, v234, v235\n\t"
"v_mfma_f32_16x16x32_bf16 v[52:55], v[236:239], a[196:199], v[52:55]\n\t"
"buffer_load_dwordx4 a[236:239], v252, s[8:11], s20 offen offset:1024 sc1\n\t"
"v_mfma_f32_16x16x32_bf16 v[56:59], v[96:99], a[192:195], v[56:59]\n\t"
"ds_read_b64_tr_b16 v[232:233], v246 offset:20480\n\t"
"v_mfma_f32_16x16x32_bf16 v[56:59], a[248:251], a[196:199], v[56:59]\n\t"
"ds_read_b64_tr_b16 v[234:235], v246 offset:23552\n\t"
"v_mfma_f32_16x16x32_bf16 v[56:59], v[236:239], a[200:203], v[56:59]\n\t"
"s_waitcnt lgkmcnt(2)\n\t"
"v_mfma_f32_16x16x32_bf16 v[228:231], v[228:231], v[220:223], 0\n\t"
"buffer_load_dwordx4 a[240:243], v252, s[8:11], s20 offen offset:2048 sc1\n\t"
"v_mfma_f32_16x16x32_bf16 v[60:63], v[96:99], a[196:199], v[60:63]\n\t"
"v_mfma_f32_16x16x32_bf16 v[60:63], a[248:251], a[200:203], v[60:63]\n\t"
"v_mfma_f32_16x16x32_bf16 v[60:63], v[236:239], a[204:207], v[60:63]\n\t"
"s_mov_b32 s20, 331776\n\t"
"buffer_load_dwordx4 v[236:239], v252, s[8:11], s20 offen offset:0 sc1\n\t"
"s_waitcnt vmcnt(10)\n\t"
"v_mfma_f32_16x16x32_bf16 v[64:67], a[252:255], a[168:171], v[64:67]\n\t"
"ds_read_b128 a[168:171], v244 offset:9216\n\t"
"v_mfma_f32_16x16x32_bf16 v[64:67], v[240:243], a[172:175], v[64:67]\n\t"
"v_cvt_pk_bf16_f32 v228, v228, v229\n\t"
"v_mfma_f32_16x16x32_bf16 v[64:67], a[208:211], a[176:179], v[64:67]\n\t"
"v_cvt_pk_bf16_f32 v229, v230, v231\n\t"
"v_mfma_f32_16x16x32_bf16 v[68:71], a[252:255], a[172:175], v[68:71]\n\t"
"ds_read_b128 a[172:175], v244 offset:9728\n\t"
"v_mfma_f32_16x16x32_bf16 v[68:71], v[240:243], a[176:179], v[68:71]\n\t"
"v_mfma_f32_16x16x32_bf16 v[68:71], a[208:211], a[180:183], v[68:71]\n\t"
"buffer_load_dwordx4 v[96:99], v252, s[8:11], s20 offen offset:1024 sc1\n\t"
"s_waitcnt lgkmcnt(2)\n\t"
"v_mfma_f32_16x16x32_bf16 v[232:235], v[232:235], v[220:223], 0\n\t"
"v_mfma_f32_16x16x32_bf16 v[72:75], a[252:255], a[176:179], v[72:75]\n\t"
"ds_read_b128 a[176:179], v244 offset:10240\n\t"
"v_mfma_f32_16x16x32_bf16 v[72:75], v[240:243], a[180:183], v[72:75]\n\t"
"v_mfma_f32_16x16x32_bf16 v[72:75], a[208:211], a[184:187], v[72:75]\n\t"
"buffer_load_dwordx4 a[244:247], v252, s[8:11], s20 offen offset:2048 sc1\n\t"
"v_mfma_f32_16x16x32_bf16 v[76:79], a[252:255], a[180:183], v[76:79]\n\t"
"ds_read_b128 a[180:183], v244 offset:10752\n\t"
"v_mfma_f32_16x16x32_bf16 v[76:79], v[240:243], a[184:187], v[76:79]\n\t"
"v_cvt_pk_bf16_f32 v230, v232, v233\n\t"
"v_cvt_pk_bf16_f32 v231, v234, v235\n\t"
"v_mfma_f32_16x16x32_bf16 v[76:79], a[208:211], a[188:191], v[76:79]\n\t"
"s_mov_b32 s20, 344064\n\t"
"buffer_load_dwordx4 a[248:251], v252, s[8:11], s20 offen offset:0 sc1\n\t"
"v_mfma_f32_16x16x32_bf16 v[80:83], a[252:255], a[184:187], v[80:83]\n\t"
"ds_read_b64_tr_b16 v[232:233], v246 offset:26624\n\t"
"v_mfma_f32_16x16x32_bf16 v[80:83], v[240:243], a[188:191], v[80:83]\n\t"
"ds_read_b64_tr_b16 v[234:235], v246 offset:29696\n\t"
"v_mfma_f32_16x16x32_bf16 v[80:83], a[208:211], a[192:195], v[80:83]\n\t"
"ds_read_b128 a[184:187], v244 offset:11264\n\t"
"v_mfma_f32_16x16x32_bf16 v[84:87], a[252:255], a[188:191], v[84:87]\n\t"
"ds_read_b128 a[188:191], v244 offset:11776\n\t"
"v_mfma_f32_16x16x32_bf16 v[84:87], v[240:243], a[192:195], v[84:87]\n\t"
"v_mfma_f32_16x16x32_bf16 v[84:87], a[208:211], a[196:199], v[84:87]\n\t"
"v_mfma_f32_16x16x32_bf16 v[88:91], a[252:255], a[192:195], v[88:91]\n\t"
"ds_read_b128 a[192:195], v244 offset:12288\n\t"
"v_mfma_f32_16x16x32_bf16 v[88:91], v[240:243], a[196:199], v[88:91]\n\t"
"v_mfma_f32_16x16x32_bf16 v[88:91], a[208:211], a[200:203], v[88:91]\n\t"
"v_mfma_f32_16x16x32_bf16 v[92:95], a[252:255], a[196:199], v[92:95]\n\t"
"ds_read_b128 a[196:199], v244 offset:12800\n\t"
"v_mfma_f32_16x16x32_bf16 v[92:95], v[240:243], a[200:203], v[92:95]\n\t"
"ds_read_b128 a[200:203], v244 offset:13312\n\t"
"v_mfma_f32_16x16x32_bf16 v[92:95], a[208:211], a[204:207], v[92:95]\n\t"
"buffer_load_dwordx4 a[252:255], v252, s[8:11], s20 offen offset:1024 sc1\n\t"
"s_waitcnt lgkmcnt(5)\n\t"
"v_mfma_f32_16x16x32_bf16 v[232:235], v[232:235], v[220:223], 0\n\t"
"ds_read_b128 a[204:207], v244 offset:13824\n\t"
"s_waitcnt vmcnt(11)\n\t"
"v_mfma_f32_16x16x32_bf16 v[100:103], v[248:251], a[168:171], v[100:103]\n\t"
"v_mfma_f32_16x16x32_bf16 v[100:103], a[212:215], a[172:175], v[100:103]\n\t"
"v_mfma_f32_16x16x32_bf16 v[100:103], a[216:219], a[176:179], v[100:103]\n\t"
"buffer_load_dwordx4 v[240:243], v252, s[8:11], s20 offen offset:2048 sc1\n\t"
"v_mfma_f32_16x16x32_bf16 v[104:107], v[248:251], a[172:175], v[104:107]\n\t"
"v_mfma_f32_16x16x32_bf16 v[104:107], a[212:215], a[176:179], v[104:107]\n\t"
"v_cvt_pk_bf16_f32 v232, v232, v233\n\t"
"v_cvt_pk_bf16_f32 v233, v234, v235\n\t"
"v_mfma_f32_16x16x32_bf16 v[104:107], a[216:219], a[180:183], v[104:107]\n\t"
"s_mov_b32 s20, 356352\n\t"
"buffer_load_dwordx4 a[208:211], v252, s[8:11], s20 offen offset:0 sc1\n\t"
"s_waitcnt lgkmcnt(0)\n\t"
"s_barrier\n\t"
"v_mfma_f32_16x16x32_bf16 v[108:111], v[248:251], a[176:179], v[108:111]\n\t"
"v_mfma_f32_16x16x32_bf16 v[108:111], a[212:215], a[180:183], v[108:111]\n\t"
"v_mfma_f32_16x16x32_bf16 v[108:111], a[216:219], a[184:187], v[108:111]\n\t"
"ds_write2st64_b64 v245, v[224:225], v[226:227] offset0:0 offset1:1\n\t"
"v_mfma_f32_16x16x32_bf16 v[112:115], v[248:251], a[180:183], v[112:115]\n\t"
"ds_read_b64_tr_b16 v[224:225], v246 offset:32768\n\t"
"v_mfma_f32_16x16x32_bf16 v[112:115], a[212:215], a[184:187], v[112:115]\n\t"
"ds_read_b64_tr_b16 v[226:227], v246 offset:35840\n\t"
"v_mfma_f32_16x16x32_bf16 v[112:115], a[216:219], a[188:191], v[112:115]\n\t"
"ds_write2st64_b64 v245, v[228:229], v[230:231] offset0:2 offset1:3\n\t"
"v_mfma_f32_16x16x32_bf16 v[116:119], v[248:251], a[184:187], v[116:119]\n\t"
"ds_read_b64_tr_b16 v[228:229], v246 offset:40448\n\t"
"v_mfma_f32_16x16x32_bf16 v[116:119], a[212:215], a[188:191], v[116:119]\n\t"
"ds_read_b64_tr_b16 v[230:231], v246 offset:43520\n\t"
"v_mfma_f32_16x16x32_bf16 v[116:119], a[216:219], a[192:195], v[116:119]\n\t"
"ds_write_b64 v245, v[232:233] offset:2048\n\t"
"v_mfma_f32_16x16x32_bf16 v[120:123], v[248:251], a[188:191], v[120:123]\n\t"
"ds_read_b64_tr_b16 v[232:233], v246 offset:46592\n\t"
"v_mfma_f32_16x16x32_bf16 v[120:123], a[212:215], a[192:195], v[120:123]\n\t"
"ds_read_b64_tr_b16 v[234:235], v246 offset:49664\n\t"
"v_mfma_f32_16x16x32_bf16 v[120:123], a[216:219], a[196:199], v[120:123]\n\t"
"s_waitcnt lgkmcnt(6)\n\t"
"v_mfma_f32_16x16x32_bf16 v[224:227], v[224:227], v[220:223], 0\n\t"
"v_mfma_f32_16x16x32_bf16 v[124:127], v[248:251], a[192:195], v[124:127]\n\t"
"v_mfma_f32_16x16x32_bf16 v[124:127], a[212:215], a[196:199], v[124:127]\n\t"
"v_mfma_f32_16x16x32_bf16 v[124:127], a[216:219], a[200:203], v[124:127]\n\t"
"s_waitcnt lgkmcnt(3)\n\t"
"v_mfma_f32_16x16x32_bf16 v[228:231], v[228:231], v[220:223], 0\n\t"
"v_mfma_f32_16x16x32_bf16 v[128:131], v[248:251], a[196:199], v[128:131]\n\t"
"s_nop 1\n\t"
"v_cvt_pk_bf16_f32 v224, v224, v225\n\t"
"v_cvt_pk_bf16_f32 v225, v226, v227\n\t"
"v_mfma_f32_16x16x32_bf16 v[128:131], a[212:215], a[200:203], v[128:131]\n\t"
"v_mfma_f32_16x16x32_bf16 v[128:131], a[216:219], a[204:207], v[128:131]\n\t"
"buffer_load_dwordx4 v[248:251], v252, s[8:11], s20 offen offset:1024 sc1\n\t"
"s_waitcnt lgkmcnt(0)\n\t"
"v_mfma_f32_16x16x32_bf16 v[232:235], v[232:235], v[220:223], 0\n\t"
"buffer_load_dwordx4 a[212:215], v252, s[8:11], s20 offen offset:2048 sc1\n\t"
"s_waitcnt vmcnt(12)\n\t"
"v_mfma_f32_16x16x32_bf16 v[132:135], a[220:223], a[168:171], v[132:135]\n\t"
"v_cvt_pk_bf16_f32 v226, v228, v229\n\t"
"v_cvt_pk_bf16_f32 v227, v230, v231\n\t"
"v_mfma_f32_16x16x32_bf16 v[132:135], a[224:227], a[172:175], v[132:135]\n\t"
"ds_read_b64_tr_b16 v[228:229], v246 offset:52736\n\t"
"v_mfma_f32_16x16x32_bf16 v[132:135], a[228:231], a[176:179], v[132:135]\n\t"
"s_mov_b32 s20, 368640\n\t"
"buffer_load_dwordx4 a[216:219], v252, s[8:11], s20 offen offset:0 sc1\n\t"
"v_mfma_f32_16x16x32_bf16 v[136:139], a[220:223], a[172:175], v[136:139]\n\t"
"ds_read_b64_tr_b16 v[230:231], v246 offset:55808\n\t"
"v_mfma_f32_16x16x32_bf16 v[136:139], a[224:227], a[176:179], v[136:139]\n\t"
"v_cvt_pk_bf16_f32 v232, v232, v233\n\t"
"v_cvt_pk_bf16_f32 v233, v234, v235\n\t"
"v_mfma_f32_16x16x32_bf16 v[136:139], a[228:231], a[180:183], v[136:139]\n\t"
"s_add_u32 m0, s24, 53120\n\t"
"s_add_u32 s28, s21, 1572864\n\t"
"buffer_load_dwordx4 v247, s[12:15], s28 offen offset:128 nt lds\n\t"
"s_add_u32 m0, s24, 86912\n\t"
"s_add_u32 s28, s21, 2555904\n\t"
"buffer_load_dwordx4 v247, s[12:15], s28 offen offset:128 nt lds\n\t"
"v_mfma_f32_16x16x32_bf16 v[140:143], a[220:223], a[176:179], v[140:143]\n\t"
"v_mfma_f32_16x16x32_bf16 v[140:143], a[224:227], a[180:183], v[140:143]\n\t"
"v_mfma_f32_16x16x32_bf16 v[140:143], a[228:231], a[184:187], v[140:143]\n\t"
"ds_write2st64_b64 v245, v[224:225], v[226:227] offset0:5 offset1:6\n\t"
"s_add_u32 m0, s24, 93056\n\t"
"s_add_u32 s28, s21, 2752512\n\t"
"buffer_load_dwordx4 v247, s[12:15], s28 offen offset:128 nt lds\n\t"
"v_mfma_f32_16x16x32_bf16 v[144:147], a[220:223], a[180:183], v[144:147]\n\t"
"v_mfma_f32_16x16x32_bf16 v[144:147], a[224:227], a[184:187], v[144:147]\n\t"
"v_mfma_f32_16x16x32_bf16 v[144:147], a[228:231], a[188:191], v[144:147]\n\t"
"s_add_u32 m0, s24, 100736\n\t"
"s_add_u32 s28, s21, 2949120\n\t"
"buffer_load_dwordx4 v247, s[12:15], s28 offen offset:128 nt lds\n\t"
"s_add_u32 m0, s24, 106880\n\t"
"s_add_u32 s28, s21, 3145728\n\t"
"buffer_load_dwordx4 v247, s[12:15], s28 offen offset:128 nt lds\n\t"
"v_mfma_f32_16x16x32_bf16 v[148:151], a[220:223], a[184:187], v[148:151]\n\t"
"v_mfma_f32_16x16x32_bf16 v[148:151], a[224:227], a[188:191], v[148:151]\n\t"
"v_mfma_f32_16x16x32_bf16 v[148:151], a[228:231], a[192:195], v[148:151]\n\t"
"s_add_u32 m0, s24, 113024\n\t"
"s_add_u32 s28, s21, 3342336\n\t"
"buffer_load_dwordx4 v247, s[12:15], s28 offen offset:128 nt lds\n\t"
"s_waitcnt lgkmcnt(1)\n\t"
"v_mfma_f32_16x16x32_bf16 v[228:231], v[228:231], v[220:223], 0\n\t"
"s_add_u32 m0, s25, 896\n\t"
"s_nop 0\n\t"
"buffer_load_dwordx4 v254, s[12:15], 0 offen offset:128 nt lds\n\t"
"v_mfma_f32_16x16x32_bf16 v[152:155], a[220:223], a[188:191], v[152:155]\n\t"
"v_mfma_f32_16x16x32_bf16 v[152:155], a[224:227], a[192:195], v[152:155]\n\t"
"v_mfma_f32_16x16x32_bf16 v[152:155], a[228:231], a[196:199], v[152:155]\n\t"
"s_add_u32 m0, s26, 896\n\t"
"s_mov_b64 exec, s[36:37]\n\t"
"buffer_load_dwordx4 v254, s[12:15], s27 offen offset:128 nt lds\n\t"
"s_mov_b64 exec, -1\n\t"
"v_mfma_f32_16x16x32_bf16 v[156:159], a[220:223], a[192:195], v[156:159]\n\t"
"v_mfma_f32_16x16x32_bf16 v[156:159], a[224:227], a[196:199], v[156:159]\n\t"
"v_cvt_pk_bf16_f32 v234, v228, v229\n\t"
"v_cvt_pk_bf16_f32 v235, v230, v231\n\t"
"v_mfma_f32_16x16x32_bf16 v[156:159], a[228:231], a[200:203], v[156:159]\n\t"
"ds_write2st64_b64 v245, v[232:233], v[234:235] offset0:7 offset1:8\n\t"
"v_mfma_f32_16x16x32_bf16 v[160:163], a[220:223], a[196:199], v[160:163]\n\t"
"v_mfma_f32_16x16x32_bf16 v[160:163], a[224:227], a[200:203], v[160:163]\n\t"
"v_mfma_f32_16x16x32_bf16 v[160:163], a[228:231], a[204:207], v[160:163]\n\t"
"s_waitcnt vmcnt(18)\n\t"
"v_mfma_f32_16x16x32_bf16 v[164:167], a[232:235], a[168:171], v[164:167]\n\t"
"v_mfma_f32_16x16x32_bf16 v[164:167], a[236:239], a[172:175], v[164:167]\n\t"
"v_mfma_f32_16x16x32_bf16 v[164:167], a[240:243], a[176:179], v[164:167]\n\t"
"s_waitcnt lgkmcnt(0)\n\t"
"s_barrier\n\t"
"v_mfma_f32_16x16x32_bf16 v[168:171], a[232:235], a[172:175], v[168:171]\n\t"
"ds_read_b128 a[168:171], v244 offset:0\n\t"
"v_mfma_f32_16x16x32_bf16 v[168:171], a[236:239], a[176:179], v[168:171]\n\t"
"ds_read_b128 a[172:175], v244 offset:512\n\t"
"v_mfma_f32_16x16x32_bf16 v[168:171], a[240:243], a[180:183], v[168:171]\n\t"
"v_mfma_f32_16x16x32_bf16 v[172:175], a[232:235], a[176:179], v[172:175]\n\t"
"ds_read_b128 a[176:179], v244 offset:1024\n\t"
"v_mfma_f32_16x16x32_bf16 v[172:175], a[236:239], a[180:183], v[172:175]\n\t"
"v_mfma_f32_16x16x32_bf16 v[172:175], a[240:243], a[184:187], v[172:175]\n\t"
"v_mfma_f32_16x16x32_bf16 v[176:179], a[232:235], a[180:183], v[176:179]\n\t"
"ds_read_b128 a[180:183], v244 offset:1536\n\t"
"v_mfma_f32_16x16x32_bf16 v[176:179], a[236:239], a[184:187], v[176:179]\n\t"
"v_mfma_f32_16x16x32_bf16 v[176:179], a[240:243], a[188:191], v[176:179]\n\t"
"v_mfma_f32_16x16x32_bf16 v[180:183], a[232:235], a[184:187], v[180:183]\n\t"
"ds_read_b128 a[184:187], v244 offset:2048\n\t"
"v_mfma_f32_16x16x32_bf16 v[180:183], a[236:239], a[188:191], v[180:183]\n\t"
"v_mfma_f32_16x16x32_bf16 v[180:183], a[240:243], a[192:195], v[180:183]\n\t"
"v_mfma_f32_16x16x32_bf16 v[184:187], a[232:235], a[188:191], v[184:187]\n\t"
"ds_read_b128 a[188:191], v244 offset:2560\n\t"
"v_mfma_f32_16x16x32_bf16 v[184:187], a[236:239], a[192:195], v[184:187]\n\t"
"v_mfma_f32_16x16x32_bf16 v[184:187], a[240:243], a[196:199], v[184:187]\n\t"
"v_mfma_f32_16x16x32_bf16 v[188:191], a[232:235], a[192:195], v[188:191]\n\t"
"ds_read_b128 a[192:195], v244 offset:3072\n\t"
"v_mfma_f32_16x16x32_bf16 v[188:191], a[236:239], a[196:199], v[188:191]\n\t"
"v_mfma_f32_16x16x32_bf16 v[188:191], a[240:243], a[200:203], v[188:191]\n\t"
"v_mfma_f32_16x16x32_bf16 v[192:195], a[232:235], a[196:199], v[192:195]\n\t"
"ds_read_b128 a[196:199], v244 offset:3584\n\t"
"v_mfma_f32_16x16x32_bf16 v[192:195], a[236:239], a[200:203], v[192:195]\n\t"
"ds_read_b128 a[200:203], v244 offset:4096\n\t"
"v_mfma_f32_16x16x32_bf16 v[192:195], a[240:243], a[204:207], v[192:195]\n\t"
"Lst5_%=:\n\t"
"s_waitcnt vmcnt(15) lgkmcnt(6)\n\t"
"v_mfma_f32_16x16x32_bf16 v[196:199], v[236:239], a[168:171], v[196:199]\n\t"
"ds_read_b128 a[204:207], v244 offset:4608\n\t"
"v_mfma_f32_16x16x32_bf16 v[196:199], v[96:99], a[172:175], v[196:199]\n\t"
"v_mfma_f32_16x16x32_bf16 v[196:199], a[244:247], a[176:179], v[196:199]\n\t"
"s_waitcnt lgkmcnt(6)\n\t"
"v_mfma_f32_16x16x32_bf16 v[204:207], v[236:239], a[172:175], v[204:207]\n\t"
"v_mfma_f32_16x16x32_bf16 v[204:207], v[96:99], a[176:179], v[204:207]\n\t"
"v_mfma_f32_16x16x32_bf16 v[204:207], a[244:247], a[180:183], v[204:207]\n\t"
"s_waitcnt lgkmcnt(5)\n\t"
"v_mfma_f32_16x16x32_bf16 v[212:215], v[236:239], a[176:179], v[212:215]\n\t"
"v_mfma_f32_16x16x32_bf16 v[212:215], v[96:99], a[180:183], v[212:215]\n\t"
"v_mfma_f32_16x16x32_bf16 v[212:215], a[244:247], a[184:187], v[212:215]\n\t"
"s_waitcnt lgkmcnt(4)\n\t"
"v_mfma_f32_16x16x32_bf16 a[0:3], v[236:239], a[180:183], a[0:3]\n\t"
"v_mfma_f32_16x16x32_bf16 a[0:3], v[96:99], a[184:187], a[0:3]\n\t"
"v_mfma_f32_16x16x32_bf16 a[0:3], a[244:247], a[188:191], a[0:3]\n\t"
"buffer_load_dwordx4 a[220:223], v252, s[8:11], s20 offen offset:1024 sc1\n\t"
"s_waitcnt lgkmcnt(3)\n\t"
"v_mfma_f32_16x16x32_bf16 a[8:11], v[236:239], a[184:187], a[8:11]\n\t"
"v_mfma_f32_16x16x32_bf16 a[8:11], v[96:99], a[188:191], a[8:11]\n\t"
"v_mfma_f32_16x16x32_bf16 a[8:11], a[244:247], a[192:195], a[8:11]\n\t"
"s_waitcnt lgkmcnt(2)\n\t"
"v_mfma_f32_16x16x32_bf16 a[16:19], v[236:239], a[188:191], a[16:19]\n\t"
"v_mfma_f32_16x16x32_bf16 a[16:19], v[96:99], a[192:195], a[16:19]\n\t"
"v_mfma_f32_16x16x32_bf16 a[16:19], a[244:247], a[196:199], a[16:19]\n\t"
"s_waitcnt lgkmcnt(1)\n\t"
"v_mfma_f32_16x16x32_bf16 a[24:27], v[236:239], a[192:195], a[24:27]\n\t"
"v_mfma_f32_16x16x32_bf16 a[24:27], v[96:99], a[196:199], a[24:27]\n\t"
"v_mfma_f32_16x16x32_bf16 a[24:27], a[244:247], a[200:203], a[24:27]\n\t"
"s_waitcnt lgkmcnt(0)\n\t"
"v_mfma_f32_16x16x32_bf16 a[32:35], v[236:239], a[196:199], a[32:35]\n\t"
"v_mfma_f32_16x16x32_bf16 a[32:35], v[96:99], a[200:203], a[32:35]\n\t"
"v_mfma_f32_16x16x32_bf16 a[32:35], a[244:247], a[204:207], a[32:35]\n\t"
"s_waitcnt vmcnt(13)\n\t"
"v_mfma_f32_16x16x32_bf16 a[40:43], a[248:251], a[168:171], a[40:43]\n\t"
"v_mfma_f32_16x16x32_bf16 a[40:43], a[252:255], a[172:175], a[40:43]\n\t"
"v_mfma_f32_16x16x32_bf16 a[40:43], v[240:243], a[176:179], a[40:43]\n\t"
"ds_read_b64_tr_b16 v[236:237], v246 offset:1024\n\t"
"ds_read_b64_tr_b16 v[238:239], v246 offset:4096\n\t"
"v_mfma_f32_16x16x32_bf16 a[48:51], a[248:251], a[172:175], a[48:51]\n\t"
"v_mfma_f32_16x16x32_bf16 a[48:51], a[252:255], a[176:179], a[48:51]\n\t"
"v_mfma_f32_16x16x32_bf16 a[48:51], v[240:243], a[180:183], a[48:51]\n\t"
"v_mfma_f32_16x16x32_bf16 a[56:59], a[248:251], a[176:179], a[56:59]\n\t"
"v_mfma_f32_16x16x32_bf16 a[56:59], a[252:255], a[180:183], a[56:59]\n\t"
"v_mfma_f32_16x16x32_bf16 a[56:59], v[240:243], a[184:187], a[56:59]\n\t"
"v_mfma_f32_16x16x32_bf16 a[64:67], a[248:251], a[180:183], a[64:67]\n\t"
"ds_read_b64_tr_b16 v[224:225], v246 offset:7168\n\t"
"v_mfma_f32_16x16x32_bf16 a[64:67], a[252:255], a[184:187], a[64:67]\n\t"
"ds_read_b64_tr_b16 v[226:227], v246 offset:10240\n\t"
"v_mfma_f32_16x16x32_bf16 a[64:67], v[240:243], a[188:191], a[64:67]\n\t"
"v_mfma_f32_16x16x32_bf16 a[72:75], a[248:251], a[184:187], a[72:75]\n\t"
"v_mfma_f32_16x16x32_bf16 a[72:75], a[252:255], a[188:191], a[72:75]\n\t"
"v_mfma_f32_16x16x32_bf16 a[72:75], v[240:243], a[192:195], a[72:75]\n\t"
"buffer_load_dwordx4 v[96:99], v252, s[8:11], s20 offen offset:2048 sc1\n\t"
"v_mfma_f32_16x16x32_bf16 a[80:83], a[248:251], a[188:191], a[80:83]\n\t"
"v_mfma_f32_16x16x32_bf16 a[80:83], a[252:255], a[192:195], a[80:83]\n\t"
"v_mfma_f32_16x16x32_bf16 a[80:83], v[240:243], a[196:199], a[80:83]\n\t"
"s_mov_b32 s20, 380928\n\t"
"buffer_load_dwordx4 a[224:227], v252, s[8:11], s20 offen offset:0 sc1\n\t"
"s_waitcnt lgkmcnt(2)\n\t"
"v_mfma_f32_16x16x32_bf16 v[236:239], v[236:239], v[220:223], 0\n\t"
"v_mfma_f32_16x16x32_bf16 a[88:91], a[248:251], a[192:195], a[88:91]\n\t"
"ds_read_b64_tr_b16 v[228:229], v246 offset:13312\n\t"
"v_mfma_f32_16x16x32_bf16 a[88:91], a[252:255], a[196:199], a[88:91]\n\t"
"ds_read_b64_tr_b16 v[230:231], v246 offset:16384\n\t"
"v_mfma_f32_16x16x32_bf16 a[88:91], v[240:243], a[200:203], a[88:91]\n\t"
"v_mfma_f32_16x16x32_bf16 a[96:99], a[248:251], a[196:199], a[96:99]\n\t"
"v_mfma_f32_16x16x32_bf16 a[96:99], a[252:255], a[200:203], a[96:99]\n\t"
"s_nop 0\n\t"
"v_cvt_pk_bf16_f32 v236, v236, v237\n\t"
"v_cvt_pk_bf16_f32 v237, v238, v239\n\t"
"v_mfma_f32_16x16x32_bf16 a[96:99], v[240:243], a[204:207], a[96:99]\n\t"
"buffer_load_dwordx4 a[228:231], v252, s[8:11], s20 offen offset:1024 sc1\n\t"
"s_waitcnt lgkmcnt(2)\n\t"
"v_mfma_f32_16x16x32_bf16 v[224:227], v[224:227], v[220:223], 0\n\t"
"buffer_load_dwordx4 a[232:235], v252, s[8:11], s20 offen offset:2048 sc1\n\t"
"s_waitcnt vmcnt(14)\n\t"
"v_mfma_f32_16x16x32_bf16 a[104:107], a[208:211], a[168:171], a[104:107]\n\t"
"ds_read_b64_tr_b16 v[232:233], v246 offset:20992\n\t"
"v_mfma_f32_16x16x32_bf16 a[104:107], v[248:251], a[172:175], a[104:107]\n\t"
"ds_read_b64_tr_b16 v[234:235], v246 offset:24064\n\t"
"v_mfma_f32_16x16x32_bf16 a[104:107], a[212:215], a[176:179], a[104:107]\n\t"
"ds_read_b128 a[168:171], v244 offset:9216\n\t"
"v_mfma_f32_16x16x32_bf16 a[112:115], a[208:211], a[172:175], a[112:115]\n\t"
"ds_read_b128 a[172:175], v244 offset:9728\n\t"
"v_mfma_f32_16x16x32_bf16 a[112:115], v[248:251], a[176:179], a[112:115]\n\t"
"v_cvt_pk_bf16_f32 v224, v224, v225\n\t"
"v_mfma_f32_16x16x32_bf16 a[112:115], a[212:215], a[180:183], a[112:115]\n\t"
"s_mov_b32 s20, 393216\n\t"
"buffer_load_dwordx4 a[236:239], v252, s[8:11], s20 offen offset:0 sc1\n\t"
"v_mfma_f32_16x16x32_bf16 a[120:123], a[208:211], a[176:179], a[120:123]\n\t"
"v_cvt_pk_bf16_f32 v225, v226, v227\n\t"
"v_mfma_f32_16x16x32_bf16 a[120:123], v[248:251], a[180:183], a[120:123]\n\t"
"ds_read_b128 a[176:179], v244 offset:10240\n\t"
"v_mfma_f32_16x16x32_bf16 a[120:123], a[212:215], a[184:187], a[120:123]\n\t"
"buffer_load_dwordx4 a[240:243], v252, s[8:11], s20 offen offset:1024 sc1\n\t"
"s_waitcnt lgkmcnt(5)\n\t"
"v_mfma_f32_16x16x32_bf16 v[228:231], v[228:231], v[220:223], 0\n\t"
"v_mfma_f32_16x16x32_bf16 a[128:131], a[208:211], a[180:183], a[128:131]\n\t"
"ds_read_b128 a[180:183], v244 offset:10752\n\t"
"v_mfma_f32_16x16x32_bf16 a[128:131], v[248:251], a[184:187], a[128:131]\n\t"
"v_mfma_f32_16x16x32_bf16 a[128:131], a[212:215], a[188:191], a[128:131]\n\t"
"buffer_load_dwordx4 a[244:247], v252, s[8:11], s20 offen offset:2048 sc1\n\t"
"v_mfma_f32_16x16x32_bf16 a[136:139], a[208:211], a[184:187], a[136:139]\n\t"
"ds_read_b128 a[184:187], v244 offset:11264\n\t"
"v_mfma_f32_16x16x32_bf16 a[136:139], v[248:251], a[188:191], a[136:139]\n\t"
"v_cvt_pk_bf16_f32 v228, v228, v229\n\t"
"v_cvt_pk_bf16_f32 v229, v230, v231\n\t"
"v_mfma_f32_16x16x32_bf16 a[136:139], a[212:215], a[192:195], a[136:139]\n\t"
"s_waitcnt lgkmcnt(5)\n\t"
"v_mfma_f32_16x16x32_bf16 v[232:235], v[232:235], v[220:223], 0\n\t"
"s_mov_b32 s20, 552960\n\t"
"buffer_load_dwordx4 a[248:251], v252, s[8:11], s20 offen offset:0 sc1\n\t"
"v_mfma_f32_16x16x32_bf16 a[144:147], a[208:211], a[188:191], a[144:147]\n\t"
"ds_read_b128 a[188:191], v244 offset:11776\n\t"
"v_mfma_f32_16x16x32_bf16 a[144:147], v[248:251], a[192:195], a[144:147]\n\t"
"v_mfma_f32_16x16x32_bf16 a[144:147], a[212:215], a[196:199], a[144:147]\n\t"
"buffer_load_dwordx4 a[252:255], v252, s[8:11], s20 offen offset:1024 sc1\n\t"
"v_mfma_f32_16x16x32_bf16 a[152:155], a[208:211], a[192:195], a[152:155]\n\t"
"ds_read_b128 a[192:195], v244 offset:12288\n\t"
"v_mfma_f32_16x16x32_bf16 a[152:155], v[248:251], a[196:199], a[152:155]\n\t"
"v_cvt_pk_bf16_f32 v232, v232, v233\n\t"
"v_mfma_f32_16x16x32_bf16 a[152:155], a[212:215], a[200:203], a[152:155]\n\t"
"v_cvt_pk_bf16_f32 v233, v234, v235\n\t"
"v_mfma_f32_16x16x32_bf16 a[160:163], a[208:211], a[196:199], a[160:163]\n\t"
"ds_read_b128 a[196:199], v244 offset:12800\n\t"
"v_mfma_f32_16x16x32_bf16 a[160:163], v[248:251], a[200:203], a[160:163]\n\t"
"ds_read_b128 a[200:203], v244 offset:13312\n\t"
"v_mfma_f32_16x16x32_bf16 a[160:163], a[212:215], a[204:207], a[160:163]\n\t"
"ds_read_b128 a[204:207], v244 offset:13824\n\t"
"s_waitcnt vmcnt(8) lgkmcnt(7)\n\t"
"v_mfma_f32_16x16x32_bf16 v[200:203], a[216:219], a[168:171], v[200:203]\n\t"
"v_mfma_f32_16x16x32_bf16 v[200:203], a[220:223], a[172:175], v[200:203]\n\t"
"v_mfma_f32_16x16x32_bf16 v[200:203], v[96:99], a[176:179], v[200:203]\n\t"
"buffer_load_dwordx4 v[248:251], v252, s[8:11], s20 offen offset:2048 sc1\n\t"
"s_waitcnt lgkmcnt(6)\n\t"
"v_mfma_f32_16x16x32_bf16 v[208:211], a[216:219], a[172:175], v[208:211]\n\t"
"v_mfma_f32_16x16x32_bf16 v[208:211], a[220:223], a[176:179], v[208:211]\n\t"
"v_mfma_f32_16x16x32_bf16 v[208:211], v[96:99], a[180:183], v[208:211]\n\t"
"s_mov_b32 s20, 565248\n\t"
"buffer_load_dwordx4 a[212:215], v252, s[8:11], s20 offen offset:0 sc1\n\t"
"s_waitcnt lgkmcnt(0)\n\t"
"s_barrier\n\t"
"v_mfma_f32_16x16x32_bf16 v[216:219], a[216:219], a[176:179], v[216:219]\n\t"
"v_mfma_f32_16x16x32_bf16 v[216:219], a[220:223], a[180:183], v[216:219]\n\t"
"v_mfma_f32_16x16x32_bf16 v[216:219], v[96:99], a[184:187], v[216:219]\n\t"
"ds_write_b64 v245, v[236:237] offset:0\n\t"
"ds_write_b64 v245, v[224:225] offset:512\n\t"
"v_mfma_f32_16x16x32_bf16 a[4:7], a[216:219], a[180:183], a[4:7]\n\t"
"ds_read_b64_tr_b16 v[236:237], v246 offset:27136\n\t"
"v_mfma_f32_16x16x32_bf16 a[4:7], a[220:223], a[184:187], a[4:7]\n\t"
"ds_read_b64_tr_b16 v[238:239], v246 offset:30208\n\t"
"v_mfma_f32_16x16x32_bf16 a[4:7], v[96:99], a[188:191], a[4:7]\n\t"
"ds_write_b64 v245, v[228:229] offset:1024\n\t"
"v_mfma_f32_16x16x32_bf16 a[12:15], a[216:219], a[184:187], a[12:15]\n\t"
"ds_read_b64_tr_b16 v[224:225], v246 offset:33280\n\t"
"v_mfma_f32_16x16x32_bf16 a[12:15], a[220:223], a[188:191], a[12:15]\n\t"
"ds_read_b64_tr_b16 v[226:227], v246 offset:36352\n\t"
"v_mfma_f32_16x16x32_bf16 a[12:15], v[96:99], a[192:195], a[12:15]\n\t"
"ds_write_b64 v245, v[232:233] offset:1536\n\t"
"v_mfma_f32_16x16x32_bf16 a[20:23], a[216:219], a[188:191], a[20:23]\n\t"
"ds_read_b64_tr_b16 v[228:229], v246 offset:40960\n\t"
"v_mfma_f32_16x16x32_bf16 a[20:23], a[220:223], a[192:195], a[20:23]\n\t"
"ds_read_b64_tr_b16 v[230:231], v246 offset:44032\n\t"
"v_mfma_f32_16x16x32_bf16 a[20:23], v[96:99], a[196:199], a[20:23]\n\t"
"global_load_dwordx4 v[240:243], v252, s[46:47] offset:0\n\t"
"s_waitcnt lgkmcnt(6)\n\t"
"v_mfma_f32_16x16x32_bf16 v[236:239], v[236:239], v[220:223], 0\n\t"
"ds_read_b64_tr_b16 v[232:233], v246 offset:47104\n\t"
"ds_read_b64_tr_b16 v[234:235], v246 offset:50176\n\t"
"v_mfma_f32_16x16x32_bf16 a[28:31], a[216:219], a[192:195], a[28:31]\n\t"
"v_mfma_f32_16x16x32_bf16 a[28:31], a[220:223], a[196:199], a[28:31]\n\t"
"v_mfma_f32_16x16x32_bf16 a[28:31], v[96:99], a[200:203], a[28:31]\n\t"
"global_load_dwordx4 a[208:211], v252, s[46:47] offset:1024\n\t"
"s_waitcnt lgkmcnt(5)\n\t"
"v_mfma_f32_16x16x32_bf16 v[224:227], v[224:227], v[220:223], 0\n\t"
"v_mfma_f32_16x16x32_bf16 a[36:39], a[216:219], a[196:199], a[36:39]\n\t"
"v_cvt_pk_bf16_f32 v236, v236, v237\n\t"
"v_cvt_pk_bf16_f32 v237, v238, v239\n\t"
"v_mfma_f32_16x16x32_bf16 a[36:39], a[220:223], a[200:203], a[36:39]\n\t"
"v_mfma_f32_16x16x32_bf16 a[36:39], v[96:99], a[204:207], a[36:39]\n\t"
"buffer_load_dwordx4 a[216:219], v252, s[8:11], s20 offen offset:1024 sc1\n\t"
"s_waitcnt lgkmcnt(2)\n\t"
"v_mfma_f32_16x16x32_bf16 v[228:231], v[228:231], v[220:223], 0\n\t"
"buffer_load_dwordx4 v[96:99], v252, s[8:11], s20 offen offset:2048 sc1\n\t"
"s_waitcnt vmcnt(11)\n\t"
"v_mfma_f32_16x16x32_bf16 a[44:47], a[224:227], a[168:171], a[44:47]\n\t"
"v_cvt_pk_bf16_f32 v224, v224, v225\n\t"
"v_cvt_pk_bf16_f32 v225, v226, v227\n\t"
"v_mfma_f32_16x16x32_bf16 a[44:47], a[228:231], a[172:175], a[44:47]\n\t"
"v_mfma_f32_16x16x32_bf16 a[44:47], a[232:235], a[176:179], a[44:47]\n\t"
"ds_write_b64 v245, v[236:237] offset:2048\n\t"
"s_waitcnt lgkmcnt(1)\n\t"
"v_mfma_f32_16x16x32_bf16 v[232:235], v[232:235], v[220:223], 0\n\t"
"ds_read_b64_tr_b16 v[236:237], v246 offset:53248\n\t"
"ds_read_b64_tr_b16 v[238:239], v246 offset:56320\n\t"
"v_mfma_f32_16x16x32_bf16 a[52:55], a[224:227], a[172:175], a[52:55]\n\t"
"v_cvt_pk_bf16_f32 v228, v228, v229\n\t"
"v_cvt_pk_bf16_f32 v229, v230, v231\n\t"
"v_mfma_f32_16x16x32_bf16 a[52:55], a[228:231], a[176:179], a[52:55]\n\t"
"v_mfma_f32_16x16x32_bf16 a[52:55], a[232:235], a[180:183], a[52:55]\n\t"
"s_mov_b32 s20, 577536\n\t"
"buffer_load_dwordx4 a[220:223], v252, s[8:11], s20 offen offset:0 sc1\n\t"
"v_mfma_f32_16x16x32_bf16 a[60:63], a[224:227], a[176:179], a[60:63]\n\t"
"v_mfma_f32_16x16x32_bf16 a[60:63], a[228:231], a[180:183], a[60:63]\n\t"
"v_cvt_pk_bf16_f32 v232, v232, v233\n\t"
"v_cvt_pk_bf16_f32 v233, v234, v235\n\t"
"v_mfma_f32_16x16x32_bf16 a[60:63], a[232:235], a[184:187], a[60:63]\n\t"
"ds_write_b64 v245, v[224:225] offset:2560\n\t"
"v_mfma_f32_16x16x32_bf16 a[68:71], a[224:227], a[180:183], a[68:71]\n\t"
"v_mfma_f32_16x16x32_bf16 a[68:71], a[228:231], a[184:187], a[68:71]\n\t"
"v_mfma_f32_16x16x32_bf16 a[68:71], a[232:235], a[188:191], a[68:71]\n\t"
"ds_write_b64 v245, v[228:229] offset:3072\n\t"
"s_waitcnt lgkmcnt(2)\n\t"
"v_mfma_f32_16x16x32_bf16 v[236:239], v[236:239], v[220:223], 0\n\t"
"ds_write_b64 v245, v[232:233] offset:3584\n\t"
"v_mfma_f32_16x16x32_bf16 a[76:79], a[224:227], a[184:187], a[76:79]\n\t"
"v_mfma_f32_16x16x32_bf16 a[76:79], a[228:231], a[188:191], a[76:79]\n\t"
"v_mfma_f32_16x16x32_bf16 a[76:79], a[232:235], a[192:195], a[76:79]\n\t"
"v_mfma_f32_16x16x32_bf16 a[84:87], a[224:227], a[188:191], a[84:87]\n\t"
"v_mfma_f32_16x16x32_bf16 a[84:87], a[228:231], a[192:195], a[84:87]\n\t"
"s_nop 1\n\t"
"v_cvt_pk_bf16_f32 v236, v236, v237\n\t"
"v_cvt_pk_bf16_f32 v237, v238, v239\n\t"
"v_mfma_f32_16x16x32_bf16 a[84:87], a[232:235], a[196:199], a[84:87]\n\t"
"v_mfma_f32_16x16x32_bf16 a[92:95], a[224:227], a[192:195], a[92:95]\n\t"
"v_mfma_f32_16x16x32_bf16 a[92:95], a[228:231], a[196:199], a[92:95]\n\t"
"v_mfma_f32_16x16x32_bf16 a[92:95], a[232:235], a[200:203], a[92:95]\n\t"
"ds_write_b64 v245, v[236:237] offset:4096\n\t"
"v_mfma_f32_16x16x32_bf16 a[100:103], a[224:227], a[196:199], a[100:103]\n\t"
"v_mfma_f32_16x16x32_bf16 a[100:103], a[228:231], a[200:203], a[100:103]\n\t"
"v_mfma_f32_16x16x32_bf16 a[100:103], a[232:235], a[204:207], a[100:103]\n\t"
"buffer_load_dwordx4 a[224:227], v252, s[8:11], s20 offen offset:1024 sc1\n\t"
"s_waitcnt vmcnt(10)\n\t"
"v_mfma_f32_16x16x32_bf16 a[108:111], a[236:239], a[168:171], a[108:111]\n\t"
"v_mfma_f32_16x16x32_bf16 a[108:111], a[240:243], a[172:175], a[108:111]\n\t"
"v_mfma_f32_16x16x32_bf16 a[108:111], a[244:247], a[176:179], a[108:111]\n\t"
"buffer_load_dwordx4 a[228:231], v252, s[8:11], s20 offen offset:2048 sc1\n\t"
"s_waitcnt lgkmcnt(0)\n\t"
"s_barrier\n\t"
"v_mfma_f32_16x16x32_bf16 a[116:119], a[236:239], a[172:175], a[116:119]\n\t"
"s_waitcnt vmcnt(6)\n\t"
"ds_read_b64_tr_b16 v[224:225], v240 offset:1024\n\t"
"v_mfma_f32_16x16x32_bf16 a[116:119], a[240:243], a[176:179], a[116:119]\n\t"
"ds_read_b64_tr_b16 v[226:227], v240 offset:2560\n\t"
"v_mfma_f32_16x16x32_bf16 a[116:119], a[244:247], a[180:183], a[116:119]\n\t"
"ds_read_b64_tr_b16 v[228:229], v241 offset:1024\n\t"
"ds_read_b64_tr_b16 v[230:231], v241 offset:2560\n\t"
"v_mfma_f32_16x16x32_bf16 a[124:127], a[236:239], a[176:179], a[124:127]\n\t"
"ds_read_b64_tr_b16 v[232:233], v240 offset:20992\n\t"
"v_mfma_f32_16x16x32_bf16 a[124:127], a[240:243], a[180:183], a[124:127]\n\t"
"ds_read_b64_tr_b16 v[234:235], v240 offset:22528\n\t"
"v_mfma_f32_16x16x32_bf16 a[124:127], a[244:247], a[184:187], a[124:127]\n\t"
"s_mov_b32 s20, 589824\n\t"
"buffer_load_dwordx4 a[232:235], v252, s[8:11], s20 offen offset:0 sc1\n\t"
"v_mfma_f32_16x16x32_bf16 a[132:135], a[236:239], a[180:183], a[132:135]\n\t"
"ds_read_b64_tr_b16 v[236:237], v241 offset:20992\n\t"
"v_mfma_f32_16x16x32_bf16 a[132:135], a[240:243], a[184:187], a[132:135]\n\t"
"ds_read_b64_tr_b16 v[238:239], v241 offset:22528\n\t"
"v_mfma_f32_16x16x32_bf16 a[132:135], a[244:247], a[188:191], a[132:135]\n\t"
"ds_read_b128 a[168:171], v244 offset:0\n\t"
"ds_read_b128 a[172:175], v244 offset:512\n\t"
"s_waitcnt vmcnt(6) lgkmcnt(8)\n\t"
"v_mfma_f32_16x16x32_bf16 v[224:227], v[224:227], a[208:211], 0\n\t"
"ds_read_b128 a[176:179], v244 offset:1024\n\t"
"ds_read_b128 a[180:183], v244 offset:1536\n\t"
"s_waitcnt lgkmcnt(8)\n\t"
"v_mfma_f32_16x16x32_bf16 v[228:231], v[228:231], a[208:211], 0\n\t"
"v_mfma_f32_16x16x32_bf16 a[140:143], a[236:239], a[184:187], a[140:143]\n\t"
"ds_read_b128 a[184:187], v244 offset:2048\n\t"
"v_mfma_f32_16x16x32_bf16 a[140:143], a[240:243], a[188:191], a[140:143]\n\t"
"v_mfma_f32_16x16x32_bf16 a[140:143], a[244:247], a[192:195], a[140:143]\n\t"
"v_mfma_f32_16x16x32_bf16 a[148:151], a[236:239], a[188:191], a[148:151]\n\t"
"v_cvt_pk_bf16_f32 v224, v224, v225\n\t"
"v_cvt_pk_bf16_f32 v225, v226, v227\n\t"
"v_mfma_f32_16x16x32_bf16 a[148:151], a[240:243], a[192:195], a[148:151]\n\t"
"v_cvt_pk_bf16_f32 v226, v228, v229\n\t"
"v_mfma_f32_16x16x32_bf16 a[148:151], a[244:247], a[196:199], a[148:151]\n\t"
"v_cvt_pk_bf16_f32 v227, v230, v231\n\t"
"ds_read_b128 a[188:191], v244 offset:2560\n\t"
"s_waitcnt lgkmcnt(8)\n\t"
"v_mfma_f32_16x16x32_bf16 v[232:235], v[232:235], a[208:211], 0\n\t"
"s_waitcnt lgkmcnt(6)\n\t"
"v_mfma_f32_16x16x32_bf16 v[236:239], v[236:239], a[208:211], 0\n\t"
"s_mov_b64 exec, s[44:45]\n\t"
"ds_write_b128 v242, v[224:227] offset:18432\n\t"
"s_mov_b64 exec, -1\n\t"
"v_mfma_f32_16x16x32_bf16 a[156:159], a[236:239], a[192:195], a[156:159]\n\t"
"ds_read_b128 a[192:195], v244 offset:3072\n\t"
"v_mfma_f32_16x16x32_bf16 a[156:159], a[240:243], a[196:199], a[156:159]\n\t"
"ds_read_b64_tr_b16 v[228:229], v240 offset:40960\n\t"
"v_mfma_f32_16x16x32_bf16 a[156:159], a[244:247], a[200:203], a[156:159]\n\t"
"ds_read_b64_tr_b16 v[230:231], v240 offset:42496\n\t"
"v_mfma_f32_16x16x32_bf16 a[164:167], a[236:239], a[196:199], a[164:167]\n\t"
"v_cvt_pk_bf16_f32 v232, v232, v233\n\t"
"v_cvt_pk_bf16_f32 v233, v234, v235\n\t"
"v_mfma_f32_16x16x32_bf16 a[164:167], a[240:243], a[200:203], a[164:167]\n\t"
"v_cvt_pk_bf16_f32 v234, v236, v237\n\t"
"v_cvt_pk_bf16_f32 v235, v238, v239\n\t"
"v_mfma_f32_16x16x32_bf16 a[164:167], a[244:247], a[204:207], a[164:167]\n\t"
"buffer_load_dwordx4 a[236:239], v252, s[8:11], s20 offen offset:1024 sc1\n\t"
"Lst6_%=:\n\t"
"s_waitcnt lgkmcnt(7)\n\t"
"v_mfma_f32_16x16x32_bf16 v[196:199], a[248:251], a[168:171], v[196:199]\n\t"
"ds_read_b64_tr_b16 v[224:225], v241 offset:40960\n\t"
"v_mfma_f32_16x16x32_bf16 v[196:199], a[252:255], a[172:175], v[196:199]\n\t"
"ds_read_b64_tr_b16 v[226:227], v241 offset:42496\n\t"
"v_add_u32 v240, 59904, v240\n\t"
"v_mfma_f32_16x16x32_bf16 v[196:199], v[248:251], a[176:179], v[196:199]\n\t"
"buffer_load_dwordx4 a[240:243], v252, s[8:11], s20 offen offset:2048 sc1\n\t"
"s_waitcnt lgkmcnt(8)\n\t"
"v_mfma_f32_16x16x32_bf16 v[204:207], a[248:251], a[172:175], v[204:207]\n\t"
"ds_read_b128 a[196:199], v244 offset:3584\n\t"
"v_add_u32 v241, 59904, v241\n\t"
"v_mfma_f32_16x16x32_bf16 v[204:207], a[252:255], a[176:179], v[204:207]\n\t"
"ds_read_b128 a[200:203], v244 offset:4096\n\t"
"v_mfma_f32_16x16x32_bf16 v[204:207], v[248:251], a[180:183], v[204:207]\n\t"
"s_mov_b64 exec, s[44:45]\n\t"
"ds_write_b128 v242, v[232:235] offset:19968\n\t"
"s_mov_b64 exec, -1\n\t"
"s_waitcnt lgkmcnt(10)\n\t"
"v_mfma_f32_16x16x32_bf16 v[212:215], a[248:251], a[176:179], v[212:215]\n\t"
"ds_read_b64_tr_b16 v[236:237], v240 offset:1024\n\t"
"v_mfma_f32_16x16x32_bf16 v[212:215], a[252:255], a[180:183], v[212:215]\n\t"
"ds_read_b64_tr_b16 v[238:239], v240 offset:2560\n\t"
"v_mfma_f32_16x16x32_bf16 v[212:215], v[248:251], a[184:187], v[212:215]\n\t"
"s_mov_b32 s20, 602112\n\t"
"buffer_load_dwordx4 a[244:247], v252, s[8:11], s20 offen offset:0 sc1\n\t"
"s_waitcnt lgkmcnt(7)\n\t"
"v_mfma_f32_16x16x32_bf16 v[228:231], v[228:231], a[208:211], 0\n\t"
"ds_read_b64_tr_b16 v[232:233], v241 offset:1024\n\t"
"ds_read_b64_tr_b16 v[234:235], v241 offset:2560\n\t"
"s_waitcnt lgkmcnt(7)\n\t"
"v_mfma_f32_16x16x32_bf16 v[224:227], v[224:227], a[208:211], 0\n\t"
"ds_read_b128 a[204:207], v244 offset:4608\n\t"
"v_mfma_f32_16x16x32_bf16 a[0:3], a[248:251], a[180:183], a[0:3]\n\t"
"v_mfma_f32_16x16x32_bf16 a[0:3], a[252:255], a[184:187], a[0:3]\n\t"
"v_mfma_f32_16x16x32_bf16 a[0:3], v[248:251], a[188:191], a[0:3]\n\t"
"v_mfma_f32_16x16x32_bf16 a[8:11], a[248:251], a[184:187], a[8:11]\n\t"
"v_cvt_pk_bf16_f32 v228, v228, v229\n\t"
"v_cvt_pk_bf16_f32 v229, v230, v231\n\t"
"v_mfma_f32_16x16x32_bf16 a[8:11], a[252:255], a[188:191], a[8:11]\n\t"
"v_cvt_pk_bf16_f32 v230, v224, v225\n\t"
"v_mfma_f32_16x16x32_bf16 a[8:11], v[248:251], a[192:195], a[8:11]\n\t"
"v_cvt_pk_bf16_f32 v231, v226, v227\n\t"
"s_waitcnt lgkmcnt(3)\n\t"
"v_mfma_f32_16x16x32_bf16 v[236:239], v[236:239], a[208:211], 0\n\t"
"s_mov_b64 exec, s[44:45]\n\t"
"ds_write_b128 v242, v[228:231] offset:21504\n\t"
"s_mov_b64 exec, -1\n\t"
"s_waitcnt lgkmcnt(2)\n\t"
"v_mfma_f32_16x16x32_bf16 v[232:235], v[232:235], a[208:211], 0\n\t"
"ds_read_b64_tr_b16 v[224:225], v240 offset:20992\n\t"
"v_mfma_f32_16x16x32_bf16 a[16:19], a[248:251], a[188:191], a[16:19]\n\t"
"v_mfma_f32_16x16x32_bf16 a[16:19], a[252:255], a[192:195], a[16:19]\n\t"
"ds_read_b64_tr_b16 v[226:227], v240 offset:22528\n\t"
"v_mfma_f32_16x16x32_bf16 a[16:19], v[248:251], a[196:199], a[16:19]\n\t"
"ds_read_b64_tr_b16 v[228:229], v241 offset:20992\n\t"
"v_mfma_f32_16x16x32_bf16 a[24:27], a[248:251], a[192:195], a[24:27]\n\t"
"v_cvt_pk_bf16_f32 v236, v236, v237\n\t"
"v_mfma_f32_16x16x32_bf16 a[24:27], a[252:255], a[196:199], a[24:27]\n\t"
"v_cvt_pk_bf16_f32 v237, v238, v239\n\t"
"v_cvt_pk_bf16_f32 v238, v232, v233\n\t"
"v_mfma_f32_16x16x32_bf16 a[24:27], v[248:251], a[200:203], a[24:27]\n\t"
"v_cvt_pk_bf16_f32 v239, v234, v235\n\t"
"ds_read_b64_tr_b16 v[230:231], v241 offset:22528\n\t"
"s_waitcnt lgkmcnt(5)\n\t"
"v_mfma_f32_16x16x32_bf16 a[32:35], a[248:251], a[196:199], a[32:35]\n\t"
"v_mfma_f32_16x16x32_bf16 a[32:35], a[252:255], a[200:203], a[32:35]\n\t"
"ds_read_b64_tr_b16 v[232:233], v240 offset:40960\n\t"
"v_mfma_f32_16x16x32_bf16 a[32:35], v[248:251], a[204:207], a[32:35]\n\t"
"buffer_load_dwordx4 a[248:251], v252, s[8:11], s20 offen offset:1024 sc1\n\t"
"s_waitcnt vmcnt(8)\n\t"
"v_mfma_f32_16x16x32_bf16 a[40:43], a[212:215], a[168:171], a[40:43]\n\t"
"ds_read_b64_tr_b16 v[234:235], v240 offset:42496\n\t"
"v_mfma_f32_16x16x32_bf16 a[40:43], a[216:219], a[172:175], a[40:43]\n\t"
"v_mfma_f32_16x16x32_bf16 a[40:43], v[96:99], a[176:179], a[40:43]\n\t"
"s_mov_b64 exec, s[44:45]\n\t"
"ds_write_b128 v242, v[236:239] offset:23040\n\t"
"s_mov_b64 exec, -1\n\t"
"v_mfma_f32_16x16x32_bf16 a[48:51], a[212:215], a[172:175], a[48:51]\n\t"
"ds_read_b64_tr_b16 v[236:237], v241 offset:40960\n\t"
"v_mfma_f32_16x16x32_bf16 a[48:51], a[216:219], a[176:179], a[48:51]\n\t"
"ds_read_b64_tr_b16 v[238:239], v241 offset:42496\n\t"
"v_mfma_f32_16x16x32_bf16 a[48:51], v[96:99], a[180:183], a[48:51]\n\t"
"buffer_load_dwordx4 v[248:251], v252, s[8:11], s20 offen offset:2048 sc1\n\t"
"s_waitcnt lgkmcnt(7)\n\t"
"v_mfma_f32_16x16x32_bf16 v[224:227], v[224:227], a[208:211], 0\n\t"
"s_mov_b32 s20, 614400\n\t"
"buffer_load_dwordx4 a[252:255], v252, s[8:11], s20 offen offset:0 sc1\n\t"
"s_waitcnt lgkmcnt(5)\n\t"
"v_mfma_f32_16x16x32_bf16 v[228:231], v[228:231], a[208:211], 0\n\t"
"v_xor_b32 v246, 0xc0, v246\n\t"
"v_mfma_f32_16x16x32_bf16 a[56:59], a[212:215], a[176:179], a[56:59]\n\t"
"v_mfma_f32_16x16x32_bf16 a[56:59], a[216:219], a[180:183], a[56:59]\n\t"
"v_mfma_f32_16x16x32_bf16 a[56:59], v[96:99], a[184:187], a[56:59]\n\t"
"v_mfma_f32_16x16x32_bf16 a[64:67], a[212:215], a[180:183], a[64:67]\n\t"
"v_cvt_pk_bf16_f32 v224, v224, v225\n\t"
"v_mfma_f32_16x16x32_bf16 a[64:67], a[216:219], a[184:187], a[64:67]\n\t"
"v_cvt_pk_bf16_f32 v225, v226, v227\n\t"
"v_cvt_pk_bf16_f32 v226, v228, v229\n\t"
"v_mfma_f32_16x16x32_bf16 a[64:67], v[96:99], a[188:191], a[64:67]\n\t"
"v_cvt_pk_bf16_f32 v227, v230, v231\n\t"
"s_waitcnt lgkmcnt(3)\n\t"
"v_mfma_f32_16x16x32_bf16 v[232:235], v[232:235], a[208:211], 0\n\t"
"s_waitcnt lgkmcnt(0)\n\t"
"v_mfma_f32_16x16x32_bf16 v[236:239], v[236:239], a[208:211], 0\n\t"
"s_mov_b64 exec, s[44:45]\n\t"
"ds_write_b128 v242, v[224:227] offset:24576\n\t"
"s_mov_b64 exec, -1\n\t"
"v_mfma_f32_16x16x32_bf16 a[72:75], a[212:215], a[184:187], a[72:75]\n\t"
"v_mfma_f32_16x16x32_bf16 a[72:75], a[216:219], a[188:191], a[72:75]\n\t"
"v_mfma_f32_16x16x32_bf16 a[72:75], v[96:99], a[192:195], a[72:75]\n\t"
"buffer_load_dwordx4 a[208:211], v252, s[8:11], s20 offen offset:1024 sc1\n\t"
"v_mfma_f32_16x16x32_bf16 a[80:83], a[212:215], a[188:191], a[80:83]\n\t"
"v_cvt_pk_bf16_f32 v232, v232, v233\n\t"
"v_cvt_pk_bf16_f32 v233, v234, v235\n\t"
"v_mfma_f32_16x16x32_bf16 a[80:83], a[216:219], a[192:195], a[80:83]\n\t"
"v_cvt_pk_bf16_f32 v234, v236, v237\n\t"
"v_mfma_f32_16x16x32_bf16 a[80:83], v[96:99], a[196:199], a[80:83]\n\t"
"v_cvt_pk_bf16_f32 v235, v238, v239\n\t"
"ds_read_b64_tr_b16 v[228:229], v246 offset:1024\n\t"
"v_mfma_f32_16x16x32_bf16 a[88:91], a[212:215], a[192:195], a[88:91]\n\t"
"ds_read_b64_tr_b16 v[230:231], v246 offset:4096\n\t"
"v_mfma_f32_16x16x32_bf16 a[88:91], a[216:219], a[196:199], a[88:91]\n\t"
"v_mfma_f32_16x16x32_bf16 a[88:91], v[96:99], a[200:203], a[88:91]\n\t"
"s_mov_b64 exec, s[44:45]\n\t"
"ds_write_b128 v242, v[232:235] offset:26112\n\t"
"s_mov_b64 exec, -1\n\t"
"v_mfma_f32_16x16x32_bf16 a[96:99], a[212:215], a[196:199], a[96:99]\n\t"
"ds_read_b64_tr_b16 v[224:225], v246 offset:7168\n\t"
"v_mfma_f32_16x16x32_bf16 a[96:99], a[216:219], a[200:203], a[96:99]\n\t"
"ds_read_b64_tr_b16 v[226:227], v246 offset:10240\n\t"
"v_mfma_f32_16x16x32_bf16 a[96:99], v[96:99], a[204:207], a[96:99]\n\t"
"s_waitcnt vmcnt(8)\n\t"
"v_mfma_f32_16x16x32_bf16 a[104:107], a[220:223], a[168:171], a[104:107]\n\t"
"ds_read_b128 a[168:171], v244 offset:9216\n\t"
"v_mfma_f32_16x16x32_bf16 a[104:107], a[224:227], a[172:175], a[104:107]\n\t"
"ds_read_b64_tr_b16 v[236:237], v246 offset:13312\n\t"
"v_mfma_f32_16x16x32_bf16 a[104:107], a[228:231], a[176:179], a[104:107]\n\t"
"buffer_load_dwordx4 a[212:215], v252, s[8:11], s20 offen offset:2048 sc1\n\t"
"v_mfma_f32_16x16x32_bf16 a[112:115], a[220:223], a[172:175], a[112:115]\n\t"
"ds_read_b64_tr_b16 v[238:239], v246 offset:16384\n\t"
"v_mfma_f32_16x16x32_bf16 a[112:115], a[224:227], a[176:179], a[112:115]\n\t"
"ds_read_b128 a[172:175], v244 offset:9728\n\t"
"v_mfma_f32_16x16x32_bf16 a[112:115], a[228:231], a[180:183], a[112:115]\n\t"
"s_mov_b32 s20, 479232\n\t"
"buffer_load_dwordx4 v[96:99], v252, s[8:11], s20 offen offset:0 sc1\n\t"
"v_mfma_f32_16x16x32_bf16 a[120:123], a[220:223], a[176:179], a[120:123]\n\t"
"ds_read_b64_tr_b16 v[232:233], v246 offset:20992\n\t"
"v_mfma_f32_16x16x32_bf16 a[120:123], a[224:227], a[180:183], a[120:123]\n\t"
"ds_read_b64_tr_b16 v[234:235], v246 offset:24064\n\t"
"v_mfma_f32_16x16x32_bf16 a[120:123], a[228:231], a[184:187], a[120:123]\n\t"
"buffer_load_dwordx4 a[216:219], v252, s[8:11], s20 offen offset:1024 sc1\n\t"
"s_waitcnt lgkmcnt(9)\n\t"
"v_mfma_f32_16x16x32_bf16 v[228:231], v[228:231], v[220:223], 0\n\t"
"v_mfma_f32_16x16x32_bf16 a[128:131], a[220:223], a[180:183], a[128:131]\n\t"
"ds_read_b128 a[176:179], v244 offset:10240\n\t"
"v_mfma_f32_16x16x32_bf16 a[128:131], a[224:227], a[184:187], a[128:131]\n\t"
"v_mfma_f32_16x16x32_bf16 a[128:131], a[228:231], a[188:191], a[128:131]\n\t"
"ds_read_b128 a[180:183], v244 offset:10752\n\t"
"s_waitcnt lgkmcnt(8)\n\t"
"v_mfma_f32_16x16x32_bf16 v[224:227], v[224:227], v[220:223], 0\n\t"
"v_mfma_f32_16x16x32_bf16 a[136:139], a[220:223], a[184:187], a[136:139]\n\t"
"v_cvt_pk_bf16_f32 v228, v228, v229\n\t"
"v_cvt_pk_bf16_f32 v229, v230, v231\n\t"
"v_mfma_f32_16x16x32_bf16 a[136:139], a[224:227], a[188:191], a[136:139]\n\t"
"ds_read_b128 a[184:187], v244 offset:11264\n\t"
"v_mfma_f32_16x16x32_bf16 a[136:139], a[228:231], a[192:195], a[136:139]\n\t"
"s_waitcnt lgkmcnt(6)\n\t"
"v_mfma_f32_16x16x32_bf16 v[236:239], v[236:239], v[220:223], 0\n\t"
"v_mfma_f32_16x16x32_bf16 a[144:147], a[220:223], a[188:191], a[144:147]\n\t"
"v_cvt_pk_bf16_f32 v230, v224, v225\n\t"
"v_cvt_pk_bf16_f32 v231, v226, v227\n\t"
"v_mfma_f32_16x16x32_bf16 a[144:147], a[224:227], a[192:195], a[144:147]\n\t"
"ds_read_b64_tr_b16 v[224:225], v246 offset:27136\n\t"
"v_mfma_f32_16x16x32_bf16 a[144:147], a[228:231], a[196:199], a[144:147]\n\t"
"ds_read_b64_tr_b16 v[226:227], v246 offset:30208\n\t"
"ds_read_b128 a[188:191], v244 offset:11776\n\t"
"s_waitcnt lgkmcnt(6)\n\t"
"v_mfma_f32_16x16x32_bf16 v[232:235], v[232:235], v[220:223], 0\n\t"
"v_mfma_f32_16x16x32_bf16 a[152:155], a[220:223], a[192:195], a[152:155]\n\t"
"v_cvt_pk_bf16_f32 v236, v236, v237\n\t"
"v_cvt_pk_bf16_f32 v237, v238, v239\n\t"
"v_mfma_f32_16x16x32_bf16 a[152:155], a[224:227], a[196:199], a[152:155]\n\t"
"ds_read_b128 a[192:195], v244 offset:12288\n\t"
"v_mfma_f32_16x16x32_bf16 a[152:155], a[228:231], a[200:203], a[152:155]\n\t"
"v_mfma_f32_16x16x32_bf16 a[160:163], a[220:223], a[196:199], a[160:163]\n\t"
"ds_read_b128 a[196:199], v244 offset:12800\n\t"
"v_mfma_f32_16x16x32_bf16 a[160:163], a[224:227], a[200:203], a[160:163]\n\t"
"v_cvt_pk_bf16_f32 v238, v232, v233\n\t"
"v_cvt_pk_bf16_f32 v239, v234, v235\n\t"
"v_mfma_f32_16x16x32_bf16 a[160:163], a[228:231], a[204:207], a[160:163]\n\t"
"buffer_load_dwordx4 a[220:223], v252, s[8:11], s20 offen offset:2048 sc1\n\t"
"s_waitcnt vmcnt(9) lgkmcnt(7)\n\t"
"v_mfma_f32_16x16x32_bf16 v[200:203], a[232:235], a[168:171], v[200:203]\n\t"
"ds_read_b128 a[200:203], v244 offset:13312\n\t"
"v_mfma_f32_16x16x32_bf16 v[200:203], a[236:239], a[172:175], v[200:203]\n\t"
"ds_read_b128 a[204:207], v244 offset:13824\n\t"
"v_mfma_f32_16x16x32_bf16 v[200:203], a[240:243], a[176:179], v[200:203]\n\t"
"s_mov_b32 s20, 491520\n\t"
"buffer_load_dwordx4 a[224:227], v252, s[8:11], s20 offen offset:0 sc1\n\t"
"s_waitcnt lgkmcnt(5)\n\t"
"v_mfma_f32_16x16x32_bf16 v[224:227], v[224:227], v[220:223], 0\n\t"
"buffer_load_dwordx4 a[228:231], v252, s[8:11], s20 offen offset:1024 sc1\n\t"
"v_mfma_f32_16x16x32_bf16 v[208:211], a[232:235], a[172:175], v[208:211]\n\t"
"v_mfma_f32_16x16x32_bf16 v[208:211], a[236:239], a[176:179], v[208:211]\n\t"
"v_mfma_f32_16x16x32_bf16 v[208:211], a[240:243], a[180:183], v[208:211]\n\t"
"s_waitcnt lgkmcnt(0)\n\t"
"s_barrier\n\t"
"v_mfma_f32_16x16x32_bf16 v[216:219], a[232:235], a[176:179], v[216:219]\n\t"
"ds_read_b64_tr_b16 v[232:233], v246 offset:33280\n\t"
"v_mfma_f32_16x16x32_bf16 v[216:219], a[236:239], a[180:183], v[216:219]\n\t"
"v_cvt_pk_bf16_f32 v224, v224, v225\n\t"
"v_cvt_pk_bf16_f32 v225, v226, v227\n\t"
"v_mfma_f32_16x16x32_bf16 v[216:219], a[240:243], a[184:187], v[216:219]\n\t"
"s_add_u32 m0, s24, 256\n\t"
"s_add_u32 s28, s21, 0\n\t"
"buffer_load_dwordx4 v247, s[12:15], s28 offen offset:256 nt lds\n\t"
"ds_write2st64_b64 v245, v[228:229], v[230:231] offset0:0 offset1:1\n\t"
"s_add_u32 m0, s24, 6400\n\t"
"s_add_u32 s28, s21, 196608\n\t"
"buffer_load_dwordx4 v247, s[12:15], s28 offen offset:256 nt lds\n\t"
"v_mfma_f32_16x16x32_bf16 a[4:7], a[232:235], a[180:183], a[4:7]\n\t"
"ds_read_b64_tr_b16 v[234:235], v246 offset:36352\n\t"
"v_mfma_f32_16x16x32_bf16 a[4:7], a[236:239], a[184:187], a[4:7]\n\t"
"ds_read_b64_tr_b16 v[228:229], v246 offset:40960\n\t"
"v_mfma_f32_16x16x32_bf16 a[4:7], a[240:243], a[188:191], a[4:7]\n\t"
"ds_write2st64_b64 v245, v[236:237], v[238:239] offset0:2 offset1:3\n\t"
"s_add_u32 m0, s24, 12544\n\t"
"s_add_u32 s28, s21, 393216\n\t"
"buffer_load_dwordx4 v247, s[12:15], s28 offen offset:256 nt lds\n\t"
"v_mfma_f32_16x16x32_bf16 a[12:15], a[232:235], a[184:187], a[12:15]\n\t"
"ds_read_b64_tr_b16 v[230:231], v246 offset:44032\n\t"
"v_mfma_f32_16x16x32_bf16 a[12:15], a[236:239], a[188:191], a[12:15]\n\t"
"v_mfma_f32_16x16x32_bf16 a[12:15], a[240:243], a[192:195], a[12:15]\n\t"
"s_add_u32 m0, s24, 20224\n\t"
"s_add_u32 s28, s21, 589824\n\t"
"buffer_load_dwordx4 v247, s[12:15], s28 offen offset:256 nt lds\n\t"
"ds_write_b64 v245, v[224:225] offset:2048\n\t"
"s_add_u32 m0, s24, 26368\n\t"
"s_add_u32 s28, s21, 786432\n\t"
"buffer_load_dwordx4 v247, s[12:15], s28 offen offset:256 nt lds\n\t"
"v_mfma_f32_16x16x32_bf16 a[20:23], a[232:235], a[188:191], a[20:23]\n\t"
"ds_read_b64_tr_b16 v[224:225], v246 offset:47104\n\t"
"v_mfma_f32_16x16x32_bf16 a[20:23], a[236:239], a[192:195], a[20:23]\n\t"
"ds_read_b64_tr_b16 v[226:227], v246 offset:50176\n\t"
"v_mfma_f32_16x16x32_bf16 a[20:23], a[240:243], a[196:199], a[20:23]\n\t"
"s_add_u32 m0, s24, 60160\n\t"
"s_add_u32 s28, s21, 1769472\n\t"
"buffer_load_dwordx4 v247, s[12:15], s28 offen offset:256 nt lds\n\t"
"s_waitcnt lgkmcnt(6)\n\t"
"v_mfma_f32_16x16x32_bf16 v[232:235], v[232:235], v[220:223], 0\n\t"
"v_mfma_f32_16x16x32_bf16 a[28:31], a[232:235], a[192:195], a[28:31]\n\t"
"v_mfma_f32_16x16x32_bf16 a[28:31], a[236:239], a[196:199], a[28:31]\n\t"
"v_mfma_f32_16x16x32_bf16 a[28:31], a[240:243], a[200:203], a[28:31]\n\t"
"s_waitcnt lgkmcnt(3)\n\t"
"v_mfma_f32_16x16x32_bf16 v[228:231], v[228:231], v[220:223], 0\n\t"
"v_mfma_f32_16x16x32_bf16 a[36:39], a[232:235], a[196:199], a[36:39]\n\t"
"s_nop 1\n\t"
"v_cvt_pk_bf16_f32 v232, v232, v233\n\t"
"v_cvt_pk_bf16_f32 v233, v234, v235\n\t"
"v_mfma_f32_16x16x32_bf16 a[36:39], a[236:239], a[200:203], a[36:39]\n\t"
"v_mfma_f32_16x16x32_bf16 a[36:39], a[240:243], a[204:207], a[36:39]\n\t"
"s_waitcnt lgkmcnt(0)\n\t"
"v_mfma_f32_16x16x32_bf16 v[224:227], v[224:227], v[220:223], 0\n\t"
"s_waitcnt vmcnt(14)\n\t"
"v_mfma_f32_16x16x32_bf16 a[44:47], a[244:247], a[168:171], a[44:47]\n\t"
"v_cvt_pk_bf16_f32 v234, v228, v229\n\t"
"v_cvt_pk_bf16_f32 v235, v230, v231\n\t"
"v_mfma_f32_16x16x32_bf16 a[44:47], a[248:251], a[172:175], a[44:47]\n\t"
"ds_read_b64_tr_b16 v[228:229], v246 offset:53248\n\t"
"v_mfma_f32_16x16x32_bf16 a[44:47], v[248:251], a[176:179], a[44:47]\n\t"
"ds_write2st64_b64 v245, v[232:233], v[234:235] offset0:5 offset1:6\n\t"
"v_mfma_f32_16x16x32_bf16 a[52:55], a[244:247], a[172:175], a[52:55]\n\t"
"ds_read_b64_tr_b16 v[230:231], v246 offset:56320\n\t"
"v_mfma_f32_16x16x32_bf16 a[52:55], a[248:251], a[176:179], a[52:55]\n\t"
"v_cvt_pk_bf16_f32 v224, v224, v225\n\t"
"v_mfma_f32_16x16x32_bf16 a[52:55], v[248:251], a[180:183], a[52:55]\n\t"
"v_cvt_pk_bf16_f32 v225, v226, v227\n\t"
"v_mfma_f32_16x16x32_bf16 a[60:63], a[244:247], a[176:179], a[60:63]\n\t"
"v_mfma_f32_16x16x32_bf16 a[60:63], a[248:251], a[180:183], a[60:63]\n\t"
"v_mfma_f32_16x16x32_bf16 a[60:63], v[248:251], a[184:187], a[60:63]\n\t"
"v_mfma_f32_16x16x32_bf16 a[68:71], a[244:247], a[180:183], a[68:71]\n\t"
"v_mfma_f32_16x16x32_bf16 a[68:71], a[248:251], a[184:187], a[68:71]\n\t"
"v_mfma_f32_16x16x32_bf16 a[68:71], v[248:251], a[188:191], a[68:71]\n\t"
"v_mfma_f32_16x16x32_bf16 a[76:79], a[244:247], a[184:187], a[76:79]\n\t"
"v_mfma_f32_16x16x32_bf16 a[76:79], a[248:251], a[188:191], a[76:79]\n\t"
"v_mfma_f32_16x16x32_bf16 a[76:79], v[248:251], a[192:195], a[76:79]\n\t"
"s_waitcnt lgkmcnt(0)\n\t"
"v_mfma_f32_16x16x32_bf16 v[228:231], v[228:231], v[220:223], 0\n\t"
"v_mfma_f32_16x16x32_bf16 a[84:87], a[244:247], a[188:191], a[84:87]\n\t"
"v_mfma_f32_16x16x32_bf16 a[84:87], a[248:251], a[192:195], a[84:87]\n\t"
"v_mfma_f32_16x16x32_bf16 a[84:87], v[248:251], a[196:199], a[84:87]\n\t"
"buffer_load_dwordx4 v[236:239], v252, s[8:11], s20 offen offset:2048 sc1\n\t"
"v_mfma_f32_16x16x32_bf16 a[92:95], a[244:247], a[192:195], a[92:95]\n\t"
"v_mfma_f32_16x16x32_bf16 a[92:95], a[248:251], a[196:199], a[92:95]\n\t"
"s_nop 1\n\t"
"v_cvt_pk_bf16_f32 v226, v228, v229\n\t"
"v_cvt_pk_bf16_f32 v227, v230, v231\n\t"
"v_mfma_f32_16x16x32_bf16 a[92:95], v[248:251], a[200:203], a[92:95]\n\t"
"ds_write2st64_b64 v245, v[224:225], v[226:227] offset0:7 offset1:8\n\t"
"v_mfma_f32_16x16x32_bf16 a[100:103], a[244:247], a[196:199], a[100:103]\n\t"
"v_mfma_f32_16x16x32_bf16 a[100:103], a[248:251], a[200:203], a[100:103]\n\t"
"v_mfma_f32_16x16x32_bf16 a[100:103], v[248:251], a[204:207], a[100:103]\n\t"
"s_waitcnt vmcnt(12)\n\t"
"v_mfma_f32_16x16x32_bf16 a[108:111], a[252:255], a[168:171], a[108:111]\n\t"
"v_mfma_f32_16x16x32_bf16 a[108:111], a[208:211], a[172:175], a[108:111]\n\t"
"v_mfma_f32_16x16x32_bf16 a[108:111], a[212:215], a[176:179], a[108:111]\n\t"
"s_waitcnt lgkmcnt(0)\n\t"
"s_barrier\n\t"
"v_mfma_f32_16x16x32_bf16 a[116:119], a[252:255], a[172:175], a[116:119]\n\t"
"ds_read_b128 a[168:171], v244 offset:0\n\t"
"v_mfma_f32_16x16x32_bf16 a[116:119], a[208:211], a[176:179], a[116:119]\n\t"
"ds_read_b128 a[172:175], v244 offset:512\n\t"
"v_mfma_f32_16x16x32_bf16 a[116:119], a[212:215], a[180:183], a[116:119]\n\t"
"v_mfma_f32_16x16x32_bf16 a[124:127], a[252:255], a[176:179], a[124:127]\n\t"
"ds_read_b128 a[176:179], v244 offset:1024\n\t"
"v_mfma_f32_16x16x32_bf16 a[124:127], a[208:211], a[180:183], a[124:127]\n\t"
"v_mfma_f32_16x16x32_bf16 a[124:127], a[212:215], a[184:187], a[124:127]\n\t"
"v_mfma_f32_16x16x32_bf16 a[132:135], a[252:255], a[180:183], a[132:135]\n\t"
"ds_read_b128 a[180:183], v244 offset:1536\n\t"
"v_mfma_f32_16x16x32_bf16 a[132:135], a[208:211], a[184:187], a[132:135]\n\t"
"v_mfma_f32_16x16x32_bf16 a[132:135], a[212:215], a[188:191], a[132:135]\n\t"
"v_mfma_f32_16x16x32_bf16 a[140:143], a[252:255], a[184:187], a[140:143]\n\t"
"ds_read_b128 a[184:187], v244 offset:2048\n\t"
"v_mfma_f32_16x16x32_bf16 a[140:143], a[208:211], a[188:191], a[140:143]\n\t"
"v_mfma_f32_16x16x32_bf16 a[140:143], a[212:215], a[192:195], a[140:143]\n\t"
"v_mfma_f32_16x16x32_bf16 a[148:151], a[252:255], a[188:191], a[148:151]\n\t"
"ds_read_b128 a[188:191], v244 offset:2560\n\t"
"v_mfma_f32_16x16x32_bf16 a[148:151], a[208:211], a[192:195], a[148:151]\n\t"
"v_mfma_f32_16x16x32_bf16 a[148:151], a[212:215], a[196:199], a[148:151]\n\t"
"v_mfma_f32_16x16x32_bf16 a[156:159], a[252:255], a[192:195], a[156:159]\n\t"
"ds_read_b128 a[192:195], v244 offset:3072\n\t"
"v_mfma_f32_16x16x32_bf16 a[156:159], a[208:211], a[196:199], a[156:159]\n\t"
"v_mfma_f32_16x16x32_bf16 a[156:159], a[212:215], a[200:203], a[156:159]\n\t"
"v_mfma_f32_16x16x32_bf16 a[164:167], a[252:255], a[196:199], a[164:167]\n\t"
"ds_read_b128 a[196:199], v244 offset:3584\n\t"
"v_mfma_f32_16x16x32_bf16 a[164:167], a[208:211], a[200:203], a[164:167]\n\t"
"ds_read_b128 a[200:203], v244 offset:4096\n\t"
"v_mfma_f32_16x16x32_bf16 a[164:167], a[212:215], a[204:207], a[164:167]\n\t"
"ds_read_b128 a[204:207], v244 offset:4608\n\t"
"Lst7_%=:\n\t"
"s_waitcnt vmcnt(9) lgkmcnt(7)\n\t"
"v_mfma_f32_16x16x32_bf16 v[0:3], v[96:99], a[168:171], v[0:3]\n\t"
"v_mfma_f32_16x16x32_bf16 v[0:3], a[216:219], a[172:175], v[0:3]\n\t"
"v_mfma_f32_16x16x32_bf16 v[0:3], a[220:223], a[176:179], v[0:3]\n\t"
"s_mov_b32 s20, 503808\n\t"
"buffer_load_dwordx4 v[240:243], v252, s[8:11], s20 offen offset:0 sc1\n\t"
"s_waitcnt lgkmcnt(6)\n\t"
"v_mfma_f32_16x16x32_bf16 v[4:7], v[96:99], a[172:175], v[4:7]\n\t"
"v_mfma_f32_16x16x32_bf16 v[4:7], a[216:219], a[176:179], v[4:7]\n\t"
"v_mfma_f32_16x16x32_bf16 v[4:7], a[220:223], a[180:183], v[4:7]\n\t"
"buffer_load_dwordx4 v[248:251], v252, s[8:11], s20 offen offset:1024 sc1\n\t"
"s_waitcnt lgkmcnt(5)\n\t"
"v_mfma_f32_16x16x32_bf16 v[8:11], v[96:99], a[176:179], v[8:11]\n\t"
"v_mfma_f32_16x16x32_bf16 v[8:11], a[216:219], a[180:183], v[8:11]\n\t"
"v_mfma_f32_16x16x32_bf16 v[8:11], a[220:223], a[184:187], v[8:11]\n\t"
"buffer_load_dwordx4 a[208:211], v252, s[8:11], s20 offen offset:2048 sc1\n\t"
"s_waitcnt lgkmcnt(4)\n\t"
"v_mfma_f32_16x16x32_bf16 v[12:15], v[96:99], a[180:183], v[12:15]\n\t"
"v_mfma_f32_16x16x32_bf16 v[12:15], a[216:219], a[184:187], v[12:15]\n\t"
"v_mfma_f32_16x16x32_bf16 v[12:15], a[220:223], a[188:191], v[12:15]\n\t"
"s_mov_b32 s20, 516096\n\t"
"buffer_load_dwordx4 a[212:215], v252, s[8:11], s20 offen offset:0 sc1\n\t"
"s_waitcnt lgkmcnt(3)\n\t"
"v_mfma_f32_16x16x32_bf16 v[16:19], v[96:99], a[184:187], v[16:19]\n\t"
"v_mfma_f32_16x16x32_bf16 v[16:19], a[216:219], a[188:191], v[16:19]\n\t"
"v_mfma_f32_16x16x32_bf16 v[16:19], a[220:223], a[192:195], v[16:19]\n\t"
"ds_read_b64_tr_b16 v[232:233], v246 offset:1536\n\t"
"ds_read_b64_tr_b16 v[234:235], v246 offset:4608\n\t"
"s_waitcnt lgkmcnt(4)\n\t"
"v_mfma_f32_16x16x32_bf16 v[20:23], v[96:99], a[188:191], v[20:23]\n\t"
"v_mfma_f32_16x16x32_bf16 v[20:23], a[216:219], a[192:195], v[20:23]\n\t"
"v_mfma_f32_16x16x32_bf16 v[20:23], a[220:223], a[196:199], v[20:23]\n\t"
"buffer_load_dwordx4 a[232:235], v252, s[8:11], s20 offen offset:1024 sc1\n\t"
"s_waitcnt lgkmcnt(3)\n\t"
"v_mfma_f32_16x16x32_bf16 v[24:27], v[96:99], a[192:195], v[24:27]\n\t"
"v_mfma_f32_16x16x32_bf16 v[24:27], a[216:219], a[196:199], v[24:27]\n\t"
"v_mfma_f32_16x16x32_bf16 v[24:27], a[220:223], a[200:203], v[24:27]\n\t"
"buffer_load_dwordx4 a[236:239], v252, s[8:11], s20 offen offset:2048 sc1\n\t"
"s_waitcnt lgkmcnt(2)\n\t"
"v_mfma_f32_16x16x32_bf16 v[28:31], v[96:99], a[196:199], v[28:31]\n\t"
"ds_read_b64_tr_b16 v[228:229], v246 offset:7680\n\t"
"v_mfma_f32_16x16x32_bf16 v[28:31], a[216:219], a[200:203], v[28:31]\n\t"
"ds_read_b64_tr_b16 v[230:231], v246 offset:10752\n\t"
"v_mfma_f32_16x16x32_bf16 v[28:31], a[220:223], a[204:207], v[28:31]\n\t"
"s_waitcnt vmcnt(6)\n\t"
"v_mfma_f32_16x16x32_bf16 v[32:35], a[224:227], a[168:171], v[32:35]\n\t"
"v_mfma_f32_16x16x32_bf16 v[32:35], a[228:231], a[172:175], v[32:35]\n\t"
"v_mfma_f32_16x16x32_bf16 v[32:35], v[236:239], a[176:179], v[32:35]\n\t"
"s_mov_b32 s20, 528384\n\t"
"buffer_load_dwordx4 v[96:99], v252, s[8:11], s20 offen offset:0 sc1\n\t"
"v_mfma_f32_16x16x32_bf16 v[36:39], a[224:227], a[172:175], v[36:39]\n\t"
"v_mfma_f32_16x16x32_bf16 v[36:39], a[228:231], a[176:179], v[36:39]\n\t"
"v_mfma_f32_16x16x32_bf16 v[36:39], v[236:239], a[180:183], v[36:39]\n\t"
"buffer_load_dwordx4 a[216:219], v252, s[8:11], s20 offen offset:1024 sc1\n\t"
"s_waitcnt lgkmcnt(2)\n\t"
"v_mfma_f32_16x16x32_bf16 v[232:235], v[232:235], v[220:223], 0\n\t"
"buffer_load_dwordx4 a[220:223], v252, s[8:11], s20 offen offset:2048 sc1\n\t"
"v_mfma_f32_16x16x32_bf16 v[40:43], a[224:227], a[176:179], v[40:43]\n\t"
"ds_read_b64_tr_b16 v[224:225], v246 offset:13824\n\t"
"v_mfma_f32_16x16x32_bf16 v[40:43], a[228:231], a[180:183], v[40:43]\n\t"
"ds_read_b64_tr_b16 v[226:227], v246 offset:16896\n\t"
"v_mfma_f32_16x16x32_bf16 v[40:43], v[236:239], a[184:187], v[40:43]\n\t"
"v_mfma_f32_16x16x32_bf16 v[44:47], a[224:227], a[180:183], v[44:47]\n\t"
"v_mfma_f32_16x16x32_bf16 v[44:47], a[228:231], a[184:187], v[44:47]\n\t"
"v_cvt_pk_bf16_f32 v232, v232, v233\n\t"
"v_cvt_pk_bf16_f32 v233, v234, v235\n\t"
"v_mfma_f32_16x16x32_bf16 v[44:47], v[236:239], a[188:191], v[44:47]\n\t"
"s_mov_b32 s20, 540672\n\t"
"buffer_load_dwordx4 a[240:243], v252, s[8:11], s20 offen offset:0 sc1\n\t"
"s_waitcnt lgkmcnt(2)\n\t"
"v_mfma_f32_16x16x32_bf16 v[228:231], v[228:231], v[220:223], 0\n\t"
"buffer_load_dwordx4 a[244:247], v252, s[8:11], s20 offen offset:1024 sc1\n\t"
"v_mfma_f32_16x16x32_bf16 v[48:51], a[224:227], a[184:187], v[48:51]\n\t"
"v_mfma_f32_16x16x32_bf16 v[48:51], a[228:231], a[188:191], v[48:51]\n\t"
"v_mfma_f32_16x16x32_bf16 v[48:51], v[236:239], a[192:195], v[48:51]\n\t"
"buffer_load_dwordx4 a[248:251], v252, s[8:11], s20 offen offset:2048 sc1\n\t"
"v_mfma_f32_16x16x32_bf16 v[52:55], a[224:227], a[188:191], v[52:55]\n\t"
"v_mfma_f32_16x16x32_bf16 v[52:55], a[228:231], a[192:195], v[52:55]\n\t"
"s_nop 0\n\t"
"v_cvt_pk_bf16_f32 v234, v228, v229\n\t"
"v_cvt_pk_bf16_f32 v235, v230, v231\n\t"
"v_mfma_f32_16x16x32_bf16 v[52:55], v[236:239], a[196:199], v[52:55]\n\t"
"s_mov_b32 s20, 700416\n\t"
"buffer_load_dwordx4 a[252:255], v252, s[8:11], s20 offen offset:0 sc1\n\t"
"v_mfma_f32_16x16x32_bf16 v[56:59], a[224:227], a[192:195], v[56:59]\n\t"
"ds_read_b64_tr_b16 v[228:229], v246 offset:21504\n\t"
"v_mfma_f32_16x16x32_bf16 v[56:59], a[228:231], a[196:199], v[56:59]\n\t"
"ds_read_b64_tr_b16 v[230:231], v246 offset:24576\n\t"
"v_mfma_f32_16x16x32_bf16 v[56:59], v[236:239], a[200:203], v[56:59]\n\t"
"s_waitcnt lgkmcnt(2)\n\t"
"v_mfma_f32_16x16x32_bf16 v[224:227], v[224:227], v[220:223], 0\n\t"
"v_mfma_f32_16x16x32_bf16 v[60:63], a[224:227], a[196:199], v[60:63]\n\t"
"v_mfma_f32_16x16x32_bf16 v[60:63], a[228:231], a[200:203], v[60:63]\n\t"
"v_mfma_f32_16x16x32_bf16 v[60:63], v[236:239], a[204:207], v[60:63]\n\t"
"buffer_load_dwordx4 a[224:227], v252, s[8:11], s20 offen offset:1024 sc1\n\t"
"s_waitcnt vmcnt(11)\n\t"
"v_mfma_f32_16x16x32_bf16 v[64:67], v[240:243], a[168:171], v[64:67]\n\t"
"ds_read_b128 a[168:171], v244 offset:9216\n\t"
"v_mfma_f32_16x16x32_bf16 v[64:67], v[248:251], a[172:175], v[64:67]\n\t"
"v_cvt_pk_bf16_f32 v224, v224, v225\n\t"
"v_cvt_pk_bf16_f32 v225, v226, v227\n\t"
"v_mfma_f32_16x16x32_bf16 v[64:67], a[208:211], a[176:179], v[64:67]\n\t"
"v_mfma_f32_16x16x32_bf16 v[68:71], v[240:243], a[172:175], v[68:71]\n\t"
"ds_read_b128 a[172:175], v244 offset:9728\n\t"
"v_mfma_f32_16x16x32_bf16 v[68:71], v[248:251], a[176:179], v[68:71]\n\t"
"v_mfma_f32_16x16x32_bf16 v[68:71], a[208:211], a[180:183], v[68:71]\n\t"
"buffer_load_dwordx4 v[236:239], v252, s[8:11], s20 offen offset:2048 sc1\n\t"
"s_waitcnt lgkmcnt(2)\n\t"
"v_mfma_f32_16x16x32_bf16 v[228:231], v[228:231], v[220:223], 0\n\t"
"s_mov_b32 s20, 712704\n\t"
"buffer_load_dwordx4 a[228:231], v252, s[8:11], s20 offen offset:0 sc1\n\t"
"v_mfma_f32_16x16x32_bf16 v[72:75], v[240:243], a[176:179], v[72:75]\n\t"
"ds_read_b128 a[176:179], v244 offset:10240\n\t"
"v_mfma_f32_16x16x32_bf16 v[72:75], v[248:251], a[180:183], v[72:75]\n\t"
"v_mfma_f32_16x16x32_bf16 v[72:75], a[208:211], a[184:187], v[72:75]\n\t"
"v_mfma_f32_16x16x32_bf16 v[76:79], v[240:243], a[180:183], v[76:79]\n\t"
"ds_read_b128 a[180:183], v244 offset:10752\n\t"
"v_mfma_f32_16x16x32_bf16 v[76:79], v[248:251], a[184:187], v[76:79]\n\t"
"v_cvt_pk_bf16_f32 v226, v228, v229\n\t"
"v_cvt_pk_bf16_f32 v227, v230, v231\n\t"
"v_mfma_f32_16x16x32_bf16 v[76:79], a[208:211], a[188:191], v[76:79]\n\t"
"ds_read_b64_tr_b16 v[228:229], v246 offset:27648\n\t"
"v_mfma_f32_16x16x32_bf16 v[80:83], v[240:243], a[184:187], v[80:83]\n\t"
"ds_read_b64_tr_b16 v[230:231], v246 offset:30720\n\t"
"v_mfma_f32_16x16x32_bf16 v[80:83], v[248:251], a[188:191], v[80:83]\n\t"
"ds_read_b128 a[184:187], v244 offset:11264\n\t"
"v_mfma_f32_16x16x32_bf16 v[80:83], a[208:211], a[192:195], v[80:83]\n\t"
"v_mfma_f32_16x16x32_bf16 v[84:87], v[240:243], a[188:191], v[84:87]\n\t"
"ds_read_b128 a[188:191], v244 offset:11776\n\t"
"v_mfma_f32_16x16x32_bf16 v[84:87], v[248:251], a[192:195], v[84:87]\n\t"
"v_mfma_f32_16x16x32_bf16 v[84:87], a[208:211], a[196:199], v[84:87]\n\t"
"v_mfma_f32_16x16x32_bf16 v[88:91], v[240:243], a[192:195], v[88:91]\n\t"
"ds_read_b128 a[192:195], v244 offset:12288\n\t"
"v_mfma_f32_16x16x32_bf16 v[88:91], v[248:251], a[196:199], v[88:91]\n\t"
"v_mfma_f32_16x16x32_bf16 v[88:91], a[208:211], a[200:203], v[88:91]\n\t"
"v_mfma_f32_16x16x32_bf16 v[92:95], v[240:243], a[196:199], v[92:95]\n\t"
"ds_read_b128 a[196:199], v244 offset:12800\n\t"
"v_mfma_f32_16x16x32_bf16 v[92:95], v[248:251], a[200:203], v[92:95]\n\t"
"ds_read_b128 a[200:203], v244 offset:13312\n\t"
"v_mfma_f32_16x16x32_bf16 v[92:95], a[208:211], a[204:207], v[92:95]\n\t"
"buffer_load_dwordx4 v[240:243], v252, s[8:11], s20 offen offset:1024 sc1\n\t"
"s_waitcnt lgkmcnt(5)\n\t"
"v_mfma_f32_16x16x32_bf16 v[228:231], v[228:231], v[220:223], 0\n\t"
"ds_read_b128 a[204:207], v244 offset:13824\n\t"
"s_waitcnt vmcnt(11)\n\t"
"v_mfma_f32_16x16x32_bf16 v[100:103], a[212:215], a[168:171], v[100:103]\n\t"
"v_mfma_f32_16x16x32_bf16 v[100:103], a[232:235], a[172:175], v[100:103]\n\t"
"v_mfma_f32_16x16x32_bf16 v[100:103], a[236:239], a[176:179], v[100:103]\n\t"
"buffer_load_dwordx4 v[248:251], v252, s[8:11], s20 offen offset:2048 sc1\n\t"
"v_mfma_f32_16x16x32_bf16 v[104:107], a[212:215], a[172:175], v[104:107]\n\t"
"v_mfma_f32_16x16x32_bf16 v[104:107], a[232:235], a[176:179], v[104:107]\n\t"
"v_cvt_pk_bf16_f32 v228, v228, v229\n\t"
"v_cvt_pk_bf16_f32 v229, v230, v231\n\t"
"v_mfma_f32_16x16x32_bf16 v[104:107], a[236:239], a[180:183], v[104:107]\n\t"
"s_mov_b32 s20, 724992\n\t"
"buffer_load_dwordx4 a[208:211], v252, s[8:11], s20 offen offset:0 sc1\n\t"
"s_waitcnt lgkmcnt(0)\n\t"
"s_barrier\n\t"
"v_mfma_f32_16x16x32_bf16 v[108:111], a[212:215], a[176:179], v[108:111]\n\t"
"v_mfma_f32_16x16x32_bf16 v[108:111], a[232:235], a[180:183], v[108:111]\n\t"
"v_mfma_f32_16x16x32_bf16 v[108:111], a[236:239], a[184:187], v[108:111]\n\t"
"ds_write2st64_b64 v245, v[232:233], v[234:235] offset0:0 offset1:1\n\t"
"v_mfma_f32_16x16x32_bf16 v[112:115], a[212:215], a[180:183], v[112:115]\n\t"
"ds_read_b64_tr_b16 v[232:233], v246 offset:33792\n\t"
"v_mfma_f32_16x16x32_bf16 v[112:115], a[232:235], a[184:187], v[112:115]\n\t"
"ds_read_b64_tr_b16 v[234:235], v246 offset:36864\n\t"
"v_mfma_f32_16x16x32_bf16 v[112:115], a[236:239], a[188:191], v[112:115]\n\t"
"ds_write2st64_b64 v245, v[224:225], v[226:227] offset0:2 offset1:3\n\t"
"v_mfma_f32_16x16x32_bf16 v[116:119], a[212:215], a[184:187], v[116:119]\n\t"
"ds_read_b64_tr_b16 v[224:225], v246 offset:41472\n\t"
"v_mfma_f32_16x16x32_bf16 v[116:119], a[232:235], a[188:191], v[116:119]\n\t"
"ds_read_b64_tr_b16 v[226:227], v246 offset:44544\n\t"
"v_mfma_f32_16x16x32_bf16 v[116:119], a[236:239], a[192:195], v[116:119]\n\t"
"ds_write_b64 v245, v[228:229] offset:2048\n\t"
"v_mfma_f32_16x16x32_bf16 v[120:123], a[212:215], a[188:191], v[120:123]\n\t"
"ds_read_b64_tr_b16 v[228:229], v246 offset:47616\n\t"
"v_mfma_f32_16x16x32_bf16 v[120:123], a[232:235], a[192:195], v[120:123]\n\t"
"ds_read_b64_tr_b16 v[230:231], v246 offset:50688\n\t"
"v_mfma_f32_16x16x32_bf16 v[120:123], a[236:239], a[196:199], v[120:123]\n\t"
"s_waitcnt lgkmcnt(6)\n\t"
"v_mfma_f32_16x16x32_bf16 v[232:235], v[232:235], v[220:223], 0\n\t"
"s_add_u32 m0, s24, 66304\n\t"
"s_add_u32 s28, s21, 1966080\n\t"
"buffer_load_dwordx4 v247, s[12:15], s28 offen offset:256 nt lds\n\t"
"v_mfma_f32_16x16x32_bf16 v[124:127], a[212:215], a[192:195], v[124:127]\n\t"
"v_mfma_f32_16x16x32_bf16 v[124:127], a[232:235], a[196:199], v[124:127]\n\t"
"v_mfma_f32_16x16x32_bf16 v[124:127], a[236:239], a[200:203], v[124:127]\n\t"
"s_add_u32 m0, s24, 72448\n\t"
"s_add_u32 s28, s21, 2162688\n\t"
"buffer_load_dwordx4 v247, s[12:15], s28 offen offset:256 nt lds\n\t"
"s_waitcnt lgkmcnt(3)\n\t"
"v_mfma_f32_16x16x32_bf16 v[224:227], v[224:227], v[220:223], 0\n\t"
"s_add_u32 m0, s24, 80128\n\t"
"s_add_u32 s28, s21, 2359296\n\t"
"buffer_load_dwordx4 v247, s[12:15], s28 offen offset:256 nt lds\n\t"
"v_mfma_f32_16x16x32_bf16 v[128:131], a[212:215], a[196:199], v[128:131]\n\t"
"v_cvt_pk_bf16_f32 v232, v232, v233\n\t"
"v_cvt_pk_bf16_f32 v233, v234, v235\n\t"
"v_mfma_f32_16x16x32_bf16 v[128:131], a[232:235], a[200:203], v[128:131]\n\t"
"v_mfma_f32_16x16x32_bf16 v[128:131], a[236:239], a[204:207], v[128:131]\n\t"
"s_add_u32 m0, s24, 86272\n\t"
"s_add_u32 s28, s21, 2555904\n\t"
"buffer_load_dwordx4 v247, s[12:15], s28 offen offset:256 nt lds\n\t"
"s_waitcnt lgkmcnt(0)\n\t"
"v_mfma_f32_16x16x32_bf16 v[228:231], v[228:231], v[220:223], 0\n\t"
"s_add_u32 m0, s24, 32512\n\t"
"s_add_u32 s28, s21, 983040\n\t"
"buffer_load_dwordx4 v247, s[12:15], s28 offen offset:256 nt lds\n\t"
"s_waitcnt vmcnt(15)\n\t"
"v_mfma_f32_16x16x32_bf16 v[132:135], v[96:99], a[168:171], v[132:135]\n\t"
"v_cvt_pk_bf16_f32 v234, v224, v225\n\t"
"v_cvt_pk_bf16_f32 v235, v226, v227\n\t"
"v_mfma_f32_16x16x32_bf16 v[132:135], a[216:219], a[172:175], v[132:135]\n\t"
"ds_read_b64_tr_b16 v[224:225], v246 offset:53760\n\t"
"v_mfma_f32_16x16x32_bf16 v[132:135], a[220:223], a[176:179], v[132:135]\n\t"
"s_add_u32 m0, s24, 40192\n\t"
"s_add_u32 s28, s21, 1179648\n\t"
"buffer_load_dwordx4 v247, s[12:15], s28 offen offset:256 nt lds\n\t"
"v_mfma_f32_16x16x32_bf16 v[136:139], v[96:99], a[172:175], v[136:139]\n\t"
"ds_read_b64_tr_b16 v[226:227], v246 offset:56832\n\t"
"v_mfma_f32_16x16x32_bf16 v[136:139], a[216:219], a[176:179], v[136:139]\n\t"
"v_cvt_pk_bf16_f32 v228, v228, v229\n\t"
"v_cvt_pk_bf16_f32 v229, v230, v231\n\t"
"v_mfma_f32_16x16x32_bf16 v[136:139], a[220:223], a[180:183], v[136:139]\n\t"
"ds_write2st64_b64 v245, v[232:233], v[234:235] offset0:5 offset1:6\n\t"
"v_mfma_f32_16x16x32_bf16 v[140:143], v[96:99], a[176:179], v[140:143]\n\t"
"v_mfma_f32_16x16x32_bf16 v[140:143], a[216:219], a[180:183], v[140:143]\n\t"
"v_mfma_f32_16x16x32_bf16 v[140:143], a[220:223], a[184:187], v[140:143]\n\t"
"v_mfma_f32_16x16x32_bf16 v[144:147], v[96:99], a[180:183], v[144:147]\n\t"
"v_mfma_f32_16x16x32_bf16 v[144:147], a[216:219], a[184:187], v[144:147]\n\t"
"v_mfma_f32_16x16x32_bf16 v[144:147], a[220:223], a[188:191], v[144:147]\n\t"
"v_mfma_f32_16x16x32_bf16 v[148:151], v[96:99], a[184:187], v[148:151]\n\t"
"v_mfma_f32_16x16x32_bf16 v[148:151], a[216:219], a[188:191], v[148:151]\n\t"
"v_mfma_f32_16x16x32_bf16 v[148:151], a[220:223], a[192:195], v[148:151]\n\t"
"s_waitcnt lgkmcnt(1)\n\t"
"v_mfma_f32_16x16x32_bf16 v[224:227], v[224:227], v[220:223], 0\n\t"
"v_mfma_f32_16x16x32_bf16 v[152:155], v[96:99], a[188:191], v[152:155]\n\t"
"v_mfma_f32_16x16x32_bf16 v[152:155], a[216:219], a[192:195], v[152:155]\n\t"
"v_mfma_f32_16x16x32_bf16 v[152:155], a[220:223], a[196:199], v[152:155]\n\t"
"v_mfma_f32_16x16x32_bf16 v[156:159], v[96:99], a[192:195], v[156:159]\n\t"
"v_mfma_f32_16x16x32_bf16 v[156:159], a[216:219], a[196:199], v[156:159]\n\t"
"s_nop 2\n\t"
"v_cvt_pk_bf16_f32 v230, v224, v225\n\t"
"v_cvt_pk_bf16_f32 v231, v226, v227\n\t"
"v_mfma_f32_16x16x32_bf16 v[156:159], a[220:223], a[200:203], v[156:159]\n\t"
"ds_write2st64_b64 v245, v[228:229], v[230:231] offset0:7 offset1:8\n\t"
"v_mfma_f32_16x16x32_bf16 v[160:163], v[96:99], a[196:199], v[160:163]\n\t"
"v_mfma_f32_16x16x32_bf16 v[160:163], a[216:219], a[200:203], v[160:163]\n\t"
"v_mfma_f32_16x16x32_bf16 v[160:163], a[220:223], a[204:207], v[160:163]\n\t"
"s_waitcnt vmcnt(13)\n\t"
"v_mfma_f32_16x16x32_bf16 v[164:167], a[240:243], a[168:171], v[164:167]\n\t"
"v_mfma_f32_16x16x32_bf16 v[164:167], a[244:247], a[172:175], v[164:167]\n\t"
"v_mfma_f32_16x16x32_bf16 v[164:167], a[248:251], a[176:179], v[164:167]\n\t"
"buffer_load_dwordx4 v[96:99], v252, s[8:11], s20 offen offset:1024 sc1\n\t"
"s_waitcnt lgkmcnt(0)\n\t"
"s_barrier\n\t"
"v_mfma_f32_16x16x32_bf16 v[168:171], a[240:243], a[172:175], v[168:171]\n\t"
"ds_read_b128 a[168:171], v244 offset:0\n\t"
"v_mfma_f32_16x16x32_bf16 v[168:171], a[244:247], a[176:179], v[168:171]\n\t"
"v_mfma_f32_16x16x32_bf16 v[168:171], a[248:251], a[180:183], v[168:171]\n\t"
"ds_read_b128 a[172:175], v244 offset:512\n\t"
"v_mfma_f32_16x16x32_bf16 v[172:175], a[240:243], a[176:179], v[172:175]\n\t"
"ds_read_b128 a[176:179], v244 offset:1024\n\t"
"v_mfma_f32_16x16x32_bf16 v[172:175], a[244:247], a[180:183], v[172:175]\n\t"
"v_mfma_f32_16x16x32_bf16 v[172:175], a[248:251], a[184:187], v[172:175]\n\t"
"v_mfma_f32_16x16x32_bf16 v[176:179], a[240:243], a[180:183], v[176:179]\n\t"
"ds_read_b128 a[180:183], v244 offset:1536\n\t"
"v_mfma_f32_16x16x32_bf16 v[176:179], a[244:247], a[184:187], v[176:179]\n\t"
"v_mfma_f32_16x16x32_bf16 v[176:179], a[248:251], a[188:191], v[176:179]\n\t"
"v_mfma_f32_16x16x32_bf16 v[180:183], a[240:243], a[184:187], v[180:183]\n\t"
"ds_read_b128 a[184:187], v244 offset:2048\n\t"
"v_mfma_f32_16x16x32_bf16 v[180:183], a[244:247], a[188:191], v[180:183]\n\t"
"v_mfma_f32_16x16x32_bf16 v[180:183], a[248:251], a[192:195], v[180:183]\n\t"
"v_mfma_f32_16x16x32_bf16 v[184:187], a[240:243], a[188:191], v[184:187]\n\t"
"ds_read_b128 a[188:191], v244 offset:2560\n\t"
"v_mfma_f32_16x16x32_bf16 v[184:187], a[244:247], a[192:195], v[184:187]\n\t"
"v_mfma_f32_16x16x32_bf16 v[184:187], a[248:251], a[196:199], v[184:187]\n\t"
"v_mfma_f32_16x16x32_bf16 v[188:191], a[240:243], a[192:195], v[188:191]\n\t"
"ds_read_b128 a[192:195], v244 offset:3072\n\t"
"v_mfma_f32_16x16x32_bf16 v[188:191], a[244:247], a[196:199], v[188:191]\n\t"
"v_mfma_f32_16x16x32_bf16 v[188:191], a[248:251], a[200:203], v[188:191]\n\t"
"v_mfma_f32_16x16x32_bf16 v[192:195], a[240:243], a[196:199], v[192:195]\n\t"
"ds_read_b128 a[196:199], v244 offset:3584\n\t"
"v_mfma_f32_16x16x32_bf16 v[192:195], a[244:247], a[200:203], v[192:195]\n\t"
"ds_read_b128 a[200:203], v244 offset:4096\n\t"
"v_mfma_f32_16x16x32_bf16 v[192:195], a[248:251], a[204:207], v[192:195]\n\t"
"Lst8_%=:\n\t"
"s_waitcnt vmcnt(11) lgkmcnt(6)\n\t"
"v_mfma_f32_16x16x32_bf16 v[0:3], a[252:255], a[168:171], v[0:3]\n\t"
"ds_read_b128 a[204:207], v244 offset:4608\n\t"
"v_mfma_f32_16x16x32_bf16 v[0:3], a[224:227], a[172:175], v[0:3]\n\t"
"v_xor_b32 v246, 0xc0, v246\n\t"
"v_mfma_f32_16x16x32_bf16 v[0:3], v[236:239], a[176:179], v[0:3]\n\t"
"s_waitcnt lgkmcnt(6)\n\t"
"v_mfma_f32_16x16x32_bf16 v[4:7], a[252:255], a[172:175], v[4:7]\n\t"
"v_mfma_f32_16x16x32_bf16 v[4:7], a[224:227], a[176:179], v[4:7]\n\t"
"v_mfma_f32_16x16x32_bf16 v[4:7], v[236:239], a[180:183], v[4:7]\n\t"
"s_waitcnt lgkmcnt(5)\n\t"
"v_mfma_f32_16x16x32_bf16 v[8:11], a[252:255], a[176:179], v[8:11]\n\t"
"v_mfma_f32_16x16x32_bf16 v[8:11], a[224:227], a[180:183], v[8:11]\n\t"
"v_mfma_f32_16x16x32_bf16 v[8:11], v[236:239], a[184:187], v[8:11]\n\t"
"s_waitcnt lgkmcnt(4)\n\t"
"v_mfma_f32_16x16x32_bf16 v[12:15], a[252:255], a[180:183], v[12:15]\n\t"
"v_mfma_f32_16x16x32_bf16 v[12:15], a[224:227], a[184:187], v[12:15]\n\t"
"v_mfma_f32_16x16x32_bf16 v[12:15], v[236:239], a[188:191], v[12:15]\n\t"
"buffer_load_dwordx4 a[212:215], v252, s[8:11], s20 offen offset:2048 sc1\n\t"
"s_waitcnt lgkmcnt(3)\n\t"
"v_mfma_f32_16x16x32_bf16 v[16:19], a[252:255], a[184:187], v[16:19]\n\t"
"v_mfma_f32_16x16x32_bf16 v[16:19], a[224:227], a[188:191], v[16:19]\n\t"
"v_mfma_f32_16x16x32_bf16 v[16:19], v[236:239], a[192:195], v[16:19]\n\t"
"ds_read_b64_tr_b16 v[232:233], v246 offset:1536\n\t"
"ds_read_b64_tr_b16 v[234:235], v246 offset:4608\n\t"
"s_waitcnt lgkmcnt(4)\n\t"
"v_mfma_f32_16x16x32_bf16 v[20:23], a[252:255], a[188:191], v[20:23]\n\t"
"v_mfma_f32_16x16x32_bf16 v[20:23], a[224:227], a[192:195], v[20:23]\n\t"
"v_mfma_f32_16x16x32_bf16 v[20:23], v[236:239], a[196:199], v[20:23]\n\t"
"s_mov_b32 s20, 737280\n\t"
"buffer_load_dwordx4 a[216:219], v252, s[8:11], s20 offen offset:0 sc1\n\t"
"s_waitcnt lgkmcnt(3)\n\t"
"v_mfma_f32_16x16x32_bf16 v[24:27], a[252:255], a[192:195], v[24:27]\n\t"
"v_mfma_f32_16x16x32_bf16 v[24:27], a[224:227], a[196:199], v[24:27]\n\t"
"v_mfma_f32_16x16x32_bf16 v[24:27], v[236:239], a[200:203], v[24:27]\n\t"
"buffer_load_dwordx4 a[220:223], v252, s[8:11], s20 offen offset:1024 sc1\n\t"
"s_waitcnt lgkmcnt(2)\n\t"
"v_mfma_f32_16x16x32_bf16 v[28:31], a[252:255], a[196:199], v[28:31]\n\t"
"ds_read_b64_tr_b16 v[224:225], v246 offset:7680\n\t"
"v_mfma_f32_16x16x32_bf16 v[28:31], a[224:227], a[200:203], v[28:31]\n\t"
"ds_read_b64_tr_b16 v[226:227], v246 offset:10752\n\t"
"v_mfma_f32_16x16x32_bf16 v[28:31], v[236:239], a[204:207], v[28:31]\n\t"
"s_waitcnt vmcnt(11)\n\t"
"v_mfma_f32_16x16x32_bf16 v[32:35], a[228:231], a[168:171], v[32:35]\n\t"
"v_mfma_f32_16x16x32_bf16 v[32:35], v[240:243], a[172:175], v[32:35]\n\t"
"v_mfma_f32_16x16x32_bf16 v[32:35], v[248:251], a[176:179], v[32:35]\n\t"
"buffer_load_dwordx4 a[224:227], v252, s[8:11], s20 offen offset:2048 sc1\n\t"
"v_mfma_f32_16x16x32_bf16 v[36:39], a[228:231], a[172:175], v[36:39]\n\t"
"v_mfma_f32_16x16x32_bf16 v[36:39], v[240:243], a[176:179], v[36:39]\n\t"
"v_mfma_f32_16x16x32_bf16 v[36:39], v[248:251], a[180:183], v[36:39]\n\t"
"s_mov_b32 s20, 749568\n\t"
"buffer_load_dwordx4 v[236:239], v252, s[8:11], s20 offen offset:0 sc1\n\t"
"s_waitcnt lgkmcnt(2)\n\t"
"v_mfma_f32_16x16x32_bf16 v[232:235], v[232:235], v[220:223], 0\n\t"
"buffer_load_dwordx4 a[232:235], v252, s[8:11], s20 offen offset:1024 sc1\n\t"
"v_mfma_f32_16x16x32_bf16 v[40:43], a[228:231], a[176:179], v[40:43]\n\t"
"ds_read_b64_tr_b16 v[228:229], v246 offset:13824\n\t"
"v_mfma_f32_16x16x32_bf16 v[40:43], v[240:243], a[180:183], v[40:43]\n\t"
"v_mfma_f32_16x16x32_bf16 v[40:43], v[248:251], a[184:187], v[40:43]\n\t"
"ds_read_b64_tr_b16 v[230:231], v246 offset:16896\n\t"
"v_mfma_f32_16x16x32_bf16 v[44:47], a[228:231], a[180:183], v[44:47]\n\t"
"v_mfma_f32_16x16x32_bf16 v[44:47], v[240:243], a[184:187], v[44:47]\n\t"
"v_cvt_pk_bf16_f32 v232, v232, v233\n\t"
"v_cvt_pk_bf16_f32 v233, v234, v235\n\t"
"v_mfma_f32_16x16x32_bf16 v[44:47], v[248:251], a[188:191], v[44:47]\n\t"
"s_waitcnt lgkmcnt(2)\n\t"
"v_mfma_f32_16x16x32_bf16 v[224:227], v[224:227], v[220:223], 0\n\t"
"buffer_load_dwordx4 a[236:239], v252, s[8:11], s20 offen offset:2048 sc1\n\t"
"v_mfma_f32_16x16x32_bf16 v[48:51], a[228:231], a[184:187], v[48:51]\n\t"
"v_mfma_f32_16x16x32_bf16 v[48:51], v[240:243], a[188:191], v[48:51]\n\t"
"v_mfma_f32_16x16x32_bf16 v[48:51], v[248:251], a[192:195], v[48:51]\n\t"
"s_mov_b32 s20, 761856\n\t"
"buffer_load_dwordx4 a[240:243], v252, s[8:11], s20 offen offset:0 sc1\n\t"
"v_mfma_f32_16x16x32_bf16 v[52:55], a[228:231], a[188:191], v[52:55]\n\t"
"v_mfma_f32_16x16x32_bf16 v[52:55], v[240:243], a[192:195], v[52:55]\n\t"
"v_cvt_pk_bf16_f32 v234, v224, v225\n\t"
"v_cvt_pk_bf16_f32 v235, v226, v227\n\t"
"v_mfma_f32_16x16x32_bf16 v[52:55], v[248:251], a[196:199], v[52:55]\n\t"
"buffer_load_dwordx4 a[244:247], v252, s[8:11], s20 offen offset:1024 sc1\n\t"
"v_mfma_f32_16x16x32_bf16 v[56:59], a[228:231], a[192:195], v[56:59]\n\t"
"ds_read_b64_tr_b16 v[224:225], v246 offset:21504\n\t"
"v_mfma_f32_16x16x32_bf16 v[56:59], v[240:243], a[196:199], v[56:59]\n\t"
"ds_read_b64_tr_b16 v[226:227], v246 offset:24576\n\t"
"v_mfma_f32_16x16x32_bf16 v[56:59], v[248:251], a[200:203], v[56:59]\n\t"
"s_waitcnt lgkmcnt(2)\n\t"
"v_mfma_f32_16x16x32_bf16 v[228:231], v[228:231], v[220:223], 0\n\t"
"buffer_load_dwordx4 a[248:251], v252, s[8:11], s20 offen offset:2048 sc1\n\t"
"v_mfma_f32_16x16x32_bf16 v[60:63], a[228:231], a[196:199], v[60:63]\n\t"
"v_mfma_f32_16x16x32_bf16 v[60:63], v[240:243], a[200:203], v[60:63]\n\t"
"v_mfma_f32_16x16x32_bf16 v[60:63], v[248:251], a[204:207], v[60:63]\n\t"
"s_mov_b32 s20, 774144\n\t"
"buffer_load_dwordx4 v[240:243], v252, s[8:11], s20 offen offset:0 sc1\n\t"
"s_waitcnt vmcnt(10)\n\t"
"v_mfma_f32_16x16x32_bf16 v[64:67], a[208:211], a[168:171], v[64:67]\n\t"
"ds_read_b128 a[168:171], v244 offset:9216\n\t"
"v_mfma_f32_16x16x32_bf16 v[64:67], v[96:99], a[172:175], v[64:67]\n\t"
"v_cvt_pk_bf16_f32 v228, v228, v229\n\t"
"v_mfma_f32_16x16x32_bf16 v[64:67], a[212:215], a[176:179], v[64:67]\n\t"
"v_cvt_pk_bf16_f32 v229, v230, v231\n\t"
"v_mfma_f32_16x16x32_bf16 v[68:71], a[208:211], a[172:175], v[68:71]\n\t"
"ds_read_b128 a[172:175], v244 offset:9728\n\t"
"v_mfma_f32_16x16x32_bf16 v[68:71], v[96:99], a[176:179], v[68:71]\n\t"
"v_mfma_f32_16x16x32_bf16 v[68:71], a[212:215], a[180:183], v[68:71]\n\t"
"buffer_load_dwordx4 v[248:251], v252, s[8:11], s20 offen offset:1024 sc1\n\t"
"s_waitcnt lgkmcnt(2)\n\t"
"v_mfma_f32_16x16x32_bf16 v[224:227], v[224:227], v[220:223], 0\n\t"
"v_mfma_f32_16x16x32_bf16 v[72:75], a[208:211], a[176:179], v[72:75]\n\t"
"ds_read_b128 a[176:179], v244 offset:10240\n\t"
"v_mfma_f32_16x16x32_bf16 v[72:75], v[96:99], a[180:183], v[72:75]\n\t"
"v_mfma_f32_16x16x32_bf16 v[72:75], a[212:215], a[184:187], v[72:75]\n\t"
"buffer_load_dwordx4 a[228:231], v252, s[8:11], s20 offen offset:2048 sc1\n\t"
"v_mfma_f32_16x16x32_bf16 v[76:79], a[208:211], a[180:183], v[76:79]\n\t"
"ds_read_b128 a[180:183], v244 offset:10752\n\t"
"v_mfma_f32_16x16x32_bf16 v[76:79], v[96:99], a[184:187], v[76:79]\n\t"
"v_cvt_pk_bf16_f32 v230, v224, v225\n\t"
"v_cvt_pk_bf16_f32 v231, v226, v227\n\t"
"v_mfma_f32_16x16x32_bf16 v[76:79], a[212:215], a[188:191], v[76:79]\n\t"
"s_mov_b32 s20, 786432\n\t"
"buffer_load_dwordx4 a[252:255], v252, s[8:11], s20 offen offset:0 sc1\n\t"
"v_mfma_f32_16x16x32_bf16 v[80:83], a[208:211], a[184:187], v[80:83]\n\t"
"ds_read_b64_tr_b16 v[224:225], v246 offset:27648\n\t"
"v_mfma_f32_16x16x32_bf16 v[80:83], v[96:99], a[188:191], v[80:83]\n\t"
"ds_read_b64_tr_b16 v[226:227], v246 offset:30720\n\t"
"v_mfma_f32_16x16x32_bf16 v[80:83], a[212:215], a[192:195], v[80:83]\n\t"
"ds_read_b128 a[184:187], v244 offset:11264\n\t"
"v_mfma_f32_16x16x32_bf16 v[84:87], a[208:211], a[188:191], v[84:87]\n\t"
"ds_read_b128 a[188:191], v244 offset:11776\n\t"
"v_mfma_f32_16x16x32_bf16 v[84:87], v[96:99], a[192:195], v[84:87]\n\t"
"v_mfma_f32_16x16x32_bf16 v[84:87], a[212:215], a[196:199], v[84:87]\n\t"
"v_mfma_f32_16x16x32_bf16 v[88:91], a[208:211], a[192:195], v[88:91]\n\t"
"ds_read_b128 a[192:195], v244 offset:12288\n\t"
"v_mfma_f32_16x16x32_bf16 v[88:91], v[96:99], a[196:199], v[88:91]\n\t"
"v_mfma_f32_16x16x32_bf16 v[88:91], a[212:215], a[200:203], v[88:91]\n\t"
"v_mfma_f32_16x16x32_bf16 v[92:95], a[208:211], a[196:199], v[92:95]\n\t"
"ds_read_b128 a[196:199], v244 offset:12800\n\t"
"v_mfma_f32_16x16x32_bf16 v[92:95], v[96:99], a[200:203], v[92:95]\n\t"
"ds_read_b128 a[200:203], v244 offset:13312\n\t"
"v_mfma_f32_16x16x32_bf16 v[92:95], a[212:215], a[204:207], v[92:95]\n\t"
"buffer_load_dwordx4 a[208:211], v252, s[8:11], s20 offen offset:1024 sc1\n\t"
"s_waitcnt lgkmcnt(5)\n\t"
"v_mfma_f32_16x16x32_bf16 v[224:227], v[224:227], v[220:223], 0\n\t"
"ds_read_b128 a[204:207], v244 offset:13824\n\t"
"s_waitcnt vmcnt(11)\n\t"
"v_mfma_f32_16x16x32_bf16 v[100:103], a[216:219], a[168:171], v[100:103]\n\t"
"v_mfma_f32_16x16x32_bf16 v[100:103], a[220:223], a[172:175], v[100:103]\n\t"
"v_mfma_f32_16x16x32_bf16 v[100:103], a[224:227], a[176:179], v[100:103]\n\t"
"buffer_load_dwordx4 v[96:99], v252, s[8:11], s20 offen offset:2048 sc1\n\t"
"v_mfma_f32_16x16x32_bf16 v[104:107], a[216:219], a[172:175], v[104:107]\n\t"
"v_mfma_f32_16x16x32_bf16 v[104:107], a[220:223], a[176:179], v[104:107]\n\t"
"v_cvt_pk_bf16_f32 v224, v224, v225\n\t"
"v_cvt_pk_bf16_f32 v225, v226, v227\n\t"
"v_mfma_f32_16x16x32_bf16 v[104:107], a[224:227], a[180:183], v[104:107]\n\t"
"s_mov_b32 s20, 798720\n\t"
"buffer_load_dwordx4 a[212:215], v252, s[8:11], s20 offen offset:0 sc1\n\t"
"s_waitcnt lgkmcnt(0)\n\t"
"s_barrier\n\t"
"v_mfma_f32_16x16x32_bf16 v[108:111], a[216:219], a[176:179], v[108:111]\n\t"
"v_mfma_f32_16x16x32_bf16 v[108:111], a[220:223], a[180:183], v[108:111]\n\t"
"v_mfma_f32_16x16x32_bf16 v[108:111], a[224:227], a[184:187], v[108:111]\n\t"
"ds_write2st64_b64 v245, v[232:233], v[234:235] offset0:0 offset1:1\n\t"
"v_mfma_f32_16x16x32_bf16 v[112:115], a[216:219], a[180:183], v[112:115]\n\t"
"ds_read_b64_tr_b16 v[232:233], v246 offset:33792\n\t"
"v_mfma_f32_16x16x32_bf16 v[112:115], a[220:223], a[184:187], v[112:115]\n\t"
"ds_read_b64_tr_b16 v[234:235], v246 offset:36864\n\t"
"v_mfma_f32_16x16x32_bf16 v[112:115], a[224:227], a[188:191], v[112:115]\n\t"
"ds_write2st64_b64 v245, v[228:229], v[230:231] offset0:2 offset1:3\n\t"
"v_mfma_f32_16x16x32_bf16 v[116:119], a[216:219], a[184:187], v[116:119]\n\t"
"ds_read_b64_tr_b16 v[228:229], v246 offset:41472\n\t"
"v_mfma_f32_16x16x32_bf16 v[116:119], a[220:223], a[188:191], v[116:119]\n\t"
"ds_read_b64_tr_b16 v[230:231], v246 offset:44544\n\t"
"v_mfma_f32_16x16x32_bf16 v[116:119], a[224:227], a[192:195], v[116:119]\n\t"
"ds_write_b64 v245, v[224:225] offset:2048\n\t"
"v_mfma_f32_16x16x32_bf16 v[120:123], a[216:219], a[188:191], v[120:123]\n\t"
"ds_read_b64_tr_b16 v[224:225], v246 offset:47616\n\t"
"v_mfma_f32_16x16x32_bf16 v[120:123], a[220:223], a[192:195], v[120:123]\n\t"
"ds_read_b64_tr_b16 v[226:227], v246 offset:50688\n\t"
"v_mfma_f32_16x16x32_bf16 v[120:123], a[224:227], a[196:199], v[120:123]\n\t"
"s_waitcnt lgkmcnt(6)\n\t"
"v_mfma_f32_16x16x32_bf16 v[232:235], v[232:235], v[220:223], 0\n\t"
"v_mfma_f32_16x16x32_bf16 v[124:127], a[216:219], a[192:195], v[124:127]\n\t"
"v_mfma_f32_16x16x32_bf16 v[124:127], a[220:223], a[196:199], v[124:127]\n\t"
"v_mfma_f32_16x16x32_bf16 v[124:127], a[224:227], a[200:203], v[124:127]\n\t"
"s_waitcnt lgkmcnt(3)\n\t"
"v_mfma_f32_16x16x32_bf16 v[228:231], v[228:231], v[220:223], 0\n\t"
"v_mfma_f32_16x16x32_bf16 v[128:131], a[216:219], a[196:199], v[128:131]\n\t"
"s_nop 1\n\t"
"v_cvt_pk_bf16_f32 v232, v232, v233\n\t"
"v_cvt_pk_bf16_f32 v233, v234, v235\n\t"
"v_mfma_f32_16x16x32_bf16 v[128:131], a[220:223], a[200:203], v[128:131]\n\t"
"v_mfma_f32_16x16x32_bf16 v[128:131], a[224:227], a[204:207], v[128:131]\n\t"
"buffer_load_dwordx4 a[216:219], v252, s[8:11], s20 offen offset:1024 sc1\n\t"
"s_waitcnt lgkmcnt(0)\n\t"
"v_mfma_f32_16x16x32_bf16 v[224:227], v[224:227], v[220:223], 0\n\t"
"buffer_load_dwordx4 a[220:223], v252, s[8:11], s20 offen offset:2048 sc1\n\t"
"s_waitcnt vmcnt(12)\n\t"
"v_mfma_f32_16x16x32_bf16 v[132:135], v[236:239], a[168:171], v[132:135]\n\t"
"v_cvt_pk_bf16_f32 v234, v228, v229\n\t"
"v_cvt_pk_bf16_f32 v235, v230, v231\n\t"
"v_mfma_f32_16x16x32_bf16 v[132:135], a[232:235], a[172:175], v[132:135]\n\t"
"ds_read_b64_tr_b16 v[228:229], v246 offset:53760\n\t"
"v_mfma_f32_16x16x32_bf16 v[132:135], a[236:239], a[176:179], v[132:135]\n\t"
"s_mov_b32 s20, 811008\n\t"
"buffer_load_dwordx4 a[224:227], v252, s[8:11], s20 offen offset:0 sc1\n\t"
"v_mfma_f32_16x16x32_bf16 v[136:139], v[236:239], a[172:175], v[136:139]\n\t"
"ds_read_b64_tr_b16 v[230:231], v246 offset:56832\n\t"
"v_mfma_f32_16x16x32_bf16 v[136:139], a[232:235], a[176:179], v[136:139]\n\t"
"v_cvt_pk_bf16_f32 v224, v224, v225\n\t"
"v_cvt_pk_bf16_f32 v225, v226, v227\n\t"
"v_mfma_f32_16x16x32_bf16 v[136:139], a[236:239], a[180:183], v[136:139]\n\t"
"s_add_u32 m0, s24, 46336\n\t"
"s_add_u32 s28, s21, 1376256\n\t"
"buffer_load_dwordx4 v247, s[12:15], s28 offen offset:256 nt lds\n\t"
"s_add_u32 m0, s24, 52480\n\t"
"s_add_u32 s28, s21, 1572864\n\t"
"buffer_load_dwordx4 v247, s[12:15], s28 offen offset:256 nt lds\n\t"
"v_mfma_f32_16x16x32_bf16 v[140:143], v[236:239], a[176:179], v[140:143]\n\t"
"v_mfma_f32_16x16x32_bf16 v[140:143], a[232:235], a[180:183], v[140:143]\n\t"
"v_mfma_f32_16x16x32_bf16 v[140:143], a[236:239], a[184:187], v[140:143]\n\t"
"ds_write2st64_b64 v245, v[232:233], v[234:235] offset0:5 offset1:6\n\t"
"s_add_u32 m0, s24, 92416\n\t"
"s_add_u32 s28, s21, 2752512\n\t"
"buffer_load_dwordx4 v247, s[12:15], s28 offen offset:256 nt lds\n\t"
"v_mfma_f32_16x16x32_bf16 v[144:147], v[236:239], a[180:183], v[144:147]\n\t"
"v_mfma_f32_16x16x32_bf16 v[144:147], a[232:235], a[184:187], v[144:147]\n\t"
"v_mfma_f32_16x16x32_bf16 v[144:147], a[236:239], a[188:191], v[144:147]\n\t"
"s_add_u32 m0, s24, 100096\n\t"
"s_add_u32 s28, s21, 2949120\n\t"
"buffer_load_dwordx4 v247, s[12:15], s28 offen offset:256 nt lds\n\t"
"s_add_u32 m0, s24, 106240\n\t"
"s_add_u32 s28, s21, 3145728\n\t"
"buffer_load_dwordx4 v247, s[12:15], s28 offen offset:256 nt lds\n\t"
"v_mfma_f32_16x16x32_bf16 v[148:151], v[236:239], a[184:187], v[148:151]\n\t"
"v_mfma_f32_16x16x32_bf16 v[148:151], a[232:235], a[188:191], v[148:151]\n\t"
"v_mfma_f32_16x16x32_bf16 v[148:151], a[236:239], a[192:195], v[148:151]\n\t"
"s_add_u32 m0, s24, 112384\n\t"
"s_add_u32 s28, s21, 3342336\n\t"
"buffer_load_dwordx4 v247, s[12:15], s28 offen offset:256 nt lds\n\t"
"s_waitcnt lgkmcnt(1)\n\t"
"v_mfma_f32_16x16x32_bf16 v[228:231], v[228:231], v[220:223], 0\n\t"
"s_add_u32 m0, s25, 256\n\t"
"s_nop 0\n\t"
"buffer_load_dwordx4 v254, s[12:15], 0 offen offset:256 nt lds\n\t"
"v_mfma_f32_16x16x32_bf16 v[152:155], v[236:239], a[188:191], v[152:155]\n\t"
"v_mfma_f32_16x16x32_bf16 v[152:155], a[232:235], a[192:195], v[152:155]\n\t"
"v_mfma_f32_16x16x32_bf16 v[152:155], a[236:239], a[196:199], v[152:155]\n\t"
"s_add_u32 m0, s26, 256\n\t"
"s_mov_b64 exec, s[36:37]\n\t"
"buffer_load_dwordx4 v254, s[12:15], s27 offen offset:256 nt lds\n\t"
"s_mov_b64 exec, -1\n\t"
"v_mfma_f32_16x16x32_bf16 v[156:159], v[236:239], a[192:195], v[156:159]\n\t"
"v_mfma_f32_16x16x32_bf16 v[156:159], a[232:235], a[196:199], v[156:159]\n\t"
"v_cvt_pk_bf16_f32 v226, v228, v229\n\t"
"v_cvt_pk_bf16_f32 v227, v230, v231\n\t"
"v_mfma_f32_16x16x32_bf16 v[156:159], a[236:239], a[200:203], v[156:159]\n\t"
"ds_write2st64_b64 v245, v[224:225], v[226:227] offset0:7 offset1:8\n\t"
"v_mfma_f32_16x16x32_bf16 v[160:163], v[236:239], a[196:199], v[160:163]\n\t"
"v_mfma_f32_16x16x32_bf16 v[160:163], a[232:235], a[200:203], v[160:163]\n\t"
"v_mfma_f32_16x16x32_bf16 v[160:163], a[236:239], a[204:207], v[160:163]\n\t"
"s_waitcnt vmcnt(18)\n\t"
"v_mfma_f32_16x16x32_bf16 v[164:167], a[240:243], a[168:171], v[164:167]\n\t"
"v_mfma_f32_16x16x32_bf16 v[164:167], a[244:247], a[172:175], v[164:167]\n\t"
"v_mfma_f32_16x16x32_bf16 v[164:167], a[248:251], a[176:179], v[164:167]\n\t"
"s_waitcnt lgkmcnt(0)\n\t"
"s_barrier\n\t"
"v_mfma_f32_16x16x32_bf16 v[168:171], a[240:243], a[172:175], v[168:171]\n\t"
"ds_read_b128 a[168:171], v244 offset:0\n\t"
"v_mfma_f32_16x16x32_bf16 v[168:171], a[244:247], a[176:179], v[168:171]\n\t"
"ds_read_b128 a[172:175], v244 offset:512\n\t"
"v_mfma_f32_16x16x32_bf16 v[168:171], a[248:251], a[180:183], v[168:171]\n\t"
"v_mfma_f32_16x16x32_bf16 v[172:175], a[240:243], a[176:179], v[172:175]\n\t"
"ds_read_b128 a[176:179], v244 offset:1024\n\t"
"v_mfma_f32_16x16x32_bf16 v[172:175], a[244:247], a[180:183], v[172:175]\n\t"
"v_mfma_f32_16x16x32_bf16 v[172:175], a[248:251], a[184:187], v[172:175]\n\t"
"v_mfma_f32_16x16x32_bf16 v[176:179], a[240:243], a[180:183], v[176:179]\n\t"
"ds_read_b128 a[180:183], v244 offset:1536\n\t"
"v_mfma_f32_16x16x32_bf16 v[176:179], a[244:247], a[184:187], v[176:179]\n\t"
"v_mfma_f32_16x16x32_bf16 v[176:179], a[248:251], a[188:191], v[176:179]\n\t"
"v_mfma_f32_16x16x32_bf16 v[180:183], a[240:243], a[184:187], v[180:183]\n\t"
"ds_read_b128 a[184:187], v244 offset:2048\n\t"
"v_mfma_f32_16x16x32_bf16 v[180:183], a[244:247], a[188:191], v[180:183]\n\t"
"v_mfma_f32_16x16x32_bf16 v[180:183], a[248:251], a[192:195], v[180:183]\n\t"
"v_mfma_f32_16x16x32_bf16 v[184:187], a[240:243], a[188:191], v[184:187]\n\t"
"ds_read_b128 a[188:191], v244 offset:2560\n\t"
"v_mfma_f32_16x16x32_bf16 v[184:187], a[244:247], a[192:195], v[184:187]\n\t"
"v_mfma_f32_16x16x32_bf16 v[184:187], a[248:251], a[196:199], v[184:187]\n\t"
"v_mfma_f32_16x16x32_bf16 v[188:191], a[240:243], a[192:195], v[188:191]\n\t"
"ds_read_b128 a[192:195], v244 offset:3072\n\t"
"v_mfma_f32_16x16x32_bf16 v[188:191], a[244:247], a[196:199], v[188:191]\n\t"
"v_mfma_f32_16x16x32_bf16 v[188:191], a[248:251], a[200:203], v[188:191]\n\t"
"v_mfma_f32_16x16x32_bf16 v[192:195], a[240:243], a[196:199], v[192:195]\n\t"
"ds_read_b128 a[196:199], v244 offset:3584\n\t"
"v_mfma_f32_16x16x32_bf16 v[192:195], a[244:247], a[200:203], v[192:195]\n\t"
"ds_read_b128 a[200:203], v244 offset:4096\n\t"
"v_mfma_f32_16x16x32_bf16 v[192:195], a[248:251], a[204:207], v[192:195]\n\t"
"Lst9_%=:\n\t"
"s_waitcnt vmcnt(15) lgkmcnt(6)\n\t"
"v_mfma_f32_16x16x32_bf16 v[196:199], v[240:243], a[168:171], v[196:199]\n\t"
"ds_read_b128 a[204:207], v244 offset:4608\n\t"
"v_mfma_f32_16x16x32_bf16 v[196:199], v[248:251], a[172:175], v[196:199]\n\t"
"v_mfma_f32_16x16x32_bf16 v[196:199], a[228:231], a[176:179], v[196:199]\n\t"
"s_waitcnt lgkmcnt(6)\n\t"
"v_mfma_f32_16x16x32_bf16 v[204:207], v[240:243], a[172:175], v[204:207]\n\t"
"v_mfma_f32_16x16x32_bf16 v[204:207], v[248:251], a[176:179], v[204:207]\n\t"
"v_mfma_f32_16x16x32_bf16 v[204:207], a[228:231], a[180:183], v[204:207]\n\t"
"s_waitcnt lgkmcnt(5)\n\t"
"v_mfma_f32_16x16x32_bf16 v[212:215], v[240:243], a[176:179], v[212:215]\n\t"
"v_mfma_f32_16x16x32_bf16 v[212:215], v[248:251], a[180:183], v[212:215]\n\t"
"v_mfma_f32_16x16x32_bf16 v[212:215], a[228:231], a[184:187], v[212:215]\n\t"
"s_waitcnt lgkmcnt(4)\n\t"
"v_mfma_f32_16x16x32_bf16 a[0:3], v[240:243], a[180:183], a[0:3]\n\t"
"v_mfma_f32_16x16x32_bf16 a[0:3], v[248:251], a[184:187], a[0:3]\n\t"
"v_mfma_f32_16x16x32_bf16 a[0:3], a[228:231], a[188:191], a[0:3]\n\t"
"buffer_load_dwordx4 v[236:239], v252, s[8:11], s20 offen offset:1024 sc1\n\t"
"s_waitcnt lgkmcnt(3)\n\t"
"v_mfma_f32_16x16x32_bf16 a[8:11], v[240:243], a[184:187], a[8:11]\n\t"
"v_mfma_f32_16x16x32_bf16 a[8:11], v[248:251], a[188:191], a[8:11]\n\t"
"v_mfma_f32_16x16x32_bf16 a[8:11], a[228:231], a[192:195], a[8:11]\n\t"
"ds_read_b64_tr_b16 v[232:233], v246 offset:512\n\t"
"ds_read_b64_tr_b16 v[234:235], v246 offset:3584\n\t"
"s_waitcnt lgkmcnt(4)\n\t"
"v_mfma_f32_16x16x32_bf16 a[16:19], v[240:243], a[188:191], a[16:19]\n\t"
"v_mfma_f32_16x16x32_bf16 a[16:19], v[248:251], a[192:195], a[16:19]\n\t"
"v_mfma_f32_16x16x32_bf16 a[16:19], a[228:231], a[196:199], a[16:19]\n\t"
"s_waitcnt lgkmcnt(3)\n\t"
"v_mfma_f32_16x16x32_bf16 a[24:27], v[240:243], a[192:195], a[24:27]\n\t"
"v_mfma_f32_16x16x32_bf16 a[24:27], v[248:251], a[196:199], a[24:27]\n\t"
"v_mfma_f32_16x16x32_bf16 a[24:27], a[228:231], a[200:203], a[24:27]\n\t"
"s_waitcnt lgkmcnt(2)\n\t"
"v_mfma_f32_16x16x32_bf16 a[32:35], v[240:243], a[196:199], a[32:35]\n\t"
"ds_read_b64_tr_b16 v[228:229], v246 offset:6656\n\t"
"v_mfma_f32_16x16x32_bf16 a[32:35], v[248:251], a[200:203], a[32:35]\n\t"
"ds_read_b64_tr_b16 v[230:231], v246 offset:9728\n\t"
"v_mfma_f32_16x16x32_bf16 a[32:35], a[228:231], a[204:207], a[32:35]\n\t"
"s_waitcnt vmcnt(13)\n\t"
"v_mfma_f32_16x16x32_bf16 a[40:43], a[252:255], a[168:171], a[40:43]\n\t"
"v_mfma_f32_16x16x32_bf16 a[40:43], a[208:211], a[172:175], a[40:43]\n\t"
"v_mfma_f32_16x16x32_bf16 a[40:43], v[96:99], a[176:179], a[40:43]\n\t"
"v_mfma_f32_16x16x32_bf16 a[48:51], a[252:255], a[172:175], a[48:51]\n\t"
"v_mfma_f32_16x16x32_bf16 a[48:51], a[208:211], a[176:179], a[48:51]\n\t"
"v_mfma_f32_16x16x32_bf16 a[48:51], v[96:99], a[180:183], a[48:51]\n\t"
"s_waitcnt lgkmcnt(2)\n\t"
"v_mfma_f32_16x16x32_bf16 v[232:235], v[232:235], v[220:223], 0\n\t"
"v_mfma_f32_16x16x32_bf16 a[56:59], a[252:255], a[176:179], a[56:59]\n\t"
"ds_read_b64_tr_b16 v[224:225], v246 offset:12800\n\t"
"v_mfma_f32_16x16x32_bf16 a[56:59], a[208:211], a[180:183], a[56:59]\n\t"
"ds_read_b64_tr_b16 v[226:227], v246 offset:15872\n\t"
"v_mfma_f32_16x16x32_bf16 a[56:59], v[96:99], a[184:187], a[56:59]\n\t"
"v_mfma_f32_16x16x32_bf16 a[64:67], a[252:255], a[180:183], a[64:67]\n\t"
"v_mfma_f32_16x16x32_bf16 a[64:67], a[208:211], a[184:187], a[64:67]\n\t"
"s_nop 0\n\t"
"v_cvt_pk_bf16_f32 v232, v232, v233\n\t"
"v_mfma_f32_16x16x32_bf16 a[64:67], v[96:99], a[188:191], a[64:67]\n\t"
"v_cvt_pk_bf16_f32 v233, v234, v235\n\t"
"s_waitcnt lgkmcnt(2)\n\t"
"v_mfma_f32_16x16x32_bf16 v[228:231], v[228:231], v[220:223], 0\n\t"
"v_mfma_f32_16x16x32_bf16 a[72:75], a[252:255], a[184:187], a[72:75]\n\t"
"v_mfma_f32_16x16x32_bf16 a[72:75], a[208:211], a[188:191], a[72:75]\n\t"
"v_mfma_f32_16x16x32_bf16 a[72:75], v[96:99], a[192:195], a[72:75]\n\t"
"buffer_load_dwordx4 v[240:243], v252, s[8:11], s20 offen offset:2048 sc1\n\t"
"v_mfma_f32_16x16x32_bf16 a[80:83], a[252:255], a[188:191], a[80:83]\n\t"
"v_mfma_f32_16x16x32_bf16 a[80:83], a[208:211], a[192:195], a[80:83]\n\t"
"s_nop 1\n\t"
"v_cvt_pk_bf16_f32 v234, v228, v229\n\t"
"v_cvt_pk_bf16_f32 v235, v230, v231\n\t"
"v_mfma_f32_16x16x32_bf16 a[80:83], v[96:99], a[196:199], a[80:83]\n\t"
"s_mov_b32 s20, 823296\n\t"
"buffer_load_dwordx4 v[248:251], v252, s[8:11], s20 offen offset:0 sc1\n\t"
"v_mfma_f32_16x16x32_bf16 a[88:91], a[252:255], a[192:195], a[88:91]\n\t"
"ds_read_b64_tr_b16 v[228:229], v246 offset:20480\n\t"
"v_mfma_f32_16x16x32_bf16 a[88:91], a[208:211], a[196:199], a[88:91]\n\t"
"ds_read_b64_tr_b16 v[230:231], v246 offset:23552\n\t"
"v_mfma_f32_16x16x32_bf16 a[88:91], v[96:99], a[200:203], a[88:91]\n\t"
"buffer_load_dwordx4 a[228:231], v252, s[8:11], s20 offen offset:1024 sc1\n\t"
"s_waitcnt lgkmcnt(2)\n\t"
"v_mfma_f32_16x16x32_bf16 v[224:227], v[224:227], v[220:223], 0\n\t"
"v_mfma_f32_16x16x32_bf16 a[96:99], a[252:255], a[196:199], a[96:99]\n\t"
"v_mfma_f32_16x16x32_bf16 a[96:99], a[208:211], a[200:203], a[96:99]\n\t"
"v_mfma_f32_16x16x32_bf16 a[96:99], v[96:99], a[204:207], a[96:99]\n\t"
"buffer_load_dwordx4 a[208:211], v252, s[8:11], s20 offen offset:2048 sc1\n\t"
"s_waitcnt vmcnt(14)\n\t"
"v_mfma_f32_16x16x32_bf16 a[104:107], a[212:215], a[168:171], a[104:107]\n\t"
"ds_read_b128 a[168:171], v244 offset:9216\n\t"
"v_mfma_f32_16x16x32_bf16 a[104:107], a[216:219], a[172:175], a[104:107]\n\t"
"v_cvt_pk_bf16_f32 v224, v224, v225\n\t"
"v_mfma_f32_16x16x32_bf16 a[104:107], a[220:223], a[176:179], a[104:107]\n\t"
"v_cvt_pk_bf16_f32 v225, v226, v227\n\t"
"v_mfma_f32_16x16x32_bf16 a[112:115], a[212:215], a[172:175], a[112:115]\n\t"
"ds_read_b128 a[172:175], v244 offset:9728\n\t"
"v_mfma_f32_16x16x32_bf16 a[112:115], a[216:219], a[176:179], a[112:115]\n\t"
"v_mfma_f32_16x16x32_bf16 a[112:115], a[220:223], a[180:183], a[112:115]\n\t"
"s_mov_b32 s20, 835584\n\t"
"buffer_load_dwordx4 v[96:99], v252, s[8:11], s20 offen offset:0 sc1\n\t"
"s_waitcnt lgkmcnt(2)\n\t"
"v_mfma_f32_16x16x32_bf16 v[228:231], v[228:231], v[220:223], 0\n\t"
"v_mfma_f32_16x16x32_bf16 a[120:123], a[212:215], a[176:179], a[120:123]\n\t"
"ds_read_b128 a[176:179], v244 offset:10240\n\t"
"v_mfma_f32_16x16x32_bf16 a[120:123], a[216:219], a[180:183], a[120:123]\n\t"
"v_mfma_f32_16x16x32_bf16 a[120:123], a[220:223], a[184:187], a[120:123]\n\t"
"buffer_load_dwordx4 a[232:235], v252, s[8:11], s20 offen offset:1024 sc1\n\t"
"v_mfma_f32_16x16x32_bf16 a[128:131], a[212:215], a[180:183], a[128:131]\n\t"
"ds_read_b128 a[180:183], v244 offset:10752\n\t"
"v_mfma_f32_16x16x32_bf16 a[128:131], a[216:219], a[184:187], a[128:131]\n\t"
"v_cvt_pk_bf16_f32 v226, v228, v229\n\t"
"v_cvt_pk_bf16_f32 v227, v230, v231\n\t"
"v_mfma_f32_16x16x32_bf16 a[128:131], a[220:223], a[188:191], a[128:131]\n\t"
"buffer_load_dwordx4 a[236:239], v252, s[8:11], s20 offen offset:2048 sc1\n\t"
"v_mfma_f32_16x16x32_bf16 a[136:139], a[212:215], a[184:187], a[136:139]\n\t"
"ds_read_b64_tr_b16 v[228:229], v246 offset:26624\n\t"
"v_mfma_f32_16x16x32_bf16 a[136:139], a[216:219], a[188:191], a[136:139]\n\t"
"ds_read_b64_tr_b16 v[230:231], v246 offset:29696\n\t"
"v_mfma_f32_16x16x32_bf16 a[136:139], a[220:223], a[192:195], a[136:139]\n\t"
"s_mov_b32 s20, 995328\n\t"
"buffer_load_dwordx4 a[240:243], v252, s[8:11], s20 offen offset:0 sc1\n\t"
"v_mfma_f32_16x16x32_bf16 a[144:147], a[212:215], a[188:191], a[144:147]\n\t"
"ds_read_b128 a[184:187], v244 offset:11264\n\t"
"v_mfma_f32_16x16x32_bf16 a[144:147], a[216:219], a[192:195], a[144:147]\n\t"
"ds_read_b128 a[188:191], v244 offset:11776\n\t"
"v_mfma_f32_16x16x32_bf16 a[144:147], a[220:223], a[196:199], a[144:147]\n\t"
"v_mfma_f32_16x16x32_bf16 a[152:155], a[212:215], a[192:195], a[152:155]\n\t"
"ds_read_b128 a[192:195], v244 offset:12288\n\t"
"v_mfma_f32_16x16x32_bf16 a[152:155], a[216:219], a[196:199], a[152:155]\n\t"
"v_mfma_f32_16x16x32_bf16 a[152:155], a[220:223], a[200:203], a[152:155]\n\t"
"buffer_load_dwordx4 a[244:247], v252, s[8:11], s20 offen offset:1024 sc1\n\t"
"v_mfma_f32_16x16x32_bf16 a[160:163], a[212:215], a[196:199], a[160:163]\n\t"
"ds_read_b128 a[196:199], v244 offset:12800\n\t"
"v_mfma_f32_16x16x32_bf16 a[160:163], a[216:219], a[200:203], a[160:163]\n\t"
"ds_read_b128 a[200:203], v244 offset:13312\n\t"
"v_mfma_f32_16x16x32_bf16 a[160:163], a[220:223], a[204:207], a[160:163]\n\t"
"buffer_load_dwordx4 a[212:215], v252, s[8:11], s20 offen offset:2048 sc1\n\t"
"s_waitcnt lgkmcnt(5)\n\t"
"v_mfma_f32_16x16x32_bf16 v[228:231], v[228:231], v[220:223], 0\n\t"
"ds_read_b128 a[204:207], v244 offset:13824\n\t"
"s_waitcnt vmcnt(9)\n\t"
"v_mfma_f32_16x16x32_bf16 v[200:203], a[224:227], a[168:171], v[200:203]\n\t"
"v_mfma_f32_16x16x32_bf16 v[200:203], v[236:239], a[172:175], v[200:203]\n\t"
"v_mfma_f32_16x16x32_bf16 v[200:203], v[240:243], a[176:179], v[200:203]\n\t"
"s_mov_b32 s20, 1007616\n\t"
"buffer_load_dwordx4 a[216:219], v252, s[8:11], s20 offen offset:0 sc1\n\t"
"v_mfma_f32_16x16x32_bf16 v[208:211], a[224:227], a[172:175], v[208:211]\n\t"
"v_mfma_f32_16x16x32_bf16 v[208:211], v[236:239], a[176:179], v[208:211]\n\t"
"v_cvt_pk_bf16_f32 v228, v228, v229\n\t"
"v_cvt_pk_bf16_f32 v229, v230, v231\n\t"
"v_mfma_f32_16x16x32_bf16 v[208:211], v[240:243], a[180:183], v[208:211]\n\t"
"buffer_load_dwordx4 a[220:223], v252, s[8:11], s20 offen offset:1024 sc1\n\t"
"s_waitcnt lgkmcnt(0)\n\t"
"s_barrier\n\t"
"v_mfma_f32_16x16x32_bf16 v[216:219], a[224:227], a[176:179], v[216:219]\n\t"
"v_mfma_f32_16x16x32_bf16 v[216:219], v[236:239], a[180:183], v[216:219]\n\t"
"v_mfma_f32_16x16x32_bf16 v[216:219], v[240:243], a[184:187], v[216:219]\n\t"
"ds_write2st64_b64 v245, v[232:233], v[234:235] offset0:0 offset1:1\n\t"
"buffer_load_dwordx4 a[248:251], v252, s[8:11], s20 offen offset:2048 sc1\n\t"
"v_mfma_f32_16x16x32_bf16 a[4:7], a[224:227], a[180:183], a[4:7]\n\t"
"ds_read_b64_tr_b16 v[232:233], v246 offset:32768\n\t"
"v_mfma_f32_16x16x32_bf16 a[4:7], v[236:239], a[184:187], a[4:7]\n\t"
"ds_read_b64_tr_b16 v[234:235], v246 offset:35840\n\t"
"v_mfma_f32_16x16x32_bf16 a[4:7], v[240:243], a[188:191], a[4:7]\n\t"
"ds_write2st64_b64 v245, v[224:225], v[226:227] offset0:2 offset1:3\n\t"
"ds_write_b64 v245, v[228:229] offset:2048\n\t"
"v_mfma_f32_16x16x32_bf16 a[12:15], a[224:227], a[184:187], a[12:15]\n\t"
"ds_read_b64_tr_b16 v[224:225], v246 offset:40448\n\t"
"v_mfma_f32_16x16x32_bf16 a[12:15], v[236:239], a[188:191], a[12:15]\n\t"
"ds_read_b64_tr_b16 v[226:227], v246 offset:43520\n\t"
"v_mfma_f32_16x16x32_bf16 a[12:15], v[240:243], a[192:195], a[12:15]\n\t"
"s_mov_b32 s20, 1019904\n\t"
"buffer_load_dwordx4 a[252:255], v252, s[8:11], s20 offen offset:0 sc1\n\t"
"v_mfma_f32_16x16x32_bf16 a[20:23], a[224:227], a[188:191], a[20:23]\n\t"
"ds_read_b64_tr_b16 v[228:229], v246 offset:46592\n\t"
"v_mfma_f32_16x16x32_bf16 a[20:23], v[236:239], a[192:195], a[20:23]\n\t"
"ds_read_b64_tr_b16 v[230:231], v246 offset:49664\n\t"
"v_mfma_f32_16x16x32_bf16 a[20:23], v[240:243], a[196:199], a[20:23]\n\t"
"s_waitcnt lgkmcnt(6)\n\t"
"v_mfma_f32_16x16x32_bf16 v[232:235], v[232:235], v[220:223], 0\n\t"
"v_mfma_f32_16x16x32_bf16 a[28:31], a[224:227], a[192:195], a[28:31]\n\t"
"v_mfma_f32_16x16x32_bf16 a[28:31], v[236:239], a[196:199], a[28:31]\n\t"
"v_mfma_f32_16x16x32_bf16 a[28:31], v[240:243], a[200:203], a[28:31]\n\t"
"s_waitcnt lgkmcnt(2)\n\t"
"v_mfma_f32_16x16x32_bf16 v[224:227], v[224:227], v[220:223], 0\n\t"
"v_mfma_f32_16x16x32_bf16 a[36:39], a[224:227], a[196:199], a[36:39]\n\t"
"s_nop 1\n\t"
"v_cvt_pk_bf16_f32 v232, v232, v233\n\t"
"v_cvt_pk_bf16_f32 v233, v234, v235\n\t"
"v_mfma_f32_16x16x32_bf16 a[36:39], v[236:239], a[200:203], a[36:39]\n\t"
"v_mfma_f32_16x16x32_bf16 a[36:39], v[240:243], a[204:207], a[36:39]\n\t"
"buffer_load_dwordx4 a[224:227], v252, s[8:11], s20 offen offset:1024 sc1\n\t"
"s_waitcnt lgkmcnt(0)\n\t"
"v_mfma_f32_16x16x32_bf16 v[228:231], v[228:231], v[220:223], 0\n\t"
"buffer_load_dwordx4 v[236:239], v252, s[8:11], s20 offen offset:2048 sc1\n\t"
"s_waitcnt vmcnt(12)\n\t"
"v_mfma_f32_16x16x32_bf16 a[44:47], v[248:251], a[168:171], a[44:47]\n\t"
"v_cvt_pk_bf16_f32 v234, v224, v225\n\t"
"v_cvt_pk_bf16_f32 v235, v226, v227\n\t"
"v_mfma_f32_16x16x32_bf16 a[44:47], a[228:231], a[172:175], a[44:47]\n\t"
"ds_read_b64_tr_b16 v[224:225], v246 offset:52736\n\t"
"v_mfma_f32_16x16x32_bf16 a[44:47], a[208:211], a[176:179], a[44:47]\n\t"
"s_mov_b32 s20, 1032192\n\t"
"buffer_load_dwordx4 v[240:243], v252, s[8:11], s20 offen offset:0 sc1\n\t"
"v_mfma_f32_16x16x32_bf16 a[52:55], v[248:251], a[172:175], a[52:55]\n\t"
"ds_read_b64_tr_b16 v[226:227], v246 offset:55808\n\t"
"v_mfma_f32_16x16x32_bf16 a[52:55], a[228:231], a[176:179], a[52:55]\n\t"
"v_cvt_pk_bf16_f32 v228, v228, v229\n\t"
"v_cvt_pk_bf16_f32 v229, v230, v231\n\t"
"v_mfma_f32_16x16x32_bf16 a[52:55], a[208:211], a[180:183], a[52:55]\n\t"
"ds_write2st64_b64 v245, v[232:233], v[234:235] offset0:5 offset1:6\n\t"
"v_mfma_f32_16x16x32_bf16 a[60:63], v[248:251], a[176:179], a[60:63]\n\t"
"v_mfma_f32_16x16x32_bf16 a[60:63], a[228:231], a[180:183], a[60:63]\n\t"
"v_mfma_f32_16x16x32_bf16 a[60:63], a[208:211], a[184:187], a[60:63]\n\t"
"v_mfma_f32_16x16x32_bf16 a[68:71], v[248:251], a[180:183], a[68:71]\n\t"
"v_mfma_f32_16x16x32_bf16 a[68:71], a[228:231], a[184:187], a[68:71]\n\t"
"v_mfma_f32_16x16x32_bf16 a[68:71], a[208:211], a[188:191], a[68:71]\n\t"
"v_mfma_f32_16x16x32_bf16 a[76:79], v[248:251], a[184:187], a[76:79]\n\t"
"v_mfma_f32_16x16x32_bf16 a[76:79], a[228:231], a[188:191], a[76:79]\n\t"
"v_mfma_f32_16x16x32_bf16 a[76:79], a[208:211], a[192:195], a[76:79]\n\t"
"s_waitcnt lgkmcnt(1)\n\t"
"v_mfma_f32_16x16x32_bf16 v[224:227], v[224:227], v[220:223], 0\n\t"
"v_mfma_f32_16x16x32_bf16 a[84:87], v[248:251], a[188:191], a[84:87]\n\t"
"v_mfma_f32_16x16x32_bf16 a[84:87], a[228:231], a[192:195], a[84:87]\n\t"
"v_mfma_f32_16x16x32_bf16 a[84:87], a[208:211], a[196:199], a[84:87]\n\t"
"v_mfma_f32_16x16x32_bf16 a[92:95], v[248:251], a[192:195], a[92:95]\n\t"
"v_mfma_f32_16x16x32_bf16 a[92:95], a[228:231], a[196:199], a[92:95]\n\t"
"s_nop 2\n\t"
"v_cvt_pk_bf16_f32 v230, v224, v225\n\t"
"v_cvt_pk_bf16_f32 v231, v226, v227\n\t"
"v_mfma_f32_16x16x32_bf16 a[92:95], a[208:211], a[200:203], a[92:95]\n\t"
"ds_write2st64_b64 v245, v[228:229], v[230:231] offset0:7 offset1:8\n\t"
"v_mfma_f32_16x16x32_bf16 a[100:103], v[248:251], a[196:199], a[100:103]\n\t"
"v_mfma_f32_16x16x32_bf16 a[100:103], a[228:231], a[200:203], a[100:103]\n\t"
"v_mfma_f32_16x16x32_bf16 a[100:103], a[208:211], a[204:207], a[100:103]\n\t"
"buffer_load_dwordx4 v[248:251], v252, s[8:11], s20 offen offset:1024 sc1\n\t"
"s_waitcnt vmcnt(11)\n\t"
"v_mfma_f32_16x16x32_bf16 a[108:111], v[96:99], a[168:171], a[108:111]\n\t"
"v_mfma_f32_16x16x32_bf16 a[108:111], a[232:235], a[172:175], a[108:111]\n\t"
"v_mfma_f32_16x16x32_bf16 a[108:111], a[236:239], a[176:179], a[108:111]\n\t"
"buffer_load_dwordx4 a[208:211], v252, s[8:11], s20 offen offset:2048 sc1\n\t"
"s_waitcnt lgkmcnt(0)\n\t"
"s_barrier\n\t"
"v_mfma_f32_16x16x32_bf16 a[116:119], v[96:99], a[172:175], a[116:119]\n\t"
"ds_read_b128 a[168:171], v244 offset:0\n\t"
"v_mfma_f32_16x16x32_bf16 a[116:119], a[232:235], a[176:179], a[116:119]\n\t"
"ds_read_b128 a[172:175], v244 offset:512\n\t"
"v_mfma_f32_16x16x32_bf16 a[116:119], a[236:239], a[180:183], a[116:119]\n\t"
"s_mov_b32 s20, 1044480\n\t"
"buffer_load_dwordx4 a[228:231], v252, s[8:11], s20 offen offset:0 sc1\n\t"
"v_mfma_f32_16x16x32_bf16 a[124:127], v[96:99], a[176:179], a[124:127]\n\t"
"ds_read_b128 a[176:179], v244 offset:1024\n\t"
"v_mfma_f32_16x16x32_bf16 a[124:127], a[232:235], a[180:183], a[124:127]\n\t"
"v_mfma_f32_16x16x32_bf16 a[124:127], a[236:239], a[184:187], a[124:127]\n\t"
"v_mfma_f32_16x16x32_bf16 a[132:135], v[96:99], a[180:183], a[132:135]\n\t"
"ds_read_b128 a[180:183], v244 offset:1536\n\t"
"v_mfma_f32_16x16x32_bf16 a[132:135], a[232:235], a[184:187], a[132:135]\n\t"
"v_mfma_f32_16x16x32_bf16 a[132:135], a[236:239], a[188:191], a[132:135]\n\t"
"v_mfma_f32_16x16x32_bf16 a[140:143], v[96:99], a[184:187], a[140:143]\n\t"
"ds_read_b128 a[184:187], v244 offset:2048\n\t"
"v_mfma_f32_16x16x32_bf16 a[140:143], a[232:235], a[188:191], a[140:143]\n\t"
"v_mfma_f32_16x16x32_bf16 a[140:143], a[236:239], a[192:195], a[140:143]\n\t"
"v_mfma_f32_16x16x32_bf16 a[148:151], v[96:99], a[188:191], a[148:151]\n\t"
"ds_read_b128 a[188:191], v244 offset:2560\n\t"
"v_mfma_f32_16x16x32_bf16 a[148:151], a[232:235], a[192:195], a[148:151]\n\t"
"v_mfma_f32_16x16x32_bf16 a[148:151], a[236:239], a[196:199], a[148:151]\n\t"
"v_mfma_f32_16x16x32_bf16 a[156:159], v[96:99], a[192:195], a[156:159]\n\t"
"ds_read_b128 a[192:195], v244 offset:3072\n\t"
"v_mfma_f32_16x16x32_bf16 a[156:159], a[232:235], a[196:199], a[156:159]\n\t"
"v_mfma_f32_16x16x32_bf16 a[156:159], a[236:239], a[200:203], a[156:159]\n\t"
"v_mfma_f32_16x16x32_bf16 a[164:167], v[96:99], a[196:199], a[164:167]\n\t"
"ds_read_b128 a[196:199], v244 offset:3584\n\t"
"v_mfma_f32_16x16x32_bf16 a[164:167], a[232:235], a[200:203], a[164:167]\n\t"
"ds_read_b128 a[200:203], v244 offset:4096\n\t"
"v_mfma_f32_16x16x32_bf16 a[164:167], a[236:239], a[204:207], a[164:167]\n\t"
"buffer_load_dwordx4 v[96:99], v252, s[8:11], s20 offen offset:1024 sc1\n\t"
"Lst10_%=:\n\t"
"s_waitcnt vmcnt(11) lgkmcnt(6)\n\t"
"v_mfma_f32_16x16x32_bf16 v[196:199], a[240:243], a[168:171], v[196:199]\n\t"
"ds_read_b128 a[204:207], v244 offset:4608\n\t"
"v_xor_b32 v246, 0xc0, v246\n\t"
"v_mfma_f32_16x16x32_bf16 v[196:199], a[244:247], a[172:175], v[196:199]\n\t"
"v_mfma_f32_16x16x32_bf16 v[196:199], a[212:215], a[176:179], v[196:199]\n\t"
"s_waitcnt lgkmcnt(6)\n\t"
"v_mfma_f32_16x16x32_bf16 v[204:207], a[240:243], a[172:175], v[204:207]\n\t"
"v_mfma_f32_16x16x32_bf16 v[204:207], a[244:247], a[176:179], v[204:207]\n\t"
"v_mfma_f32_16x16x32_bf16 v[204:207], a[212:215], a[180:183], v[204:207]\n\t"
"buffer_load_dwordx4 a[232:235], v252, s[8:11], s20 offen offset:2048 sc1\n\t"
"s_waitcnt lgkmcnt(5)\n\t"
"v_mfma_f32_16x16x32_bf16 v[212:215], a[240:243], a[176:179], v[212:215]\n\t"
"v_mfma_f32_16x16x32_bf16 v[212:215], a[244:247], a[180:183], v[212:215]\n\t"
"v_mfma_f32_16x16x32_bf16 v[212:215], a[212:215], a[184:187], v[212:215]\n\t"
"s_mov_b32 s20, 1056768\n\t"
"buffer_load_dwordx4 a[236:239], v252, s[8:11], s20 offen offset:0 sc1\n\t"
"s_waitcnt lgkmcnt(4)\n\t"
"v_mfma_f32_16x16x32_bf16 a[0:3], a[240:243], a[180:183], a[0:3]\n\t"
"v_mfma_f32_16x16x32_bf16 a[0:3], a[244:247], a[184:187], a[0:3]\n\t"
"v_mfma_f32_16x16x32_bf16 a[0:3], a[212:215], a[188:191], a[0:3]\n\t"
"s_waitcnt lgkmcnt(3)\n\t"
"v_mfma_f32_16x16x32_bf16 a[8:11], a[240:243], a[184:187], a[8:11]\n\t"
"v_mfma_f32_16x16x32_bf16 a[8:11], a[244:247], a[188:191], a[8:11]\n\t"
"v_mfma_f32_16x16x32_bf16 a[8:11], a[212:215], a[192:195], a[8:11]\n\t"
"ds_read_b64_tr_b16 v[232:233], v246 offset:512\n\t"
"ds_read_b64_tr_b16 v[234:235], v246 offset:3584\n\t"
"s_waitcnt lgkmcnt(4)\n\t"
"v_mfma_f32_16x16x32_bf16 a[16:19], a[240:243], a[188:191], a[16:19]\n\t"
"v_mfma_f32_16x16x32_bf16 a[16:19], a[244:247], a[192:195], a[16:19]\n\t"
"v_mfma_f32_16x16x32_bf16 a[16:19], a[212:215], a[196:199], a[16:19]\n\t"
"s_waitcnt lgkmcnt(3)\n\t"
"v_mfma_f32_16x16x32_bf16 a[24:27], a[240:243], a[192:195], a[24:27]\n\t"
"v_mfma_f32_16x16x32_bf16 a[24:27], a[244:247], a[196:199], a[24:27]\n\t"
"v_mfma_f32_16x16x32_bf16 a[24:27], a[212:215], a[200:203], a[24:27]\n\t"
"s_waitcnt lgkmcnt(2)\n\t"
"v_mfma_f32_16x16x32_bf16 a[32:35], a[240:243], a[196:199], a[32:35]\n\t"
"ds_read_b64_tr_b16 v[224:225], v246 offset:6656\n\t"
"v_mfma_f32_16x16x32_bf16 a[32:35], a[244:247], a[200:203], a[32:35]\n\t"
"ds_read_b64_tr_b16 v[226:227], v246 offset:9728\n\t"
"v_mfma_f32_16x16x32_bf16 a[32:35], a[212:215], a[204:207], a[32:35]\n\t"
"buffer_load_dwordx4 a[240:243], v252, s[8:11], s20 offen offset:1024 sc1\n\t"
"s_waitcnt vmcnt(11)\n\t"
"v_mfma_f32_16x16x32_bf16 a[40:43], a[216:219], a[168:171], a[40:43]\n\t"
"v_mfma_f32_16x16x32_bf16 a[40:43], a[220:223], a[172:175], a[40:43]\n\t"
"v_mfma_f32_16x16x32_bf16 a[40:43], a[248:251], a[176:179], a[40:43]\n\t"
"buffer_load_dwordx4 a[212:215], v252, s[8:11], s20 offen offset:2048 sc1\n\t"
"v_mfma_f32_16x16x32_bf16 a[48:51], a[216:219], a[172:175], a[48:51]\n\t"
"v_mfma_f32_16x16x32_bf16 a[48:51], a[220:223], a[176:179], a[48:51]\n\t"
"v_mfma_f32_16x16x32_bf16 a[48:51], a[248:251], a[180:183], a[48:51]\n\t"
"s_mov_b32 s20, 921600\n\t"
"buffer_load_dwordx4 a[244:247], v252, s[8:11], s20 offen offset:0 sc1\n\t"
"s_waitcnt lgkmcnt(2)\n\t"
"v_mfma_f32_16x16x32_bf16 v[232:235], v[232:235], v[220:223], 0\n\t"
"v_mfma_f32_16x16x32_bf16 a[56:59], a[216:219], a[176:179], a[56:59]\n\t"
"ds_read_b64_tr_b16 v[228:229], v246 offset:12800\n\t"
"v_mfma_f32_16x16x32_bf16 a[56:59], a[220:223], a[180:183], a[56:59]\n\t"
"ds_read_b64_tr_b16 v[230:231], v246 offset:15872\n\t"
"v_mfma_f32_16x16x32_bf16 a[56:59], a[248:251], a[184:187], a[56:59]\n\t"
"v_mfma_f32_16x16x32_bf16 a[64:67], a[216:219], a[180:183], a[64:67]\n\t"
"v_mfma_f32_16x16x32_bf16 a[64:67], a[220:223], a[184:187], a[64:67]\n\t"
"s_nop 0\n\t"
"v_cvt_pk_bf16_f32 v232, v232, v233\n\t"
"v_mfma_f32_16x16x32_bf16 a[64:67], a[248:251], a[188:191], a[64:67]\n\t"
"v_cvt_pk_bf16_f32 v233, v234, v235\n\t"
"s_waitcnt lgkmcnt(2)\n\t"
"v_mfma_f32_16x16x32_bf16 v[224:227], v[224:227], v[220:223], 0\n\t"
"v_mfma_f32_16x16x32_bf16 a[72:75], a[216:219], a[184:187], a[72:75]\n\t"
"v_mfma_f32_16x16x32_bf16 a[72:75], a[220:223], a[188:191], a[72:75]\n\t"
"v_mfma_f32_16x16x32_bf16 a[72:75], a[248:251], a[192:195], a[72:75]\n\t"
"v_mfma_f32_16x16x32_bf16 a[80:83], a[216:219], a[188:191], a[80:83]\n\t"
"v_mfma_f32_16x16x32_bf16 a[80:83], a[220:223], a[192:195], a[80:83]\n\t"
"s_nop 2\n\t"
"v_cvt_pk_bf16_f32 v234, v224, v225\n\t"
"v_cvt_pk_bf16_f32 v235, v226, v227\n\t"
"v_mfma_f32_16x16x32_bf16 a[80:83], a[248:251], a[196:199], a[80:83]\n\t"
"ds_read_b64_tr_b16 v[224:225], v246 offset:20480\n\t"
"v_mfma_f32_16x16x32_bf16 a[88:91], a[216:219], a[192:195], a[88:91]\n\t"
"ds_read_b64_tr_b16 v[226:227], v246 offset:23552\n\t"
"v_mfma_f32_16x16x32_bf16 a[88:91], a[220:223], a[196:199], a[88:91]\n\t"
"v_mfma_f32_16x16x32_bf16 a[88:91], a[248:251], a[200:203], a[88:91]\n\t"
"s_waitcnt lgkmcnt(2)\n\t"
"v_mfma_f32_16x16x32_bf16 v[228:231], v[228:231], v[220:223], 0\n\t"
"v_mfma_f32_16x16x32_bf16 a[96:99], a[216:219], a[196:199], a[96:99]\n\t"
"v_mfma_f32_16x16x32_bf16 a[96:99], a[220:223], a[200:203], a[96:99]\n\t"
"v_mfma_f32_16x16x32_bf16 a[96:99], a[248:251], a[204:207], a[96:99]\n\t"
"buffer_load_dwordx4 a[216:219], v252, s[8:11], s20 offen offset:1024 sc1\n\t"
"s_waitcnt vmcnt(11)\n\t"
"v_mfma_f32_16x16x32_bf16 a[104:107], a[252:255], a[168:171], a[104:107]\n\t"
"ds_read_b128 a[168:171], v244 offset:9216\n\t"
"v_mfma_f32_16x16x32_bf16 a[104:107], a[224:227], a[172:175], a[104:107]\n\t"
"v_cvt_pk_bf16_f32 v228, v228, v229\n\t"
"v_cvt_pk_bf16_f32 v229, v230, v231\n\t"
"v_mfma_f32_16x16x32_bf16 a[104:107], v[236:239], a[176:179], a[104:107]\n\t"
"v_mfma_f32_16x16x32_bf16 a[112:115], a[252:255], a[172:175], a[112:115]\n\t"
"ds_read_b128 a[172:175], v244 offset:9728\n\t"
"v_mfma_f32_16x16x32_bf16 a[112:115], a[224:227], a[176:179], a[112:115]\n\t"
"v_mfma_f32_16x16x32_bf16 a[112:115], v[236:239], a[180:183], a[112:115]\n\t"
"buffer_load_dwordx4 a[220:223], v252, s[8:11], s20 offen offset:2048 sc1\n\t"
"s_waitcnt lgkmcnt(2)\n\t"
"v_mfma_f32_16x16x32_bf16 v[224:227], v[224:227], v[220:223], 0\n\t"
"s_mov_b32 s20, 933888\n\t"
"buffer_load_dwordx4 a[248:251], v252, s[8:11], s20 offen offset:0 sc1\n\t"
"v_mfma_f32_16x16x32_bf16 a[120:123], a[252:255], a[176:179], a[120:123]\n\t"
"ds_read_b128 a[176:179], v244 offset:10240\n\t"
"v_mfma_f32_16x16x32_bf16 a[120:123], a[224:227], a[180:183], a[120:123]\n\t"
"v_mfma_f32_16x16x32_bf16 a[120:123], v[236:239], a[184:187], a[120:123]\n\t"
"v_mfma_f32_16x16x32_bf16 a[128:131], a[252:255], a[180:183], a[128:131]\n\t"
"ds_read_b128 a[180:183], v244 offset:10752\n\t"
"v_mfma_f32_16x16x32_bf16 a[128:131], a[224:227], a[184:187], a[128:131]\n\t"
"v_cvt_pk_bf16_f32 v230, v224, v225\n\t"
"v_cvt_pk_bf16_f32 v231, v226, v227\n\t"
"v_mfma_f32_16x16x32_bf16 a[128:131], v[236:239], a[188:191], a[128:131]\n\t"
"ds_read_b64_tr_b16 v[224:225], v246 offset:26624\n\t"
"v_mfma_f32_16x16x32_bf16 a[136:139], a[252:255], a[184:187], a[136:139]\n\t"
"ds_read_b64_tr_b16 v[226:227], v246 offset:29696\n\t"
"v_mfma_f32_16x16x32_bf16 a[136:139], a[224:227], a[188:191], a[136:139]\n\t"
"ds_read_b128 a[184:187], v244 offset:11264\n\t"
"v_mfma_f32_16x16x32_bf16 a[136:139], v[236:239], a[192:195], a[136:139]\n\t"
"v_mfma_f32_16x16x32_bf16 a[144:147], a[252:255], a[188:191], a[144:147]\n\t"
"ds_read_b128 a[188:191], v244 offset:11776\n\t"
"v_mfma_f32_16x16x32_bf16 a[144:147], a[224:227], a[192:195], a[144:147]\n\t"
"v_mfma_f32_16x16x32_bf16 a[144:147], v[236:239], a[196:199], a[144:147]\n\t"
"v_mfma_f32_16x16x32_bf16 a[152:155], a[252:255], a[192:195], a[152:155]\n\t"
"ds_read_b128 a[192:195], v244 offset:12288\n\t"
"v_mfma_f32_16x16x32_bf16 a[152:155], a[224:227], a[196:199], a[152:155]\n\t"
"v_mfma_f32_16x16x32_bf16 a[152:155], v[236:239], a[200:203], a[152:155]\n\t"
"v_mfma_f32_16x16x32_bf16 a[160:163], a[252:255], a[196:199], a[160:163]\n\t"
"ds_read_b128 a[196:199], v244 offset:12800\n\t"
"v_mfma_f32_16x16x32_bf16 a[160:163], a[224:227], a[200:203], a[160:163]\n\t"
"ds_read_b128 a[200:203], v244 offset:13312\n\t"
"v_mfma_f32_16x16x32_bf16 a[160:163], v[236:239], a[204:207], a[160:163]\n\t"
"buffer_load_dwordx4 a[252:255], v252, s[8:11], s20 offen offset:1024 sc1\n\t"
"s_waitcnt lgkmcnt(5)\n\t"
"v_mfma_f32_16x16x32_bf16 v[224:227], v[224:227], v[220:223], 0\n\t"
"ds_read_b128 a[204:207], v244 offset:13824\n\t"
"s_waitcnt vmcnt(11)\n\t"
"v_mfma_f32_16x16x32_bf16 v[200:203], v[240:243], a[168:171], v[200:203]\n\t"
"v_mfma_f32_16x16x32_bf16 v[200:203], v[248:251], a[172:175], v[200:203]\n\t"
"v_mfma_f32_16x16x32_bf16 v[200:203], a[208:211], a[176:179], v[200:203]\n\t"
"buffer_load_dwordx4 v[236:239], v252, s[8:11], s20 offen offset:2048 sc1\n\t"
"v_mfma_f32_16x16x32_bf16 v[208:211], v[240:243], a[172:175], v[208:211]\n\t"
"v_mfma_f32_16x16x32_bf16 v[208:211], v[248:251], a[176:179], v[208:211]\n\t"
"v_cvt_pk_bf16_f32 v224, v224, v225\n\t"
"v_cvt_pk_bf16_f32 v225, v226, v227\n\t"
"v_mfma_f32_16x16x32_bf16 v[208:211], a[208:211], a[180:183], v[208:211]\n\t"
"s_mov_b32 s20, 946176\n\t"
"buffer_load_dwordx4 a[224:227], v252, s[8:11], s20 offen offset:0 sc1\n\t"
"s_waitcnt lgkmcnt(0)\n\t"
"s_barrier\n\t"
"v_mfma_f32_16x16x32_bf16 v[216:219], v[240:243], a[176:179], v[216:219]\n\t"
"v_mfma_f32_16x16x32_bf16 v[216:219], v[248:251], a[180:183], v[216:219]\n\t"
"v_mfma_f32_16x16x32_bf16 v[216:219], a[208:211], a[184:187], v[216:219]\n\t"
"ds_write2st64_b64 v245, v[232:233], v[234:235] offset0:0 offset1:1\n\t"
"v_mfma_f32_16x16x32_bf16 a[4:7], v[240:243], a[180:183], a[4:7]\n\t"
"ds_read_b64_tr_b16 v[232:233], v246 offset:32768\n\t"
"v_mfma_f32_16x16x32_bf16 a[4:7], v[248:251], a[184:187], a[4:7]\n\t"
"ds_read_b64_tr_b16 v[234:235], v246 offset:35840\n\t"
"v_mfma_f32_16x16x32_bf16 a[4:7], a[208:211], a[188:191], a[4:7]\n\t"
"ds_write2st64_b64 v245, v[228:229], v[230:231] offset0:2 offset1:3\n\t"
"v_mfma_f32_16x16x32_bf16 a[12:15], v[240:243], a[184:187], a[12:15]\n\t"
"ds_read_b64_tr_b16 v[228:229], v246 offset:40448\n\t"
"v_mfma_f32_16x16x32_bf16 a[12:15], v[248:251], a[188:191], a[12:15]\n\t"
"ds_read_b64_tr_b16 v[230:231], v246 offset:43520\n\t"
"v_mfma_f32_16x16x32_bf16 a[12:15], a[208:211], a[192:195], a[12:15]\n\t"
"ds_write_b64 v245, v[224:225] offset:2048\n\t"
"v_mfma_f32_16x16x32_bf16 a[20:23], v[240:243], a[188:191], a[20:23]\n\t"
"ds_read_b64_tr_b16 v[224:225], v246 offset:46592\n\t"
"v_mfma_f32_16x16x32_bf16 a[20:23], v[248:251], a[192:195], a[20:23]\n\t"
"ds_read_b64_tr_b16 v[226:227], v246 offset:49664\n\t"
"v_mfma_f32_16x16x32_bf16 a[20:23], a[208:211], a[196:199], a[20:23]\n\t"
"s_waitcnt lgkmcnt(6)\n\t"
"v_mfma_f32_16x16x32_bf16 v[232:235], v[232:235], v[220:223], 0\n\t"
"v_mfma_f32_16x16x32_bf16 a[28:31], v[240:243], a[192:195], a[28:31]\n\t"
"v_mfma_f32_16x16x32_bf16 a[28:31], v[248:251], a[196:199], a[28:31]\n\t"
"v_mfma_f32_16x16x32_bf16 a[28:31], a[208:211], a[200:203], a[28:31]\n\t"
"s_waitcnt lgkmcnt(3)\n\t"
"v_mfma_f32_16x16x32_bf16 v[228:231], v[228:231], v[220:223], 0\n\t"
"v_mfma_f32_16x16x32_bf16 a[36:39], v[240:243], a[196:199], a[36:39]\n\t"
"s_nop 1\n\t"
"v_cvt_pk_bf16_f32 v232, v232, v233\n\t"
"v_cvt_pk_bf16_f32 v233, v234, v235\n\t"
"v_mfma_f32_16x16x32_bf16 a[36:39], v[248:251], a[200:203], a[36:39]\n\t"
"v_mfma_f32_16x16x32_bf16 a[36:39], a[208:211], a[204:207], a[36:39]\n\t"
"buffer_load_dwordx4 v[240:243], v252, s[8:11], s20 offen offset:1024 sc1\n\t"
"s_waitcnt lgkmcnt(0)\n\t"
"v_mfma_f32_16x16x32_bf16 v[224:227], v[224:227], v[220:223], 0\n\t"
"buffer_load_dwordx4 v[248:251], v252, s[8:11], s20 offen offset:2048 sc1\n\t"
"s_waitcnt vmcnt(12)\n\t"
"v_mfma_f32_16x16x32_bf16 a[44:47], a[228:231], a[168:171], a[44:47]\n\t"
"v_cvt_pk_bf16_f32 v234, v228, v229\n\t"
"v_cvt_pk_bf16_f32 v235, v230, v231\n\t"
"v_mfma_f32_16x16x32_bf16 a[44:47], v[96:99], a[172:175], a[44:47]\n\t"
"ds_read_b64_tr_b16 v[228:229], v246 offset:52736\n\t"
"v_mfma_f32_16x16x32_bf16 a[44:47], a[232:235], a[176:179], a[44:47]\n\t"
"s_mov_b32 s20, 958464\n\t"
"buffer_load_dwordx4 a[208:211], v252, s[8:11], s20 offen offset:0 sc1\n\t"
"v_mfma_f32_16x16x32_bf16 a[52:55], a[228:231], a[172:175], a[52:55]\n\t"
"ds_read_b64_tr_b16 v[230:231], v246 offset:55808\n\t"
"v_mfma_f32_16x16x32_bf16 a[52:55], v[96:99], a[176:179], a[52:55]\n\t"
"v_cvt_pk_bf16_f32 v224, v224, v225\n\t"
"v_cvt_pk_bf16_f32 v225, v226, v227\n\t"
"v_mfma_f32_16x16x32_bf16 a[52:55], a[232:235], a[180:183], a[52:55]\n\t"
"ds_write2st64_b64 v245, v[232:233], v[234:235] offset0:5 offset1:6\n\t"
"v_mfma_f32_16x16x32_bf16 a[60:63], a[228:231], a[176:179], a[60:63]\n\t"
"v_mfma_f32_16x16x32_bf16 a[60:63], v[96:99], a[180:183], a[60:63]\n\t"
"v_mfma_f32_16x16x32_bf16 a[60:63], a[232:235], a[184:187], a[60:63]\n\t"
"v_mfma_f32_16x16x32_bf16 a[68:71], a[228:231], a[180:183], a[68:71]\n\t"
"v_mfma_f32_16x16x32_bf16 a[68:71], v[96:99], a[184:187], a[68:71]\n\t"
"v_mfma_f32_16x16x32_bf16 a[68:71], a[232:235], a[188:191], a[68:71]\n\t"
"v_mfma_f32_16x16x32_bf16 a[76:79], a[228:231], a[184:187], a[76:79]\n\t"
"v_mfma_f32_16x16x32_bf16 a[76:79], v[96:99], a[188:191], a[76:79]\n\t"
"v_mfma_f32_16x16x32_bf16 a[76:79], a[232:235], a[192:195], a[76:79]\n\t"
"s_waitcnt lgkmcnt(1)\n\t"
"v_mfma_f32_16x16x32_bf16 v[228:231], v[228:231], v[220:223], 0\n\t"
"v_mfma_f32_16x16x32_bf16 a[84:87], a[228:231], a[188:191], a[84:87]\n\t"
"v_mfma_f32_16x16x32_bf16 a[84:87], v[96:99], a[192:195], a[84:87]\n\t"
"v_mfma_f32_16x16x32_bf16 a[84:87], a[232:235], a[196:199], a[84:87]\n\t"
"v_mfma_f32_16x16x32_bf16 a[92:95], a[228:231], a[192:195], a[92:95]\n\t"
"v_mfma_f32_16x16x32_bf16 a[92:95], v[96:99], a[196:199], a[92:95]\n\t"
"s_nop 2\n\t"
"v_cvt_pk_bf16_f32 v226, v228, v229\n\t"
"v_cvt_pk_bf16_f32 v227, v230, v231\n\t"
"v_mfma_f32_16x16x32_bf16 a[92:95], a[232:235], a[200:203], a[92:95]\n\t"
"ds_write2st64_b64 v245, v[224:225], v[226:227] offset0:7 offset1:8\n\t"
"v_mfma_f32_16x16x32_bf16 a[100:103], a[228:231], a[196:199], a[100:103]\n\t"
"v_mfma_f32_16x16x32_bf16 a[100:103], v[96:99], a[200:203], a[100:103]\n\t"
"v_mfma_f32_16x16x32_bf16 a[100:103], a[232:235], a[204:207], a[100:103]\n\t"
"buffer_load_dwordx4 a[228:231], v252, s[8:11], s20 offen offset:1024 sc1\n\t"
"s_waitcnt vmcnt(11)\n\t"
"v_mfma_f32_16x16x32_bf16 a[108:111], a[236:239], a[168:171], a[108:111]\n\t"
"v_mfma_f32_16x16x32_bf16 a[108:111], a[240:243], a[172:175], a[108:111]\n\t"
"v_mfma_f32_16x16x32_bf16 a[108:111], a[212:215], a[176:179], a[108:111]\n\t"
"buffer_load_dwordx4 v[96:99], v252, s[8:11], s20 offen offset:2048 sc1\n\t"
"s_waitcnt lgkmcnt(0)\n\t"
"s_barrier\n\t"
"v_mfma_f32_16x16x32_bf16 a[116:119], a[236:239], a[172:175], a[116:119]\n\t"
"ds_read_b128 a[168:171], v244 offset:0\n\t"
"v_mfma_f32_16x16x32_bf16 a[116:119], a[240:243], a[176:179], a[116:119]\n\t"
"ds_read_b128 a[172:175], v244 offset:512\n\t"
"v_mfma_f32_16x16x32_bf16 a[116:119], a[212:215], a[180:183], a[116:119]\n\t"
"s_mov_b32 s20, 970752\n\t"
"buffer_load_dwordx4 a[232:235], v252, s[8:11], s20 offen offset:0 sc1\n\t"
"v_mfma_f32_16x16x32_bf16 a[124:127], a[236:239], a[176:179], a[124:127]\n\t"
"ds_read_b128 a[176:179], v244 offset:1024\n\t"
"v_mfma_f32_16x16x32_bf16 a[124:127], a[240:243], a[180:183], a[124:127]\n\t"
"v_mfma_f32_16x16x32_bf16 a[124:127], a[212:215], a[184:187], a[124:127]\n\t"
"v_mfma_f32_16x16x32_bf16 a[132:135], a[236:239], a[180:183], a[132:135]\n\t"
"ds_read_b128 a[180:183], v244 offset:1536\n\t"
"v_mfma_f32_16x16x32_bf16 a[132:135], a[240:243], a[184:187], a[132:135]\n\t"
"v_mfma_f32_16x16x32_bf16 a[132:135], a[212:215], a[188:191], a[132:135]\n\t"
"v_mfma_f32_16x16x32_bf16 a[140:143], a[236:239], a[184:187], a[140:143]\n\t"
"ds_read_b128 a[184:187], v244 offset:2048\n\t"
"v_mfma_f32_16x16x32_bf16 a[140:143], a[240:243], a[188:191], a[140:143]\n\t"
"v_mfma_f32_16x16x32_bf16 a[140:143], a[212:215], a[192:195], a[140:143]\n\t"
"v_mfma_f32_16x16x32_bf16 a[148:151], a[236:239], a[188:191], a[148:151]\n\t"
"ds_read_b128 a[188:191], v244 offset:2560\n\t"
"v_mfma_f32_16x16x32_bf16 a[148:151], a[240:243], a[192:195], a[148:151]\n\t"
"v_mfma_f32_16x16x32_bf16 a[148:151], a[212:215], a[196:199], a[148:151]\n\t"
"v_mfma_f32_16x16x32_bf16 a[156:159], a[236:239], a[192:195], a[156:159]\n\t"
"ds_read_b128 a[192:195], v244 offset:3072\n\t"
"v_mfma_f32_16x16x32_bf16 a[156:159], a[240:243], a[196:199], a[156:159]\n\t"
"v_mfma_f32_16x16x32_bf16 a[156:159], a[212:215], a[200:203], a[156:159]\n\t"
"v_mfma_f32_16x16x32_bf16 a[164:167], a[236:239], a[196:199], a[164:167]\n\t"
"ds_read_b128 a[196:199], v244 offset:3584\n\t"
"v_mfma_f32_16x16x32_bf16 a[164:167], a[240:243], a[200:203], a[164:167]\n\t"
"ds_read_b128 a[200:203], v244 offset:4096\n\t"
"v_mfma_f32_16x16x32_bf16 a[164:167], a[212:215], a[204:207], a[164:167]\n\t"
"buffer_load_dwordx4 a[236:239], v252, s[8:11], s20 offen offset:1024 sc1\n\t"
"Lst11_%=:\n\t"
"s_waitcnt vmcnt(11) lgkmcnt(6)\n\t"
"v_mfma_f32_16x16x32_bf16 v[0:3], a[244:247], a[168:171], v[0:3]\n\t"
"ds_read_b128 a[204:207], v244 offset:4608\n\t"
"v_mfma_f32_16x16x32_bf16 v[0:3], a[216:219], a[172:175], v[0:3]\n\t"
"v_mfma_f32_16x16x32_bf16 v[0:3], a[220:223], a[176:179], v[0:3]\n\t"
"buffer_load_dwordx4 a[212:215], v252, s[8:11], s20 offen offset:2048 sc1\n\t"
"s_waitcnt lgkmcnt(6)\n\t"
"v_mfma_f32_16x16x32_bf16 v[4:7], a[244:247], a[172:175], v[4:7]\n\t"
"v_mfma_f32_16x16x32_bf16 v[4:7], a[216:219], a[176:179], v[4:7]\n\t"
"v_mfma_f32_16x16x32_bf16 v[4:7], a[220:223], a[180:183], v[4:7]\n\t"
"s_mov_b32 s20, 983040\n\t"
"buffer_load_dwordx4 a[240:243], v252, s[8:11], s20 offen offset:0 sc1\n\t"
"s_waitcnt lgkmcnt(5)\n\t"
"v_mfma_f32_16x16x32_bf16 v[8:11], a[244:247], a[176:179], v[8:11]\n\t"
"v_mfma_f32_16x16x32_bf16 v[8:11], a[216:219], a[180:183], v[8:11]\n\t"
"v_mfma_f32_16x16x32_bf16 v[8:11], a[220:223], a[184:187], v[8:11]\n\t"
"s_waitcnt lgkmcnt(4)\n\t"
"v_mfma_f32_16x16x32_bf16 v[12:15], a[244:247], a[180:183], v[12:15]\n\t"
"v_mfma_f32_16x16x32_bf16 v[12:15], a[216:219], a[184:187], v[12:15]\n\t"
"v_mfma_f32_16x16x32_bf16 v[12:15], a[220:223], a[188:191], v[12:15]\n\t"
"s_waitcnt lgkmcnt(3)\n\t"
"v_mfma_f32_16x16x32_bf16 v[16:19], a[244:247], a[184:187], v[16:19]\n\t"
"v_mfma_f32_16x16x32_bf16 v[16:19], a[216:219], a[188:191], v[16:19]\n\t"
"v_mfma_f32_16x16x32_bf16 v[16:19], a[220:223], a[192:195], v[16:19]\n\t"
"ds_read_b64_tr_b16 v[232:233], v246 offset:1024\n\t"
"ds_read_b64_tr_b16 v[234:235], v246 offset:4096\n\t"
"s_waitcnt lgkmcnt(4)\n\t"
"v_mfma_f32_16x16x32_bf16 v[20:23], a[244:247], a[188:191], v[20:23]\n\t"
"v_mfma_f32_16x16x32_bf16 v[20:23], a[216:219], a[192:195], v[20:23]\n\t"
"v_mfma_f32_16x16x32_bf16 v[20:23], a[220:223], a[196:199], v[20:23]\n\t"
"s_waitcnt lgkmcnt(3)\n\t"
"v_mfma_f32_16x16x32_bf16 v[24:27], a[244:247], a[192:195], v[24:27]\n\t"
"v_mfma_f32_16x16x32_bf16 v[24:27], a[216:219], a[196:199], v[24:27]\n\t"
"v_mfma_f32_16x16x32_bf16 v[24:27], a[220:223], a[200:203], v[24:27]\n\t"
"s_waitcnt lgkmcnt(2)\n\t"
"v_mfma_f32_16x16x32_bf16 v[28:31], a[244:247], a[196:199], v[28:31]\n\t"
"ds_read_b64_tr_b16 v[228:229], v246 offset:7168\n\t"
"v_mfma_f32_16x16x32_bf16 v[28:31], a[216:219], a[200:203], v[28:31]\n\t"
"ds_read_b64_tr_b16 v[230:231], v246 offset:10240\n\t"
"v_mfma_f32_16x16x32_bf16 v[28:31], a[220:223], a[204:207], v[28:31]\n\t"
"buffer_load_dwordx4 a[244:247], v252, s[8:11], s20 offen offset:1024 sc1\n\t"
"s_waitcnt vmcnt(11)\n\t"
"v_mfma_f32_16x16x32_bf16 v[32:35], a[248:251], a[168:171], v[32:35]\n\t"
"v_mfma_f32_16x16x32_bf16 v[32:35], a[252:255], a[172:175], v[32:35]\n\t"
"v_mfma_f32_16x16x32_bf16 v[32:35], v[236:239], a[176:179], v[32:35]\n\t"
"buffer_load_dwordx4 a[216:219], v252, s[8:11], s20 offen offset:2048 sc1\n\t"
"v_mfma_f32_16x16x32_bf16 v[36:39], a[248:251], a[172:175], v[36:39]\n\t"
"v_mfma_f32_16x16x32_bf16 v[36:39], a[252:255], a[176:179], v[36:39]\n\t"
"v_mfma_f32_16x16x32_bf16 v[36:39], v[236:239], a[180:183], v[36:39]\n\t"
"s_mov_b32 s20, 1142784\n\t"
"buffer_load_dwordx4 a[220:223], v252, s[8:11], s20 offen offset:0 sc1\n\t"
"s_waitcnt lgkmcnt(2)\n\t"
"v_mfma_f32_16x16x32_bf16 v[232:235], v[232:235], v[220:223], 0\n\t"
"v_mfma_f32_16x16x32_bf16 v[40:43], a[248:251], a[176:179], v[40:43]\n\t"
"ds_read_b64_tr_b16 v[224:225], v246 offset:13312\n\t"
"v_mfma_f32_16x16x32_bf16 v[40:43], a[252:255], a[180:183], v[40:43]\n\t"
"ds_read_b64_tr_b16 v[226:227], v246 offset:16384\n\t"
"v_mfma_f32_16x16x32_bf16 v[40:43], v[236:239], a[184:187], v[40:43]\n\t"
"v_mfma_f32_16x16x32_bf16 v[44:47], a[248:251], a[180:183], v[44:47]\n\t"
"v_mfma_f32_16x16x32_bf16 v[44:47], a[252:255], a[184:187], v[44:47]\n\t"
"s_nop 0\n\t"
"v_cvt_pk_bf16_f32 v232, v232, v233\n\t"
"v_mfma_f32_16x16x32_bf16 v[44:47], v[236:239], a[188:191], v[44:47]\n\t"
"v_cvt_pk_bf16_f32 v233, v234, v235\n\t"
"s_waitcnt lgkmcnt(2)\n\t"
"v_mfma_f32_16x16x32_bf16 v[228:231], v[228:231], v[220:223], 0\n\t"
"v_mfma_f32_16x16x32_bf16 v[48:51], a[248:251], a[184:187], v[48:51]\n\t"
"v_mfma_f32_16x16x32_bf16 v[48:51], a[252:255], a[188:191], v[48:51]\n\t"
"v_mfma_f32_16x16x32_bf16 v[48:51], v[236:239], a[192:195], v[48:51]\n\t"
"v_mfma_f32_16x16x32_bf16 v[52:55], a[248:251], a[188:191], v[52:55]\n\t"
"v_mfma_f32_16x16x32_bf16 v[52:55], a[252:255], a[192:195], v[52:55]\n\t"
"s_nop 2\n\t"
"v_cvt_pk_bf16_f32 v234, v228, v229\n\t"
"v_cvt_pk_bf16_f32 v235, v230, v231\n\t"
"v_mfma_f32_16x16x32_bf16 v[52:55], v[236:239], a[196:199], v[52:55]\n\t"
"ds_read_b64_tr_b16 v[228:229], v246 offset:20992\n\t"
"v_mfma_f32_16x16x32_bf16 v[56:59], a[248:251], a[192:195], v[56:59]\n\t"
"ds_read_b64_tr_b16 v[230:231], v246 offset:24064\n\t"
"v_mfma_f32_16x16x32_bf16 v[56:59], a[252:255], a[196:199], v[56:59]\n\t"
"v_mfma_f32_16x16x32_bf16 v[56:59], v[236:239], a[200:203], v[56:59]\n\t"
"s_waitcnt lgkmcnt(2)\n\t"
"v_mfma_f32_16x16x32_bf16 v[224:227], v[224:227], v[220:223], 0\n\t"
"v_mfma_f32_16x16x32_bf16 v[60:63], a[248:251], a[196:199], v[60:63]\n\t"
"v_mfma_f32_16x16x32_bf16 v[60:63], a[252:255], a[200:203], v[60:63]\n\t"
"v_mfma_f32_16x16x32_bf16 v[60:63], v[236:239], a[204:207], v[60:63]\n\t"
"buffer_load_dwordx4 a[248:251], v252, s[8:11], s20 offen offset:1024 sc1\n\t"
"s_waitcnt vmcnt(11)\n\t"
"v_mfma_f32_16x16x32_bf16 v[64:67], a[224:227], a[168:171], v[64:67]\n\t"
"ds_read_b128 a[168:171], v244 offset:9216\n\t"
"v_mfma_f32_16x16x32_bf16 v[64:67], v[240:243], a[172:175], v[64:67]\n\t"
"v_cvt_pk_bf16_f32 v224, v224, v225\n\t"
"v_cvt_pk_bf16_f32 v225, v226, v227\n\t"
"v_mfma_f32_16x16x32_bf16 v[64:67], v[248:251], a[176:179], v[64:67]\n\t"
"v_mfma_f32_16x16x32_bf16 v[68:71], a[224:227], a[172:175], v[68:71]\n\t"
"ds_read_b128 a[172:175], v244 offset:9728\n\t"
"v_mfma_f32_16x16x32_bf16 v[68:71], v[240:243], a[176:179], v[68:71]\n\t"
"v_mfma_f32_16x16x32_bf16 v[68:71], v[248:251], a[180:183], v[68:71]\n\t"
"buffer_load_dwordx4 v[236:239], v252, s[8:11], s20 offen offset:2048 sc1\n\t"
"s_waitcnt lgkmcnt(2)\n\t"
"v_mfma_f32_16x16x32_bf16 v[228:231], v[228:231], v[220:223], 0\n\t"
"s_mov_b32 s20, 1155072\n\t"
"buffer_load_dwordx4 a[252:255], v252, s[8:11], s20 offen offset:0 sc1\n\t"
"v_mfma_f32_16x16x32_bf16 v[72:75], a[224:227], a[176:179], v[72:75]\n\t"
"ds_read_b128 a[176:179], v244 offset:10240\n\t"
"v_mfma_f32_16x16x32_bf16 v[72:75], v[240:243], a[180:183], v[72:75]\n\t"
"v_mfma_f32_16x16x32_bf16 v[72:75], v[248:251], a[184:187], v[72:75]\n\t"
"v_mfma_f32_16x16x32_bf16 v[76:79], a[224:227], a[180:183], v[76:79]\n\t"
"ds_read_b128 a[180:183], v244 offset:10752\n\t"
"v_mfma_f32_16x16x32_bf16 v[76:79], v[240:243], a[184:187], v[76:79]\n\t"
"v_cvt_pk_bf16_f32 v226, v228, v229\n\t"
"v_cvt_pk_bf16_f32 v227, v230, v231\n\t"
"v_mfma_f32_16x16x32_bf16 v[76:79], v[248:251], a[188:191], v[76:79]\n\t"
"ds_read_b64_tr_b16 v[228:229], v246 offset:27136\n\t"
"v_mfma_f32_16x16x32_bf16 v[80:83], a[224:227], a[184:187], v[80:83]\n\t"
"ds_read_b64_tr_b16 v[230:231], v246 offset:30208\n\t"
"v_mfma_f32_16x16x32_bf16 v[80:83], v[240:243], a[188:191], v[80:83]\n\t"
"ds_read_b128 a[184:187], v244 offset:11264\n\t"
"v_mfma_f32_16x16x32_bf16 v[80:83], v[248:251], a[192:195], v[80:83]\n\t"
"v_mfma_f32_16x16x32_bf16 v[84:87], a[224:227], a[188:191], v[84:87]\n\t"
"ds_read_b128 a[188:191], v244 offset:11776\n\t"
"v_mfma_f32_16x16x32_bf16 v[84:87], v[240:243], a[192:195], v[84:87]\n\t"
"v_mfma_f32_16x16x32_bf16 v[84:87], v[248:251], a[196:199], v[84:87]\n\t"
"v_mfma_f32_16x16x32_bf16 v[88:91], a[224:227], a[192:195], v[88:91]\n\t"
"ds_read_b128 a[192:195], v244 offset:12288\n\t"
"v_mfma_f32_16x16x32_bf16 v[88:91], v[240:243], a[196:199], v[88:91]\n\t"
"v_mfma_f32_16x16x32_bf16 v[88:91], v[248:251], a[200:203], v[88:91]\n\t"
"v_mfma_f32_16x16x32_bf16 v[92:95], a[224:227], a[196:199], v[92:95]\n\t"
"ds_read_b128 a[196:199], v244 offset:12800\n\t"
"v_mfma_f32_16x16x32_bf16 v[92:95], v[240:243], a[200:203], v[92:95]\n\t"
"ds_read_b128 a[200:203], v244 offset:13312\n\t"
"v_mfma_f32_16x16x32_bf16 v[92:95], v[248:251], a[204:207], v[92:95]\n\t"
"buffer_load_dwordx4 a[224:227], v252, s[8:11], s20 offen offset:1024 sc1\n\t"
"s_waitcnt lgkmcnt(5)\n\t"
"v_mfma_f32_16x16x32_bf16 v[228:231], v[228:231], v[220:223], 0\n\t"
"ds_read_b128 a[204:207], v244 offset:13824\n\t"
"s_waitcnt vmcnt(11)\n\t"
"v_mfma_f32_16x16x32_bf16 v[100:103], a[208:211], a[168:171], v[100:103]\n\t"
"v_mfma_f32_16x16x32_bf16 v[100:103], a[228:231], a[172:175], v[100:103]\n\t"
"v_mfma_f32_16x16x32_bf16 v[100:103], v[96:99], a[176:179], v[100:103]\n\t"
"buffer_load_dwordx4 v[240:243], v252, s[8:11], s20 offen offset:2048 sc1\n\t"
"v_mfma_f32_16x16x32_bf16 v[104:107], a[208:211], a[172:175], v[104:107]\n\t"
"v_mfma_f32_16x16x32_bf16 v[104:107], a[228:231], a[176:179], v[104:107]\n\t"
"v_cvt_pk_bf16_f32 v228, v228, v229\n\t"
"v_cvt_pk_bf16_f32 v229, v230, v231\n\t"
"v_mfma_f32_16x16x32_bf16 v[104:107], v[96:99], a[180:183], v[104:107]\n\t"
"s_mov_b32 s20, 1167360\n\t"
"buffer_load_dwordx4 v[248:251], v252, s[8:11], s20 offen offset:0 sc1\n\t"
"s_waitcnt lgkmcnt(0)\n\t"
"s_barrier\n\t"
"v_mfma_f32_16x16x32_bf16 v[108:111], a[208:211], a[176:179], v[108:111]\n\t"
"v_mfma_f32_16x16x32_bf16 v[108:111], a[228:231], a[180:183], v[108:111]\n\t"
"v_mfma_f32_16x16x32_bf16 v[108:111], v[96:99], a[184:187], v[108:111]\n\t"
"ds_write2st64_b64 v245, v[232:233], v[234:235] offset0:0 offset1:1\n\t"
"v_mfma_f32_16x16x32_bf16 v[112:115], a[208:211], a[180:183], v[112:115]\n\t"
"ds_read_b64_tr_b16 v[232:233], v246 offset:33280\n\t"
"v_mfma_f32_16x16x32_bf16 v[112:115], a[228:231], a[184:187], v[112:115]\n\t"
"ds_read_b64_tr_b16 v[234:235], v246 offset:36352\n\t"
"v_mfma_f32_16x16x32_bf16 v[112:115], v[96:99], a[188:191], v[112:115]\n\t"
"ds_write2st64_b64 v245, v[224:225], v[226:227] offset0:2 offset1:3\n\t"
"v_mfma_f32_16x16x32_bf16 v[116:119], a[208:211], a[184:187], v[116:119]\n\t"
"ds_read_b64_tr_b16 v[224:225], v246 offset:40960\n\t"
"v_mfma_f32_16x16x32_bf16 v[116:119], a[228:231], a[188:191], v[116:119]\n\t"
"ds_read_b64_tr_b16 v[226:227], v246 offset:44032\n\t"
"v_mfma_f32_16x16x32_bf16 v[116:119], v[96:99], a[192:195], v[116:119]\n\t"
"ds_write_b64 v245, v[228:229] offset:2048\n\t"
"v_mfma_f32_16x16x32_bf16 v[120:123], a[208:211], a[188:191], v[120:123]\n\t"
"ds_read_b64_tr_b16 v[228:229], v246 offset:47104\n\t"
"v_mfma_f32_16x16x32_bf16 v[120:123], a[228:231], a[192:195], v[120:123]\n\t"
"ds_read_b64_tr_b16 v[230:231], v246 offset:50176\n\t"
"v_mfma_f32_16x16x32_bf16 v[120:123], v[96:99], a[196:199], v[120:123]\n\t"
"s_waitcnt lgkmcnt(6)\n\t"
"v_mfma_f32_16x16x32_bf16 v[232:235], v[232:235], v[220:223], 0\n\t"
"v_mfma_f32_16x16x32_bf16 v[124:127], a[208:211], a[192:195], v[124:127]\n\t"
"v_mfma_f32_16x16x32_bf16 v[124:127], a[228:231], a[196:199], v[124:127]\n\t"
"v_mfma_f32_16x16x32_bf16 v[124:127], v[96:99], a[200:203], v[124:127]\n\t"
"s_waitcnt lgkmcnt(3)\n\t"
"v_mfma_f32_16x16x32_bf16 v[224:227], v[224:227], v[220:223], 0\n\t"
"v_mfma_f32_16x16x32_bf16 v[128:131], a[208:211], a[196:199], v[128:131]\n\t"
"s_nop 1\n\t"
"v_cvt_pk_bf16_f32 v232, v232, v233\n\t"
"v_cvt_pk_bf16_f32 v233, v234, v235\n\t"
"v_mfma_f32_16x16x32_bf16 v[128:131], a[228:231], a[200:203], v[128:131]\n\t"
"v_mfma_f32_16x16x32_bf16 v[128:131], v[96:99], a[204:207], v[128:131]\n\t"
"buffer_load_dwordx4 a[208:211], v252, s[8:11], s20 offen offset:1024 sc1\n\t"
"s_waitcnt lgkmcnt(0)\n\t"
"v_mfma_f32_16x16x32_bf16 v[228:231], v[228:231], v[220:223], 0\n\t"
"buffer_load_dwordx4 v[96:99], v252, s[8:11], s20 offen offset:2048 sc1\n\t"
"s_waitcnt vmcnt(12)\n\t"
"v_mfma_f32_16x16x32_bf16 v[132:135], a[232:235], a[168:171], v[132:135]\n\t"
"v_cvt_pk_bf16_f32 v234, v224, v225\n\t"
"v_cvt_pk_bf16_f32 v235, v226, v227\n\t"
"v_mfma_f32_16x16x32_bf16 v[132:135], a[236:239], a[172:175], v[132:135]\n\t"
"ds_read_b64_tr_b16 v[224:225], v246 offset:53248\n\t"
"v_mfma_f32_16x16x32_bf16 v[132:135], a[212:215], a[176:179], v[132:135]\n\t"
"s_mov_b32 s20, 1179648\n\t"
"buffer_load_dwordx4 a[228:231], v252, s[8:11], s20 offen offset:0 sc1\n\t"
"v_mfma_f32_16x16x32_bf16 v[136:139], a[232:235], a[172:175], v[136:139]\n\t"
"ds_read_b64_tr_b16 v[226:227], v246 offset:56320\n\t"
"v_mfma_f32_16x16x32_bf16 v[136:139], a[236:239], a[176:179], v[136:139]\n\t"
"v_cvt_pk_bf16_f32 v228, v228, v229\n\t"
"v_cvt_pk_bf16_f32 v229, v230, v231\n\t"
"v_mfma_f32_16x16x32_bf16 v[136:139], a[212:215], a[180:183], v[136:139]\n\t"
"ds_write2st64_b64 v245, v[232:233], v[234:235] offset0:5 offset1:6\n\t"
"v_mfma_f32_16x16x32_bf16 v[140:143], a[232:235], a[176:179], v[140:143]\n\t"
"v_mfma_f32_16x16x32_bf16 v[140:143], a[236:239], a[180:183], v[140:143]\n\t"
"v_mfma_f32_16x16x32_bf16 v[140:143], a[212:215], a[184:187], v[140:143]\n\t"
"v_mfma_f32_16x16x32_bf16 v[144:147], a[232:235], a[180:183], v[144:147]\n\t"
"v_mfma_f32_16x16x32_bf16 v[144:147], a[236:239], a[184:187], v[144:147]\n\t"
"v_mfma_f32_16x16x32_bf16 v[144:147], a[212:215], a[188:191], v[144:147]\n\t"
"v_mfma_f32_16x16x32_bf16 v[148:151], a[232:235], a[184:187], v[148:151]\n\t"
"v_mfma_f32_16x16x32_bf16 v[148:151], a[236:239], a[188:191], v[148:151]\n\t"
"v_mfma_f32_16x16x32_bf16 v[148:151], a[212:215], a[192:195], v[148:151]\n\t"
"s_waitcnt lgkmcnt(1)\n\t"
"v_mfma_f32_16x16x32_bf16 v[224:227], v[224:227], v[220:223], 0\n\t"
"v_mfma_f32_16x16x32_bf16 v[152:155], a[232:235], a[188:191], v[152:155]\n\t"
"v_mfma_f32_16x16x32_bf16 v[152:155], a[236:239], a[192:195], v[152:155]\n\t"
"v_mfma_f32_16x16x32_bf16 v[152:155], a[212:215], a[196:199], v[152:155]\n\t"
"v_mfma_f32_16x16x32_bf16 v[156:159], a[232:235], a[192:195], v[156:159]\n\t"
"v_mfma_f32_16x16x32_bf16 v[156:159], a[236:239], a[196:199], v[156:159]\n\t"
"s_nop 2\n\t"
"v_cvt_pk_bf16_f32 v230, v224, v225\n\t"
"v_cvt_pk_bf16_f32 v231, v226, v227\n\t"
"v_mfma_f32_16x16x32_bf16 v[156:159], a[212:215], a[200:203], v[156:159]\n\t"
"ds_write2st64_b64 v245, v[228:229], v[230:231] offset0:7 offset1:8\n\t"
"v_mfma_f32_16x16x32_bf16 v[160:163], a[232:235], a[196:199], v[160:163]\n\t"
"v_mfma_f32_16x16x32_bf16 v[160:163], a[236:239], a[200:203], v[160:163]\n\t"
"v_mfma_f32_16x16x32_bf16 v[160:163], a[212:215], a[204:207], v[160:163]\n\t"
"buffer_load_dwordx4 a[232:235], v252, s[8:11], s20 offen offset:1024 sc1\n\t"
"s_waitcnt vmcnt(11)\n\t"
"v_mfma_f32_16x16x32_bf16 v[164:167], a[240:243], a[168:171], v[164:167]\n\t"
"v_mfma_f32_16x16x32_bf16 v[164:167], a[244:247], a[172:175], v[164:167]\n\t"
"v_mfma_f32_16x16x32_bf16 v[164:167], a[216:219], a[176:179], v[164:167]\n\t"
"buffer_load_dwordx4 a[212:215], v252, s[8:11], s20 offen offset:2048 sc1\n\t"
"s_waitcnt lgkmcnt(0)\n\t"
"s_barrier\n\t"
"v_mfma_f32_16x16x32_bf16 v[168:171], a[240:243], a[172:175], v[168:171]\n\t"
"ds_read_b128 a[168:171], v244 offset:0\n\t"
"v_mfma_f32_16x16x32_bf16 v[168:171], a[244:247], a[176:179], v[168:171]\n\t"
"ds_read_b128 a[172:175], v244 offset:512\n\t"
"v_mfma_f32_16x16x32_bf16 v[168:171], a[216:219], a[180:183], v[168:171]\n\t"
"s_mov_b32 s20, 1191936\n\t"
"buffer_load_dwordx4 a[236:239], v252, s[8:11], s20 offen offset:0 sc1\n\t"
"v_mfma_f32_16x16x32_bf16 v[172:175], a[240:243], a[176:179], v[172:175]\n\t"
"ds_read_b128 a[176:179], v244 offset:1024\n\t"
"v_mfma_f32_16x16x32_bf16 v[172:175], a[244:247], a[180:183], v[172:175]\n\t"
"v_mfma_f32_16x16x32_bf16 v[172:175], a[216:219], a[184:187], v[172:175]\n\t"
"v_mfma_f32_16x16x32_bf16 v[176:179], a[240:243], a[180:183], v[176:179]\n\t"
"ds_read_b128 a[180:183], v244 offset:1536\n\t"
"v_mfma_f32_16x16x32_bf16 v[176:179], a[244:247], a[184:187], v[176:179]\n\t"
"v_mfma_f32_16x16x32_bf16 v[176:179], a[216:219], a[188:191], v[176:179]\n\t"
"v_mfma_f32_16x16x32_bf16 v[180:183], a[240:243], a[184:187], v[180:183]\n\t"
"ds_read_b128 a[184:187], v244 offset:2048\n\t"
"v_mfma_f32_16x16x32_bf16 v[180:183], a[244:247], a[188:191], v[180:183]\n\t"
"v_mfma_f32_16x16x32_bf16 v[180:183], a[216:219], a[192:195], v[180:183]\n\t"
"v_mfma_f32_16x16x32_bf16 v[184:187], a[240:243], a[188:191], v[184:187]\n\t"
"ds_read_b128 a[188:191], v244 offset:2560\n\t"
"v_mfma_f32_16x16x32_bf16 v[184:187], a[244:247], a[192:195], v[184:187]\n\t"
"v_mfma_f32_16x16x32_bf16 v[184:187], a[216:219], a[196:199], v[184:187]\n\t"
"v_mfma_f32_16x16x32_bf16 v[188:191], a[240:243], a[192:195], v[188:191]\n\t"
"ds_read_b128 a[192:195], v244 offset:3072\n\t"
"v_mfma_f32_16x16x32_bf16 v[188:191], a[244:247], a[196:199], v[188:191]\n\t"
"v_mfma_f32_16x16x32_bf16 v[188:191], a[216:219], a[200:203], v[188:191]\n\t"
"v_mfma_f32_16x16x32_bf16 v[192:195], a[240:243], a[196:199], v[192:195]\n\t"
"ds_read_b128 a[196:199], v244 offset:3584\n\t"
"v_mfma_f32_16x16x32_bf16 v[192:195], a[244:247], a[200:203], v[192:195]\n\t"
"ds_read_b128 a[200:203], v244 offset:4096\n\t"
"v_mfma_f32_16x16x32_bf16 v[192:195], a[216:219], a[204:207], v[192:195]\n\t"
"buffer_load_dwordx4 a[240:243], v252, s[8:11], s20 offen offset:1024 sc1\n\t"
"Lst12_%=:\n\t"
"s_waitcnt vmcnt(11) lgkmcnt(6)\n\t"
"v_mfma_f32_16x16x32_bf16 v[0:3], a[220:223], a[168:171], v[0:3]\n\t"
"ds_read_b128 a[204:207], v244 offset:4608\n\t"
"v_xor_b32 v246, 0xc0, v246\n\t"
"v_mfma_f32_16x16x32_bf16 v[0:3], a[248:251], a[172:175], v[0:3]\n\t"
"v_mfma_f32_16x16x32_bf16 v[0:3], v[236:239], a[176:179], v[0:3]\n\t"
"s_waitcnt lgkmcnt(6)\n\t"
"v_mfma_f32_16x16x32_bf16 v[4:7], a[220:223], a[172:175], v[4:7]\n\t"
"v_mfma_f32_16x16x32_bf16 v[4:7], a[248:251], a[176:179], v[4:7]\n\t"
"v_mfma_f32_16x16x32_bf16 v[4:7], v[236:239], a[180:183], v[4:7]\n\t"
"buffer_load_dwordx4 a[216:219], v252, s[8:11], s20 offen offset:2048 sc1\n\t"
"s_waitcnt lgkmcnt(5)\n\t"
"v_mfma_f32_16x16x32_bf16 v[8:11], a[220:223], a[176:179], v[8:11]\n\t"
"v_mfma_f32_16x16x32_bf16 v[8:11], a[248:251], a[180:183], v[8:11]\n\t"
"v_mfma_f32_16x16x32_bf16 v[8:11], v[236:239], a[184:187], v[8:11]\n\t"
"s_mov_b32 s20, 1204224\n\t"
"buffer_load_dwordx4 a[244:247], v252, s[8:11], s20 offen offset:0 sc1\n\t"
"s_waitcnt lgkmcnt(4)\n\t"
"v_mfma_f32_16x16x32_bf16 v[12:15], a[220:223], a[180:183], v[12:15]\n\t"
"v_mfma_f32_16x16x32_bf16 v[12:15], a[248:251], a[184:187], v[12:15]\n\t"
"v_mfma_f32_16x16x32_bf16 v[12:15], v[236:239], a[188:191], v[12:15]\n\t"
"s_waitcnt lgkmcnt(3)\n\t"
"v_mfma_f32_16x16x32_bf16 v[16:19], a[220:223], a[184:187], v[16:19]\n\t"
"v_mfma_f32_16x16x32_bf16 v[16:19], a[248:251], a[188:191], v[16:19]\n\t"
"v_mfma_f32_16x16x32_bf16 v[16:19], v[236:239], a[192:195], v[16:19]\n\t"
"ds_read_b64_tr_b16 v[232:233], v246 offset:1024\n\t"
"ds_read_b64_tr_b16 v[234:235], v246 offset:4096\n\t"
"s_waitcnt lgkmcnt(4)\n\t"
"v_mfma_f32_16x16x32_bf16 v[20:23], a[220:223], a[188:191], v[20:23]\n\t"
"v_mfma_f32_16x16x32_bf16 v[20:23], a[248:251], a[192:195], v[20:23]\n\t"
"v_mfma_f32_16x16x32_bf16 v[20:23], v[236:239], a[196:199], v[20:23]\n\t"
"s_waitcnt lgkmcnt(3)\n\t"
"v_mfma_f32_16x16x32_bf16 v[24:27], a[220:223], a[192:195], v[24:27]\n\t"
"v_mfma_f32_16x16x32_bf16 v[24:27], a[248:251], a[196:199], v[24:27]\n\t"
"v_mfma_f32_16x16x32_bf16 v[24:27], v[236:239], a[200:203], v[24:27]\n\t"
"s_waitcnt lgkmcnt(2)\n\t"
"v_mfma_f32_16x16x32_bf16 v[28:31], a[220:223], a[196:199], v[28:31]\n\t"
"ds_read_b64_tr_b16 v[224:225], v246 offset:7168\n\t"
"v_mfma_f32_16x16x32_bf16 v[28:31], a[248:251], a[200:203], v[28:31]\n\t"
"ds_read_b64_tr_b16 v[226:227], v246 offset:10240\n\t"
"v_mfma_f32_16x16x32_bf16 v[28:31], v[236:239], a[204:207], v[28:31]\n\t"
"buffer_load_dwordx4 a[220:223], v252, s[8:11], s20 offen offset:1024 sc1\n\t"
"s_waitcnt vmcnt(11)\n\t"
"v_mfma_f32_16x16x32_bf16 v[32:35], a[252:255], a[168:171], v[32:35]\n\t"
"v_mfma_f32_16x16x32_bf16 v[32:35], a[224:227], a[172:175], v[32:35]\n\t"
"v_mfma_f32_16x16x32_bf16 v[32:35], v[240:243], a[176:179], v[32:35]\n\t"
"buffer_load_dwordx4 v[236:239], v252, s[8:11], s20 offen offset:2048 sc1\n\t"
"v_mfma_f32_16x16x32_bf16 v[36:39], a[252:255], a[172:175], v[36:39]\n\t"
"v_mfma_f32_16x16x32_bf16 v[36:39], a[224:227], a[176:179], v[36:39]\n\t"
"v_mfma_f32_16x16x32_bf16 v[36:39], v[240:243], a[180:183], v[36:39]\n\t"
"s_mov_b32 s20, 1216512\n\t"
"buffer_load_dwordx4 a[248:251], v252, s[8:11], s20 offen offset:0 sc1\n\t"
"s_waitcnt lgkmcnt(2)\n\t"
"v_mfma_f32_16x16x32_bf16 v[232:235], v[232:235], v[220:223], 0\n\t"
"v_mfma_f32_16x16x32_bf16 v[40:43], a[252:255], a[176:179], v[40:43]\n\t"
"ds_read_b64_tr_b16 v[228:229], v246 offset:13312\n\t"
"v_mfma_f32_16x16x32_bf16 v[40:43], a[224:227], a[180:183], v[40:43]\n\t"
"ds_read_b64_tr_b16 v[230:231], v246 offset:16384\n\t"
"v_mfma_f32_16x16x32_bf16 v[40:43], v[240:243], a[184:187], v[40:43]\n\t"
"v_mfma_f32_16x16x32_bf16 v[44:47], a[252:255], a[180:183], v[44:47]\n\t"
"v_mfma_f32_16x16x32_bf16 v[44:47], a[224:227], a[184:187], v[44:47]\n\t"
"s_nop 0\n\t"
"v_cvt_pk_bf16_f32 v232, v232, v233\n\t"
"v_mfma_f32_16x16x32_bf16 v[44:47], v[240:243], a[188:191], v[44:47]\n\t"
"v_cvt_pk_bf16_f32 v233, v234, v235\n\t"
"s_waitcnt lgkmcnt(2)\n\t"
"v_mfma_f32_16x16x32_bf16 v[224:227], v[224:227], v[220:223], 0\n\t"
"v_mfma_f32_16x16x32_bf16 v[48:51], a[252:255], a[184:187], v[48:51]\n\t"
"v_mfma_f32_16x16x32_bf16 v[48:51], a[224:227], a[188:191], v[48:51]\n\t"
"v_mfma_f32_16x16x32_bf16 v[48:51], v[240:243], a[192:195], v[48:51]\n\t"
"v_mfma_f32_16x16x32_bf16 v[52:55], a[252:255], a[188:191], v[52:55]\n\t"
"v_mfma_f32_16x16x32_bf16 v[52:55], a[224:227], a[192:195], v[52:55]\n\t"
"s_nop 2\n\t"
"v_cvt_pk_bf16_f32 v234, v224, v225\n\t"
"v_cvt_pk_bf16_f32 v235, v226, v227\n\t"
"v_mfma_f32_16x16x32_bf16 v[52:55], v[240:243], a[196:199], v[52:55]\n\t"
"ds_read_b64_tr_b16 v[224:225], v246 offset:20992\n\t"
"v_mfma_f32_16x16x32_bf16 v[56:59], a[252:255], a[192:195], v[56:59]\n\t"
"ds_read_b64_tr_b16 v[226:227], v246 offset:24064\n\t"
"v_mfma_f32_16x16x32_bf16 v[56:59], a[224:227], a[196:199], v[56:59]\n\t"
"v_mfma_f32_16x16x32_bf16 v[56:59], v[240:243], a[200:203], v[56:59]\n\t"
"s_waitcnt lgkmcnt(2)\n\t"
"v_mfma_f32_16x16x32_bf16 v[228:231], v[228:231], v[220:223], 0\n\t"
"v_mfma_f32_16x16x32_bf16 v[60:63], a[252:255], a[196:199], v[60:63]\n\t"
"v_mfma_f32_16x16x32_bf16 v[60:63], a[224:227], a[200:203], v[60:63]\n\t"
"v_mfma_f32_16x16x32_bf16 v[60:63], v[240:243], a[204:207], v[60:63]\n\t"
"buffer_load_dwordx4 a[252:255], v252, s[8:11], s20 offen offset:1024 sc1\n\t"
"s_waitcnt vmcnt(11)\n\t"
"v_mfma_f32_16x16x32_bf16 v[64:67], v[248:251], a[168:171], v[64:67]\n\t"
"ds_read_b128 a[168:171], v244 offset:9216\n\t"
"v_mfma_f32_16x16x32_bf16 v[64:67], a[208:211], a[172:175], v[64:67]\n\t"
"v_cvt_pk_bf16_f32 v228, v228, v229\n\t"
"v_cvt_pk_bf16_f32 v229, v230, v231\n\t"
"v_mfma_f32_16x16x32_bf16 v[64:67], v[96:99], a[176:179], v[64:67]\n\t"
"v_mfma_f32_16x16x32_bf16 v[68:71], v[248:251], a[172:175], v[68:71]\n\t"
"ds_read_b128 a[172:175], v244 offset:9728\n\t"
"v_mfma_f32_16x16x32_bf16 v[68:71], a[208:211], a[176:179], v[68:71]\n\t"
"v_mfma_f32_16x16x32_bf16 v[68:71], v[96:99], a[180:183], v[68:71]\n\t"
"buffer_load_dwordx4 v[240:243], v252, s[8:11], s20 offen offset:2048 sc1\n\t"
"s_waitcnt lgkmcnt(2)\n\t"
"v_mfma_f32_16x16x32_bf16 v[224:227], v[224:227], v[220:223], 0\n\t"
"s_mov_b32 s20, 1228800\n\t"
"buffer_load_dwordx4 a[224:227], v252, s[8:11], s20 offen offset:0 sc1\n\t"
"v_mfma_f32_16x16x32_bf16 v[72:75], v[248:251], a[176:179], v[72:75]\n\t"
"ds_read_b128 a[176:179], v244 offset:10240\n\t"
"v_mfma_f32_16x16x32_bf16 v[72:75], a[208:211], a[180:183], v[72:75]\n\t"
"v_mfma_f32_16x16x32_bf16 v[72:75], v[96:99], a[184:187], v[72:75]\n\t"
"v_mfma_f32_16x16x32_bf16 v[76:79], v[248:251], a[180:183], v[76:79]\n\t"
"ds_read_b128 a[180:183], v244 offset:10752\n\t"
"v_mfma_f32_16x16x32_bf16 v[76:79], a[208:211], a[184:187], v[76:79]\n\t"
"v_cvt_pk_bf16_f32 v230, v224, v225\n\t"
"v_cvt_pk_bf16_f32 v231, v226, v227\n\t"
"v_mfma_f32_16x16x32_bf16 v[76:79], v[96:99], a[188:191], v[76:79]\n\t"
"ds_read_b64_tr_b16 v[224:225], v246 offset:27136\n\t"
"v_mfma_f32_16x16x32_bf16 v[80:83], v[248:251], a[184:187], v[80:83]\n\t"
"ds_read_b64_tr_b16 v[226:227], v246 offset:30208\n\t"
"v_mfma_f32_16x16x32_bf16 v[80:83], a[208:211], a[188:191], v[80:83]\n\t"
"ds_read_b128 a[184:187], v244 offset:11264\n\t"
"v_mfma_f32_16x16x32_bf16 v[80:83], v[96:99], a[192:195], v[80:83]\n\t"
"v_mfma_f32_16x16x32_bf16 v[84:87], v[248:251], a[188:191], v[84:87]\n\t"
"ds_read_b128 a[188:191], v244 offset:11776\n\t"
"v_mfma_f32_16x16x32_bf16 v[84:87], a[208:211], a[192:195], v[84:87]\n\t"
"v_mfma_f32_16x16x32_bf16 v[84:87], v[96:99], a[196:199], v[84:87]\n\t"
"v_mfma_f32_16x16x32_bf16 v[88:91], v[248:251], a[192:195], v[88:91]\n\t"
"ds_read_b128 a[192:195], v244 offset:12288\n\t"
"v_mfma_f32_16x16x32_bf16 v[88:91], a[208:211], a[196:199], v[88:91]\n\t"
"v_mfma_f32_16x16x32_bf16 v[88:91], v[96:99], a[200:203], v[88:91]\n\t"
"v_mfma_f32_16x16x32_bf16 v[92:95], v[248:251], a[196:199], v[92:95]\n\t"
"ds_read_b128 a[196:199], v244 offset:12800\n\t"
"v_mfma_f32_16x16x32_bf16 v[92:95], a[208:211], a[200:203], v[92:95]\n\t"
"ds_read_b128 a[200:203], v244 offset:13312\n\t"
"v_mfma_f32_16x16x32_bf16 v[92:95], v[96:99], a[204:207], v[92:95]\n\t"
"buffer_load_dwordx4 v[248:251], v252, s[8:11], s20 offen offset:1024 sc1\n\t"
"s_waitcnt lgkmcnt(5)\n\t"
"v_mfma_f32_16x16x32_bf16 v[224:227], v[224:227], v[220:223], 0\n\t"
"ds_read_b128 a[204:207], v244 offset:13824\n\t"
"s_waitcnt vmcnt(11)\n\t"
"v_mfma_f32_16x16x32_bf16 v[100:103], a[228:231], a[168:171], v[100:103]\n\t"
"v_mfma_f32_16x16x32_bf16 v[100:103], a[232:235], a[172:175], v[100:103]\n\t"
"v_mfma_f32_16x16x32_bf16 v[100:103], a[212:215], a[176:179], v[100:103]\n\t"
"buffer_load_dwordx4 v[96:99], v252, s[8:11], s20 offen offset:2048 sc1\n\t"
"v_mfma_f32_16x16x32_bf16 v[104:107], a[228:231], a[172:175], v[104:107]\n\t"
"v_mfma_f32_16x16x32_bf16 v[104:107], a[232:235], a[176:179], v[104:107]\n\t"
"v_cvt_pk_bf16_f32 v224, v224, v225\n\t"
"v_cvt_pk_bf16_f32 v225, v226, v227\n\t"
"v_mfma_f32_16x16x32_bf16 v[104:107], a[212:215], a[180:183], v[104:107]\n\t"
"s_mov_b32 s20, 1241088\n\t"
"buffer_load_dwordx4 a[208:211], v252, s[8:11], s20 offen offset:0 sc1\n\t"
"s_waitcnt lgkmcnt(0)\n\t"
"s_barrier\n\t"
"v_mfma_f32_16x16x32_bf16 v[108:111], a[228:231], a[176:179], v[108:111]\n\t"
"v_fma_f32 v0, v100, s52, v0\n\t"
"v_fma_f32 v1, v101, s52, v1\n\t"
"v_mfma_f32_16x16x32_bf16 v[108:111], a[232:235], a[180:183], v[108:111]\n\t"
"v_fma_f32 v2, v102, s52, v2\n\t"
"v_fma_f32 v3, v103, s52, v3\n\t"
"v_mfma_f32_16x16x32_bf16 v[108:111], a[212:215], a[184:187], v[108:111]\n\t"
"ds_write2st64_b64 v245, v[232:233], v[234:235] offset0:0 offset1:1\n\t"
"v_mfma_f32_16x16x32_bf16 v[112:115], a[228:231], a[180:183], v[112:115]\n\t"
"ds_read_b64_tr_b16 v[232:233], v246 offset:33280\n\t"
"v_fma_f32 v100, v0, s54, -v100\n\t"
"v_mfma_f32_16x16x32_bf16 v[112:115], a[232:235], a[184:187], v[112:115]\n\t"
"ds_read_b64_tr_b16 v[234:235], v246 offset:36352\n\t"
"v_fma_f32 v101, v1, s54, -v101\n\t"
"v_mfma_f32_16x16x32_bf16 v[112:115], a[212:215], a[188:191], v[112:115]\n\t"
"ds_write2st64_b64 v245, v[228:229], v[230:231] offset0:2 offset1:3\n\t"
"v_mfma_f32_16x16x32_bf16 v[116:119], a[228:231], a[184:187], v[116:119]\n\t"
"ds_read_b64_tr_b16 v[228:229], v246 offset:40960\n\t"
"v_mfma_f32_16x16x32_bf16 v[116:119], a[232:235], a[188:191], v[116:119]\n\t"
"ds_read_b64_tr_b16 v[230:231], v246 offset:44032\n\t"
"v_mfma_f32_16x16x32_bf16 v[116:119], a[212:215], a[192:195], v[116:119]\n\t"
"ds_write_b64 v245, v[224:225] offset:2048\n\t"
"v_mfma_f32_16x16x32_bf16 v[120:123], a[228:231], a[188:191], v[120:123]\n\t"
"v_mfma_f32_16x16x32_bf16 v[120:123], a[232:235], a[192:195], v[120:123]\n\t"
"v_mfma_f32_16x16x32_bf16 v[120:123], a[212:215], a[196:199], v[120:123]\n\t"
"ds_read_b64_tr_b16 v[224:225], v246 offset:47104\n\t"
"s_waitcnt lgkmcnt(5)\n\t"
"v_mfma_f32_16x16x32_bf16 v[232:235], v[232:235], v[220:223], 0\n\t"
"ds_read_b64_tr_b16 v[226:227], v246 offset:50176\n\t"
"v_mfma_f32_16x16x32_bf16 v[124:127], a[228:231], a[192:195], v[124:127]\n\t"
"v_mfma_f32_16x16x32_bf16 v[124:127], a[232:235], a[196:199], v[124:127]\n\t"
"v_fma_f32 v102, v2, s54, -v102\n\t"
"v_mfma_f32_16x16x32_bf16 v[124:127], a[212:215], a[200:203], v[124:127]\n\t"
"s_waitcnt lgkmcnt(3)\n\t"
"v_mfma_f32_16x16x32_bf16 v[228:231], v[228:231], v[220:223], 0\n\t"
"v_fma_f32 v103, v3, s54, -v103\n\t"
"v_mfma_f32_16x16x32_bf16 v[128:131], a[228:231], a[196:199], v[128:131]\n\t"
"v_cvt_pk_bf16_f32 v232, v232, v233\n\t"
"v_cvt_pk_bf16_f32 v233, v234, v235\n\t"
"v_mfma_f32_16x16x32_bf16 v[128:131], a[232:235], a[200:203], v[128:131]\n\t"
"v_fma_f32 v4, v104, s52, v4\n\t"
"v_mfma_f32_16x16x32_bf16 v[128:131], a[212:215], a[204:207], v[128:131]\n\t"
"buffer_load_dwordx4 a[228:231], v252, s[8:11], s20 offen offset:1024 sc1\n\t"
"s_waitcnt lgkmcnt(0)\n\t"
"v_mfma_f32_16x16x32_bf16 v[224:227], v[224:227], v[220:223], 0\n\t"
"buffer_load_dwordx4 a[212:215], v252, s[8:11], s20 offen offset:2048 sc1\n\t"
"s_waitcnt vmcnt(12)\n\t"
"v_mfma_f32_16x16x32_bf16 v[132:135], a[236:239], a[168:171], v[132:135]\n\t"
"v_cvt_pk_bf16_f32 v234, v228, v229\n\t"
"v_cvt_pk_bf16_f32 v235, v230, v231\n\t"
"v_mfma_f32_16x16x32_bf16 v[132:135], a[240:243], a[172:175], v[132:135]\n\t"
"ds_read_b64_tr_b16 v[228:229], v246 offset:53248\n\t"
"v_fma_f32 v5, v105, s52, v5\n\t"
"v_mfma_f32_16x16x32_bf16 v[132:135], a[216:219], a[176:179], v[132:135]\n\t"
"s_mov_b32 s20, 1253376\n\t"
"buffer_load_dwordx4 a[232:235], v252, s[8:11], s20 offen offset:0 sc1\n\t"
"v_mfma_f32_16x16x32_bf16 v[136:139], a[236:239], a[172:175], v[136:139]\n\t"
"ds_read_b64_tr_b16 v[230:231], v246 offset:56320\n\t"
"v_fma_f32 v6, v106, s52, v6\n\t"
"v_mfma_f32_16x16x32_bf16 v[136:139], a[240:243], a[176:179], v[136:139]\n\t"
"v_cvt_pk_bf16_f32 v224, v224, v225\n\t"
"v_cvt_pk_bf16_f32 v225, v226, v227\n\t"
"v_mfma_f32_16x16x32_bf16 v[136:139], a[216:219], a[180:183], v[136:139]\n\t"
"ds_write2st64_b64 v245, v[232:233], v[234:235] offset0:5 offset1:6\n\t"
"v_mfma_f32_16x16x32_bf16 v[140:143], a[236:239], a[176:179], v[140:143]\n\t"
"v_fma_f32 v7, v107, s52, v7\n\t"
"v_fma_f32 v104, v4, s54, -v104\n\t"
"v_mfma_f32_16x16x32_bf16 v[140:143], a[240:243], a[180:183], v[140:143]\n\t"
"v_fma_f32 v105, v5, s54, -v105\n\t"
"v_fma_f32 v106, v6, s54, -v106\n\t"
"v_mfma_f32_16x16x32_bf16 v[140:143], a[216:219], a[184:187], v[140:143]\n\t"
"v_fma_f32 v107, v7, s54, -v107\n\t"
"v_fma_f32 v8, v108, s52, v8\n\t"
"v_mfma_f32_16x16x32_bf16 v[144:147], a[236:239], a[180:183], v[144:147]\n\t"
"v_fma_f32 v9, v109, s52, v9\n\t"
"v_fma_f32 v10, v110, s52, v10\n\t"
"v_mfma_f32_16x16x32_bf16 v[144:147], a[240:243], a[184:187], v[144:147]\n\t"
"v_fma_f32 v11, v111, s52, v11\n\t"
"v_fma_f32 v108, v8, s54, -v108\n\t"
"v_mfma_f32_16x16x32_bf16 v[144:147], a[216:219], a[188:191], v[144:147]\n\t"
"v_fma_f32 v109, v9, s54, -v109\n\t"
"v_mfma_f32_16x16x32_bf16 v[148:151], a[236:239], a[184:187], v[148:151]\n\t"
"v_fma_f32 v110, v10, s54, -v110\n\t"
"v_fma_f32 v111, v11, s54, -v111\n\t"
"v_mfma_f32_16x16x32_bf16 v[148:151], a[240:243], a[188:191], v[148:151]\n\t"
"v_fma_f32 v12, v112, s52, v12\n\t"
"v_mfma_f32_16x16x32_bf16 v[148:151], a[216:219], a[192:195], v[148:151]\n\t"
"v_fma_f32 v13, v113, s52, v13\n\t"
"s_waitcnt lgkmcnt(1)\n\t"
"v_mfma_f32_16x16x32_bf16 v[228:231], v[228:231], v[220:223], 0\n\t"
"v_fma_f32 v14, v114, s52, v14\n\t"
"v_fma_f32 v15, v115, s52, v15\n\t"
"v_mfma_f32_16x16x32_bf16 v[152:155], a[236:239], a[188:191], v[152:155]\n\t"
"v_fma_f32 v112, v12, s54, -v112\n\t"
"v_mfma_f32_16x16x32_bf16 v[152:155], a[240:243], a[192:195], v[152:155]\n\t"
"v_fma_f32 v113, v13, s54, -v113\n\t"
"v_mfma_f32_16x16x32_bf16 v[152:155], a[216:219], a[196:199], v[152:155]\n\t"
"v_fma_f32 v114, v14, s54, -v114\n\t"
"v_fma_f32 v115, v15, s54, -v115\n\t"
"v_mfma_f32_16x16x32_bf16 v[156:159], a[236:239], a[192:195], v[156:159]\n\t"
"v_fma_f32 v16, v116, s52, v16\n\t"
"v_fma_f32 v17, v117, s52, v17\n\t"
"v_mfma_f32_16x16x32_bf16 v[156:159], a[240:243], a[196:199], v[156:159]\n\t"
"v_cvt_pk_bf16_f32 v226, v228, v229\n\t"
"v_cvt_pk_bf16_f32 v227, v230, v231\n\t"
"v_mfma_f32_16x16x32_bf16 v[156:159], a[216:219], a[200:203], v[156:159]\n\t"
"ds_write2st64_b64 v245, v[224:225], v[226:227] offset0:7 offset1:8\n\t"
"v_mfma_f32_16x16x32_bf16 v[160:163], a[236:239], a[196:199], v[160:163]\n\t"
"v_fma_f32 v18, v118, s52, v18\n\t"
"v_fma_f32 v19, v119, s52, v19\n\t"
"v_mfma_f32_16x16x32_bf16 v[160:163], a[240:243], a[200:203], v[160:163]\n\t"
"v_fma_f32 v116, v16, s54, -v116\n\t"
"v_fma_f32 v117, v17, s54, -v117\n\t"
"v_mfma_f32_16x16x32_bf16 v[160:163], a[216:219], a[204:207], v[160:163]\n\t"
"buffer_load_dwordx4 a[236:239], v252, s[8:11], s20 offen offset:1024 sc1\n\t"
"s_waitcnt vmcnt(11)\n\t"
"v_mfma_f32_16x16x32_bf16 v[164:167], a[244:247], a[168:171], v[164:167]\n\t"
"v_fma_f32 v118, v18, s54, -v118\n\t"
"v_fma_f32 v119, v19, s54, -v119\n\t"
"v_mfma_f32_16x16x32_bf16 v[164:167], a[220:223], a[172:175], v[164:167]\n\t"
"v_fma_f32 v20, v120, s52, v20\n\t"
"v_fma_f32 v21, v121, s52, v21\n\t"
"v_mfma_f32_16x16x32_bf16 v[164:167], v[236:239], a[176:179], v[164:167]\n\t"
"buffer_load_dwordx4 a[216:219], v252, s[8:11], s20 offen offset:2048 sc1\n\t"
"s_waitcnt lgkmcnt(0)\n\t"
"s_barrier\n\t"
"v_mfma_f32_16x16x32_bf16 v[168:171], a[244:247], a[172:175], v[168:171]\n\t"
"ds_read_b128 a[168:171], v244 offset:0\n\t"
"v_fma_f32 v22, v122, s52, v22\n\t"
"v_mfma_f32_16x16x32_bf16 v[168:171], a[220:223], a[176:179], v[168:171]\n\t"
"ds_read_b128 a[172:175], v244 offset:512\n\t"
"v_fma_f32 v23, v123, s52, v23\n\t"
"v_mfma_f32_16x16x32_bf16 v[168:171], v[236:239], a[180:183], v[168:171]\n\t"
"s_mov_b32 s20, 1265664\n\t"
"buffer_load_dwordx4 a[240:243], v252, s[8:11], s20 offen offset:0 sc1\n\t"
"v_fma_f32 v120, v20, s54, -v120\n\t"
"v_fma_f32 v121, v21, s54, -v121\n\t"
"v_mfma_f32_16x16x32_bf16 v[172:175], a[244:247], a[176:179], v[172:175]\n\t"
"ds_read_b128 a[176:179], v244 offset:1024\n\t"
"v_fma_f32 v122, v22, s54, -v122\n\t"
"v_mfma_f32_16x16x32_bf16 v[172:175], a[220:223], a[180:183], v[172:175]\n\t"
"v_fma_f32 v123, v23, s54, -v123\n\t"
"v_fma_f32 v24, v124, s52, v24\n\t"
"v_mfma_f32_16x16x32_bf16 v[172:175], v[236:239], a[184:187], v[172:175]\n\t"
"v_fma_f32 v25, v125, s52, v25\n\t"
"v_fma_f32 v26, v126, s52, v26\n\t"
"v_mfma_f32_16x16x32_bf16 v[176:179], a[244:247], a[180:183], v[176:179]\n\t"
"ds_read_b128 a[180:183], v244 offset:1536\n\t"
"v_fma_f32 v27, v127, s52, v27\n\t"
"v_mfma_f32_16x16x32_bf16 v[176:179], a[220:223], a[184:187], v[176:179]\n\t"
"v_fma_f32 v124, v24, s54, -v124\n\t"
"v_fma_f32 v125, v25, s54, -v125\n\t"
"v_mfma_f32_16x16x32_bf16 v[176:179], v[236:239], a[188:191], v[176:179]\n\t"
"v_fma_f32 v126, v26, s54, -v126\n\t"
"v_fma_f32 v127, v27, s54, -v127\n\t"
"v_fma_f32 v28, v128, s52, v28\n\t"
"v_fma_f32 v29, v129, s52, v29\n\t"
"v_mfma_f32_16x16x32_bf16 v[180:183], a[244:247], a[184:187], v[180:183]\n\t"
"ds_read_b128 a[184:187], v244 offset:2048\n\t"
"v_fma_f32 v30, v130, s52, v30\n\t"
"v_mfma_f32_16x16x32_bf16 v[180:183], a[220:223], a[188:191], v[180:183]\n\t"
"v_fma_f32 v31, v131, s52, v31\n\t"
"v_fma_f32 v128, v28, s54, -v128\n\t"
"v_mfma_f32_16x16x32_bf16 v[180:183], v[236:239], a[192:195], v[180:183]\n\t"
"v_fma_f32 v129, v29, s54, -v129\n\t"
"v_fma_f32 v130, v30, s54, -v130\n\t"
"v_fma_f32 v131, v31, s54, -v131\n\t"
"v_fma_f32 v32, v132, s52, v32\n\t"
"v_mfma_f32_16x16x32_bf16 v[184:187], a[244:247], a[188:191], v[184:187]\n\t"
"ds_read_b128 a[188:191], v244 offset:2560\n\t"
"v_fma_f32 v33, v133, s52, v33\n\t"
"v_mfma_f32_16x16x32_bf16 v[184:187], a[220:223], a[192:195], v[184:187]\n\t"
"v_fma_f32 v34, v134, s52, v34\n\t"
"v_fma_f32 v35, v135, s52, v35\n\t"
"v_mfma_f32_16x16x32_bf16 v[184:187], v[236:239], a[196:199], v[184:187]\n\t"
"v_fma_f32 v132, v32, s54, -v132\n\t"
"v_fma_f32 v133, v33, s54, -v133\n\t"
"v_fma_f32 v134, v34, s54, -v134\n\t"
"v_fma_f32 v135, v35, s54, -v135\n\t"
"v_mfma_f32_16x16x32_bf16 v[188:191], a[244:247], a[192:195], v[188:191]\n\t"
"ds_read_b128 a[192:195], v244 offset:3072\n\t"
"v_fma_f32 v36, v136, s52, v36\n\t"
"v_mfma_f32_16x16x32_bf16 v[188:191], a[220:223], a[196:199], v[188:191]\n\t"
"v_fma_f32 v37, v137, s52, v37\n\t"
"v_fma_f32 v38, v138, s52, v38\n\t"
"v_mfma_f32_16x16x32_bf16 v[188:191], v[236:239], a[200:203], v[188:191]\n\t"
"v_fma_f32 v39, v139, s52, v39\n\t"
"v_fma_f32 v136, v36, s54, -v136\n\t"
"v_fma_f32 v137, v37, s54, -v137\n\t"
"v_fma_f32 v138, v38, s54, -v138\n\t"
"v_mfma_f32_16x16x32_bf16 v[192:195], a[244:247], a[196:199], v[192:195]\n\t"
"ds_read_b128 a[196:199], v244 offset:3584\n\t"
"v_fma_f32 v139, v39, s54, -v139\n\t"
"v_mfma_f32_16x16x32_bf16 v[192:195], a[220:223], a[200:203], v[192:195]\n\t"
"ds_read_b128 a[200:203], v244 offset:4096\n\t"
"v_fma_f32 v40, v140, s52, v40\n\t"
"v_mfma_f32_16x16x32_bf16 v[192:195], v[236:239], a[204:207], v[192:195]\n\t"
"buffer_load_dwordx4 a[244:247], v252, s[8:11], s20 offen offset:1024 sc1\n\t"
"ds_read_b128 a[204:207], v244 offset:4608\n\t"
"v_fma_f32 v41, v141, s52, v41\n\t"
"Lst13_%=:\n\t"
"s_waitcnt vmcnt(11) lgkmcnt(7)\n\t"
"v_mfma_f32_16x16x32_bf16 v[196:199], a[248:251], a[168:171], v[196:199]\n\t"
"v_fma_f32 v42, v142, s52, v42\n\t"
"v_fma_f32 v43, v143, s52, v43\n\t"
"v_mfma_f32_16x16x32_bf16 v[196:199], a[252:255], a[172:175], v[196:199]\n\t"
"v_fma_f32 v140, v40, s54, -v140\n\t"
"v_fma_f32 v141, v41, s54, -v141\n\t"
"v_mfma_f32_16x16x32_bf16 v[196:199], v[240:243], a[176:179], v[196:199]\n\t"
"buffer_load_dwordx4 a[220:223], v252, s[8:11], s20 offen offset:2048 sc1\n\t"
"v_fma_f32 v142, v42, s54, -v142\n\t"
"v_fma_f32 v143, v43, s54, -v143\n\t"
"s_waitcnt lgkmcnt(6)\n\t"
"v_mfma_f32_16x16x32_bf16 v[204:207], a[248:251], a[172:175], v[204:207]\n\t"
"v_fma_f32 v44, v144, s52, v44\n\t"
"v_fma_f32 v45, v145, s52, v45\n\t"
"v_mfma_f32_16x16x32_bf16 v[204:207], a[252:255], a[176:179], v[204:207]\n\t"
"v_fma_f32 v46, v146, s52, v46\n\t"
"v_fma_f32 v47, v147, s52, v47\n\t"
"v_mfma_f32_16x16x32_bf16 v[204:207], v[240:243], a[180:183], v[204:207]\n\t"
"v_fma_f32 v144, v44, s54, -v144\n\t"
"v_fma_f32 v145, v45, s54, -v145\n\t"
"v_fma_f32 v146, v46, s54, -v146\n\t"
"v_fma_f32 v147, v47, s54, -v147\n\t"
"s_waitcnt lgkmcnt(5)\n\t"
"v_mfma_f32_16x16x32_bf16 v[212:215], a[248:251], a[176:179], v[212:215]\n\t"
"v_fma_f32 v48, v148, s52, v48\n\t"
"v_fma_f32 v49, v149, s52, v49\n\t"
"v_mfma_f32_16x16x32_bf16 v[212:215], a[252:255], a[180:183], v[212:215]\n\t"
"v_fma_f32 v50, v150, s52, v50\n\t"
"v_fma_f32 v51, v151, s52, v51\n\t"
"v_mfma_f32_16x16x32_bf16 v[212:215], v[240:243], a[184:187], v[212:215]\n\t"
"v_fma_f32 v148, v48, s54, -v148\n\t"
"v_fma_f32 v149, v49, s54, -v149\n\t"
"v_fma_f32 v150, v50, s54, -v150\n\t"
"v_fma_f32 v151, v51, s54, -v151\n\t"
"s_waitcnt lgkmcnt(4)\n\t"
"v_mfma_f32_16x16x32_bf16 a[0:3], a[248:251], a[180:183], a[0:3]\n\t"
"v_fma_f32 v52, v152, s52, v52\n\t"
"v_fma_f32 v53, v153, s52, v53\n\t"
"v_mfma_f32_16x16x32_bf16 a[0:3], a[252:255], a[184:187], a[0:3]\n\t"
"v_fma_f32 v54, v154, s52, v54\n\t"
"v_fma_f32 v55, v155, s52, v55\n\t"
"v_mfma_f32_16x16x32_bf16 a[0:3], v[240:243], a[188:191], a[0:3]\n\t"
"v_fma_f32 v152, v52, s54, -v152\n\t"
"v_fma_f32 v153, v53, s54, -v153\n\t"
"v_fma_f32 v154, v54, s54, -v154\n\t"
"v_fma_f32 v155, v55, s54, -v155\n\t"
"s_waitcnt lgkmcnt(3)\n\t"
"v_mfma_f32_16x16x32_bf16 a[8:11], a[248:251], a[184:187], a[8:11]\n\t"
"v_fma_f32 v56, v156, s52, v56\n\t"
"v_fma_f32 v57, v157, s52, v57\n\t"
"v_mfma_f32_16x16x32_bf16 a[8:11], a[252:255], a[188:191], a[8:11]\n\t"
"v_fma_f32 v58, v158, s52, v58\n\t"
"v_fma_f32 v59, v159, s52, v59\n\t"
"v_mfma_f32_16x16x32_bf16 a[8:11], v[240:243], a[192:195], a[8:11]\n\t"
"v_fma_f32 v156, v56, s54, -v156\n\t"
"v_fma_f32 v157, v57, s54, -v157\n\t"
"v_fma_f32 v158, v58, s54, -v158\n\t"
"v_fma_f32 v159, v59, s54, -v159\n\t"
"s_waitcnt lgkmcnt(2)\n\t"
"v_mfma_f32_16x16x32_bf16 a[16:19], a[248:251], a[188:191], a[16:19]\n\t"
"v_fma_f32 v60, v160, s52, v60\n\t"
"v_fma_f32 v61, v161, s52, v61\n\t"
"v_mfma_f32_16x16x32_bf16 a[16:19], a[252:255], a[192:195], a[16:19]\n\t"
"v_fma_f32 v62, v162, s52, v62\n\t"
"v_fma_f32 v63, v163, s52, v63\n\t"
"v_mfma_f32_16x16x32_bf16 a[16:19], v[240:243], a[196:199], a[16:19]\n\t"
"v_fma_f32 v160, v60, s54, -v160\n\t"
"v_fma_f32 v161, v61, s54, -v161\n\t"
"v_fma_f32 v162, v62, s54, -v162\n\t"
"v_fma_f32 v163, v63, s54, -v163\n\t"
"s_waitcnt lgkmcnt(1)\n\t"
"v_mfma_f32_16x16x32_bf16 a[24:27], a[248:251], a[192:195], a[24:27]\n\t"
"v_fma_f32 v64, v164, s52, v64\n\t"
"v_fma_f32 v65, v165, s52, v65\n\t"
"v_mfma_f32_16x16x32_bf16 a[24:27], a[252:255], a[196:199], a[24:27]\n\t"
"v_fma_f32 v66, v166, s52, v66\n\t"
"v_fma_f32 v67, v167, s52, v67\n\t"
"v_mfma_f32_16x16x32_bf16 a[24:27], v[240:243], a[200:203], a[24:27]\n\t"
"v_fma_f32 v164, v64, s54, -v164\n\t"
"v_fma_f32 v165, v65, s54, -v165\n\t"
"s_waitcnt lgkmcnt(0)\n\t"
"v_mfma_f32_16x16x32_bf16 a[32:35], a[248:251], a[196:199], a[32:35]\n\t"
"v_fma_f32 v166, v66, s54, -v166\n\t"
"v_fma_f32 v167, v67, s54, -v167\n\t"
"v_mfma_f32_16x16x32_bf16 a[32:35], a[252:255], a[200:203], a[32:35]\n\t"
"v_fma_f32 v68, v168, s52, v68\n\t"
"v_fma_f32 v69, v169, s52, v69\n\t"
"v_mfma_f32_16x16x32_bf16 a[32:35], v[240:243], a[204:207], a[32:35]\n\t"
"s_mov_b32 s20, 1277952\n\t"
"buffer_load_dwordx4 a[248:251], v252, s[8:11], s20 offen offset:0 sc1\n\t"
"v_fma_f32 v70, v170, s52, v70\n\t"
"v_fma_f32 v71, v171, s52, v71\n\t"
"s_waitcnt vmcnt(10)\n\t"
"v_mfma_f32_16x16x32_bf16 a[40:43], a[224:227], a[168:171], a[40:43]\n\t"
"v_fma_f32 v168, v68, s54, -v168\n\t"
"v_fma_f32 v169, v69, s54, -v169\n\t"
"v_mfma_f32_16x16x32_bf16 a[40:43], v[248:251], a[172:175], a[40:43]\n\t"
"v_fma_f32 v170, v70, s54, -v170\n\t"
"v_fma_f32 v171, v71, s54, -v171\n\t"
"v_mfma_f32_16x16x32_bf16 a[40:43], v[96:99], a[176:179], a[40:43]\n\t"
"buffer_load_dwordx4 v[240:243], v252, s[8:11], s20 offen offset:1024 sc1\n\t"
"v_mfma_f32_16x16x32_bf16 a[48:51], a[224:227], a[172:175], a[48:51]\n\t"
"v_fma_f32 v72, v172, s52, v72\n\t"
"v_fma_f32 v73, v173, s52, v73\n\t"
"v_mfma_f32_16x16x32_bf16 a[48:51], v[248:251], a[176:179], a[48:51]\n\t"
"v_fma_f32 v74, v174, s52, v74\n\t"
"v_fma_f32 v75, v175, s52, v75\n\t"
"v_mfma_f32_16x16x32_bf16 a[48:51], v[96:99], a[180:183], a[48:51]\n\t"
"buffer_load_dwordx4 a[252:255], v252, s[8:11], s20 offen offset:2048 sc1\n\t"
"v_fma_f32 v172, v72, s54, -v172\n\t"
"v_fma_f32 v173, v73, s54, -v173\n\t"
"v_mfma_f32_16x16x32_bf16 a[56:59], a[224:227], a[176:179], a[56:59]\n\t"
"v_fma_f32 v174, v74, s54, -v174\n\t"
"v_fma_f32 v175, v75, s54, -v175\n\t"
"v_mfma_f32_16x16x32_bf16 a[56:59], v[248:251], a[180:183], a[56:59]\n\t"
"v_fma_f32 v76, v176, s52, v76\n\t"
"v_fma_f32 v77, v177, s52, v77\n\t"
"v_mfma_f32_16x16x32_bf16 a[56:59], v[96:99], a[184:187], a[56:59]\n\t"
"v_fma_f32 v78, v178, s52, v78\n\t"
"v_fma_f32 v79, v179, s52, v79\n\t"
"v_mfma_f32_16x16x32_bf16 a[64:67], a[224:227], a[180:183], a[64:67]\n\t"
"v_fma_f32 v176, v76, s54, -v176\n\t"
"v_fma_f32 v177, v77, s54, -v177\n\t"
"v_mfma_f32_16x16x32_bf16 a[64:67], v[248:251], a[184:187], a[64:67]\n\t"
"v_fma_f32 v178, v78, s54, -v178\n\t"
"v_fma_f32 v179, v79, s54, -v179\n\t"
"v_mfma_f32_16x16x32_bf16 a[64:67], v[96:99], a[188:191], a[64:67]\n\t"
"v_fma_f32 v80, v180, s52, v80\n\t"
"v_fma_f32 v81, v181, s52, v81\n\t"
"v_mfma_f32_16x16x32_bf16 a[72:75], a[224:227], a[184:187], a[72:75]\n\t"
"v_fma_f32 v82, v182, s52, v82\n\t"
"v_fma_f32 v83, v183, s52, v83\n\t"
"v_mfma_f32_16x16x32_bf16 a[72:75], v[248:251], a[188:191], a[72:75]\n\t"
"v_fma_f32 v180, v80, s54, -v180\n\t"
"v_fma_f32 v181, v81, s54, -v181\n\t"
"v_mfma_f32_16x16x32_bf16 a[72:75], v[96:99], a[192:195], a[72:75]\n\t"
"v_fma_f32 v182, v82, s54, -v182\n\t"
"v_fma_f32 v183, v83, s54, -v183\n\t"
"v_mfma_f32_16x16x32_bf16 a[80:83], a[224:227], a[188:191], a[80:83]\n\t"
"v_fma_f32 v84, v184, s52, v84\n\t"
"v_fma_f32 v85, v185, s52, v85\n\t"
"v_mfma_f32_16x16x32_bf16 a[80:83], v[248:251], a[192:195], a[80:83]\n\t"
"v_fma_f32 v86, v186, s52, v86\n\t"
"v_mfma_f32_16x16x32_bf16 a[80:83], v[96:99], a[196:199], a[80:83]\n\t"
"v_fma_f32 v87, v187, s52, v87\n\t"
"v_mfma_f32_16x16x32_bf16 a[88:91], a[224:227], a[192:195], a[88:91]\n\t"
"v_fma_f32 v184, v84, s54, -v184\n\t"
"v_fma_f32 v185, v85, s54, -v185\n\t"
"v_mfma_f32_16x16x32_bf16 a[88:91], v[248:251], a[196:199], a[88:91]\n\t"
"v_fma_f32 v186, v86, s54, -v186\n\t"
"v_mfma_f32_16x16x32_bf16 a[88:91], v[96:99], a[200:203], a[88:91]\n\t"
"v_fma_f32 v187, v87, s54, -v187\n\t"
"v_mfma_f32_16x16x32_bf16 a[96:99], a[224:227], a[196:199], a[96:99]\n\t"
"v_fma_f32 v88, v188, s52, v88\n\t"
"v_fma_f32 v89, v189, s52, v89\n\t"
"v_mfma_f32_16x16x32_bf16 a[96:99], v[248:251], a[200:203], a[96:99]\n\t"
"v_fma_f32 v90, v190, s52, v90\n\t"
"v_mfma_f32_16x16x32_bf16 a[96:99], v[96:99], a[204:207], a[96:99]\n\t"
"s_mov_b32 s20, 442368\n\t"
"buffer_load_dwordx4 a[224:227], v252, s[8:11], s20 offen offset:0 sc1\n\t"
"s_waitcnt vmcnt(10)\n\t"
"v_mfma_f32_16x16x32_bf16 a[104:107], a[208:211], a[168:171], a[104:107]\n\t"
"ds_read_b128 a[168:171], v244 offset:9216\n\t"
"v_fma_f32 v91, v191, s52, v91\n\t"
"v_mfma_f32_16x16x32_bf16 a[104:107], a[228:231], a[172:175], a[104:107]\n\t"
"v_fma_f32 v188, v88, s54, -v188\n\t"
"v_mfma_f32_16x16x32_bf16 a[104:107], a[212:215], a[176:179], a[104:107]\n\t"
"buffer_load_dwordx4 v[248:251], v252, s[8:11], s20 offen offset:1024 sc1\n\t"
"v_mfma_f32_16x16x32_bf16 a[112:115], a[208:211], a[172:175], a[112:115]\n\t"
"ds_read_b128 a[172:175], v244 offset:9728\n\t"
"v_fma_f32 v189, v89, s54, -v189\n\t"
"v_mfma_f32_16x16x32_bf16 a[112:115], a[228:231], a[176:179], a[112:115]\n\t"
"v_fma_f32 v190, v90, s54, -v190\n\t"
"v_mfma_f32_16x16x32_bf16 a[112:115], a[212:215], a[180:183], a[112:115]\n\t"
"v_fma_f32 v191, v91, s54, -v191\n\t"
"v_mfma_f32_16x16x32_bf16 a[120:123], a[208:211], a[176:179], a[120:123]\n\t"
"ds_read_b128 a[176:179], v244 offset:10240\n\t"
"v_mfma_f32_16x16x32_bf16 a[120:123], a[228:231], a[180:183], a[120:123]\n\t"
"v_fma_f32 v92, v192, s52, v92\n\t"
"v_mfma_f32_16x16x32_bf16 a[120:123], a[212:215], a[184:187], a[120:123]\n\t"
"v_fma_f32 v93, v193, s52, v93\n\t"
"v_mfma_f32_16x16x32_bf16 a[128:131], a[208:211], a[180:183], a[128:131]\n\t"
"ds_read_b128 a[180:183], v244 offset:10752\n\t"
"v_fma_f32 v94, v194, s52, v94\n\t"
"v_mfma_f32_16x16x32_bf16 a[128:131], a[228:231], a[184:187], a[128:131]\n\t"
"v_fma_f32 v95, v195, s52, v95\n\t"
"v_mfma_f32_16x16x32_bf16 a[128:131], a[212:215], a[188:191], a[128:131]\n\t"
"v_fma_f32 v192, v92, s54, -v192\n\t"
"v_mfma_f32_16x16x32_bf16 a[136:139], a[208:211], a[184:187], a[136:139]\n\t"
"ds_read_b128 a[184:187], v244 offset:11264\n\t"
"v_mfma_f32_16x16x32_bf16 a[136:139], a[228:231], a[188:191], a[136:139]\n\t"
"v_mfma_f32_16x16x32_bf16 a[136:139], a[212:215], a[192:195], a[136:139]\n\t"
"v_fma_f32 v193, v93, s54, -v193\n\t"
"v_mfma_f32_16x16x32_bf16 a[144:147], a[208:211], a[188:191], a[144:147]\n\t"
"ds_read_b128 a[188:191], v244 offset:11776\n\t"
"v_mfma_f32_16x16x32_bf16 a[144:147], a[228:231], a[192:195], a[144:147]\n\t"
"v_fma_f32 v194, v94, s54, -v194\n\t"
"v_mfma_f32_16x16x32_bf16 a[144:147], a[212:215], a[196:199], a[144:147]\n\t"
"v_mfma_f32_16x16x32_bf16 a[152:155], a[208:211], a[192:195], a[152:155]\n\t"
"ds_read_b128 a[192:195], v244 offset:12288\n\t"
"v_mfma_f32_16x16x32_bf16 a[152:155], a[228:231], a[196:199], a[152:155]\n\t"
"v_fma_f32 v195, v95, s54, -v195\n\t"
"v_mfma_f32_16x16x32_bf16 a[152:155], a[212:215], a[200:203], a[152:155]\n\t"
"v_mfma_f32_16x16x32_bf16 a[160:163], a[208:211], a[196:199], a[160:163]\n\t"
"ds_read_b128 a[196:199], v244 offset:12800\n\t"
"v_mfma_f32_16x16x32_bf16 a[160:163], a[228:231], a[200:203], a[160:163]\n\t"
"ds_read_b128 a[200:203], v244 offset:13312\n\t"
"v_mfma_f32_16x16x32_bf16 a[160:163], a[212:215], a[204:207], a[160:163]\n\t"
"buffer_load_dwordx4 a[208:211], v252, s[8:11], s20 offen offset:2048 sc1\n\t"
"s_waitcnt vmcnt(9) lgkmcnt(6)\n\t"
"v_mfma_f32_16x16x32_bf16 v[200:203], a[232:235], a[168:171], v[200:203]\n\t"
"ds_read_b128 a[204:207], v244 offset:13824\n\t"
"v_mfma_f32_16x16x32_bf16 v[200:203], a[236:239], a[172:175], v[200:203]\n\t"
"v_mfma_f32_16x16x32_bf16 v[200:203], a[216:219], a[176:179], v[200:203]\n\t"
"s_mov_b32 s20, 454656\n\t"
"buffer_load_dwordx4 a[212:215], v252, s[8:11], s20 offen offset:0 sc1\n\t"
"s_waitcnt lgkmcnt(6)\n\t"
"v_mfma_f32_16x16x32_bf16 v[208:211], a[232:235], a[172:175], v[208:211]\n\t"
"v_mfma_f32_16x16x32_bf16 v[208:211], a[236:239], a[176:179], v[208:211]\n\t"
"v_mfma_f32_16x16x32_bf16 v[208:211], a[216:219], a[180:183], v[208:211]\n\t"
"buffer_load_dwordx4 a[228:231], v252, s[8:11], s20 offen offset:1024 sc1\n\t"
"s_waitcnt lgkmcnt(0)\n\t"
"s_barrier\n\t"
"v_mfma_f32_16x16x32_bf16 v[216:219], a[232:235], a[176:179], v[216:219]\n\t"
"v_mfma_f32_16x16x32_bf16 v[216:219], a[236:239], a[180:183], v[216:219]\n\t"
"v_mfma_f32_16x16x32_bf16 v[216:219], a[216:219], a[184:187], v[216:219]\n\t"
"v_pk_fma_f32 v[196:197], v[200:201], s[56:57], v[196:197]\n\t"
"v_pk_fma_f32 v[200:201], v[196:197], s[58:59], v[200:201] neg_lo:[0,0,1] neg_hi:[0,0,1]\n\t"
"v_pk_fma_f32 v[228:229], v[0:1], s[60:61], v[196:197]\n\t"
"v_pk_fma_f32 v[0:1], v[196:197], s[60:61], v[0:1]\n\t"
"v_pk_fma_f32 v[196:197], v[100:101], s[62:63], v[200:201]\n\t"
"v_pk_fma_f32 v[100:101], v[200:201], s[62:63], v[100:101]\n\t"
"v_pk_fma_f32 v[198:199], v[202:203], s[56:57], v[198:199]\n\t"
"v_pk_fma_f32 v[202:203], v[198:199], s[58:59], v[202:203] neg_lo:[0,0,1] neg_hi:[0,0,1]\n\t"
"v_pk_fma_f32 v[234:235], v[2:3], s[60:61], v[198:199]\n\t"
"v_pk_fma_f32 v[2:3], v[198:199], s[60:61], v[2:3]\n\t"
"v_pk_fma_f32 v[198:199], v[102:103], s[62:63], v[202:203]\n\t"
"v_pk_fma_f32 v[102:103], v[202:203], s[62:63], v[102:103]\n\t"
"v_cvt_pk_bf16_f32 v196, v196, v197\n\t"
"v_cvt_pk_bf16_f32 v200, v228, v229\n\t"
"v_mfma_f32_16x16x32_bf16 a[4:7], a[232:235], a[180:183], a[4:7]\n\t"
"v_cvt_pk_bf16_f32 v197, v198, v199\n\t"
"v_mfma_f32_16x16x32_bf16 a[4:7], a[236:239], a[184:187], a[4:7]\n\t"
"v_cvt_pk_bf16_f32 v201, v234, v235\n\t"
"v_mfma_f32_16x16x32_bf16 a[4:7], a[216:219], a[188:191], a[4:7]\n\t"
"v_pk_fma_f32 v[204:205], v[208:209], s[56:57], v[204:205]\n\t"
"v_pk_fma_f32 v[208:209], v[204:205], s[58:59], v[208:209] neg_lo:[0,0,1] neg_hi:[0,0,1]\n\t"
"v_pk_fma_f32 v[98:99], v[4:5], s[60:61], v[204:205]\n\t"
"v_pk_fma_f32 v[4:5], v[204:205], s[60:61], v[4:5]\n\t"
"v_pk_fma_f32 v[204:205], v[104:105], s[62:63], v[208:209]\n\t"
"v_pk_fma_f32 v[104:105], v[208:209], s[62:63], v[104:105]\n\t"
"v_cvt_pk_bf16_f32 v204, v204, v205\n\t"
"v_mfma_f32_16x16x32_bf16 a[12:15], a[232:235], a[184:187], a[12:15]\n\t"
"v_cvt_pk_bf16_f32 v208, v98, v99\n\t"
"v_mfma_f32_16x16x32_bf16 a[12:15], a[236:239], a[188:191], a[12:15]\n\t"
"v_mfma_f32_16x16x32_bf16 a[12:15], a[216:219], a[192:195], a[12:15]\n\t"
"v_pk_fma_f32 v[206:207], v[210:211], s[56:57], v[206:207]\n\t"
"v_pk_fma_f32 v[210:211], v[206:207], s[58:59], v[210:211] neg_lo:[0,0,1] neg_hi:[0,0,1]\n\t"
"v_pk_fma_f32 v[228:229], v[6:7], s[60:61], v[206:207]\n\t"
"v_pk_fma_f32 v[6:7], v[206:207], s[60:61], v[6:7]\n\t"
"v_pk_fma_f32 v[206:207], v[106:107], s[62:63], v[210:211]\n\t"
"v_pk_fma_f32 v[106:107], v[210:211], s[62:63], v[106:107]\n\t"
"v_cvt_pk_bf16_f32 v205, v206, v207\n\t"
"v_mfma_f32_16x16x32_bf16 a[20:23], a[232:235], a[188:191], a[20:23]\n\t"
"v_cvt_pk_bf16_f32 v209, v228, v229\n\t"
"v_mfma_f32_16x16x32_bf16 a[20:23], a[236:239], a[192:195], a[20:23]\n\t"
"v_accvgpr_read_b32 v224, a0\n\t"
"v_mfma_f32_16x16x32_bf16 a[20:23], a[216:219], a[196:199], a[20:23]\n\t"
"v_pk_fma_f32 v[212:213], v[216:217], s[56:57], v[212:213]\n\t"
"v_pk_fma_f32 v[216:217], v[212:213], s[58:59], v[216:217] neg_lo:[0,0,1] neg_hi:[0,0,1]\n\t"
"v_pk_fma_f32 v[234:235], v[8:9], s[60:61], v[212:213]\n\t"
"v_pk_fma_f32 v[8:9], v[212:213], s[60:61], v[8:9]\n\t"
"v_pk_fma_f32 v[212:213], v[108:109], s[62:63], v[216:217]\n\t"
"v_pk_fma_f32 v[108:109], v[216:217], s[62:63], v[108:109]\n\t"
"v_cvt_pk_bf16_f32 v212, v212, v213\n\t"
"v_cvt_pk_bf16_f32 v216, v234, v235\n\t"
"v_mfma_f32_16x16x32_bf16 a[28:31], a[232:235], a[192:195], a[28:31]\n\t"
"v_accvgpr_read_b32 v226, a4\n\t"
"v_mfma_f32_16x16x32_bf16 a[28:31], a[236:239], a[196:199], a[28:31]\n\t"
"v_accvgpr_read_b32 v225, a1\n\t"
"v_accvgpr_read_b32 v227, a5\n\t"
"v_mfma_f32_16x16x32_bf16 a[28:31], a[216:219], a[200:203], a[28:31]\n\t"
"v_pk_fma_f32 v[214:215], v[218:219], s[56:57], v[214:215]\n\t"
"v_pk_fma_f32 v[218:219], v[214:215], s[58:59], v[218:219] neg_lo:[0,0,1] neg_hi:[0,0,1]\n\t"
"v_pk_fma_f32 v[98:99], v[10:11], s[60:61], v[214:215]\n\t"
"v_pk_fma_f32 v[10:11], v[214:215], s[60:61], v[10:11]\n\t"
"v_pk_fma_f32 v[214:215], v[110:111], s[62:63], v[218:219]\n\t"
"v_pk_fma_f32 v[110:111], v[218:219], s[62:63], v[110:111]\n\t"
"v_cvt_pk_bf16_f32 v213, v214, v215\n\t"
"v_mfma_f32_16x16x32_bf16 a[36:39], a[232:235], a[196:199], a[36:39]\n\t"
"v_cvt_pk_bf16_f32 v217, v98, v99\n\t"
"v_accvgpr_read_b32 v230, a2\n\t"
"v_mfma_f32_16x16x32_bf16 a[36:39], a[236:239], a[200:203], a[36:39]\n\t"
"v_accvgpr_read_b32 v232, a6\n\t"
"v_accvgpr_read_b32 v231, a3\n\t"
"v_mfma_f32_16x16x32_bf16 a[36:39], a[216:219], a[204:207], a[36:39]\n\t"
"v_pk_fma_f32 v[224:225], v[226:227], s[56:57], v[224:225]\n\t"
"v_pk_fma_f32 v[226:227], v[224:225], s[58:59], v[226:227] neg_lo:[0,0,1] neg_hi:[0,0,1]\n\t"
"v_pk_fma_f32 v[228:229], v[12:13], s[60:61], v[224:225]\n\t"
"v_pk_fma_f32 v[12:13], v[224:225], s[60:61], v[12:13]\n\t"
"v_pk_fma_f32 v[224:225], v[112:113], s[62:63], v[226:227]\n\t"
"v_pk_fma_f32 v[112:113], v[226:227], s[62:63], v[112:113]\n\t"
"buffer_load_dwordx4 a[232:235], v252, s[8:11], s20 offen offset:2048 sc1\n\t"
"s_waitcnt vmcnt(9)\n\t"
"v_mfma_f32_16x16x32_bf16 a[44:47], a[240:243], a[168:171], a[44:47]\n\t"
"v_accvgpr_read_b32 v233, a7\n\t"
"v_cvt_pk_bf16_f32 v224, v224, v225\n\t"
"v_mfma_f32_16x16x32_bf16 a[44:47], a[244:247], a[172:175], a[44:47]\n\t"
"v_cvt_pk_bf16_f32 v228, v228, v229\n\t"
"v_accvgpr_write_b32 a0, v224\n\t"
"v_mfma_f32_16x16x32_bf16 a[44:47], a[220:223], a[176:179], a[44:47]\n\t"
"v_pk_fma_f32 v[230:231], v[232:233], s[56:57], v[230:231]\n\t"
"v_pk_fma_f32 v[232:233], v[230:231], s[58:59], v[232:233] neg_lo:[0,0,1] neg_hi:[0,0,1]\n\t"
"v_pk_fma_f32 v[234:235], v[14:15], s[60:61], v[230:231]\n\t"
"v_pk_fma_f32 v[14:15], v[230:231], s[60:61], v[14:15]\n\t"
"v_pk_fma_f32 v[230:231], v[114:115], s[62:63], v[232:233]\n\t"
"v_pk_fma_f32 v[114:115], v[232:233], s[62:63], v[114:115]\n\t"
"s_mov_b32 s20, 466944\n\t"
"buffer_load_dwordx4 a[216:219], v252, s[8:11], s20 offen offset:0 sc1\n\t"
"v_accvgpr_write_b32 a4, v228\n\t"
"v_accvgpr_read_b32 v236, a8\n\t"
"v_mfma_f32_16x16x32_bf16 a[52:55], a[240:243], a[172:175], a[52:55]\n\t"
"v_accvgpr_read_b32 v238, a12\n\t"
"v_accvgpr_read_b32 v237, a9\n\t"
"v_mfma_f32_16x16x32_bf16 a[52:55], a[244:247], a[176:179], a[52:55]\n\t"
"v_accvgpr_read_b32 v239, a13\n\t"
"v_cvt_pk_bf16_f32 v230, v230, v231\n\t"
"v_mfma_f32_16x16x32_bf16 a[52:55], a[220:223], a[180:183], a[52:55]\n\t"
"v_cvt_pk_bf16_f32 v234, v234, v235\n\t"
"v_pk_fma_f32 v[236:237], v[238:239], s[56:57], v[236:237]\n\t"
"v_pk_fma_f32 v[238:239], v[236:237], s[58:59], v[238:239] neg_lo:[0,0,1] neg_hi:[0,0,1]\n\t"
"v_pk_fma_f32 v[98:99], v[16:17], s[60:61], v[236:237]\n\t"
"v_pk_fma_f32 v[16:17], v[236:237], s[60:61], v[16:17]\n\t"
"v_pk_fma_f32 v[236:237], v[116:117], s[62:63], v[238:239]\n\t"
"v_pk_fma_f32 v[116:117], v[238:239], s[62:63], v[116:117]\n\t"
"buffer_load_dwordx4 a[236:239], v252, s[8:11], s20 offen offset:1024 sc1\n\t"
"v_accvgpr_write_b32 a1, v230\n\t"
"v_mfma_f32_16x16x32_bf16 a[60:63], a[240:243], a[176:179], a[60:63]\n\t"
"v_accvgpr_write_b32 a5, v234\n\t"
"v_accvgpr_read_b32 v224, a10\n\t"
"v_mfma_f32_16x16x32_bf16 a[60:63], a[244:247], a[180:183], a[60:63]\n\t"
"v_accvgpr_read_b32 v226, a14\n\t"
"v_accvgpr_read_b32 v225, a11\n\t"
"v_mfma_f32_16x16x32_bf16 a[60:63], a[220:223], a[184:187], a[60:63]\n\t"
"v_accvgpr_read_b32 v227, a15\n\t"
"v_accvgpr_read_b32 v230, a16\n\t"
"v_accvgpr_read_b32 v232, a20\n\t"
"v_accvgpr_read_b32 v231, a17\n\t"
"v_accvgpr_read_b32 v233, a21\n\t"
"v_cvt_pk_bf16_f32 v236, v236, v237\n\t"
"v_cvt_pk_bf16_f32 v98, v98, v99\n\t"
"v_pk_fma_f32 v[224:225], v[226:227], s[56:57], v[224:225]\n\t"
"v_pk_fma_f32 v[226:227], v[224:225], s[58:59], v[226:227] neg_lo:[0,0,1] neg_hi:[0,0,1]\n\t"
"v_pk_fma_f32 v[228:229], v[18:19], s[60:61], v[224:225]\n\t"
"v_pk_fma_f32 v[18:19], v[224:225], s[60:61], v[18:19]\n\t"
"v_pk_fma_f32 v[224:225], v[118:119], s[62:63], v[226:227]\n\t"
"v_pk_fma_f32 v[118:119], v[226:227], s[62:63], v[118:119]\n\t"
"v_pk_fma_f32 v[230:231], v[232:233], s[56:57], v[230:231]\n\t"
"v_pk_fma_f32 v[232:233], v[230:231], s[58:59], v[232:233] neg_lo:[0,0,1] neg_hi:[0,0,1]\n\t"
"v_pk_fma_f32 v[234:235], v[20:21], s[60:61], v[230:231]\n\t"
"v_pk_fma_f32 v[20:21], v[230:231], s[60:61], v[20:21]\n\t"
"v_pk_fma_f32 v[230:231], v[120:121], s[62:63], v[232:233]\n\t"
"v_pk_fma_f32 v[120:121], v[232:233], s[62:63], v[120:121]\n\t"
"v_mfma_f32_16x16x32_bf16 a[68:71], a[240:243], a[180:183], a[68:71]\n\t"
"v_accvgpr_write_b32 a8, v236\n\t"
"v_accvgpr_write_b32 a12, v98\n\t"
"v_mfma_f32_16x16x32_bf16 a[68:71], a[244:247], a[184:187], a[68:71]\n\t"
"v_accvgpr_read_b32 v236, a18\n\t"
"v_accvgpr_read_b32 v238, a22\n\t"
"v_mfma_f32_16x16x32_bf16 a[68:71], a[220:223], a[188:191], a[68:71]\n\t"
"v_accvgpr_read_b32 v237, a19\n\t"
"v_accvgpr_read_b32 v239, a23\n\t"
"v_cvt_pk_bf16_f32 v224, v224, v225\n\t"
"v_cvt_pk_bf16_f32 v228, v228, v229\n\t"
"v_cvt_pk_bf16_f32 v230, v230, v231\n\t"
"v_cvt_pk_bf16_f32 v234, v234, v235\n\t"
"v_pk_fma_f32 v[236:237], v[238:239], s[56:57], v[236:237]\n\t"
"v_pk_fma_f32 v[238:239], v[236:237], s[58:59], v[238:239] neg_lo:[0,0,1] neg_hi:[0,0,1]\n\t"
"v_pk_fma_f32 v[98:99], v[22:23], s[60:61], v[236:237]\n\t"
"v_pk_fma_f32 v[22:23], v[236:237], s[60:61], v[22:23]\n\t"
"v_pk_fma_f32 v[236:237], v[122:123], s[62:63], v[238:239]\n\t"
"v_pk_fma_f32 v[122:123], v[238:239], s[62:63], v[122:123]\n\t"
"v_mfma_f32_16x16x32_bf16 a[76:79], a[240:243], a[184:187], a[76:79]\n\t"
"v_accvgpr_write_b32 a9, v224\n\t"
"v_accvgpr_write_b32 a13, v228\n\t"
"v_mfma_f32_16x16x32_bf16 a[76:79], a[244:247], a[188:191], a[76:79]\n\t"
"v_accvgpr_write_b32 a16, v230\n\t"
"v_accvgpr_write_b32 a20, v234\n\t"
"v_mfma_f32_16x16x32_bf16 a[76:79], a[220:223], a[192:195], a[76:79]\n\t"
"v_accvgpr_read_b32 v224, a24\n\t"
"v_accvgpr_read_b32 v226, a28\n\t"
"v_accvgpr_read_b32 v225, a25\n\t"
"v_accvgpr_read_b32 v227, a29\n\t"
"v_cvt_pk_bf16_f32 v236, v236, v237\n\t"
"v_cvt_pk_bf16_f32 v98, v98, v99\n\t"
"v_pk_fma_f32 v[224:225], v[226:227], s[56:57], v[224:225]\n\t"
"v_pk_fma_f32 v[226:227], v[224:225], s[58:59], v[226:227] neg_lo:[0,0,1] neg_hi:[0,0,1]\n\t"
"v_pk_fma_f32 v[228:229], v[24:25], s[60:61], v[224:225]\n\t"
"v_pk_fma_f32 v[24:25], v[224:225], s[60:61], v[24:25]\n\t"
"v_pk_fma_f32 v[224:225], v[124:125], s[62:63], v[226:227]\n\t"
"v_pk_fma_f32 v[124:125], v[226:227], s[62:63], v[124:125]\n\t"
"v_mfma_f32_16x16x32_bf16 a[84:87], a[240:243], a[188:191], a[84:87]\n\t"
"v_accvgpr_write_b32 a17, v236\n\t"
"v_accvgpr_write_b32 a21, v98\n\t"
"v_mfma_f32_16x16x32_bf16 a[84:87], a[244:247], a[192:195], a[84:87]\n\t"
"v_accvgpr_read_b32 v230, a26\n\t"
"v_accvgpr_read_b32 v232, a30\n\t"
"v_mfma_f32_16x16x32_bf16 a[84:87], a[220:223], a[196:199], a[84:87]\n\t"
"v_accvgpr_read_b32 v231, a27\n\t"
"v_accvgpr_read_b32 v233, a31\n\t"
"v_cvt_pk_bf16_f32 v224, v224, v225\n\t"
"v_cvt_pk_bf16_f32 v228, v228, v229\n\t"
"v_pk_fma_f32 v[230:231], v[232:233], s[56:57], v[230:231]\n\t"
"v_pk_fma_f32 v[232:233], v[230:231], s[58:59], v[232:233] neg_lo:[0,0,1] neg_hi:[0,0,1]\n\t"
"v_pk_fma_f32 v[234:235], v[26:27], s[60:61], v[230:231]\n\t"
"v_pk_fma_f32 v[26:27], v[230:231], s[60:61], v[26:27]\n\t"
"v_pk_fma_f32 v[230:231], v[126:127], s[62:63], v[232:233]\n\t"
"v_pk_fma_f32 v[126:127], v[232:233], s[62:63], v[126:127]\n\t"
"v_mfma_f32_16x16x32_bf16 a[92:95], a[240:243], a[192:195], a[92:95]\n\t"
"v_accvgpr_write_b32 a24, v224\n\t"
"v_accvgpr_write_b32 a28, v228\n\t"
"v_mfma_f32_16x16x32_bf16 a[92:95], a[244:247], a[196:199], a[92:95]\n\t"
"v_accvgpr_read_b32 v236, a32\n\t"
"v_accvgpr_read_b32 v238, a36\n\t"
"v_mfma_f32_16x16x32_bf16 a[92:95], a[220:223], a[200:203], a[92:95]\n\t"
"v_accvgpr_read_b32 v237, a33\n\t"
"v_accvgpr_read_b32 v239, a37\n\t"
"v_cvt_pk_bf16_f32 v230, v230, v231\n\t"
"v_cvt_pk_bf16_f32 v234, v234, v235\n\t"
"v_pk_fma_f32 v[236:237], v[238:239], s[56:57], v[236:237]\n\t"
"v_pk_fma_f32 v[238:239], v[236:237], s[58:59], v[238:239] neg_lo:[0,0,1] neg_hi:[0,0,1]\n\t"
"v_pk_fma_f32 v[98:99], v[28:29], s[60:61], v[236:237]\n\t"
"v_pk_fma_f32 v[28:29], v[236:237], s[60:61], v[28:29]\n\t"
"v_pk_fma_f32 v[236:237], v[128:129], s[62:63], v[238:239]\n\t"
"v_pk_fma_f32 v[128:129], v[238:239], s[62:63], v[128:129]\n\t"
"v_mfma_f32_16x16x32_bf16 a[100:103], a[240:243], a[196:199], a[100:103]\n\t"
"v_accvgpr_write_b32 a25, v230\n\t"
"v_accvgpr_write_b32 a29, v234\n\t"
"v_mfma_f32_16x16x32_bf16 a[100:103], a[244:247], a[200:203], a[100:103]\n\t"
"v_accvgpr_read_b32 v224, a34\n\t"
"v_accvgpr_read_b32 v226, a38\n\t"
"v_mfma_f32_16x16x32_bf16 a[100:103], a[220:223], a[204:207], a[100:103]\n\t"
"v_accvgpr_read_b32 v225, a35\n\t"
"v_accvgpr_read_b32 v227, a39\n\t"
"v_cvt_pk_bf16_f32 v236, v236, v237\n\t"
"v_cvt_pk_bf16_f32 v98, v98, v99\n\t"
"v_pk_fma_f32 v[224:225], v[226:227], s[56:57], v[224:225]\n\t"
"v_pk_fma_f32 v[226:227], v[224:225], s[58:59], v[226:227] neg_lo:[0,0,1] neg_hi:[0,0,1]\n\t"
"v_pk_fma_f32 v[228:229], v[30:31], s[60:61], v[224:225]\n\t"
"v_pk_fma_f32 v[30:31], v[224:225], s[60:61], v[30:31]\n\t"
"v_pk_fma_f32 v[224:225], v[130:131], s[62:63], v[226:227]\n\t"
"v_pk_fma_f32 v[130:131], v[226:227], s[62:63], v[130:131]\n\t"
"s_waitcnt vmcnt(8)\n\t"
"v_mfma_f32_16x16x32_bf16 a[108:111], a[248:251], a[168:171], a[108:111]\n\t"
"v_accvgpr_write_b32 a32, v236\n\t"
"v_accvgpr_write_b32 a36, v98\n\t"
"v_mfma_f32_16x16x32_bf16 a[108:111], v[240:243], a[172:175], a[108:111]\n\t"
"v_accvgpr_read_b32 v230, a40\n\t"
"v_accvgpr_read_b32 v232, a44\n\t"
"v_mfma_f32_16x16x32_bf16 a[108:111], a[252:255], a[176:179], a[108:111]\n\t"
"buffer_load_dwordx4 a[240:243], v252, s[8:11], s20 offen offset:2048 sc1\n\t"
"v_accvgpr_read_b32 v231, a41\n\t"
"v_accvgpr_read_b32 v233, a45\n\t"
"v_cvt_pk_bf16_f32 v224, v224, v225\n\t"
"v_cvt_pk_bf16_f32 v228, v228, v229\n\t"
"v_pk_fma_f32 v[230:231], v[232:233], s[56:57], v[230:231]\n\t"
"v_pk_fma_f32 v[232:233], v[230:231], s[58:59], v[232:233] neg_lo:[0,0,1] neg_hi:[0,0,1]\n\t"
"v_pk_fma_f32 v[234:235], v[32:33], s[60:61], v[230:231]\n\t"
"v_pk_fma_f32 v[32:33], v[230:231], s[60:61], v[32:33]\n\t"
"v_pk_fma_f32 v[230:231], v[132:133], s[62:63], v[232:233]\n\t"
"v_pk_fma_f32 v[132:133], v[232:233], s[62:63], v[132:133]\n\t"
"v_mfma_f32_16x16x32_bf16 a[116:119], a[248:251], a[172:175], a[116:119]\n\t"
"v_accvgpr_write_b32 a33, v224\n\t"
"v_accvgpr_write_b32 a37, v228\n\t"
"v_mfma_f32_16x16x32_bf16 a[116:119], v[240:243], a[176:179], a[116:119]\n\t"
"v_accvgpr_read_b32 v236, a42\n\t"
"v_accvgpr_read_b32 v238, a46\n\t"
"v_mfma_f32_16x16x32_bf16 a[116:119], a[252:255], a[180:183], a[116:119]\n\t"
"s_mov_b32 s20, 626688\n\t"
"buffer_load_dwordx4 a[220:223], v252, s[8:11], s20 offen offset:0 sc1\n\t"
"v_accvgpr_read_b32 v237, a43\n\t"
"v_accvgpr_read_b32 v239, a47\n\t"
"v_pk_fma_f32 v[236:237], v[238:239], s[56:57], v[236:237]\n\t"
"v_pk_fma_f32 v[238:239], v[236:237], s[58:59], v[238:239] neg_lo:[0,0,1] neg_hi:[0,0,1]\n\t"
"v_pk_fma_f32 v[98:99], v[34:35], s[60:61], v[236:237]\n\t"
"v_pk_fma_f32 v[34:35], v[236:237], s[60:61], v[34:35]\n\t"
"v_pk_fma_f32 v[236:237], v[134:135], s[62:63], v[238:239]\n\t"
"v_pk_fma_f32 v[134:135], v[238:239], s[62:63], v[134:135]\n\t"
"v_mfma_f32_16x16x32_bf16 a[124:127], a[248:251], a[176:179], a[124:127]\n\t"
"v_cvt_pk_bf16_f32 v198, v230, v231\n\t"
"v_cvt_pk_bf16_f32 v202, v234, v235\n\t"
"v_mfma_f32_16x16x32_bf16 a[124:127], v[240:243], a[180:183], a[124:127]\n\t"
"v_accvgpr_read_b32 v224, a48\n\t"
"v_accvgpr_read_b32 v226, a52\n\t"
"v_mfma_f32_16x16x32_bf16 a[124:127], a[252:255], a[184:187], a[124:127]\n\t"
"buffer_load_dwordx4 a[244:247], v252, s[8:11], s20 offen offset:1024 sc1\n\t"
"v_accvgpr_read_b32 v225, a49\n\t"
"v_accvgpr_read_b32 v227, a53\n\t"
"v_accvgpr_read_b32 v230, a50\n\t"
"v_accvgpr_read_b32 v232, a54\n\t"
"v_accvgpr_read_b32 v231, a51\n\t"
"v_accvgpr_read_b32 v233, a55\n\t"
"v_pk_fma_f32 v[224:225], v[226:227], s[56:57], v[224:225]\n\t"
"v_pk_fma_f32 v[226:227], v[224:225], s[58:59], v[226:227] neg_lo:[0,0,1] neg_hi:[0,0,1]\n\t"
"v_pk_fma_f32 v[228:229], v[36:37], s[60:61], v[224:225]\n\t"
"v_pk_fma_f32 v[36:37], v[224:225], s[60:61], v[36:37]\n\t"
"v_pk_fma_f32 v[224:225], v[136:137], s[62:63], v[226:227]\n\t"
"v_pk_fma_f32 v[136:137], v[226:227], s[62:63], v[136:137]\n\t"
"v_pk_fma_f32 v[230:231], v[232:233], s[56:57], v[230:231]\n\t"
"v_pk_fma_f32 v[232:233], v[230:231], s[58:59], v[232:233] neg_lo:[0,0,1] neg_hi:[0,0,1]\n\t"
"v_pk_fma_f32 v[234:235], v[38:39], s[60:61], v[230:231]\n\t"
"v_pk_fma_f32 v[38:39], v[230:231], s[60:61], v[38:39]\n\t"
"v_pk_fma_f32 v[230:231], v[138:139], s[62:63], v[232:233]\n\t"
"v_pk_fma_f32 v[138:139], v[232:233], s[62:63], v[138:139]\n\t"
"v_mfma_f32_16x16x32_bf16 a[132:135], a[248:251], a[180:183], a[132:135]\n\t"
"v_cvt_pk_bf16_f32 v199, v236, v237\n\t"
"v_cvt_pk_bf16_f32 v203, v98, v99\n\t"
"v_mfma_f32_16x16x32_bf16 a[132:135], v[240:243], a[184:187], a[132:135]\n\t"
"v_accvgpr_read_b32 v236, a56\n\t"
"v_accvgpr_read_b32 v238, a60\n\t"
"v_mfma_f32_16x16x32_bf16 a[132:135], a[252:255], a[188:191], a[132:135]\n\t"
"v_accvgpr_read_b32 v237, a57\n\t"
"v_accvgpr_read_b32 v239, a61\n\t"
"ds_read_b128 a[168:171], v244 offset:18432\n\t"
"ds_read_b128 a[172:175], v244 offset:18944\n\t"
"ds_read_b128 a[176:179], v244 offset:19456\n\t"
"buffer_load_dwordx4 a[40:43], v252, s[8:11], s20 offen offset:2048 sc1\n\t"
"v_pk_fma_f32 v[236:237], v[238:239], s[56:57], v[236:237]\n\t"
"v_pk_fma_f32 v[238:239], v[236:237], s[58:59], v[238:239] neg_lo:[0,0,1] neg_hi:[0,0,1]\n\t"
"v_pk_fma_f32 v[98:99], v[40:41], s[60:61], v[236:237]\n\t"
"v_pk_fma_f32 v[40:41], v[236:237], s[60:61], v[40:41]\n\t"
"v_pk_fma_f32 v[236:237], v[140:141], s[62:63], v[238:239]\n\t"
"v_pk_fma_f32 v[140:141], v[238:239], s[62:63], v[140:141]\n\t"
"v_mfma_f32_16x16x32_bf16 a[140:143], a[248:251], a[184:187], a[140:143]\n\t"
"v_cvt_pk_bf16_f32 v206, v224, v225\n\t"
"v_cvt_pk_bf16_f32 v210, v228, v229\n\t"
"v_mfma_f32_16x16x32_bf16 a[140:143], v[240:243], a[188:191], a[140:143]\n\t"
"v_cvt_pk_bf16_f32 v207, v230, v231\n\t"
"v_cvt_pk_bf16_f32 v211, v234, v235\n\t"
"v_mfma_f32_16x16x32_bf16 a[140:143], a[252:255], a[192:195], a[140:143]\n\t"
"v_accvgpr_read_b32 v224, a58\n\t"
"v_accvgpr_read_b32 v226, a62\n\t"
"v_accvgpr_read_b32 v225, a59\n\t"
"v_accvgpr_read_b32 v227, a63\n\t"
"ds_read_b128 a[180:183], v244 offset:19968\n\t"
"s_mov_b32 s20, 638976\n\t"
"buffer_load_dwordx4 a[48:51], v252, s[8:11], s20 offen offset:0 sc1\n\t"
"v_pk_fma_f32 v[224:225], v[226:227], s[56:57], v[224:225]\n\t"
"v_pk_fma_f32 v[226:227], v[224:225], s[58:59], v[226:227] neg_lo:[0,0,1] neg_hi:[0,0,1]\n\t"
"v_pk_fma_f32 v[228:229], v[42:43], s[60:61], v[224:225]\n\t"
"v_pk_fma_f32 v[42:43], v[224:225], s[60:61], v[42:43]\n\t"
"v_pk_fma_f32 v[224:225], v[142:143], s[62:63], v[226:227]\n\t"
"v_pk_fma_f32 v[142:143], v[226:227], s[62:63], v[142:143]\n\t"
"v_mfma_f32_16x16x32_bf16 a[148:151], a[248:251], a[188:191], a[148:151]\n\t"
"v_cvt_pk_bf16_f32 v214, v236, v237\n\t"
"v_cvt_pk_bf16_f32 v218, v98, v99\n\t"
"v_mfma_f32_16x16x32_bf16 a[148:151], v[240:243], a[192:195], a[148:151]\n\t"
"v_accvgpr_read_b32 v230, a64\n\t"
"v_accvgpr_read_b32 v232, a68\n\t"
"v_mfma_f32_16x16x32_bf16 a[148:151], a[252:255], a[196:199], a[148:151]\n\t"
"v_accvgpr_read_b32 v231, a65\n\t"
"v_accvgpr_read_b32 v233, a69\n\t"
"ds_read_b128 a[184:187], v244 offset:20480\n\t"
"buffer_load_dwordx4 a[44:47], v252, s[8:11], s20 offen offset:1024 sc1\n\t"
"v_pk_fma_f32 v[230:231], v[232:233], s[56:57], v[230:231]\n\t"
"v_pk_fma_f32 v[232:233], v[230:231], s[58:59], v[232:233] neg_lo:[0,0,1] neg_hi:[0,0,1]\n\t"
"v_pk_fma_f32 v[234:235], v[44:45], s[60:61], v[230:231]\n\t"
"v_pk_fma_f32 v[44:45], v[230:231], s[60:61], v[44:45]\n\t"
"v_pk_fma_f32 v[230:231], v[144:145], s[62:63], v[232:233]\n\t"
"v_pk_fma_f32 v[144:145], v[232:233], s[62:63], v[144:145]\n\t"
"v_mfma_f32_16x16x32_bf16 a[156:159], a[248:251], a[192:195], a[156:159]\n\t"
"v_cvt_pk_bf16_f32 v215, v224, v225\n\t"
"v_cvt_pk_bf16_f32 v219, v228, v229\n\t"
"v_mfma_f32_16x16x32_bf16 a[156:159], v[240:243], a[196:199], a[156:159]\n\t"
"v_accvgpr_read_b32 v236, a66\n\t"
"v_accvgpr_read_b32 v238, a70\n\t"
"v_mfma_f32_16x16x32_bf16 a[156:159], a[252:255], a[200:203], a[156:159]\n\t"
"v_accvgpr_read_b32 v237, a67\n\t"
"v_accvgpr_read_b32 v239, a71\n\t"
"ds_read_b128 a[188:191], v244 offset:20992\n\t"
"buffer_load_dwordx4 a[56:59], v252, s[8:11], s20 offen offset:2048 sc1\n\t"
"v_cvt_pk_bf16_f32 v230, v230, v231\n\t"
"v_cvt_pk_bf16_f32 v234, v234, v235\n\t"
"v_pk_fma_f32 v[236:237], v[238:239], s[56:57], v[236:237]\n\t"
"v_pk_fma_f32 v[238:239], v[236:237], s[58:59], v[238:239] neg_lo:[0,0,1] neg_hi:[0,0,1]\n\t"
"v_pk_fma_f32 v[98:99], v[46:47], s[60:61], v[236:237]\n\t"
"v_pk_fma_f32 v[46:47], v[236:237], s[60:61], v[46:47]\n\t"
"v_pk_fma_f32 v[236:237], v[146:147], s[62:63], v[238:239]\n\t"
"v_pk_fma_f32 v[146:147], v[238:239], s[62:63], v[146:147]\n\t"
"v_mfma_f32_16x16x32_bf16 a[164:167], a[248:251], a[196:199], a[164:167]\n\t"
"v_accvgpr_write_b32 a2, v230\n\t"
"v_accvgpr_write_b32 a6, v234\n\t"
"v_mfma_f32_16x16x32_bf16 a[164:167], v[240:243], a[200:203], a[164:167]\n\t"
"v_accvgpr_read_b32 v224, a72\n\t"
"v_accvgpr_read_b32 v226, a76\n\t"
"v_mfma_f32_16x16x32_bf16 a[164:167], a[252:255], a[204:207], a[164:167]\n\t"
"v_accvgpr_read_b32 v225, a73\n\t"
"v_accvgpr_read_b32 v227, a77\n\t"
"ds_read_b128 a[192:195], v244 offset:21504\n\t"
"s_mov_b32 s20, 651264\n\t"
"buffer_load_dwordx4 a[60:63], v252, s[8:11], s20 offen offset:0 sc1\n\t"
"v_cvt_pk_bf16_f32 v236, v236, v237\n\t"
"v_cvt_pk_bf16_f32 v98, v98, v99\n\t"
"v_pk_fma_f32 v[224:225], v[226:227], s[56:57], v[224:225]\n\t"
"v_pk_fma_f32 v[226:227], v[224:225], s[58:59], v[226:227] neg_lo:[0,0,1] neg_hi:[0,0,1]\n\t"
"v_pk_fma_f32 v[228:229], v[48:49], s[60:61], v[224:225]\n\t"
"v_pk_fma_f32 v[48:49], v[224:225], s[60:61], v[48:49]\n\t"
"v_pk_fma_f32 v[224:225], v[148:149], s[62:63], v[226:227]\n\t"
"v_pk_fma_f32 v[148:149], v[226:227], s[62:63], v[148:149]\n\t"
"Lst14_%=:\n\t"
"s_waitcnt vmcnt(13) lgkmcnt(4)\n\t"
"v_mfma_f32_16x16x32_bf16 v[0:3], a[224:227], a[168:171], v[0:3]\n\t"
"v_accvgpr_write_b32 a3, v236\n\t"
"v_accvgpr_write_b32 a7, v98\n\t"
"v_mfma_f32_16x16x32_bf16 v[0:3], v[248:251], a[172:175], v[0:3]\n\t"
"v_accvgpr_read_b32 v230, a74\n\t"
"v_accvgpr_read_b32 v232, a78\n\t"
"v_mfma_f32_16x16x32_bf16 v[0:3], a[208:211], a[176:179], v[0:3]\n\t"
"v_accvgpr_read_b32 v231, a75\n\t"
"v_accvgpr_read_b32 v233, a79\n\t"
"ds_read_b128 a[196:199], v244 offset:22016\n\t"
"buffer_load_dwordx4 a[64:67], v252, s[8:11], s20 offen offset:1024 sc1\n\t"
"v_cvt_pk_bf16_f32 v224, v224, v225\n\t"
"v_cvt_pk_bf16_f32 v228, v228, v229\n\t"
"v_pk_fma_f32 v[230:231], v[232:233], s[56:57], v[230:231]\n\t"
"v_pk_fma_f32 v[232:233], v[230:231], s[58:59], v[232:233] neg_lo:[0,0,1] neg_hi:[0,0,1]\n\t"
"v_pk_fma_f32 v[234:235], v[50:51], s[60:61], v[230:231]\n\t"
"v_pk_fma_f32 v[50:51], v[230:231], s[60:61], v[50:51]\n\t"
"v_pk_fma_f32 v[230:231], v[150:151], s[62:63], v[232:233]\n\t"
"v_pk_fma_f32 v[150:151], v[232:233], s[62:63], v[150:151]\n\t"
"s_waitcnt lgkmcnt(4)\n\t"
"v_mfma_f32_16x16x32_bf16 v[4:7], a[224:227], a[172:175], v[4:7]\n\t"
"v_accvgpr_write_b32 a10, v224\n\t"
"v_accvgpr_write_b32 a14, v228\n\t"
"v_mfma_f32_16x16x32_bf16 v[4:7], v[248:251], a[176:179], v[4:7]\n\t"
"v_accvgpr_read_b32 v236, a80\n\t"
"v_accvgpr_read_b32 v238, a84\n\t"
"v_mfma_f32_16x16x32_bf16 v[4:7], a[208:211], a[180:183], v[4:7]\n\t"
"v_accvgpr_read_b32 v237, a81\n\t"
"v_accvgpr_read_b32 v239, a85\n\t"
"ds_read_b128 a[200:203], v244 offset:22528\n\t"
"buffer_load_dwordx4 a[52:55], v252, s[8:11], s20 offen offset:2048 sc1\n\t"
"v_cvt_pk_bf16_f32 v230, v230, v231\n\t"
"v_cvt_pk_bf16_f32 v234, v234, v235\n\t"
"v_pk_fma_f32 v[236:237], v[238:239], s[56:57], v[236:237]\n\t"
"v_pk_fma_f32 v[238:239], v[236:237], s[58:59], v[238:239] neg_lo:[0,0,1] neg_hi:[0,0,1]\n\t"
"v_pk_fma_f32 v[98:99], v[52:53], s[60:61], v[236:237]\n\t"
"v_pk_fma_f32 v[52:53], v[236:237], s[60:61], v[52:53]\n\t"
"v_pk_fma_f32 v[236:237], v[152:153], s[62:63], v[238:239]\n\t"
"v_pk_fma_f32 v[152:153], v[238:239], s[62:63], v[152:153]\n\t"
"s_waitcnt lgkmcnt(4)\n\t"
"v_mfma_f32_16x16x32_bf16 v[8:11], a[224:227], a[176:179], v[8:11]\n\t"
"v_accvgpr_write_b32 a11, v230\n\t"
"v_accvgpr_write_b32 a15, v234\n\t"
"v_mfma_f32_16x16x32_bf16 v[8:11], v[248:251], a[180:183], v[8:11]\n\t"
"v_accvgpr_read_b32 v224, a82\n\t"
"v_accvgpr_read_b32 v226, a86\n\t"
"v_mfma_f32_16x16x32_bf16 v[8:11], a[208:211], a[184:187], v[8:11]\n\t"
"v_accvgpr_read_b32 v225, a83\n\t"
"v_accvgpr_read_b32 v227, a87\n\t"
"ds_read_b128 a[204:207], v244 offset:23040\n\t"
"s_mov_b32 s20, 663552\n\t"
"buffer_load_dwordx4 a[72:75], v252, s[8:11], s20 offen offset:0 sc1\n\t"
"v_cvt_pk_bf16_f32 v236, v236, v237\n\t"
"v_cvt_pk_bf16_f32 v98, v98, v99\n\t"
"v_pk_fma_f32 v[224:225], v[226:227], s[56:57], v[224:225]\n\t"
"v_pk_fma_f32 v[226:227], v[224:225], s[58:59], v[226:227] neg_lo:[0,0,1] neg_hi:[0,0,1]\n\t"
"v_pk_fma_f32 v[228:229], v[54:55], s[60:61], v[224:225]\n\t"
"v_pk_fma_f32 v[54:55], v[224:225], s[60:61], v[54:55]\n\t"
"v_pk_fma_f32 v[224:225], v[154:155], s[62:63], v[226:227]\n\t"
"v_pk_fma_f32 v[154:155], v[226:227], s[62:63], v[154:155]\n\t"
"s_waitcnt lgkmcnt(4)\n\t"
"v_mfma_f32_16x16x32_bf16 v[12:15], a[224:227], a[180:183], v[12:15]\n\t"
"v_accvgpr_write_b32 a18, v236\n\t"
"v_accvgpr_write_b32 a22, v98\n\t"
"v_mfma_f32_16x16x32_bf16 v[12:15], v[248:251], a[184:187], v[12:15]\n\t"
"v_accvgpr_read_b32 v230, a88\n\t"
"v_accvgpr_read_b32 v232, a92\n\t"
"v_mfma_f32_16x16x32_bf16 v[12:15], a[208:211], a[188:191], v[12:15]\n\t"
"v_accvgpr_read_b32 v231, a89\n\t"
"v_accvgpr_read_b32 v233, a93\n\t"
"v_accvgpr_read_b32 v236, a90\n\t"
"v_accvgpr_read_b32 v238, a94\n\t"
"v_accvgpr_read_b32 v237, a91\n\t"
"v_accvgpr_read_b32 v239, a95\n\t"
"buffer_load_dwordx4 a[68:71], v252, s[8:11], s20 offen offset:1024 sc1\n\t"
"v_cvt_pk_bf16_f32 v224, v224, v225\n\t"
"v_cvt_pk_bf16_f32 v228, v228, v229\n\t"
"v_pk_fma_f32 v[230:231], v[232:233], s[56:57], v[230:231]\n\t"
"v_pk_fma_f32 v[232:233], v[230:231], s[58:59], v[232:233] neg_lo:[0,0,1] neg_hi:[0,0,1]\n\t"
"v_pk_fma_f32 v[234:235], v[56:57], s[60:61], v[230:231]\n\t"
"v_pk_fma_f32 v[56:57], v[230:231], s[60:61], v[56:57]\n\t"
"v_pk_fma_f32 v[230:231], v[156:157], s[62:63], v[232:233]\n\t"
"v_pk_fma_f32 v[156:157], v[232:233], s[62:63], v[156:157]\n\t"
"v_pk_fma_f32 v[236:237], v[238:239], s[56:57], v[236:237]\n\t"
"v_pk_fma_f32 v[238:239], v[236:237], s[58:59], v[238:239] neg_lo:[0,0,1] neg_hi:[0,0,1]\n\t"
"v_pk_fma_f32 v[98:99], v[58:59], s[60:61], v[236:237]\n\t"
"v_pk_fma_f32 v[58:59], v[236:237], s[60:61], v[58:59]\n\t"
"v_pk_fma_f32 v[236:237], v[158:159], s[62:63], v[238:239]\n\t"
"v_pk_fma_f32 v[158:159], v[238:239], s[62:63], v[158:159]\n\t"
"s_waitcnt lgkmcnt(3)\n\t"
"v_mfma_f32_16x16x32_bf16 v[16:19], a[224:227], a[184:187], v[16:19]\n\t"
"v_accvgpr_write_b32 a19, v224\n\t"
"v_accvgpr_write_b32 a23, v228\n\t"
"v_mfma_f32_16x16x32_bf16 v[16:19], v[248:251], a[188:191], v[16:19]\n\t"
"v_accvgpr_read_b32 v224, a96\n\t"
"v_accvgpr_read_b32 v226, a100\n\t"
"v_mfma_f32_16x16x32_bf16 v[16:19], a[208:211], a[192:195], v[16:19]\n\t"
"v_accvgpr_read_b32 v225, a97\n\t"
"v_accvgpr_read_b32 v227, a101\n\t"
"buffer_load_dwordx4 a[80:83], v252, s[8:11], s20 offen offset:2048 sc1\n\t"
"v_cvt_pk_bf16_f32 v230, v230, v231\n\t"
"v_cvt_pk_bf16_f32 v234, v234, v235\n\t"
"v_pk_fma_f32 v[224:225], v[226:227], s[56:57], v[224:225]\n\t"
"v_pk_fma_f32 v[226:227], v[224:225], s[58:59], v[226:227] neg_lo:[0,0,1] neg_hi:[0,0,1]\n\t"
"v_pk_fma_f32 v[228:229], v[60:61], s[60:61], v[224:225]\n\t"
"v_pk_fma_f32 v[60:61], v[224:225], s[60:61], v[60:61]\n\t"
"v_pk_fma_f32 v[224:225], v[160:161], s[62:63], v[226:227]\n\t"
"v_pk_fma_f32 v[160:161], v[226:227], s[62:63], v[160:161]\n\t"
"s_waitcnt lgkmcnt(2)\n\t"
"v_mfma_f32_16x16x32_bf16 v[20:23], a[224:227], a[188:191], v[20:23]\n\t"
"v_accvgpr_write_b32 a26, v230\n\t"
"v_cvt_pk_bf16_f32 v236, v236, v237\n\t"
"v_mfma_f32_16x16x32_bf16 v[20:23], v[248:251], a[192:195], v[20:23]\n\t"
"v_accvgpr_write_b32 a30, v234\n\t"
"v_cvt_pk_bf16_f32 v98, v98, v99\n\t"
"v_mfma_f32_16x16x32_bf16 v[20:23], a[208:211], a[196:199], v[20:23]\n\t"
"v_accvgpr_write_b32 a27, v236\n\t"
"v_accvgpr_write_b32 a31, v98\n\t"
"v_accvgpr_read_b32 v230, a98\n\t"
"v_accvgpr_read_b32 v232, a102\n\t"
"v_accvgpr_read_b32 v231, a99\n\t"
"v_accvgpr_read_b32 v233, a103\n\t"
"s_mov_b32 s20, 675840\n\t"
"buffer_load_dwordx4 a[92:95], v252, s[8:11], s20 offen offset:0 sc1\n\t"
"v_cvt_pk_bf16_f32 v224, v224, v225\n\t"
"v_cvt_pk_bf16_f32 v228, v228, v229\n\t"
"v_pk_fma_f32 v[230:231], v[232:233], s[56:57], v[230:231]\n\t"
"v_pk_fma_f32 v[232:233], v[230:231], s[58:59], v[232:233] neg_lo:[0,0,1] neg_hi:[0,0,1]\n\t"
"v_pk_fma_f32 v[234:235], v[62:63], s[60:61], v[230:231]\n\t"
"v_pk_fma_f32 v[62:63], v[230:231], s[60:61], v[62:63]\n\t"
"v_pk_fma_f32 v[230:231], v[162:163], s[62:63], v[232:233]\n\t"
"v_pk_fma_f32 v[162:163], v[232:233], s[62:63], v[162:163]\n\t"
"s_waitcnt lgkmcnt(1)\n\t"
"v_mfma_f32_16x16x32_bf16 v[24:27], a[224:227], a[192:195], v[24:27]\n\t"
"v_accvgpr_write_b32 a34, v224\n\t"
"v_accvgpr_write_b32 a38, v228\n\t"
"v_mfma_f32_16x16x32_bf16 v[24:27], v[248:251], a[196:199], v[24:27]\n\t"
"v_accvgpr_read_b32 v236, a104\n\t"
"v_accvgpr_read_b32 v238, a108\n\t"
"v_mfma_f32_16x16x32_bf16 v[24:27], a[208:211], a[200:203], v[24:27]\n\t"
"v_accvgpr_read_b32 v237, a105\n\t"
"v_accvgpr_read_b32 v239, a109\n\t"
"buffer_load_dwordx4 a[88:91], v252, s[8:11], s20 offen offset:1024 sc1\n\t"
"v_cvt_pk_bf16_f32 v230, v230, v231\n\t"
"v_cvt_pk_bf16_f32 v234, v234, v235\n\t"
"v_pk_fma_f32 v[236:237], v[238:239], s[56:57], v[236:237]\n\t"
"v_pk_fma_f32 v[238:239], v[236:237], s[58:59], v[238:239] neg_lo:[0,0,1] neg_hi:[0,0,1]\n\t"
"v_pk_fma_f32 v[98:99], v[64:65], s[60:61], v[236:237]\n\t"
"v_pk_fma_f32 v[64:65], v[236:237], s[60:61], v[64:65]\n\t"
"v_pk_fma_f32 v[236:237], v[164:165], s[62:63], v[238:239]\n\t"
"v_pk_fma_f32 v[164:165], v[238:239], s[62:63], v[164:165]\n\t"
"s_waitcnt lgkmcnt(0)\n\t"
"v_mfma_f32_16x16x32_bf16 v[28:31], a[224:227], a[196:199], v[28:31]\n\t"
"v_accvgpr_write_b32 a35, v230\n\t"
"v_accvgpr_write_b32 a39, v234\n\t"
"v_mfma_f32_16x16x32_bf16 v[28:31], v[248:251], a[200:203], v[28:31]\n\t"
"v_accvgpr_read_b32 v224, a106\n\t"
"v_accvgpr_read_b32 v226, a110\n\t"
"v_mfma_f32_16x16x32_bf16 v[28:31], a[208:211], a[204:207], v[28:31]\n\t"
"v_accvgpr_read_b32 v225, a107\n\t"
"v_accvgpr_read_b32 v227, a111\n\t"
"buffer_load_dwordx4 a[96:99], v252, s[8:11], s20 offen offset:2048 sc1\n\t"
"v_cvt_pk_bf16_f32 v236, v236, v237\n\t"
"v_cvt_pk_bf16_f32 v98, v98, v99\n\t"
"v_pk_fma_f32 v[224:225], v[226:227], s[56:57], v[224:225]\n\t"
"v_pk_fma_f32 v[226:227], v[224:225], s[58:59], v[226:227] neg_lo:[0,0,1] neg_hi:[0,0,1]\n\t"
"v_pk_fma_f32 v[228:229], v[66:67], s[60:61], v[224:225]\n\t"
"v_pk_fma_f32 v[66:67], v[224:225], s[60:61], v[66:67]\n\t"
"v_pk_fma_f32 v[224:225], v[166:167], s[62:63], v[226:227]\n\t"
"v_pk_fma_f32 v[166:167], v[226:227], s[62:63], v[166:167]\n\t"
"s_waitcnt vmcnt(18)\n\t"
"v_mfma_f32_16x16x32_bf16 v[32:35], a[212:215], a[168:171], v[32:35]\n\t"
"v_accvgpr_write_b32 a104, v236\n\t"
"v_accvgpr_write_b32 a106, v98\n\t"
"v_mfma_f32_16x16x32_bf16 v[32:35], a[228:231], a[172:175], v[32:35]\n\t"
"v_accvgpr_read_b32 v230, a112\n\t"
"v_accvgpr_read_b32 v232, a116\n\t"
"v_mfma_f32_16x16x32_bf16 v[32:35], a[232:235], a[176:179], v[32:35]\n\t"
"v_accvgpr_read_b32 v231, a113\n\t"
"v_accvgpr_read_b32 v233, a117\n\t"
"s_mov_b32 s20, 688128\n\t"
"buffer_load_dwordx4 a[76:79], v252, s[8:11], s20 offen offset:0 sc1\n\t"
"v_cvt_pk_bf16_f32 v224, v224, v225\n\t"
"v_cvt_pk_bf16_f32 v228, v228, v229\n\t"
"v_pk_fma_f32 v[230:231], v[232:233], s[56:57], v[230:231]\n\t"
"v_pk_fma_f32 v[232:233], v[230:231], s[58:59], v[232:233] neg_lo:[0,0,1] neg_hi:[0,0,1]\n\t"
"v_pk_fma_f32 v[234:235], v[68:69], s[60:61], v[230:231]\n\t"
"v_pk_fma_f32 v[68:69], v[230:231], s[60:61], v[68:69]\n\t"
"v_pk_fma_f32 v[230:231], v[168:169], s[62:63], v[232:233]\n\t"
"v_pk_fma_f32 v[168:169], v[232:233], s[62:63], v[168:169]\n\t"
"v_mfma_f32_16x16x32_bf16 v[36:39], a[212:215], a[172:175], v[36:39]\n\t"
"v_accvgpr_write_b32 a105, v224\n\t"
"v_accvgpr_write_b32 a107, v228\n\t"
"v_mfma_f32_16x16x32_bf16 v[36:39], a[228:231], a[176:179], v[36:39]\n\t"
"v_accvgpr_read_b32 v236, a114\n\t"
"v_accvgpr_read_b32 v238, a118\n\t"
"v_mfma_f32_16x16x32_bf16 v[36:39], a[232:235], a[180:183], v[36:39]\n\t"
"v_accvgpr_read_b32 v237, a115\n\t"
"v_accvgpr_read_b32 v239, a119\n\t"
"buffer_load_dwordx4 a[108:111], v252, s[8:11], s20 offen offset:1024 sc1\n\t"
"v_cvt_pk_bf16_f32 v230, v230, v231\n\t"
"v_cvt_pk_bf16_f32 v234, v234, v235\n\t"
"v_pk_fma_f32 v[236:237], v[238:239], s[56:57], v[236:237]\n\t"
"v_pk_fma_f32 v[238:239], v[236:237], s[58:59], v[238:239] neg_lo:[0,0,1] neg_hi:[0,0,1]\n\t"
"v_pk_fma_f32 v[98:99], v[70:71], s[60:61], v[236:237]\n\t"
"v_pk_fma_f32 v[70:71], v[236:237], s[60:61], v[70:71]\n\t"
"v_pk_fma_f32 v[236:237], v[170:171], s[62:63], v[238:239]\n\t"
"v_pk_fma_f32 v[170:171], v[238:239], s[62:63], v[170:171]\n\t"
"v_mfma_f32_16x16x32_bf16 v[40:43], a[212:215], a[176:179], v[40:43]\n\t"
"v_accvgpr_write_b32 a112, v230\n\t"
"v_accvgpr_write_b32 a114, v234\n\t"
"v_mfma_f32_16x16x32_bf16 v[40:43], a[228:231], a[180:183], v[40:43]\n\t"
"v_accvgpr_read_b32 v224, a120\n\t"
"v_accvgpr_read_b32 v226, a124\n\t"
"v_mfma_f32_16x16x32_bf16 v[40:43], a[232:235], a[184:187], v[40:43]\n\t"
"v_accvgpr_read_b32 v225, a121\n\t"
"v_accvgpr_read_b32 v227, a125\n\t"
"global_load_dwordx4 v[240:243], v252, s[46:47] offset:0\n\t"
"buffer_load_dwordx4 a[84:87], v252, s[8:11], s20 offen offset:2048 sc1\n\t"
"v_cvt_pk_bf16_f32 v236, v236, v237\n\t"
"v_cvt_pk_bf16_f32 v98, v98, v99\n\t"
"v_pk_fma_f32 v[224:225], v[226:227], s[56:57], v[224:225]\n\t"
"v_pk_fma_f32 v[226:227], v[224:225], s[58:59], v[226:227] neg_lo:[0,0,1] neg_hi:[0,0,1]\n\t"
"v_pk_fma_f32 v[228:229], v[72:73], s[60:61], v[224:225]\n\t"
"v_pk_fma_f32 v[72:73], v[224:225], s[60:61], v[72:73]\n\t"
"v_pk_fma_f32 v[224:225], v[172:173], s[62:63], v[226:227]\n\t"
"v_pk_fma_f32 v[172:173], v[226:227], s[62:63], v[172:173]\n\t"
"v_mfma_f32_16x16x32_bf16 v[44:47], a[212:215], a[180:183], v[44:47]\n\t"
"v_accvgpr_write_b32 a113, v236\n\t"
"v_accvgpr_write_b32 a115, v98\n\t"
"v_mfma_f32_16x16x32_bf16 v[44:47], a[228:231], a[184:187], v[44:47]\n\t"
"v_accvgpr_read_b32 v230, a122\n\t"
"v_accvgpr_read_b32 v232, a126\n\t"
"v_mfma_f32_16x16x32_bf16 v[44:47], a[232:235], a[188:191], v[44:47]\n\t"
"v_accvgpr_read_b32 v231, a123\n\t"
"v_accvgpr_read_b32 v233, a127\n\t"
"v_accvgpr_read_b32 v236, a128\n\t"
"v_accvgpr_read_b32 v238, a132\n\t"
"v_accvgpr_read_b32 v237, a129\n\t"
"v_accvgpr_read_b32 v239, a133\n\t"
"s_mov_b32 s20, 847872\n\t"
"buffer_load_dwordx4 a[116:119], v252, s[8:11], s20 offen offset:0 sc1\n\t"
"v_cvt_pk_bf16_f32 v224, v224, v225\n\t"
"v_cvt_pk_bf16_f32 v228, v228, v229\n\t"
"v_pk_fma_f32 v[230:231], v[232:233], s[56:57], v[230:231]\n\t"
"v_pk_fma_f32 v[232:233], v[230:231], s[58:59], v[232:233] neg_lo:[0,0,1] neg_hi:[0,0,1]\n\t"
"v_pk_fma_f32 v[234:235], v[74:75], s[60:61], v[230:231]\n\t"
"v_pk_fma_f32 v[74:75], v[230:231], s[60:61], v[74:75]\n\t"
"v_pk_fma_f32 v[230:231], v[174:175], s[62:63], v[232:233]\n\t"
"v_pk_fma_f32 v[174:175], v[232:233], s[62:63], v[174:175]\n\t"
"v_pk_fma_f32 v[236:237], v[238:239], s[56:57], v[236:237]\n\t"
"v_pk_fma_f32 v[238:239], v[236:237], s[58:59], v[238:239] neg_lo:[0,0,1] neg_hi:[0,0,1]\n\t"
"v_pk_fma_f32 v[98:99], v[76:77], s[60:61], v[236:237]\n\t"
"v_pk_fma_f32 v[76:77], v[236:237], s[60:61], v[76:77]\n\t"
"v_pk_fma_f32 v[236:237], v[176:177], s[62:63], v[238:239]\n\t"
"v_pk_fma_f32 v[176:177], v[238:239], s[62:63], v[176:177]\n\t"
"global_load_dwordx4 a[208:211], v252, s[46:47] offset:1024\n\t"
"v_mfma_f32_16x16x32_bf16 v[48:51], a[212:215], a[184:187], v[48:51]\n\t"
"v_accvgpr_write_b32 a120, v224\n\t"
"v_accvgpr_write_b32 a122, v228\n\t"
"v_mfma_f32_16x16x32_bf16 v[48:51], a[228:231], a[188:191], v[48:51]\n\t"
"v_accvgpr_read_b32 v224, a130\n\t"
"v_accvgpr_read_b32 v226, a134\n\t"
"v_mfma_f32_16x16x32_bf16 v[48:51], a[232:235], a[192:195], v[48:51]\n\t"
"global_load_dwordx4 v[248:251], v252, s[48:49] offset:0\n\t"
"v_accvgpr_read_b32 v225, a131\n\t"
"v_accvgpr_read_b32 v227, a135\n\t"
"buffer_load_dwordx4 a[100:103], v252, s[8:11], s20 offen offset:1024 sc1\n\t"
"v_cvt_pk_bf16_f32 v230, v230, v231\n\t"
"v_cvt_pk_bf16_f32 v234, v234, v235\n\t"
"v_cvt_pk_bf16_f32 v236, v236, v237\n\t"
"v_cvt_pk_bf16_f32 v98, v98, v99\n\t"
"v_pk_fma_f32 v[224:225], v[226:227], s[56:57], v[224:225]\n\t"
"v_pk_fma_f32 v[226:227], v[224:225], s[58:59], v[226:227] neg_lo:[0,0,1] neg_hi:[0,0,1]\n\t"
"v_pk_fma_f32 v[228:229], v[78:79], s[60:61], v[224:225]\n\t"
"v_pk_fma_f32 v[78:79], v[224:225], s[60:61], v[78:79]\n\t"
"v_pk_fma_f32 v[224:225], v[178:179], s[62:63], v[226:227]\n\t"
"v_pk_fma_f32 v[178:179], v[226:227], s[62:63], v[178:179]\n\t"
"v_mfma_f32_16x16x32_bf16 v[52:55], a[212:215], a[188:191], v[52:55]\n\t"
"v_accvgpr_write_b32 a121, v230\n\t"
"v_accvgpr_write_b32 a123, v234\n\t"
"v_mfma_f32_16x16x32_bf16 v[52:55], a[228:231], a[192:195], v[52:55]\n\t"
"v_accvgpr_write_b32 a128, v236\n\t"
"v_accvgpr_write_b32 a130, v98\n\t"
"v_mfma_f32_16x16x32_bf16 v[52:55], a[232:235], a[196:199], v[52:55]\n\t"
"v_accvgpr_read_b32 v230, a136\n\t"
"v_accvgpr_read_b32 v232, a140\n\t"
"v_accvgpr_read_b32 v231, a137\n\t"
"v_accvgpr_read_b32 v233, a141\n\t"
"buffer_load_dwordx4 a[124:127], v252, s[8:11], s20 offen offset:2048 sc1\n\t"
"v_cvt_pk_bf16_f32 v224, v224, v225\n\t"
"v_cvt_pk_bf16_f32 v228, v228, v229\n\t"
"v_pk_fma_f32 v[230:231], v[232:233], s[56:57], v[230:231]\n\t"
"v_pk_fma_f32 v[232:233], v[230:231], s[58:59], v[232:233] neg_lo:[0,0,1] neg_hi:[0,0,1]\n\t"
"v_pk_fma_f32 v[234:235], v[80:81], s[60:61], v[230:231]\n\t"
"v_pk_fma_f32 v[80:81], v[230:231], s[60:61], v[80:81]\n\t"
"v_pk_fma_f32 v[230:231], v[180:181], s[62:63], v[232:233]\n\t"
"v_pk_fma_f32 v[180:181], v[232:233], s[62:63], v[180:181]\n\t"
"v_mfma_f32_16x16x32_bf16 v[56:59], a[212:215], a[192:195], v[56:59]\n\t"
"v_accvgpr_write_b32 a129, v224\n\t"
"v_accvgpr_write_b32 a131, v228\n\t"
"v_mfma_f32_16x16x32_bf16 v[56:59], a[228:231], a[196:199], v[56:59]\n\t"
"v_accvgpr_read_b32 v236, a138\n\t"
"v_accvgpr_read_b32 v238, a142\n\t"
"v_mfma_f32_16x16x32_bf16 v[56:59], a[232:235], a[200:203], v[56:59]\n\t"
"v_accvgpr_read_b32 v237, a139\n\t"
"v_accvgpr_read_b32 v239, a143\n\t"
"s_mov_b32 s20, 860160\n\t"
"buffer_load_dwordx4 a[132:135], v252, s[8:11], s20 offen offset:0 sc1\n\t"
"v_cvt_pk_bf16_f32 v230, v230, v231\n\t"
"v_cvt_pk_bf16_f32 v234, v234, v235\n\t"
"v_pk_fma_f32 v[236:237], v[238:239], s[56:57], v[236:237]\n\t"
"v_pk_fma_f32 v[238:239], v[236:237], s[58:59], v[238:239] neg_lo:[0,0,1] neg_hi:[0,0,1]\n\t"
"v_pk_fma_f32 v[98:99], v[82:83], s[60:61], v[236:237]\n\t"
"v_pk_fma_f32 v[82:83], v[236:237], s[60:61], v[82:83]\n\t"
"v_pk_fma_f32 v[236:237], v[182:183], s[62:63], v[238:239]\n\t"
"v_pk_fma_f32 v[182:183], v[238:239], s[62:63], v[182:183]\n\t"
"v_mfma_f32_16x16x32_bf16 v[60:63], a[212:215], a[196:199], v[60:63]\n\t"
"v_accvgpr_write_b32 a136, v230\n\t"
"v_accvgpr_write_b32 a138, v234\n\t"
"v_mfma_f32_16x16x32_bf16 v[60:63], a[228:231], a[200:203], v[60:63]\n\t"
"v_accvgpr_read_b32 v224, a144\n\t"
"v_accvgpr_read_b32 v226, a148\n\t"
"v_mfma_f32_16x16x32_bf16 v[60:63], a[232:235], a[204:207], v[60:63]\n\t"
"v_accvgpr_read_b32 v225, a145\n\t"
"v_accvgpr_read_b32 v227, a149\n\t"
"buffer_load_dwordx4 a[224:227], v252, s[8:11], s20 offen offset:1024 sc1\n\t"
"v_cvt_pk_bf16_f32 v236, v236, v237\n\t"
"v_cvt_pk_bf16_f32 v98, v98, v99\n\t"
"v_pk_fma_f32 v[224:225], v[226:227], s[56:57], v[224:225]\n\t"
"v_pk_fma_f32 v[226:227], v[224:225], s[58:59], v[226:227] neg_lo:[0,0,1] neg_hi:[0,0,1]\n\t"
"v_pk_fma_f32 v[228:229], v[84:85], s[60:61], v[224:225]\n\t"
"v_pk_fma_f32 v[84:85], v[224:225], s[60:61], v[84:85]\n\t"
"v_pk_fma_f32 v[224:225], v[184:185], s[62:63], v[226:227]\n\t"
"v_pk_fma_f32 v[184:185], v[226:227], s[62:63], v[184:185]\n\t"
"s_waitcnt vmcnt(26)\n\t"
"v_mfma_f32_16x16x32_bf16 v[64:67], a[216:219], a[168:171], v[64:67]\n\t"
"v_accvgpr_write_b32 a137, v236\n\t"
"v_accvgpr_write_b32 a139, v98\n\t"
"v_mfma_f32_16x16x32_bf16 v[64:67], a[236:239], a[172:175], v[64:67]\n\t"
"v_accvgpr_read_b32 v230, a146\n\t"
"v_accvgpr_read_b32 v232, a150\n\t"
"v_mfma_f32_16x16x32_bf16 v[64:67], a[240:243], a[176:179], v[64:67]\n\t"
"v_accvgpr_read_b32 v231, a147\n\t"
"v_accvgpr_read_b32 v233, a151\n\t"
"buffer_load_dwordx4 a[140:143], v252, s[8:11], s20 offen offset:2048 sc1\n\t"
"v_cvt_pk_bf16_f32 v224, v224, v225\n\t"
"v_cvt_pk_bf16_f32 v228, v228, v229\n\t"
"v_pk_fma_f32 v[230:231], v[232:233], s[56:57], v[230:231]\n\t"
"v_pk_fma_f32 v[232:233], v[230:231], s[58:59], v[232:233] neg_lo:[0,0,1] neg_hi:[0,0,1]\n\t"
"v_pk_fma_f32 v[234:235], v[86:87], s[60:61], v[230:231]\n\t"
"v_pk_fma_f32 v[86:87], v[230:231], s[60:61], v[86:87]\n\t"
"v_pk_fma_f32 v[230:231], v[186:187], s[62:63], v[232:233]\n\t"
"v_pk_fma_f32 v[186:187], v[232:233], s[62:63], v[186:187]\n\t"
"v_mfma_f32_16x16x32_bf16 v[68:71], a[216:219], a[172:175], v[68:71]\n\t"
"v_accvgpr_write_b32 a144, v224\n\t"
"v_accvgpr_write_b32 a146, v228\n\t"
"v_mfma_f32_16x16x32_bf16 v[68:71], a[236:239], a[176:179], v[68:71]\n\t"
"v_accvgpr_read_b32 v236, a152\n\t"
"v_accvgpr_read_b32 v238, a156\n\t"
"v_mfma_f32_16x16x32_bf16 v[68:71], a[240:243], a[180:183], v[68:71]\n\t"
"v_accvgpr_read_b32 v237, a153\n\t"
"v_accvgpr_read_b32 v239, a157\n\t"
"s_mov_b32 s20, 872448\n\t"
"buffer_load_dwordx4 a[212:215], v252, s[8:11], s20 offen offset:0 sc1\n\t"
"v_cvt_pk_bf16_f32 v230, v230, v231\n\t"
"v_cvt_pk_bf16_f32 v234, v234, v235\n\t"
"v_pk_fma_f32 v[236:237], v[238:239], s[56:57], v[236:237]\n\t"
"v_pk_fma_f32 v[238:239], v[236:237], s[58:59], v[238:239] neg_lo:[0,0,1] neg_hi:[0,0,1]\n\t"
"v_pk_fma_f32 v[98:99], v[88:89], s[60:61], v[236:237]\n\t"
"v_pk_fma_f32 v[88:89], v[236:237], s[60:61], v[88:89]\n\t"
"v_pk_fma_f32 v[236:237], v[188:189], s[62:63], v[238:239]\n\t"
"v_pk_fma_f32 v[188:189], v[238:239], s[62:63], v[188:189]\n\t"
"v_mfma_f32_16x16x32_bf16 v[72:75], a[216:219], a[176:179], v[72:75]\n\t"
"v_accvgpr_write_b32 a145, v230\n\t"
"v_accvgpr_write_b32 a147, v234\n\t"
"v_mfma_f32_16x16x32_bf16 v[72:75], a[236:239], a[180:183], v[72:75]\n\t"
"v_accvgpr_read_b32 v224, a154\n\t"
"v_accvgpr_read_b32 v226, a158\n\t"
"v_mfma_f32_16x16x32_bf16 v[72:75], a[240:243], a[184:187], v[72:75]\n\t"
"v_accvgpr_read_b32 v225, a155\n\t"
"v_accvgpr_read_b32 v227, a159\n\t"
"buffer_load_dwordx4 a[228:231], v252, s[8:11], s20 offen offset:1024 sc1\n\t"
"v_cvt_pk_bf16_f32 v236, v236, v237\n\t"
"v_cvt_pk_bf16_f32 v98, v98, v99\n\t"
"v_pk_fma_f32 v[224:225], v[226:227], s[56:57], v[224:225]\n\t"
"v_pk_fma_f32 v[226:227], v[224:225], s[58:59], v[226:227] neg_lo:[0,0,1] neg_hi:[0,0,1]\n\t"
"v_pk_fma_f32 v[228:229], v[90:91], s[60:61], v[224:225]\n\t"
"v_pk_fma_f32 v[90:91], v[224:225], s[60:61], v[90:91]\n\t"
"v_pk_fma_f32 v[224:225], v[190:191], s[62:63], v[226:227]\n\t"
"v_pk_fma_f32 v[190:191], v[226:227], s[62:63], v[190:191]\n\t"
"v_mfma_f32_16x16x32_bf16 v[76:79], a[216:219], a[180:183], v[76:79]\n\t"
"v_accvgpr_write_b32 a152, v236\n\t"
"v_accvgpr_write_b32 a154, v98\n\t"
"v_mfma_f32_16x16x32_bf16 v[76:79], a[236:239], a[184:187], v[76:79]\n\t"
"v_accvgpr_read_b32 v230, a160\n\t"
"v_accvgpr_read_b32 v232, a164\n\t"
"v_mfma_f32_16x16x32_bf16 v[76:79], a[240:243], a[188:191], v[76:79]\n\t"
"v_accvgpr_read_b32 v231, a161\n\t"
"v_accvgpr_read_b32 v233, a165\n\t"
"ds_read_b128 a[168:171], v244 offset:27648\n\t"
"ds_read_b128 a[172:175], v244 offset:28160\n\t"
"ds_read_b128 a[176:179], v244 offset:28672\n\t"
"buffer_load_dwordx4 a[232:235], v252, s[8:11], s20 offen offset:2048 sc1\n\t"
"v_cvt_pk_bf16_f32 v224, v224, v225\n\t"
"v_cvt_pk_bf16_f32 v228, v228, v229\n\t"
"v_pk_fma_f32 v[230:231], v[232:233], s[56:57], v[230:231]\n\t"
"v_pk_fma_f32 v[232:233], v[230:231], s[58:59], v[232:233] neg_lo:[0,0,1] neg_hi:[0,0,1]\n\t"
"v_pk_fma_f32 v[234:235], v[92:93], s[60:61], v[230:231]\n\t"
"v_pk_fma_f32 v[92:93], v[230:231], s[60:61], v[92:93]\n\t"
"v_pk_fma_f32 v[230:231], v[192:193], s[62:63], v[232:233]\n\t"
"v_pk_fma_f32 v[192:193], v[232:233], s[62:63], v[192:193]\n\t"
"v_mfma_f32_16x16x32_bf16 v[80:83], a[216:219], a[184:187], v[80:83]\n\t"
"v_accvgpr_write_b32 a153, v224\n\t"
"v_accvgpr_write_b32 a155, v228\n\t"
"v_mfma_f32_16x16x32_bf16 v[80:83], a[236:239], a[188:191], v[80:83]\n\t"
"v_accvgpr_read_b32 v236, a162\n\t"
"v_accvgpr_read_b32 v238, a166\n\t"
"v_mfma_f32_16x16x32_bf16 v[80:83], a[240:243], a[192:195], v[80:83]\n\t"
"v_accvgpr_read_b32 v237, a163\n\t"
"v_accvgpr_read_b32 v239, a167\n\t"
"s_mov_b32 s20, 884736\n\t"
"buffer_load_dwordx4 a[156:159], v252, s[8:11], s20 offen offset:0 sc1\n\t"
"v_cvt_pk_bf16_f32 v230, v230, v231\n\t"
"v_cvt_pk_bf16_f32 v234, v234, v235\n\t"
"v_pk_fma_f32 v[236:237], v[238:239], s[56:57], v[236:237]\n\t"
"v_pk_fma_f32 v[238:239], v[236:237], s[58:59], v[238:239] neg_lo:[0,0,1] neg_hi:[0,0,1]\n\t"
"v_pk_fma_f32 v[98:99], v[94:95], s[60:61], v[236:237]\n\t"
"v_pk_fma_f32 v[94:95], v[236:237], s[60:61], v[94:95]\n\t"
"v_pk_fma_f32 v[236:237], v[194:195], s[62:63], v[238:239]\n\t"
"v_pk_fma_f32 v[194:195], v[238:239], s[62:63], v[194:195]\n\t"
"v_mfma_f32_16x16x32_bf16 v[84:87], a[216:219], a[188:191], v[84:87]\n\t"
"ds_read_b128 a[180:183], v244 offset:29184\n\t"
"v_accvgpr_write_b32 a160, v230\n\t"
"v_mfma_f32_16x16x32_bf16 v[84:87], a[236:239], a[192:195], v[84:87]\n\t"
"v_accvgpr_write_b32 a162, v234\n\t"
"v_cvt_pk_bf16_f32 v236, v236, v237\n\t"
"v_mfma_f32_16x16x32_bf16 v[84:87], a[240:243], a[196:199], v[84:87]\n\t"
"buffer_load_dwordx4 a[248:251], v252, s[8:11], s20 offen offset:1024 sc1\n\t"
"v_cvt_pk_bf16_f32 v98, v98, v99\n\t"
"ds_read_b128 a[184:187], v244 offset:29696\n\t"
"v_mfma_f32_16x16x32_bf16 v[88:91], a[216:219], a[192:195], v[88:91]\n\t"
"v_accvgpr_write_b32 a161, v236\n\t"
"v_accvgpr_write_b32 a163, v98\n\t"
"v_mfma_f32_16x16x32_bf16 v[88:91], a[236:239], a[196:199], v[88:91]\n\t"
"s_waitcnt vmcnt(14)\n\t"
"ds_read_b64_tr_b16 v[236:237], v240 offset:1536\n\t"
"v_mfma_f32_16x16x32_bf16 v[88:91], a[240:243], a[200:203], v[88:91]\n\t"
"ds_read_b64_tr_b16 v[238:239], v240 offset:3072\n\t"
"buffer_load_dwordx4 a[164:167], v252, s[8:11], s20 offen offset:2048 sc1\n\t"
"ds_read_b64_tr_b16 v[232:233], v241 offset:1536\n\t"
"ds_read_b64_tr_b16 v[234:235], v241 offset:3072\n\t"
"v_mfma_f32_16x16x32_bf16 v[92:95], a[216:219], a[196:199], v[92:95]\n\t"
"ds_read_b128 a[188:191], v244 offset:30208\n\t"
"v_mfma_f32_16x16x32_bf16 v[92:95], a[236:239], a[200:203], v[92:95]\n\t"
"ds_read_b128 a[192:195], v244 offset:30720\n\t"
"v_mfma_f32_16x16x32_bf16 v[92:95], a[240:243], a[204:207], v[92:95]\n\t"
"s_mov_b32 s20, 897024\n\t"
"buffer_load_dwordx4 a[252:255], v252, s[8:11], s20 offen offset:0 sc1\n\t"
"s_waitcnt lgkmcnt(8)\n\t"
"v_mfma_f32_16x16x32_bf16 v[100:103], a[220:223], a[168:171], v[100:103]\n\t"
"ds_read_b64_tr_b16 v[228:229], v240 offset:21504\n\t"
"v_mfma_f32_16x16x32_bf16 v[100:103], a[244:247], a[172:175], v[100:103]\n\t"
"ds_read_b64_tr_b16 v[230:231], v240 offset:23040\n\t"
"v_mfma_f32_16x16x32_bf16 v[100:103], a[40:43], a[176:179], v[100:103]\n\t"
"buffer_load_dwordx4 v[96:99], v252, s[8:11], s20 offen offset:1024 sc1\n\t"
"ds_read_b128 a[196:199], v244 offset:31232\n\t"
"ds_read_b128 a[200:203], v244 offset:31744\n\t"
"s_waitcnt vmcnt(14) lgkmcnt(8)\n\t"
"v_mfma_f32_16x16x32_bf16 v[236:239], v[236:239], a[208:211], 0\n\t"
"ds_read_b128 a[204:207], v244 offset:32256\n\t"
"ds_read_b64_tr_b16 v[224:225], v241 offset:21504\n\t"
"s_waitcnt lgkmcnt(8)\n\t"
"v_mfma_f32_16x16x32_bf16 v[232:235], v[232:235], a[208:211], 0\n\t"
"ds_read_b64_tr_b16 v[226:227], v241 offset:23040\n\t"
"buffer_load_dwordx4 a[216:219], v252, s[8:11], s20 offen offset:2048 sc1\n\t"
"v_mfma_f32_16x16x32_bf16 v[104:107], a[220:223], a[172:175], v[104:107]\n\t"
"v_mfma_f32_16x16x32_bf16 v[104:107], a[244:247], a[176:179], v[104:107]\n\t"
"v_mfma_f32_16x16x32_bf16 v[104:107], a[40:43], a[180:183], v[104:107]\n\t"
"s_mov_b32 s20, 909312\n\t"
"buffer_load_dwordx4 a[236:239], v252, s[8:11], s20 offen offset:0 sc1\n\t"
"s_waitcnt lgkmcnt(2)\n\t"
"s_barrier\n\t"
"v_mfma_f32_16x16x32_bf16 v[108:111], a[220:223], a[176:179], v[108:111]\n\t"
"v_cvt_pk_bf16_f32 v236, v236, v237\n\t"
"v_cvt_pk_bf16_f32 v237, v238, v239\n\t"
"v_mfma_f32_16x16x32_bf16 v[108:111], a[244:247], a[180:183], v[108:111]\n\t"
"v_cvt_pk_bf16_f32 v238, v232, v233\n\t"
"v_cvt_pk_bf16_f32 v239, v234, v235\n\t"
"v_mfma_f32_16x16x32_bf16 v[108:111], a[40:43], a[184:187], v[108:111]\n\t"
"buffer_load_dwordx4 a[240:243], v252, s[8:11], s20 offen offset:1024 sc1\n\t"
"v_mfma_f32_16x16x32_bf16 v[228:231], v[228:231], a[208:211], 0\n\t"
"s_mov_b64 exec, s[44:45]\n\t"
"ds_write_b128 v242, v[236:239] offset:0\n\t"
"s_mov_b64 exec, -1\n\t"
"s_waitcnt lgkmcnt(1)\n\t"
"v_mfma_f32_16x16x32_bf16 v[224:227], v[224:227], a[208:211], 0\n\t"
"ds_read_b64_tr_b16 v[232:233], v240 offset:41472\n\t"
"ds_read_b64_tr_b16 v[234:235], v240 offset:43008\n\t"
"v_mfma_f32_16x16x32_bf16 v[112:115], a[220:223], a[180:183], v[112:115]\n\t"
"ds_read_b64_tr_b16 v[236:237], v241 offset:41472\n\t"
"v_mfma_f32_16x16x32_bf16 v[112:115], a[244:247], a[184:187], v[112:115]\n\t"
"ds_read_b64_tr_b16 v[238:239], v241 offset:43008\n\t"
"v_add_u32 v240, 59904, v240\n\t"
"v_mfma_f32_16x16x32_bf16 v[112:115], a[40:43], a[188:191], v[112:115]\n\t"
"v_add_u32 v241, 59904, v241\n\t"
"v_mfma_f32_16x16x32_bf16 v[116:119], a[220:223], a[184:187], v[116:119]\n\t"
"v_cvt_pk_bf16_f32 v228, v228, v229\n\t"
"v_cvt_pk_bf16_f32 v229, v230, v231\n\t"
"v_mfma_f32_16x16x32_bf16 v[116:119], a[244:247], a[188:191], v[116:119]\n\t"
"v_cvt_pk_bf16_f32 v230, v224, v225\n\t"
"v_cvt_pk_bf16_f32 v231, v226, v227\n\t"
"v_mfma_f32_16x16x32_bf16 v[116:119], a[40:43], a[192:195], v[116:119]\n\t"
"s_waitcnt vmcnt(16)\n\t"
"ds_write_b128 v249, v[196:199] offset:18432\n\t"
"v_mfma_f32_16x16x32_bf16 v[120:123], a[220:223], a[188:191], v[120:123]\n\t"
"ds_read_b64_tr_b16 v[224:225], v240 offset:1536\n\t"
"v_mfma_f32_16x16x32_bf16 v[120:123], a[244:247], a[192:195], v[120:123]\n\t"
"ds_read_b64_tr_b16 v[226:227], v240 offset:3072\n\t"
"v_mfma_f32_16x16x32_bf16 v[120:123], a[40:43], a[196:199], v[120:123]\n\t"
"s_mov_b64 exec, s[44:45]\n\t"
"ds_write_b128 v242, v[228:231] offset:1536\n\t"
"s_mov_b64 exec, -1\n\t"
"v_mfma_f32_16x16x32_bf16 v[124:127], a[220:223], a[192:195], v[124:127]\n\t"
"ds_read_b64_tr_b16 v[228:229], v241 offset:1536\n\t"
"v_mfma_f32_16x16x32_bf16 v[124:127], a[244:247], a[196:199], v[124:127]\n\t"
"ds_read_b64_tr_b16 v[230:231], v241 offset:3072\n\t"
"v_mfma_f32_16x16x32_bf16 v[124:127], a[40:43], a[200:203], v[124:127]\n\t"
"ds_write_b64 v250, a[104:105] offset:18432\n\t"
"s_waitcnt lgkmcnt(9)\n\t"
"v_mfma_f32_16x16x32_bf16 v[232:235], v[232:235], a[208:211], 0\n\t"
"ds_write_b128 v249, v[200:203] offset:24576\n\t"
"s_waitcnt lgkmcnt(8)\n\t"
"v_mfma_f32_16x16x32_bf16 v[236:239], v[236:239], a[208:211], 0\n\t"
"v_mfma_f32_16x16x32_bf16 v[128:131], a[220:223], a[196:199], v[128:131]\n\t"
"v_mfma_f32_16x16x32_bf16 v[128:131], a[244:247], a[200:203], v[128:131]\n\t"
"v_mfma_f32_16x16x32_bf16 v[128:131], a[40:43], a[204:207], v[128:131]\n\t"
"buffer_load_dwordx4 a[220:223], v252, s[8:11], s20 offen offset:2048 sc1\n\t"
"v_mfma_f32_16x16x32_bf16 v[132:135], a[48:51], a[168:171], v[132:135]\n\t"
"v_cvt_pk_bf16_f32 v232, v232, v233\n\t"
"v_cvt_pk_bf16_f32 v233, v234, v235\n\t"
"v_mfma_f32_16x16x32_bf16 v[132:135], a[44:47], a[172:175], v[132:135]\n\t"
"v_cvt_pk_bf16_f32 v234, v236, v237\n\t"
"v_cvt_pk_bf16_f32 v235, v238, v239\n\t"
"v_mfma_f32_16x16x32_bf16 v[132:135], a[56:59], a[176:179], v[132:135]\n\t"
"s_mov_b32 s20, 1069056\n\t"
"buffer_load_dwordx4 a[244:247], v252, s[8:11], s20 offen offset:0 sc1\n\t"
"s_waitcnt lgkmcnt(5)\n\t"
"v_mfma_f32_16x16x32_bf16 v[224:227], v[224:227], a[208:211], 0\n\t"
"s_mov_b64 exec, s[44:45]\n\t"
"ds_write_b128 v242, v[232:235] offset:3072\n\t"
"s_mov_b64 exec, -1\n\t"
"s_waitcnt lgkmcnt(3)\n\t"
"v_mfma_f32_16x16x32_bf16 v[228:231], v[228:231], a[208:211], 0\n\t"
"ds_read_b64_tr_b16 v[236:237], v240 offset:21504\n\t"
"ds_read_b64_tr_b16 v[238:239], v240 offset:23040\n\t"
"v_mfma_f32_16x16x32_bf16 v[136:139], a[48:51], a[172:175], v[136:139]\n\t"
"ds_read_b64_tr_b16 v[232:233], v241 offset:21504\n\t"
"v_mfma_f32_16x16x32_bf16 v[136:139], a[44:47], a[176:179], v[136:139]\n\t"
"v_mfma_f32_16x16x32_bf16 v[136:139], a[56:59], a[180:183], v[136:139]\n\t"
"ds_read_b64_tr_b16 v[234:235], v241 offset:23040\n\t"
"v_mfma_f32_16x16x32_bf16 v[140:143], a[48:51], a[176:179], v[140:143]\n\t"
"v_cvt_pk_bf16_f32 v224, v224, v225\n\t"
"v_cvt_pk_bf16_f32 v225, v226, v227\n\t"
"v_mfma_f32_16x16x32_bf16 v[140:143], a[44:47], a[180:183], v[140:143]\n\t"
"v_cvt_pk_bf16_f32 v226, v228, v229\n\t"
"v_cvt_pk_bf16_f32 v227, v230, v231\n\t"
"v_mfma_f32_16x16x32_bf16 v[140:143], a[56:59], a[184:187], v[140:143]\n\t"
"s_mov_b64 exec, s[44:45]\n\t"
"ds_write_b128 v242, v[224:227] offset:4608\n\t"
"s_mov_b64 exec, -1\n\t"
"v_mfma_f32_16x16x32_bf16 v[144:147], a[48:51], a[180:183], v[144:147]\n\t"
"ds_read_b64_tr_b16 v[228:229], v240 offset:41472\n\t"
"v_mfma_f32_16x16x32_bf16 v[144:147], a[44:47], a[184:187], v[144:147]\n\t"
"ds_read_b64_tr_b16 v[230:231], v240 offset:43008\n\t"
"v_mfma_f32_16x16x32_bf16 v[144:147], a[56:59], a[188:191], v[144:147]\n\t"
"ds_read_b64_tr_b16 v[224:225], v241 offset:41472\n\t"
"ds_read_b64_tr_b16 v[226:227], v241 offset:43008\n\t"
"v_mfma_f32_16x16x32_bf16 v[148:151], a[48:51], a[184:187], v[148:151]\n\t"
"v_mfma_f32_16x16x32_bf16 v[148:151], a[44:47], a[188:191], v[148:151]\n\t"
"v_mfma_f32_16x16x32_bf16 v[148:151], a[56:59], a[192:195], v[148:151]\n\t"
"ds_write_b64 v250, a[106:107] offset:24576\n\t"
"s_waitcnt lgkmcnt(8)\n\t"
"v_mfma_f32_16x16x32_bf16 v[236:239], v[236:239], a[208:211], 0\n\t"
"s_waitcnt lgkmcnt(6)\n\t"
"v_mfma_f32_16x16x32_bf16 v[232:235], v[232:235], a[208:211], 0\n\t"
"v_mfma_f32_16x16x32_bf16 v[152:155], a[48:51], a[188:191], v[152:155]\n\t"
"v_mfma_f32_16x16x32_bf16 v[152:155], a[44:47], a[192:195], v[152:155]\n\t"
"v_mfma_f32_16x16x32_bf16 v[152:155], a[56:59], a[196:199], v[152:155]\n\t"
"v_mfma_f32_16x16x32_bf16 v[156:159], a[48:51], a[192:195], v[156:159]\n\t"
"s_nop 1\n\t"
"v_cvt_pk_bf16_f32 v236, v236, v237\n\t"
"v_cvt_pk_bf16_f32 v237, v238, v239\n\t"
"v_mfma_f32_16x16x32_bf16 v[156:159], a[44:47], a[196:199], v[156:159]\n\t"
"v_cvt_pk_bf16_f32 v238, v232, v233\n\t"
"v_mfma_f32_16x16x32_bf16 v[156:159], a[56:59], a[200:203], v[156:159]\n\t"
"v_cvt_pk_bf16_f32 v239, v234, v235\n\t"
"s_waitcnt lgkmcnt(3)\n\t"
"v_mfma_f32_16x16x32_bf16 v[228:231], v[228:231], a[208:211], 0\n\t"
"s_waitcnt lgkmcnt(1)\n\t"
"v_mfma_f32_16x16x32_bf16 v[224:227], v[224:227], a[208:211], 0\n\t"
"s_mov_b64 exec, s[44:45]\n\t"
"ds_write_b128 v242, v[236:239] offset:6144\n\t"
"s_mov_b64 exec, -1\n\t"
"v_mfma_f32_16x16x32_bf16 v[160:163], a[48:51], a[196:199], v[160:163]\n\t"
"v_mfma_f32_16x16x32_bf16 v[160:163], a[44:47], a[200:203], v[160:163]\n\t"
"v_mfma_f32_16x16x32_bf16 v[160:163], a[56:59], a[204:207], v[160:163]\n\t"
"buffer_load_dwordx4 a[44:47], v252, s[8:11], s20 offen offset:1024 sc1\n\t"
"v_mfma_f32_16x16x32_bf16 v[164:167], a[60:63], a[168:171], v[164:167]\n\t"
"v_cvt_pk_bf16_f32 v228, v228, v229\n\t"
"v_mfma_f32_16x16x32_bf16 v[164:167], a[64:67], a[172:175], v[164:167]\n\t"
"v_cvt_pk_bf16_f32 v229, v230, v231\n\t"
"v_cvt_pk_bf16_f32 v230, v224, v225\n\t"
"v_mfma_f32_16x16x32_bf16 v[164:167], a[52:55], a[176:179], v[164:167]\n\t"
"v_cvt_pk_bf16_f32 v231, v226, v227\n\t"
"v_mfma_f32_16x16x32_bf16 v[168:171], a[60:63], a[172:175], v[168:171]\n\t"
"v_mfma_f32_16x16x32_bf16 v[168:171], a[64:67], a[176:179], v[168:171]\n\t"
"v_mfma_f32_16x16x32_bf16 v[168:171], a[52:55], a[180:183], v[168:171]\n\t"
"s_mov_b64 exec, s[44:45]\n\t"
"ds_write_b128 v242, v[228:231] offset:7680\n\t"
"s_mov_b64 exec, -1\n\t"
"v_mfma_f32_16x16x32_bf16 v[172:175], a[60:63], a[176:179], v[172:175]\n\t"
"v_mfma_f32_16x16x32_bf16 v[172:175], a[64:67], a[180:183], v[172:175]\n\t"
"v_mfma_f32_16x16x32_bf16 v[172:175], a[52:55], a[184:187], v[172:175]\n\t"
"v_mfma_f32_16x16x32_bf16 v[176:179], a[60:63], a[180:183], v[176:179]\n\t"
"v_mfma_f32_16x16x32_bf16 v[176:179], a[64:67], a[184:187], v[176:179]\n\t"
"v_mfma_f32_16x16x32_bf16 v[176:179], a[52:55], a[188:191], v[176:179]\n\t"
"v_mfma_f32_16x16x32_bf16 v[180:183], a[60:63], a[184:187], v[180:183]\n\t"
"v_mfma_f32_16x16x32_bf16 v[180:183], a[64:67], a[188:191], v[180:183]\n\t"
"v_mfma_f32_16x16x32_bf16 v[180:183], a[52:55], a[192:195], v[180:183]\n\t"
"v_mfma_f32_16x16x32_bf16 v[184:187], a[60:63], a[188:191], v[184:187]\n\t"
"v_mfma_f32_16x16x32_bf16 v[184:187], a[64:67], a[192:195], v[184:187]\n\t"
"v_mfma_f32_16x16x32_bf16 v[184:187], a[52:55], a[196:199], v[184:187]\n\t"
"s_waitcnt lgkmcnt(0)\n\t"
"s_barrier\n\t"
"v_mfma_f32_16x16x32_bf16 v[188:191], a[60:63], a[192:195], v[188:191]\n\t"
"ds_read_b128 a[168:171], v244 offset:0\n\t"
"v_mfma_f32_16x16x32_bf16 v[188:191], a[64:67], a[196:199], v[188:191]\n\t"
"ds_read_b128 a[172:175], v244 offset:512\n\t"
"v_mfma_f32_16x16x32_bf16 v[188:191], a[52:55], a[200:203], v[188:191]\n\t"
"ds_read_b128 a[176:179], v244 offset:1024\n\t"
"ds_read_b128 a[180:183], v244 offset:1536\n\t"
"ds_read_b128 a[184:187], v244 offset:2048\n\t"
"ds_read_b128 a[40:43], v252 offset:18432\n\t"
"ds_read_b128 a[188:191], v244 offset:2560\n\t"
"v_mfma_f32_16x16x32_bf16 v[192:195], a[60:63], a[196:199], v[192:195]\n\t"
"ds_read_b128 a[48:51], v252 offset:19456\n\t"
"v_mfma_f32_16x16x32_bf16 v[192:195], a[64:67], a[200:203], v[192:195]\n\t"
"ds_read_b128 a[192:195], v244 offset:3072\n\t"
"v_mfma_f32_16x16x32_bf16 v[192:195], a[52:55], a[204:207], v[192:195]\n\t"
"buffer_load_dwordx4 a[104:107], v252, s[8:11], s20 offen offset:2048 sc1\n\t"
"ds_read_b128 a[196:199], v244 offset:3584\n\t"
"ds_read_b128 a[200:203], v244 offset:4096\n\t"
"Lst15_%=:\n\t"
"s_waitcnt lgkmcnt(8)\n\t"
"v_mfma_f32_16x16x32_bf16 v[0:3], a[72:75], a[168:171], v[0:3]\n\t"
"ds_read_b128 a[56:59], v252 offset:20480\n\t"
"s_add_u32 s21, s21, 6291456\n\t"
"v_add_u32 v254, 6291456, v254\n\t"
"v_mfma_f32_16x16x32_bf16 v[0:3], a[68:71], a[172:175], v[0:3]\n\t"
"ds_read_b128 a[204:207], v244 offset:4608\n\t"
"v_mfma_f32_16x16x32_bf16 v[0:3], a[80:83], a[176:179], v[0:3]\n\t"
"s_mov_b32 s20, 1081344\n\t"
"buffer_load_dwordx4 a[64:67], v252, s[8:11], s20 offen offset:0 sc1\n\t"
"ds_write_b128 v249, v[204:207] offset:30720\n\t"
"s_waitcnt lgkmcnt(10)\n\t"
"v_mfma_f32_16x16x32_bf16 v[4:7], a[72:75], a[172:175], v[4:7]\n\t"
"v_mfma_f32_16x16x32_bf16 v[4:7], a[68:71], a[176:179], v[4:7]\n\t"
"v_mfma_f32_16x16x32_bf16 v[4:7], a[80:83], a[180:183], v[4:7]\n\t"
"buffer_load_dwordx4 a[52:55], v252, s[8:11], s20 offen offset:1024 sc1\n\t"
"s_waitcnt lgkmcnt(9)\n\t"
"v_mfma_f32_16x16x32_bf16 v[8:11], a[72:75], a[176:179], v[8:11]\n\t"
"v_mfma_f32_16x16x32_bf16 v[8:11], a[68:71], a[180:183], v[8:11]\n\t"
"v_mfma_f32_16x16x32_bf16 v[8:11], a[80:83], a[184:187], v[8:11]\n\t"
"buffer_load_dwordx4 v[200:203], v252, s[8:11], s20 offen offset:2048 sc1\n\t"
"s_add_u32 s29, s22, 384\n\t"
"s_waitcnt lgkmcnt(8)\n\t"
"buffer_store_dwordx4 a[40:43], v248, s[16:19], s29 offen offset:0 nt\n\t"
"s_waitcnt lgkmcnt(7)\n\t"
"v_mfma_f32_16x16x32_bf16 v[12:15], a[72:75], a[180:183], v[12:15]\n\t"
"v_mfma_f32_16x16x32_bf16 v[12:15], a[68:71], a[184:187], v[12:15]\n\t"
"v_mfma_f32_16x16x32_bf16 v[12:15], a[80:83], a[188:191], v[12:15]\n\t"
"ds_write_b64 v250, a[112:113] offset:30720\n\t"
"s_waitcnt lgkmcnt(6)\n\t"
"v_mfma_f32_16x16x32_bf16 v[16:19], a[72:75], a[184:187], v[16:19]\n\t"
"v_mfma_f32_16x16x32_bf16 v[16:19], a[68:71], a[188:191], v[16:19]\n\t"
"v_mfma_f32_16x16x32_bf16 v[16:19], a[80:83], a[192:195], v[16:19]\n\t"
"s_add_u32 s29, s22, 384\n\t"
"buffer_store_dwordx4 a[48:51], v248, s[16:19], s29 offen offset:128 nt\n\t"
"s_waitcnt lgkmcnt(5)\n\t"
"v_mfma_f32_16x16x32_bf16 v[20:23], a[72:75], a[188:191], v[20:23]\n\t"
"v_mfma_f32_16x16x32_bf16 v[20:23], a[68:71], a[192:195], v[20:23]\n\t"
"v_mfma_f32_16x16x32_bf16 v[20:23], a[80:83], a[196:199], v[20:23]\n\t"
"ds_write_b128 v249, v[208:211] offset:36864\n\t"
"s_waitcnt lgkmcnt(5)\n\t"
"v_mfma_f32_16x16x32_bf16 v[24:27], a[72:75], a[192:195], v[24:27]\n\t"
"v_mfma_f32_16x16x32_bf16 v[24:27], a[68:71], a[196:199], v[24:27]\n\t"
"v_mfma_f32_16x16x32_bf16 v[24:27], a[80:83], a[200:203], v[24:27]\n\t"
"s_add_u32 s29, s22, 384\n\t"
"s_waitcnt lgkmcnt(4)\n\t"
"buffer_store_dwordx4 a[56:59], v248, s[16:19], s29 offen offset:256 nt\n\t"
"s_waitcnt lgkmcnt(3)\n\t"
"v_mfma_f32_16x16x32_bf16 v[28:31], a[72:75], a[196:199], v[28:31]\n\t"
"v_mfma_f32_16x16x32_bf16 v[28:31], a[68:71], a[200:203], v[28:31]\n\t"
"v_mfma_f32_16x16x32_bf16 v[28:31], a[80:83], a[204:207], v[28:31]\n\t"
"s_mov_b32 s20, 1093632\n\t"
"buffer_load_dwordx4 a[72:75], v252, s[8:11], s20 offen offset:0 sc1\n\t"
"v_mfma_f32_16x16x32_bf16 v[32:35], a[92:95], a[168:171], v[32:35]\n\t"
"v_mfma_f32_16x16x32_bf16 v[32:35], a[88:91], a[172:175], v[32:35]\n\t"
"v_mfma_f32_16x16x32_bf16 v[32:35], a[96:99], a[176:179], v[32:35]\n\t"
"buffer_load_dwordx4 a[80:83], v252, s[8:11], s20 offen offset:1024 sc1\n\t"
"v_mfma_f32_16x16x32_bf16 v[36:39], a[92:95], a[172:175], v[36:39]\n\t"
"ds_read_b64_tr_b16 v[232:233], v240 offset:512\n\t"
"v_mfma_f32_16x16x32_bf16 v[36:39], a[88:91], a[176:179], v[36:39]\n\t"
"ds_read_b64_tr_b16 v[234:235], v240 offset:2048\n\t"
"v_mfma_f32_16x16x32_bf16 v[36:39], a[96:99], a[180:183], v[36:39]\n\t"
"ds_write_b64 v250, a[114:115] offset:36864\n\t"
"v_mfma_f32_16x16x32_bf16 v[40:43], a[92:95], a[176:179], v[40:43]\n\t"
"v_mfma_f32_16x16x32_bf16 v[40:43], a[88:91], a[180:183], v[40:43]\n\t"
"v_mfma_f32_16x16x32_bf16 v[40:43], a[96:99], a[184:187], v[40:43]\n\t"
"buffer_load_dwordx4 a[68:71], v252, s[8:11], s20 offen offset:2048 sc1\n\t"
"v_mfma_f32_16x16x32_bf16 v[44:47], a[92:95], a[180:183], v[44:47]\n\t"
"v_mfma_f32_16x16x32_bf16 v[44:47], a[88:91], a[184:187], v[44:47]\n\t"
"v_mfma_f32_16x16x32_bf16 v[44:47], a[96:99], a[188:191], v[44:47]\n\t"
"s_waitcnt lgkmcnt(0)\n\t"
"s_barrier\n\t"
"v_mfma_f32_16x16x32_bf16 v[48:51], a[92:95], a[184:187], v[48:51]\n\t"
"ds_read_b128 a[40:43], v252 offset:30720\n\t"
"v_mfma_f32_16x16x32_bf16 v[48:51], a[88:91], a[188:191], v[48:51]\n\t"
"ds_read_b64_tr_b16 v[236:237], v241 offset:512\n\t"
"v_mfma_f32_16x16x32_bf16 v[48:51], a[96:99], a[192:195], v[48:51]\n\t"
"ds_read_b64_tr_b16 v[238:239], v241 offset:2048\n\t"
"ds_read_b128 a[48:51], v252 offset:31744\n\t"
"v_mfma_f32_16x16x32_bf16 v[52:55], a[92:95], a[188:191], v[52:55]\n\t"
"ds_read_b128 a[56:59], v252 offset:32768\n\t"
"v_mfma_f32_16x16x32_bf16 v[52:55], a[88:91], a[192:195], v[52:55]\n\t"
"ds_read_b64_tr_b16 v[224:225], v240 offset:20480\n\t"
"v_mfma_f32_16x16x32_bf16 v[52:55], a[96:99], a[196:199], v[52:55]\n\t"
"s_mov_b32 s20, 1105920\n\t"
"buffer_load_dwordx4 v[208:211], v252, s[8:11], s20 offen offset:0 sc1\n\t"
"v_mfma_f32_16x16x32_bf16 v[232:235], v[232:235], a[208:211], 0\n\t"
"ds_write_b128 v249, v[212:215] offset:18432\n\t"
"v_mfma_f32_16x16x32_bf16 v[56:59], a[92:95], a[192:195], v[56:59]\n\t"
"ds_read_b64_tr_b16 v[226:227], v240 offset:22016\n\t"
"v_mfma_f32_16x16x32_bf16 v[56:59], a[88:91], a[196:199], v[56:59]\n\t"
"ds_read_b64_tr_b16 v[228:229], v241 offset:20480\n\t"
"v_mfma_f32_16x16x32_bf16 v[56:59], a[96:99], a[200:203], v[56:59]\n\t"
"s_add_u32 s29, s22, 196992\n\t"
"s_waitcnt lgkmcnt(8)\n\t"
"buffer_store_dwordx4 a[40:43], v248, s[16:19], s29 offen offset:0 nt\n\t"
"s_waitcnt lgkmcnt(6)\n\t"
"v_mfma_f32_16x16x32_bf16 v[236:239], v[236:239], a[208:211], 0\n\t"
"ds_read_b64_tr_b16 v[230:231], v241 offset:22016\n\t"
"v_mfma_f32_16x16x32_bf16 v[60:63], a[92:95], a[196:199], v[60:63]\n\t"
"v_cvt_pk_bf16_f32 v232, v232, v233\n\t"
"v_cvt_pk_bf16_f32 v233, v234, v235\n\t"
"v_mfma_f32_16x16x32_bf16 v[60:63], a[88:91], a[200:203], v[60:63]\n\t"
"v_mfma_f32_16x16x32_bf16 v[60:63], a[96:99], a[204:207], v[60:63]\n\t"
"buffer_load_dwordx4 a[88:91], v252, s[8:11], s20 offen offset:1024 sc1\n\t"
"v_mfma_f32_16x16x32_bf16 v[64:67], a[76:79], a[168:171], v[64:67]\n\t"
"ds_read_b128 a[168:171], v244 offset:9216\n\t"
"v_mfma_f32_16x16x32_bf16 v[64:67], a[108:111], a[172:175], v[64:67]\n\t"
"v_cvt_pk_bf16_f32 v234, v236, v237\n\t"
"v_cvt_pk_bf16_f32 v235, v238, v239\n\t"
"v_mfma_f32_16x16x32_bf16 v[64:67], a[84:87], a[176:179], v[64:67]\n\t"
"s_add_u32 s29, s22, 196992\n\t"
"s_waitcnt lgkmcnt(7)\n\t"
"buffer_store_dwordx4 a[48:51], v248, s[16:19], s29 offen offset:128 nt\n\t"
"v_mfma_f32_16x16x32_bf16 v[68:71], a[76:79], a[172:175], v[68:71]\n\t"
"ds_read_b128 a[172:175], v244 offset:9728\n\t"
"v_mfma_f32_16x16x32_bf16 v[68:71], a[108:111], a[176:179], v[68:71]\n\t"
"v_mfma_f32_16x16x32_bf16 v[68:71], a[84:87], a[180:183], v[68:71]\n\t"
"buffer_load_dwordx4 a[96:99], v252, s[8:11], s20 offen offset:2048 sc1\n\t"
"ds_write_b64 v250, a[120:121] offset:18432\n\t"
"s_waitcnt lgkmcnt(5)\n\t"
"v_mfma_f32_16x16x32_bf16 v[224:227], v[224:227], a[208:211], 0\n\t"
"s_add_u32 s29, s22, 196992\n\t"
"buffer_store_dwordx4 a[56:59], v248, s[16:19], s29 offen offset:256 nt\n\t"
"v_mfma_f32_16x16x32_bf16 v[72:75], a[76:79], a[176:179], v[72:75]\n\t"
"ds_read_b128 a[176:179], v244 offset:10240\n\t"
"v_mfma_f32_16x16x32_bf16 v[72:75], a[108:111], a[180:183], v[72:75]\n\t"
"v_mfma_f32_16x16x32_bf16 v[72:75], a[84:87], a[184:187], v[72:75]\n\t"
"ds_write_b128 v249, v[216:219] offset:24576\n\t"
"s_waitcnt lgkmcnt(5)\n\t"
"v_mfma_f32_16x16x32_bf16 v[228:231], v[228:231], a[208:211], 0\n\t"
"ds_write_b64 v250, a[122:123] offset:24576\n\t"
"v_mfma_f32_16x16x32_bf16 v[76:79], a[76:79], a[180:183], v[76:79]\n\t"
"v_cvt_pk_bf16_f32 v224, v224, v225\n\t"
"v_mfma_f32_16x16x32_bf16 v[76:79], a[108:111], a[184:187], v[76:79]\n\t"
"v_cvt_pk_bf16_f32 v225, v226, v227\n\t"
"v_mfma_f32_16x16x32_bf16 v[76:79], a[84:87], a[188:191], v[76:79]\n\t"
"ds_read_b128 a[180:183], v244 offset:10752\n\t"
"v_mfma_f32_16x16x32_bf16 v[80:83], a[76:79], a[184:187], v[80:83]\n\t"
"ds_read_b128 a[184:187], v244 offset:11264\n\t"
"v_mfma_f32_16x16x32_bf16 v[80:83], a[108:111], a[188:191], v[80:83]\n\t"
"v_cvt_pk_bf16_f32 v226, v228, v229\n\t"
"v_mfma_f32_16x16x32_bf16 v[80:83], a[84:87], a[192:195], v[80:83]\n\t"
"v_cvt_pk_bf16_f32 v227, v230, v231\n\t"
"v_mfma_f32_16x16x32_bf16 v[84:87], a[76:79], a[188:191], v[84:87]\n\t"
"ds_read_b128 a[188:191], v244 offset:11776\n\t"
"v_mfma_f32_16x16x32_bf16 v[84:87], a[108:111], a[192:195], v[84:87]\n\t"
"v_mfma_f32_16x16x32_bf16 v[84:87], a[84:87], a[196:199], v[84:87]\n\t"
"v_mfma_f32_16x16x32_bf16 v[88:91], a[76:79], a[192:195], v[88:91]\n\t"
"ds_read_b128 a[192:195], v244 offset:12288\n\t"
"v_mfma_f32_16x16x32_bf16 v[88:91], a[108:111], a[196:199], v[88:91]\n\t"
"v_mfma_f32_16x16x32_bf16 v[88:91], a[84:87], a[200:203], v[88:91]\n\t"
"v_mfma_f32_16x16x32_bf16 v[92:95], a[76:79], a[196:199], v[92:95]\n\t"
"ds_read_b128 a[196:199], v244 offset:12800\n\t"
"v_mfma_f32_16x16x32_bf16 v[92:95], a[108:111], a[200:203], v[92:95]\n\t"
"ds_read_b128 a[200:203], v244 offset:13312\n\t"
"v_mfma_f32_16x16x32_bf16 v[92:95], a[84:87], a[204:207], v[92:95]\n\t"
"s_mov_b32 s20, 1118208\n\t"
"buffer_load_dwordx4 a[76:79], v252, s[8:11], s20 offen offset:0 sc1\n\t"
"s_waitcnt vmcnt(34) lgkmcnt(8)\n\t"
"v_mfma_f32_16x16x32_bf16 v[100:103], a[116:119], a[168:171], v[100:103]\n\t"
"ds_read_b128 a[204:207], v244 offset:13824\n\t"
"v_mfma_f32_16x16x32_bf16 v[100:103], a[100:103], a[172:175], v[100:103]\n\t"
"v_mfma_f32_16x16x32_bf16 v[100:103], a[124:127], a[176:179], v[100:103]\n\t"
"buffer_load_dwordx4 a[108:111], v252, s[8:11], s20 offen offset:1024 sc1\n\t"
"s_waitcnt lgkmcnt(6)\n\t"
"v_mfma_f32_16x16x32_bf16 v[104:107], a[116:119], a[172:175], v[104:107]\n\t"
"v_mfma_f32_16x16x32_bf16 v[104:107], a[100:103], a[176:179], v[104:107]\n\t"
"v_mfma_f32_16x16x32_bf16 v[104:107], a[124:127], a[180:183], v[104:107]\n\t"
"buffer_load_dwordx4 a[84:87], v252, s[8:11], s20 offen offset:2048 sc1\n\t"
"s_waitcnt lgkmcnt(0)\n\t"
"s_barrier\n\t"
"v_mfma_f32_16x16x32_bf16 v[108:111], a[116:119], a[176:179], v[108:111]\n\t"
"ds_read_b128 a[40:43], v252 offset:18432\n\t"
"v_mfma_f32_16x16x32_bf16 v[108:111], a[100:103], a[180:183], v[108:111]\n\t"
"ds_read_b64_tr_b16 v[236:237], v240 offset:40448\n\t"
"v_mfma_f32_16x16x32_bf16 v[108:111], a[124:127], a[184:187], v[108:111]\n\t"
"s_add_u32 m0, s24, 59904\n\t"
"s_add_u32 s28, s21, 1769472\n\t"
"buffer_load_dwordx4 v247, s[12:15], s28 offen offset:0 nt lds\n\t"
"s_mov_b64 exec, s[44:45]\n\t"
"ds_write_b128 v242, v[232:235] offset:4608\n\t"
"s_mov_b64 exec, -1\n\t"
"s_add_u32 m0, s24, 66048\n\t"
"s_add_u32 s28, s21, 1966080\n\t"
"buffer_load_dwordx4 v247, s[12:15], s28 offen offset:0 nt lds\n\t"
"v_mfma_f32_16x16x32_bf16 v[112:115], a[116:119], a[180:183], v[112:115]\n\t"
"ds_read_b64_tr_b16 v[238:239], v240 offset:41984\n\t"
"v_mfma_f32_16x16x32_bf16 v[112:115], a[100:103], a[184:187], v[112:115]\n\t"
"ds_read_b64_tr_b16 v[228:229], v241 offset:40448\n\t"
"v_mfma_f32_16x16x32_bf16 v[112:115], a[124:127], a[188:191], v[112:115]\n\t"
"s_mov_b64 exec, s[44:45]\n\t"
"ds_write_b128 v242, v[224:227] offset:6144\n\t"
"s_mov_b64 exec, -1\n\t"
"s_add_u32 m0, s24, 72192\n\t"
"s_add_u32 s28, s21, 2162688\n\t"
"buffer_load_dwordx4 v247, s[12:15], s28 offen offset:0 nt lds\n\t"
"ds_read_b64_tr_b16 v[230:231], v241 offset:41984\n\t"
"v_mfma_f32_16x16x32_bf16 v[116:119], a[116:119], a[184:187], v[116:119]\n\t"
"v_subrev_u32 v240, 59904, v240\n\t"
"v_subrev_u32 v241, 59904, v241\n\t"
"v_mfma_f32_16x16x32_bf16 v[116:119], a[100:103], a[188:191], v[116:119]\n\t"
"ds_read_b64_tr_b16 v[232:233], v240 offset:512\n\t"
"v_mfma_f32_16x16x32_bf16 v[116:119], a[124:127], a[192:195], v[116:119]\n\t"
"s_add_u32 m0, s24, 79872\n\t"
"s_add_u32 s28, s21, 2359296\n\t"
"buffer_load_dwordx4 v247, s[12:15], s28 offen offset:0 nt lds\n\t"
"s_add_u32 m0, s24, 86016\n\t"
"s_add_u32 s28, s21, 2555904\n\t"
"buffer_load_dwordx4 v247, s[12:15], s28 offen offset:0 nt lds\n\t"
"v_mfma_f32_16x16x32_bf16 v[120:123], a[116:119], a[188:191], v[120:123]\n\t"
"ds_read_b64_tr_b16 v[234:235], v240 offset:2048\n\t"
"v_mfma_f32_16x16x32_bf16 v[120:123], a[100:103], a[192:195], v[120:123]\n\t"
"ds_read_b64_tr_b16 v[224:225], v241 offset:512\n\t"
"v_mfma_f32_16x16x32_bf16 v[120:123], a[124:127], a[196:199], v[120:123]\n\t"
"s_add_u32 m0, s24, 92160\n\t"
"s_add_u32 s28, s21, 2752512\n\t"
"buffer_load_dwordx4 v247, s[12:15], s28 offen offset:0 nt lds\n\t"
"ds_read_b64_tr_b16 v[226:227], v241 offset:2048\n\t"
"s_waitcnt lgkmcnt(7)\n\t"
"v_mfma_f32_16x16x32_bf16 v[236:239], v[236:239], a[208:211], 0\n\t"
"ds_write_b128 v249, a[0:3] offset:30720\n\t"
"s_waitcnt lgkmcnt(5)\n\t"
"v_mfma_f32_16x16x32_bf16 v[228:231], v[228:231], a[208:211], 0\n\t"
"s_add_u32 s29, s22, 393600\n\t"
"buffer_store_dwordx4 a[40:43], v248, s[16:19], s29 offen offset:0 nt\n\t"
"v_mfma_f32_16x16x32_bf16 v[124:127], a[116:119], a[192:195], v[124:127]\n\t"
"v_mfma_f32_16x16x32_bf16 v[124:127], a[100:103], a[196:199], v[124:127]\n\t"
"v_mfma_f32_16x16x32_bf16 v[124:127], a[124:127], a[200:203], v[124:127]\n\t"
"ds_write_b64 v250, a[128:129] offset:30720\n\t"
"v_mfma_f32_16x16x32_bf16 v[128:131], a[116:119], a[196:199], v[128:131]\n\t"
"v_cvt_pk_bf16_f32 v236, v236, v237\n\t"
"v_cvt_pk_bf16_f32 v237, v238, v239\n\t"
"v_mfma_f32_16x16x32_bf16 v[128:131], a[100:103], a[200:203], v[128:131]\n\t"
"v_cvt_pk_bf16_f32 v238, v228, v229\n\t"
"v_cvt_pk_bf16_f32 v239, v230, v231\n\t"
"v_mfma_f32_16x16x32_bf16 v[128:131], a[124:127], a[204:207], v[128:131]\n\t"
"s_mov_b64 exec, s[44:45]\n\t"
"ds_write_b128 v242, v[236:239] offset:7680\n\t"
"s_mov_b64 exec, -1\n\t"
"s_waitcnt lgkmcnt(5)\n\t"
"v_mfma_f32_16x16x32_bf16 v[232:235], v[232:235], a[208:211], 0\n\t"
"ds_read_b64_tr_b16 v[228:229], v240 offset:20480\n\t"
"ds_read_b64_tr_b16 v[230:231], v240 offset:22016\n\t"
"s_waitcnt lgkmcnt(5)\n\t"
"v_mfma_f32_16x16x32_bf16 v[224:227], v[224:227], a[208:211], 0\n\t"
"ds_read_b64_tr_b16 v[236:237], v241 offset:20480\n\t"
"ds_read_b64_tr_b16 v[238:239], v241 offset:22016\n\t"
"s_waitcnt vmcnt(40)\n\t"
"v_mfma_f32_16x16x32_bf16 v[132:135], a[132:135], a[168:171], v[132:135]\n\t"
"v_mfma_f32_16x16x32_bf16 v[132:135], a[224:227], a[172:175], v[132:135]\n\t"
"v_mfma_f32_16x16x32_bf16 v[132:135], a[140:143], a[176:179], v[132:135]\n\t"
"ds_write_b128 v249, a[4:7] offset:36864\n\t"
"v_mfma_f32_16x16x32_bf16 v[136:139], a[132:135], a[172:175], v[136:139]\n\t"
"v_cvt_pk_bf16_f32 v232, v232, v233\n\t"
"v_cvt_pk_bf16_f32 v233, v234, v235\n\t"
"v_mfma_f32_16x16x32_bf16 v[136:139], a[224:227], a[176:179], v[136:139]\n\t"
"v_cvt_pk_bf16_f32 v234, v224, v225\n\t"
"v_cvt_pk_bf16_f32 v235, v226, v227\n\t"
"v_mfma_f32_16x16x32_bf16 v[136:139], a[140:143], a[180:183], v[136:139]\n\t"
"s_mov_b64 exec, s[44:45]\n\t"
"ds_write_b128 v242, v[232:235] offset:0\n\t"
"s_mov_b64 exec, -1\n\t"
"v_mfma_f32_16x16x32_bf16 v[140:143], a[132:135], a[176:179], v[140:143]\n\t"
"ds_read_b64_tr_b16 v[224:225], v240 offset:40448\n\t"
"v_mfma_f32_16x16x32_bf16 v[140:143], a[224:227], a[180:183], v[140:143]\n\t"
"ds_read_b64_tr_b16 v[226:227], v240 offset:41984\n\t"
"v_mfma_f32_16x16x32_bf16 v[140:143], a[140:143], a[184:187], v[140:143]\n\t"
"ds_read_b64_tr_b16 v[232:233], v241 offset:40448\n\t"
"v_mfma_f32_16x16x32_bf16 v[144:147], a[132:135], a[180:183], v[144:147]\n\t"
"ds_read_b64_tr_b16 v[234:235], v241 offset:41984\n\t"
"v_mfma_f32_16x16x32_bf16 v[144:147], a[224:227], a[184:187], v[144:147]\n\t"
"v_mfma_f32_16x16x32_bf16 v[144:147], a[140:143], a[188:191], v[144:147]\n\t"
"ds_write_b64 v250, a[130:131] offset:36864\n\t"
"s_waitcnt lgkmcnt(9)\n\t"
"v_mfma_f32_16x16x32_bf16 v[228:231], v[228:231], a[208:211], 0\n\t"
"s_waitcnt lgkmcnt(7)\n\t"
"v_mfma_f32_16x16x32_bf16 v[236:239], v[236:239], a[208:211], 0\n\t"
"v_mfma_f32_16x16x32_bf16 v[148:151], a[132:135], a[184:187], v[148:151]\n\t"
"v_mfma_f32_16x16x32_bf16 v[148:151], a[224:227], a[188:191], v[148:151]\n\t"
"v_mfma_f32_16x16x32_bf16 v[148:151], a[140:143], a[192:195], v[148:151]\n\t"
"v_mfma_f32_16x16x32_bf16 v[152:155], a[132:135], a[188:191], v[152:155]\n\t"
"s_nop 1\n\t"
"v_cvt_pk_bf16_f32 v228, v228, v229\n\t"
"v_cvt_pk_bf16_f32 v229, v230, v231\n\t"
"v_mfma_f32_16x16x32_bf16 v[152:155], a[224:227], a[192:195], v[152:155]\n\t"
"v_cvt_pk_bf16_f32 v230, v236, v237\n\t"
"v_mfma_f32_16x16x32_bf16 v[152:155], a[140:143], a[196:199], v[152:155]\n\t"
"v_cvt_pk_bf16_f32 v231, v238, v239\n\t"
"s_waitcnt lgkmcnt(3)\n\t"
"v_mfma_f32_16x16x32_bf16 v[224:227], v[224:227], a[208:211], 0\n\t"
"s_mov_b64 exec, s[44:45]\n\t"
"ds_write_b128 v242, v[228:231] offset:1536\n\t"
"s_mov_b64 exec, -1\n\t"
"s_waitcnt lgkmcnt(2)\n\t"
"v_mfma_f32_16x16x32_bf16 v[232:235], v[232:235], a[208:211], 0\n\t"
"v_mfma_f32_16x16x32_bf16 v[156:159], a[132:135], a[192:195], v[156:159]\n\t"
"ds_read_b128 a[116:119], v252 offset:19456\n\t"
"v_mfma_f32_16x16x32_bf16 v[156:159], a[224:227], a[196:199], v[156:159]\n\t"
"v_mfma_f32_16x16x32_bf16 v[156:159], a[140:143], a[200:203], v[156:159]\n\t"
"ds_read_b128 a[148:151], v252 offset:20480\n\t"
"v_mfma_f32_16x16x32_bf16 v[160:163], a[132:135], a[196:199], v[160:163]\n\t"
"v_cvt_pk_bf16_f32 v224, v224, v225\n\t"
"v_mfma_f32_16x16x32_bf16 v[160:163], a[224:227], a[200:203], v[160:163]\n\t"
"v_cvt_pk_bf16_f32 v225, v226, v227\n\t"
"v_mfma_f32_16x16x32_bf16 v[160:163], a[140:143], a[204:207], v[160:163]\n\t"
"v_cvt_pk_bf16_f32 v226, v232, v233\n\t"
"v_cvt_pk_bf16_f32 v227, v234, v235\n\t"
"s_waitcnt vmcnt(37)\n\t"
"v_mfma_f32_16x16x32_bf16 v[164:167], a[212:215], a[168:171], v[164:167]\n\t"
"v_mfma_f32_16x16x32_bf16 v[164:167], a[228:231], a[172:175], v[164:167]\n\t"
"v_mfma_f32_16x16x32_bf16 v[164:167], a[232:235], a[176:179], v[164:167]\n\t"
"s_mov_b64 exec, s[44:45]\n\t"
"ds_write_b128 v242, v[224:227] offset:3072\n\t"
"s_mov_b64 exec, -1\n\t"
"v_mfma_f32_16x16x32_bf16 v[168:171], a[212:215], a[172:175], v[168:171]\n\t"
"v_mfma_f32_16x16x32_bf16 v[168:171], a[228:231], a[176:179], v[168:171]\n\t"
"v_mfma_f32_16x16x32_bf16 v[168:171], a[232:235], a[180:183], v[168:171]\n\t"
"v_mfma_f32_16x16x32_bf16 v[172:175], a[212:215], a[176:179], v[172:175]\n\t"
"v_mfma_f32_16x16x32_bf16 v[172:175], a[228:231], a[180:183], v[172:175]\n\t"
"v_mfma_f32_16x16x32_bf16 v[172:175], a[232:235], a[184:187], v[172:175]\n\t"
"v_mfma_f32_16x16x32_bf16 v[176:179], a[212:215], a[180:183], v[176:179]\n\t"
"v_mfma_f32_16x16x32_bf16 v[176:179], a[228:231], a[184:187], v[176:179]\n\t"
"v_mfma_f32_16x16x32_bf16 v[176:179], a[232:235], a[188:191], v[176:179]\n\t"
"s_waitcnt lgkmcnt(0)\n\t"
"s_barrier\n\t"
"v_mfma_f32_16x16x32_bf16 v[180:183], a[212:215], a[184:187], v[180:183]\n\t"
"ds_read_b128 a[168:171], v244 offset:0\n\t"
"v_mfma_f32_16x16x32_bf16 v[180:183], a[228:231], a[188:191], v[180:183]\n\t"
"ds_read_b128 a[172:175], v244 offset:512\n\t"
"v_mfma_f32_16x16x32_bf16 v[180:183], a[232:235], a[192:195], v[180:183]\n\t"
"ds_read_b128 a[176:179], v244 offset:1024\n\t"
"ds_read_b128 a[180:183], v244 offset:1536\n\t"
"v_mfma_f32_16x16x32_bf16 v[184:187], a[212:215], a[188:191], v[184:187]\n\t"
"ds_read_b128 a[184:187], v244 offset:2048\n\t"
"v_mfma_f32_16x16x32_bf16 v[184:187], a[228:231], a[192:195], v[184:187]\n\t"
"ds_read_b128 a[188:191], v244 offset:2560\n\t"
"v_mfma_f32_16x16x32_bf16 v[184:187], a[232:235], a[196:199], v[184:187]\n\t"
"ds_write_b128 v249, a[8:11] offset:18432\n\t"
"v_mfma_f32_16x16x32_bf16 v[188:191], a[212:215], a[192:195], v[188:191]\n\t"
"ds_read_b128 a[192:195], v244 offset:3072\n\t"
"v_mfma_f32_16x16x32_bf16 v[188:191], a[228:231], a[196:199], v[188:191]\n\t"
"v_mfma_f32_16x16x32_bf16 v[188:191], a[232:235], a[200:203], v[188:191]\n\t"
"s_mov_b32 s20, 1130496\n\t"
"buffer_load_dwordx4 a[100:103], v252, s[8:11], s20 offen offset:0 sc1\n\t"
"v_mfma_f32_16x16x32_bf16 v[192:195], a[212:215], a[196:199], v[192:195]\n\t"
"ds_read_b128 a[196:199], v244 offset:3584\n\t"
"v_mfma_f32_16x16x32_bf16 v[192:195], a[228:231], a[200:203], v[192:195]\n\t"
"ds_read_b128 a[200:203], v244 offset:4096\n\t"
"v_mfma_f32_16x16x32_bf16 v[192:195], a[232:235], a[204:207], v[192:195]\n\t"
"ds_read_b128 a[204:207], v244 offset:4608\n\t"
"Lst16_%=:\n\t"
"s_waitcnt vmcnt(35) lgkmcnt(8)\n\t"
"v_mfma_f32_16x16x32_bf16 v[0:3], a[156:159], a[168:171], v[0:3]\n\t"
"v_mfma_f32_16x16x32_bf16 v[0:3], a[248:251], a[172:175], v[0:3]\n\t"
"v_mfma_f32_16x16x32_bf16 v[0:3], a[164:167], a[176:179], v[0:3]\n\t"
"buffer_load_dwordx4 a[4:7], v252, s[8:11], s20 offen offset:1024 sc1\n\t"
"s_waitcnt lgkmcnt(7)\n\t"
"v_mfma_f32_16x16x32_bf16 v[4:7], a[156:159], a[172:175], v[4:7]\n\t"
"v_mfma_f32_16x16x32_bf16 v[4:7], a[248:251], a[176:179], v[4:7]\n\t"
"v_mfma_f32_16x16x32_bf16 v[4:7], a[164:167], a[180:183], v[4:7]\n\t"
"buffer_load_dwordx4 v[216:219], v252, s[8:11], s20 offen offset:2048 sc1\n\t"
"s_waitcnt lgkmcnt(6)\n\t"
"v_mfma_f32_16x16x32_bf16 v[8:11], a[156:159], a[176:179], v[8:11]\n\t"
"v_mfma_f32_16x16x32_bf16 v[8:11], a[248:251], a[180:183], v[8:11]\n\t"
"v_mfma_f32_16x16x32_bf16 v[8:11], a[164:167], a[184:187], v[8:11]\n\t"
"ds_write_b64 v250, a[136:137] offset:18432\n\t"
"s_waitcnt lgkmcnt(6)\n\t"
"v_mfma_f32_16x16x32_bf16 v[12:15], a[156:159], a[180:183], v[12:15]\n\t"
"v_mfma_f32_16x16x32_bf16 v[12:15], a[248:251], a[184:187], v[12:15]\n\t"
"v_mfma_f32_16x16x32_bf16 v[12:15], a[164:167], a[188:191], v[12:15]\n\t"
"s_add_u32 m0, s25, 0\n\t"
"s_nop 0\n\t"
"buffer_load_dwordx4 v254, s[12:15], 0 offen offset:0 nt lds\n\t"
"s_mov_b32 s20, 1290240\n\t"
"buffer_load_dwordx4 a[212:215], v252, s[8:11], s20 offen offset:0 sc1\n\t"
"s_waitcnt lgkmcnt(4)\n\t"
"v_mfma_f32_16x16x32_bf16 v[16:19], a[156:159], a[184:187], v[16:19]\n\t"
"v_mfma_f32_16x16x32_bf16 v[16:19], a[248:251], a[188:191], v[16:19]\n\t"
"v_mfma_f32_16x16x32_bf16 v[16:19], a[164:167], a[192:195], v[16:19]\n\t"
"s_add_u32 m0, s26, 0\n\t"
"s_mov_b64 exec, s[36:37]\n\t"
"buffer_load_dwordx4 v254, s[12:15], s27 offen offset:0 nt lds\n\t"
"s_mov_b64 exec, -1\n\t"
"s_waitcnt lgkmcnt(3)\n\t"
"v_mfma_f32_16x16x32_bf16 v[20:23], a[156:159], a[188:191], v[20:23]\n\t"
"ds_read_b128 a[60:63], v252 offset:30720\n\t"
"v_mfma_f32_16x16x32_bf16 v[20:23], a[248:251], a[192:195], v[20:23]\n\t"
"ds_read_b128 a[92:95], v252 offset:31744\n\t"
"v_mfma_f32_16x16x32_bf16 v[20:23], a[164:167], a[196:199], v[20:23]\n\t"
"s_add_u32 m0, s24, 0\n\t"
"s_add_u32 s28, s21, 0\n\t"
"buffer_load_dwordx4 v247, s[12:15], s28 offen offset:0 nt lds\n\t"
"s_add_u32 m0, s24, 6144\n\t"
"s_add_u32 s28, s21, 196608\n\t"
"buffer_load_dwordx4 v247, s[12:15], s28 offen offset:0 nt lds\n\t"
"s_waitcnt lgkmcnt(4)\n\t"
"v_mfma_f32_16x16x32_bf16 v[24:27], a[156:159], a[192:195], v[24:27]\n\t"
"ds_read_b128 v[196:199], v252 offset:32768\n\t"
"v_mfma_f32_16x16x32_bf16 v[24:27], a[248:251], a[196:199], v[24:27]\n\t"
"v_mfma_f32_16x16x32_bf16 v[24:27], a[164:167], a[200:203], v[24:27]\n\t"
"ds_write_b128 v249, a[12:15] offset:24576\n\t"
"s_add_u32 m0, s24, 12288\n\t"
"s_add_u32 s28, s21, 393216\n\t"
"buffer_load_dwordx4 v247, s[12:15], s28 offen offset:0 nt lds\n\t"
"s_waitcnt lgkmcnt(5)\n\t"
"v_mfma_f32_16x16x32_bf16 v[28:31], a[156:159], a[196:199], v[28:31]\n\t"
"v_mfma_f32_16x16x32_bf16 v[28:31], a[248:251], a[200:203], v[28:31]\n\t"
"v_mfma_f32_16x16x32_bf16 v[28:31], a[164:167], a[204:207], v[28:31]\n\t"
"s_add_u32 m0, s24, 19968\n\t"
"s_add_u32 s28, s21, 589824\n\t"
"buffer_load_dwordx4 v247, s[12:15], s28 offen offset:0 nt lds\n\t"
"s_add_u32 m0, s24, 26112\n\t"
"s_add_u32 s28, s21, 786432\n\t"
"buffer_load_dwordx4 v247, s[12:15], s28 offen offset:0 nt lds\n\t"
"s_waitcnt vmcnt(42)\n\t"
"v_mfma_f32_16x16x32_bf16 v[32:35], a[252:255], a[168:171], v[32:35]\n\t"
"v_mfma_f32_16x16x32_bf16 v[32:35], v[96:99], a[172:175], v[32:35]\n\t"
"v_mfma_f32_16x16x32_bf16 v[32:35], a[216:219], a[176:179], v[32:35]\n\t"
"ds_write_b64 v250, a[138:139] offset:24576\n\t"
"v_mfma_f32_16x16x32_bf16 v[36:39], a[252:255], a[172:175], v[36:39]\n\t"
"ds_read_b64_tr_b16 v[236:237], v240 offset:1024\n\t"
"v_mfma_f32_16x16x32_bf16 v[36:39], v[96:99], a[176:179], v[36:39]\n\t"
"ds_read_b64_tr_b16 v[238:239], v240 offset:2560\n\t"
"v_mfma_f32_16x16x32_bf16 v[36:39], a[216:219], a[180:183], v[36:39]\n\t"
"v_mfma_f32_16x16x32_bf16 v[40:43], a[252:255], a[176:179], v[40:43]\n\t"
"v_mfma_f32_16x16x32_bf16 v[40:43], v[96:99], a[180:183], v[40:43]\n\t"
"v_mfma_f32_16x16x32_bf16 v[40:43], a[216:219], a[184:187], v[40:43]\n\t"
"v_mfma_f32_16x16x32_bf16 v[44:47], a[252:255], a[180:183], v[44:47]\n\t"
"v_mfma_f32_16x16x32_bf16 v[44:47], v[96:99], a[184:187], v[44:47]\n\t"
"v_mfma_f32_16x16x32_bf16 v[44:47], a[216:219], a[188:191], v[44:47]\n\t"
"s_waitcnt lgkmcnt(2)\n\t"
"s_barrier\n\t"
"v_mfma_f32_16x16x32_bf16 v[48:51], a[252:255], a[184:187], v[48:51]\n\t"
"ds_read_b64_tr_b16 v[228:229], v241 offset:1024\n\t"
"v_mfma_f32_16x16x32_bf16 v[48:51], v[96:99], a[188:191], v[48:51]\n\t"
"ds_read_b64_tr_b16 v[230:231], v241 offset:2560\n\t"
"v_mfma_f32_16x16x32_bf16 v[48:51], a[216:219], a[192:195], v[48:51]\n\t"
"ds_write_b128 v249, a[16:19] offset:30720\n\t"
"v_mfma_f32_16x16x32_bf16 v[52:55], a[252:255], a[188:191], v[52:55]\n\t"
"v_mfma_f32_16x16x32_bf16 v[52:55], v[96:99], a[192:195], v[52:55]\n\t"
"ds_read_b64_tr_b16 v[232:233], v240 offset:20992\n\t"
"v_mfma_f32_16x16x32_bf16 v[52:55], a[216:219], a[196:199], v[52:55]\n\t"
"ds_read_b64_tr_b16 v[234:235], v240 offset:22528\n\t"
"s_waitcnt lgkmcnt(5)\n\t"
"v_mfma_f32_16x16x32_bf16 v[236:239], v[236:239], a[208:211], 0\n\t"
"v_mfma_f32_16x16x32_bf16 v[56:59], a[252:255], a[192:195], v[56:59]\n\t"
"v_mfma_f32_16x16x32_bf16 v[56:59], v[96:99], a[196:199], v[56:59]\n\t"
"ds_read_b64_tr_b16 v[224:225], v241 offset:20992\n\t"
"v_mfma_f32_16x16x32_bf16 v[56:59], a[216:219], a[200:203], v[56:59]\n\t"
"ds_read_b64_tr_b16 v[226:227], v241 offset:22528\n\t"
"s_waitcnt lgkmcnt(5)\n\t"
"v_mfma_f32_16x16x32_bf16 v[228:231], v[228:231], a[208:211], 0\n\t"
"ds_write_b64 v250, a[144:145] offset:30720\n\t"
"v_mfma_f32_16x16x32_bf16 v[60:63], a[252:255], a[196:199], v[60:63]\n\t"
"v_cvt_pk_bf16_f32 v236, v236, v237\n\t"
"v_mfma_f32_16x16x32_bf16 v[60:63], v[96:99], a[200:203], v[60:63]\n\t"
"v_cvt_pk_bf16_f32 v237, v238, v239\n\t"
"v_mfma_f32_16x16x32_bf16 v[60:63], a[216:219], a[204:207], v[60:63]\n\t"
"s_waitcnt vmcnt(39)\n\t"
"v_mfma_f32_16x16x32_bf16 v[64:67], a[236:239], a[168:171], v[64:67]\n\t"
"ds_read_b128 a[168:171], v244 offset:9216\n\t"
"v_mfma_f32_16x16x32_bf16 v[64:67], a[240:243], a[172:175], v[64:67]\n\t"
"v_cvt_pk_bf16_f32 v238, v228, v229\n\t"
"v_cvt_pk_bf16_f32 v239, v230, v231\n\t"
"v_mfma_f32_16x16x32_bf16 v[64:67], a[220:223], a[176:179], v[64:67]\n\t"
"v_mfma_f32_16x16x32_bf16 v[68:71], a[236:239], a[172:175], v[68:71]\n\t"
"ds_read_b128 a[172:175], v244 offset:9728\n\t"
"v_mfma_f32_16x16x32_bf16 v[68:71], a[240:243], a[176:179], v[68:71]\n\t"
"v_mfma_f32_16x16x32_bf16 v[68:71], a[220:223], a[180:183], v[68:71]\n\t"
"ds_write_b128 v249, a[20:23] offset:36864\n\t"
"s_waitcnt lgkmcnt(6)\n\t"
"v_mfma_f32_16x16x32_bf16 v[232:235], v[232:235], a[208:211], 0\n\t"
"v_mfma_f32_16x16x32_bf16 v[72:75], a[236:239], a[176:179], v[72:75]\n\t"
"ds_read_b128 a[176:179], v244 offset:10240\n\t"
"v_mfma_f32_16x16x32_bf16 v[72:75], a[240:243], a[180:183], v[72:75]\n\t"
"v_mfma_f32_16x16x32_bf16 v[72:75], a[220:223], a[184:187], v[72:75]\n\t"
"s_waitcnt lgkmcnt(5)\n\t"
"v_mfma_f32_16x16x32_bf16 v[224:227], v[224:227], a[208:211], 0\n\t"
"ds_read_b128 v[204:207], v252 offset:18432\n\t"
"v_mfma_f32_16x16x32_bf16 v[76:79], a[236:239], a[180:183], v[76:79]\n\t"
"v_cvt_pk_bf16_f32 v232, v232, v233\n\t"
"v_cvt_pk_bf16_f32 v233, v234, v235\n\t"
"v_mfma_f32_16x16x32_bf16 v[76:79], a[240:243], a[184:187], v[76:79]\n\t"
"ds_read_b128 a[180:183], v244 offset:10752\n\t"
"v_mfma_f32_16x16x32_bf16 v[76:79], a[220:223], a[188:191], v[76:79]\n\t"
"ds_read_b128 v[212:215], v252 offset:19456\n\t"
"v_mfma_f32_16x16x32_bf16 v[80:83], a[236:239], a[184:187], v[80:83]\n\t"
"ds_read_b128 a[184:187], v244 offset:11264\n\t"
"v_mfma_f32_16x16x32_bf16 v[80:83], a[240:243], a[188:191], v[80:83]\n\t"
"v_cvt_pk_bf16_f32 v234, v224, v225\n\t"
"v_cvt_pk_bf16_f32 v235, v226, v227\n\t"
"v_mfma_f32_16x16x32_bf16 v[80:83], a[220:223], a[192:195], v[80:83]\n\t"
"ds_read_b128 a[0:3], v252 offset:20480\n\t"
"v_mfma_f32_16x16x32_bf16 v[84:87], a[236:239], a[188:191], v[84:87]\n\t"
"ds_read_b128 a[188:191], v244 offset:11776\n\t"
"v_mfma_f32_16x16x32_bf16 v[84:87], a[240:243], a[192:195], v[84:87]\n\t"
"v_mfma_f32_16x16x32_bf16 v[84:87], a[220:223], a[196:199], v[84:87]\n\t"
"ds_write_b64 v250, a[146:147] offset:36864\n\t"
"v_mfma_f32_16x16x32_bf16 v[88:91], a[236:239], a[192:195], v[88:91]\n\t"
"ds_read_b128 a[192:195], v244 offset:12288\n\t"
"v_mfma_f32_16x16x32_bf16 v[88:91], a[240:243], a[196:199], v[88:91]\n\t"
"v_mfma_f32_16x16x32_bf16 v[88:91], a[220:223], a[200:203], v[88:91]\n\t"
"v_mfma_f32_16x16x32_bf16 v[92:95], a[236:239], a[196:199], v[92:95]\n\t"
"ds_read_b128 a[196:199], v244 offset:12800\n\t"
"v_mfma_f32_16x16x32_bf16 v[92:95], a[240:243], a[200:203], v[92:95]\n\t"
"ds_read_b128 a[200:203], v244 offset:13312\n\t"
"v_mfma_f32_16x16x32_bf16 v[92:95], a[220:223], a[204:207], v[92:95]\n\t"
"ds_read_b128 a[204:207], v244 offset:13824\n\t"
"s_waitcnt vmcnt(36) lgkmcnt(11)\n\t"
"v_mfma_f32_16x16x32_bf16 v[100:103], a[244:247], a[168:171], v[100:103]\n\t"
"v_mfma_f32_16x16x32_bf16 v[100:103], a[44:47], a[172:175], v[100:103]\n\t"
"v_mfma_f32_16x16x32_bf16 v[100:103], a[104:107], a[176:179], v[100:103]\n\t"
"s_waitcnt lgkmcnt(9)\n\t"
"v_mfma_f32_16x16x32_bf16 v[104:107], a[244:247], a[172:175], v[104:107]\n\t"
"v_mfma_f32_16x16x32_bf16 v[104:107], a[44:47], a[176:179], v[104:107]\n\t"
"v_mfma_f32_16x16x32_bf16 v[104:107], a[104:107], a[180:183], v[104:107]\n\t"
"buffer_load_dwordx4 v[96:99], v252, s[8:11], s20 offen offset:1024 sc1\n\t"
"s_waitcnt lgkmcnt(0)\n\t"
"s_barrier\n\t"
"v_mfma_f32_16x16x32_bf16 v[108:111], a[244:247], a[176:179], v[108:111]\n\t"
"ds_read_b64_tr_b16 v[228:229], v240 offset:40960\n\t"
"v_mfma_f32_16x16x32_bf16 v[108:111], a[44:47], a[180:183], v[108:111]\n\t"
"ds_read_b64_tr_b16 v[230:231], v240 offset:42496\n\t"
"v_mfma_f32_16x16x32_bf16 v[108:111], a[104:107], a[184:187], v[108:111]\n\t"
"s_mov_b64 exec, s[44:45]\n\t"
"ds_write_b128 v242, v[236:239] offset:0\n\t"
"s_mov_b64 exec, -1\n\t"
"v_mfma_f32_16x16x32_bf16 v[112:115], a[244:247], a[180:183], v[112:115]\n\t"
"ds_read_b64_tr_b16 v[224:225], v241 offset:40960\n\t"
"v_mfma_f32_16x16x32_bf16 v[112:115], a[44:47], a[184:187], v[112:115]\n\t"
"ds_read_b64_tr_b16 v[226:227], v241 offset:42496\n\t"
"v_add_u32 v240, 59904, v240\n\t"
"v_mfma_f32_16x16x32_bf16 v[112:115], a[104:107], a[188:191], v[112:115]\n\t"
"s_mov_b64 exec, s[44:45]\n\t"
"ds_write_b128 v242, v[232:235] offset:1536\n\t"
"s_mov_b64 exec, -1\n\t"
"v_mfma_f32_16x16x32_bf16 v[116:119], a[244:247], a[184:187], v[116:119]\n\t"
"v_add_u32 v241, 59904, v241\n\t"
"ds_read_b64_tr_b16 v[236:237], v240 offset:1024\n\t"
"v_mfma_f32_16x16x32_bf16 v[116:119], a[44:47], a[188:191], v[116:119]\n\t"
"ds_read_b64_tr_b16 v[238:239], v240 offset:2560\n\t"
"v_mfma_f32_16x16x32_bf16 v[116:119], a[104:107], a[192:195], v[116:119]\n\t"
"ds_read_b64_tr_b16 v[232:233], v241 offset:1024\n\t"
"ds_read_b64_tr_b16 v[234:235], v241 offset:2560\n\t"
"v_mfma_f32_16x16x32_bf16 v[120:123], a[244:247], a[188:191], v[120:123]\n\t"
"v_mfma_f32_16x16x32_bf16 v[120:123], a[44:47], a[192:195], v[120:123]\n\t"
"v_mfma_f32_16x16x32_bf16 v[120:123], a[104:107], a[196:199], v[120:123]\n\t"
"ds_write_b128 v249, a[24:27] offset:18432\n\t"
"s_waitcnt lgkmcnt(9)\n\t"
"v_mfma_f32_16x16x32_bf16 v[228:231], v[228:231], a[208:211], 0\n\t"
"ds_write_b64 v250, a[152:153] offset:18432\n\t"
"s_waitcnt lgkmcnt(7)\n\t"
"v_mfma_f32_16x16x32_bf16 v[224:227], v[224:227], a[208:211], 0\n\t"
"ds_write_b128 v249, a[28:31] offset:24576\n\t"
"v_mfma_f32_16x16x32_bf16 v[124:127], a[244:247], a[192:195], v[124:127]\n\t"
"v_mfma_f32_16x16x32_bf16 v[124:127], a[44:47], a[196:199], v[124:127]\n\t"
"v_mfma_f32_16x16x32_bf16 v[124:127], a[104:107], a[200:203], v[124:127]\n\t"
"s_add_u32 m0, s24, 32256\n\t"
"s_add_u32 s28, s21, 983040\n\t"
"buffer_load_dwordx4 v247, s[12:15], s28 offen offset:0 nt lds\n\t"
"s_add_u32 m0, s24, 39936\n\t"
"s_add_u32 s28, s21, 1179648\n\t"
"buffer_load_dwordx4 v247, s[12:15], s28 offen offset:0 nt lds\n\t"
"v_mfma_f32_16x16x32_bf16 v[128:131], a[244:247], a[196:199], v[128:131]\n\t"
"v_cvt_pk_bf16_f32 v228, v228, v229\n\t"
"v_mfma_f32_16x16x32_bf16 v[128:131], a[44:47], a[200:203], v[128:131]\n\t"
"v_cvt_pk_bf16_f32 v229, v230, v231\n\t"
"v_cvt_pk_bf16_f32 v230, v224, v225\n\t"
"v_mfma_f32_16x16x32_bf16 v[128:131], a[104:107], a[204:207], v[128:131]\n\t"
"s_add_u32 m0, s24, 46080\n\t"
"s_add_u32 s28, s21, 1376256\n\t"
"buffer_load_dwordx4 v247, s[12:15], s28 offen offset:0 nt lds\n\t"
"s_waitcnt lgkmcnt(5)\n\t"
"v_mfma_f32_16x16x32_bf16 v[236:239], v[236:239], a[208:211], 0\n\t"
"s_add_u32 m0, s24, 52224\n\t"
"s_add_u32 s28, s21, 1572864\n\t"
"buffer_load_dwordx4 v247, s[12:15], s28 offen offset:0 nt lds\n\t"
"s_waitcnt lgkmcnt(3)\n\t"
"v_mfma_f32_16x16x32_bf16 v[232:235], v[232:235], a[208:211], 0\n\t"
"v_cvt_pk_bf16_f32 v231, v226, v227\n\t"
"ds_read_b64_tr_b16 v[224:225], v240 offset:20992\n\t"
"s_waitcnt vmcnt(38)\n\t"
"v_mfma_f32_16x16x32_bf16 v[132:135], a[64:67], a[168:171], v[132:135]\n\t"
"ds_read_b64_tr_b16 v[226:227], v240 offset:22528\n\t"
"v_mfma_f32_16x16x32_bf16 v[132:135], a[52:55], a[172:175], v[132:135]\n\t"
"v_mfma_f32_16x16x32_bf16 v[132:135], v[200:203], a[176:179], v[132:135]\n\t"
"s_add_u32 m0, s24, 99840\n\t"
"s_add_u32 s28, s21, 2949120\n\t"
"buffer_load_dwordx4 v247, s[12:15], s28 offen offset:0 nt lds\n\t"
"s_add_u32 m0, s24, 105984\n\t"
"s_add_u32 s28, s21, 3145728\n\t"
"buffer_load_dwordx4 v247, s[12:15], s28 offen offset:0 nt lds\n\t"
"v_mfma_f32_16x16x32_bf16 v[136:139], a[64:67], a[172:175], v[136:139]\n\t"
"v_cvt_pk_bf16_f32 v236, v236, v237\n\t"
"v_cvt_pk_bf16_f32 v237, v238, v239\n\t"
"v_mfma_f32_16x16x32_bf16 v[136:139], a[52:55], a[176:179], v[136:139]\n\t"
"v_cvt_pk_bf16_f32 v238, v232, v233\n\t"
"v_cvt_pk_bf16_f32 v239, v234, v235\n\t"
"v_mfma_f32_16x16x32_bf16 v[136:139], v[200:203], a[180:183], v[136:139]\n\t"
"s_mov_b64 exec, s[44:45]\n\t"
"ds_write_b128 v242, v[228:231] offset:3072\n\t"
"s_mov_b64 exec, -1\n\t"
"s_add_u32 m0, s24, 112128\n\t"
"s_add_u32 s28, s21, 3342336\n\t"
"buffer_load_dwordx4 v247, s[12:15], s28 offen offset:0 nt lds\n\t"
"ds_read_b64_tr_b16 v[228:229], v241 offset:20992\n\t"
"ds_read_b64_tr_b16 v[230:231], v241 offset:22528\n\t"
"v_mfma_f32_16x16x32_bf16 v[140:143], a[64:67], a[176:179], v[140:143]\n\t"
"ds_read_b64_tr_b16 v[232:233], v240 offset:40960\n\t"
"v_mfma_f32_16x16x32_bf16 v[140:143], a[52:55], a[180:183], v[140:143]\n\t"
"ds_read_b64_tr_b16 v[234:235], v240 offset:42496\n\t"
"v_mfma_f32_16x16x32_bf16 v[140:143], v[200:203], a[184:187], v[140:143]\n\t"
"s_mov_b64 exec, s[44:45]\n\t"
"ds_write_b128 v242, v[236:239] offset:4608\n\t"
"s_mov_b64 exec, -1\n\t"
"v_mfma_f32_16x16x32_bf16 v[144:147], a[64:67], a[180:183], v[144:147]\n\t"
"ds_read_b64_tr_b16 v[236:237], v241 offset:40960\n\t"
"v_mfma_f32_16x16x32_bf16 v[144:147], a[52:55], a[184:187], v[144:147]\n\t"
"ds_read_b64_tr_b16 v[238:239], v241 offset:42496\n\t"
"v_mfma_f32_16x16x32_bf16 v[144:147], v[200:203], a[188:191], v[144:147]\n\t"
"ds_write_b64 v250, a[154:155] offset:24576\n\t"
"s_waitcnt lgkmcnt(9)\n\t"
"v_mfma_f32_16x16x32_bf16 v[224:227], v[224:227], a[208:211], 0\n\t"
"s_waitcnt lgkmcnt(6)\n\t"
"v_mfma_f32_16x16x32_bf16 v[228:231], v[228:231], a[208:211], 0\n\t"
"v_mfma_f32_16x16x32_bf16 v[148:151], a[64:67], a[184:187], v[148:151]\n\t"
"v_mfma_f32_16x16x32_bf16 v[148:151], a[52:55], a[188:191], v[148:151]\n\t"
"v_mfma_f32_16x16x32_bf16 v[148:151], v[200:203], a[192:195], v[148:151]\n\t"
"v_mfma_f32_16x16x32_bf16 v[152:155], a[64:67], a[188:191], v[152:155]\n\t"
"s_nop 1\n\t"
"v_cvt_pk_bf16_f32 v224, v224, v225\n\t"
"v_cvt_pk_bf16_f32 v225, v226, v227\n\t"
"v_mfma_f32_16x16x32_bf16 v[152:155], a[52:55], a[192:195], v[152:155]\n\t"
"v_cvt_pk_bf16_f32 v226, v228, v229\n\t"
"v_mfma_f32_16x16x32_bf16 v[152:155], v[200:203], a[196:199], v[152:155]\n\t"
"v_cvt_pk_bf16_f32 v227, v230, v231\n\t"
"s_waitcnt lgkmcnt(4)\n\t"
"v_mfma_f32_16x16x32_bf16 v[232:235], v[232:235], a[208:211], 0\n\t"
"s_mov_b64 exec, s[44:45]\n\t"
"ds_write_b128 v242, v[224:227] offset:6144\n\t"
"s_mov_b64 exec, -1\n\t"
"s_waitcnt lgkmcnt(2)\n\t"
"v_mfma_f32_16x16x32_bf16 v[236:239], v[236:239], a[208:211], 0\n\t"
"ds_read_b128 a[8:11], v252 offset:30720\n\t"
"v_mfma_f32_16x16x32_bf16 v[156:159], a[64:67], a[192:195], v[156:159]\n\t"
"v_mfma_f32_16x16x32_bf16 v[156:159], a[52:55], a[196:199], v[156:159]\n\t"
"ds_read_b128 a[16:19], v252 offset:31744\n\t"
"v_mfma_f32_16x16x32_bf16 v[156:159], v[200:203], a[200:203], v[156:159]\n\t"
"v_mfma_f32_16x16x32_bf16 v[160:163], a[64:67], a[196:199], v[160:163]\n\t"
"v_cvt_pk_bf16_f32 v232, v232, v233\n\t"
"v_cvt_pk_bf16_f32 v233, v234, v235\n\t"
"v_mfma_f32_16x16x32_bf16 v[160:163], a[52:55], a[200:203], v[160:163]\n\t"
"v_cvt_pk_bf16_f32 v234, v236, v237\n\t"
"v_mfma_f32_16x16x32_bf16 v[160:163], v[200:203], a[204:207], v[160:163]\n\t"
"v_cvt_pk_bf16_f32 v235, v238, v239\n\t"
"ds_read_b128 a[104:107], v252 offset:32768\n\t"
"s_waitcnt vmcnt(35)\n\t"
"v_mfma_f32_16x16x32_bf16 v[164:167], a[72:75], a[168:171], v[164:167]\n\t"
"v_mfma_f32_16x16x32_bf16 v[164:167], a[80:83], a[172:175], v[164:167]\n\t"
"v_mfma_f32_16x16x32_bf16 v[164:167], a[68:71], a[176:179], v[164:167]\n\t"
"s_mov_b64 exec, s[44:45]\n\t"
"ds_write_b128 v242, v[232:235] offset:7680\n\t"
"s_mov_b64 exec, -1\n\t"
"v_mfma_f32_16x16x32_bf16 v[168:171], a[72:75], a[172:175], v[168:171]\n\t"
"v_mfma_f32_16x16x32_bf16 v[168:171], a[80:83], a[176:179], v[168:171]\n\t"
"v_mfma_f32_16x16x32_bf16 v[168:171], a[68:71], a[180:183], v[168:171]\n\t"
"v_mfma_f32_16x16x32_bf16 v[172:175], a[72:75], a[176:179], v[172:175]\n\t"
"v_mfma_f32_16x16x32_bf16 v[172:175], a[80:83], a[180:183], v[172:175]\n\t"
"v_mfma_f32_16x16x32_bf16 v[172:175], a[68:71], a[184:187], v[172:175]\n\t"
"v_mfma_f32_16x16x32_bf16 v[176:179], a[72:75], a[180:183], v[176:179]\n\t"
"v_mfma_f32_16x16x32_bf16 v[176:179], a[80:83], a[184:187], v[176:179]\n\t"
"v_mfma_f32_16x16x32_bf16 v[176:179], a[68:71], a[188:191], v[176:179]\n\t"
"s_waitcnt lgkmcnt(0)\n\t"
"s_barrier\n\t"
"v_mfma_f32_16x16x32_bf16 v[180:183], a[72:75], a[184:187], v[180:183]\n\t"
"ds_read_b128 a[168:171], v244 offset:0\n\t"
"v_mfma_f32_16x16x32_bf16 v[180:183], a[80:83], a[188:191], v[180:183]\n\t"
"ds_read_b128 a[172:175], v244 offset:512\n\t"
"v_mfma_f32_16x16x32_bf16 v[180:183], a[68:71], a[192:195], v[180:183]\n\t"
"ds_read_b128 a[176:179], v244 offset:1024\n\t"
"ds_read_b128 a[180:183], v244 offset:1536\n\t"
"v_mfma_f32_16x16x32_bf16 v[184:187], a[72:75], a[188:191], v[184:187]\n\t"
"ds_read_b128 a[184:187], v244 offset:2048\n\t"
"v_mfma_f32_16x16x32_bf16 v[184:187], a[80:83], a[192:195], v[184:187]\n\t"
"ds_read_b128 a[188:191], v244 offset:2560\n\t"
"v_mfma_f32_16x16x32_bf16 v[184:187], a[68:71], a[196:199], v[184:187]\n\t"
"ds_write_b128 v249, a[32:35] offset:30720\n\t"
"v_mfma_f32_16x16x32_bf16 v[188:191], a[72:75], a[192:195], v[188:191]\n\t"
"ds_read_b128 a[192:195], v244 offset:3072\n\t"
"v_mfma_f32_16x16x32_bf16 v[188:191], a[80:83], a[196:199], v[188:191]\n\t"
"v_mfma_f32_16x16x32_bf16 v[188:191], a[68:71], a[200:203], v[188:191]\n\t"
"ds_write_b64 v250, a[160:161] offset:30720\n\t"
"v_mfma_f32_16x16x32_bf16 v[192:195], a[72:75], a[196:199], v[192:195]\n\t"
"ds_read_b128 a[196:199], v244 offset:3584\n\t"
"v_mfma_f32_16x16x32_bf16 v[192:195], a[80:83], a[200:203], v[192:195]\n\t"
"ds_read_b128 a[200:203], v244 offset:4096\n\t"
"v_mfma_f32_16x16x32_bf16 v[192:195], a[68:71], a[204:207], v[192:195]\n\t"
"ds_read_b128 a[204:207], v244 offset:4608\n\t"
"Lst17_%=:\n\t"
"s_waitcnt vmcnt(30) lgkmcnt(9)\n\t"
"v_mfma_f32_16x16x32_bf16 v[0:3], v[208:211], a[168:171], v[0:3]\n\t"
"v_mfma_f32_16x16x32_bf16 v[0:3], a[88:91], a[172:175], v[0:3]\n\t"
"v_mfma_f32_16x16x32_bf16 v[0:3], a[96:99], a[176:179], v[0:3]\n\t"
"ds_write_b128 v249, a[36:39] offset:36864\n\t"
"s_waitcnt lgkmcnt(9)\n\t"
"v_mfma_f32_16x16x32_bf16 v[4:7], v[208:211], a[172:175], v[4:7]\n\t"
"v_mfma_f32_16x16x32_bf16 v[4:7], a[88:91], a[176:179], v[4:7]\n\t"
"v_mfma_f32_16x16x32_bf16 v[4:7], a[96:99], a[180:183], v[4:7]\n\t"
"s_waitcnt lgkmcnt(8)\n\t"
"v_mfma_f32_16x16x32_bf16 v[8:11], v[208:211], a[176:179], v[8:11]\n\t"
"s_nop 0\n\t"
"v_cvt_pk_bf16_f32 v0, v0, v1\n\t"
"v_cvt_pk_bf16_f32 v2, v2, v3\n\t"
"v_mfma_f32_16x16x32_bf16 v[8:11], a[88:91], a[180:183], v[8:11]\n\t"
"v_mfma_f32_16x16x32_bf16 v[8:11], a[96:99], a[184:187], v[8:11]\n\t"
"v_accvgpr_write_b32 a40, v0\n\t"
"s_waitcnt lgkmcnt(7)\n\t"
"v_mfma_f32_16x16x32_bf16 v[12:15], v[208:211], a[180:183], v[12:15]\n\t"
"v_mfma_f32_16x16x32_bf16 v[12:15], a[88:91], a[184:187], v[12:15]\n\t"
"v_cvt_pk_bf16_f32 v4, v4, v5\n\t"
"v_mfma_f32_16x16x32_bf16 v[12:15], a[96:99], a[188:191], v[12:15]\n\t"
"s_waitcnt lgkmcnt(5)\n\t"
"v_mfma_f32_16x16x32_bf16 v[16:19], v[208:211], a[184:187], v[16:19]\n\t"
"v_cvt_pk_bf16_f32 v6, v6, v7\n\t"
"v_mfma_f32_16x16x32_bf16 v[16:19], a[88:91], a[188:191], v[16:19]\n\t"
"v_accvgpr_write_b32 a41, v2\n\t"
"v_mfma_f32_16x16x32_bf16 v[16:19], a[96:99], a[192:195], v[16:19]\n\t"
"s_waitcnt lgkmcnt(3)\n\t"
"v_mfma_f32_16x16x32_bf16 v[20:23], v[208:211], a[188:191], v[20:23]\n\t"
"ds_read_b128 a[24:27], v252 offset:18432\n\t"
"v_cvt_pk_bf16_f32 v8, v8, v9\n\t"
"v_mfma_f32_16x16x32_bf16 v[20:23], a[88:91], a[192:195], v[20:23]\n\t"
"ds_read_b128 a[112:115], v252 offset:19456\n\t"
"v_mfma_f32_16x16x32_bf16 v[20:23], a[96:99], a[196:199], v[20:23]\n\t"
"v_cvt_pk_bf16_f32 v10, v10, v11\n\t"
"s_waitcnt lgkmcnt(4)\n\t"
"v_mfma_f32_16x16x32_bf16 v[24:27], v[208:211], a[192:195], v[24:27]\n\t"
"ds_read_b128 a[120:123], v252 offset:20480\n\t"
"v_mfma_f32_16x16x32_bf16 v[24:27], a[88:91], a[196:199], v[24:27]\n\t"
"v_accvgpr_write_b32 a48, v4\n\t"
"v_accvgpr_write_b32 a49, v6\n\t"
"v_mfma_f32_16x16x32_bf16 v[24:27], a[96:99], a[200:203], v[24:27]\n\t"
"v_cvt_pk_bf16_f32 v12, v12, v13\n\t"
"s_waitcnt lgkmcnt(4)\n\t"
"v_mfma_f32_16x16x32_bf16 v[28:31], v[208:211], a[196:199], v[28:31]\n\t"
"v_cvt_pk_bf16_f32 v14, v14, v15\n\t"
"v_accvgpr_write_b32 a56, v8\n\t"
"v_mfma_f32_16x16x32_bf16 v[28:31], a[88:91], a[200:203], v[28:31]\n\t"
"v_accvgpr_write_b32 a57, v10\n\t"
"v_mfma_f32_16x16x32_bf16 v[28:31], a[96:99], a[204:207], v[28:31]\n\t"
"ds_write_b64 v250, a[162:163] offset:36864\n\t"
"s_waitcnt vmcnt(26)\n\t"
"v_mfma_f32_16x16x32_bf16 v[32:35], a[76:79], a[168:171], v[32:35]\n\t"
"v_cvt_pk_bf16_f32 v16, v16, v17\n\t"
"v_cvt_pk_bf16_f32 v18, v18, v19\n\t"
"v_mfma_f32_16x16x32_bf16 v[32:35], a[108:111], a[172:175], v[32:35]\n\t"
"v_accvgpr_write_b32 a64, v12\n\t"
"v_accvgpr_write_b32 a65, v14\n\t"
"v_mfma_f32_16x16x32_bf16 v[32:35], a[84:87], a[176:179], v[32:35]\n\t"
"buffer_load_dwordx4 a[144:147], v252, s[8:11], s20 offen offset:2048 sc1\n\t"
"v_mfma_f32_16x16x32_bf16 v[36:39], a[76:79], a[172:175], v[36:39]\n\t"
"v_cvt_pk_bf16_f32 v20, v20, v21\n\t"
"v_cvt_pk_bf16_f32 v22, v22, v23\n\t"
"v_mfma_f32_16x16x32_bf16 v[36:39], a[108:111], a[176:179], v[36:39]\n\t"
"v_accvgpr_write_b32 a72, v16\n\t"
"v_accvgpr_write_b32 a73, v18\n\t"
"v_mfma_f32_16x16x32_bf16 v[36:39], a[84:87], a[180:183], v[36:39]\n\t"
"v_cvt_pk_bf16_f32 v24, v24, v25\n\t"
"v_mfma_f32_16x16x32_bf16 v[40:43], a[76:79], a[176:179], v[40:43]\n\t"
"v_cvt_pk_bf16_f32 v26, v26, v27\n\t"
"v_mfma_f32_16x16x32_bf16 v[40:43], a[108:111], a[180:183], v[40:43]\n\t"
"v_accvgpr_write_b32 a80, v20\n\t"
"v_accvgpr_write_b32 a81, v22\n\t"
"v_mfma_f32_16x16x32_bf16 v[40:43], a[84:87], a[184:187], v[40:43]\n\t"
"v_cvt_pk_bf16_f32 v28, v28, v29\n\t"
"v_mfma_f32_16x16x32_bf16 v[44:47], a[76:79], a[180:183], v[44:47]\n\t"
"v_cvt_pk_bf16_f32 v30, v30, v31\n\t"
"v_mfma_f32_16x16x32_bf16 v[44:47], a[108:111], a[184:187], v[44:47]\n\t"
"v_accvgpr_write_b32 a88, v24\n\t"
"v_accvgpr_write_b32 a89, v26\n\t"
"v_mfma_f32_16x16x32_bf16 v[44:47], a[84:87], a[188:191], v[44:47]\n\t"
"v_cvt_pk_bf16_f32 v32, v32, v33\n\t"
"s_waitcnt lgkmcnt(0)\n\t"
"s_waitcnt vmcnt(14)\n\t"
"s_barrier\n\t"
"v_mfma_f32_16x16x32_bf16 v[48:51], a[76:79], a[184:187], v[48:51]\n\t"
"ds_read_b64_tr_b16 v[228:229], v240 offset:0\n\t"
"v_cvt_pk_bf16_f32 v34, v34, v35\n\t"
"v_mfma_f32_16x16x32_bf16 v[48:51], a[108:111], a[188:191], v[48:51]\n\t"
"ds_read_b64_tr_b16 v[230:231], v240 offset:1536\n\t"
"v_accvgpr_write_b32 a96, v28\n\t"
"v_mfma_f32_16x16x32_bf16 v[48:51], a[84:87], a[192:195], v[48:51]\n\t"
"ds_read_b64_tr_b16 v[224:225], v241 offset:0\n\t"
"ds_read_b64_tr_b16 v[226:227], v241 offset:1536\n\t"
"v_mfma_f32_16x16x32_bf16 v[52:55], a[76:79], a[188:191], v[52:55]\n\t"
"v_accvgpr_write_b32 a97, v30\n\t"
"v_cvt_pk_bf16_f32 v36, v36, v37\n\t"
"v_mfma_f32_16x16x32_bf16 v[52:55], a[108:111], a[192:195], v[52:55]\n\t"
"v_cvt_pk_bf16_f32 v38, v38, v39\n\t"
"v_accvgpr_write_b32 a42, v32\n\t"
"v_mfma_f32_16x16x32_bf16 v[52:55], a[84:87], a[196:199], v[52:55]\n\t"
"v_accvgpr_write_b32 a43, v34\n\t"
"v_cvt_pk_bf16_f32 v40, v40, v41\n\t"
"v_mfma_f32_16x16x32_bf16 v[56:59], a[76:79], a[192:195], v[56:59]\n\t"
"v_cvt_pk_bf16_f32 v42, v42, v43\n\t"
"v_mfma_f32_16x16x32_bf16 v[56:59], a[108:111], a[196:199], v[56:59]\n\t"
"ds_read_b64_tr_b16 v[236:237], v240 offset:19968\n\t"
"v_accvgpr_write_b32 a50, v36\n\t"
"v_mfma_f32_16x16x32_bf16 v[56:59], a[84:87], a[200:203], v[56:59]\n\t"
"ds_read_b64_tr_b16 v[238:239], v240 offset:21504\n\t"
"s_waitcnt lgkmcnt(4)\n\t"
"v_mfma_f32_16x16x32_bf16 v[228:231], v[228:231], a[208:211], 0\n\t"
"v_accvgpr_write_b32 a51, v38\n\t"
"v_cvt_pk_bf16_f32 v44, v44, v45\n\t"
"v_mfma_f32_16x16x32_bf16 v[60:63], a[76:79], a[196:199], v[60:63]\n\t"
"v_cvt_pk_bf16_f32 v46, v46, v47\n\t"
"v_mfma_f32_16x16x32_bf16 v[60:63], a[108:111], a[200:203], v[60:63]\n\t"
"ds_read_b64_tr_b16 v[232:233], v241 offset:19968\n\t"
"v_accvgpr_write_b32 a58, v40\n\t"
"v_mfma_f32_16x16x32_bf16 v[60:63], a[84:87], a[204:207], v[60:63]\n\t"
"ds_read_b64_tr_b16 v[234:235], v241 offset:21504\n\t"
"v_accvgpr_write_b32 a59, v42\n\t"
"s_waitcnt lgkmcnt(4)\n\t"
"v_mfma_f32_16x16x32_bf16 v[224:227], v[224:227], a[208:211], 0\n\t"
"v_cvt_pk_bf16_f32 v48, v48, v49\n\t"
"v_cvt_pk_bf16_f32 v50, v50, v51\n\t"
"v_mfma_f32_16x16x32_bf16 v[64:67], a[100:103], a[168:171], v[64:67]\n\t"
"v_cvt_pk_bf16_f32 v228, v228, v229\n\t"
"v_cvt_pk_bf16_f32 v229, v230, v231\n\t"
"v_mfma_f32_16x16x32_bf16 v[64:67], a[4:7], a[172:175], v[64:67]\n\t"
"v_accvgpr_write_b32 a66, v44\n\t"
"v_accvgpr_write_b32 a67, v46\n\t"
"v_mfma_f32_16x16x32_bf16 v[64:67], v[216:219], a[176:179], v[64:67]\n\t"
"s_add_u32 s29, s22, 393600\n\t"
"buffer_store_dwordx4 a[116:119], v248, s[16:19], s29 offen offset:128 nt\n\t"
"v_mfma_f32_16x16x32_bf16 v[68:71], a[100:103], a[172:175], v[68:71]\n\t"
"v_cvt_pk_bf16_f32 v52, v52, v53\n\t"
"v_cvt_pk_bf16_f32 v54, v54, v55\n\t"
"v_mfma_f32_16x16x32_bf16 v[68:71], a[4:7], a[176:179], v[68:71]\n\t"
"v_cvt_pk_bf16_f32 v230, v224, v225\n\t"
"v_cvt_pk_bf16_f32 v231, v226, v227\n\t"
"v_mfma_f32_16x16x32_bf16 v[68:71], v[216:219], a[180:183], v[68:71]\n\t"
"v_accvgpr_write_b32 a74, v48\n\t"
"v_accvgpr_write_b32 a75, v50\n\t"
"v_mfma_f32_16x16x32_bf16 v[72:75], a[100:103], a[176:179], v[72:75]\n\t"
"ds_read_b128 a[168:171], v244 offset:9216\n\t"
"v_cvt_pk_bf16_f32 v56, v56, v57\n\t"
"v_mfma_f32_16x16x32_bf16 v[72:75], a[4:7], a[180:183], v[72:75]\n\t"
"ds_read_b128 a[172:175], v244 offset:9728\n\t"
"v_cvt_pk_bf16_f32 v58, v58, v59\n\t"
"v_mfma_f32_16x16x32_bf16 v[72:75], v[216:219], a[184:187], v[72:75]\n\t"
"ds_read_b128 a[176:179], v244 offset:10240\n\t"
"v_accvgpr_write_b32 a82, v52\n\t"
"s_waitcnt lgkmcnt(5)\n\t"
"v_mfma_f32_16x16x32_bf16 v[236:239], v[236:239], a[208:211], 0\n\t"
"v_accvgpr_write_b32 a83, v54\n\t"
"v_accvgpr_write_b32 a90, v56\n\t"
"v_mfma_f32_16x16x32_bf16 v[76:79], a[100:103], a[180:183], v[76:79]\n\t"
"ds_read_b128 a[180:183], v244 offset:10752\n\t"
"v_accvgpr_write_b32 a91, v58\n\t"
"v_mfma_f32_16x16x32_bf16 v[76:79], a[4:7], a[184:187], v[76:79]\n\t"
"v_cvt_pk_bf16_f32 v60, v60, v61\n\t"
"v_cvt_pk_bf16_f32 v62, v62, v63\n\t"
"v_mfma_f32_16x16x32_bf16 v[76:79], v[216:219], a[188:191], v[76:79]\n\t"
"s_mov_b32 s20, 1302528\n\t"
"buffer_load_dwordx4 a[36:39], v252, s[8:11], s20 offen offset:0 sc1\n\t"
"s_waitcnt lgkmcnt(4)\n\t"
"v_mfma_f32_16x16x32_bf16 v[232:235], v[232:235], a[208:211], 0\n\t"
"ds_read_b128 a[32:35], v252 offset:30720\n\t"
"v_accvgpr_write_b32 a98, v60\n\t"
"v_mfma_f32_16x16x32_bf16 v[80:83], a[100:103], a[184:187], v[80:83]\n\t"
"v_cvt_pk_bf16_f32 v236, v236, v237\n\t"
"v_cvt_pk_bf16_f32 v237, v238, v239\n\t"
"v_mfma_f32_16x16x32_bf16 v[80:83], a[4:7], a[188:191], v[80:83]\n\t"
"ds_read_b128 a[184:187], v244 offset:11264\n\t"
"v_accvgpr_write_b32 a99, v62\n\t"
"v_mfma_f32_16x16x32_bf16 v[80:83], v[216:219], a[192:195], v[80:83]\n\t"
"buffer_load_dwordx4 a[160:163], v252, s[8:11], s20 offen offset:1024 sc1\n\t"
"s_add_u32 s29, s22, 393600\n\t"
"buffer_store_dwordx4 a[148:151], v248, s[16:19], s29 offen offset:256 nt\n\t"
"v_mfma_f32_16x16x32_bf16 v[84:87], a[100:103], a[188:191], v[84:87]\n\t"
"ds_read_b128 a[188:191], v244 offset:11776\n\t"
"v_cvt_pk_bf16_f32 v64, v64, v65\n\t"
"v_mfma_f32_16x16x32_bf16 v[84:87], a[4:7], a[192:195], v[84:87]\n\t"
"v_cvt_pk_bf16_f32 v238, v232, v233\n\t"
"v_cvt_pk_bf16_f32 v239, v234, v235\n\t"
"v_mfma_f32_16x16x32_bf16 v[84:87], v[216:219], a[196:199], v[84:87]\n\t"
"buffer_load_dwordx4 a[152:155], v252, s[8:11], s20 offen offset:2048 sc1\n\t"
"v_cvt_pk_bf16_f32 v66, v66, v67\n\t"
"ds_read_b128 a[128:131], v252 offset:31744\n\t"
"v_mfma_f32_16x16x32_bf16 v[88:91], a[100:103], a[192:195], v[88:91]\n\t"
"ds_read_b128 a[136:139], v252 offset:32768\n\t"
"v_cvt_pk_bf16_f32 v68, v68, v69\n\t"
"v_mfma_f32_16x16x32_bf16 v[88:91], a[4:7], a[196:199], v[88:91]\n\t"
"ds_read_b128 a[192:195], v244 offset:12288\n\t"
"v_cvt_pk_bf16_f32 v70, v70, v71\n\t"
"v_mfma_f32_16x16x32_bf16 v[88:91], v[216:219], a[200:203], v[88:91]\n\t"
"s_mov_b32 s20, 1314816\n\t"
"buffer_load_dwordx4 v[200:203], v252, s[8:11], s20 offen offset:0 sc1\n\t"
"v_accvgpr_write_b32 a108, v64\n\t"
"v_accvgpr_write_b32 a109, v66\n\t"
"v_mfma_f32_16x16x32_bf16 v[92:95], a[100:103], a[196:199], v[92:95]\n\t"
"ds_read_b128 a[196:199], v244 offset:12800\n\t"
"v_cvt_pk_bf16_f32 v72, v72, v73\n\t"
"v_mfma_f32_16x16x32_bf16 v[92:95], a[4:7], a[200:203], v[92:95]\n\t"
"ds_read_b128 a[200:203], v244 offset:13312\n\t"
"v_cvt_pk_bf16_f32 v74, v74, v75\n\t"
"v_mfma_f32_16x16x32_bf16 v[92:95], v[216:219], a[204:207], v[92:95]\n\t"
"ds_read_b128 a[204:207], v244 offset:13824\n\t"
"buffer_load_dwordx4 a[4:7], v252, s[8:11], s20 offen offset:1024 sc1\n\t"
"v_accvgpr_write_b32 a116, v68\n\t"
"s_waitcnt vmcnt(7) lgkmcnt(10)\n\t"
"v_mfma_f32_16x16x32_bf16 v[100:103], a[212:215], a[168:171], v[100:103]\n\t"
"v_accvgpr_write_b32 a117, v70\n\t"
"v_accvgpr_write_b32 a124, v72\n\t"
"v_mfma_f32_16x16x32_bf16 v[100:103], v[96:99], a[172:175], v[100:103]\n\t"
"v_accvgpr_write_b32 a125, v74\n\t"
"v_cvt_pk_bf16_f32 v76, v76, v77\n\t"
"v_mfma_f32_16x16x32_bf16 v[100:103], a[144:147], a[176:179], v[100:103]\n\t"
"buffer_load_dwordx4 v[208:211], v252, s[8:11], s20 offen offset:2048 sc1\n\t"
"s_waitcnt lgkmcnt(9)\n\t"
"v_mfma_f32_16x16x32_bf16 v[104:107], a[212:215], a[172:175], v[104:107]\n\t"
"v_cvt_pk_bf16_f32 v78, v78, v79\n\t"
"v_accvgpr_write_b32 a132, v76\n\t"
"v_mfma_f32_16x16x32_bf16 v[104:107], v[96:99], a[176:179], v[104:107]\n\t"
"v_accvgpr_write_b32 a133, v78\n\t"
"v_cvt_pk_bf16_f32 v80, v80, v81\n\t"
"v_mfma_f32_16x16x32_bf16 v[104:107], a[144:147], a[180:183], v[104:107]\n\t"
"s_add_u32 s29, s22, 590208\n\t"
"buffer_store_dwordx4 a[60:63], v248, s[16:19], s29 offen offset:0 nt\n\t"
"s_mov_b32 s20, 0\n\t"
"buffer_load_dwordx4 v[216:219], v252, s[8:11], s20 offen offset:0 sc1\n\t"
"s_waitcnt lgkmcnt(0)\n\t"
"s_barrier\n\t"
"v_mfma_f32_16x16x32_bf16 v[108:111], a[212:215], a[176:179], v[108:111]\n\t"
"ds_read_b64_tr_b16 v[224:225], v240 offset:39936\n\t"
"v_cvt_pk_bf16_f32 v82, v82, v83\n\t"
"v_mfma_f32_16x16x32_bf16 v[108:111], v[96:99], a[180:183], v[108:111]\n\t"
"ds_read_b64_tr_b16 v[226:227], v240 offset:41472\n\t"
"v_cvt_pk_bf16_f32 v84, v84, v85\n\t"
"v_mfma_f32_16x16x32_bf16 v[108:111], a[144:147], a[184:187], v[108:111]\n\t"
"s_mov_b64 exec, s[44:45]\n\t"
"ds_write_b128 v242, v[228:231] offset:4608\n\t"
"s_mov_b64 exec, -1\n\t"
"s_mov_b64 exec, s[44:45]\n\t"
"ds_write_b128 v242, v[236:239] offset:6144\n\t"
"s_mov_b64 exec, -1\n\t"
"v_mfma_f32_16x16x32_bf16 v[112:115], a[212:215], a[180:183], v[112:115]\n\t"
"ds_read_b64_tr_b16 v[232:233], v241 offset:39936\n\t"
"v_cvt_pk_bf16_f32 v86, v86, v87\n\t"
"v_mfma_f32_16x16x32_bf16 v[112:115], v[96:99], a[184:187], v[112:115]\n\t"
"ds_read_b64_tr_b16 v[234:235], v241 offset:41472\n\t"
"v_accvgpr_write_b32 a140, v80\n\t"
"v_mfma_f32_16x16x32_bf16 v[112:115], a[144:147], a[188:191], v[112:115]\n\t"
"buffer_load_dwordx4 a[12:15], v252, s[8:11], s20 offen offset:1024 sc1\n\t"
"v_accvgpr_write_b32 a141, v82\n\t"
"v_subrev_u32 v240, 59904, v240\n\t"
"v_mfma_f32_16x16x32_bf16 v[116:119], a[212:215], a[184:187], v[116:119]\n\t"
"v_subrev_u32 v241, 59904, v241\n\t"
"v_cvt_pk_bf16_f32 v88, v88, v89\n\t"
"v_mfma_f32_16x16x32_bf16 v[116:119], v[96:99], a[188:191], v[116:119]\n\t"
"v_cvt_pk_bf16_f32 v90, v90, v91\n\t"
"v_accvgpr_write_b32 a148, v84\n\t"
"v_mfma_f32_16x16x32_bf16 v[116:119], a[144:147], a[192:195], v[116:119]\n\t"
"buffer_load_dwordx4 a[20:23], v252, s[8:11], s20 offen offset:2048 sc1\n\t"
"v_accvgpr_write_b32 a149, v86\n\t"
"ds_read_b64_tr_b16 v[228:229], v240 offset:0\n\t"
"v_mfma_f32_16x16x32_bf16 v[120:123], a[212:215], a[188:191], v[120:123]\n\t"
"ds_read_b64_tr_b16 v[230:231], v240 offset:1536\n\t"
"v_cvt_pk_bf16_f32 v92, v92, v93\n\t"
"v_mfma_f32_16x16x32_bf16 v[120:123], v[96:99], a[192:195], v[120:123]\n\t"
"ds_read_b64_tr_b16 v[236:237], v241 offset:0\n\t"
"v_cvt_pk_bf16_f32 v94, v94, v95\n\t"
"v_mfma_f32_16x16x32_bf16 v[120:123], a[144:147], a[196:199], v[120:123]\n\t"
"s_mov_b32 s20, 12288\n\t"
"buffer_load_dwordx4 a[28:31], v252, s[8:11], s20 offen offset:0 sc1\n\t"
"ds_read_b64_tr_b16 v[238:239], v241 offset:1536\n\t"
"v_accvgpr_write_b32 a156, v88\n\t"
"s_waitcnt lgkmcnt(8)\n\t"
"v_mfma_f32_16x16x32_bf16 v[224:227], v[224:227], a[208:211], 0\n\t"
"ds_write_b128 v249, a[40:43] offset:18432\n\t"
"v_accvgpr_write_b32 a157, v90\n\t"
"v_cvt_pk_bf16_f32 v100, v100, v101\n\t"
"s_waitcnt lgkmcnt(5)\n\t"
"v_mfma_f32_16x16x32_bf16 v[232:235], v[232:235], a[208:211], 0\n\t"
"buffer_load_dwordx4 a[216:219], v252, s[8:11], s20 offen offset:1024 sc1\n\t"
"v_cvt_pk_bf16_f32 v102, v102, v103\n\t"
"v_accvgpr_write_b32 a164, v92\n\t"
"v_mfma_f32_16x16x32_bf16 v[124:127], a[212:215], a[192:195], v[124:127]\n\t"
"v_accvgpr_write_b32 a165, v94\n\t"
"v_cvt_pk_bf16_f32 v104, v104, v105\n\t"
"v_mfma_f32_16x16x32_bf16 v[124:127], v[96:99], a[196:199], v[124:127]\n\t"
"v_cvt_pk_bf16_f32 v106, v106, v107\n\t"
"v_accvgpr_write_b32 a44, v100\n\t"
"v_mfma_f32_16x16x32_bf16 v[124:127], a[144:147], a[200:203], v[124:127]\n\t"
"s_add_u32 s29, s22, 590208\n\t"
"buffer_store_dwordx4 a[92:95], v248, s[16:19], s29 offen offset:128 nt\n\t"
"ds_write_b128 v249, a[56:59] offset:30720\n\t"
"v_mfma_f32_16x16x32_bf16 v[128:131], a[212:215], a[196:199], v[128:131]\n\t"
"v_cvt_pk_bf16_f32 v224, v224, v225\n\t"
"v_cvt_pk_bf16_f32 v225, v226, v227\n\t"
"v_mfma_f32_16x16x32_bf16 v[128:131], v[96:99], a[200:203], v[128:131]\n\t"
"v_accvgpr_write_b32 a45, v102\n\t"
"v_cvt_pk_bf16_f32 v226, v232, v233\n\t"
"v_mfma_f32_16x16x32_bf16 v[128:131], a[144:147], a[204:207], v[128:131]\n\t"
"buffer_load_dwordx4 a[220:223], v252, s[8:11], s20 offen offset:2048 sc1\n\t"
"s_mov_b32 s20, 24576\n\t"
"buffer_load_dwordx4 a[212:215], v252, s[8:11], s20 offen offset:0 sc1\n\t"
"s_waitcnt lgkmcnt(4)\n\t"
"v_mfma_f32_16x16x32_bf16 v[228:231], v[228:231], a[208:211], 0\n\t"
"v_cvt_pk_bf16_f32 v227, v234, v235\n\t"
"v_cvt_pk_bf16_f32 v108, v108, v109\n\t"
"v_cvt_pk_bf16_f32 v110, v110, v111\n\t"
"v_accvgpr_write_b32 a52, v104\n\t"
"s_waitcnt lgkmcnt(2)\n\t"
"v_mfma_f32_16x16x32_bf16 v[236:239], v[236:239], a[208:211], 0\n\t"
"v_accvgpr_write_b32 a53, v106\n\t"
"buffer_load_dwordx4 a[144:147], v252, s[8:11], s20 offen offset:1024 sc1\n\t"
"v_cvt_pk_bf16_f32 v112, v112, v113\n\t"
"s_waitcnt vmcnt(13)\n\t"
"v_mfma_f32_16x16x32_bf16 v[132:135], a[36:39], a[168:171], v[132:135]\n\t"
"v_cvt_pk_bf16_f32 v114, v114, v115\n\t"
"ds_read_b64_tr_b16 v[232:233], v240 offset:19968\n\t"
"v_mfma_f32_16x16x32_bf16 v[132:135], a[160:163], a[172:175], v[132:135]\n\t"
"ds_read_b64_tr_b16 v[234:235], v240 offset:21504\n\t"
"v_accvgpr_write_b32 a60, v108\n\t"
"v_mfma_f32_16x16x32_bf16 v[132:135], a[152:155], a[176:179], v[132:135]\n\t"
"s_mov_b64 exec, s[44:45]\n\t"
"ds_write_b128 v242, v[224:227] offset:7680\n\t"
"s_mov_b64 exec, -1\n\t"
"v_accvgpr_write_b32 a61, v110\n\t"
"ds_read_b64_tr_b16 v[224:225], v241 offset:19968\n\t"
"v_mfma_f32_16x16x32_bf16 v[136:139], a[36:39], a[172:175], v[136:139]\n\t"
"ds_read_b64_tr_b16 v[226:227], v241 offset:21504\n\t"
"v_cvt_pk_bf16_f32 v228, v228, v229\n\t"
"v_mfma_f32_16x16x32_bf16 v[136:139], a[160:163], a[176:179], v[136:139]\n\t"
"v_cvt_pk_bf16_f32 v229, v230, v231\n\t"
"v_cvt_pk_bf16_f32 v116, v116, v117\n\t"
"v_mfma_f32_16x16x32_bf16 v[136:139], a[152:155], a[180:183], v[136:139]\n\t"
"buffer_load_dwordx4 a[224:227], v252, s[8:11], s20 offen offset:2048 sc1\n\t"
"ds_write_b64 v250, a[108:109] offset:18432\n\t"
"v_mfma_f32_16x16x32_bf16 v[140:143], a[36:39], a[176:179], v[140:143]\n\t"
"v_cvt_pk_bf16_f32 v118, v118, v119\n\t"
"v_cvt_pk_bf16_f32 v230, v236, v237\n\t"
"v_mfma_f32_16x16x32_bf16 v[140:143], a[160:163], a[180:183], v[140:143]\n\t"
"v_cvt_pk_bf16_f32 v231, v238, v239\n\t"
"v_accvgpr_write_b32 a68, v112\n\t"
"v_mfma_f32_16x16x32_bf16 v[140:143], a[152:155], a[184:187], v[140:143]\n\t"
"v_accvgpr_write_b32 a69, v114\n\t"
"ds_write_b64 v250, a[124:125] offset:30720\n\t"
"s_mov_b32 s20, 184320\n\t"
"buffer_load_dwordx4 a[228:231], v252, s[8:11], s20 offen offset:0 sc1\n\t"
"s_add_u32 s29, s22, 590208\n\t"
"buffer_store_dwordx4 v[196:199], v248, s[16:19], s29 offen offset:256 nt\n\t"
"v_mfma_f32_16x16x32_bf16 v[144:147], a[36:39], a[180:183], v[144:147]\n\t"
"v_cvt_pk_bf16_f32 v120, v120, v121\n\t"
"v_cvt_pk_bf16_f32 v122, v122, v123\n\t"
"v_mfma_f32_16x16x32_bf16 v[144:147], a[160:163], a[184:187], v[144:147]\n\t"
"v_accvgpr_write_b32 a76, v116\n\t"
"v_accvgpr_write_b32 a77, v118\n\t"
"v_mfma_f32_16x16x32_bf16 v[144:147], a[152:155], a[188:191], v[144:147]\n\t"
"buffer_load_dwordx4 a[232:235], v252, s[8:11], s20 offen offset:1024 sc1\n\t"
"s_mov_b64 exec, s[44:45]\n\t"
"ds_write_b128 v242, v[228:231] offset:0\n\t"
"s_mov_b64 exec, -1\n\t"
"s_waitcnt lgkmcnt(6)\n\t"
"v_mfma_f32_16x16x32_bf16 v[232:235], v[232:235], a[208:211], 0\n\t"
"ds_read_b64_tr_b16 v[236:237], v240 offset:39936\n\t"
"ds_read_b64_tr_b16 v[238:239], v240 offset:41472\n\t"
"ds_read_b64_tr_b16 v[228:229], v241 offset:39936\n\t"
"v_accvgpr_write_b32 a84, v120\n\t"
"s_waitcnt lgkmcnt(6)\n\t"
"v_mfma_f32_16x16x32_bf16 v[224:227], v[224:227], a[208:211], 0\n\t"
"ds_read_b64_tr_b16 v[230:231], v241 offset:41472\n\t"
"v_accvgpr_write_b32 a85, v122\n\t"
"buffer_load_dwordx4 a[236:239], v252, s[8:11], s20 offen offset:2048 sc1\n\t"
"v_mfma_f32_16x16x32_bf16 v[148:151], a[36:39], a[184:187], v[148:151]\n\t"
"v_cvt_pk_bf16_f32 v124, v124, v125\n\t"
"v_cvt_pk_bf16_f32 v126, v126, v127\n\t"
"v_mfma_f32_16x16x32_bf16 v[148:151], a[160:163], a[188:191], v[148:151]\n\t"
"v_cvt_pk_bf16_f32 v128, v128, v129\n\t"
"v_cvt_pk_bf16_f32 v130, v130, v131\n\t"
"v_mfma_f32_16x16x32_bf16 v[148:151], a[152:155], a[192:195], v[148:151]\n\t"
"s_mov_b32 s20, 196608\n\t"
"buffer_load_dwordx4 a[240:243], v252, s[8:11], s20 offen offset:0 sc1\n\t"
"v_accvgpr_write_b32 a92, v124\n\t"
"v_accvgpr_write_b32 a93, v126\n\t"
"v_mfma_f32_16x16x32_bf16 v[152:155], a[36:39], a[188:191], v[152:155]\n\t"
"v_cvt_pk_bf16_f32 v232, v232, v233\n\t"
"v_cvt_pk_bf16_f32 v233, v234, v235\n\t"
"v_mfma_f32_16x16x32_bf16 v[152:155], a[160:163], a[192:195], v[152:155]\n\t"
"v_cvt_pk_bf16_f32 v234, v224, v225\n\t"
"v_cvt_pk_bf16_f32 v235, v226, v227\n\t"
"v_mfma_f32_16x16x32_bf16 v[152:155], a[152:155], a[196:199], v[152:155]\n\t"
"ds_write_b128 v249, a[48:51] offset:24576\n\t"
"ds_write_b128 v249, a[64:67] offset:36864\n\t"
"s_waitcnt lgkmcnt(4)\n\t"
"v_mfma_f32_16x16x32_bf16 v[236:239], v[236:239], a[208:211], 0\n\t"
"v_accvgpr_write_b32 a100, v128\n\t"
"v_accvgpr_write_b32 a101, v130\n\t"
"v_cvt_pk_bf16_f32 v132, v132, v133\n\t"
"v_cvt_pk_bf16_f32 v134, v134, v135\n\t"
"s_waitcnt lgkmcnt(2)\n\t"
"v_mfma_f32_16x16x32_bf16 v[228:231], v[228:231], a[208:211], 0\n\t"
"s_add_u32 s29, s22, 786816\n\t"
"buffer_store_dwordx4 v[204:207], v248, s[16:19], s29 offen offset:0 nt\n\t"
"buffer_load_dwordx4 a[244:247], v252, s[8:11], s20 offen offset:1024 sc1\n\t"
"v_mfma_f32_16x16x32_bf16 v[156:159], a[36:39], a[192:195], v[156:159]\n\t"
"v_cvt_pk_bf16_f32 v136, v136, v137\n\t"
"v_cvt_pk_bf16_f32 v138, v138, v139\n\t"
"v_mfma_f32_16x16x32_bf16 v[156:159], a[160:163], a[196:199], v[156:159]\n\t"
"v_accvgpr_write_b32 a46, v132\n\t"
"v_accvgpr_write_b32 a47, v134\n\t"
"v_mfma_f32_16x16x32_bf16 v[156:159], a[152:155], a[200:203], v[156:159]\n\t"
"s_mov_b64 exec, s[44:45]\n\t"
"ds_write_b128 v242, v[232:235] offset:1536\n\t"
"s_mov_b64 exec, -1\n\t"
"v_cvt_pk_bf16_f32 v140, v140, v141\n\t"
"v_cvt_pk_bf16_f32 v142, v142, v143\n\t"
"v_mfma_f32_16x16x32_bf16 v[160:163], a[36:39], a[196:199], v[160:163]\n\t"
"v_cvt_pk_bf16_f32 v236, v236, v237\n\t"
"v_cvt_pk_bf16_f32 v237, v238, v239\n\t"
"v_mfma_f32_16x16x32_bf16 v[160:163], a[160:163], a[200:203], v[160:163]\n\t"
"v_cvt_pk_bf16_f32 v238, v228, v229\n\t"
"v_cvt_pk_bf16_f32 v239, v230, v231\n\t"
"v_mfma_f32_16x16x32_bf16 v[160:163], a[152:155], a[204:207], v[160:163]\n\t"
"buffer_load_dwordx4 a[36:39], v252, s[8:11], s20 offen offset:2048 sc1\n\t"
"v_accvgpr_write_b32 a54, v136\n\t"
"v_accvgpr_write_b32 a55, v138\n\t"
"s_waitcnt vmcnt(19)\n\t"
"v_mfma_f32_16x16x32_bf16 v[164:167], v[200:203], a[168:171], v[164:167]\n\t"
"v_cvt_pk_bf16_f32 v144, v144, v145\n\t"
"v_cvt_pk_bf16_f32 v146, v146, v147\n\t"
"v_mfma_f32_16x16x32_bf16 v[164:167], a[4:7], a[172:175], v[164:167]\n\t"
"v_accvgpr_write_b32 a62, v140\n\t"
"v_accvgpr_write_b32 a63, v142\n\t"
"v_mfma_f32_16x16x32_bf16 v[164:167], v[208:211], a[176:179], v[164:167]\n\t"
"ds_write_b64 v250, a[116:117] offset:24576\n\t"
"ds_write_b64 v250, a[132:133] offset:36864\n\t"
"v_mfma_f32_16x16x32_bf16 v[168:171], v[200:203], a[172:175], v[168:171]\n\t"
"v_accvgpr_write_b32 a70, v144\n\t"
"v_accvgpr_write_b32 a71, v146\n\t"
"v_mfma_f32_16x16x32_bf16 v[168:171], a[4:7], a[176:179], v[168:171]\n\t"
"v_cvt_pk_bf16_f32 v148, v148, v149\n\t"
"v_cvt_pk_bf16_f32 v150, v150, v151\n\t"
"v_mfma_f32_16x16x32_bf16 v[168:171], v[208:211], a[180:183], v[168:171]\n\t"
"s_mov_b32 s20, 208896\n\t"
"buffer_load_dwordx4 a[160:163], v252, s[8:11], s20 offen offset:0 sc1\n\t"
"s_mov_b64 exec, s[44:45]\n\t"
"ds_write_b128 v242, v[236:239] offset:3072\n\t"
"s_mov_b64 exec, -1\n\t"
"v_mfma_f32_16x16x32_bf16 v[172:175], v[200:203], a[176:179], v[172:175]\n\t"
"v_cvt_pk_bf16_f32 v152, v152, v153\n\t"
"v_cvt_pk_bf16_f32 v154, v154, v155\n\t"
"v_mfma_f32_16x16x32_bf16 v[172:175], a[4:7], a[180:183], v[172:175]\n\t"
"v_accvgpr_write_b32 a78, v148\n\t"
"v_accvgpr_write_b32 a79, v150\n\t"
"v_mfma_f32_16x16x32_bf16 v[172:175], v[208:211], a[184:187], v[172:175]\n\t"
"s_add_u32 s29, s22, 786816\n\t"
"buffer_store_dwordx4 v[212:215], v248, s[16:19], s29 offen offset:128 nt\n\t"
"v_accvgpr_write_b32 a86, v152\n\t"
"v_accvgpr_write_b32 a87, v154\n\t"
"v_mfma_f32_16x16x32_bf16 v[176:179], v[200:203], a[180:183], v[176:179]\n\t"
"v_cvt_pk_bf16_f32 v156, v156, v157\n\t"
"v_cvt_pk_bf16_f32 v158, v158, v159\n\t"
"v_mfma_f32_16x16x32_bf16 v[176:179], a[4:7], a[184:187], v[176:179]\n\t"
"v_cvt_pk_bf16_f32 v160, v160, v161\n\t"
"v_cvt_pk_bf16_f32 v162, v162, v163\n\t"
"v_mfma_f32_16x16x32_bf16 v[176:179], v[208:211], a[188:191], v[176:179]\n\t"
"buffer_load_dwordx4 a[152:155], v252, s[8:11], s20 offen offset:1024 sc1\n\t"
"s_waitcnt lgkmcnt(0)\n\t"
"s_barrier\n\t"
"v_mfma_f32_16x16x32_bf16 v[180:183], v[200:203], a[184:187], v[180:183]\n\t"
"ds_read_b128 a[168:171], v244 offset:0\n\t"
"v_accvgpr_write_b32 a94, v156\n\t"
"v_mfma_f32_16x16x32_bf16 v[180:183], a[4:7], a[188:191], v[180:183]\n\t"
"ds_read_b128 a[172:175], v244 offset:512\n\t"
"v_accvgpr_write_b32 a95, v158\n\t"
"v_mfma_f32_16x16x32_bf16 v[180:183], v[208:211], a[192:195], v[180:183]\n\t"
"ds_read_b128 a[176:179], v244 offset:1024\n\t"
"ds_read_b128 a[180:183], v244 offset:1536\n\t"
"buffer_load_dwordx4 a[248:251], v252, s[8:11], s20 offen offset:2048 sc1\n\t"
"v_mfma_f32_16x16x32_bf16 v[184:187], v[200:203], a[188:191], v[184:187]\n\t"
"ds_read_b128 a[184:187], v244 offset:2048\n\t"
"v_cvt_pk_bf16_f32 v164, v164, v165\n\t"
"v_mfma_f32_16x16x32_bf16 v[184:187], a[4:7], a[192:195], v[184:187]\n\t"
"ds_read_b128 a[188:191], v244 offset:2560\n\t"
"v_cvt_pk_bf16_f32 v166, v166, v167\n\t"
"v_mfma_f32_16x16x32_bf16 v[184:187], v[208:211], a[196:199], v[184:187]\n\t"
"s_mov_b32 s20, 221184\n\t"
"buffer_load_dwordx4 a[252:255], v252, s[8:11], s20 offen offset:0 sc1\n\t"
"s_add_u32 s29, s22, 786816\n\t"
"buffer_store_dwordx4 a[0:3], v248, s[16:19], s29 offen offset:256 nt\n\t"
"v_mfma_f32_16x16x32_bf16 v[188:191], v[200:203], a[192:195], v[188:191]\n\t"
"ds_read_b128 a[192:195], v244 offset:3072\n\t"
"v_accvgpr_write_b32 a102, v160\n\t"
"v_mfma_f32_16x16x32_bf16 v[188:191], a[4:7], a[196:199], v[188:191]\n\t"
"v_accvgpr_write_b32 a103, v162\n\t"
"v_cvt_pk_bf16_f32 v168, v168, v169\n\t"
"v_mfma_f32_16x16x32_bf16 v[188:191], v[208:211], a[200:203], v[188:191]\n\t"
"v_cvt_pk_bf16_f32 v170, v170, v171\n\t"
"v_accvgpr_write_b32 a110, v164\n\t"
"v_mfma_f32_16x16x32_bf16 v[192:195], v[200:203], a[196:199], v[192:195]\n\t"
"ds_read_b128 a[196:199], v244 offset:3584\n\t"
"v_accvgpr_write_b32 a111, v166\n\t"
"v_mfma_f32_16x16x32_bf16 v[192:195], a[4:7], a[200:203], v[192:195]\n\t"
"v_cvt_pk_bf16_f32 v172, v172, v173\n\t"
"v_cvt_pk_bf16_f32 v174, v174, v175\n\t"
"v_mfma_f32_16x16x32_bf16 v[192:195], v[208:211], a[204:207], v[192:195]\n\t"
"buffer_load_dwordx4 a[4:7], v252, s[8:11], s20 offen offset:1024 sc1\n\t"
"ds_read_b128 a[200:203], v244 offset:4096\n\t"
"v_accvgpr_write_b32 a118, v168\n\t"
"Lst18_%=:\n\t"
"s_waitcnt vmcnt(22) lgkmcnt(6)\n\t"
"v_mfma_f32_16x16x32_bf16 v[2:5], v[216:219], a[168:171], 0\n\t"
"v_accvgpr_write_b32 a119, v170\n\t"
"v_cvt_pk_bf16_f32 v176, v176, v177\n\t"
"v_mfma_f32_16x16x32_bf16 v[2:5], a[12:15], a[172:175], v[2:5]\n\t"
"v_cvt_pk_bf16_f32 v178, v178, v179\n\t"
"ds_read_b128 a[204:207], v244 offset:4608\n\t"
"v_mfma_f32_16x16x32_bf16 v[2:5], a[20:23], a[176:179], v[2:5]\n\t"
"buffer_load_dwordx4 v[200:203], v252, s[8:11], s20 offen offset:2048 sc1\n\t"
"s_waitcnt lgkmcnt(6)\n\t"
"v_mfma_f32_16x16x32_bf16 v[6:9], v[216:219], a[172:175], 0\n\t"
"v_accvgpr_write_b32 a126, v172\n\t"
"v_accvgpr_write_b32 a127, v174\n\t"
"v_mfma_f32_16x16x32_bf16 v[6:9], a[12:15], a[176:179], v[6:9]\n\t"
"v_cvt_pk_bf16_f32 v180, v180, v181\n\t"
"v_cvt_pk_bf16_f32 v182, v182, v183\n\t"
"v_mfma_f32_16x16x32_bf16 v[6:9], a[20:23], a[180:183], v[6:9]\n\t"
"v_accvgpr_write_b32 a134, v176\n\t"
"v_accvgpr_write_b32 a135, v178\n\t"
"s_waitcnt lgkmcnt(5)\n\t"
"v_mfma_f32_16x16x32_bf16 v[10:13], v[216:219], a[176:179], 0\n\t"
"v_cvt_pk_bf16_f32 v184, v184, v185\n\t"
"v_cvt_pk_bf16_f32 v186, v186, v187\n\t"
"v_mfma_f32_16x16x32_bf16 v[10:13], a[12:15], a[180:183], v[10:13]\n\t"
"v_accvgpr_write_b32 a142, v180\n\t"
"v_accvgpr_write_b32 a143, v182\n\t"
"v_mfma_f32_16x16x32_bf16 v[10:13], a[20:23], a[184:187], v[10:13]\n\t"
"s_add_u32 s29, s22, 983424\n\t"
"buffer_store_dwordx4 a[8:11], v248, s[16:19], s29 offen offset:0 nt\n\t"
"s_waitcnt lgkmcnt(4)\n\t"
"v_mfma_f32_16x16x32_bf16 v[14:17], v[216:219], a[180:183], 0\n\t"
"v_cvt_pk_bf16_f32 v188, v188, v189\n\t"
"v_cvt_pk_bf16_f32 v190, v190, v191\n\t"
"v_mfma_f32_16x16x32_bf16 v[14:17], a[12:15], a[184:187], v[14:17]\n\t"
"v_accvgpr_write_b32 a150, v184\n\t"
"v_accvgpr_write_b32 a151, v186\n\t"
"v_mfma_f32_16x16x32_bf16 v[14:17], a[20:23], a[188:191], v[14:17]\n\t"
"s_mov_b32 s20, 233472\n\t"
"buffer_load_dwordx4 v[208:211], v252, s[8:11], s20 offen offset:0 sc1\n\t"
"s_waitcnt lgkmcnt(3)\n\t"
"v_mfma_f32_16x16x32_bf16 v[18:21], v[216:219], a[184:187], 0\n\t"
"v_cvt_pk_bf16_f32 v192, v192, v193\n\t"
"v_cvt_pk_bf16_f32 v194, v194, v195\n\t"
"v_mfma_f32_16x16x32_bf16 v[18:21], a[12:15], a[188:191], v[18:21]\n\t"
"v_accvgpr_write_b32 a158, v188\n\t"
"v_accvgpr_write_b32 a159, v190\n\t"
"v_mfma_f32_16x16x32_bf16 v[18:21], a[20:23], a[192:195], v[18:21]\n\t"
"v_accvgpr_write_b32 a166, v192\n\t"
"s_waitcnt lgkmcnt(2)\n\t"
"v_mfma_f32_16x16x32_bf16 v[22:25], v[216:219], a[188:191], 0\n\t"
"v_accvgpr_write_b32 a167, v194\n\t"
"v_mfma_f32_16x16x32_bf16 v[22:25], a[12:15], a[192:195], v[22:25]\n\t"
"ds_read_b128 v[196:199], v252 offset:18432\n\t"
"v_mfma_f32_16x16x32_bf16 v[22:25], a[20:23], a[196:199], v[22:25]\n\t"
"s_waitcnt lgkmcnt(2)\n\t"
"v_mfma_f32_16x16x32_bf16 v[26:29], v[216:219], a[192:195], 0\n\t"
"ds_read_b128 a[0:3], v252 offset:30720\n\t"
"v_mfma_f32_16x16x32_bf16 v[26:29], a[12:15], a[196:199], v[26:29]\n\t"
"ds_read_b128 v[204:207], v252 offset:19456\n\t"
"v_mfma_f32_16x16x32_bf16 v[26:29], a[20:23], a[200:203], v[26:29]\n\t"
"ds_read_b128 a[8:11], v252 offset:31744\n\t"
"s_waitcnt lgkmcnt(4)\n\t"
"v_mfma_f32_16x16x32_bf16 v[30:33], v[216:219], a[196:199], 0\n\t"
"ds_read_b128 v[212:215], v252 offset:20480\n\t"
"v_mfma_f32_16x16x32_bf16 v[30:33], a[12:15], a[200:203], v[30:33]\n\t"
"ds_read_b128 a[40:43], v252 offset:32768\n\t"
"v_mfma_f32_16x16x32_bf16 v[30:33], a[20:23], a[204:207], v[30:33]\n\t"
"buffer_load_dwordx4 v[216:219], v252, s[8:11], s20 offen offset:1024 sc1\n\t"
"s_waitcnt vmcnt(22)\n\t"
"v_mfma_f32_16x16x32_bf16 v[34:37], a[28:31], a[168:171], 0\n\t"
"v_mfma_f32_16x16x32_bf16 v[34:37], a[216:219], a[172:175], v[34:37]\n\t"
"v_mfma_f32_16x16x32_bf16 v[34:37], a[220:223], a[176:179], v[34:37]\n\t"
"s_add_u32 s29, s22, 983424\n\t"
"buffer_store_dwordx4 a[16:19], v248, s[16:19], s29 offen offset:128 nt\n\t"
"v_mfma_f32_16x16x32_bf16 v[38:41], a[28:31], a[172:175], 0\n\t"
"ds_read_b64_tr_b16 v[224:225], v240 offset:512\n\t"
"v_mfma_f32_16x16x32_bf16 v[38:41], a[216:219], a[176:179], v[38:41]\n\t"
"ds_read_b64_tr_b16 v[226:227], v240 offset:2048\n\t"
"v_mfma_f32_16x16x32_bf16 v[38:41], a[220:223], a[180:183], v[38:41]\n\t"
"buffer_load_dwordx4 a[12:15], v252, s[8:11], s20 offen offset:2048 sc1\n\t"
"v_mfma_f32_16x16x32_bf16 v[42:45], a[28:31], a[176:179], 0\n\t"
"v_mfma_f32_16x16x32_bf16 v[42:45], a[216:219], a[180:183], v[42:45]\n\t"
"v_mfma_f32_16x16x32_bf16 v[42:45], a[220:223], a[184:187], v[42:45]\n\t"
"s_mov_b32 s20, 245760\n\t"
"buffer_load_dwordx4 a[20:23], v252, s[8:11], s20 offen offset:0 sc1\n\t"
"v_mfma_f32_16x16x32_bf16 v[46:49], a[28:31], a[180:183], 0\n\t"
"v_mfma_f32_16x16x32_bf16 v[46:49], a[216:219], a[184:187], v[46:49]\n\t"
"v_mfma_f32_16x16x32_bf16 v[46:49], a[220:223], a[188:191], v[46:49]\n\t"
"s_add_u32 s29, s22, 983424\n\t"
"buffer_store_dwordx4 a[104:107], v248, s[16:19], s29 offen offset:256 nt\n\t"
"s_waitcnt lgkmcnt(2)\n\t"
"s_barrier\n\t"
"v_mfma_f32_16x16x32_bf16 v[50:53], a[28:31], a[184:187], 0\n\t"
"ds_read_b64_tr_b16 v[232:233], v241 offset:512\n\t"
"v_mfma_f32_16x16x32_bf16 v[50:53], a[216:219], a[188:191], v[50:53]\n\t"
"ds_read_b64_tr_b16 v[234:235], v241 offset:2048\n\t"
"v_mfma_f32_16x16x32_bf16 v[50:53], a[220:223], a[192:195], v[50:53]\n\t"
"ds_write_b128 v249, a[72:75] offset:18432\n\t"
"v_mfma_f32_16x16x32_bf16 v[54:57], a[28:31], a[188:191], 0\n\t"
"v_mfma_f32_16x16x32_bf16 v[54:57], a[216:219], a[192:195], v[54:57]\n\t"
"ds_read_b64_tr_b16 v[228:229], v240 offset:20480\n\t"
"v_mfma_f32_16x16x32_bf16 v[54:57], a[220:223], a[196:199], v[54:57]\n\t"
"ds_write_b128 v249, a[88:91] offset:30720\n\t"
"s_waitcnt lgkmcnt(5)\n\t"
"v_mfma_f32_16x16x32_bf16 v[224:227], v[224:227], a[208:211], 0\n\t"
"ds_read_b64_tr_b16 v[230:231], v240 offset:22016\n\t"
"v_mfma_f32_16x16x32_bf16 v[58:61], a[28:31], a[192:195], 0\n\t"
"v_mfma_f32_16x16x32_bf16 v[58:61], a[216:219], a[196:199], v[58:61]\n\t"
"ds_read_b64_tr_b16 v[236:237], v241 offset:20480\n\t"
"v_mfma_f32_16x16x32_bf16 v[58:61], a[220:223], a[200:203], v[58:61]\n\t"
"buffer_load_dwordx4 a[104:107], v252, s[8:11], s20 offen offset:1024 sc1\n\t"
"s_waitcnt lgkmcnt(5)\n\t"
"v_mfma_f32_16x16x32_bf16 v[232:235], v[232:235], a[208:211], 0\n\t"
"ds_read_b64_tr_b16 v[238:239], v241 offset:22016\n\t"
"v_mfma_f32_16x16x32_bf16 v[62:65], a[28:31], a[196:199], 0\n\t"
"v_cvt_pk_bf16_f32 v224, v224, v225\n\t"
"v_cvt_pk_bf16_f32 v225, v226, v227\n\t"
"v_mfma_f32_16x16x32_bf16 v[62:65], a[216:219], a[200:203], v[62:65]\n\t"
"v_mfma_f32_16x16x32_bf16 v[62:65], a[220:223], a[204:207], v[62:65]\n\t"
"buffer_load_dwordx4 a[28:31], v252, s[8:11], s20 offen offset:2048 sc1\n\t"
"s_add_u32 s29, s22, 1180032\n\t"
"buffer_store_dwordx4 a[24:27], v248, s[16:19], s29 offen offset:0 nt\n\t"
"s_waitcnt vmcnt(26)\n\t"
"v_mfma_f32_16x16x32_bf16 v[66:69], a[212:215], a[168:171], 0\n\t"
"ds_read_b128 a[168:171], v244 offset:9216\n\t"
"v_mfma_f32_16x16x32_bf16 v[66:69], a[144:147], a[172:175], v[66:69]\n\t"
"v_cvt_pk_bf16_f32 v226, v232, v233\n\t"
"v_cvt_pk_bf16_f32 v227, v234, v235\n\t"
"v_mfma_f32_16x16x32_bf16 v[66:69], a[224:227], a[176:179], v[66:69]\n\t"
"s_mov_b32 s20, 405504\n\t"
"buffer_load_dwordx4 a[216:219], v252, s[8:11], s20 offen offset:0 sc1\n\t"
"ds_write_b64 v250, a[140:141] offset:18432\n\t"
"v_mfma_f32_16x16x32_bf16 v[70:73], a[212:215], a[172:175], 0\n\t"
"ds_read_b128 a[172:175], v244 offset:9728\n\t"
"v_mfma_f32_16x16x32_bf16 v[70:73], a[144:147], a[176:179], v[70:73]\n\t"
"v_mfma_f32_16x16x32_bf16 v[70:73], a[224:227], a[180:183], v[70:73]\n\t"
"ds_write_b64 v250, a[156:157] offset:30720\n\t"
"buffer_load_dwordx4 a[220:223], v252, s[8:11], s20 offen offset:1024 sc1\n\t"
"s_waitcnt lgkmcnt(6)\n\t"
"v_mfma_f32_16x16x32_bf16 v[228:231], v[228:231], a[208:211], 0\n\t"
"ds_write_b128 v249, a[80:83] offset:24576\n\t"
"v_mfma_f32_16x16x32_bf16 v[74:77], a[212:215], a[176:179], 0\n\t"
"ds_read_b128 a[176:179], v244 offset:10240\n\t"
"v_mfma_f32_16x16x32_bf16 v[74:77], a[144:147], a[180:183], v[74:77]\n\t"
"v_mfma_f32_16x16x32_bf16 v[74:77], a[224:227], a[184:187], v[74:77]\n\t"
"ds_write_b128 v249, a[96:99] offset:36864\n\t"
"s_waitcnt lgkmcnt(7)\n\t"
"v_mfma_f32_16x16x32_bf16 v[236:239], v[236:239], a[208:211], 0\n\t"
"ds_write_b64 v250, a[148:149] offset:24576\n\t"
"v_mfma_f32_16x16x32_bf16 v[78:81], a[212:215], a[180:183], 0\n\t"
"v_cvt_pk_bf16_f32 v228, v228, v229\n\t"
"v_cvt_pk_bf16_f32 v229, v230, v231\n\t"
"v_mfma_f32_16x16x32_bf16 v[78:81], a[144:147], a[184:187], v[78:81]\n\t"
"ds_read_b128 a[180:183], v244 offset:10752\n\t"
"v_mfma_f32_16x16x32_bf16 v[78:81], a[224:227], a[188:191], v[78:81]\n\t"
"s_add_u32 s29, s22, 1180032\n\t"
"buffer_store_dwordx4 a[112:115], v248, s[16:19], s29 offen offset:128 nt\n\t"
"v_mfma_f32_16x16x32_bf16 v[82:85], a[212:215], a[184:187], 0\n\t"
"ds_read_b128 a[184:187], v244 offset:11264\n\t"
"v_mfma_f32_16x16x32_bf16 v[82:85], a[144:147], a[188:191], v[82:85]\n\t"
"v_cvt_pk_bf16_f32 v230, v236, v237\n\t"
"v_cvt_pk_bf16_f32 v231, v238, v239\n\t"
"v_mfma_f32_16x16x32_bf16 v[82:85], a[224:227], a[192:195], v[82:85]\n\t"
"v_mfma_f32_16x16x32_bf16 v[86:89], a[212:215], a[188:191], 0\n\t"
"ds_read_b128 a[188:191], v244 offset:11776\n\t"
"v_mfma_f32_16x16x32_bf16 v[86:89], a[144:147], a[192:195], v[86:89]\n\t"
"v_mfma_f32_16x16x32_bf16 v[86:89], a[224:227], a[196:199], v[86:89]\n\t"
"ds_write_b64 v250, a[164:165] offset:36864\n\t"
"v_mfma_f32_16x16x32_bf16 v[90:93], a[212:215], a[192:195], 0\n\t"
"ds_read_b128 a[192:195], v244 offset:12288\n\t"
"v_mfma_f32_16x16x32_bf16 v[90:93], a[144:147], a[196:199], v[90:93]\n\t"
"v_mfma_f32_16x16x32_bf16 v[90:93], a[224:227], a[200:203], v[90:93]\n\t"
"buffer_load_dwordx4 a[112:115], v252, s[8:11], s20 offen offset:2048 sc1\n\t"
"v_mfma_f32_16x16x32_bf16 v[94:97], a[212:215], a[196:199], 0\n\t"
"ds_read_b128 a[196:199], v244 offset:12800\n\t"
"v_mfma_f32_16x16x32_bf16 v[94:97], a[144:147], a[200:203], v[94:97]\n\t"
"ds_read_b128 a[200:203], v244 offset:13312\n\t"
"v_mfma_f32_16x16x32_bf16 v[94:97], a[224:227], a[204:207], v[94:97]\n\t"
"ds_read_b128 a[204:207], v244 offset:13824\n\t"
"s_waitcnt vmcnt(26) lgkmcnt(10)\n\t"
"v_mfma_f32_16x16x32_bf16 v[100:103], a[228:231], a[168:171], 0\n\t"
"v_mfma_f32_16x16x32_bf16 v[100:103], a[232:235], a[172:175], v[100:103]\n\t"
"v_mfma_f32_16x16x32_bf16 v[100:103], a[236:239], a[176:179], v[100:103]\n\t"
"s_mov_b32 s20, 417792\n\t"
"buffer_load_dwordx4 a[144:147], v252, s[8:11], s20 offen offset:0 sc1\n\t"
"s_waitcnt lgkmcnt(7)\n\t"
"v_mfma_f32_16x16x32_bf16 v[104:107], a[228:231], a[172:175], 0\n\t"
"v_mfma_f32_16x16x32_bf16 v[104:107], a[232:235], a[176:179], v[104:107]\n\t"
"v_mfma_f32_16x16x32_bf16 v[104:107], a[236:239], a[180:183], v[104:107]\n\t"
"buffer_load_dwordx4 a[212:215], v252, s[8:11], s20 offen offset:1024 sc1\n\t"
"s_waitcnt lgkmcnt(0)\n\t"
"s_barrier\n\t"
"v_mfma_f32_16x16x32_bf16 v[108:111], a[228:231], a[176:179], 0\n\t"
"ds_read_b64_tr_b16 v[232:233], v240 offset:40448\n\t"
"v_mfma_f32_16x16x32_bf16 v[108:111], a[232:235], a[180:183], v[108:111]\n\t"
"ds_read_b64_tr_b16 v[234:235], v240 offset:41984\n\t"
"v_mfma_f32_16x16x32_bf16 v[108:111], a[236:239], a[184:187], v[108:111]\n\t"
"s_add_u32 s29, s22, 1180032\n\t"
"buffer_store_dwordx4 a[120:123], v248, s[16:19], s29 offen offset:256 nt\n\t"
"buffer_load_dwordx4 a[224:227], v252, s[8:11], s20 offen offset:2048 sc1\n\t"
"s_mov_b64 exec, s[44:45]\n\t"
"ds_write_b128 v242, v[224:227] offset:0\n\t"
"s_mov_b64 exec, -1\n\t"
"v_mfma_f32_16x16x32_bf16 v[112:115], a[228:231], a[180:183], 0\n\t"
"ds_read_b64_tr_b16 v[236:237], v241 offset:40448\n\t"
"v_mfma_f32_16x16x32_bf16 v[112:115], a[232:235], a[184:187], v[112:115]\n\t"
"ds_read_b64_tr_b16 v[238:239], v241 offset:41984\n\t"
"v_add_u32 v240, 59904, v240\n\t"
"v_mfma_f32_16x16x32_bf16 v[112:115], a[236:239], a[188:191], v[112:115]\n\t"
"s_mov_b64 exec, s[44:45]\n\t"
"ds_write_b128 v242, v[228:231] offset:1536\n\t"
"s_mov_b64 exec, -1\n\t"
"s_mov_b32 s20, 430080\n\t"
"buffer_load_dwordx4 a[80:83], v252, s[8:11], s20 offen offset:0 sc1\n\t"
"v_mfma_f32_16x16x32_bf16 v[116:119], a[228:231], a[184:187], 0\n\t"
"v_add_u32 v241, 59904, v241\n\t"
"ds_read_b64_tr_b16 v[224:225], v240 offset:512\n\t"
"v_mfma_f32_16x16x32_bf16 v[116:119], a[232:235], a[188:191], v[116:119]\n\t"
"ds_read_b64_tr_b16 v[226:227], v240 offset:2048\n\t"
"v_mfma_f32_16x16x32_bf16 v[116:119], a[236:239], a[192:195], v[116:119]\n\t"
"buffer_load_dwordx4 a[96:99], v252, s[8:11], s20 offen offset:1024 sc1\n\t"
"v_mfma_f32_16x16x32_bf16 v[120:123], a[228:231], a[188:191], 0\n\t"
"ds_read_b64_tr_b16 v[228:229], v241 offset:512\n\t"
"v_mfma_f32_16x16x32_bf16 v[120:123], a[232:235], a[192:195], v[120:123]\n\t"
"ds_read_b64_tr_b16 v[230:231], v241 offset:2048\n\t"
"v_mfma_f32_16x16x32_bf16 v[120:123], a[236:239], a[196:199], v[120:123]\n\t"
"s_add_u32 s29, s22, 1376640\n\t"
"buffer_store_dwordx4 a[32:35], v248, s[16:19], s29 offen offset:0 nt\n\t"
"s_waitcnt lgkmcnt(8)\n\t"
"v_mfma_f32_16x16x32_bf16 v[232:235], v[232:235], a[208:211], 0\n\t"
"s_waitcnt lgkmcnt(5)\n\t"
"v_mfma_f32_16x16x32_bf16 v[236:239], v[236:239], a[208:211], 0\n\t"
"v_mfma_f32_16x16x32_bf16 v[124:127], a[228:231], a[192:195], 0\n\t"
"v_mfma_f32_16x16x32_bf16 v[124:127], a[232:235], a[196:199], v[124:127]\n\t"
"v_mfma_f32_16x16x32_bf16 v[124:127], a[236:239], a[200:203], v[124:127]\n\t"
"v_mfma_f32_16x16x32_bf16 v[128:131], a[228:231], a[196:199], 0\n\t"
"s_nop 1\n\t"
"v_cvt_pk_bf16_f32 v232, v232, v233\n\t"
"v_cvt_pk_bf16_f32 v233, v234, v235\n\t"
"v_mfma_f32_16x16x32_bf16 v[128:131], a[232:235], a[200:203], v[128:131]\n\t"
"v_cvt_pk_bf16_f32 v234, v236, v237\n\t"
"v_cvt_pk_bf16_f32 v235, v238, v239\n\t"
"v_mfma_f32_16x16x32_bf16 v[128:131], a[236:239], a[204:207], v[128:131]\n\t"
"buffer_load_dwordx4 a[228:231], v252, s[8:11], s20 offen offset:2048 sc1\n\t"
"s_waitcnt lgkmcnt(2)\n\t"
"v_mfma_f32_16x16x32_bf16 v[224:227], v[224:227], a[208:211], 0\n\t"
"s_mov_b32 s20, 110592\n\t"
"buffer_load_dwordx4 a[232:235], v252, s[8:11], s20 offen offset:0 sc1\n\t"
"s_waitcnt lgkmcnt(0)\n\t"
"v_mfma_f32_16x16x32_bf16 v[228:231], v[228:231], a[208:211], 0\n\t"
"s_mov_b64 exec, s[44:45]\n\t"
"ds_write_b128 v242, v[232:235] offset:3072\n\t"
"s_mov_b64 exec, -1\n\t"
"s_waitcnt vmcnt(31)\n\t"
"v_mfma_f32_16x16x32_bf16 v[132:135], a[240:243], a[168:171], 0\n\t"
"ds_read_b64_tr_b16 v[236:237], v240 offset:20480\n\t"
"v_mfma_f32_16x16x32_bf16 v[132:135], a[244:247], a[172:175], v[132:135]\n\t"
"ds_read_b64_tr_b16 v[238:239], v240 offset:22016\n\t"
"v_mfma_f32_16x16x32_bf16 v[132:135], a[36:39], a[176:179], v[132:135]\n\t"
"buffer_load_dwordx4 a[236:239], v252, s[8:11], s20 offen offset:1024 sc1\n\t"
"v_mfma_f32_16x16x32_bf16 v[136:139], a[240:243], a[172:175], 0\n\t"
"ds_read_b64_tr_b16 v[232:233], v241 offset:20480\n\t"
"v_cvt_pk_bf16_f32 v224, v224, v225\n\t"
"v_mfma_f32_16x16x32_bf16 v[136:139], a[244:247], a[176:179], v[136:139]\n\t"
"ds_read_b64_tr_b16 v[234:235], v241 offset:22016\n\t"
"v_cvt_pk_bf16_f32 v225, v226, v227\n\t"
"v_mfma_f32_16x16x32_bf16 v[136:139], a[36:39], a[180:183], v[136:139]\n\t"
"v_cvt_pk_bf16_f32 v226, v228, v229\n\t"
"v_cvt_pk_bf16_f32 v227, v230, v231\n\t"
"v_mfma_f32_16x16x32_bf16 v[140:143], a[240:243], a[176:179], 0\n\t"
"ds_read_b64_tr_b16 v[228:229], v240 offset:40448\n\t"
"v_mfma_f32_16x16x32_bf16 v[140:143], a[244:247], a[180:183], v[140:143]\n\t"
"ds_read_b64_tr_b16 v[230:231], v240 offset:41984\n\t"
"v_mfma_f32_16x16x32_bf16 v[140:143], a[36:39], a[184:187], v[140:143]\n\t"
"s_add_u32 s29, s22, 1376640\n\t"
"buffer_store_dwordx4 a[128:131], v248, s[16:19], s29 offen offset:128 nt\n\t"
"v_mfma_f32_16x16x32_bf16 v[144:147], a[240:243], a[180:183], 0\n\t"
"v_mfma_f32_16x16x32_bf16 v[144:147], a[244:247], a[184:187], v[144:147]\n\t"
"v_mfma_f32_16x16x32_bf16 v[144:147], a[36:39], a[188:191], v[144:147]\n\t"
"s_mov_b64 exec, s[44:45]\n\t"
"ds_write_b128 v242, v[224:227] offset:4608\n\t"
"s_mov_b64 exec, -1\n\t"
"s_waitcnt lgkmcnt(5)\n\t"
"v_mfma_f32_16x16x32_bf16 v[236:239], v[236:239], a[208:211], 0\n\t"
"ds_read_b64_tr_b16 v[224:225], v241 offset:40448\n\t"
"ds_read_b64_tr_b16 v[226:227], v241 offset:41984\n\t"
"s_waitcnt lgkmcnt(5)\n\t"
"v_mfma_f32_16x16x32_bf16 v[232:235], v[232:235], a[208:211], 0\n\t"
"v_mfma_f32_16x16x32_bf16 v[148:151], a[240:243], a[184:187], 0\n\t"
"v_mfma_f32_16x16x32_bf16 v[148:151], a[244:247], a[188:191], v[148:151]\n\t"
"v_mfma_f32_16x16x32_bf16 v[148:151], a[36:39], a[192:195], v[148:151]\n\t"
"s_add_u32 s29, s22, 1376640\n\t"
"buffer_store_dwordx4 a[136:139], v248, s[16:19], s29 offen offset:256 nt\n\t"
"v_mfma_f32_16x16x32_bf16 v[152:155], a[240:243], a[188:191], 0\n\t"
"v_cvt_pk_bf16_f32 v236, v236, v237\n\t"
"v_mfma_f32_16x16x32_bf16 v[152:155], a[244:247], a[192:195], v[152:155]\n\t"
"v_cvt_pk_bf16_f32 v237, v238, v239\n\t"
"v_cvt_pk_bf16_f32 v238, v232, v233\n\t"
"v_mfma_f32_16x16x32_bf16 v[152:155], a[36:39], a[196:199], v[152:155]\n\t"
"v_cvt_pk_bf16_f32 v239, v234, v235\n\t"
"s_waitcnt lgkmcnt(3)\n\t"
"v_mfma_f32_16x16x32_bf16 v[228:231], v[228:231], a[208:211], 0\n\t"
"s_mov_b64 exec, s[44:45]\n\t"
"ds_write_b128 v242, v[236:239] offset:6144\n\t"
"s_mov_b64 exec, -1\n\t"
"s_waitcnt lgkmcnt(1)\n\t"
"v_mfma_f32_16x16x32_bf16 v[224:227], v[224:227], a[208:211], 0\n\t"
"ds_read_b128 a[16:19], v252 offset:18432\n\t"
"ds_read_b128 a[48:51], v252 offset:30720\n\t"
"v_mfma_f32_16x16x32_bf16 v[156:159], a[240:243], a[192:195], 0\n\t"
"ds_read_b128 a[24:27], v252 offset:19456\n\t"
"v_mfma_f32_16x16x32_bf16 v[156:159], a[244:247], a[196:199], v[156:159]\n\t"
"ds_read_b128 a[56:59], v252 offset:31744\n\t"
"v_mfma_f32_16x16x32_bf16 v[156:159], a[36:39], a[200:203], v[156:159]\n\t"
"buffer_load_dwordx4 a[208:211], v252, s[8:11], s20 offen offset:2048 sc1\n\t"
"v_mfma_f32_16x16x32_bf16 v[160:163], a[240:243], a[196:199], 0\n\t"
"v_cvt_pk_bf16_f32 v228, v228, v229\n\t"
"v_cvt_pk_bf16_f32 v229, v230, v231\n\t"
"v_mfma_f32_16x16x32_bf16 v[160:163], a[244:247], a[200:203], v[160:163]\n\t"
"v_cvt_pk_bf16_f32 v230, v224, v225\n\t"
"v_cvt_pk_bf16_f32 v231, v226, v227\n\t"
"v_mfma_f32_16x16x32_bf16 v[160:163], a[36:39], a[204:207], v[160:163]\n\t"
"s_mov_b32 s20, 122880\n\t"
"buffer_load_dwordx4 a[240:243], v252, s[8:11], s20 offen offset:0 sc1\n\t"
"s_waitcnt vmcnt(32)\n\t"
"v_mfma_f32_16x16x32_bf16 v[164:167], a[160:163], a[168:171], 0\n\t"
"ds_read_b128 a[32:35], v252 offset:20480\n\t"
"v_mfma_f32_16x16x32_bf16 v[164:167], a[152:155], a[172:175], v[164:167]\n\t"
"ds_read_b128 a[64:67], v252 offset:32768\n\t"
"v_mfma_f32_16x16x32_bf16 v[164:167], a[248:251], a[176:179], v[164:167]\n\t"
"buffer_load_dwordx4 a[244:247], v252, s[8:11], s20 offen offset:1024 sc1\n\t"
"v_mfma_f32_16x16x32_bf16 v[168:171], a[160:163], a[172:175], 0\n\t"
"v_mfma_f32_16x16x32_bf16 v[168:171], a[152:155], a[176:179], v[168:171]\n\t"
"v_mfma_f32_16x16x32_bf16 v[168:171], a[248:251], a[180:183], v[168:171]\n\t"
"s_mov_b64 exec, s[44:45]\n\t"
"ds_write_b128 v242, v[228:231] offset:7680\n\t"
"s_mov_b64 exec, -1\n\t"
"v_mfma_f32_16x16x32_bf16 v[172:175], a[160:163], a[176:179], 0\n\t"
"v_mfma_f32_16x16x32_bf16 v[172:175], a[152:155], a[180:183], v[172:175]\n\t"
"v_mfma_f32_16x16x32_bf16 v[172:175], a[248:251], a[184:187], v[172:175]\n\t"
"s_add_u32 s29, s23, 0\n\t"
"buffer_store_dwordx4 v[196:199], v248, s[16:19], s29 offen offset:0 nt\n\t"
"v_mfma_f32_16x16x32_bf16 v[176:179], a[160:163], a[180:183], 0\n\t"
"v_mfma_f32_16x16x32_bf16 v[176:179], a[152:155], a[184:187], v[176:179]\n\t"
"v_mfma_f32_16x16x32_bf16 v[176:179], a[248:251], a[188:191], v[176:179]\n\t"
"s_waitcnt lgkmcnt(0)\n\t"
"s_barrier\n\t"
"v_mfma_f32_16x16x32_bf16 v[180:183], a[160:163], a[184:187], 0\n\t"
"ds_read_b128 a[168:171], v244 offset:0\n\t"
"v_mfma_f32_16x16x32_bf16 v[180:183], a[152:155], a[188:191], v[180:183]\n\t"
"ds_read_b128 a[172:175], v244 offset:512\n\t"
"v_mfma_f32_16x16x32_bf16 v[180:183], a[248:251], a[192:195], v[180:183]\n\t"
"ds_read_b128 a[176:179], v244 offset:1024\n\t"
"ds_read_b128 a[180:183], v244 offset:1536\n\t"
"v_mfma_f32_16x16x32_bf16 v[184:187], a[160:163], a[188:191], 0\n\t"
"ds_read_b128 a[184:187], v244 offset:2048\n\t"
"v_mfma_f32_16x16x32_bf16 v[184:187], a[152:155], a[192:195], v[184:187]\n\t"
"ds_read_b128 a[188:191], v244 offset:2560\n\t"
"v_mfma_f32_16x16x32_bf16 v[184:187], a[248:251], a[196:199], v[184:187]\n\t"
"buffer_load_dwordx4 v[240:243], v252, s[8:11], s20 offen offset:2048 sc1\n\t"
"s_add_u32 s29, s23, 0\n\t"
"buffer_store_dwordx4 v[204:207], v248, s[16:19], s29 offen offset:128 nt\n\t"
"v_mfma_f32_16x16x32_bf16 v[188:191], a[160:163], a[192:195], 0\n\t"
"ds_read_b128 a[192:195], v244 offset:3072\n\t"
"v_mfma_f32_16x16x32_bf16 v[188:191], a[152:155], a[196:199], v[188:191]\n\t"
"v_mfma_f32_16x16x32_bf16 v[188:191], a[248:251], a[200:203], v[188:191]\n\t"
"ds_write_b128 v249, a[44:47] offset:18432\n\t"
"ds_write_b128 v249, a[60:63] offset:30720\n\t"
"v_mfma_f32_16x16x32_bf16 v[192:195], a[160:163], a[196:199], 0\n\t"
"ds_read_b128 a[196:199], v244 offset:3584\n\t"
"v_mfma_f32_16x16x32_bf16 v[192:195], a[152:155], a[200:203], v[192:195]\n\t"
"ds_read_b128 a[200:203], v244 offset:4096\n\t"
"v_mfma_f32_16x16x32_bf16 v[192:195], a[248:251], a[204:207], v[192:195]\n\t"
"s_mov_b32 s20, 135168\n\t"
"buffer_load_dwordx4 a[248:251], v252, s[8:11], s20 offen offset:0 sc1\n\t"
"Lst19_%=:\n\t"
"s_waitcnt vmcnt(33) lgkmcnt(8)\n\t"
"v_mfma_f32_16x16x32_bf16 v[2:5], a[252:255], a[168:171], v[2:5]\n\t"
"ds_read_b128 a[204:207], v244 offset:4608\n\t"
"v_mfma_f32_16x16x32_bf16 v[2:5], a[4:7], a[172:175], v[2:5]\n\t"
"v_mfma_f32_16x16x32_bf16 v[2:5], v[200:203], a[176:179], v[2:5]\n\t"
"ds_write_b64 v250, a[110:111] offset:18432\n\t"
"s_waitcnt lgkmcnt(9)\n\t"
"v_mfma_f32_16x16x32_bf16 v[6:9], a[252:255], a[172:175], v[6:9]\n\t"
"v_mfma_f32_16x16x32_bf16 v[6:9], a[4:7], a[176:179], v[6:9]\n\t"
"v_mfma_f32_16x16x32_bf16 v[6:9], v[200:203], a[180:183], v[6:9]\n\t"
"ds_write_b64 v250, a[126:127] offset:30720\n\t"
"s_waitcnt lgkmcnt(9)\n\t"
"v_mfma_f32_16x16x32_bf16 v[10:13], a[252:255], a[176:179], v[10:13]\n\t"
"v_mfma_f32_16x16x32_bf16 v[10:13], a[4:7], a[180:183], v[10:13]\n\t"
"v_mfma_f32_16x16x32_bf16 v[10:13], v[200:203], a[184:187], v[10:13]\n\t"
"s_add_u32 s29, s23, 0\n\t"
"buffer_store_dwordx4 v[212:215], v248, s[16:19], s29 offen offset:256 nt\n\t"
"s_waitcnt lgkmcnt(8)\n\t"
"v_mfma_f32_16x16x32_bf16 v[14:17], a[252:255], a[180:183], v[14:17]\n\t"
"v_mfma_f32_16x16x32_bf16 v[14:17], a[4:7], a[184:187], v[14:17]\n\t"
"v_mfma_f32_16x16x32_bf16 v[14:17], v[200:203], a[188:191], v[14:17]\n\t"
"ds_write_b128 v249, a[52:55] offset:24576\n\t"
"s_waitcnt lgkmcnt(8)\n\t"
"v_mfma_f32_16x16x32_bf16 v[18:21], a[252:255], a[184:187], v[18:21]\n\t"
"v_mfma_f32_16x16x32_bf16 v[18:21], a[4:7], a[188:191], v[18:21]\n\t"
"v_mfma_f32_16x16x32_bf16 v[18:21], v[200:203], a[192:195], v[18:21]\n\t"
"ds_write_b128 v249, a[68:71] offset:36864\n\t"
"s_waitcnt lgkmcnt(6)\n\t"
"v_mfma_f32_16x16x32_bf16 v[22:25], a[252:255], a[188:191], v[22:25]\n\t"
"v_mfma_f32_16x16x32_bf16 v[22:25], a[4:7], a[192:195], v[22:25]\n\t"
"v_mfma_f32_16x16x32_bf16 v[22:25], v[200:203], a[196:199], v[22:25]\n\t"
"ds_write_b64 v250, a[118:119] offset:24576\n\t"
"s_waitcnt lgkmcnt(6)\n\t"
"v_mfma_f32_16x16x32_bf16 v[26:29], a[252:255], a[192:195], v[26:29]\n\t"
"v_mfma_f32_16x16x32_bf16 v[26:29], a[4:7], a[196:199], v[26:29]\n\t"
"v_mfma_f32_16x16x32_bf16 v[26:29], v[200:203], a[200:203], v[26:29]\n\t"
"s_add_u32 s29, s23, 393216\n\t"
"buffer_store_dwordx4 a[0:3], v248, s[16:19], s29 offen offset:0 nt\n\t"
"s_waitcnt lgkmcnt(5)\n\t"
"v_mfma_f32_16x16x32_bf16 v[30:33], a[252:255], a[196:199], v[30:33]\n\t"
"v_mfma_f32_16x16x32_bf16 v[30:33], a[4:7], a[200:203], v[30:33]\n\t"
"v_mfma_f32_16x16x32_bf16 v[30:33], v[200:203], a[204:207], v[30:33]\n\t"
"buffer_load_dwordx4 a[252:255], v252, s[8:11], s20 offen offset:1024 sc1\n\t"
"s_waitcnt vmcnt(31)\n\t"
"v_mfma_f32_16x16x32_bf16 v[34:37], v[208:211], a[168:171], v[34:37]\n\t"
"v_mfma_f32_16x16x32_bf16 v[34:37], v[216:219], a[172:175], v[34:37]\n\t"
"v_mfma_f32_16x16x32_bf16 v[34:37], a[12:15], a[176:179], v[34:37]\n\t"
"ds_write_b64 v250, a[134:135] offset:36864\n\t"
"v_mfma_f32_16x16x32_bf16 v[38:41], v[208:211], a[172:175], v[38:41]\n\t"
"ds_read_b64_tr_b16 v[232:233], v246 offset:0\n\t"
"v_mfma_f32_16x16x32_bf16 v[38:41], v[216:219], a[176:179], v[38:41]\n\t"
"ds_read_b64_tr_b16 v[234:235], v246 offset:3072\n\t"
"v_mfma_f32_16x16x32_bf16 v[38:41], a[12:15], a[180:183], v[38:41]\n\t"
"v_mfma_f32_16x16x32_bf16 v[42:45], v[208:211], a[176:179], v[42:45]\n\t"
"v_mfma_f32_16x16x32_bf16 v[42:45], v[216:219], a[180:183], v[42:45]\n\t"
"v_mfma_f32_16x16x32_bf16 v[42:45], a[12:15], a[184:187], v[42:45]\n\t"
"v_mfma_f32_16x16x32_bf16 v[46:49], v[208:211], a[180:183], v[46:49]\n\t"
"v_mfma_f32_16x16x32_bf16 v[46:49], v[216:219], a[184:187], v[46:49]\n\t"
"v_mfma_f32_16x16x32_bf16 v[46:49], a[12:15], a[188:191], v[46:49]\n\t"
"s_add_u32 s29, s23, 393216\n\t"
"buffer_store_dwordx4 a[8:11], v248, s[16:19], s29 offen offset:128 nt\n\t"
"s_waitcnt lgkmcnt(2)\n\t"
"s_barrier\n\t"
"v_mfma_f32_16x16x32_bf16 v[50:53], v[208:211], a[184:187], v[50:53]\n\t"
"ds_read_b64_tr_b16 v[236:237], v246 offset:6144\n\t"
"v_mfma_f32_16x16x32_bf16 v[50:53], v[216:219], a[188:191], v[50:53]\n\t"
"ds_read_b64_tr_b16 v[238:239], v246 offset:9216\n\t"
"v_mfma_f32_16x16x32_bf16 v[50:53], a[12:15], a[192:195], v[50:53]\n\t"
"v_mfma_f32_16x16x32_bf16 v[54:57], v[208:211], a[188:191], v[54:57]\n\t"
"v_mfma_f32_16x16x32_bf16 v[54:57], v[216:219], a[192:195], v[54:57]\n\t"
"v_mfma_f32_16x16x32_bf16 v[54:57], a[12:15], a[196:199], v[54:57]\n\t"
"buffer_load_dwordx4 a[108:111], v252, s[8:11], s20 offen offset:2048 sc1\n\t"
"s_waitcnt lgkmcnt(2)\n\t"
"v_mfma_f32_16x16x32_bf16 v[232:235], v[232:235], v[220:223], 0\n\t"
"v_mfma_f32_16x16x32_bf16 v[58:61], v[208:211], a[192:195], v[58:61]\n\t"
"ds_read_b64_tr_b16 v[224:225], v246 offset:12288\n\t"
"v_mfma_f32_16x16x32_bf16 v[58:61], v[216:219], a[196:199], v[58:61]\n\t"
"ds_read_b64_tr_b16 v[226:227], v246 offset:15360\n\t"
"v_mfma_f32_16x16x32_bf16 v[58:61], a[12:15], a[200:203], v[58:61]\n\t"
"v_mfma_f32_16x16x32_bf16 v[62:65], v[208:211], a[196:199], v[62:65]\n\t"
"v_mfma_f32_16x16x32_bf16 v[62:65], v[216:219], a[200:203], v[62:65]\n\t"
"s_nop 0\n\t"
"v_cvt_pk_bf16_f32 v232, v232, v233\n\t"
"v_mfma_f32_16x16x32_bf16 v[62:65], a[12:15], a[204:207], v[62:65]\n\t"
"v_cvt_pk_bf16_f32 v233, v234, v235\n\t"
"s_waitcnt lgkmcnt(2)\n\t"
"v_mfma_f32_16x16x32_bf16 v[236:239], v[236:239], v[220:223], 0\n\t"
"s_waitcnt vmcnt(29)\n\t"
"v_mfma_f32_16x16x32_bf16 v[66:69], a[20:23], a[168:171], v[66:69]\n\t"
"ds_read_b64_tr_b16 v[228:229], v246 offset:19968\n\t"
"v_mfma_f32_16x16x32_bf16 v[66:69], a[104:107], a[172:175], v[66:69]\n\t"
"ds_read_b64_tr_b16 v[230:231], v246 offset:23040\n\t"
"v_mfma_f32_16x16x32_bf16 v[66:69], a[28:31], a[176:179], v[66:69]\n\t"
"s_add_u32 s29, s23, 393216\n\t"
"buffer_store_dwordx4 a[40:43], v248, s[16:19], s29 offen offset:256 nt\n\t"
"v_mfma_f32_16x16x32_bf16 v[70:73], a[20:23], a[172:175], v[70:73]\n\t"
"ds_read_b128 a[168:171], v244 offset:9216\n\t"
"v_mfma_f32_16x16x32_bf16 v[70:73], a[104:107], a[176:179], v[70:73]\n\t"
"v_cvt_pk_bf16_f32 v236, v236, v237\n\t"
"v_mfma_f32_16x16x32_bf16 v[70:73], a[28:31], a[180:183], v[70:73]\n\t"
"v_cvt_pk_bf16_f32 v237, v238, v239\n\t"
"v_mfma_f32_16x16x32_bf16 v[74:77], a[20:23], a[176:179], v[74:77]\n\t"
"ds_read_b128 a[172:175], v244 offset:9728\n\t"
"v_mfma_f32_16x16x32_bf16 v[74:77], a[104:107], a[180:183], v[74:77]\n\t"
"ds_read_b128 a[176:179], v244 offset:10240\n\t"
"v_mfma_f32_16x16x32_bf16 v[74:77], a[28:31], a[184:187], v[74:77]\n\t"
"s_waitcnt lgkmcnt(5)\n\t"
"v_mfma_f32_16x16x32_bf16 v[224:227], v[224:227], v[220:223], 0\n\t"
"v_mfma_f32_16x16x32_bf16 v[78:81], a[20:23], a[180:183], v[78:81]\n\t"
"ds_read_b128 a[180:183], v244 offset:10752\n\t"
"v_mfma_f32_16x16x32_bf16 v[78:81], a[104:107], a[184:187], v[78:81]\n\t"
"ds_read_b128 a[88:91], v252 offset:18432\n\t"
"v_mfma_f32_16x16x32_bf16 v[78:81], a[28:31], a[188:191], v[78:81]\n\t"
"ds_read_b128 a[4:7], v252 offset:30720\n\t"
"v_mfma_f32_16x16x32_bf16 v[82:85], a[20:23], a[184:187], v[82:85]\n\t"
"ds_read_b128 a[184:187], v244 offset:11264\n\t"
"v_mfma_f32_16x16x32_bf16 v[82:85], a[104:107], a[188:191], v[82:85]\n\t"
"v_cvt_pk_bf16_f32 v224, v224, v225\n\t"
"v_cvt_pk_bf16_f32 v225, v226, v227\n\t"
"v_mfma_f32_16x16x32_bf16 v[82:85], a[28:31], a[192:195], v[82:85]\n\t"
"s_add_u32 s29, s23, 786432\n\t"
"buffer_store_dwordx4 a[16:19], v248, s[16:19], s29 offen offset:0 nt\n\t"
"s_waitcnt lgkmcnt(7)\n\t"
"v_mfma_f32_16x16x32_bf16 v[228:231], v[228:231], v[220:223], 0\n\t"
"ds_read_b128 a[120:123], v252 offset:19456\n\t"
"v_mfma_f32_16x16x32_bf16 v[86:89], a[20:23], a[188:191], v[86:89]\n\t"
"ds_read_b128 a[188:191], v244 offset:11776\n\t"
"v_mfma_f32_16x16x32_bf16 v[86:89], a[104:107], a[192:195], v[86:89]\n\t"
"ds_read_b128 a[36:39], v252 offset:31744\n\t"
"v_mfma_f32_16x16x32_bf16 v[86:89], a[28:31], a[196:199], v[86:89]\n\t"
"ds_read_b128 a[160:163], v252 offset:20480\n\t"
"v_mfma_f32_16x16x32_bf16 v[90:93], a[20:23], a[192:195], v[90:93]\n\t"
"ds_read_b128 a[68:71], v252 offset:32768\n\t"
"v_mfma_f32_16x16x32_bf16 v[90:93], a[104:107], a[196:199], v[90:93]\n\t"
"v_cvt_pk_bf16_f32 v228, v228, v229\n\t"
"v_cvt_pk_bf16_f32 v229, v230, v231\n\t"
"v_mfma_f32_16x16x32_bf16 v[90:93], a[28:31], a[200:203], v[90:93]\n\t"
"ds_read_b128 a[192:195], v244 offset:12288\n\t"
"v_mfma_f32_16x16x32_bf16 v[94:97], a[20:23], a[196:199], v[94:97]\n\t"
"ds_read_b128 a[196:199], v244 offset:12800\n\t"
"v_mfma_f32_16x16x32_bf16 v[94:97], a[104:107], a[200:203], v[94:97]\n\t"
"ds_read_b128 a[200:203], v244 offset:13312\n\t"
"v_mfma_f32_16x16x32_bf16 v[94:97], a[28:31], a[204:207], v[94:97]\n\t"
"ds_read_b128 a[204:207], v244 offset:13824\n\t"
"s_waitcnt vmcnt(26) lgkmcnt(13)\n\t"
"v_mfma_f32_16x16x32_bf16 v[100:103], a[216:219], a[168:171], v[100:103]\n\t"
"v_mfma_f32_16x16x32_bf16 v[100:103], a[220:223], a[172:175], v[100:103]\n\t"
"v_mfma_f32_16x16x32_bf16 v[100:103], a[112:115], a[176:179], v[100:103]\n\t"
"s_add_u32 s29, s23, 786432\n\t"
"buffer_store_dwordx4 a[24:27], v248, s[16:19], s29 offen offset:128 nt\n\t"
"s_waitcnt lgkmcnt(12)\n\t"
"v_mfma_f32_16x16x32_bf16 v[104:107], a[216:219], a[172:175], v[104:107]\n\t"
"v_mfma_f32_16x16x32_bf16 v[104:107], a[220:223], a[176:179], v[104:107]\n\t"
"v_mfma_f32_16x16x32_bf16 v[104:107], a[112:115], a[180:183], v[104:107]\n\t"
"s_waitcnt lgkmcnt(0)\n\t"
"s_barrier\n\t"
"v_mfma_f32_16x16x32_bf16 v[108:111], a[216:219], a[176:179], v[108:111]\n\t"
"v_fma_f32 v196, -v2, s56, -v100\n\t"
"v_fma_f32 v197, -v3, s56, -v101\n\t"
"v_mfma_f32_16x16x32_bf16 v[108:111], a[220:223], a[180:183], v[108:111]\n\t"
"v_fma_f32 v198, -v4, s56, -v102\n\t"
"v_fma_f32 v199, -v5, s56, -v103\n\t"
"v_mfma_f32_16x16x32_bf16 v[108:111], a[112:115], a[184:187], v[108:111]\n\t"
"ds_write_b64 v245, v[232:233] offset:0\n\t"
"v_mfma_f32_16x16x32_bf16 v[112:115], a[216:219], a[180:183], v[112:115]\n\t"
"ds_read_b64_tr_b16 v[232:233], v246 offset:26112\n\t"
"v_fma_f32 v204, -v6, s56, -v104\n\t"
"v_mfma_f32_16x16x32_bf16 v[112:115], a[220:223], a[184:187], v[112:115]\n\t"
"ds_read_b64_tr_b16 v[234:235], v246 offset:29184\n\t"
"v_fma_f32 v205, -v7, s56, -v105\n\t"
"v_mfma_f32_16x16x32_bf16 v[112:115], a[112:115], a[188:191], v[112:115]\n\t"
"ds_write_b64 v245, v[236:237] offset:512\n\t"
"ds_write_b64 v245, v[224:225] offset:1024\n\t"
"v_mfma_f32_16x16x32_bf16 v[116:119], a[216:219], a[184:187], v[116:119]\n\t"
"ds_read_b64_tr_b16 v[236:237], v246 offset:32256\n\t"
"v_fma_f32 v206, -v8, s56, -v106\n\t"
"v_mfma_f32_16x16x32_bf16 v[116:119], a[220:223], a[188:191], v[116:119]\n\t"
"ds_read_b64_tr_b16 v[238:239], v246 offset:35328\n\t"
"v_fma_f32 v207, -v9, s56, -v107\n\t"
"v_mfma_f32_16x16x32_bf16 v[116:119], a[112:115], a[192:195], v[116:119]\n\t"
"ds_write_b64 v245, v[228:229] offset:1536\n\t"
"v_mfma_f32_16x16x32_bf16 v[120:123], a[216:219], a[188:191], v[120:123]\n\t"
"ds_read_b64_tr_b16 v[224:225], v246 offset:39936\n\t"
"v_fma_f32 v212, -v10, s56, -v108\n\t"
"v_mfma_f32_16x16x32_bf16 v[120:123], a[220:223], a[192:195], v[120:123]\n\t"
"ds_read_b64_tr_b16 v[226:227], v246 offset:43008\n\t"
"v_fma_f32 v213, -v11, s56, -v109\n\t"
"v_mfma_f32_16x16x32_bf16 v[120:123], a[112:115], a[196:199], v[120:123]\n\t"
"ds_write_b128 v249, a[76:79] offset:18432\n\t"
"ds_write_b128 v249, a[92:95] offset:30720\n\t"
"s_waitcnt lgkmcnt(9)\n\t"
"v_mfma_f32_16x16x32_bf16 v[232:235], v[232:235], v[220:223], 0\n\t"
"s_add_u32 s29, s23, 786432\n\t"
"buffer_store_dwordx4 a[32:35], v248, s[16:19], s29 offen offset:256 nt\n\t"
"v_mfma_f32_16x16x32_bf16 v[124:127], a[216:219], a[192:195], v[124:127]\n\t"
"ds_read_b64_tr_b16 v[228:229], v246 offset:46080\n\t"
"v_fma_f32 v214, -v12, s56, -v110\n\t"
"v_mfma_f32_16x16x32_bf16 v[124:127], a[220:223], a[196:199], v[124:127]\n\t"
"ds_read_b64_tr_b16 v[230:231], v246 offset:49152\n\t"
"v_fma_f32 v215, -v13, s56, -v111\n\t"
"v_mfma_f32_16x16x32_bf16 v[124:127], a[112:115], a[200:203], v[124:127]\n\t"
"ds_write_b64 v250, a[142:143] offset:18432\n\t"
"s_waitcnt lgkmcnt(8)\n\t"
"v_mfma_f32_16x16x32_bf16 v[236:239], v[236:239], v[220:223], 0\n\t"
"ds_write_b64 v250, a[158:159] offset:30720\n\t"
"v_mfma_f32_16x16x32_bf16 v[128:131], a[216:219], a[196:199], v[128:131]\n\t"
"v_cvt_pk_bf16_f32 v232, v232, v233\n\t"
"v_cvt_pk_bf16_f32 v233, v234, v235\n\t"
"v_mfma_f32_16x16x32_bf16 v[128:131], a[220:223], a[200:203], v[128:131]\n\t"
"v_fma_f32 v98, -v14, s56, -v112\n\t"
"v_fma_f32 v99, -v15, s56, -v113\n\t"
"v_mfma_f32_16x16x32_bf16 v[128:131], a[112:115], a[204:207], v[128:131]\n\t"
"s_mov_b32 s20, 147456\n\t"
"buffer_load_dwordx4 a[216:219], v252, s[8:11], s20 offen offset:0 sc1\n\t"
"s_waitcnt lgkmcnt(6)\n\t"
"v_mfma_f32_16x16x32_bf16 v[224:227], v[224:227], v[220:223], 0\n\t"
"buffer_load_dwordx4 a[220:223], v252, s[8:11], s20 offen offset:1024 sc1\n\t"
"s_waitcnt vmcnt(26)\n\t"
"v_mfma_f32_16x16x32_bf16 v[132:135], a[144:147], a[168:171], v[132:135]\n\t"
"v_cvt_pk_bf16_f32 v236, v236, v237\n\t"
"v_cvt_pk_bf16_f32 v237, v238, v239\n\t"
"v_mfma_f32_16x16x32_bf16 v[132:135], a[212:215], a[172:175], v[132:135]\n\t"
"v_accvgpr_write_b32 a0, v98\n\t"
"v_fma_f32 v98, -v16, s56, -v114\n\t"
"v_mfma_f32_16x16x32_bf16 v[132:135], a[224:227], a[176:179], v[132:135]\n\t"
"ds_write_b64 v245, v[232:233] offset:2048\n\t"
"ds_read_b64_tr_b16 v[232:233], v246 offset:52224\n\t"
"ds_read_b64_tr_b16 v[234:235], v246 offset:55296\n\t"
"s_waitcnt lgkmcnt(5)\n\t"
"v_mfma_f32_16x16x32_bf16 v[228:231], v[228:231], v[220:223], 0\n\t"
"s_add_u32 s29, s23, 1179648\n\t"
"buffer_store_dwordx4 a[48:51], v248, s[16:19], s29 offen offset:0 nt\n\t"
"ds_write_b64 v245, v[236:237] offset:2560\n\t"
"v_mfma_f32_16x16x32_bf16 v[136:139], a[144:147], a[172:175], v[136:139]\n\t"
"v_cvt_pk_bf16_f32 v224, v224, v225\n\t"
"v_cvt_pk_bf16_f32 v225, v226, v227\n\t"
"v_mfma_f32_16x16x32_bf16 v[136:139], a[212:215], a[176:179], v[136:139]\n\t"
"v_accvgpr_write_b32 a1, v99\n\t"
"v_fma_f32 v99, -v17, s56, -v115\n\t"
"v_mfma_f32_16x16x32_bf16 v[136:139], a[224:227], a[180:183], v[136:139]\n\t"
"ds_write_b128 v249, a[84:87] offset:24576\n\t"
"ds_write_b128 v249, a[100:103] offset:36864\n\t"
"v_mfma_f32_16x16x32_bf16 v[140:143], a[144:147], a[176:179], v[140:143]\n\t"
"v_accvgpr_write_b32 a2, v98\n\t"
"v_accvgpr_write_b32 a3, v99\n\t"
"v_mfma_f32_16x16x32_bf16 v[140:143], a[212:215], a[180:183], v[140:143]\n\t"
"v_cvt_pk_bf16_f32 v228, v228, v229\n\t"
"v_cvt_pk_bf16_f32 v229, v230, v231\n\t"
"v_mfma_f32_16x16x32_bf16 v[140:143], a[224:227], a[184:187], v[140:143]\n\t"
"ds_write_b64 v245, v[224:225] offset:3072\n\t"
"buffer_load_dwordx4 v[236:239], v252, s[8:11], s20 offen offset:2048 sc1\n\t"
"v_mfma_f32_16x16x32_bf16 v[144:147], a[144:147], a[180:183], v[144:147]\n\t"
"v_fma_f32 v98, -v18, s56, -v116\n\t"
"v_fma_f32 v99, -v19, s56, -v117\n\t"
"v_mfma_f32_16x16x32_bf16 v[144:147], a[212:215], a[184:187], v[144:147]\n\t"
"v_accvgpr_write_b32 a8, v98\n\t"
"v_fma_f32 v98, -v20, s56, -v118\n\t"
"v_mfma_f32_16x16x32_bf16 v[144:147], a[224:227], a[188:191], v[144:147]\n\t"
"ds_write_b64 v245, v[228:229] offset:3584\n\t"
"ds_write_b64 v250, a[150:151] offset:24576\n\t"
"s_waitcnt lgkmcnt(6)\n\t"
"v_mfma_f32_16x16x32_bf16 v[232:235], v[232:235], v[220:223], 0\n\t"
"ds_write_b64 v250, a[166:167] offset:36864\n\t"
"v_accvgpr_write_b32 a9, v99\n\t"
"v_fma_f32 v99, -v21, s56, -v119\n\t"
"v_mfma_f32_16x16x32_bf16 v[148:151], a[144:147], a[184:187], v[148:151]\n\t"
"v_accvgpr_write_b32 a10, v98\n\t"
"v_accvgpr_write_b32 a11, v99\n\t"
"v_mfma_f32_16x16x32_bf16 v[148:151], a[212:215], a[188:191], v[148:151]\n\t"
"v_fma_f32 v98, -v22, s56, -v120\n\t"
"v_fma_f32 v99, -v23, s56, -v121\n\t"
"v_mfma_f32_16x16x32_bf16 v[148:151], a[224:227], a[192:195], v[148:151]\n\t"
"s_add_u32 s29, s23, 1179648\n\t"
"buffer_store_dwordx4 a[56:59], v248, s[16:19], s29 offen offset:128 nt\n\t"
"v_accvgpr_write_b32 a16, v98\n\t"
"v_fma_f32 v98, -v24, s56, -v122\n\t"
"v_mfma_f32_16x16x32_bf16 v[152:155], a[144:147], a[188:191], v[152:155]\n\t"
"v_accvgpr_write_b32 a17, v99\n\t"
"v_fma_f32 v99, -v25, s56, -v123\n\t"
"v_mfma_f32_16x16x32_bf16 v[152:155], a[212:215], a[192:195], v[152:155]\n\t"
"v_cvt_pk_bf16_f32 v232, v232, v233\n\t"
"v_cvt_pk_bf16_f32 v233, v234, v235\n\t"
"v_mfma_f32_16x16x32_bf16 v[152:155], a[224:227], a[196:199], v[152:155]\n\t"
"ds_write_b64 v245, v[232:233] offset:4096\n\t"
"v_accvgpr_write_b32 a18, v98\n\t"
"v_accvgpr_write_b32 a19, v99\n\t"
"v_mfma_f32_16x16x32_bf16 v[156:159], a[144:147], a[192:195], v[156:159]\n\t"
"v_fma_f32 v98, -v26, s56, -v124\n\t"
"v_fma_f32 v99, -v27, s56, -v125\n\t"
"v_mfma_f32_16x16x32_bf16 v[156:159], a[212:215], a[196:199], v[156:159]\n\t"
"v_accvgpr_write_b32 a24, v98\n\t"
"v_fma_f32 v98, -v28, s56, -v126\n\t"
"v_mfma_f32_16x16x32_bf16 v[156:159], a[224:227], a[200:203], v[156:159]\n\t"
"v_accvgpr_write_b32 a25, v99\n\t"
"v_fma_f32 v99, -v29, s56, -v127\n\t"
"v_accvgpr_write_b32 a26, v98\n\t"
"v_accvgpr_write_b32 a27, v99\n\t"
"v_mfma_f32_16x16x32_bf16 v[160:163], a[144:147], a[196:199], v[160:163]\n\t"
"v_fma_f32 v98, -v30, s56, -v128\n\t"
"v_fma_f32 v99, -v31, s56, -v129\n\t"
"v_mfma_f32_16x16x32_bf16 v[160:163], a[212:215], a[200:203], v[160:163]\n\t"
"v_accvgpr_write_b32 a32, v98\n\t"
"v_fma_f32 v98, -v32, s56, -v130\n\t"
"v_mfma_f32_16x16x32_bf16 v[160:163], a[224:227], a[204:207], v[160:163]\n\t"
"s_mov_b32 s20, 159744\n\t"
"buffer_load_dwordx4 a[212:215], v252, s[8:11], s20 offen offset:0 sc1\n\t"
"v_accvgpr_write_b32 a33, v99\n\t"
"v_fma_f32 v99, -v33, s56, -v131\n\t"
"s_waitcnt vmcnt(26)\n\t"
"v_mfma_f32_16x16x32_bf16 v[164:167], a[80:83], a[168:171], v[164:167]\n\t"
"v_accvgpr_write_b32 a34, v98\n\t"
"v_accvgpr_write_b32 a35, v99\n\t"
"v_mfma_f32_16x16x32_bf16 v[164:167], a[96:99], a[172:175], v[164:167]\n\t"
"v_fma_f32 v98, -v34, s56, -v132\n\t"
"v_fma_f32 v99, -v35, s56, -v133\n\t"
"v_mfma_f32_16x16x32_bf16 v[164:167], a[228:231], a[176:179], v[164:167]\n\t"
"buffer_load_dwordx4 a[224:227], v252, s[8:11], s20 offen offset:1024 sc1\n\t"
"s_add_u32 s29, s23, 1179648\n\t"
"buffer_store_dwordx4 a[64:67], v248, s[16:19], s29 offen offset:256 nt\n\t"
"s_waitcnt lgkmcnt(0)\n\t"
"s_barrier\n\t"
"v_mfma_f32_16x16x32_bf16 v[168:171], a[80:83], a[172:175], v[168:171]\n\t"
"ds_read_b128 a[168:171], v244 offset:0\n\t"
"v_accvgpr_write_b32 a40, v98\n\t"
"v_mfma_f32_16x16x32_bf16 v[168:171], a[96:99], a[176:179], v[168:171]\n\t"
"ds_read_b128 a[172:175], v244 offset:512\n\t"
"v_fma_f32 v98, -v36, s56, -v134\n\t"
"v_mfma_f32_16x16x32_bf16 v[168:171], a[228:231], a[180:183], v[168:171]\n\t"
"v_accvgpr_write_b32 a41, v99\n\t"
"v_fma_f32 v99, -v37, s56, -v135\n\t"
"v_accvgpr_write_b32 a42, v98\n\t"
"v_accvgpr_write_b32 a43, v99\n\t"
"v_mfma_f32_16x16x32_bf16 v[172:175], a[80:83], a[176:179], v[172:175]\n\t"
"ds_read_b128 a[176:179], v244 offset:1024\n\t"
"v_fma_f32 v98, -v38, s56, -v136\n\t"
"v_mfma_f32_16x16x32_bf16 v[172:175], a[96:99], a[180:183], v[172:175]\n\t"
"v_fma_f32 v99, -v39, s56, -v137\n\t"
"v_accvgpr_write_b32 a48, v98\n\t"
"v_mfma_f32_16x16x32_bf16 v[172:175], a[228:231], a[184:187], v[172:175]\n\t"
"v_fma_f32 v98, -v40, s56, -v138\n\t"
"v_accvgpr_write_b32 a49, v99\n\t"
"v_fma_f32 v99, -v41, s56, -v139\n\t"
"v_accvgpr_write_b32 a50, v98\n\t"
"v_mfma_f32_16x16x32_bf16 v[176:179], a[80:83], a[180:183], v[176:179]\n\t"
"ds_read_b128 a[180:183], v244 offset:1536\n\t"
"v_accvgpr_write_b32 a51, v99\n\t"
"v_mfma_f32_16x16x32_bf16 v[176:179], a[96:99], a[184:187], v[176:179]\n\t"
"v_fma_f32 v98, -v42, s56, -v140\n\t"
"v_fma_f32 v99, -v43, s56, -v141\n\t"
"v_mfma_f32_16x16x32_bf16 v[176:179], a[228:231], a[188:191], v[176:179]\n\t"
"v_accvgpr_write_b32 a56, v98\n\t"
"v_fma_f32 v98, -v44, s56, -v142\n\t"
"v_accvgpr_write_b32 a57, v99\n\t"
"v_fma_f32 v99, -v45, s56, -v143\n\t"
"v_mfma_f32_16x16x32_bf16 v[180:183], a[80:83], a[184:187], v[180:183]\n\t"
"ds_read_b128 a[184:187], v244 offset:2048\n\t"
"v_accvgpr_write_b32 a58, v98\n\t"
"v_mfma_f32_16x16x32_bf16 v[180:183], a[96:99], a[188:191], v[180:183]\n\t"
"v_accvgpr_write_b32 a59, v99\n\t"
"v_fma_f32 v98, -v46, s56, -v144\n\t"
"v_mfma_f32_16x16x32_bf16 v[180:183], a[228:231], a[192:195], v[180:183]\n\t"
"v_fma_f32 v99, -v47, s56, -v145\n\t"
"v_accvgpr_write_b32 a64, v98\n\t"
"v_fma_f32 v98, -v48, s56, -v146\n\t"
"v_accvgpr_write_b32 a65, v99\n\t"
"v_mfma_f32_16x16x32_bf16 v[184:187], a[80:83], a[188:191], v[184:187]\n\t"
"ds_read_b128 a[188:191], v244 offset:2560\n\t"
"v_fma_f32 v99, -v49, s56, -v147\n\t"
"v_mfma_f32_16x16x32_bf16 v[184:187], a[96:99], a[192:195], v[184:187]\n\t"
"v_accvgpr_write_b32 a66, v98\n\t"
"v_accvgpr_write_b32 a67, v99\n\t"
"v_mfma_f32_16x16x32_bf16 v[184:187], a[228:231], a[196:199], v[184:187]\n\t"
"s_add_u32 s29, s23, 1152\n\t"
"buffer_store_dwordx4 a[88:91], v248, s[16:19], s29 offen offset:0 nt\n\t"
"v_fma_f32 v98, -v50, s56, -v148\n\t"
"v_fma_f32 v99, -v51, s56, -v149\n\t"
"v_mfma_f32_16x16x32_bf16 v[188:191], a[80:83], a[192:195], v[188:191]\n\t"
"ds_read_b128 a[192:195], v244 offset:3072\n\t"
"v_accvgpr_write_b32 a72, v98\n\t"
"v_mfma_f32_16x16x32_bf16 v[188:191], a[96:99], a[196:199], v[188:191]\n\t"
"v_fma_f32 v98, -v52, s56, -v150\n\t"
"v_accvgpr_write_b32 a73, v99\n\t"
"v_mfma_f32_16x16x32_bf16 v[188:191], a[228:231], a[200:203], v[188:191]\n\t"
"v_fma_f32 v99, -v53, s56, -v151\n\t"
"v_accvgpr_write_b32 a74, v98\n\t"
"v_accvgpr_write_b32 a75, v99\n\t"
"v_fma_f32 v200, v100, s64, -v2\n\t"
"v_mfma_f32_16x16x32_bf16 v[192:195], a[80:83], a[196:199], v[192:195]\n\t"
"ds_read_b128 a[196:199], v244 offset:3584\n\t"
"v_fma_f32 v98, -v54, s56, -v152\n\t"
"v_mfma_f32_16x16x32_bf16 v[192:195], a[96:99], a[200:203], v[192:195]\n\t"
"ds_read_b128 a[200:203], v244 offset:4096\n\t"
"v_fma_f32 v99, -v55, s56, -v153\n\t"
"v_mfma_f32_16x16x32_bf16 v[192:195], a[228:231], a[204:207], v[192:195]\n\t"
"buffer_load_dwordx4 a[228:231], v252, s[8:11], s20 offen offset:2048 sc1\n\t"
"ds_read_b128 a[204:207], v244 offset:4608\n\t"
"v_accvgpr_write_b32 a80, v98\n\t"
"Lst20_%=:\n\t"
"s_waitcnt vmcnt(25) lgkmcnt(7)\n\t"
"v_mfma_f32_16x16x32_bf16 v[196:199], a[232:235], a[168:171], v[196:199]\n\t"
"v_xor_b32 v246, 0xc0, v246\n\t"
"v_fma_f32 v98, -v56, s56, -v154\n\t"
"v_mfma_f32_16x16x32_bf16 v[196:199], a[236:239], a[172:175], v[196:199]\n\t"
"v_accvgpr_write_b32 a81, v99\n\t"
"v_fma_f32 v99, -v57, s56, -v155\n\t"
"v_mfma_f32_16x16x32_bf16 v[196:199], a[208:211], a[176:179], v[196:199]\n\t"
"v_accvgpr_write_b32 a82, v98\n\t"
"v_accvgpr_write_b32 a83, v99\n\t"
"v_fma_f32 v98, -v58, s56, -v156\n\t"
"v_fma_f32 v99, -v59, s56, -v157\n\t"
"s_waitcnt lgkmcnt(6)\n\t"
"v_mfma_f32_16x16x32_bf16 v[204:207], a[232:235], a[172:175], v[204:207]\n\t"
"v_accvgpr_write_b32 a88, v98\n\t"
"v_fma_f32 v98, -v60, s56, -v158\n\t"
"v_mfma_f32_16x16x32_bf16 v[204:207], a[236:239], a[176:179], v[204:207]\n\t"
"v_accvgpr_write_b32 a89, v99\n\t"
"v_fma_f32 v99, -v61, s56, -v159\n\t"
"v_mfma_f32_16x16x32_bf16 v[204:207], a[208:211], a[180:183], v[204:207]\n\t"
"v_accvgpr_write_b32 a90, v98\n\t"
"v_accvgpr_write_b32 a91, v99\n\t"
"v_fma_f32 v98, -v62, s56, -v160\n\t"
"v_fma_f32 v99, -v63, s56, -v161\n\t"
"s_waitcnt lgkmcnt(5)\n\t"
"v_mfma_f32_16x16x32_bf16 v[212:215], a[232:235], a[176:179], v[212:215]\n\t"
"v_accvgpr_write_b32 a96, v98\n\t"
"v_fma_f32 v98, -v64, s56, -v162\n\t"
"v_mfma_f32_16x16x32_bf16 v[212:215], a[236:239], a[180:183], v[212:215]\n\t"
"v_accvgpr_write_b32 a97, v99\n\t"
"v_fma_f32 v99, -v65, s56, -v163\n\t"
"v_mfma_f32_16x16x32_bf16 v[212:215], a[208:211], a[184:187], v[212:215]\n\t"
"s_add_u32 s29, s23, 1152\n\t"
"buffer_store_dwordx4 a[120:123], v248, s[16:19], s29 offen offset:128 nt\n\t"
"v_accvgpr_write_b32 a98, v98\n\t"
"v_accvgpr_write_b32 a99, v99\n\t"
"s_waitcnt lgkmcnt(4)\n\t"
"v_mfma_f32_16x16x32_bf16 a[0:3], a[232:235], a[180:183], a[0:3]\n\t"
"v_fma_f32 v98, -v66, s56, -v164\n\t"
"v_fma_f32 v99, -v67, s56, -v165\n\t"
"v_mfma_f32_16x16x32_bf16 a[0:3], a[236:239], a[184:187], a[0:3]\n\t"
"v_accvgpr_write_b32 a104, v98\n\t"
"v_fma_f32 v98, -v68, s56, -v166\n\t"
"v_mfma_f32_16x16x32_bf16 a[0:3], a[208:211], a[188:191], a[0:3]\n\t"
"v_accvgpr_write_b32 a105, v99\n\t"
"v_fma_f32 v99, -v69, s56, -v167\n\t"
"v_accvgpr_write_b32 a106, v98\n\t"
"v_accvgpr_write_b32 a107, v99\n\t"
"s_waitcnt lgkmcnt(3)\n\t"
"v_mfma_f32_16x16x32_bf16 a[8:11], a[232:235], a[184:187], a[8:11]\n\t"
"v_fma_f32 v98, -v70, s56, -v168\n\t"
"v_fma_f32 v99, -v71, s56, -v169\n\t"
"v_mfma_f32_16x16x32_bf16 a[8:11], a[236:239], a[188:191], a[8:11]\n\t"
"v_accvgpr_write_b32 a112, v98\n\t"
"v_fma_f32 v98, -v72, s56, -v170\n\t"
"v_mfma_f32_16x16x32_bf16 a[8:11], a[208:211], a[192:195], a[8:11]\n\t"
"ds_read_b64_tr_b16 v[224:225], v246 offset:0\n\t"
"ds_read_b64_tr_b16 v[226:227], v246 offset:3072\n\t"
"v_accvgpr_write_b32 a113, v99\n\t"
"v_fma_f32 v99, -v73, s56, -v171\n\t"
"s_waitcnt lgkmcnt(4)\n\t"
"v_mfma_f32_16x16x32_bf16 a[16:19], a[232:235], a[188:191], a[16:19]\n\t"
"v_accvgpr_write_b32 a114, v98\n\t"
"v_accvgpr_write_b32 a115, v99\n\t"
"v_mfma_f32_16x16x32_bf16 a[16:19], a[236:239], a[192:195], a[16:19]\n\t"
"v_fma_f32 v98, -v74, s56, -v172\n\t"
"v_fma_f32 v99, -v75, s56, -v173\n\t"
"v_mfma_f32_16x16x32_bf16 a[16:19], a[208:211], a[196:199], a[16:19]\n\t"
"v_accvgpr_write_b32 a120, v98\n\t"
"v_fma_f32 v98, -v76, s56, -v174\n\t"
"v_accvgpr_write_b32 a121, v99\n\t"
"v_fma_f32 v99, -v77, s56, -v175\n\t"
"s_waitcnt lgkmcnt(3)\n\t"
"v_mfma_f32_16x16x32_bf16 a[24:27], a[232:235], a[192:195], a[24:27]\n\t"
"v_accvgpr_write_b32 a122, v98\n\t"
"v_accvgpr_write_b32 a123, v99\n\t"
"v_mfma_f32_16x16x32_bf16 a[24:27], a[236:239], a[196:199], a[24:27]\n\t"
"v_fma_f32 v98, -v78, s56, -v176\n\t"
"v_fma_f32 v99, -v79, s56, -v177\n\t"
"v_mfma_f32_16x16x32_bf16 a[24:27], a[208:211], a[200:203], a[24:27]\n\t"
"s_add_u32 s29, s23, 1152\n\t"
"buffer_store_dwordx4 a[160:163], v248, s[16:19], s29 offen offset:256 nt\n\t"
"v_accvgpr_write_b32 a128, v98\n\t"
"v_fma_f32 v98, -v80, s56, -v178\n\t"
"s_waitcnt lgkmcnt(2)\n\t"
"v_mfma_f32_16x16x32_bf16 a[32:35], a[232:235], a[196:199], a[32:35]\n\t"
"ds_read_b64_tr_b16 v[228:229], v246 offset:6144\n\t"
"v_accvgpr_write_b32 a129, v99\n\t"
"v_mfma_f32_16x16x32_bf16 a[32:35], a[236:239], a[200:203], a[32:35]\n\t"
"ds_read_b64_tr_b16 v[230:231], v246 offset:9216\n\t"
"v_fma_f32 v99, -v81, s56, -v179\n\t"
"v_mfma_f32_16x16x32_bf16 a[32:35], a[208:211], a[204:207], a[32:35]\n\t"
"s_mov_b32 s20, 172032\n\t"
"buffer_load_dwordx4 a[232:235], v252, s[8:11], s20 offen offset:0 sc1\n\t"
"v_accvgpr_write_b32 a130, v98\n\t"
"v_accvgpr_write_b32 a131, v99\n\t"
"s_waitcnt vmcnt(24)\n\t"
"v_mfma_f32_16x16x32_bf16 a[40:43], a[240:243], a[168:171], a[40:43]\n\t"
"v_fma_f32 v98, -v82, s56, -v180\n\t"
"v_fma_f32 v99, -v83, s56, -v181\n\t"
"v_mfma_f32_16x16x32_bf16 a[40:43], a[244:247], a[172:175], a[40:43]\n\t"
"v_accvgpr_write_b32 a136, v98\n\t"
"v_fma_f32 v98, -v84, s56, -v182\n\t"
"v_mfma_f32_16x16x32_bf16 a[40:43], v[240:243], a[176:179], a[40:43]\n\t"
"buffer_load_dwordx4 a[208:211], v252, s[8:11], s20 offen offset:1024 sc1\n\t"
"v_accvgpr_write_b32 a137, v99\n\t"
"v_fma_f32 v99, -v85, s56, -v183\n\t"
"v_mfma_f32_16x16x32_bf16 a[48:51], a[240:243], a[172:175], a[48:51]\n\t"
"v_accvgpr_write_b32 a138, v98\n\t"
"v_accvgpr_write_b32 a139, v99\n\t"
"v_mfma_f32_16x16x32_bf16 a[48:51], a[244:247], a[176:179], a[48:51]\n\t"
"v_fma_f32 v98, -v86, s56, -v184\n\t"
"v_fma_f32 v99, -v87, s56, -v185\n\t"
"v_mfma_f32_16x16x32_bf16 a[48:51], v[240:243], a[180:183], a[48:51]\n\t"
"buffer_load_dwordx4 a[236:239], v252, s[8:11], s20 offen offset:2048 sc1\n\t"
"v_accvgpr_write_b32 a144, v98\n\t"
"v_fma_f32 v98, -v88, s56, -v186\n\t"
"s_waitcnt lgkmcnt(2)\n\t"
"v_mfma_f32_16x16x32_bf16 v[224:227], v[224:227], v[220:223], 0\n\t"
"v_accvgpr_write_b32 a145, v99\n\t"
"v_fma_f32 v99, -v89, s56, -v187\n\t"
"v_accvgpr_write_b32 a146, v98\n\t"
"v_accvgpr_write_b32 a147, v99\n\t"
"v_mfma_f32_16x16x32_bf16 a[56:59], a[240:243], a[176:179], a[56:59]\n\t"
"v_fma_f32 v98, -v90, s56, -v188\n\t"
"v_fma_f32 v99, -v91, s56, -v189\n\t"
"v_mfma_f32_16x16x32_bf16 a[56:59], a[244:247], a[180:183], a[56:59]\n\t"
"v_accvgpr_write_b32 a152, v98\n\t"
"v_fma_f32 v98, -v92, s56, -v190\n\t"
"v_mfma_f32_16x16x32_bf16 a[56:59], v[240:243], a[184:187], a[56:59]\n\t"
"v_accvgpr_write_b32 a153, v99\n\t"
"v_fma_f32 v99, -v93, s56, -v191\n\t"
"v_accvgpr_write_b32 a154, v98\n\t"
"v_accvgpr_write_b32 a155, v99\n\t"
"v_mfma_f32_16x16x32_bf16 a[64:67], a[240:243], a[180:183], a[64:67]\n\t"
"v_fma_f32 v98, -v94, s56, -v192\n\t"
"v_fma_f32 v99, -v95, s56, -v193\n\t"
"v_mfma_f32_16x16x32_bf16 a[64:67], a[244:247], a[184:187], a[64:67]\n\t"
"v_cvt_pk_bf16_f32 v224, v224, v225\n\t"
"v_cvt_pk_bf16_f32 v225, v226, v227\n\t"
"v_mfma_f32_16x16x32_bf16 a[64:67], v[240:243], a[188:191], a[64:67]\n\t"
"s_add_u32 s29, s23, 394368\n\t"
"buffer_store_dwordx4 a[4:7], v248, s[16:19], s29 offen offset:0 nt\n\t"
"v_accvgpr_write_b32 a160, v98\n\t"
"v_fma_f32 v98, -v96, s56, -v194\n\t"
"s_waitcnt lgkmcnt(0)\n\t"
"v_mfma_f32_16x16x32_bf16 v[228:231], v[228:231], v[220:223], 0\n\t"
"v_accvgpr_write_b32 a161, v99\n\t"
"v_fma_f32 v99, -v97, s56, -v195\n\t"
"v_accvgpr_write_b32 a162, v98\n\t"
"v_accvgpr_write_b32 a163, v99\n\t"
"s_barrier\n\t"
"v_mfma_f32_16x16x32_bf16 a[72:75], a[240:243], a[184:187], a[72:75]\n\t"
"ds_read_b64_tr_b16 v[232:233], v246 offset:12288\n\t"
"v_fma_f32 v201, v101, s64, -v3\n\t"
"v_mfma_f32_16x16x32_bf16 a[72:75], a[244:247], a[188:191], a[72:75]\n\t"
"ds_read_b64_tr_b16 v[234:235], v246 offset:15360\n\t"
"v_fma_f32 v202, v102, s64, -v4\n\t"
"v_mfma_f32_16x16x32_bf16 a[72:75], v[240:243], a[192:195], a[72:75]\n\t"
"v_fma_f32 v203, v103, s64, -v5\n\t"
"v_fma_f32 v208, v104, s64, -v6\n\t"
"v_fma_f32 v209, v105, s64, -v7\n\t"
"v_fma_f32 v210, v106, s64, -v8\n\t"
"v_mfma_f32_16x16x32_bf16 a[80:83], a[240:243], a[188:191], a[80:83]\n\t"
"v_fma_f32 v211, v107, s64, -v9\n\t"
"v_fma_f32 v216, v108, s64, -v10\n\t"
"v_mfma_f32_16x16x32_bf16 a[80:83], a[244:247], a[192:195], a[80:83]\n\t"
"v_cvt_pk_bf16_f32 v226, v228, v229\n\t"
"v_cvt_pk_bf16_f32 v227, v230, v231\n\t"
"v_mfma_f32_16x16x32_bf16 a[80:83], v[240:243], a[196:199], a[80:83]\n\t"
"ds_read_b64_tr_b16 v[228:229], v246 offset:19968\n\t"
"ds_read_b64_tr_b16 v[230:231], v246 offset:23040\n\t"
"v_fma_f32 v217, v109, s64, -v11\n\t"
"v_fma_f32 v218, v110, s64, -v12\n\t"
"v_mfma_f32_16x16x32_bf16 a[88:91], a[240:243], a[192:195], a[88:91]\n\t"
"v_fma_f32 v219, v111, s64, -v13\n\t"
"v_fma_f32 v98, v112, s64, -v14\n\t"
"v_mfma_f32_16x16x32_bf16 a[88:91], a[244:247], a[196:199], a[88:91]\n\t"
"v_fma_f32 v99, v113, s64, -v15\n\t"
"v_accvgpr_write_b32 a4, v98\n\t"
"v_mfma_f32_16x16x32_bf16 a[88:91], v[240:243], a[200:203], a[88:91]\n\t"
"v_fma_f32 v98, v114, s64, -v16\n\t"
"v_accvgpr_write_b32 a5, v99\n\t"
"v_fma_f32 v99, v115, s64, -v17\n\t"
"v_accvgpr_write_b32 a6, v98\n\t"
"s_waitcnt lgkmcnt(2)\n\t"
"v_mfma_f32_16x16x32_bf16 v[232:235], v[232:235], v[220:223], 0\n\t"
"v_accvgpr_write_b32 a7, v99\n\t"
"v_fma_f32 v98, v116, s64, -v18\n\t"
"v_fma_f32 v99, v117, s64, -v19\n\t"
"v_accvgpr_write_b32 a12, v98\n\t"
"v_mfma_f32_16x16x32_bf16 a[96:99], a[240:243], a[196:199], a[96:99]\n\t"
"v_fma_f32 v98, v118, s64, -v20\n\t"
"v_accvgpr_write_b32 a13, v99\n\t"
"v_mfma_f32_16x16x32_bf16 a[96:99], a[244:247], a[200:203], a[96:99]\n\t"
"v_fma_f32 v99, v119, s64, -v21\n\t"
"v_accvgpr_write_b32 a14, v98\n\t"
"v_mfma_f32_16x16x32_bf16 a[96:99], v[240:243], a[204:207], a[96:99]\n\t"
"s_mov_b32 s20, 36864\n\t"
"buffer_load_dwordx4 a[240:243], v252, s[8:11], s20 offen offset:0 sc1\n\t"
"s_add_u32 s29, s23, 394368\n\t"
"buffer_store_dwordx4 a[36:39], v248, s[16:19], s29 offen offset:128 nt\n\t"
"s_waitcnt vmcnt(22)\n\t"
"v_mfma_f32_16x16x32_bf16 a[104:107], a[248:251], a[168:171], a[104:107]\n\t"
"ds_read_b128 a[168:171], v244 offset:9216\n\t"
"v_accvgpr_write_b32 a15, v99\n\t"
"v_mfma_f32_16x16x32_bf16 a[104:107], a[252:255], a[172:175], a[104:107]\n\t"
"v_cvt_pk_bf16_f32 v232, v232, v233\n\t"
"v_cvt_pk_bf16_f32 v233, v234, v235\n\t"
"v_mfma_f32_16x16x32_bf16 a[104:107], a[108:111], a[176:179], a[104:107]\n\t"
"buffer_load_dwordx4 v[240:243], v252, s[8:11], s20 offen offset:1024 sc1\n\t"
"v_fma_f32 v98, v120, s64, -v22\n\t"
"v_fma_f32 v99, v121, s64, -v23\n\t"
"v_mfma_f32_16x16x32_bf16 a[112:115], a[248:251], a[172:175], a[112:115]\n\t"
"ds_read_b128 a[172:175], v244 offset:9728\n\t"
"v_accvgpr_write_b32 a20, v98\n\t"
"v_mfma_f32_16x16x32_bf16 a[112:115], a[252:255], a[176:179], a[112:115]\n\t"
"v_fma_f32 v98, v122, s64, -v24\n\t"
"v_accvgpr_write_b32 a21, v99\n\t"
"v_mfma_f32_16x16x32_bf16 a[112:115], a[108:111], a[180:183], a[112:115]\n\t"
"buffer_load_dwordx4 a[244:247], v252, s[8:11], s20 offen offset:2048 sc1\n\t"
"v_fma_f32 v99, v123, s64, -v25\n\t"
"v_accvgpr_write_b32 a22, v98\n\t"
"s_waitcnt lgkmcnt(2)\n\t"
"v_mfma_f32_16x16x32_bf16 v[228:231], v[228:231], v[220:223], 0\n\t"
"v_accvgpr_write_b32 a23, v99\n\t"
"v_fma_f32 v98, v124, s64, -v26\n\t"
"v_fma_f32 v99, v125, s64, -v27\n\t"
"v_accvgpr_write_b32 a28, v98\n\t"
"v_mfma_f32_16x16x32_bf16 a[120:123], a[248:251], a[176:179], a[120:123]\n\t"
"ds_read_b128 a[176:179], v244 offset:10240\n\t"
"v_fma_f32 v98, v126, s64, -v28\n\t"
"v_mfma_f32_16x16x32_bf16 a[120:123], a[252:255], a[180:183], a[120:123]\n\t"
"v_accvgpr_write_b32 a29, v99\n\t"
"v_fma_f32 v99, v127, s64, -v29\n\t"
"v_mfma_f32_16x16x32_bf16 a[120:123], a[108:111], a[184:187], a[120:123]\n\t"
"v_accvgpr_write_b32 a30, v98\n\t"
"v_accvgpr_write_b32 a31, v99\n\t"
"v_fma_f32 v98, v128, s64, -v30\n\t"
"v_fma_f32 v99, v129, s64, -v31\n\t"
"v_mfma_f32_16x16x32_bf16 a[128:131], a[248:251], a[180:183], a[128:131]\n\t"
"ds_read_b128 a[180:183], v244 offset:10752\n\t"
"v_accvgpr_write_b32 a36, v98\n\t"
"v_mfma_f32_16x16x32_bf16 a[128:131], a[252:255], a[184:187], a[128:131]\n\t"
"v_cvt_pk_bf16_f32 v234, v228, v229\n\t"
"v_cvt_pk_bf16_f32 v235, v230, v231\n\t"
"v_mfma_f32_16x16x32_bf16 a[128:131], a[108:111], a[188:191], a[128:131]\n\t"
"ds_read_b64_tr_b16 v[228:229], v246 offset:26112\n\t"
"ds_read_b64_tr_b16 v[230:231], v246 offset:29184\n\t"
"s_add_u32 s29, s23, 394368\n\t"
"buffer_store_dwordx4 a[68:71], v248, s[16:19], s29 offen offset:256 nt\n\t"
"v_mfma_f32_16x16x32_bf16 a[136:139], a[248:251], a[184:187], a[136:139]\n\t"
"ds_read_b128 a[184:187], v244 offset:11264\n\t"
"v_fma_f32 v98, v130, s64, -v32\n\t"
"v_mfma_f32_16x16x32_bf16 a[136:139], a[252:255], a[188:191], a[136:139]\n\t"
"v_accvgpr_write_b32 a37, v99\n\t"
"v_fma_f32 v99, v131, s64, -v33\n\t"
"v_mfma_f32_16x16x32_bf16 a[136:139], a[108:111], a[192:195], a[136:139]\n\t"
"v_accvgpr_write_b32 a38, v98\n\t"
"v_accvgpr_write_b32 a39, v99\n\t"
"v_fma_f32 v98, v132, s64, -v34\n\t"
"v_fma_f32 v99, v133, s64, -v35\n\t"
"v_mfma_f32_16x16x32_bf16 a[144:147], a[248:251], a[188:191], a[144:147]\n\t"
"ds_read_b128 a[188:191], v244 offset:11776\n\t"
"v_accvgpr_write_b32 a44, v98\n\t"
"v_mfma_f32_16x16x32_bf16 a[144:147], a[252:255], a[192:195], a[144:147]\n\t"
"v_fma_f32 v98, v134, s64, -v36\n\t"
"v_accvgpr_write_b32 a45, v99\n\t"
"v_mfma_f32_16x16x32_bf16 a[144:147], a[108:111], a[196:199], a[144:147]\n\t"
"s_mov_b32 s20, 49152\n\t"
"buffer_load_dwordx4 v[248:251], v252, s[8:11], s20 offen offset:0 sc1\n\t"
"v_fma_f32 v99, v135, s64, -v37\n\t"
"v_accvgpr_write_b32 a46, v98\n\t"
"v_mfma_f32_16x16x32_bf16 a[152:155], a[248:251], a[192:195], a[152:155]\n\t"
"ds_read_b128 a[192:195], v244 offset:12288\n\t"
"v_accvgpr_write_b32 a47, v99\n\t"
"v_mfma_f32_16x16x32_bf16 a[152:155], a[252:255], a[196:199], a[152:155]\n\t"
"v_fma_f32 v98, v136, s64, -v38\n\t"
"v_fma_f32 v99, v137, s64, -v39\n\t"
"v_mfma_f32_16x16x32_bf16 a[152:155], a[108:111], a[200:203], a[152:155]\n\t"
"v_accvgpr_write_b32 a52, v98\n\t"
"v_fma_f32 v98, v138, s64, -v40\n\t"
"v_accvgpr_write_b32 a53, v99\n\t"
"v_fma_f32 v99, v139, s64, -v41\n\t"
"v_mfma_f32_16x16x32_bf16 a[160:163], a[248:251], a[196:199], a[160:163]\n\t"
"ds_read_b128 a[196:199], v244 offset:12800\n\t"
"v_accvgpr_write_b32 a54, v98\n\t"
"v_mfma_f32_16x16x32_bf16 a[160:163], a[252:255], a[200:203], a[160:163]\n\t"
"ds_read_b128 a[200:203], v244 offset:13312\n\t"
"v_accvgpr_write_b32 a55, v99\n\t"
"v_mfma_f32_16x16x32_bf16 a[160:163], a[108:111], a[204:207], a[160:163]\n\t"
"buffer_load_dwordx4 a[248:251], v252, s[8:11], s20 offen offset:1024 sc1\n\t"
"ds_read_b128 a[204:207], v244 offset:13824\n\t"
"v_fma_f32 v98, v140, s64, -v42\n\t"
"s_waitcnt lgkmcnt(6)\n\t"
"v_mfma_f32_16x16x32_bf16 v[228:231], v[228:231], v[220:223], 0\n\t"
"buffer_load_dwordx4 a[252:255], v252, s[8:11], s20 offen offset:2048 sc1\n\t"
"v_fma_f32 v99, v141, s64, -v43\n\t"
"v_accvgpr_write_b32 a60, v98\n\t"
"s_waitcnt vmcnt(20)\n\t"
"v_mfma_f32_16x16x32_bf16 v[200:203], a[216:219], a[168:171], v[200:203]\n\t"
"v_fma_f32 v98, v142, s64, -v44\n\t"
"v_accvgpr_write_b32 a61, v99\n\t"
"v_mfma_f32_16x16x32_bf16 v[200:203], a[220:223], a[172:175], v[200:203]\n\t"
"v_fma_f32 v99, v143, s64, -v45\n\t"
"v_accvgpr_write_b32 a62, v98\n\t"
"v_mfma_f32_16x16x32_bf16 v[200:203], v[236:239], a[176:179], v[200:203]\n\t"
"v_accvgpr_write_b32 a63, v99\n\t"
"v_fma_f32 v98, v144, s64, -v46\n\t"
"v_fma_f32 v99, v145, s64, -v47\n\t"
"v_accvgpr_write_b32 a68, v98\n\t"
"v_mfma_f32_16x16x32_bf16 v[208:211], a[216:219], a[172:175], v[208:211]\n\t"
"v_fma_f32 v98, v146, s64, -v48\n\t"
"v_accvgpr_write_b32 a69, v99\n\t"
"v_mfma_f32_16x16x32_bf16 v[208:211], a[220:223], a[176:179], v[208:211]\n\t"
"v_cvt_pk_bf16_f32 v228, v228, v229\n\t"
"v_cvt_pk_bf16_f32 v229, v230, v231\n\t"
"v_mfma_f32_16x16x32_bf16 v[208:211], v[236:239], a[180:183], v[208:211]\n\t"
"v_fma_f32 v99, v147, s64, -v49\n\t"
"v_accvgpr_write_b32 a70, v98\n\t"
"v_accvgpr_write_b32 a71, v99\n\t"
"v_fma_f32 v98, v148, s64, -v50\n\t"
"s_waitcnt lgkmcnt(0)\n\t"
"s_barrier\n\t"
"v_mfma_f32_16x16x32_bf16 v[216:219], a[216:219], a[176:179], v[216:219]\n\t"
"v_fma_f32 v99, v149, s64, -v51\n\t"
"v_accvgpr_write_b32 a76, v98\n\t"
"v_mfma_f32_16x16x32_bf16 v[216:219], a[220:223], a[180:183], v[216:219]\n\t"
"v_fma_f32 v98, v150, s64, -v52\n\t"
"v_accvgpr_write_b32 a77, v99\n\t"
"v_mfma_f32_16x16x32_bf16 v[216:219], v[236:239], a[184:187], v[216:219]\n\t"
"ds_write2st64_b64 v245, v[224:225], v[226:227] offset0:0 offset1:1\n\t"
"ds_write2st64_b64 v245, v[232:233], v[234:235] offset0:2 offset1:3\n\t"
"v_mfma_f32_16x16x32_bf16 a[4:7], a[216:219], a[180:183], a[4:7]\n\t"
"ds_read_b64_tr_b16 v[224:225], v246 offset:32256\n\t"
"v_fma_f32 v99, v151, s64, -v53\n\t"
"v_mfma_f32_16x16x32_bf16 a[4:7], a[220:223], a[184:187], a[4:7]\n\t"
"ds_read_b64_tr_b16 v[226:227], v246 offset:35328\n\t"
"v_accvgpr_write_b32 a78, v98\n\t"
"v_mfma_f32_16x16x32_bf16 a[4:7], v[236:239], a[188:191], a[4:7]\n\t"
"ds_write_b64 v245, v[228:229] offset:2048\n\t"
"ds_read_b64_tr_b16 v[232:233], v246 offset:39936\n\t"
"ds_read_b64_tr_b16 v[234:235], v246 offset:43008\n\t"
"v_mfma_f32_16x16x32_bf16 a[12:15], a[216:219], a[184:187], a[12:15]\n\t"
"ds_read_b64_tr_b16 v[228:229], v246 offset:46080\n\t"
"v_accvgpr_write_b32 a79, v99\n\t"
"v_mfma_f32_16x16x32_bf16 a[12:15], a[220:223], a[188:191], a[12:15]\n\t"
"ds_read_b64_tr_b16 v[230:231], v246 offset:49152\n\t"
"v_fma_f32 v98, v152, s64, -v54\n\t"
"v_mfma_f32_16x16x32_bf16 a[12:15], v[236:239], a[192:195], a[12:15]\n\t"
"v_fma_f32 v99, v153, s64, -v55\n\t"
"v_accvgpr_write_b32 a84, v98\n\t"
"v_fma_f32 v98, v154, s64, -v56\n\t"
"v_accvgpr_write_b32 a85, v99\n\t"
"v_mfma_f32_16x16x32_bf16 a[20:23], a[216:219], a[188:191], a[20:23]\n\t"
"v_fma_f32 v99, v155, s64, -v57\n\t"
"v_accvgpr_write_b32 a86, v98\n\t"
"v_mfma_f32_16x16x32_bf16 a[20:23], a[220:223], a[192:195], a[20:23]\n\t"
"v_accvgpr_write_b32 a87, v99\n\t"
"v_fma_f32 v98, v156, s64, -v58\n\t"
"v_mfma_f32_16x16x32_bf16 a[20:23], v[236:239], a[196:199], a[20:23]\n\t"
"v_fma_f32 v99, v157, s64, -v59\n\t"
"v_accvgpr_write_b32 a92, v98\n\t"
"v_fma_f32 v98, v158, s64, -v60\n\t"
"v_accvgpr_write_b32 a93, v99\n\t"
"s_waitcnt lgkmcnt(5)\n\t"
"v_mfma_f32_16x16x32_bf16 v[224:227], v[224:227], v[220:223], 0\n\t"
"v_fma_f32 v99, v159, s64, -v61\n\t"
"v_accvgpr_write_b32 a94, v98\n\t"
"v_accvgpr_write_b32 a95, v99\n\t"
"v_fma_f32 v98, v160, s64, -v62\n\t"
"v_mfma_f32_16x16x32_bf16 a[28:31], a[216:219], a[192:195], a[28:31]\n\t"
"v_fma_f32 v99, v161, s64, -v63\n\t"
"v_accvgpr_write_b32 a100, v98\n\t"
"v_mfma_f32_16x16x32_bf16 a[28:31], a[220:223], a[196:199], a[28:31]\n\t"
"v_fma_f32 v98, v162, s64, -v64\n\t"
"v_accvgpr_write_b32 a101, v99\n\t"
"v_mfma_f32_16x16x32_bf16 a[28:31], v[236:239], a[200:203], a[28:31]\n\t"
"v_fma_f32 v99, v163, s64, -v65\n\t"
"v_accvgpr_write_b32 a102, v98\n\t"
"v_accvgpr_write_b32 a103, v99\n\t"
"v_fma_f32 v98, v164, s64, -v66\n\t"
"s_waitcnt lgkmcnt(2)\n\t"
"v_mfma_f32_16x16x32_bf16 v[232:235], v[232:235], v[220:223], 0\n\t"
"v_fma_f32 v99, v165, s64, -v67\n\t"
"v_accvgpr_write_b32 a108, v98\n\t"
"v_fma_f32 v98, v166, s64, -v68\n\t"
"v_accvgpr_write_b32 a109, v99\n\t"
"v_mfma_f32_16x16x32_bf16 a[36:39], a[216:219], a[196:199], a[36:39]\n\t"
"v_cvt_pk_bf16_f32 v224, v224, v225\n\t"
"v_cvt_pk_bf16_f32 v225, v226, v227\n\t"
"v_mfma_f32_16x16x32_bf16 a[36:39], a[220:223], a[200:203], a[36:39]\n\t"
"v_fma_f32 v99, v167, s64, -v69\n\t"
"v_accvgpr_write_b32 a110, v98\n\t"
"v_mfma_f32_16x16x32_bf16 a[36:39], v[236:239], a[204:207], a[36:39]\n\t"
"s_mov_b32 s20, 61440\n\t"
"buffer_load_dwordx4 a[216:219], v252, s[8:11], s20 offen offset:0 sc1\n\t"
"v_accvgpr_write_b32 a111, v99\n\t"
"v_fma_f32 v98, v168, s64, -v70\n\t"
"s_waitcnt lgkmcnt(0)\n\t"
"v_mfma_f32_16x16x32_bf16 v[228:231], v[228:231], v[220:223], 0\n\t"
"buffer_load_dwordx4 v[236:239], v252, s[8:11], s20 offen offset:1024 sc1\n\t"
"v_fma_f32 v99, v169, s64, -v71\n\t"
"v_accvgpr_write_b32 a116, v98\n\t"
"s_waitcnt vmcnt(16)\n\t"
"v_mfma_f32_16x16x32_bf16 a[44:47], a[212:215], a[168:171], a[44:47]\n\t"
"v_cvt_pk_bf16_f32 v226, v232, v233\n\t"
"v_cvt_pk_bf16_f32 v227, v234, v235\n\t"
"v_mfma_f32_16x16x32_bf16 a[44:47], a[224:227], a[172:175], a[44:47]\n\t"
"ds_read_b64_tr_b16 v[232:233], v246 offset:52224\n\t"
"v_fma_f32 v98, v170, s64, -v72\n\t"
"v_mfma_f32_16x16x32_bf16 a[44:47], a[228:231], a[176:179], a[44:47]\n\t"
"buffer_load_dwordx4 a[220:223], v252, s[8:11], s20 offen offset:2048 sc1\n\t"
"ds_write2st64_b64 v245, v[224:225], v[226:227] offset0:5 offset1:6\n\t"
"v_mfma_f32_16x16x32_bf16 a[52:55], a[212:215], a[172:175], a[52:55]\n\t"
"ds_read_b64_tr_b16 v[234:235], v246 offset:55296\n\t"
"v_accvgpr_write_b32 a117, v99\n\t"
"v_mfma_f32_16x16x32_bf16 a[52:55], a[224:227], a[176:179], a[52:55]\n\t"
"v_cvt_pk_bf16_f32 v228, v228, v229\n\t"
"v_cvt_pk_bf16_f32 v229, v230, v231\n\t"
"v_mfma_f32_16x16x32_bf16 a[52:55], a[228:231], a[180:183], a[52:55]\n\t"
"v_fma_f32 v99, v171, s64, -v73\n\t"
"v_accvgpr_write_b32 a118, v98\n\t"
"v_accvgpr_write_b32 a119, v99\n\t"
"v_fma_f32 v98, v172, s64, -v74\n\t"
"v_mfma_f32_16x16x32_bf16 a[60:63], a[212:215], a[176:179], a[60:63]\n\t"
"v_fma_f32 v99, v173, s64, -v75\n\t"
"v_accvgpr_write_b32 a124, v98\n\t"
"v_mfma_f32_16x16x32_bf16 a[60:63], a[224:227], a[180:183], a[60:63]\n\t"
"v_fma_f32 v98, v174, s64, -v76\n\t"
"v_accvgpr_write_b32 a125, v99\n\t"
"v_mfma_f32_16x16x32_bf16 a[60:63], a[228:231], a[184:187], a[60:63]\n\t"
"v_fma_f32 v99, v175, s64, -v77\n\t"
"v_accvgpr_write_b32 a126, v98\n\t"
"v_accvgpr_write_b32 a127, v99\n\t"
"v_fma_f32 v98, v176, s64, -v78\n\t"
"v_mfma_f32_16x16x32_bf16 a[68:71], a[212:215], a[180:183], a[68:71]\n\t"
"v_fma_f32 v99, v177, s64, -v79\n\t"
"v_accvgpr_write_b32 a132, v98\n\t"
"v_mfma_f32_16x16x32_bf16 a[68:71], a[224:227], a[184:187], a[68:71]\n\t"
"v_fma_f32 v98, v178, s64, -v80\n\t"
"v_accvgpr_write_b32 a133, v99\n\t"
"v_mfma_f32_16x16x32_bf16 a[68:71], a[228:231], a[188:191], a[68:71]\n\t"
"s_add_u32 m0, s24, 896\n\t"
"s_add_u32 s28, s21, 0\n\t"
"buffer_load_dwordx4 v247, s[12:15], s28 offen offset:128 nt lds\n\t"
"s_add_u32 m0, s24, 60800\n\t"
"s_add_u32 s28, s21, 1769472\n\t"
"buffer_load_dwordx4 v247, s[12:15], s28 offen offset:128 nt lds\n\t"
"v_mfma_f32_16x16x32_bf16 a[76:79], a[212:215], a[184:187], a[76:79]\n\t"
"v_fma_f32 v99, v179, s64, -v81\n\t"
"v_accvgpr_write_b32 a134, v98\n\t"
"v_mfma_f32_16x16x32_bf16 a[76:79], a[224:227], a[188:191], a[76:79]\n\t"
"v_accvgpr_write_b32 a135, v99\n\t"
"v_fma_f32 v98, v180, s64, -v82\n\t"
"v_mfma_f32_16x16x32_bf16 a[76:79], a[228:231], a[192:195], a[76:79]\n\t"
"s_add_u32 m0, s24, 7040\n\t"
"s_add_u32 s28, s21, 196608\n\t"
"buffer_load_dwordx4 v247, s[12:15], s28 offen offset:128 nt lds\n\t"
"v_fma_f32 v99, v181, s64, -v83\n\t"
"v_accvgpr_write_b32 a140, v98\n\t"
"s_waitcnt lgkmcnt(0)\n\t"
"v_mfma_f32_16x16x32_bf16 v[232:235], v[232:235], v[220:223], 0\n\t"
"s_add_u32 m0, s24, 13184\n\t"
"s_add_u32 s28, s21, 393216\n\t"
"buffer_load_dwordx4 v247, s[12:15], s28 offen offset:128 nt lds\n\t"
"v_fma_f32 v98, v182, s64, -v84\n\t"
"v_accvgpr_write_b32 a141, v99\n\t"
"v_mfma_f32_16x16x32_bf16 a[84:87], a[212:215], a[188:191], a[84:87]\n\t"
"v_fma_f32 v99, v183, s64, -v85\n\t"
"v_accvgpr_write_b32 a142, v98\n\t"
"v_mfma_f32_16x16x32_bf16 a[84:87], a[224:227], a[192:195], a[84:87]\n\t"
"v_accvgpr_write_b32 a143, v99\n\t"
"v_fma_f32 v98, v184, s64, -v86\n\t"
"v_mfma_f32_16x16x32_bf16 a[84:87], a[228:231], a[196:199], a[84:87]\n\t"
"s_add_u32 m0, s24, 20864\n\t"
"s_add_u32 s28, s21, 589824\n\t"
"buffer_load_dwordx4 v247, s[12:15], s28 offen offset:128 nt lds\n\t"
"v_fma_f32 v99, v185, s64, -v87\n\t"
"v_accvgpr_write_b32 a148, v98\n\t"
"v_mfma_f32_16x16x32_bf16 a[92:95], a[212:215], a[192:195], a[92:95]\n\t"
"v_fma_f32 v98, v186, s64, -v88\n\t"
"v_accvgpr_write_b32 a149, v99\n\t"
"v_mfma_f32_16x16x32_bf16 a[92:95], a[224:227], a[196:199], a[92:95]\n\t"
"v_cvt_pk_bf16_f32 v230, v232, v233\n\t"
"v_cvt_pk_bf16_f32 v231, v234, v235\n\t"
"v_mfma_f32_16x16x32_bf16 a[92:95], a[228:231], a[200:203], a[92:95]\n\t"
"s_add_u32 m0, s24, 66944\n\t"
"s_add_u32 s28, s21, 1966080\n\t"
"buffer_load_dwordx4 v247, s[12:15], s28 offen offset:128 nt lds\n\t"
"ds_write2st64_b64 v245, v[228:229], v[230:231] offset0:7 offset1:8\n\t"
"v_mfma_f32_16x16x32_bf16 a[100:103], a[212:215], a[196:199], a[100:103]\n\t"
"v_fma_f32 v99, v187, s64, -v89\n\t"
"v_accvgpr_write_b32 a150, v98\n\t"
"v_mfma_f32_16x16x32_bf16 a[100:103], a[224:227], a[200:203], a[100:103]\n\t"
"v_accvgpr_write_b32 a151, v99\n\t"
"v_fma_f32 v98, v188, s64, -v90\n\t"
"v_mfma_f32_16x16x32_bf16 a[100:103], a[228:231], a[204:207], a[100:103]\n\t"
"v_fma_f32 v99, v189, s64, -v91\n\t"
"v_accvgpr_write_b32 a156, v98\n\t"
"v_fma_f32 v98, v190, s64, -v92\n\t"
"v_accvgpr_write_b32 a157, v99\n\t"
"s_waitcnt vmcnt(18)\n\t"
"v_mfma_f32_16x16x32_bf16 a[108:111], a[232:235], a[168:171], a[108:111]\n\t"
"v_fma_f32 v99, v191, s64, -v93\n\t"
"v_accvgpr_write_b32 a158, v98\n\t"
"v_mfma_f32_16x16x32_bf16 a[108:111], a[208:211], a[172:175], a[108:111]\n\t"
"v_accvgpr_write_b32 a159, v99\n\t"
"v_fma_f32 v98, v192, s64, -v94\n\t"
"v_mfma_f32_16x16x32_bf16 a[108:111], a[236:239], a[176:179], a[108:111]\n\t"
"v_fma_f32 v99, v193, s64, -v95\n\t"
"v_accvgpr_write_b32 a164, v98\n\t"
"v_fma_f32 v98, v194, s64, -v96\n\t"
"v_accvgpr_write_b32 a165, v99\n\t"
"s_waitcnt lgkmcnt(0)\n\t"
"s_barrier\n\t"
"v_mfma_f32_16x16x32_bf16 a[116:119], a[232:235], a[172:175], a[116:119]\n\t"
"ds_read_b128 a[168:171], v244 offset:0\n\t"
"v_fma_f32 v99, v195, s64, -v97\n\t"
"v_mfma_f32_16x16x32_bf16 a[116:119], a[208:211], a[176:179], a[116:119]\n\t"
"ds_read_b128 a[172:175], v244 offset:512\n\t"
"v_accvgpr_write_b32 a166, v98\n\t"
"v_mfma_f32_16x16x32_bf16 a[116:119], a[236:239], a[180:183], a[116:119]\n\t"
"v_accvgpr_write_b32 a167, v99\n\t"
"v_fma_f32 v0, v2, s52, v100\n\t"
"v_fma_f32 v1, v3, s52, v101\n\t"
"v_fma_f32 v100, -v100, s66, v2\n\t"
"v_mfma_f32_16x16x32_bf16 a[124:127], a[232:235], a[176:179], a[124:127]\n\t"
"ds_read_b128 a[176:179], v244 offset:1024\n\t"
"v_fma_f32 v2, v4, s52, v102\n\t"
"v_mfma_f32_16x16x32_bf16 a[124:127], a[208:211], a[180:183], a[124:127]\n\t"
"v_fma_f32 v101, -v101, s66, v3\n\t"
"v_fma_f32 v3, v5, s52, v103\n\t"
"v_mfma_f32_16x16x32_bf16 a[124:127], a[236:239], a[184:187], a[124:127]\n\t"
"v_fma_f32 v102, -v102, s66, v4\n\t"
"v_fma_f32 v103, -v103, s66, v5\n\t"
"v_fma_f32 v4, v6, s52, v104\n\t"
"v_fma_f32 v5, v7, s52, v105\n\t"
"v_mfma_f32_16x16x32_bf16 a[132:135], a[232:235], a[180:183], a[132:135]\n\t"
"ds_read_b128 a[180:183], v244 offset:1536\n\t"
"v_fma_f32 v104, -v104, s66, v6\n\t"
"v_mfma_f32_16x16x32_bf16 a[132:135], a[208:211], a[184:187], a[132:135]\n\t"
"v_fma_f32 v6, v8, s52, v106\n\t"
"v_fma_f32 v105, -v105, s66, v7\n\t"
"v_mfma_f32_16x16x32_bf16 a[132:135], a[236:239], a[188:191], a[132:135]\n\t"
"s_mov_b32 s20, 73728\n\t"
"buffer_load_dwordx4 a[212:215], v252, s[8:11], s20 offen offset:0 sc1\n\t"
"v_fma_f32 v7, v9, s52, v107\n\t"
"v_fma_f32 v106, -v106, s66, v8\n\t"
"v_mfma_f32_16x16x32_bf16 a[140:143], a[232:235], a[184:187], a[140:143]\n\t"
"ds_read_b128 a[184:187], v244 offset:2048\n\t"
"v_fma_f32 v107, -v107, s66, v9\n\t"
"v_mfma_f32_16x16x32_bf16 a[140:143], a[208:211], a[188:191], a[140:143]\n\t"
"v_fma_f32 v8, v10, s52, v108\n\t"
"v_fma_f32 v9, v11, s52, v109\n\t"
"v_mfma_f32_16x16x32_bf16 a[140:143], a[236:239], a[192:195], a[140:143]\n\t"
"v_fma_f32 v108, -v108, s66, v10\n\t"
"v_fma_f32 v10, v12, s52, v110\n\t"
"v_fma_f32 v109, -v109, s66, v11\n\t"
"v_fma_f32 v11, v13, s52, v111\n\t"
"v_mfma_f32_16x16x32_bf16 a[148:151], a[232:235], a[188:191], a[148:151]\n\t"
"ds_read_b128 a[188:191], v244 offset:2560\n\t"
"v_fma_f32 v110, -v110, s66, v12\n\t"
"v_mfma_f32_16x16x32_bf16 a[148:151], a[208:211], a[192:195], a[148:151]\n\t"
"v_fma_f32 v111, -v111, s66, v13\n\t"
"v_fma_f32 v12, v14, s52, v112\n\t"
"v_mfma_f32_16x16x32_bf16 a[148:151], a[236:239], a[196:199], a[148:151]\n\t"
"v_fma_f32 v13, v15, s52, v113\n\t"
"v_fma_f32 v112, -v112, s66, v14\n\t"
"v_fma_f32 v14, v16, s52, v114\n\t"
"v_fma_f32 v113, -v113, s66, v15\n\t"
"v_mfma_f32_16x16x32_bf16 a[156:159], a[232:235], a[192:195], a[156:159]\n\t"
"ds_read_b128 a[192:195], v244 offset:3072\n\t"
"v_fma_f32 v15, v17, s52, v115\n\t"
"v_mfma_f32_16x16x32_bf16 a[156:159], a[208:211], a[196:199], a[156:159]\n\t"
"v_fma_f32 v114, -v114, s66, v16\n\t"
"v_fma_f32 v115, -v115, s66, v17\n\t"
"v_mfma_f32_16x16x32_bf16 a[156:159], a[236:239], a[200:203], a[156:159]\n\t"
"v_fma_f32 v16, v18, s52, v116\n\t"
"v_fma_f32 v17, v19, s52, v117\n\t"
"v_fma_f32 v116, -v116, s66, v18\n\t"
"v_fma_f32 v18, v20, s52, v118\n\t"
"v_mfma_f32_16x16x32_bf16 a[164:167], a[232:235], a[196:199], a[164:167]\n\t"
"ds_read_b128 a[196:199], v244 offset:3584\n\t"
"v_fma_f32 v117, -v117, s66, v19\n\t"
"v_mfma_f32_16x16x32_bf16 a[164:167], a[208:211], a[200:203], a[164:167]\n\t"
"ds_read_b128 a[200:203], v244 offset:4096\n\t"
"v_fma_f32 v19, v21, s52, v119\n\t"
"v_mfma_f32_16x16x32_bf16 a[164:167], a[236:239], a[204:207], a[164:167]\n\t"
"ds_read_b128 a[204:207], v244 offset:4608\n\t"
"v_fma_f32 v118, -v118, s66, v20\n\t"
"v_fma_f32 v119, -v119, s66, v21\n\t"
"v_fma_f32 v20, v22, s52, v120\n\t"
"Lst21_%=:\n\t"
"s_waitcnt vmcnt(14) lgkmcnt(7)\n\t"
"v_mfma_f32_16x16x32_bf16 v[0:3], a[240:243], a[168:171], v[0:3]\n\t"
"v_fma_f32 v21, v23, s52, v121\n\t"
"v_fma_f32 v120, -v120, s66, v22\n\t"
"v_mfma_f32_16x16x32_bf16 v[0:3], v[240:243], a[172:175], v[0:3]\n\t"
"v_fma_f32 v22, v24, s52, v122\n\t"
"v_fma_f32 v121, -v121, s66, v23\n\t"
"v_mfma_f32_16x16x32_bf16 v[0:3], a[244:247], a[176:179], v[0:3]\n\t"
"v_fma_f32 v23, v25, s52, v123\n\t"
"v_fma_f32 v122, -v122, s66, v24\n\t"
"v_fma_f32 v123, -v123, s66, v25\n\t"
"v_fma_f32 v24, v26, s52, v124\n\t"
"s_waitcnt lgkmcnt(6)\n\t"
"v_mfma_f32_16x16x32_bf16 v[4:7], a[240:243], a[172:175], v[4:7]\n\t"
"v_fma_f32 v25, v27, s52, v125\n\t"
"v_fma_f32 v124, -v124, s66, v26\n\t"
"v_mfma_f32_16x16x32_bf16 v[4:7], v[240:243], a[176:179], v[4:7]\n\t"
"v_fma_f32 v26, v28, s52, v126\n\t"
"v_fma_f32 v125, -v125, s66, v27\n\t"
"v_mfma_f32_16x16x32_bf16 v[4:7], a[244:247], a[180:183], v[4:7]\n\t"
"v_fma_f32 v27, v29, s52, v127\n\t"
"v_fma_f32 v126, -v126, s66, v28\n\t"
"v_fma_f32 v127, -v127, s66, v29\n\t"
"v_fma_f32 v28, v30, s52, v128\n\t"
"s_waitcnt lgkmcnt(5)\n\t"
"v_mfma_f32_16x16x32_bf16 v[8:11], a[240:243], a[176:179], v[8:11]\n\t"
"v_fma_f32 v29, v31, s52, v129\n\t"
"v_fma_f32 v128, -v128, s66, v30\n\t"
"v_mfma_f32_16x16x32_bf16 v[8:11], v[240:243], a[180:183], v[8:11]\n\t"
"v_fma_f32 v30, v32, s52, v130\n\t"
"v_fma_f32 v129, -v129, s66, v31\n\t"
"v_mfma_f32_16x16x32_bf16 v[8:11], a[244:247], a[184:187], v[8:11]\n\t"
"v_fma_f32 v31, v33, s52, v131\n\t"
"v_fma_f32 v130, -v130, s66, v32\n\t"
"v_fma_f32 v131, -v131, s66, v33\n\t"
"v_fma_f32 v32, v34, s52, v132\n\t"
"s_waitcnt lgkmcnt(4)\n\t"
"v_mfma_f32_16x16x32_bf16 v[12:15], a[240:243], a[180:183], v[12:15]\n\t"
"v_fma_f32 v33, v35, s52, v133\n\t"
"v_fma_f32 v132, -v132, s66, v34\n\t"
"v_mfma_f32_16x16x32_bf16 v[12:15], v[240:243], a[184:187], v[12:15]\n\t"
"v_fma_f32 v34, v36, s52, v134\n\t"
"v_fma_f32 v133, -v133, s66, v35\n\t"
"v_mfma_f32_16x16x32_bf16 v[12:15], a[244:247], a[188:191], v[12:15]\n\t"
"buffer_load_dwordx4 a[208:211], v252, s[8:11], s20 offen offset:1024 sc1\n\t"
"v_fma_f32 v35, v37, s52, v135\n\t"
"v_fma_f32 v134, -v134, s66, v36\n\t"
"s_waitcnt lgkmcnt(3)\n\t"
"v_mfma_f32_16x16x32_bf16 v[16:19], a[240:243], a[184:187], v[16:19]\n\t"
"v_fma_f32 v135, -v135, s66, v37\n\t"
"v_fma_f32 v36, v38, s52, v136\n\t"
"v_mfma_f32_16x16x32_bf16 v[16:19], v[240:243], a[188:191], v[16:19]\n\t"
"v_fma_f32 v37, v39, s52, v137\n\t"
"v_fma_f32 v136, -v136, s66, v38\n\t"
"v_mfma_f32_16x16x32_bf16 v[16:19], a[244:247], a[192:195], v[16:19]\n\t"
"buffer_load_dwordx4 a[224:227], v252, s[8:11], s20 offen offset:2048 sc1\n\t"
"ds_read_b64_tr_b16 v[224:225], v246 offset:512\n\t"
"ds_read_b64_tr_b16 v[226:227], v246 offset:3584\n\t"
"s_waitcnt lgkmcnt(4)\n\t"
"v_mfma_f32_16x16x32_bf16 v[20:23], a[240:243], a[188:191], v[20:23]\n\t"
"v_fma_f32 v38, v40, s52, v138\n\t"
"v_fma_f32 v137, -v137, s66, v39\n\t"
"v_mfma_f32_16x16x32_bf16 v[20:23], v[240:243], a[192:195], v[20:23]\n\t"
"v_fma_f32 v39, v41, s52, v139\n\t"
"v_fma_f32 v138, -v138, s66, v40\n\t"
"v_mfma_f32_16x16x32_bf16 v[20:23], a[244:247], a[196:199], v[20:23]\n\t"
"s_mov_b32 s20, 86016\n\t"
"buffer_load_dwordx4 a[228:231], v252, s[8:11], s20 offen offset:0 sc1\n\t"
"v_fma_f32 v139, -v139, s66, v41\n\t"
"v_fma_f32 v40, v42, s52, v140\n\t"
"s_waitcnt lgkmcnt(3)\n\t"
"v_mfma_f32_16x16x32_bf16 v[24:27], a[240:243], a[192:195], v[24:27]\n\t"
"v_fma_f32 v41, v43, s52, v141\n\t"
"v_fma_f32 v140, -v140, s66, v42\n\t"
"v_mfma_f32_16x16x32_bf16 v[24:27], v[240:243], a[196:199], v[24:27]\n\t"
"v_fma_f32 v42, v44, s52, v142\n\t"
"v_fma_f32 v141, -v141, s66, v43\n\t"
"v_mfma_f32_16x16x32_bf16 v[24:27], a[244:247], a[200:203], v[24:27]\n\t"
"buffer_load_dwordx4 a[232:235], v252, s[8:11], s20 offen offset:1024 sc1\n\t"
"v_fma_f32 v43, v45, s52, v143\n\t"
"v_fma_f32 v142, -v142, s66, v44\n\t"
"s_waitcnt lgkmcnt(2)\n\t"
"v_mfma_f32_16x16x32_bf16 v[28:31], a[240:243], a[196:199], v[28:31]\n\t"
"v_fma_f32 v143, -v143, s66, v45\n\t"
"ds_read_b64_tr_b16 v[232:233], v246 offset:6656\n\t"
"v_mfma_f32_16x16x32_bf16 v[28:31], v[240:243], a[200:203], v[28:31]\n\t"
"ds_read_b64_tr_b16 v[234:235], v246 offset:9728\n\t"
"v_fma_f32 v44, v46, s52, v144\n\t"
"v_mfma_f32_16x16x32_bf16 v[28:31], a[244:247], a[204:207], v[28:31]\n\t"
"buffer_load_dwordx4 v[240:243], v252, s[8:11], s20 offen offset:2048 sc1\n\t"
"v_fma_f32 v45, v47, s52, v145\n\t"
"v_fma_f32 v144, -v144, s66, v46\n\t"
"s_waitcnt vmcnt(15)\n\t"
"v_mfma_f32_16x16x32_bf16 v[32:35], v[248:251], a[168:171], v[32:35]\n\t"
"v_fma_f32 v46, v48, s52, v146\n\t"
"v_fma_f32 v145, -v145, s66, v47\n\t"
"v_mfma_f32_16x16x32_bf16 v[32:35], a[248:251], a[172:175], v[32:35]\n\t"
"v_fma_f32 v47, v49, s52, v147\n\t"
"v_fma_f32 v146, -v146, s66, v48\n\t"
"v_mfma_f32_16x16x32_bf16 v[32:35], a[252:255], a[176:179], v[32:35]\n\t"
"s_mov_b32 s20, 98304\n\t"
"buffer_load_dwordx4 a[236:239], v252, s[8:11], s20 offen offset:0 sc1\n\t"
"v_fma_f32 v147, -v147, s66, v49\n\t"
"v_fma_f32 v48, v50, s52, v148\n\t"
"v_mfma_f32_16x16x32_bf16 v[36:39], v[248:251], a[172:175], v[36:39]\n\t"
"v_fma_f32 v49, v51, s52, v149\n\t"
"v_fma_f32 v148, -v148, s66, v50\n\t"
"v_mfma_f32_16x16x32_bf16 v[36:39], a[248:251], a[176:179], v[36:39]\n\t"
"v_fma_f32 v50, v52, s52, v150\n\t"
"v_fma_f32 v149, -v149, s66, v51\n\t"
"v_mfma_f32_16x16x32_bf16 v[36:39], a[252:255], a[180:183], v[36:39]\n\t"
"buffer_load_dwordx4 a[240:243], v252, s[8:11], s20 offen offset:1024 sc1\n\t"
"v_fma_f32 v51, v53, s52, v151\n\t"
"v_fma_f32 v150, -v150, s66, v52\n\t"
"s_waitcnt lgkmcnt(2)\n\t"
"v_mfma_f32_16x16x32_bf16 v[224:227], v[224:227], v[220:223], 0\n\t"
"v_fma_f32 v151, -v151, s66, v53\n\t"
"v_fma_f32 v52, v54, s52, v152\n\t"
"v_fma_f32 v53, v55, s52, v153\n\t"
"v_fma_f32 v152, -v152, s66, v54\n\t"
"v_mfma_f32_16x16x32_bf16 v[40:43], v[248:251], a[176:179], v[40:43]\n\t"
"v_fma_f32 v54, v56, s52, v154\n\t"
"v_fma_f32 v153, -v153, s66, v55\n\t"
"v_mfma_f32_16x16x32_bf16 v[40:43], a[248:251], a[180:183], v[40:43]\n\t"
"v_fma_f32 v55, v57, s52, v155\n\t"
"v_fma_f32 v154, -v154, s66, v56\n\t"
"v_mfma_f32_16x16x32_bf16 v[40:43], a[252:255], a[184:187], v[40:43]\n\t"
"buffer_load_dwordx4 a[244:247], v252, s[8:11], s20 offen offset:2048 sc1\n\t"
"v_fma_f32 v155, -v155, s66, v57\n\t"
"ds_read_b64_tr_b16 v[228:229], v246 offset:12800\n\t"
"v_mfma_f32_16x16x32_bf16 v[44:47], v[248:251], a[180:183], v[44:47]\n\t"
"ds_read_b64_tr_b16 v[230:231], v246 offset:15872\n\t"
"v_fma_f32 v56, v58, s52, v156\n\t"
"v_mfma_f32_16x16x32_bf16 v[44:47], a[248:251], a[184:187], v[44:47]\n\t"
"v_cvt_pk_bf16_f32 v224, v224, v225\n\t"
"v_cvt_pk_bf16_f32 v225, v226, v227\n\t"
"v_mfma_f32_16x16x32_bf16 v[44:47], a[252:255], a[188:191], v[44:47]\n\t"
"v_fma_f32 v57, v59, s52, v157\n\t"
"v_fma_f32 v156, -v156, s66, v58\n\t"
"v_fma_f32 v58, v60, s52, v158\n\t"
"v_fma_f32 v157, -v157, s66, v59\n\t"
"s_waitcnt lgkmcnt(2)\n\t"
"v_mfma_f32_16x16x32_bf16 v[232:235], v[232:235], v[220:223], 0\n\t"
"v_fma_f32 v59, v61, s52, v159\n\t"
"v_fma_f32 v158, -v158, s66, v60\n\t"
"v_fma_f32 v159, -v159, s66, v61\n\t"
"v_fma_f32 v60, v62, s52, v160\n\t"
"v_mfma_f32_16x16x32_bf16 v[48:51], v[248:251], a[184:187], v[48:51]\n\t"
"v_fma_f32 v61, v63, s52, v161\n\t"
"v_fma_f32 v160, -v160, s66, v62\n\t"
"v_mfma_f32_16x16x32_bf16 v[48:51], a[248:251], a[188:191], v[48:51]\n\t"
"v_fma_f32 v62, v64, s52, v162\n\t"
"v_fma_f32 v161, -v161, s66, v63\n\t"
"v_mfma_f32_16x16x32_bf16 v[48:51], a[252:255], a[192:195], v[48:51]\n\t"
"v_fma_f32 v63, v65, s52, v163\n\t"
"v_fma_f32 v162, -v162, s66, v64\n\t"
"v_fma_f32 v163, -v163, s66, v65\n\t"
"v_fma_f32 v64, v66, s52, v164\n\t"
"v_mfma_f32_16x16x32_bf16 v[52:55], v[248:251], a[188:191], v[52:55]\n\t"
"v_fma_f32 v65, v67, s52, v165\n\t"
"v_fma_f32 v164, -v164, s66, v66\n\t"
"v_mfma_f32_16x16x32_bf16 v[52:55], a[248:251], a[192:195], v[52:55]\n\t"
"v_cvt_pk_bf16_f32 v226, v232, v233\n\t"
"v_cvt_pk_bf16_f32 v227, v234, v235\n\t"
"v_mfma_f32_16x16x32_bf16 v[52:55], a[252:255], a[196:199], v[52:55]\n\t"
"v_fma_f32 v66, v68, s52, v166\n\t"
"v_fma_f32 v165, -v165, s66, v67\n\t"
"v_fma_f32 v67, v69, s52, v167\n\t"
"v_fma_f32 v166, -v166, s66, v68\n\t"
"v_mfma_f32_16x16x32_bf16 v[56:59], v[248:251], a[192:195], v[56:59]\n\t"
"v_fma_f32 v167, -v167, s66, v69\n\t"
"ds_read_b64_tr_b16 v[232:233], v246 offset:20480\n\t"
"v_mfma_f32_16x16x32_bf16 v[56:59], a[248:251], a[196:199], v[56:59]\n\t"
"ds_read_b64_tr_b16 v[234:235], v246 offset:23552\n\t"
"v_fma_f32 v68, v70, s52, v168\n\t"
"v_mfma_f32_16x16x32_bf16 v[56:59], a[252:255], a[200:203], v[56:59]\n\t"
"v_fma_f32 v69, v71, s52, v169\n\t"
"v_fma_f32 v168, -v168, s66, v70\n\t"
"v_fma_f32 v70, v72, s52, v170\n\t"
"v_fma_f32 v169, -v169, s66, v71\n\t"
"s_waitcnt lgkmcnt(2)\n\t"
"v_mfma_f32_16x16x32_bf16 v[228:231], v[228:231], v[220:223], 0\n\t"
"v_fma_f32 v71, v73, s52, v171\n\t"
"v_fma_f32 v170, -v170, s66, v72\n\t"
"v_fma_f32 v171, -v171, s66, v73\n\t"
"v_fma_f32 v72, v74, s52, v172\n\t"
"v_mfma_f32_16x16x32_bf16 v[60:63], v[248:251], a[196:199], v[60:63]\n\t"
"v_fma_f32 v73, v75, s52, v173\n\t"
"v_fma_f32 v172, -v172, s66, v74\n\t"
"v_mfma_f32_16x16x32_bf16 v[60:63], a[248:251], a[200:203], v[60:63]\n\t"
"v_fma_f32 v74, v76, s52, v174\n\t"
"v_fma_f32 v173, -v173, s66, v75\n\t"
"v_mfma_f32_16x16x32_bf16 v[60:63], a[252:255], a[204:207], v[60:63]\n\t"
"s_mov_b32 s20, 258048\n\t"
"buffer_load_dwordx4 v[248:251], v252, s[8:11], s20 offen offset:0 sc1\n\t"
"v_fma_f32 v75, v77, s52, v175\n\t"
"v_fma_f32 v174, -v174, s66, v76\n\t"
"s_waitcnt vmcnt(16)\n\t"
"v_mfma_f32_16x16x32_bf16 v[64:67], a[216:219], a[168:171], v[64:67]\n\t"
"v_fma_f32 v175, -v175, s66, v77\n\t"
"v_fma_f32 v76, v78, s52, v176\n\t"
"v_mfma_f32_16x16x32_bf16 v[64:67], v[236:239], a[172:175], v[64:67]\n\t"
"v_cvt_pk_bf16_f32 v228, v228, v229\n\t"
"v_cvt_pk_bf16_f32 v229, v230, v231\n\t"
"v_mfma_f32_16x16x32_bf16 v[64:67], a[220:223], a[176:179], v[64:67]\n\t"
"buffer_load_dwordx4 a[248:251], v252, s[8:11], s20 offen offset:1024 sc1\n\t"
"v_fma_f32 v77, v79, s52, v177\n\t"
"v_fma_f32 v176, -v176, s66, v78\n\t"
"v_mfma_f32_16x16x32_bf16 v[68:71], a[216:219], a[172:175], v[68:71]\n\t"
"v_fma_f32 v78, v80, s52, v178\n\t"
"v_fma_f32 v177, -v177, s66, v79\n\t"
"v_mfma_f32_16x16x32_bf16 v[68:71], v[236:239], a[176:179], v[68:71]\n\t"
"v_fma_f32 v79, v81, s52, v179\n\t"
"v_fma_f32 v178, -v178, s66, v80\n\t"
"v_mfma_f32_16x16x32_bf16 v[68:71], a[220:223], a[180:183], v[68:71]\n\t"
"buffer_load_dwordx4 a[252:255], v252, s[8:11], s20 offen offset:2048 sc1\n\t"
"v_fma_f32 v179, -v179, s66, v81\n\t"
"v_fma_f32 v80, v82, s52, v180\n\t"
"s_waitcnt lgkmcnt(0)\n\t"
"v_mfma_f32_16x16x32_bf16 v[232:235], v[232:235], v[220:223], 0\n\t"
"v_fma_f32 v81, v83, s52, v181\n\t"
"v_fma_f32 v180, -v180, s66, v82\n\t"
"v_fma_f32 v82, v84, s52, v182\n\t"
"v_fma_f32 v181, -v181, s66, v83\n\t"
"v_mfma_f32_16x16x32_bf16 v[72:75], a[216:219], a[176:179], v[72:75]\n\t"
"v_fma_f32 v83, v85, s52, v183\n\t"
"v_fma_f32 v182, -v182, s66, v84\n\t"
"v_mfma_f32_16x16x32_bf16 v[72:75], v[236:239], a[180:183], v[72:75]\n\t"
"v_fma_f32 v183, -v183, s66, v85\n\t"
"v_fma_f32 v84, v86, s52, v184\n\t"
"v_mfma_f32_16x16x32_bf16 v[72:75], a[220:223], a[184:187], v[72:75]\n\t"
"v_fma_f32 v85, v87, s52, v185\n\t"
"ds_read_b128 a[168:171], v244 offset:9216\n\t"
"ds_read_b128 a[172:175], v244 offset:9728\n\t"
"ds_read_b128 a[176:179], v244 offset:10240\n\t"
"v_mfma_f32_16x16x32_bf16 v[76:79], a[216:219], a[180:183], v[76:79]\n\t"
"v_fma_f32 v184, -v184, s66, v86\n\t"
"v_fma_f32 v86, v88, s52, v186\n\t"
"v_mfma_f32_16x16x32_bf16 v[76:79], v[236:239], a[184:187], v[76:79]\n\t"
"v_fma_f32 v185, -v185, s66, v87\n\t"
"v_fma_f32 v87, v89, s52, v187\n\t"
"v_mfma_f32_16x16x32_bf16 v[76:79], a[220:223], a[188:191], v[76:79]\n\t"
"v_fma_f32 v186, -v186, s66, v88\n\t"
"v_fma_f32 v187, -v187, s66, v89\n\t"
"v_cvt_pk_bf16_f32 v230, v232, v233\n\t"
"v_cvt_pk_bf16_f32 v231, v234, v235\n\t"
"v_mfma_f32_16x16x32_bf16 v[80:83], a[216:219], a[184:187], v[80:83]\n\t"
"v_fma_f32 v88, v90, s52, v188\n\t"
"v_fma_f32 v89, v91, s52, v189\n\t"
"v_mfma_f32_16x16x32_bf16 v[80:83], v[236:239], a[188:191], v[80:83]\n\t"
"ds_read_b128 a[180:183], v244 offset:10752\n\t"
"v_fma_f32 v188, -v188, s66, v90\n\t"
"v_mfma_f32_16x16x32_bf16 v[80:83], a[220:223], a[192:195], v[80:83]\n\t"
"v_fma_f32 v90, v92, s52, v190\n\t"
"v_fma_f32 v189, -v189, s66, v91\n\t"
"v_fma_f32 v91, v93, s52, v191\n\t"
"v_fma_f32 v190, -v190, s66, v92\n\t"
"v_mfma_f32_16x16x32_bf16 v[84:87], a[216:219], a[188:191], v[84:87]\n\t"
"v_fma_f32 v191, -v191, s66, v93\n\t"
"ds_read_b64_tr_b16 v[232:233], v246 offset:26624\n\t"
"v_mfma_f32_16x16x32_bf16 v[84:87], v[236:239], a[192:195], v[84:87]\n\t"
"ds_read_b64_tr_b16 v[234:235], v246 offset:29696\n\t"
"v_fma_f32 v92, v94, s52, v192\n\t"
"v_mfma_f32_16x16x32_bf16 v[84:87], a[220:223], a[196:199], v[84:87]\n\t"
"v_fma_f32 v93, v95, s52, v193\n\t"
"ds_read_b128 a[184:187], v244 offset:11264\n\t"
"v_fma_f32 v192, -v192, s66, v94\n\t"
"v_fma_f32 v94, v96, s52, v194\n\t"
"v_mfma_f32_16x16x32_bf16 v[88:91], a[216:219], a[192:195], v[88:91]\n\t"
"v_fma_f32 v193, -v193, s66, v95\n\t"
"v_fma_f32 v95, v97, s52, v195\n\t"
"v_mfma_f32_16x16x32_bf16 v[88:91], v[236:239], a[196:199], v[88:91]\n\t"
"v_fma_f32 v194, -v194, s66, v96\n\t"
"v_fma_f32 v195, -v195, s66, v97\n\t"
"v_mfma_f32_16x16x32_bf16 v[88:91], a[220:223], a[200:203], v[88:91]\n\t"
"ds_read_b128 a[188:191], v244 offset:11776\n\t"
"ds_read_b128 a[192:195], v244 offset:12288\n\t"
"v_mfma_f32_16x16x32_bf16 v[92:95], a[216:219], a[196:199], v[92:95]\n\t"
"ds_read_b128 a[196:199], v244 offset:12800\n\t"
"v_mfma_f32_16x16x32_bf16 v[92:95], v[236:239], a[200:203], v[92:95]\n\t"
"ds_read_b128 a[200:203], v244 offset:13312\n\t"
"v_mfma_f32_16x16x32_bf16 v[92:95], a[220:223], a[204:207], v[92:95]\n\t"
"s_mov_b32 s20, 270336\n\t"
"buffer_load_dwordx4 v[96:99], v252, s[8:11], s20 offen offset:0 sc1\n\t"
"s_waitcnt lgkmcnt(5)\n\t"
"v_mfma_f32_16x16x32_bf16 v[232:235], v[232:235], v[220:223], 0\n\t"
"buffer_load_dwordx4 v[236:239], v252, s[8:11], s20 offen offset:1024 sc1\n\t"
"s_waitcnt vmcnt(11)\n\t"
"v_mfma_f32_16x16x32_bf16 v[100:103], a[212:215], a[168:171], v[100:103]\n\t"
"ds_read_b128 a[204:207], v244 offset:13824\n\t"
"v_mfma_f32_16x16x32_bf16 v[100:103], a[208:211], a[172:175], v[100:103]\n\t"
"v_mfma_f32_16x16x32_bf16 v[100:103], a[224:227], a[176:179], v[100:103]\n\t"
"buffer_load_dwordx4 a[216:219], v252, s[8:11], s20 offen offset:2048 sc1\n\t"
"v_mfma_f32_16x16x32_bf16 v[104:107], a[212:215], a[172:175], v[104:107]\n\t"
"v_mfma_f32_16x16x32_bf16 v[104:107], a[208:211], a[176:179], v[104:107]\n\t"
"v_cvt_pk_bf16_f32 v232, v232, v233\n\t"
"v_cvt_pk_bf16_f32 v233, v234, v235\n\t"
"v_mfma_f32_16x16x32_bf16 v[104:107], a[224:227], a[180:183], v[104:107]\n\t"
"s_mov_b32 s20, 282624\n\t"
"buffer_load_dwordx4 a[220:223], v252, s[8:11], s20 offen offset:0 sc1\n\t"
"s_waitcnt lgkmcnt(0)\n\t"
"s_barrier\n\t"
"v_mfma_f32_16x16x32_bf16 v[108:111], a[212:215], a[176:179], v[108:111]\n\t"
"v_mfma_f32_16x16x32_bf16 v[108:111], a[208:211], a[180:183], v[108:111]\n\t"
"v_mfma_f32_16x16x32_bf16 v[108:111], a[224:227], a[184:187], v[108:111]\n\t"
"ds_write2st64_b64 v245, v[224:225], v[226:227] offset0:0 offset1:1\n\t"
"v_mfma_f32_16x16x32_bf16 v[112:115], a[212:215], a[180:183], v[112:115]\n\t"
"ds_read_b64_tr_b16 v[224:225], v246 offset:32768\n\t"
"v_mfma_f32_16x16x32_bf16 v[112:115], a[208:211], a[184:187], v[112:115]\n\t"
"ds_read_b64_tr_b16 v[226:227], v246 offset:35840\n\t"
"v_mfma_f32_16x16x32_bf16 v[112:115], a[224:227], a[188:191], v[112:115]\n\t"
"ds_write2st64_b64 v245, v[228:229], v[230:231] offset0:2 offset1:3\n\t"
"v_mfma_f32_16x16x32_bf16 v[116:119], a[212:215], a[184:187], v[116:119]\n\t"
"ds_read_b64_tr_b16 v[228:229], v246 offset:40448\n\t"
"v_mfma_f32_16x16x32_bf16 v[116:119], a[208:211], a[188:191], v[116:119]\n\t"
"ds_read_b64_tr_b16 v[230:231], v246 offset:43520\n\t"
"v_mfma_f32_16x16x32_bf16 v[116:119], a[224:227], a[192:195], v[116:119]\n\t"
"ds_write_b64 v245, v[232:233] offset:2048\n\t"
"v_mfma_f32_16x16x32_bf16 v[120:123], a[212:215], a[188:191], v[120:123]\n\t"
"ds_read_b64_tr_b16 v[232:233], v246 offset:46592\n\t"
"v_mfma_f32_16x16x32_bf16 v[120:123], a[208:211], a[192:195], v[120:123]\n\t"
"ds_read_b64_tr_b16 v[234:235], v246 offset:49664\n\t"
"v_mfma_f32_16x16x32_bf16 v[120:123], a[224:227], a[196:199], v[120:123]\n\t"
"s_waitcnt lgkmcnt(6)\n\t"
"v_mfma_f32_16x16x32_bf16 v[224:227], v[224:227], v[220:223], 0\n\t"
"v_mfma_f32_16x16x32_bf16 v[124:127], a[212:215], a[192:195], v[124:127]\n\t"
"v_mfma_f32_16x16x32_bf16 v[124:127], a[208:211], a[196:199], v[124:127]\n\t"
"v_mfma_f32_16x16x32_bf16 v[124:127], a[224:227], a[200:203], v[124:127]\n\t"
"s_waitcnt lgkmcnt(3)\n\t"
"v_mfma_f32_16x16x32_bf16 v[228:231], v[228:231], v[220:223], 0\n\t"
"v_mfma_f32_16x16x32_bf16 v[128:131], a[212:215], a[196:199], v[128:131]\n\t"
"s_nop 1\n\t"
"v_cvt_pk_bf16_f32 v224, v224, v225\n\t"
"v_cvt_pk_bf16_f32 v225, v226, v227\n\t"
"v_mfma_f32_16x16x32_bf16 v[128:131], a[208:211], a[200:203], v[128:131]\n\t"
"v_mfma_f32_16x16x32_bf16 v[128:131], a[224:227], a[204:207], v[128:131]\n\t"
"s_add_u32 m0, s24, 73088\n\t"
"s_add_u32 s28, s21, 2162688\n\t"
"buffer_load_dwordx4 v247, s[12:15], s28 offen offset:128 nt lds\n\t"
"s_add_u32 m0, s24, 80768\n\t"
"s_add_u32 s28, s21, 2359296\n\t"
"buffer_load_dwordx4 v247, s[12:15], s28 offen offset:128 nt lds\n\t"
"s_waitcnt lgkmcnt(0)\n\t"
"v_mfma_f32_16x16x32_bf16 v[232:235], v[232:235], v[220:223], 0\n\t"
"s_waitcnt vmcnt(12)\n\t"
"v_mfma_f32_16x16x32_bf16 v[132:135], a[228:231], a[168:171], v[132:135]\n\t"
"v_cvt_pk_bf16_f32 v226, v228, v229\n\t"
"v_cvt_pk_bf16_f32 v227, v230, v231\n\t"
"v_mfma_f32_16x16x32_bf16 v[132:135], a[232:235], a[172:175], v[132:135]\n\t"
"ds_read_b64_tr_b16 v[228:229], v246 offset:52736\n\t"
"v_mfma_f32_16x16x32_bf16 v[132:135], v[240:243], a[176:179], v[132:135]\n\t"
"s_add_u32 m0, s24, 27008\n\t"
"s_add_u32 s28, s21, 786432\n\t"
"buffer_load_dwordx4 v247, s[12:15], s28 offen offset:128 nt lds\n\t"
"s_add_u32 m0, s24, 33152\n\t"
"s_add_u32 s28, s21, 983040\n\t"
"buffer_load_dwordx4 v247, s[12:15], s28 offen offset:128 nt lds\n\t"
"v_mfma_f32_16x16x32_bf16 v[136:139], a[228:231], a[172:175], v[136:139]\n\t"
"ds_read_b64_tr_b16 v[230:231], v246 offset:55808\n\t"
"v_mfma_f32_16x16x32_bf16 v[136:139], a[232:235], a[176:179], v[136:139]\n\t"
"v_cvt_pk_bf16_f32 v232, v232, v233\n\t"
"v_cvt_pk_bf16_f32 v233, v234, v235\n\t"
"v_mfma_f32_16x16x32_bf16 v[136:139], v[240:243], a[180:183], v[136:139]\n\t"
"s_add_u32 m0, s24, 40832\n\t"
"s_add_u32 s28, s21, 1179648\n\t"
"buffer_load_dwordx4 v247, s[12:15], s28 offen offset:128 nt lds\n\t"
"v_mfma_f32_16x16x32_bf16 v[140:143], a[228:231], a[176:179], v[140:143]\n\t"
"v_mfma_f32_16x16x32_bf16 v[140:143], a[232:235], a[180:183], v[140:143]\n\t"
"v_mfma_f32_16x16x32_bf16 v[140:143], v[240:243], a[184:187], v[140:143]\n\t"
"s_add_u32 m0, s24, 46976\n\t"
"s_add_u32 s28, s21, 1376256\n\t"
"buffer_load_dwordx4 v247, s[12:15], s28 offen offset:128 nt lds\n\t"
"ds_write2st64_b64 v245, v[224:225], v[226:227] offset0:5 offset1:6\n\t"
"v_mfma_f32_16x16x32_bf16 v[144:147], a[228:231], a[180:183], v[144:147]\n\t"
"v_mfma_f32_16x16x32_bf16 v[144:147], a[232:235], a[184:187], v[144:147]\n\t"
"v_mfma_f32_16x16x32_bf16 v[144:147], v[240:243], a[188:191], v[144:147]\n\t"
"v_mfma_f32_16x16x32_bf16 v[148:151], a[228:231], a[184:187], v[148:151]\n\t"
"v_mfma_f32_16x16x32_bf16 v[148:151], a[232:235], a[188:191], v[148:151]\n\t"
"v_mfma_f32_16x16x32_bf16 v[148:151], v[240:243], a[192:195], v[148:151]\n\t"
"s_waitcnt lgkmcnt(1)\n\t"
"v_mfma_f32_16x16x32_bf16 v[228:231], v[228:231], v[220:223], 0\n\t"
"v_mfma_f32_16x16x32_bf16 v[152:155], a[228:231], a[188:191], v[152:155]\n\t"
"v_mfma_f32_16x16x32_bf16 v[152:155], a[232:235], a[192:195], v[152:155]\n\t"
"v_mfma_f32_16x16x32_bf16 v[152:155], v[240:243], a[196:199], v[152:155]\n\t"
"v_mfma_f32_16x16x32_bf16 v[156:159], a[228:231], a[192:195], v[156:159]\n\t"
"v_mfma_f32_16x16x32_bf16 v[156:159], a[232:235], a[196:199], v[156:159]\n\t"
"s_nop 2\n\t"
"v_cvt_pk_bf16_f32 v234, v228, v229\n\t"
"v_cvt_pk_bf16_f32 v235, v230, v231\n\t"
"v_mfma_f32_16x16x32_bf16 v[156:159], v[240:243], a[200:203], v[156:159]\n\t"
"ds_write2st64_b64 v245, v[232:233], v[234:235] offset0:7 offset1:8\n\t"
"v_mfma_f32_16x16x32_bf16 v[160:163], a[228:231], a[196:199], v[160:163]\n\t"
"v_mfma_f32_16x16x32_bf16 v[160:163], a[232:235], a[200:203], v[160:163]\n\t"
"v_mfma_f32_16x16x32_bf16 v[160:163], v[240:243], a[204:207], v[160:163]\n\t"
"s_waitcnt vmcnt(13)\n\t"
"v_mfma_f32_16x16x32_bf16 v[164:167], a[236:239], a[168:171], v[164:167]\n\t"
"v_mfma_f32_16x16x32_bf16 v[164:167], a[240:243], a[172:175], v[164:167]\n\t"
"v_mfma_f32_16x16x32_bf16 v[164:167], a[244:247], a[176:179], v[164:167]\n\t"
"s_waitcnt lgkmcnt(0)\n\t"
"s_barrier\n\t"
"v_mfma_f32_16x16x32_bf16 v[168:171], a[236:239], a[172:175], v[168:171]\n\t"
"ds_read_b128 a[168:171], v244 offset:0\n\t"
"v_mfma_f32_16x16x32_bf16 v[168:171], a[240:243], a[176:179], v[168:171]\n\t"
"ds_read_b128 a[172:175], v244 offset:512\n\t"
"v_mfma_f32_16x16x32_bf16 v[168:171], a[244:247], a[180:183], v[168:171]\n\t"
"v_mfma_f32_16x16x32_bf16 v[172:175], a[236:239], a[176:179], v[172:175]\n\t"
"ds_read_b128 a[176:179], v244 offset:1024\n\t"
"v_mfma_f32_16x16x32_bf16 v[172:175], a[240:243], a[180:183], v[172:175]\n\t"
"v_mfma_f32_16x16x32_bf16 v[172:175], a[244:247], a[184:187], v[172:175]\n\t"
"buffer_load_dwordx4 v[240:243], v252, s[8:11], s20 offen offset:1024 sc1\n\t"
"v_mfma_f32_16x16x32_bf16 v[176:179], a[236:239], a[180:183], v[176:179]\n\t"
"ds_read_b128 a[180:183], v244 offset:1536\n\t"
"v_mfma_f32_16x16x32_bf16 v[176:179], a[240:243], a[184:187], v[176:179]\n\t"
"v_mfma_f32_16x16x32_bf16 v[176:179], a[244:247], a[188:191], v[176:179]\n\t"
"v_mfma_f32_16x16x32_bf16 v[180:183], a[236:239], a[184:187], v[180:183]\n\t"
"ds_read_b128 a[184:187], v244 offset:2048\n\t"
"v_mfma_f32_16x16x32_bf16 v[180:183], a[240:243], a[188:191], v[180:183]\n\t"
"v_mfma_f32_16x16x32_bf16 v[180:183], a[244:247], a[192:195], v[180:183]\n\t"
"v_mfma_f32_16x16x32_bf16 v[184:187], a[236:239], a[188:191], v[184:187]\n\t"
"ds_read_b128 a[188:191], v244 offset:2560\n\t"
"v_mfma_f32_16x16x32_bf16 v[184:187], a[240:243], a[192:195], v[184:187]\n\t"
"v_mfma_f32_16x16x32_bf16 v[184:187], a[244:247], a[196:199], v[184:187]\n\t"
"v_mfma_f32_16x16x32_bf16 v[188:191], a[236:239], a[192:195], v[188:191]\n\t"
"ds_read_b128 a[192:195], v244 offset:3072\n\t"
"v_mfma_f32_16x16x32_bf16 v[188:191], a[240:243], a[196:199], v[188:191]\n\t"
"v_mfma_f32_16x16x32_bf16 v[188:191], a[244:247], a[200:203], v[188:191]\n\t"
"v_mfma_f32_16x16x32_bf16 v[192:195], a[236:239], a[196:199], v[192:195]\n\t"
"ds_read_b128 a[196:199], v244 offset:3584\n\t"
"v_mfma_f32_16x16x32_bf16 v[192:195], a[240:243], a[200:203], v[192:195]\n\t"
"ds_read_b128 a[200:203], v244 offset:4096\n\t"
"v_mfma_f32_16x16x32_bf16 v[192:195], a[244:247], a[204:207], v[192:195]\n\t"
"Lst22_%=:\n\t"
"s_waitcnt vmcnt(11) lgkmcnt(6)\n\t"
"v_mfma_f32_16x16x32_bf16 v[0:3], v[248:251], a[168:171], v[0:3]\n\t"
"ds_read_b128 a[204:207], v244 offset:4608\n\t"
"v_mfma_f32_16x16x32_bf16 v[0:3], a[248:251], a[172:175], v[0:3]\n\t"
"v_xor_b32 v246, 0xc0, v246\n\t"
"v_mfma_f32_16x16x32_bf16 v[0:3], a[252:255], a[176:179], v[0:3]\n\t"
"s_waitcnt lgkmcnt(6)\n\t"
"v_mfma_f32_16x16x32_bf16 v[4:7], v[248:251], a[172:175], v[4:7]\n\t"
"v_mfma_f32_16x16x32_bf16 v[4:7], a[248:251], a[176:179], v[4:7]\n\t"
"v_mfma_f32_16x16x32_bf16 v[4:7], a[252:255], a[180:183], v[4:7]\n\t"
"s_waitcnt lgkmcnt(5)\n\t"
"v_mfma_f32_16x16x32_bf16 v[8:11], v[248:251], a[176:179], v[8:11]\n\t"
"v_mfma_f32_16x16x32_bf16 v[8:11], a[248:251], a[180:183], v[8:11]\n\t"
"v_mfma_f32_16x16x32_bf16 v[8:11], a[252:255], a[184:187], v[8:11]\n\t"
"s_waitcnt lgkmcnt(4)\n\t"
"v_mfma_f32_16x16x32_bf16 v[12:15], v[248:251], a[180:183], v[12:15]\n\t"
"v_mfma_f32_16x16x32_bf16 v[12:15], a[248:251], a[184:187], v[12:15]\n\t"
"v_mfma_f32_16x16x32_bf16 v[12:15], a[252:255], a[188:191], v[12:15]\n\t"
"buffer_load_dwordx4 a[208:211], v252, s[8:11], s20 offen offset:2048 sc1\n\t"
"s_waitcnt lgkmcnt(3)\n\t"
"v_mfma_f32_16x16x32_bf16 v[16:19], v[248:251], a[184:187], v[16:19]\n\t"
"v_mfma_f32_16x16x32_bf16 v[16:19], a[248:251], a[188:191], v[16:19]\n\t"
"v_mfma_f32_16x16x32_bf16 v[16:19], a[252:255], a[192:195], v[16:19]\n\t"
"ds_read_b64_tr_b16 v[224:225], v246 offset:512\n\t"
"ds_read_b64_tr_b16 v[226:227], v246 offset:3584\n\t"
"s_waitcnt lgkmcnt(4)\n\t"
"v_mfma_f32_16x16x32_bf16 v[20:23], v[248:251], a[188:191], v[20:23]\n\t"
"v_mfma_f32_16x16x32_bf16 v[20:23], a[248:251], a[192:195], v[20:23]\n\t"
"v_mfma_f32_16x16x32_bf16 v[20:23], a[252:255], a[196:199], v[20:23]\n\t"
"s_mov_b32 s20, 294912\n\t"
"buffer_load_dwordx4 a[212:215], v252, s[8:11], s20 offen offset:0 sc1\n\t"
"s_waitcnt lgkmcnt(3)\n\t"
"v_mfma_f32_16x16x32_bf16 v[24:27], v[248:251], a[192:195], v[24:27]\n\t"
"v_mfma_f32_16x16x32_bf16 v[24:27], a[248:251], a[196:199], v[24:27]\n\t"
"v_mfma_f32_16x16x32_bf16 v[24:27], a[252:255], a[200:203], v[24:27]\n\t"
"buffer_load_dwordx4 a[224:227], v252, s[8:11], s20 offen offset:1024 sc1\n\t"
"s_waitcnt lgkmcnt(2)\n\t"
"v_mfma_f32_16x16x32_bf16 v[28:31], v[248:251], a[196:199], v[28:31]\n\t"
"ds_read_b64_tr_b16 v[228:229], v246 offset:6656\n\t"
"v_mfma_f32_16x16x32_bf16 v[28:31], a[248:251], a[200:203], v[28:31]\n\t"
"ds_read_b64_tr_b16 v[230:231], v246 offset:9728\n\t"
"v_mfma_f32_16x16x32_bf16 v[28:31], a[252:255], a[204:207], v[28:31]\n\t"
"s_waitcnt vmcnt(11)\n\t"
"v_mfma_f32_16x16x32_bf16 v[32:35], v[96:99], a[168:171], v[32:35]\n\t"
"v_mfma_f32_16x16x32_bf16 v[32:35], v[236:239], a[172:175], v[32:35]\n\t"
"v_mfma_f32_16x16x32_bf16 v[32:35], a[216:219], a[176:179], v[32:35]\n\t"
"buffer_load_dwordx4 a[228:231], v252, s[8:11], s20 offen offset:2048 sc1\n\t"
"v_mfma_f32_16x16x32_bf16 v[36:39], v[96:99], a[172:175], v[36:39]\n\t"
"v_mfma_f32_16x16x32_bf16 v[36:39], v[236:239], a[176:179], v[36:39]\n\t"
"v_mfma_f32_16x16x32_bf16 v[36:39], a[216:219], a[180:183], v[36:39]\n\t"
"s_mov_b32 s20, 307200\n\t"
"buffer_load_dwordx4 a[232:235], v252, s[8:11], s20 offen offset:0 sc1\n\t"
"s_waitcnt lgkmcnt(2)\n\t"
"v_mfma_f32_16x16x32_bf16 v[224:227], v[224:227], v[220:223], 0\n\t"
"v_mfma_f32_16x16x32_bf16 v[40:43], v[96:99], a[176:179], v[40:43]\n\t"
"ds_read_b64_tr_b16 v[232:233], v246 offset:12800\n\t"
"v_mfma_f32_16x16x32_bf16 v[40:43], v[236:239], a[180:183], v[40:43]\n\t"
"ds_read_b64_tr_b16 v[234:235], v246 offset:15872\n\t"
"v_mfma_f32_16x16x32_bf16 v[40:43], a[216:219], a[184:187], v[40:43]\n\t"
"buffer_load_dwordx4 a[236:239], v252, s[8:11], s20 offen offset:1024 sc1\n\t"
"v_mfma_f32_16x16x32_bf16 v[44:47], v[96:99], a[180:183], v[44:47]\n\t"
"v_mfma_f32_16x16x32_bf16 v[44:47], v[236:239], a[184:187], v[44:47]\n\t"
"v_cvt_pk_bf16_f32 v224, v224, v225\n\t"
"v_cvt_pk_bf16_f32 v225, v226, v227\n\t"
"v_mfma_f32_16x16x32_bf16 v[44:47], a[216:219], a[188:191], v[44:47]\n\t"
"s_waitcnt lgkmcnt(2)\n\t"
"v_mfma_f32_16x16x32_bf16 v[228:231], v[228:231], v[220:223], 0\n\t"
"buffer_load_dwordx4 a[240:243], v252, s[8:11], s20 offen offset:2048 sc1\n\t"
"v_mfma_f32_16x16x32_bf16 v[48:51], v[96:99], a[184:187], v[48:51]\n\t"
"v_mfma_f32_16x16x32_bf16 v[48:51], v[236:239], a[188:191], v[48:51]\n\t"
"v_mfma_f32_16x16x32_bf16 v[48:51], a[216:219], a[192:195], v[48:51]\n\t"
"s_mov_b32 s20, 319488\n\t"
"buffer_load_dwordx4 a[244:247], v252, s[8:11], s20 offen offset:0 sc1\n\t"
"v_mfma_f32_16x16x32_bf16 v[52:55], v[96:99], a[188:191], v[52:55]\n\t"
"v_mfma_f32_16x16x32_bf16 v[52:55], v[236:239], a[192:195], v[52:55]\n\t"
"v_cvt_pk_bf16_f32 v226, v228, v229\n\t"
"v_cvt_pk_bf16_f32 v227, v230, v231\n\t"
"v_mfma_f32_16x16x32_bf16 v[52:55], a[216:219], a[196:199], v[52:55]\n\t"
"global_load_dwordx4 v[248:251], v252, s[48:49] offset:0\n\t"
"v_mfma_f32_16x16x32_bf16 v[56:59], v[96:99], a[192:195], v[56:59]\n\t"
"ds_read_b64_tr_b16 v[228:229], v246 offset:20480\n\t"
"v_mfma_f32_16x16x32_bf16 v[56:59], v[236:239], a[196:199], v[56:59]\n\t"
"ds_read_b64_tr_b16 v[230:231], v246 offset:23552\n\t"
"v_mfma_f32_16x16x32_bf16 v[56:59], a[216:219], a[200:203], v[56:59]\n\t"
"buffer_load_dwordx4 a[248:251], v252, s[8:11], s20 offen offset:1024 sc1\n\t"
"s_waitcnt lgkmcnt(2)\n\t"
"v_mfma_f32_16x16x32_bf16 v[232:235], v[232:235], v[220:223], 0\n\t"
"buffer_load_dwordx4 a[252:255], v252, s[8:11], s20 offen offset:2048 sc1\n\t"
"v_mfma_f32_16x16x32_bf16 v[60:63], v[96:99], a[196:199], v[60:63]\n\t"
"v_mfma_f32_16x16x32_bf16 v[60:63], v[236:239], a[200:203], v[60:63]\n\t"
"v_mfma_f32_16x16x32_bf16 v[60:63], a[216:219], a[204:207], v[60:63]\n\t"
"s_waitcnt vmcnt(10)\n\t"
"v_mfma_f32_16x16x32_bf16 v[64:67], a[220:223], a[168:171], v[64:67]\n\t"
"ds_read_b128 a[168:171], v244 offset:9216\n\t"
"v_mfma_f32_16x16x32_bf16 v[64:67], v[240:243], a[172:175], v[64:67]\n\t"
"v_cvt_pk_bf16_f32 v232, v232, v233\n\t"
"v_cvt_pk_bf16_f32 v233, v234, v235\n\t"
"v_mfma_f32_16x16x32_bf16 v[64:67], a[208:211], a[176:179], v[64:67]\n\t"
"v_mfma_f32_16x16x32_bf16 v[68:71], a[220:223], a[172:175], v[68:71]\n\t"
"ds_read_b128 a[172:175], v244 offset:9728\n\t"
"v_mfma_f32_16x16x32_bf16 v[68:71], v[240:243], a[176:179], v[68:71]\n\t"
"v_mfma_f32_16x16x32_bf16 v[68:71], a[208:211], a[180:183], v[68:71]\n\t"
"s_mov_b32 s20, 331776\n\t"
"buffer_load_dwordx4 v[236:239], v252, s[8:11], s20 offen offset:0 sc1\n\t"
"s_waitcnt lgkmcnt(2)\n\t"
"v_mfma_f32_16x16x32_bf16 v[228:231], v[228:231], v[220:223], 0\n\t"
"buffer_load_dwordx4 v[96:99], v252, s[8:11], s20 offen offset:1024 sc1\n\t"
"v_mfma_f32_16x16x32_bf16 v[72:75], a[220:223], a[176:179], v[72:75]\n\t"
"ds_read_b128 a[176:179], v244 offset:10240\n\t"
"v_mfma_f32_16x16x32_bf16 v[72:75], v[240:243], a[180:183], v[72:75]\n\t"
"v_mfma_f32_16x16x32_bf16 v[72:75], a[208:211], a[184:187], v[72:75]\n\t"
"buffer_load_dwordx4 a[216:219], v252, s[8:11], s20 offen offset:2048 sc1\n\t"
"v_mfma_f32_16x16x32_bf16 v[76:79], a[220:223], a[180:183], v[76:79]\n\t"
"ds_read_b128 a[180:183], v244 offset:10752\n\t"
"v_mfma_f32_16x16x32_bf16 v[76:79], v[240:243], a[184:187], v[76:79]\n\t"
"v_cvt_pk_bf16_f32 v234, v228, v229\n\t"
"v_cvt_pk_bf16_f32 v235, v230, v231\n\t"
"v_mfma_f32_16x16x32_bf16 v[76:79], a[208:211], a[188:191], v[76:79]\n\t"
"ds_read_b64_tr_b16 v[228:229], v246 offset:26624\n\t"
"v_mfma_f32_16x16x32_bf16 v[80:83], a[220:223], a[184:187], v[80:83]\n\t"
"ds_read_b64_tr_b16 v[230:231], v246 offset:29696\n\t"
"v_mfma_f32_16x16x32_bf16 v[80:83], v[240:243], a[188:191], v[80:83]\n\t"
"ds_read_b128 a[184:187], v244 offset:11264\n\t"
"v_mfma_f32_16x16x32_bf16 v[80:83], a[208:211], a[192:195], v[80:83]\n\t"
"v_mfma_f32_16x16x32_bf16 v[84:87], a[220:223], a[188:191], v[84:87]\n\t"
"ds_read_b128 a[188:191], v244 offset:11776\n\t"
"v_mfma_f32_16x16x32_bf16 v[84:87], v[240:243], a[192:195], v[84:87]\n\t"
"v_mfma_f32_16x16x32_bf16 v[84:87], a[208:211], a[196:199], v[84:87]\n\t"
"v_mfma_f32_16x16x32_bf16 v[88:91], a[220:223], a[192:195], v[88:91]\n\t"
"ds_read_b128 a[192:195], v244 offset:12288\n\t"
"v_mfma_f32_16x16x32_bf16 v[88:91], v[240:243], a[196:199], v[88:91]\n\t"
"v_mfma_f32_16x16x32_bf16 v[88:91], a[208:211], a[200:203], v[88:91]\n\t"
"v_mfma_f32_16x16x32_bf16 v[92:95], a[220:223], a[196:199], v[92:95]\n\t"
"ds_read_b128 a[196:199], v244 offset:12800\n\t"
"v_mfma_f32_16x16x32_bf16 v[92:95], v[240:243], a[200:203], v[92:95]\n\t"
"ds_read_b128 a[200:203], v244 offset:13312\n\t"
"v_mfma_f32_16x16x32_bf16 v[92:95], a[208:211], a[204:207], v[92:95]\n\t"
"s_mov_b32 s20, 344064\n\t"
"buffer_load_dwordx4 a[220:223], v252, s[8:11], s20 offen offset:0 sc1\n\t"
"s_waitcnt lgkmcnt(5)\n\t"
"v_mfma_f32_16x16x32_bf16 v[228:231], v[228:231], v[220:223], 0\n\t"
"ds_read_b128 a[204:207], v244 offset:13824\n\t"
"s_waitcnt vmcnt(11)\n\t"
"v_mfma_f32_16x16x32_bf16 v[100:103], a[212:215], a[168:171], v[100:103]\n\t"
"v_mfma_f32_16x16x32_bf16 v[100:103], a[224:227], a[172:175], v[100:103]\n\t"
"v_mfma_f32_16x16x32_bf16 v[100:103], a[228:231], a[176:179], v[100:103]\n\t"
"buffer_load_dwordx4 a[208:211], v252, s[8:11], s20 offen offset:1024 sc1\n\t"
"v_mfma_f32_16x16x32_bf16 v[104:107], a[212:215], a[172:175], v[104:107]\n\t"
"v_mfma_f32_16x16x32_bf16 v[104:107], a[224:227], a[176:179], v[104:107]\n\t"
"v_cvt_pk_bf16_f32 v228, v228, v229\n\t"
"v_cvt_pk_bf16_f32 v229, v230, v231\n\t"
"v_mfma_f32_16x16x32_bf16 v[104:107], a[228:231], a[180:183], v[104:107]\n\t"
"s_waitcnt lgkmcnt(0)\n\t"
"s_barrier\n\t"
"v_mfma_f32_16x16x32_bf16 v[108:111], a[212:215], a[176:179], v[108:111]\n\t"
"v_mfma_f32_16x16x32_bf16 v[108:111], a[224:227], a[180:183], v[108:111]\n\t"
"v_mfma_f32_16x16x32_bf16 v[108:111], a[228:231], a[184:187], v[108:111]\n\t"
"ds_write2st64_b64 v245, v[224:225], v[226:227] offset0:0 offset1:1\n\t"
"v_mfma_f32_16x16x32_bf16 v[112:115], a[212:215], a[180:183], v[112:115]\n\t"
"ds_read_b64_tr_b16 v[224:225], v246 offset:32768\n\t"
"v_mfma_f32_16x16x32_bf16 v[112:115], a[224:227], a[184:187], v[112:115]\n\t"
"ds_read_b64_tr_b16 v[226:227], v246 offset:35840\n\t"
"v_mfma_f32_16x16x32_bf16 v[112:115], a[228:231], a[188:191], v[112:115]\n\t"
"ds_write2st64_b64 v245, v[232:233], v[234:235] offset0:2 offset1:3\n\t"
"s_add_u32 m0, s24, 53120\n\t"
"s_add_u32 s28, s21, 1572864\n\t"
"buffer_load_dwordx4 v247, s[12:15], s28 offen offset:128 nt lds\n\t"
"v_mfma_f32_16x16x32_bf16 v[116:119], a[212:215], a[184:187], v[116:119]\n\t"
"ds_read_b64_tr_b16 v[232:233], v246 offset:40448\n\t"
"v_mfma_f32_16x16x32_bf16 v[116:119], a[224:227], a[188:191], v[116:119]\n\t"
"ds_read_b64_tr_b16 v[234:235], v246 offset:43520\n\t"
"v_mfma_f32_16x16x32_bf16 v[116:119], a[228:231], a[192:195], v[116:119]\n\t"
"ds_write_b64 v245, v[228:229] offset:2048\n\t"
"s_add_u32 m0, s24, 86912\n\t"
"s_add_u32 s28, s21, 2555904\n\t"
"buffer_load_dwordx4 v247, s[12:15], s28 offen offset:128 nt lds\n\t"
"s_add_u32 m0, s24, 93056\n\t"
"s_add_u32 s28, s21, 2752512\n\t"
"buffer_load_dwordx4 v247, s[12:15], s28 offen offset:128 nt lds\n\t"
"v_mfma_f32_16x16x32_bf16 v[120:123], a[212:215], a[188:191], v[120:123]\n\t"
"ds_read_b64_tr_b16 v[228:229], v246 offset:46592\n\t"
"v_mfma_f32_16x16x32_bf16 v[120:123], a[224:227], a[192:195], v[120:123]\n\t"
"ds_read_b64_tr_b16 v[230:231], v246 offset:49664\n\t"
"v_mfma_f32_16x16x32_bf16 v[120:123], a[228:231], a[196:199], v[120:123]\n\t"
"s_add_u32 m0, s24, 100736\n\t"
"s_add_u32 s28, s21, 2949120\n\t"
"buffer_load_dwordx4 v247, s[12:15], s28 offen offset:128 nt lds\n\t"
"s_waitcnt lgkmcnt(6)\n\t"
"v_mfma_f32_16x16x32_bf16 v[224:227], v[224:227], v[220:223], 0\n\t"
"s_add_u32 m0, s24, 106880\n\t"
"s_add_u32 s28, s21, 3145728\n\t"
"buffer_load_dwordx4 v247, s[12:15], s28 offen offset:128 nt lds\n\t"
"v_mfma_f32_16x16x32_bf16 v[124:127], a[212:215], a[192:195], v[124:127]\n\t"
"v_mfma_f32_16x16x32_bf16 v[124:127], a[224:227], a[196:199], v[124:127]\n\t"
"v_mfma_f32_16x16x32_bf16 v[124:127], a[228:231], a[200:203], v[124:127]\n\t"
"s_add_u32 m0, s24, 113024\n\t"
"s_add_u32 s28, s21, 3342336\n\t"
"buffer_load_dwordx4 v247, s[12:15], s28 offen offset:128 nt lds\n\t"
"s_waitcnt lgkmcnt(3)\n\t"
"v_mfma_f32_16x16x32_bf16 v[232:235], v[232:235], v[220:223], 0\n\t"
"s_add_u32 m0, s25, 896\n\t"
"s_nop 0\n\t"
"buffer_load_dwordx4 v254, s[12:15], 0 offen offset:128 nt lds\n\t"
"v_mfma_f32_16x16x32_bf16 v[128:131], a[212:215], a[196:199], v[128:131]\n\t"
"v_cvt_pk_bf16_f32 v224, v224, v225\n\t"
"v_cvt_pk_bf16_f32 v225, v226, v227\n\t"
"v_mfma_f32_16x16x32_bf16 v[128:131], a[224:227], a[200:203], v[128:131]\n\t"
"ds_read_b128 v[240:243], v252 offset:18432\n\t"
"v_mfma_f32_16x16x32_bf16 v[128:131], a[228:231], a[204:207], v[128:131]\n\t"
"s_add_u32 m0, s26, 896\n\t"
"s_mov_b64 exec, s[36:37]\n\t"
"buffer_load_dwordx4 v254, s[12:15], s27 offen offset:128 nt lds\n\t"
"s_mov_b64 exec, -1\n\t"
"s_waitcnt lgkmcnt(1)\n\t"
"v_mfma_f32_16x16x32_bf16 v[228:231], v[228:231], v[220:223], 0\n\t"
"s_waitcnt vmcnt(17)\n\t"
"v_mfma_f32_16x16x32_bf16 v[132:135], a[232:235], a[168:171], v[132:135]\n\t"
"v_cvt_pk_bf16_f32 v226, v232, v233\n\t"
"v_cvt_pk_bf16_f32 v227, v234, v235\n\t"
"v_mfma_f32_16x16x32_bf16 v[132:135], a[236:239], a[172:175], v[132:135]\n\t"
"ds_read_b64_tr_b16 v[232:233], v246 offset:52736\n\t"
"v_mfma_f32_16x16x32_bf16 v[132:135], a[240:243], a[176:179], v[132:135]\n\t"
"ds_write2st64_b64 v245, v[224:225], v[226:227] offset0:5 offset1:6\n\t"
"v_mfma_f32_16x16x32_bf16 v[136:139], a[232:235], a[172:175], v[136:139]\n\t"
"ds_read_b64_tr_b16 v[234:235], v246 offset:55808\n\t"
"v_mfma_f32_16x16x32_bf16 v[136:139], a[236:239], a[176:179], v[136:139]\n\t"
"v_cvt_pk_bf16_f32 v228, v228, v229\n\t"
"v_mfma_f32_16x16x32_bf16 v[136:139], a[240:243], a[180:183], v[136:139]\n\t"
"v_cvt_pk_bf16_f32 v229, v230, v231\n\t"
"v_mfma_f32_16x16x32_bf16 v[140:143], a[232:235], a[176:179], v[140:143]\n\t"
"v_mfma_f32_16x16x32_bf16 v[140:143], a[236:239], a[180:183], v[140:143]\n\t"
"v_mfma_f32_16x16x32_bf16 v[140:143], a[240:243], a[184:187], v[140:143]\n\t"
"s_add_u32 s29, s23, 787584\n\t"
"s_waitcnt vmcnt(15) lgkmcnt(3)\n\t"
"buffer_store_dwordx4 v[240:243], v248, s[16:19], s29 offen offset:0 nt\n\t"
"v_mfma_f32_16x16x32_bf16 v[144:147], a[232:235], a[180:183], v[144:147]\n\t"
"v_mfma_f32_16x16x32_bf16 v[144:147], a[236:239], a[184:187], v[144:147]\n\t"
"v_mfma_f32_16x16x32_bf16 v[144:147], a[240:243], a[188:191], v[144:147]\n\t"
"v_mfma_f32_16x16x32_bf16 v[148:151], a[232:235], a[184:187], v[148:151]\n\t"
"v_mfma_f32_16x16x32_bf16 v[148:151], a[236:239], a[188:191], v[148:151]\n\t"
"v_mfma_f32_16x16x32_bf16 v[148:151], a[240:243], a[192:195], v[148:151]\n\t"
"s_waitcnt lgkmcnt(0)\n\t"
"v_mfma_f32_16x16x32_bf16 v[232:235], v[232:235], v[220:223], 0\n\t"
"v_mfma_f32_16x16x32_bf16 v[152:155], a[232:235], a[188:191], v[152:155]\n\t"
"v_mfma_f32_16x16x32_bf16 v[152:155], a[236:239], a[192:195], v[152:155]\n\t"
"v_mfma_f32_16x16x32_bf16 v[152:155], a[240:243], a[196:199], v[152:155]\n\t"
"ds_read_b128 v[240:243], v252 offset:19456\n\t"
"v_mfma_f32_16x16x32_bf16 v[156:159], a[232:235], a[192:195], v[156:159]\n\t"
"v_mfma_f32_16x16x32_bf16 v[156:159], a[236:239], a[196:199], v[156:159]\n\t"
"s_nop 1\n\t"
"v_cvt_pk_bf16_f32 v230, v232, v233\n\t"
"v_cvt_pk_bf16_f32 v231, v234, v235\n\t"
"v_mfma_f32_16x16x32_bf16 v[156:159], a[240:243], a[200:203], v[156:159]\n\t"
"ds_write2st64_b64 v245, v[228:229], v[230:231] offset0:7 offset1:8\n\t"
"v_mfma_f32_16x16x32_bf16 v[160:163], a[232:235], a[196:199], v[160:163]\n\t"
"v_mfma_f32_16x16x32_bf16 v[160:163], a[236:239], a[200:203], v[160:163]\n\t"
"v_mfma_f32_16x16x32_bf16 v[160:163], a[240:243], a[204:207], v[160:163]\n\t"
"s_waitcnt vmcnt(14)\n\t"
"v_mfma_f32_16x16x32_bf16 v[164:167], a[244:247], a[168:171], v[164:167]\n\t"
"v_mfma_f32_16x16x32_bf16 v[164:167], a[248:251], a[172:175], v[164:167]\n\t"
"v_mfma_f32_16x16x32_bf16 v[164:167], a[252:255], a[176:179], v[164:167]\n\t"
"s_waitcnt lgkmcnt(0)\n\t"
"s_barrier\n\t"
"v_mfma_f32_16x16x32_bf16 v[168:171], a[244:247], a[172:175], v[168:171]\n\t"
"ds_read_b128 a[168:171], v244 offset:0\n\t"
"v_mfma_f32_16x16x32_bf16 v[168:171], a[248:251], a[176:179], v[168:171]\n\t"
"ds_read_b128 a[172:175], v244 offset:512\n\t"
"v_mfma_f32_16x16x32_bf16 v[168:171], a[252:255], a[180:183], v[168:171]\n\t"
"v_mfma_f32_16x16x32_bf16 v[172:175], a[244:247], a[176:179], v[172:175]\n\t"
"ds_read_b128 a[176:179], v244 offset:1024\n\t"
"v_mfma_f32_16x16x32_bf16 v[172:175], a[248:251], a[180:183], v[172:175]\n\t"
"v_mfma_f32_16x16x32_bf16 v[172:175], a[252:255], a[184:187], v[172:175]\n\t"
"s_add_u32 s29, s23, 787584\n\t"
"buffer_store_dwordx4 v[240:243], v248, s[16:19], s29 offen offset:128 nt\n\t"
"v_mfma_f32_16x16x32_bf16 v[176:179], a[244:247], a[180:183], v[176:179]\n\t"
"ds_read_b128 a[180:183], v244 offset:1536\n\t"
"v_mfma_f32_16x16x32_bf16 v[176:179], a[248:251], a[184:187], v[176:179]\n\t"
"v_mfma_f32_16x16x32_bf16 v[176:179], a[252:255], a[188:191], v[176:179]\n\t"
"buffer_load_dwordx4 a[212:215], v252, s[8:11], s20 offen offset:2048 sc1\n\t"
"v_mfma_f32_16x16x32_bf16 v[180:183], a[244:247], a[184:187], v[180:183]\n\t"
"ds_read_b128 a[184:187], v244 offset:2048\n\t"
"v_mfma_f32_16x16x32_bf16 v[180:183], a[248:251], a[188:191], v[180:183]\n\t"
"v_mfma_f32_16x16x32_bf16 v[180:183], a[252:255], a[192:195], v[180:183]\n\t"
"s_mov_b32 s20, 356352\n\t"
"buffer_load_dwordx4 a[224:227], v252, s[8:11], s20 offen offset:0 sc1\n\t"
"v_mfma_f32_16x16x32_bf16 v[184:187], a[244:247], a[188:191], v[184:187]\n\t"
"ds_read_b128 v[240:243], v252 offset:20480\n\t"
"v_mfma_f32_16x16x32_bf16 v[184:187], a[248:251], a[192:195], v[184:187]\n\t"
"ds_read_b128 a[188:191], v244 offset:2560\n\t"
"v_mfma_f32_16x16x32_bf16 v[184:187], a[252:255], a[196:199], v[184:187]\n\t"
"buffer_load_dwordx4 a[228:231], v252, s[8:11], s20 offen offset:1024 sc1\n\t"
"v_mfma_f32_16x16x32_bf16 v[188:191], a[244:247], a[192:195], v[188:191]\n\t"
"ds_read_b128 a[192:195], v244 offset:3072\n\t"
"v_mfma_f32_16x16x32_bf16 v[188:191], a[248:251], a[196:199], v[188:191]\n\t"
"v_mfma_f32_16x16x32_bf16 v[188:191], a[252:255], a[200:203], v[188:191]\n\t"
"buffer_load_dwordx4 a[232:235], v252, s[8:11], s20 offen offset:2048 sc1\n\t"
"v_mfma_f32_16x16x32_bf16 v[192:195], a[244:247], a[196:199], v[192:195]\n\t"
"v_mfma_f32_16x16x32_bf16 v[192:195], a[248:251], a[200:203], v[192:195]\n\t"
"ds_read_b128 a[196:199], v244 offset:3584\n\t"
"v_mfma_f32_16x16x32_bf16 v[192:195], a[252:255], a[204:207], v[192:195]\n\t"
"ds_read_b128 a[200:203], v244 offset:4096\n\t"
"Lst23_%=:\n\t"
"s_waitcnt vmcnt(16) lgkmcnt(7)\n\t"
"v_mfma_f32_16x16x32_bf16 v[196:199], v[236:239], a[168:171], v[196:199]\n\t"
"ds_read_b128 a[204:207], v244 offset:4608\n\t"
"v_mfma_f32_16x16x32_bf16 v[196:199], v[96:99], a[172:175], v[196:199]\n\t"
"v_mfma_f32_16x16x32_bf16 v[196:199], a[216:219], a[176:179], v[196:199]\n\t"
"s_mov_b32 s20, 368640\n\t"
"buffer_load_dwordx4 a[236:239], v252, s[8:11], s20 offen offset:0 sc1\n\t"
"s_waitcnt lgkmcnt(7)\n\t"
"v_mfma_f32_16x16x32_bf16 v[204:207], v[236:239], a[172:175], v[204:207]\n\t"
"v_mfma_f32_16x16x32_bf16 v[204:207], v[96:99], a[176:179], v[204:207]\n\t"
"v_mfma_f32_16x16x32_bf16 v[204:207], a[216:219], a[180:183], v[204:207]\n\t"
"s_add_u32 s29, s23, 787584\n\t"
"s_waitcnt lgkmcnt(5)\n\t"
"buffer_store_dwordx4 v[240:243], v248, s[16:19], s29 offen offset:256 nt\n\t"
"v_mfma_f32_16x16x32_bf16 v[212:215], v[236:239], a[176:179], v[212:215]\n\t"
"v_mfma_f32_16x16x32_bf16 v[212:215], v[96:99], a[180:183], v[212:215]\n\t"
"v_mfma_f32_16x16x32_bf16 v[212:215], a[216:219], a[184:187], v[212:215]\n\t"
"buffer_load_dwordx4 a[240:243], v252, s[8:11], s20 offen offset:1024 sc1\n\t"
"s_waitcnt lgkmcnt(4)\n\t"
"v_mfma_f32_16x16x32_bf16 a[0:3], v[236:239], a[180:183], a[0:3]\n\t"
"v_mfma_f32_16x16x32_bf16 a[0:3], v[96:99], a[184:187], a[0:3]\n\t"
"v_mfma_f32_16x16x32_bf16 a[0:3], a[216:219], a[188:191], a[0:3]\n\t"
"s_waitcnt lgkmcnt(3)\n\t"
"v_mfma_f32_16x16x32_bf16 a[8:11], v[236:239], a[184:187], a[8:11]\n\t"
"ds_read_b128 v[240:243], v252 offset:30720\n\t"
"v_mfma_f32_16x16x32_bf16 a[8:11], v[96:99], a[188:191], a[8:11]\n\t"
"v_mfma_f32_16x16x32_bf16 a[8:11], a[216:219], a[192:195], a[8:11]\n\t"
"buffer_load_dwordx4 a[244:247], v252, s[8:11], s20 offen offset:2048 sc1\n\t"
"s_waitcnt lgkmcnt(3)\n\t"
"v_mfma_f32_16x16x32_bf16 a[16:19], v[236:239], a[188:191], a[16:19]\n\t"
"v_mfma_f32_16x16x32_bf16 a[16:19], v[96:99], a[192:195], a[16:19]\n\t"
"v_mfma_f32_16x16x32_bf16 a[16:19], a[216:219], a[196:199], a[16:19]\n\t"
"s_mov_b32 s20, 380928\n\t"
"buffer_load_dwordx4 a[248:251], v252, s[8:11], s20 offen offset:0 sc1\n\t"
"s_waitcnt lgkmcnt(2)\n\t"
"v_mfma_f32_16x16x32_bf16 a[24:27], v[236:239], a[192:195], a[24:27]\n\t"
"v_mfma_f32_16x16x32_bf16 a[24:27], v[96:99], a[196:199], a[24:27]\n\t"
"v_mfma_f32_16x16x32_bf16 a[24:27], a[216:219], a[200:203], a[24:27]\n\t"
"buffer_load_dwordx4 a[252:255], v252, s[8:11], s20 offen offset:1024 sc1\n\t"
"s_waitcnt lgkmcnt(1)\n\t"
"v_mfma_f32_16x16x32_bf16 a[32:35], v[236:239], a[196:199], a[32:35]\n\t"
"v_mfma_f32_16x16x32_bf16 a[32:35], v[96:99], a[200:203], a[32:35]\n\t"
"v_mfma_f32_16x16x32_bf16 a[32:35], a[216:219], a[204:207], a[32:35]\n\t"
"s_waitcnt vmcnt(9)\n\t"
"v_mfma_f32_16x16x32_bf16 a[40:43], a[220:223], a[168:171], a[40:43]\n\t"
"v_mfma_f32_16x16x32_bf16 a[40:43], a[208:211], a[172:175], a[40:43]\n\t"
"v_mfma_f32_16x16x32_bf16 a[40:43], a[212:215], a[176:179], a[40:43]\n\t"
"buffer_load_dwordx4 v[96:99], v252, s[8:11], s20 offen offset:2048 sc1\n\t"
"v_mfma_f32_16x16x32_bf16 a[48:51], a[220:223], a[172:175], a[48:51]\n\t"
"ds_read_b64_tr_b16 v[236:237], v246 offset:1024\n\t"
"v_mfma_f32_16x16x32_bf16 a[48:51], a[208:211], a[176:179], a[48:51]\n\t"
"ds_read_b64_tr_b16 v[238:239], v246 offset:4096\n\t"
"v_mfma_f32_16x16x32_bf16 a[48:51], a[212:215], a[180:183], a[48:51]\n\t"
"s_add_u32 s29, s23, 1180800\n\t"
"s_waitcnt lgkmcnt(2)\n\t"
"buffer_store_dwordx4 v[240:243], v248, s[16:19], s29 offen offset:0 nt\n\t"
"v_mfma_f32_16x16x32_bf16 a[56:59], a[220:223], a[176:179], a[56:59]\n\t"
"v_mfma_f32_16x16x32_bf16 a[56:59], a[208:211], a[180:183], a[56:59]\n\t"
"v_mfma_f32_16x16x32_bf16 a[56:59], a[212:215], a[184:187], a[56:59]\n\t"
"s_mov_b32 s20, 393216\n\t"
"buffer_load_dwordx4 a[216:219], v252, s[8:11], s20 offen offset:0 sc1\n\t"
"v_mfma_f32_16x16x32_bf16 a[64:67], a[220:223], a[180:183], a[64:67]\n\t"
"ds_read_b64_tr_b16 v[224:225], v246 offset:7168\n\t"
"v_mfma_f32_16x16x32_bf16 a[64:67], a[208:211], a[184:187], a[64:67]\n\t"
"ds_read_b128 v[240:243], v252 offset:31744\n\t"
"v_mfma_f32_16x16x32_bf16 a[64:67], a[212:215], a[188:191], a[64:67]\n\t"
"ds_read_b64_tr_b16 v[226:227], v246 offset:10240\n\t"
"v_mfma_f32_16x16x32_bf16 a[72:75], a[220:223], a[184:187], a[72:75]\n\t"
"v_mfma_f32_16x16x32_bf16 a[72:75], a[208:211], a[188:191], a[72:75]\n\t"
"v_mfma_f32_16x16x32_bf16 a[72:75], a[212:215], a[192:195], a[72:75]\n\t"
"v_mfma_f32_16x16x32_bf16 a[80:83], a[220:223], a[188:191], a[80:83]\n\t"
"v_mfma_f32_16x16x32_bf16 a[80:83], a[208:211], a[192:195], a[80:83]\n\t"
"v_mfma_f32_16x16x32_bf16 a[80:83], a[212:215], a[196:199], a[80:83]\n\t"
"s_waitcnt lgkmcnt(3)\n\t"
"v_mfma_f32_16x16x32_bf16 v[236:239], v[236:239], v[220:223], 0\n\t"
"v_mfma_f32_16x16x32_bf16 a[88:91], a[220:223], a[192:195], a[88:91]\n\t"
"ds_read_b64_tr_b16 v[232:233], v246 offset:13312\n\t"
"v_mfma_f32_16x16x32_bf16 a[88:91], a[208:211], a[196:199], a[88:91]\n\t"
"ds_read_b64_tr_b16 v[234:235], v246 offset:16384\n\t"
"v_mfma_f32_16x16x32_bf16 a[88:91], a[212:215], a[200:203], a[88:91]\n\t"
"v_mfma_f32_16x16x32_bf16 a[96:99], a[220:223], a[196:199], a[96:99]\n\t"
"v_mfma_f32_16x16x32_bf16 a[96:99], a[208:211], a[200:203], a[96:99]\n\t"
"s_nop 0\n\t"
"v_cvt_pk_bf16_f32 v236, v236, v237\n\t"
"v_cvt_pk_bf16_f32 v237, v238, v239\n\t"
"v_mfma_f32_16x16x32_bf16 a[96:99], a[212:215], a[204:207], a[96:99]\n\t"
"buffer_load_dwordx4 a[220:223], v252, s[8:11], s20 offen offset:1024 sc1\n\t"
"s_waitcnt lgkmcnt(2)\n\t"
"v_mfma_f32_16x16x32_bf16 v[224:227], v[224:227], v[220:223], 0\n\t"
"s_add_u32 s29, s23, 1180800\n\t"
"buffer_store_dwordx4 v[240:243], v248, s[16:19], s29 offen offset:128 nt\n\t"
"s_waitcnt vmcnt(11)\n\t"
"v_mfma_f32_16x16x32_bf16 a[104:107], a[224:227], a[168:171], a[104:107]\n\t"
"ds_read_b64_tr_b16 v[228:229], v246 offset:20992\n\t"
"v_mfma_f32_16x16x32_bf16 a[104:107], a[228:231], a[172:175], a[104:107]\n\t"
"ds_read_b64_tr_b16 v[230:231], v246 offset:24064\n\t"
"v_mfma_f32_16x16x32_bf16 a[104:107], a[232:235], a[176:179], a[104:107]\n\t"
"buffer_load_dwordx4 a[212:215], v252, s[8:11], s20 offen offset:2048 sc1\n\t"
"v_mfma_f32_16x16x32_bf16 a[112:115], a[224:227], a[172:175], a[112:115]\n\t"
"ds_read_b128 a[168:171], v244 offset:9216\n\t"
"v_mfma_f32_16x16x32_bf16 a[112:115], a[228:231], a[176:179], a[112:115]\n\t"
"v_cvt_pk_bf16_f32 v224, v224, v225\n\t"
"v_mfma_f32_16x16x32_bf16 a[112:115], a[232:235], a[180:183], a[112:115]\n\t"
"v_cvt_pk_bf16_f32 v225, v226, v227\n\t"
"v_mfma_f32_16x16x32_bf16 a[120:123], a[224:227], a[176:179], a[120:123]\n\t"
"ds_read_b128 v[240:243], v252 offset:32768\n\t"
"v_mfma_f32_16x16x32_bf16 a[120:123], a[228:231], a[180:183], a[120:123]\n\t"
"ds_read_b128 a[172:175], v244 offset:9728\n\t"
"v_mfma_f32_16x16x32_bf16 a[120:123], a[232:235], a[184:187], a[120:123]\n\t"
"ds_read_b128 a[176:179], v244 offset:10240\n\t"
"s_waitcnt lgkmcnt(6)\n\t"
"v_mfma_f32_16x16x32_bf16 v[232:235], v[232:235], v[220:223], 0\n\t"
"v_mfma_f32_16x16x32_bf16 a[128:131], a[224:227], a[180:183], a[128:131]\n\t"
"ds_read_b128 a[180:183], v244 offset:10752\n\t"
"v_mfma_f32_16x16x32_bf16 a[128:131], a[228:231], a[184:187], a[128:131]\n\t"
"v_mfma_f32_16x16x32_bf16 a[128:131], a[232:235], a[188:191], a[128:131]\n\t"
"v_mfma_f32_16x16x32_bf16 a[136:139], a[224:227], a[184:187], a[136:139]\n\t"
"ds_read_b128 a[184:187], v244 offset:11264\n\t"
"v_mfma_f32_16x16x32_bf16 a[136:139], a[228:231], a[188:191], a[136:139]\n\t"
"s_nop 0\n\t"
"v_cvt_pk_bf16_f32 v232, v232, v233\n\t"
"v_mfma_f32_16x16x32_bf16 a[136:139], a[232:235], a[192:195], a[136:139]\n\t"
"v_cvt_pk_bf16_f32 v233, v234, v235\n\t"
"s_waitcnt lgkmcnt(6)\n\t"
"v_mfma_f32_16x16x32_bf16 v[228:231], v[228:231], v[220:223], 0\n\t"
"v_mfma_f32_16x16x32_bf16 a[144:147], a[224:227], a[188:191], a[144:147]\n\t"
"ds_read_b128 a[188:191], v244 offset:11776\n\t"
"v_mfma_f32_16x16x32_bf16 a[144:147], a[228:231], a[192:195], a[144:147]\n\t"
"v_mfma_f32_16x16x32_bf16 a[144:147], a[232:235], a[196:199], a[144:147]\n\t"
"s_add_u32 s29, s23, 1180800\n\t"
"s_waitcnt lgkmcnt(5)\n\t"
"buffer_store_dwordx4 v[240:243], v248, s[16:19], s29 offen offset:256 nt\n\t"
"v_mfma_f32_16x16x32_bf16 a[152:155], a[224:227], a[192:195], a[152:155]\n\t"
"v_mfma_f32_16x16x32_bf16 a[152:155], a[228:231], a[196:199], a[152:155]\n\t"
"v_cvt_pk_bf16_f32 v228, v228, v229\n\t"
"v_cvt_pk_bf16_f32 v229, v230, v231\n\t"
"v_mfma_f32_16x16x32_bf16 a[152:155], a[232:235], a[200:203], a[152:155]\n\t"
"ds_read_b128 a[192:195], v244 offset:12288\n\t"
"v_mfma_f32_16x16x32_bf16 a[160:163], a[224:227], a[196:199], a[160:163]\n\t"
"ds_read_b128 a[196:199], v244 offset:12800\n\t"
"v_mfma_f32_16x16x32_bf16 a[160:163], a[228:231], a[200:203], a[160:163]\n\t"
"ds_read_b128 a[200:203], v244 offset:13312\n\t"
"v_mfma_f32_16x16x32_bf16 a[160:163], a[232:235], a[204:207], a[160:163]\n\t"
"s_mov_b32 s20, 552960\n\t"
"buffer_load_dwordx4 a[224:227], v252, s[8:11], s20 offen offset:0 sc1\n\t"
"s_waitcnt vmcnt(10) lgkmcnt(6)\n\t"
"v_mfma_f32_16x16x32_bf16 v[200:203], a[236:239], a[168:171], v[200:203]\n\t"
"ds_read_b128 a[204:207], v244 offset:13824\n\t"
"v_mfma_f32_16x16x32_bf16 v[200:203], a[240:243], a[172:175], v[200:203]\n\t"
"v_mfma_f32_16x16x32_bf16 v[200:203], a[244:247], a[176:179], v[200:203]\n\t"
"buffer_load_dwordx4 v[248:251], v252, s[8:11], s20 offen offset:1024 sc1\n\t"
"s_waitcnt lgkmcnt(6)\n\t"
"v_mfma_f32_16x16x32_bf16 v[208:211], a[236:239], a[172:175], v[208:211]\n\t"
"v_mfma_f32_16x16x32_bf16 v[208:211], a[240:243], a[176:179], v[208:211]\n\t"
"v_mfma_f32_16x16x32_bf16 v[208:211], a[244:247], a[180:183], v[208:211]\n\t"
"buffer_load_dwordx4 a[228:231], v252, s[8:11], s20 offen offset:2048 sc1\n\t"
"s_waitcnt lgkmcnt(0)\n\t"
"s_barrier\n\t"
"v_mfma_f32_16x16x32_bf16 v[216:219], a[236:239], a[176:179], v[216:219]\n\t"
"v_mfma_f32_16x16x32_bf16 v[216:219], a[240:243], a[180:183], v[216:219]\n\t"
"v_mfma_f32_16x16x32_bf16 v[216:219], a[244:247], a[184:187], v[216:219]\n\t"
"ds_write_b64 v245, v[236:237] offset:0\n\t"
"s_mov_b32 s20, 565248\n\t"
"buffer_load_dwordx4 a[232:235], v252, s[8:11], s20 offen offset:0 sc1\n\t"
"v_mfma_f32_16x16x32_bf16 a[4:7], a[236:239], a[180:183], a[4:7]\n\t"
"ds_read_b64_tr_b16 v[236:237], v246 offset:27136\n\t"
"v_mfma_f32_16x16x32_bf16 a[4:7], a[240:243], a[184:187], a[4:7]\n\t"
"ds_read_b64_tr_b16 v[238:239], v246 offset:30208\n\t"
"v_mfma_f32_16x16x32_bf16 a[4:7], a[244:247], a[188:191], a[4:7]\n\t"
"ds_write_b64 v245, v[224:225] offset:512\n\t"
"ds_write_b64 v245, v[232:233] offset:1024\n\t"
"v_mfma_f32_16x16x32_bf16 a[12:15], a[236:239], a[184:187], a[12:15]\n\t"
"ds_read_b64_tr_b16 v[224:225], v246 offset:33280\n\t"
"v_mfma_f32_16x16x32_bf16 a[12:15], a[240:243], a[188:191], a[12:15]\n\t"
"ds_read_b64_tr_b16 v[226:227], v246 offset:36352\n\t"
"v_mfma_f32_16x16x32_bf16 a[12:15], a[244:247], a[192:195], a[12:15]\n\t"
"ds_write_b64 v245, v[228:229] offset:1536\n\t"
"v_mfma_f32_16x16x32_bf16 a[20:23], a[236:239], a[188:191], a[20:23]\n\t"
"ds_read_b64_tr_b16 v[232:233], v246 offset:40960\n\t"
"v_mfma_f32_16x16x32_bf16 a[20:23], a[240:243], a[192:195], a[20:23]\n\t"
"ds_read_b64_tr_b16 v[234:235], v246 offset:44032\n\t"
"v_mfma_f32_16x16x32_bf16 a[20:23], a[244:247], a[196:199], a[20:23]\n\t"
"global_load_dwordx4 v[240:243], v252, s[46:47] offset:0\n\t"
"s_waitcnt lgkmcnt(7)\n\t"
"v_mfma_f32_16x16x32_bf16 v[236:239], v[236:239], v[220:223], 0\n\t"
"ds_read_b64_tr_b16 v[228:229], v246 offset:47104\n\t"
"ds_read_b64_tr_b16 v[230:231], v246 offset:50176\n\t"
"v_mfma_f32_16x16x32_bf16 a[28:31], a[236:239], a[192:195], a[28:31]\n\t"
"v_mfma_f32_16x16x32_bf16 a[28:31], a[240:243], a[196:199], a[28:31]\n\t"
"v_mfma_f32_16x16x32_bf16 a[28:31], a[244:247], a[200:203], a[28:31]\n\t"
"global_load_dwordx4 a[208:211], v252, s[46:47] offset:1024\n\t"
"s_waitcnt lgkmcnt(5)\n\t"
"v_mfma_f32_16x16x32_bf16 v[224:227], v[224:227], v[220:223], 0\n\t"
"v_mfma_f32_16x16x32_bf16 a[36:39], a[236:239], a[196:199], a[36:39]\n\t"
"v_cvt_pk_bf16_f32 v236, v236, v237\n\t"
"v_cvt_pk_bf16_f32 v237, v238, v239\n\t"
"v_mfma_f32_16x16x32_bf16 a[36:39], a[240:243], a[200:203], a[36:39]\n\t"
"v_mfma_f32_16x16x32_bf16 a[36:39], a[244:247], a[204:207], a[36:39]\n\t"
"buffer_load_dwordx4 a[236:239], v252, s[8:11], s20 offen offset:1024 sc1\n\t"
"s_waitcnt lgkmcnt(2)\n\t"
"v_mfma_f32_16x16x32_bf16 v[232:235], v[232:235], v[220:223], 0\n\t"
"buffer_load_dwordx4 a[240:243], v252, s[8:11], s20 offen offset:2048 sc1\n\t"
"s_waitcnt vmcnt(14)\n\t"
"v_mfma_f32_16x16x32_bf16 a[44:47], a[248:251], a[168:171], a[44:47]\n\t"
"v_cvt_pk_bf16_f32 v224, v224, v225\n\t"
"v_cvt_pk_bf16_f32 v225, v226, v227\n\t"
"v_mfma_f32_16x16x32_bf16 a[44:47], a[252:255], a[172:175], a[44:47]\n\t"
"v_mfma_f32_16x16x32_bf16 a[44:47], v[96:99], a[176:179], a[44:47]\n\t"
"ds_write_b64 v245, v[236:237] offset:2048\n\t"
"s_waitcnt lgkmcnt(1)\n\t"
"v_mfma_f32_16x16x32_bf16 v[228:231], v[228:231], v[220:223], 0\n\t"
"ds_read_b64_tr_b16 v[236:237], v246 offset:53248\n\t"
"ds_read_b64_tr_b16 v[238:239], v246 offset:56320\n\t"
"v_mfma_f32_16x16x32_bf16 a[52:55], a[248:251], a[172:175], a[52:55]\n\t"
"v_cvt_pk_bf16_f32 v232, v232, v233\n\t"
"v_cvt_pk_bf16_f32 v233, v234, v235\n\t"
"v_mfma_f32_16x16x32_bf16 a[52:55], a[252:255], a[176:179], a[52:55]\n\t"
"v_mfma_f32_16x16x32_bf16 a[52:55], v[96:99], a[180:183], a[52:55]\n\t"
"s_mov_b32 s20, 577536\n\t"
"buffer_load_dwordx4 a[244:247], v252, s[8:11], s20 offen offset:0 sc1\n\t"
"v_mfma_f32_16x16x32_bf16 a[60:63], a[248:251], a[176:179], a[60:63]\n\t"
"v_mfma_f32_16x16x32_bf16 a[60:63], a[252:255], a[180:183], a[60:63]\n\t"
"v_cvt_pk_bf16_f32 v228, v228, v229\n\t"
"v_cvt_pk_bf16_f32 v229, v230, v231\n\t"
"v_mfma_f32_16x16x32_bf16 a[60:63], v[96:99], a[184:187], a[60:63]\n\t"
"ds_write_b64 v245, v[224:225] offset:2560\n\t"
"v_mfma_f32_16x16x32_bf16 a[68:71], a[248:251], a[180:183], a[68:71]\n\t"
"v_mfma_f32_16x16x32_bf16 a[68:71], a[252:255], a[184:187], a[68:71]\n\t"
"v_mfma_f32_16x16x32_bf16 a[68:71], v[96:99], a[188:191], a[68:71]\n\t"
"ds_write_b64 v245, v[232:233] offset:3072\n\t"
"s_waitcnt lgkmcnt(2)\n\t"
"v_mfma_f32_16x16x32_bf16 v[236:239], v[236:239], v[220:223], 0\n\t"
"ds_write_b64 v245, v[228:229] offset:3584\n\t"
"v_mfma_f32_16x16x32_bf16 a[76:79], a[248:251], a[184:187], a[76:79]\n\t"
"v_mfma_f32_16x16x32_bf16 a[76:79], a[252:255], a[188:191], a[76:79]\n\t"
"v_mfma_f32_16x16x32_bf16 a[76:79], v[96:99], a[192:195], a[76:79]\n\t"
"v_mfma_f32_16x16x32_bf16 a[84:87], a[248:251], a[188:191], a[84:87]\n\t"
"v_mfma_f32_16x16x32_bf16 a[84:87], a[252:255], a[192:195], a[84:87]\n\t"
"s_nop 1\n\t"
"v_cvt_pk_bf16_f32 v236, v236, v237\n\t"
"v_cvt_pk_bf16_f32 v237, v238, v239\n\t"
"v_mfma_f32_16x16x32_bf16 a[84:87], v[96:99], a[196:199], a[84:87]\n\t"
"v_mfma_f32_16x16x32_bf16 a[92:95], a[248:251], a[192:195], a[92:95]\n\t"
"v_mfma_f32_16x16x32_bf16 a[92:95], a[252:255], a[196:199], a[92:95]\n\t"
"v_mfma_f32_16x16x32_bf16 a[92:95], v[96:99], a[200:203], a[92:95]\n\t"
"ds_write_b64 v245, v[236:237] offset:4096\n\t"
"v_mfma_f32_16x16x32_bf16 a[100:103], a[248:251], a[196:199], a[100:103]\n\t"
"v_mfma_f32_16x16x32_bf16 a[100:103], a[252:255], a[200:203], a[100:103]\n\t"
"v_mfma_f32_16x16x32_bf16 a[100:103], v[96:99], a[204:207], a[100:103]\n\t"
"buffer_load_dwordx4 a[248:251], v252, s[8:11], s20 offen offset:1024 sc1\n\t"
"s_waitcnt vmcnt(11)\n\t"
"v_mfma_f32_16x16x32_bf16 a[108:111], a[216:219], a[168:171], a[108:111]\n\t"
"v_mfma_f32_16x16x32_bf16 a[108:111], a[220:223], a[172:175], a[108:111]\n\t"
"v_mfma_f32_16x16x32_bf16 a[108:111], a[212:215], a[176:179], a[108:111]\n\t"
"buffer_load_dwordx4 v[96:99], v252, s[8:11], s20 offen offset:2048 sc1\n\t"
"s_waitcnt lgkmcnt(0)\n\t"
"s_barrier\n\t"
"v_mfma_f32_16x16x32_bf16 a[116:119], a[216:219], a[172:175], a[116:119]\n\t"
"s_waitcnt vmcnt(6)\n\t"
"ds_read_b64_tr_b16 v[224:225], v240 offset:1024\n\t"
"v_mfma_f32_16x16x32_bf16 a[116:119], a[220:223], a[176:179], a[116:119]\n\t"
"ds_read_b64_tr_b16 v[226:227], v240 offset:2560\n\t"
"v_mfma_f32_16x16x32_bf16 a[116:119], a[212:215], a[180:183], a[116:119]\n\t"
"ds_read_b64_tr_b16 v[232:233], v241 offset:1024\n\t"
"ds_read_b64_tr_b16 v[234:235], v241 offset:2560\n\t"
"v_mfma_f32_16x16x32_bf16 a[124:127], a[216:219], a[176:179], a[124:127]\n\t"
"ds_read_b64_tr_b16 v[228:229], v240 offset:20992\n\t"
"v_mfma_f32_16x16x32_bf16 a[124:127], a[220:223], a[180:183], a[124:127]\n\t"
"ds_read_b64_tr_b16 v[230:231], v240 offset:22528\n\t"
"v_mfma_f32_16x16x32_bf16 a[124:127], a[212:215], a[184:187], a[124:127]\n\t"
"s_mov_b32 s20, 589824\n\t"
"buffer_load_dwordx4 a[252:255], v252, s[8:11], s20 offen offset:0 sc1\n\t"
"v_mfma_f32_16x16x32_bf16 a[132:135], a[216:219], a[180:183], a[132:135]\n\t"
"ds_read_b64_tr_b16 v[236:237], v241 offset:20992\n\t"
"v_mfma_f32_16x16x32_bf16 a[132:135], a[220:223], a[184:187], a[132:135]\n\t"
"ds_read_b64_tr_b16 v[238:239], v241 offset:22528\n\t"
"v_mfma_f32_16x16x32_bf16 a[132:135], a[212:215], a[188:191], a[132:135]\n\t"
"ds_read_b128 a[168:171], v244 offset:0\n\t"
"ds_read_b128 a[172:175], v244 offset:512\n\t"
"s_waitcnt vmcnt(6) lgkmcnt(8)\n\t"
"v_mfma_f32_16x16x32_bf16 v[224:227], v[224:227], a[208:211], 0\n\t"
"ds_read_b128 a[176:179], v244 offset:1024\n\t"
"ds_read_b128 a[180:183], v244 offset:1536\n\t"
"s_waitcnt lgkmcnt(8)\n\t"
"v_mfma_f32_16x16x32_bf16 v[232:235], v[232:235], a[208:211], 0\n\t"
"v_mfma_f32_16x16x32_bf16 a[140:143], a[216:219], a[184:187], a[140:143]\n\t"
"ds_read_b128 a[184:187], v244 offset:2048\n\t"
"v_mfma_f32_16x16x32_bf16 a[140:143], a[220:223], a[188:191], a[140:143]\n\t"
"v_mfma_f32_16x16x32_bf16 a[140:143], a[212:215], a[192:195], a[140:143]\n\t"
"v_mfma_f32_16x16x32_bf16 a[148:151], a[216:219], a[188:191], a[148:151]\n\t"
"v_cvt_pk_bf16_f32 v224, v224, v225\n\t"
"v_cvt_pk_bf16_f32 v225, v226, v227\n\t"
"v_mfma_f32_16x16x32_bf16 a[148:151], a[220:223], a[192:195], a[148:151]\n\t"
"v_cvt_pk_bf16_f32 v226, v232, v233\n\t"
"v_mfma_f32_16x16x32_bf16 a[148:151], a[212:215], a[196:199], a[148:151]\n\t"
"v_cvt_pk_bf16_f32 v227, v234, v235\n\t"
"ds_read_b128 a[188:191], v244 offset:2560\n\t"
"s_waitcnt lgkmcnt(8)\n\t"
"v_mfma_f32_16x16x32_bf16 v[228:231], v[228:231], a[208:211], 0\n\t"
"s_waitcnt lgkmcnt(6)\n\t"
"v_mfma_f32_16x16x32_bf16 v[236:239], v[236:239], a[208:211], 0\n\t"
"s_mov_b64 exec, s[44:45]\n\t"
"ds_write_b128 v242, v[224:227] offset:18432\n\t"
"s_mov_b64 exec, -1\n\t"
"v_mfma_f32_16x16x32_bf16 a[156:159], a[216:219], a[192:195], a[156:159]\n\t"
"ds_read_b128 a[192:195], v244 offset:3072\n\t"
"v_mfma_f32_16x16x32_bf16 a[156:159], a[220:223], a[196:199], a[156:159]\n\t"
"ds_read_b64_tr_b16 v[232:233], v240 offset:40960\n\t"
"v_mfma_f32_16x16x32_bf16 a[156:159], a[212:215], a[200:203], a[156:159]\n\t"
"ds_read_b64_tr_b16 v[234:235], v240 offset:42496\n\t"
"v_mfma_f32_16x16x32_bf16 a[164:167], a[216:219], a[196:199], a[164:167]\n\t"
"v_cvt_pk_bf16_f32 v228, v228, v229\n\t"
"v_cvt_pk_bf16_f32 v229, v230, v231\n\t"
"v_mfma_f32_16x16x32_bf16 a[164:167], a[220:223], a[200:203], a[164:167]\n\t"
"v_cvt_pk_bf16_f32 v230, v236, v237\n\t"
"v_cvt_pk_bf16_f32 v231, v238, v239\n\t"
"v_mfma_f32_16x16x32_bf16 a[164:167], a[212:215], a[204:207], a[164:167]\n\t"
"buffer_load_dwordx4 a[216:219], v252, s[8:11], s20 offen offset:1024 sc1\n\t"
"Lst24_%=:\n\t"
"s_waitcnt lgkmcnt(7)\n\t"
"v_mfma_f32_16x16x32_bf16 v[196:199], a[224:227], a[168:171], v[196:199]\n\t"
"ds_read_b64_tr_b16 v[224:225], v241 offset:40960\n\t"
"v_mfma_f32_16x16x32_bf16 v[196:199], v[248:251], a[172:175], v[196:199]\n\t"
"ds_read_b64_tr_b16 v[226:227], v241 offset:42496\n\t"
"v_add_u32 v240, 59904, v240\n\t"
"v_mfma_f32_16x16x32_bf16 v[196:199], a[228:231], a[176:179], v[196:199]\n\t"
"buffer_load_dwordx4 a[212:215], v252, s[8:11], s20 offen offset:2048 sc1\n\t"
"s_waitcnt lgkmcnt(8)\n\t"
"v_mfma_f32_16x16x32_bf16 v[204:207], a[224:227], a[172:175], v[204:207]\n\t"
"ds_read_b128 a[196:199], v244 offset:3584\n\t"
"v_add_u32 v241, 59904, v241\n\t"
"v_mfma_f32_16x16x32_bf16 v[204:207], v[248:251], a[176:179], v[204:207]\n\t"
"ds_read_b128 a[200:203], v244 offset:4096\n\t"
"v_mfma_f32_16x16x32_bf16 v[204:207], a[228:231], a[180:183], v[204:207]\n\t"
"s_mov_b64 exec, s[44:45]\n\t"
"ds_write_b128 v242, v[228:231] offset:19968\n\t"
"s_mov_b64 exec, -1\n\t"
"s_waitcnt lgkmcnt(10)\n\t"
"v_mfma_f32_16x16x32_bf16 v[212:215], a[224:227], a[176:179], v[212:215]\n\t"
"ds_read_b64_tr_b16 v[236:237], v240 offset:1024\n\t"
"v_mfma_f32_16x16x32_bf16 v[212:215], v[248:251], a[180:183], v[212:215]\n\t"
"ds_read_b64_tr_b16 v[238:239], v240 offset:2560\n\t"
"v_mfma_f32_16x16x32_bf16 v[212:215], a[228:231], a[184:187], v[212:215]\n\t"
"s_mov_b32 s20, 602112\n\t"
"buffer_load_dwordx4 a[220:223], v252, s[8:11], s20 offen offset:0 sc1\n\t"
"s_waitcnt lgkmcnt(7)\n\t"
"v_mfma_f32_16x16x32_bf16 v[232:235], v[232:235], a[208:211], 0\n\t"
"ds_read_b64_tr_b16 v[228:229], v241 offset:1024\n\t"
"ds_read_b64_tr_b16 v[230:231], v241 offset:2560\n\t"
"s_waitcnt lgkmcnt(7)\n\t"
"v_mfma_f32_16x16x32_bf16 v[224:227], v[224:227], a[208:211], 0\n\t"
"ds_read_b128 a[204:207], v244 offset:4608\n\t"
"v_mfma_f32_16x16x32_bf16 a[0:3], a[224:227], a[180:183], a[0:3]\n\t"
"v_mfma_f32_16x16x32_bf16 a[0:3], v[248:251], a[184:187], a[0:3]\n\t"
"v_mfma_f32_16x16x32_bf16 a[0:3], a[228:231], a[188:191], a[0:3]\n\t"
"v_mfma_f32_16x16x32_bf16 a[8:11], a[224:227], a[184:187], a[8:11]\n\t"
"v_cvt_pk_bf16_f32 v232, v232, v233\n\t"
"v_cvt_pk_bf16_f32 v233, v234, v235\n\t"
"v_mfma_f32_16x16x32_bf16 a[8:11], v[248:251], a[188:191], a[8:11]\n\t"
"v_cvt_pk_bf16_f32 v234, v224, v225\n\t"
"v_mfma_f32_16x16x32_bf16 a[8:11], a[228:231], a[192:195], a[8:11]\n\t"
"v_cvt_pk_bf16_f32 v235, v226, v227\n\t"
"s_waitcnt lgkmcnt(3)\n\t"
"v_mfma_f32_16x16x32_bf16 v[236:239], v[236:239], a[208:211], 0\n\t"
"s_mov_b64 exec, s[44:45]\n\t"
"ds_write_b128 v242, v[232:235] offset:21504\n\t"
"s_mov_b64 exec, -1\n\t"
"s_waitcnt lgkmcnt(2)\n\t"
"v_mfma_f32_16x16x32_bf16 v[228:231], v[228:231], a[208:211], 0\n\t"
"ds_read_b64_tr_b16 v[224:225], v240 offset:20992\n\t"
"v_mfma_f32_16x16x32_bf16 a[16:19], a[224:227], a[188:191], a[16:19]\n\t"
"ds_read_b64_tr_b16 v[226:227], v240 offset:22528\n\t"
"v_mfma_f32_16x16x32_bf16 a[16:19], v[248:251], a[192:195], a[16:19]\n\t"
"v_mfma_f32_16x16x32_bf16 a[16:19], a[228:231], a[196:199], a[16:19]\n\t"
"ds_read_b64_tr_b16 v[232:233], v241 offset:20992\n\t"
"v_mfma_f32_16x16x32_bf16 a[24:27], a[224:227], a[192:195], a[24:27]\n\t"
"v_cvt_pk_bf16_f32 v236, v236, v237\n\t"
"v_mfma_f32_16x16x32_bf16 a[24:27], v[248:251], a[196:199], a[24:27]\n\t"
"v_cvt_pk_bf16_f32 v237, v238, v239\n\t"
"v_cvt_pk_bf16_f32 v238, v228, v229\n\t"
"v_mfma_f32_16x16x32_bf16 a[24:27], a[228:231], a[200:203], a[24:27]\n\t"
"v_cvt_pk_bf16_f32 v239, v230, v231\n\t"
"ds_read_b64_tr_b16 v[234:235], v241 offset:22528\n\t"
"s_waitcnt lgkmcnt(5)\n\t"
"v_mfma_f32_16x16x32_bf16 a[32:35], a[224:227], a[196:199], a[32:35]\n\t"
"v_mfma_f32_16x16x32_bf16 a[32:35], v[248:251], a[200:203], a[32:35]\n\t"
"ds_read_b64_tr_b16 v[228:229], v240 offset:40960\n\t"
"v_mfma_f32_16x16x32_bf16 a[32:35], a[228:231], a[204:207], a[32:35]\n\t"
"buffer_load_dwordx4 a[224:227], v252, s[8:11], s20 offen offset:1024 sc1\n\t"
"s_waitcnt vmcnt(8)\n\t"
"v_mfma_f32_16x16x32_bf16 a[40:43], a[232:235], a[168:171], a[40:43]\n\t"
"ds_read_b64_tr_b16 v[230:231], v240 offset:42496\n\t"
"v_mfma_f32_16x16x32_bf16 a[40:43], a[236:239], a[172:175], a[40:43]\n\t"
"v_mfma_f32_16x16x32_bf16 a[40:43], a[240:243], a[176:179], a[40:43]\n\t"
"s_mov_b64 exec, s[44:45]\n\t"
"ds_write_b128 v242, v[236:239] offset:23040\n\t"
"s_mov_b64 exec, -1\n\t"
"v_mfma_f32_16x16x32_bf16 a[48:51], a[232:235], a[172:175], a[48:51]\n\t"
"ds_read_b64_tr_b16 v[236:237], v241 offset:40960\n\t"
"v_mfma_f32_16x16x32_bf16 a[48:51], a[236:239], a[176:179], a[48:51]\n\t"
"ds_read_b64_tr_b16 v[238:239], v241 offset:42496\n\t"
"v_mfma_f32_16x16x32_bf16 a[48:51], a[240:243], a[180:183], a[48:51]\n\t"
"buffer_load_dwordx4 v[248:251], v252, s[8:11], s20 offen offset:2048 sc1\n\t"
"s_waitcnt lgkmcnt(7)\n\t"
"v_mfma_f32_16x16x32_bf16 v[224:227], v[224:227], a[208:211], 0\n\t"
"s_mov_b32 s20, 614400\n\t"
"buffer_load_dwordx4 a[228:231], v252, s[8:11], s20 offen offset:0 sc1\n\t"
"s_waitcnt lgkmcnt(5)\n\t"
"v_mfma_f32_16x16x32_bf16 v[232:235], v[232:235], a[208:211], 0\n\t"
"v_xor_b32 v246, 0xc0, v246\n\t"
"v_mfma_f32_16x16x32_bf16 a[56:59], a[232:235], a[176:179], a[56:59]\n\t"
"v_mfma_f32_16x16x32_bf16 a[56:59], a[236:239], a[180:183], a[56:59]\n\t"
"v_mfma_f32_16x16x32_bf16 a[56:59], a[240:243], a[184:187], a[56:59]\n\t"
"v_mfma_f32_16x16x32_bf16 a[64:67], a[232:235], a[180:183], a[64:67]\n\t"
"v_cvt_pk_bf16_f32 v224, v224, v225\n\t"
"v_mfma_f32_16x16x32_bf16 a[64:67], a[236:239], a[184:187], a[64:67]\n\t"
"v_cvt_pk_bf16_f32 v225, v226, v227\n\t"
"v_cvt_pk_bf16_f32 v226, v232, v233\n\t"
"v_mfma_f32_16x16x32_bf16 a[64:67], a[240:243], a[188:191], a[64:67]\n\t"
"v_cvt_pk_bf16_f32 v227, v234, v235\n\t"
"s_waitcnt lgkmcnt(3)\n\t"
"v_mfma_f32_16x16x32_bf16 v[228:231], v[228:231], a[208:211], 0\n\t"
"s_waitcnt lgkmcnt(0)\n\t"
"v_mfma_f32_16x16x32_bf16 v[236:239], v[236:239], a[208:211], 0\n\t"
"s_mov_b64 exec, s[44:45]\n\t"
"ds_write_b128 v242, v[224:227] offset:24576\n\t"
"s_mov_b64 exec, -1\n\t"
"v_mfma_f32_16x16x32_bf16 a[72:75], a[232:235], a[184:187], a[72:75]\n\t"
"v_mfma_f32_16x16x32_bf16 a[72:75], a[236:239], a[188:191], a[72:75]\n\t"
"v_mfma_f32_16x16x32_bf16 a[72:75], a[240:243], a[192:195], a[72:75]\n\t"
"buffer_load_dwordx4 a[208:211], v252, s[8:11], s20 offen offset:1024 sc1\n\t"
"v_mfma_f32_16x16x32_bf16 a[80:83], a[232:235], a[188:191], a[80:83]\n\t"
"v_cvt_pk_bf16_f32 v228, v228, v229\n\t"
"v_cvt_pk_bf16_f32 v229, v230, v231\n\t"
"v_mfma_f32_16x16x32_bf16 a[80:83], a[236:239], a[192:195], a[80:83]\n\t"
"v_cvt_pk_bf16_f32 v230, v236, v237\n\t"
"v_mfma_f32_16x16x32_bf16 a[80:83], a[240:243], a[196:199], a[80:83]\n\t"
"v_cvt_pk_bf16_f32 v231, v238, v239\n\t"
"ds_read_b64_tr_b16 v[232:233], v246 offset:1024\n\t"
"v_mfma_f32_16x16x32_bf16 a[88:91], a[232:235], a[192:195], a[88:91]\n\t"
"ds_read_b64_tr_b16 v[234:235], v246 offset:4096\n\t"
"v_mfma_f32_16x16x32_bf16 a[88:91], a[236:239], a[196:199], a[88:91]\n\t"
"v_mfma_f32_16x16x32_bf16 a[88:91], a[240:243], a[200:203], a[88:91]\n\t"
"s_mov_b64 exec, s[44:45]\n\t"
"ds_write_b128 v242, v[228:231] offset:26112\n\t"
"s_mov_b64 exec, -1\n\t"
"v_mfma_f32_16x16x32_bf16 a[96:99], a[232:235], a[196:199], a[96:99]\n\t"
"ds_read_b64_tr_b16 v[224:225], v246 offset:7168\n\t"
"v_mfma_f32_16x16x32_bf16 a[96:99], a[236:239], a[200:203], a[96:99]\n\t"
"ds_read_b64_tr_b16 v[226:227], v246 offset:10240\n\t"
"v_mfma_f32_16x16x32_bf16 a[96:99], a[240:243], a[204:207], a[96:99]\n\t"
"s_waitcnt vmcnt(8)\n\t"
"v_mfma_f32_16x16x32_bf16 a[104:107], a[244:247], a[168:171], a[104:107]\n\t"
"ds_read_b128 a[168:171], v244 offset:9216\n\t"
"v_mfma_f32_16x16x32_bf16 a[104:107], a[248:251], a[172:175], a[104:107]\n\t"
"ds_read_b64_tr_b16 v[236:237], v246 offset:13312\n\t"
"v_mfma_f32_16x16x32_bf16 a[104:107], v[96:99], a[176:179], a[104:107]\n\t"
"buffer_load_dwordx4 a[232:235], v252, s[8:11], s20 offen offset:2048 sc1\n\t"
"v_mfma_f32_16x16x32_bf16 a[112:115], a[244:247], a[172:175], a[112:115]\n\t"
"ds_read_b64_tr_b16 v[238:239], v246 offset:16384\n\t"
"v_mfma_f32_16x16x32_bf16 a[112:115], a[248:251], a[176:179], a[112:115]\n\t"
"ds_read_b128 a[172:175], v244 offset:9728\n\t"
"v_mfma_f32_16x16x32_bf16 a[112:115], v[96:99], a[180:183], a[112:115]\n\t"
"s_mov_b32 s20, 479232\n\t"
"buffer_load_dwordx4 a[236:239], v252, s[8:11], s20 offen offset:0 sc1\n\t"
"v_mfma_f32_16x16x32_bf16 a[120:123], a[244:247], a[176:179], a[120:123]\n\t"
"ds_read_b64_tr_b16 v[228:229], v246 offset:20992\n\t"
"v_mfma_f32_16x16x32_bf16 a[120:123], a[248:251], a[180:183], a[120:123]\n\t"
"ds_read_b64_tr_b16 v[230:231], v246 offset:24064\n\t"
"v_mfma_f32_16x16x32_bf16 a[120:123], v[96:99], a[184:187], a[120:123]\n\t"
"buffer_load_dwordx4 a[240:243], v252, s[8:11], s20 offen offset:1024 sc1\n\t"
"s_waitcnt lgkmcnt(9)\n\t"
"v_mfma_f32_16x16x32_bf16 v[232:235], v[232:235], v[220:223], 0\n\t"
"v_mfma_f32_16x16x32_bf16 a[128:131], a[244:247], a[180:183], a[128:131]\n\t"
"ds_read_b128 a[176:179], v244 offset:10240\n\t"
"v_mfma_f32_16x16x32_bf16 a[128:131], a[248:251], a[184:187], a[128:131]\n\t"
"v_mfma_f32_16x16x32_bf16 a[128:131], v[96:99], a[188:191], a[128:131]\n\t"
"ds_read_b128 a[180:183], v244 offset:10752\n\t"
"s_waitcnt lgkmcnt(8)\n\t"
"v_mfma_f32_16x16x32_bf16 v[224:227], v[224:227], v[220:223], 0\n\t"
"v_mfma_f32_16x16x32_bf16 a[136:139], a[244:247], a[184:187], a[136:139]\n\t"
"v_cvt_pk_bf16_f32 v232, v232, v233\n\t"
"v_cvt_pk_bf16_f32 v233, v234, v235\n\t"
"v_mfma_f32_16x16x32_bf16 a[136:139], a[248:251], a[188:191], a[136:139]\n\t"
"ds_read_b128 a[184:187], v244 offset:11264\n\t"
"v_mfma_f32_16x16x32_bf16 a[136:139], v[96:99], a[192:195], a[136:139]\n\t"
"s_waitcnt lgkmcnt(6)\n\t"
"v_mfma_f32_16x16x32_bf16 v[236:239], v[236:239], v[220:223], 0\n\t"
"v_mfma_f32_16x16x32_bf16 a[144:147], a[244:247], a[188:191], a[144:147]\n\t"
"v_cvt_pk_bf16_f32 v234, v224, v225\n\t"
"v_cvt_pk_bf16_f32 v235, v226, v227\n\t"
"v_mfma_f32_16x16x32_bf16 a[144:147], a[248:251], a[192:195], a[144:147]\n\t"
"ds_read_b64_tr_b16 v[224:225], v246 offset:27136\n\t"
"v_mfma_f32_16x16x32_bf16 a[144:147], v[96:99], a[196:199], a[144:147]\n\t"
"ds_read_b64_tr_b16 v[226:227], v246 offset:30208\n\t"
"ds_read_b128 a[188:191], v244 offset:11776\n\t"
"s_waitcnt lgkmcnt(6)\n\t"
"v_mfma_f32_16x16x32_bf16 v[228:231], v[228:231], v[220:223], 0\n\t"
"v_mfma_f32_16x16x32_bf16 a[152:155], a[244:247], a[192:195], a[152:155]\n\t"
"v_cvt_pk_bf16_f32 v236, v236, v237\n\t"
"v_cvt_pk_bf16_f32 v237, v238, v239\n\t"
"v_mfma_f32_16x16x32_bf16 a[152:155], a[248:251], a[196:199], a[152:155]\n\t"
"ds_read_b128 a[192:195], v244 offset:12288\n\t"
"v_mfma_f32_16x16x32_bf16 a[152:155], v[96:99], a[200:203], a[152:155]\n\t"
"v_mfma_f32_16x16x32_bf16 a[160:163], a[244:247], a[196:199], a[160:163]\n\t"
"ds_read_b128 a[196:199], v244 offset:12800\n\t"
"v_mfma_f32_16x16x32_bf16 a[160:163], a[248:251], a[200:203], a[160:163]\n\t"
"v_cvt_pk_bf16_f32 v238, v228, v229\n\t"
"v_cvt_pk_bf16_f32 v239, v230, v231\n\t"
"v_mfma_f32_16x16x32_bf16 a[160:163], v[96:99], a[204:207], a[160:163]\n\t"
"buffer_load_dwordx4 a[244:247], v252, s[8:11], s20 offen offset:2048 sc1\n\t"
"s_waitcnt vmcnt(9) lgkmcnt(7)\n\t"
"v_mfma_f32_16x16x32_bf16 v[200:203], a[252:255], a[168:171], v[200:203]\n\t"
"ds_read_b128 a[200:203], v244 offset:13312\n\t"
"v_mfma_f32_16x16x32_bf16 v[200:203], a[216:219], a[172:175], v[200:203]\n\t"
"ds_read_b128 a[204:207], v244 offset:13824\n\t"
"v_mfma_f32_16x16x32_bf16 v[200:203], a[212:215], a[176:179], v[200:203]\n\t"
"s_mov_b32 s20, 491520\n\t"
"buffer_load_dwordx4 v[96:99], v252, s[8:11], s20 offen offset:0 sc1\n\t"
"s_waitcnt lgkmcnt(5)\n\t"
"v_mfma_f32_16x16x32_bf16 v[224:227], v[224:227], v[220:223], 0\n\t"
"buffer_load_dwordx4 a[248:251], v252, s[8:11], s20 offen offset:1024 sc1\n\t"
"v_mfma_f32_16x16x32_bf16 v[208:211], a[252:255], a[172:175], v[208:211]\n\t"
"v_mfma_f32_16x16x32_bf16 v[208:211], a[216:219], a[176:179], v[208:211]\n\t"
"v_mfma_f32_16x16x32_bf16 v[208:211], a[212:215], a[180:183], v[208:211]\n\t"
"s_waitcnt lgkmcnt(0)\n\t"
"s_barrier\n\t"
"v_mfma_f32_16x16x32_bf16 v[216:219], a[252:255], a[176:179], v[216:219]\n\t"
"ds_read_b64_tr_b16 v[228:229], v246 offset:33280\n\t"
"v_mfma_f32_16x16x32_bf16 v[216:219], a[216:219], a[180:183], v[216:219]\n\t"
"v_cvt_pk_bf16_f32 v224, v224, v225\n\t"
"v_cvt_pk_bf16_f32 v225, v226, v227\n\t"
"v_mfma_f32_16x16x32_bf16 v[216:219], a[212:215], a[184:187], v[216:219]\n\t"
"s_add_u32 m0, s24, 256\n\t"
"s_add_u32 s28, s21, 0\n\t"
"buffer_load_dwordx4 v247, s[12:15], s28 offen offset:256 nt lds\n\t"
"ds_write2st64_b64 v245, v[232:233], v[234:235] offset0:0 offset1:1\n\t"
"s_add_u32 m0, s24, 6400\n\t"
"s_add_u32 s28, s21, 196608\n\t"
"buffer_load_dwordx4 v247, s[12:15], s28 offen offset:256 nt lds\n\t"
"v_mfma_f32_16x16x32_bf16 a[4:7], a[252:255], a[180:183], a[4:7]\n\t"
"ds_read_b64_tr_b16 v[230:231], v246 offset:36352\n\t"
"v_mfma_f32_16x16x32_bf16 a[4:7], a[216:219], a[184:187], a[4:7]\n\t"
"ds_read_b64_tr_b16 v[232:233], v246 offset:40960\n\t"
"v_mfma_f32_16x16x32_bf16 a[4:7], a[212:215], a[188:191], a[4:7]\n\t"
"ds_write2st64_b64 v245, v[236:237], v[238:239] offset0:2 offset1:3\n\t"
"s_add_u32 m0, s24, 12544\n\t"
"s_add_u32 s28, s21, 393216\n\t"
"buffer_load_dwordx4 v247, s[12:15], s28 offen offset:256 nt lds\n\t"
"v_mfma_f32_16x16x32_bf16 a[12:15], a[252:255], a[184:187], a[12:15]\n\t"
"ds_read_b64_tr_b16 v[234:235], v246 offset:44032\n\t"
"v_mfma_f32_16x16x32_bf16 a[12:15], a[216:219], a[188:191], a[12:15]\n\t"
"v_mfma_f32_16x16x32_bf16 a[12:15], a[212:215], a[192:195], a[12:15]\n\t"
"s_add_u32 m0, s24, 20224\n\t"
"s_add_u32 s28, s21, 589824\n\t"
"buffer_load_dwordx4 v247, s[12:15], s28 offen offset:256 nt lds\n\t"
"ds_write_b64 v245, v[224:225] offset:2048\n\t"
"s_add_u32 m0, s24, 26368\n\t"
"s_add_u32 s28, s21, 786432\n\t"
"buffer_load_dwordx4 v247, s[12:15], s28 offen offset:256 nt lds\n\t"
"v_mfma_f32_16x16x32_bf16 a[20:23], a[252:255], a[188:191], a[20:23]\n\t"
"ds_read_b64_tr_b16 v[224:225], v246 offset:47104\n\t"
"v_mfma_f32_16x16x32_bf16 a[20:23], a[216:219], a[192:195], a[20:23]\n\t"
"ds_read_b64_tr_b16 v[226:227], v246 offset:50176\n\t"
"v_mfma_f32_16x16x32_bf16 a[20:23], a[212:215], a[196:199], a[20:23]\n\t"
"s_add_u32 m0, s24, 60160\n\t"
"s_add_u32 s28, s21, 1769472\n\t"
"buffer_load_dwordx4 v247, s[12:15], s28 offen offset:256 nt lds\n\t"
"s_waitcnt lgkmcnt(6)\n\t"
"v_mfma_f32_16x16x32_bf16 v[228:231], v[228:231], v[220:223], 0\n\t"
"v_mfma_f32_16x16x32_bf16 a[28:31], a[252:255], a[192:195], a[28:31]\n\t"
"v_mfma_f32_16x16x32_bf16 a[28:31], a[216:219], a[196:199], a[28:31]\n\t"
"v_mfma_f32_16x16x32_bf16 a[28:31], a[212:215], a[200:203], a[28:31]\n\t"
"s_waitcnt lgkmcnt(3)\n\t"
"v_mfma_f32_16x16x32_bf16 v[232:235], v[232:235], v[220:223], 0\n\t"
"v_mfma_f32_16x16x32_bf16 a[36:39], a[252:255], a[196:199], a[36:39]\n\t"
"s_nop 1\n\t"
"v_cvt_pk_bf16_f32 v228, v228, v229\n\t"
"v_cvt_pk_bf16_f32 v229, v230, v231\n\t"
"v_mfma_f32_16x16x32_bf16 a[36:39], a[216:219], a[200:203], a[36:39]\n\t"
"v_mfma_f32_16x16x32_bf16 a[36:39], a[212:215], a[204:207], a[36:39]\n\t"
"s_waitcnt lgkmcnt(0)\n\t"
"v_mfma_f32_16x16x32_bf16 v[224:227], v[224:227], v[220:223], 0\n\t"
"s_waitcnt vmcnt(14)\n\t"
"v_mfma_f32_16x16x32_bf16 a[44:47], a[220:223], a[168:171], a[44:47]\n\t"
"v_cvt_pk_bf16_f32 v230, v232, v233\n\t"
"v_cvt_pk_bf16_f32 v231, v234, v235\n\t"
"v_mfma_f32_16x16x32_bf16 a[44:47], a[224:227], a[172:175], a[44:47]\n\t"
"ds_read_b64_tr_b16 v[232:233], v246 offset:53248\n\t"
"v_mfma_f32_16x16x32_bf16 a[44:47], v[248:251], a[176:179], a[44:47]\n\t"
"ds_write2st64_b64 v245, v[228:229], v[230:231] offset0:5 offset1:6\n\t"
"v_mfma_f32_16x16x32_bf16 a[52:55], a[220:223], a[172:175], a[52:55]\n\t"
"ds_read_b64_tr_b16 v[234:235], v246 offset:56320\n\t"
"v_mfma_f32_16x16x32_bf16 a[52:55], a[224:227], a[176:179], a[52:55]\n\t"
"v_cvt_pk_bf16_f32 v224, v224, v225\n\t"
"v_mfma_f32_16x16x32_bf16 a[52:55], v[248:251], a[180:183], a[52:55]\n\t"
"v_cvt_pk_bf16_f32 v225, v226, v227\n\t"
"v_mfma_f32_16x16x32_bf16 a[60:63], a[220:223], a[176:179], a[60:63]\n\t"
"v_mfma_f32_16x16x32_bf16 a[60:63], a[224:227], a[180:183], a[60:63]\n\t"
"v_mfma_f32_16x16x32_bf16 a[60:63], v[248:251], a[184:187], a[60:63]\n\t"
"v_mfma_f32_16x16x32_bf16 a[68:71], a[220:223], a[180:183], a[68:71]\n\t"
"v_mfma_f32_16x16x32_bf16 a[68:71], a[224:227], a[184:187], a[68:71]\n\t"
"v_mfma_f32_16x16x32_bf16 a[68:71], v[248:251], a[188:191], a[68:71]\n\t"
"v_mfma_f32_16x16x32_bf16 a[76:79], a[220:223], a[184:187], a[76:79]\n\t"
"v_mfma_f32_16x16x32_bf16 a[76:79], a[224:227], a[188:191], a[76:79]\n\t"
"v_mfma_f32_16x16x32_bf16 a[76:79], v[248:251], a[192:195], a[76:79]\n\t"
"s_waitcnt lgkmcnt(0)\n\t"
"v_mfma_f32_16x16x32_bf16 v[232:235], v[232:235], v[220:223], 0\n\t"
"v_mfma_f32_16x16x32_bf16 a[84:87], a[220:223], a[188:191], a[84:87]\n\t"
"v_mfma_f32_16x16x32_bf16 a[84:87], a[224:227], a[192:195], a[84:87]\n\t"
"v_mfma_f32_16x16x32_bf16 a[84:87], v[248:251], a[196:199], a[84:87]\n\t"
"buffer_load_dwordx4 v[236:239], v252, s[8:11], s20 offen offset:2048 sc1\n\t"
"v_mfma_f32_16x16x32_bf16 a[92:95], a[220:223], a[192:195], a[92:95]\n\t"
"v_mfma_f32_16x16x32_bf16 a[92:95], a[224:227], a[196:199], a[92:95]\n\t"
"s_nop 1\n\t"
"v_cvt_pk_bf16_f32 v226, v232, v233\n\t"
"v_cvt_pk_bf16_f32 v227, v234, v235\n\t"
"v_mfma_f32_16x16x32_bf16 a[92:95], v[248:251], a[200:203], a[92:95]\n\t"
"ds_write2st64_b64 v245, v[224:225], v[226:227] offset0:7 offset1:8\n\t"
"v_mfma_f32_16x16x32_bf16 a[100:103], a[220:223], a[196:199], a[100:103]\n\t"
"v_mfma_f32_16x16x32_bf16 a[100:103], a[224:227], a[200:203], a[100:103]\n\t"
"v_mfma_f32_16x16x32_bf16 a[100:103], v[248:251], a[204:207], a[100:103]\n\t"
"s_waitcnt vmcnt(12)\n\t"
"v_mfma_f32_16x16x32_bf16 a[108:111], a[228:231], a[168:171], a[108:111]\n\t"
"v_mfma_f32_16x16x32_bf16 a[108:111], a[208:211], a[172:175], a[108:111]\n\t"
"v_mfma_f32_16x16x32_bf16 a[108:111], a[232:235], a[176:179], a[108:111]\n\t"
"s_waitcnt lgkmcnt(0)\n\t"
"s_barrier\n\t"
"v_mfma_f32_16x16x32_bf16 a[116:119], a[228:231], a[172:175], a[116:119]\n\t"
"ds_read_b128 a[168:171], v244 offset:0\n\t"
"v_mfma_f32_16x16x32_bf16 a[116:119], a[208:211], a[176:179], a[116:119]\n\t"
"ds_read_b128 a[172:175], v244 offset:512\n\t"
"v_mfma_f32_16x16x32_bf16 a[116:119], a[232:235], a[180:183], a[116:119]\n\t"
"v_mfma_f32_16x16x32_bf16 a[124:127], a[228:231], a[176:179], a[124:127]\n\t"
"ds_read_b128 a[176:179], v244 offset:1024\n\t"
"v_mfma_f32_16x16x32_bf16 a[124:127], a[208:211], a[180:183], a[124:127]\n\t"
"v_mfma_f32_16x16x32_bf16 a[124:127], a[232:235], a[184:187], a[124:127]\n\t"
"v_mfma_f32_16x16x32_bf16 a[132:135], a[228:231], a[180:183], a[132:135]\n\t"
"ds_read_b128 a[180:183], v244 offset:1536\n\t"
"v_mfma_f32_16x16x32_bf16 a[132:135], a[208:211], a[184:187], a[132:135]\n\t"
"v_mfma_f32_16x16x32_bf16 a[132:135], a[232:235], a[188:191], a[132:135]\n\t"
"v_mfma_f32_16x16x32_bf16 a[140:143], a[228:231], a[184:187], a[140:143]\n\t"
"ds_read_b128 a[184:187], v244 offset:2048\n\t"
"v_mfma_f32_16x16x32_bf16 a[140:143], a[208:211], a[188:191], a[140:143]\n\t"
"v_mfma_f32_16x16x32_bf16 a[140:143], a[232:235], a[192:195], a[140:143]\n\t"
"v_mfma_f32_16x16x32_bf16 a[148:151], a[228:231], a[188:191], a[148:151]\n\t"
"ds_read_b128 a[188:191], v244 offset:2560\n\t"
"v_mfma_f32_16x16x32_bf16 a[148:151], a[208:211], a[192:195], a[148:151]\n\t"
"v_mfma_f32_16x16x32_bf16 a[148:151], a[232:235], a[196:199], a[148:151]\n\t"
"v_mfma_f32_16x16x32_bf16 a[156:159], a[228:231], a[192:195], a[156:159]\n\t"
"ds_read_b128 a[192:195], v244 offset:3072\n\t"
"v_mfma_f32_16x16x32_bf16 a[156:159], a[208:211], a[196:199], a[156:159]\n\t"
"v_mfma_f32_16x16x32_bf16 a[156:159], a[232:235], a[200:203], a[156:159]\n\t"
"v_mfma_f32_16x16x32_bf16 a[164:167], a[228:231], a[196:199], a[164:167]\n\t"
"ds_read_b128 a[196:199], v244 offset:3584\n\t"
"v_mfma_f32_16x16x32_bf16 a[164:167], a[208:211], a[200:203], a[164:167]\n\t"
"ds_read_b128 a[200:203], v244 offset:4096\n\t"
"v_mfma_f32_16x16x32_bf16 a[164:167], a[232:235], a[204:207], a[164:167]\n\t"
"ds_read_b128 a[204:207], v244 offset:4608\n\t"
"Lst25_%=:\n\t"
"s_waitcnt vmcnt(9) lgkmcnt(7)\n\t"
"v_mfma_f32_16x16x32_bf16 v[0:3], a[236:239], a[168:171], v[0:3]\n\t"
"v_mfma_f32_16x16x32_bf16 v[0:3], a[240:243], a[172:175], v[0:3]\n\t"
"v_mfma_f32_16x16x32_bf16 v[0:3], a[244:247], a[176:179], v[0:3]\n\t"
"s_mov_b32 s20, 503808\n\t"
"buffer_load_dwordx4 v[240:243], v252, s[8:11], s20 offen offset:0 sc1\n\t"
"s_waitcnt lgkmcnt(6)\n\t"
"v_mfma_f32_16x16x32_bf16 v[4:7], a[236:239], a[172:175], v[4:7]\n\t"
"v_mfma_f32_16x16x32_bf16 v[4:7], a[240:243], a[176:179], v[4:7]\n\t"
"v_mfma_f32_16x16x32_bf16 v[4:7], a[244:247], a[180:183], v[4:7]\n\t"
"buffer_load_dwordx4 v[248:251], v252, s[8:11], s20 offen offset:1024 sc1\n\t"
"s_waitcnt lgkmcnt(5)\n\t"
"v_mfma_f32_16x16x32_bf16 v[8:11], a[236:239], a[176:179], v[8:11]\n\t"
"v_mfma_f32_16x16x32_bf16 v[8:11], a[240:243], a[180:183], v[8:11]\n\t"
"v_mfma_f32_16x16x32_bf16 v[8:11], a[244:247], a[184:187], v[8:11]\n\t"
"buffer_load_dwordx4 a[208:211], v252, s[8:11], s20 offen offset:2048 sc1\n\t"
"s_waitcnt lgkmcnt(4)\n\t"
"v_mfma_f32_16x16x32_bf16 v[12:15], a[236:239], a[180:183], v[12:15]\n\t"
"v_mfma_f32_16x16x32_bf16 v[12:15], a[240:243], a[184:187], v[12:15]\n\t"
"v_mfma_f32_16x16x32_bf16 v[12:15], a[244:247], a[188:191], v[12:15]\n\t"
"s_mov_b32 s20, 516096\n\t"
"buffer_load_dwordx4 a[212:215], v252, s[8:11], s20 offen offset:0 sc1\n\t"
"s_waitcnt lgkmcnt(3)\n\t"
"v_mfma_f32_16x16x32_bf16 v[16:19], a[236:239], a[184:187], v[16:19]\n\t"
"v_mfma_f32_16x16x32_bf16 v[16:19], a[240:243], a[188:191], v[16:19]\n\t"
"v_mfma_f32_16x16x32_bf16 v[16:19], a[244:247], a[192:195], v[16:19]\n\t"
"ds_read_b64_tr_b16 v[228:229], v246 offset:1536\n\t"
"ds_read_b64_tr_b16 v[230:231], v246 offset:4608\n\t"
"s_waitcnt lgkmcnt(4)\n\t"
"v_mfma_f32_16x16x32_bf16 v[20:23], a[236:239], a[188:191], v[20:23]\n\t"
"v_mfma_f32_16x16x32_bf16 v[20:23], a[240:243], a[192:195], v[20:23]\n\t"
"v_mfma_f32_16x16x32_bf16 v[20:23], a[244:247], a[196:199], v[20:23]\n\t"
"buffer_load_dwordx4 a[216:219], v252, s[8:11], s20 offen offset:1024 sc1\n\t"
"s_waitcnt lgkmcnt(3)\n\t"
"v_mfma_f32_16x16x32_bf16 v[24:27], a[236:239], a[192:195], v[24:27]\n\t"
"v_mfma_f32_16x16x32_bf16 v[24:27], a[240:243], a[196:199], v[24:27]\n\t"
"v_mfma_f32_16x16x32_bf16 v[24:27], a[244:247], a[200:203], v[24:27]\n\t"
"buffer_load_dwordx4 a[220:223], v252, s[8:11], s20 offen offset:2048 sc1\n\t"
"s_waitcnt lgkmcnt(2)\n\t"
"v_mfma_f32_16x16x32_bf16 v[28:31], a[236:239], a[196:199], v[28:31]\n\t"
"ds_read_b64_tr_b16 v[232:233], v246 offset:7680\n\t"
"v_mfma_f32_16x16x32_bf16 v[28:31], a[240:243], a[200:203], v[28:31]\n\t"
"ds_read_b64_tr_b16 v[234:235], v246 offset:10752\n\t"
"v_mfma_f32_16x16x32_bf16 v[28:31], a[244:247], a[204:207], v[28:31]\n\t"
"s_waitcnt vmcnt(6)\n\t"
"v_mfma_f32_16x16x32_bf16 v[32:35], v[96:99], a[168:171], v[32:35]\n\t"
"v_mfma_f32_16x16x32_bf16 v[32:35], a[248:251], a[172:175], v[32:35]\n\t"
"v_mfma_f32_16x16x32_bf16 v[32:35], v[236:239], a[176:179], v[32:35]\n\t"
"s_mov_b32 s20, 528384\n\t"
"buffer_load_dwordx4 a[224:227], v252, s[8:11], s20 offen offset:0 sc1\n\t"
"v_mfma_f32_16x16x32_bf16 v[36:39], v[96:99], a[172:175], v[36:39]\n\t"
"v_mfma_f32_16x16x32_bf16 v[36:39], a[248:251], a[176:179], v[36:39]\n\t"
"v_mfma_f32_16x16x32_bf16 v[36:39], v[236:239], a[180:183], v[36:39]\n\t"
"buffer_load_dwordx4 a[228:231], v252, s[8:11], s20 offen offset:1024 sc1\n\t"
"s_waitcnt lgkmcnt(2)\n\t"
"v_mfma_f32_16x16x32_bf16 v[228:231], v[228:231], v[220:223], 0\n\t"
"buffer_load_dwordx4 a[232:235], v252, s[8:11], s20 offen offset:2048 sc1\n\t"
"v_mfma_f32_16x16x32_bf16 v[40:43], v[96:99], a[176:179], v[40:43]\n\t"
"ds_read_b64_tr_b16 v[224:225], v246 offset:13824\n\t"
"v_mfma_f32_16x16x32_bf16 v[40:43], a[248:251], a[180:183], v[40:43]\n\t"
"ds_read_b64_tr_b16 v[226:227], v246 offset:16896\n\t"
"v_mfma_f32_16x16x32_bf16 v[40:43], v[236:239], a[184:187], v[40:43]\n\t"
"v_mfma_f32_16x16x32_bf16 v[44:47], v[96:99], a[180:183], v[44:47]\n\t"
"v_mfma_f32_16x16x32_bf16 v[44:47], a[248:251], a[184:187], v[44:47]\n\t"
"v_cvt_pk_bf16_f32 v228, v228, v229\n\t"
"v_cvt_pk_bf16_f32 v229, v230, v231\n\t"
"v_mfma_f32_16x16x32_bf16 v[44:47], v[236:239], a[188:191], v[44:47]\n\t"
"s_mov_b32 s20, 540672\n\t"
"buffer_load_dwordx4 a[236:239], v252, s[8:11], s20 offen offset:0 sc1\n\t"
"s_waitcnt lgkmcnt(2)\n\t"
"v_mfma_f32_16x16x32_bf16 v[232:235], v[232:235], v[220:223], 0\n\t"
"buffer_load_dwordx4 a[240:243], v252, s[8:11], s20 offen offset:1024 sc1\n\t"
"v_mfma_f32_16x16x32_bf16 v[48:51], v[96:99], a[184:187], v[48:51]\n\t"
"v_mfma_f32_16x16x32_bf16 v[48:51], a[248:251], a[188:191], v[48:51]\n\t"
"v_mfma_f32_16x16x32_bf16 v[48:51], v[236:239], a[192:195], v[48:51]\n\t"
"buffer_load_dwordx4 a[244:247], v252, s[8:11], s20 offen offset:2048 sc1\n\t"
"v_mfma_f32_16x16x32_bf16 v[52:55], v[96:99], a[188:191], v[52:55]\n\t"
"v_mfma_f32_16x16x32_bf16 v[52:55], a[248:251], a[192:195], v[52:55]\n\t"
"s_nop 0\n\t"
"v_cvt_pk_bf16_f32 v230, v232, v233\n\t"
"v_cvt_pk_bf16_f32 v231, v234, v235\n\t"
"v_mfma_f32_16x16x32_bf16 v[52:55], v[236:239], a[196:199], v[52:55]\n\t"
"s_mov_b32 s20, 700416\n\t"
"buffer_load_dwordx4 a[252:255], v252, s[8:11], s20 offen offset:0 sc1\n\t"
"v_mfma_f32_16x16x32_bf16 v[56:59], v[96:99], a[192:195], v[56:59]\n\t"
"ds_read_b64_tr_b16 v[232:233], v246 offset:21504\n\t"
"v_mfma_f32_16x16x32_bf16 v[56:59], a[248:251], a[196:199], v[56:59]\n\t"
"ds_read_b64_tr_b16 v[234:235], v246 offset:24576\n\t"
"v_mfma_f32_16x16x32_bf16 v[56:59], v[236:239], a[200:203], v[56:59]\n\t"
"s_waitcnt lgkmcnt(2)\n\t"
"v_mfma_f32_16x16x32_bf16 v[224:227], v[224:227], v[220:223], 0\n\t"
"v_mfma_f32_16x16x32_bf16 v[60:63], v[96:99], a[196:199], v[60:63]\n\t"
"v_mfma_f32_16x16x32_bf16 v[60:63], a[248:251], a[200:203], v[60:63]\n\t"
"v_mfma_f32_16x16x32_bf16 v[60:63], v[236:239], a[204:207], v[60:63]\n\t"
"buffer_load_dwordx4 v[96:99], v252, s[8:11], s20 offen offset:1024 sc1\n\t"
"s_waitcnt vmcnt(11)\n\t"
"v_mfma_f32_16x16x32_bf16 v[64:67], v[240:243], a[168:171], v[64:67]\n\t"
"ds_read_b128 a[168:171], v244 offset:9216\n\t"
"v_mfma_f32_16x16x32_bf16 v[64:67], v[248:251], a[172:175], v[64:67]\n\t"
"v_cvt_pk_bf16_f32 v224, v224, v225\n\t"
"v_cvt_pk_bf16_f32 v225, v226, v227\n\t"
"v_mfma_f32_16x16x32_bf16 v[64:67], a[208:211], a[176:179], v[64:67]\n\t"
"v_mfma_f32_16x16x32_bf16 v[68:71], v[240:243], a[172:175], v[68:71]\n\t"
"ds_read_b128 a[172:175], v244 offset:9728\n\t"
"v_mfma_f32_16x16x32_bf16 v[68:71], v[248:251], a[176:179], v[68:71]\n\t"
"v_mfma_f32_16x16x32_bf16 v[68:71], a[208:211], a[180:183], v[68:71]\n\t"
"buffer_load_dwordx4 v[236:239], v252, s[8:11], s20 offen offset:2048 sc1\n\t"
"s_waitcnt lgkmcnt(2)\n\t"
"v_mfma_f32_16x16x32_bf16 v[232:235], v[232:235], v[220:223], 0\n\t"
"s_mov_b32 s20, 712704\n\t"
"buffer_load_dwordx4 a[248:251], v252, s[8:11], s20 offen offset:0 sc1\n\t"
"v_mfma_f32_16x16x32_bf16 v[72:75], v[240:243], a[176:179], v[72:75]\n\t"
"ds_read_b128 a[176:179], v244 offset:10240\n\t"
"v_mfma_f32_16x16x32_bf16 v[72:75], v[248:251], a[180:183], v[72:75]\n\t"
"v_mfma_f32_16x16x32_bf16 v[72:75], a[208:211], a[184:187], v[72:75]\n\t"
"v_mfma_f32_16x16x32_bf16 v[76:79], v[240:243], a[180:183], v[76:79]\n\t"
"ds_read_b128 a[180:183], v244 offset:10752\n\t"
"v_mfma_f32_16x16x32_bf16 v[76:79], v[248:251], a[184:187], v[76:79]\n\t"
"v_cvt_pk_bf16_f32 v226, v232, v233\n\t"
"v_cvt_pk_bf16_f32 v227, v234, v235\n\t"
"v_mfma_f32_16x16x32_bf16 v[76:79], a[208:211], a[188:191], v[76:79]\n\t"
"ds_read_b64_tr_b16 v[232:233], v246 offset:27648\n\t"
"v_mfma_f32_16x16x32_bf16 v[80:83], v[240:243], a[184:187], v[80:83]\n\t"
"ds_read_b64_tr_b16 v[234:235], v246 offset:30720\n\t"
"v_mfma_f32_16x16x32_bf16 v[80:83], v[248:251], a[188:191], v[80:83]\n\t"
"ds_read_b128 a[184:187], v244 offset:11264\n\t"
"v_mfma_f32_16x16x32_bf16 v[80:83], a[208:211], a[192:195], v[80:83]\n\t"
"v_mfma_f32_16x16x32_bf16 v[84:87], v[240:243], a[188:191], v[84:87]\n\t"
"ds_read_b128 a[188:191], v244 offset:11776\n\t"
"v_mfma_f32_16x16x32_bf16 v[84:87], v[248:251], a[192:195], v[84:87]\n\t"
"v_mfma_f32_16x16x32_bf16 v[84:87], a[208:211], a[196:199], v[84:87]\n\t"
"v_mfma_f32_16x16x32_bf16 v[88:91], v[240:243], a[192:195], v[88:91]\n\t"
"ds_read_b128 a[192:195], v244 offset:12288\n\t"
"v_mfma_f32_16x16x32_bf16 v[88:91], v[248:251], a[196:199], v[88:91]\n\t"
"v_mfma_f32_16x16x32_bf16 v[88:91], a[208:211], a[200:203], v[88:91]\n\t"
"v_mfma_f32_16x16x32_bf16 v[92:95], v[240:243], a[196:199], v[92:95]\n\t"
"ds_read_b128 a[196:199], v244 offset:12800\n\t"
"v_mfma_f32_16x16x32_bf16 v[92:95], v[248:251], a[200:203], v[92:95]\n\t"
"ds_read_b128 a[200:203], v244 offset:13312\n\t"
"v_mfma_f32_16x16x32_bf16 v[92:95], a[208:211], a[204:207], v[92:95]\n\t"
"buffer_load_dwordx4 v[240:243], v252, s[8:11], s20 offen offset:1024 sc1\n\t"
"s_waitcnt lgkmcnt(5)\n\t"
"v_mfma_f32_16x16x32_bf16 v[232:235], v[232:235], v[220:223], 0\n\t"
"ds_read_b128 a[204:207], v244 offset:13824\n\t"
"s_waitcnt vmcnt(11)\n\t"
"v_mfma_f32_16x16x32_bf16 v[100:103], a[212:215], a[168:171], v[100:103]\n\t"
"v_mfma_f32_16x16x32_bf16 v[100:103], a[216:219], a[172:175], v[100:103]\n\t"
"v_mfma_f32_16x16x32_bf16 v[100:103], a[220:223], a[176:179], v[100:103]\n\t"
"buffer_load_dwordx4 v[248:251], v252, s[8:11], s20 offen offset:2048 sc1\n\t"
"v_mfma_f32_16x16x32_bf16 v[104:107], a[212:215], a[172:175], v[104:107]\n\t"
"v_mfma_f32_16x16x32_bf16 v[104:107], a[216:219], a[176:179], v[104:107]\n\t"
"v_cvt_pk_bf16_f32 v232, v232, v233\n\t"
"v_cvt_pk_bf16_f32 v233, v234, v235\n\t"
"v_mfma_f32_16x16x32_bf16 v[104:107], a[220:223], a[180:183], v[104:107]\n\t"
"s_mov_b32 s20, 724992\n\t"
"buffer_load_dwordx4 a[208:211], v252, s[8:11], s20 offen offset:0 sc1\n\t"
"s_waitcnt lgkmcnt(0)\n\t"
"s_barrier\n\t"
"v_mfma_f32_16x16x32_bf16 v[108:111], a[212:215], a[176:179], v[108:111]\n\t"
"v_mfma_f32_16x16x32_bf16 v[108:111], a[216:219], a[180:183], v[108:111]\n\t"
"v_mfma_f32_16x16x32_bf16 v[108:111], a[220:223], a[184:187], v[108:111]\n\t"
"ds_write2st64_b64 v245, v[228:229], v[230:231] offset0:0 offset1:1\n\t"
"v_mfma_f32_16x16x32_bf16 v[112:115], a[212:215], a[180:183], v[112:115]\n\t"
"ds_read_b64_tr_b16 v[228:229], v246 offset:33792\n\t"
"v_mfma_f32_16x16x32_bf16 v[112:115], a[216:219], a[184:187], v[112:115]\n\t"
"ds_read_b64_tr_b16 v[230:231], v246 offset:36864\n\t"
"v_mfma_f32_16x16x32_bf16 v[112:115], a[220:223], a[188:191], v[112:115]\n\t"
"ds_write2st64_b64 v245, v[224:225], v[226:227] offset0:2 offset1:3\n\t"
"v_mfma_f32_16x16x32_bf16 v[116:119], a[212:215], a[184:187], v[116:119]\n\t"
"ds_read_b64_tr_b16 v[224:225], v246 offset:41472\n\t"
"v_mfma_f32_16x16x32_bf16 v[116:119], a[216:219], a[188:191], v[116:119]\n\t"
"ds_read_b64_tr_b16 v[226:227], v246 offset:44544\n\t"
"v_mfma_f32_16x16x32_bf16 v[116:119], a[220:223], a[192:195], v[116:119]\n\t"
"ds_write_b64 v245, v[232:233] offset:2048\n\t"
"v_mfma_f32_16x16x32_bf16 v[120:123], a[212:215], a[188:191], v[120:123]\n\t"
"ds_read_b64_tr_b16 v[232:233], v246 offset:47616\n\t"
"v_mfma_f32_16x16x32_bf16 v[120:123], a[216:219], a[192:195], v[120:123]\n\t"
"ds_read_b64_tr_b16 v[234:235], v246 offset:50688\n\t"
"v_mfma_f32_16x16x32_bf16 v[120:123], a[220:223], a[196:199], v[120:123]\n\t"
"s_waitcnt lgkmcnt(6)\n\t"
"v_mfma_f32_16x16x32_bf16 v[228:231], v[228:231], v[220:223], 0\n\t"
"s_add_u32 m0, s24, 66304\n\t"
"s_add_u32 s28, s21, 1966080\n\t"
"buffer_load_dwordx4 v247, s[12:15], s28 offen offset:256 nt lds\n\t"
"v_mfma_f32_16x16x32_bf16 v[124:127], a[212:215], a[192:195], v[124:127]\n\t"
"v_mfma_f32_16x16x32_bf16 v[124:127], a[216:219], a[196:199], v[124:127]\n\t"
"v_mfma_f32_16x16x32_bf16 v[124:127], a[220:223], a[200:203], v[124:127]\n\t"
"s_add_u32 m0, s24, 72448\n\t"
"s_add_u32 s28, s21, 2162688\n\t"
"buffer_load_dwordx4 v247, s[12:15], s28 offen offset:256 nt lds\n\t"
"s_waitcnt lgkmcnt(3)\n\t"
"v_mfma_f32_16x16x32_bf16 v[224:227], v[224:227], v[220:223], 0\n\t"
"s_add_u32 m0, s24, 80128\n\t"
"s_add_u32 s28, s21, 2359296\n\t"
"buffer_load_dwordx4 v247, s[12:15], s28 offen offset:256 nt lds\n\t"
"v_mfma_f32_16x16x32_bf16 v[128:131], a[212:215], a[196:199], v[128:131]\n\t"
"v_cvt_pk_bf16_f32 v228, v228, v229\n\t"
"v_cvt_pk_bf16_f32 v229, v230, v231\n\t"
"v_mfma_f32_16x16x32_bf16 v[128:131], a[216:219], a[200:203], v[128:131]\n\t"
"v_mfma_f32_16x16x32_bf16 v[128:131], a[220:223], a[204:207], v[128:131]\n\t"
"s_add_u32 m0, s24, 86272\n\t"
"s_add_u32 s28, s21, 2555904\n\t"
"buffer_load_dwordx4 v247, s[12:15], s28 offen offset:256 nt lds\n\t"
"s_waitcnt lgkmcnt(0)\n\t"
"v_mfma_f32_16x16x32_bf16 v[232:235], v[232:235], v[220:223], 0\n\t"
"s_add_u32 m0, s24, 32512\n\t"
"s_add_u32 s28, s21, 983040\n\t"
"buffer_load_dwordx4 v247, s[12:15], s28 offen offset:256 nt lds\n\t"
"s_waitcnt vmcnt(15)\n\t"
"v_mfma_f32_16x16x32_bf16 v[132:135], a[224:227], a[168:171], v[132:135]\n\t"
"v_cvt_pk_bf16_f32 v230, v224, v225\n\t"
"v_cvt_pk_bf16_f32 v231, v226, v227\n\t"
"v_mfma_f32_16x16x32_bf16 v[132:135], a[228:231], a[172:175], v[132:135]\n\t"
"ds_read_b64_tr_b16 v[224:225], v246 offset:53760\n\t"
"v_mfma_f32_16x16x32_bf16 v[132:135], a[232:235], a[176:179], v[132:135]\n\t"
"s_add_u32 m0, s24, 40192\n\t"
"s_add_u32 s28, s21, 1179648\n\t"
"buffer_load_dwordx4 v247, s[12:15], s28 offen offset:256 nt lds\n\t"
"v_mfma_f32_16x16x32_bf16 v[136:139], a[224:227], a[172:175], v[136:139]\n\t"
"ds_read_b64_tr_b16 v[226:227], v246 offset:56832\n\t"
"v_mfma_f32_16x16x32_bf16 v[136:139], a[228:231], a[176:179], v[136:139]\n\t"
"v_cvt_pk_bf16_f32 v232, v232, v233\n\t"
"v_cvt_pk_bf16_f32 v233, v234, v235\n\t"
"v_mfma_f32_16x16x32_bf16 v[136:139], a[232:235], a[180:183], v[136:139]\n\t"
"ds_write2st64_b64 v245, v[228:229], v[230:231] offset0:5 offset1:6\n\t"
"v_mfma_f32_16x16x32_bf16 v[140:143], a[224:227], a[176:179], v[140:143]\n\t"
"v_mfma_f32_16x16x32_bf16 v[140:143], a[228:231], a[180:183], v[140:143]\n\t"
"v_mfma_f32_16x16x32_bf16 v[140:143], a[232:235], a[184:187], v[140:143]\n\t"
"v_mfma_f32_16x16x32_bf16 v[144:147], a[224:227], a[180:183], v[144:147]\n\t"
"v_mfma_f32_16x16x32_bf16 v[144:147], a[228:231], a[184:187], v[144:147]\n\t"
"v_mfma_f32_16x16x32_bf16 v[144:147], a[232:235], a[188:191], v[144:147]\n\t"
"v_mfma_f32_16x16x32_bf16 v[148:151], a[224:227], a[184:187], v[148:151]\n\t"
"v_mfma_f32_16x16x32_bf16 v[148:151], a[228:231], a[188:191], v[148:151]\n\t"
"v_mfma_f32_16x16x32_bf16 v[148:151], a[232:235], a[192:195], v[148:151]\n\t"
"s_waitcnt lgkmcnt(1)\n\t"
"v_mfma_f32_16x16x32_bf16 v[224:227], v[224:227], v[220:223], 0\n\t"
"v_mfma_f32_16x16x32_bf16 v[152:155], a[224:227], a[188:191], v[152:155]\n\t"
"v_mfma_f32_16x16x32_bf16 v[152:155], a[228:231], a[192:195], v[152:155]\n\t"
"v_mfma_f32_16x16x32_bf16 v[152:155], a[232:235], a[196:199], v[152:155]\n\t"
"v_mfma_f32_16x16x32_bf16 v[156:159], a[224:227], a[192:195], v[156:159]\n\t"
"v_mfma_f32_16x16x32_bf16 v[156:159], a[228:231], a[196:199], v[156:159]\n\t"
"s_nop 2\n\t"
"v_cvt_pk_bf16_f32 v234, v224, v225\n\t"
"v_cvt_pk_bf16_f32 v235, v226, v227\n\t"
"v_mfma_f32_16x16x32_bf16 v[156:159], a[232:235], a[200:203], v[156:159]\n\t"
"ds_write2st64_b64 v245, v[232:233], v[234:235] offset0:7 offset1:8\n\t"
"v_mfma_f32_16x16x32_bf16 v[160:163], a[224:227], a[196:199], v[160:163]\n\t"
"v_mfma_f32_16x16x32_bf16 v[160:163], a[228:231], a[200:203], v[160:163]\n\t"
"v_mfma_f32_16x16x32_bf16 v[160:163], a[232:235], a[204:207], v[160:163]\n\t"
"s_waitcnt vmcnt(13)\n\t"
"v_mfma_f32_16x16x32_bf16 v[164:167], a[236:239], a[168:171], v[164:167]\n\t"
"v_mfma_f32_16x16x32_bf16 v[164:167], a[240:243], a[172:175], v[164:167]\n\t"
"v_mfma_f32_16x16x32_bf16 v[164:167], a[244:247], a[176:179], v[164:167]\n\t"
"buffer_load_dwordx4 a[212:215], v252, s[8:11], s20 offen offset:1024 sc1\n\t"
"s_waitcnt lgkmcnt(0)\n\t"
"s_barrier\n\t"
"v_mfma_f32_16x16x32_bf16 v[168:171], a[236:239], a[172:175], v[168:171]\n\t"
"ds_read_b128 a[168:171], v244 offset:0\n\t"
"v_mfma_f32_16x16x32_bf16 v[168:171], a[240:243], a[176:179], v[168:171]\n\t"
"ds_read_b128 a[172:175], v244 offset:512\n\t"
"v_mfma_f32_16x16x32_bf16 v[168:171], a[244:247], a[180:183], v[168:171]\n\t"
"v_mfma_f32_16x16x32_bf16 v[172:175], a[236:239], a[176:179], v[172:175]\n\t"
"ds_read_b128 a[176:179], v244 offset:1024\n\t"
"v_mfma_f32_16x16x32_bf16 v[172:175], a[240:243], a[180:183], v[172:175]\n\t"
"v_mfma_f32_16x16x32_bf16 v[172:175], a[244:247], a[184:187], v[172:175]\n\t"
"v_mfma_f32_16x16x32_bf16 v[176:179], a[236:239], a[180:183], v[176:179]\n\t"
"ds_read_b128 a[180:183], v244 offset:1536\n\t"
"v_mfma_f32_16x16x32_bf16 v[176:179], a[240:243], a[184:187], v[176:179]\n\t"
"v_mfma_f32_16x16x32_bf16 v[176:179], a[244:247], a[188:191], v[176:179]\n\t"
"v_mfma_f32_16x16x32_bf16 v[180:183], a[236:239], a[184:187], v[180:183]\n\t"
"ds_read_b128 a[184:187], v244 offset:2048\n\t"
"v_mfma_f32_16x16x32_bf16 v[180:183], a[240:243], a[188:191], v[180:183]\n\t"
"v_mfma_f32_16x16x32_bf16 v[180:183], a[244:247], a[192:195], v[180:183]\n\t"
"v_mfma_f32_16x16x32_bf16 v[184:187], a[236:239], a[188:191], v[184:187]\n\t"
"ds_read_b128 a[188:191], v244 offset:2560\n\t"
"v_mfma_f32_16x16x32_bf16 v[184:187], a[240:243], a[192:195], v[184:187]\n\t"
"v_mfma_f32_16x16x32_bf16 v[184:187], a[244:247], a[196:199], v[184:187]\n\t"
"v_mfma_f32_16x16x32_bf16 v[188:191], a[236:239], a[192:195], v[188:191]\n\t"
"ds_read_b128 a[192:195], v244 offset:3072\n\t"
"v_mfma_f32_16x16x32_bf16 v[188:191], a[240:243], a[196:199], v[188:191]\n\t"
"v_mfma_f32_16x16x32_bf16 v[188:191], a[244:247], a[200:203], v[188:191]\n\t"
"v_mfma_f32_16x16x32_bf16 v[192:195], a[236:239], a[196:199], v[192:195]\n\t"
"ds_read_b128 a[196:199], v244 offset:3584\n\t"
"v_mfma_f32_16x16x32_bf16 v[192:195], a[240:243], a[200:203], v[192:195]\n\t"
"ds_read_b128 a[200:203], v244 offset:4096\n\t"
"v_mfma_f32_16x16x32_bf16 v[192:195], a[244:247], a[204:207], v[192:195]\n\t"
"Lst26_%=:\n\t"
"s_waitcnt vmcnt(11) lgkmcnt(6)\n\t"
"v_mfma_f32_16x16x32_bf16 v[0:3], a[252:255], a[168:171], v[0:3]\n\t"
"ds_read_b128 a[204:207], v244 offset:4608\n\t"
"v_mfma_f32_16x16x32_bf16 v[0:3], v[96:99], a[172:175], v[0:3]\n\t"
"v_xor_b32 v246, 0xc0, v246\n\t"
"v_mfma_f32_16x16x32_bf16 v[0:3], v[236:239], a[176:179], v[0:3]\n\t"
"s_add_u32 s23, s23, 6291456\n\t"
"s_add_u32 s22, s22, 6291456\n\t"
"s_waitcnt lgkmcnt(6)\n\t"
"v_mfma_f32_16x16x32_bf16 v[4:7], a[252:255], a[172:175], v[4:7]\n\t"
"v_mfma_f32_16x16x32_bf16 v[4:7], v[96:99], a[176:179], v[4:7]\n\t"
"v_mfma_f32_16x16x32_bf16 v[4:7], v[236:239], a[180:183], v[4:7]\n\t"
"s_waitcnt lgkmcnt(5)\n\t"
"v_mfma_f32_16x16x32_bf16 v[8:11], a[252:255], a[176:179], v[8:11]\n\t"
"v_mfma_f32_16x16x32_bf16 v[8:11], v[96:99], a[180:183], v[8:11]\n\t"
"v_mfma_f32_16x16x32_bf16 v[8:11], v[236:239], a[184:187], v[8:11]\n\t"
"s_waitcnt lgkmcnt(4)\n\t"
"v_mfma_f32_16x16x32_bf16 v[12:15], a[252:255], a[180:183], v[12:15]\n\t"
"v_mfma_f32_16x16x32_bf16 v[12:15], v[96:99], a[184:187], v[12:15]\n\t"
"v_mfma_f32_16x16x32_bf16 v[12:15], v[236:239], a[188:191], v[12:15]\n\t"
"buffer_load_dwordx4 a[216:219], v252, s[8:11], s20 offen offset:2048 sc1\n\t"
"s_waitcnt lgkmcnt(3)\n\t"
"v_mfma_f32_16x16x32_bf16 v[16:19], a[252:255], a[184:187], v[16:19]\n\t"
"v_mfma_f32_16x16x32_bf16 v[16:19], v[96:99], a[188:191], v[16:19]\n\t"
"v_mfma_f32_16x16x32_bf16 v[16:19], v[236:239], a[192:195], v[16:19]\n\t"
"ds_read_b64_tr_b16 v[228:229], v246 offset:1536\n\t"
"ds_read_b64_tr_b16 v[230:231], v246 offset:4608\n\t"
"s_waitcnt lgkmcnt(4)\n\t"
"v_mfma_f32_16x16x32_bf16 v[20:23], a[252:255], a[188:191], v[20:23]\n\t"
"v_mfma_f32_16x16x32_bf16 v[20:23], v[96:99], a[192:195], v[20:23]\n\t"
"v_mfma_f32_16x16x32_bf16 v[20:23], v[236:239], a[196:199], v[20:23]\n\t"
"s_mov_b32 s20, 737280\n\t"
"buffer_load_dwordx4 a[220:223], v252, s[8:11], s20 offen offset:0 sc1\n\t"
"s_waitcnt lgkmcnt(3)\n\t"
"v_mfma_f32_16x16x32_bf16 v[24:27], a[252:255], a[192:195], v[24:27]\n\t"
"v_mfma_f32_16x16x32_bf16 v[24:27], v[96:99], a[196:199], v[24:27]\n\t"
"v_mfma_f32_16x16x32_bf16 v[24:27], v[236:239], a[200:203], v[24:27]\n\t"
"buffer_load_dwordx4 a[224:227], v252, s[8:11], s20 offen offset:1024 sc1\n\t"
"s_waitcnt lgkmcnt(2)\n\t"
"v_mfma_f32_16x16x32_bf16 v[28:31], a[252:255], a[196:199], v[28:31]\n\t"
"ds_read_b64_tr_b16 v[224:225], v246 offset:7680\n\t"
"v_mfma_f32_16x16x32_bf16 v[28:31], v[96:99], a[200:203], v[28:31]\n\t"
"ds_read_b64_tr_b16 v[226:227], v246 offset:10752\n\t"
"v_mfma_f32_16x16x32_bf16 v[28:31], v[236:239], a[204:207], v[28:31]\n\t"
"s_waitcnt vmcnt(11)\n\t"
"v_mfma_f32_16x16x32_bf16 v[32:35], a[248:251], a[168:171], v[32:35]\n\t"
"v_mfma_f32_16x16x32_bf16 v[32:35], v[240:243], a[172:175], v[32:35]\n\t"
"v_mfma_f32_16x16x32_bf16 v[32:35], v[248:251], a[176:179], v[32:35]\n\t"
"buffer_load_dwordx4 v[96:99], v252, s[8:11], s20 offen offset:2048 sc1\n\t"
"v_mfma_f32_16x16x32_bf16 v[36:39], a[248:251], a[172:175], v[36:39]\n\t"
"v_mfma_f32_16x16x32_bf16 v[36:39], v[240:243], a[176:179], v[36:39]\n\t"
"v_mfma_f32_16x16x32_bf16 v[36:39], v[248:251], a[180:183], v[36:39]\n\t"
"s_mov_b32 s20, 749568\n\t"
"buffer_load_dwordx4 v[236:239], v252, s[8:11], s20 offen offset:0 sc1\n\t"
"s_waitcnt lgkmcnt(2)\n\t"
"v_mfma_f32_16x16x32_bf16 v[228:231], v[228:231], v[220:223], 0\n\t"
"buffer_load_dwordx4 a[228:231], v252, s[8:11], s20 offen offset:1024 sc1\n\t"
"v_mfma_f32_16x16x32_bf16 v[40:43], a[248:251], a[176:179], v[40:43]\n\t"
"ds_read_b64_tr_b16 v[232:233], v246 offset:13824\n\t"
"v_mfma_f32_16x16x32_bf16 v[40:43], v[240:243], a[180:183], v[40:43]\n\t"
"v_mfma_f32_16x16x32_bf16 v[40:43], v[248:251], a[184:187], v[40:43]\n\t"
"ds_read_b64_tr_b16 v[234:235], v246 offset:16896\n\t"
"v_mfma_f32_16x16x32_bf16 v[44:47], a[248:251], a[180:183], v[44:47]\n\t"
"v_mfma_f32_16x16x32_bf16 v[44:47], v[240:243], a[184:187], v[44:47]\n\t"
"v_cvt_pk_bf16_f32 v228, v228, v229\n\t"
"v_cvt_pk_bf16_f32 v229, v230, v231\n\t"
"v_mfma_f32_16x16x32_bf16 v[44:47], v[248:251], a[188:191], v[44:47]\n\t"
"s_waitcnt lgkmcnt(2)\n\t"
"v_mfma_f32_16x16x32_bf16 v[224:227], v[224:227], v[220:223], 0\n\t"
"buffer_load_dwordx4 a[232:235], v252, s[8:11], s20 offen offset:2048 sc1\n\t"
"v_mfma_f32_16x16x32_bf16 v[48:51], a[248:251], a[184:187], v[48:51]\n\t"
"v_mfma_f32_16x16x32_bf16 v[48:51], v[240:243], a[188:191], v[48:51]\n\t"
"v_mfma_f32_16x16x32_bf16 v[48:51], v[248:251], a[192:195], v[48:51]\n\t"
"s_mov_b32 s20, 761856\n\t"
"buffer_load_dwordx4 a[236:239], v252, s[8:11], s20 offen offset:0 sc1\n\t"
"v_mfma_f32_16x16x32_bf16 v[52:55], a[248:251], a[188:191], v[52:55]\n\t"
"v_mfma_f32_16x16x32_bf16 v[52:55], v[240:243], a[192:195], v[52:55]\n\t"
"v_cvt_pk_bf16_f32 v230, v224, v225\n\t"
"v_cvt_pk_bf16_f32 v231, v226, v227\n\t"
"v_mfma_f32_16x16x32_bf16 v[52:55], v[248:251], a[196:199], v[52:55]\n\t"
"buffer_load_dwordx4 a[240:243], v252, s[8:11], s20 offen offset:1024 sc1\n\t"
"v_mfma_f32_16x16x32_bf16 v[56:59], a[248:251], a[192:195], v[56:59]\n\t"
"ds_read_b64_tr_b16 v[224:225], v246 offset:21504\n\t"
"v_mfma_f32_16x16x32_bf16 v[56:59], v[240:243], a[196:199], v[56:59]\n\t"
"ds_read_b64_tr_b16 v[226:227], v246 offset:24576\n\t"
"v_mfma_f32_16x16x32_bf16 v[56:59], v[248:251], a[200:203], v[56:59]\n\t"
"s_waitcnt lgkmcnt(2)\n\t"
"v_mfma_f32_16x16x32_bf16 v[232:235], v[232:235], v[220:223], 0\n\t"
"buffer_load_dwordx4 a[244:247], v252, s[8:11], s20 offen offset:2048 sc1\n\t"
"v_mfma_f32_16x16x32_bf16 v[60:63], a[248:251], a[196:199], v[60:63]\n\t"
"v_mfma_f32_16x16x32_bf16 v[60:63], v[240:243], a[200:203], v[60:63]\n\t"
"v_mfma_f32_16x16x32_bf16 v[60:63], v[248:251], a[204:207], v[60:63]\n\t"
"s_mov_b32 s20, 774144\n\t"
"buffer_load_dwordx4 v[240:243], v252, s[8:11], s20 offen offset:0 sc1\n\t"
"s_waitcnt vmcnt(10)\n\t"
"v_mfma_f32_16x16x32_bf16 v[64:67], a[208:211], a[168:171], v[64:67]\n\t"
"ds_read_b128 a[168:171], v244 offset:9216\n\t"
"v_mfma_f32_16x16x32_bf16 v[64:67], a[212:215], a[172:175], v[64:67]\n\t"
"v_cvt_pk_bf16_f32 v232, v232, v233\n\t"
"v_mfma_f32_16x16x32_bf16 v[64:67], a[216:219], a[176:179], v[64:67]\n\t"
"v_cvt_pk_bf16_f32 v233, v234, v235\n\t"
"v_mfma_f32_16x16x32_bf16 v[68:71], a[208:211], a[172:175], v[68:71]\n\t"
"ds_read_b128 a[172:175], v244 offset:9728\n\t"
"v_mfma_f32_16x16x32_bf16 v[68:71], a[212:215], a[176:179], v[68:71]\n\t"
"v_mfma_f32_16x16x32_bf16 v[68:71], a[216:219], a[180:183], v[68:71]\n\t"
"buffer_load_dwordx4 v[248:251], v252, s[8:11], s20 offen offset:1024 sc1\n\t"
"s_waitcnt lgkmcnt(2)\n\t"
"v_mfma_f32_16x16x32_bf16 v[224:227], v[224:227], v[220:223], 0\n\t"
"v_mfma_f32_16x16x32_bf16 v[72:75], a[208:211], a[176:179], v[72:75]\n\t"
"ds_read_b128 a[176:179], v244 offset:10240\n\t"
"v_mfma_f32_16x16x32_bf16 v[72:75], a[212:215], a[180:183], v[72:75]\n\t"
"v_mfma_f32_16x16x32_bf16 v[72:75], a[216:219], a[184:187], v[72:75]\n\t"
"buffer_load_dwordx4 a[248:251], v252, s[8:11], s20 offen offset:2048 sc1\n\t"
"v_mfma_f32_16x16x32_bf16 v[76:79], a[208:211], a[180:183], v[76:79]\n\t"
"ds_read_b128 a[180:183], v244 offset:10752\n\t"
"v_mfma_f32_16x16x32_bf16 v[76:79], a[212:215], a[184:187], v[76:79]\n\t"
"v_cvt_pk_bf16_f32 v234, v224, v225\n\t"
"v_cvt_pk_bf16_f32 v235, v226, v227\n\t"
"v_mfma_f32_16x16x32_bf16 v[76:79], a[216:219], a[188:191], v[76:79]\n\t"
"s_mov_b32 s20, 786432\n\t"
"buffer_load_dwordx4 a[252:255], v252, s[8:11], s20 offen offset:0 sc1\n\t"
"v_mfma_f32_16x16x32_bf16 v[80:83], a[208:211], a[184:187], v[80:83]\n\t"
"ds_read_b64_tr_b16 v[224:225], v246 offset:27648\n\t"
"v_mfma_f32_16x16x32_bf16 v[80:83], a[212:215], a[188:191], v[80:83]\n\t"
"ds_read_b64_tr_b16 v[226:227], v246 offset:30720\n\t"
"v_mfma_f32_16x16x32_bf16 v[80:83], a[216:219], a[192:195], v[80:83]\n\t"
"ds_read_b128 a[184:187], v244 offset:11264\n\t"
"v_mfma_f32_16x16x32_bf16 v[84:87], a[208:211], a[188:191], v[84:87]\n\t"
"ds_read_b128 a[188:191], v244 offset:11776\n\t"
"v_mfma_f32_16x16x32_bf16 v[84:87], a[212:215], a[192:195], v[84:87]\n\t"
"v_mfma_f32_16x16x32_bf16 v[84:87], a[216:219], a[196:199], v[84:87]\n\t"
"v_mfma_f32_16x16x32_bf16 v[88:91], a[208:211], a[192:195], v[88:91]\n\t"
"ds_read_b128 a[192:195], v244 offset:12288\n\t"
"v_mfma_f32_16x16x32_bf16 v[88:91], a[212:215], a[196:199], v[88:91]\n\t"
"v_mfma_f32_16x16x32_bf16 v[88:91], a[216:219], a[200:203], v[88:91]\n\t"
"v_mfma_f32_16x16x32_bf16 v[92:95], a[208:211], a[196:199], v[92:95]\n\t"
"ds_read_b128 a[196:199], v244 offset:12800\n\t"
"v_mfma_f32_16x16x32_bf16 v[92:95], a[212:215], a[200:203], v[92:95]\n\t"
"ds_read_b128 a[200:203], v244 offset:13312\n\t"
"v_mfma_f32_16x16x32_bf16 v[92:95], a[216:219], a[204:207], v[92:95]\n\t"
"buffer_load_dwordx4 a[208:211], v252, s[8:11], s20 offen offset:1024 sc1\n\t"
"s_waitcnt lgkmcnt(5)\n\t"
"v_mfma_f32_16x16x32_bf16 v[224:227], v[224:227], v[220:223], 0\n\t"
"ds_read_b128 a[204:207], v244 offset:13824\n\t"
"s_waitcnt vmcnt(11)\n\t"
"v_mfma_f32_16x16x32_bf16 v[100:103], a[220:223], a[168:171], v[100:103]\n\t"
"v_mfma_f32_16x16x32_bf16 v[100:103], a[224:227], a[172:175], v[100:103]\n\t"
"v_mfma_f32_16x16x32_bf16 v[100:103], v[96:99], a[176:179], v[100:103]\n\t"
"buffer_load_dwordx4 a[212:215], v252, s[8:11], s20 offen offset:2048 sc1\n\t"
"v_mfma_f32_16x16x32_bf16 v[104:107], a[220:223], a[172:175], v[104:107]\n\t"
"v_mfma_f32_16x16x32_bf16 v[104:107], a[224:227], a[176:179], v[104:107]\n\t"
"v_cvt_pk_bf16_f32 v224, v224, v225\n\t"
"v_cvt_pk_bf16_f32 v225, v226, v227\n\t"
"v_mfma_f32_16x16x32_bf16 v[104:107], v[96:99], a[180:183], v[104:107]\n\t"
"s_mov_b32 s20, 798720\n\t"
"buffer_load_dwordx4 a[216:219], v252, s[8:11], s20 offen offset:0 sc1\n\t"
"s_waitcnt lgkmcnt(0)\n\t"
"s_barrier\n\t"
"v_mfma_f32_16x16x32_bf16 v[108:111], a[220:223], a[176:179], v[108:111]\n\t"
"v_mfma_f32_16x16x32_bf16 v[108:111], a[224:227], a[180:183], v[108:111]\n\t"
"v_mfma_f32_16x16x32_bf16 v[108:111], v[96:99], a[184:187], v[108:111]\n\t"
"ds_write2st64_b64 v245, v[228:229], v[230:231] offset0:0 offset1:1\n\t"
"v_mfma_f32_16x16x32_bf16 v[112:115], a[220:223], a[180:183], v[112:115]\n\t"
"ds_read_b64_tr_b16 v[228:229], v246 offset:33792\n\t"
"v_mfma_f32_16x16x32_bf16 v[112:115], a[224:227], a[184:187], v[112:115]\n\t"
"ds_read_b64_tr_b16 v[230:231], v246 offset:36864\n\t"
"v_mfma_f32_16x16x32_bf16 v[112:115], v[96:99], a[188:191], v[112:115]\n\t"
"ds_write2st64_b64 v245, v[232:233], v[234:235] offset0:2 offset1:3\n\t"
"v_mfma_f32_16x16x32_bf16 v[116:119], a[220:223], a[184:187], v[116:119]\n\t"
"ds_read_b64_tr_b16 v[232:233], v246 offset:41472\n\t"
"v_mfma_f32_16x16x32_bf16 v[116:119], a[224:227], a[188:191], v[116:119]\n\t"
"ds_read_b64_tr_b16 v[234:235], v246 offset:44544\n\t"
"v_mfma_f32_16x16x32_bf16 v[116:119], v[96:99], a[192:195], v[116:119]\n\t"
"ds_write_b64 v245, v[224:225] offset:2048\n\t"
"v_mfma_f32_16x16x32_bf16 v[120:123], a[220:223], a[188:191], v[120:123]\n\t"
"ds_read_b64_tr_b16 v[224:225], v246 offset:47616\n\t"
"v_mfma_f32_16x16x32_bf16 v[120:123], a[224:227], a[192:195], v[120:123]\n\t"
"ds_read_b64_tr_b16 v[226:227], v246 offset:50688\n\t"
"v_mfma_f32_16x16x32_bf16 v[120:123], v[96:99], a[196:199], v[120:123]\n\t"
"s_waitcnt lgkmcnt(6)\n\t"
"v_mfma_f32_16x16x32_bf16 v[228:231], v[228:231], v[220:223], 0\n\t"
"v_mfma_f32_16x16x32_bf16 v[124:127], a[220:223], a[192:195], v[124:127]\n\t"
"v_mfma_f32_16x16x32_bf16 v[124:127], a[224:227], a[196:199], v[124:127]\n\t"
"v_mfma_f32_16x16x32_bf16 v[124:127], v[96:99], a[200:203], v[124:127]\n\t"
"s_waitcnt lgkmcnt(3)\n\t"
"v_mfma_f32_16x16x32_bf16 v[232:235], v[232:235], v[220:223], 0\n\t"
"v_mfma_f32_16x16x32_bf16 v[128:131], a[220:223], a[196:199], v[128:131]\n\t"
"s_nop 1\n\t"
"v_cvt_pk_bf16_f32 v228, v228, v229\n\t"
"v_cvt_pk_bf16_f32 v229, v230, v231\n\t"
"v_mfma_f32_16x16x32_bf16 v[128:131], a[224:227], a[200:203], v[128:131]\n\t"
"v_mfma_f32_16x16x32_bf16 v[128:131], v[96:99], a[204:207], v[128:131]\n\t"
"buffer_load_dwordx4 a[220:223], v252, s[8:11], s20 offen offset:1024 sc1\n\t"
"s_waitcnt lgkmcnt(0)\n\t"
"v_mfma_f32_16x16x32_bf16 v[224:227], v[224:227], v[220:223], 0\n\t"
"buffer_load_dwordx4 v[96:99], v252, s[8:11], s20 offen offset:2048 sc1\n\t"
"s_waitcnt vmcnt(12)\n\t"
"v_mfma_f32_16x16x32_bf16 v[132:135], v[236:239], a[168:171], v[132:135]\n\t"
"v_cvt_pk_bf16_f32 v230, v232, v233\n\t"
"v_cvt_pk_bf16_f32 v231, v234, v235\n\t"
"v_mfma_f32_16x16x32_bf16 v[132:135], a[228:231], a[172:175], v[132:135]\n\t"
"ds_read_b64_tr_b16 v[232:233], v246 offset:53760\n\t"
"v_mfma_f32_16x16x32_bf16 v[132:135], a[232:235], a[176:179], v[132:135]\n\t"
"s_mov_b32 s20, 811008\n\t"
"buffer_load_dwordx4 a[224:227], v252, s[8:11], s20 offen offset:0 sc1\n\t"
"v_mfma_f32_16x16x32_bf16 v[136:139], v[236:239], a[172:175], v[136:139]\n\t"
"ds_read_b64_tr_b16 v[234:235], v246 offset:56832\n\t"
"v_mfma_f32_16x16x32_bf16 v[136:139], a[228:231], a[176:179], v[136:139]\n\t"
"v_cvt_pk_bf16_f32 v224, v224, v225\n\t"
"v_cvt_pk_bf16_f32 v225, v226, v227\n\t"
"v_mfma_f32_16x16x32_bf16 v[136:139], a[232:235], a[180:183], v[136:139]\n\t"
"s_add_u32 m0, s24, 46336\n\t"
"s_add_u32 s28, s21, 1376256\n\t"
"buffer_load_dwordx4 v247, s[12:15], s28 offen offset:256 nt lds\n\t"
"s_add_u32 m0, s24, 52480\n\t"
"s_add_u32 s28, s21, 1572864\n\t"
"buffer_load_dwordx4 v247, s[12:15], s28 offen offset:256 nt lds\n\t"
"v_mfma_f32_16x16x32_bf16 v[140:143], v[236:239], a[176:179], v[140:143]\n\t"
"v_mfma_f32_16x16x32_bf16 v[140:143], a[228:231], a[180:183], v[140:143]\n\t"
"v_mfma_f32_16x16x32_bf16 v[140:143], a[232:235], a[184:187], v[140:143]\n\t"
"ds_write2st64_b64 v245, v[228:229], v[230:231] offset0:5 offset1:6\n\t"
"s_add_u32 m0, s24, 92416\n\t"
"s_add_u32 s28, s21, 2752512\n\t"
"buffer_load_dwordx4 v247, s[12:15], s28 offen offset:256 nt lds\n\t"
"v_mfma_f32_16x16x32_bf16 v[144:147], v[236:239], a[180:183], v[144:147]\n\t"
"v_mfma_f32_16x16x32_bf16 v[144:147], a[228:231], a[184:187], v[144:147]\n\t"
"v_mfma_f32_16x16x32_bf16 v[144:147], a[232:235], a[188:191], v[144:147]\n\t"
"s_add_u32 m0, s24, 100096\n\t"
"s_add_u32 s28, s21, 2949120\n\t"
"buffer_load_dwordx4 v247, s[12:15], s28 offen offset:256 nt lds\n\t"
"s_add_u32 m0, s24, 106240\n\t"
"s_add_u32 s28, s21, 3145728\n\t"
"buffer_load_dwordx4 v247, s[12:15], s28 offen offset:256 nt lds\n\t"
"v_mfma_f32_16x16x32_bf16 v[148:151], v[236:239], a[184:187], v[148:151]\n\t"
"v_mfma_f32_16x16x32_bf16 v[148:151], a[228:231], a[188:191], v[148:151]\n\t"
"v_mfma_f32_16x16x32_bf16 v[148:151], a[232:235], a[192:195], v[148:151]\n\t"
"s_add_u32 m0, s24, 112384\n\t"
"s_add_u32 s28, s21, 3342336\n\t"
"buffer_load_dwordx4 v247, s[12:15], s28 offen offset:256 nt lds\n\t"
"s_waitcnt lgkmcnt(1)\n\t"
"v_mfma_f32_16x16x32_bf16 v[232:235], v[232:235], v[220:223], 0\n\t"
"s_add_u32 m0, s25, 256\n\t"
"s_nop 0\n\t"
"buffer_load_dwordx4 v254, s[12:15], 0 offen offset:256 nt lds\n\t"
"v_mfma_f32_16x16x32_bf16 v[152:155], v[236:239], a[188:191], v[152:155]\n\t"
"v_mfma_f32_16x16x32_bf16 v[152:155], a[228:231], a[192:195], v[152:155]\n\t"
"v_mfma_f32_16x16x32_bf16 v[152:155], a[232:235], a[196:199], v[152:155]\n\t"
"s_add_u32 m0, s26, 256\n\t"
"s_mov_b64 exec, s[36:37]\n\t"
"buffer_load_dwordx4 v254, s[12:15], s27 offen offset:256 nt lds\n\t"
"s_mov_b64 exec, -1\n\t"
"v_mfma_f32_16x16x32_bf16 v[156:159], v[236:239], a[192:195], v[156:159]\n\t"
"v_mfma_f32_16x16x32_bf16 v[156:159], a[228:231], a[196:199], v[156:159]\n\t"
"v_cvt_pk_bf16_f32 v226, v232, v233\n\t"
"v_cvt_pk_bf16_f32 v227, v234, v235\n\t"
"v_mfma_f32_16x16x32_bf16 v[156:159], a[232:235], a[200:203], v[156:159]\n\t"
"ds_write2st64_b64 v245, v[224:225], v[226:227] offset0:7 offset1:8\n\t"
"v_mfma_f32_16x16x32_bf16 v[160:163], v[236:239], a[196:199], v[160:163]\n\t"
"v_mfma_f32_16x16x32_bf16 v[160:163], a[228:231], a[200:203], v[160:163]\n\t"
"v_mfma_f32_16x16x32_bf16 v[160:163], a[232:235], a[204:207], v[160:163]\n\t"
"s_waitcnt vmcnt(18)\n\t"
"v_mfma_f32_16x16x32_bf16 v[164:167], a[236:239], a[168:171], v[164:167]\n\t"
"v_mfma_f32_16x16x32_bf16 v[164:167], a[240:243], a[172:175], v[164:167]\n\t"
"v_mfma_f32_16x16x32_bf16 v[164:167], a[244:247], a[176:179], v[164:167]\n\t"
"s_waitcnt lgkmcnt(0)\n\t"
"s_barrier\n\t"
"v_mfma_f32_16x16x32_bf16 v[168:171], a[236:239], a[172:175], v[168:171]\n\t"
"ds_read_b128 a[168:171], v244 offset:0\n\t"
"v_mfma_f32_16x16x32_bf16 v[168:171], a[240:243], a[176:179], v[168:171]\n\t"
"ds_read_b128 a[172:175], v244 offset:512\n\t"
"v_mfma_f32_16x16x32_bf16 v[168:171], a[244:247], a[180:183], v[168:171]\n\t"
"v_mfma_f32_16x16x32_bf16 v[172:175], a[236:239], a[176:179], v[172:175]\n\t"
"ds_read_b128 a[176:179], v244 offset:1024\n\t"
"v_mfma_f32_16x16x32_bf16 v[172:175], a[240:243], a[180:183], v[172:175]\n\t"
"v_mfma_f32_16x16x32_bf16 v[172:175], a[244:247], a[184:187], v[172:175]\n\t"
"v_mfma_f32_16x16x32_bf16 v[176:179], a[236:239], a[180:183], v[176:179]\n\t"
"ds_read_b128 a[180:183], v244 offset:1536\n\t"
"v_mfma_f32_16x16x32_bf16 v[176:179], a[240:243], a[184:187], v[176:179]\n\t"
"v_mfma_f32_16x16x32_bf16 v[176:179], a[244:247], a[188:191], v[176:179]\n\t"
"v_mfma_f32_16x16x32_bf16 v[180:183], a[236:239], a[184:187], v[180:183]\n\t"
"ds_read_b128 a[184:187], v244 offset:2048\n\t"
"v_mfma_f32_16x16x32_bf16 v[180:183], a[240:243], a[188:191], v[180:183]\n\t"
"v_mfma_f32_16x16x32_bf16 v[180:183], a[244:247], a[192:195], v[180:183]\n\t"
"v_mfma_f32_16x16x32_bf16 v[184:187], a[236:239], a[188:191], v[184:187]\n\t"
"ds_read_b128 a[188:191], v244 offset:2560\n\t"
"v_mfma_f32_16x16x32_bf16 v[184:187], a[240:243], a[192:195], v[184:187]\n\t"
"v_mfma_f32_16x16x32_bf16 v[184:187], a[244:247], a[196:199], v[184:187]\n\t"
"v_mfma_f32_16x16x32_bf16 v[188:191], a[236:239], a[192:195], v[188:191]\n\t"
"ds_read_b128 a[192:195], v244 offset:3072\n\t"
"v_mfma_f32_16x16x32_bf16 v[188:191], a[240:243], a[196:199], v[188:191]\n\t"
"v_mfma_f32_16x16x32_bf16 v[188:191], a[244:247], a[200:203], v[188:191]\n\t"
"v_mfma_f32_16x16x32_bf16 v[192:195], a[236:239], a[196:199], v[192:195]\n\t"
"ds_read_b128 a[196:199], v244 offset:3584\n\t"
"v_mfma_f32_16x16x32_bf16 v[192:195], a[240:243], a[200:203], v[192:195]\n\t"
"ds_read_b128 a[200:203], v244 offset:4096\n\t"
"v_mfma_f32_16x16x32_bf16 v[192:195], a[244:247], a[204:207], v[192:195]\n\t"
"Lst27_%=:\n\t"
"s_waitcnt vmcnt(15) lgkmcnt(6)\n\t"
"v_mfma_f32_16x16x32_bf16 v[196:199], v[240:243], a[168:171], v[196:199]\n\t"
"ds_read_b128 a[204:207], v244 offset:4608\n\t"
"v_mfma_f32_16x16x32_bf16 v[196:199], v[248:251], a[172:175], v[196:199]\n\t"
"v_mfma_f32_16x16x32_bf16 v[196:199], a[248:251], a[176:179], v[196:199]\n\t"
"s_waitcnt lgkmcnt(6)\n\t"
"v_mfma_f32_16x16x32_bf16 v[204:207], v[240:243], a[172:175], v[204:207]\n\t"
"v_mfma_f32_16x16x32_bf16 v[204:207], v[248:251], a[176:179], v[204:207]\n\t"
"v_mfma_f32_16x16x32_bf16 v[204:207], a[248:251], a[180:183], v[204:207]\n\t"
"s_waitcnt lgkmcnt(5)\n\t"
"v_mfma_f32_16x16x32_bf16 v[212:215], v[240:243], a[176:179], v[212:215]\n\t"
"v_mfma_f32_16x16x32_bf16 v[212:215], v[248:251], a[180:183], v[212:215]\n\t"
"v_mfma_f32_16x16x32_bf16 v[212:215], a[248:251], a[184:187], v[212:215]\n\t"
"s_waitcnt lgkmcnt(4)\n\t"
"v_mfma_f32_16x16x32_bf16 a[0:3], v[240:243], a[180:183], a[0:3]\n\t"
"v_mfma_f32_16x16x32_bf16 a[0:3], v[248:251], a[184:187], a[0:3]\n\t"
"v_mfma_f32_16x16x32_bf16 a[0:3], a[248:251], a[188:191], a[0:3]\n\t"
"buffer_load_dwordx4 v[236:239], v252, s[8:11], s20 offen offset:1024 sc1\n\t"
"s_waitcnt lgkmcnt(3)\n\t"
"v_mfma_f32_16x16x32_bf16 a[8:11], v[240:243], a[184:187], a[8:11]\n\t"
"v_mfma_f32_16x16x32_bf16 a[8:11], v[248:251], a[188:191], a[8:11]\n\t"
"v_mfma_f32_16x16x32_bf16 a[8:11], a[248:251], a[192:195], a[8:11]\n\t"
"ds_read_b64_tr_b16 v[228:229], v246 offset:512\n\t"
"ds_read_b64_tr_b16 v[230:231], v246 offset:3584\n\t"
"s_waitcnt lgkmcnt(4)\n\t"
"v_mfma_f32_16x16x32_bf16 a[16:19], v[240:243], a[188:191], a[16:19]\n\t"
"v_mfma_f32_16x16x32_bf16 a[16:19], v[248:251], a[192:195], a[16:19]\n\t"
"v_mfma_f32_16x16x32_bf16 a[16:19], a[248:251], a[196:199], a[16:19]\n\t"
"s_waitcnt lgkmcnt(3)\n\t"
"v_mfma_f32_16x16x32_bf16 a[24:27], v[240:243], a[192:195], a[24:27]\n\t"
"v_mfma_f32_16x16x32_bf16 a[24:27], v[248:251], a[196:199], a[24:27]\n\t"
"v_mfma_f32_16x16x32_bf16 a[24:27], a[248:251], a[200:203], a[24:27]\n\t"
"s_waitcnt lgkmcnt(2)\n\t"
"v_mfma_f32_16x16x32_bf16 a[32:35], v[240:243], a[196:199], a[32:35]\n\t"
"ds_read_b64_tr_b16 v[232:233], v246 offset:6656\n\t"
"v_mfma_f32_16x16x32_bf16 a[32:35], v[248:251], a[200:203], a[32:35]\n\t"
"ds_read_b64_tr_b16 v[234:235], v246 offset:9728\n\t"
"v_mfma_f32_16x16x32_bf16 a[32:35], a[248:251], a[204:207], a[32:35]\n\t"
"s_waitcnt vmcnt(13)\n\t"
"v_mfma_f32_16x16x32_bf16 a[40:43], a[252:255], a[168:171], a[40:43]\n\t"
"v_mfma_f32_16x16x32_bf16 a[40:43], a[208:211], a[172:175], a[40:43]\n\t"
"v_mfma_f32_16x16x32_bf16 a[40:43], a[212:215], a[176:179], a[40:43]\n\t"
"v_mfma_f32_16x16x32_bf16 a[48:51], a[252:255], a[172:175], a[48:51]\n\t"
"v_mfma_f32_16x16x32_bf16 a[48:51], a[208:211], a[176:179], a[48:51]\n\t"
"v_mfma_f32_16x16x32_bf16 a[48:51], a[212:215], a[180:183], a[48:51]\n\t"
"s_waitcnt lgkmcnt(2)\n\t"
"v_mfma_f32_16x16x32_bf16 v[228:231], v[228:231], v[220:223], 0\n\t"
"v_mfma_f32_16x16x32_bf16 a[56:59], a[252:255], a[176:179], a[56:59]\n\t"
"ds_read_b64_tr_b16 v[224:225], v246 offset:12800\n\t"
"v_mfma_f32_16x16x32_bf16 a[56:59], a[208:211], a[180:183], a[56:59]\n\t"
"ds_read_b64_tr_b16 v[226:227], v246 offset:15872\n\t"
"v_mfma_f32_16x16x32_bf16 a[56:59], a[212:215], a[184:187], a[56:59]\n\t"
"v_mfma_f32_16x16x32_bf16 a[64:67], a[252:255], a[180:183], a[64:67]\n\t"
"v_mfma_f32_16x16x32_bf16 a[64:67], a[208:211], a[184:187], a[64:67]\n\t"
"s_nop 0\n\t"
"v_cvt_pk_bf16_f32 v228, v228, v229\n\t"
"v_mfma_f32_16x16x32_bf16 a[64:67], a[212:215], a[188:191], a[64:67]\n\t"
"v_cvt_pk_bf16_f32 v229, v230, v231\n\t"
"s_waitcnt lgkmcnt(2)\n\t"
"v_mfma_f32_16x16x32_bf16 v[232:235], v[232:235], v[220:223], 0\n\t"
"v_mfma_f32_16x16x32_bf16 a[72:75], a[252:255], a[184:187], a[72:75]\n\t"
"v_mfma_f32_16x16x32_bf16 a[72:75], a[208:211], a[188:191], a[72:75]\n\t"
"v_mfma_f32_16x16x32_bf16 a[72:75], a[212:215], a[192:195], a[72:75]\n\t"
"buffer_load_dwordx4 v[240:243], v252, s[8:11], s20 offen offset:2048 sc1\n\t"
"v_mfma_f32_16x16x32_bf16 a[80:83], a[252:255], a[188:191], a[80:83]\n\t"
"v_mfma_f32_16x16x32_bf16 a[80:83], a[208:211], a[192:195], a[80:83]\n\t"
"s_nop 1\n\t"
"v_cvt_pk_bf16_f32 v230, v232, v233\n\t"
"v_cvt_pk_bf16_f32 v231, v234, v235\n\t"
"v_mfma_f32_16x16x32_bf16 a[80:83], a[212:215], a[196:199], a[80:83]\n\t"
"s_mov_b32 s20, 823296\n\t"
"buffer_load_dwordx4 v[248:251], v252, s[8:11], s20 offen offset:0 sc1\n\t"
"v_mfma_f32_16x16x32_bf16 a[88:91], a[252:255], a[192:195], a[88:91]\n\t"
"ds_read_b64_tr_b16 v[232:233], v246 offset:20480\n\t"
"v_mfma_f32_16x16x32_bf16 a[88:91], a[208:211], a[196:199], a[88:91]\n\t"
"ds_read_b64_tr_b16 v[234:235], v246 offset:23552\n\t"
"v_mfma_f32_16x16x32_bf16 a[88:91], a[212:215], a[200:203], a[88:91]\n\t"
"buffer_load_dwordx4 a[228:231], v252, s[8:11], s20 offen offset:1024 sc1\n\t"
"s_waitcnt lgkmcnt(2)\n\t"
"v_mfma_f32_16x16x32_bf16 v[224:227], v[224:227], v[220:223], 0\n\t"
"v_mfma_f32_16x16x32_bf16 a[96:99], a[252:255], a[196:199], a[96:99]\n\t"
"v_mfma_f32_16x16x32_bf16 a[96:99], a[208:211], a[200:203], a[96:99]\n\t"
"v_mfma_f32_16x16x32_bf16 a[96:99], a[212:215], a[204:207], a[96:99]\n\t"
"buffer_load_dwordx4 a[208:211], v252, s[8:11], s20 offen offset:2048 sc1\n\t"
"s_waitcnt vmcnt(14)\n\t"
"v_mfma_f32_16x16x32_bf16 a[104:107], a[216:219], a[168:171], a[104:107]\n\t"
"ds_read_b128 a[168:171], v244 offset:9216\n\t"
"v_mfma_f32_16x16x32_bf16 a[104:107], a[220:223], a[172:175], a[104:107]\n\t"
"v_cvt_pk_bf16_f32 v224, v224, v225\n\t"
"v_mfma_f32_16x16x32_bf16 a[104:107], v[96:99], a[176:179], a[104:107]\n\t"
"v_cvt_pk_bf16_f32 v225, v226, v227\n\t"
"v_mfma_f32_16x16x32_bf16 a[112:115], a[216:219], a[172:175], a[112:115]\n\t"
"ds_read_b128 a[172:175], v244 offset:9728\n\t"
"v_mfma_f32_16x16x32_bf16 a[112:115], a[220:223], a[176:179], a[112:115]\n\t"
"v_mfma_f32_16x16x32_bf16 a[112:115], v[96:99], a[180:183], a[112:115]\n\t"
"s_mov_b32 s20, 835584\n\t"
"buffer_load_dwordx4 a[212:215], v252, s[8:11], s20 offen offset:0 sc1\n\t"
"s_waitcnt lgkmcnt(2)\n\t"
"v_mfma_f32_16x16x32_bf16 v[232:235], v[232:235], v[220:223], 0\n\t"
"v_mfma_f32_16x16x32_bf16 a[120:123], a[216:219], a[176:179], a[120:123]\n\t"
"ds_read_b128 a[176:179], v244 offset:10240\n\t"
"v_mfma_f32_16x16x32_bf16 a[120:123], a[220:223], a[180:183], a[120:123]\n\t"
"v_mfma_f32_16x16x32_bf16 a[120:123], v[96:99], a[184:187], a[120:123]\n\t"
"buffer_load_dwordx4 a[232:235], v252, s[8:11], s20 offen offset:1024 sc1\n\t"
"v_mfma_f32_16x16x32_bf16 a[128:131], a[216:219], a[180:183], a[128:131]\n\t"
"ds_read_b128 a[180:183], v244 offset:10752\n\t"
"v_mfma_f32_16x16x32_bf16 a[128:131], a[220:223], a[184:187], a[128:131]\n\t"
"v_cvt_pk_bf16_f32 v226, v232, v233\n\t"
"v_cvt_pk_bf16_f32 v227, v234, v235\n\t"
"v_mfma_f32_16x16x32_bf16 a[128:131], v[96:99], a[188:191], a[128:131]\n\t"
"buffer_load_dwordx4 a[236:239], v252, s[8:11], s20 offen offset:2048 sc1\n\t"
"v_mfma_f32_16x16x32_bf16 a[136:139], a[216:219], a[184:187], a[136:139]\n\t"
"ds_read_b64_tr_b16 v[232:233], v246 offset:26624\n\t"
"v_mfma_f32_16x16x32_bf16 a[136:139], a[220:223], a[188:191], a[136:139]\n\t"
"ds_read_b64_tr_b16 v[234:235], v246 offset:29696\n\t"
"v_mfma_f32_16x16x32_bf16 a[136:139], v[96:99], a[192:195], a[136:139]\n\t"
"s_mov_b32 s20, 995328\n\t"
"buffer_load_dwordx4 a[240:243], v252, s[8:11], s20 offen offset:0 sc1\n\t"
"v_mfma_f32_16x16x32_bf16 a[144:147], a[216:219], a[188:191], a[144:147]\n\t"
"ds_read_b128 a[184:187], v244 offset:11264\n\t"
"v_mfma_f32_16x16x32_bf16 a[144:147], a[220:223], a[192:195], a[144:147]\n\t"
"ds_read_b128 a[188:191], v244 offset:11776\n\t"
"v_mfma_f32_16x16x32_bf16 a[144:147], v[96:99], a[196:199], a[144:147]\n\t"
"buffer_load_dwordx4 a[244:247], v252, s[8:11], s20 offen offset:1024 sc1\n\t"
"v_mfma_f32_16x16x32_bf16 a[152:155], a[216:219], a[192:195], a[152:155]\n\t"
"v_mfma_f32_16x16x32_bf16 a[152:155], a[220:223], a[196:199], a[152:155]\n\t"
"ds_read_b128 a[192:195], v244 offset:12288\n\t"
"v_mfma_f32_16x16x32_bf16 a[152:155], v[96:99], a[200:203], a[152:155]\n\t"
"v_mfma_f32_16x16x32_bf16 a[160:163], a[216:219], a[196:199], a[160:163]\n\t"
"ds_read_b128 a[196:199], v244 offset:12800\n\t"
"v_mfma_f32_16x16x32_bf16 a[160:163], a[220:223], a[200:203], a[160:163]\n\t"
"ds_read_b128 a[200:203], v244 offset:13312\n\t"
"v_mfma_f32_16x16x32_bf16 a[160:163], v[96:99], a[204:207], a[160:163]\n\t"
"buffer_load_dwordx4 a[216:219], v252, s[8:11], s20 offen offset:2048 sc1\n\t"
"s_waitcnt lgkmcnt(5)\n\t"
"v_mfma_f32_16x16x32_bf16 v[232:235], v[232:235], v[220:223], 0\n\t"
"ds_read_b128 a[204:207], v244 offset:13824\n\t"
"s_waitcnt vmcnt(9)\n\t"
"v_mfma_f32_16x16x32_bf16 v[200:203], a[224:227], a[168:171], v[200:203]\n\t"
"v_mfma_f32_16x16x32_bf16 v[200:203], v[236:239], a[172:175], v[200:203]\n\t"
"v_mfma_f32_16x16x32_bf16 v[200:203], v[240:243], a[176:179], v[200:203]\n\t"
"s_mov_b32 s20, 1007616\n\t"
"buffer_load_dwordx4 v[96:99], v252, s[8:11], s20 offen offset:0 sc1\n\t"
"v_mfma_f32_16x16x32_bf16 v[208:211], a[224:227], a[172:175], v[208:211]\n\t"
"v_mfma_f32_16x16x32_bf16 v[208:211], v[236:239], a[176:179], v[208:211]\n\t"
"v_cvt_pk_bf16_f32 v232, v232, v233\n\t"
"v_cvt_pk_bf16_f32 v233, v234, v235\n\t"
"v_mfma_f32_16x16x32_bf16 v[208:211], v[240:243], a[180:183], v[208:211]\n\t"
"buffer_load_dwordx4 a[220:223], v252, s[8:11], s20 offen offset:1024 sc1\n\t"
"s_waitcnt lgkmcnt(0)\n\t"
"s_barrier\n\t"
"v_mfma_f32_16x16x32_bf16 v[216:219], a[224:227], a[176:179], v[216:219]\n\t"
"v_mfma_f32_16x16x32_bf16 v[216:219], v[236:239], a[180:183], v[216:219]\n\t"
"v_mfma_f32_16x16x32_bf16 v[216:219], v[240:243], a[184:187], v[216:219]\n\t"
"ds_write2st64_b64 v245, v[228:229], v[230:231] offset0:0 offset1:1\n\t"
"buffer_load_dwordx4 a[248:251], v252, s[8:11], s20 offen offset:2048 sc1\n\t"
"v_mfma_f32_16x16x32_bf16 a[4:7], a[224:227], a[180:183], a[4:7]\n\t"
"ds_read_b64_tr_b16 v[228:229], v246 offset:32768\n\t"
"v_mfma_f32_16x16x32_bf16 a[4:7], v[236:239], a[184:187], a[4:7]\n\t"
"ds_read_b64_tr_b16 v[230:231], v246 offset:35840\n\t"
"v_mfma_f32_16x16x32_bf16 a[4:7], v[240:243], a[188:191], a[4:7]\n\t"
"ds_write2st64_b64 v245, v[224:225], v[226:227] offset0:2 offset1:3\n\t"
"ds_write_b64 v245, v[232:233] offset:2048\n\t"
"v_mfma_f32_16x16x32_bf16 a[12:15], a[224:227], a[184:187], a[12:15]\n\t"
"ds_read_b64_tr_b16 v[224:225], v246 offset:40448\n\t"
"v_mfma_f32_16x16x32_bf16 a[12:15], v[236:239], a[188:191], a[12:15]\n\t"
"ds_read_b64_tr_b16 v[226:227], v246 offset:43520\n\t"
"v_mfma_f32_16x16x32_bf16 a[12:15], v[240:243], a[192:195], a[12:15]\n\t"
"s_mov_b32 s20, 1019904\n\t"
"buffer_load_dwordx4 a[252:255], v252, s[8:11], s20 offen offset:0 sc1\n\t"
"v_mfma_f32_16x16x32_bf16 a[20:23], a[224:227], a[188:191], a[20:23]\n\t"
"ds_read_b64_tr_b16 v[232:233], v246 offset:46592\n\t"
"v_mfma_f32_16x16x32_bf16 a[20:23], v[236:239], a[192:195], a[20:23]\n\t"
"ds_read_b64_tr_b16 v[234:235], v246 offset:49664\n\t"
"v_mfma_f32_16x16x32_bf16 a[20:23], v[240:243], a[196:199], a[20:23]\n\t"
"s_waitcnt lgkmcnt(6)\n\t"
"v_mfma_f32_16x16x32_bf16 v[228:231], v[228:231], v[220:223], 0\n\t"
"v_mfma_f32_16x16x32_bf16 a[28:31], a[224:227], a[192:195], a[28:31]\n\t"
"v_mfma_f32_16x16x32_bf16 a[28:31], v[236:239], a[196:199], a[28:31]\n\t"
"v_mfma_f32_16x16x32_bf16 a[28:31], v[240:243], a[200:203], a[28:31]\n\t"
"s_waitcnt lgkmcnt(2)\n\t"
"v_mfma_f32_16x16x32_bf16 v[224:227], v[224:227], v[220:223], 0\n\t"
"v_mfma_f32_16x16x32_bf16 a[36:39], a[224:227], a[196:199], a[36:39]\n\t"
"s_nop 1\n\t"
"v_cvt_pk_bf16_f32 v228, v228, v229\n\t"
"v_cvt_pk_bf16_f32 v229, v230, v231\n\t"
"v_mfma_f32_16x16x32_bf16 a[36:39], v[236:239], a[200:203], a[36:39]\n\t"
"v_mfma_f32_16x16x32_bf16 a[36:39], v[240:243], a[204:207], a[36:39]\n\t"
"buffer_load_dwordx4 a[224:227], v252, s[8:11], s20 offen offset:1024 sc1\n\t"
"s_waitcnt lgkmcnt(0)\n\t"
"v_mfma_f32_16x16x32_bf16 v[232:235], v[232:235], v[220:223], 0\n\t"
"buffer_load_dwordx4 v[236:239], v252, s[8:11], s20 offen offset:2048 sc1\n\t"
"s_waitcnt vmcnt(12)\n\t"
"v_mfma_f32_16x16x32_bf16 a[44:47], v[248:251], a[168:171], a[44:47]\n\t"
"v_cvt_pk_bf16_f32 v230, v224, v225\n\t"
"v_cvt_pk_bf16_f32 v231, v226, v227\n\t"
"v_mfma_f32_16x16x32_bf16 a[44:47], a[228:231], a[172:175], a[44:47]\n\t"
"ds_read_b64_tr_b16 v[224:225], v246 offset:52736\n\t"
"v_mfma_f32_16x16x32_bf16 a[44:47], a[208:211], a[176:179], a[44:47]\n\t"
"s_mov_b32 s20, 1032192\n\t"
"buffer_load_dwordx4 v[240:243], v252, s[8:11], s20 offen offset:0 sc1\n\t"
"v_mfma_f32_16x16x32_bf16 a[52:55], v[248:251], a[172:175], a[52:55]\n\t"
"ds_read_b64_tr_b16 v[226:227], v246 offset:55808\n\t"
"v_mfma_f32_16x16x32_bf16 a[52:55], a[228:231], a[176:179], a[52:55]\n\t"
"v_cvt_pk_bf16_f32 v232, v232, v233\n\t"
"v_cvt_pk_bf16_f32 v233, v234, v235\n\t"
"v_mfma_f32_16x16x32_bf16 a[52:55], a[208:211], a[180:183], a[52:55]\n\t"
"ds_write2st64_b64 v245, v[228:229], v[230:231] offset0:5 offset1:6\n\t"
"v_mfma_f32_16x16x32_bf16 a[60:63], v[248:251], a[176:179], a[60:63]\n\t"
"v_mfma_f32_16x16x32_bf16 a[60:63], a[228:231], a[180:183], a[60:63]\n\t"
"v_mfma_f32_16x16x32_bf16 a[60:63], a[208:211], a[184:187], a[60:63]\n\t"
"v_mfma_f32_16x16x32_bf16 a[68:71], v[248:251], a[180:183], a[68:71]\n\t"
"v_mfma_f32_16x16x32_bf16 a[68:71], a[228:231], a[184:187], a[68:71]\n\t"
"v_mfma_f32_16x16x32_bf16 a[68:71], a[208:211], a[188:191], a[68:71]\n\t"
"v_mfma_f32_16x16x32_bf16 a[76:79], v[248:251], a[184:187], a[76:79]\n\t"
"v_mfma_f32_16x16x32_bf16 a[76:79], a[228:231], a[188:191], a[76:79]\n\t"
"v_mfma_f32_16x16x32_bf16 a[76:79], a[208:211], a[192:195], a[76:79]\n\t"
"s_waitcnt lgkmcnt(1)\n\t"
"v_mfma_f32_16x16x32_bf16 v[224:227], v[224:227], v[220:223], 0\n\t"
"v_mfma_f32_16x16x32_bf16 a[84:87], v[248:251], a[188:191], a[84:87]\n\t"
"v_mfma_f32_16x16x32_bf16 a[84:87], a[228:231], a[192:195], a[84:87]\n\t"
"v_mfma_f32_16x16x32_bf16 a[84:87], a[208:211], a[196:199], a[84:87]\n\t"
"v_mfma_f32_16x16x32_bf16 a[92:95], v[248:251], a[192:195], a[92:95]\n\t"
"v_mfma_f32_16x16x32_bf16 a[92:95], a[228:231], a[196:199], a[92:95]\n\t"
"s_nop 2\n\t"
"v_cvt_pk_bf16_f32 v234, v224, v225\n\t"
"v_cvt_pk_bf16_f32 v235, v226, v227\n\t"
"v_mfma_f32_16x16x32_bf16 a[92:95], a[208:211], a[200:203], a[92:95]\n\t"
"ds_write2st64_b64 v245, v[232:233], v[234:235] offset0:7 offset1:8\n\t"
"v_mfma_f32_16x16x32_bf16 a[100:103], v[248:251], a[196:199], a[100:103]\n\t"
"v_mfma_f32_16x16x32_bf16 a[100:103], a[228:231], a[200:203], a[100:103]\n\t"
"v_mfma_f32_16x16x32_bf16 a[100:103], a[208:211], a[204:207], a[100:103]\n\t"
"buffer_load_dwordx4 v[248:251], v252, s[8:11], s20 offen offset:1024 sc1\n\t"
"s_waitcnt vmcnt(11)\n\t"
"v_mfma_f32_16x16x32_bf16 a[108:111], a[212:215], a[168:171], a[108:111]\n\t"
"v_mfma_f32_16x16x32_bf16 a[108:111], a[232:235], a[172:175], a[108:111]\n\t"
"v_mfma_f32_16x16x32_bf16 a[108:111], a[236:239], a[176:179], a[108:111]\n\t"
"buffer_load_dwordx4 a[208:211], v252, s[8:11], s20 offen offset:2048 sc1\n\t"
"s_waitcnt lgkmcnt(0)\n\t"
"s_barrier\n\t"
"v_mfma_f32_16x16x32_bf16 a[116:119], a[212:215], a[172:175], a[116:119]\n\t"
"ds_read_b128 a[168:171], v244 offset:0\n\t"
"v_mfma_f32_16x16x32_bf16 a[116:119], a[232:235], a[176:179], a[116:119]\n\t"
"ds_read_b128 a[172:175], v244 offset:512\n\t"
"v_mfma_f32_16x16x32_bf16 a[116:119], a[236:239], a[180:183], a[116:119]\n\t"
"s_mov_b32 s20, 1044480\n\t"
"buffer_load_dwordx4 a[228:231], v252, s[8:11], s20 offen offset:0 sc1\n\t"
"v_mfma_f32_16x16x32_bf16 a[124:127], a[212:215], a[176:179], a[124:127]\n\t"
"ds_read_b128 a[176:179], v244 offset:1024\n\t"
"v_mfma_f32_16x16x32_bf16 a[124:127], a[232:235], a[180:183], a[124:127]\n\t"
"v_mfma_f32_16x16x32_bf16 a[124:127], a[236:239], a[184:187], a[124:127]\n\t"
"v_mfma_f32_16x16x32_bf16 a[132:135], a[212:215], a[180:183], a[132:135]\n\t"
"ds_read_b128 a[180:183], v244 offset:1536\n\t"
"v_mfma_f32_16x16x32_bf16 a[132:135], a[232:235], a[184:187], a[132:135]\n\t"
"v_mfma_f32_16x16x32_bf16 a[132:135], a[236:239], a[188:191], a[132:135]\n\t"
"v_mfma_f32_16x16x32_bf16 a[140:143], a[212:215], a[184:187], a[140:143]\n\t"
"ds_read_b128 a[184:187], v244 offset:2048\n\t"
"v_mfma_f32_16x16x32_bf16 a[140:143], a[232:235], a[188:191], a[140:143]\n\t"
"v_mfma_f32_16x16x32_bf16 a[140:143], a[236:239], a[192:195], a[140:143]\n\t"
"v_mfma_f32_16x16x32_bf16 a[148:151], a[212:215], a[188:191], a[148:151]\n\t"
"ds_read_b128 a[188:191], v244 offset:2560\n\t"
"v_mfma_f32_16x16x32_bf16 a[148:151], a[232:235], a[192:195], a[148:151]\n\t"
"v_mfma_f32_16x16x32_bf16 a[148:151], a[236:239], a[196:199], a[148:151]\n\t"
"v_mfma_f32_16x16x32_bf16 a[156:159], a[212:215], a[192:195], a[156:159]\n\t"
"ds_read_b128 a[192:195], v244 offset:3072\n\t"
"v_mfma_f32_16x16x32_bf16 a[156:159], a[232:235], a[196:199], a[156:159]\n\t"
"v_mfma_f32_16x16x32_bf16 a[156:159], a[236:239], a[200:203], a[156:159]\n\t"
"v_mfma_f32_16x16x32_bf16 a[164:167], a[212:215], a[196:199], a[164:167]\n\t"
"ds_read_b128 a[196:199], v244 offset:3584\n\t"
"v_mfma_f32_16x16x32_bf16 a[164:167], a[232:235], a[200:203], a[164:167]\n\t"
"ds_read_b128 a[200:203], v244 offset:4096\n\t"
"v_mfma_f32_16x16x32_bf16 a[164:167], a[236:239], a[204:207], a[164:167]\n\t"
"buffer_load_dwordx4 a[212:215], v252, s[8:11], s20 offen offset:1024 sc1\n\t"
"Lst28_%=:\n\t"
"s_waitcnt vmcnt(11) lgkmcnt(6)\n\t"
"v_mfma_f32_16x16x32_bf16 v[196:199], a[240:243], a[168:171], v[196:199]\n\t"
"ds_read_b128 a[204:207], v244 offset:4608\n\t"
"v_xor_b32 v246, 0xc0, v246\n\t"
"v_mfma_f32_16x16x32_bf16 v[196:199], a[244:247], a[172:175], v[196:199]\n\t"
"v_mfma_f32_16x16x32_bf16 v[196:199], a[216:219], a[176:179], v[196:199]\n\t"
"s_waitcnt lgkmcnt(6)\n\t"
"v_mfma_f32_16x16x32_bf16 v[204:207], a[240:243], a[172:175], v[204:207]\n\t"
"v_mfma_f32_16x16x32_bf16 v[204:207], a[244:247], a[176:179], v[204:207]\n\t"
"v_mfma_f32_16x16x32_bf16 v[204:207], a[216:219], a[180:183], v[204:207]\n\t"
"buffer_load_dwordx4 a[232:235], v252, s[8:11], s20 offen offset:2048 sc1\n\t"
"s_waitcnt lgkmcnt(5)\n\t"
"v_mfma_f32_16x16x32_bf16 v[212:215], a[240:243], a[176:179], v[212:215]\n\t"
"v_mfma_f32_16x16x32_bf16 v[212:215], a[244:247], a[180:183], v[212:215]\n\t"
"v_mfma_f32_16x16x32_bf16 v[212:215], a[216:219], a[184:187], v[212:215]\n\t"
"s_mov_b32 s20, 1056768\n\t"
"buffer_load_dwordx4 a[236:239], v252, s[8:11], s20 offen offset:0 sc1\n\t"
"s_waitcnt lgkmcnt(4)\n\t"
"v_mfma_f32_16x16x32_bf16 a[0:3], a[240:243], a[180:183], a[0:3]\n\t"
"v_mfma_f32_16x16x32_bf16 a[0:3], a[244:247], a[184:187], a[0:3]\n\t"
"v_mfma_f32_16x16x32_bf16 a[0:3], a[216:219], a[188:191], a[0:3]\n\t"
"s_waitcnt lgkmcnt(3)\n\t"
"v_mfma_f32_16x16x32_bf16 a[8:11], a[240:243], a[184:187], a[8:11]\n\t"
"v_mfma_f32_16x16x32_bf16 a[8:11], a[244:247], a[188:191], a[8:11]\n\t"
"v_mfma_f32_16x16x32_bf16 a[8:11], a[216:219], a[192:195], a[8:11]\n\t"
"ds_read_b64_tr_b16 v[228:229], v246 offset:512\n\t"
"ds_read_b64_tr_b16 v[230:231], v246 offset:3584\n\t"
"s_waitcnt lgkmcnt(4)\n\t"
"v_mfma_f32_16x16x32_bf16 a[16:19], a[240:243], a[188:191], a[16:19]\n\t"
"v_mfma_f32_16x16x32_bf16 a[16:19], a[244:247], a[192:195], a[16:19]\n\t"
"v_mfma_f32_16x16x32_bf16 a[16:19], a[216:219], a[196:199], a[16:19]\n\t"
"s_waitcnt lgkmcnt(3)\n\t"
"v_mfma_f32_16x16x32_bf16 a[24:27], a[240:243], a[192:195], a[24:27]\n\t"
"v_mfma_f32_16x16x32_bf16 a[24:27], a[244:247], a[196:199], a[24:27]\n\t"
"v_mfma_f32_16x16x32_bf16 a[24:27], a[216:219], a[200:203], a[24:27]\n\t"
"s_waitcnt lgkmcnt(2)\n\t"
"v_mfma_f32_16x16x32_bf16 a[32:35], a[240:243], a[196:199], a[32:35]\n\t"
"ds_read_b64_tr_b16 v[224:225], v246 offset:6656\n\t"
"v_mfma_f32_16x16x32_bf16 a[32:35], a[244:247], a[200:203], a[32:35]\n\t"
"ds_read_b64_tr_b16 v[226:227], v246 offset:9728\n\t"
"v_mfma_f32_16x16x32_bf16 a[32:35], a[216:219], a[204:207], a[32:35]\n\t"
"buffer_load_dwordx4 a[240:243], v252, s[8:11], s20 offen offset:1024 sc1\n\t"
"s_waitcnt vmcnt(11)\n\t"
"v_mfma_f32_16x16x32_bf16 a[40:43], v[96:99], a[168:171], a[40:43]\n\t"
"v_mfma_f32_16x16x32_bf16 a[40:43], a[220:223], a[172:175], a[40:43]\n\t"
"v_mfma_f32_16x16x32_bf16 a[40:43], a[248:251], a[176:179], a[40:43]\n\t"
"buffer_load_dwordx4 a[216:219], v252, s[8:11], s20 offen offset:2048 sc1\n\t"
"v_mfma_f32_16x16x32_bf16 a[48:51], v[96:99], a[172:175], a[48:51]\n\t"
"v_mfma_f32_16x16x32_bf16 a[48:51], a[220:223], a[176:179], a[48:51]\n\t"
"v_mfma_f32_16x16x32_bf16 a[48:51], a[248:251], a[180:183], a[48:51]\n\t"
"s_mov_b32 s20, 921600\n\t"
"buffer_load_dwordx4 a[244:247], v252, s[8:11], s20 offen offset:0 sc1\n\t"
"s_waitcnt lgkmcnt(2)\n\t"
"v_mfma_f32_16x16x32_bf16 v[228:231], v[228:231], v[220:223], 0\n\t"
"v_mfma_f32_16x16x32_bf16 a[56:59], v[96:99], a[176:179], a[56:59]\n\t"
"ds_read_b64_tr_b16 v[232:233], v246 offset:12800\n\t"
"v_mfma_f32_16x16x32_bf16 a[56:59], a[220:223], a[180:183], a[56:59]\n\t"
"ds_read_b64_tr_b16 v[234:235], v246 offset:15872\n\t"
"v_mfma_f32_16x16x32_bf16 a[56:59], a[248:251], a[184:187], a[56:59]\n\t"
"v_mfma_f32_16x16x32_bf16 a[64:67], v[96:99], a[180:183], a[64:67]\n\t"
"v_mfma_f32_16x16x32_bf16 a[64:67], a[220:223], a[184:187], a[64:67]\n\t"
"s_nop 0\n\t"
"v_cvt_pk_bf16_f32 v228, v228, v229\n\t"
"v_mfma_f32_16x16x32_bf16 a[64:67], a[248:251], a[188:191], a[64:67]\n\t"
"v_cvt_pk_bf16_f32 v229, v230, v231\n\t"
"s_waitcnt lgkmcnt(2)\n\t"
"v_mfma_f32_16x16x32_bf16 v[224:227], v[224:227], v[220:223], 0\n\t"
"v_mfma_f32_16x16x32_bf16 a[72:75], v[96:99], a[184:187], a[72:75]\n\t"
"v_mfma_f32_16x16x32_bf16 a[72:75], a[220:223], a[188:191], a[72:75]\n\t"
"v_mfma_f32_16x16x32_bf16 a[72:75], a[248:251], a[192:195], a[72:75]\n\t"
"v_mfma_f32_16x16x32_bf16 a[80:83], v[96:99], a[188:191], a[80:83]\n\t"
"v_mfma_f32_16x16x32_bf16 a[80:83], a[220:223], a[192:195], a[80:83]\n\t"
"s_nop 2\n\t"
"v_cvt_pk_bf16_f32 v230, v224, v225\n\t"
"v_cvt_pk_bf16_f32 v231, v226, v227\n\t"
"v_mfma_f32_16x16x32_bf16 a[80:83], a[248:251], a[196:199], a[80:83]\n\t"
"ds_read_b64_tr_b16 v[224:225], v246 offset:20480\n\t"
"v_mfma_f32_16x16x32_bf16 a[88:91], v[96:99], a[192:195], a[88:91]\n\t"
"ds_read_b64_tr_b16 v[226:227], v246 offset:23552\n\t"
"v_mfma_f32_16x16x32_bf16 a[88:91], a[220:223], a[196:199], a[88:91]\n\t"
"v_mfma_f32_16x16x32_bf16 a[88:91], a[248:251], a[200:203], a[88:91]\n\t"
"s_waitcnt lgkmcnt(2)\n\t"
"v_mfma_f32_16x16x32_bf16 v[232:235], v[232:235], v[220:223], 0\n\t"
"v_mfma_f32_16x16x32_bf16 a[96:99], v[96:99], a[196:199], a[96:99]\n\t"
"v_mfma_f32_16x16x32_bf16 a[96:99], a[220:223], a[200:203], a[96:99]\n\t"
"v_mfma_f32_16x16x32_bf16 a[96:99], a[248:251], a[204:207], a[96:99]\n\t"
"buffer_load_dwordx4 v[96:99], v252, s[8:11], s20 offen offset:1024 sc1\n\t"
"s_waitcnt vmcnt(11)\n\t"
"v_mfma_f32_16x16x32_bf16 a[104:107], a[252:255], a[168:171], a[104:107]\n\t"
"ds_read_b128 a[168:171], v244 offset:9216\n\t"
"v_mfma_f32_16x16x32_bf16 a[104:107], a[224:227], a[172:175], a[104:107]\n\t"
"v_cvt_pk_bf16_f32 v232, v232, v233\n\t"
"v_cvt_pk_bf16_f32 v233, v234, v235\n\t"
"v_mfma_f32_16x16x32_bf16 a[104:107], v[236:239], a[176:179], a[104:107]\n\t"
"v_mfma_f32_16x16x32_bf16 a[112:115], a[252:255], a[172:175], a[112:115]\n\t"
"ds_read_b128 a[172:175], v244 offset:9728\n\t"
"v_mfma_f32_16x16x32_bf16 a[112:115], a[224:227], a[176:179], a[112:115]\n\t"
"v_mfma_f32_16x16x32_bf16 a[112:115], v[236:239], a[180:183], a[112:115]\n\t"
"buffer_load_dwordx4 a[220:223], v252, s[8:11], s20 offen offset:2048 sc1\n\t"
"s_waitcnt lgkmcnt(2)\n\t"
"v_mfma_f32_16x16x32_bf16 v[224:227], v[224:227], v[220:223], 0\n\t"
"s_mov_b32 s20, 933888\n\t"
"buffer_load_dwordx4 a[248:251], v252, s[8:11], s20 offen offset:0 sc1\n\t"
"v_mfma_f32_16x16x32_bf16 a[120:123], a[252:255], a[176:179], a[120:123]\n\t"
"ds_read_b128 a[176:179], v244 offset:10240\n\t"
"v_mfma_f32_16x16x32_bf16 a[120:123], a[224:227], a[180:183], a[120:123]\n\t"
"v_mfma_f32_16x16x32_bf16 a[120:123], v[236:239], a[184:187], a[120:123]\n\t"
"v_mfma_f32_16x16x32_bf16 a[128:131], a[252:255], a[180:183], a[128:131]\n\t"
"ds_read_b128 a[180:183], v244 offset:10752\n\t"
"v_mfma_f32_16x16x32_bf16 a[128:131], a[224:227], a[184:187], a[128:131]\n\t"
"v_cvt_pk_bf16_f32 v234, v224, v225\n\t"
"v_cvt_pk_bf16_f32 v235, v226, v227\n\t"
"v_mfma_f32_16x16x32_bf16 a[128:131], v[236:239], a[188:191], a[128:131]\n\t"
"ds_read_b64_tr_b16 v[224:225], v246 offset:26624\n\t"
"v_mfma_f32_16x16x32_bf16 a[136:139], a[252:255], a[184:187], a[136:139]\n\t"
"ds_read_b64_tr_b16 v[226:227], v246 offset:29696\n\t"
"v_mfma_f32_16x16x32_bf16 a[136:139], a[224:227], a[188:191], a[136:139]\n\t"
"ds_read_b128 a[184:187], v244 offset:11264\n\t"
"v_mfma_f32_16x16x32_bf16 a[136:139], v[236:239], a[192:195], a[136:139]\n\t"
"v_mfma_f32_16x16x32_bf16 a[144:147], a[252:255], a[188:191], a[144:147]\n\t"
"ds_read_b128 a[188:191], v244 offset:11776\n\t"
"v_mfma_f32_16x16x32_bf16 a[144:147], a[224:227], a[192:195], a[144:147]\n\t"
"v_mfma_f32_16x16x32_bf16 a[144:147], v[236:239], a[196:199], a[144:147]\n\t"
"v_mfma_f32_16x16x32_bf16 a[152:155], a[252:255], a[192:195], a[152:155]\n\t"
"ds_read_b128 a[192:195], v244 offset:12288\n\t"
"v_mfma_f32_16x16x32_bf16 a[152:155], a[224:227], a[196:199], a[152:155]\n\t"
"v_mfma_f32_16x16x32_bf16 a[152:155], v[236:239], a[200:203], a[152:155]\n\t"
"v_mfma_f32_16x16x32_bf16 a[160:163], a[252:255], a[196:199], a[160:163]\n\t"
"ds_read_b128 a[196:199], v244 offset:12800\n\t"
"v_mfma_f32_16x16x32_bf16 a[160:163], a[224:227], a[200:203], a[160:163]\n\t"
"ds_read_b128 a[200:203], v244 offset:13312\n\t"
"v_mfma_f32_16x16x32_bf16 a[160:163], v[236:239], a[204:207], a[160:163]\n\t"
"buffer_load_dwordx4 a[252:255], v252, s[8:11], s20 offen offset:1024 sc1\n\t"
"s_waitcnt lgkmcnt(5)\n\t"
"v_mfma_f32_16x16x32_bf16 v[224:227], v[224:227], v[220:223], 0\n\t"
"ds_read_b128 a[204:207], v244 offset:13824\n\t"
"s_waitcnt vmcnt(11)\n\t"
"v_mfma_f32_16x16x32_bf16 v[200:203], v[240:243], a[168:171], v[200:203]\n\t"
"v_mfma_f32_16x16x32_bf16 v[200:203], v[248:251], a[172:175], v[200:203]\n\t"
"v_mfma_f32_16x16x32_bf16 v[200:203], a[208:211], a[176:179], v[200:203]\n\t"
"buffer_load_dwordx4 v[236:239], v252, s[8:11], s20 offen offset:2048 sc1\n\t"
"v_mfma_f32_16x16x32_bf16 v[208:211], v[240:243], a[172:175], v[208:211]\n\t"
"v_mfma_f32_16x16x32_bf16 v[208:211], v[248:251], a[176:179], v[208:211]\n\t"
"v_cvt_pk_bf16_f32 v224, v224, v225\n\t"
"v_cvt_pk_bf16_f32 v225, v226, v227\n\t"
"v_mfma_f32_16x16x32_bf16 v[208:211], a[208:211], a[180:183], v[208:211]\n\t"
"s_mov_b32 s20, 946176\n\t"
"buffer_load_dwordx4 a[224:227], v252, s[8:11], s20 offen offset:0 sc1\n\t"
"s_waitcnt lgkmcnt(0)\n\t"
"s_barrier\n\t"
"v_mfma_f32_16x16x32_bf16 v[216:219], v[240:243], a[176:179], v[216:219]\n\t"
"v_mfma_f32_16x16x32_bf16 v[216:219], v[248:251], a[180:183], v[216:219]\n\t"
"v_mfma_f32_16x16x32_bf16 v[216:219], a[208:211], a[184:187], v[216:219]\n\t"
"ds_write2st64_b64 v245, v[228:229], v[230:231] offset0:0 offset1:1\n\t"
"v_mfma_f32_16x16x32_bf16 a[4:7], v[240:243], a[180:183], a[4:7]\n\t"
"ds_read_b64_tr_b16 v[228:229], v246 offset:32768\n\t"
"v_mfma_f32_16x16x32_bf16 a[4:7], v[248:251], a[184:187], a[4:7]\n\t"
"ds_read_b64_tr_b16 v[230:231], v246 offset:35840\n\t"
"v_mfma_f32_16x16x32_bf16 a[4:7], a[208:211], a[188:191], a[4:7]\n\t"
"ds_write2st64_b64 v245, v[232:233], v[234:235] offset0:2 offset1:3\n\t"
"v_mfma_f32_16x16x32_bf16 a[12:15], v[240:243], a[184:187], a[12:15]\n\t"
"ds_read_b64_tr_b16 v[232:233], v246 offset:40448\n\t"
"v_mfma_f32_16x16x32_bf16 a[12:15], v[248:251], a[188:191], a[12:15]\n\t"
"ds_read_b64_tr_b16 v[234:235], v246 offset:43520\n\t"
"v_mfma_f32_16x16x32_bf16 a[12:15], a[208:211], a[192:195], a[12:15]\n\t"
"ds_write_b64 v245, v[224:225] offset:2048\n\t"
"v_mfma_f32_16x16x32_bf16 a[20:23], v[240:243], a[188:191], a[20:23]\n\t"
"ds_read_b64_tr_b16 v[224:225], v246 offset:46592\n\t"
"v_mfma_f32_16x16x32_bf16 a[20:23], v[248:251], a[192:195], a[20:23]\n\t"
"ds_read_b64_tr_b16 v[226:227], v246 offset:49664\n\t"
"v_mfma_f32_16x16x32_bf16 a[20:23], a[208:211], a[196:199], a[20:23]\n\t"
"s_waitcnt lgkmcnt(6)\n\t"
"v_mfma_f32_16x16x32_bf16 v[228:231], v[228:231], v[220:223], 0\n\t"
"v_mfma_f32_16x16x32_bf16 a[28:31], v[240:243], a[192:195], a[28:31]\n\t"
"v_mfma_f32_16x16x32_bf16 a[28:31], v[248:251], a[196:199], a[28:31]\n\t"
"v_mfma_f32_16x16x32_bf16 a[28:31], a[208:211], a[200:203], a[28:31]\n\t"
"s_waitcnt lgkmcnt(3)\n\t"
"v_mfma_f32_16x16x32_bf16 v[232:235], v[232:235], v[220:223], 0\n\t"
"v_mfma_f32_16x16x32_bf16 a[36:39], v[240:243], a[196:199], a[36:39]\n\t"
"s_nop 1\n\t"
"v_cvt_pk_bf16_f32 v228, v228, v229\n\t"
"v_cvt_pk_bf16_f32 v229, v230, v231\n\t"
"v_mfma_f32_16x16x32_bf16 a[36:39], v[248:251], a[200:203], a[36:39]\n\t"
"v_mfma_f32_16x16x32_bf16 a[36:39], a[208:211], a[204:207], a[36:39]\n\t"
"buffer_load_dwordx4 v[240:243], v252, s[8:11], s20 offen offset:1024 sc1\n\t"
"s_waitcnt lgkmcnt(0)\n\t"
"v_mfma_f32_16x16x32_bf16 v[224:227], v[224:227], v[220:223], 0\n\t"
"buffer_load_dwordx4 v[248:251], v252, s[8:11], s20 offen offset:2048 sc1\n\t"
"s_waitcnt vmcnt(12)\n\t"
"v_mfma_f32_16x16x32_bf16 a[44:47], a[228:231], a[168:171], a[44:47]\n\t"
"v_cvt_pk_bf16_f32 v230, v232, v233\n\t"
"v_cvt_pk_bf16_f32 v231, v234, v235\n\t"
"v_mfma_f32_16x16x32_bf16 a[44:47], a[212:215], a[172:175], a[44:47]\n\t"
"ds_read_b64_tr_b16 v[232:233], v246 offset:52736\n\t"
"v_mfma_f32_16x16x32_bf16 a[44:47], a[232:235], a[176:179], a[44:47]\n\t"
"s_mov_b32 s20, 958464\n\t"
"buffer_load_dwordx4 a[208:211], v252, s[8:11], s20 offen offset:0 sc1\n\t"
"v_mfma_f32_16x16x32_bf16 a[52:55], a[228:231], a[172:175], a[52:55]\n\t"
"ds_read_b64_tr_b16 v[234:235], v246 offset:55808\n\t"
"v_mfma_f32_16x16x32_bf16 a[52:55], a[212:215], a[176:179], a[52:55]\n\t"
"v_cvt_pk_bf16_f32 v224, v224, v225\n\t"
"v_cvt_pk_bf16_f32 v225, v226, v227\n\t"
"v_mfma_f32_16x16x32_bf16 a[52:55], a[232:235], a[180:183], a[52:55]\n\t"
"ds_write2st64_b64 v245, v[228:229], v[230:231] offset0:5 offset1:6\n\t"
"v_mfma_f32_16x16x32_bf16 a[60:63], a[228:231], a[176:179], a[60:63]\n\t"
"v_mfma_f32_16x16x32_bf16 a[60:63], a[212:215], a[180:183], a[60:63]\n\t"
"v_mfma_f32_16x16x32_bf16 a[60:63], a[232:235], a[184:187], a[60:63]\n\t"
"v_mfma_f32_16x16x32_bf16 a[68:71], a[228:231], a[180:183], a[68:71]\n\t"
"v_mfma_f32_16x16x32_bf16 a[68:71], a[212:215], a[184:187], a[68:71]\n\t"
"v_mfma_f32_16x16x32_bf16 a[68:71], a[232:235], a[188:191], a[68:71]\n\t"
"v_mfma_f32_16x16x32_bf16 a[76:79], a[228:231], a[184:187], a[76:79]\n\t"
"v_mfma_f32_16x16x32_bf16 a[76:79], a[212:215], a[188:191], a[76:79]\n\t"
"v_mfma_f32_16x16x32_bf16 a[76:79], a[232:235], a[192:195], a[76:79]\n\t"
"s_waitcnt lgkmcnt(1)\n\t"
"v_mfma_f32_16x16x32_bf16 v[232:235], v[232:235], v[220:223], 0\n\t"
"v_mfma_f32_16x16x32_bf16 a[84:87], a[228:231], a[188:191], a[84:87]\n\t"
"v_mfma_f32_16x16x32_bf16 a[84:87], a[212:215], a[192:195], a[84:87]\n\t"
"v_mfma_f32_16x16x32_bf16 a[84:87], a[232:235], a[196:199], a[84:87]\n\t"
"v_mfma_f32_16x16x32_bf16 a[92:95], a[228:231], a[192:195], a[92:95]\n\t"
"v_mfma_f32_16x16x32_bf16 a[92:95], a[212:215], a[196:199], a[92:95]\n\t"
"s_nop 2\n\t"
"v_cvt_pk_bf16_f32 v226, v232, v233\n\t"
"v_cvt_pk_bf16_f32 v227, v234, v235\n\t"
"v_mfma_f32_16x16x32_bf16 a[92:95], a[232:235], a[200:203], a[92:95]\n\t"
"ds_write2st64_b64 v245, v[224:225], v[226:227] offset0:7 offset1:8\n\t"
"v_mfma_f32_16x16x32_bf16 a[100:103], a[228:231], a[196:199], a[100:103]\n\t"
"v_mfma_f32_16x16x32_bf16 a[100:103], a[212:215], a[200:203], a[100:103]\n\t"
"v_mfma_f32_16x16x32_bf16 a[100:103], a[232:235], a[204:207], a[100:103]\n\t"
"buffer_load_dwordx4 a[228:231], v252, s[8:11], s20 offen offset:1024 sc1\n\t"
"s_waitcnt vmcnt(11)\n\t"
"v_mfma_f32_16x16x32_bf16 a[108:111], a[236:239], a[168:171], a[108:111]\n\t"
"v_mfma_f32_16x16x32_bf16 a[108:111], a[240:243], a[172:175], a[108:111]\n\t"
"v_mfma_f32_16x16x32_bf16 a[108:111], a[216:219], a[176:179], a[108:111]\n\t"
"buffer_load_dwordx4 a[212:215], v252, s[8:11], s20 offen offset:2048 sc1\n\t"
"s_waitcnt lgkmcnt(0)\n\t"
"s_barrier\n\t"
"v_mfma_f32_16x16x32_bf16 a[116:119], a[236:239], a[172:175], a[116:119]\n\t"
"ds_read_b128 a[168:171], v244 offset:0\n\t"
"v_mfma_f32_16x16x32_bf16 a[116:119], a[240:243], a[176:179], a[116:119]\n\t"
"ds_read_b128 a[172:175], v244 offset:512\n\t"
"v_mfma_f32_16x16x32_bf16 a[116:119], a[216:219], a[180:183], a[116:119]\n\t"
"s_mov_b32 s20, 970752\n\t"
"buffer_load_dwordx4 a[232:235], v252, s[8:11], s20 offen offset:0 sc1\n\t"
"v_mfma_f32_16x16x32_bf16 a[124:127], a[236:239], a[176:179], a[124:127]\n\t"
"ds_read_b128 a[176:179], v244 offset:1024\n\t"
"v_mfma_f32_16x16x32_bf16 a[124:127], a[240:243], a[180:183], a[124:127]\n\t"
"v_mfma_f32_16x16x32_bf16 a[124:127], a[216:219], a[184:187], a[124:127]\n\t"
"v_mfma_f32_16x16x32_bf16 a[132:135], a[236:239], a[180:183], a[132:135]\n\t"
"ds_read_b128 a[180:183], v244 offset:1536\n\t"
"v_mfma_f32_16x16x32_bf16 a[132:135], a[240:243], a[184:187], a[132:135]\n\t"
"v_mfma_f32_16x16x32_bf16 a[132:135], a[216:219], a[188:191], a[132:135]\n\t"
"v_mfma_f32_16x16x32_bf16 a[140:143], a[236:239], a[184:187], a[140:143]\n\t"
"ds_read_b128 a[184:187], v244 offset:2048\n\t"
"v_mfma_f32_16x16x32_bf16 a[140:143], a[240:243], a[188:191], a[140:143]\n\t"
"v_mfma_f32_16x16x32_bf16 a[140:143], a[216:219], a[192:195], a[140:143]\n\t"
"v_mfma_f32_16x16x32_bf16 a[148:151], a[236:239], a[188:191], a[148:151]\n\t"
"ds_read_b128 a[188:191], v244 offset:2560\n\t"
"v_mfma_f32_16x16x32_bf16 a[148:151], a[240:243], a[192:195], a[148:151]\n\t"
"v_mfma_f32_16x16x32_bf16 a[148:151], a[216:219], a[196:199], a[148:151]\n\t"
"v_mfma_f32_16x16x32_bf16 a[156:159], a[236:239], a[192:195], a[156:159]\n\t"
"ds_read_b128 a[192:195], v244 offset:3072\n\t"
"v_mfma_f32_16x16x32_bf16 a[156:159], a[240:243], a[196:199], a[156:159]\n\t"
"v_mfma_f32_16x16x32_bf16 a[156:159], a[216:219], a[200:203], a[156:159]\n\t"
"v_mfma_f32_16x16x32_bf16 a[164:167], a[236:239], a[196:199], a[164:167]\n\t"
"ds_read_b128 a[196:199], v244 offset:3584\n\t"
"v_mfma_f32_16x16x32_bf16 a[164:167], a[240:243], a[200:203], a[164:167]\n\t"
"ds_read_b128 a[200:203], v244 offset:4096\n\t"
"v_mfma_f32_16x16x32_bf16 a[164:167], a[216:219], a[204:207], a[164:167]\n\t"
"buffer_load_dwordx4 a[236:239], v252, s[8:11], s20 offen offset:1024 sc1\n\t"
"Lst29_%=:\n\t"
"s_waitcnt vmcnt(11) lgkmcnt(6)\n\t"
"v_mfma_f32_16x16x32_bf16 v[0:3], a[244:247], a[168:171], v[0:3]\n\t"
"ds_read_b128 a[204:207], v244 offset:4608\n\t"
"v_mfma_f32_16x16x32_bf16 v[0:3], v[96:99], a[172:175], v[0:3]\n\t"
"v_mfma_f32_16x16x32_bf16 v[0:3], a[220:223], a[176:179], v[0:3]\n\t"
"buffer_load_dwordx4 a[216:219], v252, s[8:11], s20 offen offset:2048 sc1\n\t"
"s_waitcnt lgkmcnt(6)\n\t"
"v_mfma_f32_16x16x32_bf16 v[4:7], a[244:247], a[172:175], v[4:7]\n\t"
"v_mfma_f32_16x16x32_bf16 v[4:7], v[96:99], a[176:179], v[4:7]\n\t"
"v_mfma_f32_16x16x32_bf16 v[4:7], a[220:223], a[180:183], v[4:7]\n\t"
"s_mov_b32 s20, 983040\n\t"
"buffer_load_dwordx4 a[240:243], v252, s[8:11], s20 offen offset:0 sc1\n\t"
"s_waitcnt lgkmcnt(5)\n\t"
"v_mfma_f32_16x16x32_bf16 v[8:11], a[244:247], a[176:179], v[8:11]\n\t"
"v_mfma_f32_16x16x32_bf16 v[8:11], v[96:99], a[180:183], v[8:11]\n\t"
"v_mfma_f32_16x16x32_bf16 v[8:11], a[220:223], a[184:187], v[8:11]\n\t"
"s_waitcnt lgkmcnt(4)\n\t"
"v_mfma_f32_16x16x32_bf16 v[12:15], a[244:247], a[180:183], v[12:15]\n\t"
"v_mfma_f32_16x16x32_bf16 v[12:15], v[96:99], a[184:187], v[12:15]\n\t"
"v_mfma_f32_16x16x32_bf16 v[12:15], a[220:223], a[188:191], v[12:15]\n\t"
"s_waitcnt lgkmcnt(3)\n\t"
"v_mfma_f32_16x16x32_bf16 v[16:19], a[244:247], a[184:187], v[16:19]\n\t"
"v_mfma_f32_16x16x32_bf16 v[16:19], v[96:99], a[188:191], v[16:19]\n\t"
"v_mfma_f32_16x16x32_bf16 v[16:19], a[220:223], a[192:195], v[16:19]\n\t"
"ds_read_b64_tr_b16 v[228:229], v246 offset:1024\n\t"
"ds_read_b64_tr_b16 v[230:231], v246 offset:4096\n\t"
"s_waitcnt lgkmcnt(4)\n\t"
"v_mfma_f32_16x16x32_bf16 v[20:23], a[244:247], a[188:191], v[20:23]\n\t"
"v_mfma_f32_16x16x32_bf16 v[20:23], v[96:99], a[192:195], v[20:23]\n\t"
"v_mfma_f32_16x16x32_bf16 v[20:23], a[220:223], a[196:199], v[20:23]\n\t"
"s_waitcnt lgkmcnt(3)\n\t"
"v_mfma_f32_16x16x32_bf16 v[24:27], a[244:247], a[192:195], v[24:27]\n\t"
"v_mfma_f32_16x16x32_bf16 v[24:27], v[96:99], a[196:199], v[24:27]\n\t"
"v_mfma_f32_16x16x32_bf16 v[24:27], a[220:223], a[200:203], v[24:27]\n\t"
"s_waitcnt lgkmcnt(2)\n\t"
"v_mfma_f32_16x16x32_bf16 v[28:31], a[244:247], a[196:199], v[28:31]\n\t"
"ds_read_b64_tr_b16 v[232:233], v246 offset:7168\n\t"
"v_mfma_f32_16x16x32_bf16 v[28:31], v[96:99], a[200:203], v[28:31]\n\t"
"ds_read_b64_tr_b16 v[234:235], v246 offset:10240\n\t"
"v_mfma_f32_16x16x32_bf16 v[28:31], a[220:223], a[204:207], v[28:31]\n\t"
"buffer_load_dwordx4 a[244:247], v252, s[8:11], s20 offen offset:1024 sc1\n\t"
"s_waitcnt vmcnt(11)\n\t"
"v_mfma_f32_16x16x32_bf16 v[32:35], a[248:251], a[168:171], v[32:35]\n\t"
"v_mfma_f32_16x16x32_bf16 v[32:35], a[252:255], a[172:175], v[32:35]\n\t"
"v_mfma_f32_16x16x32_bf16 v[32:35], v[236:239], a[176:179], v[32:35]\n\t"
"buffer_load_dwordx4 v[96:99], v252, s[8:11], s20 offen offset:2048 sc1\n\t"
"v_mfma_f32_16x16x32_bf16 v[36:39], a[248:251], a[172:175], v[36:39]\n\t"
"v_mfma_f32_16x16x32_bf16 v[36:39], a[252:255], a[176:179], v[36:39]\n\t"
"v_mfma_f32_16x16x32_bf16 v[36:39], v[236:239], a[180:183], v[36:39]\n\t"
"s_mov_b32 s20, 1142784\n\t"
"buffer_load_dwordx4 a[220:223], v252, s[8:11], s20 offen offset:0 sc1\n\t"
"s_waitcnt lgkmcnt(2)\n\t"
"v_mfma_f32_16x16x32_bf16 v[228:231], v[228:231], v[220:223], 0\n\t"
"v_mfma_f32_16x16x32_bf16 v[40:43], a[248:251], a[176:179], v[40:43]\n\t"
"ds_read_b64_tr_b16 v[224:225], v246 offset:13312\n\t"
"v_mfma_f32_16x16x32_bf16 v[40:43], a[252:255], a[180:183], v[40:43]\n\t"
"ds_read_b64_tr_b16 v[226:227], v246 offset:16384\n\t"
"v_mfma_f32_16x16x32_bf16 v[40:43], v[236:239], a[184:187], v[40:43]\n\t"
"v_mfma_f32_16x16x32_bf16 v[44:47], a[248:251], a[180:183], v[44:47]\n\t"
"v_mfma_f32_16x16x32_bf16 v[44:47], a[252:255], a[184:187], v[44:47]\n\t"
"s_nop 0\n\t"
"v_cvt_pk_bf16_f32 v228, v228, v229\n\t"
"v_mfma_f32_16x16x32_bf16 v[44:47], v[236:239], a[188:191], v[44:47]\n\t"
"v_cvt_pk_bf16_f32 v229, v230, v231\n\t"
"s_waitcnt lgkmcnt(2)\n\t"
"v_mfma_f32_16x16x32_bf16 v[232:235], v[232:235], v[220:223], 0\n\t"
"v_mfma_f32_16x16x32_bf16 v[48:51], a[248:251], a[184:187], v[48:51]\n\t"
"v_mfma_f32_16x16x32_bf16 v[48:51], a[252:255], a[188:191], v[48:51]\n\t"
"v_mfma_f32_16x16x32_bf16 v[48:51], v[236:239], a[192:195], v[48:51]\n\t"
"v_mfma_f32_16x16x32_bf16 v[52:55], a[248:251], a[188:191], v[52:55]\n\t"
"v_mfma_f32_16x16x32_bf16 v[52:55], a[252:255], a[192:195], v[52:55]\n\t"
"s_nop 2\n\t"
"v_cvt_pk_bf16_f32 v230, v232, v233\n\t"
"v_cvt_pk_bf16_f32 v231, v234, v235\n\t"
"v_mfma_f32_16x16x32_bf16 v[52:55], v[236:239], a[196:199], v[52:55]\n\t"
"ds_read_b64_tr_b16 v[232:233], v246 offset:20992\n\t"
"v_mfma_f32_16x16x32_bf16 v[56:59], a[248:251], a[192:195], v[56:59]\n\t"
"ds_read_b64_tr_b16 v[234:235], v246 offset:24064\n\t"
"v_mfma_f32_16x16x32_bf16 v[56:59], a[252:255], a[196:199], v[56:59]\n\t"
"v_mfma_f32_16x16x32_bf16 v[56:59], v[236:239], a[200:203], v[56:59]\n\t"
"s_waitcnt lgkmcnt(2)\n\t"
"v_mfma_f32_16x16x32_bf16 v[224:227], v[224:227], v[220:223], 0\n\t"
"v_mfma_f32_16x16x32_bf16 v[60:63], a[248:251], a[196:199], v[60:63]\n\t"
"v_mfma_f32_16x16x32_bf16 v[60:63], a[252:255], a[200:203], v[60:63]\n\t"
"v_mfma_f32_16x16x32_bf16 v[60:63], v[236:239], a[204:207], v[60:63]\n\t"
"buffer_load_dwordx4 a[248:251], v252, s[8:11], s20 offen offset:1024 sc1\n\t"
"s_waitcnt vmcnt(11)\n\t"
"v_mfma_f32_16x16x32_bf16 v[64:67], a[224:227], a[168:171], v[64:67]\n\t"
"ds_read_b128 a[168:171], v244 offset:9216\n\t"
"v_mfma_f32_16x16x32_bf16 v[64:67], v[240:243], a[172:175], v[64:67]\n\t"
"v_cvt_pk_bf16_f32 v224, v224, v225\n\t"
"v_cvt_pk_bf16_f32 v225, v226, v227\n\t"
"v_mfma_f32_16x16x32_bf16 v[64:67], v[248:251], a[176:179], v[64:67]\n\t"
"v_mfma_f32_16x16x32_bf16 v[68:71], a[224:227], a[172:175], v[68:71]\n\t"
"ds_read_b128 a[172:175], v244 offset:9728\n\t"
"v_mfma_f32_16x16x32_bf16 v[68:71], v[240:243], a[176:179], v[68:71]\n\t"
"v_mfma_f32_16x16x32_bf16 v[68:71], v[248:251], a[180:183], v[68:71]\n\t"
"buffer_load_dwordx4 v[236:239], v252, s[8:11], s20 offen offset:2048 sc1\n\t"
"s_waitcnt lgkmcnt(2)\n\t"
"v_mfma_f32_16x16x32_bf16 v[232:235], v[232:235], v[220:223], 0\n\t"
"s_mov_b32 s20, 1155072\n\t"
"buffer_load_dwordx4 a[252:255], v252, s[8:11], s20 offen offset:0 sc1\n\t"
"v_mfma_f32_16x16x32_bf16 v[72:75], a[224:227], a[176:179], v[72:75]\n\t"
"ds_read_b128 a[176:179], v244 offset:10240\n\t"
"v_mfma_f32_16x16x32_bf16 v[72:75], v[240:243], a[180:183], v[72:75]\n\t"
"v_mfma_f32_16x16x32_bf16 v[72:75], v[248:251], a[184:187], v[72:75]\n\t"
"v_mfma_f32_16x16x32_bf16 v[76:79], a[224:227], a[180:183], v[76:79]\n\t"
"ds_read_b128 a[180:183], v244 offset:10752\n\t"
"v_mfma_f32_16x16x32_bf16 v[76:79], v[240:243], a[184:187], v[76:79]\n\t"
"v_cvt_pk_bf16_f32 v226, v232, v233\n\t"
"v_cvt_pk_bf16_f32 v227, v234, v235\n\t"
"v_mfma_f32_16x16x32_bf16 v[76:79], v[248:251], a[188:191], v[76:79]\n\t"
"ds_read_b64_tr_b16 v[232:233], v246 offset:27136\n\t"
"v_mfma_f32_16x16x32_bf16 v[80:83], a[224:227], a[184:187], v[80:83]\n\t"
"ds_read_b64_tr_b16 v[234:235], v246 offset:30208\n\t"
"v_mfma_f32_16x16x32_bf16 v[80:83], v[240:243], a[188:191], v[80:83]\n\t"
"ds_read_b128 a[184:187], v244 offset:11264\n\t"
"v_mfma_f32_16x16x32_bf16 v[80:83], v[248:251], a[192:195], v[80:83]\n\t"
"v_mfma_f32_16x16x32_bf16 v[84:87], a[224:227], a[188:191], v[84:87]\n\t"
"ds_read_b128 a[188:191], v244 offset:11776\n\t"
"v_mfma_f32_16x16x32_bf16 v[84:87], v[240:243], a[192:195], v[84:87]\n\t"
"v_mfma_f32_16x16x32_bf16 v[84:87], v[248:251], a[196:199], v[84:87]\n\t"
"v_mfma_f32_16x16x32_bf16 v[88:91], a[224:227], a[192:195], v[88:91]\n\t"
"ds_read_b128 a[192:195], v244 offset:12288\n\t"
"v_mfma_f32_16x16x32_bf16 v[88:91], v[240:243], a[196:199], v[88:91]\n\t"
"v_mfma_f32_16x16x32_bf16 v[88:91], v[248:251], a[200:203], v[88:91]\n\t"
"v_mfma_f32_16x16x32_bf16 v[92:95], a[224:227], a[196:199], v[92:95]\n\t"
"ds_read_b128 a[196:199], v244 offset:12800\n\t"
"v_mfma_f32_16x16x32_bf16 v[92:95], v[240:243], a[200:203], v[92:95]\n\t"
"ds_read_b128 a[200:203], v244 offset:13312\n\t"
"v_mfma_f32_16x16x32_bf16 v[92:95], v[248:251], a[204:207], v[92:95]\n\t"
"buffer_load_dwordx4 a[224:227], v252, s[8:11], s20 offen offset:1024 sc1\n\t"
"s_waitcnt lgkmcnt(5)\n\t"
"v_mfma_f32_16x16x32_bf16 v[232:235], v[232:235], v[220:223], 0\n\t"
"ds_read_b128 a[204:207], v244 offset:13824\n\t"
"s_waitcnt vmcnt(11)\n\t"
"v_mfma_f32_16x16x32_bf16 v[100:103], a[208:211], a[168:171], v[100:103]\n\t"
"v_mfma_f32_16x16x32_bf16 v[100:103], a[228:231], a[172:175], v[100:103]\n\t"
"v_mfma_f32_16x16x32_bf16 v[100:103], a[212:215], a[176:179], v[100:103]\n\t"
"buffer_load_dwordx4 v[240:243], v252, s[8:11], s20 offen offset:2048 sc1\n\t"
"v_mfma_f32_16x16x32_bf16 v[104:107], a[208:211], a[172:175], v[104:107]\n\t"
"v_mfma_f32_16x16x32_bf16 v[104:107], a[228:231], a[176:179], v[104:107]\n\t"
"v_cvt_pk_bf16_f32 v232, v232, v233\n\t"
"v_cvt_pk_bf16_f32 v233, v234, v235\n\t"
"v_mfma_f32_16x16x32_bf16 v[104:107], a[212:215], a[180:183], v[104:107]\n\t"
"s_mov_b32 s20, 1167360\n\t"
"buffer_load_dwordx4 v[248:251], v252, s[8:11], s20 offen offset:0 sc1\n\t"
"s_waitcnt lgkmcnt(0)\n\t"
"s_barrier\n\t"
"v_mfma_f32_16x16x32_bf16 v[108:111], a[208:211], a[176:179], v[108:111]\n\t"
"v_mfma_f32_16x16x32_bf16 v[108:111], a[228:231], a[180:183], v[108:111]\n\t"
"v_mfma_f32_16x16x32_bf16 v[108:111], a[212:215], a[184:187], v[108:111]\n\t"
"ds_write2st64_b64 v245, v[228:229], v[230:231] offset0:0 offset1:1\n\t"
"v_mfma_f32_16x16x32_bf16 v[112:115], a[208:211], a[180:183], v[112:115]\n\t"
"ds_read_b64_tr_b16 v[228:229], v246 offset:33280\n\t"
"v_mfma_f32_16x16x32_bf16 v[112:115], a[228:231], a[184:187], v[112:115]\n\t"
"ds_read_b64_tr_b16 v[230:231], v246 offset:36352\n\t"
"v_mfma_f32_16x16x32_bf16 v[112:115], a[212:215], a[188:191], v[112:115]\n\t"
"ds_write2st64_b64 v245, v[224:225], v[226:227] offset0:2 offset1:3\n\t"
"v_mfma_f32_16x16x32_bf16 v[116:119], a[208:211], a[184:187], v[116:119]\n\t"
"ds_read_b64_tr_b16 v[224:225], v246 offset:40960\n\t"
"v_mfma_f32_16x16x32_bf16 v[116:119], a[228:231], a[188:191], v[116:119]\n\t"
"ds_read_b64_tr_b16 v[226:227], v246 offset:44032\n\t"
"v_mfma_f32_16x16x32_bf16 v[116:119], a[212:215], a[192:195], v[116:119]\n\t"
"ds_write_b64 v245, v[232:233] offset:2048\n\t"
"v_mfma_f32_16x16x32_bf16 v[120:123], a[208:211], a[188:191], v[120:123]\n\t"
"ds_read_b64_tr_b16 v[232:233], v246 offset:47104\n\t"
"v_mfma_f32_16x16x32_bf16 v[120:123], a[228:231], a[192:195], v[120:123]\n\t"
"ds_read_b64_tr_b16 v[234:235], v246 offset:50176\n\t"
"v_mfma_f32_16x16x32_bf16 v[120:123], a[212:215], a[196:199], v[120:123]\n\t"
"s_waitcnt lgkmcnt(6)\n\t"
"v_mfma_f32_16x16x32_bf16 v[228:231], v[228:231], v[220:223], 0\n\t"
"v_mfma_f32_16x16x32_bf16 v[124:127], a[208:211], a[192:195], v[124:127]\n\t"
"v_mfma_f32_16x16x32_bf16 v[124:127], a[228:231], a[196:199], v[124:127]\n\t"
"v_mfma_f32_16x16x32_bf16 v[124:127], a[212:215], a[200:203], v[124:127]\n\t"
"s_waitcnt lgkmcnt(3)\n\t"
"v_mfma_f32_16x16x32_bf16 v[224:227], v[224:227], v[220:223], 0\n\t"
"v_mfma_f32_16x16x32_bf16 v[128:131], a[208:211], a[196:199], v[128:131]\n\t"
"s_nop 1\n\t"
"v_cvt_pk_bf16_f32 v228, v228, v229\n\t"
"v_cvt_pk_bf16_f32 v229, v230, v231\n\t"
"v_mfma_f32_16x16x32_bf16 v[128:131], a[228:231], a[200:203], v[128:131]\n\t"
"v_mfma_f32_16x16x32_bf16 v[128:131], a[212:215], a[204:207], v[128:131]\n\t"
"buffer_load_dwordx4 a[208:211], v252, s[8:11], s20 offen offset:1024 sc1\n\t"
"s_waitcnt lgkmcnt(0)\n\t"
"v_mfma_f32_16x16x32_bf16 v[232:235], v[232:235], v[220:223], 0\n\t"
"buffer_load_dwordx4 a[212:215], v252, s[8:11], s20 offen offset:2048 sc1\n\t"
"s_waitcnt vmcnt(12)\n\t"
"v_mfma_f32_16x16x32_bf16 v[132:135], a[232:235], a[168:171], v[132:135]\n\t"
"v_cvt_pk_bf16_f32 v230, v224, v225\n\t"
"v_cvt_pk_bf16_f32 v231, v226, v227\n\t"
"v_mfma_f32_16x16x32_bf16 v[132:135], a[236:239], a[172:175], v[132:135]\n\t"
"ds_read_b64_tr_b16 v[224:225], v246 offset:53248\n\t"
"v_mfma_f32_16x16x32_bf16 v[132:135], a[216:219], a[176:179], v[132:135]\n\t"
"s_mov_b32 s20, 1179648\n\t"
"buffer_load_dwordx4 a[228:231], v252, s[8:11], s20 offen offset:0 sc1\n\t"
"v_mfma_f32_16x16x32_bf16 v[136:139], a[232:235], a[172:175], v[136:139]\n\t"
"ds_read_b64_tr_b16 v[226:227], v246 offset:56320\n\t"
"v_mfma_f32_16x16x32_bf16 v[136:139], a[236:239], a[176:179], v[136:139]\n\t"
"v_cvt_pk_bf16_f32 v232, v232, v233\n\t"
"v_cvt_pk_bf16_f32 v233, v234, v235\n\t"
"v_mfma_f32_16x16x32_bf16 v[136:139], a[216:219], a[180:183], v[136:139]\n\t"
"ds_write2st64_b64 v245, v[228:229], v[230:231] offset0:5 offset1:6\n\t"
"v_mfma_f32_16x16x32_bf16 v[140:143], a[232:235], a[176:179], v[140:143]\n\t"
"v_mfma_f32_16x16x32_bf16 v[140:143], a[236:239], a[180:183], v[140:143]\n\t"
"v_mfma_f32_16x16x32_bf16 v[140:143], a[216:219], a[184:187], v[140:143]\n\t"
"v_mfma_f32_16x16x32_bf16 v[144:147], a[232:235], a[180:183], v[144:147]\n\t"
"v_mfma_f32_16x16x32_bf16 v[144:147], a[236:239], a[184:187], v[144:147]\n\t"
"v_mfma_f32_16x16x32_bf16 v[144:147], a[216:219], a[188:191], v[144:147]\n\t"
"v_mfma_f32_16x16x32_bf16 v[148:151], a[232:235], a[184:187], v[148:151]\n\t"
"v_mfma_f32_16x16x32_bf16 v[148:151], a[236:239], a[188:191], v[148:151]\n\t"
"v_mfma_f32_16x16x32_bf16 v[148:151], a[216:219], a[192:195], v[148:151]\n\t"
"s_waitcnt lgkmcnt(1)\n\t"
"v_mfma_f32_16x16x32_bf16 v[224:227], v[224:227], v[220:223], 0\n\t"
"v_mfma_f32_16x16x32_bf16 v[152:155], a[232:235], a[188:191], v[152:155]\n\t"
"v_mfma_f32_16x16x32_bf16 v[152:155], a[236:239], a[192:195], v[152:155]\n\t"
"v_mfma_f32_16x16x32_bf16 v[152:155], a[216:219], a[196:199], v[152:155]\n\t"
"v_mfma_f32_16x16x32_bf16 v[156:159], a[232:235], a[192:195], v[156:159]\n\t"
"v_mfma_f32_16x16x32_bf16 v[156:159], a[236:239], a[196:199], v[156:159]\n\t"
"s_nop 2\n\t"
"v_cvt_pk_bf16_f32 v234, v224, v225\n\t"
"v_cvt_pk_bf16_f32 v235, v226, v227\n\t"
"v_mfma_f32_16x16x32_bf16 v[156:159], a[216:219], a[200:203], v[156:159]\n\t"
"ds_write2st64_b64 v245, v[232:233], v[234:235] offset0:7 offset1:8\n\t"
"v_mfma_f32_16x16x32_bf16 v[160:163], a[232:235], a[196:199], v[160:163]\n\t"
"v_mfma_f32_16x16x32_bf16 v[160:163], a[236:239], a[200:203], v[160:163]\n\t"
"v_mfma_f32_16x16x32_bf16 v[160:163], a[216:219], a[204:207], v[160:163]\n\t"
"buffer_load_dwordx4 a[232:235], v252, s[8:11], s20 offen offset:1024 sc1\n\t"
"s_waitcnt vmcnt(11)\n\t"
"v_mfma_f32_16x16x32_bf16 v[164:167], a[240:243], a[168:171], v[164:167]\n\t"
"v_mfma_f32_16x16x32_bf16 v[164:167], a[244:247], a[172:175], v[164:167]\n\t"
"v_mfma_f32_16x16x32_bf16 v[164:167], v[96:99], a[176:179], v[164:167]\n\t"
"buffer_load_dwordx4 a[216:219], v252, s[8:11], s20 offen offset:2048 sc1\n\t"
"s_waitcnt lgkmcnt(0)\n\t"
"s_barrier\n\t"
"v_mfma_f32_16x16x32_bf16 v[168:171], a[240:243], a[172:175], v[168:171]\n\t"
"ds_read_b128 a[168:171], v244 offset:0\n\t"
"v_mfma_f32_16x16x32_bf16 v[168:171], a[244:247], a[176:179], v[168:171]\n\t"
"ds_read_b128 a[172:175], v244 offset:512\n\t"
"v_mfma_f32_16x16x32_bf16 v[168:171], v[96:99], a[180:183], v[168:171]\n\t"
"s_mov_b32 s20, 1191936\n\t"
"buffer_load_dwordx4 a[236:239], v252, s[8:11], s20 offen offset:0 sc1\n\t"
"v_mfma_f32_16x16x32_bf16 v[172:175], a[240:243], a[176:179], v[172:175]\n\t"
"ds_read_b128 a[176:179], v244 offset:1024\n\t"
"v_mfma_f32_16x16x32_bf16 v[172:175], a[244:247], a[180:183], v[172:175]\n\t"
"v_mfma_f32_16x16x32_bf16 v[172:175], v[96:99], a[184:187], v[172:175]\n\t"
"v_mfma_f32_16x16x32_bf16 v[176:179], a[240:243], a[180:183], v[176:179]\n\t"
"ds_read_b128 a[180:183], v244 offset:1536\n\t"
"v_mfma_f32_16x16x32_bf16 v[176:179], a[244:247], a[184:187], v[176:179]\n\t"
"v_mfma_f32_16x16x32_bf16 v[176:179], v[96:99], a[188:191], v[176:179]\n\t"
"v_mfma_f32_16x16x32_bf16 v[180:183], a[240:243], a[184:187], v[180:183]\n\t"
"ds_read_b128 a[184:187], v244 offset:2048\n\t"
"v_mfma_f32_16x16x32_bf16 v[180:183], a[244:247], a[188:191], v[180:183]\n\t"
"v_mfma_f32_16x16x32_bf16 v[180:183], v[96:99], a[192:195], v[180:183]\n\t"
"v_mfma_f32_16x16x32_bf16 v[184:187], a[240:243], a[188:191], v[184:187]\n\t"
"ds_read_b128 a[188:191], v244 offset:2560\n\t"
"v_mfma_f32_16x16x32_bf16 v[184:187], a[244:247], a[192:195], v[184:187]\n\t"
"v_mfma_f32_16x16x32_bf16 v[184:187], v[96:99], a[196:199], v[184:187]\n\t"
"v_mfma_f32_16x16x32_bf16 v[188:191], a[240:243], a[192:195], v[188:191]\n\t"
"ds_read_b128 a[192:195], v244 offset:3072\n\t"
"v_mfma_f32_16x16x32_bf16 v[188:191], a[244:247], a[196:199], v[188:191]\n\t"
"v_mfma_f32_16x16x32_bf16 v[188:191], v[96:99], a[200:203], v[188:191]\n\t"
"v_mfma_f32_16x16x32_bf16 v[192:195], a[240:243], a[196:199], v[192:195]\n\t"
"ds_read_b128 a[196:199], v244 offset:3584\n\t"
"v_mfma_f32_16x16x32_bf16 v[192:195], a[244:247], a[200:203], v[192:195]\n\t"
"ds_read_b128 a[200:203], v244 offset:4096\n\t"
"v_mfma_f32_16x16x32_bf16 v[192:195], v[96:99], a[204:207], v[192:195]\n\t"
"buffer_load_dwordx4 a[240:243], v252, s[8:11], s20 offen offset:1024 sc1\n\t"
"Lst30_%=:\n\t"
"s_waitcnt vmcnt(11) lgkmcnt(6)\n\t"
"v_mfma_f32_16x16x32_bf16 v[0:3], a[220:223], a[168:171], v[0:3]\n\t"
"ds_read_b128 a[204:207], v244 offset:4608\n\t"
"v_xor_b32 v246, 0xc0, v246\n\t"
"v_mfma_f32_16x16x32_bf16 v[0:3], a[248:251], a[172:175], v[0:3]\n\t"
"v_mfma_f32_16x16x32_bf16 v[0:3], v[236:239], a[176:179], v[0:3]\n\t"
"s_waitcnt lgkmcnt(6)\n\t"
"v_mfma_f32_16x16x32_bf16 v[4:7], a[220:223], a[172:175], v[4:7]\n\t"
"v_mfma_f32_16x16x32_bf16 v[4:7], a[248:251], a[176:179], v[4:7]\n\t"
"v_mfma_f32_16x16x32_bf16 v[4:7], v[236:239], a[180:183], v[4:7]\n\t"
"buffer_load_dwordx4 v[96:99], v252, s[8:11], s20 offen offset:2048 sc1\n\t"
"s_waitcnt lgkmcnt(5)\n\t"
"v_mfma_f32_16x16x32_bf16 v[8:11], a[220:223], a[176:179], v[8:11]\n\t"
"v_mfma_f32_16x16x32_bf16 v[8:11], a[248:251], a[180:183], v[8:11]\n\t"
"v_mfma_f32_16x16x32_bf16 v[8:11], v[236:239], a[184:187], v[8:11]\n\t"
"s_mov_b32 s20, 1204224\n\t"
"buffer_load_dwordx4 a[244:247], v252, s[8:11], s20 offen offset:0 sc1\n\t"
"s_waitcnt lgkmcnt(4)\n\t"
"v_mfma_f32_16x16x32_bf16 v[12:15], a[220:223], a[180:183], v[12:15]\n\t"
"v_mfma_f32_16x16x32_bf16 v[12:15], a[248:251], a[184:187], v[12:15]\n\t"
"v_mfma_f32_16x16x32_bf16 v[12:15], v[236:239], a[188:191], v[12:15]\n\t"
"s_waitcnt lgkmcnt(3)\n\t"
"v_mfma_f32_16x16x32_bf16 v[16:19], a[220:223], a[184:187], v[16:19]\n\t"
"v_mfma_f32_16x16x32_bf16 v[16:19], a[248:251], a[188:191], v[16:19]\n\t"
"v_mfma_f32_16x16x32_bf16 v[16:19], v[236:239], a[192:195], v[16:19]\n\t"
"ds_read_b64_tr_b16 v[228:229], v246 offset:1024\n\t"
"ds_read_b64_tr_b16 v[230:231], v246 offset:4096\n\t"
"s_waitcnt lgkmcnt(4)\n\t"
"v_mfma_f32_16x16x32_bf16 v[20:23], a[220:223], a[188:191], v[20:23]\n\t"
"v_mfma_f32_16x16x32_bf16 v[20:23], a[248:251], a[192:195], v[20:23]\n\t"
"v_mfma_f32_16x16x32_bf16 v[20:23], v[236:239], a[196:199], v[20:23]\n\t"
"s_waitcnt lgkmcnt(3)\n\t"
"v_mfma_f32_16x16x32_bf16 v[24:27], a[220:223], a[192:195], v[24:27]\n\t"
"v_mfma_f32_16x16x32_bf16 v[24:27], a[248:251], a[196:199], v[24:27]\n\t"
"v_mfma_f32_16x16x32_bf16 v[24:27], v[236:239], a[200:203], v[24:27]\n\t"
"s_waitcnt lgkmcnt(2)\n\t"
"v_mfma_f32_16x16x32_bf16 v[28:31], a[220:223], a[196:199], v[28:31]\n\t"
"ds_read_b64_tr_b16 v[224:225], v246 offset:7168\n\t"
"v_mfma_f32_16x16x32_bf16 v[28:31], a[248:251], a[200:203], v[28:31]\n\t"
"ds_read_b64_tr_b16 v[226:227], v246 offset:10240\n\t"
"v_mfma_f32_16x16x32_bf16 v[28:31], v[236:239], a[204:207], v[28:31]\n\t"
"buffer_load_dwordx4 a[220:223], v252, s[8:11], s20 offen offset:1024 sc1\n\t"
"s_waitcnt vmcnt(11)\n\t"
"v_mfma_f32_16x16x32_bf16 v[32:35], a[252:255], a[168:171], v[32:35]\n\t"
"v_mfma_f32_16x16x32_bf16 v[32:35], a[224:227], a[172:175], v[32:35]\n\t"
"v_mfma_f32_16x16x32_bf16 v[32:35], v[240:243], a[176:179], v[32:35]\n\t"
"buffer_load_dwordx4 v[236:239], v252, s[8:11], s20 offen offset:2048 sc1\n\t"
"v_mfma_f32_16x16x32_bf16 v[36:39], a[252:255], a[172:175], v[36:39]\n\t"
"v_mfma_f32_16x16x32_bf16 v[36:39], a[224:227], a[176:179], v[36:39]\n\t"
"v_mfma_f32_16x16x32_bf16 v[36:39], v[240:243], a[180:183], v[36:39]\n\t"
"s_mov_b32 s20, 1216512\n\t"
"buffer_load_dwordx4 a[248:251], v252, s[8:11], s20 offen offset:0 sc1\n\t"
"s_waitcnt lgkmcnt(2)\n\t"
"v_mfma_f32_16x16x32_bf16 v[228:231], v[228:231], v[220:223], 0\n\t"
"v_mfma_f32_16x16x32_bf16 v[40:43], a[252:255], a[176:179], v[40:43]\n\t"
"ds_read_b64_tr_b16 v[232:233], v246 offset:13312\n\t"
"v_mfma_f32_16x16x32_bf16 v[40:43], a[224:227], a[180:183], v[40:43]\n\t"
"ds_read_b64_tr_b16 v[234:235], v246 offset:16384\n\t"
"v_mfma_f32_16x16x32_bf16 v[40:43], v[240:243], a[184:187], v[40:43]\n\t"
"v_mfma_f32_16x16x32_bf16 v[44:47], a[252:255], a[180:183], v[44:47]\n\t"
"v_mfma_f32_16x16x32_bf16 v[44:47], a[224:227], a[184:187], v[44:47]\n\t"
"s_nop 0\n\t"
"v_cvt_pk_bf16_f32 v228, v228, v229\n\t"
"v_mfma_f32_16x16x32_bf16 v[44:47], v[240:243], a[188:191], v[44:47]\n\t"
"v_cvt_pk_bf16_f32 v229, v230, v231\n\t"
"s_waitcnt lgkmcnt(2)\n\t"
"v_mfma_f32_16x16x32_bf16 v[224:227], v[224:227], v[220:223], 0\n\t"
"v_mfma_f32_16x16x32_bf16 v[48:51], a[252:255], a[184:187], v[48:51]\n\t"
"v_mfma_f32_16x16x32_bf16 v[48:51], a[224:227], a[188:191], v[48:51]\n\t"
"v_mfma_f32_16x16x32_bf16 v[48:51], v[240:243], a[192:195], v[48:51]\n\t"
"v_mfma_f32_16x16x32_bf16 v[52:55], a[252:255], a[188:191], v[52:55]\n\t"
"v_mfma_f32_16x16x32_bf16 v[52:55], a[224:227], a[192:195], v[52:55]\n\t"
"s_nop 2\n\t"
"v_cvt_pk_bf16_f32 v230, v224, v225\n\t"
"v_cvt_pk_bf16_f32 v231, v226, v227\n\t"
"v_mfma_f32_16x16x32_bf16 v[52:55], v[240:243], a[196:199], v[52:55]\n\t"
"ds_read_b64_tr_b16 v[224:225], v246 offset:20992\n\t"
"v_mfma_f32_16x16x32_bf16 v[56:59], a[252:255], a[192:195], v[56:59]\n\t"
"ds_read_b64_tr_b16 v[226:227], v246 offset:24064\n\t"
"v_mfma_f32_16x16x32_bf16 v[56:59], a[224:227], a[196:199], v[56:59]\n\t"
"v_mfma_f32_16x16x32_bf16 v[56:59], v[240:243], a[200:203], v[56:59]\n\t"
"s_waitcnt lgkmcnt(2)\n\t"
"v_mfma_f32_16x16x32_bf16 v[232:235], v[232:235], v[220:223], 0\n\t"
"v_mfma_f32_16x16x32_bf16 v[60:63], a[252:255], a[196:199], v[60:63]\n\t"
"v_mfma_f32_16x16x32_bf16 v[60:63], a[224:227], a[200:203], v[60:63]\n\t"
"v_mfma_f32_16x16x32_bf16 v[60:63], v[240:243], a[204:207], v[60:63]\n\t"
"buffer_load_dwordx4 a[252:255], v252, s[8:11], s20 offen offset:1024 sc1\n\t"
"s_waitcnt vmcnt(11)\n\t"
"v_mfma_f32_16x16x32_bf16 v[64:67], v[248:251], a[168:171], v[64:67]\n\t"
"ds_read_b128 a[168:171], v244 offset:9216\n\t"
"v_mfma_f32_16x16x32_bf16 v[64:67], a[208:211], a[172:175], v[64:67]\n\t"
"v_cvt_pk_bf16_f32 v232, v232, v233\n\t"
"v_cvt_pk_bf16_f32 v233, v234, v235\n\t"
"v_mfma_f32_16x16x32_bf16 v[64:67], a[212:215], a[176:179], v[64:67]\n\t"
"v_mfma_f32_16x16x32_bf16 v[68:71], v[248:251], a[172:175], v[68:71]\n\t"
"ds_read_b128 a[172:175], v244 offset:9728\n\t"
"v_mfma_f32_16x16x32_bf16 v[68:71], a[208:211], a[176:179], v[68:71]\n\t"
"v_mfma_f32_16x16x32_bf16 v[68:71], a[212:215], a[180:183], v[68:71]\n\t"
"buffer_load_dwordx4 v[240:243], v252, s[8:11], s20 offen offset:2048 sc1\n\t"
"s_waitcnt lgkmcnt(2)\n\t"
"v_mfma_f32_16x16x32_bf16 v[224:227], v[224:227], v[220:223], 0\n\t"
"s_mov_b32 s20, 1228800\n\t"
"buffer_load_dwordx4 a[224:227], v252, s[8:11], s20 offen offset:0 sc1\n\t"
"v_mfma_f32_16x16x32_bf16 v[72:75], v[248:251], a[176:179], v[72:75]\n\t"
"ds_read_b128 a[176:179], v244 offset:10240\n\t"
"v_mfma_f32_16x16x32_bf16 v[72:75], a[208:211], a[180:183], v[72:75]\n\t"
"v_mfma_f32_16x16x32_bf16 v[72:75], a[212:215], a[184:187], v[72:75]\n\t"
"v_mfma_f32_16x16x32_bf16 v[76:79], v[248:251], a[180:183], v[76:79]\n\t"
"ds_read_b128 a[180:183], v244 offset:10752\n\t"
"v_mfma_f32_16x16x32_bf16 v[76:79], a[208:211], a[184:187], v[76:79]\n\t"
"v_cvt_pk_bf16_f32 v234, v224, v225\n\t"
"v_cvt_pk_bf16_f32 v235, v226, v227\n\t"
"v_mfma_f32_16x16x32_bf16 v[76:79], a[212:215], a[188:191], v[76:79]\n\t"
"ds_read_b64_tr_b16 v[224:225], v246 offset:27136\n\t"
"v_mfma_f32_16x16x32_bf16 v[80:83], v[248:251], a[184:187], v[80:83]\n\t"
"ds_read_b64_tr_b16 v[226:227], v246 offset:30208\n\t"
"v_mfma_f32_16x16x32_bf16 v[80:83], a[208:211], a[188:191], v[80:83]\n\t"
"ds_read_b128 a[184:187], v244 offset:11264\n\t"
"v_mfma_f32_16x16x32_bf16 v[80:83], a[212:215], a[192:195], v[80:83]\n\t"
"v_mfma_f32_16x16x32_bf16 v[84:87], v[248:251], a[188:191], v[84:87]\n\t"
"ds_read_b128 a[188:191], v244 offset:11776\n\t"
"v_mfma_f32_16x16x32_bf16 v[84:87], a[208:211], a[192:195], v[84:87]\n\t"
"v_mfma_f32_16x16x32_bf16 v[84:87], a[212:215], a[196:199], v[84:87]\n\t"
"v_mfma_f32_16x16x32_bf16 v[88:91], v[248:251], a[192:195], v[88:91]\n\t"
"ds_read_b128 a[192:195], v244 offset:12288\n\t"
"v_mfma_f32_16x16x32_bf16 v[88:91], a[208:211], a[196:199], v[88:91]\n\t"
"v_mfma_f32_16x16x32_bf16 v[88:91], a[212:215], a[200:203], v[88:91]\n\t"
"v_mfma_f32_16x16x32_bf16 v[92:95], v[248:251], a[196:199], v[92:95]\n\t"
"ds_read_b128 a[196:199], v244 offset:12800\n\t"
"v_mfma_f32_16x16x32_bf16 v[92:95], a[208:211], a[200:203], v[92:95]\n\t"
"ds_read_b128 a[200:203], v244 offset:13312\n\t"
"v_mfma_f32_16x16x32_bf16 v[92:95], a[212:215], a[204:207], v[92:95]\n\t"
"buffer_load_dwordx4 v[248:251], v252, s[8:11], s20 offen offset:1024 sc1\n\t"
"s_waitcnt lgkmcnt(5)\n\t"
"v_mfma_f32_16x16x32_bf16 v[224:227], v[224:227], v[220:223], 0\n\t"
"ds_read_b128 a[204:207], v244 offset:13824\n\t"
"s_waitcnt vmcnt(11)\n\t"
"v_mfma_f32_16x16x32_bf16 v[100:103], a[228:231], a[168:171], v[100:103]\n\t"
"v_mfma_f32_16x16x32_bf16 v[100:103], a[232:235], a[172:175], v[100:103]\n\t"
"v_mfma_f32_16x16x32_bf16 v[100:103], a[216:219], a[176:179], v[100:103]\n\t"
"buffer_load_dwordx4 a[208:211], v252, s[8:11], s20 offen offset:2048 sc1\n\t"
"v_mfma_f32_16x16x32_bf16 v[104:107], a[228:231], a[172:175], v[104:107]\n\t"
"v_mfma_f32_16x16x32_bf16 v[104:107], a[232:235], a[176:179], v[104:107]\n\t"
"v_cvt_pk_bf16_f32 v224, v224, v225\n\t"
"v_cvt_pk_bf16_f32 v225, v226, v227\n\t"
"v_mfma_f32_16x16x32_bf16 v[104:107], a[216:219], a[180:183], v[104:107]\n\t"
"s_mov_b32 s20, 1241088\n\t"
"buffer_load_dwordx4 a[212:215], v252, s[8:11], s20 offen offset:0 sc1\n\t"
"s_waitcnt lgkmcnt(0)\n\t"
"s_barrier\n\t"
"v_mfma_f32_16x16x32_bf16 v[108:111], a[228:231], a[176:179], v[108:111]\n\t"
"v_fma_f32 v0, v100, s52, v0\n\t"
"v_fma_f32 v1, v101, s52, v1\n\t"
"v_mfma_f32_16x16x32_bf16 v[108:111], a[232:235], a[180:183], v[108:111]\n\t"
"v_fma_f32 v2, v102, s52, v2\n\t"
"v_fma_f32 v3, v103, s52, v3\n\t"
"v_mfma_f32_16x16x32_bf16 v[108:111], a[216:219], a[184:187], v[108:111]\n\t"
"ds_write2st64_b64 v245, v[228:229], v[230:231] offset0:0 offset1:1\n\t"
"v_mfma_f32_16x16x32_bf16 v[112:115], a[228:231], a[180:183], v[112:115]\n\t"
"ds_read_b64_tr_b16 v[228:229], v246 offset:33280\n\t"
"v_fma_f32 v100, v0, s54, -v100\n\t"
"v_mfma_f32_16x16x32_bf16 v[112:115], a[232:235], a[184:187], v[112:115]\n\t"
"ds_read_b64_tr_b16 v[230:231], v246 offset:36352\n\t"
"v_fma_f32 v101, v1, s54, -v101\n\t"
"v_mfma_f32_16x16x32_bf16 v[112:115], a[216:219], a[188:191], v[112:115]\n\t"
"ds_write2st64_b64 v245, v[232:233], v[234:235] offset0:2 offset1:3\n\t"
"v_mfma_f32_16x16x32_bf16 v[116:119], a[228:231], a[184:187], v[116:119]\n\t"
"ds_read_b64_tr_b16 v[232:233], v246 offset:40960\n\t"
"v_mfma_f32_16x16x32_bf16 v[116:119], a[232:235], a[188:191], v[116:119]\n\t"
"ds_read_b64_tr_b16 v[234:235], v246 offset:44032\n\t"
"v_mfma_f32_16x16x32_bf16 v[116:119], a[216:219], a[192:195], v[116:119]\n\t"
"ds_write_b64 v245, v[224:225] offset:2048\n\t"
"v_mfma_f32_16x16x32_bf16 v[120:123], a[228:231], a[188:191], v[120:123]\n\t"
"v_mfma_f32_16x16x32_bf16 v[120:123], a[232:235], a[192:195], v[120:123]\n\t"
"v_mfma_f32_16x16x32_bf16 v[120:123], a[216:219], a[196:199], v[120:123]\n\t"
"ds_read_b64_tr_b16 v[224:225], v246 offset:47104\n\t"
"s_waitcnt lgkmcnt(5)\n\t"
"v_mfma_f32_16x16x32_bf16 v[228:231], v[228:231], v[220:223], 0\n\t"
"ds_read_b64_tr_b16 v[226:227], v246 offset:50176\n\t"
"v_mfma_f32_16x16x32_bf16 v[124:127], a[228:231], a[192:195], v[124:127]\n\t"
"v_mfma_f32_16x16x32_bf16 v[124:127], a[232:235], a[196:199], v[124:127]\n\t"
"v_fma_f32 v102, v2, s54, -v102\n\t"
"v_mfma_f32_16x16x32_bf16 v[124:127], a[216:219], a[200:203], v[124:127]\n\t"
"s_waitcnt lgkmcnt(3)\n\t"
"v_mfma_f32_16x16x32_bf16 v[232:235], v[232:235], v[220:223], 0\n\t"
"v_fma_f32 v103, v3, s54, -v103\n\t"
"v_mfma_f32_16x16x32_bf16 v[128:131], a[228:231], a[196:199], v[128:131]\n\t"
"v_cvt_pk_bf16_f32 v228, v228, v229\n\t"
"v_cvt_pk_bf16_f32 v229, v230, v231\n\t"
"v_mfma_f32_16x16x32_bf16 v[128:131], a[232:235], a[200:203], v[128:131]\n\t"
"v_fma_f32 v4, v104, s52, v4\n\t"
"v_mfma_f32_16x16x32_bf16 v[128:131], a[216:219], a[204:207], v[128:131]\n\t"
"buffer_load_dwordx4 a[228:231], v252, s[8:11], s20 offen offset:1024 sc1\n\t"
"s_waitcnt lgkmcnt(0)\n\t"
"v_mfma_f32_16x16x32_bf16 v[224:227], v[224:227], v[220:223], 0\n\t"
"buffer_load_dwordx4 a[216:219], v252, s[8:11], s20 offen offset:2048 sc1\n\t"
"s_waitcnt vmcnt(12)\n\t"
"v_mfma_f32_16x16x32_bf16 v[132:135], a[236:239], a[168:171], v[132:135]\n\t"
"v_cvt_pk_bf16_f32 v230, v232, v233\n\t"
"v_cvt_pk_bf16_f32 v231, v234, v235\n\t"
"v_mfma_f32_16x16x32_bf16 v[132:135], a[240:243], a[172:175], v[132:135]\n\t"
"ds_read_b64_tr_b16 v[232:233], v246 offset:53248\n\t"
"v_fma_f32 v5, v105, s52, v5\n\t"
"v_mfma_f32_16x16x32_bf16 v[132:135], v[96:99], a[176:179], v[132:135]\n\t"
"s_mov_b32 s20, 1253376\n\t"
"buffer_load_dwordx4 a[232:235], v252, s[8:11], s20 offen offset:0 sc1\n\t"
"v_mfma_f32_16x16x32_bf16 v[136:139], a[236:239], a[172:175], v[136:139]\n\t"
"ds_read_b64_tr_b16 v[234:235], v246 offset:56320\n\t"
"v_fma_f32 v6, v106, s52, v6\n\t"
"v_mfma_f32_16x16x32_bf16 v[136:139], a[240:243], a[176:179], v[136:139]\n\t"
"v_cvt_pk_bf16_f32 v224, v224, v225\n\t"
"v_cvt_pk_bf16_f32 v225, v226, v227\n\t"
"v_mfma_f32_16x16x32_bf16 v[136:139], v[96:99], a[180:183], v[136:139]\n\t"
"ds_write2st64_b64 v245, v[228:229], v[230:231] offset0:5 offset1:6\n\t"
"v_mfma_f32_16x16x32_bf16 v[140:143], a[236:239], a[176:179], v[140:143]\n\t"
"v_fma_f32 v7, v107, s52, v7\n\t"
"v_fma_f32 v104, v4, s54, -v104\n\t"
"v_mfma_f32_16x16x32_bf16 v[140:143], a[240:243], a[180:183], v[140:143]\n\t"
"v_fma_f32 v105, v5, s54, -v105\n\t"
"v_fma_f32 v106, v6, s54, -v106\n\t"
"v_mfma_f32_16x16x32_bf16 v[140:143], v[96:99], a[184:187], v[140:143]\n\t"
"v_fma_f32 v107, v7, s54, -v107\n\t"
"v_fma_f32 v8, v108, s52, v8\n\t"
"v_mfma_f32_16x16x32_bf16 v[144:147], a[236:239], a[180:183], v[144:147]\n\t"
"v_fma_f32 v9, v109, s52, v9\n\t"
"v_fma_f32 v10, v110, s52, v10\n\t"
"v_mfma_f32_16x16x32_bf16 v[144:147], a[240:243], a[184:187], v[144:147]\n\t"
"v_fma_f32 v11, v111, s52, v11\n\t"
"v_fma_f32 v108, v8, s54, -v108\n\t"
"v_mfma_f32_16x16x32_bf16 v[144:147], v[96:99], a[188:191], v[144:147]\n\t"
"v_fma_f32 v109, v9, s54, -v109\n\t"
"v_mfma_f32_16x16x32_bf16 v[148:151], a[236:239], a[184:187], v[148:151]\n\t"
"v_fma_f32 v110, v10, s54, -v110\n\t"
"v_fma_f32 v111, v11, s54, -v111\n\t"
"v_mfma_f32_16x16x32_bf16 v[148:151], a[240:243], a[188:191], v[148:151]\n\t"
"v_fma_f32 v12, v112, s52, v12\n\t"
"v_mfma_f32_16x16x32_bf16 v[148:151], v[96:99], a[192:195], v[148:151]\n\t"
"v_fma_f32 v13, v113, s52, v13\n\t"
"s_waitcnt lgkmcnt(1)\n\t"
"v_mfma_f32_16x16x32_bf16 v[232:235], v[232:235], v[220:223], 0\n\t"
"v_fma_f32 v14, v114, s52, v14\n\t"
"v_fma_f32 v15, v115, s52, v15\n\t"
"v_mfma_f32_16x16x32_bf16 v[152:155], a[236:239], a[188:191], v[152:155]\n\t"
"v_fma_f32 v112, v12, s54, -v112\n\t"
"v_mfma_f32_16x16x32_bf16 v[152:155], a[240:243], a[192:195], v[152:155]\n\t"
"v_fma_f32 v113, v13, s54, -v113\n\t"
"v_mfma_f32_16x16x32_bf16 v[152:155], v[96:99], a[196:199], v[152:155]\n\t"
"v_fma_f32 v114, v14, s54, -v114\n\t"
"v_fma_f32 v115, v15, s54, -v115\n\t"
"v_mfma_f32_16x16x32_bf16 v[156:159], a[236:239], a[192:195], v[156:159]\n\t"
"v_fma_f32 v16, v116, s52, v16\n\t"
"v_fma_f32 v17, v117, s52, v17\n\t"
"v_mfma_f32_16x16x32_bf16 v[156:159], a[240:243], a[196:199], v[156:159]\n\t"
"v_cvt_pk_bf16_f32 v226, v232, v233\n\t"
"v_cvt_pk_bf16_f32 v227, v234, v235\n\t"
"v_mfma_f32_16x16x32_bf16 v[156:159], v[96:99], a[200:203], v[156:159]\n\t"
"ds_write2st64_b64 v245, v[224:225], v[226:227] offset0:7 offset1:8\n\t"
"v_mfma_f32_16x16x32_bf16 v[160:163], a[236:239], a[196:199], v[160:163]\n\t"
"v_fma_f32 v18, v118, s52, v18\n\t"
"v_fma_f32 v19, v119, s52, v19\n\t"
"v_mfma_f32_16x16x32_bf16 v[160:163], a[240:243], a[200:203], v[160:163]\n\t"
"v_fma_f32 v116, v16, s54, -v116\n\t"
"v_fma_f32 v117, v17, s54, -v117\n\t"
"v_mfma_f32_16x16x32_bf16 v[160:163], v[96:99], a[204:207], v[160:163]\n\t"
"buffer_load_dwordx4 a[236:239], v252, s[8:11], s20 offen offset:1024 sc1\n\t"
"s_waitcnt vmcnt(11)\n\t"
"v_mfma_f32_16x16x32_bf16 v[164:167], a[244:247], a[168:171], v[164:167]\n\t"
"v_fma_f32 v118, v18, s54, -v118\n\t"
"v_fma_f32 v119, v19, s54, -v119\n\t"
"v_mfma_f32_16x16x32_bf16 v[164:167], a[220:223], a[172:175], v[164:167]\n\t"
"v_fma_f32 v20, v120, s52, v20\n\t"
"v_fma_f32 v21, v121, s52, v21\n\t"
"v_mfma_f32_16x16x32_bf16 v[164:167], v[236:239], a[176:179], v[164:167]\n\t"
"buffer_load_dwordx4 a[240:243], v252, s[8:11], s20 offen offset:2048 sc1\n\t"
"s_waitcnt lgkmcnt(0)\n\t"
"s_barrier\n\t"
"v_mfma_f32_16x16x32_bf16 v[168:171], a[244:247], a[172:175], v[168:171]\n\t"
"ds_read_b128 a[168:171], v244 offset:0\n\t"
"v_fma_f32 v22, v122, s52, v22\n\t"
"v_mfma_f32_16x16x32_bf16 v[168:171], a[220:223], a[176:179], v[168:171]\n\t"
"ds_read_b128 a[172:175], v244 offset:512\n\t"
"v_fma_f32 v23, v123, s52, v23\n\t"
"v_mfma_f32_16x16x32_bf16 v[168:171], v[236:239], a[180:183], v[168:171]\n\t"
"v_fma_f32 v120, v20, s54, -v120\n\t"
"v_fma_f32 v121, v21, s54, -v121\n\t"
"v_mfma_f32_16x16x32_bf16 v[172:175], a[244:247], a[176:179], v[172:175]\n\t"
"ds_read_b128 a[176:179], v244 offset:1024\n\t"
"v_fma_f32 v122, v22, s54, -v122\n\t"
"v_mfma_f32_16x16x32_bf16 v[172:175], a[220:223], a[180:183], v[172:175]\n\t"
"v_fma_f32 v123, v23, s54, -v123\n\t"
"v_fma_f32 v24, v124, s52, v24\n\t"
"v_mfma_f32_16x16x32_bf16 v[172:175], v[236:239], a[184:187], v[172:175]\n\t"
"v_fma_f32 v25, v125, s52, v25\n\t"
"v_fma_f32 v26, v126, s52, v26\n\t"
"v_mfma_f32_16x16x32_bf16 v[176:179], a[244:247], a[180:183], v[176:179]\n\t"
"ds_read_b128 a[180:183], v244 offset:1536\n\t"
"v_fma_f32 v27, v127, s52, v27\n\t"
"v_mfma_f32_16x16x32_bf16 v[176:179], a[220:223], a[184:187], v[176:179]\n\t"
"v_fma_f32 v124, v24, s54, -v124\n\t"
"v_fma_f32 v125, v25, s54, -v125\n\t"
"v_mfma_f32_16x16x32_bf16 v[176:179], v[236:239], a[188:191], v[176:179]\n\t"
"v_fma_f32 v126, v26, s54, -v126\n\t"
"v_fma_f32 v127, v27, s54, -v127\n\t"
"v_fma_f32 v28, v128, s52, v28\n\t"
"v_fma_f32 v29, v129, s52, v29\n\t"
"v_mfma_f32_16x16x32_bf16 v[180:183], a[244:247], a[184:187], v[180:183]\n\t"
"ds_read_b128 a[184:187], v244 offset:2048\n\t"
"v_fma_f32 v30, v130, s52, v30\n\t"
"v_mfma_f32_16x16x32_bf16 v[180:183], a[220:223], a[188:191], v[180:183]\n\t"
"v_fma_f32 v31, v131, s52, v31\n\t"
"v_fma_f32 v128, v28, s54, -v128\n\t"
"v_mfma_f32_16x16x32_bf16 v[180:183], v[236:239], a[192:195], v[180:183]\n\t"
"v_fma_f32 v129, v29, s54, -v129\n\t"
"v_fma_f32 v130, v30, s54, -v130\n\t"
"v_fma_f32 v131, v31, s54, -v131\n\t"
"v_fma_f32 v32, v132, s52, v32\n\t"
"v_mfma_f32_16x16x32_bf16 v[184:187], a[244:247], a[188:191], v[184:187]\n\t"
"ds_read_b128 a[188:191], v244 offset:2560\n\t"
"v_fma_f32 v33, v133, s52, v33\n\t"
"v_mfma_f32_16x16x32_bf16 v[184:187], a[220:223], a[192:195], v[184:187]\n\t"
"v_fma_f32 v34, v134, s52, v34\n\t"
"v_fma_f32 v35, v135, s52, v35\n\t"
"v_mfma_f32_16x16x32_bf16 v[184:187], v[236:239], a[196:199], v[184:187]\n\t"
"v_fma_f32 v132, v32, s54, -v132\n\t"
"v_fma_f32 v133, v33, s54, -v133\n\t"
"v_fma_f32 v134, v34, s54, -v134\n\t"
"v_fma_f32 v135, v35, s54, -v135\n\t"
"v_mfma_f32_16x16x32_bf16 v[188:191], a[244:247], a[192:195], v[188:191]\n\t"
"ds_read_b128 a[192:195], v244 offset:3072\n\t"
"v_fma_f32 v36, v136, s52, v36\n\t"
"v_mfma_f32_16x16x32_bf16 v[188:191], a[220:223], a[196:199], v[188:191]\n\t"
"v_fma_f32 v37, v137, s52, v37\n\t"
"v_fma_f32 v38, v138, s52, v38\n\t"
"v_mfma_f32_16x16x32_bf16 v[188:191], v[236:239], a[200:203], v[188:191]\n\t"
"v_fma_f32 v39, v139, s52, v39\n\t"
"v_fma_f32 v136, v36, s54, -v136\n\t"
"v_fma_f32 v137, v37, s54, -v137\n\t"
"v_fma_f32 v138, v38, s54, -v138\n\t"
"v_mfma_f32_16x16x32_bf16 v[192:195], a[244:247], a[196:199], v[192:195]\n\t"
"ds_read_b128 a[196:199], v244 offset:3584\n\t"
"v_fma_f32 v139, v39, s54, -v139\n\t"
"v_mfma_f32_16x16x32_bf16 v[192:195], a[220:223], a[200:203], v[192:195]\n\t"
"ds_read_b128 a[200:203], v244 offset:4096\n\t"
"v_fma_f32 v40, v140, s52, v40\n\t"
"v_mfma_f32_16x16x32_bf16 v[192:195], v[236:239], a[204:207], v[192:195]\n\t"
"s_mov_b32 s20, 1265664\n\t"
"buffer_load_dwordx4 a[244:247], v252, s[8:11], s20 offen offset:0 sc1\n\t"
"ds_read_b128 a[204:207], v244 offset:4608\n\t"
"v_fma_f32 v41, v141, s52, v41\n\t"
"Lst31_%=:\n\t"
"s_waitcnt vmcnt(10) lgkmcnt(7)\n\t"
"v_mfma_f32_16x16x32_bf16 v[196:199], a[248:251], a[168:171], v[196:199]\n\t"
"v_fma_f32 v42, v142, s52, v42\n\t"
"v_fma_f32 v43, v143, s52, v43\n\t"
"v_mfma_f32_16x16x32_bf16 v[196:199], a[252:255], a[172:175], v[196:199]\n\t"
"v_fma_f32 v140, v40, s54, -v140\n\t"
"v_fma_f32 v141, v41, s54, -v141\n\t"
"v_mfma_f32_16x16x32_bf16 v[196:199], v[240:243], a[176:179], v[196:199]\n\t"
"buffer_load_dwordx4 a[220:223], v252, s[8:11], s20 offen offset:1024 sc1\n\t"
"v_fma_f32 v142, v42, s54, -v142\n\t"
"v_fma_f32 v143, v43, s54, -v143\n\t"
"s_waitcnt lgkmcnt(6)\n\t"
"v_mfma_f32_16x16x32_bf16 v[204:207], a[248:251], a[172:175], v[204:207]\n\t"
"v_fma_f32 v44, v144, s52, v44\n\t"
"v_fma_f32 v45, v145, s52, v45\n\t"
"v_mfma_f32_16x16x32_bf16 v[204:207], a[252:255], a[176:179], v[204:207]\n\t"
"v_fma_f32 v46, v146, s52, v46\n\t"
"v_fma_f32 v47, v147, s52, v47\n\t"
"v_mfma_f32_16x16x32_bf16 v[204:207], v[240:243], a[180:183], v[204:207]\n\t"
"v_fma_f32 v144, v44, s54, -v144\n\t"
"v_fma_f32 v145, v45, s54, -v145\n\t"
"v_fma_f32 v146, v46, s54, -v146\n\t"
"v_fma_f32 v147, v47, s54, -v147\n\t"
"s_waitcnt lgkmcnt(5)\n\t"
"v_mfma_f32_16x16x32_bf16 v[212:215], a[248:251], a[176:179], v[212:215]\n\t"
"v_fma_f32 v48, v148, s52, v48\n\t"
"v_fma_f32 v49, v149, s52, v49\n\t"
"v_mfma_f32_16x16x32_bf16 v[212:215], a[252:255], a[180:183], v[212:215]\n\t"
"v_fma_f32 v50, v150, s52, v50\n\t"
"v_fma_f32 v51, v151, s52, v51\n\t"
"v_mfma_f32_16x16x32_bf16 v[212:215], v[240:243], a[184:187], v[212:215]\n\t"
"v_fma_f32 v148, v48, s54, -v148\n\t"
"v_fma_f32 v149, v49, s54, -v149\n\t"
"v_fma_f32 v150, v50, s54, -v150\n\t"
"v_fma_f32 v151, v51, s54, -v151\n\t"
"s_waitcnt lgkmcnt(4)\n\t"
"v_mfma_f32_16x16x32_bf16 a[0:3], a[248:251], a[180:183], a[0:3]\n\t"
"v_fma_f32 v52, v152, s52, v52\n\t"
"v_fma_f32 v53, v153, s52, v53\n\t"
"v_mfma_f32_16x16x32_bf16 a[0:3], a[252:255], a[184:187], a[0:3]\n\t"
"v_fma_f32 v54, v154, s52, v54\n\t"
"v_fma_f32 v55, v155, s52, v55\n\t"
"v_mfma_f32_16x16x32_bf16 a[0:3], v[240:243], a[188:191], a[0:3]\n\t"
"v_fma_f32 v152, v52, s54, -v152\n\t"
"v_fma_f32 v153, v53, s54, -v153\n\t"
"v_fma_f32 v154, v54, s54, -v154\n\t"
"v_fma_f32 v155, v55, s54, -v155\n\t"
"s_waitcnt lgkmcnt(3)\n\t"
"v_mfma_f32_16x16x32_bf16 a[8:11], a[248:251], a[184:187], a[8:11]\n\t"
"v_fma_f32 v56, v156, s52, v56\n\t"
"v_fma_f32 v57, v157, s52, v57\n\t"
"v_mfma_f32_16x16x32_bf16 a[8:11], a[252:255], a[188:191], a[8:11]\n\t"
"v_fma_f32 v58, v158, s52, v58\n\t"
"v_fma_f32 v59, v159, s52, v59\n\t"
"v_mfma_f32_16x16x32_bf16 a[8:11], v[240:243], a[192:195], a[8:11]\n\t"
"v_fma_f32 v156, v56, s54, -v156\n\t"
"v_fma_f32 v157, v57, s54, -v157\n\t"
"v_fma_f32 v158, v58, s54, -v158\n\t"
"v_fma_f32 v159, v59, s54, -v159\n\t"
"s_waitcnt lgkmcnt(2)\n\t"
"v_mfma_f32_16x16x32_bf16 a[16:19], a[248:251], a[188:191], a[16:19]\n\t"
"v_fma_f32 v60, v160, s52, v60\n\t"
"v_fma_f32 v61, v161, s52, v61\n\t"
"v_mfma_f32_16x16x32_bf16 a[16:19], a[252:255], a[192:195], a[16:19]\n\t"
"v_fma_f32 v62, v162, s52, v62\n\t"
"v_fma_f32 v63, v163, s52, v63\n\t"
"v_mfma_f32_16x16x32_bf16 a[16:19], v[240:243], a[196:199], a[16:19]\n\t"
"v_fma_f32 v160, v60, s54, -v160\n\t"
"v_fma_f32 v161, v61, s54, -v161\n\t"
"v_fma_f32 v162, v62, s54, -v162\n\t"
"v_fma_f32 v163, v63, s54, -v163\n\t"
"s_waitcnt lgkmcnt(1)\n\t"
"v_mfma_f32_16x16x32_bf16 a[24:27], a[248:251], a[192:195], a[24:27]\n\t"
"v_fma_f32 v64, v164, s52, v64\n\t"
"v_fma_f32 v65, v165, s52, v65\n\t"
"v_mfma_f32_16x16x32_bf16 a[24:27], a[252:255], a[196:199], a[24:27]\n\t"
"v_fma_f32 v66, v166, s52, v66\n\t"
"v_fma_f32 v67, v167, s52, v67\n\t"
"v_mfma_f32_16x16x32_bf16 a[24:27], v[240:243], a[200:203], a[24:27]\n\t"
"v_fma_f32 v164, v64, s54, -v164\n\t"
"v_fma_f32 v165, v65, s54, -v165\n\t"
"s_waitcnt lgkmcnt(0)\n\t"
"v_mfma_f32_16x16x32_bf16 a[32:35], a[248:251], a[196:199], a[32:35]\n\t"
"v_fma_f32 v166, v66, s54, -v166\n\t"
"v_fma_f32 v167, v67, s54, -v167\n\t"
"v_mfma_f32_16x16x32_bf16 a[32:35], a[252:255], a[200:203], a[32:35]\n\t"
"v_fma_f32 v68, v168, s52, v68\n\t"
"v_fma_f32 v69, v169, s52, v69\n\t"
"v_mfma_f32_16x16x32_bf16 a[32:35], v[240:243], a[204:207], a[32:35]\n\t"
"buffer_load_dwordx4 a[248:251], v252, s[8:11], s20 offen offset:2048 sc1\n\t"
"v_fma_f32 v70, v170, s52, v70\n\t"
"v_fma_f32 v71, v171, s52, v71\n\t"
"s_waitcnt vmcnt(9)\n\t"
"v_mfma_f32_16x16x32_bf16 a[40:43], a[224:227], a[168:171], a[40:43]\n\t"
"v_fma_f32 v168, v68, s54, -v168\n\t"
"v_fma_f32 v169, v69, s54, -v169\n\t"
"v_mfma_f32_16x16x32_bf16 a[40:43], v[248:251], a[172:175], a[40:43]\n\t"
"v_fma_f32 v170, v70, s54, -v170\n\t"
"v_fma_f32 v171, v71, s54, -v171\n\t"
"v_mfma_f32_16x16x32_bf16 a[40:43], a[208:211], a[176:179], a[40:43]\n\t"
"s_mov_b32 s20, 1277952\n\t"
"buffer_load_dwordx4 v[240:243], v252, s[8:11], s20 offen offset:0 sc1\n\t"
"v_mfma_f32_16x16x32_bf16 a[48:51], a[224:227], a[172:175], a[48:51]\n\t"
"v_fma_f32 v72, v172, s52, v72\n\t"
"v_fma_f32 v73, v173, s52, v73\n\t"
"v_mfma_f32_16x16x32_bf16 a[48:51], v[248:251], a[176:179], a[48:51]\n\t"
"v_fma_f32 v74, v174, s52, v74\n\t"
"v_fma_f32 v75, v175, s52, v75\n\t"
"v_mfma_f32_16x16x32_bf16 a[48:51], a[208:211], a[180:183], a[48:51]\n\t"
"buffer_load_dwordx4 a[252:255], v252, s[8:11], s20 offen offset:1024 sc1\n\t"
"v_fma_f32 v172, v72, s54, -v172\n\t"
"v_fma_f32 v173, v73, s54, -v173\n\t"
"v_mfma_f32_16x16x32_bf16 a[56:59], a[224:227], a[176:179], a[56:59]\n\t"
"v_fma_f32 v174, v74, s54, -v174\n\t"
"v_fma_f32 v175, v75, s54, -v175\n\t"
"v_mfma_f32_16x16x32_bf16 a[56:59], v[248:251], a[180:183], a[56:59]\n\t"
"v_fma_f32 v76, v176, s52, v76\n\t"
"v_fma_f32 v77, v177, s52, v77\n\t"
"v_mfma_f32_16x16x32_bf16 a[56:59], a[208:211], a[184:187], a[56:59]\n\t"
"v_fma_f32 v78, v178, s52, v78\n\t"
"v_fma_f32 v79, v179, s52, v79\n\t"
"v_mfma_f32_16x16x32_bf16 a[64:67], a[224:227], a[180:183], a[64:67]\n\t"
"v_fma_f32 v176, v76, s54, -v176\n\t"
"v_fma_f32 v177, v77, s54, -v177\n\t"
"v_mfma_f32_16x16x32_bf16 a[64:67], v[248:251], a[184:187], a[64:67]\n\t"
"v_fma_f32 v178, v78, s54, -v178\n\t"
"v_fma_f32 v179, v79, s54, -v179\n\t"
"v_mfma_f32_16x16x32_bf16 a[64:67], a[208:211], a[188:191], a[64:67]\n\t"
"v_fma_f32 v80, v180, s52, v80\n\t"
"v_fma_f32 v81, v181, s52, v81\n\t"
"v_mfma_f32_16x16x32_bf16 a[72:75], a[224:227], a[184:187], a[72:75]\n\t"
"v_fma_f32 v82, v182, s52, v82\n\t"
"v_fma_f32 v83, v183, s52, v83\n\t"
"v_mfma_f32_16x16x32_bf16 a[72:75], v[248:251], a[188:191], a[72:75]\n\t"
"v_fma_f32 v180, v80, s54, -v180\n\t"
"v_fma_f32 v181, v81, s54, -v181\n\t"
"v_mfma_f32_16x16x32_bf16 a[72:75], a[208:211], a[192:195], a[72:75]\n\t"
"v_fma_f32 v182, v82, s54, -v182\n\t"
"v_fma_f32 v183, v83, s54, -v183\n\t"
"v_mfma_f32_16x16x32_bf16 a[80:83], a[224:227], a[188:191], a[80:83]\n\t"
"v_fma_f32 v84, v184, s52, v84\n\t"
"v_fma_f32 v85, v185, s52, v85\n\t"
"v_mfma_f32_16x16x32_bf16 a[80:83], v[248:251], a[192:195], a[80:83]\n\t"
"v_fma_f32 v86, v186, s52, v86\n\t"
"v_mfma_f32_16x16x32_bf16 a[80:83], a[208:211], a[196:199], a[80:83]\n\t"
"v_fma_f32 v87, v187, s52, v87\n\t"
"v_mfma_f32_16x16x32_bf16 a[88:91], a[224:227], a[192:195], a[88:91]\n\t"
"v_fma_f32 v184, v84, s54, -v184\n\t"
"v_fma_f32 v185, v85, s54, -v185\n\t"
"v_mfma_f32_16x16x32_bf16 a[88:91], v[248:251], a[196:199], a[88:91]\n\t"
"v_fma_f32 v186, v86, s54, -v186\n\t"
"v_mfma_f32_16x16x32_bf16 a[88:91], a[208:211], a[200:203], a[88:91]\n\t"
"v_fma_f32 v187, v87, s54, -v187\n\t"
"v_mfma_f32_16x16x32_bf16 a[96:99], a[224:227], a[196:199], a[96:99]\n\t"
"v_fma_f32 v88, v188, s52, v88\n\t"
"v_fma_f32 v89, v189, s52, v89\n\t"
"v_mfma_f32_16x16x32_bf16 a[96:99], v[248:251], a[200:203], a[96:99]\n\t"
"v_fma_f32 v90, v190, s52, v90\n\t"
"v_mfma_f32_16x16x32_bf16 a[96:99], a[208:211], a[204:207], a[96:99]\n\t"
"buffer_load_dwordx4 a[224:227], v252, s[8:11], s20 offen offset:2048 sc1\n\t"
"s_waitcnt vmcnt(9)\n\t"
"v_mfma_f32_16x16x32_bf16 a[104:107], a[212:215], a[168:171], a[104:107]\n\t"
"ds_read_b128 a[168:171], v244 offset:9216\n\t"
"v_fma_f32 v91, v191, s52, v91\n\t"
"v_mfma_f32_16x16x32_bf16 a[104:107], a[228:231], a[172:175], a[104:107]\n\t"
"v_fma_f32 v188, v88, s54, -v188\n\t"
"v_mfma_f32_16x16x32_bf16 a[104:107], a[216:219], a[176:179], a[104:107]\n\t"
"s_mov_b32 s20, 442368\n\t"
"buffer_load_dwordx4 v[248:251], v252, s[8:11], s20 offen offset:0 sc1\n\t"
"v_mfma_f32_16x16x32_bf16 a[112:115], a[212:215], a[172:175], a[112:115]\n\t"
"ds_read_b128 a[172:175], v244 offset:9728\n\t"
"v_fma_f32 v189, v89, s54, -v189\n\t"
"v_mfma_f32_16x16x32_bf16 a[112:115], a[228:231], a[176:179], a[112:115]\n\t"
"v_fma_f32 v190, v90, s54, -v190\n\t"
"v_fma_f32 v191, v91, s54, -v191\n\t"
"v_mfma_f32_16x16x32_bf16 a[112:115], a[216:219], a[180:183], a[112:115]\n\t"
"v_fma_f32 v92, v192, s52, v92\n\t"
"v_mfma_f32_16x16x32_bf16 a[120:123], a[212:215], a[176:179], a[120:123]\n\t"
"ds_read_b128 a[176:179], v244 offset:10240\n\t"
"v_fma_f32 v93, v193, s52, v93\n\t"
"v_mfma_f32_16x16x32_bf16 a[120:123], a[228:231], a[180:183], a[120:123]\n\t"
"v_fma_f32 v94, v194, s52, v94\n\t"
"v_mfma_f32_16x16x32_bf16 a[120:123], a[216:219], a[184:187], a[120:123]\n\t"
"buffer_load_dwordx4 a[208:211], v252, s[8:11], s20 offen offset:1024 sc1\n\t"
"v_mfma_f32_16x16x32_bf16 a[128:131], a[212:215], a[180:183], a[128:131]\n\t"
"ds_read_b128 a[180:183], v244 offset:10752\n\t"
"v_mfma_f32_16x16x32_bf16 a[128:131], a[228:231], a[184:187], a[128:131]\n\t"
"v_fma_f32 v95, v195, s52, v95\n\t"
"v_mfma_f32_16x16x32_bf16 a[128:131], a[216:219], a[188:191], a[128:131]\n\t"
"v_fma_f32 v192, v92, s54, -v192\n\t"
"v_mfma_f32_16x16x32_bf16 a[136:139], a[212:215], a[184:187], a[136:139]\n\t"
"ds_read_b128 a[184:187], v244 offset:11264\n\t"
"v_mfma_f32_16x16x32_bf16 a[136:139], a[228:231], a[188:191], a[136:139]\n\t"
"v_mfma_f32_16x16x32_bf16 a[136:139], a[216:219], a[192:195], a[136:139]\n\t"
"v_fma_f32 v193, v93, s54, -v193\n\t"
"v_mfma_f32_16x16x32_bf16 a[144:147], a[212:215], a[188:191], a[144:147]\n\t"
"ds_read_b128 a[188:191], v244 offset:11776\n\t"
"v_mfma_f32_16x16x32_bf16 a[144:147], a[228:231], a[192:195], a[144:147]\n\t"
"v_fma_f32 v194, v94, s54, -v194\n\t"
"v_mfma_f32_16x16x32_bf16 a[144:147], a[216:219], a[196:199], a[144:147]\n\t"
"v_mfma_f32_16x16x32_bf16 a[152:155], a[212:215], a[192:195], a[152:155]\n\t"
"ds_read_b128 a[192:195], v244 offset:12288\n\t"
"v_mfma_f32_16x16x32_bf16 a[152:155], a[228:231], a[196:199], a[152:155]\n\t"
"v_fma_f32 v195, v95, s54, -v195\n\t"
"v_mfma_f32_16x16x32_bf16 a[152:155], a[216:219], a[200:203], a[152:155]\n\t"
"v_mfma_f32_16x16x32_bf16 a[160:163], a[212:215], a[196:199], a[160:163]\n\t"
"ds_read_b128 a[196:199], v244 offset:12800\n\t"
"v_mfma_f32_16x16x32_bf16 a[160:163], a[228:231], a[200:203], a[160:163]\n\t"
"ds_read_b128 a[200:203], v244 offset:13312\n\t"
"v_mfma_f32_16x16x32_bf16 a[160:163], a[216:219], a[204:207], a[160:163]\n\t"
"buffer_load_dwordx4 a[212:215], v252, s[8:11], s20 offen offset:2048 sc1\n\t"
"s_waitcnt vmcnt(9) lgkmcnt(6)\n\t"
"v_mfma_f32_16x16x32_bf16 v[200:203], a[232:235], a[168:171], v[200:203]\n\t"
"ds_read_b128 a[204:207], v244 offset:13824\n\t"
"v_mfma_f32_16x16x32_bf16 v[200:203], a[236:239], a[172:175], v[200:203]\n\t"
"v_mfma_f32_16x16x32_bf16 v[200:203], a[240:243], a[176:179], v[200:203]\n\t"
"s_mov_b32 s20, 454656\n\t"
"buffer_load_dwordx4 a[216:219], v252, s[8:11], s20 offen offset:0 sc1\n\t"
"s_waitcnt lgkmcnt(6)\n\t"
"v_mfma_f32_16x16x32_bf16 v[208:211], a[232:235], a[172:175], v[208:211]\n\t"
"v_mfma_f32_16x16x32_bf16 v[208:211], a[236:239], a[176:179], v[208:211]\n\t"
"v_mfma_f32_16x16x32_bf16 v[208:211], a[240:243], a[180:183], v[208:211]\n\t"
"buffer_load_dwordx4 a[228:231], v252, s[8:11], s20 offen offset:1024 sc1\n\t"
"s_waitcnt lgkmcnt(0)\n\t"
"s_barrier\n\t"
"v_mfma_f32_16x16x32_bf16 v[216:219], a[232:235], a[176:179], v[216:219]\n\t"
"v_mfma_f32_16x16x32_bf16 v[216:219], a[236:239], a[180:183], v[216:219]\n\t"
"v_mfma_f32_16x16x32_bf16 v[216:219], a[240:243], a[184:187], v[216:219]\n\t"
"v_pk_fma_f32 v[196:197], v[200:201], s[56:57], v[196:197]\n\t"
"v_pk_fma_f32 v[200:201], v[196:197], s[58:59], v[200:201] neg_lo:[0,0,1] neg_hi:[0,0,1]\n\t"
"v_pk_fma_f32 v[228:229], v[0:1], s[60:61], v[196:197]\n\t"
"v_pk_fma_f32 v[0:1], v[196:197], s[60:61], v[0:1]\n\t"
"v_pk_fma_f32 v[196:197], v[100:101], s[62:63], v[200:201]\n\t"
"v_pk_fma_f32 v[100:101], v[200:201], s[62:63], v[100:101]\n\t"
"v_pk_fma_f32 v[198:199], v[202:203], s[56:57], v[198:199]\n\t"
"v_pk_fma_f32 v[202:203], v[198:199], s[58:59], v[202:203] neg_lo:[0,0,1] neg_hi:[0,0,1]\n\t"
"v_pk_fma_f32 v[234:235], v[2:3], s[60:61], v[198:199]\n\t"
"v_pk_fma_f32 v[2:3], v[198:199], s[60:61], v[2:3]\n\t"
"v_pk_fma_f32 v[198:199], v[102:103], s[62:63], v[202:203]\n\t"
"v_pk_fma_f32 v[102:103], v[202:203], s[62:63], v[102:103]\n\t"
"v_cvt_pk_bf16_f32 v196, v196, v197\n\t"
"v_cvt_pk_bf16_f32 v200, v228, v229\n\t"
"v_mfma_f32_16x16x32_bf16 a[4:7], a[232:235], a[180:183], a[4:7]\n\t"
"v_cvt_pk_bf16_f32 v197, v198, v199\n\t"
"v_mfma_f32_16x16x32_bf16 a[4:7], a[236:239], a[184:187], a[4:7]\n\t"
"v_cvt_pk_bf16_f32 v201, v234, v235\n\t"
"v_mfma_f32_16x16x32_bf16 a[4:7], a[240:243], a[188:191], a[4:7]\n\t"
"v_pk_fma_f32 v[204:205], v[208:209], s[56:57], v[204:205]\n\t"
"v_pk_fma_f32 v[208:209], v[204:205], s[58:59], v[208:209] neg_lo:[0,0,1] neg_hi:[0,0,1]\n\t"
"v_pk_fma_f32 v[98:99], v[4:5], s[60:61], v[204:205]\n\t"
"v_pk_fma_f32 v[4:5], v[204:205], s[60:61], v[4:5]\n\t"
"v_pk_fma_f32 v[204:205], v[104:105], s[62:63], v[208:209]\n\t"
"v_pk_fma_f32 v[104:105], v[208:209], s[62:63], v[104:105]\n\t"
"v_cvt_pk_bf16_f32 v204, v204, v205\n\t"
"v_mfma_f32_16x16x32_bf16 a[12:15], a[232:235], a[184:187], a[12:15]\n\t"
"v_cvt_pk_bf16_f32 v208, v98, v99\n\t"
"v_mfma_f32_16x16x32_bf16 a[12:15], a[236:239], a[188:191], a[12:15]\n\t"
"v_mfma_f32_16x16x32_bf16 a[12:15], a[240:243], a[192:195], a[12:15]\n\t"
"v_pk_fma_f32 v[206:207], v[210:211], s[56:57], v[206:207]\n\t"
"v_pk_fma_f32 v[210:211], v[206:207], s[58:59], v[210:211] neg_lo:[0,0,1] neg_hi:[0,0,1]\n\t"
"v_pk_fma_f32 v[228:229], v[6:7], s[60:61], v[206:207]\n\t"
"v_pk_fma_f32 v[6:7], v[206:207], s[60:61], v[6:7]\n\t"
"v_pk_fma_f32 v[206:207], v[106:107], s[62:63], v[210:211]\n\t"
"v_pk_fma_f32 v[106:107], v[210:211], s[62:63], v[106:107]\n\t"
"v_cvt_pk_bf16_f32 v205, v206, v207\n\t"
"v_cvt_pk_bf16_f32 v209, v228, v229\n\t"
"v_mfma_f32_16x16x32_bf16 a[20:23], a[232:235], a[188:191], a[20:23]\n\t"
"v_mfma_f32_16x16x32_bf16 a[20:23], a[236:239], a[192:195], a[20:23]\n\t"
"v_accvgpr_read_b32 v224, a0\n\t"
"v_mfma_f32_16x16x32_bf16 a[20:23], a[240:243], a[196:199], a[20:23]\n\t"
"v_pk_fma_f32 v[212:213], v[216:217], s[56:57], v[212:213]\n\t"
"v_pk_fma_f32 v[216:217], v[212:213], s[58:59], v[216:217] neg_lo:[0,0,1] neg_hi:[0,0,1]\n\t"
"v_pk_fma_f32 v[234:235], v[8:9], s[60:61], v[212:213]\n\t"
"v_pk_fma_f32 v[8:9], v[212:213], s[60:61], v[8:9]\n\t"
"v_pk_fma_f32 v[212:213], v[108:109], s[62:63], v[216:217]\n\t"
"v_pk_fma_f32 v[108:109], v[216:217], s[62:63], v[108:109]\n\t"
"v_cvt_pk_bf16_f32 v212, v212, v213\n\t"
"v_cvt_pk_bf16_f32 v216, v234, v235\n\t"
"v_mfma_f32_16x16x32_bf16 a[28:31], a[232:235], a[192:195], a[28:31]\n\t"
"v_accvgpr_read_b32 v226, a4\n\t"
"v_mfma_f32_16x16x32_bf16 a[28:31], a[236:239], a[196:199], a[28:31]\n\t"
"v_accvgpr_read_b32 v225, a1\n\t"
"v_accvgpr_read_b32 v227, a5\n\t"
"v_mfma_f32_16x16x32_bf16 a[28:31], a[240:243], a[200:203], a[28:31]\n\t"
"v_pk_fma_f32 v[214:215], v[218:219], s[56:57], v[214:215]\n\t"
"v_pk_fma_f32 v[218:219], v[214:215], s[58:59], v[218:219] neg_lo:[0,0,1] neg_hi:[0,0,1]\n\t"
"v_pk_fma_f32 v[98:99], v[10:11], s[60:61], v[214:215]\n\t"
"v_pk_fma_f32 v[10:11], v[214:215], s[60:61], v[10:11]\n\t"
"v_pk_fma_f32 v[214:215], v[110:111], s[62:63], v[218:219]\n\t"
"v_pk_fma_f32 v[110:111], v[218:219], s[62:63], v[110:111]\n\t"
"v_cvt_pk_bf16_f32 v213, v214, v215\n\t"
"v_cvt_pk_bf16_f32 v217, v98, v99\n\t"
"v_mfma_f32_16x16x32_bf16 a[36:39], a[232:235], a[196:199], a[36:39]\n\t"
"v_accvgpr_read_b32 v230, a2\n\t"
"v_accvgpr_read_b32 v232, a6\n\t"
"v_mfma_f32_16x16x32_bf16 a[36:39], a[236:239], a[200:203], a[36:39]\n\t"
"v_accvgpr_read_b32 v231, a3\n\t"
"v_accvgpr_read_b32 v233, a7\n\t"
"v_mfma_f32_16x16x32_bf16 a[36:39], a[240:243], a[204:207], a[36:39]\n\t"
"v_pk_fma_f32 v[224:225], v[226:227], s[56:57], v[224:225]\n\t"
"v_pk_fma_f32 v[226:227], v[224:225], s[58:59], v[226:227] neg_lo:[0,0,1] neg_hi:[0,0,1]\n\t"
"v_pk_fma_f32 v[228:229], v[12:13], s[60:61], v[224:225]\n\t"
"v_pk_fma_f32 v[12:13], v[224:225], s[60:61], v[12:13]\n\t"
"v_pk_fma_f32 v[224:225], v[112:113], s[62:63], v[226:227]\n\t"
"v_pk_fma_f32 v[112:113], v[226:227], s[62:63], v[112:113]\n\t"
"buffer_load_dwordx4 a[232:235], v252, s[8:11], s20 offen offset:2048 sc1\n\t"
"s_waitcnt vmcnt(9)\n\t"
"v_mfma_f32_16x16x32_bf16 a[44:47], a[244:247], a[168:171], a[44:47]\n\t"
"v_cvt_pk_bf16_f32 v224, v224, v225\n\t"
"v_cvt_pk_bf16_f32 v228, v228, v229\n\t"
"v_mfma_f32_16x16x32_bf16 a[44:47], a[220:223], a[172:175], a[44:47]\n\t"
"v_accvgpr_write_b32 a0, v224\n\t"
"v_accvgpr_write_b32 a4, v228\n\t"
"v_mfma_f32_16x16x32_bf16 a[44:47], a[248:251], a[176:179], a[44:47]\n\t"
"v_pk_fma_f32 v[230:231], v[232:233], s[56:57], v[230:231]\n\t"
"v_pk_fma_f32 v[232:233], v[230:231], s[58:59], v[232:233] neg_lo:[0,0,1] neg_hi:[0,0,1]\n\t"
"v_pk_fma_f32 v[234:235], v[14:15], s[60:61], v[230:231]\n\t"
"v_pk_fma_f32 v[14:15], v[230:231], s[60:61], v[14:15]\n\t"
"v_pk_fma_f32 v[230:231], v[114:115], s[62:63], v[232:233]\n\t"
"v_pk_fma_f32 v[114:115], v[232:233], s[62:63], v[114:115]\n\t"
"s_mov_b32 s20, 466944\n\t"
"buffer_load_dwordx4 a[236:239], v252, s[8:11], s20 offen offset:0 sc1\n\t"
"v_accvgpr_read_b32 v236, a8\n\t"
"v_accvgpr_read_b32 v238, a12\n\t"
"v_mfma_f32_16x16x32_bf16 a[52:55], a[244:247], a[172:175], a[52:55]\n\t"
"v_accvgpr_read_b32 v237, a9\n\t"
"v_accvgpr_read_b32 v239, a13\n\t"
"v_mfma_f32_16x16x32_bf16 a[52:55], a[220:223], a[176:179], a[52:55]\n\t"
"v_cvt_pk_bf16_f32 v230, v230, v231\n\t"
"v_cvt_pk_bf16_f32 v234, v234, v235\n\t"
"v_mfma_f32_16x16x32_bf16 a[52:55], a[248:251], a[180:183], a[52:55]\n\t"
"v_pk_fma_f32 v[236:237], v[238:239], s[56:57], v[236:237]\n\t"
"v_pk_fma_f32 v[238:239], v[236:237], s[58:59], v[238:239] neg_lo:[0,0,1] neg_hi:[0,0,1]\n\t"
"v_pk_fma_f32 v[98:99], v[16:17], s[60:61], v[236:237]\n\t"
"v_pk_fma_f32 v[16:17], v[236:237], s[60:61], v[16:17]\n\t"
"v_pk_fma_f32 v[236:237], v[116:117], s[62:63], v[238:239]\n\t"
"v_pk_fma_f32 v[116:117], v[238:239], s[62:63], v[116:117]\n\t"
"buffer_load_dwordx4 a[240:243], v252, s[8:11], s20 offen offset:1024 sc1\n\t"
"v_accvgpr_write_b32 a1, v230\n\t"
"v_accvgpr_write_b32 a5, v234\n\t"
"v_mfma_f32_16x16x32_bf16 a[60:63], a[244:247], a[176:179], a[60:63]\n\t"
"v_accvgpr_read_b32 v224, a10\n\t"
"v_accvgpr_read_b32 v226, a14\n\t"
"v_mfma_f32_16x16x32_bf16 a[60:63], a[220:223], a[180:183], a[60:63]\n\t"
"v_accvgpr_read_b32 v225, a11\n\t"
"v_accvgpr_read_b32 v227, a15\n\t"
"v_mfma_f32_16x16x32_bf16 a[60:63], a[248:251], a[184:187], a[60:63]\n\t"
"v_accvgpr_read_b32 v230, a16\n\t"
"v_accvgpr_read_b32 v232, a20\n\t"
"v_accvgpr_read_b32 v231, a17\n\t"
"v_accvgpr_read_b32 v233, a21\n\t"
"v_cvt_pk_bf16_f32 v236, v236, v237\n\t"
"v_cvt_pk_bf16_f32 v98, v98, v99\n\t"
"v_pk_fma_f32 v[224:225], v[226:227], s[56:57], v[224:225]\n\t"
"v_pk_fma_f32 v[226:227], v[224:225], s[58:59], v[226:227] neg_lo:[0,0,1] neg_hi:[0,0,1]\n\t"
"v_pk_fma_f32 v[228:229], v[18:19], s[60:61], v[224:225]\n\t"
"v_pk_fma_f32 v[18:19], v[224:225], s[60:61], v[18:19]\n\t"
"v_pk_fma_f32 v[224:225], v[118:119], s[62:63], v[226:227]\n\t"
"v_pk_fma_f32 v[118:119], v[226:227], s[62:63], v[118:119]\n\t"
"v_pk_fma_f32 v[230:231], v[232:233], s[56:57], v[230:231]\n\t"
"v_pk_fma_f32 v[232:233], v[230:231], s[58:59], v[232:233] neg_lo:[0,0,1] neg_hi:[0,0,1]\n\t"
"v_pk_fma_f32 v[234:235], v[20:21], s[60:61], v[230:231]\n\t"
"v_pk_fma_f32 v[20:21], v[230:231], s[60:61], v[20:21]\n\t"
"v_pk_fma_f32 v[230:231], v[120:121], s[62:63], v[232:233]\n\t"
"v_pk_fma_f32 v[120:121], v[232:233], s[62:63], v[120:121]\n\t"
"v_mfma_f32_16x16x32_bf16 a[68:71], a[244:247], a[180:183], a[68:71]\n\t"
"v_accvgpr_write_b32 a8, v236\n\t"
"v_accvgpr_write_b32 a12, v98\n\t"
"v_mfma_f32_16x16x32_bf16 a[68:71], a[220:223], a[184:187], a[68:71]\n\t"
"v_accvgpr_read_b32 v236, a18\n\t"
"v_accvgpr_read_b32 v238, a22\n\t"
"v_mfma_f32_16x16x32_bf16 a[68:71], a[248:251], a[188:191], a[68:71]\n\t"
"v_accvgpr_read_b32 v237, a19\n\t"
"v_accvgpr_read_b32 v239, a23\n\t"
"v_cvt_pk_bf16_f32 v224, v224, v225\n\t"
"v_cvt_pk_bf16_f32 v228, v228, v229\n\t"
"v_cvt_pk_bf16_f32 v230, v230, v231\n\t"
"v_cvt_pk_bf16_f32 v234, v234, v235\n\t"
"v_pk_fma_f32 v[236:237], v[238:239], s[56:57], v[236:237]\n\t"
"v_pk_fma_f32 v[238:239], v[236:237], s[58:59], v[238:239] neg_lo:[0,0,1] neg_hi:[0,0,1]\n\t"
"v_pk_fma_f32 v[98:99], v[22:23], s[60:61], v[236:237]\n\t"
"v_pk_fma_f32 v[22:23], v[236:237], s[60:61], v[22:23]\n\t"
"v_pk_fma_f32 v[236:237], v[122:123], s[62:63], v[238:239]\n\t"
"v_pk_fma_f32 v[122:123], v[238:239], s[62:63], v[122:123]\n\t"
"v_mfma_f32_16x16x32_bf16 a[76:79], a[244:247], a[184:187], a[76:79]\n\t"
"v_accvgpr_write_b32 a9, v224\n\t"
"v_accvgpr_write_b32 a13, v228\n\t"
"v_mfma_f32_16x16x32_bf16 a[76:79], a[220:223], a[188:191], a[76:79]\n\t"
"v_accvgpr_write_b32 a16, v230\n\t"
"v_accvgpr_write_b32 a20, v234\n\t"
"v_mfma_f32_16x16x32_bf16 a[76:79], a[248:251], a[192:195], a[76:79]\n\t"
"v_accvgpr_read_b32 v224, a24\n\t"
"v_accvgpr_read_b32 v226, a28\n\t"
"v_accvgpr_read_b32 v225, a25\n\t"
"v_accvgpr_read_b32 v227, a29\n\t"
"v_cvt_pk_bf16_f32 v236, v236, v237\n\t"
"v_cvt_pk_bf16_f32 v98, v98, v99\n\t"
"v_pk_fma_f32 v[224:225], v[226:227], s[56:57], v[224:225]\n\t"
"v_pk_fma_f32 v[226:227], v[224:225], s[58:59], v[226:227] neg_lo:[0,0,1] neg_hi:[0,0,1]\n\t"
"v_pk_fma_f32 v[228:229], v[24:25], s[60:61], v[224:225]\n\t"
"v_pk_fma_f32 v[24:25], v[224:225], s[60:61], v[24:25]\n\t"
"v_pk_fma_f32 v[224:225], v[124:125], s[62:63], v[226:227]\n\t"
"v_pk_fma_f32 v[124:125], v[226:227], s[62:63], v[124:125]\n\t"
"v_mfma_f32_16x16x32_bf16 a[84:87], a[244:247], a[188:191], a[84:87]\n\t"
"v_accvgpr_write_b32 a17, v236\n\t"
"v_accvgpr_write_b32 a21, v98\n\t"
"v_mfma_f32_16x16x32_bf16 a[84:87], a[220:223], a[192:195], a[84:87]\n\t"
"v_accvgpr_read_b32 v230, a26\n\t"
"v_accvgpr_read_b32 v232, a30\n\t"
"v_mfma_f32_16x16x32_bf16 a[84:87], a[248:251], a[196:199], a[84:87]\n\t"
"v_accvgpr_read_b32 v231, a27\n\t"
"v_accvgpr_read_b32 v233, a31\n\t"
"v_cvt_pk_bf16_f32 v224, v224, v225\n\t"
"v_cvt_pk_bf16_f32 v228, v228, v229\n\t"
"v_pk_fma_f32 v[230:231], v[232:233], s[56:57], v[230:231]\n\t"
"v_pk_fma_f32 v[232:233], v[230:231], s[58:59], v[232:233] neg_lo:[0,0,1] neg_hi:[0,0,1]\n\t"
"v_pk_fma_f32 v[234:235], v[26:27], s[60:61], v[230:231]\n\t"
"v_pk_fma_f32 v[26:27], v[230:231], s[60:61], v[26:27]\n\t"
"v_pk_fma_f32 v[230:231], v[126:127], s[62:63], v[232:233]\n\t"
"v_pk_fma_f32 v[126:127], v[232:233], s[62:63], v[126:127]\n\t"
"v_mfma_f32_16x16x32_bf16 a[92:95], a[244:247], a[192:195], a[92:95]\n\t"
"v_accvgpr_write_b32 a24, v224\n\t"
"v_accvgpr_write_b32 a28, v228\n\t"
"v_mfma_f32_16x16x32_bf16 a[92:95], a[220:223], a[196:199], a[92:95]\n\t"
"v_accvgpr_read_b32 v236, a32\n\t"
"v_accvgpr_read_b32 v238, a36\n\t"
"v_mfma_f32_16x16x32_bf16 a[92:95], a[248:251], a[200:203], a[92:95]\n\t"
"v_accvgpr_read_b32 v237, a33\n\t"
"v_accvgpr_read_b32 v239, a37\n\t"
"v_cvt_pk_bf16_f32 v230, v230, v231\n\t"
"v_cvt_pk_bf16_f32 v234, v234, v235\n\t"
"v_pk_fma_f32 v[236:237], v[238:239], s[56:57], v[236:237]\n\t"
"v_pk_fma_f32 v[238:239], v[236:237], s[58:59], v[238:239] neg_lo:[0,0,1] neg_hi:[0,0,1]\n\t"
"v_pk_fma_f32 v[98:99], v[28:29], s[60:61], v[236:237]\n\t"
"v_pk_fma_f32 v[28:29], v[236:237], s[60:61], v[28:29]\n\t"
"v_pk_fma_f32 v[236:237], v[128:129], s[62:63], v[238:239]\n\t"
"v_pk_fma_f32 v[128:129], v[238:239], s[62:63], v[128:129]\n\t"
"v_mfma_f32_16x16x32_bf16 a[100:103], a[244:247], a[196:199], a[100:103]\n\t"
"v_accvgpr_write_b32 a25, v230\n\t"
"v_accvgpr_write_b32 a29, v234\n\t"
"v_mfma_f32_16x16x32_bf16 a[100:103], a[220:223], a[200:203], a[100:103]\n\t"
"v_accvgpr_read_b32 v224, a34\n\t"
"v_accvgpr_read_b32 v226, a38\n\t"
"v_mfma_f32_16x16x32_bf16 a[100:103], a[248:251], a[204:207], a[100:103]\n\t"
"v_accvgpr_read_b32 v225, a35\n\t"
"v_accvgpr_read_b32 v227, a39\n\t"
"v_cvt_pk_bf16_f32 v236, v236, v237\n\t"
"v_cvt_pk_bf16_f32 v98, v98, v99\n\t"
"v_pk_fma_f32 v[224:225], v[226:227], s[56:57], v[224:225]\n\t"
"v_pk_fma_f32 v[226:227], v[224:225], s[58:59], v[226:227] neg_lo:[0,0,1] neg_hi:[0,0,1]\n\t"
"v_pk_fma_f32 v[228:229], v[30:31], s[60:61], v[224:225]\n\t"
"v_pk_fma_f32 v[30:31], v[224:225], s[60:61], v[30:31]\n\t"
"v_pk_fma_f32 v[224:225], v[130:131], s[62:63], v[226:227]\n\t"
"v_pk_fma_f32 v[130:131], v[226:227], s[62:63], v[130:131]\n\t"
"s_waitcnt vmcnt(8)\n\t"
"v_mfma_f32_16x16x32_bf16 a[108:111], v[240:243], a[168:171], a[108:111]\n\t"
"v_accvgpr_write_b32 a32, v236\n\t"
"v_accvgpr_write_b32 a36, v98\n\t"
"v_mfma_f32_16x16x32_bf16 a[108:111], a[252:255], a[172:175], a[108:111]\n\t"
"v_accvgpr_read_b32 v230, a40\n\t"
"v_accvgpr_read_b32 v232, a44\n\t"
"v_mfma_f32_16x16x32_bf16 a[108:111], a[224:227], a[176:179], a[108:111]\n\t"
"buffer_load_dwordx4 a[244:247], v252, s[8:11], s20 offen offset:2048 sc1\n\t"
"v_accvgpr_read_b32 v231, a41\n\t"
"v_accvgpr_read_b32 v233, a45\n\t"
"v_cvt_pk_bf16_f32 v224, v224, v225\n\t"
"v_cvt_pk_bf16_f32 v228, v228, v229\n\t"
"v_pk_fma_f32 v[230:231], v[232:233], s[56:57], v[230:231]\n\t"
"v_pk_fma_f32 v[232:233], v[230:231], s[58:59], v[232:233] neg_lo:[0,0,1] neg_hi:[0,0,1]\n\t"
"v_pk_fma_f32 v[234:235], v[32:33], s[60:61], v[230:231]\n\t"
"v_pk_fma_f32 v[32:33], v[230:231], s[60:61], v[32:33]\n\t"
"v_pk_fma_f32 v[230:231], v[132:133], s[62:63], v[232:233]\n\t"
"v_pk_fma_f32 v[132:133], v[232:233], s[62:63], v[132:133]\n\t"
"v_mfma_f32_16x16x32_bf16 a[116:119], v[240:243], a[172:175], a[116:119]\n\t"
"v_accvgpr_write_b32 a33, v224\n\t"
"v_accvgpr_write_b32 a37, v228\n\t"
"v_mfma_f32_16x16x32_bf16 a[116:119], a[252:255], a[176:179], a[116:119]\n\t"
"v_accvgpr_read_b32 v236, a42\n\t"
"v_accvgpr_read_b32 v238, a46\n\t"
"v_mfma_f32_16x16x32_bf16 a[116:119], a[224:227], a[180:183], a[116:119]\n\t"
"s_mov_b32 s20, 626688\n\t"
"buffer_load_dwordx4 a[220:223], v252, s[8:11], s20 offen offset:0 sc1\n\t"
"v_accvgpr_read_b32 v237, a43\n\t"
"v_accvgpr_read_b32 v239, a47\n\t"
"v_pk_fma_f32 v[236:237], v[238:239], s[56:57], v[236:237]\n\t"
"v_pk_fma_f32 v[238:239], v[236:237], s[58:59], v[238:239] neg_lo:[0,0,1] neg_hi:[0,0,1]\n\t"
"v_pk_fma_f32 v[98:99], v[34:35], s[60:61], v[236:237]\n\t"
"v_pk_fma_f32 v[34:35], v[236:237], s[60:61], v[34:35]\n\t"
"v_pk_fma_f32 v[236:237], v[134:135], s[62:63], v[238:239]\n\t"
"v_pk_fma_f32 v[134:135], v[238:239], s[62:63], v[134:135]\n\t"
"v_mfma_f32_16x16x32_bf16 a[124:127], v[240:243], a[176:179], a[124:127]\n\t"
"v_cvt_pk_bf16_f32 v198, v230, v231\n\t"
"v_cvt_pk_bf16_f32 v202, v234, v235\n\t"
"v_mfma_f32_16x16x32_bf16 a[124:127], a[252:255], a[180:183], a[124:127]\n\t"
"v_accvgpr_read_b32 v224, a48\n\t"
"v_accvgpr_read_b32 v226, a52\n\t"
"v_mfma_f32_16x16x32_bf16 a[124:127], a[224:227], a[184:187], a[124:127]\n\t"
"buffer_load_dwordx4 a[248:251], v252, s[8:11], s20 offen offset:1024 sc1\n\t"
"v_accvgpr_read_b32 v225, a49\n\t"
"v_accvgpr_read_b32 v227, a53\n\t"
"v_accvgpr_read_b32 v230, a50\n\t"
"v_accvgpr_read_b32 v232, a54\n\t"
"v_accvgpr_read_b32 v231, a51\n\t"
"v_accvgpr_read_b32 v233, a55\n\t"
"v_pk_fma_f32 v[224:225], v[226:227], s[56:57], v[224:225]\n\t"
"v_pk_fma_f32 v[226:227], v[224:225], s[58:59], v[226:227] neg_lo:[0,0,1] neg_hi:[0,0,1]\n\t"
"v_pk_fma_f32 v[228:229], v[36:37], s[60:61], v[224:225]\n\t"
"v_pk_fma_f32 v[36:37], v[224:225], s[60:61], v[36:37]\n\t"
"v_pk_fma_f32 v[224:225], v[136:137], s[62:63], v[226:227]\n\t"
"v_pk_fma_f32 v[136:137], v[226:227], s[62:63], v[136:137]\n\t"
"v_pk_fma_f32 v[230:231], v[232:233], s[56:57], v[230:231]\n\t"
"v_pk_fma_f32 v[232:233], v[230:231], s[58:59], v[232:233] neg_lo:[0,0,1] neg_hi:[0,0,1]\n\t"
"v_pk_fma_f32 v[234:235], v[38:39], s[60:61], v[230:231]\n\t"
"v_pk_fma_f32 v[38:39], v[230:231], s[60:61], v[38:39]\n\t"
"v_pk_fma_f32 v[230:231], v[138:139], s[62:63], v[232:233]\n\t"
"v_pk_fma_f32 v[138:139], v[232:233], s[62:63], v[138:139]\n\t"
"v_mfma_f32_16x16x32_bf16 a[132:135], v[240:243], a[180:183], a[132:135]\n\t"
"v_cvt_pk_bf16_f32 v199, v236, v237\n\t"
"v_cvt_pk_bf16_f32 v203, v98, v99\n\t"
"v_mfma_f32_16x16x32_bf16 a[132:135], a[252:255], a[184:187], a[132:135]\n\t"
"v_accvgpr_read_b32 v236, a56\n\t"
"v_accvgpr_read_b32 v238, a60\n\t"
"v_mfma_f32_16x16x32_bf16 a[132:135], a[224:227], a[188:191], a[132:135]\n\t"
"v_accvgpr_read_b32 v237, a57\n\t"
"v_accvgpr_read_b32 v239, a61\n\t"
"ds_read_b128 a[168:171], v244 offset:18432\n\t"
"ds_read_b128 a[172:175], v244 offset:18944\n\t"
"ds_read_b128 a[176:179], v244 offset:19456\n\t"
"buffer_load_dwordx4 a[40:43], v252, s[8:11], s20 offen offset:2048 sc1\n\t"
"v_pk_fma_f32 v[236:237], v[238:239], s[56:57], v[236:237]\n\t"
"v_pk_fma_f32 v[238:239], v[236:237], s[58:59], v[238:239] neg_lo:[0,0,1] neg_hi:[0,0,1]\n\t"
"v_pk_fma_f32 v[98:99], v[40:41], s[60:61], v[236:237]\n\t"
"v_pk_fma_f32 v[40:41], v[236:237], s[60:61], v[40:41]\n\t"
"v_pk_fma_f32 v[236:237], v[140:141], s[62:63], v[238:239]\n\t"
"v_pk_fma_f32 v[140:141], v[238:239], s[62:63], v[140:141]\n\t"
"v_mfma_f32_16x16x32_bf16 a[140:143], v[240:243], a[184:187], a[140:143]\n\t"
"v_cvt_pk_bf16_f32 v206, v224, v225\n\t"
"v_cvt_pk_bf16_f32 v210, v228, v229\n\t"
"v_mfma_f32_16x16x32_bf16 a[140:143], a[252:255], a[188:191], a[140:143]\n\t"
"v_cvt_pk_bf16_f32 v207, v230, v231\n\t"
"v_cvt_pk_bf16_f32 v211, v234, v235\n\t"
"v_mfma_f32_16x16x32_bf16 a[140:143], a[224:227], a[192:195], a[140:143]\n\t"
"v_accvgpr_read_b32 v224, a58\n\t"
"v_accvgpr_read_b32 v226, a62\n\t"
"v_accvgpr_read_b32 v225, a59\n\t"
"v_accvgpr_read_b32 v227, a63\n\t"
"ds_read_b128 a[180:183], v244 offset:19968\n\t"
"s_mov_b32 s20, 638976\n\t"
"buffer_load_dwordx4 a[44:47], v252, s[8:11], s20 offen offset:0 sc1\n\t"
"v_pk_fma_f32 v[224:225], v[226:227], s[56:57], v[224:225]\n\t"
"v_pk_fma_f32 v[226:227], v[224:225], s[58:59], v[226:227] neg_lo:[0,0,1] neg_hi:[0,0,1]\n\t"
"v_pk_fma_f32 v[228:229], v[42:43], s[60:61], v[224:225]\n\t"
"v_pk_fma_f32 v[42:43], v[224:225], s[60:61], v[42:43]\n\t"
"v_pk_fma_f32 v[224:225], v[142:143], s[62:63], v[226:227]\n\t"
"v_pk_fma_f32 v[142:143], v[226:227], s[62:63], v[142:143]\n\t"
"v_mfma_f32_16x16x32_bf16 a[148:151], v[240:243], a[188:191], a[148:151]\n\t"
"v_cvt_pk_bf16_f32 v214, v236, v237\n\t"
"v_cvt_pk_bf16_f32 v218, v98, v99\n\t"
"v_mfma_f32_16x16x32_bf16 a[148:151], a[252:255], a[192:195], a[148:151]\n\t"
"v_accvgpr_read_b32 v230, a64\n\t"
"v_accvgpr_read_b32 v232, a68\n\t"
"v_mfma_f32_16x16x32_bf16 a[148:151], a[224:227], a[196:199], a[148:151]\n\t"
"v_accvgpr_read_b32 v231, a65\n\t"
"v_accvgpr_read_b32 v233, a69\n\t"
"ds_read_b128 a[184:187], v244 offset:20480\n\t"
"buffer_load_dwordx4 a[48:51], v252, s[8:11], s20 offen offset:1024 sc1\n\t"
"v_pk_fma_f32 v[230:231], v[232:233], s[56:57], v[230:231]\n\t"
"v_pk_fma_f32 v[232:233], v[230:231], s[58:59], v[232:233] neg_lo:[0,0,1] neg_hi:[0,0,1]\n\t"
"v_pk_fma_f32 v[234:235], v[44:45], s[60:61], v[230:231]\n\t"
"v_pk_fma_f32 v[44:45], v[230:231], s[60:61], v[44:45]\n\t"
"v_pk_fma_f32 v[230:231], v[144:145], s[62:63], v[232:233]\n\t"
"v_pk_fma_f32 v[144:145], v[232:233], s[62:63], v[144:145]\n\t"
"v_mfma_f32_16x16x32_bf16 a[156:159], v[240:243], a[192:195], a[156:159]\n\t"
"v_cvt_pk_bf16_f32 v215, v224, v225\n\t"
"v_cvt_pk_bf16_f32 v219, v228, v229\n\t"
"v_mfma_f32_16x16x32_bf16 a[156:159], a[252:255], a[196:199], a[156:159]\n\t"
"v_accvgpr_read_b32 v236, a66\n\t"
"v_accvgpr_read_b32 v238, a70\n\t"
"v_mfma_f32_16x16x32_bf16 a[156:159], a[224:227], a[200:203], a[156:159]\n\t"
"v_accvgpr_read_b32 v237, a67\n\t"
"v_accvgpr_read_b32 v239, a71\n\t"
"ds_read_b128 a[188:191], v244 offset:20992\n\t"
"buffer_load_dwordx4 a[56:59], v252, s[8:11], s20 offen offset:2048 sc1\n\t"
"v_cvt_pk_bf16_f32 v230, v230, v231\n\t"
"v_cvt_pk_bf16_f32 v234, v234, v235\n\t"
"v_pk_fma_f32 v[236:237], v[238:239], s[56:57], v[236:237]\n\t"
"v_pk_fma_f32 v[238:239], v[236:237], s[58:59], v[238:239] neg_lo:[0,0,1] neg_hi:[0,0,1]\n\t"
"v_pk_fma_f32 v[98:99], v[46:47], s[60:61], v[236:237]\n\t"
"v_pk_fma_f32 v[46:47], v[236:237], s[60:61], v[46:47]\n\t"
"v_pk_fma_f32 v[236:237], v[146:147], s[62:63], v[238:239]\n\t"
"v_pk_fma_f32 v[146:147], v[238:239], s[62:63], v[146:147]\n\t"
"v_mfma_f32_16x16x32_bf16 a[164:167], v[240:243], a[196:199], a[164:167]\n\t"
"v_accvgpr_write_b32 a2, v230\n\t"
"v_accvgpr_write_b32 a6, v234\n\t"
"v_mfma_f32_16x16x32_bf16 a[164:167], a[252:255], a[200:203], a[164:167]\n\t"
"v_accvgpr_read_b32 v224, a72\n\t"
"v_accvgpr_read_b32 v226, a76\n\t"
"v_mfma_f32_16x16x32_bf16 a[164:167], a[224:227], a[204:207], a[164:167]\n\t"
"v_accvgpr_read_b32 v225, a73\n\t"
"v_accvgpr_read_b32 v227, a77\n\t"
"ds_read_b128 a[192:195], v244 offset:21504\n\t"
"s_mov_b32 s20, 651264\n\t"
"buffer_load_dwordx4 a[60:63], v252, s[8:11], s20 offen offset:0 sc1\n\t"
"v_cvt_pk_bf16_f32 v236, v236, v237\n\t"
"v_cvt_pk_bf16_f32 v98, v98, v99\n\t"
"v_pk_fma_f32 v[224:225], v[226:227], s[56:57], v[224:225]\n\t"
"v_pk_fma_f32 v[226:227], v[224:225], s[58:59], v[226:227] neg_lo:[0,0,1] neg_hi:[0,0,1]\n\t"
"v_pk_fma_f32 v[228:229], v[48:49], s[60:61], v[224:225]\n\t"
"v_pk_fma_f32 v[48:49], v[224:225], s[60:61], v[48:49]\n\t"
"v_pk_fma_f32 v[224:225], v[148:149], s[62:63], v[226:227]\n\t"
"v_pk_fma_f32 v[148:149], v[226:227], s[62:63], v[148:149]\n\t"
"Lst32_%=:\n\t"
"s_waitcnt vmcnt(13) lgkmcnt(4)\n\t"
"v_mfma_f32_16x16x32_bf16 v[0:3], v[248:251], a[168:171], v[0:3]\n\t"
"v_accvgpr_write_b32 a3, v236\n\t"
"v_accvgpr_write_b32 a7, v98\n\t"
"v_mfma_f32_16x16x32_bf16 v[0:3], a[208:211], a[172:175], v[0:3]\n\t"
"v_accvgpr_read_b32 v230, a74\n\t"
"v_accvgpr_read_b32 v232, a78\n\t"
"v_mfma_f32_16x16x32_bf16 v[0:3], a[212:215], a[176:179], v[0:3]\n\t"
"v_accvgpr_read_b32 v231, a75\n\t"
"v_accvgpr_read_b32 v233, a79\n\t"
"ds_read_b128 a[196:199], v244 offset:22016\n\t"
"buffer_load_dwordx4 a[252:255], v252, s[8:11], s20 offen offset:1024 sc1\n\t"
"v_cvt_pk_bf16_f32 v224, v224, v225\n\t"
"v_cvt_pk_bf16_f32 v228, v228, v229\n\t"
"v_pk_fma_f32 v[230:231], v[232:233], s[56:57], v[230:231]\n\t"
"v_pk_fma_f32 v[232:233], v[230:231], s[58:59], v[232:233] neg_lo:[0,0,1] neg_hi:[0,0,1]\n\t"
"v_pk_fma_f32 v[234:235], v[50:51], s[60:61], v[230:231]\n\t"
"v_pk_fma_f32 v[50:51], v[230:231], s[60:61], v[50:51]\n\t"
"v_pk_fma_f32 v[230:231], v[150:151], s[62:63], v[232:233]\n\t"
"v_pk_fma_f32 v[150:151], v[232:233], s[62:63], v[150:151]\n\t"
"s_waitcnt lgkmcnt(4)\n\t"
"v_mfma_f32_16x16x32_bf16 v[4:7], v[248:251], a[172:175], v[4:7]\n\t"
"v_accvgpr_write_b32 a10, v224\n\t"
"v_accvgpr_write_b32 a14, v228\n\t"
"v_mfma_f32_16x16x32_bf16 v[4:7], a[208:211], a[176:179], v[4:7]\n\t"
"v_accvgpr_read_b32 v236, a80\n\t"
"v_accvgpr_read_b32 v238, a84\n\t"
"v_mfma_f32_16x16x32_bf16 v[4:7], a[212:215], a[180:183], v[4:7]\n\t"
"v_accvgpr_read_b32 v237, a81\n\t"
"v_accvgpr_read_b32 v239, a85\n\t"
"ds_read_b128 a[200:203], v244 offset:22528\n\t"
"buffer_load_dwordx4 a[224:227], v252, s[8:11], s20 offen offset:2048 sc1\n\t"
"v_cvt_pk_bf16_f32 v230, v230, v231\n\t"
"v_cvt_pk_bf16_f32 v234, v234, v235\n\t"
"v_pk_fma_f32 v[236:237], v[238:239], s[56:57], v[236:237]\n\t"
"v_pk_fma_f32 v[238:239], v[236:237], s[58:59], v[238:239] neg_lo:[0,0,1] neg_hi:[0,0,1]\n\t"
"v_pk_fma_f32 v[98:99], v[52:53], s[60:61], v[236:237]\n\t"
"v_pk_fma_f32 v[52:53], v[236:237], s[60:61], v[52:53]\n\t"
"v_pk_fma_f32 v[236:237], v[152:153], s[62:63], v[238:239]\n\t"
"v_pk_fma_f32 v[152:153], v[238:239], s[62:63], v[152:153]\n\t"
"s_waitcnt lgkmcnt(4)\n\t"
"v_mfma_f32_16x16x32_bf16 v[8:11], v[248:251], a[176:179], v[8:11]\n\t"
"v_accvgpr_write_b32 a11, v230\n\t"
"v_accvgpr_write_b32 a15, v234\n\t"
"v_mfma_f32_16x16x32_bf16 v[8:11], a[208:211], a[180:183], v[8:11]\n\t"
"v_accvgpr_read_b32 v224, a82\n\t"
"v_accvgpr_read_b32 v226, a86\n\t"
"v_mfma_f32_16x16x32_bf16 v[8:11], a[212:215], a[184:187], v[8:11]\n\t"
"v_accvgpr_read_b32 v225, a83\n\t"
"v_accvgpr_read_b32 v227, a87\n\t"
"ds_read_b128 a[204:207], v244 offset:23040\n\t"
"s_mov_b32 s20, 663552\n\t"
"buffer_load_dwordx4 a[64:67], v252, s[8:11], s20 offen offset:0 sc1\n\t"
"v_cvt_pk_bf16_f32 v236, v236, v237\n\t"
"v_cvt_pk_bf16_f32 v98, v98, v99\n\t"
"v_pk_fma_f32 v[224:225], v[226:227], s[56:57], v[224:225]\n\t"
"v_pk_fma_f32 v[226:227], v[224:225], s[58:59], v[226:227] neg_lo:[0,0,1] neg_hi:[0,0,1]\n\t"
"v_pk_fma_f32 v[228:229], v[54:55], s[60:61], v[224:225]\n\t"
"v_pk_fma_f32 v[54:55], v[224:225], s[60:61], v[54:55]\n\t"
"v_pk_fma_f32 v[224:225], v[154:155], s[62:63], v[226:227]\n\t"
"v_pk_fma_f32 v[154:155], v[226:227], s[62:63], v[154:155]\n\t"
"s_waitcnt lgkmcnt(4)\n\t"
"v_mfma_f32_16x16x32_bf16 v[12:15], v[248:251], a[180:183], v[12:15]\n\t"
"v_accvgpr_write_b32 a18, v236\n\t"
"v_accvgpr_write_b32 a22, v98\n\t"
"v_mfma_f32_16x16x32_bf16 v[12:15], a[208:211], a[184:187], v[12:15]\n\t"
"v_accvgpr_read_b32 v230, a88\n\t"
"v_accvgpr_read_b32 v232, a92\n\t"
"v_mfma_f32_16x16x32_bf16 v[12:15], a[212:215], a[188:191], v[12:15]\n\t"
"v_accvgpr_read_b32 v231, a89\n\t"
"v_accvgpr_read_b32 v233, a93\n\t"
"v_accvgpr_read_b32 v236, a90\n\t"
"v_accvgpr_read_b32 v238, a94\n\t"
"v_accvgpr_read_b32 v237, a91\n\t"
"v_accvgpr_read_b32 v239, a95\n\t"
"buffer_load_dwordx4 a[68:71], v252, s[8:11], s20 offen offset:1024 sc1\n\t"
"v_cvt_pk_bf16_f32 v224, v224, v225\n\t"
"v_cvt_pk_bf16_f32 v228, v228, v229\n\t"
"v_pk_fma_f32 v[230:231], v[232:233], s[56:57], v[230:231]\n\t"
"v_pk_fma_f32 v[232:233], v[230:231], s[58:59], v[232:233] neg_lo:[0,0,1] neg_hi:[0,0,1]\n\t"
"v_pk_fma_f32 v[234:235], v[56:57], s[60:61], v[230:231]\n\t"
"v_pk_fma_f32 v[56:57], v[230:231], s[60:61], v[56:57]\n\t"
"v_pk_fma_f32 v[230:231], v[156:157], s[62:63], v[232:233]\n\t"
"v_pk_fma_f32 v[156:157], v[232:233], s[62:63], v[156:157]\n\t"
"v_pk_fma_f32 v[236:237], v[238:239], s[56:57], v[236:237]\n\t"
"v_pk_fma_f32 v[238:239], v[236:237], s[58:59], v[238:239] neg_lo:[0,0,1] neg_hi:[0,0,1]\n\t"
"v_pk_fma_f32 v[98:99], v[58:59], s[60:61], v[236:237]\n\t"
"v_pk_fma_f32 v[58:59], v[236:237], s[60:61], v[58:59]\n\t"
"v_pk_fma_f32 v[236:237], v[158:159], s[62:63], v[238:239]\n\t"
"v_pk_fma_f32 v[158:159], v[238:239], s[62:63], v[158:159]\n\t"
"s_waitcnt lgkmcnt(3)\n\t"
"v_mfma_f32_16x16x32_bf16 v[16:19], v[248:251], a[184:187], v[16:19]\n\t"
"v_accvgpr_write_b32 a19, v224\n\t"
"v_accvgpr_write_b32 a23, v228\n\t"
"v_mfma_f32_16x16x32_bf16 v[16:19], a[208:211], a[188:191], v[16:19]\n\t"
"v_accvgpr_read_b32 v224, a96\n\t"
"v_accvgpr_read_b32 v226, a100\n\t"
"v_mfma_f32_16x16x32_bf16 v[16:19], a[212:215], a[192:195], v[16:19]\n\t"
"v_accvgpr_read_b32 v225, a97\n\t"
"v_accvgpr_read_b32 v227, a101\n\t"
"buffer_load_dwordx4 a[72:75], v252, s[8:11], s20 offen offset:2048 sc1\n\t"
"v_cvt_pk_bf16_f32 v230, v230, v231\n\t"
"v_cvt_pk_bf16_f32 v234, v234, v235\n\t"
"v_pk_fma_f32 v[224:225], v[226:227], s[56:57], v[224:225]\n\t"
"v_pk_fma_f32 v[226:227], v[224:225], s[58:59], v[226:227] neg_lo:[0,0,1] neg_hi:[0,0,1]\n\t"
"v_pk_fma_f32 v[228:229], v[60:61], s[60:61], v[224:225]\n\t"
"v_pk_fma_f32 v[60:61], v[224:225], s[60:61], v[60:61]\n\t"
"v_pk_fma_f32 v[224:225], v[160:161], s[62:63], v[226:227]\n\t"
"v_pk_fma_f32 v[160:161], v[226:227], s[62:63], v[160:161]\n\t"
"s_waitcnt lgkmcnt(2)\n\t"
"v_mfma_f32_16x16x32_bf16 v[20:23], v[248:251], a[188:191], v[20:23]\n\t"
"v_accvgpr_write_b32 a26, v230\n\t"
"v_cvt_pk_bf16_f32 v236, v236, v237\n\t"
"v_mfma_f32_16x16x32_bf16 v[20:23], a[208:211], a[192:195], v[20:23]\n\t"
"v_accvgpr_write_b32 a30, v234\n\t"
"v_cvt_pk_bf16_f32 v98, v98, v99\n\t"
"v_mfma_f32_16x16x32_bf16 v[20:23], a[212:215], a[196:199], v[20:23]\n\t"
"v_accvgpr_write_b32 a27, v236\n\t"
"v_accvgpr_write_b32 a31, v98\n\t"
"v_accvgpr_read_b32 v230, a98\n\t"
"v_accvgpr_read_b32 v232, a102\n\t"
"v_accvgpr_read_b32 v231, a99\n\t"
"v_accvgpr_read_b32 v233, a103\n\t"
"s_mov_b32 s20, 675840\n\t"
"buffer_load_dwordx4 a[76:79], v252, s[8:11], s20 offen offset:0 sc1\n\t"
"v_cvt_pk_bf16_f32 v224, v224, v225\n\t"
"v_cvt_pk_bf16_f32 v228, v228, v229\n\t"
"v_pk_fma_f32 v[230:231], v[232:233], s[56:57], v[230:231]\n\t"
"v_pk_fma_f32 v[232:233], v[230:231], s[58:59], v[232:233] neg_lo:[0,0,1] neg_hi:[0,0,1]\n\t"
"v_pk_fma_f32 v[234:235], v[62:63], s[60:61], v[230:231]\n\t"
"v_pk_fma_f32 v[62:63], v[230:231], s[60:61], v[62:63]\n\t"
"v_pk_fma_f32 v[230:231], v[162:163], s[62:63], v[232:233]\n\t"
"v_pk_fma_f32 v[162:163], v[232:233], s[62:63], v[162:163]\n\t"
"s_waitcnt lgkmcnt(1)\n\t"
"v_mfma_f32_16x16x32_bf16 v[24:27], v[248:251], a[192:195], v[24:27]\n\t"
"v_accvgpr_write_b32 a34, v224\n\t"
"v_accvgpr_write_b32 a38, v228\n\t"
"v_mfma_f32_16x16x32_bf16 v[24:27], a[208:211], a[196:199], v[24:27]\n\t"
"v_accvgpr_read_b32 v236, a104\n\t"
"v_accvgpr_read_b32 v238, a108\n\t"
"v_mfma_f32_16x16x32_bf16 v[24:27], a[212:215], a[200:203], v[24:27]\n\t"
"v_accvgpr_read_b32 v237, a105\n\t"
"v_accvgpr_read_b32 v239, a109\n\t"
"buffer_load_dwordx4 a[80:83], v252, s[8:11], s20 offen offset:1024 sc1\n\t"
"v_cvt_pk_bf16_f32 v230, v230, v231\n\t"
"v_cvt_pk_bf16_f32 v234, v234, v235\n\t"
"v_pk_fma_f32 v[236:237], v[238:239], s[56:57], v[236:237]\n\t"
"v_pk_fma_f32 v[238:239], v[236:237], s[58:59], v[238:239] neg_lo:[0,0,1] neg_hi:[0,0,1]\n\t"
"v_pk_fma_f32 v[98:99], v[64:65], s[60:61], v[236:237]\n\t"
"v_pk_fma_f32 v[64:65], v[236:237], s[60:61], v[64:65]\n\t"
"v_pk_fma_f32 v[236:237], v[164:165], s[62:63], v[238:239]\n\t"
"v_pk_fma_f32 v[164:165], v[238:239], s[62:63], v[164:165]\n\t"
"s_waitcnt lgkmcnt(0)\n\t"
"v_mfma_f32_16x16x32_bf16 v[28:31], v[248:251], a[196:199], v[28:31]\n\t"
"v_accvgpr_write_b32 a35, v230\n\t"
"v_accvgpr_write_b32 a39, v234\n\t"
"v_mfma_f32_16x16x32_bf16 v[28:31], a[208:211], a[200:203], v[28:31]\n\t"
"v_accvgpr_read_b32 v224, a106\n\t"
"v_accvgpr_read_b32 v226, a110\n\t"
"v_mfma_f32_16x16x32_bf16 v[28:31], a[212:215], a[204:207], v[28:31]\n\t"
"v_accvgpr_read_b32 v225, a107\n\t"
"v_accvgpr_read_b32 v227, a111\n\t"
"buffer_load_dwordx4 a[84:87], v252, s[8:11], s20 offen offset:2048 sc1\n\t"
"v_cvt_pk_bf16_f32 v236, v236, v237\n\t"
"v_cvt_pk_bf16_f32 v98, v98, v99\n\t"
"v_pk_fma_f32 v[224:225], v[226:227], s[56:57], v[224:225]\n\t"
"v_pk_fma_f32 v[226:227], v[224:225], s[58:59], v[226:227] neg_lo:[0,0,1] neg_hi:[0,0,1]\n\t"
"v_pk_fma_f32 v[228:229], v[66:67], s[60:61], v[224:225]\n\t"
"v_pk_fma_f32 v[66:67], v[224:225], s[60:61], v[66:67]\n\t"
"v_pk_fma_f32 v[224:225], v[166:167], s[62:63], v[226:227]\n\t"
"v_pk_fma_f32 v[166:167], v[226:227], s[62:63], v[166:167]\n\t"
"s_waitcnt vmcnt(18)\n\t"
"v_mfma_f32_16x16x32_bf16 v[32:35], a[216:219], a[168:171], v[32:35]\n\t"
"v_accvgpr_write_b32 a104, v236\n\t"
"v_accvgpr_write_b32 a106, v98\n\t"
"v_mfma_f32_16x16x32_bf16 v[32:35], a[228:231], a[172:175], v[32:35]\n\t"
"v_accvgpr_read_b32 v230, a112\n\t"
"v_accvgpr_read_b32 v232, a116\n\t"
"v_mfma_f32_16x16x32_bf16 v[32:35], a[232:235], a[176:179], v[32:35]\n\t"
"v_accvgpr_read_b32 v231, a113\n\t"
"v_accvgpr_read_b32 v233, a117\n\t"
"s_mov_b32 s20, 688128\n\t"
"buffer_load_dwordx4 a[208:211], v252, s[8:11], s20 offen offset:0 sc1\n\t"
"v_cvt_pk_bf16_f32 v224, v224, v225\n\t"
"v_cvt_pk_bf16_f32 v228, v228, v229\n\t"
"v_pk_fma_f32 v[230:231], v[232:233], s[56:57], v[230:231]\n\t"
"v_pk_fma_f32 v[232:233], v[230:231], s[58:59], v[232:233] neg_lo:[0,0,1] neg_hi:[0,0,1]\n\t"
"v_pk_fma_f32 v[234:235], v[68:69], s[60:61], v[230:231]\n\t"
"v_pk_fma_f32 v[68:69], v[230:231], s[60:61], v[68:69]\n\t"
"v_pk_fma_f32 v[230:231], v[168:169], s[62:63], v[232:233]\n\t"
"v_pk_fma_f32 v[168:169], v[232:233], s[62:63], v[168:169]\n\t"
"v_mfma_f32_16x16x32_bf16 v[36:39], a[216:219], a[172:175], v[36:39]\n\t"
"v_accvgpr_write_b32 a105, v224\n\t"
"v_accvgpr_write_b32 a107, v228\n\t"
"v_mfma_f32_16x16x32_bf16 v[36:39], a[228:231], a[176:179], v[36:39]\n\t"
"v_accvgpr_read_b32 v236, a114\n\t"
"v_accvgpr_read_b32 v238, a118\n\t"
"v_mfma_f32_16x16x32_bf16 v[36:39], a[232:235], a[180:183], v[36:39]\n\t"
"v_accvgpr_read_b32 v237, a115\n\t"
"v_accvgpr_read_b32 v239, a119\n\t"
"buffer_load_dwordx4 a[212:215], v252, s[8:11], s20 offen offset:1024 sc1\n\t"
"v_cvt_pk_bf16_f32 v230, v230, v231\n\t"
"v_cvt_pk_bf16_f32 v234, v234, v235\n\t"
"v_pk_fma_f32 v[236:237], v[238:239], s[56:57], v[236:237]\n\t"
"v_pk_fma_f32 v[238:239], v[236:237], s[58:59], v[238:239] neg_lo:[0,0,1] neg_hi:[0,0,1]\n\t"
"v_pk_fma_f32 v[98:99], v[70:71], s[60:61], v[236:237]\n\t"
"v_pk_fma_f32 v[70:71], v[236:237], s[60:61], v[70:71]\n\t"
"v_pk_fma_f32 v[236:237], v[170:171], s[62:63], v[238:239]\n\t"
"v_pk_fma_f32 v[170:171], v[238:239], s[62:63], v[170:171]\n\t"
"v_mfma_f32_16x16x32_bf16 v[40:43], a[216:219], a[176:179], v[40:43]\n\t"
"v_accvgpr_write_b32 a112, v230\n\t"
"v_accvgpr_write_b32 a114, v234\n\t"
"v_mfma_f32_16x16x32_bf16 v[40:43], a[228:231], a[180:183], v[40:43]\n\t"
"v_accvgpr_read_b32 v224, a120\n\t"
"v_accvgpr_read_b32 v226, a124\n\t"
"v_mfma_f32_16x16x32_bf16 v[40:43], a[232:235], a[184:187], v[40:43]\n\t"
"v_accvgpr_read_b32 v225, a121\n\t"
"v_accvgpr_read_b32 v227, a125\n\t"
"global_load_dwordx4 v[240:243], v252, s[46:47] offset:0\n\t"
"buffer_load_dwordx4 a[88:91], v252, s[8:11], s20 offen offset:2048 sc1\n\t"
"v_cvt_pk_bf16_f32 v236, v236, v237\n\t"
"v_cvt_pk_bf16_f32 v98, v98, v99\n\t"
"v_pk_fma_f32 v[224:225], v[226:227], s[56:57], v[224:225]\n\t"
"v_pk_fma_f32 v[226:227], v[224:225], s[58:59], v[226:227] neg_lo:[0,0,1] neg_hi:[0,0,1]\n\t"
"v_pk_fma_f32 v[228:229], v[72:73], s[60:61], v[224:225]\n\t"
"v_pk_fma_f32 v[72:73], v[224:225], s[60:61], v[72:73]\n\t"
"v_pk_fma_f32 v[224:225], v[172:173], s[62:63], v[226:227]\n\t"
"v_pk_fma_f32 v[172:173], v[226:227], s[62:63], v[172:173]\n\t"
"v_mfma_f32_16x16x32_bf16 v[44:47], a[216:219], a[180:183], v[44:47]\n\t"
"v_accvgpr_write_b32 a113, v236\n\t"
"v_accvgpr_write_b32 a115, v98\n\t"
"v_mfma_f32_16x16x32_bf16 v[44:47], a[228:231], a[184:187], v[44:47]\n\t"
"v_accvgpr_read_b32 v230, a122\n\t"
"v_accvgpr_read_b32 v232, a126\n\t"
"v_mfma_f32_16x16x32_bf16 v[44:47], a[232:235], a[188:191], v[44:47]\n\t"
"v_accvgpr_read_b32 v231, a123\n\t"
"v_accvgpr_read_b32 v233, a127\n\t"
"v_accvgpr_read_b32 v236, a128\n\t"
"v_accvgpr_read_b32 v238, a132\n\t"
"v_accvgpr_read_b32 v237, a129\n\t"
"v_accvgpr_read_b32 v239, a133\n\t"
"s_mov_b32 s20, 847872\n\t"
"buffer_load_dwordx4 a[92:95], v252, s[8:11], s20 offen offset:0 sc1\n\t"
"v_cvt_pk_bf16_f32 v224, v224, v225\n\t"
"v_cvt_pk_bf16_f32 v228, v228, v229\n\t"
"v_pk_fma_f32 v[230:231], v[232:233], s[56:57], v[230:231]\n\t"
"v_pk_fma_f32 v[232:233], v[230:231], s[58:59], v[232:233] neg_lo:[0,0,1] neg_hi:[0,0,1]\n\t"
"v_pk_fma_f32 v[234:235], v[74:75], s[60:61], v[230:231]\n\t"
"v_pk_fma_f32 v[74:75], v[230:231], s[60:61], v[74:75]\n\t"
"v_pk_fma_f32 v[230:231], v[174:175], s[62:63], v[232:233]\n\t"
"v_pk_fma_f32 v[174:175], v[232:233], s[62:63], v[174:175]\n\t"
"v_pk_fma_f32 v[236:237], v[238:239], s[56:57], v[236:237]\n\t"
"v_pk_fma_f32 v[238:239], v[236:237], s[58:59], v[238:239] neg_lo:[0,0,1] neg_hi:[0,0,1]\n\t"
"v_pk_fma_f32 v[98:99], v[76:77], s[60:61], v[236:237]\n\t"
"v_pk_fma_f32 v[76:77], v[236:237], s[60:61], v[76:77]\n\t"
"v_pk_fma_f32 v[236:237], v[176:177], s[62:63], v[238:239]\n\t"
"v_pk_fma_f32 v[176:177], v[238:239], s[62:63], v[176:177]\n\t"
"global_load_dwordx4 a[52:55], v252, s[46:47] offset:1024\n\t"
"v_mfma_f32_16x16x32_bf16 v[48:51], a[216:219], a[184:187], v[48:51]\n\t"
"v_accvgpr_write_b32 a120, v224\n\t"
"v_accvgpr_write_b32 a122, v228\n\t"
"v_mfma_f32_16x16x32_bf16 v[48:51], a[228:231], a[188:191], v[48:51]\n\t"
"v_accvgpr_read_b32 v224, a130\n\t"
"v_accvgpr_read_b32 v226, a134\n\t"
"v_mfma_f32_16x16x32_bf16 v[48:51], a[232:235], a[192:195], v[48:51]\n\t"
"global_load_dwordx4 v[248:251], v252, s[48:49] offset:0\n\t"
"v_accvgpr_read_b32 v225, a131\n\t"
"v_accvgpr_read_b32 v227, a135\n\t"
"buffer_load_dwordx4 a[96:99], v252, s[8:11], s20 offen offset:1024 sc1\n\t"
"v_cvt_pk_bf16_f32 v230, v230, v231\n\t"
"v_cvt_pk_bf16_f32 v234, v234, v235\n\t"
"v_cvt_pk_bf16_f32 v236, v236, v237\n\t"
"v_cvt_pk_bf16_f32 v98, v98, v99\n\t"
"v_pk_fma_f32 v[224:225], v[226:227], s[56:57], v[224:225]\n\t"
"v_pk_fma_f32 v[226:227], v[224:225], s[58:59], v[226:227] neg_lo:[0,0,1] neg_hi:[0,0,1]\n\t"
"v_pk_fma_f32 v[228:229], v[78:79], s[60:61], v[224:225]\n\t"
"v_pk_fma_f32 v[78:79], v[224:225], s[60:61], v[78:79]\n\t"
"v_pk_fma_f32 v[224:225], v[178:179], s[62:63], v[226:227]\n\t"
"v_pk_fma_f32 v[178:179], v[226:227], s[62:63], v[178:179]\n\t"
"v_mfma_f32_16x16x32_bf16 v[52:55], a[216:219], a[188:191], v[52:55]\n\t"
"v_accvgpr_write_b32 a121, v230\n\t"
"v_accvgpr_write_b32 a123, v234\n\t"
"v_mfma_f32_16x16x32_bf16 v[52:55], a[228:231], a[192:195], v[52:55]\n\t"
"v_accvgpr_write_b32 a128, v236\n\t"
"v_accvgpr_write_b32 a130, v98\n\t"
"v_mfma_f32_16x16x32_bf16 v[52:55], a[232:235], a[196:199], v[52:55]\n\t"
"v_accvgpr_read_b32 v230, a136\n\t"
"v_accvgpr_read_b32 v232, a140\n\t"
"v_accvgpr_read_b32 v231, a137\n\t"
"v_accvgpr_read_b32 v233, a141\n\t"
"buffer_load_dwordx4 a[100:103], v252, s[8:11], s20 offen offset:2048 sc1\n\t"
"v_cvt_pk_bf16_f32 v224, v224, v225\n\t"
"v_cvt_pk_bf16_f32 v228, v228, v229\n\t"
"v_pk_fma_f32 v[230:231], v[232:233], s[56:57], v[230:231]\n\t"
"v_pk_fma_f32 v[232:233], v[230:231], s[58:59], v[232:233] neg_lo:[0,0,1] neg_hi:[0,0,1]\n\t"
"v_pk_fma_f32 v[234:235], v[80:81], s[60:61], v[230:231]\n\t"
"v_pk_fma_f32 v[80:81], v[230:231], s[60:61], v[80:81]\n\t"
"v_pk_fma_f32 v[230:231], v[180:181], s[62:63], v[232:233]\n\t"
"v_pk_fma_f32 v[180:181], v[232:233], s[62:63], v[180:181]\n\t"
"v_mfma_f32_16x16x32_bf16 v[56:59], a[216:219], a[192:195], v[56:59]\n\t"
"v_accvgpr_write_b32 a129, v224\n\t"
"v_accvgpr_write_b32 a131, v228\n\t"
"v_mfma_f32_16x16x32_bf16 v[56:59], a[228:231], a[196:199], v[56:59]\n\t"
"v_accvgpr_read_b32 v236, a138\n\t"
"v_accvgpr_read_b32 v238, a142\n\t"
"v_mfma_f32_16x16x32_bf16 v[56:59], a[232:235], a[200:203], v[56:59]\n\t"
"v_accvgpr_read_b32 v237, a139\n\t"
"v_accvgpr_read_b32 v239, a143\n\t"
"s_mov_b32 s20, 860160\n\t"
"buffer_load_dwordx4 a[108:111], v252, s[8:11], s20 offen offset:0 sc1\n\t"
"v_cvt_pk_bf16_f32 v230, v230, v231\n\t"
"v_cvt_pk_bf16_f32 v234, v234, v235\n\t"
"v_pk_fma_f32 v[236:237], v[238:239], s[56:57], v[236:237]\n\t"
"v_pk_fma_f32 v[238:239], v[236:237], s[58:59], v[238:239] neg_lo:[0,0,1] neg_hi:[0,0,1]\n\t"
"v_pk_fma_f32 v[98:99], v[82:83], s[60:61], v[236:237]\n\t"
"v_pk_fma_f32 v[82:83], v[236:237], s[60:61], v[82:83]\n\t"
"v_pk_fma_f32 v[236:237], v[182:183], s[62:63], v[238:239]\n\t"
"v_pk_fma_f32 v[182:183], v[238:239], s[62:63], v[182:183]\n\t"
"v_mfma_f32_16x16x32_bf16 v[60:63], a[216:219], a[196:199], v[60:63]\n\t"
"v_accvgpr_write_b32 a136, v230\n\t"
"v_accvgpr_write_b32 a138, v234\n\t"
"v_mfma_f32_16x16x32_bf16 v[60:63], a[228:231], a[200:203], v[60:63]\n\t"
"v_accvgpr_read_b32 v224, a144\n\t"
"v_accvgpr_read_b32 v226, a148\n\t"
"v_mfma_f32_16x16x32_bf16 v[60:63], a[232:235], a[204:207], v[60:63]\n\t"
"v_accvgpr_read_b32 v225, a145\n\t"
"v_accvgpr_read_b32 v227, a149\n\t"
"buffer_load_dwordx4 a[116:119], v252, s[8:11], s20 offen offset:1024 sc1\n\t"
"v_cvt_pk_bf16_f32 v236, v236, v237\n\t"
"v_cvt_pk_bf16_f32 v98, v98, v99\n\t"
"v_pk_fma_f32 v[224:225], v[226:227], s[56:57], v[224:225]\n\t"
"v_pk_fma_f32 v[226:227], v[224:225], s[58:59], v[226:227] neg_lo:[0,0,1] neg_hi:[0,0,1]\n\t"
"v_pk_fma_f32 v[228:229], v[84:85], s[60:61], v[224:225]\n\t"
"v_pk_fma_f32 v[84:85], v[224:225], s[60:61], v[84:85]\n\t"
"v_pk_fma_f32 v[224:225], v[184:185], s[62:63], v[226:227]\n\t"
"v_pk_fma_f32 v[184:185], v[226:227], s[62:63], v[184:185]\n\t"
"s_waitcnt vmcnt(26)\n\t"
"v_mfma_f32_16x16x32_bf16 v[64:67], a[236:239], a[168:171], v[64:67]\n\t"
"v_accvgpr_write_b32 a137, v236\n\t"
"v_accvgpr_write_b32 a139, v98\n\t"
"v_mfma_f32_16x16x32_bf16 v[64:67], a[240:243], a[172:175], v[64:67]\n\t"
"v_accvgpr_read_b32 v230, a146\n\t"
"v_accvgpr_read_b32 v232, a150\n\t"
"v_mfma_f32_16x16x32_bf16 v[64:67], a[244:247], a[176:179], v[64:67]\n\t"
"v_accvgpr_read_b32 v231, a147\n\t"
"v_accvgpr_read_b32 v233, a151\n\t"
"buffer_load_dwordx4 a[216:219], v252, s[8:11], s20 offen offset:2048 sc1\n\t"
"v_cvt_pk_bf16_f32 v224, v224, v225\n\t"
"v_cvt_pk_bf16_f32 v228, v228, v229\n\t"
"v_pk_fma_f32 v[230:231], v[232:233], s[56:57], v[230:231]\n\t"
"v_pk_fma_f32 v[232:233], v[230:231], s[58:59], v[232:233] neg_lo:[0,0,1] neg_hi:[0,0,1]\n\t"
"v_pk_fma_f32 v[234:235], v[86:87], s[60:61], v[230:231]\n\t"
"v_pk_fma_f32 v[86:87], v[230:231], s[60:61], v[86:87]\n\t"
"v_pk_fma_f32 v[230:231], v[186:187], s[62:63], v[232:233]\n\t"
"v_pk_fma_f32 v[186:187], v[232:233], s[62:63], v[186:187]\n\t"
"v_mfma_f32_16x16x32_bf16 v[68:71], a[236:239], a[172:175], v[68:71]\n\t"
"v_accvgpr_write_b32 a144, v224\n\t"
"v_accvgpr_write_b32 a146, v228\n\t"
"v_mfma_f32_16x16x32_bf16 v[68:71], a[240:243], a[176:179], v[68:71]\n\t"
"v_accvgpr_read_b32 v236, a152\n\t"
"v_accvgpr_read_b32 v238, a156\n\t"
"v_mfma_f32_16x16x32_bf16 v[68:71], a[244:247], a[180:183], v[68:71]\n\t"
"v_accvgpr_read_b32 v237, a153\n\t"
"v_accvgpr_read_b32 v239, a157\n\t"
"s_mov_b32 s20, 872448\n\t"
"buffer_load_dwordx4 a[228:231], v252, s[8:11], s20 offen offset:0 sc1\n\t"
"v_cvt_pk_bf16_f32 v230, v230, v231\n\t"
"v_cvt_pk_bf16_f32 v234, v234, v235\n\t"
"v_pk_fma_f32 v[236:237], v[238:239], s[56:57], v[236:237]\n\t"
"v_pk_fma_f32 v[238:239], v[236:237], s[58:59], v[238:239] neg_lo:[0,0,1] neg_hi:[0,0,1]\n\t"
"v_pk_fma_f32 v[98:99], v[88:89], s[60:61], v[236:237]\n\t"
"v_pk_fma_f32 v[88:89], v[236:237], s[60:61], v[88:89]\n\t"
"v_pk_fma_f32 v[236:237], v[188:189], s[62:63], v[238:239]\n\t"
"v_pk_fma_f32 v[188:189], v[238:239], s[62:63], v[188:189]\n\t"
"v_mfma_f32_16x16x32_bf16 v[72:75], a[236:239], a[176:179], v[72:75]\n\t"
"v_accvgpr_write_b32 a145, v230\n\t"
"v_accvgpr_write_b32 a147, v234\n\t"
"v_mfma_f32_16x16x32_bf16 v[72:75], a[240:243], a[180:183], v[72:75]\n\t"
"v_accvgpr_read_b32 v224, a154\n\t"
"v_accvgpr_read_b32 v226, a158\n\t"
"v_mfma_f32_16x16x32_bf16 v[72:75], a[244:247], a[184:187], v[72:75]\n\t"
"v_accvgpr_read_b32 v225, a155\n\t"
"v_accvgpr_read_b32 v227, a159\n\t"
"buffer_load_dwordx4 a[232:235], v252, s[8:11], s20 offen offset:1024 sc1\n\t"
"v_cvt_pk_bf16_f32 v236, v236, v237\n\t"
"v_cvt_pk_bf16_f32 v98, v98, v99\n\t"
"v_pk_fma_f32 v[224:225], v[226:227], s[56:57], v[224:225]\n\t"
"v_pk_fma_f32 v[226:227], v[224:225], s[58:59], v[226:227] neg_lo:[0,0,1] neg_hi:[0,0,1]\n\t"
"v_pk_fma_f32 v[228:229], v[90:91], s[60:61], v[224:225]\n\t"
"v_pk_fma_f32 v[90:91], v[224:225], s[60:61], v[90:91]\n\t"
"v_pk_fma_f32 v[224:225], v[190:191], s[62:63], v[226:227]\n\t"
"v_pk_fma_f32 v[190:191], v[226:227], s[62:63], v[190:191]\n\t"
"v_mfma_f32_16x16x32_bf16 v[76:79], a[236:239], a[180:183], v[76:79]\n\t"
"v_accvgpr_write_b32 a152, v236\n\t"
"v_accvgpr_write_b32 a154, v98\n\t"
"v_mfma_f32_16x16x32_bf16 v[76:79], a[240:243], a[184:187], v[76:79]\n\t"
"v_accvgpr_read_b32 v230, a160\n\t"
"v_accvgpr_read_b32 v232, a164\n\t"
"v_mfma_f32_16x16x32_bf16 v[76:79], a[244:247], a[188:191], v[76:79]\n\t"
"v_accvgpr_read_b32 v231, a161\n\t"
"v_accvgpr_read_b32 v233, a165\n\t"
"ds_read_b128 a[168:171], v244 offset:27648\n\t"
"ds_read_b128 a[172:175], v244 offset:28160\n\t"
"ds_read_b128 a[176:179], v244 offset:28672\n\t"
"buffer_load_dwordx4 a[124:127], v252, s[8:11], s20 offen offset:2048 sc1\n\t"
"v_cvt_pk_bf16_f32 v224, v224, v225\n\t"
"v_cvt_pk_bf16_f32 v228, v228, v229\n\t"
"v_pk_fma_f32 v[230:231], v[232:233], s[56:57], v[230:231]\n\t"
"v_pk_fma_f32 v[232:233], v[230:231], s[58:59], v[232:233] neg_lo:[0,0,1] neg_hi:[0,0,1]\n\t"
"v_pk_fma_f32 v[234:235], v[92:93], s[60:61], v[230:231]\n\t"
"v_pk_fma_f32 v[92:93], v[230:231], s[60:61], v[92:93]\n\t"
"v_pk_fma_f32 v[230:231], v[192:193], s[62:63], v[232:233]\n\t"
"v_pk_fma_f32 v[192:193], v[232:233], s[62:63], v[192:193]\n\t"
"v_mfma_f32_16x16x32_bf16 v[80:83], a[236:239], a[184:187], v[80:83]\n\t"
"v_accvgpr_write_b32 a153, v224\n\t"
"v_accvgpr_write_b32 a155, v228\n\t"
"v_mfma_f32_16x16x32_bf16 v[80:83], a[240:243], a[188:191], v[80:83]\n\t"
"v_accvgpr_read_b32 v236, a162\n\t"
"v_accvgpr_read_b32 v238, a166\n\t"
"v_mfma_f32_16x16x32_bf16 v[80:83], a[244:247], a[192:195], v[80:83]\n\t"
"v_accvgpr_read_b32 v237, a163\n\t"
"v_accvgpr_read_b32 v239, a167\n\t"
"s_mov_b32 s20, 884736\n\t"
"buffer_load_dwordx4 a[132:135], v252, s[8:11], s20 offen offset:0 sc1\n\t"
"v_cvt_pk_bf16_f32 v230, v230, v231\n\t"
"v_cvt_pk_bf16_f32 v234, v234, v235\n\t"
"v_pk_fma_f32 v[236:237], v[238:239], s[56:57], v[236:237]\n\t"
"v_pk_fma_f32 v[238:239], v[236:237], s[58:59], v[238:239] neg_lo:[0,0,1] neg_hi:[0,0,1]\n\t"
"v_pk_fma_f32 v[98:99], v[94:95], s[60:61], v[236:237]\n\t"
"v_pk_fma_f32 v[94:95], v[236:237], s[60:61], v[94:95]\n\t"
"v_pk_fma_f32 v[236:237], v[194:195], s[62:63], v[238:239]\n\t"
"v_pk_fma_f32 v[194:195], v[238:239], s[62:63], v[194:195]\n\t"
"v_mfma_f32_16x16x32_bf16 v[84:87], a[236:239], a[188:191], v[84:87]\n\t"
"ds_read_b128 a[180:183], v244 offset:29184\n\t"
"v_accvgpr_write_b32 a160, v230\n\t"
"v_mfma_f32_16x16x32_bf16 v[84:87], a[240:243], a[192:195], v[84:87]\n\t"
"v_accvgpr_write_b32 a162, v234\n\t"
"v_cvt_pk_bf16_f32 v236, v236, v237\n\t"
"v_mfma_f32_16x16x32_bf16 v[84:87], a[244:247], a[196:199], v[84:87]\n\t"
"buffer_load_dwordx4 a[140:143], v252, s[8:11], s20 offen offset:1024 sc1\n\t"
"v_cvt_pk_bf16_f32 v98, v98, v99\n\t"
"ds_read_b128 a[184:187], v244 offset:29696\n\t"
"v_mfma_f32_16x16x32_bf16 v[88:91], a[236:239], a[192:195], v[88:91]\n\t"
"v_accvgpr_write_b32 a161, v236\n\t"
"v_accvgpr_write_b32 a163, v98\n\t"
"v_mfma_f32_16x16x32_bf16 v[88:91], a[240:243], a[196:199], v[88:91]\n\t"
"s_waitcnt vmcnt(14)\n\t"
"ds_read_b64_tr_b16 v[236:237], v240 offset:1536\n\t"
"v_mfma_f32_16x16x32_bf16 v[88:91], a[244:247], a[200:203], v[88:91]\n\t"
"ds_read_b64_tr_b16 v[238:239], v240 offset:3072\n\t"
"buffer_load_dwordx4 a[148:151], v252, s[8:11], s20 offen offset:2048 sc1\n\t"
"ds_read_b64_tr_b16 v[228:229], v241 offset:1536\n\t"
"ds_read_b64_tr_b16 v[230:231], v241 offset:3072\n\t"
"v_mfma_f32_16x16x32_bf16 v[92:95], a[236:239], a[196:199], v[92:95]\n\t"
"ds_read_b128 a[188:191], v244 offset:30208\n\t"
"v_mfma_f32_16x16x32_bf16 v[92:95], a[240:243], a[200:203], v[92:95]\n\t"
"ds_read_b128 a[192:195], v244 offset:30720\n\t"
"v_mfma_f32_16x16x32_bf16 v[92:95], a[244:247], a[204:207], v[92:95]\n\t"
"s_mov_b32 s20, 897024\n\t"
"buffer_load_dwordx4 a[156:159], v252, s[8:11], s20 offen offset:0 sc1\n\t"
"s_waitcnt lgkmcnt(8)\n\t"
"v_mfma_f32_16x16x32_bf16 v[100:103], a[220:223], a[168:171], v[100:103]\n\t"
"ds_read_b64_tr_b16 v[232:233], v240 offset:21504\n\t"
"v_mfma_f32_16x16x32_bf16 v[100:103], a[248:251], a[172:175], v[100:103]\n\t"
"ds_read_b64_tr_b16 v[234:235], v240 offset:23040\n\t"
"v_mfma_f32_16x16x32_bf16 v[100:103], a[40:43], a[176:179], v[100:103]\n\t"
"buffer_load_dwordx4 a[236:239], v252, s[8:11], s20 offen offset:1024 sc1\n\t"
"ds_read_b128 a[196:199], v244 offset:31232\n\t"
"ds_read_b128 a[200:203], v244 offset:31744\n\t"
"s_waitcnt vmcnt(14) lgkmcnt(8)\n\t"
"v_mfma_f32_16x16x32_bf16 v[236:239], v[236:239], a[52:55], 0\n\t"
"ds_read_b128 a[204:207], v244 offset:32256\n\t"
"ds_read_b64_tr_b16 v[224:225], v241 offset:21504\n\t"
"s_waitcnt lgkmcnt(8)\n\t"
"v_mfma_f32_16x16x32_bf16 v[228:231], v[228:231], a[52:55], 0\n\t"
"ds_read_b64_tr_b16 v[226:227], v241 offset:23040\n\t"
"buffer_load_dwordx4 a[240:243], v252, s[8:11], s20 offen offset:2048 sc1\n\t"
"v_mfma_f32_16x16x32_bf16 v[104:107], a[220:223], a[172:175], v[104:107]\n\t"
"v_mfma_f32_16x16x32_bf16 v[104:107], a[248:251], a[176:179], v[104:107]\n\t"
"v_mfma_f32_16x16x32_bf16 v[104:107], a[40:43], a[180:183], v[104:107]\n\t"
"s_mov_b32 s20, 909312\n\t"
"buffer_load_dwordx4 a[244:247], v252, s[8:11], s20 offen offset:0 sc1\n\t"
"s_waitcnt lgkmcnt(2)\n\t"
"s_barrier\n\t"
"v_mfma_f32_16x16x32_bf16 v[108:111], a[220:223], a[176:179], v[108:111]\n\t"
"v_cvt_pk_bf16_f32 v236, v236, v237\n\t"
"v_cvt_pk_bf16_f32 v237, v238, v239\n\t"
"v_mfma_f32_16x16x32_bf16 v[108:111], a[248:251], a[180:183], v[108:111]\n\t"
"v_cvt_pk_bf16_f32 v238, v228, v229\n\t"
"v_cvt_pk_bf16_f32 v239, v230, v231\n\t"
"v_mfma_f32_16x16x32_bf16 v[108:111], a[40:43], a[184:187], v[108:111]\n\t"
"buffer_load_dwordx4 a[164:167], v252, s[8:11], s20 offen offset:1024 sc1\n\t"
"v_mfma_f32_16x16x32_bf16 v[232:235], v[232:235], a[52:55], 0\n\t"
"buffer_load_dwordx4 v[96:99], v252, s[8:11], s20 offen offset:2048 sc1\n\t"
"s_waitcnt lgkmcnt(0)\n\t"
"v_mfma_f32_16x16x32_bf16 v[224:227], v[224:227], a[52:55], 0\n\t"
"s_mov_b64 exec, s[44:45]\n\t"
"ds_write_b128 v242, v[236:239] offset:0\n\t"
"s_mov_b64 exec, -1\n\t"
"v_mfma_f32_16x16x32_bf16 v[112:115], a[220:223], a[180:183], v[112:115]\n\t"
"ds_read_b64_tr_b16 v[228:229], v240 offset:41472\n\t"
"v_mfma_f32_16x16x32_bf16 v[112:115], a[248:251], a[184:187], v[112:115]\n\t"
"ds_read_b64_tr_b16 v[230:231], v240 offset:43008\n\t"
"v_mfma_f32_16x16x32_bf16 v[112:115], a[40:43], a[188:191], v[112:115]\n\t"
"ds_read_b64_tr_b16 v[236:237], v241 offset:41472\n\t"
"ds_read_b64_tr_b16 v[238:239], v241 offset:43008\n\t"
"v_mfma_f32_16x16x32_bf16 v[116:119], a[220:223], a[184:187], v[116:119]\n\t"
"v_cvt_pk_bf16_f32 v232, v232, v233\n\t"
"v_cvt_pk_bf16_f32 v233, v234, v235\n\t"
"v_mfma_f32_16x16x32_bf16 v[116:119], a[248:251], a[188:191], v[116:119]\n\t"
"v_cvt_pk_bf16_f32 v234, v224, v225\n\t"
"v_cvt_pk_bf16_f32 v235, v226, v227\n\t"
"v_mfma_f32_16x16x32_bf16 v[116:119], a[40:43], a[192:195], v[116:119]\n\t"
"v_add_u32 v240, 59904, v240\n\t"
"v_add_u32 v241, 59904, v241\n\t"
"v_mfma_f32_16x16x32_bf16 v[120:123], a[220:223], a[188:191], v[120:123]\n\t"
"ds_read_b64_tr_b16 v[224:225], v240 offset:1536\n\t"
"v_mfma_f32_16x16x32_bf16 v[120:123], a[248:251], a[192:195], v[120:123]\n\t"
"ds_read_b64_tr_b16 v[226:227], v240 offset:3072\n\t"
"v_mfma_f32_16x16x32_bf16 v[120:123], a[40:43], a[196:199], v[120:123]\n\t"
"s_waitcnt vmcnt(17)\n\t"
"ds_write_b128 v249, v[196:199] offset:18432\n\t"
"v_mfma_f32_16x16x32_bf16 v[124:127], a[220:223], a[192:195], v[124:127]\n\t"
"v_mfma_f32_16x16x32_bf16 v[124:127], a[248:251], a[196:199], v[124:127]\n\t"
"v_mfma_f32_16x16x32_bf16 v[124:127], a[40:43], a[200:203], v[124:127]\n\t"
"s_mov_b64 exec, s[44:45]\n\t"
"ds_write_b128 v242, v[232:235] offset:1536\n\t"
"s_mov_b64 exec, -1\n\t"
"s_waitcnt lgkmcnt(6)\n\t"
"v_mfma_f32_16x16x32_bf16 v[228:231], v[228:231], a[52:55], 0\n\t"
"ds_read_b64_tr_b16 v[232:233], v241 offset:1536\n\t"
"ds_read_b64_tr_b16 v[234:235], v241 offset:3072\n\t"
"s_waitcnt lgkmcnt(6)\n\t"
"v_mfma_f32_16x16x32_bf16 v[236:239], v[236:239], a[52:55], 0\n\t"
"ds_write_b64 v250, a[104:105] offset:18432\n\t"
"v_mfma_f32_16x16x32_bf16 v[128:131], a[220:223], a[196:199], v[128:131]\n\t"
"v_mfma_f32_16x16x32_bf16 v[128:131], a[248:251], a[200:203], v[128:131]\n\t"
"v_mfma_f32_16x16x32_bf16 v[128:131], a[40:43], a[204:207], v[128:131]\n\t"
"s_mov_b32 s20, 1069056\n\t"
"buffer_load_dwordx4 a[220:223], v252, s[8:11], s20 offen offset:0 sc1\n\t"
"v_mfma_f32_16x16x32_bf16 v[132:135], a[44:47], a[168:171], v[132:135]\n\t"
"v_cvt_pk_bf16_f32 v228, v228, v229\n\t"
"v_cvt_pk_bf16_f32 v229, v230, v231\n\t"
"v_mfma_f32_16x16x32_bf16 v[132:135], a[48:51], a[172:175], v[132:135]\n\t"
"v_cvt_pk_bf16_f32 v230, v236, v237\n\t"
"v_cvt_pk_bf16_f32 v231, v238, v239\n\t"
"v_mfma_f32_16x16x32_bf16 v[132:135], a[56:59], a[176:179], v[132:135]\n\t"
"buffer_load_dwordx4 a[248:251], v252, s[8:11], s20 offen offset:1024 sc1\n\t"
"s_waitcnt lgkmcnt(5)\n\t"
"v_mfma_f32_16x16x32_bf16 v[224:227], v[224:227], a[52:55], 0\n\t"
"s_mov_b64 exec, s[44:45]\n\t"
"ds_write_b128 v242, v[228:231] offset:3072\n\t"
"s_mov_b64 exec, -1\n\t"
"s_waitcnt lgkmcnt(2)\n\t"
"v_mfma_f32_16x16x32_bf16 v[232:235], v[232:235], a[52:55], 0\n\t"
"ds_read_b64_tr_b16 v[236:237], v240 offset:21504\n\t"
"ds_read_b64_tr_b16 v[238:239], v240 offset:23040\n\t"
"v_mfma_f32_16x16x32_bf16 v[136:139], a[44:47], a[172:175], v[136:139]\n\t"
"ds_read_b64_tr_b16 v[228:229], v241 offset:21504\n\t"
"v_mfma_f32_16x16x32_bf16 v[136:139], a[48:51], a[176:179], v[136:139]\n\t"
"ds_read_b64_tr_b16 v[230:231], v241 offset:23040\n\t"
"v_mfma_f32_16x16x32_bf16 v[136:139], a[56:59], a[180:183], v[136:139]\n\t"
"ds_write_b128 v249, v[200:203] offset:24576\n\t"
"v_mfma_f32_16x16x32_bf16 v[140:143], a[44:47], a[176:179], v[140:143]\n\t"
"v_cvt_pk_bf16_f32 v224, v224, v225\n\t"
"v_cvt_pk_bf16_f32 v225, v226, v227\n\t"
"v_mfma_f32_16x16x32_bf16 v[140:143], a[48:51], a[180:183], v[140:143]\n\t"
"v_cvt_pk_bf16_f32 v226, v232, v233\n\t"
"v_cvt_pk_bf16_f32 v227, v234, v235\n\t"
"v_mfma_f32_16x16x32_bf16 v[140:143], a[56:59], a[184:187], v[140:143]\n\t"
"s_mov_b64 exec, s[44:45]\n\t"
"ds_write_b128 v242, v[224:227] offset:4608\n\t"
"s_mov_b64 exec, -1\n\t"
"v_mfma_f32_16x16x32_bf16 v[144:147], a[44:47], a[180:183], v[144:147]\n\t"
"ds_read_b64_tr_b16 v[232:233], v240 offset:41472\n\t"
"v_mfma_f32_16x16x32_bf16 v[144:147], a[48:51], a[184:187], v[144:147]\n\t"
"ds_read_b64_tr_b16 v[234:235], v240 offset:43008\n\t"
"v_mfma_f32_16x16x32_bf16 v[144:147], a[56:59], a[188:191], v[144:147]\n\t"
"ds_read_b64_tr_b16 v[224:225], v241 offset:41472\n\t"
"ds_read_b64_tr_b16 v[226:227], v241 offset:43008\n\t"
"v_mfma_f32_16x16x32_bf16 v[148:151], a[44:47], a[184:187], v[148:151]\n\t"
"v_mfma_f32_16x16x32_bf16 v[148:151], a[48:51], a[188:191], v[148:151]\n\t"
"v_mfma_f32_16x16x32_bf16 v[148:151], a[56:59], a[192:195], v[148:151]\n\t"
"ds_write_b64 v250, a[106:107] offset:24576\n\t"
"s_waitcnt lgkmcnt(9)\n\t"
"v_mfma_f32_16x16x32_bf16 v[236:239], v[236:239], a[52:55], 0\n\t"
"s_waitcnt lgkmcnt(7)\n\t"
"v_mfma_f32_16x16x32_bf16 v[228:231], v[228:231], a[52:55], 0\n\t"
"v_mfma_f32_16x16x32_bf16 v[152:155], a[44:47], a[188:191], v[152:155]\n\t"
"v_mfma_f32_16x16x32_bf16 v[152:155], a[48:51], a[192:195], v[152:155]\n\t"
"v_mfma_f32_16x16x32_bf16 v[152:155], a[56:59], a[196:199], v[152:155]\n\t"
"v_mfma_f32_16x16x32_bf16 v[156:159], a[44:47], a[192:195], v[156:159]\n\t"
"s_nop 1\n\t"
"v_cvt_pk_bf16_f32 v236, v236, v237\n\t"
"v_cvt_pk_bf16_f32 v237, v238, v239\n\t"
"v_mfma_f32_16x16x32_bf16 v[156:159], a[48:51], a[196:199], v[156:159]\n\t"
"v_cvt_pk_bf16_f32 v238, v228, v229\n\t"
"v_mfma_f32_16x16x32_bf16 v[156:159], a[56:59], a[200:203], v[156:159]\n\t"
"v_cvt_pk_bf16_f32 v239, v230, v231\n\t"
"s_waitcnt lgkmcnt(3)\n\t"
"v_mfma_f32_16x16x32_bf16 v[232:235], v[232:235], a[52:55], 0\n\t"
"s_waitcnt lgkmcnt(1)\n\t"
"v_mfma_f32_16x16x32_bf16 v[224:227], v[224:227], a[52:55], 0\n\t"
"s_mov_b64 exec, s[44:45]\n\t"
"ds_write_b128 v242, v[236:239] offset:6144\n\t"
"s_mov_b64 exec, -1\n\t"
"v_mfma_f32_16x16x32_bf16 v[160:163], a[44:47], a[196:199], v[160:163]\n\t"
"v_mfma_f32_16x16x32_bf16 v[160:163], a[48:51], a[200:203], v[160:163]\n\t"
"v_mfma_f32_16x16x32_bf16 v[160:163], a[56:59], a[204:207], v[160:163]\n\t"
"v_mfma_f32_16x16x32_bf16 v[164:167], a[60:63], a[168:171], v[164:167]\n\t"
"v_cvt_pk_bf16_f32 v232, v232, v233\n\t"
"v_cvt_pk_bf16_f32 v233, v234, v235\n\t"
"v_mfma_f32_16x16x32_bf16 v[164:167], a[252:255], a[172:175], v[164:167]\n\t"
"v_cvt_pk_bf16_f32 v234, v224, v225\n\t"
"v_cvt_pk_bf16_f32 v235, v226, v227\n\t"
"v_mfma_f32_16x16x32_bf16 v[164:167], a[224:227], a[176:179], v[164:167]\n\t"
"buffer_load_dwordx4 a[56:59], v252, s[8:11], s20 offen offset:2048 sc1\n\t"
"v_mfma_f32_16x16x32_bf16 v[168:171], a[60:63], a[172:175], v[168:171]\n\t"
"v_mfma_f32_16x16x32_bf16 v[168:171], a[252:255], a[176:179], v[168:171]\n\t"
"v_mfma_f32_16x16x32_bf16 v[168:171], a[224:227], a[180:183], v[168:171]\n\t"
"s_mov_b64 exec, s[44:45]\n\t"
"ds_write_b128 v242, v[232:235] offset:7680\n\t"
"s_mov_b64 exec, -1\n\t"
"v_mfma_f32_16x16x32_bf16 v[172:175], a[60:63], a[176:179], v[172:175]\n\t"
"v_mfma_f32_16x16x32_bf16 v[172:175], a[252:255], a[180:183], v[172:175]\n\t"
"v_mfma_f32_16x16x32_bf16 v[172:175], a[224:227], a[184:187], v[172:175]\n\t"
"v_mfma_f32_16x16x32_bf16 v[176:179], a[60:63], a[180:183], v[176:179]\n\t"
"v_mfma_f32_16x16x32_bf16 v[176:179], a[252:255], a[184:187], v[176:179]\n\t"
"v_mfma_f32_16x16x32_bf16 v[176:179], a[224:227], a[188:191], v[176:179]\n\t"
"v_mfma_f32_16x16x32_bf16 v[180:183], a[60:63], a[184:187], v[180:183]\n\t"
"v_mfma_f32_16x16x32_bf16 v[180:183], a[252:255], a[188:191], v[180:183]\n\t"
"v_mfma_f32_16x16x32_bf16 v[180:183], a[224:227], a[192:195], v[180:183]\n\t"
"v_mfma_f32_16x16x32_bf16 v[184:187], a[60:63], a[188:191], v[184:187]\n\t"
"v_mfma_f32_16x16x32_bf16 v[184:187], a[252:255], a[192:195], v[184:187]\n\t"
"v_mfma_f32_16x16x32_bf16 v[184:187], a[224:227], a[196:199], v[184:187]\n\t"
"s_waitcnt lgkmcnt(0)\n\t"
"s_barrier\n\t"
"v_mfma_f32_16x16x32_bf16 v[188:191], a[60:63], a[192:195], v[188:191]\n\t"
"ds_read_b128 a[168:171], v244 offset:0\n\t"
"v_mfma_f32_16x16x32_bf16 v[188:191], a[252:255], a[196:199], v[188:191]\n\t"
"ds_read_b128 a[172:175], v244 offset:512\n\t"
"v_mfma_f32_16x16x32_bf16 v[188:191], a[224:227], a[200:203], v[188:191]\n\t"
"ds_read_b128 a[176:179], v244 offset:1024\n\t"
"ds_read_b128 a[180:183], v244 offset:1536\n\t"
"ds_read_b128 a[184:187], v244 offset:2048\n\t"
"ds_read_b128 a[40:43], v252 offset:18432\n\t"
"ds_read_b128 a[188:191], v244 offset:2560\n\t"
"v_mfma_f32_16x16x32_bf16 v[192:195], a[60:63], a[196:199], v[192:195]\n\t"
"ds_read_b128 a[44:47], v252 offset:19456\n\t"
"v_mfma_f32_16x16x32_bf16 v[192:195], a[252:255], a[200:203], v[192:195]\n\t"
"ds_read_b128 a[192:195], v244 offset:3072\n\t"
"v_mfma_f32_16x16x32_bf16 v[192:195], a[224:227], a[204:207], v[192:195]\n\t"
"s_mov_b32 s20, 1081344\n\t"
"buffer_load_dwordx4 a[104:107], v252, s[8:11], s20 offen offset:0 sc1\n\t"
"ds_read_b128 a[48:51], v252 offset:20480\n\t"
"ds_read_b128 a[196:199], v244 offset:3584\n\t"
"Lst33_%=:\n\t"
"s_waitcnt lgkmcnt(8)\n\t"
"v_mfma_f32_16x16x32_bf16 v[0:3], a[64:67], a[168:171], v[0:3]\n\t"
"ds_read_b128 a[200:203], v244 offset:4096\n\t"
"v_mfma_f32_16x16x32_bf16 v[0:3], a[68:71], a[172:175], v[0:3]\n\t"
"ds_read_b128 a[204:207], v244 offset:4608\n\t"
"v_mfma_f32_16x16x32_bf16 v[0:3], a[72:75], a[176:179], v[0:3]\n\t"
"buffer_load_dwordx4 a[252:255], v252, s[8:11], s20 offen offset:1024 sc1\n\t"
"ds_write_b128 v249, v[204:207] offset:30720\n\t"
"s_waitcnt lgkmcnt(10)\n\t"
"v_mfma_f32_16x16x32_bf16 v[4:7], a[64:67], a[172:175], v[4:7]\n\t"
"v_mfma_f32_16x16x32_bf16 v[4:7], a[68:71], a[176:179], v[4:7]\n\t"
"v_mfma_f32_16x16x32_bf16 v[4:7], a[72:75], a[180:183], v[4:7]\n\t"
"buffer_load_dwordx4 a[224:227], v252, s[8:11], s20 offen offset:2048 sc1\n\t"
"s_mov_b32 s20, 1093632\n\t"
"buffer_load_dwordx4 a[60:63], v252, s[8:11], s20 offen offset:0 sc1\n\t"
"s_waitcnt lgkmcnt(9)\n\t"
"v_mfma_f32_16x16x32_bf16 v[8:11], a[64:67], a[176:179], v[8:11]\n\t"
"v_mfma_f32_16x16x32_bf16 v[8:11], a[68:71], a[180:183], v[8:11]\n\t"
"v_mfma_f32_16x16x32_bf16 v[8:11], a[72:75], a[184:187], v[8:11]\n\t"
"s_add_u32 s29, s22, 384\n\t"
"s_waitcnt lgkmcnt(8)\n\t"
"buffer_store_dwordx4 a[40:43], v248, s[16:19], s29 offen offset:0 nt\n\t"
"ds_write_b64 v250, a[112:113] offset:30720\n\t"
"s_waitcnt lgkmcnt(8)\n\t"
"v_mfma_f32_16x16x32_bf16 v[12:15], a[64:67], a[180:183], v[12:15]\n\t"
"v_mfma_f32_16x16x32_bf16 v[12:15], a[68:71], a[184:187], v[12:15]\n\t"
"v_mfma_f32_16x16x32_bf16 v[12:15], a[72:75], a[188:191], v[12:15]\n\t"
"s_add_u32 s29, s22, 384\n\t"
"s_waitcnt lgkmcnt(7)\n\t"
"buffer_store_dwordx4 a[44:47], v248, s[16:19], s29 offen offset:128 nt\n\t"
"s_waitcnt lgkmcnt(6)\n\t"
"v_mfma_f32_16x16x32_bf16 v[16:19], a[64:67], a[184:187], v[16:19]\n\t"
"v_mfma_f32_16x16x32_bf16 v[16:19], a[68:71], a[188:191], v[16:19]\n\t"
"v_mfma_f32_16x16x32_bf16 v[16:19], a[72:75], a[192:195], v[16:19]\n\t"
"s_add_u32 s29, s22, 384\n\t"
"s_waitcnt lgkmcnt(5)\n\t"
"buffer_store_dwordx4 a[48:51], v248, s[16:19], s29 offen offset:256 nt\n\t"
"s_waitcnt lgkmcnt(4)\n\t"
"v_mfma_f32_16x16x32_bf16 v[20:23], a[64:67], a[188:191], v[20:23]\n\t"
"v_mfma_f32_16x16x32_bf16 v[20:23], a[68:71], a[192:195], v[20:23]\n\t"
"v_mfma_f32_16x16x32_bf16 v[20:23], a[72:75], a[196:199], v[20:23]\n\t"
"buffer_load_dwordx4 a[44:47], v252, s[8:11], s20 offen offset:1024 sc1\n\t"
"s_waitcnt lgkmcnt(3)\n\t"
"v_mfma_f32_16x16x32_bf16 v[24:27], a[64:67], a[192:195], v[24:27]\n\t"
"v_mfma_f32_16x16x32_bf16 v[24:27], a[68:71], a[196:199], v[24:27]\n\t"
"v_mfma_f32_16x16x32_bf16 v[24:27], a[72:75], a[200:203], v[24:27]\n\t"
"ds_write_b128 v249, v[208:211] offset:36864\n\t"
"s_waitcnt lgkmcnt(3)\n\t"
"v_mfma_f32_16x16x32_bf16 v[28:31], a[64:67], a[196:199], v[28:31]\n\t"
"v_mfma_f32_16x16x32_bf16 v[28:31], a[68:71], a[200:203], v[28:31]\n\t"
"v_mfma_f32_16x16x32_bf16 v[28:31], a[72:75], a[204:207], v[28:31]\n\t"
"buffer_load_dwordx4 a[48:51], v252, s[8:11], s20 offen offset:2048 sc1\n\t"
"v_mfma_f32_16x16x32_bf16 v[32:35], a[76:79], a[168:171], v[32:35]\n\t"
"v_mfma_f32_16x16x32_bf16 v[32:35], a[80:83], a[172:175], v[32:35]\n\t"
"v_mfma_f32_16x16x32_bf16 v[32:35], a[84:87], a[176:179], v[32:35]\n\t"
"s_mov_b32 s20, 1105920\n\t"
"buffer_load_dwordx4 a[64:67], v252, s[8:11], s20 offen offset:0 sc1\n\t"
"v_mfma_f32_16x16x32_bf16 v[36:39], a[76:79], a[172:175], v[36:39]\n\t"
"ds_read_b64_tr_b16 v[228:229], v240 offset:512\n\t"
"v_mfma_f32_16x16x32_bf16 v[36:39], a[80:83], a[176:179], v[36:39]\n\t"
"ds_read_b64_tr_b16 v[230:231], v240 offset:2048\n\t"
"v_mfma_f32_16x16x32_bf16 v[36:39], a[84:87], a[180:183], v[36:39]\n\t"
"buffer_load_dwordx4 a[68:71], v252, s[8:11], s20 offen offset:1024 sc1\n\t"
"ds_write_b64 v250, a[114:115] offset:36864\n\t"
"v_mfma_f32_16x16x32_bf16 v[40:43], a[76:79], a[176:179], v[40:43]\n\t"
"v_mfma_f32_16x16x32_bf16 v[40:43], a[80:83], a[180:183], v[40:43]\n\t"
"v_mfma_f32_16x16x32_bf16 v[40:43], a[84:87], a[184:187], v[40:43]\n\t"
"buffer_load_dwordx4 a[72:75], v252, s[8:11], s20 offen offset:2048 sc1\n\t"
"v_mfma_f32_16x16x32_bf16 v[44:47], a[76:79], a[180:183], v[44:47]\n\t"
"v_mfma_f32_16x16x32_bf16 v[44:47], a[80:83], a[184:187], v[44:47]\n\t"
"v_mfma_f32_16x16x32_bf16 v[44:47], a[84:87], a[188:191], v[44:47]\n\t"
"s_waitcnt lgkmcnt(0)\n\t"
"s_barrier\n\t"
"v_mfma_f32_16x16x32_bf16 v[48:51], a[76:79], a[184:187], v[48:51]\n\t"
"ds_read_b128 v[196:199], v252 offset:30720\n\t"
"v_mfma_f32_16x16x32_bf16 v[48:51], a[80:83], a[188:191], v[48:51]\n\t"
"ds_read_b64_tr_b16 v[236:237], v241 offset:512\n\t"
"v_mfma_f32_16x16x32_bf16 v[48:51], a[84:87], a[192:195], v[48:51]\n\t"
"ds_read_b64_tr_b16 v[238:239], v241 offset:2048\n\t"
"ds_read_b128 v[200:203], v252 offset:31744\n\t"
"v_mfma_f32_16x16x32_bf16 v[52:55], a[76:79], a[188:191], v[52:55]\n\t"
"ds_read_b128 a[40:43], v252 offset:32768\n\t"
"v_mfma_f32_16x16x32_bf16 v[52:55], a[80:83], a[192:195], v[52:55]\n\t"
"ds_read_b64_tr_b16 v[224:225], v240 offset:20480\n\t"
"v_mfma_f32_16x16x32_bf16 v[52:55], a[84:87], a[196:199], v[52:55]\n\t"
"s_mov_b32 s20, 1118208\n\t"
"buffer_load_dwordx4 v[208:211], v252, s[8:11], s20 offen offset:0 sc1\n\t"
"v_mfma_f32_16x16x32_bf16 v[228:231], v[228:231], a[52:55], 0\n\t"
"buffer_load_dwordx4 a[112:115], v252, s[8:11], s20 offen offset:1024 sc1\n\t"
"v_mfma_f32_16x16x32_bf16 v[56:59], a[76:79], a[192:195], v[56:59]\n\t"
"ds_read_b64_tr_b16 v[226:227], v240 offset:22016\n\t"
"v_mfma_f32_16x16x32_bf16 v[56:59], a[80:83], a[196:199], v[56:59]\n\t"
"ds_read_b64_tr_b16 v[232:233], v241 offset:20480\n\t"
"v_mfma_f32_16x16x32_bf16 v[56:59], a[84:87], a[200:203], v[56:59]\n\t"
"ds_write_b128 v249, v[212:215] offset:18432\n\t"
"s_waitcnt lgkmcnt(6)\n\t"
"v_mfma_f32_16x16x32_bf16 v[236:239], v[236:239], a[52:55], 0\n\t"
"s_add_u32 s29, s22, 196992\n\t"
"buffer_store_dwordx4 v[196:199], v248, s[16:19], s29 offen offset:0 nt\n\t"
"v_mfma_f32_16x16x32_bf16 v[60:63], a[76:79], a[196:199], v[60:63]\n\t"
"v_cvt_pk_bf16_f32 v228, v228, v229\n\t"
"v_cvt_pk_bf16_f32 v229, v230, v231\n\t"
"v_mfma_f32_16x16x32_bf16 v[60:63], a[80:83], a[200:203], v[60:63]\n\t"
"ds_read_b64_tr_b16 v[234:235], v241 offset:22016\n\t"
"v_mfma_f32_16x16x32_bf16 v[60:63], a[84:87], a[204:207], v[60:63]\n\t"
"buffer_load_dwordx4 a[76:79], v252, s[8:11], s20 offen offset:2048 sc1\n\t"
"v_mfma_f32_16x16x32_bf16 v[64:67], a[208:211], a[168:171], v[64:67]\n\t"
"ds_read_b128 a[168:171], v244 offset:9216\n\t"
"v_mfma_f32_16x16x32_bf16 v[64:67], a[212:215], a[172:175], v[64:67]\n\t"
"v_cvt_pk_bf16_f32 v230, v236, v237\n\t"
"v_cvt_pk_bf16_f32 v231, v238, v239\n\t"
"v_mfma_f32_16x16x32_bf16 v[64:67], a[88:91], a[176:179], v[64:67]\n\t"
"s_mov_b32 s20, 1130496\n\t"
"buffer_load_dwordx4 a[80:83], v252, s[8:11], s20 offen offset:0 sc1\n\t"
"v_mfma_f32_16x16x32_bf16 v[68:71], a[208:211], a[172:175], v[68:71]\n\t"
"ds_read_b128 a[172:175], v244 offset:9728\n\t"
"v_mfma_f32_16x16x32_bf16 v[68:71], a[212:215], a[176:179], v[68:71]\n\t"
"v_mfma_f32_16x16x32_bf16 v[68:71], a[88:91], a[180:183], v[68:71]\n\t"
"s_add_u32 s29, s22, 196992\n\t"
"s_waitcnt lgkmcnt(8)\n\t"
"buffer_store_dwordx4 v[200:203], v248, s[16:19], s29 offen offset:128 nt\n\t"
"ds_write_b64 v250, a[120:121] offset:18432\n\t"
"s_waitcnt lgkmcnt(6)\n\t"
"v_mfma_f32_16x16x32_bf16 v[224:227], v[224:227], a[52:55], 0\n\t"
"buffer_load_dwordx4 a[84:87], v252, s[8:11], s20 offen offset:1024 sc1\n\t"
"v_mfma_f32_16x16x32_bf16 v[72:75], a[208:211], a[176:179], v[72:75]\n\t"
"ds_read_b128 a[176:179], v244 offset:10240\n\t"
"v_mfma_f32_16x16x32_bf16 v[72:75], a[212:215], a[180:183], v[72:75]\n\t"
"v_mfma_f32_16x16x32_bf16 v[72:75], a[88:91], a[184:187], v[72:75]\n\t"
"s_add_u32 s29, s22, 196992\n\t"
"buffer_store_dwordx4 a[40:43], v248, s[16:19], s29 offen offset:256 nt\n\t"
"s_waitcnt lgkmcnt(4)\n\t"
"v_mfma_f32_16x16x32_bf16 v[232:235], v[232:235], a[52:55], 0\n\t"
"buffer_load_dwordx4 a[40:43], v252, s[8:11], s20 offen offset:2048 sc1\n\t"
"v_mfma_f32_16x16x32_bf16 v[76:79], a[208:211], a[180:183], v[76:79]\n\t"
"v_cvt_pk_bf16_f32 v224, v224, v225\n\t"
"v_cvt_pk_bf16_f32 v225, v226, v227\n\t"
"v_mfma_f32_16x16x32_bf16 v[76:79], a[212:215], a[184:187], v[76:79]\n\t"
"ds_read_b128 a[180:183], v244 offset:10752\n\t"
"v_mfma_f32_16x16x32_bf16 v[76:79], a[88:91], a[188:191], v[76:79]\n\t"
"ds_write_b128 v249, v[216:219] offset:24576\n\t"
"v_mfma_f32_16x16x32_bf16 v[80:83], a[208:211], a[184:187], v[80:83]\n\t"
"ds_read_b128 a[184:187], v244 offset:11264\n\t"
"v_mfma_f32_16x16x32_bf16 v[80:83], a[212:215], a[188:191], v[80:83]\n\t"
"v_cvt_pk_bf16_f32 v226, v232, v233\n\t"
"v_cvt_pk_bf16_f32 v227, v234, v235\n\t"
"v_mfma_f32_16x16x32_bf16 v[80:83], a[88:91], a[192:195], v[80:83]\n\t"
"ds_write_b64 v250, a[122:123] offset:24576\n\t"
"v_mfma_f32_16x16x32_bf16 v[84:87], a[208:211], a[188:191], v[84:87]\n\t"
"ds_read_b128 a[188:191], v244 offset:11776\n\t"
"v_mfma_f32_16x16x32_bf16 v[84:87], a[212:215], a[192:195], v[84:87]\n\t"
"v_mfma_f32_16x16x32_bf16 v[84:87], a[88:91], a[196:199], v[84:87]\n\t"
"v_mfma_f32_16x16x32_bf16 v[88:91], a[208:211], a[192:195], v[88:91]\n\t"
"ds_read_b128 a[192:195], v244 offset:12288\n\t"
"v_mfma_f32_16x16x32_bf16 v[88:91], a[212:215], a[196:199], v[88:91]\n\t"
"v_mfma_f32_16x16x32_bf16 v[88:91], a[88:91], a[200:203], v[88:91]\n\t"
"v_mfma_f32_16x16x32_bf16 v[92:95], a[208:211], a[196:199], v[92:95]\n\t"
"ds_read_b128 a[196:199], v244 offset:12800\n\t"
"v_mfma_f32_16x16x32_bf16 v[92:95], a[212:215], a[200:203], v[92:95]\n\t"
"ds_read_b128 a[200:203], v244 offset:13312\n\t"
"v_mfma_f32_16x16x32_bf16 v[92:95], a[88:91], a[204:207], v[92:95]\n\t"
"s_mov_b32 s20, 1290240\n\t"
"buffer_load_dwordx4 a[208:211], v252, s[8:11], s20 offen offset:0 sc1\n\t"
"s_waitcnt vmcnt(40) lgkmcnt(8)\n\t"
"v_mfma_f32_16x16x32_bf16 v[100:103], a[92:95], a[168:171], v[100:103]\n\t"
"ds_read_b128 a[204:207], v244 offset:13824\n\t"
"v_mfma_f32_16x16x32_bf16 v[100:103], a[96:99], a[172:175], v[100:103]\n\t"
"v_mfma_f32_16x16x32_bf16 v[100:103], a[100:103], a[176:179], v[100:103]\n\t"
"buffer_load_dwordx4 a[212:215], v252, s[8:11], s20 offen offset:1024 sc1\n\t"
"s_waitcnt lgkmcnt(8)\n\t"
"v_mfma_f32_16x16x32_bf16 v[104:107], a[92:95], a[172:175], v[104:107]\n\t"
"v_mfma_f32_16x16x32_bf16 v[104:107], a[96:99], a[176:179], v[104:107]\n\t"
"v_mfma_f32_16x16x32_bf16 v[104:107], a[100:103], a[180:183], v[104:107]\n\t"
"buffer_load_dwordx4 a[88:91], v252, s[8:11], s20 offen offset:2048 sc1\n\t"
"s_waitcnt lgkmcnt(0)\n\t"
"s_barrier\n\t"
"v_mfma_f32_16x16x32_bf16 v[108:111], a[92:95], a[176:179], v[108:111]\n\t"
"ds_read_b128 v[196:199], v252 offset:18432\n\t"
"v_mfma_f32_16x16x32_bf16 v[108:111], a[96:99], a[180:183], v[108:111]\n\t"
"ds_read_b64_tr_b16 v[236:237], v240 offset:40448\n\t"
"v_mfma_f32_16x16x32_bf16 v[108:111], a[100:103], a[184:187], v[108:111]\n\t"
"s_mov_b64 exec, s[44:45]\n\t"
"ds_write_b128 v242, v[228:231] offset:4608\n\t"
"s_mov_b64 exec, -1\n\t"
"s_mov_b64 exec, s[44:45]\n\t"
"ds_write_b128 v242, v[224:227] offset:6144\n\t"
"s_mov_b64 exec, -1\n\t"
"v_mfma_f32_16x16x32_bf16 v[112:115], a[92:95], a[180:183], v[112:115]\n\t"
"ds_read_b64_tr_b16 v[238:239], v240 offset:41984\n\t"
"v_mfma_f32_16x16x32_bf16 v[112:115], a[96:99], a[184:187], v[112:115]\n\t"
"ds_read_b128 v[200:203], v252 offset:19456\n\t"
"v_mfma_f32_16x16x32_bf16 v[112:115], a[100:103], a[188:191], v[112:115]\n\t"
"s_mov_b32 s20, 1302528\n\t"
"buffer_load_dwordx4 v[212:215], v252, s[8:11], s20 offen offset:0 sc1\n\t"
"ds_read_b64_tr_b16 v[232:233], v241 offset:40448\n\t"
"ds_read_b64_tr_b16 v[234:235], v241 offset:41984\n\t"
"v_mfma_f32_16x16x32_bf16 v[116:119], a[92:95], a[184:187], v[116:119]\n\t"
"ds_read_b128 v[204:207], v252 offset:20480\n\t"
"v_subrev_u32 v240, 59904, v240\n\t"
"v_mfma_f32_16x16x32_bf16 v[116:119], a[96:99], a[188:191], v[116:119]\n\t"
"v_subrev_u32 v241, 59904, v241\n\t"
"ds_read_b64_tr_b16 v[228:229], v240 offset:512\n\t"
"v_mfma_f32_16x16x32_bf16 v[116:119], a[100:103], a[192:195], v[116:119]\n\t"
"buffer_load_dwordx4 v[216:219], v252, s[8:11], s20 offen offset:1024 sc1\n\t"
"buffer_load_dwordx4 a[120:123], v252, s[8:11], s20 offen offset:2048 sc1\n\t"
"v_mfma_f32_16x16x32_bf16 v[120:123], a[92:95], a[188:191], v[120:123]\n\t"
"ds_read_b64_tr_b16 v[230:231], v240 offset:2048\n\t"
"v_mfma_f32_16x16x32_bf16 v[120:123], a[96:99], a[192:195], v[120:123]\n\t"
"ds_read_b64_tr_b16 v[224:225], v241 offset:512\n\t"
"v_mfma_f32_16x16x32_bf16 v[120:123], a[100:103], a[196:199], v[120:123]\n\t"
"ds_read_b64_tr_b16 v[226:227], v241 offset:2048\n\t"
"s_waitcnt lgkmcnt(8)\n\t"
"v_mfma_f32_16x16x32_bf16 v[236:239], v[236:239], a[52:55], 0\n\t"
"ds_write_b128 v249, a[0:3] offset:30720\n\t"
"s_waitcnt lgkmcnt(6)\n\t"
"v_mfma_f32_16x16x32_bf16 v[232:235], v[232:235], a[52:55], 0\n\t"
"s_add_u32 s29, s22, 393600\n\t"
"buffer_store_dwordx4 v[196:199], v248, s[16:19], s29 offen offset:0 nt\n\t"
"v_mfma_f32_16x16x32_bf16 v[124:127], a[92:95], a[192:195], v[124:127]\n\t"
"v_mfma_f32_16x16x32_bf16 v[124:127], a[96:99], a[196:199], v[124:127]\n\t"
"v_mfma_f32_16x16x32_bf16 v[124:127], a[100:103], a[200:203], v[124:127]\n\t"
"s_add_u32 s29, s22, 393600\n\t"
"buffer_store_dwordx4 v[200:203], v248, s[16:19], s29 offen offset:128 nt\n\t"
"v_mfma_f32_16x16x32_bf16 v[128:131], a[92:95], a[196:199], v[128:131]\n\t"
"v_cvt_pk_bf16_f32 v236, v236, v237\n\t"
"v_cvt_pk_bf16_f32 v237, v238, v239\n\t"
"v_mfma_f32_16x16x32_bf16 v[128:131], a[96:99], a[200:203], v[128:131]\n\t"
"v_cvt_pk_bf16_f32 v238, v232, v233\n\t"
"v_cvt_pk_bf16_f32 v239, v234, v235\n\t"
"v_mfma_f32_16x16x32_bf16 v[128:131], a[100:103], a[204:207], v[128:131]\n\t"
"s_mov_b32 s20, 1314816\n\t"
"buffer_load_dwordx4 a[92:95], v252, s[8:11], s20 offen offset:0 sc1\n\t"
"s_waitcnt lgkmcnt(3)\n\t"
"v_mfma_f32_16x16x32_bf16 v[228:231], v[228:231], a[52:55], 0\n\t"
"s_add_u32 s29, s22, 393600\n\t"
"buffer_store_dwordx4 v[204:207], v248, s[16:19], s29 offen offset:256 nt\n\t"
"buffer_load_dwordx4 a[96:99], v252, s[8:11], s20 offen offset:1024 sc1\n\t"
"s_waitcnt lgkmcnt(1)\n\t"
"v_mfma_f32_16x16x32_bf16 v[224:227], v[224:227], a[52:55], 0\n\t"
"s_mov_b64 exec, s[44:45]\n\t"
"ds_write_b128 v242, v[236:239] offset:7680\n\t"
"s_mov_b64 exec, -1\n\t"
"s_waitcnt vmcnt(47)\n\t"
"v_mfma_f32_16x16x32_bf16 v[132:135], a[108:111], a[168:171], v[132:135]\n\t"
"ds_read_b64_tr_b16 v[232:233], v240 offset:20480\n\t"
"v_mfma_f32_16x16x32_bf16 v[132:135], a[116:119], a[172:175], v[132:135]\n\t"
"ds_read_b64_tr_b16 v[234:235], v240 offset:22016\n\t"
"v_mfma_f32_16x16x32_bf16 v[132:135], a[216:219], a[176:179], v[132:135]\n\t"
"buffer_load_dwordx4 a[100:103], v252, s[8:11], s20 offen offset:2048 sc1\n\t"
"v_mfma_f32_16x16x32_bf16 v[136:139], a[108:111], a[172:175], v[136:139]\n\t"
"ds_read_b64_tr_b16 v[236:237], v241 offset:20480\n\t"
"v_cvt_pk_bf16_f32 v228, v228, v229\n\t"
"v_mfma_f32_16x16x32_bf16 v[136:139], a[116:119], a[176:179], v[136:139]\n\t"
"ds_read_b64_tr_b16 v[238:239], v241 offset:22016\n\t"
"v_cvt_pk_bf16_f32 v229, v230, v231\n\t"
"v_mfma_f32_16x16x32_bf16 v[136:139], a[216:219], a[180:183], v[136:139]\n\t"
"ds_write_b64 v250, a[128:129] offset:30720\n\t"
"v_cvt_pk_bf16_f32 v230, v224, v225\n\t"
"v_cvt_pk_bf16_f32 v231, v226, v227\n\t"
"v_mfma_f32_16x16x32_bf16 v[140:143], a[108:111], a[176:179], v[140:143]\n\t"
"ds_read_b64_tr_b16 v[224:225], v240 offset:40448\n\t"
"v_mfma_f32_16x16x32_bf16 v[140:143], a[116:119], a[180:183], v[140:143]\n\t"
"ds_read_b64_tr_b16 v[226:227], v240 offset:41984\n\t"
"v_mfma_f32_16x16x32_bf16 v[140:143], a[216:219], a[184:187], v[140:143]\n\t"
"s_mov_b64 exec, s[44:45]\n\t"
"ds_write_b128 v242, v[228:231] offset:0\n\t"
"s_mov_b64 exec, -1\n\t"
"v_mfma_f32_16x16x32_bf16 v[144:147], a[108:111], a[180:183], v[144:147]\n\t"
"ds_read_b64_tr_b16 v[228:229], v241 offset:40448\n\t"
"v_mfma_f32_16x16x32_bf16 v[144:147], a[116:119], a[184:187], v[144:147]\n\t"
"ds_read_b64_tr_b16 v[230:231], v241 offset:41984\n\t"
"v_mfma_f32_16x16x32_bf16 v[144:147], a[216:219], a[188:191], v[144:147]\n\t"
"ds_write_b128 v249, a[4:7] offset:36864\n\t"
"s_waitcnt lgkmcnt(9)\n\t"
"v_mfma_f32_16x16x32_bf16 v[232:235], v[232:235], a[52:55], 0\n\t"
"ds_write_b64 v250, a[130:131] offset:36864\n\t"
"s_waitcnt lgkmcnt(8)\n\t"
"v_mfma_f32_16x16x32_bf16 v[236:239], v[236:239], a[52:55], 0\n\t"
"v_mfma_f32_16x16x32_bf16 v[148:151], a[108:111], a[184:187], v[148:151]\n\t"
"v_mfma_f32_16x16x32_bf16 v[148:151], a[116:119], a[188:191], v[148:151]\n\t"
"v_mfma_f32_16x16x32_bf16 v[148:151], a[216:219], a[192:195], v[148:151]\n\t"
"v_mfma_f32_16x16x32_bf16 v[152:155], a[108:111], a[188:191], v[152:155]\n\t"
"s_nop 0\n\t"
"v_cvt_pk_bf16_f32 v232, v232, v233\n\t"
"v_cvt_pk_bf16_f32 v233, v234, v235\n\t"
"v_mfma_f32_16x16x32_bf16 v[152:155], a[116:119], a[192:195], v[152:155]\n\t"
"v_cvt_pk_bf16_f32 v234, v236, v237\n\t"
"v_mfma_f32_16x16x32_bf16 v[152:155], a[216:219], a[196:199], v[152:155]\n\t"
"v_cvt_pk_bf16_f32 v235, v238, v239\n\t"
"s_waitcnt lgkmcnt(5)\n\t"
"v_mfma_f32_16x16x32_bf16 v[224:227], v[224:227], a[52:55], 0\n\t"
"s_waitcnt lgkmcnt(2)\n\t"
"v_mfma_f32_16x16x32_bf16 v[228:231], v[228:231], a[52:55], 0\n\t"
"s_mov_b64 exec, s[44:45]\n\t"
"ds_write_b128 v242, v[232:235] offset:1536\n\t"
"s_mov_b64 exec, -1\n\t"
"v_mfma_f32_16x16x32_bf16 v[156:159], a[108:111], a[192:195], v[156:159]\n\t"
"v_mfma_f32_16x16x32_bf16 v[156:159], a[116:119], a[196:199], v[156:159]\n\t"
"v_mfma_f32_16x16x32_bf16 v[156:159], a[216:219], a[200:203], v[156:159]\n\t"
"v_mfma_f32_16x16x32_bf16 v[160:163], a[108:111], a[196:199], v[160:163]\n\t"
"v_cvt_pk_bf16_f32 v224, v224, v225\n\t"
"v_cvt_pk_bf16_f32 v225, v226, v227\n\t"
"v_mfma_f32_16x16x32_bf16 v[160:163], a[116:119], a[200:203], v[160:163]\n\t"
"v_cvt_pk_bf16_f32 v226, v228, v229\n\t"
"v_mfma_f32_16x16x32_bf16 v[160:163], a[216:219], a[204:207], v[160:163]\n\t"
"v_cvt_pk_bf16_f32 v227, v230, v231\n\t"
"s_waitcnt vmcnt(45)\n\t"
"v_mfma_f32_16x16x32_bf16 v[164:167], a[228:231], a[168:171], v[164:167]\n\t"
"v_mfma_f32_16x16x32_bf16 v[164:167], a[232:235], a[172:175], v[164:167]\n\t"
"v_mfma_f32_16x16x32_bf16 v[164:167], a[124:127], a[176:179], v[164:167]\n\t"
"s_mov_b64 exec, s[44:45]\n\t"
"ds_write_b128 v242, v[224:227] offset:3072\n\t"
"s_mov_b64 exec, -1\n\t"
"v_mfma_f32_16x16x32_bf16 v[168:171], a[228:231], a[172:175], v[168:171]\n\t"
"v_mfma_f32_16x16x32_bf16 v[168:171], a[232:235], a[176:179], v[168:171]\n\t"
"v_mfma_f32_16x16x32_bf16 v[168:171], a[124:127], a[180:183], v[168:171]\n\t"
"v_mfma_f32_16x16x32_bf16 v[172:175], a[228:231], a[176:179], v[172:175]\n\t"
"v_mfma_f32_16x16x32_bf16 v[172:175], a[232:235], a[180:183], v[172:175]\n\t"
"v_mfma_f32_16x16x32_bf16 v[172:175], a[124:127], a[184:187], v[172:175]\n\t"
"v_mfma_f32_16x16x32_bf16 v[176:179], a[228:231], a[180:183], v[176:179]\n\t"
"v_mfma_f32_16x16x32_bf16 v[176:179], a[232:235], a[184:187], v[176:179]\n\t"
"v_mfma_f32_16x16x32_bf16 v[176:179], a[124:127], a[188:191], v[176:179]\n\t"
"s_waitcnt lgkmcnt(0)\n\t"
"s_barrier\n\t"
"v_mfma_f32_16x16x32_bf16 v[180:183], a[228:231], a[184:187], v[180:183]\n\t"
"ds_read_b128 a[168:171], v244 offset:0\n\t"
"v_mfma_f32_16x16x32_bf16 v[180:183], a[232:235], a[188:191], v[180:183]\n\t"
"ds_read_b128 a[172:175], v244 offset:512\n\t"
"v_mfma_f32_16x16x32_bf16 v[180:183], a[124:127], a[192:195], v[180:183]\n\t"
"ds_read_b128 a[176:179], v244 offset:1024\n\t"
"ds_read_b128 v[196:199], v252 offset:30720\n\t"
"ds_read_b128 a[180:183], v244 offset:1536\n\t"
"v_mfma_f32_16x16x32_bf16 v[184:187], a[228:231], a[188:191], v[184:187]\n\t"
"ds_read_b128 v[200:203], v252 offset:31744\n\t"
"v_mfma_f32_16x16x32_bf16 v[184:187], a[232:235], a[192:195], v[184:187]\n\t"
"ds_read_b128 a[184:187], v244 offset:2048\n\t"
"v_mfma_f32_16x16x32_bf16 v[184:187], a[124:127], a[196:199], v[184:187]\n\t"
"ds_read_b128 v[204:207], v252 offset:32768\n\t"
"ds_read_b128 a[188:191], v244 offset:2560\n\t"
"v_mfma_f32_16x16x32_bf16 v[188:191], a[228:231], a[192:195], v[188:191]\n\t"
"ds_read_b128 a[192:195], v244 offset:3072\n\t"
"v_mfma_f32_16x16x32_bf16 v[188:191], a[232:235], a[196:199], v[188:191]\n\t"
"v_mfma_f32_16x16x32_bf16 v[188:191], a[124:127], a[200:203], v[188:191]\n\t"
"ds_write_b128 v249, a[8:11] offset:18432\n\t"
"v_mfma_f32_16x16x32_bf16 v[192:195], a[228:231], a[196:199], v[192:195]\n\t"
"ds_read_b128 a[196:199], v244 offset:3584\n\t"
"v_mfma_f32_16x16x32_bf16 v[192:195], a[232:235], a[200:203], v[192:195]\n\t"
"ds_read_b128 a[200:203], v244 offset:4096\n\t"
"v_mfma_f32_16x16x32_bf16 v[192:195], a[124:127], a[204:207], v[192:195]\n\t"
"s_add_u32 s29, s22, 590208\n\t"
"s_waitcnt lgkmcnt(9)\n\t"
"buffer_store_dwordx4 v[196:199], v248, s[16:19], s29 offen offset:0 nt\n\t"
"Lst34_%=:\n\t"
"s_waitcnt vmcnt(43)\n\t"
"v_mfma_f32_16x16x32_bf16 v[0:3], a[132:135], a[168:171], v[0:3]\n\t"
"ds_read_b128 a[204:207], v244 offset:4608\n\t"
"v_mfma_f32_16x16x32_bf16 v[0:3], a[140:143], a[172:175], v[0:3]\n\t"
"v_mfma_f32_16x16x32_bf16 v[0:3], a[148:151], a[176:179], v[0:3]\n\t"
"s_add_u32 s29, s22, 590208\n\t"
"s_waitcnt lgkmcnt(8)\n\t"
"buffer_store_dwordx4 v[200:203], v248, s[16:19], s29 offen offset:128 nt\n\t"
"v_mfma_f32_16x16x32_bf16 v[4:7], a[132:135], a[172:175], v[4:7]\n\t"
"v_mfma_f32_16x16x32_bf16 v[4:7], a[140:143], a[176:179], v[4:7]\n\t"
"v_mfma_f32_16x16x32_bf16 v[4:7], a[148:151], a[180:183], v[4:7]\n\t"
"ds_write_b64 v250, a[136:137] offset:18432\n\t"
"s_waitcnt lgkmcnt(8)\n\t"
"v_mfma_f32_16x16x32_bf16 v[8:11], a[132:135], a[176:179], v[8:11]\n\t"
"v_mfma_f32_16x16x32_bf16 v[8:11], a[140:143], a[180:183], v[8:11]\n\t"
"v_mfma_f32_16x16x32_bf16 v[8:11], a[148:151], a[184:187], v[8:11]\n\t"
"s_add_u32 s29, s22, 590208\n\t"
"s_waitcnt lgkmcnt(7)\n\t"
"buffer_store_dwordx4 v[204:207], v248, s[16:19], s29 offen offset:256 nt\n\t"
"s_waitcnt lgkmcnt(6)\n\t"
"v_mfma_f32_16x16x32_bf16 v[12:15], a[132:135], a[180:183], v[12:15]\n\t"
"v_mfma_f32_16x16x32_bf16 v[12:15], a[140:143], a[184:187], v[12:15]\n\t"
"v_mfma_f32_16x16x32_bf16 v[12:15], a[148:151], a[188:191], v[12:15]\n\t"
"ds_write_b128 v249, a[12:15] offset:24576\n\t"
"s_waitcnt lgkmcnt(6)\n\t"
"v_mfma_f32_16x16x32_bf16 v[16:19], a[132:135], a[184:187], v[16:19]\n\t"
"v_mfma_f32_16x16x32_bf16 v[16:19], a[140:143], a[188:191], v[16:19]\n\t"
"v_mfma_f32_16x16x32_bf16 v[16:19], a[148:151], a[192:195], v[16:19]\n\t"
"ds_write_b64 v250, a[138:139] offset:24576\n\t"
"s_waitcnt lgkmcnt(5)\n\t"
"v_mfma_f32_16x16x32_bf16 v[20:23], a[132:135], a[188:191], v[20:23]\n\t"
"v_mfma_f32_16x16x32_bf16 v[20:23], a[140:143], a[192:195], v[20:23]\n\t"
"v_mfma_f32_16x16x32_bf16 v[20:23], a[148:151], a[196:199], v[20:23]\n\t"
"s_waitcnt lgkmcnt(4)\n\t"
"v_mfma_f32_16x16x32_bf16 v[24:27], a[132:135], a[192:195], v[24:27]\n\t"
"v_mfma_f32_16x16x32_bf16 v[24:27], a[140:143], a[196:199], v[24:27]\n\t"
"v_mfma_f32_16x16x32_bf16 v[24:27], a[148:151], a[200:203], v[24:27]\n\t"
"s_waitcnt lgkmcnt(3)\n\t"
"v_mfma_f32_16x16x32_bf16 v[28:31], a[132:135], a[196:199], v[28:31]\n\t"
"v_mfma_f32_16x16x32_bf16 v[28:31], a[140:143], a[200:203], v[28:31]\n\t"
"v_mfma_f32_16x16x32_bf16 v[28:31], a[148:151], a[204:207], v[28:31]\n\t"
"s_waitcnt vmcnt(42)\n\t"
"v_mfma_f32_16x16x32_bf16 v[32:35], a[156:159], a[168:171], v[32:35]\n\t"
"v_mfma_f32_16x16x32_bf16 v[32:35], a[236:239], a[172:175], v[32:35]\n\t"
"v_mfma_f32_16x16x32_bf16 v[32:35], a[240:243], a[176:179], v[32:35]\n\t"
"ds_read_b64_tr_b16 v[236:237], v240 offset:1024\n\t"
"ds_read_b64_tr_b16 v[238:239], v240 offset:2560\n\t"
"v_mfma_f32_16x16x32_bf16 v[36:39], a[156:159], a[172:175], v[36:39]\n\t"
"v_mfma_f32_16x16x32_bf16 v[36:39], a[236:239], a[176:179], v[36:39]\n\t"
"v_mfma_f32_16x16x32_bf16 v[36:39], a[240:243], a[180:183], v[36:39]\n\t"
"v_mfma_f32_16x16x32_bf16 v[40:43], a[156:159], a[176:179], v[40:43]\n\t"
"v_mfma_f32_16x16x32_bf16 v[40:43], a[236:239], a[180:183], v[40:43]\n\t"
"v_mfma_f32_16x16x32_bf16 v[40:43], a[240:243], a[184:187], v[40:43]\n\t"
"v_mfma_f32_16x16x32_bf16 v[44:47], a[156:159], a[180:183], v[44:47]\n\t"
"v_mfma_f32_16x16x32_bf16 v[44:47], a[236:239], a[184:187], v[44:47]\n\t"
"v_mfma_f32_16x16x32_bf16 v[44:47], a[240:243], a[188:191], v[44:47]\n\t"
"s_waitcnt lgkmcnt(2)\n\t"
"s_barrier\n\t"
"v_mfma_f32_16x16x32_bf16 v[48:51], a[156:159], a[184:187], v[48:51]\n\t"
"ds_read_b128 v[196:199], v252 offset:18432\n\t"
"v_mfma_f32_16x16x32_bf16 v[48:51], a[236:239], a[188:191], v[48:51]\n\t"
"ds_read_b64_tr_b16 v[232:233], v241 offset:1024\n\t"
"v_mfma_f32_16x16x32_bf16 v[48:51], a[240:243], a[192:195], v[48:51]\n\t"
"ds_read_b64_tr_b16 v[234:235], v241 offset:2560\n\t"
"ds_read_b128 v[200:203], v252 offset:19456\n\t"
"v_mfma_f32_16x16x32_bf16 v[52:55], a[156:159], a[188:191], v[52:55]\n\t"
"ds_read_b128 v[204:207], v252 offset:20480\n\t"
"v_mfma_f32_16x16x32_bf16 v[52:55], a[236:239], a[192:195], v[52:55]\n\t"
"ds_read_b64_tr_b16 v[228:229], v240 offset:20992\n\t"
"v_mfma_f32_16x16x32_bf16 v[52:55], a[240:243], a[196:199], v[52:55]\n\t"
"ds_write_b128 v249, a[16:19] offset:30720\n\t"
"s_waitcnt lgkmcnt(7)\n\t"
"v_mfma_f32_16x16x32_bf16 v[236:239], v[236:239], a[52:55], 0\n\t"
"ds_read_b64_tr_b16 v[230:231], v240 offset:22528\n\t"
"v_mfma_f32_16x16x32_bf16 v[56:59], a[156:159], a[192:195], v[56:59]\n\t"
"v_mfma_f32_16x16x32_bf16 v[56:59], a[236:239], a[196:199], v[56:59]\n\t"
"ds_read_b64_tr_b16 v[224:225], v241 offset:20992\n\t"
"v_mfma_f32_16x16x32_bf16 v[56:59], a[240:243], a[200:203], v[56:59]\n\t"
"s_add_u32 s29, s22, 786816\n\t"
"s_waitcnt lgkmcnt(8)\n\t"
"buffer_store_dwordx4 v[196:199], v248, s[16:19], s29 offen offset:0 nt\n\t"
"s_waitcnt lgkmcnt(6)\n\t"
"v_mfma_f32_16x16x32_bf16 v[232:235], v[232:235], a[52:55], 0\n\t"
"ds_read_b64_tr_b16 v[226:227], v241 offset:22528\n\t"
"v_mfma_f32_16x16x32_bf16 v[60:63], a[156:159], a[196:199], v[60:63]\n\t"
"v_cvt_pk_bf16_f32 v236, v236, v237\n\t"
"v_cvt_pk_bf16_f32 v237, v238, v239\n\t"
"v_mfma_f32_16x16x32_bf16 v[60:63], a[236:239], a[200:203], v[60:63]\n\t"
"v_mfma_f32_16x16x32_bf16 v[60:63], a[240:243], a[204:207], v[60:63]\n\t"
"s_add_u32 s29, s22, 786816\n\t"
"s_waitcnt lgkmcnt(6)\n\t"
"buffer_store_dwordx4 v[200:203], v248, s[16:19], s29 offen offset:128 nt\n\t"
"s_waitcnt vmcnt(41)\n\t"
"v_mfma_f32_16x16x32_bf16 v[64:67], a[244:247], a[168:171], v[64:67]\n\t"
"ds_read_b128 a[168:171], v244 offset:9216\n\t"
"v_mfma_f32_16x16x32_bf16 v[64:67], a[164:167], a[172:175], v[64:67]\n\t"
"v_cvt_pk_bf16_f32 v238, v232, v233\n\t"
"v_cvt_pk_bf16_f32 v239, v234, v235\n\t"
"v_mfma_f32_16x16x32_bf16 v[64:67], v[96:99], a[176:179], v[64:67]\n\t"
"ds_write_b64 v250, a[144:145] offset:30720\n\t"
"v_mfma_f32_16x16x32_bf16 v[68:71], a[244:247], a[172:175], v[68:71]\n\t"
"ds_read_b128 a[172:175], v244 offset:9728\n\t"
"v_mfma_f32_16x16x32_bf16 v[68:71], a[164:167], a[176:179], v[68:71]\n\t"
"v_mfma_f32_16x16x32_bf16 v[68:71], v[96:99], a[180:183], v[68:71]\n\t"
"s_add_u32 s29, s22, 786816\n\t"
"s_waitcnt lgkmcnt(8)\n\t"
"buffer_store_dwordx4 v[204:207], v248, s[16:19], s29 offen offset:256 nt\n\t"
"s_waitcnt lgkmcnt(5)\n\t"
"v_mfma_f32_16x16x32_bf16 v[228:231], v[228:231], a[52:55], 0\n\t"
"ds_write_b128 v249, a[20:23] offset:36864\n\t"
"v_mfma_f32_16x16x32_bf16 v[72:75], a[244:247], a[176:179], v[72:75]\n\t"
"ds_read_b128 a[176:179], v244 offset:10240\n\t"
"v_mfma_f32_16x16x32_bf16 v[72:75], a[164:167], a[180:183], v[72:75]\n\t"
"v_mfma_f32_16x16x32_bf16 v[72:75], v[96:99], a[184:187], v[72:75]\n\t"
"s_waitcnt lgkmcnt(5)\n\t"
"v_mfma_f32_16x16x32_bf16 v[224:227], v[224:227], a[52:55], 0\n\t"
"ds_write_b64 v250, a[146:147] offset:36864\n\t"
"v_mfma_f32_16x16x32_bf16 v[76:79], a[244:247], a[180:183], v[76:79]\n\t"
"v_cvt_pk_bf16_f32 v228, v228, v229\n\t"
"v_mfma_f32_16x16x32_bf16 v[76:79], a[164:167], a[184:187], v[76:79]\n\t"
"v_cvt_pk_bf16_f32 v229, v230, v231\n\t"
"v_mfma_f32_16x16x32_bf16 v[76:79], v[96:99], a[188:191], v[76:79]\n\t"
"ds_read_b128 a[180:183], v244 offset:10752\n\t"
"v_mfma_f32_16x16x32_bf16 v[80:83], a[244:247], a[184:187], v[80:83]\n\t"
"ds_read_b128 a[184:187], v244 offset:11264\n\t"
"v_mfma_f32_16x16x32_bf16 v[80:83], a[164:167], a[188:191], v[80:83]\n\t"
"v_cvt_pk_bf16_f32 v230, v224, v225\n\t"
"v_mfma_f32_16x16x32_bf16 v[80:83], v[96:99], a[192:195], v[80:83]\n\t"
"v_cvt_pk_bf16_f32 v231, v226, v227\n\t"
"v_mfma_f32_16x16x32_bf16 v[84:87], a[244:247], a[188:191], v[84:87]\n\t"
"ds_read_b128 a[188:191], v244 offset:11776\n\t"
"v_mfma_f32_16x16x32_bf16 v[84:87], a[164:167], a[192:195], v[84:87]\n\t"
"v_mfma_f32_16x16x32_bf16 v[84:87], v[96:99], a[196:199], v[84:87]\n\t"
"v_mfma_f32_16x16x32_bf16 v[88:91], a[244:247], a[192:195], v[88:91]\n\t"
"ds_read_b128 a[192:195], v244 offset:12288\n\t"
"v_mfma_f32_16x16x32_bf16 v[88:91], a[164:167], a[196:199], v[88:91]\n\t"
"v_mfma_f32_16x16x32_bf16 v[88:91], v[96:99], a[200:203], v[88:91]\n\t"
"v_mfma_f32_16x16x32_bf16 v[92:95], a[244:247], a[196:199], v[92:95]\n\t"
"ds_read_b128 a[196:199], v244 offset:12800\n\t"
"v_mfma_f32_16x16x32_bf16 v[92:95], a[164:167], a[200:203], v[92:95]\n\t"
"ds_read_b128 a[200:203], v244 offset:13312\n\t"
"v_mfma_f32_16x16x32_bf16 v[92:95], v[96:99], a[204:207], v[92:95]\n\t"
"ds_read_b128 a[204:207], v244 offset:13824\n\t"
"s_waitcnt vmcnt(39) lgkmcnt(8)\n\t"
"v_mfma_f32_16x16x32_bf16 v[100:103], a[220:223], a[168:171], v[100:103]\n\t"
"v_mfma_f32_16x16x32_bf16 v[100:103], a[248:251], a[172:175], v[100:103]\n\t"
"v_mfma_f32_16x16x32_bf16 v[100:103], a[56:59], a[176:179], v[100:103]\n\t"
"s_waitcnt lgkmcnt(6)\n\t"
"v_mfma_f32_16x16x32_bf16 v[104:107], a[220:223], a[172:175], v[104:107]\n\t"
"v_mfma_f32_16x16x32_bf16 v[104:107], a[248:251], a[176:179], v[104:107]\n\t"
"v_mfma_f32_16x16x32_bf16 v[104:107], a[56:59], a[180:183], v[104:107]\n\t"
"s_waitcnt lgkmcnt(0)\n\t"
"s_barrier\n\t"
"v_mfma_f32_16x16x32_bf16 v[108:111], a[220:223], a[176:179], v[108:111]\n\t"
"ds_read_b128 v[196:199], v252 offset:30720\n\t"
"v_mfma_f32_16x16x32_bf16 v[108:111], a[248:251], a[180:183], v[108:111]\n\t"
"ds_read_b64_tr_b16 v[232:233], v240 offset:40960\n\t"
"v_mfma_f32_16x16x32_bf16 v[108:111], a[56:59], a[184:187], v[108:111]\n\t"
"s_mov_b64 exec, s[44:45]\n\t"
"ds_write_b128 v242, v[236:239] offset:0\n\t"
"s_mov_b64 exec, -1\n\t"
"s_mov_b64 exec, s[44:45]\n\t"
"ds_write_b128 v242, v[228:231] offset:1536\n\t"
"s_mov_b64 exec, -1\n\t"
"v_mfma_f32_16x16x32_bf16 v[112:115], a[220:223], a[180:183], v[112:115]\n\t"
"ds_read_b64_tr_b16 v[234:235], v240 offset:42496\n\t"
"v_mfma_f32_16x16x32_bf16 v[112:115], a[248:251], a[184:187], v[112:115]\n\t"
"ds_read_b128 v[200:203], v252 offset:31744\n\t"
"v_mfma_f32_16x16x32_bf16 v[112:115], a[56:59], a[188:191], v[112:115]\n\t"
"ds_read_b64_tr_b16 v[224:225], v241 offset:40960\n\t"
"ds_read_b64_tr_b16 v[226:227], v241 offset:42496\n\t"
"ds_read_b128 v[204:207], v252 offset:32768\n\t"
"v_add_u32 v240, 59904, v240\n\t"
"v_mfma_f32_16x16x32_bf16 v[116:119], a[220:223], a[184:187], v[116:119]\n\t"
"v_add_u32 v241, 59904, v241\n\t"
"ds_read_b64_tr_b16 v[236:237], v240 offset:1024\n\t"
"v_mfma_f32_16x16x32_bf16 v[116:119], a[248:251], a[188:191], v[116:119]\n\t"
"ds_read_b64_tr_b16 v[238:239], v240 offset:2560\n\t"
"v_mfma_f32_16x16x32_bf16 v[116:119], a[56:59], a[192:195], v[116:119]\n\t"
"ds_read_b64_tr_b16 v[228:229], v241 offset:1024\n\t"
"ds_read_b64_tr_b16 v[230:231], v241 offset:2560\n\t"
"v_mfma_f32_16x16x32_bf16 v[120:123], a[220:223], a[188:191], v[120:123]\n\t"
"v_mfma_f32_16x16x32_bf16 v[120:123], a[248:251], a[192:195], v[120:123]\n\t"
"v_mfma_f32_16x16x32_bf16 v[120:123], a[56:59], a[196:199], v[120:123]\n\t"
"ds_write_b128 v249, a[24:27] offset:18432\n\t"
"s_waitcnt lgkmcnt(9)\n\t"
"v_mfma_f32_16x16x32_bf16 v[232:235], v[232:235], a[52:55], 0\n\t"
"s_add_u32 s29, s22, 983424\n\t"
"buffer_store_dwordx4 v[196:199], v248, s[16:19], s29 offen offset:0 nt\n\t"
"s_waitcnt lgkmcnt(6)\n\t"
"v_mfma_f32_16x16x32_bf16 v[224:227], v[224:227], a[52:55], 0\n\t"
"s_add_u32 s29, s22, 983424\n\t"
"buffer_store_dwordx4 v[200:203], v248, s[16:19], s29 offen offset:128 nt\n\t"
"v_mfma_f32_16x16x32_bf16 v[124:127], a[220:223], a[192:195], v[124:127]\n\t"
"v_mfma_f32_16x16x32_bf16 v[124:127], a[248:251], a[196:199], v[124:127]\n\t"
"v_mfma_f32_16x16x32_bf16 v[124:127], a[56:59], a[200:203], v[124:127]\n\t"
"s_add_u32 s29, s22, 983424\n\t"
"s_waitcnt lgkmcnt(5)\n\t"
"buffer_store_dwordx4 v[204:207], v248, s[16:19], s29 offen offset:256 nt\n\t"
"v_mfma_f32_16x16x32_bf16 v[128:131], a[220:223], a[196:199], v[128:131]\n\t"
"v_cvt_pk_bf16_f32 v232, v232, v233\n\t"
"v_cvt_pk_bf16_f32 v233, v234, v235\n\t"
"v_mfma_f32_16x16x32_bf16 v[128:131], a[248:251], a[200:203], v[128:131]\n\t"
"v_cvt_pk_bf16_f32 v234, v224, v225\n\t"
"v_cvt_pk_bf16_f32 v235, v226, v227\n\t"
"v_mfma_f32_16x16x32_bf16 v[128:131], a[56:59], a[204:207], v[128:131]\n\t"
"s_mov_b64 exec, s[44:45]\n\t"
"ds_write_b128 v242, v[232:235] offset:3072\n\t"
"s_mov_b64 exec, -1\n\t"
"s_waitcnt lgkmcnt(4)\n\t"
"v_mfma_f32_16x16x32_bf16 v[236:239], v[236:239], a[52:55], 0\n\t"
"ds_read_b64_tr_b16 v[224:225], v240 offset:20992\n\t"
"ds_read_b64_tr_b16 v[226:227], v240 offset:22528\n\t"
"s_waitcnt lgkmcnt(4)\n\t"
"v_mfma_f32_16x16x32_bf16 v[228:231], v[228:231], a[52:55], 0\n\t"
"ds_read_b64_tr_b16 v[232:233], v241 offset:20992\n\t"
"ds_read_b64_tr_b16 v[234:235], v241 offset:22528\n\t"
"s_waitcnt vmcnt(39)\n\t"
"v_mfma_f32_16x16x32_bf16 v[132:135], a[104:107], a[168:171], v[132:135]\n\t"
"v_mfma_f32_16x16x32_bf16 v[132:135], a[252:255], a[172:175], v[132:135]\n\t"
"v_mfma_f32_16x16x32_bf16 v[132:135], a[224:227], a[176:179], v[132:135]\n\t"
"ds_write_b64 v250, a[152:153] offset:18432\n\t"
"v_mfma_f32_16x16x32_bf16 v[136:139], a[104:107], a[172:175], v[136:139]\n\t"
"v_cvt_pk_bf16_f32 v236, v236, v237\n\t"
"v_cvt_pk_bf16_f32 v237, v238, v239\n\t"
"v_mfma_f32_16x16x32_bf16 v[136:139], a[252:255], a[176:179], v[136:139]\n\t"
"v_cvt_pk_bf16_f32 v238, v228, v229\n\t"
"v_cvt_pk_bf16_f32 v239, v230, v231\n\t"
"v_mfma_f32_16x16x32_bf16 v[136:139], a[224:227], a[180:183], v[136:139]\n\t"
"s_mov_b64 exec, s[44:45]\n\t"
"ds_write_b128 v242, v[236:239] offset:4608\n\t"
"s_mov_b64 exec, -1\n\t"
"v_mfma_f32_16x16x32_bf16 v[140:143], a[104:107], a[176:179], v[140:143]\n\t"
"ds_read_b64_tr_b16 v[228:229], v240 offset:40960\n\t"
"v_mfma_f32_16x16x32_bf16 v[140:143], a[252:255], a[180:183], v[140:143]\n\t"
"ds_read_b64_tr_b16 v[230:231], v240 offset:42496\n\t"
"v_mfma_f32_16x16x32_bf16 v[140:143], a[224:227], a[184:187], v[140:143]\n\t"
"ds_read_b64_tr_b16 v[236:237], v241 offset:40960\n\t"
"ds_read_b64_tr_b16 v[238:239], v241 offset:42496\n\t"
"v_mfma_f32_16x16x32_bf16 v[144:147], a[104:107], a[180:183], v[144:147]\n\t"
"v_mfma_f32_16x16x32_bf16 v[144:147], a[252:255], a[184:187], v[144:147]\n\t"
"v_mfma_f32_16x16x32_bf16 v[144:147], a[224:227], a[188:191], v[144:147]\n\t"
"ds_write_b128 v249, a[28:31] offset:24576\n\t"
"s_waitcnt lgkmcnt(9)\n\t"
"v_mfma_f32_16x16x32_bf16 v[224:227], v[224:227], a[52:55], 0\n\t"
"ds_write_b64 v250, a[154:155] offset:24576\n\t"
"s_waitcnt lgkmcnt(8)\n\t"
"v_mfma_f32_16x16x32_bf16 v[232:235], v[232:235], a[52:55], 0\n\t"
"v_mfma_f32_16x16x32_bf16 v[148:151], a[104:107], a[184:187], v[148:151]\n\t"
"v_mfma_f32_16x16x32_bf16 v[148:151], a[252:255], a[188:191], v[148:151]\n\t"
"v_mfma_f32_16x16x32_bf16 v[148:151], a[224:227], a[192:195], v[148:151]\n\t"
"v_mfma_f32_16x16x32_bf16 v[152:155], a[104:107], a[188:191], v[152:155]\n\t"
"s_nop 0\n\t"
"v_cvt_pk_bf16_f32 v224, v224, v225\n\t"
"v_cvt_pk_bf16_f32 v225, v226, v227\n\t"
"v_mfma_f32_16x16x32_bf16 v[152:155], a[252:255], a[192:195], v[152:155]\n\t"
"v_cvt_pk_bf16_f32 v226, v232, v233\n\t"
"v_mfma_f32_16x16x32_bf16 v[152:155], a[224:227], a[196:199], v[152:155]\n\t"
"v_cvt_pk_bf16_f32 v227, v234, v235\n\t"
"s_waitcnt lgkmcnt(4)\n\t"
"v_mfma_f32_16x16x32_bf16 v[228:231], v[228:231], a[52:55], 0\n\t"
"s_waitcnt lgkmcnt(2)\n\t"
"v_mfma_f32_16x16x32_bf16 v[236:239], v[236:239], a[52:55], 0\n\t"
"s_mov_b64 exec, s[44:45]\n\t"
"ds_write_b128 v242, v[224:227] offset:6144\n\t"
"s_mov_b64 exec, -1\n\t"
"v_mfma_f32_16x16x32_bf16 v[156:159], a[104:107], a[192:195], v[156:159]\n\t"
"v_mfma_f32_16x16x32_bf16 v[156:159], a[252:255], a[196:199], v[156:159]\n\t"
"v_mfma_f32_16x16x32_bf16 v[156:159], a[224:227], a[200:203], v[156:159]\n\t"
"v_mfma_f32_16x16x32_bf16 v[160:163], a[104:107], a[196:199], v[160:163]\n\t"
"v_cvt_pk_bf16_f32 v228, v228, v229\n\t"
"v_cvt_pk_bf16_f32 v229, v230, v231\n\t"
"v_mfma_f32_16x16x32_bf16 v[160:163], a[252:255], a[200:203], v[160:163]\n\t"
"v_cvt_pk_bf16_f32 v230, v236, v237\n\t"
"v_mfma_f32_16x16x32_bf16 v[160:163], a[224:227], a[204:207], v[160:163]\n\t"
"v_cvt_pk_bf16_f32 v231, v238, v239\n\t"
"s_waitcnt vmcnt(33)\n\t"
"v_mfma_f32_16x16x32_bf16 v[164:167], a[60:63], a[168:171], v[164:167]\n\t"
"v_mfma_f32_16x16x32_bf16 v[164:167], a[44:47], a[172:175], v[164:167]\n\t"
"v_mfma_f32_16x16x32_bf16 v[164:167], a[48:51], a[176:179], v[164:167]\n\t"
"s_mov_b64 exec, s[44:45]\n\t"
"ds_write_b128 v242, v[228:231] offset:7680\n\t"
"s_mov_b64 exec, -1\n\t"
"v_mfma_f32_16x16x32_bf16 v[168:171], a[60:63], a[172:175], v[168:171]\n\t"
"v_mfma_f32_16x16x32_bf16 v[168:171], a[44:47], a[176:179], v[168:171]\n\t"
"v_mfma_f32_16x16x32_bf16 v[168:171], a[48:51], a[180:183], v[168:171]\n\t"
"v_mfma_f32_16x16x32_bf16 v[172:175], a[60:63], a[176:179], v[172:175]\n\t"
"v_mfma_f32_16x16x32_bf16 v[172:175], a[44:47], a[180:183], v[172:175]\n\t"
"v_mfma_f32_16x16x32_bf16 v[172:175], a[48:51], a[184:187], v[172:175]\n\t"
"v_mfma_f32_16x16x32_bf16 v[176:179], a[60:63], a[180:183], v[176:179]\n\t"
"v_mfma_f32_16x16x32_bf16 v[176:179], a[44:47], a[184:187], v[176:179]\n\t"
"v_mfma_f32_16x16x32_bf16 v[176:179], a[48:51], a[188:191], v[176:179]\n\t"
"s_waitcnt lgkmcnt(0)\n\t"
"s_barrier\n\t"
"v_mfma_f32_16x16x32_bf16 v[180:183], a[60:63], a[184:187], v[180:183]\n\t"
"ds_read_b128 a[168:171], v244 offset:0\n\t"
"v_add_u32 v224, 67584, v249\n\t"
"v_mfma_f32_16x16x32_bf16 v[180:183], a[44:47], a[188:191], v[180:183]\n\t"
"ds_read_b128 a[172:175], v244 offset:512\n\t"
"v_add_u32 v225, 67584, v250\n\t"
"v_mfma_f32_16x16x32_bf16 v[180:183], a[48:51], a[192:195], v[180:183]\n\t"
"ds_read_b128 a[176:179], v244 offset:1024\n\t"
"ds_read_b128 v[196:199], v252 offset:18432\n\t"
"ds_read_b128 a[180:183], v244 offset:1536\n\t"
"v_mfma_f32_16x16x32_bf16 v[184:187], a[60:63], a[188:191], v[184:187]\n\t"
"v_add_u32 v226, 67584, v252\n\t"
"ds_read_b128 v[200:203], v252 offset:19456\n\t"
"v_mfma_f32_16x16x32_bf16 v[184:187], a[44:47], a[192:195], v[184:187]\n\t"
"ds_read_b128 a[184:187], v244 offset:2048\n\t"
"v_mfma_f32_16x16x32_bf16 v[184:187], a[48:51], a[196:199], v[184:187]\n\t"
"ds_read_b128 v[204:207], v252 offset:20480\n\t"
"ds_read_b128 a[188:191], v244 offset:2560\n\t"
"v_mfma_f32_16x16x32_bf16 v[188:191], a[60:63], a[192:195], v[188:191]\n\t"
"ds_read_b128 a[192:195], v244 offset:3072\n\t"
"v_mfma_f32_16x16x32_bf16 v[188:191], a[44:47], a[196:199], v[188:191]\n\t"
"v_mfma_f32_16x16x32_bf16 v[188:191], a[48:51], a[200:203], v[188:191]\n\t"
"ds_write_b128 v249, a[32:35] offset:30720\n\t"
"v_mfma_f32_16x16x32_bf16 v[192:195], a[60:63], a[196:199], v[192:195]\n\t"
"ds_read_b128 a[196:199], v244 offset:3584\n\t"
"v_mfma_f32_16x16x32_bf16 v[192:195], a[44:47], a[200:203], v[192:195]\n\t"
"ds_read_b128 a[200:203], v244 offset:4096\n\t"
"v_mfma_f32_16x16x32_bf16 v[192:195], a[48:51], a[204:207], v[192:195]\n\t"
"s_add_u32 s29, s22, 1180032\n\t"
"s_waitcnt lgkmcnt(9)\n\t"
"buffer_store_dwordx4 v[196:199], v248, s[16:19], s29 offen offset:0 nt\n\t"
"Lst35_%=:\n\t"
"s_waitcnt vmcnt(31)\n\t"
"v_mfma_f32_16x16x32_bf16 v[0:3], a[64:67], a[168:171], v[0:3]\n\t"
"ds_read_b128 a[204:207], v244 offset:4608\n\t"
"v_mfma_f32_16x16x32_bf16 v[0:3], a[68:71], a[172:175], v[0:3]\n\t"
"v_mfma_f32_16x16x32_bf16 v[0:3], a[72:75], a[176:179], v[0:3]\n\t"
"s_add_u32 s29, s22, 1180032\n\t"
"s_waitcnt lgkmcnt(8)\n\t"
"buffer_store_dwordx4 v[200:203], v248, s[16:19], s29 offen offset:128 nt\n\t"
"v_mfma_f32_16x16x32_bf16 v[4:7], a[64:67], a[172:175], v[4:7]\n\t"
"v_mfma_f32_16x16x32_bf16 v[4:7], a[68:71], a[176:179], v[4:7]\n\t"
"v_mfma_f32_16x16x32_bf16 v[4:7], a[72:75], a[180:183], v[4:7]\n\t"
"ds_write_b64 v250, a[160:161] offset:30720\n\t"
"s_waitcnt lgkmcnt(8)\n\t"
"v_mfma_f32_16x16x32_bf16 v[8:11], a[64:67], a[176:179], v[8:11]\n\t"
"v_cvt_pk_bf16_f32 v0, v0, v1\n\t"
"v_cvt_pk_bf16_f32 v1, v2, v3\n\t"
"v_mfma_f32_16x16x32_bf16 v[8:11], a[68:71], a[180:183], v[8:11]\n\t"
"v_mfma_f32_16x16x32_bf16 v[8:11], a[72:75], a[184:187], v[8:11]\n\t"
"s_add_u32 s29, s22, 1180032\n\t"
"s_waitcnt lgkmcnt(7)\n\t"
"buffer_store_dwordx4 v[204:207], v248, s[16:19], s29 offen offset:256 nt\n\t"
"s_waitcnt lgkmcnt(6)\n\t"
"v_mfma_f32_16x16x32_bf16 v[12:15], a[64:67], a[180:183], v[12:15]\n\t"
"v_cvt_pk_bf16_f32 v4, v4, v5\n\t"
"v_cvt_pk_bf16_f32 v5, v6, v7\n\t"
"v_mfma_f32_16x16x32_bf16 v[12:15], a[68:71], a[184:187], v[12:15]\n\t"
"v_mfma_f32_16x16x32_bf16 v[12:15], a[72:75], a[188:191], v[12:15]\n\t"
"s_waitcnt lgkmcnt(5)\n\t"
"v_mfma_f32_16x16x32_bf16 v[16:19], a[64:67], a[184:187], v[16:19]\n\t"
"v_cvt_pk_bf16_f32 v8, v8, v9\n\t"
"v_cvt_pk_bf16_f32 v9, v10, v11\n\t"
"v_mfma_f32_16x16x32_bf16 v[16:19], a[68:71], a[188:191], v[16:19]\n\t"
"v_mfma_f32_16x16x32_bf16 v[16:19], a[72:75], a[192:195], v[16:19]\n\t"
"s_waitcnt lgkmcnt(3)\n\t"
"v_mfma_f32_16x16x32_bf16 v[20:23], a[64:67], a[188:191], v[20:23]\n\t"
"v_cvt_pk_bf16_f32 v12, v12, v13\n\t"
"v_mfma_f32_16x16x32_bf16 v[20:23], a[68:71], a[192:195], v[20:23]\n\t"
"v_cvt_pk_bf16_f32 v13, v14, v15\n\t"
"v_mfma_f32_16x16x32_bf16 v[20:23], a[72:75], a[196:199], v[20:23]\n\t"
"s_waitcnt lgkmcnt(2)\n\t"
"v_mfma_f32_16x16x32_bf16 v[24:27], a[64:67], a[192:195], v[24:27]\n\t"
"v_cvt_pk_bf16_f32 v16, v16, v17\n\t"
"v_mfma_f32_16x16x32_bf16 v[24:27], a[68:71], a[196:199], v[24:27]\n\t"
"v_cvt_pk_bf16_f32 v17, v18, v19\n\t"
"v_mfma_f32_16x16x32_bf16 v[24:27], a[72:75], a[200:203], v[24:27]\n\t"
"ds_write_b128 v249, a[36:39] offset:36864\n\t"
"s_waitcnt lgkmcnt(2)\n\t"
"v_mfma_f32_16x16x32_bf16 v[28:31], a[64:67], a[196:199], v[28:31]\n\t"
"v_mfma_f32_16x16x32_bf16 v[28:31], a[68:71], a[200:203], v[28:31]\n\t"
"v_mfma_f32_16x16x32_bf16 v[28:31], a[72:75], a[204:207], v[28:31]\n\t"
"v_cvt_pk_bf16_f32 v20, v20, v21\n\t"
"s_waitcnt vmcnt(29)\n\t"
"v_mfma_f32_16x16x32_bf16 v[32:35], v[208:211], a[168:171], v[32:35]\n\t"
"v_mfma_f32_16x16x32_bf16 v[32:35], a[112:115], a[172:175], v[32:35]\n\t"
"v_cvt_pk_bf16_f32 v21, v22, v23\n\t"
"v_mfma_f32_16x16x32_bf16 v[32:35], a[76:79], a[176:179], v[32:35]\n\t"
"v_cvt_pk_bf16_f32 v24, v24, v25\n\t"
"v_mfma_f32_16x16x32_bf16 v[36:39], v[208:211], a[172:175], v[36:39]\n\t"
"v_mfma_f32_16x16x32_bf16 v[36:39], a[112:115], a[176:179], v[36:39]\n\t"
"v_cvt_pk_bf16_f32 v25, v26, v27\n\t"
"v_mfma_f32_16x16x32_bf16 v[36:39], a[76:79], a[180:183], v[36:39]\n\t"
"ds_write_b64 v250, a[162:163] offset:36864\n\t"
"v_mfma_f32_16x16x32_bf16 v[40:43], v[208:211], a[176:179], v[40:43]\n\t"
"v_mfma_f32_16x16x32_bf16 v[40:43], a[112:115], a[180:183], v[40:43]\n\t"
"v_mfma_f32_16x16x32_bf16 v[40:43], a[76:79], a[184:187], v[40:43]\n\t"
"v_cvt_pk_bf16_f32 v28, v28, v29\n\t"
"v_mfma_f32_16x16x32_bf16 v[44:47], v[208:211], a[180:183], v[44:47]\n\t"
"v_cvt_pk_bf16_f32 v29, v30, v31\n\t"
"v_mfma_f32_16x16x32_bf16 v[44:47], a[112:115], a[184:187], v[44:47]\n\t"
"v_mfma_f32_16x16x32_bf16 v[44:47], a[76:79], a[188:191], v[44:47]\n\t"
"v_cvt_pk_bf16_f32 v2, v32, v33\n\t"
"s_waitcnt lgkmcnt(0)\n\t"
"s_barrier\n\t"
"v_mfma_f32_16x16x32_bf16 v[48:51], v[208:211], a[184:187], v[48:51]\n\t"
"ds_read_b128 v[196:199], v252 offset:30720\n\t"
"v_cvt_pk_bf16_f32 v3, v34, v35\n\t"
"v_mfma_f32_16x16x32_bf16 v[48:51], a[112:115], a[188:191], v[48:51]\n\t"
"ds_read_b128 v[200:203], v252 offset:31744\n\t"
"v_mfma_f32_16x16x32_bf16 v[48:51], a[76:79], a[192:195], v[48:51]\n\t"
"ds_read_b128 v[204:207], v252 offset:32768\n\t"
"v_cvt_pk_bf16_f32 v6, v36, v37\n\t"
"v_mfma_f32_16x16x32_bf16 v[52:55], v[208:211], a[188:191], v[52:55]\n\t"
"v_cvt_pk_bf16_f32 v7, v38, v39\n\t"
"v_mfma_f32_16x16x32_bf16 v[52:55], a[112:115], a[192:195], v[52:55]\n\t"
"v_cvt_pk_bf16_f32 v10, v40, v41\n\t"
"v_mfma_f32_16x16x32_bf16 v[52:55], a[76:79], a[196:199], v[52:55]\n\t"
"v_cvt_pk_bf16_f32 v11, v42, v43\n\t"
"v_mfma_f32_16x16x32_bf16 v[56:59], v[208:211], a[192:195], v[56:59]\n\t"
"v_cvt_pk_bf16_f32 v14, v44, v45\n\t"
"v_mfma_f32_16x16x32_bf16 v[56:59], a[112:115], a[196:199], v[56:59]\n\t"
"v_cvt_pk_bf16_f32 v15, v46, v47\n\t"
"v_mfma_f32_16x16x32_bf16 v[56:59], a[76:79], a[200:203], v[56:59]\n\t"
"v_cvt_pk_bf16_f32 v18, v48, v49\n\t"
"v_mfma_f32_16x16x32_bf16 v[60:63], v[208:211], a[196:199], v[60:63]\n\t"
"v_cvt_pk_bf16_f32 v19, v50, v51\n\t"
"v_mfma_f32_16x16x32_bf16 v[60:63], a[112:115], a[200:203], v[60:63]\n\t"
"v_cvt_pk_bf16_f32 v22, v52, v53\n\t"
"v_mfma_f32_16x16x32_bf16 v[60:63], a[76:79], a[204:207], v[60:63]\n\t"
"s_add_u32 s29, s22, 1376640\n\t"
"s_waitcnt lgkmcnt(2)\n\t"
"buffer_store_dwordx4 v[196:199], v248, s[16:19], s29 offen offset:0 nt\n\t"
"s_waitcnt vmcnt(25)\n\t"
"v_mfma_f32_16x16x32_bf16 v[64:67], a[80:83], a[168:171], v[64:67]\n\t"
"ds_read_b128 a[168:171], v244 offset:9216\n\t"
"v_mfma_f32_16x16x32_bf16 v[64:67], a[84:87], a[172:175], v[64:67]\n\t"
"v_cvt_pk_bf16_f32 v23, v54, v55\n\t"
"v_cvt_pk_bf16_f32 v26, v56, v57\n\t"
"v_mfma_f32_16x16x32_bf16 v[64:67], a[40:43], a[176:179], v[64:67]\n\t"
"v_cvt_pk_bf16_f32 v27, v58, v59\n\t"
"v_mfma_f32_16x16x32_bf16 v[68:71], a[80:83], a[172:175], v[68:71]\n\t"
"ds_read_b128 a[172:175], v244 offset:9728\n\t"
"v_cvt_pk_bf16_f32 v30, v60, v61\n\t"
"v_mfma_f32_16x16x32_bf16 v[68:71], a[84:87], a[176:179], v[68:71]\n\t"
"v_cvt_pk_bf16_f32 v31, v62, v63\n\t"
"v_mfma_f32_16x16x32_bf16 v[68:71], a[40:43], a[180:183], v[68:71]\n\t"
"s_add_u32 s29, s22, 1376640\n\t"
"s_waitcnt lgkmcnt(3)\n\t"
"buffer_store_dwordx4 v[200:203], v248, s[16:19], s29 offen offset:128 nt\n\t"
"v_mfma_f32_16x16x32_bf16 v[72:75], a[80:83], a[176:179], v[72:75]\n\t"
"v_cvt_pk_bf16_f32 v64, v64, v65\n\t"
"v_cvt_pk_bf16_f32 v65, v66, v67\n\t"
"v_mfma_f32_16x16x32_bf16 v[72:75], a[84:87], a[180:183], v[72:75]\n\t"
"ds_read_b128 a[176:179], v244 offset:10240\n\t"
"v_mfma_f32_16x16x32_bf16 v[72:75], a[40:43], a[184:187], v[72:75]\n\t"
"s_add_u32 s29, s22, 1376640\n\t"
"s_waitcnt lgkmcnt(3)\n\t"
"buffer_store_dwordx4 v[204:207], v248, s[16:19], s29 offen offset:256 nt\n\t"
"v_mfma_f32_16x16x32_bf16 v[76:79], a[80:83], a[180:183], v[76:79]\n\t"
"v_cvt_pk_bf16_f32 v68, v68, v69\n\t"
"v_cvt_pk_bf16_f32 v69, v70, v71\n\t"
"v_mfma_f32_16x16x32_bf16 v[76:79], a[84:87], a[184:187], v[76:79]\n\t"
"ds_read_b128 a[180:183], v244 offset:10752\n\t"
"v_mfma_f32_16x16x32_bf16 v[76:79], a[40:43], a[188:191], v[76:79]\n\t"
"ds_write_b128 v249, v[0:3] offset:18432\n\t"
"v_mfma_f32_16x16x32_bf16 v[80:83], a[80:83], a[184:187], v[80:83]\n\t"
"v_cvt_pk_bf16_f32 v72, v72, v73\n\t"
"v_cvt_pk_bf16_f32 v73, v74, v75\n\t"
"v_mfma_f32_16x16x32_bf16 v[80:83], a[84:87], a[188:191], v[80:83]\n\t"
"ds_read_b128 a[184:187], v244 offset:11264\n\t"
"v_mfma_f32_16x16x32_bf16 v[80:83], a[40:43], a[192:195], v[80:83]\n\t"
"ds_write_b64 v250, v[64:65] offset:18432\n\t"
"v_mfma_f32_16x16x32_bf16 v[84:87], a[80:83], a[188:191], v[84:87]\n\t"
"v_cvt_pk_bf16_f32 v76, v76, v77\n\t"
"v_cvt_pk_bf16_f32 v77, v78, v79\n\t"
"v_mfma_f32_16x16x32_bf16 v[84:87], a[84:87], a[192:195], v[84:87]\n\t"
"ds_read_b128 a[188:191], v244 offset:11776\n\t"
"v_mfma_f32_16x16x32_bf16 v[84:87], a[40:43], a[196:199], v[84:87]\n\t"
"ds_write_b128 v249, v[4:7] offset:24576\n\t"
"v_mfma_f32_16x16x32_bf16 v[88:91], a[80:83], a[192:195], v[88:91]\n\t"
"v_cvt_pk_bf16_f32 v80, v80, v81\n\t"
"v_cvt_pk_bf16_f32 v81, v82, v83\n\t"
"v_mfma_f32_16x16x32_bf16 v[88:91], a[84:87], a[196:199], v[88:91]\n\t"
"ds_read_b128 a[192:195], v244 offset:12288\n\t"
"v_mfma_f32_16x16x32_bf16 v[88:91], a[40:43], a[200:203], v[88:91]\n\t"
"ds_write_b64 v250, v[68:69] offset:24576\n\t"
"v_mfma_f32_16x16x32_bf16 v[92:95], a[80:83], a[196:199], v[92:95]\n\t"
"v_cvt_pk_bf16_f32 v84, v84, v85\n\t"
"v_cvt_pk_bf16_f32 v85, v86, v87\n\t"
"v_mfma_f32_16x16x32_bf16 v[92:95], a[84:87], a[200:203], v[92:95]\n\t"
"ds_read_b128 a[196:199], v244 offset:12800\n\t"
"v_mfma_f32_16x16x32_bf16 v[92:95], a[40:43], a[204:207], v[92:95]\n\t"
"ds_read_b128 a[200:203], v244 offset:13312\n\t"
"ds_read_b128 a[204:207], v244 offset:13824\n\t"
"s_waitcnt vmcnt(24) lgkmcnt(11)\n\t"
"v_mfma_f32_16x16x32_bf16 v[100:103], a[208:211], a[168:171], v[100:103]\n\t"
"v_cvt_pk_bf16_f32 v88, v88, v89\n\t"
"v_mfma_f32_16x16x32_bf16 v[100:103], a[212:215], a[172:175], v[100:103]\n\t"
"v_cvt_pk_bf16_f32 v89, v90, v91\n\t"
"v_mfma_f32_16x16x32_bf16 v[100:103], a[88:91], a[176:179], v[100:103]\n\t"
"s_waitcnt lgkmcnt(10)\n\t"
"v_mfma_f32_16x16x32_bf16 v[104:107], a[208:211], a[172:175], v[104:107]\n\t"
"v_cvt_pk_bf16_f32 v92, v92, v93\n\t"
"v_cvt_pk_bf16_f32 v93, v94, v95\n\t"
"v_mfma_f32_16x16x32_bf16 v[104:107], a[212:215], a[176:179], v[104:107]\n\t"
"v_mfma_f32_16x16x32_bf16 v[104:107], a[88:91], a[180:183], v[104:107]\n\t"
"s_waitcnt lgkmcnt(0)\n\t"
"s_barrier\n\t"
"v_mfma_f32_16x16x32_bf16 v[108:111], a[208:211], a[176:179], v[108:111]\n\t"
"ds_read_b128 v[196:199], v252 offset:18432\n\t"
"v_cvt_pk_bf16_f32 v100, v100, v101\n\t"
"v_mfma_f32_16x16x32_bf16 v[108:111], a[212:215], a[180:183], v[108:111]\n\t"
"ds_read_b128 v[200:203], v252 offset:19456\n\t"
"v_cvt_pk_bf16_f32 v101, v102, v103\n\t"
"v_mfma_f32_16x16x32_bf16 v[108:111], a[88:91], a[184:187], v[108:111]\n\t"
"ds_read_b128 v[204:207], v252 offset:20480\n\t"
"v_mfma_f32_16x16x32_bf16 v[112:115], a[208:211], a[180:183], v[112:115]\n\t"
"v_cvt_pk_bf16_f32 v104, v104, v105\n\t"
"v_cvt_pk_bf16_f32 v105, v106, v107\n\t"
"v_mfma_f32_16x16x32_bf16 v[112:115], a[212:215], a[184:187], v[112:115]\n\t"
"v_mfma_f32_16x16x32_bf16 v[112:115], a[88:91], a[188:191], v[112:115]\n\t"
"ds_write_b128 v249, v[8:11] offset:30720\n\t"
"v_mfma_f32_16x16x32_bf16 v[116:119], a[208:211], a[184:187], v[116:119]\n\t"
"v_cvt_pk_bf16_f32 v108, v108, v109\n\t"
"v_cvt_pk_bf16_f32 v109, v110, v111\n\t"
"v_mfma_f32_16x16x32_bf16 v[116:119], a[212:215], a[188:191], v[116:119]\n\t"
"v_mfma_f32_16x16x32_bf16 v[116:119], a[88:91], a[192:195], v[116:119]\n\t"
"ds_write_b128 v249, v[16:19] offset:43008\n\t"
"v_mfma_f32_16x16x32_bf16 v[120:123], a[208:211], a[188:191], v[120:123]\n\t"
"v_cvt_pk_bf16_f32 v112, v112, v113\n\t"
"v_cvt_pk_bf16_f32 v113, v114, v115\n\t"
"v_mfma_f32_16x16x32_bf16 v[120:123], a[212:215], a[192:195], v[120:123]\n\t"
"v_mfma_f32_16x16x32_bf16 v[120:123], a[88:91], a[196:199], v[120:123]\n\t"
"ds_write_b128 v249, v[24:27] offset:55296\n\t"
"v_mfma_f32_16x16x32_bf16 v[124:127], a[208:211], a[192:195], v[124:127]\n\t"
"v_cvt_pk_bf16_f32 v116, v116, v117\n\t"
"v_cvt_pk_bf16_f32 v117, v118, v119\n\t"
"v_mfma_f32_16x16x32_bf16 v[124:127], a[212:215], a[196:199], v[124:127]\n\t"
"v_mfma_f32_16x16x32_bf16 v[124:127], a[88:91], a[200:203], v[124:127]\n\t"
"s_add_u32 s29, s23, 0\n\t"
"s_waitcnt lgkmcnt(5)\n\t"
"buffer_store_dwordx4 v[196:199], v248, s[16:19], s29 offen offset:0 nt\n\t"
"v_mfma_f32_16x16x32_bf16 v[128:131], a[208:211], a[196:199], v[128:131]\n\t"
"v_cvt_pk_bf16_f32 v120, v120, v121\n\t"
"v_cvt_pk_bf16_f32 v121, v122, v123\n\t"
"v_mfma_f32_16x16x32_bf16 v[128:131], a[212:215], a[200:203], v[128:131]\n\t"
"v_mfma_f32_16x16x32_bf16 v[128:131], a[88:91], a[204:207], v[128:131]\n\t"
"s_add_u32 s29, s23, 0\n\t"
"s_waitcnt lgkmcnt(4)\n\t"
"buffer_store_dwordx4 v[200:203], v248, s[16:19], s29 offen offset:128 nt\n\t"
"ds_write_b64 v250, v[72:73] offset:30720\n\t"
"s_waitcnt vmcnt(23)\n\t"
"v_mfma_f32_16x16x32_bf16 v[132:135], v[212:215], a[168:171], v[132:135]\n\t"
"v_cvt_pk_bf16_f32 v124, v124, v125\n\t"
"v_cvt_pk_bf16_f32 v125, v126, v127\n\t"
"v_mfma_f32_16x16x32_bf16 v[132:135], v[216:219], a[172:175], v[132:135]\n\t"
"v_mfma_f32_16x16x32_bf16 v[132:135], a[120:123], a[176:179], v[132:135]\n\t"
"s_add_u32 s29, s23, 0\n\t"
"s_waitcnt lgkmcnt(4)\n\t"
"buffer_store_dwordx4 v[204:207], v248, s[16:19], s29 offen offset:256 nt\n\t"
"ds_write_b64 v250, v[80:81] offset:43008\n\t"
"v_mfma_f32_16x16x32_bf16 v[136:139], v[212:215], a[172:175], v[136:139]\n\t"
"v_cvt_pk_bf16_f32 v128, v128, v129\n\t"
"v_cvt_pk_bf16_f32 v129, v130, v131\n\t"
"v_mfma_f32_16x16x32_bf16 v[136:139], v[216:219], a[176:179], v[136:139]\n\t"
"v_mfma_f32_16x16x32_bf16 v[136:139], a[120:123], a[180:183], v[136:139]\n\t"
"ds_write_b64 v250, v[88:89] offset:55296\n\t"
"v_mfma_f32_16x16x32_bf16 v[140:143], v[212:215], a[176:179], v[140:143]\n\t"
"v_cvt_pk_bf16_f32 v102, v132, v133\n\t"
"v_cvt_pk_bf16_f32 v103, v134, v135\n\t"
"v_mfma_f32_16x16x32_bf16 v[140:143], v[216:219], a[180:183], v[140:143]\n\t"
"v_mfma_f32_16x16x32_bf16 v[140:143], a[120:123], a[184:187], v[140:143]\n\t"
"ds_write_b128 v249, v[12:15] offset:36864\n\t"
"v_mfma_f32_16x16x32_bf16 v[144:147], v[212:215], a[180:183], v[144:147]\n\t"
"v_cvt_pk_bf16_f32 v106, v136, v137\n\t"
"v_cvt_pk_bf16_f32 v107, v138, v139\n\t"
"v_mfma_f32_16x16x32_bf16 v[144:147], v[216:219], a[184:187], v[144:147]\n\t"
"v_mfma_f32_16x16x32_bf16 v[144:147], a[120:123], a[188:191], v[144:147]\n\t"
"ds_write_b128 v249, v[20:23] offset:49152\n\t"
"v_mfma_f32_16x16x32_bf16 v[148:151], v[212:215], a[184:187], v[148:151]\n\t"
"v_cvt_pk_bf16_f32 v110, v140, v141\n\t"
"v_cvt_pk_bf16_f32 v111, v142, v143\n\t"
"v_mfma_f32_16x16x32_bf16 v[148:151], v[216:219], a[188:191], v[148:151]\n\t"
"v_mfma_f32_16x16x32_bf16 v[148:151], a[120:123], a[192:195], v[148:151]\n\t"
"ds_write_b128 v249, v[28:31] offset:61440\n\t"
"v_mfma_f32_16x16x32_bf16 v[152:155], v[212:215], a[188:191], v[152:155]\n\t"
"v_cvt_pk_bf16_f32 v114, v144, v145\n\t"
"v_cvt_pk_bf16_f32 v115, v146, v147\n\t"
"v_mfma_f32_16x16x32_bf16 v[152:155], v[216:219], a[192:195], v[152:155]\n\t"
"v_mfma_f32_16x16x32_bf16 v[152:155], a[120:123], a[196:199], v[152:155]\n\t"
"v_mfma_f32_16x16x32_bf16 v[156:159], v[212:215], a[192:195], v[156:159]\n\t"
"s_nop 0\n\t"
"v_cvt_pk_bf16_f32 v118, v148, v149\n\t"
"v_cvt_pk_bf16_f32 v119, v150, v151\n\t"
"v_mfma_f32_16x16x32_bf16 v[156:159], v[216:219], a[196:199], v[156:159]\n\t"
"v_mfma_f32_16x16x32_bf16 v[156:159], a[120:123], a[200:203], v[156:159]\n\t"
"ds_write_b64 v250, v[76:77] offset:36864\n\t"
"v_mfma_f32_16x16x32_bf16 v[160:163], v[212:215], a[196:199], v[160:163]\n\t"
"v_cvt_pk_bf16_f32 v122, v152, v153\n\t"
"v_mfma_f32_16x16x32_bf16 v[160:163], v[216:219], a[200:203], v[160:163]\n\t"
"v_mfma_f32_16x16x32_bf16 v[160:163], a[120:123], a[204:207], v[160:163]\n\t"
"v_cvt_pk_bf16_f32 v123, v154, v155\n\t"
"s_waitcnt vmcnt(18)\n\t"
"v_mfma_f32_16x16x32_bf16 v[164:167], a[92:95], a[168:171], v[164:167]\n\t"
"v_cvt_pk_bf16_f32 v126, v156, v157\n\t"
"v_mfma_f32_16x16x32_bf16 v[164:167], a[96:99], a[172:175], v[164:167]\n\t"
"v_cvt_pk_bf16_f32 v127, v158, v159\n\t"
"v_mfma_f32_16x16x32_bf16 v[164:167], a[100:103], a[176:179], v[164:167]\n\t"
"v_mfma_f32_16x16x32_bf16 v[168:171], a[92:95], a[172:175], v[168:171]\n\t"
"v_cvt_pk_bf16_f32 v130, v160, v161\n\t"
"v_mfma_f32_16x16x32_bf16 v[168:171], a[96:99], a[176:179], v[168:171]\n\t"
"v_cvt_pk_bf16_f32 v131, v162, v163\n\t"
"v_mfma_f32_16x16x32_bf16 v[168:171], a[100:103], a[180:183], v[168:171]\n\t"
"ds_write_b64 v250, v[84:85] offset:49152\n\t"
"ds_write_b64 v250, v[92:93] offset:61440\n\t"
"v_mfma_f32_16x16x32_bf16 v[172:175], a[92:95], a[176:179], v[172:175]\n\t"
"v_mfma_f32_16x16x32_bf16 v[172:175], a[96:99], a[180:183], v[172:175]\n\t"
"v_mfma_f32_16x16x32_bf16 v[172:175], a[100:103], a[184:187], v[172:175]\n\t"
"v_cvt_pk_bf16_f32 v164, v164, v165\n\t"
"v_cvt_pk_bf16_f32 v165, v166, v167\n\t"
"v_mfma_f32_16x16x32_bf16 v[176:179], a[92:95], a[180:183], v[176:179]\n\t"
"v_mfma_f32_16x16x32_bf16 v[176:179], a[96:99], a[184:187], v[176:179]\n\t"
"v_cvt_pk_bf16_f32 v168, v168, v169\n\t"
"v_mfma_f32_16x16x32_bf16 v[176:179], a[100:103], a[188:191], v[176:179]\n\t"
"v_cvt_pk_bf16_f32 v169, v170, v171\n\t"
"s_waitcnt lgkmcnt(0)\n\t"
"s_barrier\n\t"
"v_mfma_f32_16x16x32_bf16 v[180:183], a[92:95], a[184:187], v[180:183]\n\t"
"ds_read_b128 v[196:199], v252 offset:30720\n\t"
"v_cvt_pk_bf16_f32 v172, v172, v173\n\t"
"v_mfma_f32_16x16x32_bf16 v[180:183], a[96:99], a[188:191], v[180:183]\n\t"
"ds_read_b128 v[208:211], v252 offset:43008\n\t"
"v_cvt_pk_bf16_f32 v173, v174, v175\n\t"
"v_mfma_f32_16x16x32_bf16 v[180:183], a[100:103], a[192:195], v[180:183]\n\t"
"ds_read_b128 a[0:3], v252 offset:55296\n\t"
"ds_read_b128 v[200:203], v252 offset:31744\n\t"
"ds_read_b128 v[212:215], v252 offset:44032\n\t"
"ds_read_b128 a[4:7], v252 offset:56320\n\t"
"v_mfma_f32_16x16x32_bf16 v[184:187], a[92:95], a[188:191], v[184:187]\n\t"
"ds_read_b128 v[204:207], v252 offset:32768\n\t"
"v_cvt_pk_bf16_f32 v176, v176, v177\n\t"
"v_mfma_f32_16x16x32_bf16 v[184:187], a[96:99], a[192:195], v[184:187]\n\t"
"ds_read_b128 v[216:219], v252 offset:45056\n\t"
"v_cvt_pk_bf16_f32 v177, v178, v179\n\t"
"v_mfma_f32_16x16x32_bf16 v[184:187], a[100:103], a[196:199], v[184:187]\n\t"
"ds_write_b128 v249, v[100:103] offset:18432\n\t"
"ds_write_b64 v250, v[164:165] offset:18432\n\t"
"ds_write_b128 v249, v[104:107] offset:24576\n\t"
"ds_write_b64 v250, v[168:169] offset:24576\n\t"
"ds_read_b128 a[8:11], v252 offset:57344\n\t"
"v_mfma_f32_16x16x32_bf16 v[188:191], a[92:95], a[192:195], v[188:191]\n\t"
"v_cvt_pk_bf16_f32 v180, v180, v181\n\t"
"v_cvt_pk_bf16_f32 v181, v182, v183\n\t"
"v_mfma_f32_16x16x32_bf16 v[188:191], a[96:99], a[196:199], v[188:191]\n\t"
"v_mfma_f32_16x16x32_bf16 v[188:191], a[100:103], a[200:203], v[188:191]\n\t"
"ds_write_b128 v224, v[108:111] offset:0\n\t"
"ds_write_b64 v225, v[172:173] offset:0\n\t"
"v_mfma_f32_16x16x32_bf16 v[192:195], a[92:95], a[196:199], v[192:195]\n\t"
"v_cvt_pk_bf16_f32 v184, v184, v185\n\t"
"v_cvt_pk_bf16_f32 v185, v186, v187\n\t"
"v_mfma_f32_16x16x32_bf16 v[192:195], a[96:99], a[200:203], v[192:195]\n\t"
"v_mfma_f32_16x16x32_bf16 v[192:195], a[100:103], a[204:207], v[192:195]\n\t"
"ds_write_b128 v224, v[112:115] offset:6144\n\t"
"ds_write_b64 v225, v[176:177] offset:6144\n\t"
"ds_write_b128 v224, v[116:119] offset:12288\n\t"
"ds_write_b64 v225, v[180:181] offset:12288\n\t"
"ds_write_b128 v224, v[120:123] offset:18432\n\t"
"ds_write_b64 v225, v[184:185] offset:18432\n\t"
"ds_write_b128 v224, v[124:127] offset:24576\n\t"
"v_cvt_pk_bf16_f32 v188, v188, v189\n\t"
"v_cvt_pk_bf16_f32 v189, v190, v191\n\t"
"ds_write_b64 v225, v[188:189] offset:24576\n\t"
"ds_write_b128 v224, v[128:131] offset:30720\n\t"
"v_cvt_pk_bf16_f32 v192, v192, v193\n\t"
"v_cvt_pk_bf16_f32 v193, v194, v195\n\t"
"ds_write_b64 v225, v[192:193] offset:30720\n\t"
"s_waitcnt lgkmcnt(0)\n\t"
"s_barrier\n\t"
"ds_read_b128 a[12:15], v252 offset:18432\n\t"
"ds_read_b128 a[16:19], v252 offset:19456\n\t"
"ds_read_b128 a[20:23], v252 offset:20480\n\t"
"ds_read_b128 a[24:27], v226 offset:0\n\t"
"ds_read_b128 a[28:31], v226 offset:1024\n\t"
"ds_read_b128 a[32:35], v226 offset:2048\n\t"
"ds_read_b128 a[36:39], v226 offset:12288\n\t"
"ds_read_b128 a[40:43], v226 offset:13312\n\t"
"ds_read_b128 a[44:47], v226 offset:14336\n\t"
"ds_read_b128 a[48:51], v226 offset:24576\n\t"
"ds_read_b128 a[52:55], v226 offset:25600\n\t"
"ds_read_b128 a[56:59], v226 offset:26624\n\t"
"s_add_u32 s29, s23, 393216\n\t"
"buffer_store_dwordx4 v[196:199], v248, s[16:19], s29 offen offset:0 nt\n\t"
"s_add_u32 s29, s23, 393216\n\t"
"buffer_store_dwordx4 v[200:203], v248, s[16:19], s29 offen offset:128 nt\n\t"
"s_add_u32 s29, s23, 393216\n\t"
"buffer_store_dwordx4 v[204:207], v248, s[16:19], s29 offen offset:256 nt\n\t"
"s_add_u32 s29, s23, 786432\n\t"
"buffer_store_dwordx4 v[208:211], v248, s[16:19], s29 offen offset:0 nt\n\t"
"s_add_u32 s29, s23, 786432\n\t"
"buffer_store_dwordx4 v[212:215], v248, s[16:19], s29 offen offset:128 nt\n\t"
"s_add_u32 s29, s23, 786432\n\t"
"buffer_store_dwordx4 v[216:219], v248, s[16:19], s29 offen offset:256 nt\n\t"
"s_add_u32 s29, s23, 1179648\n\t"
"buffer_store_dwordx4 a[0:3], v248, s[16:19], s29 offen offset:0 nt\n\t"
"s_add_u32 s29, s23, 1179648\n\t"
"buffer_store_dwordx4 a[4:7], v248, s[16:19], s29 offen offset:128 nt\n\t"
"s_add_u32 s29, s23, 1179648\n\t"
"buffer_store_dwordx4 a[8:11], v248, s[16:19], s29 offen offset:256 nt\n\t"
"s_add_u32 s29, s23, 1152\n\t"
"s_waitcnt lgkmcnt(11)\n\t"
"buffer_store_dwordx4 a[12:15], v248, s[16:19], s29 offen offset:0 nt\n\t"
"s_add_u32 s29, s23, 1152\n\t"
"s_waitcnt lgkmcnt(10)\n\t"
"buffer_store_dwordx4 a[16:19], v248, s[16:19], s29 offen offset:128 nt\n\t"
"s_add_u32 s29, s23, 1152\n\t"
"s_waitcnt lgkmcnt(9)\n\t"
"buffer_store_dwordx4 a[20:23], v248, s[16:19], s29 offen offset:256 nt\n\t"
"s_add_u32 s29, s23, 394368\n\t"
"s_waitcnt lgkmcnt(8)\n\t"
"buffer_store_dwordx4 a[24:27], v248, s[16:19], s29 offen offset:0 nt\n\t"
"s_add_u32 s29, s23, 394368\n\t"
"s_waitcnt lgkmcnt(7)\n\t"
"buffer_store_dwordx4 a[28:31], v248, s[16:19], s29 offen offset:128 nt\n\t"
"s_add_u32 s29, s23, 394368\n\t"
"s_waitcnt lgkmcnt(6)\n\t"
"buffer_store_dwordx4 a[32:35], v248, s[16:19], s29 offen offset:256 nt\n\t"
"s_add_u32 s29, s23, 787584\n\t"
"s_waitcnt lgkmcnt(5)\n\t"
"buffer_store_dwordx4 a[36:39], v248, s[16:19], s29 offen offset:0 nt\n\t"
"s_add_u32 s29, s23, 787584\n\t"
"s_waitcnt lgkmcnt(4)\n\t"
"buffer_store_dwordx4 a[40:43], v248, s[16:19], s29 offen offset:128 nt\n\t"
"s_add_u32 s29, s23, 787584\n\t"
"s_waitcnt lgkmcnt(3)\n\t"
"buffer_store_dwordx4 a[44:47], v248, s[16:19], s29 offen offset:256 nt\n\t"
"s_add_u32 s29, s23, 1180800\n\t"
"s_waitcnt lgkmcnt(2)\n\t"
"buffer_store_dwordx4 a[48:51], v248, s[16:19], s29 offen offset:0 nt\n\t"
"s_add_u32 s29, s23, 1180800\n\t"
"s_waitcnt lgkmcnt(1)\n\t"
"buffer_store_dwordx4 a[52:55], v248, s[16:19], s29 offen offset:128 nt\n\t"
"s_add_u32 s29, s23, 1180800\n\t"
"s_waitcnt lgkmcnt(0)\n\t"
"buffer_store_dwordx4 a[56:59], v248, s[16:19], s29 offen offset:256 nt\n\t"
"s_waitcnt vmcnt(0)\n\t"
"Lwino_end:\n\t"
  : "+{v205}"(dummy)
  : "s"((unsigned)pu), "s"((unsigned)(pu >> 32)), "s"((unsigned)px), "s"((unsigned)(px >> 32)), "s"((unsigned)py), "s"((unsigned)(py >> 32)),
    "s"(xbase), "s"(ybase), "s"(w), "s"((unsigned)(size_t)(__attribute__((address_space(3))) char*)smem), "s"(blockIdx.x), "s"((unsigned long long)lane_tab),
    "{v252}"(w * 3072 + lane * 16), "s"(medge), "s"((unsigned)pw), "s"((unsigned)(pw >> 32)), "s"(fso), "s"(fuo), "s"(fk0), "s"(fk5), "s"(fs8),
    "{v206}"(fwo), "{v207}"(flw), "{v208}"(fst), "{v209}"((lane >> 5) * 8),
    "{v28}"((8 * w + ((lane >> 2) & 7)) * PXB + (lane >> 5) * 64 + 16 * ((lane & 3) ^ ((lane >> 3) & 2))),
    "{v29}"((lane >> 2) * PXB + (lane & 3) * 16 + w * 64), "{v30}"((lane >> 1) * PXB + (lane & 1) * 16 + 256 + w * 32), "{v31}"(18432 + w * 3072 + lane * 8)
  : "v0", "v1", "v2", "v3", "v4", "v5", "v6", "v7", "v8", "v9", "v10", "v11", "v12", "v13", "v14", "v15", "v16", "v17", "v18", "v19", "v20", "v21", "v22", "v23", "v24", "v25", "v26", "v27", "v32", "v33", "v34", "v35", "v36", "v37", "v38", "v39", "v40", "v41", "v42", "v43", "v44", "v45", "v46", "v47", "v48", "v49", "v50", "v51", "v52", "v53", "v54", "v55", "v56", "v57", "v58", "v59", "v60", "v61", "v62", "v63", "v64", "v65", "v66", "v67", "v68", "v69", "v70", "v71", "v72", "v73", "v74", "v75", "v76", "v77", "v78", "v79", "v80", "v81", "v82", "v83", "v84", "v85", "v86", "v87", "v88", "v89", "v90", "v91", "v92", "v93", "v94", "v95", "v96", "v97", "v98", "v99", "v100", "v101", "v102", "v103", "v104", "v105", "v106", "v107", "v108", "v109", "v110", "v111", "v112", "v113", "v114", "v115", "v116", "v117", "v118", "v119", "v120", "v121", "v122", "v123", "v124", "v125", "v126", "v127", "v128", "v129", "v130", "v131", "v132", "v133", "v134", "v135", "v136", "v137", "v138", "v139", "v140", "v141", "v142", "v143", "v144", "v145", "v146", "v147", "v148", "v149", "v150", "v151", "v152", "v153", "v154", "v155", "v156", "v157", "v158", "v159", "v160", "v161", "v162", "v163", "v164", "v165", "v166", "v167", "v168", "v169", "v170", "v171", "v172", "v173", "v174", "v175", "v176", "v177", "v178", "v179", "v180", "v181", "v182", "v183", "v184", "v185", "v186", "v187", "v188", "v189", "v190", "v191", "v192", "v193", "v194", "v195", "v196", "v197", "v198", "v199", "v200", "v201", "v202", "v203", "v204", "v210", "v211", "v212", "v213", "v214", "v215", "v216", "v217", "v218", "v219", "v220", "v221", "v222", "v223", "v224", "v225", "v226", "v227", "v228", "v229", "v230", "v231", "v232", "v233", "v234", "v235", "v236", "v237", "v238", "v239", "v240", "v241", "v242", "v243", "v244", "v245", "v246", "v247", "v248", "v249", "v250", "v251", "v253", "v254", "v255", "a0", "a1", "a2", "a3", "a4", "a5", "a6", "a7", "a8", "a9", "a10", "a11", "a12", "a13", "a14", "a15", "a16", "a17", "a18", "a19", "a20", "a21", "a22", "a23", "a24", "a25", "a26", "a27", "a28", "a29", "a30", "a31", "a32", "a33", "a34", "a35", "a36", "a37", "a38", "a39", "a40", "a41", "a42", "a43", "a44", "a45", "a46", "a47", "a48", "a49", "a50", "a51", "a52", "a53", "a54", "a55", "a56", "a57", "a58", "a59", "a60", "a61", "a62", "a63", "a64", "a65", "a66", "a67", "a68", "a69", "a70", "a71", "a72", "a73", "a74", "a75", "a76", "a77", "a78", "a79", "a80", "a81", "a82", "a83", "a84", "a85", "a86", "a87", "a88", "a89", "a90", "a91", "a92", "a93", "a94", "a95", "a96", "a97", "a98", "a99", "a100", "a101", "a102", "a103", "a104", "a105", "a106", "a107", "a108", "a109", "a110", "a111", "a112", "a113", "a114", "a115", "a116", "a117", "a118", "a119", "a120", "a121", "a122", "a123", "a124", "a125", "a126", "a127", "a128", "a129", "a130", "a131", "a132", "a133", "a134", "a135", "a136", "a137", "a138", "a139", "a140", "a141", "a142", "a143", "a144", "a145", "a146", "a147", "a148", "a149", "a150", "a151", "a152", "a153", "a154", "a155", "a156", "a157", "a158", "a159", "a160", "a161", "a162", "a163", "a164", "a165", "a166", "a167", "a168", "a169", "a170", "a171", "a172", "a173", "a174", "a175", "a176", "a177", "a178", "a179", "a180", "a181", "a182", "a183", "a184", "a185", "a186", "a187", "a188", "a189", "a190", "a191", "a192", "a193", "a194", "a195", "a196", "a197", "a198", "a199", "a200", "a201", "a202", "a203", "a204", "a205", "a206", "a207", "a208", "a209", "a210", "a211", "a212", "a213", "a214", "a215", "a216", "a217", "a218", "a219", "a220", "a221", "a222", "a223", "a224", "a225", "a226", "a227", "a228", "a229", "a230", "a231", "a232", "a233", "a234", "a235", "a236", "a237", "a238", "a239", "a240", "a241", "a242", "a243", "a244", "a245", "a246", "a247", "a248", "a249", "a250", "a251", "a252", "a253", "a254", "a255", "s8", "s9", "s10", "s11", "s12", "s13", "s14", "s15", "s16", "s17", "s18", "s19", "s20", "s21", "s22", "s23", "s24", "s25", "s26", "s27", "s28", "s29", "s30", "s31", "s32", "s33", "s34", "s35", "s36", "s37", "s38", "s39", "s40", "s41", "s42", "s43", "s44", "s45", "s46", "s47", "s48", "s49", "s50", "s51", "s52", "s53", "s54", "s55", "s56", "s57", "s58", "s59", "s60", "s61", "s62", "s63", "s64", "s65", "s66", "s67", "s68", "s69", "s70", "s71", "s72", "s73", "s74", "s75", "s76", "s77", "scc", "vcc", "memory");
}

extern "C" void kernel_launch(void* const* d_in, const int* in_sizes, int n_in, void* d_out, int out_size, void* d_ws,
                              size_t ws_size, hipStream_t stream) {
  wino_main_kernel<<<256, 256, 0, stream>>>((const char*)d_in[0], (char*)d_ws, (char*)d_out, (const char*)d_in[1]);
}
```

```cpp
#include <hip/hip_runtime.h>
#include <hip/hip_bf16.h>
#include <stdint.h>

#define IMG_W 512
#define CH 192
#define PXB 384
#define ROWB (IMG_W * PXB)
#define U_BYTES 1327104

__device__ const __attribute__((aligned(1024))) unsigned lane_tab[5888] = {
  0xa800,0xa820,0x0,0x3ea80a81,0xa808,0xa828,0x40,0x3ea80a81,0xa810,0xa830,0x90,0x3ea80a81,0xa818,0xa838,0xd0,0x3ea80a81,
  0xa920,0xa900,0x2400,0xbebd0bd1,0xa928,0xa908,0x2440,0xbebd0bd1,0xa930,0xa910,0x2490,0xbebd0bd1,0xa938,0xa918,0x24d0,0xbebd0bd1,
  0xa840,0xa860,0x200,0x3ea80a81,0xa848,0xa868,0x240,0x3ea80a81,0xa850,0xa870,0x290,0x3ea80a81,0xa858,0xa878,0x2d0,0x3ea80a81,
  0xa960,0xa940,0x200,0x3ea80a81,0xa968,0xa948,0x240,0x3ea80a81,0xa970,0xa950,0x290,0x3ea80a81,0xa978,0xa958,0x2d0,0x3ea80a81,
  0xea80,0xeaa0,0x10,0x3ea80a81,0xea88,0xeaa8,0x50,0x3ea80a81,0xea90,0xeab0,0x80,0x3ea80a81,0xea98,0xeab8,0xc0,0x3ea80a81,
  0xa9a0,0xa980,0x2410,0xbebd0bd1,0xa9a8,0xa988,0x2450,0xbebd0bd1,0xa9b0,0xa990,0x2480,0xbebd0bd1,0xa9b8,0xa998,0x24c0,0xbebd0bd1,
  0xc0c0,0xc0e0,0x210,0x3ea80a81,0xc0c8,0xc0e8,0x250,0x3ea80a81,0xc0d0,0xc0f0,0x280,0x3ea80a81,0xc0d8,0xc0f8,0x2c0,0x3ea80a81,
  0xc1e0,0xc1c0,0x210,0x3ea80a81,0xc1e8,0xc1c8,0x250,0x3ea80a81,0xc1f0,0xc1d0,0x280,0x3ea80a81,0xc1f8,0xc1d8,0x2c0,0x3ea80a81,
  0xae00,0xae20,0x20,0x3ea80a81,0xae08,0xae28,0x60,0x3ea80a81,0xae10,0xae30,0xb0,0x3ea80a81,0xae18,0xae38,0xf0,0x3ea80a81,
  0xeb20,0xeb00,0x2420,0xbebd0bd1,0xeb28,0xeb08,0x2460,0xbebd0bd1,0xeb30,0xeb10,0x24b0,0xbebd0bd1,0xeb38,0xeb18,0x24f0,0xbebd0bd1,
  0xc040,0xc060,0x220,0x3ea80a81,0xc048,0xc068,0x260,0x3ea80a81,0xc050,0xc070,0x2b0,0x3ea80a81,0xc058,0xc078,0x2f0,0x3ea80a81,
  0xc160,0xc140,0x220,0x3ea80a81,0xc168,0xc148,0x260,0x3ea80a81,0xc170,0xc150,0x2b0,0x3ea80a81,0xc178,0xc158,0x2f0,0x3ea80a81,
  0xa880,0xa8a0,0x30,0x3ea80a81,0xa888,0xa8a8,0x70,0x3ea80a81,0xa890,0xa8b0,0xa0,0x3ea80a81,0xa898,0xa8b8,0xe0,0x3ea80a81,
  0xa8c0,0xa8e0,0x2430,0xbebd0bd1,0xa8c8,0xa8e8,0x2470,0xbebd0bd1,0xa8d0,0xa8f0,0x24a0,0xbebd0bd1,0xa8d8,0xa8f8,0x24e0,0xbebd0bd1,
  0xa9e0,0xa9c0,0x230,0x3ea80a81,0xa9e8,0xa9c8,0x270,0x3ea80a81,0xa9f0,0xa9d0,0x2a0,0x3ea80a81,0xa9f8,0xa9d8,0x2e0,0x3ea80a81,
  0xae00,0xae20,0x230,0x3ea80a81,0xae08,0xae28,0x270,0x3ea80a81,0xae10,0xae30,0x2a0,0x3ea80a81,0xae18,0xae38,0x2e0,0x3ea80a81,
  0x0,0xc042,0x0,0x0,0x0,0xc0420000,0x0,0x0,0x0,0x0,0x0,0xc042,0x0,0x0,0x0,0xc0420000,
  0x3f903f90,0x0,0x0,0x0,0x3f900000,0x0,0x3f90,0x0,0x0,0x0,0x3f903f90,0x0,0x0,0x0,0x3f900000,0x0,
  0x0,0x0,0x0,0x0,0x0,0x0,0x0,0x0,0x0,0x0,0x0,0x0,0x0,0x0,0x0,0x0,
  0x0,0x0,0x0,0x0,0x0,0x0,0x0,0x0,0x0,0x0,0x0,0x0,0x0,0x0,0x0,0x0,
  0x0,0x0,0x3f90,0x0,0x0,0x0,0x0,0x0,0x0,0x0,0x0,0x0,0x0,0x0,0x0,0x0,
  0x0,0x0,0x0,0x0,0xc0420000,0x0,0x0,0x0,0x0,0x0,0x0,0x0,0x0,0x0,0xc0420000,0x0,
  0x0,0x3f90,0x0,0x0,0x0,0x3f903f90,0x0,0x0,0x0,0x3f900000,0x0,0x3f90,0x0,0x0,0x0,0x3f903f90,
  0x0,0x0,0x0,0x0,0x0,0x0,0x0,0x0,0x0,0x0,0x0,0x0,0x0,0x0,0x0,0x0,
  0x0,0x0,0x0,0x0,0x0,0x0,0x0,0x0,0x0,0x0,0x0,0x0,0x0,0x0,0x0,0x0,
  0x0,0x0,0x0,0x0,0x0,0x0,0x0,0x0,0x0,0x0,0x0,0x0,0x0,0x0,0x3f90,0x0,
  0x0,0xc042,0x3f900000,0x0,0x0,0xc0420000,0x0,0x0,0x0,0x0,0x0,0xc042,0x0,0x0,0x0,0xc0420000,
  0x0,0x0,0x0,0x0,0x0,0x0,0x0,0x0,0x0,0x0,0x0,0x0,0x0,0x0,0x0,0x0,
  0x3f900000,0x0,0x0,0x0,0x3f900000,0x3f90,0x0,0x0,0x0,0x3f90,0x3f900000,0x0,0x0,0x0,0x3f900000,0x3f90,
  0xc042,0x0,0x0,0x0,0x0,0x0,0x0,0x0,0x0,0x0,0xc042,0x0,0x0,0x0,0x0,0x0,
  0x0,0x0,0x0,0x0,0x0,0x0,0x0,0x0,0x0,0x0,0x0,0x0,0x0,0x0,0x0,0x0,
  0x0,0x0,0x0,0x0,0x0,0x0,0x0,0x0,0x0,0x0,0x0,0x0,0x0,0x0,0x0,0x0,
  0x0,0x0,0xa800,0x0,0x40,0x40,0xa808,0x10,0x90,0x90,0xa810,0x20,0xd0,0xd0,0xa818,0x30,
  0x120,0x120,0xa840,0x180,0x160,0x160,0xa848,0x190,0x1b0,0x1b0,0xa850,0x1a0,0x1f0,0x1f0,0xa858,0x1b0,
  0x1000,0x2408,0xa880,0x300,0x1040,0x2448,0xa888,0x310,0x1090,0x2498,0xa890,0x320,0x10d0,0x24d8,0xa898,0x330,
  0x1120,0x2528,0xa8c0,0x480,0x1160,0x2568,0xa8c8,0x490,0x11b0,0x25b8,0xa8d0,0x4a0,0x11f0,0x25f8,0xa8d8,0x4b0,
  0x10,0x10,0xa920,0x620,0x50,0x50,0xa928,0x630,0x80,0x80,0xa930,0x600,0xc0,0xc0,0xa938,0x610,
  0x130,0x130,0xa960,0x7a0,0x170,0x170,0xa968,0x7b0,0x1a0,0x1a0,0xa970,0x780,0x1e0,0x1e0,0xa978,0x790,
  0x1010,0x2418,0xa9a0,0x920,0x1050,0x2458,0xa9a8,0x930,0x1080,0x2488,0xa9b0,0x900,0x10c0,0x24c8,0xa9b8,0x910,
  0x1130,0x2538,0xa9e0,0xaa0,0x1170,0x2578,0xa9e8,0xab0,0x11a0,0x25a8,0xa9f0,0xa80,0x11e0,0x25e8,0xa9f8,0xa90,
  0x20,0x20,0xae00,0x40,0x60,0x60,0xae08,0x50,0xb0,0xb0,0xae10,0x60,0xf0,0xf0,0xae18,0x70,
  0x100,0x100,0xae40,0x1c0,0x140,0x140,0xae48,0x1d0,0x190,0x190,0xae50,0x1e0,0x1d0,0x1d0,0xae58,0x1f0,
  0x1020,0x2428,0xae80,0x340,0x1060,0x2468,0xae88,0x350,0x10b0,0x24b8,0xae90,0x360,0x10f0,0x24f8,0xae98,0x370,
  0x1100,0x2508,0xaec0,0x4c0,0x1140,0x2548,0xaec8,0x4d0,0x1190,0x2598,0xaed0,0x4e0,0x11d0,0x25d8,0xaed8,0x4f0,
  0x30,0x30,0xaf20,0x660,0x70,0x70,0xaf28,0x670,0xa0,0xa0,0xaf30,0x640,0xe0,0xe0,0xaf38,0x650,
  0x110,0x110,0xaf60,0x7e0,0x150,0x150,0xaf68,0x7f0,0x180,0x180,0xaf70,0x7c0,0x1c0,0x1c0,0xaf78,0x7d0,
  0x1030,0x2438,0xafa0,0x960,0x1070,0x2478,0xafa8,0x970,0x10a0,0x24a8,0xafb0,0x940,0x10e0,0x24e8,0xafb8,0x950,
  0x1110,0x2518,0xafe0,0xae0,0x1150,0x2558,0xafe8,0xaf0,0x1180,0x2588,0xaff0,0xac0,0x11c0,0x25c8,0xaff8,0xad0,
  0xb400,0xb420,0x120,0x3ea80a81,0xb408,0xb428,0x160,0x3ea80a81,0xb410,0xb430,0x1b0,0x3ea80a81,0xb418,0xb438,0x1f0,0x3ea80a81,
  0xb440,0xb460,0x2520,0xbebd0bd1,0xb448,0xb468,0x2560,0xbebd0bd1,0xb450,0xb470,0x25b0,0xbebd0bd1,0xb458,0xb478,0x25f0,0xbebd0bd1,
  0xcc40,0xcc60,0x320,0x3ea80a81,0xcc48,0xcc68,0x360,0x3ea80a81,0xcc50,0xcc70,0x3b0,0x3ea80a81,0xcc58,0xcc78,0x3f0,0x3ea80a81,
  0xcd60,0xcd40,0x320,0x3ea80a81,0xcd68,0xcd48,0x360,0x3ea80a81,0xcd70,0xcd50,0x3b0,0x3ea80a81,0xcd78,0xcd58,0x3f0,0x3ea80a81,
  0xb5a0,0xb580,0x130,0x3ea80a81,0xb5a8,0xb588,0x170,0x3ea80a81,0xb5b0,0xb590,0x1a0,0x3ea80a81,0xb5b8,0xb598,0x1e0,0x3ea80a81,
  0xccc0,0xcce0,0x2530,0xbebd0bd1,0xccc8,0xcce8,0x2570,0xbebd0bd1,0xccd0,0xccf0,0x25a0,0xbebd0bd1,0xccd8,0xccf8,0x25e0,0xbebd0bd1,
  0xa9e0,0xa9c0,0x330,0x3ea80a81,0xa9e8,0xa9c8,0x370,0x3ea80a81,0xa9f0,0xa9d0,0x3a0,0x3ea80a81,0xa9f8,0xa9d8,0x3e0,0x3ea80a81,
  0xc1e0,0xc1c0,0x330,0x3ea80a81,0xc1e8,0xc1c8,0x370,0x3ea80a81,0xc1f0,0xc1d0,0x3a0,0x3ea80a81,0xc1f8,0xc1d8,0x3e0,0x3ea80a81,
  0xb520,0xb500,0x100,0x3ea80a81,0xb528,0xb508,0x140,0x3ea80a81,0xb530,0xb510,0x190,0x3ea80a81,0xb538,0xb518,0x1d0,0x3ea80a81,
  0xea40,0xea60,0x2500,0xbebd0bd1,0xea48,0xea68,0x2540,0xbebd0bd1,0xea50,0xea70,0x2590,0xbebd0bd1,0xea58,0xea78,0x25d0,0xbebd0bd1,
  0xb560,0xb540,0x300,0x3ea80a81,0xb568,0xb548,0x340,0x3ea80a81,0xb570,0xb550,0x390,0x3ea80a81,0xb578,0xb558,0x3d0,0x3ea80a81,
  0xb480,0xb4a0,0x300,0x3ea80a81,0xb488,0xb4a8,0x340,0x3ea80a81,0xb490,0xb4b0,0x390,0x3ea80a81,0xb498,0xb4b8,0x3d0,0x3ea80a81,
  0xb4c0,0xb4e0,0x110,0x3ea80a81,0xb4c8,0xb4e8,0x150,0x3ea80a81,0xb4d0,0xb4f0,0x180,0x3ea80a81,0xb4d8,0xb4f8,0x1c0,0x3ea80a81,
  0xb5e0,0xb5c0,0x2510,0xbebd0bd1,0xb5e8,0xb5c8,0x2550,0xbebd0bd1,0xb5f0,0xb5d0,0x2580,0xbebd0bd1,0xb5f8,0xb5d8,0x25c0,0xbebd0bd1,
  0xcde0,0xcdc0,0x310,0x3ea80a81,0xcde8,0xcdc8,0x350,0x3ea80a81,0xcdf0,0xcdd0,0x380,0x3ea80a81,0xcdf8,0xcdd8,0x3c0,0x3ea80a81,
  0xb520,0xb500,0x310,0x3ea80a81,0xb528,0xb508,0x350,0x3ea80a81,0xb530,0xb510,0x380,0x3ea80a81,0xb538,0xb518,0x3c0,0x3ea80a81,
  0xc0420000,0x0,0x0,0x0,0x0,0x0,0x0,0x0,0x0,0x0,0xc0420000,0x0,0x0,0x0,0x0,0x0,
  0x3f90,0x0,0x0,0x0,0x0,0x0,0x3f90,0x0,0x0,0x0,0x3f90,0x0,0x0,0x0,0x0,0x0,
  0x0,0xc042,0x0,0x0,0x0,0xc0420000,0x0,0x0,0x0,0x0,0x0,0xc042,0x0,0x0,0x0,0xc0420000,
  0x0,0x0,0x0,0x0,0x0,0x0,0x0,0x0,0x0,0x0,0x0,0x0,0x0,0x0,0x0,0x0,
  0x0,0x0,0x0,0x3f90,0x0,0x0,0x0,0x0,0x0,0x0,0x0,0x0,0x0,0x0,0x0,0x0,
  0x0,0x0,0x0,0x0,0xc042,0x0,0x0,0x0,0x0,0x0,0x0,0x0,0x0,0x0,0xc042,0x0,
  0x3f900000,0x0,0x0,0x3f900000,0x3f900000,0x0,0x0,0x0,0x0,0x0,0x3f900000,0x0,0x0,0x0,0x3f900000,0x0,
  0x0,0x0,0x0,0x0,0x0,0x0,0x0,0x0,0x0,0x0,0x0,0x0,0x0,0x0,0x0,0x0,
  0x0,0x0,0x0,0x0,0x0,0xc042,0x0,0x0,0x0,0x0,0x0,0x0,0x0,0x0,0x0,0xc042,
  0x3f90,0xc0420000,0x0,0x0,0x3f90,0x0,0x0,0x0,0x0,0x0,0x3f90,0xc0420000,0x0,0x0,0x3f903f90,0x0,
  0x0,0x0,0x0,0x0,0x0,0x0,0x0,0x0,0x0,0x0,0x0,0x0,0x0,0x0,0x0,0x0,
  0x0,0x0,0x0,0x0,0x0,0x0,0x0,0x0,0x0,0x0,0x0,0x0,0x0,0x0,0x0,0x0,
  0x3f90,0x0,0x0,0x0,0x3f903f90,0x0,0x0,0x0,0x3f900000,0x0,0x3f90,0x0,0x0,0x0,0x3f903f90,0x0,
  0x0,0x0,0x0,0x0,0x0,0x0,0x0,0x0,0x0,0x0,0x0,0x0,0x0,0x0,0x0,0x0,
  0x0,0x0,0x0,0x0,0x0,0x3f90,0x0,0x0,0x0,0x3f90,0x0,0x0,0x0,0x0,0x0,0x3f90,
  0x0,0x0,0x0,0x0,0x0,0x0,0x0,0x0,0x0,0x0,0x0,0x0,0x0,0x0,0x0,0x0,
  0x0,0x8,0xa820,0xc00,0x40,0x48,0xa828,0xc10,0x90,0x98,0xa830,0xc20,0xd0,0xd8,0xa838,0xc30,
  0x120,0x128,0xa860,0xd80,0x160,0x168,0xa868,0xd90,0x1b0,0x1b8,0xa870,0xda0,0x1f0,0x1f8,0xa878,0xdb0,
  0x1000,0x2400,0xa8a0,0xf00,0x1040,0x2440,0xa8a8,0xf10,0x1090,0x2490,0xa8b0,0xf20,0x10d0,0x24d0,0xa8b8,0xf30,
  0x1120,0x2520,0xa8e0,0x1080,0x1160,0x2560,0xa8e8,0x1090,0x11b0,0x25b0,0xa8f0,0x10a0,0x11f0,0x25f0,0xa8f8,0x10b0,
  0x10,0x18,0xa900,0x1220,0x50,0x58,0xa908,0x1230,0x80,0x88,0xa910,0x1200,0xc0,0xc8,0xa918,0x1210,
  0x130,0x138,0xa940,0x13a0,0x170,0x178,0xa948,0x13b0,0x1a0,0x1a8,0xa950,0x1380,0x1e0,0x1e8,0xa958,0x1390,
  0x1010,0x2410,0xa980,0x1520,0x1050,0x2450,0xa988,0x1530,0x1080,0x2480,0xa990,0x1500,0x10c0,0x24c0,0xa998,0x1510,
  0x1130,0x2530,0xa9c0,0x16a0,0x1170,0x2570,0xa9c8,0x16b0,0x11a0,0x25a0,0xa9d0,0x1680,0x11e0,0x25e0,0xa9d8,0x1690,
  0x20,0x28,0xae20,0xc40,0x60,0x68,0xae28,0xc50,0xb0,0xb8,0xae30,0xc60,0xf0,0xf8,0xae38,0xc70,
  0x100,0x108,0xae60,0xdc0,0x140,0x148,0xae68,0xdd0,0x190,0x198,0xae70,0xde0,0x1d0,0x1d8,0xae78,0xdf0,
  0x1020,0x2420,0xaea0,0xf40,0x1060,0x2460,0xaea8,0xf50,0x10b0,0x24b0,0xaeb0,0xf60,0x10f0,0x24f0,0xaeb8,0xf70,
  0x1100,0x2500,0xaee0,0x10c0,0x1140,0x2540,0xaee8,0x10d0,0x1190,0x2590,0xaef0,0x10e0,0x11d0,0x25d0,0xaef8,0x10f0,
  0x30,0x38,0xaf00,0x1260,0x70,0x78,0xaf08,0x1270,0xa0,0xa8,0xaf10,0x1240,0xe0,0xe8,0xaf18,0x1250,
  0x110,0x118,0xaf40,0x13e0,0x150,0x158,0xaf48,0x13f0,0x180,0x188,0xaf50,0x13c0,0x1c0,0x1c8,0xaf58,0x13d0,
  0x1030,0x2430,0xaf80,0x1560,0x1070,0x2470,0xaf88,0x1570,0x10a0,0x24a0,0xaf90,0x1540,0x10e0,0x24e0,0xaf98,0x1550,
  0x1110,0x2510,0xafc0,0x16e0,0x1150,0x2550,0xafc8,0x16f0,0x1180,0x2580,0xafd0,0x16c0,0x11c0,0x25c0,0xafd8,0x16d0,
  0xc000,0xc020,0x2600,0xbebd0bd1,0xc008,0xc028,0x2640,0xbebd0bd1,0xc010,0xc030,0x2690,0xbebd0bd1,0xc018,0xc038,0x26d0,0xbebd0bd1,
  0xd800,0xd820,0x400,0x3ea80a81,0xd808,0xd828,0x440,0x3ea80a81,0xd810,0xd830,0x490,0x3ea80a81,0xd818,0xd838,0x4d0,0x3ea80a81,
  0xc120,0xc100,0x2800,0xbebd0bd1,0xc128,0xc108,0x2840,0xbebd0bd1,0xc130,0xc110,0x2890,0xbebd0bd1,0xc138,0xc118,0x28d0,0xbebd0bd1,
  0xd840,0xd860,0x2800,0xbebd0bd1,0xd848,0xd868,0x2840,0xbebd0bd1,0xd850,0xd870,0x2890,0xbebd0bd1,0xd858,0xd878,0x28d0,0xbebd0bd1,
  0xc080,0xc0a0,0x2610,0xbebd0bd1,0xc088,0xc0a8,0x2650,0xbebd0bd1,0xc090,0xc0b0,0x2680,0xbebd0bd1,0xc098,0xc0b8,0x26c0,0xbebd0bd1,
  0xc1a0,0xc180,0x410,0x3ea80a81,0xc1a8,0xc188,0x450,0x3ea80a81,0xc1b0,0xc190,0x480,0x3ea80a81,0xc1b8,0xc198,0x4c0,0x3ea80a81,
  0xd9a0,0xd980,0x2810,0xbebd0bd1,0xd9a8,0xd988,0x2850,0xbebd0bd1,0xd9b0,0xd990,0x2880,0xbebd0bd1,0xd9b8,0xd998,0x28c0,0xbebd0bd1,
  0xd9e0,0xd9c0,0x2810,0xbebd0bd1,0xd9e8,0xd9c8,0x2850,0xbebd0bd1,0xd9f0,0xd9d0,0x2880,0xbebd0bd1,0xd9f8,0xd9d8,0x28c0,0xbebd0bd1,
  0xc600,0xc620,0x2620,0xbebd0bd1,0xc608,0xc628,0x2660,0xbebd0bd1,0xc610,0xc630,0x26b0,0xbebd0bd1,0xc618,0xc638,0x26f0,0xbebd0bd1,
  0xde00,0xde20,0x420,0x3ea80a81,0xde08,0xde28,0x460,0x3ea80a81,0xde10,0xde30,0x4b0,0x3ea80a81,0xde18,0xde38,0x4f0,0x3ea80a81,
  0xd920,0xd900,0x2820,0xbebd0bd1,0xd928,0xd908,0x2860,0xbebd0bd1,0xd930,0xd910,0x28b0,0xbebd0bd1,0xd938,0xd918,0x28f0,0xbebd0bd1,
  0xd960,0xd940,0x2820,0xbebd0bd1,0xd968,0xd948,0x2860,0xbebd0bd1,0xd970,0xd950,0x28b0,0xbebd0bd1,0xd978,0xd958,0x28f0,0xbebd0bd1,
  0xd880,0xd8a0,0x2630,0xbebd0bd1,0xd888,0xd8a8,0x2670,0xbebd0bd1,0xd890,0xd8b0,0x26a0,0xbebd0bd1,0xd898,0xd8b8,0x26e0,0xbebd0bd1,
  0xeba0,0xeb80,0x430,0x3ea80a81,0xeba8,0xeb88,0x470,0x3ea80a81,0xebb0,0xeb90,0x4a0,0x3ea80a81,0xebb8,0xeb98,0x4e0,0x3ea80a81,
  0xd8c0,0xd8e0,0x2830,0xbebd0bd1,0xd8c8,0xd8e8,0x2870,0xbebd0bd1,0xd8d0,0xd8f0,0x28a0,0xbebd0bd1,0xd8d8,0xd8f8,0x28e0,0xbebd0bd1,
  0xc600,0xc620,0x2830,0xbebd0bd1,0xc608,0xc628,0x2870,0xbebd0bd1,0xc610,0xc630,0x28a0,0xbebd0bd1,0xc618,0xc638,0x28e0,0xbebd0bd1,
  0x3f90,0x3f90,0x0,0x0,0x0,0x3f90,0x3f90,0x0,0x0,0x0,0x3f90,0x3f90,0x0,0x0,0x0,0x3f90,
  0x0,0xc0420000,0x0,0x0,0x0,0x0,0x0,0x0,0x0,0x0,0x0,0xc0420000,0x0,0x0,0x0,0x0,
  0x3f900000,0x0,0x0,0x0,0x0,0x0,0x3f900000,0x0,0x0,0x0,0x3f900000,0x0,0x0,0x0,0x0,0x0,
  0x0,0x0,0x0,0x0,0x0,0x0,0x0,0x0,0x0,0x0,0x0,0x0,0x0,0x0,0x0,0x0,
  0xc042,0x0,0x0,0x0,0xc0420000,0x0,0x0,0x0,0x0,0x0,0xc042,0x0,0x0,0x0,0xc0420000,0x0,
  0x0,0x0,0x0,0x0,0x0,0x3f900000,0x0,0x0,0x0,0x3f900000,0x0,0x0,0x0,0x0,0x0,0x3f900000,
  0x0,0x0,0x0,0x0,0x0,0xc042,0x0,0x0,0x0,0x0,0x0,0x0,0x0,0x0,0x0,0xc042,
  0x0,0x0,0x0,0x0,0x0,0x0,0x0,0x0,0x0,0x0,0x0,0x0,0x0,0x0,0x0,0x0,
  0x0,0x0,0x0,0x0,0x0,0x0,0x0,0x0,0x0,0x0,0x0,0x0,0x0,0x0,0x3f90,0x0,
  0x0,0x0,0x0,0x0,0x0,0xc0420000,0x0,0x0,0x0,0x0,0x0,0x0,0x0,0x0,0x0,0xc0420000,
  0x0,0x3f90,0x0,0x0,0x0,0x3f90,0x0,0x0,0x0,0x0,0x0,0x3f90,0x0,0x0,0x3f900000,0x3f90,
  0x0,0x0,0x0,0x0,0x0,0x0,0x0,0x0,0x0,0x0,0x0,0x0,0x0,0x0,0x0,0x0,
  0x0,0x0,0x0,0x0,0x0,0x0,0x0,0x0,0x0,0x0,0x0,0x0,0x0,0x0,0x0,0x0,
  0x0,0x3f90,0x3f900000,0x0,0x0,0x3f90,0x0,0x0,0x0,0x0,0x0,0x3f90,0x0,0x0,0x0,0x3f90,
  0xc042,0x0,0x0,0x0,0x0,0x0,0x0,0x0,0x0,0x0,0xc042,0x0,0x0,0x0,0x0,0x0,
  0x0,0x0,0x0,0x0,0x0,0x0,0x0,0x0,0x0,0x0,0x0,0x0,0x0,0x0,0x0,0x0,
  0x0,0x1200,0x19200,0x1800,0x40,0x1240,0x19208,0x1810,0x90,0x1290,0x19210,0x1820,0xd0,0x12d0,0x19218,0x1830,
  0x120,0x1320,0x19240,0x1980,0x160,0x1360,0x19248,0x1990,0x1b0,0x13b0,0x19250,0x19a0,0x1f0,0x13f0,0x19258,0x19b0,
  0x1000,0x3608,0x19280,0x1b00,0x1040,0x3648,0x19288,0x1b10,0x1090,0x3698,0x19290,0x1b20,0x10d0,0x36d8,0x19298,0x1b30,
  0x1120,0x3728,0x192c0,0x1c80,0x1160,0x3768,0x192c8,0x1c90,0x11b0,0x37b8,0x192d0,0x1ca0,0x11f0,0x37f8,0x192d8,0x1cb0,
  0x10,0x1210,0x19320,0x1e20,0x50,0x1250,0x19328,0x1e30,0x80,0x1280,0x19330,0x1e00,0xc0,0x12c0,0x19338,0x1e10,
  0x130,0x1330,0x19360,0x1fa0,0x170,0x1370,0x19368,0x1fb0,0x1a0,0x13a0,0x19370,0x1f80,0x1e0,0x13e0,0x19378,0x1f90,
  0x1010,0x3618,0x193a0,0x2120,0x1050,0x3658,0x193a8,0x2130,0x1080,0x3688,0x193b0,0x2100,0x10c0,0x36c8,0x193b8,0x2110,
  0x1130,0x3738,0x193e0,0x22a0,0x1170,0x3778,0x193e8,0x22b0,0x11a0,0x37a8,0x193f0,0x2280,0x11e0,0x37e8,0x193f8,0x2290,
  0x20,0x1220,0x19800,0x1840,0x60,0x1260,0x19808,0x1850,0xb0,0x12b0,0x19810,0x1860,0xf0,0x12f0,0x19818,0x1870,
  0x100,0x1300,0x19840,0x19c0,0x140,0x1340,0x19848,0x19d0,0x190,0x1390,0x19850,0x19e0,0x1d0,0x13d0,0x19858,0x19f0,
  0x1020,0x3628,0x19880,0x1b40,0x1060,0x3668,0x19888,0x1b50,0x10b0,0x36b8,0x19890,0x1b60,0x10f0,0x36f8,0x19898,0x1b70,
  0x1100,0x3708,0x198c0,0x1cc0,0x1140,0x3748,0x198c8,0x1cd0,0x1190,0x3798,0x198d0,0x1ce0,0x11d0,0x37d8,0x198d8,0x1cf0,
  0x30,0x1230,0x19920,0x1e60,0x70,0x1270,0x19928,0x1e70,0xa0,0x12a0,0x19930,0x1e40,0xe0,0x12e0,0x19938,0x1e50,
  0x110,0x1310,0x19960,0x1fe0,0x150,0x1350,0x19968,0x1ff0,0x180,0x1380,0x19970,0x1fc0,0x1c0,0x13c0,0x19978,0x1fd0,
  0x1030,0x3638,0x199a0,0x2160,0x1070,0x3678,0x199a8,0x2170,0x10a0,0x36a8,0x199b0,0x2140,0x10e0,0x36e8,0x199b8,0x2150,
  0x1110,0x3718,0x199e0,0x22e0,0x1150,0x3758,0x199e8,0x22f0,0x1180,0x3788,0x199f0,0x22c0,0x11c0,0x37c8,0x199f8,0x22d0,
  0xcc00,0xcc20,0x2720,0xbebd0bd1,0xcc08,0xcc28,0x2760,0xbebd0bd1,0xcc10,0xcc30,0x27b0,0xbebd0bd1,0xcc18,0xcc38,0x27f0,0xbebd0bd1,
  0xcd20,0xcd00,0x520,0x3ea80a81,0xcd28,0xcd08,0x560,0x3ea80a81,0xcd30,0xcd10,0x5b0,0x3ea80a81,0xcd38,0xcd18,0x5f0,0x3ea80a81,
  0xe440,0xe460,0x2920,0xbebd0bd1,0xe448,0xe468,0x2960,0xbebd0bd1,0xe450,0xe470,0x29b0,0xbebd0bd1,0xe458,0xe478,0x29f0,0xbebd0bd1,
  0xeb60,0xeb40,0x2920,0xbebd0bd1,0xeb68,0xeb48,0x2960,0xbebd0bd1,0xeb70,0xeb50,0x29b0,0xbebd0bd1,0xeb78,0xeb58,0x29f0,0xbebd0bd1,
  0xe480,0xe4a0,0x2730,0xbebd0bd1,0xe488,0xe4a8,0x2770,0xbebd0bd1,0xe490,0xe4b0,0x27a0,0xbebd0bd1,0xe498,0xe4b8,0x27e0,0xbebd0bd1,
  0xe5a0,0xe580,0x530,0x3ea80a81,0xe5a8,0xe588,0x570,0x3ea80a81,0xe5b0,0xe590,0x5a0,0x3ea80a81,0xe5b8,0xe598,0x5e0,0x3ea80a81,
  0xe4c0,0xe4e0,0x2930,0xbebd0bd1,0xe4c8,0xe4e8,0x2970,0xbebd0bd1,0xe4d0,0xe4f0,0x29a0,0xbebd0bd1,0xe4d8,0xe4f8,0x29e0,0xbebd0bd1,
  0xd9e0,0xd9c0,0x2930,0xbebd0bd1,0xd9e8,0xd9c8,0x2970,0xbebd0bd1,0xd9f0,0xd9d0,0x29a0,0xbebd0bd1,0xd9f8,0xd9d8,0x29e0,0xbebd0bd1,
  0xe400,0xe420,0x2700,0xbebd0bd1,0xe408,0xe428,0x2740,0xbebd0bd1,0xe410,0xe430,0x2790,0xbebd0bd1,0xe418,0xe438,0x27d0,0xbebd0bd1,
  0xe520,0xe500,0x500,0x3ea80a81,0xe528,0xe508,0x540,0x3ea80a81,0xe530,0xe510,0x590,0x3ea80a81,0xe538,0xe518,0x5d0,0x3ea80a81,
  0xe560,0xe540,0x2900,0xbebd0bd1,0xe568,0xe548,0x2940,0xbebd0bd1,0xe570,0xe550,0x2990,0xbebd0bd1,0xe578,0xe558,0x29d0,0xbebd0bd1,
  0xcc80,0xcca0,0x2900,0xbebd0bd1,0xcc88,0xcca8,0x2940,0xbebd0bd1,0xcc90,0xccb0,0x2990,0xbebd0bd1,0xcc98,0xccb8,0x29d0,0xbebd0bd1,
  0xcda0,0xcd80,0x2710,0xbebd0bd1,0xcda8,0xcd88,0x2750,0xbebd0bd1,0xcdb0,0xcd90,0x2780,0xbebd0bd1,0xcdb8,0xcd98,0x27c0,0xbebd0bd1,
  0xeac0,0xeae0,0x510,0x3ea80a81,0xeac8,0xeae8,0x550,0x3ea80a81,0xead0,0xeaf0,0x580,0x3ea80a81,0xead8,0xeaf8,0x5c0,0x3ea80a81,
  0xe5e0,0xe5c0,0x2910,0xbebd0bd1,0xe5e8,0xe5c8,0x2950,0xbebd0bd1,0xe5f0,0xe5d0,0x2980,0xbebd0bd1,0xe5f8,0xe5d8,0x29c0,0xbebd0bd1,
  0xe400,0xe420,0x2910,0xbebd0bd1,0xe408,0xe428,0x2950,0xbebd0bd1,0xe410,0xe430,0x2980,0xbebd0bd1,0xe418,0xe438,0x29c0,0xbebd0bd1,
  0x3f903f90,0x0,0x0,0x0,0x3f900000,0x0,0x3f90,0x0,0x0,0x0,0x3f903f90,0x0,0x0,0x0,0x3f900000,0x0,
  0x0,0xc042,0x0,0x0,0x0,0x0,0x0,0x0,0x0,0x0,0x0,0xc042,0x0,0x0,0x0,0x0,
  0x0,0x0,0x0,0x0,0x0,0x0,0x0,0x0,0x0,0x0,0x0,0x0,0x0,0x0,0x0,0x3f900000,
  0x0,0x0,0x0,0x0,0x0,0x0,0x0,0x0,0x0,0x0,0x0,0x0,0x0,0x0,0x0,0x0,
  0x0,0x0,0x0,0x0,0x0,0x0,0x0,0x0,0x0,0x0,0x0,0x0,0x0,0x0,0x0,0x0,
  0x0,0x3f90,0x0,0x3f900000,0x0,0x3f90,0x0,0x0,0x0,0x0,0x0,0x3f90,0x0,0x0,0x0,0x3f90,
  0xc042,0x0,0x0,0x0,0xc0420000,0x0,0x0,0x0,0x0,0x0,0xc042,0x0,0x0,0x0,0xc0420000,0x0,
  0x0,0x0,0x0,0x0,0x0,0x0,0x0,0x0,0x0,0x0,0x0,0x0,0x0,0x0,0x0,0x0,
  0x0,0xc0420000,0x0,0x0,0x0,0x0,0x0,0x0,0x0,0x0,0x0,0xc0420000,0x0,0x0,0x0,0x0,
  0x0,0x0,0x0,0x0,0x0,0xc042,0x0,0x0,0x0,0x0,0x0,0x0,0x0,0x0,0x0,0xc042,
  0x3f903f90,0x0,0x0,0x0,0x3f900000,0x0,0x3f90,0x0,0x0,0x0,0x3f903f90,0x0,0x0,0x0,0x3f900000,0x0,
  0x0,0x0,0x0,0x0,0x0,0x0,0x0,0x0,0x0,0x0,0x0,0x0,0x0,0x0,0x0,0x0,
  0x0,0x0,0x0,0x0,0xc042,0x0,0x0,0x0,0x0,0x0,0x0,0x0,0x0,0x0,0x3f90c042,0x0,
  0x0,0x0,0x0,0x0,0x0,0x3f90,0x0,0x0,0x0,0x3f90,0x0,0x0,0x0,0x0,0x0,0x3f90,
  0x0,0x0,0x0,0x0,0x0,0x0,0x0,0x0,0x0,0x0,0x0,0x0,0x0,0x0,0x0,0x0,
  0x0,0x0,0x0,0x0,0x0,0x0,0x0,0x0,0x0,0x0,0x0,0x0,0x0,0x0,0x0,0x0,
  0x0,0x1208,0x19220,0x2400,0x40,0x1248,0x19228,0x2410,0x90,0x1298,0x19230,0x2420,0xd0,0x12d8,0x19238,0x2430,
  0x120,0x1328,0x19260,0x2580,0x160,0x1368,0x19268,0x2590,0x1b0,0x13b8,0x19270,0x25a0,0x1f0,0x13f8,0x19278,0x25b0,
  0x1000,0x3600,0x192a0,0x2700,0x1040,0x3640,0x192a8,0x2710,0x1090,0x3690,0x192b0,0x2720,0x10d0,0x36d0,0x192b8,0x2730,
  0x1120,0x3720,0x192e0,0x2880,0x1160,0x3760,0x192e8,0x2890,0x11b0,0x37b0,0x192f0,0x28a0,0x11f0,0x37f0,0x192f8,0x28b0,
  0x10,0x1218,0x19300,0x2a20,0x50,0x1258,0x19308,0x2a30,0x80,0x1288,0x19310,0x2a00,0xc0,0x12c8,0x19318,0x2a10,
  0x130,0x1338,0x19340,0x2ba0,0x170,0x1378,0x19348,0x2bb0,0x1a0,0x13a8,0x19350,0x2b80,0x1e0,0x13e8,0x19358,0x2b90,
  0x1010,0x3610,0x19380,0x2d20,0x1050,0x3650,0x19388,0x2d30,0x1080,0x3680,0x19390,0x2d00,0x10c0,0x36c0,0x19398,0x2d10,
  0x1130,0x3730,0x193c0,0x2ea0,0x1170,0x3770,0x193c8,0x2eb0,0x11a0,0x37a0,0x193d0,0x2e80,0x11e0,0x37e0,0x193d8,0x2e90,
  0x20,0x1228,0x19820,0x2440,0x60,0x1268,0x19828,0x2450,0xb0,0x12b8,0x19830,0x2460,0xf0,0x12f8,0x19838,0x2470,
  0x100,0x1308,0x19860,0x25c0,0x140,0x1348,0x19868,0x25d0,0x190,0x1398,0x19870,0x25e0,0x1d0,0x13d8,0x19878,0x25f0,
  0x1020,0x3620,0x198a0,0x2740,0x1060,0x3660,0x198a8,0x2750,0x10b0,0x36b0,0x198b0,0x2760,0x10f0,0x36f0,0x198b8,0x2770,
  0x1100,0x3700,0x198e0,0x28c0,0x1140,0x3740,0x198e8,0x28d0,0x1190,0x3790,0x198f0,0x28e0,0x11d0,0x37d0,0x198f8,0x28f0,
  0x30,0x1238,0x19900,0x2a60,0x70,0x1278,0x19908,0x2a70,0xa0,0x12a8,0x19910,0x2a40,0xe0,0x12e8,0x19918,0x2a50,
  0x110,0x1318,0x19940,0x2be0,0x150,0x1358,0x19948,0x2bf0,0x180,0x1388,0x19950,0x2bc0,0x1c0,0x13c8,0x19958,0x2bd0,
  0x1030,0x3630,0x19980,0x2d60,0x1070,0x3670,0x19988,0x2d70,0x10a0,0x36a0,0x19990,0x2d40,0x10e0,0x36e0,0x19998,0x2d50,
  0x1110,0x3710,0x199c0,0x2ee0,0x1150,0x3750,0x199c8,0x2ef0,0x1180,0x3780,0x199d0,0x2ec0,0x11c0,0x37c0,0x199d8,0x2ed0,
  0x0,0x0,0x800,0x10000000,0x10,0x90,0x890,0x10000000,0x20,0x120,0x920,0x10000000,0x30,0x1b0,0x9b0,0x10000000,
  0x40,0x240,0xa40,0x20003180,0x50,0x2d0,0xad0,0x20003190,0x60,0x360,0xb60,0x200031a0,0x70,0x3f0,0xbf0,0x200031b0,
  0x610,0xc00,0x1400,0x40000000,0x600,0xc90,0x1490,0x40000010,0x630,0xd20,0x1520,0x40000020,0x620,0xdb0,0x15b0,0x40000030,
  0x650,0xe40,0x1640,0x20033180,0x640,0xed0,0x16d0,0x20033190,0x670,0xf60,0x1760,0x200331a0,0x660,0xff0,0x17f0,0x200331b0,
  0xc20,0x10,0x808,0x40030020,0xc30,0x80,0x898,0x40030030,0xc00,0x130,0x928,0x40030000,0xc10,0x1a0,0x9b8,0x40030010,
  0xc60,0x250,0xa48,0x200631a0,0xc70,0x2c0,0xad8,0x200631b0,0xc40,0x370,0xb68,0x20063180,0xc50,0x3e0,0xbf8,0x20063190,
  0x1230,0xc10,0x1408,0x40060020,0x1220,0xc80,0x1498,0x40060030,0x1210,0xd30,0x1528,0x40060000,0x1200,0xda0,0x15b8,0x40060010,
  0x1270,0xe50,0x1648,0x10000000,0x1260,0xec0,0x16d8,0x10000000,0x1250,0xf70,0x1768,0x10000000,0x1240,0xfe0,0x17f8,0x10000000,
  0x1840,0x20,0x810,0x10000000,0x1850,0xb0,0x880,0x10000000,0x1860,0x100,0x930,0x10000000,0x1870,0x190,0x9a0,0x10000000,
  0x1800,0x260,0xa50,0x200031c0,0x1810,0x2f0,0xac0,0x200031d0,0x1820,0x340,0xb70,0x200031e0,0x1830,0x3d0,0xbe0,0x200031f0,
  0x1e50,0xc20,0x1410,0x40000040,0x1e40,0xcb0,0x1480,0x40000050,0x1e70,0xd00,0x1530,0x40000060,0x1e60,0xd90,0x15a0,0x40000070,
  0x1e10,0xe60,0x1650,0x200331c0,0x1e00,0xef0,0x16c0,0x200331d0,0x1e30,0xf40,0x1770,0x200331e0,0x1e20,0xfd0,0x17e0,0x200331f0,
  0x2460,0x30,0x818,0x40030060,0x2470,0xa0,0x888,0x40030070,0x2440,0x110,0x938,0x40030040,0x2450,0x180,0x9a8,0x40030050,
  0x2420,0x270,0xa58,0x200631e0,0x2430,0x2e0,0xac8,0x200631f0,0x2400,0x350,0xb78,0x200631c0,0x2410,0x3c0,0xbe8,0x200631d0,
  0x2a70,0xc30,0x1418,0x40060060,0x2a60,0xca0,0x1488,0x40060070,0x2a50,0xd10,0x1538,0x40060040,0x2a40,0xd80,0x15a8,0x40060050,
  0x2a30,0xe70,0x1658,0x10000000,0x2a20,0xee0,0x16c8,0x10000000,0x2a10,0xf50,0x1778,0x10000000,0x2a00,0xfc0,0x17e8,0x10000000,
  0xbfd8bf90,0x3f403f00,0x0,0x0,0x0,0x0,0x0,0x0,0x0,0x0,0x0,0x0,0x0,0x0,0x0,0x0,
  0x0,0x0,0xbfd8bf90,0x3f403f00,0x0,0x0,0x0,0x0,0x0,0x0,0x0,0x0,0x0,0x0,0x0,0x0,
  0xbfd83f90,0x3f40bf00,0x0,0x0,0x0,0x0,0x0,0x0,0x0,0x0,0x0,0x0,0x0,0x0,0x0,0x0,
  0x0,0x0,0xbfd83f90,0x3f40bf00,0x0,0x0,0x0,0x0,0x0,0x0,0x0,0x0,0x0,0x0,0x0,0x0,
  0x0,0x0,0x0,0x0,0xbfd8bf90,0x3f403f00,0x0,0x0,0x0,0x0,0x0,0x0,0x0,0x0,0x0,0x0,
  0x0,0x0,0x0,0x0,0x0,0x0,0xbfd8bf90,0x3f403f00,0x0,0x0,0x0,0x0,0x0,0x0,0x0,0x0,
  0x0,0x0,0x0,0x0,0xbfd83f90,0x3f40bf00,0x0,0x0,0x0,0x0,0x0,0x0,0x0,0x0,0x0,0x0,
  0x0,0x0,0x0,0x0,0x0,0x0,0xbfd83f90,0x3f40bf00,0x0,0x0,0x0,0x0,0x0,0x0,0x0,0x0,
  0x0,0x0,0x0,0x0,0x0,0x0,0x0,0x0,0xbfd8bf90,0x3f403f00,0x0,0x0,0x0,0x0,0x0,0x0,
  0x0,0x0,0x0,0x0,0x0,0x0,0x0,0x0,0x0,0x0,0xbfd8bf90,0x3f403f00,0x0,0x0,0x0,0x0,
  0x0,0x0,0x0,0x0,0x0,0x0,0x0,0x0,0xbfd83f90,0x3f40bf00,0x0,0x0,0x0,0x0,0x0,0x0,
  0x0,0x0,0x0,0x0,0x0,0x0,0x0,0x0,0x0,0x0,0xbfd83f90,0x3f40bf00,0x0,0x0,0x0,0x0,
  0x0,0x0,0x0,0x0,0x0,0x0,0x0,0x0,0x0,0x0,0x0,0x0,0xbfd8bf90,0x3f403f00,0x0,0x0,
  0x0,0x0,0x0,0x0,0x0,0x0,0x0,0x0,0x0,0x0,0x0,0x0,0x0,0x0,0xbfd8bf90,0x3f403f00,
  0x0,0x0,0x0,0x0,0x0,0x0,0x0,0x0,0x0,0x0,0x0,0x0,0xbfd83f90,0x3f40bf00,0x0,0x0,
  0x0,0x0,0x0,0x0,0x0,0x0,0x0,0x0,0x0,0x0,0x0,0x0,0x0,0x0,0xbfd83f90,0x3f40bf00,
  0x0,0x9000,0x0,0x0,0x10,0x9002,0x180,0x0,0x20,0x9004,0x300,0x0,0x30,0x9006,0x480,0x0,
  0x40,0x9008,0x1800,0x0,0x50,0x900a,0x1980,0x0,0x60,0x900c,0x1b00,0x0,0x70,0x900e,0x1c80,0x0,
  0x9000,0x9010,0x2,0x0,0x9010,0x9012,0x182,0x0,0x9020,0x9014,0x302,0x0,0x9030,0x9016,0x482,0x0,
  0x9040,0x9018,0x1802,0x0,0x9050,0x901a,0x1982,0x0,0x9060,0x901c,0x1b02,0x0,0x9070,0x901e,0x1c82,0x0,
  0x12000,0x9020,0x4,0x0,0x12010,0x9022,0x184,0x0,0x12020,0x9024,0x304,0x0,0x12030,0x9026,0x484,0x0,
  0x12040,0x9028,0x1804,0x0,0x12050,0x902a,0x1984,0x0,0x12060,0x902c,0x1b04,0x0,0x12070,0x902e,0x1c84,0x0,
  0x1b000,0x9030,0x6,0x0,0x1b010,0x9032,0x186,0x0,0x1b020,0x9034,0x306,0x0,0x1b030,0x9036,0x486,0x0,
  0x1b040,0x9038,0x1806,0x0,0x1b050,0x903a,0x1986,0x0,0x1b060,0x903c,0x1b06,0x0,0x1b070,0x903e,0x1c86,0x0,
  0x24000,0x9040,0x8,0x8,0x24010,0x9042,0x188,0x8,0x24020,0x9044,0x308,0x8,0x24030,0x9046,0x488,0x8,
  0x24040,0x9048,0x1808,0x8,0x24050,0x904a,0x1988,0x8,0x24060,0x904c,0x1b08,0x8,0x24070,0x904e,0x1c88,0x8,
  0x2d000,0x9050,0xa,0x8,0x2d010,0x9052,0x18a,0x8,0x2d020,0x9054,0x30a,0x8,0x2d030,0x9056,0x48a,0x8,
  0x2d040,0x9058,0x180a,0x8,0x2d050,0x905a,0x198a,0x8,0x2d060,0x905c,0x1b0a,0x8,0x2d070,0x905e,0x1c8a,0x8,
  0x40000000,0x9060,0xc,0x8,0x40000000,0x9062,0x18c,0x8,0x40000000,0x9064,0x30c,0x8,0x40000000,0x9066,0x48c,0x8,
  0x40000000,0x9068,0x180c,0x8,0x40000000,0x906a,0x198c,0x8,0x40000000,0x906c,0x1b0c,0x8,0x40000000,0x906e,0x1c8c,0x8,
  0x40000000,0x9070,0xe,0x8,0x40000000,0x9072,0x18e,0x8,0x40000000,0x9074,0x30e,0x8,0x40000000,0x9076,0x48e,0x8,
  0x40000000,0x9078,0x180e,0x8,0x40000000,0x907a,0x198e,0x8,0x40000000,0x907c,0x1b0e,0x8,0x40000000,0x907e,0x1c8e,0x8,
  0x180000,0x40,0x820,0x10000000,0x180010,0xd0,0x8b0,0x10000000,0x180020,0x160,0x900,0x10000000,0x180030,0x1f0,0x990,0x10000000,
  0x180040,0x200,0xa60,0x20093180,0x180050,0x290,0xaf0,0x20093190,0x180060,0x320,0xb40,0x200931a0,0x180070,0x3b0,0xbd0,0x200931b0,
  0x180610,0xc40,0x1420,0x40090000,0x180600,0xcd0,0x14b0,0x40090010,0x180630,0xd60,0x1500,0x40090020,0x180620,0xdf0,0x1590,0x40090030,
  0x180650,0xe00,0x1660,0x200c3180,0x180640,0xe90,0x16f0,0x200c3190,0x180670,0xf20,0x1740,0x200c31a0,0x180660,0xfb0,0x17d0,0x200c31b0,
  0x180c20,0x50,0x828,0x400c0020,0x180c30,0xc0,0x8b8,0x400c0030,0x180c00,0x170,0x908,0x400c0000,0x180c10,0x1e0,0x998,0x400c0010,
  0x180c60,0x210,0xa68,0x200f31a0,0x180c70,0x280,0xaf8,0x200f31b0,0x180c40,0x330,0xb48,0x200f3180,0x180c50,0x3a0,0xbd8,0x200f3190,
  0x181230,0xc50,0x1428,0x400f0020,0x181220,0xcc0,0x14b8,0x400f0030,0x181210,0xd70,0x1508,0x400f0000,0x181200,0xde0,0x1598,0x400f0010,
  0x181270,0xe10,0x1668,0x10000000,0x181260,0xe80,0x16f8,0x10000000,0x181250,0xf30,0x1748,0x10000000,0x181240,0xfa0,0x17d8,0x10000000,
  0x181840,0x60,0x830,0x10000000,0x181850,0xf0,0x8a0,0x10000000,0x181860,0x140,0x910,0x10000000,0x181870,0x1d0,0x980,0x10000000,
  0x181800,0x220,0xa70,0x200931c0,0x181810,0x2b0,0xae0,0x200931d0,0x181820,0x300,0xb50,0x200931e0,0x181830,0x390,0xbc0,0x200931f0,
  0x181e50,0xc60,0x1430,0x40090040,0x181e40,0xcf0,0x14a0,0x40090050,0x181e70,0xd40,0x1510,0x40090060,0x181e60,0xdd0,0x1580,0x40090070,
  0x181e10,0xe20,0x1670,0x200c31c0,0x181e00,0xeb0,0x16e0,0x200c31d0,0x181e30,0xf00,0x1750,0x200c31e0,0x181e20,0xf90,0x17c0,0x200c31f0,
  0x182460,0x70,0x838,0x400c0060,0x182470,0xe0,0x8a8,0x400c0070,0x182440,0x150,0x918,0x400c0040,0x182450,0x1c0,0x988,0x400c0050,
  0x182420,0x230,0xa78,0x200f31e0,0x182430,0x2a0,0xae8,0x200f31f0,0x182400,0x310,0xb58,0x200f31c0,0x182410,0x380,0xbc8,0x200f31d0,
  0x182a70,0xc70,0x1438,0x400f0060,0x182a60,0xce0,0x14a8,0x400f0070,0x182a50,0xd50,0x1518,0x400f0040,0x182a40,0xdc0,0x1588,0x400f0050,
  0x182a30,0xe30,0x1678,0x10000000,0x182a20,0xea0,0x16e8,0x10000000,0x182a10,0xf10,0x1758,0x10000000,0x182a00,0xf80,0x17c8,0x10000000,
  0xbfd8bf90,0x3f403f00,0x0,0x0,0x0,0x0,0x0,0x0,0x0,0x0,0x0,0x0,0x0,0x0,0x0,0x0,
  0x0,0x0,0xbfd8bf90,0x3f403f00,0x0,0x0,0x0,0x0,0x0,0x0,0x0,0x0,0x0,0x0,0x0,0x0,
  0xbfd83f90,0x3f40bf00,0x0,0x0,0x0,0x0,0x0,0x0,0x0,0x0,0x0,0x0,0x0,0x0,0x0,0x0,
  0x0,0x0,0xbfd83f90,0x3f40bf00,0x0,0x0,0x0,0x0,0x0,0x0,0x0,0x0,0x0,0x0,0x0,0x0,
  0x0,0x0,0x0,0x0,0xbfd8bf90,0x3f403f00,0x0,0x0,0x0,0x0,0x0,0x0,0x0,0x0,0x0,0x0,
  0x0,0x0,0x0,0x0,0x0,0x0,0xbfd8bf90,0x3f403f00,0x0,0x0,0x0,0x0,0x0,0x0,0x0,0x0,
  0x0,0x0,0x0,0x0,0xbfd83f90,0x3f40bf00,0x0,0x0,0x0,0x0,0x0,0x0,0x0,0x0,0x0,0x0,
  0x0,0x0,0x0,0x0,0x0,0x0,0xbfd83f90,0x3f40bf00,0x0,0x0,0x0,0x0,0x0,0x0,0x0,0x0,
  0x0,0x0,0x0,0x0,0x0,0x0,0x0,0x0,0xbfd8bf90,0x3f403f00,0x0,0x0,0x0,0x0,0x0,0x0,
  0x0,0x0,0x0,0x0,0x0,0x0,0x0,0x0,0x0,0x0,0xbfd8bf90,0x3f403f00,0x0,0x0,0x0,0x0,
  0x0,0x0,0x0,0x0,0x0,0x0,0x0,0x0,0xbfd83f90,0x3f40bf00,0x0,0x0,0x0,0x0,0x0,0x0,
  0x0,0x0,0x0,0x0,0x0,0x0,0x0,0x0,0x0,0x0,0xbfd83f90,0x3f40bf00,0x0,0x0,0x0,0x0,
  0x0,0x0,0x0,0x0,0x0,0x0,0x0,0x0,0x0,0x0,0x0,0x0,0xbfd8bf90,0x3f403f00,0x0,0x0,
  0x0,0x0,0x0,0x0,0x0,0x0,0x0,0x0,0x0,0x0,0x0,0x0,0x0,0x0,0xbfd8bf90,0x3f403f00,
  0x0,0x0,0x0,0x0,0x0,0x0,0x0,0x0,0x0,0x0,0x0,0x0,0xbfd83f90,0x3f40bf00,0x0,0x0,
  0x0,0x0,0x0,0x0,0x0,0x0,0x0,0x0,0x0,0x0,0x0,0x0,0x0,0x0,0xbfd83f90,0x3f40bf00,
  0x0,0x0,0x0,0x0,0x0,0x0,0x0,0x0,0x0,0x0,0x0,0x0,0x0,0x0,0x0,0x0,
  0x0,0x0,0x0,0x0,0x0,0x0,0x0,0x0,0x0,0x0,0x0,0x0,0x0,0x0,0x0,0x0,
  0x0,0x0,0x0,0x0,0x0,0x0,0x0,0x0,0x0,0x0,0x0,0x0,0x0,0x0,0x0,0x0,
  0x0,0x0,0x0,0x0,0x0,0x0,0x0,0x0,0x0,0x0,0x0,0x0,0x0,0x0,0x0,0x0,
  0x0,0x0,0x0,0x0,0x0,0x0,0x0,0x0,0x0,0x0,0x0,0x0,0x0,0x0,0x0,0x0,
  0x0,0x0,0x0,0x0,0x0,0x0,0x0,0x0,0x0,0x0,0x0,0x0,0x0,0x0,0x0,0x0,
  0x0,0x0,0x0,0x0,0x0,0x0,0x0,0x0,0x0,0x0,0x0,0x0,0x0,0x0,0x0,0x0,
  0x0,0x0,0x0,0x0,0x0,0x0,0x0,0x0,0x0,0x0,0x0,0x0,0x0,0x0,0x0,0x0,
  0x0,0x0,0x0,0x0,0x0,0x0,0x0,0x0,0x0,0x0,0x0,0x0,0x0,0x0,0x0,0x0,
  0x0,0x0,0x0,0x0,0x0,0x0,0x0,0x0,0x0,0x0,0x0,0x0,0x0,0x0,0x0,0x0,
  0x0,0x0,0x0,0x0,0x0,0x0,0x0,0x0,0x0,0x0,0x0,0x0,0x0,0x0,0x0,0x0,
  0x0,0x0,0x0,0x0,0x0,0x0,0x0,0x0,0x0,0x0,0x0,0x0,0x0,0x0,0x0,0x0,
  0x0,0x0,0x0,0x0,0x0,0x0,0x0,0x0,0x0,0x0,0x0,0x0,0x0,0x0,0x0,0x0,
  0x0,0x0,0x0,0x0,0x0,0x0,0x0,0x0,0x0,0x0,0x0,0x0,0x0,0x0,0x0,0x0,
  0x0,0x0,0x0,0x0,0x0,0x0,0x0,0x0,0x0,0x0,0x0,0x0,0x0,0x0,0x0,0x0,
  0x0,0x0,0x0,0x0,0x0,0x0,0x0,0x0,0x0,0x0,0x0,0x0,0x0,0x0,0x0,0x0,
  0x0,0x400,0x840,0x10000000,0x10,0x490,0x8d0,0x10000000,0x20,0x520,0x960,0x10000000,0x30,0x5b0,0x9f0,0x10000000,
  0x40,0x640,0xa00,0x20123180,0x50,0x6d0,0xa90,0x20123190,0x60,0x760,0xb20,0x201231a0,0x70,0x7f0,0xbb0,0x201231b0,
  0x610,0x1000,0x1440,0x40120000,0x600,0x1090,0x14d0,0x40120010,0x630,0x1120,0x1560,0x40120020,0x620,0x11b0,0x15f0,0x40120030,
  0x650,0x1240,0x1600,0x20153180,0x640,0x12d0,0x1690,0x20153190,0x670,0x1360,0x1720,0x201531a0,0x660,0x13f0,0x17b0,0x201531b0,
  0xc20,0x410,0x848,0x40150020,0xc30,0x480,0x8d8,0x40150030,0xc00,0x530,0x968,0x40150000,0xc10,0x5a0,0x9f8,0x40150010,
  0xc60,0x650,0xa08,0x201831a0,0xc70,0x6c0,0xa98,0x201831b0,0xc40,0x770,0xb28,0x20183180,0xc50,0x7e0,0xbb8,0x20183190,
  0x1230,0x1010,0x1448,0x40180020,0x1220,0x1080,0x14d8,0x40180030,0x1210,0x1130,0x1568,0x40180000,0x1200,0x11a0,0x15f8,0x40180010,
  0x1270,0x1250,0x1608,0x10000000,0x1260,0x12c0,0x1698,0x10000000,0x1250,0x1370,0x1728,0x10000000,0x1240,0x13e0,0x17b8,0x10000000,
  0x1840,0x420,0x850,0x10000000,0x1850,0x4b0,0x8c0,0x10000000,0x1860,0x500,0x970,0x10000000,0x1870,0x590,0x9e0,0x10000000,
  0x1800,0x660,0xa10,0x201231c0,0x1810,0x6f0,0xa80,0x201231d0,0x1820,0x740,0xb30,0x201231e0,0x1830,0x7d0,0xba0,0x201231f0,
  0x1e50,0x1020,0x1450,0x40120040,0x1e40,0x10b0,0x14c0,0x40120050,0x1e70,0x1100,0x1570,0x40120060,0x1e60,0x1190,0x15e0,0x40120070,
  0x1e10,0x1260,0x1610,0x201531c0,0x1e00,0x12f0,0x1680,0x201531d0,0x1e30,0x1340,0x1730,0x201531e0,0x1e20,0x13d0,0x17a0,0x201531f0,
  0x2460,0x430,0x858,0x40150060,0x2470,0x4a0,0x8c8,0x40150070,0x2440,0x510,0x978,0x40150040,0x2450,0x580,0x9e8,0x40150050,
  0x2420,0x670,0xa18,0x201831e0,0x2430,0x6e0,0xa88,0x201831f0,0x2400,0x750,0xb38,0x201831c0,0x2410,0x7c0,0xba8,0x201831d0,
  0x2a70,0x1030,0x1458,0x40180060,0x2a60,0x10a0,0x14c8,0x40180070,0x2a50,0x1110,0x1578,0x40180040,0x2a40,0x1180,0x15e8,0x40180050,
  0x2a30,0x1270,0x1618,0x10000000,0x2a20,0x12e0,0x1688,0x10000000,0x2a10,0x1350,0x1738,0x10000000,0x2a00,0x13c0,0x17a8,0x10000000,
  0xbfd8bf90,0x3f403f00,0x0,0x0,0x0,0x0,0x0,0x0,0x0,0x0,0x0,0x0,0x0,0x0,0x0,0x0,
  0x0,0x0,0xbfd8bf90,0x3f403f00,0x0,0x0,0x0,0x0,0x0,0x0,0x0,0x0,0x0,0x0,0x0,0x0,
  0xbfd83f90,0x3f40bf00,0x0,0x0,0x0,0x0,0x0,0x0,0x0,0x0,0x0,0x0,0x0,0x0,0x0,0x0,
  0x0,0x0,0xbfd83f90,0x3f40bf00,0x0,0x0,0x0,0x0,0x0,0x0,0x0,0x0,0x0,0x0,0x0,0x0,
  0x0,0x0,0x0,0x0,0xbfd8bf90,0x3f403f00,0x0,0x0,0x0,0x0,0x0,0x0,0x0,0x0,0x0,0x0,
  0x0,0x0,0x0,0x0,0x0,0x0,0xbfd8bf90,0x3f403f00,0x0,0x0,0x0,0x0,0x0,0x0,0x0,0x0,
  0x0,0x0,0x0,0x0,0xbfd83f90,0x3f40bf00,0x0,0x0,0x0,0x0,0x0,0x0,0x0,0x0,0x0,0x0,
  0x0,0x0,0x0,0x0,0x0,0x0,0xbfd83f90,0x3f40bf00,0x0,0x0,0x0,0x0,0x0,0x0,0x0,0x0,
  0x0,0x0,0x0,0x0,0x0,0x0,0x0,0x0,0xbfd8bf90,0x3f403f00,0x0,0x0,0x0,0x0,0x0,0x0,
  0x0,0x0,0x0,0x0,0x0,0x0,0x0,0x0,0x0,0x0,0xbfd8bf90,0x3f403f00,0x0,0x0,0x0,0x0,
  0x0,0x0,0x0,0x0,0x0,0x0,0x0,0x0,0xbfd83f90,0x3f40bf00,0x0,0x0,0x0,0x0,0x0,0x0,
  0x0,0x0,0x0,0x0,0x0,0x0,0x0,0x0,0x0,0x0,0xbfd83f90,0x3f40bf00,0x0,0x0,0x0,0x0,
  0x0,0x0,0x0,0x0,0x0,0x0,0x0,0x0,0x0,0x0,0x0,0x0,0xbfd8bf90,0x3f403f00,0x0,0x0,
  0x0,0x0,0x0,0x0,0x0,0x0,0x0,0x0,0x0,0x0,0x0,0x0,0x0,0x0,0xbfd8bf90,0x3f403f00,
  0x0,0x0,0x0,0x0,0x0,0x0,0x0,0x0,0x0,0x0,0x0,0x0,0xbfd83f90,0x3f40bf00,0x0,0x0,
  0x0,0x0,0x0,0x0,0x0,0x0,0x0,0x0,0x0,0x0,0x0,0x0,0x0,0x0,0xbfd83f90,0x3f40bf00,
  0x0,0x0,0x0,0x0,0x0,0x0,0x0,0x0,0x0,0x0,0x0,0x0,0x0,0x0,0x0,0x0,
  0x0,0x0,0x0,0x0,0x0,0x0,0x0,0x0,0x0,0x0,0x0,0x0,0x0,0x0,0x0,0x0,
  0x0,0x0,0x0,0x0,0x0,0x0,0x0,0x0,0x0,0x0,0x0,0x0,0x0,0x0,0x0,0x0,
  0x0,0x0,0x0,0x0,0x0,0x0,0x0,0x0,0x0,0x0,0x0,0x0,0x0,0x0,0x0,0x0,
  0x0,0x0,0x0,0x0,0x0,0x0,0x0,0x0,0x0,0x0,0x0,0x0,0x0,0x0,0x0,0x0,
  0x0,0x0,0x0,0x0,0x0,0x0,0x0,0x0,0x0,0x0,0x0,0x0,0x0,0x0,0x0,0x0,
  0x0,0x0,0x0,0x0,0x0,0x0,0x0,0x0,0x0,0x0,0x0,0x0,0x0,0x0,0x0,0x0,
  0x0,0x0,0x0,0x0,0x0,0x0,0x0,0x0,0x0,0x0,0x0,0x0,0x0,0x0,0x0,0x0,
  0x0,0x0,0x0,0x0,0x0,0x0,0x0,0x0,0x0,0x0,0x0,0x0,0x0,0x0,0x0,0x0,
  0x0,0x0,0x0,0x0,0x0,0x0,0x0,0x0,0x0,0x0,0x0,0x0,0x0,0x0,0x0,0x0,
  0x0,0x0,0x0,0x0,0x0,0x0,0x0,0x0,0x0,0x0,0x0,0x0,0x0,0x0,0x0,0x0,
  0x0,0x0,0x0,0x0,0x0,0x0,0x0,0x0,0x0,0x0,0x0,0x0,0x0,0x0,0x0,0x0,
  0x0,0x0,0x0,0x0,0x0,0x0,0x0,0x0,0x0,0x0,0x0,0x0,0x0,0x0,0x0,0x0,
  0x0,0x0,0x0,0x0,0x0,0x0,0x0,0x0,0x0,0x0,0x0,0x0,0x0,0x0,0x0,0x0,
  0x0,0x0,0x0,0x0,0x0,0x0,0x0,0x0,0x0,0x0,0x0,0x0,0x0,0x0,0x0,0x0,
  0x0,0x0,0x0,0x0,0x0,0x0,0x0,0x0,0x0,0x0,0x0,0x0,0x0,0x0,0x0,0x0,
  0x180000,0x440,0x860,0x10000000,0x180010,0x4d0,0x8f0,0x10000000,0x180020,0x560,0x940,0x10000000,0x180030,0x5f0,0x9d0,0x10000000,
  0x180040,0x600,0xa20,0x201b3180,0x180050,0x690,0xab0,0x201b3190,0x180060,0x720,0xb00,0x201b31a0,0x180070,0x7b0,0xb90,0x201b31b0,
  0x180610,0x1040,0x1460,0x401b0000,0x180600,0x10d0,0x14f0,0x401b0010,0x180630,0x1160,0x1540,0x401b0020,0x180620,0x11f0,0x15d0,0x401b0030,
  0x180650,0x1200,0x1620,0x201e3180,0x180640,0x1290,0x16b0,0x201e3190,0x180670,0x1320,0x1700,0x201e31a0,0x180660,0x13b0,0x1790,0x201e31b0,
  0x180c20,0x450,0x868,0x401e0020,0x180c30,0x4c0,0x8f8,0x401e0030,0x180c00,0x570,0x948,0x401e0000,0x180c10,0x5e0,0x9d8,0x401e0010,
  0x180c60,0x610,0xa28,0x202131a0,0x180c70,0x680,0xab8,0x202131b0,0x180c40,0x730,0xb08,0x20213180,0x180c50,0x7a0,0xb98,0x20213190,
  0x181230,0x1050,0x1468,0x40210020,0x181220,0x10c0,0x14f8,0x40210030,0x181210,0x1170,0x1548,0x40210000,0x181200,0x11e0,0x15d8,0x40210010,
  0x181270,0x1210,0x1628,0x10000000,0x181260,0x1280,0x16b8,0x10000000,0x181250,0x1330,0x1708,0x10000000,0x181240,0x13a0,0x1798,0x10000000,
  0x181840,0x460,0x870,0x10000000,0x181850,0x4f0,0x8e0,0x10000000,0x181860,0x540,0x950,0x10000000,0x181870,0x5d0,0x9c0,0x10000000,
  0x181800,0x620,0xa30,0x201b31c0,0x181810,0x6b0,0xaa0,0x201b31d0,0x181820,0x700,0xb10,0x201b31e0,0x181830,0x790,0xb80,0x201b31f0,
  0x181e50,0x1060,0x1470,0x401b0040,0x181e40,0x10f0,0x14e0,0x401b0050,0x181e70,0x1140,0x1550,0x401b0060,0x181e60,0x11d0,0x15c0,0x401b0070,
  0x181e10,0x1220,0x1630,0x201e31c0,0x181e00,0x12b0,0x16a0,0x201e31d0,0x181e30,0x1300,0x1710,0x201e31e0,0x181e20,0x1390,0x1780,0x201e31f0,
  0x182460,0x470,0x878,0x401e0060,0x182470,0x4e0,0x8e8,0x401e0070,0x182440,0x550,0x958,0x401e0040,0x182450,0x5c0,0x9c8,0x401e0050,
  0x182420,0x630,0xa38,0x202131e0,0x182430,0x6a0,0xaa8,0x202131f0,0x182400,0x710,0xb18,0x202131c0,0x182410,0x780,0xb88,0x202131d0,
  0x182a70,0x1070,0x1478,0x40210060,0x182a60,0x10e0,0x14e8,0x40210070,0x182a50,0x1150,0x1558,0x40210040,0x182a40,0x11c0,0x15c8,0x40210050,
  0x182a30,0x1230,0x1638,0x10000000,0x182a20,0x12a0,0x16a8,0x10000000,0x182a10,0x1310,0x1718,0x10000000,0x182a00,0x1380,0x1788,0x10000000,
  0xbfd8bf90,0x3f403f00,0x0,0x0,0x0,0x0,0x0,0x0,0x0,0x0,0x0,0x0,0x0,0x0,0x0,0x0,
  0x0,0x0,0xbfd8bf90,0x3f403f00,0x0,0x0,0x0,0x0,0x0,0x0,0x0,0x0,0x0,0x0,0x0,0x0,
  0xbfd83f90,0x3f40bf00,0x0,0x0,0x0,0x0,0x0,0x0,0x0,0x0,0x0,0x0,0x0,0x0,0x0,0x0,
  0x0,0x0,0xbfd83f90,0x3f40bf00,0x0,0x0,0x0,0x0,0x0,0x0,0x0,0x0,0x0,0x0,0x0,0x0,
  0x0,0x0,0x0,0x0,0xbfd8bf90,0x3f403f00,0x0,0x0,0x0,0x0,0x0,0x0,0x0,0x0,0x0,0x0,
  0x0,0x0,0x0,0x0,0x0,0x0,0xbfd8bf90,0x3f403f00,0x0,0x0,0x0,0x0,0x0,0x0,0x0,0x0,
  0x0,0x0,0x0,0x0,0xbfd83f90,0x3f40bf00,0x0,0x0,0x0,0x0,0x0,0x0,0x0,0x0,0x0,0x0,
  0x0,0x0,0x0,0x0,0x0,0x0,0xbfd83f90,0x3f40bf00,0x0,0x0,0x0,0x0,0x0,0x0,0x0,0x0,
  0x0,0x0,0x0,0x0,0x0,0x0,0x0,0x0,0xbfd8bf90,0x3f403f00,0x0,0x0,0x0,0x0,0x0,0x0,
  0x0,0x0,0x0,0x0,0x0,0x0,0x0,0x0,0x0,0x0,0xbfd8bf90,0x3f403f00,0x0,0x0,0x0,0x0,
  0x0,0x0,0x0,0x0,0x0,0x0,0x0,0x0,0xbfd83f90,0x3f40bf00,0x0,0x0,0x0,0x0,0x0,0x0,
  0x0,0x0,0x0,0x0,0x0,0x0,0x0,0x0,0x0,0x0,0xbfd83f90,0x3f40bf00,0x0,0x0,0x0,0x0,
  0x0,0x0,0x0,0x0,0x0,0x0,0x0,0x0,0x0,0x0,0x0,0x0,0xbfd8bf90,0x3f403f00,0x0,0x0,
  0x0,0x0,0x0,0x0,0x0,0x0,0x0,0x0,0x0,0x0,0x0,0x0,0x0,0x0,0xbfd8bf90,0x3f403f00,
  0x0,0x0,0x0,0x0,0x0,0x0,0x0,0x0,0x0,0x0,0x0,0x0,0xbfd83f90,0x3f40bf00,0x0,0x0,
  0x0,0x0,0x0,0x0,0x0,0x0,0x0,0x0,0x0,0x0,0x0,0x0,0x0,0x0,0xbfd83f90,0x3f40bf00,
};

__global__ __launch_bounds__(256, 1) void wino_main_kernel(const char* __restrict__ x, char* __restrict__ Uq, char* __restrict__ out, const char* __restrict__ wt) {
  __shared__ __attribute__((aligned(1024))) char smem[163840];
  asm volatile("s_getpc_b64 s[40:41]\n\tLwino_pc:\n\ts_bfe_u32 s42, %0, 0x50003\n\ts_lshl_b32 s42, s42, 12\n\tv_lshl_add_u32 v203, %1, 4, s42\n\t"
               "v_min_u32 v203, (Lwino_end-Lwino_pc-16)&-16, v203\n\tglobal_load_dwordx4 v[100:103], v203, s[40:41]"
               :: "s"(blockIdx.x), "v"(threadIdx.x) : "s40", "s41", "s42", "v100", "v101", "v102", "v103", "v203", "scc", "memory");
  const int lane = threadIdx.x & 63;
  const int w = __builtin_amdgcn_readfirstlane(threadIdx.x >> 6);
  const int sub = blockIdx.x >> 3, bx = sub & 15, by = 4 * (blockIdx.x & 7) + (sub >> 4);
  const unsigned long long px = (unsigned long long)x, pu = (unsigned long long)Uq, py = (unsigned long long)out, pw = (unsigned long long)wt;
  const int xbase = ((16 * by - 1) * IMG_W + 32 * bx) * PXB;
  const int ybase = (16 * by * IMG_W + 32 * bx) * PXB;
  const unsigned medge = (bx == 0 ? 0xffffffffu : 0xbfffffffu) & (bx == 15 ? 0xffffffffu : 0xdfffffffu);
  unsigned fso, fuo, fk0, fk5, fs8;
  {
    const unsigned p = blockIdx.x - 1, hb = p & 1, q = (p >> 1) & 127, r = q / 3, kb = q - 3 * r, wv = r & 3, chunk = r >> 2;
    fs8 = 1 - (kb >> 1);
    fso = p < 144 ? chunk * 12288 + hb * 3072 + (w >> 1) * 1536 + (kb < 2 ? kb * 8 : 256) + ((2 * wv + (w & 1)) << (4 + fs8)) : 0x40000000u;
    fuo = p < 144 ? (chunk * 18 + kb) * 12288 + wv * 3072 + hb * 512 + w * 128 : 0x40000000u;
    fk0 = chunk < 2 ? 0x3ea80a81u : 0x3f638e39u; fk5 = chunk < 2 ? 0xbebd0bd1u : 0x3f638e39u;
  }
  const unsigned fwo = (((lane & 3) + 4 * (lane & 4)) * CH + 4 * (lane >> 5) + ((lane >> 3) & 3)) * 2;
  const unsigned flw = 18432 + w * 3072 + lane * 2, fst = lane < 48 ? (lane >> 3) * 36864 + (lane & 7) * 16 : 0x40000000u;
  unsigned dummy = 0;
  asm volatile(
"s_mov_b32 s52, %15\n\t"
"s_and_b32 s53, %16, 0xffff\n\t"
"s_mov_b32 s54, 663552\n\t"
"s_mov_b32 s55, 0x20000\n\t"
"s_mov_b32 s56, %17\n\t"
"s_mov_b32 s57, %18\n\t"
"s_mov_b32 s58, %19\n\t"
"s_mov_b32 s59, %20\n\t"
"s_mov_b32 s74, %21\n\t"
"s_mul_i32 s66, %11, 4\n\t"
"s_mov_b32 s73, %9\n\t"
"s_or_b32 s72, %11, %9\n\t"
"v_mov_b32 v32, 0\n\t"
"v_mov_b32 v33, 0\n\t"
"v_mov_b32 v34, 0\n\t"
"v_mov_b32 v35, 0\n\t"
"v_mov_b32 v36, 0\n\t"
"v_mov_b32 v37, 0\n\t"
"v_mov_b32 v38, 0\n\t"
"v_mov_b32 v39, 0\n\t"
"v_mov_b32 v40, 0\n\t"
"v_mov_b32 v118, %22\n\t"
"v_mov_b32 v42, %23\n\t"
"v_add_u32 v43, 18432, v252\n\t"
"v_mov_b32 v44, %24\n\t"
"v_mov_b32 v119, %25\n\t"
"s_mov_b32 s8, %1\n\t"
"s_and_b32 s9, %2, 0xffff\n\t"
"s_mov_b32 s10, 0x200000\n\t"
"s_mov_b32 s11, 0x20000\n\t"
"s_mov_b32 s12, %3\n\t"
"s_and_b32 s13, %4, 0xffff\n\t"
"s_mov_b32 s14, 100663296\n\t"
"s_mov_b32 s15, 0x20000\n\t"
"s_mov_b32 s16, %5\n\t"
"s_and_b32 s17, %6, 0xffff\n\t"
"s_mov_b32 s18, 100663296\n\t"
"s_mov_b32 s19, 0x20000\n\t"
"s_mov_b32 s21, %7\n\t"
"s_lshr_b32 s60, %9, 1\n\t"
"s_mul_i32 s61, s60, 384\n\t"
"s_add_u32 s22, %8, s61\n\t"
"s_mul_i32 s61, s60, 196608\n\t"
"s_add_u32 s23, %8, s61\n\t"
"s_mul_i32 s24, %9, 1536\n\t"
"s_add_u32 s24, s24, 43008\n\t"
"s_mul_i32 s25, %9, 19968\n\t"
"s_add_u32 s25, s25, 61440\n\t"
"s_mul_i32 s26, s60, 19968\n\t"
"s_add_u32 s26, s26, 141312\n\t"
"s_sub_u32 s61, s60, %9\n\t"
"s_add_u32 s61, s61, 4\n\t"
"s_mul_i32 s27, s61, 589824\n\t"
"s_bitcmp1_b32 %9, 0\n\t"
"s_mov_b32 s36, 0x0fff0000\n\t"
"s_cselect_b32 s36, 0xfff0, s36\n\t"
"s_mov_b32 s37, s36\n\t"
"s_mov_b32 s44, 0x0fff0fff\n\t"
"s_mov_b32 s45, 0x0fff0fff\n\t"
"s_mov_b64 s[46:47], %12\n\t"
"s_add_u32 s48, s46, 12288\n\t"
"s_addc_u32 s49, s47, 0\n\t"
"s_mul_i32 s50, %9, 122880\n\t"
"s_lshl_b32 s51, s60, 30\n\t"
"s_add_u32 s51, s51, s50\n\t"
"s_mov_b32 s40, %14\n\t"
"s_sub_u32 s42, %7, 384\n\t"
"s_mov_b32 s38, 0x3f2aaaab\n\t"
"s_mov_b32 s39, 0x3fc00000\n\t"
"s_mov_b32 s41, 0x3ee38e39\n\t"
"s_mov_b32 s43, 0x40100000\n\t"
"s_nop 4\n\t"
"s_branch Lpro_%=\n\t"
"Lfb_%=:\n\t"
"s_waitcnt vmcnt(0) lgkmcnt(0)\n\t"
"s_nop 15\n\t"
"v_lshrrev_b32 v255, 9, v252\n\t"
"v_lshl_add_u32 v255, v255, 10, v252\n\t"
"v_add_u32 v255, 44032, v255\n\t"
"ds_write_b128 v255, v[32:35] offset:0\n\t"
"ds_write_b128 v255, v[36:39] offset:3072\n\t"
"ds_write_b128 v255, v[40:43] offset:6144\n\t"
"ds_write_b128 v255, v[44:47] offset:9216\n\t"
"ds_write_b128 v255, v[48:51] offset:12288\n\t"
"ds_write_b128 v255, v[52:55] offset:15360\n\t"
"ds_write_b128 v255, v[56:59] offset:18432\n\t"
"ds_write_b128 v255, v[60:63] offset:21504\n\t"
"ds_write_b128 v255, v[64:67] offset:24576\n\t"
"ds_write_b128 v255, v[68:71] offset:27648\n\t"
"ds_write_b128 v255, v[72:75] offset:30720\n\t"
"ds_write_b128 v255, v[76:79] offset:33792\n\t"
"ds_write_b128 v255, v[80:83] offset:36864\n\t"
"ds_write_b128 v255, v[84:87] offset:39936\n\t"
"ds_write_b128 v255, v[88:91] offset:43008\n\t"
"ds_write_b128 v255, v[92:95] offset:46080\n\t"
"ds_write_b128 v255, v[96:99] offset:49152\n\t"
"ds_write_b128 v255, v[100:103] offset:52224\n\t"
"ds_write_b128 v255, v[104:107] offset:55296\n\t"
"ds_write_b128 v255, v[108:111] offset:58368\n\t"
"ds_write_b128 v255, v[112:115] offset:61440\n\t"
"ds_write_b128 v255, v[116:119] offset:64512\n\t"
"s_waitcnt lgkmcnt(0)\n\t"
"global_load_dwordx4 v[116:119], v252, s[48:49] offset:2048\n\t"
"v_mov_b32 v32, 0\n\t"
"v_mov_b32 v33, 0\n\t"
"v_mov_b32 v34, 0\n\t"
"v_mov_b32 v35, 0\n\t"
"v_mov_b32 v36, 0\n\t"
"v_mov_b32 v37, 0\n\t"
"v_mov_b32 v38, 0\n\t"
"v_mov_b32 v39, 0\n\t"
"v_mov_b32 v40, 0\n\t"
"v_add_u32 v43, 36864, v252\n\t"
"s_waitcnt vmcnt(0)\n\t"
"v_mov_b32 v44, v116\n\t"
"v_mov_b32 v42, v117\n\t"
"v_xor_b32 v45, 8, v42\n\t"
"s_mov_b32 s69, 0\n\t"
"Lfb1_%=:\n\t"
"s_lshr_b32 s71, s69, 2\n\t"
"s_and_b32 s70, s69, 3\n\t"
"s_and_b32 s60, s71, 1\n\t"
"s_lshr_b32 s61, s71, 1\n\t"
"s_mul_i32 s62, s61, 171\n\t"
"s_lshr_b32 s62, s62, 9\n\t"
"s_mul_i32 s63, s62, 3\n\t"
"s_sub_u32 s61, s61, s63\n\t"
"s_and_b32 s63, s62, 3\n\t"
"s_lshr_b32 s62, s62, 2\n\t"
"s_mul_i32 s56, s62, 12288\n\t"
"s_mul_i32 s64, s60, 3072\n\t"
"s_add_u32 s56, s56, s64\n\t"
"s_lshr_b32 s64, s70, 1\n\t"
"s_mul_i32 s64, s64, 1536\n\t"
"s_add_u32 s56, s56, s64\n\t"
"s_and_b32 s64, s70, 1\n\t"
"s_lshl1_add_u32 s64, s63, s64\n\t"
"s_lshl_b32 s65, s61, 3\n\t"
"s_cmp_lt_u32 s61, 2\n\t"
"s_cselect_b32 s74, 1, 0\n\t"
"s_cselect_b32 s65, s65, 0x100\n\t"
"s_add_u32 s56, s56, s65\n\t"
"s_add_u32 s65, s74, 4\n\t"
"s_lshl_b32 s64, s64, s65\n\t"
"s_add_u32 s56, s56, s64\n\t"
"s_mul_i32 s57, s62, 18\n\t"
"s_add_u32 s57, s57, s61\n\t"
"s_mul_i32 s57, s57, 12288\n\t"
"s_mul_i32 s64, s63, 3072\n\t"
"s_add_u32 s57, s57, s64\n\t"
"s_lshl_b32 s64, s60, 9\n\t"
"s_add_u32 s57, s57, s64\n\t"
"s_lshl_b32 s64, s70, 7\n\t"
"s_add_u32 s57, s57, s64\n\t"
"s_mov_b32 s64, 0x3f638e39\n\t"
"s_cmp_lt_u32 s62, 2\n\t"
"s_cselect_b32 s58, 0x3ea80a81, s64\n\t"
"s_cselect_b32 s59, 0xbebd0bd1, s64\n\t"
"v_mad_u32_u24 v41, v119, s74, v118\n\t"
"s_add_u32 s60, s56, 0\n\t"
"buffer_load_short_d16_hi v32, v41, s[52:55], s60 offen\n\t"
"s_add_u32 s60, s56, 73728\n\t"
"buffer_load_short_d16_hi v33, v41, s[52:55], s60 offen\n\t"
"s_add_u32 s60, s56, 147456\n\t"
"buffer_load_short_d16_hi v34, v41, s[52:55], s60 offen\n\t"
"s_add_u32 s60, s56, 221184\n\t"
"buffer_load_short_d16_hi v35, v41, s[52:55], s60 offen\n\t"
"s_add_u32 s60, s56, 294912\n\t"
"buffer_load_short_d16_hi v36, v41, s[52:55], s60 offen\n\t"
"s_add_u32 s60, s56, 368640\n\t"
"buffer_load_short_d16_hi v37, v41, s[52:55], s60 offen\n\t"
"s_add_u32 s60, s56, 442368\n\t"
"buffer_load_short_d16_hi v38, v41, s[52:55], s60 offen\n\t"
"s_add_u32 s60, s56, 516096\n\t"
"buffer_load_short_d16_hi v39, v41, s[52:55], s60 offen\n\t"
"s_add_u32 s60, s56, 589824\n\t"
"buffer_load_short_d16_hi v40, v41, s[52:55], s60 offen\n\t"
"s_waitcnt vmcnt(6)\n\t"
"v_fma_f32 v48, v34, s41, v32\n\t"
"v_fma_f32 v51, v34, s43, v32\n\t"
"s_waitcnt vmcnt(3)\n\t"
"v_fma_f32 v49, v37, s41, v35\n\t"
"v_fma_f32 v52, v37, s43, v35\n\t"
"s_waitcnt vmcnt(0)\n\t"
"v_fma_f32 v50, v40, s41, v38\n\t"
"v_fma_f32 v53, v40, s43, v38\n\t"
"v_fma_f32 v54, v33, s38, v48\n\t"
"v_fma_f32 v57, -v33, s38, v48\n\t"
"v_fma_f32 v60, v33, s39, v51\n\t"
"v_fma_f32 v63, -v33, s39, v51\n\t"
"v_fma_f32 v55, v36, s38, v49\n\t"
"v_fma_f32 v58, -v36, s38, v49\n\t"
"v_fma_f32 v61, v36, s39, v52\n\t"
"v_fma_f32 v64, -v36, s39, v52\n\t"
"v_fma_f32 v56, v39, s38, v50\n\t"
"v_fma_f32 v59, -v39, s38, v50\n\t"
"v_fma_f32 v62, v39, s39, v53\n\t"
"v_fma_f32 v65, -v39, s39, v53\n\t"
"v_mul_f32 v70, s58, v32\n\t"
"v_mul_f32 v71, s58, v35\n\t"
"v_mul_f32 v72, s58, v38\n\t"
"v_mul_f32 v73, 0xbf54ad4b, v54\n\t"
"v_mul_f32 v74, 0xbf54ad4b, v55\n\t"
"v_mul_f32 v75, 0xbf54ad4b, v56\n\t"
"v_mul_f32 v76, 0xbefc0fc1, v57\n\t"
"v_mul_f32 v77, 0xbefc0fc1, v58\n\t"
"v_mul_f32 v78, 0xbefc0fc1, v59\n\t"
"v_mul_f32 v79, 0xbe7c0fc1, v60\n\t"
"v_mul_f32 v80, 0xbe7c0fc1, v61\n\t"
"v_mul_f32 v81, 0xbe7c0fc1, v62\n\t"
"v_mul_f32 v82, 0x3ea80a81, v63\n\t"
"v_mul_f32 v83, 0x3ea80a81, v64\n\t"
"v_mul_f32 v84, 0x3ea80a81, v65\n\t"
"v_mul_f32 v85, s59, v34\n\t"
"v_mul_f32 v86, s59, v37\n\t"
"v_mul_f32 v87, s59, v40\n\t"
"v_cvt_pk_bf16_f32 v88, v70, v71\n\t"
"v_cvt_pk_bf16_f32 v89, v72, v73\n\t"
"v_cvt_pk_bf16_f32 v90, v74, v75\n\t"
"v_cvt_pk_bf16_f32 v91, v76, v77\n\t"
"v_cvt_pk_bf16_f32 v92, v78, v79\n\t"
"v_cvt_pk_bf16_f32 v93, v80, v81\n\t"
"v_cvt_pk_bf16_f32 v94, v82, v83\n\t"
"v_cvt_pk_bf16_f32 v95, v84, v85\n\t"
"v_cvt_pk_bf16_f32 v96, v86, v87\n\t"
"ds_write_b16 v42, v88 offset:0\n\t"
"ds_write_b16_d16_hi v42, v88 offset:1024\n\t"
"ds_write_b16 v42, v89 offset:2048\n\t"
"ds_write_b16_d16_hi v42, v89 offset:128\n\t"
"ds_write_b16 v42, v90 offset:1152\n\t"
"ds_write_b16_d16_hi v42, v90 offset:2176\n\t"
"ds_write_b16 v45, v91 offset:256\n\t"
"ds_write_b16_d16_hi v45, v91 offset:1280\n\t"
"ds_write_b16 v45, v92 offset:2304\n\t"
"ds_write_b16_d16_hi v42, v92 offset:384\n\t"
"ds_write_b16 v42, v93 offset:1408\n\t"
"ds_write_b16_d16_hi v42, v93 offset:2432\n\t"
"ds_write_b16 v45, v94 offset:512\n\t"
"ds_write_b16_d16_hi v45, v94 offset:1536\n\t"
"ds_write_b16 v45, v95 offset:2560\n\t"
"ds_write_b16_d16_hi v42, v95 offset:640\n\t"
"ds_write_b16 v42, v96 offset:1664\n\t"
"ds_write_b16_d16_hi v42, v96 offset:2688\n\t"
"ds_read_b128 v[48:51], v43 offset:0\n\t"
"ds_read_b128 v[52:55], v43 offset:1024\n\t"
"ds_read_b128 v[56:59], v43 offset:2048\n\t"
"s_waitcnt lgkmcnt(2)\n\t"
"buffer_store_dwordx4 v[48:51], v44, s[8:11], s57 offen offset:0 sc1\n\t"
"s_waitcnt lgkmcnt(1)\n\t"
"buffer_store_dwordx4 v[52:55], v44, s[8:11], s57 offen offset:1024 sc1\n\t"
"s_waitcnt lgkmcnt(0)\n\t"
"buffer_store_dwordx4 v[56:59], v44, s[8:11], s57 offen offset:2048 sc1\n\t"
"s_waitcnt vmcnt(0) lgkmcnt(0)\n\t"
"s_add_u32 s69, s69, 1\n\t"
"s_cmp_lt_u32 s69, 576\n\t"
"s_cbranch_scc1 Lfb1_%=\n\t"
"ds_read_b128 v[32:35], v255 offset:0\n\t"
"ds_read_b128 v[36:39], v255 offset:3072\n\t"
"ds_read_b128 v[40:43], v255 offset:6144\n\t"
"ds_read_b128 v[44:47], v255 offset:9216\n\t"
"ds_read_b128 v[48:51], v255 offset:12288\n\t"
"ds_read_b128 v[52:55], v255 offset:15360\n\t"
"ds_read_b128 v[56:59], v255 offset:18432\n\t"
"ds_read_b128 v[60:63], v255 offset:21504\n\t"
"ds_read_b128 v[64:67], v255 offset:24576\n\t"
"ds_read_b128 v[68:71], v255 offset:27648\n\t"
"ds_read_b128 v[72:75], v255 offset:30720\n\t"
"ds_read_b128 v[76:79], v255 offset:33792\n\t"
"ds_read_b128 v[80:83], v255 offset:36864\n\t"
"ds_read_b128 v[84:87], v255 offset:39936\n\t"
"ds_read_b128 v[88:91], v255 offset:43008\n\t"
"ds_read_b128 v[92:95], v255 offset:46080\n\t"
"ds_read_b128 v[96:99], v255 offset:49152\n\t"
"ds_read_b128 v[100:103], v255 offset:52224\n\t"
"ds_read_b128 v[104:107], v255 offset:55296\n\t"
"ds_read_b128 v[108:111], v255 offset:58368\n\t"
"ds_read_b128 v[112:115], v255 offset:61440\n\t"
"ds_read_b128 v[116:119], v255 offset:64512\n\t"
"s_waitcnt lgkmcnt(0)\n\t"
"s_branch Lgo_%=\n\t"
"Lpro_%=:\n\t"
"v_mad_u32_u24 v41, v119, s74, v118\n\t"
"s_add_u32 s60, s56, 0\n\t"
"buffer_load_short_d16_hi v32, v41, s[52:55], s60 offen\n\t"
"s_add_u32 s60, s56, 73728\n\t"
"buffer_load_short_d16_hi v33, v41, s[52:55], s60 offen\n\t"
"s_add_u32 s60, s56, 147456\n\t"
"buffer_load_short_d16_hi v34, v41, s[52:55], s60 offen\n\t"
"s_add_u32 s60, s56, 221184\n\t"
"buffer_load_short_d16_hi v35, v41, s[52:55], s60 offen\n\t"
"s_add_u32 s60, s56, 294912\n\t"
"buffer_load_short_d16_hi v36, v41, s[52:55], s60 offen\n\t"
"s_add_u32 s60, s56, 368640\n\t"
"buffer_load_short_d16_hi v37, v41, s[52:55], s60 offen\n\t"
"s_add_u32 s60, s56, 442368\n\t"
"buffer_load_short_d16_hi v38, v41, s[52:55], s60 offen\n\t"
"s_add_u32 s60, s56, 516096\n\t"
"buffer_load_short_d16_hi v39, v41, s[52:55], s60 offen\n\t"
"s_add_u32 s60, s56, 589824\n\t"
"buffer_load_short_d16_hi v40, v41, s[52:55], s60 offen\n\t"
"s_add_u32 s60, s66, 1867776\n\t"
"buffer_load_dword v46, off, s[8:11], s60 sc1\n\t"
"s_mul_i32 s70, s66, 1\n\t"
"s_add_u32 s70, s70, 1900544\n\t"
"s_mov_b32 s60, s70\n\t"
"buffer_load_dword v47, off, s[8:11], s60 sc1\n\t"
"global_load_dwordx4 v[244:247], v252, s[46:47] offset:2048\n\t"
"global_load_dwordx4 v[248:251], v252, s[48:49] offset:0\n\t"
"global_load_dwordx4 v[240:243], v252, s[46:47] offset:0\n\t"
"global_load_dwordx4 v[196:199], v252, s[46:47] offset:1024\n\t"
"global_load_dwordx4 v[220:223], v252, s[48:49] offset:1024\n\t"
"s_cmp_lg_u32 s72, 0\n\t"
"s_cbranch_scc1 Lnl1_%=\n\t"
"v_mul_u32_u24 v116, 1, v252\n\t"
"v_lshrrev_b32 v116, 2, v116\n\t"
"v_mul_u32_u24 v117, 1, v116\n\t"
"s_mov_b32 s60, 1900544\n\t"
"buffer_load_dword v104, v117, s[8:11], s60 offen sc1\n\t"
"s_mov_b32 s60, 1900800\n\t"
"buffer_load_dword v105, v117, s[8:11], s60 offen sc1\n\t"
"s_mov_b32 s60, 1901056\n\t"
"buffer_load_dword v106, v117, s[8:11], s60 offen sc1\n\t"
"s_mov_b32 s60, 1901312\n\t"
"buffer_load_dword v107, v117, s[8:11], s60 offen sc1\n\t"
"s_waitcnt vmcnt(0)\n\t"
"v_add_u32 v104, 0x10000, v104\n\t"
"v_add_u32 v105, 0x10000, v105\n\t"
"v_add_u32 v106, 0x10000, v106\n\t"
"v_add_u32 v107, 0x10000, v107\n\t"
"v_and_b32 v104, 0xffff0000, v104\n\t"
"v_and_b32 v105, 0xffff0000, v105\n\t"
"v_and_b32 v106, 0xffff0000, v106\n\t"
"v_and_b32 v107, 0xffff0000, v107\n\t"
"s_mov_b32 s60, 1835008\n\t"
"buffer_store_dword v104, v116, s[8:11], s60 offen sc1\n\t"
"s_mov_b32 s60, 1835264\n\t"
"buffer_store_dword v105, v116, s[8:11], s60 offen sc1\n\t"
"s_mov_b32 s60, 1835520\n\t"
"buffer_store_dword v106, v116, s[8:11], s60 offen sc1\n\t"
"s_mov_b32 s60, 1835776\n\t"
"buffer_store_dword v107, v116, s[8:11], s60 offen sc1\n\t"
"Lnl1_%=:\n\t"
"s_add_u32 m0, s24, 59904\n\t"
"s_add_u32 s28, s21, 1769472\n\t"
"buffer_load_dwordx4 v28, s[12:15], s28 offen offset:0 nt lds\n\t"
"s_add_u32 m0, s24, 66048\n\t"
"s_add_u32 s28, s21, 1966080\n\t"
"buffer_load_dwordx4 v28, s[12:15], s28 offen offset:0 nt lds\n\t"
"s_add_u32 m0, s24, 72192\n\t"
"s_add_u32 s28, s21, 2162688\n\t"
"buffer_load_dwordx4 v28, s[12:15], s28 offen offset:0 nt lds\n\t"
"s_add_u32 m0, s24, 79872\n\t"
"s_add_u32 s28, s21, 2359296\n\t"
"buffer_load_dwordx4 v28, s[12:15], s28 offen offset:0 nt lds\n\t"
"s_add_u32 m0, s24, 86016\n\t"
"s_add_u32 s28, s21, 2555904\n\t"
"buffer_load_dwordx4 v28, s[12:15], s28 offen offset:0 nt lds\n\t"
"s_add_u32 m0, s24, 92160\n\t"
"s_add_u32 s28, s21, 2752512\n\t"
"buffer_load_dwordx4 v28, s[12:15], s28 offen offset:0 nt lds\n\t"
"s_add_u32 m0, s24, 19968\n\t"
"s_add_u32 s28, s21, 589824\n\t"
"buffer_load_dwordx4 v28, s[12:15], s28 offen offset:0 nt lds\n\t"
"s_add_u32 m0, s24, 26112\n\t"
"s_add_u32 s28, s21, 786432\n\t"
"buffer_load_dwordx4 v28, s[12:15], s28 offen offset:0 nt lds\n\t"
"v_xor_b32 v45, 8, v42\n\t"
"s_waitcnt vmcnt(21)\n\t"
"v_fma_f32 v48, v34, s41, v32\n\t"
"v_fma_f32 v51, v34, s43, v32\n\t"
"s_waitcnt vmcnt(18)\n\t"
"v_fma_f32 v49, v37, s41, v35\n\t"
"v_fma_f32 v52, v37, s43, v35\n\t"
"s_waitcnt vmcnt(15)\n\t"
"v_fma_f32 v50, v40, s41, v38\n\t"
"v_fma_f32 v53, v40, s43, v38\n\t"
"v_fma_f32 v54, v33, s38, v48\n\t"
"v_fma_f32 v57, -v33, s38, v48\n\t"
"v_fma_f32 v60, v33, s39, v51\n\t"
"v_fma_f32 v63, -v33, s39, v51\n\t"
"v_fma_f32 v55, v36, s38, v49\n\t"
"v_fma_f32 v58, -v36, s38, v49\n\t"
"v_fma_f32 v61, v36, s39, v52\n\t"
"v_fma_f32 v64, -v36, s39, v52\n\t"
"v_fma_f32 v56, v39, s38, v50\n\t"
"v_fma_f32 v59, -v39, s38, v50\n\t"
"v_fma_f32 v62, v39, s39, v53\n\t"
"v_fma_f32 v65, -v39, s39, v53\n\t"
"v_mul_f32 v70, s58, v32\n\t"
"v_mul_f32 v71, s58, v35\n\t"
"v_mul_f32 v72, s58, v38\n\t"
"v_mul_f32 v73, 0xbf54ad4b, v54\n\t"
"v_mul_f32 v74, 0xbf54ad4b, v55\n\t"
"v_mul_f32 v75, 0xbf54ad4b, v56\n\t"
"v_mul_f32 v76, 0xbefc0fc1, v57\n\t"
"v_mul_f32 v77, 0xbefc0fc1, v58\n\t"
"v_mul_f32 v78, 0xbefc0fc1, v59\n\t"
"v_mul_f32 v79, 0xbe7c0fc1, v60\n\t"
"v_mul_f32 v80, 0xbe7c0fc1, v61\n\t"
"v_mul_f32 v81, 0xbe7c0fc1, v62\n\t"
"v_mul_f32 v82, 0x3ea80a81, v63\n\t"
"v_mul_f32 v83, 0x3ea80a81, v64\n\t"
"v_mul_f32 v84, 0x3ea80a81, v65\n\t"
"v_mul_f32 v85, s59, v34\n\t"
"v_mul_f32 v86, s59, v37\n\t"
"v_mul_f32 v87, s59, v40\n\t"
"v_cvt_pk_bf16_f32 v88, v70, v71\n\t"
"v_cvt_pk_bf16_f32 v89, v72, v73\n\t"
"v_cvt_pk_bf16_f32 v90, v74, v75\n\t"
"v_cvt_pk_bf16_f32 v91, v76, v77\n\t"
"v_cvt_pk_bf16_f32 v92, v78, v79\n\t"
"v_cvt_pk_bf16_f32 v93, v80, v81\n\t"
"v_cvt_pk_bf16_f32 v94, v82, v83\n\t"
"v_cvt_pk_bf16_f32 v95, v84, v85\n\t"
"v_cvt_pk_bf16_f32 v96, v86, v87\n\t"
"ds_write_b16 v42, v88 offset:0\n\t"
"ds_write_b16_d16_hi v42, v88 offset:1024\n\t"
"ds_write_b16 v42, v89 offset:2048\n\t"
"ds_write_b16_d16_hi v42, v89 offset:128\n\t"
"ds_write_b16 v42, v90 offset:1152\n\t"
"ds_write_b16_d16_hi v42, v90 offset:2176\n\t"
"ds_write_b16 v45, v91 offset:256\n\t"
"ds_write_b16_d16_hi v45, v91 offset:1280\n\t"
"ds_write_b16 v45, v92 offset:2304\n\t"
"ds_write_b16_d16_hi v42, v92 offset:384\n\t"
"ds_write_b16 v42, v93 offset:1408\n\t"
"ds_write_b16_d16_hi v42, v93 offset:2432\n\t"
"ds_write_b16 v45, v94 offset:512\n\t"
"ds_write_b16_d16_hi v45, v94 offset:1536\n\t"
"ds_write_b16 v45, v95 offset:2560\n\t"
"ds_write_b16_d16_hi v42, v95 offset:640\n\t"
"ds_write_b16 v42, v96 offset:1664\n\t"
"ds_write_b16_d16_hi v42, v96 offset:2688\n\t"
"ds_read_b128 v[48:51], v43 offset:0\n\t"
"ds_read_b128 v[52:55], v43 offset:1024\n\t"
"ds_read_b128 v[56:59], v43 offset:2048\n\t"
"s_waitcnt lgkmcnt(2)\n\t"
"buffer_store_dwordx4 v[48:51], v44, s[8:11], s57 offen offset:0 sc1\n\t"
"s_waitcnt lgkmcnt(1)\n\t"
"buffer_store_dwordx4 v[52:55], v44, s[8:11], s57 offen offset:1024 sc1\n\t"
"s_waitcnt lgkmcnt(0)\n\t"
"buffer_store_dwordx4 v[56:59], v44, s[8:11], s57 offen offset:2048 sc1\n\t"
"s_waitcnt vmcnt(14)\n\t"
"v_and_b32 v254, s40, v251\n\t"
"v_add_u32 v254, s42, v254\n\t"
"s_lshr_b32 s40, s73, 1\n\t"
"s_mul_i32 s40, s40, 768\n\t"
"s_add_u32 s40, s40, s22\n\t"
"s_mov_b32 s60, 0\n\t"
"buffer_load_dwordx4 a[0:3], v29, s[52:55], s60 offen\n\t"
"s_mov_b32 s60, 6144\n\t"
"buffer_load_dwordx4 a[24:27], v29, s[52:55], s60 offen\n\t"
"s_mov_b32 s60, 0\n\t"
"buffer_load_dwordx4 a[48:51], v30, s[52:55], s60 offen\n\t"
"s_mov_b32 s60, 221184\n\t"
"buffer_load_dwordx4 a[8:11], v29, s[52:55], s60 offen\n\t"
"s_mov_b32 s60, 227328\n\t"
"buffer_load_dwordx4 a[32:35], v29, s[52:55], s60 offen\n\t"
"s_mov_b32 s60, 221184\n\t"
"buffer_load_dwordx4 a[56:59], v30, s[52:55], s60 offen\n\t"
"s_mov_b32 s60, 442368\n\t"
"buffer_load_dwordx4 a[16:19], v29, s[52:55], s60 offen\n\t"
"s_mov_b32 s60, 448512\n\t"
"buffer_load_dwordx4 a[40:43], v29, s[52:55], s60 offen\n\t"
"s_mov_b32 s60, 442368\n\t"
"buffer_load_dwordx4 a[64:67], v30, s[52:55], s60 offen\n\t"
"s_mov_b32 s60, 147456\n\t"
"buffer_load_dwordx4 a[72:75], v29, s[52:55], s60 offen\n\t"
"s_mov_b32 s60, 153600\n\t"
"buffer_load_dwordx4 a[96:99], v29, s[52:55], s60 offen\n\t"
"s_mov_b32 s60, 147456\n\t"
"buffer_load_dwordx4 a[120:123], v30, s[52:55], s60 offen\n\t"
"s_waitcnt vmcnt(12)\n\t"
"s_barrier\n\t"
"v_readfirstlane_b32 s67, v46\n\t"
"v_readfirstlane_b32 s71, v47\n\t"
"s_add_u32 s67, s67, 1\n\t"
"s_and_b32 s67, s67, 0xffff\n\t"
"s_add_u32 s71, s71, 0x10000\n\t"
"s_and_b32 s71, s71, 0xffff0000\n\t"
"s_or_b32 s71, s71, s67\n\t"
"s_mov_b32 s68, 3000\n\t"
"s_cmp_lg_u32 s73, 0\n\t"
"s_cbranch_scc1 Lnl2_%=\n\t"
"v_mov_b32 v47, s71\n\t"
"s_mov_b64 exec, 1\n\t"
"buffer_store_dword v47, off, s[8:11], s70 sc1\n\t"
"s_mov_b64 exec, -1\n\t"
"Lnl2_%=:\n\t"
"s_mov_b32 s60, 368640\n\t"
"buffer_load_dwordx4 a[80:83], v29, s[52:55], s60 offen\n\t"
"s_mov_b32 s60, 374784\n\t"
"buffer_load_dwordx4 a[104:107], v29, s[52:55], s60 offen\n\t"
"s_mov_b32 s60, 368640\n\t"
"buffer_load_dwordx4 a[128:131], v30, s[52:55], s60 offen\n\t"
"s_waitcnt vmcnt(14)\n\t"
"ds_write_b128 v43, a[0:3] offset:0\n\t"
"s_waitcnt vmcnt(13)\n\t"
"ds_write_b128 v43, a[24:27] offset:1024\n\t"
"s_waitcnt vmcnt(12)\n\t"
"ds_write_b128 v43, a[48:51] offset:2048\n\t"
"ds_read_b64_tr_b16 a[0:1], v43 offset:0\n\t"
"ds_read_b64_tr_b16 a[2:3], v43 offset:1024\n\t"
"ds_read_b64_tr_b16 a[24:25], v43 offset:8\n\t"
"ds_read_b64_tr_b16 a[26:27], v43 offset:1032\n\t"
"ds_read_b64_tr_b16 a[48:49], v31 offset:2048\n\t"
"ds_read_b64_tr_b16 a[50:51], v31 offset:2560\n\t"
"s_mov_b32 s60, 589824\n\t"
"buffer_load_dwordx4 a[88:91], v29, s[52:55], s60 offen\n\t"
"s_mov_b32 s60, 595968\n\t"
"buffer_load_dwordx4 a[112:115], v29, s[52:55], s60 offen\n\t"
"s_mov_b32 s60, 589824\n\t"
"buffer_load_dwordx4 a[136:139], v30, s[52:55], s60 offen\n\t"
"s_waitcnt vmcnt(14)\n\t"
"ds_write_b128 v43, a[8:11] offset:0\n\t"
"s_waitcnt vmcnt(13)\n\t"
"ds_write_b128 v43, a[32:35] offset:1024\n\t"
"s_waitcnt vmcnt(12)\n\t"
"ds_write_b128 v43, a[56:59] offset:2048\n\t"
"ds_read_b64_tr_b16 a[8:9], v43 offset:0\n\t"
"ds_read_b64_tr_b16 a[10:11], v43 offset:1024\n\t"
"ds_read_b64_tr_b16 a[32:33], v43 offset:8\n\t"
"ds_read_b64_tr_b16 a[34:35], v43 offset:1032\n\t"
"ds_read_b64_tr_b16 a[56:57], v31 offset:2048\n\t"
"ds_read_b64_tr_b16 a[58:59], v31 offset:2560\n\t"
"s_add_u32 m0, s25, 0\n\t"
"s_nop 0\n\t"
"buffer_load_dwordx4 v254, s[12:15], 0 offen offset:0 nt lds\n\t"
"s_add_u32 m0, s26, 0\n\t"
"s_mov_b64 exec, s[36:37]\n\t"
"buffer_load_dwordx4 v254, s[12:15], s27 offen offset:0 nt lds\n\t"
"s_mov_b64 exec, -1\n\t"
"s_add_u32 m0, s24, 32256\n\t"
"s_add_u32 s28, s21, 983040\n\t"
"buffer_load_dwordx4 v247, s[12:15], s28 offen offset:0 nt lds\n\t"
"s_waitcnt vmcnt(14)\n\t"
"ds_write_b128 v43, a[16:19] offset:0\n\t"
"s_waitcnt vmcnt(13)\n\t"
"ds_write_b128 v43, a[40:43] offset:1024\n\t"
"s_waitcnt vmcnt(12)\n\t"
"ds_write_b128 v43, a[64:67] offset:2048\n\t"
"ds_read_b64_tr_b16 a[16:17], v43 offset:0\n\t"
"ds_read_b64_tr_b16 a[18:19], v43 offset:1024\n\t"
"ds_read_b64_tr_b16 a[40:41], v43 offset:8\n\t"
"ds_read_b64_tr_b16 a[42:43], v43 offset:1032\n\t"
"ds_read_b64_tr_b16 a[64:65], v31 offset:2048\n\t"
"ds_read_b64_tr_b16 a[66:67], v31 offset:2560\n\t"
"s_add_u32 m0, s24, 0\n\t"
"s_add_u32 s28, s21, 0\n\t"
"buffer_load_dwordx4 v247, s[12:15], s28 offen offset:0 nt lds\n\t"
"s_add_u32 m0, s24, 6144\n\t"
"s_add_u32 s28, s21, 196608\n\t"
"buffer_load_dwordx4 v247, s[12:15], s28 offen offset:0 nt lds\n\t"
"s_add_u32 m0, s24, 12288\n\t"
"s_add_u32 s28, s21, 393216\n\t"
"buffer_load_dwordx4 v247, s[12:15], s28 offen offset:0 nt lds\n\t"
"s_waitcnt vmcnt(14)\n\t"
"ds_write_b128 v43, a[72:75] offset:0\n\t"
"s_waitcnt vmcnt(13)\n\t"
"ds_write_b128 v43, a[96:99] offset:1024\n\t"
"s_waitcnt vmcnt(12)\n\t"
"ds_write_b128 v43, a[120:123] offset:2048\n\t"
"ds_read_b64_tr_b16 a[72:73], v43 offset:0\n\t"
"ds_read_b64_tr_b16 a[74:75], v43 offset:1024\n\t"
"ds_read_b64_tr_b16 a[96:97], v43 offset:8\n\t"
"ds_read_b64_tr_b16 a[98:99], v43 offset:1032\n\t"
"ds_read_b64_tr_b16 a[120:121], v31 offset:2048\n\t"
"ds_read_b64_tr_b16 a[122:123], v31 offset:2560\n\t"
"s_cmp_lg_u32 s66, 0\n\t"
"s_cbranch_scc1 Lld4_%=\n\t"
"s_cmp_lg_u32 s73, 0\n\t"
"s_cbranch_scc1 Lld2_%=\n\t"
"Lld1_%=:\n\t"
"s_mov_b32 s60, 1900544\n\t"
"buffer_load_dword v108, v117, s[8:11], s60 offen sc1\n\t"
"s_mov_b32 s60, 1900800\n\t"
"buffer_load_dword v109, v117, s[8:11], s60 offen sc1\n\t"
"s_mov_b32 s60, 1901056\n\t"
"buffer_load_dword v110, v117, s[8:11], s60 offen sc1\n\t"
"s_mov_b32 s60, 1901312\n\t"
"buffer_load_dword v111, v117, s[8:11], s60 offen sc1\n\t"
"s_waitcnt vmcnt(0)\n\t"
"v_xor_b32 v112, v108, v104\n\t"
"v_xor_b32 v113, v109, v105\n\t"
"v_xor_b32 v114, v110, v106\n\t"
"v_xor_b32 v115, v111, v107\n\t"
"s_nop 1\n\t"
"v_writelane_b32 v112, 0, 0\n\t"
"s_nop 1\n\t"
"v_or3_b32 v112, v112, v113, v114\n\t"
"v_or_b32 v112, v112, v115\n\t"
"v_cmp_lt_u32 vcc, 0xffff, v112\n\t"
"s_nop 4\n\t"
"s_cmp_eq_u64 vcc, 0\n\t"
"s_cbranch_scc1 Lld3_%=\n\t"
"s_sub_u32 s68, s68, 1\n\t"
"s_cmp_lg_u32 s68, 0\n\t"
"s_cbranch_scc1 Lld1_%=\n\t"
"s_branch Lld2_%=\n\t"
"Lld3_%=:\n\t"
"s_mov_b32 s60, 1867776\n\t"
"buffer_store_dword v108, v116, s[8:11], s60 offen sc1\n\t"
"s_mov_b32 s60, 1868032\n\t"
"buffer_store_dword v109, v116, s[8:11], s60 offen sc1\n\t"
"s_mov_b32 s60, 1868288\n\t"
"buffer_store_dword v110, v116, s[8:11], s60 offen sc1\n\t"
"s_mov_b32 s60, 1868544\n\t"
"buffer_store_dword v111, v116, s[8:11], s60 offen sc1\n\t"
"s_mov_b32 s68, -1\n\t"
"Lld2_%=:\n\t"
"s_barrier\n\t"
"Lld4_%=:\n\t"
"s_add_u32 m0, s24, 39936\n\t"
"s_add_u32 s28, s21, 1179648\n\t"
"buffer_load_dwordx4 v247, s[12:15], s28 offen offset:0 nt lds\n\t"
"s_add_u32 m0, s24, 46080\n\t"
"s_add_u32 s28, s21, 1376256\n\t"
"buffer_load_dwordx4 v247, s[12:15], s28 offen offset:0 nt lds\n\t"
"s_add_u32 m0, s24, 52224\n\t"
"s_add_u32 s28, s21, 1572864\n\t"
"buffer_load_dwordx4 v247, s[12:15], s28 offen offset:0 nt lds\n\t"
"s_waitcnt vmcnt(14)\n\t"
"ds_write_b128 v43, a[80:83] offset:0\n\t"
"s_waitcnt vmcnt(13)\n\t"
"ds_write_b128 v43, a[104:107] offset:1024\n\t"
"s_waitcnt vmcnt(12)\n\t"
"ds_write_b128 v43, a[128:131] offset:2048\n\t"
"ds_read_b64_tr_b16 a[80:81], v43 offset:0\n\t"
"ds_read_b64_tr_b16 a[82:83], v43 offset:1024\n\t"
"ds_read_b64_tr_b16 a[104:105], v43 offset:8\n\t"
"ds_read_b64_tr_b16 a[106:107], v43 offset:1032\n\t"
"ds_read_b64_tr_b16 a[128:129], v31 offset:2048\n\t"
"ds_read_b64_tr_b16 a[130:131], v31 offset:2560\n\t"
"s_waitcnt vmcnt(11)\n\t"
"ds_write_b128 v43, a[88:91] offset:0\n\t"
"s_waitcnt vmcnt(10)\n\t"
"ds_write_b128 v43, a[112:115] offset:1024\n\t"
"s_waitcnt vmcnt(9)\n\t"
"ds_write_b128 v43, a[136:139] offset:2048\n\t"
"ds_read_b64_tr_b16 a[88:89], v43 offset:0\n\t"
"ds_read_b64_tr_b16 a[90:91], v43 offset:1024\n\t"
"ds_read_b64_tr_b16 a[112:113], v43 offset:8\n\t"
"ds_read_b64_tr_b16 a[114:115], v43 offset:1032\n\t"
"ds_read_b64_tr_b16 a[136:137], v31 offset:2048\n\t"
"ds_read_b64_tr_b16 a[138:139], v31 offset:2560\n\t"
"s_waitcnt lgkmcnt(0)\n\t"
"v_add_u32 v240, 59904, v240\n\t"
"v_add_u32 v241, 59904, v241\n\t"
"s_waitcnt vmcnt(3)\n\t"
"s_barrier\n\t"
"ds_read_b64_tr_b16 v[120:121], v240 offset:0\n\t"
"ds_read_b64_tr_b16 v[122:123], v240 offset:1536\n\t"
"ds_read_b64_tr_b16 v[124:125], v241 offset:0\n\t"
"ds_read_b64_tr_b16 v[126:127], v241 offset:1536\n\t"
"ds_read_b64_tr_b16 v[128:129], v240 offset:19968\n\t"
"ds_read_b64_tr_b16 v[130:131], v240 offset:21504\n\t"
"ds_read_b64_tr_b16 v[132:133], v241 offset:19968\n\t"
"ds_read_b64_tr_b16 v[134:135], v241 offset:21504\n\t"
"v_subrev_u32 v240, 59904, v240\n\t"
"v_subrev_u32 v241, 59904, v241\n\t"
"ds_read_b64_tr_b16 v[136:137], v240 offset:19968\n\t"
"ds_read_b64_tr_b16 v[138:139], v240 offset:21504\n\t"
"ds_read_b64_tr_b16 v[140:141], v241 offset:19968\n\t"
"ds_read_b64_tr_b16 v[142:143], v241 offset:21504\n\t"
"ds_read_b64_tr_b16 v[144:145], v240 offset:0\n\t"
"ds_read_b64_tr_b16 v[146:147], v240 offset:1536\n\t"
"ds_read_b64_tr_b16 v[148:149], v241 offset:0\n\t"
"ds_read_b64_tr_b16 v[150:151], v241 offset:1536\n\t"
"s_add_u32 m0, s24, 99840\n\t"
"s_add_u32 s28, s21, 2949120\n\t"
"buffer_load_dwordx4 v247, s[12:15], s28 offen offset:0 nt lds\n\t"
"s_add_u32 m0, s24, 105984\n\t"
"s_add_u32 s28, s21, 3145728\n\t"
"buffer_load_dwordx4 v247, s[12:15], s28 offen offset:0 nt lds\n\t"
"s_add_u32 m0, s24, 112128\n\t"
"s_add_u32 s28, s21, 3342336\n\t"
"buffer_load_dwordx4 v247, s[12:15], s28 offen offset:0 nt lds\n\t"
"s_waitcnt lgkmcnt(14)\n\t"
"v_mfma_f32_16x16x32_bf16 v[120:123], v[120:123], v[196:199], 0\n\t"
"s_waitcnt lgkmcnt(12)\n\t"
"v_mfma_f32_16x16x32_bf16 v[124:127], v[124:127], v[196:199], 0\n\t"
"s_waitcnt lgkmcnt(10)\n\t"
"v_mfma_f32_16x16x32_bf16 v[128:131], v[128:131], v[196:199], 0\n\t"
"s_waitcnt lgkmcnt(8)\n\t"
"v_mfma_f32_16x16x32_bf16 v[132:135], v[132:135], v[196:199], 0\n\t"
"s_waitcnt lgkmcnt(6)\n\t"
"v_mfma_f32_16x16x32_bf16 v[136:139], v[136:139], v[196:199], 0\n\t"
"s_waitcnt lgkmcnt(4)\n\t"
"v_mfma_f32_16x16x32_bf16 v[140:143], v[140:143], v[196:199], 0\n\t"
"s_waitcnt lgkmcnt(2)\n\t"
"v_mfma_f32_16x16x32_bf16 v[144:147], v[144:147], v[196:199], 0\n\t"
"s_waitcnt lgkmcnt(0)\n\t"
"v_mfma_f32_16x16x32_bf16 v[148:151], v[148:151], v[196:199], 0\n\t"
"v_pk_mul_f32 v[120:121], v[120:121], v[242:243] op_sel:[0,1] op_sel_hi:[1,1]\n\t"
"v_pk_mul_f32 v[122:123], v[122:123], v[242:243] op_sel:[0,1] op_sel_hi:[1,1]\n\t"
"v_cvt_pk_bf16_f32 v120, v120, v121\n\t"
"v_cvt_pk_bf16_f32 v121, v122, v123\n\t"
"v_pk_mul_f32 v[124:125], v[124:125], v[242:243] op_sel:[0,1] op_sel_hi:[1,1]\n\t"
"v_pk_mul_f32 v[126:127], v[126:127], v[242:243] op_sel:[0,1] op_sel_hi:[1,1]\n\t"
"v_cvt_pk_bf16_f32 v122, v124, v125\n\t"
"v_cvt_pk_bf16_f32 v123, v126, v127\n\t"
"s_mov_b64 exec, s[44:45]\n\t"
"ds_write_b128 v242, v[120:123] offset:23040\n\t"
"s_mov_b64 exec, -1\n\t"
"v_pk_mul_f32 v[128:129], v[128:129], v[242:243] op_sel:[0,1] op_sel_hi:[1,1]\n\t"
"v_pk_mul_f32 v[130:131], v[130:131], v[242:243] op_sel:[0,1] op_sel_hi:[1,1]\n\t"
"v_cvt_pk_bf16_f32 v128, v128, v129\n\t"
"v_cvt_pk_bf16_f32 v129, v130, v131\n\t"
"v_pk_mul_f32 v[132:133], v[132:133], v[242:243] op_sel:[0,1] op_sel_hi:[1,1]\n\t"
"v_pk_mul_f32 v[134:135], v[134:135], v[242:243] op_sel:[0,1] op_sel_hi:[1,1]\n\t"
"v_cvt_pk_bf16_f32 v130, v132, v133\n\t"
"v_cvt_pk_bf16_f32 v131, v134, v135\n\t"
"s_mov_b64 exec, s[44:45]\n\t"
"ds_write_b128 v242, v[128:131] offset:24576\n\t"
"s_mov_b64 exec, -1\n\t"
"v_pk_mul_f32 v[136:137], v[136:137], v[242:243] op_sel:[0,1] op_sel_hi:[1,1]\n\t"
"v_pk_mul_f32 v[138:139], v[138:139], v[242:243] op_sel:[0,1] op_sel_hi:[1,1]\n\t"
"v_cvt_pk_bf16_f32 v136, v136, v137\n\t"
"v_cvt_pk_bf16_f32 v137, v138, v139\n\t"
"v_pk_mul_f32 v[140:141], v[140:141], v[242:243] op_sel:[0,1] op_sel_hi:[1,1]\n\t"
"v_pk_mul_f32 v[142:143], v[142:143], v[242:243] op_sel:[0,1] op_sel_hi:[1,1]\n\t"
"v_cvt_pk_bf16_f32 v138, v140, v141\n\t"
"v_cvt_pk_bf16_f32 v139, v142, v143\n\t"
"s_mov_b64 exec, s[44:45]\n\t"
"ds_write_b128 v242, v[136:139] offset:19968\n\t"
"s_mov_b64 exec, -1\n\t"
"v_pk_mul_f32 v[144:145], v[144:145], v[242:243] op_sel:[0,1] op_sel_hi:[1,1]\n\t"
"v_pk_mul_f32 v[146:147], v[146:147], v[242:243] op_sel:[0,1] op_sel_hi:[1,1]\n\t"
"v_cvt_pk_bf16_f32 v144, v144, v145\n\t"
"v_cvt_pk_bf16_f32 v145, v146, v147\n\t"
"v_pk_mul_f32 v[148:149], v[148:149], v[242:243] op_sel:[0,1] op_sel_hi:[1,1]\n\t"
"v_pk_mul_f32 v[150:151], v[150:151], v[242:243] op_sel:[0,1] op_sel_hi:[1,1]\n\t"
"v_cvt_pk_bf16_f32 v146, v148, v149\n\t"
"v_cvt_pk_bf16_f32 v147, v150, v151\n\t"
"s_mov_b64 exec, s[44:45]\n\t"
"ds_write_b128 v242, v[144:147] offset:18432\n\t"
"s_mov_b64 exec, -1\n\t"
"s_waitcnt lgkmcnt(0)\n\t"
"s_barrier\n\t"
"ds_read_b128 a[168:171], v244 offset:19968\n\t"
"ds_read_b128 a[172:175], v244 offset:20480\n\t"
"ds_read_b128 a[176:179], v244 offset:20992\n\t"
"ds_read_b128 a[180:183], v244 offset:29184\n\t"
"ds_read_b128 a[184:187], v244 offset:29696\n\t"
"ds_read_b128 a[188:191], v244 offset:30208\n\t"
"ds_read_b128 a[192:195], v244 offset:19456\n\t"
"s_waitcnt lgkmcnt(4)\n\t"
"v_mfma_f32_16x16x32_bf16 v[14:17], a[0:3], a[168:171], 0\n\t"
"ds_read_b64_tr_b16 v[224:225], v240 offset:512\n\t"
"v_mfma_f32_16x16x32_bf16 v[14:17], a[8:11], a[172:175], v[14:17]\n\t"
"ds_read_b64_tr_b16 v[226:227], v240 offset:2048\n\t"
"v_mfma_f32_16x16x32_bf16 v[14:17], a[16:19], a[176:179], v[14:17]\n\t"
"ds_read_b64_tr_b16 v[228:229], v241 offset:512\n\t"
"ds_read_b64_tr_b16 v[230:231], v241 offset:2048\n\t"
"v_mfma_f32_16x16x32_bf16 v[46:49], a[24:27], a[168:171], 0\n\t"
"v_mfma_f32_16x16x32_bf16 v[46:49], a[32:35], a[172:175], v[46:49]\n\t"
"v_mfma_f32_16x16x32_bf16 v[46:49], a[40:43], a[176:179], v[46:49]\n\t"
"v_mfma_f32_16x16x32_bf16 v[78:81], a[48:51], a[168:171], 0\n\t"
"v_mfma_f32_16x16x32_bf16 v[78:81], a[56:59], a[172:175], v[78:81]\n\t"
"ds_read_b64_tr_b16 v[232:233], v240 offset:20480\n\t"
"v_mfma_f32_16x16x32_bf16 v[78:81], a[64:67], a[176:179], v[78:81]\n\t"
"ds_read_b64_tr_b16 v[234:235], v240 offset:22016\n\t"
"s_waitcnt lgkmcnt(7)\n\t"
"v_mfma_f32_16x16x32_bf16 v[112:115], a[72:75], a[180:183], 0\n\t"
"ds_read_b64_tr_b16 v[236:237], v241 offset:20480\n\t"
"v_mfma_f32_16x16x32_bf16 v[112:115], a[80:83], a[184:187], v[112:115]\n\t"
"v_mfma_f32_16x16x32_bf16 v[112:115], a[88:91], a[188:191], v[112:115]\n\t"
"ds_read_b64_tr_b16 v[238:239], v241 offset:22016\n\t"
"s_waitcnt lgkmcnt(6)\n\t"
"v_mfma_f32_16x16x32_bf16 v[224:227], v[224:227], v[196:199], 0\n\t"
"s_waitcnt lgkmcnt(4)\n\t"
"v_mfma_f32_16x16x32_bf16 v[228:231], v[228:231], v[196:199], 0\n\t"
"ds_read_b128 a[196:199], v244 offset:28672\n\t"
"v_mfma_f32_16x16x32_bf16 v[144:147], a[96:99], a[180:183], 0\n\t"
"v_mfma_f32_16x16x32_bf16 v[144:147], a[104:107], a[184:187], v[144:147]\n\t"
"v_mfma_f32_16x16x32_bf16 v[144:147], a[112:115], a[188:191], v[144:147]\n\t"
"v_mfma_f32_16x16x32_bf16 v[176:179], a[120:123], a[180:183], 0\n\t"
"s_nop 0\n\t"
"v_cvt_pk_bf16_f32 v224, v224, v225\n\t"
"v_cvt_pk_bf16_f32 v225, v226, v227\n\t"
"v_mfma_f32_16x16x32_bf16 v[176:179], a[128:131], a[184:187], v[176:179]\n\t"
"v_cvt_pk_bf16_f32 v226, v228, v229\n\t"
"v_cvt_pk_bf16_f32 v227, v230, v231\n\t"
"v_mfma_f32_16x16x32_bf16 v[176:179], a[136:139], a[188:191], v[176:179]\n\t"
"ds_read_b128 a[176:179], v244 offset:18944\n\t"
"v_mfma_f32_16x16x32_bf16 v[10:13], a[0:3], a[192:195], 0\n\t"
"v_mfma_f32_16x16x32_bf16 v[10:13], a[8:11], a[168:171], v[10:13]\n\t"
"v_mfma_f32_16x16x32_bf16 v[10:13], a[16:19], a[172:175], v[10:13]\n\t"
"s_mov_b64 exec, s[44:45]\n\t"
"ds_write_b128 v242, v[224:227] offset:0\n\t"
"s_mov_b64 exec, -1\n\t"
"s_waitcnt lgkmcnt(5)\n\t"
"v_mfma_f32_16x16x32_bf16 v[232:235], v[232:235], v[196:199], 0\n\t"
"s_waitcnt lgkmcnt(3)\n\t"
"v_mfma_f32_16x16x32_bf16 v[236:239], v[236:239], v[196:199], 0\n\t"
"s_waitcnt vmcnt(3)\n\t"
"s_barrier\n\t"
"v_mfma_f32_16x16x32_bf16 v[42:45], a[24:27], a[192:195], 0\n\t"
"ds_read_b64_tr_b16 v[228:229], v240 offset:39936\n\t"
"v_mfma_f32_16x16x32_bf16 v[42:45], a[32:35], a[168:171], v[42:45]\n\t"
"ds_read_b64_tr_b16 v[230:231], v240 offset:41472\n\t"
"v_mfma_f32_16x16x32_bf16 v[42:45], a[40:43], a[172:175], v[42:45]\n\t"
"ds_read_b64_tr_b16 v[224:225], v241 offset:39936\n\t"
"ds_read_b64_tr_b16 v[226:227], v241 offset:41472\n\t"
"v_mfma_f32_16x16x32_bf16 v[74:77], a[48:51], a[192:195], 0\n\t"
"v_cvt_pk_bf16_f32 v232, v232, v233\n\t"
"v_cvt_pk_bf16_f32 v233, v234, v235\n\t"
"v_mfma_f32_16x16x32_bf16 v[74:77], a[56:59], a[168:171], v[74:77]\n\t"
"v_cvt_pk_bf16_f32 v234, v236, v237\n\t"
"v_cvt_pk_bf16_f32 v235, v238, v239\n\t"
"v_mfma_f32_16x16x32_bf16 v[74:77], a[64:67], a[172:175], v[74:77]\n\t"
"ds_read_b128 a[188:191], v244 offset:28160\n\t"
"s_waitcnt lgkmcnt(7)\n\t"
"v_mfma_f32_16x16x32_bf16 v[108:111], a[72:75], a[196:199], 0\n\t"
"v_mfma_f32_16x16x32_bf16 v[108:111], a[80:83], a[180:183], v[108:111]\n\t"
"ds_read_b64_tr_b16 v[236:237], v240 offset:40448\n\t"
"v_mfma_f32_16x16x32_bf16 v[108:111], a[88:91], a[184:187], v[108:111]\n\t"
"s_mov_b64 exec, s[44:45]\n\t"
"ds_write_b128 v242, v[232:235] offset:1536\n\t"
"s_mov_b64 exec, -1\n\t"
"s_waitcnt lgkmcnt(5)\n\t"
"v_mfma_f32_16x16x32_bf16 v[228:231], v[228:231], v[196:199], 0\n\t"
"ds_read_b64_tr_b16 v[238:239], v240 offset:41984\n\t"
"s_waitcnt lgkmcnt(4)\n\t"
"v_mfma_f32_16x16x32_bf16 v[224:227], v[224:227], v[196:199], 0\n\t"
"v_mfma_f32_16x16x32_bf16 v[140:143], a[96:99], a[196:199], 0\n\t"
"ds_read_b64_tr_b16 v[232:233], v241 offset:40448\n\t"
"v_mfma_f32_16x16x32_bf16 v[140:143], a[104:107], a[180:183], v[140:143]\n\t"
"ds_read_b64_tr_b16 v[234:235], v241 offset:41984\n\t"
"v_mfma_f32_16x16x32_bf16 v[140:143], a[112:115], a[184:187], v[140:143]\n\t"
"v_pk_mul_f32 v[228:229], v[228:229], v[242:243] op_sel:[0,1] op_sel_hi:[1,1]\n\t"
"v_mfma_f32_16x16x32_bf16 v[172:175], a[120:123], a[196:199], 0\n\t"
"v_cvt_pk_bf16_f32 v228, v228, v229\n\t"
"v_mfma_f32_16x16x32_bf16 v[172:175], a[128:131], a[180:183], v[172:175]\n\t"
"v_mfma_f32_16x16x32_bf16 v[172:175], a[136:139], a[184:187], v[172:175]\n\t"
"v_pk_mul_f32 v[230:231], v[230:231], v[242:243] op_sel:[0,1] op_sel_hi:[1,1]\n\t"
"v_cvt_pk_bf16_f32 v229, v230, v231\n\t"
"v_mfma_f32_16x16x32_bf16 v[6:9], a[0:3], a[176:179], 0\n\t"
"v_add_u32 v240, 59904, v240\n\t"
"v_add_u32 v241, 59904, v241\n\t"
"v_mfma_f32_16x16x32_bf16 v[6:9], a[8:11], a[192:195], v[6:9]\n\t"
"v_mfma_f32_16x16x32_bf16 v[6:9], a[16:19], a[168:171], v[6:9]\n\t"
"v_pk_mul_f32 v[224:225], v[224:225], v[242:243] op_sel:[0,1] op_sel_hi:[1,1]\n\t"
"v_pk_mul_f32 v[226:227], v[226:227], v[242:243] op_sel:[0,1] op_sel_hi:[1,1]\n\t"
"v_cvt_pk_bf16_f32 v230, v224, v225\n\t"
"v_cvt_pk_bf16_f32 v231, v226, v227\n\t"
"v_mfma_f32_16x16x32_bf16 v[38:41], a[24:27], a[176:179], 0\n\t"
"v_mfma_f32_16x16x32_bf16 v[38:41], a[32:35], a[192:195], v[38:41]\n\t"
"ds_read_b64_tr_b16 v[224:225], v240 offset:512\n\t"
"v_mfma_f32_16x16x32_bf16 v[38:41], a[40:43], a[168:171], v[38:41]\n\t"
"s_mov_b64 exec, s[44:45]\n\t"
"ds_write_b128 v242, v[228:231] offset:21504\n\t"
"s_mov_b64 exec, -1\n\t"
"s_waitcnt lgkmcnt(4)\n\t"
"v_mfma_f32_16x16x32_bf16 v[236:239], v[236:239], v[196:199], 0\n\t"
"ds_read_b64_tr_b16 v[226:227], v240 offset:2048\n\t"
"s_waitcnt lgkmcnt(3)\n\t"
"v_mfma_f32_16x16x32_bf16 v[232:235], v[232:235], v[196:199], 0\n\t"
"ds_read_b64_tr_b16 v[228:229], v241 offset:512\n\t"
"v_mfma_f32_16x16x32_bf16 v[70:73], a[48:51], a[176:179], 0\n\t"
"v_mfma_f32_16x16x32_bf16 v[70:73], a[56:59], a[192:195], v[70:73]\n\t"
"ds_read_b64_tr_b16 v[230:231], v241 offset:2048\n\t"
"v_mfma_f32_16x16x32_bf16 v[70:73], a[64:67], a[168:171], v[70:73]\n\t"
"v_mfma_f32_16x16x32_bf16 v[104:107], a[72:75], a[188:191], 0\n\t"
"v_cvt_pk_bf16_f32 v236, v236, v237\n\t"
"v_cvt_pk_bf16_f32 v237, v238, v239\n\t"
"v_mfma_f32_16x16x32_bf16 v[104:107], a[80:83], a[196:199], v[104:107]\n\t"
"v_cvt_pk_bf16_f32 v238, v232, v233\n\t"
"v_mfma_f32_16x16x32_bf16 v[104:107], a[88:91], a[180:183], v[104:107]\n\t"
"v_cvt_pk_bf16_f32 v239, v234, v235\n\t"
"s_waitcnt lgkmcnt(3)\n\t"
"s_barrier\n\t"
"v_mfma_f32_16x16x32_bf16 v[136:139], a[96:99], a[188:191], 0\n\t"
"ds_read_b128 a[168:171], v244 offset:18432\n\t"
"v_mfma_f32_16x16x32_bf16 v[136:139], a[104:107], a[196:199], v[136:139]\n\t"
"ds_read_b128 a[172:175], v244 offset:27648\n\t"
"v_mfma_f32_16x16x32_bf16 v[136:139], a[112:115], a[180:183], v[136:139]\n\t"
"s_mov_b64 exec, s[44:45]\n\t"
"ds_write_b128 v242, v[236:239] offset:3072\n\t"
"s_mov_b64 exec, -1\n\t"
"v_mfma_f32_16x16x32_bf16 v[168:171], a[120:123], a[188:191], 0\n\t"
"v_mfma_f32_16x16x32_bf16 v[168:171], a[128:131], a[196:199], v[168:171]\n\t"
"ds_read_b128 a[184:187], v244 offset:20480\n\t"
"v_mfma_f32_16x16x32_bf16 v[168:171], a[136:139], a[180:183], v[168:171]\n\t"
"ds_read_b128 a[200:203], v244 offset:20992\n\t"
"s_waitcnt lgkmcnt(7)\n\t"
"v_mfma_f32_16x16x32_bf16 v[224:227], v[224:227], v[196:199], 0\n\t"
"s_waitcnt lgkmcnt(5)\n\t"
"v_mfma_f32_16x16x32_bf16 v[228:231], v[228:231], v[196:199], 0\n\t"
"ds_read_b128 a[204:207], v244 offset:21504\n\t"
"s_waitcnt lgkmcnt(5)\n\t"
"v_mfma_f32_16x16x32_bf16 v[2:5], a[0:3], a[168:171], 0\n\t"
"v_mfma_f32_16x16x32_bf16 v[2:5], a[8:11], a[176:179], v[2:5]\n\t"
"v_mfma_f32_16x16x32_bf16 v[2:5], a[16:19], a[192:195], v[2:5]\n\t"
"s_waitcnt vmcnt(0)\n\t"
"s_barrier\n\t"
"s_add_u32 s60, s66, 1867776\n\t"
"buffer_load_dword v98, off, s[8:11], s60 sc1\n\t"
"v_mfma_f32_16x16x32_bf16 v[34:37], a[24:27], a[168:171], 0\n\t"
"ds_read_b64_tr_b16 v[232:233], v240 offset:39936\n\t"
"v_cvt_pk_bf16_f32 v224, v224, v225\n\t"
"v_mfma_f32_16x16x32_bf16 v[34:37], a[32:35], a[176:179], v[34:37]\n\t"
"ds_read_b64_tr_b16 v[234:235], v240 offset:41472\n\t"
"v_cvt_pk_bf16_f32 v225, v226, v227\n\t"
"v_mfma_f32_16x16x32_bf16 v[34:37], a[40:43], a[192:195], v[34:37]\n\t"
"ds_read_b64_tr_b16 v[236:237], v241 offset:39936\n\t"
"ds_read_b64_tr_b16 v[238:239], v241 offset:41472\n\t"
"v_mfma_f32_16x16x32_bf16 v[66:69], a[48:51], a[168:171], 0\n\t"
"v_cvt_pk_bf16_f32 v226, v228, v229\n\t"
"v_cvt_pk_bf16_f32 v227, v230, v231\n\t"
"v_mfma_f32_16x16x32_bf16 v[66:69], a[56:59], a[176:179], v[66:69]\n\t"
"ds_read_b128 a[168:171], v244 offset:29696\n\t"
"v_mfma_f32_16x16x32_bf16 v[66:69], a[64:67], a[192:195], v[66:69]\n\t"
"s_mov_b64 exec, s[44:45]\n\t"
"ds_write_b128 v242, v[224:227] offset:4608\n\t"
"s_mov_b64 exec, -1\n\t"
"s_waitcnt lgkmcnt(10)\n\t"
"v_mfma_f32_16x16x32_bf16 v[100:103], a[72:75], a[172:175], 0\n\t"
"ds_read_b128 a[176:179], v244 offset:30208\n\t"
"v_mfma_f32_16x16x32_bf16 v[100:103], a[80:83], a[188:191], v[100:103]\n\t"
"ds_read_b128 a[180:183], v244 offset:30720\n\t"
"v_mfma_f32_16x16x32_bf16 v[100:103], a[88:91], a[196:199], v[100:103]\n\t"
"ds_read_b64_tr_b16 v[228:229], v240 offset:20480\n\t"
"s_waitcnt lgkmcnt(7)\n\t"
"v_mfma_f32_16x16x32_bf16 v[232:235], v[232:235], v[196:199], 0\n\t"
"s_waitcnt lgkmcnt(5)\n\t"
"v_mfma_f32_16x16x32_bf16 v[236:239], v[236:239], v[196:199], 0\n\t"
"ds_read_b64_tr_b16 v[230:231], v240 offset:22016\n\t"
"v_mfma_f32_16x16x32_bf16 v[132:135], a[96:99], a[172:175], 0\n\t"
"ds_read_b64_tr_b16 v[224:225], v241 offset:20480\n\t"
"v_mfma_f32_16x16x32_bf16 v[132:135], a[104:107], a[188:191], v[132:135]\n\t"
"v_mfma_f32_16x16x32_bf16 v[132:135], a[112:115], a[196:199], v[132:135]\n\t"
"s_nop 0\n\t"
"v_pk_mul_f32 v[232:233], v[232:233], v[242:243] op_sel:[0,1] op_sel_hi:[1,1]\n\t"
"v_pk_mul_f32 v[234:235], v[234:235], v[242:243] op_sel:[0,1] op_sel_hi:[1,1]\n\t"
"v_mfma_f32_16x16x32_bf16 v[164:167], a[120:123], a[172:175], 0\n\t"
"ds_read_b64_tr_b16 v[226:227], v241 offset:22016\n\t"
"v_cvt_pk_bf16_f32 v232, v232, v233\n\t"
"v_mfma_f32_16x16x32_bf16 v[164:167], a[128:131], a[188:191], v[164:167]\n\t"
"v_cvt_pk_bf16_f32 v233, v234, v235\n\t"
"v_mfma_f32_16x16x32_bf16 v[164:167], a[136:139], a[196:199], v[164:167]\n\t"
"v_pk_mul_f32 v[236:237], v[236:237], v[242:243] op_sel:[0,1] op_sel_hi:[1,1]\n\t"
"v_pk_mul_f32 v[238:239], v[238:239], v[242:243] op_sel:[0,1] op_sel_hi:[1,1]\n\t"
"v_mfma_f32_16x16x32_bf16 v[18:21], a[0:3], a[184:187], 0\n\t"
"v_cvt_pk_bf16_f32 v234, v236, v237\n\t"
"v_cvt_pk_bf16_f32 v235, v238, v239\n\t"
"v_mfma_f32_16x16x32_bf16 v[18:21], a[8:11], a[200:203], v[18:21]\n\t"
"ds_read_b64_tr_b16 v[236:237], v240 offset:40448\n\t"
"v_mfma_f32_16x16x32_bf16 v[18:21], a[16:19], a[204:207], v[18:21]\n\t"
"s_mov_b64 exec, s[44:45]\n\t"
"ds_write_b128 v242, v[232:235] offset:26112\n\t"
"s_mov_b64 exec, -1\n\t"
"v_mfma_f32_16x16x32_bf16 v[50:53], a[24:27], a[184:187], 0\n\t"
"ds_read_b64_tr_b16 v[238:239], v240 offset:41984\n\t"
"v_mfma_f32_16x16x32_bf16 v[50:53], a[32:35], a[200:203], v[50:53]\n\t"
"ds_read_b64_tr_b16 v[232:233], v241 offset:40448\n\t"
"v_mfma_f32_16x16x32_bf16 v[50:53], a[40:43], a[204:207], v[50:53]\n\t"
"ds_read_b64_tr_b16 v[234:235], v241 offset:41984\n\t"
"s_waitcnt lgkmcnt(7)\n\t"
"v_mfma_f32_16x16x32_bf16 v[228:231], v[228:231], v[196:199], 0\n\t"
"s_waitcnt lgkmcnt(5)\n\t"
"v_mfma_f32_16x16x32_bf16 v[224:227], v[224:227], v[196:199], 0\n\t"
"v_mfma_f32_16x16x32_bf16 v[82:85], a[48:51], a[184:187], 0\n\t"
"v_mfma_f32_16x16x32_bf16 v[82:85], a[56:59], a[200:203], v[82:85]\n\t"
"v_mfma_f32_16x16x32_bf16 v[82:85], a[64:67], a[204:207], v[82:85]\n\t"
"v_mfma_f32_16x16x32_bf16 v[116:119], a[72:75], a[168:171], 0\n\t"
"s_nop 1\n\t"
"v_cvt_pk_bf16_f32 v228, v228, v229\n\t"
"v_cvt_pk_bf16_f32 v229, v230, v231\n\t"
"v_mfma_f32_16x16x32_bf16 v[116:119], a[80:83], a[176:179], v[116:119]\n\t"
"v_cvt_pk_bf16_f32 v230, v224, v225\n\t"
"v_mfma_f32_16x16x32_bf16 v[116:119], a[88:91], a[180:183], v[116:119]\n\t"
"v_cvt_pk_bf16_f32 v231, v226, v227\n\t"
"s_waitcnt lgkmcnt(3)\n\t"
"s_barrier\n\t"
"v_mfma_f32_16x16x32_bf16 v[148:151], a[96:99], a[168:171], 0\n\t"
"ds_read_b128 a[188:191], v244 offset:22016\n\t"
"v_mfma_f32_16x16x32_bf16 v[148:151], a[104:107], a[176:179], v[148:151]\n\t"
"ds_read_b128 v[204:207], v244 offset:31232\n\t"
"v_mfma_f32_16x16x32_bf16 v[148:151], a[112:115], a[180:183], v[148:151]\n\t"
"s_mov_b64 exec, s[44:45]\n\t"
"ds_write_b128 v242, v[228:231] offset:6144\n\t"
"s_mov_b64 exec, -1\n\t"
"s_waitcnt lgkmcnt(5)\n\t"
"v_mfma_f32_16x16x32_bf16 v[236:239], v[236:239], v[196:199], 0\n\t"
"ds_read_b128 a[192:195], v244 offset:22528\n\t"
"s_waitcnt lgkmcnt(4)\n\t"
"v_mfma_f32_16x16x32_bf16 v[232:235], v[232:235], v[196:199], 0\n\t"
"v_mfma_f32_16x16x32_bf16 v[180:183], a[120:123], a[168:171], 0\n\t"
"v_mfma_f32_16x16x32_bf16 v[180:183], a[128:131], a[176:179], v[180:183]\n\t"
"v_mfma_f32_16x16x32_bf16 v[180:183], a[136:139], a[180:183], v[180:183]\n\t"
"s_waitcnt lgkmcnt(3)\n\t"
"v_mfma_f32_16x16x32_bf16 v[22:25], a[0:3], a[200:203], 0\n\t"
"v_cvt_pk_bf16_f32 v236, v236, v237\n\t"
"v_cvt_pk_bf16_f32 v237, v238, v239\n\t"
"v_mfma_f32_16x16x32_bf16 v[22:25], a[8:11], a[204:207], v[22:25]\n\t"
"v_cvt_pk_bf16_f32 v238, v232, v233\n\t"
"v_mfma_f32_16x16x32_bf16 v[22:25], a[16:19], a[188:191], v[22:25]\n\t"
"v_cvt_pk_bf16_f32 v239, v234, v235\n\t"
"v_mfma_f32_16x16x32_bf16 v[54:57], a[24:27], a[200:203], 0\n\t"
"ds_read_b128 v[196:199], v244 offset:31744\n\t"
"v_mfma_f32_16x16x32_bf16 v[54:57], a[32:35], a[204:207], v[54:57]\n\t"
"v_mfma_f32_16x16x32_bf16 v[54:57], a[40:43], a[188:191], v[54:57]\n\t"
"s_mov_b64 exec, s[44:45]\n\t"
"ds_write_b128 v242, v[236:239] offset:7680\n\t"
"s_mov_b64 exec, -1\n\t"
"v_mfma_f32_16x16x32_bf16 v[86:89], a[48:51], a[200:203], 0\n\t"
"v_mfma_f32_16x16x32_bf16 v[86:89], a[56:59], a[204:207], v[86:89]\n\t"
"v_mfma_f32_16x16x32_bf16 v[86:89], a[64:67], a[188:191], v[86:89]\n\t"
"s_waitcnt lgkmcnt(4)\n\t"
"v_mfma_f32_16x16x32_bf16 v[120:123], a[72:75], a[176:179], 0\n\t"
"v_mfma_f32_16x16x32_bf16 v[120:123], a[80:83], a[180:183], v[120:123]\n\t"
"v_mfma_f32_16x16x32_bf16 v[120:123], a[88:91], v[204:207], v[120:123]\n\t"
"v_mfma_f32_16x16x32_bf16 v[152:155], a[96:99], a[176:179], 0\n\t"
"ds_read_b128 a[196:199], v244 offset:23040\n\t"
"v_mfma_f32_16x16x32_bf16 v[152:155], a[104:107], a[180:183], v[152:155]\n\t"
"v_mfma_f32_16x16x32_bf16 v[152:155], a[112:115], v[204:207], v[152:155]\n\t"
"s_waitcnt lgkmcnt(1)\n\t"
"s_waitcnt vmcnt(0)\n\t"
"s_cmp_lg_u32 s73, 0\n\t"
"s_cbranch_scc1 Lgo_%=\n\t"
"s_cmp_lg_u32 s66, 0\n\t"
"s_cbranch_scc1 Lchk_%=\n\t"
"s_cmp_eq_u32 s68, -1\n\t"
"s_cbranch_scc1 Lgo_%=\n\t"
"s_branch Lfb_%=\n\t"
"Lchk_%=:\n\t"
"s_waitcnt vmcnt(0)\n\t"
"v_readfirstlane_b32 s60, v98\n\t"
"s_and_b32 s60, s60, 0xffff\n\t"
"s_cmp_eq_u32 s60, s67\n\t"
"s_cbranch_scc1 Lgo_%=\n\t"
"s_sub_u32 s68, s68, 1\n\t"
"s_cmp_eq_u32 s68, 0\n\t"
"s_cbranch_scc1 Lfb_%=\n\t"
"s_add_u32 s60, s66, 1835008\n\t"
"buffer_load_dword v99, off, s[8:11], s60 sc1\n\t"
"s_add_u32 s60, s66, 1867776\n\t"
"buffer_load_dword v98, off, s[8:11], s60 sc1\n\t"
"s_waitcnt vmcnt(1)\n\t"
"v_readfirstlane_b32 s60, v99\n\t"
"s_and_b32 s60, s60, 0xffff0000\n\t"
"s_or_b32 s60, s60, s67\n\t"
"s_cmp_eq_u32 s60, s71\n\t"
"s_cbranch_scc1 Lchk_%=\n\t"
"s_mov_b32 s71, s60\n\t"
"v_mov_b32 v99, s71\n\t"
"s_mov_b64 exec, 1\n\t"
"buffer_store_dword v99, off, s[8:11], s70 sc1\n\t"
"s_mov_b64 exec, -1\n\t"
"s_branch Lchk_%=\n\t"
"Lgo_%=:\n\t"
"s_barrier\n\t"
"s_mov_b32 s52, 0x3fd80000\n\t"
"s_mov_b32 s53, s52\n\t"
"s_mov_b32 s54, 0x3e97b426\n\t"
"s_mov_b32 s55, s54\n\t"
"s_mov_b32 s56, 0x3f400000\n\t"
"s_mov_b32 s57, s56\n\t"
"s_mov_b32 s58, 0x3f2aaaab\n\t"
"s_mov_b32 s59, s58\n\t"
"s_mov_b32 s60, 0x3ee38e39\n\t"
"s_mov_b32 s61, s60\n\t"
"s_mov_b32 s62, 0x40100000\n\t"
"s_mov_b32 s63, s62\n\t"
"s_mov_b32 s64, 0x3faaaaab\n\t"
"s_mov_b32 s65, s64\n\t"
"s_mov_b32 s66, 0x3f17b426\n\t"
"s_mov_b32 s67, s66\n\t"
"v_mfma_f32_16x16x32_bf16 v[184:187], a[120:123], a[176:179], 0\n\t"
"v_mfma_f32_16x16x32_bf16 v[184:187], a[128:131], a[180:183], v[184:187]\n\t"
"v_mfma_f32_16x16x32_bf16 v[184:187], a[136:139], v[204:207], v[184:187]\n\t"
"s_mov_b32 s20, 221184\n\t"
"buffer_load_dwordx4 v[236:239], v252, s[8:11], s20 offen offset:0 sc1\n\t"
"v_mfma_f32_16x16x32_bf16 v[26:29], a[0:3], a[204:207], 0\n\t"
"v_mfma_f32_16x16x32_bf16 v[26:29], a[8:11], a[188:191], v[26:29]\n\t"
"v_mfma_f32_16x16x32_bf16 v[26:29], a[16:19], a[192:195], v[26:29]\n\t"
"buffer_load_dwordx4 a[144:147], v252, s[8:11], s20 offen offset:1024 sc1\n\t"
"v_mfma_f32_16x16x32_bf16 v[58:61], a[24:27], a[204:207], 0\n\t"
"ds_read_b128 v[212:215], v244 offset:32256\n\t"
"v_mfma_f32_16x16x32_bf16 v[58:61], a[32:35], a[188:191], v[58:61]\n\t"
"v_mfma_f32_16x16x32_bf16 v[58:61], a[40:43], a[192:195], v[58:61]\n\t"
"buffer_load_dwordx4 a[152:155], v252, s[8:11], s20 offen offset:2048 sc1\n\t"
"v_mfma_f32_16x16x32_bf16 v[90:93], a[48:51], a[204:207], 0\n\t"
"v_mfma_f32_16x16x32_bf16 v[90:93], a[56:59], a[188:191], v[90:93]\n\t"
"v_mfma_f32_16x16x32_bf16 v[90:93], a[64:67], a[192:195], v[90:93]\n\t"
"s_mov_b32 s20, 233472\n\t"
"buffer_load_dwordx4 a[160:163], v252, s[8:11], s20 offen offset:0 sc1\n\t"
"v_mfma_f32_16x16x32_bf16 v[124:127], a[72:75], a[180:183], 0\n\t"
"v_mfma_f32_16x16x32_bf16 v[124:127], a[80:83], v[204:207], v[124:127]\n\t"
"v_mfma_f32_16x16x32_bf16 v[124:127], a[88:91], v[196:199], v[124:127]\n\t"
"buffer_load_dwordx4 v[200:203], v252, s[8:11], s20 offen offset:1024 sc1\n\t"
"v_mfma_f32_16x16x32_bf16 v[156:159], a[96:99], a[180:183], 0\n\t"
"v_mfma_f32_16x16x32_bf16 v[156:159], a[104:107], v[204:207], v[156:159]\n\t"
"v_mfma_f32_16x16x32_bf16 v[156:159], a[112:115], v[196:199], v[156:159]\n\t"
"buffer_load_dwordx4 v[208:211], v252, s[8:11], s20 offen offset:2048 sc1\n\t"
"v_mfma_f32_16x16x32_bf16 v[188:191], a[120:123], a[180:183], 0\n\t"
"v_mfma_f32_16x16x32_bf16 v[188:191], a[128:131], v[204:207], v[188:191]\n\t"
"ds_read_b128 a[168:171], v244 offset:0\n\t"
"v_mfma_f32_16x16x32_bf16 v[188:191], a[136:139], v[196:199], v[188:191]\n\t"
"s_mov_b32 s20, 245760\n\t"
"buffer_load_dwordx4 v[216:219], v252, s[8:11], s20 offen offset:0 sc1\n\t"
"s_waitcnt lgkmcnt(2)\n\t"
"v_mfma_f32_16x16x32_bf16 v[30:33], a[0:3], a[188:191], 0\n\t"
"ds_read_b128 a[172:175], v244 offset:512\n\t"
"v_mfma_f32_16x16x32_bf16 v[30:33], a[8:11], a[192:195], v[30:33]\n\t"
"ds_read_b128 a[176:179], v244 offset:1024\n\t"
"v_mfma_f32_16x16x32_bf16 v[30:33], a[16:19], a[196:199], v[30:33]\n\t"
"buffer_load_dwordx4 a[0:3], v252, s[8:11], s20 offen offset:1024 sc1\n\t"
"v_mfma_f32_16x16x32_bf16 v[62:65], a[24:27], a[188:191], 0\n\t"
"ds_read_b128 a[180:183], v244 offset:1536\n\t"
"v_mfma_f32_16x16x32_bf16 v[62:65], a[32:35], a[192:195], v[62:65]\n\t"
"ds_read_b128 a[184:187], v244 offset:2048\n\t"
"v_mfma_f32_16x16x32_bf16 v[62:65], a[40:43], a[196:199], v[62:65]\n\t"
"buffer_load_dwordx4 a[8:11], v252, s[8:11], s20 offen offset:2048 sc1\n\t"
"v_mfma_f32_16x16x32_bf16 v[94:97], a[48:51], a[188:191], 0\n\t"
"v_mfma_f32_16x16x32_bf16 v[94:97], a[56:59], a[192:195], v[94:97]\n\t"
"ds_read_b128 a[188:191], v244 offset:2560\n\t"
"v_mfma_f32_16x16x32_bf16 v[94:97], a[64:67], a[196:199], v[94:97]\n\t"
"s_mov_b32 s20, 405504\n\t"
"buffer_load_dwordx4 a[16:19], v252, s[8:11], s20 offen offset:0 sc1\n\t"
"s_waitcnt lgkmcnt(6)\n\t"
"v_mfma_f32_16x16x32_bf16 v[128:131], a[72:75], v[204:207], 0\n\t"
"v_mfma_f32_16x16x32_bf16 v[128:131], a[80:83], v[196:199], v[128:131]\n\t"
"ds_read_b128 a[192:195], v244 offset:3072\n\t"
"v_mfma_f32_16x16x32_bf16 v[128:131], a[88:91], v[212:215], v[128:131]\n\t"
"buffer_load_dwordx4 a[24:27], v252, s[8:11], s20 offen offset:1024 sc1\n\t"
"v_mfma_f32_16x16x32_bf16 v[160:163], a[96:99], v[204:207], 0\n\t"
"v_mfma_f32_16x16x32_bf16 v[160:163], a[104:107], v[196:199], v[160:163]\n\t"
"ds_read_b128 a[196:199], v244 offset:3584\n\t"
"v_mfma_f32_16x16x32_bf16 v[160:163], a[112:115], v[212:215], v[160:163]\n\t"
"buffer_load_dwordx4 a[32:35], v252, s[8:11], s20 offen offset:2048 sc1\n\t"
"v_mfma_f32_16x16x32_bf16 v[192:195], a[120:123], v[204:207], 0\n\t"
"v_mfma_f32_16x16x32_bf16 v[192:195], a[128:131], v[196:199], v[192:195]\n\t"
"ds_read_b128 a[200:203], v244 offset:4096\n\t"
"v_mfma_f32_16x16x32_bf16 v[192:195], a[136:139], v[212:215], v[192:195]\n\t"
"s_mov_b32 s20, 417792\n\t"
"buffer_load_dwordx4 a[40:43], v252, s[8:11], s20 offen offset:0 sc1\n\t"
"Lst1_%=:\n\t"
"s_waitcnt vmcnt(10) lgkmcnt(6)\n\t"
"v_mfma_f32_16x16x32_bf16 v[2:5], v[236:239], a[168:171], v[2:5]\n\t"
"v_mfma_f32_16x16x32_bf16 v[2:5], a[144:147], a[172:175], v[2:5]\n\t"
"ds_read_b128 a[204:207], v244 offset:4608\n\t"
"v_mfma_f32_16x16x32_bf16 v[2:5], a[152:155], a[176:179], v[2:5]\n\t"
"buffer_load_dwordx4 a[48:51], v252, s[8:11], s20 offen offset:1024 sc1\n\t"
"s_waitcnt lgkmcnt(6)\n\t"
"v_mfma_f32_16x16x32_bf16 v[6:9], v[236:239], a[172:175], v[6:9]\n\t"
"v_mfma_f32_16x16x32_bf16 v[6:9], a[144:147], a[176:179], v[6:9]\n\t"
"v_mfma_f32_16x16x32_bf16 v[6:9], a[152:155], a[180:183], v[6:9]\n\t"
"buffer_load_dwordx4 a[56:59], v252, s[8:11], s20 offen offset:2048 sc1\n\t"
"s_waitcnt lgkmcnt(5)\n\t"
"v_mfma_f32_16x16x32_bf16 v[10:13], v[236:239], a[176:179], v[10:13]\n\t"
"v_mfma_f32_16x16x32_bf16 v[10:13], a[144:147], a[180:183], v[10:13]\n\t"
"v_mfma_f32_16x16x32_bf16 v[10:13], a[152:155], a[184:187], v[10:13]\n\t"
"s_mov_b32 s20, 430080\n\t"
"buffer_load_dwordx4 a[80:83], v252, s[8:11], s20 offen offset:0 sc1\n\t"
"s_waitcnt lgkmcnt(4)\n\t"
"v_mfma_f32_16x16x32_bf16 v[14:17], v[236:239], a[180:183], v[14:17]\n\t"
"v_mfma_f32_16x16x32_bf16 v[14:17], a[144:147], a[184:187], v[14:17]\n\t"
"v_mfma_f32_16x16x32_bf16 v[14:17], a[152:155], a[188:191], v[14:17]\n\t"
"buffer_load_dwordx4 a[88:91], v252, s[8:11], s20 offen offset:1024 sc1\n\t"
"s_waitcnt lgkmcnt(3)\n\t"
"v_mfma_f32_16x16x32_bf16 v[18:21], v[236:239], a[184:187], v[18:21]\n\t"
"v_xor_b32 v246, 0xc0, v246\n\t"
"v_mfma_f32_16x16x32_bf16 v[18:21], a[144:147], a[188:191], v[18:21]\n\t"
"v_mfma_f32_16x16x32_bf16 v[18:21], a[152:155], a[192:195], v[18:21]\n\t"
"buffer_load_dwordx4 a[96:99], v252, s[8:11], s20 offen offset:2048 sc1\n\t"
"s_waitcnt lgkmcnt(2)\n\t"
"v_mfma_f32_16x16x32_bf16 v[22:25], v[236:239], a[188:191], v[22:25]\n\t"
"v_mfma_f32_16x16x32_bf16 v[22:25], a[144:147], a[192:195], v[22:25]\n\t"
"v_mfma_f32_16x16x32_bf16 v[22:25], a[152:155], a[196:199], v[22:25]\n\t"
"s_mov_b32 s20, 110592\n\t"
"buffer_load_dwordx4 a[136:139], v252, s[8:11], s20 offen offset:0 sc1\n\t"
"s_waitcnt lgkmcnt(1)\n\t"
"v_mfma_f32_16x16x32_bf16 v[26:29], v[236:239], a[192:195], v[26:29]\n\t"
"v_mfma_f32_16x16x32_bf16 v[26:29], a[144:147], a[196:199], v[26:29]\n\t"
"v_mfma_f32_16x16x32_bf16 v[26:29], a[152:155], a[200:203], v[26:29]\n\t"
"buffer_load_dwordx4 a[4:7], v252, s[8:11], s20 offen offset:1024 sc1\n\t"
"s_waitcnt lgkmcnt(0)\n\t"
"v_mfma_f32_16x16x32_bf16 v[30:33], v[236:239], a[196:199], v[30:33]\n\t"
"v_mfma_f32_16x16x32_bf16 v[30:33], a[144:147], a[200:203], v[30:33]\n\t"
"v_mfma_f32_16x16x32_bf16 v[30:33], a[152:155], a[204:207], v[30:33]\n\t"
"buffer_load_dwordx4 a[12:15], v252, s[8:11], s20 offen offset:2048 sc1\n\t"
"s_waitcnt vmcnt(15)\n\t"
"v_mfma_f32_16x16x32_bf16 v[34:37], a[160:163], a[168:171], v[34:37]\n\t"
"v_mfma_f32_16x16x32_bf16 v[34:37], v[200:203], a[172:175], v[34:37]\n\t"
"v_mfma_f32_16x16x32_bf16 v[34:37], v[208:211], a[176:179], v[34:37]\n\t"
"s_mov_b32 s20, 122880\n\t"
"buffer_load_dwordx4 a[20:23], v252, s[8:11], s20 offen offset:0 sc1\n\t"
"v_mfma_f32_16x16x32_bf16 v[38:41], a[160:163], a[172:175], v[38:41]\n\t"
"ds_read_b64_tr_b16 v[224:225], v246 offset:0\n\t"
"v_mfma_f32_16x16x32_bf16 v[38:41], v[200:203], a[176:179], v[38:41]\n\t"
"ds_read_b64_tr_b16 v[226:227], v246 offset:3072\n\t"
"v_mfma_f32_16x16x32_bf16 v[38:41], v[208:211], a[180:183], v[38:41]\n\t"
"buffer_load_dwordx4 a[28:31], v252, s[8:11], s20 offen offset:1024 sc1\n\t"
"v_mfma_f32_16x16x32_bf16 v[42:45], a[160:163], a[176:179], v[42:45]\n\t"
"v_mfma_f32_16x16x32_bf16 v[42:45], v[200:203], a[180:183], v[42:45]\n\t"
"v_mfma_f32_16x16x32_bf16 v[42:45], v[208:211], a[184:187], v[42:45]\n\t"
"buffer_load_dwordx4 a[36:39], v252, s[8:11], s20 offen offset:2048 sc1\n\t"
"v_mfma_f32_16x16x32_bf16 v[46:49], a[160:163], a[180:183], v[46:49]\n\t"
"ds_read_b64_tr_b16 v[228:229], v246 offset:6144\n\t"
"v_mfma_f32_16x16x32_bf16 v[46:49], v[200:203], a[184:187], v[46:49]\n\t"
"ds_read_b64_tr_b16 v[230:231], v246 offset:9216\n\t"
"v_mfma_f32_16x16x32_bf16 v[46:49], v[208:211], a[188:191], v[46:49]\n\t"
"s_mov_b32 s20, 135168\n\t"
"buffer_load_dwordx4 a[76:79], v252, s[8:11], s20 offen offset:0 sc1\n\t"
"v_mfma_f32_16x16x32_bf16 v[50:53], a[160:163], a[184:187], v[50:53]\n\t"
"v_mfma_f32_16x16x32_bf16 v[50:53], v[200:203], a[188:191], v[50:53]\n\t"
"v_mfma_f32_16x16x32_bf16 v[50:53], v[208:211], a[192:195], v[50:53]\n\t"
"buffer_load_dwordx4 a[84:87], v252, s[8:11], s20 offen offset:1024 sc1\n\t"
"v_mfma_f32_16x16x32_bf16 v[54:57], a[160:163], a[188:191], v[54:57]\n\t"
"v_mfma_f32_16x16x32_bf16 v[54:57], v[200:203], a[192:195], v[54:57]\n\t"
"v_mfma_f32_16x16x32_bf16 v[54:57], v[208:211], a[196:199], v[54:57]\n\t"
"buffer_load_dwordx4 a[92:95], v252, s[8:11], s20 offen offset:2048 sc1\n\t"
"s_waitcnt lgkmcnt(2)\n\t"
"v_mfma_f32_16x16x32_bf16 v[224:227], v[224:227], v[220:223], 0\n\t"
"s_mov_b32 s20, 147456\n\t"
"buffer_load_dwordx4 a[148:151], v252, s[8:11], s20 offen offset:0 sc1\n\t"
"v_mfma_f32_16x16x32_bf16 v[58:61], a[160:163], a[192:195], v[58:61]\n\t"
"ds_read_b64_tr_b16 v[232:233], v246 offset:12288\n\t"
"v_mfma_f32_16x16x32_bf16 v[58:61], v[200:203], a[196:199], v[58:61]\n\t"
"v_mfma_f32_16x16x32_bf16 v[58:61], v[208:211], a[200:203], v[58:61]\n\t"
"ds_read_b64_tr_b16 v[234:235], v246 offset:15360\n\t"
"v_mfma_f32_16x16x32_bf16 v[62:65], a[160:163], a[196:199], v[62:65]\n\t"
"v_mfma_f32_16x16x32_bf16 v[62:65], v[200:203], a[200:203], v[62:65]\n\t"
"v_cvt_pk_bf16_f32 v224, v224, v225\n\t"
"v_cvt_pk_bf16_f32 v225, v226, v227\n\t"
"v_mfma_f32_16x16x32_bf16 v[62:65], v[208:211], a[204:207], v[62:65]\n\t"
"s_waitcnt lgkmcnt(2)\n\t"
"v_mfma_f32_16x16x32_bf16 v[228:231], v[228:231], v[220:223], 0\n\t"
"buffer_load_dwordx4 a[156:159], v252, s[8:11], s20 offen offset:1024 sc1\n\t"
"s_waitcnt vmcnt(20)\n\t"
"v_mfma_f32_16x16x32_bf16 v[66:69], v[216:219], a[168:171], v[66:69]\n\t"
"ds_read_b64_tr_b16 v[236:237], v246 offset:19968\n\t"
"v_mfma_f32_16x16x32_bf16 v[66:69], a[0:3], a[172:175], v[66:69]\n\t"
"ds_read_b64_tr_b16 v[238:239], v246 offset:23040\n\t"
"v_mfma_f32_16x16x32_bf16 v[66:69], a[8:11], a[176:179], v[66:69]\n\t"
"buffer_load_dwordx4 a[164:167], v252, s[8:11], s20 offen offset:2048 sc1\n\t"
"v_mfma_f32_16x16x32_bf16 v[70:73], v[216:219], a[172:175], v[70:73]\n\t"
"ds_read_b128 a[168:171], v244 offset:9216\n\t"
"v_mfma_f32_16x16x32_bf16 v[70:73], a[0:3], a[176:179], v[70:73]\n\t"
"v_cvt_pk_bf16_f32 v228, v228, v229\n\t"
"v_mfma_f32_16x16x32_bf16 v[70:73], a[8:11], a[180:183], v[70:73]\n\t"
"v_cvt_pk_bf16_f32 v229, v230, v231\n\t"
"ds_read_b128 a[172:175], v244 offset:9728\n\t"
"v_mfma_f32_16x16x32_bf16 v[74:77], v[216:219], a[176:179], v[74:77]\n\t"
"ds_read_b128 a[176:179], v244 offset:10240\n\t"
"v_mfma_f32_16x16x32_bf16 v[74:77], a[0:3], a[180:183], v[74:77]\n\t"
"v_mfma_f32_16x16x32_bf16 v[74:77], a[8:11], a[184:187], v[74:77]\n\t"
"s_mov_b32 s20, 159744\n\t"
"buffer_load_dwordx4 v[240:243], v252, s[8:11], s20 offen offset:0 sc1\n\t"
"s_waitcnt lgkmcnt(5)\n\t"
"v_mfma_f32_16x16x32_bf16 v[232:235], v[232:235], v[220:223], 0\n\t"
"buffer_load_dwordx4 a[208:211], v252, s[8:11], s20 offen offset:1024 sc1\n\t"
"v_mfma_f32_16x16x32_bf16 v[78:81], v[216:219], a[180:183], v[78:81]\n\t"
"ds_read_b128 a[180:183], v244 offset:10752\n\t"
"v_mfma_f32_16x16x32_bf16 v[78:81], a[0:3], a[184:187], v[78:81]\n\t"
"v_mfma_f32_16x16x32_bf16 v[78:81], a[8:11], a[188:191], v[78:81]\n\t"
"buffer_load_dwordx4 v[248:251], v252, s[8:11], s20 offen offset:2048 sc1\n\t"
"v_mfma_f32_16x16x32_bf16 v[82:85], v[216:219], a[184:187], v[82:85]\n\t"
"v_mfma_f32_16x16x32_bf16 v[82:85], a[0:3], a[188:191], v[82:85]\n\t"
"v_cvt_pk_bf16_f32 v232, v232, v233\n\t"
"v_cvt_pk_bf16_f32 v233, v234, v235\n\t"
"v_mfma_f32_16x16x32_bf16 v[82:85], a[8:11], a[192:195], v[82:85]\n\t"
"ds_read_b128 a[184:187], v244 offset:11264\n\t"
"s_waitcnt lgkmcnt(5)\n\t"
"v_mfma_f32_16x16x32_bf16 v[236:239], v[236:239], v[220:223], 0\n\t"
"v_mfma_f32_16x16x32_bf16 v[86:89], v[216:219], a[188:191], v[86:89]\n\t"
"ds_read_b128 a[188:191], v244 offset:11776\n\t"
"v_mfma_f32_16x16x32_bf16 v[86:89], a[0:3], a[192:195], v[86:89]\n\t"
"v_mfma_f32_16x16x32_bf16 v[86:89], a[8:11], a[196:199], v[86:89]\n\t"
"s_mov_b32 s20, 172032\n\t"
"buffer_load_dwordx4 a[212:215], v252, s[8:11], s20 offen offset:0 sc1\n\t"
"v_mfma_f32_16x16x32_bf16 v[90:93], v[216:219], a[192:195], v[90:93]\n\t"
"ds_read_b128 a[192:195], v244 offset:12288\n\t"
"v_mfma_f32_16x16x32_bf16 v[90:93], a[0:3], a[196:199], v[90:93]\n\t"
"v_cvt_pk_bf16_f32 v236, v236, v237\n\t"
"v_cvt_pk_bf16_f32 v237, v238, v239\n\t"
"v_mfma_f32_16x16x32_bf16 v[90:93], a[8:11], a[200:203], v[90:93]\n\t"
"buffer_load_dwordx4 a[216:219], v252, s[8:11], s20 offen offset:1024 sc1\n\t"
"v_mfma_f32_16x16x32_bf16 v[94:97], v[216:219], a[196:199], v[94:97]\n\t"
"ds_read_b128 a[196:199], v244 offset:12800\n\t"
"v_mfma_f32_16x16x32_bf16 v[94:97], a[0:3], a[200:203], v[94:97]\n\t"
"ds_read_b128 a[200:203], v244 offset:13312\n\t"
"v_mfma_f32_16x16x32_bf16 v[94:97], a[8:11], a[204:207], v[94:97]\n\t"
"ds_read_b128 a[204:207], v244 offset:13824\n\t"
"s_waitcnt vmcnt(23) lgkmcnt(7)\n\t"
"v_mfma_f32_16x16x32_bf16 v[100:103], a[16:19], a[168:171], v[100:103]\n\t"
"v_mfma_f32_16x16x32_bf16 v[100:103], a[24:27], a[172:175], v[100:103]\n\t"
"v_mfma_f32_16x16x32_bf16 v[100:103], a[32:35], a[176:179], v[100:103]\n\t"
"buffer_load_dwordx4 a[220:223], v252, s[8:11], s20 offen offset:2048 sc1\n\t"
"s_waitcnt lgkmcnt(6)\n\t"
"v_mfma_f32_16x16x32_bf16 v[104:107], a[16:19], a[172:175], v[104:107]\n\t"
"v_mfma_f32_16x16x32_bf16 v[104:107], a[24:27], a[176:179], v[104:107]\n\t"
"v_mfma_f32_16x16x32_bf16 v[104:107], a[32:35], a[180:183], v[104:107]\n\t"
"s_mov_b32 s20, 36864\n\t"
"buffer_load_dwordx4 a[224:227], v252, s[8:11], s20 offen offset:0 sc1\n\t"
"s_waitcnt lgkmcnt(0)\n\t"
"s_barrier\n\t"
"v_mfma_f32_16x16x32_bf16 v[108:111], a[16:19], a[176:179], v[108:111]\n\t"
"v_fma_f32 v196, -v2, s56, -v100\n\t"
"v_fma_f32 v197, -v3, s56, -v101\n\t"
"v_mfma_f32_16x16x32_bf16 v[108:111], a[24:27], a[180:183], v[108:111]\n\t"
"v_fma_f32 v198, -v4, s56, -v102\n\t"
"v_fma_f32 v199, -v5, s56, -v103\n\t"
"v_mfma_f32_16x16x32_bf16 v[108:111], a[32:35], a[184:187], v[108:111]\n\t"
"buffer_load_dwordx4 a[228:231], v252, s[8:11], s20 offen offset:1024 sc1\n\t"
"ds_write_b64 v245, v[224:225] offset:0\n\t"
"v_mfma_f32_16x16x32_bf16 v[112:115], a[16:19], a[180:183], v[112:115]\n\t"
"ds_read_b64_tr_b16 v[224:225], v246 offset:26112\n\t"
"v_fma_f32 v204, -v6, s56, -v104\n\t"
"v_mfma_f32_16x16x32_bf16 v[112:115], a[24:27], a[184:187], v[112:115]\n\t"
"ds_read_b64_tr_b16 v[226:227], v246 offset:29184\n\t"
"v_fma_f32 v205, -v7, s56, -v105\n\t"
"v_mfma_f32_16x16x32_bf16 v[112:115], a[32:35], a[188:191], v[112:115]\n\t"
"ds_write_b64 v245, v[228:229] offset:512\n\t"
"buffer_load_dwordx4 a[232:235], v252, s[8:11], s20 offen offset:2048 sc1\n\t"
"v_mfma_f32_16x16x32_bf16 v[116:119], a[16:19], a[184:187], v[116:119]\n\t"
"ds_read_b64_tr_b16 v[228:229], v246 offset:32256\n\t"
"v_fma_f32 v206, -v8, s56, -v106\n\t"
"v_mfma_f32_16x16x32_bf16 v[116:119], a[24:27], a[188:191], v[116:119]\n\t"
"ds_read_b64_tr_b16 v[230:231], v246 offset:35328\n\t"
"v_fma_f32 v207, -v9, s56, -v107\n\t"
"v_mfma_f32_16x16x32_bf16 v[116:119], a[32:35], a[192:195], v[116:119]\n\t"
"ds_write_b64 v245, v[232:233] offset:1024\n\t"
"s_mov_b32 s20, 49152\n\t"
"buffer_load_dwordx4 a[236:239], v252, s[8:11], s20 offen offset:0 sc1\n\t"
"ds_write_b64 v245, v[236:237] offset:1536\n\t"
"v_mfma_f32_16x16x32_bf16 v[120:123], a[16:19], a[188:191], v[120:123]\n\t"
"v_mfma_f32_16x16x32_bf16 v[120:123], a[24:27], a[192:195], v[120:123]\n\t"
"ds_read_b64_tr_b16 v[232:233], v246 offset:39936\n\t"
"v_fma_f32 v212, -v10, s56, -v108\n\t"
"v_mfma_f32_16x16x32_bf16 v[120:123], a[32:35], a[196:199], v[120:123]\n\t"
"buffer_load_dwordx4 a[240:243], v252, s[8:11], s20 offen offset:1024 sc1\n\t"
"s_waitcnt lgkmcnt(6)\n\t"
"v_mfma_f32_16x16x32_bf16 v[224:227], v[224:227], v[220:223], 0\n\t"
"ds_read_b64_tr_b16 v[234:235], v246 offset:43008\n\t"
"v_mfma_f32_16x16x32_bf16 v[124:127], a[16:19], a[192:195], v[124:127]\n\t"
"ds_read_b64_tr_b16 v[236:237], v246 offset:46080\n\t"
"v_fma_f32 v213, -v11, s56, -v109\n\t"
"v_mfma_f32_16x16x32_bf16 v[124:127], a[24:27], a[196:199], v[124:127]\n\t"
"ds_read_b64_tr_b16 v[238:239], v246 offset:49152\n\t"
"v_mfma_f32_16x16x32_bf16 v[124:127], a[32:35], a[200:203], v[124:127]\n\t"
"buffer_load_dwordx4 a[244:247], v252, s[8:11], s20 offen offset:2048 sc1\n\t"
"s_waitcnt lgkmcnt(6)\n\t"
"v_mfma_f32_16x16x32_bf16 v[228:231], v[228:231], v[220:223], 0\n\t"
"s_mov_b32 s20, 61440\n\t"
"buffer_load_dwordx4 a[248:251], v252, s[8:11], s20 offen offset:0 sc1\n\t"
"v_mfma_f32_16x16x32_bf16 v[128:131], a[16:19], a[196:199], v[128:131]\n\t"
"v_cvt_pk_bf16_f32 v224, v224, v225\n\t"
"v_cvt_pk_bf16_f32 v225, v226, v227\n\t"
"v_mfma_f32_16x16x32_bf16 v[128:131], a[24:27], a[200:203], v[128:131]\n\t"
"v_fma_f32 v214, -v12, s56, -v110\n\t"
"v_fma_f32 v215, -v13, s56, -v111\n\t"
"v_mfma_f32_16x16x32_bf16 v[128:131], a[32:35], a[204:207], v[128:131]\n\t"
"buffer_load_dwordx4 a[252:255], v252, s[8:11], s20 offen offset:1024 sc1\n\t"
"s_waitcnt lgkmcnt(2)\n\t"
"v_mfma_f32_16x16x32_bf16 v[232:235], v[232:235], v[220:223], 0\n\t"
"ds_write_b64 v245, v[224:225] offset:2048\n\t"
"s_waitcnt vmcnt(29)\n\t"
"v_mfma_f32_16x16x32_bf16 v[132:135], a[40:43], a[168:171], v[132:135]\n\t"
"v_cvt_pk_bf16_f32 v228, v228, v229\n\t"
"v_cvt_pk_bf16_f32 v229, v230, v231\n\t"
"v_mfma_f32_16x16x32_bf16 v[132:135], a[48:51], a[172:175], v[132:135]\n\t"
"ds_read_b64_tr_b16 v[224:225], v246 offset:52224\n\t"
"v_fma_f32 v98, -v14, s56, -v112\n\t"
"v_mfma_f32_16x16x32_bf16 v[132:135], a[56:59], a[176:179], v[132:135]\n\t"
"ds_read_b64_tr_b16 v[226:227], v246 offset:55296\n\t"
"v_fma_f32 v99, -v15, s56, -v113\n\t"
"s_waitcnt lgkmcnt(3)\n\t"
"v_mfma_f32_16x16x32_bf16 v[236:239], v[236:239], v[220:223], 0\n\t"
"ds_write_b64 v245, v[228:229] offset:2560\n\t"
"v_mfma_f32_16x16x32_bf16 v[136:139], a[40:43], a[172:175], v[136:139]\n\t"
"v_cvt_pk_bf16_f32 v232, v232, v233\n\t"
"v_cvt_pk_bf16_f32 v233, v234, v235\n\t"
"v_mfma_f32_16x16x32_bf16 v[136:139], a[48:51], a[176:179], v[136:139]\n\t"
"v_accvgpr_write_b32 a0, v98\n\t"
"v_fma_f32 v98, -v16, s56, -v114\n\t"
"v_mfma_f32_16x16x32_bf16 v[136:139], a[56:59], a[180:183], v[136:139]\n\t"
"ds_write_b64 v245, v[232:233] offset:3072\n\t"
"v_mfma_f32_16x16x32_bf16 v[140:143], a[40:43], a[176:179], v[140:143]\n\t"
"v_accvgpr_write_b32 a1, v99\n\t"
"v_fma_f32 v99, -v17, s56, -v115\n\t"
"v_mfma_f32_16x16x32_bf16 v[140:143], a[48:51], a[180:183], v[140:143]\n\t"
"v_cvt_pk_bf16_f32 v236, v236, v237\n\t"
"v_cvt_pk_bf16_f32 v237, v238, v239\n\t"
"v_mfma_f32_16x16x32_bf16 v[140:143], a[56:59], a[184:187], v[140:143]\n\t"
"ds_write_b64 v245, v[236:237] offset:3584\n\t"
"v_accvgpr_write_b32 a2, v98\n\t"
"v_accvgpr_write_b32 a3, v99\n\t"
"v_mfma_f32_16x16x32_bf16 v[144:147], a[40:43], a[180:183], v[144:147]\n\t"
"v_fma_f32 v98, -v18, s56, -v116\n\t"
"v_fma_f32 v99, -v19, s56, -v117\n\t"
"v_mfma_f32_16x16x32_bf16 v[144:147], a[48:51], a[184:187], v[144:147]\n\t"
"v_accvgpr_write_b32 a8, v98\n\t"
"v_fma_f32 v98, -v20, s56, -v118\n\t"
"v_mfma_f32_16x16x32_bf16 v[144:147], a[56:59], a[188:191], v[144:147]\n\t"
"v_accvgpr_write_b32 a9, v99\n\t"
"v_fma_f32 v99, -v21, s56, -v119\n\t"
"s_waitcnt lgkmcnt(3)\n\t"
"v_mfma_f32_16x16x32_bf16 v[224:227], v[224:227], v[220:223], 0\n\t"
"v_accvgpr_write_b32 a10, v98\n\t"
"v_accvgpr_write_b32 a11, v99\n\t"
"v_mfma_f32_16x16x32_bf16 v[148:151], a[40:43], a[184:187], v[148:151]\n\t"
"v_fma_f32 v98, -v22, s56, -v120\n\t"
"v_fma_f32 v99, -v23, s56, -v121\n\t"
"v_mfma_f32_16x16x32_bf16 v[148:151], a[48:51], a[188:191], v[148:151]\n\t"
"v_accvgpr_write_b32 a16, v98\n\t"
"v_fma_f32 v98, -v24, s56, -v122\n\t"
"v_mfma_f32_16x16x32_bf16 v[148:151], a[56:59], a[192:195], v[148:151]\n\t"
"buffer_load_dwordx4 v[236:239], v252, s[8:11], s20 offen offset:2048 sc1\n\t"
"v_mfma_f32_16x16x32_bf16 v[152:155], a[40:43], a[188:191], v[152:155]\n\t"
"v_accvgpr_write_b32 a17, v99\n\t"
"v_fma_f32 v99, -v25, s56, -v123\n\t"
"v_mfma_f32_16x16x32_bf16 v[152:155], a[48:51], a[192:195], v[152:155]\n\t"
"v_cvt_pk_bf16_f32 v224, v224, v225\n\t"
"v_cvt_pk_bf16_f32 v225, v226, v227\n\t"
"v_mfma_f32_16x16x32_bf16 v[152:155], a[56:59], a[196:199], v[152:155]\n\t"
"ds_write_b64 v245, v[224:225] offset:4096\n\t"
"v_accvgpr_write_b32 a18, v98\n\t"
"v_accvgpr_write_b32 a19, v99\n\t"
"v_mfma_f32_16x16x32_bf16 v[156:159], a[40:43], a[192:195], v[156:159]\n\t"
"v_fma_f32 v98, -v26, s56, -v124\n\t"
"v_fma_f32 v99, -v27, s56, -v125\n\t"
"v_mfma_f32_16x16x32_bf16 v[156:159], a[48:51], a[196:199], v[156:159]\n\t"
"v_accvgpr_write_b32 a24, v98\n\t"
"v_fma_f32 v98, -v28, s56, -v126\n\t"
"v_mfma_f32_16x16x32_bf16 v[156:159], a[56:59], a[200:203], v[156:159]\n\t"
"v_accvgpr_write_b32 a25, v99\n\t"
"v_fma_f32 v99, -v29, s56, -v127\n\t"
"v_accvgpr_write_b32 a26, v98\n\t"
"v_accvgpr_write_b32 a27, v99\n\t"
"v_mfma_f32_16x16x32_bf16 v[160:163], a[40:43], a[196:199], v[160:163]\n\t"
"v_fma_f32 v98, -v30, s56, -v128\n\t"
"v_fma_f32 v99, -v31, s56, -v129\n\t"
"v_mfma_f32_16x16x32_bf16 v[160:163], a[48:51], a[200:203], v[160:163]\n\t"
"v_accvgpr_write_b32 a32, v98\n\t"
"v_fma_f32 v98, -v32, s56, -v130\n\t"
"v_mfma_f32_16x16x32_bf16 v[160:163], a[56:59], a[204:207], v[160:163]\n\t"
"v_accvgpr_write_b32 a33, v99\n\t"
"v_fma_f32 v99, -v33, s56, -v131\n\t"
"v_accvgpr_write_b32 a34, v98\n\t"
"v_accvgpr_write_b32 a35, v99\n\t"
"s_waitcnt vmcnt(27)\n\t"
"v_mfma_f32_16x16x32_bf16 v[164:167], a[80:83], a[168:171], v[164:167]\n\t"
"v_fma_f32 v98, -v34, s56, -v132\n\t"
"v_fma_f32 v99, -v35, s56, -v133\n\t"
"v_mfma_f32_16x16x32_bf16 v[164:167], a[88:91], a[172:175], v[164:167]\n\t"
"v_accvgpr_write_b32 a40, v98\n\t"
"v_fma_f32 v98, -v36, s56, -v134\n\t"
"v_mfma_f32_16x16x32_bf16 v[164:167], a[96:99], a[176:179], v[164:167]\n\t"
"v_accvgpr_write_b32 a41, v99\n\t"
"v_fma_f32 v99, -v37, s56, -v135\n\t"
"v_accvgpr_write_b32 a42, v98\n\t"
"v_accvgpr_write_b32 a43, v99\n\t"
"s_waitcnt lgkmcnt(0)\n\t"
"s_barrier\n\t"
"v_mfma_f32_16x16x32_bf16 v[168:171], a[80:83], a[172:175], v[168:171]\n\t"
"ds_read_b128 a[168:171], v244 offset:0\n\t"
"v_fma_f32 v98, -v38, s56, -v136\n\t"
"v_mfma_f32_16x16x32_bf16 v[168:171], a[88:91], a[176:179], v[168:171]\n\t"
"ds_read_b128 a[172:175], v244 offset:512\n\t"
"v_fma_f32 v99, -v39, s56, -v137\n\t"
"v_mfma_f32_16x16x32_bf16 v[168:171], a[96:99], a[180:183], v[168:171]\n\t"
"v_accvgpr_write_b32 a48, v98\n\t"
"v_fma_f32 v98, -v40, s56, -v138\n\t"
"v_accvgpr_write_b32 a49, v99\n\t"
"v_fma_f32 v99, -v41, s56, -v139\n\t"
"v_mfma_f32_16x16x32_bf16 v[172:175], a[80:83], a[176:179], v[172:175]\n\t"
"ds_read_b128 a[176:179], v244 offset:1024\n\t"
"v_accvgpr_write_b32 a50, v98\n\t"
"v_mfma_f32_16x16x32_bf16 v[172:175], a[88:91], a[180:183], v[172:175]\n\t"
"v_accvgpr_write_b32 a51, v99\n\t"
"v_fma_f32 v98, -v42, s56, -v140\n\t"
"v_mfma_f32_16x16x32_bf16 v[172:175], a[96:99], a[184:187], v[172:175]\n\t"
"v_fma_f32 v99, -v43, s56, -v141\n\t"
"v_accvgpr_write_b32 a56, v98\n\t"
"v_fma_f32 v98, -v44, s56, -v142\n\t"
"v_accvgpr_write_b32 a57, v99\n\t"
"v_mfma_f32_16x16x32_bf16 v[176:179], a[80:83], a[180:183], v[176:179]\n\t"
"ds_read_b128 a[180:183], v244 offset:1536\n\t"
"v_fma_f32 v99, -v45, s56, -v143\n\t"
"v_mfma_f32_16x16x32_bf16 v[176:179], a[88:91], a[184:187], v[176:179]\n\t"
"v_accvgpr_write_b32 a58, v98\n\t"
"v_accvgpr_write_b32 a59, v99\n\t"
"v_mfma_f32_16x16x32_bf16 v[176:179], a[96:99], a[188:191], v[176:179]\n\t"
"v_fma_f32 v98, -v46, s56, -v144\n\t"
"v_fma_f32 v99, -v47, s56, -v145\n\t"
"v_accvgpr_write_b32 a64, v98\n\t"
"v_fma_f32 v98, -v48, s56, -v146\n\t"
"v_mfma_f32_16x16x32_bf16 v[180:183], a[80:83], a[184:187], v[180:183]\n\t"
"ds_read_b128 a[184:187], v244 offset:2048\n\t"
"v_accvgpr_write_b32 a65, v99\n\t"
"v_mfma_f32_16x16x32_bf16 v[180:183], a[88:91], a[188:191], v[180:183]\n\t"
"v_fma_f32 v99, -v49, s56, -v147\n\t"
"v_accvgpr_write_b32 a66, v98\n\t"
"v_mfma_f32_16x16x32_bf16 v[180:183], a[96:99], a[192:195], v[180:183]\n\t"
"v_accvgpr_write_b32 a67, v99\n\t"
"v_fma_f32 v98, -v50, s56, -v148\n\t"
"v_fma_f32 v99, -v51, s56, -v149\n\t"
"v_accvgpr_write_b32 a72, v98\n\t"
"v_mfma_f32_16x16x32_bf16 v[184:187], a[80:83], a[188:191], v[184:187]\n\t"
"ds_read_b128 a[188:191], v244 offset:2560\n\t"
"v_fma_f32 v98, -v52, s56, -v150\n\t"
"v_mfma_f32_16x16x32_bf16 v[184:187], a[88:91], a[192:195], v[184:187]\n\t"
"v_accvgpr_write_b32 a73, v99\n\t"
"v_fma_f32 v99, -v53, s56, -v151\n\t"
"v_mfma_f32_16x16x32_bf16 v[184:187], a[96:99], a[196:199], v[184:187]\n\t"
"v_accvgpr_write_b32 a74, v98\n\t"
"v_accvgpr_write_b32 a75, v99\n\t"
"v_fma_f32 v200, v100, s64, -v2\n\t"
"v_fma_f32 v201, v101, s64, -v3\n\t"
"v_mfma_f32_16x16x32_bf16 v[188:191], a[80:83], a[192:195], v[188:191]\n\t"
"ds_read_b128 a[192:195], v244 offset:3072\n\t"
"v_fma_f32 v202, v102, s64, -v4\n\t"
"v_mfma_f32_16x16x32_bf16 v[188:191], a[88:91], a[196:199], v[188:191]\n\t"
"v_fma_f32 v203, v103, s64, -v5\n\t"
"v_fma_f32 v208, v104, s64, -v6\n\t"
"v_mfma_f32_16x16x32_bf16 v[188:191], a[96:99], a[200:203], v[188:191]\n\t"
"v_fma_f32 v209, v105, s64, -v7\n\t"
"v_fma_f32 v210, v106, s64, -v8\n\t"
"v_fma_f32 v211, v107, s64, -v9\n\t"
"v_fma_f32 v216, v108, s64, -v10\n\t"
"v_mfma_f32_16x16x32_bf16 v[192:195], a[80:83], a[196:199], v[192:195]\n\t"
"ds_read_b128 a[196:199], v244 offset:3584\n\t"
"v_fma_f32 v98, -v54, s56, -v152\n\t"
"v_mfma_f32_16x16x32_bf16 v[192:195], a[88:91], a[200:203], v[192:195]\n\t"
"ds_read_b128 a[200:203], v244 offset:4096\n\t"
"v_fma_f32 v99, -v55, s56, -v153\n\t"
"v_mfma_f32_16x16x32_bf16 v[192:195], a[96:99], a[204:207], v[192:195]\n\t"
"ds_read_b128 a[204:207], v244 offset:4608\n\t"
"v_accvgpr_write_b32 a80, v98\n\t"
"v_fma_f32 v98, -v56, s56, -v154\n\t"
"v_accvgpr_write_b32 a81, v99\n\t"
"Lst2_%=:\n\t"
"s_waitcnt vmcnt(24) lgkmcnt(7)\n\t"
"v_mfma_f32_16x16x32_bf16 v[196:199], a[136:139], a[168:171], v[196:199]\n\t"
"v_xor_b32 v246, 0xc0, v246\n\t"
"v_fma_f32 v99, -v57, s56, -v155\n\t"
"v_mfma_f32_16x16x32_bf16 v[196:199], a[4:7], a[172:175], v[196:199]\n\t"
"v_accvgpr_write_b32 a82, v98\n\t"
"v_accvgpr_write_b32 a83, v99\n\t"
"v_mfma_f32_16x16x32_bf16 v[196:199], a[12:15], a[176:179], v[196:199]\n\t"
"v_fma_f32 v98, -v58, s56, -v156\n\t"
"v_fma_f32 v99, -v59, s56, -v157\n\t"
"v_accvgpr_write_b32 a88, v98\n\t"
"v_fma_f32 v98, -v60, s56, -v158\n\t"
"s_waitcnt lgkmcnt(6)\n\t"
"v_mfma_f32_16x16x32_bf16 v[204:207], a[136:139], a[172:175], v[204:207]\n\t"
"v_accvgpr_write_b32 a89, v99\n\t"
"v_fma_f32 v99, -v61, s56, -v159\n\t"
"v_mfma_f32_16x16x32_bf16 v[204:207], a[4:7], a[176:179], v[204:207]\n\t"
"v_accvgpr_write_b32 a90, v98\n\t"
"v_accvgpr_write_b32 a91, v99\n\t"
"v_mfma_f32_16x16x32_bf16 v[204:207], a[12:15], a[180:183], v[204:207]\n\t"
"v_fma_f32 v98, -v62, s56, -v160\n\t"
"v_fma_f32 v99, -v63, s56, -v161\n\t"
"v_accvgpr_write_b32 a96, v98\n\t"
"v_fma_f32 v98, -v64, s56, -v162\n\t"
"s_waitcnt lgkmcnt(5)\n\t"
"v_mfma_f32_16x16x32_bf16 v[212:215], a[136:139], a[176:179], v[212:215]\n\t"
"v_accvgpr_write_b32 a97, v99\n\t"
"v_fma_f32 v99, -v65, s56, -v163\n\t"
"v_mfma_f32_16x16x32_bf16 v[212:215], a[4:7], a[180:183], v[212:215]\n\t"
"v_accvgpr_write_b32 a98, v98\n\t"
"v_accvgpr_write_b32 a99, v99\n\t"
"v_mfma_f32_16x16x32_bf16 v[212:215], a[12:15], a[184:187], v[212:215]\n\t"
"v_fma_f32 v98, -v66, s56, -v164\n\t"
"v_fma_f32 v99, -v67, s56, -v165\n\t"
"v_accvgpr_write_b32 a104, v98\n\t"
"v_fma_f32 v98, -v68, s56, -v166\n\t"
"s_waitcnt lgkmcnt(4)\n\t"
"v_mfma_f32_16x16x32_bf16 a[0:3], a[136:139], a[180:183], a[0:3]\n\t"
"v_accvgpr_write_b32 a105, v99\n\t"
"v_fma_f32 v99, -v69, s56, -v167\n\t"
"v_mfma_f32_16x16x32_bf16 a[0:3], a[4:7], a[184:187], a[0:3]\n\t"
"v_accvgpr_write_b32 a106, v98\n\t"
"v_accvgpr_write_b32 a107, v99\n\t"
"v_mfma_f32_16x16x32_bf16 a[0:3], a[12:15], a[188:191], a[0:3]\n\t"
"v_fma_f32 v98, -v70, s56, -v168\n\t"
"v_fma_f32 v99, -v71, s56, -v169\n\t"
"v_accvgpr_write_b32 a112, v98\n\t"
"v_fma_f32 v98, -v72, s56, -v170\n\t"
"s_waitcnt lgkmcnt(3)\n\t"
"v_mfma_f32_16x16x32_bf16 a[8:11], a[136:139], a[184:187], a[8:11]\n\t"
"v_accvgpr_write_b32 a113, v99\n\t"
"v_fma_f32 v99, -v73, s56, -v171\n\t"
"v_mfma_f32_16x16x32_bf16 a[8:11], a[4:7], a[188:191], a[8:11]\n\t"
"v_accvgpr_write_b32 a114, v98\n\t"
"v_accvgpr_write_b32 a115, v99\n\t"
"v_mfma_f32_16x16x32_bf16 a[8:11], a[12:15], a[192:195], a[8:11]\n\t"
"ds_read_b64_tr_b16 v[228:229], v246 offset:0\n\t"
"ds_read_b64_tr_b16 v[230:231], v246 offset:3072\n\t"
"v_fma_f32 v98, -v74, s56, -v172\n\t"
"v_fma_f32 v99, -v75, s56, -v173\n\t"
"s_waitcnt lgkmcnt(4)\n\t"
"v_mfma_f32_16x16x32_bf16 a[16:19], a[136:139], a[188:191], a[16:19]\n\t"
"v_accvgpr_write_b32 a120, v98\n\t"
"v_fma_f32 v98, -v76, s56, -v174\n\t"
"v_mfma_f32_16x16x32_bf16 a[16:19], a[4:7], a[192:195], a[16:19]\n\t"
"v_accvgpr_write_b32 a121, v99\n\t"
"v_fma_f32 v99, -v77, s56, -v175\n\t"
"v_mfma_f32_16x16x32_bf16 a[16:19], a[12:15], a[196:199], a[16:19]\n\t"
"v_accvgpr_write_b32 a122, v98\n\t"
"v_accvgpr_write_b32 a123, v99\n\t"
"v_fma_f32 v98, -v78, s56, -v176\n\t"
"v_fma_f32 v99, -v79, s56, -v177\n\t"
"s_waitcnt lgkmcnt(3)\n\t"
"v_mfma_f32_16x16x32_bf16 a[24:27], a[136:139], a[192:195], a[24:27]\n\t"
"v_accvgpr_write_b32 a128, v98\n\t"
"v_fma_f32 v98, -v80, s56, -v178\n\t"
"v_mfma_f32_16x16x32_bf16 a[24:27], a[4:7], a[196:199], a[24:27]\n\t"
"v_accvgpr_write_b32 a129, v99\n\t"
"v_fma_f32 v99, -v81, s56, -v179\n\t"
"v_mfma_f32_16x16x32_bf16 a[24:27], a[12:15], a[200:203], a[24:27]\n\t"
"v_accvgpr_write_b32 a130, v98\n\t"
"v_accvgpr_write_b32 a131, v99\n\t"
"v_fma_f32 v217, v109, s64, -v11\n\t"
"v_fma_f32 v218, v110, s64, -v12\n\t"
"s_waitcnt lgkmcnt(2)\n\t"
"v_mfma_f32_16x16x32_bf16 a[32:35], a[136:139], a[196:199], a[32:35]\n\t"
"ds_read_b64_tr_b16 v[232:233], v246 offset:6144\n\t"
"v_fma_f32 v98, -v82, s56, -v180\n\t"
"v_mfma_f32_16x16x32_bf16 a[32:35], a[4:7], a[200:203], a[32:35]\n\t"
"ds_read_b64_tr_b16 v[234:235], v246 offset:9216\n\t"
"v_fma_f32 v99, -v83, s56, -v181\n\t"
"v_mfma_f32_16x16x32_bf16 a[32:35], a[12:15], a[204:207], a[32:35]\n\t"
"v_accvgpr_write_b32 a136, v98\n\t"
"v_fma_f32 v98, -v84, s56, -v182\n\t"
"v_accvgpr_write_b32 a137, v99\n\t"
"v_fma_f32 v99, -v85, s56, -v183\n\t"
"s_waitcnt vmcnt(21)\n\t"
"v_mfma_f32_16x16x32_bf16 a[40:43], a[20:23], a[168:171], a[40:43]\n\t"
"v_accvgpr_write_b32 a138, v98\n\t"
"v_accvgpr_write_b32 a139, v99\n\t"
"v_mfma_f32_16x16x32_bf16 a[40:43], a[28:31], a[172:175], a[40:43]\n\t"
"v_fma_f32 v98, -v86, s56, -v184\n\t"
"v_fma_f32 v99, -v87, s56, -v185\n\t"
"v_mfma_f32_16x16x32_bf16 a[40:43], a[36:39], a[176:179], a[40:43]\n\t"
"v_accvgpr_write_b32 a144, v98\n\t"
"v_fma_f32 v98, -v88, s56, -v186\n\t"
"v_accvgpr_write_b32 a145, v99\n\t"
"v_fma_f32 v99, -v89, s56, -v187\n\t"
"v_mfma_f32_16x16x32_bf16 a[48:51], a[20:23], a[172:175], a[48:51]\n\t"
"v_accvgpr_write_b32 a146, v98\n\t"
"v_accvgpr_write_b32 a147, v99\n\t"
"v_mfma_f32_16x16x32_bf16 a[48:51], a[28:31], a[176:179], a[48:51]\n\t"
"v_fma_f32 v98, -v90, s56, -v188\n\t"
"v_fma_f32 v99, -v91, s56, -v189\n\t"
"v_mfma_f32_16x16x32_bf16 a[48:51], a[36:39], a[180:183], a[48:51]\n\t"
"v_accvgpr_write_b32 a152, v98\n\t"
"v_fma_f32 v98, -v92, s56, -v190\n\t"
"v_accvgpr_write_b32 a153, v99\n\t"
"v_fma_f32 v99, -v93, s56, -v191\n\t"
"s_waitcnt lgkmcnt(2)\n\t"
"v_mfma_f32_16x16x32_bf16 v[228:231], v[228:231], v[220:223], 0\n\t"
"v_accvgpr_write_b32 a154, v98\n\t"
"v_accvgpr_write_b32 a155, v99\n\t"
"v_fma_f32 v98, -v94, s56, -v192\n\t"
"v_fma_f32 v99, -v95, s56, -v193\n\t"
"v_mfma_f32_16x16x32_bf16 a[56:59], a[20:23], a[176:179], a[56:59]\n\t"
"ds_read_b64_tr_b16 v[224:225], v246 offset:12288\n\t"
"v_accvgpr_write_b32 a160, v98\n\t"
"v_mfma_f32_16x16x32_bf16 a[56:59], a[28:31], a[180:183], a[56:59]\n\t"
"ds_read_b64_tr_b16 v[226:227], v246 offset:15360\n\t"
"v_fma_f32 v98, -v96, s56, -v194\n\t"
"v_mfma_f32_16x16x32_bf16 a[56:59], a[36:39], a[184:187], a[56:59]\n\t"
"v_accvgpr_write_b32 a161, v99\n\t"
"v_fma_f32 v99, -v97, s56, -v195\n\t"
"v_accvgpr_write_b32 a162, v98\n\t"
"v_accvgpr_write_b32 a163, v99\n\t"
"v_mfma_f32_16x16x32_bf16 a[64:67], a[20:23], a[180:183], a[64:67]\n\t"
"v_fma_f32 v219, v111, s64, -v13\n\t"
"v_fma_f32 v98, v112, s64, -v14\n\t"
"v_mfma_f32_16x16x32_bf16 a[64:67], a[28:31], a[184:187], a[64:67]\n\t"
"v_cvt_pk_bf16_f32 v228, v228, v229\n\t"
"v_cvt_pk_bf16_f32 v229, v230, v231\n\t"
"v_mfma_f32_16x16x32_bf16 a[64:67], a[36:39], a[188:191], a[64:67]\n\t"
"v_fma_f32 v99, v113, s64, -v15\n\t"
"v_accvgpr_write_b32 a4, v98\n\t"
"v_fma_f32 v98, v114, s64, -v16\n\t"
"v_accvgpr_write_b32 a5, v99\n\t"
"s_waitcnt lgkmcnt(2)\n\t"
"v_mfma_f32_16x16x32_bf16 v[232:235], v[232:235], v[220:223], 0\n\t"
"v_fma_f32 v99, v115, s64, -v17\n\t"
"v_accvgpr_write_b32 a6, v98\n\t"
"v_accvgpr_write_b32 a7, v99\n\t"
"v_fma_f32 v98, v116, s64, -v18\n\t"
"v_mfma_f32_16x16x32_bf16 a[72:75], a[20:23], a[184:187], a[72:75]\n\t"
"v_fma_f32 v99, v117, s64, -v19\n\t"
"v_accvgpr_write_b32 a12, v98\n\t"
"v_mfma_f32_16x16x32_bf16 a[72:75], a[28:31], a[188:191], a[72:75]\n\t"
"v_fma_f32 v98, v118, s64, -v20\n\t"
"v_accvgpr_write_b32 a13, v99\n\t"
"v_mfma_f32_16x16x32_bf16 a[72:75], a[36:39], a[192:195], a[72:75]\n\t"
"v_fma_f32 v99, v119, s64, -v21\n\t"
"v_accvgpr_write_b32 a14, v98\n\t"
"v_accvgpr_write_b32 a15, v99\n\t"
"v_fma_f32 v0, v2, s52, v100\n\t"
"v_mfma_f32_16x16x32_bf16 a[80:83], a[20:23], a[188:191], a[80:83]\n\t"
"v_fma_f32 v1, v3, s52, v101\n\t"
"v_fma_f32 v100, -v100, s66, v2\n\t"
"v_mfma_f32_16x16x32_bf16 a[80:83], a[28:31], a[192:195], a[80:83]\n\t"
"v_cvt_pk_bf16_f32 v230, v232, v233\n\t"
"v_cvt_pk_bf16_f32 v231, v234, v235\n\t"
"v_mfma_f32_16x16x32_bf16 a[80:83], a[36:39], a[196:199], a[80:83]\n\t"
"ds_read_b64_tr_b16 v[232:233], v246 offset:19968\n\t"
"ds_read_b64_tr_b16 v[234:235], v246 offset:23040\n\t"
"v_fma_f32 v2, v4, s52, v102\n\t"
"v_fma_f32 v101, -v101, s66, v3\n\t"
"v_mfma_f32_16x16x32_bf16 a[88:91], a[20:23], a[192:195], a[88:91]\n\t"
"v_fma_f32 v3, v5, s52, v103\n\t"
"v_fma_f32 v102, -v102, s66, v4\n\t"
"v_mfma_f32_16x16x32_bf16 a[88:91], a[28:31], a[196:199], a[88:91]\n\t"
"v_fma_f32 v103, -v103, s66, v5\n\t"
"v_fma_f32 v4, v6, s52, v104\n\t"
"v_mfma_f32_16x16x32_bf16 a[88:91], a[36:39], a[200:203], a[88:91]\n\t"
"v_fma_f32 v5, v7, s52, v105\n\t"
"v_fma_f32 v104, -v104, s66, v6\n\t"
"v_fma_f32 v6, v8, s52, v106\n\t"
"v_fma_f32 v105, -v105, s66, v7\n\t"
"s_waitcnt lgkmcnt(2)\n\t"
"v_mfma_f32_16x16x32_bf16 v[224:227], v[224:227], v[220:223], 0\n\t"
"v_fma_f32 v7, v9, s52, v107\n\t"
"v_fma_f32 v106, -v106, s66, v8\n\t"
"v_fma_f32 v107, -v107, s66, v9\n\t"
"v_fma_f32 v8, v10, s52, v108\n\t"
"v_mfma_f32_16x16x32_bf16 a[96:99], a[20:23], a[196:199], a[96:99]\n\t"
"v_fma_f32 v98, v120, s64, -v22\n\t"
"v_fma_f32 v99, v121, s64, -v23\n\t"
"v_mfma_f32_16x16x32_bf16 a[96:99], a[28:31], a[200:203], a[96:99]\n\t"
"v_accvgpr_write_b32 a20, v98\n\t"
"v_fma_f32 v98, v122, s64, -v24\n\t"
"v_mfma_f32_16x16x32_bf16 a[96:99], a[36:39], a[204:207], a[96:99]\n\t"
"v_accvgpr_write_b32 a21, v99\n\t"
"v_fma_f32 v99, v123, s64, -v25\n\t"
"v_accvgpr_write_b32 a22, v98\n\t"
"v_accvgpr_write_b32 a23, v99\n\t"
"s_waitcnt vmcnt(18)\n\t"
"v_mfma_f32_16x16x32_bf16 a[104:107], a[76:79], a[168:171], a[104:107]\n\t"
"ds_read_b128 a[168:171], v244 offset:9216\n\t"
"v_fma_f32 v98, v124, s64, -v26\n\t"
"v_mfma_f32_16x16x32_bf16 a[104:107], a[84:87], a[172:175], a[104:107]\n\t"
"v_cvt_pk_bf16_f32 v224, v224, v225\n\t"
"v_cvt_pk_bf16_f32 v225, v226, v227\n\t"
"v_mfma_f32_16x16x32_bf16 a[104:107], a[92:95], a[176:179], a[104:107]\n\t"
"v_fma_f32 v99, v125, s64, -v27\n\t"
"v_accvgpr_write_b32 a28, v98\n\t"
"v_fma_f32 v98, v126, s64, -v28\n\t"
"v_accvgpr_write_b32 a29, v99\n\t"
"v_mfma_f32_16x16x32_bf16 a[112:115], a[76:79], a[172:175], a[112:115]\n\t"
"ds_read_b128 a[172:175], v244 offset:9728\n\t"
"v_fma_f32 v99, v127, s64, -v29\n\t"
"v_mfma_f32_16x16x32_bf16 a[112:115], a[84:87], a[176:179], a[112:115]\n\t"
"v_accvgpr_write_b32 a30, v98\n\t"
"v_accvgpr_write_b32 a31, v99\n\t"
"v_mfma_f32_16x16x32_bf16 a[112:115], a[92:95], a[180:183], a[112:115]\n\t"
"v_fma_f32 v98, v128, s64, -v30\n\t"
"v_fma_f32 v99, v129, s64, -v31\n\t"
"v_accvgpr_write_b32 a36, v98\n\t"
"v_fma_f32 v98, v130, s64, -v32\n\t"
"s_waitcnt lgkmcnt(2)\n\t"
"v_mfma_f32_16x16x32_bf16 v[232:235], v[232:235], v[220:223], 0\n\t"
"v_accvgpr_write_b32 a37, v99\n\t"
"v_fma_f32 v99, v131, s64, -v33\n\t"
"v_accvgpr_write_b32 a38, v98\n\t"
"v_accvgpr_write_b32 a39, v99\n\t"
"v_mfma_f32_16x16x32_bf16 a[120:123], a[76:79], a[176:179], a[120:123]\n\t"
"ds_read_b128 a[176:179], v244 offset:10240\n\t"
"v_fma_f32 v98, v132, s64, -v34\n\t"
"v_mfma_f32_16x16x32_bf16 a[120:123], a[84:87], a[180:183], a[120:123]\n\t"
"v_fma_f32 v99, v133, s64, -v35\n\t"
"v_accvgpr_write_b32 a44, v98\n\t"
"v_mfma_f32_16x16x32_bf16 a[120:123], a[92:95], a[184:187], a[120:123]\n\t"
"v_fma_f32 v98, v134, s64, -v36\n\t"
"v_accvgpr_write_b32 a45, v99\n\t"
"v_fma_f32 v99, v135, s64, -v37\n\t"
"v_accvgpr_write_b32 a46, v98\n\t"
"v_mfma_f32_16x16x32_bf16 a[128:131], a[76:79], a[180:183], a[128:131]\n\t"
"ds_read_b128 a[180:183], v244 offset:10752\n\t"
"v_accvgpr_write_b32 a47, v99\n\t"
"v_mfma_f32_16x16x32_bf16 a[128:131], a[84:87], a[184:187], a[128:131]\n\t"
"v_cvt_pk_bf16_f32 v226, v232, v233\n\t"
"v_cvt_pk_bf16_f32 v227, v234, v235\n\t"
"v_mfma_f32_16x16x32_bf16 a[128:131], a[92:95], a[188:191], a[128:131]\n\t"
"ds_read_b64_tr_b16 v[232:233], v246 offset:26112\n\t"
"ds_read_b64_tr_b16 v[234:235], v246 offset:29184\n\t"
"v_fma_f32 v98, v136, s64, -v38\n\t"
"v_fma_f32 v99, v137, s64, -v39\n\t"
"v_mfma_f32_16x16x32_bf16 a[136:139], a[76:79], a[184:187], a[136:139]\n\t"
"ds_read_b128 a[184:187], v244 offset:11264\n\t"
"v_accvgpr_write_b32 a52, v98\n\t"
"v_mfma_f32_16x16x32_bf16 a[136:139], a[84:87], a[188:191], a[136:139]\n\t"
"v_fma_f32 v98, v138, s64, -v40\n\t"
"v_accvgpr_write_b32 a53, v99\n\t"
"v_mfma_f32_16x16x32_bf16 a[136:139], a[92:95], a[192:195], a[136:139]\n\t"
"v_fma_f32 v99, v139, s64, -v41\n\t"
"v_accvgpr_write_b32 a54, v98\n\t"
"v_accvgpr_write_b32 a55, v99\n\t"
"v_fma_f32 v98, v140, s64, -v42\n\t"
"v_mfma_f32_16x16x32_bf16 a[144:147], a[76:79], a[188:191], a[144:147]\n\t"
"ds_read_b128 a[188:191], v244 offset:11776\n\t"
"v_fma_f32 v99, v141, s64, -v43\n\t"
"v_mfma_f32_16x16x32_bf16 a[144:147], a[84:87], a[192:195], a[144:147]\n\t"
"v_accvgpr_write_b32 a60, v98\n\t"
"v_fma_f32 v98, v142, s64, -v44\n\t"
"v_mfma_f32_16x16x32_bf16 a[144:147], a[92:95], a[196:199], a[144:147]\n\t"
"v_accvgpr_write_b32 a61, v99\n\t"
"v_fma_f32 v99, v143, s64, -v45\n\t"
"v_accvgpr_write_b32 a62, v98\n\t"
"v_accvgpr_write_b32 a63, v99\n\t"
"v_mfma_f32_16x16x32_bf16 a[152:155], a[76:79], a[192:195], a[152:155]\n\t"
"ds_read_b128 a[192:195], v244 offset:12288\n\t"
"v_fma_f32 v98, v144, s64, -v46\n\t"
"v_mfma_f32_16x16x32_bf16 a[152:155], a[84:87], a[196:199], a[152:155]\n\t"
"v_fma_f32 v99, v145, s64, -v47\n\t"
"v_accvgpr_write_b32 a68, v98\n\t"
"v_mfma_f32_16x16x32_bf16 a[152:155], a[92:95], a[200:203], a[152:155]\n\t"
"v_fma_f32 v98, v146, s64, -v48\n\t"
"v_accvgpr_write_b32 a69, v99\n\t"
"v_fma_f32 v99, v147, s64, -v49\n\t"
"v_accvgpr_write_b32 a70, v98\n\t"
"v_mfma_f32_16x16x32_bf16 a[160:163], a[76:79], a[196:199], a[160:163]\n\t"
"ds_read_b128 a[196:199], v244 offset:12800\n\t"
"v_accvgpr_write_b32 a71, v99\n\t"
"v_mfma_f32_16x16x32_bf16 a[160:163], a[84:87], a[200:203], a[160:163]\n\t"
"ds_read_b128 a[200:203], v244 offset:13312\n\t"
"v_fma_f32 v98, v148, s64, -v50\n\t"
"v_mfma_f32_16x16x32_bf16 a[160:163], a[92:95], a[204:207], a[160:163]\n\t"
"ds_read_b128 a[204:207], v244 offset:13824\n\t"
"v_fma_f32 v99, v149, s64, -v51\n\t"
"v_accvgpr_write_b32 a76, v98\n\t"
"v_fma_f32 v98, v150, s64, -v52\n\t"
"s_waitcnt lgkmcnt(6)\n\t"
"v_mfma_f32_16x16x32_bf16 v[232:235], v[232:235], v[220:223], 0\n\t"
"v_accvgpr_write_b32 a77, v99\n\t"
"v_fma_f32 v99, v151, s64, -v53\n\t"
"v_accvgpr_write_b32 a78, v98\n\t"
"v_accvgpr_write_b32 a79, v99\n\t"
"s_waitcnt vmcnt(15)\n\t"
"v_mfma_f32_16x16x32_bf16 v[200:203], a[148:151], a[168:171], v[200:203]\n\t"
"v_fma_f32 v98, v152, s64, -v54\n\t"
"v_fma_f32 v99, v153, s64, -v55\n\t"
"v_mfma_f32_16x16x32_bf16 v[200:203], a[156:159], a[172:175], v[200:203]\n\t"
"v_accvgpr_write_b32 a84, v98\n\t"
"v_fma_f32 v98, v154, s64, -v56\n\t"
"v_mfma_f32_16x16x32_bf16 v[200:203], a[164:167], a[176:179], v[200:203]\n\t"
"v_accvgpr_write_b32 a85, v99\n\t"
"v_fma_f32 v99, v155, s64, -v57\n\t"
"v_accvgpr_write_b32 a86, v98\n\t"
"v_accvgpr_write_b32 a87, v99\n\t"
"v_mfma_f32_16x16x32_bf16 v[208:211], a[148:151], a[172:175], v[208:211]\n\t"
"v_fma_f32 v98, v156, s64, -v58\n\t"
"v_fma_f32 v99, v157, s64, -v59\n\t"
"v_mfma_f32_16x16x32_bf16 v[208:211], a[156:159], a[176:179], v[208:211]\n\t"
"v_cvt_pk_bf16_f32 v232, v232, v233\n\t"
"v_cvt_pk_bf16_f32 v233, v234, v235\n\t"
"v_mfma_f32_16x16x32_bf16 v[208:211], a[164:167], a[180:183], v[208:211]\n\t"
"v_accvgpr_write_b32 a92, v98\n\t"
"v_fma_f32 v98, v158, s64, -v60\n\t"
"v_accvgpr_write_b32 a93, v99\n\t"
"v_fma_f32 v99, v159, s64, -v61\n\t"
"s_waitcnt lgkmcnt(0)\n\t"
"s_barrier\n\t"
"v_mfma_f32_16x16x32_bf16 v[216:219], a[148:151], a[176:179], v[216:219]\n\t"
"v_accvgpr_write_b32 a94, v98\n\t"
"v_accvgpr_write_b32 a95, v99\n\t"
"v_mfma_f32_16x16x32_bf16 v[216:219], a[156:159], a[180:183], v[216:219]\n\t"
"v_fma_f32 v98, v160, s64, -v62\n\t"
"v_fma_f32 v99, v161, s64, -v63\n\t"
"v_mfma_f32_16x16x32_bf16 v[216:219], a[164:167], a[184:187], v[216:219]\n\t"
"s_add_u32 m0, s24, 896\n\t"
"s_add_u32 s28, s21, 0\n\t"
"buffer_load_dwordx4 v247, s[12:15], s28 offen offset:128 nt lds\n\t"
"ds_write2st64_b64 v245, v[228:229], v[230:231] offset0:0 offset1:1\n\t"
"s_add_u32 m0, s24, 7040\n\t"
"s_add_u32 s28, s21, 196608\n\t"
"buffer_load_dwordx4 v247, s[12:15], s28 offen offset:128 nt lds\n\t"
"v_mfma_f32_16x16x32_bf16 a[4:7], a[148:151], a[180:183], a[4:7]\n\t"
"ds_read_b64_tr_b16 v[228:229], v246 offset:32256\n\t"
"v_accvgpr_write_b32 a100, v98\n\t"
"v_mfma_f32_16x16x32_bf16 a[4:7], a[156:159], a[184:187], a[4:7]\n\t"
"ds_read_b64_tr_b16 v[230:231], v246 offset:35328\n\t"
"v_fma_f32 v98, v162, s64, -v64\n\t"
"v_mfma_f32_16x16x32_bf16 a[4:7], a[164:167], a[188:191], a[4:7]\n\t"
"ds_write2st64_b64 v245, v[224:225], v[226:227] offset0:2 offset1:3\n\t"
"s_add_u32 m0, s24, 13184\n\t"
"s_add_u32 s28, s21, 393216\n\t"
"buffer_load_dwordx4 v247, s[12:15], s28 offen offset:128 nt lds\n\t"
"v_mfma_f32_16x16x32_bf16 a[12:15], a[148:151], a[184:187], a[12:15]\n\t"
"ds_read_b64_tr_b16 v[224:225], v246 offset:39936\n\t"
"v_accvgpr_write_b32 a101, v99\n\t"
"v_mfma_f32_16x16x32_bf16 a[12:15], a[156:159], a[188:191], a[12:15]\n\t"
"ds_read_b64_tr_b16 v[226:227], v246 offset:43008\n\t"
"v_fma_f32 v99, v163, s64, -v65\n\t"
"v_mfma_f32_16x16x32_bf16 a[12:15], a[164:167], a[192:195], a[12:15]\n\t"
"ds_write_b64 v245, v[232:233] offset:2048\n\t"
"s_add_u32 m0, s24, 20864\n\t"
"s_add_u32 s28, s21, 589824\n\t"
"buffer_load_dwordx4 v247, s[12:15], s28 offen offset:128 nt lds\n\t"
"s_add_u32 m0, s24, 60800\n\t"
"s_add_u32 s28, s21, 1769472\n\t"
"buffer_load_dwordx4 v247, s[12:15], s28 offen offset:128 nt lds\n\t"
"v_mfma_f32_16x16x32_bf16 a[20:23], a[148:151], a[188:191], a[20:23]\n\t"
"ds_read_b64_tr_b16 v[232:233], v246 offset:46080\n\t"
"v_accvgpr_write_b32 a102, v98\n\t"
"v_mfma_f32_16x16x32_bf16 a[20:23], a[156:159], a[192:195], a[20:23]\n\t"
"ds_read_b64_tr_b16 v[234:235], v246 offset:49152\n\t"
"v_accvgpr_write_b32 a103, v99\n\t"
"v_mfma_f32_16x16x32_bf16 a[20:23], a[164:167], a[196:199], a[20:23]\n\t"
"s_add_u32 m0, s24, 66944\n\t"
"s_add_u32 s28, s21, 1966080\n\t"
"buffer_load_dwordx4 v247, s[12:15], s28 offen offset:128 nt lds\n\t"
"v_fma_f32 v98, v164, s64, -v66\n\t"
"v_fma_f32 v99, v165, s64, -v67\n\t"
"s_waitcnt lgkmcnt(6)\n\t"
"v_mfma_f32_16x16x32_bf16 v[228:231], v[228:231], v[220:223], 0\n\t"
"v_accvgpr_write_b32 a108, v98\n\t"
"v_fma_f32 v98, v166, s64, -v68\n\t"
"v_accvgpr_write_b32 a109, v99\n\t"
"v_fma_f32 v99, v167, s64, -v69\n\t"
"v_mfma_f32_16x16x32_bf16 a[28:31], a[148:151], a[192:195], a[28:31]\n\t"
"v_accvgpr_write_b32 a110, v98\n\t"
"v_accvgpr_write_b32 a111, v99\n\t"
"v_mfma_f32_16x16x32_bf16 a[28:31], a[156:159], a[196:199], a[28:31]\n\t"
"v_fma_f32 v98, v168, s64, -v70\n\t"
"v_fma_f32 v99, v169, s64, -v71\n\t"
"v_mfma_f32_16x16x32_bf16 a[28:31], a[164:167], a[200:203], a[28:31]\n\t"
"v_accvgpr_write_b32 a116, v98\n\t"
"v_fma_f32 v98, v170, s64, -v72\n\t"
"v_accvgpr_write_b32 a117, v99\n\t"
"v_fma_f32 v99, v171, s64, -v73\n\t"
"s_waitcnt lgkmcnt(3)\n\t"
"v_mfma_f32_16x16x32_bf16 v[224:227], v[224:227], v[220:223], 0\n\t"
"v_accvgpr_write_b32 a118, v98\n\t"
"v_accvgpr_write_b32 a119, v99\n\t"
"v_fma_f32 v98, v172, s64, -v74\n\t"
"v_fma_f32 v99, v173, s64, -v75\n\t"
"v_mfma_f32_16x16x32_bf16 a[36:39], a[148:151], a[196:199], a[36:39]\n\t"
"v_cvt_pk_bf16_f32 v228, v228, v229\n\t"
"v_cvt_pk_bf16_f32 v229, v230, v231\n\t"
"v_mfma_f32_16x16x32_bf16 a[36:39], a[156:159], a[200:203], a[36:39]\n\t"
"v_accvgpr_write_b32 a124, v98\n\t"
"v_fma_f32 v98, v174, s64, -v76\n\t"
"v_mfma_f32_16x16x32_bf16 a[36:39], a[164:167], a[204:207], a[36:39]\n\t"
"v_accvgpr_write_b32 a125, v99\n\t"
"v_fma_f32 v99, v175, s64, -v77\n\t"
"v_accvgpr_write_b32 a126, v98\n\t"
"v_accvgpr_write_b32 a127, v99\n\t"
"s_waitcnt lgkmcnt(0)\n\t"
"v_mfma_f32_16x16x32_bf16 v[232:235], v[232:235], v[220:223], 0\n\t"
"v_fma_f32 v98, v176, s64, -v78\n\t"
"v_fma_f32 v99, v177, s64, -v79\n\t"
"v_accvgpr_write_b32 a132, v98\n\t"
"v_fma_f32 v98, v178, s64, -v80\n\t"
"s_waitcnt vmcnt(18)\n\t"
"v_mfma_f32_16x16x32_bf16 a[44:47], v[240:243], a[168:171], a[44:47]\n\t"
"v_cvt_pk_bf16_f32 v230, v224, v225\n\t"
"v_cvt_pk_bf16_f32 v231, v226, v227\n\t"
"v_mfma_f32_16x16x32_bf16 a[44:47], a[208:211], a[172:175], a[44:47]\n\t"
"ds_read_b64_tr_b16 v[224:225], v246 offset:52224\n\t"
"v_accvgpr_write_b32 a133, v99\n\t"
"v_mfma_f32_16x16x32_bf16 a[44:47], v[248:251], a[176:179], a[44:47]\n\t"
"ds_write2st64_b64 v245, v[228:229], v[230:231] offset0:5 offset1:6\n\t"
"ds_read_b64_tr_b16 v[226:227], v246 offset:55296\n\t"
"v_fma_f32 v99, v179, s64, -v81\n\t"
"v_mfma_f32_16x16x32_bf16 a[52:55], v[240:243], a[172:175], a[52:55]\n\t"
"v_accvgpr_write_b32 a134, v98\n\t"
"v_accvgpr_write_b32 a135, v99\n\t"
"v_mfma_f32_16x16x32_bf16 a[52:55], a[208:211], a[176:179], a[52:55]\n\t"
"v_cvt_pk_bf16_f32 v232, v232, v233\n\t"
"v_cvt_pk_bf16_f32 v233, v234, v235\n\t"
"v_mfma_f32_16x16x32_bf16 a[52:55], v[248:251], a[180:183], a[52:55]\n\t"
"v_fma_f32 v98, v180, s64, -v82\n\t"
"v_fma_f32 v99, v181, s64, -v83\n\t"
"v_accvgpr_write_b32 a140, v98\n\t"
"v_fma_f32 v98, v182, s64, -v84\n\t"
"v_mfma_f32_16x16x32_bf16 a[60:63], v[240:243], a[176:179], a[60:63]\n\t"
"v_accvgpr_write_b32 a141, v99\n\t"
"v_fma_f32 v99, v183, s64, -v85\n\t"
"v_mfma_f32_16x16x32_bf16 a[60:63], a[208:211], a[180:183], a[60:63]\n\t"
"v_accvgpr_write_b32 a142, v98\n\t"
"v_accvgpr_write_b32 a143, v99\n\t"
"v_mfma_f32_16x16x32_bf16 a[60:63], v[248:251], a[184:187], a[60:63]\n\t"
"v_fma_f32 v98, v184, s64, -v86\n\t"
"v_fma_f32 v99, v185, s64, -v87\n\t"
"v_accvgpr_write_b32 a148, v98\n\t"
"v_fma_f32 v98, v186, s64, -v88\n\t"
"v_mfma_f32_16x16x32_bf16 a[68:71], v[240:243], a[180:183], a[68:71]\n\t"
"v_accvgpr_write_b32 a149, v99\n\t"
"v_fma_f32 v99, v187, s64, -v89\n\t"
"v_mfma_f32_16x16x32_bf16 a[68:71], a[208:211], a[184:187], a[68:71]\n\t"
"v_accvgpr_write_b32 a150, v98\n\t"
"v_accvgpr_write_b32 a151, v99\n\t"
"v_mfma_f32_16x16x32_bf16 a[68:71], v[248:251], a[188:191], a[68:71]\n\t"
"v_fma_f32 v98, v188, s64, -v90\n\t"
"v_fma_f32 v99, v189, s64, -v91\n\t"
"v_accvgpr_write_b32 a156, v98\n\t"
"v_fma_f32 v98, v190, s64, -v92\n\t"
"v_mfma_f32_16x16x32_bf16 a[76:79], v[240:243], a[184:187], a[76:79]\n\t"
"v_accvgpr_write_b32 a157, v99\n\t"
"v_fma_f32 v99, v191, s64, -v93\n\t"
"v_mfma_f32_16x16x32_bf16 a[76:79], a[208:211], a[188:191], a[76:79]\n\t"
"v_accvgpr_write_b32 a158, v98\n\t"
"v_accvgpr_write_b32 a159, v99\n\t"
"v_mfma_f32_16x16x32_bf16 a[76:79], v[248:251], a[192:195], a[76:79]\n\t"
"v_fma_f32 v98, v192, s64, -v94\n\t"
"v_fma_f32 v99, v193, s64, -v95\n\t"
"v_accvgpr_write_b32 a164, v98\n\t"
"v_fma_f32 v98, v194, s64, -v96\n\t"
"s_waitcnt lgkmcnt(0)\n\t"
"v_mfma_f32_16x16x32_bf16 v[224:227], v[224:227], v[220:223], 0\n\t"
"v_accvgpr_write_b32 a165, v99\n\t"
"v_fma_f32 v99, v195, s64, -v97\n\t"
"v_accvgpr_write_b32 a166, v98\n\t"
"v_accvgpr_write_b32 a167, v99\n\t"
"v_mfma_f32_16x16x32_bf16 a[84:87], v[240:243], a[188:191], a[84:87]\n\t"
"v_fma_f32 v9, v11, s52, v109\n\t"
"v_fma_f32 v108, -v108, s66, v10\n\t"
"v_mfma_f32_16x16x32_bf16 a[84:87], a[208:211], a[192:195], a[84:87]\n\t"
"v_fma_f32 v10, v12, s52, v110\n\t"
"v_fma_f32 v109, -v109, s66, v11\n\t"
"v_mfma_f32_16x16x32_bf16 a[84:87], v[248:251], a[196:199], a[84:87]\n\t"
"v_fma_f32 v11, v13, s52, v111\n\t"
"v_fma_f32 v110, -v110, s66, v12\n\t"
"v_fma_f32 v111, -v111, s66, v13\n\t"
"v_fma_f32 v12, v14, s52, v112\n\t"
"v_mfma_f32_16x16x32_bf16 a[92:95], v[240:243], a[192:195], a[92:95]\n\t"
"v_fma_f32 v13, v15, s52, v113\n\t"
"v_fma_f32 v112, -v112, s66, v14\n\t"
"v_mfma_f32_16x16x32_bf16 a[92:95], a[208:211], a[196:199], a[92:95]\n\t"
"v_cvt_pk_bf16_f32 v234, v224, v225\n\t"
"v_cvt_pk_bf16_f32 v235, v226, v227\n\t"
"v_mfma_f32_16x16x32_bf16 a[92:95], v[248:251], a[200:203], a[92:95]\n\t"
"ds_write2st64_b64 v245, v[232:233], v[234:235] offset0:7 offset1:8\n\t"
"v_fma_f32 v14, v16, s52, v114\n\t"
"v_fma_f32 v113, -v113, s66, v15\n\t"
"v_mfma_f32_16x16x32_bf16 a[100:103], v[240:243], a[196:199], a[100:103]\n\t"
"v_fma_f32 v15, v17, s52, v115\n\t"
"v_fma_f32 v114, -v114, s66, v16\n\t"
"v_mfma_f32_16x16x32_bf16 a[100:103], a[208:211], a[200:203], a[100:103]\n\t"
"v_fma_f32 v115, -v115, s66, v17\n\t"
"v_fma_f32 v16, v18, s52, v116\n\t"
"v_mfma_f32_16x16x32_bf16 a[100:103], v[248:251], a[204:207], a[100:103]\n\t"
"s_mov_b32 s20, 73728\n\t"
"buffer_load_dwordx4 v[240:243], v252, s[8:11], s20 offen offset:0 sc1\n\t"
"v_fma_f32 v17, v19, s52, v117\n\t"
"v_fma_f32 v116, -v116, s66, v18\n\t"
"s_waitcnt vmcnt(16)\n\t"
"v_mfma_f32_16x16x32_bf16 a[108:111], a[212:215], a[168:171], a[108:111]\n\t"
"v_fma_f32 v18, v20, s52, v118\n\t"
"v_fma_f32 v117, -v117, s66, v19\n\t"
"v_mfma_f32_16x16x32_bf16 a[108:111], a[216:219], a[172:175], a[108:111]\n\t"
"v_fma_f32 v19, v21, s52, v119\n\t"
"v_fma_f32 v118, -v118, s66, v20\n\t"
"v_mfma_f32_16x16x32_bf16 a[108:111], a[220:223], a[176:179], a[108:111]\n\t"
"v_fma_f32 v119, -v119, s66, v21\n\t"
"v_fma_f32 v20, v22, s52, v120\n\t"
"v_fma_f32 v21, v23, s52, v121\n\t"
"v_fma_f32 v120, -v120, s66, v22\n\t"
"s_waitcnt lgkmcnt(0)\n\t"
"s_barrier\n\t"
"v_mfma_f32_16x16x32_bf16 a[116:119], a[212:215], a[172:175], a[116:119]\n\t"
"ds_read_b128 a[168:171], v244 offset:0\n\t"
"v_fma_f32 v22, v24, s52, v122\n\t"
"v_mfma_f32_16x16x32_bf16 a[116:119], a[216:219], a[176:179], a[116:119]\n\t"
"ds_read_b128 a[172:175], v244 offset:512\n\t"
"v_fma_f32 v121, -v121, s66, v23\n\t"
"v_mfma_f32_16x16x32_bf16 a[116:119], a[220:223], a[180:183], a[116:119]\n\t"
"buffer_load_dwordx4 a[208:211], v252, s[8:11], s20 offen offset:1024 sc1\n\t"
"v_fma_f32 v23, v25, s52, v123\n\t"
"v_fma_f32 v122, -v122, s66, v24\n\t"
"v_mfma_f32_16x16x32_bf16 a[124:127], a[212:215], a[176:179], a[124:127]\n\t"
"ds_read_b128 a[176:179], v244 offset:1024\n\t"
"v_fma_f32 v123, -v123, s66, v25\n\t"
"v_mfma_f32_16x16x32_bf16 a[124:127], a[216:219], a[180:183], a[124:127]\n\t"
"v_fma_f32 v24, v26, s52, v124\n\t"
"v_fma_f32 v25, v27, s52, v125\n\t"
"v_mfma_f32_16x16x32_bf16 a[124:127], a[220:223], a[184:187], a[124:127]\n\t"
"buffer_load_dwordx4 v[248:251], v252, s[8:11], s20 offen offset:2048 sc1\n\t"
"v_fma_f32 v124, -v124, s66, v26\n\t"
"v_fma_f32 v26, v28, s52, v126\n\t"
"v_mfma_f32_16x16x32_bf16 a[132:135], a[212:215], a[180:183], a[132:135]\n\t"
"ds_read_b128 a[180:183], v244 offset:1536\n\t"
"v_fma_f32 v125, -v125, s66, v27\n\t"
"v_mfma_f32_16x16x32_bf16 a[132:135], a[216:219], a[184:187], a[132:135]\n\t"
"v_fma_f32 v27, v29, s52, v127\n\t"
"v_fma_f32 v126, -v126, s66, v28\n\t"
"v_mfma_f32_16x16x32_bf16 a[132:135], a[220:223], a[188:191], a[132:135]\n\t"
"v_fma_f32 v127, -v127, s66, v29\n\t"
"v_fma_f32 v28, v30, s52, v128\n\t"
"v_fma_f32 v29, v31, s52, v129\n\t"
"v_fma_f32 v128, -v128, s66, v30\n\t"
"v_mfma_f32_16x16x32_bf16 a[140:143], a[212:215], a[184:187], a[140:143]\n\t"
"ds_read_b128 a[184:187], v244 offset:2048\n\t"
"v_fma_f32 v30, v32, s52, v130\n\t"
"v_mfma_f32_16x16x32_bf16 a[140:143], a[216:219], a[188:191], a[140:143]\n\t"
"v_fma_f32 v129, -v129, s66, v31\n\t"
"v_fma_f32 v31, v33, s52, v131\n\t"
"v_mfma_f32_16x16x32_bf16 a[140:143], a[220:223], a[192:195], a[140:143]\n\t"
"v_fma_f32 v130, -v130, s66, v32\n\t"
"v_fma_f32 v131, -v131, s66, v33\n\t"
"v_fma_f32 v32, v34, s52, v132\n\t"
"v_fma_f32 v33, v35, s52, v133\n\t"
"v_mfma_f32_16x16x32_bf16 a[148:151], a[212:215], a[188:191], a[148:151]\n\t"
"ds_read_b128 a[188:191], v244 offset:2560\n\t"
"v_fma_f32 v132, -v132, s66, v34\n\t"
"v_mfma_f32_16x16x32_bf16 a[148:151], a[216:219], a[192:195], a[148:151]\n\t"
"v_fma_f32 v34, v36, s52, v134\n\t"
"v_fma_f32 v133, -v133, s66, v35\n\t"
"v_mfma_f32_16x16x32_bf16 a[148:151], a[220:223], a[196:199], a[148:151]\n\t"
"v_fma_f32 v35, v37, s52, v135\n\t"
"v_fma_f32 v134, -v134, s66, v36\n\t"
"v_fma_f32 v135, -v135, s66, v37\n\t"
"v_fma_f32 v36, v38, s52, v136\n\t"
"v_mfma_f32_16x16x32_bf16 a[156:159], a[212:215], a[192:195], a[156:159]\n\t"
"ds_read_b128 a[192:195], v244 offset:3072\n\t"
"v_fma_f32 v37, v39, s52, v137\n\t"
"v_mfma_f32_16x16x32_bf16 a[156:159], a[216:219], a[196:199], a[156:159]\n\t"
"v_fma_f32 v136, -v136, s66, v38\n\t"
"v_fma_f32 v38, v40, s52, v138\n\t"
"v_mfma_f32_16x16x32_bf16 a[156:159], a[220:223], a[200:203], a[156:159]\n\t"
"v_fma_f32 v137, -v137, s66, v39\n\t"
"v_fma_f32 v39, v41, s52, v139\n\t"
"v_fma_f32 v138, -v138, s66, v40\n\t"
"v_fma_f32 v139, -v139, s66, v41\n\t"
"v_mfma_f32_16x16x32_bf16 a[164:167], a[212:215], a[196:199], a[164:167]\n\t"
"ds_read_b128 a[196:199], v244 offset:3584\n\t"
"v_fma_f32 v40, v42, s52, v140\n\t"
"v_mfma_f32_16x16x32_bf16 a[164:167], a[216:219], a[200:203], a[164:167]\n\t"
"ds_read_b128 a[200:203], v244 offset:4096\n\t"
"v_fma_f32 v41, v43, s52, v141\n\t"
"v_mfma_f32_16x16x32_bf16 a[164:167], a[220:223], a[204:207], a[164:167]\n\t"
"s_mov_b32 s20, 86016\n\t"
"buffer_load_dwordx4 a[212:215], v252, s[8:11], s20 offen offset:0 sc1\n\t"
"ds_read_b128 a[204:207], v244 offset:4608\n\t"
"v_fma_f32 v140, -v140, s66, v42\n\t"
"Lst3_%=:\n\t"
"s_waitcnt vmcnt(16) lgkmcnt(7)\n\t"
"v_mfma_f32_16x16x32_bf16 v[0:3], a[224:227], a[168:171], v[0:3]\n\t"
"v_fma_f32 v42, v44, s52, v142\n\t"
"v_fma_f32 v141, -v141, s66, v43\n\t"
"v_mfma_f32_16x16x32_bf16 v[0:3], a[228:231], a[172:175], v[0:3]\n\t"
"v_fma_f32 v43, v45, s52, v143\n\t"
"v_fma_f32 v142, -v142, s66, v44\n\t"
"v_mfma_f32_16x16x32_bf16 v[0:3], a[232:235], a[176:179], v[0:3]\n\t"
"buffer_load_dwordx4 a[216:219], v252, s[8:11], s20 offen offset:1024 sc1\n\t"
"v_fma_f32 v143, -v143, s66, v45\n\t"
"v_fma_f32 v44, v46, s52, v144\n\t"
"s_waitcnt lgkmcnt(6)\n\t"
"v_mfma_f32_16x16x32_bf16 v[4:7], a[224:227], a[172:175], v[4:7]\n\t"
"v_fma_f32 v45, v47, s52, v145\n\t"
"v_fma_f32 v144, -v144, s66, v46\n\t"
"v_mfma_f32_16x16x32_bf16 v[4:7], a[228:231], a[176:179], v[4:7]\n\t"
"v_fma_f32 v46, v48, s52, v146\n\t"
"v_fma_f32 v145, -v145, s66, v47\n\t"
"v_mfma_f32_16x16x32_bf16 v[4:7], a[232:235], a[180:183], v[4:7]\n\t"
"buffer_load_dwordx4 a[220:223], v252, s[8:11], s20 offen offset:2048 sc1\n\t"
"v_fma_f32 v47, v49, s52, v147\n\t"
"v_fma_f32 v146, -v146, s66, v48\n\t"
"s_waitcnt lgkmcnt(5)\n\t"
"v_mfma_f32_16x16x32_bf16 v[8:11], a[224:227], a[176:179], v[8:11]\n\t"
"v_fma_f32 v147, -v147, s66, v49\n\t"
"v_fma_f32 v48, v50, s52, v148\n\t"
"v_mfma_f32_16x16x32_bf16 v[8:11], a[228:231], a[180:183], v[8:11]\n\t"
"v_fma_f32 v49, v51, s52, v149\n\t"
"v_fma_f32 v148, -v148, s66, v50\n\t"
"v_mfma_f32_16x16x32_bf16 v[8:11], a[232:235], a[184:187], v[8:11]\n\t"
"v_fma_f32 v50, v52, s52, v150\n\t"
"v_fma_f32 v149, -v149, s66, v51\n\t"
"v_fma_f32 v51, v53, s52, v151\n\t"
"v_fma_f32 v150, -v150, s66, v52\n\t"
"s_waitcnt lgkmcnt(4)\n\t"
"v_mfma_f32_16x16x32_bf16 v[12:15], a[224:227], a[180:183], v[12:15]\n\t"
"v_fma_f32 v151, -v151, s66, v53\n\t"
"v_fma_f32 v52, v54, s52, v152\n\t"
"v_mfma_f32_16x16x32_bf16 v[12:15], a[228:231], a[184:187], v[12:15]\n\t"
"v_fma_f32 v53, v55, s52, v153\n\t"
"v_fma_f32 v152, -v152, s66, v54\n\t"
"v_mfma_f32_16x16x32_bf16 v[12:15], a[232:235], a[188:191], v[12:15]\n\t"
"v_fma_f32 v54, v56, s52, v154\n\t"
"v_fma_f32 v153, -v153, s66, v55\n\t"
"v_fma_f32 v55, v57, s52, v155\n\t"
"v_fma_f32 v154, -v154, s66, v56\n\t"
"s_waitcnt lgkmcnt(3)\n\t"
"v_mfma_f32_16x16x32_bf16 v[16:19], a[224:227], a[184:187], v[16:19]\n\t"
"v_fma_f32 v155, -v155, s66, v57\n\t"
"v_fma_f32 v56, v58, s52, v156\n\t"
"v_mfma_f32_16x16x32_bf16 v[16:19], a[228:231], a[188:191], v[16:19]\n\t"
"v_fma_f32 v57, v59, s52, v157\n\t"
"v_fma_f32 v156, -v156, s66, v58\n\t"
"v_mfma_f32_16x16x32_bf16 v[16:19], a[232:235], a[192:195], v[16:19]\n\t"
"ds_read_b64_tr_b16 v[228:229], v246 offset:512\n\t"
"ds_read_b64_tr_b16 v[230:231], v246 offset:3584\n\t"
"v_fma_f32 v58, v60, s52, v158\n\t"
"v_fma_f32 v157, -v157, s66, v59\n\t"
"s_waitcnt lgkmcnt(4)\n\t"
"v_mfma_f32_16x16x32_bf16 v[20:23], a[224:227], a[188:191], v[20:23]\n\t"
"v_fma_f32 v59, v61, s52, v159\n\t"
"v_fma_f32 v158, -v158, s66, v60\n\t"
"v_mfma_f32_16x16x32_bf16 v[20:23], a[228:231], a[192:195], v[20:23]\n\t"
"v_fma_f32 v159, -v159, s66, v61\n\t"
"v_fma_f32 v60, v62, s52, v160\n\t"
"v_mfma_f32_16x16x32_bf16 v[20:23], a[232:235], a[196:199], v[20:23]\n\t"
"v_fma_f32 v61, v63, s52, v161\n\t"
"v_fma_f32 v160, -v160, s66, v62\n\t"
"v_fma_f32 v62, v64, s52, v162\n\t"
"v_fma_f32 v161, -v161, s66, v63\n\t"
"s_waitcnt lgkmcnt(3)\n\t"
"v_mfma_f32_16x16x32_bf16 v[24:27], a[224:227], a[192:195], v[24:27]\n\t"
"v_fma_f32 v63, v65, s52, v163\n\t"
"v_fma_f32 v162, -v162, s66, v64\n\t"
"v_mfma_f32_16x16x32_bf16 v[24:27], a[228:231], a[196:199], v[24:27]\n\t"
"v_fma_f32 v163, -v163, s66, v65\n\t"
"v_fma_f32 v64, v66, s52, v164\n\t"
"v_mfma_f32_16x16x32_bf16 v[24:27], a[232:235], a[200:203], v[24:27]\n\t"
"v_fma_f32 v65, v67, s52, v165\n\t"
"v_fma_f32 v164, -v164, s66, v66\n\t"
"v_fma_f32 v66, v68, s52, v166\n\t"
"v_fma_f32 v165, -v165, s66, v67\n\t"
"s_waitcnt lgkmcnt(2)\n\t"
"v_mfma_f32_16x16x32_bf16 v[28:31], a[224:227], a[196:199], v[28:31]\n\t"
"ds_read_b64_tr_b16 v[224:225], v246 offset:6656\n\t"
"v_fma_f32 v67, v69, s52, v167\n\t"
"v_mfma_f32_16x16x32_bf16 v[28:31], a[228:231], a[200:203], v[28:31]\n\t"
"ds_read_b64_tr_b16 v[226:227], v246 offset:9728\n\t"
"v_fma_f32 v166, -v166, s66, v68\n\t"
"v_mfma_f32_16x16x32_bf16 v[28:31], a[232:235], a[204:207], v[28:31]\n\t"
"s_mov_b32 s20, 98304\n\t"
"buffer_load_dwordx4 a[224:227], v252, s[8:11], s20 offen offset:0 sc1\n\t"
"v_fma_f32 v167, -v167, s66, v69\n\t"
"v_fma_f32 v68, v70, s52, v168\n\t"
"s_waitcnt vmcnt(16)\n\t"
"v_mfma_f32_16x16x32_bf16 v[32:35], a[236:239], a[168:171], v[32:35]\n\t"
"v_fma_f32 v69, v71, s52, v169\n\t"
"v_fma_f32 v168, -v168, s66, v70\n\t"
"v_mfma_f32_16x16x32_bf16 v[32:35], a[240:243], a[172:175], v[32:35]\n\t"
"v_fma_f32 v70, v72, s52, v170\n\t"
"v_fma_f32 v169, -v169, s66, v71\n\t"
"v_mfma_f32_16x16x32_bf16 v[32:35], a[244:247], a[176:179], v[32:35]\n\t"
"buffer_load_dwordx4 a[228:231], v252, s[8:11], s20 offen offset:1024 sc1\n\t"
"v_fma_f32 v71, v73, s52, v171\n\t"
"v_fma_f32 v170, -v170, s66, v72\n\t"
"v_mfma_f32_16x16x32_bf16 v[36:39], a[236:239], a[172:175], v[36:39]\n\t"
"v_fma_f32 v171, -v171, s66, v73\n\t"
"v_fma_f32 v72, v74, s52, v172\n\t"
"v_mfma_f32_16x16x32_bf16 v[36:39], a[240:243], a[176:179], v[36:39]\n\t"
"v_fma_f32 v73, v75, s52, v173\n\t"
"v_fma_f32 v172, -v172, s66, v74\n\t"
"v_mfma_f32_16x16x32_bf16 v[36:39], a[244:247], a[180:183], v[36:39]\n\t"
"buffer_load_dwordx4 a[232:235], v252, s[8:11], s20 offen offset:2048 sc1\n\t"
"v_fma_f32 v74, v76, s52, v174\n\t"
"v_fma_f32 v173, -v173, s66, v75\n\t"
"s_waitcnt lgkmcnt(2)\n\t"
"v_mfma_f32_16x16x32_bf16 v[228:231], v[228:231], v[220:223], 0\n\t"
"v_fma_f32 v75, v77, s52, v175\n\t"
"v_fma_f32 v174, -v174, s66, v76\n\t"
"v_fma_f32 v175, -v175, s66, v77\n\t"
"v_fma_f32 v76, v78, s52, v176\n\t"
"v_mfma_f32_16x16x32_bf16 v[40:43], a[236:239], a[176:179], v[40:43]\n\t"
"ds_read_b64_tr_b16 v[232:233], v246 offset:12800\n\t"
"v_fma_f32 v77, v79, s52, v177\n\t"
"v_mfma_f32_16x16x32_bf16 v[40:43], a[240:243], a[180:183], v[40:43]\n\t"
"ds_read_b64_tr_b16 v[234:235], v246 offset:15872\n\t"
"v_fma_f32 v176, -v176, s66, v78\n\t"
"v_mfma_f32_16x16x32_bf16 v[40:43], a[244:247], a[184:187], v[40:43]\n\t"
"v_fma_f32 v78, v80, s52, v178\n\t"
"v_fma_f32 v177, -v177, s66, v79\n\t"
"v_fma_f32 v79, v81, s52, v179\n\t"
"v_fma_f32 v178, -v178, s66, v80\n\t"
"v_mfma_f32_16x16x32_bf16 v[44:47], a[236:239], a[180:183], v[44:47]\n\t"
"v_fma_f32 v179, -v179, s66, v81\n\t"
"v_fma_f32 v80, v82, s52, v180\n\t"
"v_mfma_f32_16x16x32_bf16 v[44:47], a[240:243], a[184:187], v[44:47]\n\t"
"v_cvt_pk_bf16_f32 v228, v228, v229\n\t"
"v_cvt_pk_bf16_f32 v229, v230, v231\n\t"
"v_mfma_f32_16x16x32_bf16 v[44:47], a[244:247], a[188:191], v[44:47]\n\t"
"v_fma_f32 v81, v83, s52, v181\n\t"
"v_fma_f32 v180, -v180, s66, v82\n\t"
"v_fma_f32 v82, v84, s52, v182\n\t"
"v_fma_f32 v181, -v181, s66, v83\n\t"
"s_waitcnt lgkmcnt(2)\n\t"
"v_mfma_f32_16x16x32_bf16 v[224:227], v[224:227], v[220:223], 0\n\t"
"v_fma_f32 v83, v85, s52, v183\n\t"
"v_fma_f32 v182, -v182, s66, v84\n\t"
"v_fma_f32 v183, -v183, s66, v85\n\t"
"v_fma_f32 v84, v86, s52, v184\n\t"
"v_mfma_f32_16x16x32_bf16 v[48:51], a[236:239], a[184:187], v[48:51]\n\t"
"v_fma_f32 v85, v87, s52, v185\n\t"
"v_fma_f32 v184, -v184, s66, v86\n\t"
"v_mfma_f32_16x16x32_bf16 v[48:51], a[240:243], a[188:191], v[48:51]\n\t"
"v_fma_f32 v86, v88, s52, v186\n\t"
"v_fma_f32 v185, -v185, s66, v87\n\t"
"v_mfma_f32_16x16x32_bf16 v[48:51], a[244:247], a[192:195], v[48:51]\n\t"
"v_fma_f32 v87, v89, s52, v187\n\t"
"v_fma_f32 v186, -v186, s66, v88\n\t"
"v_fma_f32 v187, -v187, s66, v89\n\t"
"v_fma_f32 v88, v90, s52, v188\n\t"
"v_mfma_f32_16x16x32_bf16 v[52:55], a[236:239], a[188:191], v[52:55]\n\t"
"v_fma_f32 v89, v91, s52, v189\n\t"
"v_fma_f32 v188, -v188, s66, v90\n\t"
"v_mfma_f32_16x16x32_bf16 v[52:55], a[240:243], a[192:195], v[52:55]\n\t"
"v_cvt_pk_bf16_f32 v230, v224, v225\n\t"
"v_cvt_pk_bf16_f32 v231, v226, v227\n\t"
"v_mfma_f32_16x16x32_bf16 v[52:55], a[244:247], a[196:199], v[52:55]\n\t"
"ds_read_b64_tr_b16 v[224:225], v246 offset:20480\n\t"
"ds_read_b64_tr_b16 v[226:227], v246 offset:23552\n\t"
"v_fma_f32 v90, v92, s52, v190\n\t"
"v_fma_f32 v189, -v189, s66, v91\n\t"
"v_mfma_f32_16x16x32_bf16 v[56:59], a[236:239], a[192:195], v[56:59]\n\t"
"v_fma_f32 v91, v93, s52, v191\n\t"
"v_fma_f32 v190, -v190, s66, v92\n\t"
"v_mfma_f32_16x16x32_bf16 v[56:59], a[240:243], a[196:199], v[56:59]\n\t"
"v_fma_f32 v191, -v191, s66, v93\n\t"
"v_fma_f32 v92, v94, s52, v192\n\t"
"v_mfma_f32_16x16x32_bf16 v[56:59], a[244:247], a[200:203], v[56:59]\n\t"
"v_fma_f32 v93, v95, s52, v193\n\t"
"v_fma_f32 v192, -v192, s66, v94\n\t"
"s_waitcnt lgkmcnt(2)\n\t"
"v_mfma_f32_16x16x32_bf16 v[232:235], v[232:235], v[220:223], 0\n\t"
"v_fma_f32 v94, v96, s52, v194\n\t"
"v_fma_f32 v193, -v193, s66, v95\n\t"
"v_mfma_f32_16x16x32_bf16 v[60:63], a[236:239], a[196:199], v[60:63]\n\t"
"v_fma_f32 v95, v97, s52, v195\n\t"
"v_fma_f32 v194, -v194, s66, v96\n\t"
"v_mfma_f32_16x16x32_bf16 v[60:63], a[240:243], a[200:203], v[60:63]\n\t"
"v_fma_f32 v195, -v195, s66, v97\n\t"
"v_mfma_f32_16x16x32_bf16 v[60:63], a[244:247], a[204:207], v[60:63]\n\t"
"s_waitcnt vmcnt(15)\n\t"
"v_mfma_f32_16x16x32_bf16 v[64:67], a[248:251], a[168:171], v[64:67]\n\t"
"ds_read_b128 a[168:171], v244 offset:9216\n\t"
"v_mfma_f32_16x16x32_bf16 v[64:67], a[252:255], a[172:175], v[64:67]\n\t"
"v_cvt_pk_bf16_f32 v232, v232, v233\n\t"
"v_cvt_pk_bf16_f32 v233, v234, v235\n\t"
"v_mfma_f32_16x16x32_bf16 v[64:67], v[236:239], a[176:179], v[64:67]\n\t"
"s_mov_b32 s20, 258048\n\t"
"buffer_load_dwordx4 a[236:239], v252, s[8:11], s20 offen offset:0 sc1\n\t"
"v_mfma_f32_16x16x32_bf16 v[68:71], a[248:251], a[172:175], v[68:71]\n\t"
"ds_read_b128 a[172:175], v244 offset:9728\n\t"
"v_mfma_f32_16x16x32_bf16 v[68:71], a[252:255], a[176:179], v[68:71]\n\t"
"v_mfma_f32_16x16x32_bf16 v[68:71], v[236:239], a[180:183], v[68:71]\n\t"
"buffer_load_dwordx4 a[240:243], v252, s[8:11], s20 offen offset:1024 sc1\n\t"
"s_waitcnt lgkmcnt(2)\n\t"
"v_mfma_f32_16x16x32_bf16 v[224:227], v[224:227], v[220:223], 0\n\t"
"buffer_load_dwordx4 a[244:247], v252, s[8:11], s20 offen offset:2048 sc1\n\t"
"v_mfma_f32_16x16x32_bf16 v[72:75], a[248:251], a[176:179], v[72:75]\n\t"
"v_mfma_f32_16x16x32_bf16 v[72:75], a[252:255], a[180:183], v[72:75]\n\t"
"v_mfma_f32_16x16x32_bf16 v[72:75], v[236:239], a[184:187], v[72:75]\n\t"
"ds_read_b128 a[176:179], v244 offset:10240\n\t"
"v_mfma_f32_16x16x32_bf16 v[76:79], a[248:251], a[180:183], v[76:79]\n\t"
"ds_read_b128 a[180:183], v244 offset:10752\n\t"
"v_mfma_f32_16x16x32_bf16 v[76:79], a[252:255], a[184:187], v[76:79]\n\t"
"v_cvt_pk_bf16_f32 v234, v224, v225\n\t"
"v_mfma_f32_16x16x32_bf16 v[76:79], v[236:239], a[188:191], v[76:79]\n\t"
"v_cvt_pk_bf16_f32 v235, v226, v227\n\t"
"ds_read_b64_tr_b16 v[224:225], v246 offset:26624\n\t"
"v_mfma_f32_16x16x32_bf16 v[80:83], a[248:251], a[184:187], v[80:83]\n\t"
"ds_read_b64_tr_b16 v[226:227], v246 offset:29696\n\t"
"v_mfma_f32_16x16x32_bf16 v[80:83], a[252:255], a[188:191], v[80:83]\n\t"
"ds_read_b128 a[184:187], v244 offset:11264\n\t"
"v_mfma_f32_16x16x32_bf16 v[80:83], v[236:239], a[192:195], v[80:83]\n\t"
"v_mfma_f32_16x16x32_bf16 v[84:87], a[248:251], a[188:191], v[84:87]\n\t"
"ds_read_b128 a[188:191], v244 offset:11776\n\t"
"v_mfma_f32_16x16x32_bf16 v[84:87], a[252:255], a[192:195], v[84:87]\n\t"
"v_mfma_f32_16x16x32_bf16 v[84:87], v[236:239], a[196:199], v[84:87]\n\t"
"v_mfma_f32_16x16x32_bf16 v[88:91], a[248:251], a[192:195], v[88:91]\n\t"
"ds_read_b128 a[192:195], v244 offset:12288\n\t"
"v_mfma_f32_16x16x32_bf16 v[88:91], a[252:255], a[196:199], v[88:91]\n\t"
"v_mfma_f32_16x16x32_bf16 v[88:91], v[236:239], a[200:203], v[88:91]\n\t"
"s_mov_b32 s20, 270336\n\t"
"buffer_load_dwordx4 v[96:99], v252, s[8:11], s20 offen offset:0 sc1\n\t"
"v_mfma_f32_16x16x32_bf16 v[92:95], a[248:251], a[196:199], v[92:95]\n\t"
"ds_read_b128 a[196:199], v244 offset:12800\n\t"
"v_mfma_f32_16x16x32_bf16 v[92:95], a[252:255], a[200:203], v[92:95]\n\t"
"ds_read_b128 a[200:203], v244 offset:13312\n\t"
"v_mfma_f32_16x16x32_bf16 v[92:95], v[236:239], a[204:207], v[92:95]\n\t"
"buffer_load_dwordx4 a[248:251], v252, s[8:11], s20 offen offset:1024 sc1\n\t"
"s_waitcnt lgkmcnt(5)\n\t"
"v_mfma_f32_16x16x32_bf16 v[224:227], v[224:227], v[220:223], 0\n\t"
"ds_read_b128 a[204:207], v244 offset:13824\n\t"
"s_waitcnt vmcnt(11)\n\t"
"v_mfma_f32_16x16x32_bf16 v[100:103], v[240:243], a[168:171], v[100:103]\n\t"
"v_mfma_f32_16x16x32_bf16 v[100:103], a[208:211], a[172:175], v[100:103]\n\t"
"v_mfma_f32_16x16x32_bf16 v[100:103], v[248:251], a[176:179], v[100:103]\n\t"
"buffer_load_dwordx4 v[236:239], v252, s[8:11], s20 offen offset:2048 sc1\n\t"
"v_mfma_f32_16x16x32_bf16 v[104:107], v[240:243], a[172:175], v[104:107]\n\t"
"v_mfma_f32_16x16x32_bf16 v[104:107], a[208:211], a[176:179], v[104:107]\n\t"
"v_cvt_pk_bf16_f32 v224, v224, v225\n\t"
"v_cvt_pk_bf16_f32 v225, v226, v227\n\t"
"v_mfma_f32_16x16x32_bf16 v[104:107], v[248:251], a[180:183], v[104:107]\n\t"
"s_mov_b32 s20, 282624\n\t"
"buffer_load_dwordx4 a[252:255], v252, s[8:11], s20 offen offset:0 sc1\n\t"
"s_waitcnt lgkmcnt(0)\n\t"
"s_barrier\n\t"
"v_mfma_f32_16x16x32_bf16 v[108:111], v[240:243], a[176:179], v[108:111]\n\t"
"v_mfma_f32_16x16x32_bf16 v[108:111], a[208:211], a[180:183], v[108:111]\n\t"
"v_mfma_f32_16x16x32_bf16 v[108:111], v[248:251], a[184:187], v[108:111]\n\t"
"ds_write2st64_b64 v245, v[228:229], v[230:231] offset0:0 offset1:1\n\t"
"v_mfma_f32_16x16x32_bf16 v[112:115], v[240:243], a[180:183], v[112:115]\n\t"
"ds_read_b64_tr_b16 v[228:229], v246 offset:32768\n\t"
"v_mfma_f32_16x16x32_bf16 v[112:115], a[208:211], a[184:187], v[112:115]\n\t"
"ds_read_b64_tr_b16 v[230:231], v246 offset:35840\n\t"
"v_mfma_f32_16x16x32_bf16 v[112:115], v[248:251], a[188:191], v[112:115]\n\t"
"ds_write2st64_b64 v245, v[232:233], v[234:235] offset0:2 offset1:3\n\t"
"v_mfma_f32_16x16x32_bf16 v[116:119], v[240:243], a[184:187], v[116:119]\n\t"
"ds_read_b64_tr_b16 v[232:233], v246 offset:40448\n\t"
"v_mfma_f32_16x16x32_bf16 v[116:119], a[208:211], a[188:191], v[116:119]\n\t"
"ds_read_b64_tr_b16 v[234:235], v246 offset:43520\n\t"
"v_mfma_f32_16x16x32_bf16 v[116:119], v[248:251], a[192:195], v[116:119]\n\t"
"ds_write_b64 v245, v[224:225] offset:2048\n\t"
"v_mfma_f32_16x16x32_bf16 v[120:123], v[240:243], a[188:191], v[120:123]\n\t"
"ds_read_b64_tr_b16 v[224:225], v246 offset:46592\n\t"
"v_mfma_f32_16x16x32_bf16 v[120:123], a[208:211], a[192:195], v[120:123]\n\t"
"ds_read_b64_tr_b16 v[226:227], v246 offset:49664\n\t"
"v_mfma_f32_16x16x32_bf16 v[120:123], v[248:251], a[196:199], v[120:123]\n\t"
"s_waitcnt lgkmcnt(6)\n\t"
"v_mfma_f32_16x16x32_bf16 v[228:231], v[228:231], v[220:223], 0\n\t"
"s_add_u32 m0, s24, 73088\n\t"
"s_add_u32 s28, s21, 2162688\n\t"
"buffer_load_dwordx4 v247, s[12:15], s28 offen offset:128 nt lds\n\t"
"v_mfma_f32_16x16x32_bf16 v[124:127], v[240:243], a[192:195], v[124:127]\n\t"
"v_mfma_f32_16x16x32_bf16 v[124:127], a[208:211], a[196:199], v[124:127]\n\t"
"v_mfma_f32_16x16x32_bf16 v[124:127], v[248:251], a[200:203], v[124:127]\n\t"
"s_add_u32 m0, s24, 80768\n\t"
"s_add_u32 s28, s21, 2359296\n\t"
"buffer_load_dwordx4 v247, s[12:15], s28 offen offset:128 nt lds\n\t"
"s_waitcnt lgkmcnt(3)\n\t"
"v_mfma_f32_16x16x32_bf16 v[232:235], v[232:235], v[220:223], 0\n\t"
"s_add_u32 m0, s24, 27008\n\t"
"s_add_u32 s28, s21, 786432\n\t"
"buffer_load_dwordx4 v247, s[12:15], s28 offen offset:128 nt lds\n\t"
"v_mfma_f32_16x16x32_bf16 v[128:131], v[240:243], a[196:199], v[128:131]\n\t"
"v_cvt_pk_bf16_f32 v228, v228, v229\n\t"
"v_cvt_pk_bf16_f32 v229, v230, v231\n\t"
"v_mfma_f32_16x16x32_bf16 v[128:131], a[208:211], a[200:203], v[128:131]\n\t"
"v_mfma_f32_16x16x32_bf16 v[128:131], v[248:251], a[204:207], v[128:131]\n\t"
"s_add_u32 m0, s24, 33152\n\t"
"s_add_u32 s28, s21, 983040\n\t"
"buffer_load_dwordx4 v247, s[12:15], s28 offen offset:128 nt lds\n\t"
"s_waitcnt lgkmcnt(0)\n\t"
"v_mfma_f32_16x16x32_bf16 v[224:227], v[224:227], v[220:223], 0\n\t"
"s_add_u32 m0, s24, 40832\n\t"
"s_add_u32 s28, s21, 1179648\n\t"
"buffer_load_dwordx4 v247, s[12:15], s28 offen offset:128 nt lds\n\t"
"s_waitcnt vmcnt(15)\n\t"
"v_mfma_f32_16x16x32_bf16 v[132:135], a[212:215], a[168:171], v[132:135]\n\t"
"v_cvt_pk_bf16_f32 v230, v232, v233\n\t"
"v_cvt_pk_bf16_f32 v231, v234, v235\n\t"
"v_mfma_f32_16x16x32_bf16 v[132:135], a[216:219], a[172:175], v[132:135]\n\t"
"ds_read_b64_tr_b16 v[232:233], v246 offset:52736\n\t"
"v_mfma_f32_16x16x32_bf16 v[132:135], a[220:223], a[176:179], v[132:135]\n\t"
"s_add_u32 m0, s24, 46976\n\t"
"s_add_u32 s28, s21, 1376256\n\t"
"buffer_load_dwordx4 v247, s[12:15], s28 offen offset:128 nt lds\n\t"
"v_mfma_f32_16x16x32_bf16 v[136:139], a[212:215], a[172:175], v[136:139]\n\t"
"ds_read_b64_tr_b16 v[234:235], v246 offset:55808\n\t"
"v_mfma_f32_16x16x32_bf16 v[136:139], a[216:219], a[176:179], v[136:139]\n\t"
"v_cvt_pk_bf16_f32 v224, v224, v225\n\t"
"v_cvt_pk_bf16_f32 v225, v226, v227\n\t"
"v_mfma_f32_16x16x32_bf16 v[136:139], a[220:223], a[180:183], v[136:139]\n\t"
"ds_write2st64_b64 v245, v[228:229], v[230:231] offset0:5 offset1:6\n\t"
"v_mfma_f32_16x16x32_bf16 v[140:143], a[212:215], a[176:179], v[140:143]\n\t"
"v_mfma_f32_16x16x32_bf16 v[140:143], a[216:219], a[180:183], v[140:143]\n\t"
"v_mfma_f32_16x16x32_bf16 v[140:143], a[220:223], a[184:187], v[140:143]\n\t"
"v_mfma_f32_16x16x32_bf16 v[144:147], a[212:215], a[180:183], v[144:147]\n\t"
"v_mfma_f32_16x16x32_bf16 v[144:147], a[216:219], a[184:187], v[144:147]\n\t"
"v_mfma_f32_16x16x32_bf16 v[144:147], a[220:223], a[188:191], v[144:147]\n\t"
"v_mfma_f32_16x16x32_bf16 v[148:151], a[212:215], a[184:187], v[148:151]\n\t"
"v_mfma_f32_16x16x32_bf16 v[148:151], a[216:219], a[188:191], v[148:151]\n\t"
"v_mfma_f32_16x16x32_bf16 v[148:151], a[220:223], a[192:195], v[148:151]\n\t"
"s_waitcnt lgkmcnt(1)\n\t"
"v_mfma_f32_16x16x32_bf16 v[232:235], v[232:235], v[220:223], 0\n\t"
"v_mfma_f32_16x16x32_bf16 v[152:155], a[212:215], a[188:191], v[152:155]\n\t"
"v_mfma_f32_16x16x32_bf16 v[152:155], a[216:219], a[192:195], v[152:155]\n\t"
"v_mfma_f32_16x16x32_bf16 v[152:155], a[220:223], a[196:199], v[152:155]\n\t"
"v_mfma_f32_16x16x32_bf16 v[156:159], a[212:215], a[192:195], v[156:159]\n\t"
"v_mfma_f32_16x16x32_bf16 v[156:159], a[216:219], a[196:199], v[156:159]\n\t"
"s_nop 2\n\t"
"v_cvt_pk_bf16_f32 v226, v232, v233\n\t"
"v_cvt_pk_bf16_f32 v227, v234, v235\n\t"
"v_mfma_f32_16x16x32_bf16 v[156:159], a[220:223], a[200:203], v[156:159]\n\t"
"ds_write2st64_b64 v245, v[224:225], v[226:227] offset0:7 offset1:8\n\t"
"v_mfma_f32_16x16x32_bf16 v[160:163], a[212:215], a[196:199], v[160:163]\n\t"
"v_mfma_f32_16x16x32_bf16 v[160:163], a[216:219], a[200:203], v[160:163]\n\t"
"v_mfma_f32_16x16x32_bf16 v[160:163], a[220:223], a[204:207], v[160:163]\n\t"
"s_waitcnt vmcnt(13)\n\t"
"v_mfma_f32_16x16x32_bf16 v[164:167], a[224:227], a[168:171], v[164:167]\n\t"
"v_mfma_f32_16x16x32_bf16 v[164:167], a[228:231], a[172:175], v[164:167]\n\t"
"v_mfma_f32_16x16x32_bf16 v[164:167], a[232:235], a[176:179], v[164:167]\n\t"
"buffer_load_dwordx4 v[240:243], v252, s[8:11], s20 offen offset:1024 sc1\n\t"
"s_waitcnt lgkmcnt(0)\n\t"
"s_barrier\n\t"
"v_mfma_f32_16x16x32_bf16 v[168:171], a[224:227], a[172:175], v[168:171]\n\t"
"ds_read_b128 a[168:171], v244 offset:0\n\t"
"v_mfma_f32_16x16x32_bf16 v[168:171], a[228:231], a[176:179], v[168:171]\n\t"
"ds_read_b128 a[172:175], v244 offset:512\n\t"
"v_mfma_f32_16x16x32_bf16 v[168:171], a[232:235], a[180:183], v[168:171]\n\t"
"v_mfma_f32_16x16x32_bf16 v[172:175], a[224:227], a[176:179], v[172:175]\n\t"
"ds_read_b128 a[176:179], v244 offset:1024\n\t"
"v_mfma_f32_16x16x32_bf16 v[172:175], a[228:231], a[180:183], v[172:175]\n\t"
"v_mfma_f32_16x16x32_bf16 v[172:175], a[232:235], a[184:187], v[172:175]\n\t"
"v_mfma_f32_16x16x32_bf16 v[176:179], a[224:227], a[180:183], v[176:179]\n\t"
"ds_read_b128 a[180:183], v244 offset:1536\n\t"
"v_mfma_f32_16x16x32_bf16 v[176:179], a[228:231], a[184:187], v[176:179]\n\t"
"v_mfma_f32_16x16x32_bf16 v[176:179], a[232:235], a[188:191], v[176:179]\n\t"
"v_mfma_f32_16x16x32_bf16 v[180:183], a[224:227], a[184:187], v[180:183]\n\t"
"ds_read_b128 a[184:187], v244 offset:2048\n\t"
"v_mfma_f32_16x16x32_bf16 v[180:183], a[228:231], a[188:191], v[180:183]\n\t"
"v_mfma_f32_16x16x32_bf16 v[180:183], a[232:235], a[192:195], v[180:183]\n\t"
"v_mfma_f32_16x16x32_bf16 v[184:187], a[224:227], a[188:191], v[184:187]\n\t"
"ds_read_b128 a[188:191], v244 offset:2560\n\t"
"v_mfma_f32_16x16x32_bf16 v[184:187], a[228:231], a[192:195], v[184:187]\n\t"
"v_mfma_f32_16x16x32_bf16 v[184:187], a[232:235], a[196:199], v[184:187]\n\t"
"v_mfma_f32_16x16x32_bf16 v[188:191], a[224:227], a[192:195], v[188:191]\n\t"
"ds_read_b128 a[192:195], v244 offset:3072\n\t"
"v_mfma_f32_16x16x32_bf16 v[188:191], a[228:231], a[196:199], v[188:191]\n\t"
"v_mfma_f32_16x16x32_bf16 v[188:191], a[232:235], a[200:203], v[188:191]\n\t"
"v_mfma_f32_16x16x32_bf16 v[192:195], a[224:227], a[196:199], v[192:195]\n\t"
"ds_read_b128 a[196:199], v244 offset:3584\n\t"
"v_mfma_f32_16x16x32_bf16 v[192:195], a[228:231], a[200:203], v[192:195]\n\t"
"ds_read_b128 a[200:203], v244 offset:4096\n\t"
"v_mfma_f32_16x16x32_bf16 v[192:195], a[232:235], a[204:207], v[192:195]\n\t"
"Lst4_%=:\n\t"
"s_waitcnt vmcnt(11) lgkmcnt(6)\n\t"
"v_mfma_f32_16x16x32_bf16 v[0:3], a[236:239], a[168:171], v[0:3]\n\t"
"ds_read_b128 a[204:207], v244 offset:4608\n\t"
"v_mfma_f32_16x16x32_bf16 v[0:3], a[240:243], a[172:175], v[0:3]\n\t"
"v_xor_b32 v246, 0xc0, v246\n\t"
"v_mfma_f32_16x16x32_bf16 v[0:3], a[244:247], a[176:179], v[0:3]\n\t"
"s_waitcnt lgkmcnt(6)\n\t"
"v_mfma_f32_16x16x32_bf16 v[4:7], a[236:239], a[172:175], v[4:7]\n\t"
"v_mfma_f32_16x16x32_bf16 v[4:7], a[240:243], a[176:179], v[4:7]\n\t"
"v_mfma_f32_16x16x32_bf16 v[4:7], a[244:247], a[180:183], v[4:7]\n\t"
"s_waitcnt lgkmcnt(5)\n\t"
"v_mfma_f32_16x16x32_bf16 v[8:11], a[236:239], a[176:179], v[8:11]\n\t"
"v_mfma_f32_16x16x32_bf16 v[8:11], a[240:243], a[180:183], v[8:11]\n\t"
"v_mfma_f32_16x16x32_bf16 v[8:11], a[244:247], a[184:187], v[8:11]\n\t"
"s_waitcnt lgkmcnt(4)\n\t"
"v_mfma_f32_16x16x32_bf16 v[12:15], a[236:239], a[180:183], v[12:15]\n\t"
"v_mfma_f32_16x16x32_bf16 v[12:15], a[240:243], a[184:187], v[12:15]\n\t"
"v_mfma_f32_16x16x32_bf16 v[12:15], a[244:247], a[188:191], v[12:15]\n\t"
"buffer_load_dwordx4 a[208:211], v252, s[8:11], s20 offen offset:2048 sc1\n\t"
"s_waitcnt lgkmcnt(3)\n\t"
"v_mfma_f32_16x16x32_bf16 v[16:19], a[236:239], a[184:187], v[16:19]\n\t"
"v_mfma_f32_16x16x32_bf16 v[16:19], a[240:243], a[188:191], v[16:19]\n\t"
"v_mfma_f32_16x16x32_bf16 v[16:19], a[244:247], a[192:195], v[16:19]\n\t"
"ds_read_b64_tr_b16 v[228:229], v246 offset:512\n\t"
"ds_read_b64_tr_b16 v[230:231], v246 offset:3584\n\t"
"s_waitcnt lgkmcnt(4)\n\t"
"v_mfma_f32_16x16x32_bf16 v[20:23], a[236:239], a[188:191], v[20:23]\n\t"
"v_mfma_f32_16x16x32_bf16 v[20:23], a[240:243], a[192:195], v[20:23]\n\t"
"v_mfma_f32_16x16x32_bf16 v[20:23], a[244:247], a[196:199], v[20:23]\n\t"
"s_mov_b32 s20, 294912\n\t"
"buffer_load_dwordx4 v[248:251], v252, s[8:11], s20 offen offset:0 sc1\n\t"
"s_waitcnt lgkmcnt(3)\n\t"
"v_mfma_f32_16x16x32_bf16 v[24:27], a[236:239], a[192:195], v[24:27]\n\t"
"v_mfma_f32_16x16x32_bf16 v[24:27], a[240:243], a[196:199], v[24:27]\n\t"
"v_mfma_f32_16x16x32_bf16 v[24:27], a[244:247], a[200:203], v[24:27]\n\t"
"buffer_load_dwordx4 a[212:215], v252, s[8:11], s20 offen offset:1024 sc1\n\t"
"s_waitcnt lgkmcnt(2)\n\t"
"v_mfma_f32_16x16x32_bf16 v[28:31], a[236:239], a[196:199], v[28:31]\n\t"
"ds_read_b64_tr_b16 v[232:233], v246 offset:6656\n\t"
"v_mfma_f32_16x16x32_bf16 v[28:31], a[240:243], a[200:203], v[28:31]\n\t"
"ds_read_b64_tr_b16 v[234:235], v246 offset:9728\n\t"
"v_mfma_f32_16x16x32_bf16 v[28:31], a[244:247], a[204:207], v[28:31]\n\t"
"s_waitcnt vmcnt(11)\n\t"
"v_mfma_f32_16x16x32_bf16 v[32:35], v[96:99], a[168:171], v[32:35]\n\t"
"v_mfma_f32_16x16x32_bf16 v[32:35], a[248:251], a[172:175], v[32:35]\n\t"
"v_mfma_f32_16x16x32_bf16 v[32:35], v[236:239], a[176:179], v[32:35]\n\t"
"buffer_load_dwordx4 a[216:219], v252, s[8:11], s20 offen offset:2048 sc1\n\t"
"v_mfma_f32_16x16x32_bf16 v[36:39], v[96:99], a[172:175], v[36:39]\n\t"
"v_mfma_f32_16x16x32_bf16 v[36:39], a[248:251], a[176:179], v[36:39]\n\t"
"v_mfma_f32_16x16x32_bf16 v[36:39], v[236:239], a[180:183], v[36:39]\n\t"
"s_mov_b32 s20, 307200\n\t"
"buffer_load_dwordx4 a[220:223], v252, s[8:11], s20 offen offset:0 sc1\n\t"
"s_waitcnt lgkmcnt(2)\n\t"
"v_mfma_f32_16x16x32_bf16 v[228:231], v[228:231], v[220:223], 0\n\t"
"buffer_load_dwordx4 a[224:227], v252, s[8:11], s20 offen offset:1024 sc1\n\t"
"v_mfma_f32_16x16x32_bf16 v[40:43], v[96:99], a[176:179], v[40:43]\n\t"
"ds_read_b64_tr_b16 v[224:225], v246 offset:12800\n\t"
"v_mfma_f32_16x16x32_bf16 v[40:43], a[248:251], a[180:183], v[40:43]\n\t"
"v_mfma_f32_16x16x32_bf16 v[40:43], v[236:239], a[184:187], v[40:43]\n\t"
"ds_read_b64_tr_b16 v[226:227], v246 offset:15872\n\t"
"v_mfma_f32_16x16x32_bf16 v[44:47], v[96:99], a[180:183], v[44:47]\n\t"
"v_mfma_f32_16x16x32_bf16 v[44:47], a[248:251], a[184:187], v[44:47]\n\t"
"v_cvt_pk_bf16_f32 v228, v228, v229\n\t"
"v_cvt_pk_bf16_f32 v229, v230, v231\n\t"
"v_mfma_f32_16x16x32_bf16 v[44:47], v[236:239], a[188:191], v[44:47]\n\t"
"s_waitcnt lgkmcnt(2)\n\t"
"v_mfma_f32_16x16x32_bf16 v[232:235], v[232:235], v[220:223], 0\n\t"
"buffer_load_dwordx4 a[228:231], v252, s[8:11], s20 offen offset:2048 sc1\n\t"
"v_mfma_f32_16x16x32_bf16 v[48:51], v[96:99], a[184:187], v[48:51]\n\t"
"v_mfma_f32_16x16x32_bf16 v[48:51], a[248:251], a[188:191], v[48:51]\n\t"
"v_mfma_f32_16x16x32_bf16 v[48:51], v[236:239], a[192:195], v[48:51]\n\t"
"s_mov_b32 s20, 319488\n\t"
"buffer_load_dwordx4 a[232:235], v252, s[8:11], s20 offen offset:0 sc1\n\t"
"v_mfma_f32_16x16x32_bf16 v[52:55], v[96:99], a[188:191], v[52:55]\n\t"
"v_mfma_f32_16x16x32_bf16 v[52:55], a[248:251], a[192:195], v[52:55]\n\t"
"v_cvt_pk_bf16_f32 v230, v232, v233\n\t"
"v_cvt_pk_bf16_f32 v231, v234, v235\n\t"
"v_mfma_f32_16x16x32_bf16 v[52:55], v[236:239], a[196:199], v[52:55]\n\t"
"buffer_load_dwordx4 a[236:239], v252, s[8:11], s20 offen offset:1024 sc1\n\t"
"v_mfma_f32_16x16x32_bf16 v[56:59], v[96:99], a[192:195], v[56:59]\n\t"
"ds_read_b64_tr_b16 v[232:233], v246 offset:20480\n\t"
"v_mfma_f32_16x16x32_bf16 v[56:59], a[248:251], a[196:199], v[56:59]\n\t"
"ds_read_b64_tr_b16 v[234:235], v246 offset:23552\n\t"
"v_mfma_f32_16x16x32_bf16 v[56:59], v[236:239], a[200:203], v[56:59]\n\t"
"s_waitcnt lgkmcnt(2)\n\t"
"v_mfma_f32_16x16x32_bf16 v[224:227], v[224:227], v[220:223], 0\n\t"
"buffer_load_dwordx4 a[240:243], v252, s[8:11], s20 offen offset:2048 sc1\n\t"
"v_mfma_f32_16x16x32_bf16 v[60:63], v[96:99], a[196:199], v[60:63]\n\t"
"v_mfma_f32_16x16x32_bf16 v[60:63], a[248:251], a[200:203], v[60:63]\n\t"
"v_mfma_f32_16x16x32_bf16 v[60:63], v[236:239], a[204:207], v[60:63]\n\t"
"s_mov_b32 s20, 331776\n\t"
"buffer_load_dwordx4 v[236:239], v252, s[8:11], s20 offen offset:0 sc1\n\t"
"s_waitcnt vmcnt(10)\n\t"
"v_mfma_f32_16x16x32_bf16 v[64:67], a[252:255], a[168:171], v[64:67]\n\t"
"ds_read_b128 a[168:171], v244 offset:9216\n\t"
"v_mfma_f32_16x16x32_bf16 v[64:67], v[240:243], a[172:175], v[64:67]\n\t"
"v_cvt_pk_bf16_f32 v224, v224, v225\n\t"
"v_mfma_f32_16x16x32_bf16 v[64:67], a[208:211], a[176:179], v[64:67]\n\t"
"v_cvt_pk_bf16_f32 v225, v226, v227\n\t"
"v_mfma_f32_16x16x32_bf16 v[68:71], a[252:255], a[172:175], v[68:71]\n\t"
"ds_read_b128 a[172:175], v244 offset:9728\n\t"
"v_mfma_f32_16x16x32_bf16 v[68:71], v[240:243], a[176:179], v[68:71]\n\t"
"v_mfma_f32_16x16x32_bf16 v[68:71], a[208:211], a[180:183], v[68:71]\n\t"
"buffer_load_dwordx4 v[96:99], v252, s[8:11], s20 offen offset:1024 sc1\n\t"
"s_waitcnt lgkmcnt(2)\n\t"
"v_mfma_f32_16x16x32_bf16 v[232:235], v[232:235], v[220:223], 0\n\t"
"v_mfma_f32_16x16x32_bf16 v[72:75], a[252:255], a[176:179], v[72:75]\n\t"
"ds_read_b128 a[176:179], v244 offset:10240\n\t"
"v_mfma_f32_16x16x32_bf16 v[72:75], v[240:243], a[180:183], v[72:75]\n\t"
"v_mfma_f32_16x16x32_bf16 v[72:75], a[208:211], a[184:187], v[72:75]\n\t"
"buffer_load_dwordx4 a[244:247], v252, s[8:11], s20 offen offset:2048 sc1\n\t"
"v_mfma_f32_16x16x32_bf16 v[76:79], a[252:255], a[180:183], v[76:79]\n\t"
"ds_read_b128 a[180:183], v244 offset:10752\n\t"
"v_mfma_f32_16x16x32_bf16 v[76:79], v[240:243], a[184:187], v[76:79]\n\t"
"v_cvt_pk_bf16_f32 v226, v232, v233\n\t"
"v_cvt_pk_bf16_f32 v227, v234, v235\n\t"
"v_mfma_f32_16x16x32_bf16 v[76:79], a[208:211], a[188:191], v[76:79]\n\t"
"s_mov_b32 s20, 344064\n\t"
"buffer_load_dwordx4 a[248:251], v252, s[8:11], s20 offen offset:0 sc1\n\t"
"v_mfma_f32_16x16x32_bf16 v[80:83], a[252:255], a[184:187], v[80:83]\n\t"
"ds_read_b64_tr_b16 v[232:233], v246 offset:26624\n\t"
"v_mfma_f32_16x16x32_bf16 v[80:83], v[240:243], a[188:191], v[80:83]\n\t"
"ds_read_b64_tr_b16 v[234:235], v246 offset:29696\n\t"
"v_mfma_f32_16x16x32_bf16 v[80:83], a[208:211], a[192:195], v[80:83]\n\t"
"ds_read_b128 a[184:187], v244 offset:11264\n\t"
"v_mfma_f32_16x16x32_bf16 v[84:87], a[252:255], a[188:191], v[84:87]\n\t"
"ds_read_b128 a[188:191], v244 offset:11776\n\t"
"v_mfma_f32_16x16x32_bf16 v[84:87], v[240:243], a[192:195], v[84:87]\n\t"
"v_mfma_f32_16x16x32_bf16 v[84:87], a[208:211], a[196:199], v[84:87]\n\t"
"v_mfma_f32_16x16x32_bf16 v[88:91], a[252:255], a[192:195], v[88:91]\n\t"
"ds_read_b128 a[192:195], v244 offset:12288\n\t"
"v_mfma_f32_16x16x32_bf16 v[88:91], v[240:243], a[196:199], v[88:91]\n\t"
"v_mfma_f32_16x16x32_bf16 v[88:91], a[208:211], a[200:203], v[88:91]\n\t"
"v_mfma_f32_16x16x32_bf16 v[92:95], a[252:255], a[196:199], v[92:95]\n\t"
"ds_read_b128 a[196:199], v244 offset:12800\n\t"
"v_mfma_f32_16x16x32_bf16 v[92:95], v[240:243], a[200:203], v[92:95]\n\t"
"ds_read_b128 a[200:203], v244 offset:13312\n\t"
"v_mfma_f32_16x16x32_bf16 v[92:95], a[208:211], a[204:207], v[92:95]\n\t"
"buffer_load_dwordx4 a[252:255], v252, s[8:11], s20 offen offset:1024 sc1\n\t"
"s_waitcnt lgkmcnt(5)\n\t"
"v_mfma_f32_16x16x32_bf16 v[232:235], v[232:235], v[220:223], 0\n\t"
"ds_read_b128 a[204:207], v244 offset:13824\n\t"
"s_waitcnt vmcnt(11)\n\t"
"v_mfma_f32_16x16x32_bf16 v[100:103], v[248:251], a[168:171], v[100:103]\n\t"
"v_mfma_f32_16x16x32_bf16 v[100:103], a[212:215], a[172:175], v[100:103]\n\t"
"v_mfma_f32_16x16x32_bf16 v[100:103], a[216:219], a[176:179], v[100:103]\n\t"
"buffer_load_dwordx4 v[240:243], v252, s[8:11], s20 offen offset:2048 sc1\n\t"
"v_mfma_f32_16x16x32_bf16 v[104:107], v[248:251], a[172:175], v[104:107]\n\t"
"v_mfma_f32_16x16x32_bf16 v[104:107], a[212:215], a[176:179], v[104:107]\n\t"
"v_cvt_pk_bf16_f32 v232, v232, v233\n\t"
"v_cvt_pk_bf16_f32 v233, v234, v235\n\t"
"v_mfma_f32_16x16x32_bf16 v[104:107], a[216:219], a[180:183], v[104:107]\n\t"
"s_mov_b32 s20, 356352\n\t"
"buffer_load_dwordx4 a[208:211], v252, s[8:11], s20 offen offset:0 sc1\n\t"
"s_waitcnt lgkmcnt(0)\n\t"
"s_barrier\n\t"
"v_mfma_f32_16x16x32_bf16 v[108:111], v[248:251], a[176:179], v[108:111]\n\t"
"v_mfma_f32_16x16x32_bf16 v[108:111], a[212:215], a[180:183], v[108:111]\n\t"
"v_mfma_f32_16x16x32_bf16 v[108:111], a[216:219], a[184:187], v[108:111]\n\t"
"ds_write2st64_b64 v245, v[228:229], v[230:231] offset0:0 offset1:1\n\t"
"v_mfma_f32_16x16x32_bf16 v[112:115], v[248:251], a[180:183], v[112:115]\n\t"
"ds_read_b64_tr_b16 v[228:229], v246 offset:32768\n\t"
"v_mfma_f32_16x16x32_bf16 v[112:115], a[212:215], a[184:187], v[112:115]\n\t"
"ds_read_b64_tr_b16 v[230:231], v246 offset:35840\n\t"
"v_mfma_f32_16x16x32_bf16 v[112:115], a[216:219], a[188:191], v[112:115]\n\t"
"ds_write2st64_b64 v245, v[224:225], v[226:227] offset0:2 offset1:3\n\t"
"v_mfma_f32_16x16x32_bf16 v[116:119], v[248:251], a[184:187], v[116:119]\n\t"
"ds_read_b64_tr_b16 v[224:225], v246 offset:40448\n\t"
"v_mfma_f32_16x16x32_bf16 v[116:119], a[212:215], a[188:191], v[116:119]\n\t"
"ds_read_b64_tr_b16 v[226:227], v246 offset:43520\n\t"
"v_mfma_f32_16x16x32_bf16 v[116:119], a[216:219], a[192:195], v[116:119]\n\t"
"ds_write_b64 v245, v[232:233] offset:2048\n\t"
"v_mfma_f32_16x16x32_bf16 v[120:123], v[248:251], a[188:191], v[120:123]\n\t"
"ds_read_b64_tr_b16 v[232:233], v246 offset:46592\n\t"
"v_mfma_f32_16x16x32_bf16 v[120:123], a[212:215], a[192:195], v[120:123]\n\t"
"ds_read_b64_tr_b16 v[234:235], v246 offset:49664\n\t"
"v_mfma_f32_16x16x32_bf16 v[120:123], a[216:219], a[196:199], v[120:123]\n\t"
"s_waitcnt lgkmcnt(6)\n\t"
"v_mfma_f32_16x16x32_bf16 v[228:231], v[228:231], v[220:223], 0\n\t"
"v_mfma_f32_16x16x32_bf16 v[124:127], v[248:251], a[192:195], v[124:127]\n\t"
"v_mfma_f32_16x16x32_bf16 v[124:127], a[212:215], a[196:199], v[124:127]\n\t"
"v_mfma_f32_16x16x32_bf16 v[124:127], a[216:219], a[200:203], v[124:127]\n\t"
"s_waitcnt lgkmcnt(3)\n\t"
"v_mfma_f32_16x16x32_bf16 v[224:227], v[224:227], v[220:223], 0\n\t"
"v_mfma_f32_16x16x32_bf16 v[128:131], v[248:251], a[196:199], v[128:131]\n\t"
"s_nop 1\n\t"
"v_cvt_pk_bf16_f32 v228, v228, v229\n\t"
"v_cvt_pk_bf16_f32 v229, v230, v231\n\t"
"v_mfma_f32_16x16x32_bf16 v[128:131], a[212:215], a[200:203], v[128:131]\n\t"
"v_mfma_f32_16x16x32_bf16 v[128:131], a[216:219], a[204:207], v[128:131]\n\t"
"buffer_load_dwordx4 v[248:251], v252, s[8:11], s20 offen offset:1024 sc1\n\t"
"s_waitcnt lgkmcnt(0)\n\t"
"v_mfma_f32_16x16x32_bf16 v[232:235], v[232:235], v[220:223], 0\n\t"
"buffer_load_dwordx4 a[212:215], v252, s[8:11], s20 offen offset:2048 sc1\n\t"
"s_waitcnt vmcnt(12)\n\t"
"v_mfma_f32_16x16x32_bf16 v[132:135], a[220:223], a[168:171], v[132:135]\n\t"
"v_cvt_pk_bf16_f32 v230, v224, v225\n\t"
"v_cvt_pk_bf16_f32 v231, v226, v227\n\t"
"v_mfma_f32_16x16x32_bf16 v[132:135], a[224:227], a[172:175], v[132:135]\n\t"
"ds_read_b64_tr_b16 v[224:225], v246 offset:52736\n\t"
"v_mfma_f32_16x16x32_bf16 v[132:135], a[228:231], a[176:179], v[132:135]\n\t"
"s_mov_b32 s20, 368640\n\t"
"buffer_load_dwordx4 a[216:219], v252, s[8:11], s20 offen offset:0 sc1\n\t"
"v_mfma_f32_16x16x32_bf16 v[136:139], a[220:223], a[172:175], v[136:139]\n\t"
"ds_read_b64_tr_b16 v[226:227], v246 offset:55808\n\t"
"v_mfma_f32_16x16x32_bf16 v[136:139], a[224:227], a[176:179], v[136:139]\n\t"
"v_cvt_pk_bf16_f32 v232, v232, v233\n\t"
"v_cvt_pk_bf16_f32 v233, v234, v235\n\t"
"v_mfma_f32_16x16x32_bf16 v[136:139], a[228:231], a[180:183], v[136:139]\n\t"
"s_add_u32 m0, s24, 53120\n\t"
"s_add_u32 s28, s21, 1572864\n\t"
"buffer_load_dwordx4 v247, s[12:15], s28 offen offset:128 nt lds\n\t"
"s_add_u32 m0, s24, 86912\n\t"
"s_add_u32 s28, s21, 2555904\n\t"
"buffer_load_dwordx4 v247, s[12:15], s28 offen offset:128 nt lds\n\t"
"v_mfma_f32_16x16x32_bf16 v[140:143], a[220:223], a[176:179], v[140:143]\n\t"
"v_mfma_f32_16x16x32_bf16 v[140:143], a[224:227], a[180:183], v[140:143]\n\t"
"v_mfma_f32_16x16x32_bf16 v[140:143], a[228:231], a[184:187], v[140:143]\n\t"
"ds_write2st64_b64 v245, v[228:229], v[230:231] offset0:5 offset1:6\n\t"
"s_add_u32 m0, s24, 93056\n\t"
"s_add_u32 s28, s21, 2752512\n\t"
"buffer_load_dwordx4 v247, s[12:15], s28 offen offset:128 nt lds\n\t"
"v_mfma_f32_16x16x32_bf16 v[144:147], a[220:223], a[180:183], v[144:147]\n\t"
"v_mfma_f32_16x16x32_bf16 v[144:147], a[224:227], a[184:187], v[144:147]\n\t"
"v_mfma_f32_16x16x32_bf16 v[144:147], a[228:231], a[188:191], v[144:147]\n\t"
"s_add_u32 m0, s24, 100736\n\t"
"s_add_u32 s28, s21, 2949120\n\t"
"buffer_load_dwordx4 v247, s[12:15], s28 offen offset:128 nt lds\n\t"
"s_add_u32 m0, s24, 106880\n\t"
"s_add_u32 s28, s21, 3145728\n\t"
"buffer_load_dwordx4 v247, s[12:15], s28 offen offset:128 nt lds\n\t"
"v_mfma_f32_16x16x32_bf16 v[148:151], a[220:223], a[184:187], v[148:151]\n\t"
"v_mfma_f32_16x16x32_bf16 v[148:151], a[224:227], a[188:191], v[148:151]\n\t"
"v_mfma_f32_16x16x32_bf16 v[148:151], a[228:231], a[192:195], v[148:151]\n\t"
"s_add_u32 m0, s24, 113024\n\t"
"s_add_u32 s28, s21, 3342336\n\t"
"buffer_load_dwordx4 v247, s[12:15], s28 offen offset:128 nt lds\n\t"
"s_waitcnt lgkmcnt(1)\n\t"
"v_mfma_f32_16x16x32_bf16 v[224:227], v[224:227], v[220:223], 0\n\t"
"s_add_u32 m0, s25, 896\n\t"
"s_nop 0\n\t"
"buffer_load_dwordx4 v254, s[12:15], 0 offen offset:128 nt lds\n\t"
"v_mfma_f32_16x16x32_bf16 v[152:155], a[220:223], a[188:191], v[152:155]\n\t"
"v_mfma_f32_16x16x32_bf16 v[152:155], a[224:227], a[192:195], v[152:155]\n\t"
"v_mfma_f32_16x16x32_bf16 v[152:155], a[228:231], a[196:199], v[152:155]\n\t"
"s_add_u32 m0, s26, 896\n\t"
"s_mov_b64 exec, s[36:37]\n\t"
"buffer_load_dwordx4 v254, s[12:15], s27 offen offset:128 nt lds\n\t"
"s_mov_b64 exec, -1\n\t"
"v_mfma_f32_16x16x32_bf16 v[156:159], a[220:223], a[192:195], v[156:159]\n\t"
"v_mfma_f32_16x16x32_bf16 v[156:159], a[224:227], a[196:199], v[156:159]\n\t"
"v_cvt_pk_bf16_f32 v234, v224, v225\n\t"
"v_cvt_pk_bf16_f32 v235, v226, v227\n\t"
"v_mfma_f32_16x16x32_bf16 v[156:159], a[228:231], a[200:203], v[156:159]\n\t"
"ds_write2st64_b64 v245, v[232:233], v[234:235] offset0:7 offset1:8\n\t"
"v_mfma_f32_16x16x32_bf16 v[160:163], a[220:223], a[196:199], v[160:163]\n\t"
"v_mfma_f32_16x16x32_bf16 v[160:163], a[224:227], a[200:203], v[160:163]\n\t"
"v_mfma_f32_16x16x32_bf16 v[160:163], a[228:231], a[204:207], v[160:163]\n\t"
"s_waitcnt vmcnt(18)\n\t"
"v_mfma_f32_16x16x32_bf16 v[164:167], a[232:235], a[168:171], v[164:167]\n\t"
"v_mfma_f32_16x16x32_bf16 v[164:167], a[236:239], a[172:175], v[164:167]\n\t"
"v_mfma_f32_16x16x32_bf16 v[164:167], a[240:243], a[176:179], v[164:167]\n\t"
"s_waitcnt lgkmcnt(0)\n\t"
"s_barrier\n\t"
"v_mfma_f32_16x16x32_bf16 v[168:171], a[232:235], a[172:175], v[168:171]\n\t"
"ds_read_b128 a[168:171], v244 offset:0\n\t"
"v_mfma_f32_16x16x32_bf16 v[168:171], a[236:239], a[176:179], v[168:171]\n\t"
"ds_read_b128 a[172:175], v244 offset:512\n\t"
"v_mfma_f32_16x16x32_bf16 v[168:171], a[240:243], a[180:183], v[168:171]\n\t"
"v_mfma_f32_16x16x32_bf16 v[172:175], a[232:235], a[176:179], v[172:175]\n\t"
"ds_read_b128 a[176:179], v244 offset:1024\n\t"
"v_mfma_f32_16x16x32_bf16 v[172:175], a[236:239], a[180:183], v[172:175]\n\t"
"v_mfma_f32_16x16x32_bf16 v[172:175], a[240:243], a[184:187], v[172:175]\n\t"
"v_mfma_f32_16x16x32_bf16 v[176:179], a[232:235], a[180:183], v[176:179]\n\t"
"ds_read_b128 a[180:183], v244 offset:1536\n\t"
"v_mfma_f32_16x16x32_bf16 v[176:179], a[236:239], a[184:187], v[176:179]\n\t"
"v_mfma_f32_16x16x32_bf16 v[176:179], a[240:243], a[188:191], v[176:179]\n\t"
"v_mfma_f32_16x16x32_bf16 v[180:183], a[232:235], a[184:187], v[180:183]\n\t"
"ds_read_b128 a[184:187], v244 offset:2048\n\t"
"v_mfma_f32_16x16x32_bf16 v[180:183], a[236:239], a[188:191], v[180:183]\n\t"
"v_mfma_f32_16x16x32_bf16 v[180:183], a[240:243], a[192:195], v[180:183]\n\t"
"v_mfma_f32_16x16x32_bf16 v[184:187], a[232:235], a[188:191], v[184:187]\n\t"
"ds_read_b128 a[188:191], v244 offset:2560\n\t"
"v_mfma_f32_16x16x32_bf16 v[184:187], a[236:239], a[192:195], v[184:187]\n\t"
"v_mfma_f32_16x16x32_bf16 v[184:187], a[240:243], a[196:199], v[184:187]\n\t"
"v_mfma_f32_16x16x32_bf16 v[188:191], a[232:235], a[192:195], v[188:191]\n\t"
"ds_read_b128 a[192:195], v244 offset:3072\n\t"
"v_mfma_f32_16x16x32_bf16 v[188:191], a[236:239], a[196:199], v[188:191]\n\t"
"v_mfma_f32_16x16x32_bf16 v[188:191], a[240:243], a[200:203], v[188:191]\n\t"
"v_mfma_f32_16x16x32_bf16 v[192:195], a[232:235], a[196:199], v[192:195]\n\t"
"ds_read_b128 a[196:199], v244 offset:3584\n\t"
"v_mfma_f32_16x16x32_bf16 v[192:195], a[236:239], a[200:203], v[192:195]\n\t"
"ds_read_b128 a[200:203], v244 offset:4096\n\t"
"v_mfma_f32_16x16x32_bf16 v[192:195], a[240:243], a[204:207], v[192:195]\n\t"
"Lst5_%=:\n\t"
"s_waitcnt vmcnt(15) lgkmcnt(6)\n\t"
"v_mfma_f32_16x16x32_bf16 v[196:199], v[236:239], a[168:171], v[196:199]\n\t"
"ds_read_b128 a[204:207], v244 offset:4608\n\t"
"v_mfma_f32_16x16x32_bf16 v[196:199], v[96:99], a[172:175], v[196:199]\n\t"
"v_mfma_f32_16x16x32_bf16 v[196:199], a[244:247], a[176:179], v[196:199]\n\t"
"s_waitcnt lgkmcnt(6)\n\t"
"v_mfma_f32_16x16x32_bf16 v[204:207], v[236:239], a[172:175], v[204:207]\n\t"
"v_mfma_f32_16x16x32_bf16 v[204:207], v[96:99], a[176:179], v[204:207]\n\t"
"v_mfma_f32_16x16x32_bf16 v[204:207], a[244:247], a[180:183], v[204:207]\n\t"
"s_waitcnt lgkmcnt(5)\n\t"
"v_mfma_f32_16x16x32_bf16 v[212:215], v[236:239], a[176:179], v[212:215]\n\t"
"v_mfma_f32_16x16x32_bf16 v[212:215], v[96:99], a[180:183], v[212:215]\n\t"
"v_mfma_f32_16x16x32_bf16 v[212:215], a[244:247], a[184:187], v[212:215]\n\t"
"s_waitcnt lgkmcnt(4)\n\t"
"v_mfma_f32_16x16x32_bf16 a[0:3], v[236:239], a[180:183], a[0:3]\n\t"
"v_mfma_f32_16x16x32_bf16 a[0:3], v[96:99], a[184:187], a[0:3]\n\t"
"v_mfma_f32_16x16x32_bf16 a[0:3], a[244:247], a[188:191], a[0:3]\n\t"
"buffer_load_dwordx4 a[220:223], v252, s[8:11], s20 offen offset:1024 sc1\n\t"
"s_waitcnt lgkmcnt(3)\n\t"
"v_mfma_f32_16x16x32_bf16 a[8:11], v[236:239], a[184:187], a[8:11]\n\t"
"v_mfma_f32_16x16x32_bf16 a[8:11], v[96:99], a[188:191], a[8:11]\n\t"
"v_mfma_f32_16x16x32_bf16 a[8:11], a[244:247], a[192:195], a[8:11]\n\t"
"s_waitcnt lgkmcnt(2)\n\t"
"v_mfma_f32_16x16x32_bf16 a[16:19], v[236:239], a[188:191], a[16:19]\n\t"
"v_mfma_f32_16x16x32_bf16 a[16:19], v[96:99], a[192:195], a[16:19]\n\t"
"v_mfma_f32_16x16x32_bf16 a[16:19], a[244:247], a[196:199], a[16:19]\n\t"
"s_waitcnt lgkmcnt(1)\n\t"
"v_mfma_f32_16x16x32_bf16 a[24:27], v[236:239], a[192:195], a[24:27]\n\t"
"v_mfma_f32_16x16x32_bf16 a[24:27], v[96:99], a[196:199], a[24:27]\n\t"
"v_mfma_f32_16x16x32_bf16 a[24:27], a[244:247], a[200:203], a[24:27]\n\t"
"s_waitcnt lgkmcnt(0)\n\t"
"v_mfma_f32_16x16x32_bf16 a[32:35], v[236:239], a[196:199], a[32:35]\n\t"
"v_mfma_f32_16x16x32_bf16 a[32:35], v[96:99], a[200:203], a[32:35]\n\t"
"v_mfma_f32_16x16x32_bf16 a[32:35], a[244:247], a[204:207], a[32:35]\n\t"
"s_waitcnt vmcnt(13)\n\t"
"v_mfma_f32_16x16x32_bf16 a[40:43], a[248:251], a[168:171], a[40:43]\n\t"
"v_mfma_f32_16x16x32_bf16 a[40:43], a[252:255], a[172:175], a[40:43]\n\t"
"v_mfma_f32_16x16x32_bf16 a[40:43], v[240:243], a[176:179], a[40:43]\n\t"
"ds_read_b64_tr_b16 v[236:237], v246 offset:1024\n\t"
"ds_read_b64_tr_b16 v[238:239], v246 offset:4096\n\t"
"v_mfma_f32_16x16x32_bf16 a[48:51], a[248:251], a[172:175], a[48:51]\n\t"
"v_mfma_f32_16x16x32_bf16 a[48:51], a[252:255], a[176:179], a[48:51]\n\t"
"v_mfma_f32_16x16x32_bf16 a[48:51], v[240:243], a[180:183], a[48:51]\n\t"
"v_mfma_f32_16x16x32_bf16 a[56:59], a[248:251], a[176:179], a[56:59]\n\t"
"v_mfma_f32_16x16x32_bf16 a[56:59], a[252:255], a[180:183], a[56:59]\n\t"
"v_mfma_f32_16x16x32_bf16 a[56:59], v[240:243], a[184:187], a[56:59]\n\t"
"v_mfma_f32_16x16x32_bf16 a[64:67], a[248:251], a[180:183], a[64:67]\n\t"
"ds_read_b64_tr_b16 v[228:229], v246 offset:7168\n\t"
"v_mfma_f32_16x16x32_bf16 a[64:67], a[252:255], a[184:187], a[64:67]\n\t"
"ds_read_b64_tr_b16 v[230:231], v246 offset:10240\n\t"
"v_mfma_f32_16x16x32_bf16 a[64:67], v[240:243], a[188:191], a[64:67]\n\t"
"v_mfma_f32_16x16x32_bf16 a[72:75], a[248:251], a[184:187], a[72:75]\n\t"
"v_mfma_f32_16x16x32_bf16 a[72:75], a[252:255], a[188:191], a[72:75]\n\t"
"v_mfma_f32_16x16x32_bf16 a[72:75], v[240:243], a[192:195], a[72:75]\n\t"
"buffer_load_dwordx4 v[96:99], v252, s[8:11], s20 offen offset:2048 sc1\n\t"
"v_mfma_f32_16x16x32_bf16 a[80:83], a[248:251], a[188:191], a[80:83]\n\t"
"v_mfma_f32_16x16x32_bf16 a[80:83], a[252:255], a[192:195], a[80:83]\n\t"
"v_mfma_f32_16x16x32_bf16 a[80:83], v[240:243], a[196:199], a[80:83]\n\t"
"s_mov_b32 s20, 380928\n\t"
"buffer_load_dwordx4 a[224:227], v252, s[8:11], s20 offen offset:0 sc1\n\t"
"s_waitcnt lgkmcnt(2)\n\t"
"v_mfma_f32_16x16x32_bf16 v[236:239], v[236:239], v[220:223], 0\n\t"
"v_mfma_f32_16x16x32_bf16 a[88:91], a[248:251], a[192:195], a[88:91]\n\t"
"ds_read_b64_tr_b16 v[224:225], v246 offset:13312\n\t"
"v_mfma_f32_16x16x32_bf16 a[88:91], a[252:255], a[196:199], a[88:91]\n\t"
"ds_read_b64_tr_b16 v[226:227], v246 offset:16384\n\t"
"v_mfma_f32_16x16x32_bf16 a[88:91], v[240:243], a[200:203], a[88:91]\n\t"
"v_mfma_f32_16x16x32_bf16 a[96:99], a[248:251], a[196:199], a[96:99]\n\t"
"v_mfma_f32_16x16x32_bf16 a[96:99], a[252:255], a[200:203], a[96:99]\n\t"
"s_nop 0\n\t"
"v_cvt_pk_bf16_f32 v236, v236, v237\n\t"
"v_cvt_pk_bf16_f32 v237, v238, v239\n\t"
"v_mfma_f32_16x16x32_bf16 a[96:99], v[240:243], a[204:207], a[96:99]\n\t"
"buffer_load_dwordx4 a[228:231], v252, s[8:11], s20 offen offset:1024 sc1\n\t"
"s_waitcnt lgkmcnt(2)\n\t"
"v_mfma_f32_16x16x32_bf16 v[228:231], v[228:231], v[220:223], 0\n\t"
"buffer_load_dwordx4 a[232:235], v252, s[8:11], s20 offen offset:2048 sc1\n\t"
"s_waitcnt vmcnt(14)\n\t"
"v_mfma_f32_16x16x32_bf16 a[104:107], a[208:211], a[168:171], a[104:107]\n\t"
"ds_read_b64_tr_b16 v[232:233], v246 offset:20992\n\t"
"v_mfma_f32_16x16x32_bf16 a[104:107], v[248:251], a[172:175], a[104:107]\n\t"
"ds_read_b64_tr_b16 v[234:235], v246 offset:24064\n\t"
"v_mfma_f32_16x16x32_bf16 a[104:107], a[212:215], a[176:179], a[104:107]\n\t"
"ds_read_b128 a[168:171], v244 offset:9216\n\t"
"v_mfma_f32_16x16x32_bf16 a[112:115], a[208:211], a[172:175], a[112:115]\n\t"
"ds_read_b128 a[172:175], v244 offset:9728\n\t"
"v_mfma_f32_16x16x32_bf16 a[112:115], v[248:251], a[176:179], a[112:115]\n\t"
"v_cvt_pk_bf16_f32 v228, v228, v229\n\t"
"v_mfma_f32_16x16x32_bf16 a[112:115], a[212:215], a[180:183], a[112:115]\n\t"
"s_mov_b32 s20, 393216\n\t"
"buffer_load_dwordx4 a[236:239], v252, s[8:11], s20 offen offset:0 sc1\n\t"
"v_mfma_f32_16x16x32_bf16 a[120:123], a[208:211], a[176:179], a[120:123]\n\t"
"v_cvt_pk_bf16_f32 v229, v230, v231\n\t"
"v_mfma_f32_16x16x32_bf16 a[120:123], v[248:251], a[180:183], a[120:123]\n\t"
"ds_read_b128 a[176:179], v244 offset:10240\n\t"
"v_mfma_f32_16x16x32_bf16 a[120:123], a[212:215], a[184:187], a[120:123]\n\t"
"buffer_load_dwordx4 a[240:243], v252, s[8:11], s20 offen offset:1024 sc1\n\t"
"s_waitcnt lgkmcnt(5)\n\t"
"v_mfma_f32_16x16x32_bf16 v[224:227], v[224:227], v[220:223], 0\n\t"
"v_mfma_f32_16x16x32_bf16 a[128:131], a[208:211], a[180:183], a[128:131]\n\t"
"ds_read_b128 a[180:183], v244 offset:10752\n\t"
"v_mfma_f32_16x16x32_bf16 a[128:131], v[248:251], a[184:187], a[128:131]\n\t"
"v_mfma_f32_16x16x32_bf16 a[128:131], a[212:215], a[188:191], a[128:131]\n\t"
"buffer_load_dwordx4 a[244:247], v252, s[8:11], s20 offen offset:2048 sc1\n\t"
"v_mfma_f32_16x16x32_bf16 a[136:139], a[208:211], a[184:187], a[136:139]\n\t"
"ds_read_b128 a[184:187], v244 offset:11264\n\t"
"v_mfma_f32_16x16x32_bf16 a[136:139], v[248:251], a[188:191], a[136:139]\n\t"
"v_cvt_pk_bf16_f32 v224, v224, v225\n\t"
"v_cvt_pk_bf16_f32 v225, v226, v227\n\t"
"v_mfma_f32_16x16x32_bf16 a[136:139], a[212:215], a[192:195], a[136:139]\n\t"
"s_waitcnt lgkmcnt(5)\n\t"
"v_mfma_f32_16x16x32_bf16 v[232:235], v[232:235], v[220:223], 0\n\t"
"s_mov_b32 s20, 552960\n\t"
"buffer_load_dwordx4 a[248:251], v252, s[8:11], s20 offen offset:0 sc1\n\t"
"v_mfma_f32_16x16x32_bf16 a[144:147], a[208:211], a[188:191], a[144:147]\n\t"
"ds_read_b128 a[188:191], v244 offset:11776\n\t"
"v_mfma_f32_16x16x32_bf16 a[144:147], v[248:251], a[192:195], a[144:147]\n\t"
"v_mfma_f32_16x16x32_bf16 a[144:147], a[212:215], a[196:199], a[144:147]\n\t"
"buffer_load_dwordx4 a[252:255], v252, s[8:11], s20 offen offset:1024 sc1\n\t"
"v_mfma_f32_16x16x32_bf16 a[152:155], a[208:211], a[192:195], a[152:155]\n\t"
"ds_read_b128 a[192:195], v244 offset:12288\n\t"
"v_mfma_f32_16x16x32_bf16 a[152:155], v[248:251], a[196:199], a[152:155]\n\t"
"v_cvt_pk_bf16_f32 v232, v232, v233\n\t"
"v_mfma_f32_16x16x32_bf16 a[152:155], a[212:215], a[200:203], a[152:155]\n\t"
"v_cvt_pk_bf16_f32 v233, v234, v235\n\t"
"v_mfma_f32_16x16x32_bf16 a[160:163], a[208:211], a[196:199], a[160:163]\n\t"
"ds_read_b128 a[196:199], v244 offset:12800\n\t"
"v_mfma_f32_16x16x32_bf16 a[160:163], v[248:251], a[200:203], a[160:163]\n\t"
"ds_read_b128 a[200:203], v244 offset:13312\n\t"
"v_mfma_f32_16x16x32_bf16 a[160:163], a[212:215], a[204:207], a[160:163]\n\t"
"ds_read_b128 a[204:207], v244 offset:13824\n\t"
"s_waitcnt vmcnt(8) lgkmcnt(7)\n\t"
"v_mfma_f32_16x16x32_bf16 v[200:203], a[216:219], a[168:171], v[200:203]\n\t"
"v_mfma_f32_16x16x32_bf16 v[200:203], a[220:223], a[172:175], v[200:203]\n\t"
"v_mfma_f32_16x16x32_bf16 v[200:203], v[96:99], a[176:179], v[200:203]\n\t"
"buffer_load_dwordx4 v[248:251], v252, s[8:11], s20 offen offset:2048 sc1\n\t"
"s_waitcnt lgkmcnt(6)\n\t"
"v_mfma_f32_16x16x32_bf16 v[208:211], a[216:219], a[172:175], v[208:211]\n\t"
"v_mfma_f32_16x16x32_bf16 v[208:211], a[220:223], a[176:179], v[208:211]\n\t"
"v_mfma_f32_16x16x32_bf16 v[208:211], v[96:99], a[180:183], v[208:211]\n\t"
"s_mov_b32 s20, 565248\n\t"
"buffer_load_dwordx4 a[212:215], v252, s[8:11], s20 offen offset:0 sc1\n\t"
"s_waitcnt lgkmcnt(0)\n\t"
"s_barrier\n\t"
"v_mfma_f32_16x16x32_bf16 v[216:219], a[216:219], a[176:179], v[216:219]\n\t"
"v_mfma_f32_16x16x32_bf16 v[216:219], a[220:223], a[180:183], v[216:219]\n\t"
"v_mfma_f32_16x16x32_bf16 v[216:219], v[96:99], a[184:187], v[216:219]\n\t"
"ds_write_b64 v245, v[236:237] offset:0\n\t"
"ds_write_b64 v245, v[228:229] offset:512\n\t"
"v_mfma_f32_16x16x32_bf16 a[4:7], a[216:219], a[180:183], a[4:7]\n\t"
"ds_read_b64_tr_b16 v[236:237], v246 offset:27136\n\t"
"v_mfma_f32_16x16x32_bf16 a[4:7], a[220:223], a[184:187], a[4:7]\n\t"
"ds_read_b64_tr_b16 v[238:239], v246 offset:30208\n\t"
"v_mfma_f32_16x16x32_bf16 a[4:7], v[96:99], a[188:191], a[4:7]\n\t"
"ds_write_b64 v245, v[224:225] offset:1024\n\t"
"v_mfma_f32_16x16x32_bf16 a[12:15], a[216:219], a[184:187], a[12:15]\n\t"
"ds_read_b64_tr_b16 v[228:229], v246 offset:33280\n\t"
"v_mfma_f32_16x16x32_bf16 a[12:15], a[220:223], a[188:191], a[12:15]\n\t"
"ds_read_b64_tr_b16 v[230:231], v246 offset:36352\n\t"
"v_mfma_f32_16x16x32_bf16 a[12:15], v[96:99], a[192:195], a[12:15]\n\t"
"ds_write_b64 v245, v[232:233] offset:1536\n\t"
"v_mfma_f32_16x16x32_bf16 a[20:23], a[216:219], a[188:191], a[20:23]\n\t"
"ds_read_b64_tr_b16 v[224:225], v246 offset:40960\n\t"
"v_mfma_f32_16x16x32_bf16 a[20:23], a[220:223], a[192:195], a[20:23]\n\t"
"ds_read_b64_tr_b16 v[226:227], v246 offset:44032\n\t"
"v_mfma_f32_16x16x32_bf16 a[20:23], v[96:99], a[196:199], a[20:23]\n\t"
"global_load_dwordx4 v[240:243], v252, s[46:47] offset:0\n\t"
"s_waitcnt lgkmcnt(6)\n\t"
"v_mfma_f32_16x16x32_bf16 v[236:239], v[236:239], v[220:223], 0\n\t"
"ds_read_b64_tr_b16 v[232:233], v246 offset:47104\n\t"
"ds_read_b64_tr_b16 v[234:235], v246 offset:50176\n\t"
"v_mfma_f32_16x16x32_bf16 a[28:31], a[216:219], a[192:195], a[28:31]\n\t"
"v_mfma_f32_16x16x32_bf16 a[28:31], a[220:223], a[196:199], a[28:31]\n\t"
"v_mfma_f32_16x16x32_bf16 a[28:31], v[96:99], a[200:203], a[28:31]\n\t"
"global_load_dwordx4 a[208:211], v252, s[46:47] offset:1024\n\t"
"s_waitcnt lgkmcnt(5)\n\t"
"v_mfma_f32_16x16x32_bf16 v[228:231], v[228:231], v[220:223], 0\n\t"
"v_mfma_f32_16x16x32_bf16 a[36:39], a[216:219], a[196:199], a[36:39]\n\t"
"v_cvt_pk_bf16_f32 v236, v236, v237\n\t"
"v_cvt_pk_bf16_f32 v237, v238, v239\n\t"
"v_mfma_f32_16x16x32_bf16 a[36:39], a[220:223], a[200:203], a[36:39]\n\t"
"v_mfma_f32_16x16x32_bf16 a[36:39], v[96:99], a[204:207], a[36:39]\n\t"
"buffer_load_dwordx4 a[216:219], v252, s[8:11], s20 offen offset:1024 sc1\n\t"
"s_waitcnt lgkmcnt(2)\n\t"
"v_mfma_f32_16x16x32_bf16 v[224:227], v[224:227], v[220:223], 0\n\t"
"buffer_load_dwordx4 v[96:99], v252, s[8:11], s20 offen offset:2048 sc1\n\t"
"s_waitcnt vmcnt(11)\n\t"
"v_mfma_f32_16x16x32_bf16 a[44:47], a[224:227], a[168:171], a[44:47]\n\t"
"v_cvt_pk_bf16_f32 v228, v228, v229\n\t"
"v_cvt_pk_bf16_f32 v229, v230, v231\n\t"
"v_mfma_f32_16x16x32_bf16 a[44:47], a[228:231], a[172:175], a[44:47]\n\t"
"v_mfma_f32_16x16x32_bf16 a[44:47], a[232:235], a[176:179], a[44:47]\n\t"
"ds_write_b64 v245, v[236:237] offset:2048\n\t"
"s_waitcnt lgkmcnt(1)\n\t"
"v_mfma_f32_16x16x32_bf16 v[232:235], v[232:235], v[220:223], 0\n\t"
"ds_read_b64_tr_b16 v[236:237], v246 offset:53248\n\t"
"ds_read_b64_tr_b16 v[238:239], v246 offset:56320\n\t"
"v_mfma_f32_16x16x32_bf16 a[52:55], a[224:227], a[172:175], a[52:55]\n\t"
"v_cvt_pk_bf16_f32 v224, v224, v225\n\t"
"v_cvt_pk_bf16_f32 v225, v226, v227\n\t"
"v_mfma_f32_16x16x32_bf16 a[52:55], a[228:231], a[176:179], a[52:55]\n\t"
"v_mfma_f32_16x16x32_bf16 a[52:55], a[232:235], a[180:183], a[52:55]\n\t"
"s_mov_b32 s20, 577536\n\t"
"buffer_load_dwordx4 a[220:223], v252, s[8:11], s20 offen offset:0 sc1\n\t"
"v_mfma_f32_16x16x32_bf16 a[60:63], a[224:227], a[176:179], a[60:63]\n\t"
"v_mfma_f32_16x16x32_bf16 a[60:63], a[228:231], a[180:183], a[60:63]\n\t"
"v_cvt_pk_bf16_f32 v232, v232, v233\n\t"
"v_cvt_pk_bf16_f32 v233, v234, v235\n\t"
"v_mfma_f32_16x16x32_bf16 a[60:63], a[232:235], a[184:187], a[60:63]\n\t"
"ds_write_b64 v245, v[228:229] offset:2560\n\t"
"v_mfma_f32_16x16x32_bf16 a[68:71], a[224:227], a[180:183], a[68:71]\n\t"
"v_mfma_f32_16x16x32_bf16 a[68:71], a[228:231], a[184:187], a[68:71]\n\t"
"v_mfma_f32_16x16x32_bf16 a[68:71], a[232:235], a[188:191], a[68:71]\n\t"
"ds_write_b64 v245, v[224:225] offset:3072\n\t"
"s_waitcnt lgkmcnt(2)\n\t"
"v_mfma_f32_16x16x32_bf16 v[236:239], v[236:239], v[220:223], 0\n\t"
"ds_write_b64 v245, v[232:233] offset:3584\n\t"
"v_mfma_f32_16x16x32_bf16 a[76:79], a[224:227], a[184:187], a[76:79]\n\t"
"v_mfma_f32_16x16x32_bf16 a[76:79], a[228:231], a[188:191], a[76:79]\n\t"
"v_mfma_f32_16x16x32_bf16 a[76:79], a[232:235], a[192:195], a[76:79]\n\t"
"v_mfma_f32_16x16x32_bf16 a[84:87], a[224:227], a[188:191], a[84:87]\n\t"
"v_mfma_f32_16x16x32_bf16 a[84:87], a[228:231], a[192:195], a[84:87]\n\t"
"s_nop 1\n\t"
"v_cvt_pk_bf16_f32 v236, v236, v237\n\t"
"v_cvt_pk_bf16_f32 v237, v238, v239\n\t"
"v_mfma_f32_16x16x32_bf16 a[84:87], a[232:235], a[196:199], a[84:87]\n\t"
"v_mfma_f32_16x16x32_bf16 a[92:95], a[224:227], a[192:195], a[92:95]\n\t"
"v_mfma_f32_16x16x32_bf16 a[92:95], a[228:231], a[196:199], a[92:95]\n\t"
"v_mfma_f32_16x16x32_bf16 a[92:95], a[232:235], a[200:203], a[92:95]\n\t"
"ds_write_b64 v245, v[236:237] offset:4096\n\t"
"v_mfma_f32_16x16x32_bf16 a[100:103], a[224:227], a[196:199], a[100:103]\n\t"
"v_mfma_f32_16x16x32_bf16 a[100:103], a[228:231], a[200:203], a[100:103]\n\t"
"v_mfma_f32_16x16x32_bf16 a[100:103], a[232:235], a[204:207], a[100:103]\n\t"
"buffer_load_dwordx4 a[224:227], v252, s[8:11], s20 offen offset:1024 sc1\n\t"
"s_waitcnt vmcnt(10)\n\t"
"v_mfma_f32_16x16x32_bf16 a[108:111], a[236:239], a[168:171], a[108:111]\n\t"
"v_mfma_f32_16x16x32_bf16 a[108:111], a[240:243], a[172:175], a[108:111]\n\t"
"v_mfma_f32_16x16x32_bf16 a[108:111], a[244:247], a[176:179], a[108:111]\n\t"
"buffer_load_dwordx4 a[228:231], v252, s[8:11], s20 offen offset:2048 sc1\n\t"
"s_waitcnt lgkmcnt(0)\n\t"
"s_barrier\n\t"
"v_mfma_f32_16x16x32_bf16 a[116:119], a[236:239], a[172:175], a[116:119]\n\t"
"s_waitcnt vmcnt(6)\n\t"
"ds_read_b64_tr_b16 v[228:229], v240 offset:1024\n\t"
"v_mfma_f32_16x16x32_bf16 a[116:119], a[240:243], a[176:179], a[116:119]\n\t"
"ds_read_b64_tr_b16 v[230:231], v240 offset:2560\n\t"
"v_mfma_f32_16x16x32_bf16 a[116:119], a[244:247], a[180:183], a[116:119]\n\t"
"ds_read_b64_tr_b16 v[224:225], v241 offset:1024\n\t"
"ds_read_b64_tr_b16 v[226:227], v241 offset:2560\n\t"
"v_mfma_f32_16x16x32_bf16 a[124:127], a[236:239], a[176:179], a[124:127]\n\t"
"ds_read_b64_tr_b16 v[232:233], v240 offset:20992\n\t"
"v_mfma_f32_16x16x32_bf16 a[124:127], a[240:243], a[180:183], a[124:127]\n\t"
"ds_read_b64_tr_b16 v[234:235], v240 offset:22528\n\t"
"v_mfma_f32_16x16x32_bf16 a[124:127], a[244:247], a[184:187], a[124:127]\n\t"
"s_mov_b32 s20, 589824\n\t"
"buffer_load_dwordx4 a[232:235], v252, s[8:11], s20 offen offset:0 sc1\n\t"
"v_mfma_f32_16x16x32_bf16 a[132:135], a[236:239], a[180:183], a[132:135]\n\t"
"ds_read_b64_tr_b16 v[236:237], v241 offset:20992\n\t"
"v_mfma_f32_16x16x32_bf16 a[132:135], a[240:243], a[184:187], a[132:135]\n\t"
"ds_read_b64_tr_b16 v[238:239], v241 offset:22528\n\t"
"v_mfma_f32_16x16x32_bf16 a[132:135], a[244:247], a[188:191], a[132:135]\n\t"
"ds_read_b128 a[168:171], v244 offset:0\n\t"
"ds_read_b128 a[172:175], v244 offset:512\n\t"
"s_waitcnt vmcnt(6) lgkmcnt(8)\n\t"
"v_mfma_f32_16x16x32_bf16 v[228:231], v[228:231], a[208:211], 0\n\t"
"ds_read_b128 a[176:179], v244 offset:1024\n\t"
"ds_read_b128 a[180:183], v244 offset:1536\n\t"
"s_waitcnt lgkmcnt(8)\n\t"
"v_mfma_f32_16x16x32_bf16 v[224:227], v[224:227], a[208:211], 0\n\t"
"v_mfma_f32_16x16x32_bf16 a[140:143], a[236:239], a[184:187], a[140:143]\n\t"
"ds_read_b128 a[184:187], v244 offset:2048\n\t"
"v_mfma_f32_16x16x32_bf16 a[140:143], a[240:243], a[188:191], a[140:143]\n\t"
"v_mfma_f32_16x16x32_bf16 a[140:143], a[244:247], a[192:195], a[140:143]\n\t"
"v_mfma_f32_16x16x32_bf16 a[148:151], a[236:239], a[188:191], a[148:151]\n\t"
"v_cvt_pk_bf16_f32 v228, v228, v229\n\t"
"v_cvt_pk_bf16_f32 v229, v230, v231\n\t"
"v_mfma_f32_16x16x32_bf16 a[148:151], a[240:243], a[192:195], a[148:151]\n\t"
"v_cvt_pk_bf16_f32 v230, v224, v225\n\t"
"v_mfma_f32_16x16x32_bf16 a[148:151], a[244:247], a[196:199], a[148:151]\n\t"
"v_cvt_pk_bf16_f32 v231, v226, v227\n\t"
"ds_read_b128 a[188:191], v244 offset:2560\n\t"
"s_waitcnt lgkmcnt(8)\n\t"
"v_mfma_f32_16x16x32_bf16 v[232:235], v[232:235], a[208:211], 0\n\t"
"s_waitcnt lgkmcnt(6)\n\t"
"v_mfma_f32_16x16x32_bf16 v[236:239], v[236:239], a[208:211], 0\n\t"
"s_mov_b64 exec, s[44:45]\n\t"
"ds_write_b128 v242, v[228:231] offset:18432\n\t"
"s_mov_b64 exec, -1\n\t"
"v_mfma_f32_16x16x32_bf16 a[156:159], a[236:239], a[192:195], a[156:159]\n\t"
"ds_read_b128 a[192:195], v244 offset:3072\n\t"
"v_mfma_f32_16x16x32_bf16 a[156:159], a[240:243], a[196:199], a[156:159]\n\t"
"ds_read_b64_tr_b16 v[224:225], v240 offset:40960\n\t"
"v_mfma_f32_16x16x32_bf16 a[156:159], a[244:247], a[200:203], a[156:159]\n\t"
"ds_read_b64_tr_b16 v[226:227], v240 offset:42496\n\t"
"v_mfma_f32_16x16x32_bf16 a[164:167], a[236:239], a[196:199], a[164:167]\n\t"
"v_cvt_pk_bf16_f32 v232, v232, v233\n\t"
"v_cvt_pk_bf16_f32 v233, v234, v235\n\t"
"v_mfma_f32_16x16x32_bf16 a[164:167], a[240:243], a[200:203], a[164:167]\n\t"
"v_cvt_pk_bf16_f32 v234, v236, v237\n\t"
"v_cvt_pk_bf16_f32 v235, v238, v239\n\t"
"v_mfma_f32_16x16x32_bf16 a[164:167], a[244:247], a[204:207], a[164:167]\n\t"
"buffer_load_dwordx4 a[236:239], v252, s[8:11], s20 offen offset:1024 sc1\n\t"
"Lst6_%=:\n\t"
"s_waitcnt lgkmcnt(7)\n\t"
"v_mfma_f32_16x16x32_bf16 v[196:199], a[248:251], a[168:171], v[196:199]\n\t"
"ds_read_b64_tr_b16 v[228:229], v241 offset:40960\n\t"
"v_mfma_f32_16x16x32_bf16 v[196:199], a[252:255], a[172:175], v[196:199]\n\t"
"ds_read_b64_tr_b16 v[230:231], v241 offset:42496\n\t"
"v_add_u32 v240, 59904, v240\n\t"
"v_mfma_f32_16x16x32_bf16 v[196:199], v[248:251], a[176:179], v[196:199]\n\t"
"buffer_load_dwordx4 a[240:243], v252, s[8:11], s20 offen offset:2048 sc1\n\t"
"s_waitcnt lgkmcnt(8)\n\t"
"v_mfma_f32_16x16x32_bf16 v[204:207], a[248:251], a[172:175], v[204:207]\n\t"
"ds_read_b128 a[196:199], v244 offset:3584\n\t"
"v_add_u32 v241, 59904, v241\n\t"
"v_mfma_f32_16x16x32_bf16 v[204:207], a[252:255], a[176:179], v[204:207]\n\t"
"ds_read_b128 a[200:203], v244 offset:4096\n\t"
"v_mfma_f32_16x16x32_bf16 v[204:207], v[248:251], a[180:183], v[204:207]\n\t"
"s_mov_b64 exec, s[44:45]\n\t"
"ds_write_b128 v242, v[232:235] offset:19968\n\t"
"s_mov_b64 exec, -1\n\t"
"s_waitcnt lgkmcnt(10)\n\t"
"v_mfma_f32_16x16x32_bf16 v[212:215], a[248:251], a[176:179], v[212:215]\n\t"
"ds_read_b64_tr_b16 v[236:237], v240 offset:1024\n\t"
"v_mfma_f32_16x16x32_bf16 v[212:215], a[252:255], a[180:183], v[212:215]\n\t"
"ds_read_b64_tr_b16 v[238:239], v240 offset:2560\n\t"
"v_mfma_f32_16x16x32_bf16 v[212:215], v[248:251], a[184:187], v[212:215]\n\t"
"s_mov_b32 s20, 602112\n\t"
"buffer_load_dwordx4 a[244:247], v252, s[8:11], s20 offen offset:0 sc1\n\t"
"s_waitcnt lgkmcnt(7)\n\t"
"v_mfma_f32_16x16x32_bf16 v[224:227], v[224:227], a[208:211], 0\n\t"
"ds_read_b64_tr_b16 v[232:233], v241 offset:1024\n\t"
"ds_read_b64_tr_b16 v[234:235], v241 offset:2560\n\t"
"s_waitcnt lgkmcnt(7)\n\t"
"v_mfma_f32_16x16x32_bf16 v[228:231], v[228:231], a[208:211], 0\n\t"
"ds_read_b128 a[204:207], v244 offset:4608\n\t"
"v_mfma_f32_16x16x32_bf16 a[0:3], a[248:251], a[180:183], a[0:3]\n\t"
"v_mfma_f32_16x16x32_bf16 a[0:3], a[252:255], a[184:187], a[0:3]\n\t"
"v_mfma_f32_16x16x32_bf16 a[0:3], v[248:251], a[188:191], a[0:3]\n\t"
"v_mfma_f32_16x16x32_bf16 a[8:11], a[248:251], a[184:187], a[8:11]\n\t"
"v_cvt_pk_bf16_f32 v224, v224, v225\n\t"
"v_cvt_pk_bf16_f32 v225, v226, v227\n\t"
"v_mfma_f32_16x16x32_bf16 a[8:11], a[252:255], a[188:191], a[8:11]\n\t"
"v_cvt_pk_bf16_f32 v226, v228, v229\n\t"
"v_mfma_f32_16x16x32_bf16 a[8:11], v[248:251], a[192:195], a[8:11]\n\t"
"v_cvt_pk_bf16_f32 v227, v230, v231\n\t"
"s_waitcnt lgkmcnt(3)\n\t"
"v_mfma_f32_16x16x32_bf16 v[236:239], v[236:239], a[208:211], 0\n\t"
"s_mov_b64 exec, s[44:45]\n\t"
"ds_write_b128 v242, v[224:227] offset:21504\n\t"
"s_mov_b64 exec, -1\n\t"
"s_waitcnt lgkmcnt(2)\n\t"
"v_mfma_f32_16x16x32_bf16 v[232:235], v[232:235], a[208:211], 0\n\t"
"ds_read_b64_tr_b16 v[228:229], v240 offset:20992\n\t"
"v_mfma_f32_16x16x32_bf16 a[16:19], a[248:251], a[188:191], a[16:19]\n\t"
"v_mfma_f32_16x16x32_bf16 a[16:19], a[252:255], a[192:195], a[16:19]\n\t"
"ds_read_b64_tr_b16 v[230:231], v240 offset:22528\n\t"
"v_mfma_f32_16x16x32_bf16 a[16:19], v[248:251], a[196:199], a[16:19]\n\t"
"ds_read_b64_tr_b16 v[224:225], v241 offset:20992\n\t"
"v_mfma_f32_16x16x32_bf16 a[24:27], a[248:251], a[192:195], a[24:27]\n\t"
"v_cvt_pk_bf16_f32 v236, v236, v237\n\t"
"v_mfma_f32_16x16x32_bf16 a[24:27], a[252:255], a[196:199], a[24:27]\n\t"
"v_cvt_pk_bf16_f32 v237, v238, v239\n\t"
"v_cvt_pk_bf16_f32 v238, v232, v233\n\t"
"v_mfma_f32_16x16x32_bf16 a[24:27], v[248:251], a[200:203], a[24:27]\n\t"
"v_cvt_pk_bf16_f32 v239, v234, v235\n\t"
"ds_read_b64_tr_b16 v[226:227], v241 offset:22528\n\t"
"s_waitcnt lgkmcnt(5)\n\t"
"v_mfma_f32_16x16x32_bf16 a[32:35], a[248:251], a[196:199], a[32:35]\n\t"
"v_mfma_f32_16x16x32_bf16 a[32:35], a[252:255], a[200:203], a[32:35]\n\t"
"ds_read_b64_tr_b16 v[232:233], v240 offset:40960\n\t"
"v_mfma_f32_16x16x32_bf16 a[32:35], v[248:251], a[204:207], a[32:35]\n\t"
"buffer_load_dwordx4 a[248:251], v252, s[8:11], s20 offen offset:1024 sc1\n\t"
"s_waitcnt vmcnt(8)\n\t"
"v_mfma_f32_16x16x32_bf16 a[40:43], a[212:215], a[168:171], a[40:43]\n\t"
"ds_read_b64_tr_b16 v[234:235], v240 offset:42496\n\t"
"v_mfma_f32_16x16x32_bf16 a[40:43], a[216:219], a[172:175], a[40:43]\n\t"
"v_mfma_f32_16x16x32_bf16 a[40:43], v[96:99], a[176:179], a[40:43]\n\t"
"s_mov_b64 exec, s[44:45]\n\t"
"ds_write_b128 v242, v[236:239] offset:23040\n\t"
"s_mov_b64 exec, -1\n\t"
"v_mfma_f32_16x16x32_bf16 a[48:51], a[212:215], a[172:175], a[48:51]\n\t"
"ds_read_b64_tr_b16 v[236:237], v241 offset:40960\n\t"
"v_mfma_f32_16x16x32_bf16 a[48:51], a[216:219], a[176:179], a[48:51]\n\t"
"ds_read_b64_tr_b16 v[238:239], v241 offset:42496\n\t"
"v_mfma_f32_16x16x32_bf16 a[48:51], v[96:99], a[180:183], a[48:51]\n\t"
"buffer_load_dwordx4 v[248:251], v252, s[8:11], s20 offen offset:2048 sc1\n\t"
"s_waitcnt lgkmcnt(7)\n\t"
"v_mfma_f32_16x16x32_bf16 v[228:231], v[228:231], a[208:211], 0\n\t"
"s_mov_b32 s20, 614400\n\t"
"buffer_load_dwordx4 a[252:255], v252, s[8:11], s20 offen offset:0 sc1\n\t"
"s_waitcnt lgkmcnt(5)\n\t"
"v_mfma_f32_16x16x32_bf16 v[224:227], v[224:227], a[208:211], 0\n\t"
"v_xor_b32 v246, 0xc0, v246\n\t"
"v_mfma_f32_16x16x32_bf16 a[56:59], a[212:215], a[176:179], a[56:59]\n\t"
"v_mfma_f32_16x16x32_bf16 a[56:59], a[216:219], a[180:183], a[56:59]\n\t"
"v_mfma_f32_16x16x32_bf16 a[56:59], v[96:99], a[184:187], a[56:59]\n\t"
"v_mfma_f32_16x16x32_bf16 a[64:67], a[212:215], a[180:183], a[64:67]\n\t"
"v_cvt_pk_bf16_f32 v228, v228, v229\n\t"
"v_mfma_f32_16x16x32_bf16 a[64:67], a[216:219], a[184:187], a[64:67]\n\t"
"v_cvt_pk_bf16_f32 v229, v230, v231\n\t"
"v_cvt_pk_bf16_f32 v230, v224, v225\n\t"
"v_mfma_f32_16x16x32_bf16 a[64:67], v[96:99], a[188:191], a[64:67]\n\t"
"v_cvt_pk_bf16_f32 v231, v226, v227\n\t"
"s_waitcnt lgkmcnt(3)\n\t"
"v_mfma_f32_16x16x32_bf16 v[232:235], v[232:235], a[208:211], 0\n\t"
"s_waitcnt lgkmcnt(0)\n\t"
"v_mfma_f32_16x16x32_bf16 v[236:239], v[236:239], a[208:211], 0\n\t"
"s_mov_b64 exec, s[44:45]\n\t"
"ds_write_b128 v242, v[228:231] offset:24576\n\t"
"s_mov_b64 exec, -1\n\t"
"v_mfma_f32_16x16x32_bf16 a[72:75], a[212:215], a[184:187], a[72:75]\n\t"
"v_mfma_f32_16x16x32_bf16 a[72:75], a[216:219], a[188:191], a[72:75]\n\t"
"v_mfma_f32_16x16x32_bf16 a[72:75], v[96:99], a[192:195], a[72:75]\n\t"
"buffer_load_dwordx4 a[208:211], v252, s[8:11], s20 offen offset:1024 sc1\n\t"
"v_mfma_f32_16x16x32_bf16 a[80:83], a[212:215], a[188:191], a[80:83]\n\t"
"v_cvt_pk_bf16_f32 v232, v232, v233\n\t"
"v_cvt_pk_bf16_f32 v233, v234, v235\n\t"
"v_mfma_f32_16x16x32_bf16 a[80:83], a[216:219], a[192:195], a[80:83]\n\t"
"v_cvt_pk_bf16_f32 v234, v236, v237\n\t"
"v_mfma_f32_16x16x32_bf16 a[80:83], v[96:99], a[196:199], a[80:83]\n\t"
"v_cvt_pk_bf16_f32 v235, v238, v239\n\t"
"ds_read_b64_tr_b16 v[224:225], v246 offset:1024\n\t"
"v_mfma_f32_16x16x32_bf16 a[88:91], a[212:215], a[192:195], a[88:91]\n\t"
"ds_read_b64_tr_b16 v[226:227], v246 offset:4096\n\t"
"v_mfma_f32_16x16x32_bf16 a[88:91], a[216:219], a[196:199], a[88:91]\n\t"
"v_mfma_f32_16x16x32_bf16 a[88:91], v[96:99], a[200:203], a[88:91]\n\t"
"s_mov_b64 exec, s[44:45]\n\t"
"ds_write_b128 v242, v[232:235] offset:26112\n\t"
"s_mov_b64 exec, -1\n\t"
"v_mfma_f32_16x16x32_bf16 a[96:99], a[212:215], a[196:199], a[96:99]\n\t"
"ds_read_b64_tr_b16 v[228:229], v246 offset:7168\n\t"
"v_mfma_f32_16x16x32_bf16 a[96:99], a[216:219], a[200:203], a[96:99]\n\t"
"ds_read_b64_tr_b16 v[230:231], v246 offset:10240\n\t"
"v_mfma_f32_16x16x32_bf16 a[96:99], v[96:99], a[204:207], a[96:99]\n\t"
"s_waitcnt vmcnt(8)\n\t"
"v_mfma_f32_16x16x32_bf16 a[104:107], a[220:223], a[168:171], a[104:107]\n\t"
"ds_read_b128 a[168:171], v244 offset:9216\n\t"
"v_mfma_f32_16x16x32_bf16 a[104:107], a[224:227], a[172:175], a[104:107]\n\t"
"ds_read_b64_tr_b16 v[236:237], v246 offset:13312\n\t"
"v_mfma_f32_16x16x32_bf16 a[104:107], a[228:231], a[176:179], a[104:107]\n\t"
"buffer_load_dwordx4 a[212:215], v252, s[8:11], s20 offen offset:2048 sc1\n\t"
"v_mfma_f32_16x16x32_bf16 a[112:115], a[220:223], a[172:175], a[112:115]\n\t"
"ds_read_b64_tr_b16 v[238:239], v246 offset:16384\n\t"
"v_mfma_f32_16x16x32_bf16 a[112:115], a[224:227], a[176:179], a[112:115]\n\t"
"ds_read_b128 a[172:175], v244 offset:9728\n\t"
"v_mfma_f32_16x16x32_bf16 a[112:115], a[228:231], a[180:183], a[112:115]\n\t"
"s_mov_b32 s20, 479232\n\t"
"buffer_load_dwordx4 v[96:99], v252, s[8:11], s20 offen offset:0 sc1\n\t"
"v_mfma_f32_16x16x32_bf16 a[120:123], a[220:223], a[176:179], a[120:123]\n\t"
"ds_read_b64_tr_b16 v[232:233], v246 offset:20992\n\t"
"v_mfma_f32_16x16x32_bf16 a[120:123], a[224:227], a[180:183], a[120:123]\n\t"
"ds_read_b64_tr_b16 v[234:235], v246 offset:24064\n\t"
"v_mfma_f32_16x16x32_bf16 a[120:123], a[228:231], a[184:187], a[120:123]\n\t"
"buffer_load_dwordx4 a[216:219], v252, s[8:11], s20 offen offset:1024 sc1\n\t"
"s_waitcnt lgkmcnt(9)\n\t"
"v_mfma_f32_16x16x32_bf16 v[224:227], v[224:227], v[220:223], 0\n\t"
"v_mfma_f32_16x16x32_bf16 a[128:131], a[220:223], a[180:183], a[128:131]\n\t"
"ds_read_b128 a[176:179], v244 offset:10240\n\t"
"v_mfma_f32_16x16x32_bf16 a[128:131], a[224:227], a[184:187], a[128:131]\n\t"
"v_mfma_f32_16x16x32_bf16 a[128:131], a[228:231], a[188:191], a[128:131]\n\t"
"ds_read_b128 a[180:183], v244 offset:10752\n\t"
"s_waitcnt lgkmcnt(8)\n\t"
"v_mfma_f32_16x16x32_bf16 v[228:231], v[228:231], v[220:223], 0\n\t"
"v_mfma_f32_16x16x32_bf16 a[136:139], a[220:223], a[184:187], a[136:139]\n\t"
"v_cvt_pk_bf16_f32 v224, v224, v225\n\t"
"v_cvt_pk_bf16_f32 v225, v226, v227\n\t"
"v_mfma_f32_16x16x32_bf16 a[136:139], a[224:227], a[188:191], a[136:139]\n\t"
"ds_read_b128 a[184:187], v244 offset:11264\n\t"
"v_mfma_f32_16x16x32_bf16 a[136:139], a[228:231], a[192:195], a[136:139]\n\t"
"s_waitcnt lgkmcnt(6)\n\t"
"v_mfma_f32_16x16x32_bf16 v[236:239], v[236:239], v[220:223], 0\n\t"
"v_mfma_f32_16x16x32_bf16 a[144:147], a[220:223], a[188:191], a[144:147]\n\t"
"v_cvt_pk_bf16_f32 v226, v228, v229\n\t"
"v_cvt_pk_bf16_f32 v227, v230, v231\n\t"
"v_mfma_f32_16x16x32_bf16 a[144:147], a[224:227], a[192:195], a[144:147]\n\t"
"ds_read_b64_tr_b16 v[228:229], v246 offset:27136\n\t"
"v_mfma_f32_16x16x32_bf16 a[144:147], a[228:231], a[196:199], a[144:147]\n\t"
"ds_read_b64_tr_b16 v[230:231], v246 offset:30208\n\t"
"ds_read_b128 a[188:191], v244 offset:11776\n\t"
"s_waitcnt lgkmcnt(6)\n\t"
"v_mfma_f32_16x16x32_bf16 v[232:235], v[232:235], v[220:223], 0\n\t"
"v_mfma_f32_16x16x32_bf16 a[152:155], a[220:223], a[192:195], a[152:155]\n\t"
"v_cvt_pk_bf16_f32 v236, v236, v237\n\t"
"v_cvt_pk_bf16_f32 v237, v238, v239\n\t"
"v_mfma_f32_16x16x32_bf16 a[152:155], a[224:227], a[196:199], a[152:155]\n\t"
"ds_read_b128 a[192:195], v244 offset:12288\n\t"
"v_mfma_f32_16x16x32_bf16 a[152:155], a[228:231], a[200:203], a[152:155]\n\t"
"v_mfma_f32_16x16x32_bf16 a[160:163], a[220:223], a[196:199], a[160:163]\n\t"
"ds_read_b128 a[196:199], v244 offset:12800\n\t"
"v_mfma_f32_16x16x32_bf16 a[160:163], a[224:227], a[200:203], a[160:163]\n\t"
"v_cvt_pk_bf16_f32 v238, v232, v233\n\t"
"v_cvt_pk_bf16_f32 v239, v234, v235\n\t"
"v_mfma_f32_16x16x32_bf16 a[160:163], a[228:231], a[204:207], a[160:163]\n\t"
"buffer_load_dwordx4 a[220:223], v252, s[8:11], s20 offen offset:2048 sc1\n\t"
"s_waitcnt vmcnt(9) lgkmcnt(7)\n\t"
"v_mfma_f32_16x16x32_bf16 v[200:203], a[232:235], a[168:171], v[200:203]\n\t"
"ds_read_b128 a[200:203], v244 offset:13312\n\t"
"v_mfma_f32_16x16x32_bf16 v[200:203], a[236:239], a[172:175], v[200:203]\n\t"
"ds_read_b128 a[204:207], v244 offset:13824\n\t"
"v_mfma_f32_16x16x32_bf16 v[200:203], a[240:243], a[176:179], v[200:203]\n\t"
"s_mov_b32 s20, 491520\n\t"
"buffer_load_dwordx4 a[224:227], v252, s[8:11], s20 offen offset:0 sc1\n\t"
"s_waitcnt lgkmcnt(5)\n\t"
"v_mfma_f32_16x16x32_bf16 v[228:231], v[228:231], v[220:223], 0\n\t"
"buffer_load_dwordx4 a[228:231], v252, s[8:11], s20 offen offset:1024 sc1\n\t"
"v_mfma_f32_16x16x32_bf16 v[208:211], a[232:235], a[172:175], v[208:211]\n\t"
"v_mfma_f32_16x16x32_bf16 v[208:211], a[236:239], a[176:179], v[208:211]\n\t"
"v_mfma_f32_16x16x32_bf16 v[208:211], a[240:243], a[180:183], v[208:211]\n\t"
"s_waitcnt lgkmcnt(0)\n\t"
"s_barrier\n\t"
"v_mfma_f32_16x16x32_bf16 v[216:219], a[232:235], a[176:179], v[216:219]\n\t"
"ds_read_b64_tr_b16 v[232:233], v246 offset:33280\n\t"
"v_mfma_f32_16x16x32_bf16 v[216:219], a[236:239], a[180:183], v[216:219]\n\t"
"v_cvt_pk_bf16_f32 v228, v228, v229\n\t"
"v_cvt_pk_bf16_f32 v229, v230, v231\n\t"
"v_mfma_f32_16x16x32_bf16 v[216:219], a[240:243], a[184:187], v[216:219]\n\t"
"s_add_u32 m0, s24, 256\n\t"
"s_add_u32 s28, s21, 0\n\t"
"buffer_load_dwordx4 v247, s[12:15], s28 offen offset:256 nt lds\n\t"
"ds_write2st64_b64 v245, v[224:225], v[226:227] offset0:0 offset1:1\n\t"
"s_add_u32 m0, s24, 6400\n\t"
"s_add_u32 s28, s21, 196608\n\t"
"buffer_load_dwordx4 v247, s[12:15], s28 offen offset:256 nt lds\n\t"
"v_mfma_f32_16x16x32_bf16 a[4:7], a[232:235], a[180:183], a[4:7]\n\t"
"ds_read_b64_tr_b16 v[234:235], v246 offset:36352\n\t"
"v_mfma_f32_16x16x32_bf16 a[4:7], a[236:239], a[184:187], a[4:7]\n\t"
"ds_read_b64_tr_b16 v[224:225], v246 offset:40960\n\t"
"v_mfma_f32_16x16x32_bf16 a[4:7], a[240:243], a[188:191], a[4:7]\n\t"
"ds_write2st64_b64 v245, v[236:237], v[238:239] offset0:2 offset1:3\n\t"
"s_add_u32 m0, s24, 12544\n\t"
"s_add_u32 s28, s21, 393216\n\t"
"buffer_load_dwordx4 v247, s[12:15], s28 offen offset:256 nt lds\n\t"
"v_mfma_f32_16x16x32_bf16 a[12:15], a[232:235], a[184:187], a[12:15]\n\t"
"ds_read_b64_tr_b16 v[226:227], v246 offset:44032\n\t"
"v_mfma_f32_16x16x32_bf16 a[12:15], a[236:239], a[188:191], a[12:15]\n\t"
"v_mfma_f32_16x16x32_bf16 a[12:15], a[240:243], a[192:195], a[12:15]\n\t"
"s_add_u32 m0, s24, 20224\n\t"
"s_add_u32 s28, s21, 589824\n\t"
"buffer_load_dwordx4 v247, s[12:15], s28 offen offset:256 nt lds\n\t"
"ds_write_b64 v245, v[228:229] offset:2048\n\t"
"s_add_u32 m0, s24, 26368\n\t"
"s_add_u32 s28, s21, 786432\n\t"
"buffer_load_dwordx4 v247, s[12:15], s28 offen offset:256 nt lds\n\t"
"v_mfma_f32_16x16x32_bf16 a[20:23], a[232:235], a[188:191], a[20:23]\n\t"
"ds_read_b64_tr_b16 v[228:229], v246 offset:47104\n\t"
"v_mfma_f32_16x16x32_bf16 a[20:23], a[236:239], a[192:195], a[20:23]\n\t"
"ds_read_b64_tr_b16 v[230:231], v246 offset:50176\n\t"
"v_mfma_f32_16x16x32_bf16 a[20:23], a[240:243], a[196:199], a[20:23]\n\t"
"s_add_u32 m0, s24, 60160\n\t"
"s_add_u32 s28, s21, 1769472\n\t"
"buffer_load_dwordx4 v247, s[12:15], s28 offen offset:256 nt lds\n\t"
"s_waitcnt lgkmcnt(6)\n\t"
"v_mfma_f32_16x16x32_bf16 v[232:235], v[232:235], v[220:223], 0\n\t"
"v_mfma_f32_16x16x32_bf16 a[28:31], a[232:235], a[192:195], a[28:31]\n\t"
"v_mfma_f32_16x16x32_bf16 a[28:31], a[236:239], a[196:199], a[28:31]\n\t"
"v_mfma_f32_16x16x32_bf16 a[28:31], a[240:243], a[200:203], a[28:31]\n\t"
"s_waitcnt lgkmcnt(3)\n\t"
"v_mfma_f32_16x16x32_bf16 v[224:227], v[224:227], v[220:223], 0\n\t"
"v_mfma_f32_16x16x32_bf16 a[36:39], a[232:235], a[196:199], a[36:39]\n\t"
"s_nop 1\n\t"
"v_cvt_pk_bf16_f32 v232, v232, v233\n\t"
"v_cvt_pk_bf16_f32 v233, v234, v235\n\t"
"v_mfma_f32_16x16x32_bf16 a[36:39], a[236:239], a[200:203], a[36:39]\n\t"
"v_mfma_f32_16x16x32_bf16 a[36:39], a[240:243], a[204:207], a[36:39]\n\t"
"s_waitcnt lgkmcnt(0)\n\t"
"v_mfma_f32_16x16x32_bf16 v[228:231], v[228:231], v[220:223], 0\n\t"
"s_waitcnt vmcnt(14)\n\t"
"v_mfma_f32_16x16x32_bf16 a[44:47], a[244:247], a[168:171], a[44:47]\n\t"
"v_cvt_pk_bf16_f32 v234, v224, v225\n\t"
"v_cvt_pk_bf16_f32 v235, v226, v227\n\t"
"v_mfma_f32_16x16x32_bf16 a[44:47], a[248:251], a[172:175], a[44:47]\n\t"
"ds_read_b64_tr_b16 v[224:225], v246 offset:53248\n\t"
"v_mfma_f32_16x16x32_bf16 a[44:47], v[248:251], a[176:179], a[44:47]\n\t"
"ds_write2st64_b64 v245, v[232:233], v[234:235] offset0:5 offset1:6\n\t"
"v_mfma_f32_16x16x32_bf16 a[52:55], a[244:247], a[172:175], a[52:55]\n\t"
"ds_read_b64_tr_b16 v[226:227], v246 offset:56320\n\t"
"v_mfma_f32_16x16x32_bf16 a[52:55], a[248:251], a[176:179], a[52:55]\n\t"
"v_cvt_pk_bf16_f32 v228, v228, v229\n\t"
"v_mfma_f32_16x16x32_bf16 a[52:55], v[248:251], a[180:183], a[52:55]\n\t"
"v_cvt_pk_bf16_f32 v229, v230, v231\n\t"
"v_mfma_f32_16x16x32_bf16 a[60:63], a[244:247], a[176:179], a[60:63]\n\t"
"v_mfma_f32_16x16x32_bf16 a[60:63], a[248:251], a[180:183], a[60:63]\n\t"
"v_mfma_f32_16x16x32_bf16 a[60:63], v[248:251], a[184:187], a[60:63]\n\t"
"v_mfma_f32_16x16x32_bf16 a[68:71], a[244:247], a[180:183], a[68:71]\n\t"
"v_mfma_f32_16x16x32_bf16 a[68:71], a[248:251], a[184:187], a[68:71]\n\t"
"v_mfma_f32_16x16x32_bf16 a[68:71], v[248:251], a[188:191], a[68:71]\n\t"
"v_mfma_f32_16x16x32_bf16 a[76:79], a[244:247], a[184:187], a[76:79]\n\t"
"v_mfma_f32_16x16x32_bf16 a[76:79], a[248:251], a[188:191], a[76:79]\n\t"
"v_mfma_f32_16x16x32_bf16 a[76:79], v[248:251], a[192:195], a[76:79]\n\t"
"s_waitcnt lgkmcnt(0)\n\t"
"v_mfma_f32_16x16x32_bf16 v[224:227], v[224:227], v[220:223], 0\n\t"
"v_mfma_f32_16x16x32_bf16 a[84:87], a[244:247], a[188:191], a[84:87]\n\t"
"v_mfma_f32_16x16x32_bf16 a[84:87], a[248:251], a[192:195], a[84:87]\n\t"
"v_mfma_f32_16x16x32_bf16 a[84:87], v[248:251], a[196:199], a[84:87]\n\t"
"buffer_load_dwordx4 v[236:239], v252, s[8:11], s20 offen offset:2048 sc1\n\t"
"v_mfma_f32_16x16x32_bf16 a[92:95], a[244:247], a[192:195], a[92:95]\n\t"
"v_mfma_f32_16x16x32_bf16 a[92:95], a[248:251], a[196:199], a[92:95]\n\t"
"s_nop 1\n\t"
"v_cvt_pk_bf16_f32 v230, v224, v225\n\t"
"v_cvt_pk_bf16_f32 v231, v226, v227\n\t"
"v_mfma_f32_16x16x32_bf16 a[92:95], v[248:251], a[200:203], a[92:95]\n\t"
"ds_write2st64_b64 v245, v[228:229], v[230:231] offset0:7 offset1:8\n\t"
"v_mfma_f32_16x16x32_bf16 a[100:103], a[244:247], a[196:199], a[100:103]\n\t"
"v_mfma_f32_16x16x32_bf16 a[100:103], a[248:251], a[200:203], a[100:103]\n\t"
"v_mfma_f32_16x16x32_bf16 a[100:103], v[248:251], a[204:207], a[100:103]\n\t"
"s_waitcnt vmcnt(12)\n\t"
"v_mfma_f32_16x16x32_bf16 a[108:111], a[252:255], a[168:171], a[108:111]\n\t"
"v_mfma_f32_16x16x32_bf16 a[108:111], a[208:211], a[172:175], a[108:111]\n\t"
"v_mfma_f32_16x16x32_bf16 a[108:111], a[212:215], a[176:179], a[108:111]\n\t"
"s_waitcnt lgkmcnt(0)\n\t"
"s_barrier\n\t"
"v_mfma_f32_16x16x32_bf16 a[116:119], a[252:255], a[172:175], a[116:119]\n\t"
"ds_read_b128 a[168:171], v244 offset:0\n\t"
"v_mfma_f32_16x16x32_bf16 a[116:119], a[208:211], a[176:179], a[116:119]\n\t"
"ds_read_b128 a[172:175], v244 offset:512\n\t"
"v_mfma_f32_16x16x32_bf16 a[116:119], a[212:215], a[180:183], a[116:119]\n\t"
"v_mfma_f32_16x16x32_bf16 a[124:127], a[252:255], a[176:179], a[124:127]\n\t"
"ds_read_b128 a[176:179], v244 offset:1024\n\t"
"v_mfma_f32_16x16x32_bf16 a[124:127], a[208:211], a[180:183], a[124:127]\n\t"
"v_mfma_f32_16x16x32_bf16 a[124:127], a[212:215], a[184:187], a[124:127]\n\t"
"v_mfma_f32_16x16x32_bf16 a[132:135], a[252:255], a[180:183], a[132:135]\n\t"
"ds_read_b128 a[180:183], v244 offset:1536\n\t"
"v_mfma_f32_16x16x32_bf16 a[132:135], a[208:211], a[184:187], a[132:135]\n\t"
"v_mfma_f32_16x16x32_bf16 a[132:135], a[212:215], a[188:191], a[132:135]\n\t"
"v_mfma_f32_16x16x32_bf16 a[140:143], a[252:255], a[184:187], a[140:143]\n\t"
"ds_read_b128 a[184:187], v244 offset:2048\n\t"
"v_mfma_f32_16x16x32_bf16 a[140:143], a[208:211], a[188:191], a[140:143]\n\t"
"v_mfma_f32_16x16x32_bf16 a[140:143], a[212:215], a[192:195], a[140:143]\n\t"
"v_mfma_f32_16x16x32_bf16 a[148:151], a[252:255], a[188:191], a[148:151]\n\t"
"ds_read_b128 a[188:191], v244 offset:2560\n\t"
"v_mfma_f32_16x16x32_bf16 a[148:151], a[208:211], a[192:195], a[148:151]\n\t"
"v_mfma_f32_16x16x32_bf16 a[148:151], a[212:215], a[196:199], a[148:151]\n\t"
"v_mfma_f32_16x16x32_bf16 a[156:159], a[252:255], a[192:195], a[156:159]\n\t"
"ds_read_b128 a[192:195], v244 offset:3072\n\t"
"v_mfma_f32_16x16x32_bf16 a[156:159], a[208:211], a[196:199], a[156:159]\n\t"
"v_mfma_f32_16x16x32_bf16 a[156:159], a[212:215], a[200:203], a[156:159]\n\t"
"v_mfma_f32_16x16x32_bf16 a[164:167], a[252:255], a[196:199], a[164:167]\n\t"
"ds_read_b128 a[196:199], v244 offset:3584\n\t"
"v_mfma_f32_16x16x32_bf16 a[164:167], a[208:211], a[200:203], a[164:167]\n\t"
"ds_read_b128 a[200:203], v244 offset:4096\n\t"
"v_mfma_f32_16x16x32_bf16 a[164:167], a[212:215], a[204:207], a[164:167]\n\t"
"ds_read_b128 a[204:207], v244 offset:4608\n\t"
"Lst7_%=:\n\t"
"s_waitcnt vmcnt(9) lgkmcnt(7)\n\t"
"v_mfma_f32_16x16x32_bf16 v[0:3], v[96:99], a[168:171], v[0:3]\n\t"
"v_mfma_f32_16x16x32_bf16 v[0:3], a[216:219], a[172:175], v[0:3]\n\t"
"v_mfma_f32_16x16x32_bf16 v[0:3], a[220:223], a[176:179], v[0:3]\n\t"
"s_mov_b32 s20, 503808\n\t"
"buffer_load_dwordx4 v[240:243], v252, s[8:11], s20 offen offset:0 sc1\n\t"
"s_waitcnt lgkmcnt(6)\n\t"
"v_mfma_f32_16x16x32_bf16 v[4:7], v[96:99], a[172:175], v[4:7]\n\t"
"v_mfma_f32_16x16x32_bf16 v[4:7], a[216:219], a[176:179], v[4:7]\n\t"
"v_mfma_f32_16x16x32_bf16 v[4:7], a[220:223], a[180:183], v[4:7]\n\t"
"buffer_load_dwordx4 v[248:251], v252, s[8:11], s20 offen offset:1024 sc1\n\t"
"s_waitcnt lgkmcnt(5)\n\t"
"v_mfma_f32_16x16x32_bf16 v[8:11], v[96:99], a[176:179], v[8:11]\n\t"
"v_mfma_f32_16x16x32_bf16 v[8:11], a[216:219], a[180:183], v[8:11]\n\t"
"v_mfma_f32_16x16x32_bf16 v[8:11], a[220:223], a[184:187], v[8:11]\n\t"
"buffer_load_dwordx4 a[208:211], v252, s[8:11], s20 offen offset:2048 sc1\n\t"
"s_waitcnt lgkmcnt(4)\n\t"
"v_mfma_f32_16x16x32_bf16 v[12:15], v[96:99], a[180:183], v[12:15]\n\t"
"v_mfma_f32_16x16x32_bf16 v[12:15], a[216:219], a[184:187], v[12:15]\n\t"
"v_mfma_f32_16x16x32_bf16 v[12:15], a[220:223], a[188:191], v[12:15]\n\t"
"s_mov_b32 s20, 516096\n\t"
"buffer_load_dwordx4 a[212:215], v252, s[8:11], s20 offen offset:0 sc1\n\t"
"s_waitcnt lgkmcnt(3)\n\t"
"v_mfma_f32_16x16x32_bf16 v[16:19], v[96:99], a[184:187], v[16:19]\n\t"
"v_mfma_f32_16x16x32_bf16 v[16:19], a[216:219], a[188:191], v[16:19]\n\t"
"v_mfma_f32_16x16x32_bf16 v[16:19], a[220:223], a[192:195], v[16:19]\n\t"
"ds_read_b64_tr_b16 v[232:233], v246 offset:1536\n\t"
"ds_read_b64_tr_b16 v[234:235], v246 offset:4608\n\t"
"s_waitcnt lgkmcnt(4)\n\t"
"v_mfma_f32_16x16x32_bf16 v[20:23], v[96:99], a[188:191], v[20:23]\n\t"
"v_mfma_f32_16x16x32_bf16 v[20:23], a[216:219], a[192:195], v[20:23]\n\t"
"v_mfma_f32_16x16x32_bf16 v[20:23], a[220:223], a[196:199], v[20:23]\n\t"
"buffer_load_dwordx4 a[232:235], v252, s[8:11], s20 offen offset:1024 sc1\n\t"
"s_waitcnt lgkmcnt(3)\n\t"
"v_mfma_f32_16x16x32_bf16 v[24:27], v[96:99], a[192:195], v[24:27]\n\t"
"v_mfma_f32_16x16x32_bf16 v[24:27], a[216:219], a[196:199], v[24:27]\n\t"
"v_mfma_f32_16x16x32_bf16 v[24:27], a[220:223], a[200:203], v[24:27]\n\t"
"buffer_load_dwordx4 a[236:239], v252, s[8:11], s20 offen offset:2048 sc1\n\t"
"s_waitcnt lgkmcnt(2)\n\t"
"v_mfma_f32_16x16x32_bf16 v[28:31], v[96:99], a[196:199], v[28:31]\n\t"
"ds_read_b64_tr_b16 v[224:225], v246 offset:7680\n\t"
"v_mfma_f32_16x16x32_bf16 v[28:31], a[216:219], a[200:203], v[28:31]\n\t"
"ds_read_b64_tr_b16 v[226:227], v246 offset:10752\n\t"
"v_mfma_f32_16x16x32_bf16 v[28:31], a[220:223], a[204:207], v[28:31]\n\t"
"s_waitcnt vmcnt(6)\n\t"
"v_mfma_f32_16x16x32_bf16 v[32:35], a[224:227], a[168:171], v[32:35]\n\t"
"v_mfma_f32_16x16x32_bf16 v[32:35], a[228:231], a[172:175], v[32:35]\n\t"
"v_mfma_f32_16x16x32_bf16 v[32:35], v[236:239], a[176:179], v[32:35]\n\t"
"s_mov_b32 s20, 528384\n\t"
"buffer_load_dwordx4 v[96:99], v252, s[8:11], s20 offen offset:0 sc1\n\t"
"v_mfma_f32_16x16x32_bf16 v[36:39], a[224:227], a[172:175], v[36:39]\n\t"
"v_mfma_f32_16x16x32_bf16 v[36:39], a[228:231], a[176:179], v[36:39]\n\t"
"v_mfma_f32_16x16x32_bf16 v[36:39], v[236:239], a[180:183], v[36:39]\n\t"
"buffer_load_dwordx4 a[216:219], v252, s[8:11], s20 offen offset:1024 sc1\n\t"
"s_waitcnt lgkmcnt(2)\n\t"
"v_mfma_f32_16x16x32_bf16 v[232:235], v[232:235], v[220:223], 0\n\t"
"buffer_load_dwordx4 a[220:223], v252, s[8:11], s20 offen offset:2048 sc1\n\t"
"v_mfma_f32_16x16x32_bf16 v[40:43], a[224:227], a[176:179], v[40:43]\n\t"
"ds_read_b64_tr_b16 v[228:229], v246 offset:13824\n\t"
"v_mfma_f32_16x16x32_bf16 v[40:43], a[228:231], a[180:183], v[40:43]\n\t"
"ds_read_b64_tr_b16 v[230:231], v246 offset:16896\n\t"
"v_mfma_f32_16x16x32_bf16 v[40:43], v[236:239], a[184:187], v[40:43]\n\t"
"v_mfma_f32_16x16x32_bf16 v[44:47], a[224:227], a[180:183], v[44:47]\n\t"
"v_mfma_f32_16x16x32_bf16 v[44:47], a[228:231], a[184:187], v[44:47]\n\t"
"v_cvt_pk_bf16_f32 v232, v232, v233\n\t"
"v_cvt_pk_bf16_f32 v233, v234, v235\n\t"
"v_mfma_f32_16x16x32_bf16 v[44:47], v[236:239], a[188:191], v[44:47]\n\t"
"s_mov_b32 s20, 540672\n\t"
"buffer_load_dwordx4 a[240:243], v252, s[8:11], s20 offen offset:0 sc1\n\t"
"s_waitcnt lgkmcnt(2)\n\t"
"v_mfma_f32_16x16x32_bf16 v[224:227], v[224:227], v[220:223], 0\n\t"
"buffer_load_dwordx4 a[244:247], v252, s[8:11], s20 offen offset:1024 sc1\n\t"
"v_mfma_f32_16x16x32_bf16 v[48:51], a[224:227], a[184:187], v[48:51]\n\t"
"v_mfma_f32_16x16x32_bf16 v[48:51], a[228:231], a[188:191], v[48:51]\n\t"
"v_mfma_f32_16x16x32_bf16 v[48:51], v[236:239], a[192:195], v[48:51]\n\t"
"buffer_load_dwordx4 a[248:251], v252, s[8:11], s20 offen offset:2048 sc1\n\t"
"v_mfma_f32_16x16x32_bf16 v[52:55], a[224:227], a[188:191], v[52:55]\n\t"
"v_mfma_f32_16x16x32_bf16 v[52:55], a[228:231], a[192:195], v[52:55]\n\t"
"s_nop 0\n\t"
"v_cvt_pk_bf16_f32 v234, v224, v225\n\t"
"v_cvt_pk_bf16_f32 v235, v226, v227\n\t"
"v_mfma_f32_16x16x32_bf16 v[52:55], v[236:239], a[196:199], v[52:55]\n\t"
"s_mov_b32 s20, 700416\n\t"
"buffer_load_dwordx4 a[252:255], v252, s[8:11], s20 offen offset:0 sc1\n\t"
"v_mfma_f32_16x16x32_bf16 v[56:59], a[224:227], a[192:195], v[56:59]\n\t"
"ds_read_b64_tr_b16 v[224:225], v246 offset:21504\n\t"
"v_mfma_f32_16x16x32_bf16 v[56:59], a[228:231], a[196:199], v[56:59]\n\t"
"ds_read_b64_tr_b16 v[226:227], v246 offset:24576\n\t"
"v_mfma_f32_16x16x32_bf16 v[56:59], v[236:239], a[200:203], v[56:59]\n\t"
"s_waitcnt lgkmcnt(2)\n\t"
"v_mfma_f32_16x16x32_bf16 v[228:231], v[228:231], v[220:223], 0\n\t"
"v_mfma_f32_16x16x32_bf16 v[60:63], a[224:227], a[196:199], v[60:63]\n\t"
"v_mfma_f32_16x16x32_bf16 v[60:63], a[228:231], a[200:203], v[60:63]\n\t"
"v_mfma_f32_16x16x32_bf16 v[60:63], v[236:239], a[204:207], v[60:63]\n\t"
"buffer_load_dwordx4 a[224:227], v252, s[8:11], s20 offen offset:1024 sc1\n\t"
"s_waitcnt vmcnt(11)\n\t"
"v_mfma_f32_16x16x32_bf16 v[64:67], v[240:243], a[168:171], v[64:67]\n\t"
"ds_read_b128 a[168:171], v244 offset:9216\n\t"
"v_mfma_f32_16x16x32_bf16 v[64:67], v[248:251], a[172:175], v[64:67]\n\t"
"v_cvt_pk_bf16_f32 v228, v228, v229\n\t"
"v_cvt_pk_bf16_f32 v229, v230, v231\n\t"
"v_mfma_f32_16x16x32_bf16 v[64:67], a[208:211], a[176:179], v[64:67]\n\t"
"v_mfma_f32_16x16x32_bf16 v[68:71], v[240:243], a[172:175], v[68:71]\n\t"
"ds_read_b128 a[172:175], v244 offset:9728\n\t"
"v_mfma_f32_16x16x32_bf16 v[68:71], v[248:251], a[176:179], v[68:71]\n\t"
"v_mfma_f32_16x16x32_bf16 v[68:71], a[208:211], a[180:183], v[68:71]\n\t"
"buffer_load_dwordx4 v[236:239], v252, s[8:11], s20 offen offset:2048 sc1\n\t"
"s_waitcnt lgkmcnt(2)\n\t"
"v_mfma_f32_16x16x32_bf16 v[224:227], v[224:227], v[220:223], 0\n\t"
"s_mov_b32 s20, 712704\n\t"
"buffer_load_dwordx4 a[228:231], v252, s[8:11], s20 offen offset:0 sc1\n\t"
"v_mfma_f32_16x16x32_bf16 v[72:75], v[240:243], a[176:179], v[72:75]\n\t"
"ds_read_b128 a[176:179], v244 offset:10240\n\t"
"v_mfma_f32_16x16x32_bf16 v[72:75], v[248:251], a[180:183], v[72:75]\n\t"
"v_mfma_f32_16x16x32_bf16 v[72:75], a[208:211], a[184:187], v[72:75]\n\t"
"v_mfma_f32_16x16x32_bf16 v[76:79], v[240:243], a[180:183], v[76:79]\n\t"
"ds_read_b128 a[180:183], v244 offset:10752\n\t"
"v_mfma_f32_16x16x32_bf16 v[76:79], v[248:251], a[184:187], v[76:79]\n\t"
"v_cvt_pk_bf16_f32 v230, v224, v225\n\t"
"v_cvt_pk_bf16_f32 v231, v226, v227\n\t"
"v_mfma_f32_16x16x32_bf16 v[76:79], a[208:211], a[188:191], v[76:79]\n\t"
"ds_read_b64_tr_b16 v[224:225], v246 offset:27648\n\t"
"v_mfma_f32_16x16x32_bf16 v[80:83], v[240:243], a[184:187], v[80:83]\n\t"
"ds_read_b64_tr_b16 v[226:227], v246 offset:30720\n\t"
"v_mfma_f32_16x16x32_bf16 v[80:83], v[248:251], a[188:191], v[80:83]\n\t"
"ds_read_b128 a[184:187], v244 offset:11264\n\t"
"v_mfma_f32_16x16x32_bf16 v[80:83], a[208:211], a[192:195], v[80:83]\n\t"
"v_mfma_f32_16x16x32_bf16 v[84:87], v[240:243], a[188:191], v[84:87]\n\t"
"ds_read_b128 a[188:191], v244 offset:11776\n\t"
"v_mfma_f32_16x16x32_bf16 v[84:87], v[248:251], a[192:195], v[84:87]\n\t"
"v_mfma_f32_16x16x32_bf16 v[84:87], a[208:211], a[196:199], v[84:87]\n\t"
"v_mfma_f32_16x16x32_bf16 v[88:91], v[240:243], a[192:195], v[88:91]\n\t"
"ds_read_b128 a[192:195], v244 offset:12288\n\t"
"v_mfma_f32_16x16x32_bf16 v[88:91], v[248:251], a[196:199], v[88:91]\n\t"
"v_mfma_f32_16x16x32_bf16 v[88:91], a[208:211], a[200:203], v[88:91]\n\t"
"v_mfma_f32_16x16x32_bf16 v[92:95], v[240:243], a[196:199], v[92:95]\n\t"
"ds_read_b128 a[196:199], v244 offset:12800\n\t"
"v_mfma_f32_16x16x32_bf16 v[92:95], v[248:251], a[200:203], v[92:95]\n\t"
"ds_read_b128 a[200:203], v244 offset:13312\n\t"
"v_mfma_f32_16x16x32_bf16 v[92:95], a[208:211], a[204:207], v[92:95]\n\t"
"buffer_load_dwordx4 v[240:243], v252, s[8:11], s20 offen offset:1024 sc1\n\t"
"s_waitcnt lgkmcnt(5)\n\t"
"v_mfma_f32_16x16x32_bf16 v[224:227], v[224:227], v[220:223], 0\n\t"
"ds_read_b128 a[204:207], v244 offset:13824\n\t"
"s_waitcnt vmcnt(11)\n\t"
"v_mfma_f32_16x16x32_bf16 v[100:103], a[212:215], a[168:171], v[100:103]\n\t"
"v_mfma_f32_16x16x32_bf16 v[100:103], a[232:235], a[172:175], v[100:103]\n\t"
"v_mfma_f32_16x16x32_bf16 v[100:103], a[236:239], a[176:179], v[100:103]\n\t"
"buffer_load_dwordx4 v[248:251], v252, s[8:11], s20 offen offset:2048 sc1\n\t"
"v_mfma_f32_16x16x32_bf16 v[104:107], a[212:215], a[172:175], v[104:107]\n\t"
"v_mfma_f32_16x16x32_bf16 v[104:107], a[232:235], a[176:179], v[104:107]\n\t"
"v_cvt_pk_bf16_f32 v224, v224, v225\n\t"
"v_cvt_pk_bf16_f32 v225, v226, v227\n\t"
"v_mfma_f32_16x16x32_bf16 v[104:107], a[236:239], a[180:183], v[104:107]\n\t"
"s_mov_b32 s20, 724992\n\t"
"buffer_load_dwordx4 a[208:211], v252, s[8:11], s20 offen offset:0 sc1\n\t"
"s_waitcnt lgkmcnt(0)\n\t"
"s_barrier\n\t"
"v_mfma_f32_16x16x32_bf16 v[108:111], a[212:215], a[176:179], v[108:111]\n\t"
"v_mfma_f32_16x16x32_bf16 v[108:111], a[232:235], a[180:183], v[108:111]\n\t"
"v_mfma_f32_16x16x32_bf16 v[108:111], a[236:239], a[184:187], v[108:111]\n\t"
"ds_write2st64_b64 v245, v[232:233], v[234:235] offset0:0 offset1:1\n\t"
"v_mfma_f32_16x16x32_bf16 v[112:115], a[212:215], a[180:183], v[112:115]\n\t"
"ds_read_b64_tr_b16 v[232:233], v246 offset:33792\n\t"
"v_mfma_f32_16x16x32_bf16 v[112:115], a[232:235], a[184:187], v[112:115]\n\t"
"ds_read_b64_tr_b16 v[234:235], v246 offset:36864\n\t"
"v_mfma_f32_16x16x32_bf16 v[112:115], a[236:239], a[188:191], v[112:115]\n\t"
"ds_write2st64_b64 v245, v[228:229], v[230:231] offset0:2 offset1:3\n\t"
"v_mfma_f32_16x16x32_bf16 v[116:119], a[212:215], a[184:187], v[116:119]\n\t"
"ds_read_b64_tr_b16 v[228:229], v246 offset:41472\n\t"
"v_mfma_f32_16x16x32_bf16 v[116:119], a[232:235], a[188:191], v[116:119]\n\t"
"ds_read_b64_tr_b16 v[230:231], v246 offset:44544\n\t"
"v_mfma_f32_16x16x32_bf16 v[116:119], a[236:239], a[192:195], v[116:119]\n\t"
"ds_write_b64 v245, v[224:225] offset:2048\n\t"
"v_mfma_f32_16x16x32_bf16 v[120:123], a[212:215], a[188:191], v[120:123]\n\t"
"ds_read_b64_tr_b16 v[224:225], v246 offset:47616\n\t"
"v_mfma_f32_16x16x32_bf16 v[120:123], a[232:235], a[192:195], v[120:123]\n\t"
"ds_read_b64_tr_b16 v[226:227], v246 offset:50688\n\t"
"v_mfma_f32_16x16x32_bf16 v[120:123], a[236:239], a[196:199], v[120:123]\n\t"
"s_waitcnt lgkmcnt(6)\n\t"
"v_mfma_f32_16x16x32_bf16 v[232:235], v[232:235], v[220:223], 0\n\t"
"s_add_u32 m0, s24, 66304\n\t"
"s_add_u32 s28, s21, 1966080\n\t"
"buffer_load_dwordx4 v247, s[12:15], s28 offen offset:256 nt lds\n\t"
"v_mfma_f32_16x16x32_bf16 v[124:127], a[212:215], a[192:195], v[124:127]\n\t"
"v_mfma_f32_16x16x32_bf16 v[124:127], a[232:235], a[196:199], v[124:127]\n\t"
"v_mfma_f32_16x16x32_bf16 v[124:127], a[236:239], a[200:203], v[124:127]\n\t"
"s_add_u32 m0, s24, 72448\n\t"
"s_add_u32 s28, s21, 2162688\n\t"
"buffer_load_dwordx4 v247, s[12:15], s28 offen offset:256 nt lds\n\t"
"s_waitcnt lgkmcnt(3)\n\t"
"v_mfma_f32_16x16x32_bf16 v[228:231], v[228:231], v[220:223], 0\n\t"
"s_add_u32 m0, s24, 80128\n\t"
"s_add_u32 s28, s21, 2359296\n\t"
"buffer_load_dwordx4 v247, s[12:15], s28 offen offset:256 nt lds\n\t"
"v_mfma_f32_16x16x32_bf16 v[128:131], a[212:215], a[196:199], v[128:131]\n\t"
"v_cvt_pk_bf16_f32 v232, v232, v233\n\t"
"v_cvt_pk_bf16_f32 v233, v234, v235\n\t"
"v_mfma_f32_16x16x32_bf16 v[128:131], a[232:235], a[200:203], v[128:131]\n\t"
"v_mfma_f32_16x16x32_bf16 v[128:131], a[236:239], a[204:207], v[128:131]\n\t"
"s_add_u32 m0, s24, 86272\n\t"
"s_add_u32 s28, s21, 2555904\n\t"
"buffer_load_dwordx4 v247, s[12:15], s28 offen offset:256 nt lds\n\t"
"s_waitcnt lgkmcnt(0)\n\t"
"v_mfma_f32_16x16x32_bf16 v[224:227], v[224:227], v[220:223], 0\n\t"
"s_add_u32 m0, s24, 32512\n\t"
"s_add_u32 s28, s21, 983040\n\t"
"buffer_load_dwordx4 v247, s[12:15], s28 offen offset:256 nt lds\n\t"
"s_waitcnt vmcnt(15)\n\t"
"v_mfma_f32_16x16x32_bf16 v[132:135], v[96:99], a[168:171], v[132:135]\n\t"
"v_cvt_pk_bf16_f32 v234, v228, v229\n\t"
"v_cvt_pk_bf16_f32 v235, v230, v231\n\t"
"v_mfma_f32_16x16x32_bf16 v[132:135], a[216:219], a[172:175], v[132:135]\n\t"
"ds_read_b64_tr_b16 v[228:229], v246 offset:53760\n\t"
"v_mfma_f32_16x16x32_bf16 v[132:135], a[220:223], a[176:179], v[132:135]\n\t"
"s_add_u32 m0, s24, 40192\n\t"
"s_add_u32 s28, s21, 1179648\n\t"
"buffer_load_dwordx4 v247, s[12:15], s28 offen offset:256 nt lds\n\t"
"v_mfma_f32_16x16x32_bf16 v[136:139], v[96:99], a[172:175], v[136:139]\n\t"
"ds_read_b64_tr_b16 v[230:231], v246 offset:56832\n\t"
"v_mfma_f32_16x16x32_bf16 v[136:139], a[216:219], a[176:179], v[136:139]\n\t"
"v_cvt_pk_bf16_f32 v224, v224, v225\n\t"
"v_cvt_pk_bf16_f32 v225, v226, v227\n\t"
"v_mfma_f32_16x16x32_bf16 v[136:139], a[220:223], a[180:183], v[136:139]\n\t"
"ds_write2st64_b64 v245, v[232:233], v[234:235] offset0:5 offset1:6\n\t"
"v_mfma_f32_16x16x32_bf16 v[140:143], v[96:99], a[176:179], v[140:143]\n\t"
"v_mfma_f32_16x16x32_bf16 v[140:143], a[216:219], a[180:183], v[140:143]\n\t"
"v_mfma_f32_16x16x32_bf16 v[140:143], a[220:223], a[184:187], v[140:143]\n\t"
"v_mfma_f32_16x16x32_bf16 v[144:147], v[96:99], a[180:183], v[144:147]\n\t"
"v_mfma_f32_16x16x32_bf16 v[144:147], a[216:219], a[184:187], v[144:147]\n\t"
"v_mfma_f32_16x16x32_bf16 v[144:147], a[220:223], a[188:191], v[144:147]\n\t"
"v_mfma_f32_16x16x32_bf16 v[148:151], v[96:99], a[184:187], v[148:151]\n\t"
"v_mfma_f32_16x16x32_bf16 v[148:151], a[216:219], a[188:191], v[148:151]\n\t"
"v_mfma_f32_16x16x32_bf16 v[148:151], a[220:223], a[192:195], v[148:151]\n\t"
"s_waitcnt lgkmcnt(1)\n\t"
"v_mfma_f32_16x16x32_bf16 v[228:231], v[228:231], v[220:223], 0\n\t"
"v_mfma_f32_16x16x32_bf16 v[152:155], v[96:99], a[188:191], v[152:155]\n\t"
"v_mfma_f32_16x16x32_bf16 v[152:155], a[216:219], a[192:195], v[152:155]\n\t"
"v_mfma_f32_16x16x32_bf16 v[152:155], a[220:223], a[196:199], v[152:155]\n\t"
"v_mfma_f32_16x16x32_bf16 v[156:159], v[96:99], a[192:195], v[156:159]\n\t"
"v_mfma_f32_16x16x32_bf16 v[156:159], a[216:219], a[196:199], v[156:159]\n\t"
"s_nop 2\n\t"
"v_cvt_pk_bf16_f32 v226, v228, v229\n\t"
"v_cvt_pk_bf16_f32 v227, v230, v231\n\t"
"v_mfma_f32_16x16x32_bf16 v[156:159], a[220:223], a[200:203], v[156:159]\n\t"
"ds_write2st64_b64 v245, v[224:225], v[226:227] offset0:7 offset1:8\n\t"
"v_mfma_f32_16x16x32_bf16 v[160:163], v[96:99], a[196:199], v[160:163]\n\t"
"v_mfma_f32_16x16x32_bf16 v[160:163], a[216:219], a[200:203], v[160:163]\n\t"
"v_mfma_f32_16x16x32_bf16 v[160:163], a[220:223], a[204:207], v[160:163]\n\t"
"s_waitcnt vmcnt(13)\n\t"
"v_mfma_f32_16x16x32_bf16 v[164:167], a[240:243], a[168:171], v[164:167]\n\t"
"v_mfma_f32_16x16x32_bf16 v[164:167], a[244:247], a[172:175], v[164:167]\n\t"
"v_mfma_f32_16x16x32_bf16 v[164:167], a[248:251], a[176:179], v[164:167]\n\t"
"buffer_load_dwordx4 v[96:99], v252, s[8:11], s20 offen offset:1024 sc1\n\t"
"s_waitcnt lgkmcnt(0)\n\t"
"s_barrier\n\t"
"v_mfma_f32_16x16x32_bf16 v[168:171], a[240:243], a[172:175], v[168:171]\n\t"
"ds_read_b128 a[168:171], v244 offset:0\n\t"
"v_mfma_f32_16x16x32_bf16 v[168:171], a[244:247], a[176:179], v[168:171]\n\t"
"v_mfma_f32_16x16x32_bf16 v[168:171], a[248:251], a[180:183], v[168:171]\n\t"
"ds_read_b128 a[172:175], v244 offset:512\n\t"
"v_mfma_f32_16x16x32_bf16 v[172:175], a[240:243], a[176:179], v[172:175]\n\t"
"ds_read_b128 a[176:179], v244 offset:1024\n\t"
"v_mfma_f32_16x16x32_bf16 v[172:175], a[244:247], a[180:183], v[172:175]\n\t"
"v_mfma_f32_16x16x32_bf16 v[172:175], a[248:251], a[184:187], v[172:175]\n\t"
"v_mfma_f32_16x16x32_bf16 v[176:179], a[240:243], a[180:183], v[176:179]\n\t"
"ds_read_b128 a[180:183], v244 offset:1536\n\t"
"v_mfma_f32_16x16x32_bf16 v[176:179], a[244:247], a[184:187], v[176:179]\n\t"
"v_mfma_f32_16x16x32_bf16 v[176:179], a[248:251], a[188:191], v[176:179]\n\t"
"v_mfma_f32_16x16x32_bf16 v[180:183], a[240:243], a[184:187], v[180:183]\n\t"
"ds_read_b128 a[184:187], v244 offset:2048\n\t"
"v_mfma_f32_16x16x32_bf16 v[180:183], a[244:247], a[188:191], v[180:183]\n\t"
"v_mfma_f32_16x16x32_bf16 v[180:183], a[248:251], a[192:195], v[180:183]\n\t"
"v_mfma_f32_16x16x32_bf16 v[184:187], a[240:243], a[188:191], v[184:187]\n\t"
"ds_read_b128 a[188:191], v244 offset:2560\n\t"
"v_mfma_f32_16x16x32_bf16 v[184:187], a[244:247], a[192:195], v[184:187]\n\t"
"v_mfma_f32_16x16x32_bf16 v[184:187], a[248:251], a[196:199], v[184:187]\n\t"
"v_mfma_f32_16x16x32_bf16 v[188:191], a[240:243], a[192:195], v[188:191]\n\t"
"ds_read_b128 a[192:195], v244 offset:3072\n\t"
"v_mfma_f32_16x16x32_bf16 v[188:191], a[244:247], a[196:199], v[188:191]\n\t"
"v_mfma_f32_16x16x32_bf16 v[188:191], a[248:251], a[200:203], v[188:191]\n\t"
"v_mfma_f32_16x16x32_bf16 v[192:195], a[240:243], a[196:199], v[192:195]\n\t"
"ds_read_b128 a[196:199], v244 offset:3584\n\t"
"v_mfma_f32_16x16x32_bf16 v[192:195], a[244:247], a[200:203], v[192:195]\n\t"
"ds_read_b128 a[200:203], v244 offset:4096\n\t"
"v_mfma_f32_16x16x32_bf16 v[192:195], a[248:251], a[204:207], v[192:195]\n\t"
"Lst8_%=:\n\t"
"s_waitcnt vmcnt(11) lgkmcnt(6)\n\t"
"v_mfma_f32_16x16x32_bf16 v[0:3], a[252:255], a[168:171], v[0:3]\n\t"
"ds_read_b128 a[204:207], v244 offset:4608\n\t"
"v_mfma_f32_16x16x32_bf16 v[0:3], a[224:227], a[172:175], v[0:3]\n\t"
"v_xor_b32 v246, 0xc0, v246\n\t"
"v_mfma_f32_16x16x32_bf16 v[0:3], v[236:239], a[176:179], v[0:3]\n\t"
"s_waitcnt lgkmcnt(6)\n\t"
"v_mfma_f32_16x16x32_bf16 v[4:7], a[252:255], a[172:175], v[4:7]\n\t"
"v_mfma_f32_16x16x32_bf16 v[4:7], a[224:227], a[176:179], v[4:7]\n\t"
"v_mfma_f32_16x16x32_bf16 v[4:7], v[236:239], a[180:183], v[4:7]\n\t"
"s_waitcnt lgkmcnt(5)\n\t"
"v_mfma_f32_16x16x32_bf16 v[8:11], a[252:255], a[176:179], v[8:11]\n\t"
"v_mfma_f32_16x16x32_bf16 v[8:11], a[224:227], a[180:183], v[8:11]\n\t"
"v_mfma_f32_16x16x32_bf16 v[8:11], v[236:239], a[184:187], v[8:11]\n\t"
"s_waitcnt lgkmcnt(4)\n\t"
"v_mfma_f32_16x16x32_bf16 v[12:15], a[252:255], a[180:183], v[12:15]\n\t"
"v_mfma_f32_16x16x32_bf16 v[12:15], a[224:227], a[184:187], v[12:15]\n\t"
"v_mfma_f32_16x16x32_bf16 v[12:15], v[236:239], a[188:191], v[12:15]\n\t"
"buffer_load_dwordx4 a[212:215], v252, s[8:11], s20 offen offset:2048 sc1\n\t"
"s_waitcnt lgkmcnt(3)\n\t"
"v_mfma_f32_16x16x32_bf16 v[16:19], a[252:255], a[184:187], v[16:19]\n\t"
"v_mfma_f32_16x16x32_bf16 v[16:19], a[224:227], a[188:191], v[16:19]\n\t"
"v_mfma_f32_16x16x32_bf16 v[16:19], v[236:239], a[192:195], v[16:19]\n\t"
"ds_read_b64_tr_b16 v[232:233], v246 offset:1536\n\t"
"ds_read_b64_tr_b16 v[234:235], v246 offset:4608\n\t"
"s_waitcnt lgkmcnt(4)\n\t"
"v_mfma_f32_16x16x32_bf16 v[20:23], a[252:255], a[188:191], v[20:23]\n\t"
"v_mfma_f32_16x16x32_bf16 v[20:23], a[224:227], a[192:195], v[20:23]\n\t"
"v_mfma_f32_16x16x32_bf16 v[20:23], v[236:239], a[196:199], v[20:23]\n\t"
"s_mov_b32 s20, 737280\n\t"
"buffer_load_dwordx4 a[216:219], v252, s[8:11], s20 offen offset:0 sc1\n\t"
"s_waitcnt lgkmcnt(3)\n\t"
"v_mfma_f32_16x16x32_bf16 v[24:27], a[252:255], a[192:195], v[24:27]\n\t"
"v_mfma_f32_16x16x32_bf16 v[24:27], a[224:227], a[196:199], v[24:27]\n\t"
"v_mfma_f32_16x16x32_bf16 v[24:27], v[236:239], a[200:203], v[24:27]\n\t"
"buffer_load_dwordx4 a[220:223], v252, s[8:11], s20 offen offset:1024 sc1\n\t"
"s_waitcnt lgkmcnt(2)\n\t"
"v_mfma_f32_16x16x32_bf16 v[28:31], a[252:255], a[196:199], v[28:31]\n\t"
"ds_read_b64_tr_b16 v[228:229], v246 offset:7680\n\t"
"v_mfma_f32_16x16x32_bf16 v[28:31], a[224:227], a[200:203], v[28:31]\n\t"
"ds_read_b64_tr_b16 v[230:231], v246 offset:10752\n\t"
"v_mfma_f32_16x16x32_bf16 v[28:31], v[236:239], a[204:207], v[28:31]\n\t"
"s_waitcnt vmcnt(11)\n\t"
"v_mfma_f32_16x16x32_bf16 v[32:35], a[228:231], a[168:171], v[32:35]\n\t"
"v_mfma_f32_16x16x32_bf16 v[32:35], v[240:243], a[172:175], v[32:35]\n\t"
"v_mfma_f32_16x16x32_bf16 v[32:35], v[248:251], a[176:179], v[32:35]\n\t"
"buffer_load_dwordx4 a[224:227], v252, s[8:11], s20 offen offset:2048 sc1\n\t"
"v_mfma_f32_16x16x32_bf16 v[36:39], a[228:231], a[172:175], v[36:39]\n\t"
"v_mfma_f32_16x16x32_bf16 v[36:39], v[240:243], a[176:179], v[36:39]\n\t"
"v_mfma_f32_16x16x32_bf16 v[36:39], v[248:251], a[180:183], v[36:39]\n\t"
"s_mov_b32 s20, 749568\n\t"
"buffer_load_dwordx4 v[236:239], v252, s[8:11], s20 offen offset:0 sc1\n\t"
"s_waitcnt lgkmcnt(2)\n\t"
"v_mfma_f32_16x16x32_bf16 v[232:235], v[232:235], v[220:223], 0\n\t"
"buffer_load_dwordx4 a[232:235], v252, s[8:11], s20 offen offset:1024 sc1\n\t"
"v_mfma_f32_16x16x32_bf16 v[40:43], a[228:231], a[176:179], v[40:43]\n\t"
"ds_read_b64_tr_b16 v[224:225], v246 offset:13824\n\t"
"v_mfma_f32_16x16x32_bf16 v[40:43], v[240:243], a[180:183], v[40:43]\n\t"
"v_mfma_f32_16x16x32_bf16 v[40:43], v[248:251], a[184:187], v[40:43]\n\t"
"ds_read_b64_tr_b16 v[226:227], v246 offset:16896\n\t"
"v_mfma_f32_16x16x32_bf16 v[44:47], a[228:231], a[180:183], v[44:47]\n\t"
"v_mfma_f32_16x16x32_bf16 v[44:47], v[240:243], a[184:187], v[44:47]\n\t"
"v_cvt_pk_bf16_f32 v232, v232, v233\n\t"
"v_cvt_pk_bf16_f32 v233, v234, v235\n\t"
"v_mfma_f32_16x16x32_bf16 v[44:47], v[248:251], a[188:191], v[44:47]\n\t"
"s_waitcnt lgkmcnt(2)\n\t"
"v_mfma_f32_16x16x32_bf16 v[228:231], v[228:231], v[220:223], 0\n\t"
"buffer_load_dwordx4 a[236:239], v252, s[8:11], s20 offen offset:2048 sc1\n\t"
"v_mfma_f32_16x16x32_bf16 v[48:51], a[228:231], a[184:187], v[48:51]\n\t"
"v_mfma_f32_16x16x32_bf16 v[48:51], v[240:243], a[188:191], v[48:51]\n\t"
"v_mfma_f32_16x16x32_bf16 v[48:51], v[248:251], a[192:195], v[48:51]\n\t"
"s_mov_b32 s20, 761856\n\t"
"buffer_load_dwordx4 a[240:243], v252, s[8:11], s20 offen offset:0 sc1\n\t"
"v_mfma_f32_16x16x32_bf16 v[52:55], a[228:231], a[188:191], v[52:55]\n\t"
"v_mfma_f32_16x16x32_bf16 v[52:55], v[240:243], a[192:195], v[52:55]\n\t"
"v_cvt_pk_bf16_f32 v234, v228, v229\n\t"
"v_cvt_pk_bf16_f32 v235, v230, v231\n\t"
"v_mfma_f32_16x16x32_bf16 v[52:55], v[248:251], a[196:199], v[52:55]\n\t"
"buffer_load_dwordx4 a[244:247], v252, s[8:11], s20 offen offset:1024 sc1\n\t"
"v_mfma_f32_16x16x32_bf16 v[56:59], a[228:231], a[192:195], v[56:59]\n\t"
"ds_read_b64_tr_b16 v[228:229], v246 offset:21504\n\t"
"v_mfma_f32_16x16x32_bf16 v[56:59], v[240:243], a[196:199], v[56:59]\n\t"
"ds_read_b64_tr_b16 v[230:231], v246 offset:24576\n\t"
"v_mfma_f32_16x16x32_bf16 v[56:59], v[248:251], a[200:203], v[56:59]\n\t"
"s_waitcnt lgkmcnt(2)\n\t"
"v_mfma_f32_16x16x32_bf16 v[224:227], v[224:227], v[220:223], 0\n\t"
"buffer_load_dwordx4 a[248:251], v252, s[8:11], s20 offen offset:2048 sc1\n\t"
"v_mfma_f32_16x16x32_bf16 v[60:63], a[228:231], a[196:199], v[60:63]\n\t"
"v_mfma_f32_16x16x32_bf16 v[60:63], v[240:243], a[200:203], v[60:63]\n\t"
"v_mfma_f32_16x16x32_bf16 v[60:63], v[248:251], a[204:207], v[60:63]\n\t"
"s_mov_b32 s20, 774144\n\t"
"buffer_load_dwordx4 v[240:243], v252, s[8:11], s20 offen offset:0 sc1\n\t"
"s_waitcnt vmcnt(10)\n\t"
"v_mfma_f32_16x16x32_bf16 v[64:67], a[208:211], a[168:171], v[64:67]\n\t"
"ds_read_b128 a[168:171], v244 offset:9216\n\t"
"v_mfma_f32_16x16x32_bf16 v[64:67], v[96:99], a[172:175], v[64:67]\n\t"
"v_cvt_pk_bf16_f32 v224, v224, v225\n\t"
"v_mfma_f32_16x16x32_bf16 v[64:67], a[212:215], a[176:179], v[64:67]\n\t"
"v_cvt_pk_bf16_f32 v225, v226, v227\n\t"
"v_mfma_f32_16x16x32_bf16 v[68:71], a[208:211], a[172:175], v[68:71]\n\t"
"ds_read_b128 a[172:175], v244 offset:9728\n\t"
"v_mfma_f32_16x16x32_bf16 v[68:71], v[96:99], a[176:179], v[68:71]\n\t"
"v_mfma_f32_16x16x32_bf16 v[68:71], a[212:215], a[180:183], v[68:71]\n\t"
"buffer_load_dwordx4 v[248:251], v252, s[8:11], s20 offen offset:1024 sc1\n\t"
"s_waitcnt lgkmcnt(2)\n\t"
"v_mfma_f32_16x16x32_bf16 v[228:231], v[228:231], v[220:223], 0\n\t"
"v_mfma_f32_16x16x32_bf16 v[72:75], a[208:211], a[176:179], v[72:75]\n\t"
"ds_read_b128 a[176:179], v244 offset:10240\n\t"
"v_mfma_f32_16x16x32_bf16 v[72:75], v[96:99], a[180:183], v[72:75]\n\t"
"v_mfma_f32_16x16x32_bf16 v[72:75], a[212:215], a[184:187], v[72:75]\n\t"
"buffer_load_dwordx4 a[228:231], v252, s[8:11], s20 offen offset:2048 sc1\n\t"
"v_mfma_f32_16x16x32_bf16 v[76:79], a[208:211], a[180:183], v[76:79]\n\t"
"ds_read_b128 a[180:183], v244 offset:10752\n\t"
"v_mfma_f32_16x16x32_bf16 v[76:79], v[96:99], a[184:187], v[76:79]\n\t"
"v_cvt_pk_bf16_f32 v226, v228, v229\n\t"
"v_cvt_pk_bf16_f32 v227, v230, v231\n\t"
"v_mfma_f32_16x16x32_bf16 v[76:79], a[212:215], a[188:191], v[76:79]\n\t"
"s_mov_b32 s20, 786432\n\t"
"buffer_load_dwordx4 a[252:255], v252, s[8:11], s20 offen offset:0 sc1\n\t"
"v_mfma_f32_16x16x32_bf16 v[80:83], a[208:211], a[184:187], v[80:83]\n\t"
"ds_read_b64_tr_b16 v[228:229], v246 offset:27648\n\t"
"v_mfma_f32_16x16x32_bf16 v[80:83], v[96:99], a[188:191], v[80:83]\n\t"
"ds_read_b64_tr_b16 v[230:231], v246 offset:30720\n\t"
"v_mfma_f32_16x16x32_bf16 v[80:83], a[212:215], a[192:195], v[80:83]\n\t"
"ds_read_b128 a[184:187], v244 offset:11264\n\t"
"v_mfma_f32_16x16x32_bf16 v[84:87], a[208:211], a[188:191], v[84:87]\n\t"
"ds_read_b128 a[188:191], v244 offset:11776\n\t"
"v_mfma_f32_16x16x32_bf16 v[84:87], v[96:99], a[192:195], v[84:87]\n\t"
"v_mfma_f32_16x16x32_bf16 v[84:87], a[212:215], a[196:199], v[84:87]\n\t"
"v_mfma_f32_16x16x32_bf16 v[88:91], a[208:211], a[192:195], v[88:91]\n\t"
"ds_read_b128 a[192:195], v244 offset:12288\n\t"
"v_mfma_f32_16x16x32_bf16 v[88:91], v[96:99], a[196:199], v[88:91]\n\t"
"v_mfma_f32_16x16x32_bf16 v[88:91], a[212:215], a[200:203], v[88:91]\n\t"
"v_mfma_f32_16x16x32_bf16 v[92:95], a[208:211], a[196:199], v[92:95]\n\t"
"ds_read_b128 a[196:199], v244 offset:12800\n\t"
"v_mfma_f32_16x16x32_bf16 v[92:95], v[96:99], a[200:203], v[92:95]\n\t"
"ds_read_b128 a[200:203], v244 offset:13312\n\t"
"v_mfma_f32_16x16x32_bf16 v[92:95], a[212:215], a[204:207], v[92:95]\n\t"
"buffer_load_dwordx4 a[208:211], v252, s[8:11], s20 offen offset:1024 sc1\n\t"
"s_waitcnt lgkmcnt(5)\n\t"
"v_mfma_f32_16x16x32_bf16 v[228:231], v[228:231], v[220:223], 0\n\t"
"ds_read_b128 a[204:207], v244 offset:13824\n\t"
"s_waitcnt vmcnt(11)\n\t"
"v_mfma_f32_16x16x32_bf16 v[100:103], a[216:219], a[168:171], v[100:103]\n\t"
"v_mfma_f32_16x16x32_bf16 v[100:103], a[220:223], a[172:175], v[100:103]\n\t"
"v_mfma_f32_16x16x32_bf16 v[100:103], a[224:227], a[176:179], v[100:103]\n\t"
"buffer_load_dwordx4 v[96:99], v252, s[8:11], s20 offen offset:2048 sc1\n\t"
"v_mfma_f32_16x16x32_bf16 v[104:107], a[216:219], a[172:175], v[104:107]\n\t"
"v_mfma_f32_16x16x32_bf16 v[104:107], a[220:223], a[176:179], v[104:107]\n\t"
"v_cvt_pk_bf16_f32 v228, v228, v229\n\t"
"v_cvt_pk_bf16_f32 v229, v230, v231\n\t"
"v_mfma_f32_16x16x32_bf16 v[104:107], a[224:227], a[180:183], v[104:107]\n\t"
"s_mov_b32 s20, 798720\n\t"
"buffer_load_dwordx4 a[212:215], v252, s[8:11], s20 offen offset:0 sc1\n\t"
"s_waitcnt lgkmcnt(0)\n\t"
"s_barrier\n\t"
"v_mfma_f32_16x16x32_bf16 v[108:111], a[216:219], a[176:179], v[108:111]\n\t"
"v_mfma_f32_16x16x32_bf16 v[108:111], a[220:223], a[180:183], v[108:111]\n\t"
"v_mfma_f32_16x16x32_bf16 v[108:111], a[224:227], a[184:187], v[108:111]\n\t"
"ds_write2st64_b64 v245, v[232:233], v[234:235] offset0:0 offset1:1\n\t"
"v_mfma_f32_16x16x32_bf16 v[112:115], a[216:219], a[180:183], v[112:115]\n\t"
"ds_read_b64_tr_b16 v[232:233], v246 offset:33792\n\t"
"v_mfma_f32_16x16x32_bf16 v[112:115], a[220:223], a[184:187], v[112:115]\n\t"
"ds_read_b64_tr_b16 v[234:235], v246 offset:36864\n\t"
"v_mfma_f32_16x16x32_bf16 v[112:115], a[224:227], a[188:191], v[112:115]\n\t"
"ds_write2st64_b64 v245, v[224:225], v[226:227] offset0:2 offset1:3\n\t"
"v_mfma_f32_16x16x32_bf16 v[116:119], a[216:219], a[184:187], v[116:119]\n\t"
"ds_read_b64_tr_b16 v[224:225], v246 offset:41472\n\t"
"v_mfma_f32_16x16x32_bf16 v[116:119], a[220:223], a[188:191], v[116:119]\n\t"
"ds_read_b64_tr_b16 v[226:227], v246 offset:44544\n\t"
"v_mfma_f32_16x16x32_bf16 v[116:119], a[224:227], a[192:195], v[116:119]\n\t"
"ds_write_b64 v245, v[228:229] offset:2048\n\t"
"v_mfma_f32_16x16x32_bf16 v[120:123], a[216:219], a[188:191], v[120:123]\n\t"
"ds_read_b64_tr_b16 v[228:229], v246 offset:47616\n\t"
"v_mfma_f32_16x16x32_bf16 v[120:123], a[220:223], a[192:195], v[120:123]\n\t"
"ds_read_b64_tr_b16 v[230:231], v246 offset:50688\n\t"
"v_mfma_f32_16x16x32_bf16 v[120:123], a[224:227], a[196:199], v[120:123]\n\t"
"s_waitcnt lgkmcnt(6)\n\t"
"v_mfma_f32_16x16x32_bf16 v[232:235], v[232:235], v[220:223], 0\n\t"
"v_mfma_f32_16x16x32_bf16 v[124:127], a[216:219], a[192:195], v[124:127]\n\t"
"v_mfma_f32_16x16x32_bf16 v[124:127], a[220:223], a[196:199], v[124:127]\n\t"
"v_mfma_f32_16x16x32_bf16 v[124:127], a[224:227], a[200:203], v[124:127]\n\t"
"s_waitcnt lgkmcnt(3)\n\t"
"v_mfma_f32_16x16x32_bf16 v[224:227], v[224:227], v[220:223], 0\n\t"
"v_mfma_f32_16x16x32_bf16 v[128:131], a[216:219], a[196:199], v[128:131]\n\t"
"s_nop 1\n\t"
"v_cvt_pk_bf16_f32 v232, v232, v233\n\t"
"v_cvt_pk_bf16_f32 v233, v234, v235\n\t"
"v_mfma_f32_16x16x32_bf16 v[128:131], a[220:223], a[200:203], v[128:131]\n\t"
"v_mfma_f32_16x16x32_bf16 v[128:131], a[224:227], a[204:207], v[128:131]\n\t"
"buffer_load_dwordx4 a[216:219], v252, s[8:11], s20 offen offset:1024 sc1\n\t"
"s_waitcnt lgkmcnt(0)\n\t"
"v_mfma_f32_16x16x32_bf16 v[228:231], v[228:231], v[220:223], 0\n\t"
"buffer_load_dwordx4 a[220:223], v252, s[8:11], s20 offen offset:2048 sc1\n\t"
"s_waitcnt vmcnt(12)\n\t"
"v_mfma_f32_16x16x32_bf16 v[132:135], v[236:239], a[168:171], v[132:135]\n\t"
"v_cvt_pk_bf16_f32 v234, v224, v225\n\t"
"v_cvt_pk_bf16_f32 v235, v226, v227\n\t"
"v_mfma_f32_16x16x32_bf16 v[132:135], a[232:235], a[172:175], v[132:135]\n\t"
"ds_read_b64_tr_b16 v[224:225], v246 offset:53760\n\t"
"v_mfma_f32_16x16x32_bf16 v[132:135], a[236:239], a[176:179], v[132:135]\n\t"
"s_mov_b32 s20, 811008\n\t"
"buffer_load_dwordx4 a[224:227], v252, s[8:11], s20 offen offset:0 sc1\n\t"
"v_mfma_f32_16x16x32_bf16 v[136:139], v[236:239], a[172:175], v[136:139]\n\t"
"ds_read_b64_tr_b16 v[226:227], v246 offset:56832\n\t"
"v_mfma_f32_16x16x32_bf16 v[136:139], a[232:235], a[176:179], v[136:139]\n\t"
"v_cvt_pk_bf16_f32 v228, v228, v229\n\t"
"v_cvt_pk_bf16_f32 v229, v230, v231\n\t"
"v_mfma_f32_16x16x32_bf16 v[136:139], a[236:239], a[180:183], v[136:139]\n\t"
"s_add_u32 m0, s24, 46336\n\t"
"s_add_u32 s28, s21, 1376256\n\t"
"buffer_load_dwordx4 v247, s[12:15], s28 offen offset:256 nt lds\n\t"
"s_add_u32 m0, s24, 52480\n\t"
"s_add_u32 s28, s21, 1572864\n\t"
"buffer_load_dwordx4 v247, s[12:15], s28 offen offset:256 nt lds\n\t"
"v_mfma_f32_16x16x32_bf16 v[140:143], v[236:239], a[176:179], v[140:143]\n\t"
"v_mfma_f32_16x16x32_bf16 v[140:143], a[232:235], a[180:183], v[140:143]\n\t"
"v_mfma_f32_16x16x32_bf16 v[140:143], a[236:239], a[184:187], v[140:143]\n\t"
"ds_write2st64_b64 v245, v[232:233], v[234:235] offset0:5 offset1:6\n\t"
"s_add_u32 m0, s24, 92416\n\t"
"s_add_u32 s28, s21, 2752512\n\t"
"buffer_load_dwordx4 v247, s[12:15], s28 offen offset:256 nt lds\n\t"
"v_mfma_f32_16x16x32_bf16 v[144:147], v[236:239], a[180:183], v[144:147]\n\t"
"v_mfma_f32_16x16x32_bf16 v[144:147], a[232:235], a[184:187], v[144:147]\n\t"
"v_mfma_f32_16x16x32_bf16 v[144:147], a[236:239], a[188:191], v[144:147]\n\t"
"s_add_u32 m0, s24, 100096\n\t"
"s_add_u32 s28, s21, 2949120\n\t"
"buffer_load_dwordx4 v247, s[12:15], s28 offen offset:256 nt lds\n\t"
"s_add_u32 m0, s24, 106240\n\t"
"s_add_u32 s28, s21, 3145728\n\t"
"buffer_load_dwordx4 v247, s[12:15], s28 offen offset:256 nt lds\n\t"
"v_mfma_f32_16x16x32_bf16 v[148:151], v[236:239], a[184:187], v[148:151]\n\t"
"v_mfma_f32_16x16x32_bf16 v[148:151], a[232:235], a[188:191], v[148:151]\n\t"
"v_mfma_f32_16x16x32_bf16 v[148:151], a[236:239], a[192:195], v[148:151]\n\t"
"s_add_u32 m0, s24, 112384\n\t"
"s_add_u32 s28, s21, 3342336\n\t"
"buffer_load_dwordx4 v247, s[12:15], s28 offen offset:256 nt lds\n\t"
"s_waitcnt lgkmcnt(1)\n\t"
"v_mfma_f32_16x16x32_bf16 v[224:227], v[224:227], v[220:223], 0\n\t"
"s_add_u32 m0, s25, 256\n\t"
"s_nop 0\n\t"
"buffer_load_dwordx4 v254, s[12:15], 0 offen offset:256 nt lds\n\t"
"v_mfma_f32_16x16x32_bf16 v[152:155], v[236:239], a[188:191], v[152:155]\n\t"
"v_mfma_f32_16x16x32_bf16 v[152:155], a[232:235], a[192:195], v[152:155]\n\t"
"v_mfma_f32_16x16x32_bf16 v[152:155], a[236:239], a[196:199], v[152:155]\n\t"
"s_add_u32 m0, s26, 256\n\t"
"s_mov_b64 exec, s[36:37]\n\t"
"buffer_load_dwordx4 v254, s[12:15], s27 offen offset:256 nt lds\n\t"
"s_mov_b64 exec, -1\n\t"
"v_mfma_f32_16x16x32_bf16 v[156:159], v[236:239], a[192:195], v[156:159]\n\t"
"v_mfma_f32_16x16x32_bf16 v[156:159], a[232:235], a[196:199], v[156:159]\n\t"
"v_cvt_pk_bf16_f32 v230, v224, v225\n\t"
"v_cvt_pk_bf16_f32 v231, v226, v227\n\t"
"v_mfma_f32_16x16x32_bf16 v[156:159], a[236:239], a[200:203], v[156:159]\n\t"
"ds_write2st64_b64 v245, v[228:229], v[230:231] offset0:7 offset1:8\n\t"
"v_mfma_f32_16x16x32_bf16 v[160:163], v[236:239], a[196:199], v[160:163]\n\t"
"v_mfma_f32_16x16x32_bf16 v[160:163], a[232:235], a[200:203], v[160:163]\n\t"
"v_mfma_f32_16x16x32_bf16 v[160:163], a[236:239], a[204:207], v[160:163]\n\t"
"s_waitcnt vmcnt(18)\n\t"
"v_mfma_f32_16x16x32_bf16 v[164:167], a[240:243], a[168:171], v[164:167]\n\t"
"v_mfma_f32_16x16x32_bf16 v[164:167], a[244:247], a[172:175], v[164:167]\n\t"
"v_mfma_f32_16x16x32_bf16 v[164:167], a[248:251], a[176:179], v[164:167]\n\t"
"s_waitcnt lgkmcnt(0)\n\t"
"s_barrier\n\t"
"v_mfma_f32_16x16x32_bf16 v[168:171], a[240:243], a[172:175], v[168:171]\n\t"
"ds_read_b128 a[168:171], v244 offset:0\n\t"
"v_mfma_f32_16x16x32_bf16 v[168:171], a[244:247], a[176:179], v[168:171]\n\t"
"ds_read_b128 a[172:175], v244 offset:512\n\t"
"v_mfma_f32_16x16x32_bf16 v[168:171], a[248:251], a[180:183], v[168:171]\n\t"
"v_mfma_f32_16x16x32_bf16 v[172:175], a[240:243], a[176:179], v[172:175]\n\t"
"ds_read_b128 a[176:179], v244 offset:1024\n\t"
"v_mfma_f32_16x16x32_bf16 v[172:175], a[244:247], a[180:183], v[172:175]\n\t"
"v_mfma_f32_16x16x32_bf16 v[172:175], a[248:251], a[184:187], v[172:175]\n\t"
"v_mfma_f32_16x16x32_bf16 v[176:179], a[240:243], a[180:183], v[176:179]\n\t"
"ds_read_b128 a[180:183], v244 offset:1536\n\t"
"v_mfma_f32_16x16x32_bf16 v[176:179], a[244:247], a[184:187], v[176:179]\n\t"
"v_mfma_f32_16x16x32_bf16 v[176:179], a[248:251], a[188:191], v[176:179]\n\t"
"v_mfma_f32_16x16x32_bf16 v[180:183], a[240:243], a[184:187], v[180:183]\n\t"
"ds_read_b128 a[184:187], v244 offset:2048\n\t"
"v_mfma_f32_16x16x32_bf16 v[180:183], a[244:247], a[188:191], v[180:183]\n\t"
"v_mfma_f32_16x16x32_bf16 v[180:183], a[248:251], a[192:195], v[180:183]\n\t"
"v_mfma_f32_16x16x32_bf16 v[184:187], a[240:243], a[188:191], v[184:187]\n\t"
"ds_read_b128 a[188:191], v244 offset:2560\n\t"
"v_mfma_f32_16x16x32_bf16 v[184:187], a[244:247], a[192:195], v[184:187]\n\t"
"v_mfma_f32_16x16x32_bf16 v[184:187], a[248:251], a[196:199], v[184:187]\n\t"
"v_mfma_f32_16x16x32_bf16 v[188:191], a[240:243], a[192:195], v[188:191]\n\t"
"ds_read_b128 a[192:195], v244 offset:3072\n\t"
"v_mfma_f32_16x16x32_bf16 v[188:191], a[244:247], a[196:199], v[188:191]\n\t"
"v_mfma_f32_16x16x32_bf16 v[188:191], a[248:251], a[200:203], v[188:191]\n\t"
"v_mfma_f32_16x16x32_bf16 v[192:195], a[240:243], a[196:199], v[192:195]\n\t"
"ds_read_b128 a[196:199], v244 offset:3584\n\t"
"v_mfma_f32_16x16x32_bf16 v[192:195], a[244:247], a[200:203], v[192:195]\n\t"
"ds_read_b128 a[200:203], v244 offset:4096\n\t"
"v_mfma_f32_16x16x32_bf16 v[192:195], a[248:251], a[204:207], v[192:195]\n\t"
"Lst9_%=:\n\t"
"s_waitcnt vmcnt(15) lgkmcnt(6)\n\t"
"v_mfma_f32_16x16x32_bf16 v[196:199], v[240:243], a[168:171], v[196:199]\n\t"
"ds_read_b128 a[204:207], v244 offset:4608\n\t"
"v_mfma_f32_16x16x32_bf16 v[196:199], v[248:251], a[172:175], v[196:199]\n\t"
"v_mfma_f32_16x16x32_bf16 v[196:199], a[228:231], a[176:179], v[196:199]\n\t"
"s_waitcnt lgkmcnt(6)\n\t"
"v_mfma_f32_16x16x32_bf16 v[204:207], v[240:243], a[172:175], v[204:207]\n\t"
"v_mfma_f32_16x16x32_bf16 v[204:207], v[248:251], a[176:179], v[204:207]\n\t"
"v_mfma_f32_16x16x32_bf16 v[204:207], a[228:231], a[180:183], v[204:207]\n\t"
"s_waitcnt lgkmcnt(5)\n\t"
"v_mfma_f32_16x16x32_bf16 v[212:215], v[240:243], a[176:179], v[212:215]\n\t"
"v_mfma_f32_16x16x32_bf16 v[212:215], v[248:251], a[180:183], v[212:215]\n\t"
"v_mfma_f32_16x16x32_bf16 v[212:215], a[228:231], a[184:187], v[212:215]\n\t"
"s_waitcnt lgkmcnt(4)\n\t"
"v_mfma_f32_16x16x32_bf16 a[0:3], v[240:243], a[180:183], a[0:3]\n\t"
"v_mfma_f32_16x16x32_bf16 a[0:3], v[248:251], a[184:187], a[0:3]\n\t"
"v_mfma_f32_16x16x32_bf16 a[0:3], a[228:231], a[188:191], a[0:3]\n\t"
"buffer_load_dwordx4 v[236:239], v252, s[8:11], s20 offen offset:1024 sc1\n\t"
"s_waitcnt lgkmcnt(3)\n\t"
"v_mfma_f32_16x16x32_bf16 a[8:11], v[240:243], a[184:187], a[8:11]\n\t"
"v_mfma_f32_16x16x32_bf16 a[8:11], v[248:251], a[188:191], a[8:11]\n\t"
"v_mfma_f32_16x16x32_bf16 a[8:11], a[228:231], a[192:195], a[8:11]\n\t"
"ds_read_b64_tr_b16 v[232:233], v246 offset:512\n\t"
"ds_read_b64_tr_b16 v[234:235], v246 offset:3584\n\t"
"s_waitcnt lgkmcnt(4)\n\t"
"v_mfma_f32_16x16x32_bf16 a[16:19], v[240:243], a[188:191], a[16:19]\n\t"
"v_mfma_f32_16x16x32_bf16 a[16:19], v[248:251], a[192:195], a[16:19]\n\t"
"v_mfma_f32_16x16x32_bf16 a[16:19], a[228:231], a[196:199], a[16:19]\n\t"
"s_waitcnt lgkmcnt(3)\n\t"
"v_mfma_f32_16x16x32_bf16 a[24:27], v[240:243], a[192:195], a[24:27]\n\t"
"v_mfma_f32_16x16x32_bf16 a[24:27], v[248:251], a[196:199], a[24:27]\n\t"
"v_mfma_f32_16x16x32_bf16 a[24:27], a[228:231], a[200:203], a[24:27]\n\t"
"s_waitcnt lgkmcnt(2)\n\t"
"v_mfma_f32_16x16x32_bf16 a[32:35], v[240:243], a[196:199], a[32:35]\n\t"
"ds_read_b64_tr_b16 v[224:225], v246 offset:6656\n\t"
"v_mfma_f32_16x16x32_bf16 a[32:35], v[248:251], a[200:203], a[32:35]\n\t"
"ds_read_b64_tr_b16 v[226:227], v246 offset:9728\n\t"
"v_mfma_f32_16x16x32_bf16 a[32:35], a[228:231], a[204:207], a[32:35]\n\t"
"s_waitcnt vmcnt(13)\n\t"
"v_mfma_f32_16x16x32_bf16 a[40:43], a[252:255], a[168:171], a[40:43]\n\t"
"v_mfma_f32_16x16x32_bf16 a[40:43], a[208:211], a[172:175], a[40:43]\n\t"
"v_mfma_f32_16x16x32_bf16 a[40:43], v[96:99], a[176:179], a[40:43]\n\t"
"v_mfma_f32_16x16x32_bf16 a[48:51], a[252:255], a[172:175], a[48:51]\n\t"
"v_mfma_f32_16x16x32_bf16 a[48:51], a[208:211], a[176:179], a[48:51]\n\t"
"v_mfma_f32_16x16x32_bf16 a[48:51], v[96:99], a[180:183], a[48:51]\n\t"
"s_waitcnt lgkmcnt(2)\n\t"
"v_mfma_f32_16x16x32_bf16 v[232:235], v[232:235], v[220:223], 0\n\t"
"v_mfma_f32_16x16x32_bf16 a[56:59], a[252:255], a[176:179], a[56:59]\n\t"
"ds_read_b64_tr_b16 v[228:229], v246 offset:12800\n\t"
"v_mfma_f32_16x16x32_bf16 a[56:59], a[208:211], a[180:183], a[56:59]\n\t"
"ds_read_b64_tr_b16 v[230:231], v246 offset:15872\n\t"
"v_mfma_f32_16x16x32_bf16 a[56:59], v[96:99], a[184:187], a[56:59]\n\t"
"v_mfma_f32_16x16x32_bf16 a[64:67], a[252:255], a[180:183], a[64:67]\n\t"
"v_mfma_f32_16x16x32_bf16 a[64:67], a[208:211], a[184:187], a[64:67]\n\t"
"s_nop 0\n\t"
"v_cvt_pk_bf16_f32 v232, v232, v233\n\t"
"v_mfma_f32_16x16x32_bf16 a[64:67], v[96:99], a[188:191], a[64:67]\n\t"
"v_cvt_pk_bf16_f32 v233, v234, v235\n\t"
"s_waitcnt lgkmcnt(2)\n\t"
"v_mfma_f32_16x16x32_bf16 v[224:227], v[224:227], v[220:223], 0\n\t"
"v_mfma_f32_16x16x32_bf16 a[72:75], a[252:255], a[184:187], a[72:75]\n\t"
"v_mfma_f32_16x16x32_bf16 a[72:75], a[208:211], a[188:191], a[72:75]\n\t"
"v_mfma_f32_16x16x32_bf16 a[72:75], v[96:99], a[192:195], a[72:75]\n\t"
"buffer_load_dwordx4 v[240:243], v252, s[8:11], s20 offen offset:2048 sc1\n\t"
"v_mfma_f32_16x16x32_bf16 a[80:83], a[252:255], a[188:191], a[80:83]\n\t"
"v_mfma_f32_16x16x32_bf16 a[80:83], a[208:211], a[192:195], a[80:83]\n\t"
"s_nop 1\n\t"
"v_cvt_pk_bf16_f32 v234, v224, v225\n\t"
"v_cvt_pk_bf16_f32 v235, v226, v227\n\t"
"v_mfma_f32_16x16x32_bf16 a[80:83], v[96:99], a[196:199], a[80:83]\n\t"
"s_mov_b32 s20, 823296\n\t"
"buffer_load_dwordx4 v[248:251], v252, s[8:11], s20 offen offset:0 sc1\n\t"
"v_mfma_f32_16x16x32_bf16 a[88:91], a[252:255], a[192:195], a[88:91]\n\t"
"ds_read_b64_tr_b16 v[224:225], v246 offset:20480\n\t"
"v_mfma_f32_16x16x32_bf16 a[88:91], a[208:211], a[196:199], a[88:91]\n\t"
"ds_read_b64_tr_b16 v[226:227], v246 offset:23552\n\t"
"v_mfma_f32_16x16x32_bf16 a[88:91], v[96:99], a[200:203], a[88:91]\n\t"
"buffer_load_dwordx4 a[228:231], v252, s[8:11], s20 offen offset:1024 sc1\n\t"
"s_waitcnt lgkmcnt(2)\n\t"
"v_mfma_f32_16x16x32_bf16 v[228:231], v[228:231], v[220:223], 0\n\t"
"v_mfma_f32_16x16x32_bf16 a[96:99], a[252:255], a[196:199], a[96:99]\n\t"
"v_mfma_f32_16x16x32_bf16 a[96:99], a[208:211], a[200:203], a[96:99]\n\t"
"v_mfma_f32_16x16x32_bf16 a[96:99], v[96:99], a[204:207], a[96:99]\n\t"
"buffer_load_dwordx4 a[208:211], v252, s[8:11], s20 offen offset:2048 sc1\n\t"
"s_waitcnt vmcnt(14)\n\t"
"v_mfma_f32_16x16x32_bf16 a[104:107], a[212:215], a[168:171], a[104:107]\n\t"
"ds_read_b128 a[168:171], v244 offset:9216\n\t"
"v_mfma_f32_16x16x32_bf16 a[104:107], a[216:219], a[172:175], a[104:107]\n\t"
"v_cvt_pk_bf16_f32 v228, v228, v229\n\t"
"v_mfma_f32_16x16x32_bf16 a[104:107], a[220:223], a[176:179], a[104:107]\n\t"
"v_cvt_pk_bf16_f32 v229, v230, v231\n\t"
"v_mfma_f32_16x16x32_bf16 a[112:115], a[212:215], a[172:175], a[112:115]\n\t"
"ds_read_b128 a[172:175], v244 offset:9728\n\t"
"v_mfma_f32_16x16x32_bf16 a[112:115], a[216:219], a[176:179], a[112:115]\n\t"
"v_mfma_f32_16x16x32_bf16 a[112:115], a[220:223], a[180:183], a[112:115]\n\t"
"s_mov_b32 s20, 835584\n\t"
"buffer_load_dwordx4 v[96:99], v252, s[8:11], s20 offen offset:0 sc1\n\t"
"s_waitcnt lgkmcnt(2)\n\t"
"v_mfma_f32_16x16x32_bf16 v[224:227], v[224:227], v[220:223], 0\n\t"
"v_mfma_f32_16x16x32_bf16 a[120:123], a[212:215], a[176:179], a[120:123]\n\t"
"ds_read_b128 a[176:179], v244 offset:10240\n\t"
"v_mfma_f32_16x16x32_bf16 a[120:123], a[216:219], a[180:183], a[120:123]\n\t"
"v_mfma_f32_16x16x32_bf16 a[120:123], a[220:223], a[184:187], a[120:123]\n\t"
"buffer_load_dwordx4 a[232:235], v252, s[8:11], s20 offen offset:1024 sc1\n\t"
"v_mfma_f32_16x16x32_bf16 a[128:131], a[212:215], a[180:183], a[128:131]\n\t"
"ds_read_b128 a[180:183], v244 offset:10752\n\t"
"v_mfma_f32_16x16x32_bf16 a[128:131], a[216:219], a[184:187], a[128:131]\n\t"
"v_cvt_pk_bf16_f32 v230, v224, v225\n\t"
"v_cvt_pk_bf16_f32 v231, v226, v227\n\t"
"v_mfma_f32_16x16x32_bf16 a[128:131], a[220:223], a[188:191], a[128:131]\n\t"
"buffer_load_dwordx4 a[236:239], v252, s[8:11], s20 offen offset:2048 sc1\n\t"
"v_mfma_f32_16x16x32_bf16 a[136:139], a[212:215], a[184:187], a[136:139]\n\t"
"ds_read_b64_tr_b16 v[224:225], v246 offset:26624\n\t"
"v_mfma_f32_16x16x32_bf16 a[136:139], a[216:219], a[188:191], a[136:139]\n\t"
"ds_read_b64_tr_b16 v[226:227], v246 offset:29696\n\t"
"v_mfma_f32_16x16x32_bf16 a[136:139], a[220:223], a[192:195], a[136:139]\n\t"
"s_mov_b32 s20, 995328\n\t"
"buffer_load_dwordx4 a[240:243], v252, s[8:11], s20 offen offset:0 sc1\n\t"
"v_mfma_f32_16x16x32_bf16 a[144:147], a[212:215], a[188:191], a[144:147]\n\t"
"ds_read_b128 a[184:187], v244 offset:11264\n\t"
"v_mfma_f32_16x16x32_bf16 a[144:147], a[216:219], a[192:195], a[144:147]\n\t"
"ds_read_b128 a[188:191], v244 offset:11776\n\t"
"v_mfma_f32_16x16x32_bf16 a[144:147], a[220:223], a[196:199], a[144:147]\n\t"
"v_mfma_f32_16x16x32_bf16 a[152:155], a[212:215], a[192:195], a[152:155]\n\t"
"ds_read_b128 a[192:195], v244 offset:12288\n\t"
"v_mfma_f32_16x16x32_bf16 a[152:155], a[216:219], a[196:199], a[152:155]\n\t"
"v_mfma_f32_16x16x32_bf16 a[152:155], a[220:223], a[200:203], a[152:155]\n\t"
"buffer_load_dwordx4 a[244:247], v252, s[8:11], s20 offen offset:1024 sc1\n\t"
"v_mfma_f32_16x16x32_bf16 a[160:163], a[212:215], a[196:199], a[160:163]\n\t"
"ds_read_b128 a[196:199], v244 offset:12800\n\t"
"v_mfma_f32_16x16x32_bf16 a[160:163], a[216:219], a[200:203], a[160:163]\n\t"
"ds_read_b128 a[200:203], v244 offset:13312\n\t"
"v_mfma_f32_16x16x32_bf16 a[160:163], a[220:223], a[204:207], a[160:163]\n\t"
"buffer_load_dwordx4 a[212:215], v252, s[8:11], s20 offen offset:2048 sc1\n\t"
"s_waitcnt lgkmcnt(5)\n\t"
"v_mfma_f32_16x16x32_bf16 v[224:227], v[224:227], v[220:223], 0\n\t"
"ds_read_b128 a[204:207], v244 offset:13824\n\t"
"s_waitcnt vmcnt(9)\n\t"
"v_mfma_f32_16x16x32_bf16 v[200:203], a[224:227], a[168:171], v[200:203]\n\t"
"v_mfma_f32_16x16x32_bf16 v[200:203], v[236:239], a[172:175], v[200:203]\n\t"
"v_mfma_f32_16x16x32_bf16 v[200:203], v[240:243], a[176:179], v[200:203]\n\t"
"s_mov_b32 s20, 1007616\n\t"
"buffer_load_dwordx4 a[216:219], v252, s[8:11], s20 offen offset:0 sc1\n\t"
"v_mfma_f32_16x16x32_bf16 v[208:211], a[224:227], a[172:175], v[208:211]\n\t"
"v_mfma_f32_16x16x32_bf16 v[208:211], v[236:239], a[176:179], v[208:211]\n\t"
"v_cvt_pk_bf16_f32 v224, v224, v225\n\t"
"v_cvt_pk_bf16_f32 v225, v226, v227\n\t"
"v_mfma_f32_16x16x32_bf16 v[208:211], v[240:243], a[180:183], v[208:211]\n\t"
"buffer_load_dwordx4 a[220:223], v252, s[8:11], s20 offen offset:1024 sc1\n\t"
"s_waitcnt lgkmcnt(0)\n\t"
"s_barrier\n\t"
"v_mfma_f32_16x16x32_bf16 v[216:219], a[224:227], a[176:179], v[216:219]\n\t"
"v_mfma_f32_16x16x32_bf16 v[216:219], v[236:239], a[180:183], v[216:219]\n\t"
"v_mfma_f32_16x16x32_bf16 v[216:219], v[240:243], a[184:187], v[216:219]\n\t"
"ds_write2st64_b64 v245, v[232:233], v[234:235] offset0:0 offset1:1\n\t"
"buffer_load_dwordx4 a[248:251], v252, s[8:11], s20 offen offset:2048 sc1\n\t"
"v_mfma_f32_16x16x32_bf16 a[4:7], a[224:227], a[180:183], a[4:7]\n\t"
"ds_read_b64_tr_b16 v[232:233], v246 offset:32768\n\t"
"v_mfma_f32_16x16x32_bf16 a[4:7], v[236:239], a[184:187], a[4:7]\n\t"
"ds_read_b64_tr_b16 v[234:235], v246 offset:35840\n\t"
"v_mfma_f32_16x16x32_bf16 a[4:7], v[240:243], a[188:191], a[4:7]\n\t"
"ds_write2st64_b64 v245, v[228:229], v[230:231] offset0:2 offset1:3\n\t"
"ds_write_b64 v245, v[224:225] offset:2048\n\t"
"v_mfma_f32_16x16x32_bf16 a[12:15], a[224:227], a[184:187], a[12:15]\n\t"
"ds_read_b64_tr_b16 v[228:229], v246 offset:40448\n\t"
"v_mfma_f32_16x16x32_bf16 a[12:15], v[236:239], a[188:191], a[12:15]\n\t"
"ds_read_b64_tr_b16 v[230:231], v246 offset:43520\n\t"
"v_mfma_f32_16x16x32_bf16 a[12:15], v[240:243], a[192:195], a[12:15]\n\t"
"s_mov_b32 s20, 1019904\n\t"
"buffer_load_dwordx4 a[252:255], v252, s[8:11], s20 offen offset:0 sc1\n\t"
"v_mfma_f32_16x16x32_bf16 a[20:23], a[224:227], a[188:191], a[20:23]\n\t"
"ds_read_b64_tr_b16 v[224:225], v246 offset:46592\n\t"
"v_mfma_f32_16x16x32_bf16 a[20:23], v[236:239], a[192:195], a[20:23]\n\t"
"ds_read_b64_tr_b16 v[226:227], v246 offset:49664\n\t"
"v_mfma_f32_16x16x32_bf16 a[20:23], v[240:243], a[196:199], a[20:23]\n\t"
"s_waitcnt lgkmcnt(6)\n\t"
"v_mfma_f32_16x16x32_bf16 v[232:235], v[232:235], v[220:223], 0\n\t"
"v_mfma_f32_16x16x32_bf16 a[28:31], a[224:227], a[192:195], a[28:31]\n\t"
"v_mfma_f32_16x16x32_bf16 a[28:31], v[236:239], a[196:199], a[28:31]\n\t"
"v_mfma_f32_16x16x32_bf16 a[28:31], v[240:243], a[200:203], a[28:31]\n\t"
"s_waitcnt lgkmcnt(2)\n\t"
"v_mfma_f32_16x16x32_bf16 v[228:231], v[228:231], v[220:223], 0\n\t"
"v_mfma_f32_16x16x32_bf16 a[36:39], a[224:227], a[196:199], a[36:39]\n\t"
"s_nop 1\n\t"
"v_cvt_pk_bf16_f32 v232, v232, v233\n\t"
"v_cvt_pk_bf16_f32 v233, v234, v235\n\t"
"v_mfma_f32_16x16x32_bf16 a[36:39], v[236:239], a[200:203], a[36:39]\n\t"
"v_mfma_f32_16x16x32_bf16 a[36:39], v[240:243], a[204:207], a[36:39]\n\t"
"buffer_load_dwordx4 a[224:227], v252, s[8:11], s20 offen offset:1024 sc1\n\t"
"s_waitcnt lgkmcnt(0)\n\t"
"v_mfma_f32_16x16x32_bf16 v[224:227], v[224:227], v[220:223], 0\n\t"
"buffer_load_dwordx4 v[236:239], v252, s[8:11], s20 offen offset:2048 sc1\n\t"
"s_waitcnt vmcnt(12)\n\t"
"v_mfma_f32_16x16x32_bf16 a[44:47], v[248:251], a[168:171], a[44:47]\n\t"
"v_cvt_pk_bf16_f32 v234, v228, v229\n\t"
"v_cvt_pk_bf16_f32 v235, v230, v231\n\t"
"v_mfma_f32_16x16x32_bf16 a[44:47], a[228:231], a[172:175], a[44:47]\n\t"
"ds_read_b64_tr_b16 v[228:229], v246 offset:52736\n\t"
"v_mfma_f32_16x16x32_bf16 a[44:47], a[208:211], a[176:179], a[44:47]\n\t"
"s_mov_b32 s20, 1032192\n\t"
"buffer_load_dwordx4 v[240:243], v252, s[8:11], s20 offen offset:0 sc1\n\t"
"v_mfma_f32_16x16x32_bf16 a[52:55], v[248:251], a[172:175], a[52:55]\n\t"
"ds_read_b64_tr_b16 v[230:231], v246 offset:55808\n\t"
"v_mfma_f32_16x16x32_bf16 a[52:55], a[228:231], a[176:179], a[52:55]\n\t"
"v_cvt_pk_bf16_f32 v224, v224, v225\n\t"
"v_cvt_pk_bf16_f32 v225, v226, v227\n\t"
"v_mfma_f32_16x16x32_bf16 a[52:55], a[208:211], a[180:183], a[52:55]\n\t"
"ds_write2st64_b64 v245, v[232:233], v[234:235] offset0:5 offset1:6\n\t"
"v_mfma_f32_16x16x32_bf16 a[60:63], v[248:251], a[176:179], a[60:63]\n\t"
"v_mfma_f32_16x16x32_bf16 a[60:63], a[228:231], a[180:183], a[60:63]\n\t"
"v_mfma_f32_16x16x32_bf16 a[60:63], a[208:211], a[184:187], a[60:63]\n\t"
"v_mfma_f32_16x16x32_bf16 a[68:71], v[248:251], a[180:183], a[68:71]\n\t"
"v_mfma_f32_16x16x32_bf16 a[68:71], a[228:231], a[184:187], a[68:71]\n\t"
"v_mfma_f32_16x16x32_bf16 a[68:71], a[208:211], a[188:191], a[68:71]\n\t"
"v_mfma_f32_16x16x32_bf16 a[76:79], v[248:251], a[184:187], a[76:79]\n\t"
"v_mfma_f32_16x16x32_bf16 a[76:79], a[228:231], a[188:191], a[76:79]\n\t"
"v_mfma_f32_16x16x32_bf16 a[76:79], a[208:211], a[192:195], a[76:79]\n\t"
"s_waitcnt lgkmcnt(1)\n\t"
"v_mfma_f32_16x16x32_bf16 v[228:231], v[228:231], v[220:223], 0\n\t"
"v_mfma_f32_16x16x32_bf16 a[84:87], v[248:251], a[188:191], a[84:87]\n\t"
"v_mfma_f32_16x16x32_bf16 a[84:87], a[228:231], a[192:195], a[84:87]\n\t"
"v_mfma_f32_16x16x32_bf16 a[84:87], a[208:211], a[196:199], a[84:87]\n\t"
"v_mfma_f32_16x16x32_bf16 a[92:95], v[248:251], a[192:195], a[92:95]\n\t"
"v_mfma_f32_16x16x32_bf16 a[92:95], a[228:231], a[196:199], a[92:95]\n\t"
"s_nop 2\n\t"
"v_cvt_pk_bf16_f32 v226, v228, v229\n\t"
"v_cvt_pk_bf16_f32 v227, v230, v231\n\t"
"v_mfma_f32_16x16x32_bf16 a[92:95], a[208:211], a[200:203], a[92:95]\n\t"
"ds_write2st64_b64 v245, v[224:225], v[226:227] offset0:7 offset1:8\n\t"
"v_mfma_f32_16x16x32_bf16 a[100:103], v[248:251], a[196:199], a[100:103]\n\t"
"v_mfma_f32_16x16x32_bf16 a[100:103], a[228:231], a[200:203], a[100:103]\n\t"
"v_mfma_f32_16x16x32_bf16 a[100:103], a[208:211], a[204:207], a[100:103]\n\t"
"buffer_load_dwordx4 v[248:251], v252, s[8:11], s20 offen offset:1024 sc1\n\t"
"s_waitcnt vmcnt(11)\n\t"
"v_mfma_f32_16x16x32_bf16 a[108:111], v[96:99], a[168:171], a[108:111]\n\t"
"v_mfma_f32_16x16x32_bf16 a[108:111], a[232:235], a[172:175], a[108:111]\n\t"
"v_mfma_f32_16x16x32_bf16 a[108:111], a[236:239], a[176:179], a[108:111]\n\t"
"buffer_load_dwordx4 a[208:211], v252, s[8:11], s20 offen offset:2048 sc1\n\t"
"s_waitcnt lgkmcnt(0)\n\t"
"s_barrier\n\t"
"v_mfma_f32_16x16x32_bf16 a[116:119], v[96:99], a[172:175], a[116:119]\n\t"
"ds_read_b128 a[168:171], v244 offset:0\n\t"
"v_mfma_f32_16x16x32_bf16 a[116:119], a[232:235], a[176:179], a[116:119]\n\t"
"ds_read_b128 a[172:175], v244 offset:512\n\t"
"v_mfma_f32_16x16x32_bf16 a[116:119], a[236:239], a[180:183], a[116:119]\n\t"
"s_mov_b32 s20, 1044480\n\t"
"buffer_load_dwordx4 a[228:231], v252, s[8:11], s20 offen offset:0 sc1\n\t"
"v_mfma_f32_16x16x32_bf16 a[124:127], v[96:99], a[176:179], a[124:127]\n\t"
"ds_read_b128 a[176:179], v244 offset:1024\n\t"
"v_mfma_f32_16x16x32_bf16 a[124:127], a[232:235], a[180:183], a[124:127]\n\t"
"v_mfma_f32_16x16x32_bf16 a[124:127], a[236:239], a[184:187], a[124:127]\n\t"
"v_mfma_f32_16x16x32_bf16 a[132:135], v[96:99], a[180:183], a[132:135]\n\t"
"ds_read_b128 a[180:183], v244 offset:1536\n\t"
"v_mfma_f32_16x16x32_bf16 a[132:135], a[232:235], a[184:187], a[132:135]\n\t"
"v_mfma_f32_16x16x32_bf16 a[132:135], a[236:239], a[188:191], a[132:135]\n\t"
"v_mfma_f32_16x16x32_bf16 a[140:143], v[96:99], a[184:187], a[140:143]\n\t"
"ds_read_b128 a[184:187], v244 offset:2048\n\t"
"v_mfma_f32_16x16x32_bf16 a[140:143], a[232:235], a[188:191], a[140:143]\n\t"
"v_mfma_f32_16x16x32_bf16 a[140:143], a[236:239], a[192:195], a[140:143]\n\t"
"v_mfma_f32_16x16x32_bf16 a[148:151], v[96:99], a[188:191], a[148:151]\n\t"
"ds_read_b128 a[188:191], v244 offset:2560\n\t"
"v_mfma_f32_16x16x32_bf16 a[148:151], a[232:235], a[192:195], a[148:151]\n\t"
"v_mfma_f32_16x16x32_bf16 a[148:151], a[236:239], a[196:199], a[148:151]\n\t"
"v_mfma_f32_16x16x32_bf16 a[156:159], v[96:99], a[192:195], a[156:159]\n\t"
"ds_read_b128 a[192:195], v244 offset:3072\n\t"
"v_mfma_f32_16x16x32_bf16 a[156:159], a[232:235], a[196:199], a[156:159]\n\t"
"v_mfma_f32_16x16x32_bf16 a[156:159], a[236:239], a[200:203], a[156:159]\n\t"
"v_mfma_f32_16x16x32_bf16 a[164:167], v[96:99], a[196:199], a[164:167]\n\t"
"ds_read_b128 a[196:199], v244 offset:3584\n\t"
"v_mfma_f32_16x16x32_bf16 a[164:167], a[232:235], a[200:203], a[164:167]\n\t"
"ds_read_b128 a[200:203], v244 offset:4096\n\t"
"v_mfma_f32_16x16x32_bf16 a[164:167], a[236:239], a[204:207], a[164:167]\n\t"
"buffer_load_dwordx4 v[96:99], v252, s[8:11], s20 offen offset:1024 sc1\n\t"
"Lst10_%=:\n\t"
"s_waitcnt vmcnt(11) lgkmcnt(6)\n\t"
"v_mfma_f32_16x16x32_bf16 v[196:199], a[240:243], a[168:171], v[196:199]\n\t"
"ds_read_b128 a[204:207], v244 offset:4608\n\t"
"v_xor_b32 v246, 0xc0, v246\n\t"
"v_mfma_f32_16x16x32_bf16 v[196:199], a[244:247], a[172:175], v[196:199]\n\t"
"v_mfma_f32_16x16x32_bf16 v[196:199], a[212:215], a[176:179], v[196:199]\n\t"
"s_waitcnt lgkmcnt(6)\n\t"
"v_mfma_f32_16x16x32_bf16 v[204:207], a[240:243], a[172:175], v[204:207]\n\t"
"v_mfma_f32_16x16x32_bf16 v[204:207], a[244:247], a[176:179], v[204:207]\n\t"
"v_mfma_f32_16x16x32_bf16 v[204:207], a[212:215], a[180:183], v[204:207]\n\t"
"buffer_load_dwordx4 a[232:235], v252, s[8:11], s20 offen offset:2048 sc1\n\t"
"s_waitcnt lgkmcnt(5)\n\t"
"v_mfma_f32_16x16x32_bf16 v[212:215], a[240:243], a[176:179], v[212:215]\n\t"
"v_mfma_f32_16x16x32_bf16 v[212:215], a[244:247], a[180:183], v[212:215]\n\t"
"v_mfma_f32_16x16x32_bf16 v[212:215], a[212:215], a[184:187], v[212:215]\n\t"
"s_mov_b32 s20, 1056768\n\t"
"buffer_load_dwordx4 a[236:239], v252, s[8:11], s20 offen offset:0 sc1\n\t"
"s_waitcnt lgkmcnt(4)\n\t"
"v_mfma_f32_16x16x32_bf16 a[0:3], a[240:243], a[180:183], a[0:3]\n\t"
"v_mfma_f32_16x16x32_bf16 a[0:3], a[244:247], a[184:187], a[0:3]\n\t"
"v_mfma_f32_16x16x32_bf16 a[0:3], a[212:215], a[188:191], a[0:3]\n\t"
"s_waitcnt lgkmcnt(3)\n\t"
"v_mfma_f32_16x16x32_bf16 a[8:11], a[240:243], a[184:187], a[8:11]\n\t"
"v_mfma_f32_16x16x32_bf16 a[8:11], a[244:247], a[188:191], a[8:11]\n\t"
"v_mfma_f32_16x16x32_bf16 a[8:11], a[212:215], a[192:195], a[8:11]\n\t"
"ds_read_b64_tr_b16 v[232:233], v246 offset:512\n\t"
"ds_read_b64_tr_b16 v[234:235], v246 offset:3584\n\t"
"s_waitcnt lgkmcnt(4)\n\t"
"v_mfma_f32_16x16x32_bf16 a[16:19], a[240:243], a[188:191], a[16:19]\n\t"
"v_mfma_f32_16x16x32_bf16 a[16:19], a[244:247], a[192:195], a[16:19]\n\t"
"v_mfma_f32_16x16x32_bf16 a[16:19], a[212:215], a[196:199], a[16:19]\n\t"
"s_waitcnt lgkmcnt(3)\n\t"
"v_mfma_f32_16x16x32_bf16 a[24:27], a[240:243], a[192:195], a[24:27]\n\t"
"v_mfma_f32_16x16x32_bf16 a[24:27], a[244:247], a[196:199], a[24:27]\n\t"
"v_mfma_f32_16x16x32_bf16 a[24:27], a[212:215], a[200:203], a[24:27]\n\t"
"s_waitcnt lgkmcnt(2)\n\t"
"v_mfma_f32_16x16x32_bf16 a[32:35], a[240:243], a[196:199], a[32:35]\n\t"
"ds_read_b64_tr_b16 v[228:229], v246 offset:6656\n\t"
"v_mfma_f32_16x16x32_bf16 a[32:35], a[244:247], a[200:203], a[32:35]\n\t"
"ds_read_b64_tr_b16 v[230:231], v246 offset:9728\n\t"
"v_mfma_f32_16x16x32_bf16 a[32:35], a[212:215], a[204:207], a[32:35]\n\t"
"buffer_load_dwordx4 a[240:243], v252, s[8:11], s20 offen offset:1024 sc1\n\t"
"s_waitcnt vmcnt(11)\n\t"
"v_mfma_f32_16x16x32_bf16 a[40:43], a[216:219], a[168:171], a[40:43]\n\t"
"v_mfma_f32_16x16x32_bf16 a[40:43], a[220:223], a[172:175], a[40:43]\n\t"
"v_mfma_f32_16x16x32_bf16 a[40:43], a[248:251], a[176:179], a[40:43]\n\t"
"buffer_load_dwordx4 a[212:215], v252, s[8:11], s20 offen offset:2048 sc1\n\t"
"v_mfma_f32_16x16x32_bf16 a[48:51], a[216:219], a[172:175], a[48:51]\n\t"
"v_mfma_f32_16x16x32_bf16 a[48:51], a[220:223], a[176:179], a[48:51]\n\t"
"v_mfma_f32_16x16x32_bf16 a[48:51], a[248:251], a[180:183], a[48:51]\n\t"
"s_mov_b32 s20, 921600\n\t"
"buffer_load_dwordx4 a[244:247], v252, s[8:11], s20 offen offset:0 sc1\n\t"
"s_waitcnt lgkmcnt(2)\n\t"
"v_mfma_f32_16x16x32_bf16 v[232:235], v[232:235], v[220:223], 0\n\t"
"v_mfma_f32_16x16x32_bf16 a[56:59], a[216:219], a[176:179], a[56:59]\n\t"
"ds_read_b64_tr_b16 v[224:225], v246 offset:12800\n\t"
"v_mfma_f32_16x16x32_bf16 a[56:59], a[220:223], a[180:183], a[56:59]\n\t"
"ds_read_b64_tr_b16 v[226:227], v246 offset:15872\n\t"
"v_mfma_f32_16x16x32_bf16 a[56:59], a[248:251], a[184:187], a[56:59]\n\t"
"v_mfma_f32_16x16x32_bf16 a[64:67], a[216:219], a[180:183], a[64:67]\n\t"
"v_mfma_f32_16x16x32_bf16 a[64:67], a[220:223], a[184:187], a[64:67]\n\t"
"s_nop 0\n\t"
"v_cvt_pk_bf16_f32 v232, v232, v233\n\t"
"v_mfma_f32_16x16x32_bf16 a[64:67], a[248:251], a[188:191], a[64:67]\n\t"
"v_cvt_pk_bf16_f32 v233, v234, v235\n\t"
"s_waitcnt lgkmcnt(2)\n\t"
"v_mfma_f32_16x16x32_bf16 v[228:231], v[228:231], v[220:223], 0\n\t"
"v_mfma_f32_16x16x32_bf16 a[72:75], a[216:219], a[184:187], a[72:75]\n\t"
"v_mfma_f32_16x16x32_bf16 a[72:75], a[220:223], a[188:191], a[72:75]\n\t"
"v_mfma_f32_16x16x32_bf16 a[72:75], a[248:251], a[192:195], a[72:75]\n\t"
"v_mfma_f32_16x16x32_bf16 a[80:83], a[216:219], a[188:191], a[80:83]\n\t"
"v_mfma_f32_16x16x32_bf16 a[80:83], a[220:223], a[192:195], a[80:83]\n\t"
"s_nop 2\n\t"
"v_cvt_pk_bf16_f32 v234, v228, v229\n\t"
"v_cvt_pk_bf16_f32 v235, v230, v231\n\t"
"v_mfma_f32_16x16x32_bf16 a[80:83], a[248:251], a[196:199], a[80:83]\n\t"
"ds_read_b64_tr_b16 v[228:229], v246 offset:20480\n\t"
"v_mfma_f32_16x16x32_bf16 a[88:91], a[216:219], a[192:195], a[88:91]\n\t"
"ds_read_b64_tr_b16 v[230:231], v246 offset:23552\n\t"
"v_mfma_f32_16x16x32_bf16 a[88:91], a[220:223], a[196:199], a[88:91]\n\t"
"v_mfma_f32_16x16x32_bf16 a[88:91], a[248:251], a[200:203], a[88:91]\n\t"
"s_waitcnt lgkmcnt(2)\n\t"
"v_mfma_f32_16x16x32_bf16 v[224:227], v[224:227], v[220:223], 0\n\t"
"v_mfma_f32_16x16x32_bf16 a[96:99], a[216:219], a[196:199], a[96:99]\n\t"
"v_mfma_f32_16x16x32_bf16 a[96:99], a[220:223], a[200:203], a[96:99]\n\t"
"v_mfma_f32_16x16x32_bf16 a[96:99], a[248:251], a[204:207], a[96:99]\n\t"
"buffer_load_dwordx4 a[216:219], v252, s[8:11], s20 offen offset:1024 sc1\n\t"
"s_waitcnt vmcnt(11)\n\t"
"v_mfma_f32_16x16x32_bf16 a[104:107], a[252:255], a[168:171], a[104:107]\n\t"
"ds_read_b128 a[168:171], v244 offset:9216\n\t"
"v_mfma_f32_16x16x32_bf16 a[104:107], a[224:227], a[172:175], a[104:107]\n\t"
"v_cvt_pk_bf16_f32 v224, v224, v225\n\t"
"v_cvt_pk_bf16_f32 v225, v226, v227\n\t"
"v_mfma_f32_16x16x32_bf16 a[104:107], v[236:239], a[176:179], a[104:107]\n\t"
"v_mfma_f32_16x16x32_bf16 a[112:115], a[252:255], a[172:175], a[112:115]\n\t"
"ds_read_b128 a[172:175], v244 offset:9728\n\t"
"v_mfma_f32_16x16x32_bf16 a[112:115], a[224:227], a[176:179], a[112:115]\n\t"
"v_mfma_f32_16x16x32_bf16 a[112:115], v[236:239], a[180:183], a[112:115]\n\t"
"buffer_load_dwordx4 a[220:223], v252, s[8:11], s20 offen offset:2048 sc1\n\t"
"s_waitcnt lgkmcnt(2)\n\t"
"v_mfma_f32_16x16x32_bf16 v[228:231], v[228:231], v[220:223], 0\n\t"
"s_mov_b32 s20, 933888\n\t"
"buffer_load_dwordx4 a[248:251], v252, s[8:11], s20 offen offset:0 sc1\n\t"
"v_mfma_f32_16x16x32_bf16 a[120:123], a[252:255], a[176:179], a[120:123]\n\t"
"ds_read_b128 a[176:179], v244 offset:10240\n\t"
"v_mfma_f32_16x16x32_bf16 a[120:123], a[224:227], a[180:183], a[120:123]\n\t"
"v_mfma_f32_16x16x32_bf16 a[120:123], v[236:239], a[184:187], a[120:123]\n\t"
"v_mfma_f32_16x16x32_bf16 a[128:131], a[252:255], a[180:183], a[128:131]\n\t"
"ds_read_b128 a[180:183], v244 offset:10752\n\t"
"v_mfma_f32_16x16x32_bf16 a[128:131], a[224:227], a[184:187], a[128:131]\n\t"
"v_cvt_pk_bf16_f32 v226, v228, v229\n\t"
"v_cvt_pk_bf16_f32 v227, v230, v231\n\t"
"v_mfma_f32_16x16x32_bf16 a[128:131], v[236:239], a[188:191], a[128:131]\n\t"
"ds_read_b64_tr_b16 v[228:229], v246 offset:26624\n\t"
"v_mfma_f32_16x16x32_bf16 a[136:139], a[252:255], a[184:187], a[136:139]\n\t"
"ds_read_b64_tr_b16 v[230:231], v246 offset:29696\n\t"
"v_mfma_f32_16x16x32_bf16 a[136:139], a[224:227], a[188:191], a[136:139]\n\t"
"ds_read_b128 a[184:187], v244 offset:11264\n\t"
"v_mfma_f32_16x16x32_bf16 a[136:139], v[236:239], a[192:195], a[136:139]\n\t"
"v_mfma_f32_16x16x32_bf16 a[144:147], a[252:255], a[188:191], a[144:147]\n\t"
"ds_read_b128 a[188:191], v244 offset:11776\n\t"
"v_mfma_f32_16x16x32_bf16 a[144:147], a[224:227], a[192:195], a[144:147]\n\t"
"v_mfma_f32_16x16x32_bf16 a[144:147], v[236:239], a[196:199], a[144:147]\n\t"
"v_mfma_f32_16x16x32_bf16 a[152:155], a[252:255], a[192:195], a[152:155]\n\t"
"ds_read_b128 a[192:195], v244 offset:12288\n\t"
"v_mfma_f32_16x16x32_bf16 a[152:155], a[224:227], a[196:199], a[152:155]\n\t"
"v_mfma_f32_16x16x32_bf16 a[152:155], v[236:239], a[200:203], a[152:155]\n\t"
"v_mfma_f32_16x16x32_bf16 a[160:163], a[252:255], a[196:199], a[160:163]\n\t"
"ds_read_b128 a[196:199], v244 offset:12800\n\t"
"v_mfma_f32_16x16x32_bf16 a[160:163], a[224:227], a[200:203], a[160:163]\n\t"
"ds_read_b128 a[200:203], v244 offset:13312\n\t"
"v_mfma_f32_16x16x32_bf16 a[160:163], v[236:239], a[204:207], a[160:163]\n\t"
"buffer_load_dwordx4 a[252:255], v252, s[8:11], s20 offen offset:1024 sc1\n\t"
"s_waitcnt lgkmcnt(5)\n\t"
"v_mfma_f32_16x16x32_bf16 v[228:231], v[228:231], v[220:223], 0\n\t"
"ds_read_b128 a[204:207], v244 offset:13824\n\t"
"s_waitcnt vmcnt(11)\n\t"
"v_mfma_f32_16x16x32_bf16 v[200:203], v[240:243], a[168:171], v[200:203]\n\t"
"v_mfma_f32_16x16x32_bf16 v[200:203], v[248:251], a[172:175], v[200:203]\n\t"
"v_mfma_f32_16x16x32_bf16 v[200:203], a[208:211], a[176:179], v[200:203]\n\t"
"buffer_load_dwordx4 v[236:239], v252, s[8:11], s20 offen offset:2048 sc1\n\t"
"v_mfma_f32_16x16x32_bf16 v[208:211], v[240:243], a[172:175], v[208:211]\n\t"
"v_mfma_f32_16x16x32_bf16 v[208:211], v[248:251], a[176:179], v[208:211]\n\t"
"v_cvt_pk_bf16_f32 v228, v228, v229\n\t"
"v_cvt_pk_bf16_f32 v229, v230, v231\n\t"
"v_mfma_f32_16x16x32_bf16 v[208:211], a[208:211], a[180:183], v[208:211]\n\t"
"s_mov_b32 s20, 946176\n\t"
"buffer_load_dwordx4 a[224:227], v252, s[8:11], s20 offen offset:0 sc1\n\t"
"s_waitcnt lgkmcnt(0)\n\t"
"s_barrier\n\t"
"v_mfma_f32_16x16x32_bf16 v[216:219], v[240:243], a[176:179], v[216:219]\n\t"
"v_mfma_f32_16x16x32_bf16 v[216:219], v[248:251], a[180:183], v[216:219]\n\t"
"v_mfma_f32_16x16x32_bf16 v[216:219], a[208:211], a[184:187], v[216:219]\n\t"
"ds_write2st64_b64 v245, v[232:233], v[234:235] offset0:0 offset1:1\n\t"
"v_mfma_f32_16x16x32_bf16 a[4:7], v[240:243], a[180:183], a[4:7]\n\t"
"ds_read_b64_tr_b16 v[232:233], v246 offset:32768\n\t"
"v_mfma_f32_16x16x32_bf16 a[4:7], v[248:251], a[184:187], a[4:7]\n\t"
"ds_read_b64_tr_b16 v[234:235], v246 offset:35840\n\t"
"v_mfma_f32_16x16x32_bf16 a[4:7], a[208:211], a[188:191], a[4:7]\n\t"
"ds_write2st64_b64 v245, v[224:225], v[226:227] offset0:2 offset1:3\n\t"
"v_mfma_f32_16x16x32_bf16 a[12:15], v[240:243], a[184:187], a[12:15]\n\t"
"ds_read_b64_tr_b16 v[224:225], v246 offset:40448\n\t"
"v_mfma_f32_16x16x32_bf16 a[12:15], v[248:251], a[188:191], a[12:15]\n\t"
"ds_read_b64_tr_b16 v[226:227], v246 offset:43520\n\t"
"v_mfma_f32_16x16x32_bf16 a[12:15], a[208:211], a[192:195], a[12:15]\n\t"
"ds_write_b64 v245, v[228:229] offset:2048\n\t"
"v_mfma_f32_16x16x32_bf16 a[20:23], v[240:243], a[188:191], a[20:23]\n\t"
"ds_read_b64_tr_b16 v[228:229], v246 offset:46592\n\t"
"v_mfma_f32_16x16x32_bf16 a[20:23], v[248:251], a[192:195], a[20:23]\n\t"
"ds_read_b64_tr_b16 v[230:231], v246 offset:49664\n\t"
"v_mfma_f32_16x16x32_bf16 a[20:23], a[208:211], a[196:199], a[20:23]\n\t"
"s_waitcnt lgkmcnt(6)\n\t"
"v_mfma_f32_16x16x32_bf16 v[232:235], v[232:235], v[220:223], 0\n\t"
"v_mfma_f32_16x16x32_bf16 a[28:31], v[240:243], a[192:195], a[28:31]\n\t"
"v_mfma_f32_16x16x32_bf16 a[28:31], v[248:251], a[196:199], a[28:31]\n\t"
"v_mfma_f32_16x16x32_bf16 a[28:31], a[208:211], a[200:203], a[28:31]\n\t"
"s_waitcnt lgkmcnt(3)\n\t"
"v_mfma_f32_16x16x32_bf16 v[224:227], v[224:227], v[220:223], 0\n\t"
"v_mfma_f32_16x16x32_bf16 a[36:39], v[240:243], a[196:199], a[36:39]\n\t"
"s_nop 1\n\t"
"v_cvt_pk_bf16_f32 v232, v232, v233\n\t"
"v_cvt_pk_bf16_f32 v233, v234, v235\n\t"
"v_mfma_f32_16x16x32_bf16 a[36:39], v[248:251], a[200:203], a[36:39]\n\t"
"v_mfma_f32_16x16x32_bf16 a[36:39], a[208:211], a[204:207], a[36:39]\n\t"
"buffer_load_dwordx4 v[240:243], v252, s[8:11], s20 offen offset:1024 sc1\n\t"
"s_waitcnt lgkmcnt(0)\n\t"
"v_mfma_f32_16x16x32_bf16 v[228:231], v[228:231], v[220:223], 0\n\t"
"buffer_load_dwordx4 v[248:251], v252, s[8:11], s20 offen offset:2048 sc1\n\t"
"s_waitcnt vmcnt(12)\n\t"
"v_mfma_f32_16x16x32_bf16 a[44:47], a[228:231], a[168:171], a[44:47]\n\t"
"v_cvt_pk_bf16_f32 v234, v224, v225\n\t"
"v_cvt_pk_bf16_f32 v235, v226, v227\n\t"
"v_mfma_f32_16x16x32_bf16 a[44:47], v[96:99], a[172:175], a[44:47]\n\t"
"ds_read_b64_tr_b16 v[224:225], v246 offset:52736\n\t"
"v_mfma_f32_16x16x32_bf16 a[44:47], a[232:235], a[176:179], a[44:47]\n\t"
"s_mov_b32 s20, 958464\n\t"
"buffer_load_dwordx4 a[208:211], v252, s[8:11], s20 offen offset:0 sc1\n\t"
"v_mfma_f32_16x16x32_bf16 a[52:55], a[228:231], a[172:175], a[52:55]\n\t"
"ds_read_b64_tr_b16 v[226:227], v246 offset:55808\n\t"
"v_mfma_f32_16x16x32_bf16 a[52:55], v[96:99], a[176:179], a[52:55]\n\t"
"v_cvt_pk_bf16_f32 v228, v228, v229\n\t"
"v_cvt_pk_bf16_f32 v229, v230, v231\n\t"
"v_mfma_f32_16x16x32_bf16 a[52:55], a[232:235], a[180:183], a[52:55]\n\t"
"ds_write2st64_b64 v245, v[232:233], v[234:235] offset0:5 offset1:6\n\t"
"v_mfma_f32_16x16x32_bf16 a[60:63], a[228:231], a[176:179], a[60:63]\n\t"
"v_mfma_f32_16x16x32_bf16 a[60:63], v[96:99], a[180:183], a[60:63]\n\t"
"v_mfma_f32_16x16x32_bf16 a[60:63], a[232:235], a[184:187], a[60:63]\n\t"
"v_mfma_f32_16x16x32_bf16 a[68:71], a[228:231], a[180:183], a[68:71]\n\t"
"v_mfma_f32_16x16x32_bf16 a[68:71], v[96:99], a[184:187], a[68:71]\n\t"
"v_mfma_f32_16x16x32_bf16 a[68:71], a[232:235], a[188:191], a[68:71]\n\t"
"v_mfma_f32_16x16x32_bf16 a[76:79], a[228:231], a[184:187], a[76:79]\n\t"
"v_mfma_f32_16x16x32_bf16 a[76:79], v[96:99], a[188:191], a[76:79]\n\t"
"v_mfma_f32_16x16x32_bf16 a[76:79], a[232:235], a[192:195], a[76:79]\n\t"
"s_waitcnt lgkmcnt(1)\n\t"
"v_mfma_f32_16x16x32_bf16 v[224:227], v[224:227], v[220:223], 0\n\t"
"v_mfma_f32_16x16x32_bf16 a[84:87], a[228:231], a[188:191], a[84:87]\n\t"
"v_mfma_f32_16x16x32_bf16 a[84:87], v[96:99], a[192:195], a[84:87]\n\t"
"v_mfma_f32_16x16x32_bf16 a[84:87], a[232:235], a[196:199], a[84:87]\n\t"
"v_mfma_f32_16x16x32_bf16 a[92:95], a[228:231], a[192:195], a[92:95]\n\t"
"v_mfma_f32_16x16x32_bf16 a[92:95], v[96:99], a[196:199], a[92:95]\n\t"
"s_nop 2\n\t"
"v_cvt_pk_bf16_f32 v230, v224, v225\n\t"
"v_cvt_pk_bf16_f32 v231, v226, v227\n\t"
"v_mfma_f32_16x16x32_bf16 a[92:95], a[232:235], a[200:203], a[92:95]\n\t"
"ds_write2st64_b64 v245, v[228:229], v[230:231] offset0:7 offset1:8\n\t"
"v_mfma_f32_16x16x32_bf16 a[100:103], a[228:231], a[196:199], a[100:103]\n\t"
"v_mfma_f32_16x16x32_bf16 a[100:103], v[96:99], a[200:203], a[100:103]\n\t"
"v_mfma_f32_16x16x32_bf16 a[100:103], a[232:235], a[204:207], a[100:103]\n\t"
"buffer_load_dwordx4 a[228:231], v252, s[8:11], s20 offen offset:1024 sc1\n\t"
"s_waitcnt vmcnt(11)\n\t"
"v_mfma_f32_16x16x32_bf16 a[108:111], a[236:239], a[168:171], a[108:111]\n\t"
"v_mfma_f32_16x16x32_bf16 a[108:111], a[240:243], a[172:175], a[108:111]\n\t"
"v_mfma_f32_16x16x32_bf16 a[108:111], a[212:215], a[176:179], a[108:111]\n\t"
"buffer_load_dwordx4 v[96:99], v252, s[8:11], s20 offen offset:2048 sc1\n\t"
"s_waitcnt lgkmcnt(0)\n\t"
"s_barrier\n\t"
"v_mfma_f32_16x16x32_bf16 a[116:119], a[236:239], a[172:175], a[116:119]\n\t"
"ds_read_b128 a[168:171], v244 offset:0\n\t"
"v_mfma_f32_16x16x32_bf16 a[116:119], a[240:243], a[176:179], a[116:119]\n\t"
"ds_read_b128 a[172:175], v244 offset:512\n\t"
"v_mfma_f32_16x16x32_bf16 a[116:119], a[212:215], a[180:183], a[116:119]\n\t"
"s_mov_b32 s20, 970752\n\t"
"buffer_load_dwordx4 a[232:235], v252, s[8:11], s20 offen offset:0 sc1\n\t"
"v_mfma_f32_16x16x32_bf16 a[124:127], a[236:239], a[176:179], a[124:127]\n\t"
"ds_read_b128 a[176:179], v244 offset:1024\n\t"
"v_mfma_f32_16x16x32_bf16 a[124:127], a[240:243], a[180:183], a[124:127]\n\t"
"v_mfma_f32_16x16x32_bf16 a[124:127], a[212:215], a[184:187], a[124:127]\n\t"
"v_mfma_f32_16x16x32_bf16 a[132:135], a[236:239], a[180:183], a[132:135]\n\t"
"ds_read_b128 a[180:183], v244 offset:1536\n\t"
"v_mfma_f32_16x16x32_bf16 a[132:135], a[240:243], a[184:187], a[132:135]\n\t"
"v_mfma_f32_16x16x32_bf16 a[132:135], a[212:215], a[188:191], a[132:135]\n\t"
"v_mfma_f32_16x16x32_bf16 a[140:143], a[236:239], a[184:187], a[140:143]\n\t"
"ds_read_b128 a[184:187], v244 offset:2048\n\t"
"v_mfma_f32_16x16x32_bf16 a[140:143], a[240:243], a[188:191], a[140:143]\n\t"
"v_mfma_f32_16x16x32_bf16 a[140:143], a[212:215], a[192:195], a[140:143]\n\t"
"v_mfma_f32_16x16x32_bf16 a[148:151], a[236:239], a[188:191], a[148:151]\n\t"
"ds_read_b128 a[188:191], v244 offset:2560\n\t"
"v_mfma_f32_16x16x32_bf16 a[148:151], a[240:243], a[192:195], a[148:151]\n\t"
"v_mfma_f32_16x16x32_bf16 a[148:151], a[212:215], a[196:199], a[148:151]\n\t"
"v_mfma_f32_16x16x32_bf16 a[156:159], a[236:239], a[192:195], a[156:159]\n\t"
"ds_read_b128 a[192:195], v244 offset:3072\n\t"
"v_mfma_f32_16x16x32_bf16 a[156:159], a[240:243], a[196:199], a[156:159]\n\t"
"v_mfma_f32_16x16x32_bf16 a[156:159], a[212:215], a[200:203], a[156:159]\n\t"
"v_mfma_f32_16x16x32_bf16 a[164:167], a[236:239], a[196:199], a[164:167]\n\t"
"ds_read_b128 a[196:199], v244 offset:3584\n\t"
"v_mfma_f32_16x16x32_bf16 a[164:167], a[240:243], a[200:203], a[164:167]\n\t"
"ds_read_b128 a[200:203], v244 offset:4096\n\t"
"v_mfma_f32_16x16x32_bf16 a[164:167], a[212:215], a[204:207], a[164:167]\n\t"
"buffer_load_dwordx4 a[236:239], v252, s[8:11], s20 offen offset:1024 sc1\n\t"
"Lst11_%=:\n\t"
"s_waitcnt vmcnt(11) lgkmcnt(6)\n\t"
"v_mfma_f32_16x16x32_bf16 v[0:3], a[244:247], a[168:171], v[0:3]\n\t"
"ds_read_b128 a[204:207], v244 offset:4608\n\t"
"v_mfma_f32_16x16x32_bf16 v[0:3], a[216:219], a[172:175], v[0:3]\n\t"
"v_mfma_f32_16x16x32_bf16 v[0:3], a[220:223], a[176:179], v[0:3]\n\t"
"buffer_load_dwordx4 a[212:215], v252, s[8:11], s20 offen offset:2048 sc1\n\t"
"s_waitcnt lgkmcnt(6)\n\t"
"v_mfma_f32_16x16x32_bf16 v[4:7], a[244:247], a[172:175], v[4:7]\n\t"
"v_mfma_f32_16x16x32_bf16 v[4:7], a[216:219], a[176:179], v[4:7]\n\t"
"v_mfma_f32_16x16x32_bf16 v[4:7], a[220:223], a[180:183], v[4:7]\n\t"
"s_mov_b32 s20, 983040\n\t"
"buffer_load_dwordx4 a[240:243], v252, s[8:11], s20 offen offset:0 sc1\n\t"
"s_waitcnt lgkmcnt(5)\n\t"
"v_mfma_f32_16x16x32_bf16 v[8:11], a[244:247], a[176:179], v[8:11]\n\t"
"v_mfma_f32_16x16x32_bf16 v[8:11], a[216:219], a[180:183], v[8:11]\n\t"
"v_mfma_f32_16x16x32_bf16 v[8:11], a[220:223], a[184:187], v[8:11]\n\t"
"s_waitcnt lgkmcnt(4)\n\t"
"v_mfma_f32_16x16x32_bf16 v[12:15], a[244:247], a[180:183], v[12:15]\n\t"
"v_mfma_f32_16x16x32_bf16 v[12:15], a[216:219], a[184:187], v[12:15]\n\t"
"v_mfma_f32_16x16x32_bf16 v[12:15], a[220:223], a[188:191], v[12:15]\n\t"
"s_waitcnt lgkmcnt(3)\n\t"
"v_mfma_f32_16x16x32_bf16 v[16:19], a[244:247], a[184:187], v[16:19]\n\t"
"v_mfma_f32_16x16x32_bf16 v[16:19], a[216:219], a[188:191], v[16:19]\n\t"
"v_mfma_f32_16x16x32_bf16 v[16:19], a[220:223], a[192:195], v[16:19]\n\t"
"ds_read_b64_tr_b16 v[232:233], v246 offset:1024\n\t"
"ds_read_b64_tr_b16 v[234:235], v246 offset:4096\n\t"
"s_waitcnt lgkmcnt(4)\n\t"
"v_mfma_f32_16x16x32_bf16 v[20:23], a[244:247], a[188:191], v[20:23]\n\t"
"v_mfma_f32_16x16x32_bf16 v[20:23], a[216:219], a[192:195], v[20:23]\n\t"
"v_mfma_f32_16x16x32_bf16 v[20:23], a[220:223], a[196:199], v[20:23]\n\t"
"s_waitcnt lgkmcnt(3)\n\t"
"v_mfma_f32_16x16x32_bf16 v[24:27], a[244:247], a[192:195], v[24:27]\n\t"
"v_mfma_f32_16x16x32_bf16 v[24:27], a[216:219], a[196:199], v[24:27]\n\t"
"v_mfma_f32_16x16x32_bf16 v[24:27], a[220:223], a[200:203], v[24:27]\n\t"
"s_waitcnt lgkmcnt(2)\n\t"
"v_mfma_f32_16x16x32_bf16 v[28:31], a[244:247], a[196:199], v[28:31]\n\t"
"ds_read_b64_tr_b16 v[224:225], v246 offset:7168\n\t"
"v_mfma_f32_16x16x32_bf16 v[28:31], a[216:219], a[200:203], v[28:31]\n\t"
"ds_read_b64_tr_b16 v[226:227], v246 offset:10240\n\t"
"v_mfma_f32_16x16x32_bf16 v[28:31], a[220:223], a[204:207], v[28:31]\n\t"
"buffer_load_dwordx4 a[244:247], v252, s[8:11], s20 offen offset:1024 sc1\n\t"
"s_waitcnt vmcnt(11)\n\t"
"v_mfma_f32_16x16x32_bf16 v[32:35], a[248:251], a[168:171], v[32:35]\n\t"
"v_mfma_f32_16x16x32_bf16 v[32:35], a[252:255], a[172:175], v[32:35]\n\t"
"v_mfma_f32_16x16x32_bf16 v[32:35], v[236:239], a[176:179], v[32:35]\n\t"
"buffer_load_dwordx4 a[216:219], v252, s[8:11], s20 offen offset:2048 sc1\n\t"
"v_mfma_f32_16x16x32_bf16 v[36:39], a[248:251], a[172:175], v[36:39]\n\t"
"v_mfma_f32_16x16x32_bf16 v[36:39], a[252:255], a[176:179], v[36:39]\n\t"
"v_mfma_f32_16x16x32_bf16 v[36:39], v[236:239], a[180:183], v[36:39]\n\t"
"s_mov_b32 s20, 1142784\n\t"
"buffer_load_dwordx4 a[220:223], v252, s[8:11], s20 offen offset:0 sc1\n\t"
"s_waitcnt lgkmcnt(2)\n\t"
"v_mfma_f32_16x16x32_bf16 v[232:235], v[232:235], v[220:223], 0\n\t"
"v_mfma_f32_16x16x32_bf16 v[40:43], a[248:251], a[176:179], v[40:43]\n\t"
"ds_read_b64_tr_b16 v[228:229], v246 offset:13312\n\t"
"v_mfma_f32_16x16x32_bf16 v[40:43], a[252:255], a[180:183], v[40:43]\n\t"
"ds_read_b64_tr_b16 v[230:231], v246 offset:16384\n\t"
"v_mfma_f32_16x16x32_bf16 v[40:43], v[236:239], a[184:187], v[40:43]\n\t"
"v_mfma_f32_16x16x32_bf16 v[44:47], a[248:251], a[180:183], v[44:47]\n\t"
"v_mfma_f32_16x16x32_bf16 v[44:47], a[252:255], a[184:187], v[44:47]\n\t"
"s_nop 0\n\t"
"v_cvt_pk_bf16_f32 v232, v232, v233\n\t"
"v_mfma_f32_16x16x32_bf16 v[44:47], v[236:239], a[188:191], v[44:47]\n\t"
"v_cvt_pk_bf16_f32 v233, v234, v235\n\t"
"s_waitcnt lgkmcnt(2)\n\t"
"v_mfma_f32_16x16x32_bf16 v[224:227], v[224:227], v[220:223], 0\n\t"
"v_mfma_f32_16x16x32_bf16 v[48:51], a[248:251], a[184:187], v[48:51]\n\t"
"v_mfma_f32_16x16x32_bf16 v[48:51], a[252:255], a[188:191], v[48:51]\n\t"
"v_mfma_f32_16x16x32_bf16 v[48:51], v[236:239], a[192:195], v[48:51]\n\t"
"v_mfma_f32_16x16x32_bf16 v[52:55], a[248:251], a[188:191], v[52:55]\n\t"
"v_mfma_f32_16x16x32_bf16 v[52:55], a[252:255], a[192:195], v[52:55]\n\t"
"s_nop 2\n\t"
"v_cvt_pk_bf16_f32 v234, v224, v225\n\t"
"v_cvt_pk_bf16_f32 v235, v226, v227\n\t"
"v_mfma_f32_16x16x32_bf16 v[52:55], v[236:239], a[196:199], v[52:55]\n\t"
"ds_read_b64_tr_b16 v[224:225], v246 offset:20992\n\t"
"v_mfma_f32_16x16x32_bf16 v[56:59], a[248:251], a[192:195], v[56:59]\n\t"
"ds_read_b64_tr_b16 v[226:227], v246 offset:24064\n\t"
"v_mfma_f32_16x16x32_bf16 v[56:59], a[252:255], a[196:199], v[56:59]\n\t"
"v_mfma_f32_16x16x32_bf16 v[56:59], v[236:239], a[200:203], v[56:59]\n\t"
"s_waitcnt lgkmcnt(2)\n\t"
"v_mfma_f32_16x16x32_bf16 v[228:231], v[228:231], v[220:223], 0\n\t"
"v_mfma_f32_16x16x32_bf16 v[60:63], a[248:251], a[196:199], v[60:63]\n\t"
"v_mfma_f32_16x16x32_bf16 v[60:63], a[252:255], a[200:203], v[60:63]\n\t"
"v_mfma_f32_16x16x32_bf16 v[60:63], v[236:239], a[204:207], v[60:63]\n\t"
"buffer_load_dwordx4 a[248:251], v252, s[8:11], s20 offen offset:1024 sc1\n\t"
"s_waitcnt vmcnt(11)\n\t"
"v_mfma_f32_16x16x32_bf16 v[64:67], a[224:227], a[168:171], v[64:67]\n\t"
"ds_read_b128 a[168:171], v244 offset:9216\n\t"
"v_mfma_f32_16x16x32_bf16 v[64:67], v[240:243], a[172:175], v[64:67]\n\t"
"v_cvt_pk_bf16_f32 v228, v228, v229\n\t"
"v_cvt_pk_bf16_f32 v229, v230, v231\n\t"
"v_mfma_f32_16x16x32_bf16 v[64:67], v[248:251], a[176:179], v[64:67]\n\t"
"v_mfma_f32_16x16x32_bf16 v[68:71], a[224:227], a[172:175], v[68:71]\n\t"
"ds_read_b128 a[172:175], v244 offset:9728\n\t"
"v_mfma_f32_16x16x32_bf16 v[68:71], v[240:243], a[176:179], v[68:71]\n\t"
"v_mfma_f32_16x16x32_bf16 v[68:71], v[248:251], a[180:183], v[68:71]\n\t"
"buffer_load_dwordx4 v[236:239], v252, s[8:11], s20 offen offset:2048 sc1\n\t"
"s_waitcnt lgkmcnt(2)\n\t"
"v_mfma_f32_16x16x32_bf16 v[224:227], v[224:227], v[220:223], 0\n\t"
"s_mov_b32 s20, 1155072\n\t"
"buffer_load_dwordx4 a[252:255], v252, s[8:11], s20 offen offset:0 sc1\n\t"
"v_mfma_f32_16x16x32_bf16 v[72:75], a[224:227], a[176:179], v[72:75]\n\t"
"ds_read_b128 a[176:179], v244 offset:10240\n\t"
"v_mfma_f32_16x16x32_bf16 v[72:75], v[240:243], a[180:183], v[72:75]\n\t"
"v_mfma_f32_16x16x32_bf16 v[72:75], v[248:251], a[184:187], v[72:75]\n\t"
"v_mfma_f32_16x16x32_bf16 v[76:79], a[224:227], a[180:183], v[76:79]\n\t"
"ds_read_b128 a[180:183], v244 offset:10752\n\t"
"v_mfma_f32_16x16x32_bf16 v[76:79], v[240:243], a[184:187], v[76:79]\n\t"
"v_cvt_pk_bf16_f32 v230, v224, v225\n\t"
"v_cvt_pk_bf16_f32 v231, v226, v227\n\t"
"v_mfma_f32_16x16x32_bf16 v[76:79], v[248:251], a[188:191], v[76:79]\n\t"
"ds_read_b64_tr_b16 v[224:225], v246 offset:27136\n\t"
"v_mfma_f32_16x16x32_bf16 v[80:83], a[224:227], a[184:187], v[80:83]\n\t"
"ds_read_b64_tr_b16 v[226:227], v246 offset:30208\n\t"
"v_mfma_f32_16x16x32_bf16 v[80:83], v[240:243], a[188:191], v[80:83]\n\t"
"ds_read_b128 a[184:187], v244 offset:11264\n\t"
"v_mfma_f32_16x16x32_bf16 v[80:83], v[248:251], a[192:195], v[80:83]\n\t"
"v_mfma_f32_16x16x32_bf16 v[84:87], a[224:227], a[188:191], v[84:87]\n\t"
"ds_read_b128 a[188:191], v244 offset:11776\n\t"
"v_mfma_f32_16x16x32_bf16 v[84:87], v[240:243], a[192:195], v[84:87]\n\t"
"v_mfma_f32_16x16x32_bf16 v[84:87], v[248:251], a[196:199], v[84:87]\n\t"
"v_mfma_f32_16x16x32_bf16 v[88:91], a[224:227], a[192:195], v[88:91]\n\t"
"ds_read_b128 a[192:195], v244 offset:12288\n\t"
"v_mfma_f32_16x16x32_bf16 v[88:91], v[240:243], a[196:199], v[88:91]\n\t"
"v_mfma_f32_16x16x32_bf16 v[88:91], v[248:251], a[200:203], v[88:91]\n\t"
"v_mfma_f32_16x16x32_bf16 v[92:95], a[224:227], a[196:199], v[92:95]\n\t"
"ds_read_b128 a[196:199], v244 offset:12800\n\t"
"v_mfma_f32_16x16x32_bf16 v[92:95], v[240:243], a[200:203], v[92:95]\n\t"
"ds_read_b128 a[200:203], v244 offset:13312\n\t"
"v_mfma_f32_16x16x32_bf16 v[92:95], v[248:251], a[204:207], v[92:95]\n\t"
"buffer_load_dwordx4 a[224:227], v252, s[8:11], s20 offen offset:1024 sc1\n\t"
"s_waitcnt lgkmcnt(5)\n\t"
"v_mfma_f32_16x16x32_bf16 v[224:227], v[224:227], v[220:223], 0\n\t"
"ds_read_b128 a[204:207], v244 offset:13824\n\t"
"s_waitcnt vmcnt(11)\n\t"
"v_mfma_f32_16x16x32_bf16 v[100:103], a[208:211], a[168:171], v[100:103]\n\t"
"v_mfma_f32_16x16x32_bf16 v[100:103], a[228:231], a[172:175], v[100:103]\n\t"
"v_mfma_f32_16x16x32_bf16 v[100:103], v[96:99], a[176:179], v[100:103]\n\t"
"buffer_load_dwordx4 v[240:243], v252, s[8:11], s20 offen offset:2048 sc1\n\t"
"v_mfma_f32_16x16x32_bf16 v[104:107], a[208:211], a[172:175], v[104:107]\n\t"
"v_mfma_f32_16x16x32_bf16 v[104:107], a[228:231], a[176:179], v[104:107]\n\t"
"v_cvt_pk_bf16_f32 v224, v224, v225\n\t"
"v_cvt_pk_bf16_f32 v225, v226, v227\n\t"
"v_mfma_f32_16x16x32_bf16 v[104:107], v[96:99], a[180:183], v[104:107]\n\t"
"s_mov_b32 s20, 1167360\n\t"
"buffer_load_dwordx4 v[248:251], v252, s[8:11], s20 offen offset:0 sc1\n\t"
"s_waitcnt lgkmcnt(0)\n\t"
"s_barrier\n\t"
"v_mfma_f32_16x16x32_bf16 v[108:111], a[208:211], a[176:179], v[108:111]\n\t"
"v_mfma_f32_16x16x32_bf16 v[108:111], a[228:231], a[180:183], v[108:111]\n\t"
"v_mfma_f32_16x16x32_bf16 v[108:111], v[96:99], a[184:187], v[108:111]\n\t"
"ds_write2st64_b64 v245, v[232:233], v[234:235] offset0:0 offset1:1\n\t"
"v_mfma_f32_16x16x32_bf16 v[112:115], a[208:211], a[180:183], v[112:115]\n\t"
"ds_read_b64_tr_b16 v[232:233], v246 offset:33280\n\t"
"v_mfma_f32_16x16x32_bf16 v[112:115], a[228:231], a[184:187], v[112:115]\n\t"
"ds_read_b64_tr_b16 v[234:235], v246 offset:36352\n\t"
"v_mfma_f32_16x16x32_bf16 v[112:115], v[96:99], a[188:191], v[112:115]\n\t"
"ds_write2st64_b64 v245, v[228:229], v[230:231] offset0:2 offset1:3\n\t"
"v_mfma_f32_16x16x32_bf16 v[116:119], a[208:211], a[184:187], v[116:119]\n\t"
"ds_read_b64_tr_b16 v[228:229], v246 offset:40960\n\t"
"v_mfma_f32_16x16x32_bf16 v[116:119], a[228:231], a[188:191], v[116:119]\n\t"
"ds_read_b64_tr_b16 v[230:231], v246 offset:44032\n\t"
"v_mfma_f32_16x16x32_bf16 v[116:119], v[96:99], a[192:195], v[116:119]\n\t"
"ds_write_b64 v245, v[224:225] offset:2048\n\t"
"v_mfma_f32_16x16x32_bf16 v[120:123], a[208:211], a[188:191], v[120:123]\n\t"
"ds_read_b64_tr_b16 v[224:225], v246 offset:47104\n\t"
"v_mfma_f32_16x16x32_bf16 v[120:123], a[228:231], a[192:195], v[120:123]\n\t"
"ds_read_b64_tr_b16 v[226:227], v246 offset:50176\n\t"
"v_mfma_f32_16x16x32_bf16 v[120:123], v[96:99], a[196:199], v[120:123]\n\t"
"s_waitcnt lgkmcnt(6)\n\t"
"v_mfma_f32_16x16x32_bf16 v[232:235], v[232:235], v[220:223], 0\n\t"
"v_mfma_f32_16x16x32_bf16 v[124:127], a[208:211], a[192:195], v[124:127]\n\t"
"v_mfma_f32_16x16x32_bf16 v[124:127], a[228:231], a[196:199], v[124:127]\n\t"
"v_mfma_f32_16x16x32_bf16 v[124:127], v[96:99], a[200:203], v[124:127]\n\t"
"s_waitcnt lgkmcnt(3)\n\t"
"v_mfma_f32_16x16x32_bf16 v[228:231], v[228:231], v[220:223], 0\n\t"
"v_mfma_f32_16x16x32_bf16 v[128:131], a[208:211], a[196:199], v[128:131]\n\t"
"s_nop 1\n\t"
"v_cvt_pk_bf16_f32 v232, v232, v233\n\t"
"v_cvt_pk_bf16_f32 v233, v234, v235\n\t"
"v_mfma_f32_16x16x32_bf16 v[128:131], a[228:231], a[200:203], v[128:131]\n\t"
"v_mfma_f32_16x16x32_bf16 v[128:131], v[96:99], a[204:207], v[128:131]\n\t"
"buffer_load_dwordx4 a[208:211], v252, s[8:11], s20 offen offset:1024 sc1\n\t"
"s_waitcnt lgkmcnt(0)\n\t"
"v_mfma_f32_16x16x32_bf16 v[224:227], v[224:227], v[220:223], 0\n\t"
"buffer_load_dwordx4 v[96:99], v252, s[8:11], s20 offen offset:2048 sc1\n\t"
"s_waitcnt vmcnt(12)\n\t"
"v_mfma_f32_16x16x32_bf16 v[132:135], a[232:235], a[168:171], v[132:135]\n\t"
"v_cvt_pk_bf16_f32 v234, v228, v229\n\t"
"v_cvt_pk_bf16_f32 v235, v230, v231\n\t"
"v_mfma_f32_16x16x32_bf16 v[132:135], a[236:239], a[172:175], v[132:135]\n\t"
"ds_read_b64_tr_b16 v[228:229], v246 offset:53248\n\t"
"v_mfma_f32_16x16x32_bf16 v[132:135], a[212:215], a[176:179], v[132:135]\n\t"
"s_mov_b32 s20, 1179648\n\t"
"buffer_load_dwordx4 a[228:231], v252, s[8:11], s20 offen offset:0 sc1\n\t"
"v_mfma_f32_16x16x32_bf16 v[136:139], a[232:235], a[172:175], v[136:139]\n\t"
"ds_read_b64_tr_b16 v[230:231], v246 offset:56320\n\t"
"v_mfma_f32_16x16x32_bf16 v[136:139], a[236:239], a[176:179], v[136:139]\n\t"
"v_cvt_pk_bf16_f32 v224, v224, v225\n\t"
"v_cvt_pk_bf16_f32 v225, v226, v227\n\t"
"v_mfma_f32_16x16x32_bf16 v[136:139], a[212:215], a[180:183], v[136:139]\n\t"
"ds_write2st64_b64 v245, v[232:233], v[234:235] offset0:5 offset1:6\n\t"
"v_mfma_f32_16x16x32_bf16 v[140:143], a[232:235], a[176:179], v[140:143]\n\t"
"v_mfma_f32_16x16x32_bf16 v[140:143], a[236:239], a[180:183], v[140:143]\n\t"
"v_mfma_f32_16x16x32_bf16 v[140:143], a[212:215], a[184:187], v[140:143]\n\t"
"v_mfma_f32_16x16x32_bf16 v[144:147], a[232:235], a[180:183], v[144:147]\n\t"
"v_mfma_f32_16x16x32_bf16 v[144:147], a[236:239], a[184:187], v[144:147]\n\t"
"v_mfma_f32_16x16x32_bf16 v[144:147], a[212:215], a[188:191], v[144:147]\n\t"
"v_mfma_f32_16x16x32_bf16 v[148:151], a[232:235], a[184:187], v[148:151]\n\t"
"v_mfma_f32_16x16x32_bf16 v[148:151], a[236:239], a[188:191], v[148:151]\n\t"
"v_mfma_f32_16x16x32_bf16 v[148:151], a[212:215], a[192:195], v[148:151]\n\t"
"s_waitcnt lgkmcnt(1)\n\t"
"v_mfma_f32_16x16x32_bf16 v[228:231], v[228:231], v[220:223], 0\n\t"
"v_mfma_f32_16x16x32_bf16 v[152:155], a[232:235], a[188:191], v[152:155]\n\t"
"v_mfma_f32_16x16x32_bf16 v[152:155], a[236:239], a[192:195], v[152:155]\n\t"
"v_mfma_f32_16x16x32_bf16 v[152:155], a[212:215], a[196:199], v[152:155]\n\t"
"v_mfma_f32_16x16x32_bf16 v[156:159], a[232:235], a[192:195], v[156:159]\n\t"
"v_mfma_f32_16x16x32_bf16 v[156:159], a[236:239], a[196:199], v[156:159]\n\t"
"s_nop 2\n\t"
"v_cvt_pk_bf16_f32 v226, v228, v229\n\t"
"v_cvt_pk_bf16_f32 v227, v230, v231\n\t"
"v_mfma_f32_16x16x32_bf16 v[156:159], a[212:215], a[200:203], v[156:159]\n\t"
"ds_write2st64_b64 v245, v[224:225], v[226:227] offset0:7 offset1:8\n\t"
"v_mfma_f32_16x16x32_bf16 v[160:163], a[232:235], a[196:199], v[160:163]\n\t"
"v_mfma_f32_16x16x32_bf16 v[160:163], a[236:239], a[200:203], v[160:163]\n\t"
"v_mfma_f32_16x16x32_bf16 v[160:163], a[212:215], a[204:207], v[160:163]\n\t"
"buffer_load_dwordx4 a[232:235], v252, s[8:11], s20 offen offset:1024 sc1\n\t"
"s_waitcnt vmcnt(11)\n\t"
"v_mfma_f32_16x16x32_bf16 v[164:167], a[240:243], a[168:171], v[164:167]\n\t"
"v_mfma_f32_16x16x32_bf16 v[164:167], a[244:247], a[172:175], v[164:167]\n\t"
"v_mfma_f32_16x16x32_bf16 v[164:167], a[216:219], a[176:179], v[164:167]\n\t"
"buffer_load_dwordx4 a[212:215], v252, s[8:11], s20 offen offset:2048 sc1\n\t"
"s_waitcnt lgkmcnt(0)\n\t"
"s_barrier\n\t"
"v_mfma_f32_16x16x32_bf16 v[168:171], a[240:243], a[172:175], v[168:171]\n\t"
"ds_read_b128 a[168:171], v244 offset:0\n\t"
"v_mfma_f32_16x16x32_bf16 v[168:171], a[244:247], a[176:179], v[168:171]\n\t"
"ds_read_b128 a[172:175], v244 offset:512\n\t"
"v_mfma_f32_16x16x32_bf16 v[168:171], a[216:219], a[180:183], v[168:171]\n\t"
"s_mov_b32 s20, 1191936\n\t"
"buffer_load_dwordx4 a[236:239], v252, s[8:11], s20 offen offset:0 sc1\n\t"
"v_mfma_f32_16x16x32_bf16 v[172:175], a[240:243], a[176:179], v[172:175]\n\t"
"ds_read_b128 a[176:179], v244 offset:1024\n\t"
"v_mfma_f32_16x16x32_bf16 v[172:175], a[244:247], a[180:183], v[172:175]\n\t"
"v_mfma_f32_16x16x32_bf16 v[172:175], a[216:219], a[184:187], v[172:175]\n\t"
"v_mfma_f32_16x16x32_bf16 v[176:179], a[240:243], a[180:183], v[176:179]\n\t"
"ds_read_b128 a[180:183], v244 offset:1536\n\t"
"v_mfma_f32_16x16x32_bf16 v[176:179], a[244:247], a[184:187], v[176:179]\n\t"
"v_mfma_f32_16x16x32_bf16 v[176:179], a[216:219], a[188:191], v[176:179]\n\t"
"v_mfma_f32_16x16x32_bf16 v[180:183], a[240:243], a[184:187], v[180:183]\n\t"
"ds_read_b128 a[184:187], v244 offset:2048\n\t"
"v_mfma_f32_16x16x32_bf16 v[180:183], a[244:247], a[188:191], v[180:183]\n\t"
"v_mfma_f32_16x16x32_bf16 v[180:183], a[216:219], a[192:195], v[180:183]\n\t"
"v_mfma_f32_16x16x32_bf16 v[184:187], a[240:243], a[188:191], v[184:187]\n\t"
"ds_read_b128 a[188:191], v244 offset:2560\n\t"
"v_mfma_f32_16x16x32_bf16 v[184:187], a[244:247], a[192:195], v[184:187]\n\t"
"v_mfma_f32_16x16x32_bf16 v[184:187], a[216:219], a[196:199], v[184:187]\n\t"
"v_mfma_f32_16x16x32_bf16 v[188:191], a[240:243], a[192:195], v[188:191]\n\t"
"ds_read_b128 a[192:195], v244 offset:3072\n\t"
"v_mfma_f32_16x16x32_bf16 v[188:191], a[244:247], a[196:199], v[188:191]\n\t"
"v_mfma_f32_16x16x32_bf16 v[188:191], a[216:219], a[200:203], v[188:191]\n\t"
"v_mfma_f32_16x16x32_bf16 v[192:195], a[240:243], a[196:199], v[192:195]\n\t"
"ds_read_b128 a[196:199], v244 offset:3584\n\t"
"v_mfma_f32_16x16x32_bf16 v[192:195], a[244:247], a[200:203], v[192:195]\n\t"
"ds_read_b128 a[200:203], v244 offset:4096\n\t"
"v_mfma_f32_16x16x32_bf16 v[192:195], a[216:219], a[204:207], v[192:195]\n\t"
"buffer_load_dwordx4 a[240:243], v252, s[8:11], s20 offen offset:1024 sc1\n\t"
"Lst12_%=:\n\t"
"s_waitcnt vmcnt(11) lgkmcnt(6)\n\t"
"v_mfma_f32_16x16x32_bf16 v[0:3], a[220:223], a[168:171], v[0:3]\n\t"
"ds_read_b128 a[204:207], v244 offset:4608\n\t"
"v_xor_b32 v246, 0xc0, v246\n\t"
"v_mfma_f32_16x16x32_bf16 v[0:3], a[248:251], a[172:175], v[0:3]\n\t"
"v_mfma_f32_16x16x32_bf16 v[0:3], v[236:239], a[176:179], v[0:3]\n\t"
"s_waitcnt lgkmcnt(6)\n\t"
"v_mfma_f32_16x16x32_bf16 v[4:7], a[220:223], a[172:175], v[4:7]\n\t"
"v_mfma_f32_16x16x32_bf16 v[4:7], a[248:251], a[176:179], v[4:7]\n\t"
"v_mfma_f32_16x16x32_bf16 v[4:7], v[236:239], a[180:183], v[4:7]\n\t"
"buffer_load_dwordx4 a[216:219], v252, s[8:11], s20 offen offset:2048 sc1\n\t"
"s_waitcnt lgkmcnt(5)\n\t"
"v_mfma_f32_16x16x32_bf16 v[8:11], a[220:223], a[176:179], v[8:11]\n\t"
"v_mfma_f32_16x16x32_bf16 v[8:11], a[248:251], a[180:183], v[8:11]\n\t"
"v_mfma_f32_16x16x32_bf16 v[8:11], v[236:239], a[184:187], v[8:11]\n\t"
"s_mov_b32 s20, 1204224\n\t"
"buffer_load_dwordx4 a[244:247], v252, s[8:11], s20 offen offset:0 sc1\n\t"
"s_waitcnt lgkmcnt(4)\n\t"
"v_mfma_f32_16x16x32_bf16 v[12:15], a[220:223], a[180:183], v[12:15]\n\t"
"v_mfma_f32_16x16x32_bf16 v[12:15], a[248:251], a[184:187], v[12:15]\n\t"
"v_mfma_f32_16x16x32_bf16 v[12:15], v[236:239], a[188:191], v[12:15]\n\t"
"s_waitcnt lgkmcnt(3)\n\t"
"v_mfma_f32_16x16x32_bf16 v[16:19], a[220:223], a[184:187], v[16:19]\n\t"
"v_mfma_f32_16x16x32_bf16 v[16:19], a[248:251], a[188:191], v[16:19]\n\t"
"v_mfma_f32_16x16x32_bf16 v[16:19], v[236:239], a[192:195], v[16:19]\n\t"
"ds_read_b64_tr_b16 v[232:233], v246 offset:1024\n\t"
"ds_read_b64_tr_b16 v[234:235], v246 offset:4096\n\t"
"s_waitcnt lgkmcnt(4)\n\t"
"v_mfma_f32_16x16x32_bf16 v[20:23], a[220:223], a[188:191], v[20:23]\n\t"
"v_mfma_f32_16x16x32_bf16 v[20:23], a[248:251], a[192:195], v[20:23]\n\t"
"v_mfma_f32_16x16x32_bf16 v[20:23], v[236:239], a[196:199], v[20:23]\n\t"
"s_waitcnt lgkmcnt(3)\n\t"
"v_mfma_f32_16x16x32_bf16 v[24:27], a[220:223], a[192:195], v[24:27]\n\t"
"v_mfma_f32_16x16x32_bf16 v[24:27], a[248:251], a[196:199], v[24:27]\n\t"
"v_mfma_f32_16x16x32_bf16 v[24:27], v[236:239], a[200:203], v[24:27]\n\t"
"s_waitcnt lgkmcnt(2)\n\t"
"v_mfma_f32_16x16x32_bf16 v[28:31], a[220:223], a[196:199], v[28:31]\n\t"
"ds_read_b64_tr_b16 v[228:229], v246 offset:7168\n\t"
"v_mfma_f32_16x16x32_bf16 v[28:31], a[248:251], a[200:203], v[28:31]\n\t"
"ds_read_b64_tr_b16 v[230:231], v246 offset:10240\n\t"
"v_mfma_f32_16x16x32_bf16 v[28:31], v[236:239], a[204:207], v[28:31]\n\t"
"buffer_load_dwordx4 a[220:223], v252, s[8:11], s20 offen offset:1024 sc1\n\t"
"s_waitcnt vmcnt(11)\n\t"
"v_mfma_f32_16x16x32_bf16 v[32:35], a[252:255], a[168:171], v[32:35]\n\t"
"v_mfma_f32_16x16x32_bf16 v[32:35], a[224:227], a[172:175], v[32:35]\n\t"
"v_mfma_f32_16x16x32_bf16 v[32:35], v[240:243], a[176:179], v[32:35]\n\t"
"buffer_load_dwordx4 v[236:239], v252, s[8:11], s20 offen offset:2048 sc1\n\t"
"v_mfma_f32_16x16x32_bf16 v[36:39], a[252:255], a[172:175], v[36:39]\n\t"
"v_mfma_f32_16x16x32_bf16 v[36:39], a[224:227], a[176:179], v[36:39]\n\t"
"v_mfma_f32_16x16x32_bf16 v[36:39], v[240:243], a[180:183], v[36:39]\n\t"
"s_mov_b32 s20, 1216512\n\t"
"buffer_load_dwordx4 a[248:251], v252, s[8:11], s20 offen offset:0 sc1\n\t"
"s_waitcnt lgkmcnt(2)\n\t"
"v_mfma_f32_16x16x32_bf16 v[232:235], v[232:235], v[220:223], 0\n\t"
"v_mfma_f32_16x16x32_bf16 v[40:43], a[252:255], a[176:179], v[40:43]\n\t"
"ds_read_b64_tr_b16 v[224:225], v246 offset:13312\n\t"
"v_mfma_f32_16x16x32_bf16 v[40:43], a[224:227], a[180:183], v[40:43]\n\t"
"ds_read_b64_tr_b16 v[226:227], v246 offset:16384\n\t"
"v_mfma_f32_16x16x32_bf16 v[40:43], v[240:243], a[184:187], v[40:43]\n\t"
"v_mfma_f32_16x16x32_bf16 v[44:47], a[252:255], a[180:183], v[44:47]\n\t"
"v_mfma_f32_16x16x32_bf16 v[44:47], a[224:227], a[184:187], v[44:47]\n\t"
"s_nop 0\n\t"
"v_cvt_pk_bf16_f32 v232, v232, v233\n\t"
"v_mfma_f32_16x16x32_bf16 v[44:47], v[240:243], a[188:191], v[44:47]\n\t"
"v_cvt_pk_bf16_f32 v233, v234, v235\n\t"
"s_waitcnt lgkmcnt(2)\n\t"
"v_mfma_f32_16x16x32_bf16 v[228:231], v[228:231], v[220:223], 0\n\t"
"v_mfma_f32_16x16x32_bf16 v[48:51], a[252:255], a[184:187], v[48:51]\n\t"
"v_mfma_f32_16x16x32_bf16 v[48:51], a[224:227], a[188:191], v[48:51]\n\t"
"v_mfma_f32_16x16x32_bf16 v[48:51], v[240:243], a[192:195], v[48:51]\n\t"
"v_mfma_f32_16x16x32_bf16 v[52:55], a[252:255], a[188:191], v[52:55]\n\t"
"v_mfma_f32_16x16x32_bf16 v[52:55], a[224:227], a[192:195], v[52:55]\n\t"
"s_nop 2\n\t"
"v_cvt_pk_bf16_f32 v234, v228, v229\n\t"
"v_cvt_pk_bf16_f32 v235, v230, v231\n\t"
"v_mfma_f32_16x16x32_bf16 v[52:55], v[240:243], a[196:199], v[52:55]\n\t"
"ds_read_b64_tr_b16 v[228:229], v246 offset:20992\n\t"
"v_mfma_f32_16x16x32_bf16 v[56:59], a[252:255], a[192:195], v[56:59]\n\t"
"ds_read_b64_tr_b16 v[230:231], v246 offset:24064\n\t"
"v_mfma_f32_16x16x32_bf16 v[56:59], a[224:227], a[196:199], v[56:59]\n\t"
"v_mfma_f32_16x16x32_bf16 v[56:59], v[240:243], a[200:203], v[56:59]\n\t"
"s_waitcnt lgkmcnt(2)\n\t"
"v_mfma_f32_16x16x32_bf16 v[224:227], v[224:227], v[220:223], 0\n\t"
"v_mfma_f32_16x16x32_bf16 v[60:63], a[252:255], a[196:199], v[60:63]\n\t"
"v_mfma_f32_16x16x32_bf16 v[60:63], a[224:227], a[200:203], v[60:63]\n\t"
"v_mfma_f32_16x16x32_bf16 v[60:63], v[240:243], a[204:207], v[60:63]\n\t"
"buffer_load_dwordx4 a[252:255], v252, s[8:11], s20 offen offset:1024 sc1\n\t"
"s_waitcnt vmcnt(11)\n\t"
"v_mfma_f32_16x16x32_bf16 v[64:67], v[248:251], a[168:171], v[64:67]\n\t"
"ds_read_b128 a[168:171], v244 offset:9216\n\t"
"v_mfma_f32_16x16x32_bf16 v[64:67], a[208:211], a[172:175], v[64:67]\n\t"
"v_cvt_pk_bf16_f32 v224, v224, v225\n\t"
"v_cvt_pk_bf16_f32 v225, v226, v227\n\t"
"v_mfma_f32_16x16x32_bf16 v[64:67], v[96:99], a[176:179], v[64:67]\n\t"
"v_mfma_f32_16x16x32_bf16 v[68:71], v[248:251], a[172:175], v[68:71]\n\t"
"ds_read_b128 a[172:175], v244 offset:9728\n\t"
"v_mfma_f32_16x16x32_bf16 v[68:71], a[208:211], a[176:179], v[68:71]\n\t"
"v_mfma_f32_16x16x32_bf16 v[68:71], v[96:99], a[180:183], v[68:71]\n\t"
"buffer_load_dwordx4 v[240:243], v252, s[8:11], s20 offen offset:2048 sc1\n\t"
"s_waitcnt lgkmcnt(2)\n\t"
"v_mfma_f32_16x16x32_bf16 v[228:231], v[228:231], v[220:223], 0\n\t"
"s_mov_b32 s20, 1228800\n\t"
"buffer_load_dwordx4 a[224:227], v252, s[8:11], s20 offen offset:0 sc1\n\t"
"v_mfma_f32_16x16x32_bf16 v[72:75], v[248:251], a[176:179], v[72:75]\n\t"
"ds_read_b128 a[176:179], v244 offset:10240\n\t"
"v_mfma_f32_16x16x32_bf16 v[72:75], a[208:211], a[180:183], v[72:75]\n\t"
"v_mfma_f32_16x16x32_bf16 v[72:75], v[96:99], a[184:187], v[72:75]\n\t"
"v_mfma_f32_16x16x32_bf16 v[76:79], v[248:251], a[180:183], v[76:79]\n\t"
"ds_read_b128 a[180:183], v244 offset:10752\n\t"
"v_mfma_f32_16x16x32_bf16 v[76:79], a[208:211], a[184:187], v[76:79]\n\t"
"v_cvt_pk_bf16_f32 v226, v228, v229\n\t"
"v_cvt_pk_bf16_f32 v227, v230, v231\n\t"
"v_mfma_f32_16x16x32_bf16 v[76:79], v[96:99], a[188:191], v[76:79]\n\t"
"ds_read_b64_tr_b16 v[228:229], v246 offset:27136\n\t"
"v_mfma_f32_16x16x32_bf16 v[80:83], v[248:251], a[184:187], v[80:83]\n\t"
"ds_read_b64_tr_b16 v[230:231], v246 offset:30208\n\t"
"v_mfma_f32_16x16x32_bf16 v[80:83], a[208:211], a[188:191], v[80:83]\n\t"
"ds_read_b128 a[184:187], v244 offset:11264\n\t"
"v_mfma_f32_16x16x32_bf16 v[80:83], v[96:99], a[192:195], v[80:83]\n\t"
"v_mfma_f32_16x16x32_bf16 v[84:87], v[248:251], a[188:191], v[84:87]\n\t"
"ds_read_b128 a[188:191], v244 offset:11776\n\t"
"v_mfma_f32_16x16x32_bf16 v[84:87], a[208:211], a[192:195], v[84:87]\n\t"
"v_mfma_f32_16x16x32_bf16 v[84:87], v[96:99], a[196:199], v[84:87]\n\t"
"v_mfma_f32_16x16x32_bf16 v[88:91], v[248:251], a[192:195], v[88:91]\n\t"
"ds_read_b128 a[192:195], v244 offset:12288\n\t"
"v_mfma_f32_16x16x32_bf16 v[88:91], a[208:211], a[196:199], v[88:91]\n\t"
"v_mfma_f32_16x16x32_bf16 v[88:91], v[96:99], a[200:203], v[88:91]\n\t"
"v_mfma_f32_16x16x32_bf16 v[92:95], v[248:251], a[196:199], v[92:95]\n\t"
"ds_read_b128 a[196:199], v244 offset:12800\n\t"
"v_mfma_f32_16x16x32_bf16 v[92:95], a[208:211], a[200:203], v[92:95]\n\t"
"ds_read_b128 a[200:203], v244 offset:13312\n\t"
"v_mfma_f32_16x16x32_bf16 v[92:95], v[96:99], a[204:207], v[92:95]\n\t"
"buffer_load_dwordx4 v[248:251], v252, s[8:11], s20 offen offset:1024 sc1\n\t"
"s_waitcnt lgkmcnt(5)\n\t"
"v_mfma_f32_16x16x32_bf16 v[228:231], v[228:231], v[220:223], 0\n\t"
"ds_read_b128 a[204:207], v244 offset:13824\n\t"
"s_waitcnt vmcnt(11)\n\t"
"v_mfma_f32_16x16x32_bf16 v[100:103], a[228:231], a[168:171], v[100:103]\n\t"
"v_mfma_f32_16x16x32_bf16 v[100:103], a[232:235], a[172:175], v[100:103]\n\t"
"v_mfma_f32_16x16x32_bf16 v[100:103], a[212:215], a[176:179], v[100:103]\n\t"
"buffer_load_dwordx4 v[96:99], v252, s[8:11], s20 offen offset:2048 sc1\n\t"
"v_mfma_f32_16x16x32_bf16 v[104:107], a[228:231], a[172:175], v[104:107]\n\t"
"v_mfma_f32_16x16x32_bf16 v[104:107], a[232:235], a[176:179], v[104:107]\n\t"
"v_cvt_pk_bf16_f32 v228, v228, v229\n\t"
"v_cvt_pk_bf16_f32 v229, v230, v231\n\t"
"v_mfma_f32_16x16x32_bf16 v[104:107], a[212:215], a[180:183], v[104:107]\n\t"
"s_mov_b32 s20, 1241088\n\t"
"buffer_load_dwordx4 a[208:211], v252, s[8:11], s20 offen offset:0 sc1\n\t"
"s_waitcnt lgkmcnt(0)\n\t"
"s_barrier\n\t"
"v_mfma_f32_16x16x32_bf16 v[108:111], a[228:231], a[176:179], v[108:111]\n\t"
"v_fma_f32 v0, v100, s52, v0\n\t"
"v_fma_f32 v1, v101, s52, v1\n\t"
"v_mfma_f32_16x16x32_bf16 v[108:111], a[232:235], a[180:183], v[108:111]\n\t"
"v_fma_f32 v2, v102, s52, v2\n\t"
"v_fma_f32 v3, v103, s52, v3\n\t"
"v_mfma_f32_16x16x32_bf16 v[108:111], a[212:215], a[184:187], v[108:111]\n\t"
"ds_write2st64_b64 v245, v[232:233], v[234:235] offset0:0 offset1:1\n\t"
"v_mfma_f32_16x16x32_bf16 v[112:115], a[228:231], a[180:183], v[112:115]\n\t"
"ds_read_b64_tr_b16 v[232:233], v246 offset:33280\n\t"
"v_fma_f32 v100, v0, s54, -v100\n\t"
"v_mfma_f32_16x16x32_bf16 v[112:115], a[232:235], a[184:187], v[112:115]\n\t"
"ds_read_b64_tr_b16 v[234:235], v246 offset:36352\n\t"
"v_fma_f32 v101, v1, s54, -v101\n\t"
"v_mfma_f32_16x16x32_bf16 v[112:115], a[212:215], a[188:191], v[112:115]\n\t"
"ds_write2st64_b64 v245, v[224:225], v[226:227] offset0:2 offset1:3\n\t"
"v_mfma_f32_16x16x32_bf16 v[116:119], a[228:231], a[184:187], v[116:119]\n\t"
"ds_read_b64_tr_b16 v[224:225], v246 offset:40960\n\t"
"v_mfma_f32_16x16x32_bf16 v[116:119], a[232:235], a[188:191], v[116:119]\n\t"
"ds_read_b64_tr_b16 v[226:227], v246 offset:44032\n\t"
"v_mfma_f32_16x16x32_bf16 v[116:119], a[212:215], a[192:195], v[116:119]\n\t"
"ds_write_b64 v245, v[228:229] offset:2048\n\t"
"v_mfma_f32_16x16x32_bf16 v[120:123], a[228:231], a[188:191], v[120:123]\n\t"
"v_mfma_f32_16x16x32_bf16 v[120:123], a[232:235], a[192:195], v[120:123]\n\t"
"v_mfma_f32_16x16x32_bf16 v[120:123], a[212:215], a[196:199], v[120:123]\n\t"
"ds_read_b64_tr_b16 v[228:229], v246 offset:47104\n\t"
"s_waitcnt lgkmcnt(5)\n\t"
"v_mfma_f32_16x16x32_bf16 v[232:235], v[232:235], v[220:223], 0\n\t"
"ds_read_b64_tr_b16 v[230:231], v246 offset:50176\n\t"
"v_mfma_f32_16x16x32_bf16 v[124:127], a[228:231], a[192:195], v[124:127]\n\t"
"v_mfma_f32_16x16x32_bf16 v[124:127], a[232:235], a[196:199], v[124:127]\n\t"
"v_fma_f32 v102, v2, s54, -v102\n\t"
"v_mfma_f32_16x16x32_bf16 v[124:127], a[212:215], a[200:203], v[124:127]\n\t"
"s_waitcnt lgkmcnt(3)\n\t"
"v_mfma_f32_16x16x32_bf16 v[224:227], v[224:227], v[220:223], 0\n\t"
"v_fma_f32 v103, v3, s54, -v103\n\t"
"v_mfma_f32_16x16x32_bf16 v[128:131], a[228:231], a[196:199], v[128:131]\n\t"
"v_cvt_pk_bf16_f32 v232, v232, v233\n\t"
"v_cvt_pk_bf16_f32 v233, v234, v235\n\t"
"v_mfma_f32_16x16x32_bf16 v[128:131], a[232:235], a[200:203], v[128:131]\n\t"
"v_fma_f32 v4, v104, s52, v4\n\t"
"v_mfma_f32_16x16x32_bf16 v[128:131], a[212:215], a[204:207], v[128:131]\n\t"
"buffer_load_dwordx4 a[228:231], v252, s[8:11], s20 offen offset:1024 sc1\n\t"
"s_waitcnt lgkmcnt(0)\n\t"
"v_mfma_f32_16x16x32_bf16 v[228:231], v[228:231], v[220:223], 0\n\t"
"buffer_load_dwordx4 a[212:215], v252, s[8:11], s20 offen offset:2048 sc1\n\t"
"s_waitcnt vmcnt(12)\n\t"
"v_mfma_f32_16x16x32_bf16 v[132:135], a[236:239], a[168:171], v[132:135]\n\t"
"v_cvt_pk_bf16_f32 v234, v224, v225\n\t"
"v_cvt_pk_bf16_f32 v235, v226, v227\n\t"
"v_mfma_f32_16x16x32_bf16 v[132:135], a[240:243], a[172:175], v[132:135]\n\t"
"ds_read_b64_tr_b16 v[224:225], v246 offset:53248\n\t"
"v_fma_f32 v5, v105, s52, v5\n\t"
"v_mfma_f32_16x16x32_bf16 v[132:135], a[216:219], a[176:179], v[132:135]\n\t"
"s_mov_b32 s20, 1253376\n\t"
"buffer_load_dwordx4 a[232:235], v252, s[8:11], s20 offen offset:0 sc1\n\t"
"v_mfma_f32_16x16x32_bf16 v[136:139], a[236:239], a[172:175], v[136:139]\n\t"
"ds_read_b64_tr_b16 v[226:227], v246 offset:56320\n\t"
"v_fma_f32 v6, v106, s52, v6\n\t"
"v_mfma_f32_16x16x32_bf16 v[136:139], a[240:243], a[176:179], v[136:139]\n\t"
"v_cvt_pk_bf16_f32 v228, v228, v229\n\t"
"v_cvt_pk_bf16_f32 v229, v230, v231\n\t"
"v_mfma_f32_16x16x32_bf16 v[136:139], a[216:219], a[180:183], v[136:139]\n\t"
"ds_write2st64_b64 v245, v[232:233], v[234:235] offset0:5 offset1:6\n\t"
"v_mfma_f32_16x16x32_bf16 v[140:143], a[236:239], a[176:179], v[140:143]\n\t"
"v_fma_f32 v7, v107, s52, v7\n\t"
"v_fma_f32 v104, v4, s54, -v104\n\t"
"v_mfma_f32_16x16x32_bf16 v[140:143], a[240:243], a[180:183], v[140:143]\n\t"
"v_fma_f32 v105, v5, s54, -v105\n\t"
"v_fma_f32 v106, v6, s54, -v106\n\t"
"v_mfma_f32_16x16x32_bf16 v[140:143], a[216:219], a[184:187], v[140:143]\n\t"
"v_fma_f32 v107, v7, s54, -v107\n\t"
"v_fma_f32 v8, v108, s52, v8\n\t"
"v_mfma_f32_16x16x32_bf16 v[144:147], a[236:239], a[180:183], v[144:147]\n\t"
"v_fma_f32 v9, v109, s52, v9\n\t"
"v_fma_f32 v10, v110, s52, v10\n\t"
"v_mfma_f32_16x16x32_bf16 v[144:147], a[240:243], a[184:187], v[144:147]\n\t"
"v_fma_f32 v11, v111, s52, v11\n\t"
"v_fma_f32 v108, v8, s54, -v108\n\t"
"v_mfma_f32_16x16x32_bf16 v[144:147], a[216:219], a[188:191], v[144:147]\n\t"
"v_fma_f32 v109, v9, s54, -v109\n\t"
"v_mfma_f32_16x16x32_bf16 v[148:151], a[236:239], a[184:187], v[148:151]\n\t"
"v_fma_f32 v110, v10, s54, -v110\n\t"
"v_fma_f32 v111, v11, s54, -v111\n\t"
"v_mfma_f32_16x16x32_bf16 v[148:151], a[240:243], a[188:191], v[148:151]\n\t"
"v_fma_f32 v12, v112, s52, v12\n\t"
"v_mfma_f32_16x16x32_bf16 v[148:151], a[216:219], a[192:195], v[148:151]\n\t"
"v_fma_f32 v13, v113, s52, v13\n\t"
"s_waitcnt lgkmcnt(1)\n\t"
"v_mfma_f32_16x16x32_bf16 v[224:227], v[224:227], v[220:223], 0\n\t"
"v_fma_f32 v14, v114, s52, v14\n\t"
"v_fma_f32 v15, v115, s52, v15\n\t"
"v_mfma_f32_16x16x32_bf16 v[152:155], a[236:239], a[188:191], v[152:155]\n\t"
"v_fma_f32 v112, v12, s54, -v112\n\t"
"v_mfma_f32_16x16x32_bf16 v[152:155], a[240:243], a[192:195], v[152:155]\n\t"
"v_fma_f32 v113, v13, s54, -v113\n\t"
"v_mfma_f32_16x16x32_bf16 v[152:155], a[216:219], a[196:199], v[152:155]\n\t"
"v_fma_f32 v114, v14, s54, -v114\n\t"
"v_fma_f32 v115, v15, s54, -v115\n\t"
"v_mfma_f32_16x16x32_bf16 v[156:159], a[236:239], a[192:195], v[156:159]\n\t"
"v_fma_f32 v16, v116, s52, v16\n\t"
"v_fma_f32 v17, v117, s52, v17\n\t"
"v_mfma_f32_16x16x32_bf16 v[156:159], a[240:243], a[196:199], v[156:159]\n\t"
"v_cvt_pk_bf16_f32 v230, v224, v225\n\t"
"v_cvt_pk_bf16_f32 v231, v226, v227\n\t"
"v_mfma_f32_16x16x32_bf16 v[156:159], a[216:219], a[200:203], v[156:159]\n\t"
"ds_write2st64_b64 v245, v[228:229], v[230:231] offset0:7 offset1:8\n\t"
"v_mfma_f32_16x16x32_bf16 v[160:163], a[236:239], a[196:199], v[160:163]\n\t"
"v_fma_f32 v18, v118, s52, v18\n\t"
"v_fma_f32 v19, v119, s52, v19\n\t"
"v_mfma_f32_16x16x32_bf16 v[160:163], a[240:243], a[200:203], v[160:163]\n\t"
"v_fma_f32 v116, v16, s54, -v116\n\t"
"v_fma_f32 v117, v17, s54, -v117\n\t"
"v_mfma_f32_16x16x32_bf16 v[160:163], a[216:219], a[204:207], v[160:163]\n\t"
"buffer_load_dwordx4 a[236:239], v252, s[8:11], s20 offen offset:1024 sc1\n\t"
"s_waitcnt vmcnt(11)\n\t"
"v_mfma_f32_16x16x32_bf16 v[164:167], a[244:247], a[168:171], v[164:167]\n\t"
"v_fma_f32 v118, v18, s54, -v118\n\t"
"v_fma_f32 v119, v19, s54, -v119\n\t"
"v_mfma_f32_16x16x32_bf16 v[164:167], a[220:223], a[172:175], v[164:167]\n\t"
"v_fma_f32 v20, v120, s52, v20\n\t"
"v_fma_f32 v21, v121, s52, v21\n\t"
"v_mfma_f32_16x16x32_bf16 v[164:167], v[236:239], a[176:179], v[164:167]\n\t"
"buffer_load_dwordx4 a[216:219], v252, s[8:11], s20 offen offset:2048 sc1\n\t"
"s_waitcnt lgkmcnt(0)\n\t"
"s_barrier\n\t"
"v_mfma_f32_16x16x32_bf16 v[168:171], a[244:247], a[172:175], v[168:171]\n\t"
"ds_read_b128 a[168:171], v244 offset:0\n\t"
"v_fma_f32 v22, v122, s52, v22\n\t"
"v_mfma_f32_16x16x32_bf16 v[168:171], a[220:223], a[176:179], v[168:171]\n\t"
"ds_read_b128 a[172:175], v244 offset:512\n\t"
"v_fma_f32 v23, v123, s52, v23\n\t"
"v_mfma_f32_16x16x32_bf16 v[168:171], v[236:239], a[180:183], v[168:171]\n\t"
"s_mov_b32 s20, 1265664\n\t"
"buffer_load_dwordx4 a[240:243], v252, s[8:11], s20 offen offset:0 sc1\n\t"
"v_fma_f32 v120, v20, s54, -v120\n\t"
"v_fma_f32 v121, v21, s54, -v121\n\t"
"v_mfma_f32_16x16x32_bf16 v[172:175], a[244:247], a[176:179], v[172:175]\n\t"
"ds_read_b128 a[176:179], v244 offset:1024\n\t"
"v_fma_f32 v122, v22, s54, -v122\n\t"
"v_mfma_f32_16x16x32_bf16 v[172:175], a[220:223], a[180:183], v[172:175]\n\t"
"v_fma_f32 v123, v23, s54, -v123\n\t"
"v_fma_f32 v24, v124, s52, v24\n\t"
"v_mfma_f32_16x16x32_bf16 v[172:175], v[236:239], a[184:187], v[172:175]\n\t"
"v_fma_f32 v25, v125, s52, v25\n\t"
"v_fma_f32 v26, v126, s52, v26\n\t"
"v_mfma_f32_16x16x32_bf16 v[176:179], a[244:247], a[180:183], v[176:179]\n\t"
"ds_read_b128 a[180:183], v244 offset:1536\n\t"
"v_fma_f32 v27, v127, s52, v27\n\t"
"v_mfma_f32_16x16x32_bf16 v[176:179], a[220:223], a[184:187], v[176:179]\n\t"
"v_fma_f32 v124, v24, s54, -v124\n\t"
"v_fma_f32 v125, v25, s54, -v125\n\t"
"v_mfma_f32_16x16x32_bf16 v[176:179], v[236:239], a[188:191], v[176:179]\n\t"
"v_fma_f32 v126, v26, s54, -v126\n\t"
"v_fma_f32 v127, v27, s54, -v127\n\t"
"v_fma_f32 v28, v128, s52, v28\n\t"
"v_fma_f32 v29, v129, s52, v29\n\t"
"v_mfma_f32_16x16x32_bf16 v[180:183], a[244:247], a[184:187], v[180:183]\n\t"
"ds_read_b128 a[184:187], v244 offset:2048\n\t"
"v_fma_f32 v30, v130, s52, v30\n\t"
"v_mfma_f32_16x16x32_bf16 v[180:183], a[220:223], a[188:191], v[180:183]\n\t"
"v_fma_f32 v31, v131, s52, v31\n\t"
"v_fma_f32 v128, v28, s54, -v128\n\t"
"v_mfma_f32_16x16x32_bf16 v[180:183], v[236:239], a[192:195], v[180:183]\n\t"
"v_fma_f32 v129, v29, s54, -v129\n\t"
"v_fma_f32 v130, v30, s54, -v130\n\t"
"v_fma_f32 v131, v31, s54, -v131\n\t"
"v_fma_f32 v32, v132, s52, v32\n\t"
"v_mfma_f32_16x16x32_bf16 v[184:187], a[244:247], a[188:191], v[184:187]\n\t"
"ds_read_b128 a[188:191], v244 offset:2560\n\t"
"v_fma_f32 v33, v133, s52, v33\n\t"
"v_mfma_f32_16x16x32_bf16 v[184:187], a[220:223], a[192:195], v[184:187]\n\t"
"v_fma_f32 v34, v134, s52, v34\n\t"
"v_fma_f32 v35, v135, s52, v35\n\t"
"v_mfma_f32_16x16x32_bf16 v[184:187], v[236:239], a[196:199], v[184:187]\n\t"
"v_fma_f32 v132, v32, s54, -v132\n\t"
"v_fma_f32 v133, v33, s54, -v133\n\t"
"v_fma_f32 v134, v34, s54, -v134\n\t"
"v_fma_f32 v135, v35, s54, -v135\n\t"
"v_mfma_f32_16x16x32_bf16 v[188:191], a[244:247], a[192:195], v[188:191]\n\t"
"ds_read_b128 a[192:195], v244 offset:3072\n\t"
"v_fma_f32 v36, v136, s52, v36\n\t"
"v_mfma_f32_16x16x32_bf16 v[188:191], a[220:223], a[196:199], v[188:191]\n\t"
"v_fma_f32 v37, v137, s52, v37\n\t"
"v_fma_f32 v38, v138, s52, v38\n\t"
"v_mfma_f32_16x16x32_bf16 v[188:191], v[236:239], a[200:203], v[188:191]\n\t"
"v_fma_f32 v39, v139, s52, v39\n\t"
"v_fma_f32 v136, v36, s54, -v136\n\t"
"v_fma_f32 v137, v37, s54, -v137\n\t"
"v_fma_f32 v138, v38, s54, -v138\n\t"
"v_mfma_f32_16x16x32_bf16 v[192:195], a[244:247], a[196:199], v[192:195]\n\t"
"ds_read_b128 a[196:199], v244 offset:3584\n\t"
"v_fma_f32 v139, v39, s54, -v139\n\t"
"v_mfma_f32_16x16x32_bf16 v[192:195], a[220:223], a[200:203], v[192:195]\n\t"
"ds_read_b128 a[200:203], v244 offset:4096\n\t"
"v_fma_f32 v40, v140, s52, v40\n\t"
"v_mfma_f32_16x16x32_bf16 v[192:195], v[236:239], a[204:207], v[192:195]\n\t"
"buffer_load_dwordx4 a[244:247], v252, s[8:11], s20 offen offset:1024 sc1\n\t"
"ds_read_b128 a[204:207], v244 offset:4608\n\t"
"v_fma_f32 v41, v141, s52, v41\n\t"
"Lst13_%=:\n\t"
"s_waitcnt vmcnt(11) lgkmcnt(7)\n\t"
"v_mfma_f32_16x16x32_bf16 v[196:199], a[248:251], a[168:171], v[196:199]\n\t"
"v_fma_f32 v42, v142, s52, v42\n\t"
"v_fma_f32 v43, v143, s52, v43\n\t"
"v_mfma_f32_16x16x32_bf16 v[196:199], a[252:255], a[172:175], v[196:199]\n\t"
"v_fma_f32 v140, v40, s54, -v140\n\t"
"v_fma_f32 v141, v41, s54, -v141\n\t"
"v_mfma_f32_16x16x32_bf16 v[196:199], v[240:243], a[176:179], v[196:199]\n\t"
"buffer_load_dwordx4 a[220:223], v252, s[8:11], s20 offen offset:2048 sc1\n\t"
"v_fma_f32 v142, v42, s54, -v142\n\t"
"v_fma_f32 v143, v43, s54, -v143\n\t"
"s_waitcnt lgkmcnt(6)\n\t"
"v_mfma_f32_16x16x32_bf16 v[204:207], a[248:251], a[172:175], v[204:207]\n\t"
"v_fma_f32 v44, v144, s52, v44\n\t"
"v_fma_f32 v45, v145, s52, v45\n\t"
"v_mfma_f32_16x16x32_bf16 v[204:207], a[252:255], a[176:179], v[204:207]\n\t"
"v_fma_f32 v46, v146, s52, v46\n\t"
"v_fma_f32 v47, v147, s52, v47\n\t"
"v_mfma_f32_16x16x32_bf16 v[204:207], v[240:243], a[180:183], v[204:207]\n\t"
"v_fma_f32 v144, v44, s54, -v144\n\t"
"v_fma_f32 v145, v45, s54, -v145\n\t"
"v_fma_f32 v146, v46, s54, -v146\n\t"
"v_fma_f32 v147, v47, s54, -v147\n\t"
"s_waitcnt lgkmcnt(5)\n\t"
"v_mfma_f32_16x16x32_bf16 v[212:215], a[248:251], a[176:179], v[212:215]\n\t"
"v_fma_f32 v48, v148, s52, v48\n\t"
"v_fma_f32 v49, v149, s52, v49\n\t"
"v_mfma_f32_16x16x32_bf16 v[212:215], a[252:255], a[180:183], v[212:215]\n\t"
"v_fma_f32 v50, v150, s52, v50\n\t"
"v_fma_f32 v51, v151, s52, v51\n\t"
"v_mfma_f32_16x16x32_bf16 v[212:215], v[240:243], a[184:187], v[212:215]\n\t"
"v_fma_f32 v148, v48, s54, -v148\n\t"
"v_fma_f32 v149, v49, s54, -v149\n\t"
"v_fma_f32 v150, v50, s54, -v150\n\t"
"v_fma_f32 v151, v51, s54, -v151\n\t"
"s_waitcnt lgkmcnt(4)\n\t"
"v_mfma_f32_16x16x32_bf16 a[0:3], a[248:251], a[180:183], a[0:3]\n\t"
"v_fma_f32 v52, v152, s52, v52\n\t"
"v_fma_f32 v53, v153, s52, v53\n\t"
"v_mfma_f32_16x16x32_bf16 a[0:3], a[252:255], a[184:187], a[0:3]\n\t"
"v_fma_f32 v54, v154, s52, v54\n\t"
"v_fma_f32 v55, v155, s52, v55\n\t"
"v_mfma_f32_16x16x32_bf16 a[0:3], v[240:243], a[188:191], a[0:3]\n\t"
"v_fma_f32 v152, v52, s54, -v152\n\t"
"v_fma_f32 v153, v53, s54, -v153\n\t"
"v_fma_f32 v154, v54, s54, -v154\n\t"
"v_fma_f32 v155, v55, s54, -v155\n\t"
"s_waitcnt lgkmcnt(3)\n\t"
"v_mfma_f32_16x16x32_bf16 a[8:11], a[248:251], a[184:187], a[8:11]\n\t"
"v_fma_f32 v56, v156, s52, v56\n\t"
"v_fma_f32 v57, v157, s52, v57\n\t"
"v_mfma_f32_16x16x32_bf16 a[8:11], a[252:255], a[188:191], a[8:11]\n\t"
"v_fma_f32 v58, v158, s52, v58\n\t"
"v_fma_f32 v59, v159, s52, v59\n\t"
"v_mfma_f32_16x16x32_bf16 a[8:11], v[240:243], a[192:195], a[8:11]\n\t"
"v_fma_f32 v156, v56, s54, -v156\n\t"
"v_fma_f32 v157, v57, s54, -v157\n\t"
"v_fma_f32 v158, v58, s54, -v158\n\t"
"v_fma_f32 v159, v59, s54, -v159\n\t"
"s_waitcnt lgkmcnt(2)\n\t"
"v_mfma_f32_16x16x32_bf16 a[16:19], a[248:251], a[188:191], a[16:19]\n\t"
"v_fma_f32 v60, v160, s52, v60\n\t"
"v_fma_f32 v61, v161, s52, v61\n\t"
"v_mfma_f32_16x16x32_bf16 a[16:19], a[252:255], a[192:195], a[16:19]\n\t"
"v_fma_f32 v62, v162, s52, v62\n\t"
"v_fma_f32 v63, v163, s52, v63\n\t"
"v_mfma_f32_16x16x32_bf16 a[16:19], v[240:243], a[196:199], a[16:19]\n\t"
"v_fma_f32 v160, v60, s54, -v160\n\t"
"v_fma_f32 v161, v61, s54, -v161\n\t"
"v_fma_f32 v162, v62, s54, -v162\n\t"
"v_fma_f32 v163, v63, s54, -v163\n\t"
"s_waitcnt lgkmcnt(1)\n\t"
"v_mfma_f32_16x16x32_bf16 a[24:27], a[248:251], a[192:195], a[24:27]\n\t"
"v_fma_f32 v64, v164, s52, v64\n\t"
"v_fma_f32 v65, v165, s52, v65\n\t"
"v_mfma_f32_16x16x32_bf16 a[24:27], a[252:255], a[196:199], a[24:27]\n\t"
"v_fma_f32 v66, v166, s52, v66\n\t"
"v_fma_f32 v67, v167, s52, v67\n\t"
"v_mfma_f32_16x16x32_bf16 a[24:27], v[240:243], a[200:203], a[24:27]\n\t"
"v_fma_f32 v164, v64, s54, -v164\n\t"
"v_fma_f32 v165, v65, s54, -v165\n\t"
"s_waitcnt lgkmcnt(0)\n\t"
"v_mfma_f32_16x16x32_bf16 a[32:35], a[248:251], a[196:199], a[32:35]\n\t"
"v_fma_f32 v166, v66, s54, -v166\n\t"
"v_fma_f32 v167, v67, s54, -v167\n\t"
"v_mfma_f32_16x16x32_bf16 a[32:35], a[252:255], a[200:203], a[32:35]\n\t"
"v_fma_f32 v68, v168, s52, v68\n\t"
"v_fma_f32 v69, v169, s52, v69\n\t"
"v_mfma_f32_16x16x32_bf16 a[32:35], v[240:243], a[204:207], a[32:35]\n\t"
"s_mov_b32 s20, 1277952\n\t"
"buffer_load_dwordx4 a[248:251], v252, s[8:11], s20 offen offset:0 sc1\n\t"
"v_fma_f32 v70, v170, s52, v70\n\t"
"v_fma_f32 v71, v171, s52, v71\n\t"
"s_waitcnt vmcnt(10)\n\t"
"v_mfma_f32_16x16x32_bf16 a[40:43], a[224:227], a[168:171], a[40:43]\n\t"
"v_fma_f32 v168, v68, s54, -v168\n\t"
"v_fma_f32 v169, v69, s54, -v169\n\t"
"v_mfma_f32_16x16x32_bf16 a[40:43], v[248:251], a[172:175], a[40:43]\n\t"
"v_fma_f32 v170, v70, s54, -v170\n\t"
"v_fma_f32 v171, v71, s54, -v171\n\t"
"v_mfma_f32_16x16x32_bf16 a[40:43], v[96:99], a[176:179], a[40:43]\n\t"
"buffer_load_dwordx4 v[240:243], v252, s[8:11], s20 offen offset:1024 sc1\n\t"
"v_mfma_f32_16x16x32_bf16 a[48:51], a[224:227], a[172:175], a[48:51]\n\t"
"v_fma_f32 v72, v172, s52, v72\n\t"
"v_fma_f32 v73, v173, s52, v73\n\t"
"v_mfma_f32_16x16x32_bf16 a[48:51], v[248:251], a[176:179], a[48:51]\n\t"
"v_fma_f32 v74, v174, s52, v74\n\t"
"v_fma_f32 v75, v175, s52, v75\n\t"
"v_mfma_f32_16x16x32_bf16 a[48:51], v[96:99], a[180:183], a[48:51]\n\t"
"buffer_load_dwordx4 a[252:255], v252, s[8:11], s20 offen offset:2048 sc1\n\t"
"v_fma_f32 v172, v72, s54, -v172\n\t"
"v_fma_f32 v173, v73, s54, -v173\n\t"
"v_mfma_f32_16x16x32_bf16 a[56:59], a[224:227], a[176:179], a[56:59]\n\t"
"v_fma_f32 v174, v74, s54, -v174\n\t"
"v_fma_f32 v175, v75, s54, -v175\n\t"
"v_mfma_f32_16x16x32_bf16 a[56:59], v[248:251], a[180:183], a[56:59]\n\t"
"v_fma_f32 v76, v176, s52, v76\n\t"
"v_fma_f32 v77, v177, s52, v77\n\t"
"v_mfma_f32_16x16x32_bf16 a[56:59], v[96:99], a[184:187], a[56:59]\n\t"
"v_fma_f32 v78, v178, s52, v78\n\t"
"v_fma_f32 v79, v179, s52, v79\n\t"
"v_mfma_f32_16x16x32_bf16 a[64:67], a[224:227], a[180:183], a[64:67]\n\t"
"v_fma_f32 v176, v76, s54, -v176\n\t"
"v_fma_f32 v177, v77, s54, -v177\n\t"
"v_mfma_f32_16x16x32_bf16 a[64:67], v[248:251], a[184:187], a[64:67]\n\t"
"v_fma_f32 v178, v78, s54, -v178\n\t"
"v_fma_f32 v179, v79, s54, -v179\n\t"
"v_mfma_f32_16x16x32_bf16 a[64:67], v[96:99], a[188:191], a[64:67]\n\t"
"v_fma_f32 v80, v180, s52, v80\n\t"
"v_fma_f32 v81, v181, s52, v81\n\t"
"v_mfma_f32_16x16x32_bf16 a[72:75], a[224:227], a[184:187], a[72:75]\n\t"
"v_fma_f32 v82, v182, s52, v82\n\t"
"v_fma_f32 v83, v183, s52, v83\n\t"
"v_mfma_f32_16x16x32_bf16 a[72:75], v[248:251], a[188:191], a[72:75]\n\t"
"v_fma_f32 v180, v80, s54, -v180\n\t"
"v_fma_f32 v181, v81, s54, -v181\n\t"
"v_mfma_f32_16x16x32_bf16 a[72:75], v[96:99], a[192:195], a[72:75]\n\t"
"v_fma_f32 v182, v82, s54, -v182\n\t"
"v_fma_f32 v183, v83, s54, -v183\n\t"
"v_mfma_f32_16x16x32_bf16 a[80:83], a[224:227], a[188:191], a[80:83]\n\t"
"v_fma_f32 v84, v184, s52, v84\n\t"
"v_fma_f32 v85, v185, s52, v85\n\t"
"v_mfma_f32_16x16x32_bf16 a[80:83], v[248:251], a[192:195], a[80:83]\n\t"
"v_fma_f32 v86, v186, s52, v86\n\t"
"v_mfma_f32_16x16x32_bf16 a[80:83], v[96:99], a[196:199], a[80:83]\n\t"
"v_fma_f32 v87, v187, s52, v87\n\t"
"v_mfma_f32_16x16x32_bf16 a[88:91], a[224:227], a[192:195], a[88:91]\n\t"
"v_fma_f32 v184, v84, s54, -v184\n\t"
"v_fma_f32 v185, v85, s54, -v185\n\t"
"v_mfma_f32_16x16x32_bf16 a[88:91], v[248:251], a[196:199], a[88:91]\n\t"
"v_fma_f32 v186, v86, s54, -v186\n\t"
"v_mfma_f32_16x16x32_bf16 a[88:91], v[96:99], a[200:203], a[88:91]\n\t"
"v_fma_f32 v187, v87, s54, -v187\n\t"
"v_mfma_f32_16x16x32_bf16 a[96:99], a[224:227], a[196:199], a[96:99]\n\t"
"v_fma_f32 v88, v188, s52, v88\n\t"
"v_fma_f32 v89, v189, s52, v89\n\t"
"v_mfma_f32_16x16x32_bf16 a[96:99], v[248:251], a[200:203], a[96:99]\n\t"
"v_fma_f32 v90, v190, s52, v90\n\t"
"v_mfma_f32_16x16x32_bf16 a[96:99], v[96:99], a[204:207], a[96:99]\n\t"
"s_mov_b32 s20, 442368\n\t"
"buffer_load_dwordx4 a[224:227], v252, s[8:11], s20 offen offset:0 sc1\n\t"
"s_waitcnt vmcnt(10)\n\t"
"v_mfma_f32_16x16x32_bf16 a[104:107], a[208:211], a[168:171], a[104:107]\n\t"
"ds_read_b128 a[168:171], v244 offset:9216\n\t"
"v_fma_f32 v91, v191, s52, v91\n\t"
"v_mfma_f32_16x16x32_bf16 a[104:107], a[228:231], a[172:175], a[104:107]\n\t"
"v_fma_f32 v188, v88, s54, -v188\n\t"
"v_mfma_f32_16x16x32_bf16 a[104:107], a[212:215], a[176:179], a[104:107]\n\t"
"buffer_load_dwordx4 v[248:251], v252, s[8:11], s20 offen offset:1024 sc1\n\t"
"v_mfma_f32_16x16x32_bf16 a[112:115], a[208:211], a[172:175], a[112:115]\n\t"
"ds_read_b128 a[172:175], v244 offset:9728\n\t"
"v_fma_f32 v189, v89, s54, -v189\n\t"
"v_mfma_f32_16x16x32_bf16 a[112:115], a[228:231], a[176:179], a[112:115]\n\t"
"v_fma_f32 v190, v90, s54, -v190\n\t"
"v_mfma_f32_16x16x32_bf16 a[112:115], a[212:215], a[180:183], a[112:115]\n\t"
"v_fma_f32 v191, v91, s54, -v191\n\t"
"v_mfma_f32_16x16x32_bf16 a[120:123], a[208:211], a[176:179], a[120:123]\n\t"
"ds_read_b128 a[176:179], v244 offset:10240\n\t"
"v_mfma_f32_16x16x32_bf16 a[120:123], a[228:231], a[180:183], a[120:123]\n\t"
"v_fma_f32 v92, v192, s52, v92\n\t"
"v_mfma_f32_16x16x32_bf16 a[120:123], a[212:215], a[184:187], a[120:123]\n\t"
"v_fma_f32 v93, v193, s52, v93\n\t"
"v_mfma_f32_16x16x32_bf16 a[128:131], a[208:211], a[180:183], a[128:131]\n\t"
"ds_read_b128 a[180:183], v244 offset:10752\n\t"
"v_fma_f32 v94, v194, s52, v94\n\t"
"v_mfma_f32_16x16x32_bf16 a[128:131], a[228:231], a[184:187], a[128:131]\n\t"
"v_fma_f32 v95, v195, s52, v95\n\t"
"v_mfma_f32_16x16x32_bf16 a[128:131], a[212:215], a[188:191], a[128:131]\n\t"
"v_fma_f32 v192, v92, s54, -v192\n\t"
"v_mfma_f32_16x16x32_bf16 a[136:139], a[208:211], a[184:187], a[136:139]\n\t"
"ds_read_b128 a[184:187], v244 offset:11264\n\t"
"v_mfma_f32_16x16x32_bf16 a[136:139], a[228:231], a[188:191], a[136:139]\n\t"
"v_mfma_f32_16x16x32_bf16 a[136:139], a[212:215], a[192:195], a[136:139]\n\t"
"v_fma_f32 v193, v93, s54, -v193\n\t"
"v_mfma_f32_16x16x32_bf16 a[144:147], a[208:211], a[188:191], a[144:147]\n\t"
"ds_read_b128 a[188:191], v244 offset:11776\n\t"
"v_mfma_f32_16x16x32_bf16 a[144:147], a[228:231], a[192:195], a[144:147]\n\t"
"v_fma_f32 v194, v94, s54, -v194\n\t"
"v_mfma_f32_16x16x32_bf16 a[144:147], a[212:215], a[196:199], a[144:147]\n\t"
"v_mfma_f32_16x16x32_bf16 a[152:155], a[208:211], a[192:195], a[152:155]\n\t"
"ds_read_b128 a[192:195], v244 offset:12288\n\t"
"v_mfma_f32_16x16x32_bf16 a[152:155], a[228:231], a[196:199], a[152:155]\n\t"
"v_fma_f32 v195, v95, s54, -v195\n\t"
"v_mfma_f32_16x16x32_bf16 a[152:155], a[212:215], a[200:203], a[152:155]\n\t"
"v_mfma_f32_16x16x32_bf16 a[160:163], a[208:211], a[196:199], a[160:163]\n\t"
"ds_read_b128 a[196:199], v244 offset:12800\n\t"
"v_mfma_f32_16x16x32_bf16 a[160:163], a[228:231], a[200:203], a[160:163]\n\t"
"ds_read_b128 a[200:203], v244 offset:13312\n\t"
"v_mfma_f32_16x16x32_bf16 a[160:163], a[212:215], a[204:207], a[160:163]\n\t"
"buffer_load_dwordx4 a[208:211], v252, s[8:11], s20 offen offset:2048 sc1\n\t"
"s_waitcnt vmcnt(9) lgkmcnt(6)\n\t"
"v_mfma_f32_16x16x32_bf16 v[200:203], a[232:235], a[168:171], v[200:203]\n\t"
"ds_read_b128 a[204:207], v244 offset:13824\n\t"
"v_mfma_f32_16x16x32_bf16 v[200:203], a[236:239], a[172:175], v[200:203]\n\t"
"v_mfma_f32_16x16x32_bf16 v[200:203], a[216:219], a[176:179], v[200:203]\n\t"
"s_mov_b32 s20, 454656\n\t"
"buffer_load_dwordx4 a[212:215], v252, s[8:11], s20 offen offset:0 sc1\n\t"
"s_waitcnt lgkmcnt(6)\n\t"
"v_mfma_f32_16x16x32_bf16 v[208:211], a[232:235], a[172:175], v[208:211]\n\t"
"v_mfma_f32_16x16x32_bf16 v[208:211], a[236:239], a[176:179], v[208:211]\n\t"
"v_mfma_f32_16x16x32_bf16 v[208:211], a[216:219], a[180:183], v[208:211]\n\t"
"buffer_load_dwordx4 a[228:231], v252, s[8:11], s20 offen offset:1024 sc1\n\t"
"s_waitcnt lgkmcnt(0)\n\t"
"s_barrier\n\t"
"v_mfma_f32_16x16x32_bf16 v[216:219], a[232:235], a[176:179], v[216:219]\n\t"
"v_mfma_f32_16x16x32_bf16 v[216:219], a[236:239], a[180:183], v[216:219]\n\t"
"v_mfma_f32_16x16x32_bf16 v[216:219], a[216:219], a[184:187], v[216:219]\n\t"
"v_pk_fma_f32 v[196:197], v[200:201], s[56:57], v[196:197]\n\t"
"v_pk_fma_f32 v[200:201], v[196:197], s[58:59], v[200:201] neg_lo:[0,0,1] neg_hi:[0,0,1]\n\t"
"v_pk_fma_f32 v[228:229], v[0:1], s[60:61], v[196:197]\n\t"
"v_pk_fma_f32 v[0:1], v[196:197], s[60:61], v[0:1]\n\t"
"v_pk_fma_f32 v[196:197], v[100:101], s[62:63], v[200:201]\n\t"
"v_pk_fma_f32 v[100:101], v[200:201], s[62:63], v[100:101]\n\t"
"v_pk_fma_f32 v[198:199], v[202:203], s[56:57], v[198:199]\n\t"
"v_pk_fma_f32 v[202:203], v[198:199], s[58:59], v[202:203] neg_lo:[0,0,1] neg_hi:[0,0,1]\n\t"
"v_pk_fma_f32 v[234:235], v[2:3], s[60:61], v[198:199]\n\t"
"v_pk_fma_f32 v[2:3], v[198:199], s[60:61], v[2:3]\n\t"
"v_pk_fma_f32 v[198:199], v[102:103], s[62:63], v[202:203]\n\t"
"v_pk_fma_f32 v[102:103], v[202:203], s[62:63], v[102:103]\n\t"
"v_cvt_pk_bf16_f32 v196, v196, v197\n\t"
"v_cvt_pk_bf16_f32 v200, v228, v229\n\t"
"v_mfma_f32_16x16x32_bf16 a[4:7], a[232:235], a[180:183], a[4:7]\n\t"
"v_cvt_pk_bf16_f32 v197, v198, v199\n\t"
"v_mfma_f32_16x16x32_bf16 a[4:7], a[236:239], a[184:187], a[4:7]\n\t"
"v_cvt_pk_bf16_f32 v201, v234, v235\n\t"
"v_mfma_f32_16x16x32_bf16 a[4:7], a[216:219], a[188:191], a[4:7]\n\t"
"v_pk_fma_f32 v[204:205], v[208:209], s[56:57], v[204:205]\n\t"
"v_pk_fma_f32 v[208:209], v[204:205], s[58:59], v[208:209] neg_lo:[0,0,1] neg_hi:[0,0,1]\n\t"
"v_pk_fma_f32 v[98:99], v[4:5], s[60:61], v[204:205]\n\t"
"v_pk_fma_f32 v[4:5], v[204:205], s[60:61], v[4:5]\n\t"
"v_pk_fma_f32 v[204:205], v[104:105], s[62:63], v[208:209]\n\t"
"v_pk_fma_f32 v[104:105], v[208:209], s[62:63], v[104:105]\n\t"
"v_cvt_pk_bf16_f32 v204, v204, v205\n\t"
"v_mfma_f32_16x16x32_bf16 a[12:15], a[232:235], a[184:187], a[12:15]\n\t"
"v_cvt_pk_bf16_f32 v208, v98, v99\n\t"
"v_mfma_f32_16x16x32_bf16 a[12:15], a[236:239], a[188:191], a[12:15]\n\t"
"v_mfma_f32_16x16x32_bf16 a[12:15], a[216:219], a[192:195], a[12:15]\n\t"
"v_pk_fma_f32 v[206:207], v[210:211], s[56:57], v[206:207]\n\t"
"v_pk_fma_f32 v[210:211], v[206:207], s[58:59], v[210:211] neg_lo:[0,0,1] neg_hi:[0,0,1]\n\t"
"v_pk_fma_f32 v[228:229], v[6:7], s[60:61], v[206:207]\n\t"
"v_pk_fma_f32 v[6:7], v[206:207], s[60:61], v[6:7]\n\t"
"v_pk_fma_f32 v[206:207], v[106:107], s[62:63], v[210:211]\n\t"
"v_pk_fma_f32 v[106:107], v[210:211], s[62:63], v[106:107]\n\t"
"v_cvt_pk_bf16_f32 v205, v206, v207\n\t"
"v_mfma_f32_16x16x32_bf16 a[20:23], a[232:235], a[188:191], a[20:23]\n\t"
"v_cvt_pk_bf16_f32 v209, v228, v229\n\t"
"v_mfma_f32_16x16x32_bf16 a[20:23], a[236:239], a[192:195], a[20:23]\n\t"
"v_accvgpr_read_b32 v224, a0\n\t"
"v_mfma_f32_16x16x32_bf16 a[20:23], a[216:219], a[196:199], a[20:23]\n\t"
"v_pk_fma_f32 v[212:213], v[216:217], s[56:57], v[212:213]\n\t"
"v_pk_fma_f32 v[216:217], v[212:213], s[58:59], v[216:217] neg_lo:[0,0,1] neg_hi:[0,0,1]\n\t"
"v_pk_fma_f32 v[234:235], v[8:9], s[60:61], v[212:213]\n\t"
"v_pk_fma_f32 v[8:9], v[212:213], s[60:61], v[8:9]\n\t"
"v_pk_fma_f32 v[212:213], v[108:109], s[62:63], v[216:217]\n\t"
"v_pk_fma_f32 v[108:109], v[216:217], s[62:63], v[108:109]\n\t"
"v_cvt_pk_bf16_f32 v212, v212, v213\n\t"
"v_cvt_pk_bf16_f32 v216, v234, v235\n\t"
"v_mfma_f32_16x16x32_bf16 a[28:31], a[232:235], a[192:195], a[28:31]\n\t"
"v_accvgpr_read_b32 v226, a4\n\t"
"v_mfma_f32_16x16x32_bf16 a[28:31], a[236:239], a[196:199], a[28:31]\n\t"
"v_accvgpr_read_b32 v225, a1\n\t"
"v_accvgpr_read_b32 v227, a5\n\t"
"v_mfma_f32_16x16x32_bf16 a[28:31], a[216:219], a[200:203], a[28:31]\n\t"
"v_pk_fma_f32 v[214:215], v[218:219], s[56:57], v[214:215]\n\t"
"v_pk_fma_f32 v[218:219], v[214:215], s[58:59], v[218:219] neg_lo:[0,0,1] neg_hi:[0,0,1]\n\t"
"v_pk_fma_f32 v[98:99], v[10:11], s[60:61], v[214:215]\n\t"
"v_pk_fma_f32 v[10:11], v[214:215], s[60:61], v[10:11]\n\t"
"v_pk_fma_f32 v[214:215], v[110:111], s[62:63], v[218:219]\n\t"
"v_pk_fma_f32 v[110:111], v[218:219], s[62:63], v[110:111]\n\t"
"v_cvt_pk_bf16_f32 v213, v214, v215\n\t"
"v_mfma_f32_16x16x32_bf16 a[36:39], a[232:235], a[196:199], a[36:39]\n\t"
"v_cvt_pk_bf16_f32 v217, v98, v99\n\t"
"v_accvgpr_read_b32 v230, a2\n\t"
"v_mfma_f32_16x16x32_bf16 a[36:39], a[236:239], a[200:203], a[36:39]\n\t"
"v_accvgpr_read_b32 v232, a6\n\t"
"v_accvgpr_read_b32 v231, a3\n\t"
"v_mfma_f32_16x16x32_bf16 a[36:39], a[216:219], a[204:207], a[36:39]\n\t"
"v_pk_fma_f32 v[224:225], v[226:227], s[56:57], v[224:225]\n\t"
"v_pk_fma_f32 v[226:227], v[224:225], s[58:59], v[226:227] neg_lo:[0,0,1] neg_hi:[0,0,1]\n\t"
"v_pk_fma_f32 v[228:229], v[12:13], s[60:61], v[224:225]\n\t"
"v_pk_fma_f32 v[12:13], v[224:225], s[60:61], v[12:13]\n\t"
"v_pk_fma_f32 v[224:225], v[112:113], s[62:63], v[226:227]\n\t"
"v_pk_fma_f32 v[112:113], v[226:227], s[62:63], v[112:113]\n\t"
"buffer_load_dwordx4 a[232:235], v252, s[8:11], s20 offen offset:2048 sc1\n\t"
"s_waitcnt vmcnt(9)\n\t"
"v_mfma_f32_16x16x32_bf16 a[44:47], a[240:243], a[168:171], a[44:47]\n\t"
"v_accvgpr_read_b32 v233, a7\n\t"
"v_cvt_pk_bf16_f32 v224, v224, v225\n\t"
"v_mfma_f32_16x16x32_bf16 a[44:47], a[244:247], a[172:175], a[44:47]\n\t"
"v_cvt_pk_bf16_f32 v228, v228, v229\n\t"
"v_accvgpr_write_b32 a0, v224\n\t"
"v_mfma_f32_16x16x32_bf16 a[44:47], a[220:223], a[176:179], a[44:47]\n\t"
"v_pk_fma_f32 v[230:231], v[232:233], s[56:57], v[230:231]\n\t"
"v_pk_fma_f32 v[232:233], v[230:231], s[58:59], v[232:233] neg_lo:[0,0,1] neg_hi:[0,0,1]\n\t"
"v_pk_fma_f32 v[234:235], v[14:15], s[60:61], v[230:231]\n\t"
"v_pk_fma_f32 v[14:15], v[230:231], s[60:61], v[14:15]\n\t"
"v_pk_fma_f32 v[230:231], v[114:115], s[62:63], v[232:233]\n\t"
"v_pk_fma_f32 v[114:115], v[232:233], s[62:63], v[114:115]\n\t"
"s_mov_b32 s20, 466944\n\t"
"buffer_load_dwordx4 a[216:219], v252, s[8:11], s20 offen offset:0 sc1\n\t"
"v_accvgpr_write_b32 a4, v228\n\t"
"v_accvgpr_read_b32 v236, a8\n\t"
"v_mfma_f32_16x16x32_bf16 a[52:55], a[240:243], a[172:175], a[52:55]\n\t"
"v_accvgpr_read_b32 v238, a12\n\t"
"v_accvgpr_read_b32 v237, a9\n\t"
"v_mfma_f32_16x16x32_bf16 a[52:55], a[244:247], a[176:179], a[52:55]\n\t"
"v_accvgpr_read_b32 v239, a13\n\t"
"v_cvt_pk_bf16_f32 v230, v230, v231\n\t"
"v_mfma_f32_16x16x32_bf16 a[52:55], a[220:223], a[180:183], a[52:55]\n\t"
"v_cvt_pk_bf16_f32 v234, v234, v235\n\t"
"v_pk_fma_f32 v[236:237], v[238:239], s[56:57], v[236:237]\n\t"
"v_pk_fma_f32 v[238:239], v[236:237], s[58:59], v[238:239] neg_lo:[0,0,1] neg_hi:[0,0,1]\n\t"
"v_pk_fma_f32 v[98:99], v[16:17], s[60:61], v[236:237]\n\t"
"v_pk_fma_f32 v[16:17], v[236:237], s[60:61], v[16:17]\n\t"
"v_pk_fma_f32 v[236:237], v[116:117], s[62:63], v[238:239]\n\t"
"v_pk_fma_f32 v[116:117], v[238:239], s[62:63], v[116:117]\n\t"
"buffer_load_dwordx4 a[236:239], v252, s[8:11], s20 offen offset:1024 sc1\n\t"
"v_accvgpr_write_b32 a1, v230\n\t"
"v_mfma_f32_16x16x32_bf16 a[60:63], a[240:243], a[176:179], a[60:63]\n\t"
"v_accvgpr_write_b32 a5, v234\n\t"
"v_accvgpr_read_b32 v224, a10\n\t"
"v_mfma_f32_16x16x32_bf16 a[60:63], a[244:247], a[180:183], a[60:63]\n\t"
"v_accvgpr_read_b32 v226, a14\n\t"
"v_accvgpr_read_b32 v225, a11\n\t"
"v_mfma_f32_16x16x32_bf16 a[60:63], a[220:223], a[184:187], a[60:63]\n\t"
"v_accvgpr_read_b32 v227, a15\n\t"
"v_accvgpr_read_b32 v230, a16\n\t"
"v_accvgpr_read_b32 v232, a20\n\t"
"v_accvgpr_read_b32 v231, a17\n\t"
"v_accvgpr_read_b32 v233, a21\n\t"
"v_cvt_pk_bf16_f32 v236, v236, v237\n\t"
"v_cvt_pk_bf16_f32 v98, v98, v99\n\t"
"v_pk_fma_f32 v[224:225], v[226:227], s[56:57], v[224:225]\n\t"
"v_pk_fma_f32 v[226:227], v[224:225], s[58:59], v[226:227] neg_lo:[0,0,1] neg_hi:[0,0,1]\n\t"
"v_pk_fma_f32 v[228:229], v[18:19], s[60:61], v[224:225]\n\t"
"v_pk_fma_f32 v[18:19], v[224:225], s[60:61], v[18:19]\n\t"
"v_pk_fma_f32 v[224:225], v[118:119], s[62:63], v[226:227]\n\t"
"v_pk_fma_f32 v[118:119], v[226:227], s[62:63], v[118:119]\n\t"
"v_pk_fma_f32 v[230:231], v[232:233], s[56:57], v[230:231]\n\t"
"v_pk_fma_f32 v[232:233], v[230:231], s[58:59], v[232:233] neg_lo:[0,0,1] neg_hi:[0,0,1]\n\t"
"v_pk_fma_f32 v[234:235], v[20:21], s[60:61], v[230:231]\n\t"
"v_pk_fma_f32 v[20:21], v[230:231], s[60:61], v[20:21]\n\t"
"v_pk_fma_f32 v[230:231], v[120:121], s[62:63], v[232:233]\n\t"
"v_pk_fma_f32 v[120:121], v[232:233], s[62:63], v[120:121]\n\t"
"v_mfma_f32_16x16x32_bf16 a[68:71], a[240:243], a[180:183], a[68:71]\n\t"
"v_accvgpr_write_b32 a8, v236\n\t"
"v_accvgpr_write_b32 a12, v98\n\t"
"v_mfma_f32_16x16x32_bf16 a[68:71], a[244:247], a[184:187], a[68:71]\n\t"
"v_accvgpr_read_b32 v236, a18\n\t"
"v_accvgpr_read_b32 v238, a22\n\t"
"v_mfma_f32_16x16x32_bf16 a[68:71], a[220:223], a[188:191], a[68:71]\n\t"
"v_accvgpr_read_b32 v237, a19\n\t"
"v_accvgpr_read_b32 v239, a23\n\t"
"v_cvt_pk_bf16_f32 v224, v224, v225\n\t"
"v_cvt_pk_bf16_f32 v228, v228, v229\n\t"
"v_cvt_pk_bf16_f32 v230, v230, v231\n\t"
"v_cvt_pk_bf16_f32 v234, v234, v235\n\t"
"v_pk_fma_f32 v[236:237], v[238:239], s[56:57], v[236:237]\n\t"
"v_pk_fma_f32 v[238:239], v[236:237], s[58:59], v[238:239] neg_lo:[0,0,1] neg_hi:[0,0,1]\n\t"
"v_pk_fma_f32 v[98:99], v[22:23], s[60:61], v[236:237]\n\t"
"v_pk_fma_f32 v[22:23], v[236:237], s[60:61], v[22:23]\n\t"
"v_pk_fma_f32 v[236:237], v[122:123], s[62:63], v[238:239]\n\t"
"v_pk_fma_f32 v[122:123], v[238:239], s[62:63], v[122:123]\n\t"
"v_mfma_f32_16x16x32_bf16 a[76:79], a[240:243], a[184:187], a[76:79]\n\t"
"v_accvgpr_write_b32 a9, v224\n\t"
"v_accvgpr_write_b32 a13, v228\n\t"
"v_mfma_f32_16x16x32_bf16 a[76:79], a[244:247], a[188:191], a[76:79]\n\t"
"v_accvgpr_write_b32 a16, v230\n\t"
"v_accvgpr_write_b32 a20, v234\n\t"
"v_mfma_f32_16x16x32_bf16 a[76:79], a[220:223], a[192:195], a[76:79]\n\t"
"v_accvgpr_read_b32 v224, a24\n\t"
"v_accvgpr_read_b32 v226, a28\n\t"
"v_accvgpr_read_b32 v225, a25\n\t"
"v_accvgpr_read_b32 v227, a29\n\t"
"v_cvt_pk_bf16_f32 v236, v236, v237\n\t"
"v_cvt_pk_bf16_f32 v98, v98, v99\n\t"
"v_pk_fma_f32 v[224:225], v[226:227], s[56:57], v[224:225]\n\t"
"v_pk_fma_f32 v[226:227], v[224:225], s[58:59], v[226:227] neg_lo:[0,0,1] neg_hi:[0,0,1]\n\t"
"v_pk_fma_f32 v[228:229], v[24:25], s[60:61], v[224:225]\n\t"
"v_pk_fma_f32 v[24:25], v[224:225], s[60:61], v[24:25]\n\t"
"v_pk_fma_f32 v[224:225], v[124:125], s[62:63], v[226:227]\n\t"
"v_pk_fma_f32 v[124:125], v[226:227], s[62:63], v[124:125]\n\t"
"v_mfma_f32_16x16x32_bf16 a[84:87], a[240:243], a[188:191], a[84:87]\n\t"
"v_accvgpr_write_b32 a17, v236\n\t"
"v_accvgpr_write_b32 a21, v98\n\t"
"v_mfma_f32_16x16x32_bf16 a[84:87], a[244:247], a[192:195], a[84:87]\n\t"
"v_accvgpr_read_b32 v230, a26\n\t"
"v_accvgpr_read_b32 v232, a30\n\t"
"v_mfma_f32_16x16x32_bf16 a[84:87], a[220:223], a[196:199], a[84:87]\n\t"
"v_accvgpr_read_b32 v231, a27\n\t"
"v_accvgpr_read_b32 v233, a31\n\t"
"v_cvt_pk_bf16_f32 v224, v224, v225\n\t"
"v_cvt_pk_bf16_f32 v228, v228, v229\n\t"
"v_pk_fma_f32 v[230:231], v[232:233], s[56:57], v[230:231]\n\t"
"v_pk_fma_f32 v[232:233], v[230:231], s[58:59], v[232:233] neg_lo:[0,0,1] neg_hi:[0,0,1]\n\t"
"v_pk_fma_f32 v[234:235], v[26:27], s[60:61], v[230:231]\n\t"
"v_pk_fma_f32 v[26:27], v[230:231], s[60:61], v[26:27]\n\t"
"v_pk_fma_f32 v[230:231], v[126:127], s[62:63], v[232:233]\n\t"
"v_pk_fma_f32 v[126:127], v[232:233], s[62:63], v[126:127]\n\t"
"v_mfma_f32_16x16x32_bf16 a[92:95], a[240:243], a[192:195], a[92:95]\n\t"
"v_accvgpr_write_b32 a24, v224\n\t"
"v_accvgpr_write_b32 a28, v228\n\t"
"v_mfma_f32_16x16x32_bf16 a[92:95], a[244:247], a[196:199], a[92:95]\n\t"
"v_accvgpr_read_b32 v236, a32\n\t"
"v_accvgpr_read_b32 v238, a36\n\t"
"v_mfma_f32_16x16x32_bf16 a[92:95], a[220:223], a[200:203], a[92:95]\n\t"
"v_accvgpr_read_b32 v237, a33\n\t"
"v_accvgpr_read_b32 v239, a37\n\t"
"v_cvt_pk_bf16_f32 v230, v230, v231\n\t"
"v_cvt_pk_bf16_f32 v234, v234, v235\n\t"
"v_pk_fma_f32 v[236:237], v[238:239], s[56:57], v[236:237]\n\t"
"v_pk_fma_f32 v[238:239], v[236:237], s[58:59], v[238:239] neg_lo:[0,0,1] neg_hi:[0,0,1]\n\t"
"v_pk_fma_f32 v[98:99], v[28:29], s[60:61], v[236:237]\n\t"
"v_pk_fma_f32 v[28:29], v[236:237], s[60:61], v[28:29]\n\t"
"v_pk_fma_f32 v[236:237], v[128:129], s[62:63], v[238:239]\n\t"
"v_pk_fma_f32 v[128:129], v[238:239], s[62:63], v[128:129]\n\t"
"v_mfma_f32_16x16x32_bf16 a[100:103], a[240:243], a[196:199], a[100:103]\n\t"
"v_accvgpr_write_b32 a25, v230\n\t"
"v_accvgpr_write_b32 a29, v234\n\t"
"v_mfma_f32_16x16x32_bf16 a[100:103], a[244:247], a[200:203], a[100:103]\n\t"
"v_accvgpr_read_b32 v224, a34\n\t"
"v_accvgpr_read_b32 v226, a38\n\t"
"v_mfma_f32_16x16x32_bf16 a[100:103], a[220:223], a[204:207], a[100:103]\n\t"
"v_accvgpr_read_b32 v225, a35\n\t"
"v_accvgpr_read_b32 v227, a39\n\t"
"v_cvt_pk_bf16_f32 v236, v236, v237\n\t"
"v_cvt_pk_bf16_f32 v98, v98, v99\n\t"
"v_pk_fma_f32 v[224:225], v[226:227], s[56:57], v[224:225]\n\t"
"v_pk_fma_f32 v[226:227], v[224:225], s[58:59], v[226:227] neg_lo:[0,0,1] neg_hi:[0,0,1]\n\t"
"v_pk_fma_f32 v[228:229], v[30:31], s[60:61], v[224:225]\n\t"
"v_pk_fma_f32 v[30:31], v[224:225], s[60:61], v[30:31]\n\t"
"v_pk_fma_f32 v[224:225], v[130:131], s[62:63], v[226:227]\n\t"
"v_pk_fma_f32 v[130:131], v[226:227], s[62:63], v[130:131]\n\t"
"s_waitcnt vmcnt(8)\n\t"
"v_mfma_f32_16x16x32_bf16 a[108:111], a[248:251], a[168:171], a[108:111]\n\t"
"v_accvgpr_write_b32 a32, v236\n\t"
"v_accvgpr_write_b32 a36, v98\n\t"
"v_mfma_f32_16x16x32_bf16 a[108:111], v[240:243], a[172:175], a[108:111]\n\t"
"v_accvgpr_read_b32 v230, a40\n\t"
"v_accvgpr_read_b32 v232, a44\n\t"
"v_mfma_f32_16x16x32_bf16 a[108:111], a[252:255], a[176:179], a[108:111]\n\t"
"buffer_load_dwordx4 a[240:243], v252, s[8:11], s20 offen offset:2048 sc1\n\t"
"v_accvgpr_read_b32 v231, a41\n\t"
"v_accvgpr_read_b32 v233, a45\n\t"
"v_cvt_pk_bf16_f32 v224, v224, v225\n\t"
"v_cvt_pk_bf16_f32 v228, v228, v229\n\t"
"v_pk_fma_f32 v[230:231], v[232:233], s[56:57], v[230:231]\n\t"
"v_pk_fma_f32 v[232:233], v[230:231], s[58:59], v[232:233] neg_lo:[0,0,1] neg_hi:[0,0,1]\n\t"
"v_pk_fma_f32 v[234:235], v[32:33], s[60:61], v[230:231]\n\t"
"v_pk_fma_f32 v[32:33], v[230:231], s[60:61], v[32:33]\n\t"
"v_pk_fma_f32 v[230:231], v[132:133], s[62:63], v[232:233]\n\t"
"v_pk_fma_f32 v[132:133], v[232:233], s[62:63], v[132:133]\n\t"
"v_mfma_f32_16x16x32_bf16 a[116:119], a[248:251], a[172:175], a[116:119]\n\t"
"v_accvgpr_write_b32 a33, v224\n\t"
"v_accvgpr_write_b32 a37, v228\n\t"
"v_mfma_f32_16x16x32_bf16 a[116:119], v[240:243], a[176:179], a[116:119]\n\t"
"v_accvgpr_read_b32 v236, a42\n\t"
"v_accvgpr_read_b32 v238, a46\n\t"
"v_mfma_f32_16x16x32_bf16 a[116:119], a[252:255], a[180:183], a[116:119]\n\t"
"s_mov_b32 s20, 626688\n\t"
"buffer_load_dwordx4 a[220:223], v252, s[8:11], s20 offen offset:0 sc1\n\t"
"v_accvgpr_read_b32 v237, a43\n\t"
"v_accvgpr_read_b32 v239, a47\n\t"
"v_pk_fma_f32 v[236:237], v[238:239], s[56:57], v[236:237]\n\t"
"v_pk_fma_f32 v[238:239], v[236:237], s[58:59], v[238:239] neg_lo:[0,0,1] neg_hi:[0,0,1]\n\t"
"v_pk_fma_f32 v[98:99], v[34:35], s[60:61], v[236:237]\n\t"
"v_pk_fma_f32 v[34:35], v[236:237], s[60:61], v[34:35]\n\t"
"v_pk_fma_f32 v[236:237], v[134:135], s[62:63], v[238:239]\n\t"
"v_pk_fma_f32 v[134:135], v[238:239], s[62:63], v[134:135]\n\t"
"v_mfma_f32_16x16x32_bf16 a[124:127], a[248:251], a[176:179], a[124:127]\n\t"
"v_cvt_pk_bf16_f32 v198, v230, v231\n\t"
"v_cvt_pk_bf16_f32 v202, v234, v235\n\t"
"v_mfma_f32_16x16x32_bf16 a[124:127], v[240:243], a[180:183], a[124:127]\n\t"
"v_accvgpr_read_b32 v224, a48\n\t"
"v_accvgpr_read_b32 v226, a52\n\t"
"v_mfma_f32_16x16x32_bf16 a[124:127], a[252:255], a[184:187], a[124:127]\n\t"
"buffer_load_dwordx4 a[244:247], v252, s[8:11], s20 offen offset:1024 sc1\n\t"
"v_accvgpr_read_b32 v225, a49\n\t"
"v_accvgpr_read_b32 v227, a53\n\t"
"v_accvgpr_read_b32 v230, a50\n\t"
"v_accvgpr_read_b32 v232, a54\n\t"
"v_accvgpr_read_b32 v231, a51\n\t"
"v_accvgpr_read_b32 v233, a55\n\t"
"v_pk_fma_f32 v[224:225], v[226:227], s[56:57], v[224:225]\n\t"
"v_pk_fma_f32 v[226:227], v[224:225], s[58:59], v[226:227] neg_lo:[0,0,1] neg_hi:[0,0,1]\n\t"
"v_pk_fma_f32 v[228:229], v[36:37], s[60:61], v[224:225]\n\t"
"v_pk_fma_f32 v[36:37], v[224:225], s[60:61], v[36:37]\n\t"
"v_pk_fma_f32 v[224:225], v[136:137], s[62:63], v[226:227]\n\t"
"v_pk_fma_f32 v[136:137], v[226:227], s[62:63], v[136:137]\n\t"
"v_pk_fma_f32 v[230:231], v[232:233], s[56:57], v[230:231]\n\t"
"v_pk_fma_f32 v[232:233], v[230:231], s[58:59], v[232:233] neg_lo:[0,0,1] neg_hi:[0,0,1]\n\t"
"v_pk_fma_f32 v[234:235], v[38:39], s[60:61], v[230:231]\n\t"
"v_pk_fma_f32 v[38:39], v[230:231], s[60:61], v[38:39]\n\t"
"v_pk_fma_f32 v[230:231], v[138:139], s[62:63], v[232:233]\n\t"
"v_pk_fma_f32 v[138:139], v[232:233], s[62:63], v[138:139]\n\t"
"v_mfma_f32_16x16x32_bf16 a[132:135], a[248:251], a[180:183], a[132:135]\n\t"
"v_cvt_pk_bf16_f32 v199, v236, v237\n\t"
"v_cvt_pk_bf16_f32 v203, v98, v99\n\t"
"v_mfma_f32_16x16x32_bf16 a[132:135], v[240:243], a[184:187], a[132:135]\n\t"
"v_accvgpr_read_b32 v236, a56\n\t"
"v_accvgpr_read_b32 v238, a60\n\t"
"v_mfma_f32_16x16x32_bf16 a[132:135], a[252:255], a[188:191], a[132:135]\n\t"
"v_accvgpr_read_b32 v237, a57\n\t"
"v_accvgpr_read_b32 v239, a61\n\t"
"ds_read_b128 a[168:171], v244 offset:18432\n\t"
"ds_read_b128 a[172:175], v244 offset:18944\n\t"
"ds_read_b128 a[176:179], v244 offset:19456\n\t"
"buffer_load_dwordx4 a[40:43], v252, s[8:11], s20 offen offset:2048 sc1\n\t"
"v_pk_fma_f32 v[236:237], v[238:239], s[56:57], v[236:237]\n\t"
"v_pk_fma_f32 v[238:239], v[236:237], s[58:59], v[238:239] neg_lo:[0,0,1] neg_hi:[0,0,1]\n\t"
"v_pk_fma_f32 v[98:99], v[40:41], s[60:61], v[236:237]\n\t"
"v_pk_fma_f32 v[40:41], v[236:237], s[60:61], v[40:41]\n\t"
"v_pk_fma_f32 v[236:237], v[140:141], s[62:63], v[238:239]\n\t"
"v_pk_fma_f32 v[140:141], v[238:239], s[62:63], v[140:141]\n\t"
"v_mfma_f32_16x16x32_bf16 a[140:143], a[248:251], a[184:187], a[140:143]\n\t"
"v_cvt_pk_bf16_f32 v206, v224, v225\n\t"
"v_cvt_pk_bf16_f32 v210, v228, v229\n\t"
"v_mfma_f32_16x16x32_bf16 a[140:143], v[240:243], a[188:191], a[140:143]\n\t"
"v_cvt_pk_bf16_f32 v207, v230, v231\n\t"
"v_cvt_pk_bf16_f32 v211, v234, v235\n\t"
"v_mfma_f32_16x16x32_bf16 a[140:143], a[252:255], a[192:195], a[140:143]\n\t"
"v_accvgpr_read_b32 v224, a58\n\t"
"v_accvgpr_read_b32 v226, a62\n\t"
"v_accvgpr_read_b32 v225, a59\n\t"
"v_accvgpr_read_b32 v227, a63\n\t"
"ds_read_b128 a[180:183], v244 offset:19968\n\t"
"s_mov_b32 s20, 638976\n\t"
"buffer_load_dwordx4 a[48:51], v252, s[8:11], s20 offen offset:0 sc1\n\t"
"v_pk_fma_f32 v[224:225], v[226:227], s[56:57], v[224:225]\n\t"
"v_pk_fma_f32 v[226:227], v[224:225], s[58:59], v[226:227] neg_lo:[0,0,1] neg_hi:[0,0,1]\n\t"
"v_pk_fma_f32 v[228:229], v[42:43], s[60:61], v[224:225]\n\t"
"v_pk_fma_f32 v[42:43], v[224:225], s[60:61], v[42:43]\n\t"
"v_pk_fma_f32 v[224:225], v[142:143], s[62:63], v[226:227]\n\t"
"v_pk_fma_f32 v[142:143], v[226:227], s[62:63], v[142:143]\n\t"
"v_mfma_f32_16x16x32_bf16 a[148:151], a[248:251], a[188:191], a[148:151]\n\t"
"v_cvt_pk_bf16_f32 v214, v236, v237\n\t"
"v_cvt_pk_bf16_f32 v218, v98, v99\n\t"
"v_mfma_f32_16x16x32_bf16 a[148:151], v[240:243], a[192:195], a[148:151]\n\t"
"v_accvgpr_read_b32 v230, a64\n\t"
"v_accvgpr_read_b32 v232, a68\n\t"
"v_mfma_f32_16x16x32_bf16 a[148:151], a[252:255], a[196:199], a[148:151]\n\t"
"v_accvgpr_read_b32 v231, a65\n\t"
"v_accvgpr_read_b32 v233, a69\n\t"
"ds_read_b128 a[184:187], v244 offset:20480\n\t"
"buffer_load_dwordx4 a[44:47], v252, s[8:11], s20 offen offset:1024 sc1\n\t"
"v_pk_fma_f32 v[230:231], v[232:233], s[56:57], v[230:231]\n\t"
"v_pk_fma_f32 v[232:233], v[230:231], s[58:59], v[232:233] neg_lo:[0,0,1] neg_hi:[0,0,1]\n\t"
"v_pk_fma_f32 v[234:235], v[44:45], s[60:61], v[230:231]\n\t"
"v_pk_fma_f32 v[44:45], v[230:231], s[60:61], v[44:45]\n\t"
"v_pk_fma_f32 v[230:231], v[144:145], s[62:63], v[232:233]\n\t"
"v_pk_fma_f32 v[144:145], v[232:233], s[62:63], v[144:145]\n\t"
"v_mfma_f32_16x16x32_bf16 a[156:159], a[248:251], a[192:195], a[156:159]\n\t"
"v_cvt_pk_bf16_f32 v215, v224, v225\n\t"
"v_cvt_pk_bf16_f32 v219, v228, v229\n\t"
"v_mfma_f32_16x16x32_bf16 a[156:159], v[240:243], a[196:199], a[156:159]\n\t"
"v_accvgpr_read_b32 v236, a66\n\t"
"v_accvgpr_read_b32 v238, a70\n\t"
"v_mfma_f32_16x16x32_bf16 a[156:159], a[252:255], a[200:203], a[156:159]\n\t"
"v_accvgpr_read_b32 v237, a67\n\t"
"v_accvgpr_read_b32 v239, a71\n\t"
"ds_read_b128 a[188:191], v244 offset:20992\n\t"
"buffer_load_dwordx4 a[56:59], v252, s[8:11], s20 offen offset:2048 sc1\n\t"
"v_cvt_pk_bf16_f32 v230, v230, v231\n\t"
"v_cvt_pk_bf16_f32 v234, v234, v235\n\t"
"v_pk_fma_f32 v[236:237], v[238:239], s[56:57], v[236:237]\n\t"
"v_pk_fma_f32 v[238:239], v[236:237], s[58:59], v[238:239] neg_lo:[0,0,1] neg_hi:[0,0,1]\n\t"
"v_pk_fma_f32 v[98:99], v[46:47], s[60:61], v[236:237]\n\t"
"v_pk_fma_f32 v[46:47], v[236:237], s[60:61], v[46:47]\n\t"
"v_pk_fma_f32 v[236:237], v[146:147], s[62:63], v[238:239]\n\t"
"v_pk_fma_f32 v[146:147], v[238:239], s[62:63], v[146:147]\n\t"
"v_mfma_f32_16x16x32_bf16 a[164:167], a[248:251], a[196:199], a[164:167]\n\t"
"v_accvgpr_write_b32 a2, v230\n\t"
"v_accvgpr_write_b32 a6, v234\n\t"
"v_mfma_f32_16x16x32_bf16 a[164:167], v[240:243], a[200:203], a[164:167]\n\t"
"v_accvgpr_read_b32 v224, a72\n\t"
"v_accvgpr_read_b32 v226, a76\n\t"
"v_mfma_f32_16x16x32_bf16 a[164:167], a[252:255], a[204:207], a[164:167]\n\t"
"v_accvgpr_read_b32 v225, a73\n\t"
"v_accvgpr_read_b32 v227, a77\n\t"
"ds_read_b128 a[192:195], v244 offset:21504\n\t"
"s_mov_b32 s20, 651264\n\t"
"buffer_load_dwordx4 a[60:63], v252, s[8:11], s20 offen offset:0 sc1\n\t"
"v_cvt_pk_bf16_f32 v236, v236, v237\n\t"
"v_cvt_pk_bf16_f32 v98, v98, v99\n\t"
"v_pk_fma_f32 v[224:225], v[226:227], s[56:57], v[224:225]\n\t"
"v_pk_fma_f32 v[226:227], v[224:225], s[58:59], v[226:227] neg_lo:[0,0,1] neg_hi:[0,0,1]\n\t"
"v_pk_fma_f32 v[228:229], v[48:49], s[60:61], v[224:225]\n\t"
"v_pk_fma_f32 v[48:49], v[224:225], s[60:61], v[48:49]\n\t"
"v_pk_fma_f32 v[224:225], v[148:149], s[62:63], v[226:227]\n\t"
"v_pk_fma_f32 v[148:149], v[226:227], s[62:63], v[148:149]\n\t"
"Lst14_%=:\n\t"
"s_waitcnt vmcnt(13) lgkmcnt(4)\n\t"
"v_mfma_f32_16x16x32_bf16 v[0:3], a[224:227], a[168:171], v[0:3]\n\t"
"v_accvgpr_write_b32 a3, v236\n\t"
"v_accvgpr_write_b32 a7, v98\n\t"
"v_mfma_f32_16x16x32_bf16 v[0:3], v[248:251], a[172:175], v[0:3]\n\t"
"v_accvgpr_read_b32 v230, a74\n\t"
"v_accvgpr_read_b32 v232, a78\n\t"
"v_mfma_f32_16x16x32_bf16 v[0:3], a[208:211], a[176:179], v[0:3]\n\t"
"v_accvgpr_read_b32 v231, a75\n\t"
"v_accvgpr_read_b32 v233, a79\n\t"
"ds_read_b128 a[196:199], v244 offset:22016\n\t"
"buffer_load_dwordx4 a[64:67], v252, s[8:11], s20 offen offset:1024 sc1\n\t"
"v_cvt_pk_bf16_f32 v224, v224, v225\n\t"
"v_cvt_pk_bf16_f32 v228, v228, v229\n\t"
"v_pk_fma_f32 v[230:231], v[232:233], s[56:57], v[230:231]\n\t"
"v_pk_fma_f32 v[232:233], v[230:231], s[58:59], v[232:233] neg_lo:[0,0,1] neg_hi:[0,0,1]\n\t"
"v_pk_fma_f32 v[234:235], v[50:51], s[60:61], v[230:231]\n\t"
"v_pk_fma_f32 v[50:51], v[230:231], s[60:61], v[50:51]\n\t"
"v_pk_fma_f32 v[230:231], v[150:151], s[62:63], v[232:233]\n\t"
"v_pk_fma_f32 v[150:151], v[232:233], s[62:63], v[150:151]\n\t"
"s_waitcnt lgkmcnt(4)\n\t"
"v_mfma_f32_16x16x32_bf16 v[4:7], a[224:227], a[172:175], v[4:7]\n\t"
"v_accvgpr_write_b32 a10, v224\n\t"
"v_accvgpr_write_b32 a14, v228\n\t"
"v_mfma_f32_16x16x32_bf16 v[4:7], v[248:251], a[176:179], v[4:7]\n\t"
"v_accvgpr_read_b32 v236, a80\n\t"
"v_accvgpr_read_b32 v238, a84\n\t"
"v_mfma_f32_16x16x32_bf16 v[4:7], a[208:211], a[180:183], v[4:7]\n\t"
"v_accvgpr_read_b32 v237, a81\n\t"
"v_accvgpr_read_b32 v239, a85\n\t"
"ds_read_b128 a[200:203], v244 offset:22528\n\t"
"buffer_load_dwordx4 a[52:55], v252, s[8:11], s20 offen offset:2048 sc1\n\t"
"v_cvt_pk_bf16_f32 v230, v230, v231\n\t"
"v_cvt_pk_bf16_f32 v234, v234, v235\n\t"
"v_pk_fma_f32 v[236:237], v[238:239], s[56:57], v[236:237]\n\t"
"v_pk_fma_f32 v[238:239], v[236:237], s[58:59], v[238:239] neg_lo:[0,0,1] neg_hi:[0,0,1]\n\t"
"v_pk_fma_f32 v[98:99], v[52:53], s[60:61], v[236:237]\n\t"
"v_pk_fma_f32 v[52:53], v[236:237], s[60:61], v[52:53]\n\t"
"v_pk_fma_f32 v[236:237], v[152:153], s[62:63], v[238:239]\n\t"
"v_pk_fma_f32 v[152:153], v[238:239], s[62:63], v[152:153]\n\t"
"s_waitcnt lgkmcnt(4)\n\t"
"v_mfma_f32_16x16x32_bf16 v[8:11], a[224:227], a[176:179], v[8:11]\n\t"
"v_accvgpr_write_b32 a11, v230\n\t"
"v_accvgpr_write_b32 a15, v234\n\t"
"v_mfma_f32_16x16x32_bf16 v[8:11], v[248:251], a[180:183], v[8:11]\n\t"
"v_accvgpr_read_b32 v224, a82\n\t"
"v_accvgpr_read_b32 v226, a86\n\t"
"v_mfma_f32_16x16x32_bf16 v[8:11], a[208:211], a[184:187], v[8:11]\n\t"
"v_accvgpr_read_b32 v225, a83\n\t"
"v_accvgpr_read_b32 v227, a87\n\t"
"ds_read_b128 a[204:207], v244 offset:23040\n\t"
"s_mov_b32 s20, 663552\n\t"
"buffer_load_dwordx4 a[72:75], v252, s[8:11], s20 offen offset:0 sc1\n\t"
"v_cvt_pk_bf16_f32 v236, v236, v237\n\t"
"v_cvt_pk_bf16_f32 v98, v98, v99\n\t"
"v_pk_fma_f32 v[224:225], v[226:227], s[56:57], v[224:225]\n\t"
"v_pk_fma_f32 v[226:227], v[224:225], s[58:59], v[226:227] neg_lo:[0,0,1] neg_hi:[0,0,1]\n\t"
"v_pk_fma_f32 v[228:229], v[54:55], s[60:61], v[224:225]\n\t"
"v_pk_fma_f32 v[54:55], v[224:225], s[60:61], v[54:55]\n\t"
"v_pk_fma_f32 v[224:225], v[154:155], s[62:63], v[226:227]\n\t"
"v_pk_fma_f32 v[154:155], v[226:227], s[62:63], v[154:155]\n\t"
"s_waitcnt lgkmcnt(4)\n\t"
"v_mfma_f32_16x16x32_bf16 v[12:15], a[224:227], a[180:183], v[12:15]\n\t"
"v_accvgpr_write_b32 a18, v236\n\t"
"v_accvgpr_write_b32 a22, v98\n\t"
"v_mfma_f32_16x16x32_bf16 v[12:15], v[248:251], a[184:187], v[12:15]\n\t"
"v_accvgpr_read_b32 v230, a88\n\t"
"v_accvgpr_read_b32 v232, a92\n\t"
"v_mfma_f32_16x16x32_bf16 v[12:15], a[208:211], a[188:191], v[12:15]\n\t"
"v_accvgpr_read_b32 v231, a89\n\t"
"v_accvgpr_read_b32 v233, a93\n\t"
"v_accvgpr_read_b32 v236, a90\n\t"
"v_accvgpr_read_b32 v238, a94\n\t"
"v_accvgpr_read_b32 v237, a91\n\t"
"v_accvgpr_read_b32 v239, a95\n\t"
"buffer_load_dwordx4 a[68:71], v252, s[8:11], s20 offen offset:1024 sc1\n\t"
"v_cvt_pk_bf16_f32 v224, v224, v225\n\t"
"v_cvt_pk_bf16_f32 v228, v228, v229\n\t"
"v_pk_fma_f32 v[230:231], v[232:233], s[56:57], v[230:231]\n\t"
"v_pk_fma_f32 v[232:233], v[230:231], s[58:59], v[232:233] neg_lo:[0,0,1] neg_hi:[0,0,1]\n\t"
"v_pk_fma_f32 v[234:235], v[56:57], s[60:61], v[230:231]\n\t"
"v_pk_fma_f32 v[56:57], v[230:231], s[60:61], v[56:57]\n\t"
"v_pk_fma_f32 v[230:231], v[156:157], s[62:63], v[232:233]\n\t"
"v_pk_fma_f32 v[156:157], v[232:233], s[62:63], v[156:157]\n\t"
"v_pk_fma_f32 v[236:237], v[238:239], s[56:57], v[236:237]\n\t"
"v_pk_fma_f32 v[238:239], v[236:237], s[58:59], v[238:239] neg_lo:[0,0,1] neg_hi:[0,0,1]\n\t"
"v_pk_fma_f32 v[98:99], v[58:59], s[60:61], v[236:237]\n\t"
"v_pk_fma_f32 v[58:59], v[236:237], s[60:61], v[58:59]\n\t"
"v_pk_fma_f32 v[236:237], v[158:159], s[62:63], v[238:239]\n\t"
"v_pk_fma_f32 v[158:159], v[238:239], s[62:63], v[158:159]\n\t"
"s_waitcnt lgkmcnt(3)\n\t"
"v_mfma_f32_16x16x32_bf16 v[16:19], a[224:227], a[184:187], v[16:19]\n\t"
"v_accvgpr_write_b32 a19, v224\n\t"
"v_accvgpr_write_b32 a23, v228\n\t"
"v_mfma_f32_16x16x32_bf16 v[16:19], v[248:251], a[188:191], v[16:19]\n\t"
"v_accvgpr_read_b32 v224, a96\n\t"
"v_accvgpr_read_b32 v226, a100\n\t"
"v_mfma_f32_16x16x32_bf16 v[16:19], a[208:211], a[192:195], v[16:19]\n\t"
"v_accvgpr_read_b32 v225, a97\n\t"
"v_accvgpr_read_b32 v227, a101\n\t"
"buffer_load_dwordx4 a[80:83], v252, s[8:11], s20 offen offset:2048 sc1\n\t"
"v_cvt_pk_bf16_f32 v230, v230, v231\n\t"
"v_cvt_pk_bf16_f32 v234, v234, v235\n\t"
"v_pk_fma_f32 v[224:225], v[226:227], s[56:57], v[224:225]\n\t"
"v_pk_fma_f32 v[226:227], v[224:225], s[58:59], v[226:227] neg_lo:[0,0,1] neg_hi:[0,0,1]\n\t"
"v_pk_fma_f32 v[228:229], v[60:61], s[60:61], v[224:225]\n\t"
"v_pk_fma_f32 v[60:61], v[224:225], s[60:61], v[60:61]\n\t"
"v_pk_fma_f32 v[224:225], v[160:161], s[62:63], v[226:227]\n\t"
"v_pk_fma_f32 v[160:161], v[226:227], s[62:63], v[160:161]\n\t"
"s_waitcnt lgkmcnt(2)\n\t"
"v_mfma_f32_16x16x32_bf16 v[20:23], a[224:227], a[188:191], v[20:23]\n\t"
"v_accvgpr_write_b32 a26, v230\n\t"
"v_cvt_pk_bf16_f32 v236, v236, v237\n\t"
"v_mfma_f32_16x16x32_bf16 v[20:23], v[248:251], a[192:195], v[20:23]\n\t"
"v_accvgpr_write_b32 a30, v234\n\t"
"v_cvt_pk_bf16_f32 v98, v98, v99\n\t"
"v_mfma_f32_16x16x32_bf16 v[20:23], a[208:211], a[196:199], v[20:23]\n\t"
"v_accvgpr_write_b32 a27, v236\n\t"
"v_accvgpr_write_b32 a31, v98\n\t"
"v_accvgpr_read_b32 v230, a98\n\t"
"v_accvgpr_read_b32 v232, a102\n\t"
"v_accvgpr_read_b32 v231, a99\n\t"
"v_accvgpr_read_b32 v233, a103\n\t"
"s_mov_b32 s20, 675840\n\t"
"buffer_load_dwordx4 a[92:95], v252, s[8:11], s20 offen offset:0 sc1\n\t"
"v_cvt_pk_bf16_f32 v224, v224, v225\n\t"
"v_cvt_pk_bf16_f32 v228, v228, v229\n\t"
"v_pk_fma_f32 v[230:231], v[232:233], s[56:57], v[230:231]\n\t"
"v_pk_fma_f32 v[232:233], v[230:231], s[58:59], v[232:233] neg_lo:[0,0,1] neg_hi:[0,0,1]\n\t"
"v_pk_fma_f32 v[234:235], v[62:63], s[60:61], v[230:231]\n\t"
"v_pk_fma_f32 v[62:63], v[230:231], s[60:61], v[62:63]\n\t"
"v_pk_fma_f32 v[230:231], v[162:163], s[62:63], v[232:233]\n\t"
"v_pk_fma_f32 v[162:163], v[232:233], s[62:63], v[162:163]\n\t"
"s_waitcnt lgkmcnt(1)\n\t"
"v_mfma_f32_16x16x32_bf16 v[24:27], a[224:227], a[192:195], v[24:27]\n\t"
"v_accvgpr_write_b32 a34, v224\n\t"
"v_accvgpr_write_b32 a38, v228\n\t"
"v_mfma_f32_16x16x32_bf16 v[24:27], v[248:251], a[196:199], v[24:27]\n\t"
"v_accvgpr_read_b32 v236, a104\n\t"
"v_accvgpr_read_b32 v238, a108\n\t"
"v_mfma_f32_16x16x32_bf16 v[24:27], a[208:211], a[200:203], v[24:27]\n\t"
"v_accvgpr_read_b32 v237, a105\n\t"
"v_accvgpr_read_b32 v239, a109\n\t"
"buffer_load_dwordx4 a[88:91], v252, s[8:11], s20 offen offset:1024 sc1\n\t"
"v_cvt_pk_bf16_f32 v230, v230, v231\n\t"
"v_cvt_pk_bf16_f32 v234, v234, v235\n\t"
"v_pk_fma_f32 v[236:237], v[238:239], s[56:57], v[236:237]\n\t"
"v_pk_fma_f32 v[238:239], v[236:237], s[58:59], v[238:239] neg_lo:[0,0,1] neg_hi:[0,0,1]\n\t"
"v_pk_fma_f32 v[98:99], v[64:65], s[60:61], v[236:237]\n\t"
"v_pk_fma_f32 v[64:65], v[236:237], s[60:61], v[64:65]\n\t"
"v_pk_fma_f32 v[236:237], v[164:165], s[62:63], v[238:239]\n\t"
"v_pk_fma_f32 v[164:165], v[238:239], s[62:63], v[164:165]\n\t"
"s_waitcnt lgkmcnt(0)\n\t"
"v_mfma_f32_16x16x32_bf16 v[28:31], a[224:227], a[196:199], v[28:31]\n\t"
"v_accvgpr_write_b32 a35, v230\n\t"
"v_accvgpr_write_b32 a39, v234\n\t"
"v_mfma_f32_16x16x32_bf16 v[28:31], v[248:251], a[200:203], v[28:31]\n\t"
"v_accvgpr_read_b32 v224, a106\n\t"
"v_accvgpr_read_b32 v226, a110\n\t"
"v_mfma_f32_16x16x32_bf16 v[28:31], a[208:211], a[204:207], v[28:31]\n\t"
"v_accvgpr_read_b32 v225, a107\n\t"
"v_accvgpr_read_b32 v227, a111\n\t"
"buffer_load_dwordx4 a[96:99], v252, s[8:11], s20 offen offset:2048 sc1\n\t"
"v_cvt_pk_bf16_f32 v236, v236, v237\n\t"
"v_cvt_pk_bf16_f32 v98, v98, v99\n\t"
"v_pk_fma_f32 v[224:225], v[226:227], s[56:57], v[224:225]\n\t"
"v_pk_fma_f32 v[226:227], v[224:225], s[58:59], v[226:227] neg_lo:[0,0,1] neg_hi:[0,0,1]\n\t"
"v_pk_fma_f32 v[228:229], v[66:67], s[60:61], v[224:225]\n\t"
"v_pk_fma_f32 v[66:67], v[224:225], s[60:61], v[66:67]\n\t"
"v_pk_fma_f32 v[224:225], v[166:167], s[62:63], v[226:227]\n\t"
"v_pk_fma_f32 v[166:167], v[226:227], s[62:63], v[166:167]\n\t"
"s_waitcnt vmcnt(18)\n\t"
"v_mfma_f32_16x16x32_bf16 v[32:35], a[212:215], a[168:171], v[32:35]\n\t"
"v_accvgpr_write_b32 a104, v236\n\t"
"v_accvgpr_write_b32 a106, v98\n\t"
"v_mfma_f32_16x16x32_bf16 v[32:35], a[228:231], a[172:175], v[32:35]\n\t"
"v_accvgpr_read_b32 v230, a112\n\t"
"v_accvgpr_read_b32 v232, a116\n\t"
"v_mfma_f32_16x16x32_bf16 v[32:35], a[232:235], a[176:179], v[32:35]\n\t"
"v_accvgpr_read_b32 v231, a113\n\t"
"v_accvgpr_read_b32 v233, a117\n\t"
"s_mov_b32 s20, 688128\n\t"
"buffer_load_dwordx4 a[76:79], v252, s[8:11], s20 offen offset:0 sc1\n\t"
"v_cvt_pk_bf16_f32 v224, v224, v225\n\t"
"v_cvt_pk_bf16_f32 v228, v228, v229\n\t"
"v_pk_fma_f32 v[230:231], v[232:233], s[56:57], v[230:231]\n\t"
"v_pk_fma_f32 v[232:233], v[230:231], s[58:59], v[232:233] neg_lo:[0,0,1] neg_hi:[0,0,1]\n\t"
"v_pk_fma_f32 v[234:235], v[68:69], s[60:61], v[230:231]\n\t"
"v_pk_fma_f32 v[68:69], v[230:231], s[60:61], v[68:69]\n\t"
"v_pk_fma_f32 v[230:231], v[168:169], s[62:63], v[232:233]\n\t"
"v_pk_fma_f32 v[168:169], v[232:233], s[62:63], v[168:169]\n\t"
"v_mfma_f32_16x16x32_bf16 v[36:39], a[212:215], a[172:175], v[36:39]\n\t"
"v_accvgpr_write_b32 a105, v224\n\t"
"v_accvgpr_write_b32 a107, v228\n\t"
"v_mfma_f32_16x16x32_bf16 v[36:39], a[228:231], a[176:179], v[36:39]\n\t"
"v_accvgpr_read_b32 v236, a114\n\t"
"v_accvgpr_read_b32 v238, a118\n\t"
"v_mfma_f32_16x16x32_bf16 v[36:39], a[232:235], a[180:183], v[36:39]\n\t"
"v_accvgpr_read_b32 v237, a115\n\t"
"v_accvgpr_read_b32 v239, a119\n\t"
"buffer_load_dwordx4 a[108:111], v252, s[8:11], s20 offen offset:1024 sc1\n\t"
"v_cvt_pk_bf16_f32 v230, v230, v231\n\t"
"v_cvt_pk_bf16_f32 v234, v234, v235\n\t"
"v_pk_fma_f32 v[236:237], v[238:239], s[56:57], v[236:237]\n\t"
"v_pk_fma_f32 v[238:239], v[236:237], s[58:59], v[238:239] neg_lo:[0,0,1] neg_hi:[0,0,1]\n\t"
"v_pk_fma_f32 v[98:99], v[70:71], s[60:61], v[236:237]\n\t"
"v_pk_fma_f32 v[70:71], v[236:237], s[60:61], v[70:71]\n\t"
"v_pk_fma_f32 v[236:237], v[170:171], s[62:63], v[238:239]\n\t"
"v_pk_fma_f32 v[170:171], v[238:239], s[62:63], v[170:171]\n\t"
"v_mfma_f32_16x16x32_bf16 v[40:43], a[212:215], a[176:179], v[40:43]\n\t"
"v_accvgpr_write_b32 a112, v230\n\t"
"v_accvgpr_write_b32 a114, v234\n\t"
"v_mfma_f32_16x16x32_bf16 v[40:43], a[228:231], a[180:183], v[40:43]\n\t"
"v_accvgpr_read_b32 v224, a120\n\t"
"v_accvgpr_read_b32 v226, a124\n\t"
"v_mfma_f32_16x16x32_bf16 v[40:43], a[232:235], a[184:187], v[40:43]\n\t"
"v_accvgpr_read_b32 v225, a121\n\t"
"v_accvgpr_read_b32 v227, a125\n\t"
"global_load_dwordx4 v[240:243], v252, s[46:47] offset:0\n\t"
"buffer_load_dwordx4 a[84:87], v252, s[8:11], s20 offen offset:2048 sc1\n\t"
"v_cvt_pk_bf16_f32 v236, v236, v237\n\t"
"v_cvt_pk_bf16_f32 v98, v98, v99\n\t"
"v_pk_fma_f32 v[224:225], v[226:227], s[56:57], v[224:225]\n\t"
"v_pk_fma_f32 v[226:227], v[224:225], s[58:59], v[226:227] neg_lo:[0,0,1] neg_hi:[0,0,1]\n\t"
"v_pk_fma_f32 v[228:229], v[72:73], s[60:61], v[224:225]\n\t"
"v_pk_fma_f32 v[72:73], v[224:225], s[60:61], v[72:73]\n\t"
"v_pk_fma_f32 v[224:225], v[172:173], s[62:63], v[226:227]\n\t"
"v_pk_fma_f32 v[172:173], v[226:227], s[62:63], v[172:173]\n\t"
"v_mfma_f32_16x16x32_bf16 v[44:47], a[212:215], a[180:183], v[44:47]\n\t"
"v_accvgpr_write_b32 a113, v236\n\t"
"v_accvgpr_write_b32 a115, v98\n\t"
"v_mfma_f32_16x16x32_bf16 v[44:47], a[228:231], a[184:187], v[44:47]\n\t"
"v_accvgpr_read_b32 v230, a122\n\t"
"v_accvgpr_read_b32 v232, a126\n\t"
"v_mfma_f32_16x16x32_bf16 v[44:47], a[232:235], a[188:191], v[44:47]\n\t"
"v_accvgpr_read_b32 v231, a123\n\t"
"v_accvgpr_read_b32 v233, a127\n\t"
"v_accvgpr_read_b32 v236, a128\n\t"
"v_accvgpr_read_b32 v238, a132\n\t"
"v_accvgpr_read_b32 v237, a129\n\t"
"v_accvgpr_read_b32 v239, a133\n\t"
"s_mov_b32 s20, 847872\n\t"
"buffer_load_dwordx4 a[116:119], v252, s[8:11], s20 offen offset:0 sc1\n\t"
"v_cvt_pk_bf16_f32 v224, v224, v225\n\t"
"v_cvt_pk_bf16_f32 v228, v228, v229\n\t"
"v_pk_fma_f32 v[230:231], v[232:233], s[56:57], v[230:231]\n\t"
"v_pk_fma_f32 v[232:233], v[230:231], s[58:59], v[232:233] neg_lo:[0,0,1] neg_hi:[0,0,1]\n\t"
"v_pk_fma_f32 v[234:235], v[74:75], s[60:61], v[230:231]\n\t"
"v_pk_fma_f32 v[74:75], v[230:231], s[60:61], v[74:75]\n\t"
"v_pk_fma_f32 v[230:231], v[174:175], s[62:63], v[232:233]\n\t"
"v_pk_fma_f32 v[174:175], v[232:233], s[62:63], v[174:175]\n\t"
"v_pk_fma_f32 v[236:237], v[238:239], s[56:57], v[236:237]\n\t"
"v_pk_fma_f32 v[238:239], v[236:237], s[58:59], v[238:239] neg_lo:[0,0,1] neg_hi:[0,0,1]\n\t"
"v_pk_fma_f32 v[98:99], v[76:77], s[60:61], v[236:237]\n\t"
"v_pk_fma_f32 v[76:77], v[236:237], s[60:61], v[76:77]\n\t"
"v_pk_fma_f32 v[236:237], v[176:177], s[62:63], v[238:239]\n\t"
"v_pk_fma_f32 v[176:177], v[238:239], s[62:63], v[176:177]\n\t"
"global_load_dwordx4 a[208:211], v252, s[46:47] offset:1024\n\t"
"v_mfma_f32_16x16x32_bf16 v[48:51], a[212:215], a[184:187], v[48:51]\n\t"
"v_accvgpr_write_b32 a120, v224\n\t"
"v_accvgpr_write_b32 a122, v228\n\t"
"v_mfma_f32_16x16x32_bf16 v[48:51], a[228:231], a[188:191], v[48:51]\n\t"
"v_accvgpr_read_b32 v224, a130\n\t"
"v_accvgpr_read_b32 v226, a134\n\t"
"v_mfma_f32_16x16x32_bf16 v[48:51], a[232:235], a[192:195], v[48:51]\n\t"
"global_load_dwordx4 v[248:251], v252, s[48:49] offset:0\n\t"
"v_accvgpr_read_b32 v225, a131\n\t"
"v_accvgpr_read_b32 v227, a135\n\t"
"buffer_load_dwordx4 a[100:103], v252, s[8:11], s20 offen offset:1024 sc1\n\t"
"v_cvt_pk_bf16_f32 v230, v230, v231\n\t"
"v_cvt_pk_bf16_f32 v234, v234, v235\n\t"
"v_cvt_pk_bf16_f32 v236, v236, v237\n\t"
"v_cvt_pk_bf16_f32 v98, v98, v99\n\t"
"v_pk_fma_f32 v[224:225], v[226:227], s[56:57], v[224:225]\n\t"
"v_pk_fma_f32 v[226:227], v[224:225], s[58:59], v[226:227] neg_lo:[0,0,1] neg_hi:[0,0,1]\n\t"
"v_pk_fma_f32 v[228:229], v[78:79], s[60:61], v[224:225]\n\t"
"v_pk_fma_f32 v[78:79], v[224:225], s[60:61], v[78:79]\n\t"
"v_pk_fma_f32 v[224:225], v[178:179], s[62:63], v[226:227]\n\t"
"v_pk_fma_f32 v[178:179], v[226:227], s[62:63], v[178:179]\n\t"
"v_mfma_f32_16x16x32_bf16 v[52:55], a[212:215], a[188:191], v[52:55]\n\t"
"v_accvgpr_write_b32 a121, v230\n\t"
"v_accvgpr_write_b32 a123, v234\n\t"
"v_mfma_f32_16x16x32_bf16 v[52:55], a[228:231], a[192:195], v[52:55]\n\t"
"v_accvgpr_write_b32 a128, v236\n\t"
"v_accvgpr_write_b32 a130, v98\n\t"
"v_mfma_f32_16x16x32_bf16 v[52:55], a[232:235], a[196:199], v[52:55]\n\t"
"v_accvgpr_read_b32 v230, a136\n\t"
"v_accvgpr_read_b32 v232, a140\n\t"
"v_accvgpr_read_b32 v231, a137\n\t"
"v_accvgpr_read_b32 v233, a141\n\t"
"buffer_load_dwordx4 a[124:127], v252, s[8:11], s20 offen offset:2048 sc1\n\t"
"v_cvt_pk_bf16_f32 v224, v224, v225\n\t"
"v_cvt_pk_bf16_f32 v228, v228, v229\n\t"
"v_pk_fma_f32 v[230:231], v[232:233], s[56:57], v[230:231]\n\t"
"v_pk_fma_f32 v[232:233], v[230:231], s[58:59], v[232:233] neg_lo:[0,0,1] neg_hi:[0,0,1]\n\t"
"v_pk_fma_f32 v[234:235], v[80:81], s[60:61], v[230:231]\n\t"
"v_pk_fma_f32 v[80:81], v[230:231], s[60:61], v[80:81]\n\t"
"v_pk_fma_f32 v[230:231], v[180:181], s[62:63], v[232:233]\n\t"
"v_pk_fma_f32 v[180:181], v[232:233], s[62:63], v[180:181]\n\t"
"v_mfma_f32_16x16x32_bf16 v[56:59], a[212:215], a[192:195], v[56:59]\n\t"
"v_accvgpr_write_b32 a129, v224\n\t"
"v_accvgpr_write_b32 a131, v228\n\t"
"v_mfma_f32_16x16x32_bf16 v[56:59], a[228:231], a[196:199], v[56:59]\n\t"
"v_accvgpr_read_b32 v236, a138\n\t"
"v_accvgpr_read_b32 v238, a142\n\t"
"v_mfma_f32_16x16x32_bf16 v[56:59], a[232:235], a[200:203], v[56:59]\n\t"
"v_accvgpr_read_b32 v237, a139\n\t"
"v_accvgpr_read_b32 v239, a143\n\t"
"s_mov_b32 s20, 860160\n\t"
"buffer_load_dwordx4 a[132:135], v252, s[8:11], s20 offen offset:0 sc1\n\t"
"v_cvt_pk_bf16_f32 v230, v230, v231\n\t"
"v_cvt_pk_bf16_f32 v234, v234, v235\n\t"
"v_pk_fma_f32 v[236:237], v[238:239], s[56:57], v[236:237]\n\t"
"v_pk_fma_f32 v[238:239], v[236:237], s[58:59], v[238:239] neg_lo:[0,0,1] neg_hi:[0,0,1]\n\t"
"v_pk_fma_f32 v[98:99], v[82:83], s[60:61], v[236:237]\n\t"
"v_pk_fma_f32 v[82:83], v[236:237], s[60:61], v[82:83]\n\t"
"v_pk_fma_f32 v[236:237], v[182:183], s[62:63], v[238:239]\n\t"
"v_pk_fma_f32 v[182:183], v[238:239], s[62:63], v[182:183]\n\t"
"v_mfma_f32_16x16x32_bf16 v[60:63], a[212:215], a[196:199], v[60:63]\n\t"
"v_accvgpr_write_b32 a136, v230\n\t"
"v_accvgpr_write_b32 a138, v234\n\t"
"v_mfma_f32_16x16x32_bf16 v[60:63], a[228:231], a[200:203], v[60:63]\n\t"
"v_accvgpr_read_b32 v224, a144\n\t"
"v_accvgpr_read_b32 v226, a148\n\t"
"v_mfma_f32_16x16x32_bf16 v[60:63], a[232:235], a[204:207], v[60:63]\n\t"
"v_accvgpr_read_b32 v225, a145\n\t"
"v_accvgpr_read_b32 v227, a149\n\t"
"buffer_load_dwordx4 a[224:227], v252, s[8:11], s20 offen offset:1024 sc1\n\t"
"v_cvt_pk_bf16_f32 v236, v236, v237\n\t"
"v_cvt_pk_bf16_f32 v98, v98, v99\n\t"
"v_pk_fma_f32 v[224:225], v[226:227], s[56:57], v[224:225]\n\t"
"v_pk_fma_f32 v[226:227], v[224:225], s[58:59], v[226:227] neg_lo:[0,0,1] neg_hi:[0,0,1]\n\t"
"v_pk_fma_f32 v[228:229], v[84:85], s[60:61], v[224:225]\n\t"
"v_pk_fma_f32 v[84:85], v[224:225], s[60:61], v[84:85]\n\t"
"v_pk_fma_f32 v[224:225], v[184:185], s[62:63], v[226:227]\n\t"
"v_pk_fma_f32 v[184:185], v[226:227], s[62:63], v[184:185]\n\t"
"s_waitcnt vmcnt(26)\n\t"
"v_mfma_f32_16x16x32_bf16 v[64:67], a[216:219], a[168:171], v[64:67]\n\t"
"v_accvgpr_write_b32 a137, v236\n\t"
"v_accvgpr_write_b32 a139, v98\n\t"
"v_mfma_f32_16x16x32_bf16 v[64:67], a[236:239], a[172:175], v[64:67]\n\t"
"v_accvgpr_read_b32 v230, a146\n\t"
"v_accvgpr_read_b32 v232, a150\n\t"
"v_mfma_f32_16x16x32_bf16 v[64:67], a[240:243], a[176:179], v[64:67]\n\t"
"v_accvgpr_read_b32 v231, a147\n\t"
"v_accvgpr_read_b32 v233, a151\n\t"
"buffer_load_dwordx4 a[140:143], v252, s[8:11], s20 offen offset:2048 sc1\n\t"
"v_cvt_pk_bf16_f32 v224, v224, v225\n\t"
"v_cvt_pk_bf16_f32 v228, v228, v229\n\t"
"v_pk_fma_f32 v[230:231], v[232:233], s[56:57], v[230:231]\n\t"
"v_pk_fma_f32 v[232:233], v[230:231], s[58:59], v[232:233] neg_lo:[0,0,1] neg_hi:[0,0,1]\n\t"
"v_pk_fma_f32 v[234:235], v[86:87], s[60:61], v[230:231]\n\t"
"v_pk_fma_f32 v[86:87], v[230:231], s[60:61], v[86:87]\n\t"
"v_pk_fma_f32 v[230:231], v[186:187], s[62:63], v[232:233]\n\t"
"v_pk_fma_f32 v[186:187], v[232:233], s[62:63], v[186:187]\n\t"
"v_mfma_f32_16x16x32_bf16 v[68:71], a[216:219], a[172:175], v[68:71]\n\t"
"v_accvgpr_write_b32 a144, v224\n\t"
"v_accvgpr_write_b32 a146, v228\n\t"
"v_mfma_f32_16x16x32_bf16 v[68:71], a[236:239], a[176:179], v[68:71]\n\t"
"v_accvgpr_read_b32 v236, a152\n\t"
"v_accvgpr_read_b32 v238, a156\n\t"
"v_mfma_f32_16x16x32_bf16 v[68:71], a[240:243], a[180:183], v[68:71]\n\t"
"v_accvgpr_read_b32 v237, a153\n\t"
"v_accvgpr_read_b32 v239, a157\n\t"
"s_mov_b32 s20, 872448\n\t"
"buffer_load_dwordx4 a[212:215], v252, s[8:11], s20 offen offset:0 sc1\n\t"
"v_cvt_pk_bf16_f32 v230, v230, v231\n\t"
"v_cvt_pk_bf16_f32 v234, v234, v235\n\t"
"v_pk_fma_f32 v[236:237], v[238:239], s[56:57], v[236:237]\n\t"
"v_pk_fma_f32 v[238:239], v[236:237], s[58:59], v[238:239] neg_lo:[0,0,1] neg_hi:[0,0,1]\n\t"
"v_pk_fma_f32 v[98:99], v[88:89], s[60:61], v[236:237]\n\t"
"v_pk_fma_f32 v[88:89], v[236:237], s[60:61], v[88:89]\n\t"
"v_pk_fma_f32 v[236:237], v[188:189], s[62:63], v[238:239]\n\t"
"v_pk_fma_f32 v[188:189], v[238:239], s[62:63], v[188:189]\n\t"
"v_mfma_f32_16x16x32_bf16 v[72:75], a[216:219], a[176:179], v[72:75]\n\t"
"v_accvgpr_write_b32 a145, v230\n\t"
"v_accvgpr_write_b32 a147, v234\n\t"
"v_mfma_f32_16x16x32_bf16 v[72:75], a[236:239], a[180:183], v[72:75]\n\t"
"v_accvgpr_read_b32 v224, a154\n\t"
"v_accvgpr_read_b32 v226, a158\n\t"
"v_mfma_f32_16x16x32_bf16 v[72:75], a[240:243], a[184:187], v[72:75]\n\t"
"v_accvgpr_read_b32 v225, a155\n\t"
"v_accvgpr_read_b32 v227, a159\n\t"
"buffer_load_dwordx4 a[228:231], v252, s[8:11], s20 offen offset:1024 sc1\n\t"
"v_cvt_pk_bf16_f32 v236, v236, v237\n\t"
"v_cvt_pk_bf16_f32 v98, v98, v99\n\t"
"v_pk_fma_f32 v[224:225], v[226:227], s[56:57], v[224:225]\n\t"
"v_pk_fma_f32 v[226:227], v[224:225], s[58:59], v[226:227] neg_lo:[0,0,1] neg_hi:[0,0,1]\n\t"
"v_pk_fma_f32 v[228:229], v[90:91], s[60:61], v[224:225]\n\t"
"v_pk_fma_f32 v[90:91], v[224:225], s[60:61], v[90:91]\n\t"
"v_pk_fma_f32 v[224:225], v[190:191], s[62:63], v[226:227]\n\t"
"v_pk_fma_f32 v[190:191], v[226:227], s[62:63], v[190:191]\n\t"
"v_mfma_f32_16x16x32_bf16 v[76:79], a[216:219], a[180:183], v[76:79]\n\t"
"v_accvgpr_write_b32 a152, v236\n\t"
"v_accvgpr_write_b32 a154, v98\n\t"
"v_mfma_f32_16x16x32_bf16 v[76:79], a[236:239], a[184:187], v[76:79]\n\t"
"v_accvgpr_read_b32 v230, a160\n\t"
"v_accvgpr_read_b32 v232, a164\n\t"
"v_mfma_f32_16x16x32_bf16 v[76:79], a[240:243], a[188:191], v[76:79]\n\t"
"v_accvgpr_read_b32 v231, a161\n\t"
"v_accvgpr_read_b32 v233, a165\n\t"
"ds_read_b128 a[168:171], v244 offset:27648\n\t"
"ds_read_b128 a[172:175], v244 offset:28160\n\t"
"ds_read_b128 a[176:179], v244 offset:28672\n\t"
"buffer_load_dwordx4 a[232:235], v252, s[8:11], s20 offen offset:2048 sc1\n\t"
"v_cvt_pk_bf16_f32 v224, v224, v225\n\t"
"v_cvt_pk_bf16_f32 v228, v228, v229\n\t"
"v_pk_fma_f32 v[230:231], v[232:233], s[56:57], v[230:231]\n\t"
"v_pk_fma_f32 v[232:233], v[230:231], s[58:59], v[232:233] neg_lo:[0,0,1] neg_hi:[0,0,1]\n\t"
"v_pk_fma_f32 v[234:235], v[92:93], s[60:61], v[230:231]\n\t"
"v_pk_fma_f32 v[92:93], v[230:231], s[60:61], v[92:93]\n\t"
"v_pk_fma_f32 v[230:231], v[192:193], s[62:63], v[232:233]\n\t"
"v_pk_fma_f32 v[192:193], v[232:233], s[62:63], v[192:193]\n\t"
"v_mfma_f32_16x16x32_bf16 v[80:83], a[216:219], a[184:187], v[80:83]\n\t"
"v_accvgpr_write_b32 a153, v224\n\t"
"v_accvgpr_write_b32 a155, v228\n\t"
"v_mfma_f32_16x16x32_bf16 v[80:83], a[236:239], a[188:191], v[80:83]\n\t"
"v_accvgpr_read_b32 v236, a162\n\t"
"v_accvgpr_read_b32 v238, a166\n\t"
"v_mfma_f32_16x16x32_bf16 v[80:83], a[240:243], a[192:195], v[80:83]\n\t"
"v_accvgpr_read_b32 v237, a163\n\t"
"v_accvgpr_read_b32 v239, a167\n\t"
"s_mov_b32 s20, 884736\n\t"
"buffer_load_dwordx4 a[156:159], v252, s[8:11], s20 offen offset:0 sc1\n\t"
"v_cvt_pk_bf16_f32 v230, v230, v231\n\t"
"v_cvt_pk_bf16_f32 v234, v234, v235\n\t"
"v_pk_fma_f32 v[236:237], v[238:239], s[56:57], v[236:237]\n\t"
"v_pk_fma_f32 v[238:239], v[236:237], s[58:59], v[238:239] neg_lo:[0,0,1] neg_hi:[0,0,1]\n\t"
"v_pk_fma_f32 v[98:99], v[94:95], s[60:61], v[236:237]\n\t"
"v_pk_fma_f32 v[94:95], v[236:237], s[60:61], v[94:95]\n\t"
"v_pk_fma_f32 v[236:237], v[194:195], s[62:63], v[238:239]\n\t"
"v_pk_fma_f32 v[194:195], v[238:239], s[62:63], v[194:195]\n\t"
"v_mfma_f32_16x16x32_bf16 v[84:87], a[216:219], a[188:191], v[84:87]\n\t"
"ds_read_b128 a[180:183], v244 offset:29184\n\t"
"v_accvgpr_write_b32 a160, v230\n\t"
"v_mfma_f32_16x16x32_bf16 v[84:87], a[236:239], a[192:195], v[84:87]\n\t"
"v_accvgpr_write_b32 a162, v234\n\t"
"v_cvt_pk_bf16_f32 v236, v236, v237\n\t"
"v_mfma_f32_16x16x32_bf16 v[84:87], a[240:243], a[196:199], v[84:87]\n\t"
"buffer_load_dwordx4 a[248:251], v252, s[8:11], s20 offen offset:1024 sc1\n\t"
"v_cvt_pk_bf16_f32 v98, v98, v99\n\t"
"ds_read_b128 a[184:187], v244 offset:29696\n\t"
"v_mfma_f32_16x16x32_bf16 v[88:91], a[216:219], a[192:195], v[88:91]\n\t"
"v_accvgpr_write_b32 a161, v236\n\t"
"v_accvgpr_write_b32 a163, v98\n\t"
"v_mfma_f32_16x16x32_bf16 v[88:91], a[236:239], a[196:199], v[88:91]\n\t"
"s_waitcnt vmcnt(14)\n\t"
"ds_read_b64_tr_b16 v[236:237], v240 offset:1536\n\t"
"v_mfma_f32_16x16x32_bf16 v[88:91], a[240:243], a[200:203], v[88:91]\n\t"
"ds_read_b64_tr_b16 v[238:239], v240 offset:3072\n\t"
"buffer_load_dwordx4 a[164:167], v252, s[8:11], s20 offen offset:2048 sc1\n\t"
"ds_read_b64_tr_b16 v[232:233], v241 offset:1536\n\t"
"ds_read_b64_tr_b16 v[234:235], v241 offset:3072\n\t"
"v_mfma_f32_16x16x32_bf16 v[92:95], a[216:219], a[196:199], v[92:95]\n\t"
"ds_read_b128 a[188:191], v244 offset:30208\n\t"
"v_mfma_f32_16x16x32_bf16 v[92:95], a[236:239], a[200:203], v[92:95]\n\t"
"ds_read_b128 a[192:195], v244 offset:30720\n\t"
"v_mfma_f32_16x16x32_bf16 v[92:95], a[240:243], a[204:207], v[92:95]\n\t"
"s_mov_b32 s20, 897024\n\t"
"buffer_load_dwordx4 a[252:255], v252, s[8:11], s20 offen offset:0 sc1\n\t"
"s_waitcnt lgkmcnt(8)\n\t"
"v_mfma_f32_16x16x32_bf16 v[100:103], a[220:223], a[168:171], v[100:103]\n\t"
"ds_read_b64_tr_b16 v[224:225], v240 offset:21504\n\t"
"v_mfma_f32_16x16x32_bf16 v[100:103], a[244:247], a[172:175], v[100:103]\n\t"
"ds_read_b64_tr_b16 v[226:227], v240 offset:23040\n\t"
"v_mfma_f32_16x16x32_bf16 v[100:103], a[40:43], a[176:179], v[100:103]\n\t"
"buffer_load_dwordx4 v[96:99], v252, s[8:11], s20 offen offset:1024 sc1\n\t"
"ds_read_b128 a[196:199], v244 offset:31232\n\t"
"ds_read_b128 a[200:203], v244 offset:31744\n\t"
"s_waitcnt vmcnt(14) lgkmcnt(8)\n\t"
"v_mfma_f32_16x16x32_bf16 v[236:239], v[236:239], a[208:211], 0\n\t"
"ds_read_b128 a[204:207], v244 offset:32256\n\t"
"ds_read_b64_tr_b16 v[228:229], v241 offset:21504\n\t"
"s_waitcnt lgkmcnt(8)\n\t"
"v_mfma_f32_16x16x32_bf16 v[232:235], v[232:235], a[208:211], 0\n\t"
"ds_read_b64_tr_b16 v[230:231], v241 offset:23040\n\t"
"buffer_load_dwordx4 a[216:219], v252, s[8:11], s20 offen offset:2048 sc1\n\t"
"v_mfma_f32_16x16x32_bf16 v[104:107], a[220:223], a[172:175], v[104:107]\n\t"
"v_mfma_f32_16x16x32_bf16 v[104:107], a[244:247], a[176:179], v[104:107]\n\t"
"v_mfma_f32_16x16x32_bf16 v[104:107], a[40:43], a[180:183], v[104:107]\n\t"
"s_mov_b32 s20, 909312\n\t"
"buffer_load_dwordx4 a[236:239], v252, s[8:11], s20 offen offset:0 sc1\n\t"
"s_waitcnt lgkmcnt(2)\n\t"
"s_barrier\n\t"
"v_mfma_f32_16x16x32_bf16 v[108:111], a[220:223], a[176:179], v[108:111]\n\t"
"v_cvt_pk_bf16_f32 v236, v236, v237\n\t"
"v_cvt_pk_bf16_f32 v237, v238, v239\n\t"
"v_mfma_f32_16x16x32_bf16 v[108:111], a[244:247], a[180:183], v[108:111]\n\t"
"v_cvt_pk_bf16_f32 v238, v232, v233\n\t"
"v_cvt_pk_bf16_f32 v239, v234, v235\n\t"
"v_mfma_f32_16x16x32_bf16 v[108:111], a[40:43], a[184:187], v[108:111]\n\t"
"buffer_load_dwordx4 a[240:243], v252, s[8:11], s20 offen offset:1024 sc1\n\t"
"v_mfma_f32_16x16x32_bf16 v[224:227], v[224:227], a[208:211], 0\n\t"
"s_mov_b64 exec, s[44:45]\n\t"
"ds_write_b128 v242, v[236:239] offset:0\n\t"
"s_mov_b64 exec, -1\n\t"
"s_waitcnt lgkmcnt(1)\n\t"
"v_mfma_f32_16x16x32_bf16 v[228:231], v[228:231], a[208:211], 0\n\t"
"ds_read_b64_tr_b16 v[232:233], v240 offset:41472\n\t"
"ds_read_b64_tr_b16 v[234:235], v240 offset:43008\n\t"
"v_mfma_f32_16x16x32_bf16 v[112:115], a[220:223], a[180:183], v[112:115]\n\t"
"ds_read_b64_tr_b16 v[236:237], v241 offset:41472\n\t"
"v_mfma_f32_16x16x32_bf16 v[112:115], a[244:247], a[184:187], v[112:115]\n\t"
"ds_read_b64_tr_b16 v[238:239], v241 offset:43008\n\t"
"v_add_u32 v240, 59904, v240\n\t"
"v_mfma_f32_16x16x32_bf16 v[112:115], a[40:43], a[188:191], v[112:115]\n\t"
"v_add_u32 v241, 59904, v241\n\t"
"v_mfma_f32_16x16x32_bf16 v[116:119], a[220:223], a[184:187], v[116:119]\n\t"
"v_cvt_pk_bf16_f32 v224, v224, v225\n\t"
"v_cvt_pk_bf16_f32 v225, v226, v227\n\t"
"v_mfma_f32_16x16x32_bf16 v[116:119], a[244:247], a[188:191], v[116:119]\n\t"
"v_cvt_pk_bf16_f32 v226, v228, v229\n\t"
"v_cvt_pk_bf16_f32 v227, v230, v231\n\t"
"v_mfma_f32_16x16x32_bf16 v[116:119], a[40:43], a[192:195], v[116:119]\n\t"
"s_waitcnt vmcnt(16)\n\t"
"ds_write_b128 v249, v[196:199] offset:18432\n\t"
"v_mfma_f32_16x16x32_bf16 v[120:123], a[220:223], a[188:191], v[120:123]\n\t"
"ds_read_b64_tr_b16 v[228:229], v240 offset:1536\n\t"
"v_mfma_f32_16x16x32_bf16 v[120:123], a[244:247], a[192:195], v[120:123]\n\t"
"ds_read_b64_tr_b16 v[230:231], v240 offset:3072\n\t"
"v_mfma_f32_16x16x32_bf16 v[120:123], a[40:43], a[196:199], v[120:123]\n\t"
"s_mov_b64 exec, s[44:45]\n\t"
"ds_write_b128 v242, v[224:227] offset:1536\n\t"
"s_mov_b64 exec, -1\n\t"
"v_mfma_f32_16x16x32_bf16 v[124:127], a[220:223], a[192:195], v[124:127]\n\t"
"ds_read_b64_tr_b16 v[224:225], v241 offset:1536\n\t"
"v_mfma_f32_16x16x32_bf16 v[124:127], a[244:247], a[196:199], v[124:127]\n\t"
"ds_read_b64_tr_b16 v[226:227], v241 offset:3072\n\t"
"v_mfma_f32_16x16x32_bf16 v[124:127], a[40:43], a[200:203], v[124:127]\n\t"
"ds_write_b64 v250, a[104:105] offset:18432\n\t"
"s_waitcnt lgkmcnt(9)\n\t"
"v_mfma_f32_16x16x32_bf16 v[232:235], v[232:235], a[208:211], 0\n\t"
"ds_write_b128 v249, v[200:203] offset:24576\n\t"
"s_waitcnt lgkmcnt(8)\n\t"
"v_mfma_f32_16x16x32_bf16 v[236:239], v[236:239], a[208:211], 0\n\t"
"v_mfma_f32_16x16x32_bf16 v[128:131], a[220:223], a[196:199], v[128:131]\n\t"
"v_mfma_f32_16x16x32_bf16 v[128:131], a[244:247], a[200:203], v[128:131]\n\t"
"v_mfma_f32_16x16x32_bf16 v[128:131], a[40:43], a[204:207], v[128:131]\n\t"
"buffer_load_dwordx4 a[220:223], v252, s[8:11], s20 offen offset:2048 sc1\n\t"
"v_mfma_f32_16x16x32_bf16 v[132:135], a[48:51], a[168:171], v[132:135]\n\t"
"v_cvt_pk_bf16_f32 v232, v232, v233\n\t"
"v_cvt_pk_bf16_f32 v233, v234, v235\n\t"
"v_mfma_f32_16x16x32_bf16 v[132:135], a[44:47], a[172:175], v[132:135]\n\t"
"v_cvt_pk_bf16_f32 v234, v236, v237\n\t"
"v_cvt_pk_bf16_f32 v235, v238, v239\n\t"
"v_mfma_f32_16x16x32_bf16 v[132:135], a[56:59], a[176:179], v[132:135]\n\t"
"s_mov_b32 s20, 1069056\n\t"
"buffer_load_dwordx4 a[244:247], v252, s[8:11], s20 offen offset:0 sc1\n\t"
"s_waitcnt lgkmcnt(5)\n\t"
"v_mfma_f32_16x16x32_bf16 v[228:231], v[228:231], a[208:211], 0\n\t"
"s_mov_b64 exec, s[44:45]\n\t"
"ds_write_b128 v242, v[232:235] offset:3072\n\t"
"s_mov_b64 exec, -1\n\t"
"s_waitcnt lgkmcnt(3)\n\t"
"v_mfma_f32_16x16x32_bf16 v[224:227], v[224:227], a[208:211], 0\n\t"
"ds_read_b64_tr_b16 v[236:237], v240 offset:21504\n\t"
"ds_read_b64_tr_b16 v[238:239], v240 offset:23040\n\t"
"v_mfma_f32_16x16x32_bf16 v[136:139], a[48:51], a[172:175], v[136:139]\n\t"
"ds_read_b64_tr_b16 v[232:233], v241 offset:21504\n\t"
"v_mfma_f32_16x16x32_bf16 v[136:139], a[44:47], a[176:179], v[136:139]\n\t"
"v_mfma_f32_16x16x32_bf16 v[136:139], a[56:59], a[180:183], v[136:139]\n\t"
"ds_read_b64_tr_b16 v[234:235], v241 offset:23040\n\t"
"v_mfma_f32_16x16x32_bf16 v[140:143], a[48:51], a[176:179], v[140:143]\n\t"
"v_cvt_pk_bf16_f32 v228, v228, v229\n\t"
"v_cvt_pk_bf16_f32 v229, v230, v231\n\t"
"v_mfma_f32_16x16x32_bf16 v[140:143], a[44:47], a[180:183], v[140:143]\n\t"
"v_cvt_pk_bf16_f32 v230, v224, v225\n\t"
"v_cvt_pk_bf16_f32 v231, v226, v227\n\t"
"v_mfma_f32_16x16x32_bf16 v[140:143], a[56:59], a[184:187], v[140:143]\n\t"
"s_mov_b64 exec, s[44:45]\n\t"
"ds_write_b128 v242, v[228:231] offset:4608\n\t"
"s_mov_b64 exec, -1\n\t"
"v_mfma_f32_16x16x32_bf16 v[144:147], a[48:51], a[180:183], v[144:147]\n\t"
"ds_read_b64_tr_b16 v[224:225], v240 offset:41472\n\t"
"v_mfma_f32_16x16x32_bf16 v[144:147], a[44:47], a[184:187], v[144:147]\n\t"
"ds_read_b64_tr_b16 v[226:227], v240 offset:43008\n\t"
"v_mfma_f32_16x16x32_bf16 v[144:147], a[56:59], a[188:191], v[144:147]\n\t"
"ds_read_b64_tr_b16 v[228:229], v241 offset:41472\n\t"
"ds_read_b64_tr_b16 v[230:231], v241 offset:43008\n\t"
"v_mfma_f32_16x16x32_bf16 v[148:151], a[48:51], a[184:187], v[148:151]\n\t"
"v_mfma_f32_16x16x32_bf16 v[148:151], a[44:47], a[188:191], v[148:151]\n\t"
"v_mfma_f32_16x16x32_bf16 v[148:151], a[56:59], a[192:195], v[148:151]\n\t"
"ds_write_b64 v250, a[106:107] offset:24576\n\t"
"s_waitcnt lgkmcnt(8)\n\t"
"v_mfma_f32_16x16x32_bf16 v[236:239], v[236:239], a[208:211], 0\n\t"
"s_waitcnt lgkmcnt(6)\n\t"
"v_mfma_f32_16x16x32_bf16 v[232:235], v[232:235], a[208:211], 0\n\t"
"v_mfma_f32_16x16x32_bf16 v[152:155], a[48:51], a[188:191], v[152:155]\n\t"
"v_mfma_f32_16x16x32_bf16 v[152:155], a[44:47], a[192:195], v[152:155]\n\t"
"v_mfma_f32_16x16x32_bf16 v[152:155], a[56:59], a[196:199], v[152:155]\n\t"
"v_mfma_f32_16x16x32_bf16 v[156:159], a[48:51], a[192:195], v[156:159]\n\t"
"s_nop 1\n\t"
"v_cvt_pk_bf16_f32 v236, v236, v237\n\t"
"v_cvt_pk_bf16_f32 v237, v238, v239\n\t"
"v_mfma_f32_16x16x32_bf16 v[156:159], a[44:47], a[196:199], v[156:159]\n\t"
"v_cvt_pk_bf16_f32 v238, v232, v233\n\t"
"v_mfma_f32_16x16x32_bf16 v[156:159], a[56:59], a[200:203], v[156:159]\n\t"
"v_cvt_pk_bf16_f32 v239, v234, v235\n\t"
"s_waitcnt lgkmcnt(3)\n\t"
"v_mfma_f32_16x16x32_bf16 v[224:227], v[224:227], a[208:211], 0\n\t"
"s_waitcnt lgkmcnt(1)\n\t"
"v_mfma_f32_16x16x32_bf16 v[228:231], v[228:231], a[208:211], 0\n\t"
"s_mov_b64 exec, s[44:45]\n\t"
"ds_write_b128 v242, v[236:239] offset:6144\n\t"
"s_mov_b64 exec, -1\n\t"
"v_mfma_f32_16x16x32_bf16 v[160:163], a[48:51], a[196:199], v[160:163]\n\t"
"v_mfma_f32_16x16x32_bf16 v[160:163], a[44:47], a[200:203], v[160:163]\n\t"
"v_mfma_f32_16x16x32_bf16 v[160:163], a[56:59], a[204:207], v[160:163]\n\t"
"buffer_load_dwordx4 a[44:47], v252, s[8:11], s20 offen offset:1024 sc1\n\t"
"v_mfma_f32_16x16x32_bf16 v[164:167], a[60:63], a[168:171], v[164:167]\n\t"
"v_cvt_pk_bf16_f32 v224, v224, v225\n\t"
"v_mfma_f32_16x16x32_bf16 v[164:167], a[64:67], a[172:175], v[164:167]\n\t"
"v_cvt_pk_bf16_f32 v225, v226, v227\n\t"
"v_cvt_pk_bf16_f32 v226, v228, v229\n\t"
"v_mfma_f32_16x16x32_bf16 v[164:167], a[52:55], a[176:179], v[164:167]\n\t"
"v_cvt_pk_bf16_f32 v227, v230, v231\n\t"
"v_mfma_f32_16x16x32_bf16 v[168:171], a[60:63], a[172:175], v[168:171]\n\t"
"v_mfma_f32_16x16x32_bf16 v[168:171], a[64:67], a[176:179], v[168:171]\n\t"
"v_mfma_f32_16x16x32_bf16 v[168:171], a[52:55], a[180:183], v[168:171]\n\t"
"s_mov_b64 exec, s[44:45]\n\t"
"ds_write_b128 v242, v[224:227] offset:7680\n\t"
"s_mov_b64 exec, -1\n\t"
"v_mfma_f32_16x16x32_bf16 v[172:175], a[60:63], a[176:179], v[172:175]\n\t"
"v_mfma_f32_16x16x32_bf16 v[172:175], a[64:67], a[180:183], v[172:175]\n\t"
"v_mfma_f32_16x16x32_bf16 v[172:175], a[52:55], a[184:187], v[172:175]\n\t"
"v_mfma_f32_16x16x32_bf16 v[176:179], a[60:63], a[180:183], v[176:179]\n\t"
"v_mfma_f32_16x16x32_bf16 v[176:179], a[64:67], a[184:187], v[176:179]\n\t"
"v_mfma_f32_16x16x32_bf16 v[176:179], a[52:55], a[188:191], v[176:179]\n\t"
"v_mfma_f32_16x16x32_bf16 v[180:183], a[60:63], a[184:187], v[180:183]\n\t"
"v_mfma_f32_16x16x32_bf16 v[180:183], a[64:67], a[188:191], v[180:183]\n\t"
"v_mfma_f32_16x16x32_bf16 v[180:183], a[52:55], a[192:195], v[180:183]\n\t"
"v_mfma_f32_16x16x32_bf16 v[184:187], a[60:63], a[188:191], v[184:187]\n\t"
"v_mfma_f32_16x16x32_bf16 v[184:187], a[64:67], a[192:195], v[184:187]\n\t"
"v_mfma_f32_16x16x32_bf16 v[184:187], a[52:55], a[196:199], v[184:187]\n\t"
"s_waitcnt lgkmcnt(0)\n\t"
"s_barrier\n\t"
"v_mfma_f32_16x16x32_bf16 v[188:191], a[60:63], a[192:195], v[188:191]\n\t"
"ds_read_b128 a[168:171], v244 offset:0\n\t"
"v_mfma_f32_16x16x32_bf16 v[188:191], a[64:67], a[196:199], v[188:191]\n\t"
"ds_read_b128 a[172:175], v244 offset:512\n\t"
"v_mfma_f32_16x16x32_bf16 v[188:191], a[52:55], a[200:203], v[188:191]\n\t"
"ds_read_b128 a[176:179], v244 offset:1024\n\t"
"ds_read_b128 a[180:183], v244 offset:1536\n\t"
"ds_read_b128 a[184:187], v244 offset:2048\n\t"
"ds_read_b128 a[40:43], v252 offset:18432\n\t"
"ds_read_b128 a[188:191], v244 offset:2560\n\t"
"v_mfma_f32_16x16x32_bf16 v[192:195], a[60:63], a[196:199], v[192:195]\n\t"
"ds_read_b128 a[48:51], v252 offset:19456\n\t"
"v_mfma_f32_16x16x32_bf16 v[192:195], a[64:67], a[200:203], v[192:195]\n\t"
"ds_read_b128 a[192:195], v244 offset:3072\n\t"
"v_mfma_f32_16x16x32_bf16 v[192:195], a[52:55], a[204:207], v[192:195]\n\t"
"buffer_load_dwordx4 a[104:107], v252, s[8:11], s20 offen offset:2048 sc1\n\t"
"ds_read_b128 a[196:199], v244 offset:3584\n\t"
"ds_read_b128 a[200:203], v244 offset:4096\n\t"
"Lst15_%=:\n\t"
"s_waitcnt lgkmcnt(8)\n\t"
"v_mfma_f32_16x16x32_bf16 v[0:3], a[72:75], a[168:171], v[0:3]\n\t"
"ds_read_b128 a[56:59], v252 offset:20480\n\t"
"s_add_u32 s21, s21, 6291456\n\t"
"v_add_u32 v254, 6291456, v254\n\t"
"v_mfma_f32_16x16x32_bf16 v[0:3], a[68:71], a[172:175], v[0:3]\n\t"
"ds_read_b128 a[204:207], v244 offset:4608\n\t"
"v_mfma_f32_16x16x32_bf16 v[0:3], a[80:83], a[176:179], v[0:3]\n\t"
"s_mov_b32 s20, 1081344\n\t"
"buffer_load_dwordx4 a[64:67], v252, s[8:11], s20 offen offset:0 sc1\n\t"
"ds_write_b128 v249, v[204:207] offset:30720\n\t"
"s_waitcnt lgkmcnt(10)\n\t"
"v_mfma_f32_16x16x32_bf16 v[4:7], a[72:75], a[172:175], v[4:7]\n\t"
"v_mfma_f32_16x16x32_bf16 v[4:7], a[68:71], a[176:179], v[4:7]\n\t"
"v_mfma_f32_16x16x32_bf16 v[4:7], a[80:83], a[180:183], v[4:7]\n\t"
"buffer_load_dwordx4 a[52:55], v252, s[8:11], s20 offen offset:1024 sc1\n\t"
"s_waitcnt lgkmcnt(9)\n\t"
"v_mfma_f32_16x16x32_bf16 v[8:11], a[72:75], a[176:179], v[8:11]\n\t"
"v_mfma_f32_16x16x32_bf16 v[8:11], a[68:71], a[180:183], v[8:11]\n\t"
"v_mfma_f32_16x16x32_bf16 v[8:11], a[80:83], a[184:187], v[8:11]\n\t"
"buffer_load_dwordx4 v[200:203], v252, s[8:11], s20 offen offset:2048 sc1\n\t"
"s_add_u32 s29, s22, 384\n\t"
"s_waitcnt lgkmcnt(8)\n\t"
"buffer_store_dwordx4 a[40:43], v248, s[16:19], s29 offen offset:0 nt\n\t"
"s_waitcnt lgkmcnt(7)\n\t"
"v_mfma_f32_16x16x32_bf16 v[12:15], a[72:75], a[180:183], v[12:15]\n\t"
"v_mfma_f32_16x16x32_bf16 v[12:15], a[68:71], a[184:187], v[12:15]\n\t"
"v_mfma_f32_16x16x32_bf16 v[12:15], a[80:83], a[188:191], v[12:15]\n\t"
"ds_write_b64 v250, a[112:113] offset:30720\n\t"
"s_waitcnt lgkmcnt(6)\n\t"
"v_mfma_f32_16x16x32_bf16 v[16:19], a[72:75], a[184:187], v[16:19]\n\t"
"v_mfma_f32_16x16x32_bf16 v[16:19], a[68:71], a[188:191], v[16:19]\n\t"
"v_mfma_f32_16x16x32_bf16 v[16:19], a[80:83], a[192:195], v[16:19]\n\t"
"s_add_u32 s29, s22, 384\n\t"
"buffer_store_dwordx4 a[48:51], v248, s[16:19], s29 offen offset:128 nt\n\t"
"s_waitcnt lgkmcnt(5)\n\t"
"v_mfma_f32_16x16x32_bf16 v[20:23], a[72:75], a[188:191], v[20:23]\n\t"
"v_mfma_f32_16x16x32_bf16 v[20:23], a[68:71], a[192:195], v[20:23]\n\t"
"v_mfma_f32_16x16x32_bf16 v[20:23], a[80:83], a[196:199], v[20:23]\n\t"
"ds_write_b128 v249, v[208:211] offset:36864\n\t"
"s_waitcnt lgkmcnt(5)\n\t"
"v_mfma_f32_16x16x32_bf16 v[24:27], a[72:75], a[192:195], v[24:27]\n\t"
"v_mfma_f32_16x16x32_bf16 v[24:27], a[68:71], a[196:199], v[24:27]\n\t"
"v_mfma_f32_16x16x32_bf16 v[24:27], a[80:83], a[200:203], v[24:27]\n\t"
"s_add_u32 s29, s22, 384\n\t"
"s_waitcnt lgkmcnt(4)\n\t"
"buffer_store_dwordx4 a[56:59], v248, s[16:19], s29 offen offset:256 nt\n\t"
"s_waitcnt lgkmcnt(3)\n\t"
"v_mfma_f32_16x16x32_bf16 v[28:31], a[72:75], a[196:199], v[28:31]\n\t"
"v_mfma_f32_16x16x32_bf16 v[28:31], a[68:71], a[200:203], v[28:31]\n\t"
"v_mfma_f32_16x16x32_bf16 v[28:31], a[80:83], a[204:207], v[28:31]\n\t"
"s_mov_b32 s20, 1093632\n\t"
"buffer_load_dwordx4 a[72:75], v252, s[8:11], s20 offen offset:0 sc1\n\t"
"v_mfma_f32_16x16x32_bf16 v[32:35], a[92:95], a[168:171], v[32:35]\n\t"
"v_mfma_f32_16x16x32_bf16 v[32:35], a[88:91], a[172:175], v[32:35]\n\t"
"v_mfma_f32_16x16x32_bf16 v[32:35], a[96:99], a[176:179], v[32:35]\n\t"
"buffer_load_dwordx4 a[80:83], v252, s[8:11], s20 offen offset:1024 sc1\n\t"
"v_mfma_f32_16x16x32_bf16 v[36:39], a[92:95], a[172:175], v[36:39]\n\t"
"ds_read_b64_tr_b16 v[232:233], v240 offset:512\n\t"
"v_mfma_f32_16x16x32_bf16 v[36:39], a[88:91], a[176:179], v[36:39]\n\t"
"ds_read_b64_tr_b16 v[234:235], v240 offset:2048\n\t"
"v_mfma_f32_16x16x32_bf16 v[36:39], a[96:99], a[180:183], v[36:39]\n\t"
"ds_write_b64 v250, a[114:115] offset:36864\n\t"
"v_mfma_f32_16x16x32_bf16 v[40:43], a[92:95], a[176:179], v[40:43]\n\t"
"v_mfma_f32_16x16x32_bf16 v[40:43], a[88:91], a[180:183], v[40:43]\n\t"
"v_mfma_f32_16x16x32_bf16 v[40:43], a[96:99], a[184:187], v[40:43]\n\t"
"buffer_load_dwordx4 a[68:71], v252, s[8:11], s20 offen offset:2048 sc1\n\t"
"v_mfma_f32_16x16x32_bf16 v[44:47], a[92:95], a[180:183], v[44:47]\n\t"
"v_mfma_f32_16x16x32_bf16 v[44:47], a[88:91], a[184:187], v[44:47]\n\t"
"v_mfma_f32_16x16x32_bf16 v[44:47], a[96:99], a[188:191], v[44:47]\n\t"
"s_waitcnt lgkmcnt(0)\n\t"
"s_barrier\n\t"
"v_mfma_f32_16x16x32_bf16 v[48:51], a[92:95], a[184:187], v[48:51]\n\t"
"ds_read_b128 a[40:43], v252 offset:30720\n\t"
"v_mfma_f32_16x16x32_bf16 v[48:51], a[88:91], a[188:191], v[48:51]\n\t"
"ds_read_b64_tr_b16 v[236:237], v241 offset:512\n\t"
"v_mfma_f32_16x16x32_bf16 v[48:51], a[96:99], a[192:195], v[48:51]\n\t"
"ds_read_b64_tr_b16 v[238:239], v241 offset:2048\n\t"
"ds_read_b128 a[48:51], v252 offset:31744\n\t"
"v_mfma_f32_16x16x32_bf16 v[52:55], a[92:95], a[188:191], v[52:55]\n\t"
"ds_read_b128 a[56:59], v252 offset:32768\n\t"
"v_mfma_f32_16x16x32_bf16 v[52:55], a[88:91], a[192:195], v[52:55]\n\t"
"ds_read_b64_tr_b16 v[228:229], v240 offset:20480\n\t"
"v_mfma_f32_16x16x32_bf16 v[52:55], a[96:99], a[196:199], v[52:55]\n\t"
"s_mov_b32 s20, 1105920\n\t"
"buffer_load_dwordx4 v[208:211], v252, s[8:11], s20 offen offset:0 sc1\n\t"
"v_mfma_f32_16x16x32_bf16 v[232:235], v[232:235], a[208:211], 0\n\t"
"ds_write_b128 v249, v[212:215] offset:18432\n\t"
"v_mfma_f32_16x16x32_bf16 v[56:59], a[92:95], a[192:195], v[56:59]\n\t"
"ds_read_b64_tr_b16 v[230:231], v240 offset:22016\n\t"
"v_mfma_f32_16x16x32_bf16 v[56:59], a[88:91], a[196:199], v[56:59]\n\t"
"ds_read_b64_tr_b16 v[224:225], v241 offset:20480\n\t"
"v_mfma_f32_16x16x32_bf16 v[56:59], a[96:99], a[200:203], v[56:59]\n\t"
"s_add_u32 s29, s22, 196992\n\t"
"s_waitcnt lgkmcnt(8)\n\t"
"buffer_store_dwordx4 a[40:43], v248, s[16:19], s29 offen offset:0 nt\n\t"
"s_waitcnt lgkmcnt(6)\n\t"
"v_mfma_f32_16x16x32_bf16 v[236:239], v[236:239], a[208:211], 0\n\t"
"ds_read_b64_tr_b16 v[226:227], v241 offset:22016\n\t"
"v_mfma_f32_16x16x32_bf16 v[60:63], a[92:95], a[196:199], v[60:63]\n\t"
"v_cvt_pk_bf16_f32 v232, v232, v233\n\t"
"v_cvt_pk_bf16_f32 v233, v234, v235\n\t"
"v_mfma_f32_16x16x32_bf16 v[60:63], a[88:91], a[200:203], v[60:63]\n\t"
"v_mfma_f32_16x16x32_bf16 v[60:63], a[96:99], a[204:207], v[60:63]\n\t"
"buffer_load_dwordx4 a[88:91], v252, s[8:11], s20 offen offset:1024 sc1\n\t"
"v_mfma_f32_16x16x32_bf16 v[64:67], a[76:79], a[168:171], v[64:67]\n\t"
"ds_read_b128 a[168:171], v244 offset:9216\n\t"
"v_mfma_f32_16x16x32_bf16 v[64:67], a[108:111], a[172:175], v[64:67]\n\t"
"v_cvt_pk_bf16_f32 v234, v236, v237\n\t"
"v_cvt_pk_bf16_f32 v235, v238, v239\n\t"
"v_mfma_f32_16x16x32_bf16 v[64:67], a[84:87], a[176:179], v[64:67]\n\t"
"s_add_u32 s29, s22, 196992\n\t"
"s_waitcnt lgkmcnt(7)\n\t"
"buffer_store_dwordx4 a[48:51], v248, s[16:19], s29 offen offset:128 nt\n\t"
"v_mfma_f32_16x16x32_bf16 v[68:71], a[76:79], a[172:175], v[68:71]\n\t"
"ds_read_b128 a[172:175], v244 offset:9728\n\t"
"v_mfma_f32_16x16x32_bf16 v[68:71], a[108:111], a[176:179], v[68:71]\n\t"
"v_mfma_f32_16x16x32_bf16 v[68:71], a[84:87], a[180:183], v[68:71]\n\t"
"buffer_load_dwordx4 a[96:99], v252, s[8:11], s20 offen offset:2048 sc1\n\t"
"ds_write_b64 v250, a[120:121] offset:18432\n\t"
"s_waitcnt lgkmcnt(5)\n\t"
"v_mfma_f32_16x16x32_bf16 v[228:231], v[228:231], a[208:211], 0\n\t"
"s_add_u32 s29, s22, 196992\n\t"
"buffer_store_dwordx4 a[56:59], v248, s[16:19], s29 offen offset:256 nt\n\t"
"v_mfma_f32_16x16x32_bf16 v[72:75], a[76:79], a[176:179], v[72:75]\n\t"
"ds_read_b128 a[176:179], v244 offset:10240\n\t"
"v_mfma_f32_16x16x32_bf16 v[72:75], a[108:111], a[180:183], v[72:75]\n\t"
"v_mfma_f32_16x16x32_bf16 v[72:75], a[84:87], a[184:187], v[72:75]\n\t"
"ds_write_b128 v249, v[216:219] offset:24576\n\t"
"s_waitcnt lgkmcnt(5)\n\t"
"v_mfma_f32_16x16x32_bf16 v[224:227], v[224:227], a[208:211], 0\n\t"
"ds_write_b64 v250, a[122:123] offset:24576\n\t"
"v_mfma_f32_16x16x32_bf16 v[76:79], a[76:79], a[180:183], v[76:79]\n\t"
"v_cvt_pk_bf16_f32 v228, v228, v229\n\t"
"v_mfma_f32_16x16x32_bf16 v[76:79], a[108:111], a[184:187], v[76:79]\n\t"
"v_cvt_pk_bf16_f32 v229, v230, v231\n\t"
"v_mfma_f32_16x16x32_bf16 v[76:79], a[84:87], a[188:191], v[76:79]\n\t"
"ds_read_b128 a[180:183], v244 offset:10752\n\t"
"v_mfma_f32_16x16x32_bf16 v[80:83], a[76:79], a[184:187], v[80:83]\n\t"
"ds_read_b128 a[184:187], v244 offset:11264\n\t"
"v_mfma_f32_16x16x32_bf16 v[80:83], a[108:111], a[188:191], v[80:83]\n\t"
"v_cvt_pk_bf16_f32 v230, v224, v225\n\t"
"v_mfma_f32_16x16x32_bf16 v[80:83], a[84:87], a[192:195], v[80:83]\n\t"
"v_cvt_pk_bf16_f32 v231, v226, v227\n\t"
"v_mfma_f32_16x16x32_bf16 v[84:87], a[76:79], a[188:191], v[84:87]\n\t"
"ds_read_b128 a[188:191], v244 offset:11776\n\t"
"v_mfma_f32_16x16x32_bf16 v[84:87], a[108:111], a[192:195], v[84:87]\n\t"
"v_mfma_f32_16x16x32_bf16 v[84:87], a[84:87], a[196:199], v[84:87]\n\t"
"v_mfma_f32_16x16x32_bf16 v[88:91], a[76:79], a[192:195], v[88:91]\n\t"
"ds_read_b128 a[192:195], v244 offset:12288\n\t"
"v_mfma_f32_16x16x32_bf16 v[88:91], a[108:111], a[196:199], v[88:91]\n\t"
"v_mfma_f32_16x16x32_bf16 v[88:91], a[84:87], a[200:203], v[88:91]\n\t"
"v_mfma_f32_16x16x32_bf16 v[92:95], a[76:79], a[196:199], v[92:95]\n\t"
"ds_read_b128 a[196:199], v244 offset:12800\n\t"
"v_mfma_f32_16x16x32_bf16 v[92:95], a[108:111], a[200:203], v[92:95]\n\t"
"ds_read_b128 a[200:203], v244 offset:13312\n\t"
"v_mfma_f32_16x16x32_bf16 v[92:95], a[84:87], a[204:207], v[92:95]\n\t"
"s_mov_b32 s20, 1118208\n\t"
"buffer_load_dwordx4 a[76:79], v252, s[8:11], s20 offen offset:0 sc1\n\t"
"s_waitcnt vmcnt(34) lgkmcnt(8)\n\t"
"v_mfma_f32_16x16x32_bf16 v[100:103], a[116:119], a[168:171], v[100:103]\n\t"
"ds_read_b128 a[204:207], v244 offset:13824\n\t"
"v_mfma_f32_16x16x32_bf16 v[100:103], a[100:103], a[172:175], v[100:103]\n\t"
"v_mfma_f32_16x16x32_bf16 v[100:103], a[124:127], a[176:179], v[100:103]\n\t"
"buffer_load_dwordx4 a[108:111], v252, s[8:11], s20 offen offset:1024 sc1\n\t"
"s_waitcnt lgkmcnt(6)\n\t"
"v_mfma_f32_16x16x32_bf16 v[104:107], a[116:119], a[172:175], v[104:107]\n\t"
"v_mfma_f32_16x16x32_bf16 v[104:107], a[100:103], a[176:179], v[104:107]\n\t"
"v_mfma_f32_16x16x32_bf16 v[104:107], a[124:127], a[180:183], v[104:107]\n\t"
"buffer_load_dwordx4 a[84:87], v252, s[8:11], s20 offen offset:2048 sc1\n\t"
"s_waitcnt lgkmcnt(0)\n\t"
"s_barrier\n\t"
"v_mfma_f32_16x16x32_bf16 v[108:111], a[116:119], a[176:179], v[108:111]\n\t"
"ds_read_b128 a[40:43], v252 offset:18432\n\t"
"v_mfma_f32_16x16x32_bf16 v[108:111], a[100:103], a[180:183], v[108:111]\n\t"
"ds_read_b64_tr_b16 v[236:237], v240 offset:40448\n\t"
"v_mfma_f32_16x16x32_bf16 v[108:111], a[124:127], a[184:187], v[108:111]\n\t"
"s_add_u32 m0, s24, 59904\n\t"
"s_add_u32 s28, s21, 1769472\n\t"
"buffer_load_dwordx4 v247, s[12:15], s28 offen offset:0 nt lds\n\t"
"s_mov_b64 exec, s[44:45]\n\t"
"ds_write_b128 v242, v[232:235] offset:4608\n\t"
"s_mov_b64 exec, -1\n\t"
"s_add_u32 m0, s24, 66048\n\t"
"s_add_u32 s28, s21, 1966080\n\t"
"buffer_load_dwordx4 v247, s[12:15], s28 offen offset:0 nt lds\n\t"
"v_mfma_f32_16x16x32_bf16 v[112:115], a[116:119], a[180:183], v[112:115]\n\t"
"ds_read_b64_tr_b16 v[238:239], v240 offset:41984\n\t"
"v_mfma_f32_16x16x32_bf16 v[112:115], a[100:103], a[184:187], v[112:115]\n\t"
"ds_read_b64_tr_b16 v[224:225], v241 offset:40448\n\t"
"v_mfma_f32_16x16x32_bf16 v[112:115], a[124:127], a[188:191], v[112:115]\n\t"
"s_mov_b64 exec, s[44:45]\n\t"
"ds_write_b128 v242, v[228:231] offset:6144\n\t"
"s_mov_b64 exec, -1\n\t"
"s_add_u32 m0, s24, 72192\n\t"
"s_add_u32 s28, s21, 2162688\n\t"
"buffer_load_dwordx4 v247, s[12:15], s28 offen offset:0 nt lds\n\t"
"ds_read_b64_tr_b16 v[226:227], v241 offset:41984\n\t"
"v_mfma_f32_16x16x32_bf16 v[116:119], a[116:119], a[184:187], v[116:119]\n\t"
"v_subrev_u32 v240, 59904, v240\n\t"
"v_subrev_u32 v241, 59904, v241\n\t"
"v_mfma_f32_16x16x32_bf16 v[116:119], a[100:103], a[188:191], v[116:119]\n\t"
"ds_read_b64_tr_b16 v[232:233], v240 offset:512\n\t"
"v_mfma_f32_16x16x32_bf16 v[116:119], a[124:127], a[192:195], v[116:119]\n\t"
"s_add_u32 m0, s24, 79872\n\t"
"s_add_u32 s28, s21, 2359296\n\t"
"buffer_load_dwordx4 v247, s[12:15], s28 offen offset:0 nt lds\n\t"
"s_add_u32 m0, s24, 86016\n\t"
"s_add_u32 s28, s21, 2555904\n\t"
"buffer_load_dwordx4 v247, s[12:15], s28 offen offset:0 nt lds\n\t"
"v_mfma_f32_16x16x32_bf16 v[120:123], a[116:119], a[188:191], v[120:123]\n\t"
"ds_read_b64_tr_b16 v[234:235], v240 offset:2048\n\t"
"v_mfma_f32_16x16x32_bf16 v[120:123], a[100:103], a[192:195], v[120:123]\n\t"
"ds_read_b64_tr_b16 v[228:229], v241 offset:512\n\t"
"v_mfma_f32_16x16x32_bf16 v[120:123], a[124:127], a[196:199], v[120:123]\n\t"
"s_add_u32 m0, s24, 92160\n\t"
"s_add_u32 s28, s21, 2752512\n\t"
"buffer_load_dwordx4 v247, s[12:15], s28 offen offset:0 nt lds\n\t"
"ds_read_b64_tr_b16 v[230:231], v241 offset:2048\n\t"
"s_waitcnt lgkmcnt(7)\n\t"
"v_mfma_f32_16x16x32_bf16 v[236:239], v[236:239], a[208:211], 0\n\t"
"ds_write_b128 v249, a[0:3] offset:30720\n\t"
"s_waitcnt lgkmcnt(5)\n\t"
"v_mfma_f32_16x16x32_bf16 v[224:227], v[224:227], a[208:211], 0\n\t"
"s_add_u32 s29, s22, 393600\n\t"
"buffer_store_dwordx4 a[40:43], v248, s[16:19], s29 offen offset:0 nt\n\t"
"v_mfma_f32_16x16x32_bf16 v[124:127], a[116:119], a[192:195], v[124:127]\n\t"
"v_mfma_f32_16x16x32_bf16 v[124:127], a[100:103], a[196:199], v[124:127]\n\t"
"v_mfma_f32_16x16x32_bf16 v[124:127], a[124:127], a[200:203], v[124:127]\n\t"
"ds_write_b64 v250, a[128:129] offset:30720\n\t"
"v_mfma_f32_16x16x32_bf16 v[128:131], a[116:119], a[196:199], v[128:131]\n\t"
"v_cvt_pk_bf16_f32 v236, v236, v237\n\t"
"v_cvt_pk_bf16_f32 v237, v238, v239\n\t"
"v_mfma_f32_16x16x32_bf16 v[128:131], a[100:103], a[200:203], v[128:131]\n\t"
"v_cvt_pk_bf16_f32 v238, v224, v225\n\t"
"v_cvt_pk_bf16_f32 v239, v226, v227\n\t"
"v_mfma_f32_16x16x32_bf16 v[128:131], a[124:127], a[204:207], v[128:131]\n\t"
"s_mov_b64 exec, s[44:45]\n\t"
"ds_write_b128 v242, v[236:239] offset:7680\n\t"
"s_mov_b64 exec, -1\n\t"
"s_waitcnt lgkmcnt(5)\n\t"
"v_mfma_f32_16x16x32_bf16 v[232:235], v[232:235], a[208:211], 0\n\t"
"ds_read_b64_tr_b16 v[224:225], v240 offset:20480\n\t"
"ds_read_b64_tr_b16 v[226:227], v240 offset:22016\n\t"
"s_waitcnt lgkmcnt(5)\n\t"
"v_mfma_f32_16x16x32_bf16 v[228:231], v[228:231], a[208:211], 0\n\t"
"ds_read_b64_tr_b16 v[236:237], v241 offset:20480\n\t"
"ds_read_b64_tr_b16 v[238:239], v241 offset:22016\n\t"
"s_waitcnt vmcnt(40)\n\t"
"v_mfma_f32_16x16x32_bf16 v[132:135], a[132:135], a[168:171], v[132:135]\n\t"
"v_mfma_f32_16x16x32_bf16 v[132:135], a[224:227], a[172:175], v[132:135]\n\t"
"v_mfma_f32_16x16x32_bf16 v[132:135], a[140:143], a[176:179], v[132:135]\n\t"
"ds_write_b128 v249, a[4:7] offset:36864\n\t"
"v_mfma_f32_16x16x32_bf16 v[136:139], a[132:135], a[172:175], v[136:139]\n\t"
"v_cvt_pk_bf16_f32 v232, v232, v233\n\t"
"v_cvt_pk_bf16_f32 v233, v234, v235\n\t"
"v_mfma_f32_16x16x32_bf16 v[136:139], a[224:227], a[176:179], v[136:139]\n\t"
"v_cvt_pk_bf16_f32 v234, v228, v229\n\t"
"v_cvt_pk_bf16_f32 v235, v230, v231\n\t"
"v_mfma_f32_16x16x32_bf16 v[136:139], a[140:143], a[180:183], v[136:139]\n\t"
"s_mov_b64 exec, s[44:45]\n\t"
"ds_write_b128 v242, v[232:235] offset:0\n\t"
"s_mov_b64 exec, -1\n\t"
"v_mfma_f32_16x16x32_bf16 v[140:143], a[132:135], a[176:179], v[140:143]\n\t"
"ds_read_b64_tr_b16 v[228:229], v240 offset:40448\n\t"
"v_mfma_f32_16x16x32_bf16 v[140:143], a[224:227], a[180:183], v[140:143]\n\t"
"ds_read_b64_tr_b16 v[230:231], v240 offset:41984\n\t"
"v_mfma_f32_16x16x32_bf16 v[140:143], a[140:143], a[184:187], v[140:143]\n\t"
"ds_read_b64_tr_b16 v[232:233], v241 offset:40448\n\t"
"v_mfma_f32_16x16x32_bf16 v[144:147], a[132:135], a[180:183], v[144:147]\n\t"
"ds_read_b64_tr_b16 v[234:235], v241 offset:41984\n\t"
"v_mfma_f32_16x16x32_bf16 v[144:147], a[224:227], a[184:187], v[144:147]\n\t"
"v_mfma_f32_16x16x32_bf16 v[144:147], a[140:143], a[188:191], v[144:147]\n\t"
"ds_write_b64 v250, a[130:131] offset:36864\n\t"
"s_waitcnt lgkmcnt(9)\n\t"
"v_mfma_f32_16x16x32_bf16 v[224:227], v[224:227], a[208:211], 0\n\t"
"s_waitcnt lgkmcnt(7)\n\t"
"v_mfma_f32_16x16x32_bf16 v[236:239], v[236:239], a[208:211], 0\n\t"
"v_mfma_f32_16x16x32_bf16 v[148:151], a[132:135], a[184:187], v[148:151]\n\t"
"v_mfma_f32_16x16x32_bf16 v[148:151], a[224:227], a[188:191], v[148:151]\n\t"
"v_mfma_f32_16x16x32_bf16 v[148:151], a[140:143], a[192:195], v[148:151]\n\t"
"v_mfma_f32_16x16x32_bf16 v[152:155], a[132:135], a[188:191], v[152:155]\n\t"
"s_nop 1\n\t"
"v_cvt_pk_bf16_f32 v224, v224, v225\n\t"
"v_cvt_pk_bf16_f32 v225, v226, v227\n\t"
"v_mfma_f32_16x16x32_bf16 v[152:155], a[224:227], a[192:195], v[152:155]\n\t"
"v_cvt_pk_bf16_f32 v226, v236, v237\n\t"
"v_mfma_f32_16x16x32_bf16 v[152:155], a[140:143], a[196:199], v[152:155]\n\t"
"v_cvt_pk_bf16_f32 v227, v238, v239\n\t"
"s_waitcnt lgkmcnt(3)\n\t"
"v_mfma_f32_16x16x32_bf16 v[228:231], v[228:231], a[208:211], 0\n\t"
"s_mov_b64 exec, s[44:45]\n\t"
"ds_write_b128 v242, v[224:227] offset:1536\n\t"
"s_mov_b64 exec, -1\n\t"
"s_waitcnt lgkmcnt(2)\n\t"
"v_mfma_f32_16x16x32_bf16 v[232:235], v[232:235], a[208:211], 0\n\t"
"v_mfma_f32_16x16x32_bf16 v[156:159], a[132:135], a[192:195], v[156:159]\n\t"
"ds_read_b128 a[116:119], v252 offset:19456\n\t"
"v_mfma_f32_16x16x32_bf16 v[156:159], a[224:227], a[196:199], v[156:159]\n\t"
"v_mfma_f32_16x16x32_bf16 v[156:159], a[140:143], a[200:203], v[156:159]\n\t"
"ds_read_b128 a[148:151], v252 offset:20480\n\t"
"v_mfma_f32_16x16x32_bf16 v[160:163], a[132:135], a[196:199], v[160:163]\n\t"
"v_cvt_pk_bf16_f32 v228, v228, v229\n\t"
"v_mfma_f32_16x16x32_bf16 v[160:163], a[224:227], a[200:203], v[160:163]\n\t"
"v_cvt_pk_bf16_f32 v229, v230, v231\n\t"
"v_mfma_f32_16x16x32_bf16 v[160:163], a[140:143], a[204:207], v[160:163]\n\t"
"v_cvt_pk_bf16_f32 v230, v232, v233\n\t"
"v_cvt_pk_bf16_f32 v231, v234, v235\n\t"
"s_waitcnt vmcnt(37)\n\t"
"v_mfma_f32_16x16x32_bf16 v[164:167], a[212:215], a[168:171], v[164:167]\n\t"
"v_mfma_f32_16x16x32_bf16 v[164:167], a[228:231], a[172:175], v[164:167]\n\t"
"v_mfma_f32_16x16x32_bf16 v[164:167], a[232:235], a[176:179], v[164:167]\n\t"
"s_mov_b64 exec, s[44:45]\n\t"
"ds_write_b128 v242, v[228:231] offset:3072\n\t"
"s_mov_b64 exec, -1\n\t"
"v_mfma_f32_16x16x32_bf16 v[168:171], a[212:215], a[172:175], v[168:171]\n\t"
"v_mfma_f32_16x16x32_bf16 v[168:171], a[228:231], a[176:179], v[168:171]\n\t"
"v_mfma_f32_16x16x32_bf16 v[168:171], a[232:235], a[180:183], v[168:171]\n\t"
"v_mfma_f32_16x16x32_bf16 v[172:175], a[212:215], a[176:179], v[172:175]\n\t"
"v_mfma_f32_16x16x32_bf16 v[172:175], a[228:231], a[180:183], v[172:175]\n\t"
"v_mfma_f32_16x16x32_bf16 v[172:175], a[232:235], a[184:187], v[172:175]\n\t"
"v_mfma_f32_16x16x32_bf16 v[176:179], a[212:215], a[180:183], v[176:179]\n\t"
"v_mfma_f32_16x16x32_bf16 v[176:179], a[228:231], a[184:187], v[176:179]\n\t"
"v_mfma_f32_16x16x32_bf16 v[176:179], a[232:235], a[188:191], v[176:179]\n\t"
"s_waitcnt lgkmcnt(0)\n\t"
"s_barrier\n\t"
"v_mfma_f32_16x16x32_bf16 v[180:183], a[212:215], a[184:187], v[180:183]\n\t"
"ds_read_b128 a[168:171], v244 offset:0\n\t"
"v_mfma_f32_16x16x32_bf16 v[180:183], a[228:231], a[188:191], v[180:183]\n\t"
"ds_read_b128 a[172:175], v244 offset:512\n\t"
"v_mfma_f32_16x16x32_bf16 v[180:183], a[232:235], a[192:195], v[180:183]\n\t"
"ds_read_b128 a[176:179], v244 offset:1024\n\t"
"ds_read_b128 a[180:183], v244 offset:1536\n\t"
"v_mfma_f32_16x16x32_bf16 v[184:187], a[212:215], a[188:191], v[184:187]\n\t"
"ds_read_b128 a[184:187], v244 offset:2048\n\t"
"v_mfma_f32_16x16x32_bf16 v[184:187], a[228:231], a[192:195], v[184:187]\n\t"
"ds_read_b128 a[188:191], v244 offset:2560\n\t"
"v_mfma_f32_16x16x32_bf16 v[184:187], a[232:235], a[196:199], v[184:187]\n\t"
"ds_write_b128 v249, a[8:11] offset:18432\n\t"
"v_mfma_f32_16x16x32_bf16 v[188:191], a[212:215], a[192:195], v[188:191]\n\t"
"ds_read_b128 a[192:195], v244 offset:3072\n\t"
"v_mfma_f32_16x16x32_bf16 v[188:191], a[228:231], a[196:199], v[188:191]\n\t"
"v_mfma_f32_16x16x32_bf16 v[188:191], a[232:235], a[200:203], v[188:191]\n\t"
"s_mov_b32 s20, 1130496\n\t"
"buffer_load_dwordx4 a[100:103], v252, s[8:11], s20 offen offset:0 sc1\n\t"
"v_mfma_f32_16x16x32_bf16 v[192:195], a[212:215], a[196:199], v[192:195]\n\t"
"ds_read_b128 a[196:199], v244 offset:3584\n\t"
"v_mfma_f32_16x16x32_bf16 v[192:195], a[228:231], a[200:203], v[192:195]\n\t"
"ds_read_b128 a[200:203], v244 offset:4096\n\t"
"v_mfma_f32_16x16x32_bf16 v[192:195], a[232:235], a[204:207], v[192:195]\n\t"
"ds_read_b128 a[204:207], v244 offset:4608\n\t"
"Lst16_%=:\n\t"
"s_waitcnt vmcnt(35) lgkmcnt(8)\n\t"
"v_mfma_f32_16x16x32_bf16 v[0:3], a[156:159], a[168:171], v[0:3]\n\t"
"v_mfma_f32_16x16x32_bf16 v[0:3], a[248:251], a[172:175], v[0:3]\n\t"
"v_mfma_f32_16x16x32_bf16 v[0:3], a[164:167], a[176:179], v[0:3]\n\t"
"buffer_load_dwordx4 a[4:7], v252, s[8:11], s20 offen offset:1024 sc1\n\t"
"s_waitcnt lgkmcnt(7)\n\t"
"v_mfma_f32_16x16x32_bf16 v[4:7], a[156:159], a[172:175], v[4:7]\n\t"
"v_mfma_f32_16x16x32_bf16 v[4:7], a[248:251], a[176:179], v[4:7]\n\t"
"v_mfma_f32_16x16x32_bf16 v[4:7], a[164:167], a[180:183], v[4:7]\n\t"
"buffer_load_dwordx4 v[216:219], v252, s[8:11], s20 offen offset:2048 sc1\n\t"
"s_waitcnt lgkmcnt(6)\n\t"
"v_mfma_f32_16x16x32_bf16 v[8:11], a[156:159], a[176:179], v[8:11]\n\t"
"v_mfma_f32_16x16x32_bf16 v[8:11], a[248:251], a[180:183], v[8:11]\n\t"
"v_mfma_f32_16x16x32_bf16 v[8:11], a[164:167], a[184:187], v[8:11]\n\t"
"ds_write_b64 v250, a[136:137] offset:18432\n\t"
"s_waitcnt lgkmcnt(6)\n\t"
"v_mfma_f32_16x16x32_bf16 v[12:15], a[156:159], a[180:183], v[12:15]\n\t"
"v_mfma_f32_16x16x32_bf16 v[12:15], a[248:251], a[184:187], v[12:15]\n\t"
"v_mfma_f32_16x16x32_bf16 v[12:15], a[164:167], a[188:191], v[12:15]\n\t"
"s_add_u32 m0, s25, 0\n\t"
"s_nop 0\n\t"
"buffer_load_dwordx4 v254, s[12:15], 0 offen offset:0 nt lds\n\t"
"s_mov_b32 s20, 1290240\n\t"
"buffer_load_dwordx4 a[212:215], v252, s[8:11], s20 offen offset:0 sc1\n\t"
"s_waitcnt lgkmcnt(4)\n\t"
"v_mfma_f32_16x16x32_bf16 v[16:19], a[156:159], a[184:187], v[16:19]\n\t"
"v_mfma_f32_16x16x32_bf16 v[16:19], a[248:251], a[188:191], v[16:19]\n\t"
"v_mfma_f32_16x16x32_bf16 v[16:19], a[164:167], a[192:195], v[16:19]\n\t"
"s_add_u32 m0, s26, 0\n\t"
"s_mov_b64 exec, s[36:37]\n\t"
"buffer_load_dwordx4 v254, s[12:15], s27 offen offset:0 nt lds\n\t"
"s_mov_b64 exec, -1\n\t"
"s_waitcnt lgkmcnt(3)\n\t"
"v_mfma_f32_16x16x32_bf16 v[20:23], a[156:159], a[188:191], v[20:23]\n\t"
"ds_read_b128 a[60:63], v252 offset:30720\n\t"
"v_mfma_f32_16x16x32_bf16 v[20:23], a[248:251], a[192:195], v[20:23]\n\t"
"ds_read_b128 a[92:95], v252 offset:31744\n\t"
"v_mfma_f32_16x16x32_bf16 v[20:23], a[164:167], a[196:199], v[20:23]\n\t"
"s_add_u32 m0, s24, 0\n\t"
"s_add_u32 s28, s21, 0\n\t"
"buffer_load_dwordx4 v247, s[12:15], s28 offen offset:0 nt lds\n\t"
"s_add_u32 m0, s24, 6144\n\t"
"s_add_u32 s28, s21, 196608\n\t"
"buffer_load_dwordx4 v247, s[12:15], s28 offen offset:0 nt lds\n\t"
"s_waitcnt lgkmcnt(4)\n\t"
"v_mfma_f32_16x16x32_bf16 v[24:27], a[156:159], a[192:195], v[24:27]\n\t"
"ds_read_b128 v[196:199], v252 offset:32768\n\t"
"v_mfma_f32_16x16x32_bf16 v[24:27], a[248:251], a[196:199], v[24:27]\n\t"
"v_mfma_f32_16x16x32_bf16 v[24:27], a[164:167], a[200:203], v[24:27]\n\t"
"ds_write_b128 v249, a[12:15] offset:24576\n\t"
"s_add_u32 m0, s24, 12288\n\t"
"s_add_u32 s28, s21, 393216\n\t"
"buffer_load_dwordx4 v247, s[12:15], s28 offen offset:0 nt lds\n\t"
"s_waitcnt lgkmcnt(5)\n\t"
"v_mfma_f32_16x16x32_bf16 v[28:31], a[156:159], a[196:199], v[28:31]\n\t"
"v_mfma_f32_16x16x32_bf16 v[28:31], a[248:251], a[200:203], v[28:31]\n\t"
"v_mfma_f32_16x16x32_bf16 v[28:31], a[164:167], a[204:207], v[28:31]\n\t"
"s_add_u32 m0, s24, 19968\n\t"
"s_add_u32 s28, s21, 589824\n\t"
"buffer_load_dwordx4 v247, s[12:15], s28 offen offset:0 nt lds\n\t"
"s_add_u32 m0, s24, 26112\n\t"
"s_add_u32 s28, s21, 786432\n\t"
"buffer_load_dwordx4 v247, s[12:15], s28 offen offset:0 nt lds\n\t"
"s_waitcnt vmcnt(42)\n\t"
"v_mfma_f32_16x16x32_bf16 v[32:35], a[252:255], a[168:171], v[32:35]\n\t"
"v_mfma_f32_16x16x32_bf16 v[32:35], v[96:99], a[172:175], v[32:35]\n\t"
"v_mfma_f32_16x16x32_bf16 v[32:35], a[216:219], a[176:179], v[32:35]\n\t"
"ds_write_b64 v250, a[138:139] offset:24576\n\t"
"v_mfma_f32_16x16x32_bf16 v[36:39], a[252:255], a[172:175], v[36:39]\n\t"
"ds_read_b64_tr_b16 v[236:237], v240 offset:1024\n\t"
"v_mfma_f32_16x16x32_bf16 v[36:39], v[96:99], a[176:179], v[36:39]\n\t"
"ds_read_b64_tr_b16 v[238:239], v240 offset:2560\n\t"
"v_mfma_f32_16x16x32_bf16 v[36:39], a[216:219], a[180:183], v[36:39]\n\t"
"v_mfma_f32_16x16x32_bf16 v[40:43], a[252:255], a[176:179], v[40:43]\n\t"
"v_mfma_f32_16x16x32_bf16 v[40:43], v[96:99], a[180:183], v[40:43]\n\t"
"v_mfma_f32_16x16x32_bf16 v[40:43], a[216:219], a[184:187], v[40:43]\n\t"
"v_mfma_f32_16x16x32_bf16 v[44:47], a[252:255], a[180:183], v[44:47]\n\t"
"v_mfma_f32_16x16x32_bf16 v[44:47], v[96:99], a[184:187], v[44:47]\n\t"
"v_mfma_f32_16x16x32_bf16 v[44:47], a[216:219], a[188:191], v[44:47]\n\t"
"s_waitcnt lgkmcnt(2)\n\t"
"s_barrier\n\t"
"v_mfma_f32_16x16x32_bf16 v[48:51], a[252:255], a[184:187], v[48:51]\n\t"
"ds_read_b64_tr_b16 v[224:225], v241 offset:1024\n\t"
"v_mfma_f32_16x16x32_bf16 v[48:51], v[96:99], a[188:191], v[48:51]\n\t"
"ds_read_b64_tr_b16 v[226:227], v241 offset:2560\n\t"
"v_mfma_f32_16x16x32_bf16 v[48:51], a[216:219], a[192:195], v[48:51]\n\t"
"ds_write_b128 v249, a[16:19] offset:30720\n\t"
"v_mfma_f32_16x16x32_bf16 v[52:55], a[252:255], a[188:191], v[52:55]\n\t"
"v_mfma_f32_16x16x32_bf16 v[52:55], v[96:99], a[192:195], v[52:55]\n\t"
"ds_read_b64_tr_b16 v[232:233], v240 offset:20992\n\t"
"v_mfma_f32_16x16x32_bf16 v[52:55], a[216:219], a[196:199], v[52:55]\n\t"
"ds_read_b64_tr_b16 v[234:235], v240 offset:22528\n\t"
"s_waitcnt lgkmcnt(5)\n\t"
"v_mfma_f32_16x16x32_bf16 v[236:239], v[236:239], a[208:211], 0\n\t"
"v_mfma_f32_16x16x32_bf16 v[56:59], a[252:255], a[192:195], v[56:59]\n\t"
"v_mfma_f32_16x16x32_bf16 v[56:59], v[96:99], a[196:199], v[56:59]\n\t"
"ds_read_b64_tr_b16 v[228:229], v241 offset:20992\n\t"
"v_mfma_f32_16x16x32_bf16 v[56:59], a[216:219], a[200:203], v[56:59]\n\t"
"ds_read_b64_tr_b16 v[230:231], v241 offset:22528\n\t"
"s_waitcnt lgkmcnt(5)\n\t"
"v_mfma_f32_16x16x32_bf16 v[224:227], v[224:227], a[208:211], 0\n\t"
"ds_write_b64 v250, a[144:145] offset:30720\n\t"
"v_mfma_f32_16x16x32_bf16 v[60:63], a[252:255], a[196:199], v[60:63]\n\t"
"v_cvt_pk_bf16_f32 v236, v236, v237\n\t"
"v_mfma_f32_16x16x32_bf16 v[60:63], v[96:99], a[200:203], v[60:63]\n\t"
"v_cvt_pk_bf16_f32 v237, v238, v239\n\t"
"v_mfma_f32_16x16x32_bf16 v[60:63], a[216:219], a[204:207], v[60:63]\n\t"
"s_waitcnt vmcnt(39)\n\t"
"v_mfma_f32_16x16x32_bf16 v[64:67], a[236:239], a[168:171], v[64:67]\n\t"
"ds_read_b128 a[168:171], v244 offset:9216\n\t"
"v_mfma_f32_16x16x32_bf16 v[64:67], a[240:243], a[172:175], v[64:67]\n\t"
"v_cvt_pk_bf16_f32 v238, v224, v225\n\t"
"v_cvt_pk_bf16_f32 v239, v226, v227\n\t"
"v_mfma_f32_16x16x32_bf16 v[64:67], a[220:223], a[176:179], v[64:67]\n\t"
"v_mfma_f32_16x16x32_bf16 v[68:71], a[236:239], a[172:175], v[68:71]\n\t"
"ds_read_b128 a[172:175], v244 offset:9728\n\t"
"v_mfma_f32_16x16x32_bf16 v[68:71], a[240:243], a[176:179], v[68:71]\n\t"
"v_mfma_f32_16x16x32_bf16 v[68:71], a[220:223], a[180:183], v[68:71]\n\t"
"ds_write_b128 v249, a[20:23] offset:36864\n\t"
"s_waitcnt lgkmcnt(6)\n\t"
"v_mfma_f32_16x16x32_bf16 v[232:235], v[232:235], a[208:211], 0\n\t"
"v_mfma_f32_16x16x32_bf16 v[72:75], a[236:239], a[176:179], v[72:75]\n\t"
"ds_read_b128 a[176:179], v244 offset:10240\n\t"
"v_mfma_f32_16x16x32_bf16 v[72:75], a[240:243], a[180:183], v[72:75]\n\t"
"v_mfma_f32_16x16x32_bf16 v[72:75], a[220:223], a[184:187], v[72:75]\n\t"
"s_waitcnt lgkmcnt(5)\n\t"
"v_mfma_f32_16x16x32_bf16 v[228:231], v[228:231], a[208:211], 0\n\t"
"ds_read_b128 v[204:207], v252 offset:18432\n\t"
"v_mfma_f32_16x16x32_bf16 v[76:79], a[236:239], a[180:183], v[76:79]\n\t"
"v_cvt_pk_bf16_f32 v232, v232, v233\n\t"
"v_cvt_pk_bf16_f32 v233, v234, v235\n\t"
"v_mfma_f32_16x16x32_bf16 v[76:79], a[240:243], a[184:187], v[76:79]\n\t"
"ds_read_b128 a[180:183], v244 offset:10752\n\t"
"v_mfma_f32_16x16x32_bf16 v[76:79], a[220:223], a[188:191], v[76:79]\n\t"
"ds_read_b128 v[212:215], v252 offset:19456\n\t"
"v_mfma_f32_16x16x32_bf16 v[80:83], a[236:239], a[184:187], v[80:83]\n\t"
"ds_read_b128 a[184:187], v244 offset:11264\n\t"
"v_mfma_f32_16x16x32_bf16 v[80:83], a[240:243], a[188:191], v[80:83]\n\t"
"v_cvt_pk_bf16_f32 v234, v228, v229\n\t"
"v_cvt_pk_bf16_f32 v235, v230, v231\n\t"
"v_mfma_f32_16x16x32_bf16 v[80:83], a[220:223], a[192:195], v[80:83]\n\t"
"ds_read_b128 a[0:3], v252 offset:20480\n\t"
"v_mfma_f32_16x16x32_bf16 v[84:87], a[236:239], a[188:191], v[84:87]\n\t"
"ds_read_b128 a[188:191], v244 offset:11776\n\t"
"v_mfma_f32_16x16x32_bf16 v[84:87], a[240:243], a[192:195], v[84:87]\n\t"
"v_mfma_f32_16x16x32_bf16 v[84:87], a[220:223], a[196:199], v[84:87]\n\t"
"ds_write_b64 v250, a[146:147] offset:36864\n\t"
"v_mfma_f32_16x16x32_bf16 v[88:91], a[236:239], a[192:195], v[88:91]\n\t"
"ds_read_b128 a[192:195], v244 offset:12288\n\t"
"v_mfma_f32_16x16x32_bf16 v[88:91], a[240:243], a[196:199], v[88:91]\n\t"
"v_mfma_f32_16x16x32_bf16 v[88:91], a[220:223], a[200:203], v[88:91]\n\t"
"v_mfma_f32_16x16x32_bf16 v[92:95], a[236:239], a[196:199], v[92:95]\n\t"
"ds_read_b128 a[196:199], v244 offset:12800\n\t"
"v_mfma_f32_16x16x32_bf16 v[92:95], a[240:243], a[200:203], v[92:95]\n\t"
"ds_read_b128 a[200:203], v244 offset:13312\n\t"
"v_mfma_f32_16x16x32_bf16 v[92:95], a[220:223], a[204:207], v[92:95]\n\t"
"ds_read_b128 a[204:207], v244 offset:13824\n\t"
"s_waitcnt vmcnt(36) lgkmcnt(11)\n\t"
"v_mfma_f32_16x16x32_bf16 v[100:103], a[244:247], a[168:171], v[100:103]\n\t"
"v_mfma_f32_16x16x32_bf16 v[100:103], a[44:47], a[172:175], v[100:103]\n\t"
"v_mfma_f32_16x16x32_bf16 v[100:103], a[104:107], a[176:179], v[100:103]\n\t"
"s_waitcnt lgkmcnt(9)\n\t"
"v_mfma_f32_16x16x32_bf16 v[104:107], a[244:247], a[172:175], v[104:107]\n\t"
"v_mfma_f32_16x16x32_bf16 v[104:107], a[44:47], a[176:179], v[104:107]\n\t"
"v_mfma_f32_16x16x32_bf16 v[104:107], a[104:107], a[180:183], v[104:107]\n\t"
"buffer_load_dwordx4 v[96:99], v252, s[8:11], s20 offen offset:1024 sc1\n\t"
"s_waitcnt lgkmcnt(0)\n\t"
"s_barrier\n\t"
"v_mfma_f32_16x16x32_bf16 v[108:111], a[244:247], a[176:179], v[108:111]\n\t"
"ds_read_b64_tr_b16 v[224:225], v240 offset:40960\n\t"
"v_mfma_f32_16x16x32_bf16 v[108:111], a[44:47], a[180:183], v[108:111]\n\t"
"ds_read_b64_tr_b16 v[226:227], v240 offset:42496\n\t"
"v_mfma_f32_16x16x32_bf16 v[108:111], a[104:107], a[184:187], v[108:111]\n\t"
"s_mov_b64 exec, s[44:45]\n\t"
"ds_write_b128 v242, v[236:239] offset:0\n\t"
"s_mov_b64 exec, -1\n\t"
"v_mfma_f32_16x16x32_bf16 v[112:115], a[244:247], a[180:183], v[112:115]\n\t"
"ds_read_b64_tr_b16 v[228:229], v241 offset:40960\n\t"
"v_mfma_f32_16x16x32_bf16 v[112:115], a[44:47], a[184:187], v[112:115]\n\t"
"ds_read_b64_tr_b16 v[230:231], v241 offset:42496\n\t"
"v_add_u32 v240, 59904, v240\n\t"
"v_mfma_f32_16x16x32_bf16 v[112:115], a[104:107], a[188:191], v[112:115]\n\t"
"s_mov_b64 exec, s[44:45]\n\t"
"ds_write_b128 v242, v[232:235] offset:1536\n\t"
"s_mov_b64 exec, -1\n\t"
"v_mfma_f32_16x16x32_bf16 v[116:119], a[244:247], a[184:187], v[116:119]\n\t"
"v_add_u32 v241, 59904, v241\n\t"
"ds_read_b64_tr_b16 v[236:237], v240 offset:1024\n\t"
"v_mfma_f32_16x16x32_bf16 v[116:119], a[44:47], a[188:191], v[116:119]\n\t"
"ds_read_b64_tr_b16 v[238:239], v240 offset:2560\n\t"
"v_mfma_f32_16x16x32_bf16 v[116:119], a[104:107], a[192:195], v[116:119]\n\t"
"ds_read_b64_tr_b16 v[232:233], v241 offset:1024\n\t"
"ds_read_b64_tr_b16 v[234:235], v241 offset:2560\n\t"
"v_mfma_f32_16x16x32_bf16 v[120:123], a[244:247], a[188:191], v[120:123]\n\t"
"v_mfma_f32_16x16x32_bf16 v[120:123], a[44:47], a[192:195], v[120:123]\n\t"
"v_mfma_f32_16x16x32_bf16 v[120:123], a[104:107], a[196:199], v[120:123]\n\t"
"ds_write_b128 v249, a[24:27] offset:18432\n\t"
"s_waitcnt lgkmcnt(9)\n\t"
"v_mfma_f32_16x16x32_bf16 v[224:227], v[224:227], a[208:211], 0\n\t"
"ds_write_b64 v250, a[152:153] offset:18432\n\t"
"s_waitcnt lgkmcnt(7)\n\t"
"v_mfma_f32_16x16x32_bf16 v[228:231], v[228:231], a[208:211], 0\n\t"
"ds_write_b128 v249, a[28:31] offset:24576\n\t"
"v_mfma_f32_16x16x32_bf16 v[124:127], a[244:247], a[192:195], v[124:127]\n\t"
"v_mfma_f32_16x16x32_bf16 v[124:127], a[44:47], a[196:199], v[124:127]\n\t"
"v_mfma_f32_16x16x32_bf16 v[124:127], a[104:107], a[200:203], v[124:127]\n\t"
"s_add_u32 m0, s24, 32256\n\t"
"s_add_u32 s28, s21, 983040\n\t"
"buffer_load_dwordx4 v247, s[12:15], s28 offen offset:0 nt lds\n\t"
"s_add_u32 m0, s24, 39936\n\t"
"s_add_u32 s28, s21, 1179648\n\t"
"buffer_load_dwordx4 v247, s[12:15], s28 offen offset:0 nt lds\n\t"
"v_mfma_f32_16x16x32_bf16 v[128:131], a[244:247], a[196:199], v[128:131]\n\t"
"v_cvt_pk_bf16_f32 v224, v224, v225\n\t"
"v_mfma_f32_16x16x32_bf16 v[128:131], a[44:47], a[200:203], v[128:131]\n\t"
"v_cvt_pk_bf16_f32 v225, v226, v227\n\t"
"v_cvt_pk_bf16_f32 v226, v228, v229\n\t"
"v_mfma_f32_16x16x32_bf16 v[128:131], a[104:107], a[204:207], v[128:131]\n\t"
"s_add_u32 m0, s24, 46080\n\t"
"s_add_u32 s28, s21, 1376256\n\t"
"buffer_load_dwordx4 v247, s[12:15], s28 offen offset:0 nt lds\n\t"
"s_waitcnt lgkmcnt(5)\n\t"
"v_mfma_f32_16x16x32_bf16 v[236:239], v[236:239], a[208:211], 0\n\t"
"s_add_u32 m0, s24, 52224\n\t"
"s_add_u32 s28, s21, 1572864\n\t"
"buffer_load_dwordx4 v247, s[12:15], s28 offen offset:0 nt lds\n\t"
"s_waitcnt lgkmcnt(3)\n\t"
"v_mfma_f32_16x16x32_bf16 v[232:235], v[232:235], a[208:211], 0\n\t"
"v_cvt_pk_bf16_f32 v227, v230, v231\n\t"
"ds_read_b64_tr_b16 v[228:229], v240 offset:20992\n\t"
"s_waitcnt vmcnt(38)\n\t"
"v_mfma_f32_16x16x32_bf16 v[132:135], a[64:67], a[168:171], v[132:135]\n\t"
"ds_read_b64_tr_b16 v[230:231], v240 offset:22528\n\t"
"v_mfma_f32_16x16x32_bf16 v[132:135], a[52:55], a[172:175], v[132:135]\n\t"
"v_mfma_f32_16x16x32_bf16 v[132:135], v[200:203], a[176:179], v[132:135]\n\t"
"s_add_u32 m0, s24, 99840\n\t"
"s_add_u32 s28, s21, 2949120\n\t"
"buffer_load_dwordx4 v247, s[12:15], s28 offen offset:0 nt lds\n\t"
"s_add_u32 m0, s24, 105984\n\t"
"s_add_u32 s28, s21, 3145728\n\t"
"buffer_load_dwordx4 v247, s[12:15], s28 offen offset:0 nt lds\n\t"
"v_mfma_f32_16x16x32_bf16 v[136:139], a[64:67], a[172:175], v[136:139]\n\t"
"v_cvt_pk_bf16_f32 v236, v236, v237\n\t"
"v_cvt_pk_bf16_f32 v237, v238, v239\n\t"
"v_mfma_f32_16x16x32_bf16 v[136:139], a[52:55], a[176:179], v[136:139]\n\t"
"v_cvt_pk_bf16_f32 v238, v232, v233\n\t"
"v_cvt_pk_bf16_f32 v239, v234, v235\n\t"
"v_mfma_f32_16x16x32_bf16 v[136:139], v[200:203], a[180:183], v[136:139]\n\t"
"s_mov_b64 exec, s[44:45]\n\t"
"ds_write_b128 v242, v[224:227] offset:3072\n\t"
"s_mov_b64 exec, -1\n\t"
"s_add_u32 m0, s24, 112128\n\t"
"s_add_u32 s28, s21, 3342336\n\t"
"buffer_load_dwordx4 v247, s[12:15], s28 offen offset:0 nt lds\n\t"
"ds_read_b64_tr_b16 v[224:225], v241 offset:20992\n\t"
"ds_read_b64_tr_b16 v[226:227], v241 offset:22528\n\t"
"v_mfma_f32_16x16x32_bf16 v[140:143], a[64:67], a[176:179], v[140:143]\n\t"
"ds_read_b64_tr_b16 v[232:233], v240 offset:40960\n\t"
"v_mfma_f32_16x16x32_bf16 v[140:143], a[52:55], a[180:183], v[140:143]\n\t"
"ds_read_b64_tr_b16 v[234:235], v240 offset:42496\n\t"
"v_mfma_f32_16x16x32_bf16 v[140:143], v[200:203], a[184:187], v[140:143]\n\t"
"s_mov_b64 exec, s[44:45]\n\t"
"ds_write_b128 v242, v[236:239] offset:4608\n\t"
"s_mov_b64 exec, -1\n\t"
"v_mfma_f32_16x16x32_bf16 v[144:147], a[64:67], a[180:183], v[144:147]\n\t"
"ds_read_b64_tr_b16 v[236:237], v241 offset:40960\n\t"
"v_mfma_f32_16x16x32_bf16 v[144:147], a[52:55], a[184:187], v[144:147]\n\t"
"ds_read_b64_tr_b16 v[238:239], v241 offset:42496\n\t"
"v_mfma_f32_16x16x32_bf16 v[144:147], v[200:203], a[188:191], v[144:147]\n\t"
"ds_write_b64 v250, a[154:155] offset:24576\n\t"
"s_waitcnt lgkmcnt(9)\n\t"
"v_mfma_f32_16x16x32_bf16 v[228:231], v[228:231], a[208:211], 0\n\t"
"s_waitcnt lgkmcnt(6)\n\t"
"v_mfma_f32_16x16x32_bf16 v[224:227], v[224:227], a[208:211], 0\n\t"
"v_mfma_f32_16x16x32_bf16 v[148:151], a[64:67], a[184:187], v[148:151]\n\t"
"v_mfma_f32_16x16x32_bf16 v[148:151], a[52:55], a[188:191], v[148:151]\n\t"
"v_mfma_f32_16x16x32_bf16 v[148:151], v[200:203], a[192:195], v[148:151]\n\t"
"v_mfma_f32_16x16x32_bf16 v[152:155], a[64:67], a[188:191], v[152:155]\n\t"
"s_nop 1\n\t"
"v_cvt_pk_bf16_f32 v228, v228, v229\n\t"
"v_cvt_pk_bf16_f32 v229, v230, v231\n\t"
"v_mfma_f32_16x16x32_bf16 v[152:155], a[52:55], a[192:195], v[152:155]\n\t"
"v_cvt_pk_bf16_f32 v230, v224, v225\n\t"
"v_mfma_f32_16x16x32_bf16 v[152:155], v[200:203], a[196:199], v[152:155]\n\t"
"v_cvt_pk_bf16_f32 v231, v226, v227\n\t"
"s_waitcnt lgkmcnt(4)\n\t"
"v_mfma_f32_16x16x32_bf16 v[232:235], v[232:235], a[208:211], 0\n\t"
"s_mov_b64 exec, s[44:45]\n\t"
"ds_write_b128 v242, v[228:231] offset:6144\n\t"
"s_mov_b64 exec, -1\n\t"
"s_waitcnt lgkmcnt(2)\n\t"
"v_mfma_f32_16x16x32_bf16 v[236:239], v[236:239], a[208:211], 0\n\t"
"ds_read_b128 a[8:11], v252 offset:30720\n\t"
"v_mfma_f32_16x16x32_bf16 v[156:159], a[64:67], a[192:195], v[156:159]\n\t"
"v_mfma_f32_16x16x32_bf16 v[156:159], a[52:55], a[196:199], v[156:159]\n\t"
"ds_read_b128 a[16:19], v252 offset:31744\n\t"
"v_mfma_f32_16x16x32_bf16 v[156:159], v[200:203], a[200:203], v[156:159]\n\t"
"v_mfma_f32_16x16x32_bf16 v[160:163], a[64:67], a[196:199], v[160:163]\n\t"
"v_cvt_pk_bf16_f32 v232, v232, v233\n\t"
"v_cvt_pk_bf16_f32 v233, v234, v235\n\t"
"v_mfma_f32_16x16x32_bf16 v[160:163], a[52:55], a[200:203], v[160:163]\n\t"
"v_cvt_pk_bf16_f32 v234, v236, v237\n\t"
"v_mfma_f32_16x16x32_bf16 v[160:163], v[200:203], a[204:207], v[160:163]\n\t"
"v_cvt_pk_bf16_f32 v235, v238, v239\n\t"
"ds_read_b128 a[104:107], v252 offset:32768\n\t"
"s_waitcnt vmcnt(35)\n\t"
"v_mfma_f32_16x16x32_bf16 v[164:167], a[72:75], a[168:171], v[164:167]\n\t"
"v_mfma_f32_16x16x32_bf16 v[164:167], a[80:83], a[172:175], v[164:167]\n\t"
"v_mfma_f32_16x16x32_bf16 v[164:167], a[68:71], a[176:179], v[164:167]\n\t"
"s_mov_b64 exec, s[44:45]\n\t"
"ds_write_b128 v242, v[232:235] offset:7680\n\t"
"s_mov_b64 exec, -1\n\t"
"v_mfma_f32_16x16x32_bf16 v[168:171], a[72:75], a[172:175], v[168:171]\n\t"
"v_mfma_f32_16x16x32_bf16 v[168:171], a[80:83], a[176:179], v[168:171]\n\t"
"v_mfma_f32_16x16x32_bf16 v[168:171], a[68:71], a[180:183], v[168:171]\n\t"
"v_mfma_f32_16x16x32_bf16 v[172:175], a[72:75], a[176:179], v[172:175]\n\t"
"v_mfma_f32_16x16x32_bf16 v[172:175], a[80:83], a[180:183], v[172:175]\n\t"
"v_mfma_f32_16x16x32_bf16 v[172:175], a[68:71], a[184:187], v[172:175]\n\t"
"v_mfma_f32_16x16x32_bf16 v[176:179], a[72:75], a[180:183], v[176:179]\n\t"
"v_mfma_f32_16x16x32_bf16 v[176:179], a[80:83], a[184:187], v[176:179]\n\t"
"v_mfma_f32_16x16x32_bf16 v[176:179], a[68:71], a[188:191], v[176:179]\n\t"
"s_waitcnt lgkmcnt(0)\n\t"
"s_barrier\n\t"
"v_mfma_f32_16x16x32_bf16 v[180:183], a[72:75], a[184:187], v[180:183]\n\t"
"ds_read_b128 a[168:171], v244 offset:0\n\t"
"v_mfma_f32_16x16x32_bf16 v[180:183], a[80:83], a[188:191], v[180:183]\n\t"
"ds_read_b128 a[172:175], v244 offset:512\n\t"
"v_mfma_f32_16x16x32_bf16 v[180:183], a[68:71], a[192:195], v[180:183]\n\t"
"ds_read_b128 a[176:179], v244 offset:1024\n\t"
"ds_read_b128 a[180:183], v244 offset:1536\n\t"
"v_mfma_f32_16x16x32_bf16 v[184:187], a[72:75], a[188:191], v[184:187]\n\t"
"ds_read_b128 a[184:187], v244 offset:2048\n\t"
"v_mfma_f32_16x16x32_bf16 v[184:187], a[80:83], a[192:195], v[184:187]\n\t"
"ds_read_b128 a[188:191], v244 offset:2560\n\t"
"v_mfma_f32_16x16x32_bf16 v[184:187], a[68:71], a[196:199], v[184:187]\n\t"
"ds_write_b128 v249, a[32:35] offset:30720\n\t"
"v_mfma_f32_16x16x32_bf16 v[188:191], a[72:75], a[192:195], v[188:191]\n\t"
"ds_read_b128 a[192:195], v244 offset:3072\n\t"
"v_mfma_f32_16x16x32_bf16 v[188:191], a[80:83], a[196:199], v[188:191]\n\t"
"v_mfma_f32_16x16x32_bf16 v[188:191], a[68:71], a[200:203], v[188:191]\n\t"
"ds_write_b64 v250, a[160:161] offset:30720\n\t"
"v_mfma_f32_16x16x32_bf16 v[192:195], a[72:75], a[196:199], v[192:195]\n\t"
"ds_read_b128 a[196:199], v244 offset:3584\n\t"
"v_mfma_f32_16x16x32_bf16 v[192:195], a[80:83], a[200:203], v[192:195]\n\t"
"ds_read_b128 a[200:203], v244 offset:4096\n\t"
"v_mfma_f32_16x16x32_bf16 v[192:195], a[68:71], a[204:207], v[192:195]\n\t"
"ds_read_b128 a[204:207], v244 offset:4608\n\t"
"Lst17_%=:\n\t"
"s_waitcnt vmcnt(30) lgkmcnt(9)\n\t"
"v_mfma_f32_16x16x32_bf16 v[0:3], v[208:211], a[168:171], v[0:3]\n\t"
"v_mfma_f32_16x16x32_bf16 v[0:3], a[88:91], a[172:175], v[0:3]\n\t"
"v_mfma_f32_16x16x32_bf16 v[0:3], a[96:99], a[176:179], v[0:3]\n\t"
"ds_write_b128 v249, a[36:39] offset:36864\n\t"
"s_waitcnt lgkmcnt(9)\n\t"
"v_mfma_f32_16x16x32_bf16 v[4:7], v[208:211], a[172:175], v[4:7]\n\t"
"v_mfma_f32_16x16x32_bf16 v[4:7], a[88:91], a[176:179], v[4:7]\n\t"
"v_mfma_f32_16x16x32_bf16 v[4:7], a[96:99], a[180:183], v[4:7]\n\t"
"s_waitcnt lgkmcnt(8)\n\t"
"v_mfma_f32_16x16x32_bf16 v[8:11], v[208:211], a[176:179], v[8:11]\n\t"
"s_nop 0\n\t"
"v_cvt_pk_bf16_f32 v0, v0, v1\n\t"
"v_cvt_pk_bf16_f32 v2, v2, v3\n\t"
"v_mfma_f32_16x16x32_bf16 v[8:11], a[88:91], a[180:183], v[8:11]\n\t"
"v_mfma_f32_16x16x32_bf16 v[8:11], a[96:99], a[184:187], v[8:11]\n\t"
"v_accvgpr_write_b32 a40, v0\n\t"
"s_waitcnt lgkmcnt(7)\n\t"
"v_mfma_f32_16x16x32_bf16 v[12:15], v[208:211], a[180:183], v[12:15]\n\t"
"v_mfma_f32_16x16x32_bf16 v[12:15], a[88:91], a[184:187], v[12:15]\n\t"
"v_cvt_pk_bf16_f32 v4, v4, v5\n\t"
"v_mfma_f32_16x16x32_bf16 v[12:15], a[96:99], a[188:191], v[12:15]\n\t"
"s_waitcnt lgkmcnt(5)\n\t"
"v_mfma_f32_16x16x32_bf16 v[16:19], v[208:211], a[184:187], v[16:19]\n\t"
"v_cvt_pk_bf16_f32 v6, v6, v7\n\t"
"v_mfma_f32_16x16x32_bf16 v[16:19], a[88:91], a[188:191], v[16:19]\n\t"
"v_accvgpr_write_b32 a41, v2\n\t"
"v_mfma_f32_16x16x32_bf16 v[16:19], a[96:99], a[192:195], v[16:19]\n\t"
"s_waitcnt lgkmcnt(3)\n\t"
"v_mfma_f32_16x16x32_bf16 v[20:23], v[208:211], a[188:191], v[20:23]\n\t"
"ds_read_b128 a[24:27], v252 offset:18432\n\t"
"v_cvt_pk_bf16_f32 v8, v8, v9\n\t"
"v_mfma_f32_16x16x32_bf16 v[20:23], a[88:91], a[192:195], v[20:23]\n\t"
"ds_read_b128 a[112:115], v252 offset:19456\n\t"
"v_mfma_f32_16x16x32_bf16 v[20:23], a[96:99], a[196:199], v[20:23]\n\t"
"v_cvt_pk_bf16_f32 v10, v10, v11\n\t"
"s_waitcnt lgkmcnt(4)\n\t"
"v_mfma_f32_16x16x32_bf16 v[24:27], v[208:211], a[192:195], v[24:27]\n\t"
"ds_read_b128 a[120:123], v252 offset:20480\n\t"
"v_mfma_f32_16x16x32_bf16 v[24:27], a[88:91], a[196:199], v[24:27]\n\t"
"v_accvgpr_write_b32 a48, v4\n\t"
"v_accvgpr_write_b32 a49, v6\n\t"
"v_mfma_f32_16x16x32_bf16 v[24:27], a[96:99], a[200:203], v[24:27]\n\t"
"v_cvt_pk_bf16_f32 v12, v12, v13\n\t"
"s_waitcnt lgkmcnt(4)\n\t"
"v_mfma_f32_16x16x32_bf16 v[28:31], v[208:211], a[196:199], v[28:31]\n\t"
"v_cvt_pk_bf16_f32 v14, v14, v15\n\t"
"v_accvgpr_write_b32 a56, v8\n\t"
"v_mfma_f32_16x16x32_bf16 v[28:31], a[88:91], a[200:203], v[28:31]\n\t"
"v_accvgpr_write_b32 a57, v10\n\t"
"v_mfma_f32_16x16x32_bf16 v[28:31], a[96:99], a[204:207], v[28:31]\n\t"
"ds_write_b64 v250, a[162:163] offset:36864\n\t"
"s_waitcnt vmcnt(26)\n\t"
"v_mfma_f32_16x16x32_bf16 v[32:35], a[76:79], a[168:171], v[32:35]\n\t"
"v_cvt_pk_bf16_f32 v16, v16, v17\n\t"
"v_cvt_pk_bf16_f32 v18, v18, v19\n\t"
"v_mfma_f32_16x16x32_bf16 v[32:35], a[108:111], a[172:175], v[32:35]\n\t"
"v_accvgpr_write_b32 a64, v12\n\t"
"v_accvgpr_write_b32 a65, v14\n\t"
"v_mfma_f32_16x16x32_bf16 v[32:35], a[84:87], a[176:179], v[32:35]\n\t"
"buffer_load_dwordx4 a[144:147], v252, s[8:11], s20 offen offset:2048 sc1\n\t"
"v_mfma_f32_16x16x32_bf16 v[36:39], a[76:79], a[172:175], v[36:39]\n\t"
"v_cvt_pk_bf16_f32 v20, v20, v21\n\t"
"v_cvt_pk_bf16_f32 v22, v22, v23\n\t"
"v_mfma_f32_16x16x32_bf16 v[36:39], a[108:111], a[176:179], v[36:39]\n\t"
"v_accvgpr_write_b32 a72, v16\n\t"
"v_accvgpr_write_b32 a73, v18\n\t"
"v_mfma_f32_16x16x32_bf16 v[36:39], a[84:87], a[180:183], v[36:39]\n\t"
"v_cvt_pk_bf16_f32 v24, v24, v25\n\t"
"v_mfma_f32_16x16x32_bf16 v[40:43], a[76:79], a[176:179], v[40:43]\n\t"
"v_cvt_pk_bf16_f32 v26, v26, v27\n\t"
"v_mfma_f32_16x16x32_bf16 v[40:43], a[108:111], a[180:183], v[40:43]\n\t"
"v_accvgpr_write_b32 a80, v20\n\t"
"v_accvgpr_write_b32 a81, v22\n\t"
"v_mfma_f32_16x16x32_bf16 v[40:43], a[84:87], a[184:187], v[40:43]\n\t"
"v_cvt_pk_bf16_f32 v28, v28, v29\n\t"
"v_mfma_f32_16x16x32_bf16 v[44:47], a[76:79], a[180:183], v[44:47]\n\t"
"v_cvt_pk_bf16_f32 v30, v30, v31\n\t"
"v_mfma_f32_16x16x32_bf16 v[44:47], a[108:111], a[184:187], v[44:47]\n\t"
"v_accvgpr_write_b32 a88, v24\n\t"
"v_accvgpr_write_b32 a89, v26\n\t"
"v_mfma_f32_16x16x32_bf16 v[44:47], a[84:87], a[188:191], v[44:47]\n\t"
"v_cvt_pk_bf16_f32 v32, v32, v33\n\t"
"s_waitcnt lgkmcnt(0)\n\t"
"s_waitcnt vmcnt(14)\n\t"
"s_barrier\n\t"
"v_mfma_f32_16x16x32_bf16 v[48:51], a[76:79], a[184:187], v[48:51]\n\t"
"ds_read_b64_tr_b16 v[224:225], v240 offset:0\n\t"
"v_cvt_pk_bf16_f32 v34, v34, v35\n\t"
"v_mfma_f32_16x16x32_bf16 v[48:51], a[108:111], a[188:191], v[48:51]\n\t"
"ds_read_b64_tr_b16 v[226:227], v240 offset:1536\n\t"
"v_accvgpr_write_b32 a96, v28\n\t"
"v_mfma_f32_16x16x32_bf16 v[48:51], a[84:87], a[192:195], v[48:51]\n\t"
"ds_read_b64_tr_b16 v[228:229], v241 offset:0\n\t"
"ds_read_b64_tr_b16 v[230:231], v241 offset:1536\n\t"
"v_mfma_f32_16x16x32_bf16 v[52:55], a[76:79], a[188:191], v[52:55]\n\t"
"v_accvgpr_write_b32 a97, v30\n\t"
"v_cvt_pk_bf16_f32 v36, v36, v37\n\t"
"v_mfma_f32_16x16x32_bf16 v[52:55], a[108:111], a[192:195], v[52:55]\n\t"
"v_cvt_pk_bf16_f32 v38, v38, v39\n\t"
"v_accvgpr_write_b32 a42, v32\n\t"
"v_mfma_f32_16x16x32_bf16 v[52:55], a[84:87], a[196:199], v[52:55]\n\t"
"v_accvgpr_write_b32 a43, v34\n\t"
"v_cvt_pk_bf16_f32 v40, v40, v41\n\t"
"v_mfma_f32_16x16x32_bf16 v[56:59], a[76:79], a[192:195], v[56:59]\n\t"
"v_cvt_pk_bf16_f32 v42, v42, v43\n\t"
"v_mfma_f32_16x16x32_bf16 v[56:59], a[108:111], a[196:199], v[56:59]\n\t"
"ds_read_b64_tr_b16 v[236:237], v240 offset:19968\n\t"
"v_accvgpr_write_b32 a50, v36\n\t"
"v_mfma_f32_16x16x32_bf16 v[56:59], a[84:87], a[200:203], v[56:59]\n\t"
"ds_read_b64_tr_b16 v[238:239], v240 offset:21504\n\t"
"s_waitcnt lgkmcnt(4)\n\t"
"v_mfma_f32_16x16x32_bf16 v[224:227], v[224:227], a[208:211], 0\n\t"
"v_accvgpr_write_b32 a51, v38\n\t"
"v_cvt_pk_bf16_f32 v44, v44, v45\n\t"
"v_mfma_f32_16x16x32_bf16 v[60:63], a[76:79], a[196:199], v[60:63]\n\t"
"v_cvt_pk_bf16_f32 v46, v46, v47\n\t"
"v_mfma_f32_16x16x32_bf16 v[60:63], a[108:111], a[200:203], v[60:63]\n\t"
"ds_read_b64_tr_b16 v[232:233], v241 offset:19968\n\t"
"v_accvgpr_write_b32 a58, v40\n\t"
"v_mfma_f32_16x16x32_bf16 v[60:63], a[84:87], a[204:207], v[60:63]\n\t"
"ds_read_b64_tr_b16 v[234:235], v241 offset:21504\n\t"
"v_accvgpr_write_b32 a59, v42\n\t"
"s_waitcnt lgkmcnt(4)\n\t"
"v_mfma_f32_16x16x32_bf16 v[228:231], v[228:231], a[208:211], 0\n\t"
"v_cvt_pk_bf16_f32 v48, v48, v49\n\t"
"v_cvt_pk_bf16_f32 v50, v50, v51\n\t"
"v_mfma_f32_16x16x32_bf16 v[64:67], a[100:103], a[168:171], v[64:67]\n\t"
"v_cvt_pk_bf16_f32 v224, v224, v225\n\t"
"v_cvt_pk_bf16_f32 v225, v226, v227\n\t"
"v_mfma_f32_16x16x32_bf16 v[64:67], a[4:7], a[172:175], v[64:67]\n\t"
"v_accvgpr_write_b32 a66, v44\n\t"
"v_accvgpr_write_b32 a67, v46\n\t"
"v_mfma_f32_16x16x32_bf16 v[64:67], v[216:219], a[176:179], v[64:67]\n\t"
"s_add_u32 s29, s22, 393600\n\t"
"buffer_store_dwordx4 a[116:119], v248, s[16:19], s29 offen offset:128 nt\n\t"
"v_mfma_f32_16x16x32_bf16 v[68:71], a[100:103], a[172:175], v[68:71]\n\t"
"v_cvt_pk_bf16_f32 v52, v52, v53\n\t"
"v_cvt_pk_bf16_f32 v54, v54, v55\n\t"
"v_mfma_f32_16x16x32_bf16 v[68:71], a[4:7], a[176:179], v[68:71]\n\t"
"v_cvt_pk_bf16_f32 v226, v228, v229\n\t"
"v_cvt_pk_bf16_f32 v227, v230, v231\n\t"
"v_mfma_f32_16x16x32_bf16 v[68:71], v[216:219], a[180:183], v[68:71]\n\t"
"v_accvgpr_write_b32 a74, v48\n\t"
"v_accvgpr_write_b32 a75, v50\n\t"
"v_mfma_f32_16x16x32_bf16 v[72:75], a[100:103], a[176:179], v[72:75]\n\t"
"ds_read_b128 a[168:171], v244 offset:9216\n\t"
"v_cvt_pk_bf16_f32 v56, v56, v57\n\t"
"v_mfma_f32_16x16x32_bf16 v[72:75], a[4:7], a[180:183], v[72:75]\n\t"
"ds_read_b128 a[172:175], v244 offset:9728\n\t"
"v_cvt_pk_bf16_f32 v58, v58, v59\n\t"
"v_mfma_f32_16x16x32_bf16 v[72:75], v[216:219], a[184:187], v[72:75]\n\t"
"ds_read_b128 a[176:179], v244 offset:10240\n\t"
"v_accvgpr_write_b32 a82, v52\n\t"
"s_waitcnt lgkmcnt(5)\n\t"
"v_mfma_f32_16x16x32_bf16 v[236:239], v[236:239], a[208:211], 0\n\t"
"v_accvgpr_write_b32 a83, v54\n\t"
"v_accvgpr_write_b32 a90, v56\n\t"
"v_mfma_f32_16x16x32_bf16 v[76:79], a[100:103], a[180:183], v[76:79]\n\t"
"ds_read_b128 a[180:183], v244 offset:10752\n\t"
"v_accvgpr_write_b32 a91, v58\n\t"
"v_mfma_f32_16x16x32_bf16 v[76:79], a[4:7], a[184:187], v[76:79]\n\t"
"v_cvt_pk_bf16_f32 v60, v60, v61\n\t"
"v_cvt_pk_bf16_f32 v62, v62, v63\n\t"
"v_mfma_f32_16x16x32_bf16 v[76:79], v[216:219], a[188:191], v[76:79]\n\t"
"s_mov_b32 s20, 1302528\n\t"
"buffer_load_dwordx4 a[36:39], v252, s[8:11], s20 offen offset:0 sc1\n\t"
"s_waitcnt lgkmcnt(4)\n\t"
"v_mfma_f32_16x16x32_bf16 v[232:235], v[232:235], a[208:211], 0\n\t"
"ds_read_b128 a[32:35], v252 offset:30720\n\t"
"v_accvgpr_write_b32 a98, v60\n\t"
"v_mfma_f32_16x16x32_bf16 v[80:83], a[100:103], a[184:187], v[80:83]\n\t"
"v_cvt_pk_bf16_f32 v236, v236, v237\n\t"
"v_cvt_pk_bf16_f32 v237, v238, v239\n\t"
"v_mfma_f32_16x16x32_bf16 v[80:83], a[4:7], a[188:191], v[80:83]\n\t"
"ds_read_b128 a[184:187], v244 offset:11264\n\t"
"v_accvgpr_write_b32 a99, v62\n\t"
"v_mfma_f32_16x16x32_bf16 v[80:83], v[216:219], a[192:195], v[80:83]\n\t"
"buffer_load_dwordx4 a[160:163], v252, s[8:11], s20 offen offset:1024 sc1\n\t"
"s_add_u32 s29, s22, 393600\n\t"
"buffer_store_dwordx4 a[148:151], v248, s[16:19], s29 offen offset:256 nt\n\t"
"v_mfma_f32_16x16x32_bf16 v[84:87], a[100:103], a[188:191], v[84:87]\n\t"
"ds_read_b128 a[188:191], v244 offset:11776\n\t"
"v_cvt_pk_bf16_f32 v64, v64, v65\n\t"
"v_mfma_f32_16x16x32_bf16 v[84:87], a[4:7], a[192:195], v[84:87]\n\t"
"v_cvt_pk_bf16_f32 v238, v232, v233\n\t"
"v_cvt_pk_bf16_f32 v239, v234, v235\n\t"
"v_mfma_f32_16x16x32_bf16 v[84:87], v[216:219], a[196:199], v[84:87]\n\t"
"buffer_load_dwordx4 a[152:155], v252, s[8:11], s20 offen offset:2048 sc1\n\t"
"v_cvt_pk_bf16_f32 v66, v66, v67\n\t"
"ds_read_b128 a[128:131], v252 offset:31744\n\t"
"v_mfma_f32_16x16x32_bf16 v[88:91], a[100:103], a[192:195], v[88:91]\n\t"
"ds_read_b128 a[136:139], v252 offset:32768\n\t"
"v_cvt_pk_bf16_f32 v68, v68, v69\n\t"
"v_mfma_f32_16x16x32_bf16 v[88:91], a[4:7], a[196:199], v[88:91]\n\t"
"ds_read_b128 a[192:195], v244 offset:12288\n\t"
"v_cvt_pk_bf16_f32 v70, v70, v71\n\t"
"v_mfma_f32_16x16x32_bf16 v[88:91], v[216:219], a[200:203], v[88:91]\n\t"
"s_mov_b32 s20, 1314816\n\t"
"buffer_load_dwordx4 v[200:203], v252, s[8:11], s20 offen offset:0 sc1\n\t"
"v_accvgpr_write_b32 a108, v64\n\t"
"v_accvgpr_write_b32 a109, v66\n\t"
"v_mfma_f32_16x16x32_bf16 v[92:95], a[100:103], a[196:199], v[92:95]\n\t"
"ds_read_b128 a[196:199], v244 offset:12800\n\t"
"v_cvt_pk_bf16_f32 v72, v72, v73\n\t"
"v_mfma_f32_16x16x32_bf16 v[92:95], a[4:7], a[200:203], v[92:95]\n\t"
"ds_read_b128 a[200:203], v244 offset:13312\n\t"
"v_cvt_pk_bf16_f32 v74, v74, v75\n\t"
"v_mfma_f32_16x16x32_bf16 v[92:95], v[216:219], a[204:207], v[92:95]\n\t"
"ds_read_b128 a[204:207], v244 offset:13824\n\t"
"buffer_load_dwordx4 a[4:7], v252, s[8:11], s20 offen offset:1024 sc1\n\t"
"v_accvgpr_write_b32 a116, v68\n\t"
"s_waitcnt vmcnt(7) lgkmcnt(10)\n\t"
"v_mfma_f32_16x16x32_bf16 v[100:103], a[212:215], a[168:171], v[100:103]\n\t"
"v_accvgpr_write_b32 a117, v70\n\t"
"v_accvgpr_write_b32 a124, v72\n\t"
"v_mfma_f32_16x16x32_bf16 v[100:103], v[96:99], a[172:175], v[100:103]\n\t"
"v_accvgpr_write_b32 a125, v74\n\t"
"v_cvt_pk_bf16_f32 v76, v76, v77\n\t"
"v_mfma_f32_16x16x32_bf16 v[100:103], a[144:147], a[176:179], v[100:103]\n\t"
"buffer_load_dwordx4 v[208:211], v252, s[8:11], s20 offen offset:2048 sc1\n\t"
"s_waitcnt lgkmcnt(9)\n\t"
"v_mfma_f32_16x16x32_bf16 v[104:107], a[212:215], a[172:175], v[104:107]\n\t"
"v_cvt_pk_bf16_f32 v78, v78, v79\n\t"
"v_accvgpr_write_b32 a132, v76\n\t"
"v_mfma_f32_16x16x32_bf16 v[104:107], v[96:99], a[176:179], v[104:107]\n\t"
"v_accvgpr_write_b32 a133, v78\n\t"
"v_cvt_pk_bf16_f32 v80, v80, v81\n\t"
"v_mfma_f32_16x16x32_bf16 v[104:107], a[144:147], a[180:183], v[104:107]\n\t"
"s_add_u32 s29, s22, 590208\n\t"
"buffer_store_dwordx4 a[60:63], v248, s[16:19], s29 offen offset:0 nt\n\t"
"s_mov_b32 s20, 0\n\t"
"buffer_load_dwordx4 v[216:219], v252, s[8:11], s20 offen offset:0 sc1\n\t"
"s_waitcnt lgkmcnt(0)\n\t"
"s_barrier\n\t"
"v_mfma_f32_16x16x32_bf16 v[108:111], a[212:215], a[176:179], v[108:111]\n\t"
"ds_read_b64_tr_b16 v[228:229], v240 offset:39936\n\t"
"v_cvt_pk_bf16_f32 v82, v82, v83\n\t"
"v_mfma_f32_16x16x32_bf16 v[108:111], v[96:99], a[180:183], v[108:111]\n\t"
"ds_read_b64_tr_b16 v[230:231], v240 offset:41472\n\t"
"v_cvt_pk_bf16_f32 v84, v84, v85\n\t"
"v_mfma_f32_16x16x32_bf16 v[108:111], a[144:147], a[184:187], v[108:111]\n\t"
"s_mov_b64 exec, s[44:45]\n\t"
"ds_write_b128 v242, v[224:227] offset:4608\n\t"
"s_mov_b64 exec, -1\n\t"
"s_mov_b64 exec, s[44:45]\n\t"
"ds_write_b128 v242, v[236:239] offset:6144\n\t"
"s_mov_b64 exec, -1\n\t"
"v_mfma_f32_16x16x32_bf16 v[112:115], a[212:215], a[180:183], v[112:115]\n\t"
"ds_read_b64_tr_b16 v[232:233], v241 offset:39936\n\t"
"v_cvt_pk_bf16_f32 v86, v86, v87\n\t"
"v_mfma_f32_16x16x32_bf16 v[112:115], v[96:99], a[184:187], v[112:115]\n\t"
"ds_read_b64_tr_b16 v[234:235], v241 offset:41472\n\t"
"v_accvgpr_write_b32 a140, v80\n\t"
"v_mfma_f32_16x16x32_bf16 v[112:115], a[144:147], a[188:191], v[112:115]\n\t"
"buffer_load_dwordx4 a[12:15], v252, s[8:11], s20 offen offset:1024 sc1\n\t"
"v_accvgpr_write_b32 a141, v82\n\t"
"v_subrev_u32 v240, 59904, v240\n\t"
"v_mfma_f32_16x16x32_bf16 v[116:119], a[212:215], a[184:187], v[116:119]\n\t"
"v_subrev_u32 v241, 59904, v241\n\t"
"v_cvt_pk_bf16_f32 v88, v88, v89\n\t"
"v_mfma_f32_16x16x32_bf16 v[116:119], v[96:99], a[188:191], v[116:119]\n\t"
"v_cvt_pk_bf16_f32 v90, v90, v91\n\t"
"v_accvgpr_write_b32 a148, v84\n\t"
"v_mfma_f32_16x16x32_bf16 v[116:119], a[144:147], a[192:195], v[116:119]\n\t"
"buffer_load_dwordx4 a[20:23], v252, s[8:11], s20 offen offset:2048 sc1\n\t"
"v_accvgpr_write_b32 a149, v86\n\t"
"ds_read_b64_tr_b16 v[224:225], v240 offset:0\n\t"
"v_mfma_f32_16x16x32_bf16 v[120:123], a[212:215], a[188:191], v[120:123]\n\t"
"ds_read_b64_tr_b16 v[226:227], v240 offset:1536\n\t"
"v_cvt_pk_bf16_f32 v92, v92, v93\n\t"
"v_mfma_f32_16x16x32_bf16 v[120:123], v[96:99], a[192:195], v[120:123]\n\t"
"ds_read_b64_tr_b16 v[236:237], v241 offset:0\n\t"
"v_cvt_pk_bf16_f32 v94, v94, v95\n\t"
"v_mfma_f32_16x16x32_bf16 v[120:123], a[144:147], a[196:199], v[120:123]\n\t"
"s_mov_b32 s20, 12288\n\t"
"buffer_load_dwordx4 a[28:31], v252, s[8:11], s20 offen offset:0 sc1\n\t"
"ds_read_b64_tr_b16 v[238:239], v241 offset:1536\n\t"
"v_accvgpr_write_b32 a156, v88\n\t"
"s_waitcnt lgkmcnt(8)\n\t"
"v_mfma_f32_16x16x32_bf16 v[228:231], v[228:231], a[208:211], 0\n\t"
"ds_write_b128 v249, a[40:43] offset:18432\n\t"
"v_accvgpr_write_b32 a157, v90\n\t"
"v_cvt_pk_bf16_f32 v100, v100, v101\n\t"
"s_waitcnt lgkmcnt(5)\n\t"
"v_mfma_f32_16x16x32_bf16 v[232:235], v[232:235], a[208:211], 0\n\t"
"buffer_load_dwordx4 a[216:219], v252, s[8:11], s20 offen offset:1024 sc1\n\t"
"v_cvt_pk_bf16_f32 v102, v102, v103\n\t"
"v_accvgpr_write_b32 a164, v92\n\t"
"v_mfma_f32_16x16x32_bf16 v[124:127], a[212:215], a[192:195], v[124:127]\n\t"
"v_accvgpr_write_b32 a165, v94\n\t"
"v_cvt_pk_bf16_f32 v104, v104, v105\n\t"
"v_mfma_f32_16x16x32_bf16 v[124:127], v[96:99], a[196:199], v[124:127]\n\t"
"v_cvt_pk_bf16_f32 v106, v106, v107\n\t"
"v_accvgpr_write_b32 a44, v100\n\t"
"v_mfma_f32_16x16x32_bf16 v[124:127], a[144:147], a[200:203], v[124:127]\n\t"
"s_add_u32 s29, s22, 590208\n\t"
"buffer_store_dwordx4 a[92:95], v248, s[16:19], s29 offen offset:128 nt\n\t"
"ds_write_b128 v249, a[56:59] offset:30720\n\t"
"v_mfma_f32_16x16x32_bf16 v[128:131], a[212:215], a[196:199], v[128:131]\n\t"
"v_cvt_pk_bf16_f32 v228, v228, v229\n\t"
"v_cvt_pk_bf16_f32 v229, v230, v231\n\t"
"v_mfma_f32_16x16x32_bf16 v[128:131], v[96:99], a[200:203], v[128:131]\n\t"
"v_accvgpr_write_b32 a45, v102\n\t"
"v_cvt_pk_bf16_f32 v230, v232, v233\n\t"
"v_mfma_f32_16x16x32_bf16 v[128:131], a[144:147], a[204:207], v[128:131]\n\t"
"buffer_load_dwordx4 a[220:223], v252, s[8:11], s20 offen offset:2048 sc1\n\t"
"s_mov_b32 s20, 24576\n\t"
"buffer_load_dwordx4 a[212:215], v252, s[8:11], s20 offen offset:0 sc1\n\t"
"s_waitcnt lgkmcnt(4)\n\t"
"v_mfma_f32_16x16x32_bf16 v[224:227], v[224:227], a[208:211], 0\n\t"
"v_cvt_pk_bf16_f32 v231, v234, v235\n\t"
"v_cvt_pk_bf16_f32 v108, v108, v109\n\t"
"v_cvt_pk_bf16_f32 v110, v110, v111\n\t"
"v_accvgpr_write_b32 a52, v104\n\t"
"s_waitcnt lgkmcnt(2)\n\t"
"v_mfma_f32_16x16x32_bf16 v[236:239], v[236:239], a[208:211], 0\n\t"
"v_accvgpr_write_b32 a53, v106\n\t"
"buffer_load_dwordx4 a[144:147], v252, s[8:11], s20 offen offset:1024 sc1\n\t"
"v_cvt_pk_bf16_f32 v112, v112, v113\n\t"
"s_waitcnt vmcnt(13)\n\t"
"v_mfma_f32_16x16x32_bf16 v[132:135], a[36:39], a[168:171], v[132:135]\n\t"
"v_cvt_pk_bf16_f32 v114, v114, v115\n\t"
"ds_read_b64_tr_b16 v[232:233], v240 offset:19968\n\t"
"v_mfma_f32_16x16x32_bf16 v[132:135], a[160:163], a[172:175], v[132:135]\n\t"
"ds_read_b64_tr_b16 v[234:235], v240 offset:21504\n\t"
"v_accvgpr_write_b32 a60, v108\n\t"
"v_mfma_f32_16x16x32_bf16 v[132:135], a[152:155], a[176:179], v[132:135]\n\t"
"s_mov_b64 exec, s[44:45]\n\t"
"ds_write_b128 v242, v[228:231] offset:7680\n\t"
"s_mov_b64 exec, -1\n\t"
"v_accvgpr_write_b32 a61, v110\n\t"
"ds_read_b64_tr_b16 v[228:229], v241 offset:19968\n\t"
"v_mfma_f32_16x16x32_bf16 v[136:139], a[36:39], a[172:175], v[136:139]\n\t"
"ds_read_b64_tr_b16 v[230:231], v241 offset:21504\n\t"
"v_cvt_pk_bf16_f32 v224, v224, v225\n\t"
"v_mfma_f32_16x16x32_bf16 v[136:139], a[160:163], a[176:179], v[136:139]\n\t"
"v_cvt_pk_bf16_f32 v225, v226, v227\n\t"
"v_cvt_pk_bf16_f32 v116, v116, v117\n\t"
"v_mfma_f32_16x16x32_bf16 v[136:139], a[152:155], a[180:183], v[136:139]\n\t"
"buffer_load_dwordx4 a[224:227], v252, s[8:11], s20 offen offset:2048 sc1\n\t"
"ds_write_b64 v250, a[108:109] offset:18432\n\t"
"v_mfma_f32_16x16x32_bf16 v[140:143], a[36:39], a[176:179], v[140:143]\n\t"
"v_cvt_pk_bf16_f32 v118, v118, v119\n\t"
"v_cvt_pk_bf16_f32 v226, v236, v237\n\t"
"v_mfma_f32_16x16x32_bf16 v[140:143], a[160:163], a[180:183], v[140:143]\n\t"
"v_cvt_pk_bf16_f32 v227, v238, v239\n\t"
"v_accvgpr_write_b32 a68, v112\n\t"
"v_mfma_f32_16x16x32_bf16 v[140:143], a[152:155], a[184:187], v[140:143]\n\t"
"v_accvgpr_write_b32 a69, v114\n\t"
"ds_write_b64 v250, a[124:125] offset:30720\n\t"
"s_mov_b32 s20, 184320\n\t"
"buffer_load_dwordx4 a[228:231], v252, s[8:11], s20 offen offset:0 sc1\n\t"
"s_add_u32 s29, s22, 590208\n\t"
"buffer_store_dwordx4 v[196:199], v248, s[16:19], s29 offen offset:256 nt\n\t"
"v_mfma_f32_16x16x32_bf16 v[144:147], a[36:39], a[180:183], v[144:147]\n\t"
"v_cvt_pk_bf16_f32 v120, v120, v121\n\t"
"v_cvt_pk_bf16_f32 v122, v122, v123\n\t"
"v_mfma_f32_16x16x32_bf16 v[144:147], a[160:163], a[184:187], v[144:147]\n\t"
"v_accvgpr_write_b32 a76, v116\n\t"
"v_accvgpr_write_b32 a77, v118\n\t"
"v_mfma_f32_16x16x32_bf16 v[144:147], a[152:155], a[188:191], v[144:147]\n\t"
"buffer_load_dwordx4 a[232:235], v252, s[8:11], s20 offen offset:1024 sc1\n\t"
"s_mov_b64 exec, s[44:45]\n\t"
"ds_write_b128 v242, v[224:227] offset:0\n\t"
"s_mov_b64 exec, -1\n\t"
"s_waitcnt lgkmcnt(6)\n\t"
"v_mfma_f32_16x16x32_bf16 v[232:235], v[232:235], a[208:211], 0\n\t"
"ds_read_b64_tr_b16 v[236:237], v240 offset:39936\n\t"
"ds_read_b64_tr_b16 v[238:239], v240 offset:41472\n\t"
"ds_read_b64_tr_b16 v[224:225], v241 offset:39936\n\t"
"v_accvgpr_write_b32 a84, v120\n\t"
"s_waitcnt lgkmcnt(6)\n\t"
"v_mfma_f32_16x16x32_bf16 v[228:231], v[228:231], a[208:211], 0\n\t"
"ds_read_b64_tr_b16 v[226:227], v241 offset:41472\n\t"
"v_accvgpr_write_b32 a85, v122\n\t"
"buffer_load_dwordx4 a[236:239], v252, s[8:11], s20 offen offset:2048 sc1\n\t"
"v_mfma_f32_16x16x32_bf16 v[148:151], a[36:39], a[184:187], v[148:151]\n\t"
"v_cvt_pk_bf16_f32 v124, v124, v125\n\t"
"v_cvt_pk_bf16_f32 v126, v126, v127\n\t"
"v_mfma_f32_16x16x32_bf16 v[148:151], a[160:163], a[188:191], v[148:151]\n\t"
"v_cvt_pk_bf16_f32 v128, v128, v129\n\t"
"v_cvt_pk_bf16_f32 v130, v130, v131\n\t"
"v_mfma_f32_16x16x32_bf16 v[148:151], a[152:155], a[192:195], v[148:151]\n\t"
"s_mov_b32 s20, 196608\n\t"
"buffer_load_dwordx4 a[240:243], v252, s[8:11], s20 offen offset:0 sc1\n\t"
"v_accvgpr_write_b32 a92, v124\n\t"
"v_accvgpr_write_b32 a93, v126\n\t"
"v_mfma_f32_16x16x32_bf16 v[152:155], a[36:39], a[188:191], v[152:155]\n\t"
"v_cvt_pk_bf16_f32 v232, v232, v233\n\t"
"v_cvt_pk_bf16_f32 v233, v234, v235\n\t"
"v_mfma_f32_16x16x32_bf16 v[152:155], a[160:163], a[192:195], v[152:155]\n\t"
"v_cvt_pk_bf16_f32 v234, v228, v229\n\t"
"v_cvt_pk_bf16_f32 v235, v230, v231\n\t"
"v_mfma_f32_16x16x32_bf16 v[152:155], a[152:155], a[196:199], v[152:155]\n\t"
"ds_write_b128 v249, a[48:51] offset:24576\n\t"
"ds_write_b128 v249, a[64:67] offset:36864\n\t"
"s_waitcnt lgkmcnt(4)\n\t"
"v_mfma_f32_16x16x32_bf16 v[236:239], v[236:239], a[208:211], 0\n\t"
"v_accvgpr_write_b32 a100, v128\n\t"
"v_accvgpr_write_b32 a101, v130\n\t"
"v_cvt_pk_bf16_f32 v132, v132, v133\n\t"
"v_cvt_pk_bf16_f32 v134, v134, v135\n\t"
"s_waitcnt lgkmcnt(2)\n\t"
"v_mfma_f32_16x16x32_bf16 v[224:227], v[224:227], a[208:211], 0\n\t"
"s_add_u32 s29, s22, 786816\n\t"
"buffer_store_dwordx4 v[204:207], v248, s[16:19], s29 offen offset:0 nt\n\t"
"buffer_load_dwordx4 a[244:247], v252, s[8:11], s20 offen offset:1024 sc1\n\t"
"v_mfma_f32_16x16x32_bf16 v[156:159], a[36:39], a[192:195], v[156:159]\n\t"
"v_cvt_pk_bf16_f32 v136, v136, v137\n\t"
"v_cvt_pk_bf16_f32 v138, v138, v139\n\t"
"v_mfma_f32_16x16x32_bf16 v[156:159], a[160:163], a[196:199], v[156:159]\n\t"
"v_accvgpr_write_b32 a46, v132\n\t"
"v_accvgpr_write_b32 a47, v134\n\t"
"v_mfma_f32_16x16x32_bf16 v[156:159], a[152:155], a[200:203], v[156:159]\n\t"
"s_mov_b64 exec, s[44:45]\n\t"
"ds_write_b128 v242, v[232:235] offset:1536\n\t"
"s_mov_b64 exec, -1\n\t"
"v_cvt_pk_bf16_f32 v140, v140, v141\n\t"
"v_cvt_pk_bf16_f32 v142, v142, v143\n\t"
"v_mfma_f32_16x16x32_bf16 v[160:163], a[36:39], a[196:199], v[160:163]\n\t"
"v_cvt_pk_bf16_f32 v236, v236, v237\n\t"
"v_cvt_pk_bf16_f32 v237, v238, v239\n\t"
"v_mfma_f32_16x16x32_bf16 v[160:163], a[160:163], a[200:203], v[160:163]\n\t"
"v_cvt_pk_bf16_f32 v238, v224, v225\n\t"
"v_cvt_pk_bf16_f32 v239, v226, v227\n\t"
"v_mfma_f32_16x16x32_bf16 v[160:163], a[152:155], a[204:207], v[160:163]\n\t"
"buffer_load_dwordx4 a[36:39], v252, s[8:11], s20 offen offset:2048 sc1\n\t"
"v_accvgpr_write_b32 a54, v136\n\t"
"v_accvgpr_write_b32 a55, v138\n\t"
"s_waitcnt vmcnt(19)\n\t"
"v_mfma_f32_16x16x32_bf16 v[164:167], v[200:203], a[168:171], v[164:167]\n\t"
"v_cvt_pk_bf16_f32 v144, v144, v145\n\t"
"v_cvt_pk_bf16_f32 v146, v146, v147\n\t"
"v_mfma_f32_16x16x32_bf16 v[164:167], a[4:7], a[172:175], v[164:167]\n\t"
"v_accvgpr_write_b32 a62, v140\n\t"
"v_accvgpr_write_b32 a63, v142\n\t"
"v_mfma_f32_16x16x32_bf16 v[164:167], v[208:211], a[176:179], v[164:167]\n\t"
"ds_write_b64 v250, a[116:117] offset:24576\n\t"
"ds_write_b64 v250, a[132:133] offset:36864\n\t"
"v_mfma_f32_16x16x32_bf16 v[168:171], v[200:203], a[172:175], v[168:171]\n\t"
"v_accvgpr_write_b32 a70, v144\n\t"
"v_accvgpr_write_b32 a71, v146\n\t"
"v_mfma_f32_16x16x32_bf16 v[168:171], a[4:7], a[176:179], v[168:171]\n\t"
"v_cvt_pk_bf16_f32 v148, v148, v149\n\t"
"v_cvt_pk_bf16_f32 v150, v150, v151\n\t"
"v_mfma_f32_16x16x32_bf16 v[168:171], v[208:211], a[180:183], v[168:171]\n\t"
"s_mov_b32 s20, 208896\n\t"
"buffer_load_dwordx4 a[160:163], v252, s[8:11], s20 offen offset:0 sc1\n\t"
"s_mov_b64 exec, s[44:45]\n\t"
"ds_write_b128 v242, v[236:239] offset:3072\n\t"
"s_mov_b64 exec, -1\n\t"
"v_mfma_f32_16x16x32_bf16 v[172:175], v[200:203], a[176:179], v[172:175]\n\t"
"v_cvt_pk_bf16_f32 v152, v152, v153\n\t"
"v_cvt_pk_bf16_f32 v154, v154, v155\n\t"
"v_mfma_f32_16x16x32_bf16 v[172:175], a[4:7], a[180:183], v[172:175]\n\t"
"v_accvgpr_write_b32 a78, v148\n\t"
"v_accvgpr_write_b32 a79, v150\n\t"
"v_mfma_f32_16x16x32_bf16 v[172:175], v[208:211], a[184:187], v[172:175]\n\t"
"s_add_u32 s29, s22, 786816\n\t"
"buffer_store_dwordx4 v[212:215], v248, s[16:19], s29 offen offset:128 nt\n\t"
"v_accvgpr_write_b32 a86, v152\n\t"
"v_accvgpr_write_b32 a87, v154\n\t"
"v_mfma_f32_16x16x32_bf16 v[176:179], v[200:203], a[180:183], v[176:179]\n\t"
"v_cvt_pk_bf16_f32 v156, v156, v157\n\t"
"v_cvt_pk_bf16_f32 v158, v158, v159\n\t"
"v_mfma_f32_16x16x32_bf16 v[176:179], a[4:7], a[184:187], v[176:179]\n\t"
"v_cvt_pk_bf16_f32 v160, v160, v161\n\t"
"v_cvt_pk_bf16_f32 v162, v162, v163\n\t"
"v_mfma_f32_16x16x32_bf16 v[176:179], v[208:211], a[188:191], v[176:179]\n\t"
"buffer_load_dwordx4 a[152:155], v252, s[8:11], s20 offen offset:1024 sc1\n\t"
"s_waitcnt lgkmcnt(0)\n\t"
"s_barrier\n\t"
"v_mfma_f32_16x16x32_bf16 v[180:183], v[200:203], a[184:187], v[180:183]\n\t"
"ds_read_b128 a[168:171], v244 offset:0\n\t"
"v_accvgpr_write_b32 a94, v156\n\t"
"v_mfma_f32_16x16x32_bf16 v[180:183], a[4:7], a[188:191], v[180:183]\n\t"
"ds_read_b128 a[172:175], v244 offset:512\n\t"
"v_accvgpr_write_b32 a95, v158\n\t"
"v_mfma_f32_16x16x32_bf16 v[180:183], v[208:211], a[192:195], v[180:183]\n\t"
"ds_read_b128 a[176:179], v244 offset:1024\n\t"
"ds_read_b128 a[180:183], v244 offset:1536\n\t"
"buffer_load_dwordx4 a[248:251], v252, s[8:11], s20 offen offset:2048 sc1\n\t"
"v_mfma_f32_16x16x32_bf16 v[184:187], v[200:203], a[188:191], v[184:187]\n\t"
"ds_read_b128 a[184:187], v244 offset:2048\n\t"
"v_cvt_pk_bf16_f32 v164, v164, v165\n\t"
"v_mfma_f32_16x16x32_bf16 v[184:187], a[4:7], a[192:195], v[184:187]\n\t"
"ds_read_b128 a[188:191], v244 offset:2560\n\t"
"v_cvt_pk_bf16_f32 v166, v166, v167\n\t"
"v_mfma_f32_16x16x32_bf16 v[184:187], v[208:211], a[196:199], v[184:187]\n\t"
"s_mov_b32 s20, 221184\n\t"
"buffer_load_dwordx4 a[252:255], v252, s[8:11], s20 offen offset:0 sc1\n\t"
"s_add_u32 s29, s22, 786816\n\t"
"buffer_store_dwordx4 a[0:3], v248, s[16:19], s29 offen offset:256 nt\n\t"
"v_mfma_f32_16x16x32_bf16 v[188:191], v[200:203], a[192:195], v[188:191]\n\t"
"ds_read_b128 a[192:195], v244 offset:3072\n\t"
"v_accvgpr_write_b32 a102, v160\n\t"
"v_mfma_f32_16x16x32_bf16 v[188:191], a[4:7], a[196:199], v[188:191]\n\t"
"v_accvgpr_write_b32 a103, v162\n\t"
"v_cvt_pk_bf16_f32 v168, v168, v169\n\t"
"v_mfma_f32_16x16x32_bf16 v[188:191], v[208:211], a[200:203], v[188:191]\n\t"
"v_cvt_pk_bf16_f32 v170, v170, v171\n\t"
"v_accvgpr_write_b32 a110, v164\n\t"
"v_mfma_f32_16x16x32_bf16 v[192:195], v[200:203], a[196:199], v[192:195]\n\t"
"ds_read_b128 a[196:199], v244 offset:3584\n\t"
"v_accvgpr_write_b32 a111, v166\n\t"
"v_mfma_f32_16x16x32_bf16 v[192:195], a[4:7], a[200:203], v[192:195]\n\t"
"v_cvt_pk_bf16_f32 v172, v172, v173\n\t"
"v_cvt_pk_bf16_f32 v174, v174, v175\n\t"
"v_mfma_f32_16x16x32_bf16 v[192:195], v[208:211], a[204:207], v[192:195]\n\t"
"buffer_load_dwordx4 a[4:7], v252, s[8:11], s20 offen offset:1024 sc1\n\t"
"ds_read_b128 a[200:203], v244 offset:4096\n\t"
"v_accvgpr_write_b32 a118, v168\n\t"
"Lst18_%=:\n\t"
"s_waitcnt vmcnt(22) lgkmcnt(6)\n\t"
"v_mfma_f32_16x16x32_bf16 v[2:5], v[216:219], a[168:171], 0\n\t"
"v_accvgpr_write_b32 a119, v170\n\t"
"v_cvt_pk_bf16_f32 v176, v176, v177\n\t"
"v_mfma_f32_16x16x32_bf16 v[2:5], a[12:15], a[172:175], v[2:5]\n\t"
"v_cvt_pk_bf16_f32 v178, v178, v179\n\t"
"ds_read_b128 a[204:207], v244 offset:4608\n\t"
"v_mfma_f32_16x16x32_bf16 v[2:5], a[20:23], a[176:179], v[2:5]\n\t"
"buffer_load_dwordx4 v[200:203], v252, s[8:11], s20 offen offset:2048 sc1\n\t"
"s_waitcnt lgkmcnt(6)\n\t"
"v_mfma_f32_16x16x32_bf16 v[6:9], v[216:219], a[172:175], 0\n\t"
"v_accvgpr_write_b32 a126, v172\n\t"
"v_accvgpr_write_b32 a127, v174\n\t"
"v_mfma_f32_16x16x32_bf16 v[6:9], a[12:15], a[176:179], v[6:9]\n\t"
"v_cvt_pk_bf16_f32 v180, v180, v181\n\t"
"v_cvt_pk_bf16_f32 v182, v182, v183\n\t"
"v_mfma_f32_16x16x32_bf16 v[6:9], a[20:23], a[180:183], v[6:9]\n\t"
"v_accvgpr_write_b32 a134, v176\n\t"
"v_accvgpr_write_b32 a135, v178\n\t"
"s_waitcnt lgkmcnt(5)\n\t"
"v_mfma_f32_16x16x32_bf16 v[10:13], v[216:219], a[176:179], 0\n\t"
"v_cvt_pk_bf16_f32 v184, v184, v185\n\t"
"v_cvt_pk_bf16_f32 v186, v186, v187\n\t"
"v_mfma_f32_16x16x32_bf16 v[10:13], a[12:15], a[180:183], v[10:13]\n\t"
"v_accvgpr_write_b32 a142, v180\n\t"
"v_accvgpr_write_b32 a143, v182\n\t"
"v_mfma_f32_16x16x32_bf16 v[10:13], a[20:23], a[184:187], v[10:13]\n\t"
"s_add_u32 s29, s22, 983424\n\t"
"buffer_store_dwordx4 a[8:11], v248, s[16:19], s29 offen offset:0 nt\n\t"
"s_waitcnt lgkmcnt(4)\n\t"
"v_mfma_f32_16x16x32_bf16 v[14:17], v[216:219], a[180:183], 0\n\t"
"v_cvt_pk_bf16_f32 v188, v188, v189\n\t"
"v_cvt_pk_bf16_f32 v190, v190, v191\n\t"
"v_mfma_f32_16x16x32_bf16 v[14:17], a[12:15], a[184:187], v[14:17]\n\t"
"v_accvgpr_write_b32 a150, v184\n\t"
"v_accvgpr_write_b32 a151, v186\n\t"
"v_mfma_f32_16x16x32_bf16 v[14:17], a[20:23], a[188:191], v[14:17]\n\t"
"s_mov_b32 s20, 233472\n\t"
"buffer_load_dwordx4 v[208:211], v252, s[8:11], s20 offen offset:0 sc1\n\t"
"s_waitcnt lgkmcnt(3)\n\t"
"v_mfma_f32_16x16x32_bf16 v[18:21], v[216:219], a[184:187], 0\n\t"
"v_cvt_pk_bf16_f32 v192, v192, v193\n\t"
"v_cvt_pk_bf16_f32 v194, v194, v195\n\t"
"v_mfma_f32_16x16x32_bf16 v[18:21], a[12:15], a[188:191], v[18:21]\n\t"
"v_accvgpr_write_b32 a158, v188\n\t"
"v_accvgpr_write_b32 a159, v190\n\t"
"v_mfma_f32_16x16x32_bf16 v[18:21], a[20:23], a[192:195], v[18:21]\n\t"
"v_accvgpr_write_b32 a166, v192\n\t"
"s_waitcnt lgkmcnt(2)\n\t"
"v_mfma_f32_16x16x32_bf16 v[22:25], v[216:219], a[188:191], 0\n\t"
"v_accvgpr_write_b32 a167, v194\n\t"
"v_mfma_f32_16x16x32_bf16 v[22:25], a[12:15], a[192:195], v[22:25]\n\t"
"ds_read_b128 v[196:199], v252 offset:18432\n\t"
"v_mfma_f32_16x16x32_bf16 v[22:25], a[20:23], a[196:199], v[22:25]\n\t"
"s_waitcnt lgkmcnt(2)\n\t"
"v_mfma_f32_16x16x32_bf16 v[26:29], v[216:219], a[192:195], 0\n\t"
"ds_read_b128 a[0:3], v252 offset:30720\n\t"
"v_mfma_f32_16x16x32_bf16 v[26:29], a[12:15], a[196:199], v[26:29]\n\t"
"ds_read_b128 v[204:207], v252 offset:19456\n\t"
"v_mfma_f32_16x16x32_bf16 v[26:29], a[20:23], a[200:203], v[26:29]\n\t"
"ds_read_b128 a[8:11], v252 offset:31744\n\t"
"s_waitcnt lgkmcnt(4)\n\t"
"v_mfma_f32_16x16x32_bf16 v[30:33], v[216:219], a[196:199], 0\n\t"
"ds_read_b128 v[212:215], v252 offset:20480\n\t"
"v_mfma_f32_16x16x32_bf16 v[30:33], a[12:15], a[200:203], v[30:33]\n\t"
"ds_read_b128 a[40:43], v252 offset:32768\n\t"
"v_mfma_f32_16x16x32_bf16 v[30:33], a[20:23], a[204:207], v[30:33]\n\t"
"buffer_load_dwordx4 v[216:219], v252, s[8:11], s20 offen offset:1024 sc1\n\t"
"s_waitcnt vmcnt(22)\n\t"
"v_mfma_f32_16x16x32_bf16 v[34:37], a[28:31], a[168:171], 0\n\t"
"v_mfma_f32_16x16x32_bf16 v[34:37], a[216:219], a[172:175], v[34:37]\n\t"
"v_mfma_f32_16x16x32_bf16 v[34:37], a[220:223], a[176:179], v[34:37]\n\t"
"s_add_u32 s29, s22, 983424\n\t"
"buffer_store_dwordx4 a[16:19], v248, s[16:19], s29 offen offset:128 nt\n\t"
"v_mfma_f32_16x16x32_bf16 v[38:41], a[28:31], a[172:175], 0\n\t"
"ds_read_b64_tr_b16 v[228:229], v240 offset:512\n\t"
"v_mfma_f32_16x16x32_bf16 v[38:41], a[216:219], a[176:179], v[38:41]\n\t"
"ds_read_b64_tr_b16 v[230:231], v240 offset:2048\n\t"
"v_mfma_f32_16x16x32_bf16 v[38:41], a[220:223], a[180:183], v[38:41]\n\t"
"buffer_load_dwordx4 a[12:15], v252, s[8:11], s20 offen offset:2048 sc1\n\t"
"v_mfma_f32_16x16x32_bf16 v[42:45], a[28:31], a[176:179], 0\n\t"
"v_mfma_f32_16x16x32_bf16 v[42:45], a[216:219], a[180:183], v[42:45]\n\t"
"v_mfma_f32_16x16x32_bf16 v[42:45], a[220:223], a[184:187], v[42:45]\n\t"
"s_mov_b32 s20, 245760\n\t"
"buffer_load_dwordx4 a[20:23], v252, s[8:11], s20 offen offset:0 sc1\n\t"
"v_mfma_f32_16x16x32_bf16 v[46:49], a[28:31], a[180:183], 0\n\t"
"v_mfma_f32_16x16x32_bf16 v[46:49], a[216:219], a[184:187], v[46:49]\n\t"
"v_mfma_f32_16x16x32_bf16 v[46:49], a[220:223], a[188:191], v[46:49]\n\t"
"s_add_u32 s29, s22, 983424\n\t"
"buffer_store_dwordx4 a[104:107], v248, s[16:19], s29 offen offset:256 nt\n\t"
"s_waitcnt lgkmcnt(2)\n\t"
"s_barrier\n\t"
"v_mfma_f32_16x16x32_bf16 v[50:53], a[28:31], a[184:187], 0\n\t"
"ds_read_b64_tr_b16 v[232:233], v241 offset:512\n\t"
"v_mfma_f32_16x16x32_bf16 v[50:53], a[216:219], a[188:191], v[50:53]\n\t"
"ds_read_b64_tr_b16 v[234:235], v241 offset:2048\n\t"
"v_mfma_f32_16x16x32_bf16 v[50:53], a[220:223], a[192:195], v[50:53]\n\t"
"ds_write_b128 v249, a[72:75] offset:18432\n\t"
"v_mfma_f32_16x16x32_bf16 v[54:57], a[28:31], a[188:191], 0\n\t"
"v_mfma_f32_16x16x32_bf16 v[54:57], a[216:219], a[192:195], v[54:57]\n\t"
"ds_read_b64_tr_b16 v[224:225], v240 offset:20480\n\t"
"v_mfma_f32_16x16x32_bf16 v[54:57], a[220:223], a[196:199], v[54:57]\n\t"
"ds_write_b128 v249, a[88:91] offset:30720\n\t"
"s_waitcnt lgkmcnt(5)\n\t"
"v_mfma_f32_16x16x32_bf16 v[228:231], v[228:231], a[208:211], 0\n\t"
"ds_read_b64_tr_b16 v[226:227], v240 offset:22016\n\t"
"v_mfma_f32_16x16x32_bf16 v[58:61], a[28:31], a[192:195], 0\n\t"
"v_mfma_f32_16x16x32_bf16 v[58:61], a[216:219], a[196:199], v[58:61]\n\t"
"ds_read_b64_tr_b16 v[236:237], v241 offset:20480\n\t"
"v_mfma_f32_16x16x32_bf16 v[58:61], a[220:223], a[200:203], v[58:61]\n\t"
"buffer_load_dwordx4 a[104:107], v252, s[8:11], s20 offen offset:1024 sc1\n\t"
"s_waitcnt lgkmcnt(5)\n\t"
"v_mfma_f32_16x16x32_bf16 v[232:235], v[232:235], a[208:211], 0\n\t"
"ds_read_b64_tr_b16 v[238:239], v241 offset:22016\n\t"
"v_mfma_f32_16x16x32_bf16 v[62:65], a[28:31], a[196:199], 0\n\t"
"v_cvt_pk_bf16_f32 v228, v228, v229\n\t"
"v_cvt_pk_bf16_f32 v229, v230, v231\n\t"
"v_mfma_f32_16x16x32_bf16 v[62:65], a[216:219], a[200:203], v[62:65]\n\t"
"v_mfma_f32_16x16x32_bf16 v[62:65], a[220:223], a[204:207], v[62:65]\n\t"
"buffer_load_dwordx4 a[28:31], v252, s[8:11], s20 offen offset:2048 sc1\n\t"
"s_add_u32 s29, s22, 1180032\n\t"
"buffer_store_dwordx4 a[24:27], v248, s[16:19], s29 offen offset:0 nt\n\t"
"s_waitcnt vmcnt(26)\n\t"
"v_mfma_f32_16x16x32_bf16 v[66:69], a[212:215], a[168:171], 0\n\t"
"ds_read_b128 a[168:171], v244 offset:9216\n\t"
"v_mfma_f32_16x16x32_bf16 v[66:69], a[144:147], a[172:175], v[66:69]\n\t"
"v_cvt_pk_bf16_f32 v230, v232, v233\n\t"
"v_cvt_pk_bf16_f32 v231, v234, v235\n\t"
"v_mfma_f32_16x16x32_bf16 v[66:69], a[224:227], a[176:179], v[66:69]\n\t"
"s_mov_b32 s20, 405504\n\t"
"buffer_load_dwordx4 a[216:219], v252, s[8:11], s20 offen offset:0 sc1\n\t"
"ds_write_b64 v250, a[140:141] offset:18432\n\t"
"v_mfma_f32_16x16x32_bf16 v[70:73], a[212:215], a[172:175], 0\n\t"
"ds_read_b128 a[172:175], v244 offset:9728\n\t"
"v_mfma_f32_16x16x32_bf16 v[70:73], a[144:147], a[176:179], v[70:73]\n\t"
"v_mfma_f32_16x16x32_bf16 v[70:73], a[224:227], a[180:183], v[70:73]\n\t"
"ds_write_b64 v250, a[156:157] offset:30720\n\t"
"buffer_load_dwordx4 a[220:223], v252, s[8:11], s20 offen offset:1024 sc1\n\t"
"s_waitcnt lgkmcnt(6)\n\t"
"v_mfma_f32_16x16x32_bf16 v[224:227], v[224:227], a[208:211], 0\n\t"
"ds_write_b128 v249, a[80:83] offset:24576\n\t"
"v_mfma_f32_16x16x32_bf16 v[74:77], a[212:215], a[176:179], 0\n\t"
"ds_read_b128 a[176:179], v244 offset:10240\n\t"
"v_mfma_f32_16x16x32_bf16 v[74:77], a[144:147], a[180:183], v[74:77]\n\t"
"v_mfma_f32_16x16x32_bf16 v[74:77], a[224:227], a[184:187], v[74:77]\n\t"
"ds_write_b128 v249, a[96:99] offset:36864\n\t"
"s_waitcnt lgkmcnt(7)\n\t"
"v_mfma_f32_16x16x32_bf16 v[236:239], v[236:239], a[208:211], 0\n\t"
"ds_write_b64 v250, a[148:149] offset:24576\n\t"
"v_mfma_f32_16x16x32_bf16 v[78:81], a[212:215], a[180:183], 0\n\t"
"v_cvt_pk_bf16_f32 v224, v224, v225\n\t"
"v_cvt_pk_bf16_f32 v225, v226, v227\n\t"
"v_mfma_f32_16x16x32_bf16 v[78:81], a[144:147], a[184:187], v[78:81]\n\t"
"ds_read_b128 a[180:183], v244 offset:10752\n\t"
"v_mfma_f32_16x16x32_bf16 v[78:81], a[224:227], a[188:191], v[78:81]\n\t"
"s_add_u32 s29, s22, 1180032\n\t"
"buffer_store_dwordx4 a[112:115], v248, s[16:19], s29 offen offset:128 nt\n\t"
"v_mfma_f32_16x16x32_bf16 v[82:85], a[212:215], a[184:187], 0\n\t"
"ds_read_b128 a[184:187], v244 offset:11264\n\t"
"v_mfma_f32_16x16x32_bf16 v[82:85], a[144:147], a[188:191], v[82:85]\n\t"
"v_cvt_pk_bf16_f32 v226, v236, v237\n\t"
"v_cvt_pk_bf16_f32 v227, v238, v239\n\t"
"v_mfma_f32_16x16x32_bf16 v[82:85], a[224:227], a[192:195], v[82:85]\n\t"
"v_mfma_f32_16x16x32_bf16 v[86:89], a[212:215], a[188:191], 0\n\t"
"ds_read_b128 a[188:191], v244 offset:11776\n\t"
"v_mfma_f32_16x16x32_bf16 v[86:89], a[144:147], a[192:195], v[86:89]\n\t"
"v_mfma_f32_16x16x32_bf16 v[86:89], a[224:227], a[196:199], v[86:89]\n\t"
"ds_write_b64 v250, a[164:165] offset:36864\n\t"
"v_mfma_f32_16x16x32_bf16 v[90:93], a[212:215], a[192:195], 0\n\t"
"ds_read_b128 a[192:195], v244 offset:12288\n\t"
"v_mfma_f32_16x16x32_bf16 v[90:93], a[144:147], a[196:199], v[90:93]\n\t"
"v_mfma_f32_16x16x32_bf16 v[90:93], a[224:227], a[200:203], v[90:93]\n\t"
"buffer_load_dwordx4 a[112:115], v252, s[8:11], s20 offen offset:2048 sc1\n\t"
"v_mfma_f32_16x16x32_bf16 v[94:97], a[212:215], a[196:199], 0\n\t"
"ds_read_b128 a[196:199], v244 offset:12800\n\t"
"v_mfma_f32_16x16x32_bf16 v[94:97], a[144:147], a[200:203], v[94:97]\n\t"
"ds_read_b128 a[200:203], v244 offset:13312\n\t"
"v_mfma_f32_16x16x32_bf16 v[94:97], a[224:227], a[204:207], v[94:97]\n\t"
"ds_read_b128 a[204:207], v244 offset:13824\n\t"
"s_waitcnt vmcnt(26) lgkmcnt(10)\n\t"
"v_mfma_f32_16x16x32_bf16 v[100:103], a[228:231], a[168:171], 0\n\t"
"v_mfma_f32_16x16x32_bf16 v[100:103], a[232:235], a[172:175], v[100:103]\n\t"
"v_mfma_f32_16x16x32_bf16 v[100:103], a[236:239], a[176:179], v[100:103]\n\t"
"s_mov_b32 s20, 417792\n\t"
"buffer_load_dwordx4 a[144:147], v252, s[8:11], s20 offen offset:0 sc1\n\t"
"s_waitcnt lgkmcnt(7)\n\t"
"v_mfma_f32_16x16x32_bf16 v[104:107], a[228:231], a[172:175], 0\n\t"
"v_mfma_f32_16x16x32_bf16 v[104:107], a[232:235], a[176:179], v[104:107]\n\t"
"v_mfma_f32_16x16x32_bf16 v[104:107], a[236:239], a[180:183], v[104:107]\n\t"
"buffer_load_dwordx4 a[212:215], v252, s[8:11], s20 offen offset:1024 sc1\n\t"
"s_waitcnt lgkmcnt(0)\n\t"
"s_barrier\n\t"
"v_mfma_f32_16x16x32_bf16 v[108:111], a[228:231], a[176:179], 0\n\t"
"ds_read_b64_tr_b16 v[232:233], v240 offset:40448\n\t"
"v_mfma_f32_16x16x32_bf16 v[108:111], a[232:235], a[180:183], v[108:111]\n\t"
"ds_read_b64_tr_b16 v[234:235], v240 offset:41984\n\t"
"v_mfma_f32_16x16x32_bf16 v[108:111], a[236:239], a[184:187], v[108:111]\n\t"
"s_add_u32 s29, s22, 1180032\n\t"
"buffer_store_dwordx4 a[120:123], v248, s[16:19], s29 offen offset:256 nt\n\t"
"buffer_load_dwordx4 a[224:227], v252, s[8:11], s20 offen offset:2048 sc1\n\t"
"s_mov_b64 exec, s[44:45]\n\t"
"ds_write_b128 v242, v[228:231] offset:0\n\t"
"s_mov_b64 exec, -1\n\t"
"v_mfma_f32_16x16x32_bf16 v[112:115], a[228:231], a[180:183], 0\n\t"
"ds_read_b64_tr_b16 v[236:237], v241 offset:40448\n\t"
"v_mfma_f32_16x16x32_bf16 v[112:115], a[232:235], a[184:187], v[112:115]\n\t"
"ds_read_b64_tr_b16 v[238:239], v241 offset:41984\n\t"
"v_add_u32 v240, 59904, v240\n\t"
"v_mfma_f32_16x16x32_bf16 v[112:115], a[236:239], a[188:191], v[112:115]\n\t"
"s_mov_b64 exec, s[44:45]\n\t"
"ds_write_b128 v242, v[224:227] offset:1536\n\t"
"s_mov_b64 exec, -1\n\t"
"s_mov_b32 s20, 430080\n\t"
"buffer_load_dwordx4 a[80:83], v252, s[8:11], s20 offen offset:0 sc1\n\t"
"v_mfma_f32_16x16x32_bf16 v[116:119], a[228:231], a[184:187], 0\n\t"
"v_add_u32 v241, 59904, v241\n\t"
"ds_read_b64_tr_b16 v[228:229], v240 offset:512\n\t"
"v_mfma_f32_16x16x32_bf16 v[116:119], a[232:235], a[188:191], v[116:119]\n\t"
"ds_read_b64_tr_b16 v[230:231], v240 offset:2048\n\t"
"v_mfma_f32_16x16x32_bf16 v[116:119], a[236:239], a[192:195], v[116:119]\n\t"
"buffer_load_dwordx4 a[96:99], v252, s[8:11], s20 offen offset:1024 sc1\n\t"
"v_mfma_f32_16x16x32_bf16 v[120:123], a[228:231], a[188:191], 0\n\t"
"ds_read_b64_tr_b16 v[224:225], v241 offset:512\n\t"
"v_mfma_f32_16x16x32_bf16 v[120:123], a[232:235], a[192:195], v[120:123]\n\t"
"ds_read_b64_tr_b16 v[226:227], v241 offset:2048\n\t"
"v_mfma_f32_16x16x32_bf16 v[120:123], a[236:239], a[196:199], v[120:123]\n\t"
"s_add_u32 s29, s22, 1376640\n\t"
"buffer_store_dwordx4 a[32:35], v248, s[16:19], s29 offen offset:0 nt\n\t"
"s_waitcnt lgkmcnt(8)\n\t"
"v_mfma_f32_16x16x32_bf16 v[232:235], v[232:235], a[208:211], 0\n\t"
"s_waitcnt lgkmcnt(5)\n\t"
"v_mfma_f32_16x16x32_bf16 v[236:239], v[236:239], a[208:211], 0\n\t"
"v_mfma_f32_16x16x32_bf16 v[124:127], a[228:231], a[192:195], 0\n\t"
"v_mfma_f32_16x16x32_bf16 v[124:127], a[232:235], a[196:199], v[124:127]\n\t"
"v_mfma_f32_16x16x32_bf16 v[124:127], a[236:239], a[200:203], v[124:127]\n\t"
"v_mfma_f32_16x16x32_bf16 v[128:131], a[228:231], a[196:199], 0\n\t"
"s_nop 1\n\t"
"v_cvt_pk_bf16_f32 v232, v232, v233\n\t"
"v_cvt_pk_bf16_f32 v233, v234, v235\n\t"
"v_mfma_f32_16x16x32_bf16 v[128:131], a[232:235], a[200:203], v[128:131]\n\t"
"v_cvt_pk_bf16_f32 v234, v236, v237\n\t"
"v_cvt_pk_bf16_f32 v235, v238, v239\n\t"
"v_mfma_f32_16x16x32_bf16 v[128:131], a[236:239], a[204:207], v[128:131]\n\t"
"buffer_load_dwordx4 a[228:231], v252, s[8:11], s20 offen offset:2048 sc1\n\t"
"s_waitcnt lgkmcnt(2)\n\t"
"v_mfma_f32_16x16x32_bf16 v[228:231], v[228:231], a[208:211], 0\n\t"
"s_mov_b32 s20, 110592\n\t"
"buffer_load_dwordx4 a[232:235], v252, s[8:11], s20 offen offset:0 sc1\n\t"
"s_waitcnt lgkmcnt(0)\n\t"
"v_mfma_f32_16x16x32_bf16 v[224:227], v[224:227], a[208:211], 0\n\t"
"s_mov_b64 exec, s[44:45]\n\t"
"ds_write_b128 v242, v[232:235] offset:3072\n\t"
"s_mov_b64 exec, -1\n\t"
"s_waitcnt vmcnt(31)\n\t"
"v_mfma_f32_16x16x32_bf16 v[132:135], a[240:243], a[168:171], 0\n\t"
"ds_read_b64_tr_b16 v[236:237], v240 offset:20480\n\t"
"v_mfma_f32_16x16x32_bf16 v[132:135], a[244:247], a[172:175], v[132:135]\n\t"
"ds_read_b64_tr_b16 v[238:239], v240 offset:22016\n\t"
"v_mfma_f32_16x16x32_bf16 v[132:135], a[36:39], a[176:179], v[132:135]\n\t"
"buffer_load_dwordx4 a[236:239], v252, s[8:11], s20 offen offset:1024 sc1\n\t"
"v_mfma_f32_16x16x32_bf16 v[136:139], a[240:243], a[172:175], 0\n\t"
"ds_read_b64_tr_b16 v[232:233], v241 offset:20480\n\t"
"v_cvt_pk_bf16_f32 v228, v228, v229\n\t"
"v_mfma_f32_16x16x32_bf16 v[136:139], a[244:247], a[176:179], v[136:139]\n\t"
"ds_read_b64_tr_b16 v[234:235], v241 offset:22016\n\t"
"v_cvt_pk_bf16_f32 v229, v230, v231\n\t"
"v_mfma_f32_16x16x32_bf16 v[136:139], a[36:39], a[180:183], v[136:139]\n\t"
"v_cvt_pk_bf16_f32 v230, v224, v225\n\t"
"v_cvt_pk_bf16_f32 v231, v226, v227\n\t"
"v_mfma_f32_16x16x32_bf16 v[140:143], a[240:243], a[176:179], 0\n\t"
"ds_read_b64_tr_b16 v[224:225], v240 offset:40448\n\t"
"v_mfma_f32_16x16x32_bf16 v[140:143], a[244:247], a[180:183], v[140:143]\n\t"
"ds_read_b64_tr_b16 v[226:227], v240 offset:41984\n\t"
"v_mfma_f32_16x16x32_bf16 v[140:143], a[36:39], a[184:187], v[140:143]\n\t"
"s_add_u32 s29, s22, 1376640\n\t"
"buffer_store_dwordx4 a[128:131], v248, s[16:19], s29 offen offset:128 nt\n\t"
"v_mfma_f32_16x16x32_bf16 v[144:147], a[240:243], a[180:183], 0\n\t"
"v_mfma_f32_16x16x32_bf16 v[144:147], a[244:247], a[184:187], v[144:147]\n\t"
"v_mfma_f32_16x16x32_bf16 v[144:147], a[36:39], a[188:191], v[144:147]\n\t"
"s_mov_b64 exec, s[44:45]\n\t"
"ds_write_b128 v242, v[228:231] offset:4608\n\t"
"s_mov_b64 exec, -1\n\t"
"s_waitcnt lgkmcnt(5)\n\t"
"v_mfma_f32_16x16x32_bf16 v[236:239], v[236:239], a[208:211], 0\n\t"
"ds_read_b64_tr_b16 v[228:229], v241 offset:40448\n\t"
"ds_read_b64_tr_b16 v[230:231], v241 offset:41984\n\t"
"s_waitcnt lgkmcnt(5)\n\t"
"v_mfma_f32_16x16x32_bf16 v[232:235], v[232:235], a[208:211], 0\n\t"
"v_mfma_f32_16x16x32_bf16 v[148:151], a[240:243], a[184:187], 0\n\t"
"v_mfma_f32_16x16x32_bf16 v[148:151], a[244:247], a[188:191], v[148:151]\n\t"
"v_mfma_f32_16x16x32_bf16 v[148:151], a[36:39], a[192:195], v[148:151]\n\t"
"s_add_u32 s29, s22, 1376640\n\t"
"buffer_store_dwordx4 a[136:139], v248, s[16:19], s29 offen offset:256 nt\n\t"
"v_mfma_f32_16x16x32_bf16 v[152:155], a[240:243], a[188:191], 0\n\t"
"v_cvt_pk_bf16_f32 v236, v236, v237\n\t"
"v_mfma_f32_16x16x32_bf16 v[152:155], a[244:247], a[192:195], v[152:155]\n\t"
"v_cvt_pk_bf16_f32 v237, v238, v239\n\t"
"v_cvt_pk_bf16_f32 v238, v232, v233\n\t"
"v_mfma_f32_16x16x32_bf16 v[152:155], a[36:39], a[196:199], v[152:155]\n\t"
"v_cvt_pk_bf16_f32 v239, v234, v235\n\t"
"s_waitcnt lgkmcnt(3)\n\t"
"v_mfma_f32_16x16x32_bf16 v[224:227], v[224:227], a[208:211], 0\n\t"
"s_mov_b64 exec, s[44:45]\n\t"
"ds_write_b128 v242, v[236:239] offset:6144\n\t"
"s_mov_b64 exec, -1\n\t"
"s_waitcnt lgkmcnt(1)\n\t"
"v_mfma_f32_16x16x32_bf16 v[228:231], v[228:231], a[208:211], 0\n\t"
"ds_read_b128 a[16:19], v252 offset:18432\n\t"
"ds_read_b128 a[48:51], v252 offset:30720\n\t"
"v_mfma_f32_16x16x32_bf16 v[156:159], a[240:243], a[192:195], 0\n\t"
"ds_read_b128 a[24:27], v252 offset:19456\n\t"
"v_mfma_f32_16x16x32_bf16 v[156:159], a[244:247], a[196:199], v[156:159]\n\t"
"ds_read_b128 a[56:59], v252 offset:31744\n\t"
"v_mfma_f32_16x16x32_bf16 v[156:159], a[36:39], a[200:203], v[156:159]\n\t"
"buffer_load_dwordx4 a[208:211], v252, s[8:11], s20 offen offset:2048 sc1\n\t"
"v_mfma_f32_16x16x32_bf16 v[160:163], a[240:243], a[196:199], 0\n\t"
"v_cvt_pk_bf16_f32 v224, v224, v225\n\t"
"v_cvt_pk_bf16_f32 v225, v226, v227\n\t"
"v_mfma_f32_16x16x32_bf16 v[160:163], a[244:247], a[200:203], v[160:163]\n\t"
"v_cvt_pk_bf16_f32 v226, v228, v229\n\t"
"v_cvt_pk_bf16_f32 v227, v230, v231\n\t"
"v_mfma_f32_16x16x32_bf16 v[160:163], a[36:39], a[204:207], v[160:163]\n\t"
"s_mov_b32 s20, 122880\n\t"
"buffer_load_dwordx4 a[240:243], v252, s[8:11], s20 offen offset:0 sc1\n\t"
"s_waitcnt vmcnt(32)\n\t"
"v_mfma_f32_16x16x32_bf16 v[164:167], a[160:163], a[168:171], 0\n\t"
"ds_read_b128 a[32:35], v252 offset:20480\n\t"
"v_mfma_f32_16x16x32_bf16 v[164:167], a[152:155], a[172:175], v[164:167]\n\t"
"ds_read_b128 a[64:67], v252 offset:32768\n\t"
"v_mfma_f32_16x16x32_bf16 v[164:167], a[248:251], a[176:179], v[164:167]\n\t"
"buffer_load_dwordx4 a[244:247], v252, s[8:11], s20 offen offset:1024 sc1\n\t"
"v_mfma_f32_16x16x32_bf16 v[168:171], a[160:163], a[172:175], 0\n\t"
"v_mfma_f32_16x16x32_bf16 v[168:171], a[152:155], a[176:179], v[168:171]\n\t"
"v_mfma_f32_16x16x32_bf16 v[168:171], a[248:251], a[180:183], v[168:171]\n\t"
"s_mov_b64 exec, s[44:45]\n\t"
"ds_write_b128 v242, v[224:227] offset:7680\n\t"
"s_mov_b64 exec, -1\n\t"
"v_mfma_f32_16x16x32_bf16 v[172:175], a[160:163], a[176:179], 0\n\t"
"v_mfma_f32_16x16x32_bf16 v[172:175], a[152:155], a[180:183], v[172:175]\n\t"
"v_mfma_f32_16x16x32_bf16 v[172:175], a[248:251], a[184:187], v[172:175]\n\t"
"s_add_u32 s29, s23, 0\n\t"
"buffer_store_dwordx4 v[196:199], v248, s[16:19], s29 offen offset:0 nt\n\t"
"v_mfma_f32_16x16x32_bf16 v[176:179], a[160:163], a[180:183], 0\n\t"
"v_mfma_f32_16x16x32_bf16 v[176:179], a[152:155], a[184:187], v[176:179]\n\t"
"v_mfma_f32_16x16x32_bf16 v[176:179], a[248:251], a[188:191], v[176:179]\n\t"
"s_waitcnt lgkmcnt(0)\n\t"
"s_barrier\n\t"
"v_mfma_f32_16x16x32_bf16 v[180:183], a[160:163], a[184:187], 0\n\t"
"ds_read_b128 a[168:171], v244 offset:0\n\t"
"v_mfma_f32_16x16x32_bf16 v[180:183], a[152:155], a[188:191], v[180:183]\n\t"
"ds_read_b128 a[172:175], v244 offset:512\n\t"
"v_mfma_f32_16x16x32_bf16 v[180:183], a[248:251], a[192:195], v[180:183]\n\t"
"ds_read_b128 a[176:179], v244 offset:1024\n\t"
"ds_read_b128 a[180:183], v244 offset:1536\n\t"
"v_mfma_f32_16x16x32_bf16 v[184:187], a[160:163], a[188:191], 0\n\t"
"ds_read_b128 a[184:187], v244 offset:2048\n\t"
"v_mfma_f32_16x16x32_bf16 v[184:187], a[152:155], a[192:195], v[184:187]\n\t"
"ds_read_b128 a[188:191], v244 offset:2560\n\t"
"v_mfma_f32_16x16x32_bf16 v[184:187], a[248:251], a[196:199], v[184:187]\n\t"
"buffer_load_dwordx4 v[240:243], v252, s[8:11], s20 offen offset:2048 sc1\n\t"
"s_add_u32 s29, s23, 0\n\t"
"buffer_store_dwordx4 v[204:207], v248, s[16:19], s29 offen offset:128 nt\n\t"
"v_mfma_f32_16x16x32_bf16 v[188:191], a[160:163], a[192:195], 0\n\t"
"ds_read_b128 a[192:195], v244 offset:3072\n\t"
"v_mfma_f32_16x16x32_bf16 v[188:191], a[152:155], a[196:199], v[188:191]\n\t"
"v_mfma_f32_16x16x32_bf16 v[188:191], a[248:251], a[200:203], v[188:191]\n\t"
"ds_write_b128 v249, a[44:47] offset:18432\n\t"
"ds_write_b128 v249, a[60:63] offset:30720\n\t"
"v_mfma_f32_16x16x32_bf16 v[192:195], a[160:163], a[196:199], 0\n\t"
"ds_read_b128 a[196:199], v244 offset:3584\n\t"
"v_mfma_f32_16x16x32_bf16 v[192:195], a[152:155], a[200:203], v[192:195]\n\t"
"ds_read_b128 a[200:203], v244 offset:4096\n\t"
"v_mfma_f32_16x16x32_bf16 v[192:195], a[248:251], a[204:207], v[192:195]\n\t"
"s_mov_b32 s20, 135168\n\t"
"buffer_load_dwordx4 a[248:251], v252, s[8:11], s20 offen offset:0 sc1\n\t"
"Lst19_%=:\n\t"
"s_waitcnt vmcnt(33) lgkmcnt(8)\n\t"
"v_mfma_f32_16x16x32_bf16 v[2:5], a[252:255], a[168:171], v[2:5]\n\t"
"ds_read_b128 a[204:207], v244 offset:4608\n\t"
"v_mfma_f32_16x16x32_bf16 v[2:5], a[4:7], a[172:175], v[2:5]\n\t"
"v_mfma_f32_16x16x32_bf16 v[2:5], v[200:203], a[176:179], v[2:5]\n\t"
"ds_write_b64 v250, a[110:111] offset:18432\n\t"
"s_waitcnt lgkmcnt(9)\n\t"
"v_mfma_f32_16x16x32_bf16 v[6:9], a[252:255], a[172:175], v[6:9]\n\t"
"v_mfma_f32_16x16x32_bf16 v[6:9], a[4:7], a[176:179], v[6:9]\n\t"
"v_mfma_f32_16x16x32_bf16 v[6:9], v[200:203], a[180:183], v[6:9]\n\t"
"ds_write_b64 v250, a[126:127] offset:30720\n\t"
"s_waitcnt lgkmcnt(9)\n\t"
"v_mfma_f32_16x16x32_bf16 v[10:13], a[252:255], a[176:179], v[10:13]\n\t"
"v_mfma_f32_16x16x32_bf16 v[10:13], a[4:7], a[180:183], v[10:13]\n\t"
"v_mfma_f32_16x16x32_bf16 v[10:13], v[200:203], a[184:187], v[10:13]\n\t"
"s_add_u32 s29, s23, 0\n\t"
"buffer_store_dwordx4 v[212:215], v248, s[16:19], s29 offen offset:256 nt\n\t"
"s_waitcnt lgkmcnt(8)\n\t"
"v_mfma_f32_16x16x32_bf16 v[14:17], a[252:255], a[180:183], v[14:17]\n\t"
"v_mfma_f32_16x16x32_bf16 v[14:17], a[4:7], a[184:187], v[14:17]\n\t"
"v_mfma_f32_16x16x32_bf16 v[14:17], v[200:203], a[188:191], v[14:17]\n\t"
"ds_write_b128 v249, a[52:55] offset:24576\n\t"
"s_waitcnt lgkmcnt(8)\n\t"
"v_mfma_f32_16x16x32_bf16 v[18:21], a[252:255], a[184:187], v[18:21]\n\t"
"v_mfma_f32_16x16x32_bf16 v[18:21], a[4:7], a[188:191], v[18:21]\n\t"
"v_mfma_f32_16x16x32_bf16 v[18:21], v[200:203], a[192:195], v[18:21]\n\t"
"ds_write_b128 v249, a[68:71] offset:36864\n\t"
"s_waitcnt lgkmcnt(6)\n\t"
"v_mfma_f32_16x16x32_bf16 v[22:25], a[252:255], a[188:191], v[22:25]\n\t"
"v_mfma_f32_16x16x32_bf16 v[22:25], a[4:7], a[192:195], v[22:25]\n\t"
"v_mfma_f32_16x16x32_bf16 v[22:25], v[200:203], a[196:199], v[22:25]\n\t"
"ds_write_b64 v250, a[118:119] offset:24576\n\t"
"s_waitcnt lgkmcnt(6)\n\t"
"v_mfma_f32_16x16x32_bf16 v[26:29], a[252:255], a[192:195], v[26:29]\n\t"
"v_mfma_f32_16x16x32_bf16 v[26:29], a[4:7], a[196:199], v[26:29]\n\t"
"v_mfma_f32_16x16x32_bf16 v[26:29], v[200:203], a[200:203], v[26:29]\n\t"
"s_add_u32 s29, s23, 393216\n\t"
"buffer_store_dwordx4 a[0:3], v248, s[16:19], s29 offen offset:0 nt\n\t"
"s_waitcnt lgkmcnt(5)\n\t"
"v_mfma_f32_16x16x32_bf16 v[30:33], a[252:255], a[196:199], v[30:33]\n\t"
"v_mfma_f32_16x16x32_bf16 v[30:33], a[4:7], a[200:203], v[30:33]\n\t"
"v_mfma_f32_16x16x32_bf16 v[30:33], v[200:203], a[204:207], v[30:33]\n\t"
"buffer_load_dwordx4 a[252:255], v252, s[8:11], s20 offen offset:1024 sc1\n\t"
"s_waitcnt vmcnt(31)\n\t"
"v_mfma_f32_16x16x32_bf16 v[34:37], v[208:211], a[168:171], v[34:37]\n\t"
"v_mfma_f32_16x16x32_bf16 v[34:37], v[216:219], a[172:175], v[34:37]\n\t"
"v_mfma_f32_16x16x32_bf16 v[34:37], a[12:15], a[176:179], v[34:37]\n\t"
"ds_write_b64 v250, a[134:135] offset:36864\n\t"
"v_mfma_f32_16x16x32_bf16 v[38:41], v[208:211], a[172:175], v[38:41]\n\t"
"ds_read_b64_tr_b16 v[232:233], v246 offset:0\n\t"
"v_mfma_f32_16x16x32_bf16 v[38:41], v[216:219], a[176:179], v[38:41]\n\t"
"ds_read_b64_tr_b16 v[234:235], v246 offset:3072\n\t"
"v_mfma_f32_16x16x32_bf16 v[38:41], a[12:15], a[180:183], v[38:41]\n\t"
"v_mfma_f32_16x16x32_bf16 v[42:45], v[208:211], a[176:179], v[42:45]\n\t"
"v_mfma_f32_16x16x32_bf16 v[42:45], v[216:219], a[180:183], v[42:45]\n\t"
"v_mfma_f32_16x16x32_bf16 v[42:45], a[12:15], a[184:187], v[42:45]\n\t"
"v_mfma_f32_16x16x32_bf16 v[46:49], v[208:211], a[180:183], v[46:49]\n\t"
"v_mfma_f32_16x16x32_bf16 v[46:49], v[216:219], a[184:187], v[46:49]\n\t"
"v_mfma_f32_16x16x32_bf16 v[46:49], a[12:15], a[188:191], v[46:49]\n\t"
"s_add_u32 s29, s23, 393216\n\t"
"buffer_store_dwordx4 a[8:11], v248, s[16:19], s29 offen offset:128 nt\n\t"
"s_waitcnt lgkmcnt(2)\n\t"
"s_barrier\n\t"
"v_mfma_f32_16x16x32_bf16 v[50:53], v[208:211], a[184:187], v[50:53]\n\t"
"ds_read_b64_tr_b16 v[236:237], v246 offset:6144\n\t"
"v_mfma_f32_16x16x32_bf16 v[50:53], v[216:219], a[188:191], v[50:53]\n\t"
"ds_read_b64_tr_b16 v[238:239], v246 offset:9216\n\t"
"v_mfma_f32_16x16x32_bf16 v[50:53], a[12:15], a[192:195], v[50:53]\n\t"
"v_mfma_f32_16x16x32_bf16 v[54:57], v[208:211], a[188:191], v[54:57]\n\t"
"v_mfma_f32_16x16x32_bf16 v[54:57], v[216:219], a[192:195], v[54:57]\n\t"
"v_mfma_f32_16x16x32_bf16 v[54:57], a[12:15], a[196:199], v[54:57]\n\t"
"buffer_load_dwordx4 a[108:111], v252, s[8:11], s20 offen offset:2048 sc1\n\t"
"s_waitcnt lgkmcnt(2)\n\t"
"v_mfma_f32_16x16x32_bf16 v[232:235], v[232:235], v[220:223], 0\n\t"
"v_mfma_f32_16x16x32_bf16 v[58:61], v[208:211], a[192:195], v[58:61]\n\t"
"ds_read_b64_tr_b16 v[228:229], v246 offset:12288\n\t"
"v_mfma_f32_16x16x32_bf16 v[58:61], v[216:219], a[196:199], v[58:61]\n\t"
"ds_read_b64_tr_b16 v[230:231], v246 offset:15360\n\t"
"v_mfma_f32_16x16x32_bf16 v[58:61], a[12:15], a[200:203], v[58:61]\n\t"
"v_mfma_f32_16x16x32_bf16 v[62:65], v[208:211], a[196:199], v[62:65]\n\t"
"v_mfma_f32_16x16x32_bf16 v[62:65], v[216:219], a[200:203], v[62:65]\n\t"
"s_nop 0\n\t"
"v_cvt_pk_bf16_f32 v232, v232, v233\n\t"
"v_mfma_f32_16x16x32_bf16 v[62:65], a[12:15], a[204:207], v[62:65]\n\t"
"v_cvt_pk_bf16_f32 v233, v234, v235\n\t"
"s_waitcnt lgkmcnt(2)\n\t"
"v_mfma_f32_16x16x32_bf16 v[236:239], v[236:239], v[220:223], 0\n\t"
"s_waitcnt vmcnt(29)\n\t"
"v_mfma_f32_16x16x32_bf16 v[66:69], a[20:23], a[168:171], v[66:69]\n\t"
"ds_read_b64_tr_b16 v[224:225], v246 offset:19968\n\t"
"v_mfma_f32_16x16x32_bf16 v[66:69], a[104:107], a[172:175], v[66:69]\n\t"
"ds_read_b64_tr_b16 v[226:227], v246 offset:23040\n\t"
"v_mfma_f32_16x16x32_bf16 v[66:69], a[28:31], a[176:179], v[66:69]\n\t"
"s_add_u32 s29, s23, 393216\n\t"
"buffer_store_dwordx4 a[40:43], v248, s[16:19], s29 offen offset:256 nt\n\t"
"v_mfma_f32_16x16x32_bf16 v[70:73], a[20:23], a[172:175], v[70:73]\n\t"
"ds_read_b128 a[168:171], v244 offset:9216\n\t"
"v_mfma_f32_16x16x32_bf16 v[70:73], a[104:107], a[176:179], v[70:73]\n\t"
"v_cvt_pk_bf16_f32 v236, v236, v237\n\t"
"v_mfma_f32_16x16x32_bf16 v[70:73], a[28:31], a[180:183], v[70:73]\n\t"
"v_cvt_pk_bf16_f32 v237, v238, v239\n\t"
"v_mfma_f32_16x16x32_bf16 v[74:77], a[20:23], a[176:179], v[74:77]\n\t"
"ds_read_b128 a[172:175], v244 offset:9728\n\t"
"v_mfma_f32_16x16x32_bf16 v[74:77], a[104:107], a[180:183], v[74:77]\n\t"
"ds_read_b128 a[176:179], v244 offset:10240\n\t"
"v_mfma_f32_16x16x32_bf16 v[74:77], a[28:31], a[184:187], v[74:77]\n\t"
"s_waitcnt lgkmcnt(5)\n\t"
"v_mfma_f32_16x16x32_bf16 v[228:231], v[228:231], v[220:223], 0\n\t"
"v_mfma_f32_16x16x32_bf16 v[78:81], a[20:23], a[180:183], v[78:81]\n\t"
"ds_read_b128 a[180:183], v244 offset:10752\n\t"
"v_mfma_f32_16x16x32_bf16 v[78:81], a[104:107], a[184:187], v[78:81]\n\t"
"ds_read_b128 a[88:91], v252 offset:18432\n\t"
"v_mfma_f32_16x16x32_bf16 v[78:81], a[28:31], a[188:191], v[78:81]\n\t"
"ds_read_b128 a[4:7], v252 offset:30720\n\t"
"v_mfma_f32_16x16x32_bf16 v[82:85], a[20:23], a[184:187], v[82:85]\n\t"
"ds_read_b128 a[184:187], v244 offset:11264\n\t"
"v_mfma_f32_16x16x32_bf16 v[82:85], a[104:107], a[188:191], v[82:85]\n\t"
"v_cvt_pk_bf16_f32 v228, v228, v229\n\t"
"v_cvt_pk_bf16_f32 v229, v230, v231\n\t"
"v_mfma_f32_16x16x32_bf16 v[82:85], a[28:31], a[192:195], v[82:85]\n\t"
"s_add_u32 s29, s23, 786432\n\t"
"buffer_store_dwordx4 a[16:19], v248, s[16:19], s29 offen offset:0 nt\n\t"
"s_waitcnt lgkmcnt(7)\n\t"
"v_mfma_f32_16x16x32_bf16 v[224:227], v[224:227], v[220:223], 0\n\t"
"ds_read_b128 a[120:123], v252 offset:19456\n\t"
"v_mfma_f32_16x16x32_bf16 v[86:89], a[20:23], a[188:191], v[86:89]\n\t"
"ds_read_b128 a[188:191], v244 offset:11776\n\t"
"v_mfma_f32_16x16x32_bf16 v[86:89], a[104:107], a[192:195], v[86:89]\n\t"
"ds_read_b128 a[36:39], v252 offset:31744\n\t"
"v_mfma_f32_16x16x32_bf16 v[86:89], a[28:31], a[196:199], v[86:89]\n\t"
"ds_read_b128 a[160:163], v252 offset:20480\n\t"
"v_mfma_f32_16x16x32_bf16 v[90:93], a[20:23], a[192:195], v[90:93]\n\t"
"ds_read_b128 a[68:71], v252 offset:32768\n\t"
"v_mfma_f32_16x16x32_bf16 v[90:93], a[104:107], a[196:199], v[90:93]\n\t"
"v_cvt_pk_bf16_f32 v224, v224, v225\n\t"
"v_cvt_pk_bf16_f32 v225, v226, v227\n\t"
"v_mfma_f32_16x16x32_bf16 v[90:93], a[28:31], a[200:203], v[90:93]\n\t"
"ds_read_b128 a[192:195], v244 offset:12288\n\t"
"v_mfma_f32_16x16x32_bf16 v[94:97], a[20:23], a[196:199], v[94:97]\n\t"
"ds_read_b128 a[196:199], v244 offset:12800\n\t"
"v_mfma_f32_16x16x32_bf16 v[94:97], a[104:107], a[200:203], v[94:97]\n\t"
"ds_read_b128 a[200:203], v244 offset:13312\n\t"
"v_mfma_f32_16x16x32_bf16 v[94:97], a[28:31], a[204:207], v[94:97]\n\t"
"ds_read_b128 a[204:207], v244 offset:13824\n\t"
"s_waitcnt vmcnt(26) lgkmcnt(13)\n\t"
"v_mfma_f32_16x16x32_bf16 v[100:103], a[216:219], a[168:171], v[100:103]\n\t"
"v_mfma_f32_16x16x32_bf16 v[100:103], a[220:223], a[172:175], v[100:103]\n\t"
"v_mfma_f32_16x16x32_bf16 v[100:103], a[112:115], a[176:179], v[100:103]\n\t"
"s_add_u32 s29, s23, 786432\n\t"
"buffer_store_dwordx4 a[24:27], v248, s[16:19], s29 offen offset:128 nt\n\t"
"s_waitcnt lgkmcnt(12)\n\t"
"v_mfma_f32_16x16x32_bf16 v[104:107], a[216:219], a[172:175], v[104:107]\n\t"
"v_mfma_f32_16x16x32_bf16 v[104:107], a[220:223], a[176:179], v[104:107]\n\t"
"v_mfma_f32_16x16x32_bf16 v[104:107], a[112:115], a[180:183], v[104:107]\n\t"
"s_waitcnt lgkmcnt(0)\n\t"
"s_barrier\n\t"
"v_mfma_f32_16x16x32_bf16 v[108:111], a[216:219], a[176:179], v[108:111]\n\t"
"v_fma_f32 v196, -v2, s56, -v100\n\t"
"v_fma_f32 v197, -v3, s56, -v101\n\t"
"v_mfma_f32_16x16x32_bf16 v[108:111], a[220:223], a[180:183], v[108:111]\n\t"
"v_fma_f32 v198, -v4, s56, -v102\n\t"
"v_fma_f32 v199, -v5, s56, -v103\n\t"
"v_mfma_f32_16x16x32_bf16 v[108:111], a[112:115], a[184:187], v[108:111]\n\t"
"ds_write_b64 v245, v[232:233] offset:0\n\t"
"v_mfma_f32_16x16x32_bf16 v[112:115], a[216:219], a[180:183], v[112:115]\n\t"
"ds_read_b64_tr_b16 v[232:233], v246 offset:26112\n\t"
"v_fma_f32 v204, -v6, s56, -v104\n\t"
"v_mfma_f32_16x16x32_bf16 v[112:115], a[220:223], a[184:187], v[112:115]\n\t"
"ds_read_b64_tr_b16 v[234:235], v246 offset:29184\n\t"
"v_fma_f32 v205, -v7, s56, -v105\n\t"
"v_mfma_f32_16x16x32_bf16 v[112:115], a[112:115], a[188:191], v[112:115]\n\t"
"ds_write_b64 v245, v[236:237] offset:512\n\t"
"ds_write_b64 v245, v[228:229] offset:1024\n\t"
"v_mfma_f32_16x16x32_bf16 v[116:119], a[216:219], a[184:187], v[116:119]\n\t"
"ds_read_b64_tr_b16 v[236:237], v246 offset:32256\n\t"
"v_fma_f32 v206, -v8, s56, -v106\n\t"
"v_mfma_f32_16x16x32_bf16 v[116:119], a[220:223], a[188:191], v[116:119]\n\t"
"ds_read_b64_tr_b16 v[238:239], v246 offset:35328\n\t"
"v_fma_f32 v207, -v9, s56, -v107\n\t"
"v_mfma_f32_16x16x32_bf16 v[116:119], a[112:115], a[192:195], v[116:119]\n\t"
"ds_write_b64 v245, v[224:225] offset:1536\n\t"
"v_mfma_f32_16x16x32_bf16 v[120:123], a[216:219], a[188:191], v[120:123]\n\t"
"ds_read_b64_tr_b16 v[228:229], v246 offset:39936\n\t"
"v_fma_f32 v212, -v10, s56, -v108\n\t"
"v_mfma_f32_16x16x32_bf16 v[120:123], a[220:223], a[192:195], v[120:123]\n\t"
"ds_read_b64_tr_b16 v[230:231], v246 offset:43008\n\t"
"v_fma_f32 v213, -v11, s56, -v109\n\t"
"v_mfma_f32_16x16x32_bf16 v[120:123], a[112:115], a[196:199], v[120:123]\n\t"
"ds_write_b128 v249, a[76:79] offset:18432\n\t"
"ds_write_b128 v249, a[92:95] offset:30720\n\t"
"s_waitcnt lgkmcnt(9)\n\t"
"v_mfma_f32_16x16x32_bf16 v[232:235], v[232:235], v[220:223], 0\n\t"
"s_add_u32 s29, s23, 786432\n\t"
"buffer_store_dwordx4 a[32:35], v248, s[16:19], s29 offen offset:256 nt\n\t"
"v_mfma_f32_16x16x32_bf16 v[124:127], a[216:219], a[192:195], v[124:127]\n\t"
"ds_read_b64_tr_b16 v[224:225], v246 offset:46080\n\t"
"v_fma_f32 v214, -v12, s56, -v110\n\t"
"v_mfma_f32_16x16x32_bf16 v[124:127], a[220:223], a[196:199], v[124:127]\n\t"
"ds_read_b64_tr_b16 v[226:227], v246 offset:49152\n\t"
"v_fma_f32 v215, -v13, s56, -v111\n\t"
"v_mfma_f32_16x16x32_bf16 v[124:127], a[112:115], a[200:203], v[124:127]\n\t"
"ds_write_b64 v250, a[142:143] offset:18432\n\t"
"s_waitcnt lgkmcnt(8)\n\t"
"v_mfma_f32_16x16x32_bf16 v[236:239], v[236:239], v[220:223], 0\n\t"
"ds_write_b64 v250, a[158:159] offset:30720\n\t"
"v_mfma_f32_16x16x32_bf16 v[128:131], a[216:219], a[196:199], v[128:131]\n\t"
"v_cvt_pk_bf16_f32 v232, v232, v233\n\t"
"v_cvt_pk_bf16_f32 v233, v234, v235\n\t"
"v_mfma_f32_16x16x32_bf16 v[128:131], a[220:223], a[200:203], v[128:131]\n\t"
"v_fma_f32 v98, -v14, s56, -v112\n\t"
"v_fma_f32 v99, -v15, s56, -v113\n\t"
"v_mfma_f32_16x16x32_bf16 v[128:131], a[112:115], a[204:207], v[128:131]\n\t"
"s_mov_b32 s20, 147456\n\t"
"buffer_load_dwordx4 a[216:219], v252, s[8:11], s20 offen offset:0 sc1\n\t"
"s_waitcnt lgkmcnt(6)\n\t"
"v_mfma_f32_16x16x32_bf16 v[228:231], v[228:231], v[220:223], 0\n\t"
"buffer_load_dwordx4 a[220:223], v252, s[8:11], s20 offen offset:1024 sc1\n\t"
"s_waitcnt vmcnt(26)\n\t"
"v_mfma_f32_16x16x32_bf16 v[132:135], a[144:147], a[168:171], v[132:135]\n\t"
"v_cvt_pk_bf16_f32 v236, v236, v237\n\t"
"v_cvt_pk_bf16_f32 v237, v238, v239\n\t"
"v_mfma_f32_16x16x32_bf16 v[132:135], a[212:215], a[172:175], v[132:135]\n\t"
"v_accvgpr_write_b32 a0, v98\n\t"
"v_fma_f32 v98, -v16, s56, -v114\n\t"
"v_mfma_f32_16x16x32_bf16 v[132:135], a[224:227], a[176:179], v[132:135]\n\t"
"ds_write_b64 v245, v[232:233] offset:2048\n\t"
"ds_read_b64_tr_b16 v[232:233], v246 offset:52224\n\t"
"ds_read_b64_tr_b16 v[234:235], v246 offset:55296\n\t"
"s_waitcnt lgkmcnt(5)\n\t"
"v_mfma_f32_16x16x32_bf16 v[224:227], v[224:227], v[220:223], 0\n\t"
"s_add_u32 s29, s23, 1179648\n\t"
"buffer_store_dwordx4 a[48:51], v248, s[16:19], s29 offen offset:0 nt\n\t"
"ds_write_b64 v245, v[236:237] offset:2560\n\t"
"v_mfma_f32_16x16x32_bf16 v[136:139], a[144:147], a[172:175], v[136:139]\n\t"
"v_cvt_pk_bf16_f32 v228, v228, v229\n\t"
"v_cvt_pk_bf16_f32 v229, v230, v231\n\t"
"v_mfma_f32_16x16x32_bf16 v[136:139], a[212:215], a[176:179], v[136:139]\n\t"
"v_accvgpr_write_b32 a1, v99\n\t"
"v_fma_f32 v99, -v17, s56, -v115\n\t"
"v_mfma_f32_16x16x32_bf16 v[136:139], a[224:227], a[180:183], v[136:139]\n\t"
"ds_write_b128 v249, a[84:87] offset:24576\n\t"
"ds_write_b128 v249, a[100:103] offset:36864\n\t"
"v_mfma_f32_16x16x32_bf16 v[140:143], a[144:147], a[176:179], v[140:143]\n\t"
"v_accvgpr_write_b32 a2, v98\n\t"
"v_accvgpr_write_b32 a3, v99\n\t"
"v_mfma_f32_16x16x32_bf16 v[140:143], a[212:215], a[180:183], v[140:143]\n\t"
"v_cvt_pk_bf16_f32 v224, v224, v225\n\t"
"v_cvt_pk_bf16_f32 v225, v226, v227\n\t"
"v_mfma_f32_16x16x32_bf16 v[140:143], a[224:227], a[184:187], v[140:143]\n\t"
"ds_write_b64 v245, v[228:229] offset:3072\n\t"
"buffer_load_dwordx4 v[236:239], v252, s[8:11], s20 offen offset:2048 sc1\n\t"
"v_mfma_f32_16x16x32_bf16 v[144:147], a[144:147], a[180:183], v[144:147]\n\t"
"v_fma_f32 v98, -v18, s56, -v116\n\t"
"v_fma_f32 v99, -v19, s56, -v117\n\t"
"v_mfma_f32_16x16x32_bf16 v[144:147], a[212:215], a[184:187], v[144:147]\n\t"
"v_accvgpr_write_b32 a8, v98\n\t"
"v_fma_f32 v98, -v20, s56, -v118\n\t"
"v_mfma_f32_16x16x32_bf16 v[144:147], a[224:227], a[188:191], v[144:147]\n\t"
"ds_write_b64 v245, v[224:225] offset:3584\n\t"
"ds_write_b64 v250, a[150:151] offset:24576\n\t"
"s_waitcnt lgkmcnt(6)\n\t"
"v_mfma_f32_16x16x32_bf16 v[232:235], v[232:235], v[220:223], 0\n\t"
"ds_write_b64 v250, a[166:167] offset:36864\n\t"
"v_accvgpr_write_b32 a9, v99\n\t"
"v_fma_f32 v99, -v21, s56, -v119\n\t"
"v_mfma_f32_16x16x32_bf16 v[148:151], a[144:147], a[184:187], v[148:151]\n\t"
"v_accvgpr_write_b32 a10, v98\n\t"
"v_accvgpr_write_b32 a11, v99\n\t"
"v_mfma_f32_16x16x32_bf16 v[148:151], a[212:215], a[188:191], v[148:151]\n\t"
"v_fma_f32 v98, -v22, s56, -v120\n\t"
"v_fma_f32 v99, -v23, s56, -v121\n\t"
"v_mfma_f32_16x16x32_bf16 v[148:151], a[224:227], a[192:195], v[148:151]\n\t"
"s_add_u32 s29, s23, 1179648\n\t"
"buffer_store_dwordx4 a[56:59], v248, s[16:19], s29 offen offset:128 nt\n\t"
"v_accvgpr_write_b32 a16, v98\n\t"
"v_fma_f32 v98, -v24, s56, -v122\n\t"
"v_mfma_f32_16x16x32_bf16 v[152:155], a[144:147], a[188:191], v[152:155]\n\t"
"v_accvgpr_write_b32 a17, v99\n\t"
"v_fma_f32 v99, -v25, s56, -v123\n\t"
"v_mfma_f32_16x16x32_bf16 v[152:155], a[212:215], a[192:195], v[152:155]\n\t"
"v_cvt_pk_bf16_f32 v232, v232, v233\n\t"
"v_cvt_pk_bf16_f32 v233, v234, v235\n\t"
"v_mfma_f32_16x16x32_bf16 v[152:155], a[224:227], a[196:199], v[152:155]\n\t"
"ds_write_b64 v245, v[232:233] offset:4096\n\t"
"v_accvgpr_write_b32 a18, v98\n\t"
"v_accvgpr_write_b32 a19, v99\n\t"
"v_mfma_f32_16x16x32_bf16 v[156:159], a[144:147], a[192:195], v[156:159]\n\t"
"v_fma_f32 v98, -v26, s56, -v124\n\t"
"v_fma_f32 v99, -v27, s56, -v125\n\t"
"v_mfma_f32_16x16x32_bf16 v[156:159], a[212:215], a[196:199], v[156:159]\n\t"
"v_accvgpr_write_b32 a24, v98\n\t"
"v_fma_f32 v98, -v28, s56, -v126\n\t"
"v_mfma_f32_16x16x32_bf16 v[156:159], a[224:227], a[200:203], v[156:159]\n\t"
"v_accvgpr_write_b32 a25, v99\n\t"
"v_fma_f32 v99, -v29, s56, -v127\n\t"
"v_accvgpr_write_b32 a26, v98\n\t"
"v_accvgpr_write_b32 a27, v99\n\t"
"v_mfma_f32_16x16x32_bf16 v[160:163], a[144:147], a[196:199], v[160:163]\n\t"
"v_fma_f32 v98, -v30, s56, -v128\n\t"
"v_fma_f32 v99, -v31, s56, -v129\n\t"
"v_mfma_f32_16x16x32_bf16 v[160:163], a[212:215], a[200:203], v[160:163]\n\t"
"v_accvgpr_write_b32 a32, v98\n\t"
"v_fma_f32 v98, -v32, s56, -v130\n\t"
"v_mfma_f32_16x16x32_bf16 v[160:163], a[224:227], a[204:207], v[160:163]\n\t"
"s_mov_b32 s20, 159744\n\t"
"buffer_load_dwordx4 a[212:215], v252, s[8:11], s20 offen offset:0 sc1\n\t"
"v_accvgpr_write_b32 a33, v99\n\t"
"v_fma_f32 v99, -v33, s56, -v131\n\t"
"s_waitcnt vmcnt(26)\n\t"
"v_mfma_f32_16x16x32_bf16 v[164:167], a[80:83], a[168:171], v[164:167]\n\t"
"v_accvgpr_write_b32 a34, v98\n\t"
"v_accvgpr_write_b32 a35, v99\n\t"
"v_mfma_f32_16x16x32_bf16 v[164:167], a[96:99], a[172:175], v[164:167]\n\t"
"v_fma_f32 v98, -v34, s56, -v132\n\t"
"v_fma_f32 v99, -v35, s56, -v133\n\t"
"v_mfma_f32_16x16x32_bf16 v[164:167], a[228:231], a[176:179], v[164:167]\n\t"
"buffer_load_dwordx4 a[224:227], v252, s[8:11], s20 offen offset:1024 sc1\n\t"
"s_add_u32 s29, s23, 1179648\n\t"
"buffer_store_dwordx4 a[64:67], v248, s[16:19], s29 offen offset:256 nt\n\t"
"s_waitcnt lgkmcnt(0)\n\t"
"s_barrier\n\t"
"v_mfma_f32_16x16x32_bf16 v[168:171], a[80:83], a[172:175], v[168:171]\n\t"
"ds_read_b128 a[168:171], v244 offset:0\n\t"
"v_accvgpr_write_b32 a40, v98\n\t"
"v_mfma_f32_16x16x32_bf16 v[168:171], a[96:99], a[176:179], v[168:171]\n\t"
"ds_read_b128 a[172:175], v244 offset:512\n\t"
"v_fma_f32 v98, -v36, s56, -v134\n\t"
"v_mfma_f32_16x16x32_bf16 v[168:171], a[228:231], a[180:183], v[168:171]\n\t"
"v_accvgpr_write_b32 a41, v99\n\t"
"v_fma_f32 v99, -v37, s56, -v135\n\t"
"v_accvgpr_write_b32 a42, v98\n\t"
"v_accvgpr_write_b32 a43, v99\n\t"
"v_mfma_f32_16x16x32_bf16 v[172:175], a[80:83], a[176:179], v[172:175]\n\t"
"ds_read_b128 a[176:179], v244 offset:1024\n\t"
"v_fma_f32 v98, -v38, s56, -v136\n\t"
"v_mfma_f32_16x16x32_bf16 v[172:175], a[96:99], a[180:183], v[172:175]\n\t"
"v_fma_f32 v99, -v39, s56, -v137\n\t"
"v_accvgpr_write_b32 a48, v98\n\t"
"v_mfma_f32_16x16x32_bf16 v[172:175], a[228:231], a[184:187], v[172:175]\n\t"
"v_fma_f32 v98, -v40, s56, -v138\n\t"
"v_accvgpr_write_b32 a49, v99\n\t"
"v_fma_f32 v99, -v41, s56, -v139\n\t"
"v_accvgpr_write_b32 a50, v98\n\t"
"v_mfma_f32_16x16x32_bf16 v[176:179], a[80:83], a[180:183], v[176:179]\n\t"
"ds_read_b128 a[180:183], v244 offset:1536\n\t"
"v_accvgpr_write_b32 a51, v99\n\t"
"v_mfma_f32_16x16x32_bf16 v[176:179], a[96:99], a[184:187], v[176:179]\n\t"
"v_fma_f32 v98, -v42, s56, -v140\n\t"
"v_fma_f32 v99, -v43, s56, -v141\n\t"
"v_mfma_f32_16x16x32_bf16 v[176:179], a[228:231], a[188:191], v[176:179]\n\t"
"v_accvgpr_write_b32 a56, v98\n\t"
"v_fma_f32 v98, -v44, s56, -v142\n\t"
"v_accvgpr_write_b32 a57, v99\n\t"
"v_fma_f32 v99, -v45, s56, -v143\n\t"
"v_mfma_f32_16x16x32_bf16 v[180:183], a[80:83], a[184:187], v[180:183]\n\t"
"ds_read_b128 a[184:187], v244 offset:2048\n\t"
"v_accvgpr_write_b32 a58, v98\n\t"
"v_mfma_f32_16x16x32_bf16 v[180:183], a[96:99], a[188:191], v[180:183]\n\t"
"v_accvgpr_write_b32 a59, v99\n\t"
"v_fma_f32 v98, -v46, s56, -v144\n\t"
"v_mfma_f32_16x16x32_bf16 v[180:183], a[228:231], a[192:195], v[180:183]\n\t"
"v_fma_f32 v99, -v47, s56, -v145\n\t"
"v_accvgpr_write_b32 a64, v98\n\t"
"v_fma_f32 v98, -v48, s56, -v146\n\t"
"v_accvgpr_write_b32 a65, v99\n\t"
"v_mfma_f32_16x16x32_bf16 v[184:187], a[80:83], a[188:191], v[184:187]\n\t"
"ds_read_b128 a[188:191], v244 offset:2560\n\t"
"v_fma_f32 v99, -v49, s56, -v147\n\t"
"v_mfma_f32_16x16x32_bf16 v[184:187], a[96:99], a[192:195], v[184:187]\n\t"
"v_accvgpr_write_b32 a66, v98\n\t"
"v_accvgpr_write_b32 a67, v99\n\t"
"v_mfma_f32_16x16x32_bf16 v[184:187], a[228:231], a[196:199], v[184:187]\n\t"
"s_add_u32 s29, s23, 1152\n\t"
"buffer_store_dwordx4 a[88:91], v248, s[16:19], s29 offen offset:0 nt\n\t"
"v_fma_f32 v98, -v50, s56, -v148\n\t"
"v_fma_f32 v99, -v51, s56, -v149\n\t"
"v_mfma_f32_16x16x32_bf16 v[188:191], a[80:83], a[192:195], v[188:191]\n\t"
"ds_read_b128 a[192:195], v244 offset:3072\n\t"
"v_accvgpr_write_b32 a72, v98\n\t"
"v_mfma_f32_16x16x32_bf16 v[188:191], a[96:99], a[196:199], v[188:191]\n\t"
"v_fma_f32 v98, -v52, s56, -v150\n\t"
"v_accvgpr_write_b32 a73, v99\n\t"
"v_mfma_f32_16x16x32_bf16 v[188:191], a[228:231], a[200:203], v[188:191]\n\t"
"v_fma_f32 v99, -v53, s56, -v151\n\t"
"v_accvgpr_write_b32 a74, v98\n\t"
"v_accvgpr_write_b32 a75, v99\n\t"
"v_fma_f32 v200, v100, s64, -v2\n\t"
"v_mfma_f32_16x16x32_bf16 v[192:195], a[80:83], a[196:199], v[192:195]\n\t"
"ds_read_b128 a[196:199], v244 offset:3584\n\t"
"v_fma_f32 v98, -v54, s56, -v152\n\t"
"v_mfma_f32_16x16x32_bf16 v[192:195], a[96:99], a[200:203], v[192:195]\n\t"
"ds_read_b128 a[200:203], v244 offset:4096\n\t"
"v_fma_f32 v99, -v55, s56, -v153\n\t"
"v_mfma_f32_16x16x32_bf16 v[192:195], a[228:231], a[204:207], v[192:195]\n\t"
"buffer_load_dwordx4 a[228:231], v252, s[8:11], s20 offen offset:2048 sc1\n\t"
"ds_read_b128 a[204:207], v244 offset:4608\n\t"
"v_accvgpr_write_b32 a80, v98\n\t"
"Lst20_%=:\n\t"
"s_waitcnt vmcnt(25) lgkmcnt(7)\n\t"
"v_mfma_f32_16x16x32_bf16 v[196:199], a[232:235], a[168:171], v[196:199]\n\t"
"v_xor_b32 v246, 0xc0, v246\n\t"
"v_fma_f32 v98, -v56, s56, -v154\n\t"
"v_mfma_f32_16x16x32_bf16 v[196:199], a[236:239], a[172:175], v[196:199]\n\t"
"v_accvgpr_write_b32 a81, v99\n\t"
"v_fma_f32 v99, -v57, s56, -v155\n\t"
"v_mfma_f32_16x16x32_bf16 v[196:199], a[208:211], a[176:179], v[196:199]\n\t"
"v_accvgpr_write_b32 a82, v98\n\t"
"v_accvgpr_write_b32 a83, v99\n\t"
"v_fma_f32 v98, -v58, s56, -v156\n\t"
"v_fma_f32 v99, -v59, s56, -v157\n\t"
"s_waitcnt lgkmcnt(6)\n\t"
"v_mfma_f32_16x16x32_bf16 v[204:207], a[232:235], a[172:175], v[204:207]\n\t"
"v_accvgpr_write_b32 a88, v98\n\t"
"v_fma_f32 v98, -v60, s56, -v158\n\t"
"v_mfma_f32_16x16x32_bf16 v[204:207], a[236:239], a[176:179], v[204:207]\n\t"
"v_accvgpr_write_b32 a89, v99\n\t"
"v_fma_f32 v99, -v61, s56, -v159\n\t"
"v_mfma_f32_16x16x32_bf16 v[204:207], a[208:211], a[180:183], v[204:207]\n\t"
"v_accvgpr_write_b32 a90, v98\n\t"
"v_accvgpr_write_b32 a91, v99\n\t"
"v_fma_f32 v98, -v62, s56, -v160\n\t"
"v_fma_f32 v99, -v63, s56, -v161\n\t"
"s_waitcnt lgkmcnt(5)\n\t"
"v_mfma_f32_16x16x32_bf16 v[212:215], a[232:235], a[176:179], v[212:215]\n\t"
"v_accvgpr_write_b32 a96, v98\n\t"
"v_fma_f32 v98, -v64, s56, -v162\n\t"
"v_mfma_f32_16x16x32_bf16 v[212:215], a[236:239], a[180:183], v[212:215]\n\t"
"v_accvgpr_write_b32 a97, v99\n\t"
"v_fma_f32 v99, -v65, s56, -v163\n\t"
"v_mfma_f32_16x16x32_bf16 v[212:215], a[208:211], a[184:187], v[212:215]\n\t"
"s_add_u32 s29, s23, 1152\n\t"
"buffer_store_dwordx4 a[120:123], v248, s[16:19], s29 offen offset:128 nt\n\t"
"v_accvgpr_write_b32 a98, v98\n\t"
"v_accvgpr_write_b32 a99, v99\n\t"
"s_waitcnt lgkmcnt(4)\n\t"
"v_mfma_f32_16x16x32_bf16 a[0:3], a[232:235], a[180:183], a[0:3]\n\t"
"v_fma_f32 v98, -v66, s56, -v164\n\t"
"v_fma_f32 v99, -v67, s56, -v165\n\t"
"v_mfma_f32_16x16x32_bf16 a[0:3], a[236:239], a[184:187], a[0:3]\n\t"
"v_accvgpr_write_b32 a104, v98\n\t"
"v_fma_f32 v98, -v68, s56, -v166\n\t"
"v_mfma_f32_16x16x32_bf16 a[0:3], a[208:211], a[188:191], a[0:3]\n\t"
"v_accvgpr_write_b32 a105, v99\n\t"
"v_fma_f32 v99, -v69, s56, -v167\n\t"
"v_accvgpr_write_b32 a106, v98\n\t"
"v_accvgpr_write_b32 a107, v99\n\t"
"s_waitcnt lgkmcnt(3)\n\t"
"v_mfma_f32_16x16x32_bf16 a[8:11], a[232:235], a[184:187], a[8:11]\n\t"
"v_fma_f32 v98, -v70, s56, -v168\n\t"
"v_fma_f32 v99, -v71, s56, -v169\n\t"
"v_mfma_f32_16x16x32_bf16 a[8:11], a[236:239], a[188:191], a[8:11]\n\t"
"v_accvgpr_write_b32 a112, v98\n\t"
"v_fma_f32 v98, -v72, s56, -v170\n\t"
"v_mfma_f32_16x16x32_bf16 a[8:11], a[208:211], a[192:195], a[8:11]\n\t"
"ds_read_b64_tr_b16 v[228:229], v246 offset:0\n\t"
"ds_read_b64_tr_b16 v[230:231], v246 offset:3072\n\t"
"v_accvgpr_write_b32 a113, v99\n\t"
"v_fma_f32 v99, -v73, s56, -v171\n\t"
"s_waitcnt lgkmcnt(4)\n\t"
"v_mfma_f32_16x16x32_bf16 a[16:19], a[232:235], a[188:191], a[16:19]\n\t"
"v_accvgpr_write_b32 a114, v98\n\t"
"v_accvgpr_write_b32 a115, v99\n\t"
"v_mfma_f32_16x16x32_bf16 a[16:19], a[236:239], a[192:195], a[16:19]\n\t"
"v_fma_f32 v98, -v74, s56, -v172\n\t"
"v_fma_f32 v99, -v75, s56, -v173\n\t"
"v_mfma_f32_16x16x32_bf16 a[16:19], a[208:211], a[196:199], a[16:19]\n\t"
"v_accvgpr_write_b32 a120, v98\n\t"
"v_fma_f32 v98, -v76, s56, -v174\n\t"
"v_accvgpr_write_b32 a121, v99\n\t"
"v_fma_f32 v99, -v77, s56, -v175\n\t"
"s_waitcnt lgkmcnt(3)\n\t"
"v_mfma_f32_16x16x32_bf16 a[24:27], a[232:235], a[192:195], a[24:27]\n\t"
"v_accvgpr_write_b32 a122, v98\n\t"
"v_accvgpr_write_b32 a123, v99\n\t"
"v_mfma_f32_16x16x32_bf16 a[24:27], a[236:239], a[196:199], a[24:27]\n\t"
"v_fma_f32 v98, -v78, s56, -v176\n\t"
"v_fma_f32 v99, -v79, s56, -v177\n\t"
"v_mfma_f32_16x16x32_bf16 a[24:27], a[208:211], a[200:203], a[24:27]\n\t"
"s_add_u32 s29, s23, 1152\n\t"
"buffer_store_dwordx4 a[160:163], v248, s[16:19], s29 offen offset:256 nt\n\t"
"v_accvgpr_write_b32 a128, v98\n\t"
"v_fma_f32 v98, -v80, s56, -v178\n\t"
"s_waitcnt lgkmcnt(2)\n\t"
"v_mfma_f32_16x16x32_bf16 a[32:35], a[232:235], a[196:199], a[32:35]\n\t"
"ds_read_b64_tr_b16 v[224:225], v246 offset:6144\n\t"
"v_accvgpr_write_b32 a129, v99\n\t"
"v_mfma_f32_16x16x32_bf16 a[32:35], a[236:239], a[200:203], a[32:35]\n\t"
"ds_read_b64_tr_b16 v[226:227], v246 offset:9216\n\t"
"v_fma_f32 v99, -v81, s56, -v179\n\t"
"v_mfma_f32_16x16x32_bf16 a[32:35], a[208:211], a[204:207], a[32:35]\n\t"
"s_mov_b32 s20, 172032\n\t"
"buffer_load_dwordx4 a[232:235], v252, s[8:11], s20 offen offset:0 sc1\n\t"
"v_accvgpr_write_b32 a130, v98\n\t"
"v_accvgpr_write_b32 a131, v99\n\t"
"s_waitcnt vmcnt(24)\n\t"
"v_mfma_f32_16x16x32_bf16 a[40:43], a[240:243], a[168:171], a[40:43]\n\t"
"v_fma_f32 v98, -v82, s56, -v180\n\t"
"v_fma_f32 v99, -v83, s56, -v181\n\t"
"v_mfma_f32_16x16x32_bf16 a[40:43], a[244:247], a[172:175], a[40:43]\n\t"
"v_accvgpr_write_b32 a136, v98\n\t"
"v_fma_f32 v98, -v84, s56, -v182\n\t"
"v_mfma_f32_16x16x32_bf16 a[40:43], v[240:243], a[176:179], a[40:43]\n\t"
"buffer_load_dwordx4 a[208:211], v252, s[8:11], s20 offen offset:1024 sc1\n\t"
"v_accvgpr_write_b32 a137, v99\n\t"
"v_fma_f32 v99, -v85, s56, -v183\n\t"
"v_mfma_f32_16x16x32_bf16 a[48:51], a[240:243], a[172:175], a[48:51]\n\t"
"v_accvgpr_write_b32 a138, v98\n\t"
"v_accvgpr_write_b32 a139, v99\n\t"
"v_mfma_f32_16x16x32_bf16 a[48:51], a[244:247], a[176:179], a[48:51]\n\t"
"v_fma_f32 v98, -v86, s56, -v184\n\t"
"v_fma_f32 v99, -v87, s56, -v185\n\t"
"v_mfma_f32_16x16x32_bf16 a[48:51], v[240:243], a[180:183], a[48:51]\n\t"
"buffer_load_dwordx4 a[236:239], v252, s[8:11], s20 offen offset:2048 sc1\n\t"
"v_accvgpr_write_b32 a144, v98\n\t"
"v_fma_f32 v98, -v88, s56, -v186\n\t"
"s_waitcnt lgkmcnt(2)\n\t"
"v_mfma_f32_16x16x32_bf16 v[228:231], v[228:231], v[220:223], 0\n\t"
"v_accvgpr_write_b32 a145, v99\n\t"
"v_fma_f32 v99, -v89, s56, -v187\n\t"
"v_accvgpr_write_b32 a146, v98\n\t"
"v_accvgpr_write_b32 a147, v99\n\t"
"v_mfma_f32_16x16x32_bf16 a[56:59], a[240:243], a[176:179], a[56:59]\n\t"
"v_fma_f32 v98, -v90, s56, -v188\n\t"
"v_fma_f32 v99, -v91, s56, -v189\n\t"
"v_mfma_f32_16x16x32_bf16 a[56:59], a[244:247], a[180:183], a[56:59]\n\t"
"v_accvgpr_write_b32 a152, v98\n\t"
"v_fma_f32 v98, -v92, s56, -v190\n\t"
"v_mfma_f32_16x16x32_bf16 a[56:59], v[240:243], a[184:187], a[56:59]\n\t"
"v_accvgpr_write_b32 a153, v99\n\t"
"v_fma_f32 v99, -v93, s56, -v191\n\t"
"v_accvgpr_write_b32 a154, v98\n\t"
"v_accvgpr_write_b32 a155, v99\n\t"
"v_mfma_f32_16x16x32_bf16 a[64:67], a[240:243], a[180:183], a[64:67]\n\t"
"v_fma_f32 v98, -v94, s56, -v192\n\t"
"v_fma_f32 v99, -v95, s56, -v193\n\t"
"v_mfma_f32_16x16x32_bf16 a[64:67], a[244:247], a[184:187], a[64:67]\n\t"
"v_cvt_pk_bf16_f32 v228, v228, v229\n\t"
"v_cvt_pk_bf16_f32 v229, v230, v231\n\t"
"v_mfma_f32_16x16x32_bf16 a[64:67], v[240:243], a[188:191], a[64:67]\n\t"
"s_add_u32 s29, s23, 394368\n\t"
"buffer_store_dwordx4 a[4:7], v248, s[16:19], s29 offen offset:0 nt\n\t"
"v_accvgpr_write_b32 a160, v98\n\t"
"v_fma_f32 v98, -v96, s56, -v194\n\t"
"s_waitcnt lgkmcnt(0)\n\t"
"v_mfma_f32_16x16x32_bf16 v[224:227], v[224:227], v[220:223], 0\n\t"
"v_accvgpr_write_b32 a161, v99\n\t"
"v_fma_f32 v99, -v97, s56, -v195\n\t"
"v_accvgpr_write_b32 a162, v98\n\t"
"v_accvgpr_write_b32 a163, v99\n\t"
"s_barrier\n\t"
"v_mfma_f32_16x16x32_bf16 a[72:75], a[240:243], a[184:187], a[72:75]\n\t"
"ds_read_b64_tr_b16 v[232:233], v246 offset:12288\n\t"
"v_fma_f32 v201, v101, s64, -v3\n\t"
"v_mfma_f32_16x16x32_bf16 a[72:75], a[244:247], a[188:191], a[72:75]\n\t"
"ds_read_b64_tr_b16 v[234:235], v246 offset:15360\n\t"
"v_fma_f32 v202, v102, s64, -v4\n\t"
"v_mfma_f32_16x16x32_bf16 a[72:75], v[240:243], a[192:195], a[72:75]\n\t"
"v_fma_f32 v203, v103, s64, -v5\n\t"
"v_fma_f32 v208, v104, s64, -v6\n\t"
"v_fma_f32 v209, v105, s64, -v7\n\t"
"v_fma_f32 v210, v106, s64, -v8\n\t"
"v_mfma_f32_16x16x32_bf16 a[80:83], a[240:243], a[188:191], a[80:83]\n\t"
"v_fma_f32 v211, v107, s64, -v9\n\t"
"v_fma_f32 v216, v108, s64, -v10\n\t"
"v_mfma_f32_16x16x32_bf16 a[80:83], a[244:247], a[192:195], a[80:83]\n\t"
"v_cvt_pk_bf16_f32 v230, v224, v225\n\t"
"v_cvt_pk_bf16_f32 v231, v226, v227\n\t"
"v_mfma_f32_16x16x32_bf16 a[80:83], v[240:243], a[196:199], a[80:83]\n\t"
"ds_read_b64_tr_b16 v[224:225], v246 offset:19968\n\t"
"ds_read_b64_tr_b16 v[226:227], v246 offset:23040\n\t"
"v_fma_f32 v217, v109, s64, -v11\n\t"
"v_fma_f32 v218, v110, s64, -v12\n\t"
"v_mfma_f32_16x16x32_bf16 a[88:91], a[240:243], a[192:195], a[88:91]\n\t"
"v_fma_f32 v219, v111, s64, -v13\n\t"
"v_fma_f32 v98, v112, s64, -v14\n\t"
"v_mfma_f32_16x16x32_bf16 a[88:91], a[244:247], a[196:199], a[88:91]\n\t"
"v_fma_f32 v99, v113, s64, -v15\n\t"
"v_accvgpr_write_b32 a4, v98\n\t"
"v_mfma_f32_16x16x32_bf16 a[88:91], v[240:243], a[200:203], a[88:91]\n\t"
"v_fma_f32 v98, v114, s64, -v16\n\t"
"v_accvgpr_write_b32 a5, v99\n\t"
"v_fma_f32 v99, v115, s64, -v17\n\t"
"v_accvgpr_write_b32 a6, v98\n\t"
"s_waitcnt lgkmcnt(2)\n\t"
"v_mfma_f32_16x16x32_bf16 v[232:235], v[232:235], v[220:223], 0\n\t"
"v_accvgpr_write_b32 a7, v99\n\t"
"v_fma_f32 v98, v116, s64, -v18\n\t"
"v_fma_f32 v99, v117, s64, -v19\n\t"
"v_accvgpr_write_b32 a12, v98\n\t"
"v_mfma_f32_16x16x32_bf16 a[96:99], a[240:243], a[196:199], a[96:99]\n\t"
"v_fma_f32 v98, v118, s64, -v20\n\t"
"v_accvgpr_write_b32 a13, v99\n\t"
"v_mfma_f32_16x16x32_bf16 a[96:99], a[244:247], a[200:203], a[96:99]\n\t"
"v_fma_f32 v99, v119, s64, -v21\n\t"
"v_accvgpr_write_b32 a14, v98\n\t"
"v_mfma_f32_16x16x32_bf16 a[96:99], v[240:243], a[204:207], a[96:99]\n\t"
"s_mov_b32 s20, 36864\n\t"
"buffer_load_dwordx4 a[240:243], v252, s[8:11], s20 offen offset:0 sc1\n\t"
"s_add_u32 s29, s23, 394368\n\t"
"buffer_store_dwordx4 a[36:39], v248, s[16:19], s29 offen offset:128 nt\n\t"
"s_waitcnt vmcnt(22)\n\t"
"v_mfma_f32_16x16x32_bf16 a[104:107], a[248:251], a[168:171], a[104:107]\n\t"
"ds_read_b128 a[168:171], v244 offset:9216\n\t"
"v_accvgpr_write_b32 a15, v99\n\t"
"v_mfma_f32_16x16x32_bf16 a[104:107], a[252:255], a[172:175], a[104:107]\n\t"
"v_cvt_pk_bf16_f32 v232, v232, v233\n\t"
"v_cvt_pk_bf16_f32 v233, v234, v235\n\t"
"v_mfma_f32_16x16x32_bf16 a[104:107], a[108:111], a[176:179], a[104:107]\n\t"
"buffer_load_dwordx4 v[240:243], v252, s[8:11], s20 offen offset:1024 sc1\n\t"
"v_fma_f32 v98, v120, s64, -v22\n\t"
"v_fma_f32 v99, v121, s64, -v23\n\t"
"v_mfma_f32_16x16x32_bf16 a[112:115], a[248:251], a[172:175], a[112:115]\n\t"
"ds_read_b128 a[172:175], v244 offset:9728\n\t"
"v_accvgpr_write_b32 a20, v98\n\t"
"v_mfma_f32_16x16x32_bf16 a[112:115], a[252:255], a[176:179], a[112:115]\n\t"
"v_fma_f32 v98, v122, s64, -v24\n\t"
"v_accvgpr_write_b32 a21, v99\n\t"
"v_mfma_f32_16x16x32_bf16 a[112:115], a[108:111], a[180:183], a[112:115]\n\t"
"buffer_load_dwordx4 a[244:247], v252, s[8:11], s20 offen offset:2048 sc1\n\t"
"v_fma_f32 v99, v123, s64, -v25\n\t"
"v_accvgpr_write_b32 a22, v98\n\t"
"s_waitcnt lgkmcnt(2)\n\t"
"v_mfma_f32_16x16x32_bf16 v[224:227], v[224:227], v[220:223], 0\n\t"
"v_accvgpr_write_b32 a23, v99\n\t"
"v_fma_f32 v98, v124, s64, -v26\n\t"
"v_fma_f32 v99, v125, s64, -v27\n\t"
"v_accvgpr_write_b32 a28, v98\n\t"
"v_mfma_f32_16x16x32_bf16 a[120:123], a[248:251], a[176:179], a[120:123]\n\t"
"ds_read_b128 a[176:179], v244 offset:10240\n\t"
"v_fma_f32 v98, v126, s64, -v28\n\t"
"v_mfma_f32_16x16x32_bf16 a[120:123], a[252:255], a[180:183], a[120:123]\n\t"
"v_accvgpr_write_b32 a29, v99\n\t"
"v_fma_f32 v99, v127, s64, -v29\n\t"
"v_mfma_f32_16x16x32_bf16 a[120:123], a[108:111], a[184:187], a[120:123]\n\t"
"v_accvgpr_write_b32 a30, v98\n\t"
"v_accvgpr_write_b32 a31, v99\n\t"
"v_fma_f32 v98, v128, s64, -v30\n\t"
"v_fma_f32 v99, v129, s64, -v31\n\t"
"v_mfma_f32_16x16x32_bf16 a[128:131], a[248:251], a[180:183], a[128:131]\n\t"
"ds_read_b128 a[180:183], v244 offset:10752\n\t"
"v_accvgpr_write_b32 a36, v98\n\t"
"v_mfma_f32_16x16x32_bf16 a[128:131], a[252:255], a[184:187], a[128:131]\n\t"
"v_cvt_pk_bf16_f32 v234, v224, v225\n\t"
"v_cvt_pk_bf16_f32 v235, v226, v227\n\t"
"v_mfma_f32_16x16x32_bf16 a[128:131], a[108:111], a[188:191], a[128:131]\n\t"
"ds_read_b64_tr_b16 v[224:225], v246 offset:26112\n\t"
"ds_read_b64_tr_b16 v[226:227], v246 offset:29184\n\t"
"s_add_u32 s29, s23, 394368\n\t"
"buffer_store_dwordx4 a[68:71], v248, s[16:19], s29 offen offset:256 nt\n\t"
"v_mfma_f32_16x16x32_bf16 a[136:139], a[248:251], a[184:187], a[136:139]\n\t"
"ds_read_b128 a[184:187], v244 offset:11264\n\t"
"v_fma_f32 v98, v130, s64, -v32\n\t"
"v_mfma_f32_16x16x32_bf16 a[136:139], a[252:255], a[188:191], a[136:139]\n\t"
"v_accvgpr_write_b32 a37, v99\n\t"
"v_fma_f32 v99, v131, s64, -v33\n\t"
"v_mfma_f32_16x16x32_bf16 a[136:139], a[108:111], a[192:195], a[136:139]\n\t"
"v_accvgpr_write_b32 a38, v98\n\t"
"v_accvgpr_write_b32 a39, v99\n\t"
"v_fma_f32 v98, v132, s64, -v34\n\t"
"v_fma_f32 v99, v133, s64, -v35\n\t"
"v_mfma_f32_16x16x32_bf16 a[144:147], a[248:251], a[188:191], a[144:147]\n\t"
"ds_read_b128 a[188:191], v244 offset:11776\n\t"
"v_accvgpr_write_b32 a44, v98\n\t"
"v_mfma_f32_16x16x32_bf16 a[144:147], a[252:255], a[192:195], a[144:147]\n\t"
"v_fma_f32 v98, v134, s64, -v36\n\t"
"v_accvgpr_write_b32 a45, v99\n\t"
"v_mfma_f32_16x16x32_bf16 a[144:147], a[108:111], a[196:199], a[144:147]\n\t"
"s_mov_b32 s20, 49152\n\t"
"buffer_load_dwordx4 v[248:251], v252, s[8:11], s20 offen offset:0 sc1\n\t"
"v_fma_f32 v99, v135, s64, -v37\n\t"
"v_accvgpr_write_b32 a46, v98\n\t"
"v_mfma_f32_16x16x32_bf16 a[152:155], a[248:251], a[192:195], a[152:155]\n\t"
"ds_read_b128 a[192:195], v244 offset:12288\n\t"
"v_accvgpr_write_b32 a47, v99\n\t"
"v_mfma_f32_16x16x32_bf16 a[152:155], a[252:255], a[196:199], a[152:155]\n\t"
"v_fma_f32 v98, v136, s64, -v38\n\t"
"v_fma_f32 v99, v137, s64, -v39\n\t"
"v_mfma_f32_16x16x32_bf16 a[152:155], a[108:111], a[200:203], a[152:155]\n\t"
"v_accvgpr_write_b32 a52, v98\n\t"
"v_fma_f32 v98, v138, s64, -v40\n\t"
"v_accvgpr_write_b32 a53, v99\n\t"
"v_fma_f32 v99, v139, s64, -v41\n\t"
"v_mfma_f32_16x16x32_bf16 a[160:163], a[248:251], a[196:199], a[160:163]\n\t"
"ds_read_b128 a[196:199], v244 offset:12800\n\t"
"v_accvgpr_write_b32 a54, v98\n\t"
"v_mfma_f32_16x16x32_bf16 a[160:163], a[252:255], a[200:203], a[160:163]\n\t"
"ds_read_b128 a[200:203], v244 offset:13312\n\t"
"v_accvgpr_write_b32 a55, v99\n\t"
"v_mfma_f32_16x16x32_bf16 a[160:163], a[108:111], a[204:207], a[160:163]\n\t"
"buffer_load_dwordx4 a[248:251], v252, s[8:11], s20 offen offset:1024 sc1\n\t"
"ds_read_b128 a[204:207], v244 offset:13824\n\t"
"v_fma_f32 v98, v140, s64, -v42\n\t"
"s_waitcnt lgkmcnt(6)\n\t"
"v_mfma_f32_16x16x32_bf16 v[224:227], v[224:227], v[220:223], 0\n\t"
"buffer_load_dwordx4 a[252:255], v252, s[8:11], s20 offen offset:2048 sc1\n\t"
"v_fma_f32 v99, v141, s64, -v43\n\t"
"v_accvgpr_write_b32 a60, v98\n\t"
"s_waitcnt vmcnt(20)\n\t"
"v_mfma_f32_16x16x32_bf16 v[200:203], a[216:219], a[168:171], v[200:203]\n\t"
"v_fma_f32 v98, v142, s64, -v44\n\t"
"v_accvgpr_write_b32 a61, v99\n\t"
"v_mfma_f32_16x16x32_bf16 v[200:203], a[220:223], a[172:175], v[200:203]\n\t"
"v_fma_f32 v99, v143, s64, -v45\n\t"
"v_accvgpr_write_b32 a62, v98\n\t"
"v_mfma_f32_16x16x32_bf16 v[200:203], v[236:239], a[176:179], v[200:203]\n\t"
"v_accvgpr_write_b32 a63, v99\n\t"
"v_fma_f32 v98, v144, s64, -v46\n\t"
"v_fma_f32 v99, v145, s64, -v47\n\t"
"v_accvgpr_write_b32 a68, v98\n\t"
"v_mfma_f32_16x16x32_bf16 v[208:211], a[216:219], a[172:175], v[208:211]\n\t"
"v_fma_f32 v98, v146, s64, -v48\n\t"
"v_accvgpr_write_b32 a69, v99\n\t"
"v_mfma_f32_16x16x32_bf16 v[208:211], a[220:223], a[176:179], v[208:211]\n\t"
"v_cvt_pk_bf16_f32 v224, v224, v225\n\t"
"v_cvt_pk_bf16_f32 v225, v226, v227\n\t"
"v_mfma_f32_16x16x32_bf16 v[208:211], v[236:239], a[180:183], v[208:211]\n\t"
"v_fma_f32 v99, v147, s64, -v49\n\t"
"v_accvgpr_write_b32 a70, v98\n\t"
"v_accvgpr_write_b32 a71, v99\n\t"
"v_fma_f32 v98, v148, s64, -v50\n\t"
"s_waitcnt lgkmcnt(0)\n\t"
"s_barrier\n\t"
"v_mfma_f32_16x16x32_bf16 v[216:219], a[216:219], a[176:179], v[216:219]\n\t"
"v_fma_f32 v99, v149, s64, -v51\n\t"
"v_accvgpr_write_b32 a76, v98\n\t"
"v_mfma_f32_16x16x32_bf16 v[216:219], a[220:223], a[180:183], v[216:219]\n\t"
"v_fma_f32 v98, v150, s64, -v52\n\t"
"v_accvgpr_write_b32 a77, v99\n\t"
"v_mfma_f32_16x16x32_bf16 v[216:219], v[236:239], a[184:187], v[216:219]\n\t"
"ds_write2st64_b64 v245, v[228:229], v[230:231] offset0:0 offset1:1\n\t"
"ds_write2st64_b64 v245, v[232:233], v[234:235] offset0:2 offset1:3\n\t"
"v_mfma_f32_16x16x32_bf16 a[4:7], a[216:219], a[180:183], a[4:7]\n\t"
"ds_read_b64_tr_b16 v[228:229], v246 offset:32256\n\t"
"v_fma_f32 v99, v151, s64, -v53\n\t"
"v_mfma_f32_16x16x32_bf16 a[4:7], a[220:223], a[184:187], a[4:7]\n\t"
"ds_read_b64_tr_b16 v[230:231], v246 offset:35328\n\t"
"v_accvgpr_write_b32 a78, v98\n\t"
"v_mfma_f32_16x16x32_bf16 a[4:7], v[236:239], a[188:191], a[4:7]\n\t"
"ds_write_b64 v245, v[224:225] offset:2048\n\t"
"ds_read_b64_tr_b16 v[232:233], v246 offset:39936\n\t"
"ds_read_b64_tr_b16 v[234:235], v246 offset:43008\n\t"
"v_mfma_f32_16x16x32_bf16 a[12:15], a[216:219], a[184:187], a[12:15]\n\t"
"ds_read_b64_tr_b16 v[224:225], v246 offset:46080\n\t"
"v_accvgpr_write_b32 a79, v99\n\t"
"v_mfma_f32_16x16x32_bf16 a[12:15], a[220:223], a[188:191], a[12:15]\n\t"
"ds_read_b64_tr_b16 v[226:227], v246 offset:49152\n\t"
"v_fma_f32 v98, v152, s64, -v54\n\t"
"v_mfma_f32_16x16x32_bf16 a[12:15], v[236:239], a[192:195], a[12:15]\n\t"
"v_fma_f32 v99, v153, s64, -v55\n\t"
"v_accvgpr_write_b32 a84, v98\n\t"
"v_fma_f32 v98, v154, s64, -v56\n\t"
"v_accvgpr_write_b32 a85, v99\n\t"
"v_mfma_f32_16x16x32_bf16 a[20:23], a[216:219], a[188:191], a[20:23]\n\t"
"v_fma_f32 v99, v155, s64, -v57\n\t"
"v_accvgpr_write_b32 a86, v98\n\t"
"v_mfma_f32_16x16x32_bf16 a[20:23], a[220:223], a[192:195], a[20:23]\n\t"
"v_accvgpr_write_b32 a87, v99\n\t"
"v_fma_f32 v98, v156, s64, -v58\n\t"
"v_mfma_f32_16x16x32_bf16 a[20:23], v[236:239], a[196:199], a[20:23]\n\t"
"v_fma_f32 v99, v157, s64, -v59\n\t"
"v_accvgpr_write_b32 a92, v98\n\t"
"v_fma_f32 v98, v158, s64, -v60\n\t"
"v_accvgpr_write_b32 a93, v99\n\t"
"s_waitcnt lgkmcnt(5)\n\t"
"v_mfma_f32_16x16x32_bf16 v[228:231], v[228:231], v[220:223], 0\n\t"
"v_fma_f32 v99, v159, s64, -v61\n\t"
"v_accvgpr_write_b32 a94, v98\n\t"
"v_accvgpr_write_b32 a95, v99\n\t"
"v_fma_f32 v98, v160, s64, -v62\n\t"
"v_mfma_f32_16x16x32_bf16 a[28:31], a[216:219], a[192:195], a[28:31]\n\t"
"v_fma_f32 v99, v161, s64, -v63\n\t"
"v_accvgpr_write_b32 a100, v98\n\t"
"v_mfma_f32_16x16x32_bf16 a[28:31], a[220:223], a[196:199], a[28:31]\n\t"
"v_fma_f32 v98, v162, s64, -v64\n\t"
"v_accvgpr_write_b32 a101, v99\n\t"
"v_mfma_f32_16x16x32_bf16 a[28:31], v[236:239], a[200:203], a[28:31]\n\t"
"v_fma_f32 v99, v163, s64, -v65\n\t"
"v_accvgpr_write_b32 a102, v98\n\t"
"v_accvgpr_write_b32 a103, v99\n\t"
"v_fma_f32 v98, v164, s64, -v66\n\t"
"s_waitcnt lgkmcnt(2)\n\t"
"v_mfma_f32_16x16x32_bf16 v[232:235], v[232:235], v[220:223], 0\n\t"
"v_fma_f32 v99, v165, s64, -v67\n\t"
"v_accvgpr_write_b32 a108, v98\n\t"
"v_fma_f32 v98, v166, s64, -v68\n\t"
"v_accvgpr_write_b32 a109, v99\n\t"
"v_mfma_f32_16x16x32_bf16 a[36:39], a[216:219], a[196:199], a[36:39]\n\t"
"v_cvt_pk_bf16_f32 v228, v228, v229\n\t"
"v_cvt_pk_bf16_f32 v229, v230, v231\n\t"
"v_mfma_f32_16x16x32_bf16 a[36:39], a[220:223], a[200:203], a[36:39]\n\t"
"v_fma_f32 v99, v167, s64, -v69\n\t"
"v_accvgpr_write_b32 a110, v98\n\t"
"v_mfma_f32_16x16x32_bf16 a[36:39], v[236:239], a[204:207], a[36:39]\n\t"
"s_mov_b32 s20, 61440\n\t"
"buffer_load_dwordx4 a[216:219], v252, s[8:11], s20 offen offset:0 sc1\n\t"
"v_accvgpr_write_b32 a111, v99\n\t"
"v_fma_f32 v98, v168, s64, -v70\n\t"
"s_waitcnt lgkmcnt(0)\n\t"
"v_mfma_f32_16x16x32_bf16 v[224:227], v[224:227], v[220:223], 0\n\t"
"buffer_load_dwordx4 v[236:239], v252, s[8:11], s20 offen offset:1024 sc1\n\t"
"v_fma_f32 v99, v169, s64, -v71\n\t"
"v_accvgpr_write_b32 a116, v98\n\t"
"s_waitcnt vmcnt(16)\n\t"
"v_mfma_f32_16x16x32_bf16 a[44:47], a[212:215], a[168:171], a[44:47]\n\t"
"v_cvt_pk_bf16_f32 v230, v232, v233\n\t"
"v_cvt_pk_bf16_f32 v231, v234, v235\n\t"
"v_mfma_f32_16x16x32_bf16 a[44:47], a[224:227], a[172:175], a[44:47]\n\t"
"ds_read_b64_tr_b16 v[232:233], v246 offset:52224\n\t"
"v_fma_f32 v98, v170, s64, -v72\n\t"
"v_mfma_f32_16x16x32_bf16 a[44:47], a[228:231], a[176:179], a[44:47]\n\t"
"buffer_load_dwordx4 a[220:223], v252, s[8:11], s20 offen offset:2048 sc1\n\t"
"ds_write2st64_b64 v245, v[228:229], v[230:231] offset0:5 offset1:6\n\t"
"v_mfma_f32_16x16x32_bf16 a[52:55], a[212:215], a[172:175], a[52:55]\n\t"
"ds_read_b64_tr_b16 v[234:235], v246 offset:55296\n\t"
"v_accvgpr_write_b32 a117, v99\n\t"
"v_mfma_f32_16x16x32_bf16 a[52:55], a[224:227], a[176:179], a[52:55]\n\t"
"v_cvt_pk_bf16_f32 v224, v224, v225\n\t"
"v_cvt_pk_bf16_f32 v225, v226, v227\n\t"
"v_mfma_f32_16x16x32_bf16 a[52:55], a[228:231], a[180:183], a[52:55]\n\t"
"v_fma_f32 v99, v171, s64, -v73\n\t"
"v_accvgpr_write_b32 a118, v98\n\t"
"v_accvgpr_write_b32 a119, v99\n\t"
"v_fma_f32 v98, v172, s64, -v74\n\t"
"v_mfma_f32_16x16x32_bf16 a[60:63], a[212:215], a[176:179], a[60:63]\n\t"
"v_fma_f32 v99, v173, s64, -v75\n\t"
"v_accvgpr_write_b32 a124, v98\n\t"
"v_mfma_f32_16x16x32_bf16 a[60:63], a[224:227], a[180:183], a[60:63]\n\t"
"v_fma_f32 v98, v174, s64, -v76\n\t"
"v_accvgpr_write_b32 a125, v99\n\t"
"v_mfma_f32_16x16x32_bf16 a[60:63], a[228:231], a[184:187], a[60:63]\n\t"
"v_fma_f32 v99, v175, s64, -v77\n\t"
"v_accvgpr_write_b32 a126, v98\n\t"
"v_accvgpr_write_b32 a127, v99\n\t"
"v_fma_f32 v98, v176, s64, -v78\n\t"
"v_mfma_f32_16x16x32_bf16 a[68:71], a[212:215], a[180:183], a[68:71]\n\t"
"v_fma_f32 v99, v177, s64, -v79\n\t"
"v_accvgpr_write_b32 a132, v98\n\t"
"v_mfma_f32_16x16x32_bf16 a[68:71], a[224:227], a[184:187], a[68:71]\n\t"
"v_fma_f32 v98, v178, s64, -v80\n\t"
"v_accvgpr_write_b32 a133, v99\n\t"
"v_mfma_f32_16x16x32_bf16 a[68:71], a[228:231], a[188:191], a[68:71]\n\t"
"s_add_u32 m0, s24, 896\n\t"
"s_add_u32 s28, s21, 0\n\t"
"buffer_load_dwordx4 v247, s[12:15], s28 offen offset:128 nt lds\n\t"
"s_add_u32 m0, s24, 60800\n\t"
"s_add_u32 s28, s21, 1769472\n\t"
"buffer_load_dwordx4 v247, s[12:15], s28 offen offset:128 nt lds\n\t"
"v_mfma_f32_16x16x32_bf16 a[76:79], a[212:215], a[184:187], a[76:79]\n\t"
"v_fma_f32 v99, v179, s64, -v81\n\t"
"v_accvgpr_write_b32 a134, v98\n\t"
"v_mfma_f32_16x16x32_bf16 a[76:79], a[224:227], a[188:191], a[76:79]\n\t"
"v_accvgpr_write_b32 a135, v99\n\t"
"v_fma_f32 v98, v180, s64, -v82\n\t"
"v_mfma_f32_16x16x32_bf16 a[76:79], a[228:231], a[192:195], a[76:79]\n\t"
"s_add_u32 m0, s24, 7040\n\t"
"s_add_u32 s28, s21, 196608\n\t"
"buffer_load_dwordx4 v247, s[12:15], s28 offen offset:128 nt lds\n\t"
"v_fma_f32 v99, v181, s64, -v83\n\t"
"v_accvgpr_write_b32 a140, v98\n\t"
"s_waitcnt lgkmcnt(0)\n\t"
"v_mfma_f32_16x16x32_bf16 v[232:235], v[232:235], v[220:223], 0\n\t"
"s_add_u32 m0, s24, 13184\n\t"
"s_add_u32 s28, s21, 393216\n\t"
"buffer_load_dwordx4 v247, s[12:15], s28 offen offset:128 nt lds\n\t"
"v_fma_f32 v98, v182, s64, -v84\n\t"
"v_accvgpr_write_b32 a141, v99\n\t"
"v_mfma_f32_16x16x32_bf16 a[84:87], a[212:215], a[188:191], a[84:87]\n\t"
"v_fma_f32 v99, v183, s64, -v85\n\t"
"v_accvgpr_write_b32 a142, v98\n\t"
"v_mfma_f32_16x16x32_bf16 a[84:87], a[224:227], a[192:195], a[84:87]\n\t"
"v_accvgpr_write_b32 a143, v99\n\t"
"v_fma_f32 v98, v184, s64, -v86\n\t"
"v_mfma_f32_16x16x32_bf16 a[84:87], a[228:231], a[196:199], a[84:87]\n\t"
"s_add_u32 m0, s24, 20864\n\t"
"s_add_u32 s28, s21, 589824\n\t"
"buffer_load_dwordx4 v247, s[12:15], s28 offen offset:128 nt lds\n\t"
"v_fma_f32 v99, v185, s64, -v87\n\t"
"v_accvgpr_write_b32 a148, v98\n\t"
"v_mfma_f32_16x16x32_bf16 a[92:95], a[212:215], a[192:195], a[92:95]\n\t"
"v_fma_f32 v98, v186, s64, -v88\n\t"
"v_accvgpr_write_b32 a149, v99\n\t"
"v_mfma_f32_16x16x32_bf16 a[92:95], a[224:227], a[196:199], a[92:95]\n\t"
"v_cvt_pk_bf16_f32 v226, v232, v233\n\t"
"v_cvt_pk_bf16_f32 v227, v234, v235\n\t"
"v_mfma_f32_16x16x32_bf16 a[92:95], a[228:231], a[200:203], a[92:95]\n\t"
"s_add_u32 m0, s24, 66944\n\t"
"s_add_u32 s28, s21, 1966080\n\t"
"buffer_load_dwordx4 v247, s[12:15], s28 offen offset:128 nt lds\n\t"
"ds_write2st64_b64 v245, v[224:225], v[226:227] offset0:7 offset1:8\n\t"
"v_mfma_f32_16x16x32_bf16 a[100:103], a[212:215], a[196:199], a[100:103]\n\t"
"v_fma_f32 v99, v187, s64, -v89\n\t"
"v_accvgpr_write_b32 a150, v98\n\t"
"v_mfma_f32_16x16x32_bf16 a[100:103], a[224:227], a[200:203], a[100:103]\n\t"
"v_accvgpr_write_b32 a151, v99\n\t"
"v_fma_f32 v98, v188, s64, -v90\n\t"
"v_mfma_f32_16x16x32_bf16 a[100:103], a[228:231], a[204:207], a[100:103]\n\t"
"v_fma_f32 v99, v189, s64, -v91\n\t"
"v_accvgpr_write_b32 a156, v98\n\t"
"v_fma_f32 v98, v190, s64, -v92\n\t"
"v_accvgpr_write_b32 a157, v99\n\t"
"s_waitcnt vmcnt(18)\n\t"
"v_mfma_f32_16x16x32_bf16 a[108:111], a[232:235], a[168:171], a[108:111]\n\t"
"v_fma_f32 v99, v191, s64, -v93\n\t"
"v_accvgpr_write_b32 a158, v98\n\t"
"v_mfma_f32_16x16x32_bf16 a[108:111], a[208:211], a[172:175], a[108:111]\n\t"
"v_accvgpr_write_b32 a159, v99\n\t"
"v_fma_f32 v98, v192, s64, -v94\n\t"
"v_mfma_f32_16x16x32_bf16 a[108:111], a[236:239], a[176:179], a[108:111]\n\t"
"v_fma_f32 v99, v193, s64, -v95\n\t"
"v_accvgpr_write_b32 a164, v98\n\t"
"v_fma_f32 v98, v194, s64, -v96\n\t"
"v_accvgpr_write_b32 a165, v99\n\t"
"s_waitcnt lgkmcnt(0)\n\t"
"s_barrier\n\t"
"v_mfma_f32_16x16x32_bf16 a[116:119], a[232:235], a[172:175], a[116:119]\n\t"
"ds_read_b128 a[168:171], v244 offset:0\n\t"
"v_fma_f32 v99, v195, s64, -v97\n\t"
"v_mfma_f32_16x16x32_bf16 a[116:119], a[208:211], a[176:179], a[116:119]\n\t"
"ds_read_b128 a[172:175], v244 offset:512\n\t"
"v_accvgpr_write_b32 a166, v98\n\t"
"v_mfma_f32_16x16x32_bf16 a[116:119], a[236:239], a[180:183], a[116:119]\n\t"
"v_accvgpr_write_b32 a167, v99\n\t"
"v_fma_f32 v0, v2, s52, v100\n\t"
"v_fma_f32 v1, v3, s52, v101\n\t"
"v_fma_f32 v100, -v100, s66, v2\n\t"
"v_mfma_f32_16x16x32_bf16 a[124:127], a[232:235], a[176:179], a[124:127]\n\t"
"ds_read_b128 a[176:179], v244 offset:1024\n\t"
"v_fma_f32 v2, v4, s52, v102\n\t"
"v_mfma_f32_16x16x32_bf16 a[124:127], a[208:211], a[180:183], a[124:127]\n\t"
"v_fma_f32 v101, -v101, s66, v3\n\t"
"v_fma_f32 v3, v5, s52, v103\n\t"
"v_mfma_f32_16x16x32_bf16 a[124:127], a[236:239], a[184:187], a[124:127]\n\t"
"v_fma_f32 v102, -v102, s66, v4\n\t"
"v_fma_f32 v103, -v103, s66, v5\n\t"
"v_fma_f32 v4, v6, s52, v104\n\t"
"v_fma_f32 v5, v7, s52, v105\n\t"
"v_mfma_f32_16x16x32_bf16 a[132:135], a[232:235], a[180:183], a[132:135]\n\t"
"ds_read_b128 a[180:183], v244 offset:1536\n\t"
"v_fma_f32 v104, -v104, s66, v6\n\t"
"v_mfma_f32_16x16x32_bf16 a[132:135], a[208:211], a[184:187], a[132:135]\n\t"
"v_fma_f32 v6, v8, s52, v106\n\t"
"v_fma_f32 v105, -v105, s66, v7\n\t"
"v_mfma_f32_16x16x32_bf16 a[132:135], a[236:239], a[188:191], a[132:135]\n\t"
"s_mov_b32 s20, 73728\n\t"
"buffer_load_dwordx4 a[212:215], v252, s[8:11], s20 offen offset:0 sc1\n\t"
"v_fma_f32 v7, v9, s52, v107\n\t"
"v_fma_f32 v106, -v106, s66, v8\n\t"
"v_mfma_f32_16x16x32_bf16 a[140:143], a[232:235], a[184:187], a[140:143]\n\t"
"ds_read_b128 a[184:187], v244 offset:2048\n\t"
"v_fma_f32 v107, -v107, s66, v9\n\t"
"v_mfma_f32_16x16x32_bf16 a[140:143], a[208:211], a[188:191], a[140:143]\n\t"
"v_fma_f32 v8, v10, s52, v108\n\t"
"v_fma_f32 v9, v11, s52, v109\n\t"
"v_mfma_f32_16x16x32_bf16 a[140:143], a[236:239], a[192:195], a[140:143]\n\t"
"v_fma_f32 v108, -v108, s66, v10\n\t"
"v_fma_f32 v10, v12, s52, v110\n\t"
"v_fma_f32 v109, -v109, s66, v11\n\t"
"v_fma_f32 v11, v13, s52, v111\n\t"
"v_mfma_f32_16x16x32_bf16 a[148:151], a[232:235], a[188:191], a[148:151]\n\t"
"ds_read_b128 a[188:191], v244 offset:2560\n\t"
"v_fma_f32 v110, -v110, s66, v12\n\t"
"v_mfma_f32_16x16x32_bf16 a[148:151], a[208:211], a[192:195], a[148:151]\n\t"
"v_fma_f32 v111, -v111, s66, v13\n\t"
"v_fma_f32 v12, v14, s52, v112\n\t"
"v_mfma_f32_16x16x32_bf16 a[148:151], a[236:239], a[196:199], a[148:151]\n\t"
"v_fma_f32 v13, v15, s52, v113\n\t"
"v_fma_f32 v112, -v112, s66, v14\n\t"
"v_fma_f32 v14, v16, s52, v114\n\t"
"v_fma_f32 v113, -v113, s66, v15\n\t"
"v_mfma_f32_16x16x32_bf16 a[156:159], a[232:235], a[192:195], a[156:159]\n\t"
"ds_read_b128 a[192:195], v244 offset:3072\n\t"
"v_fma_f32 v15, v17, s52, v115\n\t"
"v_mfma_f32_16x16x32_bf16 a[156:159], a[208:211], a[196:199], a[156:159]\n\t"
"v_fma_f32 v114, -v114, s66, v16\n\t"
"v_fma_f32 v115, -v115, s66, v17\n\t"
"v_mfma_f32_16x16x32_bf16 a[156:159], a[236:239], a[200:203], a[156:159]\n\t"
"v_fma_f32 v16, v18, s52, v116\n\t"
"v_fma_f32 v17, v19, s52, v117\n\t"
"v_fma_f32 v116, -v116, s66, v18\n\t"
"v_fma_f32 v18, v20, s52, v118\n\t"
"v_mfma_f32_16x16x32_bf16 a[164:167], a[232:235], a[196:199], a[164:167]\n\t"
"ds_read_b128 a[196:199], v244 offset:3584\n\t"
"v_fma_f32 v117, -v117, s66, v19\n\t"
"v_mfma_f32_16x16x32_bf16 a[164:167], a[208:211], a[200:203], a[164:167]\n\t"
"ds_read_b128 a[200:203], v244 offset:4096\n\t"
"v_fma_f32 v19, v21, s52, v119\n\t"
"v_mfma_f32_16x16x32_bf16 a[164:167], a[236:239], a[204:207], a[164:167]\n\t"
"ds_read_b128 a[204:207], v244 offset:4608\n\t"
"v_fma_f32 v118, -v118, s66, v20\n\t"
"v_fma_f32 v119, -v119, s66, v21\n\t"
"v_fma_f32 v20, v22, s52, v120\n\t"
"Lst21_%=:\n\t"
"s_waitcnt vmcnt(14) lgkmcnt(7)\n\t"
"v_mfma_f32_16x16x32_bf16 v[0:3], a[240:243], a[168:171], v[0:3]\n\t"
"v_fma_f32 v21, v23, s52, v121\n\t"
"v_fma_f32 v120, -v120, s66, v22\n\t"
"v_mfma_f32_16x16x32_bf16 v[0:3], v[240:243], a[172:175], v[0:3]\n\t"
"v_fma_f32 v22, v24, s52, v122\n\t"
"v_fma_f32 v121, -v121, s66, v23\n\t"
"v_mfma_f32_16x16x32_bf16 v[0:3], a[244:247], a[176:179], v[0:3]\n\t"
"v_fma_f32 v23, v25, s52, v123\n\t"
"v_fma_f32 v122, -v122, s66, v24\n\t"
"v_fma_f32 v123, -v123, s66, v25\n\t"
"v_fma_f32 v24, v26, s52, v124\n\t"
"s_waitcnt lgkmcnt(6)\n\t"
"v_mfma_f32_16x16x32_bf16 v[4:7], a[240:243], a[172:175], v[4:7]\n\t"
"v_fma_f32 v25, v27, s52, v125\n\t"
"v_fma_f32 v124, -v124, s66, v26\n\t"
"v_mfma_f32_16x16x32_bf16 v[4:7], v[240:243], a[176:179], v[4:7]\n\t"
"v_fma_f32 v26, v28, s52, v126\n\t"
"v_fma_f32 v125, -v125, s66, v27\n\t"
"v_mfma_f32_16x16x32_bf16 v[4:7], a[244:247], a[180:183], v[4:7]\n\t"
"v_fma_f32 v27, v29, s52, v127\n\t"
"v_fma_f32 v126, -v126, s66, v28\n\t"
"v_fma_f32 v127, -v127, s66, v29\n\t"
"v_fma_f32 v28, v30, s52, v128\n\t"
"s_waitcnt lgkmcnt(5)\n\t"
"v_mfma_f32_16x16x32_bf16 v[8:11], a[240:243], a[176:179], v[8:11]\n\t"
"v_fma_f32 v29, v31, s52, v129\n\t"
"v_fma_f32 v128, -v128, s66, v30\n\t"
"v_mfma_f32_16x16x32_bf16 v[8:11], v[240:243], a[180:183], v[8:11]\n\t"
"v_fma_f32 v30, v32, s52, v130\n\t"
"v_fma_f32 v129, -v129, s66, v31\n\t"
"v_mfma_f32_16x16x32_bf16 v[8:11], a[244:247], a[184:187], v[8:11]\n\t"
"v_fma_f32 v31, v33, s52, v131\n\t"
"v_fma_f32 v130, -v130, s66, v32\n\t"
"v_fma_f32 v131, -v131, s66, v33\n\t"
"v_fma_f32 v32, v34, s52, v132\n\t"
"s_waitcnt lgkmcnt(4)\n\t"
"v_mfma_f32_16x16x32_bf16 v[12:15], a[240:243], a[180:183], v[12:15]\n\t"
"v_fma_f32 v33, v35, s52, v133\n\t"
"v_fma_f32 v132, -v132, s66, v34\n\t"
"v_mfma_f32_16x16x32_bf16 v[12:15], v[240:243], a[184:187], v[12:15]\n\t"
"v_fma_f32 v34, v36, s52, v134\n\t"
"v_fma_f32 v133, -v133, s66, v35\n\t"
"v_mfma_f32_16x16x32_bf16 v[12:15], a[244:247], a[188:191], v[12:15]\n\t"
"buffer_load_dwordx4 a[208:211], v252, s[8:11], s20 offen offset:1024 sc1\n\t"
"v_fma_f32 v35, v37, s52, v135\n\t"
"v_fma_f32 v134, -v134, s66, v36\n\t"
"s_waitcnt lgkmcnt(3)\n\t"
"v_mfma_f32_16x16x32_bf16 v[16:19], a[240:243], a[184:187], v[16:19]\n\t"
"v_fma_f32 v135, -v135, s66, v37\n\t"
"v_fma_f32 v36, v38, s52, v136\n\t"
"v_mfma_f32_16x16x32_bf16 v[16:19], v[240:243], a[188:191], v[16:19]\n\t"
"v_fma_f32 v37, v39, s52, v137\n\t"
"v_fma_f32 v136, -v136, s66, v38\n\t"
"v_mfma_f32_16x16x32_bf16 v[16:19], a[244:247], a[192:195], v[16:19]\n\t"
"buffer_load_dwordx4 a[224:227], v252, s[8:11], s20 offen offset:2048 sc1\n\t"
"ds_read_b64_tr_b16 v[228:229], v246 offset:512\n\t"
"ds_read_b64_tr_b16 v[230:231], v246 offset:3584\n\t"
"s_waitcnt lgkmcnt(4)\n\t"
"v_mfma_f32_16x16x32_bf16 v[20:23], a[240:243], a[188:191], v[20:23]\n\t"
"v_fma_f32 v38, v40, s52, v138\n\t"
"v_fma_f32 v137, -v137, s66, v39\n\t"
"v_mfma_f32_16x16x32_bf16 v[20:23], v[240:243], a[192:195], v[20:23]\n\t"
"v_fma_f32 v39, v41, s52, v139\n\t"
"v_fma_f32 v138, -v138, s66, v40\n\t"
"v_mfma_f32_16x16x32_bf16 v[20:23], a[244:247], a[196:199], v[20:23]\n\t"
"s_mov_b32 s20, 86016\n\t"
"buffer_load_dwordx4 a[228:231], v252, s[8:11], s20 offen offset:0 sc1\n\t"
"v_fma_f32 v139, -v139, s66, v41\n\t"
"v_fma_f32 v40, v42, s52, v140\n\t"
"s_waitcnt lgkmcnt(3)\n\t"
"v_mfma_f32_16x16x32_bf16 v[24:27], a[240:243], a[192:195], v[24:27]\n\t"
"v_fma_f32 v41, v43, s52, v141\n\t"
"v_fma_f32 v140, -v140, s66, v42\n\t"
"v_mfma_f32_16x16x32_bf16 v[24:27], v[240:243], a[196:199], v[24:27]\n\t"
"v_fma_f32 v42, v44, s52, v142\n\t"
"v_fma_f32 v141, -v141, s66, v43\n\t"
"v_mfma_f32_16x16x32_bf16 v[24:27], a[244:247], a[200:203], v[24:27]\n\t"
"buffer_load_dwordx4 a[232:235], v252, s[8:11], s20 offen offset:1024 sc1\n\t"
"v_fma_f32 v43, v45, s52, v143\n\t"
"v_fma_f32 v142, -v142, s66, v44\n\t"
"s_waitcnt lgkmcnt(2)\n\t"
"v_mfma_f32_16x16x32_bf16 v[28:31], a[240:243], a[196:199], v[28:31]\n\t"
"v_fma_f32 v143, -v143, s66, v45\n\t"
"ds_read_b64_tr_b16 v[232:233], v246 offset:6656\n\t"
"v_mfma_f32_16x16x32_bf16 v[28:31], v[240:243], a[200:203], v[28:31]\n\t"
"ds_read_b64_tr_b16 v[234:235], v246 offset:9728\n\t"
"v_fma_f32 v44, v46, s52, v144\n\t"
"v_mfma_f32_16x16x32_bf16 v[28:31], a[244:247], a[204:207], v[28:31]\n\t"
"buffer_load_dwordx4 v[240:243], v252, s[8:11], s20 offen offset:2048 sc1\n\t"
"v_fma_f32 v45, v47, s52, v145\n\t"
"v_fma_f32 v144, -v144, s66, v46\n\t"
"s_waitcnt vmcnt(15)\n\t"
"v_mfma_f32_16x16x32_bf16 v[32:35], v[248:251], a[168:171], v[32:35]\n\t"
"v_fma_f32 v46, v48, s52, v146\n\t"
"v_fma_f32 v145, -v145, s66, v47\n\t"
"v_mfma_f32_16x16x32_bf16 v[32:35], a[248:251], a[172:175], v[32:35]\n\t"
"v_fma_f32 v47, v49, s52, v147\n\t"
"v_fma_f32 v146, -v146, s66, v48\n\t"
"v_mfma_f32_16x16x32_bf16 v[32:35], a[252:255], a[176:179], v[32:35]\n\t"
"s_mov_b32 s20, 98304\n\t"
"buffer_load_dwordx4 a[236:239], v252, s[8:11], s20 offen offset:0 sc1\n\t"
"v_fma_f32 v147, -v147, s66, v49\n\t"
"v_fma_f32 v48, v50, s52, v148\n\t"
"v_mfma_f32_16x16x32_bf16 v[36:39], v[248:251], a[172:175], v[36:39]\n\t"
"v_fma_f32 v49, v51, s52, v149\n\t"
"v_fma_f32 v148, -v148, s66, v50\n\t"
"v_mfma_f32_16x16x32_bf16 v[36:39], a[248:251], a[176:179], v[36:39]\n\t"
"v_fma_f32 v50, v52, s52, v150\n\t"
"v_fma_f32 v149, -v149, s66, v51\n\t"
"v_mfma_f32_16x16x32_bf16 v[36:39], a[252:255], a[180:183], v[36:39]\n\t"
"buffer_load_dwordx4 a[240:243], v252, s[8:11], s20 offen offset:1024 sc1\n\t"
"v_fma_f32 v51, v53, s52, v151\n\t"
"v_fma_f32 v150, -v150, s66, v52\n\t"
"s_waitcnt lgkmcnt(2)\n\t"
"v_mfma_f32_16x16x32_bf16 v[228:231], v[228:231], v[220:223], 0\n\t"
"v_fma_f32 v151, -v151, s66, v53\n\t"
"v_fma_f32 v52, v54, s52, v152\n\t"
"v_fma_f32 v53, v55, s52, v153\n\t"
"v_fma_f32 v152, -v152, s66, v54\n\t"
"v_mfma_f32_16x16x32_bf16 v[40:43], v[248:251], a[176:179], v[40:43]\n\t"
"v_fma_f32 v54, v56, s52, v154\n\t"
"v_fma_f32 v153, -v153, s66, v55\n\t"
"v_mfma_f32_16x16x32_bf16 v[40:43], a[248:251], a[180:183], v[40:43]\n\t"
"v_fma_f32 v55, v57, s52, v155\n\t"
"v_fma_f32 v154, -v154, s66, v56\n\t"
"v_mfma_f32_16x16x32_bf16 v[40:43], a[252:255], a[184:187], v[40:43]\n\t"
"buffer_load_dwordx4 a[244:247], v252, s[8:11], s20 offen offset:2048 sc1\n\t"
"v_fma_f32 v155, -v155, s66, v57\n\t"
"ds_read_b64_tr_b16 v[224:225], v246 offset:12800\n\t"
"v_mfma_f32_16x16x32_bf16 v[44:47], v[248:251], a[180:183], v[44:47]\n\t"
"ds_read_b64_tr_b16 v[226:227], v246 offset:15872\n\t"
"v_fma_f32 v56, v58, s52, v156\n\t"
"v_mfma_f32_16x16x32_bf16 v[44:47], a[248:251], a[184:187], v[44:47]\n\t"
"v_cvt_pk_bf16_f32 v228, v228, v229\n\t"
"v_cvt_pk_bf16_f32 v229, v230, v231\n\t"
"v_mfma_f32_16x16x32_bf16 v[44:47], a[252:255], a[188:191], v[44:47]\n\t"
"v_fma_f32 v57, v59, s52, v157\n\t"
"v_fma_f32 v156, -v156, s66, v58\n\t"
"v_fma_f32 v58, v60, s52, v158\n\t"
"v_fma_f32 v157, -v157, s66, v59\n\t"
"s_waitcnt lgkmcnt(2)\n\t"
"v_mfma_f32_16x16x32_bf16 v[232:235], v[232:235], v[220:223], 0\n\t"
"v_fma_f32 v59, v61, s52, v159\n\t"
"v_fma_f32 v158, -v158, s66, v60\n\t"
"v_fma_f32 v159, -v159, s66, v61\n\t"
"v_fma_f32 v60, v62, s52, v160\n\t"
"v_mfma_f32_16x16x32_bf16 v[48:51], v[248:251], a[184:187], v[48:51]\n\t"
"v_fma_f32 v61, v63, s52, v161\n\t"
"v_fma_f32 v160, -v160, s66, v62\n\t"
"v_mfma_f32_16x16x32_bf16 v[48:51], a[248:251], a[188:191], v[48:51]\n\t"
"v_fma_f32 v62, v64, s52, v162\n\t"
"v_fma_f32 v161, -v161, s66, v63\n\t"
"v_mfma_f32_16x16x32_bf16 v[48:51], a[252:255], a[192:195], v[48:51]\n\t"
"v_fma_f32 v63, v65, s52, v163\n\t"
"v_fma_f32 v162, -v162, s66, v64\n\t"
"v_fma_f32 v163, -v163, s66, v65\n\t"
"v_fma_f32 v64, v66, s52, v164\n\t"
"v_mfma_f32_16x16x32_bf16 v[52:55], v[248:251], a[188:191], v[52:55]\n\t"
"v_fma_f32 v65, v67, s52, v165\n\t"
"v_fma_f32 v164, -v164, s66, v66\n\t"
"v_mfma_f32_16x16x32_bf16 v[52:55], a[248:251], a[192:195], v[52:55]\n\t"
"v_cvt_pk_bf16_f32 v230, v232, v233\n\t"
"v_cvt_pk_bf16_f32 v231, v234, v235\n\t"
"v_mfma_f32_16x16x32_bf16 v[52:55], a[252:255], a[196:199], v[52:55]\n\t"
"v_fma_f32 v66, v68, s52, v166\n\t"
"v_fma_f32 v165, -v165, s66, v67\n\t"
"v_fma_f32 v67, v69, s52, v167\n\t"
"v_fma_f32 v166, -v166, s66, v68\n\t"
"v_mfma_f32_16x16x32_bf16 v[56:59], v[248:251], a[192:195], v[56:59]\n\t"
"v_fma_f32 v167, -v167, s66, v69\n\t"
"ds_read_b64_tr_b16 v[232:233], v246 offset:20480\n\t"
"v_mfma_f32_16x16x32_bf16 v[56:59], a[248:251], a[196:199], v[56:59]\n\t"
"ds_read_b64_tr_b16 v[234:235], v246 offset:23552\n\t"
"v_fma_f32 v68, v70, s52, v168\n\t"
"v_mfma_f32_16x16x32_bf16 v[56:59], a[252:255], a[200:203], v[56:59]\n\t"
"v_fma_f32 v69, v71, s52, v169\n\t"
"v_fma_f32 v168, -v168, s66, v70\n\t"
"v_fma_f32 v70, v72, s52, v170\n\t"
"v_fma_f32 v169, -v169, s66, v71\n\t"
"s_waitcnt lgkmcnt(2)\n\t"
"v_mfma_f32_16x16x32_bf16 v[224:227], v[224:227], v[220:223], 0\n\t"
"v_fma_f32 v71, v73, s52, v171\n\t"
"v_fma_f32 v170, -v170, s66, v72\n\t"
"v_fma_f32 v171, -v171, s66, v73\n\t"
"v_fma_f32 v72, v74, s52, v172\n\t"
"v_mfma_f32_16x16x32_bf16 v[60:63], v[248:251], a[196:199], v[60:63]\n\t"
"v_fma_f32 v73, v75, s52, v173\n\t"
"v_fma_f32 v172, -v172, s66, v74\n\t"
"v_mfma_f32_16x16x32_bf16 v[60:63], a[248:251], a[200:203], v[60:63]\n\t"
"v_fma_f32 v74, v76, s52, v174\n\t"
"v_fma_f32 v173, -v173, s66, v75\n\t"
"v_mfma_f32_16x16x32_bf16 v[60:63], a[252:255], a[204:207], v[60:63]\n\t"
"s_mov_b32 s20, 258048\n\t"
"buffer_load_dwordx4 v[248:251], v252, s[8:11], s20 offen offset:0 sc1\n\t"
"v_fma_f32 v75, v77, s52, v175\n\t"
"v_fma_f32 v174, -v174, s66, v76\n\t"
"s_waitcnt vmcnt(16)\n\t"
"v_mfma_f32_16x16x32_bf16 v[64:67], a[216:219], a[168:171], v[64:67]\n\t"
"v_fma_f32 v175, -v175, s66, v77\n\t"
"v_fma_f32 v76, v78, s52, v176\n\t"
"v_mfma_f32_16x16x32_bf16 v[64:67], v[236:239], a[172:175], v[64:67]\n\t"
"v_cvt_pk_bf16_f32 v224, v224, v225\n\t"
"v_cvt_pk_bf16_f32 v225, v226, v227\n\t"
"v_mfma_f32_16x16x32_bf16 v[64:67], a[220:223], a[176:179], v[64:67]\n\t"
"buffer_load_dwordx4 a[248:251], v252, s[8:11], s20 offen offset:1024 sc1\n\t"
"v_fma_f32 v77, v79, s52, v177\n\t"
"v_fma_f32 v176, -v176, s66, v78\n\t"
"v_mfma_f32_16x16x32_bf16 v[68:71], a[216:219], a[172:175], v[68:71]\n\t"
"v_fma_f32 v78, v80, s52, v178\n\t"
"v_fma_f32 v177, -v177, s66, v79\n\t"
"v_mfma_f32_16x16x32_bf16 v[68:71], v[236:239], a[176:179], v[68:71]\n\t"
"v_fma_f32 v79, v81, s52, v179\n\t"
"v_fma_f32 v178, -v178, s66, v80\n\t"
"v_mfma_f32_16x16x32_bf16 v[68:71], a[220:223], a[180:183], v[68:71]\n\t"
"buffer_load_dwordx4 a[252:255], v252, s[8:11], s20 offen offset:2048 sc1\n\t"
"v_fma_f32 v179, -v179, s66, v81\n\t"
"v_fma_f32 v80, v82, s52, v180\n\t"
"s_waitcnt lgkmcnt(0)\n\t"
"v_mfma_f32_16x16x32_bf16 v[232:235], v[232:235], v[220:223], 0\n\t"
"v_fma_f32 v81, v83, s52, v181\n\t"
"v_fma_f32 v180, -v180, s66, v82\n\t"
"v_fma_f32 v82, v84, s52, v182\n\t"
"v_fma_f32 v181, -v181, s66, v83\n\t"
"v_mfma_f32_16x16x32_bf16 v[72:75], a[216:219], a[176:179], v[72:75]\n\t"
"v_fma_f32 v83, v85, s52, v183\n\t"
"v_fma_f32 v182, -v182, s66, v84\n\t"
"v_mfma_f32_16x16x32_bf16 v[72:75], v[236:239], a[180:183], v[72:75]\n\t"
"v_fma_f32 v183, -v183, s66, v85\n\t"
"v_fma_f32 v84, v86, s52, v184\n\t"
"v_mfma_f32_16x16x32_bf16 v[72:75], a[220:223], a[184:187], v[72:75]\n\t"
"v_fma_f32 v85, v87, s52, v185\n\t"
"ds_read_b128 a[168:171], v244 offset:9216\n\t"
"ds_read_b128 a[172:175], v244 offset:9728\n\t"
"ds_read_b128 a[176:179], v244 offset:10240\n\t"
"v_mfma_f32_16x16x32_bf16 v[76:79], a[216:219], a[180:183], v[76:79]\n\t"
"v_fma_f32 v184, -v184, s66, v86\n\t"
"v_fma_f32 v86, v88, s52, v186\n\t"
"v_mfma_f32_16x16x32_bf16 v[76:79], v[236:239], a[184:187], v[76:79]\n\t"
"v_fma_f32 v185, -v185, s66, v87\n\t"
"v_fma_f32 v87, v89, s52, v187\n\t"
"v_mfma_f32_16x16x32_bf16 v[76:79], a[220:223], a[188:191], v[76:79]\n\t"
"v_fma_f32 v186, -v186, s66, v88\n\t"
"v_fma_f32 v187, -v187, s66, v89\n\t"
"v_cvt_pk_bf16_f32 v226, v232, v233\n\t"
"v_cvt_pk_bf16_f32 v227, v234, v235\n\t"
"v_mfma_f32_16x16x32_bf16 v[80:83], a[216:219], a[184:187], v[80:83]\n\t"
"v_fma_f32 v88, v90, s52, v188\n\t"
"v_fma_f32 v89, v91, s52, v189\n\t"
"v_mfma_f32_16x16x32_bf16 v[80:83], v[236:239], a[188:191], v[80:83]\n\t"
"ds_read_b128 a[180:183], v244 offset:10752\n\t"
"v_fma_f32 v188, -v188, s66, v90\n\t"
"v_mfma_f32_16x16x32_bf16 v[80:83], a[220:223], a[192:195], v[80:83]\n\t"
"v_fma_f32 v90, v92, s52, v190\n\t"
"v_fma_f32 v189, -v189, s66, v91\n\t"
"v_fma_f32 v91, v93, s52, v191\n\t"
"v_fma_f32 v190, -v190, s66, v92\n\t"
"v_mfma_f32_16x16x32_bf16 v[84:87], a[216:219], a[188:191], v[84:87]\n\t"
"v_fma_f32 v191, -v191, s66, v93\n\t"
"ds_read_b64_tr_b16 v[232:233], v246 offset:26624\n\t"
"v_mfma_f32_16x16x32_bf16 v[84:87], v[236:239], a[192:195], v[84:87]\n\t"
"ds_read_b64_tr_b16 v[234:235], v246 offset:29696\n\t"
"v_fma_f32 v92, v94, s52, v192\n\t"
"v_mfma_f32_16x16x32_bf16 v[84:87], a[220:223], a[196:199], v[84:87]\n\t"
"v_fma_f32 v93, v95, s52, v193\n\t"
"ds_read_b128 a[184:187], v244 offset:11264\n\t"
"v_fma_f32 v192, -v192, s66, v94\n\t"
"v_fma_f32 v94, v96, s52, v194\n\t"
"v_mfma_f32_16x16x32_bf16 v[88:91], a[216:219], a[192:195], v[88:91]\n\t"
"v_fma_f32 v193, -v193, s66, v95\n\t"
"v_fma_f32 v95, v97, s52, v195\n\t"
"v_mfma_f32_16x16x32_bf16 v[88:91], v[236:239], a[196:199], v[88:91]\n\t"
"v_fma_f32 v194, -v194, s66, v96\n\t"
"v_fma_f32 v195, -v195, s66, v97\n\t"
"v_mfma_f32_16x16x32_bf16 v[88:91], a[220:223], a[200:203], v[88:91]\n\t"
"ds_read_b128 a[188:191], v244 offset:11776\n\t"
"ds_read_b128 a[192:195], v244 offset:12288\n\t"
"v_mfma_f32_16x16x32_bf16 v[92:95], a[216:219], a[196:199], v[92:95]\n\t"
"ds_read_b128 a[196:199], v244 offset:12800\n\t"
"v_mfma_f32_16x16x32_bf16 v[92:95], v[236:239], a[200:203], v[92:95]\n\t"
"ds_read_b128 a[200:203], v244 offset:13312\n\t"
"v_mfma_f32_16x16x32_bf16 v[92:95], a[220:223], a[204:207], v[92:95]\n\t"
"s_mov_b32 s20, 270336\n\t"
"buffer_load_dwordx4 v[96:99], v252, s[8:11], s20 offen offset:0 sc1\n\t"
"s_waitcnt lgkmcnt(5)\n\t"
"v_mfma_f32_16x16x32_bf16 v[232:235], v[232:235], v[220:223], 0\n\t"
"buffer_load_dwordx4 v[236:239], v252, s[8:11], s20 offen offset:1024 sc1\n\t"
"s_waitcnt vmcnt(11)\n\t"
"v_mfma_f32_16x16x32_bf16 v[100:103], a[212:215], a[168:171], v[100:103]\n\t"
"ds_read_b128 a[204:207], v244 offset:13824\n\t"
"v_mfma_f32_16x16x32_bf16 v[100:103], a[208:211], a[172:175], v[100:103]\n\t"
"v_mfma_f32_16x16x32_bf16 v[100:103], a[224:227], a[176:179], v[100:103]\n\t"
"buffer_load_dwordx4 a[216:219], v252, s[8:11], s20 offen offset:2048 sc1\n\t"
"v_mfma_f32_16x16x32_bf16 v[104:107], a[212:215], a[172:175], v[104:107]\n\t"
"v_mfma_f32_16x16x32_bf16 v[104:107], a[208:211], a[176:179], v[104:107]\n\t"
"v_cvt_pk_bf16_f32 v232, v232, v233\n\t"
"v_cvt_pk_bf16_f32 v233, v234, v235\n\t"
"v_mfma_f32_16x16x32_bf16 v[104:107], a[224:227], a[180:183], v[104:107]\n\t"
"s_mov_b32 s20, 282624\n\t"
"buffer_load_dwordx4 a[220:223], v252, s[8:11], s20 offen offset:0 sc1\n\t"
"s_waitcnt lgkmcnt(0)\n\t"
"s_barrier\n\t"
"v_mfma_f32_16x16x32_bf16 v[108:111], a[212:215], a[176:179], v[108:111]\n\t"
"v_mfma_f32_16x16x32_bf16 v[108:111], a[208:211], a[180:183], v[108:111]\n\t"
"v_mfma_f32_16x16x32_bf16 v[108:111], a[224:227], a[184:187], v[108:111]\n\t"
"ds_write2st64_b64 v245, v[228:229], v[230:231] offset0:0 offset1:1\n\t"
"v_mfma_f32_16x16x32_bf16 v[112:115], a[212:215], a[180:183], v[112:115]\n\t"
"ds_read_b64_tr_b16 v[228:229], v246 offset:32768\n\t"
"v_mfma_f32_16x16x32_bf16 v[112:115], a[208:211], a[184:187], v[112:115]\n\t"
"ds_read_b64_tr_b16 v[230:231], v246 offset:35840\n\t"
"v_mfma_f32_16x16x32_bf16 v[112:115], a[224:227], a[188:191], v[112:115]\n\t"
"ds_write2st64_b64 v245, v[224:225], v[226:227] offset0:2 offset1:3\n\t"
"v_mfma_f32_16x16x32_bf16 v[116:119], a[212:215], a[184:187], v[116:119]\n\t"
"ds_read_b64_tr_b16 v[224:225], v246 offset:40448\n\t"
"v_mfma_f32_16x16x32_bf16 v[116:119], a[208:211], a[188:191], v[116:119]\n\t"
"ds_read_b64_tr_b16 v[226:227], v246 offset:43520\n\t"
"v_mfma_f32_16x16x32_bf16 v[116:119], a[224:227], a[192:195], v[116:119]\n\t"
"ds_write_b64 v245, v[232:233] offset:2048\n\t"
"v_mfma_f32_16x16x32_bf16 v[120:123], a[212:215], a[188:191], v[120:123]\n\t"
"ds_read_b64_tr_b16 v[232:233], v246 offset:46592\n\t"
"v_mfma_f32_16x16x32_bf16 v[120:123], a[208:211], a[192:195], v[120:123]\n\t"
"ds_read_b64_tr_b16 v[234:235], v246 offset:49664\n\t"
"v_mfma_f32_16x16x32_bf16 v[120:123], a[224:227], a[196:199], v[120:123]\n\t"
"s_waitcnt lgkmcnt(6)\n\t"
"v_mfma_f32_16x16x32_bf16 v[228:231], v[228:231], v[220:223], 0\n\t"
"v_mfma_f32_16x16x32_bf16 v[124:127], a[212:215], a[192:195], v[124:127]\n\t"
"v_mfma_f32_16x16x32_bf16 v[124:127], a[208:211], a[196:199], v[124:127]\n\t"
"v_mfma_f32_16x16x32_bf16 v[124:127], a[224:227], a[200:203], v[124:127]\n\t"
"s_waitcnt lgkmcnt(3)\n\t"
"v_mfma_f32_16x16x32_bf16 v[224:227], v[224:227], v[220:223], 0\n\t"
"v_mfma_f32_16x16x32_bf16 v[128:131], a[212:215], a[196:199], v[128:131]\n\t"
"s_nop 1\n\t"
"v_cvt_pk_bf16_f32 v228, v228, v229\n\t"
"v_cvt_pk_bf16_f32 v229, v230, v231\n\t"
"v_mfma_f32_16x16x32_bf16 v[128:131], a[208:211], a[200:203], v[128:131]\n\t"
"v_mfma_f32_16x16x32_bf16 v[128:131], a[224:227], a[204:207], v[128:131]\n\t"
"s_add_u32 m0, s24, 73088\n\t"
"s_add_u32 s28, s21, 2162688\n\t"
"buffer_load_dwordx4 v247, s[12:15], s28 offen offset:128 nt lds\n\t"
"s_add_u32 m0, s24, 80768\n\t"
"s_add_u32 s28, s21, 2359296\n\t"
"buffer_load_dwordx4 v247, s[12:15], s28 offen offset:128 nt lds\n\t"
"s_waitcnt lgkmcnt(0)\n\t"
"v_mfma_f32_16x16x32_bf16 v[232:235], v[232:235], v[220:223], 0\n\t"
"s_waitcnt vmcnt(12)\n\t"
"v_mfma_f32_16x16x32_bf16 v[132:135], a[228:231], a[168:171], v[132:135]\n\t"
"v_cvt_pk_bf16_f32 v230, v224, v225\n\t"
"v_cvt_pk_bf16_f32 v231, v226, v227\n\t"
"v_mfma_f32_16x16x32_bf16 v[132:135], a[232:235], a[172:175], v[132:135]\n\t"
"ds_read_b64_tr_b16 v[224:225], v246 offset:52736\n\t"
"v_mfma_f32_16x16x32_bf16 v[132:135], v[240:243], a[176:179], v[132:135]\n\t"
"s_add_u32 m0, s24, 27008\n\t"
"s_add_u32 s28, s21, 786432\n\t"
"buffer_load_dwordx4 v247, s[12:15], s28 offen offset:128 nt lds\n\t"
"s_add_u32 m0, s24, 33152\n\t"
"s_add_u32 s28, s21, 983040\n\t"
"buffer_load_dwordx4 v247, s[12:15], s28 offen offset:128 nt lds\n\t"
"v_mfma_f32_16x16x32_bf16 v[136:139], a[228:231], a[172:175], v[136:139]\n\t"
"ds_read_b64_tr_b16 v[226:227], v246 offset:55808\n\t"
"v_mfma_f32_16x16x32_bf16 v[136:139], a[232:235], a[176:179], v[136:139]\n\t"
"v_cvt_pk_bf16_f32 v232, v232, v233\n\t"
"v_cvt_pk_bf16_f32 v233, v234, v235\n\t"
"v_mfma_f32_16x16x32_bf16 v[136:139], v[240:243], a[180:183], v[136:139]\n\t"
"s_add_u32 m0, s24, 40832\n\t"
"s_add_u32 s28, s21, 1179648\n\t"
"buffer_load_dwordx4 v247, s[12:15], s28 offen offset:128 nt lds\n\t"
"v_mfma_f32_16x16x32_bf16 v[140:143], a[228:231], a[176:179], v[140:143]\n\t"
"v_mfma_f32_16x16x32_bf16 v[140:143], a[232:235], a[180:183], v[140:143]\n\t"
"v_mfma_f32_16x16x32_bf16 v[140:143], v[240:243], a[184:187], v[140:143]\n\t"
"s_add_u32 m0, s24, 46976\n\t"
"s_add_u32 s28, s21, 1376256\n\t"
"buffer_load_dwordx4 v247, s[12:15], s28 offen offset:128 nt lds\n\t"
"ds_write2st64_b64 v245, v[228:229], v[230:231] offset0:5 offset1:6\n\t"
"v_mfma_f32_16x16x32_bf16 v[144:147], a[228:231], a[180:183], v[144:147]\n\t"
"v_mfma_f32_16x16x32_bf16 v[144:147], a[232:235], a[184:187], v[144:147]\n\t"
"v_mfma_f32_16x16x32_bf16 v[144:147], v[240:243], a[188:191], v[144:147]\n\t"
"v_mfma_f32_16x16x32_bf16 v[148:151], a[228:231], a[184:187], v[148:151]\n\t"
"v_mfma_f32_16x16x32_bf16 v[148:151], a[232:235], a[188:191], v[148:151]\n\t"
"v_mfma_f32_16x16x32_bf16 v[148:151], v[240:243], a[192:195], v[148:151]\n\t"
"s_waitcnt lgkmcnt(1)\n\t"
"v_mfma_f32_16x16x32_bf16 v[224:227], v[224:227], v[220:223], 0\n\t"
"v_mfma_f32_16x16x32_bf16 v[152:155], a[228:231], a[188:191], v[152:155]\n\t"
"v_mfma_f32_16x16x32_bf16 v[152:155], a[232:235], a[192:195], v[152:155]\n\t"
"v_mfma_f32_16x16x32_bf16 v[152:155], v[240:243], a[196:199], v[152:155]\n\t"
"v_mfma_f32_16x16x32_bf16 v[156:159], a[228:231], a[192:195], v[156:159]\n\t"
"v_mfma_f32_16x16x32_bf16 v[156:159], a[232:235], a[196:199], v[156:159]\n\t"
"s_nop 2\n\t"
"v_cvt_pk_bf16_f32 v234, v224, v225\n\t"
"v_cvt_pk_bf16_f32 v235, v226, v227\n\t"
"v_mfma_f32_16x16x32_bf16 v[156:159], v[240:243], a[200:203], v[156:159]\n\t"
"ds_write2st64_b64 v245, v[232:233], v[234:235] offset0:7 offset1:8\n\t"
"v_mfma_f32_16x16x32_bf16 v[160:163], a[228:231], a[196:199], v[160:163]\n\t"
"v_mfma_f32_16x16x32_bf16 v[160:163], a[232:235], a[200:203], v[160:163]\n\t"
"v_mfma_f32_16x16x32_bf16 v[160:163], v[240:243], a[204:207], v[160:163]\n\t"
"s_waitcnt vmcnt(13)\n\t"
"v_mfma_f32_16x16x32_bf16 v[164:167], a[236:239], a[168:171], v[164:167]\n\t"
"v_mfma_f32_16x16x32_bf16 v[164:167], a[240:243], a[172:175], v[164:167]\n\t"
"v_mfma_f32_16x16x32_bf16 v[164:167], a[244:247], a[176:179], v[164:167]\n\t"
"s_waitcnt lgkmcnt(0)\n\t"
"s_barrier\n\t"
"v_mfma_f32_16x16x32_bf16 v[168:171], a[236:239], a[172:175], v[168:171]\n\t"
"ds_read_b128 a[168:171], v244 offset:0\n\t"
"v_mfma_f32_16x16x32_bf16 v[168:171], a[240:243], a[176:179], v[168:171]\n\t"
"ds_read_b128 a[172:175], v244 offset:512\n\t"
"v_mfma_f32_16x16x32_bf16 v[168:171], a[244:247], a[180:183], v[168:171]\n\t"
"v_mfma_f32_16x16x32_bf16 v[172:175], a[236:239], a[176:179], v[172:175]\n\t"
"ds_read_b128 a[176:179], v244 offset:1024\n\t"
"v_mfma_f32_16x16x32_bf16 v[172:175], a[240:243], a[180:183], v[172:175]\n\t"
"v_mfma_f32_16x16x32_bf16 v[172:175], a[244:247], a[184:187], v[172:175]\n\t"
"buffer_load_dwordx4 v[240:243], v252, s[8:11], s20 offen offset:1024 sc1\n\t"
"v_mfma_f32_16x16x32_bf16 v[176:179], a[236:239], a[180:183], v[176:179]\n\t"
"ds_read_b128 a[180:183], v244 offset:1536\n\t"
"v_mfma_f32_16x16x32_bf16 v[176:179], a[240:243], a[184:187], v[176:179]\n\t"
"v_mfma_f32_16x16x32_bf16 v[176:179], a[244:247], a[188:191], v[176:179]\n\t"
"v_mfma_f32_16x16x32_bf16 v[180:183], a[236:239], a[184:187], v[180:183]\n\t"
"ds_read_b128 a[184:187], v244 offset:2048\n\t"
"v_mfma_f32_16x16x32_bf16 v[180:183], a[240:243], a[188:191], v[180:183]\n\t"
"v_mfma_f32_16x16x32_bf16 v[180:183], a[244:247], a[192:195], v[180:183]\n\t"
"v_mfma_f32_16x16x32_bf16 v[184:187], a[236:239], a[188:191], v[184:187]\n\t"
"ds_read_b128 a[188:191], v244 offset:2560\n\t"
"v_mfma_f32_16x16x32_bf16 v[184:187], a[240:243], a[192:195], v[184:187]\n\t"
"v_mfma_f32_16x16x32_bf16 v[184:187], a[244:247], a[196:199], v[184:187]\n\t"
"v_mfma_f32_16x16x32_bf16 v[188:191], a[236:239], a[192:195], v[188:191]\n\t"
"ds_read_b128 a[192:195], v244 offset:3072\n\t"
"v_mfma_f32_16x16x32_bf16 v[188:191], a[240:243], a[196:199], v[188:191]\n\t"
"v_mfma_f32_16x16x32_bf16 v[188:191], a[244:247], a[200:203], v[188:191]\n\t"
"v_mfma_f32_16x16x32_bf16 v[192:195], a[236:239], a[196:199], v[192:195]\n\t"
"ds_read_b128 a[196:199], v244 offset:3584\n\t"
"v_mfma_f32_16x16x32_bf16 v[192:195], a[240:243], a[200:203], v[192:195]\n\t"
"ds_read_b128 a[200:203], v244 offset:4096\n\t"
"v_mfma_f32_16x16x32_bf16 v[192:195], a[244:247], a[204:207], v[192:195]\n\t"
"Lst22_%=:\n\t"
"s_waitcnt vmcnt(11) lgkmcnt(6)\n\t"
"v_mfma_f32_16x16x32_bf16 v[0:3], v[248:251], a[168:171], v[0:3]\n\t"
"ds_read_b128 a[204:207], v244 offset:4608\n\t"
"v_mfma_f32_16x16x32_bf16 v[0:3], a[248:251], a[172:175], v[0:3]\n\t"
"v_xor_b32 v246, 0xc0, v246\n\t"
"v_mfma_f32_16x16x32_bf16 v[0:3], a[252:255], a[176:179], v[0:3]\n\t"
"s_waitcnt lgkmcnt(6)\n\t"
"v_mfma_f32_16x16x32_bf16 v[4:7], v[248:251], a[172:175], v[4:7]\n\t"
"v_mfma_f32_16x16x32_bf16 v[4:7], a[248:251], a[176:179], v[4:7]\n\t"
"v_mfma_f32_16x16x32_bf16 v[4:7], a[252:255], a[180:183], v[4:7]\n\t"
"s_waitcnt lgkmcnt(5)\n\t"
"v_mfma_f32_16x16x32_bf16 v[8:11], v[248:251], a[176:179], v[8:11]\n\t"
"v_mfma_f32_16x16x32_bf16 v[8:11], a[248:251], a[180:183], v[8:11]\n\t"
"v_mfma_f32_16x16x32_bf16 v[8:11], a[252:255], a[184:187], v[8:11]\n\t"
"s_waitcnt lgkmcnt(4)\n\t"
"v_mfma_f32_16x16x32_bf16 v[12:15], v[248:251], a[180:183], v[12:15]\n\t"
"v_mfma_f32_16x16x32_bf16 v[12:15], a[248:251], a[184:187], v[12:15]\n\t"
"v_mfma_f32_16x16x32_bf16 v[12:15], a[252:255], a[188:191], v[12:15]\n\t"
"buffer_load_dwordx4 a[208:211], v252, s[8:11], s20 offen offset:2048 sc1\n\t"
"s_waitcnt lgkmcnt(3)\n\t"
"v_mfma_f32_16x16x32_bf16 v[16:19], v[248:251], a[184:187], v[16:19]\n\t"
"v_mfma_f32_16x16x32_bf16 v[16:19], a[248:251], a[188:191], v[16:19]\n\t"
"v_mfma_f32_16x16x32_bf16 v[16:19], a[252:255], a[192:195], v[16:19]\n\t"
"ds_read_b64_tr_b16 v[228:229], v246 offset:512\n\t"
"ds_read_b64_tr_b16 v[230:231], v246 offset:3584\n\t"
"s_waitcnt lgkmcnt(4)\n\t"
"v_mfma_f32_16x16x32_bf16 v[20:23], v[248:251], a[188:191], v[20:23]\n\t"
"v_mfma_f32_16x16x32_bf16 v[20:23], a[248:251], a[192:195], v[20:23]\n\t"
"v_mfma_f32_16x16x32_bf16 v[20:23], a[252:255], a[196:199], v[20:23]\n\t"
"s_mov_b32 s20, 294912\n\t"
"buffer_load_dwordx4 a[212:215], v252, s[8:11], s20 offen offset:0 sc1\n\t"
"s_waitcnt lgkmcnt(3)\n\t"
"v_mfma_f32_16x16x32_bf16 v[24:27], v[248:251], a[192:195], v[24:27]\n\t"
"v_mfma_f32_16x16x32_bf16 v[24:27], a[248:251], a[196:199], v[24:27]\n\t"
"v_mfma_f32_16x16x32_bf16 v[24:27], a[252:255], a[200:203], v[24:27]\n\t"
"buffer_load_dwordx4 a[224:227], v252, s[8:11], s20 offen offset:1024 sc1\n\t"
"s_waitcnt lgkmcnt(2)\n\t"
"v_mfma_f32_16x16x32_bf16 v[28:31], v[248:251], a[196:199], v[28:31]\n\t"
"ds_read_b64_tr_b16 v[224:225], v246 offset:6656\n\t"
"v_mfma_f32_16x16x32_bf16 v[28:31], a[248:251], a[200:203], v[28:31]\n\t"
"ds_read_b64_tr_b16 v[226:227], v246 offset:9728\n\t"
"v_mfma_f32_16x16x32_bf16 v[28:31], a[252:255], a[204:207], v[28:31]\n\t"
"s_waitcnt vmcnt(11)\n\t"
"v_mfma_f32_16x16x32_bf16 v[32:35], v[96:99], a[168:171], v[32:35]\n\t"
"v_mfma_f32_16x16x32_bf16 v[32:35], v[236:239], a[172:175], v[32:35]\n\t"
"v_mfma_f32_16x16x32_bf16 v[32:35], a[216:219], a[176:179], v[32:35]\n\t"
"buffer_load_dwordx4 a[228:231], v252, s[8:11], s20 offen offset:2048 sc1\n\t"
"v_mfma_f32_16x16x32_bf16 v[36:39], v[96:99], a[172:175], v[36:39]\n\t"
"v_mfma_f32_16x16x32_bf16 v[36:39], v[236:239], a[176:179], v[36:39]\n\t"
"v_mfma_f32_16x16x32_bf16 v[36:39], a[216:219], a[180:183], v[36:39]\n\t"
"s_mov_b32 s20, 307200\n\t"
"buffer_load_dwordx4 a[232:235], v252, s[8:11], s20 offen offset:0 sc1\n\t"
"s_waitcnt lgkmcnt(2)\n\t"
"v_mfma_f32_16x16x32_bf16 v[228:231], v[228:231], v[220:223], 0\n\t"
"v_mfma_f32_16x16x32_bf16 v[40:43], v[96:99], a[176:179], v[40:43]\n\t"
"ds_read_b64_tr_b16 v[232:233], v246 offset:12800\n\t"
"v_mfma_f32_16x16x32_bf16 v[40:43], v[236:239], a[180:183], v[40:43]\n\t"
"ds_read_b64_tr_b16 v[234:235], v246 offset:15872\n\t"
"v_mfma_f32_16x16x32_bf16 v[40:43], a[216:219], a[184:187], v[40:43]\n\t"
"buffer_load_dwordx4 a[236:239], v252, s[8:11], s20 offen offset:1024 sc1\n\t"
"v_mfma_f32_16x16x32_bf16 v[44:47], v[96:99], a[180:183], v[44:47]\n\t"
"v_mfma_f32_16x16x32_bf16 v[44:47], v[236:239], a[184:187], v[44:47]\n\t"
"v_cvt_pk_bf16_f32 v228, v228, v229\n\t"
"v_cvt_pk_bf16_f32 v229, v230, v231\n\t"
"v_mfma_f32_16x16x32_bf16 v[44:47], a[216:219], a[188:191], v[44:47]\n\t"
"s_waitcnt lgkmcnt(2)\n\t"
"v_mfma_f32_16x16x32_bf16 v[224:227], v[224:227], v[220:223], 0\n\t"
"buffer_load_dwordx4 a[240:243], v252, s[8:11], s20 offen offset:2048 sc1\n\t"
"v_mfma_f32_16x16x32_bf16 v[48:51], v[96:99], a[184:187], v[48:51]\n\t"
"v_mfma_f32_16x16x32_bf16 v[48:51], v[236:239], a[188:191], v[48:51]\n\t"
"v_mfma_f32_16x16x32_bf16 v[48:51], a[216:219], a[192:195], v[48:51]\n\t"
"s_mov_b32 s20, 319488\n\t"
"buffer_load_dwordx4 a[244:247], v252, s[8:11], s20 offen offset:0 sc1\n\t"
"v_mfma_f32_16x16x32_bf16 v[52:55], v[96:99], a[188:191], v[52:55]\n\t"
"v_mfma_f32_16x16x32_bf16 v[52:55], v[236:239], a[192:195], v[52:55]\n\t"
"v_cvt_pk_bf16_f32 v230, v224, v225\n\t"
"v_cvt_pk_bf16_f32 v231, v226, v227\n\t"
"v_mfma_f32_16x16x32_bf16 v[52:55], a[216:219], a[196:199], v[52:55]\n\t"
"global_load_dwordx4 v[248:251], v252, s[48:49] offset:0\n\t"
"v_mfma_f32_16x16x32_bf16 v[56:59], v[96:99], a[192:195], v[56:59]\n\t"
"ds_read_b64_tr_b16 v[224:225], v246 offset:20480\n\t"
"v_mfma_f32_16x16x32_bf16 v[56:59], v[236:239], a[196:199], v[56:59]\n\t"
"ds_read_b64_tr_b16 v[226:227], v246 offset:23552\n\t"
"v_mfma_f32_16x16x32_bf16 v[56:59], a[216:219], a[200:203], v[56:59]\n\t"
"buffer_load_dwordx4 a[248:251], v252, s[8:11], s20 offen offset:1024 sc1\n\t"
"s_waitcnt lgkmcnt(2)\n\t"
"v_mfma_f32_16x16x32_bf16 v[232:235], v[232:235], v[220:223], 0\n\t"
"buffer_load_dwordx4 a[252:255], v252, s[8:11], s20 offen offset:2048 sc1\n\t"
"v_mfma_f32_16x16x32_bf16 v[60:63], v[96:99], a[196:199], v[60:63]\n\t"
"v_mfma_f32_16x16x32_bf16 v[60:63], v[236:239], a[200:203], v[60:63]\n\t"
"v_mfma_f32_16x16x32_bf16 v[60:63], a[216:219], a[204:207], v[60:63]\n\t"
"s_waitcnt vmcnt(10)\n\t"
"v_mfma_f32_16x16x32_bf16 v[64:67], a[220:223], a[168:171], v[64:67]\n\t"
"ds_read_b128 a[168:171], v244 offset:9216\n\t"
"v_mfma_f32_16x16x32_bf16 v[64:67], v[240:243], a[172:175], v[64:67]\n\t"
"v_cvt_pk_bf16_f32 v232, v232, v233\n\t"
"v_cvt_pk_bf16_f32 v233, v234, v235\n\t"
"v_mfma_f32_16x16x32_bf16 v[64:67], a[208:211], a[176:179], v[64:67]\n\t"
"v_mfma_f32_16x16x32_bf16 v[68:71], a[220:223], a[172:175], v[68:71]\n\t"
"ds_read_b128 a[172:175], v244 offset:9728\n\t"
"v_mfma_f32_16x16x32_bf16 v[68:71], v[240:243], a[176:179], v[68:71]\n\t"
"v_mfma_f32_16x16x32_bf16 v[68:71], a[208:211], a[180:183], v[68:71]\n\t"
"s_mov_b32 s20, 331776\n\t"
"buffer_load_dwordx4 v[236:239], v252, s[8:11], s20 offen offset:0 sc1\n\t"
"s_waitcnt lgkmcnt(2)\n\t"
"v_mfma_f32_16x16x32_bf16 v[224:227], v[224:227], v[220:223], 0\n\t"
"buffer_load_dwordx4 v[96:99], v252, s[8:11], s20 offen offset:1024 sc1\n\t"
"v_mfma_f32_16x16x32_bf16 v[72:75], a[220:223], a[176:179], v[72:75]\n\t"
"ds_read_b128 a[176:179], v244 offset:10240\n\t"
"v_mfma_f32_16x16x32_bf16 v[72:75], v[240:243], a[180:183], v[72:75]\n\t"
"v_mfma_f32_16x16x32_bf16 v[72:75], a[208:211], a[184:187], v[72:75]\n\t"
"buffer_load_dwordx4 a[216:219], v252, s[8:11], s20 offen offset:2048 sc1\n\t"
"v_mfma_f32_16x16x32_bf16 v[76:79], a[220:223], a[180:183], v[76:79]\n\t"
"ds_read_b128 a[180:183], v244 offset:10752\n\t"
"v_mfma_f32_16x16x32_bf16 v[76:79], v[240:243], a[184:187], v[76:79]\n\t"
"v_cvt_pk_bf16_f32 v234, v224, v225\n\t"
"v_cvt_pk_bf16_f32 v235, v226, v227\n\t"
"v_mfma_f32_16x16x32_bf16 v[76:79], a[208:211], a[188:191], v[76:79]\n\t"
"ds_read_b64_tr_b16 v[224:225], v246 offset:26624\n\t"
"v_mfma_f32_16x16x32_bf16 v[80:83], a[220:223], a[184:187], v[80:83]\n\t"
"ds_read_b64_tr_b16 v[226:227], v246 offset:29696\n\t"
"v_mfma_f32_16x16x32_bf16 v[80:83], v[240:243], a[188:191], v[80:83]\n\t"
"ds_read_b128 a[184:187], v244 offset:11264\n\t"
"v_mfma_f32_16x16x32_bf16 v[80:83], a[208:211], a[192:195], v[80:83]\n\t"
"v_mfma_f32_16x16x32_bf16 v[84:87], a[220:223], a[188:191], v[84:87]\n\t"
"ds_read_b128 a[188:191], v244 offset:11776\n\t"
"v_mfma_f32_16x16x32_bf16 v[84:87], v[240:243], a[192:195], v[84:87]\n\t"
"v_mfma_f32_16x16x32_bf16 v[84:87], a[208:211], a[196:199], v[84:87]\n\t"
"v_mfma_f32_16x16x32_bf16 v[88:91], a[220:223], a[192:195], v[88:91]\n\t"
"ds_read_b128 a[192:195], v244 offset:12288\n\t"
"v_mfma_f32_16x16x32_bf16 v[88:91], v[240:243], a[196:199], v[88:91]\n\t"
"v_mfma_f32_16x16x32_bf16 v[88:91], a[208:211], a[200:203], v[88:91]\n\t"
"v_mfma_f32_16x16x32_bf16 v[92:95], a[220:223], a[196:199], v[92:95]\n\t"
"ds_read_b128 a[196:199], v244 offset:12800\n\t"
"v_mfma_f32_16x16x32_bf16 v[92:95], v[240:243], a[200:203], v[92:95]\n\t"
"ds_read_b128 a[200:203], v244 offset:13312\n\t"
"v_mfma_f32_16x16x32_bf16 v[92:95], a[208:211], a[204:207], v[92:95]\n\t"
"s_mov_b32 s20, 344064\n\t"
"buffer_load_dwordx4 a[220:223], v252, s[8:11], s20 offen offset:0 sc1\n\t"
"s_waitcnt lgkmcnt(5)\n\t"
"v_mfma_f32_16x16x32_bf16 v[224:227], v[224:227], v[220:223], 0\n\t"
"ds_read_b128 a[204:207], v244 offset:13824\n\t"
"s_waitcnt vmcnt(11)\n\t"
"v_mfma_f32_16x16x32_bf16 v[100:103], a[212:215], a[168:171], v[100:103]\n\t"
"v_mfma_f32_16x16x32_bf16 v[100:103], a[224:227], a[172:175], v[100:103]\n\t"
"v_mfma_f32_16x16x32_bf16 v[100:103], a[228:231], a[176:179], v[100:103]\n\t"
"buffer_load_dwordx4 a[208:211], v252, s[8:11], s20 offen offset:1024 sc1\n\t"
"v_mfma_f32_16x16x32_bf16 v[104:107], a[212:215], a[172:175], v[104:107]\n\t"
"v_mfma_f32_16x16x32_bf16 v[104:107], a[224:227], a[176:179], v[104:107]\n\t"
"v_cvt_pk_bf16_f32 v224, v224, v225\n\t"
"v_cvt_pk_bf16_f32 v225, v226, v227\n\t"
"v_mfma_f32_16x16x32_bf16 v[104:107], a[228:231], a[180:183], v[104:107]\n\t"
"s_waitcnt lgkmcnt(0)\n\t"
"s_barrier\n\t"
"v_mfma_f32_16x16x32_bf16 v[108:111], a[212:215], a[176:179], v[108:111]\n\t"
"v_mfma_f32_16x16x32_bf16 v[108:111], a[224:227], a[180:183], v[108:111]\n\t"
"v_mfma_f32_16x16x32_bf16 v[108:111], a[228:231], a[184:187], v[108:111]\n\t"
"ds_write2st64_b64 v245, v[228:229], v[230:231] offset0:0 offset1:1\n\t"
"v_mfma_f32_16x16x32_bf16 v[112:115], a[212:215], a[180:183], v[112:115]\n\t"
"ds_read_b64_tr_b16 v[228:229], v246 offset:32768\n\t"
"v_mfma_f32_16x16x32_bf16 v[112:115], a[224:227], a[184:187], v[112:115]\n\t"
"ds_read_b64_tr_b16 v[230:231], v246 offset:35840\n\t"
"v_mfma_f32_16x16x32_bf16 v[112:115], a[228:231], a[188:191], v[112:115]\n\t"
"ds_write2st64_b64 v245, v[232:233], v[234:235] offset0:2 offset1:3\n\t"
"s_add_u32 m0, s24, 53120\n\t"
"s_add_u32 s28, s21, 1572864\n\t"
"buffer_load_dwordx4 v247, s[12:15], s28 offen offset:128 nt lds\n\t"
"v_mfma_f32_16x16x32_bf16 v[116:119], a[212:215], a[184:187], v[116:119]\n\t"
"ds_read_b64_tr_b16 v[232:233], v246 offset:40448\n\t"
"v_mfma_f32_16x16x32_bf16 v[116:119], a[224:227], a[188:191], v[116:119]\n\t"
"ds_read_b64_tr_b16 v[234:235], v246 offset:43520\n\t"
"v_mfma_f32_16x16x32_bf16 v[116:119], a[228:231], a[192:195], v[116:119]\n\t"
"ds_write_b64 v245, v[224:225] offset:2048\n\t"
"s_add_u32 m0, s24, 86912\n\t"
"s_add_u32 s28, s21, 2555904\n\t"
"buffer_load_dwordx4 v247, s[12:15], s28 offen offset:128 nt lds\n\t"
"s_add_u32 m0, s24, 93056\n\t"
"s_add_u32 s28, s21, 2752512\n\t"
"buffer_load_dwordx4 v247, s[12:15], s28 offen offset:128 nt lds\n\t"
"v_mfma_f32_16x16x32_bf16 v[120:123], a[212:215], a[188:191], v[120:123]\n\t"
"ds_read_b64_tr_b16 v[224:225], v246 offset:46592\n\t"
"v_mfma_f32_16x16x32_bf16 v[120:123], a[224:227], a[192:195], v[120:123]\n\t"
"ds_read_b64_tr_b16 v[226:227], v246 offset:49664\n\t"
"v_mfma_f32_16x16x32_bf16 v[120:123], a[228:231], a[196:199], v[120:123]\n\t"
"s_add_u32 m0, s24, 100736\n\t"
"s_add_u32 s28, s21, 2949120\n\t"
"buffer_load_dwordx4 v247, s[12:15], s28 offen offset:128 nt lds\n\t"
"s_waitcnt lgkmcnt(6)\n\t"
"v_mfma_f32_16x16x32_bf16 v[228:231], v[228:231], v[220:223], 0\n\t"
"s_add_u32 m0, s24, 106880\n\t"
"s_add_u32 s28, s21, 3145728\n\t"
"buffer_load_dwordx4 v247, s[12:15], s28 offen offset:128 nt lds\n\t"
"v_mfma_f32_16x16x32_bf16 v[124:127], a[212:215], a[192:195], v[124:127]\n\t"
"v_mfma_f32_16x16x32_bf16 v[124:127], a[224:227], a[196:199], v[124:127]\n\t"
"v_mfma_f32_16x16x32_bf16 v[124:127], a[228:231], a[200:203], v[124:127]\n\t"
"s_add_u32 m0, s24, 113024\n\t"
"s_add_u32 s28, s21, 3342336\n\t"
"buffer_load_dwordx4 v247, s[12:15], s28 offen offset:128 nt lds\n\t"
"s_waitcnt lgkmcnt(3)\n\t"
"v_mfma_f32_16x16x32_bf16 v[232:235], v[232:235], v[220:223], 0\n\t"
"s_add_u32 m0, s25, 896\n\t"
"s_nop 0\n\t"
"buffer_load_dwordx4 v254, s[12:15], 0 offen offset:128 nt lds\n\t"
"v_mfma_f32_16x16x32_bf16 v[128:131], a[212:215], a[196:199], v[128:131]\n\t"
"v_cvt_pk_bf16_f32 v228, v228, v229\n\t"
"v_cvt_pk_bf16_f32 v229, v230, v231\n\t"
"v_mfma_f32_16x16x32_bf16 v[128:131], a[224:227], a[200:203], v[128:131]\n\t"
"ds_read_b128 v[240:243], v252 offset:18432\n\t"
"v_mfma_f32_16x16x32_bf16 v[128:131], a[228:231], a[204:207], v[128:131]\n\t"
"s_add_u32 m0, s26, 896\n\t"
"s_mov_b64 exec, s[36:37]\n\t"
"buffer_load_dwordx4 v254, s[12:15], s27 offen offset:128 nt lds\n\t"
"s_mov_b64 exec, -1\n\t"
"s_waitcnt lgkmcnt(1)\n\t"
"v_mfma_f32_16x16x32_bf16 v[224:227], v[224:227], v[220:223], 0\n\t"
"s_waitcnt vmcnt(17)\n\t"
"v_mfma_f32_16x16x32_bf16 v[132:135], a[232:235], a[168:171], v[132:135]\n\t"
"v_cvt_pk_bf16_f32 v230, v232, v233\n\t"
"v_cvt_pk_bf16_f32 v231, v234, v235\n\t"
"v_mfma_f32_16x16x32_bf16 v[132:135], a[236:239], a[172:175], v[132:135]\n\t"
"ds_read_b64_tr_b16 v[232:233], v246 offset:52736\n\t"
"v_mfma_f32_16x16x32_bf16 v[132:135], a[240:243], a[176:179], v[132:135]\n\t"
"ds_write2st64_b64 v245, v[228:229], v[230:231] offset0:5 offset1:6\n\t"
"v_mfma_f32_16x16x32_bf16 v[136:139], a[232:235], a[172:175], v[136:139]\n\t"
"ds_read_b64_tr_b16 v[234:235], v246 offset:55808\n\t"
"v_mfma_f32_16x16x32_bf16 v[136:139], a[236:239], a[176:179], v[136:139]\n\t"
"v_cvt_pk_bf16_f32 v224, v224, v225\n\t"
"v_mfma_f32_16x16x32_bf16 v[136:139], a[240:243], a[180:183], v[136:139]\n\t"
"v_cvt_pk_bf16_f32 v225, v226, v227\n\t"
"v_mfma_f32_16x16x32_bf16 v[140:143], a[232:235], a[176:179], v[140:143]\n\t"
"v_mfma_f32_16x16x32_bf16 v[140:143], a[236:239], a[180:183], v[140:143]\n\t"
"v_mfma_f32_16x16x32_bf16 v[140:143], a[240:243], a[184:187], v[140:143]\n\t"
"s_add_u32 s29, s23, 787584\n\t"
"s_waitcnt vmcnt(15) lgkmcnt(3)\n\t"
"buffer_store_dwordx4 v[240:243], v248, s[16:19], s29 offen offset:0 nt\n\t"
"v_mfma_f32_16x16x32_bf16 v[144:147], a[232:235], a[180:183], v[144:147]\n\t"
"v_mfma_f32_16x16x32_bf16 v[144:147], a[236:239], a[184:187], v[144:147]\n\t"
"v_mfma_f32_16x16x32_bf16 v[144:147], a[240:243], a[188:191], v[144:147]\n\t"
"v_mfma_f32_16x16x32_bf16 v[148:151], a[232:235], a[184:187], v[148:151]\n\t"
"v_mfma_f32_16x16x32_bf16 v[148:151], a[236:239], a[188:191], v[148:151]\n\t"
"v_mfma_f32_16x16x32_bf16 v[148:151], a[240:243], a[192:195], v[148:151]\n\t"
"s_waitcnt lgkmcnt(0)\n\t"
"v_mfma_f32_16x16x32_bf16 v[232:235], v[232:235], v[220:223], 0\n\t"
"v_mfma_f32_16x16x32_bf16 v[152:155], a[232:235], a[188:191], v[152:155]\n\t"
"v_mfma_f32_16x16x32_bf16 v[152:155], a[236:239], a[192:195], v[152:155]\n\t"
"v_mfma_f32_16x16x32_bf16 v[152:155], a[240:243], a[196:199], v[152:155]\n\t"
"ds_read_b128 v[240:243], v252 offset:19456\n\t"
"v_mfma_f32_16x16x32_bf16 v[156:159], a[232:235], a[192:195], v[156:159]\n\t"
"v_mfma_f32_16x16x32_bf16 v[156:159], a[236:239], a[196:199], v[156:159]\n\t"
"s_nop 1\n\t"
"v_cvt_pk_bf16_f32 v226, v232, v233\n\t"
"v_cvt_pk_bf16_f32 v227, v234, v235\n\t"
"v_mfma_f32_16x16x32_bf16 v[156:159], a[240:243], a[200:203], v[156:159]\n\t"
"ds_write2st64_b64 v245, v[224:225], v[226:227] offset0:7 offset1:8\n\t"
"v_mfma_f32_16x16x32_bf16 v[160:163], a[232:235], a[196:199], v[160:163]\n\t"
"v_mfma_f32_16x16x32_bf16 v[160:163], a[236:239], a[200:203], v[160:163]\n\t"
"v_mfma_f32_16x16x32_bf16 v[160:163], a[240:243], a[204:207], v[160:163]\n\t"
"s_waitcnt vmcnt(14)\n\t"
"v_mfma_f32_16x16x32_bf16 v[164:167], a[244:247], a[168:171], v[164:167]\n\t"
"v_mfma_f32_16x16x32_bf16 v[164:167], a[248:251], a[172:175], v[164:167]\n\t"
"v_mfma_f32_16x16x32_bf16 v[164:167], a[252:255], a[176:179], v[164:167]\n\t"
"s_waitcnt lgkmcnt(0)\n\t"
"s_barrier\n\t"
"v_mfma_f32_16x16x32_bf16 v[168:171], a[244:247], a[172:175], v[168:171]\n\t"
"ds_read_b128 a[168:171], v244 offset:0\n\t"
"v_mfma_f32_16x16x32_bf16 v[168:171], a[248:251], a[176:179], v[168:171]\n\t"
"ds_read_b128 a[172:175], v244 offset:512\n\t"
"v_mfma_f32_16x16x32_bf16 v[168:171], a[252:255], a[180:183], v[168:171]\n\t"
"v_mfma_f32_16x16x32_bf16 v[172:175], a[244:247], a[176:179], v[172:175]\n\t"
"ds_read_b128 a[176:179], v244 offset:1024\n\t"
"v_mfma_f32_16x16x32_bf16 v[172:175], a[248:251], a[180:183], v[172:175]\n\t"
"v_mfma_f32_16x16x32_bf16 v[172:175], a[252:255], a[184:187], v[172:175]\n\t"
"s_add_u32 s29, s23, 787584\n\t"
"buffer_store_dwordx4 v[240:243], v248, s[16:19], s29 offen offset:128 nt\n\t"
"v_mfma_f32_16x16x32_bf16 v[176:179], a[244:247], a[180:183], v[176:179]\n\t"
"ds_read_b128 a[180:183], v244 offset:1536\n\t"
"v_mfma_f32_16x16x32_bf16 v[176:179], a[248:251], a[184:187], v[176:179]\n\t"
"v_mfma_f32_16x16x32_bf16 v[176:179], a[252:255], a[188:191], v[176:179]\n\t"
"buffer_load_dwordx4 a[212:215], v252, s[8:11], s20 offen offset:2048 sc1\n\t"
"v_mfma_f32_16x16x32_bf16 v[180:183], a[244:247], a[184:187], v[180:183]\n\t"
"ds_read_b128 a[184:187], v244 offset:2048\n\t"
"v_mfma_f32_16x16x32_bf16 v[180:183], a[248:251], a[188:191], v[180:183]\n\t"
"v_mfma_f32_16x16x32_bf16 v[180:183], a[252:255], a[192:195], v[180:183]\n\t"
"s_mov_b32 s20, 356352\n\t"
"buffer_load_dwordx4 a[224:227], v252, s[8:11], s20 offen offset:0 sc1\n\t"
"v_mfma_f32_16x16x32_bf16 v[184:187], a[244:247], a[188:191], v[184:187]\n\t"
"ds_read_b128 v[240:243], v252 offset:20480\n\t"
"v_mfma_f32_16x16x32_bf16 v[184:187], a[248:251], a[192:195], v[184:187]\n\t"
"ds_read_b128 a[188:191], v244 offset:2560\n\t"
"v_mfma_f32_16x16x32_bf16 v[184:187], a[252:255], a[196:199], v[184:187]\n\t"
"buffer_load_dwordx4 a[228:231], v252, s[8:11], s20 offen offset:1024 sc1\n\t"
"v_mfma_f32_16x16x32_bf16 v[188:191], a[244:247], a[192:195], v[188:191]\n\t"
"ds_read_b128 a[192:195], v244 offset:3072\n\t"
"v_mfma_f32_16x16x32_bf16 v[188:191], a[248:251], a[196:199], v[188:191]\n\t"
"v_mfma_f32_16x16x32_bf16 v[188:191], a[252:255], a[200:203], v[188:191]\n\t"
"buffer_load_dwordx4 a[232:235], v252, s[8:11], s20 offen offset:2048 sc1\n\t"
"v_mfma_f32_16x16x32_bf16 v[192:195], a[244:247], a[196:199], v[192:195]\n\t"
"ds_read_b128 a[196:199], v244 offset:3584\n\t"
"v_mfma_f32_16x16x32_bf16 v[192:195], a[248:251], a[200:203], v[192:195]\n\t"
"ds_read_b128 a[200:203], v244 offset:4096\n\t"
"v_mfma_f32_16x16x32_bf16 v[192:195], a[252:255], a[204:207], v[192:195]\n\t"
"Lst23_%=:\n\t"
"s_waitcnt vmcnt(16) lgkmcnt(7)\n\t"
"v_mfma_f32_16x16x32_bf16 v[196:199], v[236:239], a[168:171], v[196:199]\n\t"
"ds_read_b128 a[204:207], v244 offset:4608\n\t"
"v_mfma_f32_16x16x32_bf16 v[196:199], v[96:99], a[172:175], v[196:199]\n\t"
"v_mfma_f32_16x16x32_bf16 v[196:199], a[216:219], a[176:179], v[196:199]\n\t"
"s_mov_b32 s20, 368640\n\t"
"buffer_load_dwordx4 a[236:239], v252, s[8:11], s20 offen offset:0 sc1\n\t"
"s_waitcnt lgkmcnt(7)\n\t"
"v_mfma_f32_16x16x32_bf16 v[204:207], v[236:239], a[172:175], v[204:207]\n\t"
"v_mfma_f32_16x16x32_bf16 v[204:207], v[96:99], a[176:179], v[204:207]\n\t"
"v_mfma_f32_16x16x32_bf16 v[204:207], a[216:219], a[180:183], v[204:207]\n\t"
"s_add_u32 s29, s23, 787584\n\t"
"s_waitcnt lgkmcnt(5)\n\t"
"buffer_store_dwordx4 v[240:243], v248, s[16:19], s29 offen offset:256 nt\n\t"
"v_mfma_f32_16x16x32_bf16 v[212:215], v[236:239], a[176:179], v[212:215]\n\t"
"v_mfma_f32_16x16x32_bf16 v[212:215], v[96:99], a[180:183], v[212:215]\n\t"
"v_mfma_f32_16x16x32_bf16 v[212:215], a[216:219], a[184:187], v[212:215]\n\t"
"buffer_load_dwordx4 a[240:243], v252, s[8:11], s20 offen offset:1024 sc1\n\t"
"s_waitcnt lgkmcnt(4)\n\t"
"v_mfma_f32_16x16x32_bf16 a[0:3], v[236:239], a[180:183], a[0:3]\n\t"
"v_mfma_f32_16x16x32_bf16 a[0:3], v[96:99], a[184:187], a[0:3]\n\t"
"v_mfma_f32_16x16x32_bf16 a[0:3], a[216:219], a[188:191], a[0:3]\n\t"
"s_waitcnt lgkmcnt(3)\n\t"
"v_mfma_f32_16x16x32_bf16 a[8:11], v[236:239], a[184:187], a[8:11]\n\t"
"ds_read_b128 v[240:243], v252 offset:30720\n\t"
"v_mfma_f32_16x16x32_bf16 a[8:11], v[96:99], a[188:191], a[8:11]\n\t"
"v_mfma_f32_16x16x32_bf16 a[8:11], a[216:219], a[192:195], a[8:11]\n\t"
"buffer_load_dwordx4 a[244:247], v252, s[8:11], s20 offen offset:2048 sc1\n\t"
"s_waitcnt lgkmcnt(3)\n\t"
"v_mfma_f32_16x16x32_bf16 a[16:19], v[236:239], a[188:191], a[16:19]\n\t"
"v_mfma_f32_16x16x32_bf16 a[16:19], v[96:99], a[192:195], a[16:19]\n\t"
"v_mfma_f32_16x16x32_bf16 a[16:19], a[216:219], a[196:199], a[16:19]\n\t"
"s_mov_b32 s20, 380928\n\t"
"buffer_load_dwordx4 a[248:251], v252, s[8:11], s20 offen offset:0 sc1\n\t"
"s_waitcnt lgkmcnt(2)\n\t"
"v_mfma_f32_16x16x32_bf16 a[24:27], v[236:239], a[192:195], a[24:27]\n\t"
"v_mfma_f32_16x16x32_bf16 a[24:27], v[96:99], a[196:199], a[24:27]\n\t"
"v_mfma_f32_16x16x32_bf16 a[24:27], a[216:219], a[200:203], a[24:27]\n\t"
"buffer_load_dwordx4 a[252:255], v252, s[8:11], s20 offen offset:1024 sc1\n\t"
"s_waitcnt lgkmcnt(1)\n\t"
"v_mfma_f32_16x16x32_bf16 a[32:35], v[236:239], a[196:199], a[32:35]\n\t"
"v_mfma_f32_16x16x32_bf16 a[32:35], v[96:99], a[200:203], a[32:35]\n\t"
"v_mfma_f32_16x16x32_bf16 a[32:35], a[216:219], a[204:207], a[32:35]\n\t"
"s_waitcnt vmcnt(9)\n\t"
"v_mfma_f32_16x16x32_bf16 a[40:43], a[220:223], a[168:171], a[40:43]\n\t"
"v_mfma_f32_16x16x32_bf16 a[40:43], a[208:211], a[172:175], a[40:43]\n\t"
"v_mfma_f32_16x16x32_bf16 a[40:43], a[212:215], a[176:179], a[40:43]\n\t"
"buffer_load_dwordx4 v[96:99], v252, s[8:11], s20 offen offset:2048 sc1\n\t"
"v_mfma_f32_16x16x32_bf16 a[48:51], a[220:223], a[172:175], a[48:51]\n\t"
"ds_read_b64_tr_b16 v[236:237], v246 offset:1024\n\t"
"v_mfma_f32_16x16x32_bf16 a[48:51], a[208:211], a[176:179], a[48:51]\n\t"
"ds_read_b64_tr_b16 v[238:239], v246 offset:4096\n\t"
"v_mfma_f32_16x16x32_bf16 a[48:51], a[212:215], a[180:183], a[48:51]\n\t"
"s_add_u32 s29, s23, 1180800\n\t"
"s_waitcnt lgkmcnt(2)\n\t"
"buffer_store_dwordx4 v[240:243], v248, s[16:19], s29 offen offset:0 nt\n\t"
"v_mfma_f32_16x16x32_bf16 a[56:59], a[220:223], a[176:179], a[56:59]\n\t"
"v_mfma_f32_16x16x32_bf16 a[56:59], a[208:211], a[180:183], a[56:59]\n\t"
"v_mfma_f32_16x16x32_bf16 a[56:59], a[212:215], a[184:187], a[56:59]\n\t"
"s_mov_b32 s20, 393216\n\t"
"buffer_load_dwordx4 a[216:219], v252, s[8:11], s20 offen offset:0 sc1\n\t"
"v_mfma_f32_16x16x32_bf16 a[64:67], a[220:223], a[180:183], a[64:67]\n\t"
"ds_read_b64_tr_b16 v[228:229], v246 offset:7168\n\t"
"v_mfma_f32_16x16x32_bf16 a[64:67], a[208:211], a[184:187], a[64:67]\n\t"
"ds_read_b128 v[240:243], v252 offset:31744\n\t"
"v_mfma_f32_16x16x32_bf16 a[64:67], a[212:215], a[188:191], a[64:67]\n\t"
"ds_read_b64_tr_b16 v[230:231], v246 offset:10240\n\t"
"v_mfma_f32_16x16x32_bf16 a[72:75], a[220:223], a[184:187], a[72:75]\n\t"
"v_mfma_f32_16x16x32_bf16 a[72:75], a[208:211], a[188:191], a[72:75]\n\t"
"v_mfma_f32_16x16x32_bf16 a[72:75], a[212:215], a[192:195], a[72:75]\n\t"
"v_mfma_f32_16x16x32_bf16 a[80:83], a[220:223], a[188:191], a[80:83]\n\t"
"v_mfma_f32_16x16x32_bf16 a[80:83], a[208:211], a[192:195], a[80:83]\n\t"
"v_mfma_f32_16x16x32_bf16 a[80:83], a[212:215], a[196:199], a[80:83]\n\t"
"s_waitcnt lgkmcnt(3)\n\t"
"v_mfma_f32_16x16x32_bf16 v[236:239], v[236:239], v[220:223], 0\n\t"
"v_mfma_f32_16x16x32_bf16 a[88:91], a[220:223], a[192:195], a[88:91]\n\t"
"ds_read_b64_tr_b16 v[232:233], v246 offset:13312\n\t"
"v_mfma_f32_16x16x32_bf16 a[88:91], a[208:211], a[196:199], a[88:91]\n\t"
"ds_read_b64_tr_b16 v[234:235], v246 offset:16384\n\t"
"v_mfma_f32_16x16x32_bf16 a[88:91], a[212:215], a[200:203], a[88:91]\n\t"
"v_mfma_f32_16x16x32_bf16 a[96:99], a[220:223], a[196:199], a[96:99]\n\t"
"v_mfma_f32_16x16x32_bf16 a[96:99], a[208:211], a[200:203], a[96:99]\n\t"
"s_nop 0\n\t"
"v_cvt_pk_bf16_f32 v236, v236, v237\n\t"
"v_cvt_pk_bf16_f32 v237, v238, v239\n\t"
"v_mfma_f32_16x16x32_bf16 a[96:99], a[212:215], a[204:207], a[96:99]\n\t"
"buffer_load_dwordx4 a[220:223], v252, s[8:11], s20 offen offset:1024 sc1\n\t"
"s_waitcnt lgkmcnt(2)\n\t"
"v_mfma_f32_16x16x32_bf16 v[228:231], v[228:231], v[220:223], 0\n\t"
"s_add_u32 s29, s23, 1180800\n\t"
"buffer_store_dwordx4 v[240:243], v248, s[16:19], s29 offen offset:128 nt\n\t"
"s_waitcnt vmcnt(11)\n\t"
"v_mfma_f32_16x16x32_bf16 a[104:107], a[224:227], a[168:171], a[104:107]\n\t"
"ds_read_b64_tr_b16 v[224:225], v246 offset:20992\n\t"
"v_mfma_f32_16x16x32_bf16 a[104:107], a[228:231], a[172:175], a[104:107]\n\t"
"ds_read_b64_tr_b16 v[226:227], v246 offset:24064\n\t"
"v_mfma_f32_16x16x32_bf16 a[104:107], a[232:235], a[176:179], a[104:107]\n\t"
"buffer_load_dwordx4 a[212:215], v252, s[8:11], s20 offen offset:2048 sc1\n\t"
"v_mfma_f32_16x16x32_bf16 a[112:115], a[224:227], a[172:175], a[112:115]\n\t"
"ds_read_b128 a[168:171], v244 offset:9216\n\t"
"v_mfma_f32_16x16x32_bf16 a[112:115], a[228:231], a[176:179], a[112:115]\n\t"
"v_cvt_pk_bf16_f32 v228, v228, v229\n\t"
"v_mfma_f32_16x16x32_bf16 a[112:115], a[232:235], a[180:183], a[112:115]\n\t"
"v_cvt_pk_bf16_f32 v229, v230, v231\n\t"
"v_mfma_f32_16x16x32_bf16 a[120:123], a[224:227], a[176:179], a[120:123]\n\t"
"ds_read_b128 v[240:243], v252 offset:32768\n\t"
"v_mfma_f32_16x16x32_bf16 a[120:123], a[228:231], a[180:183], a[120:123]\n\t"
"ds_read_b128 a[172:175], v244 offset:9728\n\t"
"v_mfma_f32_16x16x32_bf16 a[120:123], a[232:235], a[184:187], a[120:123]\n\t"
"ds_read_b128 a[176:179], v244 offset:10240\n\t"
"s_waitcnt lgkmcnt(6)\n\t"
"v_mfma_f32_16x16x32_bf16 v[232:235], v[232:235], v[220:223], 0\n\t"
"v_mfma_f32_16x16x32_bf16 a[128:131], a[224:227], a[180:183], a[128:131]\n\t"
"ds_read_b128 a[180:183], v244 offset:10752\n\t"
"v_mfma_f32_16x16x32_bf16 a[128:131], a[228:231], a[184:187], a[128:131]\n\t"
"v_mfma_f32_16x16x32_bf16 a[128:131], a[232:235], a[188:191], a[128:131]\n\t"
"v_mfma_f32_16x16x32_bf16 a[136:139], a[224:227], a[184:187], a[136:139]\n\t"
"ds_read_b128 a[184:187], v244 offset:11264\n\t"
"v_mfma_f32_16x16x32_bf16 a[136:139], a[228:231], a[188:191], a[136:139]\n\t"
"s_nop 0\n\t"
"v_cvt_pk_bf16_f32 v232, v232, v233\n\t"
"v_mfma_f32_16x16x32_bf16 a[136:139], a[232:235], a[192:195], a[136:139]\n\t"
"v_cvt_pk_bf16_f32 v233, v234, v235\n\t"
"s_waitcnt lgkmcnt(6)\n\t"
"v_mfma_f32_16x16x32_bf16 v[224:227], v[224:227], v[220:223], 0\n\t"
"v_mfma_f32_16x16x32_bf16 a[144:147], a[224:227], a[188:191], a[144:147]\n\t"
"ds_read_b128 a[188:191], v244 offset:11776\n\t"
"v_mfma_f32_16x16x32_bf16 a[144:147], a[228:231], a[192:195], a[144:147]\n\t"
"v_mfma_f32_16x16x32_bf16 a[144:147], a[232:235], a[196:199], a[144:147]\n\t"
"s_add_u32 s29, s23, 1180800\n\t"
"s_waitcnt lgkmcnt(5)\n\t"
"buffer_store_dwordx4 v[240:243], v248, s[16:19], s29 offen offset:256 nt\n\t"
"v_mfma_f32_16x16x32_bf16 a[152:155], a[224:227], a[192:195], a[152:155]\n\t"
"v_mfma_f32_16x16x32_bf16 a[152:155], a[228:231], a[196:199], a[152:155]\n\t"
"v_cvt_pk_bf16_f32 v224, v224, v225\n\t"
"v_cvt_pk_bf16_f32 v225, v226, v227\n\t"
"v_mfma_f32_16x16x32_bf16 a[152:155], a[232:235], a[200:203], a[152:155]\n\t"
"ds_read_b128 a[192:195], v244 offset:12288\n\t"
"v_mfma_f32_16x16x32_bf16 a[160:163], a[224:227], a[196:199], a[160:163]\n\t"
"ds_read_b128 a[196:199], v244 offset:12800\n\t"
"v_mfma_f32_16x16x32_bf16 a[160:163], a[228:231], a[200:203], a[160:163]\n\t"
"ds_read_b128 a[200:203], v244 offset:13312\n\t"
"v_mfma_f32_16x16x32_bf16 a[160:163], a[232:235], a[204:207], a[160:163]\n\t"
"s_mov_b32 s20, 552960\n\t"
"buffer_load_dwordx4 a[224:227], v252, s[8:11], s20 offen offset:0 sc1\n\t"
"s_waitcnt vmcnt(10) lgkmcnt(6)\n\t"
"v_mfma_f32_16x16x32_bf16 v[200:203], a[236:239], a[168:171], v[200:203]\n\t"
"ds_read_b128 a[204:207], v244 offset:13824\n\t"
"v_mfma_f32_16x16x32_bf16 v[200:203], a[240:243], a[172:175], v[200:203]\n\t"
"v_mfma_f32_16x16x32_bf16 v[200:203], a[244:247], a[176:179], v[200:203]\n\t"
"buffer_load_dwordx4 v[248:251], v252, s[8:11], s20 offen offset:1024 sc1\n\t"
"s_waitcnt lgkmcnt(6)\n\t"
"v_mfma_f32_16x16x32_bf16 v[208:211], a[236:239], a[172:175], v[208:211]\n\t"
"v_mfma_f32_16x16x32_bf16 v[208:211], a[240:243], a[176:179], v[208:211]\n\t"
"v_mfma_f32_16x16x32_bf16 v[208:211], a[244:247], a[180:183], v[208:211]\n\t"
"buffer_load_dwordx4 a[228:231], v252, s[8:11], s20 offen offset:2048 sc1\n\t"
"s_waitcnt lgkmcnt(0)\n\t"
"s_barrier\n\t"
"v_mfma_f32_16x16x32_bf16 v[216:219], a[236:239], a[176:179], v[216:219]\n\t"
"v_mfma_f32_16x16x32_bf16 v[216:219], a[240:243], a[180:183], v[216:219]\n\t"
"v_mfma_f32_16x16x32_bf16 v[216:219], a[244:247], a[184:187], v[216:219]\n\t"
"ds_write_b64 v245, v[236:237] offset:0\n\t"
"s_mov_b32 s20, 565248\n\t"
"buffer_load_dwordx4 a[232:235], v252, s[8:11], s20 offen offset:0 sc1\n\t"
"v_mfma_f32_16x16x32_bf16 a[4:7], a[236:239], a[180:183], a[4:7]\n\t"
"ds_read_b64_tr_b16 v[236:237], v246 offset:27136\n\t"
"v_mfma_f32_16x16x32_bf16 a[4:7], a[240:243], a[184:187], a[4:7]\n\t"
"ds_read_b64_tr_b16 v[238:239], v246 offset:30208\n\t"
"v_mfma_f32_16x16x32_bf16 a[4:7], a[244:247], a[188:191], a[4:7]\n\t"
"ds_write_b64 v245, v[228:229] offset:512\n\t"
"ds_write_b64 v245, v[232:233] offset:1024\n\t"
"v_mfma_f32_16x16x32_bf16 a[12:15], a[236:239], a[184:187], a[12:15]\n\t"
"ds_read_b64_tr_b16 v[228:229], v246 offset:33280\n\t"
"v_mfma_f32_16x16x32_bf16 a[12:15], a[240:243], a[188:191], a[12:15]\n\t"
"ds_read_b64_tr_b16 v[230:231], v246 offset:36352\n\t"
"v_mfma_f32_16x16x32_bf16 a[12:15], a[244:247], a[192:195], a[12:15]\n\t"
"ds_write_b64 v245, v[224:225] offset:1536\n\t"
"v_mfma_f32_16x16x32_bf16 a[20:23], a[236:239], a[188:191], a[20:23]\n\t"
"ds_read_b64_tr_b16 v[232:233], v246 offset:40960\n\t"
"v_mfma_f32_16x16x32_bf16 a[20:23], a[240:243], a[192:195], a[20:23]\n\t"
"ds_read_b64_tr_b16 v[234:235], v246 offset:44032\n\t"
"v_mfma_f32_16x16x32_bf16 a[20:23], a[244:247], a[196:199], a[20:23]\n\t"
"global_load_dwordx4 v[240:243], v252, s[46:47] offset:0\n\t"
"s_waitcnt lgkmcnt(7)\n\t"
"v_mfma_f32_16x16x32_bf16 v[236:239], v[236:239], v[220:223], 0\n\t"
"ds_read_b64_tr_b16 v[224:225], v246 offset:47104\n\t"
"ds_read_b64_tr_b16 v[226:227], v246 offset:50176\n\t"
"v_mfma_f32_16x16x32_bf16 a[28:31], a[236:239], a[192:195], a[28:31]\n\t"
"v_mfma_f32_16x16x32_bf16 a[28:31], a[240:243], a[196:199], a[28:31]\n\t"
"v_mfma_f32_16x16x32_bf16 a[28:31], a[244:247], a[200:203], a[28:31]\n\t"
"global_load_dwordx4 a[208:211], v252, s[46:47] offset:1024\n\t"
"s_waitcnt lgkmcnt(5)\n\t"
"v_mfma_f32_16x16x32_bf16 v[228:231], v[228:231], v[220:223], 0\n\t"
"v_mfma_f32_16x16x32_bf16 a[36:39], a[236:239], a[196:199], a[36:39]\n\t"
"v_cvt_pk_bf16_f32 v236, v236, v237\n\t"
"v_cvt_pk_bf16_f32 v237, v238, v239\n\t"
"v_mfma_f32_16x16x32_bf16 a[36:39], a[240:243], a[200:203], a[36:39]\n\t"
"v_mfma_f32_16x16x32_bf16 a[36:39], a[244:247], a[204:207], a[36:39]\n\t"
"buffer_load_dwordx4 a[236:239], v252, s[8:11], s20 offen offset:1024 sc1\n\t"
"s_waitcnt lgkmcnt(2)\n\t"
"v_mfma_f32_16x16x32_bf16 v[232:235], v[232:235], v[220:223], 0\n\t"
"buffer_load_dwordx4 a[240:243], v252, s[8:11], s20 offen offset:2048 sc1\n\t"
"s_waitcnt vmcnt(14)\n\t"
"v_mfma_f32_16x16x32_bf16 a[44:47], a[248:251], a[168:171], a[44:47]\n\t"
"v_cvt_pk_bf16_f32 v228, v228, v229\n\t"
"v_cvt_pk_bf16_f32 v229, v230, v231\n\t"
"v_mfma_f32_16x16x32_bf16 a[44:47], a[252:255], a[172:175], a[44:47]\n\t"
"v_mfma_f32_16x16x32_bf16 a[44:47], v[96:99], a[176:179], a[44:47]\n\t"
"ds_write_b64 v245, v[236:237] offset:2048\n\t"
"s_waitcnt lgkmcnt(1)\n\t"
"v_mfma_f32_16x16x32_bf16 v[224:227], v[224:227], v[220:223], 0\n\t"
"ds_read_b64_tr_b16 v[236:237], v246 offset:53248\n\t"
"ds_read_b64_tr_b16 v[238:239], v246 offset:56320\n\t"
"v_mfma_f32_16x16x32_bf16 a[52:55], a[248:251], a[172:175], a[52:55]\n\t"
"v_cvt_pk_bf16_f32 v232, v232, v233\n\t"
"v_cvt_pk_bf16_f32 v233, v234, v235\n\t"
"v_mfma_f32_16x16x32_bf16 a[52:55], a[252:255], a[176:179], a[52:55]\n\t"
"v_mfma_f32_16x16x32_bf16 a[52:55], v[96:99], a[180:183], a[52:55]\n\t"
"s_mov_b32 s20, 577536\n\t"
"buffer_load_dwordx4 a[244:247], v252, s[8:11], s20 offen offset:0 sc1\n\t"
"v_mfma_f32_16x16x32_bf16 a[60:63], a[248:251], a[176:179], a[60:63]\n\t"
"v_mfma_f32_16x16x32_bf16 a[60:63], a[252:255], a[180:183], a[60:63]\n\t"
"v_cvt_pk_bf16_f32 v224, v224, v225\n\t"
"v_cvt_pk_bf16_f32 v225, v226, v227\n\t"
"v_mfma_f32_16x16x32_bf16 a[60:63], v[96:99], a[184:187], a[60:63]\n\t"
"ds_write_b64 v245, v[228:229] offset:2560\n\t"
"v_mfma_f32_16x16x32_bf16 a[68:71], a[248:251], a[180:183], a[68:71]\n\t"
"v_mfma_f32_16x16x32_bf16 a[68:71], a[252:255], a[184:187], a[68:71]\n\t"
"v_mfma_f32_16x16x32_bf16 a[68:71], v[96:99], a[188:191], a[68:71]\n\t"
"ds_write_b64 v245, v[232:233] offset:3072\n\t"
"s_waitcnt lgkmcnt(2)\n\t"
"v_mfma_f32_16x16x32_bf16 v[236:239], v[236:239], v[220:223], 0\n\t"
"ds_write_b64 v245, v[224:225] offset:3584\n\t"
"v_mfma_f32_16x16x32_bf16 a[76:79], a[248:251], a[184:187], a[76:79]\n\t"
"v_mfma_f32_16x16x32_bf16 a[76:79], a[252:255], a[188:191], a[76:79]\n\t"
"v_mfma_f32_16x16x32_bf16 a[76:79], v[96:99], a[192:195], a[76:79]\n\t"
"v_mfma_f32_16x16x32_bf16 a[84:87], a[248:251], a[188:191], a[84:87]\n\t"
"v_mfma_f32_16x16x32_bf16 a[84:87], a[252:255], a[192:195], a[84:87]\n\t"
"s_nop 1\n\t"
"v_cvt_pk_bf16_f32 v236, v236, v237\n\t"
"v_cvt_pk_bf16_f32 v237, v238, v239\n\t"
"v_mfma_f32_16x16x32_bf16 a[84:87], v[96:99], a[196:199], a[84:87]\n\t"
"v_mfma_f32_16x16x32_bf16 a[92:95], a[248:251], a[192:195], a[92:95]\n\t"
"v_mfma_f32_16x16x32_bf16 a[92:95], a[252:255], a[196:199], a[92:95]\n\t"
"v_mfma_f32_16x16x32_bf16 a[92:95], v[96:99], a[200:203], a[92:95]\n\t"
"ds_write_b64 v245, v[236:237] offset:4096\n\t"
"v_mfma_f32_16x16x32_bf16 a[100:103], a[248:251], a[196:199], a[100:103]\n\t"
"v_mfma_f32_16x16x32_bf16 a[100:103], a[252:255], a[200:203], a[100:103]\n\t"
"v_mfma_f32_16x16x32_bf16 a[100:103], v[96:99], a[204:207], a[100:103]\n\t"
"buffer_load_dwordx4 a[248:251], v252, s[8:11], s20 offen offset:1024 sc1\n\t"
"s_waitcnt vmcnt(11)\n\t"
"v_mfma_f32_16x16x32_bf16 a[108:111], a[216:219], a[168:171], a[108:111]\n\t"
"v_mfma_f32_16x16x32_bf16 a[108:111], a[220:223], a[172:175], a[108:111]\n\t"
"v_mfma_f32_16x16x32_bf16 a[108:111], a[212:215], a[176:179], a[108:111]\n\t"
"buffer_load_dwordx4 v[96:99], v252, s[8:11], s20 offen offset:2048 sc1\n\t"
"s_waitcnt lgkmcnt(0)\n\t"
"s_barrier\n\t"
"v_mfma_f32_16x16x32_bf16 a[116:119], a[216:219], a[172:175], a[116:119]\n\t"
"s_waitcnt vmcnt(6)\n\t"
"ds_read_b64_tr_b16 v[228:229], v240 offset:1024\n\t"
"v_mfma_f32_16x16x32_bf16 a[116:119], a[220:223], a[176:179], a[116:119]\n\t"
"ds_read_b64_tr_b16 v[230:231], v240 offset:2560\n\t"
"v_mfma_f32_16x16x32_bf16 a[116:119], a[212:215], a[180:183], a[116:119]\n\t"
"ds_read_b64_tr_b16 v[232:233], v241 offset:1024\n\t"
"ds_read_b64_tr_b16 v[234:235], v241 offset:2560\n\t"
"v_mfma_f32_16x16x32_bf16 a[124:127], a[216:219], a[176:179], a[124:127]\n\t"
"ds_read_b64_tr_b16 v[224:225], v240 offset:20992\n\t"
"v_mfma_f32_16x16x32_bf16 a[124:127], a[220:223], a[180:183], a[124:127]\n\t"
"ds_read_b64_tr_b16 v[226:227], v240 offset:22528\n\t"
"v_mfma_f32_16x16x32_bf16 a[124:127], a[212:215], a[184:187], a[124:127]\n\t"
"s_mov_b32 s20, 589824\n\t"
"buffer_load_dwordx4 a[252:255], v252, s[8:11], s20 offen offset:0 sc1\n\t"
"v_mfma_f32_16x16x32_bf16 a[132:135], a[216:219], a[180:183], a[132:135]\n\t"
"ds_read_b64_tr_b16 v[236:237], v241 offset:20992\n\t"
"v_mfma_f32_16x16x32_bf16 a[132:135], a[220:223], a[184:187], a[132:135]\n\t"
"ds_read_b64_tr_b16 v[238:239], v241 offset:22528\n\t"
"v_mfma_f32_16x16x32_bf16 a[132:135], a[212:215], a[188:191], a[132:135]\n\t"
"ds_read_b128 a[168:171], v244 offset:0\n\t"
"ds_read_b128 a[172:175], v244 offset:512\n\t"
"s_waitcnt vmcnt(6) lgkmcnt(8)\n\t"
"v_mfma_f32_16x16x32_bf16 v[228:231], v[228:231], a[208:211], 0\n\t"
"ds_read_b128 a[176:179], v244 offset:1024\n\t"
"ds_read_b128 a[180:183], v244 offset:1536\n\t"
"s_waitcnt lgkmcnt(8)\n\t"
"v_mfma_f32_16x16x32_bf16 v[232:235], v[232:235], a[208:211], 0\n\t"
"v_mfma_f32_16x16x32_bf16 a[140:143], a[216:219], a[184:187], a[140:143]\n\t"
"ds_read_b128 a[184:187], v244 offset:2048\n\t"
"v_mfma_f32_16x16x32_bf16 a[140:143], a[220:223], a[188:191], a[140:143]\n\t"
"v_mfma_f32_16x16x32_bf16 a[140:143], a[212:215], a[192:195], a[140:143]\n\t"
"v_mfma_f32_16x16x32_bf16 a[148:151], a[216:219], a[188:191], a[148:151]\n\t"
"v_cvt_pk_bf16_f32 v228, v228, v229\n\t"
"v_cvt_pk_bf16_f32 v229, v230, v231\n\t"
"v_mfma_f32_16x16x32_bf16 a[148:151], a[220:223], a[192:195], a[148:151]\n\t"
"v_cvt_pk_bf16_f32 v230, v232, v233\n\t"
"v_mfma_f32_16x16x32_bf16 a[148:151], a[212:215], a[196:199], a[148:151]\n\t"
"v_cvt_pk_bf16_f32 v231, v234, v235\n\t"
"ds_read_b128 a[188:191], v244 offset:2560\n\t"
"s_waitcnt lgkmcnt(8)\n\t"
"v_mfma_f32_16x16x32_bf16 v[224:227], v[224:227], a[208:211], 0\n\t"
"s_waitcnt lgkmcnt(6)\n\t"
"v_mfma_f32_16x16x32_bf16 v[236:239], v[236:239], a[208:211], 0\n\t"
"s_mov_b64 exec, s[44:45]\n\t"
"ds_write_b128 v242, v[228:231] offset:18432\n\t"
"s_mov_b64 exec, -1\n\t"
"v_mfma_f32_16x16x32_bf16 a[156:159], a[216:219], a[192:195], a[156:159]\n\t"
"ds_read_b128 a[192:195], v244 offset:3072\n\t"
"v_mfma_f32_16x16x32_bf16 a[156:159], a[220:223], a[196:199], a[156:159]\n\t"
"ds_read_b64_tr_b16 v[232:233], v240 offset:40960\n\t"
"v_mfma_f32_16x16x32_bf16 a[156:159], a[212:215], a[200:203], a[156:159]\n\t"
"ds_read_b64_tr_b16 v[234:235], v240 offset:42496\n\t"
"v_mfma_f32_16x16x32_bf16 a[164:167], a[216:219], a[196:199], a[164:167]\n\t"
"v_cvt_pk_bf16_f32 v224, v224, v225\n\t"
"v_cvt_pk_bf16_f32 v225, v226, v227\n\t"
"v_mfma_f32_16x16x32_bf16 a[164:167], a[220:223], a[200:203], a[164:167]\n\t"
"v_cvt_pk_bf16_f32 v226, v236, v237\n\t"
"v_cvt_pk_bf16_f32 v227, v238, v239\n\t"
"v_mfma_f32_16x16x32_bf16 a[164:167], a[212:215], a[204:207], a[164:167]\n\t"
"buffer_load_dwordx4 a[216:219], v252, s[8:11], s20 offen offset:1024 sc1\n\t"
"Lst24_%=:\n\t"
"s_waitcnt lgkmcnt(7)\n\t"
"v_mfma_f32_16x16x32_bf16 v[196:199], a[224:227], a[168:171], v[196:199]\n\t"
"ds_read_b64_tr_b16 v[228:229], v241 offset:40960\n\t"
"v_mfma_f32_16x16x32_bf16 v[196:199], v[248:251], a[172:175], v[196:199]\n\t"
"ds_read_b64_tr_b16 v[230:231], v241 offset:42496\n\t"
"v_add_u32 v240, 59904, v240\n\t"
"v_mfma_f32_16x16x32_bf16 v[196:199], a[228:231], a[176:179], v[196:199]\n\t"
"buffer_load_dwordx4 a[212:215], v252, s[8:11], s20 offen offset:2048 sc1\n\t"
"s_waitcnt lgkmcnt(8)\n\t"
"v_mfma_f32_16x16x32_bf16 v[204:207], a[224:227], a[172:175], v[204:207]\n\t"
"ds_read_b128 a[196:199], v244 offset:3584\n\t"
"v_add_u32 v241, 59904, v241\n\t"
"v_mfma_f32_16x16x32_bf16 v[204:207], v[248:251], a[176:179], v[204:207]\n\t"
"ds_read_b128 a[200:203], v244 offset:4096\n\t"
"v_mfma_f32_16x16x32_bf16 v[204:207], a[228:231], a[180:183], v[204:207]\n\t"
"s_mov_b64 exec, s[44:45]\n\t"
"ds_write_b128 v242, v[224:227] offset:19968\n\t"
"s_mov_b64 exec, -1\n\t"
"s_waitcnt lgkmcnt(10)\n\t"
"v_mfma_f32_16x16x32_bf16 v[212:215], a[224:227], a[176:179], v[212:215]\n\t"
"ds_read_b64_tr_b16 v[236:237], v240 offset:1024\n\t"
"v_mfma_f32_16x16x32_bf16 v[212:215], v[248:251], a[180:183], v[212:215]\n\t"
"ds_read_b64_tr_b16 v[238:239], v240 offset:2560\n\t"
"v_mfma_f32_16x16x32_bf16 v[212:215], a[228:231], a[184:187], v[212:215]\n\t"
"s_mov_b32 s20, 602112\n\t"
"buffer_load_dwordx4 a[220:223], v252, s[8:11], s20 offen offset:0 sc1\n\t"
"s_waitcnt lgkmcnt(7)\n\t"
"v_mfma_f32_16x16x32_bf16 v[232:235], v[232:235], a[208:211], 0\n\t"
"ds_read_b64_tr_b16 v[224:225], v241 offset:1024\n\t"
"ds_read_b64_tr_b16 v[226:227], v241 offset:2560\n\t"
"s_waitcnt lgkmcnt(7)\n\t"
"v_mfma_f32_16x16x32_bf16 v[228:231], v[228:231], a[208:211], 0\n\t"
"ds_read_b128 a[204:207], v244 offset:4608\n\t"
"v_mfma_f32_16x16x32_bf16 a[0:3], a[224:227], a[180:183], a[0:3]\n\t"
"v_mfma_f32_16x16x32_bf16 a[0:3], v[248:251], a[184:187], a[0:3]\n\t"
"v_mfma_f32_16x16x32_bf16 a[0:3], a[228:231], a[188:191], a[0:3]\n\t"
"v_mfma_f32_16x16x32_bf16 a[8:11], a[224:227], a[184:187], a[8:11]\n\t"
"v_cvt_pk_bf16_f32 v232, v232, v233\n\t"
"v_cvt_pk_bf16_f32 v233, v234, v235\n\t"
"v_mfma_f32_16x16x32_bf16 a[8:11], v[248:251], a[188:191], a[8:11]\n\t"
"v_cvt_pk_bf16_f32 v234, v228, v229\n\t"
"v_mfma_f32_16x16x32_bf16 a[8:11], a[228:231], a[192:195], a[8:11]\n\t"
"v_cvt_pk_bf16_f32 v235, v230, v231\n\t"
"s_waitcnt lgkmcnt(3)\n\t"
"v_mfma_f32_16x16x32_bf16 v[236:239], v[236:239], a[208:211], 0\n\t"
"s_mov_b64 exec, s[44:45]\n\t"
"ds_write_b128 v242, v[232:235] offset:21504\n\t"
"s_mov_b64 exec, -1\n\t"
"s_waitcnt lgkmcnt(2)\n\t"
"v_mfma_f32_16x16x32_bf16 v[224:227], v[224:227], a[208:211], 0\n\t"
"ds_read_b64_tr_b16 v[228:229], v240 offset:20992\n\t"
"v_mfma_f32_16x16x32_bf16 a[16:19], a[224:227], a[188:191], a[16:19]\n\t"
"ds_read_b64_tr_b16 v[230:231], v240 offset:22528\n\t"
"v_mfma_f32_16x16x32_bf16 a[16:19], v[248:251], a[192:195], a[16:19]\n\t"
"v_mfma_f32_16x16x32_bf16 a[16:19], a[228:231], a[196:199], a[16:19]\n\t"
"ds_read_b64_tr_b16 v[232:233], v241 offset:20992\n\t"
"v_mfma_f32_16x16x32_bf16 a[24:27], a[224:227], a[192:195], a[24:27]\n\t"
"v_cvt_pk_bf16_f32 v236, v236, v237\n\t"
"v_mfma_f32_16x16x32_bf16 a[24:27], v[248:251], a[196:199], a[24:27]\n\t"
"v_cvt_pk_bf16_f32 v237, v238, v239\n\t"
"v_cvt_pk_bf16_f32 v238, v224, v225\n\t"
"v_mfma_f32_16x16x32_bf16 a[24:27], a[228:231], a[200:203], a[24:27]\n\t"
"v_cvt_pk_bf16_f32 v239, v226, v227\n\t"
"ds_read_b64_tr_b16 v[234:235], v241 offset:22528\n\t"
"s_waitcnt lgkmcnt(5)\n\t"
"v_mfma_f32_16x16x32_bf16 a[32:35], a[224:227], a[196:199], a[32:35]\n\t"
"v_mfma_f32_16x16x32_bf16 a[32:35], v[248:251], a[200:203], a[32:35]\n\t"
"ds_read_b64_tr_b16 v[224:225], v240 offset:40960\n\t"
"v_mfma_f32_16x16x32_bf16 a[32:35], a[228:231], a[204:207], a[32:35]\n\t"
"buffer_load_dwordx4 a[224:227], v252, s[8:11], s20 offen offset:1024 sc1\n\t"
"s_waitcnt vmcnt(8)\n\t"
"v_mfma_f32_16x16x32_bf16 a[40:43], a[232:235], a[168:171], a[40:43]\n\t"
"ds_read_b64_tr_b16 v[226:227], v240 offset:42496\n\t"
"v_mfma_f32_16x16x32_bf16 a[40:43], a[236:239], a[172:175], a[40:43]\n\t"
"v_mfma_f32_16x16x32_bf16 a[40:43], a[240:243], a[176:179], a[40:43]\n\t"
"s_mov_b64 exec, s[44:45]\n\t"
"ds_write_b128 v242, v[236:239] offset:23040\n\t"
"s_mov_b64 exec, -1\n\t"
"v_mfma_f32_16x16x32_bf16 a[48:51], a[232:235], a[172:175], a[48:51]\n\t"
"ds_read_b64_tr_b16 v[236:237], v241 offset:40960\n\t"
"v_mfma_f32_16x16x32_bf16 a[48:51], a[236:239], a[176:179], a[48:51]\n\t"
"ds_read_b64_tr_b16 v[238:239], v241 offset:42496\n\t"
"v_mfma_f32_16x16x32_bf16 a[48:51], a[240:243], a[180:183], a[48:51]\n\t"
"buffer_load_dwordx4 v[248:251], v252, s[8:11], s20 offen offset:2048 sc1\n\t"
"s_waitcnt lgkmcnt(7)\n\t"
"v_mfma_f32_16x16x32_bf16 v[228:231], v[228:231], a[208:211], 0\n\t"
"s_mov_b32 s20, 614400\n\t"
"buffer_load_dwordx4 a[228:231], v252, s[8:11], s20 offen offset:0 sc1\n\t"
"s_waitcnt lgkmcnt(5)\n\t"
"v_mfma_f32_16x16x32_bf16 v[232:235], v[232:235], a[208:211], 0\n\t"
"v_xor_b32 v246, 0xc0, v246\n\t"
"v_mfma_f32_16x16x32_bf16 a[56:59], a[232:235], a[176:179], a[56:59]\n\t"
"v_mfma_f32_16x16x32_bf16 a[56:59], a[236:239], a[180:183], a[56:59]\n\t"
"v_mfma_f32_16x16x32_bf16 a[56:59], a[240:243], a[184:187], a[56:59]\n\t"
"v_mfma_f32_16x16x32_bf16 a[64:67], a[232:235], a[180:183], a[64:67]\n\t"
"v_cvt_pk_bf16_f32 v228, v228, v229\n\t"
"v_mfma_f32_16x16x32_bf16 a[64:67], a[236:239], a[184:187], a[64:67]\n\t"
"v_cvt_pk_bf16_f32 v229, v230, v231\n\t"
"v_cvt_pk_bf16_f32 v230, v232, v233\n\t"
"v_mfma_f32_16x16x32_bf16 a[64:67], a[240:243], a[188:191], a[64:67]\n\t"
"v_cvt_pk_bf16_f32 v231, v234, v235\n\t"
"s_waitcnt lgkmcnt(3)\n\t"
"v_mfma_f32_16x16x32_bf16 v[224:227], v[224:227], a[208:211], 0\n\t"
"s_waitcnt lgkmcnt(0)\n\t"
"v_mfma_f32_16x16x32_bf16 v[236:239], v[236:239], a[208:211], 0\n\t"
"s_mov_b64 exec, s[44:45]\n\t"
"ds_write_b128 v242, v[228:231] offset:24576\n\t"
"s_mov_b64 exec, -1\n\t"
"v_mfma_f32_16x16x32_bf16 a[72:75], a[232:235], a[184:187], a[72:75]\n\t"
"v_mfma_f32_16x16x32_bf16 a[72:75], a[236:239], a[188:191], a[72:75]\n\t"
"v_mfma_f32_16x16x32_bf16 a[72:75], a[240:243], a[192:195], a[72:75]\n\t"
"buffer_load_dwordx4 a[208:211], v252, s[8:11], s20 offen offset:1024 sc1\n\t"
"v_mfma_f32_16x16x32_bf16 a[80:83], a[232:235], a[188:191], a[80:83]\n\t"
"v_cvt_pk_bf16_f32 v224, v224, v225\n\t"
"v_cvt_pk_bf16_f32 v225, v226, v227\n\t"
"v_mfma_f32_16x16x32_bf16 a[80:83], a[236:239], a[192:195], a[80:83]\n\t"
"v_cvt_pk_bf16_f32 v226, v236, v237\n\t"
"v_mfma_f32_16x16x32_bf16 a[80:83], a[240:243], a[196:199], a[80:83]\n\t"
"v_cvt_pk_bf16_f32 v227, v238, v239\n\t"
"ds_read_b64_tr_b16 v[232:233], v246 offset:1024\n\t"
"v_mfma_f32_16x16x32_bf16 a[88:91], a[232:235], a[192:195], a[88:91]\n\t"
"ds_read_b64_tr_b16 v[234:235], v246 offset:4096\n\t"
"v_mfma_f32_16x16x32_bf16 a[88:91], a[236:239], a[196:199], a[88:91]\n\t"
"v_mfma_f32_16x16x32_bf16 a[88:91], a[240:243], a[200:203], a[88:91]\n\t"
"s_mov_b64 exec, s[44:45]\n\t"
"ds_write_b128 v242, v[224:227] offset:26112\n\t"
"s_mov_b64 exec, -1\n\t"
"v_mfma_f32_16x16x32_bf16 a[96:99], a[232:235], a[196:199], a[96:99]\n\t"
"ds_read_b64_tr_b16 v[228:229], v246 offset:7168\n\t"
"v_mfma_f32_16x16x32_bf16 a[96:99], a[236:239], a[200:203], a[96:99]\n\t"
"ds_read_b64_tr_b16 v[230:231], v246 offset:10240\n\t"
"v_mfma_f32_16x16x32_bf16 a[96:99], a[240:243], a[204:207], a[96:99]\n\t"
"s_waitcnt vmcnt(8)\n\t"
"v_mfma_f32_16x16x32_bf16 a[104:107], a[244:247], a[168:171], a[104:107]\n\t"
"ds_read_b128 a[168:171], v244 offset:9216\n\t"
"v_mfma_f32_16x16x32_bf16 a[104:107], a[248:251], a[172:175], a[104:107]\n\t"
"ds_read_b64_tr_b16 v[236:237], v246 offset:13312\n\t"
"v_mfma_f32_16x16x32_bf16 a[104:107], v[96:99], a[176:179], a[104:107]\n\t"
"buffer_load_dwordx4 a[232:235], v252, s[8:11], s20 offen offset:2048 sc1\n\t"
"v_mfma_f32_16x16x32_bf16 a[112:115], a[244:247], a[172:175], a[112:115]\n\t"
"ds_read_b64_tr_b16 v[238:239], v246 offset:16384\n\t"
"v_mfma_f32_16x16x32_bf16 a[112:115], a[248:251], a[176:179], a[112:115]\n\t"
"ds_read_b128 a[172:175], v244 offset:9728\n\t"
"v_mfma_f32_16x16x32_bf16 a[112:115], v[96:99], a[180:183], a[112:115]\n\t"
"s_mov_b32 s20, 479232\n\t"
"buffer_load_dwordx4 a[236:239], v252, s[8:11], s20 offen offset:0 sc1\n\t"
"v_mfma_f32_16x16x32_bf16 a[120:123], a[244:247], a[176:179], a[120:123]\n\t"
"ds_read_b64_tr_b16 v[224:225], v246 offset:20992\n\t"
"v_mfma_f32_16x16x32_bf16 a[120:123], a[248:251], a[180:183], a[120:123]\n\t"
"ds_read_b64_tr_b16 v[226:227], v246 offset:24064\n\t"
"v_mfma_f32_16x16x32_bf16 a[120:123], v[96:99], a[184:187], a[120:123]\n\t"
"buffer_load_dwordx4 a[240:243], v252, s[8:11], s20 offen offset:1024 sc1\n\t"
"s_waitcnt lgkmcnt(9)\n\t"
"v_mfma_f32_16x16x32_bf16 v[232:235], v[232:235], v[220:223], 0\n\t"
"v_mfma_f32_16x16x32_bf16 a[128:131], a[244:247], a[180:183], a[128:131]\n\t"
"ds_read_b128 a[176:179], v244 offset:10240\n\t"
"v_mfma_f32_16x16x32_bf16 a[128:131], a[248:251], a[184:187], a[128:131]\n\t"
"v_mfma_f32_16x16x32_bf16 a[128:131], v[96:99], a[188:191], a[128:131]\n\t"
"ds_read_b128 a[180:183], v244 offset:10752\n\t"
"s_waitcnt lgkmcnt(8)\n\t"
"v_mfma_f32_16x16x32_bf16 v[228:231], v[228:231], v[220:223], 0\n\t"
"v_mfma_f32_16x16x32_bf16 a[136:139], a[244:247], a[184:187], a[136:139]\n\t"
"v_cvt_pk_bf16_f32 v232, v232, v233\n\t"
"v_cvt_pk_bf16_f32 v233, v234, v235\n\t"
"v_mfma_f32_16x16x32_bf16 a[136:139], a[248:251], a[188:191], a[136:139]\n\t"
"ds_read_b128 a[184:187], v244 offset:11264\n\t"
"v_mfma_f32_16x16x32_bf16 a[136:139], v[96:99], a[192:195], a[136:139]\n\t"
"s_waitcnt lgkmcnt(6)\n\t"
"v_mfma_f32_16x16x32_bf16 v[236:239], v[236:239], v[220:223], 0\n\t"
"v_mfma_f32_16x16x32_bf16 a[144:147], a[244:247], a[188:191], a[144:147]\n\t"
"v_cvt_pk_bf16_f32 v234, v228, v229\n\t"
"v_cvt_pk_bf16_f32 v235, v230, v231\n\t"
"v_mfma_f32_16x16x32_bf16 a[144:147], a[248:251], a[192:195], a[144:147]\n\t"
"ds_read_b64_tr_b16 v[228:229], v246 offset:27136\n\t"
"v_mfma_f32_16x16x32_bf16 a[144:147], v[96:99], a[196:199], a[144:147]\n\t"
"ds_read_b64_tr_b16 v[230:231], v246 offset:30208\n\t"
"ds_read_b128 a[188:191], v244 offset:11776\n\t"
"s_waitcnt lgkmcnt(6)\n\t"
"v_mfma_f32_16x16x32_bf16 v[224:227], v[224:227], v[220:223], 0\n\t"
"v_mfma_f32_16x16x32_bf16 a[152:155], a[244:247], a[192:195], a[152:155]\n\t"
"v_cvt_pk_bf16_f32 v236, v236, v237\n\t"
"v_cvt_pk_bf16_f32 v237, v238, v239\n\t"
"v_mfma_f32_16x16x32_bf16 a[152:155], a[248:251], a[196:199], a[152:155]\n\t"
"ds_read_b128 a[192:195], v244 offset:12288\n\t"
"v_mfma_f32_16x16x32_bf16 a[152:155], v[96:99], a[200:203], a[152:155]\n\t"
"v_mfma_f32_16x16x32_bf16 a[160:163], a[244:247], a[196:199], a[160:163]\n\t"
"ds_read_b128 a[196:199], v244 offset:12800\n\t"
"v_mfma_f32_16x16x32_bf16 a[160:163], a[248:251], a[200:203], a[160:163]\n\t"
"v_cvt_pk_bf16_f32 v238, v224, v225\n\t"
"v_cvt_pk_bf16_f32 v239, v226, v227\n\t"
"v_mfma_f32_16x16x32_bf16 a[160:163], v[96:99], a[204:207], a[160:163]\n\t"
"buffer_load_dwordx4 a[244:247], v252, s[8:11], s20 offen offset:2048 sc1\n\t"
"s_waitcnt vmcnt(9) lgkmcnt(7)\n\t"
"v_mfma_f32_16x16x32_bf16 v[200:203], a[252:255], a[168:171], v[200:203]\n\t"
"ds_read_b128 a[200:203], v244 offset:13312\n\t"
"v_mfma_f32_16x16x32_bf16 v[200:203], a[216:219], a[172:175], v[200:203]\n\t"
"ds_read_b128 a[204:207], v244 offset:13824\n\t"
"v_mfma_f32_16x16x32_bf16 v[200:203], a[212:215], a[176:179], v[200:203]\n\t"
"s_mov_b32 s20, 491520\n\t"
"buffer_load_dwordx4 v[96:99], v252, s[8:11], s20 offen offset:0 sc1\n\t"
"s_waitcnt lgkmcnt(5)\n\t"
"v_mfma_f32_16x16x32_bf16 v[228:231], v[228:231], v[220:223], 0\n\t"
"buffer_load_dwordx4 a[248:251], v252, s[8:11], s20 offen offset:1024 sc1\n\t"
"v_mfma_f32_16x16x32_bf16 v[208:211], a[252:255], a[172:175], v[208:211]\n\t"
"v_mfma_f32_16x16x32_bf16 v[208:211], a[216:219], a[176:179], v[208:211]\n\t"
"v_mfma_f32_16x16x32_bf16 v[208:211], a[212:215], a[180:183], v[208:211]\n\t"
"s_waitcnt lgkmcnt(0)\n\t"
"s_barrier\n\t"
"v_mfma_f32_16x16x32_bf16 v[216:219], a[252:255], a[176:179], v[216:219]\n\t"
"ds_read_b64_tr_b16 v[224:225], v246 offset:33280\n\t"
"v_mfma_f32_16x16x32_bf16 v[216:219], a[216:219], a[180:183], v[216:219]\n\t"
"v_cvt_pk_bf16_f32 v228, v228, v229\n\t"
"v_cvt_pk_bf16_f32 v229, v230, v231\n\t"
"v_mfma_f32_16x16x32_bf16 v[216:219], a[212:215], a[184:187], v[216:219]\n\t"
"s_add_u32 m0, s24, 256\n\t"
"s_add_u32 s28, s21, 0\n\t"
"buffer_load_dwordx4 v247, s[12:15], s28 offen offset:256 nt lds\n\t"
"ds_write2st64_b64 v245, v[232:233], v[234:235] offset0:0 offset1:1\n\t"
"s_add_u32 m0, s24, 6400\n\t"
"s_add_u32 s28, s21, 196608\n\t"
"buffer_load_dwordx4 v247, s[12:15], s28 offen offset:256 nt lds\n\t"
"v_mfma_f32_16x16x32_bf16 a[4:7], a[252:255], a[180:183], a[4:7]\n\t"
"ds_read_b64_tr_b16 v[226:227], v246 offset:36352\n\t"
"v_mfma_f32_16x16x32_bf16 a[4:7], a[216:219], a[184:187], a[4:7]\n\t"
"ds_read_b64_tr_b16 v[232:233], v246 offset:40960\n\t"
"v_mfma_f32_16x16x32_bf16 a[4:7], a[212:215], a[188:191], a[4:7]\n\t"
"ds_write2st64_b64 v245, v[236:237], v[238:239] offset0:2 offset1:3\n\t"
"s_add_u32 m0, s24, 12544\n\t"
"s_add_u32 s28, s21, 393216\n\t"
"buffer_load_dwordx4 v247, s[12:15], s28 offen offset:256 nt lds\n\t"
"v_mfma_f32_16x16x32_bf16 a[12:15], a[252:255], a[184:187], a[12:15]\n\t"
"ds_read_b64_tr_b16 v[234:235], v246 offset:44032\n\t"
"v_mfma_f32_16x16x32_bf16 a[12:15], a[216:219], a[188:191], a[12:15]\n\t"
"v_mfma_f32_16x16x32_bf16 a[12:15], a[212:215], a[192:195], a[12:15]\n\t"
"s_add_u32 m0, s24, 20224\n\t"
"s_add_u32 s28, s21, 589824\n\t"
"buffer_load_dwordx4 v247, s[12:15], s28 offen offset:256 nt lds\n\t"
"ds_write_b64 v245, v[228:229] offset:2048\n\t"
"s_add_u32 m0, s24, 26368\n\t"
"s_add_u32 s28, s21, 786432\n\t"
"buffer_load_dwordx4 v247, s[12:15], s28 offen offset:256 nt lds\n\t"
"v_mfma_f32_16x16x32_bf16 a[20:23], a[252:255], a[188:191], a[20:23]\n\t"
"ds_read_b64_tr_b16 v[228:229], v246 offset:47104\n\t"
"v_mfma_f32_16x16x32_bf16 a[20:23], a[216:219], a[192:195], a[20:23]\n\t"
"ds_read_b64_tr_b16 v[230:231], v246 offset:50176\n\t"
"v_mfma_f32_16x16x32_bf16 a[20:23], a[212:215], a[196:199], a[20:23]\n\t"
"s_add_u32 m0, s24, 60160\n\t"
"s_add_u32 s28, s21, 1769472\n\t"
"buffer_load_dwordx4 v247, s[12:15], s28 offen offset:256 nt lds\n\t"
"s_waitcnt lgkmcnt(6)\n\t"
"v_mfma_f32_16x16x32_bf16 v[224:227], v[224:227], v[220:223], 0\n\t"
"v_mfma_f32_16x16x32_bf16 a[28:31], a[252:255], a[192:195], a[28:31]\n\t"
"v_mfma_f32_16x16x32_bf16 a[28:31], a[216:219], a[196:199], a[28:31]\n\t"
"v_mfma_f32_16x16x32_bf16 a[28:31], a[212:215], a[200:203], a[28:31]\n\t"
"s_waitcnt lgkmcnt(3)\n\t"
"v_mfma_f32_16x16x32_bf16 v[232:235], v[232:235], v[220:223], 0\n\t"
"v_mfma_f32_16x16x32_bf16 a[36:39], a[252:255], a[196:199], a[36:39]\n\t"
"s_nop 1\n\t"
"v_cvt_pk_bf16_f32 v224, v224, v225\n\t"
"v_cvt_pk_bf16_f32 v225, v226, v227\n\t"
"v_mfma_f32_16x16x32_bf16 a[36:39], a[216:219], a[200:203], a[36:39]\n\t"
"v_mfma_f32_16x16x32_bf16 a[36:39], a[212:215], a[204:207], a[36:39]\n\t"
"s_waitcnt lgkmcnt(0)\n\t"
"v_mfma_f32_16x16x32_bf16 v[228:231], v[228:231], v[220:223], 0\n\t"
"s_waitcnt vmcnt(14)\n\t"
"v_mfma_f32_16x16x32_bf16 a[44:47], a[220:223], a[168:171], a[44:47]\n\t"
"v_cvt_pk_bf16_f32 v226, v232, v233\n\t"
"v_cvt_pk_bf16_f32 v227, v234, v235\n\t"
"v_mfma_f32_16x16x32_bf16 a[44:47], a[224:227], a[172:175], a[44:47]\n\t"
"ds_read_b64_tr_b16 v[232:233], v246 offset:53248\n\t"
"v_mfma_f32_16x16x32_bf16 a[44:47], v[248:251], a[176:179], a[44:47]\n\t"
"ds_write2st64_b64 v245, v[224:225], v[226:227] offset0:5 offset1:6\n\t"
"v_mfma_f32_16x16x32_bf16 a[52:55], a[220:223], a[172:175], a[52:55]\n\t"
"ds_read_b64_tr_b16 v[234:235], v246 offset:56320\n\t"
"v_mfma_f32_16x16x32_bf16 a[52:55], a[224:227], a[176:179], a[52:55]\n\t"
"v_cvt_pk_bf16_f32 v228, v228, v229\n\t"
"v_mfma_f32_16x16x32_bf16 a[52:55], v[248:251], a[180:183], a[52:55]\n\t"
"v_cvt_pk_bf16_f32 v229, v230, v231\n\t"
"v_mfma_f32_16x16x32_bf16 a[60:63], a[220:223], a[176:179], a[60:63]\n\t"
"v_mfma_f32_16x16x32_bf16 a[60:63], a[224:227], a[180:183], a[60:63]\n\t"
"v_mfma_f32_16x16x32_bf16 a[60:63], v[248:251], a[184:187], a[60:63]\n\t"
"v_mfma_f32_16x16x32_bf16 a[68:71], a[220:223], a[180:183], a[68:71]\n\t"
"v_mfma_f32_16x16x32_bf16 a[68:71], a[224:227], a[184:187], a[68:71]\n\t"
"v_mfma_f32_16x16x32_bf16 a[68:71], v[248:251], a[188:191], a[68:71]\n\t"
"v_mfma_f32_16x16x32_bf16 a[76:79], a[220:223], a[184:187], a[76:79]\n\t"
"v_mfma_f32_16x16x32_bf16 a[76:79], a[224:227], a[188:191], a[76:79]\n\t"
"v_mfma_f32_16x16x32_bf16 a[76:79], v[248:251], a[192:195], a[76:79]\n\t"
"s_waitcnt lgkmcnt(0)\n\t"
"v_mfma_f32_16x16x32_bf16 v[232:235], v[232:235], v[220:223], 0\n\t"
"v_mfma_f32_16x16x32_bf16 a[84:87], a[220:223], a[188:191], a[84:87]\n\t"
"v_mfma_f32_16x16x32_bf16 a[84:87], a[224:227], a[192:195], a[84:87]\n\t"
"v_mfma_f32_16x16x32_bf16 a[84:87], v[248:251], a[196:199], a[84:87]\n\t"
"buffer_load_dwordx4 v[236:239], v252, s[8:11], s20 offen offset:2048 sc1\n\t"
"v_mfma_f32_16x16x32_bf16 a[92:95], a[220:223], a[192:195], a[92:95]\n\t"
"v_mfma_f32_16x16x32_bf16 a[92:95], a[224:227], a[196:199], a[92:95]\n\t"
"s_nop 1\n\t"
"v_cvt_pk_bf16_f32 v230, v232, v233\n\t"
"v_cvt_pk_bf16_f32 v231, v234, v235\n\t"
"v_mfma_f32_16x16x32_bf16 a[92:95], v[248:251], a[200:203], a[92:95]\n\t"
"ds_write2st64_b64 v245, v[228:229], v[230:231] offset0:7 offset1:8\n\t"
"v_mfma_f32_16x16x32_bf16 a[100:103], a[220:223], a[196:199], a[100:103]\n\t"
"v_mfma_f32_16x16x32_bf16 a[100:103], a[224:227], a[200:203], a[100:103]\n\t"
"v_mfma_f32_16x16x32_bf16 a[100:103], v[248:251], a[204:207], a[100:103]\n\t"
"s_waitcnt vmcnt(12)\n\t"
"v_mfma_f32_16x16x32_bf16 a[108:111], a[228:231], a[168:171], a[108:111]\n\t"
"v_mfma_f32_16x16x32_bf16 a[108:111], a[208:211], a[172:175], a[108:111]\n\t"
"v_mfma_f32_16x16x32_bf16 a[108:111], a[232:235], a[176:179], a[108:111]\n\t"
"s_waitcnt lgkmcnt(0)\n\t"
"s_barrier\n\t"
"v_mfma_f32_16x16x32_bf16 a[116:119], a[228:231], a[172:175], a[116:119]\n\t"
"ds_read_b128 a[168:171], v244 offset:0\n\t"
"v_mfma_f32_16x16x32_bf16 a[116:119], a[208:211], a[176:179], a[116:119]\n\t"
"ds_read_b128 a[172:175], v244 offset:512\n\t"
"v_mfma_f32_16x16x32_bf16 a[116:119], a[232:235], a[180:183], a[116:119]\n\t"
"v_mfma_f32_16x16x32_bf16 a[124:127], a[228:231], a[176:179], a[124:127]\n\t"
"ds_read_b128 a[176:179], v244 offset:1024\n\t"
"v_mfma_f32_16x16x32_bf16 a[124:127], a[208:211], a[180:183], a[124:127]\n\t"
"v_mfma_f32_16x16x32_bf16 a[124:127], a[232:235], a[184:187], a[124:127]\n\t"
"v_mfma_f32_16x16x32_bf16 a[132:135], a[228:231], a[180:183], a[132:135]\n\t"
"ds_read_b128 a[180:183], v244 offset:1536\n\t"
"v_mfma_f32_16x16x32_bf16 a[132:135], a[208:211], a[184:187], a[132:135]\n\t"
"v_mfma_f32_16x16x32_bf16 a[132:135], a[232:235], a[188:191], a[132:135]\n\t"
"v_mfma_f32_16x16x32_bf16 a[140:143], a[228:231], a[184:187], a[140:143]\n\t"
"ds_read_b128 a[184:187], v244 offset:2048\n\t"
"v_mfma_f32_16x16x32_bf16 a[140:143], a[208:211], a[188:191], a[140:143]\n\t"
"v_mfma_f32_16x16x32_bf16 a[140:143], a[232:235], a[192:195], a[140:143]\n\t"
"v_mfma_f32_16x16x32_bf16 a[148:151], a[228:231], a[188:191], a[148:151]\n\t"
"ds_read_b128 a[188:191], v244 offset:2560\n\t"
"v_mfma_f32_16x16x32_bf16 a[148:151], a[208:211], a[192:195], a[148:151]\n\t"
"v_mfma_f32_16x16x32_bf16 a[148:151], a[232:235], a[196:199], a[148:151]\n\t"
"v_mfma_f32_16x16x32_bf16 a[156:159], a[228:231], a[192:195], a[156:159]\n\t"
"ds_read_b128 a[192:195], v244 offset:3072\n\t"
"v_mfma_f32_16x16x32_bf16 a[156:159], a[208:211], a[196:199], a[156:159]\n\t"
"v_mfma_f32_16x16x32_bf16 a[156:159], a[232:235], a[200:203], a[156:159]\n\t"
"v_mfma_f32_16x16x32_bf16 a[164:167], a[228:231], a[196:199], a[164:167]\n\t"
"ds_read_b128 a[196:199], v244 offset:3584\n\t"
"v_mfma_f32_16x16x32_bf16 a[164:167], a[208:211], a[200:203], a[164:167]\n\t"
"ds_read_b128 a[200:203], v244 offset:4096\n\t"
"v_mfma_f32_16x16x32_bf16 a[164:167], a[232:235], a[204:207], a[164:167]\n\t"
"ds_read_b128 a[204:207], v244 offset:4608\n\t"
"Lst25_%=:\n\t"
"s_waitcnt vmcnt(9) lgkmcnt(7)\n\t"
"v_mfma_f32_16x16x32_bf16 v[0:3], a[236:239], a[168:171], v[0:3]\n\t"
"v_mfma_f32_16x16x32_bf16 v[0:3], a[240:243], a[172:175], v[0:3]\n\t"
"v_mfma_f32_16x16x32_bf16 v[0:3], a[244:247], a[176:179], v[0:3]\n\t"
"s_mov_b32 s20, 503808\n\t"
"buffer_load_dwordx4 v[240:243], v252, s[8:11], s20 offen offset:0 sc1\n\t"
"s_waitcnt lgkmcnt(6)\n\t"
"v_mfma_f32_16x16x32_bf16 v[4:7], a[236:239], a[172:175], v[4:7]\n\t"
"v_mfma_f32_16x16x32_bf16 v[4:7], a[240:243], a[176:179], v[4:7]\n\t"
"v_mfma_f32_16x16x32_bf16 v[4:7], a[244:247], a[180:183], v[4:7]\n\t"
"buffer_load_dwordx4 v[248:251], v252, s[8:11], s20 offen offset:1024 sc1\n\t"
"s_waitcnt lgkmcnt(5)\n\t"
"v_mfma_f32_16x16x32_bf16 v[8:11], a[236:239], a[176:179], v[8:11]\n\t"
"v_mfma_f32_16x16x32_bf16 v[8:11], a[240:243], a[180:183], v[8:11]\n\t"
"v_mfma_f32_16x16x32_bf16 v[8:11], a[244:247], a[184:187], v[8:11]\n\t"
"buffer_load_dwordx4 a[208:211], v252, s[8:11], s20 offen offset:2048 sc1\n\t"
"s_waitcnt lgkmcnt(4)\n\t"
"v_mfma_f32_16x16x32_bf16 v[12:15], a[236:239], a[180:183], v[12:15]\n\t"
"v_mfma_f32_16x16x32_bf16 v[12:15], a[240:243], a[184:187], v[12:15]\n\t"
"v_mfma_f32_16x16x32_bf16 v[12:15], a[244:247], a[188:191], v[12:15]\n\t"
"s_mov_b32 s20, 516096\n\t"
"buffer_load_dwordx4 a[212:215], v252, s[8:11], s20 offen offset:0 sc1\n\t"
"s_waitcnt lgkmcnt(3)\n\t"
"v_mfma_f32_16x16x32_bf16 v[16:19], a[236:239], a[184:187], v[16:19]\n\t"
"v_mfma_f32_16x16x32_bf16 v[16:19], a[240:243], a[188:191], v[16:19]\n\t"
"v_mfma_f32_16x16x32_bf16 v[16:19], a[244:247], a[192:195], v[16:19]\n\t"
"ds_read_b64_tr_b16 v[224:225], v246 offset:1536\n\t"
"ds_read_b64_tr_b16 v[226:227], v246 offset:4608\n\t"
"s_waitcnt lgkmcnt(4)\n\t"
"v_mfma_f32_16x16x32_bf16 v[20:23], a[236:239], a[188:191], v[20:23]\n\t"
"v_mfma_f32_16x16x32_bf16 v[20:23], a[240:243], a[192:195], v[20:23]\n\t"
"v_mfma_f32_16x16x32_bf16 v[20:23], a[244:247], a[196:199], v[20:23]\n\t"
"buffer_load_dwordx4 a[216:219], v252, s[8:11], s20 offen offset:1024 sc1\n\t"
"s_waitcnt lgkmcnt(3)\n\t"
"v_mfma_f32_16x16x32_bf16 v[24:27], a[236:239], a[192:195], v[24:27]\n\t"
"v_mfma_f32_16x16x32_bf16 v[24:27], a[240:243], a[196:199], v[24:27]\n\t"
"v_mfma_f32_16x16x32_bf16 v[24:27], a[244:247], a[200:203], v[24:27]\n\t"
"buffer_load_dwordx4 a[220:223], v252, s[8:11], s20 offen offset:2048 sc1\n\t"
"s_waitcnt lgkmcnt(2)\n\t"
"v_mfma_f32_16x16x32_bf16 v[28:31], a[236:239], a[196:199], v[28:31]\n\t"
"ds_read_b64_tr_b16 v[232:233], v246 offset:7680\n\t"
"v_mfma_f32_16x16x32_bf16 v[28:31], a[240:243], a[200:203], v[28:31]\n\t"
"ds_read_b64_tr_b16 v[234:235], v246 offset:10752\n\t"
"v_mfma_f32_16x16x32_bf16 v[28:31], a[244:247], a[204:207], v[28:31]\n\t"
"s_waitcnt vmcnt(6)\n\t"
"v_mfma_f32_16x16x32_bf16 v[32:35], v[96:99], a[168:171], v[32:35]\n\t"
"v_mfma_f32_16x16x32_bf16 v[32:35], a[248:251], a[172:175], v[32:35]\n\t"
"v_mfma_f32_16x16x32_bf16 v[32:35], v[236:239], a[176:179], v[32:35]\n\t"
"s_mov_b32 s20, 528384\n\t"
"buffer_load_dwordx4 a[224:227], v252, s[8:11], s20 offen offset:0 sc1\n\t"
"v_mfma_f32_16x16x32_bf16 v[36:39], v[96:99], a[172:175], v[36:39]\n\t"
"v_mfma_f32_16x16x32_bf16 v[36:39], a[248:251], a[176:179], v[36:39]\n\t"
"v_mfma_f32_16x16x32_bf16 v[36:39], v[236:239], a[180:183], v[36:39]\n\t"
"buffer_load_dwordx4 a[228:231], v252, s[8:11], s20 offen offset:1024 sc1\n\t"
"s_waitcnt lgkmcnt(2)\n\t"
"v_mfma_f32_16x16x32_bf16 v[224:227], v[224:227], v[220:223], 0\n\t"
"buffer_load_dwordx4 a[232:235], v252, s[8:11], s20 offen offset:2048 sc1\n\t"
"v_mfma_f32_16x16x32_bf16 v[40:43], v[96:99], a[176:179], v[40:43]\n\t"
"ds_read_b64_tr_b16 v[228:229], v246 offset:13824\n\t"
"v_mfma_f32_16x16x32_bf16 v[40:43], a[248:251], a[180:183], v[40:43]\n\t"
"ds_read_b64_tr_b16 v[230:231], v246 offset:16896\n\t"
"v_mfma_f32_16x16x32_bf16 v[40:43], v[236:239], a[184:187], v[40:43]\n\t"
"v_mfma_f32_16x16x32_bf16 v[44:47], v[96:99], a[180:183], v[44:47]\n\t"
"v_mfma_f32_16x16x32_bf16 v[44:47], a[248:251], a[184:187], v[44:47]\n\t"
"v_cvt_pk_bf16_f32 v224, v224, v225\n\t"
"v_cvt_pk_bf16_f32 v225, v226, v227\n\t"
"v_mfma_f32_16x16x32_bf16 v[44:47], v[236:239], a[188:191], v[44:47]\n\t"
"s_mov_b32 s20, 540672\n\t"
"buffer_load_dwordx4 a[236:239], v252, s[8:11], s20 offen offset:0 sc1\n\t"
"s_waitcnt lgkmcnt(2)\n\t"
"v_mfma_f32_16x16x32_bf16 v[232:235], v[232:235], v[220:223], 0\n\t"
"buffer_load_dwordx4 a[240:243], v252, s[8:11], s20 offen offset:1024 sc1\n\t"
"v_mfma_f32_16x16x32_bf16 v[48:51], v[96:99], a[184:187], v[48:51]\n\t"
"v_mfma_f32_16x16x32_bf16 v[48:51], a[248:251], a[188:191], v[48:51]\n\t"
"v_mfma_f32_16x16x32_bf16 v[48:51], v[236:239], a[192:195], v[48:51]\n\t"
"buffer_load_dwordx4 a[244:247], v252, s[8:11], s20 offen offset:2048 sc1\n\t"
"v_mfma_f32_16x16x32_bf16 v[52:55], v[96:99], a[188:191], v[52:55]\n\t"
"v_mfma_f32_16x16x32_bf16 v[52:55], a[248:251], a[192:195], v[52:55]\n\t"
"s_nop 0\n\t"
"v_cvt_pk_bf16_f32 v226, v232, v233\n\t"
"v_cvt_pk_bf16_f32 v227, v234, v235\n\t"
"v_mfma_f32_16x16x32_bf16 v[52:55], v[236:239], a[196:199], v[52:55]\n\t"
"s_mov_b32 s20, 700416\n\t"
"buffer_load_dwordx4 a[252:255], v252, s[8:11], s20 offen offset:0 sc1\n\t"
"v_mfma_f32_16x16x32_bf16 v[56:59], v[96:99], a[192:195], v[56:59]\n\t"
"ds_read_b64_tr_b16 v[232:233], v246 offset:21504\n\t"
"v_mfma_f32_16x16x32_bf16 v[56:59], a[248:251], a[196:199], v[56:59]\n\t"
"ds_read_b64_tr_b16 v[234:235], v246 offset:24576\n\t"
"v_mfma_f32_16x16x32_bf16 v[56:59], v[236:239], a[200:203], v[56:59]\n\t"
"s_waitcnt lgkmcnt(2)\n\t"
"v_mfma_f32_16x16x32_bf16 v[228:231], v[228:231], v[220:223], 0\n\t"
"v_mfma_f32_16x16x32_bf16 v[60:63], v[96:99], a[196:199], v[60:63]\n\t"
"v_mfma_f32_16x16x32_bf16 v[60:63], a[248:251], a[200:203], v[60:63]\n\t"
"v_mfma_f32_16x16x32_bf16 v[60:63], v[236:239], a[204:207], v[60:63]\n\t"
"buffer_load_dwordx4 v[96:99], v252, s[8:11], s20 offen offset:1024 sc1\n\t"
"s_waitcnt vmcnt(11)\n\t"
"v_mfma_f32_16x16x32_bf16 v[64:67], v[240:243], a[168:171], v[64:67]\n\t"
"ds_read_b128 a[168:171], v244 offset:9216\n\t"
"v_mfma_f32_16x16x32_bf16 v[64:67], v[248:251], a[172:175], v[64:67]\n\t"
"v_cvt_pk_bf16_f32 v228, v228, v229\n\t"
"v_cvt_pk_bf16_f32 v229, v230, v231\n\t"
"v_mfma_f32_16x16x32_bf16 v[64:67], a[208:211], a[176:179], v[64:67]\n\t"
"v_mfma_f32_16x16x32_bf16 v[68:71], v[240:243], a[172:175], v[68:71]\n\t"
"ds_read_b128 a[172:175], v244 offset:9728\n\t"
"v_mfma_f32_16x16x32_bf16 v[68:71], v[248:251], a[176:179], v[68:71]\n\t"
"v_mfma_f32_16x16x32_bf16 v[68:71], a[208:211], a[180:183], v[68:71]\n\t"
"buffer_load_dwordx4 v[236:239], v252, s[8:11], s20 offen offset:2048 sc1\n\t"
"s_waitcnt lgkmcnt(2)\n\t"
"v_mfma_f32_16x16x32_bf16 v[232:235], v[232:235], v[220:223], 0\n\t"
"s_mov_b32 s20, 712704\n\t"
"buffer_load_dwordx4 a[248:251], v252, s[8:11], s20 offen offset:0 sc1\n\t"
"v_mfma_f32_16x16x32_bf16 v[72:75], v[240:243], a[176:179], v[72:75]\n\t"
"ds_read_b128 a[176:179], v244 offset:10240\n\t"
"v_mfma_f32_16x16x32_bf16 v[72:75], v[248:251], a[180:183], v[72:75]\n\t"
"v_mfma_f32_16x16x32_bf16 v[72:75], a[208:211], a[184:187], v[72:75]\n\t"
"v_mfma_f32_16x16x32_bf16 v[76:79], v[240:243], a[180:183], v[76:79]\n\t"
"ds_read_b128 a[180:183], v244 offset:10752\n\t"
"v_mfma_f32_16x16x32_bf16 v[76:79], v[248:251], a[184:187], v[76:79]\n\t"
"v_cvt_pk_bf16_f32 v230, v232, v233\n\t"
"v_cvt_pk_bf16_f32 v231, v234, v235\n\t"
"v_mfma_f32_16x16x32_bf16 v[76:79], a[208:211], a[188:191], v[76:79]\n\t"
"ds_read_b64_tr_b16 v[232:233], v246 offset:27648\n\t"
"v_mfma_f32_16x16x32_bf16 v[80:83], v[240:243], a[184:187], v[80:83]\n\t"
"ds_read_b64_tr_b16 v[234:235], v246 offset:30720\n\t"
"v_mfma_f32_16x16x32_bf16 v[80:83], v[248:251], a[188:191], v[80:83]\n\t"
"ds_read_b128 a[184:187], v244 offset:11264\n\t"
"v_mfma_f32_16x16x32_bf16 v[80:83], a[208:211], a[192:195], v[80:83]\n\t"
"v_mfma_f32_16x16x32_bf16 v[84:87], v[240:243], a[188:191], v[84:87]\n\t"
"ds_read_b128 a[188:191], v244 offset:11776\n\t"
"v_mfma_f32_16x16x32_bf16 v[84:87], v[248:251], a[192:195], v[84:87]\n\t"
"v_mfma_f32_16x16x32_bf16 v[84:87], a[208:211], a[196:199], v[84:87]\n\t"
"v_mfma_f32_16x16x32_bf16 v[88:91], v[240:243], a[192:195], v[88:91]\n\t"
"ds_read_b128 a[192:195], v244 offset:12288\n\t"
"v_mfma_f32_16x16x32_bf16 v[88:91], v[248:251], a[196:199], v[88:91]\n\t"
"v_mfma_f32_16x16x32_bf16 v[88:91], a[208:211], a[200:203], v[88:91]\n\t"
"v_mfma_f32_16x16x32_bf16 v[92:95], v[240:243], a[196:199], v[92:95]\n\t"
"ds_read_b128 a[196:199], v244 offset:12800\n\t"
"v_mfma_f32_16x16x32_bf16 v[92:95], v[248:251], a[200:203], v[92:95]\n\t"
"ds_read_b128 a[200:203], v244 offset:13312\n\t"
"v_mfma_f32_16x16x32_bf16 v[92:95], a[208:211], a[204:207], v[92:95]\n\t"
"buffer_load_dwordx4 v[240:243], v252, s[8:11], s20 offen offset:1024 sc1\n\t"
"s_waitcnt lgkmcnt(5)\n\t"
"v_mfma_f32_16x16x32_bf16 v[232:235], v[232:235], v[220:223], 0\n\t"
"ds_read_b128 a[204:207], v244 offset:13824\n\t"
"s_waitcnt vmcnt(11)\n\t"
"v_mfma_f32_16x16x32_bf16 v[100:103], a[212:215], a[168:171], v[100:103]\n\t"
"v_mfma_f32_16x16x32_bf16 v[100:103], a[216:219], a[172:175], v[100:103]\n\t"
"v_mfma_f32_16x16x32_bf16 v[100:103], a[220:223], a[176:179], v[100:103]\n\t"
"buffer_load_dwordx4 v[248:251], v252, s[8:11], s20 offen offset:2048 sc1\n\t"
"v_mfma_f32_16x16x32_bf16 v[104:107], a[212:215], a[172:175], v[104:107]\n\t"
"v_mfma_f32_16x16x32_bf16 v[104:107], a[216:219], a[176:179], v[104:107]\n\t"
"v_cvt_pk_bf16_f32 v232, v232, v233\n\t"
"v_cvt_pk_bf16_f32 v233, v234, v235\n\t"
"v_mfma_f32_16x16x32_bf16 v[104:107], a[220:223], a[180:183], v[104:107]\n\t"
"s_mov_b32 s20, 724992\n\t"
"buffer_load_dwordx4 a[208:211], v252, s[8:11], s20 offen offset:0 sc1\n\t"
"s_waitcnt lgkmcnt(0)\n\t"
"s_barrier\n\t"
"v_mfma_f32_16x16x32_bf16 v[108:111], a[212:215], a[176:179], v[108:111]\n\t"
"v_mfma_f32_16x16x32_bf16 v[108:111], a[216:219], a[180:183], v[108:111]\n\t"
"v_mfma_f32_16x16x32_bf16 v[108:111], a[220:223], a[184:187], v[108:111]\n\t"
"ds_write2st64_b64 v245, v[224:225], v[226:227] offset0:0 offset1:1\n\t"
"v_mfma_f32_16x16x32_bf16 v[112:115], a[212:215], a[180:183], v[112:115]\n\t"
"ds_read_b64_tr_b16 v[224:225], v246 offset:33792\n\t"
"v_mfma_f32_16x16x32_bf16 v[112:115], a[216:219], a[184:187], v[112:115]\n\t"
"ds_read_b64_tr_b16 v[226:227], v246 offset:36864\n\t"
"v_mfma_f32_16x16x32_bf16 v[112:115], a[220:223], a[188:191], v[112:115]\n\t"
"ds_write2st64_b64 v245, v[228:229], v[230:231] offset0:2 offset1:3\n\t"
"v_mfma_f32_16x16x32_bf16 v[116:119], a[212:215], a[184:187], v[116:119]\n\t"
"ds_read_b64_tr_b16 v[228:229], v246 offset:41472\n\t"
"v_mfma_f32_16x16x32_bf16 v[116:119], a[216:219], a[188:191], v[116:119]\n\t"
"ds_read_b64_tr_b16 v[230:231], v246 offset:44544\n\t"
"v_mfma_f32_16x16x32_bf16 v[116:119], a[220:223], a[192:195], v[116:119]\n\t"
"ds_write_b64 v245, v[232:233] offset:2048\n\t"
"v_mfma_f32_16x16x32_bf16 v[120:123], a[212:215], a[188:191], v[120:123]\n\t"
"ds_read_b64_tr_b16 v[232:233], v246 offset:47616\n\t"
"v_mfma_f32_16x16x32_bf16 v[120:123], a[216:219], a[192:195], v[120:123]\n\t"
"ds_read_b64_tr_b16 v[234:235], v246 offset:50688\n\t"
"v_mfma_f32_16x16x32_bf16 v[120:123], a[220:223], a[196:199], v[120:123]\n\t"
"s_waitcnt lgkmcnt(6)\n\t"
"v_mfma_f32_16x16x32_bf16 v[224:227], v[224:227], v[220:223], 0\n\t"
"s_add_u32 m0, s24, 66304\n\t"
"s_add_u32 s28, s21, 1966080\n\t"
"buffer_load_dwordx4 v247, s[12:15], s28 offen offset:256 nt lds\n\t"
"v_mfma_f32_16x16x32_bf16 v[124:127], a[212:215], a[192:195], v[124:127]\n\t"
"v_mfma_f32_16x16x32_bf16 v[124:127], a[216:219], a[196:199], v[124:127]\n\t"
"v_mfma_f32_16x16x32_bf16 v[124:127], a[220:223], a[200:203], v[124:127]\n\t"
"s_add_u32 m0, s24, 72448\n\t"
"s_add_u32 s28, s21, 2162688\n\t"
"buffer_load_dwordx4 v247, s[12:15], s28 offen offset:256 nt lds\n\t"
"s_waitcnt lgkmcnt(3)\n\t"
"v_mfma_f32_16x16x32_bf16 v[228:231], v[228:231], v[220:223], 0\n\t"
"s_add_u32 m0, s24, 80128\n\t"
"s_add_u32 s28, s21, 2359296\n\t"
"buffer_load_dwordx4 v247, s[12:15], s28 offen offset:256 nt lds\n\t"
"v_mfma_f32_16x16x32_bf16 v[128:131], a[212:215], a[196:199], v[128:131]\n\t"
"v_cvt_pk_bf16_f32 v224, v224, v225\n\t"
"v_cvt_pk_bf16_f32 v225, v226, v227\n\t"
"v_mfma_f32_16x16x32_bf16 v[128:131], a[216:219], a[200:203], v[128:131]\n\t"
"v_mfma_f32_16x16x32_bf16 v[128:131], a[220:223], a[204:207], v[128:131]\n\t"
"s_add_u32 m0, s24, 86272\n\t"
"s_add_u32 s28, s21, 2555904\n\t"
"buffer_load_dwordx4 v247, s[12:15], s28 offen offset:256 nt lds\n\t"
"s_waitcnt lgkmcnt(0)\n\t"
"v_mfma_f32_16x16x32_bf16 v[232:235], v[232:235], v[220:223], 0\n\t"
"s_add_u32 m0, s24, 32512\n\t"
"s_add_u32 s28, s21, 983040\n\t"
"buffer_load_dwordx4 v247, s[12:15], s28 offen offset:256 nt lds\n\t"
"s_waitcnt vmcnt(15)\n\t"
"v_mfma_f32_16x16x32_bf16 v[132:135], a[224:227], a[168:171], v[132:135]\n\t"
"v_cvt_pk_bf16_f32 v226, v228, v229\n\t"
"v_cvt_pk_bf16_f32 v227, v230, v231\n\t"
"v_mfma_f32_16x16x32_bf16 v[132:135], a[228:231], a[172:175], v[132:135]\n\t"
"ds_read_b64_tr_b16 v[228:229], v246 offset:53760\n\t"
"v_mfma_f32_16x16x32_bf16 v[132:135], a[232:235], a[176:179], v[132:135]\n\t"
"s_add_u32 m0, s24, 40192\n\t"
"s_add_u32 s28, s21, 1179648\n\t"
"buffer_load_dwordx4 v247, s[12:15], s28 offen offset:256 nt lds\n\t"
"v_mfma_f32_16x16x32_bf16 v[136:139], a[224:227], a[172:175], v[136:139]\n\t"
"ds_read_b64_tr_b16 v[230:231], v246 offset:56832\n\t"
"v_mfma_f32_16x16x32_bf16 v[136:139], a[228:231], a[176:179], v[136:139]\n\t"
"v_cvt_pk_bf16_f32 v232, v232, v233\n\t"
"v_cvt_pk_bf16_f32 v233, v234, v235\n\t"
"v_mfma_f32_16x16x32_bf16 v[136:139], a[232:235], a[180:183], v[136:139]\n\t"
"ds_write2st64_b64 v245, v[224:225], v[226:227] offset0:5 offset1:6\n\t"
"v_mfma_f32_16x16x32_bf16 v[140:143], a[224:227], a[176:179], v[140:143]\n\t"
"v_mfma_f32_16x16x32_bf16 v[140:143], a[228:231], a[180:183], v[140:143]\n\t"
"v_mfma_f32_16x16x32_bf16 v[140:143], a[232:235], a[184:187], v[140:143]\n\t"
"v_mfma_f32_16x16x32_bf16 v[144:147], a[224:227], a[180:183], v[144:147]\n\t"
"v_mfma_f32_16x16x32_bf16 v[144:147], a[228:231], a[184:187], v[144:147]\n\t"
"v_mfma_f32_16x16x32_bf16 v[144:147], a[232:235], a[188:191], v[144:147]\n\t"
"v_mfma_f32_16x16x32_bf16 v[148:151], a[224:227], a[184:187], v[148:151]\n\t"
"v_mfma_f32_16x16x32_bf16 v[148:151], a[228:231], a[188:191], v[148:151]\n\t"
"v_mfma_f32_16x16x32_bf16 v[148:151], a[232:235], a[192:195], v[148:151]\n\t"
"s_waitcnt lgkmcnt(1)\n\t"
"v_mfma_f32_16x16x32_bf16 v[228:231], v[228:231], v[220:223], 0\n\t"
"v_mfma_f32_16x16x32_bf16 v[152:155], a[224:227], a[188:191], v[152:155]\n\t"
"v_mfma_f32_16x16x32_bf16 v[152:155], a[228:231], a[192:195], v[152:155]\n\t"
"v_mfma_f32_16x16x32_bf16 v[152:155], a[232:235], a[196:199], v[152:155]\n\t"
"v_mfma_f32_16x16x32_bf16 v[156:159], a[224:227], a[192:195], v[156:159]\n\t"
"v_mfma_f32_16x16x32_bf16 v[156:159], a[228:231], a[196:199], v[156:159]\n\t"
"s_nop 2\n\t"
"v_cvt_pk_bf16_f32 v234, v228, v229\n\t"
"v_cvt_pk_bf16_f32 v235, v230, v231\n\t"
"v_mfma_f32_16x16x32_bf16 v[156:159], a[232:235], a[200:203], v[156:159]\n\t"
"ds_write2st64_b64 v245, v[232:233], v[234:235] offset0:7 offset1:8\n\t"
"v_mfma_f32_16x16x32_bf16 v[160:163], a[224:227], a[196:199], v[160:163]\n\t"
"v_mfma_f32_16x16x32_bf16 v[160:163], a[228:231], a[200:203], v[160:163]\n\t"
"v_mfma_f32_16x16x32_bf16 v[160:163], a[232:235], a[204:207], v[160:163]\n\t"
"s_waitcnt vmcnt(13)\n\t"
"v_mfma_f32_16x16x32_bf16 v[164:167], a[236:239], a[168:171], v[164:167]\n\t"
"v_mfma_f32_16x16x32_bf16 v[164:167], a[240:243], a[172:175], v[164:167]\n\t"
"v_mfma_f32_16x16x32_bf16 v[164:167], a[244:247], a[176:179], v[164:167]\n\t"
"buffer_load_dwordx4 a[212:215], v252, s[8:11], s20 offen offset:1024 sc1\n\t"
"s_waitcnt lgkmcnt(0)\n\t"
"s_barrier\n\t"
"v_mfma_f32_16x16x32_bf16 v[168:171], a[236:239], a[172:175], v[168:171]\n\t"
"ds_read_b128 a[168:171], v244 offset:0\n\t"
"v_mfma_f32_16x16x32_bf16 v[168:171], a[240:243], a[176:179], v[168:171]\n\t"
"ds_read_b128 a[172:175], v244 offset:512\n\t"
"v_mfma_f32_16x16x32_bf16 v[168:171], a[244:247], a[180:183], v[168:171]\n\t"
"v_mfma_f32_16x16x32_bf16 v[172:175], a[236:239], a[176:179], v[172:175]\n\t"
"ds_read_b128 a[176:179], v244 offset:1024\n\t"
"v_mfma_f32_16x16x32_bf16 v[172:175], a[240:243], a[180:183], v[172:175]\n\t"
"v_mfma_f32_16x16x32_bf16 v[172:175], a[244:247], a[184:187], v[172:175]\n\t"
"v_mfma_f32_16x16x32_bf16 v[176:179], a[236:239], a[180:183], v[176:179]\n\t"
"ds_read_b128 a[180:183], v244 offset:1536\n\t"
"v_mfma_f32_16x16x32_bf16 v[176:179], a[240:243], a[184:187], v[176:179]\n\t"
"v_mfma_f32_16x16x32_bf16 v[176:179], a[244:247], a[188:191], v[176:179]\n\t"
"v_mfma_f32_16x16x32_bf16 v[180:183], a[236:239], a[184:187], v[180:183]\n\t"
"ds_read_b128 a[184:187], v244 offset:2048\n\t"
"v_mfma_f32_16x16x32_bf16 v[180:183], a[240:243], a[188:191], v[180:183]\n\t"
"v_mfma_f32_16x16x32_bf16 v[180:183], a[244:247], a[192:195], v[180:183]\n\t"
"v_mfma_f32_16x16x32_bf16 v[184:187], a[236:239], a[188:191], v[184:187]\n\t"
"ds_read_b128 a[188:191], v244 offset:2560\n\t"
"v_mfma_f32_16x16x32_bf16 v[184:187], a[240:243], a[192:195], v[184:187]\n\t"
"v_mfma_f32_16x16x32_bf16 v[184:187], a[244:247], a[196:199], v[184:187]\n\t"
"v_mfma_f32_16x16x32_bf16 v[188:191], a[236:239], a[192:195], v[188:191]\n\t"
"ds_read_b128 a[192:195], v244 offset:3072\n\t"
"v_mfma_f32_16x16x32_bf16 v[188:191], a[240:243], a[196:199], v[188:191]\n\t"
"v_mfma_f32_16x16x32_bf16 v[188:191], a[244:247], a[200:203], v[188:191]\n\t"
"v_mfma_f32_16x16x32_bf16 v[192:195], a[236:239], a[196:199], v[192:195]\n\t"
"ds_read_b128 a[196:199], v244 offset:3584\n\t"
"v_mfma_f32_16x16x32_bf16 v[192:195], a[240:243], a[200:203], v[192:195]\n\t"
"ds_read_b128 a[200:203], v244 offset:4096\n\t"
"v_mfma_f32_16x16x32_bf16 v[192:195], a[244:247], a[204:207], v[192:195]\n\t"
"Lst26_%=:\n\t"
"s_waitcnt vmcnt(11) lgkmcnt(6)\n\t"
"v_mfma_f32_16x16x32_bf16 v[0:3], a[252:255], a[168:171], v[0:3]\n\t"
"ds_read_b128 a[204:207], v244 offset:4608\n\t"
"v_mfma_f32_16x16x32_bf16 v[0:3], v[96:99], a[172:175], v[0:3]\n\t"
"v_xor_b32 v246, 0xc0, v246\n\t"
"v_mfma_f32_16x16x32_bf16 v[0:3], v[236:239], a[176:179], v[0:3]\n\t"
"s_add_u32 s23, s40, 6291456\n\t"
"s_add_u32 s22, s22, 6291456\n\t"
"s_waitcnt lgkmcnt(6)\n\t"
"v_mfma_f32_16x16x32_bf16 v[4:7], a[252:255], a[172:175], v[4:7]\n\t"
"v_mfma_f32_16x16x32_bf16 v[4:7], v[96:99], a[176:179], v[4:7]\n\t"
"v_mfma_f32_16x16x32_bf16 v[4:7], v[236:239], a[180:183], v[4:7]\n\t"
"s_waitcnt lgkmcnt(5)\n\t"
"v_mfma_f32_16x16x32_bf16 v[8:11], a[252:255], a[176:179], v[8:11]\n\t"
"v_mfma_f32_16x16x32_bf16 v[8:11], v[96:99], a[180:183], v[8:11]\n\t"
"v_mfma_f32_16x16x32_bf16 v[8:11], v[236:239], a[184:187], v[8:11]\n\t"
"s_waitcnt lgkmcnt(4)\n\t"
"v_mfma_f32_16x16x32_bf16 v[12:15], a[252:255], a[180:183], v[12:15]\n\t"
"v_mfma_f32_16x16x32_bf16 v[12:15], v[96:99], a[184:187], v[12:15]\n\t"
"v_mfma_f32_16x16x32_bf16 v[12:15], v[236:239], a[188:191], v[12:15]\n\t"
"buffer_load_dwordx4 a[216:219], v252, s[8:11], s20 offen offset:2048 sc1\n\t"
"s_waitcnt lgkmcnt(3)\n\t"
"v_mfma_f32_16x16x32_bf16 v[16:19], a[252:255], a[184:187], v[16:19]\n\t"
"v_mfma_f32_16x16x32_bf16 v[16:19], v[96:99], a[188:191], v[16:19]\n\t"
"v_mfma_f32_16x16x32_bf16 v[16:19], v[236:239], a[192:195], v[16:19]\n\t"
"ds_read_b64_tr_b16 v[224:225], v246 offset:1536\n\t"
"ds_read_b64_tr_b16 v[226:227], v246 offset:4608\n\t"
"s_waitcnt lgkmcnt(4)\n\t"
"v_mfma_f32_16x16x32_bf16 v[20:23], a[252:255], a[188:191], v[20:23]\n\t"
"v_mfma_f32_16x16x32_bf16 v[20:23], v[96:99], a[192:195], v[20:23]\n\t"
"v_mfma_f32_16x16x32_bf16 v[20:23], v[236:239], a[196:199], v[20:23]\n\t"
"s_mov_b32 s20, 737280\n\t"
"buffer_load_dwordx4 a[220:223], v252, s[8:11], s20 offen offset:0 sc1\n\t"
"s_waitcnt lgkmcnt(3)\n\t"
"v_mfma_f32_16x16x32_bf16 v[24:27], a[252:255], a[192:195], v[24:27]\n\t"
"v_mfma_f32_16x16x32_bf16 v[24:27], v[96:99], a[196:199], v[24:27]\n\t"
"v_mfma_f32_16x16x32_bf16 v[24:27], v[236:239], a[200:203], v[24:27]\n\t"
"buffer_load_dwordx4 a[224:227], v252, s[8:11], s20 offen offset:1024 sc1\n\t"
"s_waitcnt lgkmcnt(2)\n\t"
"v_mfma_f32_16x16x32_bf16 v[28:31], a[252:255], a[196:199], v[28:31]\n\t"
"ds_read_b64_tr_b16 v[228:229], v246 offset:7680\n\t"
"v_mfma_f32_16x16x32_bf16 v[28:31], v[96:99], a[200:203], v[28:31]\n\t"
"ds_read_b64_tr_b16 v[230:231], v246 offset:10752\n\t"
"v_mfma_f32_16x16x32_bf16 v[28:31], v[236:239], a[204:207], v[28:31]\n\t"
"s_waitcnt vmcnt(11)\n\t"
"v_mfma_f32_16x16x32_bf16 v[32:35], a[248:251], a[168:171], v[32:35]\n\t"
"v_mfma_f32_16x16x32_bf16 v[32:35], v[240:243], a[172:175], v[32:35]\n\t"
"v_mfma_f32_16x16x32_bf16 v[32:35], v[248:251], a[176:179], v[32:35]\n\t"
"buffer_load_dwordx4 v[96:99], v252, s[8:11], s20 offen offset:2048 sc1\n\t"
"v_mfma_f32_16x16x32_bf16 v[36:39], a[248:251], a[172:175], v[36:39]\n\t"
"v_mfma_f32_16x16x32_bf16 v[36:39], v[240:243], a[176:179], v[36:39]\n\t"
"v_mfma_f32_16x16x32_bf16 v[36:39], v[248:251], a[180:183], v[36:39]\n\t"
"s_mov_b32 s20, 749568\n\t"
"buffer_load_dwordx4 v[236:239], v252, s[8:11], s20 offen offset:0 sc1\n\t"
"s_waitcnt lgkmcnt(2)\n\t"
"v_mfma_f32_16x16x32_bf16 v[224:227], v[224:227], v[220:223], 0\n\t"
"buffer_load_dwordx4 a[228:231], v252, s[8:11], s20 offen offset:1024 sc1\n\t"
"v_mfma_f32_16x16x32_bf16 v[40:43], a[248:251], a[176:179], v[40:43]\n\t"
"ds_read_b64_tr_b16 v[232:233], v246 offset:13824\n\t"
"v_mfma_f32_16x16x32_bf16 v[40:43], v[240:243], a[180:183], v[40:43]\n\t"
"v_mfma_f32_16x16x32_bf16 v[40:43], v[248:251], a[184:187], v[40:43]\n\t"
"ds_read_b64_tr_b16 v[234:235], v246 offset:16896\n\t"
"v_mfma_f32_16x16x32_bf16 v[44:47], a[248:251], a[180:183], v[44:47]\n\t"
"v_mfma_f32_16x16x32_bf16 v[44:47], v[240:243], a[184:187], v[44:47]\n\t"
"v_cvt_pk_bf16_f32 v224, v224, v225\n\t"
"v_cvt_pk_bf16_f32 v225, v226, v227\n\t"
"v_mfma_f32_16x16x32_bf16 v[44:47], v[248:251], a[188:191], v[44:47]\n\t"
"s_waitcnt lgkmcnt(2)\n\t"
"v_mfma_f32_16x16x32_bf16 v[228:231], v[228:231], v[220:223], 0\n\t"
"buffer_load_dwordx4 a[232:235], v252, s[8:11], s20 offen offset:2048 sc1\n\t"
"v_mfma_f32_16x16x32_bf16 v[48:51], a[248:251], a[184:187], v[48:51]\n\t"
"v_mfma_f32_16x16x32_bf16 v[48:51], v[240:243], a[188:191], v[48:51]\n\t"
"v_mfma_f32_16x16x32_bf16 v[48:51], v[248:251], a[192:195], v[48:51]\n\t"
"s_mov_b32 s20, 761856\n\t"
"buffer_load_dwordx4 a[236:239], v252, s[8:11], s20 offen offset:0 sc1\n\t"
"v_mfma_f32_16x16x32_bf16 v[52:55], a[248:251], a[188:191], v[52:55]\n\t"
"v_mfma_f32_16x16x32_bf16 v[52:55], v[240:243], a[192:195], v[52:55]\n\t"
"v_cvt_pk_bf16_f32 v226, v228, v229\n\t"
"v_cvt_pk_bf16_f32 v227, v230, v231\n\t"
"v_mfma_f32_16x16x32_bf16 v[52:55], v[248:251], a[196:199], v[52:55]\n\t"
"buffer_load_dwordx4 a[240:243], v252, s[8:11], s20 offen offset:1024 sc1\n\t"
"v_mfma_f32_16x16x32_bf16 v[56:59], a[248:251], a[192:195], v[56:59]\n\t"
"ds_read_b64_tr_b16 v[228:229], v246 offset:21504\n\t"
"v_mfma_f32_16x16x32_bf16 v[56:59], v[240:243], a[196:199], v[56:59]\n\t"
"ds_read_b64_tr_b16 v[230:231], v246 offset:24576\n\t"
"v_mfma_f32_16x16x32_bf16 v[56:59], v[248:251], a[200:203], v[56:59]\n\t"
"s_waitcnt lgkmcnt(2)\n\t"
"v_mfma_f32_16x16x32_bf16 v[232:235], v[232:235], v[220:223], 0\n\t"
"buffer_load_dwordx4 a[244:247], v252, s[8:11], s20 offen offset:2048 sc1\n\t"
"v_mfma_f32_16x16x32_bf16 v[60:63], a[248:251], a[196:199], v[60:63]\n\t"
"v_mfma_f32_16x16x32_bf16 v[60:63], v[240:243], a[200:203], v[60:63]\n\t"
"v_mfma_f32_16x16x32_bf16 v[60:63], v[248:251], a[204:207], v[60:63]\n\t"
"s_mov_b32 s20, 774144\n\t"
"buffer_load_dwordx4 v[240:243], v252, s[8:11], s20 offen offset:0 sc1\n\t"
"s_waitcnt vmcnt(10)\n\t"
"v_mfma_f32_16x16x32_bf16 v[64:67], a[208:211], a[168:171], v[64:67]\n\t"
"ds_read_b128 a[168:171], v244 offset:9216\n\t"
"v_mfma_f32_16x16x32_bf16 v[64:67], a[212:215], a[172:175], v[64:67]\n\t"
"v_cvt_pk_bf16_f32 v232, v232, v233\n\t"
"v_mfma_f32_16x16x32_bf16 v[64:67], a[216:219], a[176:179], v[64:67]\n\t"
"v_cvt_pk_bf16_f32 v233, v234, v235\n\t"
"v_mfma_f32_16x16x32_bf16 v[68:71], a[208:211], a[172:175], v[68:71]\n\t"
"ds_read_b128 a[172:175], v244 offset:9728\n\t"
"v_mfma_f32_16x16x32_bf16 v[68:71], a[212:215], a[176:179], v[68:71]\n\t"
"v_mfma_f32_16x16x32_bf16 v[68:71], a[216:219], a[180:183], v[68:71]\n\t"
"buffer_load_dwordx4 v[248:251], v252, s[8:11], s20 offen offset:1024 sc1\n\t"
"s_waitcnt lgkmcnt(2)\n\t"
"v_mfma_f32_16x16x32_bf16 v[228:231], v[228:231], v[220:223], 0\n\t"
"v_mfma_f32_16x16x32_bf16 v[72:75], a[208:211], a[176:179], v[72:75]\n\t"
"ds_read_b128 a[176:179], v244 offset:10240\n\t"
"v_mfma_f32_16x16x32_bf16 v[72:75], a[212:215], a[180:183], v[72:75]\n\t"
"v_mfma_f32_16x16x32_bf16 v[72:75], a[216:219], a[184:187], v[72:75]\n\t"
"buffer_load_dwordx4 a[248:251], v252, s[8:11], s20 offen offset:2048 sc1\n\t"
"v_mfma_f32_16x16x32_bf16 v[76:79], a[208:211], a[180:183], v[76:79]\n\t"
"ds_read_b128 a[180:183], v244 offset:10752\n\t"
"v_mfma_f32_16x16x32_bf16 v[76:79], a[212:215], a[184:187], v[76:79]\n\t"
"v_cvt_pk_bf16_f32 v234, v228, v229\n\t"
"v_cvt_pk_bf16_f32 v235, v230, v231\n\t"
"v_mfma_f32_16x16x32_bf16 v[76:79], a[216:219], a[188:191], v[76:79]\n\t"
"s_mov_b32 s20, 786432\n\t"
"buffer_load_dwordx4 a[252:255], v252, s[8:11], s20 offen offset:0 sc1\n\t"
"v_mfma_f32_16x16x32_bf16 v[80:83], a[208:211], a[184:187], v[80:83]\n\t"
"ds_read_b64_tr_b16 v[228:229], v246 offset:27648\n\t"
"v_mfma_f32_16x16x32_bf16 v[80:83], a[212:215], a[188:191], v[80:83]\n\t"
"ds_read_b64_tr_b16 v[230:231], v246 offset:30720\n\t"
"v_mfma_f32_16x16x32_bf16 v[80:83], a[216:219], a[192:195], v[80:83]\n\t"
"ds_read_b128 a[184:187], v244 offset:11264\n\t"
"v_mfma_f32_16x16x32_bf16 v[84:87], a[208:211], a[188:191], v[84:87]\n\t"
"ds_read_b128 a[188:191], v244 offset:11776\n\t"
"v_mfma_f32_16x16x32_bf16 v[84:87], a[212:215], a[192:195], v[84:87]\n\t"
"v_mfma_f32_16x16x32_bf16 v[84:87], a[216:219], a[196:199], v[84:87]\n\t"
"v_mfma_f32_16x16x32_bf16 v[88:91], a[208:211], a[192:195], v[88:91]\n\t"
"ds_read_b128 a[192:195], v244 offset:12288\n\t"
"v_mfma_f32_16x16x32_bf16 v[88:91], a[212:215], a[196:199], v[88:91]\n\t"
"v_mfma_f32_16x16x32_bf16 v[88:91], a[216:219], a[200:203], v[88:91]\n\t"
"v_mfma_f32_16x16x32_bf16 v[92:95], a[208:211], a[196:199], v[92:95]\n\t"
"ds_read_b128 a[196:199], v244 offset:12800\n\t"
"v_mfma_f32_16x16x32_bf16 v[92:95], a[212:215], a[200:203], v[92:95]\n\t"
"ds_read_b128 a[200:203], v244 offset:13312\n\t"
"v_mfma_f32_16x16x32_bf16 v[92:95], a[216:219], a[204:207], v[92:95]\n\t"
"buffer_load_dwordx4 a[208:211], v252, s[8:11], s20 offen offset:1024 sc1\n\t"
"s_waitcnt lgkmcnt(5)\n\t"
"v_mfma_f32_16x16x32_bf16 v[228:231], v[228:231], v[220:223], 0\n\t"
"ds_read_b128 a[204:207], v244 offset:13824\n\t"
"s_waitcnt vmcnt(11)\n\t"
"v_mfma_f32_16x16x32_bf16 v[100:103], a[220:223], a[168:171], v[100:103]\n\t"
"v_mfma_f32_16x16x32_bf16 v[100:103], a[224:227], a[172:175], v[100:103]\n\t"
"v_mfma_f32_16x16x32_bf16 v[100:103], v[96:99], a[176:179], v[100:103]\n\t"
"buffer_load_dwordx4 a[212:215], v252, s[8:11], s20 offen offset:2048 sc1\n\t"
"v_mfma_f32_16x16x32_bf16 v[104:107], a[220:223], a[172:175], v[104:107]\n\t"
"v_mfma_f32_16x16x32_bf16 v[104:107], a[224:227], a[176:179], v[104:107]\n\t"
"v_cvt_pk_bf16_f32 v228, v228, v229\n\t"
"v_cvt_pk_bf16_f32 v229, v230, v231\n\t"
"v_mfma_f32_16x16x32_bf16 v[104:107], v[96:99], a[180:183], v[104:107]\n\t"
"s_mov_b32 s20, 798720\n\t"
"buffer_load_dwordx4 a[216:219], v252, s[8:11], s20 offen offset:0 sc1\n\t"
"s_waitcnt lgkmcnt(0)\n\t"
"s_barrier\n\t"
"v_mfma_f32_16x16x32_bf16 v[108:111], a[220:223], a[176:179], v[108:111]\n\t"
"v_mfma_f32_16x16x32_bf16 v[108:111], a[224:227], a[180:183], v[108:111]\n\t"
"v_mfma_f32_16x16x32_bf16 v[108:111], v[96:99], a[184:187], v[108:111]\n\t"
"ds_write2st64_b64 v245, v[224:225], v[226:227] offset0:0 offset1:1\n\t"
"v_mfma_f32_16x16x32_bf16 v[112:115], a[220:223], a[180:183], v[112:115]\n\t"
"ds_read_b64_tr_b16 v[224:225], v246 offset:33792\n\t"
"v_mfma_f32_16x16x32_bf16 v[112:115], a[224:227], a[184:187], v[112:115]\n\t"
"ds_read_b64_tr_b16 v[226:227], v246 offset:36864\n\t"
"v_mfma_f32_16x16x32_bf16 v[112:115], v[96:99], a[188:191], v[112:115]\n\t"
"ds_write2st64_b64 v245, v[232:233], v[234:235] offset0:2 offset1:3\n\t"
"v_mfma_f32_16x16x32_bf16 v[116:119], a[220:223], a[184:187], v[116:119]\n\t"
"ds_read_b64_tr_b16 v[232:233], v246 offset:41472\n\t"
"v_mfma_f32_16x16x32_bf16 v[116:119], a[224:227], a[188:191], v[116:119]\n\t"
"ds_read_b64_tr_b16 v[234:235], v246 offset:44544\n\t"
"v_mfma_f32_16x16x32_bf16 v[116:119], v[96:99], a[192:195], v[116:119]\n\t"
"ds_write_b64 v245, v[228:229] offset:2048\n\t"
"v_mfma_f32_16x16x32_bf16 v[120:123], a[220:223], a[188:191], v[120:123]\n\t"
"ds_read_b64_tr_b16 v[228:229], v246 offset:47616\n\t"
"v_mfma_f32_16x16x32_bf16 v[120:123], a[224:227], a[192:195], v[120:123]\n\t"
"ds_read_b64_tr_b16 v[230:231], v246 offset:50688\n\t"
"v_mfma_f32_16x16x32_bf16 v[120:123], v[96:99], a[196:199], v[120:123]\n\t"
"s_waitcnt lgkmcnt(6)\n\t"
"v_mfma_f32_16x16x32_bf16 v[224:227], v[224:227], v[220:223], 0\n\t"
"v_mfma_f32_16x16x32_bf16 v[124:127], a[220:223], a[192:195], v[124:127]\n\t"
"v_mfma_f32_16x16x32_bf16 v[124:127], a[224:227], a[196:199], v[124:127]\n\t"
"v_mfma_f32_16x16x32_bf16 v[124:127], v[96:99], a[200:203], v[124:127]\n\t"
"s_waitcnt lgkmcnt(3)\n\t"
"v_mfma_f32_16x16x32_bf16 v[232:235], v[232:235], v[220:223], 0\n\t"
"v_mfma_f32_16x16x32_bf16 v[128:131], a[220:223], a[196:199], v[128:131]\n\t"
"s_nop 1\n\t"
"v_cvt_pk_bf16_f32 v224, v224, v225\n\t"
"v_cvt_pk_bf16_f32 v225, v226, v227\n\t"
"v_mfma_f32_16x16x32_bf16 v[128:131], a[224:227], a[200:203], v[128:131]\n\t"
"v_mfma_f32_16x16x32_bf16 v[128:131], v[96:99], a[204:207], v[128:131]\n\t"
"buffer_load_dwordx4 a[220:223], v252, s[8:11], s20 offen offset:1024 sc1\n\t"
"s_waitcnt lgkmcnt(0)\n\t"
"v_mfma_f32_16x16x32_bf16 v[228:231], v[228:231], v[220:223], 0\n\t"
"buffer_load_dwordx4 v[96:99], v252, s[8:11], s20 offen offset:2048 sc1\n\t"
"s_waitcnt vmcnt(12)\n\t"
"v_mfma_f32_16x16x32_bf16 v[132:135], v[236:239], a[168:171], v[132:135]\n\t"
"v_cvt_pk_bf16_f32 v226, v232, v233\n\t"
"v_cvt_pk_bf16_f32 v227, v234, v235\n\t"
"v_mfma_f32_16x16x32_bf16 v[132:135], a[228:231], a[172:175], v[132:135]\n\t"
"ds_read_b64_tr_b16 v[232:233], v246 offset:53760\n\t"
"v_mfma_f32_16x16x32_bf16 v[132:135], a[232:235], a[176:179], v[132:135]\n\t"
"s_mov_b32 s20, 811008\n\t"
"buffer_load_dwordx4 a[224:227], v252, s[8:11], s20 offen offset:0 sc1\n\t"
"v_mfma_f32_16x16x32_bf16 v[136:139], v[236:239], a[172:175], v[136:139]\n\t"
"ds_read_b64_tr_b16 v[234:235], v246 offset:56832\n\t"
"v_mfma_f32_16x16x32_bf16 v[136:139], a[228:231], a[176:179], v[136:139]\n\t"
"v_cvt_pk_bf16_f32 v228, v228, v229\n\t"
"v_cvt_pk_bf16_f32 v229, v230, v231\n\t"
"v_mfma_f32_16x16x32_bf16 v[136:139], a[232:235], a[180:183], v[136:139]\n\t"
"s_add_u32 m0, s24, 46336\n\t"
"s_add_u32 s28, s21, 1376256\n\t"
"buffer_load_dwordx4 v247, s[12:15], s28 offen offset:256 nt lds\n\t"
"s_add_u32 m0, s24, 52480\n\t"
"s_add_u32 s28, s21, 1572864\n\t"
"buffer_load_dwordx4 v247, s[12:15], s28 offen offset:256 nt lds\n\t"
"v_mfma_f32_16x16x32_bf16 v[140:143], v[236:239], a[176:179], v[140:143]\n\t"
"v_mfma_f32_16x16x32_bf16 v[140:143], a[228:231], a[180:183], v[140:143]\n\t"
"v_mfma_f32_16x16x32_bf16 v[140:143], a[232:235], a[184:187], v[140:143]\n\t"
"ds_write2st64_b64 v245, v[224:225], v[226:227] offset0:5 offset1:6\n\t"
"s_add_u32 m0, s24, 92416\n\t"
"s_add_u32 s28, s21, 2752512\n\t"
"buffer_load_dwordx4 v247, s[12:15], s28 offen offset:256 nt lds\n\t"
"v_mfma_f32_16x16x32_bf16 v[144:147], v[236:239], a[180:183], v[144:147]\n\t"
"v_mfma_f32_16x16x32_bf16 v[144:147], a[228:231], a[184:187], v[144:147]\n\t"
"v_mfma_f32_16x16x32_bf16 v[144:147], a[232:235], a[188:191], v[144:147]\n\t"
"s_add_u32 m0, s24, 100096\n\t"
"s_add_u32 s28, s21, 2949120\n\t"
"buffer_load_dwordx4 v247, s[12:15], s28 offen offset:256 nt lds\n\t"
"s_add_u32 m0, s24, 106240\n\t"
"s_add_u32 s28, s21, 3145728\n\t"
"buffer_load_dwordx4 v247, s[12:15], s28 offen offset:256 nt lds\n\t"
"v_mfma_f32_16x16x32_bf16 v[148:151], v[236:239], a[184:187], v[148:151]\n\t"
"v_mfma_f32_16x16x32_bf16 v[148:151], a[228:231], a[188:191], v[148:151]\n\t"
"v_mfma_f32_16x16x32_bf16 v[148:151], a[232:235], a[192:195], v[148:151]\n\t"
"s_add_u32 m0, s24, 112384\n\t"
"s_add_u32 s28, s21, 3342336\n\t"
"buffer_load_dwordx4 v247, s[12:15], s28 offen offset:256 nt lds\n\t"
"s_waitcnt lgkmcnt(1)\n\t"
"v_mfma_f32_16x16x32_bf16 v[232:235], v[232:235], v[220:223], 0\n\t"
"s_add_u32 m0, s25, 256\n\t"
"s_nop 0\n\t"
"buffer_load_dwordx4 v254, s[12:15], 0 offen offset:256 nt lds\n\t"
"v_mfma_f32_16x16x32_bf16 v[152:155], v[236:239], a[188:191], v[152:155]\n\t"
"v_mfma_f32_16x16x32_bf16 v[152:155], a[228:231], a[192:195], v[152:155]\n\t"
"v_mfma_f32_16x16x32_bf16 v[152:155], a[232:235], a[196:199], v[152:155]\n\t"
"s_add_u32 m0, s26, 256\n\t"
"s_mov_b64 exec, s[36:37]\n\t"
"buffer_load_dwordx4 v254, s[12:15], s27 offen offset:256 nt lds\n\t"
"s_mov_b64 exec, -1\n\t"
"v_mfma_f32_16x16x32_bf16 v[156:159], v[236:239], a[192:195], v[156:159]\n\t"
"v_mfma_f32_16x16x32_bf16 v[156:159], a[228:231], a[196:199], v[156:159]\n\t"
"v_cvt_pk_bf16_f32 v230, v232, v233\n\t"
"v_cvt_pk_bf16_f32 v231, v234, v235\n\t"
"v_mfma_f32_16x16x32_bf16 v[156:159], a[232:235], a[200:203], v[156:159]\n\t"
"ds_write2st64_b64 v245, v[228:229], v[230:231] offset0:7 offset1:8\n\t"
"v_mfma_f32_16x16x32_bf16 v[160:163], v[236:239], a[196:199], v[160:163]\n\t"
"v_mfma_f32_16x16x32_bf16 v[160:163], a[228:231], a[200:203], v[160:163]\n\t"
"v_mfma_f32_16x16x32_bf16 v[160:163], a[232:235], a[204:207], v[160:163]\n\t"
"s_waitcnt vmcnt(18)\n\t"
"v_mfma_f32_16x16x32_bf16 v[164:167], a[236:239], a[168:171], v[164:167]\n\t"
"v_mfma_f32_16x16x32_bf16 v[164:167], a[240:243], a[172:175], v[164:167]\n\t"
"v_mfma_f32_16x16x32_bf16 v[164:167], a[244:247], a[176:179], v[164:167]\n\t"
"s_waitcnt lgkmcnt(0)\n\t"
"s_barrier\n\t"
"v_mfma_f32_16x16x32_bf16 v[168:171], a[236:239], a[172:175], v[168:171]\n\t"
"ds_read_b128 a[168:171], v244 offset:0\n\t"
"v_mfma_f32_16x16x32_bf16 v[168:171], a[240:243], a[176:179], v[168:171]\n\t"
"ds_read_b128 a[172:175], v244 offset:512\n\t"
"v_mfma_f32_16x16x32_bf16 v[168:171], a[244:247], a[180:183], v[168:171]\n\t"
"v_mfma_f32_16x16x32_bf16 v[172:175], a[236:239], a[176:179], v[172:175]\n\t"
"ds_read_b128 a[176:179], v244 offset:1024\n\t"
"v_mfma_f32_16x16x32_bf16 v[172:175], a[240:243], a[180:183], v[172:175]\n\t"
"v_mfma_f32_16x16x32_bf16 v[172:175], a[244:247], a[184:187], v[172:175]\n\t"
"v_mfma_f32_16x16x32_bf16 v[176:179], a[236:239], a[180:183], v[176:179]\n\t"
"ds_read_b128 a[180:183], v244 offset:1536\n\t"
"v_mfma_f32_16x16x32_bf16 v[176:179], a[240:243], a[184:187], v[176:179]\n\t"
"v_mfma_f32_16x16x32_bf16 v[176:179], a[244:247], a[188:191], v[176:179]\n\t"
"v_mfma_f32_16x16x32_bf16 v[180:183], a[236:239], a[184:187], v[180:183]\n\t"
"ds_read_b128 a[184:187], v244 offset:2048\n\t"
"v_mfma_f32_16x16x32_bf16 v[180:183], a[240:243], a[188:191], v[180:183]\n\t"
"v_mfma_f32_16x16x32_bf16 v[180:183], a[244:247], a[192:195], v[180:183]\n\t"
"v_mfma_f32_16x16x32_bf16 v[184:187], a[236:239], a[188:191], v[184:187]\n\t"
"ds_read_b128 a[188:191], v244 offset:2560\n\t"
"v_mfma_f32_16x16x32_bf16 v[184:187], a[240:243], a[192:195], v[184:187]\n\t"
"v_mfma_f32_16x16x32_bf16 v[184:187], a[244:247], a[196:199], v[184:187]\n\t"
"v_mfma_f32_16x16x32_bf16 v[188:191], a[236:239], a[192:195], v[188:191]\n\t"
"ds_read_b128 a[192:195], v244 offset:3072\n\t"
"v_mfma_f32_16x16x32_bf16 v[188:191], a[240:243], a[196:199], v[188:191]\n\t"
"v_mfma_f32_16x16x32_bf16 v[188:191], a[244:247], a[200:203], v[188:191]\n\t"
"v_mfma_f32_16x16x32_bf16 v[192:195], a[236:239], a[196:199], v[192:195]\n\t"
"ds_read_b128 a[196:199], v244 offset:3584\n\t"
"v_mfma_f32_16x16x32_bf16 v[192:195], a[240:243], a[200:203], v[192:195]\n\t"
"ds_read_b128 a[200:203], v244 offset:4096\n\t"
"v_mfma_f32_16x16x32_bf16 v[192:195], a[244:247], a[204:207], v[192:195]\n\t"
"Lst27_%=:\n\t"
"s_waitcnt vmcnt(15) lgkmcnt(6)\n\t"
"v_mfma_f32_16x16x32_bf16 v[196:199], v[240:243], a[168:171], v[196:199]\n\t"
"ds_read_b128 a[204:207], v244 offset:4608\n\t"
"v_mfma_f32_16x16x32_bf16 v[196:199], v[248:251], a[172:175], v[196:199]\n\t"
"v_mfma_f32_16x16x32_bf16 v[196:199], a[248:251], a[176:179], v[196:199]\n\t"
"s_waitcnt lgkmcnt(6)\n\t"
"v_mfma_f32_16x16x32_bf16 v[204:207], v[240:243], a[172:175], v[204:207]\n\t"
"v_mfma_f32_16x16x32_bf16 v[204:207], v[248:251], a[176:179], v[204:207]\n\t"
"v_mfma_f32_16x16x32_bf16 v[204:207], a[248:251], a[180:183], v[204:207]\n\t"
"s_waitcnt lgkmcnt(5)\n\t"
"v_mfma_f32_16x16x32_bf16 v[212:215], v[240:243], a[176:179], v[212:215]\n\t"
"v_mfma_f32_16x16x32_bf16 v[212:215], v[248:251], a[180:183], v[212:215]\n\t"
"v_mfma_f32_16x16x32_bf16 v[212:215], a[248:251], a[184:187], v[212:215]\n\t"
"s_waitcnt lgkmcnt(4)\n\t"
"v_mfma_f32_16x16x32_bf16 a[0:3], v[240:243], a[180:183], a[0:3]\n\t"
"v_mfma_f32_16x16x32_bf16 a[0:3], v[248:251], a[184:187], a[0:3]\n\t"
"v_mfma_f32_16x16x32_bf16 a[0:3], a[248:251], a[188:191], a[0:3]\n\t"
"buffer_load_dwordx4 v[236:239], v252, s[8:11], s20 offen offset:1024 sc1\n\t"
"s_waitcnt lgkmcnt(3)\n\t"
"v_mfma_f32_16x16x32_bf16 a[8:11], v[240:243], a[184:187], a[8:11]\n\t"
"v_mfma_f32_16x16x32_bf16 a[8:11], v[248:251], a[188:191], a[8:11]\n\t"
"v_mfma_f32_16x16x32_bf16 a[8:11], a[248:251], a[192:195], a[8:11]\n\t"
"ds_read_b64_tr_b16 v[224:225], v246 offset:512\n\t"
"ds_read_b64_tr_b16 v[226:227], v246 offset:3584\n\t"
"s_waitcnt lgkmcnt(4)\n\t"
"v_mfma_f32_16x16x32_bf16 a[16:19], v[240:243], a[188:191], a[16:19]\n\t"
"v_mfma_f32_16x16x32_bf16 a[16:19], v[248:251], a[192:195], a[16:19]\n\t"
"v_mfma_f32_16x16x32_bf16 a[16:19], a[248:251], a[196:199], a[16:19]\n\t"
"s_waitcnt lgkmcnt(3)\n\t"
"v_mfma_f32_16x16x32_bf16 a[24:27], v[240:243], a[192:195], a[24:27]\n\t"
"v_mfma_f32_16x16x32_bf16 a[24:27], v[248:251], a[196:199], a[24:27]\n\t"
"v_mfma_f32_16x16x32_bf16 a[24:27], a[248:251], a[200:203], a[24:27]\n\t"
"s_waitcnt lgkmcnt(2)\n\t"
"v_mfma_f32_16x16x32_bf16 a[32:35], v[240:243], a[196:199], a[32:35]\n\t"
"ds_read_b64_tr_b16 v[232:233], v246 offset:6656\n\t"
"v_mfma_f32_16x16x32_bf16 a[32:35], v[248:251], a[200:203], a[32:35]\n\t"
"ds_read_b64_tr_b16 v[234:235], v246 offset:9728\n\t"
"v_mfma_f32_16x16x32_bf16 a[32:35], a[248:251], a[204:207], a[32:35]\n\t"
"s_waitcnt vmcnt(13)\n\t"
"v_mfma_f32_16x16x32_bf16 a[40:43], a[252:255], a[168:171], a[40:43]\n\t"
"v_mfma_f32_16x16x32_bf16 a[40:43], a[208:211], a[172:175], a[40:43]\n\t"
"v_mfma_f32_16x16x32_bf16 a[40:43], a[212:215], a[176:179], a[40:43]\n\t"
"v_mfma_f32_16x16x32_bf16 a[48:51], a[252:255], a[172:175], a[48:51]\n\t"
"v_mfma_f32_16x16x32_bf16 a[48:51], a[208:211], a[176:179], a[48:51]\n\t"
"v_mfma_f32_16x16x32_bf16 a[48:51], a[212:215], a[180:183], a[48:51]\n\t"
"s_waitcnt lgkmcnt(2)\n\t"
"v_mfma_f32_16x16x32_bf16 v[224:227], v[224:227], v[220:223], 0\n\t"
"v_mfma_f32_16x16x32_bf16 a[56:59], a[252:255], a[176:179], a[56:59]\n\t"
"ds_read_b64_tr_b16 v[228:229], v246 offset:12800\n\t"
"v_mfma_f32_16x16x32_bf16 a[56:59], a[208:211], a[180:183], a[56:59]\n\t"
"ds_read_b64_tr_b16 v[230:231], v246 offset:15872\n\t"
"v_mfma_f32_16x16x32_bf16 a[56:59], a[212:215], a[184:187], a[56:59]\n\t"
"v_mfma_f32_16x16x32_bf16 a[64:67], a[252:255], a[180:183], a[64:67]\n\t"
"v_mfma_f32_16x16x32_bf16 a[64:67], a[208:211], a[184:187], a[64:67]\n\t"
"s_nop 0\n\t"
"v_cvt_pk_bf16_f32 v224, v224, v225\n\t"
"v_mfma_f32_16x16x32_bf16 a[64:67], a[212:215], a[188:191], a[64:67]\n\t"
"v_cvt_pk_bf16_f32 v225, v226, v227\n\t"
"s_waitcnt lgkmcnt(2)\n\t"
"v_mfma_f32_16x16x32_bf16 v[232:235], v[232:235], v[220:223], 0\n\t"
"v_mfma_f32_16x16x32_bf16 a[72:75], a[252:255], a[184:187], a[72:75]\n\t"
"v_mfma_f32_16x16x32_bf16 a[72:75], a[208:211], a[188:191], a[72:75]\n\t"
"v_mfma_f32_16x16x32_bf16 a[72:75], a[212:215], a[192:195], a[72:75]\n\t"
"buffer_load_dwordx4 v[240:243], v252, s[8:11], s20 offen offset:2048 sc1\n\t"
"v_mfma_f32_16x16x32_bf16 a[80:83], a[252:255], a[188:191], a[80:83]\n\t"
"v_mfma_f32_16x16x32_bf16 a[80:83], a[208:211], a[192:195], a[80:83]\n\t"
"s_nop 1\n\t"
"v_cvt_pk_bf16_f32 v226, v232, v233\n\t"
"v_cvt_pk_bf16_f32 v227, v234, v235\n\t"
"v_mfma_f32_16x16x32_bf16 a[80:83], a[212:215], a[196:199], a[80:83]\n\t"
"s_mov_b32 s20, 823296\n\t"
"buffer_load_dwordx4 v[248:251], v252, s[8:11], s20 offen offset:0 sc1\n\t"
"v_mfma_f32_16x16x32_bf16 a[88:91], a[252:255], a[192:195], a[88:91]\n\t"
"ds_read_b64_tr_b16 v[232:233], v246 offset:20480\n\t"
"v_mfma_f32_16x16x32_bf16 a[88:91], a[208:211], a[196:199], a[88:91]\n\t"
"ds_read_b64_tr_b16 v[234:235], v246 offset:23552\n\t"
"v_mfma_f32_16x16x32_bf16 a[88:91], a[212:215], a[200:203], a[88:91]\n\t"
"buffer_load_dwordx4 a[228:231], v252, s[8:11], s20 offen offset:1024 sc1\n\t"
"s_waitcnt lgkmcnt(2)\n\t"
"v_mfma_f32_16x16x32_bf16 v[228:231], v[228:231], v[220:223], 0\n\t"
"v_mfma_f32_16x16x32_bf16 a[96:99], a[252:255], a[196:199], a[96:99]\n\t"
"v_mfma_f32_16x16x32_bf16 a[96:99], a[208:211], a[200:203], a[96:99]\n\t"
"v_mfma_f32_16x16x32_bf16 a[96:99], a[212:215], a[204:207], a[96:99]\n\t"
"buffer_load_dwordx4 a[208:211], v252, s[8:11], s20 offen offset:2048 sc1\n\t"
"s_waitcnt vmcnt(14)\n\t"
"v_mfma_f32_16x16x32_bf16 a[104:107], a[216:219], a[168:171], a[104:107]\n\t"
"ds_read_b128 a[168:171], v244 offset:9216\n\t"
"v_mfma_f32_16x16x32_bf16 a[104:107], a[220:223], a[172:175], a[104:107]\n\t"
"v_cvt_pk_bf16_f32 v228, v228, v229\n\t"
"v_mfma_f32_16x16x32_bf16 a[104:107], v[96:99], a[176:179], a[104:107]\n\t"
"v_cvt_pk_bf16_f32 v229, v230, v231\n\t"
"v_mfma_f32_16x16x32_bf16 a[112:115], a[216:219], a[172:175], a[112:115]\n\t"
"ds_read_b128 a[172:175], v244 offset:9728\n\t"
"v_mfma_f32_16x16x32_bf16 a[112:115], a[220:223], a[176:179], a[112:115]\n\t"
"v_mfma_f32_16x16x32_bf16 a[112:115], v[96:99], a[180:183], a[112:115]\n\t"
"s_mov_b32 s20, 835584\n\t"
"buffer_load_dwordx4 a[212:215], v252, s[8:11], s20 offen offset:0 sc1\n\t"
"s_waitcnt lgkmcnt(2)\n\t"
"v_mfma_f32_16x16x32_bf16 v[232:235], v[232:235], v[220:223], 0\n\t"
"v_mfma_f32_16x16x32_bf16 a[120:123], a[216:219], a[176:179], a[120:123]\n\t"
"ds_read_b128 a[176:179], v244 offset:10240\n\t"
"v_mfma_f32_16x16x32_bf16 a[120:123], a[220:223], a[180:183], a[120:123]\n\t"
"v_mfma_f32_16x16x32_bf16 a[120:123], v[96:99], a[184:187], a[120:123]\n\t"
"buffer_load_dwordx4 a[232:235], v252, s[8:11], s20 offen offset:1024 sc1\n\t"
"v_mfma_f32_16x16x32_bf16 a[128:131], a[216:219], a[180:183], a[128:131]\n\t"
"ds_read_b128 a[180:183], v244 offset:10752\n\t"
"v_mfma_f32_16x16x32_bf16 a[128:131], a[220:223], a[184:187], a[128:131]\n\t"
"v_cvt_pk_bf16_f32 v230, v232, v233\n\t"
"v_cvt_pk_bf16_f32 v231, v234, v235\n\t"
"v_mfma_f32_16x16x32_bf16 a[128:131], v[96:99], a[188:191], a[128:131]\n\t"
"buffer_load_dwordx4 a[236:239], v252, s[8:11], s20 offen offset:2048 sc1\n\t"
"v_mfma_f32_16x16x32_bf16 a[136:139], a[216:219], a[184:187], a[136:139]\n\t"
"ds_read_b64_tr_b16 v[232:233], v246 offset:26624\n\t"
"v_mfma_f32_16x16x32_bf16 a[136:139], a[220:223], a[188:191], a[136:139]\n\t"
"ds_read_b64_tr_b16 v[234:235], v246 offset:29696\n\t"
"v_mfma_f32_16x16x32_bf16 a[136:139], v[96:99], a[192:195], a[136:139]\n\t"
"s_mov_b32 s20, 995328\n\t"
"buffer_load_dwordx4 a[240:243], v252, s[8:11], s20 offen offset:0 sc1\n\t"
"v_mfma_f32_16x16x32_bf16 a[144:147], a[216:219], a[188:191], a[144:147]\n\t"
"ds_read_b128 a[184:187], v244 offset:11264\n\t"
"v_mfma_f32_16x16x32_bf16 a[144:147], a[220:223], a[192:195], a[144:147]\n\t"
"ds_read_b128 a[188:191], v244 offset:11776\n\t"
"v_mfma_f32_16x16x32_bf16 a[144:147], v[96:99], a[196:199], a[144:147]\n\t"
"buffer_load_dwordx4 a[244:247], v252, s[8:11], s20 offen offset:1024 sc1\n\t"
"v_mfma_f32_16x16x32_bf16 a[152:155], a[216:219], a[192:195], a[152:155]\n\t"
"v_mfma_f32_16x16x32_bf16 a[152:155], a[220:223], a[196:199], a[152:155]\n\t"
"ds_read_b128 a[192:195], v244 offset:12288\n\t"
"v_mfma_f32_16x16x32_bf16 a[152:155], v[96:99], a[200:203], a[152:155]\n\t"
"v_mfma_f32_16x16x32_bf16 a[160:163], a[216:219], a[196:199], a[160:163]\n\t"
"ds_read_b128 a[196:199], v244 offset:12800\n\t"
"v_mfma_f32_16x16x32_bf16 a[160:163], a[220:223], a[200:203], a[160:163]\n\t"
"ds_read_b128 a[200:203], v244 offset:13312\n\t"
"v_mfma_f32_16x16x32_bf16 a[160:163], v[96:99], a[204:207], a[160:163]\n\t"
"buffer_load_dwordx4 a[216:219], v252, s[8:11], s20 offen offset:2048 sc1\n\t"
"s_waitcnt lgkmcnt(5)\n\t"
"v_mfma_f32_16x16x32_bf16 v[232:235], v[232:235], v[220:223], 0\n\t"
"ds_read_b128 a[204:207], v244 offset:13824\n\t"
"s_waitcnt vmcnt(9)\n\t"
"v_mfma_f32_16x16x32_bf16 v[200:203], a[224:227], a[168:171], v[200:203]\n\t"
"v_mfma_f32_16x16x32_bf16 v[200:203], v[236:239], a[172:175], v[200:203]\n\t"
"v_mfma_f32_16x16x32_bf16 v[200:203], v[240:243], a[176:179], v[200:203]\n\t"
"s_mov_b32 s20, 1007616\n\t"
"buffer_load_dwordx4 v[96:99], v252, s[8:11], s20 offen offset:0 sc1\n\t"
"v_mfma_f32_16x16x32_bf16 v[208:211], a[224:227], a[172:175], v[208:211]\n\t"
"v_mfma_f32_16x16x32_bf16 v[208:211], v[236:239], a[176:179], v[208:211]\n\t"
"v_cvt_pk_bf16_f32 v232, v232, v233\n\t"
"v_cvt_pk_bf16_f32 v233, v234, v235\n\t"
"v_mfma_f32_16x16x32_bf16 v[208:211], v[240:243], a[180:183], v[208:211]\n\t"
"buffer_load_dwordx4 a[220:223], v252, s[8:11], s20 offen offset:1024 sc1\n\t"
"s_waitcnt lgkmcnt(0)\n\t"
"s_barrier\n\t"
"v_mfma_f32_16x16x32_bf16 v[216:219], a[224:227], a[176:179], v[216:219]\n\t"
"v_mfma_f32_16x16x32_bf16 v[216:219], v[236:239], a[180:183], v[216:219]\n\t"
"v_mfma_f32_16x16x32_bf16 v[216:219], v[240:243], a[184:187], v[216:219]\n\t"
"ds_write2st64_b64 v245, v[224:225], v[226:227] offset0:0 offset1:1\n\t"
"buffer_load_dwordx4 a[248:251], v252, s[8:11], s20 offen offset:2048 sc1\n\t"
"v_mfma_f32_16x16x32_bf16 a[4:7], a[224:227], a[180:183], a[4:7]\n\t"
"ds_read_b64_tr_b16 v[224:225], v246 offset:32768\n\t"
"v_mfma_f32_16x16x32_bf16 a[4:7], v[236:239], a[184:187], a[4:7]\n\t"
"ds_read_b64_tr_b16 v[226:227], v246 offset:35840\n\t"
"v_mfma_f32_16x16x32_bf16 a[4:7], v[240:243], a[188:191], a[4:7]\n\t"
"ds_write2st64_b64 v245, v[228:229], v[230:231] offset0:2 offset1:3\n\t"
"ds_write_b64 v245, v[232:233] offset:2048\n\t"
"v_mfma_f32_16x16x32_bf16 a[12:15], a[224:227], a[184:187], a[12:15]\n\t"
"ds_read_b64_tr_b16 v[228:229], v246 offset:40448\n\t"
"v_mfma_f32_16x16x32_bf16 a[12:15], v[236:239], a[188:191], a[12:15]\n\t"
"ds_read_b64_tr_b16 v[230:231], v246 offset:43520\n\t"
"v_mfma_f32_16x16x32_bf16 a[12:15], v[240:243], a[192:195], a[12:15]\n\t"
"s_mov_b32 s20, 1019904\n\t"
"buffer_load_dwordx4 a[252:255], v252, s[8:11], s20 offen offset:0 sc1\n\t"
"v_mfma_f32_16x16x32_bf16 a[20:23], a[224:227], a[188:191], a[20:23]\n\t"
"ds_read_b64_tr_b16 v[232:233], v246 offset:46592\n\t"
"v_mfma_f32_16x16x32_bf16 a[20:23], v[236:239], a[192:195], a[20:23]\n\t"
"ds_read_b64_tr_b16 v[234:235], v246 offset:49664\n\t"
"v_mfma_f32_16x16x32_bf16 a[20:23], v[240:243], a[196:199], a[20:23]\n\t"
"s_waitcnt lgkmcnt(6)\n\t"
"v_mfma_f32_16x16x32_bf16 v[224:227], v[224:227], v[220:223], 0\n\t"
"v_mfma_f32_16x16x32_bf16 a[28:31], a[224:227], a[192:195], a[28:31]\n\t"
"v_mfma_f32_16x16x32_bf16 a[28:31], v[236:239], a[196:199], a[28:31]\n\t"
"v_mfma_f32_16x16x32_bf16 a[28:31], v[240:243], a[200:203], a[28:31]\n\t"
"s_waitcnt lgkmcnt(2)\n\t"
"v_mfma_f32_16x16x32_bf16 v[228:231], v[228:231], v[220:223], 0\n\t"
"v_mfma_f32_16x16x32_bf16 a[36:39], a[224:227], a[196:199], a[36:39]\n\t"
"s_nop 1\n\t"
"v_cvt_pk_bf16_f32 v224, v224, v225\n\t"
"v_cvt_pk_bf16_f32 v225, v226, v227\n\t"
"v_mfma_f32_16x16x32_bf16 a[36:39], v[236:239], a[200:203], a[36:39]\n\t"
"v_mfma_f32_16x16x32_bf16 a[36:39], v[240:243], a[204:207], a[36:39]\n\t"
"buffer_load_dwordx4 a[224:227], v252, s[8:11], s20 offen offset:1024 sc1\n\t"
"s_waitcnt lgkmcnt(0)\n\t"
"v_mfma_f32_16x16x32_bf16 v[232:235], v[232:235], v[220:223], 0\n\t"
"buffer_load_dwordx4 v[236:239], v252, s[8:11], s20 offen offset:2048 sc1\n\t"
"s_waitcnt vmcnt(12)\n\t"
"v_mfma_f32_16x16x32_bf16 a[44:47], v[248:251], a[168:171], a[44:47]\n\t"
"v_cvt_pk_bf16_f32 v226, v228, v229\n\t"
"v_cvt_pk_bf16_f32 v227, v230, v231\n\t"
"v_mfma_f32_16x16x32_bf16 a[44:47], a[228:231], a[172:175], a[44:47]\n\t"
"ds_read_b64_tr_b16 v[228:229], v246 offset:52736\n\t"
"v_mfma_f32_16x16x32_bf16 a[44:47], a[208:211], a[176:179], a[44:47]\n\t"
"s_mov_b32 s20, 1032192\n\t"
"buffer_load_dwordx4 v[240:243], v252, s[8:11], s20 offen offset:0 sc1\n\t"
"v_mfma_f32_16x16x32_bf16 a[52:55], v[248:251], a[172:175], a[52:55]\n\t"
"ds_read_b64_tr_b16 v[230:231], v246 offset:55808\n\t"
"v_mfma_f32_16x16x32_bf16 a[52:55], a[228:231], a[176:179], a[52:55]\n\t"
"v_cvt_pk_bf16_f32 v232, v232, v233\n\t"
"v_cvt_pk_bf16_f32 v233, v234, v235\n\t"
"v_mfma_f32_16x16x32_bf16 a[52:55], a[208:211], a[180:183], a[52:55]\n\t"
"ds_write2st64_b64 v245, v[224:225], v[226:227] offset0:5 offset1:6\n\t"
"v_mfma_f32_16x16x32_bf16 a[60:63], v[248:251], a[176:179], a[60:63]\n\t"
"v_mfma_f32_16x16x32_bf16 a[60:63], a[228:231], a[180:183], a[60:63]\n\t"
"v_mfma_f32_16x16x32_bf16 a[60:63], a[208:211], a[184:187], a[60:63]\n\t"
"v_mfma_f32_16x16x32_bf16 a[68:71], v[248:251], a[180:183], a[68:71]\n\t"
"v_mfma_f32_16x16x32_bf16 a[68:71], a[228:231], a[184:187], a[68:71]\n\t"
"v_mfma_f32_16x16x32_bf16 a[68:71], a[208:211], a[188:191], a[68:71]\n\t"
"v_mfma_f32_16x16x32_bf16 a[76:79], v[248:251], a[184:187], a[76:79]\n\t"
"v_mfma_f32_16x16x32_bf16 a[76:79], a[228:231], a[188:191], a[76:79]\n\t"
"v_mfma_f32_16x16x32_bf16 a[76:79], a[208:211], a[192:195], a[76:79]\n\t"
"s_waitcnt lgkmcnt(1)\n\t"
"v_mfma_f32_16x16x32_bf16 v[228:231], v[228:231], v[220:223], 0\n\t"
"v_mfma_f32_16x16x32_bf16 a[84:87], v[248:251], a[188:191], a[84:87]\n\t"
"v_mfma_f32_16x16x32_bf16 a[84:87], a[228:231], a[192:195], a[84:87]\n\t"
"v_mfma_f32_16x16x32_bf16 a[84:87], a[208:211], a[196:199], a[84:87]\n\t"
"v_mfma_f32_16x16x32_bf16 a[92:95], v[248:251], a[192:195], a[92:95]\n\t"
"v_mfma_f32_16x16x32_bf16 a[92:95], a[228:231], a[196:199], a[92:95]\n\t"
"s_nop 2\n\t"
"v_cvt_pk_bf16_f32 v234, v228, v229\n\t"
"v_cvt_pk_bf16_f32 v235, v230, v231\n\t"
"v_mfma_f32_16x16x32_bf16 a[92:95], a[208:211], a[200:203], a[92:95]\n\t"
"ds_write2st64_b64 v245, v[232:233], v[234:235] offset0:7 offset1:8\n\t"
"v_mfma_f32_16x16x32_bf16 a[100:103], v[248:251], a[196:199], a[100:103]\n\t"
"v_mfma_f32_16x16x32_bf16 a[100:103], a[228:231], a[200:203], a[100:103]\n\t"
"v_mfma_f32_16x16x32_bf16 a[100:103], a[208:211], a[204:207], a[100:103]\n\t"
"buffer_load_dwordx4 v[248:251], v252, s[8:11], s20 offen offset:1024 sc1\n\t"
"s_waitcnt vmcnt(11)\n\t"
"v_mfma_f32_16x16x32_bf16 a[108:111], a[212:215], a[168:171], a[108:111]\n\t"
"v_mfma_f32_16x16x32_bf16 a[108:111], a[232:235], a[172:175], a[108:111]\n\t"
"v_mfma_f32_16x16x32_bf16 a[108:111], a[236:239], a[176:179], a[108:111]\n\t"
"buffer_load_dwordx4 a[208:211], v252, s[8:11], s20 offen offset:2048 sc1\n\t"
"s_waitcnt lgkmcnt(0)\n\t"
"s_barrier\n\t"
"v_mfma_f32_16x16x32_bf16 a[116:119], a[212:215], a[172:175], a[116:119]\n\t"
"ds_read_b128 a[168:171], v244 offset:0\n\t"
"v_mfma_f32_16x16x32_bf16 a[116:119], a[232:235], a[176:179], a[116:119]\n\t"
"ds_read_b128 a[172:175], v244 offset:512\n\t"
"v_mfma_f32_16x16x32_bf16 a[116:119], a[236:239], a[180:183], a[116:119]\n\t"
"s_mov_b32 s20, 1044480\n\t"
"buffer_load_dwordx4 a[228:231], v252, s[8:11], s20 offen offset:0 sc1\n\t"
"v_mfma_f32_16x16x32_bf16 a[124:127], a[212:215], a[176:179], a[124:127]\n\t"
"ds_read_b128 a[176:179], v244 offset:1024\n\t"
"v_mfma_f32_16x16x32_bf16 a[124:127], a[232:235], a[180:183], a[124:127]\n\t"
"v_mfma_f32_16x16x32_bf16 a[124:127], a[236:239], a[184:187], a[124:127]\n\t"
"v_mfma_f32_16x16x32_bf16 a[132:135], a[212:215], a[180:183], a[132:135]\n\t"
"ds_read_b128 a[180:183], v244 offset:1536\n\t"
"v_mfma_f32_16x16x32_bf16 a[132:135], a[232:235], a[184:187], a[132:135]\n\t"
"v_mfma_f32_16x16x32_bf16 a[132:135], a[236:239], a[188:191], a[132:135]\n\t"
"v_mfma_f32_16x16x32_bf16 a[140:143], a[212:215], a[184:187], a[140:143]\n\t"
"ds_read_b128 a[184:187], v244 offset:2048\n\t"
"v_mfma_f32_16x16x32_bf16 a[140:143], a[232:235], a[188:191], a[140:143]\n\t"
"v_mfma_f32_16x16x32_bf16 a[140:143], a[236:239], a[192:195], a[140:143]\n\t"
"v_mfma_f32_16x16x32_bf16 a[148:151], a[212:215], a[188:191], a[148:151]\n\t"
"ds_read_b128 a[188:191], v244 offset:2560\n\t"
"v_mfma_f32_16x16x32_bf16 a[148:151], a[232:235], a[192:195], a[148:151]\n\t"
"v_mfma_f32_16x16x32_bf16 a[148:151], a[236:239], a[196:199], a[148:151]\n\t"
"v_mfma_f32_16x16x32_bf16 a[156:159], a[212:215], a[192:195], a[156:159]\n\t"
"ds_read_b128 a[192:195], v244 offset:3072\n\t"
"v_mfma_f32_16x16x32_bf16 a[156:159], a[232:235], a[196:199], a[156:159]\n\t"
"v_mfma_f32_16x16x32_bf16 a[156:159], a[236:239], a[200:203], a[156:159]\n\t"
"v_mfma_f32_16x16x32_bf16 a[164:167], a[212:215], a[196:199], a[164:167]\n\t"
"ds_read_b128 a[196:199], v244 offset:3584\n\t"
"v_mfma_f32_16x16x32_bf16 a[164:167], a[232:235], a[200:203], a[164:167]\n\t"
"ds_read_b128 a[200:203], v244 offset:4096\n\t"
"v_mfma_f32_16x16x32_bf16 a[164:167], a[236:239], a[204:207], a[164:167]\n\t"
"buffer_load_dwordx4 a[212:215], v252, s[8:11], s20 offen offset:1024 sc1\n\t"
"Lst28_%=:\n\t"
"s_waitcnt vmcnt(11) lgkmcnt(6)\n\t"
"v_mfma_f32_16x16x32_bf16 v[196:199], a[240:243], a[168:171], v[196:199]\n\t"
"ds_read_b128 a[204:207], v244 offset:4608\n\t"
"v_xor_b32 v246, 0xc0, v246\n\t"
"v_mfma_f32_16x16x32_bf16 v[196:199], a[244:247], a[172:175], v[196:199]\n\t"
"v_mfma_f32_16x16x32_bf16 v[196:199], a[216:219], a[176:179], v[196:199]\n\t"
"s_waitcnt lgkmcnt(6)\n\t"
"v_mfma_f32_16x16x32_bf16 v[204:207], a[240:243], a[172:175], v[204:207]\n\t"
"v_mfma_f32_16x16x32_bf16 v[204:207], a[244:247], a[176:179], v[204:207]\n\t"
"v_mfma_f32_16x16x32_bf16 v[204:207], a[216:219], a[180:183], v[204:207]\n\t"
"buffer_load_dwordx4 a[232:235], v252, s[8:11], s20 offen offset:2048 sc1\n\t"
"s_waitcnt lgkmcnt(5)\n\t"
"v_mfma_f32_16x16x32_bf16 v[212:215], a[240:243], a[176:179], v[212:215]\n\t"
"v_mfma_f32_16x16x32_bf16 v[212:215], a[244:247], a[180:183], v[212:215]\n\t"
"v_mfma_f32_16x16x32_bf16 v[212:215], a[216:219], a[184:187], v[212:215]\n\t"
"s_mov_b32 s20, 1056768\n\t"
"buffer_load_dwordx4 a[236:239], v252, s[8:11], s20 offen offset:0 sc1\n\t"
"s_waitcnt lgkmcnt(4)\n\t"
"v_mfma_f32_16x16x32_bf16 a[0:3], a[240:243], a[180:183], a[0:3]\n\t"
"v_mfma_f32_16x16x32_bf16 a[0:3], a[244:247], a[184:187], a[0:3]\n\t"
"v_mfma_f32_16x16x32_bf16 a[0:3], a[216:219], a[188:191], a[0:3]\n\t"
"s_waitcnt lgkmcnt(3)\n\t"
"v_mfma_f32_16x16x32_bf16 a[8:11], a[240:243], a[184:187], a[8:11]\n\t"
"v_mfma_f32_16x16x32_bf16 a[8:11], a[244:247], a[188:191], a[8:11]\n\t"
"v_mfma_f32_16x16x32_bf16 a[8:11], a[216:219], a[192:195], a[8:11]\n\t"
"ds_read_b64_tr_b16 v[224:225], v246 offset:512\n\t"
"ds_read_b64_tr_b16 v[226:227], v246 offset:3584\n\t"
"s_waitcnt lgkmcnt(4)\n\t"
"v_mfma_f32_16x16x32_bf16 a[16:19], a[240:243], a[188:191], a[16:19]\n\t"
"v_mfma_f32_16x16x32_bf16 a[16:19], a[244:247], a[192:195], a[16:19]\n\t"
"v_mfma_f32_16x16x32_bf16 a[16:19], a[216:219], a[196:199], a[16:19]\n\t"
"s_waitcnt lgkmcnt(3)\n\t"
"v_mfma_f32_16x16x32_bf16 a[24:27], a[240:243], a[192:195], a[24:27]\n\t"
"v_mfma_f32_16x16x32_bf16 a[24:27], a[244:247], a[196:199], a[24:27]\n\t"
"v_mfma_f32_16x16x32_bf16 a[24:27], a[216:219], a[200:203], a[24:27]\n\t"
"s_waitcnt lgkmcnt(2)\n\t"
"v_mfma_f32_16x16x32_bf16 a[32:35], a[240:243], a[196:199], a[32:35]\n\t"
"ds_read_b64_tr_b16 v[228:229], v246 offset:6656\n\t"
"v_mfma_f32_16x16x32_bf16 a[32:35], a[244:247], a[200:203], a[32:35]\n\t"
"ds_read_b64_tr_b16 v[230:231], v246 offset:9728\n\t"
"v_mfma_f32_16x16x32_bf16 a[32:35], a[216:219], a[204:207], a[32:35]\n\t"
"buffer_load_dwordx4 a[240:243], v252, s[8:11], s20 offen offset:1024 sc1\n\t"
"s_waitcnt vmcnt(11)\n\t"
"v_mfma_f32_16x16x32_bf16 a[40:43], v[96:99], a[168:171], a[40:43]\n\t"
"v_mfma_f32_16x16x32_bf16 a[40:43], a[220:223], a[172:175], a[40:43]\n\t"
"v_mfma_f32_16x16x32_bf16 a[40:43], a[248:251], a[176:179], a[40:43]\n\t"
"buffer_load_dwordx4 a[216:219], v252, s[8:11], s20 offen offset:2048 sc1\n\t"
"v_mfma_f32_16x16x32_bf16 a[48:51], v[96:99], a[172:175], a[48:51]\n\t"
"v_mfma_f32_16x16x32_bf16 a[48:51], a[220:223], a[176:179], a[48:51]\n\t"
"v_mfma_f32_16x16x32_bf16 a[48:51], a[248:251], a[180:183], a[48:51]\n\t"
"s_mov_b32 s20, 921600\n\t"
"buffer_load_dwordx4 a[244:247], v252, s[8:11], s20 offen offset:0 sc1\n\t"
"s_waitcnt lgkmcnt(2)\n\t"
"v_mfma_f32_16x16x32_bf16 v[224:227], v[224:227], v[220:223], 0\n\t"
"v_mfma_f32_16x16x32_bf16 a[56:59], v[96:99], a[176:179], a[56:59]\n\t"
"ds_read_b64_tr_b16 v[232:233], v246 offset:12800\n\t"
"v_mfma_f32_16x16x32_bf16 a[56:59], a[220:223], a[180:183], a[56:59]\n\t"
"ds_read_b64_tr_b16 v[234:235], v246 offset:15872\n\t"
"v_mfma_f32_16x16x32_bf16 a[56:59], a[248:251], a[184:187], a[56:59]\n\t"
"v_mfma_f32_16x16x32_bf16 a[64:67], v[96:99], a[180:183], a[64:67]\n\t"
"v_mfma_f32_16x16x32_bf16 a[64:67], a[220:223], a[184:187], a[64:67]\n\t"
"s_nop 0\n\t"
"v_cvt_pk_bf16_f32 v224, v224, v225\n\t"
"v_mfma_f32_16x16x32_bf16 a[64:67], a[248:251], a[188:191], a[64:67]\n\t"
"v_cvt_pk_bf16_f32 v225, v226, v227\n\t"
"s_waitcnt lgkmcnt(2)\n\t"
"v_mfma_f32_16x16x32_bf16 v[228:231], v[228:231], v[220:223], 0\n\t"
"v_mfma_f32_16x16x32_bf16 a[72:75], v[96:99], a[184:187], a[72:75]\n\t"
"v_mfma_f32_16x16x32_bf16 a[72:75], a[220:223], a[188:191], a[72:75]\n\t"
"v_mfma_f32_16x16x32_bf16 a[72:75], a[248:251], a[192:195], a[72:75]\n\t"
"v_mfma_f32_16x16x32_bf16 a[80:83], v[96:99], a[188:191], a[80:83]\n\t"
"v_mfma_f32_16x16x32_bf16 a[80:83], a[220:223], a[192:195], a[80:83]\n\t"
"s_nop 2\n\t"
"v_cvt_pk_bf16_f32 v226, v228, v229\n\t"
"v_cvt_pk_bf16_f32 v227, v230, v231\n\t"
"v_mfma_f32_16x16x32_bf16 a[80:83], a[248:251], a[196:199], a[80:83]\n\t"
"ds_read_b64_tr_b16 v[228:229], v246 offset:20480\n\t"
"v_mfma_f32_16x16x32_bf16 a[88:91], v[96:99], a[192:195], a[88:91]\n\t"
"ds_read_b64_tr_b16 v[230:231], v246 offset:23552\n\t"
"v_mfma_f32_16x16x32_bf16 a[88:91], a[220:223], a[196:199], a[88:91]\n\t"
"v_mfma_f32_16x16x32_bf16 a[88:91], a[248:251], a[200:203], a[88:91]\n\t"
"s_waitcnt lgkmcnt(2)\n\t"
"v_mfma_f32_16x16x32_bf16 v[232:235], v[232:235], v[220:223], 0\n\t"
"v_mfma_f32_16x16x32_bf16 a[96:99], v[96:99], a[196:199], a[96:99]\n\t"
"v_mfma_f32_16x16x32_bf16 a[96:99], a[220:223], a[200:203], a[96:99]\n\t"
"v_mfma_f32_16x16x32_bf16 a[96:99], a[248:251], a[204:207], a[96:99]\n\t"
"buffer_load_dwordx4 v[96:99], v252, s[8:11], s20 offen offset:1024 sc1\n\t"
"s_waitcnt vmcnt(11)\n\t"
"v_mfma_f32_16x16x32_bf16 a[104:107], a[252:255], a[168:171], a[104:107]\n\t"
"ds_read_b128 a[168:171], v244 offset:9216\n\t"
"v_mfma_f32_16x16x32_bf16 a[104:107], a[224:227], a[172:175], a[104:107]\n\t"
"v_cvt_pk_bf16_f32 v232, v232, v233\n\t"
"v_cvt_pk_bf16_f32 v233, v234, v235\n\t"
"v_mfma_f32_16x16x32_bf16 a[104:107], v[236:239], a[176:179], a[104:107]\n\t"
"v_mfma_f32_16x16x32_bf16 a[112:115], a[252:255], a[172:175], a[112:115]\n\t"
"ds_read_b128 a[172:175], v244 offset:9728\n\t"
"v_mfma_f32_16x16x32_bf16 a[112:115], a[224:227], a[176:179], a[112:115]\n\t"
"v_mfma_f32_16x16x32_bf16 a[112:115], v[236:239], a[180:183], a[112:115]\n\t"
"buffer_load_dwordx4 a[220:223], v252, s[8:11], s20 offen offset:2048 sc1\n\t"
"s_waitcnt lgkmcnt(2)\n\t"
"v_mfma_f32_16x16x32_bf16 v[228:231], v[228:231], v[220:223], 0\n\t"
"s_mov_b32 s20, 933888\n\t"
"buffer_load_dwordx4 a[248:251], v252, s[8:11], s20 offen offset:0 sc1\n\t"
"v_mfma_f32_16x16x32_bf16 a[120:123], a[252:255], a[176:179], a[120:123]\n\t"
"ds_read_b128 a[176:179], v244 offset:10240\n\t"
"v_mfma_f32_16x16x32_bf16 a[120:123], a[224:227], a[180:183], a[120:123]\n\t"
"v_mfma_f32_16x16x32_bf16 a[120:123], v[236:239], a[184:187], a[120:123]\n\t"
"v_mfma_f32_16x16x32_bf16 a[128:131], a[252:255], a[180:183], a[128:131]\n\t"
"ds_read_b128 a[180:183], v244 offset:10752\n\t"
"v_mfma_f32_16x16x32_bf16 a[128:131], a[224:227], a[184:187], a[128:131]\n\t"
"v_cvt_pk_bf16_f32 v234, v228, v229\n\t"
"v_cvt_pk_bf16_f32 v235, v230, v231\n\t"
"v_mfma_f32_16x16x32_bf16 a[128:131], v[236:239], a[188:191], a[128:131]\n\t"
"ds_read_b64_tr_b16 v[228:229], v246 offset:26624\n\t"
"v_mfma_f32_16x16x32_bf16 a[136:139], a[252:255], a[184:187], a[136:139]\n\t"
"ds_read_b64_tr_b16 v[230:231], v246 offset:29696\n\t"
"v_mfma_f32_16x16x32_bf16 a[136:139], a[224:227], a[188:191], a[136:139]\n\t"
"ds_read_b128 a[184:187], v244 offset:11264\n\t"
"v_mfma_f32_16x16x32_bf16 a[136:139], v[236:239], a[192:195], a[136:139]\n\t"
"v_mfma_f32_16x16x32_bf16 a[144:147], a[252:255], a[188:191], a[144:147]\n\t"
"ds_read_b128 a[188:191], v244 offset:11776\n\t"
"v_mfma_f32_16x16x32_bf16 a[144:147], a[224:227], a[192:195], a[144:147]\n\t"
"v_mfma_f32_16x16x32_bf16 a[144:147], v[236:239], a[196:199], a[144:147]\n\t"
"v_mfma_f32_16x16x32_bf16 a[152:155], a[252:255], a[192:195], a[152:155]\n\t"
"ds_read_b128 a[192:195], v244 offset:12288\n\t"
"v_mfma_f32_16x16x32_bf16 a[152:155], a[224:227], a[196:199], a[152:155]\n\t"
"v_mfma_f32_16x16x32_bf16 a[152:155], v[236:239], a[200:203], a[152:155]\n\t"
"v_mfma_f32_16x16x32_bf16 a[160:163], a[252:255], a[196:199], a[160:163]\n\t"
"ds_read_b128 a[196:199], v244 offset:12800\n\t"
"v_mfma_f32_16x16x32_bf16 a[160:163], a[224:227], a[200:203], a[160:163]\n\t"
"ds_read_b128 a[200:203], v244 offset:13312\n\t"
"v_mfma_f32_16x16x32_bf16 a[160:163], v[236:239], a[204:207], a[160:163]\n\t"
"buffer_load_dwordx4 a[252:255], v252, s[8:11], s20 offen offset:1024 sc1\n\t"
"s_waitcnt lgkmcnt(5)\n\t"
"v_mfma_f32_16x16x32_bf16 v[228:231], v[228:231], v[220:223], 0\n\t"
"ds_read_b128 a[204:207], v244 offset:13824\n\t"
"s_waitcnt vmcnt(11)\n\t"
"v_mfma_f32_16x16x32_bf16 v[200:203], v[240:243], a[168:171], v[200:203]\n\t"
"v_mfma_f32_16x16x32_bf16 v[200:203], v[248:251], a[172:175], v[200:203]\n\t"
"v_mfma_f32_16x16x32_bf16 v[200:203], a[208:211], a[176:179], v[200:203]\n\t"
"buffer_load_dwordx4 v[236:239], v252, s[8:11], s20 offen offset:2048 sc1\n\t"
"v_mfma_f32_16x16x32_bf16 v[208:211], v[240:243], a[172:175], v[208:211]\n\t"
"v_mfma_f32_16x16x32_bf16 v[208:211], v[248:251], a[176:179], v[208:211]\n\t"
"v_cvt_pk_bf16_f32 v228, v228, v229\n\t"
"v_cvt_pk_bf16_f32 v229, v230, v231\n\t"
"v_mfma_f32_16x16x32_bf16 v[208:211], a[208:211], a[180:183], v[208:211]\n\t"
"s_mov_b32 s20, 946176\n\t"
"buffer_load_dwordx4 a[224:227], v252, s[8:11], s20 offen offset:0 sc1\n\t"
"s_waitcnt lgkmcnt(0)\n\t"
"s_barrier\n\t"
"v_mfma_f32_16x16x32_bf16 v[216:219], v[240:243], a[176:179], v[216:219]\n\t"
"v_mfma_f32_16x16x32_bf16 v[216:219], v[248:251], a[180:183], v[216:219]\n\t"
"v_mfma_f32_16x16x32_bf16 v[216:219], a[208:211], a[184:187], v[216:219]\n\t"
"ds_write2st64_b64 v245, v[224:225], v[226:227] offset0:0 offset1:1\n\t"
"v_mfma_f32_16x16x32_bf16 a[4:7], v[240:243], a[180:183], a[4:7]\n\t"
"ds_read_b64_tr_b16 v[224:225], v246 offset:32768\n\t"
"v_mfma_f32_16x16x32_bf16 a[4:7], v[248:251], a[184:187], a[4:7]\n\t"
"ds_read_b64_tr_b16 v[226:227], v246 offset:35840\n\t"
"v_mfma_f32_16x16x32_bf16 a[4:7], a[208:211], a[188:191], a[4:7]\n\t"
"ds_write2st64_b64 v245, v[232:233], v[234:235] offset0:2 offset1:3\n\t"
"v_mfma_f32_16x16x32_bf16 a[12:15], v[240:243], a[184:187], a[12:15]\n\t"
"ds_read_b64_tr_b16 v[232:233], v246 offset:40448\n\t"
"v_mfma_f32_16x16x32_bf16 a[12:15], v[248:251], a[188:191], a[12:15]\n\t"
"ds_read_b64_tr_b16 v[234:235], v246 offset:43520\n\t"
"v_mfma_f32_16x16x32_bf16 a[12:15], a[208:211], a[192:195], a[12:15]\n\t"
"ds_write_b64 v245, v[228:229] offset:2048\n\t"
"v_mfma_f32_16x16x32_bf16 a[20:23], v[240:243], a[188:191], a[20:23]\n\t"
"ds_read_b64_tr_b16 v[228:229], v246 offset:46592\n\t"
"v_mfma_f32_16x16x32_bf16 a[20:23], v[248:251], a[192:195], a[20:23]\n\t"
"ds_read_b64_tr_b16 v[230:231], v246 offset:49664\n\t"
"v_mfma_f32_16x16x32_bf16 a[20:23], a[208:211], a[196:199], a[20:23]\n\t"
"s_waitcnt lgkmcnt(6)\n\t"
"v_mfma_f32_16x16x32_bf16 v[224:227], v[224:227], v[220:223], 0\n\t"
"v_mfma_f32_16x16x32_bf16 a[28:31], v[240:243], a[192:195], a[28:31]\n\t"
"v_mfma_f32_16x16x32_bf16 a[28:31], v[248:251], a[196:199], a[28:31]\n\t"
"v_mfma_f32_16x16x32_bf16 a[28:31], a[208:211], a[200:203], a[28:31]\n\t"
"s_waitcnt lgkmcnt(3)\n\t"
"v_mfma_f32_16x16x32_bf16 v[232:235], v[232:235], v[220:223], 0\n\t"
"v_mfma_f32_16x16x32_bf16 a[36:39], v[240:243], a[196:199], a[36:39]\n\t"
"s_nop 1\n\t"
"v_cvt_pk_bf16_f32 v224, v224, v225\n\t"
"v_cvt_pk_bf16_f32 v225, v226, v227\n\t"
"v_mfma_f32_16x16x32_bf16 a[36:39], v[248:251], a[200:203], a[36:39]\n\t"
"v_mfma_f32_16x16x32_bf16 a[36:39], a[208:211], a[204:207], a[36:39]\n\t"
"buffer_load_dwordx4 v[240:243], v252, s[8:11], s20 offen offset:1024 sc1\n\t"
"s_waitcnt lgkmcnt(0)\n\t"
"v_mfma_f32_16x16x32_bf16 v[228:231], v[228:231], v[220:223], 0\n\t"
"buffer_load_dwordx4 v[248:251], v252, s[8:11], s20 offen offset:2048 sc1\n\t"
"s_waitcnt vmcnt(12)\n\t"
"v_mfma_f32_16x16x32_bf16 a[44:47], a[228:231], a[168:171], a[44:47]\n\t"
"v_cvt_pk_bf16_f32 v226, v232, v233\n\t"
"v_cvt_pk_bf16_f32 v227, v234, v235\n\t"
"v_mfma_f32_16x16x32_bf16 a[44:47], a[212:215], a[172:175], a[44:47]\n\t"
"ds_read_b64_tr_b16 v[232:233], v246 offset:52736\n\t"
"v_mfma_f32_16x16x32_bf16 a[44:47], a[232:235], a[176:179], a[44:47]\n\t"
"s_mov_b32 s20, 958464\n\t"
"buffer_load_dwordx4 a[208:211], v252, s[8:11], s20 offen offset:0 sc1\n\t"
"v_mfma_f32_16x16x32_bf16 a[52:55], a[228:231], a[172:175], a[52:55]\n\t"
"ds_read_b64_tr_b16 v[234:235], v246 offset:55808\n\t"
"v_mfma_f32_16x16x32_bf16 a[52:55], a[212:215], a[176:179], a[52:55]\n\t"
"v_cvt_pk_bf16_f32 v228, v228, v229\n\t"
"v_cvt_pk_bf16_f32 v229, v230, v231\n\t"
"v_mfma_f32_16x16x32_bf16 a[52:55], a[232:235], a[180:183], a[52:55]\n\t"
"ds_write2st64_b64 v245, v[224:225], v[226:227] offset0:5 offset1:6\n\t"
"v_mfma_f32_16x16x32_bf16 a[60:63], a[228:231], a[176:179], a[60:63]\n\t"
"v_mfma_f32_16x16x32_bf16 a[60:63], a[212:215], a[180:183], a[60:63]\n\t"
"v_mfma_f32_16x16x32_bf16 a[60:63], a[232:235], a[184:187], a[60:63]\n\t"
"v_mfma_f32_16x16x32_bf16 a[68:71], a[228:231], a[180:183], a[68:71]\n\t"
"v_mfma_f32_16x16x32_bf16 a[68:71], a[212:215], a[184:187], a[68:71]\n\t"
"v_mfma_f32_16x16x32_bf16 a[68:71], a[232:235], a[188:191], a[68:71]\n\t"
"v_mfma_f32_16x16x32_bf16 a[76:79], a[228:231], a[184:187], a[76:79]\n\t"
"v_mfma_f32_16x16x32_bf16 a[76:79], a[212:215], a[188:191], a[76:79]\n\t"
"v_mfma_f32_16x16x32_bf16 a[76:79], a[232:235], a[192:195], a[76:79]\n\t"
"s_waitcnt lgkmcnt(1)\n\t"
"v_mfma_f32_16x16x32_bf16 v[232:235], v[232:235], v[220:223], 0\n\t"
"v_mfma_f32_16x16x32_bf16 a[84:87], a[228:231], a[188:191], a[84:87]\n\t"
"v_mfma_f32_16x16x32_bf16 a[84:87], a[212:215], a[192:195], a[84:87]\n\t"
"v_mfma_f32_16x16x32_bf16 a[84:87], a[232:235], a[196:199], a[84:87]\n\t"
"v_mfma_f32_16x16x32_bf16 a[92:95], a[228:231], a[192:195], a[92:95]\n\t"
"v_mfma_f32_16x16x32_bf16 a[92:95], a[212:215], a[196:199], a[92:95]\n\t"
"s_nop 2\n\t"
"v_cvt_pk_bf16_f32 v230, v232, v233\n\t"
"v_cvt_pk_bf16_f32 v231, v234, v235\n\t"
"v_mfma_f32_16x16x32_bf16 a[92:95], a[232:235], a[200:203], a[92:95]\n\t"
"ds_write2st64_b64 v245, v[228:229], v[230:231] offset0:7 offset1:8\n\t"
"v_mfma_f32_16x16x32_bf16 a[100:103], a[228:231], a[196:199], a[100:103]\n\t"
"v_mfma_f32_16x16x32_bf16 a[100:103], a[212:215], a[200:203], a[100:103]\n\t"
"v_mfma_f32_16x16x32_bf16 a[100:103], a[232:235], a[204:207], a[100:103]\n\t"
"buffer_load_dwordx4 a[228:231], v252, s[8:11], s20 offen offset:1024 sc1\n\t"
"s_waitcnt vmcnt(11)\n\t"
"v_mfma_f32_16x16x32_bf16 a[108:111], a[236:239], a[168:171], a[108:111]\n\t"
"v_mfma_f32_16x16x32_bf16 a[108:111], a[240:243], a[172:175], a[108:111]\n\t"
"v_mfma_f32_16x16x32_bf16 a[108:111], a[216:219], a[176:179], a[108:111]\n\t"
"buffer_load_dwordx4 a[212:215], v252, s[8:11], s20 offen offset:2048 sc1\n\t"
"s_waitcnt lgkmcnt(0)\n\t"
"s_barrier\n\t"
"v_mfma_f32_16x16x32_bf16 a[116:119], a[236:239], a[172:175], a[116:119]\n\t"
"ds_read_b128 a[168:171], v244 offset:0\n\t"
"v_mfma_f32_16x16x32_bf16 a[116:119], a[240:243], a[176:179], a[116:119]\n\t"
"ds_read_b128 a[172:175], v244 offset:512\n\t"
"v_mfma_f32_16x16x32_bf16 a[116:119], a[216:219], a[180:183], a[116:119]\n\t"
"s_mov_b32 s20, 970752\n\t"
"buffer_load_dwordx4 a[232:235], v252, s[8:11], s20 offen offset:0 sc1\n\t"
"v_mfma_f32_16x16x32_bf16 a[124:127], a[236:239], a[176:179], a[124:127]\n\t"
"ds_read_b128 a[176:179], v244 offset:1024\n\t"
"v_mfma_f32_16x16x32_bf16 a[124:127], a[240:243], a[180:183], a[124:127]\n\t"
"v_mfma_f32_16x16x32_bf16 a[124:127], a[216:219], a[184:187], a[124:127]\n\t"
"v_mfma_f32_16x16x32_bf16 a[132:135], a[236:239], a[180:183], a[132:135]\n\t"
"ds_read_b128 a[180:183], v244 offset:1536\n\t"
"v_mfma_f32_16x16x32_bf16 a[132:135], a[240:243], a[184:187], a[132:135]\n\t"
"v_mfma_f32_16x16x32_bf16 a[132:135], a[216:219], a[188:191], a[132:135]\n\t"
"v_mfma_f32_16x16x32_bf16 a[140:143], a[236:239], a[184:187], a[140:143]\n\t"
"ds_read_b128 a[184:187], v244 offset:2048\n\t"
"v_mfma_f32_16x16x32_bf16 a[140:143], a[240:243], a[188:191], a[140:143]\n\t"
"v_mfma_f32_16x16x32_bf16 a[140:143], a[216:219], a[192:195], a[140:143]\n\t"
"v_mfma_f32_16x16x32_bf16 a[148:151], a[236:239], a[188:191], a[148:151]\n\t"
"ds_read_b128 a[188:191], v244 offset:2560\n\t"
"v_mfma_f32_16x16x32_bf16 a[148:151], a[240:243], a[192:195], a[148:151]\n\t"
"v_mfma_f32_16x16x32_bf16 a[148:151], a[216:219], a[196:199], a[148:151]\n\t"
"v_mfma_f32_16x16x32_bf16 a[156:159], a[236:239], a[192:195], a[156:159]\n\t"
"ds_read_b128 a[192:195], v244 offset:3072\n\t"
"v_mfma_f32_16x16x32_bf16 a[156:159], a[240:243], a[196:199], a[156:159]\n\t"
"v_mfma_f32_16x16x32_bf16 a[156:159], a[216:219], a[200:203], a[156:159]\n\t"
"v_mfma_f32_16x16x32_bf16 a[164:167], a[236:239], a[196:199], a[164:167]\n\t"
"ds_read_b128 a[196:199], v244 offset:3584\n\t"
"v_mfma_f32_16x16x32_bf16 a[164:167], a[240:243], a[200:203], a[164:167]\n\t"
"ds_read_b128 a[200:203], v244 offset:4096\n\t"
"v_mfma_f32_16x16x32_bf16 a[164:167], a[216:219], a[204:207], a[164:167]\n\t"
"buffer_load_dwordx4 a[236:239], v252, s[8:11], s20 offen offset:1024 sc1\n\t"
"Lst29_%=:\n\t"
"s_waitcnt vmcnt(11) lgkmcnt(6)\n\t"
"v_mfma_f32_16x16x32_bf16 v[0:3], a[244:247], a[168:171], v[0:3]\n\t"
"ds_read_b128 a[204:207], v244 offset:4608\n\t"
"v_mfma_f32_16x16x32_bf16 v[0:3], v[96:99], a[172:175], v[0:3]\n\t"
"v_mfma_f32_16x16x32_bf16 v[0:3], a[220:223], a[176:179], v[0:3]\n\t"
"buffer_load_dwordx4 a[216:219], v252, s[8:11], s20 offen offset:2048 sc1\n\t"
"s_waitcnt lgkmcnt(6)\n\t"
"v_mfma_f32_16x16x32_bf16 v[4:7], a[244:247], a[172:175], v[4:7]\n\t"
"v_mfma_f32_16x16x32_bf16 v[4:7], v[96:99], a[176:179], v[4:7]\n\t"
"v_mfma_f32_16x16x32_bf16 v[4:7], a[220:223], a[180:183], v[4:7]\n\t"
"s_mov_b32 s20, 983040\n\t"
"buffer_load_dwordx4 a[240:243], v252, s[8:11], s20 offen offset:0 sc1\n\t"
"s_waitcnt lgkmcnt(5)\n\t"
"v_mfma_f32_16x16x32_bf16 v[8:11], a[244:247], a[176:179], v[8:11]\n\t"
"v_mfma_f32_16x16x32_bf16 v[8:11], v[96:99], a[180:183], v[8:11]\n\t"
"v_mfma_f32_16x16x32_bf16 v[8:11], a[220:223], a[184:187], v[8:11]\n\t"
"s_waitcnt lgkmcnt(4)\n\t"
"v_mfma_f32_16x16x32_bf16 v[12:15], a[244:247], a[180:183], v[12:15]\n\t"
"v_mfma_f32_16x16x32_bf16 v[12:15], v[96:99], a[184:187], v[12:15]\n\t"
"v_mfma_f32_16x16x32_bf16 v[12:15], a[220:223], a[188:191], v[12:15]\n\t"
"s_waitcnt lgkmcnt(3)\n\t"
"v_mfma_f32_16x16x32_bf16 v[16:19], a[244:247], a[184:187], v[16:19]\n\t"
"v_mfma_f32_16x16x32_bf16 v[16:19], v[96:99], a[188:191], v[16:19]\n\t"
"v_mfma_f32_16x16x32_bf16 v[16:19], a[220:223], a[192:195], v[16:19]\n\t"
"ds_read_b64_tr_b16 v[224:225], v246 offset:1024\n\t"
"ds_read_b64_tr_b16 v[226:227], v246 offset:4096\n\t"
"s_waitcnt lgkmcnt(4)\n\t"
"v_mfma_f32_16x16x32_bf16 v[20:23], a[244:247], a[188:191], v[20:23]\n\t"
"v_mfma_f32_16x16x32_bf16 v[20:23], v[96:99], a[192:195], v[20:23]\n\t"
"v_mfma_f32_16x16x32_bf16 v[20:23], a[220:223], a[196:199], v[20:23]\n\t"
"s_waitcnt lgkmcnt(3)\n\t"
"v_mfma_f32_16x16x32_bf16 v[24:27], a[244:247], a[192:195], v[24:27]\n\t"
"v_mfma_f32_16x16x32_bf16 v[24:27], v[96:99], a[196:199], v[24:27]\n\t"
"v_mfma_f32_16x16x32_bf16 v[24:27], a[220:223], a[200:203], v[24:27]\n\t"
"s_waitcnt lgkmcnt(2)\n\t"
"v_mfma_f32_16x16x32_bf16 v[28:31], a[244:247], a[196:199], v[28:31]\n\t"
"ds_read_b64_tr_b16 v[232:233], v246 offset:7168\n\t"
"v_mfma_f32_16x16x32_bf16 v[28:31], v[96:99], a[200:203], v[28:31]\n\t"
"ds_read_b64_tr_b16 v[234:235], v246 offset:10240\n\t"
"v_mfma_f32_16x16x32_bf16 v[28:31], a[220:223], a[204:207], v[28:31]\n\t"
"buffer_load_dwordx4 a[244:247], v252, s[8:11], s20 offen offset:1024 sc1\n\t"
"s_waitcnt vmcnt(11)\n\t"
"v_mfma_f32_16x16x32_bf16 v[32:35], a[248:251], a[168:171], v[32:35]\n\t"
"v_mfma_f32_16x16x32_bf16 v[32:35], a[252:255], a[172:175], v[32:35]\n\t"
"v_mfma_f32_16x16x32_bf16 v[32:35], v[236:239], a[176:179], v[32:35]\n\t"
"buffer_load_dwordx4 v[96:99], v252, s[8:11], s20 offen offset:2048 sc1\n\t"
"v_mfma_f32_16x16x32_bf16 v[36:39], a[248:251], a[172:175], v[36:39]\n\t"
"v_mfma_f32_16x16x32_bf16 v[36:39], a[252:255], a[176:179], v[36:39]\n\t"
"v_mfma_f32_16x16x32_bf16 v[36:39], v[236:239], a[180:183], v[36:39]\n\t"
"s_mov_b32 s20, 1142784\n\t"
"buffer_load_dwordx4 a[220:223], v252, s[8:11], s20 offen offset:0 sc1\n\t"
"s_waitcnt lgkmcnt(2)\n\t"
"v_mfma_f32_16x16x32_bf16 v[224:227], v[224:227], v[220:223], 0\n\t"
"v_mfma_f32_16x16x32_bf16 v[40:43], a[248:251], a[176:179], v[40:43]\n\t"
"ds_read_b64_tr_b16 v[228:229], v246 offset:13312\n\t"
"v_mfma_f32_16x16x32_bf16 v[40:43], a[252:255], a[180:183], v[40:43]\n\t"
"ds_read_b64_tr_b16 v[230:231], v246 offset:16384\n\t"
"v_mfma_f32_16x16x32_bf16 v[40:43], v[236:239], a[184:187], v[40:43]\n\t"
"v_mfma_f32_16x16x32_bf16 v[44:47], a[248:251], a[180:183], v[44:47]\n\t"
"v_mfma_f32_16x16x32_bf16 v[44:47], a[252:255], a[184:187], v[44:47]\n\t"
"s_nop 0\n\t"
"v_cvt_pk_bf16_f32 v224, v224, v225\n\t"
"v_mfma_f32_16x16x32_bf16 v[44:47], v[236:239], a[188:191], v[44:47]\n\t"
"v_cvt_pk_bf16_f32 v225, v226, v227\n\t"
"s_waitcnt lgkmcnt(2)\n\t"
"v_mfma_f32_16x16x32_bf16 v[232:235], v[232:235], v[220:223], 0\n\t"
"v_mfma_f32_16x16x32_bf16 v[48:51], a[248:251], a[184:187], v[48:51]\n\t"
"v_mfma_f32_16x16x32_bf16 v[48:51], a[252:255], a[188:191], v[48:51]\n\t"
"v_mfma_f32_16x16x32_bf16 v[48:51], v[236:239], a[192:195], v[48:51]\n\t"
"v_mfma_f32_16x16x32_bf16 v[52:55], a[248:251], a[188:191], v[52:55]\n\t"
"v_mfma_f32_16x16x32_bf16 v[52:55], a[252:255], a[192:195], v[52:55]\n\t"
"s_nop 2\n\t"
"v_cvt_pk_bf16_f32 v226, v232, v233\n\t"
"v_cvt_pk_bf16_f32 v227, v234, v235\n\t"
"v_mfma_f32_16x16x32_bf16 v[52:55], v[236:239], a[196:199], v[52:55]\n\t"
"ds_read_b64_tr_b16 v[232:233], v246 offset:20992\n\t"
"v_mfma_f32_16x16x32_bf16 v[56:59], a[248:251], a[192:195], v[56:59]\n\t"
"ds_read_b64_tr_b16 v[234:235], v246 offset:24064\n\t"
"v_mfma_f32_16x16x32_bf16 v[56:59], a[252:255], a[196:199], v[56:59]\n\t"
"v_mfma_f32_16x16x32_bf16 v[56:59], v[236:239], a[200:203], v[56:59]\n\t"
"s_waitcnt lgkmcnt(2)\n\t"
"v_mfma_f32_16x16x32_bf16 v[228:231], v[228:231], v[220:223], 0\n\t"
"v_mfma_f32_16x16x32_bf16 v[60:63], a[248:251], a[196:199], v[60:63]\n\t"
"v_mfma_f32_16x16x32_bf16 v[60:63], a[252:255], a[200:203], v[60:63]\n\t"
"v_mfma_f32_16x16x32_bf16 v[60:63], v[236:239], a[204:207], v[60:63]\n\t"
"buffer_load_dwordx4 a[248:251], v252, s[8:11], s20 offen offset:1024 sc1\n\t"
"s_waitcnt vmcnt(11)\n\t"
"v_mfma_f32_16x16x32_bf16 v[64:67], a[224:227], a[168:171], v[64:67]\n\t"
"ds_read_b128 a[168:171], v244 offset:9216\n\t"
"v_mfma_f32_16x16x32_bf16 v[64:67], v[240:243], a[172:175], v[64:67]\n\t"
"v_cvt_pk_bf16_f32 v228, v228, v229\n\t"
"v_cvt_pk_bf16_f32 v229, v230, v231\n\t"
"v_mfma_f32_16x16x32_bf16 v[64:67], v[248:251], a[176:179], v[64:67]\n\t"
"v_mfma_f32_16x16x32_bf16 v[68:71], a[224:227], a[172:175], v[68:71]\n\t"
"ds_read_b128 a[172:175], v244 offset:9728\n\t"
"v_mfma_f32_16x16x32_bf16 v[68:71], v[240:243], a[176:179], v[68:71]\n\t"
"v_mfma_f32_16x16x32_bf16 v[68:71], v[248:251], a[180:183], v[68:71]\n\t"
"buffer_load_dwordx4 v[236:239], v252, s[8:11], s20 offen offset:2048 sc1\n\t"
"s_waitcnt lgkmcnt(2)\n\t"
"v_mfma_f32_16x16x32_bf16 v[232:235], v[232:235], v[220:223], 0\n\t"
"s_mov_b32 s20, 1155072\n\t"
"buffer_load_dwordx4 a[252:255], v252, s[8:11], s20 offen offset:0 sc1\n\t"
"v_mfma_f32_16x16x32_bf16 v[72:75], a[224:227], a[176:179], v[72:75]\n\t"
"ds_read_b128 a[176:179], v244 offset:10240\n\t"
"v_mfma_f32_16x16x32_bf16 v[72:75], v[240:243], a[180:183], v[72:75]\n\t"
"v_mfma_f32_16x16x32_bf16 v[72:75], v[248:251], a[184:187], v[72:75]\n\t"
"v_mfma_f32_16x16x32_bf16 v[76:79], a[224:227], a[180:183], v[76:79]\n\t"
"ds_read_b128 a[180:183], v244 offset:10752\n\t"
"v_mfma_f32_16x16x32_bf16 v[76:79], v[240:243], a[184:187], v[76:79]\n\t"
"v_cvt_pk_bf16_f32 v230, v232, v233\n\t"
"v_cvt_pk_bf16_f32 v231, v234, v235\n\t"
"v_mfma_f32_16x16x32_bf16 v[76:79], v[248:251], a[188:191], v[76:79]\n\t"
"ds_read_b64_tr_b16 v[232:233], v246 offset:27136\n\t"
"v_mfma_f32_16x16x32_bf16 v[80:83], a[224:227], a[184:187], v[80:83]\n\t"
"ds_read_b64_tr_b16 v[234:235], v246 offset:30208\n\t"
"v_mfma_f32_16x16x32_bf16 v[80:83], v[240:243], a[188:191], v[80:83]\n\t"
"ds_read_b128 a[184:187], v244 offset:11264\n\t"
"v_mfma_f32_16x16x32_bf16 v[80:83], v[248:251], a[192:195], v[80:83]\n\t"
"v_mfma_f32_16x16x32_bf16 v[84:87], a[224:227], a[188:191], v[84:87]\n\t"
"ds_read_b128 a[188:191], v244 offset:11776\n\t"
"v_mfma_f32_16x16x32_bf16 v[84:87], v[240:243], a[192:195], v[84:87]\n\t"
"v_mfma_f32_16x16x32_bf16 v[84:87], v[248:251], a[196:199], v[84:87]\n\t"
"v_mfma_f32_16x16x32_bf16 v[88:91], a[224:227], a[192:195], v[88:91]\n\t"
"ds_read_b128 a[192:195], v244 offset:12288\n\t"
"v_mfma_f32_16x16x32_bf16 v[88:91], v[240:243], a[196:199], v[88:91]\n\t"
"v_mfma_f32_16x16x32_bf16 v[88:91], v[248:251], a[200:203], v[88:91]\n\t"
"v_mfma_f32_16x16x32_bf16 v[92:95], a[224:227], a[196:199], v[92:95]\n\t"
"ds_read_b128 a[196:199], v244 offset:12800\n\t"
"v_mfma_f32_16x16x32_bf16 v[92:95], v[240:243], a[200:203], v[92:95]\n\t"
"ds_read_b128 a[200:203], v244 offset:13312\n\t"
"v_mfma_f32_16x16x32_bf16 v[92:95], v[248:251], a[204:207], v[92:95]\n\t"
"buffer_load_dwordx4 a[224:227], v252, s[8:11], s20 offen offset:1024 sc1\n\t"
"s_waitcnt lgkmcnt(5)\n\t"
"v_mfma_f32_16x16x32_bf16 v[232:235], v[232:235], v[220:223], 0\n\t"
"ds_read_b128 a[204:207], v244 offset:13824\n\t"
"s_waitcnt vmcnt(11)\n\t"
"v_mfma_f32_16x16x32_bf16 v[100:103], a[208:211], a[168:171], v[100:103]\n\t"
"v_mfma_f32_16x16x32_bf16 v[100:103], a[228:231], a[172:175], v[100:103]\n\t"
"v_mfma_f32_16x16x32_bf16 v[100:103], a[212:215], a[176:179], v[100:103]\n\t"
"buffer_load_dwordx4 v[240:243], v252, s[8:11], s20 offen offset:2048 sc1\n\t"
"v_mfma_f32_16x16x32_bf16 v[104:107], a[208:211], a[172:175], v[104:107]\n\t"
"v_mfma_f32_16x16x32_bf16 v[104:107], a[228:231], a[176:179], v[104:107]\n\t"
"v_cvt_pk_bf16_f32 v232, v232, v233\n\t"
"v_cvt_pk_bf16_f32 v233, v234, v235\n\t"
"v_mfma_f32_16x16x32_bf16 v[104:107], a[212:215], a[180:183], v[104:107]\n\t"
"s_mov_b32 s20, 1167360\n\t"
"buffer_load_dwordx4 v[248:251], v252, s[8:11], s20 offen offset:0 sc1\n\t"
"s_waitcnt lgkmcnt(0)\n\t"
"s_barrier\n\t"
"v_mfma_f32_16x16x32_bf16 v[108:111], a[208:211], a[176:179], v[108:111]\n\t"
"v_mfma_f32_16x16x32_bf16 v[108:111], a[228:231], a[180:183], v[108:111]\n\t"
"v_mfma_f32_16x16x32_bf16 v[108:111], a[212:215], a[184:187], v[108:111]\n\t"
"ds_write2st64_b64 v245, v[224:225], v[226:227] offset0:0 offset1:1\n\t"
"v_mfma_f32_16x16x32_bf16 v[112:115], a[208:211], a[180:183], v[112:115]\n\t"
"ds_read_b64_tr_b16 v[224:225], v246 offset:33280\n\t"
"v_mfma_f32_16x16x32_bf16 v[112:115], a[228:231], a[184:187], v[112:115]\n\t"
"ds_read_b64_tr_b16 v[226:227], v246 offset:36352\n\t"
"v_mfma_f32_16x16x32_bf16 v[112:115], a[212:215], a[188:191], v[112:115]\n\t"
"ds_write2st64_b64 v245, v[228:229], v[230:231] offset0:2 offset1:3\n\t"
"v_mfma_f32_16x16x32_bf16 v[116:119], a[208:211], a[184:187], v[116:119]\n\t"
"ds_read_b64_tr_b16 v[228:229], v246 offset:40960\n\t"
"v_mfma_f32_16x16x32_bf16 v[116:119], a[228:231], a[188:191], v[116:119]\n\t"
"ds_read_b64_tr_b16 v[230:231], v246 offset:44032\n\t"
"v_mfma_f32_16x16x32_bf16 v[116:119], a[212:215], a[192:195], v[116:119]\n\t"
"ds_write_b64 v245, v[232:233] offset:2048\n\t"
"v_mfma_f32_16x16x32_bf16 v[120:123], a[208:211], a[188:191], v[120:123]\n\t"
"ds_read_b64_tr_b16 v[232:233], v246 offset:47104\n\t"
"v_mfma_f32_16x16x32_bf16 v[120:123], a[228:231], a[192:195], v[120:123]\n\t"
"ds_read_b64_tr_b16 v[234:235], v246 offset:50176\n\t"
"v_mfma_f32_16x16x32_bf16 v[120:123], a[212:215], a[196:199], v[120:123]\n\t"
"s_waitcnt lgkmcnt(6)\n\t"
"v_mfma_f32_16x16x32_bf16 v[224:227], v[224:227], v[220:223], 0\n\t"
"v_mfma_f32_16x16x32_bf16 v[124:127], a[208:211], a[192:195], v[124:127]\n\t"
"v_mfma_f32_16x16x32_bf16 v[124:127], a[228:231], a[196:199], v[124:127]\n\t"
"v_mfma_f32_16x16x32_bf16 v[124:127], a[212:215], a[200:203], v[124:127]\n\t"
"s_waitcnt lgkmcnt(3)\n\t"
"v_mfma_f32_16x16x32_bf16 v[228:231], v[228:231], v[220:223], 0\n\t"
"v_mfma_f32_16x16x32_bf16 v[128:131], a[208:211], a[196:199], v[128:131]\n\t"
"s_nop 1\n\t"
"v_cvt_pk_bf16_f32 v224, v224, v225\n\t"
"v_cvt_pk_bf16_f32 v225, v226, v227\n\t"
"v_mfma_f32_16x16x32_bf16 v[128:131], a[228:231], a[200:203], v[128:131]\n\t"
"v_mfma_f32_16x16x32_bf16 v[128:131], a[212:215], a[204:207], v[128:131]\n\t"
"buffer_load_dwordx4 a[208:211], v252, s[8:11], s20 offen offset:1024 sc1\n\t"
"s_waitcnt lgkmcnt(0)\n\t"
"v_mfma_f32_16x16x32_bf16 v[232:235], v[232:235], v[220:223], 0\n\t"
"buffer_load_dwordx4 a[212:215], v252, s[8:11], s20 offen offset:2048 sc1\n\t"
"s_waitcnt vmcnt(12)\n\t"
"v_mfma_f32_16x16x32_bf16 v[132:135], a[232:235], a[168:171], v[132:135]\n\t"
"v_cvt_pk_bf16_f32 v226, v228, v229\n\t"
"v_cvt_pk_bf16_f32 v227, v230, v231\n\t"
"v_mfma_f32_16x16x32_bf16 v[132:135], a[236:239], a[172:175], v[132:135]\n\t"
"ds_read_b64_tr_b16 v[228:229], v246 offset:53248\n\t"
"v_mfma_f32_16x16x32_bf16 v[132:135], a[216:219], a[176:179], v[132:135]\n\t"
"s_mov_b32 s20, 1179648\n\t"
"buffer_load_dwordx4 a[228:231], v252, s[8:11], s20 offen offset:0 sc1\n\t"
"v_mfma_f32_16x16x32_bf16 v[136:139], a[232:235], a[172:175], v[136:139]\n\t"
"ds_read_b64_tr_b16 v[230:231], v246 offset:56320\n\t"
"v_mfma_f32_16x16x32_bf16 v[136:139], a[236:239], a[176:179], v[136:139]\n\t"
"v_cvt_pk_bf16_f32 v232, v232, v233\n\t"
"v_cvt_pk_bf16_f32 v233, v234, v235\n\t"
"v_mfma_f32_16x16x32_bf16 v[136:139], a[216:219], a[180:183], v[136:139]\n\t"
"ds_write2st64_b64 v245, v[224:225], v[226:227] offset0:5 offset1:6\n\t"
"v_mfma_f32_16x16x32_bf16 v[140:143], a[232:235], a[176:179], v[140:143]\n\t"
"v_mfma_f32_16x16x32_bf16 v[140:143], a[236:239], a[180:183], v[140:143]\n\t"
"v_mfma_f32_16x16x32_bf16 v[140:143], a[216:219], a[184:187], v[140:143]\n\t"
"v_mfma_f32_16x16x32_bf16 v[144:147], a[232:235], a[180:183], v[144:147]\n\t"
"v_mfma_f32_16x16x32_bf16 v[144:147], a[236:239], a[184:187], v[144:147]\n\t"
"v_mfma_f32_16x16x32_bf16 v[144:147], a[216:219], a[188:191], v[144:147]\n\t"
"v_mfma_f32_16x16x32_bf16 v[148:151], a[232:235], a[184:187], v[148:151]\n\t"
"v_mfma_f32_16x16x32_bf16 v[148:151], a[236:239], a[188:191], v[148:151]\n\t"
"v_mfma_f32_16x16x32_bf16 v[148:151], a[216:219], a[192:195], v[148:151]\n\t"
"s_waitcnt lgkmcnt(1)\n\t"
"v_mfma_f32_16x16x32_bf16 v[228:231], v[228:231], v[220:223], 0\n\t"
"v_mfma_f32_16x16x32_bf16 v[152:155], a[232:235], a[188:191], v[152:155]\n\t"
"v_mfma_f32_16x16x32_bf16 v[152:155], a[236:239], a[192:195], v[152:155]\n\t"
"v_mfma_f32_16x16x32_bf16 v[152:155], a[216:219], a[196:199], v[152:155]\n\t"
"v_mfma_f32_16x16x32_bf16 v[156:159], a[232:235], a[192:195], v[156:159]\n\t"
"v_mfma_f32_16x16x32_bf16 v[156:159], a[236:239], a[196:199], v[156:159]\n\t"
"s_nop 2\n\t"
"v_cvt_pk_bf16_f32 v234, v228, v229\n\t"
"v_cvt_pk_bf16_f32 v235, v230, v231\n\t"
"v_mfma_f32_16x16x32_bf16 v[156:159], a[216:219], a[200:203], v[156:159]\n\t"
"ds_write2st64_b64 v245, v[232:233], v[234:235] offset0:7 offset1:8\n\t"
"v_mfma_f32_16x16x32_bf16 v[160:163], a[232:235], a[196:199], v[160:163]\n\t"
"v_mfma_f32_16x16x32_bf16 v[160:163], a[236:239], a[200:203], v[160:163]\n\t"
"v_mfma_f32_16x16x32_bf16 v[160:163], a[216:219], a[204:207], v[160:163]\n\t"
"buffer_load_dwordx4 a[232:235], v252, s[8:11], s20 offen offset:1024 sc1\n\t"
"s_waitcnt vmcnt(11)\n\t"
"v_mfma_f32_16x16x32_bf16 v[164:167], a[240:243], a[168:171], v[164:167]\n\t"
"v_mfma_f32_16x16x32_bf16 v[164:167], a[244:247], a[172:175], v[164:167]\n\t"
"v_mfma_f32_16x16x32_bf16 v[164:167], v[96:99], a[176:179], v[164:167]\n\t"
"buffer_load_dwordx4 a[216:219], v252, s[8:11], s20 offen offset:2048 sc1\n\t"
"s_waitcnt lgkmcnt(0)\n\t"
"s_barrier\n\t"
"v_mfma_f32_16x16x32_bf16 v[168:171], a[240:243], a[172:175], v[168:171]\n\t"
"ds_read_b128 a[168:171], v244 offset:0\n\t"
"v_mfma_f32_16x16x32_bf16 v[168:171], a[244:247], a[176:179], v[168:171]\n\t"
"ds_read_b128 a[172:175], v244 offset:512\n\t"
"v_mfma_f32_16x16x32_bf16 v[168:171], v[96:99], a[180:183], v[168:171]\n\t"
"s_mov_b32 s20, 1191936\n\t"
"buffer_load_dwordx4 a[236:239], v252, s[8:11], s20 offen offset:0 sc1\n\t"
"v_mfma_f32_16x16x32_bf16 v[172:175], a[240:243], a[176:179], v[172:175]\n\t"
"ds_read_b128 a[176:179], v244 offset:1024\n\t"
"v_mfma_f32_16x16x32_bf16 v[172:175], a[244:247], a[180:183], v[172:175]\n\t"
"v_mfma_f32_16x16x32_bf16 v[172:175], v[96:99], a[184:187], v[172:175]\n\t"
"v_mfma_f32_16x16x32_bf16 v[176:179], a[240:243], a[180:183], v[176:179]\n\t"
"ds_read_b128 a[180:183], v244 offset:1536\n\t"
"v_mfma_f32_16x16x32_bf16 v[176:179], a[244:247], a[184:187], v[176:179]\n\t"
"v_mfma_f32_16x16x32_bf16 v[176:179], v[96:99], a[188:191], v[176:179]\n\t"
"v_mfma_f32_16x16x32_bf16 v[180:183], a[240:243], a[184:187], v[180:183]\n\t"
"ds_read_b128 a[184:187], v244 offset:2048\n\t"
"v_mfma_f32_16x16x32_bf16 v[180:183], a[244:247], a[188:191], v[180:183]\n\t"
"v_mfma_f32_16x16x32_bf16 v[180:183], v[96:99], a[192:195], v[180:183]\n\t"
"v_mfma_f32_16x16x32_bf16 v[184:187], a[240:243], a[188:191], v[184:187]\n\t"
"ds_read_b128 a[188:191], v244 offset:2560\n\t"
"v_mfma_f32_16x16x32_bf16 v[184:187], a[244:247], a[192:195], v[184:187]\n\t"
"v_mfma_f32_16x16x32_bf16 v[184:187], v[96:99], a[196:199], v[184:187]\n\t"
"v_mfma_f32_16x16x32_bf16 v[188:191], a[240:243], a[192:195], v[188:191]\n\t"
"ds_read_b128 a[192:195], v244 offset:3072\n\t"
"v_mfma_f32_16x16x32_bf16 v[188:191], a[244:247], a[196:199], v[188:191]\n\t"
"v_mfma_f32_16x16x32_bf16 v[188:191], v[96:99], a[200:203], v[188:191]\n\t"
"v_mfma_f32_16x16x32_bf16 v[192:195], a[240:243], a[196:199], v[192:195]\n\t"
"ds_read_b128 a[196:199], v244 offset:3584\n\t"
"v_mfma_f32_16x16x32_bf16 v[192:195], a[244:247], a[200:203], v[192:195]\n\t"
"ds_read_b128 a[200:203], v244 offset:4096\n\t"
"v_mfma_f32_16x16x32_bf16 v[192:195], v[96:99], a[204:207], v[192:195]\n\t"
"buffer_load_dwordx4 a[240:243], v252, s[8:11], s20 offen offset:1024 sc1\n\t"
"Lst30_%=:\n\t"
"s_waitcnt vmcnt(11) lgkmcnt(6)\n\t"
"v_mfma_f32_16x16x32_bf16 v[0:3], a[220:223], a[168:171], v[0:3]\n\t"
"ds_read_b128 a[204:207], v244 offset:4608\n\t"
"v_xor_b32 v246, 0xc0, v246\n\t"
"v_mfma_f32_16x16x32_bf16 v[0:3], a[248:251], a[172:175], v[0:3]\n\t"
"v_mfma_f32_16x16x32_bf16 v[0:3], v[236:239], a[176:179], v[0:3]\n\t"
"s_waitcnt lgkmcnt(6)\n\t"
"v_mfma_f32_16x16x32_bf16 v[4:7], a[220:223], a[172:175], v[4:7]\n\t"
"v_mfma_f32_16x16x32_bf16 v[4:7], a[248:251], a[176:179], v[4:7]\n\t"
"v_mfma_f32_16x16x32_bf16 v[4:7], v[236:239], a[180:183], v[4:7]\n\t"
"buffer_load_dwordx4 v[96:99], v252, s[8:11], s20 offen offset:2048 sc1\n\t"
"s_waitcnt lgkmcnt(5)\n\t"
"v_mfma_f32_16x16x32_bf16 v[8:11], a[220:223], a[176:179], v[8:11]\n\t"
"v_mfma_f32_16x16x32_bf16 v[8:11], a[248:251], a[180:183], v[8:11]\n\t"
"v_mfma_f32_16x16x32_bf16 v[8:11], v[236:239], a[184:187], v[8:11]\n\t"
"s_mov_b32 s20, 1204224\n\t"
"buffer_load_dwordx4 a[244:247], v252, s[8:11], s20 offen offset:0 sc1\n\t"
"s_waitcnt lgkmcnt(4)\n\t"
"v_mfma_f32_16x16x32_bf16 v[12:15], a[220:223], a[180:183], v[12:15]\n\t"
"v_mfma_f32_16x16x32_bf16 v[12:15], a[248:251], a[184:187], v[12:15]\n\t"
"v_mfma_f32_16x16x32_bf16 v[12:15], v[236:239], a[188:191], v[12:15]\n\t"
"s_waitcnt lgkmcnt(3)\n\t"
"v_mfma_f32_16x16x32_bf16 v[16:19], a[220:223], a[184:187], v[16:19]\n\t"
"v_mfma_f32_16x16x32_bf16 v[16:19], a[248:251], a[188:191], v[16:19]\n\t"
"v_mfma_f32_16x16x32_bf16 v[16:19], v[236:239], a[192:195], v[16:19]\n\t"
"ds_read_b64_tr_b16 v[224:225], v246 offset:1024\n\t"
"ds_read_b64_tr_b16 v[226:227], v246 offset:4096\n\t"
"s_waitcnt lgkmcnt(4)\n\t"
"v_mfma_f32_16x16x32_bf16 v[20:23], a[220:223], a[188:191], v[20:23]\n\t"
"v_mfma_f32_16x16x32_bf16 v[20:23], a[248:251], a[192:195], v[20:23]\n\t"
"v_mfma_f32_16x16x32_bf16 v[20:23], v[236:239], a[196:199], v[20:23]\n\t"
"s_waitcnt lgkmcnt(3)\n\t"
"v_mfma_f32_16x16x32_bf16 v[24:27], a[220:223], a[192:195], v[24:27]\n\t"
"v_mfma_f32_16x16x32_bf16 v[24:27], a[248:251], a[196:199], v[24:27]\n\t"
"v_mfma_f32_16x16x32_bf16 v[24:27], v[236:239], a[200:203], v[24:27]\n\t"
"s_waitcnt lgkmcnt(2)\n\t"
"v_mfma_f32_16x16x32_bf16 v[28:31], a[220:223], a[196:199], v[28:31]\n\t"
"ds_read_b64_tr_b16 v[228:229], v246 offset:7168\n\t"
"v_mfma_f32_16x16x32_bf16 v[28:31], a[248:251], a[200:203], v[28:31]\n\t"
"ds_read_b64_tr_b16 v[230:231], v246 offset:10240\n\t"
"v_mfma_f32_16x16x32_bf16 v[28:31], v[236:239], a[204:207], v[28:31]\n\t"
"buffer_load_dwordx4 a[220:223], v252, s[8:11], s20 offen offset:1024 sc1\n\t"
"s_waitcnt vmcnt(11)\n\t"
"v_mfma_f32_16x16x32_bf16 v[32:35], a[252:255], a[168:171], v[32:35]\n\t"
"v_mfma_f32_16x16x32_bf16 v[32:35], a[224:227], a[172:175], v[32:35]\n\t"
"v_mfma_f32_16x16x32_bf16 v[32:35], v[240:243], a[176:179], v[32:35]\n\t"
"buffer_load_dwordx4 v[236:239], v252, s[8:11], s20 offen offset:2048 sc1\n\t"
"v_mfma_f32_16x16x32_bf16 v[36:39], a[252:255], a[172:175], v[36:39]\n\t"
"v_mfma_f32_16x16x32_bf16 v[36:39], a[224:227], a[176:179], v[36:39]\n\t"
"v_mfma_f32_16x16x32_bf16 v[36:39], v[240:243], a[180:183], v[36:39]\n\t"
"s_mov_b32 s20, 1216512\n\t"
"buffer_load_dwordx4 a[248:251], v252, s[8:11], s20 offen offset:0 sc1\n\t"
"s_waitcnt lgkmcnt(2)\n\t"
"v_mfma_f32_16x16x32_bf16 v[224:227], v[224:227], v[220:223], 0\n\t"
"v_mfma_f32_16x16x32_bf16 v[40:43], a[252:255], a[176:179], v[40:43]\n\t"
"ds_read_b64_tr_b16 v[232:233], v246 offset:13312\n\t"
"v_mfma_f32_16x16x32_bf16 v[40:43], a[224:227], a[180:183], v[40:43]\n\t"
"ds_read_b64_tr_b16 v[234:235], v246 offset:16384\n\t"
"v_mfma_f32_16x16x32_bf16 v[40:43], v[240:243], a[184:187], v[40:43]\n\t"
"v_mfma_f32_16x16x32_bf16 v[44:47], a[252:255], a[180:183], v[44:47]\n\t"
"v_mfma_f32_16x16x32_bf16 v[44:47], a[224:227], a[184:187], v[44:47]\n\t"
"s_nop 0\n\t"
"v_cvt_pk_bf16_f32 v224, v224, v225\n\t"
"v_mfma_f32_16x16x32_bf16 v[44:47], v[240:243], a[188:191], v[44:47]\n\t"
"v_cvt_pk_bf16_f32 v225, v226, v227\n\t"
"s_waitcnt lgkmcnt(2)\n\t"
"v_mfma_f32_16x16x32_bf16 v[228:231], v[228:231], v[220:223], 0\n\t"
"v_mfma_f32_16x16x32_bf16 v[48:51], a[252:255], a[184:187], v[48:51]\n\t"
"v_mfma_f32_16x16x32_bf16 v[48:51], a[224:227], a[188:191], v[48:51]\n\t"
"v_mfma_f32_16x16x32_bf16 v[48:51], v[240:243], a[192:195], v[48:51]\n\t"
"v_mfma_f32_16x16x32_bf16 v[52:55], a[252:255], a[188:191], v[52:55]\n\t"
"v_mfma_f32_16x16x32_bf16 v[52:55], a[224:227], a[192:195], v[52:55]\n\t"
"s_nop 2\n\t"
"v_cvt_pk_bf16_f32 v226, v228, v229\n\t"
"v_cvt_pk_bf16_f32 v227, v230, v231\n\t"
"v_mfma_f32_16x16x32_bf16 v[52:55], v[240:243], a[196:199], v[52:55]\n\t"
"ds_read_b64_tr_b16 v[228:229], v246 offset:20992\n\t"
"v_mfma_f32_16x16x32_bf16 v[56:59], a[252:255], a[192:195], v[56:59]\n\t"
"ds_read_b64_tr_b16 v[230:231], v246 offset:24064\n\t"
"v_mfma_f32_16x16x32_bf16 v[56:59], a[224:227], a[196:199], v[56:59]\n\t"
"v_mfma_f32_16x16x32_bf16 v[56:59], v[240:243], a[200:203], v[56:59]\n\t"
"s_waitcnt lgkmcnt(2)\n\t"
"v_mfma_f32_16x16x32_bf16 v[232:235], v[232:235], v[220:223], 0\n\t"
"v_mfma_f32_16x16x32_bf16 v[60:63], a[252:255], a[196:199], v[60:63]\n\t"
"v_mfma_f32_16x16x32_bf16 v[60:63], a[224:227], a[200:203], v[60:63]\n\t"
"v_mfma_f32_16x16x32_bf16 v[60:63], v[240:243], a[204:207], v[60:63]\n\t"
"buffer_load_dwordx4 a[252:255], v252, s[8:11], s20 offen offset:1024 sc1\n\t"
"s_waitcnt vmcnt(11)\n\t"
"v_mfma_f32_16x16x32_bf16 v[64:67], v[248:251], a[168:171], v[64:67]\n\t"
"ds_read_b128 a[168:171], v244 offset:9216\n\t"
"v_mfma_f32_16x16x32_bf16 v[64:67], a[208:211], a[172:175], v[64:67]\n\t"
"v_cvt_pk_bf16_f32 v232, v232, v233\n\t"
"v_cvt_pk_bf16_f32 v233, v234, v235\n\t"
"v_mfma_f32_16x16x32_bf16 v[64:67], a[212:215], a[176:179], v[64:67]\n\t"
"v_mfma_f32_16x16x32_bf16 v[68:71], v[248:251], a[172:175], v[68:71]\n\t"
"ds_read_b128 a[172:175], v244 offset:9728\n\t"
"v_mfma_f32_16x16x32_bf16 v[68:71], a[208:211], a[176:179], v[68:71]\n\t"
"v_mfma_f32_16x16x32_bf16 v[68:71], a[212:215], a[180:183], v[68:71]\n\t"
"buffer_load_dwordx4 v[240:243], v252, s[8:11], s20 offen offset:2048 sc1\n\t"
"s_waitcnt lgkmcnt(2)\n\t"
"v_mfma_f32_16x16x32_bf16 v[228:231], v[228:231], v[220:223], 0\n\t"
"s_mov_b32 s20, 1228800\n\t"
"buffer_load_dwordx4 a[224:227], v252, s[8:11], s20 offen offset:0 sc1\n\t"
"v_mfma_f32_16x16x32_bf16 v[72:75], v[248:251], a[176:179], v[72:75]\n\t"
"ds_read_b128 a[176:179], v244 offset:10240\n\t"
"v_mfma_f32_16x16x32_bf16 v[72:75], a[208:211], a[180:183], v[72:75]\n\t"
"v_mfma_f32_16x16x32_bf16 v[72:75], a[212:215], a[184:187], v[72:75]\n\t"
"v_mfma_f32_16x16x32_bf16 v[76:79], v[248:251], a[180:183], v[76:79]\n\t"
"ds_read_b128 a[180:183], v244 offset:10752\n\t"
"v_mfma_f32_16x16x32_bf16 v[76:79], a[208:211], a[184:187], v[76:79]\n\t"
"v_cvt_pk_bf16_f32 v234, v228, v229\n\t"
"v_cvt_pk_bf16_f32 v235, v230, v231\n\t"
"v_mfma_f32_16x16x32_bf16 v[76:79], a[212:215], a[188:191], v[76:79]\n\t"
"ds_read_b64_tr_b16 v[228:229], v246 offset:27136\n\t"
"v_mfma_f32_16x16x32_bf16 v[80:83], v[248:251], a[184:187], v[80:83]\n\t"
"ds_read_b64_tr_b16 v[230:231], v246 offset:30208\n\t"
"v_mfma_f32_16x16x32_bf16 v[80:83], a[208:211], a[188:191], v[80:83]\n\t"
"ds_read_b128 a[184:187], v244 offset:11264\n\t"
"v_mfma_f32_16x16x32_bf16 v[80:83], a[212:215], a[192:195], v[80:83]\n\t"
"v_mfma_f32_16x16x32_bf16 v[84:87], v[248:251], a[188:191], v[84:87]\n\t"
"ds_read_b128 a[188:191], v244 offset:11776\n\t"
"v_mfma_f32_16x16x32_bf16 v[84:87], a[208:211], a[192:195], v[84:87]\n\t"
"v_mfma_f32_16x16x32_bf16 v[84:87], a[212:215], a[196:199], v[84:87]\n\t"
"v_mfma_f32_16x16x32_bf16 v[88:91], v[248:251], a[192:195], v[88:91]\n\t"
"ds_read_b128 a[192:195], v244 offset:12288\n\t"
"v_mfma_f32_16x16x32_bf16 v[88:91], a[208:211], a[196:199], v[88:91]\n\t"
"v_mfma_f32_16x16x32_bf16 v[88:91], a[212:215], a[200:203], v[88:91]\n\t"
"v_mfma_f32_16x16x32_bf16 v[92:95], v[248:251], a[196:199], v[92:95]\n\t"
"ds_read_b128 a[196:199], v244 offset:12800\n\t"
"v_mfma_f32_16x16x32_bf16 v[92:95], a[208:211], a[200:203], v[92:95]\n\t"
"ds_read_b128 a[200:203], v244 offset:13312\n\t"
"v_mfma_f32_16x16x32_bf16 v[92:95], a[212:215], a[204:207], v[92:95]\n\t"
"buffer_load_dwordx4 v[248:251], v252, s[8:11], s20 offen offset:1024 sc1\n\t"
"s_waitcnt lgkmcnt(5)\n\t"
"v_mfma_f32_16x16x32_bf16 v[228:231], v[228:231], v[220:223], 0\n\t"
"ds_read_b128 a[204:207], v244 offset:13824\n\t"
"s_waitcnt vmcnt(11)\n\t"
"v_mfma_f32_16x16x32_bf16 v[100:103], a[228:231], a[168:171], v[100:103]\n\t"
"v_mfma_f32_16x16x32_bf16 v[100:103], a[232:235], a[172:175], v[100:103]\n\t"
"v_mfma_f32_16x16x32_bf16 v[100:103], a[216:219], a[176:179], v[100:103]\n\t"
"buffer_load_dwordx4 a[208:211], v252, s[8:11], s20 offen offset:2048 sc1\n\t"
"v_mfma_f32_16x16x32_bf16 v[104:107], a[228:231], a[172:175], v[104:107]\n\t"
"v_mfma_f32_16x16x32_bf16 v[104:107], a[232:235], a[176:179], v[104:107]\n\t"
"v_cvt_pk_bf16_f32 v228, v228, v229\n\t"
"v_cvt_pk_bf16_f32 v229, v230, v231\n\t"
"v_mfma_f32_16x16x32_bf16 v[104:107], a[216:219], a[180:183], v[104:107]\n\t"
"s_mov_b32 s20, 1241088\n\t"
"buffer_load_dwordx4 a[212:215], v252, s[8:11], s20 offen offset:0 sc1\n\t"
"s_waitcnt lgkmcnt(0)\n\t"
"s_barrier\n\t"
"v_mfma_f32_16x16x32_bf16 v[108:111], a[228:231], a[176:179], v[108:111]\n\t"
"v_fma_f32 v0, v100, s52, v0\n\t"
"v_fma_f32 v1, v101, s52, v1\n\t"
"v_mfma_f32_16x16x32_bf16 v[108:111], a[232:235], a[180:183], v[108:111]\n\t"
"v_fma_f32 v2, v102, s52, v2\n\t"
"v_fma_f32 v3, v103, s52, v3\n\t"
"v_mfma_f32_16x16x32_bf16 v[108:111], a[216:219], a[184:187], v[108:111]\n\t"
"ds_write2st64_b64 v245, v[224:225], v[226:227] offset0:0 offset1:1\n\t"
"v_mfma_f32_16x16x32_bf16 v[112:115], a[228:231], a[180:183], v[112:115]\n\t"
"ds_read_b64_tr_b16 v[224:225], v246 offset:33280\n\t"
"v_fma_f32 v100, v0, s54, -v100\n\t"
"v_mfma_f32_16x16x32_bf16 v[112:115], a[232:235], a[184:187], v[112:115]\n\t"
"ds_read_b64_tr_b16 v[226:227], v246 offset:36352\n\t"
"v_fma_f32 v101, v1, s54, -v101\n\t"
"v_mfma_f32_16x16x32_bf16 v[112:115], a[216:219], a[188:191], v[112:115]\n\t"
"ds_write2st64_b64 v245, v[232:233], v[234:235] offset0:2 offset1:3\n\t"
"v_mfma_f32_16x16x32_bf16 v[116:119], a[228:231], a[184:187], v[116:119]\n\t"
"ds_read_b64_tr_b16 v[232:233], v246 offset:40960\n\t"
"v_mfma_f32_16x16x32_bf16 v[116:119], a[232:235], a[188:191], v[116:119]\n\t"
"ds_read_b64_tr_b16 v[234:235], v246 offset:44032\n\t"
"v_mfma_f32_16x16x32_bf16 v[116:119], a[216:219], a[192:195], v[116:119]\n\t"
"ds_write_b64 v245, v[228:229] offset:2048\n\t"
"v_mfma_f32_16x16x32_bf16 v[120:123], a[228:231], a[188:191], v[120:123]\n\t"
"v_mfma_f32_16x16x32_bf16 v[120:123], a[232:235], a[192:195], v[120:123]\n\t"
"v_mfma_f32_16x16x32_bf16 v[120:123], a[216:219], a[196:199], v[120:123]\n\t"
"ds_read_b64_tr_b16 v[228:229], v246 offset:47104\n\t"
"s_waitcnt lgkmcnt(5)\n\t"
"v_mfma_f32_16x16x32_bf16 v[224:227], v[224:227], v[220:223], 0\n\t"
"ds_read_b64_tr_b16 v[230:231], v246 offset:50176\n\t"
"v_mfma_f32_16x16x32_bf16 v[124:127], a[228:231], a[192:195], v[124:127]\n\t"
"v_mfma_f32_16x16x32_bf16 v[124:127], a[232:235], a[196:199], v[124:127]\n\t"
"v_fma_f32 v102, v2, s54, -v102\n\t"
"v_mfma_f32_16x16x32_bf16 v[124:127], a[216:219], a[200:203], v[124:127]\n\t"
"s_waitcnt lgkmcnt(3)\n\t"
"v_mfma_f32_16x16x32_bf16 v[232:235], v[232:235], v[220:223], 0\n\t"
"v_fma_f32 v103, v3, s54, -v103\n\t"
"v_mfma_f32_16x16x32_bf16 v[128:131], a[228:231], a[196:199], v[128:131]\n\t"
"v_cvt_pk_bf16_f32 v224, v224, v225\n\t"
"v_cvt_pk_bf16_f32 v225, v226, v227\n\t"
"v_mfma_f32_16x16x32_bf16 v[128:131], a[232:235], a[200:203], v[128:131]\n\t"
"v_fma_f32 v4, v104, s52, v4\n\t"
"v_mfma_f32_16x16x32_bf16 v[128:131], a[216:219], a[204:207], v[128:131]\n\t"
"buffer_load_dwordx4 a[228:231], v252, s[8:11], s20 offen offset:1024 sc1\n\t"
"s_waitcnt lgkmcnt(0)\n\t"
"v_mfma_f32_16x16x32_bf16 v[228:231], v[228:231], v[220:223], 0\n\t"
"buffer_load_dwordx4 a[216:219], v252, s[8:11], s20 offen offset:2048 sc1\n\t"
"s_waitcnt vmcnt(12)\n\t"
"v_mfma_f32_16x16x32_bf16 v[132:135], a[236:239], a[168:171], v[132:135]\n\t"
"v_cvt_pk_bf16_f32 v226, v232, v233\n\t"
"v_cvt_pk_bf16_f32 v227, v234, v235\n\t"
"v_mfma_f32_16x16x32_bf16 v[132:135], a[240:243], a[172:175], v[132:135]\n\t"
"ds_read_b64_tr_b16 v[232:233], v246 offset:53248\n\t"
"v_fma_f32 v5, v105, s52, v5\n\t"
"v_mfma_f32_16x16x32_bf16 v[132:135], v[96:99], a[176:179], v[132:135]\n\t"
"s_mov_b32 s20, 1253376\n\t"
"buffer_load_dwordx4 a[232:235], v252, s[8:11], s20 offen offset:0 sc1\n\t"
"v_mfma_f32_16x16x32_bf16 v[136:139], a[236:239], a[172:175], v[136:139]\n\t"
"ds_read_b64_tr_b16 v[234:235], v246 offset:56320\n\t"
"v_fma_f32 v6, v106, s52, v6\n\t"
"v_mfma_f32_16x16x32_bf16 v[136:139], a[240:243], a[176:179], v[136:139]\n\t"
"v_cvt_pk_bf16_f32 v228, v228, v229\n\t"
"v_cvt_pk_bf16_f32 v229, v230, v231\n\t"
"v_mfma_f32_16x16x32_bf16 v[136:139], v[96:99], a[180:183], v[136:139]\n\t"
"ds_write2st64_b64 v245, v[224:225], v[226:227] offset0:5 offset1:6\n\t"
"v_mfma_f32_16x16x32_bf16 v[140:143], a[236:239], a[176:179], v[140:143]\n\t"
"v_fma_f32 v7, v107, s52, v7\n\t"
"v_fma_f32 v104, v4, s54, -v104\n\t"
"v_mfma_f32_16x16x32_bf16 v[140:143], a[240:243], a[180:183], v[140:143]\n\t"
"v_fma_f32 v105, v5, s54, -v105\n\t"
"v_fma_f32 v106, v6, s54, -v106\n\t"
"v_mfma_f32_16x16x32_bf16 v[140:143], v[96:99], a[184:187], v[140:143]\n\t"
"v_fma_f32 v107, v7, s54, -v107\n\t"
"v_fma_f32 v8, v108, s52, v8\n\t"
"v_mfma_f32_16x16x32_bf16 v[144:147], a[236:239], a[180:183], v[144:147]\n\t"
"v_fma_f32 v9, v109, s52, v9\n\t"
"v_fma_f32 v10, v110, s52, v10\n\t"
"v_mfma_f32_16x16x32_bf16 v[144:147], a[240:243], a[184:187], v[144:147]\n\t"
"v_fma_f32 v11, v111, s52, v11\n\t"
"v_fma_f32 v108, v8, s54, -v108\n\t"
"v_mfma_f32_16x16x32_bf16 v[144:147], v[96:99], a[188:191], v[144:147]\n\t"
"v_fma_f32 v109, v9, s54, -v109\n\t"
"v_mfma_f32_16x16x32_bf16 v[148:151], a[236:239], a[184:187], v[148:151]\n\t"
"v_fma_f32 v110, v10, s54, -v110\n\t"
"v_fma_f32 v111, v11, s54, -v111\n\t"
"v_mfma_f32_16x16x32_bf16 v[148:151], a[240:243], a[188:191], v[148:151]\n\t"
"v_fma_f32 v12, v112, s52, v12\n\t"
"v_mfma_f32_16x16x32_bf16 v[148:151], v[96:99], a[192:195], v[148:151]\n\t"
"v_fma_f32 v13, v113, s52, v13\n\t"
"s_waitcnt lgkmcnt(1)\n\t"
"v_mfma_f32_16x16x32_bf16 v[232:235], v[232:235], v[220:223], 0\n\t"
"v_fma_f32 v14, v114, s52, v14\n\t"
"v_fma_f32 v15, v115, s52, v15\n\t"
"v_mfma_f32_16x16x32_bf16 v[152:155], a[236:239], a[188:191], v[152:155]\n\t"
"v_fma_f32 v112, v12, s54, -v112\n\t"
"v_mfma_f32_16x16x32_bf16 v[152:155], a[240:243], a[192:195], v[152:155]\n\t"
"v_fma_f32 v113, v13, s54, -v113\n\t"
"v_mfma_f32_16x16x32_bf16 v[152:155], v[96:99], a[196:199], v[152:155]\n\t"
"v_fma_f32 v114, v14, s54, -v114\n\t"
"v_fma_f32 v115, v15, s54, -v115\n\t"
"v_mfma_f32_16x16x32_bf16 v[156:159], a[236:239], a[192:195], v[156:159]\n\t"
"v_fma_f32 v16, v116, s52, v16\n\t"
"v_fma_f32 v17, v117, s52, v17\n\t"
"v_mfma_f32_16x16x32_bf16 v[156:159], a[240:243], a[196:199], v[156:159]\n\t"
"v_cvt_pk_bf16_f32 v230, v232, v233\n\t"
"v_cvt_pk_bf16_f32 v231, v234, v235\n\t"
"v_mfma_f32_16x16x32_bf16 v[156:159], v[96:99], a[200:203], v[156:159]\n\t"
"ds_write2st64_b64 v245, v[228:229], v[230:231] offset0:7 offset1:8\n\t"
"v_mfma_f32_16x16x32_bf16 v[160:163], a[236:239], a[196:199], v[160:163]\n\t"
"v_fma_f32 v18, v118, s52, v18\n\t"
"v_fma_f32 v19, v119, s52, v19\n\t"
"v_mfma_f32_16x16x32_bf16 v[160:163], a[240:243], a[200:203], v[160:163]\n\t"
"v_fma_f32 v116, v16, s54, -v116\n\t"
"v_fma_f32 v117, v17, s54, -v117\n\t"
"v_mfma_f32_16x16x32_bf16 v[160:163], v[96:99], a[204:207], v[160:163]\n\t"
"buffer_load_dwordx4 a[236:239], v252, s[8:11], s20 offen offset:1024 sc1\n\t"
"s_waitcnt vmcnt(11)\n\t"
"v_mfma_f32_16x16x32_bf16 v[164:167], a[244:247], a[168:171], v[164:167]\n\t"
"v_fma_f32 v118, v18, s54, -v118\n\t"
"v_fma_f32 v119, v19, s54, -v119\n\t"
"v_mfma_f32_16x16x32_bf16 v[164:167], a[220:223], a[172:175], v[164:167]\n\t"
"v_fma_f32 v20, v120, s52, v20\n\t"
"v_fma_f32 v21, v121, s52, v21\n\t"
"v_mfma_f32_16x16x32_bf16 v[164:167], v[236:239], a[176:179], v[164:167]\n\t"
"buffer_load_dwordx4 a[240:243], v252, s[8:11], s20 offen offset:2048 sc1\n\t"
"s_waitcnt lgkmcnt(0)\n\t"
"s_barrier\n\t"
"v_mfma_f32_16x16x32_bf16 v[168:171], a[244:247], a[172:175], v[168:171]\n\t"
"ds_read_b128 a[168:171], v244 offset:0\n\t"
"v_fma_f32 v22, v122, s52, v22\n\t"
"v_mfma_f32_16x16x32_bf16 v[168:171], a[220:223], a[176:179], v[168:171]\n\t"
"ds_read_b128 a[172:175], v244 offset:512\n\t"
"v_fma_f32 v23, v123, s52, v23\n\t"
"v_mfma_f32_16x16x32_bf16 v[168:171], v[236:239], a[180:183], v[168:171]\n\t"
"v_fma_f32 v120, v20, s54, -v120\n\t"
"v_fma_f32 v121, v21, s54, -v121\n\t"
"v_mfma_f32_16x16x32_bf16 v[172:175], a[244:247], a[176:179], v[172:175]\n\t"
"ds_read_b128 a[176:179], v244 offset:1024\n\t"
"v_fma_f32 v122, v22, s54, -v122\n\t"
"v_mfma_f32_16x16x32_bf16 v[172:175], a[220:223], a[180:183], v[172:175]\n\t"
"v_fma_f32 v123, v23, s54, -v123\n\t"
"v_fma_f32 v24, v124, s52, v24\n\t"
"v_mfma_f32_16x16x32_bf16 v[172:175], v[236:239], a[184:187], v[172:175]\n\t"
"v_fma_f32 v25, v125, s52, v25\n\t"
"v_fma_f32 v26, v126, s52, v26\n\t"
"v_mfma_f32_16x16x32_bf16 v[176:179], a[244:247], a[180:183], v[176:179]\n\t"
"ds_read_b128 a[180:183], v244 offset:1536\n\t"
"v_fma_f32 v27, v127, s52, v27\n\t"
"v_mfma_f32_16x16x32_bf16 v[176:179], a[220:223], a[184:187], v[176:179]\n\t"
"v_fma_f32 v124, v24, s54, -v124\n\t"
"v_fma_f32 v125, v25, s54, -v125\n\t"
"v_mfma_f32_16x16x32_bf16 v[176:179], v[236:239], a[188:191], v[176:179]\n\t"
"v_fma_f32 v126, v26, s54, -v126\n\t"
"v_fma_f32 v127, v27, s54, -v127\n\t"
"v_fma_f32 v28, v128, s52, v28\n\t"
"v_fma_f32 v29, v129, s52, v29\n\t"
"v_mfma_f32_16x16x32_bf16 v[180:183], a[244:247], a[184:187], v[180:183]\n\t"
"ds_read_b128 a[184:187], v244 offset:2048\n\t"
"v_fma_f32 v30, v130, s52, v30\n\t"
"v_mfma_f32_16x16x32_bf16 v[180:183], a[220:223], a[188:191], v[180:183]\n\t"
"v_fma_f32 v31, v131, s52, v31\n\t"
"v_fma_f32 v128, v28, s54, -v128\n\t"
"v_mfma_f32_16x16x32_bf16 v[180:183], v[236:239], a[192:195], v[180:183]\n\t"
"v_fma_f32 v129, v29, s54, -v129\n\t"
"v_fma_f32 v130, v30, s54, -v130\n\t"
"v_fma_f32 v131, v31, s54, -v131\n\t"
"v_fma_f32 v32, v132, s52, v32\n\t"
"v_mfma_f32_16x16x32_bf16 v[184:187], a[244:247], a[188:191], v[184:187]\n\t"
"ds_read_b128 a[188:191], v244 offset:2560\n\t"
"v_fma_f32 v33, v133, s52, v33\n\t"
"v_mfma_f32_16x16x32_bf16 v[184:187], a[220:223], a[192:195], v[184:187]\n\t"
"v_fma_f32 v34, v134, s52, v34\n\t"
"v_fma_f32 v35, v135, s52, v35\n\t"
"v_mfma_f32_16x16x32_bf16 v[184:187], v[236:239], a[196:199], v[184:187]\n\t"
"v_fma_f32 v132, v32, s54, -v132\n\t"
"v_fma_f32 v133, v33, s54, -v133\n\t"
"v_fma_f32 v134, v34, s54, -v134\n\t"
"v_fma_f32 v135, v35, s54, -v135\n\t"
"v_mfma_f32_16x16x32_bf16 v[188:191], a[244:247], a[192:195], v[188:191]\n\t"
"ds_read_b128 a[192:195], v244 offset:3072\n\t"
"v_fma_f32 v36, v136, s52, v36\n\t"
"v_mfma_f32_16x16x32_bf16 v[188:191], a[220:223], a[196:199], v[188:191]\n\t"
"v_fma_f32 v37, v137, s52, v37\n\t"
"v_fma_f32 v38, v138, s52, v38\n\t"
"v_mfma_f32_16x16x32_bf16 v[188:191], v[236:239], a[200:203], v[188:191]\n\t"
"v_fma_f32 v39, v139, s52, v39\n\t"
"v_fma_f32 v136, v36, s54, -v136\n\t"
"v_fma_f32 v137, v37, s54, -v137\n\t"
"v_fma_f32 v138, v38, s54, -v138\n\t"
"v_mfma_f32_16x16x32_bf16 v[192:195], a[244:247], a[196:199], v[192:195]\n\t"
"ds_read_b128 a[196:199], v244 offset:3584\n\t"
"v_fma_f32 v139, v39, s54, -v139\n\t"
"v_mfma_f32_16x16x32_bf16 v[192:195], a[220:223], a[200:203], v[192:195]\n\t"
"ds_read_b128 a[200:203], v244 offset:4096\n\t"
"v_fma_f32 v40, v140, s52, v40\n\t"
"v_mfma_f32_16x16x32_bf16 v[192:195], v[236:239], a[204:207], v[192:195]\n\t"
"s_mov_b32 s20, 1265664\n\t"
"buffer_load_dwordx4 a[244:247], v252, s[8:11], s20 offen offset:0 sc1\n\t"
"ds_read_b128 a[204:207], v244 offset:4608\n\t"
"v_fma_f32 v41, v141, s52, v41\n\t"
"Lst31_%=:\n\t"
"s_waitcnt vmcnt(10) lgkmcnt(7)\n\t"
"v_mfma_f32_16x16x32_bf16 v[196:199], a[248:251], a[168:171], v[196:199]\n\t"
"v_fma_f32 v42, v142, s52, v42\n\t"
"v_fma_f32 v43, v143, s52, v43\n\t"
"v_mfma_f32_16x16x32_bf16 v[196:199], a[252:255], a[172:175], v[196:199]\n\t"
"v_fma_f32 v140, v40, s54, -v140\n\t"
"v_fma_f32 v141, v41, s54, -v141\n\t"
"v_mfma_f32_16x16x32_bf16 v[196:199], v[240:243], a[176:179], v[196:199]\n\t"
"buffer_load_dwordx4 a[220:223], v252, s[8:11], s20 offen offset:1024 sc1\n\t"
"v_fma_f32 v142, v42, s54, -v142\n\t"
"v_fma_f32 v143, v43, s54, -v143\n\t"
"s_waitcnt lgkmcnt(6)\n\t"
"v_mfma_f32_16x16x32_bf16 v[204:207], a[248:251], a[172:175], v[204:207]\n\t"
"v_fma_f32 v44, v144, s52, v44\n\t"
"v_fma_f32 v45, v145, s52, v45\n\t"
"v_mfma_f32_16x16x32_bf16 v[204:207], a[252:255], a[176:179], v[204:207]\n\t"
"v_fma_f32 v46, v146, s52, v46\n\t"
"v_fma_f32 v47, v147, s52, v47\n\t"
"v_mfma_f32_16x16x32_bf16 v[204:207], v[240:243], a[180:183], v[204:207]\n\t"
"v_fma_f32 v144, v44, s54, -v144\n\t"
"v_fma_f32 v145, v45, s54, -v145\n\t"
"v_fma_f32 v146, v46, s54, -v146\n\t"
"v_fma_f32 v147, v47, s54, -v147\n\t"
"s_waitcnt lgkmcnt(5)\n\t"
"v_mfma_f32_16x16x32_bf16 v[212:215], a[248:251], a[176:179], v[212:215]\n\t"
"v_fma_f32 v48, v148, s52, v48\n\t"
"v_fma_f32 v49, v149, s52, v49\n\t"
"v_mfma_f32_16x16x32_bf16 v[212:215], a[252:255], a[180:183], v[212:215]\n\t"
"v_fma_f32 v50, v150, s52, v50\n\t"
"v_fma_f32 v51, v151, s52, v51\n\t"
"v_mfma_f32_16x16x32_bf16 v[212:215], v[240:243], a[184:187], v[212:215]\n\t"
"v_fma_f32 v148, v48, s54, -v148\n\t"
"v_fma_f32 v149, v49, s54, -v149\n\t"
"v_fma_f32 v150, v50, s54, -v150\n\t"
"v_fma_f32 v151, v51, s54, -v151\n\t"
"s_waitcnt lgkmcnt(4)\n\t"
"v_mfma_f32_16x16x32_bf16 a[0:3], a[248:251], a[180:183], a[0:3]\n\t"
"v_fma_f32 v52, v152, s52, v52\n\t"
"v_fma_f32 v53, v153, s52, v53\n\t"
"v_mfma_f32_16x16x32_bf16 a[0:3], a[252:255], a[184:187], a[0:3]\n\t"
"v_fma_f32 v54, v154, s52, v54\n\t"
"v_fma_f32 v55, v155, s52, v55\n\t"
"v_mfma_f32_16x16x32_bf16 a[0:3], v[240:243], a[188:191], a[0:3]\n\t"
"v_fma_f32 v152, v52, s54, -v152\n\t"
"v_fma_f32 v153, v53, s54, -v153\n\t"
"v_fma_f32 v154, v54, s54, -v154\n\t"
"v_fma_f32 v155, v55, s54, -v155\n\t"
"s_waitcnt lgkmcnt(3)\n\t"
"v_mfma_f32_16x16x32_bf16 a[8:11], a[248:251], a[184:187], a[8:11]\n\t"
"v_fma_f32 v56, v156, s52, v56\n\t"
"v_fma_f32 v57, v157, s52, v57\n\t"
"v_mfma_f32_16x16x32_bf16 a[8:11], a[252:255], a[188:191], a[8:11]\n\t"
"v_fma_f32 v58, v158, s52, v58\n\t"
"v_fma_f32 v59, v159, s52, v59\n\t"
"v_mfma_f32_16x16x32_bf16 a[8:11], v[240:243], a[192:195], a[8:11]\n\t"
"v_fma_f32 v156, v56, s54, -v156\n\t"
"v_fma_f32 v157, v57, s54, -v157\n\t"
"v_fma_f32 v158, v58, s54, -v158\n\t"
"v_fma_f32 v159, v59, s54, -v159\n\t"
"s_waitcnt lgkmcnt(2)\n\t"
"v_mfma_f32_16x16x32_bf16 a[16:19], a[248:251], a[188:191], a[16:19]\n\t"
"v_fma_f32 v60, v160, s52, v60\n\t"
"v_fma_f32 v61, v161, s52, v61\n\t"
"v_mfma_f32_16x16x32_bf16 a[16:19], a[252:255], a[192:195], a[16:19]\n\t"
"v_fma_f32 v62, v162, s52, v62\n\t"
"v_fma_f32 v63, v163, s52, v63\n\t"
"v_mfma_f32_16x16x32_bf16 a[16:19], v[240:243], a[196:199], a[16:19]\n\t"
"v_fma_f32 v160, v60, s54, -v160\n\t"
"v_fma_f32 v161, v61, s54, -v161\n\t"
"v_fma_f32 v162, v62, s54, -v162\n\t"
"v_fma_f32 v163, v63, s54, -v163\n\t"
"s_waitcnt lgkmcnt(1)\n\t"
"v_mfma_f32_16x16x32_bf16 a[24:27], a[248:251], a[192:195], a[24:27]\n\t"
"v_fma_f32 v64, v164, s52, v64\n\t"
"v_fma_f32 v65, v165, s52, v65\n\t"
"v_mfma_f32_16x16x32_bf16 a[24:27], a[252:255], a[196:199], a[24:27]\n\t"
"v_fma_f32 v66, v166, s52, v66\n\t"
"v_fma_f32 v67, v167, s52, v67\n\t"
"v_mfma_f32_16x16x32_bf16 a[24:27], v[240:243], a[200:203], a[24:27]\n\t"
"v_fma_f32 v164, v64, s54, -v164\n\t"
"v_fma_f32 v165, v65, s54, -v165\n\t"
"s_waitcnt lgkmcnt(0)\n\t"
"v_mfma_f32_16x16x32_bf16 a[32:35], a[248:251], a[196:199], a[32:35]\n\t"
"v_fma_f32 v166, v66, s54, -v166\n\t"
"v_fma_f32 v167, v67, s54, -v167\n\t"
"v_mfma_f32_16x16x32_bf16 a[32:35], a[252:255], a[200:203], a[32:35]\n\t"
"v_fma_f32 v68, v168, s52, v68\n\t"
"v_fma_f32 v69, v169, s52, v69\n\t"
"v_mfma_f32_16x16x32_bf16 a[32:35], v[240:243], a[204:207], a[32:35]\n\t"
"buffer_load_dwordx4 a[248:251], v252, s[8:11], s20 offen offset:2048 sc1\n\t"
"v_fma_f32 v70, v170, s52, v70\n\t"
"v_fma_f32 v71, v171, s52, v71\n\t"
"s_waitcnt vmcnt(9)\n\t"
"v_mfma_f32_16x16x32_bf16 a[40:43], a[224:227], a[168:171], a[40:43]\n\t"
"v_fma_f32 v168, v68, s54, -v168\n\t"
"v_fma_f32 v169, v69, s54, -v169\n\t"
"v_mfma_f32_16x16x32_bf16 a[40:43], v[248:251], a[172:175], a[40:43]\n\t"
"v_fma_f32 v170, v70, s54, -v170\n\t"
"v_fma_f32 v171, v71, s54, -v171\n\t"
"v_mfma_f32_16x16x32_bf16 a[40:43], a[208:211], a[176:179], a[40:43]\n\t"
"s_mov_b32 s20, 1277952\n\t"
"buffer_load_dwordx4 v[240:243], v252, s[8:11], s20 offen offset:0 sc1\n\t"
"v_mfma_f32_16x16x32_bf16 a[48:51], a[224:227], a[172:175], a[48:51]\n\t"
"v_fma_f32 v72, v172, s52, v72\n\t"
"v_fma_f32 v73, v173, s52, v73\n\t"
"v_mfma_f32_16x16x32_bf16 a[48:51], v[248:251], a[176:179], a[48:51]\n\t"
"v_fma_f32 v74, v174, s52, v74\n\t"
"v_fma_f32 v75, v175, s52, v75\n\t"
"v_mfma_f32_16x16x32_bf16 a[48:51], a[208:211], a[180:183], a[48:51]\n\t"
"buffer_load_dwordx4 a[252:255], v252, s[8:11], s20 offen offset:1024 sc1\n\t"
"v_fma_f32 v172, v72, s54, -v172\n\t"
"v_fma_f32 v173, v73, s54, -v173\n\t"
"v_mfma_f32_16x16x32_bf16 a[56:59], a[224:227], a[176:179], a[56:59]\n\t"
"v_fma_f32 v174, v74, s54, -v174\n\t"
"v_fma_f32 v175, v75, s54, -v175\n\t"
"v_mfma_f32_16x16x32_bf16 a[56:59], v[248:251], a[180:183], a[56:59]\n\t"
"v_fma_f32 v76, v176, s52, v76\n\t"
"v_fma_f32 v77, v177, s52, v77\n\t"
"v_mfma_f32_16x16x32_bf16 a[56:59], a[208:211], a[184:187], a[56:59]\n\t"
"v_fma_f32 v78, v178, s52, v78\n\t"
"v_fma_f32 v79, v179, s52, v79\n\t"
"v_mfma_f32_16x16x32_bf16 a[64:67], a[224:227], a[180:183], a[64:67]\n\t"
"v_fma_f32 v176, v76, s54, -v176\n\t"
"v_fma_f32 v177, v77, s54, -v177\n\t"
"v_mfma_f32_16x16x32_bf16 a[64:67], v[248:251], a[184:187], a[64:67]\n\t"
"v_fma_f32 v178, v78, s54, -v178\n\t"
"v_fma_f32 v179, v79, s54, -v179\n\t"
"v_mfma_f32_16x16x32_bf16 a[64:67], a[208:211], a[188:191], a[64:67]\n\t"
"v_fma_f32 v80, v180, s52, v80\n\t"
"v_fma_f32 v81, v181, s52, v81\n\t"
"v_mfma_f32_16x16x32_bf16 a[72:75], a[224:227], a[184:187], a[72:75]\n\t"
"v_fma_f32 v82, v182, s52, v82\n\t"
"v_fma_f32 v83, v183, s52, v83\n\t"
"v_mfma_f32_16x16x32_bf16 a[72:75], v[248:251], a[188:191], a[72:75]\n\t"
"v_fma_f32 v180, v80, s54, -v180\n\t"
"v_fma_f32 v181, v81, s54, -v181\n\t"
"v_mfma_f32_16x16x32_bf16 a[72:75], a[208:211], a[192:195], a[72:75]\n\t"
"v_fma_f32 v182, v82, s54, -v182\n\t"
"v_fma_f32 v183, v83, s54, -v183\n\t"
"v_mfma_f32_16x16x32_bf16 a[80:83], a[224:227], a[188:191], a[80:83]\n\t"
"v_fma_f32 v84, v184, s52, v84\n\t"
"v_fma_f32 v85, v185, s52, v85\n\t"
"v_mfma_f32_16x16x32_bf16 a[80:83], v[248:251], a[192:195], a[80:83]\n\t"
"v_fma_f32 v86, v186, s52, v86\n\t"
"v_mfma_f32_16x16x32_bf16 a[80:83], a[208:211], a[196:199], a[80:83]\n\t"
"v_fma_f32 v87, v187, s52, v87\n\t"
"v_mfma_f32_16x16x32_bf16 a[88:91], a[224:227], a[192:195], a[88:91]\n\t"
"v_fma_f32 v184, v84, s54, -v184\n\t"
"v_fma_f32 v185, v85, s54, -v185\n\t"
"v_mfma_f32_16x16x32_bf16 a[88:91], v[248:251], a[196:199], a[88:91]\n\t"
"v_fma_f32 v186, v86, s54, -v186\n\t"
"v_mfma_f32_16x16x32_bf16 a[88:91], a[208:211], a[200:203], a[88:91]\n\t"
"v_fma_f32 v187, v87, s54, -v187\n\t"
"v_mfma_f32_16x16x32_bf16 a[96:99], a[224:227], a[196:199], a[96:99]\n\t"
"v_fma_f32 v88, v188, s52, v88\n\t"
"v_fma_f32 v89, v189, s52, v89\n\t"
"v_mfma_f32_16x16x32_bf16 a[96:99], v[248:251], a[200:203], a[96:99]\n\t"
"v_fma_f32 v90, v190, s52, v90\n\t"
"v_mfma_f32_16x16x32_bf16 a[96:99], a[208:211], a[204:207], a[96:99]\n\t"
"buffer_load_dwordx4 a[224:227], v252, s[8:11], s20 offen offset:2048 sc1\n\t"
"s_waitcnt vmcnt(9)\n\t"
"v_mfma_f32_16x16x32_bf16 a[104:107], a[212:215], a[168:171], a[104:107]\n\t"
"ds_read_b128 a[168:171], v244 offset:9216\n\t"
"v_fma_f32 v91, v191, s52, v91\n\t"
"v_mfma_f32_16x16x32_bf16 a[104:107], a[228:231], a[172:175], a[104:107]\n\t"
"v_fma_f32 v188, v88, s54, -v188\n\t"
"v_mfma_f32_16x16x32_bf16 a[104:107], a[216:219], a[176:179], a[104:107]\n\t"
"s_mov_b32 s20, 442368\n\t"
"buffer_load_dwordx4 v[248:251], v252, s[8:11], s20 offen offset:0 sc1\n\t"
"v_mfma_f32_16x16x32_bf16 a[112:115], a[212:215], a[172:175], a[112:115]\n\t"
"ds_read_b128 a[172:175], v244 offset:9728\n\t"
"v_fma_f32 v189, v89, s54, -v189\n\t"
"v_mfma_f32_16x16x32_bf16 a[112:115], a[228:231], a[176:179], a[112:115]\n\t"
"v_fma_f32 v190, v90, s54, -v190\n\t"
"v_fma_f32 v191, v91, s54, -v191\n\t"
"v_mfma_f32_16x16x32_bf16 a[112:115], a[216:219], a[180:183], a[112:115]\n\t"
"v_fma_f32 v92, v192, s52, v92\n\t"
"v_mfma_f32_16x16x32_bf16 a[120:123], a[212:215], a[176:179], a[120:123]\n\t"
"ds_read_b128 a[176:179], v244 offset:10240\n\t"
"v_fma_f32 v93, v193, s52, v93\n\t"
"v_mfma_f32_16x16x32_bf16 a[120:123], a[228:231], a[180:183], a[120:123]\n\t"
"v_fma_f32 v94, v194, s52, v94\n\t"
"v_mfma_f32_16x16x32_bf16 a[120:123], a[216:219], a[184:187], a[120:123]\n\t"
"buffer_load_dwordx4 a[208:211], v252, s[8:11], s20 offen offset:1024 sc1\n\t"
"v_mfma_f32_16x16x32_bf16 a[128:131], a[212:215], a[180:183], a[128:131]\n\t"
"ds_read_b128 a[180:183], v244 offset:10752\n\t"
"v_mfma_f32_16x16x32_bf16 a[128:131], a[228:231], a[184:187], a[128:131]\n\t"
"v_fma_f32 v95, v195, s52, v95\n\t"
"v_mfma_f32_16x16x32_bf16 a[128:131], a[216:219], a[188:191], a[128:131]\n\t"
"v_fma_f32 v192, v92, s54, -v192\n\t"
"v_mfma_f32_16x16x32_bf16 a[136:139], a[212:215], a[184:187], a[136:139]\n\t"
"ds_read_b128 a[184:187], v244 offset:11264\n\t"
"v_mfma_f32_16x16x32_bf16 a[136:139], a[228:231], a[188:191], a[136:139]\n\t"
"v_mfma_f32_16x16x32_bf16 a[136:139], a[216:219], a[192:195], a[136:139]\n\t"
"v_fma_f32 v193, v93, s54, -v193\n\t"
"v_mfma_f32_16x16x32_bf16 a[144:147], a[212:215], a[188:191], a[144:147]\n\t"
"ds_read_b128 a[188:191], v244 offset:11776\n\t"
"v_mfma_f32_16x16x32_bf16 a[144:147], a[228:231], a[192:195], a[144:147]\n\t"
"v_fma_f32 v194, v94, s54, -v194\n\t"
"v_mfma_f32_16x16x32_bf16 a[144:147], a[216:219], a[196:199], a[144:147]\n\t"
"v_mfma_f32_16x16x32_bf16 a[152:155], a[212:215], a[192:195], a[152:155]\n\t"
"ds_read_b128 a[192:195], v244 offset:12288\n\t"
"v_mfma_f32_16x16x32_bf16 a[152:155], a[228:231], a[196:199], a[152:155]\n\t"
"v_fma_f32 v195, v95, s54, -v195\n\t"
"v_mfma_f32_16x16x32_bf16 a[152:155], a[216:219], a[200:203], a[152:155]\n\t"
"v_mfma_f32_16x16x32_bf16 a[160:163], a[212:215], a[196:199], a[160:163]\n\t"
"ds_read_b128 a[196:199], v244 offset:12800\n\t"
"v_mfma_f32_16x16x32_bf16 a[160:163], a[228:231], a[200:203], a[160:163]\n\t"
"ds_read_b128 a[200:203], v244 offset:13312\n\t"
"v_mfma_f32_16x16x32_bf16 a[160:163], a[216:219], a[204:207], a[160:163]\n\t"
"buffer_load_dwordx4 a[212:215], v252, s[8:11], s20 offen offset:2048 sc1\n\t"
"s_waitcnt vmcnt(9) lgkmcnt(6)\n\t"
"v_mfma_f32_16x16x32_bf16 v[200:203], a[232:235], a[168:171], v[200:203]\n\t"
"ds_read_b128 a[204:207], v244 offset:13824\n\t"
"v_mfma_f32_16x16x32_bf16 v[200:203], a[236:239], a[172:175], v[200:203]\n\t"
"v_mfma_f32_16x16x32_bf16 v[200:203], a[240:243], a[176:179], v[200:203]\n\t"
"s_mov_b32 s20, 454656\n\t"
"buffer_load_dwordx4 a[216:219], v252, s[8:11], s20 offen offset:0 sc1\n\t"
"s_waitcnt lgkmcnt(6)\n\t"
"v_mfma_f32_16x16x32_bf16 v[208:211], a[232:235], a[172:175], v[208:211]\n\t"
"v_mfma_f32_16x16x32_bf16 v[208:211], a[236:239], a[176:179], v[208:211]\n\t"
"v_mfma_f32_16x16x32_bf16 v[208:211], a[240:243], a[180:183], v[208:211]\n\t"
"buffer_load_dwordx4 a[228:231], v252, s[8:11], s20 offen offset:1024 sc1\n\t"
"s_waitcnt lgkmcnt(0)\n\t"
"s_barrier\n\t"
"v_mfma_f32_16x16x32_bf16 v[216:219], a[232:235], a[176:179], v[216:219]\n\t"
"v_mfma_f32_16x16x32_bf16 v[216:219], a[236:239], a[180:183], v[216:219]\n\t"
"v_mfma_f32_16x16x32_bf16 v[216:219], a[240:243], a[184:187], v[216:219]\n\t"
"v_pk_fma_f32 v[196:197], v[200:201], s[56:57], v[196:197]\n\t"
"v_pk_fma_f32 v[200:201], v[196:197], s[58:59], v[200:201] neg_lo:[0,0,1] neg_hi:[0,0,1]\n\t"
"v_pk_fma_f32 v[228:229], v[0:1], s[60:61], v[196:197]\n\t"
"v_pk_fma_f32 v[0:1], v[196:197], s[60:61], v[0:1]\n\t"
"v_pk_fma_f32 v[196:197], v[100:101], s[62:63], v[200:201]\n\t"
"v_pk_fma_f32 v[100:101], v[200:201], s[62:63], v[100:101]\n\t"
"v_pk_fma_f32 v[198:199], v[202:203], s[56:57], v[198:199]\n\t"
"v_pk_fma_f32 v[202:203], v[198:199], s[58:59], v[202:203] neg_lo:[0,0,1] neg_hi:[0,0,1]\n\t"
"v_pk_fma_f32 v[234:235], v[2:3], s[60:61], v[198:199]\n\t"
"v_pk_fma_f32 v[2:3], v[198:199], s[60:61], v[2:3]\n\t"
"v_pk_fma_f32 v[198:199], v[102:103], s[62:63], v[202:203]\n\t"
"v_pk_fma_f32 v[102:103], v[202:203], s[62:63], v[102:103]\n\t"
"v_cvt_pk_bf16_f32 v196, v196, v197\n\t"
"v_cvt_pk_bf16_f32 v200, v228, v229\n\t"
"v_mfma_f32_16x16x32_bf16 a[4:7], a[232:235], a[180:183], a[4:7]\n\t"
"v_cvt_pk_bf16_f32 v197, v198, v199\n\t"
"v_mfma_f32_16x16x32_bf16 a[4:7], a[236:239], a[184:187], a[4:7]\n\t"
"v_cvt_pk_bf16_f32 v201, v234, v235\n\t"
"v_mfma_f32_16x16x32_bf16 a[4:7], a[240:243], a[188:191], a[4:7]\n\t"
"v_pk_fma_f32 v[204:205], v[208:209], s[56:57], v[204:205]\n\t"
"v_pk_fma_f32 v[208:209], v[204:205], s[58:59], v[208:209] neg_lo:[0,0,1] neg_hi:[0,0,1]\n\t"
"v_pk_fma_f32 v[98:99], v[4:5], s[60:61], v[204:205]\n\t"
"v_pk_fma_f32 v[4:5], v[204:205], s[60:61], v[4:5]\n\t"
"v_pk_fma_f32 v[204:205], v[104:105], s[62:63], v[208:209]\n\t"
"v_pk_fma_f32 v[104:105], v[208:209], s[62:63], v[104:105]\n\t"
"v_cvt_pk_bf16_f32 v204, v204, v205\n\t"
"v_mfma_f32_16x16x32_bf16 a[12:15], a[232:235], a[184:187], a[12:15]\n\t"
"v_cvt_pk_bf16_f32 v208, v98, v99\n\t"
"v_mfma_f32_16x16x32_bf16 a[12:15], a[236:239], a[188:191], a[12:15]\n\t"
"v_mfma_f32_16x16x32_bf16 a[12:15], a[240:243], a[192:195], a[12:15]\n\t"
"v_pk_fma_f32 v[206:207], v[210:211], s[56:57], v[206:207]\n\t"
"v_pk_fma_f32 v[210:211], v[206:207], s[58:59], v[210:211] neg_lo:[0,0,1] neg_hi:[0,0,1]\n\t"
"v_pk_fma_f32 v[228:229], v[6:7], s[60:61], v[206:207]\n\t"
"v_pk_fma_f32 v[6:7], v[206:207], s[60:61], v[6:7]\n\t"
"v_pk_fma_f32 v[206:207], v[106:107], s[62:63], v[210:211]\n\t"
"v_pk_fma_f32 v[106:107], v[210:211], s[62:63], v[106:107]\n\t"
"v_cvt_pk_bf16_f32 v205, v206, v207\n\t"
"v_cvt_pk_bf16_f32 v209, v228, v229\n\t"
"v_mfma_f32_16x16x32_bf16 a[20:23], a[232:235], a[188:191], a[20:23]\n\t"
"v_mfma_f32_16x16x32_bf16 a[20:23], a[236:239], a[192:195], a[20:23]\n\t"
"v_accvgpr_read_b32 v224, a0\n\t"
"v_mfma_f32_16x16x32_bf16 a[20:23], a[240:243], a[196:199], a[20:23]\n\t"
"v_pk_fma_f32 v[212:213], v[216:217], s[56:57], v[212:213]\n\t"
"v_pk_fma_f32 v[216:217], v[212:213], s[58:59], v[216:217] neg_lo:[0,0,1] neg_hi:[0,0,1]\n\t"
"v_pk_fma_f32 v[234:235], v[8:9], s[60:61], v[212:213]\n\t"
"v_pk_fma_f32 v[8:9], v[212:213], s[60:61], v[8:9]\n\t"
"v_pk_fma_f32 v[212:213], v[108:109], s[62:63], v[216:217]\n\t"
"v_pk_fma_f32 v[108:109], v[216:217], s[62:63], v[108:109]\n\t"
"v_cvt_pk_bf16_f32 v212, v212, v213\n\t"
"v_cvt_pk_bf16_f32 v216, v234, v235\n\t"
"v_mfma_f32_16x16x32_bf16 a[28:31], a[232:235], a[192:195], a[28:31]\n\t"
"v_accvgpr_read_b32 v226, a4\n\t"
"v_mfma_f32_16x16x32_bf16 a[28:31], a[236:239], a[196:199], a[28:31]\n\t"
"v_accvgpr_read_b32 v225, a1\n\t"
"v_accvgpr_read_b32 v227, a5\n\t"
"v_mfma_f32_16x16x32_bf16 a[28:31], a[240:243], a[200:203], a[28:31]\n\t"
"v_pk_fma_f32 v[214:215], v[218:219], s[56:57], v[214:215]\n\t"
"v_pk_fma_f32 v[218:219], v[214:215], s[58:59], v[218:219] neg_lo:[0,0,1] neg_hi:[0,0,1]\n\t"
"v_pk_fma_f32 v[98:99], v[10:11], s[60:61], v[214:215]\n\t"
"v_pk_fma_f32 v[10:11], v[214:215], s[60:61], v[10:11]\n\t"
"v_pk_fma_f32 v[214:215], v[110:111], s[62:63], v[218:219]\n\t"
"v_pk_fma_f32 v[110:111], v[218:219], s[62:63], v[110:111]\n\t"
"v_cvt_pk_bf16_f32 v213, v214, v215\n\t"
"v_cvt_pk_bf16_f32 v217, v98, v99\n\t"
"v_mfma_f32_16x16x32_bf16 a[36:39], a[232:235], a[196:199], a[36:39]\n\t"
"v_accvgpr_read_b32 v230, a2\n\t"
"v_accvgpr_read_b32 v232, a6\n\t"
"v_mfma_f32_16x16x32_bf16 a[36:39], a[236:239], a[200:203], a[36:39]\n\t"
"v_accvgpr_read_b32 v231, a3\n\t"
"v_accvgpr_read_b32 v233, a7\n\t"
"v_mfma_f32_16x16x32_bf16 a[36:39], a[240:243], a[204:207], a[36:39]\n\t"
"v_pk_fma_f32 v[224:225], v[226:227], s[56:57], v[224:225]\n\t"
"v_pk_fma_f32 v[226:227], v[224:225], s[58:59], v[226:227] neg_lo:[0,0,1] neg_hi:[0,0,1]\n\t"
"v_pk_fma_f32 v[228:229], v[12:13], s[60:61], v[224:225]\n\t"
"v_pk_fma_f32 v[12:13], v[224:225], s[60:61], v[12:13]\n\t"
"v_pk_fma_f32 v[224:225], v[112:113], s[62:63], v[226:227]\n\t"
"v_pk_fma_f32 v[112:113], v[226:227], s[62:63], v[112:113]\n\t"
"buffer_load_dwordx4 a[232:235], v252, s[8:11], s20 offen offset:2048 sc1\n\t"
"s_waitcnt vmcnt(9)\n\t"
"v_mfma_f32_16x16x32_bf16 a[44:47], a[244:247], a[168:171], a[44:47]\n\t"
"v_cvt_pk_bf16_f32 v224, v224, v225\n\t"
"v_cvt_pk_bf16_f32 v228, v228, v229\n\t"
"v_mfma_f32_16x16x32_bf16 a[44:47], a[220:223], a[172:175], a[44:47]\n\t"
"v_accvgpr_write_b32 a0, v224\n\t"
"v_accvgpr_write_b32 a4, v228\n\t"
"v_mfma_f32_16x16x32_bf16 a[44:47], a[248:251], a[176:179], a[44:47]\n\t"
"v_pk_fma_f32 v[230:231], v[232:233], s[56:57], v[230:231]\n\t"
"v_pk_fma_f32 v[232:233], v[230:231], s[58:59], v[232:233] neg_lo:[0,0,1] neg_hi:[0,0,1]\n\t"
"v_pk_fma_f32 v[234:235], v[14:15], s[60:61], v[230:231]\n\t"
"v_pk_fma_f32 v[14:15], v[230:231], s[60:61], v[14:15]\n\t"
"v_pk_fma_f32 v[230:231], v[114:115], s[62:63], v[232:233]\n\t"
"v_pk_fma_f32 v[114:115], v[232:233], s[62:63], v[114:115]\n\t"
"s_mov_b32 s20, 466944\n\t"
"buffer_load_dwordx4 a[236:239], v252, s[8:11], s20 offen offset:0 sc1\n\t"
"v_accvgpr_read_b32 v236, a8\n\t"
"v_accvgpr_read_b32 v238, a12\n\t"
"v_mfma_f32_16x16x32_bf16 a[52:55], a[244:247], a[172:175], a[52:55]\n\t"
"v_accvgpr_read_b32 v237, a9\n\t"
"v_accvgpr_read_b32 v239, a13\n\t"
"v_mfma_f32_16x16x32_bf16 a[52:55], a[220:223], a[176:179], a[52:55]\n\t"
"v_cvt_pk_bf16_f32 v230, v230, v231\n\t"
"v_cvt_pk_bf16_f32 v234, v234, v235\n\t"
"v_mfma_f32_16x16x32_bf16 a[52:55], a[248:251], a[180:183], a[52:55]\n\t"
"v_pk_fma_f32 v[236:237], v[238:239], s[56:57], v[236:237]\n\t"
"v_pk_fma_f32 v[238:239], v[236:237], s[58:59], v[238:239] neg_lo:[0,0,1] neg_hi:[0,0,1]\n\t"
"v_pk_fma_f32 v[98:99], v[16:17], s[60:61], v[236:237]\n\t"
"v_pk_fma_f32 v[16:17], v[236:237], s[60:61], v[16:17]\n\t"
"v_pk_fma_f32 v[236:237], v[116:117], s[62:63], v[238:239]\n\t"
"v_pk_fma_f32 v[116:117], v[238:239], s[62:63], v[116:117]\n\t"
"buffer_load_dwordx4 a[240:243], v252, s[8:11], s20 offen offset:1024 sc1\n\t"
"v_accvgpr_write_b32 a1, v230\n\t"
"v_accvgpr_write_b32 a5, v234\n\t"
"v_mfma_f32_16x16x32_bf16 a[60:63], a[244:247], a[176:179], a[60:63]\n\t"
"v_accvgpr_read_b32 v224, a10\n\t"
"v_accvgpr_read_b32 v226, a14\n\t"
"v_mfma_f32_16x16x32_bf16 a[60:63], a[220:223], a[180:183], a[60:63]\n\t"
"v_accvgpr_read_b32 v225, a11\n\t"
"v_accvgpr_read_b32 v227, a15\n\t"
"v_mfma_f32_16x16x32_bf16 a[60:63], a[248:251], a[184:187], a[60:63]\n\t"
"v_accvgpr_read_b32 v230, a16\n\t"
"v_accvgpr_read_b32 v232, a20\n\t"
"v_accvgpr_read_b32 v231, a17\n\t"
"v_accvgpr_read_b32 v233, a21\n\t"
"v_cvt_pk_bf16_f32 v236, v236, v237\n\t"
"v_cvt_pk_bf16_f32 v98, v98, v99\n\t"
"v_pk_fma_f32 v[224:225], v[226:227], s[56:57], v[224:225]\n\t"
"v_pk_fma_f32 v[226:227], v[224:225], s[58:59], v[226:227] neg_lo:[0,0,1] neg_hi:[0,0,1]\n\t"
"v_pk_fma_f32 v[228:229], v[18:19], s[60:61], v[224:225]\n\t"
"v_pk_fma_f32 v[18:19], v[224:225], s[60:61], v[18:19]\n\t"
"v_pk_fma_f32 v[224:225], v[118:119], s[62:63], v[226:227]\n\t"
"v_pk_fma_f32 v[118:119], v[226:227], s[62:63], v[118:119]\n\t"
"v_pk_fma_f32 v[230:231], v[232:233], s[56:57], v[230:231]\n\t"
"v_pk_fma_f32 v[232:233], v[230:231], s[58:59], v[232:233] neg_lo:[0,0,1] neg_hi:[0,0,1]\n\t"
"v_pk_fma_f32 v[234:235], v[20:21], s[60:61], v[230:231]\n\t"
"v_pk_fma_f32 v[20:21], v[230:231], s[60:61], v[20:21]\n\t"
"v_pk_fma_f32 v[230:231], v[120:121], s[62:63], v[232:233]\n\t"
"v_pk_fma_f32 v[120:121], v[232:233], s[62:63], v[120:121]\n\t"
"v_mfma_f32_16x16x32_bf16 a[68:71], a[244:247], a[180:183], a[68:71]\n\t"
"v_accvgpr_write_b32 a8, v236\n\t"
"v_accvgpr_write_b32 a12, v98\n\t"
"v_mfma_f32_16x16x32_bf16 a[68:71], a[220:223], a[184:187], a[68:71]\n\t"
"v_accvgpr_read_b32 v236, a18\n\t"
"v_accvgpr_read_b32 v238, a22\n\t"
"v_mfma_f32_16x16x32_bf16 a[68:71], a[248:251], a[188:191], a[68:71]\n\t"
"v_accvgpr_read_b32 v237, a19\n\t"
"v_accvgpr_read_b32 v239, a23\n\t"
"v_cvt_pk_bf16_f32 v224, v224, v225\n\t"
"v_cvt_pk_bf16_f32 v228, v228, v229\n\t"
"v_cvt_pk_bf16_f32 v230, v230, v231\n\t"
"v_cvt_pk_bf16_f32 v234, v234, v235\n\t"
"v_pk_fma_f32 v[236:237], v[238:239], s[56:57], v[236:237]\n\t"
"v_pk_fma_f32 v[238:239], v[236:237], s[58:59], v[238:239] neg_lo:[0,0,1] neg_hi:[0,0,1]\n\t"
"v_pk_fma_f32 v[98:99], v[22:23], s[60:61], v[236:237]\n\t"
"v_pk_fma_f32 v[22:23], v[236:237], s[60:61], v[22:23]\n\t"
"v_pk_fma_f32 v[236:237], v[122:123], s[62:63], v[238:239]\n\t"
"v_pk_fma_f32 v[122:123], v[238:239], s[62:63], v[122:123]\n\t"
"v_mfma_f32_16x16x32_bf16 a[76:79], a[244:247], a[184:187], a[76:79]\n\t"
"v_accvgpr_write_b32 a9, v224\n\t"
"v_accvgpr_write_b32 a13, v228\n\t"
"v_mfma_f32_16x16x32_bf16 a[76:79], a[220:223], a[188:191], a[76:79]\n\t"
"v_accvgpr_write_b32 a16, v230\n\t"
"v_accvgpr_write_b32 a20, v234\n\t"
"v_mfma_f32_16x16x32_bf16 a[76:79], a[248:251], a[192:195], a[76:79]\n\t"
"v_accvgpr_read_b32 v224, a24\n\t"
"v_accvgpr_read_b32 v226, a28\n\t"
"v_accvgpr_read_b32 v225, a25\n\t"
"v_accvgpr_read_b32 v227, a29\n\t"
"v_cvt_pk_bf16_f32 v236, v236, v237\n\t"
"v_cvt_pk_bf16_f32 v98, v98, v99\n\t"
"v_pk_fma_f32 v[224:225], v[226:227], s[56:57], v[224:225]\n\t"
"v_pk_fma_f32 v[226:227], v[224:225], s[58:59], v[226:227] neg_lo:[0,0,1] neg_hi:[0,0,1]\n\t"
"v_pk_fma_f32 v[228:229], v[24:25], s[60:61], v[224:225]\n\t"
"v_pk_fma_f32 v[24:25], v[224:225], s[60:61], v[24:25]\n\t"
"v_pk_fma_f32 v[224:225], v[124:125], s[62:63], v[226:227]\n\t"
"v_pk_fma_f32 v[124:125], v[226:227], s[62:63], v[124:125]\n\t"
"v_mfma_f32_16x16x32_bf16 a[84:87], a[244:247], a[188:191], a[84:87]\n\t"
"v_accvgpr_write_b32 a17, v236\n\t"
"v_accvgpr_write_b32 a21, v98\n\t"
"v_mfma_f32_16x16x32_bf16 a[84:87], a[220:223], a[192:195], a[84:87]\n\t"
"v_accvgpr_read_b32 v230, a26\n\t"
"v_accvgpr_read_b32 v232, a30\n\t"
"v_mfma_f32_16x16x32_bf16 a[84:87], a[248:251], a[196:199], a[84:87]\n\t"
"v_accvgpr_read_b32 v231, a27\n\t"
"v_accvgpr_read_b32 v233, a31\n\t"
"v_cvt_pk_bf16_f32 v224, v224, v225\n\t"
"v_cvt_pk_bf16_f32 v228, v228, v229\n\t"
"v_pk_fma_f32 v[230:231], v[232:233], s[56:57], v[230:231]\n\t"
"v_pk_fma_f32 v[232:233], v[230:231], s[58:59], v[232:233] neg_lo:[0,0,1] neg_hi:[0,0,1]\n\t"
"v_pk_fma_f32 v[234:235], v[26:27], s[60:61], v[230:231]\n\t"
"v_pk_fma_f32 v[26:27], v[230:231], s[60:61], v[26:27]\n\t"
"v_pk_fma_f32 v[230:231], v[126:127], s[62:63], v[232:233]\n\t"
"v_pk_fma_f32 v[126:127], v[232:233], s[62:63], v[126:127]\n\t"
"v_mfma_f32_16x16x32_bf16 a[92:95], a[244:247], a[192:195], a[92:95]\n\t"
"v_accvgpr_write_b32 a24, v224\n\t"
"v_accvgpr_write_b32 a28, v228\n\t"
"v_mfma_f32_16x16x32_bf16 a[92:95], a[220:223], a[196:199], a[92:95]\n\t"
"v_accvgpr_read_b32 v236, a32\n\t"
"v_accvgpr_read_b32 v238, a36\n\t"
"v_mfma_f32_16x16x32_bf16 a[92:95], a[248:251], a[200:203], a[92:95]\n\t"
"v_accvgpr_read_b32 v237, a33\n\t"
"v_accvgpr_read_b32 v239, a37\n\t"
"v_cvt_pk_bf16_f32 v230, v230, v231\n\t"
"v_cvt_pk_bf16_f32 v234, v234, v235\n\t"
"v_pk_fma_f32 v[236:237], v[238:239], s[56:57], v[236:237]\n\t"
"v_pk_fma_f32 v[238:239], v[236:237], s[58:59], v[238:239] neg_lo:[0,0,1] neg_hi:[0,0,1]\n\t"
"v_pk_fma_f32 v[98:99], v[28:29], s[60:61], v[236:237]\n\t"
"v_pk_fma_f32 v[28:29], v[236:237], s[60:61], v[28:29]\n\t"
"v_pk_fma_f32 v[236:237], v[128:129], s[62:63], v[238:239]\n\t"
"v_pk_fma_f32 v[128:129], v[238:239], s[62:63], v[128:129]\n\t"
"v_mfma_f32_16x16x32_bf16 a[100:103], a[244:247], a[196:199], a[100:103]\n\t"
"v_accvgpr_write_b32 a25, v230\n\t"
"v_accvgpr_write_b32 a29, v234\n\t"
"v_mfma_f32_16x16x32_bf16 a[100:103], a[220:223], a[200:203], a[100:103]\n\t"
"v_accvgpr_read_b32 v224, a34\n\t"
"v_accvgpr_read_b32 v226, a38\n\t"
"v_mfma_f32_16x16x32_bf16 a[100:103], a[248:251], a[204:207], a[100:103]\n\t"
"v_accvgpr_read_b32 v225, a35\n\t"
"v_accvgpr_read_b32 v227, a39\n\t"
"v_cvt_pk_bf16_f32 v236, v236, v237\n\t"
"v_cvt_pk_bf16_f32 v98, v98, v99\n\t"
"v_pk_fma_f32 v[224:225], v[226:227], s[56:57], v[224:225]\n\t"
"v_pk_fma_f32 v[226:227], v[224:225], s[58:59], v[226:227] neg_lo:[0,0,1] neg_hi:[0,0,1]\n\t"
"v_pk_fma_f32 v[228:229], v[30:31], s[60:61], v[224:225]\n\t"
"v_pk_fma_f32 v[30:31], v[224:225], s[60:61], v[30:31]\n\t"
"v_pk_fma_f32 v[224:225], v[130:131], s[62:63], v[226:227]\n\t"
"v_pk_fma_f32 v[130:131], v[226:227], s[62:63], v[130:131]\n\t"
"s_waitcnt vmcnt(8)\n\t"
"v_mfma_f32_16x16x32_bf16 a[108:111], v[240:243], a[168:171], a[108:111]\n\t"
"v_accvgpr_write_b32 a32, v236\n\t"
"v_accvgpr_write_b32 a36, v98\n\t"
"v_mfma_f32_16x16x32_bf16 a[108:111], a[252:255], a[172:175], a[108:111]\n\t"
"v_accvgpr_read_b32 v230, a40\n\t"
"v_accvgpr_read_b32 v232, a44\n\t"
"v_mfma_f32_16x16x32_bf16 a[108:111], a[224:227], a[176:179], a[108:111]\n\t"
"buffer_load_dwordx4 a[244:247], v252, s[8:11], s20 offen offset:2048 sc1\n\t"
"v_accvgpr_read_b32 v231, a41\n\t"
"v_accvgpr_read_b32 v233, a45\n\t"
"v_cvt_pk_bf16_f32 v224, v224, v225\n\t"
"v_cvt_pk_bf16_f32 v228, v228, v229\n\t"
"v_pk_fma_f32 v[230:231], v[232:233], s[56:57], v[230:231]\n\t"
"v_pk_fma_f32 v[232:233], v[230:231], s[58:59], v[232:233] neg_lo:[0,0,1] neg_hi:[0,0,1]\n\t"
"v_pk_fma_f32 v[234:235], v[32:33], s[60:61], v[230:231]\n\t"
"v_pk_fma_f32 v[32:33], v[230:231], s[60:61], v[32:33]\n\t"
"v_pk_fma_f32 v[230:231], v[132:133], s[62:63], v[232:233]\n\t"
"v_pk_fma_f32 v[132:133], v[232:233], s[62:63], v[132:133]\n\t"
"v_mfma_f32_16x16x32_bf16 a[116:119], v[240:243], a[172:175], a[116:119]\n\t"
"v_accvgpr_write_b32 a33, v224\n\t"
"v_accvgpr_write_b32 a37, v228\n\t"
"v_mfma_f32_16x16x32_bf16 a[116:119], a[252:255], a[176:179], a[116:119]\n\t"
"v_accvgpr_read_b32 v236, a42\n\t"
"v_accvgpr_read_b32 v238, a46\n\t"
"v_mfma_f32_16x16x32_bf16 a[116:119], a[224:227], a[180:183], a[116:119]\n\t"
"s_mov_b32 s20, 626688\n\t"
"buffer_load_dwordx4 a[220:223], v252, s[8:11], s20 offen offset:0 sc1\n\t"
"v_accvgpr_read_b32 v237, a43\n\t"
"v_accvgpr_read_b32 v239, a47\n\t"
"v_pk_fma_f32 v[236:237], v[238:239], s[56:57], v[236:237]\n\t"
"v_pk_fma_f32 v[238:239], v[236:237], s[58:59], v[238:239] neg_lo:[0,0,1] neg_hi:[0,0,1]\n\t"
"v_pk_fma_f32 v[98:99], v[34:35], s[60:61], v[236:237]\n\t"
"v_pk_fma_f32 v[34:35], v[236:237], s[60:61], v[34:35]\n\t"
"v_pk_fma_f32 v[236:237], v[134:135], s[62:63], v[238:239]\n\t"
"v_pk_fma_f32 v[134:135], v[238:239], s[62:63], v[134:135]\n\t"
"v_mfma_f32_16x16x32_bf16 a[124:127], v[240:243], a[176:179], a[124:127]\n\t"
"v_cvt_pk_bf16_f32 v198, v230, v231\n\t"
"v_cvt_pk_bf16_f32 v202, v234, v235\n\t"
"v_mfma_f32_16x16x32_bf16 a[124:127], a[252:255], a[180:183], a[124:127]\n\t"
"v_accvgpr_read_b32 v224, a48\n\t"
"v_accvgpr_read_b32 v226, a52\n\t"
"v_mfma_f32_16x16x32_bf16 a[124:127], a[224:227], a[184:187], a[124:127]\n\t"
"buffer_load_dwordx4 a[248:251], v252, s[8:11], s20 offen offset:1024 sc1\n\t"
"v_accvgpr_read_b32 v225, a49\n\t"
"v_accvgpr_read_b32 v227, a53\n\t"
"v_accvgpr_read_b32 v230, a50\n\t"
"v_accvgpr_read_b32 v232, a54\n\t"
"v_accvgpr_read_b32 v231, a51\n\t"
"v_accvgpr_read_b32 v233, a55\n\t"
"v_pk_fma_f32 v[224:225], v[226:227], s[56:57], v[224:225]\n\t"
"v_pk_fma_f32 v[226:227], v[224:225], s[58:59], v[226:227] neg_lo:[0,0,1] neg_hi:[0,0,1]\n\t"
"v_pk_fma_f32 v[228:229], v[36:37], s[60:61], v[224:225]\n\t"
"v_pk_fma_f32 v[36:37], v[224:225], s[60:61], v[36:37]\n\t"
"v_pk_fma_f32 v[224:225], v[136:137], s[62:63], v[226:227]\n\t"
"v_pk_fma_f32 v[136:137], v[226:227], s[62:63], v[136:137]\n\t"
"v_pk_fma_f32 v[230:231], v[232:233], s[56:57], v[230:231]\n\t"
"v_pk_fma_f32 v[232:233], v[230:231], s[58:59], v[232:233] neg_lo:[0,0,1] neg_hi:[0,0,1]\n\t"
"v_pk_fma_f32 v[234:235], v[38:39], s[60:61], v[230:231]\n\t"
"v_pk_fma_f32 v[38:39], v[230:231], s[60:61], v[38:39]\n\t"
"v_pk_fma_f32 v[230:231], v[138:139], s[62:63], v[232:233]\n\t"
"v_pk_fma_f32 v[138:139], v[232:233], s[62:63], v[138:139]\n\t"
"v_mfma_f32_16x16x32_bf16 a[132:135], v[240:243], a[180:183], a[132:135]\n\t"
"v_cvt_pk_bf16_f32 v199, v236, v237\n\t"
"v_cvt_pk_bf16_f32 v203, v98, v99\n\t"
"v_mfma_f32_16x16x32_bf16 a[132:135], a[252:255], a[184:187], a[132:135]\n\t"
"v_accvgpr_read_b32 v236, a56\n\t"
"v_accvgpr_read_b32 v238, a60\n\t"
"v_mfma_f32_16x16x32_bf16 a[132:135], a[224:227], a[188:191], a[132:135]\n\t"
"v_accvgpr_read_b32 v237, a57\n\t"
"v_accvgpr_read_b32 v239, a61\n\t"
"ds_read_b128 a[168:171], v244 offset:18432\n\t"
"ds_read_b128 a[172:175], v244 offset:18944\n\t"
"ds_read_b128 a[176:179], v244 offset:19456\n\t"
"buffer_load_dwordx4 a[40:43], v252, s[8:11], s20 offen offset:2048 sc1\n\t"
"v_pk_fma_f32 v[236:237], v[238:239], s[56:57], v[236:237]\n\t"
"v_pk_fma_f32 v[238:239], v[236:237], s[58:59], v[238:239] neg_lo:[0,0,1] neg_hi:[0,0,1]\n\t"
"v_pk_fma_f32 v[98:99], v[40:41], s[60:61], v[236:237]\n\t"
"v_pk_fma_f32 v[40:41], v[236:237], s[60:61], v[40:41]\n\t"
"v_pk_fma_f32 v[236:237], v[140:141], s[62:63], v[238:239]\n\t"
"v_pk_fma_f32 v[140:141], v[238:239], s[62:63], v[140:141]\n\t"
"v_mfma_f32_16x16x32_bf16 a[140:143], v[240:243], a[184:187], a[140:143]\n\t"
"v_cvt_pk_bf16_f32 v206, v224, v225\n\t"
"v_cvt_pk_bf16_f32 v210, v228, v229\n\t"
"v_mfma_f32_16x16x32_bf16 a[140:143], a[252:255], a[188:191], a[140:143]\n\t"
"v_cvt_pk_bf16_f32 v207, v230, v231\n\t"
"v_cvt_pk_bf16_f32 v211, v234, v235\n\t"
"v_mfma_f32_16x16x32_bf16 a[140:143], a[224:227], a[192:195], a[140:143]\n\t"
"v_accvgpr_read_b32 v224, a58\n\t"
"v_accvgpr_read_b32 v226, a62\n\t"
"v_accvgpr_read_b32 v225, a59\n\t"
"v_accvgpr_read_b32 v227, a63\n\t"
"ds_read_b128 a[180:183], v244 offset:19968\n\t"
"s_mov_b32 s20, 638976\n\t"
"buffer_load_dwordx4 a[44:47], v252, s[8:11], s20 offen offset:0 sc1\n\t"
"v_pk_fma_f32 v[224:225], v[226:227], s[56:57], v[224:225]\n\t"
"v_pk_fma_f32 v[226:227], v[224:225], s[58:59], v[226:227] neg_lo:[0,0,1] neg_hi:[0,0,1]\n\t"
"v_pk_fma_f32 v[228:229], v[42:43], s[60:61], v[224:225]\n\t"
"v_pk_fma_f32 v[42:43], v[224:225], s[60:61], v[42:43]\n\t"
"v_pk_fma_f32 v[224:225], v[142:143], s[62:63], v[226:227]\n\t"
"v_pk_fma_f32 v[142:143], v[226:227], s[62:63], v[142:143]\n\t"
"v_mfma_f32_16x16x32_bf16 a[148:151], v[240:243], a[188:191], a[148:151]\n\t"
"v_cvt_pk_bf16_f32 v214, v236, v237\n\t"
"v_cvt_pk_bf16_f32 v218, v98, v99\n\t"
"v_mfma_f32_16x16x32_bf16 a[148:151], a[252:255], a[192:195], a[148:151]\n\t"
"v_accvgpr_read_b32 v230, a64\n\t"
"v_accvgpr_read_b32 v232, a68\n\t"
"v_mfma_f32_16x16x32_bf16 a[148:151], a[224:227], a[196:199], a[148:151]\n\t"
"v_accvgpr_read_b32 v231, a65\n\t"
"v_accvgpr_read_b32 v233, a69\n\t"
"ds_read_b128 a[184:187], v244 offset:20480\n\t"
"buffer_load_dwordx4 a[48:51], v252, s[8:11], s20 offen offset:1024 sc1\n\t"
"v_pk_fma_f32 v[230:231], v[232:233], s[56:57], v[230:231]\n\t"
"v_pk_fma_f32 v[232:233], v[230:231], s[58:59], v[232:233] neg_lo:[0,0,1] neg_hi:[0,0,1]\n\t"
"v_pk_fma_f32 v[234:235], v[44:45], s[60:61], v[230:231]\n\t"
"v_pk_fma_f32 v[44:45], v[230:231], s[60:61], v[44:45]\n\t"
"v_pk_fma_f32 v[230:231], v[144:145], s[62:63], v[232:233]\n\t"
"v_pk_fma_f32 v[144:145], v[232:233], s[62:63], v[144:145]\n\t"
"v_mfma_f32_16x16x32_bf16 a[156:159], v[240:243], a[192:195], a[156:159]\n\t"
"v_cvt_pk_bf16_f32 v215, v224, v225\n\t"
"v_cvt_pk_bf16_f32 v219, v228, v229\n\t"
"v_mfma_f32_16x16x32_bf16 a[156:159], a[252:255], a[196:199], a[156:159]\n\t"
"v_accvgpr_read_b32 v236, a66\n\t"
"v_accvgpr_read_b32 v238, a70\n\t"
"v_mfma_f32_16x16x32_bf16 a[156:159], a[224:227], a[200:203], a[156:159]\n\t"
"v_accvgpr_read_b32 v237, a67\n\t"
"v_accvgpr_read_b32 v239, a71\n\t"
"ds_read_b128 a[188:191], v244 offset:20992\n\t"
"buffer_load_dwordx4 a[56:59], v252, s[8:11], s20 offen offset:2048 sc1\n\t"
"v_cvt_pk_bf16_f32 v230, v230, v231\n\t"
"v_cvt_pk_bf16_f32 v234, v234, v235\n\t"
"v_pk_fma_f32 v[236:237], v[238:239], s[56:57], v[236:237]\n\t"
"v_pk_fma_f32 v[238:239], v[236:237], s[58:59], v[238:239] neg_lo:[0,0,1] neg_hi:[0,0,1]\n\t"
"v_pk_fma_f32 v[98:99], v[46:47], s[60:61], v[236:237]\n\t"
"v_pk_fma_f32 v[46:47], v[236:237], s[60:61], v[46:47]\n\t"
"v_pk_fma_f32 v[236:237], v[146:147], s[62:63], v[238:239]\n\t"
"v_pk_fma_f32 v[146:147], v[238:239], s[62:63], v[146:147]\n\t"
"v_mfma_f32_16x16x32_bf16 a[164:167], v[240:243], a[196:199], a[164:167]\n\t"
"v_accvgpr_write_b32 a2, v230\n\t"
"v_accvgpr_write_b32 a6, v234\n\t"
"v_mfma_f32_16x16x32_bf16 a[164:167], a[252:255], a[200:203], a[164:167]\n\t"
"v_accvgpr_read_b32 v224, a72\n\t"
"v_accvgpr_read_b32 v226, a76\n\t"
"v_mfma_f32_16x16x32_bf16 a[164:167], a[224:227], a[204:207], a[164:167]\n\t"
"v_accvgpr_read_b32 v225, a73\n\t"
"v_accvgpr_read_b32 v227, a77\n\t"
"ds_read_b128 a[192:195], v244 offset:21504\n\t"
"s_mov_b32 s20, 651264\n\t"
"buffer_load_dwordx4 a[60:63], v252, s[8:11], s20 offen offset:0 sc1\n\t"
"v_cvt_pk_bf16_f32 v236, v236, v237\n\t"
"v_cvt_pk_bf16_f32 v98, v98, v99\n\t"
"v_pk_fma_f32 v[224:225], v[226:227], s[56:57], v[224:225]\n\t"
"v_pk_fma_f32 v[226:227], v[224:225], s[58:59], v[226:227] neg_lo:[0,0,1] neg_hi:[0,0,1]\n\t"
"v_pk_fma_f32 v[228:229], v[48:49], s[60:61], v[224:225]\n\t"
"v_pk_fma_f32 v[48:49], v[224:225], s[60:61], v[48:49]\n\t"
"v_pk_fma_f32 v[224:225], v[148:149], s[62:63], v[226:227]\n\t"
"v_pk_fma_f32 v[148:149], v[226:227], s[62:63], v[148:149]\n\t"
"Lst32_%=:\n\t"
"s_waitcnt vmcnt(13) lgkmcnt(4)\n\t"
"v_mfma_f32_16x16x32_bf16 v[0:3], v[248:251], a[168:171], v[0:3]\n\t"
"v_accvgpr_write_b32 a3, v236\n\t"
"v_accvgpr_write_b32 a7, v98\n\t"
"v_mfma_f32_16x16x32_bf16 v[0:3], a[208:211], a[172:175], v[0:3]\n\t"
"v_accvgpr_read_b32 v230, a74\n\t"
"v_accvgpr_read_b32 v232, a78\n\t"
"v_mfma_f32_16x16x32_bf16 v[0:3], a[212:215], a[176:179], v[0:3]\n\t"
"v_accvgpr_read_b32 v231, a75\n\t"
"v_accvgpr_read_b32 v233, a79\n\t"
"ds_read_b128 a[196:199], v244 offset:22016\n\t"
"buffer_load_dwordx4 a[252:255], v252, s[8:11], s20 offen offset:1024 sc1\n\t"
"v_cvt_pk_bf16_f32 v224, v224, v225\n\t"
"v_cvt_pk_bf16_f32 v228, v228, v229\n\t"
"v_pk_fma_f32 v[230:231], v[232:233], s[56:57], v[230:231]\n\t"
"v_pk_fma_f32 v[232:233], v[230:231], s[58:59], v[232:233] neg_lo:[0,0,1] neg_hi:[0,0,1]\n\t"
"v_pk_fma_f32 v[234:235], v[50:51], s[60:61], v[230:231]\n\t"
"v_pk_fma_f32 v[50:51], v[230:231], s[60:61], v[50:51]\n\t"
"v_pk_fma_f32 v[230:231], v[150:151], s[62:63], v[232:233]\n\t"
"v_pk_fma_f32 v[150:151], v[232:233], s[62:63], v[150:151]\n\t"
"s_waitcnt lgkmcnt(4)\n\t"
"v_mfma_f32_16x16x32_bf16 v[4:7], v[248:251], a[172:175], v[4:7]\n\t"
"v_accvgpr_write_b32 a10, v224\n\t"
"v_accvgpr_write_b32 a14, v228\n\t"
"v_mfma_f32_16x16x32_bf16 v[4:7], a[208:211], a[176:179], v[4:7]\n\t"
"v_accvgpr_read_b32 v236, a80\n\t"
"v_accvgpr_read_b32 v238, a84\n\t"
"v_mfma_f32_16x16x32_bf16 v[4:7], a[212:215], a[180:183], v[4:7]\n\t"
"v_accvgpr_read_b32 v237, a81\n\t"
"v_accvgpr_read_b32 v239, a85\n\t"
"ds_read_b128 a[200:203], v244 offset:22528\n\t"
"buffer_load_dwordx4 a[224:227], v252, s[8:11], s20 offen offset:2048 sc1\n\t"
"v_cvt_pk_bf16_f32 v230, v230, v231\n\t"
"v_cvt_pk_bf16_f32 v234, v234, v235\n\t"
"v_pk_fma_f32 v[236:237], v[238:239], s[56:57], v[236:237]\n\t"
"v_pk_fma_f32 v[238:239], v[236:237], s[58:59], v[238:239] neg_lo:[0,0,1] neg_hi:[0,0,1]\n\t"
"v_pk_fma_f32 v[98:99], v[52:53], s[60:61], v[236:237]\n\t"
"v_pk_fma_f32 v[52:53], v[236:237], s[60:61], v[52:53]\n\t"
"v_pk_fma_f32 v[236:237], v[152:153], s[62:63], v[238:239]\n\t"
"v_pk_fma_f32 v[152:153], v[238:239], s[62:63], v[152:153]\n\t"
"s_waitcnt lgkmcnt(4)\n\t"
"v_mfma_f32_16x16x32_bf16 v[8:11], v[248:251], a[176:179], v[8:11]\n\t"
"v_accvgpr_write_b32 a11, v230\n\t"
"v_accvgpr_write_b32 a15, v234\n\t"
"v_mfma_f32_16x16x32_bf16 v[8:11], a[208:211], a[180:183], v[8:11]\n\t"
"v_accvgpr_read_b32 v224, a82\n\t"
"v_accvgpr_read_b32 v226, a86\n\t"
"v_mfma_f32_16x16x32_bf16 v[8:11], a[212:215], a[184:187], v[8:11]\n\t"
"v_accvgpr_read_b32 v225, a83\n\t"
"v_accvgpr_read_b32 v227, a87\n\t"
"ds_read_b128 a[204:207], v244 offset:23040\n\t"
"s_mov_b32 s20, 663552\n\t"
"buffer_load_dwordx4 a[64:67], v252, s[8:11], s20 offen offset:0 sc1\n\t"
"v_cvt_pk_bf16_f32 v236, v236, v237\n\t"
"v_cvt_pk_bf16_f32 v98, v98, v99\n\t"
"v_pk_fma_f32 v[224:225], v[226:227], s[56:57], v[224:225]\n\t"
"v_pk_fma_f32 v[226:227], v[224:225], s[58:59], v[226:227] neg_lo:[0,0,1] neg_hi:[0,0,1]\n\t"
"v_pk_fma_f32 v[228:229], v[54:55], s[60:61], v[224:225]\n\t"
"v_pk_fma_f32 v[54:55], v[224:225], s[60:61], v[54:55]\n\t"
"v_pk_fma_f32 v[224:225], v[154:155], s[62:63], v[226:227]\n\t"
"v_pk_fma_f32 v[154:155], v[226:227], s[62:63], v[154:155]\n\t"
"s_waitcnt lgkmcnt(4)\n\t"
"v_mfma_f32_16x16x32_bf16 v[12:15], v[248:251], a[180:183], v[12:15]\n\t"
"v_accvgpr_write_b32 a18, v236\n\t"
"v_accvgpr_write_b32 a22, v98\n\t"
"v_mfma_f32_16x16x32_bf16 v[12:15], a[208:211], a[184:187], v[12:15]\n\t"
"v_accvgpr_read_b32 v230, a88\n\t"
"v_accvgpr_read_b32 v232, a92\n\t"
"v_mfma_f32_16x16x32_bf16 v[12:15], a[212:215], a[188:191], v[12:15]\n\t"
"v_accvgpr_read_b32 v231, a89\n\t"
"v_accvgpr_read_b32 v233, a93\n\t"
"v_accvgpr_read_b32 v236, a90\n\t"
"v_accvgpr_read_b32 v238, a94\n\t"
"v_accvgpr_read_b32 v237, a91\n\t"
"v_accvgpr_read_b32 v239, a95\n\t"
"buffer_load_dwordx4 a[68:71], v252, s[8:11], s20 offen offset:1024 sc1\n\t"
"v_cvt_pk_bf16_f32 v224, v224, v225\n\t"
"v_cvt_pk_bf16_f32 v228, v228, v229\n\t"
"v_pk_fma_f32 v[230:231], v[232:233], s[56:57], v[230:231]\n\t"
"v_pk_fma_f32 v[232:233], v[230:231], s[58:59], v[232:233] neg_lo:[0,0,1] neg_hi:[0,0,1]\n\t"
"v_pk_fma_f32 v[234:235], v[56:57], s[60:61], v[230:231]\n\t"
"v_pk_fma_f32 v[56:57], v[230:231], s[60:61], v[56:57]\n\t"
"v_pk_fma_f32 v[230:231], v[156:157], s[62:63], v[232:233]\n\t"
"v_pk_fma_f32 v[156:157], v[232:233], s[62:63], v[156:157]\n\t"
"v_pk_fma_f32 v[236:237], v[238:239], s[56:57], v[236:237]\n\t"
"v_pk_fma_f32 v[238:239], v[236:237], s[58:59], v[238:239] neg_lo:[0,0,1] neg_hi:[0,0,1]\n\t"
"v_pk_fma_f32 v[98:99], v[58:59], s[60:61], v[236:237]\n\t"
"v_pk_fma_f32 v[58:59], v[236:237], s[60:61], v[58:59]\n\t"
"v_pk_fma_f32 v[236:237], v[158:159], s[62:63], v[238:239]\n\t"
"v_pk_fma_f32 v[158:159], v[238:239], s[62:63], v[158:159]\n\t"
"s_waitcnt lgkmcnt(3)\n\t"
"v_mfma_f32_16x16x32_bf16 v[16:19], v[248:251], a[184:187], v[16:19]\n\t"
"v_accvgpr_write_b32 a19, v224\n\t"
"v_accvgpr_write_b32 a23, v228\n\t"
"v_mfma_f32_16x16x32_bf16 v[16:19], a[208:211], a[188:191], v[16:19]\n\t"
"v_accvgpr_read_b32 v224, a96\n\t"
"v_accvgpr_read_b32 v226, a100\n\t"
"v_mfma_f32_16x16x32_bf16 v[16:19], a[212:215], a[192:195], v[16:19]\n\t"
"v_accvgpr_read_b32 v225, a97\n\t"
"v_accvgpr_read_b32 v227, a101\n\t"
"buffer_load_dwordx4 a[72:75], v252, s[8:11], s20 offen offset:2048 sc1\n\t"
"v_cvt_pk_bf16_f32 v230, v230, v231\n\t"
"v_cvt_pk_bf16_f32 v234, v234, v235\n\t"
"v_pk_fma_f32 v[224:225], v[226:227], s[56:57], v[224:225]\n\t"
"v_pk_fma_f32 v[226:227], v[224:225], s[58:59], v[226:227] neg_lo:[0,0,1] neg_hi:[0,0,1]\n\t"
"v_pk_fma_f32 v[228:229], v[60:61], s[60:61], v[224:225]\n\t"
"v_pk_fma_f32 v[60:61], v[224:225], s[60:61], v[60:61]\n\t"
"v_pk_fma_f32 v[224:225], v[160:161], s[62:63], v[226:227]\n\t"
"v_pk_fma_f32 v[160:161], v[226:227], s[62:63], v[160:161]\n\t"
"s_waitcnt lgkmcnt(2)\n\t"
"v_mfma_f32_16x16x32_bf16 v[20:23], v[248:251], a[188:191], v[20:23]\n\t"
"v_accvgpr_write_b32 a26, v230\n\t"
"v_cvt_pk_bf16_f32 v236, v236, v237\n\t"
"v_mfma_f32_16x16x32_bf16 v[20:23], a[208:211], a[192:195], v[20:23]\n\t"
"v_accvgpr_write_b32 a30, v234\n\t"
"v_cvt_pk_bf16_f32 v98, v98, v99\n\t"
"v_mfma_f32_16x16x32_bf16 v[20:23], a[212:215], a[196:199], v[20:23]\n\t"
"v_accvgpr_write_b32 a27, v236\n\t"
"v_accvgpr_write_b32 a31, v98\n\t"
"v_accvgpr_read_b32 v230, a98\n\t"
"v_accvgpr_read_b32 v232, a102\n\t"
"v_accvgpr_read_b32 v231, a99\n\t"
"v_accvgpr_read_b32 v233, a103\n\t"
"s_mov_b32 s20, 675840\n\t"
"buffer_load_dwordx4 a[76:79], v252, s[8:11], s20 offen offset:0 sc1\n\t"
"v_cvt_pk_bf16_f32 v224, v224, v225\n\t"
"v_cvt_pk_bf16_f32 v228, v228, v229\n\t"
"v_pk_fma_f32 v[230:231], v[232:233], s[56:57], v[230:231]\n\t"
"v_pk_fma_f32 v[232:233], v[230:231], s[58:59], v[232:233] neg_lo:[0,0,1] neg_hi:[0,0,1]\n\t"
"v_pk_fma_f32 v[234:235], v[62:63], s[60:61], v[230:231]\n\t"
"v_pk_fma_f32 v[62:63], v[230:231], s[60:61], v[62:63]\n\t"
"v_pk_fma_f32 v[230:231], v[162:163], s[62:63], v[232:233]\n\t"
"v_pk_fma_f32 v[162:163], v[232:233], s[62:63], v[162:163]\n\t"
"s_waitcnt lgkmcnt(1)\n\t"
"v_mfma_f32_16x16x32_bf16 v[24:27], v[248:251], a[192:195], v[24:27]\n\t"
"v_accvgpr_write_b32 a34, v224\n\t"
"v_accvgpr_write_b32 a38, v228\n\t"
"v_mfma_f32_16x16x32_bf16 v[24:27], a[208:211], a[196:199], v[24:27]\n\t"
"v_accvgpr_read_b32 v236, a104\n\t"
"v_accvgpr_read_b32 v238, a108\n\t"
"v_mfma_f32_16x16x32_bf16 v[24:27], a[212:215], a[200:203], v[24:27]\n\t"
"v_accvgpr_read_b32 v237, a105\n\t"
"v_accvgpr_read_b32 v239, a109\n\t"
"buffer_load_dwordx4 a[80:83], v252, s[8:11], s20 offen offset:1024 sc1\n\t"
"v_cvt_pk_bf16_f32 v230, v230, v231\n\t"
"v_cvt_pk_bf16_f32 v234, v234, v235\n\t"
"v_pk_fma_f32 v[236:237], v[238:239], s[56:57], v[236:237]\n\t"
"v_pk_fma_f32 v[238:239], v[236:237], s[58:59], v[238:239] neg_lo:[0,0,1] neg_hi:[0,0,1]\n\t"
"v_pk_fma_f32 v[98:99], v[64:65], s[60:61], v[236:237]\n\t"
"v_pk_fma_f32 v[64:65], v[236:237], s[60:61], v[64:65]\n\t"
"v_pk_fma_f32 v[236:237], v[164:165], s[62:63], v[238:239]\n\t"
"v_pk_fma_f32 v[164:165], v[238:239], s[62:63], v[164:165]\n\t"
"s_waitcnt lgkmcnt(0)\n\t"
"v_mfma_f32_16x16x32_bf16 v[28:31], v[248:251], a[196:199], v[28:31]\n\t"
"v_accvgpr_write_b32 a35, v230\n\t"
"v_accvgpr_write_b32 a39, v234\n\t"
"v_mfma_f32_16x16x32_bf16 v[28:31], a[208:211], a[200:203], v[28:31]\n\t"
"v_accvgpr_read_b32 v224, a106\n\t"
"v_accvgpr_read_b32 v226, a110\n\t"
"v_mfma_f32_16x16x32_bf16 v[28:31], a[212:215], a[204:207], v[28:31]\n\t"
"v_accvgpr_read_b32 v225, a107\n\t"
"v_accvgpr_read_b32 v227, a111\n\t"
"buffer_load_dwordx4 a[84:87], v252, s[8:11], s20 offen offset:2048 sc1\n\t"
"v_cvt_pk_bf16_f32 v236, v236, v237\n\t"
"v_cvt_pk_bf16_f32 v98, v98, v99\n\t"
"v_pk_fma_f32 v[224:225], v[226:227], s[56:57], v[224:225]\n\t"
"v_pk_fma_f32 v[226:227], v[224:225], s[58:59], v[226:227] neg_lo:[0,0,1] neg_hi:[0,0,1]\n\t"
"v_pk_fma_f32 v[228:229], v[66:67], s[60:61], v[224:225]\n\t"
"v_pk_fma_f32 v[66:67], v[224:225], s[60:61], v[66:67]\n\t"
"v_pk_fma_f32 v[224:225], v[166:167], s[62:63], v[226:227]\n\t"
"v_pk_fma_f32 v[166:167], v[226:227], s[62:63], v[166:167]\n\t"
"s_waitcnt vmcnt(18)\n\t"
"v_mfma_f32_16x16x32_bf16 v[32:35], a[216:219], a[168:171], v[32:35]\n\t"
"v_accvgpr_write_b32 a104, v236\n\t"
"v_accvgpr_write_b32 a106, v98\n\t"
"v_mfma_f32_16x16x32_bf16 v[32:35], a[228:231], a[172:175], v[32:35]\n\t"
"v_accvgpr_read_b32 v230, a112\n\t"
"v_accvgpr_read_b32 v232, a116\n\t"
"v_mfma_f32_16x16x32_bf16 v[32:35], a[232:235], a[176:179], v[32:35]\n\t"
"v_accvgpr_read_b32 v231, a113\n\t"
"v_accvgpr_read_b32 v233, a117\n\t"
"s_mov_b32 s20, 688128\n\t"
"buffer_load_dwordx4 a[208:211], v252, s[8:11], s20 offen offset:0 sc1\n\t"
"v_cvt_pk_bf16_f32 v224, v224, v225\n\t"
"v_cvt_pk_bf16_f32 v228, v228, v229\n\t"
"v_pk_fma_f32 v[230:231], v[232:233], s[56:57], v[230:231]\n\t"
"v_pk_fma_f32 v[232:233], v[230:231], s[58:59], v[232:233] neg_lo:[0,0,1] neg_hi:[0,0,1]\n\t"
"v_pk_fma_f32 v[234:235], v[68:69], s[60:61], v[230:231]\n\t"
"v_pk_fma_f32 v[68:69], v[230:231], s[60:61], v[68:69]\n\t"
"v_pk_fma_f32 v[230:231], v[168:169], s[62:63], v[232:233]\n\t"
"v_pk_fma_f32 v[168:169], v[232:233], s[62:63], v[168:169]\n\t"
"v_mfma_f32_16x16x32_bf16 v[36:39], a[216:219], a[172:175], v[36:39]\n\t"
"v_accvgpr_write_b32 a105, v224\n\t"
"v_accvgpr_write_b32 a107, v228\n\t"
"v_mfma_f32_16x16x32_bf16 v[36:39], a[228:231], a[176:179], v[36:39]\n\t"
"v_accvgpr_read_b32 v236, a114\n\t"
"v_accvgpr_read_b32 v238, a118\n\t"
"v_mfma_f32_16x16x32_bf16 v[36:39], a[232:235], a[180:183], v[36:39]\n\t"
"v_accvgpr_read_b32 v237, a115\n\t"
"v_accvgpr_read_b32 v239, a119\n\t"
"buffer_load_dwordx4 a[212:215], v252, s[8:11], s20 offen offset:1024 sc1\n\t"
"v_cvt_pk_bf16_f32 v230, v230, v231\n\t"
"v_cvt_pk_bf16_f32 v234, v234, v235\n\t"
"v_pk_fma_f32 v[236:237], v[238:239], s[56:57], v[236:237]\n\t"
"v_pk_fma_f32 v[238:239], v[236:237], s[58:59], v[238:239] neg_lo:[0,0,1] neg_hi:[0,0,1]\n\t"
"v_pk_fma_f32 v[98:99], v[70:71], s[60:61], v[236:237]\n\t"
"v_pk_fma_f32 v[70:71], v[236:237], s[60:61], v[70:71]\n\t"
"v_pk_fma_f32 v[236:237], v[170:171], s[62:63], v[238:239]\n\t"
"v_pk_fma_f32 v[170:171], v[238:239], s[62:63], v[170:171]\n\t"
"v_mfma_f32_16x16x32_bf16 v[40:43], a[216:219], a[176:179], v[40:43]\n\t"
"v_accvgpr_write_b32 a112, v230\n\t"
"v_accvgpr_write_b32 a114, v234\n\t"
"v_mfma_f32_16x16x32_bf16 v[40:43], a[228:231], a[180:183], v[40:43]\n\t"
"v_accvgpr_read_b32 v224, a120\n\t"
"v_accvgpr_read_b32 v226, a124\n\t"
"v_mfma_f32_16x16x32_bf16 v[40:43], a[232:235], a[184:187], v[40:43]\n\t"
"v_accvgpr_read_b32 v225, a121\n\t"
"v_accvgpr_read_b32 v227, a125\n\t"
"global_load_dwordx4 v[240:243], v252, s[46:47] offset:0\n\t"
"buffer_load_dwordx4 a[88:91], v252, s[8:11], s20 offen offset:2048 sc1\n\t"
"v_cvt_pk_bf16_f32 v236, v236, v237\n\t"
"v_cvt_pk_bf16_f32 v98, v98, v99\n\t"
"v_pk_fma_f32 v[224:225], v[226:227], s[56:57], v[224:225]\n\t"
"v_pk_fma_f32 v[226:227], v[224:225], s[58:59], v[226:227] neg_lo:[0,0,1] neg_hi:[0,0,1]\n\t"
"v_pk_fma_f32 v[228:229], v[72:73], s[60:61], v[224:225]\n\t"
"v_pk_fma_f32 v[72:73], v[224:225], s[60:61], v[72:73]\n\t"
"v_pk_fma_f32 v[224:225], v[172:173], s[62:63], v[226:227]\n\t"
"v_pk_fma_f32 v[172:173], v[226:227], s[62:63], v[172:173]\n\t"
"v_mfma_f32_16x16x32_bf16 v[44:47], a[216:219], a[180:183], v[44:47]\n\t"
"v_accvgpr_write_b32 a113, v236\n\t"
"v_accvgpr_write_b32 a115, v98\n\t"
"v_mfma_f32_16x16x32_bf16 v[44:47], a[228:231], a[184:187], v[44:47]\n\t"
"v_accvgpr_read_b32 v230, a122\n\t"
"v_accvgpr_read_b32 v232, a126\n\t"
"v_mfma_f32_16x16x32_bf16 v[44:47], a[232:235], a[188:191], v[44:47]\n\t"
"v_accvgpr_read_b32 v231, a123\n\t"
"v_accvgpr_read_b32 v233, a127\n\t"
"v_accvgpr_read_b32 v236, a128\n\t"
"v_accvgpr_read_b32 v238, a132\n\t"
"v_accvgpr_read_b32 v237, a129\n\t"
"v_accvgpr_read_b32 v239, a133\n\t"
"s_mov_b32 s20, 847872\n\t"
"buffer_load_dwordx4 a[92:95], v252, s[8:11], s20 offen offset:0 sc1\n\t"
"v_cvt_pk_bf16_f32 v224, v224, v225\n\t"
"v_cvt_pk_bf16_f32 v228, v228, v229\n\t"
"v_pk_fma_f32 v[230:231], v[232:233], s[56:57], v[230:231]\n\t"
"v_pk_fma_f32 v[232:233], v[230:231], s[58:59], v[232:233] neg_lo:[0,0,1] neg_hi:[0,0,1]\n\t"
"v_pk_fma_f32 v[234:235], v[74:75], s[60:61], v[230:231]\n\t"
"v_pk_fma_f32 v[74:75], v[230:231], s[60:61], v[74:75]\n\t"
"v_pk_fma_f32 v[230:231], v[174:175], s[62:63], v[232:233]\n\t"
"v_pk_fma_f32 v[174:175], v[232:233], s[62:63], v[174:175]\n\t"
"v_pk_fma_f32 v[236:237], v[238:239], s[56:57], v[236:237]\n\t"
"v_pk_fma_f32 v[238:239], v[236:237], s[58:59], v[238:239] neg_lo:[0,0,1] neg_hi:[0,0,1]\n\t"
"v_pk_fma_f32 v[98:99], v[76:77], s[60:61], v[236:237]\n\t"
"v_pk_fma_f32 v[76:77], v[236:237], s[60:61], v[76:77]\n\t"
"v_pk_fma_f32 v[236:237], v[176:177], s[62:63], v[238:239]\n\t"
"v_pk_fma_f32 v[176:177], v[238:239], s[62:63], v[176:177]\n\t"
"global_load_dwordx4 a[52:55], v252, s[46:47] offset:1024\n\t"
"v_mfma_f32_16x16x32_bf16 v[48:51], a[216:219], a[184:187], v[48:51]\n\t"
"v_accvgpr_write_b32 a120, v224\n\t"
"v_accvgpr_write_b32 a122, v228\n\t"
"v_mfma_f32_16x16x32_bf16 v[48:51], a[228:231], a[188:191], v[48:51]\n\t"
"v_accvgpr_read_b32 v224, a130\n\t"
"v_accvgpr_read_b32 v226, a134\n\t"
"v_mfma_f32_16x16x32_bf16 v[48:51], a[232:235], a[192:195], v[48:51]\n\t"
"global_load_dwordx4 v[248:251], v252, s[48:49] offset:0\n\t"
"v_accvgpr_read_b32 v225, a131\n\t"
"v_accvgpr_read_b32 v227, a135\n\t"
"buffer_load_dwordx4 a[96:99], v252, s[8:11], s20 offen offset:1024 sc1\n\t"
"v_cvt_pk_bf16_f32 v230, v230, v231\n\t"
"v_cvt_pk_bf16_f32 v234, v234, v235\n\t"
"v_cvt_pk_bf16_f32 v236, v236, v237\n\t"
"v_cvt_pk_bf16_f32 v98, v98, v99\n\t"
"v_pk_fma_f32 v[224:225], v[226:227], s[56:57], v[224:225]\n\t"
"v_pk_fma_f32 v[226:227], v[224:225], s[58:59], v[226:227] neg_lo:[0,0,1] neg_hi:[0,0,1]\n\t"
"v_pk_fma_f32 v[228:229], v[78:79], s[60:61], v[224:225]\n\t"
"v_pk_fma_f32 v[78:79], v[224:225], s[60:61], v[78:79]\n\t"
"v_pk_fma_f32 v[224:225], v[178:179], s[62:63], v[226:227]\n\t"
"v_pk_fma_f32 v[178:179], v[226:227], s[62:63], v[178:179]\n\t"
"v_mfma_f32_16x16x32_bf16 v[52:55], a[216:219], a[188:191], v[52:55]\n\t"
"v_accvgpr_write_b32 a121, v230\n\t"
"v_accvgpr_write_b32 a123, v234\n\t"
"v_mfma_f32_16x16x32_bf16 v[52:55], a[228:231], a[192:195], v[52:55]\n\t"
"v_accvgpr_write_b32 a128, v236\n\t"
"v_accvgpr_write_b32 a130, v98\n\t"
"v_mfma_f32_16x16x32_bf16 v[52:55], a[232:235], a[196:199], v[52:55]\n\t"
"v_accvgpr_read_b32 v230, a136\n\t"
"v_accvgpr_read_b32 v232, a140\n\t"
"v_accvgpr_read_b32 v231, a137\n\t"
"v_accvgpr_read_b32 v233, a141\n\t"
"buffer_load_dwordx4 a[100:103], v252, s[8:11], s20 offen offset:2048 sc1\n\t"
"v_cvt_pk_bf16_f32 v224, v224, v225\n\t"
"v_cvt_pk_bf16_f32 v228, v228, v229\n\t"
"v_pk_fma_f32 v[230:231], v[232:233], s[56:57], v[230:231]\n\t"
"v_pk_fma_f32 v[232:233], v[230:231], s[58:59], v[232:233] neg_lo:[0,0,1] neg_hi:[0,0,1]\n\t"
"v_pk_fma_f32 v[234:235], v[80:81], s[60:61], v[230:231]\n\t"
"v_pk_fma_f32 v[80:81], v[230:231], s[60:61], v[80:81]\n\t"
"v_pk_fma_f32 v[230:231], v[180:181], s[62:63], v[232:233]\n\t"
"v_pk_fma_f32 v[180:181], v[232:233], s[62:63], v[180:181]\n\t"
"v_mfma_f32_16x16x32_bf16 v[56:59], a[216:219], a[192:195], v[56:59]\n\t"
"v_accvgpr_write_b32 a129, v224\n\t"
"v_accvgpr_write_b32 a131, v228\n\t"
"v_mfma_f32_16x16x32_bf16 v[56:59], a[228:231], a[196:199], v[56:59]\n\t"
"v_accvgpr_read_b32 v236, a138\n\t"
"v_accvgpr_read_b32 v238, a142\n\t"
"v_mfma_f32_16x16x32_bf16 v[56:59], a[232:235], a[200:203], v[56:59]\n\t"
"v_accvgpr_read_b32 v237, a139\n\t"
"v_accvgpr_read_b32 v239, a143\n\t"
"s_mov_b32 s20, 860160\n\t"
"buffer_load_dwordx4 a[108:111], v252, s[8:11], s20 offen offset:0 sc1\n\t"
"v_cvt_pk_bf16_f32 v230, v230, v231\n\t"
"v_cvt_pk_bf16_f32 v234, v234, v235\n\t"
"v_pk_fma_f32 v[236:237], v[238:239], s[56:57], v[236:237]\n\t"
"v_pk_fma_f32 v[238:239], v[236:237], s[58:59], v[238:239] neg_lo:[0,0,1] neg_hi:[0,0,1]\n\t"
"v_pk_fma_f32 v[98:99], v[82:83], s[60:61], v[236:237]\n\t"
"v_pk_fma_f32 v[82:83], v[236:237], s[60:61], v[82:83]\n\t"
"v_pk_fma_f32 v[236:237], v[182:183], s[62:63], v[238:239]\n\t"
"v_pk_fma_f32 v[182:183], v[238:239], s[62:63], v[182:183]\n\t"
"v_mfma_f32_16x16x32_bf16 v[60:63], a[216:219], a[196:199], v[60:63]\n\t"
"v_accvgpr_write_b32 a136, v230\n\t"
"v_accvgpr_write_b32 a138, v234\n\t"
"v_mfma_f32_16x16x32_bf16 v[60:63], a[228:231], a[200:203], v[60:63]\n\t"
"v_accvgpr_read_b32 v224, a144\n\t"
"v_accvgpr_read_b32 v226, a148\n\t"
"v_mfma_f32_16x16x32_bf16 v[60:63], a[232:235], a[204:207], v[60:63]\n\t"
"v_accvgpr_read_b32 v225, a145\n\t"
"v_accvgpr_read_b32 v227, a149\n\t"
"buffer_load_dwordx4 a[116:119], v252, s[8:11], s20 offen offset:1024 sc1\n\t"
"v_cvt_pk_bf16_f32 v236, v236, v237\n\t"
"v_cvt_pk_bf16_f32 v98, v98, v99\n\t"
"v_pk_fma_f32 v[224:225], v[226:227], s[56:57], v[224:225]\n\t"
"v_pk_fma_f32 v[226:227], v[224:225], s[58:59], v[226:227] neg_lo:[0,0,1] neg_hi:[0,0,1]\n\t"
"v_pk_fma_f32 v[228:229], v[84:85], s[60:61], v[224:225]\n\t"
"v_pk_fma_f32 v[84:85], v[224:225], s[60:61], v[84:85]\n\t"
"v_pk_fma_f32 v[224:225], v[184:185], s[62:63], v[226:227]\n\t"
"v_pk_fma_f32 v[184:185], v[226:227], s[62:63], v[184:185]\n\t"
"s_waitcnt vmcnt(26)\n\t"
"v_mfma_f32_16x16x32_bf16 v[64:67], a[236:239], a[168:171], v[64:67]\n\t"
"v_accvgpr_write_b32 a137, v236\n\t"
"v_accvgpr_write_b32 a139, v98\n\t"
"v_mfma_f32_16x16x32_bf16 v[64:67], a[240:243], a[172:175], v[64:67]\n\t"
"v_accvgpr_read_b32 v230, a146\n\t"
"v_accvgpr_read_b32 v232, a150\n\t"
"v_mfma_f32_16x16x32_bf16 v[64:67], a[244:247], a[176:179], v[64:67]\n\t"
"v_accvgpr_read_b32 v231, a147\n\t"
"v_accvgpr_read_b32 v233, a151\n\t"
"buffer_load_dwordx4 a[216:219], v252, s[8:11], s20 offen offset:2048 sc1\n\t"
"v_cvt_pk_bf16_f32 v224, v224, v225\n\t"
"v_cvt_pk_bf16_f32 v228, v228, v229\n\t"
"v_pk_fma_f32 v[230:231], v[232:233], s[56:57], v[230:231]\n\t"
"v_pk_fma_f32 v[232:233], v[230:231], s[58:59], v[232:233] neg_lo:[0,0,1] neg_hi:[0,0,1]\n\t"
"v_pk_fma_f32 v[234:235], v[86:87], s[60:61], v[230:231]\n\t"
"v_pk_fma_f32 v[86:87], v[230:231], s[60:61], v[86:87]\n\t"
"v_pk_fma_f32 v[230:231], v[186:187], s[62:63], v[232:233]\n\t"
"v_pk_fma_f32 v[186:187], v[232:233], s[62:63], v[186:187]\n\t"
"v_mfma_f32_16x16x32_bf16 v[68:71], a[236:239], a[172:175], v[68:71]\n\t"
"v_accvgpr_write_b32 a144, v224\n\t"
"v_accvgpr_write_b32 a146, v228\n\t"
"v_mfma_f32_16x16x32_bf16 v[68:71], a[240:243], a[176:179], v[68:71]\n\t"
"v_accvgpr_read_b32 v236, a152\n\t"
"v_accvgpr_read_b32 v238, a156\n\t"
"v_mfma_f32_16x16x32_bf16 v[68:71], a[244:247], a[180:183], v[68:71]\n\t"
"v_accvgpr_read_b32 v237, a153\n\t"
"v_accvgpr_read_b32 v239, a157\n\t"
"s_mov_b32 s20, 872448\n\t"
"buffer_load_dwordx4 a[228:231], v252, s[8:11], s20 offen offset:0 sc1\n\t"
"v_cvt_pk_bf16_f32 v230, v230, v231\n\t"
"v_cvt_pk_bf16_f32 v234, v234, v235\n\t"
"v_pk_fma_f32 v[236:237], v[238:239], s[56:57], v[236:237]\n\t"
"v_pk_fma_f32 v[238:239], v[236:237], s[58:59], v[238:239] neg_lo:[0,0,1] neg_hi:[0,0,1]\n\t"
"v_pk_fma_f32 v[98:99], v[88:89], s[60:61], v[236:237]\n\t"
"v_pk_fma_f32 v[88:89], v[236:237], s[60:61], v[88:89]\n\t"
"v_pk_fma_f32 v[236:237], v[188:189], s[62:63], v[238:239]\n\t"
"v_pk_fma_f32 v[188:189], v[238:239], s[62:63], v[188:189]\n\t"
"v_mfma_f32_16x16x32_bf16 v[72:75], a[236:239], a[176:179], v[72:75]\n\t"
"v_accvgpr_write_b32 a145, v230\n\t"
"v_accvgpr_write_b32 a147, v234\n\t"
"v_mfma_f32_16x16x32_bf16 v[72:75], a[240:243], a[180:183], v[72:75]\n\t"
"v_accvgpr_read_b32 v224, a154\n\t"
"v_accvgpr_read_b32 v226, a158\n\t"
"v_mfma_f32_16x16x32_bf16 v[72:75], a[244:247], a[184:187], v[72:75]\n\t"
"v_accvgpr_read_b32 v225, a155\n\t"
"v_accvgpr_read_b32 v227, a159\n\t"
"buffer_load_dwordx4 a[232:235], v252, s[8:11], s20 offen offset:1024 sc1\n\t"
"v_cvt_pk_bf16_f32 v236, v236, v237\n\t"
"v_cvt_pk_bf16_f32 v98, v98, v99\n\t"
"v_pk_fma_f32 v[224:225], v[226:227], s[56:57], v[224:225]\n\t"
"v_pk_fma_f32 v[226:227], v[224:225], s[58:59], v[226:227] neg_lo:[0,0,1] neg_hi:[0,0,1]\n\t"
"v_pk_fma_f32 v[228:229], v[90:91], s[60:61], v[224:225]\n\t"
"v_pk_fma_f32 v[90:91], v[224:225], s[60:61], v[90:91]\n\t"
"v_pk_fma_f32 v[224:225], v[190:191], s[62:63], v[226:227]\n\t"
"v_pk_fma_f32 v[190:191], v[226:227], s[62:63], v[190:191]\n\t"
"v_mfma_f32_16x16x32_bf16 v[76:79], a[236:239], a[180:183], v[76:79]\n\t"
"v_accvgpr_write_b32 a152, v236\n\t"
"v_accvgpr_write_b32 a154, v98\n\t"
"v_mfma_f32_16x16x32_bf16 v[76:79], a[240:243], a[184:187], v[76:79]\n\t"
"v_accvgpr_read_b32 v230, a160\n\t"
"v_accvgpr_read_b32 v232, a164\n\t"
"v_mfma_f32_16x16x32_bf16 v[76:79], a[244:247], a[188:191], v[76:79]\n\t"
"v_accvgpr_read_b32 v231, a161\n\t"
"v_accvgpr_read_b32 v233, a165\n\t"
"ds_read_b128 a[168:171], v244 offset:27648\n\t"
"ds_read_b128 a[172:175], v244 offset:28160\n\t"
"ds_read_b128 a[176:179], v244 offset:28672\n\t"
"buffer_load_dwordx4 a[124:127], v252, s[8:11], s20 offen offset:2048 sc1\n\t"
"v_cvt_pk_bf16_f32 v224, v224, v225\n\t"
"v_cvt_pk_bf16_f32 v228, v228, v229\n\t"
"v_pk_fma_f32 v[230:231], v[232:233], s[56:57], v[230:231]\n\t"
"v_pk_fma_f32 v[232:233], v[230:231], s[58:59], v[232:233] neg_lo:[0,0,1] neg_hi:[0,0,1]\n\t"
"v_pk_fma_f32 v[234:235], v[92:93], s[60:61], v[230:231]\n\t"
"v_pk_fma_f32 v[92:93], v[230:231], s[60:61], v[92:93]\n\t"
"v_pk_fma_f32 v[230:231], v[192:193], s[62:63], v[232:233]\n\t"
"v_pk_fma_f32 v[192:193], v[232:233], s[62:63], v[192:193]\n\t"
"v_mfma_f32_16x16x32_bf16 v[80:83], a[236:239], a[184:187], v[80:83]\n\t"
"v_accvgpr_write_b32 a153, v224\n\t"
"v_accvgpr_write_b32 a155, v228\n\t"
"v_mfma_f32_16x16x32_bf16 v[80:83], a[240:243], a[188:191], v[80:83]\n\t"
"v_accvgpr_read_b32 v236, a162\n\t"
"v_accvgpr_read_b32 v238, a166\n\t"
"v_mfma_f32_16x16x32_bf16 v[80:83], a[244:247], a[192:195], v[80:83]\n\t"
"v_accvgpr_read_b32 v237, a163\n\t"
"v_accvgpr_read_b32 v239, a167\n\t"
"s_mov_b32 s20, 884736\n\t"
"buffer_load_dwordx4 a[132:135], v252, s[8:11], s20 offen offset:0 sc1\n\t"
"v_cvt_pk_bf16_f32 v230, v230, v231\n\t"
"v_cvt_pk_bf16_f32 v234, v234, v235\n\t"
"v_pk_fma_f32 v[236:237], v[238:239], s[56:57], v[236:237]\n\t"
"v_pk_fma_f32 v[238:239], v[236:237], s[58:59], v[238:239] neg_lo:[0,0,1] neg_hi:[0,0,1]\n\t"
"v_pk_fma_f32 v[98:99], v[94:95], s[60:61], v[236:237]\n\t"
"v_pk_fma_f32 v[94:95], v[236:237], s[60:61], v[94:95]\n\t"
"v_pk_fma_f32 v[236:237], v[194:195], s[62:63], v[238:239]\n\t"
"v_pk_fma_f32 v[194:195], v[238:239], s[62:63], v[194:195]\n\t"
"v_mfma_f32_16x16x32_bf16 v[84:87], a[236:239], a[188:191], v[84:87]\n\t"
"ds_read_b128 a[180:183], v244 offset:29184\n\t"
"v_accvgpr_write_b32 a160, v230\n\t"
"v_mfma_f32_16x16x32_bf16 v[84:87], a[240:243], a[192:195], v[84:87]\n\t"
"v_accvgpr_write_b32 a162, v234\n\t"
"v_cvt_pk_bf16_f32 v236, v236, v237\n\t"
"v_mfma_f32_16x16x32_bf16 v[84:87], a[244:247], a[196:199], v[84:87]\n\t"
"buffer_load_dwordx4 a[140:143], v252, s[8:11], s20 offen offset:1024 sc1\n\t"
"v_cvt_pk_bf16_f32 v98, v98, v99\n\t"
"ds_read_b128 a[184:187], v244 offset:29696\n\t"
"v_mfma_f32_16x16x32_bf16 v[88:91], a[236:239], a[192:195], v[88:91]\n\t"
"v_accvgpr_write_b32 a161, v236\n\t"
"v_accvgpr_write_b32 a163, v98\n\t"
"v_mfma_f32_16x16x32_bf16 v[88:91], a[240:243], a[196:199], v[88:91]\n\t"
"s_waitcnt vmcnt(14)\n\t"
"ds_read_b64_tr_b16 v[236:237], v240 offset:1536\n\t"
"v_mfma_f32_16x16x32_bf16 v[88:91], a[244:247], a[200:203], v[88:91]\n\t"
"ds_read_b64_tr_b16 v[238:239], v240 offset:3072\n\t"
"buffer_load_dwordx4 a[148:151], v252, s[8:11], s20 offen offset:2048 sc1\n\t"
"ds_read_b64_tr_b16 v[224:225], v241 offset:1536\n\t"
"ds_read_b64_tr_b16 v[226:227], v241 offset:3072\n\t"
"v_mfma_f32_16x16x32_bf16 v[92:95], a[236:239], a[196:199], v[92:95]\n\t"
"ds_read_b128 a[188:191], v244 offset:30208\n\t"
"v_mfma_f32_16x16x32_bf16 v[92:95], a[240:243], a[200:203], v[92:95]\n\t"
"ds_read_b128 a[192:195], v244 offset:30720\n\t"
"v_mfma_f32_16x16x32_bf16 v[92:95], a[244:247], a[204:207], v[92:95]\n\t"
"s_mov_b32 s20, 1105920\n\t"
"buffer_load_dwordx4 a[156:159], v252, s[8:11], s20 offen offset:0 sc1\n\t"
"s_waitcnt lgkmcnt(8)\n\t"
"v_mfma_f32_16x16x32_bf16 v[100:103], a[220:223], a[168:171], v[100:103]\n\t"
"ds_read_b64_tr_b16 v[232:233], v240 offset:21504\n\t"
"v_mfma_f32_16x16x32_bf16 v[100:103], a[248:251], a[172:175], v[100:103]\n\t"
"ds_read_b64_tr_b16 v[234:235], v240 offset:23040\n\t"
"v_mfma_f32_16x16x32_bf16 v[100:103], a[40:43], a[176:179], v[100:103]\n\t"
"buffer_load_dwordx4 a[236:239], v252, s[8:11], s20 offen offset:1024 sc1\n\t"
"ds_read_b128 a[196:199], v244 offset:31232\n\t"
"ds_read_b128 a[200:203], v244 offset:31744\n\t"
"s_waitcnt vmcnt(14) lgkmcnt(8)\n\t"
"v_mfma_f32_16x16x32_bf16 v[236:239], v[236:239], a[52:55], 0\n\t"
"ds_read_b128 a[204:207], v244 offset:32256\n\t"
"ds_read_b64_tr_b16 v[228:229], v241 offset:21504\n\t"
"s_waitcnt lgkmcnt(8)\n\t"
"v_mfma_f32_16x16x32_bf16 v[224:227], v[224:227], a[52:55], 0\n\t"
"ds_read_b64_tr_b16 v[230:231], v241 offset:23040\n\t"
"buffer_load_dwordx4 a[240:243], v252, s[8:11], s20 offen offset:2048 sc1\n\t"
"v_mfma_f32_16x16x32_bf16 v[104:107], a[220:223], a[172:175], v[104:107]\n\t"
"v_mfma_f32_16x16x32_bf16 v[104:107], a[248:251], a[176:179], v[104:107]\n\t"
"v_mfma_f32_16x16x32_bf16 v[104:107], a[40:43], a[180:183], v[104:107]\n\t"
"s_mov_b32 s20, 897024\n\t"
"buffer_load_dwordx4 a[244:247], v252, s[8:11], s20 offen offset:0 sc1\n\t"
"s_waitcnt lgkmcnt(2)\n\t"
"s_barrier\n\t"
"v_mfma_f32_16x16x32_bf16 v[108:111], a[220:223], a[176:179], v[108:111]\n\t"
"v_cvt_pk_bf16_f32 v236, v236, v237\n\t"
"v_cvt_pk_bf16_f32 v237, v238, v239\n\t"
"v_mfma_f32_16x16x32_bf16 v[108:111], a[248:251], a[180:183], v[108:111]\n\t"
"v_cvt_pk_bf16_f32 v238, v224, v225\n\t"
"v_cvt_pk_bf16_f32 v239, v226, v227\n\t"
"v_mfma_f32_16x16x32_bf16 v[108:111], a[40:43], a[184:187], v[108:111]\n\t"
"buffer_load_dwordx4 a[164:167], v252, s[8:11], s20 offen offset:1024 sc1\n\t"
"v_mfma_f32_16x16x32_bf16 v[232:235], v[232:235], a[52:55], 0\n\t"
"buffer_load_dwordx4 v[96:99], v252, s[8:11], s20 offen offset:2048 sc1\n\t"
"s_waitcnt lgkmcnt(0)\n\t"
"v_mfma_f32_16x16x32_bf16 v[228:231], v[228:231], a[52:55], 0\n\t"
"s_mov_b64 exec, s[44:45]\n\t"
"ds_write_b128 v242, v[236:239] offset:0\n\t"
"s_mov_b64 exec, -1\n\t"
"v_mfma_f32_16x16x32_bf16 v[112:115], a[220:223], a[180:183], v[112:115]\n\t"
"ds_read_b64_tr_b16 v[224:225], v240 offset:41472\n\t"
"v_mfma_f32_16x16x32_bf16 v[112:115], a[248:251], a[184:187], v[112:115]\n\t"
"ds_read_b64_tr_b16 v[226:227], v240 offset:43008\n\t"
"v_mfma_f32_16x16x32_bf16 v[112:115], a[40:43], a[188:191], v[112:115]\n\t"
"ds_read_b64_tr_b16 v[236:237], v241 offset:41472\n\t"
"ds_read_b64_tr_b16 v[238:239], v241 offset:43008\n\t"
"v_mfma_f32_16x16x32_bf16 v[116:119], a[220:223], a[184:187], v[116:119]\n\t"
"v_cvt_pk_bf16_f32 v232, v232, v233\n\t"
"v_cvt_pk_bf16_f32 v233, v234, v235\n\t"
"v_mfma_f32_16x16x32_bf16 v[116:119], a[248:251], a[188:191], v[116:119]\n\t"
"v_cvt_pk_bf16_f32 v234, v228, v229\n\t"
"v_cvt_pk_bf16_f32 v235, v230, v231\n\t"
"v_mfma_f32_16x16x32_bf16 v[116:119], a[40:43], a[192:195], v[116:119]\n\t"
"v_add_u32 v240, 59904, v240\n\t"
"v_add_u32 v241, 59904, v241\n\t"
"v_mfma_f32_16x16x32_bf16 v[120:123], a[220:223], a[188:191], v[120:123]\n\t"
"ds_read_b64_tr_b16 v[228:229], v240 offset:1536\n\t"
"v_mfma_f32_16x16x32_bf16 v[120:123], a[248:251], a[192:195], v[120:123]\n\t"
"ds_read_b64_tr_b16 v[230:231], v240 offset:3072\n\t"
"v_mfma_f32_16x16x32_bf16 v[120:123], a[40:43], a[196:199], v[120:123]\n\t"
"s_waitcnt vmcnt(17)\n\t"
"ds_write_b128 v249, v[196:199] offset:18432\n\t"
"v_mfma_f32_16x16x32_bf16 v[124:127], a[220:223], a[192:195], v[124:127]\n\t"
"v_mfma_f32_16x16x32_bf16 v[124:127], a[248:251], a[196:199], v[124:127]\n\t"
"v_mfma_f32_16x16x32_bf16 v[124:127], a[40:43], a[200:203], v[124:127]\n\t"
"s_mov_b64 exec, s[44:45]\n\t"
"ds_write_b128 v242, v[232:235] offset:1536\n\t"
"s_mov_b64 exec, -1\n\t"
"s_waitcnt lgkmcnt(6)\n\t"
"v_mfma_f32_16x16x32_bf16 v[224:227], v[224:227], a[52:55], 0\n\t"
"ds_read_b64_tr_b16 v[232:233], v241 offset:1536\n\t"
"ds_read_b64_tr_b16 v[234:235], v241 offset:3072\n\t"
"s_waitcnt lgkmcnt(6)\n\t"
"v_mfma_f32_16x16x32_bf16 v[236:239], v[236:239], a[52:55], 0\n\t"
"ds_write_b64 v250, a[104:105] offset:18432\n\t"
"v_mfma_f32_16x16x32_bf16 v[128:131], a[220:223], a[196:199], v[128:131]\n\t"
"v_mfma_f32_16x16x32_bf16 v[128:131], a[248:251], a[200:203], v[128:131]\n\t"
"v_mfma_f32_16x16x32_bf16 v[128:131], a[40:43], a[204:207], v[128:131]\n\t"
"s_mov_b32 s20, 1118208\n\t"
"buffer_load_dwordx4 a[220:223], v252, s[8:11], s20 offen offset:0 sc1\n\t"
"v_mfma_f32_16x16x32_bf16 v[132:135], a[44:47], a[168:171], v[132:135]\n\t"
"v_cvt_pk_bf16_f32 v224, v224, v225\n\t"
"v_cvt_pk_bf16_f32 v225, v226, v227\n\t"
"v_mfma_f32_16x16x32_bf16 v[132:135], a[48:51], a[172:175], v[132:135]\n\t"
"v_cvt_pk_bf16_f32 v226, v236, v237\n\t"
"v_cvt_pk_bf16_f32 v227, v238, v239\n\t"
"v_mfma_f32_16x16x32_bf16 v[132:135], a[56:59], a[176:179], v[132:135]\n\t"
"buffer_load_dwordx4 a[248:251], v252, s[8:11], s20 offen offset:1024 sc1\n\t"
"s_waitcnt lgkmcnt(5)\n\t"
"v_mfma_f32_16x16x32_bf16 v[228:231], v[228:231], a[52:55], 0\n\t"
"s_mov_b64 exec, s[44:45]\n\t"
"ds_write_b128 v242, v[224:227] offset:3072\n\t"
"s_mov_b64 exec, -1\n\t"
"s_waitcnt lgkmcnt(2)\n\t"
"v_mfma_f32_16x16x32_bf16 v[232:235], v[232:235], a[52:55], 0\n\t"
"ds_read_b64_tr_b16 v[236:237], v240 offset:21504\n\t"
"ds_read_b64_tr_b16 v[238:239], v240 offset:23040\n\t"
"v_mfma_f32_16x16x32_bf16 v[136:139], a[44:47], a[172:175], v[136:139]\n\t"
"ds_read_b64_tr_b16 v[224:225], v241 offset:21504\n\t"
"v_mfma_f32_16x16x32_bf16 v[136:139], a[48:51], a[176:179], v[136:139]\n\t"
"ds_read_b64_tr_b16 v[226:227], v241 offset:23040\n\t"
"v_mfma_f32_16x16x32_bf16 v[136:139], a[56:59], a[180:183], v[136:139]\n\t"
"ds_write_b128 v249, v[200:203] offset:24576\n\t"
"v_mfma_f32_16x16x32_bf16 v[140:143], a[44:47], a[176:179], v[140:143]\n\t"
"v_cvt_pk_bf16_f32 v228, v228, v229\n\t"
"v_cvt_pk_bf16_f32 v229, v230, v231\n\t"
"v_mfma_f32_16x16x32_bf16 v[140:143], a[48:51], a[180:183], v[140:143]\n\t"
"v_cvt_pk_bf16_f32 v230, v232, v233\n\t"
"v_cvt_pk_bf16_f32 v231, v234, v235\n\t"
"v_mfma_f32_16x16x32_bf16 v[140:143], a[56:59], a[184:187], v[140:143]\n\t"
"s_mov_b64 exec, s[44:45]\n\t"
"ds_write_b128 v242, v[228:231] offset:4608\n\t"
"s_mov_b64 exec, -1\n\t"
"v_mfma_f32_16x16x32_bf16 v[144:147], a[44:47], a[180:183], v[144:147]\n\t"
"ds_read_b64_tr_b16 v[232:233], v240 offset:41472\n\t"
"v_mfma_f32_16x16x32_bf16 v[144:147], a[48:51], a[184:187], v[144:147]\n\t"
"ds_read_b64_tr_b16 v[234:235], v240 offset:43008\n\t"
"v_mfma_f32_16x16x32_bf16 v[144:147], a[56:59], a[188:191], v[144:147]\n\t"
"ds_read_b64_tr_b16 v[228:229], v241 offset:41472\n\t"
"ds_read_b64_tr_b16 v[230:231], v241 offset:43008\n\t"
"v_mfma_f32_16x16x32_bf16 v[148:151], a[44:47], a[184:187], v[148:151]\n\t"
"v_mfma_f32_16x16x32_bf16 v[148:151], a[48:51], a[188:191], v[148:151]\n\t"
"v_mfma_f32_16x16x32_bf16 v[148:151], a[56:59], a[192:195], v[148:151]\n\t"
"ds_write_b64 v250, a[106:107] offset:24576\n\t"
"s_waitcnt lgkmcnt(9)\n\t"
"v_mfma_f32_16x16x32_bf16 v[236:239], v[236:239], a[52:55], 0\n\t"
"s_waitcnt lgkmcnt(7)\n\t"
"v_mfma_f32_16x16x32_bf16 v[224:227], v[224:227], a[52:55], 0\n\t"
"v_mfma_f32_16x16x32_bf16 v[152:155], a[44:47], a[188:191], v[152:155]\n\t"
"v_mfma_f32_16x16x32_bf16 v[152:155], a[48:51], a[192:195], v[152:155]\n\t"
"v_mfma_f32_16x16x32_bf16 v[152:155], a[56:59], a[196:199], v[152:155]\n\t"
"v_mfma_f32_16x16x32_bf16 v[156:159], a[44:47], a[192:195], v[156:159]\n\t"
"s_nop 1\n\t"
"v_cvt_pk_bf16_f32 v236, v236, v237\n\t"
"v_cvt_pk_bf16_f32 v237, v238, v239\n\t"
"v_mfma_f32_16x16x32_bf16 v[156:159], a[48:51], a[196:199], v[156:159]\n\t"
"v_cvt_pk_bf16_f32 v238, v224, v225\n\t"
"v_mfma_f32_16x16x32_bf16 v[156:159], a[56:59], a[200:203], v[156:159]\n\t"
"v_cvt_pk_bf16_f32 v239, v226, v227\n\t"
"s_waitcnt lgkmcnt(3)\n\t"
"v_mfma_f32_16x16x32_bf16 v[232:235], v[232:235], a[52:55], 0\n\t"
"s_waitcnt lgkmcnt(1)\n\t"
"v_mfma_f32_16x16x32_bf16 v[228:231], v[228:231], a[52:55], 0\n\t"
"s_mov_b64 exec, s[44:45]\n\t"
"ds_write_b128 v242, v[236:239] offset:6144\n\t"
"s_mov_b64 exec, -1\n\t"
"v_mfma_f32_16x16x32_bf16 v[160:163], a[44:47], a[196:199], v[160:163]\n\t"
"v_mfma_f32_16x16x32_bf16 v[160:163], a[48:51], a[200:203], v[160:163]\n\t"
"v_mfma_f32_16x16x32_bf16 v[160:163], a[56:59], a[204:207], v[160:163]\n\t"
"v_mfma_f32_16x16x32_bf16 v[164:167], a[60:63], a[168:171], v[164:167]\n\t"
"v_cvt_pk_bf16_f32 v232, v232, v233\n\t"
"v_cvt_pk_bf16_f32 v233, v234, v235\n\t"
"v_mfma_f32_16x16x32_bf16 v[164:167], a[252:255], a[172:175], v[164:167]\n\t"
"v_cvt_pk_bf16_f32 v234, v228, v229\n\t"
"v_cvt_pk_bf16_f32 v235, v230, v231\n\t"
"v_mfma_f32_16x16x32_bf16 v[164:167], a[224:227], a[176:179], v[164:167]\n\t"
"buffer_load_dwordx4 a[56:59], v252, s[8:11], s20 offen offset:2048 sc1\n\t"
"v_mfma_f32_16x16x32_bf16 v[168:171], a[60:63], a[172:175], v[168:171]\n\t"
"v_mfma_f32_16x16x32_bf16 v[168:171], a[252:255], a[176:179], v[168:171]\n\t"
"v_mfma_f32_16x16x32_bf16 v[168:171], a[224:227], a[180:183], v[168:171]\n\t"
"s_mov_b64 exec, s[44:45]\n\t"
"ds_write_b128 v242, v[232:235] offset:7680\n\t"
"s_mov_b64 exec, -1\n\t"
"v_mfma_f32_16x16x32_bf16 v[172:175], a[60:63], a[176:179], v[172:175]\n\t"
"v_mfma_f32_16x16x32_bf16 v[172:175], a[252:255], a[180:183], v[172:175]\n\t"
"v_mfma_f32_16x16x32_bf16 v[172:175], a[224:227], a[184:187], v[172:175]\n\t"
"v_mfma_f32_16x16x32_bf16 v[176:179], a[60:63], a[180:183], v[176:179]\n\t"
"v_mfma_f32_16x16x32_bf16 v[176:179], a[252:255], a[184:187], v[176:179]\n\t"
"v_mfma_f32_16x16x32_bf16 v[176:179], a[224:227], a[188:191], v[176:179]\n\t"
"v_mfma_f32_16x16x32_bf16 v[180:183], a[60:63], a[184:187], v[180:183]\n\t"
"v_mfma_f32_16x16x32_bf16 v[180:183], a[252:255], a[188:191], v[180:183]\n\t"
"v_mfma_f32_16x16x32_bf16 v[180:183], a[224:227], a[192:195], v[180:183]\n\t"
"v_mfma_f32_16x16x32_bf16 v[184:187], a[60:63], a[188:191], v[184:187]\n\t"
"v_mfma_f32_16x16x32_bf16 v[184:187], a[252:255], a[192:195], v[184:187]\n\t"
"v_subrev_u32 v240, 59904, v240\n\t"
"v_subrev_u32 v241, 59904, v241\n\t"
"v_mfma_f32_16x16x32_bf16 v[184:187], a[224:227], a[196:199], v[184:187]\n\t"
"s_waitcnt lgkmcnt(0)\n\t"
"s_barrier\n\t"
"v_mfma_f32_16x16x32_bf16 v[188:191], a[60:63], a[192:195], v[188:191]\n\t"
"ds_read_b128 a[168:171], v244 offset:0\n\t"
"v_mfma_f32_16x16x32_bf16 v[188:191], a[252:255], a[196:199], v[188:191]\n\t"
"ds_read_b128 a[172:175], v244 offset:512\n\t"
"v_mfma_f32_16x16x32_bf16 v[188:191], a[224:227], a[200:203], v[188:191]\n\t"
"ds_read_b128 a[176:179], v244 offset:1024\n\t"
"ds_read_b128 a[180:183], v244 offset:1536\n\t"
"ds_read_b128 a[184:187], v244 offset:2048\n\t"
"ds_read_b128 a[40:43], v252 offset:18432\n\t"
"ds_read_b128 a[188:191], v244 offset:2560\n\t"
"v_mfma_f32_16x16x32_bf16 v[192:195], a[60:63], a[196:199], v[192:195]\n\t"
"ds_read_b128 a[44:47], v252 offset:19456\n\t"
"v_mfma_f32_16x16x32_bf16 v[192:195], a[252:255], a[200:203], v[192:195]\n\t"
"ds_read_b128 a[192:195], v244 offset:3072\n\t"
"v_mfma_f32_16x16x32_bf16 v[192:195], a[224:227], a[204:207], v[192:195]\n\t"
"s_mov_b32 s20, 909312\n\t"
"buffer_load_dwordx4 a[104:107], v252, s[8:11], s20 offen offset:0 sc1\n\t"
"ds_read_b128 a[48:51], v252 offset:20480\n\t"
"ds_read_b128 a[196:199], v244 offset:3584\n\t"
"Lst33_%=:\n\t"
"s_waitcnt lgkmcnt(8)\n\t"
"v_mfma_f32_16x16x32_bf16 v[0:3], a[64:67], a[168:171], v[0:3]\n\t"
"ds_read_b64_tr_b16 v[224:225], v240 offset:1024\n\t"
"v_mfma_f32_16x16x32_bf16 v[0:3], a[68:71], a[172:175], v[0:3]\n\t"
"ds_read_b64_tr_b16 v[226:227], v240 offset:2560\n\t"
"v_mfma_f32_16x16x32_bf16 v[0:3], a[72:75], a[176:179], v[0:3]\n\t"
"buffer_load_dwordx4 a[252:255], v252, s[8:11], s20 offen offset:1024 sc1\n\t"
"ds_write_b128 v249, v[204:207] offset:30720\n\t"
"s_waitcnt lgkmcnt(10)\n\t"
"v_mfma_f32_16x16x32_bf16 v[4:7], a[64:67], a[172:175], v[4:7]\n\t"
"ds_read_b64_tr_b16 v[236:237], v241 offset:1024\n\t"
"v_mfma_f32_16x16x32_bf16 v[4:7], a[68:71], a[176:179], v[4:7]\n\t"
"ds_read_b64_tr_b16 v[238:239], v241 offset:2560\n\t"
"v_mfma_f32_16x16x32_bf16 v[4:7], a[72:75], a[180:183], v[4:7]\n\t"
"buffer_load_dwordx4 a[224:227], v252, s[8:11], s20 offen offset:2048 sc1\n\t"
"ds_read_b128 a[200:203], v244 offset:4096\n\t"
"ds_read_b128 a[204:207], v244 offset:4608\n\t"
"s_waitcnt lgkmcnt(13)\n\t"
"v_mfma_f32_16x16x32_bf16 v[8:11], a[64:67], a[176:179], v[8:11]\n\t"
"v_mfma_f32_16x16x32_bf16 v[8:11], a[68:71], a[180:183], v[8:11]\n\t"
"ds_read_b64_tr_b16 v[228:229], v240 offset:512\n\t"
"v_mfma_f32_16x16x32_bf16 v[8:11], a[72:75], a[184:187], v[8:11]\n\t"
"s_mov_b32 s20, 1130496\n\t"
"buffer_load_dwordx4 a[60:63], v252, s[8:11], s20 offen offset:0 sc1\n\t"
"s_add_u32 s29, s22, 384\n\t"
"s_waitcnt lgkmcnt(13)\n\t"
"buffer_store_dwordx4 a[40:43], v248, s[16:19], s29 offen offset:0 nt\n\t"
"s_waitcnt lgkmcnt(12)\n\t"
"v_mfma_f32_16x16x32_bf16 v[12:15], a[64:67], a[180:183], v[12:15]\n\t"
"ds_read_b64_tr_b16 v[230:231], v240 offset:2048\n\t"
"v_mfma_f32_16x16x32_bf16 v[12:15], a[68:71], a[184:187], v[12:15]\n\t"
"ds_read_b64_tr_b16 v[232:233], v241 offset:512\n\t"
"v_mfma_f32_16x16x32_bf16 v[12:15], a[72:75], a[188:191], v[12:15]\n\t"
"s_add_u32 s29, s22, 384\n\t"
"s_waitcnt lgkmcnt(13)\n\t"
"buffer_store_dwordx4 a[44:47], v248, s[16:19], s29 offen offset:128 nt\n\t"
"s_waitcnt lgkmcnt(8)\n\t"
"v_mfma_f32_16x16x32_bf16 v[224:227], v[224:227], a[52:55], 0\n\t"
"ds_write_b64 v250, a[112:113] offset:30720\n\t"
"s_waitcnt lgkmcnt(6)\n\t"
"v_mfma_f32_16x16x32_bf16 v[236:239], v[236:239], a[52:55], 0\n\t"
"s_add_u32 s29, s22, 384\n\t"
"buffer_store_dwordx4 a[48:51], v248, s[16:19], s29 offen offset:256 nt\n\t"
"v_mfma_f32_16x16x32_bf16 v[16:19], a[64:67], a[184:187], v[16:19]\n\t"
"ds_read_b64_tr_b16 v[234:235], v241 offset:2048\n\t"
"v_mfma_f32_16x16x32_bf16 v[16:19], a[68:71], a[188:191], v[16:19]\n\t"
"v_mfma_f32_16x16x32_bf16 v[16:19], a[72:75], a[192:195], v[16:19]\n\t"
"ds_write_b128 v249, v[208:211] offset:36864\n\t"
"v_mfma_f32_16x16x32_bf16 v[20:23], a[64:67], a[188:191], v[20:23]\n\t"
"v_cvt_pk_bf16_f32 v224, v224, v225\n\t"
"v_cvt_pk_bf16_f32 v225, v226, v227\n\t"
"v_mfma_f32_16x16x32_bf16 v[20:23], a[68:71], a[192:195], v[20:23]\n\t"
"v_cvt_pk_bf16_f32 v226, v236, v237\n\t"
"v_cvt_pk_bf16_f32 v227, v238, v239\n\t"
"v_mfma_f32_16x16x32_bf16 v[20:23], a[72:75], a[196:199], v[20:23]\n\t"
"s_waitcnt lgkmcnt(7)\n\t"
"v_mfma_f32_16x16x32_bf16 v[24:27], a[64:67], a[192:195], v[24:27]\n\t"
"v_mfma_f32_16x16x32_bf16 v[24:27], a[68:71], a[196:199], v[24:27]\n\t"
"v_mfma_f32_16x16x32_bf16 v[24:27], a[72:75], a[200:203], v[24:27]\n\t"
"ds_write_b64 v250, a[114:115] offset:36864\n\t"
"s_waitcnt lgkmcnt(5)\n\t"
"v_mfma_f32_16x16x32_bf16 v[228:231], v[228:231], a[52:55], 0\n\t"
"s_waitcnt lgkmcnt(2)\n\t"
"v_mfma_f32_16x16x32_bf16 v[232:235], v[232:235], a[52:55], 0\n\t"
"v_mfma_f32_16x16x32_bf16 v[28:31], a[64:67], a[196:199], v[28:31]\n\t"
"v_mfma_f32_16x16x32_bf16 v[28:31], a[68:71], a[200:203], v[28:31]\n\t"
"v_mfma_f32_16x16x32_bf16 v[28:31], a[72:75], a[204:207], v[28:31]\n\t"
"buffer_load_dwordx4 a[64:67], v252, s[8:11], s20 offen offset:1024 sc1\n\t"
"v_mfma_f32_16x16x32_bf16 v[32:35], a[76:79], a[168:171], v[32:35]\n\t"
"s_nop 0\n\t"
"v_cvt_pk_bf16_f32 v228, v228, v229\n\t"
"v_cvt_pk_bf16_f32 v229, v230, v231\n\t"
"v_mfma_f32_16x16x32_bf16 v[32:35], a[80:83], a[172:175], v[32:35]\n\t"
"v_cvt_pk_bf16_f32 v230, v232, v233\n\t"
"v_cvt_pk_bf16_f32 v231, v234, v235\n\t"
"v_mfma_f32_16x16x32_bf16 v[32:35], a[84:87], a[176:179], v[32:35]\n\t"
"v_mfma_f32_16x16x32_bf16 v[36:39], a[76:79], a[172:175], v[36:39]\n\t"
"v_mfma_f32_16x16x32_bf16 v[36:39], a[80:83], a[176:179], v[36:39]\n\t"
"v_mfma_f32_16x16x32_bf16 v[36:39], a[84:87], a[180:183], v[36:39]\n\t"
"buffer_load_dwordx4 a[68:71], v252, s[8:11], s20 offen offset:2048 sc1\n\t"
"v_mfma_f32_16x16x32_bf16 v[40:43], a[76:79], a[176:179], v[40:43]\n\t"
"v_mfma_f32_16x16x32_bf16 v[40:43], a[80:83], a[180:183], v[40:43]\n\t"
"v_mfma_f32_16x16x32_bf16 v[40:43], a[84:87], a[184:187], v[40:43]\n\t"
"s_mov_b32 s20, 1069056\n\t"
"buffer_load_dwordx4 a[72:75], v252, s[8:11], s20 offen offset:0 sc1\n\t"
"v_mfma_f32_16x16x32_bf16 v[44:47], a[76:79], a[180:183], v[44:47]\n\t"
"v_mfma_f32_16x16x32_bf16 v[44:47], a[80:83], a[184:187], v[44:47]\n\t"
"v_mfma_f32_16x16x32_bf16 v[44:47], a[84:87], a[188:191], v[44:47]\n\t"
"s_waitcnt lgkmcnt(0)\n\t"
"s_barrier\n\t"
"v_mfma_f32_16x16x32_bf16 v[48:51], a[76:79], a[184:187], v[48:51]\n\t"
"ds_read_b64_tr_b16 v[236:237], v240 offset:20992\n\t"
"v_mfma_f32_16x16x32_bf16 v[48:51], a[80:83], a[188:191], v[48:51]\n\t"
"ds_read_b64_tr_b16 v[238:239], v240 offset:22528\n\t"
"v_mfma_f32_16x16x32_bf16 v[48:51], a[84:87], a[192:195], v[48:51]\n\t"
"ds_read_b128 v[196:199], v252 offset:30720\n\t"
"s_mov_b64 exec, s[44:45]\n\t"
"ds_write_b128 v242, v[224:227] offset:43008\n\t"
"s_mov_b64 exec, -1\n\t"
"ds_read_b64_tr_b16 v[232:233], v241 offset:20992\n\t"
"ds_read_b64_tr_b16 v[234:235], v241 offset:22528\n\t"
"v_mfma_f32_16x16x32_bf16 v[52:55], a[76:79], a[188:191], v[52:55]\n\t"
"ds_read_b128 v[200:203], v252 offset:31744\n\t"
"v_mfma_f32_16x16x32_bf16 v[52:55], a[80:83], a[192:195], v[52:55]\n\t"
"ds_read_b128 a[40:43], v252 offset:32768\n\t"
"v_mfma_f32_16x16x32_bf16 v[52:55], a[84:87], a[196:199], v[52:55]\n\t"
"buffer_load_dwordx4 a[112:115], v252, s[8:11], s20 offen offset:1024 sc1\n\t"
"v_mfma_f32_16x16x32_bf16 v[56:59], a[76:79], a[192:195], v[56:59]\n\t"
"ds_read_b64_tr_b16 v[224:225], v240 offset:40960\n\t"
"v_mfma_f32_16x16x32_bf16 v[56:59], a[80:83], a[196:199], v[56:59]\n\t"
"ds_read_b64_tr_b16 v[226:227], v240 offset:42496\n\t"
"v_mfma_f32_16x16x32_bf16 v[56:59], a[84:87], a[200:203], v[56:59]\n\t"
"ds_write_b128 v249, v[212:215] offset:18432\n\t"
"s_waitcnt lgkmcnt(9)\n\t"
"v_mfma_f32_16x16x32_bf16 v[236:239], v[236:239], a[52:55], 0\n\t"
"s_add_u32 s29, s22, 196992\n\t"
"s_waitcnt lgkmcnt(8)\n\t"
"buffer_store_dwordx4 v[196:199], v248, s[16:19], s29 offen offset:0 nt\n\t"
"s_waitcnt lgkmcnt(5)\n\t"
"v_mfma_f32_16x16x32_bf16 v[232:235], v[232:235], a[52:55], 0\n\t"
"ds_write_b64 v250, a[120:121] offset:18432\n\t"
"v_mfma_f32_16x16x32_bf16 v[60:63], a[76:79], a[196:199], v[60:63]\n\t"
"v_mfma_f32_16x16x32_bf16 v[60:63], a[80:83], a[200:203], v[60:63]\n\t"
"v_mfma_f32_16x16x32_bf16 v[60:63], a[84:87], a[204:207], v[60:63]\n\t"
"buffer_load_dwordx4 a[76:79], v252, s[8:11], s20 offen offset:2048 sc1\n\t"
"v_mfma_f32_16x16x32_bf16 v[64:67], a[208:211], a[168:171], v[64:67]\n\t"
"v_cvt_pk_bf16_f32 v236, v236, v237\n\t"
"v_cvt_pk_bf16_f32 v237, v238, v239\n\t"
"v_mfma_f32_16x16x32_bf16 v[64:67], a[212:215], a[172:175], v[64:67]\n\t"
"v_cvt_pk_bf16_f32 v238, v232, v233\n\t"
"v_cvt_pk_bf16_f32 v239, v234, v235\n\t"
"v_mfma_f32_16x16x32_bf16 v[64:67], a[88:91], a[176:179], v[64:67]\n\t"
"ds_read_b64_tr_b16 v[232:233], v241 offset:40960\n\t"
"ds_read_b64_tr_b16 v[234:235], v241 offset:42496\n\t"
"s_mov_b32 s20, 1290240\n\t"
"buffer_load_dwordx4 a[80:83], v252, s[8:11], s20 offen offset:0 sc1\n\t"
"v_mfma_f32_16x16x32_bf16 v[68:71], a[208:211], a[172:175], v[68:71]\n\t"
"v_add_u32 v240, 59904, v240\n\t"
"v_add_u32 v241, 59904, v241\n\t"
"v_mfma_f32_16x16x32_bf16 v[68:71], a[212:215], a[176:179], v[68:71]\n\t"
"ds_read_b128 a[168:171], v244 offset:9216\n\t"
"v_mfma_f32_16x16x32_bf16 v[68:71], a[88:91], a[180:183], v[68:71]\n\t"
"s_add_u32 s29, s22, 196992\n\t"
"s_waitcnt lgkmcnt(8)\n\t"
"buffer_store_dwordx4 v[200:203], v248, s[16:19], s29 offen offset:128 nt\n\t"
"buffer_load_dwordx4 a[84:87], v252, s[8:11], s20 offen offset:1024 sc1\n\t"
"s_waitcnt lgkmcnt(5)\n\t"
"v_mfma_f32_16x16x32_bf16 v[224:227], v[224:227], a[52:55], 0\n\t"
"s_add_u32 s29, s22, 196992\n\t"
"buffer_store_dwordx4 a[40:43], v248, s[16:19], s29 offen offset:256 nt\n\t"
"s_mov_b64 exec, s[44:45]\n\t"
"ds_write_b128 v242, v[236:239] offset:44544\n\t"
"s_mov_b64 exec, -1\n\t"
"s_waitcnt lgkmcnt(2)\n\t"
"v_mfma_f32_16x16x32_bf16 v[232:235], v[232:235], a[52:55], 0\n\t"
"ds_read_b64_tr_b16 v[236:237], v240 offset:1024\n\t"
"ds_read_b64_tr_b16 v[238:239], v240 offset:2560\n\t"
"v_mfma_f32_16x16x32_bf16 v[72:75], a[208:211], a[176:179], v[72:75]\n\t"
"ds_read_b128 a[172:175], v244 offset:9728\n\t"
"v_mfma_f32_16x16x32_bf16 v[72:75], a[212:215], a[180:183], v[72:75]\n\t"
"ds_read_b128 a[176:179], v244 offset:10240\n\t"
"v_mfma_f32_16x16x32_bf16 v[72:75], a[88:91], a[184:187], v[72:75]\n\t"
"ds_write_b128 v249, v[216:219] offset:24576\n\t"
"v_mfma_f32_16x16x32_bf16 v[76:79], a[208:211], a[180:183], v[76:79]\n\t"
"v_cvt_pk_bf16_f32 v224, v224, v225\n\t"
"v_cvt_pk_bf16_f32 v225, v226, v227\n\t"
"v_mfma_f32_16x16x32_bf16 v[76:79], a[212:215], a[184:187], v[76:79]\n\t"
"v_cvt_pk_bf16_f32 v226, v232, v233\n\t"
"v_cvt_pk_bf16_f32 v227, v234, v235\n\t"
"v_mfma_f32_16x16x32_bf16 v[76:79], a[88:91], a[188:191], v[76:79]\n\t"
"ds_read_b64_tr_b16 v[232:233], v241 offset:1024\n\t"
"ds_read_b64_tr_b16 v[234:235], v241 offset:2560\n\t"
"v_mfma_f32_16x16x32_bf16 v[80:83], a[208:211], a[184:187], v[80:83]\n\t"
"ds_read_b128 a[180:183], v244 offset:10752\n\t"
"v_mfma_f32_16x16x32_bf16 v[80:83], a[212:215], a[188:191], v[80:83]\n\t"
"ds_read_b128 a[184:187], v244 offset:11264\n\t"
"v_mfma_f32_16x16x32_bf16 v[80:83], a[88:91], a[192:195], v[80:83]\n\t"
"s_mov_b64 exec, s[44:45]\n\t"
"ds_write_b128 v242, v[224:227] offset:46080\n\t"
"s_mov_b64 exec, -1\n\t"
"s_waitcnt lgkmcnt(8)\n\t"
"v_mfma_f32_16x16x32_bf16 v[236:239], v[236:239], a[52:55], 0\n\t"
"ds_read_b64_tr_b16 v[224:225], v240 offset:20992\n\t"
"ds_read_b64_tr_b16 v[226:227], v240 offset:22528\n\t"
"s_waitcnt lgkmcnt(5)\n\t"
"v_mfma_f32_16x16x32_bf16 v[232:235], v[232:235], a[52:55], 0\n\t"
"ds_write_b64 v250, a[122:123] offset:24576\n\t"
"v_mfma_f32_16x16x32_bf16 v[84:87], a[208:211], a[188:191], v[84:87]\n\t"
"ds_read_b128 a[188:191], v244 offset:11776\n\t"
"v_mfma_f32_16x16x32_bf16 v[84:87], a[212:215], a[192:195], v[84:87]\n\t"
"v_mfma_f32_16x16x32_bf16 v[84:87], a[88:91], a[196:199], v[84:87]\n\t"
"v_mfma_f32_16x16x32_bf16 v[88:91], a[208:211], a[192:195], v[88:91]\n\t"
"v_cvt_pk_bf16_f32 v236, v236, v237\n\t"
"v_cvt_pk_bf16_f32 v237, v238, v239\n\t"
"v_mfma_f32_16x16x32_bf16 v[88:91], a[212:215], a[196:199], v[88:91]\n\t"
"v_cvt_pk_bf16_f32 v238, v232, v233\n\t"
"v_cvt_pk_bf16_f32 v239, v234, v235\n\t"
"v_mfma_f32_16x16x32_bf16 v[88:91], a[88:91], a[200:203], v[88:91]\n\t"
"ds_read_b64_tr_b16 v[232:233], v241 offset:20992\n\t"
"ds_read_b64_tr_b16 v[234:235], v241 offset:22528\n\t"
"v_mfma_f32_16x16x32_bf16 v[92:95], a[208:211], a[196:199], v[92:95]\n\t"
"ds_read_b128 a[192:195], v244 offset:12288\n\t"
"v_mfma_f32_16x16x32_bf16 v[92:95], a[212:215], a[200:203], v[92:95]\n\t"
"ds_read_b128 a[196:199], v244 offset:12800\n\t"
"v_mfma_f32_16x16x32_bf16 v[92:95], a[88:91], a[204:207], v[92:95]\n\t"
"buffer_load_dwordx4 a[208:211], v252, s[8:11], s20 offen offset:2048 sc1\n\t"
"s_waitcnt lgkmcnt(6)\n\t"
"v_mfma_f32_16x16x32_bf16 v[224:227], v[224:227], a[52:55], 0\n\t"
"s_mov_b64 exec, s[44:45]\n\t"
"ds_write_b128 v242, v[236:239] offset:47616\n\t"
"s_mov_b64 exec, -1\n\t"
"s_waitcnt lgkmcnt(3)\n\t"
"v_mfma_f32_16x16x32_bf16 v[232:235], v[232:235], a[52:55], 0\n\t"
"ds_read_b64_tr_b16 v[236:237], v240 offset:40960\n\t"
"ds_read_b64_tr_b16 v[238:239], v240 offset:42496\n\t"
"s_waitcnt vmcnt(36)\n\t"
"v_mfma_f32_16x16x32_bf16 v[100:103], a[92:95], a[168:171], v[100:103]\n\t"
"ds_read_b128 a[200:203], v244 offset:13312\n\t"
"v_mfma_f32_16x16x32_bf16 v[100:103], a[96:99], a[172:175], v[100:103]\n\t"
"ds_read_b128 a[204:207], v244 offset:13824\n\t"
"v_mfma_f32_16x16x32_bf16 v[100:103], a[100:103], a[176:179], v[100:103]\n\t"
"s_mov_b32 s20, 1081344\n\t"
"buffer_load_dwordx4 a[212:215], v252, s[8:11], s20 offen offset:0 sc1\n\t"
"v_mfma_f32_16x16x32_bf16 v[104:107], a[92:95], a[172:175], v[104:107]\n\t"
"v_cvt_pk_bf16_f32 v224, v224, v225\n\t"
"v_cvt_pk_bf16_f32 v225, v226, v227\n\t"
"v_mfma_f32_16x16x32_bf16 v[104:107], a[96:99], a[176:179], v[104:107]\n\t"
"v_cvt_pk_bf16_f32 v226, v232, v233\n\t"
"v_cvt_pk_bf16_f32 v227, v234, v235\n\t"
"v_mfma_f32_16x16x32_bf16 v[104:107], a[100:103], a[180:183], v[104:107]\n\t"
"ds_read_b64_tr_b16 v[232:233], v241 offset:40960\n\t"
"ds_read_b64_tr_b16 v[234:235], v241 offset:42496\n\t"
"buffer_load_dwordx4 a[88:91], v252, s[8:11], s20 offen offset:1024 sc1\n\t"
"s_waitcnt lgkmcnt(2)\n\t"
"s_barrier\n\t"
"v_mfma_f32_16x16x32_bf16 v[108:111], a[92:95], a[176:179], v[108:111]\n\t"
"ds_read_b128 v[196:199], v252 offset:18432\n\t"
"v_mfma_f32_16x16x32_bf16 v[108:111], a[96:99], a[180:183], v[108:111]\n\t"
"ds_read_b128 v[200:203], v252 offset:19456\n\t"
"v_mfma_f32_16x16x32_bf16 v[108:111], a[100:103], a[184:187], v[108:111]\n\t"
"s_mov_b64 exec, s[44:45]\n\t"
"ds_write_b128 v242, v[228:231] offset:0\n\t"
"s_mov_b64 exec, -1\n\t"
"buffer_load_dwordx4 a[120:123], v252, s[8:11], s20 offen offset:2048 sc1\n\t"
"v_mfma_f32_16x16x32_bf16 v[236:239], v[236:239], a[52:55], 0\n\t"
"s_mov_b64 exec, s[44:45]\n\t"
"ds_write_b128 v242, v[224:227] offset:49152\n\t"
"s_mov_b64 exec, -1\n\t"
"s_waitcnt lgkmcnt(4)\n\t"
"v_mfma_f32_16x16x32_bf16 v[232:235], v[232:235], a[52:55], 0\n\t"
"ds_read_b64_tr_b16 v[228:229], v240 offset:512\n\t"
"ds_read_b64_tr_b16 v[230:231], v240 offset:2048\n\t"
"v_mfma_f32_16x16x32_bf16 v[112:115], a[92:95], a[180:183], v[112:115]\n\t"
"ds_read_b64_tr_b16 v[224:225], v241 offset:512\n\t"
"v_mfma_f32_16x16x32_bf16 v[112:115], a[96:99], a[184:187], v[112:115]\n\t"
"ds_read_b64_tr_b16 v[226:227], v241 offset:2048\n\t"
"v_mfma_f32_16x16x32_bf16 v[112:115], a[100:103], a[188:191], v[112:115]\n\t"
"ds_read_b128 v[204:207], v252 offset:20480\n\t"
"v_mfma_f32_16x16x32_bf16 v[116:119], a[92:95], a[184:187], v[116:119]\n\t"
"v_cvt_pk_bf16_f32 v236, v236, v237\n\t"
"v_cvt_pk_bf16_f32 v237, v238, v239\n\t"
"v_mfma_f32_16x16x32_bf16 v[116:119], a[96:99], a[188:191], v[116:119]\n\t"
"v_cvt_pk_bf16_f32 v238, v232, v233\n\t"
"v_cvt_pk_bf16_f32 v239, v234, v235\n\t"
"v_mfma_f32_16x16x32_bf16 v[116:119], a[100:103], a[192:195], v[116:119]\n\t"
"s_add_u32 s29, s22, 393600\n\t"
"s_waitcnt lgkmcnt(8)\n\t"
"buffer_store_dwordx4 v[196:199], v248, s[16:19], s29 offen offset:0 nt\n\t"
"v_mfma_f32_16x16x32_bf16 v[120:123], a[92:95], a[188:191], v[120:123]\n\t"
"ds_read_b64_tr_b16 v[232:233], v240 offset:20480\n\t"
"v_mfma_f32_16x16x32_bf16 v[120:123], a[96:99], a[192:195], v[120:123]\n\t"
"ds_read_b64_tr_b16 v[234:235], v240 offset:22016\n\t"
"v_mfma_f32_16x16x32_bf16 v[120:123], a[100:103], a[196:199], v[120:123]\n\t"
"s_mov_b64 exec, s[44:45]\n\t"
"ds_write_b128 v242, v[236:239] offset:50688\n\t"
"s_mov_b64 exec, -1\n\t"
"s_waitcnt lgkmcnt(6)\n\t"
"v_mfma_f32_16x16x32_bf16 v[228:231], v[228:231], a[52:55], 0\n\t"
"ds_write_b128 v249, a[0:3] offset:30720\n\t"
"s_waitcnt lgkmcnt(5)\n\t"
"v_mfma_f32_16x16x32_bf16 v[224:227], v[224:227], a[52:55], 0\n\t"
"ds_read_b64_tr_b16 v[236:237], v241 offset:20480\n\t"
"ds_read_b64_tr_b16 v[238:239], v241 offset:22016\n\t"
"v_mfma_f32_16x16x32_bf16 v[124:127], a[92:95], a[192:195], v[124:127]\n\t"
"v_mfma_f32_16x16x32_bf16 v[124:127], a[96:99], a[196:199], v[124:127]\n\t"
"v_mfma_f32_16x16x32_bf16 v[124:127], a[100:103], a[200:203], v[124:127]\n\t"
"s_add_u32 s29, s22, 393600\n\t"
"buffer_store_dwordx4 v[200:203], v248, s[16:19], s29 offen offset:128 nt\n\t"
"v_mfma_f32_16x16x32_bf16 v[128:131], a[92:95], a[196:199], v[128:131]\n\t"
"v_cvt_pk_bf16_f32 v228, v228, v229\n\t"
"v_cvt_pk_bf16_f32 v229, v230, v231\n\t"
"v_mfma_f32_16x16x32_bf16 v[128:131], a[96:99], a[200:203], v[128:131]\n\t"
"v_cvt_pk_bf16_f32 v230, v224, v225\n\t"
"v_cvt_pk_bf16_f32 v231, v226, v227\n\t"
"v_mfma_f32_16x16x32_bf16 v[128:131], a[100:103], a[204:207], v[128:131]\n\t"
"s_add_u32 s29, s22, 393600\n\t"
"s_waitcnt lgkmcnt(6)\n\t"
"buffer_store_dwordx4 v[204:207], v248, s[16:19], s29 offen offset:256 nt\n\t"
"s_mov_b32 s20, 1302528\n\t"
"buffer_load_dwordx4 a[92:95], v252, s[8:11], s20 offen offset:0 sc1\n\t"
"s_waitcnt vmcnt(40)\n\t"
"v_mfma_f32_16x16x32_bf16 v[132:135], a[108:111], a[168:171], v[132:135]\n\t"
"ds_read_b64_tr_b16 v[224:225], v240 offset:40448\n\t"
"v_mfma_f32_16x16x32_bf16 v[132:135], a[116:119], a[172:175], v[132:135]\n\t"
"ds_read_b64_tr_b16 v[226:227], v240 offset:41984\n\t"
"v_mfma_f32_16x16x32_bf16 v[132:135], a[216:219], a[176:179], v[132:135]\n\t"
"buffer_load_dwordx4 a[96:99], v252, s[8:11], s20 offen offset:1024 sc1\n\t"
"s_mov_b64 exec, s[44:45]\n\t"
"ds_write_b128 v242, v[228:231] offset:4608\n\t"
"s_mov_b64 exec, -1\n\t"
"s_waitcnt lgkmcnt(7)\n\t"
"v_mfma_f32_16x16x32_bf16 v[232:235], v[232:235], a[52:55], 0\n\t"
"buffer_load_dwordx4 a[100:103], v252, s[8:11], s20 offen offset:2048 sc1\n\t"
"s_waitcnt lgkmcnt(3)\n\t"
"v_mfma_f32_16x16x32_bf16 v[236:239], v[236:239], a[52:55], 0\n\t"
"ds_read_b64_tr_b16 v[228:229], v241 offset:40448\n\t"
"ds_read_b64_tr_b16 v[230:231], v241 offset:41984\n\t"
"v_mfma_f32_16x16x32_bf16 v[136:139], a[108:111], a[172:175], v[136:139]\n\t"
"v_subrev_u32 v240, 59904, v240\n\t"
"v_subrev_u32 v241, 59904, v241\n\t"
"v_mfma_f32_16x16x32_bf16 v[136:139], a[116:119], a[176:179], v[136:139]\n\t"
"v_mfma_f32_16x16x32_bf16 v[136:139], a[216:219], a[180:183], v[136:139]\n\t"
"ds_write_b64 v250, a[128:129] offset:30720\n\t"
"v_mfma_f32_16x16x32_bf16 v[140:143], a[108:111], a[176:179], v[140:143]\n\t"
"v_cvt_pk_bf16_f32 v232, v232, v233\n\t"
"v_cvt_pk_bf16_f32 v233, v234, v235\n\t"
"v_mfma_f32_16x16x32_bf16 v[140:143], a[116:119], a[180:183], v[140:143]\n\t"
"v_cvt_pk_bf16_f32 v234, v236, v237\n\t"
"v_cvt_pk_bf16_f32 v235, v238, v239\n\t"
"v_mfma_f32_16x16x32_bf16 v[140:143], a[216:219], a[184:187], v[140:143]\n\t"
"ds_read_b64_tr_b16 v[236:237], v240 offset:20480\n\t"
"ds_read_b64_tr_b16 v[238:239], v240 offset:22016\n\t"
"v_mfma_f32_16x16x32_bf16 v[144:147], a[108:111], a[180:183], v[144:147]\n\t"
"v_mfma_f32_16x16x32_bf16 v[144:147], a[116:119], a[184:187], v[144:147]\n\t"
"v_mfma_f32_16x16x32_bf16 v[144:147], a[216:219], a[188:191], v[144:147]\n\t"
"s_mov_b64 exec, s[44:45]\n\t"
"ds_write_b128 v242, v[232:235] offset:6144\n\t"
"s_mov_b64 exec, -1\n\t"
"s_waitcnt lgkmcnt(7)\n\t"
"v_mfma_f32_16x16x32_bf16 v[224:227], v[224:227], a[52:55], 0\n\t"
"ds_read_b64_tr_b16 v[232:233], v241 offset:20480\n\t"
"ds_read_b64_tr_b16 v[234:235], v241 offset:22016\n\t"
"s_waitcnt lgkmcnt(6)\n\t"
"v_mfma_f32_16x16x32_bf16 v[228:231], v[228:231], a[52:55], 0\n\t"
"ds_write_b128 v249, a[4:7] offset:36864\n\t"
"v_mfma_f32_16x16x32_bf16 v[148:151], a[108:111], a[184:187], v[148:151]\n\t"
"v_mfma_f32_16x16x32_bf16 v[148:151], a[116:119], a[188:191], v[148:151]\n\t"
"v_mfma_f32_16x16x32_bf16 v[148:151], a[216:219], a[192:195], v[148:151]\n\t"
"ds_write_b64 v250, a[130:131] offset:36864\n\t"
"v_mfma_f32_16x16x32_bf16 v[152:155], a[108:111], a[188:191], v[152:155]\n\t"
"v_cvt_pk_bf16_f32 v224, v224, v225\n\t"
"v_cvt_pk_bf16_f32 v225, v226, v227\n\t"
"v_mfma_f32_16x16x32_bf16 v[152:155], a[116:119], a[192:195], v[152:155]\n\t"
"v_cvt_pk_bf16_f32 v226, v228, v229\n\t"
"v_cvt_pk_bf16_f32 v227, v230, v231\n\t"
"v_mfma_f32_16x16x32_bf16 v[152:155], a[216:219], a[196:199], v[152:155]\n\t"
"ds_read_b64_tr_b16 v[228:229], v240 offset:40448\n\t"
"ds_read_b64_tr_b16 v[230:231], v240 offset:41984\n\t"
"s_waitcnt lgkmcnt(7)\n\t"
"v_mfma_f32_16x16x32_bf16 v[236:239], v[236:239], a[52:55], 0\n\t"
"s_mov_b64 exec, s[44:45]\n\t"
"ds_write_b128 v242, v[224:227] offset:7680\n\t"
"s_mov_b64 exec, -1\n\t"
"s_waitcnt lgkmcnt(5)\n\t"
"v_mfma_f32_16x16x32_bf16 v[232:235], v[232:235], a[52:55], 0\n\t"
"ds_read_b64_tr_b16 v[224:225], v241 offset:40448\n\t"
"ds_read_b64_tr_b16 v[226:227], v241 offset:41984\n\t"
"v_mfma_f32_16x16x32_bf16 v[156:159], a[108:111], a[192:195], v[156:159]\n\t"
"v_mfma_f32_16x16x32_bf16 v[156:159], a[116:119], a[196:199], v[156:159]\n\t"
"v_mfma_f32_16x16x32_bf16 v[156:159], a[216:219], a[200:203], v[156:159]\n\t"
"v_mfma_f32_16x16x32_bf16 v[160:163], a[108:111], a[196:199], v[160:163]\n\t"
"v_cvt_pk_bf16_f32 v236, v236, v237\n\t"
"v_cvt_pk_bf16_f32 v237, v238, v239\n\t"
"v_mfma_f32_16x16x32_bf16 v[160:163], a[116:119], a[200:203], v[160:163]\n\t"
"v_cvt_pk_bf16_f32 v238, v232, v233\n\t"
"v_cvt_pk_bf16_f32 v239, v234, v235\n\t"
"v_mfma_f32_16x16x32_bf16 v[160:163], a[216:219], a[204:207], v[160:163]\n\t"
"s_mov_b32 s20, 1093632\n\t"
"buffer_load_dwordx4 a[108:111], v252, s[8:11], s20 offen offset:0 sc1\n\t"
"s_waitcnt lgkmcnt(3)\n\t"
"v_mfma_f32_16x16x32_bf16 v[228:231], v[228:231], a[52:55], 0\n\t"
"buffer_load_dwordx4 a[216:219], v252, s[8:11], s20 offen offset:1024 sc1\n\t"
"s_waitcnt lgkmcnt(0)\n\t"
"v_mfma_f32_16x16x32_bf16 v[224:227], v[224:227], a[52:55], 0\n\t"
"s_waitcnt vmcnt(41)\n\t"
"v_mfma_f32_16x16x32_bf16 v[164:167], a[228:231], a[168:171], v[164:167]\n\t"
"v_mfma_f32_16x16x32_bf16 v[164:167], a[232:235], a[172:175], v[164:167]\n\t"
"v_mfma_f32_16x16x32_bf16 v[164:167], a[124:127], a[176:179], v[164:167]\n\t"
"s_mov_b64 exec, s[44:45]\n\t"
"ds_write_b128 v242, v[236:239] offset:1536\n\t"
"s_mov_b64 exec, -1\n\t"
"v_mfma_f32_16x16x32_bf16 v[168:171], a[228:231], a[172:175], v[168:171]\n\t"
"v_cvt_pk_bf16_f32 v228, v228, v229\n\t"
"v_cvt_pk_bf16_f32 v229, v230, v231\n\t"
"v_mfma_f32_16x16x32_bf16 v[168:171], a[232:235], a[176:179], v[168:171]\n\t"
"v_cvt_pk_bf16_f32 v230, v224, v225\n\t"
"v_cvt_pk_bf16_f32 v231, v226, v227\n\t"
"v_mfma_f32_16x16x32_bf16 v[168:171], a[124:127], a[180:183], v[168:171]\n\t"
"buffer_load_dwordx4 a[116:119], v252, s[8:11], s20 offen offset:2048 sc1\n\t"
"v_mfma_f32_16x16x32_bf16 v[172:175], a[228:231], a[176:179], v[172:175]\n\t"
"v_mfma_f32_16x16x32_bf16 v[172:175], a[232:235], a[180:183], v[172:175]\n\t"
"v_mfma_f32_16x16x32_bf16 v[172:175], a[124:127], a[184:187], v[172:175]\n\t"
"s_mov_b32 s20, 1314816\n\t"
"buffer_load_dwordx4 v[236:239], v252, s[8:11], s20 offen offset:0 sc1\n\t"
"v_mfma_f32_16x16x32_bf16 v[176:179], a[228:231], a[180:183], v[176:179]\n\t"
"v_mfma_f32_16x16x32_bf16 v[176:179], a[232:235], a[184:187], v[176:179]\n\t"
"v_mfma_f32_16x16x32_bf16 v[176:179], a[124:127], a[188:191], v[176:179]\n\t"
"s_mov_b64 exec, s[44:45]\n\t"
"ds_write_b128 v242, v[228:231] offset:3072\n\t"
"s_mov_b64 exec, -1\n\t"
"v_mfma_f32_16x16x32_bf16 v[180:183], a[228:231], a[184:187], v[180:183]\n\t"
"v_mfma_f32_16x16x32_bf16 v[180:183], a[232:235], a[188:191], v[180:183]\n\t"
"v_mfma_f32_16x16x32_bf16 v[180:183], a[124:127], a[192:195], v[180:183]\n\t"
"v_mfma_f32_16x16x32_bf16 v[184:187], a[228:231], a[188:191], v[184:187]\n\t"
"v_mfma_f32_16x16x32_bf16 v[184:187], a[232:235], a[192:195], v[184:187]\n\t"
"v_mfma_f32_16x16x32_bf16 v[184:187], a[124:127], a[196:199], v[184:187]\n\t"
"s_waitcnt lgkmcnt(0)\n\t"
"s_barrier\n\t"
"v_mfma_f32_16x16x32_bf16 v[188:191], a[228:231], a[192:195], v[188:191]\n\t"
"ds_read_b128 a[168:171], v244 offset:0\n\t"
"v_add_u32 v224, 61440, v249\n\t"
"v_mfma_f32_16x16x32_bf16 v[188:191], a[232:235], a[196:199], v[188:191]\n\t"
"ds_read_b128 a[172:175], v244 offset:512\n\t"
"v_add_u32 v225, 61440, v250\n\t"
"v_mfma_f32_16x16x32_bf16 v[188:191], a[124:127], a[200:203], v[188:191]\n\t"
"ds_read_b128 a[176:179], v244 offset:1024\n\t"
"ds_read_b128 a[180:183], v244 offset:43008\n\t"
"ds_read_b128 a[184:187], v244 offset:43520\n\t"
"ds_read_b128 a[188:191], v244 offset:44032\n\t"
"ds_read_b128 v[196:199], v252 offset:30720\n\t"
"v_mfma_f32_16x16x32_bf16 v[192:195], a[228:231], a[196:199], v[192:195]\n\t"
"v_add_u32 v226, 61440, v252\n\t"
"ds_read_b128 v[200:203], v252 offset:31744\n\t"
"v_mfma_f32_16x16x32_bf16 v[192:195], a[232:235], a[200:203], v[192:195]\n\t"
"ds_read_b128 v[204:207], v252 offset:32768\n\t"
"v_mfma_f32_16x16x32_bf16 v[192:195], a[124:127], a[204:207], v[192:195]\n\t"
"ds_write_b128 v249, a[8:11] offset:18432\n\t"
"ds_write_b128 v224, a[16:19] offset:0\n\t"
"ds_write_b128 v224, a[24:27] offset:12288\n\t"
"ds_write_b128 v224, a[32:35] offset:24576\n\t"
"buffer_load_dwordx4 a[128:131], v252, s[8:11], s20 offen offset:1024 sc1\n\t"
"Lst34_%=:\n\t"
"s_waitcnt vmcnt(41) lgkmcnt(10)\n\t"
"v_mfma_f32_16x16x32_bf16 v[0:3], a[132:135], a[168:171], v[0:3]\n\t"
"ds_read_b128 a[192:195], v244 offset:9216\n\t"
"v_mfma_f32_16x16x32_bf16 v[0:3], a[140:143], a[172:175], v[0:3]\n\t"
"ds_read_b128 a[40:43], v244 offset:9728\n\t"
"v_mfma_f32_16x16x32_bf16 v[0:3], a[148:151], a[176:179], v[0:3]\n\t"
"ds_write_b64 v250, a[136:137] offset:18432\n\t"
"ds_write_b64 v225, a[144:145] offset:0\n\t"
"ds_write_b64 v225, a[152:153] offset:12288\n\t"
"ds_write_b64 v225, a[160:161] offset:24576\n\t"
"buffer_load_dwordx4 a[228:231], v252, s[8:11], s20 offen offset:2048 sc1\n\t"
"s_waitcnt vmcnt(39) lgkmcnt(13)\n\t"
"v_mfma_f32_16x16x32_bf16 v[0:3], a[156:159], a[180:183], v[0:3]\n\t"
"ds_read_b128 a[44:47], v244 offset:10240\n\t"
"v_mfma_f32_16x16x32_bf16 v[0:3], a[236:239], a[184:187], v[0:3]\n\t"
"ds_read_b128 a[196:199], v244 offset:52224\n\t"
"v_mfma_f32_16x16x32_bf16 v[0:3], a[240:243], a[188:191], v[0:3]\n\t"
"ds_write_b128 v249, a[12:15] offset:24576\n\t"
"ds_write_b128 v224, a[20:23] offset:6144\n\t"
"ds_write_b128 v224, a[28:31] offset:18432\n\t"
"ds_write_b128 v224, a[36:39] offset:30720\n\t"
"s_waitcnt vmcnt(36)\n\t"
"v_mfma_f32_16x16x32_bf16 v[32:35], a[244:247], a[168:171], v[32:35]\n\t"
"ds_read_b128 a[48:51], v244 offset:52736\n\t"
"v_mfma_f32_16x16x32_bf16 v[32:35], a[164:167], a[172:175], v[32:35]\n\t"
"ds_read_b128 a[52:55], v244 offset:53248\n\t"
"v_mfma_f32_16x16x32_bf16 v[32:35], v[96:99], a[176:179], v[32:35]\n\t"
"ds_write_b64 v250, a[138:139] offset:24576\n\t"
"ds_write_b64 v225, a[146:147] offset:6144\n\t"
"ds_write_b64 v225, a[154:155] offset:18432\n\t"
"ds_write_b64 v225, a[162:163] offset:30720\n\t"
"s_waitcnt vmcnt(33)\n\t"
"v_mfma_f32_16x16x32_bf16 v[32:35], a[220:223], a[180:183], v[32:35]\n\t"
"v_cvt_pk_bf16_f32 v0, v0, v1\n\t"
"v_cvt_pk_bf16_f32 v1, v2, v3\n\t"
"v_mfma_f32_16x16x32_bf16 v[32:35], a[248:251], a[184:187], v[32:35]\n\t"
"v_mfma_f32_16x16x32_bf16 v[32:35], a[56:59], a[188:191], v[32:35]\n\t"
"s_add_u32 s29, s22, 590208\n\t"
"s_waitcnt lgkmcnt(15)\n\t"
"buffer_store_dwordx4 v[196:199], v248, s[16:19], s29 offen offset:0 nt\n\t"
"s_waitcnt vmcnt(31)\n\t"
"v_mfma_f32_16x16x32_bf16 v[64:67], a[104:107], a[168:171], v[64:67]\n\t"
"ds_read_b128 a[168:171], v244 offset:1536\n\t"
"v_mfma_f32_16x16x32_bf16 v[64:67], a[252:255], a[172:175], v[64:67]\n\t"
"v_mfma_f32_16x16x32_bf16 v[64:67], a[224:227], a[176:179], v[64:67]\n\t"
"s_add_u32 s29, s22, 590208\n\t"
"buffer_store_dwordx4 v[200:203], v248, s[16:19], s29 offen offset:128 nt\n\t"
"s_waitcnt lgkmcnt(1)\n\t"
"s_barrier\n\t"
"s_waitcnt vmcnt(26)\n\t"
"v_mfma_f32_16x16x32_bf16 v[64:67], a[60:63], a[180:183], v[64:67]\n\t"
"ds_read_b128 v[196:199], v252 offset:18432\n\t"
"v_cvt_pk_bf16_f32 v2, v32, v33\n\t"
"v_mfma_f32_16x16x32_bf16 v[64:67], a[64:67], a[184:187], v[64:67]\n\t"
"v_cvt_pk_bf16_f32 v3, v34, v35\n\t"
"ds_read_b128 v[200:203], v252 offset:19456\n\t"
"v_mfma_f32_16x16x32_bf16 v[64:67], a[68:71], a[188:191], v[64:67]\n\t"
"s_add_u32 s29, s22, 590208\n\t"
"buffer_store_dwordx4 v[204:207], v248, s[16:19], s29 offen offset:256 nt\n\t"
"s_waitcnt vmcnt(23)\n\t"
"v_mfma_f32_16x16x32_bf16 v[100:103], a[72:75], a[192:195], v[100:103]\n\t"
"ds_read_b128 v[208:211], v252 offset:20480\n\t"
"v_mfma_f32_16x16x32_bf16 v[100:103], a[112:115], a[40:43], v[100:103]\n\t"
"ds_read_b128 v[212:215], v226 offset:0\n\t"
"v_mfma_f32_16x16x32_bf16 v[100:103], a[76:79], a[44:47], v[100:103]\n\t"
"ds_read_b128 a[180:183], v244 offset:44544\n\t"
"ds_read_b128 v[204:207], v226 offset:1024\n\t"
"s_waitcnt vmcnt(18)\n\t"
"v_mfma_f32_16x16x32_bf16 v[100:103], a[80:83], a[196:199], v[100:103]\n\t"
"v_cvt_pk_bf16_f32 v64, v64, v65\n\t"
"v_cvt_pk_bf16_f32 v65, v66, v67\n\t"
"v_mfma_f32_16x16x32_bf16 v[100:103], a[84:87], a[48:51], v[100:103]\n\t"
"ds_read_b128 v[216:219], v226 offset:2048\n\t"
"v_mfma_f32_16x16x32_bf16 v[100:103], a[208:211], a[52:55], v[100:103]\n\t"
"ds_write_b128 v249, v[0:3] offset:30720\n\t"
"s_waitcnt vmcnt(15)\n\t"
"v_mfma_f32_16x16x32_bf16 v[132:135], a[212:215], a[192:195], v[132:135]\n\t"
"ds_read_b128 a[0:3], v226 offset:12288\n\t"
"v_mfma_f32_16x16x32_bf16 v[132:135], a[88:91], a[40:43], v[132:135]\n\t"
"v_mfma_f32_16x16x32_bf16 v[132:135], a[120:123], a[44:47], v[132:135]\n\t"
"s_add_u32 s29, s22, 786816\n\t"
"s_waitcnt lgkmcnt(8)\n\t"
"buffer_store_dwordx4 v[196:199], v248, s[16:19], s29 offen offset:0 nt\n\t"
"s_waitcnt vmcnt(10)\n\t"
"v_mfma_f32_16x16x32_bf16 v[132:135], a[92:95], a[196:199], v[132:135]\n\t"
"v_cvt_pk_bf16_f32 v100, v100, v101\n\t"
"v_cvt_pk_bf16_f32 v101, v102, v103\n\t"
"v_mfma_f32_16x16x32_bf16 v[132:135], a[96:99], a[48:51], v[132:135]\n\t"
"ds_read_b128 a[4:7], v226 offset:13312\n\t"
"v_mfma_f32_16x16x32_bf16 v[132:135], a[100:103], a[52:55], v[132:135]\n\t"
"s_add_u32 s29, s22, 786816\n\t"
"s_waitcnt lgkmcnt(8)\n\t"
"buffer_store_dwordx4 v[200:203], v248, s[16:19], s29 offen offset:128 nt\n\t"
"s_waitcnt vmcnt(8)\n\t"
"v_mfma_f32_16x16x32_bf16 v[164:167], a[108:111], a[192:195], v[164:167]\n\t"
"ds_read_b128 a[12:15], v226 offset:24576\n\t"
"v_mfma_f32_16x16x32_bf16 v[164:167], a[216:219], a[40:43], v[164:167]\n\t"
"ds_read_b128 a[8:11], v226 offset:14336\n\t"
"v_mfma_f32_16x16x32_bf16 v[164:167], a[116:119], a[44:47], v[164:167]\n\t"
"s_add_u32 s29, s22, 786816\n\t"
"s_waitcnt lgkmcnt(9)\n\t"
"buffer_store_dwordx4 v[208:211], v248, s[16:19], s29 offen offset:256 nt\n\t"
"s_waitcnt vmcnt(6)\n\t"
"v_mfma_f32_16x16x32_bf16 v[164:167], v[236:239], a[196:199], v[164:167]\n\t"
"v_cvt_pk_bf16_f32 v102, v132, v133\n\t"
"v_cvt_pk_bf16_f32 v103, v134, v135\n\t"
"v_mfma_f32_16x16x32_bf16 v[164:167], a[128:131], a[48:51], v[164:167]\n\t"
"ds_read_b128 a[192:195], v244 offset:10752\n\t"
"v_mfma_f32_16x16x32_bf16 v[164:167], a[228:231], a[52:55], v[164:167]\n\t"
"s_add_u32 s29, s22, 983424\n\t"
"s_waitcnt lgkmcnt(9)\n\t"
"buffer_store_dwordx4 v[212:215], v248, s[16:19], s29 offen offset:0 nt\n\t"
"v_mfma_f32_16x16x32_bf16 v[4:7], a[132:135], a[172:175], v[4:7]\n\t"
"ds_read_b128 a[16:19], v226 offset:25600\n\t"
"v_mfma_f32_16x16x32_bf16 v[4:7], a[140:143], a[176:179], v[4:7]\n\t"
"ds_read_b128 v[196:199], v226 offset:26624\n\t"
"v_mfma_f32_16x16x32_bf16 v[4:7], a[148:151], a[168:171], v[4:7]\n\t"
"s_add_u32 s29, s22, 983424\n\t"
"s_waitcnt lgkmcnt(9)\n\t"
"buffer_store_dwordx4 v[204:207], v248, s[16:19], s29 offen offset:128 nt\n\t"
"v_mfma_f32_16x16x32_bf16 v[4:7], a[156:159], a[184:187], v[4:7]\n\t"
"v_cvt_pk_bf16_f32 v164, v164, v165\n\t"
"v_cvt_pk_bf16_f32 v165, v166, v167\n\t"
"v_mfma_f32_16x16x32_bf16 v[4:7], a[236:239], a[188:191], v[4:7]\n\t"
"ds_read_b128 a[196:199], v244 offset:53760\n\t"
"v_mfma_f32_16x16x32_bf16 v[4:7], a[240:243], a[180:183], v[4:7]\n\t"
"s_add_u32 s29, s22, 983424\n\t"
"s_waitcnt lgkmcnt(9)\n\t"
"buffer_store_dwordx4 v[216:219], v248, s[16:19], s29 offen offset:256 nt\n\t"
"v_mfma_f32_16x16x32_bf16 v[36:39], a[244:247], a[172:175], v[36:39]\n\t"
"v_mfma_f32_16x16x32_bf16 v[36:39], a[164:167], a[176:179], v[36:39]\n\t"
"v_mfma_f32_16x16x32_bf16 v[36:39], v[96:99], a[168:171], v[36:39]\n\t"
"ds_write_b64 v250, v[64:65] offset:30720\n\t"
"ds_write_b128 v249, v[100:103] offset:36864\n\t"
"v_mfma_f32_16x16x32_bf16 v[36:39], a[220:223], a[184:187], v[36:39]\n\t"
"v_cvt_pk_bf16_f32 v4, v4, v5\n\t"
"v_cvt_pk_bf16_f32 v5, v6, v7\n\t"
"v_mfma_f32_16x16x32_bf16 v[36:39], a[248:251], a[188:191], v[36:39]\n\t"
"v_mfma_f32_16x16x32_bf16 v[36:39], a[56:59], a[180:183], v[36:39]\n\t"
"ds_write_b64 v250, v[164:165] offset:36864\n\t"
"v_mfma_f32_16x16x32_bf16 v[68:71], a[104:107], a[172:175], v[68:71]\n\t"
"ds_read_b128 a[172:175], v244 offset:2048\n\t"
"v_mfma_f32_16x16x32_bf16 v[68:71], a[252:255], a[176:179], v[68:71]\n\t"
"v_mfma_f32_16x16x32_bf16 v[68:71], a[224:227], a[168:171], v[68:71]\n\t"
"s_add_u32 s29, s22, 1180032\n\t"
"s_waitcnt lgkmcnt(11)\n\t"
"buffer_store_dwordx4 a[0:3], v248, s[16:19], s29 offen offset:0 nt\n\t"
"s_waitcnt lgkmcnt(1)\n\t"
"s_barrier\n\t"
"v_mfma_f32_16x16x32_bf16 v[68:71], a[60:63], a[184:187], v[68:71]\n\t"
"ds_read_b128 v[200:203], v252 offset:30720\n\t"
"v_cvt_pk_bf16_f32 v6, v36, v37\n\t"
"v_mfma_f32_16x16x32_bf16 v[68:71], a[64:67], a[188:191], v[68:71]\n\t"
"v_cvt_pk_bf16_f32 v7, v38, v39\n\t"
"ds_read_b128 v[204:207], v252 offset:31744\n\t"
"v_mfma_f32_16x16x32_bf16 v[68:71], a[68:71], a[180:183], v[68:71]\n\t"
"s_add_u32 s29, s22, 1180032\n\t"
"buffer_store_dwordx4 a[4:7], v248, s[16:19], s29 offen offset:128 nt\n\t"
"v_mfma_f32_16x16x32_bf16 v[104:107], a[72:75], a[40:43], v[104:107]\n\t"
"ds_read_b128 v[208:211], v252 offset:32768\n\t"
"v_mfma_f32_16x16x32_bf16 v[104:107], a[112:115], a[44:47], v[104:107]\n\t"
"ds_read_b128 a[184:187], v244 offset:45056\n\t"
"v_mfma_f32_16x16x32_bf16 v[104:107], a[76:79], a[192:195], v[104:107]\n\t"
"s_add_u32 s29, s22, 1180032\n\t"
"buffer_store_dwordx4 a[8:11], v248, s[16:19], s29 offen offset:256 nt\n\t"
"v_mfma_f32_16x16x32_bf16 v[104:107], a[80:83], a[48:51], v[104:107]\n\t"
"v_cvt_pk_bf16_f32 v68, v68, v69\n\t"
"v_cvt_pk_bf16_f32 v69, v70, v71\n\t"
"v_mfma_f32_16x16x32_bf16 v[104:107], a[84:87], a[52:55], v[104:107]\n\t"
"v_mfma_f32_16x16x32_bf16 v[104:107], a[208:211], a[196:199], v[104:107]\n\t"
"s_add_u32 s29, s22, 1376640\n\t"
"buffer_store_dwordx4 a[12:15], v248, s[16:19], s29 offen offset:0 nt\n\t"
"ds_write_b128 v249, v[4:7] offset:18432\n\t"
"v_mfma_f32_16x16x32_bf16 v[136:139], a[212:215], a[40:43], v[136:139]\n\t"
"v_mfma_f32_16x16x32_bf16 v[136:139], a[88:91], a[44:47], v[136:139]\n\t"
"v_mfma_f32_16x16x32_bf16 v[136:139], a[120:123], a[192:195], v[136:139]\n\t"
"s_add_u32 s29, s22, 1376640\n\t"
"buffer_store_dwordx4 a[16:19], v248, s[16:19], s29 offen offset:128 nt\n\t"
"v_mfma_f32_16x16x32_bf16 v[136:139], a[92:95], a[48:51], v[136:139]\n\t"
"v_cvt_pk_bf16_f32 v104, v104, v105\n\t"
"v_cvt_pk_bf16_f32 v105, v106, v107\n\t"
"v_mfma_f32_16x16x32_bf16 v[136:139], a[96:99], a[52:55], v[136:139]\n\t"
"v_mfma_f32_16x16x32_bf16 v[136:139], a[100:103], a[196:199], v[136:139]\n\t"
"s_add_u32 s29, s22, 1376640\n\t"
"buffer_store_dwordx4 v[196:199], v248, s[16:19], s29 offen offset:256 nt\n\t"
"ds_write_b64 v250, v[68:69] offset:18432\n\t"
"v_mfma_f32_16x16x32_bf16 v[168:171], a[108:111], a[40:43], v[168:171]\n\t"
"ds_read_b128 a[200:203], v244 offset:11264\n\t"
"v_mfma_f32_16x16x32_bf16 v[168:171], a[216:219], a[44:47], v[168:171]\n\t"
"v_mfma_f32_16x16x32_bf16 v[168:171], a[116:119], a[192:195], v[168:171]\n\t"
"s_add_u32 s29, s23, 0\n\t"
"s_waitcnt lgkmcnt(6)\n\t"
"buffer_store_dwordx4 v[200:203], v248, s[16:19], s29 offen offset:0 nt\n\t"
"v_mfma_f32_16x16x32_bf16 v[168:171], v[236:239], a[48:51], v[168:171]\n\t"
"v_cvt_pk_bf16_f32 v106, v136, v137\n\t"
"v_cvt_pk_bf16_f32 v107, v138, v139\n\t"
"v_mfma_f32_16x16x32_bf16 v[168:171], a[128:131], a[52:55], v[168:171]\n\t"
"ds_read_b128 a[204:207], v244 offset:54272\n\t"
"v_mfma_f32_16x16x32_bf16 v[168:171], a[228:231], a[196:199], v[168:171]\n\t"
"s_add_u32 s29, s23, 0\n\t"
"s_waitcnt lgkmcnt(6)\n\t"
"buffer_store_dwordx4 v[204:207], v248, s[16:19], s29 offen offset:128 nt\n\t"
"v_mfma_f32_16x16x32_bf16 v[8:11], a[132:135], a[176:179], v[8:11]\n\t"
"v_mfma_f32_16x16x32_bf16 v[8:11], a[140:143], a[168:171], v[8:11]\n\t"
"v_mfma_f32_16x16x32_bf16 v[8:11], a[148:151], a[172:175], v[8:11]\n\t"
"s_add_u32 s29, s23, 0\n\t"
"s_waitcnt lgkmcnt(5)\n\t"
"buffer_store_dwordx4 v[208:211], v248, s[16:19], s29 offen offset:256 nt\n\t"
"s_waitcnt lgkmcnt(4)\n\t"
"v_mfma_f32_16x16x32_bf16 v[8:11], a[156:159], a[188:191], v[8:11]\n\t"
"v_cvt_pk_bf16_f32 v168, v168, v169\n\t"
"v_cvt_pk_bf16_f32 v169, v170, v171\n\t"
"v_mfma_f32_16x16x32_bf16 v[8:11], a[236:239], a[180:183], v[8:11]\n\t"
"v_mfma_f32_16x16x32_bf16 v[8:11], a[240:243], a[184:187], v[8:11]\n\t"
"ds_write_b128 v249, v[104:107] offset:24576\n\t"
"v_mfma_f32_16x16x32_bf16 v[40:43], a[244:247], a[176:179], v[40:43]\n\t"
"v_mfma_f32_16x16x32_bf16 v[40:43], a[164:167], a[168:171], v[40:43]\n\t"
"v_mfma_f32_16x16x32_bf16 v[40:43], v[96:99], a[172:175], v[40:43]\n\t"
"ds_write_b64 v250, v[168:169] offset:24576\n\t"
"v_mfma_f32_16x16x32_bf16 v[40:43], a[220:223], a[188:191], v[40:43]\n\t"
"s_nop 1\n\t"
"v_cvt_pk_bf16_f32 v8, v8, v9\n\t"
"v_cvt_pk_bf16_f32 v9, v10, v11\n\t"
"v_mfma_f32_16x16x32_bf16 v[40:43], a[248:251], a[180:183], v[40:43]\n\t"
"v_mfma_f32_16x16x32_bf16 v[40:43], a[56:59], a[184:187], v[40:43]\n\t"
"v_mfma_f32_16x16x32_bf16 v[72:75], a[104:107], a[176:179], v[72:75]\n\t"
"ds_read_b128 a[176:179], v244 offset:2560\n\t"
"v_mfma_f32_16x16x32_bf16 v[72:75], a[252:255], a[168:171], v[72:75]\n\t"
"v_mfma_f32_16x16x32_bf16 v[72:75], a[224:227], a[172:175], v[72:75]\n\t"
"s_waitcnt lgkmcnt(1)\n\t"
"s_barrier\n\t"
"v_mfma_f32_16x16x32_bf16 v[72:75], a[60:63], a[188:191], v[72:75]\n\t"
"ds_read_b128 v[196:199], v252 offset:18432\n\t"
"v_cvt_pk_bf16_f32 v10, v40, v41\n\t"
"v_mfma_f32_16x16x32_bf16 v[72:75], a[64:67], a[180:183], v[72:75]\n\t"
"v_cvt_pk_bf16_f32 v11, v42, v43\n\t"
"ds_read_b128 v[200:203], v252 offset:19456\n\t"
"v_mfma_f32_16x16x32_bf16 v[72:75], a[68:71], a[184:187], v[72:75]\n\t"
"ds_read_b128 v[204:207], v252 offset:20480\n\t"
"ds_read_b128 a[188:191], v244 offset:45568\n\t"
"v_mfma_f32_16x16x32_bf16 v[108:111], a[72:75], a[44:47], v[108:111]\n\t"
"v_mfma_f32_16x16x32_bf16 v[108:111], a[112:115], a[192:195], v[108:111]\n\t"
"v_mfma_f32_16x16x32_bf16 v[108:111], a[76:79], a[200:203], v[108:111]\n\t"
"ds_write_b128 v249, v[8:11] offset:30720\n\t"
"v_mfma_f32_16x16x32_bf16 v[108:111], a[80:83], a[52:55], v[108:111]\n\t"
"s_nop 0\n\t"
"v_cvt_pk_bf16_f32 v72, v72, v73\n\t"
"v_mfma_f32_16x16x32_bf16 v[108:111], a[84:87], a[196:199], v[108:111]\n\t"
"v_cvt_pk_bf16_f32 v73, v74, v75\n\t"
"v_mfma_f32_16x16x32_bf16 v[108:111], a[208:211], a[204:207], v[108:111]\n\t"
"ds_write_b64 v250, v[72:73] offset:30720\n\t"
"v_mfma_f32_16x16x32_bf16 v[140:143], a[212:215], a[44:47], v[140:143]\n\t"
"v_mfma_f32_16x16x32_bf16 v[140:143], a[88:91], a[192:195], v[140:143]\n\t"
"v_mfma_f32_16x16x32_bf16 v[140:143], a[120:123], a[200:203], v[140:143]\n\t"
"v_mfma_f32_16x16x32_bf16 v[140:143], a[92:95], a[52:55], v[140:143]\n\t"
"s_nop 2\n\t"
"v_cvt_pk_bf16_f32 v108, v108, v109\n\t"
"v_cvt_pk_bf16_f32 v109, v110, v111\n\t"
"v_mfma_f32_16x16x32_bf16 v[140:143], a[96:99], a[196:199], v[140:143]\n\t"
"v_mfma_f32_16x16x32_bf16 v[140:143], a[100:103], a[204:207], v[140:143]\n\t"
"s_add_u32 s29, s23, 196608\n\t"
"s_waitcnt lgkmcnt(5)\n\t"
"buffer_store_dwordx4 v[196:199], v248, s[16:19], s29 offen offset:0 nt\n\t"
"v_mfma_f32_16x16x32_bf16 v[172:175], a[108:111], a[44:47], v[172:175]\n\t"
"ds_read_b128 v[208:211], v244 offset:11776\n\t"
"v_mfma_f32_16x16x32_bf16 v[172:175], a[216:219], a[192:195], v[172:175]\n\t"
"v_mfma_f32_16x16x32_bf16 v[172:175], a[116:119], a[200:203], v[172:175]\n\t"
"s_add_u32 s29, s23, 196608\n\t"
"s_waitcnt lgkmcnt(5)\n\t"
"buffer_store_dwordx4 v[200:203], v248, s[16:19], s29 offen offset:128 nt\n\t"
"v_mfma_f32_16x16x32_bf16 v[172:175], v[236:239], a[52:55], v[172:175]\n\t"
"v_cvt_pk_bf16_f32 v110, v140, v141\n\t"
"v_cvt_pk_bf16_f32 v111, v142, v143\n\t"
"v_mfma_f32_16x16x32_bf16 v[172:175], a[128:131], a[196:199], v[172:175]\n\t"
"ds_read_b128 v[212:215], v244 offset:54784\n\t"
"v_mfma_f32_16x16x32_bf16 v[172:175], a[228:231], a[204:207], v[172:175]\n\t"
"s_add_u32 s29, s23, 196608\n\t"
"s_waitcnt lgkmcnt(5)\n\t"
"buffer_store_dwordx4 v[204:207], v248, s[16:19], s29 offen offset:256 nt\n\t"
"v_mfma_f32_16x16x32_bf16 v[12:15], a[132:135], a[168:171], v[12:15]\n\t"
"v_mfma_f32_16x16x32_bf16 v[12:15], a[140:143], a[172:175], v[12:15]\n\t"
"v_mfma_f32_16x16x32_bf16 v[12:15], a[148:151], a[176:179], v[12:15]\n\t"
"ds_write_b128 v249, v[108:111] offset:36864\n\t"
"s_waitcnt lgkmcnt(5)\n\t"
"v_mfma_f32_16x16x32_bf16 v[12:15], a[156:159], a[180:183], v[12:15]\n\t"
"v_cvt_pk_bf16_f32 v172, v172, v173\n\t"
"v_cvt_pk_bf16_f32 v173, v174, v175\n\t"
"v_mfma_f32_16x16x32_bf16 v[12:15], a[236:239], a[184:187], v[12:15]\n\t"
"v_mfma_f32_16x16x32_bf16 v[12:15], a[240:243], a[188:191], v[12:15]\n\t"
"ds_write_b64 v250, v[172:173] offset:36864\n\t"
"v_mfma_f32_16x16x32_bf16 v[44:47], a[244:247], a[168:171], v[44:47]\n\t"
"v_mfma_f32_16x16x32_bf16 v[44:47], a[164:167], a[172:175], v[44:47]\n\t"
"v_mfma_f32_16x16x32_bf16 v[44:47], v[96:99], a[176:179], v[44:47]\n\t"
"v_mfma_f32_16x16x32_bf16 v[44:47], a[220:223], a[180:183], v[44:47]\n\t"
"s_nop 2\n\t"
"v_cvt_pk_bf16_f32 v12, v12, v13\n\t"
"v_cvt_pk_bf16_f32 v13, v14, v15\n\t"
"v_mfma_f32_16x16x32_bf16 v[44:47], a[248:251], a[184:187], v[44:47]\n\t"
"v_mfma_f32_16x16x32_bf16 v[44:47], a[56:59], a[188:191], v[44:47]\n\t"
"v_mfma_f32_16x16x32_bf16 v[76:79], a[104:107], a[168:171], v[76:79]\n\t"
"ds_read_b128 a[168:171], v244 offset:3072\n\t"
"v_mfma_f32_16x16x32_bf16 v[76:79], a[252:255], a[172:175], v[76:79]\n\t"
"v_mfma_f32_16x16x32_bf16 v[76:79], a[224:227], a[176:179], v[76:79]\n\t"
"s_waitcnt lgkmcnt(1)\n\t"
"s_barrier\n\t"
"v_mfma_f32_16x16x32_bf16 v[76:79], a[60:63], a[180:183], v[76:79]\n\t"
"ds_read_b128 v[196:199], v252 offset:30720\n\t"
"v_cvt_pk_bf16_f32 v14, v44, v45\n\t"
"v_mfma_f32_16x16x32_bf16 v[76:79], a[64:67], a[184:187], v[76:79]\n\t"
"v_cvt_pk_bf16_f32 v15, v46, v47\n\t"
"ds_read_b128 v[200:203], v252 offset:31744\n\t"
"v_mfma_f32_16x16x32_bf16 v[76:79], a[68:71], a[188:191], v[76:79]\n\t"
"ds_read_b128 v[204:207], v252 offset:32768\n\t"
"ds_read_b128 a[180:183], v244 offset:46080\n\t"
"v_mfma_f32_16x16x32_bf16 v[112:115], a[72:75], a[192:195], v[112:115]\n\t"
"v_mfma_f32_16x16x32_bf16 v[112:115], a[112:115], a[200:203], v[112:115]\n\t"
"v_mfma_f32_16x16x32_bf16 v[112:115], a[76:79], v[208:211], v[112:115]\n\t"
"ds_write_b128 v249, v[12:15] offset:18432\n\t"
"v_mfma_f32_16x16x32_bf16 v[112:115], a[80:83], a[196:199], v[112:115]\n\t"
"s_nop 0\n\t"
"v_cvt_pk_bf16_f32 v76, v76, v77\n\t"
"v_mfma_f32_16x16x32_bf16 v[112:115], a[84:87], a[204:207], v[112:115]\n\t"
"v_cvt_pk_bf16_f32 v77, v78, v79\n\t"
"v_mfma_f32_16x16x32_bf16 v[112:115], a[208:211], v[212:215], v[112:115]\n\t"
"ds_write_b64 v250, v[76:77] offset:18432\n\t"
"v_mfma_f32_16x16x32_bf16 v[144:147], a[212:215], a[192:195], v[144:147]\n\t"
"v_mfma_f32_16x16x32_bf16 v[144:147], a[88:91], a[200:203], v[144:147]\n\t"
"v_mfma_f32_16x16x32_bf16 v[144:147], a[120:123], v[208:211], v[144:147]\n\t"
"v_mfma_f32_16x16x32_bf16 v[144:147], a[92:95], a[196:199], v[144:147]\n\t"
"s_nop 2\n\t"
"v_cvt_pk_bf16_f32 v112, v112, v113\n\t"
"v_cvt_pk_bf16_f32 v113, v114, v115\n\t"
"v_mfma_f32_16x16x32_bf16 v[144:147], a[96:99], a[204:207], v[144:147]\n\t"
"v_mfma_f32_16x16x32_bf16 v[144:147], a[100:103], v[212:215], v[144:147]\n\t"
"s_add_u32 s29, s23, 393216\n\t"
"s_waitcnt lgkmcnt(5)\n\t"
"buffer_store_dwordx4 v[196:199], v248, s[16:19], s29 offen offset:0 nt\n\t"
"v_mfma_f32_16x16x32_bf16 v[176:179], a[108:111], a[192:195], v[176:179]\n\t"
"ds_read_b128 a[192:195], v244 offset:12288\n\t"
"v_mfma_f32_16x16x32_bf16 v[176:179], a[216:219], a[200:203], v[176:179]\n\t"
"v_mfma_f32_16x16x32_bf16 v[176:179], a[116:119], v[208:211], v[176:179]\n\t"
"s_add_u32 s29, s23, 393216\n\t"
"s_waitcnt lgkmcnt(5)\n\t"
"buffer_store_dwordx4 v[200:203], v248, s[16:19], s29 offen offset:128 nt\n\t"
"v_mfma_f32_16x16x32_bf16 v[176:179], v[236:239], a[196:199], v[176:179]\n\t"
"v_cvt_pk_bf16_f32 v114, v144, v145\n\t"
"v_cvt_pk_bf16_f32 v115, v146, v147\n\t"
"v_mfma_f32_16x16x32_bf16 v[176:179], a[128:131], a[204:207], v[176:179]\n\t"
"ds_read_b128 a[196:199], v244 offset:55296\n\t"
"v_mfma_f32_16x16x32_bf16 v[176:179], a[228:231], v[212:215], v[176:179]\n\t"
"s_add_u32 s29, s23, 393216\n\t"
"s_waitcnt lgkmcnt(5)\n\t"
"buffer_store_dwordx4 v[204:207], v248, s[16:19], s29 offen offset:256 nt\n\t"
"Lst35_%=:\n\t"
"v_mfma_f32_16x16x32_bf16 v[16:19], a[132:135], a[172:175], v[16:19]\n\t"
"v_mfma_f32_16x16x32_bf16 v[16:19], a[140:143], a[176:179], v[16:19]\n\t"
"v_mfma_f32_16x16x32_bf16 v[16:19], a[148:151], a[168:171], v[16:19]\n\t"
"ds_write_b128 v249, v[112:115] offset:24576\n\t"
"s_waitcnt lgkmcnt(5)\n\t"
"v_mfma_f32_16x16x32_bf16 v[16:19], a[156:159], a[184:187], v[16:19]\n\t"
"v_cvt_pk_bf16_f32 v176, v176, v177\n\t"
"v_cvt_pk_bf16_f32 v177, v178, v179\n\t"
"v_mfma_f32_16x16x32_bf16 v[16:19], a[236:239], a[188:191], v[16:19]\n\t"
"v_mfma_f32_16x16x32_bf16 v[16:19], a[240:243], a[180:183], v[16:19]\n\t"
"ds_write_b64 v250, v[176:177] offset:24576\n\t"
"v_mfma_f32_16x16x32_bf16 v[48:51], a[244:247], a[172:175], v[48:51]\n\t"
"v_mfma_f32_16x16x32_bf16 v[48:51], a[164:167], a[176:179], v[48:51]\n\t"
"v_mfma_f32_16x16x32_bf16 v[48:51], v[96:99], a[168:171], v[48:51]\n\t"
"v_mfma_f32_16x16x32_bf16 v[48:51], a[220:223], a[184:187], v[48:51]\n\t"
"s_nop 2\n\t"
"v_cvt_pk_bf16_f32 v16, v16, v17\n\t"
"v_cvt_pk_bf16_f32 v17, v18, v19\n\t"
"v_mfma_f32_16x16x32_bf16 v[48:51], a[248:251], a[188:191], v[48:51]\n\t"
"v_mfma_f32_16x16x32_bf16 v[48:51], a[56:59], a[180:183], v[48:51]\n\t"
"v_mfma_f32_16x16x32_bf16 v[80:83], a[104:107], a[172:175], v[80:83]\n\t"
"ds_read_b128 a[172:175], v244 offset:3584\n\t"
"v_mfma_f32_16x16x32_bf16 v[80:83], a[252:255], a[176:179], v[80:83]\n\t"
"v_mfma_f32_16x16x32_bf16 v[80:83], a[224:227], a[168:171], v[80:83]\n\t"
"s_waitcnt lgkmcnt(1)\n\t"
"s_barrier\n\t"
"v_mfma_f32_16x16x32_bf16 v[80:83], a[60:63], a[184:187], v[80:83]\n\t"
"ds_read_b128 v[196:199], v252 offset:18432\n\t"
"v_cvt_pk_bf16_f32 v18, v48, v49\n\t"
"v_mfma_f32_16x16x32_bf16 v[80:83], a[64:67], a[188:191], v[80:83]\n\t"
"v_cvt_pk_bf16_f32 v19, v50, v51\n\t"
"ds_read_b128 v[200:203], v252 offset:19456\n\t"
"v_mfma_f32_16x16x32_bf16 v[80:83], a[68:71], a[180:183], v[80:83]\n\t"
"ds_read_b128 v[204:207], v252 offset:20480\n\t"
"ds_read_b128 a[184:187], v244 offset:46592\n\t"
"v_mfma_f32_16x16x32_bf16 v[116:119], a[72:75], a[200:203], v[116:119]\n\t"
"v_mfma_f32_16x16x32_bf16 v[116:119], a[112:115], v[208:211], v[116:119]\n\t"
"v_mfma_f32_16x16x32_bf16 v[116:119], a[76:79], a[192:195], v[116:119]\n\t"
"ds_write_b128 v249, v[16:19] offset:30720\n\t"
"v_mfma_f32_16x16x32_bf16 v[116:119], a[80:83], a[204:207], v[116:119]\n\t"
"s_nop 0\n\t"
"v_cvt_pk_bf16_f32 v80, v80, v81\n\t"
"v_mfma_f32_16x16x32_bf16 v[116:119], a[84:87], v[212:215], v[116:119]\n\t"
"v_cvt_pk_bf16_f32 v81, v82, v83\n\t"
"v_mfma_f32_16x16x32_bf16 v[116:119], a[208:211], a[196:199], v[116:119]\n\t"
"ds_write_b64 v250, v[80:81] offset:30720\n\t"
"v_mfma_f32_16x16x32_bf16 v[148:151], a[212:215], a[200:203], v[148:151]\n\t"
"v_mfma_f32_16x16x32_bf16 v[148:151], a[88:91], v[208:211], v[148:151]\n\t"
"v_mfma_f32_16x16x32_bf16 v[148:151], a[120:123], a[192:195], v[148:151]\n\t"
"v_mfma_f32_16x16x32_bf16 v[148:151], a[92:95], a[204:207], v[148:151]\n\t"
"s_nop 2\n\t"
"v_cvt_pk_bf16_f32 v116, v116, v117\n\t"
"v_cvt_pk_bf16_f32 v117, v118, v119\n\t"
"v_mfma_f32_16x16x32_bf16 v[148:151], a[96:99], v[212:215], v[148:151]\n\t"
"v_mfma_f32_16x16x32_bf16 v[148:151], a[100:103], a[196:199], v[148:151]\n\t"
"s_add_u32 s29, s23, 589824\n\t"
"s_waitcnt lgkmcnt(5)\n\t"
"buffer_store_dwordx4 v[196:199], v248, s[16:19], s29 offen offset:0 nt\n\t"
"v_mfma_f32_16x16x32_bf16 v[180:183], a[108:111], a[200:203], v[180:183]\n\t"
"ds_read_b128 a[200:203], v244 offset:12800\n\t"
"v_mfma_f32_16x16x32_bf16 v[180:183], a[216:219], v[208:211], v[180:183]\n\t"
"v_mfma_f32_16x16x32_bf16 v[180:183], a[116:119], a[192:195], v[180:183]\n\t"
"s_add_u32 s29, s23, 589824\n\t"
"s_waitcnt lgkmcnt(5)\n\t"
"buffer_store_dwordx4 v[200:203], v248, s[16:19], s29 offen offset:128 nt\n\t"
"v_mfma_f32_16x16x32_bf16 v[180:183], v[236:239], a[204:207], v[180:183]\n\t"
"v_cvt_pk_bf16_f32 v118, v148, v149\n\t"
"v_cvt_pk_bf16_f32 v119, v150, v151\n\t"
"v_mfma_f32_16x16x32_bf16 v[180:183], a[128:131], v[212:215], v[180:183]\n\t"
"ds_read_b128 a[204:207], v244 offset:55808\n\t"
"v_mfma_f32_16x16x32_bf16 v[180:183], a[228:231], a[196:199], v[180:183]\n\t"
"s_add_u32 s29, s23, 589824\n\t"
"s_waitcnt lgkmcnt(5)\n\t"
"buffer_store_dwordx4 v[204:207], v248, s[16:19], s29 offen offset:256 nt\n\t"
"v_mfma_f32_16x16x32_bf16 v[20:23], a[132:135], a[176:179], v[20:23]\n\t"
"v_mfma_f32_16x16x32_bf16 v[20:23], a[140:143], a[168:171], v[20:23]\n\t"
"v_mfma_f32_16x16x32_bf16 v[20:23], a[148:151], a[172:175], v[20:23]\n\t"
"ds_write_b128 v249, v[116:119] offset:36864\n\t"
"s_waitcnt lgkmcnt(5)\n\t"
"v_mfma_f32_16x16x32_bf16 v[20:23], a[156:159], a[188:191], v[20:23]\n\t"
"v_cvt_pk_bf16_f32 v180, v180, v181\n\t"
"v_cvt_pk_bf16_f32 v181, v182, v183\n\t"
"v_mfma_f32_16x16x32_bf16 v[20:23], a[236:239], a[180:183], v[20:23]\n\t"
"v_mfma_f32_16x16x32_bf16 v[20:23], a[240:243], a[184:187], v[20:23]\n\t"
"ds_write_b64 v250, v[180:181] offset:36864\n\t"
"v_mfma_f32_16x16x32_bf16 v[52:55], a[244:247], a[176:179], v[52:55]\n\t"
"v_mfma_f32_16x16x32_bf16 v[52:55], a[164:167], a[168:171], v[52:55]\n\t"
"v_mfma_f32_16x16x32_bf16 v[52:55], v[96:99], a[172:175], v[52:55]\n\t"
"v_mfma_f32_16x16x32_bf16 v[52:55], a[220:223], a[188:191], v[52:55]\n\t"
"s_nop 2\n\t"
"v_cvt_pk_bf16_f32 v20, v20, v21\n\t"
"v_cvt_pk_bf16_f32 v21, v22, v23\n\t"
"v_mfma_f32_16x16x32_bf16 v[52:55], a[248:251], a[180:183], v[52:55]\n\t"
"v_mfma_f32_16x16x32_bf16 v[52:55], a[56:59], a[184:187], v[52:55]\n\t"
"v_mfma_f32_16x16x32_bf16 v[84:87], a[104:107], a[176:179], v[84:87]\n\t"
"ds_read_b128 a[176:179], v244 offset:4096\n\t"
"v_mfma_f32_16x16x32_bf16 v[84:87], a[252:255], a[168:171], v[84:87]\n\t"
"v_mfma_f32_16x16x32_bf16 v[84:87], a[224:227], a[172:175], v[84:87]\n\t"
"s_waitcnt lgkmcnt(1)\n\t"
"s_barrier\n\t"
"v_mfma_f32_16x16x32_bf16 v[84:87], a[60:63], a[188:191], v[84:87]\n\t"
"ds_read_b128 v[196:199], v252 offset:30720\n\t"
"v_cvt_pk_bf16_f32 v22, v52, v53\n\t"
"v_mfma_f32_16x16x32_bf16 v[84:87], a[64:67], a[180:183], v[84:87]\n\t"
"v_cvt_pk_bf16_f32 v23, v54, v55\n\t"
"ds_read_b128 v[200:203], v252 offset:31744\n\t"
"v_mfma_f32_16x16x32_bf16 v[84:87], a[68:71], a[184:187], v[84:87]\n\t"
"ds_read_b128 v[204:207], v252 offset:32768\n\t"
"ds_read_b128 a[188:191], v244 offset:47104\n\t"
"v_mfma_f32_16x16x32_bf16 v[120:123], a[72:75], v[208:211], v[120:123]\n\t"
"v_mfma_f32_16x16x32_bf16 v[120:123], a[112:115], a[192:195], v[120:123]\n\t"
"v_mfma_f32_16x16x32_bf16 v[120:123], a[76:79], a[200:203], v[120:123]\n\t"
"ds_write_b128 v249, v[20:23] offset:18432\n\t"
"v_mfma_f32_16x16x32_bf16 v[120:123], a[80:83], v[212:215], v[120:123]\n\t"
"s_nop 0\n\t"
"v_cvt_pk_bf16_f32 v84, v84, v85\n\t"
"v_mfma_f32_16x16x32_bf16 v[120:123], a[84:87], a[196:199], v[120:123]\n\t"
"v_cvt_pk_bf16_f32 v85, v86, v87\n\t"
"v_mfma_f32_16x16x32_bf16 v[120:123], a[208:211], a[204:207], v[120:123]\n\t"
"ds_write_b64 v250, v[84:85] offset:18432\n\t"
"v_mfma_f32_16x16x32_bf16 v[152:155], a[212:215], v[208:211], v[152:155]\n\t"
"v_mfma_f32_16x16x32_bf16 v[152:155], a[88:91], a[192:195], v[152:155]\n\t"
"v_mfma_f32_16x16x32_bf16 v[152:155], a[120:123], a[200:203], v[152:155]\n\t"
"v_mfma_f32_16x16x32_bf16 v[152:155], a[92:95], v[212:215], v[152:155]\n\t"
"s_nop 2\n\t"
"v_cvt_pk_bf16_f32 v120, v120, v121\n\t"
"v_cvt_pk_bf16_f32 v121, v122, v123\n\t"
"v_mfma_f32_16x16x32_bf16 v[152:155], a[96:99], a[196:199], v[152:155]\n\t"
"v_mfma_f32_16x16x32_bf16 v[152:155], a[100:103], a[204:207], v[152:155]\n\t"
"s_add_u32 s29, s23, 786432\n\t"
"s_waitcnt lgkmcnt(5)\n\t"
"buffer_store_dwordx4 v[196:199], v248, s[16:19], s29 offen offset:0 nt\n\t"
"v_mfma_f32_16x16x32_bf16 v[184:187], a[108:111], v[208:211], v[184:187]\n\t"
"ds_read_b128 v[208:211], v244 offset:13312\n\t"
"v_mfma_f32_16x16x32_bf16 v[184:187], a[216:219], a[192:195], v[184:187]\n\t"
"v_mfma_f32_16x16x32_bf16 v[184:187], a[116:119], a[200:203], v[184:187]\n\t"
"s_add_u32 s29, s23, 786432\n\t"
"s_waitcnt lgkmcnt(5)\n\t"
"buffer_store_dwordx4 v[200:203], v248, s[16:19], s29 offen offset:128 nt\n\t"
"v_mfma_f32_16x16x32_bf16 v[184:187], v[236:239], v[212:215], v[184:187]\n\t"
"v_cvt_pk_bf16_f32 v122, v152, v153\n\t"
"v_cvt_pk_bf16_f32 v123, v154, v155\n\t"
"v_mfma_f32_16x16x32_bf16 v[184:187], a[128:131], a[196:199], v[184:187]\n\t"
"ds_read_b128 v[212:215], v244 offset:56320\n\t"
"v_mfma_f32_16x16x32_bf16 v[184:187], a[228:231], a[204:207], v[184:187]\n\t"
"s_add_u32 s29, s23, 786432\n\t"
"s_waitcnt lgkmcnt(5)\n\t"
"buffer_store_dwordx4 v[204:207], v248, s[16:19], s29 offen offset:256 nt\n\t"
"v_mfma_f32_16x16x32_bf16 v[24:27], a[132:135], a[168:171], v[24:27]\n\t"
"v_mfma_f32_16x16x32_bf16 v[24:27], a[140:143], a[172:175], v[24:27]\n\t"
"v_mfma_f32_16x16x32_bf16 v[24:27], a[148:151], a[176:179], v[24:27]\n\t"
"ds_write_b128 v249, v[120:123] offset:24576\n\t"
"s_waitcnt lgkmcnt(5)\n\t"
"v_mfma_f32_16x16x32_bf16 v[24:27], a[156:159], a[180:183], v[24:27]\n\t"
"v_cvt_pk_bf16_f32 v184, v184, v185\n\t"
"v_cvt_pk_bf16_f32 v185, v186, v187\n\t"
"v_mfma_f32_16x16x32_bf16 v[24:27], a[236:239], a[184:187], v[24:27]\n\t"
"v_mfma_f32_16x16x32_bf16 v[24:27], a[240:243], a[188:191], v[24:27]\n\t"
"ds_write_b64 v250, v[184:185] offset:24576\n\t"
"v_mfma_f32_16x16x32_bf16 v[56:59], a[244:247], a[168:171], v[56:59]\n\t"
"v_mfma_f32_16x16x32_bf16 v[56:59], a[164:167], a[172:175], v[56:59]\n\t"
"v_mfma_f32_16x16x32_bf16 v[56:59], v[96:99], a[176:179], v[56:59]\n\t"
"v_mfma_f32_16x16x32_bf16 v[56:59], a[220:223], a[180:183], v[56:59]\n\t"
"s_nop 2\n\t"
"v_cvt_pk_bf16_f32 v24, v24, v25\n\t"
"v_cvt_pk_bf16_f32 v25, v26, v27\n\t"
"v_mfma_f32_16x16x32_bf16 v[56:59], a[248:251], a[184:187], v[56:59]\n\t"
"v_mfma_f32_16x16x32_bf16 v[56:59], a[56:59], a[188:191], v[56:59]\n\t"
"v_mfma_f32_16x16x32_bf16 v[88:91], a[104:107], a[168:171], v[88:91]\n\t"
"ds_read_b128 a[168:171], v244 offset:4608\n\t"
"v_mfma_f32_16x16x32_bf16 v[88:91], a[252:255], a[172:175], v[88:91]\n\t"
"v_mfma_f32_16x16x32_bf16 v[88:91], a[224:227], a[176:179], v[88:91]\n\t"
"s_waitcnt lgkmcnt(1)\n\t"
"s_barrier\n\t"
"v_mfma_f32_16x16x32_bf16 v[88:91], a[60:63], a[180:183], v[88:91]\n\t"
"ds_read_b128 v[196:199], v252 offset:18432\n\t"
"v_cvt_pk_bf16_f32 v26, v56, v57\n\t"
"v_mfma_f32_16x16x32_bf16 v[88:91], a[64:67], a[184:187], v[88:91]\n\t"
"v_cvt_pk_bf16_f32 v27, v58, v59\n\t"
"ds_read_b128 v[200:203], v252 offset:19456\n\t"
"v_mfma_f32_16x16x32_bf16 v[88:91], a[68:71], a[188:191], v[88:91]\n\t"
"ds_read_b128 v[204:207], v252 offset:20480\n\t"
"ds_read_b128 a[180:183], v244 offset:47616\n\t"
"v_mfma_f32_16x16x32_bf16 v[124:127], a[72:75], a[192:195], v[124:127]\n\t"
"v_mfma_f32_16x16x32_bf16 v[124:127], a[112:115], a[200:203], v[124:127]\n\t"
"v_mfma_f32_16x16x32_bf16 v[124:127], a[76:79], v[208:211], v[124:127]\n\t"
"ds_write_b128 v249, v[24:27] offset:30720\n\t"
"v_mfma_f32_16x16x32_bf16 v[124:127], a[80:83], a[196:199], v[124:127]\n\t"
"s_nop 0\n\t"
"v_cvt_pk_bf16_f32 v88, v88, v89\n\t"
"v_mfma_f32_16x16x32_bf16 v[124:127], a[84:87], a[204:207], v[124:127]\n\t"
"v_cvt_pk_bf16_f32 v89, v90, v91\n\t"
"v_mfma_f32_16x16x32_bf16 v[124:127], a[208:211], v[212:215], v[124:127]\n\t"
"ds_write_b64 v250, v[88:89] offset:30720\n\t"
"v_mfma_f32_16x16x32_bf16 v[156:159], a[212:215], a[192:195], v[156:159]\n\t"
"v_mfma_f32_16x16x32_bf16 v[156:159], a[88:91], a[200:203], v[156:159]\n\t"
"v_mfma_f32_16x16x32_bf16 v[156:159], a[120:123], v[208:211], v[156:159]\n\t"
"v_mfma_f32_16x16x32_bf16 v[156:159], a[92:95], a[196:199], v[156:159]\n\t"
"s_nop 2\n\t"
"v_cvt_pk_bf16_f32 v124, v124, v125\n\t"
"v_cvt_pk_bf16_f32 v125, v126, v127\n\t"
"v_mfma_f32_16x16x32_bf16 v[156:159], a[96:99], a[204:207], v[156:159]\n\t"
"v_mfma_f32_16x16x32_bf16 v[156:159], a[100:103], v[212:215], v[156:159]\n\t"
"s_add_u32 s29, s23, 983040\n\t"
"s_waitcnt lgkmcnt(5)\n\t"
"buffer_store_dwordx4 v[196:199], v248, s[16:19], s29 offen offset:0 nt\n\t"
"v_mfma_f32_16x16x32_bf16 v[188:191], a[108:111], a[192:195], v[188:191]\n\t"
"ds_read_b128 a[192:195], v244 offset:13824\n\t"
"v_mfma_f32_16x16x32_bf16 v[188:191], a[216:219], a[200:203], v[188:191]\n\t"
"v_mfma_f32_16x16x32_bf16 v[188:191], a[116:119], v[208:211], v[188:191]\n\t"
"s_add_u32 s29, s23, 983040\n\t"
"s_waitcnt lgkmcnt(5)\n\t"
"buffer_store_dwordx4 v[200:203], v248, s[16:19], s29 offen offset:128 nt\n\t"
"v_mfma_f32_16x16x32_bf16 v[188:191], v[236:239], a[196:199], v[188:191]\n\t"
"v_cvt_pk_bf16_f32 v126, v156, v157\n\t"
"v_cvt_pk_bf16_f32 v127, v158, v159\n\t"
"v_mfma_f32_16x16x32_bf16 v[188:191], a[128:131], a[204:207], v[188:191]\n\t"
"ds_read_b128 a[196:199], v244 offset:56832\n\t"
"v_mfma_f32_16x16x32_bf16 v[188:191], a[228:231], v[212:215], v[188:191]\n\t"
"s_add_u32 s29, s23, 983040\n\t"
"s_waitcnt lgkmcnt(5)\n\t"
"buffer_store_dwordx4 v[204:207], v248, s[16:19], s29 offen offset:256 nt\n\t"
"v_mfma_f32_16x16x32_bf16 v[28:31], a[132:135], a[172:175], v[28:31]\n\t"
"v_mfma_f32_16x16x32_bf16 v[28:31], a[140:143], a[176:179], v[28:31]\n\t"
"v_mfma_f32_16x16x32_bf16 v[28:31], a[148:151], a[168:171], v[28:31]\n\t"
"ds_write_b128 v249, v[124:127] offset:36864\n\t"
"s_waitcnt lgkmcnt(5)\n\t"
"v_mfma_f32_16x16x32_bf16 v[28:31], a[156:159], a[184:187], v[28:31]\n\t"
"v_cvt_pk_bf16_f32 v188, v188, v189\n\t"
"v_cvt_pk_bf16_f32 v189, v190, v191\n\t"
"v_mfma_f32_16x16x32_bf16 v[28:31], a[236:239], a[188:191], v[28:31]\n\t"
"v_mfma_f32_16x16x32_bf16 v[28:31], a[240:243], a[180:183], v[28:31]\n\t"
"ds_write_b64 v250, v[188:189] offset:36864\n\t"
"v_mfma_f32_16x16x32_bf16 v[60:63], a[244:247], a[172:175], v[60:63]\n\t"
"v_mfma_f32_16x16x32_bf16 v[60:63], a[164:167], a[176:179], v[60:63]\n\t"
"v_mfma_f32_16x16x32_bf16 v[60:63], v[96:99], a[168:171], v[60:63]\n\t"
"v_mfma_f32_16x16x32_bf16 v[60:63], a[220:223], a[184:187], v[60:63]\n\t"
"s_nop 2\n\t"
"v_cvt_pk_bf16_f32 v28, v28, v29\n\t"
"v_cvt_pk_bf16_f32 v29, v30, v31\n\t"
"v_mfma_f32_16x16x32_bf16 v[60:63], a[248:251], a[188:191], v[60:63]\n\t"
"v_mfma_f32_16x16x32_bf16 v[60:63], a[56:59], a[180:183], v[60:63]\n\t"
"v_mfma_f32_16x16x32_bf16 v[92:95], a[104:107], a[172:175], v[92:95]\n\t"
"v_mfma_f32_16x16x32_bf16 v[92:95], a[252:255], a[176:179], v[92:95]\n\t"
"v_mfma_f32_16x16x32_bf16 v[92:95], a[224:227], a[168:171], v[92:95]\n\t"
"s_waitcnt lgkmcnt(0)\n\t"
"s_barrier\n\t"
"v_mfma_f32_16x16x32_bf16 v[92:95], a[60:63], a[184:187], v[92:95]\n\t"
"ds_read_b128 v[196:199], v252 offset:30720\n\t"
"s_nop 0\n\t"
"v_cvt_pk_bf16_f32 v30, v60, v61\n\t"
"v_mfma_f32_16x16x32_bf16 v[92:95], a[64:67], a[188:191], v[92:95]\n\t"
"v_cvt_pk_bf16_f32 v31, v62, v63\n\t"
"ds_read_b128 v[200:203], v252 offset:31744\n\t"
"v_mfma_f32_16x16x32_bf16 v[92:95], a[68:71], a[180:183], v[92:95]\n\t"
"ds_read_b128 v[204:207], v252 offset:32768\n\t"
"v_mfma_f32_16x16x32_bf16 v[128:131], a[72:75], a[200:203], v[128:131]\n\t"
"v_mfma_f32_16x16x32_bf16 v[128:131], a[112:115], v[208:211], v[128:131]\n\t"
"v_mfma_f32_16x16x32_bf16 v[128:131], a[76:79], a[192:195], v[128:131]\n\t"
"ds_write_b128 v249, v[28:31] offset:18432\n\t"
"v_mfma_f32_16x16x32_bf16 v[128:131], a[80:83], a[204:207], v[128:131]\n\t"
"s_nop 1\n\t"
"v_cvt_pk_bf16_f32 v92, v92, v93\n\t"
"v_cvt_pk_bf16_f32 v93, v94, v95\n\t"
"v_mfma_f32_16x16x32_bf16 v[128:131], a[84:87], v[212:215], v[128:131]\n\t"
"v_mfma_f32_16x16x32_bf16 v[128:131], a[208:211], a[196:199], v[128:131]\n\t"
"ds_write_b64 v250, v[92:93] offset:18432\n\t"
"v_mfma_f32_16x16x32_bf16 v[160:163], a[212:215], a[200:203], v[160:163]\n\t"
"v_mfma_f32_16x16x32_bf16 v[160:163], a[88:91], v[208:211], v[160:163]\n\t"
"v_mfma_f32_16x16x32_bf16 v[160:163], a[120:123], a[192:195], v[160:163]\n\t"
"s_add_u32 s29, s23, 1179648\n\t"
"s_waitcnt lgkmcnt(4)\n\t"
"buffer_store_dwordx4 v[196:199], v248, s[16:19], s29 offen offset:0 nt\n\t"
"v_mfma_f32_16x16x32_bf16 v[160:163], a[92:95], a[204:207], v[160:163]\n\t"
"v_cvt_pk_bf16_f32 v128, v128, v129\n\t"
"v_cvt_pk_bf16_f32 v129, v130, v131\n\t"
"v_mfma_f32_16x16x32_bf16 v[160:163], a[96:99], v[212:215], v[160:163]\n\t"
"v_mfma_f32_16x16x32_bf16 v[160:163], a[100:103], a[196:199], v[160:163]\n\t"
"s_add_u32 s29, s23, 1179648\n\t"
"s_waitcnt lgkmcnt(3)\n\t"
"buffer_store_dwordx4 v[200:203], v248, s[16:19], s29 offen offset:128 nt\n\t"
"v_mfma_f32_16x16x32_bf16 v[192:195], a[108:111], a[200:203], v[192:195]\n\t"
"v_mfma_f32_16x16x32_bf16 v[192:195], a[216:219], v[208:211], v[192:195]\n\t"
"v_mfma_f32_16x16x32_bf16 v[192:195], a[116:119], a[192:195], v[192:195]\n\t"
"s_add_u32 s29, s23, 1179648\n\t"
"s_waitcnt lgkmcnt(2)\n\t"
"buffer_store_dwordx4 v[204:207], v248, s[16:19], s29 offen offset:256 nt\n\t"
"v_mfma_f32_16x16x32_bf16 v[192:195], v[236:239], a[204:207], v[192:195]\n\t"
"v_cvt_pk_bf16_f32 v130, v160, v161\n\t"
"v_cvt_pk_bf16_f32 v131, v162, v163\n\t"
"v_mfma_f32_16x16x32_bf16 v[192:195], a[128:131], v[212:215], v[192:195]\n\t"
"v_mfma_f32_16x16x32_bf16 v[192:195], a[228:231], a[196:199], v[192:195]\n\t"
"ds_write_b128 v249, v[128:131] offset:24576\n\t"
"s_nop 6\n\t"
"v_cvt_pk_bf16_f32 v192, v192, v193\n\t"
"v_cvt_pk_bf16_f32 v193, v194, v195\n\t"
"ds_write_b64 v250, v[192:193] offset:24576\n\t"
"s_waitcnt lgkmcnt(0)\n\t"
"s_barrier\n\t"
"ds_read_b128 v[196:199], v252 offset:18432\n\t"
"ds_read_b128 v[200:203], v252 offset:19456\n\t"
"ds_read_b128 v[208:211], v252 offset:20480\n\t"
"s_add_u32 s29, s23, 1376256\n\t"
"s_waitcnt lgkmcnt(2)\n\t"
"buffer_store_dwordx4 v[196:199], v248, s[16:19], s29 offen offset:0 nt\n\t"
"s_add_u32 s29, s23, 1376256\n\t"
"s_waitcnt lgkmcnt(1)\n\t"
"buffer_store_dwordx4 v[200:203], v248, s[16:19], s29 offen offset:128 nt\n\t"
"s_add_u32 s29, s23, 1376256\n\t"
"s_waitcnt lgkmcnt(0)\n\t"
"buffer_store_dwordx4 v[208:211], v248, s[16:19], s29 offen offset:256 nt\n\t"
"s_waitcnt vmcnt(0)\n\t"
"Lwino_end:\n\t"
  : "+{v205}"(dummy)
  : "s"((unsigned)pu), "s"((unsigned)(pu >> 32)), "s"((unsigned)px), "s"((unsigned)(px >> 32)), "s"((unsigned)py), "s"((unsigned)(py >> 32)),
    "s"(xbase), "s"(ybase), "s"(w), "s"((unsigned)(size_t)(__attribute__((address_space(3))) char*)smem), "s"(blockIdx.x), "s"((unsigned long long)lane_tab),
    "{v252}"(w * 3072 + lane * 16), "s"(medge), "s"((unsigned)pw), "s"((unsigned)(pw >> 32)), "s"(fso), "s"(fuo), "s"(fk0), "s"(fk5), "s"(fs8),
    "{v206}"(fwo), "{v207}"(flw), "{v208}"(fst), "{v209}"((lane >> 5) * 8),
    "{v28}"((8 * w + ((lane >> 2) & 7)) * PXB + (lane >> 5) * 64 + 16 * ((lane & 3) ^ ((lane >> 3) & 2))),
    "{v29}"((lane >> 2) * PXB + (lane & 3) * 16 + w * 64), "{v30}"((lane >> 1) * PXB + (lane & 1) * 16 + 256 + w * 32), "{v31}"(18432 + w * 3072 + lane * 8)
  : "v0", "v1", "v2", "v3", "v4", "v5", "v6", "v7", "v8", "v9", "v10", "v11", "v12", "v13", "v14", "v15", "v16", "v17", "v18", "v19", "v20", "v21", "v22", "v23", "v24", "v25", "v26", "v27", "v32", "v33", "v34", "v35", "v36", "v37", "v38", "v39", "v40", "v41", "v42", "v43", "v44", "v45", "v46", "v47", "v48", "v49", "v50", "v51", "v52", "v53", "v54", "v55", "v56", "v57", "v58", "v59", "v60", "v61", "v62", "v63", "v64", "v65", "v66", "v67", "v68", "v69", "v70", "v71", "v72", "v73", "v74", "v75", "v76", "v77", "v78", "v79", "v80", "v81", "v82", "v83", "v84", "v85", "v86", "v87", "v88", "v89", "v90", "v91", "v92", "v93", "v94", "v95", "v96", "v97", "v98", "v99", "v100", "v101", "v102", "v103", "v104", "v105", "v106", "v107", "v108", "v109", "v110", "v111", "v112", "v113", "v114", "v115", "v116", "v117", "v118", "v119", "v120", "v121", "v122", "v123", "v124", "v125", "v126", "v127", "v128", "v129", "v130", "v131", "v132", "v133", "v134", "v135", "v136", "v137", "v138", "v139", "v140", "v141", "v142", "v143", "v144", "v145", "v146", "v147", "v148", "v149", "v150", "v151", "v152", "v153", "v154", "v155", "v156", "v157", "v158", "v159", "v160", "v161", "v162", "v163", "v164", "v165", "v166", "v167", "v168", "v169", "v170", "v171", "v172", "v173", "v174", "v175", "v176", "v177", "v178", "v179", "v180", "v181", "v182", "v183", "v184", "v185", "v186", "v187", "v188", "v189", "v190", "v191", "v192", "v193", "v194", "v195", "v196", "v197", "v198", "v199", "v200", "v201", "v202", "v203", "v204", "v210", "v211", "v212", "v213", "v214", "v215", "v216", "v217", "v218", "v219", "v220", "v221", "v222", "v223", "v224", "v225", "v226", "v227", "v228", "v229", "v230", "v231", "v232", "v233", "v234", "v235", "v236", "v237", "v238", "v239", "v240", "v241", "v242", "v243", "v244", "v245", "v246", "v247", "v248", "v249", "v250", "v251", "v253", "v254", "v255", "a0", "a1", "a2", "a3", "a4", "a5", "a6", "a7", "a8", "a9", "a10", "a11", "a12", "a13", "a14", "a15", "a16", "a17", "a18", "a19", "a20", "a21", "a22", "a23", "a24", "a25", "a26", "a27", "a28", "a29", "a30", "a31", "a32", "a33", "a34", "a35", "a36", "a37", "a38", "a39", "a40", "a41", "a42", "a43", "a44", "a45", "a46", "a47", "a48", "a49", "a50", "a51", "a52", "a53", "a54", "a55", "a56", "a57", "a58", "a59", "a60", "a61", "a62", "a63", "a64", "a65", "a66", "a67", "a68", "a69", "a70", "a71", "a72", "a73", "a74", "a75", "a76", "a77", "a78", "a79", "a80", "a81", "a82", "a83", "a84", "a85", "a86", "a87", "a88", "a89", "a90", "a91", "a92", "a93", "a94", "a95", "a96", "a97", "a98", "a99", "a100", "a101", "a102", "a103", "a104", "a105", "a106", "a107", "a108", "a109", "a110", "a111", "a112", "a113", "a114", "a115", "a116", "a117", "a118", "a119", "a120", "a121", "a122", "a123", "a124", "a125", "a126", "a127", "a128", "a129", "a130", "a131", "a132", "a133", "a134", "a135", "a136", "a137", "a138", "a139", "a140", "a141", "a142", "a143", "a144", "a145", "a146", "a147", "a148", "a149", "a150", "a151", "a152", "a153", "a154", "a155", "a156", "a157", "a158", "a159", "a160", "a161", "a162", "a163", "a164", "a165", "a166", "a167", "a168", "a169", "a170", "a171", "a172", "a173", "a174", "a175", "a176", "a177", "a178", "a179", "a180", "a181", "a182", "a183", "a184", "a185", "a186", "a187", "a188", "a189", "a190", "a191", "a192", "a193", "a194", "a195", "a196", "a197", "a198", "a199", "a200", "a201", "a202", "a203", "a204", "a205", "a206", "a207", "a208", "a209", "a210", "a211", "a212", "a213", "a214", "a215", "a216", "a217", "a218", "a219", "a220", "a221", "a222", "a223", "a224", "a225", "a226", "a227", "a228", "a229", "a230", "a231", "a232", "a233", "a234", "a235", "a236", "a237", "a238", "a239", "a240", "a241", "a242", "a243", "a244", "a245", "a246", "a247", "a248", "a249", "a250", "a251", "a252", "a253", "a254", "a255", "s8", "s9", "s10", "s11", "s12", "s13", "s14", "s15", "s16", "s17", "s18", "s19", "s20", "s21", "s22", "s23", "s24", "s25", "s26", "s27", "s28", "s29", "s30", "s31", "s32", "s33", "s34", "s35", "s36", "s37", "s38", "s39", "s40", "s41", "s42", "s43", "s44", "s45", "s46", "s47", "s48", "s49", "s50", "s51", "s52", "s53", "s54", "s55", "s56", "s57", "s58", "s59", "s60", "s61", "s62", "s63", "s64", "s65", "s66", "s67", "s68", "s69", "s70", "s71", "s72", "s73", "s74", "s75", "s76", "s77", "scc", "vcc", "memory");
}

extern "C" void kernel_launch(void* const* d_in, const int* in_sizes, int n_in, void* d_out, int out_size, void* d_ws,
                              size_t ws_size, hipStream_t stream) {
  wino_main_kernel<<<256, 256, 0, stream>>>((const char*)d_in[0], (char*)d_ws, (char*)d_out, (const char*)d_in[1]);
}
```

```cpp
#include <hip/hip_runtime.h>
#include <hip/hip_bf16.h>
#include <stdint.h>

#define IMG_W 512
#define CH 192
#define PXB 384
#define ROWB (IMG_W * PXB)
#define U_BYTES 1769472

__device__ const __attribute__((aligned(1024))) unsigned lane_tab[6400] = {
  0xa800,0xa820,0x0,0x3ea80a81,0xa808,0xa828,0x40,0x3ea80a81,0xa810,0xa830,0x90,0x3ea80a81,0xa818,0xa838,0xd0,0x3ea80a81,
  0xa920,0xa900,0x2400,0xbebd0bd1,0xa928,0xa908,0x2440,0xbebd0bd1,0xa930,0xa910,0x2490,0xbebd0bd1,0xa938,0xa918,0x24d0,0xbebd0bd1,
  0xa840,0xa860,0x200,0x3ea80a81,0xa848,0xa868,0x240,0x3ea80a81,0xa850,0xa870,0x290,0x3ea80a81,0xa858,0xa878,0x2d0,0x3ea80a81,
  0xa960,0xa940,0x200,0x3ea80a81,0xa968,0xa948,0x240,0x3ea80a81,0xa970,0xa950,0x290,0x3ea80a81,0xa978,0xa958,0x2d0,0x3ea80a81,
  0xea80,0xeaa0,0x10,0x3ea80a81,0xea88,0xeaa8,0x50,0x3ea80a81,0xea90,0xeab0,0x80,0x3ea80a81,0xea98,0xeab8,0xc0,0x3ea80a81,
  0xa9a0,0xa980,0x2410,0xbebd0bd1,0xa9a8,0xa988,0x2450,0xbebd0bd1,0xa9b0,0xa990,0x2480,0xbebd0bd1,0xa9b8,0xa998,0x24c0,0xbebd0bd1,
  0xc0c0,0xc0e0,0x210,0x3ea80a81,0xc0c8,0xc0e8,0x250,0x3ea80a81,0xc0d0,0xc0f0,0x280,0x3ea80a81,0xc0d8,0xc0f8,0x2c0,0x3ea80a81,
  0xc1e0,0xc1c0,0x210,0x3ea80a81,0xc1e8,0xc1c8,0x250,0x3ea80a81,0xc1f0,0xc1d0,0x280,0x3ea80a81,0xc1f8,0xc1d8,0x2c0,0x3ea80a81,
  0xae00,0xae20,0x20,0x3ea80a81,0xae08,0xae28,0x60,0x3ea80a81,0xae10,0xae30,0xb0,0x3ea80a81,0xae18,0xae38,0xf0,0x3ea80a81,
  0xeb20,0xeb00,0x2420,0xbebd0bd1,0xeb28,0xeb08,0x2460,0xbebd0bd1,0xeb30,0xeb10,0x24b0,0xbebd0bd1,0xeb38,0xeb18,0x24f0,0xbebd0bd1,
  0xc040,0xc060,0x220,0x3ea80a81,0xc048,0xc068,0x260,0x3ea80a81,0xc050,0xc070,0x2b0,0x3ea80a81,0xc058,0xc078,0x2f0,0x3ea80a81,
  0xc160,0xc140,0x220,0x3ea80a81,0xc168,0xc148,0x260,0x3ea80a81,0xc170,0xc150,0x2b0,0x3ea80a81,0xc178,0xc158,0x2f0,0x3ea80a81,
  0xa880,0xa8a0,0x30,0x3ea80a81,0xa888,0xa8a8,0x70,0x3ea80a81,0xa890,0xa8b0,0xa0,0x3ea80a81,0xa898,0xa8b8,0xe0,0x3ea80a81,
  0xa8c0,0xa8e0,0x2430,0xbebd0bd1,0xa8c8,0xa8e8,0x2470,0xbebd0bd1,0xa8d0,0xa8f0,0x24a0,0xbebd0bd1,0xa8d8,0xa8f8,0x24e0,0xbebd0bd1,
  0xa9e0,0xa9c0,0x230,0x3ea80a81,0xa9e8,0xa9c8,0x270,0x3ea80a81,0xa9f0,0xa9d0,0x2a0,0x3ea80a81,0xa9f8,0xa9d8,0x2e0,0x3ea80a81,
  0xae00,0xae20,0x230,0x3ea80a81,0xae08,0xae28,0x270,0x3ea80a81,0xae10,0xae30,0x2a0,0x3ea80a81,0xae18,0xae38,0x2e0,0x3ea80a81,
  0x0,0xc042,0x0,0x0,0x0,0xc0420000,0x0,0x0,0x0,0x0,0x0,0xc042,0x0,0x0,0x0,0xc0420000,
  0x3f903f90,0x0,0x0,0x0,0x3f900000,0x0,0x3f90,0x0,0x0,0x0,0x3f903f90,0x0,0x0,0x0,0x3f900000,0x0,
  0x0,0x0,0x0,0x0,0x0,0x0,0x0,0x0,0x0,0x0,0x0,0x0,0x0,0x0,0x0,0x0,
  0x0,0x0,0x0,0x0,0x0,0x0,0x0,0x0,0x0,0x0,0x0,0x0,0x0,0x0,0x0,0x0,
  0x0,0x0,0x3f90,0x0,0x0,0x0,0x0,0x0,0x0,0x0,0x0,0x0,0x0,0x0,0x0,0x0,
  0x0,0x0,0x0,0x0,0xc0420000,0x0,0x0,0x0,0x0,0x0,0x0,0x0,0x0,0x0,0xc0420000,0x0,
  0x0,0x3f90,0x0,0x0,0x0,0x3f903f90,0x0,0x0,0x0,0x3f900000,0x0,0x3f90,0x0,0x0,0x0,0x3f903f90,
  0x0,0x3f90,0x0,0x0,0x0,0x3f903f90,0x0,0x0,0x0,0x3f900000,0x0,0x3f90,0x0,0x0,0x0,0x3f903f90,
  0x0,0x0,0x0,0x0,0x0,0x0,0x0,0x0,0x0,0x0,0x0,0x0,0x0,0x0,0x0,0x0,
  0x0,0x0,0x0,0x0,0x0,0x0,0x0,0x0,0x0,0x0,0x0,0x0,0x0,0x0,0x3f90,0x0,
  0x0,0xc042,0x3f900000,0x0,0x0,0xc0420000,0x0,0x0,0x0,0x0,0x0,0xc042,0x0,0x0,0x0,0xc0420000,
  0x0,0xc042,0x3f900000,0x0,0x0,0xc0420000,0x0,0x0,0x0,0x0,0x0,0xc042,0x0,0x0,0x0,0xc0420000,
  0x3f900000,0x0,0x0,0x0,0x3f900000,0x3f90,0x0,0x0,0x0,0x3f90,0x3f900000,0x0,0x0,0x0,0x3f900000,0x3f90,
  0xc042,0x0,0x0,0x0,0x0,0x0,0x0,0x0,0x0,0x0,0xc042,0x0,0x0,0x0,0x0,0x0,
  0x0,0x0,0x0,0x0,0x0,0x0,0x0,0x0,0x0,0x0,0x0,0x0,0x0,0x0,0x0,0x0,
  0x0,0x0,0x0,0x0,0x0,0x0,0x0,0x0,0x0,0x0,0x0,0x0,0x0,0x0,0x0,0x0,
  0x0,0x0,0xa800,0x0,0x40,0x40,0xa808,0x10,0x90,0x90,0xa810,0x20,0xd0,0xd0,0xa818,0x30,
  0x120,0x120,0xa840,0x180,0x160,0x160,0xa848,0x190,0x1b0,0x1b0,0xa850,0x1a0,0x1f0,0x1f0,0xa858,0x1b0,
  0x1000,0x2408,0xa880,0x300,0x1040,0x2448,0xa888,0x310,0x1090,0x2498,0xa890,0x320,0x10d0,0x24d8,0xa898,0x330,
  0x1120,0x2528,0xa8c0,0x480,0x1160,0x2568,0xa8c8,0x490,0x11b0,0x25b8,0xa8d0,0x4a0,0x11f0,0x25f8,0xa8d8,0x4b0,
  0x10,0x10,0xa920,0x620,0x50,0x50,0xa928,0x630,0x80,0x80,0xa930,0x600,0xc0,0xc0,0xa938,0x610,
  0x130,0x130,0xa960,0x7a0,0x170,0x170,0xa968,0x7b0,0x1a0,0x1a0,0xa970,0x780,0x1e0,0x1e0,0xa978,0x790,
  0x1010,0x2418,0xa9a0,0x920,0x1050,0x2458,0xa9a8,0x930,0x1080,0x2488,0xa9b0,0x900,0x10c0,0x24c8,0xa9b8,0x910,
  0x1130,0x2538,0xa9e0,0xaa0,0x1170,0x2578,0xa9e8,0xab0,0x11a0,0x25a8,0xa9f0,0xa80,0x11e0,0x25e8,0xa9f8,0xa90,
  0x20,0x20,0xae00,0x40,0x60,0x60,0xae08,0x50,0xb0,0xb0,0xae10,0x60,0xf0,0xf0,0xae18,0x70,
  0x100,0x100,0xae40,0x1c0,0x140,0x140,0xae48,0x1d0,0x190,0x190,0xae50,0x1e0,0x1d0,0x1d0,0xae58,0x1f0,
  0x1020,0x2428,0xae80,0x340,0x1060,0x2468,0xae88,0x350,0x10b0,0x24b8,0xae90,0x360,0x10f0,0x24f8,0xae98,0x370,
  0x1100,0x2508,0xaec0,0x4c0,0x1140,0x2548,0xaec8,0x4d0,0x1190,0x2598,0xaed0,0x4e0,0x11d0,0x25d8,0xaed8,0x4f0,
  0x30,0x30,0xaf20,0x660,0x70,0x70,0xaf28,0x670,0xa0,0xa0,0xaf30,0x640,0xe0,0xe0,0xaf38,0x650,
  0x110,0x110,0xaf60,0x7e0,0x150,0x150,0xaf68,0x7f0,0x180,0x180,0xaf70,0x7c0,0x1c0,0x1c0,0xaf78,0x7d0,
  0x1030,0x2438,0xafa0,0x960,0x1070,0x2478,0xafa8,0x970,0x10a0,0x24a8,0xafb0,0x940,0x10e0,0x24e8,0xafb8,0x950,
  0x1110,0x2518,0xafe0,0xae0,0x1150,0x2558,0xafe8,0xaf0,0x1180,0x2588,0xaff0,0xac0,0x11c0,0x25c8,0xaff8,0xad0,
  0xb400,0xb420,0x120,0x3ea80a81,0xb408,0xb428,0x160,0x3ea80a81,0xb410,0xb430,0x1b0,0x3ea80a81,0xb418,0xb438,0x1f0,0x3ea80a81,
  0xb440,0xb460,0x2520,0xbebd0bd1,0xb448,0xb468,0x2560,0xbebd0bd1,0xb450,0xb470,0x25b0,0xbebd0bd1,0xb458,0xb478,0x25f0,0xbebd0bd1,
  0xcc40,0xcc60,0x320,0x3ea80a81,0xcc48,0xcc68,0x360,0x3ea80a81,0xcc50,0xcc70,0x3b0,0x3ea80a81,0xcc58,0xcc78,0x3f0,0x3ea80a81,
  0xcd60,0xcd40,0x320,0x3ea80a81,0xcd68,0xcd48,0x360,0x3ea80a81,0xcd70,0xcd50,0x3b0,0x3ea80a81,0xcd78,0xcd58,0x3f0,0x3ea80a81,
  0xb5a0,0xb580,0x130,0x3ea80a81,0xb5a8,0xb588,0x170,0x3ea80a81,0xb5b0,0xb590,0x1a0,0x3ea80a81,0xb5b8,0xb598,0x1e0,0x3ea80a81,
  0xccc0,0xcce0,0x2530,0xbebd0bd1,0xccc8,0xcce8,0x2570,0xbebd0bd1,0xccd0,0xccf0,0x25a0,0xbebd0bd1,0xccd8,0xccf8,0x25e0,0xbebd0bd1,
  0xa9e0,0xa9c0,0x330,0x3ea80a81,0xa9e8,0xa9c8,0x370,0x3ea80a81,0xa9f0,0xa9d0,0x3a0,0x3ea80a81,0xa9f8,0xa9d8,0x3e0,0x3ea80a81,
  0xc1e0,0xc1c0,0x330,0x3ea80a81,0xc1e8,0xc1c8,0x370,0x3ea80a81,0xc1f0,0xc1d0,0x3a0,0x3ea80a81,0xc1f8,0xc1d8,0x3e0,0x3ea80a81,
  0xb520,0xb500,0x100,0x3ea80a81,0xb528,0xb508,0x140,0x3ea80a81,0xb530,0xb510,0x190,0x3ea80a81,0xb538,0xb518,0x1d0,0x3ea80a81,
  0xea40,0xea60,0x2500,0xbebd0bd1,0xea48,0xea68,0x2540,0xbebd0bd1,0xea50,0xea70,0x2590,0xbebd0bd1,0xea58,0xea78,0x25d0,0xbebd0bd1,
  0xb560,0xb540,0x300,0x3ea80a81,0xb568,0xb548,0x340,0x3ea80a81,0xb570,0xb550,0x390,0x3ea80a81,0xb578,0xb558,0x3d0,0x3ea80a81,
  0xb480,0xb4a0,0x300,0x3ea80a81,0xb488,0xb4a8,0x340,0x3ea80a81,0xb490,0xb4b0,0x390,0x3ea80a81,0xb498,0xb4b8,0x3d0,0x3ea80a81,
  0xb4c0,0xb4e0,0x110,0x3ea80a81,0xb4c8,0xb4e8,0x150,0x3ea80a81,0xb4d0,0xb4f0,0x180,0x3ea80a81,0xb4d8,0xb4f8,0x1c0,0x3ea80a81,
  0xb5e0,0xb5c0,0x2510,0xbebd0bd1,0xb5e8,0xb5c8,0x2550,0xbebd0bd1,0xb5f0,0xb5d0,0x2580,0xbebd0bd1,0xb5f8,0xb5d8,0x25c0,0xbebd0bd1,
  0xcde0,0xcdc0,0x310,0x3ea80a81,0xcde8,0xcdc8,0x350,0x3ea80a81,0xcdf0,0xcdd0,0x380,0x3ea80a81,0xcdf8,0xcdd8,0x3c0,0x3ea80a81,
  0xb520,0xb500,0x310,0x3ea80a81,0xb528,0xb508,0x350,0x3ea80a81,0xb530,0xb510,0x380,0x3ea80a81,0xb538,0xb518,0x3c0,0x3ea80a81,
  0xc0420000,0x0,0x0,0x0,0x0,0x0,0x0,0x0,0x0,0x0,0xc0420000,0x0,0x0,0x0,0x0,0x0,
  0x3f90,0x0,0x0,0x0,0x0,0x0,0x3f90,0x0,0x0,0x0,0x3f90,0x0,0x0,0x0,0x0,0x0,
  0x0,0xc042,0x0,0x0,0x0,0xc0420000,0x0,0x0,0x0,0x0,0x0,0xc042,0x0,0x0,0x0,0xc0420000,
  0x0,0xc042,0x0,0x0,0x0,0xc0420000,0x0,0x0,0x0,0x0,0x0,0xc042,0x0,0x0,0x0,0xc0420000,
  0x0,0x0,0x0,0x3f90,0x0,0x0,0x0,0x0,0x0,0x0,0x0,0x0,0x0,0x0,0x0,0x0,
  0x0,0x0,0x0,0x0,0xc042,0x0,0x0,0x0,0x0,0x0,0x0,0x0,0x0,0x0,0xc042,0x0,
  0x3f900000,0x0,0x0,0x3f900000,0x3f900000,0x0,0x0,0x0,0x0,0x0,0x3f900000,0x0,0x0,0x0,0x3f900000,0x0,
  0x3f900000,0x0,0x0,0x3f900000,0x3f900000,0x0,0x0,0x0,0x0,0x0,0x3f900000,0x0,0x0,0x0,0x3f900000,0x0,
  0x0,0x0,0x0,0x0,0x0,0xc042,0x0,0x0,0x0,0x0,0x0,0x0,0x0,0x0,0x0,0xc042,
  0x3f90,0xc0420000,0x0,0x0,0x3f90,0x0,0x0,0x0,0x0,0x0,0x3f90,0xc0420000,0x0,0x0,0x3f903f90,0x0,
  0x0,0x0,0x0,0x0,0x0,0x0,0x0,0x0,0x0,0x0,0x0,0x0,0x0,0x0,0x0,0x0,
  0x0,0x0,0x0,0x0,0x0,0x0,0x0,0x0,0x0,0x0,0x0,0x0,0x0,0x0,0x0,0x0,
  0x3f90,0x0,0x0,0x0,0x3f903f90,0x0,0x0,0x0,0x3f900000,0x0,0x3f90,0x0,0x0,0x0,0x3f903f90,0x0,
  0x0,0x0,0x0,0x0,0x0,0x0,0x0,0x0,0x0,0x0,0x0,0x0,0x0,0x0,0x0,0x0,
  0x0,0x0,0x0,0x0,0x0,0x3f90,0x0,0x0,0x0,0x3f90,0x0,0x0,0x0,0x0,0x0,0x3f90,
  0x0,0x0,0x0,0x0,0x0,0x3f90,0x0,0x0,0x0,0x3f90,0x0,0x0,0x0,0x0,0x0,0x3f90,
  0x0,0x8,0xa820,0xc00,0x40,0x48,0xa828,0xc10,0x90,0x98,0xa830,0xc20,0xd0,0xd8,0xa838,0xc30,
  0x120,0x128,0xa860,0xd80,0x160,0x168,0xa868,0xd90,0x1b0,0x1b8,0xa870,0xda0,0x1f0,0x1f8,0xa878,0xdb0,
  0x1000,0x2400,0xa8a0,0xf00,0x1040,0x2440,0xa8a8,0xf10,0x1090,0x2490,0xa8b0,0xf20,0x10d0,0x24d0,0xa8b8,0xf30,
  0x1120,0x2520,0xa8e0,0x1080,0x1160,0x2560,0xa8e8,0x1090,0x11b0,0x25b0,0xa8f0,0x10a0,0x11f0,0x25f0,0xa8f8,0x10b0,
  0x10,0x18,0xa900,0x1220,0x50,0x58,0xa908,0x1230,0x80,0x88,0xa910,0x1200,0xc0,0xc8,0xa918,0x1210,
  0x130,0x138,0xa940,0x13a0,0x170,0x178,0xa948,0x13b0,0x1a0,0x1a8,0xa950,0x1380,0x1e0,0x1e8,0xa958,0x1390,
  0x1010,0x2410,0xa980,0x1520,0x1050,0x2450,0xa988,0x1530,0x1080,0x2480,0xa990,0x1500,0x10c0,0x24c0,0xa998,0x1510,
  0x1130,0x2530,0xa9c0,0x16a0,0x1170,0x2570,0xa9c8,0x16b0,0x11a0,0x25a0,0xa9d0,0x1680,0x11e0,0x25e0,0xa9d8,0x1690,
  0x20,0x28,0xae20,0xc40,0x60,0x68,0xae28,0xc50,0xb0,0xb8,0xae30,0xc60,0xf0,0xf8,0xae38,0xc70,
  0x100,0x108,0xae60,0xdc0,0x140,0x148,0xae68,0xdd0,0x190,0x198,0xae70,0xde0,0x1d0,0x1d8,0xae78,0xdf0,
  0x1020,0x2420,0xaea0,0xf40,0x1060,0x2460,0xaea8,0xf50,0x10b0,0x24b0,0xaeb0,0xf60,0x10f0,0x24f0,0xaeb8,0xf70,
  0x1100,0x2500,0xaee0,0x10c0,0x1140,0x2540,0xaee8,0x10d0,0x1190,0x2590,0xaef0,0x10e0,0x11d0,0x25d0,0xaef8,0x10f0,
  0x30,0x38,0xaf00,0x1260,0x70,0x78,0xaf08,0x1270,0xa0,0xa8,0xaf10,0x1240,0xe0,0xe8,0xaf18,0x1250,
  0x110,0x118,0xaf40,0x13e0,0x150,0x158,0xaf48,0x13f0,0x180,0x188,0xaf50,0x13c0,0x1c0,0x1c8,0xaf58,0x13d0,
  0x1030,0x2430,0xaf80,0x1560,0x1070,0x2470,0xaf88,0x1570,0x10a0,0x24a0,0xaf90,0x1540,0x10e0,0x24e0,0xaf98,0x1550,
  0x1110,0x2510,0xafc0,0x16e0,0x1150,0x2550,0xafc8,0x16f0,0x1180,0x2580,0xafd0,0x16c0,0x11c0,0x25c0,0xafd8,0x16d0,
  0xc000,0xc020,0x2600,0xbebd0bd1,0xc008,0xc028,0x2640,0xbebd0bd1,0xc010,0xc030,0x2690,0xbebd0bd1,0xc018,0xc038,0x26d0,0xbebd0bd1,
  0xd800,0xd820,0x400,0x3ea80a81,0xd808,0xd828,0x440,0x3ea80a81,0xd810,0xd830,0x490,0x3ea80a81,0xd818,0xd838,0x4d0,0x3ea80a81,
  0xc120,0xc100,0x2800,0xbebd0bd1,0xc128,0xc108,0x2840,0xbebd0bd1,0xc130,0xc110,0x2890,0xbebd0bd1,0xc138,0xc118,0x28d0,0xbebd0bd1,
  0xd840,0xd860,0x2800,0xbebd0bd1,0xd848,0xd868,0x2840,0xbebd0bd1,0xd850,0xd870,0x2890,0xbebd0bd1,0xd858,0xd878,0x28d0,0xbebd0bd1,
  0xc080,0xc0a0,0x2610,0xbebd0bd1,0xc088,0xc0a8,0x2650,0xbebd0bd1,0xc090,0xc0b0,0x2680,0xbebd0bd1,0xc098,0xc0b8,0x26c0,0xbebd0bd1,
  0xc1a0,0xc180,0x410,0x3ea80a81,0xc1a8,0xc188,0x450,0x3ea80a81,0xc1b0,0xc190,0x480,0x3ea80a81,0xc1b8,0xc198,0x4c0,0x3ea80a81,
  0xd9a0,0xd980,0x2810,0xbebd0bd1,0xd9a8,0xd988,0x2850,0xbebd0bd1,0xd9b0,0xd990,0x2880,0xbebd0bd1,0xd9b8,0xd998,0x28c0,0xbebd0bd1,
  0xd9e0,0xd9c0,0x2810,0xbebd0bd1,0xd9e8,0xd9c8,0x2850,0xbebd0bd1,0xd9f0,0xd9d0,0x2880,0xbebd0bd1,0xd9f8,0xd9d8,0x28c0,0xbebd0bd1,
  0xc600,0xc620,0x2620,0xbebd0bd1,0xc608,0xc628,0x2660,0xbebd0bd1,0xc610,0xc630,0x26b0,0xbebd0bd1,0xc618,0xc638,0x26f0,0xbebd0bd1,
  0xde00,0xde20,0x420,0x3ea80a81,0xde08,0xde28,0x460,0x3ea80a81,0xde10,0xde30,0x4b0,0x3ea80a81,0xde18,0xde38,0x4f0,0x3ea80a81,
  0xd920,0xd900,0x2820,0xbebd0bd1,0xd928,0xd908,0x2860,0xbebd0bd1,0xd930,0xd910,0x28b0,0xbebd0bd1,0xd938,0xd918,0x28f0,0xbebd0bd1,
  0xd960,0xd940,0x2820,0xbebd0bd1,0xd968,0xd948,0x2860,0xbebd0bd1,0xd970,0xd950,0x28b0,0xbebd0bd1,0xd978,0xd958,0x28f0,0xbebd0bd1,
  0xd880,0xd8a0,0x2630,0xbebd0bd1,0xd888,0xd8a8,0x2670,0xbebd0bd1,0xd890,0xd8b0,0x26a0,0xbebd0bd1,0xd898,0xd8b8,0x26e0,0xbebd0bd1,
  0xeba0,0xeb80,0x430,0x3ea80a81,0xeba8,0xeb88,0x470,0x3ea80a81,0xebb0,0xeb90,0x4a0,0x3ea80a81,0xebb8,0xeb98,0x4e0,0x3ea80a81,
  0xd8c0,0xd8e0,0x2830,0xbebd0bd1,0xd8c8,0xd8e8,0x2870,0xbebd0bd1,0xd8d0,0xd8f0,0x28a0,0xbebd0bd1,0xd8d8,0xd8f8,0x28e0,0xbebd0bd1,
  0xc600,0xc620,0x2830,0xbebd0bd1,0xc608,0xc628,0x2870,0xbebd0bd1,0xc610,0xc630,0x28a0,0xbebd0bd1,0xc618,0xc638,0x28e0,0xbebd0bd1,
  0x3f90,0x3f90,0x0,0x0,0x0,0x3f90,0x3f90,0x0,0x0,0x0,0x3f90,0x3f90,0x0,0x0,0x0,0x3f90,
  0x0,0xc0420000,0x0,0x0,0x0,0x0,0x0,0x0,0x0,0x0,0x0,0xc0420000,0x0,0x0,0x0,0x0,
  0x3f900000,0x0,0x0,0x0,0x0,0x0,0x3f900000,0x0,0x0,0x0,0x3f900000,0x0,0x0,0x0,0x0,0x0,
  0x3f900000,0x0,0x0,0x0,0x0,0x0,0x3f900000,0x0,0x0,0x0,0x3f900000,0x0,0x0,0x0,0x0,0x0,
  0xc042,0x0,0x0,0x0,0xc0420000,0x0,0x0,0x0,0x0,0x0,0xc042,0x0,0x0,0x0,0xc0420000,0x0,
  0x0,0x0,0x0,0x0,0x0,0x3f900000,0x0,0x0,0x0,0x3f900000,0x0,0x0,0x0,0x0,0x0,0x3f900000,
  0x0,0x0,0x0,0x0,0x0,0xc042,0x0,0x0,0x0,0x0,0x0,0x0,0x0,0x0,0x0,0xc042,
  0x0,0x0,0x0,0x0,0x0,0xc042,0x0,0x0,0x0,0x0,0x0,0x0,0x0,0x0,0x0,0xc042,
  0x0,0x0,0x0,0x0,0x0,0x0,0x0,0x0,0x0,0x0,0x0,0x0,0x0,0x0,0x3f90,0x0,
  0x0,0x0,0x0,0x0,0x0,0xc0420000,0x0,0x0,0x0,0x0,0x0,0x0,0x0,0x0,0x0,0xc0420000,
  0x0,0x3f90,0x0,0x0,0x0,0x3f90,0x0,0x0,0x0,0x0,0x0,0x3f90,0x0,0x0,0x3f900000,0x3f90,
  0x0,0x3f90,0x0,0x0,0x0,0x3f90,0x0,0x0,0x0,0x0,0x0,0x3f90,0x0,0x0,0x3f900000,0x3f90,
  0x0,0x0,0x0,0x0,0x0,0x0,0x0,0x0,0x0,0x0,0x0,0x0,0x0,0x0,0x0,0x0,
  0x0,0x3f90,0x3f900000,0x0,0x0,0x3f90,0x0,0x0,0x0,0x0,0x0,0x3f90,0x0,0x0,0x0,0x3f90,
  0xc042,0x0,0x0,0x0,0x0,0x0,0x0,0x0,0x0,0x0,0xc042,0x0,0x0,0x0,0x0,0x0,
  0xc042,0x0,0x0,0x0,0x0,0x0,0x0,0x0,0x0,0x0,0xc042,0x0,0x0,0x0,0x0,0x0,
  0x0,0x1200,0x19200,0x1800,0x40,0x1240,0x19208,0x1810,0x90,0x1290,0x19210,0x1820,0xd0,0x12d0,0x19218,0x1830,
  0x120,0x1320,0x19240,0x1980,0x160,0x1360,0x19248,0x1990,0x1b0,0x13b0,0x19250,0x19a0,0x1f0,0x13f0,0x19258,0x19b0,
  0x1000,0x3608,0x19280,0x1b00,0x1040,0x3648,0x19288,0x1b10,0x1090,0x3698,0x19290,0x1b20,0x10d0,0x36d8,0x19298,0x1b30,
  0x1120,0x3728,0x192c0,0x1c80,0x1160,0x3768,0x192c8,0x1c90,0x11b0,0x37b8,0x192d0,0x1ca0,0x11f0,0x37f8,0x192d8,0x1cb0,
  0x10,0x1210,0x19320,0x1e20,0x50,0x1250,0x19328,0x1e30,0x80,0x1280,0x19330,0x1e00,0xc0,0x12c0,0x19338,0x1e10,
  0x130,0x1330,0x19360,0x1fa0,0x170,0x1370,0x19368,0x1fb0,0x1a0,0x13a0,0x19370,0x1f80,0x1e0,0x13e0,0x19378,0x1f90,
  0x1010,0x3618,0x193a0,0x2120,0x1050,0x3658,0x193a8,0x2130,0x1080,0x3688,0x193b0,0x2100,0x10c0,0x36c8,0x193b8,0x2110,
  0x1130,0x3738,0x193e0,0x22a0,0x1170,0x3778,0x193e8,0x22b0,0x11a0,0x37a8,0x193f0,0x2280,0x11e0,0x37e8,0x193f8,0x2290,
  0x20,0x1220,0x19800,0x1840,0x60,0x1260,0x19808,0x1850,0xb0,0x12b0,0x19810,0x1860,0xf0,0x12f0,0x19818,0x1870,
  0x100,0x1300,0x19840,0x19c0,0x140,0x1340,0x19848,0x19d0,0x190,0x1390,0x19850,0x19e0,0x1d0,0x13d0,0x19858,0x19f0,
  0x1020,0x3628,0x19880,0x1b40,0x1060,0x3668,0x19888,0x1b50,0x10b0,0x36b8,0x19890,0x1b60,0x10f0,0x36f8,0x19898,0x1b70,
  0x1100,0x3708,0x198c0,0x1cc0,0x1140,0x3748,0x198c8,0x1cd0,0x1190,0x3798,0x198d0,0x1ce0,0x11d0,0x37d8,0x198d8,0x1cf0,
  0x30,0x1230,0x19920,0x1e60,0x70,0x1270,0x19928,0x1e70,0xa0,0x12a0,0x19930,0x1e40,0xe0,0x12e0,0x19938,0x1e50,
  0x110,0x1310,0x19960,0x1fe0,0x150,0x1350,0x19968,0x1ff0,0x180,0x1380,0x19970,0x1fc0,0x1c0,0x13c0,0x19978,0x1fd0,
  0x1030,0x3638,0x199a0,0x2160,0x1070,0x3678,0x199a8,0x2170,0x10a0,0x36a8,0x199b0,0x2140,0x10e0,0x36e8,0x199b8,0x2150,
  0x1110,0x3718,0x199e0,0x22e0,0x1150,0x3758,0x199e8,0x22f0,0x1180,0x3788,0x199f0,0x22c0,0x11c0,0x37c8,0x199f8,0x22d0,
  0xcc00,0xcc20,0x2720,0xbebd0bd1,0xcc08,0xcc28,0x2760,0xbebd0bd1,0xcc10,0xcc30,0x27b0,0xbebd0bd1,0xcc18,0xcc38,0x27f0,0xbebd0bd1,
  0xcd20,0xcd00,0x520,0x3ea80a81,0xcd28,0xcd08,0x560,0x3ea80a81,0xcd30,0xcd10,0x5b0,0x3ea80a81,0xcd38,0xcd18,0x5f0,0x3ea80a81,
  0xe440,0xe460,0x2920,0xbebd0bd1,0xe448,0xe468,0x2960,0xbebd0bd1,0xe450,0xe470,0x29b0,0xbebd0bd1,0xe458,0xe478,0x29f0,0xbebd0bd1,
  0xeb60,0xeb40,0x2920,0xbebd0bd1,0xeb68,0xeb48,0x2960,0xbebd0bd1,0xeb70,0xeb50,0x29b0,0xbebd0bd1,0xeb78,0xeb58,0x29f0,0xbebd0bd1,
  0xe480,0xe4a0,0x2730,0xbebd0bd1,0xe488,0xe4a8,0x2770,0xbebd0bd1,0xe490,0xe4b0,0x27a0,0xbebd0bd1,0xe498,0xe4b8,0x27e0,0xbebd0bd1,
  0xe5a0,0xe580,0x530,0x3ea80a81,0xe5a8,0xe588,0x570,0x3ea80a81,0xe5b0,0xe590,0x5a0,0x3ea80a81,0xe5b8,0xe598,0x5e0,0x3ea80a81,
  0xe4c0,0xe4e0,0x2930,0xbebd0bd1,0xe4c8,0xe4e8,0x2970,0xbebd0bd1,0xe4d0,0xe4f0,0x29a0,0xbebd0bd1,0xe4d8,0xe4f8,0x29e0,0xbebd0bd1,
  0xd9e0,0xd9c0,0x2930,0xbebd0bd1,0xd9e8,0xd9c8,0x2970,0xbebd0bd1,0xd9f0,0xd9d0,0x29a0,0xbebd0bd1,0xd9f8,0xd9d8,0x29e0,0xbebd0bd1,
  0xe400,0xe420,0x2700,0xbebd0bd1,0xe408,0xe428,0x2740,0xbebd0bd1,0xe410,0xe430,0x2790,0xbebd0bd1,0xe418,0xe438,0x27d0,0xbebd0bd1,
  0xe520,0xe500,0x500,0x3ea80a81,0xe528,0xe508,0x540,0x3ea80a81,0xe530,0xe510,0x590,0x3ea80a81,0xe538,0xe518,0x5d0,0x3ea80a81,
  0xe560,0xe540,0x2900,0xbebd0bd1,0xe568,0xe548,0x2940,0xbebd0bd1,0xe570,0xe550,0x2990,0xbebd0bd1,0xe578,0xe558,0x29d0,0xbebd0bd1,
  0xcc80,0xcca0,0x2900,0xbebd0bd1,0xcc88,0xcca8,0x2940,0xbebd0bd1,0xcc90,0xccb0,0x2990,0xbebd0bd1,0xcc98,0xccb8,0x29d0,0xbebd0bd1,
  0xcda0,0xcd80,0x2710,0xbebd0bd1,0xcda8,0xcd88,0x2750,0xbebd0bd1,0xcdb0,0xcd90,0x2780,0xbebd0bd1,0xcdb8,0xcd98,0x27c0,0xbebd0bd1,
  0xeac0,0xeae0,0x510,0x3ea80a81,0xeac8,0xeae8,0x550,0x3ea80a81,0xead0,0xeaf0,0x580,0x3ea80a81,0xead8,0xeaf8,0x5c0,0x3ea80a81,
  0xe5e0,0xe5c0,0x2910,0xbebd0bd1,0xe5e8,0xe5c8,0x2950,0xbebd0bd1,0xe5f0,0xe5d0,0x2980,0xbebd0bd1,0xe5f8,0xe5d8,0x29c0,0xbebd0bd1,
  0xe400,0xe420,0x2910,0xbebd0bd1,0xe408,0xe428,0x2950,0xbebd0bd1,0xe410,0xe430,0x2980,0xbebd0bd1,0xe418,0xe438,0x29c0,0xbebd0bd1,
  0x3f903f90,0x0,0x0,0x0,0x3f900000,0x0,0x3f90,0x0,0x0,0x0,0x3f903f90,0x0,0x0,0x0,0x3f900000,0x0,
  0x0,0xc042,0x0,0x0,0x0,0x0,0x0,0x0,0x0,0x0,0x0,0xc042,0x0,0x0,0x0,0x0,
  0x0,0x0,0x0,0x0,0x0,0x0,0x0,0x0,0x0,0x0,0x0,0x0,0x0,0x0,0x0,0x3f900000,
  0x0,0x0,0x0,0x0,0x0,0x0,0x0,0x0,0x0,0x0,0x0,0x0,0x0,0x0,0x0,0x3f900000,
  0x0,0x0,0x0,0x0,0x0,0x0,0x0,0x0,0x0,0x0,0x0,0x0,0x0,0x0,0x0,0x0,
  0x0,0x3f90,0x0,0x3f900000,0x0,0x3f90,0x0,0x0,0x0,0x0,0x0,0x3f90,0x0,0x0,0x0,0x3f90,
  0xc042,0x0,0x0,0x0,0xc0420000,0x0,0x0,0x0,0x0,0x0,0xc042,0x0,0x0,0x0,0xc0420000,0x0,
  0xc042,0x0,0x0,0x0,0xc0420000,0x0,0x0,0x0,0x0,0x0,0xc042,0x0,0x0,0x0,0xc0420000,0x0,
  0x0,0xc0420000,0x0,0x0,0x0,0x0,0x0,0x0,0x0,0x0,0x0,0xc0420000,0x0,0x0,0x0,0x0,
  0x0,0x0,0x0,0x0,0x0,0xc042,0x0,0x0,0x0,0x0,0x0,0x0,0x0,0x0,0x0,0xc042,
  0x3f903f90,0x0,0x0,0x0,0x3f900000,0x0,0x3f90,0x0,0x0,0x0,0x3f903f90,0x0,0x0,0x0,0x3f900000,0x0,
  0x3f903f90,0x0,0x0,0x0,0x3f900000,0x0,0x3f90,0x0,0x0,0x0,0x3f903f90,0x0,0x0,0x0,0x3f900000,0x0,
  0x0,0x0,0x0,0x0,0xc042,0x0,0x0,0x0,0x0,0x0,0x0,0x0,0x0,0x0,0x3f90c042,0x0,
  0x0,0x0,0x0,0x0,0x0,0x3f90,0x0,0x0,0x0,0x3f90,0x0,0x0,0x0,0x0,0x0,0x3f90,
  0x0,0x0,0x0,0x0,0x0,0x0,0x0,0x0,0x0,0x0,0x0,0x0,0x0,0x0,0x0,0x0,
  0x0,0x0,0x0,0x0,0x0,0x0,0x0,0x0,0x0,0x0,0x0,0x0,0x0,0x0,0x0,0x0,
  0x0,0x1208,0x19220,0x2400,0x40,0x1248,0x19228,0x2410,0x90,0x1298,0x19230,0x2420,0xd0,0x12d8,0x19238,0x2430,
  0x120,0x1328,0x19260,0x2580,0x160,0x1368,0x19268,0x2590,0x1b0,0x13b8,0x19270,0x25a0,0x1f0,0x13f8,0x19278,0x25b0,
  0x1000,0x3600,0x192a0,0x2700,0x1040,0x3640,0x192a8,0x2710,0x1090,0x3690,0x192b0,0x2720,0x10d0,0x36d0,0x192b8,0x2730,
  0x1120,0x3720,0x192e0,0x2880,0x1160,0x3760,0x192e8,0x2890,0x11b0,0x37b0,0x192f0,0x28a0,0x11f0,0x37f0,0x192f8,0x28b0,
  0x10,0x1218,0x19300,0x2a20,0x50,0x1258,0x19308,0x2a30,0x80,0x1288,0x19310,0x2a00,0xc0,0x12c8,0x19318,0x2a10,
  0x130,0x1338,0x19340,0x2ba0,0x170,0x1378,0x19348,0x2bb0,0x1a0,0x13a8,0x19350,0x2b80,0x1e0,0x13e8,0x19358,0x2b90,
  0x1010,0x3610,0x19380,0x2d20,0x1050,0x3650,0x19388,0x2d30,0x1080,0x3680,0x19390,0x2d00,0x10c0,0x36c0,0x19398,0x2d10,
  0x1130,0x3730,0x193c0,0x2ea0,0x1170,0x3770,0x193c8,0x2eb0,0x11a0,0x37a0,0x193d0,0x2e80,0x11e0,0x37e0,0x193d8,0x2e90,
  0x20,0x1228,0x19820,0x2440,0x60,0x1268,0x19828,0x2450,0xb0,0x12b8,0x19830,0x2460,0xf0,0x12f8,0x19838,0x2470,
  0x100,0x1308,0x19860,0x25c0,0x140,0x1348,0x19868,0x25d0,0x190,0x1398,0x19870,0x25e0,0x1d0,0x13d8,0x19878,0x25f0,
  0x1020,0x3620,0x198a0,0x2740,0x1060,0x3660,0x198a8,0x2750,0x10b0,0x36b0,0x198b0,0x2760,0x10f0,0x36f0,0x198b8,0x2770,
  0x1100,0x3700,0x198e0,0x28c0,0x1140,0x3740,0x198e8,0x28d0,0x1190,0x3790,0x198f0,0x28e0,0x11d0,0x37d0,0x198f8,0x28f0,
  0x30,0x1238,0x19900,0x2a60,0x70,0x1278,0x19908,0x2a70,0xa0,0x12a8,0x19910,0x2a40,0xe0,0x12e8,0x19918,0x2a50,
  0x110,0x1318,0x19940,0x2be0,0x150,0x1358,0x19948,0x2bf0,0x180,0x1388,0x19950,0x2bc0,0x1c0,0x13c8,0x19958,0x2bd0,
  0x1030,0x3630,0x19980,0x2d60,0x1070,0x3670,0x19988,0x2d70,0x10a0,0x36a0,0x19990,0x2d40,0x10e0,0x36e0,0x19998,0x2d50,
  0x1110,0x3710,0x199c0,0x2ee0,0x1150,0x3750,0x199c8,0x2ef0,0x1180,0x3780,0x199d0,0x2ec0,0x11c0,0x37c0,0x199d8,0x2ed0,
  0x0,0x0,0x800,0x10000000,0x10,0x90,0x890,0x10000000,0x20,0x120,0x920,0x10000000,0x30,0x1b0,0x9b0,0x10000000,
  0x40,0x240,0xa40,0x20003180,0x50,0x2d0,0xad0,0x20003190,0x60,0x360,0xb60,0x200031a0,0x70,0x3f0,0xbf0,0x200031b0,
  0x610,0xc00,0x1400,0x40000000,0x600,0xc90,0x1490,0x40000010,0x630,0xd20,0x1520,0x40000020,0x620,0xdb0,0x15b0,0x40000030,
  0x650,0xe40,0x1640,0x20033180,0x640,0xed0,0x16d0,0x20033190,0x670,0xf60,0x1760,0x200331a0,0x660,0xff0,0x17f0,0x200331b0,
  0xc20,0x10,0x808,0x40030020,0xc30,0x80,0x898,0x40030030,0xc00,0x130,0x928,0x40030000,0xc10,0x1a0,0x9b8,0x40030010,
  0xc60,0x250,0xa48,0x200631a0,0xc70,0x2c0,0xad8,0x200631b0,0xc40,0x370,0xb68,0x20063180,0xc50,0x3e0,0xbf8,0x20063190,
  0x1230,0xc10,0x1408,0x40060020,0x1220,0xc80,0x1498,0x40060030,0x1210,0xd30,0x1528,0x40060000,0x1200,0xda0,0x15b8,0x40060010,
  0x1270,0xe50,0x1648,0x10000000,0x1260,0xec0,0x16d8,0x10000000,0x1250,0xf70,0x1768,0x10000000,0x1240,0xfe0,0x17f8,0x10000000,
  0x1840,0x20,0x810,0x10000000,0x1850,0xb0,0x880,0x10000000,0x1860,0x100,0x930,0x10000000,0x1870,0x190,0x9a0,0x10000000,
  0x1800,0x260,0xa50,0x200031c0,0x1810,0x2f0,0xac0,0x200031d0,0x1820,0x340,0xb70,0x200031e0,0x1830,0x3d0,0xbe0,0x200031f0,
  0x1e50,0xc20,0x1410,0x40000040,0x1e40,0xcb0,0x1480,0x40000050,0x1e70,0xd00,0x1530,0x40000060,0x1e60,0xd90,0x15a0,0x40000070,
  0x1e10,0xe60,0x1650,0x200331c0,0x1e00,0xef0,0x16c0,0x200331d0,0x1e30,0xf40,0x1770,0x200331e0,0x1e20,0xfd0,0x17e0,0x200331f0,
  0x2460,0x30,0x818,0x40030060,0x2470,0xa0,0x888,0x40030070,0x2440,0x110,0x938,0x40030040,0x2450,0x180,0x9a8,0x40030050,
  0x2420,0x270,0xa58,0x200631e0,0x2430,0x2e0,0xac8,0x200631f0,0x2400,0x350,0xb78,0x200631c0,0x2410,0x3c0,0xbe8,0x200631d0,
  0x2a70,0xc30,0x1418,0x40060060,0x2a60,0xca0,0x1488,0x40060070,0x2a50,0xd10,0x1538,0x40060040,0x2a40,0xd80,0x15a8,0x40060050,
  0x2a30,0xe70,0x1658,0x10000000,0x2a20,0xee0,0x16c8,0x10000000,0x2a10,0xf50,0x1778,0x10000000,0x2a00,0xfc0,0x17e8,0x10000000,
  0xbfd8bf90,0x3f403f00,0x0,0x0,0x0,0x0,0x0,0x0,0x0,0x0,0x0,0x0,0x0,0x0,0x0,0x0,
  0x0,0x0,0xbfd8bf90,0x3f403f00,0x0,0x0,0x0,0x0,0x0,0x0,0x0,0x0,0x0,0x0,0x0,0x0,
  0xbfd83f90,0x3f40bf00,0x0,0x0,0x0,0x0,0x0,0x0,0x0,0x0,0x0,0x0,0x0,0x0,0x0,0x0,
  0x0,0x0,0xbfd83f90,0x3f40bf00,0x0,0x0,0x0,0x0,0x0,0x0,0x0,0x0,0x0,0x0,0x0,0x0,
  0x0,0x0,0x0,0x0,0xbfd8bf90,0x3f403f00,0x0,0x0,0x0,0x0,0x0,0x0,0x0,0x0,0x0,0x0,
  0x0,0x0,0x0,0x0,0x0,0x0,0xbfd8bf90,0x3f403f00,0x0,0x0,0x0,0x0,0x0,0x0,0x0,0x0,
  0x0,0x0,0x0,0x0,0xbfd83f90,0x3f40bf00,0x0,0x0,0x0,0x0,0x0,0x0,0x0,0x0,0x0,0x0,
  0x0,0x0,0x0,0x0,0x0,0x0,0xbfd83f90,0x3f40bf00,0x0,0x0,0x0,0x0,0x0,0x0,0x0,0x0,
  0x0,0x0,0x0,0x0,0x0,0x0,0x0,0x0,0xbfd8bf90,0x3f403f00,0x0,0x0,0x0,0x0,0x0,0x0,
  0x0,0x0,0x0,0x0,0x0,0x0,0x0,0x0,0x0,0x0,0xbfd8bf90,0x3f403f00,0x0,0x0,0x0,0x0,
  0x0,0x0,0x0,0x0,0x0,0x0,0x0,0x0,0xbfd83f90,0x3f40bf00,0x0,0x0,0x0,0x0,0x0,0x0,
  0x0,0x0,0x0,0x0,0x0,0x0,0x0,0x0,0x0,0x0,0xbfd83f90,0x3f40bf00,0x0,0x0,0x0,0x0,
  0x0,0x0,0x0,0x0,0x0,0x0,0x0,0x0,0x0,0x0,0x0,0x0,0xbfd8bf90,0x3f403f00,0x0,0x0,
  0x0,0x0,0x0,0x0,0x0,0x0,0x0,0x0,0x0,0x0,0x0,0x0,0x0,0x0,0xbfd8bf90,0x3f403f00,
  0x0,0x0,0x0,0x0,0x0,0x0,0x0,0x0,0x0,0x0,0x0,0x0,0xbfd83f90,0x3f40bf00,0x0,0x0,
  0x0,0x0,0x0,0x0,0x0,0x0,0x0,0x0,0x0,0x0,0x0,0x0,0x0,0x0,0xbfd83f90,0x3f40bf00,
  0xa800,0xa820,0x0,0xa800,0xa808,0xa828,0x40,0xa840,0xa810,0xa830,0x90,0xa890,0xa818,0xa838,0xd0,0xa8d0,
  0xa920,0xa900,0x2400,0xae00,0xa928,0xa908,0x2440,0xae40,0xa930,0xa910,0x2490,0xae90,0xa938,0xa918,0x24d0,0xaed0,
  0xa840,0xa860,0x200,0xb400,0xa848,0xa868,0x240,0xb440,0xa850,0xa870,0x290,0xb490,0xa858,0xa878,0x2d0,0xb4d0,
  0xa960,0xa940,0x200,0xb400,0xa968,0xa948,0x240,0xb440,0xa970,0xa950,0x290,0xb490,0xa978,0xa958,0x2d0,0xb4d0,
  0xea80,0xeaa0,0x10,0xa810,0xea88,0xeaa8,0x50,0xa850,0xea90,0xeab0,0x80,0xa880,0xea98,0xeab8,0xc0,0xa8c0,
  0xa9a0,0xa980,0x2410,0xae10,0xa9a8,0xa988,0x2450,0xae50,0xa9b0,0xa990,0x2480,0xae80,0xa9b8,0xa998,0x24c0,0xaec0,
  0xc0c0,0xc0e0,0x210,0xb410,0xc0c8,0xc0e8,0x250,0xb450,0xc0d0,0xc0f0,0x280,0xb480,0xc0d8,0xc0f8,0x2c0,0xb4c0,
  0xc1e0,0xc1c0,0x210,0xb410,0xc1e8,0xc1c8,0x250,0xb450,0xc1f0,0xc1d0,0x280,0xb480,0xc1f8,0xc1d8,0x2c0,0xb4c0,
  0xae00,0xae20,0x20,0xa820,0xae08,0xae28,0x60,0xa860,0xae10,0xae30,0xb0,0xa8b0,0xae18,0xae38,0xf0,0xa8f0,
  0xeb20,0xeb00,0x2420,0xae20,0xeb28,0xeb08,0x2460,0xae60,0xeb30,0xeb10,0x24b0,0xaeb0,0xeb38,0xeb18,0x24f0,0xaef0,
  0xc040,0xc060,0x220,0xb420,0xc048,0xc068,0x260,0xb460,0xc050,0xc070,0x2b0,0xb4b0,0xc058,0xc078,0x2f0,0xb4f0,
  0xc160,0xc140,0x220,0xb420,0xc168,0xc148,0x260,0xb460,0xc170,0xc150,0x2b0,0xb4b0,0xc178,0xc158,0x2f0,0xb4f0,
  0xa880,0xa8a0,0x30,0xa830,0xa888,0xa8a8,0x70,0xa870,0xa890,0xa8b0,0xa0,0xa8a0,0xa898,0xa8b8,0xe0,0xa8e0,
  0xa8c0,0xa8e0,0x2430,0xae30,0xa8c8,0xa8e8,0x2470,0xae70,0xa8d0,0xa8f0,0x24a0,0xaea0,0xa8d8,0xa8f8,0x24e0,0xaee0,
  0xa9e0,0xa9c0,0x230,0xb430,0xa9e8,0xa9c8,0x270,0xb470,0xa9f0,0xa9d0,0x2a0,0xb4a0,0xa9f8,0xa9d8,0x2e0,0xb4e0,
  0xae00,0xae20,0x230,0xb430,0xae08,0xae28,0x270,0xb470,0xae10,0xae30,0x2a0,0xb4a0,0xae18,0xae38,0x2e0,0xb4e0,
  0x180000,0x40,0x820,0x10000000,0x180010,0xd0,0x8b0,0x10000000,0x180020,0x160,0x900,0x10000000,0x180030,0x1f0,0x990,0x10000000,
  0x180040,0x200,0xa60,0x20093180,0x180050,0x290,0xaf0,0x20093190,0x180060,0x320,0xb40,0x200931a0,0x180070,0x3b0,0xbd0,0x200931b0,
  0x180610,0xc40,0x1420,0x40090000,0x180600,0xcd0,0x14b0,0x40090010,0x180630,0xd60,0x1500,0x40090020,0x180620,0xdf0,0x1590,0x40090030,
  0x180650,0xe00,0x1660,0x200c3180,0x180640,0xe90,0x16f0,0x200c3190,0x180670,0xf20,0x1740,0x200c31a0,0x180660,0xfb0,0x17d0,0x200c31b0,
  0x180c20,0x50,0x828,0x400c0020,0x180c30,0xc0,0x8b8,0x400c0030,0x180c00,0x170,0x908,0x400c0000,0x180c10,0x1e0,0x998,0x400c0010,
  0x180c60,0x210,0xa68,0x200f31a0,0x180c70,0x280,0xaf8,0x200f31b0,0x180c40,0x330,0xb48,0x200f3180,0x180c50,0x3a0,0xbd8,0x200f3190,
  0x181230,0xc50,0x1428,0x400f0020,0x181220,0xcc0,0x14b8,0x400f0030,0x181210,0xd70,0x1508,0x400f0000,0x181200,0xde0,0x1598,0x400f0010,
  0x181270,0xe10,0x1668,0x10000000,0x181260,0xe80,0x16f8,0x10000000,0x181250,0xf30,0x1748,0x10000000,0x181240,0xfa0,0x17d8,0x10000000,
  0x181840,0x60,0x830,0x10000000,0x181850,0xf0,0x8a0,0x10000000,0x181860,0x140,0x910,0x10000000,0x181870,0x1d0,0x980,0x10000000,
  0x181800,0x220,0xa70,0x200931c0,0x181810,0x2b0,0xae0,0x200931d0,0x181820,0x300,0xb50,0x200931e0,0x181830,0x390,0xbc0,0x200931f0,
  0x181e50,0xc60,0x1430,0x40090040,0x181e40,0xcf0,0x14a0,0x40090050,0x181e70,0xd40,0x1510,0x40090060,0x181e60,0xdd0,0x1580,0x40090070,
  0x181e10,0xe20,0x1670,0x200c31c0,0x181e00,0xeb0,0x16e0,0x200c31d0,0x181e30,0xf00,0x1750,0x200c31e0,0x181e20,0xf90,0x17c0,0x200c31f0,
  0x182460,0x70,0x838,0x400c0060,0x182470,0xe0,0x8a8,0x400c0070,0x182440,0x150,0x918,0x400c0040,0x182450,0x1c0,0x988,0x400c0050,
  0x182420,0x230,0xa78,0x200f31e0,0x182430,0x2a0,0xae8,0x200f31f0,0x182400,0x310,0xb58,0x200f31c0,0x182410,0x380,0xbc8,0x200f31d0,
  0x182a70,0xc70,0x1438,0x400f0060,0x182a60,0xce0,0x14a8,0x400f0070,0x182a50,0xd50,0x1518,0x400f0040,0x182a40,0xdc0,0x1588,0x400f0050,
  0x182a30,0xe30,0x1678,0x10000000,0x182a20,0xea0,0x16e8,0x10000000,0x182a10,0xf10,0x1758,0x10000000,0x182a00,0xf80,0x17c8,0x10000000,
  0xbfd8bf90,0x3f403f00,0x0,0x0,0x0,0x0,0x0,0x0,0x0,0x0,0x0,0x0,0x0,0x0,0x0,0x0,
  0x0,0x0,0xbfd8bf90,0x3f403f00,0x0,0x0,0x0,0x0,0x0,0x0,0x0,0x0,0x0,0x0,0x0,0x0,
  0xbfd83f90,0x3f40bf00,0x0,0x0,0x0,0x0,0x0,0x0,0x0,0x0,0x0,0x0,0x0,0x0,0x0,0x0,
  0x0,0x0,0xbfd83f90,0x3f40bf00,0x0,0x0,0x0,0x0,0x0,0x0,0x0,0x0,0x0,0x0,0x0,0x0,
  0x0,0x0,0x0,0x0,0xbfd8bf90,0x3f403f00,0x0,0x0,0x0,0x0,0x0,0x0,0x0,0x0,0x0,0x0,
  0x0,0x0,0x0,0x0,0x0,0x0,0xbfd8bf90,0x3f403f00,0x0,0x0,0x0,0x0,0x0,0x0,0x0,0x0,
  0x0,0x0,0x0,0x0,0xbfd83f90,0x3f40bf00,0x0,0x0,0x0,0x0,0x0,0x0,0x0,0x0,0x0,0x0,
  0x0,0x0,0x0,0x0,0x0,0x0,0xbfd83f90,0x3f40bf00,0x0,0x0,0x0,0x0,0x0,0x0,0x0,0x0,
  0x0,0x0,0x0,0x0,0x0,0x0,0x0,0x0,0xbfd8bf90,0x3f403f00,0x0,0x0,0x0,0x0,0x0,0x0,
  0x0,0x0,0x0,0x0,0x0,0x0,0x0,0x0,0x0,0x0,0xbfd8bf90,0x3f403f00,0x0,0x0,0x0,0x0,
  0x0,0x0,0x0,0x0,0x0,0x0,0x0,0x0,0xbfd83f90,0x3f40bf00,0x0,0x0,0x0,0x0,0x0,0x0,
  0x0,0x0,0x0,0x0,0x0,0x0,0x0,0x0,0x0,0x0,0xbfd83f90,0x3f40bf00,0x0,0x0,0x0,0x0,
  0x0,0x0,0x0,0x0,0x0,0x0,0x0,0x0,0x0,0x0,0x0,0x0,0xbfd8bf90,0x3f403f00,0x0,0x0,
  0x0,0x0,0x0,0x0,0x0,0x0,0x0,0x0,0x0,0x0,0x0,0x0,0x0,0x0,0xbfd8bf90,0x3f403f00,
  0x0,0x0,0x0,0x0,0x0,0x0,0x0,0x0,0x0,0x0,0x0,0x0,0xbfd83f90,0x3f40bf00,0x0,0x0,
  0x0,0x0,0x0,0x0,0x0,0x0,0x0,0x0,0x0,0x0,0x0,0x0,0x0,0x0,0xbfd83f90,0x3f40bf00,
  0xb400,0xb420,0x120,0xa920,0xb408,0xb428,0x160,0xa960,0xb410,0xb430,0x1b0,0xa9b0,0xb418,0xb438,0x1f0,0xa9f0,
  0xb440,0xb460,0x2520,0xaf20,0xb448,0xb468,0x2560,0xaf60,0xb450,0xb470,0x25b0,0xafb0,0xb458,0xb478,0x25f0,0xaff0,
  0xcc40,0xcc60,0x320,0xb520,0xcc48,0xcc68,0x360,0xb560,0xcc50,0xcc70,0x3b0,0xb5b0,0xcc58,0xcc78,0x3f0,0xb5f0,
  0xcd60,0xcd40,0x320,0xb520,0xcd68,0xcd48,0x360,0xb560,0xcd70,0xcd50,0x3b0,0xb5b0,0xcd78,0xcd58,0x3f0,0xb5f0,
  0xb5a0,0xb580,0x130,0xa930,0xb5a8,0xb588,0x170,0xa970,0xb5b0,0xb590,0x1a0,0xa9a0,0xb5b8,0xb598,0x1e0,0xa9e0,
  0xccc0,0xcce0,0x2530,0xaf30,0xccc8,0xcce8,0x2570,0xaf70,0xccd0,0xccf0,0x25a0,0xafa0,0xccd8,0xccf8,0x25e0,0xafe0,
  0xa9e0,0xa9c0,0x330,0xb530,0xa9e8,0xa9c8,0x370,0xb570,0xa9f0,0xa9d0,0x3a0,0xb5a0,0xa9f8,0xa9d8,0x3e0,0xb5e0,
  0xc1e0,0xc1c0,0x330,0xb530,0xc1e8,0xc1c8,0x370,0xb570,0xc1f0,0xc1d0,0x3a0,0xb5a0,0xc1f8,0xc1d8,0x3e0,0xb5e0,
  0xb520,0xb500,0x100,0xa900,0xb528,0xb508,0x140,0xa940,0xb530,0xb510,0x190,0xa990,0xb538,0xb518,0x1d0,0xa9d0,
  0xea40,0xea60,0x2500,0xaf00,0xea48,0xea68,0x2540,0xaf40,0xea50,0xea70,0x2590,0xaf90,0xea58,0xea78,0x25d0,0xafd0,
  0xb560,0xb540,0x300,0xb500,0xb568,0xb548,0x340,0xb540,0xb570,0xb550,0x390,0xb590,0xb578,0xb558,0x3d0,0xb5d0,
  0xb480,0xb4a0,0x300,0xb500,0xb488,0xb4a8,0x340,0xb540,0xb490,0xb4b0,0x390,0xb590,0xb498,0xb4b8,0x3d0,0xb5d0,
  0xb4c0,0xb4e0,0x110,0xa910,0xb4c8,0xb4e8,0x150,0xa950,0xb4d0,0xb4f0,0x180,0xa980,0xb4d8,0xb4f8,0x1c0,0xa9c0,
  0xb5e0,0xb5c0,0x2510,0xaf10,0xb5e8,0xb5c8,0x2550,0xaf50,0xb5f0,0xb5d0,0x2580,0xaf80,0xb5f8,0xb5d8,0x25c0,0xafc0,
  0xcde0,0xcdc0,0x310,0xb510,0xcde8,0xcdc8,0x350,0xb550,0xcdf0,0xcdd0,0x380,0xb580,0xcdf8,0xcdd8,0x3c0,0xb5c0,
  0xb520,0xb500,0x310,0xb510,0xb528,0xb508,0x350,0xb550,0xb530,0xb510,0x380,0xb580,0xb538,0xb518,0x3c0,0xb5c0,
  0x0,0x400,0x840,0x10000000,0x10,0x490,0x8d0,0x10000000,0x20,0x520,0x960,0x10000000,0x30,0x5b0,0x9f0,0x10000000,
  0x40,0x640,0xa00,0x20123180,0x50,0x6d0,0xa90,0x20123190,0x60,0x760,0xb20,0x201231a0,0x70,0x7f0,0xbb0,0x201231b0,
  0x610,0x1000,0x1440,0x40120000,0x600,0x1090,0x14d0,0x40120010,0x630,0x1120,0x1560,0x40120020,0x620,0x11b0,0x15f0,0x40120030,
  0x650,0x1240,0x1600,0x20153180,0x640,0x12d0,0x1690,0x20153190,0x670,0x1360,0x1720,0x201531a0,0x660,0x13f0,0x17b0,0x201531b0,
  0xc20,0x410,0x848,0x40150020,0xc30,0x480,0x8d8,0x40150030,0xc00,0x530,0x968,0x40150000,0xc10,0x5a0,0x9f8,0x40150010,
  0xc60,0x650,0xa08,0x201831a0,0xc70,0x6c0,0xa98,0x201831b0,0xc40,0x770,0xb28,0x20183180,0xc50,0x7e0,0xbb8,0x20183190,
  0x1230,0x1010,0x1448,0x40180020,0x1220,0x1080,0x14d8,0x40180030,0x1210,0x1130,0x1568,0x40180000,0x1200,0x11a0,0x15f8,0x40180010,
  0x1270,0x1250,0x1608,0x10000000,0x1260,0x12c0,0x1698,0x10000000,0x1250,0x1370,0x1728,0x10000000,0x1240,0x13e0,0x17b8,0x10000000,
  0x1840,0x420,0x850,0x10000000,0x1850,0x4b0,0x8c0,0x10000000,0x1860,0x500,0x970,0x10000000,0x1870,0x590,0x9e0,0x10000000,
  0x1800,0x660,0xa10,0x201231c0,0x1810,0x6f0,0xa80,0x201231d0,0x1820,0x740,0xb30,0x201231e0,0x1830,0x7d0,0xba0,0x201231f0,
  0x1e50,0x1020,0x1450,0x40120040,0x1e40,0x10b0,0x14c0,0x40120050,0x1e70,0x1100,0x1570,0x40120060,0x1e60,0x1190,0x15e0,0x40120070,
  0x1e10,0x1260,0x1610,0x201531c0,0x1e00,0x12f0,0x1680,0x201531d0,0x1e30,0x1340,0x1730,0x201531e0,0x1e20,0x13d0,0x17a0,0x201531f0,
  0x2460,0x430,0x858,0x40150060,0x2470,0x4a0,0x8c8,0x40150070,0x2440,0x510,0x978,0x40150040,0x2450,0x580,0x9e8,0x40150050,
  0x2420,0x670,0xa18,0x201831e0,0x2430,0x6e0,0xa88,0x201831f0,0x2400,0x750,0xb38,0x201831c0,0x2410,0x7c0,0xba8,0x201831d0,
  0x2a70,0x1030,0x1458,0x40180060,0x2a60,0x10a0,0x14c8,0x40180070,0x2a50,0x1110,0x1578,0x40180040,0x2a40,0x1180,0x15e8,0x40180050,
  0x2a30,0x1270,0x1618,0x10000000,0x2a20,0x12e0,0x1688,0x10000000,0x2a10,0x1350,0x1738,0x10000000,0x2a00,0x13c0,0x17a8,0x10000000,
  0xbfd8bf90,0x3f403f00,0x0,0x0,0x0,0x0,0x0,0x0,0x0,0x0,0x0,0x0,0x0,0x0,0x0,0x0,
  0x0,0x0,0xbfd8bf90,0x3f403f00,0x0,0x0,0x0,0x0,0x0,0x0,0x0,0x0,0x0,0x0,0x0,0x0,
  0xbfd83f90,0x3f40bf00,0x0,0x0,0x0,0x0,0x0,0x0,0x0,0x0,0x0,0x0,0x0,0x0,0x0,0x0,
  0x0,0x0,0xbfd83f90,0x3f40bf00,0x0,0x0,0x0,0x0,0x0,0x0,0x0,0x0,0x0,0x0,0x0,0x0,
  0x0,0x0,0x0,0x0,0xbfd8bf90,0x3f403f00,0x0,0x0,0x0,0x0,0x0,0x0,0x0,0x0,0x0,0x0,
  0x0,0x0,0x0,0x0,0x0,0x0,0xbfd8bf90,0x3f403f00,0x0,0x0,0x0,0x0,0x0,0x0,0x0,0x0,
  0x0,0x0,0x0,0x0,0xbfd83f90,0x3f40bf00,0x0,0x0,0x0,0x0,0x0,0x0,0x0,0x0,0x0,0x0,
  0x0,0x0,0x0,0x0,0x0,0x0,0xbfd83f90,0x3f40bf00,0x0,0x0,0x0,0x0,0x0,0x0,0x0,0x0,
  0x0,0x0,0x0,0x0,0x0,0x0,0x0,0x0,0xbfd8bf90,0x3f403f00,0x0,0x0,0x0,0x0,0x0,0x0,
  0x0,0x0,0x0,0x0,0x0,0x0,0x0,0x0,0x0,0x0,0xbfd8bf90,0x3f403f00,0x0,0x0,0x0,0x0,
  0x0,0x0,0x0,0x0,0x0,0x0,0x0,0x0,0xbfd83f90,0x3f40bf00,0x0,0x0,0x0,0x0,0x0,0x0,
  0x0,0x0,0x0,0x0,0x0,0x0,0x0,0x0,0x0,0x0,0xbfd83f90,0x3f40bf00,0x0,0x0,0x0,0x0,
  0x0,0x0,0x0,0x0,0x0,0x0,0x0,0x0,0x0,0x0,0x0,0x0,0xbfd8bf90,0x3f403f00,0x0,0x0,
  0x0,0x0,0x0,0x0,0x0,0x0,0x0,0x0,0x0,0x0,0x0,0x0,0x0,0x0,0xbfd8bf90,0x3f403f00,
  0x0,0x0,0x0,0x0,0x0,0x0,0x0,0x0,0x0,0x0,0x0,0x0,0xbfd83f90,0x3f40bf00,0x0,0x0,
  0x0,0x0,0x0,0x0,0x0,0x0,0x0,0x0,0x0,0x0,0x0,0x0,0x0,0x0,0xbfd83f90,0x3f40bf00,
  0xc000,0xc020,0x2600,0xba00,0xc008,0xc028,0x2640,0xba40,0xc010,0xc030,0x2690,0xba90,0xc018,0xc038,0x26d0,0xbad0,
  0xd800,0xd820,0x400,0xc000,0xd808,0xd828,0x440,0xc040,0xd810,0xd830,0x490,0xc090,0xd818,0xd838,0x4d0,0xc0d0,
  0xc120,0xc100,0x2800,0xc600,0xc128,0xc108,0x2840,0xc640,0xc130,0xc110,0x2890,0xc690,0xc138,0xc118,0x28d0,0xc6d0,
  0xd840,0xd860,0x2800,0xc600,0xd848,0xd868,0x2840,0xc640,0xd850,0xd870,0x2890,0xc690,0xd858,0xd878,0x28d0,0xc6d0,
  0xc080,0xc0a0,0x2610,0xba10,0xc088,0xc0a8,0x2650,0xba50,0xc090,0xc0b0,0x2680,0xba80,0xc098,0xc0b8,0x26c0,0xbac0,
  0xc1a0,0xc180,0x410,0xc010,0xc1a8,0xc188,0x450,0xc050,0xc1b0,0xc190,0x480,0xc080,0xc1b8,0xc198,0x4c0,0xc0c0,
  0xd9a0,0xd980,0x2810,0xc610,0xd9a8,0xd988,0x2850,0xc650,0xd9b0,0xd990,0x2880,0xc680,0xd9b8,0xd998,0x28c0,0xc6c0,
  0xd9e0,0xd9c0,0x2810,0xc610,0xd9e8,0xd9c8,0x2850,0xc650,0xd9f0,0xd9d0,0x2880,0xc680,0xd9f8,0xd9d8,0x28c0,0xc6c0,
  0xc600,0xc620,0x2620,0xba20,0xc608,0xc628,0x2660,0xba60,0xc610,0xc630,0x26b0,0xbab0,0xc618,0xc638,0x26f0,0xbaf0,
  0xde00,0xde20,0x420,0xc020,0xde08,0xde28,0x460,0xc060,0xde10,0xde30,0x4b0,0xc0b0,0xde18,0xde38,0x4f0,0xc0f0,
  0xd920,0xd900,0x2820,0xc620,0xd928,0xd908,0x2860,0xc660,0xd930,0xd910,0x28b0,0xc6b0,0xd938,0xd918,0x28f0,0xc6f0,
  0xd960,0xd940,0x2820,0xc620,0xd968,0xd948,0x2860,0xc660,0xd970,0xd950,0x28b0,0xc6b0,0xd978,0xd958,0x28f0,0xc6f0,
  0xd880,0xd8a0,0x2630,0xba30,0xd888,0xd8a8,0x2670,0xba70,0xd890,0xd8b0,0x26a0,0xbaa0,0xd898,0xd8b8,0x26e0,0xbae0,
  0xeba0,0xeb80,0x430,0xc030,0xeba8,0xeb88,0x470,0xc070,0xebb0,0xeb90,0x4a0,0xc0a0,0xebb8,0xeb98,0x4e0,0xc0e0,
  0xd8c0,0xd8e0,0x2830,0xc630,0xd8c8,0xd8e8,0x2870,0xc670,0xd8d0,0xd8f0,0x28a0,0xc6a0,0xd8d8,0xd8f8,0x28e0,0xc6e0,
  0xc600,0xc620,0x2830,0xc630,0xc608,0xc628,0x2870,0xc670,0xc610,0xc630,0x28a0,0xc6a0,0xc618,0xc638,0x28e0,0xc6e0,
  0x180000,0x440,0x860,0x10000000,0x180010,0x4d0,0x8f0,0x10000000,0x180020,0x560,0x940,0x10000000,0x180030,0x5f0,0x9d0,0x10000000,
  0x180040,0x600,0xa20,0x201b3180,0x180050,0x690,0xab0,0x201b3190,0x180060,0x720,0xb00,0x201b31a0,0x180070,0x7b0,0xb90,0x201b31b0,
  0x180610,0x1040,0x1460,0x401b0000,0x180600,0x10d0,0x14f0,0x401b0010,0x180630,0x1160,0x1540,0x401b0020,0x180620,0x11f0,0x15d0,0x401b0030,
  0x180650,0x1200,0x1620,0x201e3180,0x180640,0x1290,0x16b0,0x201e3190,0x180670,0x1320,0x1700,0x201e31a0,0x180660,0x13b0,0x1790,0x201e31b0,
  0x180c20,0x450,0x868,0x401e0020,0x180c30,0x4c0,0x8f8,0x401e0030,0x180c00,0x570,0x948,0x401e0000,0x180c10,0x5e0,0x9d8,0x401e0010,
  0x180c60,0x610,0xa28,0x202131a0,0x180c70,0x680,0xab8,0x202131b0,0x180c40,0x730,0xb08,0x20213180,0x180c50,0x7a0,0xb98,0x20213190,
  0x181230,0x1050,0x1468,0x40210020,0x181220,0x10c0,0x14f8,0x40210030,0x181210,0x1170,0x1548,0x40210000,0x181200,0x11e0,0x15d8,0x40210010,
  0x181270,0x1210,0x1628,0x10000000,0x181260,0x1280,0x16b8,0x10000000,0x181250,0x1330,0x1708,0x10000000,0x181240,0x13a0,0x1798,0x10000000,
  0x181840,0x460,0x870,0x10000000,0x181850,0x4f0,0x8e0,0x10000000,0x181860,0x540,0x950,0x10000000,0x181870,0x5d0,0x9c0,0x10000000,
  0x181800,0x620,0xa30,0x201b31c0,0x181810,0x6b0,0xaa0,0x201b31d0,0x181820,0x700,0xb10,0x201b31e0,0x181830,0x790,0xb80,0x201b31f0,
  0x181e50,0x1060,0x1470,0x401b0040,0x181e40,0x10f0,0x14e0,0x401b0050,0x181e70,0x1140,0x1550,0x401b0060,0x181e60,0x11d0,0x15c0,0x401b0070,
  0x181e10,0x1220,0x1630,0x201e31c0,0x181e00,0x12b0,0x16a0,0x201e31d0,0x181e30,0x1300,0x1710,0x201e31e0,0x181e20,0x1390,0x1780,0x201e31f0,
  0x182460,0x470,0x878,0x401e0060,0x182470,0x4e0,0x8e8,0x401e0070,0x182440,0x550,0x958,0x401e0040,0x182450,0x5c0,0x9c8,0x401e0050,
  0x182420,0x630,0xa38,0x202131e0,0x182430,0x6a0,0xaa8,0x202131f0,0x182400,0x710,0xb18,0x202131c0,0x182410,0x780,0xb88,0x202131d0,
  0x182a70,0x1070,0x1478,0x40210060,0x182a60,0x10e0,0x14e8,0x40210070,0x182a50,0x1150,0x1558,0x40210040,0x182a40,0x11c0,0x15c8,0x40210050,
  0x182a30,0x1230,0x1638,0x10000000,0x182a20,0x12a0,0x16a8,0x10000000,0x182a10,0x1310,0x1718,0x10000000,0x182a00,0x1380,0x1788,0x10000000,
  0xbfd8bf90,0x3f403f00,0x0,0x0,0x0,0x0,0x0,0x0,0x0,0x0,0x0,0x0,0x0,0x0,0x0,0x0,
  0x0,0x0,0xbfd8bf90,0x3f403f00,0x0,0x0,0x0,0x0,0x0,0x0,0x0,0x0,0x0,0x0,0x0,0x0,
  0xbfd83f90,0x3f40bf00,0x0,0x0,0x0,0x0,0x0,0x0,0x0,0x0,0x0,0x0,0x0,0x0,0x0,0x0,
  0x0,0x0,0xbfd83f90,0x3f40bf00,0x0,0x0,0x0,0x0,0x0,0x0,0x0,0x0,0x0,0x0,0x0,0x0,
  0x0,0x0,0x0,0x0,0xbfd8bf90,0x3f403f00,0x0,0x0,0x0,0x0,0x0,0x0,0x0,0x0,0x0,0x0,
  0x0,0x0,0x0,0x0,0x0,0x0,0xbfd8bf90,0x3f403f00,0x0,0x0,0x0,0x0,0x0,0x0,0x0,0x0,
  0x0,0x0,0x0,0x0,0xbfd83f90,0x3f40bf00,0x0,0x0,0x0,0x0,0x0,0x0,0x0,0x0,0x0,0x0,
  0x0,0x0,0x0,0x0,0x0,0x0,0xbfd83f90,0x3f40bf00,0x0,0x0,0x0,0x0,0x0,0x0,0x0,0x0,
  0x0,0x0,0x0,0x0,0x0,0x0,0x0,0x0,0xbfd8bf90,0x3f403f00,0x0,0x0,0x0,0x0,0x0,0x0,
  0x0,0x0,0x0,0x0,0x0,0x0,0x0,0x0,0x0,0x0,0xbfd8bf90,0x3f403f00,0x0,0x0,0x0,0x0,
  0x0,0x0,0x0,0x0,0x0,0x0,0x0,0x0,0xbfd83f90,0x3f40bf00,0x0,0x0,0x0,0x0,0x0,0x0,
  0x0,0x0,0x0,0x0,0x0,0x0,0x0,0x0,0x0,0x0,0xbfd83f90,0x3f40bf00,0x0,0x0,0x0,0x0,
  0x0,0x0,0x0,0x0,0x0,0x0,0x0,0x0,0x0,0x0,0x0,0x0,0xbfd8bf90,0x3f403f00,0x0,0x0,
  0x0,0x0,0x0,0x0,0x0,0x0,0x0,0x0,0x0,0x0,0x0,0x0,0x0,0x0,0xbfd8bf90,0x3f403f00,
  0x0,0x0,0x0,0x0,0x0,0x0,0x0,0x0,0x0,0x0,0x0,0x0,0xbfd83f90,0x3f40bf00,0x0,0x0,
  0x0,0x0,0x0,0x0,0x0,0x0,0x0,0x0,0x0,0x0,0x0,0x0,0x0,0x0,0xbfd83f90,0x3f40bf00,
  0xcc00,0xcc20,0x2720,0xbb20,0xcc08,0xcc28,0x2760,0xbb60,0xcc10,0xcc30,0x27b0,0xbbb0,0xcc18,0xcc38,0x27f0,0xbbf0,
  0xcd20,0xcd00,0x520,0xc120,0xcd28,0xcd08,0x560,0xc160,0xcd30,0xcd10,0x5b0,0xc1b0,0xcd38,0xcd18,0x5f0,0xc1f0,
  0xe440,0xe460,0x2920,0xc720,0xe448,0xe468,0x2960,0xc760,0xe450,0xe470,0x29b0,0xc7b0,0xe458,0xe478,0x29f0,0xc7f0,
  0xeb60,0xeb40,0x2920,0xc720,0xeb68,0xeb48,0x2960,0xc760,0xeb70,0xeb50,0x29b0,0xc7b0,0xeb78,0xeb58,0x29f0,0xc7f0,
  0xe480,0xe4a0,0x2730,0xbb30,0xe488,0xe4a8,0x2770,0xbb70,0xe490,0xe4b0,0x27a0,0xbba0,0xe498,0xe4b8,0x27e0,0xbbe0,
  0xe5a0,0xe580,0x530,0xc130,0xe5a8,0xe588,0x570,0xc170,0xe5b0,0xe590,0x5a0,0xc1a0,0xe5b8,0xe598,0x5e0,0xc1e0,
  0xe4c0,0xe4e0,0x2930,0xc730,0xe4c8,0xe4e8,0x2970,0xc770,0xe4d0,0xe4f0,0x29a0,0xc7a0,0xe4d8,0xe4f8,0x29e0,0xc7e0,
  0xd9e0,0xd9c0,0x2930,0xc730,0xd9e8,0xd9c8,0x2970,0xc770,0xd9f0,0xd9d0,0x29a0,0xc7a0,0xd9f8,0xd9d8,0x29e0,0xc7e0,
  0xe400,0xe420,0x2700,0xbb00,0xe408,0xe428,0x2740,0xbb40,0xe410,0xe430,0x2790,0xbb90,0xe418,0xe438,0x27d0,0xbbd0,
  0xe520,0xe500,0x500,0xc100,0xe528,0xe508,0x540,0xc140,0xe530,0xe510,0x590,0xc190,0xe538,0xe518,0x5d0,0xc1d0,
  0xe560,0xe540,0x2900,0xc700,0xe568,0xe548,0x2940,0xc740,0xe570,0xe550,0x2990,0xc790,0xe578,0xe558,0x29d0,0xc7d0,
  0xcc80,0xcca0,0x2900,0xc700,0xcc88,0xcca8,0x2940,0xc740,0xcc90,0xccb0,0x2990,0xc790,0xcc98,0xccb8,0x29d0,0xc7d0,
  0xcda0,0xcd80,0x2710,0xbb10,0xcda8,0xcd88,0x2750,0xbb50,0xcdb0,0xcd90,0x2780,0xbb80,0xcdb8,0xcd98,0x27c0,0xbbc0,
  0xeac0,0xeae0,0x510,0xc110,0xeac8,0xeae8,0x550,0xc150,0xead0,0xeaf0,0x580,0xc180,0xead8,0xeaf8,0x5c0,0xc1c0,
  0xe5e0,0xe5c0,0x2910,0xc710,0xe5e8,0xe5c8,0x2950,0xc750,0xe5f0,0xe5d0,0x2980,0xc780,0xe5f8,0xe5d8,0x29c0,0xc7c0,
  0xe400,0xe420,0x2910,0xc710,0xe408,0xe428,0x2950,0xc750,0xe410,0xe430,0x2980,0xc780,0xe418,0xe438,0x29c0,0xc7c0,
  0x0,0x4800,0x0,0x0,0x10,0x4802,0x180,0x0,0x20,0x4804,0x300,0x0,0x30,0x4806,0x480,0x0,
  0x40,0x4808,0x1800,0x0,0x50,0x480a,0x1980,0x0,0x60,0x480c,0x1b00,0x0,0x70,0x480e,0x1c80,0x0,
  0x9000,0x4810,0x2,0x0,0x9010,0x4812,0x182,0x0,0x9020,0x4814,0x302,0x0,0x9030,0x4816,0x482,0x0,
  0x9040,0x4818,0x1802,0x0,0x9050,0x481a,0x1982,0x0,0x9060,0x481c,0x1b02,0x0,0x9070,0x481e,0x1c82,0x0,
  0x12000,0x4820,0x4,0x0,0x12010,0x4822,0x184,0x0,0x12020,0x4824,0x304,0x0,0x12030,0x4826,0x484,0x0,
  0x12040,0x4828,0x1804,0x0,0x12050,0x482a,0x1984,0x0,0x12060,0x482c,0x1b04,0x0,0x12070,0x482e,0x1c84,0x0,
  0x1b000,0x4830,0x6,0x0,0x1b010,0x4832,0x186,0x0,0x1b020,0x4834,0x306,0x0,0x1b030,0x4836,0x486,0x0,
  0x1b040,0x4838,0x1806,0x0,0x1b050,0x483a,0x1986,0x0,0x1b060,0x483c,0x1b06,0x0,0x1b070,0x483e,0x1c86,0x0,
  0x24000,0x4840,0x8,0x8,0x24010,0x4842,0x188,0x8,0x24020,0x4844,0x308,0x8,0x24030,0x4846,0x488,0x8,
  0x24040,0x4848,0x1808,0x8,0x24050,0x484a,0x1988,0x8,0x24060,0x484c,0x1b08,0x8,0x24070,0x484e,0x1c88,0x8,
  0x2d000,0x4850,0xa,0x8,0x2d010,0x4852,0x18a,0x8,0x2d020,0x4854,0x30a,0x8,0x2d030,0x4856,0x48a,0x8,
  0x2d040,0x4858,0x180a,0x8,0x2d050,0x485a,0x198a,0x8,0x2d060,0x485c,0x1b0a,0x8,0x2d070,0x485e,0x1c8a,0x8,
  0x36000,0x4860,0xc,0x8,0x36010,0x4862,0x18c,0x8,0x36020,0x4864,0x30c,0x8,0x36030,0x4866,0x48c,0x8,
  0x36040,0x4868,0x180c,0x8,0x36050,0x486a,0x198c,0x8,0x36060,0x486c,0x1b0c,0x8,0x36070,0x486e,0x1c8c,0x8,
  0x3f000,0x4870,0xe,0x8,0x3f010,0x4872,0x18e,0x8,0x3f020,0x4874,0x30e,0x8,0x3f030,0x4876,0x48e,0x8,
  0x3f040,0x4878,0x180e,0x8,0x3f050,0x487a,0x198e,0x8,0x3f060,0x487c,0x1b0e,0x8,0x3f070,0x487e,0x1c8e,0x8,
};

__global__ __launch_bounds__(256, 1) void wino_main_kernel(const char* __restrict__ x, char* __restrict__ Uq, char* __restrict__ out, const char* __restrict__ wt) {
  __shared__ __attribute__((aligned(1024))) char smem[163840];
  asm volatile("s_getpc_b64 s[40:41]\n\tLwino_pc:\n\ts_bfe_u32 s42, %0, 0x50003\n\ts_lshl_b32 s42, s42, 12\n\tv_lshl_add_u32 v203, %1, 4, s42\n\t"
               "v_min_u32 v203, (Lwino_end-Lwino_pc-16)&-16, v203\n\tglobal_load_dwordx4 v[100:103], v203, s[40:41]"
               :: "s"(blockIdx.x), "v"(threadIdx.x) : "s40", "s41", "s42", "v100", "v101", "v102", "v103", "v203", "scc", "memory");
  const int lane = threadIdx.x & 63;
  const int w = __builtin_amdgcn_readfirstlane(threadIdx.x >> 6);
  const int sub = blockIdx.x >> 3, bx = sub & 15, by = 4 * (blockIdx.x & 7) + (sub >> 4);
  const unsigned long long px = (unsigned long long)x, pu = (unsigned long long)Uq, py = (unsigned long long)out, pw = (unsigned long long)wt;
  const int xbase = ((16 * by - 1) * IMG_W + 32 * bx) * PXB;
  const int ybase = (16 * by * IMG_W + 32 * bx) * PXB;
  const unsigned medge = (bx == 0 ? 0xffffffffu : 0xbfffffffu) & (bx == 15 ? 0xffffffffu : 0xdfffffffu);
  unsigned fso, fuo, fs8, fln;
  {
    const unsigned p = blockIdx.x - 1, hb = p & 1, q = (p >> 1) & 127, r = q / 3, kb = q - 3 * r, wv = r & 3, chunk = r >> 2;
    fs8 = 1 - (kb >> 1);
    fso = p < 144 ? chunk * 12288 + hb * 3072 + (w >> 1) * 1536 + (kb < 2 ? kb * 8 : 256) + ((2 * wv + (w & 1)) << (4 + fs8)) : 0x40000000u;
    fuo = p < 144 ? (chunk * 24 + kb) * 12288 + wv * 3072 + hb * 512 + w * 128 : 0x40000000u;
    fln = chunk < 2 ? 64 : 48;
  }
  const unsigned fwo = (((lane & 3) + 4 * (lane & 4)) * CH + 4 * (lane >> 5) + ((lane >> 3) & 3)) * 2;
  const unsigned flw = 18432 + w * 3072 + lane * 2, fst = lane < fln ? (lane >> 3) * 36864 + (lane & 7) * 16 : 0x40000000u;
  unsigned dummy = 0;
  asm volatile(
"s_mov_b32 s52, %15\n\t"
"s_and_b32 s53, %16, 0xffff\n\t"
"s_mov_b32 s54, 663552\n\t"
"s_mov_b32 s55, 0x20000\n\t"
"s_mov_b32 s56, %17\n\t"
"s_mov_b32 s57, %18\n\t"
"s_mov_b32 s74, %21\n\t"
"s_mul_i32 s66, %11, 4\n\t"
"s_mov_b32 s73, %9\n\t"
"s_or_b32 s72, %11, %9\n\t"
"v_mov_b32 v32, 0\n\t"
"v_mov_b32 v33, 0\n\t"
"v_mov_b32 v34, 0\n\t"
"v_mov_b32 v35, 0\n\t"
"v_mov_b32 v36, 0\n\t"
"v_mov_b32 v37, 0\n\t"
"v_mov_b32 v38, 0\n\t"
"v_mov_b32 v39, 0\n\t"
"v_mov_b32 v40, 0\n\t"
"v_mov_b32 v118, %22\n\t"
"v_mov_b32 v42, %23\n\t"
"v_add_u32 v43, 18432, v252\n\t"
"v_mov_b32 v44, %24\n\t"
"v_mov_b32 v119, %25\n\t"
"s_mov_b32 s8, %1\n\t"
"s_and_b32 s9, %2, 0xffff\n\t"
"s_mov_b32 s10, 0x200000\n\t"
"s_mov_b32 s11, 0x20000\n\t"
"s_mov_b32 s12, %3\n\t"
"s_and_b32 s13, %4, 0xffff\n\t"
"s_mov_b32 s14, 100663296\n\t"
"s_mov_b32 s15, 0x20000\n\t"
"s_mov_b32 s16, %5\n\t"
"s_and_b32 s17, %6, 0xffff\n\t"
"s_mov_b32 s18, 100663296\n\t"
"s_mov_b32 s19, 0x20000\n\t"
"s_mov_b32 s21, %7\n\t"
"s_lshr_b32 s60, %9, 1\n\t"
"s_mul_i32 s61, s60, 384\n\t"
"s_add_u32 s22, %8, s61\n\t"
"s_mul_i32 s61, s60, 196608\n\t"
"s_add_u32 s23, %8, s61\n\t"
"s_mul_i32 s24, %9, 1536\n\t"
"s_add_u32 s24, s24, 43008\n\t"
"s_mul_i32 s25, %9, 19968\n\t"
"s_add_u32 s25, s25, 61440\n\t"
"s_mul_i32 s26, s60, 19968\n\t"
"s_add_u32 s26, s26, 141312\n\t"
"s_sub_u32 s61, s60, %9\n\t"
"s_add_u32 s61, s61, 4\n\t"
"s_mul_i32 s27, s61, 589824\n\t"
"s_bitcmp1_b32 %9, 0\n\t"
"s_mov_b32 s36, 0x0fff0000\n\t"
"s_cselect_b32 s36, 0xfff0, s36\n\t"
"s_mov_b32 s37, s36\n\t"
"s_mov_b32 s44, 0x0fff0fff\n\t"
"s_mov_b32 s45, 0x0fff0fff\n\t"
"s_mov_b64 s[46:47], %12\n\t"
"s_add_u32 s48, s46, 12288\n\t"
"s_addc_u32 s49, s47, 0\n\t"
"s_mul_i32 s50, %9, 122880\n\t"
"s_lshl_b32 s51, s60, 30\n\t"
"s_add_u32 s51, s51, s50\n\t"
"s_mov_b32 s40, %14\n\t"
"s_sub_u32 s42, %7, 384\n\t"
"s_mov_b32 s38, 0x3f2aaaab\n\t"
"s_mov_b32 s39, 0x3fc00000\n\t"
"s_mov_b32 s41, 0x3ee38e39\n\t"
"s_mov_b32 s43, 0x40100000\n\t"
"s_nop 4\n\t"
"s_branch Lpro_%=\n\t"
"Lfb_%=:\n\t"
"s_waitcnt vmcnt(0) lgkmcnt(0)\n\t"
"s_nop 15\n\t"
"v_lshrrev_b32 v255, 9, v252\n\t"
"v_lshl_add_u32 v255, v255, 10, v252\n\t"
"v_add_u32 v255, 44032, v255\n\t"
"ds_write_b128 v255, v[32:35] offset:0\n\t"
"ds_write_b128 v255, v[36:39] offset:3072\n\t"
"ds_write_b128 v255, v[40:43] offset:6144\n\t"
"ds_write_b128 v255, v[44:47] offset:9216\n\t"
"ds_write_b128 v255, v[48:51] offset:12288\n\t"
"ds_write_b128 v255, v[52:55] offset:15360\n\t"
"ds_write_b128 v255, v[56:59] offset:18432\n\t"
"ds_write_b128 v255, v[60:63] offset:21504\n\t"
"ds_write_b128 v255, v[64:67] offset:24576\n\t"
"ds_write_b128 v255, v[68:71] offset:27648\n\t"
"ds_write_b128 v255, v[72:75] offset:30720\n\t"
"ds_write_b128 v255, v[76:79] offset:33792\n\t"
"ds_write_b128 v255, v[80:83] offset:36864\n\t"
"ds_write_b128 v255, v[84:87] offset:39936\n\t"
"ds_write_b128 v255, v[88:91] offset:43008\n\t"
"ds_write_b128 v255, v[92:95] offset:46080\n\t"
"ds_write_b128 v255, v[96:99] offset:49152\n\t"
"ds_write_b128 v255, v[100:103] offset:52224\n\t"
"ds_write_b128 v255, v[104:107] offset:55296\n\t"
"ds_write_b128 v255, v[108:111] offset:58368\n\t"
"ds_write_b128 v255, v[112:115] offset:61440\n\t"
"ds_write_b128 v255, v[116:119] offset:64512\n\t"
"s_waitcnt lgkmcnt(0)\n\t"
"s_add_u32 s60, s48, 12288\n\t"
"s_addc_u32 s61, s49, 0\n\t"
"global_load_dwordx4 v[116:119], v252, s[60:61]\n\t"
"v_mov_b32 v32, 0\n\t"
"v_mov_b32 v33, 0\n\t"
"v_mov_b32 v34, 0\n\t"
"v_mov_b32 v35, 0\n\t"
"v_mov_b32 v36, 0\n\t"
"v_mov_b32 v37, 0\n\t"
"v_mov_b32 v38, 0\n\t"
"v_mov_b32 v39, 0\n\t"
"v_mov_b32 v40, 0\n\t"
"v_add_u32 v43, 18432, v252\n\t"
"s_waitcnt vmcnt(0)\n\t"
"v_mov_b32 v44, v116\n\t"
"v_mov_b32 v42, v117\n\t"
"v_xor_b32 v45, 8, v42\n\t"
"s_mov_b32 s69, 0\n\t"
"Lfb1_%=:\n\t"
"s_lshr_b32 s71, s69, 2\n\t"
"s_and_b32 s70, s69, 3\n\t"
"s_and_b32 s60, s71, 1\n\t"
"s_lshr_b32 s61, s71, 1\n\t"
"s_mul_i32 s62, s61, 171\n\t"
"s_lshr_b32 s62, s62, 9\n\t"
"s_mul_i32 s63, s62, 3\n\t"
"s_sub_u32 s61, s61, s63\n\t"
"s_and_b32 s63, s62, 3\n\t"
"s_lshr_b32 s62, s62, 2\n\t"
"s_mul_i32 s56, s62, 12288\n\t"
"s_mul_i32 s64, s60, 3072\n\t"
"s_add_u32 s56, s56, s64\n\t"
"s_lshr_b32 s64, s70, 1\n\t"
"s_mul_i32 s64, s64, 1536\n\t"
"s_add_u32 s56, s56, s64\n\t"
"s_and_b32 s64, s70, 1\n\t"
"s_lshl1_add_u32 s64, s63, s64\n\t"
"s_lshl_b32 s65, s61, 3\n\t"
"s_cmp_lt_u32 s61, 2\n\t"
"s_cselect_b32 s74, 1, 0\n\t"
"s_cselect_b32 s65, s65, 0x100\n\t"
"s_add_u32 s56, s56, s65\n\t"
"s_add_u32 s65, s74, 4\n\t"
"s_lshl_b32 s64, s64, s65\n\t"
"s_add_u32 s56, s56, s64\n\t"
"s_mul_i32 s57, s62, 24\n\t"
"s_add_u32 s57, s57, s61\n\t"
"s_mul_i32 s57, s57, 12288\n\t"
"s_mul_i32 s64, s63, 3072\n\t"
"s_add_u32 s57, s57, s64\n\t"
"s_lshl_b32 s64, s60, 9\n\t"
"s_add_u32 s57, s57, s64\n\t"
"s_lshl_b32 s64, s70, 7\n\t"
"s_add_u32 s57, s57, s64\n\t"
"v_mad_u32_u24 v41, v119, s74, v118\n\t"
"s_add_u32 s60, s56, 0\n\t"
"buffer_load_short_d16_hi v32, v41, s[52:55], s60 offen\n\t"
"s_add_u32 s60, s56, 73728\n\t"
"buffer_load_short_d16_hi v33, v41, s[52:55], s60 offen\n\t"
"s_add_u32 s60, s56, 147456\n\t"
"buffer_load_short_d16_hi v34, v41, s[52:55], s60 offen\n\t"
"s_add_u32 s60, s56, 221184\n\t"
"buffer_load_short_d16_hi v35, v41, s[52:55], s60 offen\n\t"
"s_add_u32 s60, s56, 294912\n\t"
"buffer_load_short_d16_hi v36, v41, s[52:55], s60 offen\n\t"
"s_add_u32 s60, s56, 368640\n\t"
"buffer_load_short_d16_hi v37, v41, s[52:55], s60 offen\n\t"
"s_add_u32 s60, s56, 442368\n\t"
"buffer_load_short_d16_hi v38, v41, s[52:55], s60 offen\n\t"
"s_add_u32 s60, s56, 516096\n\t"
"buffer_load_short_d16_hi v39, v41, s[52:55], s60 offen\n\t"
"s_add_u32 s60, s56, 589824\n\t"
"buffer_load_short_d16_hi v40, v41, s[52:55], s60 offen\n\t"
"s_waitcnt vmcnt(6)\n\t"
"v_fma_f32 v48, v34, s41, v32\n\t"
"v_fma_f32 v51, v34, s43, v32\n\t"
"s_waitcnt vmcnt(3)\n\t"
"v_fma_f32 v49, v37, s41, v35\n\t"
"v_fma_f32 v52, v37, s43, v35\n\t"
"s_waitcnt vmcnt(0)\n\t"
"v_fma_f32 v50, v40, s41, v38\n\t"
"v_fma_f32 v53, v40, s43, v38\n\t"
"v_fma_f32 v54, v33, s38, v48\n\t"
"v_fma_f32 v57, -v33, s38, v48\n\t"
"v_fma_f32 v60, v33, s39, v51\n\t"
"v_fma_f32 v63, -v33, s39, v51\n\t"
"v_fma_f32 v55, v36, s38, v49\n\t"
"v_fma_f32 v58, -v36, s38, v49\n\t"
"v_fma_f32 v61, v36, s39, v52\n\t"
"v_fma_f32 v64, -v36, s39, v52\n\t"
"v_fma_f32 v56, v39, s38, v50\n\t"
"v_fma_f32 v59, -v39, s38, v50\n\t"
"v_fma_f32 v62, v39, s39, v53\n\t"
"v_fma_f32 v65, -v39, s39, v53\n\t"
"v_mul_f32 v70, 0x3f638e39, v32\n\t"
"v_mul_f32 v71, 0x3f638e39, v35\n\t"
"v_mul_f32 v72, 0x3f638e39, v38\n\t"
"v_mul_f32 v73, 0xbf54ad4b, v54\n\t"
"v_mul_f32 v74, 0xbf54ad4b, v55\n\t"
"v_mul_f32 v75, 0xbf54ad4b, v56\n\t"
"v_mul_f32 v76, 0xbefc0fc1, v57\n\t"
"v_mul_f32 v77, 0xbefc0fc1, v58\n\t"
"v_mul_f32 v78, 0xbefc0fc1, v59\n\t"
"v_mul_f32 v79, 0xbe7c0fc1, v60\n\t"
"v_mul_f32 v80, 0xbe7c0fc1, v61\n\t"
"v_mul_f32 v81, 0xbe7c0fc1, v62\n\t"
"v_mul_f32 v82, 0x3ea80a81, v63\n\t"
"v_mul_f32 v83, 0x3ea80a81, v64\n\t"
"v_mul_f32 v84, 0x3ea80a81, v65\n\t"
"v_mul_f32 v85, 0x3f638e39, v34\n\t"
"v_mul_f32 v86, 0x3f638e39, v37\n\t"
"v_mul_f32 v87, 0x3f638e39, v40\n\t"
"v_mul_f32 v88, 0x3ea80a81, v32\n\t"
"v_mul_f32 v89, 0x3ea80a81, v35\n\t"
"v_mul_f32 v90, 0x3ea80a81, v38\n\t"
"v_mul_f32 v91, 0xbebd0bd1, v34\n\t"
"v_mul_f32 v92, 0xbebd0bd1, v37\n\t"
"v_mul_f32 v93, 0xbebd0bd1, v40\n\t"
"v_cvt_pk_bf16_f32 v70, v70, v71\n\t"
"v_cvt_pk_bf16_f32 v72, v72, v73\n\t"
"v_cvt_pk_bf16_f32 v74, v74, v75\n\t"
"v_cvt_pk_bf16_f32 v76, v76, v77\n\t"
"v_cvt_pk_bf16_f32 v78, v78, v79\n\t"
"v_cvt_pk_bf16_f32 v80, v80, v81\n\t"
"v_cvt_pk_bf16_f32 v82, v82, v83\n\t"
"v_cvt_pk_bf16_f32 v84, v84, v85\n\t"
"v_cvt_pk_bf16_f32 v86, v86, v87\n\t"
"v_cvt_pk_bf16_f32 v88, v88, v89\n\t"
"v_cvt_pk_bf16_f32 v90, v90, v91\n\t"
"v_cvt_pk_bf16_f32 v92, v92, v93\n\t"
"ds_write_b16 v42, v70 offset:0\n\t"
"ds_write_b16_d16_hi v42, v70 offset:1024\n\t"
"ds_write_b16 v42, v72 offset:2048\n\t"
"ds_write_b16_d16_hi v42, v72 offset:128\n\t"
"ds_write_b16 v42, v74 offset:1152\n\t"
"ds_write_b16_d16_hi v42, v74 offset:2176\n\t"
"ds_write_b16 v45, v76 offset:256\n\t"
"ds_write_b16_d16_hi v45, v76 offset:1280\n\t"
"ds_write_b16 v45, v78 offset:2304\n\t"
"ds_write_b16_d16_hi v42, v78 offset:384\n\t"
"ds_write_b16 v42, v80 offset:1408\n\t"
"ds_write_b16_d16_hi v42, v80 offset:2432\n\t"
"ds_write_b16 v45, v82 offset:512\n\t"
"ds_write_b16_d16_hi v45, v82 offset:1536\n\t"
"ds_write_b16 v45, v84 offset:2560\n\t"
"ds_write_b16_d16_hi v42, v84 offset:640\n\t"
"ds_write_b16 v42, v86 offset:1664\n\t"
"ds_write_b16_d16_hi v42, v86 offset:2688\n\t"
"ds_write_b16 v42, v88 offset:768\n\t"
"ds_write_b16_d16_hi v42, v88 offset:1792\n\t"
"ds_write_b16 v42, v90 offset:2816\n\t"
"ds_write_b16_d16_hi v42, v90 offset:896\n\t"
"ds_write_b16 v42, v92 offset:1920\n\t"
"ds_write_b16_d16_hi v42, v92 offset:2944\n\t"
"ds_read_b128 v[48:51], v43 offset:0\n\t"
"ds_read_b128 v[52:55], v43 offset:1024\n\t"
"ds_read_b128 v[56:59], v43 offset:2048\n\t"
"s_waitcnt lgkmcnt(2)\n\t"
"buffer_store_dwordx4 v[48:51], v44, s[8:11], s57 offen offset:0 sc1\n\t"
"s_waitcnt lgkmcnt(1)\n\t"
"buffer_store_dwordx4 v[52:55], v44, s[8:11], s57 offen offset:1024 sc1\n\t"
"s_waitcnt lgkmcnt(0)\n\t"
"buffer_store_dwordx4 v[56:59], v44, s[8:11], s57 offen offset:2048 sc1\n\t"
"s_waitcnt vmcnt(0) lgkmcnt(0)\n\t"
"s_add_u32 s69, s69, 1\n\t"
"s_cmp_lt_u32 s69, 576\n\t"
"s_cbranch_scc1 Lfb1_%=\n\t"
"ds_read_b128 v[32:35], v255 offset:0\n\t"
"ds_read_b128 v[36:39], v255 offset:3072\n\t"
"ds_read_b128 v[40:43], v255 offset:6144\n\t"
"ds_read_b128 v[44:47], v255 offset:9216\n\t"
"ds_read_b128 v[48:51], v255 offset:12288\n\t"
"ds_read_b128 v[52:55], v255 offset:15360\n\t"
"ds_read_b128 v[56:59], v255 offset:18432\n\t"
"ds_read_b128 v[60:63], v255 offset:21504\n\t"
"ds_read_b128 v[64:67], v255 offset:24576\n\t"
"ds_read_b128 v[68:71], v255 offset:27648\n\t"
"ds_read_b128 v[72:75], v255 offset:30720\n\t"
"ds_read_b128 v[76:79], v255 offset:33792\n\t"
"ds_read_b128 v[80:83], v255 offset:36864\n\t"
"ds_read_b128 v[84:87], v255 offset:39936\n\t"
"ds_read_b128 v[88:91], v255 offset:43008\n\t"
"ds_read_b128 v[92:95], v255 offset:46080\n\t"
"ds_read_b128 v[96:99], v255 offset:49152\n\t"
"ds_read_b128 v[100:103], v255 offset:52224\n\t"
"ds_read_b128 v[104:107], v255 offset:55296\n\t"
"ds_read_b128 v[108:111], v255 offset:58368\n\t"
"ds_read_b128 v[112:115], v255 offset:61440\n\t"
"ds_read_b128 v[116:119], v255 offset:64512\n\t"
"s_waitcnt lgkmcnt(0)\n\t"
"s_branch Lgo_%=\n\t"
"Lpro_%=:\n\t"
"v_mad_u32_u24 v41, v119, s74, v118\n\t"
"s_add_u32 s60, s56, 0\n\t"
"buffer_load_short_d16_hi v32, v41, s[52:55], s60 offen\n\t"
"s_add_u32 s60, s56, 73728\n\t"
"buffer_load_short_d16_hi v33, v41, s[52:55], s60 offen\n\t"
"s_add_u32 s60, s56, 147456\n\t"
"buffer_load_short_d16_hi v34, v41, s[52:55], s60 offen\n\t"
"s_add_u32 s60, s56, 221184\n\t"
"buffer_load_short_d16_hi v35, v41, s[52:55], s60 offen\n\t"
"s_add_u32 s60, s56, 294912\n\t"
"buffer_load_short_d16_hi v36, v41, s[52:55], s60 offen\n\t"
"s_add_u32 s60, s56, 368640\n\t"
"buffer_load_short_d16_hi v37, v41, s[52:55], s60 offen\n\t"
"s_add_u32 s60, s56, 442368\n\t"
"buffer_load_short_d16_hi v38, v41, s[52:55], s60 offen\n\t"
"s_add_u32 s60, s56, 516096\n\t"
"buffer_load_short_d16_hi v39, v41, s[52:55], s60 offen\n\t"
"s_add_u32 s60, s56, 589824\n\t"
"buffer_load_short_d16_hi v40, v41, s[52:55], s60 offen\n\t"
"s_add_u32 s60, s66, 1867776\n\t"
"buffer_load_dword v46, off, s[8:11], s60 sc1\n\t"
"s_mul_i32 s70, s66, 1\n\t"
"s_add_u32 s70, s70, 1900544\n\t"
"s_mov_b32 s60, s70\n\t"
"buffer_load_dword v47, off, s[8:11], s60 sc1\n\t"
"global_load_dwordx4 v[244:247], v252, s[46:47] offset:2048\n\t"
"global_load_dwordx4 v[248:251], v252, s[48:49] offset:0\n\t"
"global_load_dwordx4 v[240:243], v252, s[46:47] offset:0\n\t"
"global_load_dwordx4 v[196:199], v252, s[46:47] offset:1024\n\t"
"global_load_dwordx4 v[220:223], v252, s[48:49] offset:1024\n\t"
"s_cmp_lg_u32 s72, 0\n\t"
"s_cbranch_scc1 Lnl1_%=\n\t"
"v_mul_u32_u24 v116, 1, v252\n\t"
"v_lshrrev_b32 v116, 2, v116\n\t"
"v_mul_u32_u24 v117, 1, v116\n\t"
"s_mov_b32 s60, 1900544\n\t"
"buffer_load_dword v104, v117, s[8:11], s60 offen sc1\n\t"
"s_mov_b32 s60, 1900800\n\t"
"buffer_load_dword v105, v117, s[8:11], s60 offen sc1\n\t"
"s_mov_b32 s60, 1901056\n\t"
"buffer_load_dword v106, v117, s[8:11], s60 offen sc1\n\t"
"s_mov_b32 s60, 1901312\n\t"
"buffer_load_dword v107, v117, s[8:11], s60 offen sc1\n\t"
"s_waitcnt vmcnt(0)\n\t"
"v_add_u32 v104, 0x10000, v104\n\t"
"v_add_u32 v105, 0x10000, v105\n\t"
"v_add_u32 v106, 0x10000, v106\n\t"
"v_add_u32 v107, 0x10000, v107\n\t"
"v_and_b32 v104, 0xffff0000, v104\n\t"
"v_and_b32 v105, 0xffff0000, v105\n\t"
"v_and_b32 v106, 0xffff0000, v106\n\t"
"v_and_b32 v107, 0xffff0000, v107\n\t"
"s_mov_b32 s60, 1835008\n\t"
"buffer_store_dword v104, v116, s[8:11], s60 offen sc1\n\t"
"s_mov_b32 s60, 1835264\n\t"
"buffer_store_dword v105, v116, s[8:11], s60 offen sc1\n\t"
"s_mov_b32 s60, 1835520\n\t"
"buffer_store_dword v106, v116, s[8:11], s60 offen sc1\n\t"
"s_mov_b32 s60, 1835776\n\t"
"buffer_store_dword v107, v116, s[8:11], s60 offen sc1\n\t"
"Lnl1_%=:\n\t"
"s_add_u32 m0, s24, 59904\n\t"
"s_add_u32 s28, s21, 1769472\n\t"
"buffer_load_dwordx4 v28, s[12:15], s28 offen offset:0 nt lds\n\t"
"s_add_u32 m0, s24, 66048\n\t"
"s_add_u32 s28, s21, 1966080\n\t"
"buffer_load_dwordx4 v28, s[12:15], s28 offen offset:0 nt lds\n\t"
"s_add_u32 m0, s24, 72192\n\t"
"s_add_u32 s28, s21, 2162688\n\t"
"buffer_load_dwordx4 v28, s[12:15], s28 offen offset:0 nt lds\n\t"
"s_add_u32 m0, s24, 79872\n\t"
"s_add_u32 s28, s21, 2359296\n\t"
"buffer_load_dwordx4 v28, s[12:15], s28 offen offset:0 nt lds\n\t"
"s_add_u32 m0, s24, 86016\n\t"
"s_add_u32 s28, s21, 2555904\n\t"
"buffer_load_dwordx4 v28, s[12:15], s28 offen offset:0 nt lds\n\t"
"s_add_u32 m0, s24, 92160\n\t"
"s_add_u32 s28, s21, 2752512\n\t"
"buffer_load_dwordx4 v28, s[12:15], s28 offen offset:0 nt lds\n\t"
"s_add_u32 m0, s24, 19968\n\t"
"s_add_u32 s28, s21, 589824\n\t"
"buffer_load_dwordx4 v28, s[12:15], s28 offen offset:0 nt lds\n\t"
"s_add_u32 m0, s24, 26112\n\t"
"s_add_u32 s28, s21, 786432\n\t"
"buffer_load_dwordx4 v28, s[12:15], s28 offen offset:0 nt lds\n\t"
"v_xor_b32 v45, 8, v42\n\t"
"s_waitcnt vmcnt(21)\n\t"
"v_fma_f32 v48, v34, s41, v32\n\t"
"v_fma_f32 v51, v34, s43, v32\n\t"
"s_waitcnt vmcnt(18)\n\t"
"v_fma_f32 v49, v37, s41, v35\n\t"
"v_fma_f32 v52, v37, s43, v35\n\t"
"s_waitcnt vmcnt(15)\n\t"
"v_fma_f32 v50, v40, s41, v38\n\t"
"v_fma_f32 v53, v40, s43, v38\n\t"
"v_fma_f32 v54, v33, s38, v48\n\t"
"v_fma_f32 v57, -v33, s38, v48\n\t"
"v_fma_f32 v60, v33, s39, v51\n\t"
"v_fma_f32 v63, -v33, s39, v51\n\t"
"v_fma_f32 v55, v36, s38, v49\n\t"
"v_fma_f32 v58, -v36, s38, v49\n\t"
"v_fma_f32 v61, v36, s39, v52\n\t"
"v_fma_f32 v64, -v36, s39, v52\n\t"
"v_fma_f32 v56, v39, s38, v50\n\t"
"v_fma_f32 v59, -v39, s38, v50\n\t"
"v_fma_f32 v62, v39, s39, v53\n\t"
"v_fma_f32 v65, -v39, s39, v53\n\t"
"v_mul_f32 v70, 0x3f638e39, v32\n\t"
"v_mul_f32 v71, 0x3f638e39, v35\n\t"
"v_mul_f32 v72, 0x3f638e39, v38\n\t"
"v_mul_f32 v73, 0xbf54ad4b, v54\n\t"
"v_mul_f32 v74, 0xbf54ad4b, v55\n\t"
"v_mul_f32 v75, 0xbf54ad4b, v56\n\t"
"v_mul_f32 v76, 0xbefc0fc1, v57\n\t"
"v_mul_f32 v77, 0xbefc0fc1, v58\n\t"
"v_mul_f32 v78, 0xbefc0fc1, v59\n\t"
"v_mul_f32 v79, 0xbe7c0fc1, v60\n\t"
"v_mul_f32 v80, 0xbe7c0fc1, v61\n\t"
"v_mul_f32 v81, 0xbe7c0fc1, v62\n\t"
"v_mul_f32 v82, 0x3ea80a81, v63\n\t"
"v_mul_f32 v83, 0x3ea80a81, v64\n\t"
"v_mul_f32 v84, 0x3ea80a81, v65\n\t"
"v_mul_f32 v85, 0x3f638e39, v34\n\t"
"v_mul_f32 v86, 0x3f638e39, v37\n\t"
"v_mul_f32 v87, 0x3f638e39, v40\n\t"
"v_mul_f32 v88, 0x3ea80a81, v32\n\t"
"v_mul_f32 v89, 0x3ea80a81, v35\n\t"
"v_mul_f32 v90, 0x3ea80a81, v38\n\t"
"v_mul_f32 v91, 0xbebd0bd1, v34\n\t"
"v_mul_f32 v92, 0xbebd0bd1, v37\n\t"
"v_mul_f32 v93, 0xbebd0bd1, v40\n\t"
"v_cvt_pk_bf16_f32 v70, v70, v71\n\t"
"v_cvt_pk_bf16_f32 v72, v72, v73\n\t"
"v_cvt_pk_bf16_f32 v74, v74, v75\n\t"
"v_cvt_pk_bf16_f32 v76, v76, v77\n\t"
"v_cvt_pk_bf16_f32 v78, v78, v79\n\t"
"v_cvt_pk_bf16_f32 v80, v80, v81\n\t"
"v_cvt_pk_bf16_f32 v82, v82, v83\n\t"
"v_cvt_pk_bf16_f32 v84, v84, v85\n\t"
"v_cvt_pk_bf16_f32 v86, v86, v87\n\t"
"v_cvt_pk_bf16_f32 v88, v88, v89\n\t"
"v_cvt_pk_bf16_f32 v90, v90, v91\n\t"
"v_cvt_pk_bf16_f32 v92, v92, v93\n\t"
"ds_write_b16 v42, v70 offset:0\n\t"
"ds_write_b16_d16_hi v42, v70 offset:1024\n\t"
"ds_write_b16 v42, v72 offset:2048\n\t"
"ds_write_b16_d16_hi v42, v72 offset:128\n\t"
"ds_write_b16 v42, v74 offset:1152\n\t"
"ds_write_b16_d16_hi v42, v74 offset:2176\n\t"
"ds_write_b16 v45, v76 offset:256\n\t"
"ds_write_b16_d16_hi v45, v76 offset:1280\n\t"
"ds_write_b16 v45, v78 offset:2304\n\t"
"ds_write_b16_d16_hi v42, v78 offset:384\n\t"
"ds_write_b16 v42, v80 offset:1408\n\t"
"ds_write_b16_d16_hi v42, v80 offset:2432\n\t"
"ds_write_b16 v45, v82 offset:512\n\t"
"ds_write_b16_d16_hi v45, v82 offset:1536\n\t"
"ds_write_b16 v45, v84 offset:2560\n\t"
"ds_write_b16_d16_hi v42, v84 offset:640\n\t"
"ds_write_b16 v42, v86 offset:1664\n\t"
"ds_write_b16_d16_hi v42, v86 offset:2688\n\t"
"ds_write_b16 v42, v88 offset:768\n\t"
"ds_write_b16_d16_hi v42, v88 offset:1792\n\t"
"ds_write_b16 v42, v90 offset:2816\n\t"
"ds_write_b16_d16_hi v42, v90 offset:896\n\t"
"ds_write_b16 v42, v92 offset:1920\n\t"
"ds_write_b16_d16_hi v42, v92 offset:2944\n\t"
"ds_read_b128 v[48:51], v43 offset:0\n\t"
"ds_read_b128 v[52:55], v43 offset:1024\n\t"
"ds_read_b128 v[56:59], v43 offset:2048\n\t"
"s_waitcnt lgkmcnt(2)\n\t"
"buffer_store_dwordx4 v[48:51], v44, s[8:11], s57 offen offset:0 sc1\n\t"
"s_waitcnt lgkmcnt(1)\n\t"
"buffer_store_dwordx4 v[52:55], v44, s[8:11], s57 offen offset:1024 sc1\n\t"
"s_waitcnt lgkmcnt(0)\n\t"
"buffer_store_dwordx4 v[56:59], v44, s[8:11], s57 offen offset:2048 sc1\n\t"
"s_waitcnt vmcnt(14)\n\t"
"v_and_b32 v254, s40, v251\n\t"
"v_add_u32 v254, s42, v254\n\t"
"s_lshr_b32 s40, s73, 1\n\t"
"s_mul_i32 s40, s40, 768\n\t"
"s_add_u32 s40, s40, s22\n\t"
"s_mov_b32 s60, 0\n\t"
"buffer_load_dwordx4 a[0:3], v29, s[52:55], s60 offen\n\t"
"s_mov_b32 s60, 6144\n\t"
"buffer_load_dwordx4 a[24:27], v29, s[52:55], s60 offen\n\t"
"s_mov_b32 s60, 0\n\t"
"buffer_load_dwordx4 a[48:51], v30, s[52:55], s60 offen\n\t"
"s_mov_b32 s60, 221184\n\t"
"buffer_load_dwordx4 a[8:11], v29, s[52:55], s60 offen\n\t"
"s_mov_b32 s60, 227328\n\t"
"buffer_load_dwordx4 a[32:35], v29, s[52:55], s60 offen\n\t"
"s_mov_b32 s60, 221184\n\t"
"buffer_load_dwordx4 a[56:59], v30, s[52:55], s60 offen\n\t"
"s_mov_b32 s60, 442368\n\t"
"buffer_load_dwordx4 a[16:19], v29, s[52:55], s60 offen\n\t"
"s_mov_b32 s60, 448512\n\t"
"buffer_load_dwordx4 a[40:43], v29, s[52:55], s60 offen\n\t"
"s_mov_b32 s60, 442368\n\t"
"buffer_load_dwordx4 a[64:67], v30, s[52:55], s60 offen\n\t"
"s_mov_b32 s60, 147456\n\t"
"buffer_load_dwordx4 a[72:75], v29, s[52:55], s60 offen\n\t"
"s_mov_b32 s60, 153600\n\t"
"buffer_load_dwordx4 a[96:99], v29, s[52:55], s60 offen\n\t"
"s_mov_b32 s60, 147456\n\t"
"buffer_load_dwordx4 a[120:123], v30, s[52:55], s60 offen\n\t"
"s_waitcnt vmcnt(12)\n\t"
"s_barrier\n\t"
"v_readfirstlane_b32 s67, v46\n\t"
"v_readfirstlane_b32 s71, v47\n\t"
"s_add_u32 s67, s67, 1\n\t"
"s_and_b32 s67, s67, 0xffff\n\t"
"s_add_u32 s71, s71, 0x10000\n\t"
"s_and_b32 s71, s71, 0xffff0000\n\t"
"s_or_b32 s71, s71, s67\n\t"
"s_mov_b32 s68, 3000\n\t"
"s_cmp_lg_u32 s73, 0\n\t"
"s_cbranch_scc1 Lnl2_%=\n\t"
"v_mov_b32 v47, s71\n\t"
"s_mov_b64 exec, 1\n\t"
"buffer_store_dword v47, off, s[8:11], s70 sc1\n\t"
"s_mov_b64 exec, -1\n\t"
"Lnl2_%=:\n\t"
"s_mov_b32 s60, 368640\n\t"
"buffer_load_dwordx4 a[80:83], v29, s[52:55], s60 offen\n\t"
"s_mov_b32 s60, 374784\n\t"
"buffer_load_dwordx4 a[104:107], v29, s[52:55], s60 offen\n\t"
"s_mov_b32 s60, 368640\n\t"
"buffer_load_dwordx4 a[128:131], v30, s[52:55], s60 offen\n\t"
"s_waitcnt vmcnt(14)\n\t"
"ds_write_b128 v43, a[0:3] offset:0\n\t"
"s_waitcnt vmcnt(13)\n\t"
"ds_write_b128 v43, a[24:27] offset:1024\n\t"
"s_waitcnt vmcnt(12)\n\t"
"ds_write_b128 v43, a[48:51] offset:2048\n\t"
"ds_read_b64_tr_b16 a[0:1], v43 offset:0\n\t"
"ds_read_b64_tr_b16 a[2:3], v43 offset:1024\n\t"
"ds_read_b64_tr_b16 a[24:25], v43 offset:8\n\t"
"ds_read_b64_tr_b16 a[26:27], v43 offset:1032\n\t"
"ds_read_b64_tr_b16 a[48:49], v31 offset:2048\n\t"
"ds_read_b64_tr_b16 a[50:51], v31 offset:2560\n\t"
"s_mov_b32 s60, 589824\n\t"
"buffer_load_dwordx4 a[88:91], v29, s[52:55], s60 offen\n\t"
"s_mov_b32 s60, 595968\n\t"
"buffer_load_dwordx4 a[112:115], v29, s[52:55], s60 offen\n\t"
"s_mov_b32 s60, 589824\n\t"
"buffer_load_dwordx4 a[136:139], v30, s[52:55], s60 offen\n\t"
"s_waitcnt vmcnt(14)\n\t"
"ds_write_b128 v43, a[8:11] offset:0\n\t"
"s_waitcnt vmcnt(13)\n\t"
"ds_write_b128 v43, a[32:35] offset:1024\n\t"
"s_waitcnt vmcnt(12)\n\t"
"ds_write_b128 v43, a[56:59] offset:2048\n\t"
"ds_read_b64_tr_b16 a[8:9], v43 offset:0\n\t"
"ds_read_b64_tr_b16 a[10:11], v43 offset:1024\n\t"
"ds_read_b64_tr_b16 a[32:33], v43 offset:8\n\t"
"ds_read_b64_tr_b16 a[34:35], v43 offset:1032\n\t"
"ds_read_b64_tr_b16 a[56:57], v31 offset:2048\n\t"
"ds_read_b64_tr_b16 a[58:59], v31 offset:2560\n\t"
"s_add_u32 m0, s25, 0\n\t"
"s_nop 0\n\t"
"buffer_load_dwordx4 v254, s[12:15], 0 offen offset:0 nt lds\n\t"
"s_add_u32 m0, s26, 0\n\t"
"s_mov_b64 exec, s[36:37]\n\t"
"buffer_load_dwordx4 v254, s[12:15], s27 offen offset:0 nt lds\n\t"
"s_mov_b64 exec, -1\n\t"
"s_add_u32 m0, s24, 32256\n\t"
"s_add_u32 s28, s21, 983040\n\t"
"buffer_load_dwordx4 v247, s[12:15], s28 offen offset:0 nt lds\n\t"
"s_waitcnt vmcnt(14)\n\t"
"ds_write_b128 v43, a[16:19] offset:0\n\t"
"s_waitcnt vmcnt(13)\n\t"
"ds_write_b128 v43, a[40:43] offset:1024\n\t"
"s_waitcnt vmcnt(12)\n\t"
"ds_write_b128 v43, a[64:67] offset:2048\n\t"
"ds_read_b64_tr_b16 a[16:17], v43 offset:0\n\t"
"ds_read_b64_tr_b16 a[18:19], v43 offset:1024\n\t"
"ds_read_b64_tr_b16 a[40:41], v43 offset:8\n\t"
"ds_read_b64_tr_b16 a[42:43], v43 offset:1032\n\t"
"ds_read_b64_tr_b16 a[64:65], v31 offset:2048\n\t"
"ds_read_b64_tr_b16 a[66:67], v31 offset:2560\n\t"
"s_add_u32 m0, s24, 0\n\t"
"s_add_u32 s28, s21, 0\n\t"
"buffer_load_dwordx4 v247, s[12:15], s28 offen offset:0 nt lds\n\t"
"s_add_u32 m0, s24, 6144\n\t"
"s_add_u32 s28, s21, 196608\n\t"
"buffer_load_dwordx4 v247, s[12:15], s28 offen offset:0 nt lds\n\t"
"s_add_u32 m0, s24, 12288\n\t"
"s_add_u32 s28, s21, 393216\n\t"
"buffer_load_dwordx4 v247, s[12:15], s28 offen offset:0 nt lds\n\t"
"s_waitcnt vmcnt(14)\n\t"
"ds_write_b128 v43, a[72:75] offset:0\n\t"
"s_waitcnt vmcnt(13)\n\t"
"ds_write_b128 v43, a[96:99] offset:1024\n\t"
"s_waitcnt vmcnt(12)\n\t"
"ds_write_b128 v43, a[120:123] offset:2048\n\t"
"ds_read_b64_tr_b16 a[72:73], v43 offset:0\n\t"
"ds_read_b64_tr_b16 a[74:75], v43 offset:1024\n\t"
"ds_read_b64_tr_b16 a[96:97], v43 offset:8\n\t"
"ds_read_b64_tr_b16 a[98:99], v43 offset:1032\n\t"
"ds_read_b64_tr_b16 a[120:121], v31 offset:2048\n\t"
"ds_read_b64_tr_b16 a[122:123], v31 offset:2560\n\t"
"s_cmp_lg_u32 s66, 0\n\t"
"s_cbranch_scc1 Lld4_%=\n\t"
"s_cmp_lg_u32 s73, 0\n\t"
"s_cbranch_scc1 Lld2_%=\n\t"
"Lld1_%=:\n\t"
"s_mov_b32 s60, 1900544\n\t"
"buffer_load_dword v108, v117, s[8:11], s60 offen sc1\n\t"
"s_mov_b32 s60, 1900800\n\t"
"buffer_load_dword v109, v117, s[8:11], s60 offen sc1\n\t"
"s_mov_b32 s60, 1901056\n\t"
"buffer_load_dword v110, v117, s[8:11], s60 offen sc1\n\t"
"s_mov_b32 s60, 1901312\n\t"
"buffer_load_dword v111, v117, s[8:11], s60 offen sc1\n\t"
"s_waitcnt vmcnt(0)\n\t"
"v_xor_b32 v112, v108, v104\n\t"
"v_xor_b32 v113, v109, v105\n\t"
"v_xor_b32 v114, v110, v106\n\t"
"v_xor_b32 v115, v111, v107\n\t"
"s_nop 1\n\t"
"v_writelane_b32 v112, 0, 0\n\t"
"s_nop 1\n\t"
"v_or3_b32 v112, v112, v113, v114\n\t"
"v_or_b32 v112, v112, v115\n\t"
"v_cmp_lt_u32 vcc, 0xffff, v112\n\t"
"s_nop 4\n\t"
"s_cmp_eq_u64 vcc, 0\n\t"
"s_cbranch_scc1 Lld3_%=\n\t"
"s_sub_u32 s68, s68, 1\n\t"
"s_cmp_lg_u32 s68, 0\n\t"
"s_cbranch_scc1 Lld1_%=\n\t"
"s_branch Lld2_%=\n\t"
"Lld3_%=:\n\t"
"s_mov_b32 s60, 1867776\n\t"
"buffer_store_dword v108, v116, s[8:11], s60 offen sc1\n\t"
"s_mov_b32 s60, 1868032\n\t"
"buffer_store_dword v109, v116, s[8:11], s60 offen sc1\n\t"
"s_mov_b32 s60, 1868288\n\t"
"buffer_store_dword v110, v116, s[8:11], s60 offen sc1\n\t"
"s_mov_b32 s60, 1868544\n\t"
"buffer_store_dword v111, v116, s[8:11], s60 offen sc1\n\t"
"s_mov_b32 s68, -1\n\t"
"Lld2_%=:\n\t"
"s_barrier\n\t"
"Lld4_%=:\n\t"
"s_add_u32 m0, s24, 39936\n\t"
"s_add_u32 s28, s21, 1179648\n\t"
"buffer_load_dwordx4 v247, s[12:15], s28 offen offset:0 nt lds\n\t"
"s_add_u32 m0, s24, 46080\n\t"
"s_add_u32 s28, s21, 1376256\n\t"
"buffer_load_dwordx4 v247, s[12:15], s28 offen offset:0 nt lds\n\t"
"s_add_u32 m0, s24, 52224\n\t"
"s_add_u32 s28, s21, 1572864\n\t"
"buffer_load_dwordx4 v247, s[12:15], s28 offen offset:0 nt lds\n\t"
"s_waitcnt vmcnt(14)\n\t"
"ds_write_b128 v43, a[80:83] offset:0\n\t"
"s_waitcnt vmcnt(13)\n\t"
"ds_write_b128 v43, a[104:107] offset:1024\n\t"
"s_waitcnt vmcnt(12)\n\t"
"ds_write_b128 v43, a[128:131] offset:2048\n\t"
"ds_read_b64_tr_b16 a[80:81], v43 offset:0\n\t"
"ds_read_b64_tr_b16 a[82:83], v43 offset:1024\n\t"
"ds_read_b64_tr_b16 a[104:105], v43 offset:8\n\t"
"ds_read_b64_tr_b16 a[106:107], v43 offset:1032\n\t"
"ds_read_b64_tr_b16 a[128:129], v31 offset:2048\n\t"
"ds_read_b64_tr_b16 a[130:131], v31 offset:2560\n\t"
"s_waitcnt vmcnt(11)\n\t"
"ds_write_b128 v43, a[88:91] offset:0\n\t"
"s_waitcnt vmcnt(10)\n\t"
"ds_write_b128 v43, a[112:115] offset:1024\n\t"
"s_waitcnt vmcnt(9)\n\t"
"ds_write_b128 v43, a[136:139] offset:2048\n\t"
"ds_read_b64_tr_b16 a[88:89], v43 offset:0\n\t"
"ds_read_b64_tr_b16 a[90:91], v43 offset:1024\n\t"
"ds_read_b64_tr_b16 a[112:113], v43 offset:8\n\t"
"ds_read_b64_tr_b16 a[114:115], v43 offset:1032\n\t"
"ds_read_b64_tr_b16 a[136:137], v31 offset:2048\n\t"
"ds_read_b64_tr_b16 a[138:139], v31 offset:2560\n\t"
"s_waitcnt lgkmcnt(0)\n\t"
"v_add_u32 v240, 59904, v240\n\t"
"v_add_u32 v241, 59904, v241\n\t"
"s_waitcnt vmcnt(3)\n\t"
"s_barrier\n\t"
"ds_read_b64_tr_b16 v[120:121], v240 offset:0\n\t"
"ds_read_b64_tr_b16 v[122:123], v240 offset:1536\n\t"
"ds_read_b64_tr_b16 v[124:125], v241 offset:0\n\t"
"ds_read_b64_tr_b16 v[126:127], v241 offset:1536\n\t"
"ds_read_b64_tr_b16 v[128:129], v240 offset:19968\n\t"
"ds_read_b64_tr_b16 v[130:131], v240 offset:21504\n\t"
"ds_read_b64_tr_b16 v[132:133], v241 offset:19968\n\t"
"ds_read_b64_tr_b16 v[134:135], v241 offset:21504\n\t"
"v_subrev_u32 v240, 59904, v240\n\t"
"v_subrev_u32 v241, 59904, v241\n\t"
"ds_read_b64_tr_b16 v[136:137], v240 offset:19968\n\t"
"ds_read_b64_tr_b16 v[138:139], v240 offset:21504\n\t"
"ds_read_b64_tr_b16 v[140:141], v241 offset:19968\n\t"
"ds_read_b64_tr_b16 v[142:143], v241 offset:21504\n\t"
"ds_read_b64_tr_b16 v[144:145], v240 offset:0\n\t"
"ds_read_b64_tr_b16 v[146:147], v240 offset:1536\n\t"
"ds_read_b64_tr_b16 v[148:149], v241 offset:0\n\t"
"ds_read_b64_tr_b16 v[150:151], v241 offset:1536\n\t"
"s_add_u32 m0, s24, 99840\n\t"
"s_add_u32 s28, s21, 2949120\n\t"
"buffer_load_dwordx4 v247, s[12:15], s28 offen offset:0 nt lds\n\t"
"s_add_u32 m0, s24, 105984\n\t"
"s_add_u32 s28, s21, 3145728\n\t"
"buffer_load_dwordx4 v247, s[12:15], s28 offen offset:0 nt lds\n\t"
"s_add_u32 m0, s24, 112128\n\t"
"s_add_u32 s28, s21, 3342336\n\t"
"buffer_load_dwordx4 v247, s[12:15], s28 offen offset:0 nt lds\n\t"
"s_waitcnt lgkmcnt(14)\n\t"
"v_mfma_f32_16x16x32_bf16 v[120:123], v[120:123], v[196:199], 0\n\t"
"s_waitcnt lgkmcnt(12)\n\t"
"v_mfma_f32_16x16x32_bf16 v[124:127], v[124:127], v[196:199], 0\n\t"
"s_waitcnt lgkmcnt(10)\n\t"
"v_mfma_f32_16x16x32_bf16 v[128:131], v[128:131], v[196:199], 0\n\t"
"s_waitcnt lgkmcnt(8)\n\t"
"v_mfma_f32_16x16x32_bf16 v[132:135], v[132:135], v[196:199], 0\n\t"
"s_waitcnt lgkmcnt(6)\n\t"
"v_mfma_f32_16x16x32_bf16 v[136:139], v[136:139], v[196:199], 0\n\t"
"s_waitcnt lgkmcnt(4)\n\t"
"v_mfma_f32_16x16x32_bf16 v[140:143], v[140:143], v[196:199], 0\n\t"
"s_waitcnt lgkmcnt(2)\n\t"
"v_mfma_f32_16x16x32_bf16 v[144:147], v[144:147], v[196:199], 0\n\t"
"s_waitcnt lgkmcnt(0)\n\t"
"v_mfma_f32_16x16x32_bf16 v[148:151], v[148:151], v[196:199], 0\n\t"
"v_pk_mul_f32 v[120:121], v[120:121], v[242:243] op_sel:[0,1] op_sel_hi:[1,1]\n\t"
"v_pk_mul_f32 v[122:123], v[122:123], v[242:243] op_sel:[0,1] op_sel_hi:[1,1]\n\t"
"v_cvt_pk_bf16_f32 v120, v120, v121\n\t"
"v_cvt_pk_bf16_f32 v121, v122, v123\n\t"
"v_pk_mul_f32 v[124:125], v[124:125], v[242:243] op_sel:[0,1] op_sel_hi:[1,1]\n\t"
"v_pk_mul_f32 v[126:127], v[126:127], v[242:243] op_sel:[0,1] op_sel_hi:[1,1]\n\t"
"v_cvt_pk_bf16_f32 v122, v124, v125\n\t"
"v_cvt_pk_bf16_f32 v123, v126, v127\n\t"
"ds_write_b128 v242, v[120:123] offset:29184\n\t"
"v_pk_mul_f32 v[128:129], v[128:129], v[242:243] op_sel:[0,1] op_sel_hi:[1,1]\n\t"
"v_pk_mul_f32 v[130:131], v[130:131], v[242:243] op_sel:[0,1] op_sel_hi:[1,1]\n\t"
"v_cvt_pk_bf16_f32 v128, v128, v129\n\t"
"v_cvt_pk_bf16_f32 v129, v130, v131\n\t"
"v_pk_mul_f32 v[132:133], v[132:133], v[242:243] op_sel:[0,1] op_sel_hi:[1,1]\n\t"
"v_pk_mul_f32 v[134:135], v[134:135], v[242:243] op_sel:[0,1] op_sel_hi:[1,1]\n\t"
"v_cvt_pk_bf16_f32 v130, v132, v133\n\t"
"v_cvt_pk_bf16_f32 v131, v134, v135\n\t"
"ds_write_b128 v242, v[128:131] offset:30720\n\t"
"v_pk_mul_f32 v[136:137], v[136:137], v[242:243] op_sel:[0,1] op_sel_hi:[1,1]\n\t"
"v_pk_mul_f32 v[138:139], v[138:139], v[242:243] op_sel:[0,1] op_sel_hi:[1,1]\n\t"
"v_cvt_pk_bf16_f32 v136, v136, v137\n\t"
"v_cvt_pk_bf16_f32 v137, v138, v139\n\t"
"v_pk_mul_f32 v[140:141], v[140:141], v[242:243] op_sel:[0,1] op_sel_hi:[1,1]\n\t"
"v_pk_mul_f32 v[142:143], v[142:143], v[242:243] op_sel:[0,1] op_sel_hi:[1,1]\n\t"
"v_cvt_pk_bf16_f32 v138, v140, v141\n\t"
"v_cvt_pk_bf16_f32 v139, v142, v143\n\t"
"ds_write_b128 v242, v[136:139] offset:26112\n\t"
"v_pk_mul_f32 v[144:145], v[144:145], v[242:243] op_sel:[0,1] op_sel_hi:[1,1]\n\t"
"v_pk_mul_f32 v[146:147], v[146:147], v[242:243] op_sel:[0,1] op_sel_hi:[1,1]\n\t"
"v_cvt_pk_bf16_f32 v144, v144, v145\n\t"
"v_cvt_pk_bf16_f32 v145, v146, v147\n\t"
"v_pk_mul_f32 v[148:149], v[148:149], v[242:243] op_sel:[0,1] op_sel_hi:[1,1]\n\t"
"v_pk_mul_f32 v[150:151], v[150:151], v[242:243] op_sel:[0,1] op_sel_hi:[1,1]\n\t"
"v_cvt_pk_bf16_f32 v146, v148, v149\n\t"
"v_cvt_pk_bf16_f32 v147, v150, v151\n\t"
"ds_write_b128 v242, v[144:147] offset:24576\n\t"
"s_waitcnt lgkmcnt(0)\n\t"
"s_barrier\n\t"
"ds_read_b128 a[168:171], v244 offset:26112\n\t"
"ds_read_b128 a[172:175], v244 offset:26624\n\t"
"ds_read_b128 a[176:179], v244 offset:27136\n\t"
"ds_read_b128 a[180:183], v244 offset:35328\n\t"
"ds_read_b128 a[184:187], v244 offset:35840\n\t"
"ds_read_b128 a[188:191], v244 offset:36352\n\t"
"ds_read_b128 a[192:195], v244 offset:25600\n\t"
"s_waitcnt lgkmcnt(4)\n\t"
"v_mfma_f32_16x16x32_bf16 v[14:17], a[0:3], a[168:171], 0\n\t"
"ds_read_b64_tr_b16 v[224:225], v240 offset:512\n\t"
"v_mfma_f32_16x16x32_bf16 v[14:17], a[8:11], a[172:175], v[14:17]\n\t"
"ds_read_b64_tr_b16 v[226:227], v240 offset:2048\n\t"
"v_mfma_f32_16x16x32_bf16 v[14:17], a[16:19], a[176:179], v[14:17]\n\t"
"ds_read_b64_tr_b16 v[228:229], v241 offset:512\n\t"
"v_mfma_f32_16x16x32_bf16 v[46:49], a[24:27], a[168:171], 0\n\t"
"ds_read_b64_tr_b16 v[230:231], v241 offset:2048\n\t"
"v_mfma_f32_16x16x32_bf16 v[46:49], a[32:35], a[172:175], v[46:49]\n\t"
"v_mfma_f32_16x16x32_bf16 v[46:49], a[40:43], a[176:179], v[46:49]\n\t"
"v_mfma_f32_16x16x32_bf16 v[78:81], a[48:51], a[168:171], 0\n\t"
"s_waitcnt lgkmcnt(5)\n\t"
"v_mfma_f32_16x16x32_bf16 v[78:81], a[56:59], a[172:175], v[78:81]\n\t"
"ds_read_b64_tr_b16 v[232:233], v240 offset:20480\n\t"
"v_mfma_f32_16x16x32_bf16 v[78:81], a[64:67], a[176:179], v[78:81]\n\t"
"ds_read_b64_tr_b16 v[234:235], v240 offset:22016\n\t"
"v_mfma_f32_16x16x32_bf16 v[112:115], a[72:75], a[180:183], 0\n\t"
"s_waitcnt lgkmcnt(4)\n\t"
"ds_read_b64_tr_b16 v[236:237], v241 offset:20480\n\t"
"v_mfma_f32_16x16x32_bf16 v[112:115], a[80:83], a[184:187], v[112:115]\n\t"
"s_waitcnt lgkmcnt(3)\n\t"
"ds_read_b64_tr_b16 v[238:239], v241 offset:22016\n\t"
"v_mfma_f32_16x16x32_bf16 v[112:115], a[88:91], a[188:191], v[112:115]\n\t"
"ds_read_b128 a[196:199], v244 offset:34816\n\t"
"v_mfma_f32_16x16x32_bf16 v[224:227], v[224:227], v[196:199], 0\n\t"
"v_mfma_f32_16x16x32_bf16 v[228:231], v[228:231], v[196:199], 0\n\t"
"v_mfma_f32_16x16x32_bf16 v[144:147], a[96:99], a[180:183], 0\n\t"
"v_mfma_f32_16x16x32_bf16 v[144:147], a[104:107], a[184:187], v[144:147]\n\t"
"v_mfma_f32_16x16x32_bf16 v[144:147], a[112:115], a[188:191], v[144:147]\n\t"
"v_mfma_f32_16x16x32_bf16 v[176:179], a[120:123], a[180:183], 0\n\t"
"v_cvt_pk_bf16_f32 v224, v224, v225\n\t"
"v_cvt_pk_bf16_f32 v225, v226, v227\n\t"
"v_mfma_f32_16x16x32_bf16 v[176:179], a[128:131], a[184:187], v[176:179]\n\t"
"ds_read_b128 a[176:179], v244 offset:25088\n\t"
"v_cvt_pk_bf16_f32 v226, v228, v229\n\t"
"v_mfma_f32_16x16x32_bf16 v[176:179], a[136:139], a[188:191], v[176:179]\n\t"
"v_cvt_pk_bf16_f32 v227, v230, v231\n\t"
"v_mfma_f32_16x16x32_bf16 v[10:13], a[0:3], a[192:195], 0\n\t"
"s_waitcnt lgkmcnt(4)\n\t"
"v_mfma_f32_16x16x32_bf16 v[10:13], a[8:11], a[168:171], v[10:13]\n\t"
"s_waitcnt lgkmcnt(2)\n\t"
"v_mfma_f32_16x16x32_bf16 v[10:13], a[16:19], a[172:175], v[10:13]\n\t"
"ds_write_b128 v242, v[224:227] offset:0\n\t"
"v_mfma_f32_16x16x32_bf16 v[232:235], v[232:235], v[196:199], 0\n\t"
"v_mfma_f32_16x16x32_bf16 v[236:239], v[236:239], v[196:199], 0\n\t"
"s_waitcnt vmcnt(3)\n\t"
"s_barrier\n\t"
"v_mfma_f32_16x16x32_bf16 v[42:45], a[24:27], a[192:195], 0\n\t"
"ds_read_b64_tr_b16 v[228:229], v240 offset:39936\n\t"
"v_mfma_f32_16x16x32_bf16 v[42:45], a[32:35], a[168:171], v[42:45]\n\t"
"ds_read_b64_tr_b16 v[230:231], v240 offset:41472\n\t"
"v_mfma_f32_16x16x32_bf16 v[42:45], a[40:43], a[172:175], v[42:45]\n\t"
"ds_read_b64_tr_b16 v[224:225], v241 offset:39936\n\t"
"v_mfma_f32_16x16x32_bf16 v[74:77], a[48:51], a[192:195], 0\n\t"
"s_waitcnt lgkmcnt(5)\n\t"
"ds_read_b64_tr_b16 v[226:227], v241 offset:41472\n\t"
"v_cvt_pk_bf16_f32 v232, v232, v233\n\t"
"v_mfma_f32_16x16x32_bf16 v[74:77], a[56:59], a[168:171], v[74:77]\n\t"
"ds_read_b128 a[188:191], v244 offset:34304\n\t"
"v_cvt_pk_bf16_f32 v233, v234, v235\n\t"
"v_mfma_f32_16x16x32_bf16 v[74:77], a[64:67], a[172:175], v[74:77]\n\t"
"v_cvt_pk_bf16_f32 v234, v236, v237\n\t"
"v_cvt_pk_bf16_f32 v235, v238, v239\n\t"
"v_mfma_f32_16x16x32_bf16 v[108:111], a[72:75], a[196:199], 0\n\t"
"s_waitcnt lgkmcnt(3)\n\t"
"v_mfma_f32_16x16x32_bf16 v[108:111], a[80:83], a[180:183], v[108:111]\n\t"
"ds_read_b64_tr_b16 v[236:237], v240 offset:40448\n\t"
"v_mfma_f32_16x16x32_bf16 v[108:111], a[88:91], a[184:187], v[108:111]\n\t"
"s_waitcnt lgkmcnt(2)\n\t"
"ds_write_b128 v242, v[232:235] offset:1536\n\t"
"v_mfma_f32_16x16x32_bf16 v[228:231], v[228:231], v[196:199], 0\n\t"
"v_mfma_f32_16x16x32_bf16 v[224:227], v[224:227], v[196:199], 0\n\t"
"ds_read_b64_tr_b16 v[238:239], v240 offset:41984\n\t"
"v_mfma_f32_16x16x32_bf16 v[140:143], a[96:99], a[196:199], 0\n\t"
"ds_read_b64_tr_b16 v[232:233], v241 offset:40448\n\t"
"v_mfma_f32_16x16x32_bf16 v[140:143], a[104:107], a[180:183], v[140:143]\n\t"
"ds_read_b64_tr_b16 v[234:235], v241 offset:41984\n\t"
"v_mfma_f32_16x16x32_bf16 v[140:143], a[112:115], a[184:187], v[140:143]\n\t"
"v_pk_mul_f32 v[228:229], v[228:229], v[242:243] op_sel:[0,1] op_sel_hi:[1,1]\n\t"
"v_pk_mul_f32 v[230:231], v[230:231], v[242:243] op_sel:[0,1] op_sel_hi:[1,1]\n\t"
"v_mfma_f32_16x16x32_bf16 v[172:175], a[120:123], a[196:199], 0\n\t"
"v_cvt_pk_bf16_f32 v228, v228, v229\n\t"
"v_cvt_pk_bf16_f32 v229, v230, v231\n\t"
"v_mfma_f32_16x16x32_bf16 v[172:175], a[128:131], a[180:183], v[172:175]\n\t"
"v_mfma_f32_16x16x32_bf16 v[172:175], a[136:139], a[184:187], v[172:175]\n\t"
"v_pk_mul_f32 v[224:225], v[224:225], v[242:243] op_sel:[0,1] op_sel_hi:[1,1]\n\t"
"v_pk_mul_f32 v[226:227], v[226:227], v[242:243] op_sel:[0,1] op_sel_hi:[1,1]\n\t"
"v_mfma_f32_16x16x32_bf16 v[6:9], a[0:3], a[176:179], 0\n\t"
"v_cvt_pk_bf16_f32 v230, v224, v225\n\t"
"v_cvt_pk_bf16_f32 v231, v226, v227\n\t"
"v_mfma_f32_16x16x32_bf16 v[6:9], a[8:11], a[192:195], v[6:9]\n\t"
"v_add_u32 v240, 59904, v240\n\t"
"v_add_u32 v241, 59904, v241\n\t"
"v_mfma_f32_16x16x32_bf16 v[6:9], a[16:19], a[168:171], v[6:9]\n\t"
"ds_write_b128 v242, v[228:231] offset:27648\n\t"
"v_mfma_f32_16x16x32_bf16 v[38:41], a[24:27], a[176:179], 0\n\t"
"s_waitcnt lgkmcnt(3)\n\t"
"v_mfma_f32_16x16x32_bf16 v[38:41], a[32:35], a[192:195], v[38:41]\n\t"
"s_waitcnt lgkmcnt(1)\n\t"
"ds_read_b64_tr_b16 v[224:225], v240 offset:512\n\t"
"v_mfma_f32_16x16x32_bf16 v[38:41], a[40:43], a[168:171], v[38:41]\n\t"
"ds_read_b64_tr_b16 v[226:227], v240 offset:2048\n\t"
"v_mfma_f32_16x16x32_bf16 v[236:239], v[236:239], v[196:199], 0\n\t"
"ds_read_b64_tr_b16 v[228:229], v241 offset:512\n\t"
"v_mfma_f32_16x16x32_bf16 v[232:235], v[232:235], v[196:199], 0\n\t"
"ds_read_b64_tr_b16 v[230:231], v241 offset:2048\n\t"
"v_mfma_f32_16x16x32_bf16 v[70:73], a[48:51], a[176:179], 0\n\t"
"v_mfma_f32_16x16x32_bf16 v[70:73], a[56:59], a[192:195], v[70:73]\n\t"
"v_mfma_f32_16x16x32_bf16 v[70:73], a[64:67], a[168:171], v[70:73]\n\t"
"v_mfma_f32_16x16x32_bf16 v[104:107], a[72:75], a[188:191], 0\n\t"
"v_cvt_pk_bf16_f32 v236, v236, v237\n\t"
"v_cvt_pk_bf16_f32 v237, v238, v239\n\t"
"v_mfma_f32_16x16x32_bf16 v[104:107], a[80:83], a[196:199], v[104:107]\n\t"
"v_cvt_pk_bf16_f32 v238, v232, v233\n\t"
"v_mfma_f32_16x16x32_bf16 v[104:107], a[88:91], a[180:183], v[104:107]\n\t"
"s_waitcnt lgkmcnt(4)\n\t"
"s_barrier\n\t"
"v_mfma_f32_16x16x32_bf16 v[136:139], a[96:99], a[188:191], 0\n\t"
"ds_read_b128 a[168:171], v244 offset:24576\n\t"
"v_mfma_f32_16x16x32_bf16 v[136:139], a[104:107], a[196:199], v[136:139]\n\t"
"ds_read_b128 a[172:175], v244 offset:33792\n\t"
"v_cvt_pk_bf16_f32 v239, v234, v235\n\t"
"v_mfma_f32_16x16x32_bf16 v[136:139], a[112:115], a[180:183], v[136:139]\n\t"
"ds_write_b128 v242, v[236:239] offset:3072\n\t"
"v_mfma_f32_16x16x32_bf16 v[168:171], a[120:123], a[188:191], 0\n\t"
"s_waitcnt lgkmcnt(5)\n\t"
"v_mfma_f32_16x16x32_bf16 v[168:171], a[128:131], a[196:199], v[168:171]\n\t"
"s_waitcnt lgkmcnt(3)\n\t"
"ds_read_b128 a[184:187], v244 offset:26624\n\t"
"v_mfma_f32_16x16x32_bf16 v[168:171], a[136:139], a[180:183], v[168:171]\n\t"
"s_waitcnt lgkmcnt(3)\n\t"
"ds_read_b128 a[200:203], v244 offset:27136\n\t"
"v_mfma_f32_16x16x32_bf16 v[224:227], v[224:227], v[196:199], 0\n\t"
"ds_read_b128 a[204:207], v244 offset:27648\n\t"
"v_mfma_f32_16x16x32_bf16 v[228:231], v[228:231], v[196:199], 0\n\t"
"v_mfma_f32_16x16x32_bf16 v[2:5], a[0:3], a[168:171], 0\n\t"
"v_mfma_f32_16x16x32_bf16 v[2:5], a[8:11], a[176:179], v[2:5]\n\t"
"v_mfma_f32_16x16x32_bf16 v[2:5], a[16:19], a[192:195], v[2:5]\n\t"
"s_waitcnt vmcnt(0)\n\t"
"s_barrier\n\t"
"s_add_u32 s60, s66, 1867776\n\t"
"buffer_load_dword v98, off, s[8:11], s60 sc1\n\t"
"v_mfma_f32_16x16x32_bf16 v[34:37], a[24:27], a[168:171], 0\n\t"
"ds_read_b64_tr_b16 v[232:233], v240 offset:39936\n\t"
"v_mfma_f32_16x16x32_bf16 v[34:37], a[32:35], a[176:179], v[34:37]\n\t"
"ds_read_b64_tr_b16 v[234:235], v240 offset:41472\n\t"
"v_cvt_pk_bf16_f32 v224, v224, v225\n\t"
"v_mfma_f32_16x16x32_bf16 v[34:37], a[40:43], a[192:195], v[34:37]\n\t"
"ds_read_b64_tr_b16 v[236:237], v241 offset:39936\n\t"
"v_cvt_pk_bf16_f32 v225, v226, v227\n\t"
"v_mfma_f32_16x16x32_bf16 v[66:69], a[48:51], a[168:171], 0\n\t"
"s_waitcnt lgkmcnt(7)\n\t"
"ds_read_b64_tr_b16 v[238:239], v241 offset:41472\n\t"
"v_cvt_pk_bf16_f32 v226, v228, v229\n\t"
"v_mfma_f32_16x16x32_bf16 v[66:69], a[56:59], a[176:179], v[66:69]\n\t"
"ds_read_b128 a[168:171], v244 offset:35840\n\t"
"v_cvt_pk_bf16_f32 v227, v230, v231\n\t"
"v_mfma_f32_16x16x32_bf16 v[66:69], a[64:67], a[192:195], v[66:69]\n\t"
"ds_write_b128 v242, v[224:227] offset:4608\n\t"
"v_mfma_f32_16x16x32_bf16 v[100:103], a[72:75], a[172:175], 0\n\t"
"s_waitcnt lgkmcnt(4)\n\t"
"v_mfma_f32_16x16x32_bf16 v[100:103], a[80:83], a[188:191], v[100:103]\n\t"
"ds_read_b128 a[176:179], v244 offset:36352\n\t"
"v_mfma_f32_16x16x32_bf16 v[100:103], a[88:91], a[196:199], v[100:103]\n\t"
"s_waitcnt lgkmcnt(3)\n\t"
"ds_read_b128 a[180:183], v244 offset:36864\n\t"
"v_mfma_f32_16x16x32_bf16 v[232:235], v[232:235], v[196:199], 0\n\t"
"ds_read_b64_tr_b16 v[228:229], v240 offset:20480\n\t"
"v_mfma_f32_16x16x32_bf16 v[236:239], v[236:239], v[196:199], 0\n\t"
"ds_read_b64_tr_b16 v[230:231], v240 offset:22016\n\t"
"v_mfma_f32_16x16x32_bf16 v[132:135], a[96:99], a[172:175], 0\n\t"
"ds_read_b64_tr_b16 v[224:225], v241 offset:20480\n\t"
"v_mfma_f32_16x16x32_bf16 v[132:135], a[104:107], a[188:191], v[132:135]\n\t"
"ds_read_b64_tr_b16 v[226:227], v241 offset:22016\n\t"
"v_mfma_f32_16x16x32_bf16 v[132:135], a[112:115], a[196:199], v[132:135]\n\t"
"v_pk_mul_f32 v[232:233], v[232:233], v[242:243] op_sel:[0,1] op_sel_hi:[1,1]\n\t"
"v_pk_mul_f32 v[234:235], v[234:235], v[242:243] op_sel:[0,1] op_sel_hi:[1,1]\n\t"
"v_mfma_f32_16x16x32_bf16 v[164:167], a[120:123], a[172:175], 0\n\t"
"v_cvt_pk_bf16_f32 v232, v232, v233\n\t"
"v_cvt_pk_bf16_f32 v233, v234, v235\n\t"
"v_mfma_f32_16x16x32_bf16 v[164:167], a[128:131], a[188:191], v[164:167]\n\t"
"v_mfma_f32_16x16x32_bf16 v[164:167], a[136:139], a[196:199], v[164:167]\n\t"
"v_pk_mul_f32 v[236:237], v[236:237], v[242:243] op_sel:[0,1] op_sel_hi:[1,1]\n\t"
"v_pk_mul_f32 v[238:239], v[238:239], v[242:243] op_sel:[0,1] op_sel_hi:[1,1]\n\t"
"v_mfma_f32_16x16x32_bf16 v[18:21], a[0:3], a[184:187], 0\n\t"
"v_cvt_pk_bf16_f32 v234, v236, v237\n\t"
"v_cvt_pk_bf16_f32 v235, v238, v239\n\t"
"v_mfma_f32_16x16x32_bf16 v[18:21], a[8:11], a[200:203], v[18:21]\n\t"
"ds_read_b64_tr_b16 v[236:237], v240 offset:40448\n\t"
"v_mfma_f32_16x16x32_bf16 v[18:21], a[16:19], a[204:207], v[18:21]\n\t"
"ds_write_b128 v242, v[232:235] offset:32256\n\t"
"v_mfma_f32_16x16x32_bf16 v[50:53], a[24:27], a[184:187], 0\n\t"
"s_waitcnt lgkmcnt(4)\n\t"
"v_mfma_f32_16x16x32_bf16 v[50:53], a[32:35], a[200:203], v[50:53]\n\t"
"s_waitcnt lgkmcnt(2)\n\t"
"ds_read_b64_tr_b16 v[238:239], v240 offset:41984\n\t"
"v_mfma_f32_16x16x32_bf16 v[50:53], a[40:43], a[204:207], v[50:53]\n\t"
"ds_read_b64_tr_b16 v[232:233], v241 offset:40448\n\t"
"v_mfma_f32_16x16x32_bf16 v[228:231], v[228:231], v[196:199], 0\n\t"
"ds_read_b64_tr_b16 v[234:235], v241 offset:41984\n\t"
"v_mfma_f32_16x16x32_bf16 v[224:227], v[224:227], v[196:199], 0\n\t"
"v_mfma_f32_16x16x32_bf16 v[82:85], a[48:51], a[184:187], 0\n\t"
"v_mfma_f32_16x16x32_bf16 v[82:85], a[56:59], a[200:203], v[82:85]\n\t"
"v_mfma_f32_16x16x32_bf16 v[82:85], a[64:67], a[204:207], v[82:85]\n\t"
"v_mfma_f32_16x16x32_bf16 v[116:119], a[72:75], a[168:171], 0\n\t"
"v_cvt_pk_bf16_f32 v228, v228, v229\n\t"
"v_cvt_pk_bf16_f32 v229, v230, v231\n\t"
"v_mfma_f32_16x16x32_bf16 v[116:119], a[80:83], a[176:179], v[116:119]\n\t"
"v_cvt_pk_bf16_f32 v230, v224, v225\n\t"
"v_mfma_f32_16x16x32_bf16 v[116:119], a[88:91], a[180:183], v[116:119]\n\t"
"s_waitcnt lgkmcnt(3)\n\t"
"s_barrier\n\t"
"v_mfma_f32_16x16x32_bf16 v[148:151], a[96:99], a[168:171], 0\n\t"
"s_waitcnt lgkmcnt(2)\n\t"
"ds_read_b128 a[188:191], v244 offset:28160\n\t"
"v_mfma_f32_16x16x32_bf16 v[148:151], a[104:107], a[176:179], v[148:151]\n\t"
"s_waitcnt lgkmcnt(1)\n\t"
"ds_read_b128 v[204:207], v244 offset:37376\n\t"
"v_cvt_pk_bf16_f32 v231, v226, v227\n\t"
"v_mfma_f32_16x16x32_bf16 v[148:151], a[112:115], a[180:183], v[148:151]\n\t"
"ds_write_b128 v242, v[228:231] offset:6144\n\t"
"v_mfma_f32_16x16x32_bf16 v[236:239], v[236:239], v[196:199], 0\n\t"
"v_mfma_f32_16x16x32_bf16 v[232:235], v[232:235], v[196:199], 0\n\t"
"ds_read_b128 a[192:195], v244 offset:28672\n\t"
"v_mfma_f32_16x16x32_bf16 v[180:183], a[120:123], a[168:171], 0\n\t"
"s_waitcnt lgkmcnt(3)\n\t"
"v_mfma_f32_16x16x32_bf16 v[180:183], a[128:131], a[176:179], v[180:183]\n\t"
"v_mfma_f32_16x16x32_bf16 v[180:183], a[136:139], a[180:183], v[180:183]\n\t"
"v_mfma_f32_16x16x32_bf16 v[22:25], a[0:3], a[200:203], 0\n\t"
"v_cvt_pk_bf16_f32 v236, v236, v237\n\t"
"v_cvt_pk_bf16_f32 v237, v238, v239\n\t"
"v_mfma_f32_16x16x32_bf16 v[22:25], a[8:11], a[204:207], v[22:25]\n\t"
"v_cvt_pk_bf16_f32 v238, v232, v233\n\t"
"v_cvt_pk_bf16_f32 v239, v234, v235\n\t"
"v_mfma_f32_16x16x32_bf16 v[22:25], a[16:19], a[188:191], v[22:25]\n\t"
"ds_write_b128 v242, v[236:239] offset:7680\n\t"
"v_mfma_f32_16x16x32_bf16 v[54:57], a[24:27], a[200:203], 0\n\t"
"v_mfma_f32_16x16x32_bf16 v[54:57], a[32:35], a[204:207], v[54:57]\n\t"
"ds_read_b128 v[196:199], v244 offset:37888\n\t"
"v_mfma_f32_16x16x32_bf16 v[54:57], a[40:43], a[188:191], v[54:57]\n\t"
"v_mfma_f32_16x16x32_bf16 v[86:89], a[48:51], a[200:203], 0\n\t"
"s_waitcnt lgkmcnt(4)\n\t"
"v_mfma_f32_16x16x32_bf16 v[86:89], a[56:59], a[204:207], v[86:89]\n\t"
"v_mfma_f32_16x16x32_bf16 v[86:89], a[64:67], a[188:191], v[86:89]\n\t"
"v_mfma_f32_16x16x32_bf16 v[120:123], a[72:75], a[176:179], 0\n\t"
"s_mov_b32 s20, 516096\n\t"
"v_mfma_f32_16x16x32_bf16 v[120:123], a[80:83], a[180:183], v[120:123]\n\t"
"v_mfma_f32_16x16x32_bf16 v[120:123], a[88:91], v[204:207], v[120:123]\n\t"
"v_mfma_f32_16x16x32_bf16 v[152:155], a[96:99], a[176:179], 0\n\t"
"ds_read_b128 a[196:199], v244 offset:29184\n\t"
"v_mfma_f32_16x16x32_bf16 v[152:155], a[104:107], a[180:183], v[152:155]\n\t"
"v_mfma_f32_16x16x32_bf16 v[152:155], a[112:115], v[204:207], v[152:155]\n\t"
"s_waitcnt lgkmcnt(2)\n\t"
"s_waitcnt vmcnt(0)\n\t"
"s_cmp_lg_u32 s73, 0\n\t"
"s_cbranch_scc1 Lgo_%=\n\t"
"s_cmp_lg_u32 s66, 0\n\t"
"s_cbranch_scc1 Lchk_%=\n\t"
"s_cmp_eq_u32 s68, -1\n\t"
"s_cbranch_scc1 Lgo_%=\n\t"
"s_branch Lfb_%=\n\t"
"Lchk_%=:\n\t"
"s_waitcnt vmcnt(0)\n\t"
"v_readfirstlane_b32 s60, v98\n\t"
"s_and_b32 s60, s60, 0xffff\n\t"
"s_cmp_eq_u32 s60, s67\n\t"
"s_cbranch_scc1 Lgo_%=\n\t"
"s_sub_u32 s68, s68, 1\n\t"
"s_cmp_eq_u32 s68, 0\n\t"
"s_cbranch_scc1 Lfb_%=\n\t"
"s_add_u32 s60, s66, 1835008\n\t"
"buffer_load_dword v99, off, s[8:11], s60 sc1\n\t"
"s_add_u32 s60, s66, 1867776\n\t"
"buffer_load_dword v98, off, s[8:11], s60 sc1\n\t"
"s_waitcnt vmcnt(1)\n\t"
"v_readfirstlane_b32 s60, v99\n\t"
"s_and_b32 s60, s60, 0xffff0000\n\t"
"s_or_b32 s60, s60, s67\n\t"
"s_cmp_eq_u32 s60, s71\n\t"
"s_cbranch_scc1 Lchk_%=\n\t"
"s_mov_b32 s71, s60\n\t"
"v_mov_b32 v99, s71\n\t"
"s_mov_b64 exec, 1\n\t"
"buffer_store_dword v99, off, s[8:11], s70 sc1\n\t"
"s_mov_b64 exec, -1\n\t"
"s_branch Lchk_%=\n\t"
"Lgo_%=:\n\t"
"s_barrier\n\t"
"s_mov_b32 s52, 0x3fd80000\n\t"
"s_mov_b32 s53, s52\n\t"
"s_mov_b32 s54, 0x3e97b426\n\t"
"s_mov_b32 s55, s54\n\t"
"s_mov_b32 s56, 0x3f400000\n\t"
"s_mov_b32 s57, s56\n\t"
"s_mov_b32 s58, 0x3f2aaaab\n\t"
"s_mov_b32 s59, s58\n\t"
"s_mov_b32 s60, 0x3ee38e39\n\t"
"s_mov_b32 s61, s60\n\t"
"s_mov_b32 s62, 0x40100000\n\t"
"s_mov_b32 s63, s62\n\t"
"s_mov_b32 s64, 0x3faaaaab\n\t"
"s_mov_b32 s65, s64\n\t"
"s_mov_b32 s66, 0x3f17b426\n\t"
"s_mov_b32 s67, s66\n\t"
"v_mfma_f32_16x16x32_bf16 v[184:187], a[120:123], a[176:179], 0\n\t"
"v_mfma_f32_16x16x32_bf16 v[184:187], a[128:131], a[180:183], v[184:187]\n\t"
"buffer_load_dwordx4 v[236:239], v252, s[8:11], s20 offen offset:0 sc1\n\t"
"v_mfma_f32_16x16x32_bf16 v[184:187], a[136:139], v[204:207], v[184:187]\n\t"
"v_mfma_f32_16x16x32_bf16 v[26:29], a[0:3], a[204:207], 0\n\t"
"buffer_load_dwordx4 a[144:147], v252, s[8:11], s20 offen offset:1024 sc1\n\t"
"v_mfma_f32_16x16x32_bf16 v[26:29], a[8:11], a[188:191], v[26:29]\n\t"
"v_mfma_f32_16x16x32_bf16 v[26:29], a[16:19], a[192:195], v[26:29]\n\t"
"v_mfma_f32_16x16x32_bf16 v[58:61], a[24:27], a[204:207], 0\n\t"
"buffer_load_dwordx4 a[152:155], v252, s[8:11], s20 offen offset:2048 sc1\n\t"
"v_mfma_f32_16x16x32_bf16 v[58:61], a[32:35], a[188:191], v[58:61]\n\t"
"s_mov_b32 s20, 528384\n\t"
"ds_read_b128 v[212:215], v244 offset:38400\n\t"
"v_mfma_f32_16x16x32_bf16 v[58:61], a[40:43], a[192:195], v[58:61]\n\t"
"v_mfma_f32_16x16x32_bf16 v[90:93], a[48:51], a[204:207], 0\n\t"
"buffer_load_dwordx4 a[160:163], v252, s[8:11], s20 offen offset:0 sc1\n\t"
"v_mfma_f32_16x16x32_bf16 v[90:93], a[56:59], a[188:191], v[90:93]\n\t"
"s_waitcnt lgkmcnt(2)\n\t"
"v_mfma_f32_16x16x32_bf16 v[90:93], a[64:67], a[192:195], v[90:93]\n\t"
"v_mfma_f32_16x16x32_bf16 v[124:127], a[72:75], a[180:183], 0\n\t"
"buffer_load_dwordx4 v[200:203], v252, s[8:11], s20 offen offset:1024 sc1\n\t"
"v_mfma_f32_16x16x32_bf16 v[124:127], a[80:83], v[204:207], v[124:127]\n\t"
"v_mfma_f32_16x16x32_bf16 v[124:127], a[88:91], v[196:199], v[124:127]\n\t"
"v_mfma_f32_16x16x32_bf16 v[156:159], a[96:99], a[180:183], 0\n\t"
"buffer_load_dwordx4 v[208:211], v252, s[8:11], s20 offen offset:2048 sc1\n\t"
"v_mfma_f32_16x16x32_bf16 v[156:159], a[104:107], v[204:207], v[156:159]\n\t"
"s_mov_b32 s20, 540672\n\t"
"v_mfma_f32_16x16x32_bf16 v[156:159], a[112:115], v[196:199], v[156:159]\n\t"
"v_mfma_f32_16x16x32_bf16 v[188:191], a[120:123], a[180:183], 0\n\t"
"buffer_load_dwordx4 v[216:219], v252, s[8:11], s20 offen offset:0 sc1\n\t"
"v_mfma_f32_16x16x32_bf16 v[188:191], a[128:131], v[204:207], v[188:191]\n\t"
"s_waitcnt lgkmcnt(1)\n\t"
"ds_read_b128 a[168:171], v244 offset:0\n\t"
"v_mfma_f32_16x16x32_bf16 v[188:191], a[136:139], v[196:199], v[188:191]\n\t"
"ds_read_b128 a[172:175], v244 offset:512\n\t"
"v_mfma_f32_16x16x32_bf16 v[30:33], a[0:3], a[188:191], 0\n\t"
"buffer_load_dwordx4 a[0:3], v252, s[8:11], s20 offen offset:1024 sc1\n\t"
"v_mfma_f32_16x16x32_bf16 v[30:33], a[8:11], a[192:195], v[30:33]\n\t"
"ds_read_b128 a[176:179], v244 offset:1024\n\t"
"v_mfma_f32_16x16x32_bf16 v[30:33], a[16:19], a[196:199], v[30:33]\n\t"
"ds_read_b128 a[180:183], v244 offset:1536\n\t"
"v_mfma_f32_16x16x32_bf16 v[62:65], a[24:27], a[188:191], 0\n\t"
"buffer_load_dwordx4 a[8:11], v252, s[8:11], s20 offen offset:2048 sc1\n\t"
"v_mfma_f32_16x16x32_bf16 v[62:65], a[32:35], a[192:195], v[62:65]\n\t"
"s_mov_b32 s20, 552960\n\t"
"ds_read_b128 a[184:187], v244 offset:2048\n\t"
"v_mfma_f32_16x16x32_bf16 v[62:65], a[40:43], a[196:199], v[62:65]\n\t"
"v_mfma_f32_16x16x32_bf16 v[94:97], a[48:51], a[188:191], 0\n\t"
"buffer_load_dwordx4 a[16:19], v252, s[8:11], s20 offen offset:0 sc1\n\t"
"v_mfma_f32_16x16x32_bf16 v[94:97], a[56:59], a[192:195], v[94:97]\n\t"
"s_waitcnt lgkmcnt(5)\n\t"
"ds_read_b128 a[188:191], v244 offset:2560\n\t"
"v_mfma_f32_16x16x32_bf16 v[94:97], a[64:67], a[196:199], v[94:97]\n\t"
"v_mfma_f32_16x16x32_bf16 v[128:131], a[72:75], v[204:207], 0\n\t"
"buffer_load_dwordx4 a[24:27], v252, s[8:11], s20 offen offset:1024 sc1\n\t"
"v_mfma_f32_16x16x32_bf16 v[128:131], a[80:83], v[196:199], v[128:131]\n\t"
"ds_read_b128 a[192:195], v244 offset:3072\n\t"
"v_mfma_f32_16x16x32_bf16 v[128:131], a[88:91], v[212:215], v[128:131]\n\t"
"v_mfma_f32_16x16x32_bf16 v[160:163], a[96:99], v[204:207], 0\n\t"
"buffer_load_dwordx4 a[32:35], v252, s[8:11], s20 offen offset:2048 sc1\n\t"
"v_mfma_f32_16x16x32_bf16 v[160:163], a[104:107], v[196:199], v[160:163]\n\t"
"s_mov_b32 s20, 565248\n\t"
"ds_read_b128 a[196:199], v244 offset:3584\n\t"
"v_mfma_f32_16x16x32_bf16 v[160:163], a[112:115], v[212:215], v[160:163]\n\t"
"v_mfma_f32_16x16x32_bf16 v[192:195], a[120:123], v[204:207], 0\n\t"
"buffer_load_dwordx4 a[40:43], v252, s[8:11], s20 offen offset:0 sc1\n\t"
"v_mfma_f32_16x16x32_bf16 v[192:195], a[128:131], v[196:199], v[192:195]\n\t"
"s_waitcnt vmcnt(10) lgkmcnt(5)\n\t"
"ds_read_b128 a[200:203], v244 offset:4096\n\t"
"v_mfma_f32_16x16x32_bf16 v[192:195], a[136:139], v[212:215], v[192:195]\n\t"
"Lst1_%=:\n\t"
"v_mfma_f32_16x16x32_bf16 v[2:5], v[236:239], a[168:171], v[2:5]\n\t"
"buffer_load_dwordx4 a[48:51], v252, s[8:11], s20 offen offset:1024 sc1\n\t"
"v_mfma_f32_16x16x32_bf16 v[2:5], a[144:147], a[172:175], v[2:5]\n\t"
"s_waitcnt lgkmcnt(5)\n\t"
"ds_read_b128 a[204:207], v244 offset:4608\n\t"
"v_mfma_f32_16x16x32_bf16 v[2:5], a[152:155], a[176:179], v[2:5]\n\t"
"v_mfma_f32_16x16x32_bf16 v[6:9], v[236:239], a[172:175], v[6:9]\n\t"
"buffer_load_dwordx4 a[56:59], v252, s[8:11], s20 offen offset:2048 sc1\n\t"
"v_mfma_f32_16x16x32_bf16 v[6:9], a[144:147], a[176:179], v[6:9]\n\t"
"s_waitcnt lgkmcnt(5)\n\t"
"s_mov_b32 s20, 577536\n\t"
"v_mfma_f32_16x16x32_bf16 v[6:9], a[152:155], a[180:183], v[6:9]\n\t"
"v_mfma_f32_16x16x32_bf16 v[10:13], v[236:239], a[176:179], v[10:13]\n\t"
"buffer_load_dwordx4 a[80:83], v252, s[8:11], s20 offen offset:0 sc1\n\t"
"v_mfma_f32_16x16x32_bf16 v[10:13], a[144:147], a[180:183], v[10:13]\n\t"
"s_waitcnt lgkmcnt(4)\n\t"
"v_mfma_f32_16x16x32_bf16 v[10:13], a[152:155], a[184:187], v[10:13]\n\t"
"v_mfma_f32_16x16x32_bf16 v[14:17], v[236:239], a[180:183], v[14:17]\n\t"
"buffer_load_dwordx4 a[88:91], v252, s[8:11], s20 offen offset:1024 sc1\n\t"
"v_mfma_f32_16x16x32_bf16 v[14:17], a[144:147], a[184:187], v[14:17]\n\t"
"s_waitcnt lgkmcnt(3)\n\t"
"v_mfma_f32_16x16x32_bf16 v[14:17], a[152:155], a[188:191], v[14:17]\n\t"
"v_mfma_f32_16x16x32_bf16 v[18:21], v[236:239], a[184:187], v[18:21]\n\t"
"buffer_load_dwordx4 a[96:99], v252, s[8:11], s20 offen offset:2048 sc1\n\t"
"v_mfma_f32_16x16x32_bf16 v[18:21], a[144:147], a[188:191], v[18:21]\n\t"
"s_waitcnt lgkmcnt(2)\n\t"
"s_mov_b32 s20, 110592\n\t"
"v_xor_b32 v246, 0xc0, v246\n\t"
"v_mfma_f32_16x16x32_bf16 v[18:21], a[152:155], a[192:195], v[18:21]\n\t"
"v_mfma_f32_16x16x32_bf16 v[22:25], v[236:239], a[188:191], v[22:25]\n\t"
"buffer_load_dwordx4 a[136:139], v252, s[8:11], s20 offen offset:0 sc1\n\t"
"v_mfma_f32_16x16x32_bf16 v[22:25], a[144:147], a[192:195], v[22:25]\n\t"
"s_waitcnt lgkmcnt(1)\n\t"
"v_mfma_f32_16x16x32_bf16 v[22:25], a[152:155], a[196:199], v[22:25]\n\t"
"v_mfma_f32_16x16x32_bf16 v[26:29], v[236:239], a[192:195], v[26:29]\n\t"
"buffer_load_dwordx4 a[4:7], v252, s[8:11], s20 offen offset:1024 sc1\n\t"
"v_mfma_f32_16x16x32_bf16 v[26:29], a[144:147], a[196:199], v[26:29]\n\t"
"s_waitcnt lgkmcnt(0)\n\t"
"v_mfma_f32_16x16x32_bf16 v[26:29], a[152:155], a[200:203], v[26:29]\n\t"
"v_mfma_f32_16x16x32_bf16 v[30:33], v[236:239], a[196:199], v[30:33]\n\t"
"buffer_load_dwordx4 a[12:15], v252, s[8:11], s20 offen offset:2048 sc1\n\t"
"v_mfma_f32_16x16x32_bf16 v[30:33], a[144:147], a[200:203], v[30:33]\n\t"
"s_waitcnt vmcnt(15)\n\t"
"s_mov_b32 s20, 122880\n\t"
"v_mfma_f32_16x16x32_bf16 v[30:33], a[152:155], a[204:207], v[30:33]\n\t"
"v_mfma_f32_16x16x32_bf16 v[34:37], a[160:163], a[168:171], v[34:37]\n\t"
"buffer_load_dwordx4 a[20:23], v252, s[8:11], s20 offen offset:0 sc1\n\t"
"v_mfma_f32_16x16x32_bf16 v[34:37], v[200:203], a[172:175], v[34:37]\n\t"
"v_mfma_f32_16x16x32_bf16 v[34:37], v[208:211], a[176:179], v[34:37]\n\t"
"ds_read_b64_tr_b16 v[224:225], v246 offset:0\n\t"
"v_mfma_f32_16x16x32_bf16 v[38:41], a[160:163], a[172:175], v[38:41]\n\t"
"buffer_load_dwordx4 a[28:31], v252, s[8:11], s20 offen offset:1024 sc1\n\t"
"v_mfma_f32_16x16x32_bf16 v[38:41], v[200:203], a[176:179], v[38:41]\n\t"
"ds_read_b64_tr_b16 v[226:227], v246 offset:3072\n\t"
"v_mfma_f32_16x16x32_bf16 v[38:41], v[208:211], a[180:183], v[38:41]\n\t"
"v_mfma_f32_16x16x32_bf16 v[42:45], a[160:163], a[176:179], v[42:45]\n\t"
"buffer_load_dwordx4 a[36:39], v252, s[8:11], s20 offen offset:2048 sc1\n\t"
"v_mfma_f32_16x16x32_bf16 v[42:45], v[200:203], a[180:183], v[42:45]\n\t"
"s_mov_b32 s20, 135168\n\t"
"v_mfma_f32_16x16x32_bf16 v[42:45], v[208:211], a[184:187], v[42:45]\n\t"
"v_mfma_f32_16x16x32_bf16 v[46:49], a[160:163], a[180:183], v[46:49]\n\t"
"buffer_load_dwordx4 a[76:79], v252, s[8:11], s20 offen offset:0 sc1\n\t"
"v_mfma_f32_16x16x32_bf16 v[46:49], v[200:203], a[184:187], v[46:49]\n\t"
"ds_read_b64_tr_b16 v[228:229], v246 offset:6144\n\t"
"v_mfma_f32_16x16x32_bf16 v[46:49], v[208:211], a[188:191], v[46:49]\n\t"
"ds_read_b64_tr_b16 v[230:231], v246 offset:9216\n\t"
"v_mfma_f32_16x16x32_bf16 v[50:53], a[160:163], a[184:187], v[50:53]\n\t"
"buffer_load_dwordx4 a[84:87], v252, s[8:11], s20 offen offset:1024 sc1\n\t"
"v_mfma_f32_16x16x32_bf16 v[50:53], v[200:203], a[188:191], v[50:53]\n\t"
"v_mfma_f32_16x16x32_bf16 v[50:53], v[208:211], a[192:195], v[50:53]\n\t"
"v_mfma_f32_16x16x32_bf16 v[54:57], a[160:163], a[188:191], v[54:57]\n\t"
"buffer_load_dwordx4 a[92:95], v252, s[8:11], s20 offen offset:2048 sc1\n\t"
"v_mfma_f32_16x16x32_bf16 v[54:57], v[200:203], a[192:195], v[54:57]\n\t"
"s_waitcnt lgkmcnt(2)\n\t"
"s_mov_b32 s20, 147456\n\t"
"v_mfma_f32_16x16x32_bf16 v[54:57], v[208:211], a[196:199], v[54:57]\n\t"
"v_mfma_f32_16x16x32_bf16 v[224:227], v[224:227], v[220:223], 0\n\t"
"buffer_load_dwordx4 a[148:151], v252, s[8:11], s20 offen offset:0 sc1\n\t"
"v_mfma_f32_16x16x32_bf16 v[58:61], a[160:163], a[192:195], v[58:61]\n\t"
"ds_read_b64_tr_b16 v[232:233], v246 offset:12288\n\t"
"v_mfma_f32_16x16x32_bf16 v[58:61], v[200:203], a[196:199], v[58:61]\n\t"
"ds_read_b64_tr_b16 v[234:235], v246 offset:15360\n\t"
"v_mfma_f32_16x16x32_bf16 v[58:61], v[208:211], a[200:203], v[58:61]\n\t"
"v_mfma_f32_16x16x32_bf16 v[62:65], a[160:163], a[196:199], v[62:65]\n\t"
"buffer_load_dwordx4 a[156:159], v252, s[8:11], s20 offen offset:1024 sc1\n\t"
"v_mfma_f32_16x16x32_bf16 v[62:65], v[200:203], a[200:203], v[62:65]\n\t"
"s_waitcnt vmcnt(20) lgkmcnt(2)\n\t"
"v_cvt_pk_bf16_f32 v224, v224, v225\n\t"
"v_cvt_pk_bf16_f32 v225, v226, v227\n\t"
"v_mfma_f32_16x16x32_bf16 v[62:65], v[208:211], a[204:207], v[62:65]\n\t"
"v_mfma_f32_16x16x32_bf16 v[228:231], v[228:231], v[220:223], 0\n\t"
"v_mfma_f32_16x16x32_bf16 v[66:69], v[216:219], a[168:171], v[66:69]\n\t"
"buffer_load_dwordx4 a[164:167], v252, s[8:11], s20 offen offset:2048 sc1\n\t"
"v_mfma_f32_16x16x32_bf16 v[66:69], a[0:3], a[172:175], v[66:69]\n\t"
"s_mov_b32 s20, 159744\n\t"
"ds_read_b64_tr_b16 v[236:237], v246 offset:19968\n\t"
"v_mfma_f32_16x16x32_bf16 v[66:69], a[8:11], a[176:179], v[66:69]\n\t"
"ds_read_b64_tr_b16 v[238:239], v246 offset:23040\n\t"
"v_mfma_f32_16x16x32_bf16 v[70:73], v[216:219], a[172:175], v[70:73]\n\t"
"ds_read_b128 a[168:171], v244 offset:9216\n\t"
"v_mfma_f32_16x16x32_bf16 v[70:73], a[0:3], a[176:179], v[70:73]\n\t"
"buffer_load_dwordx4 v[240:243], v252, s[8:11], s20 offen offset:0 sc1\n\t"
"v_mfma_f32_16x16x32_bf16 v[70:73], a[8:11], a[180:183], v[70:73]\n\t"
"ds_read_b128 a[172:175], v244 offset:9728\n\t"
"v_cvt_pk_bf16_f32 v228, v228, v229\n\t"
"v_mfma_f32_16x16x32_bf16 v[74:77], v[216:219], a[176:179], v[74:77]\n\t"
"s_waitcnt lgkmcnt(4)\n\t"
"ds_read_b128 a[176:179], v244 offset:10240\n\t"
"v_cvt_pk_bf16_f32 v229, v230, v231\n\t"
"v_mfma_f32_16x16x32_bf16 v[74:77], a[0:3], a[180:183], v[74:77]\n\t"
"v_mfma_f32_16x16x32_bf16 v[74:77], a[8:11], a[184:187], v[74:77]\n\t"
"buffer_load_dwordx4 a[208:211], v252, s[8:11], s20 offen offset:1024 sc1\n\t"
"v_mfma_f32_16x16x32_bf16 v[232:235], v[232:235], v[220:223], 0\n\t"
"v_mfma_f32_16x16x32_bf16 v[78:81], v[216:219], a[180:183], v[78:81]\n\t"
"ds_read_b128 a[180:183], v244 offset:10752\n\t"
"v_mfma_f32_16x16x32_bf16 v[78:81], a[0:3], a[184:187], v[78:81]\n\t"
"v_mfma_f32_16x16x32_bf16 v[78:81], a[8:11], a[188:191], v[78:81]\n\t"
"buffer_load_dwordx4 v[248:251], v252, s[8:11], s20 offen offset:2048 sc1\n\t"
"v_mfma_f32_16x16x32_bf16 v[82:85], v[216:219], a[184:187], v[82:85]\n\t"
"s_waitcnt lgkmcnt(4)\n\t"
"ds_read_b128 a[184:187], v244 offset:11264\n\t"
"s_mov_b32 s20, 172032\n\t"
"v_mfma_f32_16x16x32_bf16 v[82:85], a[0:3], a[188:191], v[82:85]\n\t"
"v_cvt_pk_bf16_f32 v232, v232, v233\n\t"
"v_cvt_pk_bf16_f32 v233, v234, v235\n\t"
"v_mfma_f32_16x16x32_bf16 v[82:85], a[8:11], a[192:195], v[82:85]\n\t"
"v_mfma_f32_16x16x32_bf16 v[236:239], v[236:239], v[220:223], 0\n\t"
"buffer_load_dwordx4 a[212:215], v252, s[8:11], s20 offen offset:0 sc1\n\t"
"v_mfma_f32_16x16x32_bf16 v[86:89], v[216:219], a[188:191], v[86:89]\n\t"
"ds_read_b128 a[188:191], v244 offset:11776\n\t"
"v_mfma_f32_16x16x32_bf16 v[86:89], a[0:3], a[192:195], v[86:89]\n\t"
"v_mfma_f32_16x16x32_bf16 v[86:89], a[8:11], a[196:199], v[86:89]\n\t"
"v_mfma_f32_16x16x32_bf16 v[90:93], v[216:219], a[192:195], v[90:93]\n\t"
"buffer_load_dwordx4 a[216:219], v252, s[8:11], s20 offen offset:1024 sc1\n\t"
"v_mfma_f32_16x16x32_bf16 v[90:93], a[0:3], a[196:199], v[90:93]\n\t"
"ds_read_b128 a[192:195], v244 offset:12288\n\t"
"v_cvt_pk_bf16_f32 v236, v236, v237\n\t"
"v_mfma_f32_16x16x32_bf16 v[90:93], a[8:11], a[200:203], v[90:93]\n\t"
"v_cvt_pk_bf16_f32 v237, v238, v239\n\t"
"v_mfma_f32_16x16x32_bf16 v[94:97], v[216:219], a[196:199], v[94:97]\n\t"
"s_waitcnt vmcnt(23) lgkmcnt(4)\n\t"
"ds_read_b128 a[196:199], v244 offset:12800\n\t"
"v_mfma_f32_16x16x32_bf16 v[94:97], a[0:3], a[200:203], v[94:97]\n\t"
"ds_read_b128 a[200:203], v244 offset:13312\n\t"
"v_mfma_f32_16x16x32_bf16 v[94:97], a[8:11], a[204:207], v[94:97]\n\t"
"ds_read_b128 a[204:207], v244 offset:13824\n\t"
"v_mfma_f32_16x16x32_bf16 v[100:103], a[16:19], a[168:171], v[100:103]\n\t"
"buffer_load_dwordx4 a[220:223], v252, s[8:11], s20 offen offset:2048 sc1\n\t"
"v_mfma_f32_16x16x32_bf16 v[100:103], a[24:27], a[172:175], v[100:103]\n\t"
"s_waitcnt lgkmcnt(6)\n\t"
"s_mov_b32 s20, 36864\n\t"
"v_mfma_f32_16x16x32_bf16 v[100:103], a[32:35], a[176:179], v[100:103]\n\t"
"buffer_load_dwordx4 a[224:227], v252, s[8:11], s20 offen offset:0 sc1\n\t"
"v_mfma_f32_16x16x32_bf16 v[104:107], a[16:19], a[172:175], v[104:107]\n\t"
"s_waitcnt lgkmcnt(5)\n\t"
"v_mfma_f32_16x16x32_bf16 v[104:107], a[24:27], a[176:179], v[104:107]\n\t"
"v_mfma_f32_16x16x32_bf16 v[104:107], a[32:35], a[180:183], v[104:107]\n\t"
"s_waitcnt lgkmcnt(0)\n\t"
"s_barrier\n\t"
"v_mfma_f32_16x16x32_bf16 v[108:111], a[16:19], a[176:179], v[108:111]\n\t"
"v_fma_f32 v196, -v2, s56, -v100\n\t"
"v_mfma_f32_16x16x32_bf16 v[108:111], a[24:27], a[180:183], v[108:111]\n\t"
"buffer_load_dwordx4 a[228:231], v252, s[8:11], s20 offen offset:1024 sc1\n\t"
"v_mfma_f32_16x16x32_bf16 v[108:111], a[32:35], a[184:187], v[108:111]\n\t"
"ds_write_b64 v245, v[224:225] offset:0\n\t"
"v_mfma_f32_16x16x32_bf16 v[112:115], a[16:19], a[180:183], v[112:115]\n\t"
"ds_read_b64_tr_b16 v[224:225], v246 offset:26112\n\t"
"v_fma_f32 v197, -v3, s56, -v101\n\t"
"v_mfma_f32_16x16x32_bf16 v[112:115], a[24:27], a[184:187], v[112:115]\n\t"
"ds_read_b64_tr_b16 v[226:227], v246 offset:29184\n\t"
"v_fma_f32 v198, -v4, s56, -v102\n\t"
"v_mfma_f32_16x16x32_bf16 v[112:115], a[32:35], a[188:191], v[112:115]\n\t"
"ds_write_b64 v245, v[228:229] offset:512\n\t"
"v_mfma_f32_16x16x32_bf16 v[116:119], a[16:19], a[184:187], v[116:119]\n\t"
"buffer_load_dwordx4 a[232:235], v252, s[8:11], s20 offen offset:2048 sc1\n\t"
"v_mfma_f32_16x16x32_bf16 v[116:119], a[24:27], a[188:191], v[116:119]\n\t"
"s_mov_b32 s20, 49152\n\t"
"ds_read_b64_tr_b16 v[228:229], v246 offset:32256\n\t"
"v_fma_f32 v199, -v5, s56, -v103\n\t"
"v_mfma_f32_16x16x32_bf16 v[116:119], a[32:35], a[192:195], v[116:119]\n\t"
"ds_write_b64 v245, v[232:233] offset:1024\n\t"
"v_mfma_f32_16x16x32_bf16 v[120:123], a[16:19], a[188:191], v[120:123]\n\t"
"buffer_load_dwordx4 a[236:239], v252, s[8:11], s20 offen offset:0 sc1\n\t"
"v_mfma_f32_16x16x32_bf16 v[120:123], a[24:27], a[192:195], v[120:123]\n\t"
"s_waitcnt lgkmcnt(3)\n\t"
"ds_read_b64_tr_b16 v[230:231], v246 offset:35328\n\t"
"v_fma_f32 v204, -v6, s56, -v104\n\t"
"v_mfma_f32_16x16x32_bf16 v[120:123], a[32:35], a[196:199], v[120:123]\n\t"
"ds_write_b64 v245, v[236:237] offset:1536\n\t"
"v_mfma_f32_16x16x32_bf16 v[224:227], v[224:227], v[220:223], 0\n\t"
"buffer_load_dwordx4 a[240:243], v252, s[8:11], s20 offen offset:1024 sc1\n\t"
"v_mfma_f32_16x16x32_bf16 v[124:127], a[16:19], a[192:195], v[124:127]\n\t"
"ds_read_b64_tr_b16 v[232:233], v246 offset:39936\n\t"
"v_fma_f32 v205, -v7, s56, -v105\n\t"
"v_mfma_f32_16x16x32_bf16 v[124:127], a[24:27], a[196:199], v[124:127]\n\t"
"ds_read_b64_tr_b16 v[234:235], v246 offset:43008\n\t"
"v_fma_f32 v206, -v8, s56, -v106\n\t"
"v_mfma_f32_16x16x32_bf16 v[124:127], a[32:35], a[200:203], v[124:127]\n\t"
"s_waitcnt lgkmcnt(3)\n\t"
"buffer_load_dwordx4 a[244:247], v252, s[8:11], s20 offen offset:2048 sc1\n\t"
"s_mov_b32 s20, 61440\n\t"
"v_mfma_f32_16x16x32_bf16 v[228:231], v[228:231], v[220:223], 0\n\t"
"ds_read_b64_tr_b16 v[236:237], v246 offset:46080\n\t"
"v_fma_f32 v207, -v9, s56, -v107\n\t"
"v_mfma_f32_16x16x32_bf16 v[128:131], a[16:19], a[196:199], v[128:131]\n\t"
"ds_read_b64_tr_b16 v[238:239], v246 offset:49152\n\t"
"v_cvt_pk_bf16_f32 v224, v224, v225\n\t"
"v_mfma_f32_16x16x32_bf16 v[128:131], a[24:27], a[200:203], v[128:131]\n\t"
"s_waitcnt vmcnt(27)\n\t"
"v_cvt_pk_bf16_f32 v225, v226, v227\n\t"
"v_fma_f32 v212, -v10, s56, -v108\n\t"
"v_mfma_f32_16x16x32_bf16 v[128:131], a[32:35], a[204:207], v[128:131]\n\t"
"s_waitcnt lgkmcnt(2)\n\t"
"buffer_load_dwordx4 a[248:251], v252, s[8:11], s20 offen offset:0 sc1\n\t"
"v_mfma_f32_16x16x32_bf16 v[232:235], v[232:235], v[220:223], 0\n\t"
"buffer_load_dwordx4 a[252:255], v252, s[8:11], s20 offen offset:1024 sc1\n\t"
"v_mfma_f32_16x16x32_bf16 v[132:135], a[40:43], a[168:171], v[132:135]\n\t"
"v_cvt_pk_bf16_f32 v228, v228, v229\n\t"
"v_cvt_pk_bf16_f32 v229, v230, v231\n\t"
"v_mfma_f32_16x16x32_bf16 v[132:135], a[48:51], a[172:175], v[132:135]\n\t"
"s_waitcnt lgkmcnt(0)\n\t"
"v_fma_f32 v213, -v11, s56, -v109\n\t"
"v_fma_f32 v214, -v12, s56, -v110\n\t"
"v_mfma_f32_16x16x32_bf16 v[132:135], a[56:59], a[176:179], v[132:135]\n\t"
"ds_write_b64 v245, v[224:225] offset:2048\n\t"
"v_mfma_f32_16x16x32_bf16 v[236:239], v[236:239], v[220:223], 0\n\t"
"ds_read_b64_tr_b16 v[224:225], v246 offset:52224\n\t"
"v_fma_f32 v215, -v13, s56, -v111\n\t"
"v_mfma_f32_16x16x32_bf16 v[136:139], a[40:43], a[172:175], v[136:139]\n\t"
"ds_read_b64_tr_b16 v[226:227], v246 offset:55296\n\t"
"v_cvt_pk_bf16_f32 v232, v232, v233\n\t"
"v_mfma_f32_16x16x32_bf16 v[136:139], a[48:51], a[176:179], v[136:139]\n\t"
"v_cvt_pk_bf16_f32 v233, v234, v235\n\t"
"v_fma_f32 v98, -v14, s56, -v112\n\t"
"v_mfma_f32_16x16x32_bf16 v[136:139], a[56:59], a[180:183], v[136:139]\n\t"
"ds_write_b64 v245, v[228:229] offset:2560\n\t"
"v_mfma_f32_16x16x32_bf16 v[140:143], a[40:43], a[176:179], v[140:143]\n\t"
"v_fma_f32 v99, -v15, s56, -v113\n\t"
"v_accvgpr_write_b32 a0, v98\n\t"
"v_mfma_f32_16x16x32_bf16 v[140:143], a[48:51], a[180:183], v[140:143]\n\t"
"v_cvt_pk_bf16_f32 v236, v236, v237\n\t"
"v_cvt_pk_bf16_f32 v237, v238, v239\n\t"
"v_mfma_f32_16x16x32_bf16 v[140:143], a[56:59], a[184:187], v[140:143]\n\t"
"ds_write_b64 v245, v[232:233] offset:3072\n\t"
"v_mfma_f32_16x16x32_bf16 v[144:147], a[40:43], a[180:183], v[144:147]\n\t"
"s_waitcnt lgkmcnt(2)\n\t"
"v_fma_f32 v98, -v16, s56, -v114\n\t"
"v_accvgpr_write_b32 a1, v99\n\t"
"v_mfma_f32_16x16x32_bf16 v[144:147], a[48:51], a[184:187], v[144:147]\n\t"
"v_fma_f32 v99, -v17, s56, -v115\n\t"
"v_accvgpr_write_b32 a2, v98\n\t"
"v_mfma_f32_16x16x32_bf16 v[144:147], a[56:59], a[188:191], v[144:147]\n\t"
"ds_write_b64 v245, v[236:237] offset:3584\n\t"
"v_mfma_f32_16x16x32_bf16 v[224:227], v[224:227], v[220:223], 0\n\t"
"v_accvgpr_write_b32 a3, v99\n\t"
"v_fma_f32 v98, -v18, s56, -v116\n\t"
"v_mfma_f32_16x16x32_bf16 v[148:151], a[40:43], a[184:187], v[148:151]\n\t"
"v_fma_f32 v99, -v19, s56, -v117\n\t"
"v_accvgpr_write_b32 a8, v98\n\t"
"v_mfma_f32_16x16x32_bf16 v[148:151], a[48:51], a[188:191], v[148:151]\n\t"
"v_fma_f32 v98, -v20, s56, -v118\n\t"
"v_accvgpr_write_b32 a9, v99\n\t"
"v_mfma_f32_16x16x32_bf16 v[148:151], a[56:59], a[192:195], v[148:151]\n\t"
"v_fma_f32 v99, -v21, s56, -v119\n\t"
"v_accvgpr_write_b32 a10, v98\n\t"
"v_mfma_f32_16x16x32_bf16 v[152:155], a[40:43], a[188:191], v[152:155]\n\t"
"v_accvgpr_write_b32 a11, v99\n\t"
"v_fma_f32 v98, -v22, s56, -v120\n\t"
"v_mfma_f32_16x16x32_bf16 v[152:155], a[48:51], a[192:195], v[152:155]\n\t"
"v_cvt_pk_bf16_f32 v224, v224, v225\n\t"
"v_cvt_pk_bf16_f32 v225, v226, v227\n\t"
"v_mfma_f32_16x16x32_bf16 v[152:155], a[56:59], a[196:199], v[152:155]\n\t"
"buffer_load_dwordx4 v[236:239], v252, s[8:11], s20 offen offset:2048 sc1\n\t"
"v_mfma_f32_16x16x32_bf16 v[156:159], a[40:43], a[192:195], v[156:159]\n\t"
"v_fma_f32 v99, -v23, s56, -v121\n\t"
"v_accvgpr_write_b32 a16, v98\n\t"
"v_mfma_f32_16x16x32_bf16 v[156:159], a[48:51], a[196:199], v[156:159]\n\t"
"v_fma_f32 v98, -v24, s56, -v122\n\t"
"v_accvgpr_write_b32 a17, v99\n\t"
"v_mfma_f32_16x16x32_bf16 v[156:159], a[56:59], a[200:203], v[156:159]\n\t"
"ds_write_b64 v245, v[224:225] offset:4096\n\t"
"v_mfma_f32_16x16x32_bf16 v[160:163], a[40:43], a[196:199], v[160:163]\n\t"
"s_waitcnt vmcnt(27)\n\t"
"v_fma_f32 v99, -v25, s56, -v123\n\t"
"v_accvgpr_write_b32 a18, v98\n\t"
"v_mfma_f32_16x16x32_bf16 v[160:163], a[48:51], a[200:203], v[160:163]\n\t"
"v_accvgpr_write_b32 a19, v99\n\t"
"v_fma_f32 v98, -v26, s56, -v124\n\t"
"v_mfma_f32_16x16x32_bf16 v[160:163], a[56:59], a[204:207], v[160:163]\n\t"
"v_fma_f32 v99, -v27, s56, -v125\n\t"
"v_accvgpr_write_b32 a24, v98\n\t"
"v_mfma_f32_16x16x32_bf16 v[164:167], a[80:83], a[168:171], v[164:167]\n\t"
"v_fma_f32 v98, -v28, s56, -v126\n\t"
"v_accvgpr_write_b32 a25, v99\n\t"
"v_mfma_f32_16x16x32_bf16 v[164:167], a[88:91], a[172:175], v[164:167]\n\t"
"v_fma_f32 v99, -v29, s56, -v127\n\t"
"v_mfma_f32_16x16x32_bf16 v[164:167], a[96:99], a[176:179], v[164:167]\n\t"
"s_waitcnt lgkmcnt(0)\n\t"
"s_barrier\n\t"
"v_mfma_f32_16x16x32_bf16 v[168:171], a[80:83], a[172:175], v[168:171]\n\t"
"ds_read_b128 a[168:171], v244 offset:0\n\t"
"v_mfma_f32_16x16x32_bf16 v[168:171], a[88:91], a[176:179], v[168:171]\n\t"
"ds_read_b128 a[172:175], v244 offset:512\n\t"
"v_accvgpr_write_b32 a26, v98\n\t"
"v_mfma_f32_16x16x32_bf16 v[168:171], a[96:99], a[180:183], v[168:171]\n\t"
"v_accvgpr_write_b32 a27, v99\n\t"
"v_fma_f32 v98, -v30, s56, -v128\n\t"
"v_mfma_f32_16x16x32_bf16 v[172:175], a[80:83], a[176:179], v[172:175]\n\t"
"ds_read_b128 a[176:179], v244 offset:1024\n\t"
"v_fma_f32 v99, -v31, s56, -v129\n\t"
"v_mfma_f32_16x16x32_bf16 v[172:175], a[88:91], a[180:183], v[172:175]\n\t"
"v_accvgpr_write_b32 a32, v98\n\t"
"v_fma_f32 v98, -v32, s56, -v130\n\t"
"v_mfma_f32_16x16x32_bf16 v[172:175], a[96:99], a[184:187], v[172:175]\n\t"
"v_accvgpr_write_b32 a33, v99\n\t"
"v_fma_f32 v99, -v33, s56, -v131\n\t"
"v_mfma_f32_16x16x32_bf16 v[176:179], a[80:83], a[180:183], v[176:179]\n\t"
"ds_read_b128 a[180:183], v244 offset:1536\n\t"
"v_accvgpr_write_b32 a34, v98\n\t"
"v_mfma_f32_16x16x32_bf16 v[176:179], a[88:91], a[184:187], v[176:179]\n\t"
"v_accvgpr_write_b32 a35, v99\n\t"
"v_fma_f32 v98, -v34, s56, -v132\n\t"
"v_mfma_f32_16x16x32_bf16 v[176:179], a[96:99], a[188:191], v[176:179]\n\t"
"v_fma_f32 v99, -v35, s56, -v133\n\t"
"v_accvgpr_write_b32 a40, v98\n\t"
"v_mfma_f32_16x16x32_bf16 v[180:183], a[80:83], a[184:187], v[180:183]\n\t"
"ds_read_b128 a[184:187], v244 offset:2048\n\t"
"v_fma_f32 v98, -v36, s56, -v134\n\t"
"v_mfma_f32_16x16x32_bf16 v[180:183], a[88:91], a[188:191], v[180:183]\n\t"
"v_accvgpr_write_b32 a41, v99\n\t"
"v_fma_f32 v99, -v37, s56, -v135\n\t"
"v_mfma_f32_16x16x32_bf16 v[180:183], a[96:99], a[192:195], v[180:183]\n\t"
"v_accvgpr_write_b32 a42, v98\n\t"
"v_accvgpr_write_b32 a43, v99\n\t"
"v_mfma_f32_16x16x32_bf16 v[184:187], a[80:83], a[188:191], v[184:187]\n\t"
"ds_read_b128 a[188:191], v244 offset:2560\n\t"
"v_fma_f32 v98, -v38, s56, -v136\n\t"
"v_mfma_f32_16x16x32_bf16 v[184:187], a[88:91], a[192:195], v[184:187]\n\t"
"v_fma_f32 v99, -v39, s56, -v137\n\t"
"v_accvgpr_write_b32 a48, v98\n\t"
"v_mfma_f32_16x16x32_bf16 v[184:187], a[96:99], a[196:199], v[184:187]\n\t"
"v_fma_f32 v98, -v40, s56, -v138\n\t"
"v_accvgpr_write_b32 a49, v99\n\t"
"v_mfma_f32_16x16x32_bf16 v[188:191], a[80:83], a[192:195], v[188:191]\n\t"
"ds_read_b128 a[192:195], v244 offset:3072\n\t"
"v_fma_f32 v99, -v41, s56, -v139\n\t"
"v_mfma_f32_16x16x32_bf16 v[188:191], a[88:91], a[196:199], v[188:191]\n\t"
"v_accvgpr_write_b32 a50, v98\n\t"
"v_accvgpr_write_b32 a51, v99\n\t"
"v_mfma_f32_16x16x32_bf16 v[188:191], a[96:99], a[200:203], v[188:191]\n\t"
"v_fma_f32 v98, -v42, s56, -v140\n\t"
"v_fma_f32 v99, -v43, s56, -v141\n\t"
"v_mfma_f32_16x16x32_bf16 v[192:195], a[80:83], a[196:199], v[192:195]\n\t"
"s_waitcnt vmcnt(24) lgkmcnt(4)\n\t"
"ds_read_b128 a[196:199], v244 offset:3584\n\t"
"v_accvgpr_write_b32 a56, v98\n\t"
"v_mfma_f32_16x16x32_bf16 v[192:195], a[88:91], a[200:203], v[192:195]\n\t"
"ds_read_b128 a[200:203], v244 offset:4096\n\t"
"v_fma_f32 v98, -v44, s56, -v142\n\t"
"v_mfma_f32_16x16x32_bf16 v[192:195], a[96:99], a[204:207], v[192:195]\n\t"
"ds_read_b128 a[204:207], v244 offset:4608\n\t"
"v_accvgpr_write_b32 a57, v99\n\t"
"Lst2_%=:\n\t"
"v_mfma_f32_16x16x32_bf16 v[196:199], a[136:139], a[168:171], v[196:199]\n\t"
"s_waitcnt lgkmcnt(6)\n\t"
"v_xor_b32 v246, 0xc0, v246\n\t"
"v_fma_f32 v99, -v45, s56, -v143\n\t"
"v_mfma_f32_16x16x32_bf16 v[196:199], a[4:7], a[172:175], v[196:199]\n\t"
"v_accvgpr_write_b32 a58, v98\n\t"
"v_accvgpr_write_b32 a59, v99\n\t"
"v_mfma_f32_16x16x32_bf16 v[196:199], a[12:15], a[176:179], v[196:199]\n\t"
"v_fma_f32 v98, -v46, s56, -v144\n\t"
"v_fma_f32 v99, -v47, s56, -v145\n\t"
"v_mfma_f32_16x16x32_bf16 v[204:207], a[136:139], a[172:175], v[204:207]\n\t"
"s_waitcnt lgkmcnt(5)\n\t"
"v_accvgpr_write_b32 a64, v98\n\t"
"v_fma_f32 v98, -v48, s56, -v146\n\t"
"v_mfma_f32_16x16x32_bf16 v[204:207], a[4:7], a[176:179], v[204:207]\n\t"
"v_accvgpr_write_b32 a65, v99\n\t"
"v_fma_f32 v99, -v49, s56, -v147\n\t"
"v_mfma_f32_16x16x32_bf16 v[204:207], a[12:15], a[180:183], v[204:207]\n\t"
"v_accvgpr_write_b32 a66, v98\n\t"
"v_accvgpr_write_b32 a67, v99\n\t"
"v_mfma_f32_16x16x32_bf16 v[212:215], a[136:139], a[176:179], v[212:215]\n\t"
"s_waitcnt lgkmcnt(4)\n\t"
"v_fma_f32 v98, -v50, s56, -v148\n\t"
"v_fma_f32 v99, -v51, s56, -v149\n\t"
"v_mfma_f32_16x16x32_bf16 v[212:215], a[4:7], a[180:183], v[212:215]\n\t"
"v_accvgpr_write_b32 a72, v98\n\t"
"v_fma_f32 v98, -v52, s56, -v150\n\t"
"v_mfma_f32_16x16x32_bf16 v[212:215], a[12:15], a[184:187], v[212:215]\n\t"
"v_accvgpr_write_b32 a73, v99\n\t"
"v_fma_f32 v99, -v53, s56, -v151\n\t"
"v_mfma_f32_16x16x32_bf16 a[0:3], a[136:139], a[180:183], a[0:3]\n\t"
"s_waitcnt lgkmcnt(3)\n\t"
"v_accvgpr_write_b32 a74, v98\n\t"
"v_accvgpr_write_b32 a75, v99\n\t"
"v_mfma_f32_16x16x32_bf16 a[0:3], a[4:7], a[184:187], a[0:3]\n\t"
"v_fma_f32 v98, -v54, s56, -v152\n\t"
"v_fma_f32 v99, -v55, s56, -v153\n\t"
"v_mfma_f32_16x16x32_bf16 a[0:3], a[12:15], a[188:191], a[0:3]\n\t"
"v_accvgpr_write_b32 a80, v98\n\t"
"v_fma_f32 v98, -v56, s56, -v154\n\t"
"v_mfma_f32_16x16x32_bf16 a[8:11], a[136:139], a[184:187], a[8:11]\n\t"
"s_waitcnt lgkmcnt(2)\n\t"
"v_accvgpr_write_b32 a81, v99\n\t"
"v_fma_f32 v99, -v57, s56, -v155\n\t"
"v_mfma_f32_16x16x32_bf16 a[8:11], a[4:7], a[188:191], a[8:11]\n\t"
"v_accvgpr_write_b32 a82, v98\n\t"
"v_accvgpr_write_b32 a83, v99\n\t"
"v_mfma_f32_16x16x32_bf16 a[8:11], a[12:15], a[192:195], a[8:11]\n\t"
"ds_read_b64_tr_b16 v[228:229], v246 offset:0\n\t"
"v_fma_f32 v98, -v58, s56, -v156\n\t"
"v_mfma_f32_16x16x32_bf16 a[16:19], a[136:139], a[188:191], a[16:19]\n\t"
"s_waitcnt lgkmcnt(2)\n\t"
"ds_read_b64_tr_b16 v[230:231], v246 offset:3072\n\t"
"v_fma_f32 v99, -v59, s56, -v157\n\t"
"v_mfma_f32_16x16x32_bf16 a[16:19], a[4:7], a[192:195], a[16:19]\n\t"
"v_accvgpr_write_b32 a88, v98\n\t"
"v_fma_f32 v98, -v60, s56, -v158\n\t"
"v_mfma_f32_16x16x32_bf16 a[16:19], a[12:15], a[196:199], a[16:19]\n\t"
"v_accvgpr_write_b32 a89, v99\n\t"
"v_fma_f32 v99, -v61, s56, -v159\n\t"
"v_mfma_f32_16x16x32_bf16 a[24:27], a[136:139], a[192:195], a[24:27]\n\t"
"s_waitcnt lgkmcnt(2)\n\t"
"v_accvgpr_write_b32 a90, v98\n\t"
"v_accvgpr_write_b32 a91, v99\n\t"
"v_mfma_f32_16x16x32_bf16 a[24:27], a[4:7], a[196:199], a[24:27]\n\t"
"v_fma_f32 v98, -v62, s56, -v160\n\t"
"v_fma_f32 v99, -v63, s56, -v161\n\t"
"v_mfma_f32_16x16x32_bf16 a[24:27], a[12:15], a[200:203], a[24:27]\n\t"
"v_accvgpr_write_b32 a96, v98\n\t"
"v_fma_f32 v98, -v64, s56, -v162\n\t"
"v_mfma_f32_16x16x32_bf16 a[32:35], a[136:139], a[196:199], a[32:35]\n\t"
"s_waitcnt vmcnt(21)\n\t"
"ds_read_b64_tr_b16 v[232:233], v246 offset:6144\n\t"
"v_accvgpr_write_b32 a97, v99\n\t"
"v_mfma_f32_16x16x32_bf16 a[32:35], a[4:7], a[200:203], a[32:35]\n\t"
"ds_read_b64_tr_b16 v[234:235], v246 offset:9216\n\t"
"v_fma_f32 v99, -v65, s56, -v163\n\t"
"v_mfma_f32_16x16x32_bf16 a[32:35], a[12:15], a[204:207], a[32:35]\n\t"
"v_accvgpr_write_b32 a98, v98\n\t"
"v_accvgpr_write_b32 a99, v99\n\t"
"v_mfma_f32_16x16x32_bf16 a[40:43], a[20:23], a[168:171], a[40:43]\n\t"
"v_fma_f32 v98, -v66, s56, -v164\n\t"
"v_fma_f32 v99, -v67, s56, -v165\n\t"
"v_mfma_f32_16x16x32_bf16 a[40:43], a[28:31], a[172:175], a[40:43]\n\t"
"v_accvgpr_write_b32 a104, v98\n\t"
"v_fma_f32 v98, -v68, s56, -v166\n\t"
"v_mfma_f32_16x16x32_bf16 a[40:43], a[36:39], a[176:179], a[40:43]\n\t"
"v_accvgpr_write_b32 a105, v99\n\t"
"v_fma_f32 v99, -v69, s56, -v167\n\t"
"v_mfma_f32_16x16x32_bf16 a[48:51], a[20:23], a[172:175], a[48:51]\n\t"
"s_waitcnt lgkmcnt(2)\n\t"
"v_accvgpr_write_b32 a106, v98\n\t"
"v_accvgpr_write_b32 a107, v99\n\t"
"v_mfma_f32_16x16x32_bf16 a[48:51], a[28:31], a[176:179], a[48:51]\n\t"
"v_fma_f32 v98, -v70, s56, -v168\n\t"
"v_fma_f32 v99, -v71, s56, -v169\n\t"
"v_mfma_f32_16x16x32_bf16 a[48:51], a[36:39], a[180:183], a[48:51]\n\t"
"v_accvgpr_write_b32 a112, v98\n\t"
"v_fma_f32 v98, -v72, s56, -v170\n\t"
"v_mfma_f32_16x16x32_bf16 v[228:231], v[228:231], v[220:223], 0\n\t"
"v_accvgpr_write_b32 a113, v99\n\t"
"v_fma_f32 v99, -v73, s56, -v171\n\t"
"v_mfma_f32_16x16x32_bf16 a[56:59], a[20:23], a[176:179], a[56:59]\n\t"
"ds_read_b64_tr_b16 v[224:225], v246 offset:12288\n\t"
"v_accvgpr_write_b32 a114, v98\n\t"
"v_mfma_f32_16x16x32_bf16 a[56:59], a[28:31], a[180:183], a[56:59]\n\t"
"ds_read_b64_tr_b16 v[226:227], v246 offset:15360\n\t"
"v_accvgpr_write_b32 a115, v99\n\t"
"v_mfma_f32_16x16x32_bf16 a[56:59], a[36:39], a[184:187], a[56:59]\n\t"
"v_fma_f32 v98, -v74, s56, -v172\n\t"
"v_fma_f32 v99, -v75, s56, -v173\n\t"
"v_mfma_f32_16x16x32_bf16 a[64:67], a[20:23], a[180:183], a[64:67]\n\t"
"s_waitcnt lgkmcnt(2)\n\t"
"v_accvgpr_write_b32 a120, v98\n\t"
"v_fma_f32 v98, -v76, s56, -v174\n\t"
"v_mfma_f32_16x16x32_bf16 a[64:67], a[28:31], a[184:187], a[64:67]\n\t"
"v_cvt_pk_bf16_f32 v228, v228, v229\n\t"
"v_cvt_pk_bf16_f32 v229, v230, v231\n\t"
"v_mfma_f32_16x16x32_bf16 a[64:67], a[36:39], a[188:191], a[64:67]\n\t"
"v_accvgpr_write_b32 a121, v99\n\t"
"v_fma_f32 v99, -v77, s56, -v175\n\t"
"v_mfma_f32_16x16x32_bf16 v[232:235], v[232:235], v[220:223], 0\n\t"
"v_accvgpr_write_b32 a122, v98\n\t"
"v_accvgpr_write_b32 a123, v99\n\t"
"v_mfma_f32_16x16x32_bf16 a[72:75], a[20:23], a[184:187], a[72:75]\n\t"
"v_fma_f32 v98, -v78, s56, -v176\n\t"
"v_fma_f32 v99, -v79, s56, -v177\n\t"
"v_mfma_f32_16x16x32_bf16 a[72:75], a[28:31], a[188:191], a[72:75]\n\t"
"v_accvgpr_write_b32 a128, v98\n\t"
"v_fma_f32 v98, -v80, s56, -v178\n\t"
"v_mfma_f32_16x16x32_bf16 a[72:75], a[36:39], a[192:195], a[72:75]\n\t"
"v_accvgpr_write_b32 a129, v99\n\t"
"v_fma_f32 v99, -v81, s56, -v179\n\t"
"v_mfma_f32_16x16x32_bf16 a[80:83], a[20:23], a[188:191], a[80:83]\n\t"
"v_accvgpr_write_b32 a130, v98\n\t"
"v_accvgpr_write_b32 a131, v99\n\t"
"v_mfma_f32_16x16x32_bf16 a[80:83], a[28:31], a[192:195], a[80:83]\n\t"
"v_cvt_pk_bf16_f32 v230, v232, v233\n\t"
"v_cvt_pk_bf16_f32 v231, v234, v235\n\t"
"v_mfma_f32_16x16x32_bf16 a[80:83], a[36:39], a[196:199], a[80:83]\n\t"
"ds_read_b64_tr_b16 v[232:233], v246 offset:19968\n\t"
"v_fma_f32 v98, -v82, s56, -v180\n\t"
"v_mfma_f32_16x16x32_bf16 a[88:91], a[20:23], a[192:195], a[88:91]\n\t"
"s_waitcnt lgkmcnt(1)\n\t"
"ds_read_b64_tr_b16 v[234:235], v246 offset:23040\n\t"
"v_fma_f32 v99, -v83, s56, -v181\n\t"
"v_mfma_f32_16x16x32_bf16 a[88:91], a[28:31], a[196:199], a[88:91]\n\t"
"v_accvgpr_write_b32 a136, v98\n\t"
"v_fma_f32 v98, -v84, s56, -v182\n\t"
"v_mfma_f32_16x16x32_bf16 a[88:91], a[36:39], a[200:203], a[88:91]\n\t"
"v_accvgpr_write_b32 a137, v99\n\t"
"v_fma_f32 v99, -v85, s56, -v183\n\t"
"v_mfma_f32_16x16x32_bf16 v[224:227], v[224:227], v[220:223], 0\n\t"
"v_accvgpr_write_b32 a138, v98\n\t"
"v_accvgpr_write_b32 a139, v99\n\t"
"v_mfma_f32_16x16x32_bf16 a[96:99], a[20:23], a[196:199], a[96:99]\n\t"
"s_waitcnt vmcnt(18)\n\t"
"v_fma_f32 v98, -v86, s56, -v184\n\t"
"v_fma_f32 v99, -v87, s56, -v185\n\t"
"v_mfma_f32_16x16x32_bf16 a[96:99], a[28:31], a[200:203], a[96:99]\n\t"
"v_accvgpr_write_b32 a144, v98\n\t"
"v_fma_f32 v98, -v88, s56, -v186\n\t"
"v_mfma_f32_16x16x32_bf16 a[96:99], a[36:39], a[204:207], a[96:99]\n\t"
"v_accvgpr_write_b32 a145, v99\n\t"
"v_fma_f32 v99, -v89, s56, -v187\n\t"
"v_mfma_f32_16x16x32_bf16 a[104:107], a[76:79], a[168:171], a[104:107]\n\t"
"ds_read_b128 a[168:171], v244 offset:9216\n\t"
"v_accvgpr_write_b32 a146, v98\n\t"
"v_mfma_f32_16x16x32_bf16 a[104:107], a[84:87], a[172:175], a[104:107]\n\t"
"v_cvt_pk_bf16_f32 v224, v224, v225\n\t"
"v_cvt_pk_bf16_f32 v225, v226, v227\n\t"
"v_mfma_f32_16x16x32_bf16 a[104:107], a[92:95], a[176:179], a[104:107]\n\t"
"v_accvgpr_write_b32 a147, v99\n\t"
"v_fma_f32 v98, -v90, s56, -v188\n\t"
"v_mfma_f32_16x16x32_bf16 a[112:115], a[76:79], a[172:175], a[112:115]\n\t"
"s_waitcnt lgkmcnt(1)\n\t"
"ds_read_b128 a[172:175], v244 offset:9728\n\t"
"v_fma_f32 v99, -v91, s56, -v189\n\t"
"v_mfma_f32_16x16x32_bf16 a[112:115], a[84:87], a[176:179], a[112:115]\n\t"
"v_accvgpr_write_b32 a152, v98\n\t"
"v_fma_f32 v98, -v92, s56, -v190\n\t"
"v_mfma_f32_16x16x32_bf16 a[112:115], a[92:95], a[180:183], a[112:115]\n\t"
"v_accvgpr_write_b32 a153, v99\n\t"
"v_fma_f32 v99, -v93, s56, -v191\n\t"
"v_mfma_f32_16x16x32_bf16 v[232:235], v[232:235], v[220:223], 0\n\t"
"v_accvgpr_write_b32 a154, v98\n\t"
"v_accvgpr_write_b32 a155, v99\n\t"
"v_mfma_f32_16x16x32_bf16 a[120:123], a[76:79], a[176:179], a[120:123]\n\t"
"ds_read_b128 a[176:179], v244 offset:10240\n\t"
"v_fma_f32 v98, -v94, s56, -v192\n\t"
"v_mfma_f32_16x16x32_bf16 a[120:123], a[84:87], a[180:183], a[120:123]\n\t"
"v_fma_f32 v99, -v95, s56, -v193\n\t"
"v_accvgpr_write_b32 a160, v98\n\t"
"v_mfma_f32_16x16x32_bf16 a[120:123], a[92:95], a[184:187], a[120:123]\n\t"
"v_fma_f32 v98, -v96, s56, -v194\n\t"
"v_accvgpr_write_b32 a161, v99\n\t"
"v_mfma_f32_16x16x32_bf16 a[128:131], a[76:79], a[180:183], a[128:131]\n\t"
"ds_read_b128 a[180:183], v244 offset:10752\n\t"
"v_fma_f32 v99, -v97, s56, -v195\n\t"
"v_mfma_f32_16x16x32_bf16 a[128:131], a[84:87], a[184:187], a[128:131]\n\t"
"v_cvt_pk_bf16_f32 v226, v232, v233\n\t"
"v_cvt_pk_bf16_f32 v227, v234, v235\n\t"
"v_mfma_f32_16x16x32_bf16 a[128:131], a[92:95], a[188:191], a[128:131]\n\t"
"ds_read_b64_tr_b16 v[232:233], v246 offset:26112\n\t"
"v_accvgpr_write_b32 a162, v98\n\t"
"v_mfma_f32_16x16x32_bf16 a[136:139], a[76:79], a[184:187], a[136:139]\n\t"
"ds_read_b64_tr_b16 v[234:235], v246 offset:29184\n\t"
"v_accvgpr_write_b32 a163, v99\n\t"
"v_mfma_f32_16x16x32_bf16 a[136:139], a[84:87], a[188:191], a[136:139]\n\t"
"ds_read_b128 a[184:187], v244 offset:11264\n\t"
"v_fma_f32 v200, v100, s64, -v2\n\t"
"v_mfma_f32_16x16x32_bf16 a[136:139], a[92:95], a[192:195], a[136:139]\n\t"
"v_fma_f32 v201, v101, s64, -v3\n\t"
"v_fma_f32 v202, v102, s64, -v4\n\t"
"v_mfma_f32_16x16x32_bf16 a[144:147], a[76:79], a[188:191], a[144:147]\n\t"
"ds_read_b128 a[188:191], v244 offset:11776\n\t"
"v_fma_f32 v203, v103, s64, -v5\n\t"
"v_mfma_f32_16x16x32_bf16 a[144:147], a[84:87], a[192:195], a[144:147]\n\t"
"v_fma_f32 v208, v104, s64, -v6\n\t"
"v_fma_f32 v209, v105, s64, -v7\n\t"
"v_mfma_f32_16x16x32_bf16 a[144:147], a[92:95], a[196:199], a[144:147]\n\t"
"v_fma_f32 v210, v106, s64, -v8\n\t"
"v_fma_f32 v211, v107, s64, -v9\n\t"
"v_mfma_f32_16x16x32_bf16 a[152:155], a[76:79], a[192:195], a[152:155]\n\t"
"ds_read_b128 a[192:195], v244 offset:12288\n\t"
"v_fma_f32 v216, v108, s64, -v10\n\t"
"v_mfma_f32_16x16x32_bf16 a[152:155], a[84:87], a[196:199], a[152:155]\n\t"
"v_fma_f32 v217, v109, s64, -v11\n\t"
"v_fma_f32 v218, v110, s64, -v12\n\t"
"v_mfma_f32_16x16x32_bf16 a[152:155], a[92:95], a[200:203], a[152:155]\n\t"
"v_fma_f32 v219, v111, s64, -v13\n\t"
"v_fma_f32 v98, v112, s64, -v14\n\t"
"v_mfma_f32_16x16x32_bf16 a[160:163], a[76:79], a[196:199], a[160:163]\n\t"
"s_waitcnt lgkmcnt(3)\n\t"
"ds_read_b128 a[196:199], v244 offset:12800\n\t"
"v_fma_f32 v99, v113, s64, -v15\n\t"
"v_mfma_f32_16x16x32_bf16 a[160:163], a[84:87], a[200:203], a[160:163]\n\t"
"s_waitcnt vmcnt(15)\n\t"
"ds_read_b128 a[200:203], v244 offset:13312\n\t"
"v_accvgpr_write_b32 a4, v98\n\t"
"v_mfma_f32_16x16x32_bf16 a[160:163], a[92:95], a[204:207], a[160:163]\n\t"
"ds_read_b128 a[204:207], v244 offset:13824\n\t"
"v_fma_f32 v98, v114, s64, -v16\n\t"
"v_mfma_f32_16x16x32_bf16 v[232:235], v[232:235], v[220:223], 0\n\t"
"v_accvgpr_write_b32 a5, v99\n\t"
"v_fma_f32 v99, v115, s64, -v17\n\t"
"v_mfma_f32_16x16x32_bf16 v[200:203], a[148:151], a[168:171], v[200:203]\n\t"
"v_accvgpr_write_b32 a6, v98\n\t"
"v_accvgpr_write_b32 a7, v99\n\t"
"v_mfma_f32_16x16x32_bf16 v[200:203], a[156:159], a[172:175], v[200:203]\n\t"
"v_fma_f32 v98, v116, s64, -v18\n\t"
"v_fma_f32 v99, v117, s64, -v19\n\t"
"v_mfma_f32_16x16x32_bf16 v[200:203], a[164:167], a[176:179], v[200:203]\n\t"
"s_add_u32 m0, s24, 896\n\t"
"s_add_u32 s28, s21, 0\n\t"
"v_accvgpr_write_b32 a12, v98\n\t"
"v_mfma_f32_16x16x32_bf16 v[208:211], a[148:151], a[172:175], v[208:211]\n\t"
"s_waitcnt lgkmcnt(5)\n\t"
"v_fma_f32 v98, v118, s64, -v20\n\t"
"v_accvgpr_write_b32 a13, v99\n\t"
"v_mfma_f32_16x16x32_bf16 v[208:211], a[156:159], a[176:179], v[208:211]\n\t"
"v_fma_f32 v99, v119, s64, -v21\n\t"
"v_mfma_f32_16x16x32_bf16 v[208:211], a[164:167], a[180:183], v[208:211]\n\t"
"s_waitcnt lgkmcnt(0)\n\t"
"s_barrier\n\t"
"v_mfma_f32_16x16x32_bf16 v[216:219], a[148:151], a[176:179], v[216:219]\n\t"
"v_cvt_pk_bf16_f32 v232, v232, v233\n\t"
"v_mfma_f32_16x16x32_bf16 v[216:219], a[156:159], a[180:183], v[216:219]\n\t"
"buffer_load_dwordx4 v247, s[12:15], s28 offen offset:128 nt lds\n\t"
"v_mfma_f32_16x16x32_bf16 v[216:219], a[164:167], a[184:187], v[216:219]\n\t"
"s_add_u32 m0, s24, 7040\n\t"
"s_add_u32 s28, s21, 196608\n\t"
"ds_write2st64_b64 v245, v[228:229], v[230:231] offset0:0 offset1:1\n\t"
"v_cvt_pk_bf16_f32 v233, v234, v235\n\t"
"v_mfma_f32_16x16x32_bf16 a[4:7], a[148:151], a[180:183], a[4:7]\n\t"
"v_accvgpr_write_b32 a14, v98\n\t"
"v_accvgpr_write_b32 a15, v99\n\t"
"v_mfma_f32_16x16x32_bf16 a[4:7], a[156:159], a[184:187], a[4:7]\n\t"
"ds_read_b64_tr_b16 v[228:229], v246 offset:32256\n\t"
"v_fma_f32 v98, v120, s64, -v22\n\t"
"v_mfma_f32_16x16x32_bf16 a[4:7], a[164:167], a[188:191], a[4:7]\n\t"
"buffer_load_dwordx4 v247, s[12:15], s28 offen offset:128 nt lds\n\t"
"v_fma_f32 v99, v121, s64, -v23\n\t"
"s_add_u32 m0, s24, 13184\n\t"
"s_add_u32 s28, s21, 393216\n\t"
"v_mfma_f32_16x16x32_bf16 a[12:15], a[148:151], a[184:187], a[12:15]\n\t"
"ds_read_b64_tr_b16 v[230:231], v246 offset:35328\n\t"
"v_accvgpr_write_b32 a20, v98\n\t"
"v_mfma_f32_16x16x32_bf16 a[12:15], a[156:159], a[188:191], a[12:15]\n\t"
"v_fma_f32 v98, v122, s64, -v24\n\t"
"v_accvgpr_write_b32 a21, v99\n\t"
"v_mfma_f32_16x16x32_bf16 a[12:15], a[164:167], a[192:195], a[12:15]\n\t"
"ds_write2st64_b64 v245, v[224:225], v[226:227] offset0:2 offset1:3\n\t"
"buffer_load_dwordx4 v247, s[12:15], s28 offen offset:128 nt lds\n\t"
"v_fma_f32 v99, v123, s64, -v25\n\t"
"v_accvgpr_write_b32 a22, v98\n\t"
"v_accvgpr_write_b32 a23, v99\n\t"
"v_fma_f32 v98, v124, s64, -v26\n\t"
"v_fma_f32 v99, v125, s64, -v27\n\t"
"s_add_u32 m0, s24, 20864\n\t"
"s_add_u32 s28, s21, 589824\n\t"
"v_mfma_f32_16x16x32_bf16 a[20:23], a[148:151], a[188:191], a[20:23]\n\t"
"buffer_load_dwordx4 v247, s[12:15], s28 offen offset:128 nt lds\n\t"
"v_mfma_f32_16x16x32_bf16 a[20:23], a[156:159], a[192:195], a[20:23]\n\t"
"s_add_u32 m0, s24, 60800\n\t"
"s_add_u32 s28, s21, 1769472\n\t"
"ds_read_b64_tr_b16 v[224:225], v246 offset:39936\n\t"
"v_mfma_f32_16x16x32_bf16 a[20:23], a[164:167], a[196:199], a[20:23]\n\t"
"s_waitcnt lgkmcnt(2)\n\t"
"ds_write_b64 v245, v[232:233] offset:2048\n\t"
"ds_read_b64_tr_b16 v[226:227], v246 offset:43008\n\t"
"v_accvgpr_write_b32 a28, v98\n\t"
"v_fma_f32 v98, v126, s64, -v28\n\t"
"v_accvgpr_write_b32 a29, v99\n\t"
"v_fma_f32 v99, v127, s64, -v29\n\t"
"v_accvgpr_write_b32 a30, v98\n\t"
"v_accvgpr_write_b32 a31, v99\n\t"
"v_mfma_f32_16x16x32_bf16 v[228:231], v[228:231], v[220:223], 0\n\t"
"buffer_load_dwordx4 v247, s[12:15], s28 offen offset:128 nt lds\n\t"
"v_fma_f32 v98, v128, s64, -v30\n\t"
"v_fma_f32 v99, v129, s64, -v31\n\t"
"s_add_u32 m0, s24, 66944\n\t"
"s_add_u32 s28, s21, 1966080\n\t"
"v_mfma_f32_16x16x32_bf16 a[28:31], a[148:151], a[192:195], a[28:31]\n\t"
"buffer_load_dwordx4 v247, s[12:15], s28 offen offset:128 nt lds\n\t"
"v_mfma_f32_16x16x32_bf16 a[28:31], a[156:159], a[196:199], a[28:31]\n\t"
"ds_read_b64_tr_b16 v[232:233], v246 offset:46080\n\t"
"v_accvgpr_write_b32 a36, v98\n\t"
"v_mfma_f32_16x16x32_bf16 a[28:31], a[164:167], a[200:203], a[28:31]\n\t"
"s_waitcnt lgkmcnt(1)\n\t"
"ds_read_b64_tr_b16 v[234:235], v246 offset:49152\n\t"
"v_fma_f32 v98, v130, s64, -v32\n\t"
"v_accvgpr_write_b32 a37, v99\n\t"
"v_fma_f32 v99, v131, s64, -v33\n\t"
"v_accvgpr_write_b32 a38, v98\n\t"
"v_accvgpr_write_b32 a39, v99\n\t"
"v_mfma_f32_16x16x32_bf16 v[224:227], v[224:227], v[220:223], 0\n\t"
"v_fma_f32 v98, v132, s64, -v34\n\t"
"v_fma_f32 v99, v133, s64, -v35\n\t"
"v_mfma_f32_16x16x32_bf16 a[36:39], a[148:151], a[196:199], a[36:39]\n\t"
"v_accvgpr_write_b32 a44, v98\n\t"
"v_fma_f32 v98, v134, s64, -v36\n\t"
"v_mfma_f32_16x16x32_bf16 a[36:39], a[156:159], a[200:203], a[36:39]\n\t"
"v_accvgpr_write_b32 a45, v99\n\t"
"v_fma_f32 v99, v135, s64, -v37\n\t"
"v_mfma_f32_16x16x32_bf16 a[36:39], a[164:167], a[204:207], a[36:39]\n\t"
"s_waitcnt lgkmcnt(0)\n\t"
"v_accvgpr_write_b32 a46, v98\n\t"
"v_accvgpr_write_b32 a47, v99\n\t"
"v_fma_f32 v98, v136, s64, -v38\n\t"
"v_fma_f32 v99, v137, s64, -v39\n\t"
"v_mfma_f32_16x16x32_bf16 v[232:235], v[232:235], v[220:223], 0\n\t"
"s_waitcnt vmcnt(18)\n\t"
"v_cvt_pk_bf16_f32 v228, v228, v229\n\t"
"v_cvt_pk_bf16_f32 v229, v230, v231\n\t"
"v_mfma_f32_16x16x32_bf16 a[44:47], v[240:243], a[168:171], a[44:47]\n\t"
"v_accvgpr_write_b32 a52, v98\n\t"
"v_fma_f32 v98, v138, s64, -v40\n\t"
"v_mfma_f32_16x16x32_bf16 a[44:47], a[208:211], a[172:175], a[44:47]\n\t"
"v_accvgpr_write_b32 a53, v99\n\t"
"v_fma_f32 v99, v139, s64, -v41\n\t"
"v_mfma_f32_16x16x32_bf16 a[44:47], v[248:251], a[176:179], a[44:47]\n\t"
"v_accvgpr_write_b32 a54, v98\n\t"
"v_accvgpr_write_b32 a55, v99\n\t"
"v_fma_f32 v98, v140, s64, -v42\n\t"
"v_fma_f32 v99, v141, s64, -v43\n\t"
"v_mfma_f32_16x16x32_bf16 a[52:55], v[240:243], a[172:175], a[52:55]\n\t"
"v_accvgpr_write_b32 a60, v98\n\t"
"v_fma_f32 v98, v142, s64, -v44\n\t"
"v_mfma_f32_16x16x32_bf16 a[52:55], a[208:211], a[176:179], a[52:55]\n\t"
"v_accvgpr_write_b32 a61, v99\n\t"
"v_fma_f32 v99, v143, s64, -v45\n\t"
"v_mfma_f32_16x16x32_bf16 a[52:55], v[248:251], a[180:183], a[52:55]\n\t"
"v_accvgpr_write_b32 a62, v98\n\t"
"v_accvgpr_write_b32 a63, v99\n\t"
"v_cvt_pk_bf16_f32 v230, v224, v225\n\t"
"v_cvt_pk_bf16_f32 v231, v226, v227\n\t"
"v_fma_f32 v98, v144, s64, -v46\n\t"
"v_fma_f32 v99, v145, s64, -v47\n\t"
"v_mfma_f32_16x16x32_bf16 a[60:63], v[240:243], a[176:179], a[60:63]\n\t"
"ds_read_b64_tr_b16 v[224:225], v246 offset:52224\n\t"
"v_accvgpr_write_b32 a68, v98\n\t"
"v_mfma_f32_16x16x32_bf16 a[60:63], a[208:211], a[180:183], a[60:63]\n\t"
"ds_read_b64_tr_b16 v[226:227], v246 offset:55296\n\t"
"v_fma_f32 v98, v146, s64, -v48\n\t"
"v_mfma_f32_16x16x32_bf16 a[60:63], v[248:251], a[184:187], a[60:63]\n\t"
"ds_write2st64_b64 v245, v[228:229], v[230:231] offset0:5 offset1:6\n\t"
"v_accvgpr_write_b32 a69, v99\n\t"
"v_fma_f32 v99, v147, s64, -v49\n\t"
"v_accvgpr_write_b32 a70, v98\n\t"
"v_accvgpr_write_b32 a71, v99\n\t"
"v_fma_f32 v98, v148, s64, -v50\n\t"
"v_fma_f32 v99, v149, s64, -v51\n\t"
"v_cvt_pk_bf16_f32 v232, v232, v233\n\t"
"v_cvt_pk_bf16_f32 v233, v234, v235\n\t"
"v_mfma_f32_16x16x32_bf16 a[68:71], v[240:243], a[180:183], a[68:71]\n\t"
"v_accvgpr_write_b32 a76, v98\n\t"
"v_fma_f32 v98, v150, s64, -v52\n\t"
"v_mfma_f32_16x16x32_bf16 a[68:71], a[208:211], a[184:187], a[68:71]\n\t"
"v_accvgpr_write_b32 a77, v99\n\t"
"v_fma_f32 v99, v151, s64, -v53\n\t"
"v_mfma_f32_16x16x32_bf16 a[68:71], v[248:251], a[188:191], a[68:71]\n\t"
"v_accvgpr_write_b32 a78, v98\n\t"
"v_accvgpr_write_b32 a79, v99\n\t"
"v_fma_f32 v98, v152, s64, -v54\n\t"
"v_fma_f32 v99, v153, s64, -v55\n\t"
"v_mfma_f32_16x16x32_bf16 a[76:79], v[240:243], a[184:187], a[76:79]\n\t"
"s_waitcnt lgkmcnt(1)\n\t"
"v_accvgpr_write_b32 a84, v98\n\t"
"v_fma_f32 v98, v154, s64, -v56\n\t"
"v_mfma_f32_16x16x32_bf16 a[76:79], a[208:211], a[188:191], a[76:79]\n\t"
"v_accvgpr_write_b32 a85, v99\n\t"
"v_fma_f32 v99, v155, s64, -v57\n\t"
"v_mfma_f32_16x16x32_bf16 a[76:79], v[248:251], a[192:195], a[76:79]\n\t"
"v_accvgpr_write_b32 a86, v98\n\t"
"v_accvgpr_write_b32 a87, v99\n\t"
"v_fma_f32 v98, v156, s64, -v58\n\t"
"v_fma_f32 v99, v157, s64, -v59\n\t"
"v_mfma_f32_16x16x32_bf16 v[224:227], v[224:227], v[220:223], 0\n\t"
"v_accvgpr_write_b32 a92, v98\n\t"
"v_fma_f32 v98, v158, s64, -v60\n\t"
"v_mfma_f32_16x16x32_bf16 a[84:87], v[240:243], a[188:191], a[84:87]\n\t"
"s_mov_b32 s20, 73728\n\t"
"v_accvgpr_write_b32 a93, v99\n\t"
"v_fma_f32 v99, v159, s64, -v61\n\t"
"v_mfma_f32_16x16x32_bf16 a[84:87], a[208:211], a[192:195], a[84:87]\n\t"
"v_accvgpr_write_b32 a94, v98\n\t"
"v_accvgpr_write_b32 a95, v99\n\t"
"v_mfma_f32_16x16x32_bf16 a[84:87], v[248:251], a[196:199], a[84:87]\n\t"
"v_fma_f32 v98, v160, s64, -v62\n\t"
"v_fma_f32 v99, v161, s64, -v63\n\t"
"v_mfma_f32_16x16x32_bf16 a[92:95], v[240:243], a[192:195], a[92:95]\n\t"
"v_accvgpr_write_b32 a100, v98\n\t"
"v_fma_f32 v98, v162, s64, -v64\n\t"
"v_mfma_f32_16x16x32_bf16 a[92:95], a[208:211], a[196:199], a[92:95]\n\t"
"v_accvgpr_write_b32 a101, v99\n\t"
"v_fma_f32 v99, v163, s64, -v65\n\t"
"v_mfma_f32_16x16x32_bf16 a[92:95], v[248:251], a[200:203], a[92:95]\n\t"
"v_accvgpr_write_b32 a102, v98\n\t"
"v_accvgpr_write_b32 a103, v99\n\t"
"v_fma_f32 v98, v164, s64, -v66\n\t"
"v_fma_f32 v99, v165, s64, -v67\n\t"
"v_cvt_pk_bf16_f32 v234, v224, v225\n\t"
"v_cvt_pk_bf16_f32 v235, v226, v227\n\t"
"v_mfma_f32_16x16x32_bf16 a[100:103], v[240:243], a[196:199], a[100:103]\n\t"
"v_accvgpr_write_b32 a108, v98\n\t"
"v_fma_f32 v98, v166, s64, -v68\n\t"
"v_mfma_f32_16x16x32_bf16 a[100:103], a[208:211], a[200:203], a[100:103]\n\t"
"v_accvgpr_write_b32 a109, v99\n\t"
"v_fma_f32 v99, v167, s64, -v69\n\t"
"v_mfma_f32_16x16x32_bf16 a[100:103], v[248:251], a[204:207], a[100:103]\n\t"
"ds_write2st64_b64 v245, v[232:233], v[234:235] offset0:7 offset1:8\n\t"
"v_accvgpr_write_b32 a110, v98\n\t"
"v_accvgpr_write_b32 a111, v99\n\t"
"v_fma_f32 v98, v168, s64, -v70\n\t"
"v_fma_f32 v99, v169, s64, -v71\n\t"
"s_waitcnt vmcnt(15)\n\t"
"v_mfma_f32_16x16x32_bf16 a[108:111], a[212:215], a[168:171], a[108:111]\n\t"
"buffer_load_dwordx4 v[240:243], v252, s[8:11], s20 offen offset:0 sc1\n\t"
"v_mfma_f32_16x16x32_bf16 a[108:111], a[216:219], a[172:175], a[108:111]\n\t"
"v_accvgpr_write_b32 a116, v98\n\t"
"v_mfma_f32_16x16x32_bf16 a[108:111], a[220:223], a[176:179], a[108:111]\n\t"
"v_accvgpr_write_b32 a117, v99\n\t"
"v_fma_f32 v98, v170, s64, -v72\n\t"
"v_accvgpr_write_b32 a118, v98\n\t"
"v_fma_f32 v99, v171, s64, -v73\n\t"
"v_accvgpr_write_b32 a119, v99\n\t"
"s_waitcnt lgkmcnt(0)\n\t"
"s_barrier\n\t"
"v_mfma_f32_16x16x32_bf16 a[116:119], a[212:215], a[172:175], a[116:119]\n\t"
"ds_read_b128 a[168:171], v244 offset:0\n\t"
"v_mfma_f32_16x16x32_bf16 a[116:119], a[216:219], a[176:179], a[116:119]\n\t"
"buffer_load_dwordx4 a[208:211], v252, s[8:11], s20 offen offset:1024 sc1\n\t"
"v_mfma_f32_16x16x32_bf16 a[116:119], a[220:223], a[180:183], a[116:119]\n\t"
"ds_read_b128 a[172:175], v244 offset:512\n\t"
"v_fma_f32 v98, v172, s64, -v74\n\t"
"v_fma_f32 v99, v173, s64, -v75\n\t"
"v_accvgpr_write_b32 a124, v98\n\t"
"v_fma_f32 v98, v174, s64, -v76\n\t"
"v_accvgpr_write_b32 a125, v99\n\t"
"v_fma_f32 v99, v175, s64, -v77\n\t"
"v_accvgpr_write_b32 a126, v98\n\t"
"v_accvgpr_write_b32 a127, v99\n\t"
"v_fma_f32 v98, v176, s64, -v78\n\t"
"v_fma_f32 v99, v177, s64, -v79\n\t"
"v_mfma_f32_16x16x32_bf16 a[124:127], a[212:215], a[176:179], a[124:127]\n\t"
"ds_read_b128 a[176:179], v244 offset:1024\n\t"
"v_accvgpr_write_b32 a132, v98\n\t"
"v_mfma_f32_16x16x32_bf16 a[124:127], a[216:219], a[180:183], a[124:127]\n\t"
"v_fma_f32 v98, v178, s64, -v80\n\t"
"v_accvgpr_write_b32 a133, v99\n\t"
"v_mfma_f32_16x16x32_bf16 a[124:127], a[220:223], a[184:187], a[124:127]\n\t"
"v_fma_f32 v99, v179, s64, -v81\n\t"
"v_accvgpr_write_b32 a134, v98\n\t"
"v_accvgpr_write_b32 a135, v99\n\t"
"v_fma_f32 v98, v180, s64, -v82\n\t"
"v_fma_f32 v99, v181, s64, -v83\n\t"
"v_mfma_f32_16x16x32_bf16 a[132:135], a[212:215], a[180:183], a[132:135]\n\t"
"ds_read_b128 a[180:183], v244 offset:1536\n\t"
"v_accvgpr_write_b32 a140, v98\n\t"
"v_mfma_f32_16x16x32_bf16 a[132:135], a[216:219], a[184:187], a[132:135]\n\t"
"v_fma_f32 v98, v182, s64, -v84\n\t"
"v_accvgpr_write_b32 a141, v99\n\t"
"v_mfma_f32_16x16x32_bf16 a[132:135], a[220:223], a[188:191], a[132:135]\n\t"
"buffer_load_dwordx4 v[248:251], v252, s[8:11], s20 offen offset:2048 sc1\n\t"
"v_fma_f32 v99, v183, s64, -v85\n\t"
"v_accvgpr_write_b32 a142, v98\n\t"
"v_accvgpr_write_b32 a143, v99\n\t"
"v_fma_f32 v98, v184, s64, -v86\n\t"
"v_fma_f32 v99, v185, s64, -v87\n\t"
"v_mfma_f32_16x16x32_bf16 a[140:143], a[212:215], a[184:187], a[140:143]\n\t"
"ds_read_b128 a[184:187], v244 offset:2048\n\t"
"v_accvgpr_write_b32 a148, v98\n\t"
"v_mfma_f32_16x16x32_bf16 a[140:143], a[216:219], a[188:191], a[140:143]\n\t"
"v_fma_f32 v98, v186, s64, -v88\n\t"
"v_accvgpr_write_b32 a149, v99\n\t"
"v_mfma_f32_16x16x32_bf16 a[140:143], a[220:223], a[192:195], a[140:143]\n\t"
"v_fma_f32 v99, v187, s64, -v89\n\t"
"v_accvgpr_write_b32 a150, v98\n\t"
"v_accvgpr_write_b32 a151, v99\n\t"
"v_fma_f32 v98, v188, s64, -v90\n\t"
"v_fma_f32 v99, v189, s64, -v91\n\t"
"v_mfma_f32_16x16x32_bf16 a[148:151], a[212:215], a[188:191], a[148:151]\n\t"
"s_mov_b32 s20, 86016\n\t"
"ds_read_b128 a[188:191], v244 offset:2560\n\t"
"v_accvgpr_write_b32 a156, v98\n\t"
"v_mfma_f32_16x16x32_bf16 a[148:151], a[216:219], a[192:195], a[148:151]\n\t"
"v_fma_f32 v98, v190, s64, -v92\n\t"
"v_accvgpr_write_b32 a157, v99\n\t"
"v_mfma_f32_16x16x32_bf16 a[148:151], a[220:223], a[196:199], a[148:151]\n\t"
"v_fma_f32 v99, v191, s64, -v93\n\t"
"v_accvgpr_write_b32 a158, v98\n\t"
"v_accvgpr_write_b32 a159, v99\n\t"
"v_fma_f32 v98, v192, s64, -v94\n\t"
"v_fma_f32 v99, v193, s64, -v95\n\t"
"v_mfma_f32_16x16x32_bf16 a[156:159], a[212:215], a[192:195], a[156:159]\n\t"
"ds_read_b128 a[192:195], v244 offset:3072\n\t"
"v_accvgpr_write_b32 a164, v98\n\t"
"v_mfma_f32_16x16x32_bf16 a[156:159], a[216:219], a[196:199], a[156:159]\n\t"
"v_fma_f32 v98, v194, s64, -v96\n\t"
"v_accvgpr_write_b32 a165, v99\n\t"
"v_mfma_f32_16x16x32_bf16 a[156:159], a[220:223], a[200:203], a[156:159]\n\t"
"v_fma_f32 v99, v195, s64, -v97\n\t"
"v_accvgpr_write_b32 a166, v98\n\t"
"v_accvgpr_write_b32 a167, v99\n\t"
"v_fma_f32 v0, v2, s52, v100\n\t"
"v_fma_f32 v1, v3, s52, v101\n\t"
"v_mfma_f32_16x16x32_bf16 a[164:167], a[212:215], a[196:199], a[164:167]\n\t"
"ds_read_b128 a[196:199], v244 offset:3584\n\t"
"v_fma_f32 v100, -v100, s66, v2\n\t"
"v_mfma_f32_16x16x32_bf16 a[164:167], a[216:219], a[200:203], a[164:167]\n\t"
"ds_read_b128 a[200:203], v244 offset:4096\n\t"
"v_fma_f32 v2, v4, s52, v102\n\t"
"v_mfma_f32_16x16x32_bf16 a[164:167], a[220:223], a[204:207], a[164:167]\n\t"
"ds_read_b128 a[204:207], v244 offset:4608\n\t"
"v_fma_f32 v101, -v101, s66, v3\n\t"
"v_fma_f32 v3, v5, s52, v103\n\t"
"v_fma_f32 v102, -v102, s66, v4\n\t"
"v_fma_f32 v103, -v103, s66, v5\n\t"
"v_fma_f32 v4, v6, s52, v104\n\t"
"v_fma_f32 v5, v7, s52, v105\n\t"
"Lst3_%=:\n\t"
"s_waitcnt vmcnt(15) lgkmcnt(7)\n\t"
"v_mfma_f32_16x16x32_bf16 v[0:3], a[224:227], a[168:171], v[0:3]\n\t"
"buffer_load_dwordx4 a[212:215], v252, s[8:11], s20 offen offset:0 sc1\n\t"
"v_mfma_f32_16x16x32_bf16 v[0:3], a[228:231], a[172:175], v[0:3]\n\t"
"buffer_load_dwordx4 a[216:219], v252, s[8:11], s20 offen offset:1024 sc1\n\t"
"v_mfma_f32_16x16x32_bf16 v[0:3], a[232:235], a[176:179], v[0:3]\n\t"
"v_fma_f32 v104, -v104, s66, v6\n\t"
"v_fma_f32 v6, v8, s52, v106\n\t"
"v_fma_f32 v105, -v105, s66, v7\n\t"
"v_fma_f32 v7, v9, s52, v107\n\t"
"v_fma_f32 v106, -v106, s66, v8\n\t"
"v_fma_f32 v107, -v107, s66, v9\n\t"
"v_fma_f32 v8, v10, s52, v108\n\t"
"v_fma_f32 v9, v11, s52, v109\n\t"
"s_waitcnt lgkmcnt(6)\n\t"
"v_mfma_f32_16x16x32_bf16 v[4:7], a[224:227], a[172:175], v[4:7]\n\t"
"buffer_load_dwordx4 a[220:223], v252, s[8:11], s20 offen offset:2048 sc1\n\t"
"v_mfma_f32_16x16x32_bf16 v[4:7], a[228:231], a[176:179], v[4:7]\n\t"
"v_fma_f32 v108, -v108, s66, v10\n\t"
"v_fma_f32 v10, v12, s52, v110\n\t"
"v_mfma_f32_16x16x32_bf16 v[4:7], a[232:235], a[180:183], v[4:7]\n\t"
"v_fma_f32 v109, -v109, s66, v11\n\t"
"v_fma_f32 v11, v13, s52, v111\n\t"
"v_fma_f32 v110, -v110, s66, v12\n\t"
"v_fma_f32 v111, -v111, s66, v13\n\t"
"v_fma_f32 v12, v14, s52, v112\n\t"
"v_fma_f32 v13, v15, s52, v113\n\t"
"s_waitcnt lgkmcnt(5)\n\t"
"v_mfma_f32_16x16x32_bf16 v[8:11], a[224:227], a[176:179], v[8:11]\n\t"
"v_fma_f32 v112, -v112, s66, v14\n\t"
"v_fma_f32 v14, v16, s52, v114\n\t"
"v_mfma_f32_16x16x32_bf16 v[8:11], a[228:231], a[180:183], v[8:11]\n\t"
"v_fma_f32 v113, -v113, s66, v15\n\t"
"v_fma_f32 v15, v17, s52, v115\n\t"
"v_mfma_f32_16x16x32_bf16 v[8:11], a[232:235], a[184:187], v[8:11]\n\t"
"v_fma_f32 v114, -v114, s66, v16\n\t"
"v_fma_f32 v115, -v115, s66, v17\n\t"
"v_fma_f32 v16, v18, s52, v116\n\t"
"v_fma_f32 v17, v19, s52, v117\n\t"
"s_waitcnt lgkmcnt(4)\n\t"
"v_mfma_f32_16x16x32_bf16 v[12:15], a[224:227], a[180:183], v[12:15]\n\t"
"v_fma_f32 v116, -v116, s66, v18\n\t"
"v_fma_f32 v18, v20, s52, v118\n\t"
"v_mfma_f32_16x16x32_bf16 v[12:15], a[228:231], a[184:187], v[12:15]\n\t"
"v_fma_f32 v117, -v117, s66, v19\n\t"
"v_fma_f32 v19, v21, s52, v119\n\t"
"v_mfma_f32_16x16x32_bf16 v[12:15], a[232:235], a[188:191], v[12:15]\n\t"
"v_fma_f32 v118, -v118, s66, v20\n\t"
"v_fma_f32 v119, -v119, s66, v21\n\t"
"v_fma_f32 v20, v22, s52, v120\n\t"
"v_fma_f32 v21, v23, s52, v121\n\t"
"s_waitcnt lgkmcnt(3)\n\t"
"v_mfma_f32_16x16x32_bf16 v[16:19], a[224:227], a[184:187], v[16:19]\n\t"
"v_fma_f32 v120, -v120, s66, v22\n\t"
"v_fma_f32 v22, v24, s52, v122\n\t"
"v_mfma_f32_16x16x32_bf16 v[16:19], a[228:231], a[188:191], v[16:19]\n\t"
"v_fma_f32 v121, -v121, s66, v23\n\t"
"v_fma_f32 v23, v25, s52, v123\n\t"
"v_mfma_f32_16x16x32_bf16 v[16:19], a[232:235], a[192:195], v[16:19]\n\t"
"ds_read_b64_tr_b16 v[228:229], v246 offset:512\n\t"
"v_fma_f32 v122, -v122, s66, v24\n\t"
"v_fma_f32 v123, -v123, s66, v25\n\t"
"v_fma_f32 v24, v26, s52, v124\n\t"
"v_fma_f32 v25, v27, s52, v125\n\t"
"s_waitcnt lgkmcnt(3)\n\t"
"v_mfma_f32_16x16x32_bf16 v[20:23], a[224:227], a[188:191], v[20:23]\n\t"
"s_mov_b32 s20, 98304\n\t"
"ds_read_b64_tr_b16 v[230:231], v246 offset:3584\n\t"
"v_fma_f32 v124, -v124, s66, v26\n\t"
"v_mfma_f32_16x16x32_bf16 v[20:23], a[228:231], a[192:195], v[20:23]\n\t"
"v_fma_f32 v26, v28, s52, v126\n\t"
"v_fma_f32 v125, -v125, s66, v27\n\t"
"v_mfma_f32_16x16x32_bf16 v[20:23], a[232:235], a[196:199], v[20:23]\n\t"
"v_fma_f32 v27, v29, s52, v127\n\t"
"v_fma_f32 v126, -v126, s66, v28\n\t"
"v_fma_f32 v127, -v127, s66, v29\n\t"
"v_fma_f32 v28, v30, s52, v128\n\t"
"v_fma_f32 v29, v31, s52, v129\n\t"
"s_waitcnt lgkmcnt(3)\n\t"
"v_mfma_f32_16x16x32_bf16 v[24:27], a[224:227], a[192:195], v[24:27]\n\t"
"v_fma_f32 v128, -v128, s66, v30\n\t"
"v_fma_f32 v30, v32, s52, v130\n\t"
"v_mfma_f32_16x16x32_bf16 v[24:27], a[228:231], a[196:199], v[24:27]\n\t"
"v_fma_f32 v129, -v129, s66, v31\n\t"
"v_fma_f32 v31, v33, s52, v131\n\t"
"v_mfma_f32_16x16x32_bf16 v[24:27], a[232:235], a[200:203], v[24:27]\n\t"
"v_fma_f32 v130, -v130, s66, v32\n\t"
"v_fma_f32 v131, -v131, s66, v33\n\t"
"v_fma_f32 v32, v34, s52, v132\n\t"
"v_fma_f32 v33, v35, s52, v133\n\t"
"s_waitcnt lgkmcnt(2)\n\t"
"v_mfma_f32_16x16x32_bf16 v[28:31], a[224:227], a[196:199], v[28:31]\n\t"
"buffer_load_dwordx4 a[224:227], v252, s[8:11], s20 offen offset:0 sc1\n\t"
"v_mfma_f32_16x16x32_bf16 v[28:31], a[228:231], a[200:203], v[28:31]\n\t"
"ds_read_b64_tr_b16 v[224:225], v246 offset:6656\n\t"
"v_fma_f32 v132, -v132, s66, v34\n\t"
"v_mfma_f32_16x16x32_bf16 v[28:31], a[232:235], a[204:207], v[28:31]\n\t"
"ds_read_b64_tr_b16 v[226:227], v246 offset:9728\n\t"
"v_fma_f32 v34, v36, s52, v134\n\t"
"v_fma_f32 v133, -v133, s66, v35\n\t"
"v_fma_f32 v35, v37, s52, v135\n\t"
"v_fma_f32 v134, -v134, s66, v36\n\t"
"v_fma_f32 v135, -v135, s66, v37\n\t"
"v_fma_f32 v36, v38, s52, v136\n\t"
"v_fma_f32 v37, v39, s52, v137\n\t"
"s_waitcnt vmcnt(16)\n\t"
"v_mfma_f32_16x16x32_bf16 v[32:35], a[236:239], a[168:171], v[32:35]\n\t"
"buffer_load_dwordx4 a[228:231], v252, s[8:11], s20 offen offset:1024 sc1\n\t"
"v_mfma_f32_16x16x32_bf16 v[32:35], a[240:243], a[172:175], v[32:35]\n\t"
"v_fma_f32 v136, -v136, s66, v38\n\t"
"v_fma_f32 v38, v40, s52, v138\n\t"
"v_mfma_f32_16x16x32_bf16 v[32:35], a[244:247], a[176:179], v[32:35]\n\t"
"v_fma_f32 v137, -v137, s66, v39\n\t"
"v_fma_f32 v39, v41, s52, v139\n\t"
"v_fma_f32 v138, -v138, s66, v40\n\t"
"v_fma_f32 v139, -v139, s66, v41\n\t"
"v_mfma_f32_16x16x32_bf16 v[36:39], a[236:239], a[172:175], v[36:39]\n\t"
"s_waitcnt lgkmcnt(2)\n\t"
"v_fma_f32 v40, v42, s52, v140\n\t"
"v_fma_f32 v41, v43, s52, v141\n\t"
"v_mfma_f32_16x16x32_bf16 v[36:39], a[240:243], a[176:179], v[36:39]\n\t"
"v_fma_f32 v140, -v140, s66, v42\n\t"
"v_fma_f32 v42, v44, s52, v142\n\t"
"v_mfma_f32_16x16x32_bf16 v[36:39], a[244:247], a[180:183], v[36:39]\n\t"
"v_fma_f32 v141, -v141, s66, v43\n\t"
"v_fma_f32 v43, v45, s52, v143\n\t"
"v_fma_f32 v142, -v142, s66, v44\n\t"
"v_fma_f32 v143, -v143, s66, v45\n\t"
"v_mfma_f32_16x16x32_bf16 v[228:231], v[228:231], v[220:223], 0\n\t"
"buffer_load_dwordx4 a[232:235], v252, s[8:11], s20 offen offset:2048 sc1\n\t"
"v_fma_f32 v44, v46, s52, v144\n\t"
"v_fma_f32 v45, v47, s52, v145\n\t"
"v_mfma_f32_16x16x32_bf16 v[40:43], a[236:239], a[176:179], v[40:43]\n\t"
"ds_read_b64_tr_b16 v[232:233], v246 offset:12800\n\t"
"v_fma_f32 v144, -v144, s66, v46\n\t"
"v_mfma_f32_16x16x32_bf16 v[40:43], a[240:243], a[180:183], v[40:43]\n\t"
"ds_read_b64_tr_b16 v[234:235], v246 offset:15872\n\t"
"v_fma_f32 v46, v48, s52, v146\n\t"
"v_mfma_f32_16x16x32_bf16 v[40:43], a[244:247], a[184:187], v[40:43]\n\t"
"v_fma_f32 v145, -v145, s66, v47\n\t"
"v_fma_f32 v47, v49, s52, v147\n\t"
"v_fma_f32 v146, -v146, s66, v48\n\t"
"v_fma_f32 v147, -v147, s66, v49\n\t"
"v_mfma_f32_16x16x32_bf16 v[44:47], a[236:239], a[180:183], v[44:47]\n\t"
"s_waitcnt lgkmcnt(2)\n\t"
"v_fma_f32 v48, v50, s52, v148\n\t"
"v_fma_f32 v49, v51, s52, v149\n\t"
"v_mfma_f32_16x16x32_bf16 v[44:47], a[240:243], a[184:187], v[44:47]\n\t"
"v_fma_f32 v148, -v148, s66, v50\n\t"
"v_fma_f32 v50, v52, s52, v150\n\t"
"v_mfma_f32_16x16x32_bf16 v[44:47], a[244:247], a[188:191], v[44:47]\n\t"
"v_fma_f32 v149, -v149, s66, v51\n\t"
"v_fma_f32 v51, v53, s52, v151\n\t"
"v_fma_f32 v150, -v150, s66, v52\n\t"
"v_fma_f32 v151, -v151, s66, v53\n\t"
"v_mfma_f32_16x16x32_bf16 v[224:227], v[224:227], v[220:223], 0\n\t"
"v_fma_f32 v52, v54, s52, v152\n\t"
"v_fma_f32 v53, v55, s52, v153\n\t"
"v_mfma_f32_16x16x32_bf16 v[48:51], a[236:239], a[184:187], v[48:51]\n\t"
"v_fma_f32 v152, -v152, s66, v54\n\t"
"v_fma_f32 v54, v56, s52, v154\n\t"
"v_mfma_f32_16x16x32_bf16 v[48:51], a[240:243], a[188:191], v[48:51]\n\t"
"v_fma_f32 v153, -v153, s66, v55\n\t"
"v_fma_f32 v55, v57, s52, v155\n\t"
"v_mfma_f32_16x16x32_bf16 v[48:51], a[244:247], a[192:195], v[48:51]\n\t"
"v_fma_f32 v154, -v154, s66, v56\n\t"
"v_fma_f32 v155, -v155, s66, v57\n\t"
"v_cvt_pk_bf16_f32 v228, v228, v229\n\t"
"v_cvt_pk_bf16_f32 v229, v230, v231\n\t"
"v_fma_f32 v56, v58, s52, v156\n\t"
"v_fma_f32 v57, v59, s52, v157\n\t"
"v_mfma_f32_16x16x32_bf16 v[52:55], a[236:239], a[188:191], v[52:55]\n\t"
"v_fma_f32 v156, -v156, s66, v58\n\t"
"v_fma_f32 v58, v60, s52, v158\n\t"
"v_mfma_f32_16x16x32_bf16 v[52:55], a[240:243], a[192:195], v[52:55]\n\t"
"s_mov_b32 s20, 331776\n\t"
"v_fma_f32 v157, -v157, s66, v59\n\t"
"v_fma_f32 v59, v61, s52, v159\n\t"
"v_mfma_f32_16x16x32_bf16 v[52:55], a[244:247], a[196:199], v[52:55]\n\t"
"v_fma_f32 v158, -v158, s66, v60\n\t"
"v_fma_f32 v159, -v159, s66, v61\n\t"
"v_mfma_f32_16x16x32_bf16 v[56:59], a[236:239], a[192:195], v[56:59]\n\t"
"s_waitcnt lgkmcnt(0)\n\t"
"v_fma_f32 v60, v62, s52, v160\n\t"
"v_fma_f32 v61, v63, s52, v161\n\t"
"v_mfma_f32_16x16x32_bf16 v[56:59], a[240:243], a[196:199], v[56:59]\n\t"
"v_fma_f32 v160, -v160, s66, v62\n\t"
"v_fma_f32 v62, v64, s52, v162\n\t"
"v_mfma_f32_16x16x32_bf16 v[56:59], a[244:247], a[200:203], v[56:59]\n\t"
"v_fma_f32 v161, -v161, s66, v63\n\t"
"v_fma_f32 v63, v65, s52, v163\n\t"
"v_fma_f32 v162, -v162, s66, v64\n\t"
"v_fma_f32 v163, -v163, s66, v65\n\t"
"v_cvt_pk_bf16_f32 v230, v224, v225\n\t"
"v_cvt_pk_bf16_f32 v231, v226, v227\n\t"
"v_mfma_f32_16x16x32_bf16 v[232:235], v[232:235], v[220:223], 0\n\t"
"ds_read_b64_tr_b16 v[224:225], v246 offset:20480\n\t"
"v_fma_f32 v64, v66, s52, v164\n\t"
"v_fma_f32 v65, v67, s52, v165\n\t"
"v_mfma_f32_16x16x32_bf16 v[60:63], a[236:239], a[196:199], v[60:63]\n\t"
"ds_read_b64_tr_b16 v[226:227], v246 offset:23552\n\t"
"v_fma_f32 v164, -v164, s66, v66\n\t"
"v_mfma_f32_16x16x32_bf16 v[60:63], a[240:243], a[200:203], v[60:63]\n\t"
"v_fma_f32 v66, v68, s52, v166\n\t"
"v_fma_f32 v165, -v165, s66, v67\n\t"
"v_mfma_f32_16x16x32_bf16 v[60:63], a[244:247], a[204:207], v[60:63]\n\t"
"v_fma_f32 v67, v69, s52, v167\n\t"
"v_fma_f32 v166, -v166, s66, v68\n\t"
"v_fma_f32 v167, -v167, s66, v69\n\t"
"v_fma_f32 v68, v70, s52, v168\n\t"
"v_fma_f32 v69, v71, s52, v169\n\t"
"s_waitcnt vmcnt(15)\n\t"
"v_mfma_f32_16x16x32_bf16 v[64:67], a[248:251], a[168:171], v[64:67]\n\t"
"buffer_load_dwordx4 a[236:239], v252, s[8:11], s20 offen offset:0 sc1\n\t"
"v_mfma_f32_16x16x32_bf16 v[64:67], a[252:255], a[172:175], v[64:67]\n\t"
"buffer_load_dwordx4 a[240:243], v252, s[8:11], s20 offen offset:1024 sc1\n\t"
"v_mfma_f32_16x16x32_bf16 v[64:67], v[236:239], a[176:179], v[64:67]\n\t"
"ds_read_b128 a[168:171], v244 offset:9216\n\t"
"v_fma_f32 v168, -v168, s66, v70\n\t"
"v_fma_f32 v70, v72, s52, v170\n\t"
"v_fma_f32 v169, -v169, s66, v71\n\t"
"v_fma_f32 v71, v73, s52, v171\n\t"
"v_fma_f32 v170, -v170, s66, v72\n\t"
"v_fma_f32 v171, -v171, s66, v73\n\t"
"v_mfma_f32_16x16x32_bf16 v[68:71], a[248:251], a[172:175], v[68:71]\n\t"
"s_waitcnt lgkmcnt(1)\n\t"
"ds_read_b128 a[172:175], v244 offset:9728\n\t"
"v_fma_f32 v72, v74, s52, v172\n\t"
"v_mfma_f32_16x16x32_bf16 v[68:71], a[252:255], a[176:179], v[68:71]\n\t"
"v_fma_f32 v73, v75, s52, v173\n\t"
"v_fma_f32 v172, -v172, s66, v74\n\t"
"v_mfma_f32_16x16x32_bf16 v[68:71], v[236:239], a[180:183], v[68:71]\n\t"
"v_fma_f32 v74, v76, s52, v174\n\t"
"v_fma_f32 v173, -v173, s66, v75\n\t"
"v_fma_f32 v75, v77, s52, v175\n\t"
"v_fma_f32 v174, -v174, s66, v76\n\t"
"v_fma_f32 v175, -v175, s66, v77\n\t"
"v_cvt_pk_bf16_f32 v232, v232, v233\n\t"
"v_cvt_pk_bf16_f32 v233, v234, v235\n\t"
"v_mfma_f32_16x16x32_bf16 v[224:227], v[224:227], v[220:223], 0\n\t"
"buffer_load_dwordx4 a[244:247], v252, s[8:11], s20 offen offset:2048 sc1\n\t"
"v_fma_f32 v76, v78, s52, v176\n\t"
"v_fma_f32 v77, v79, s52, v177\n\t"
"v_mfma_f32_16x16x32_bf16 v[72:75], a[248:251], a[176:179], v[72:75]\n\t"
"ds_read_b128 a[176:179], v244 offset:10240\n\t"
"v_fma_f32 v176, -v176, s66, v78\n\t"
"v_mfma_f32_16x16x32_bf16 v[72:75], a[252:255], a[180:183], v[72:75]\n\t"
"v_fma_f32 v78, v80, s52, v178\n\t"
"v_fma_f32 v177, -v177, s66, v79\n\t"
"v_mfma_f32_16x16x32_bf16 v[72:75], v[236:239], a[184:187], v[72:75]\n\t"
"v_fma_f32 v79, v81, s52, v179\n\t"
"v_fma_f32 v178, -v178, s66, v80\n\t"
"v_fma_f32 v179, -v179, s66, v81\n\t"
"v_fma_f32 v80, v82, s52, v180\n\t"
"v_fma_f32 v81, v83, s52, v181\n\t"
"v_mfma_f32_16x16x32_bf16 v[76:79], a[248:251], a[180:183], v[76:79]\n\t"
"ds_read_b128 a[180:183], v244 offset:10752\n\t"
"v_fma_f32 v180, -v180, s66, v82\n\t"
"v_mfma_f32_16x16x32_bf16 v[76:79], a[252:255], a[184:187], v[76:79]\n\t"
"v_fma_f32 v82, v84, s52, v182\n\t"
"v_fma_f32 v181, -v181, s66, v83\n\t"
"v_mfma_f32_16x16x32_bf16 v[76:79], v[236:239], a[188:191], v[76:79]\n\t"
"v_fma_f32 v83, v85, s52, v183\n\t"
"v_fma_f32 v182, -v182, s66, v84\n\t"
"v_fma_f32 v183, -v183, s66, v85\n\t"
"v_fma_f32 v84, v86, s52, v184\n\t"
"v_fma_f32 v85, v87, s52, v185\n\t"
"v_mfma_f32_16x16x32_bf16 v[80:83], a[248:251], a[184:187], v[80:83]\n\t"
"ds_read_b128 a[184:187], v244 offset:11264\n\t"
"v_fma_f32 v184, -v184, s66, v86\n\t"
"v_mfma_f32_16x16x32_bf16 v[80:83], a[252:255], a[188:191], v[80:83]\n\t"
"s_mov_b32 s20, 344064\n\t"
"v_fma_f32 v86, v88, s52, v186\n\t"
"v_fma_f32 v185, -v185, s66, v87\n\t"
"v_mfma_f32_16x16x32_bf16 v[80:83], v[236:239], a[192:195], v[80:83]\n\t"
"v_fma_f32 v87, v89, s52, v187\n\t"
"v_fma_f32 v186, -v186, s66, v88\n\t"
"v_fma_f32 v187, -v187, s66, v89\n\t"
"v_cvt_pk_bf16_f32 v234, v224, v225\n\t"
"v_cvt_pk_bf16_f32 v235, v226, v227\n\t"
"v_fma_f32 v88, v90, s52, v188\n\t"
"v_fma_f32 v89, v91, s52, v189\n\t"
"v_mfma_f32_16x16x32_bf16 v[84:87], a[248:251], a[188:191], v[84:87]\n\t"
"ds_read_b64_tr_b16 v[224:225], v246 offset:26624\n\t"
"v_fma_f32 v188, -v188, s66, v90\n\t"
"v_mfma_f32_16x16x32_bf16 v[84:87], a[252:255], a[192:195], v[84:87]\n\t"
"ds_read_b64_tr_b16 v[226:227], v246 offset:29696\n\t"
"v_fma_f32 v90, v92, s52, v190\n\t"
"v_mfma_f32_16x16x32_bf16 v[84:87], v[236:239], a[196:199], v[84:87]\n\t"
"ds_read_b128 a[188:191], v244 offset:11776\n\t"
"v_fma_f32 v189, -v189, s66, v91\n\t"
"v_fma_f32 v91, v93, s52, v191\n\t"
"v_fma_f32 v190, -v190, s66, v92\n\t"
"v_fma_f32 v191, -v191, s66, v93\n\t"
"v_fma_f32 v92, v94, s52, v192\n\t"
"v_fma_f32 v93, v95, s52, v193\n\t"
"v_mfma_f32_16x16x32_bf16 v[88:91], a[248:251], a[192:195], v[88:91]\n\t"
"ds_read_b128 a[192:195], v244 offset:12288\n\t"
"v_fma_f32 v192, -v192, s66, v94\n\t"
"v_mfma_f32_16x16x32_bf16 v[88:91], a[252:255], a[196:199], v[88:91]\n\t"
"v_fma_f32 v94, v96, s52, v194\n\t"
"v_fma_f32 v193, -v193, s66, v95\n\t"
"v_mfma_f32_16x16x32_bf16 v[88:91], v[236:239], a[200:203], v[88:91]\n\t"
"v_fma_f32 v95, v97, s52, v195\n\t"
"v_fma_f32 v194, -v194, s66, v96\n\t"
"v_fma_f32 v195, -v195, s66, v97\n\t"
"v_mfma_f32_16x16x32_bf16 v[92:95], a[248:251], a[196:199], v[92:95]\n\t"
"s_waitcnt lgkmcnt(2)\n\t"
"ds_read_b128 a[196:199], v244 offset:12800\n\t"
"v_mfma_f32_16x16x32_bf16 v[92:95], a[252:255], a[200:203], v[92:95]\n\t"
"buffer_load_dwordx4 v[96:99], v252, s[8:11], s20 offen offset:0 sc1\n\t"
"v_mfma_f32_16x16x32_bf16 v[92:95], v[236:239], a[204:207], v[92:95]\n\t"
"buffer_load_dwordx4 a[248:251], v252, s[8:11], s20 offen offset:1024 sc1\n\t"
"v_mfma_f32_16x16x32_bf16 v[224:227], v[224:227], v[220:223], 0\n\t"
"s_waitcnt vmcnt(11)\n\t"
"ds_read_b128 a[200:203], v244 offset:13312\n\t"
"v_mfma_f32_16x16x32_bf16 v[100:103], v[240:243], a[168:171], v[100:103]\n\t"
"ds_read_b128 a[204:207], v244 offset:13824\n\t"
"v_mfma_f32_16x16x32_bf16 v[100:103], a[208:211], a[172:175], v[100:103]\n\t"
"buffer_load_dwordx4 v[236:239], v252, s[8:11], s20 offen offset:2048 sc1\n\t"
"v_mfma_f32_16x16x32_bf16 v[100:103], v[248:251], a[176:179], v[100:103]\n\t"
"s_mov_b32 s20, 356352\n\t"
"v_mfma_f32_16x16x32_bf16 v[104:107], v[240:243], a[172:175], v[104:107]\n\t"
"buffer_load_dwordx4 a[252:255], v252, s[8:11], s20 offen offset:0 sc1\n\t"
"v_mfma_f32_16x16x32_bf16 v[104:107], a[208:211], a[176:179], v[104:107]\n\t"
"v_cvt_pk_bf16_f32 v224, v224, v225\n\t"
"v_mfma_f32_16x16x32_bf16 v[104:107], v[248:251], a[180:183], v[104:107]\n\t"
"s_waitcnt lgkmcnt(0)\n\t"
"s_barrier\n\t"
"v_mfma_f32_16x16x32_bf16 v[108:111], v[240:243], a[176:179], v[108:111]\n\t"
"v_cvt_pk_bf16_f32 v225, v226, v227\n\t"
"v_mfma_f32_16x16x32_bf16 v[108:111], a[208:211], a[180:183], v[108:111]\n\t"
"v_mfma_f32_16x16x32_bf16 v[108:111], v[248:251], a[184:187], v[108:111]\n\t"
"ds_write2st64_b64 v245, v[228:229], v[230:231] offset0:0 offset1:1\n\t"
"v_mfma_f32_16x16x32_bf16 v[112:115], v[240:243], a[180:183], v[112:115]\n\t"
"v_mfma_f32_16x16x32_bf16 v[112:115], a[208:211], a[184:187], v[112:115]\n\t"
"ds_read_b64_tr_b16 v[228:229], v246 offset:32768\n\t"
"v_mfma_f32_16x16x32_bf16 v[112:115], v[248:251], a[188:191], v[112:115]\n\t"
"ds_write2st64_b64 v245, v[232:233], v[234:235] offset0:2 offset1:3\n\t"
"v_mfma_f32_16x16x32_bf16 v[116:119], v[240:243], a[184:187], v[116:119]\n\t"
"v_mfma_f32_16x16x32_bf16 v[116:119], a[208:211], a[188:191], v[116:119]\n\t"
"ds_read_b64_tr_b16 v[230:231], v246 offset:35840\n\t"
"v_mfma_f32_16x16x32_bf16 v[116:119], v[248:251], a[192:195], v[116:119]\n\t"
"s_add_u32 m0, s24, 73088\n\t"
"s_add_u32 s28, s21, 2162688\n\t"
"ds_write_b64 v245, v[224:225] offset:2048\n\t"
"v_mfma_f32_16x16x32_bf16 v[120:123], v[240:243], a[188:191], v[120:123]\n\t"
"ds_read_b64_tr_b16 v[232:233], v246 offset:40448\n\t"
"v_mfma_f32_16x16x32_bf16 v[120:123], a[208:211], a[192:195], v[120:123]\n\t"
"ds_read_b64_tr_b16 v[234:235], v246 offset:43520\n\t"
"v_mfma_f32_16x16x32_bf16 v[120:123], v[248:251], a[196:199], v[120:123]\n\t"
"s_waitcnt lgkmcnt(3)\n\t"
"ds_read_b64_tr_b16 v[224:225], v246 offset:46592\n\t"
"v_mfma_f32_16x16x32_bf16 v[228:231], v[228:231], v[220:223], 0\n\t"
"ds_read_b64_tr_b16 v[226:227], v246 offset:49664\n\t"
"v_mfma_f32_16x16x32_bf16 v[124:127], v[240:243], a[192:195], v[124:127]\n\t"
"buffer_load_dwordx4 v247, s[12:15], s28 offen offset:128 nt lds\n\t"
"v_mfma_f32_16x16x32_bf16 v[124:127], a[208:211], a[196:199], v[124:127]\n\t"
"s_add_u32 m0, s24, 80768\n\t"
"s_add_u32 s28, s21, 2359296\n\t"
"v_mfma_f32_16x16x32_bf16 v[124:127], v[248:251], a[200:203], v[124:127]\n\t"
"s_waitcnt lgkmcnt(2)\n\t"
"buffer_load_dwordx4 v247, s[12:15], s28 offen offset:128 nt lds\n\t"
"s_add_u32 m0, s24, 27008\n\t"
"s_add_u32 s28, s21, 786432\n\t"
"v_mfma_f32_16x16x32_bf16 v[232:235], v[232:235], v[220:223], 0\n\t"
"buffer_load_dwordx4 v247, s[12:15], s28 offen offset:128 nt lds\n\t"
"v_mfma_f32_16x16x32_bf16 v[128:131], v[240:243], a[196:199], v[128:131]\n\t"
"s_waitcnt lgkmcnt(0)\n\t"
"s_add_u32 m0, s24, 86912\n\t"
"s_add_u32 s28, s21, 2555904\n\t"
"v_mfma_f32_16x16x32_bf16 v[128:131], a[208:211], a[200:203], v[128:131]\n\t"
"buffer_load_dwordx4 v247, s[12:15], s28 offen offset:128 nt lds\n\t"
"v_mfma_f32_16x16x32_bf16 v[128:131], v[248:251], a[204:207], v[128:131]\n\t"
"s_waitcnt vmcnt(14)\n\t"
"s_add_u32 m0, s24, 33152\n\t"
"s_add_u32 s28, s21, 983040\n\t"
"v_mfma_f32_16x16x32_bf16 v[224:227], v[224:227], v[220:223], 0\n\t"
"buffer_load_dwordx4 v247, s[12:15], s28 offen offset:128 nt lds\n\t"
"v_cvt_pk_bf16_f32 v228, v228, v229\n\t"
"v_cvt_pk_bf16_f32 v229, v230, v231\n\t"
"v_mfma_f32_16x16x32_bf16 v[132:135], a[212:215], a[168:171], v[132:135]\n\t"
"s_add_u32 m0, s24, 40832\n\t"
"s_add_u32 s28, s21, 1179648\n\t"
"v_cvt_pk_bf16_f32 v230, v232, v233\n\t"
"v_mfma_f32_16x16x32_bf16 v[132:135], a[216:219], a[172:175], v[132:135]\n\t"
"v_cvt_pk_bf16_f32 v231, v234, v235\n\t"
"v_mfma_f32_16x16x32_bf16 v[132:135], a[220:223], a[176:179], v[132:135]\n\t"
"buffer_load_dwordx4 v247, s[12:15], s28 offen offset:128 nt lds\n\t"
"v_mfma_f32_16x16x32_bf16 v[136:139], a[212:215], a[172:175], v[136:139]\n\t"
"ds_read_b64_tr_b16 v[232:233], v246 offset:52736\n\t"
"v_mfma_f32_16x16x32_bf16 v[136:139], a[216:219], a[176:179], v[136:139]\n\t"
"ds_read_b64_tr_b16 v[234:235], v246 offset:55808\n\t"
"v_cvt_pk_bf16_f32 v224, v224, v225\n\t"
"v_mfma_f32_16x16x32_bf16 v[136:139], a[220:223], a[180:183], v[136:139]\n\t"
"ds_write2st64_b64 v245, v[228:229], v[230:231] offset0:5 offset1:6\n\t"
"v_mfma_f32_16x16x32_bf16 v[140:143], a[212:215], a[176:179], v[140:143]\n\t"
"v_cvt_pk_bf16_f32 v225, v226, v227\n\t"
"v_mfma_f32_16x16x32_bf16 v[140:143], a[216:219], a[180:183], v[140:143]\n\t"
"v_mfma_f32_16x16x32_bf16 v[140:143], a[220:223], a[184:187], v[140:143]\n\t"
"v_mfma_f32_16x16x32_bf16 v[144:147], a[212:215], a[180:183], v[144:147]\n\t"
"v_mfma_f32_16x16x32_bf16 v[144:147], a[216:219], a[184:187], v[144:147]\n\t"
"v_mfma_f32_16x16x32_bf16 v[144:147], a[220:223], a[188:191], v[144:147]\n\t"
"v_mfma_f32_16x16x32_bf16 v[148:151], a[212:215], a[184:187], v[148:151]\n\t"
"s_waitcnt lgkmcnt(1)\n\t"
"v_mfma_f32_16x16x32_bf16 v[148:151], a[216:219], a[188:191], v[148:151]\n\t"
"v_mfma_f32_16x16x32_bf16 v[148:151], a[220:223], a[192:195], v[148:151]\n\t"
"v_mfma_f32_16x16x32_bf16 v[232:235], v[232:235], v[220:223], 0\n\t"
"v_mfma_f32_16x16x32_bf16 v[152:155], a[212:215], a[188:191], v[152:155]\n\t"
"v_mfma_f32_16x16x32_bf16 v[152:155], a[216:219], a[192:195], v[152:155]\n\t"
"v_mfma_f32_16x16x32_bf16 v[152:155], a[220:223], a[196:199], v[152:155]\n\t"
"v_mfma_f32_16x16x32_bf16 v[156:159], a[212:215], a[192:195], v[156:159]\n\t"
"v_mfma_f32_16x16x32_bf16 v[156:159], a[216:219], a[196:199], v[156:159]\n\t"
"v_cvt_pk_bf16_f32 v226, v232, v233\n\t"
"v_cvt_pk_bf16_f32 v227, v234, v235\n\t"
"v_mfma_f32_16x16x32_bf16 v[156:159], a[220:223], a[200:203], v[156:159]\n\t"
"ds_write2st64_b64 v245, v[224:225], v[226:227] offset0:7 offset1:8\n\t"
"v_mfma_f32_16x16x32_bf16 v[160:163], a[212:215], a[196:199], v[160:163]\n\t"
"s_waitcnt vmcnt(13)\n\t"
"v_mfma_f32_16x16x32_bf16 v[160:163], a[216:219], a[200:203], v[160:163]\n\t"
"v_mfma_f32_16x16x32_bf16 v[160:163], a[220:223], a[204:207], v[160:163]\n\t"
"v_mfma_f32_16x16x32_bf16 v[164:167], a[224:227], a[168:171], v[164:167]\n\t"
"v_mfma_f32_16x16x32_bf16 v[164:167], a[228:231], a[172:175], v[164:167]\n\t"
"v_mfma_f32_16x16x32_bf16 v[164:167], a[232:235], a[176:179], v[164:167]\n\t"
"s_waitcnt lgkmcnt(0)\n\t"
"s_barrier\n\t"
"v_mfma_f32_16x16x32_bf16 v[168:171], a[224:227], a[172:175], v[168:171]\n\t"
"ds_read_b128 a[168:171], v244 offset:0\n\t"
"v_mfma_f32_16x16x32_bf16 v[168:171], a[228:231], a[176:179], v[168:171]\n\t"
"buffer_load_dwordx4 v[240:243], v252, s[8:11], s20 offen offset:1024 sc1\n\t"
"v_mfma_f32_16x16x32_bf16 v[168:171], a[232:235], a[180:183], v[168:171]\n\t"
"ds_read_b128 a[172:175], v244 offset:512\n\t"
"v_mfma_f32_16x16x32_bf16 v[172:175], a[224:227], a[176:179], v[172:175]\n\t"
"ds_read_b128 a[176:179], v244 offset:1024\n\t"
"v_mfma_f32_16x16x32_bf16 v[172:175], a[228:231], a[180:183], v[172:175]\n\t"
"v_mfma_f32_16x16x32_bf16 v[172:175], a[232:235], a[184:187], v[172:175]\n\t"
"v_mfma_f32_16x16x32_bf16 v[176:179], a[224:227], a[180:183], v[176:179]\n\t"
"ds_read_b128 a[180:183], v244 offset:1536\n\t"
"v_mfma_f32_16x16x32_bf16 v[176:179], a[228:231], a[184:187], v[176:179]\n\t"
"v_mfma_f32_16x16x32_bf16 v[176:179], a[232:235], a[188:191], v[176:179]\n\t"
"v_mfma_f32_16x16x32_bf16 v[180:183], a[224:227], a[184:187], v[180:183]\n\t"
"ds_read_b128 a[184:187], v244 offset:2048\n\t"
"v_mfma_f32_16x16x32_bf16 v[180:183], a[228:231], a[188:191], v[180:183]\n\t"
"v_mfma_f32_16x16x32_bf16 v[180:183], a[232:235], a[192:195], v[180:183]\n\t"
"v_mfma_f32_16x16x32_bf16 v[184:187], a[224:227], a[188:191], v[184:187]\n\t"
"ds_read_b128 a[188:191], v244 offset:2560\n\t"
"v_mfma_f32_16x16x32_bf16 v[184:187], a[228:231], a[192:195], v[184:187]\n\t"
"v_mfma_f32_16x16x32_bf16 v[184:187], a[232:235], a[196:199], v[184:187]\n\t"
"v_mfma_f32_16x16x32_bf16 v[188:191], a[224:227], a[192:195], v[188:191]\n\t"
"ds_read_b128 a[192:195], v244 offset:3072\n\t"
"v_mfma_f32_16x16x32_bf16 v[188:191], a[228:231], a[196:199], v[188:191]\n\t"
"v_mfma_f32_16x16x32_bf16 v[188:191], a[232:235], a[200:203], v[188:191]\n\t"
"v_mfma_f32_16x16x32_bf16 v[192:195], a[224:227], a[196:199], v[192:195]\n\t"
"s_waitcnt vmcnt(11) lgkmcnt(4)\n\t"
"ds_read_b128 a[196:199], v244 offset:3584\n\t"
"v_mfma_f32_16x16x32_bf16 v[192:195], a[228:231], a[200:203], v[192:195]\n\t"
"ds_read_b128 a[200:203], v244 offset:4096\n\t"
"v_mfma_f32_16x16x32_bf16 v[192:195], a[232:235], a[204:207], v[192:195]\n\t"
"ds_read_b128 a[204:207], v244 offset:4608\n\t"
"Lst4_%=:\n\t"
"v_mfma_f32_16x16x32_bf16 v[0:3], a[236:239], a[168:171], v[0:3]\n\t"
"s_waitcnt lgkmcnt(6)\n\t"
"v_xor_b32 v246, 0xc0, v246\n\t"
"v_mfma_f32_16x16x32_bf16 v[0:3], a[240:243], a[172:175], v[0:3]\n\t"
"v_mfma_f32_16x16x32_bf16 v[0:3], a[244:247], a[176:179], v[0:3]\n\t"
"v_mfma_f32_16x16x32_bf16 v[4:7], a[236:239], a[172:175], v[4:7]\n\t"
"s_waitcnt lgkmcnt(5)\n\t"
"v_mfma_f32_16x16x32_bf16 v[4:7], a[240:243], a[176:179], v[4:7]\n\t"
"v_mfma_f32_16x16x32_bf16 v[4:7], a[244:247], a[180:183], v[4:7]\n\t"
"v_mfma_f32_16x16x32_bf16 v[8:11], a[236:239], a[176:179], v[8:11]\n\t"
"s_waitcnt lgkmcnt(4)\n\t"
"v_mfma_f32_16x16x32_bf16 v[8:11], a[240:243], a[180:183], v[8:11]\n\t"
"v_mfma_f32_16x16x32_bf16 v[8:11], a[244:247], a[184:187], v[8:11]\n\t"
"v_mfma_f32_16x16x32_bf16 v[12:15], a[236:239], a[180:183], v[12:15]\n\t"
"s_waitcnt lgkmcnt(3)\n\t"
"v_mfma_f32_16x16x32_bf16 v[12:15], a[240:243], a[184:187], v[12:15]\n\t"
"v_mfma_f32_16x16x32_bf16 v[12:15], a[244:247], a[188:191], v[12:15]\n\t"
"buffer_load_dwordx4 a[208:211], v252, s[8:11], s20 offen offset:2048 sc1\n\t"
"v_mfma_f32_16x16x32_bf16 v[16:19], a[236:239], a[184:187], v[16:19]\n\t"
"s_waitcnt lgkmcnt(2)\n\t"
"s_mov_b32 s20, 368640\n\t"
"v_mfma_f32_16x16x32_bf16 v[16:19], a[240:243], a[188:191], v[16:19]\n\t"
"v_mfma_f32_16x16x32_bf16 v[16:19], a[244:247], a[192:195], v[16:19]\n\t"
"ds_read_b64_tr_b16 v[228:229], v246 offset:512\n\t"
"v_mfma_f32_16x16x32_bf16 v[20:23], a[236:239], a[188:191], v[20:23]\n\t"
"buffer_load_dwordx4 v[248:251], v252, s[8:11], s20 offen offset:0 sc1\n\t"
"v_mfma_f32_16x16x32_bf16 v[20:23], a[240:243], a[192:195], v[20:23]\n\t"
"s_waitcnt lgkmcnt(2)\n\t"
"ds_read_b64_tr_b16 v[230:231], v246 offset:3584\n\t"
"v_mfma_f32_16x16x32_bf16 v[20:23], a[244:247], a[196:199], v[20:23]\n\t"
"v_mfma_f32_16x16x32_bf16 v[24:27], a[236:239], a[192:195], v[24:27]\n\t"
"buffer_load_dwordx4 a[212:215], v252, s[8:11], s20 offen offset:1024 sc1\n\t"
"v_mfma_f32_16x16x32_bf16 v[24:27], a[240:243], a[196:199], v[24:27]\n\t"
"s_waitcnt lgkmcnt(2)\n\t"
"v_mfma_f32_16x16x32_bf16 v[24:27], a[244:247], a[200:203], v[24:27]\n\t"
"v_mfma_f32_16x16x32_bf16 v[28:31], a[236:239], a[196:199], v[28:31]\n\t"
"s_waitcnt vmcnt(11)\n\t"
"ds_read_b64_tr_b16 v[232:233], v246 offset:6656\n\t"
"v_mfma_f32_16x16x32_bf16 v[28:31], a[240:243], a[200:203], v[28:31]\n\t"
"buffer_load_dwordx4 a[216:219], v252, s[8:11], s20 offen offset:2048 sc1\n\t"
"v_mfma_f32_16x16x32_bf16 v[28:31], a[244:247], a[204:207], v[28:31]\n\t"
"s_mov_b32 s20, 380928\n\t"
"ds_read_b64_tr_b16 v[234:235], v246 offset:9728\n\t"
"v_mfma_f32_16x16x32_bf16 v[32:35], v[96:99], a[168:171], v[32:35]\n\t"
"v_mfma_f32_16x16x32_bf16 v[32:35], a[248:251], a[172:175], v[32:35]\n\t"
"v_mfma_f32_16x16x32_bf16 v[32:35], v[236:239], a[176:179], v[32:35]\n\t"
"buffer_load_dwordx4 a[220:223], v252, s[8:11], s20 offen offset:0 sc1\n\t"
"v_mfma_f32_16x16x32_bf16 v[36:39], v[96:99], a[172:175], v[36:39]\n\t"
"s_waitcnt lgkmcnt(2)\n\t"
"v_mfma_f32_16x16x32_bf16 v[36:39], a[248:251], a[176:179], v[36:39]\n\t"
"v_mfma_f32_16x16x32_bf16 v[36:39], v[236:239], a[180:183], v[36:39]\n\t"
"v_mfma_f32_16x16x32_bf16 v[228:231], v[228:231], v[220:223], 0\n\t"
"buffer_load_dwordx4 a[224:227], v252, s[8:11], s20 offen offset:1024 sc1\n\t"
"v_mfma_f32_16x16x32_bf16 v[40:43], v[96:99], a[176:179], v[40:43]\n\t"
"ds_read_b64_tr_b16 v[224:225], v246 offset:12800\n\t"
"v_mfma_f32_16x16x32_bf16 v[40:43], a[248:251], a[180:183], v[40:43]\n\t"
"ds_read_b64_tr_b16 v[226:227], v246 offset:15872\n\t"
"v_mfma_f32_16x16x32_bf16 v[40:43], v[236:239], a[184:187], v[40:43]\n\t"
"v_mfma_f32_16x16x32_bf16 v[44:47], v[96:99], a[180:183], v[44:47]\n\t"
"buffer_load_dwordx4 a[228:231], v252, s[8:11], s20 offen offset:2048 sc1\n\t"
"v_mfma_f32_16x16x32_bf16 v[44:47], a[248:251], a[184:187], v[44:47]\n\t"
"s_waitcnt lgkmcnt(2)\n\t"
"v_cvt_pk_bf16_f32 v228, v228, v229\n\t"
"v_cvt_pk_bf16_f32 v229, v230, v231\n\t"
"v_mfma_f32_16x16x32_bf16 v[44:47], v[236:239], a[188:191], v[44:47]\n\t"
"s_mov_b32 s20, 393216\n\t"
"v_mfma_f32_16x16x32_bf16 v[232:235], v[232:235], v[220:223], 0\n\t"
"v_mfma_f32_16x16x32_bf16 v[48:51], v[96:99], a[184:187], v[48:51]\n\t"
"buffer_load_dwordx4 a[232:235], v252, s[8:11], s20 offen offset:0 sc1\n\t"
"v_mfma_f32_16x16x32_bf16 v[48:51], a[248:251], a[188:191], v[48:51]\n\t"
"v_mfma_f32_16x16x32_bf16 v[48:51], v[236:239], a[192:195], v[48:51]\n\t"
"v_mfma_f32_16x16x32_bf16 v[52:55], v[96:99], a[188:191], v[52:55]\n\t"
"v_mfma_f32_16x16x32_bf16 v[52:55], a[248:251], a[192:195], v[52:55]\n\t"
"buffer_load_dwordx4 a[236:239], v252, s[8:11], s20 offen offset:1024 sc1\n\t"
"v_mfma_f32_16x16x32_bf16 v[52:55], v[236:239], a[196:199], v[52:55]\n\t"
"v_cvt_pk_bf16_f32 v230, v232, v233\n\t"
"v_cvt_pk_bf16_f32 v231, v234, v235\n\t"
"v_mfma_f32_16x16x32_bf16 v[56:59], v[96:99], a[192:195], v[56:59]\n\t"
"s_waitcnt lgkmcnt(0)\n\t"
"ds_read_b64_tr_b16 v[232:233], v246 offset:20480\n\t"
"v_mfma_f32_16x16x32_bf16 v[56:59], a[248:251], a[196:199], v[56:59]\n\t"
"ds_read_b64_tr_b16 v[234:235], v246 offset:23552\n\t"
"v_mfma_f32_16x16x32_bf16 v[56:59], v[236:239], a[200:203], v[56:59]\n\t"
"buffer_load_dwordx4 a[240:243], v252, s[8:11], s20 offen offset:2048 sc1\n\t"
"v_mfma_f32_16x16x32_bf16 v[224:227], v[224:227], v[220:223], 0\n\t"
"s_mov_b32 s20, 405504\n\t"
"v_mfma_f32_16x16x32_bf16 v[60:63], v[96:99], a[196:199], v[60:63]\n\t"
"s_waitcnt vmcnt(9)\n\t"
"v_mfma_f32_16x16x32_bf16 v[60:63], a[248:251], a[200:203], v[60:63]\n\t"
"v_mfma_f32_16x16x32_bf16 v[60:63], v[236:239], a[204:207], v[60:63]\n\t"
"buffer_load_dwordx4 v[236:239], v252, s[8:11], s20 offen offset:0 sc1\n\t"
"v_mfma_f32_16x16x32_bf16 v[64:67], a[252:255], a[168:171], v[64:67]\n\t"
"ds_read_b128 a[168:171], v244 offset:9216\n\t"
"v_mfma_f32_16x16x32_bf16 v[64:67], v[240:243], a[172:175], v[64:67]\n\t"
"v_cvt_pk_bf16_f32 v224, v224, v225\n\t"
"v_cvt_pk_bf16_f32 v225, v226, v227\n\t"
"v_mfma_f32_16x16x32_bf16 v[64:67], a[208:211], a[176:179], v[64:67]\n\t"
"v_mfma_f32_16x16x32_bf16 v[68:71], a[252:255], a[172:175], v[68:71]\n\t"
"buffer_load_dwordx4 v[96:99], v252, s[8:11], s20 offen offset:1024 sc1\n\t"
"v_mfma_f32_16x16x32_bf16 v[68:71], v[240:243], a[176:179], v[68:71]\n\t"
"s_waitcnt lgkmcnt(1)\n\t"
"ds_read_b128 a[172:175], v244 offset:9728\n\t"
"v_mfma_f32_16x16x32_bf16 v[68:71], a[208:211], a[180:183], v[68:71]\n\t"
"buffer_load_dwordx4 a[244:247], v252, s[8:11], s20 offen offset:2048 sc1\n\t"
"v_mfma_f32_16x16x32_bf16 v[232:235], v[232:235], v[220:223], 0\n\t"
"s_mov_b32 s20, 417792\n\t"
"v_mfma_f32_16x16x32_bf16 v[72:75], a[252:255], a[176:179], v[72:75]\n\t"
"ds_read_b128 a[176:179], v244 offset:10240\n\t"
"v_mfma_f32_16x16x32_bf16 v[72:75], v[240:243], a[180:183], v[72:75]\n\t"
"buffer_load_dwordx4 a[248:251], v252, s[8:11], s20 offen offset:0 sc1\n\t"
"v_mfma_f32_16x16x32_bf16 v[72:75], a[208:211], a[184:187], v[72:75]\n\t"
"v_mfma_f32_16x16x32_bf16 v[76:79], a[252:255], a[180:183], v[76:79]\n\t"
"ds_read_b128 a[180:183], v244 offset:10752\n\t"
"v_mfma_f32_16x16x32_bf16 v[76:79], v[240:243], a[184:187], v[76:79]\n\t"
"v_cvt_pk_bf16_f32 v226, v232, v233\n\t"
"v_cvt_pk_bf16_f32 v227, v234, v235\n\t"
"v_mfma_f32_16x16x32_bf16 v[76:79], a[208:211], a[188:191], v[76:79]\n\t"
"ds_read_b64_tr_b16 v[232:233], v246 offset:26624\n\t"
"v_mfma_f32_16x16x32_bf16 v[80:83], a[252:255], a[184:187], v[80:83]\n\t"
"ds_read_b64_tr_b16 v[234:235], v246 offset:29696\n\t"
"v_mfma_f32_16x16x32_bf16 v[80:83], v[240:243], a[188:191], v[80:83]\n\t"
"ds_read_b128 a[184:187], v244 offset:11264\n\t"
"v_mfma_f32_16x16x32_bf16 v[80:83], a[208:211], a[192:195], v[80:83]\n\t"
"v_mfma_f32_16x16x32_bf16 v[84:87], a[252:255], a[188:191], v[84:87]\n\t"
"ds_read_b128 a[188:191], v244 offset:11776\n\t"
"v_mfma_f32_16x16x32_bf16 v[84:87], v[240:243], a[192:195], v[84:87]\n\t"
"v_mfma_f32_16x16x32_bf16 v[84:87], a[208:211], a[196:199], v[84:87]\n\t"
"v_mfma_f32_16x16x32_bf16 v[88:91], a[252:255], a[192:195], v[88:91]\n\t"
"ds_read_b128 a[192:195], v244 offset:12288\n\t"
"v_mfma_f32_16x16x32_bf16 v[88:91], v[240:243], a[196:199], v[88:91]\n\t"
"v_mfma_f32_16x16x32_bf16 v[88:91], a[208:211], a[200:203], v[88:91]\n\t"
"v_mfma_f32_16x16x32_bf16 v[92:95], a[252:255], a[196:199], v[92:95]\n\t"
"buffer_load_dwordx4 a[252:255], v252, s[8:11], s20 offen offset:1024 sc1\n\t"
"v_mfma_f32_16x16x32_bf16 v[92:95], v[240:243], a[200:203], v[92:95]\n\t"
"s_waitcnt vmcnt(11) lgkmcnt(3)\n\t"
"ds_read_b128 a[196:199], v244 offset:12800\n\t"
"v_mfma_f32_16x16x32_bf16 v[92:95], a[208:211], a[204:207], v[92:95]\n\t"
"ds_read_b128 a[200:203], v244 offset:13312\n\t"
"v_mfma_f32_16x16x32_bf16 v[232:235], v[232:235], v[220:223], 0\n\t"
"ds_read_b128 a[204:207], v244 offset:13824\n\t"
"v_mfma_f32_16x16x32_bf16 v[100:103], v[248:251], a[168:171], v[100:103]\n\t"
"buffer_load_dwordx4 v[240:243], v252, s[8:11], s20 offen offset:2048 sc1\n\t"
"v_mfma_f32_16x16x32_bf16 v[100:103], a[212:215], a[172:175], v[100:103]\n\t"
"s_mov_b32 s20, 430080\n\t"
"v_mfma_f32_16x16x32_bf16 v[100:103], a[216:219], a[176:179], v[100:103]\n\t"
"buffer_load_dwordx4 a[208:211], v252, s[8:11], s20 offen offset:0 sc1\n\t"
"v_mfma_f32_16x16x32_bf16 v[104:107], v[248:251], a[172:175], v[104:107]\n\t"
"s_waitcnt lgkmcnt(5)\n\t"
"v_mfma_f32_16x16x32_bf16 v[104:107], a[212:215], a[176:179], v[104:107]\n\t"
"v_cvt_pk_bf16_f32 v232, v232, v233\n\t"
"v_mfma_f32_16x16x32_bf16 v[104:107], a[216:219], a[180:183], v[104:107]\n\t"
"s_waitcnt lgkmcnt(0)\n\t"
"s_barrier\n\t"
"v_mfma_f32_16x16x32_bf16 v[108:111], v[248:251], a[176:179], v[108:111]\n\t"
"v_cvt_pk_bf16_f32 v233, v234, v235\n\t"
"v_mfma_f32_16x16x32_bf16 v[108:111], a[212:215], a[180:183], v[108:111]\n\t"
"v_mfma_f32_16x16x32_bf16 v[108:111], a[216:219], a[184:187], v[108:111]\n\t"
"ds_write2st64_b64 v245, v[228:229], v[230:231] offset0:0 offset1:1\n\t"
"v_mfma_f32_16x16x32_bf16 v[112:115], v[248:251], a[180:183], v[112:115]\n\t"
"v_mfma_f32_16x16x32_bf16 v[112:115], a[212:215], a[184:187], v[112:115]\n\t"
"ds_read_b64_tr_b16 v[228:229], v246 offset:32768\n\t"
"v_mfma_f32_16x16x32_bf16 v[112:115], a[216:219], a[188:191], v[112:115]\n\t"
"ds_write2st64_b64 v245, v[224:225], v[226:227] offset0:2 offset1:3\n\t"
"v_mfma_f32_16x16x32_bf16 v[116:119], v[248:251], a[184:187], v[116:119]\n\t"
"v_mfma_f32_16x16x32_bf16 v[116:119], a[212:215], a[188:191], v[116:119]\n\t"
"ds_read_b64_tr_b16 v[230:231], v246 offset:35840\n\t"
"v_mfma_f32_16x16x32_bf16 v[116:119], a[216:219], a[192:195], v[116:119]\n\t"
"ds_write_b64 v245, v[232:233] offset:2048\n\t"
"v_mfma_f32_16x16x32_bf16 v[120:123], v[248:251], a[188:191], v[120:123]\n\t"
"ds_read_b64_tr_b16 v[224:225], v246 offset:40448\n\t"
"v_mfma_f32_16x16x32_bf16 v[120:123], a[212:215], a[192:195], v[120:123]\n\t"
"ds_read_b64_tr_b16 v[226:227], v246 offset:43520\n\t"
"v_mfma_f32_16x16x32_bf16 v[120:123], a[216:219], a[196:199], v[120:123]\n\t"
"s_waitcnt lgkmcnt(3)\n\t"
"ds_read_b64_tr_b16 v[232:233], v246 offset:46592\n\t"
"v_mfma_f32_16x16x32_bf16 v[228:231], v[228:231], v[220:223], 0\n\t"
"ds_read_b64_tr_b16 v[234:235], v246 offset:49664\n\t"
"v_mfma_f32_16x16x32_bf16 v[124:127], v[248:251], a[192:195], v[124:127]\n\t"
"v_mfma_f32_16x16x32_bf16 v[124:127], a[212:215], a[196:199], v[124:127]\n\t"
"v_mfma_f32_16x16x32_bf16 v[124:127], a[216:219], a[200:203], v[124:127]\n\t"
"s_waitcnt lgkmcnt(2)\n\t"
"v_mfma_f32_16x16x32_bf16 v[224:227], v[224:227], v[220:223], 0\n\t"
"v_mfma_f32_16x16x32_bf16 v[128:131], v[248:251], a[196:199], v[128:131]\n\t"
"buffer_load_dwordx4 v[248:251], v252, s[8:11], s20 offen offset:1024 sc1\n\t"
"v_mfma_f32_16x16x32_bf16 v[128:131], a[212:215], a[200:203], v[128:131]\n\t"
"s_waitcnt vmcnt(11) lgkmcnt(0)\n\t"
"v_cvt_pk_bf16_f32 v228, v228, v229\n\t"
"v_cvt_pk_bf16_f32 v229, v230, v231\n\t"
"v_mfma_f32_16x16x32_bf16 v[128:131], a[216:219], a[204:207], v[128:131]\n\t"
"v_mfma_f32_16x16x32_bf16 v[232:235], v[232:235], v[220:223], 0\n\t"
"buffer_load_dwordx4 a[212:215], v252, s[8:11], s20 offen offset:2048 sc1\n\t"
"v_mfma_f32_16x16x32_bf16 v[132:135], a[220:223], a[168:171], v[132:135]\n\t"
"s_mov_b32 s20, 442368\n\t"
"v_cvt_pk_bf16_f32 v230, v224, v225\n\t"
"v_cvt_pk_bf16_f32 v231, v226, v227\n\t"
"v_mfma_f32_16x16x32_bf16 v[132:135], a[224:227], a[172:175], v[132:135]\n\t"
"s_add_u32 m0, s24, 46976\n\t"
"s_add_u32 s28, s21, 1376256\n\t"
"ds_read_b64_tr_b16 v[224:225], v246 offset:52736\n\t"
"v_mfma_f32_16x16x32_bf16 v[132:135], a[228:231], a[176:179], v[132:135]\n\t"
"buffer_load_dwordx4 a[216:219], v252, s[8:11], s20 offen offset:0 sc1\n\t"
"v_mfma_f32_16x16x32_bf16 v[136:139], a[220:223], a[172:175], v[136:139]\n\t"
"buffer_load_dwordx4 v247, s[12:15], s28 offen offset:128 nt lds\n\t"
"v_mfma_f32_16x16x32_bf16 v[136:139], a[224:227], a[176:179], v[136:139]\n\t"
"s_add_u32 m0, s24, 53120\n\t"
"s_add_u32 s28, s21, 1572864\n\t"
"ds_read_b64_tr_b16 v[226:227], v246 offset:55808\n\t"
"v_mfma_f32_16x16x32_bf16 v[136:139], a[228:231], a[180:183], v[136:139]\n\t"
"buffer_load_dwordx4 v247, s[12:15], s28 offen offset:128 nt lds\n\t"
"v_mfma_f32_16x16x32_bf16 v[140:143], a[220:223], a[176:179], v[140:143]\n\t"
"s_add_u32 m0, s24, 93056\n\t"
"s_add_u32 s28, s21, 2752512\n\t"
"v_cvt_pk_bf16_f32 v232, v232, v233\n\t"
"v_mfma_f32_16x16x32_bf16 v[140:143], a[224:227], a[180:183], v[140:143]\n\t"
"v_cvt_pk_bf16_f32 v233, v234, v235\n\t"
"v_mfma_f32_16x16x32_bf16 v[140:143], a[228:231], a[184:187], v[140:143]\n\t"
"ds_write2st64_b64 v245, v[228:229], v[230:231] offset0:5 offset1:6\n\t"
"v_mfma_f32_16x16x32_bf16 v[144:147], a[220:223], a[180:183], v[144:147]\n\t"
"buffer_load_dwordx4 v247, s[12:15], s28 offen offset:128 nt lds\n\t"
"v_mfma_f32_16x16x32_bf16 v[144:147], a[224:227], a[184:187], v[144:147]\n\t"
"s_add_u32 m0, s24, 100736\n\t"
"s_add_u32 s28, s21, 2949120\n\t"
"v_mfma_f32_16x16x32_bf16 v[144:147], a[228:231], a[188:191], v[144:147]\n\t"
"buffer_load_dwordx4 v247, s[12:15], s28 offen offset:128 nt lds\n\t"
"s_add_u32 m0, s24, 106880\n\t"
"s_add_u32 s28, s21, 3145728\n\t"
"v_mfma_f32_16x16x32_bf16 v[148:151], a[220:223], a[184:187], v[148:151]\n\t"
"buffer_load_dwordx4 v247, s[12:15], s28 offen offset:128 nt lds\n\t"
"v_mfma_f32_16x16x32_bf16 v[148:151], a[224:227], a[188:191], v[148:151]\n\t"
"s_waitcnt lgkmcnt(1)\n\t"
"s_add_u32 m0, s24, 113024\n\t"
"s_add_u32 s28, s21, 3342336\n\t"
"v_mfma_f32_16x16x32_bf16 v[148:151], a[228:231], a[192:195], v[148:151]\n\t"
"buffer_load_dwordx4 v247, s[12:15], s28 offen offset:128 nt lds\n\t"
"s_add_u32 m0, s25, 896\n\t"
"v_mfma_f32_16x16x32_bf16 v[224:227], v[224:227], v[220:223], 0\n\t"
"buffer_load_dwordx4 v254, s[12:15], 0 offen offset:128 nt lds\n\t"
"v_mfma_f32_16x16x32_bf16 v[152:155], a[220:223], a[188:191], v[152:155]\n\t"
"s_add_u32 m0, s26, 896\n\t"
"v_mfma_f32_16x16x32_bf16 v[152:155], a[224:227], a[192:195], v[152:155]\n\t"
"v_mfma_f32_16x16x32_bf16 v[152:155], a[228:231], a[196:199], v[152:155]\n\t"
"s_mov_b64 exec, s[36:37]\n\t"
"buffer_load_dwordx4 v254, s[12:15], s27 offen offset:128 nt lds\n\t"
"s_mov_b64 exec, -1\n\t"
"v_mfma_f32_16x16x32_bf16 v[156:159], a[220:223], a[192:195], v[156:159]\n\t"
"v_mfma_f32_16x16x32_bf16 v[156:159], a[224:227], a[196:199], v[156:159]\n\t"
"v_cvt_pk_bf16_f32 v234, v224, v225\n\t"
"v_cvt_pk_bf16_f32 v235, v226, v227\n\t"
"v_mfma_f32_16x16x32_bf16 v[156:159], a[228:231], a[200:203], v[156:159]\n\t"
"ds_write2st64_b64 v245, v[232:233], v[234:235] offset0:7 offset1:8\n\t"
"v_mfma_f32_16x16x32_bf16 v[160:163], a[220:223], a[196:199], v[160:163]\n\t"
"s_waitcnt vmcnt(18)\n\t"
"v_mfma_f32_16x16x32_bf16 v[160:163], a[224:227], a[200:203], v[160:163]\n\t"
"v_mfma_f32_16x16x32_bf16 v[160:163], a[228:231], a[204:207], v[160:163]\n\t"
"v_mfma_f32_16x16x32_bf16 v[164:167], a[232:235], a[168:171], v[164:167]\n\t"
"v_mfma_f32_16x16x32_bf16 v[164:167], a[236:239], a[172:175], v[164:167]\n\t"
"v_mfma_f32_16x16x32_bf16 v[164:167], a[240:243], a[176:179], v[164:167]\n\t"
"s_waitcnt lgkmcnt(0)\n\t"
"s_barrier\n\t"
"v_mfma_f32_16x16x32_bf16 v[168:171], a[232:235], a[172:175], v[168:171]\n\t"
"ds_read_b128 a[168:171], v244 offset:0\n\t"
"v_mfma_f32_16x16x32_bf16 v[168:171], a[236:239], a[176:179], v[168:171]\n\t"
"ds_read_b128 a[172:175], v244 offset:512\n\t"
"v_mfma_f32_16x16x32_bf16 v[168:171], a[240:243], a[180:183], v[168:171]\n\t"
"v_mfma_f32_16x16x32_bf16 v[172:175], a[232:235], a[176:179], v[172:175]\n\t"
"ds_read_b128 a[176:179], v244 offset:1024\n\t"
"v_mfma_f32_16x16x32_bf16 v[172:175], a[236:239], a[180:183], v[172:175]\n\t"
"v_mfma_f32_16x16x32_bf16 v[172:175], a[240:243], a[184:187], v[172:175]\n\t"
"v_mfma_f32_16x16x32_bf16 v[176:179], a[232:235], a[180:183], v[176:179]\n\t"
"ds_read_b128 a[180:183], v244 offset:1536\n\t"
"v_mfma_f32_16x16x32_bf16 v[176:179], a[236:239], a[184:187], v[176:179]\n\t"
"v_mfma_f32_16x16x32_bf16 v[176:179], a[240:243], a[188:191], v[176:179]\n\t"
"v_mfma_f32_16x16x32_bf16 v[180:183], a[232:235], a[184:187], v[180:183]\n\t"
"ds_read_b128 a[184:187], v244 offset:2048\n\t"
"v_mfma_f32_16x16x32_bf16 v[180:183], a[236:239], a[188:191], v[180:183]\n\t"
"v_mfma_f32_16x16x32_bf16 v[180:183], a[240:243], a[192:195], v[180:183]\n\t"
"v_mfma_f32_16x16x32_bf16 v[184:187], a[232:235], a[188:191], v[184:187]\n\t"
"ds_read_b128 a[188:191], v244 offset:2560\n\t"
"v_mfma_f32_16x16x32_bf16 v[184:187], a[236:239], a[192:195], v[184:187]\n\t"
"v_mfma_f32_16x16x32_bf16 v[184:187], a[240:243], a[196:199], v[184:187]\n\t"
"v_mfma_f32_16x16x32_bf16 v[188:191], a[232:235], a[192:195], v[188:191]\n\t"
"ds_read_b128 a[192:195], v244 offset:3072\n\t"
"v_mfma_f32_16x16x32_bf16 v[188:191], a[236:239], a[196:199], v[188:191]\n\t"
"v_mfma_f32_16x16x32_bf16 v[188:191], a[240:243], a[200:203], v[188:191]\n\t"
"v_mfma_f32_16x16x32_bf16 v[192:195], a[232:235], a[196:199], v[192:195]\n\t"
"s_waitcnt vmcnt(15) lgkmcnt(4)\n\t"
"ds_read_b128 a[196:199], v244 offset:3584\n\t"
"v_mfma_f32_16x16x32_bf16 v[192:195], a[236:239], a[200:203], v[192:195]\n\t"
"ds_read_b128 a[200:203], v244 offset:4096\n\t"
"v_mfma_f32_16x16x32_bf16 v[192:195], a[240:243], a[204:207], v[192:195]\n\t"
"ds_read_b128 a[204:207], v244 offset:4608\n\t"
"Lst5_%=:\n\t"
"v_mfma_f32_16x16x32_bf16 v[196:199], v[236:239], a[168:171], v[196:199]\n\t"
"s_waitcnt lgkmcnt(6)\n\t"
"v_mfma_f32_16x16x32_bf16 v[196:199], v[96:99], a[172:175], v[196:199]\n\t"
"v_mfma_f32_16x16x32_bf16 v[196:199], a[244:247], a[176:179], v[196:199]\n\t"
"v_mfma_f32_16x16x32_bf16 v[204:207], v[236:239], a[172:175], v[204:207]\n\t"
"s_waitcnt lgkmcnt(5)\n\t"
"v_mfma_f32_16x16x32_bf16 v[204:207], v[96:99], a[176:179], v[204:207]\n\t"
"v_mfma_f32_16x16x32_bf16 v[204:207], a[244:247], a[180:183], v[204:207]\n\t"
"v_mfma_f32_16x16x32_bf16 v[212:215], v[236:239], a[176:179], v[212:215]\n\t"
"s_waitcnt lgkmcnt(4)\n\t"
"v_mfma_f32_16x16x32_bf16 v[212:215], v[96:99], a[180:183], v[212:215]\n\t"
"v_mfma_f32_16x16x32_bf16 v[212:215], a[244:247], a[184:187], v[212:215]\n\t"
"v_mfma_f32_16x16x32_bf16 a[0:3], v[236:239], a[180:183], a[0:3]\n\t"
"s_waitcnt lgkmcnt(3)\n\t"
"v_mfma_f32_16x16x32_bf16 a[0:3], v[96:99], a[184:187], a[0:3]\n\t"
"buffer_load_dwordx4 a[220:223], v252, s[8:11], s20 offen offset:1024 sc1\n\t"
"v_mfma_f32_16x16x32_bf16 a[0:3], a[244:247], a[188:191], a[0:3]\n\t"
"v_mfma_f32_16x16x32_bf16 a[8:11], v[236:239], a[184:187], a[8:11]\n\t"
"s_waitcnt lgkmcnt(2)\n\t"
"v_mfma_f32_16x16x32_bf16 a[8:11], v[96:99], a[188:191], a[8:11]\n\t"
"v_mfma_f32_16x16x32_bf16 a[8:11], a[244:247], a[192:195], a[8:11]\n\t"
"ds_read_b64_tr_b16 v[228:229], v246 offset:1024\n\t"
"v_mfma_f32_16x16x32_bf16 a[16:19], v[236:239], a[188:191], a[16:19]\n\t"
"s_waitcnt lgkmcnt(2)\n\t"
"ds_read_b64_tr_b16 v[230:231], v246 offset:4096\n\t"
"v_mfma_f32_16x16x32_bf16 a[16:19], v[96:99], a[192:195], a[16:19]\n\t"
"v_mfma_f32_16x16x32_bf16 a[16:19], a[244:247], a[196:199], a[16:19]\n\t"
"v_mfma_f32_16x16x32_bf16 a[24:27], v[236:239], a[192:195], a[24:27]\n\t"
"s_waitcnt lgkmcnt(2)\n\t"
"v_mfma_f32_16x16x32_bf16 a[24:27], v[96:99], a[196:199], a[24:27]\n\t"
"v_mfma_f32_16x16x32_bf16 a[24:27], a[244:247], a[200:203], a[24:27]\n\t"
"v_mfma_f32_16x16x32_bf16 a[32:35], v[236:239], a[196:199], a[32:35]\n\t"
"s_waitcnt vmcnt(13)\n\t"
"ds_read_b64_tr_b16 v[224:225], v246 offset:7168\n\t"
"v_mfma_f32_16x16x32_bf16 a[32:35], v[96:99], a[200:203], a[32:35]\n\t"
"ds_read_b64_tr_b16 v[226:227], v246 offset:10240\n\t"
"v_mfma_f32_16x16x32_bf16 a[32:35], a[244:247], a[204:207], a[32:35]\n\t"
"v_mfma_f32_16x16x32_bf16 a[40:43], a[248:251], a[168:171], a[40:43]\n\t"
"v_mfma_f32_16x16x32_bf16 a[40:43], a[252:255], a[172:175], a[40:43]\n\t"
"v_mfma_f32_16x16x32_bf16 a[40:43], v[240:243], a[176:179], a[40:43]\n\t"
"v_mfma_f32_16x16x32_bf16 a[48:51], a[248:251], a[172:175], a[48:51]\n\t"
"s_waitcnt lgkmcnt(2)\n\t"
"v_mfma_f32_16x16x32_bf16 a[48:51], a[252:255], a[176:179], a[48:51]\n\t"
"v_mfma_f32_16x16x32_bf16 a[48:51], v[240:243], a[180:183], a[48:51]\n\t"
"v_mfma_f32_16x16x32_bf16 v[228:231], v[228:231], v[220:223], 0\n\t"
"v_mfma_f32_16x16x32_bf16 a[56:59], a[248:251], a[176:179], a[56:59]\n\t"
"ds_read_b64_tr_b16 v[232:233], v246 offset:13312\n\t"
"v_mfma_f32_16x16x32_bf16 a[56:59], a[252:255], a[180:183], a[56:59]\n\t"
"ds_read_b64_tr_b16 v[234:235], v246 offset:16384\n\t"
"v_mfma_f32_16x16x32_bf16 a[56:59], v[240:243], a[184:187], a[56:59]\n\t"
"v_mfma_f32_16x16x32_bf16 a[64:67], a[248:251], a[180:183], a[64:67]\n\t"
"s_waitcnt lgkmcnt(2)\n\t"
"v_mfma_f32_16x16x32_bf16 a[64:67], a[252:255], a[184:187], a[64:67]\n\t"
"v_cvt_pk_bf16_f32 v228, v228, v229\n\t"
"v_cvt_pk_bf16_f32 v229, v230, v231\n\t"
"v_mfma_f32_16x16x32_bf16 a[64:67], v[240:243], a[188:191], a[64:67]\n\t"
"v_mfma_f32_16x16x32_bf16 v[224:227], v[224:227], v[220:223], 0\n\t"
"buffer_load_dwordx4 v[236:239], v252, s[8:11], s20 offen offset:2048 sc1\n\t"
"v_mfma_f32_16x16x32_bf16 a[72:75], a[248:251], a[184:187], a[72:75]\n\t"
"s_mov_b32 s20, 454656\n\t"
"v_mfma_f32_16x16x32_bf16 a[72:75], a[252:255], a[188:191], a[72:75]\n\t"
"v_mfma_f32_16x16x32_bf16 a[72:75], v[240:243], a[192:195], a[72:75]\n\t"
"v_mfma_f32_16x16x32_bf16 a[80:83], a[248:251], a[188:191], a[80:83]\n\t"
"buffer_load_dwordx4 v[96:99], v252, s[8:11], s20 offen offset:0 sc1\n\t"
"v_mfma_f32_16x16x32_bf16 a[80:83], a[252:255], a[192:195], a[80:83]\n\t"
"v_cvt_pk_bf16_f32 v230, v224, v225\n\t"
"v_cvt_pk_bf16_f32 v231, v226, v227\n\t"
"v_mfma_f32_16x16x32_bf16 a[80:83], v[240:243], a[196:199], a[80:83]\n\t"
"ds_read_b64_tr_b16 v[224:225], v246 offset:20992\n\t"
"v_mfma_f32_16x16x32_bf16 a[88:91], a[248:251], a[192:195], a[88:91]\n\t"
"s_waitcnt lgkmcnt(1)\n\t"
"ds_read_b64_tr_b16 v[226:227], v246 offset:24064\n\t"
"v_mfma_f32_16x16x32_bf16 a[88:91], a[252:255], a[196:199], a[88:91]\n\t"
"buffer_load_dwordx4 a[224:227], v252, s[8:11], s20 offen offset:1024 sc1\n\t"
"v_mfma_f32_16x16x32_bf16 a[88:91], v[240:243], a[200:203], a[88:91]\n\t"
"v_mfma_f32_16x16x32_bf16 v[232:235], v[232:235], v[220:223], 0\n\t"
"v_mfma_f32_16x16x32_bf16 a[96:99], a[248:251], a[196:199], a[96:99]\n\t"
"s_waitcnt vmcnt(13)\n\t"
"v_mfma_f32_16x16x32_bf16 a[96:99], a[252:255], a[200:203], a[96:99]\n\t"
"buffer_load_dwordx4 a[228:231], v252, s[8:11], s20 offen offset:2048 sc1\n\t"
"v_mfma_f32_16x16x32_bf16 a[96:99], v[240:243], a[204:207], a[96:99]\n\t"
"s_mov_b32 s20, 466944\n\t"
"v_mfma_f32_16x16x32_bf16 a[104:107], a[208:211], a[168:171], a[104:107]\n\t"
"ds_read_b128 a[168:171], v244 offset:9216\n\t"
"v_mfma_f32_16x16x32_bf16 a[104:107], v[248:251], a[172:175], a[104:107]\n\t"
"v_cvt_pk_bf16_f32 v232, v232, v233\n\t"
"v_cvt_pk_bf16_f32 v233, v234, v235\n\t"
"v_mfma_f32_16x16x32_bf16 a[104:107], a[212:215], a[176:179], a[104:107]\n\t"
"v_mfma_f32_16x16x32_bf16 a[112:115], a[208:211], a[172:175], a[112:115]\n\t"
"buffer_load_dwordx4 a[232:235], v252, s[8:11], s20 offen offset:0 sc1\n\t"
"v_mfma_f32_16x16x32_bf16 a[112:115], v[248:251], a[176:179], a[112:115]\n\t"
"s_waitcnt lgkmcnt(1)\n\t"
"ds_read_b128 a[172:175], v244 offset:9728\n\t"
"v_mfma_f32_16x16x32_bf16 a[112:115], a[212:215], a[180:183], a[112:115]\n\t"
"v_mfma_f32_16x16x32_bf16 v[224:227], v[224:227], v[220:223], 0\n\t"
"buffer_load_dwordx4 a[236:239], v252, s[8:11], s20 offen offset:1024 sc1\n\t"
"v_mfma_f32_16x16x32_bf16 a[120:123], a[208:211], a[176:179], a[120:123]\n\t"
"ds_read_b128 a[176:179], v244 offset:10240\n\t"
"v_mfma_f32_16x16x32_bf16 a[120:123], v[248:251], a[180:183], a[120:123]\n\t"
"v_mfma_f32_16x16x32_bf16 a[120:123], a[212:215], a[184:187], a[120:123]\n\t"
"v_mfma_f32_16x16x32_bf16 a[128:131], a[208:211], a[180:183], a[128:131]\n\t"
"buffer_load_dwordx4 a[240:243], v252, s[8:11], s20 offen offset:2048 sc1\n\t"
"v_mfma_f32_16x16x32_bf16 a[128:131], v[248:251], a[184:187], a[128:131]\n\t"
"s_mov_b32 s20, 700416\n\t"
"ds_read_b128 a[180:183], v244 offset:10752\n\t"
"v_cvt_pk_bf16_f32 v234, v224, v225\n\t"
"v_mfma_f32_16x16x32_bf16 a[128:131], a[212:215], a[188:191], a[128:131]\n\t"
"v_cvt_pk_bf16_f32 v235, v226, v227\n\t"
"v_mfma_f32_16x16x32_bf16 a[136:139], a[208:211], a[184:187], a[136:139]\n\t"
"ds_read_b64_tr_b16 v[224:225], v246 offset:27136\n\t"
"v_mfma_f32_16x16x32_bf16 a[136:139], v[248:251], a[188:191], a[136:139]\n\t"
"ds_read_b64_tr_b16 v[226:227], v246 offset:30208\n\t"
"v_mfma_f32_16x16x32_bf16 a[136:139], a[212:215], a[192:195], a[136:139]\n\t"
"buffer_load_dwordx4 a[244:247], v252, s[8:11], s20 offen offset:0 sc1\n\t"
"v_mfma_f32_16x16x32_bf16 a[144:147], a[208:211], a[188:191], a[144:147]\n\t"
"ds_read_b128 a[184:187], v244 offset:11264\n\t"
"v_mfma_f32_16x16x32_bf16 a[144:147], v[248:251], a[192:195], a[144:147]\n\t"
"ds_read_b128 a[188:191], v244 offset:11776\n\t"
"v_mfma_f32_16x16x32_bf16 a[144:147], a[212:215], a[196:199], a[144:147]\n\t"
"buffer_load_dwordx4 a[248:251], v252, s[8:11], s20 offen offset:1024 sc1\n\t"
"v_mfma_f32_16x16x32_bf16 a[152:155], a[208:211], a[192:195], a[152:155]\n\t"
"ds_read_b128 a[192:195], v244 offset:12288\n\t"
"v_mfma_f32_16x16x32_bf16 a[152:155], v[248:251], a[196:199], a[152:155]\n\t"
"v_mfma_f32_16x16x32_bf16 a[152:155], a[212:215], a[200:203], a[152:155]\n\t"
"v_mfma_f32_16x16x32_bf16 a[160:163], a[208:211], a[196:199], a[160:163]\n\t"
"buffer_load_dwordx4 a[252:255], v252, s[8:11], s20 offen offset:2048 sc1\n\t"
"v_mfma_f32_16x16x32_bf16 a[160:163], v[248:251], a[200:203], a[160:163]\n\t"
"s_waitcnt vmcnt(9) lgkmcnt(3)\n\t"
"ds_read_b128 a[196:199], v244 offset:12800\n\t"
"s_mov_b32 s20, 712704\n\t"
"v_mfma_f32_16x16x32_bf16 a[160:163], a[212:215], a[204:207], a[160:163]\n\t"
"ds_read_b128 a[200:203], v244 offset:13312\n\t"
"v_mfma_f32_16x16x32_bf16 v[224:227], v[224:227], v[220:223], 0\n\t"
"ds_read_b128 a[204:207], v244 offset:13824\n\t"
"v_mfma_f32_16x16x32_bf16 v[200:203], a[216:219], a[168:171], v[200:203]\n\t"
"buffer_load_dwordx4 v[248:251], v252, s[8:11], s20 offen offset:0 sc1\n\t"
"v_mfma_f32_16x16x32_bf16 v[200:203], a[220:223], a[172:175], v[200:203]\n\t"
"v_mfma_f32_16x16x32_bf16 v[200:203], v[236:239], a[176:179], v[200:203]\n\t"
"v_mfma_f32_16x16x32_bf16 v[208:211], a[216:219], a[172:175], v[208:211]\n\t"
"buffer_load_dwordx4 a[212:215], v252, s[8:11], s20 offen offset:1024 sc1\n\t"
"v_mfma_f32_16x16x32_bf16 v[208:211], a[220:223], a[176:179], v[208:211]\n\t"
"s_waitcnt lgkmcnt(5)\n\t"
"v_cvt_pk_bf16_f32 v224, v224, v225\n\t"
"v_mfma_f32_16x16x32_bf16 v[208:211], v[236:239], a[180:183], v[208:211]\n\t"
"s_waitcnt lgkmcnt(0)\n\t"
"s_barrier\n\t"
"v_mfma_f32_16x16x32_bf16 v[216:219], a[216:219], a[176:179], v[216:219]\n\t"
"v_cvt_pk_bf16_f32 v225, v226, v227\n\t"
"v_mfma_f32_16x16x32_bf16 v[216:219], a[220:223], a[180:183], v[216:219]\n\t"
"v_mfma_f32_16x16x32_bf16 v[216:219], v[236:239], a[184:187], v[216:219]\n\t"
"ds_write2st64_b64 v245, v[228:229], v[230:231] offset0:0 offset1:1\n\t"
"v_mfma_f32_16x16x32_bf16 a[4:7], a[216:219], a[180:183], a[4:7]\n\t"
"v_mfma_f32_16x16x32_bf16 a[4:7], a[220:223], a[184:187], a[4:7]\n\t"
"ds_read_b64_tr_b16 v[228:229], v246 offset:33280\n\t"
"v_mfma_f32_16x16x32_bf16 a[4:7], v[236:239], a[188:191], a[4:7]\n\t"
"ds_write2st64_b64 v245, v[232:233], v[234:235] offset0:2 offset1:3\n\t"
"v_mfma_f32_16x16x32_bf16 a[12:15], a[216:219], a[184:187], a[12:15]\n\t"
"v_mfma_f32_16x16x32_bf16 a[12:15], a[220:223], a[188:191], a[12:15]\n\t"
"ds_read_b64_tr_b16 v[230:231], v246 offset:36352\n\t"
"v_mfma_f32_16x16x32_bf16 a[12:15], v[236:239], a[192:195], a[12:15]\n\t"
"ds_write_b64 v245, v[224:225] offset:2048\n\t"
"v_mfma_f32_16x16x32_bf16 a[20:23], a[216:219], a[188:191], a[20:23]\n\t"
"ds_read_b64_tr_b16 v[232:233], v246 offset:40960\n\t"
"v_mfma_f32_16x16x32_bf16 a[20:23], a[220:223], a[192:195], a[20:23]\n\t"
"ds_read_b64_tr_b16 v[234:235], v246 offset:44032\n\t"
"v_mfma_f32_16x16x32_bf16 a[20:23], v[236:239], a[196:199], a[20:23]\n\t"
"s_waitcnt lgkmcnt(3)\n\t"
"ds_read_b64_tr_b16 v[224:225], v246 offset:47104\n\t"
"v_mfma_f32_16x16x32_bf16 v[228:231], v[228:231], v[220:223], 0\n\t"
"ds_read_b64_tr_b16 v[226:227], v246 offset:50176\n\t"
"v_mfma_f32_16x16x32_bf16 a[28:31], a[216:219], a[192:195], a[28:31]\n\t"
"global_load_dwordx4 v[240:243], v252, s[48:49] offset:2048\n\t"
"v_mfma_f32_16x16x32_bf16 a[28:31], a[220:223], a[196:199], a[28:31]\n\t"
"global_load_dwordx4 a[208:211], v252, s[46:47] offset:1024\n\t"
"v_mfma_f32_16x16x32_bf16 a[28:31], v[236:239], a[200:203], a[28:31]\n\t"
"s_waitcnt lgkmcnt(2)\n\t"
"v_mfma_f32_16x16x32_bf16 v[232:235], v[232:235], v[220:223], 0\n\t"
"v_mfma_f32_16x16x32_bf16 a[36:39], a[216:219], a[196:199], a[36:39]\n\t"
"buffer_load_dwordx4 a[216:219], v252, s[8:11], s20 offen offset:2048 sc1\n\t"
"v_mfma_f32_16x16x32_bf16 a[36:39], a[220:223], a[200:203], a[36:39]\n\t"
"s_waitcnt vmcnt(11) lgkmcnt(0)\n\t"
"s_mov_b32 s20, 724992\n\t"
"v_cvt_pk_bf16_f32 v228, v228, v229\n\t"
"v_mfma_f32_16x16x32_bf16 a[36:39], v[236:239], a[204:207], a[36:39]\n\t"
"v_cvt_pk_bf16_f32 v229, v230, v231\n\t"
"v_mfma_f32_16x16x32_bf16 v[224:227], v[224:227], v[220:223], 0\n\t"
"buffer_load_dwordx4 a[220:223], v252, s[8:11], s20 offen offset:0 sc1\n\t"
"v_mfma_f32_16x16x32_bf16 a[44:47], v[96:99], a[168:171], a[44:47]\n\t"
"v_cvt_pk_bf16_f32 v230, v232, v233\n\t"
"v_cvt_pk_bf16_f32 v231, v234, v235\n\t"
"v_mfma_f32_16x16x32_bf16 a[44:47], a[224:227], a[172:175], a[44:47]\n\t"
"ds_read_b64_tr_b16 v[232:233], v246 offset:53248\n\t"
"v_mfma_f32_16x16x32_bf16 a[44:47], a[228:231], a[176:179], a[44:47]\n\t"
"ds_write2st64_b64 v245, v[228:229], v[230:231] offset0:5 offset1:6\n\t"
"v_mfma_f32_16x16x32_bf16 a[52:55], v[96:99], a[172:175], a[52:55]\n\t"
"v_mfma_f32_16x16x32_bf16 a[52:55], a[224:227], a[176:179], a[52:55]\n\t"
"ds_read_b64_tr_b16 v[234:235], v246 offset:56320\n\t"
"v_mfma_f32_16x16x32_bf16 a[52:55], a[228:231], a[180:183], a[52:55]\n\t"
"v_cvt_pk_bf16_f32 v224, v224, v225\n\t"
"v_cvt_pk_bf16_f32 v225, v226, v227\n\t"
"v_mfma_f32_16x16x32_bf16 a[60:63], v[96:99], a[176:179], a[60:63]\n\t"
"v_mfma_f32_16x16x32_bf16 a[60:63], a[224:227], a[180:183], a[60:63]\n\t"
"v_mfma_f32_16x16x32_bf16 a[60:63], a[228:231], a[184:187], a[60:63]\n\t"
"v_mfma_f32_16x16x32_bf16 a[68:71], v[96:99], a[180:183], a[68:71]\n\t"
"v_mfma_f32_16x16x32_bf16 a[68:71], a[224:227], a[184:187], a[68:71]\n\t"
"v_mfma_f32_16x16x32_bf16 a[68:71], a[228:231], a[188:191], a[68:71]\n\t"
"v_mfma_f32_16x16x32_bf16 a[76:79], v[96:99], a[184:187], a[76:79]\n\t"
"s_waitcnt lgkmcnt(0)\n\t"
"v_mfma_f32_16x16x32_bf16 a[76:79], a[224:227], a[188:191], a[76:79]\n\t"
"v_mfma_f32_16x16x32_bf16 a[76:79], a[228:231], a[192:195], a[76:79]\n\t"
"v_mfma_f32_16x16x32_bf16 v[232:235], v[232:235], v[220:223], 0\n\t"
"v_mfma_f32_16x16x32_bf16 a[84:87], v[96:99], a[188:191], a[84:87]\n\t"
"v_mfma_f32_16x16x32_bf16 a[84:87], a[224:227], a[192:195], a[84:87]\n\t"
"v_mfma_f32_16x16x32_bf16 a[84:87], a[228:231], a[196:199], a[84:87]\n\t"
"v_mfma_f32_16x16x32_bf16 a[92:95], v[96:99], a[192:195], a[92:95]\n\t"
"v_mfma_f32_16x16x32_bf16 a[92:95], a[224:227], a[196:199], a[92:95]\n\t"
"v_cvt_pk_bf16_f32 v226, v232, v233\n\t"
"v_cvt_pk_bf16_f32 v227, v234, v235\n\t"
"v_mfma_f32_16x16x32_bf16 a[92:95], a[228:231], a[200:203], a[92:95]\n\t"
"ds_write2st64_b64 v245, v[224:225], v[226:227] offset0:7 offset1:8\n\t"
"v_mfma_f32_16x16x32_bf16 a[100:103], v[96:99], a[196:199], a[100:103]\n\t"
"buffer_load_dwordx4 v[96:99], v252, s[8:11], s20 offen offset:1024 sc1\n\t"
"v_mfma_f32_16x16x32_bf16 a[100:103], a[224:227], a[200:203], a[100:103]\n\t"
"s_waitcnt vmcnt(10)\n\t"
"v_mfma_f32_16x16x32_bf16 a[100:103], a[228:231], a[204:207], a[100:103]\n\t"
"v_mfma_f32_16x16x32_bf16 a[108:111], a[232:235], a[168:171], a[108:111]\n\t"
"buffer_load_dwordx4 a[224:227], v252, s[8:11], s20 offen offset:2048 sc1\n\t"
"v_mfma_f32_16x16x32_bf16 a[108:111], a[236:239], a[172:175], a[108:111]\n\t"
"s_mov_b32 s20, 737280\n\t"
"v_mfma_f32_16x16x32_bf16 a[108:111], a[240:243], a[176:179], a[108:111]\n\t"
"s_waitcnt lgkmcnt(0)\n\t"
"s_barrier\n\t"
"v_mfma_f32_16x16x32_bf16 a[116:119], a[232:235], a[172:175], a[116:119]\n\t"
"s_waitcnt vmcnt(5)\n\t"
"ds_read_b64_tr_b16 v[236:237], v240 offset:1024\n\t"
"v_mfma_f32_16x16x32_bf16 a[116:119], a[236:239], a[176:179], a[116:119]\n\t"
"ds_read_b64_tr_b16 v[238:239], v240 offset:2560\n\t"
"v_mfma_f32_16x16x32_bf16 a[116:119], a[240:243], a[180:183], a[116:119]\n\t"
"ds_read_b64_tr_b16 v[228:229], v241 offset:1024\n\t"
"v_mfma_f32_16x16x32_bf16 a[124:127], a[232:235], a[176:179], a[124:127]\n\t"
"ds_read_b64_tr_b16 v[230:231], v241 offset:2560\n\t"
"v_mfma_f32_16x16x32_bf16 a[124:127], a[236:239], a[180:183], a[124:127]\n\t"
"buffer_load_dwordx4 a[228:231], v252, s[8:11], s20 offen offset:0 sc1\n\t"
"v_mfma_f32_16x16x32_bf16 a[124:127], a[240:243], a[184:187], a[124:127]\n\t"
"ds_read_b64_tr_b16 v[232:233], v240 offset:20992\n\t"
"v_mfma_f32_16x16x32_bf16 a[132:135], a[232:235], a[180:183], a[132:135]\n\t"
"s_waitcnt vmcnt(5) lgkmcnt(3)\n\t"
"ds_read_b64_tr_b16 v[234:235], v240 offset:22528\n\t"
"v_mfma_f32_16x16x32_bf16 a[132:135], a[236:239], a[184:187], a[132:135]\n\t"
"ds_read_b64_tr_b16 v[224:225], v241 offset:20992\n\t"
"v_mfma_f32_16x16x32_bf16 a[132:135], a[240:243], a[188:191], a[132:135]\n\t"
"s_waitcnt lgkmcnt(3)\n\t"
"ds_read_b64_tr_b16 v[226:227], v241 offset:22528\n\t"
"v_mfma_f32_16x16x32_bf16 v[236:239], v[236:239], a[208:211], 0\n\t"
"ds_read_b128 a[168:171], v244 offset:0\n\t"
"v_mfma_f32_16x16x32_bf16 v[228:231], v[228:231], a[208:211], 0\n\t"
"ds_read_b128 a[172:175], v244 offset:512\n\t"
"v_mfma_f32_16x16x32_bf16 a[140:143], a[232:235], a[184:187], a[140:143]\n\t"
"ds_read_b128 a[176:179], v244 offset:1024\n\t"
"v_mfma_f32_16x16x32_bf16 a[140:143], a[236:239], a[188:191], a[140:143]\n\t"
"ds_read_b128 a[180:183], v244 offset:1536\n\t"
"v_mfma_f32_16x16x32_bf16 a[140:143], a[240:243], a[192:195], a[140:143]\n\t"
"ds_read_b128 a[184:187], v244 offset:2048\n\t"
"v_mfma_f32_16x16x32_bf16 a[148:151], a[232:235], a[188:191], a[148:151]\n\t"
"s_waitcnt lgkmcnt(7)\n\t"
"ds_read_b128 a[188:191], v244 offset:2560\n\t"
"v_cvt_pk_bf16_f32 v236, v236, v237\n\t"
"v_mfma_f32_16x16x32_bf16 a[148:151], a[236:239], a[192:195], a[148:151]\n\t"
"s_waitcnt lgkmcnt(6)\n\t"
"v_cvt_pk_bf16_f32 v237, v238, v239\n\t"
"v_cvt_pk_bf16_f32 v238, v228, v229\n\t"
"v_mfma_f32_16x16x32_bf16 a[148:151], a[240:243], a[196:199], a[148:151]\n\t"
"v_cvt_pk_bf16_f32 v239, v230, v231\n\t"
"v_mfma_f32_16x16x32_bf16 v[232:235], v[232:235], a[208:211], 0\n\t"
"ds_write_b128 v242, v[236:239] offset:24576\n\t"
"v_mfma_f32_16x16x32_bf16 v[224:227], v[224:227], a[208:211], 0\n\t"
"v_mfma_f32_16x16x32_bf16 a[156:159], a[232:235], a[192:195], a[156:159]\n\t"
"ds_read_b128 a[192:195], v244 offset:3072\n\t"
"v_mfma_f32_16x16x32_bf16 a[156:159], a[236:239], a[196:199], a[156:159]\n\t"
"ds_read_b64_tr_b16 v[228:229], v240 offset:40960\n\t"
"v_mfma_f32_16x16x32_bf16 a[156:159], a[240:243], a[200:203], a[156:159]\n\t"
"ds_read_b64_tr_b16 v[230:231], v240 offset:42496\n\t"
"v_mfma_f32_16x16x32_bf16 a[164:167], a[232:235], a[196:199], a[164:167]\n\t"
"buffer_load_dwordx4 a[232:235], v252, s[8:11], s20 offen offset:1024 sc1\n\t"
"v_mfma_f32_16x16x32_bf16 a[164:167], a[236:239], a[200:203], a[164:167]\n\t"
"s_waitcnt lgkmcnt(7)\n\t"
"ds_read_b64_tr_b16 v[236:237], v241 offset:40960\n\t"
"v_cvt_pk_bf16_f32 v232, v232, v233\n\t"
"v_mfma_f32_16x16x32_bf16 a[164:167], a[240:243], a[204:207], a[164:167]\n\t"
"ds_read_b64_tr_b16 v[238:239], v241 offset:42496\n\t"
"v_cvt_pk_bf16_f32 v233, v234, v235\n\t"
"Lst6_%=:\n\t"
"v_mfma_f32_16x16x32_bf16 v[196:199], a[244:247], a[168:171], v[196:199]\n\t"
"s_waitcnt lgkmcnt(8)\n\t"
"ds_read_b128 a[196:199], v244 offset:3584\n\t"
"v_cvt_pk_bf16_f32 v234, v224, v225\n\t"
"v_mfma_f32_16x16x32_bf16 v[196:199], a[248:251], a[172:175], v[196:199]\n\t"
"ds_read_b128 a[200:203], v244 offset:4096\n\t"
"v_cvt_pk_bf16_f32 v235, v226, v227\n\t"
"v_mfma_f32_16x16x32_bf16 v[196:199], a[252:255], a[176:179], v[196:199]\n\t"
"ds_write_b128 v242, v[232:235] offset:26112\n\t"
"v_mfma_f32_16x16x32_bf16 v[204:207], a[244:247], a[172:175], v[204:207]\n\t"
"buffer_load_dwordx4 a[236:239], v252, s[8:11], s20 offen offset:2048 sc1\n\t"
"v_mfma_f32_16x16x32_bf16 v[204:207], a[248:251], a[176:179], v[204:207]\n\t"
"s_waitcnt lgkmcnt(10)\n\t"
"ds_read_b128 a[204:207], v244 offset:4608\n\t"
"v_mfma_f32_16x16x32_bf16 v[204:207], a[252:255], a[180:183], v[204:207]\n\t"
"s_mov_b32 s20, 749568\n\t"
"v_add_u32 v240, 59904, v240\n\t"
"v_add_u32 v241, 59904, v241\n\t"
"v_mfma_f32_16x16x32_bf16 v[212:215], a[244:247], a[176:179], v[212:215]\n\t"
"s_waitcnt lgkmcnt(6)\n\t"
"ds_read_b64_tr_b16 v[224:225], v240 offset:1024\n\t"
"v_mfma_f32_16x16x32_bf16 v[212:215], a[248:251], a[180:183], v[212:215]\n\t"
"buffer_load_dwordx4 a[240:243], v252, s[8:11], s20 offen offset:0 sc1\n\t"
"v_mfma_f32_16x16x32_bf16 v[212:215], a[252:255], a[184:187], v[212:215]\n\t"
"s_waitcnt lgkmcnt(5)\n\t"
"ds_read_b64_tr_b16 v[226:227], v240 offset:2560\n\t"
"v_mfma_f32_16x16x32_bf16 v[228:231], v[228:231], a[208:211], 0\n\t"
"ds_read_b64_tr_b16 v[232:233], v241 offset:1024\n\t"
"v_mfma_f32_16x16x32_bf16 v[236:239], v[236:239], a[208:211], 0\n\t"
"ds_read_b64_tr_b16 v[234:235], v241 offset:2560\n\t"
"v_mfma_f32_16x16x32_bf16 a[0:3], a[244:247], a[180:183], a[0:3]\n\t"
"v_mfma_f32_16x16x32_bf16 a[0:3], a[248:251], a[184:187], a[0:3]\n\t"
"v_mfma_f32_16x16x32_bf16 a[0:3], a[252:255], a[188:191], a[0:3]\n\t"
"v_mfma_f32_16x16x32_bf16 a[8:11], a[244:247], a[184:187], a[8:11]\n\t"
"s_waitcnt lgkmcnt(2)\n\t"
"v_cvt_pk_bf16_f32 v228, v228, v229\n\t"
"v_cvt_pk_bf16_f32 v229, v230, v231\n\t"
"v_mfma_f32_16x16x32_bf16 a[8:11], a[248:251], a[188:191], a[8:11]\n\t"
"s_waitcnt lgkmcnt(0)\n\t"
"v_cvt_pk_bf16_f32 v230, v236, v237\n\t"
"v_cvt_pk_bf16_f32 v231, v238, v239\n\t"
"v_mfma_f32_16x16x32_bf16 a[8:11], a[252:255], a[192:195], a[8:11]\n\t"
"ds_write_b128 v242, v[228:231] offset:27648\n\t"
"v_mfma_f32_16x16x32_bf16 v[224:227], v[224:227], a[208:211], 0\n\t"
"v_mfma_f32_16x16x32_bf16 v[232:235], v[232:235], a[208:211], 0\n\t"
"ds_read_b64_tr_b16 v[236:237], v240 offset:20992\n\t"
"v_mfma_f32_16x16x32_bf16 a[16:19], a[244:247], a[188:191], a[16:19]\n\t"
"ds_read_b64_tr_b16 v[238:239], v240 offset:22528\n\t"
"v_mfma_f32_16x16x32_bf16 a[16:19], a[248:251], a[192:195], a[16:19]\n\t"
"ds_read_b64_tr_b16 v[228:229], v241 offset:20992\n\t"
"v_mfma_f32_16x16x32_bf16 a[16:19], a[252:255], a[196:199], a[16:19]\n\t"
"ds_read_b64_tr_b16 v[230:231], v241 offset:22528\n\t"
"v_mfma_f32_16x16x32_bf16 a[24:27], a[244:247], a[192:195], a[24:27]\n\t"
"v_cvt_pk_bf16_f32 v224, v224, v225\n\t"
"v_cvt_pk_bf16_f32 v225, v226, v227\n\t"
"v_mfma_f32_16x16x32_bf16 a[24:27], a[248:251], a[196:199], a[24:27]\n\t"
"v_cvt_pk_bf16_f32 v226, v232, v233\n\t"
"v_cvt_pk_bf16_f32 v227, v234, v235\n\t"
"v_mfma_f32_16x16x32_bf16 a[24:27], a[252:255], a[200:203], a[24:27]\n\t"
"ds_write_b128 v242, v[224:227] offset:29184\n\t"
"v_mfma_f32_16x16x32_bf16 a[32:35], a[244:247], a[196:199], a[32:35]\n\t"
"buffer_load_dwordx4 a[244:247], v252, s[8:11], s20 offen offset:1024 sc1\n\t"
"v_mfma_f32_16x16x32_bf16 a[32:35], a[248:251], a[200:203], a[32:35]\n\t"
"s_waitcnt vmcnt(8)\n\t"
"ds_read_b64_tr_b16 v[232:233], v240 offset:40960\n\t"
"v_mfma_f32_16x16x32_bf16 a[32:35], a[252:255], a[204:207], a[32:35]\n\t"
"ds_read_b64_tr_b16 v[234:235], v240 offset:42496\n\t"
"v_mfma_f32_16x16x32_bf16 a[40:43], v[248:251], a[168:171], a[40:43]\n\t"
"buffer_load_dwordx4 a[248:251], v252, s[8:11], s20 offen offset:2048 sc1\n\t"
"v_mfma_f32_16x16x32_bf16 a[40:43], a[212:215], a[172:175], a[40:43]\n\t"
"s_mov_b32 s20, 761856\n\t"
"ds_read_b64_tr_b16 v[224:225], v241 offset:40960\n\t"
"v_mfma_f32_16x16x32_bf16 a[40:43], a[216:219], a[176:179], a[40:43]\n\t"
"ds_read_b64_tr_b16 v[226:227], v241 offset:42496\n\t"
"v_mfma_f32_16x16x32_bf16 a[48:51], v[248:251], a[172:175], a[48:51]\n\t"
"buffer_load_dwordx4 a[252:255], v252, s[8:11], s20 offen offset:0 sc1\n\t"
"v_mfma_f32_16x16x32_bf16 a[48:51], a[212:215], a[176:179], a[48:51]\n\t"
"s_waitcnt lgkmcnt(5)\n\t"
"v_mfma_f32_16x16x32_bf16 a[48:51], a[216:219], a[180:183], a[48:51]\n\t"
"v_mfma_f32_16x16x32_bf16 v[236:239], v[236:239], a[208:211], 0\n\t"
"v_mfma_f32_16x16x32_bf16 v[228:231], v[228:231], a[208:211], 0\n\t"
"v_xor_b32 v246, 0xc0, v246\n\t"
"v_mfma_f32_16x16x32_bf16 a[56:59], v[248:251], a[176:179], a[56:59]\n\t"
"v_mfma_f32_16x16x32_bf16 a[56:59], a[212:215], a[180:183], a[56:59]\n\t"
"v_mfma_f32_16x16x32_bf16 a[56:59], a[216:219], a[184:187], a[56:59]\n\t"
"v_mfma_f32_16x16x32_bf16 a[64:67], v[248:251], a[180:183], a[64:67]\n\t"
"s_waitcnt lgkmcnt(2)\n\t"
"v_cvt_pk_bf16_f32 v236, v236, v237\n\t"
"v_cvt_pk_bf16_f32 v237, v238, v239\n\t"
"v_mfma_f32_16x16x32_bf16 a[64:67], a[212:215], a[184:187], a[64:67]\n\t"
"s_waitcnt lgkmcnt(0)\n\t"
"v_cvt_pk_bf16_f32 v238, v228, v229\n\t"
"v_cvt_pk_bf16_f32 v239, v230, v231\n\t"
"v_mfma_f32_16x16x32_bf16 a[64:67], a[216:219], a[188:191], a[64:67]\n\t"
"ds_write_b128 v242, v[236:239] offset:30720\n\t"
"v_mfma_f32_16x16x32_bf16 v[232:235], v[232:235], a[208:211], 0\n\t"
"v_mfma_f32_16x16x32_bf16 v[224:227], v[224:227], a[208:211], 0\n\t"
"v_mfma_f32_16x16x32_bf16 a[72:75], v[248:251], a[184:187], a[72:75]\n\t"
"buffer_load_dwordx4 a[208:211], v252, s[8:11], s20 offen offset:1024 sc1\n\t"
"v_mfma_f32_16x16x32_bf16 a[72:75], a[212:215], a[188:191], a[72:75]\n\t"
"v_mfma_f32_16x16x32_bf16 a[72:75], a[216:219], a[192:195], a[72:75]\n\t"
"v_mfma_f32_16x16x32_bf16 a[80:83], v[248:251], a[188:191], a[80:83]\n\t"
"v_cvt_pk_bf16_f32 v232, v232, v233\n\t"
"v_cvt_pk_bf16_f32 v233, v234, v235\n\t"
"v_mfma_f32_16x16x32_bf16 a[80:83], a[212:215], a[192:195], a[80:83]\n\t"
"v_cvt_pk_bf16_f32 v234, v224, v225\n\t"
"v_cvt_pk_bf16_f32 v235, v226, v227\n\t"
"v_mfma_f32_16x16x32_bf16 a[80:83], a[216:219], a[196:199], a[80:83]\n\t"
"ds_write_b128 v242, v[232:235] offset:32256\n\t"
"v_mfma_f32_16x16x32_bf16 a[88:91], v[248:251], a[192:195], a[88:91]\n\t"
"v_mfma_f32_16x16x32_bf16 a[88:91], a[212:215], a[196:199], a[88:91]\n\t"
"ds_read_b64_tr_b16 v[228:229], v246 offset:1024\n\t"
"v_mfma_f32_16x16x32_bf16 a[88:91], a[216:219], a[200:203], a[88:91]\n\t"
"ds_read_b64_tr_b16 v[230:231], v246 offset:4096\n\t"
"v_mfma_f32_16x16x32_bf16 a[96:99], v[248:251], a[196:199], a[96:99]\n\t"
"buffer_load_dwordx4 v[248:251], v252, s[8:11], s20 offen offset:2048 sc1\n\t"
"v_mfma_f32_16x16x32_bf16 a[96:99], a[212:215], a[200:203], a[96:99]\n\t"
"s_waitcnt vmcnt(9)\n\t"
"ds_read_b64_tr_b16 v[236:237], v246 offset:7168\n\t"
"s_mov_b32 s20, 626688\n\t"
"v_mfma_f32_16x16x32_bf16 a[96:99], a[216:219], a[204:207], a[96:99]\n\t"
"ds_read_b64_tr_b16 v[238:239], v246 offset:10240\n\t"
"v_mfma_f32_16x16x32_bf16 a[104:107], a[220:223], a[168:171], a[104:107]\n\t"
"buffer_load_dwordx4 v[240:243], v252, s[8:11], s20 offen offset:0 sc1\n\t"
"v_mfma_f32_16x16x32_bf16 a[104:107], v[96:99], a[172:175], a[104:107]\n\t"
"ds_read_b64_tr_b16 v[224:225], v246 offset:13312\n\t"
"v_mfma_f32_16x16x32_bf16 a[104:107], a[224:227], a[176:179], a[104:107]\n\t"
"ds_read_b64_tr_b16 v[226:227], v246 offset:16384\n\t"
"v_mfma_f32_16x16x32_bf16 a[112:115], a[220:223], a[172:175], a[112:115]\n\t"
"buffer_load_dwordx4 a[212:215], v252, s[8:11], s20 offen offset:1024 sc1\n\t"
"v_mfma_f32_16x16x32_bf16 a[112:115], v[96:99], a[176:179], a[112:115]\n\t"
"ds_read_b128 a[168:171], v244 offset:9216\n\t"
"v_mfma_f32_16x16x32_bf16 a[112:115], a[224:227], a[180:183], a[112:115]\n\t"
"ds_read_b64_tr_b16 v[232:233], v246 offset:20992\n\t"
"v_mfma_f32_16x16x32_bf16 a[120:123], a[220:223], a[176:179], a[120:123]\n\t"
"buffer_load_dwordx4 a[216:219], v252, s[8:11], s20 offen offset:2048 sc1\n\t"
"v_mfma_f32_16x16x32_bf16 a[120:123], v[96:99], a[180:183], a[120:123]\n\t"
"s_waitcnt lgkmcnt(6)\n\t"
"ds_read_b64_tr_b16 v[234:235], v246 offset:24064\n\t"
"v_mfma_f32_16x16x32_bf16 a[120:123], a[224:227], a[184:187], a[120:123]\n\t"
"ds_read_b128 a[172:175], v244 offset:9728\n\t"
"v_mfma_f32_16x16x32_bf16 v[228:231], v[228:231], v[220:223], 0\n\t"
"ds_read_b128 a[176:179], v244 offset:10240\n\t"
"v_mfma_f32_16x16x32_bf16 a[128:131], a[220:223], a[180:183], a[128:131]\n\t"
"s_waitcnt lgkmcnt(7)\n\t"
"ds_read_b128 a[180:183], v244 offset:10752\n\t"
"v_mfma_f32_16x16x32_bf16 a[128:131], v[96:99], a[184:187], a[128:131]\n\t"
"v_mfma_f32_16x16x32_bf16 a[128:131], a[224:227], a[188:191], a[128:131]\n\t"
"v_mfma_f32_16x16x32_bf16 v[236:239], v[236:239], v[220:223], 0\n\t"
"v_mfma_f32_16x16x32_bf16 a[136:139], a[220:223], a[184:187], a[136:139]\n\t"
"s_waitcnt lgkmcnt(6)\n\t"
"ds_read_b128 a[184:187], v244 offset:11264\n\t"
"v_cvt_pk_bf16_f32 v228, v228, v229\n\t"
"v_mfma_f32_16x16x32_bf16 a[136:139], v[96:99], a[188:191], a[136:139]\n\t"
"v_cvt_pk_bf16_f32 v229, v230, v231\n\t"
"v_mfma_f32_16x16x32_bf16 a[136:139], a[224:227], a[192:195], a[136:139]\n\t"
"s_mov_b32 s20, 638976\n\t"
"v_mfma_f32_16x16x32_bf16 v[224:227], v[224:227], v[220:223], 0\n\t"
"v_mfma_f32_16x16x32_bf16 a[144:147], a[220:223], a[188:191], a[144:147]\n\t"
"s_waitcnt lgkmcnt(4)\n\t"
"ds_read_b128 a[188:191], v244 offset:11776\n\t"
"v_cvt_pk_bf16_f32 v230, v236, v237\n\t"
"v_mfma_f32_16x16x32_bf16 a[144:147], v[96:99], a[192:195], a[144:147]\n\t"
"v_cvt_pk_bf16_f32 v231, v238, v239\n\t"
"v_mfma_f32_16x16x32_bf16 a[144:147], a[224:227], a[196:199], a[144:147]\n\t"
"v_mfma_f32_16x16x32_bf16 v[232:235], v[232:235], v[220:223], 0\n\t"
"v_mfma_f32_16x16x32_bf16 a[152:155], a[220:223], a[192:195], a[152:155]\n\t"
"buffer_load_dwordx4 v[236:239], v252, s[8:11], s20 offen offset:0 sc1\n\t"
"v_mfma_f32_16x16x32_bf16 a[152:155], v[96:99], a[196:199], a[152:155]\n\t"
"ds_read_b128 a[192:195], v244 offset:12288\n\t"
"v_cvt_pk_bf16_f32 v224, v224, v225\n\t"
"v_mfma_f32_16x16x32_bf16 a[152:155], a[224:227], a[200:203], a[152:155]\n\t"
"v_cvt_pk_bf16_f32 v225, v226, v227\n\t"
"v_mfma_f32_16x16x32_bf16 a[160:163], a[220:223], a[196:199], a[160:163]\n\t"
"buffer_load_dwordx4 a[220:223], v252, s[8:11], s20 offen offset:1024 sc1\n\t"
"v_mfma_f32_16x16x32_bf16 a[160:163], v[96:99], a[200:203], a[160:163]\n\t"
"s_waitcnt vmcnt(11) lgkmcnt(4)\n\t"
"ds_read_b128 a[196:199], v244 offset:12800\n\t"
"v_cvt_pk_bf16_f32 v226, v232, v233\n\t"
"v_mfma_f32_16x16x32_bf16 a[160:163], a[224:227], a[204:207], a[160:163]\n\t"
"ds_read_b128 a[200:203], v244 offset:13312\n\t"
"v_cvt_pk_bf16_f32 v227, v234, v235\n\t"
"v_mfma_f32_16x16x32_bf16 v[200:203], a[228:231], a[168:171], v[200:203]\n\t"
"ds_read_b64_tr_b16 v[232:233], v246 offset:27136\n\t"
"v_mfma_f32_16x16x32_bf16 v[200:203], a[232:235], a[172:175], v[200:203]\n\t"
"s_waitcnt lgkmcnt(6)\n\t"
"ds_read_b64_tr_b16 v[234:235], v246 offset:30208\n\t"
"v_mfma_f32_16x16x32_bf16 v[200:203], a[236:239], a[176:179], v[200:203]\n\t"
"s_waitcnt lgkmcnt(0)\n\t"
"ds_read_b128 a[204:207], v244 offset:13824\n\t"
"v_mfma_f32_16x16x32_bf16 v[232:235], v[232:235], v[220:223], 0\n\t"
"buffer_load_dwordx4 v[96:99], v252, s[8:11], s20 offen offset:2048 sc1\n\t"
"s_mov_b32 s20, 651264\n\t"
"v_mfma_f32_16x16x32_bf16 v[208:211], a[228:231], a[172:175], v[208:211]\n\t"
"buffer_load_dwordx4 a[224:227], v252, s[8:11], s20 offen offset:0 sc1\n\t"
"v_mfma_f32_16x16x32_bf16 v[208:211], a[232:235], a[176:179], v[208:211]\n\t"
"s_add_u32 m0, s24, 256\n\t"
"s_add_u32 s28, s21, 0\n\t"
"v_mfma_f32_16x16x32_bf16 v[208:211], a[236:239], a[180:183], v[208:211]\n\t"
"s_waitcnt lgkmcnt(0)\n\t"
"s_barrier\n\t"
"v_mfma_f32_16x16x32_bf16 v[216:219], a[228:231], a[176:179], v[216:219]\n\t"
"v_mfma_f32_16x16x32_bf16 v[216:219], a[232:235], a[180:183], v[216:219]\n\t"
"buffer_load_dwordx4 v247, s[12:15], s28 offen offset:256 nt lds\n\t"
"v_mfma_f32_16x16x32_bf16 v[216:219], a[236:239], a[184:187], v[216:219]\n\t"
"s_add_u32 m0, s24, 6400\n\t"
"s_add_u32 s28, s21, 196608\n\t"
"ds_write2st64_b64 v245, v[228:229], v[230:231] offset0:0 offset1:1\n\t"
"v_mfma_f32_16x16x32_bf16 a[4:7], a[228:231], a[180:183], a[4:7]\n\t"
"buffer_load_dwordx4 v247, s[12:15], s28 offen offset:256 nt lds\n\t"
"v_mfma_f32_16x16x32_bf16 a[4:7], a[232:235], a[184:187], a[4:7]\n\t"
"s_add_u32 m0, s24, 12544\n\t"
"s_add_u32 s28, s21, 393216\n\t"
"ds_read_b64_tr_b16 v[228:229], v246 offset:33280\n\t"
"v_mfma_f32_16x16x32_bf16 a[4:7], a[236:239], a[188:191], a[4:7]\n\t"
"ds_write2st64_b64 v245, v[224:225], v[226:227] offset0:2 offset1:3\n\t"
"v_cvt_pk_bf16_f32 v232, v232, v233\n\t"
"v_cvt_pk_bf16_f32 v233, v234, v235\n\t"
"v_mfma_f32_16x16x32_bf16 a[12:15], a[228:231], a[184:187], a[12:15]\n\t"
"v_mfma_f32_16x16x32_bf16 a[12:15], a[232:235], a[188:191], a[12:15]\n\t"
"ds_read_b64_tr_b16 v[230:231], v246 offset:36352\n\t"
"v_mfma_f32_16x16x32_bf16 a[12:15], a[236:239], a[192:195], a[12:15]\n\t"
"buffer_load_dwordx4 v247, s[12:15], s28 offen offset:256 nt lds\n\t"
"s_add_u32 m0, s24, 20224\n\t"
"s_add_u32 s28, s21, 589824\n\t"
"v_mfma_f32_16x16x32_bf16 a[20:23], a[228:231], a[188:191], a[20:23]\n\t"
"buffer_load_dwordx4 v247, s[12:15], s28 offen offset:256 nt lds\n\t"
"v_mfma_f32_16x16x32_bf16 a[20:23], a[232:235], a[192:195], a[20:23]\n\t"
"s_add_u32 m0, s24, 26368\n\t"
"s_add_u32 s28, s21, 786432\n\t"
"ds_read_b64_tr_b16 v[224:225], v246 offset:40960\n\t"
"v_mfma_f32_16x16x32_bf16 a[20:23], a[236:239], a[196:199], a[20:23]\n\t"
"s_waitcnt lgkmcnt(1)\n\t"
"ds_write_b64 v245, v[232:233] offset:2048\n\t"
"ds_read_b64_tr_b16 v[226:227], v246 offset:44032\n\t"
"v_mfma_f32_16x16x32_bf16 v[228:231], v[228:231], v[220:223], 0\n\t"
"buffer_load_dwordx4 v247, s[12:15], s28 offen offset:256 nt lds\n\t"
"s_add_u32 m0, s24, 60160\n\t"
"s_add_u32 s28, s21, 1769472\n\t"
"v_mfma_f32_16x16x32_bf16 a[28:31], a[228:231], a[192:195], a[28:31]\n\t"
"buffer_load_dwordx4 v247, s[12:15], s28 offen offset:256 nt lds\n\t"
"v_mfma_f32_16x16x32_bf16 a[28:31], a[232:235], a[196:199], a[28:31]\n\t"
"ds_read_b64_tr_b16 v[232:233], v246 offset:47104\n\t"
"v_mfma_f32_16x16x32_bf16 a[28:31], a[236:239], a[200:203], a[28:31]\n\t"
"s_waitcnt lgkmcnt(1)\n\t"
"ds_read_b64_tr_b16 v[234:235], v246 offset:50176\n\t"
"v_mfma_f32_16x16x32_bf16 v[224:227], v[224:227], v[220:223], 0\n\t"
"v_mfma_f32_16x16x32_bf16 a[36:39], a[228:231], a[196:199], a[36:39]\n\t"
"v_cvt_pk_bf16_f32 v228, v228, v229\n\t"
"v_cvt_pk_bf16_f32 v229, v230, v231\n\t"
"v_mfma_f32_16x16x32_bf16 a[36:39], a[232:235], a[200:203], a[36:39]\n\t"
"s_waitcnt vmcnt(16)\n\t"
"v_mfma_f32_16x16x32_bf16 a[36:39], a[236:239], a[204:207], a[36:39]\n\t"
"s_waitcnt lgkmcnt(0)\n\t"
"v_mfma_f32_16x16x32_bf16 v[232:235], v[232:235], v[220:223], 0\n\t"
"v_mfma_f32_16x16x32_bf16 a[44:47], a[240:243], a[168:171], a[44:47]\n\t"
"v_cvt_pk_bf16_f32 v230, v224, v225\n\t"
"v_cvt_pk_bf16_f32 v231, v226, v227\n\t"
"v_mfma_f32_16x16x32_bf16 a[44:47], a[244:247], a[172:175], a[44:47]\n\t"
"ds_read_b64_tr_b16 v[224:225], v246 offset:53248\n\t"
"v_mfma_f32_16x16x32_bf16 a[44:47], a[248:251], a[176:179], a[44:47]\n\t"
"ds_write2st64_b64 v245, v[228:229], v[230:231] offset0:5 offset1:6\n\t"
"v_mfma_f32_16x16x32_bf16 a[52:55], a[240:243], a[172:175], a[52:55]\n\t"
"v_mfma_f32_16x16x32_bf16 a[52:55], a[244:247], a[176:179], a[52:55]\n\t"
"ds_read_b64_tr_b16 v[226:227], v246 offset:56320\n\t"
"v_mfma_f32_16x16x32_bf16 a[52:55], a[248:251], a[180:183], a[52:55]\n\t"
"v_cvt_pk_bf16_f32 v232, v232, v233\n\t"
"v_cvt_pk_bf16_f32 v233, v234, v235\n\t"
"v_mfma_f32_16x16x32_bf16 a[60:63], a[240:243], a[176:179], a[60:63]\n\t"
"v_mfma_f32_16x16x32_bf16 a[60:63], a[244:247], a[180:183], a[60:63]\n\t"
"v_mfma_f32_16x16x32_bf16 a[60:63], a[248:251], a[184:187], a[60:63]\n\t"
"v_mfma_f32_16x16x32_bf16 a[68:71], a[240:243], a[180:183], a[68:71]\n\t"
"v_mfma_f32_16x16x32_bf16 a[68:71], a[244:247], a[184:187], a[68:71]\n\t"
"v_mfma_f32_16x16x32_bf16 a[68:71], a[248:251], a[188:191], a[68:71]\n\t"
"v_mfma_f32_16x16x32_bf16 a[76:79], a[240:243], a[184:187], a[76:79]\n\t"
"s_waitcnt lgkmcnt(0)\n\t"
"v_mfma_f32_16x16x32_bf16 a[76:79], a[244:247], a[188:191], a[76:79]\n\t"
"v_mfma_f32_16x16x32_bf16 a[76:79], a[248:251], a[192:195], a[76:79]\n\t"
"v_mfma_f32_16x16x32_bf16 v[224:227], v[224:227], v[220:223], 0\n\t"
"v_mfma_f32_16x16x32_bf16 a[84:87], a[240:243], a[188:191], a[84:87]\n\t"
"v_mfma_f32_16x16x32_bf16 a[84:87], a[244:247], a[192:195], a[84:87]\n\t"
"buffer_load_dwordx4 a[228:231], v252, s[8:11], s20 offen offset:1024 sc1\n\t"
"v_mfma_f32_16x16x32_bf16 a[84:87], a[248:251], a[196:199], a[84:87]\n\t"
"v_mfma_f32_16x16x32_bf16 a[92:95], a[240:243], a[192:195], a[92:95]\n\t"
"v_mfma_f32_16x16x32_bf16 a[92:95], a[244:247], a[196:199], a[92:95]\n\t"
"v_cvt_pk_bf16_f32 v234, v224, v225\n\t"
"v_cvt_pk_bf16_f32 v235, v226, v227\n\t"
"v_mfma_f32_16x16x32_bf16 a[92:95], a[248:251], a[200:203], a[92:95]\n\t"
"ds_write2st64_b64 v245, v[232:233], v[234:235] offset0:7 offset1:8\n\t"
"v_mfma_f32_16x16x32_bf16 a[100:103], a[240:243], a[196:199], a[100:103]\n\t"
"s_waitcnt vmcnt(14)\n\t"
"v_mfma_f32_16x16x32_bf16 a[100:103], a[244:247], a[200:203], a[100:103]\n\t"
"v_mfma_f32_16x16x32_bf16 a[100:103], a[248:251], a[204:207], a[100:103]\n\t"
"v_mfma_f32_16x16x32_bf16 a[108:111], a[252:255], a[168:171], a[108:111]\n\t"
"v_mfma_f32_16x16x32_bf16 a[108:111], a[208:211], a[172:175], a[108:111]\n\t"
"v_mfma_f32_16x16x32_bf16 a[108:111], v[248:251], a[176:179], a[108:111]\n\t"
"s_waitcnt lgkmcnt(0)\n\t"
"s_barrier\n\t"
"v_mfma_f32_16x16x32_bf16 a[116:119], a[252:255], a[172:175], a[116:119]\n\t"
"ds_read_b128 a[168:171], v244 offset:0\n\t"
"v_mfma_f32_16x16x32_bf16 a[116:119], a[208:211], a[176:179], a[116:119]\n\t"
"ds_read_b128 a[172:175], v244 offset:512\n\t"
"v_mfma_f32_16x16x32_bf16 a[116:119], v[248:251], a[180:183], a[116:119]\n\t"
"v_mfma_f32_16x16x32_bf16 a[124:127], a[252:255], a[176:179], a[124:127]\n\t"
"ds_read_b128 a[176:179], v244 offset:1024\n\t"
"v_mfma_f32_16x16x32_bf16 a[124:127], a[208:211], a[180:183], a[124:127]\n\t"
"v_mfma_f32_16x16x32_bf16 a[124:127], v[248:251], a[184:187], a[124:127]\n\t"
"v_mfma_f32_16x16x32_bf16 a[132:135], a[252:255], a[180:183], a[132:135]\n\t"
"ds_read_b128 a[180:183], v244 offset:1536\n\t"
"v_mfma_f32_16x16x32_bf16 a[132:135], a[208:211], a[184:187], a[132:135]\n\t"
"v_mfma_f32_16x16x32_bf16 a[132:135], v[248:251], a[188:191], a[132:135]\n\t"
"v_mfma_f32_16x16x32_bf16 a[140:143], a[252:255], a[184:187], a[140:143]\n\t"
"ds_read_b128 a[184:187], v244 offset:2048\n\t"
"v_mfma_f32_16x16x32_bf16 a[140:143], a[208:211], a[188:191], a[140:143]\n\t"
"v_mfma_f32_16x16x32_bf16 a[140:143], v[248:251], a[192:195], a[140:143]\n\t"
"v_mfma_f32_16x16x32_bf16 a[148:151], a[252:255], a[188:191], a[148:151]\n\t"
"ds_read_b128 a[188:191], v244 offset:2560\n\t"
"v_mfma_f32_16x16x32_bf16 a[148:151], a[208:211], a[192:195], a[148:151]\n\t"
"v_mfma_f32_16x16x32_bf16 a[148:151], v[248:251], a[196:199], a[148:151]\n\t"
"v_mfma_f32_16x16x32_bf16 a[156:159], a[252:255], a[192:195], a[156:159]\n\t"
"ds_read_b128 a[192:195], v244 offset:3072\n\t"
"v_mfma_f32_16x16x32_bf16 a[156:159], a[208:211], a[196:199], a[156:159]\n\t"
"v_mfma_f32_16x16x32_bf16 a[156:159], v[248:251], a[200:203], a[156:159]\n\t"
"v_mfma_f32_16x16x32_bf16 a[164:167], a[252:255], a[196:199], a[164:167]\n\t"
"s_waitcnt vmcnt(11) lgkmcnt(4)\n\t"
"ds_read_b128 a[196:199], v244 offset:3584\n\t"
"v_mfma_f32_16x16x32_bf16 a[164:167], a[208:211], a[200:203], a[164:167]\n\t"
"ds_read_b128 a[200:203], v244 offset:4096\n\t"
"v_mfma_f32_16x16x32_bf16 a[164:167], v[248:251], a[204:207], a[164:167]\n\t"
"ds_read_b128 a[204:207], v244 offset:4608\n\t"
"Lst7_%=:\n\t"
"v_mfma_f32_16x16x32_bf16 v[0:3], v[240:243], a[168:171], v[0:3]\n\t"
"buffer_load_dwordx4 a[208:211], v252, s[8:11], s20 offen offset:2048 sc1\n\t"
"v_mfma_f32_16x16x32_bf16 v[0:3], a[212:215], a[172:175], v[0:3]\n\t"
"s_waitcnt lgkmcnt(6)\n\t"
"s_mov_b32 s20, 663552\n\t"
"v_mfma_f32_16x16x32_bf16 v[0:3], a[216:219], a[176:179], v[0:3]\n\t"
"v_mfma_f32_16x16x32_bf16 v[4:7], v[240:243], a[172:175], v[4:7]\n\t"
"s_waitcnt lgkmcnt(5)\n\t"
"v_mfma_f32_16x16x32_bf16 v[4:7], a[212:215], a[176:179], v[4:7]\n\t"
"buffer_load_dwordx4 v[248:251], v252, s[8:11], s20 offen offset:0 sc1\n\t"
"v_mfma_f32_16x16x32_bf16 v[4:7], a[216:219], a[180:183], v[4:7]\n\t"
"v_mfma_f32_16x16x32_bf16 v[8:11], v[240:243], a[176:179], v[8:11]\n\t"
"s_waitcnt lgkmcnt(4)\n\t"
"v_mfma_f32_16x16x32_bf16 v[8:11], a[212:215], a[180:183], v[8:11]\n\t"
"v_mfma_f32_16x16x32_bf16 v[8:11], a[216:219], a[184:187], v[8:11]\n\t"
"buffer_load_dwordx4 a[232:235], v252, s[8:11], s20 offen offset:1024 sc1\n\t"
"v_mfma_f32_16x16x32_bf16 v[12:15], v[240:243], a[180:183], v[12:15]\n\t"
"s_waitcnt lgkmcnt(3)\n\t"
"v_mfma_f32_16x16x32_bf16 v[12:15], a[212:215], a[184:187], v[12:15]\n\t"
"v_mfma_f32_16x16x32_bf16 v[12:15], a[216:219], a[188:191], v[12:15]\n\t"
"v_mfma_f32_16x16x32_bf16 v[16:19], v[240:243], a[184:187], v[16:19]\n\t"
"buffer_load_dwordx4 a[236:239], v252, s[8:11], s20 offen offset:2048 sc1\n\t"
"v_mfma_f32_16x16x32_bf16 v[16:19], a[212:215], a[188:191], v[16:19]\n\t"
"s_waitcnt lgkmcnt(2)\n\t"
"s_mov_b32 s20, 675840\n\t"
"v_mfma_f32_16x16x32_bf16 v[16:19], a[216:219], a[192:195], v[16:19]\n\t"
"ds_read_b64_tr_b16 v[228:229], v246 offset:1536\n\t"
"v_mfma_f32_16x16x32_bf16 v[20:23], v[240:243], a[188:191], v[20:23]\n\t"
"s_waitcnt lgkmcnt(2)\n\t"
"ds_read_b64_tr_b16 v[230:231], v246 offset:4608\n\t"
"v_mfma_f32_16x16x32_bf16 v[20:23], a[212:215], a[192:195], v[20:23]\n\t"
"buffer_load_dwordx4 a[240:243], v252, s[8:11], s20 offen offset:0 sc1\n\t"
"v_mfma_f32_16x16x32_bf16 v[20:23], a[216:219], a[196:199], v[20:23]\n\t"
"v_mfma_f32_16x16x32_bf16 v[24:27], v[240:243], a[192:195], v[24:27]\n\t"
"s_waitcnt lgkmcnt(2)\n\t"
"v_mfma_f32_16x16x32_bf16 v[24:27], a[212:215], a[196:199], v[24:27]\n\t"
"v_mfma_f32_16x16x32_bf16 v[24:27], a[216:219], a[200:203], v[24:27]\n\t"
"buffer_load_dwordx4 a[244:247], v252, s[8:11], s20 offen offset:1024 sc1\n\t"
"v_mfma_f32_16x16x32_bf16 v[28:31], v[240:243], a[196:199], v[28:31]\n\t"
"s_waitcnt vmcnt(14)\n\t"
"ds_read_b64_tr_b16 v[224:225], v246 offset:7680\n\t"
"v_mfma_f32_16x16x32_bf16 v[28:31], a[212:215], a[200:203], v[28:31]\n\t"
"ds_read_b64_tr_b16 v[226:227], v246 offset:10752\n\t"
"v_mfma_f32_16x16x32_bf16 v[28:31], a[216:219], a[204:207], v[28:31]\n\t"
"v_mfma_f32_16x16x32_bf16 v[32:35], v[236:239], a[168:171], v[32:35]\n\t"
"buffer_load_dwordx4 v[240:243], v252, s[8:11], s20 offen offset:2048 sc1\n\t"
"v_mfma_f32_16x16x32_bf16 v[32:35], a[220:223], a[172:175], v[32:35]\n\t"
"s_mov_b32 s20, 688128\n\t"
"v_mfma_f32_16x16x32_bf16 v[32:35], v[96:99], a[176:179], v[32:35]\n\t"
"v_mfma_f32_16x16x32_bf16 v[36:39], v[236:239], a[172:175], v[36:39]\n\t"
"s_waitcnt lgkmcnt(2)\n\t"
"v_mfma_f32_16x16x32_bf16 v[36:39], a[220:223], a[176:179], v[36:39]\n\t"
"buffer_load_dwordx4 a[212:215], v252, s[8:11], s20 offen offset:0 sc1\n\t"
"v_mfma_f32_16x16x32_bf16 v[36:39], v[96:99], a[180:183], v[36:39]\n\t"
"v_mfma_f32_16x16x32_bf16 v[228:231], v[228:231], v[220:223], 0\n\t"
"v_mfma_f32_16x16x32_bf16 v[40:43], v[236:239], a[176:179], v[40:43]\n\t"
"ds_read_b64_tr_b16 v[232:233], v246 offset:13824\n\t"
"v_mfma_f32_16x16x32_bf16 v[40:43], a[220:223], a[180:183], v[40:43]\n\t"
"buffer_load_dwordx4 a[216:219], v252, s[8:11], s20 offen offset:1024 sc1\n\t"
"v_mfma_f32_16x16x32_bf16 v[40:43], v[96:99], a[184:187], v[40:43]\n\t"
"ds_read_b64_tr_b16 v[234:235], v246 offset:16896\n\t"
"v_mfma_f32_16x16x32_bf16 v[44:47], v[236:239], a[180:183], v[44:47]\n\t"
"s_waitcnt lgkmcnt(2)\n\t"
"v_mfma_f32_16x16x32_bf16 v[44:47], a[220:223], a[184:187], v[44:47]\n\t"
"buffer_load_dwordx4 a[248:251], v252, s[8:11], s20 offen offset:2048 sc1\n\t"
"v_mfma_f32_16x16x32_bf16 v[44:47], v[96:99], a[188:191], v[44:47]\n\t"
"s_mov_b32 s20, 921600\n\t"
"v_cvt_pk_bf16_f32 v228, v228, v229\n\t"
"v_cvt_pk_bf16_f32 v229, v230, v231\n\t"
"v_mfma_f32_16x16x32_bf16 v[224:227], v[224:227], v[220:223], 0\n\t"
"v_mfma_f32_16x16x32_bf16 v[48:51], v[236:239], a[184:187], v[48:51]\n\t"
"v_mfma_f32_16x16x32_bf16 v[48:51], a[220:223], a[188:191], v[48:51]\n\t"
"buffer_load_dwordx4 a[252:255], v252, s[8:11], s20 offen offset:0 sc1\n\t"
"v_mfma_f32_16x16x32_bf16 v[48:51], v[96:99], a[192:195], v[48:51]\n\t"
"v_mfma_f32_16x16x32_bf16 v[52:55], v[236:239], a[188:191], v[52:55]\n\t"
"v_mfma_f32_16x16x32_bf16 v[52:55], a[220:223], a[192:195], v[52:55]\n\t"
"v_cvt_pk_bf16_f32 v230, v224, v225\n\t"
"v_cvt_pk_bf16_f32 v231, v226, v227\n\t"
"v_mfma_f32_16x16x32_bf16 v[52:55], v[96:99], a[196:199], v[52:55]\n\t"
"ds_read_b64_tr_b16 v[224:225], v246 offset:21504\n\t"
"v_mfma_f32_16x16x32_bf16 v[56:59], v[236:239], a[192:195], v[56:59]\n\t"
"s_waitcnt lgkmcnt(1)\n\t"
"ds_read_b64_tr_b16 v[226:227], v246 offset:24576\n\t"
"v_mfma_f32_16x16x32_bf16 v[56:59], a[220:223], a[196:199], v[56:59]\n\t"
"v_mfma_f32_16x16x32_bf16 v[56:59], v[96:99], a[200:203], v[56:59]\n\t"
"v_mfma_f32_16x16x32_bf16 v[232:235], v[232:235], v[220:223], 0\n\t"
"v_mfma_f32_16x16x32_bf16 v[60:63], v[236:239], a[196:199], v[60:63]\n\t"
"buffer_load_dwordx4 v[236:239], v252, s[8:11], s20 offen offset:1024 sc1\n\t"
"v_mfma_f32_16x16x32_bf16 v[60:63], a[220:223], a[200:203], v[60:63]\n\t"
"s_waitcnt vmcnt(11)\n\t"
"v_mfma_f32_16x16x32_bf16 v[60:63], v[96:99], a[204:207], v[60:63]\n\t"
"v_mfma_f32_16x16x32_bf16 v[64:67], a[224:227], a[168:171], v[64:67]\n\t"
"buffer_load_dwordx4 v[96:99], v252, s[8:11], s20 offen offset:2048 sc1\n\t"
"v_mfma_f32_16x16x32_bf16 v[64:67], a[228:231], a[172:175], v[64:67]\n\t"
"s_mov_b32 s20, 933888\n\t"
"ds_read_b128 a[168:171], v244 offset:9216\n\t"
"v_cvt_pk_bf16_f32 v232, v232, v233\n\t"
"v_mfma_f32_16x16x32_bf16 v[64:67], a[208:211], a[176:179], v[64:67]\n\t"
"v_cvt_pk_bf16_f32 v233, v234, v235\n\t"
"v_mfma_f32_16x16x32_bf16 v[68:71], a[224:227], a[172:175], v[68:71]\n\t"
"buffer_load_dwordx4 a[220:223], v252, s[8:11], s20 offen offset:0 sc1\n\t"
"v_mfma_f32_16x16x32_bf16 v[68:71], a[228:231], a[176:179], v[68:71]\n\t"
"s_waitcnt lgkmcnt(1)\n\t"
"ds_read_b128 a[172:175], v244 offset:9728\n\t"
"v_mfma_f32_16x16x32_bf16 v[68:71], a[208:211], a[180:183], v[68:71]\n\t"
"v_mfma_f32_16x16x32_bf16 v[224:227], v[224:227], v[220:223], 0\n\t"
"v_mfma_f32_16x16x32_bf16 v[72:75], a[224:227], a[176:179], v[72:75]\n\t"
"ds_read_b128 a[176:179], v244 offset:10240\n\t"
"v_mfma_f32_16x16x32_bf16 v[72:75], a[228:231], a[180:183], v[72:75]\n\t"
"v_mfma_f32_16x16x32_bf16 v[72:75], a[208:211], a[184:187], v[72:75]\n\t"
"v_mfma_f32_16x16x32_bf16 v[76:79], a[224:227], a[180:183], v[76:79]\n\t"
"ds_read_b128 a[180:183], v244 offset:10752\n\t"
"v_mfma_f32_16x16x32_bf16 v[76:79], a[228:231], a[184:187], v[76:79]\n\t"
"v_cvt_pk_bf16_f32 v234, v224, v225\n\t"
"v_cvt_pk_bf16_f32 v235, v226, v227\n\t"
"v_mfma_f32_16x16x32_bf16 v[76:79], a[208:211], a[188:191], v[76:79]\n\t"
"ds_read_b64_tr_b16 v[224:225], v246 offset:27648\n\t"
"v_mfma_f32_16x16x32_bf16 v[80:83], a[224:227], a[184:187], v[80:83]\n\t"
"ds_read_b64_tr_b16 v[226:227], v246 offset:30720\n\t"
"v_mfma_f32_16x16x32_bf16 v[80:83], a[228:231], a[188:191], v[80:83]\n\t"
"ds_read_b128 a[184:187], v244 offset:11264\n\t"
"v_mfma_f32_16x16x32_bf16 v[80:83], a[208:211], a[192:195], v[80:83]\n\t"
"v_mfma_f32_16x16x32_bf16 v[84:87], a[224:227], a[188:191], v[84:87]\n\t"
"ds_read_b128 a[188:191], v244 offset:11776\n\t"
"v_mfma_f32_16x16x32_bf16 v[84:87], a[228:231], a[192:195], v[84:87]\n\t"
"v_mfma_f32_16x16x32_bf16 v[84:87], a[208:211], a[196:199], v[84:87]\n\t"
"v_mfma_f32_16x16x32_bf16 v[88:91], a[224:227], a[192:195], v[88:91]\n\t"
"ds_read_b128 a[192:195], v244 offset:12288\n\t"
"v_mfma_f32_16x16x32_bf16 v[88:91], a[228:231], a[196:199], v[88:91]\n\t"
"v_mfma_f32_16x16x32_bf16 v[88:91], a[208:211], a[200:203], v[88:91]\n\t"
"v_mfma_f32_16x16x32_bf16 v[92:95], a[224:227], a[196:199], v[92:95]\n\t"
"buffer_load_dwordx4 a[224:227], v252, s[8:11], s20 offen offset:1024 sc1\n\t"
"v_mfma_f32_16x16x32_bf16 v[92:95], a[228:231], a[200:203], v[92:95]\n\t"
"s_waitcnt vmcnt(11) lgkmcnt(3)\n\t"
"ds_read_b128 a[196:199], v244 offset:12800\n\t"
"v_mfma_f32_16x16x32_bf16 v[92:95], a[208:211], a[204:207], v[92:95]\n\t"
"ds_read_b128 a[200:203], v244 offset:13312\n\t"
"v_mfma_f32_16x16x32_bf16 v[224:227], v[224:227], v[220:223], 0\n\t"
"ds_read_b128 a[204:207], v244 offset:13824\n\t"
"v_mfma_f32_16x16x32_bf16 v[100:103], v[248:251], a[168:171], v[100:103]\n\t"
"buffer_load_dwordx4 a[208:211], v252, s[8:11], s20 offen offset:2048 sc1\n\t"
"v_mfma_f32_16x16x32_bf16 v[100:103], a[232:235], a[172:175], v[100:103]\n\t"
"s_mov_b32 s20, 946176\n\t"
"v_mfma_f32_16x16x32_bf16 v[100:103], a[236:239], a[176:179], v[100:103]\n\t"
"buffer_load_dwordx4 a[228:231], v252, s[8:11], s20 offen offset:0 sc1\n\t"
"v_mfma_f32_16x16x32_bf16 v[104:107], v[248:251], a[172:175], v[104:107]\n\t"
"s_waitcnt lgkmcnt(5)\n\t"
"v_mfma_f32_16x16x32_bf16 v[104:107], a[232:235], a[176:179], v[104:107]\n\t"
"v_cvt_pk_bf16_f32 v224, v224, v225\n\t"
"v_mfma_f32_16x16x32_bf16 v[104:107], a[236:239], a[180:183], v[104:107]\n\t"
"s_waitcnt lgkmcnt(0)\n\t"
"s_barrier\n\t"
"v_mfma_f32_16x16x32_bf16 v[108:111], v[248:251], a[176:179], v[108:111]\n\t"
"v_cvt_pk_bf16_f32 v225, v226, v227\n\t"
"v_mfma_f32_16x16x32_bf16 v[108:111], a[232:235], a[180:183], v[108:111]\n\t"
"v_mfma_f32_16x16x32_bf16 v[108:111], a[236:239], a[184:187], v[108:111]\n\t"
"ds_write2st64_b64 v245, v[228:229], v[230:231] offset0:0 offset1:1\n\t"
"v_mfma_f32_16x16x32_bf16 v[112:115], v[248:251], a[180:183], v[112:115]\n\t"
"v_mfma_f32_16x16x32_bf16 v[112:115], a[232:235], a[184:187], v[112:115]\n\t"
"ds_read_b64_tr_b16 v[228:229], v246 offset:33792\n\t"
"v_mfma_f32_16x16x32_bf16 v[112:115], a[236:239], a[188:191], v[112:115]\n\t"
"ds_write2st64_b64 v245, v[232:233], v[234:235] offset0:2 offset1:3\n\t"
"v_mfma_f32_16x16x32_bf16 v[116:119], v[248:251], a[184:187], v[116:119]\n\t"
"v_mfma_f32_16x16x32_bf16 v[116:119], a[232:235], a[188:191], v[116:119]\n\t"
"ds_read_b64_tr_b16 v[230:231], v246 offset:36864\n\t"
"v_mfma_f32_16x16x32_bf16 v[116:119], a[236:239], a[192:195], v[116:119]\n\t"
"s_add_u32 m0, s24, 66304\n\t"
"s_add_u32 s28, s21, 1966080\n\t"
"ds_write_b64 v245, v[224:225] offset:2048\n\t"
"v_mfma_f32_16x16x32_bf16 v[120:123], v[248:251], a[188:191], v[120:123]\n\t"
"ds_read_b64_tr_b16 v[232:233], v246 offset:41472\n\t"
"v_mfma_f32_16x16x32_bf16 v[120:123], a[232:235], a[192:195], v[120:123]\n\t"
"ds_read_b64_tr_b16 v[234:235], v246 offset:44544\n\t"
"v_mfma_f32_16x16x32_bf16 v[120:123], a[236:239], a[196:199], v[120:123]\n\t"
"s_waitcnt lgkmcnt(3)\n\t"
"ds_read_b64_tr_b16 v[224:225], v246 offset:47616\n\t"
"v_mfma_f32_16x16x32_bf16 v[228:231], v[228:231], v[220:223], 0\n\t"
"ds_read_b64_tr_b16 v[226:227], v246 offset:50688\n\t"
"v_mfma_f32_16x16x32_bf16 v[124:127], v[248:251], a[192:195], v[124:127]\n\t"
"buffer_load_dwordx4 v247, s[12:15], s28 offen offset:256 nt lds\n\t"
"v_mfma_f32_16x16x32_bf16 v[124:127], a[232:235], a[196:199], v[124:127]\n\t"
"s_add_u32 m0, s24, 72448\n\t"
"s_add_u32 s28, s21, 2162688\n\t"
"v_mfma_f32_16x16x32_bf16 v[124:127], a[236:239], a[200:203], v[124:127]\n\t"
"s_waitcnt lgkmcnt(2)\n\t"
"buffer_load_dwordx4 v247, s[12:15], s28 offen offset:256 nt lds\n\t"
"s_add_u32 m0, s24, 80128\n\t"
"s_add_u32 s28, s21, 2359296\n\t"
"v_mfma_f32_16x16x32_bf16 v[232:235], v[232:235], v[220:223], 0\n\t"
"buffer_load_dwordx4 v247, s[12:15], s28 offen offset:256 nt lds\n\t"
"v_mfma_f32_16x16x32_bf16 v[128:131], v[248:251], a[196:199], v[128:131]\n\t"
"s_waitcnt lgkmcnt(0)\n\t"
"s_add_u32 m0, s24, 86272\n\t"
"s_add_u32 s28, s21, 2555904\n\t"
"v_mfma_f32_16x16x32_bf16 v[128:131], a[232:235], a[200:203], v[128:131]\n\t"
"buffer_load_dwordx4 v247, s[12:15], s28 offen offset:256 nt lds\n\t"
"v_mfma_f32_16x16x32_bf16 v[128:131], a[236:239], a[204:207], v[128:131]\n\t"
"s_waitcnt vmcnt(14)\n\t"
"s_add_u32 m0, s24, 32512\n\t"
"s_add_u32 s28, s21, 983040\n\t"
"v_mfma_f32_16x16x32_bf16 v[224:227], v[224:227], v[220:223], 0\n\t"
"buffer_load_dwordx4 v247, s[12:15], s28 offen offset:256 nt lds\n\t"
"v_cvt_pk_bf16_f32 v228, v228, v229\n\t"
"v_cvt_pk_bf16_f32 v229, v230, v231\n\t"
"v_mfma_f32_16x16x32_bf16 v[132:135], a[240:243], a[168:171], v[132:135]\n\t"
"s_add_u32 m0, s24, 40192\n\t"
"s_add_u32 s28, s21, 1179648\n\t"
"v_cvt_pk_bf16_f32 v230, v232, v233\n\t"
"v_mfma_f32_16x16x32_bf16 v[132:135], a[244:247], a[172:175], v[132:135]\n\t"
"v_cvt_pk_bf16_f32 v231, v234, v235\n\t"
"v_mfma_f32_16x16x32_bf16 v[132:135], v[240:243], a[176:179], v[132:135]\n\t"
"buffer_load_dwordx4 v247, s[12:15], s28 offen offset:256 nt lds\n\t"
"v_mfma_f32_16x16x32_bf16 v[136:139], a[240:243], a[172:175], v[136:139]\n\t"
"ds_read_b64_tr_b16 v[232:233], v246 offset:53760\n\t"
"v_mfma_f32_16x16x32_bf16 v[136:139], a[244:247], a[176:179], v[136:139]\n\t"
"ds_read_b64_tr_b16 v[234:235], v246 offset:56832\n\t"
"v_cvt_pk_bf16_f32 v224, v224, v225\n\t"
"v_mfma_f32_16x16x32_bf16 v[136:139], v[240:243], a[180:183], v[136:139]\n\t"
"ds_write2st64_b64 v245, v[228:229], v[230:231] offset0:5 offset1:6\n\t"
"v_mfma_f32_16x16x32_bf16 v[140:143], a[240:243], a[176:179], v[140:143]\n\t"
"v_cvt_pk_bf16_f32 v225, v226, v227\n\t"
"v_mfma_f32_16x16x32_bf16 v[140:143], a[244:247], a[180:183], v[140:143]\n\t"
"v_mfma_f32_16x16x32_bf16 v[140:143], v[240:243], a[184:187], v[140:143]\n\t"
"v_mfma_f32_16x16x32_bf16 v[144:147], a[240:243], a[180:183], v[144:147]\n\t"
"v_mfma_f32_16x16x32_bf16 v[144:147], a[244:247], a[184:187], v[144:147]\n\t"
"v_mfma_f32_16x16x32_bf16 v[144:147], v[240:243], a[188:191], v[144:147]\n\t"
"v_mfma_f32_16x16x32_bf16 v[148:151], a[240:243], a[184:187], v[148:151]\n\t"
"s_waitcnt lgkmcnt(1)\n\t"
"v_mfma_f32_16x16x32_bf16 v[148:151], a[244:247], a[188:191], v[148:151]\n\t"
"v_mfma_f32_16x16x32_bf16 v[148:151], v[240:243], a[192:195], v[148:151]\n\t"
"v_mfma_f32_16x16x32_bf16 v[232:235], v[232:235], v[220:223], 0\n\t"
"v_mfma_f32_16x16x32_bf16 v[152:155], a[240:243], a[188:191], v[152:155]\n\t"
"v_mfma_f32_16x16x32_bf16 v[152:155], a[244:247], a[192:195], v[152:155]\n\t"
"v_mfma_f32_16x16x32_bf16 v[152:155], v[240:243], a[196:199], v[152:155]\n\t"
"v_mfma_f32_16x16x32_bf16 v[156:159], a[240:243], a[192:195], v[156:159]\n\t"
"v_mfma_f32_16x16x32_bf16 v[156:159], a[244:247], a[196:199], v[156:159]\n\t"
"v_cvt_pk_bf16_f32 v226, v232, v233\n\t"
"v_cvt_pk_bf16_f32 v227, v234, v235\n\t"
"v_mfma_f32_16x16x32_bf16 v[156:159], v[240:243], a[200:203], v[156:159]\n\t"
"ds_write2st64_b64 v245, v[224:225], v[226:227] offset0:7 offset1:8\n\t"
"v_mfma_f32_16x16x32_bf16 v[160:163], a[240:243], a[196:199], v[160:163]\n\t"
"s_waitcnt vmcnt(13)\n\t"
"v_mfma_f32_16x16x32_bf16 v[160:163], a[244:247], a[200:203], v[160:163]\n\t"
"v_mfma_f32_16x16x32_bf16 v[160:163], v[240:243], a[204:207], v[160:163]\n\t"
"v_mfma_f32_16x16x32_bf16 v[164:167], a[212:215], a[168:171], v[164:167]\n\t"
"v_mfma_f32_16x16x32_bf16 v[164:167], a[216:219], a[172:175], v[164:167]\n\t"
"v_mfma_f32_16x16x32_bf16 v[164:167], a[248:251], a[176:179], v[164:167]\n\t"
"s_waitcnt lgkmcnt(0)\n\t"
"s_barrier\n\t"
"v_mfma_f32_16x16x32_bf16 v[168:171], a[212:215], a[172:175], v[168:171]\n\t"
"ds_read_b128 a[168:171], v244 offset:0\n\t"
"v_mfma_f32_16x16x32_bf16 v[168:171], a[216:219], a[176:179], v[168:171]\n\t"
"buffer_load_dwordx4 v[240:243], v252, s[8:11], s20 offen offset:1024 sc1\n\t"
"v_mfma_f32_16x16x32_bf16 v[168:171], a[248:251], a[180:183], v[168:171]\n\t"
"ds_read_b128 a[172:175], v244 offset:512\n\t"
"v_mfma_f32_16x16x32_bf16 v[172:175], a[212:215], a[176:179], v[172:175]\n\t"
"ds_read_b128 a[176:179], v244 offset:1024\n\t"
"v_mfma_f32_16x16x32_bf16 v[172:175], a[216:219], a[180:183], v[172:175]\n\t"
"v_mfma_f32_16x16x32_bf16 v[172:175], a[248:251], a[184:187], v[172:175]\n\t"
"v_mfma_f32_16x16x32_bf16 v[176:179], a[212:215], a[180:183], v[176:179]\n\t"
"ds_read_b128 a[180:183], v244 offset:1536\n\t"
"v_mfma_f32_16x16x32_bf16 v[176:179], a[216:219], a[184:187], v[176:179]\n\t"
"v_mfma_f32_16x16x32_bf16 v[176:179], a[248:251], a[188:191], v[176:179]\n\t"
"v_mfma_f32_16x16x32_bf16 v[180:183], a[212:215], a[184:187], v[180:183]\n\t"
"ds_read_b128 a[184:187], v244 offset:2048\n\t"
"v_mfma_f32_16x16x32_bf16 v[180:183], a[216:219], a[188:191], v[180:183]\n\t"
"v_mfma_f32_16x16x32_bf16 v[180:183], a[248:251], a[192:195], v[180:183]\n\t"
"v_mfma_f32_16x16x32_bf16 v[184:187], a[212:215], a[188:191], v[184:187]\n\t"
"ds_read_b128 a[188:191], v244 offset:2560\n\t"
"v_mfma_f32_16x16x32_bf16 v[184:187], a[216:219], a[192:195], v[184:187]\n\t"
"v_mfma_f32_16x16x32_bf16 v[184:187], a[248:251], a[196:199], v[184:187]\n\t"
"v_mfma_f32_16x16x32_bf16 v[188:191], a[212:215], a[192:195], v[188:191]\n\t"
"ds_read_b128 a[192:195], v244 offset:3072\n\t"
"v_mfma_f32_16x16x32_bf16 v[188:191], a[216:219], a[196:199], v[188:191]\n\t"
"v_mfma_f32_16x16x32_bf16 v[188:191], a[248:251], a[200:203], v[188:191]\n\t"
"v_mfma_f32_16x16x32_bf16 v[192:195], a[212:215], a[196:199], v[192:195]\n\t"
"s_waitcnt vmcnt(11) lgkmcnt(4)\n\t"
"ds_read_b128 a[196:199], v244 offset:3584\n\t"
"v_mfma_f32_16x16x32_bf16 v[192:195], a[216:219], a[200:203], v[192:195]\n\t"
"ds_read_b128 a[200:203], v244 offset:4096\n\t"
"v_mfma_f32_16x16x32_bf16 v[192:195], a[248:251], a[204:207], v[192:195]\n\t"
"ds_read_b128 a[204:207], v244 offset:4608\n\t"
"Lst8_%=:\n\t"
"v_mfma_f32_16x16x32_bf16 v[0:3], a[252:255], a[168:171], v[0:3]\n\t"
"s_waitcnt lgkmcnt(6)\n\t"
"v_xor_b32 v246, 0xc0, v246\n\t"
"v_mfma_f32_16x16x32_bf16 v[0:3], v[236:239], a[172:175], v[0:3]\n\t"
"v_mfma_f32_16x16x32_bf16 v[0:3], v[96:99], a[176:179], v[0:3]\n\t"
"v_mfma_f32_16x16x32_bf16 v[4:7], a[252:255], a[172:175], v[4:7]\n\t"
"s_waitcnt lgkmcnt(5)\n\t"
"v_mfma_f32_16x16x32_bf16 v[4:7], v[236:239], a[176:179], v[4:7]\n\t"
"v_mfma_f32_16x16x32_bf16 v[4:7], v[96:99], a[180:183], v[4:7]\n\t"
"v_mfma_f32_16x16x32_bf16 v[8:11], a[252:255], a[176:179], v[8:11]\n\t"
"s_waitcnt lgkmcnt(4)\n\t"
"v_mfma_f32_16x16x32_bf16 v[8:11], v[236:239], a[180:183], v[8:11]\n\t"
"v_mfma_f32_16x16x32_bf16 v[8:11], v[96:99], a[184:187], v[8:11]\n\t"
"v_mfma_f32_16x16x32_bf16 v[12:15], a[252:255], a[180:183], v[12:15]\n\t"
"s_waitcnt lgkmcnt(3)\n\t"
"v_mfma_f32_16x16x32_bf16 v[12:15], v[236:239], a[184:187], v[12:15]\n\t"
"v_mfma_f32_16x16x32_bf16 v[12:15], v[96:99], a[188:191], v[12:15]\n\t"
"buffer_load_dwordx4 v[248:251], v252, s[8:11], s20 offen offset:2048 sc1\n\t"
"v_mfma_f32_16x16x32_bf16 v[16:19], a[252:255], a[184:187], v[16:19]\n\t"
"s_waitcnt lgkmcnt(2)\n\t"
"s_mov_b32 s20, 958464\n\t"
"v_mfma_f32_16x16x32_bf16 v[16:19], v[236:239], a[188:191], v[16:19]\n\t"
"v_mfma_f32_16x16x32_bf16 v[16:19], v[96:99], a[192:195], v[16:19]\n\t"
"ds_read_b64_tr_b16 v[228:229], v246 offset:1536\n\t"
"v_mfma_f32_16x16x32_bf16 v[20:23], a[252:255], a[188:191], v[20:23]\n\t"
"buffer_load_dwordx4 a[212:215], v252, s[8:11], s20 offen offset:0 sc1\n\t"
"v_mfma_f32_16x16x32_bf16 v[20:23], v[236:239], a[192:195], v[20:23]\n\t"
"s_waitcnt lgkmcnt(2)\n\t"
"ds_read_b64_tr_b16 v[230:231], v246 offset:4608\n\t"
"v_mfma_f32_16x16x32_bf16 v[20:23], v[96:99], a[196:199], v[20:23]\n\t"
"v_mfma_f32_16x16x32_bf16 v[24:27], a[252:255], a[192:195], v[24:27]\n\t"
"buffer_load_dwordx4 a[216:219], v252, s[8:11], s20 offen offset:1024 sc1\n\t"
"v_mfma_f32_16x16x32_bf16 v[24:27], v[236:239], a[196:199], v[24:27]\n\t"
"s_waitcnt lgkmcnt(2)\n\t"
"v_mfma_f32_16x16x32_bf16 v[24:27], v[96:99], a[200:203], v[24:27]\n\t"
"v_mfma_f32_16x16x32_bf16 v[28:31], a[252:255], a[196:199], v[28:31]\n\t"
"s_waitcnt vmcnt(11)\n\t"
"ds_read_b64_tr_b16 v[232:233], v246 offset:7680\n\t"
"v_mfma_f32_16x16x32_bf16 v[28:31], v[236:239], a[200:203], v[28:31]\n\t"
"buffer_load_dwordx4 v[236:239], v252, s[8:11], s20 offen offset:2048 sc1\n\t"
"v_mfma_f32_16x16x32_bf16 v[28:31], v[96:99], a[204:207], v[28:31]\n\t"
"s_mov_b32 s20, 970752\n\t"
"ds_read_b64_tr_b16 v[234:235], v246 offset:10752\n\t"
"v_mfma_f32_16x16x32_bf16 v[32:35], a[220:223], a[168:171], v[32:35]\n\t"
"v_mfma_f32_16x16x32_bf16 v[32:35], a[224:227], a[172:175], v[32:35]\n\t"
"v_mfma_f32_16x16x32_bf16 v[32:35], a[208:211], a[176:179], v[32:35]\n\t"
"buffer_load_dwordx4 v[96:99], v252, s[8:11], s20 offen offset:0 sc1\n\t"
"v_mfma_f32_16x16x32_bf16 v[36:39], a[220:223], a[172:175], v[36:39]\n\t"
"s_waitcnt lgkmcnt(2)\n\t"
"v_mfma_f32_16x16x32_bf16 v[36:39], a[224:227], a[176:179], v[36:39]\n\t"
"v_mfma_f32_16x16x32_bf16 v[36:39], a[208:211], a[180:183], v[36:39]\n\t"
"v_mfma_f32_16x16x32_bf16 v[228:231], v[228:231], v[220:223], 0\n\t"
"buffer_load_dwordx4 a[232:235], v252, s[8:11], s20 offen offset:1024 sc1\n\t"
"v_mfma_f32_16x16x32_bf16 v[40:43], a[220:223], a[176:179], v[40:43]\n\t"
"ds_read_b64_tr_b16 v[224:225], v246 offset:13824\n\t"
"v_mfma_f32_16x16x32_bf16 v[40:43], a[224:227], a[180:183], v[40:43]\n\t"
"ds_read_b64_tr_b16 v[226:227], v246 offset:16896\n\t"
"v_mfma_f32_16x16x32_bf16 v[40:43], a[208:211], a[184:187], v[40:43]\n\t"
"v_mfma_f32_16x16x32_bf16 v[44:47], a[220:223], a[180:183], v[44:47]\n\t"
"buffer_load_dwordx4 a[236:239], v252, s[8:11], s20 offen offset:2048 sc1\n\t"
"v_mfma_f32_16x16x32_bf16 v[44:47], a[224:227], a[184:187], v[44:47]\n\t"
"s_waitcnt lgkmcnt(2)\n\t"
"v_cvt_pk_bf16_f32 v228, v228, v229\n\t"
"v_cvt_pk_bf16_f32 v229, v230, v231\n\t"
"v_mfma_f32_16x16x32_bf16 v[44:47], a[208:211], a[188:191], v[44:47]\n\t"
"s_mov_b32 s20, 983040\n\t"
"v_mfma_f32_16x16x32_bf16 v[232:235], v[232:235], v[220:223], 0\n\t"
"v_mfma_f32_16x16x32_bf16 v[48:51], a[220:223], a[184:187], v[48:51]\n\t"
"buffer_load_dwordx4 a[240:243], v252, s[8:11], s20 offen offset:0 sc1\n\t"
"v_mfma_f32_16x16x32_bf16 v[48:51], a[224:227], a[188:191], v[48:51]\n\t"
"v_mfma_f32_16x16x32_bf16 v[48:51], a[208:211], a[192:195], v[48:51]\n\t"
"v_mfma_f32_16x16x32_bf16 v[52:55], a[220:223], a[188:191], v[52:55]\n\t"
"v_mfma_f32_16x16x32_bf16 v[52:55], a[224:227], a[192:195], v[52:55]\n\t"
"buffer_load_dwordx4 a[244:247], v252, s[8:11], s20 offen offset:1024 sc1\n\t"
"v_mfma_f32_16x16x32_bf16 v[52:55], a[208:211], a[196:199], v[52:55]\n\t"
"v_cvt_pk_bf16_f32 v230, v232, v233\n\t"
"v_cvt_pk_bf16_f32 v231, v234, v235\n\t"
"v_mfma_f32_16x16x32_bf16 v[56:59], a[220:223], a[192:195], v[56:59]\n\t"
"s_waitcnt lgkmcnt(0)\n\t"
"ds_read_b64_tr_b16 v[232:233], v246 offset:21504\n\t"
"v_mfma_f32_16x16x32_bf16 v[56:59], a[224:227], a[196:199], v[56:59]\n\t"
"ds_read_b64_tr_b16 v[234:235], v246 offset:24576\n\t"
"v_mfma_f32_16x16x32_bf16 v[56:59], a[208:211], a[200:203], v[56:59]\n\t"
"buffer_load_dwordx4 a[248:251], v252, s[8:11], s20 offen offset:2048 sc1\n\t"
"v_mfma_f32_16x16x32_bf16 v[224:227], v[224:227], v[220:223], 0\n\t"
"s_mov_b32 s20, 995328\n\t"
"v_mfma_f32_16x16x32_bf16 v[60:63], a[220:223], a[196:199], v[60:63]\n\t"
"s_waitcnt vmcnt(9)\n\t"
"v_mfma_f32_16x16x32_bf16 v[60:63], a[224:227], a[200:203], v[60:63]\n\t"
"v_mfma_f32_16x16x32_bf16 v[60:63], a[208:211], a[204:207], v[60:63]\n\t"
"buffer_load_dwordx4 a[220:223], v252, s[8:11], s20 offen offset:0 sc1\n\t"
"v_mfma_f32_16x16x32_bf16 v[64:67], a[228:231], a[168:171], v[64:67]\n\t"
"ds_read_b128 a[168:171], v244 offset:9216\n\t"
"v_mfma_f32_16x16x32_bf16 v[64:67], v[240:243], a[172:175], v[64:67]\n\t"
"v_cvt_pk_bf16_f32 v224, v224, v225\n\t"
"v_cvt_pk_bf16_f32 v225, v226, v227\n\t"
"v_mfma_f32_16x16x32_bf16 v[64:67], v[248:251], a[176:179], v[64:67]\n\t"
"v_mfma_f32_16x16x32_bf16 v[68:71], a[228:231], a[172:175], v[68:71]\n\t"
"buffer_load_dwordx4 a[224:227], v252, s[8:11], s20 offen offset:1024 sc1\n\t"
"v_mfma_f32_16x16x32_bf16 v[68:71], v[240:243], a[176:179], v[68:71]\n\t"
"s_waitcnt lgkmcnt(1)\n\t"
"ds_read_b128 a[172:175], v244 offset:9728\n\t"
"v_mfma_f32_16x16x32_bf16 v[68:71], v[248:251], a[180:183], v[68:71]\n\t"
"buffer_load_dwordx4 a[252:255], v252, s[8:11], s20 offen offset:2048 sc1\n\t"
"v_mfma_f32_16x16x32_bf16 v[232:235], v[232:235], v[220:223], 0\n\t"
"v_mfma_f32_16x16x32_bf16 v[72:75], a[228:231], a[176:179], v[72:75]\n\t"
"ds_read_b128 a[176:179], v244 offset:10240\n\t"
"v_mfma_f32_16x16x32_bf16 v[72:75], v[240:243], a[180:183], v[72:75]\n\t"
"v_mfma_f32_16x16x32_bf16 v[72:75], v[248:251], a[184:187], v[72:75]\n\t"
"v_mfma_f32_16x16x32_bf16 v[76:79], a[228:231], a[180:183], v[76:79]\n\t"
"ds_read_b128 a[180:183], v244 offset:10752\n\t"
"v_mfma_f32_16x16x32_bf16 v[76:79], v[240:243], a[184:187], v[76:79]\n\t"
"v_cvt_pk_bf16_f32 v226, v232, v233\n\t"
"v_cvt_pk_bf16_f32 v227, v234, v235\n\t"
"v_mfma_f32_16x16x32_bf16 v[76:79], v[248:251], a[188:191], v[76:79]\n\t"
"ds_read_b64_tr_b16 v[232:233], v246 offset:27648\n\t"
"v_mfma_f32_16x16x32_bf16 v[80:83], a[228:231], a[184:187], v[80:83]\n\t"
"ds_read_b64_tr_b16 v[234:235], v246 offset:30720\n\t"
"v_mfma_f32_16x16x32_bf16 v[80:83], v[240:243], a[188:191], v[80:83]\n\t"
"ds_read_b128 a[184:187], v244 offset:11264\n\t"
"v_mfma_f32_16x16x32_bf16 v[80:83], v[248:251], a[192:195], v[80:83]\n\t"
"v_mfma_f32_16x16x32_bf16 v[84:87], a[228:231], a[188:191], v[84:87]\n\t"
"s_mov_b32 s20, 1007616\n\t"
"ds_read_b128 a[188:191], v244 offset:11776\n\t"
"v_mfma_f32_16x16x32_bf16 v[84:87], v[240:243], a[192:195], v[84:87]\n\t"
"v_mfma_f32_16x16x32_bf16 v[84:87], v[248:251], a[196:199], v[84:87]\n\t"
"v_mfma_f32_16x16x32_bf16 v[88:91], a[228:231], a[192:195], v[88:91]\n\t"
"ds_read_b128 a[192:195], v244 offset:12288\n\t"
"v_mfma_f32_16x16x32_bf16 v[88:91], v[240:243], a[196:199], v[88:91]\n\t"
"v_mfma_f32_16x16x32_bf16 v[88:91], v[248:251], a[200:203], v[88:91]\n\t"
"v_mfma_f32_16x16x32_bf16 v[92:95], a[228:231], a[196:199], v[92:95]\n\t"
"buffer_load_dwordx4 a[228:231], v252, s[8:11], s20 offen offset:0 sc1\n\t"
"v_mfma_f32_16x16x32_bf16 v[92:95], v[240:243], a[200:203], v[92:95]\n\t"
"s_waitcnt vmcnt(10) lgkmcnt(3)\n\t"
"ds_read_b128 a[196:199], v244 offset:12800\n\t"
"v_mfma_f32_16x16x32_bf16 v[92:95], v[248:251], a[204:207], v[92:95]\n\t"
"ds_read_b128 a[200:203], v244 offset:13312\n\t"
"v_mfma_f32_16x16x32_bf16 v[232:235], v[232:235], v[220:223], 0\n\t"
"ds_read_b128 a[204:207], v244 offset:13824\n\t"
"v_mfma_f32_16x16x32_bf16 v[100:103], a[212:215], a[168:171], v[100:103]\n\t"
"buffer_load_dwordx4 v[248:251], v252, s[8:11], s20 offen offset:1024 sc1\n\t"
"v_mfma_f32_16x16x32_bf16 v[100:103], a[216:219], a[172:175], v[100:103]\n\t"
"v_mfma_f32_16x16x32_bf16 v[100:103], v[236:239], a[176:179], v[100:103]\n\t"
"v_mfma_f32_16x16x32_bf16 v[104:107], a[212:215], a[172:175], v[104:107]\n\t"
"s_waitcnt lgkmcnt(5)\n\t"
"v_mfma_f32_16x16x32_bf16 v[104:107], a[216:219], a[176:179], v[104:107]\n\t"
"v_cvt_pk_bf16_f32 v232, v232, v233\n\t"
"v_mfma_f32_16x16x32_bf16 v[104:107], v[236:239], a[180:183], v[104:107]\n\t"
"s_waitcnt lgkmcnt(0)\n\t"
"s_barrier\n\t"
"v_mfma_f32_16x16x32_bf16 v[108:111], a[212:215], a[176:179], v[108:111]\n\t"
"v_cvt_pk_bf16_f32 v233, v234, v235\n\t"
"v_mfma_f32_16x16x32_bf16 v[108:111], a[216:219], a[180:183], v[108:111]\n\t"
"v_mfma_f32_16x16x32_bf16 v[108:111], v[236:239], a[184:187], v[108:111]\n\t"
"ds_write2st64_b64 v245, v[228:229], v[230:231] offset0:0 offset1:1\n\t"
"v_mfma_f32_16x16x32_bf16 v[112:115], a[212:215], a[180:183], v[112:115]\n\t"
"v_mfma_f32_16x16x32_bf16 v[112:115], a[216:219], a[184:187], v[112:115]\n\t"
"ds_read_b64_tr_b16 v[228:229], v246 offset:33792\n\t"
"v_mfma_f32_16x16x32_bf16 v[112:115], v[236:239], a[188:191], v[112:115]\n\t"
"ds_write2st64_b64 v245, v[224:225], v[226:227] offset0:2 offset1:3\n\t"
"v_mfma_f32_16x16x32_bf16 v[116:119], a[212:215], a[184:187], v[116:119]\n\t"
"global_load_dwordx4 v[240:243], v252, s[48:49] offset:2048\n\t"
"v_mfma_f32_16x16x32_bf16 v[116:119], a[216:219], a[188:191], v[116:119]\n\t"
"ds_read_b64_tr_b16 v[230:231], v246 offset:36864\n\t"
"v_mfma_f32_16x16x32_bf16 v[116:119], v[236:239], a[192:195], v[116:119]\n\t"
"ds_write_b64 v245, v[232:233] offset:2048\n\t"
"v_mfma_f32_16x16x32_bf16 v[120:123], a[212:215], a[188:191], v[120:123]\n\t"
"ds_read_b64_tr_b16 v[224:225], v246 offset:41472\n\t"
"v_mfma_f32_16x16x32_bf16 v[120:123], a[216:219], a[192:195], v[120:123]\n\t"
"ds_read_b64_tr_b16 v[226:227], v246 offset:44544\n\t"
"v_mfma_f32_16x16x32_bf16 v[120:123], v[236:239], a[196:199], v[120:123]\n\t"
"s_waitcnt lgkmcnt(3)\n\t"
"ds_read_b64_tr_b16 v[232:233], v246 offset:47616\n\t"
"v_mfma_f32_16x16x32_bf16 v[228:231], v[228:231], v[220:223], 0\n\t"
"ds_read_b64_tr_b16 v[234:235], v246 offset:50688\n\t"
"v_mfma_f32_16x16x32_bf16 v[124:127], a[212:215], a[192:195], v[124:127]\n\t"
"global_load_dwordx4 a[208:211], v252, s[46:47] offset:1024\n\t"
"v_mfma_f32_16x16x32_bf16 v[124:127], a[216:219], a[196:199], v[124:127]\n\t"
"v_mfma_f32_16x16x32_bf16 v[124:127], v[236:239], a[200:203], v[124:127]\n\t"
"s_waitcnt lgkmcnt(2)\n\t"
"v_mfma_f32_16x16x32_bf16 v[224:227], v[224:227], v[220:223], 0\n\t"
"v_mfma_f32_16x16x32_bf16 v[128:131], a[212:215], a[196:199], v[128:131]\n\t"
"buffer_load_dwordx4 a[212:215], v252, s[8:11], s20 offen offset:2048 sc1\n\t"
"v_mfma_f32_16x16x32_bf16 v[128:131], a[216:219], a[200:203], v[128:131]\n\t"
"s_waitcnt vmcnt(11) lgkmcnt(0)\n\t"
"s_mov_b32 s20, 1019904\n\t"
"v_cvt_pk_bf16_f32 v228, v228, v229\n\t"
"v_mfma_f32_16x16x32_bf16 v[128:131], v[236:239], a[204:207], v[128:131]\n\t"
"v_cvt_pk_bf16_f32 v229, v230, v231\n\t"
"v_mfma_f32_16x16x32_bf16 v[232:235], v[232:235], v[220:223], 0\n\t"
"buffer_load_dwordx4 a[216:219], v252, s[8:11], s20 offen offset:0 sc1\n\t"
"v_mfma_f32_16x16x32_bf16 v[132:135], v[96:99], a[168:171], v[132:135]\n\t"
"s_add_u32 m0, s24, 46336\n\t"
"s_add_u32 s28, s21, 1376256\n\t"
"v_cvt_pk_bf16_f32 v230, v224, v225\n\t"
"v_mfma_f32_16x16x32_bf16 v[132:135], a[232:235], a[172:175], v[132:135]\n\t"
"v_cvt_pk_bf16_f32 v231, v226, v227\n\t"
"v_mfma_f32_16x16x32_bf16 v[132:135], a[236:239], a[176:179], v[132:135]\n\t"
"ds_write2st64_b64 v245, v[228:229], v[230:231] offset0:5 offset1:6\n\t"
"v_mfma_f32_16x16x32_bf16 v[136:139], v[96:99], a[172:175], v[136:139]\n\t"
"buffer_load_dwordx4 v247, s[12:15], s28 offen offset:256 nt lds\n\t"
"v_mfma_f32_16x16x32_bf16 v[136:139], a[232:235], a[176:179], v[136:139]\n\t"
"s_add_u32 m0, s24, 52480\n\t"
"s_add_u32 s28, s21, 1572864\n\t"
"ds_read_b64_tr_b16 v[224:225], v246 offset:53760\n\t"
"v_mfma_f32_16x16x32_bf16 v[136:139], a[236:239], a[180:183], v[136:139]\n\t"
"buffer_load_dwordx4 v247, s[12:15], s28 offen offset:256 nt lds\n\t"
"v_mfma_f32_16x16x32_bf16 v[140:143], v[96:99], a[176:179], v[140:143]\n\t"
"s_add_u32 m0, s24, 92416\n\t"
"s_add_u32 s28, s21, 2752512\n\t"
"ds_read_b64_tr_b16 v[226:227], v246 offset:56832\n\t"
"v_mfma_f32_16x16x32_bf16 v[140:143], a[232:235], a[180:183], v[140:143]\n\t"
"buffer_load_dwordx4 v247, s[12:15], s28 offen offset:256 nt lds\n\t"
"v_mfma_f32_16x16x32_bf16 v[140:143], a[236:239], a[184:187], v[140:143]\n\t"
"s_add_u32 m0, s24, 100096\n\t"
"s_add_u32 s28, s21, 2949120\n\t"
"v_cvt_pk_bf16_f32 v232, v232, v233\n\t"
"v_cvt_pk_bf16_f32 v233, v234, v235\n\t"
"v_mfma_f32_16x16x32_bf16 v[144:147], v[96:99], a[180:183], v[144:147]\n\t"
"v_mfma_f32_16x16x32_bf16 v[144:147], a[232:235], a[184:187], v[144:147]\n\t"
"buffer_load_dwordx4 v247, s[12:15], s28 offen offset:256 nt lds\n\t"
"v_mfma_f32_16x16x32_bf16 v[144:147], a[236:239], a[188:191], v[144:147]\n\t"
"s_add_u32 m0, s24, 106240\n\t"
"s_add_u32 s28, s21, 3145728\n\t"
"v_mfma_f32_16x16x32_bf16 v[148:151], v[96:99], a[184:187], v[148:151]\n\t"
"buffer_load_dwordx4 v247, s[12:15], s28 offen offset:256 nt lds\n\t"
"v_mfma_f32_16x16x32_bf16 v[148:151], a[232:235], a[188:191], v[148:151]\n\t"
"s_waitcnt lgkmcnt(0)\n\t"
"s_add_u32 m0, s24, 112384\n\t"
"s_add_u32 s28, s21, 3342336\n\t"
"v_mfma_f32_16x16x32_bf16 v[148:151], a[236:239], a[192:195], v[148:151]\n\t"
"buffer_load_dwordx4 v247, s[12:15], s28 offen offset:256 nt lds\n\t"
"s_add_u32 m0, s25, 256\n\t"
"v_mfma_f32_16x16x32_bf16 v[224:227], v[224:227], v[220:223], 0\n\t"
"buffer_load_dwordx4 v254, s[12:15], 0 offen offset:256 nt lds\n\t"
"v_mfma_f32_16x16x32_bf16 v[152:155], v[96:99], a[188:191], v[152:155]\n\t"
"s_add_u32 m0, s26, 256\n\t"
"v_mfma_f32_16x16x32_bf16 v[152:155], a[232:235], a[192:195], v[152:155]\n\t"
"v_mfma_f32_16x16x32_bf16 v[152:155], a[236:239], a[196:199], v[152:155]\n\t"
"s_mov_b64 exec, s[36:37]\n\t"
"buffer_load_dwordx4 v254, s[12:15], s27 offen offset:256 nt lds\n\t"
"s_mov_b64 exec, -1\n\t"
"v_mfma_f32_16x16x32_bf16 v[156:159], v[96:99], a[192:195], v[156:159]\n\t"
"s_waitcnt vmcnt(11)\n\t"
"ds_read_b64_tr_b16 v[236:237], v240 offset:1536\n\t"
"v_mfma_f32_16x16x32_bf16 v[156:159], a[232:235], a[196:199], v[156:159]\n\t"
"ds_read_b64_tr_b16 v[238:239], v240 offset:3072\n\t"
"v_cvt_pk_bf16_f32 v234, v224, v225\n\t"
"v_mfma_f32_16x16x32_bf16 v[156:159], a[236:239], a[200:203], v[156:159]\n\t"
"ds_read_b64_tr_b16 v[228:229], v241 offset:1536\n\t"
"v_cvt_pk_bf16_f32 v235, v226, v227\n\t"
"v_mfma_f32_16x16x32_bf16 v[160:163], v[96:99], a[196:199], v[160:163]\n\t"
"ds_read_b64_tr_b16 v[230:231], v241 offset:3072\n\t"
"v_mfma_f32_16x16x32_bf16 v[160:163], a[232:235], a[200:203], v[160:163]\n\t"
"v_mfma_f32_16x16x32_bf16 v[160:163], a[236:239], a[204:207], v[160:163]\n\t"
"ds_write2st64_b64 v245, v[232:233], v[234:235] offset0:7 offset1:8\n\t"
"v_mfma_f32_16x16x32_bf16 v[164:167], a[240:243], a[168:171], v[164:167]\n\t"
"v_mfma_f32_16x16x32_bf16 v[164:167], a[244:247], a[172:175], v[164:167]\n\t"
"v_mfma_f32_16x16x32_bf16 v[164:167], a[248:251], a[176:179], v[164:167]\n\t"
"s_waitcnt lgkmcnt(0)\n\t"
"s_barrier\n\t"
"v_mfma_f32_16x16x32_bf16 v[168:171], a[240:243], a[172:175], v[168:171]\n\t"
"ds_read_b64_tr_b16 v[224:225], v240 offset:21504\n\t"
"v_mfma_f32_16x16x32_bf16 v[168:171], a[244:247], a[176:179], v[168:171]\n\t"
"ds_read_b64_tr_b16 v[226:227], v240 offset:23040\n\t"
"v_mfma_f32_16x16x32_bf16 v[168:171], a[248:251], a[180:183], v[168:171]\n\t"
"ds_read_b64_tr_b16 v[232:233], v241 offset:21504\n\t"
"v_mfma_f32_16x16x32_bf16 v[172:175], a[240:243], a[176:179], v[172:175]\n\t"
"s_waitcnt vmcnt(10)\n\t"
"ds_read_b64_tr_b16 v[234:235], v241 offset:23040\n\t"
"v_mfma_f32_16x16x32_bf16 v[172:175], a[244:247], a[180:183], v[172:175]\n\t"
"ds_read_b128 a[168:171], v244 offset:0\n\t"
"v_mfma_f32_16x16x32_bf16 v[172:175], a[248:251], a[184:187], v[172:175]\n\t"
"ds_read_b128 a[172:175], v244 offset:512\n\t"
"v_mfma_f32_16x16x32_bf16 v[236:239], v[236:239], a[208:211], 0\n\t"
"ds_read_b128 a[176:179], v244 offset:1024\n\t"
"v_mfma_f32_16x16x32_bf16 v[228:231], v[228:231], a[208:211], 0\n\t"
"v_mfma_f32_16x16x32_bf16 v[176:179], a[240:243], a[180:183], v[176:179]\n\t"
"ds_read_b128 a[180:183], v244 offset:1536\n\t"
"v_mfma_f32_16x16x32_bf16 v[176:179], a[244:247], a[184:187], v[176:179]\n\t"
"v_mfma_f32_16x16x32_bf16 v[176:179], a[248:251], a[188:191], v[176:179]\n\t"
"v_mfma_f32_16x16x32_bf16 v[180:183], a[240:243], a[184:187], v[180:183]\n\t"
"s_waitcnt lgkmcnt(6)\n\t"
"ds_read_b128 a[184:187], v244 offset:2048\n\t"
"v_cvt_pk_bf16_f32 v236, v236, v237\n\t"
"v_mfma_f32_16x16x32_bf16 v[180:183], a[244:247], a[188:191], v[180:183]\n\t"
"s_waitcnt lgkmcnt(5)\n\t"
"v_cvt_pk_bf16_f32 v237, v238, v239\n\t"
"v_cvt_pk_bf16_f32 v238, v228, v229\n\t"
"v_mfma_f32_16x16x32_bf16 v[180:183], a[248:251], a[192:195], v[180:183]\n\t"
"v_cvt_pk_bf16_f32 v239, v230, v231\n\t"
"v_mfma_f32_16x16x32_bf16 v[224:227], v[224:227], a[208:211], 0\n\t"
"v_mfma_f32_16x16x32_bf16 v[232:235], v[232:235], a[208:211], 0\n\t"
"ds_read_b64_tr_b16 v[228:229], v240 offset:41472\n\t"
"v_mfma_f32_16x16x32_bf16 v[184:187], a[240:243], a[188:191], v[184:187]\n\t"
"ds_read_b128 a[188:191], v244 offset:2560\n\t"
"v_mfma_f32_16x16x32_bf16 v[184:187], a[244:247], a[192:195], v[184:187]\n\t"
"ds_read_b64_tr_b16 v[230:231], v240 offset:43008\n\t"
"v_mfma_f32_16x16x32_bf16 v[184:187], a[248:251], a[196:199], v[184:187]\n\t"
"v_mfma_f32_16x16x32_bf16 v[188:191], a[240:243], a[192:195], v[188:191]\n\t"
"ds_read_b128 a[192:195], v244 offset:3072\n\t"
"v_cvt_pk_bf16_f32 v224, v224, v225\n\t"
"v_mfma_f32_16x16x32_bf16 v[188:191], a[244:247], a[196:199], v[188:191]\n\t"
"v_cvt_pk_bf16_f32 v225, v226, v227\n\t"
"v_cvt_pk_bf16_f32 v226, v232, v233\n\t"
"v_mfma_f32_16x16x32_bf16 v[188:191], a[248:251], a[200:203], v[188:191]\n\t"
"v_cvt_pk_bf16_f32 v227, v234, v235\n\t"
"v_mfma_f32_16x16x32_bf16 v[192:195], a[240:243], a[196:199], v[192:195]\n\t"
"s_waitcnt lgkmcnt(6)\n\t"
"ds_read_b64_tr_b16 v[232:233], v241 offset:41472\n\t"
"v_mfma_f32_16x16x32_bf16 v[192:195], a[244:247], a[200:203], v[192:195]\n\t"
"ds_read_b64_tr_b16 v[234:235], v241 offset:43008\n\t"
"v_mfma_f32_16x16x32_bf16 v[192:195], a[248:251], a[204:207], v[192:195]\n\t"
"ds_read_b128 a[196:199], v244 offset:3584\n\t"
"Lst9_%=:\n\t"
"v_mfma_f32_16x16x32_bf16 v[196:199], a[220:223], a[168:171], v[196:199]\n\t"
"s_waitcnt lgkmcnt(8)\n\t"
"ds_read_b128 a[200:203], v244 offset:4096\n\t"
"v_mfma_f32_16x16x32_bf16 v[196:199], a[224:227], a[172:175], v[196:199]\n\t"
"ds_read_b128 a[204:207], v244 offset:4608\n\t"
"v_mfma_f32_16x16x32_bf16 v[196:199], a[252:255], a[176:179], v[196:199]\n\t"
"buffer_load_dwordx4 v[96:99], v252, s[8:11], s20 offen offset:1024 sc1\n\t"
"v_mfma_f32_16x16x32_bf16 v[204:207], a[220:223], a[172:175], v[204:207]\n\t"
"s_waitcnt lgkmcnt(6)\n\t"
"v_mfma_f32_16x16x32_bf16 v[204:207], a[224:227], a[176:179], v[204:207]\n\t"
"s_waitcnt lgkmcnt(3)\n\t"
"v_mfma_f32_16x16x32_bf16 v[204:207], a[252:255], a[180:183], v[204:207]\n\t"
"v_mfma_f32_16x16x32_bf16 v[228:231], v[228:231], a[208:211], 0\n\t"
"v_add_u32 v240, 59904, v240\n\t"
"v_add_u32 v241, 59904, v241\n\t"
"v_mfma_f32_16x16x32_bf16 v[232:235], v[232:235], a[208:211], 0\n\t"
"v_mfma_f32_16x16x32_bf16 v[212:215], a[220:223], a[176:179], v[212:215]\n\t"
"v_mfma_f32_16x16x32_bf16 v[212:215], a[224:227], a[180:183], v[212:215]\n\t"
"v_mfma_f32_16x16x32_bf16 v[212:215], a[252:255], a[184:187], v[212:215]\n\t"
"s_barrier\n\t"
"v_mfma_f32_16x16x32_bf16 a[0:3], a[220:223], a[180:183], a[0:3]\n\t"
"v_cvt_pk_bf16_f32 v228, v228, v229\n\t"
"v_mfma_f32_16x16x32_bf16 a[0:3], a[224:227], a[184:187], a[0:3]\n\t"
"v_cvt_pk_bf16_f32 v229, v230, v231\n\t"
"v_cvt_pk_bf16_f32 v230, v232, v233\n\t"
"v_mfma_f32_16x16x32_bf16 a[0:3], a[252:255], a[188:191], a[0:3]\n\t"
"ds_write_b128 v243, v[236:239] offset:0\n\t"
"v_mfma_f32_16x16x32_bf16 a[8:11], a[220:223], a[184:187], a[8:11]\n\t"
"s_waitcnt lgkmcnt(3)\n\t"
"v_cvt_pk_bf16_f32 v231, v234, v235\n\t"
"v_mfma_f32_16x16x32_bf16 a[8:11], a[224:227], a[188:191], a[8:11]\n\t"
"ds_read_b64_tr_b16 v[236:237], v240 offset:1536\n\t"
"v_mfma_f32_16x16x32_bf16 a[8:11], a[252:255], a[192:195], a[8:11]\n\t"
"ds_write_b128 v243, v[224:227] offset:9216\n\t"
"v_mfma_f32_16x16x32_bf16 a[16:19], a[220:223], a[188:191], a[16:19]\n\t"
"buffer_load_dwordx4 a[232:235], v252, s[8:11], s20 offen offset:2048 sc1\n\t"
"v_mfma_f32_16x16x32_bf16 a[16:19], a[224:227], a[192:195], a[16:19]\n\t"
"s_waitcnt lgkmcnt(4)\n\t"
"ds_read_b64_tr_b16 v[238:239], v240 offset:3072\n\t"
"v_mfma_f32_16x16x32_bf16 a[16:19], a[252:255], a[196:199], a[16:19]\n\t"
"s_mov_b32 s20, 1032192\n\t"
"ds_write_b128 v243, v[228:231] offset:18432\n\t"
"v_mfma_f32_16x16x32_bf16 a[24:27], a[220:223], a[192:195], a[24:27]\n\t"
"buffer_load_dwordx4 a[236:239], v252, s[8:11], s20 offen offset:0 sc1\n\t"
"v_mfma_f32_16x16x32_bf16 a[24:27], a[224:227], a[196:199], a[24:27]\n\t"
"s_waitcnt lgkmcnt(5)\n\t"
"ds_read_b64_tr_b16 v[224:225], v241 offset:1536\n\t"
"v_mfma_f32_16x16x32_bf16 a[24:27], a[252:255], a[200:203], a[24:27]\n\t"
"ds_read_b64_tr_b16 v[226:227], v241 offset:3072\n\t"
"v_mfma_f32_16x16x32_bf16 a[32:35], a[220:223], a[196:199], a[32:35]\n\t"
"s_waitcnt lgkmcnt(3)\n\t"
"ds_read_b64_tr_b16 v[232:233], v240 offset:21504\n\t"
"v_mfma_f32_16x16x32_bf16 a[32:35], a[224:227], a[200:203], a[32:35]\n\t"
"ds_read_b64_tr_b16 v[234:235], v240 offset:23040\n\t"
"v_mfma_f32_16x16x32_bf16 a[32:35], a[252:255], a[204:207], a[32:35]\n\t"
"s_waitcnt vmcnt(12)\n\t"
"ds_read_b64_tr_b16 v[228:229], v241 offset:21504\n\t"
"v_mfma_f32_16x16x32_bf16 v[236:239], v[236:239], a[208:211], 0\n\t"
"s_waitcnt lgkmcnt(3)\n\t"
"ds_read_b64_tr_b16 v[230:231], v241 offset:23040\n\t"
"v_mfma_f32_16x16x32_bf16 v[224:227], v[224:227], a[208:211], 0\n\t"
"buffer_load_dwordx4 a[220:223], v252, s[8:11], s20 offen offset:1024 sc1\n\t"
"v_mfma_f32_16x16x32_bf16 a[40:43], a[228:231], a[168:171], a[40:43]\n\t"
"buffer_load_dwordx4 a[224:227], v252, s[8:11], s20 offen offset:2048 sc1\n\t"
"v_mfma_f32_16x16x32_bf16 a[40:43], v[248:251], a[172:175], a[40:43]\n\t"
"s_mov_b32 s20, 1044480\n\t"
"v_mfma_f32_16x16x32_bf16 a[40:43], a[212:215], a[176:179], a[40:43]\n\t"
"v_mfma_f32_16x16x32_bf16 a[48:51], a[228:231], a[172:175], a[48:51]\n\t"
"s_waitcnt lgkmcnt(2)\n\t"
"v_cvt_pk_bf16_f32 v236, v236, v237\n\t"
"v_cvt_pk_bf16_f32 v237, v238, v239\n\t"
"v_mfma_f32_16x16x32_bf16 a[48:51], v[248:251], a[176:179], a[48:51]\n\t"
"s_waitcnt lgkmcnt(0)\n\t"
"v_cvt_pk_bf16_f32 v238, v224, v225\n\t"
"v_cvt_pk_bf16_f32 v239, v226, v227\n\t"
"v_mfma_f32_16x16x32_bf16 a[48:51], a[212:215], a[180:183], a[48:51]\n\t"
"ds_write_b128 v243, v[236:239] offset:27648\n\t"
"v_mfma_f32_16x16x32_bf16 v[232:235], v[232:235], a[208:211], 0\n\t"
"buffer_load_dwordx4 a[240:243], v252, s[8:11], s20 offen offset:0 sc1\n\t"
"v_mfma_f32_16x16x32_bf16 v[228:231], v[228:231], a[208:211], 0\n\t"
"ds_read_b64_tr_b16 v[224:225], v240 offset:41472\n\t"
"v_mfma_f32_16x16x32_bf16 a[56:59], a[228:231], a[176:179], a[56:59]\n\t"
"buffer_load_dwordx4 a[244:247], v252, s[8:11], s20 offen offset:1024 sc1\n\t"
"v_mfma_f32_16x16x32_bf16 a[56:59], v[248:251], a[180:183], a[56:59]\n\t"
"ds_read_b64_tr_b16 v[226:227], v240 offset:43008\n\t"
"v_mfma_f32_16x16x32_bf16 a[56:59], a[212:215], a[184:187], a[56:59]\n\t"
"ds_read_b64_tr_b16 v[236:237], v241 offset:41472\n\t"
"v_mfma_f32_16x16x32_bf16 a[64:67], a[228:231], a[180:183], a[64:67]\n\t"
"ds_read_b64_tr_b16 v[238:239], v241 offset:43008\n\t"
"v_cvt_pk_bf16_f32 v232, v232, v233\n\t"
"v_mfma_f32_16x16x32_bf16 a[64:67], v[248:251], a[184:187], a[64:67]\n\t"
"v_cvt_pk_bf16_f32 v233, v234, v235\n\t"
"v_cvt_pk_bf16_f32 v234, v228, v229\n\t"
"v_mfma_f32_16x16x32_bf16 a[64:67], a[212:215], a[188:191], a[64:67]\n\t"
"v_cvt_pk_bf16_f32 v235, v230, v231\n\t"
"v_mfma_f32_16x16x32_bf16 a[72:75], a[228:231], a[184:187], a[72:75]\n\t"
"buffer_load_dwordx4 a[248:251], v252, s[8:11], s20 offen offset:2048 sc1\n\t"
"v_mfma_f32_16x16x32_bf16 a[72:75], v[248:251], a[188:191], a[72:75]\n\t"
"s_mov_b32 s20, 1056768\n\t"
"v_mfma_f32_16x16x32_bf16 a[72:75], a[212:215], a[192:195], a[72:75]\n\t"
"ds_write_b128 v243, v[232:235] offset:36864\n\t"
"v_mfma_f32_16x16x32_bf16 a[80:83], a[228:231], a[188:191], a[80:83]\n\t"
"buffer_load_dwordx4 a[252:255], v252, s[8:11], s20 offen offset:0 sc1\n\t"
"v_mfma_f32_16x16x32_bf16 a[80:83], v[248:251], a[192:195], a[80:83]\n\t"
"v_mfma_f32_16x16x32_bf16 a[80:83], a[212:215], a[196:199], a[80:83]\n\t"
"v_mfma_f32_16x16x32_bf16 a[88:91], a[228:231], a[192:195], a[88:91]\n\t"
"s_waitcnt lgkmcnt(3)\n\t"
"v_mfma_f32_16x16x32_bf16 a[88:91], v[248:251], a[196:199], a[88:91]\n\t"
"s_waitcnt lgkmcnt(1)\n\t"
"v_mfma_f32_16x16x32_bf16 a[88:91], a[212:215], a[200:203], a[88:91]\n\t"
"v_mfma_f32_16x16x32_bf16 v[224:227], v[224:227], a[208:211], 0\n\t"
"v_mfma_f32_16x16x32_bf16 v[236:239], v[236:239], a[208:211], 0\n\t"
"ds_read_b64_tr_b16 v[228:229], v246 offset:512\n\t"
"v_mfma_f32_16x16x32_bf16 a[96:99], a[228:231], a[196:199], a[96:99]\n\t"
"buffer_load_dwordx4 a[228:231], v252, s[8:11], s20 offen offset:1024 sc1\n\t"
"v_mfma_f32_16x16x32_bf16 a[96:99], v[248:251], a[200:203], a[96:99]\n\t"
"s_waitcnt vmcnt(8)\n\t"
"ds_read_b64_tr_b16 v[230:231], v246 offset:3584\n\t"
"v_mfma_f32_16x16x32_bf16 a[96:99], a[212:215], a[204:207], a[96:99]\n\t"
"v_mfma_f32_16x16x32_bf16 a[104:107], a[216:219], a[168:171], a[104:107]\n\t"
"buffer_load_dwordx4 v[248:251], v252, s[8:11], s20 offen offset:2048 sc1\n\t"
"v_mfma_f32_16x16x32_bf16 a[104:107], v[96:99], a[172:175], a[104:107]\n\t"
"s_mov_b32 s20, 1290240\n\t"
"ds_read_b64_tr_b16 v[232:233], v246 offset:6656\n\t"
"v_cvt_pk_bf16_f32 v224, v224, v225\n\t"
"v_mfma_f32_16x16x32_bf16 a[104:107], a[232:235], a[176:179], a[104:107]\n\t"
"ds_read_b64_tr_b16 v[234:235], v246 offset:9728\n\t"
"v_cvt_pk_bf16_f32 v225, v226, v227\n\t"
"v_mfma_f32_16x16x32_bf16 a[112:115], a[216:219], a[172:175], a[112:115]\n\t"
"ds_read_b128 a[168:171], v244 offset:9216\n\t"
"v_cvt_pk_bf16_f32 v226, v236, v237\n\t"
"v_mfma_f32_16x16x32_bf16 a[112:115], v[96:99], a[176:179], a[112:115]\n\t"
"ds_read_b128 a[172:175], v244 offset:9728\n\t"
"v_cvt_pk_bf16_f32 v227, v238, v239\n\t"
"v_mfma_f32_16x16x32_bf16 a[112:115], a[232:235], a[180:183], a[112:115]\n\t"
"ds_write_b128 v243, v[224:227] offset:46080\n\t"
"v_mfma_f32_16x16x32_bf16 a[120:123], a[216:219], a[176:179], a[120:123]\n\t"
"buffer_load_dwordx4 a[212:215], v252, s[8:11], s20 offen offset:0 sc1\n\t"
"v_mfma_f32_16x16x32_bf16 a[120:123], v[96:99], a[180:183], a[120:123]\n\t"
"ds_read_b64_tr_b16 v[236:237], v246 offset:12800\n\t"
"v_mfma_f32_16x16x32_bf16 a[120:123], a[232:235], a[184:187], a[120:123]\n\t"
"ds_read_b64_tr_b16 v[238:239], v246 offset:15872\n\t"
"v_mfma_f32_16x16x32_bf16 a[128:131], a[216:219], a[180:183], a[128:131]\n\t"
"s_waitcnt lgkmcnt(7)\n\t"
"ds_read_b128 a[176:179], v244 offset:10240\n\t"
"v_mfma_f32_16x16x32_bf16 a[128:131], v[96:99], a[184:187], a[128:131]\n\t"
"ds_read_b64_tr_b16 v[224:225], v246 offset:20480\n\t"
"v_mfma_f32_16x16x32_bf16 a[128:131], a[232:235], a[188:191], a[128:131]\n\t"
"ds_read_b64_tr_b16 v[226:227], v246 offset:23552\n\t"
"v_mfma_f32_16x16x32_bf16 v[228:231], v[228:231], v[220:223], 0\n\t"
"ds_read_b128 a[180:183], v244 offset:10752\n\t"
"v_mfma_f32_16x16x32_bf16 a[136:139], a[216:219], a[184:187], a[136:139]\n\t"
"s_waitcnt lgkmcnt(9)\n\t"
"ds_read_b128 a[184:187], v244 offset:11264\n\t"
"v_mfma_f32_16x16x32_bf16 a[136:139], v[96:99], a[188:191], a[136:139]\n\t"
"v_mfma_f32_16x16x32_bf16 a[136:139], a[232:235], a[192:195], a[136:139]\n\t"
"v_mfma_f32_16x16x32_bf16 v[232:235], v[232:235], v[220:223], 0\n\t"
"v_mfma_f32_16x16x32_bf16 a[144:147], a[216:219], a[188:191], a[144:147]\n\t"
"s_waitcnt lgkmcnt(5)\n\t"
"ds_read_b128 a[188:191], v244 offset:11776\n\t"
"v_cvt_pk_bf16_f32 v228, v228, v229\n\t"
"v_mfma_f32_16x16x32_bf16 a[144:147], v[96:99], a[192:195], a[144:147]\n\t"
"v_cvt_pk_bf16_f32 v229, v230, v231\n\t"
"v_mfma_f32_16x16x32_bf16 a[144:147], a[232:235], a[196:199], a[144:147]\n\t"
"v_mfma_f32_16x16x32_bf16 v[236:239], v[236:239], v[220:223], 0\n\t"
"v_mfma_f32_16x16x32_bf16 a[152:155], a[216:219], a[192:195], a[152:155]\n\t"
"s_waitcnt lgkmcnt(3)\n\t"
"ds_read_b128 a[192:195], v244 offset:12288\n\t"
"v_cvt_pk_bf16_f32 v230, v232, v233\n\t"
"v_mfma_f32_16x16x32_bf16 a[152:155], v[96:99], a[196:199], a[152:155]\n\t"
"v_cvt_pk_bf16_f32 v231, v234, v235\n\t"
"v_mfma_f32_16x16x32_bf16 a[152:155], a[232:235], a[200:203], a[152:155]\n\t"
"ds_read_b64_tr_b16 v[232:233], v246 offset:26624\n\t"
"v_mfma_f32_16x16x32_bf16 v[224:227], v[224:227], v[220:223], 0\n\t"
"ds_read_b64_tr_b16 v[234:235], v246 offset:29696\n\t"
"v_mfma_f32_16x16x32_bf16 a[160:163], a[216:219], a[196:199], a[160:163]\n\t"
"buffer_load_dwordx4 a[216:219], v252, s[8:11], s20 offen offset:1024 sc1\n\t"
"v_mfma_f32_16x16x32_bf16 a[160:163], v[96:99], a[200:203], a[160:163]\n\t"
"s_waitcnt vmcnt(8)\n\t"
"ds_read_b128 a[196:199], v244 offset:12800\n\t"
"v_cvt_pk_bf16_f32 v236, v236, v237\n\t"
"v_mfma_f32_16x16x32_bf16 a[160:163], a[232:235], a[204:207], a[160:163]\n\t"
"ds_read_b128 a[200:203], v244 offset:13312\n\t"
"v_cvt_pk_bf16_f32 v237, v238, v239\n\t"
"v_mfma_f32_16x16x32_bf16 v[200:203], a[236:239], a[168:171], v[200:203]\n\t"
"ds_read_b128 a[204:207], v244 offset:13824\n\t"
"v_mfma_f32_16x16x32_bf16 v[200:203], a[220:223], a[172:175], v[200:203]\n\t"
"buffer_load_dwordx4 v[96:99], v252, s[8:11], s20 offen offset:2048 sc1\n\t"
"v_mfma_f32_16x16x32_bf16 v[200:203], a[224:227], a[176:179], v[200:203]\n\t"
"s_waitcnt lgkmcnt(3)\n\t"
"s_mov_b32 s20, 1302528\n\t"
"v_cvt_pk_bf16_f32 v238, v224, v225\n\t"
"v_mfma_f32_16x16x32_bf16 v[232:235], v[232:235], v[220:223], 0\n\t"
"v_cvt_pk_bf16_f32 v239, v226, v227\n\t"
"v_mfma_f32_16x16x32_bf16 v[208:211], a[236:239], a[172:175], v[208:211]\n\t"
"buffer_load_dwordx4 a[232:235], v252, s[8:11], s20 offen offset:0 sc1\n\t"
"v_mfma_f32_16x16x32_bf16 v[208:211], a[220:223], a[176:179], v[208:211]\n\t"
"v_mfma_f32_16x16x32_bf16 v[208:211], a[224:227], a[180:183], v[208:211]\n\t"
"s_waitcnt lgkmcnt(0)\n\t"
"s_barrier\n\t"
"v_mfma_f32_16x16x32_bf16 v[216:219], a[236:239], a[176:179], v[216:219]\n\t"
"ds_read_b64_tr_b16 v[224:225], v246 offset:32768\n\t"
"v_mfma_f32_16x16x32_bf16 v[216:219], a[220:223], a[180:183], v[216:219]\n\t"
"ds_read_b64_tr_b16 v[226:227], v246 offset:35840\n\t"
"v_cvt_pk_bf16_f32 v232, v232, v233\n\t"
"v_mfma_f32_16x16x32_bf16 v[216:219], a[224:227], a[184:187], v[216:219]\n\t"
"ds_write2st64_b64 v245, v[228:229], v[230:231] offset0:0 offset1:1\n\t"
"v_mfma_f32_16x16x32_bf16 a[4:7], a[236:239], a[180:183], a[4:7]\n\t"
"v_cvt_pk_bf16_f32 v233, v234, v235\n\t"
"v_mfma_f32_16x16x32_bf16 a[4:7], a[220:223], a[184:187], a[4:7]\n\t"
"ds_read_b64_tr_b16 v[228:229], v246 offset:40448\n\t"
"v_mfma_f32_16x16x32_bf16 a[4:7], a[224:227], a[188:191], a[4:7]\n\t"
"ds_write2st64_b64 v245, v[236:237], v[238:239] offset0:2 offset1:3\n\t"
"v_mfma_f32_16x16x32_bf16 a[12:15], a[236:239], a[184:187], a[12:15]\n\t"
"v_mfma_f32_16x16x32_bf16 a[12:15], a[220:223], a[188:191], a[12:15]\n\t"
"ds_read_b64_tr_b16 v[230:231], v246 offset:43520\n\t"
"v_mfma_f32_16x16x32_bf16 a[12:15], a[224:227], a[192:195], a[12:15]\n\t"
"ds_write_b64 v245, v[232:233] offset:2048\n\t"
"v_mfma_f32_16x16x32_bf16 a[20:23], a[236:239], a[188:191], a[20:23]\n\t"
"s_waitcnt lgkmcnt(5)\n\t"
"ds_read_b64_tr_b16 v[232:233], v246 offset:46592\n\t"
"v_mfma_f32_16x16x32_bf16 a[20:23], a[220:223], a[192:195], a[20:23]\n\t"
"ds_read_b64_tr_b16 v[234:235], v246 offset:49664\n\t"
"v_mfma_f32_16x16x32_bf16 a[20:23], a[224:227], a[196:199], a[20:23]\n\t"
"v_mfma_f32_16x16x32_bf16 v[224:227], v[224:227], v[220:223], 0\n\t"
"v_mfma_f32_16x16x32_bf16 a[28:31], a[236:239], a[192:195], a[28:31]\n\t"
"s_waitcnt lgkmcnt(3)\n\t"
"v_mfma_f32_16x16x32_bf16 a[28:31], a[220:223], a[196:199], a[28:31]\n\t"
"v_mfma_f32_16x16x32_bf16 a[28:31], a[224:227], a[200:203], a[28:31]\n\t"
"v_mfma_f32_16x16x32_bf16 v[228:231], v[228:231], v[220:223], 0\n\t"
"v_mfma_f32_16x16x32_bf16 a[36:39], a[236:239], a[196:199], a[36:39]\n\t"
"buffer_load_dwordx4 a[236:239], v252, s[8:11], s20 offen offset:1024 sc1\n\t"
"v_mfma_f32_16x16x32_bf16 a[36:39], a[220:223], a[200:203], a[36:39]\n\t"
"s_waitcnt vmcnt(8) lgkmcnt(0)\n\t"
"v_cvt_pk_bf16_f32 v224, v224, v225\n\t"
"v_cvt_pk_bf16_f32 v225, v226, v227\n\t"
"v_mfma_f32_16x16x32_bf16 a[36:39], a[224:227], a[204:207], a[36:39]\n\t"
"v_mfma_f32_16x16x32_bf16 v[232:235], v[232:235], v[220:223], 0\n\t"
"buffer_load_dwordx4 a[220:223], v252, s[8:11], s20 offen offset:2048 sc1\n\t"
"v_mfma_f32_16x16x32_bf16 a[44:47], a[240:243], a[168:171], a[44:47]\n\t"
"s_mov_b32 s20, 1314816\n\t"
"v_cvt_pk_bf16_f32 v226, v228, v229\n\t"
"v_cvt_pk_bf16_f32 v227, v230, v231\n\t"
"v_mfma_f32_16x16x32_bf16 a[44:47], a[244:247], a[172:175], a[44:47]\n\t"
"ds_read_b64_tr_b16 v[228:229], v246 offset:52736\n\t"
"v_mfma_f32_16x16x32_bf16 a[44:47], a[248:251], a[176:179], a[44:47]\n\t"
"buffer_load_dwordx4 a[224:227], v252, s[8:11], s20 offen offset:0 sc1\n\t"
"v_mfma_f32_16x16x32_bf16 a[52:55], a[240:243], a[172:175], a[52:55]\n\t"
"ds_read_b64_tr_b16 v[230:231], v246 offset:55808\n\t"
"v_mfma_f32_16x16x32_bf16 a[52:55], a[244:247], a[176:179], a[52:55]\n\t"
"v_cvt_pk_bf16_f32 v232, v232, v233\n\t"
"v_cvt_pk_bf16_f32 v233, v234, v235\n\t"
"v_mfma_f32_16x16x32_bf16 a[52:55], a[248:251], a[180:183], a[52:55]\n\t"
"ds_write2st64_b64 v245, v[224:225], v[226:227] offset0:5 offset1:6\n\t"
"v_mfma_f32_16x16x32_bf16 a[60:63], a[240:243], a[176:179], a[60:63]\n\t"
"v_mfma_f32_16x16x32_bf16 a[60:63], a[244:247], a[180:183], a[60:63]\n\t"
"v_add_u32 v243, 55296, v243\n\t"
"v_mfma_f32_16x16x32_bf16 a[60:63], a[248:251], a[184:187], a[60:63]\n\t"
"v_mfma_f32_16x16x32_bf16 a[68:71], a[240:243], a[180:183], a[68:71]\n\t"
"v_mfma_f32_16x16x32_bf16 a[68:71], a[244:247], a[184:187], a[68:71]\n\t"
"v_mfma_f32_16x16x32_bf16 a[68:71], a[248:251], a[188:191], a[68:71]\n\t"
"v_mfma_f32_16x16x32_bf16 a[76:79], a[240:243], a[184:187], a[76:79]\n\t"
"s_waitcnt lgkmcnt(1)\n\t"
"v_mfma_f32_16x16x32_bf16 a[76:79], a[244:247], a[188:191], a[76:79]\n\t"
"v_mfma_f32_16x16x32_bf16 a[76:79], a[248:251], a[192:195], a[76:79]\n\t"
"v_mfma_f32_16x16x32_bf16 v[228:231], v[228:231], v[220:223], 0\n\t"
"v_mfma_f32_16x16x32_bf16 a[84:87], a[240:243], a[188:191], a[84:87]\n\t"
"v_mfma_f32_16x16x32_bf16 a[84:87], a[244:247], a[192:195], a[84:87]\n\t"
"v_mfma_f32_16x16x32_bf16 a[84:87], a[248:251], a[196:199], a[84:87]\n\t"
"v_mfma_f32_16x16x32_bf16 a[92:95], a[240:243], a[192:195], a[92:95]\n\t"
"v_mfma_f32_16x16x32_bf16 a[92:95], a[244:247], a[196:199], a[92:95]\n\t"
"v_cvt_pk_bf16_f32 v234, v228, v229\n\t"
"v_cvt_pk_bf16_f32 v235, v230, v231\n\t"
"v_mfma_f32_16x16x32_bf16 a[92:95], a[248:251], a[200:203], a[92:95]\n\t"
"ds_write2st64_b64 v245, v[232:233], v[234:235] offset0:7 offset1:8\n\t"
"v_mfma_f32_16x16x32_bf16 a[100:103], a[240:243], a[196:199], a[100:103]\n\t"
"buffer_load_dwordx4 a[240:243], v252, s[8:11], s20 offen offset:1024 sc1\n\t"
"v_mfma_f32_16x16x32_bf16 a[100:103], a[244:247], a[200:203], a[100:103]\n\t"
"s_waitcnt vmcnt(8)\n\t"
"v_mfma_f32_16x16x32_bf16 a[100:103], a[248:251], a[204:207], a[100:103]\n\t"
"v_mfma_f32_16x16x32_bf16 a[108:111], a[252:255], a[168:171], a[108:111]\n\t"
"buffer_load_dwordx4 a[244:247], v252, s[8:11], s20 offen offset:2048 sc1\n\t"
"v_mfma_f32_16x16x32_bf16 a[108:111], a[228:231], a[172:175], a[108:111]\n\t"
"s_mov_b32 s20, 1327104\n\t"
"v_mfma_f32_16x16x32_bf16 a[108:111], v[248:251], a[176:179], a[108:111]\n\t"
"s_waitcnt lgkmcnt(0)\n\t"
"s_barrier\n\t"
"v_mfma_f32_16x16x32_bf16 a[116:119], a[252:255], a[172:175], a[116:119]\n\t"
"ds_read_b64_tr_b16 v[236:237], v240 offset:512\n\t"
"v_mfma_f32_16x16x32_bf16 a[116:119], a[228:231], a[176:179], a[116:119]\n\t"
"ds_read_b64_tr_b16 v[238:239], v240 offset:2048\n\t"
"v_mfma_f32_16x16x32_bf16 a[116:119], v[248:251], a[180:183], a[116:119]\n\t"
"ds_read_b64_tr_b16 v[224:225], v241 offset:512\n\t"
"v_mfma_f32_16x16x32_bf16 a[124:127], a[252:255], a[176:179], a[124:127]\n\t"
"ds_read_b64_tr_b16 v[226:227], v241 offset:2048\n\t"
"v_mfma_f32_16x16x32_bf16 a[124:127], a[228:231], a[180:183], a[124:127]\n\t"
"buffer_load_dwordx4 a[248:251], v252, s[8:11], s20 offen offset:0 sc1\n\t"
"v_mfma_f32_16x16x32_bf16 a[124:127], v[248:251], a[184:187], a[124:127]\n\t"
"ds_read_b64_tr_b16 v[228:229], v240 offset:20480\n\t"
"v_mfma_f32_16x16x32_bf16 a[132:135], a[252:255], a[180:183], a[132:135]\n\t"
"s_waitcnt lgkmcnt(3)\n\t"
"ds_read_b64_tr_b16 v[230:231], v240 offset:22016\n\t"
"v_mfma_f32_16x16x32_bf16 a[132:135], a[228:231], a[184:187], a[132:135]\n\t"
"ds_read_b64_tr_b16 v[232:233], v241 offset:20480\n\t"
"v_mfma_f32_16x16x32_bf16 a[132:135], v[248:251], a[188:191], a[132:135]\n\t"
"s_waitcnt lgkmcnt(3)\n\t"
"ds_read_b64_tr_b16 v[234:235], v241 offset:22016\n\t"
"v_mfma_f32_16x16x32_bf16 v[236:239], v[236:239], a[208:211], 0\n\t"
"ds_read_b128 a[168:171], v244 offset:0\n\t"
"v_mfma_f32_16x16x32_bf16 v[224:227], v[224:227], a[208:211], 0\n\t"
"ds_read_b128 a[172:175], v244 offset:512\n\t"
"v_mfma_f32_16x16x32_bf16 a[140:143], a[252:255], a[184:187], a[140:143]\n\t"
"ds_read_b128 a[176:179], v244 offset:1024\n\t"
"v_mfma_f32_16x16x32_bf16 a[140:143], a[228:231], a[188:191], a[140:143]\n\t"
"ds_read_b128 a[180:183], v244 offset:1536\n\t"
"v_mfma_f32_16x16x32_bf16 a[140:143], v[248:251], a[192:195], a[140:143]\n\t"
"ds_read_b128 a[184:187], v244 offset:2048\n\t"
"v_mfma_f32_16x16x32_bf16 a[148:151], a[252:255], a[188:191], a[148:151]\n\t"
"s_waitcnt lgkmcnt(7)\n\t"
"ds_read_b128 a[188:191], v244 offset:2560\n\t"
"v_cvt_pk_bf16_f32 v236, v236, v237\n\t"
"v_mfma_f32_16x16x32_bf16 a[148:151], a[228:231], a[192:195], a[148:151]\n\t"
"s_waitcnt lgkmcnt(6)\n\t"
"v_cvt_pk_bf16_f32 v237, v238, v239\n\t"
"v_cvt_pk_bf16_f32 v238, v224, v225\n\t"
"v_mfma_f32_16x16x32_bf16 a[148:151], v[248:251], a[196:199], a[148:151]\n\t"
"v_cvt_pk_bf16_f32 v239, v226, v227\n\t"
"v_mfma_f32_16x16x32_bf16 v[228:231], v[228:231], a[208:211], 0\n\t"
"ds_write_b128 v243, v[236:239] offset:27648\n\t"
"v_mfma_f32_16x16x32_bf16 v[232:235], v[232:235], a[208:211], 0\n\t"
"v_mfma_f32_16x16x32_bf16 a[156:159], a[252:255], a[192:195], a[156:159]\n\t"
"ds_read_b128 a[192:195], v244 offset:3072\n\t"
"v_mfma_f32_16x16x32_bf16 a[156:159], a[228:231], a[196:199], a[156:159]\n\t"
"ds_read_b64_tr_b16 v[224:225], v240 offset:40448\n\t"
"v_mfma_f32_16x16x32_bf16 a[156:159], v[248:251], a[200:203], a[156:159]\n\t"
"ds_read_b64_tr_b16 v[226:227], v240 offset:41984\n\t"
"v_mfma_f32_16x16x32_bf16 a[164:167], a[252:255], a[196:199], a[164:167]\n\t"
"buffer_load_dwordx4 a[252:255], v252, s[8:11], s20 offen offset:1024 sc1\n\t"
"v_mfma_f32_16x16x32_bf16 a[164:167], a[228:231], a[200:203], a[164:167]\n\t"
"s_waitcnt vmcnt(8) lgkmcnt(7)\n\t"
"ds_read_b64_tr_b16 v[236:237], v241 offset:40448\n\t"
"v_cvt_pk_bf16_f32 v228, v228, v229\n\t"
"v_mfma_f32_16x16x32_bf16 a[164:167], v[248:251], a[204:207], a[164:167]\n\t"
"ds_read_b64_tr_b16 v[238:239], v241 offset:41984\n\t"
"v_cvt_pk_bf16_f32 v229, v230, v231\n\t"
"Lst10_%=:\n\t"
"v_mfma_f32_16x16x32_bf16 v[196:199], a[212:215], a[168:171], v[196:199]\n\t"
"s_waitcnt lgkmcnt(8)\n\t"
"ds_read_b128 a[196:199], v244 offset:3584\n\t"
"v_cvt_pk_bf16_f32 v230, v232, v233\n\t"
"v_mfma_f32_16x16x32_bf16 v[196:199], a[216:219], a[172:175], v[196:199]\n\t"
"ds_read_b128 a[200:203], v244 offset:4096\n\t"
"v_cvt_pk_bf16_f32 v231, v234, v235\n\t"
"v_mfma_f32_16x16x32_bf16 v[196:199], v[96:99], a[176:179], v[196:199]\n\t"
"ds_write_b128 v243, v[228:231] offset:36864\n\t"
"v_mfma_f32_16x16x32_bf16 v[204:207], a[212:215], a[172:175], v[204:207]\n\t"
"buffer_load_dwordx4 v[248:251], v252, s[8:11], s20 offen offset:2048 sc1\n\t"
"v_mfma_f32_16x16x32_bf16 v[204:207], a[216:219], a[176:179], v[204:207]\n\t"
"s_waitcnt lgkmcnt(10)\n\t"
"ds_read_b128 a[204:207], v244 offset:4608\n\t"
"v_mfma_f32_16x16x32_bf16 v[204:207], v[96:99], a[180:183], v[204:207]\n\t"
"s_mov_b32 s20, 1339392\n\t"
"v_subrev_u32 v240, 59904, v240\n\t"
"v_subrev_u32 v241, 59904, v241\n\t"
"v_mfma_f32_16x16x32_bf16 v[212:215], a[212:215], a[176:179], v[212:215]\n\t"
"s_waitcnt lgkmcnt(6)\n\t"
"ds_read_b64_tr_b16 v[232:233], v240 offset:512\n\t"
"v_mfma_f32_16x16x32_bf16 v[212:215], a[216:219], a[180:183], v[212:215]\n\t"
"buffer_load_dwordx4 a[228:231], v252, s[8:11], s20 offen offset:0 sc1\n\t"
"v_mfma_f32_16x16x32_bf16 v[212:215], v[96:99], a[184:187], v[212:215]\n\t"
"s_waitcnt lgkmcnt(5)\n\t"
"ds_read_b64_tr_b16 v[234:235], v240 offset:2048\n\t"
"v_mfma_f32_16x16x32_bf16 v[224:227], v[224:227], a[208:211], 0\n\t"
"ds_read_b64_tr_b16 v[228:229], v241 offset:512\n\t"
"v_mfma_f32_16x16x32_bf16 v[236:239], v[236:239], a[208:211], 0\n\t"
"ds_read_b64_tr_b16 v[230:231], v241 offset:2048\n\t"
"v_mfma_f32_16x16x32_bf16 a[0:3], a[212:215], a[180:183], a[0:3]\n\t"
"v_mfma_f32_16x16x32_bf16 a[0:3], a[216:219], a[184:187], a[0:3]\n\t"
"v_mfma_f32_16x16x32_bf16 a[0:3], v[96:99], a[188:191], a[0:3]\n\t"
"v_mfma_f32_16x16x32_bf16 a[8:11], a[212:215], a[184:187], a[8:11]\n\t"
"s_waitcnt lgkmcnt(2)\n\t"
"v_cvt_pk_bf16_f32 v224, v224, v225\n\t"
"v_cvt_pk_bf16_f32 v225, v226, v227\n\t"
"v_mfma_f32_16x16x32_bf16 a[8:11], a[216:219], a[188:191], a[8:11]\n\t"
"s_waitcnt lgkmcnt(0)\n\t"
"v_cvt_pk_bf16_f32 v226, v236, v237\n\t"
"v_cvt_pk_bf16_f32 v227, v238, v239\n\t"
"v_mfma_f32_16x16x32_bf16 a[8:11], v[96:99], a[192:195], a[8:11]\n\t"
"ds_write_b128 v243, v[224:227] offset:46080\n\t"
"v_mfma_f32_16x16x32_bf16 v[232:235], v[232:235], a[208:211], 0\n\t"
"v_mfma_f32_16x16x32_bf16 v[228:231], v[228:231], a[208:211], 0\n\t"
"ds_read_b64_tr_b16 v[236:237], v240 offset:20480\n\t"
"v_mfma_f32_16x16x32_bf16 a[16:19], a[212:215], a[188:191], a[16:19]\n\t"
"ds_read_b64_tr_b16 v[238:239], v240 offset:22016\n\t"
"v_mfma_f32_16x16x32_bf16 a[16:19], a[216:219], a[192:195], a[16:19]\n\t"
"ds_read_b64_tr_b16 v[224:225], v241 offset:20480\n\t"
"v_mfma_f32_16x16x32_bf16 a[16:19], v[96:99], a[196:199], a[16:19]\n\t"
"ds_read_b64_tr_b16 v[226:227], v241 offset:22016\n\t"
"v_mfma_f32_16x16x32_bf16 a[24:27], a[212:215], a[192:195], a[24:27]\n\t"
"v_cvt_pk_bf16_f32 v232, v232, v233\n\t"
"v_cvt_pk_bf16_f32 v233, v234, v235\n\t"
"v_mfma_f32_16x16x32_bf16 a[24:27], a[216:219], a[196:199], a[24:27]\n\t"
"v_cvt_pk_bf16_f32 v234, v228, v229\n\t"
"v_cvt_pk_bf16_f32 v235, v230, v231\n\t"
"v_mfma_f32_16x16x32_bf16 a[24:27], v[96:99], a[200:203], a[24:27]\n\t"
"ds_write_b128 v243, v[232:235] offset:0\n\t"
"v_mfma_f32_16x16x32_bf16 a[32:35], a[212:215], a[196:199], a[32:35]\n\t"
"buffer_load_dwordx4 a[212:215], v252, s[8:11], s20 offen offset:1024 sc1\n\t"
"v_mfma_f32_16x16x32_bf16 a[32:35], a[216:219], a[200:203], a[32:35]\n\t"
"s_waitcnt vmcnt(8)\n\t"
"ds_read_b64_tr_b16 v[228:229], v240 offset:40448\n\t"
"v_mfma_f32_16x16x32_bf16 a[32:35], v[96:99], a[204:207], a[32:35]\n\t"
"ds_read_b64_tr_b16 v[230:231], v240 offset:41984\n\t"
"v_mfma_f32_16x16x32_bf16 a[40:43], a[232:235], a[168:171], a[40:43]\n\t"
"buffer_load_dwordx4 v[96:99], v252, s[8:11], s20 offen offset:2048 sc1\n\t"
"v_mfma_f32_16x16x32_bf16 a[40:43], a[236:239], a[172:175], a[40:43]\n\t"
"s_mov_b32 s20, 1351680\n\t"
"ds_read_b64_tr_b16 v[232:233], v241 offset:40448\n\t"
"v_mfma_f32_16x16x32_bf16 a[40:43], a[220:223], a[176:179], a[40:43]\n\t"
"ds_read_b64_tr_b16 v[234:235], v241 offset:41984\n\t"
"v_mfma_f32_16x16x32_bf16 a[48:51], a[232:235], a[172:175], a[48:51]\n\t"
"buffer_load_dwordx4 a[216:219], v252, s[8:11], s20 offen offset:0 sc1\n\t"
"v_mfma_f32_16x16x32_bf16 a[48:51], a[236:239], a[176:179], a[48:51]\n\t"
"s_waitcnt lgkmcnt(5)\n\t"
"v_mfma_f32_16x16x32_bf16 a[48:51], a[220:223], a[180:183], a[48:51]\n\t"
"v_mfma_f32_16x16x32_bf16 v[236:239], v[236:239], a[208:211], 0\n\t"
"v_mfma_f32_16x16x32_bf16 v[224:227], v[224:227], a[208:211], 0\n\t"
"v_xor_b32 v246, 0xc0, v246\n\t"
"v_mfma_f32_16x16x32_bf16 a[56:59], a[232:235], a[176:179], a[56:59]\n\t"
"v_mfma_f32_16x16x32_bf16 a[56:59], a[236:239], a[180:183], a[56:59]\n\t"
"v_mfma_f32_16x16x32_bf16 a[56:59], a[220:223], a[184:187], a[56:59]\n\t"
"v_mfma_f32_16x16x32_bf16 a[64:67], a[232:235], a[180:183], a[64:67]\n\t"
"s_waitcnt lgkmcnt(2)\n\t"
"v_cvt_pk_bf16_f32 v236, v236, v237\n\t"
"v_cvt_pk_bf16_f32 v237, v238, v239\n\t"
"v_mfma_f32_16x16x32_bf16 a[64:67], a[236:239], a[184:187], a[64:67]\n\t"
"s_waitcnt lgkmcnt(0)\n\t"
"v_cvt_pk_bf16_f32 v238, v224, v225\n\t"
"v_cvt_pk_bf16_f32 v239, v226, v227\n\t"
"v_mfma_f32_16x16x32_bf16 a[64:67], a[220:223], a[188:191], a[64:67]\n\t"
"ds_write_b128 v243, v[236:239] offset:9216\n\t"
"v_mfma_f32_16x16x32_bf16 v[228:231], v[228:231], a[208:211], 0\n\t"
"v_mfma_f32_16x16x32_bf16 v[232:235], v[232:235], a[208:211], 0\n\t"
"v_mfma_f32_16x16x32_bf16 a[72:75], a[232:235], a[184:187], a[72:75]\n\t"
"v_mfma_f32_16x16x32_bf16 a[72:75], a[236:239], a[188:191], a[72:75]\n\t"
"v_mfma_f32_16x16x32_bf16 a[72:75], a[220:223], a[192:195], a[72:75]\n\t"
"v_mfma_f32_16x16x32_bf16 a[80:83], a[232:235], a[188:191], a[80:83]\n\t"
"v_cvt_pk_bf16_f32 v228, v228, v229\n\t"
"v_cvt_pk_bf16_f32 v229, v230, v231\n\t"
"v_mfma_f32_16x16x32_bf16 a[80:83], a[236:239], a[192:195], a[80:83]\n\t"
"v_cvt_pk_bf16_f32 v230, v232, v233\n\t"
"v_cvt_pk_bf16_f32 v231, v234, v235\n\t"
"v_mfma_f32_16x16x32_bf16 a[80:83], a[220:223], a[196:199], a[80:83]\n\t"
"ds_write_b128 v243, v[228:231] offset:18432\n\t"
"v_mfma_f32_16x16x32_bf16 a[88:91], a[232:235], a[192:195], a[88:91]\n\t"
"v_mfma_f32_16x16x32_bf16 a[88:91], a[236:239], a[196:199], a[88:91]\n\t"
"ds_read_b64_tr_b16 v[224:225], v246 offset:512\n\t"
"v_mfma_f32_16x16x32_bf16 a[88:91], a[220:223], a[200:203], a[88:91]\n\t"
"ds_read_b64_tr_b16 v[226:227], v246 offset:3584\n\t"
"v_mfma_f32_16x16x32_bf16 a[96:99], a[232:235], a[196:199], a[96:99]\n\t"
"buffer_load_dwordx4 a[232:235], v252, s[8:11], s20 offen offset:1024 sc1\n\t"
"v_mfma_f32_16x16x32_bf16 a[96:99], a[236:239], a[200:203], a[96:99]\n\t"
"s_waitcnt vmcnt(8)\n\t"
"ds_read_b64_tr_b16 v[236:237], v246 offset:6656\n\t"
"v_mfma_f32_16x16x32_bf16 a[96:99], a[220:223], a[204:207], a[96:99]\n\t"
"ds_read_b64_tr_b16 v[238:239], v246 offset:9728\n\t"
"v_mfma_f32_16x16x32_bf16 a[104:107], a[224:227], a[168:171], a[104:107]\n\t"
"buffer_load_dwordx4 a[220:223], v252, s[8:11], s20 offen offset:2048 sc1\n\t"
"v_mfma_f32_16x16x32_bf16 a[104:107], a[240:243], a[172:175], a[104:107]\n\t"
"s_mov_b32 s20, 1216512\n\t"
"ds_read_b64_tr_b16 v[232:233], v246 offset:12800\n\t"
"v_mfma_f32_16x16x32_bf16 a[104:107], a[244:247], a[176:179], a[104:107]\n\t"
"ds_read_b64_tr_b16 v[234:235], v246 offset:15872\n\t"
"v_mfma_f32_16x16x32_bf16 a[112:115], a[224:227], a[172:175], a[112:115]\n\t"
"buffer_load_dwordx4 a[236:239], v252, s[8:11], s20 offen offset:0 sc1\n\t"
"v_mfma_f32_16x16x32_bf16 a[112:115], a[240:243], a[176:179], a[112:115]\n\t"
"ds_read_b128 a[168:171], v244 offset:9216\n\t"
"v_mfma_f32_16x16x32_bf16 a[112:115], a[244:247], a[180:183], a[112:115]\n\t"
"ds_read_b64_tr_b16 v[228:229], v246 offset:20480\n\t"
"v_mfma_f32_16x16x32_bf16 a[120:123], a[224:227], a[176:179], a[120:123]\n\t"
"s_waitcnt lgkmcnt(6)\n\t"
"ds_read_b64_tr_b16 v[230:231], v246 offset:23552\n\t"
"v_mfma_f32_16x16x32_bf16 a[120:123], a[240:243], a[180:183], a[120:123]\n\t"
"ds_read_b128 a[172:175], v244 offset:9728\n\t"
"v_mfma_f32_16x16x32_bf16 a[120:123], a[244:247], a[184:187], a[120:123]\n\t"
"ds_read_b128 a[176:179], v244 offset:10240\n\t"
"v_mfma_f32_16x16x32_bf16 v[224:227], v[224:227], v[220:223], 0\n\t"
"v_mfma_f32_16x16x32_bf16 a[128:131], a[224:227], a[180:183], a[128:131]\n\t"
"s_waitcnt lgkmcnt(7)\n\t"
"ds_read_b128 a[180:183], v244 offset:10752\n\t"
"v_mfma_f32_16x16x32_bf16 a[128:131], a[240:243], a[184:187], a[128:131]\n\t"
"v_mfma_f32_16x16x32_bf16 a[128:131], a[244:247], a[188:191], a[128:131]\n\t"
"v_mfma_f32_16x16x32_bf16 v[236:239], v[236:239], v[220:223], 0\n\t"
"v_mfma_f32_16x16x32_bf16 a[136:139], a[224:227], a[184:187], a[136:139]\n\t"
"s_waitcnt lgkmcnt(6)\n\t"
"ds_read_b128 a[184:187], v244 offset:11264\n\t"
"v_cvt_pk_bf16_f32 v224, v224, v225\n\t"
"v_mfma_f32_16x16x32_bf16 a[136:139], a[240:243], a[188:191], a[136:139]\n\t"
"v_cvt_pk_bf16_f32 v225, v226, v227\n\t"
"v_mfma_f32_16x16x32_bf16 a[136:139], a[244:247], a[192:195], a[136:139]\n\t"
"v_mfma_f32_16x16x32_bf16 v[232:235], v[232:235], v[220:223], 0\n\t"
"v_mfma_f32_16x16x32_bf16 a[144:147], a[224:227], a[188:191], a[144:147]\n\t"
"s_waitcnt lgkmcnt(4)\n\t"
"ds_read_b128 a[188:191], v244 offset:11776\n\t"
"v_cvt_pk_bf16_f32 v226, v236, v237\n\t"
"v_mfma_f32_16x16x32_bf16 a[144:147], a[240:243], a[192:195], a[144:147]\n\t"
"v_cvt_pk_bf16_f32 v227, v238, v239\n\t"
"v_mfma_f32_16x16x32_bf16 a[144:147], a[244:247], a[196:199], a[144:147]\n\t"
"v_mfma_f32_16x16x32_bf16 v[228:231], v[228:231], v[220:223], 0\n\t"
"v_mfma_f32_16x16x32_bf16 a[152:155], a[224:227], a[192:195], a[152:155]\n\t"
"ds_read_b128 a[192:195], v244 offset:12288\n\t"
"v_cvt_pk_bf16_f32 v232, v232, v233\n\t"
"v_mfma_f32_16x16x32_bf16 a[152:155], a[240:243], a[196:199], a[152:155]\n\t"
"v_cvt_pk_bf16_f32 v233, v234, v235\n\t"
"v_mfma_f32_16x16x32_bf16 a[152:155], a[244:247], a[200:203], a[152:155]\n\t"
"v_mfma_f32_16x16x32_bf16 a[160:163], a[224:227], a[196:199], a[160:163]\n\t"
"buffer_load_dwordx4 a[224:227], v252, s[8:11], s20 offen offset:1024 sc1\n\t"
"v_mfma_f32_16x16x32_bf16 a[160:163], a[240:243], a[200:203], a[160:163]\n\t"
"s_waitcnt vmcnt(8) lgkmcnt(4)\n\t"
"ds_read_b128 a[196:199], v244 offset:12800\n\t"
"v_cvt_pk_bf16_f32 v234, v228, v229\n\t"
"v_mfma_f32_16x16x32_bf16 a[160:163], a[244:247], a[204:207], a[160:163]\n\t"
"ds_read_b128 a[200:203], v244 offset:13312\n\t"
"v_cvt_pk_bf16_f32 v235, v230, v231\n\t"
"v_mfma_f32_16x16x32_bf16 v[200:203], a[248:251], a[168:171], v[200:203]\n\t"
"ds_read_b64_tr_b16 v[228:229], v246 offset:26624\n\t"
"v_mfma_f32_16x16x32_bf16 v[200:203], a[252:255], a[172:175], v[200:203]\n\t"
"s_waitcnt lgkmcnt(6)\n\t"
"ds_read_b64_tr_b16 v[230:231], v246 offset:29696\n\t"
"v_mfma_f32_16x16x32_bf16 v[200:203], v[248:251], a[176:179], v[200:203]\n\t"
"s_waitcnt lgkmcnt(0)\n\t"
"ds_read_b128 a[204:207], v244 offset:13824\n\t"
"v_mfma_f32_16x16x32_bf16 v[228:231], v[228:231], v[220:223], 0\n\t"
"buffer_load_dwordx4 a[240:243], v252, s[8:11], s20 offen offset:2048 sc1\n\t"
"s_mov_b32 s20, 1228800\n\t"
"v_mfma_f32_16x16x32_bf16 v[208:211], a[248:251], a[172:175], v[208:211]\n\t"
"buffer_load_dwordx4 a[244:247], v252, s[8:11], s20 offen offset:0 sc1\n\t"
"v_mfma_f32_16x16x32_bf16 v[208:211], a[252:255], a[176:179], v[208:211]\n\t"
"v_mfma_f32_16x16x32_bf16 v[208:211], v[248:251], a[180:183], v[208:211]\n\t"
"s_waitcnt lgkmcnt(0)\n\t"
"s_barrier\n\t"
"v_mfma_f32_16x16x32_bf16 v[216:219], a[248:251], a[176:179], v[216:219]\n\t"
"v_mfma_f32_16x16x32_bf16 v[216:219], a[252:255], a[180:183], v[216:219]\n\t"
"v_cvt_pk_bf16_f32 v228, v228, v229\n\t"
"v_cvt_pk_bf16_f32 v229, v230, v231\n\t"
"v_mfma_f32_16x16x32_bf16 v[216:219], v[248:251], a[184:187], v[216:219]\n\t"
"ds_write2st64_b64 v245, v[224:225], v[226:227] offset0:0 offset1:1\n\t"
"v_mfma_f32_16x16x32_bf16 a[4:7], a[248:251], a[180:183], a[4:7]\n\t"
"v_mfma_f32_16x16x32_bf16 a[4:7], a[252:255], a[184:187], a[4:7]\n\t"
"ds_read_b64_tr_b16 v[224:225], v246 offset:32768\n\t"
"v_mfma_f32_16x16x32_bf16 a[4:7], v[248:251], a[188:191], a[4:7]\n\t"
"ds_write2st64_b64 v245, v[232:233], v[234:235] offset0:2 offset1:3\n\t"
"v_mfma_f32_16x16x32_bf16 a[12:15], a[248:251], a[184:187], a[12:15]\n\t"
"v_mfma_f32_16x16x32_bf16 a[12:15], a[252:255], a[188:191], a[12:15]\n\t"
"ds_read_b64_tr_b16 v[226:227], v246 offset:35840\n\t"
"v_mfma_f32_16x16x32_bf16 a[12:15], v[248:251], a[192:195], a[12:15]\n\t"
"ds_write_b64 v245, v[228:229] offset:2048\n\t"
"v_mfma_f32_16x16x32_bf16 a[20:23], a[248:251], a[188:191], a[20:23]\n\t"
"ds_read_b64_tr_b16 v[232:233], v246 offset:40448\n\t"
"v_mfma_f32_16x16x32_bf16 a[20:23], a[252:255], a[192:195], a[20:23]\n\t"
"ds_read_b64_tr_b16 v[234:235], v246 offset:43520\n\t"
"v_mfma_f32_16x16x32_bf16 a[20:23], v[248:251], a[196:199], a[20:23]\n\t"
"s_waitcnt lgkmcnt(3)\n\t"
"ds_read_b64_tr_b16 v[228:229], v246 offset:46592\n\t"
"v_mfma_f32_16x16x32_bf16 v[224:227], v[224:227], v[220:223], 0\n\t"
"ds_read_b64_tr_b16 v[230:231], v246 offset:49664\n\t"
"v_mfma_f32_16x16x32_bf16 a[28:31], a[248:251], a[192:195], a[28:31]\n\t"
"v_mfma_f32_16x16x32_bf16 a[28:31], a[252:255], a[196:199], a[28:31]\n\t"
"v_mfma_f32_16x16x32_bf16 a[28:31], v[248:251], a[200:203], a[28:31]\n\t"
"s_waitcnt lgkmcnt(2)\n\t"
"v_mfma_f32_16x16x32_bf16 v[232:235], v[232:235], v[220:223], 0\n\t"
"v_mfma_f32_16x16x32_bf16 a[36:39], a[248:251], a[196:199], a[36:39]\n\t"
"buffer_load_dwordx4 a[248:251], v252, s[8:11], s20 offen offset:1024 sc1\n\t"
"v_mfma_f32_16x16x32_bf16 a[36:39], a[252:255], a[200:203], a[36:39]\n\t"
"s_waitcnt vmcnt(8) lgkmcnt(0)\n\t"
"v_cvt_pk_bf16_f32 v224, v224, v225\n\t"
"v_cvt_pk_bf16_f32 v225, v226, v227\n\t"
"v_mfma_f32_16x16x32_bf16 a[36:39], v[248:251], a[204:207], a[36:39]\n\t"
"v_mfma_f32_16x16x32_bf16 v[228:231], v[228:231], v[220:223], 0\n\t"
"buffer_load_dwordx4 v[248:251], v252, s[8:11], s20 offen offset:2048 sc1\n\t"
"v_mfma_f32_16x16x32_bf16 a[44:47], a[228:231], a[168:171], a[44:47]\n\t"
"s_mov_b32 s20, 1241088\n\t"
"v_cvt_pk_bf16_f32 v226, v232, v233\n\t"
"v_cvt_pk_bf16_f32 v227, v234, v235\n\t"
"v_mfma_f32_16x16x32_bf16 a[44:47], a[212:215], a[172:175], a[44:47]\n\t"
"ds_read_b64_tr_b16 v[232:233], v246 offset:52736\n\t"
"v_mfma_f32_16x16x32_bf16 a[44:47], v[96:99], a[176:179], a[44:47]\n\t"
"buffer_load_dwordx4 a[252:255], v252, s[8:11], s20 offen offset:0 sc1\n\t"
"v_mfma_f32_16x16x32_bf16 a[52:55], a[228:231], a[172:175], a[52:55]\n\t"
"ds_read_b64_tr_b16 v[234:235], v246 offset:55808\n\t"
"v_mfma_f32_16x16x32_bf16 a[52:55], a[212:215], a[176:179], a[52:55]\n\t"
"v_cvt_pk_bf16_f32 v228, v228, v229\n\t"
"v_cvt_pk_bf16_f32 v229, v230, v231\n\t"
"v_mfma_f32_16x16x32_bf16 a[52:55], v[96:99], a[180:183], a[52:55]\n\t"
"ds_write2st64_b64 v245, v[224:225], v[226:227] offset0:5 offset1:6\n\t"
"v_mfma_f32_16x16x32_bf16 a[60:63], a[228:231], a[176:179], a[60:63]\n\t"
"v_mfma_f32_16x16x32_bf16 a[60:63], a[212:215], a[180:183], a[60:63]\n\t"
"v_subrev_u32 v243, 55296, v243\n\t"
"v_mfma_f32_16x16x32_bf16 a[60:63], v[96:99], a[184:187], a[60:63]\n\t"
"v_mfma_f32_16x16x32_bf16 a[68:71], a[228:231], a[180:183], a[68:71]\n\t"
"v_mfma_f32_16x16x32_bf16 a[68:71], a[212:215], a[184:187], a[68:71]\n\t"
"v_mfma_f32_16x16x32_bf16 a[68:71], v[96:99], a[188:191], a[68:71]\n\t"
"v_mfma_f32_16x16x32_bf16 a[76:79], a[228:231], a[184:187], a[76:79]\n\t"
"s_waitcnt lgkmcnt(1)\n\t"
"v_mfma_f32_16x16x32_bf16 a[76:79], a[212:215], a[188:191], a[76:79]\n\t"
"v_mfma_f32_16x16x32_bf16 a[76:79], v[96:99], a[192:195], a[76:79]\n\t"
"v_mfma_f32_16x16x32_bf16 v[232:235], v[232:235], v[220:223], 0\n\t"
"v_mfma_f32_16x16x32_bf16 a[84:87], a[228:231], a[188:191], a[84:87]\n\t"
"v_mfma_f32_16x16x32_bf16 a[84:87], a[212:215], a[192:195], a[84:87]\n\t"
"v_mfma_f32_16x16x32_bf16 a[84:87], v[96:99], a[196:199], a[84:87]\n\t"
"v_mfma_f32_16x16x32_bf16 a[92:95], a[228:231], a[192:195], a[92:95]\n\t"
"v_mfma_f32_16x16x32_bf16 a[92:95], a[212:215], a[196:199], a[92:95]\n\t"
"v_cvt_pk_bf16_f32 v230, v232, v233\n\t"
"v_cvt_pk_bf16_f32 v231, v234, v235\n\t"
"v_mfma_f32_16x16x32_bf16 a[92:95], v[96:99], a[200:203], a[92:95]\n\t"
"ds_write2st64_b64 v245, v[228:229], v[230:231] offset0:7 offset1:8\n\t"
"v_mfma_f32_16x16x32_bf16 a[100:103], a[228:231], a[196:199], a[100:103]\n\t"
"buffer_load_dwordx4 a[228:231], v252, s[8:11], s20 offen offset:1024 sc1\n\t"
"v_mfma_f32_16x16x32_bf16 a[100:103], a[212:215], a[200:203], a[100:103]\n\t"
"s_waitcnt vmcnt(8)\n\t"
"ds_read_b64_tr_b16 v[236:237], v240 offset:1024\n\t"
"v_mfma_f32_16x16x32_bf16 a[100:103], v[96:99], a[204:207], a[100:103]\n\t"
"ds_read_b64_tr_b16 v[238:239], v240 offset:2560\n\t"
"v_mfma_f32_16x16x32_bf16 a[108:111], a[216:219], a[168:171], a[108:111]\n\t"
"buffer_load_dwordx4 v[96:99], v252, s[8:11], s20 offen offset:2048 sc1\n\t"
"v_mfma_f32_16x16x32_bf16 a[108:111], a[232:235], a[172:175], a[108:111]\n\t"
"s_mov_b32 s20, 1253376\n\t"
"ds_read_b64_tr_b16 v[224:225], v241 offset:1024\n\t"
"v_mfma_f32_16x16x32_bf16 a[108:111], a[220:223], a[176:179], a[108:111]\n\t"
"s_waitcnt lgkmcnt(3)\n\t"
"s_barrier\n\t"
"v_mfma_f32_16x16x32_bf16 a[116:119], a[216:219], a[172:175], a[116:119]\n\t"
"ds_read_b64_tr_b16 v[226:227], v241 offset:2560\n\t"
"v_mfma_f32_16x16x32_bf16 a[116:119], a[232:235], a[176:179], a[116:119]\n\t"
"buffer_load_dwordx4 a[212:215], v252, s[8:11], s20 offen offset:0 sc1\n\t"
"v_mfma_f32_16x16x32_bf16 a[116:119], a[220:223], a[180:183], a[116:119]\n\t"
"ds_read_b64_tr_b16 v[232:233], v240 offset:20992\n\t"
"v_mfma_f32_16x16x32_bf16 a[124:127], a[216:219], a[176:179], a[124:127]\n\t"
"ds_read_b64_tr_b16 v[234:235], v240 offset:22528\n\t"
"v_mfma_f32_16x16x32_bf16 a[124:127], a[232:235], a[180:183], a[124:127]\n\t"
"ds_read_b64_tr_b16 v[228:229], v241 offset:20992\n\t"
"v_mfma_f32_16x16x32_bf16 a[124:127], a[220:223], a[184:187], a[124:127]\n\t"
"ds_read_b64_tr_b16 v[230:231], v241 offset:22528\n\t"
"v_mfma_f32_16x16x32_bf16 a[132:135], a[216:219], a[180:183], a[132:135]\n\t"
"s_waitcnt lgkmcnt(6)\n\t"
"ds_read_b128 a[168:171], v244 offset:0\n\t"
"v_mfma_f32_16x16x32_bf16 a[132:135], a[232:235], a[184:187], a[132:135]\n\t"
"s_waitcnt lgkmcnt(5)\n\t"
"ds_read_b128 a[172:175], v244 offset:512\n\t"
"v_mfma_f32_16x16x32_bf16 a[132:135], a[220:223], a[188:191], a[132:135]\n\t"
"ds_read_b128 a[176:179], v244 offset:1024\n\t"
"v_mfma_f32_16x16x32_bf16 v[236:239], v[236:239], a[208:211], 0\n\t"
"ds_read_b128 a[180:183], v244 offset:1536\n\t"
"v_mfma_f32_16x16x32_bf16 v[224:227], v[224:227], a[208:211], 0\n\t"
"v_mfma_f32_16x16x32_bf16 a[140:143], a[216:219], a[184:187], a[140:143]\n\t"
"ds_read_b128 a[184:187], v244 offset:2048\n\t"
"v_mfma_f32_16x16x32_bf16 a[140:143], a[232:235], a[188:191], a[140:143]\n\t"
"v_mfma_f32_16x16x32_bf16 a[140:143], a[220:223], a[192:195], a[140:143]\n\t"
"v_mfma_f32_16x16x32_bf16 a[148:151], a[216:219], a[188:191], a[148:151]\n\t"
"s_waitcnt lgkmcnt(7)\n\t"
"ds_read_b128 a[188:191], v244 offset:2560\n\t"
"v_cvt_pk_bf16_f32 v236, v236, v237\n\t"
"v_mfma_f32_16x16x32_bf16 a[148:151], a[232:235], a[192:195], a[148:151]\n\t"
"s_waitcnt lgkmcnt(6)\n\t"
"v_cvt_pk_bf16_f32 v237, v238, v239\n\t"
"v_cvt_pk_bf16_f32 v238, v224, v225\n\t"
"v_mfma_f32_16x16x32_bf16 a[148:151], a[220:223], a[196:199], a[148:151]\n\t"
"v_cvt_pk_bf16_f32 v239, v226, v227\n\t"
"v_mfma_f32_16x16x32_bf16 v[232:235], v[232:235], a[208:211], 0\n\t"
"ds_write_b128 v243, v[236:239] offset:512\n\t"
"v_mfma_f32_16x16x32_bf16 v[228:231], v[228:231], a[208:211], 0\n\t"
"v_mfma_f32_16x16x32_bf16 a[156:159], a[216:219], a[192:195], a[156:159]\n\t"
"ds_read_b128 a[192:195], v244 offset:3072\n\t"
"v_mfma_f32_16x16x32_bf16 a[156:159], a[232:235], a[196:199], a[156:159]\n\t"
"ds_read_b64_tr_b16 v[224:225], v240 offset:40960\n\t"
"v_mfma_f32_16x16x32_bf16 a[156:159], a[220:223], a[200:203], a[156:159]\n\t"
"ds_read_b64_tr_b16 v[226:227], v240 offset:42496\n\t"
"v_mfma_f32_16x16x32_bf16 a[164:167], a[216:219], a[196:199], a[164:167]\n\t"
"buffer_load_dwordx4 a[216:219], v252, s[8:11], s20 offen offset:1024 sc1\n\t"
"v_mfma_f32_16x16x32_bf16 a[164:167], a[232:235], a[200:203], a[164:167]\n\t"
"s_waitcnt vmcnt(8) lgkmcnt(7)\n\t"
"ds_read_b64_tr_b16 v[236:237], v241 offset:40960\n\t"
"v_cvt_pk_bf16_f32 v232, v232, v233\n\t"
"v_mfma_f32_16x16x32_bf16 a[164:167], a[220:223], a[204:207], a[164:167]\n\t"
"ds_read_b64_tr_b16 v[238:239], v241 offset:42496\n\t"
"v_cvt_pk_bf16_f32 v233, v234, v235\n\t"
"Lst11_%=:\n\t"
"v_mfma_f32_16x16x32_bf16 v[0:3], a[236:239], a[168:171], v[0:3]\n\t"
"s_waitcnt lgkmcnt(8)\n\t"
"ds_read_b128 a[196:199], v244 offset:3584\n\t"
"v_cvt_pk_bf16_f32 v234, v228, v229\n\t"
"v_mfma_f32_16x16x32_bf16 v[0:3], a[224:227], a[172:175], v[0:3]\n\t"
"ds_read_b128 a[200:203], v244 offset:4096\n\t"
"v_cvt_pk_bf16_f32 v235, v230, v231\n\t"
"v_mfma_f32_16x16x32_bf16 v[0:3], a[240:243], a[176:179], v[0:3]\n\t"
"ds_write_b128 v243, v[232:235] offset:9728\n\t"
"v_mfma_f32_16x16x32_bf16 v[4:7], a[236:239], a[172:175], v[4:7]\n\t"
"buffer_load_dwordx4 a[220:223], v252, s[8:11], s20 offen offset:2048 sc1\n\t"
"v_mfma_f32_16x16x32_bf16 v[4:7], a[224:227], a[176:179], v[4:7]\n\t"
"s_waitcnt lgkmcnt(10)\n\t"
"ds_read_b128 a[204:207], v244 offset:4608\n\t"
"v_mfma_f32_16x16x32_bf16 v[4:7], a[240:243], a[180:183], v[4:7]\n\t"
"s_mov_b32 s20, 1265664\n\t"
"v_add_u32 v240, 59904, v240\n\t"
"v_add_u32 v241, 59904, v241\n\t"
"v_mfma_f32_16x16x32_bf16 v[8:11], a[236:239], a[176:179], v[8:11]\n\t"
"s_waitcnt lgkmcnt(6)\n\t"
"ds_read_b64_tr_b16 v[228:229], v240 offset:1024\n\t"
"v_mfma_f32_16x16x32_bf16 v[8:11], a[224:227], a[180:183], v[8:11]\n\t"
"buffer_load_dwordx4 a[232:235], v252, s[8:11], s20 offen offset:0 sc1\n\t"
"v_mfma_f32_16x16x32_bf16 v[8:11], a[240:243], a[184:187], v[8:11]\n\t"
"s_waitcnt lgkmcnt(5)\n\t"
"ds_read_b64_tr_b16 v[230:231], v240 offset:2560\n\t"
"v_mfma_f32_16x16x32_bf16 v[224:227], v[224:227], a[208:211], 0\n\t"
"ds_read_b64_tr_b16 v[232:233], v241 offset:1024\n\t"
"v_mfma_f32_16x16x32_bf16 v[236:239], v[236:239], a[208:211], 0\n\t"
"ds_read_b64_tr_b16 v[234:235], v241 offset:2560\n\t"
"v_mfma_f32_16x16x32_bf16 v[12:15], a[236:239], a[180:183], v[12:15]\n\t"
"v_mfma_f32_16x16x32_bf16 v[12:15], a[224:227], a[184:187], v[12:15]\n\t"
"v_mfma_f32_16x16x32_bf16 v[12:15], a[240:243], a[188:191], v[12:15]\n\t"
"v_mfma_f32_16x16x32_bf16 v[16:19], a[236:239], a[184:187], v[16:19]\n\t"
"s_waitcnt lgkmcnt(2)\n\t"
"v_cvt_pk_bf16_f32 v224, v224, v225\n\t"
"v_cvt_pk_bf16_f32 v225, v226, v227\n\t"
"v_mfma_f32_16x16x32_bf16 v[16:19], a[224:227], a[188:191], v[16:19]\n\t"
"s_waitcnt lgkmcnt(0)\n\t"
"v_cvt_pk_bf16_f32 v226, v236, v237\n\t"
"v_cvt_pk_bf16_f32 v227, v238, v239\n\t"
"v_mfma_f32_16x16x32_bf16 v[16:19], a[240:243], a[192:195], v[16:19]\n\t"
"ds_write_b128 v243, v[224:227] offset:18944\n\t"
"v_mfma_f32_16x16x32_bf16 v[228:231], v[228:231], a[208:211], 0\n\t"
"v_mfma_f32_16x16x32_bf16 v[232:235], v[232:235], a[208:211], 0\n\t"
"ds_read_b64_tr_b16 v[236:237], v240 offset:20992\n\t"
"v_mfma_f32_16x16x32_bf16 v[20:23], a[236:239], a[188:191], v[20:23]\n\t"
"ds_read_b64_tr_b16 v[238:239], v240 offset:22528\n\t"
"v_mfma_f32_16x16x32_bf16 v[20:23], a[224:227], a[192:195], v[20:23]\n\t"
"ds_read_b64_tr_b16 v[224:225], v241 offset:20992\n\t"
"v_mfma_f32_16x16x32_bf16 v[20:23], a[240:243], a[196:199], v[20:23]\n\t"
"ds_read_b64_tr_b16 v[226:227], v241 offset:22528\n\t"
"v_mfma_f32_16x16x32_bf16 v[24:27], a[236:239], a[192:195], v[24:27]\n\t"
"v_cvt_pk_bf16_f32 v228, v228, v229\n\t"
"v_cvt_pk_bf16_f32 v229, v230, v231\n\t"
"v_mfma_f32_16x16x32_bf16 v[24:27], a[224:227], a[196:199], v[24:27]\n\t"
"v_cvt_pk_bf16_f32 v230, v232, v233\n\t"
"v_cvt_pk_bf16_f32 v231, v234, v235\n\t"
"v_mfma_f32_16x16x32_bf16 v[24:27], a[240:243], a[200:203], v[24:27]\n\t"
"ds_write_b128 v243, v[228:231] offset:28160\n\t"
"v_mfma_f32_16x16x32_bf16 v[28:31], a[236:239], a[196:199], v[28:31]\n\t"
"buffer_load_dwordx4 a[236:239], v252, s[8:11], s20 offen offset:1024 sc1\n\t"
"v_mfma_f32_16x16x32_bf16 v[28:31], a[224:227], a[200:203], v[28:31]\n\t"
"s_waitcnt vmcnt(8)\n\t"
"ds_read_b64_tr_b16 v[232:233], v240 offset:40960\n\t"
"v_mfma_f32_16x16x32_bf16 v[28:31], a[240:243], a[204:207], v[28:31]\n\t"
"ds_read_b64_tr_b16 v[234:235], v240 offset:42496\n\t"
"v_mfma_f32_16x16x32_bf16 v[32:35], a[244:247], a[168:171], v[32:35]\n\t"
"buffer_load_dwordx4 a[224:227], v252, s[8:11], s20 offen offset:2048 sc1\n\t"
"v_mfma_f32_16x16x32_bf16 v[32:35], a[248:251], a[172:175], v[32:35]\n\t"
"s_mov_b32 s20, 1277952\n\t"
"ds_read_b64_tr_b16 v[228:229], v241 offset:40960\n\t"
"v_mfma_f32_16x16x32_bf16 v[32:35], v[248:251], a[176:179], v[32:35]\n\t"
"ds_read_b64_tr_b16 v[230:231], v241 offset:42496\n\t"
"v_mfma_f32_16x16x32_bf16 v[36:39], a[244:247], a[172:175], v[36:39]\n\t"
"buffer_load_dwordx4 a[240:243], v252, s[8:11], s20 offen offset:0 sc1\n\t"
"v_mfma_f32_16x16x32_bf16 v[36:39], a[248:251], a[176:179], v[36:39]\n\t"
"s_waitcnt lgkmcnt(5)\n\t"
"v_mfma_f32_16x16x32_bf16 v[36:39], v[248:251], a[180:183], v[36:39]\n\t"
"v_mfma_f32_16x16x32_bf16 v[236:239], v[236:239], a[208:211], 0\n\t"
"v_mfma_f32_16x16x32_bf16 v[224:227], v[224:227], a[208:211], 0\n\t"
"v_mfma_f32_16x16x32_bf16 v[40:43], a[244:247], a[176:179], v[40:43]\n\t"
"v_mfma_f32_16x16x32_bf16 v[40:43], a[248:251], a[180:183], v[40:43]\n\t"
"v_mfma_f32_16x16x32_bf16 v[40:43], v[248:251], a[184:187], v[40:43]\n\t"
"v_mfma_f32_16x16x32_bf16 v[44:47], a[244:247], a[180:183], v[44:47]\n\t"
"s_waitcnt lgkmcnt(2)\n\t"
"v_cvt_pk_bf16_f32 v236, v236, v237\n\t"
"v_cvt_pk_bf16_f32 v237, v238, v239\n\t"
"v_mfma_f32_16x16x32_bf16 v[44:47], a[248:251], a[184:187], v[44:47]\n\t"
"s_waitcnt lgkmcnt(0)\n\t"
"v_cvt_pk_bf16_f32 v238, v224, v225\n\t"
"v_cvt_pk_bf16_f32 v239, v226, v227\n\t"
"v_mfma_f32_16x16x32_bf16 v[44:47], v[248:251], a[188:191], v[44:47]\n\t"
"ds_write_b128 v243, v[236:239] offset:37376\n\t"
"v_mfma_f32_16x16x32_bf16 v[232:235], v[232:235], a[208:211], 0\n\t"
"v_mfma_f32_16x16x32_bf16 v[228:231], v[228:231], a[208:211], 0\n\t"
"v_mfma_f32_16x16x32_bf16 v[48:51], a[244:247], a[184:187], v[48:51]\n\t"
"buffer_load_dwordx4 a[208:211], v252, s[8:11], s20 offen offset:1024 sc1\n\t"
"v_mfma_f32_16x16x32_bf16 v[48:51], a[248:251], a[188:191], v[48:51]\n\t"
"v_mfma_f32_16x16x32_bf16 v[48:51], v[248:251], a[192:195], v[48:51]\n\t"
"v_mfma_f32_16x16x32_bf16 v[52:55], a[244:247], a[188:191], v[52:55]\n\t"
"v_cvt_pk_bf16_f32 v232, v232, v233\n\t"
"v_cvt_pk_bf16_f32 v233, v234, v235\n\t"
"v_mfma_f32_16x16x32_bf16 v[52:55], a[248:251], a[192:195], v[52:55]\n\t"
"v_cvt_pk_bf16_f32 v234, v228, v229\n\t"
"v_cvt_pk_bf16_f32 v235, v230, v231\n\t"
"v_mfma_f32_16x16x32_bf16 v[52:55], v[248:251], a[196:199], v[52:55]\n\t"
"ds_write_b128 v243, v[232:235] offset:46592\n\t"
"v_mfma_f32_16x16x32_bf16 v[56:59], a[244:247], a[192:195], v[56:59]\n\t"
"v_mfma_f32_16x16x32_bf16 v[56:59], a[248:251], a[196:199], v[56:59]\n\t"
"ds_read_b64_tr_b16 v[224:225], v246 offset:1024\n\t"
"v_mfma_f32_16x16x32_bf16 v[56:59], v[248:251], a[200:203], v[56:59]\n\t"
"ds_read_b64_tr_b16 v[226:227], v246 offset:4096\n\t"
"v_mfma_f32_16x16x32_bf16 v[60:63], a[244:247], a[196:199], v[60:63]\n\t"
"buffer_load_dwordx4 a[244:247], v252, s[8:11], s20 offen offset:2048 sc1\n\t"
"v_mfma_f32_16x16x32_bf16 v[60:63], a[248:251], a[200:203], v[60:63]\n\t"
"s_waitcnt vmcnt(9)\n\t"
"ds_read_b64_tr_b16 v[236:237], v246 offset:7168\n\t"
"s_mov_b32 s20, 1511424\n\t"
"v_mfma_f32_16x16x32_bf16 v[60:63], v[248:251], a[204:207], v[60:63]\n\t"
"ds_read_b64_tr_b16 v[238:239], v246 offset:10240\n\t"
"v_mfma_f32_16x16x32_bf16 v[64:67], a[252:255], a[168:171], v[64:67]\n\t"
"buffer_load_dwordx4 v[248:251], v252, s[8:11], s20 offen offset:0 sc1\n\t"
"v_mfma_f32_16x16x32_bf16 v[64:67], a[228:231], a[172:175], v[64:67]\n\t"
"ds_read_b64_tr_b16 v[228:229], v246 offset:13312\n\t"
"v_mfma_f32_16x16x32_bf16 v[64:67], v[96:99], a[176:179], v[64:67]\n\t"
"ds_read_b64_tr_b16 v[230:231], v246 offset:16384\n\t"
"v_mfma_f32_16x16x32_bf16 v[68:71], a[252:255], a[172:175], v[68:71]\n\t"
"buffer_load_dwordx4 v[240:243], v252, s[8:11], s20 offen offset:1024 sc1\n\t"
"v_mfma_f32_16x16x32_bf16 v[68:71], a[228:231], a[176:179], v[68:71]\n\t"
"ds_read_b128 a[168:171], v244 offset:9216\n\t"
"v_mfma_f32_16x16x32_bf16 v[68:71], v[96:99], a[180:183], v[68:71]\n\t"
"ds_read_b64_tr_b16 v[232:233], v246 offset:20992\n\t"
"v_mfma_f32_16x16x32_bf16 v[72:75], a[252:255], a[176:179], v[72:75]\n\t"
"buffer_load_dwordx4 a[248:251], v252, s[8:11], s20 offen offset:2048 sc1\n\t"
"v_mfma_f32_16x16x32_bf16 v[72:75], a[228:231], a[180:183], v[72:75]\n\t"
"s_waitcnt lgkmcnt(6)\n\t"
"ds_read_b64_tr_b16 v[234:235], v246 offset:24064\n\t"
"v_mfma_f32_16x16x32_bf16 v[72:75], v[96:99], a[184:187], v[72:75]\n\t"
"ds_read_b128 a[172:175], v244 offset:9728\n\t"
"v_mfma_f32_16x16x32_bf16 v[224:227], v[224:227], v[220:223], 0\n\t"
"ds_read_b128 a[176:179], v244 offset:10240\n\t"
"v_mfma_f32_16x16x32_bf16 v[76:79], a[252:255], a[180:183], v[76:79]\n\t"
"s_waitcnt lgkmcnt(7)\n\t"
"ds_read_b128 a[180:183], v244 offset:10752\n\t"
"v_mfma_f32_16x16x32_bf16 v[76:79], a[228:231], a[184:187], v[76:79]\n\t"
"v_mfma_f32_16x16x32_bf16 v[76:79], v[96:99], a[188:191], v[76:79]\n\t"
"v_mfma_f32_16x16x32_bf16 v[236:239], v[236:239], v[220:223], 0\n\t"
"v_mfma_f32_16x16x32_bf16 v[80:83], a[252:255], a[184:187], v[80:83]\n\t"
"s_waitcnt lgkmcnt(6)\n\t"
"ds_read_b128 a[184:187], v244 offset:11264\n\t"
"v_cvt_pk_bf16_f32 v224, v224, v225\n\t"
"v_mfma_f32_16x16x32_bf16 v[80:83], a[228:231], a[188:191], v[80:83]\n\t"
"v_cvt_pk_bf16_f32 v225, v226, v227\n\t"
"v_mfma_f32_16x16x32_bf16 v[80:83], v[96:99], a[192:195], v[80:83]\n\t"
"s_mov_b32 s20, 1523712\n\t"
"v_mfma_f32_16x16x32_bf16 v[228:231], v[228:231], v[220:223], 0\n\t"
"v_mfma_f32_16x16x32_bf16 v[84:87], a[252:255], a[188:191], v[84:87]\n\t"
"s_waitcnt lgkmcnt(4)\n\t"
"ds_read_b128 a[188:191], v244 offset:11776\n\t"
"v_cvt_pk_bf16_f32 v226, v236, v237\n\t"
"v_mfma_f32_16x16x32_bf16 v[84:87], a[228:231], a[192:195], v[84:87]\n\t"
"v_cvt_pk_bf16_f32 v227, v238, v239\n\t"
"v_mfma_f32_16x16x32_bf16 v[84:87], v[96:99], a[196:199], v[84:87]\n\t"
"v_mfma_f32_16x16x32_bf16 v[232:235], v[232:235], v[220:223], 0\n\t"
"v_mfma_f32_16x16x32_bf16 v[88:91], a[252:255], a[192:195], v[88:91]\n\t"
"buffer_load_dwordx4 v[236:239], v252, s[8:11], s20 offen offset:0 sc1\n\t"
"v_mfma_f32_16x16x32_bf16 v[88:91], a[228:231], a[196:199], v[88:91]\n\t"
"ds_read_b128 a[192:195], v244 offset:12288\n\t"
"v_cvt_pk_bf16_f32 v228, v228, v229\n\t"
"v_mfma_f32_16x16x32_bf16 v[88:91], v[96:99], a[200:203], v[88:91]\n\t"
"v_cvt_pk_bf16_f32 v229, v230, v231\n\t"
"v_mfma_f32_16x16x32_bf16 v[92:95], a[252:255], a[196:199], v[92:95]\n\t"
"buffer_load_dwordx4 a[252:255], v252, s[8:11], s20 offen offset:1024 sc1\n\t"
"v_mfma_f32_16x16x32_bf16 v[92:95], a[228:231], a[200:203], v[92:95]\n\t"
"s_waitcnt vmcnt(11) lgkmcnt(4)\n\t"
"ds_read_b128 a[196:199], v244 offset:12800\n\t"
"v_cvt_pk_bf16_f32 v230, v232, v233\n\t"
"v_mfma_f32_16x16x32_bf16 v[92:95], v[96:99], a[204:207], v[92:95]\n\t"
"ds_read_b128 a[200:203], v244 offset:13312\n\t"
"v_cvt_pk_bf16_f32 v231, v234, v235\n\t"
"v_mfma_f32_16x16x32_bf16 v[100:103], a[212:215], a[168:171], v[100:103]\n\t"
"ds_read_b64_tr_b16 v[232:233], v246 offset:27136\n\t"
"v_mfma_f32_16x16x32_bf16 v[100:103], a[216:219], a[172:175], v[100:103]\n\t"
"s_waitcnt lgkmcnt(6)\n\t"
"ds_read_b64_tr_b16 v[234:235], v246 offset:30208\n\t"
"v_mfma_f32_16x16x32_bf16 v[100:103], a[220:223], a[176:179], v[100:103]\n\t"
"s_waitcnt lgkmcnt(0)\n\t"
"ds_read_b128 a[204:207], v244 offset:13824\n\t"
"v_mfma_f32_16x16x32_bf16 v[232:235], v[232:235], v[220:223], 0\n\t"
"buffer_load_dwordx4 v[96:99], v252, s[8:11], s20 offen offset:2048 sc1\n\t"
"s_mov_b32 s20, 1536000\n\t"
"v_mfma_f32_16x16x32_bf16 v[104:107], a[212:215], a[172:175], v[104:107]\n\t"
"buffer_load_dwordx4 a[228:231], v252, s[8:11], s20 offen offset:0 sc1\n\t"
"v_mfma_f32_16x16x32_bf16 v[104:107], a[216:219], a[176:179], v[104:107]\n\t"
"v_mfma_f32_16x16x32_bf16 v[104:107], a[220:223], a[180:183], v[104:107]\n\t"
"s_waitcnt lgkmcnt(0)\n\t"
"s_barrier\n\t"
"v_mfma_f32_16x16x32_bf16 v[108:111], a[212:215], a[176:179], v[108:111]\n\t"
"v_mfma_f32_16x16x32_bf16 v[108:111], a[216:219], a[180:183], v[108:111]\n\t"
"v_cvt_pk_bf16_f32 v232, v232, v233\n\t"
"v_cvt_pk_bf16_f32 v233, v234, v235\n\t"
"v_mfma_f32_16x16x32_bf16 v[108:111], a[220:223], a[184:187], v[108:111]\n\t"
"ds_write2st64_b64 v245, v[224:225], v[226:227] offset0:0 offset1:1\n\t"
"v_mfma_f32_16x16x32_bf16 v[112:115], a[212:215], a[180:183], v[112:115]\n\t"
"v_mfma_f32_16x16x32_bf16 v[112:115], a[216:219], a[184:187], v[112:115]\n\t"
"ds_read_b64_tr_b16 v[224:225], v246 offset:33280\n\t"
"v_mfma_f32_16x16x32_bf16 v[112:115], a[220:223], a[188:191], v[112:115]\n\t"
"ds_write2st64_b64 v245, v[228:229], v[230:231] offset0:2 offset1:3\n\t"
"v_mfma_f32_16x16x32_bf16 v[116:119], a[212:215], a[184:187], v[116:119]\n\t"
"v_mfma_f32_16x16x32_bf16 v[116:119], a[216:219], a[188:191], v[116:119]\n\t"
"ds_read_b64_tr_b16 v[226:227], v246 offset:36352\n\t"
"v_mfma_f32_16x16x32_bf16 v[116:119], a[220:223], a[192:195], v[116:119]\n\t"
"ds_write_b64 v245, v[232:233] offset:2048\n\t"
"v_mfma_f32_16x16x32_bf16 v[120:123], a[212:215], a[188:191], v[120:123]\n\t"
"ds_read_b64_tr_b16 v[228:229], v246 offset:40960\n\t"
"v_mfma_f32_16x16x32_bf16 v[120:123], a[216:219], a[192:195], v[120:123]\n\t"
"ds_read_b64_tr_b16 v[230:231], v246 offset:44032\n\t"
"v_mfma_f32_16x16x32_bf16 v[120:123], a[220:223], a[196:199], v[120:123]\n\t"
"s_waitcnt lgkmcnt(3)\n\t"
"ds_read_b64_tr_b16 v[232:233], v246 offset:47104\n\t"
"v_mfma_f32_16x16x32_bf16 v[224:227], v[224:227], v[220:223], 0\n\t"
"ds_read_b64_tr_b16 v[234:235], v246 offset:50176\n\t"
"v_mfma_f32_16x16x32_bf16 v[124:127], a[212:215], a[192:195], v[124:127]\n\t"
"v_mfma_f32_16x16x32_bf16 v[124:127], a[216:219], a[196:199], v[124:127]\n\t"
"v_mfma_f32_16x16x32_bf16 v[124:127], a[220:223], a[200:203], v[124:127]\n\t"
"s_waitcnt lgkmcnt(2)\n\t"
"v_mfma_f32_16x16x32_bf16 v[228:231], v[228:231], v[220:223], 0\n\t"
"v_mfma_f32_16x16x32_bf16 v[128:131], a[212:215], a[196:199], v[128:131]\n\t"
"buffer_load_dwordx4 a[212:215], v252, s[8:11], s20 offen offset:1024 sc1\n\t"
"v_mfma_f32_16x16x32_bf16 v[128:131], a[216:219], a[200:203], v[128:131]\n\t"
"s_waitcnt vmcnt(11) lgkmcnt(0)\n\t"
"v_cvt_pk_bf16_f32 v224, v224, v225\n\t"
"v_cvt_pk_bf16_f32 v225, v226, v227\n\t"
"v_mfma_f32_16x16x32_bf16 v[128:131], a[220:223], a[204:207], v[128:131]\n\t"
"v_mfma_f32_16x16x32_bf16 v[232:235], v[232:235], v[220:223], 0\n\t"
"buffer_load_dwordx4 a[216:219], v252, s[8:11], s20 offen offset:2048 sc1\n\t"
"v_mfma_f32_16x16x32_bf16 v[132:135], a[232:235], a[168:171], v[132:135]\n\t"
"s_mov_b32 s20, 1548288\n\t"
"v_cvt_pk_bf16_f32 v226, v228, v229\n\t"
"v_cvt_pk_bf16_f32 v227, v230, v231\n\t"
"v_mfma_f32_16x16x32_bf16 v[132:135], a[236:239], a[172:175], v[132:135]\n\t"
"ds_read_b64_tr_b16 v[228:229], v246 offset:53248\n\t"
"v_mfma_f32_16x16x32_bf16 v[132:135], a[224:227], a[176:179], v[132:135]\n\t"
"buffer_load_dwordx4 a[220:223], v252, s[8:11], s20 offen offset:0 sc1\n\t"
"v_mfma_f32_16x16x32_bf16 v[136:139], a[232:235], a[172:175], v[136:139]\n\t"
"ds_read_b64_tr_b16 v[230:231], v246 offset:56320\n\t"
"v_mfma_f32_16x16x32_bf16 v[136:139], a[236:239], a[176:179], v[136:139]\n\t"
"v_cvt_pk_bf16_f32 v232, v232, v233\n\t"
"v_cvt_pk_bf16_f32 v233, v234, v235\n\t"
"v_mfma_f32_16x16x32_bf16 v[136:139], a[224:227], a[180:183], v[136:139]\n\t"
"ds_write2st64_b64 v245, v[224:225], v[226:227] offset0:5 offset1:6\n\t"
"v_mfma_f32_16x16x32_bf16 v[140:143], a[232:235], a[176:179], v[140:143]\n\t"
"v_mfma_f32_16x16x32_bf16 v[140:143], a[236:239], a[180:183], v[140:143]\n\t"
"v_mfma_f32_16x16x32_bf16 v[140:143], a[224:227], a[184:187], v[140:143]\n\t"
"v_mfma_f32_16x16x32_bf16 v[144:147], a[232:235], a[180:183], v[144:147]\n\t"
"v_mfma_f32_16x16x32_bf16 v[144:147], a[236:239], a[184:187], v[144:147]\n\t"
"v_mfma_f32_16x16x32_bf16 v[144:147], a[224:227], a[188:191], v[144:147]\n\t"
"v_mfma_f32_16x16x32_bf16 v[148:151], a[232:235], a[184:187], v[148:151]\n\t"
"s_waitcnt lgkmcnt(1)\n\t"
"v_mfma_f32_16x16x32_bf16 v[148:151], a[236:239], a[188:191], v[148:151]\n\t"
"v_mfma_f32_16x16x32_bf16 v[148:151], a[224:227], a[192:195], v[148:151]\n\t"
"v_mfma_f32_16x16x32_bf16 v[228:231], v[228:231], v[220:223], 0\n\t"
"v_mfma_f32_16x16x32_bf16 v[152:155], a[232:235], a[188:191], v[152:155]\n\t"
"v_mfma_f32_16x16x32_bf16 v[152:155], a[236:239], a[192:195], v[152:155]\n\t"
"v_mfma_f32_16x16x32_bf16 v[152:155], a[224:227], a[196:199], v[152:155]\n\t"
"v_mfma_f32_16x16x32_bf16 v[156:159], a[232:235], a[192:195], v[156:159]\n\t"
"v_mfma_f32_16x16x32_bf16 v[156:159], a[236:239], a[196:199], v[156:159]\n\t"
"v_cvt_pk_bf16_f32 v234, v228, v229\n\t"
"v_cvt_pk_bf16_f32 v235, v230, v231\n\t"
"v_mfma_f32_16x16x32_bf16 v[156:159], a[224:227], a[200:203], v[156:159]\n\t"
"ds_write2st64_b64 v245, v[232:233], v[234:235] offset0:7 offset1:8\n\t"
"v_mfma_f32_16x16x32_bf16 v[160:163], a[232:235], a[196:199], v[160:163]\n\t"
"buffer_load_dwordx4 a[232:235], v252, s[8:11], s20 offen offset:1024 sc1\n\t"
"v_mfma_f32_16x16x32_bf16 v[160:163], a[236:239], a[200:203], v[160:163]\n\t"
"s_waitcnt vmcnt(11)\n\t"
"v_mfma_f32_16x16x32_bf16 v[160:163], a[224:227], a[204:207], v[160:163]\n\t"
"v_mfma_f32_16x16x32_bf16 v[164:167], a[240:243], a[168:171], v[164:167]\n\t"
"buffer_load_dwordx4 a[224:227], v252, s[8:11], s20 offen offset:2048 sc1\n\t"
"v_mfma_f32_16x16x32_bf16 v[164:167], a[208:211], a[172:175], v[164:167]\n\t"
"s_mov_b32 s20, 1560576\n\t"
"v_mfma_f32_16x16x32_bf16 v[164:167], a[244:247], a[176:179], v[164:167]\n\t"
"s_waitcnt lgkmcnt(0)\n\t"
"s_barrier\n\t"
"v_mfma_f32_16x16x32_bf16 v[168:171], a[240:243], a[172:175], v[168:171]\n\t"
"ds_read_b128 a[168:171], v244 offset:0\n\t"
"v_mfma_f32_16x16x32_bf16 v[168:171], a[208:211], a[176:179], v[168:171]\n\t"
"buffer_load_dwordx4 a[236:239], v252, s[8:11], s20 offen offset:0 sc1\n\t"
"v_mfma_f32_16x16x32_bf16 v[168:171], a[244:247], a[180:183], v[168:171]\n\t"
"ds_read_b128 a[172:175], v244 offset:512\n\t"
"v_mfma_f32_16x16x32_bf16 v[172:175], a[240:243], a[176:179], v[172:175]\n\t"
"ds_read_b128 a[176:179], v244 offset:1024\n\t"
"v_mfma_f32_16x16x32_bf16 v[172:175], a[208:211], a[180:183], v[172:175]\n\t"
"v_mfma_f32_16x16x32_bf16 v[172:175], a[244:247], a[184:187], v[172:175]\n\t"
"v_mfma_f32_16x16x32_bf16 v[176:179], a[240:243], a[180:183], v[176:179]\n\t"
"ds_read_b128 a[180:183], v244 offset:1536\n\t"
"v_mfma_f32_16x16x32_bf16 v[176:179], a[208:211], a[184:187], v[176:179]\n\t"
"v_mfma_f32_16x16x32_bf16 v[176:179], a[244:247], a[188:191], v[176:179]\n\t"
"v_mfma_f32_16x16x32_bf16 v[180:183], a[240:243], a[184:187], v[180:183]\n\t"
"ds_read_b128 a[184:187], v244 offset:2048\n\t"
"v_mfma_f32_16x16x32_bf16 v[180:183], a[208:211], a[188:191], v[180:183]\n\t"
"v_mfma_f32_16x16x32_bf16 v[180:183], a[244:247], a[192:195], v[180:183]\n\t"
"v_mfma_f32_16x16x32_bf16 v[184:187], a[240:243], a[188:191], v[184:187]\n\t"
"ds_read_b128 a[188:191], v244 offset:2560\n\t"
"v_mfma_f32_16x16x32_bf16 v[184:187], a[208:211], a[192:195], v[184:187]\n\t"
"v_mfma_f32_16x16x32_bf16 v[184:187], a[244:247], a[196:199], v[184:187]\n\t"
"v_mfma_f32_16x16x32_bf16 v[188:191], a[240:243], a[192:195], v[188:191]\n\t"
"ds_read_b128 a[192:195], v244 offset:3072\n\t"
"v_mfma_f32_16x16x32_bf16 v[188:191], a[208:211], a[196:199], v[188:191]\n\t"
"v_mfma_f32_16x16x32_bf16 v[188:191], a[244:247], a[200:203], v[188:191]\n\t"
"v_mfma_f32_16x16x32_bf16 v[192:195], a[240:243], a[196:199], v[192:195]\n\t"
"buffer_load_dwordx4 a[240:243], v252, s[8:11], s20 offen offset:1024 sc1\n\t"
"v_mfma_f32_16x16x32_bf16 v[192:195], a[208:211], a[200:203], v[192:195]\n\t"
"s_waitcnt vmcnt(11) lgkmcnt(4)\n\t"
"ds_read_b128 a[196:199], v244 offset:3584\n\t"
"v_mfma_f32_16x16x32_bf16 v[192:195], a[244:247], a[204:207], v[192:195]\n\t"
"ds_read_b128 a[200:203], v244 offset:4096\n\t"
"Lst12_%=:\n\t"
"v_mfma_f32_16x16x32_bf16 v[0:3], v[248:251], a[168:171], v[0:3]\n\t"
"buffer_load_dwordx4 a[208:211], v252, s[8:11], s20 offen offset:2048 sc1\n\t"
"v_mfma_f32_16x16x32_bf16 v[0:3], v[240:243], a[172:175], v[0:3]\n\t"
"s_waitcnt lgkmcnt(5)\n\t"
"ds_read_b128 a[204:207], v244 offset:4608\n\t"
"v_xor_b32 v246, 0xc0, v246\n\t"
"v_mfma_f32_16x16x32_bf16 v[0:3], a[248:251], a[176:179], v[0:3]\n\t"
"s_mov_b32 s20, 1572864\n\t"
"v_mfma_f32_16x16x32_bf16 v[4:7], v[248:251], a[172:175], v[4:7]\n\t"
"buffer_load_dwordx4 a[244:247], v252, s[8:11], s20 offen offset:0 sc1\n\t"
"v_mfma_f32_16x16x32_bf16 v[4:7], v[240:243], a[176:179], v[4:7]\n\t"
"s_waitcnt lgkmcnt(5)\n\t"
"v_mfma_f32_16x16x32_bf16 v[4:7], a[248:251], a[180:183], v[4:7]\n\t"
"v_mfma_f32_16x16x32_bf16 v[8:11], v[248:251], a[176:179], v[8:11]\n\t"
"s_waitcnt lgkmcnt(4)\n\t"
"v_mfma_f32_16x16x32_bf16 v[8:11], v[240:243], a[180:183], v[8:11]\n\t"
"v_mfma_f32_16x16x32_bf16 v[8:11], a[248:251], a[184:187], v[8:11]\n\t"
"v_mfma_f32_16x16x32_bf16 v[12:15], v[248:251], a[180:183], v[12:15]\n\t"
"s_waitcnt lgkmcnt(3)\n\t"
"v_mfma_f32_16x16x32_bf16 v[12:15], v[240:243], a[184:187], v[12:15]\n\t"
"v_mfma_f32_16x16x32_bf16 v[12:15], a[248:251], a[188:191], v[12:15]\n\t"
"v_mfma_f32_16x16x32_bf16 v[16:19], v[248:251], a[184:187], v[16:19]\n\t"
"s_waitcnt lgkmcnt(2)\n\t"
"v_mfma_f32_16x16x32_bf16 v[16:19], v[240:243], a[188:191], v[16:19]\n\t"
"v_mfma_f32_16x16x32_bf16 v[16:19], a[248:251], a[192:195], v[16:19]\n\t"
"ds_read_b64_tr_b16 v[224:225], v246 offset:1024\n\t"
"v_mfma_f32_16x16x32_bf16 v[20:23], v[248:251], a[188:191], v[20:23]\n\t"
"s_waitcnt lgkmcnt(2)\n\t"
"ds_read_b64_tr_b16 v[226:227], v246 offset:4096\n\t"
"v_mfma_f32_16x16x32_bf16 v[20:23], v[240:243], a[192:195], v[20:23]\n\t"
"v_mfma_f32_16x16x32_bf16 v[20:23], a[248:251], a[196:199], v[20:23]\n\t"
"v_mfma_f32_16x16x32_bf16 v[24:27], v[248:251], a[192:195], v[24:27]\n\t"
"s_waitcnt lgkmcnt(2)\n\t"
"v_mfma_f32_16x16x32_bf16 v[24:27], v[240:243], a[196:199], v[24:27]\n\t"
"v_mfma_f32_16x16x32_bf16 v[24:27], a[248:251], a[200:203], v[24:27]\n\t"
"v_mfma_f32_16x16x32_bf16 v[28:31], v[248:251], a[196:199], v[28:31]\n\t"
"buffer_load_dwordx4 v[248:251], v252, s[8:11], s20 offen offset:1024 sc1\n\t"
"v_mfma_f32_16x16x32_bf16 v[28:31], v[240:243], a[200:203], v[28:31]\n\t"
"s_waitcnt vmcnt(11)\n\t"
"ds_read_b64_tr_b16 v[228:229], v246 offset:7168\n\t"
"v_mfma_f32_16x16x32_bf16 v[28:31], a[248:251], a[204:207], v[28:31]\n\t"
"s_add_u32 s21, s21, 6291456\n\t"
"v_add_u32 v254, 6291456, v254\n\t"
"ds_read_b64_tr_b16 v[230:231], v246 offset:10240\n\t"
"v_mfma_f32_16x16x32_bf16 v[32:35], v[236:239], a[168:171], v[32:35]\n\t"
"buffer_load_dwordx4 v[240:243], v252, s[8:11], s20 offen offset:2048 sc1\n\t"
"v_mfma_f32_16x16x32_bf16 v[32:35], a[252:255], a[172:175], v[32:35]\n\t"
"s_mov_b32 s20, 1585152\n\t"
"v_mfma_f32_16x16x32_bf16 v[32:35], v[96:99], a[176:179], v[32:35]\n\t"
"v_mfma_f32_16x16x32_bf16 v[36:39], v[236:239], a[172:175], v[36:39]\n\t"
"buffer_load_dwordx4 a[248:251], v252, s[8:11], s20 offen offset:0 sc1\n\t"
"v_mfma_f32_16x16x32_bf16 v[36:39], a[252:255], a[176:179], v[36:39]\n\t"
"s_waitcnt lgkmcnt(2)\n\t"
"v_mfma_f32_16x16x32_bf16 v[36:39], v[96:99], a[180:183], v[36:39]\n\t"
"v_mfma_f32_16x16x32_bf16 v[224:227], v[224:227], v[220:223], 0\n\t"
"v_mfma_f32_16x16x32_bf16 v[40:43], v[236:239], a[176:179], v[40:43]\n\t"
"ds_read_b64_tr_b16 v[232:233], v246 offset:13312\n\t"
"v_mfma_f32_16x16x32_bf16 v[40:43], a[252:255], a[180:183], v[40:43]\n\t"
"ds_read_b64_tr_b16 v[234:235], v246 offset:16384\n\t"
"v_mfma_f32_16x16x32_bf16 v[40:43], v[96:99], a[184:187], v[40:43]\n\t"
"v_mfma_f32_16x16x32_bf16 v[44:47], v[236:239], a[180:183], v[44:47]\n\t"
"s_waitcnt lgkmcnt(2)\n\t"
"v_mfma_f32_16x16x32_bf16 v[44:47], a[252:255], a[184:187], v[44:47]\n\t"
"v_cvt_pk_bf16_f32 v224, v224, v225\n\t"
"v_cvt_pk_bf16_f32 v225, v226, v227\n\t"
"v_mfma_f32_16x16x32_bf16 v[44:47], v[96:99], a[188:191], v[44:47]\n\t"
"v_mfma_f32_16x16x32_bf16 v[228:231], v[228:231], v[220:223], 0\n\t"
"v_mfma_f32_16x16x32_bf16 v[48:51], v[236:239], a[184:187], v[48:51]\n\t"
"v_mfma_f32_16x16x32_bf16 v[48:51], a[252:255], a[188:191], v[48:51]\n\t"
"v_mfma_f32_16x16x32_bf16 v[48:51], v[96:99], a[192:195], v[48:51]\n\t"
"v_mfma_f32_16x16x32_bf16 v[52:55], v[236:239], a[188:191], v[52:55]\n\t"
"v_mfma_f32_16x16x32_bf16 v[52:55], a[252:255], a[192:195], v[52:55]\n\t"
"v_cvt_pk_bf16_f32 v226, v228, v229\n\t"
"v_cvt_pk_bf16_f32 v227, v230, v231\n\t"
"v_mfma_f32_16x16x32_bf16 v[52:55], v[96:99], a[196:199], v[52:55]\n\t"
"ds_read_b64_tr_b16 v[228:229], v246 offset:20992\n\t"
"v_mfma_f32_16x16x32_bf16 v[56:59], v[236:239], a[192:195], v[56:59]\n\t"
"s_waitcnt lgkmcnt(1)\n\t"
"ds_read_b64_tr_b16 v[230:231], v246 offset:24064\n\t"
"v_mfma_f32_16x16x32_bf16 v[56:59], a[252:255], a[196:199], v[56:59]\n\t"
"v_mfma_f32_16x16x32_bf16 v[56:59], v[96:99], a[200:203], v[56:59]\n\t"
"v_mfma_f32_16x16x32_bf16 v[232:235], v[232:235], v[220:223], 0\n\t"
"v_mfma_f32_16x16x32_bf16 v[60:63], v[236:239], a[196:199], v[60:63]\n\t"
"buffer_load_dwordx4 v[236:239], v252, s[8:11], s20 offen offset:1024 sc1\n\t"
"v_mfma_f32_16x16x32_bf16 v[60:63], a[252:255], a[200:203], v[60:63]\n\t"
"s_waitcnt vmcnt(11)\n\t"
"v_mfma_f32_16x16x32_bf16 v[60:63], v[96:99], a[204:207], v[60:63]\n\t"
"v_mfma_f32_16x16x32_bf16 v[64:67], a[228:231], a[168:171], v[64:67]\n\t"
"buffer_load_dwordx4 v[96:99], v252, s[8:11], s20 offen offset:2048 sc1\n\t"
"v_mfma_f32_16x16x32_bf16 v[64:67], a[212:215], a[172:175], v[64:67]\n\t"
"s_mov_b32 s20, 1597440\n\t"
"ds_read_b128 a[168:171], v244 offset:9216\n\t"
"v_cvt_pk_bf16_f32 v232, v232, v233\n\t"
"v_mfma_f32_16x16x32_bf16 v[64:67], a[216:219], a[176:179], v[64:67]\n\t"
"v_cvt_pk_bf16_f32 v233, v234, v235\n\t"
"v_mfma_f32_16x16x32_bf16 v[68:71], a[228:231], a[172:175], v[68:71]\n\t"
"buffer_load_dwordx4 a[252:255], v252, s[8:11], s20 offen offset:0 sc1\n\t"
"v_mfma_f32_16x16x32_bf16 v[68:71], a[212:215], a[176:179], v[68:71]\n\t"
"s_waitcnt lgkmcnt(1)\n\t"
"ds_read_b128 a[172:175], v244 offset:9728\n\t"
"v_mfma_f32_16x16x32_bf16 v[68:71], a[216:219], a[180:183], v[68:71]\n\t"
"v_mfma_f32_16x16x32_bf16 v[228:231], v[228:231], v[220:223], 0\n\t"
"v_mfma_f32_16x16x32_bf16 v[72:75], a[228:231], a[176:179], v[72:75]\n\t"
"ds_read_b128 a[176:179], v244 offset:10240\n\t"
"v_mfma_f32_16x16x32_bf16 v[72:75], a[212:215], a[180:183], v[72:75]\n\t"
"v_mfma_f32_16x16x32_bf16 v[72:75], a[216:219], a[184:187], v[72:75]\n\t"
"v_mfma_f32_16x16x32_bf16 v[76:79], a[228:231], a[180:183], v[76:79]\n\t"
"ds_read_b128 a[180:183], v244 offset:10752\n\t"
"v_mfma_f32_16x16x32_bf16 v[76:79], a[212:215], a[184:187], v[76:79]\n\t"
"v_cvt_pk_bf16_f32 v234, v228, v229\n\t"
"v_cvt_pk_bf16_f32 v235, v230, v231\n\t"
"v_mfma_f32_16x16x32_bf16 v[76:79], a[216:219], a[188:191], v[76:79]\n\t"
"ds_read_b64_tr_b16 v[228:229], v246 offset:27136\n\t"
"v_mfma_f32_16x16x32_bf16 v[80:83], a[228:231], a[184:187], v[80:83]\n\t"
"ds_read_b64_tr_b16 v[230:231], v246 offset:30208\n\t"
"v_mfma_f32_16x16x32_bf16 v[80:83], a[212:215], a[188:191], v[80:83]\n\t"
"ds_read_b128 a[184:187], v244 offset:11264\n\t"
"v_mfma_f32_16x16x32_bf16 v[80:83], a[216:219], a[192:195], v[80:83]\n\t"
"v_mfma_f32_16x16x32_bf16 v[84:87], a[228:231], a[188:191], v[84:87]\n\t"
"ds_read_b128 a[188:191], v244 offset:11776\n\t"
"v_mfma_f32_16x16x32_bf16 v[84:87], a[212:215], a[192:195], v[84:87]\n\t"
"v_mfma_f32_16x16x32_bf16 v[84:87], a[216:219], a[196:199], v[84:87]\n\t"
"v_mfma_f32_16x16x32_bf16 v[88:91], a[228:231], a[192:195], v[88:91]\n\t"
"ds_read_b128 a[192:195], v244 offset:12288\n\t"
"v_mfma_f32_16x16x32_bf16 v[88:91], a[212:215], a[196:199], v[88:91]\n\t"
"v_mfma_f32_16x16x32_bf16 v[88:91], a[216:219], a[200:203], v[88:91]\n\t"
"v_mfma_f32_16x16x32_bf16 v[92:95], a[228:231], a[196:199], v[92:95]\n\t"
"buffer_load_dwordx4 a[228:231], v252, s[8:11], s20 offen offset:1024 sc1\n\t"
"v_mfma_f32_16x16x32_bf16 v[92:95], a[212:215], a[200:203], v[92:95]\n\t"
"s_waitcnt vmcnt(11) lgkmcnt(3)\n\t"
"ds_read_b128 a[196:199], v244 offset:12800\n\t"
"v_mfma_f32_16x16x32_bf16 v[92:95], a[216:219], a[204:207], v[92:95]\n\t"
"ds_read_b128 a[200:203], v244 offset:13312\n\t"
"v_mfma_f32_16x16x32_bf16 v[228:231], v[228:231], v[220:223], 0\n\t"
"ds_read_b128 a[204:207], v244 offset:13824\n\t"
"v_mfma_f32_16x16x32_bf16 v[100:103], a[220:223], a[168:171], v[100:103]\n\t"
"buffer_load_dwordx4 a[212:215], v252, s[8:11], s20 offen offset:2048 sc1\n\t"
"v_mfma_f32_16x16x32_bf16 v[100:103], a[232:235], a[172:175], v[100:103]\n\t"
"s_mov_b32 s20, 1609728\n\t"
"v_mfma_f32_16x16x32_bf16 v[100:103], a[224:227], a[176:179], v[100:103]\n\t"
"buffer_load_dwordx4 a[216:219], v252, s[8:11], s20 offen offset:0 sc1\n\t"
"v_mfma_f32_16x16x32_bf16 v[104:107], a[220:223], a[172:175], v[104:107]\n\t"
"s_waitcnt lgkmcnt(5)\n\t"
"s_add_u32 m0, s26, 512\n\t"
"v_mfma_f32_16x16x32_bf16 v[104:107], a[232:235], a[176:179], v[104:107]\n\t"
"v_cvt_pk_bf16_f32 v228, v228, v229\n\t"
"v_mfma_f32_16x16x32_bf16 v[104:107], a[224:227], a[180:183], v[104:107]\n\t"
"s_waitcnt lgkmcnt(0)\n\t"
"s_barrier\n\t"
"v_mfma_f32_16x16x32_bf16 v[108:111], a[220:223], a[176:179], v[108:111]\n\t"
"v_cvt_pk_bf16_f32 v229, v230, v231\n\t"
"v_mfma_f32_16x16x32_bf16 v[108:111], a[232:235], a[180:183], v[108:111]\n\t"
"v_fma_f32 v0, v100, s52, v0\n\t"
"v_fma_f32 v1, v101, s52, v1\n\t"
"v_mfma_f32_16x16x32_bf16 v[108:111], a[224:227], a[184:187], v[108:111]\n\t"
"s_mov_b64 exec, s[36:37]\n\t"
"buffer_load_dwordx4 v254, s[12:15], s27 offen offset:0 nt lds\n\t"
"s_mov_b64 exec, -1\n\t"
"s_add_u32 m0, s24, 80384\n\t"
"s_add_u32 s28, s21, 2359296\n\t"
"v_mfma_f32_16x16x32_bf16 v[112:115], a[220:223], a[180:183], v[112:115]\n\t"
"buffer_load_dwordx4 v247, s[12:15], s28 offen offset:0 nt lds\n\t"
"v_mfma_f32_16x16x32_bf16 v[112:115], a[232:235], a[184:187], v[112:115]\n\t"
"s_add_u32 m0, s24, 86528\n\t"
"s_add_u32 s28, s21, 2555904\n\t"
"v_fma_f32 v2, v102, s52, v2\n\t"
"v_mfma_f32_16x16x32_bf16 v[112:115], a[224:227], a[188:191], v[112:115]\n\t"
"ds_write2st64_b64 v245, v[224:225], v[226:227] offset0:0 offset1:1\n\t"
"v_mfma_f32_16x16x32_bf16 v[116:119], a[220:223], a[184:187], v[116:119]\n\t"
"buffer_load_dwordx4 v247, s[12:15], s28 offen offset:0 nt lds\n\t"
"v_mfma_f32_16x16x32_bf16 v[116:119], a[232:235], a[188:191], v[116:119]\n\t"
"s_add_u32 m0, s24, 60416\n\t"
"s_add_u32 s28, s21, 1769472\n\t"
"ds_read_b64_tr_b16 v[224:225], v246 offset:33280\n\t"
"v_mfma_f32_16x16x32_bf16 v[116:119], a[224:227], a[192:195], v[116:119]\n\t"
"ds_write2st64_b64 v245, v[232:233], v[234:235] offset0:2 offset1:3\n\t"
"ds_read_b64_tr_b16 v[226:227], v246 offset:36352\n\t"
"v_mfma_f32_16x16x32_bf16 v[120:123], a[220:223], a[188:191], v[120:123]\n\t"
"buffer_load_dwordx4 v247, s[12:15], s28 offen offset:0 nt lds\n\t"
"v_mfma_f32_16x16x32_bf16 v[120:123], a[232:235], a[192:195], v[120:123]\n\t"
"s_add_u32 m0, s24, 66560\n\t"
"s_add_u32 s28, s21, 1966080\n\t"
"ds_read_b64_tr_b16 v[232:233], v246 offset:40960\n\t"
"v_mfma_f32_16x16x32_bf16 v[120:123], a[224:227], a[196:199], v[120:123]\n\t"
"s_waitcnt lgkmcnt(1)\n\t"
"ds_write_b64 v245, v[228:229] offset:2048\n\t"
"ds_read_b64_tr_b16 v[234:235], v246 offset:44032\n\t"
"v_mfma_f32_16x16x32_bf16 v[224:227], v[224:227], v[220:223], 0\n\t"
"buffer_load_dwordx4 v247, s[12:15], s28 offen offset:0 nt lds\n\t"
"v_mfma_f32_16x16x32_bf16 v[124:127], a[220:223], a[192:195], v[124:127]\n\t"
"ds_read_b64_tr_b16 v[228:229], v246 offset:47104\n\t"
"v_fma_f32 v3, v103, s52, v3\n\t"
"v_mfma_f32_16x16x32_bf16 v[124:127], a[232:235], a[196:199], v[124:127]\n\t"
"ds_read_b64_tr_b16 v[230:231], v246 offset:50176\n\t"
"v_fma_f32 v100, v0, s54, -v100\n\t"
"v_mfma_f32_16x16x32_bf16 v[124:127], a[224:227], a[200:203], v[124:127]\n\t"
"s_waitcnt lgkmcnt(2)\n\t"
"v_fma_f32 v101, v1, s54, -v101\n\t"
"v_fma_f32 v102, v2, s54, -v102\n\t"
"v_mfma_f32_16x16x32_bf16 v[232:235], v[232:235], v[220:223], 0\n\t"
"v_fma_f32 v103, v3, s54, -v103\n\t"
"v_fma_f32 v4, v104, s52, v4\n\t"
"v_mfma_f32_16x16x32_bf16 v[128:131], a[220:223], a[196:199], v[128:131]\n\t"
"v_cvt_pk_bf16_f32 v224, v224, v225\n\t"
"v_cvt_pk_bf16_f32 v225, v226, v227\n\t"
"v_mfma_f32_16x16x32_bf16 v[128:131], a[232:235], a[200:203], v[128:131]\n\t"
"s_waitcnt vmcnt(15)\n\t"
"v_fma_f32 v5, v105, s52, v5\n\t"
"v_fma_f32 v6, v106, s52, v6\n\t"
"v_mfma_f32_16x16x32_bf16 v[128:131], a[224:227], a[204:207], v[128:131]\n\t"
"s_waitcnt lgkmcnt(0)\n\t"
"v_fma_f32 v7, v107, s52, v7\n\t"
"v_fma_f32 v104, v4, s54, -v104\n\t"
"v_mfma_f32_16x16x32_bf16 v[228:231], v[228:231], v[220:223], 0\n\t"
"v_fma_f32 v105, v5, s54, -v105\n\t"
"v_fma_f32 v106, v6, s54, -v106\n\t"
"v_mfma_f32_16x16x32_bf16 v[132:135], a[236:239], a[168:171], v[132:135]\n\t"
"v_cvt_pk_bf16_f32 v226, v232, v233\n\t"
"v_cvt_pk_bf16_f32 v227, v234, v235\n\t"
"v_mfma_f32_16x16x32_bf16 v[132:135], a[240:243], a[172:175], v[132:135]\n\t"
"ds_read_b64_tr_b16 v[232:233], v246 offset:53248\n\t"
"v_fma_f32 v107, v7, s54, -v107\n\t"
"v_mfma_f32_16x16x32_bf16 v[132:135], a[208:211], a[176:179], v[132:135]\n\t"
"ds_write2st64_b64 v245, v[224:225], v[226:227] offset0:5 offset1:6\n\t"
"v_mfma_f32_16x16x32_bf16 v[136:139], a[236:239], a[172:175], v[136:139]\n\t"
"v_fma_f32 v8, v108, s52, v8\n\t"
"v_fma_f32 v9, v109, s52, v9\n\t"
"v_mfma_f32_16x16x32_bf16 v[136:139], a[240:243], a[176:179], v[136:139]\n\t"
"ds_read_b64_tr_b16 v[234:235], v246 offset:56320\n\t"
"v_mfma_f32_16x16x32_bf16 v[136:139], a[208:211], a[180:183], v[136:139]\n\t"
"v_cvt_pk_bf16_f32 v228, v228, v229\n\t"
"v_cvt_pk_bf16_f32 v229, v230, v231\n\t"
"v_mfma_f32_16x16x32_bf16 v[140:143], a[236:239], a[176:179], v[140:143]\n\t"
"v_fma_f32 v10, v110, s52, v10\n\t"
"v_fma_f32 v11, v111, s52, v11\n\t"
"v_mfma_f32_16x16x32_bf16 v[140:143], a[240:243], a[180:183], v[140:143]\n\t"
"v_fma_f32 v108, v8, s54, -v108\n\t"
"v_fma_f32 v109, v9, s54, -v109\n\t"
"v_mfma_f32_16x16x32_bf16 v[140:143], a[208:211], a[184:187], v[140:143]\n\t"
"v_fma_f32 v110, v10, s54, -v110\n\t"
"v_fma_f32 v111, v11, s54, -v111\n\t"
"v_mfma_f32_16x16x32_bf16 v[144:147], a[236:239], a[180:183], v[144:147]\n\t"
"v_fma_f32 v12, v112, s52, v12\n\t"
"v_fma_f32 v13, v113, s52, v13\n\t"
"v_mfma_f32_16x16x32_bf16 v[144:147], a[240:243], a[184:187], v[144:147]\n\t"
"v_fma_f32 v14, v114, s52, v14\n\t"
"v_fma_f32 v15, v115, s52, v15\n\t"
"v_mfma_f32_16x16x32_bf16 v[144:147], a[208:211], a[188:191], v[144:147]\n\t"
"buffer_load_dwordx4 a[220:223], v252, s[8:11], s20 offen offset:1024 sc1\n\t"
"v_mfma_f32_16x16x32_bf16 v[148:151], a[236:239], a[184:187], v[148:151]\n\t"
"s_waitcnt lgkmcnt(0)\n\t"
"v_fma_f32 v112, v12, s54, -v112\n\t"
"v_fma_f32 v113, v13, s54, -v113\n\t"
"v_mfma_f32_16x16x32_bf16 v[148:151], a[240:243], a[188:191], v[148:151]\n\t"
"v_fma_f32 v114, v14, s54, -v114\n\t"
"v_fma_f32 v115, v15, s54, -v115\n\t"
"v_mfma_f32_16x16x32_bf16 v[148:151], a[208:211], a[192:195], v[148:151]\n\t"
"v_fma_f32 v16, v116, s52, v16\n\t"
"v_fma_f32 v17, v117, s52, v17\n\t"
"v_mfma_f32_16x16x32_bf16 v[232:235], v[232:235], v[220:223], 0\n\t"
"v_fma_f32 v18, v118, s52, v18\n\t"
"v_fma_f32 v19, v119, s52, v19\n\t"
"v_mfma_f32_16x16x32_bf16 v[152:155], a[236:239], a[188:191], v[152:155]\n\t"
"v_fma_f32 v116, v16, s54, -v116\n\t"
"v_fma_f32 v117, v17, s54, -v117\n\t"
"v_mfma_f32_16x16x32_bf16 v[152:155], a[240:243], a[192:195], v[152:155]\n\t"
"v_fma_f32 v118, v18, s54, -v118\n\t"
"v_fma_f32 v119, v19, s54, -v119\n\t"
"v_mfma_f32_16x16x32_bf16 v[152:155], a[208:211], a[196:199], v[152:155]\n\t"
"v_fma_f32 v20, v120, s52, v20\n\t"
"v_fma_f32 v21, v121, s52, v21\n\t"
"v_mfma_f32_16x16x32_bf16 v[156:159], a[236:239], a[192:195], v[156:159]\n\t"
"v_fma_f32 v22, v122, s52, v22\n\t"
"v_fma_f32 v23, v123, s52, v23\n\t"
"v_mfma_f32_16x16x32_bf16 v[156:159], a[240:243], a[196:199], v[156:159]\n\t"
"v_cvt_pk_bf16_f32 v230, v232, v233\n\t"
"v_cvt_pk_bf16_f32 v231, v234, v235\n\t"
"v_mfma_f32_16x16x32_bf16 v[156:159], a[208:211], a[200:203], v[156:159]\n\t"
"ds_write2st64_b64 v245, v[228:229], v[230:231] offset0:7 offset1:8\n\t"
"v_mfma_f32_16x16x32_bf16 v[160:163], a[236:239], a[196:199], v[160:163]\n\t"
"s_waitcnt vmcnt(13)\n\t"
"v_fma_f32 v120, v20, s54, -v120\n\t"
"v_fma_f32 v121, v21, s54, -v121\n\t"
"v_mfma_f32_16x16x32_bf16 v[160:163], a[240:243], a[200:203], v[160:163]\n\t"
"v_fma_f32 v122, v22, s54, -v122\n\t"
"v_fma_f32 v123, v23, s54, -v123\n\t"
"v_mfma_f32_16x16x32_bf16 v[160:163], a[208:211], a[204:207], v[160:163]\n\t"
"v_fma_f32 v24, v124, s52, v24\n\t"
"v_fma_f32 v25, v125, s52, v25\n\t"
"v_mfma_f32_16x16x32_bf16 v[164:167], a[244:247], a[168:171], v[164:167]\n\t"
"v_fma_f32 v26, v126, s52, v26\n\t"
"v_fma_f32 v27, v127, s52, v27\n\t"
"v_mfma_f32_16x16x32_bf16 v[164:167], v[248:251], a[172:175], v[164:167]\n\t"
"v_fma_f32 v124, v24, s54, -v124\n\t"
"v_mfma_f32_16x16x32_bf16 v[164:167], v[240:243], a[176:179], v[164:167]\n\t"
"s_waitcnt lgkmcnt(0)\n\t"
"s_barrier\n\t"
"v_mfma_f32_16x16x32_bf16 v[168:171], a[244:247], a[172:175], v[168:171]\n\t"
"ds_read_b128 a[168:171], v244 offset:0\n\t"
"v_mfma_f32_16x16x32_bf16 v[168:171], v[248:251], a[176:179], v[168:171]\n\t"
"ds_read_b128 a[172:175], v244 offset:512\n\t"
"v_fma_f32 v125, v25, s54, -v125\n\t"
"v_mfma_f32_16x16x32_bf16 v[168:171], v[240:243], a[180:183], v[168:171]\n\t"
"v_fma_f32 v126, v26, s54, -v126\n\t"
"v_fma_f32 v127, v27, s54, -v127\n\t"
"v_mfma_f32_16x16x32_bf16 v[172:175], a[244:247], a[176:179], v[172:175]\n\t"
"ds_read_b128 a[176:179], v244 offset:1024\n\t"
"v_fma_f32 v28, v128, s52, v28\n\t"
"v_mfma_f32_16x16x32_bf16 v[172:175], v[248:251], a[180:183], v[172:175]\n\t"
"v_fma_f32 v29, v129, s52, v29\n\t"
"v_fma_f32 v30, v130, s52, v30\n\t"
"v_mfma_f32_16x16x32_bf16 v[172:175], v[240:243], a[184:187], v[172:175]\n\t"
"v_fma_f32 v31, v131, s52, v31\n\t"
"v_fma_f32 v128, v28, s54, -v128\n\t"
"v_mfma_f32_16x16x32_bf16 v[176:179], a[244:247], a[180:183], v[176:179]\n\t"
"ds_read_b128 a[180:183], v244 offset:1536\n\t"
"v_fma_f32 v129, v29, s54, -v129\n\t"
"v_mfma_f32_16x16x32_bf16 v[176:179], v[248:251], a[184:187], v[176:179]\n\t"
"v_fma_f32 v130, v30, s54, -v130\n\t"
"v_fma_f32 v131, v31, s54, -v131\n\t"
"v_mfma_f32_16x16x32_bf16 v[176:179], v[240:243], a[188:191], v[176:179]\n\t"
"v_fma_f32 v32, v132, s52, v32\n\t"
"v_fma_f32 v33, v133, s52, v33\n\t"
"v_mfma_f32_16x16x32_bf16 v[180:183], a[244:247], a[184:187], v[180:183]\n\t"
"ds_read_b128 a[184:187], v244 offset:2048\n\t"
"v_fma_f32 v34, v134, s52, v34\n\t"
"v_mfma_f32_16x16x32_bf16 v[180:183], v[248:251], a[188:191], v[180:183]\n\t"
"v_fma_f32 v35, v135, s52, v35\n\t"
"v_fma_f32 v132, v32, s54, -v132\n\t"
"v_mfma_f32_16x16x32_bf16 v[180:183], v[240:243], a[192:195], v[180:183]\n\t"
"v_fma_f32 v133, v33, s54, -v133\n\t"
"v_fma_f32 v134, v34, s54, -v134\n\t"
"v_mfma_f32_16x16x32_bf16 v[184:187], a[244:247], a[188:191], v[184:187]\n\t"
"ds_read_b128 a[188:191], v244 offset:2560\n\t"
"v_fma_f32 v135, v35, s54, -v135\n\t"
"v_mfma_f32_16x16x32_bf16 v[184:187], v[248:251], a[192:195], v[184:187]\n\t"
"v_fma_f32 v36, v136, s52, v36\n\t"
"v_fma_f32 v37, v137, s52, v37\n\t"
"v_mfma_f32_16x16x32_bf16 v[184:187], v[240:243], a[196:199], v[184:187]\n\t"
"v_fma_f32 v38, v138, s52, v38\n\t"
"v_fma_f32 v39, v139, s52, v39\n\t"
"v_mfma_f32_16x16x32_bf16 v[188:191], a[244:247], a[192:195], v[188:191]\n\t"
"ds_read_b128 a[192:195], v244 offset:3072\n\t"
"v_fma_f32 v136, v36, s54, -v136\n\t"
"v_mfma_f32_16x16x32_bf16 v[188:191], v[248:251], a[196:199], v[188:191]\n\t"
"v_fma_f32 v137, v37, s54, -v137\n\t"
"v_fma_f32 v138, v38, s54, -v138\n\t"
"v_mfma_f32_16x16x32_bf16 v[188:191], v[240:243], a[200:203], v[188:191]\n\t"
"buffer_load_dwordx4 a[208:211], v252, s[8:11], s20 offen offset:2048 sc1\n\t"
"s_mov_b32 s20, 1622016\n\t"
"v_mfma_f32_16x16x32_bf16 v[192:195], a[244:247], a[196:199], v[192:195]\n\t"
"buffer_load_dwordx4 a[224:227], v252, s[8:11], s20 offen offset:0 sc1\n\t"
"v_mfma_f32_16x16x32_bf16 v[192:195], v[248:251], a[200:203], v[192:195]\n\t"
"s_waitcnt vmcnt(12) lgkmcnt(4)\n\t"
"ds_read_b128 a[196:199], v244 offset:3584\n\t"
"v_fma_f32 v139, v39, s54, -v139\n\t"
"v_mfma_f32_16x16x32_bf16 v[192:195], v[240:243], a[204:207], v[192:195]\n\t"
"ds_read_b128 a[200:203], v244 offset:4096\n\t"
"v_fma_f32 v40, v140, s52, v40\n\t"
"Lst13_%=:\n\t"
"v_mfma_f32_16x16x32_bf16 v[196:199], a[248:251], a[168:171], v[196:199]\n\t"
"s_waitcnt lgkmcnt(5)\n\t"
"ds_read_b128 a[204:207], v244 offset:4608\n\t"
"v_fma_f32 v41, v141, s52, v41\n\t"
"v_mfma_f32_16x16x32_bf16 v[196:199], v[236:239], a[172:175], v[196:199]\n\t"
"v_fma_f32 v42, v142, s52, v42\n\t"
"v_fma_f32 v43, v143, s52, v43\n\t"
"v_mfma_f32_16x16x32_bf16 v[196:199], v[96:99], a[176:179], v[196:199]\n\t"
"buffer_load_dwordx4 v[248:251], v252, s[8:11], s20 offen offset:1024 sc1\n\t"
"v_mfma_f32_16x16x32_bf16 v[204:207], a[248:251], a[172:175], v[204:207]\n\t"
"buffer_load_dwordx4 v[240:243], v252, s[8:11], s20 offen offset:2048 sc1\n\t"
"v_mfma_f32_16x16x32_bf16 v[204:207], v[236:239], a[176:179], v[204:207]\n\t"
"s_waitcnt lgkmcnt(5)\n\t"
"s_mov_b32 s20, 1634304\n\t"
"v_fma_f32 v140, v40, s54, -v140\n\t"
"v_mfma_f32_16x16x32_bf16 v[204:207], v[96:99], a[180:183], v[204:207]\n\t"
"v_fma_f32 v141, v41, s54, -v141\n\t"
"v_fma_f32 v142, v42, s54, -v142\n\t"
"v_mfma_f32_16x16x32_bf16 v[212:215], a[248:251], a[176:179], v[212:215]\n\t"
"s_waitcnt lgkmcnt(4)\n\t"
"v_fma_f32 v143, v43, s54, -v143\n\t"
"v_fma_f32 v44, v144, s52, v44\n\t"
"v_mfma_f32_16x16x32_bf16 v[212:215], v[236:239], a[180:183], v[212:215]\n\t"
"v_fma_f32 v45, v145, s52, v45\n\t"
"v_fma_f32 v46, v146, s52, v46\n\t"
"v_mfma_f32_16x16x32_bf16 v[212:215], v[96:99], a[184:187], v[212:215]\n\t"
"v_fma_f32 v47, v147, s52, v47\n\t"
"v_fma_f32 v144, v44, s54, -v144\n\t"
"v_mfma_f32_16x16x32_bf16 a[0:3], a[248:251], a[180:183], a[0:3]\n\t"
"s_waitcnt lgkmcnt(3)\n\t"
"v_fma_f32 v145, v45, s54, -v145\n\t"
"v_fma_f32 v146, v46, s54, -v146\n\t"
"v_mfma_f32_16x16x32_bf16 a[0:3], v[236:239], a[184:187], a[0:3]\n\t"
"v_fma_f32 v147, v47, s54, -v147\n\t"
"v_fma_f32 v48, v148, s52, v48\n\t"
"v_mfma_f32_16x16x32_bf16 a[0:3], v[96:99], a[188:191], a[0:3]\n\t"
"buffer_load_dwordx4 a[232:235], v252, s[8:11], s20 offen offset:0 sc1\n\t"
"v_mfma_f32_16x16x32_bf16 a[8:11], a[248:251], a[184:187], a[8:11]\n\t"
"buffer_load_dwordx4 a[236:239], v252, s[8:11], s20 offen offset:1024 sc1\n\t"
"v_mfma_f32_16x16x32_bf16 a[8:11], v[236:239], a[188:191], a[8:11]\n\t"
"s_waitcnt lgkmcnt(2)\n\t"
"s_add_u32 m0, s24, 72704\n\t"
"s_add_u32 s28, s21, 2162688\n\t"
"v_mfma_f32_16x16x32_bf16 a[8:11], v[96:99], a[192:195], a[8:11]\n\t"
"buffer_load_dwordx4 a[240:243], v252, s[8:11], s20 offen offset:2048 sc1\n\t"
"v_mfma_f32_16x16x32_bf16 a[16:19], a[248:251], a[188:191], a[16:19]\n\t"
"buffer_load_dwordx4 v247, s[12:15], s28 offen offset:0 nt lds\n\t"
"v_mfma_f32_16x16x32_bf16 a[16:19], v[236:239], a[192:195], a[16:19]\n\t"
"s_waitcnt lgkmcnt(1)\n\t"
"s_add_u32 m0, s24, 92672\n\t"
"s_add_u32 s28, s21, 2752512\n\t"
"v_mfma_f32_16x16x32_bf16 a[16:19], v[96:99], a[196:199], a[16:19]\n\t"
"buffer_load_dwordx4 v247, s[12:15], s28 offen offset:0 nt lds\n\t"
"v_mfma_f32_16x16x32_bf16 a[24:27], a[248:251], a[192:195], a[24:27]\n\t"
"s_waitcnt lgkmcnt(0)\n\t"
"s_add_u32 m0, s24, 100352\n\t"
"s_add_u32 s28, s21, 2949120\n\t"
"v_mfma_f32_16x16x32_bf16 a[24:27], v[236:239], a[196:199], a[24:27]\n\t"
"buffer_load_dwordx4 v247, s[12:15], s28 offen offset:0 nt lds\n\t"
"v_mfma_f32_16x16x32_bf16 a[24:27], v[96:99], a[200:203], a[24:27]\n\t"
"s_add_u32 m0, s24, 106496\n\t"
"s_add_u32 s28, s21, 3145728\n\t"
"v_fma_f32 v49, v149, s52, v49\n\t"
"v_mfma_f32_16x16x32_bf16 a[32:35], a[248:251], a[196:199], a[32:35]\n\t"
"s_waitcnt vmcnt(17)\n\t"
"v_fma_f32 v50, v150, s52, v50\n\t"
"v_fma_f32 v51, v151, s52, v51\n\t"
"v_mfma_f32_16x16x32_bf16 a[32:35], v[236:239], a[200:203], a[32:35]\n\t"
"v_fma_f32 v148, v48, s54, -v148\n\t"
"v_fma_f32 v149, v49, s54, -v149\n\t"
"v_mfma_f32_16x16x32_bf16 a[32:35], v[96:99], a[204:207], a[32:35]\n\t"
"buffer_load_dwordx4 v247, s[12:15], s28 offen offset:0 nt lds\n\t"
"s_add_u32 m0, s24, 112640\n\t"
"s_add_u32 s28, s21, 3342336\n\t"
"v_mfma_f32_16x16x32_bf16 a[40:43], a[252:255], a[168:171], a[40:43]\n\t"
"buffer_load_dwordx4 v247, s[12:15], s28 offen offset:0 nt lds\n\t"
"v_mfma_f32_16x16x32_bf16 a[40:43], a[228:231], a[172:175], a[40:43]\n\t"
"v_fma_f32 v150, v50, s54, -v150\n\t"
"v_fma_f32 v151, v51, s54, -v151\n\t"
"v_mfma_f32_16x16x32_bf16 a[40:43], a[212:215], a[176:179], a[40:43]\n\t"
"v_fma_f32 v52, v152, s52, v52\n\t"
"v_fma_f32 v53, v153, s52, v53\n\t"
"v_mfma_f32_16x16x32_bf16 a[48:51], a[252:255], a[172:175], a[48:51]\n\t"
"v_fma_f32 v54, v154, s52, v54\n\t"
"v_fma_f32 v55, v155, s52, v55\n\t"
"v_mfma_f32_16x16x32_bf16 a[48:51], a[228:231], a[176:179], a[48:51]\n\t"
"v_fma_f32 v152, v52, s54, -v152\n\t"
"v_fma_f32 v153, v53, s54, -v153\n\t"
"v_mfma_f32_16x16x32_bf16 a[48:51], a[212:215], a[180:183], a[48:51]\n\t"
"v_fma_f32 v154, v54, s54, -v154\n\t"
"v_fma_f32 v155, v55, s54, -v155\n\t"
"v_mfma_f32_16x16x32_bf16 a[56:59], a[252:255], a[176:179], a[56:59]\n\t"
"v_fma_f32 v56, v156, s52, v56\n\t"
"v_fma_f32 v57, v157, s52, v57\n\t"
"v_mfma_f32_16x16x32_bf16 a[56:59], a[228:231], a[180:183], a[56:59]\n\t"
"v_fma_f32 v58, v158, s52, v58\n\t"
"v_fma_f32 v59, v159, s52, v59\n\t"
"v_mfma_f32_16x16x32_bf16 a[56:59], a[212:215], a[184:187], a[56:59]\n\t"
"v_fma_f32 v156, v56, s54, -v156\n\t"
"v_fma_f32 v157, v57, s54, -v157\n\t"
"v_mfma_f32_16x16x32_bf16 a[64:67], a[252:255], a[180:183], a[64:67]\n\t"
"v_fma_f32 v158, v58, s54, -v158\n\t"
"v_fma_f32 v159, v59, s54, -v159\n\t"
"v_mfma_f32_16x16x32_bf16 a[64:67], a[228:231], a[184:187], a[64:67]\n\t"
"v_fma_f32 v60, v160, s52, v60\n\t"
"v_fma_f32 v61, v161, s52, v61\n\t"
"v_mfma_f32_16x16x32_bf16 a[64:67], a[212:215], a[188:191], a[64:67]\n\t"
"v_fma_f32 v62, v162, s52, v62\n\t"
"v_fma_f32 v63, v163, s52, v63\n\t"
"v_mfma_f32_16x16x32_bf16 a[72:75], a[252:255], a[184:187], a[72:75]\n\t"
"v_fma_f32 v160, v60, s54, -v160\n\t"
"v_fma_f32 v161, v61, s54, -v161\n\t"
"v_mfma_f32_16x16x32_bf16 a[72:75], a[228:231], a[188:191], a[72:75]\n\t"
"v_fma_f32 v162, v62, s54, -v162\n\t"
"v_fma_f32 v163, v63, s54, -v163\n\t"
"v_mfma_f32_16x16x32_bf16 a[72:75], a[212:215], a[192:195], a[72:75]\n\t"
"v_fma_f32 v64, v164, s52, v64\n\t"
"v_fma_f32 v65, v165, s52, v65\n\t"
"v_mfma_f32_16x16x32_bf16 a[80:83], a[252:255], a[188:191], a[80:83]\n\t"
"v_fma_f32 v66, v166, s52, v66\n\t"
"v_fma_f32 v67, v167, s52, v67\n\t"
"v_mfma_f32_16x16x32_bf16 a[80:83], a[228:231], a[192:195], a[80:83]\n\t"
"v_fma_f32 v164, v64, s54, -v164\n\t"
"v_fma_f32 v165, v65, s54, -v165\n\t"
"v_mfma_f32_16x16x32_bf16 a[80:83], a[212:215], a[196:199], a[80:83]\n\t"
"v_fma_f32 v166, v66, s54, -v166\n\t"
"v_fma_f32 v167, v67, s54, -v167\n\t"
"v_mfma_f32_16x16x32_bf16 a[88:91], a[252:255], a[192:195], a[88:91]\n\t"
"v_fma_f32 v68, v168, s52, v68\n\t"
"v_fma_f32 v69, v169, s52, v69\n\t"
"v_mfma_f32_16x16x32_bf16 a[88:91], a[228:231], a[196:199], a[88:91]\n\t"
"v_fma_f32 v70, v170, s52, v70\n\t"
"v_fma_f32 v71, v171, s52, v71\n\t"
"v_mfma_f32_16x16x32_bf16 a[88:91], a[212:215], a[200:203], a[88:91]\n\t"
"v_fma_f32 v168, v68, s54, -v168\n\t"
"v_fma_f32 v169, v69, s54, -v169\n\t"
"v_mfma_f32_16x16x32_bf16 a[96:99], a[252:255], a[196:199], a[96:99]\n\t"
"s_waitcnt vmcnt(11)\n\t"
"v_fma_f32 v170, v70, s54, -v170\n\t"
"v_fma_f32 v171, v71, s54, -v171\n\t"
"v_mfma_f32_16x16x32_bf16 a[96:99], a[228:231], a[200:203], a[96:99]\n\t"
"v_fma_f32 v72, v172, s52, v72\n\t"
"v_fma_f32 v73, v173, s52, v73\n\t"
"v_mfma_f32_16x16x32_bf16 a[96:99], a[212:215], a[204:207], a[96:99]\n\t"
"v_fma_f32 v74, v174, s52, v74\n\t"
"v_fma_f32 v75, v175, s52, v75\n\t"
"v_mfma_f32_16x16x32_bf16 a[104:107], a[216:219], a[168:171], a[104:107]\n\t"
"ds_read_b128 a[168:171], v244 offset:9216\n\t"
"v_fma_f32 v172, v72, s54, -v172\n\t"
"v_mfma_f32_16x16x32_bf16 a[104:107], a[220:223], a[172:175], a[104:107]\n\t"
"v_fma_f32 v173, v73, s54, -v173\n\t"
"v_fma_f32 v174, v74, s54, -v174\n\t"
"v_mfma_f32_16x16x32_bf16 a[104:107], a[208:211], a[176:179], a[104:107]\n\t"
"v_fma_f32 v175, v75, s54, -v175\n\t"
"v_fma_f32 v76, v176, s52, v76\n\t"
"v_mfma_f32_16x16x32_bf16 a[112:115], a[216:219], a[172:175], a[112:115]\n\t"
"ds_read_b128 a[172:175], v244 offset:9728\n\t"
"v_fma_f32 v77, v177, s52, v77\n\t"
"v_mfma_f32_16x16x32_bf16 a[112:115], a[220:223], a[176:179], a[112:115]\n\t"
"v_fma_f32 v78, v178, s52, v78\n\t"
"v_fma_f32 v79, v179, s52, v79\n\t"
"v_mfma_f32_16x16x32_bf16 a[112:115], a[208:211], a[180:183], a[112:115]\n\t"
"v_and_b32 v255, 4096, v244\n\t"
"v_mad_u32_u24 v255, v255, 5, v244\n\t"
"v_mfma_f32_16x16x32_bf16 a[120:123], a[216:219], a[176:179], a[120:123]\n\t"
"ds_read_b128 a[176:179], v244 offset:10240\n\t"
"v_add_u32 v255, 43008, v255\n\t"
"v_mfma_f32_16x16x32_bf16 a[120:123], a[220:223], a[180:183], a[120:123]\n\t"
"v_fma_f32 v176, v76, s54, -v176\n\t"
"v_fma_f32 v177, v77, s54, -v177\n\t"
"v_mfma_f32_16x16x32_bf16 a[120:123], a[208:211], a[184:187], a[120:123]\n\t"
"v_fma_f32 v178, v78, s54, -v178\n\t"
"v_fma_f32 v179, v79, s54, -v179\n\t"
"v_mfma_f32_16x16x32_bf16 a[128:131], a[216:219], a[180:183], a[128:131]\n\t"
"ds_read_b128 a[180:183], v244 offset:10752\n\t"
"v_fma_f32 v80, v180, s52, v80\n\t"
"v_mfma_f32_16x16x32_bf16 a[128:131], a[220:223], a[184:187], a[128:131]\n\t"
"v_fma_f32 v81, v181, s52, v81\n\t"
"v_fma_f32 v82, v182, s52, v82\n\t"
"v_mfma_f32_16x16x32_bf16 a[128:131], a[208:211], a[188:191], a[128:131]\n\t"
"v_fma_f32 v83, v183, s52, v83\n\t"
"v_fma_f32 v180, v80, s54, -v180\n\t"
"v_mfma_f32_16x16x32_bf16 a[136:139], a[216:219], a[184:187], a[136:139]\n\t"
"ds_read_b128 a[184:187], v244 offset:11264\n\t"
"v_fma_f32 v181, v81, s54, -v181\n\t"
"v_mfma_f32_16x16x32_bf16 a[136:139], a[220:223], a[188:191], a[136:139]\n\t"
"v_fma_f32 v182, v82, s54, -v182\n\t"
"v_fma_f32 v183, v83, s54, -v183\n\t"
"v_mfma_f32_16x16x32_bf16 a[136:139], a[208:211], a[192:195], a[136:139]\n\t"
"v_fma_f32 v84, v184, s52, v84\n\t"
"v_fma_f32 v85, v185, s52, v85\n\t"
"v_mfma_f32_16x16x32_bf16 a[144:147], a[216:219], a[188:191], a[144:147]\n\t"
"ds_read_b128 a[188:191], v244 offset:11776\n\t"
"v_fma_f32 v86, v186, s52, v86\n\t"
"v_mfma_f32_16x16x32_bf16 a[144:147], a[220:223], a[192:195], a[144:147]\n\t"
"v_fma_f32 v87, v187, s52, v87\n\t"
"v_fma_f32 v184, v84, s54, -v184\n\t"
"v_mfma_f32_16x16x32_bf16 a[144:147], a[208:211], a[196:199], a[144:147]\n\t"
"v_fma_f32 v185, v85, s54, -v185\n\t"
"v_fma_f32 v186, v86, s54, -v186\n\t"
"v_mfma_f32_16x16x32_bf16 a[152:155], a[216:219], a[192:195], a[152:155]\n\t"
"s_mov_b32 s20, 1646592\n\t"
"ds_read_b128 a[192:195], v244 offset:12288\n\t"
"v_fma_f32 v187, v87, s54, -v187\n\t"
"v_mfma_f32_16x16x32_bf16 a[152:155], a[220:223], a[196:199], a[152:155]\n\t"
"v_fma_f32 v88, v188, s52, v88\n\t"
"v_fma_f32 v89, v189, s52, v89\n\t"
"v_mfma_f32_16x16x32_bf16 a[152:155], a[208:211], a[200:203], a[152:155]\n\t"
"v_fma_f32 v90, v190, s52, v90\n\t"
"v_fma_f32 v91, v191, s52, v91\n\t"
"v_mfma_f32_16x16x32_bf16 a[160:163], a[216:219], a[196:199], a[160:163]\n\t"
"s_waitcnt vmcnt(8) lgkmcnt(4)\n\t"
"ds_read_b128 a[196:199], v244 offset:12800\n\t"
"v_fma_f32 v188, v88, s54, -v188\n\t"
"v_mfma_f32_16x16x32_bf16 a[160:163], a[220:223], a[200:203], a[160:163]\n\t"
"ds_read_b128 a[200:203], v244 offset:13312\n\t"
"v_fma_f32 v189, v89, s54, -v189\n\t"
"v_mfma_f32_16x16x32_bf16 a[160:163], a[208:211], a[204:207], a[160:163]\n\t"
"ds_read_b128 a[204:207], v244 offset:13824\n\t"
"v_fma_f32 v190, v90, s54, -v190\n\t"
"v_fma_f32 v191, v91, s54, -v191\n\t"
"v_mfma_f32_16x16x32_bf16 v[200:203], a[224:227], a[168:171], v[200:203]\n\t"
"s_waitcnt lgkmcnt(6)\n\t"
"v_fma_f32 v92, v192, s52, v92\n\t"
"v_fma_f32 v93, v193, s52, v93\n\t"
"v_mfma_f32_16x16x32_bf16 v[200:203], v[248:251], a[172:175], v[200:203]\n\t"
"v_fma_f32 v94, v194, s52, v94\n\t"
"v_fma_f32 v95, v195, s52, v95\n\t"
"v_mfma_f32_16x16x32_bf16 v[200:203], v[240:243], a[176:179], v[200:203]\n\t"
"v_fma_f32 v192, v92, s54, -v192\n\t"
"v_fma_f32 v193, v93, s54, -v193\n\t"
"v_fma_f32 v194, v94, s54, -v194\n\t"
"v_fma_f32 v195, v95, s54, -v195\n\t"
"v_mfma_f32_16x16x32_bf16 v[208:211], a[224:227], a[172:175], v[208:211]\n\t"
"s_waitcnt lgkmcnt(5)\n\t"
"v_mfma_f32_16x16x32_bf16 v[208:211], v[248:251], a[176:179], v[208:211]\n\t"
"v_mfma_f32_16x16x32_bf16 v[208:211], v[240:243], a[180:183], v[208:211]\n\t"
"s_waitcnt lgkmcnt(0)\n\t"
"s_barrier\n\t"
"v_mfma_f32_16x16x32_bf16 v[216:219], a[224:227], a[176:179], v[216:219]\n\t"
"v_mfma_f32_16x16x32_bf16 v[216:219], v[248:251], a[180:183], v[216:219]\n\t"
"buffer_load_dwordx4 a[208:211], v252, s[8:11], s20 offen offset:0 sc1\n\t"
"v_mfma_f32_16x16x32_bf16 v[216:219], v[240:243], a[184:187], v[216:219]\n\t"
"buffer_load_dwordx4 a[212:215], v252, s[8:11], s20 offen offset:1024 sc1\n\t"
"v_pk_fma_f32 v[196:197], v[200:201], s[56:57], v[196:197]\n\t"
"v_pk_fma_f32 v[200:201], v[196:197], s[58:59], v[200:201] neg_lo:[0,0,1] neg_hi:[0,0,1]\n\t"
"v_pk_fma_f32 v[228:229], v[0:1], s[60:61], v[196:197]\n\t"
"v_pk_fma_f32 v[0:1], v[196:197], s[60:61], v[0:1]\n\t"
"v_pk_fma_f32 v[196:197], v[100:101], s[62:63], v[200:201]\n\t"
"v_pk_fma_f32 v[100:101], v[200:201], s[62:63], v[100:101]\n\t"
"v_pk_fma_f32 v[198:199], v[202:203], s[56:57], v[198:199]\n\t"
"v_pk_fma_f32 v[202:203], v[198:199], s[58:59], v[202:203] neg_lo:[0,0,1] neg_hi:[0,0,1]\n\t"
"v_pk_fma_f32 v[234:235], v[2:3], s[60:61], v[198:199]\n\t"
"v_pk_fma_f32 v[2:3], v[198:199], s[60:61], v[2:3]\n\t"
"v_pk_fma_f32 v[198:199], v[102:103], s[62:63], v[202:203]\n\t"
"v_pk_fma_f32 v[102:103], v[202:203], s[62:63], v[102:103]\n\t"
"v_mfma_f32_16x16x32_bf16 a[4:7], a[224:227], a[180:183], a[4:7]\n\t"
"v_cvt_pk_bf16_f32 v196, v196, v197\n\t"
"v_cvt_pk_bf16_f32 v200, v228, v229\n\t"
"v_mfma_f32_16x16x32_bf16 a[4:7], v[248:251], a[184:187], a[4:7]\n\t"
"v_cvt_pk_bf16_f32 v197, v198, v199\n\t"
"v_cvt_pk_bf16_f32 v201, v234, v235\n\t"
"v_mfma_f32_16x16x32_bf16 a[4:7], v[240:243], a[188:191], a[4:7]\n\t"
"buffer_load_dwordx4 a[216:219], v252, s[8:11], s20 offen offset:2048 sc1\n\t"
"s_mov_b32 s20, 1474560\n\t"
"v_pk_fma_f32 v[204:205], v[208:209], s[56:57], v[204:205]\n\t"
"v_pk_fma_f32 v[208:209], v[204:205], s[58:59], v[208:209] neg_lo:[0,0,1] neg_hi:[0,0,1]\n\t"
"v_pk_fma_f32 v[98:99], v[4:5], s[60:61], v[204:205]\n\t"
"v_pk_fma_f32 v[4:5], v[204:205], s[60:61], v[4:5]\n\t"
"v_pk_fma_f32 v[204:205], v[104:105], s[62:63], v[208:209]\n\t"
"v_pk_fma_f32 v[104:105], v[208:209], s[62:63], v[104:105]\n\t"
"v_mfma_f32_16x16x32_bf16 a[12:15], a[224:227], a[184:187], a[12:15]\n\t"
"v_cvt_pk_bf16_f32 v204, v204, v205\n\t"
"v_cvt_pk_bf16_f32 v208, v98, v99\n\t"
"v_mfma_f32_16x16x32_bf16 a[12:15], v[248:251], a[188:191], a[12:15]\n\t"
"v_mfma_f32_16x16x32_bf16 a[12:15], v[240:243], a[192:195], a[12:15]\n\t"
"buffer_load_dwordx4 a[220:223], v252, s[8:11], s20 offen offset:0 sc1\n\t"
"v_pk_fma_f32 v[206:207], v[210:211], s[56:57], v[206:207]\n\t"
"v_pk_fma_f32 v[210:211], v[206:207], s[58:59], v[210:211] neg_lo:[0,0,1] neg_hi:[0,0,1]\n\t"
"v_pk_fma_f32 v[228:229], v[6:7], s[60:61], v[206:207]\n\t"
"v_pk_fma_f32 v[6:7], v[206:207], s[60:61], v[6:7]\n\t"
"v_pk_fma_f32 v[206:207], v[106:107], s[62:63], v[210:211]\n\t"
"v_pk_fma_f32 v[106:107], v[210:211], s[62:63], v[106:107]\n\t"
"v_mfma_f32_16x16x32_bf16 a[20:23], a[224:227], a[188:191], a[20:23]\n\t"
"v_cvt_pk_bf16_f32 v205, v206, v207\n\t"
"v_cvt_pk_bf16_f32 v209, v228, v229\n\t"
"v_mfma_f32_16x16x32_bf16 a[20:23], v[248:251], a[192:195], a[20:23]\n\t"
"v_accvgpr_read_b32 v224, a0\n\t"
"v_accvgpr_read_b32 v226, a4\n\t"
"v_mfma_f32_16x16x32_bf16 a[20:23], v[240:243], a[196:199], a[20:23]\n\t"
"buffer_load_dwordx4 a[228:231], v252, s[8:11], s20 offen offset:1024 sc1\n\t"
"v_accvgpr_read_b32 v225, a1\n\t"
"v_accvgpr_read_b32 v227, a5\n\t"
"v_pk_fma_f32 v[212:213], v[216:217], s[56:57], v[212:213]\n\t"
"v_pk_fma_f32 v[216:217], v[212:213], s[58:59], v[216:217] neg_lo:[0,0,1] neg_hi:[0,0,1]\n\t"
"v_pk_fma_f32 v[234:235], v[8:9], s[60:61], v[212:213]\n\t"
"v_pk_fma_f32 v[8:9], v[212:213], s[60:61], v[8:9]\n\t"
"v_pk_fma_f32 v[212:213], v[108:109], s[62:63], v[216:217]\n\t"
"v_pk_fma_f32 v[108:109], v[216:217], s[62:63], v[108:109]\n\t"
"v_mfma_f32_16x16x32_bf16 a[28:31], a[224:227], a[192:195], a[28:31]\n\t"
"v_cvt_pk_bf16_f32 v212, v212, v213\n\t"
"v_cvt_pk_bf16_f32 v216, v234, v235\n\t"
"v_mfma_f32_16x16x32_bf16 a[28:31], v[248:251], a[196:199], a[28:31]\n\t"
"v_accvgpr_read_b32 v230, a2\n\t"
"v_accvgpr_read_b32 v232, a6\n\t"
"v_mfma_f32_16x16x32_bf16 a[28:31], v[240:243], a[200:203], a[28:31]\n\t"
"buffer_load_dwordx4 a[244:247], v252, s[8:11], s20 offen offset:2048 sc1\n\t"
"v_accvgpr_read_b32 v231, a3\n\t"
"v_accvgpr_read_b32 v233, a7\n\t"
"v_pk_fma_f32 v[214:215], v[218:219], s[56:57], v[214:215]\n\t"
"v_pk_fma_f32 v[218:219], v[214:215], s[58:59], v[218:219] neg_lo:[0,0,1] neg_hi:[0,0,1]\n\t"
"v_pk_fma_f32 v[98:99], v[10:11], s[60:61], v[214:215]\n\t"
"v_pk_fma_f32 v[10:11], v[214:215], s[60:61], v[10:11]\n\t"
"v_pk_fma_f32 v[214:215], v[110:111], s[62:63], v[218:219]\n\t"
"v_pk_fma_f32 v[110:111], v[218:219], s[62:63], v[110:111]\n\t"
"v_mfma_f32_16x16x32_bf16 a[36:39], a[224:227], a[196:199], a[36:39]\n\t"
"s_waitcnt vmcnt(11)\n\t"
"s_mov_b32 s20, 1486848\n\t"
"v_cvt_pk_bf16_f32 v213, v214, v215\n\t"
"v_mfma_f32_16x16x32_bf16 a[36:39], v[248:251], a[200:203], a[36:39]\n\t"
"v_cvt_pk_bf16_f32 v217, v98, v99\n\t"
"v_accvgpr_read_b32 v236, a8\n\t"
"v_mfma_f32_16x16x32_bf16 a[36:39], v[240:243], a[204:207], a[36:39]\n\t"
"buffer_load_dwordx4 v[240:243], v252, s[8:11], s20 offen offset:0 sc1\n\t"
"v_accvgpr_read_b32 v238, a12\n\t"
"v_accvgpr_read_b32 v237, a9\n\t"
"v_pk_fma_f32 v[224:225], v[226:227], s[56:57], v[224:225]\n\t"
"v_pk_fma_f32 v[226:227], v[224:225], s[58:59], v[226:227] neg_lo:[0,0,1] neg_hi:[0,0,1]\n\t"
"v_pk_fma_f32 v[228:229], v[12:13], s[60:61], v[224:225]\n\t"
"v_pk_fma_f32 v[12:13], v[224:225], s[60:61], v[12:13]\n\t"
"v_pk_fma_f32 v[224:225], v[112:113], s[62:63], v[226:227]\n\t"
"v_pk_fma_f32 v[112:113], v[226:227], s[62:63], v[112:113]\n\t"
"v_mfma_f32_16x16x32_bf16 a[44:47], a[232:235], a[168:171], a[44:47]\n\t"
"v_cvt_pk_bf16_f32 v224, v224, v225\n\t"
"v_cvt_pk_bf16_f32 v228, v228, v229\n\t"
"v_mfma_f32_16x16x32_bf16 a[44:47], a[236:239], a[172:175], a[44:47]\n\t"
"v_accvgpr_write_b32 a0, v224\n\t"
"v_accvgpr_write_b32 a4, v228\n\t"
"v_mfma_f32_16x16x32_bf16 a[44:47], a[240:243], a[176:179], a[44:47]\n\t"
"buffer_load_dwordx4 a[224:227], v252, s[8:11], s20 offen offset:1024 sc1\n\t"
"v_accvgpr_read_b32 v239, a13\n\t"
"v_accvgpr_read_b32 v224, a10\n\t"
"v_pk_fma_f32 v[230:231], v[232:233], s[56:57], v[230:231]\n\t"
"v_pk_fma_f32 v[232:233], v[230:231], s[58:59], v[232:233] neg_lo:[0,0,1] neg_hi:[0,0,1]\n\t"
"v_pk_fma_f32 v[234:235], v[14:15], s[60:61], v[230:231]\n\t"
"v_pk_fma_f32 v[14:15], v[230:231], s[60:61], v[14:15]\n\t"
"v_pk_fma_f32 v[230:231], v[114:115], s[62:63], v[232:233]\n\t"
"v_pk_fma_f32 v[114:115], v[232:233], s[62:63], v[114:115]\n\t"
"v_mfma_f32_16x16x32_bf16 a[52:55], a[232:235], a[172:175], a[52:55]\n\t"
"v_cvt_pk_bf16_f32 v230, v230, v231\n\t"
"v_cvt_pk_bf16_f32 v234, v234, v235\n\t"
"v_mfma_f32_16x16x32_bf16 a[52:55], a[236:239], a[176:179], a[52:55]\n\t"
"v_accvgpr_write_b32 a1, v230\n\t"
"v_accvgpr_write_b32 a5, v234\n\t"
"v_mfma_f32_16x16x32_bf16 a[52:55], a[240:243], a[180:183], a[52:55]\n\t"
"buffer_load_dwordx4 a[248:251], v252, s[8:11], s20 offen offset:2048 sc1\n\t"
"v_accvgpr_read_b32 v226, a14\n\t"
"v_accvgpr_read_b32 v225, a11\n\t"
"v_pk_fma_f32 v[236:237], v[238:239], s[56:57], v[236:237]\n\t"
"v_pk_fma_f32 v[238:239], v[236:237], s[58:59], v[238:239] neg_lo:[0,0,1] neg_hi:[0,0,1]\n\t"
"v_pk_fma_f32 v[98:99], v[16:17], s[60:61], v[236:237]\n\t"
"v_pk_fma_f32 v[16:17], v[236:237], s[60:61], v[16:17]\n\t"
"v_pk_fma_f32 v[236:237], v[116:117], s[62:63], v[238:239]\n\t"
"v_pk_fma_f32 v[116:117], v[238:239], s[62:63], v[116:117]\n\t"
"v_mfma_f32_16x16x32_bf16 a[60:63], a[232:235], a[176:179], a[60:63]\n\t"
"s_mov_b32 s20, 1499136\n\t"
"v_accvgpr_read_b32 v227, a15\n\t"
"v_accvgpr_read_b32 v230, a16\n\t"
"v_mfma_f32_16x16x32_bf16 a[60:63], a[236:239], a[180:183], a[60:63]\n\t"
"v_accvgpr_read_b32 v232, a20\n\t"
"v_accvgpr_read_b32 v231, a17\n\t"
"v_mfma_f32_16x16x32_bf16 a[60:63], a[240:243], a[184:187], a[60:63]\n\t"
"buffer_load_dwordx4 a[252:255], v252, s[8:11], s20 offen offset:0 sc1\n\t"
"v_accvgpr_read_b32 v233, a21\n\t"
"v_cvt_pk_bf16_f32 v236, v236, v237\n\t"
"v_cvt_pk_bf16_f32 v98, v98, v99\n\t"
"v_pk_fma_f32 v[224:225], v[226:227], s[56:57], v[224:225]\n\t"
"v_pk_fma_f32 v[226:227], v[224:225], s[58:59], v[226:227] neg_lo:[0,0,1] neg_hi:[0,0,1]\n\t"
"v_pk_fma_f32 v[228:229], v[18:19], s[60:61], v[224:225]\n\t"
"v_pk_fma_f32 v[18:19], v[224:225], s[60:61], v[18:19]\n\t"
"v_pk_fma_f32 v[224:225], v[118:119], s[62:63], v[226:227]\n\t"
"v_pk_fma_f32 v[118:119], v[226:227], s[62:63], v[118:119]\n\t"
"v_pk_fma_f32 v[230:231], v[232:233], s[56:57], v[230:231]\n\t"
"v_pk_fma_f32 v[232:233], v[230:231], s[58:59], v[232:233] neg_lo:[0,0,1] neg_hi:[0,0,1]\n\t"
"v_pk_fma_f32 v[234:235], v[20:21], s[60:61], v[230:231]\n\t"
"v_pk_fma_f32 v[20:21], v[230:231], s[60:61], v[20:21]\n\t"
"v_pk_fma_f32 v[230:231], v[120:121], s[62:63], v[232:233]\n\t"
"v_pk_fma_f32 v[120:121], v[232:233], s[62:63], v[120:121]\n\t"
"v_mfma_f32_16x16x32_bf16 a[68:71], a[232:235], a[180:183], a[68:71]\n\t"
"v_accvgpr_write_b32 a8, v236\n\t"
"v_accvgpr_write_b32 a12, v98\n\t"
"v_mfma_f32_16x16x32_bf16 a[68:71], a[236:239], a[184:187], a[68:71]\n\t"
"v_accvgpr_read_b32 v236, a18\n\t"
"v_accvgpr_read_b32 v238, a22\n\t"
"v_mfma_f32_16x16x32_bf16 a[68:71], a[240:243], a[188:191], a[68:71]\n\t"
"v_accvgpr_read_b32 v237, a19\n\t"
"v_accvgpr_read_b32 v239, a23\n\t"
"v_cvt_pk_bf16_f32 v224, v224, v225\n\t"
"v_cvt_pk_bf16_f32 v228, v228, v229\n\t"
"v_cvt_pk_bf16_f32 v230, v230, v231\n\t"
"v_cvt_pk_bf16_f32 v234, v234, v235\n\t"
"v_pk_fma_f32 v[236:237], v[238:239], s[56:57], v[236:237]\n\t"
"v_pk_fma_f32 v[238:239], v[236:237], s[58:59], v[238:239] neg_lo:[0,0,1] neg_hi:[0,0,1]\n\t"
"v_pk_fma_f32 v[98:99], v[22:23], s[60:61], v[236:237]\n\t"
"v_pk_fma_f32 v[22:23], v[236:237], s[60:61], v[22:23]\n\t"
"v_pk_fma_f32 v[236:237], v[122:123], s[62:63], v[238:239]\n\t"
"v_pk_fma_f32 v[122:123], v[238:239], s[62:63], v[122:123]\n\t"
"v_mfma_f32_16x16x32_bf16 a[76:79], a[232:235], a[184:187], a[76:79]\n\t"
"v_accvgpr_write_b32 a9, v224\n\t"
"v_accvgpr_write_b32 a13, v228\n\t"
"v_mfma_f32_16x16x32_bf16 a[76:79], a[236:239], a[188:191], a[76:79]\n\t"
"v_accvgpr_write_b32 a16, v230\n\t"
"v_accvgpr_write_b32 a20, v234\n\t"
"v_mfma_f32_16x16x32_bf16 a[76:79], a[240:243], a[192:195], a[76:79]\n\t"
"v_accvgpr_read_b32 v224, a24\n\t"
"v_accvgpr_read_b32 v226, a28\n\t"
"v_accvgpr_read_b32 v225, a25\n\t"
"v_accvgpr_read_b32 v227, a29\n\t"
"v_cvt_pk_bf16_f32 v236, v236, v237\n\t"
"v_cvt_pk_bf16_f32 v98, v98, v99\n\t"
"v_pk_fma_f32 v[224:225], v[226:227], s[56:57], v[224:225]\n\t"
"v_pk_fma_f32 v[226:227], v[224:225], s[58:59], v[226:227] neg_lo:[0,0,1] neg_hi:[0,0,1]\n\t"
"v_pk_fma_f32 v[228:229], v[24:25], s[60:61], v[224:225]\n\t"
"v_pk_fma_f32 v[24:25], v[224:225], s[60:61], v[24:25]\n\t"
"v_pk_fma_f32 v[224:225], v[124:125], s[62:63], v[226:227]\n\t"
"v_pk_fma_f32 v[124:125], v[226:227], s[62:63], v[124:125]\n\t"
"v_mfma_f32_16x16x32_bf16 a[84:87], a[232:235], a[188:191], a[84:87]\n\t"
"v_accvgpr_write_b32 a17, v236\n\t"
"v_accvgpr_write_b32 a21, v98\n\t"
"v_mfma_f32_16x16x32_bf16 a[84:87], a[236:239], a[192:195], a[84:87]\n\t"
"v_accvgpr_read_b32 v230, a26\n\t"
"v_accvgpr_read_b32 v232, a30\n\t"
"v_mfma_f32_16x16x32_bf16 a[84:87], a[240:243], a[196:199], a[84:87]\n\t"
"v_accvgpr_read_b32 v231, a27\n\t"
"v_accvgpr_read_b32 v233, a31\n\t"
"v_cvt_pk_bf16_f32 v224, v224, v225\n\t"
"v_cvt_pk_bf16_f32 v228, v228, v229\n\t"
"v_accvgpr_write_b32 a24, v224\n\t"
"v_pk_fma_f32 v[230:231], v[232:233], s[56:57], v[230:231]\n\t"
"v_pk_fma_f32 v[232:233], v[230:231], s[58:59], v[232:233] neg_lo:[0,0,1] neg_hi:[0,0,1]\n\t"
"v_pk_fma_f32 v[234:235], v[26:27], s[60:61], v[230:231]\n\t"
"v_pk_fma_f32 v[26:27], v[230:231], s[60:61], v[26:27]\n\t"
"v_pk_fma_f32 v[230:231], v[126:127], s[62:63], v[232:233]\n\t"
"v_pk_fma_f32 v[126:127], v[232:233], s[62:63], v[126:127]\n\t"
"v_mfma_f32_16x16x32_bf16 a[92:95], a[232:235], a[192:195], a[92:95]\n\t"
"v_accvgpr_write_b32 a28, v228\n\t"
"v_accvgpr_read_b32 v236, a32\n\t"
"v_mfma_f32_16x16x32_bf16 a[92:95], a[236:239], a[196:199], a[92:95]\n\t"
"v_accvgpr_read_b32 v238, a36\n\t"
"v_accvgpr_read_b32 v237, a33\n\t"
"v_mfma_f32_16x16x32_bf16 a[92:95], a[240:243], a[200:203], a[92:95]\n\t"
"v_accvgpr_read_b32 v239, a37\n\t"
"v_cvt_pk_bf16_f32 v230, v230, v231\n\t"
"v_cvt_pk_bf16_f32 v234, v234, v235\n\t"
"v_accvgpr_write_b32 a25, v230\n\t"
"v_accvgpr_write_b32 a29, v234\n\t"
"v_pk_fma_f32 v[236:237], v[238:239], s[56:57], v[236:237]\n\t"
"v_pk_fma_f32 v[238:239], v[236:237], s[58:59], v[238:239] neg_lo:[0,0,1] neg_hi:[0,0,1]\n\t"
"v_pk_fma_f32 v[98:99], v[28:29], s[60:61], v[236:237]\n\t"
"v_pk_fma_f32 v[28:29], v[236:237], s[60:61], v[28:29]\n\t"
"v_pk_fma_f32 v[236:237], v[128:129], s[62:63], v[238:239]\n\t"
"v_pk_fma_f32 v[128:129], v[238:239], s[62:63], v[128:129]\n\t"
"v_mfma_f32_16x16x32_bf16 a[100:103], a[232:235], a[196:199], a[100:103]\n\t"
"s_waitcnt vmcnt(7)\n\t"
"v_accvgpr_read_b32 v224, a34\n\t"
"v_accvgpr_read_b32 v226, a38\n\t"
"v_mfma_f32_16x16x32_bf16 a[100:103], a[236:239], a[200:203], a[100:103]\n\t"
"v_accvgpr_read_b32 v225, a35\n\t"
"v_accvgpr_read_b32 v227, a39\n\t"
"v_mfma_f32_16x16x32_bf16 a[100:103], a[240:243], a[204:207], a[100:103]\n\t"
"buffer_load_dwordx4 a[232:235], v252, s[8:11], s20 offen offset:1024 sc1\n\t"
"v_cvt_pk_bf16_f32 v236, v236, v237\n\t"
"v_cvt_pk_bf16_f32 v98, v98, v99\n\t"
"v_pk_fma_f32 v[224:225], v[226:227], s[56:57], v[224:225]\n\t"
"v_pk_fma_f32 v[226:227], v[224:225], s[58:59], v[226:227] neg_lo:[0,0,1] neg_hi:[0,0,1]\n\t"
"v_pk_fma_f32 v[228:229], v[30:31], s[60:61], v[224:225]\n\t"
"v_pk_fma_f32 v[30:31], v[224:225], s[60:61], v[30:31]\n\t"
"v_pk_fma_f32 v[224:225], v[130:131], s[62:63], v[226:227]\n\t"
"v_pk_fma_f32 v[130:131], v[226:227], s[62:63], v[130:131]\n\t"
"v_mfma_f32_16x16x32_bf16 a[108:111], a[208:211], a[168:171], a[108:111]\n\t"
"ds_read_b128 a[168:171], v255 offset:512\n\t"
"v_accvgpr_write_b32 a32, v236\n\t"
"v_mfma_f32_16x16x32_bf16 a[108:111], a[212:215], a[172:175], a[108:111]\n\t"
"v_accvgpr_write_b32 a36, v98\n\t"
"v_accvgpr_read_b32 v230, a40\n\t"
"v_mfma_f32_16x16x32_bf16 a[108:111], a[216:219], a[176:179], a[108:111]\n\t"
"buffer_load_dwordx4 a[236:239], v252, s[8:11], s20 offen offset:2048 sc1\n\t"
"v_accvgpr_read_b32 v232, a44\n\t"
"v_accvgpr_read_b32 v231, a41\n\t"
"v_accvgpr_read_b32 v233, a45\n\t"
"v_cvt_pk_bf16_f32 v224, v224, v225\n\t"
"v_cvt_pk_bf16_f32 v228, v228, v229\n\t"
"s_mov_b32 s20, 1658880\n\t"
"v_pk_fma_f32 v[230:231], v[232:233], s[56:57], v[230:231]\n\t"
"v_pk_fma_f32 v[232:233], v[230:231], s[58:59], v[232:233] neg_lo:[0,0,1] neg_hi:[0,0,1]\n\t"
"v_pk_fma_f32 v[234:235], v[32:33], s[60:61], v[230:231]\n\t"
"v_pk_fma_f32 v[32:33], v[230:231], s[60:61], v[32:33]\n\t"
"v_pk_fma_f32 v[230:231], v[132:133], s[62:63], v[232:233]\n\t"
"v_pk_fma_f32 v[132:133], v[232:233], s[62:63], v[132:133]\n\t"
"v_mfma_f32_16x16x32_bf16 a[116:119], a[208:211], a[172:175], a[116:119]\n\t"
"ds_read_b128 a[172:175], v255 offset:3584\n\t"
"v_accvgpr_write_b32 a33, v224\n\t"
"v_mfma_f32_16x16x32_bf16 a[116:119], a[212:215], a[176:179], a[116:119]\n\t"
"v_accvgpr_write_b32 a37, v228\n\t"
"v_accvgpr_read_b32 v236, a42\n\t"
"v_mfma_f32_16x16x32_bf16 a[116:119], a[216:219], a[180:183], a[116:119]\n\t"
"buffer_load_dwordx4 a[240:243], v252, s[8:11], s20 offen offset:0 sc1\n\t"
"v_accvgpr_read_b32 v238, a46\n\t"
"v_accvgpr_read_b32 v237, a43\n\t"
"v_accvgpr_read_b32 v239, a47\n\t"
"v_pk_fma_f32 v[236:237], v[238:239], s[56:57], v[236:237]\n\t"
"v_pk_fma_f32 v[238:239], v[236:237], s[58:59], v[238:239] neg_lo:[0,0,1] neg_hi:[0,0,1]\n\t"
"v_pk_fma_f32 v[98:99], v[34:35], s[60:61], v[236:237]\n\t"
"v_pk_fma_f32 v[34:35], v[236:237], s[60:61], v[34:35]\n\t"
"v_pk_fma_f32 v[236:237], v[134:135], s[62:63], v[238:239]\n\t"
"v_pk_fma_f32 v[134:135], v[238:239], s[62:63], v[134:135]\n\t"
"v_mfma_f32_16x16x32_bf16 a[124:127], a[208:211], a[176:179], a[124:127]\n\t"
"ds_read_b128 a[176:179], v255 offset:6656\n\t"
"v_cvt_pk_bf16_f32 v198, v230, v231\n\t"
"v_mfma_f32_16x16x32_bf16 a[124:127], a[212:215], a[180:183], a[124:127]\n\t"
"v_cvt_pk_bf16_f32 v202, v234, v235\n\t"
"v_accvgpr_read_b32 v224, a48\n\t"
"v_mfma_f32_16x16x32_bf16 a[124:127], a[216:219], a[184:187], a[124:127]\n\t"
"global_load_dwordx4 v[248:251], v252, s[48:49] offset:0\n\t"
"v_accvgpr_read_b32 v226, a52\n\t"
"v_accvgpr_read_b32 v225, a49\n\t"
"v_accvgpr_read_b32 v227, a53\n\t"
"v_accvgpr_read_b32 v230, a50\n\t"
"v_accvgpr_read_b32 v232, a54\n\t"
"v_accvgpr_read_b32 v231, a51\n\t"
"v_accvgpr_read_b32 v233, a55\n\t"
"v_pk_fma_f32 v[224:225], v[226:227], s[56:57], v[224:225]\n\t"
"v_pk_fma_f32 v[226:227], v[224:225], s[58:59], v[226:227] neg_lo:[0,0,1] neg_hi:[0,0,1]\n\t"
"v_pk_fma_f32 v[228:229], v[36:37], s[60:61], v[224:225]\n\t"
"v_pk_fma_f32 v[36:37], v[224:225], s[60:61], v[36:37]\n\t"
"v_pk_fma_f32 v[224:225], v[136:137], s[62:63], v[226:227]\n\t"
"v_pk_fma_f32 v[136:137], v[226:227], s[62:63], v[136:137]\n\t"
"v_pk_fma_f32 v[230:231], v[232:233], s[56:57], v[230:231]\n\t"
"v_pk_fma_f32 v[232:233], v[230:231], s[58:59], v[232:233] neg_lo:[0,0,1] neg_hi:[0,0,1]\n\t"
"v_pk_fma_f32 v[234:235], v[38:39], s[60:61], v[230:231]\n\t"
"v_pk_fma_f32 v[38:39], v[230:231], s[60:61], v[38:39]\n\t"
"v_pk_fma_f32 v[230:231], v[138:139], s[62:63], v[232:233]\n\t"
"v_pk_fma_f32 v[138:139], v[232:233], s[62:63], v[138:139]\n\t"
"v_mfma_f32_16x16x32_bf16 a[132:135], a[208:211], a[180:183], a[132:135]\n\t"
"ds_read_b128 a[180:183], v255 offset:9728\n\t"
"v_cvt_pk_bf16_f32 v199, v236, v237\n\t"
"v_mfma_f32_16x16x32_bf16 a[132:135], a[212:215], a[184:187], a[132:135]\n\t"
"v_cvt_pk_bf16_f32 v203, v98, v99\n\t"
"v_accvgpr_read_b32 v236, a56\n\t"
"v_mfma_f32_16x16x32_bf16 a[132:135], a[216:219], a[188:191], a[132:135]\n\t"
"buffer_load_dwordx4 a[40:43], v252, s[8:11], s20 offen offset:1024 sc1\n\t"
"v_accvgpr_read_b32 v238, a60\n\t"
"v_accvgpr_read_b32 v237, a57\n\t"
"v_accvgpr_read_b32 v239, a61\n\t"
"v_pk_fma_f32 v[236:237], v[238:239], s[56:57], v[236:237]\n\t"
"v_pk_fma_f32 v[238:239], v[236:237], s[58:59], v[238:239] neg_lo:[0,0,1] neg_hi:[0,0,1]\n\t"
"v_pk_fma_f32 v[98:99], v[40:41], s[60:61], v[236:237]\n\t"
"v_pk_fma_f32 v[40:41], v[236:237], s[60:61], v[40:41]\n\t"
"v_pk_fma_f32 v[236:237], v[140:141], s[62:63], v[238:239]\n\t"
"v_pk_fma_f32 v[140:141], v[238:239], s[62:63], v[140:141]\n\t"
"v_mfma_f32_16x16x32_bf16 a[140:143], a[208:211], a[184:187], a[140:143]\n\t"
"ds_read_b128 a[184:187], v255 offset:12800\n\t"
"v_cvt_pk_bf16_f32 v206, v224, v225\n\t"
"v_mfma_f32_16x16x32_bf16 a[140:143], a[212:215], a[188:191], a[140:143]\n\t"
"v_cvt_pk_bf16_f32 v210, v228, v229\n\t"
"v_cvt_pk_bf16_f32 v207, v230, v231\n\t"
"v_mfma_f32_16x16x32_bf16 a[140:143], a[216:219], a[192:195], a[140:143]\n\t"
"buffer_load_dwordx4 a[48:51], v252, s[8:11], s20 offen offset:2048 sc1\n\t"
"v_accvgpr_read_b32 v224, a58\n\t"
"v_accvgpr_read_b32 v226, a62\n\t"
"v_accvgpr_read_b32 v225, a59\n\t"
"v_accvgpr_read_b32 v227, a63\n\t"
"v_cvt_pk_bf16_f32 v211, v234, v235\n\t"
"s_mov_b32 s20, 1671168\n\t"
"v_pk_fma_f32 v[224:225], v[226:227], s[56:57], v[224:225]\n\t"
"v_pk_fma_f32 v[226:227], v[224:225], s[58:59], v[226:227] neg_lo:[0,0,1] neg_hi:[0,0,1]\n\t"
"v_pk_fma_f32 v[228:229], v[42:43], s[60:61], v[224:225]\n\t"
"v_pk_fma_f32 v[42:43], v[224:225], s[60:61], v[42:43]\n\t"
"v_pk_fma_f32 v[224:225], v[142:143], s[62:63], v[226:227]\n\t"
"v_pk_fma_f32 v[142:143], v[226:227], s[62:63], v[142:143]\n\t"
"v_mfma_f32_16x16x32_bf16 a[148:151], a[208:211], a[188:191], a[148:151]\n\t"
"ds_read_b128 a[188:191], v255 offset:15872\n\t"
"v_cvt_pk_bf16_f32 v214, v236, v237\n\t"
"v_mfma_f32_16x16x32_bf16 a[148:151], a[212:215], a[192:195], a[148:151]\n\t"
"v_cvt_pk_bf16_f32 v218, v98, v99\n\t"
"v_accvgpr_read_b32 v230, a64\n\t"
"v_mfma_f32_16x16x32_bf16 a[148:151], a[216:219], a[196:199], a[148:151]\n\t"
"buffer_load_dwordx4 a[52:55], v252, s[8:11], s20 offen offset:0 sc1\n\t"
"v_accvgpr_read_b32 v232, a68\n\t"
"v_accvgpr_read_b32 v231, a65\n\t"
"v_accvgpr_read_b32 v233, a69\n\t"
"v_pk_fma_f32 v[230:231], v[232:233], s[56:57], v[230:231]\n\t"
"v_pk_fma_f32 v[232:233], v[230:231], s[58:59], v[232:233] neg_lo:[0,0,1] neg_hi:[0,0,1]\n\t"
"v_pk_fma_f32 v[234:235], v[44:45], s[60:61], v[230:231]\n\t"
"v_pk_fma_f32 v[44:45], v[230:231], s[60:61], v[44:45]\n\t"
"v_pk_fma_f32 v[230:231], v[144:145], s[62:63], v[232:233]\n\t"
"v_pk_fma_f32 v[144:145], v[232:233], s[62:63], v[144:145]\n\t"
"v_mfma_f32_16x16x32_bf16 a[156:159], a[208:211], a[192:195], a[156:159]\n\t"
"ds_read_b128 a[192:195], v255 offset:18944\n\t"
"v_cvt_pk_bf16_f32 v215, v224, v225\n\t"
"v_mfma_f32_16x16x32_bf16 a[156:159], a[212:215], a[196:199], a[156:159]\n\t"
"v_cvt_pk_bf16_f32 v219, v228, v229\n\t"
"v_accvgpr_read_b32 v236, a66\n\t"
"v_mfma_f32_16x16x32_bf16 a[156:159], a[216:219], a[200:203], a[156:159]\n\t"
"buffer_load_dwordx4 a[44:47], v252, s[8:11], s20 offen offset:1024 sc1\n\t"
"v_accvgpr_read_b32 v238, a70\n\t"
"v_accvgpr_read_b32 v237, a67\n\t"
"v_accvgpr_read_b32 v239, a71\n\t"
"v_cvt_pk_bf16_f32 v230, v230, v231\n\t"
"v_cvt_pk_bf16_f32 v234, v234, v235\n\t"
"v_pk_fma_f32 v[236:237], v[238:239], s[56:57], v[236:237]\n\t"
"v_pk_fma_f32 v[238:239], v[236:237], s[58:59], v[238:239] neg_lo:[0,0,1] neg_hi:[0,0,1]\n\t"
"v_pk_fma_f32 v[98:99], v[46:47], s[60:61], v[236:237]\n\t"
"v_pk_fma_f32 v[46:47], v[236:237], s[60:61], v[46:47]\n\t"
"v_pk_fma_f32 v[236:237], v[146:147], s[62:63], v[238:239]\n\t"
"v_pk_fma_f32 v[146:147], v[238:239], s[62:63], v[146:147]\n\t"
"v_mfma_f32_16x16x32_bf16 a[164:167], a[208:211], a[196:199], a[164:167]\n\t"
"s_waitcnt vmcnt(12) lgkmcnt(4)\n\t"
"ds_read_b128 a[196:199], v255 offset:22016\n\t"
"v_accvgpr_write_b32 a2, v230\n\t"
"v_mfma_f32_16x16x32_bf16 a[164:167], a[212:215], a[200:203], a[164:167]\n\t"
"ds_read_b128 a[200:203], v255 offset:25088\n\t"
"v_accvgpr_write_b32 a6, v234\n\t"
"v_mfma_f32_16x16x32_bf16 a[164:167], a[216:219], a[204:207], a[164:167]\n\t"
"buffer_load_dwordx4 a[60:63], v252, s[8:11], s20 offen offset:2048 sc1\n\t"
"ds_read_b128 a[204:207], v255 offset:28160\n\t"
"v_accvgpr_read_b32 v224, a72\n\t"
"v_accvgpr_read_b32 v226, a76\n\t"
"v_accvgpr_read_b32 v225, a73\n\t"
"v_accvgpr_read_b32 v227, a77\n\t"
"v_cvt_pk_bf16_f32 v236, v236, v237\n\t"
"v_cvt_pk_bf16_f32 v98, v98, v99\n\t"
"s_mov_b32 s20, 1683456\n\t"
"v_pk_fma_f32 v[224:225], v[226:227], s[56:57], v[224:225]\n\t"
"v_pk_fma_f32 v[226:227], v[224:225], s[58:59], v[226:227] neg_lo:[0,0,1] neg_hi:[0,0,1]\n\t"
"v_pk_fma_f32 v[228:229], v[48:49], s[60:61], v[224:225]\n\t"
"v_pk_fma_f32 v[48:49], v[224:225], s[60:61], v[48:49]\n\t"
"v_pk_fma_f32 v[224:225], v[148:149], s[62:63], v[226:227]\n\t"
"v_pk_fma_f32 v[148:149], v[226:227], s[62:63], v[148:149]\n\t"
"Lst14_%=:\n\t"
"v_mfma_f32_16x16x32_bf16 v[0:3], a[220:223], a[168:171], v[0:3]\n\t"
"s_waitcnt lgkmcnt(6)\n\t"
"v_accvgpr_write_b32 a3, v236\n\t"
"v_accvgpr_write_b32 a7, v98\n\t"
"v_mfma_f32_16x16x32_bf16 v[0:3], a[228:231], a[172:175], v[0:3]\n\t"
"v_accvgpr_read_b32 v230, a74\n\t"
"v_accvgpr_read_b32 v232, a78\n\t"
"v_mfma_f32_16x16x32_bf16 v[0:3], a[244:247], a[176:179], v[0:3]\n\t"
"buffer_load_dwordx4 a[64:67], v252, s[8:11], s20 offen offset:0 sc1\n\t"
"v_accvgpr_read_b32 v231, a75\n\t"
"v_accvgpr_read_b32 v233, a79\n\t"
"v_cvt_pk_bf16_f32 v224, v224, v225\n\t"
"v_cvt_pk_bf16_f32 v228, v228, v229\n\t"
"v_pk_fma_f32 v[230:231], v[232:233], s[56:57], v[230:231]\n\t"
"v_pk_fma_f32 v[232:233], v[230:231], s[58:59], v[232:233] neg_lo:[0,0,1] neg_hi:[0,0,1]\n\t"
"v_pk_fma_f32 v[234:235], v[50:51], s[60:61], v[230:231]\n\t"
"v_pk_fma_f32 v[50:51], v[230:231], s[60:61], v[50:51]\n\t"
"v_pk_fma_f32 v[230:231], v[150:151], s[62:63], v[232:233]\n\t"
"v_pk_fma_f32 v[150:151], v[232:233], s[62:63], v[150:151]\n\t"
"v_mfma_f32_16x16x32_bf16 v[4:7], a[220:223], a[172:175], v[4:7]\n\t"
"s_waitcnt lgkmcnt(5)\n\t"
"v_accvgpr_write_b32 a10, v224\n\t"
"v_accvgpr_write_b32 a14, v228\n\t"
"v_mfma_f32_16x16x32_bf16 v[4:7], a[228:231], a[176:179], v[4:7]\n\t"
"v_accvgpr_read_b32 v236, a80\n\t"
"v_accvgpr_read_b32 v238, a84\n\t"
"v_mfma_f32_16x16x32_bf16 v[4:7], a[244:247], a[180:183], v[4:7]\n\t"
"buffer_load_dwordx4 a[68:71], v252, s[8:11], s20 offen offset:1024 sc1\n\t"
"v_accvgpr_read_b32 v237, a81\n\t"
"v_accvgpr_read_b32 v239, a85\n\t"
"v_cvt_pk_bf16_f32 v230, v230, v231\n\t"
"v_cvt_pk_bf16_f32 v234, v234, v235\n\t"
"v_pk_fma_f32 v[236:237], v[238:239], s[56:57], v[236:237]\n\t"
"v_pk_fma_f32 v[238:239], v[236:237], s[58:59], v[238:239] neg_lo:[0,0,1] neg_hi:[0,0,1]\n\t"
"v_pk_fma_f32 v[98:99], v[52:53], s[60:61], v[236:237]\n\t"
"v_pk_fma_f32 v[52:53], v[236:237], s[60:61], v[52:53]\n\t"
"v_pk_fma_f32 v[236:237], v[152:153], s[62:63], v[238:239]\n\t"
"v_pk_fma_f32 v[152:153], v[238:239], s[62:63], v[152:153]\n\t"
"v_mfma_f32_16x16x32_bf16 v[8:11], a[220:223], a[176:179], v[8:11]\n\t"
"s_waitcnt lgkmcnt(4)\n\t"
"v_accvgpr_write_b32 a11, v230\n\t"
"v_accvgpr_write_b32 a15, v234\n\t"
"v_mfma_f32_16x16x32_bf16 v[8:11], a[228:231], a[180:183], v[8:11]\n\t"
"v_accvgpr_read_b32 v224, a82\n\t"
"v_accvgpr_read_b32 v226, a86\n\t"
"v_mfma_f32_16x16x32_bf16 v[8:11], a[244:247], a[184:187], v[8:11]\n\t"
"buffer_load_dwordx4 a[72:75], v252, s[8:11], s20 offen offset:2048 sc1\n\t"
"v_accvgpr_read_b32 v225, a83\n\t"
"v_accvgpr_read_b32 v227, a87\n\t"
"v_cvt_pk_bf16_f32 v236, v236, v237\n\t"
"v_cvt_pk_bf16_f32 v98, v98, v99\n\t"
"s_mov_b32 s20, 589824\n\t"
"v_pk_fma_f32 v[224:225], v[226:227], s[56:57], v[224:225]\n\t"
"v_pk_fma_f32 v[226:227], v[224:225], s[58:59], v[226:227] neg_lo:[0,0,1] neg_hi:[0,0,1]\n\t"
"v_pk_fma_f32 v[228:229], v[54:55], s[60:61], v[224:225]\n\t"
"v_pk_fma_f32 v[54:55], v[224:225], s[60:61], v[54:55]\n\t"
"v_pk_fma_f32 v[224:225], v[154:155], s[62:63], v[226:227]\n\t"
"v_pk_fma_f32 v[154:155], v[226:227], s[62:63], v[154:155]\n\t"
"v_mfma_f32_16x16x32_bf16 v[12:15], a[220:223], a[180:183], v[12:15]\n\t"
"s_waitcnt lgkmcnt(3)\n\t"
"v_accvgpr_write_b32 a18, v236\n\t"
"v_accvgpr_write_b32 a22, v98\n\t"
"v_mfma_f32_16x16x32_bf16 v[12:15], a[228:231], a[184:187], v[12:15]\n\t"
"v_accvgpr_read_b32 v230, a88\n\t"
"v_accvgpr_read_b32 v232, a92\n\t"
"v_mfma_f32_16x16x32_bf16 v[12:15], a[244:247], a[188:191], v[12:15]\n\t"
"buffer_load_dwordx4 a[76:79], v252, s[8:11], s20 offen offset:0 sc1\n\t"
"v_accvgpr_read_b32 v231, a89\n\t"
"v_accvgpr_read_b32 v233, a93\n\t"
"v_accvgpr_read_b32 v236, a90\n\t"
"v_accvgpr_read_b32 v238, a94\n\t"
"v_accvgpr_read_b32 v237, a91\n\t"
"v_accvgpr_read_b32 v239, a95\n\t"
"v_cvt_pk_bf16_f32 v224, v224, v225\n\t"
"v_cvt_pk_bf16_f32 v228, v228, v229\n\t"
"v_pk_fma_f32 v[230:231], v[232:233], s[56:57], v[230:231]\n\t"
"v_pk_fma_f32 v[232:233], v[230:231], s[58:59], v[232:233] neg_lo:[0,0,1] neg_hi:[0,0,1]\n\t"
"v_pk_fma_f32 v[234:235], v[56:57], s[60:61], v[230:231]\n\t"
"v_pk_fma_f32 v[56:57], v[230:231], s[60:61], v[56:57]\n\t"
"v_pk_fma_f32 v[230:231], v[156:157], s[62:63], v[232:233]\n\t"
"v_pk_fma_f32 v[156:157], v[232:233], s[62:63], v[156:157]\n\t"
"v_pk_fma_f32 v[236:237], v[238:239], s[56:57], v[236:237]\n\t"
"v_pk_fma_f32 v[238:239], v[236:237], s[58:59], v[238:239] neg_lo:[0,0,1] neg_hi:[0,0,1]\n\t"
"v_pk_fma_f32 v[98:99], v[58:59], s[60:61], v[236:237]\n\t"
"v_pk_fma_f32 v[58:59], v[236:237], s[60:61], v[58:59]\n\t"
"v_pk_fma_f32 v[236:237], v[158:159], s[62:63], v[238:239]\n\t"
"v_pk_fma_f32 v[158:159], v[238:239], s[62:63], v[158:159]\n\t"
"v_mfma_f32_16x16x32_bf16 v[16:19], a[220:223], a[184:187], v[16:19]\n\t"
"s_waitcnt lgkmcnt(2)\n\t"
"v_accvgpr_write_b32 a19, v224\n\t"
"v_accvgpr_write_b32 a23, v228\n\t"
"v_mfma_f32_16x16x32_bf16 v[16:19], a[228:231], a[188:191], v[16:19]\n\t"
"v_accvgpr_read_b32 v224, a96\n\t"
"v_accvgpr_read_b32 v226, a100\n\t"
"v_mfma_f32_16x16x32_bf16 v[16:19], a[244:247], a[192:195], v[16:19]\n\t"
"buffer_load_dwordx4 a[84:87], v252, s[8:11], s20 offen offset:1024 sc1\n\t"
"v_accvgpr_read_b32 v225, a97\n\t"
"v_accvgpr_read_b32 v227, a101\n\t"
"v_cvt_pk_bf16_f32 v230, v230, v231\n\t"
"v_cvt_pk_bf16_f32 v234, v234, v235\n\t"
"v_pk_fma_f32 v[224:225], v[226:227], s[56:57], v[224:225]\n\t"
"v_pk_fma_f32 v[226:227], v[224:225], s[58:59], v[226:227] neg_lo:[0,0,1] neg_hi:[0,0,1]\n\t"
"v_pk_fma_f32 v[228:229], v[60:61], s[60:61], v[224:225]\n\t"
"v_pk_fma_f32 v[60:61], v[224:225], s[60:61], v[60:61]\n\t"
"v_pk_fma_f32 v[224:225], v[160:161], s[62:63], v[226:227]\n\t"
"v_pk_fma_f32 v[160:161], v[226:227], s[62:63], v[160:161]\n\t"
"v_mfma_f32_16x16x32_bf16 v[20:23], a[220:223], a[188:191], v[20:23]\n\t"
"s_waitcnt lgkmcnt(1)\n\t"
"v_accvgpr_write_b32 a26, v230\n\t"
"v_cvt_pk_bf16_f32 v236, v236, v237\n\t"
"v_mfma_f32_16x16x32_bf16 v[20:23], a[228:231], a[192:195], v[20:23]\n\t"
"v_accvgpr_write_b32 a30, v234\n\t"
"v_cvt_pk_bf16_f32 v98, v98, v99\n\t"
"v_mfma_f32_16x16x32_bf16 v[20:23], a[244:247], a[196:199], v[20:23]\n\t"
"buffer_load_dwordx4 a[88:91], v252, s[8:11], s20 offen offset:2048 sc1\n\t"
"v_accvgpr_read_b32 v230, a98\n\t"
"v_accvgpr_read_b32 v232, a102\n\t"
"v_accvgpr_read_b32 v231, a99\n\t"
"v_accvgpr_read_b32 v233, a103\n\t"
"v_accvgpr_write_b32 a27, v236\n\t"
"v_accvgpr_write_b32 a31, v98\n\t"
"v_cvt_pk_bf16_f32 v224, v224, v225\n\t"
"v_cvt_pk_bf16_f32 v228, v228, v229\n\t"
"s_mov_b32 s20, 602112\n\t"
"v_pk_fma_f32 v[230:231], v[232:233], s[56:57], v[230:231]\n\t"
"v_pk_fma_f32 v[232:233], v[230:231], s[58:59], v[232:233] neg_lo:[0,0,1] neg_hi:[0,0,1]\n\t"
"v_pk_fma_f32 v[234:235], v[62:63], s[60:61], v[230:231]\n\t"
"v_pk_fma_f32 v[62:63], v[230:231], s[60:61], v[62:63]\n\t"
"v_pk_fma_f32 v[230:231], v[162:163], s[62:63], v[232:233]\n\t"
"v_pk_fma_f32 v[162:163], v[232:233], s[62:63], v[162:163]\n\t"
"v_mfma_f32_16x16x32_bf16 v[24:27], a[220:223], a[192:195], v[24:27]\n\t"
"s_waitcnt lgkmcnt(0)\n\t"
"v_accvgpr_write_b32 a34, v224\n\t"
"v_accvgpr_write_b32 a38, v228\n\t"
"v_mfma_f32_16x16x32_bf16 v[24:27], a[228:231], a[196:199], v[24:27]\n\t"
"v_accvgpr_read_b32 v236, a104\n\t"
"v_accvgpr_read_b32 v238, a108\n\t"
"v_mfma_f32_16x16x32_bf16 v[24:27], a[244:247], a[200:203], v[24:27]\n\t"
"buffer_load_dwordx4 a[92:95], v252, s[8:11], s20 offen offset:0 sc1\n\t"
"v_accvgpr_read_b32 v237, a105\n\t"
"v_accvgpr_read_b32 v239, a109\n\t"
"v_cvt_pk_bf16_f32 v230, v230, v231\n\t"
"v_cvt_pk_bf16_f32 v234, v234, v235\n\t"
"v_pk_fma_f32 v[236:237], v[238:239], s[56:57], v[236:237]\n\t"
"v_pk_fma_f32 v[238:239], v[236:237], s[58:59], v[238:239] neg_lo:[0,0,1] neg_hi:[0,0,1]\n\t"
"v_pk_fma_f32 v[98:99], v[64:65], s[60:61], v[236:237]\n\t"
"v_pk_fma_f32 v[64:65], v[236:237], s[60:61], v[64:65]\n\t"
"v_pk_fma_f32 v[236:237], v[164:165], s[62:63], v[238:239]\n\t"
"v_pk_fma_f32 v[164:165], v[238:239], s[62:63], v[164:165]\n\t"
"v_mfma_f32_16x16x32_bf16 v[28:31], a[220:223], a[196:199], v[28:31]\n\t"
"s_waitcnt vmcnt(17)\n\t"
"v_accvgpr_write_b32 a35, v230\n\t"
"v_accvgpr_write_b32 a39, v234\n\t"
"v_mfma_f32_16x16x32_bf16 v[28:31], a[228:231], a[200:203], v[28:31]\n\t"
"v_accvgpr_read_b32 v224, a106\n\t"
"v_accvgpr_read_b32 v226, a110\n\t"
"v_mfma_f32_16x16x32_bf16 v[28:31], a[244:247], a[204:207], v[28:31]\n\t"
"buffer_load_dwordx4 a[96:99], v252, s[8:11], s20 offen offset:1024 sc1\n\t"
"v_accvgpr_read_b32 v225, a107\n\t"
"v_accvgpr_read_b32 v227, a111\n\t"
"v_cvt_pk_bf16_f32 v236, v236, v237\n\t"
"v_cvt_pk_bf16_f32 v98, v98, v99\n\t"
"v_pk_fma_f32 v[224:225], v[226:227], s[56:57], v[224:225]\n\t"
"v_pk_fma_f32 v[226:227], v[224:225], s[58:59], v[226:227] neg_lo:[0,0,1] neg_hi:[0,0,1]\n\t"
"v_pk_fma_f32 v[228:229], v[66:67], s[60:61], v[224:225]\n\t"
"v_pk_fma_f32 v[66:67], v[224:225], s[60:61], v[66:67]\n\t"
"v_pk_fma_f32 v[224:225], v[166:167], s[62:63], v[226:227]\n\t"
"v_pk_fma_f32 v[166:167], v[226:227], s[62:63], v[166:167]\n\t"
"v_mfma_f32_16x16x32_bf16 v[32:35], v[240:243], a[168:171], v[32:35]\n\t"
"v_accvgpr_write_b32 a104, v236\n\t"
"v_accvgpr_write_b32 a106, v98\n\t"
"v_mfma_f32_16x16x32_bf16 v[32:35], a[224:227], a[172:175], v[32:35]\n\t"
"v_accvgpr_read_b32 v230, a112\n\t"
"v_accvgpr_read_b32 v232, a116\n\t"
"v_mfma_f32_16x16x32_bf16 v[32:35], a[248:251], a[176:179], v[32:35]\n\t"
"buffer_load_dwordx4 a[100:103], v252, s[8:11], s20 offen offset:2048 sc1\n\t"
"v_accvgpr_read_b32 v231, a113\n\t"
"v_accvgpr_read_b32 v233, a117\n\t"
"v_cvt_pk_bf16_f32 v224, v224, v225\n\t"
"v_cvt_pk_bf16_f32 v228, v228, v229\n\t"
"s_mov_b32 s20, 614400\n\t"
"v_pk_fma_f32 v[230:231], v[232:233], s[56:57], v[230:231]\n\t"
"v_pk_fma_f32 v[232:233], v[230:231], s[58:59], v[232:233] neg_lo:[0,0,1] neg_hi:[0,0,1]\n\t"
"v_pk_fma_f32 v[234:235], v[68:69], s[60:61], v[230:231]\n\t"
"v_pk_fma_f32 v[68:69], v[230:231], s[60:61], v[68:69]\n\t"
"v_pk_fma_f32 v[230:231], v[168:169], s[62:63], v[232:233]\n\t"
"v_pk_fma_f32 v[168:169], v[232:233], s[62:63], v[168:169]\n\t"
"v_mfma_f32_16x16x32_bf16 v[36:39], v[240:243], a[172:175], v[36:39]\n\t"
"v_accvgpr_write_b32 a105, v224\n\t"
"v_accvgpr_write_b32 a107, v228\n\t"
"v_mfma_f32_16x16x32_bf16 v[36:39], a[224:227], a[176:179], v[36:39]\n\t"
"v_accvgpr_read_b32 v236, a114\n\t"
"v_accvgpr_read_b32 v238, a118\n\t"
"v_mfma_f32_16x16x32_bf16 v[36:39], a[248:251], a[180:183], v[36:39]\n\t"
"buffer_load_dwordx4 a[208:211], v252, s[8:11], s20 offen offset:0 sc1\n\t"
"v_accvgpr_read_b32 v237, a115\n\t"
"v_accvgpr_read_b32 v239, a119\n\t"
"v_cvt_pk_bf16_f32 v230, v230, v231\n\t"
"v_cvt_pk_bf16_f32 v234, v234, v235\n\t"
"v_pk_fma_f32 v[236:237], v[238:239], s[56:57], v[236:237]\n\t"
"v_pk_fma_f32 v[238:239], v[236:237], s[58:59], v[238:239] neg_lo:[0,0,1] neg_hi:[0,0,1]\n\t"
"v_pk_fma_f32 v[98:99], v[70:71], s[60:61], v[236:237]\n\t"
"v_pk_fma_f32 v[70:71], v[236:237], s[60:61], v[70:71]\n\t"
"v_pk_fma_f32 v[236:237], v[170:171], s[62:63], v[238:239]\n\t"
"v_pk_fma_f32 v[170:171], v[238:239], s[62:63], v[170:171]\n\t"
"v_mfma_f32_16x16x32_bf16 v[40:43], v[240:243], a[176:179], v[40:43]\n\t"
"v_accvgpr_write_b32 a112, v230\n\t"
"v_accvgpr_write_b32 a114, v234\n\t"
"v_mfma_f32_16x16x32_bf16 v[40:43], a[224:227], a[180:183], v[40:43]\n\t"
"v_accvgpr_read_b32 v224, a120\n\t"
"v_accvgpr_read_b32 v226, a124\n\t"
"v_mfma_f32_16x16x32_bf16 v[40:43], a[248:251], a[184:187], v[40:43]\n\t"
"buffer_load_dwordx4 a[212:215], v252, s[8:11], s20 offen offset:1024 sc1\n\t"
"v_accvgpr_read_b32 v225, a121\n\t"
"v_accvgpr_read_b32 v227, a125\n\t"
"v_cvt_pk_bf16_f32 v236, v236, v237\n\t"
"v_cvt_pk_bf16_f32 v98, v98, v99\n\t"
"v_pk_fma_f32 v[224:225], v[226:227], s[56:57], v[224:225]\n\t"
"v_pk_fma_f32 v[226:227], v[224:225], s[58:59], v[226:227] neg_lo:[0,0,1] neg_hi:[0,0,1]\n\t"
"v_pk_fma_f32 v[228:229], v[72:73], s[60:61], v[224:225]\n\t"
"v_pk_fma_f32 v[72:73], v[224:225], s[60:61], v[72:73]\n\t"
"v_pk_fma_f32 v[224:225], v[172:173], s[62:63], v[226:227]\n\t"
"v_pk_fma_f32 v[172:173], v[226:227], s[62:63], v[172:173]\n\t"
"v_mfma_f32_16x16x32_bf16 v[44:47], v[240:243], a[180:183], v[44:47]\n\t"
"v_accvgpr_write_b32 a113, v236\n\t"
"v_accvgpr_write_b32 a115, v98\n\t"
"v_mfma_f32_16x16x32_bf16 v[44:47], a[224:227], a[184:187], v[44:47]\n\t"
"v_accvgpr_read_b32 v230, a122\n\t"
"v_accvgpr_read_b32 v232, a126\n\t"
"v_mfma_f32_16x16x32_bf16 v[44:47], a[248:251], a[188:191], v[44:47]\n\t"
"buffer_load_dwordx4 a[116:119], v252, s[8:11], s20 offen offset:2048 sc1\n\t"
"v_accvgpr_read_b32 v231, a123\n\t"
"v_accvgpr_read_b32 v233, a127\n\t"
"v_accvgpr_read_b32 v236, a128\n\t"
"v_accvgpr_read_b32 v238, a132\n\t"
"v_accvgpr_read_b32 v237, a129\n\t"
"v_accvgpr_read_b32 v239, a133\n\t"
"v_cvt_pk_bf16_f32 v224, v224, v225\n\t"
"v_cvt_pk_bf16_f32 v228, v228, v229\n\t"
"s_mov_b32 s20, 774144\n\t"
"v_pk_fma_f32 v[230:231], v[232:233], s[56:57], v[230:231]\n\t"
"v_pk_fma_f32 v[232:233], v[230:231], s[58:59], v[232:233] neg_lo:[0,0,1] neg_hi:[0,0,1]\n\t"
"v_pk_fma_f32 v[234:235], v[74:75], s[60:61], v[230:231]\n\t"
"v_pk_fma_f32 v[74:75], v[230:231], s[60:61], v[74:75]\n\t"
"v_pk_fma_f32 v[230:231], v[174:175], s[62:63], v[232:233]\n\t"
"v_pk_fma_f32 v[174:175], v[232:233], s[62:63], v[174:175]\n\t"
"v_pk_fma_f32 v[236:237], v[238:239], s[56:57], v[236:237]\n\t"
"v_pk_fma_f32 v[238:239], v[236:237], s[58:59], v[238:239] neg_lo:[0,0,1] neg_hi:[0,0,1]\n\t"
"v_pk_fma_f32 v[98:99], v[76:77], s[60:61], v[236:237]\n\t"
"v_pk_fma_f32 v[76:77], v[236:237], s[60:61], v[76:77]\n\t"
"v_pk_fma_f32 v[236:237], v[176:177], s[62:63], v[238:239]\n\t"
"v_pk_fma_f32 v[176:177], v[238:239], s[62:63], v[176:177]\n\t"
"v_mfma_f32_16x16x32_bf16 v[48:51], v[240:243], a[184:187], v[48:51]\n\t"
"v_accvgpr_write_b32 a120, v224\n\t"
"v_accvgpr_write_b32 a122, v228\n\t"
"v_mfma_f32_16x16x32_bf16 v[48:51], a[224:227], a[188:191], v[48:51]\n\t"
"v_accvgpr_read_b32 v224, a130\n\t"
"v_accvgpr_read_b32 v226, a134\n\t"
"v_mfma_f32_16x16x32_bf16 v[48:51], a[248:251], a[192:195], v[48:51]\n\t"
"buffer_load_dwordx4 a[216:219], v252, s[8:11], s20 offen offset:0 sc1\n\t"
"v_accvgpr_read_b32 v225, a131\n\t"
"v_accvgpr_read_b32 v227, a135\n\t"
"v_cvt_pk_bf16_f32 v230, v230, v231\n\t"
"v_cvt_pk_bf16_f32 v234, v234, v235\n\t"
"v_cvt_pk_bf16_f32 v236, v236, v237\n\t"
"v_cvt_pk_bf16_f32 v98, v98, v99\n\t"
"v_pk_fma_f32 v[224:225], v[226:227], s[56:57], v[224:225]\n\t"
"v_pk_fma_f32 v[226:227], v[224:225], s[58:59], v[226:227] neg_lo:[0,0,1] neg_hi:[0,0,1]\n\t"
"v_pk_fma_f32 v[228:229], v[78:79], s[60:61], v[224:225]\n\t"
"v_pk_fma_f32 v[78:79], v[224:225], s[60:61], v[78:79]\n\t"
"v_pk_fma_f32 v[224:225], v[178:179], s[62:63], v[226:227]\n\t"
"v_pk_fma_f32 v[178:179], v[226:227], s[62:63], v[178:179]\n\t"
"v_mfma_f32_16x16x32_bf16 v[52:55], v[240:243], a[188:191], v[52:55]\n\t"
"v_accvgpr_write_b32 a121, v230\n\t"
"v_accvgpr_write_b32 a123, v234\n\t"
"v_mfma_f32_16x16x32_bf16 v[52:55], a[224:227], a[192:195], v[52:55]\n\t"
"v_accvgpr_write_b32 a128, v236\n\t"
"v_accvgpr_write_b32 a130, v98\n\t"
"v_mfma_f32_16x16x32_bf16 v[52:55], a[248:251], a[196:199], v[52:55]\n\t"
"buffer_load_dwordx4 a[124:127], v252, s[8:11], s20 offen offset:1024 sc1\n\t"
"v_accvgpr_read_b32 v230, a136\n\t"
"v_accvgpr_read_b32 v232, a140\n\t"
"v_accvgpr_read_b32 v231, a137\n\t"
"v_accvgpr_read_b32 v233, a141\n\t"
"v_cvt_pk_bf16_f32 v224, v224, v225\n\t"
"v_cvt_pk_bf16_f32 v228, v228, v229\n\t"
"v_pk_fma_f32 v[230:231], v[232:233], s[56:57], v[230:231]\n\t"
"v_pk_fma_f32 v[232:233], v[230:231], s[58:59], v[232:233] neg_lo:[0,0,1] neg_hi:[0,0,1]\n\t"
"v_pk_fma_f32 v[234:235], v[80:81], s[60:61], v[230:231]\n\t"
"v_pk_fma_f32 v[80:81], v[230:231], s[60:61], v[80:81]\n\t"
"v_pk_fma_f32 v[230:231], v[180:181], s[62:63], v[232:233]\n\t"
"v_pk_fma_f32 v[180:181], v[232:233], s[62:63], v[180:181]\n\t"
"v_mfma_f32_16x16x32_bf16 v[56:59], v[240:243], a[192:195], v[56:59]\n\t"
"v_accvgpr_write_b32 a129, v224\n\t"
"v_accvgpr_write_b32 a131, v228\n\t"
"v_mfma_f32_16x16x32_bf16 v[56:59], a[224:227], a[196:199], v[56:59]\n\t"
"v_accvgpr_read_b32 v236, a138\n\t"
"v_accvgpr_read_b32 v238, a142\n\t"
"v_mfma_f32_16x16x32_bf16 v[56:59], a[248:251], a[200:203], v[56:59]\n\t"
"s_waitcnt vmcnt(19)\n\t"
"ds_write_b128 v249, v[196:199] offset:0\n\t"
"v_accvgpr_read_b32 v237, a139\n\t"
"v_accvgpr_read_b32 v239, a143\n\t"
"v_cvt_pk_bf16_f32 v230, v230, v231\n\t"
"v_cvt_pk_bf16_f32 v234, v234, v235\n\t"
"v_pk_fma_f32 v[236:237], v[238:239], s[56:57], v[236:237]\n\t"
"v_pk_fma_f32 v[238:239], v[236:237], s[58:59], v[238:239] neg_lo:[0,0,1] neg_hi:[0,0,1]\n\t"
"v_pk_fma_f32 v[98:99], v[82:83], s[60:61], v[236:237]\n\t"
"v_pk_fma_f32 v[82:83], v[236:237], s[60:61], v[82:83]\n\t"
"v_pk_fma_f32 v[236:237], v[182:183], s[62:63], v[238:239]\n\t"
"v_pk_fma_f32 v[182:183], v[238:239], s[62:63], v[182:183]\n\t"
"v_mfma_f32_16x16x32_bf16 v[60:63], v[240:243], a[196:199], v[60:63]\n\t"
"v_accvgpr_write_b32 a136, v230\n\t"
"v_accvgpr_write_b32 a138, v234\n\t"
"v_mfma_f32_16x16x32_bf16 v[60:63], a[224:227], a[200:203], v[60:63]\n\t"
"v_accvgpr_read_b32 v224, a144\n\t"
"v_accvgpr_read_b32 v226, a148\n\t"
"v_mfma_f32_16x16x32_bf16 v[60:63], a[248:251], a[204:207], v[60:63]\n\t"
"buffer_load_dwordx4 a[132:135], v252, s[8:11], s20 offen offset:2048 sc1\n\t"
"v_accvgpr_read_b32 v225, a145\n\t"
"v_accvgpr_read_b32 v227, a149\n\t"
"v_cvt_pk_bf16_f32 v236, v236, v237\n\t"
"v_cvt_pk_bf16_f32 v98, v98, v99\n\t"
"s_mov_b32 s20, 786432\n\t"
"v_pk_fma_f32 v[224:225], v[226:227], s[56:57], v[224:225]\n\t"
"v_pk_fma_f32 v[226:227], v[224:225], s[58:59], v[226:227] neg_lo:[0,0,1] neg_hi:[0,0,1]\n\t"
"v_pk_fma_f32 v[228:229], v[84:85], s[60:61], v[224:225]\n\t"
"v_pk_fma_f32 v[84:85], v[224:225], s[60:61], v[84:85]\n\t"
"v_pk_fma_f32 v[224:225], v[184:185], s[62:63], v[226:227]\n\t"
"v_pk_fma_f32 v[184:185], v[226:227], s[62:63], v[184:185]\n\t"
"v_mfma_f32_16x16x32_bf16 v[64:67], a[252:255], a[168:171], v[64:67]\n\t"
"ds_read_b128 a[168:171], v255 offset:2048\n\t"
"v_accvgpr_write_b32 a137, v236\n\t"
"v_mfma_f32_16x16x32_bf16 v[64:67], a[232:235], a[172:175], v[64:67]\n\t"
"v_accvgpr_write_b32 a139, v98\n\t"
"v_accvgpr_read_b32 v230, a146\n\t"
"v_mfma_f32_16x16x32_bf16 v[64:67], a[236:239], a[176:179], v[64:67]\n\t"
"buffer_load_dwordx4 a[220:223], v252, s[8:11], s20 offen offset:0 sc1\n\t"
"v_accvgpr_read_b32 v232, a150\n\t"
"v_accvgpr_read_b32 v231, a147\n\t"
"v_accvgpr_read_b32 v233, a151\n\t"
"v_cvt_pk_bf16_f32 v224, v224, v225\n\t"
"v_cvt_pk_bf16_f32 v228, v228, v229\n\t"
"v_pk_fma_f32 v[230:231], v[232:233], s[56:57], v[230:231]\n\t"
"v_pk_fma_f32 v[232:233], v[230:231], s[58:59], v[232:233] neg_lo:[0,0,1] neg_hi:[0,0,1]\n\t"
"v_pk_fma_f32 v[234:235], v[86:87], s[60:61], v[230:231]\n\t"
"v_pk_fma_f32 v[86:87], v[230:231], s[60:61], v[86:87]\n\t"
"v_pk_fma_f32 v[230:231], v[186:187], s[62:63], v[232:233]\n\t"
"v_pk_fma_f32 v[186:187], v[232:233], s[62:63], v[186:187]\n\t"
"v_mfma_f32_16x16x32_bf16 v[68:71], a[252:255], a[172:175], v[68:71]\n\t"
"ds_read_b128 a[172:175], v255 offset:5120\n\t"
"v_accvgpr_write_b32 a144, v224\n\t"
"v_mfma_f32_16x16x32_bf16 v[68:71], a[232:235], a[176:179], v[68:71]\n\t"
"v_accvgpr_write_b32 a146, v228\n\t"
"v_accvgpr_read_b32 v236, a152\n\t"
"v_mfma_f32_16x16x32_bf16 v[68:71], a[236:239], a[180:183], v[68:71]\n\t"
"ds_write_b64 v250, a[104:105] offset:0\n\t"
"v_accvgpr_read_b32 v238, a156\n\t"
"v_accvgpr_read_b32 v237, a153\n\t"
"v_accvgpr_read_b32 v239, a157\n\t"
"v_cvt_pk_bf16_f32 v230, v230, v231\n\t"
"v_cvt_pk_bf16_f32 v234, v234, v235\n\t"
"v_pk_fma_f32 v[236:237], v[238:239], s[56:57], v[236:237]\n\t"
"v_pk_fma_f32 v[238:239], v[236:237], s[58:59], v[238:239] neg_lo:[0,0,1] neg_hi:[0,0,1]\n\t"
"v_pk_fma_f32 v[98:99], v[88:89], s[60:61], v[236:237]\n\t"
"v_pk_fma_f32 v[88:89], v[236:237], s[60:61], v[88:89]\n\t"
"v_pk_fma_f32 v[236:237], v[188:189], s[62:63], v[238:239]\n\t"
"v_pk_fma_f32 v[188:189], v[238:239], s[62:63], v[188:189]\n\t"
"v_mfma_f32_16x16x32_bf16 v[72:75], a[252:255], a[176:179], v[72:75]\n\t"
"ds_read_b128 a[176:179], v255 offset:8192\n\t"
"v_accvgpr_write_b32 a145, v230\n\t"
"v_mfma_f32_16x16x32_bf16 v[72:75], a[232:235], a[180:183], v[72:75]\n\t"
"v_accvgpr_write_b32 a147, v234\n\t"
"v_accvgpr_read_b32 v224, a154\n\t"
"v_mfma_f32_16x16x32_bf16 v[72:75], a[236:239], a[184:187], v[72:75]\n\t"
"buffer_load_dwordx4 v[240:243], v252, s[8:11], s20 offen offset:1024 sc1\n\t"
"v_accvgpr_read_b32 v226, a158\n\t"
"v_accvgpr_read_b32 v225, a155\n\t"
"v_accvgpr_read_b32 v227, a159\n\t"
"v_cvt_pk_bf16_f32 v236, v236, v237\n\t"
"v_cvt_pk_bf16_f32 v98, v98, v99\n\t"
"v_pk_fma_f32 v[224:225], v[226:227], s[56:57], v[224:225]\n\t"
"v_pk_fma_f32 v[226:227], v[224:225], s[58:59], v[226:227] neg_lo:[0,0,1] neg_hi:[0,0,1]\n\t"
"v_pk_fma_f32 v[228:229], v[90:91], s[60:61], v[224:225]\n\t"
"v_pk_fma_f32 v[90:91], v[224:225], s[60:61], v[90:91]\n\t"
"v_pk_fma_f32 v[224:225], v[190:191], s[62:63], v[226:227]\n\t"
"v_pk_fma_f32 v[190:191], v[226:227], s[62:63], v[190:191]\n\t"
"v_mfma_f32_16x16x32_bf16 v[76:79], a[252:255], a[180:183], v[76:79]\n\t"
"ds_read_b128 a[180:183], v255 offset:11264\n\t"
"v_accvgpr_write_b32 a152, v236\n\t"
"v_mfma_f32_16x16x32_bf16 v[76:79], a[232:235], a[184:187], v[76:79]\n\t"
"v_accvgpr_write_b32 a154, v98\n\t"
"v_accvgpr_read_b32 v230, a160\n\t"
"v_mfma_f32_16x16x32_bf16 v[76:79], a[236:239], a[188:191], v[76:79]\n\t"
"buffer_load_dwordx4 a[140:143], v252, s[8:11], s20 offen offset:2048 sc1\n\t"
"v_accvgpr_read_b32 v232, a164\n\t"
"v_accvgpr_read_b32 v231, a161\n\t"
"v_accvgpr_read_b32 v233, a165\n\t"
"v_cvt_pk_bf16_f32 v224, v224, v225\n\t"
"v_cvt_pk_bf16_f32 v228, v228, v229\n\t"
"s_mov_b32 s20, 798720\n\t"
"v_pk_fma_f32 v[230:231], v[232:233], s[56:57], v[230:231]\n\t"
"v_pk_fma_f32 v[232:233], v[230:231], s[58:59], v[232:233] neg_lo:[0,0,1] neg_hi:[0,0,1]\n\t"
"v_pk_fma_f32 v[234:235], v[92:93], s[60:61], v[230:231]\n\t"
"v_pk_fma_f32 v[92:93], v[230:231], s[60:61], v[92:93]\n\t"
"v_pk_fma_f32 v[230:231], v[192:193], s[62:63], v[232:233]\n\t"
"v_pk_fma_f32 v[192:193], v[232:233], s[62:63], v[192:193]\n\t"
"v_mfma_f32_16x16x32_bf16 v[80:83], a[252:255], a[184:187], v[80:83]\n\t"
"ds_read_b128 a[184:187], v255 offset:14336\n\t"
"v_accvgpr_write_b32 a153, v224\n\t"
"v_mfma_f32_16x16x32_bf16 v[80:83], a[232:235], a[188:191], v[80:83]\n\t"
"v_accvgpr_write_b32 a155, v228\n\t"
"v_accvgpr_read_b32 v236, a162\n\t"
"v_mfma_f32_16x16x32_bf16 v[80:83], a[236:239], a[192:195], v[80:83]\n\t"
"buffer_load_dwordx4 a[148:151], v252, s[8:11], s20 offen offset:0 sc1\n\t"
"v_accvgpr_read_b32 v238, a166\n\t"
"v_accvgpr_read_b32 v237, a163\n\t"
"v_accvgpr_read_b32 v239, a167\n\t"
"v_cvt_pk_bf16_f32 v230, v230, v231\n\t"
"v_cvt_pk_bf16_f32 v234, v234, v235\n\t"
"v_pk_fma_f32 v[236:237], v[238:239], s[56:57], v[236:237]\n\t"
"v_pk_fma_f32 v[238:239], v[236:237], s[58:59], v[238:239] neg_lo:[0,0,1] neg_hi:[0,0,1]\n\t"
"v_pk_fma_f32 v[98:99], v[94:95], s[60:61], v[236:237]\n\t"
"v_pk_fma_f32 v[94:95], v[236:237], s[60:61], v[94:95]\n\t"
"v_pk_fma_f32 v[236:237], v[194:195], s[62:63], v[238:239]\n\t"
"v_pk_fma_f32 v[194:195], v[238:239], s[62:63], v[194:195]\n\t"
"v_mfma_f32_16x16x32_bf16 v[84:87], a[252:255], a[188:191], v[84:87]\n\t"
"ds_read_b128 a[188:191], v255 offset:17408\n\t"
"v_accvgpr_write_b32 a160, v230\n\t"
"v_mfma_f32_16x16x32_bf16 v[84:87], a[232:235], a[192:195], v[84:87]\n\t"
"v_accvgpr_write_b32 a162, v234\n\t"
"v_cvt_pk_bf16_f32 v236, v236, v237\n\t"
"v_mfma_f32_16x16x32_bf16 v[84:87], a[236:239], a[196:199], v[84:87]\n\t"
"buffer_load_dwordx4 a[224:227], v252, s[8:11], s20 offen offset:1024 sc1\n\t"
"v_cvt_pk_bf16_f32 v98, v98, v99\n\t"
"v_mfma_f32_16x16x32_bf16 v[88:91], a[252:255], a[192:195], v[88:91]\n\t"
"ds_read_b128 a[192:195], v255 offset:20480\n\t"
"v_accvgpr_write_b32 a161, v236\n\t"
"v_mfma_f32_16x16x32_bf16 v[88:91], a[232:235], a[196:199], v[88:91]\n\t"
"v_accvgpr_write_b32 a163, v98\n\t"
"v_mfma_f32_16x16x32_bf16 v[88:91], a[236:239], a[200:203], v[88:91]\n\t"
"buffer_load_dwordx4 a[228:231], v252, s[8:11], s20 offen offset:2048 sc1\n\t"
"ds_write_b128 v249, v[200:203] offset:6144\n\t"
"s_mov_b32 s20, 884736\n\t"
"v_mfma_f32_16x16x32_bf16 v[92:95], a[252:255], a[196:199], v[92:95]\n\t"
"buffer_load_dwordx4 a[244:247], v252, s[8:11], s20 offen offset:0 sc1\n\t"
"v_mfma_f32_16x16x32_bf16 v[92:95], a[232:235], a[200:203], v[92:95]\n\t"
"buffer_load_dwordx4 a[164:167], v252, s[8:11], s20 offen offset:1024 sc1\n\t"
"v_mfma_f32_16x16x32_bf16 v[92:95], a[236:239], a[204:207], v[92:95]\n\t"
"s_waitcnt vmcnt(26) lgkmcnt(5)\n\t"
"ds_write_b64 v250, a[106:107] offset:6144\n\t"
"v_mfma_f32_16x16x32_bf16 v[100:103], a[240:243], a[168:171], v[100:103]\n\t"
"buffer_load_dwordx4 a[248:251], v252, s[8:11], s20 offen offset:2048 sc1\n\t"
"v_mfma_f32_16x16x32_bf16 v[100:103], a[40:43], a[172:175], v[100:103]\n\t"
"s_waitcnt lgkmcnt(5)\n\t"
"ds_read_b128 a[196:199], v255 offset:23552\n\t"
"s_mov_b32 s20, 897024\n\t"
"v_mfma_f32_16x16x32_bf16 v[100:103], a[48:51], a[176:179], v[100:103]\n\t"
"s_add_u32 m0, s25, 512\n\t"
"ds_read_b128 a[200:203], v255 offset:26624\n\t"
"v_mfma_f32_16x16x32_bf16 v[104:107], a[240:243], a[172:175], v[104:107]\n\t"
"s_waitcnt lgkmcnt(6)\n\t"
"ds_read_b128 a[204:207], v255 offset:29696\n\t"
"v_mfma_f32_16x16x32_bf16 v[104:107], a[40:43], a[176:179], v[104:107]\n\t"
"v_mfma_f32_16x16x32_bf16 v[104:107], a[48:51], a[180:183], v[104:107]\n\t"
"s_waitcnt lgkmcnt(0)\n\t"
"s_barrier\n\t"
"v_mfma_f32_16x16x32_bf16 v[108:111], a[240:243], a[176:179], v[108:111]\n\t"
"v_mfma_f32_16x16x32_bf16 v[108:111], a[40:43], a[180:183], v[108:111]\n\t"
"buffer_load_dwordx4 v[236:239], v252, s[8:11], s20 offen offset:0 sc1\n\t"
"v_mfma_f32_16x16x32_bf16 v[108:111], a[48:51], a[184:187], v[108:111]\n\t"
"buffer_load_dwordx4 v[96:99], v252, s[8:11], s20 offen offset:1024 sc1\n\t"
"v_mfma_f32_16x16x32_bf16 v[112:115], a[240:243], a[180:183], v[112:115]\n\t"
"buffer_load_dwordx4 v254, s[12:15], 0 offen offset:0 nt lds\n\t"
"v_mfma_f32_16x16x32_bf16 v[112:115], a[40:43], a[184:187], v[112:115]\n\t"
"buffer_load_dwordx4 a[232:235], v252, s[8:11], s20 offen offset:2048 sc1\n\t"
"v_mfma_f32_16x16x32_bf16 v[112:115], a[48:51], a[188:191], v[112:115]\n\t"
"s_add_u32 m0, s24, 512\n\t"
"s_add_u32 s28, s21, 0\n\t"
"ds_write_b128 v249, v[204:207] offset:12288\n\t"
"v_mfma_f32_16x16x32_bf16 v[116:119], a[240:243], a[184:187], v[116:119]\n\t"
"buffer_load_dwordx4 v247, s[12:15], s28 offen offset:0 nt lds\n\t"
"v_mfma_f32_16x16x32_bf16 v[116:119], a[40:43], a[188:191], v[116:119]\n\t"
"s_add_u32 m0, s24, 6656\n\t"
"s_add_u32 s28, s21, 196608\n\t"
"v_mfma_f32_16x16x32_bf16 v[116:119], a[48:51], a[192:195], v[116:119]\n\t"
"buffer_load_dwordx4 v247, s[12:15], s28 offen offset:0 nt lds\n\t"
"s_add_u32 m0, s24, 12800\n\t"
"s_add_u32 s28, s21, 393216\n\t"
"v_mfma_f32_16x16x32_bf16 v[120:123], a[240:243], a[188:191], v[120:123]\n\t"
"buffer_load_dwordx4 v247, s[12:15], s28 offen offset:0 nt lds\n\t"
"v_mfma_f32_16x16x32_bf16 v[120:123], a[40:43], a[192:195], v[120:123]\n\t"
"s_add_u32 m0, s24, 20480\n\t"
"s_add_u32 s28, s21, 589824\n\t"
"v_mfma_f32_16x16x32_bf16 v[120:123], a[48:51], a[196:199], v[120:123]\n\t"
"buffer_load_dwordx4 v247, s[12:15], s28 offen offset:0 nt lds\n\t"
"v_mfma_f32_16x16x32_bf16 v[124:127], a[240:243], a[192:195], v[124:127]\n\t"
"v_mfma_f32_16x16x32_bf16 v[124:127], a[40:43], a[196:199], v[124:127]\n\t"
"v_mfma_f32_16x16x32_bf16 v[124:127], a[48:51], a[200:203], v[124:127]\n\t"
"ds_write_b64 v250, a[112:113] offset:12288\n\t"
"v_mfma_f32_16x16x32_bf16 v[128:131], a[240:243], a[196:199], v[128:131]\n\t"
"s_waitcnt vmcnt(32)\n\t"
"v_mfma_f32_16x16x32_bf16 v[128:131], a[40:43], a[200:203], v[128:131]\n\t"
"v_mfma_f32_16x16x32_bf16 v[128:131], a[48:51], a[204:207], v[128:131]\n\t"
"ds_write_b128 v249, v[208:211] offset:18432\n\t"
"v_mfma_f32_16x16x32_bf16 v[132:135], a[52:55], a[168:171], v[132:135]\n\t"
"v_mfma_f32_16x16x32_bf16 v[132:135], a[44:47], a[172:175], v[132:135]\n\t"
"v_mfma_f32_16x16x32_bf16 v[132:135], a[60:63], a[176:179], v[132:135]\n\t"
"ds_write_b64 v250, a[114:115] offset:18432\n\t"
"v_mfma_f32_16x16x32_bf16 v[136:139], a[52:55], a[172:175], v[136:139]\n\t"
"v_mfma_f32_16x16x32_bf16 v[136:139], a[44:47], a[176:179], v[136:139]\n\t"
"v_mfma_f32_16x16x32_bf16 v[136:139], a[60:63], a[180:183], v[136:139]\n\t"
"v_mfma_f32_16x16x32_bf16 v[140:143], a[52:55], a[176:179], v[140:143]\n\t"
"v_mfma_f32_16x16x32_bf16 v[140:143], a[44:47], a[180:183], v[140:143]\n\t"
"v_mfma_f32_16x16x32_bf16 v[140:143], a[60:63], a[184:187], v[140:143]\n\t"
"s_mov_b32 s20, 909312\n\t"
"v_mfma_f32_16x16x32_bf16 v[144:147], a[52:55], a[180:183], v[144:147]\n\t"
"v_mfma_f32_16x16x32_bf16 v[144:147], a[44:47], a[184:187], v[144:147]\n\t"
"v_mfma_f32_16x16x32_bf16 v[144:147], a[60:63], a[188:191], v[144:147]\n\t"
"v_mfma_f32_16x16x32_bf16 v[148:151], a[52:55], a[184:187], v[148:151]\n\t"
"v_mfma_f32_16x16x32_bf16 v[148:151], a[44:47], a[188:191], v[148:151]\n\t"
"v_mfma_f32_16x16x32_bf16 v[148:151], a[60:63], a[192:195], v[148:151]\n\t"
"buffer_load_dwordx4 a[104:107], v252, s[8:11], s20 offen offset:0 sc1\n\t"
"v_mfma_f32_16x16x32_bf16 v[152:155], a[52:55], a[188:191], v[152:155]\n\t"
"ds_read_b128 a[40:43], v252 offset:0\n\t"
"v_mfma_f32_16x16x32_bf16 v[152:155], a[44:47], a[192:195], v[152:155]\n\t"
"ds_read_b128 a[48:51], v252 offset:1024\n\t"
"v_mfma_f32_16x16x32_bf16 v[152:155], a[60:63], a[196:199], v[152:155]\n\t"
"v_mfma_f32_16x16x32_bf16 v[156:159], a[52:55], a[192:195], v[156:159]\n\t"
"s_add_u32 m0, s24, 26624\n\t"
"s_add_u32 s28, s21, 786432\n\t"
"ds_read_b128 a[56:59], v252 offset:2048\n\t"
"v_mfma_f32_16x16x32_bf16 v[156:159], a[44:47], a[196:199], v[156:159]\n\t"
"v_mfma_f32_16x16x32_bf16 v[156:159], a[60:63], a[200:203], v[156:159]\n\t"
"v_mfma_f32_16x16x32_bf16 v[160:163], a[52:55], a[196:199], v[160:163]\n\t"
"s_waitcnt vmcnt(30)\n\t"
"v_mfma_f32_16x16x32_bf16 v[160:163], a[44:47], a[200:203], v[160:163]\n\t"
"buffer_load_dwordx4 v247, s[12:15], s28 offen offset:0 nt lds\n\t"
"v_mfma_f32_16x16x32_bf16 v[160:163], a[60:63], a[204:207], v[160:163]\n\t"
"s_add_u32 m0, s24, 32768\n\t"
"s_add_u32 s28, s21, 983040\n\t"
"v_mfma_f32_16x16x32_bf16 v[164:167], a[64:67], a[168:171], v[164:167]\n\t"
"buffer_load_dwordx4 v247, s[12:15], s28 offen offset:0 nt lds\n\t"
"v_mfma_f32_16x16x32_bf16 v[164:167], a[68:71], a[172:175], v[164:167]\n\t"
"s_add_u32 m0, s24, 40448\n\t"
"s_add_u32 s28, s21, 1179648\n\t"
"ds_read_b128 a[168:171], v244 offset:24576\n\t"
"v_mfma_f32_16x16x32_bf16 v[164:167], a[72:75], a[176:179], v[164:167]\n\t"
"buffer_load_dwordx4 v247, s[12:15], s28 offen offset:0 nt lds\n\t"
"v_mfma_f32_16x16x32_bf16 v[168:171], a[64:67], a[172:175], v[168:171]\n\t"
"s_add_u32 m0, s24, 46592\n\t"
"s_add_u32 s28, s21, 1376256\n\t"
"ds_read_b128 a[172:175], v244 offset:25088\n\t"
"v_mfma_f32_16x16x32_bf16 v[168:171], a[68:71], a[176:179], v[168:171]\n\t"
"buffer_load_dwordx4 v247, s[12:15], s28 offen offset:0 nt lds\n\t"
"v_mfma_f32_16x16x32_bf16 v[168:171], a[72:75], a[180:183], v[168:171]\n\t"
"s_add_u32 m0, s24, 52736\n\t"
"s_add_u32 s28, s21, 1572864\n\t"
"v_mfma_f32_16x16x32_bf16 v[172:175], a[64:67], a[176:179], v[172:175]\n\t"
"buffer_load_dwordx4 v247, s[12:15], s28 offen offset:0 nt lds\n\t"
"v_mfma_f32_16x16x32_bf16 v[172:175], a[68:71], a[180:183], v[172:175]\n\t"
"ds_read_b128 a[176:179], v244 offset:25600\n\t"
"v_mfma_f32_16x16x32_bf16 v[172:175], a[72:75], a[184:187], v[172:175]\n\t"
"v_mfma_f32_16x16x32_bf16 v[176:179], a[64:67], a[180:183], v[176:179]\n\t"
"ds_read_b128 a[180:183], v244 offset:26112\n\t"
"v_mfma_f32_16x16x32_bf16 v[176:179], a[68:71], a[184:187], v[176:179]\n\t"
"v_mfma_f32_16x16x32_bf16 v[176:179], a[72:75], a[188:191], v[176:179]\n\t"
"s_waitcnt lgkmcnt(4)\n\t"
"s_barrier\n\t"
"v_mfma_f32_16x16x32_bf16 v[180:183], a[64:67], a[184:187], v[180:183]\n\t"
"ds_read_b128 a[184:187], v244 offset:26624\n\t"
"v_mfma_f32_16x16x32_bf16 v[180:183], a[68:71], a[188:191], v[180:183]\n\t"
"v_mfma_f32_16x16x32_bf16 v[180:183], a[72:75], a[192:195], v[180:183]\n\t"
"ds_write_b128 v249, v[212:215] offset:0\n\t"
"v_mfma_f32_16x16x32_bf16 v[184:187], a[64:67], a[188:191], v[184:187]\n\t"
"v_mfma_f32_16x16x32_bf16 v[184:187], a[68:71], a[192:195], v[184:187]\n\t"
"ds_read_b128 a[188:191], v244 offset:27136\n\t"
"v_mfma_f32_16x16x32_bf16 v[184:187], a[72:75], a[196:199], v[184:187]\n\t"
"ds_write_b64 v250, a[120:121] offset:0\n\t"
"v_mfma_f32_16x16x32_bf16 v[188:191], a[64:67], a[192:195], v[188:191]\n\t"
"ds_read_b128 a[192:195], v244 offset:27648\n\t"
"v_mfma_f32_16x16x32_bf16 v[188:191], a[68:71], a[196:199], v[188:191]\n\t"
"v_mfma_f32_16x16x32_bf16 v[188:191], a[72:75], a[200:203], v[188:191]\n\t"
"ds_write_b128 v249, v[216:219] offset:6144\n\t"
"v_mfma_f32_16x16x32_bf16 v[192:195], a[64:67], a[196:199], v[192:195]\n\t"
"s_waitcnt vmcnt(32) lgkmcnt(7)\n\t"
"v_mfma_f32_16x16x32_bf16 v[192:195], a[68:71], a[200:203], v[192:195]\n\t"
"ds_read_b128 a[196:199], v244 offset:28160\n\t"
"v_mfma_f32_16x16x32_bf16 v[192:195], a[72:75], a[204:207], v[192:195]\n\t"
"ds_read_b128 a[200:203], v244 offset:28672\n\t"
"Lst15_%=:\n\t"
"v_mfma_f32_16x16x32_bf16 v[0:3], a[76:79], a[168:171], v[0:3]\n\t"
"s_waitcnt lgkmcnt(8)\n\t"
"ds_read_b128 a[204:207], v244 offset:29184\n\t"
"v_mfma_f32_16x16x32_bf16 v[0:3], a[84:87], a[172:175], v[0:3]\n\t"
"v_mfma_f32_16x16x32_bf16 v[0:3], a[88:91], a[176:179], v[0:3]\n\t"
"ds_write_b64 v250, a[122:123] offset:6144\n\t"
"v_mfma_f32_16x16x32_bf16 v[4:7], a[76:79], a[172:175], v[4:7]\n\t"
"s_waitcnt lgkmcnt(9)\n\t"
"v_mfma_f32_16x16x32_bf16 v[4:7], a[84:87], a[176:179], v[4:7]\n\t"
"v_mfma_f32_16x16x32_bf16 v[4:7], a[88:91], a[180:183], v[4:7]\n\t"
"v_mfma_f32_16x16x32_bf16 v[8:11], a[76:79], a[176:179], v[8:11]\n\t"
"s_waitcnt lgkmcnt(7)\n\t"
"v_mfma_f32_16x16x32_bf16 v[8:11], a[84:87], a[180:183], v[8:11]\n\t"
"v_mfma_f32_16x16x32_bf16 v[8:11], a[88:91], a[184:187], v[8:11]\n\t"
"v_mfma_f32_16x16x32_bf16 v[12:15], a[76:79], a[180:183], v[12:15]\n\t"
"s_waitcnt lgkmcnt(5)\n\t"
"v_mfma_f32_16x16x32_bf16 v[12:15], a[84:87], a[184:187], v[12:15]\n\t"
"v_mfma_f32_16x16x32_bf16 v[12:15], a[88:91], a[188:191], v[12:15]\n\t"
"v_mfma_f32_16x16x32_bf16 v[16:19], a[76:79], a[184:187], v[16:19]\n\t"
"s_waitcnt lgkmcnt(3)\n\t"
"v_mfma_f32_16x16x32_bf16 v[16:19], a[84:87], a[188:191], v[16:19]\n\t"
"v_mfma_f32_16x16x32_bf16 v[16:19], a[88:91], a[192:195], v[16:19]\n\t"
"ds_read_b128 a[64:67], v252 offset:12288\n\t"
"v_mfma_f32_16x16x32_bf16 v[20:23], a[76:79], a[188:191], v[20:23]\n\t"
"s_waitcnt lgkmcnt(3)\n\t"
"v_mfma_f32_16x16x32_bf16 v[20:23], a[84:87], a[192:195], v[20:23]\n\t"
"ds_read_b128 a[72:75], v252 offset:13312\n\t"
"v_mfma_f32_16x16x32_bf16 v[20:23], a[88:91], a[196:199], v[20:23]\n\t"
"v_mfma_f32_16x16x32_bf16 v[24:27], a[76:79], a[192:195], v[24:27]\n\t"
"s_waitcnt lgkmcnt(3)\n\t"
"ds_read_b128 a[80:83], v252 offset:14336\n\t"
"v_mfma_f32_16x16x32_bf16 v[24:27], a[84:87], a[196:199], v[24:27]\n\t"
"v_mfma_f32_16x16x32_bf16 v[24:27], a[88:91], a[200:203], v[24:27]\n\t"
"v_mfma_f32_16x16x32_bf16 v[28:31], a[76:79], a[196:199], v[28:31]\n\t"
"s_waitcnt vmcnt(29)\n\t"
"v_mfma_f32_16x16x32_bf16 v[28:31], a[84:87], a[200:203], v[28:31]\n\t"
"v_mfma_f32_16x16x32_bf16 v[28:31], a[88:91], a[204:207], v[28:31]\n\t"
"v_mfma_f32_16x16x32_bf16 v[32:35], a[92:95], a[168:171], v[32:35]\n\t"
"v_mfma_f32_16x16x32_bf16 v[32:35], a[96:99], a[172:175], v[32:35]\n\t"
"v_mfma_f32_16x16x32_bf16 v[32:35], a[100:103], a[176:179], v[32:35]\n\t"
"v_mfma_f32_16x16x32_bf16 v[36:39], a[92:95], a[172:175], v[36:39]\n\t"
"v_mfma_f32_16x16x32_bf16 v[36:39], a[96:99], a[176:179], v[36:39]\n\t"
"v_mfma_f32_16x16x32_bf16 v[36:39], a[100:103], a[180:183], v[36:39]\n\t"
"v_mfma_f32_16x16x32_bf16 v[40:43], a[92:95], a[176:179], v[40:43]\n\t"
"v_mfma_f32_16x16x32_bf16 v[40:43], a[96:99], a[180:183], v[40:43]\n\t"
"v_mfma_f32_16x16x32_bf16 v[40:43], a[100:103], a[184:187], v[40:43]\n\t"
"v_mfma_f32_16x16x32_bf16 v[44:47], a[92:95], a[180:183], v[44:47]\n\t"
"v_mfma_f32_16x16x32_bf16 v[44:47], a[96:99], a[184:187], v[44:47]\n\t"
"v_mfma_f32_16x16x32_bf16 v[44:47], a[100:103], a[188:191], v[44:47]\n\t"
"s_waitcnt lgkmcnt(0)\n\t"
"s_barrier\n\t"
"v_mfma_f32_16x16x32_bf16 v[48:51], a[92:95], a[184:187], v[48:51]\n\t"
"v_mfma_f32_16x16x32_bf16 v[48:51], a[96:99], a[188:191], v[48:51]\n\t"
"v_mfma_f32_16x16x32_bf16 v[48:51], a[100:103], a[192:195], v[48:51]\n\t"
"ds_write_b128 v249, a[0:3] offset:12288\n\t"
"v_mfma_f32_16x16x32_bf16 v[52:55], a[92:95], a[188:191], v[52:55]\n\t"
"v_mfma_f32_16x16x32_bf16 v[52:55], a[96:99], a[192:195], v[52:55]\n\t"
"v_mfma_f32_16x16x32_bf16 v[52:55], a[100:103], a[196:199], v[52:55]\n\t"
"ds_write_b64 v250, a[128:129] offset:12288\n\t"
"v_mfma_f32_16x16x32_bf16 v[56:59], a[92:95], a[192:195], v[56:59]\n\t"
"v_mfma_f32_16x16x32_bf16 v[56:59], a[96:99], a[196:199], v[56:59]\n\t"
"v_mfma_f32_16x16x32_bf16 v[56:59], a[100:103], a[200:203], v[56:59]\n\t"
"ds_write_b128 v249, a[4:7] offset:18432\n\t"
"v_mfma_f32_16x16x32_bf16 v[60:63], a[92:95], a[196:199], v[60:63]\n\t"
"s_waitcnt vmcnt(26)\n\t"
"v_mfma_f32_16x16x32_bf16 v[60:63], a[96:99], a[200:203], v[60:63]\n\t"
"v_mfma_f32_16x16x32_bf16 v[60:63], a[100:103], a[204:207], v[60:63]\n\t"
"ds_write_b64 v250, a[130:131] offset:18432\n\t"
"v_mfma_f32_16x16x32_bf16 v[64:67], a[208:211], a[168:171], v[64:67]\n\t"
"ds_read_b128 a[168:171], v244 offset:33792\n\t"
"v_mfma_f32_16x16x32_bf16 v[64:67], a[212:215], a[172:175], v[64:67]\n\t"
"v_mfma_f32_16x16x32_bf16 v[64:67], a[116:119], a[176:179], v[64:67]\n\t"
"buffer_load_dwordx4 a[112:115], v252, s[8:11], s20 offen offset:1024 sc1\n\t"
"v_mfma_f32_16x16x32_bf16 v[68:71], a[208:211], a[172:175], v[68:71]\n\t"
"ds_read_b128 a[172:175], v244 offset:34304\n\t"
"v_mfma_f32_16x16x32_bf16 v[68:71], a[212:215], a[176:179], v[68:71]\n\t"
"v_mfma_f32_16x16x32_bf16 v[68:71], a[116:119], a[180:183], v[68:71]\n\t"
"v_mfma_f32_16x16x32_bf16 v[72:75], a[208:211], a[176:179], v[72:75]\n\t"
"ds_read_b128 a[176:179], v244 offset:34816\n\t"
"v_mfma_f32_16x16x32_bf16 v[72:75], a[212:215], a[180:183], v[72:75]\n\t"
"v_mfma_f32_16x16x32_bf16 v[72:75], a[116:119], a[184:187], v[72:75]\n\t"
"ds_read_b128 a[88:91], v252 offset:0\n\t"
"v_mfma_f32_16x16x32_bf16 v[76:79], a[208:211], a[180:183], v[76:79]\n\t"
"ds_read_b128 a[180:183], v244 offset:35328\n\t"
"v_mfma_f32_16x16x32_bf16 v[76:79], a[212:215], a[184:187], v[76:79]\n\t"
"ds_read_b128 a[96:99], v252 offset:1024\n\t"
"v_mfma_f32_16x16x32_bf16 v[76:79], a[116:119], a[188:191], v[76:79]\n\t"
"v_mfma_f32_16x16x32_bf16 v[80:83], a[208:211], a[184:187], v[80:83]\n\t"
"ds_read_b128 a[184:187], v244 offset:35840\n\t"
"v_mfma_f32_16x16x32_bf16 v[80:83], a[212:215], a[188:191], v[80:83]\n\t"
"ds_read_b128 a[108:111], v252 offset:2048\n\t"
"v_mfma_f32_16x16x32_bf16 v[80:83], a[116:119], a[192:195], v[80:83]\n\t"
"v_mfma_f32_16x16x32_bf16 v[84:87], a[208:211], a[188:191], v[84:87]\n\t"
"ds_read_b128 a[188:191], v244 offset:36352\n\t"
"v_mfma_f32_16x16x32_bf16 v[84:87], a[212:215], a[192:195], v[84:87]\n\t"
"v_mfma_f32_16x16x32_bf16 v[84:87], a[116:119], a[196:199], v[84:87]\n\t"
"v_mfma_f32_16x16x32_bf16 v[88:91], a[208:211], a[192:195], v[88:91]\n\t"
"ds_read_b128 a[192:195], v244 offset:36864\n\t"
"v_mfma_f32_16x16x32_bf16 v[88:91], a[212:215], a[196:199], v[88:91]\n\t"
"v_mfma_f32_16x16x32_bf16 v[88:91], a[116:119], a[200:203], v[88:91]\n\t"
"v_mfma_f32_16x16x32_bf16 v[92:95], a[208:211], a[196:199], v[92:95]\n\t"
"s_waitcnt vmcnt(24) lgkmcnt(7)\n\t"
"ds_read_b128 a[196:199], v244 offset:37376\n\t"
"v_mfma_f32_16x16x32_bf16 v[92:95], a[212:215], a[200:203], v[92:95]\n\t"
"ds_read_b128 a[200:203], v244 offset:37888\n\t"
"v_mfma_f32_16x16x32_bf16 v[92:95], a[116:119], a[204:207], v[92:95]\n\t"
"ds_read_b128 a[204:207], v244 offset:38400\n\t"
"v_mfma_f32_16x16x32_bf16 v[100:103], a[216:219], a[168:171], v[100:103]\n\t"
"s_waitcnt lgkmcnt(8)\n\t"
"v_mfma_f32_16x16x32_bf16 v[100:103], a[124:127], a[172:175], v[100:103]\n\t"
"v_mfma_f32_16x16x32_bf16 v[100:103], a[132:135], a[176:179], v[100:103]\n\t"
"v_mfma_f32_16x16x32_bf16 v[104:107], a[216:219], a[172:175], v[104:107]\n\t"
"s_waitcnt lgkmcnt(6)\n\t"
"v_mfma_f32_16x16x32_bf16 v[104:107], a[124:127], a[176:179], v[104:107]\n\t"
"v_mfma_f32_16x16x32_bf16 v[104:107], a[132:135], a[180:183], v[104:107]\n\t"
"s_waitcnt lgkmcnt(0)\n\t"
"s_barrier\n\t"
"v_mfma_f32_16x16x32_bf16 v[108:111], a[216:219], a[176:179], v[108:111]\n\t"
"v_mfma_f32_16x16x32_bf16 v[108:111], a[124:127], a[180:183], v[108:111]\n\t"
"v_mfma_f32_16x16x32_bf16 v[108:111], a[132:135], a[184:187], v[108:111]\n\t"
"ds_write_b128 v249, a[8:11] offset:0\n\t"
"v_mfma_f32_16x16x32_bf16 v[112:115], a[216:219], a[180:183], v[112:115]\n\t"
"v_mfma_f32_16x16x32_bf16 v[112:115], a[124:127], a[184:187], v[112:115]\n\t"
"buffer_load_dwordx4 a[44:47], v252, s[8:11], s20 offen offset:2048 sc1\n\t"
"v_mfma_f32_16x16x32_bf16 v[112:115], a[132:135], a[188:191], v[112:115]\n\t"
"s_mov_b32 s20, 1069056\n\t"
"ds_write_b64 v250, a[136:137] offset:0\n\t"
"v_mfma_f32_16x16x32_bf16 v[116:119], a[216:219], a[184:187], v[116:119]\n\t"
"v_mfma_f32_16x16x32_bf16 v[116:119], a[124:127], a[188:191], v[116:119]\n\t"
"v_mfma_f32_16x16x32_bf16 v[116:119], a[132:135], a[192:195], v[116:119]\n\t"
"buffer_load_dwordx4 a[60:63], v252, s[8:11], s20 offen offset:0 sc1\n\t"
"v_mfma_f32_16x16x32_bf16 v[120:123], a[216:219], a[188:191], v[120:123]\n\t"
"v_mfma_f32_16x16x32_bf16 v[120:123], a[124:127], a[192:195], v[120:123]\n\t"
"v_mfma_f32_16x16x32_bf16 v[120:123], a[132:135], a[196:199], v[120:123]\n\t"
"ds_write_b128 v249, a[12:15] offset:6144\n\t"
"v_mfma_f32_16x16x32_bf16 v[124:127], a[216:219], a[192:195], v[124:127]\n\t"
"buffer_load_dwordx4 a[68:71], v252, s[8:11], s20 offen offset:1024 sc1\n\t"
"v_mfma_f32_16x16x32_bf16 v[124:127], a[124:127], a[196:199], v[124:127]\n\t"
"v_mfma_f32_16x16x32_bf16 v[124:127], a[132:135], a[200:203], v[124:127]\n\t"
"ds_write_b64 v250, a[138:139] offset:6144\n\t"
"v_mfma_f32_16x16x32_bf16 v[128:131], a[216:219], a[196:199], v[128:131]\n\t"
"s_waitcnt vmcnt(24)\n\t"
"v_mfma_f32_16x16x32_bf16 v[128:131], a[124:127], a[200:203], v[128:131]\n\t"
"buffer_load_dwordx4 a[76:79], v252, s[8:11], s20 offen offset:2048 sc1\n\t"
"v_mfma_f32_16x16x32_bf16 v[128:131], a[132:135], a[204:207], v[128:131]\n\t"
"s_mov_b32 s20, 1081344\n\t"
"v_mfma_f32_16x16x32_bf16 v[132:135], a[220:223], a[168:171], v[132:135]\n\t"
"v_mfma_f32_16x16x32_bf16 v[132:135], v[240:243], a[172:175], v[132:135]\n\t"
"v_mfma_f32_16x16x32_bf16 v[132:135], a[140:143], a[176:179], v[132:135]\n\t"
"buffer_load_dwordx4 a[132:135], v252, s[8:11], s20 offen offset:0 sc1\n\t"
"v_mfma_f32_16x16x32_bf16 v[136:139], a[220:223], a[172:175], v[136:139]\n\t"
"v_mfma_f32_16x16x32_bf16 v[136:139], v[240:243], a[176:179], v[136:139]\n\t"
"v_mfma_f32_16x16x32_bf16 v[136:139], a[140:143], a[180:183], v[136:139]\n\t"
"v_mfma_f32_16x16x32_bf16 v[140:143], a[220:223], a[176:179], v[140:143]\n\t"
"buffer_load_dwordx4 a[92:95], v252, s[8:11], s20 offen offset:1024 sc1\n\t"
"v_mfma_f32_16x16x32_bf16 v[140:143], v[240:243], a[180:183], v[140:143]\n\t"
"v_mfma_f32_16x16x32_bf16 v[140:143], a[140:143], a[184:187], v[140:143]\n\t"
"v_mfma_f32_16x16x32_bf16 v[144:147], a[220:223], a[180:183], v[144:147]\n\t"
"v_mfma_f32_16x16x32_bf16 v[144:147], v[240:243], a[184:187], v[144:147]\n\t"
"buffer_load_dwordx4 a[100:103], v252, s[8:11], s20 offen offset:2048 sc1\n\t"
"v_mfma_f32_16x16x32_bf16 v[144:147], a[140:143], a[188:191], v[144:147]\n\t"
"s_mov_b32 s20, 1093632\n\t"
"v_mfma_f32_16x16x32_bf16 v[148:151], a[220:223], a[184:187], v[148:151]\n\t"
"v_mfma_f32_16x16x32_bf16 v[148:151], v[240:243], a[188:191], v[148:151]\n\t"
"v_mfma_f32_16x16x32_bf16 v[148:151], a[140:143], a[192:195], v[148:151]\n\t"
"buffer_load_dwordx4 v[200:203], v252, s[8:11], s20 offen offset:0 sc1\n\t"
"v_mfma_f32_16x16x32_bf16 v[152:155], a[220:223], a[188:191], v[152:155]\n\t"
"ds_read_b128 a[116:119], v252 offset:12288\n\t"
"v_mfma_f32_16x16x32_bf16 v[152:155], v[240:243], a[192:195], v[152:155]\n\t"
"ds_read_b128 a[124:127], v252 offset:13312\n\t"
"v_mfma_f32_16x16x32_bf16 v[152:155], a[140:143], a[196:199], v[152:155]\n\t"
"v_mfma_f32_16x16x32_bf16 v[156:159], a[220:223], a[192:195], v[156:159]\n\t"
"buffer_load_dwordx4 a[120:123], v252, s[8:11], s20 offen offset:1024 sc1\n\t"
"v_mfma_f32_16x16x32_bf16 v[156:159], v[240:243], a[196:199], v[156:159]\n\t"
"ds_read_b128 a[156:159], v252 offset:14336\n\t"
"v_mfma_f32_16x16x32_bf16 v[156:159], a[140:143], a[200:203], v[156:159]\n\t"
"v_mfma_f32_16x16x32_bf16 v[160:163], a[220:223], a[196:199], v[160:163]\n\t"
"s_waitcnt vmcnt(27)\n\t"
"v_mfma_f32_16x16x32_bf16 v[160:163], v[240:243], a[200:203], v[160:163]\n\t"
"buffer_load_dwordx4 a[128:131], v252, s[8:11], s20 offen offset:2048 sc1\n\t"
"v_mfma_f32_16x16x32_bf16 v[160:163], a[140:143], a[204:207], v[160:163]\n\t"
"s_mov_b32 s20, 1179648\n\t"
"v_mfma_f32_16x16x32_bf16 v[164:167], a[148:151], a[168:171], v[164:167]\n\t"
"ds_read_b128 a[168:171], v255 offset:0\n\t"
"v_mfma_f32_16x16x32_bf16 v[164:167], a[224:227], a[172:175], v[164:167]\n\t"
"v_mfma_f32_16x16x32_bf16 v[164:167], a[228:231], a[176:179], v[164:167]\n\t"
"buffer_load_dwordx4 a[140:143], v252, s[8:11], s20 offen offset:0 sc1\n\t"
"v_mfma_f32_16x16x32_bf16 v[168:171], a[148:151], a[172:175], v[168:171]\n\t"
"ds_read_b128 a[172:175], v255 offset:3072\n\t"
"v_mfma_f32_16x16x32_bf16 v[168:171], a[224:227], a[176:179], v[168:171]\n\t"
"v_mfma_f32_16x16x32_bf16 v[168:171], a[228:231], a[180:183], v[168:171]\n\t"
"v_mfma_f32_16x16x32_bf16 v[172:175], a[148:151], a[176:179], v[172:175]\n\t"
"buffer_load_dwordx4 v[208:211], v252, s[8:11], s20 offen offset:1024 sc1\n\t"
"v_mfma_f32_16x16x32_bf16 v[172:175], a[224:227], a[180:183], v[172:175]\n\t"
"ds_read_b128 a[176:179], v255 offset:6144\n\t"
"v_mfma_f32_16x16x32_bf16 v[172:175], a[228:231], a[184:187], v[172:175]\n\t"
"v_mfma_f32_16x16x32_bf16 v[176:179], a[148:151], a[180:183], v[176:179]\n\t"
"buffer_load_dwordx4 v[216:219], v252, s[8:11], s20 offen offset:2048 sc1\n\t"
"v_mfma_f32_16x16x32_bf16 v[176:179], a[224:227], a[184:187], v[176:179]\n\t"
"s_mov_b32 s20, 1191936\n\t"
"ds_read_b128 a[180:183], v255 offset:9216\n\t"
"v_mfma_f32_16x16x32_bf16 v[176:179], a[228:231], a[188:191], v[176:179]\n\t"
"s_waitcnt lgkmcnt(4)\n\t"
"s_barrier\n\t"
"v_mfma_f32_16x16x32_bf16 v[180:183], a[148:151], a[184:187], v[180:183]\n\t"
"ds_read_b128 a[184:187], v255 offset:12288\n\t"
"v_mfma_f32_16x16x32_bf16 v[180:183], a[224:227], a[188:191], v[180:183]\n\t"
"buffer_load_dwordx4 a[4:7], v252, s[8:11], s20 offen offset:0 sc1\n\t"
"v_mfma_f32_16x16x32_bf16 v[180:183], a[228:231], a[192:195], v[180:183]\n\t"
"ds_write_b128 v249, a[16:19] offset:12288\n\t"
"v_mfma_f32_16x16x32_bf16 v[184:187], a[148:151], a[188:191], v[184:187]\n\t"
"buffer_load_dwordx4 a[12:15], v252, s[8:11], s20 offen offset:1024 sc1\n\t"
"v_mfma_f32_16x16x32_bf16 v[184:187], a[224:227], a[192:195], v[184:187]\n\t"
"ds_read_b128 a[188:191], v255 offset:15360\n\t"
"v_mfma_f32_16x16x32_bf16 v[184:187], a[228:231], a[196:199], v[184:187]\n\t"
"ds_write_b64 v250, a[144:145] offset:12288\n\t"
"v_mfma_f32_16x16x32_bf16 v[188:191], a[148:151], a[192:195], v[188:191]\n\t"
"ds_read_b128 a[192:195], v255 offset:18432\n\t"
"v_mfma_f32_16x16x32_bf16 v[188:191], a[224:227], a[196:199], v[188:191]\n\t"
"buffer_load_dwordx4 a[208:211], v252, s[8:11], s20 offen offset:2048 sc1\n\t"
"v_mfma_f32_16x16x32_bf16 v[188:191], a[228:231], a[200:203], v[188:191]\n\t"
"s_mov_b32 s20, 1204224\n\t"
"ds_write_b128 v249, a[20:23] offset:18432\n\t"
"v_mfma_f32_16x16x32_bf16 v[192:195], a[148:151], a[196:199], v[192:195]\n\t"
"s_waitcnt vmcnt(31) lgkmcnt(7)\n\t"
"v_mfma_f32_16x16x32_bf16 v[192:195], a[224:227], a[200:203], v[192:195]\n\t"
"ds_read_b128 a[196:199], v255 offset:21504\n\t"
"v_mfma_f32_16x16x32_bf16 v[192:195], a[228:231], a[204:207], v[192:195]\n\t"
"buffer_load_dwordx4 a[212:215], v252, s[8:11], s20 offen offset:0 sc1\n\t"
"Lst16_%=:\n\t"
"v_mfma_f32_16x16x32_bf16 v[0:3], a[244:247], a[168:171], v[0:3]\n\t"
"s_waitcnt lgkmcnt(7)\n\t"
"ds_read_b128 a[200:203], v255 offset:24576\n\t"
"v_mfma_f32_16x16x32_bf16 v[0:3], a[164:167], a[172:175], v[0:3]\n\t"
"ds_read_b128 a[204:207], v255 offset:27648\n\t"
"v_mfma_f32_16x16x32_bf16 v[0:3], a[248:251], a[176:179], v[0:3]\n\t"
"ds_write_b64 v250, a[146:147] offset:18432\n\t"
"v_mfma_f32_16x16x32_bf16 v[4:7], a[244:247], a[172:175], v[4:7]\n\t"
"buffer_load_dwordx4 a[216:219], v252, s[8:11], s20 offen offset:1024 sc1\n\t"
"v_mfma_f32_16x16x32_bf16 v[4:7], a[164:167], a[176:179], v[4:7]\n\t"
"s_waitcnt lgkmcnt(9)\n\t"
"v_mfma_f32_16x16x32_bf16 v[4:7], a[248:251], a[180:183], v[4:7]\n\t"
"v_mfma_f32_16x16x32_bf16 v[8:11], a[244:247], a[176:179], v[8:11]\n\t"
"s_waitcnt lgkmcnt(7)\n\t"
"v_mfma_f32_16x16x32_bf16 v[8:11], a[164:167], a[180:183], v[8:11]\n\t"
"buffer_load_dwordx4 a[220:223], v252, s[8:11], s20 offen offset:2048 sc1\n\t"
"v_mfma_f32_16x16x32_bf16 v[8:11], a[248:251], a[184:187], v[8:11]\n\t"
"s_mov_b32 s20, 1363968\n\t"
"v_mfma_f32_16x16x32_bf16 v[12:15], a[244:247], a[180:183], v[12:15]\n\t"
"s_waitcnt lgkmcnt(5)\n\t"
"v_mfma_f32_16x16x32_bf16 v[12:15], a[164:167], a[184:187], v[12:15]\n\t"
"v_mfma_f32_16x16x32_bf16 v[12:15], a[248:251], a[188:191], v[12:15]\n\t"
"buffer_load_dwordx4 a[224:227], v252, s[8:11], s20 offen offset:0 sc1\n\t"
"v_mfma_f32_16x16x32_bf16 v[16:19], a[244:247], a[184:187], v[16:19]\n\t"
"s_waitcnt lgkmcnt(3)\n\t"
"v_mfma_f32_16x16x32_bf16 v[16:19], a[164:167], a[188:191], v[16:19]\n\t"
"v_mfma_f32_16x16x32_bf16 v[16:19], a[248:251], a[192:195], v[16:19]\n\t"
"ds_read_b128 a[52:55], v252 offset:0\n\t"
"v_mfma_f32_16x16x32_bf16 v[20:23], a[244:247], a[188:191], v[20:23]\n\t"
"buffer_load_dwordx4 a[228:231], v252, s[8:11], s20 offen offset:1024 sc1\n\t"
"v_mfma_f32_16x16x32_bf16 v[20:23], a[164:167], a[192:195], v[20:23]\n\t"
"s_waitcnt lgkmcnt(3)\n\t"
"ds_read_b128 a[84:87], v252 offset:1024\n\t"
"v_mfma_f32_16x16x32_bf16 v[20:23], a[248:251], a[196:199], v[20:23]\n\t"
"v_mfma_f32_16x16x32_bf16 v[24:27], a[244:247], a[192:195], v[24:27]\n\t"
"s_waitcnt lgkmcnt(3)\n\t"
"ds_read_b128 v[196:199], v252 offset:2048\n\t"
"v_mfma_f32_16x16x32_bf16 v[24:27], a[164:167], a[196:199], v[24:27]\n\t"
"buffer_load_dwordx4 a[236:239], v252, s[8:11], s20 offen offset:2048 sc1\n\t"
"v_mfma_f32_16x16x32_bf16 v[24:27], a[248:251], a[200:203], v[24:27]\n\t"
"s_mov_b32 s20, 1376256\n\t"
"v_mfma_f32_16x16x32_bf16 v[28:31], a[244:247], a[196:199], v[28:31]\n\t"
"s_waitcnt vmcnt(33)\n\t"
"v_mfma_f32_16x16x32_bf16 v[28:31], a[164:167], a[200:203], v[28:31]\n\t"
"v_mfma_f32_16x16x32_bf16 v[28:31], a[248:251], a[204:207], v[28:31]\n\t"
"buffer_load_dwordx4 a[240:243], v252, s[8:11], s20 offen offset:0 sc1\n\t"
"v_mfma_f32_16x16x32_bf16 v[32:35], v[236:239], a[168:171], v[32:35]\n\t"
"v_mfma_f32_16x16x32_bf16 v[32:35], v[96:99], a[172:175], v[32:35]\n\t"
"v_mfma_f32_16x16x32_bf16 v[32:35], a[232:235], a[176:179], v[32:35]\n\t"
"buffer_load_dwordx4 a[244:247], v252, s[8:11], s20 offen offset:1024 sc1\n\t"
"v_mfma_f32_16x16x32_bf16 v[36:39], v[236:239], a[172:175], v[36:39]\n\t"
"v_mfma_f32_16x16x32_bf16 v[36:39], v[96:99], a[176:179], v[36:39]\n\t"
"v_mfma_f32_16x16x32_bf16 v[36:39], a[232:235], a[180:183], v[36:39]\n\t"
"v_mfma_f32_16x16x32_bf16 v[40:43], v[236:239], a[176:179], v[40:43]\n\t"
"buffer_load_dwordx4 a[248:251], v252, s[8:11], s20 offen offset:2048 sc1\n\t"
"v_mfma_f32_16x16x32_bf16 v[40:43], v[96:99], a[180:183], v[40:43]\n\t"
"s_mov_b32 s20, 1388544\n\t"
"v_mfma_f32_16x16x32_bf16 v[40:43], a[232:235], a[184:187], v[40:43]\n\t"
"v_mfma_f32_16x16x32_bf16 v[44:47], v[236:239], a[180:183], v[44:47]\n\t"
"v_mfma_f32_16x16x32_bf16 v[44:47], v[96:99], a[184:187], v[44:47]\n\t"
"v_mfma_f32_16x16x32_bf16 v[44:47], a[232:235], a[188:191], v[44:47]\n\t"
"s_waitcnt lgkmcnt(0)\n\t"
"s_barrier\n\t"
"v_mfma_f32_16x16x32_bf16 v[48:51], v[236:239], a[184:187], v[48:51]\n\t"
"v_mfma_f32_16x16x32_bf16 v[48:51], v[96:99], a[188:191], v[48:51]\n\t"
"buffer_load_dwordx4 a[252:255], v252, s[8:11], s20 offen offset:0 sc1\n\t"
"v_mfma_f32_16x16x32_bf16 v[48:51], a[232:235], a[192:195], v[48:51]\n\t"
"buffer_load_dwordx4 a[144:147], v252, s[8:11], s20 offen offset:1024 sc1\n\t"
"v_mfma_f32_16x16x32_bf16 v[52:55], v[236:239], a[188:191], v[52:55]\n\t"
"v_mfma_f32_16x16x32_bf16 v[52:55], v[96:99], a[192:195], v[52:55]\n\t"
"buffer_load_dwordx4 a[20:23], v252, s[8:11], s20 offen offset:2048 sc1\n\t"
"v_mfma_f32_16x16x32_bf16 v[52:55], a[232:235], a[196:199], v[52:55]\n\t"
"s_mov_b32 s20, 221184\n\t"
"ds_write_b128 v249, a[24:27] offset:0\n\t"
"v_mfma_f32_16x16x32_bf16 v[56:59], v[236:239], a[192:195], v[56:59]\n\t"
"v_mfma_f32_16x16x32_bf16 v[56:59], v[96:99], a[196:199], v[56:59]\n\t"
"v_mfma_f32_16x16x32_bf16 v[56:59], a[232:235], a[200:203], v[56:59]\n\t"
"ds_write_b64 v250, a[152:153] offset:0\n\t"
"v_mfma_f32_16x16x32_bf16 v[60:63], v[236:239], a[196:199], v[60:63]\n\t"
"s_waitcnt vmcnt(27)\n\t"
"v_mfma_f32_16x16x32_bf16 v[60:63], v[96:99], a[200:203], v[60:63]\n\t"
"buffer_load_dwordx4 v[96:99], v252, s[8:11], s20 offen offset:0 sc1\n\t"
"v_mfma_f32_16x16x32_bf16 v[60:63], a[232:235], a[204:207], v[60:63]\n\t"
"ds_write_b128 v249, a[28:31] offset:6144\n\t"
"v_mfma_f32_16x16x32_bf16 v[64:67], a[104:107], a[168:171], v[64:67]\n\t"
"v_mfma_f32_16x16x32_bf16 v[64:67], a[112:115], a[172:175], v[64:67]\n\t"
"buffer_load_dwordx4 a[232:235], v252, s[8:11], s20 offen offset:1024 sc1\n\t"
"v_mfma_f32_16x16x32_bf16 v[64:67], a[44:47], a[176:179], v[64:67]\n\t"
"ds_read_b128 a[168:171], v255 offset:1536\n\t"
"v_mfma_f32_16x16x32_bf16 v[68:71], a[104:107], a[172:175], v[68:71]\n\t"
"ds_read_b128 a[172:175], v255 offset:4608\n\t"
"v_mfma_f32_16x16x32_bf16 v[68:71], a[112:115], a[176:179], v[68:71]\n\t"
"v_mfma_f32_16x16x32_bf16 v[68:71], a[44:47], a[180:183], v[68:71]\n\t"
"ds_write_b64 v250, a[154:155] offset:6144\n\t"
"v_mfma_f32_16x16x32_bf16 v[72:75], a[104:107], a[176:179], v[72:75]\n\t"
"ds_read_b128 a[176:179], v255 offset:7680\n\t"
"v_mfma_f32_16x16x32_bf16 v[72:75], a[112:115], a[180:183], v[72:75]\n\t"
"v_mfma_f32_16x16x32_bf16 v[72:75], a[44:47], a[184:187], v[72:75]\n\t"
"ds_read_b128 v[204:207], v252 offset:12288\n\t"
"v_mfma_f32_16x16x32_bf16 v[76:79], a[104:107], a[180:183], v[76:79]\n\t"
"ds_read_b128 a[180:183], v255 offset:10752\n\t"
"v_mfma_f32_16x16x32_bf16 v[76:79], a[112:115], a[184:187], v[76:79]\n\t"
"ds_read_b128 v[212:215], v252 offset:13312\n\t"
"v_mfma_f32_16x16x32_bf16 v[76:79], a[44:47], a[188:191], v[76:79]\n\t"
"v_mfma_f32_16x16x32_bf16 v[80:83], a[104:107], a[184:187], v[80:83]\n\t"
"ds_read_b128 a[184:187], v255 offset:13824\n\t"
"v_mfma_f32_16x16x32_bf16 v[80:83], a[112:115], a[188:191], v[80:83]\n\t"
"ds_read_b128 a[0:3], v252 offset:14336\n\t"
"v_mfma_f32_16x16x32_bf16 v[80:83], a[44:47], a[192:195], v[80:83]\n\t"
"v_mfma_f32_16x16x32_bf16 v[84:87], a[104:107], a[188:191], v[84:87]\n\t"
"ds_read_b128 a[188:191], v255 offset:16896\n\t"
"v_mfma_f32_16x16x32_bf16 v[84:87], a[112:115], a[192:195], v[84:87]\n\t"
"v_mfma_f32_16x16x32_bf16 v[84:87], a[44:47], a[196:199], v[84:87]\n\t"
"v_mfma_f32_16x16x32_bf16 v[88:91], a[104:107], a[192:195], v[88:91]\n\t"
"s_add_u32 s29, s22, 384\n\t"
"ds_read_b128 a[192:195], v255 offset:19968\n\t"
"v_mfma_f32_16x16x32_bf16 v[88:91], a[112:115], a[196:199], v[88:91]\n\t"
"v_mfma_f32_16x16x32_bf16 v[88:91], a[44:47], a[200:203], v[88:91]\n\t"
"v_mfma_f32_16x16x32_bf16 v[92:95], a[104:107], a[196:199], v[92:95]\n\t"
"s_waitcnt vmcnt(26) lgkmcnt(7)\n\t"
"ds_read_b128 a[196:199], v255 offset:23040\n\t"
"v_mfma_f32_16x16x32_bf16 v[92:95], a[112:115], a[200:203], v[92:95]\n\t"
"ds_read_b128 a[200:203], v255 offset:26112\n\t"
"v_mfma_f32_16x16x32_bf16 v[92:95], a[44:47], a[204:207], v[92:95]\n\t"
"ds_read_b128 a[204:207], v255 offset:29184\n\t"
"v_mfma_f32_16x16x32_bf16 v[100:103], a[60:63], a[168:171], v[100:103]\n\t"
"s_waitcnt lgkmcnt(8)\n\t"
"v_mfma_f32_16x16x32_bf16 v[100:103], a[68:71], a[172:175], v[100:103]\n\t"
"v_mfma_f32_16x16x32_bf16 v[100:103], a[76:79], a[176:179], v[100:103]\n\t"
"buffer_store_dwordx4 a[40:43], v248, s[16:19], s29 offen offset:0 nt\n\t"
"v_mfma_f32_16x16x32_bf16 v[104:107], a[60:63], a[172:175], v[104:107]\n\t"
"s_waitcnt lgkmcnt(6)\n\t"
"v_mfma_f32_16x16x32_bf16 v[104:107], a[68:71], a[176:179], v[104:107]\n\t"
"v_mfma_f32_16x16x32_bf16 v[104:107], a[76:79], a[180:183], v[104:107]\n\t"
"s_waitcnt lgkmcnt(0)\n\t"
"s_barrier\n\t"
"v_mfma_f32_16x16x32_bf16 v[108:111], a[60:63], a[176:179], v[108:111]\n\t"
"v_mfma_f32_16x16x32_bf16 v[108:111], a[68:71], a[180:183], v[108:111]\n\t"
"buffer_load_dwordx4 a[152:155], v252, s[8:11], s20 offen offset:2048 sc1\n\t"
"v_mfma_f32_16x16x32_bf16 v[108:111], a[76:79], a[184:187], v[108:111]\n\t"
"s_mov_b32 s20, 233472\n\t"
"ds_write_b128 v249, a[32:35] offset:12288\n\t"
"v_mfma_f32_16x16x32_bf16 v[112:115], a[60:63], a[180:183], v[112:115]\n\t"
"v_mfma_f32_16x16x32_bf16 v[112:115], a[68:71], a[184:187], v[112:115]\n\t"
"buffer_load_dwordx4 a[28:31], v252, s[8:11], s20 offen offset:0 sc1\n\t"
"v_mfma_f32_16x16x32_bf16 v[112:115], a[76:79], a[188:191], v[112:115]\n\t"
"ds_write_b64 v250, a[160:161] offset:12288\n\t"
"v_mfma_f32_16x16x32_bf16 v[116:119], a[60:63], a[184:187], v[116:119]\n\t"
"v_mfma_f32_16x16x32_bf16 v[116:119], a[68:71], a[188:191], v[116:119]\n\t"
"v_mfma_f32_16x16x32_bf16 v[116:119], a[76:79], a[192:195], v[116:119]\n\t"
"buffer_store_dwordx4 a[48:51], v248, s[16:19], s29 offen offset:128 nt\n\t"
"ds_write_b64 v250, a[162:163] offset:18432\n\t"
"v_mfma_f32_16x16x32_bf16 v[120:123], a[60:63], a[188:191], v[120:123]\n\t"
"v_mfma_f32_16x16x32_bf16 v[120:123], a[68:71], a[192:195], v[120:123]\n\t"
"v_mfma_f32_16x16x32_bf16 v[120:123], a[76:79], a[196:199], v[120:123]\n\t"
"ds_write_b128 v249, a[36:39] offset:18432\n\t"
"v_mfma_f32_16x16x32_bf16 v[124:127], a[60:63], a[192:195], v[124:127]\n\t"
"v_mfma_f32_16x16x32_bf16 v[124:127], a[68:71], a[196:199], v[124:127]\n\t"
"v_mfma_f32_16x16x32_bf16 v[124:127], a[76:79], a[200:203], v[124:127]\n\t"
"v_mfma_f32_16x16x32_bf16 v[128:131], a[60:63], a[196:199], v[128:131]\n\t"
"s_waitcnt vmcnt(27)\n\t"
"v_mfma_f32_16x16x32_bf16 v[128:131], a[68:71], a[200:203], v[128:131]\n\t"
"v_mfma_f32_16x16x32_bf16 v[128:131], a[76:79], a[204:207], v[128:131]\n\t"
"v_mfma_f32_16x16x32_bf16 v[132:135], a[132:135], a[168:171], v[132:135]\n\t"
"v_mfma_f32_16x16x32_bf16 v[132:135], a[92:95], a[172:175], v[132:135]\n\t"
"v_mfma_f32_16x16x32_bf16 v[132:135], a[100:103], a[176:179], v[132:135]\n\t"
"buffer_store_dwordx4 a[56:59], v248, s[16:19], s29 offen offset:256 nt\n\t"
"v_mfma_f32_16x16x32_bf16 v[136:139], a[132:135], a[172:175], v[136:139]\n\t"
"v_mfma_f32_16x16x32_bf16 v[136:139], a[92:95], a[176:179], v[136:139]\n\t"
"v_add_u32 v255, 55296, v255\n\t"
"v_mfma_f32_16x16x32_bf16 v[136:139], a[100:103], a[180:183], v[136:139]\n\t"
"v_mfma_f32_16x16x32_bf16 v[140:143], a[132:135], a[176:179], v[140:143]\n\t"
"v_mfma_f32_16x16x32_bf16 v[140:143], a[92:95], a[180:183], v[140:143]\n\t"
"v_mfma_f32_16x16x32_bf16 v[140:143], a[100:103], a[184:187], v[140:143]\n\t"
"s_add_u32 s29, s22, 196992\n\t"
"v_mfma_f32_16x16x32_bf16 v[144:147], a[132:135], a[180:183], v[144:147]\n\t"
"v_mfma_f32_16x16x32_bf16 v[144:147], a[92:95], a[184:187], v[144:147]\n\t"
"global_load_dwordx4 v[240:243], v252, s[48:49] offset:2048\n\t"
"v_mfma_f32_16x16x32_bf16 v[144:147], a[100:103], a[188:191], v[144:147]\n\t"
"v_mfma_f32_16x16x32_bf16 v[148:151], a[132:135], a[184:187], v[148:151]\n\t"
"v_mfma_f32_16x16x32_bf16 v[148:151], a[92:95], a[188:191], v[148:151]\n\t"
"v_mfma_f32_16x16x32_bf16 v[148:151], a[100:103], a[192:195], v[148:151]\n\t"
"buffer_store_dwordx4 a[64:67], v248, s[16:19], s29 offen offset:0 nt\n\t"
"ds_read_b128 a[8:11], v252 offset:0\n\t"
"v_mfma_f32_16x16x32_bf16 v[152:155], a[132:135], a[188:191], v[152:155]\n\t"
"v_mfma_f32_16x16x32_bf16 v[152:155], a[92:95], a[192:195], v[152:155]\n\t"
"global_load_dwordx4 a[136:139], v252, s[46:47] offset:1024\n\t"
"v_mfma_f32_16x16x32_bf16 v[152:155], a[100:103], a[196:199], v[152:155]\n\t"
"ds_read_b128 a[16:19], v252 offset:1024\n\t"
"v_mfma_f32_16x16x32_bf16 v[156:159], a[132:135], a[192:195], v[156:159]\n\t"
"ds_read_b128 a[24:27], v252 offset:2048\n\t"
"v_mfma_f32_16x16x32_bf16 v[156:159], a[92:95], a[196:199], v[156:159]\n\t"
"v_mfma_f32_16x16x32_bf16 v[156:159], a[100:103], a[200:203], v[156:159]\n\t"
"v_mfma_f32_16x16x32_bf16 v[160:163], a[132:135], a[196:199], v[160:163]\n\t"
"s_waitcnt vmcnt(28)\n\t"
"v_mfma_f32_16x16x32_bf16 v[160:163], a[92:95], a[200:203], v[160:163]\n\t"
"v_mfma_f32_16x16x32_bf16 v[160:163], a[100:103], a[204:207], v[160:163]\n\t"
"v_mfma_f32_16x16x32_bf16 v[164:167], v[200:203], a[168:171], v[164:167]\n\t"
"ds_read_b128 a[168:171], v255 offset:0\n\t"
"v_mfma_f32_16x16x32_bf16 v[164:167], a[120:123], a[172:175], v[164:167]\n\t"
"v_mfma_f32_16x16x32_bf16 v[164:167], a[128:131], a[176:179], v[164:167]\n\t"
"buffer_store_dwordx4 a[72:75], v248, s[16:19], s29 offen offset:128 nt\n\t"
"v_mfma_f32_16x16x32_bf16 v[168:171], v[200:203], a[172:175], v[168:171]\n\t"
"ds_read_b128 a[172:175], v255 offset:3072\n\t"
"v_mfma_f32_16x16x32_bf16 v[168:171], a[120:123], a[176:179], v[168:171]\n\t"
"v_mfma_f32_16x16x32_bf16 v[168:171], a[128:131], a[180:183], v[168:171]\n\t"
"v_mfma_f32_16x16x32_bf16 v[172:175], v[200:203], a[176:179], v[172:175]\n\t"
"ds_read_b128 a[176:179], v255 offset:6144\n\t"
"v_mfma_f32_16x16x32_bf16 v[172:175], a[120:123], a[180:183], v[172:175]\n\t"
"v_mfma_f32_16x16x32_bf16 v[172:175], a[128:131], a[184:187], v[172:175]\n\t"
"v_mfma_f32_16x16x32_bf16 v[176:179], v[200:203], a[180:183], v[176:179]\n\t"
"ds_read_b128 a[180:183], v255 offset:9216\n\t"
"v_mfma_f32_16x16x32_bf16 v[176:179], a[120:123], a[184:187], v[176:179]\n\t"
"v_mfma_f32_16x16x32_bf16 v[176:179], a[128:131], a[188:191], v[176:179]\n\t"
"s_waitcnt lgkmcnt(4)\n\t"
"s_barrier\n\t"
"v_mfma_f32_16x16x32_bf16 v[180:183], v[200:203], a[184:187], v[180:183]\n\t"
"ds_read_b128 a[184:187], v255 offset:12288\n\t"
"v_mfma_f32_16x16x32_bf16 v[180:183], a[120:123], a[188:191], v[180:183]\n\t"
"buffer_load_dwordx4 a[160:163], v252, s[8:11], s20 offen offset:1024 sc1\n\t"
"v_mfma_f32_16x16x32_bf16 v[180:183], a[128:131], a[192:195], v[180:183]\n\t"
"v_mfma_f32_16x16x32_bf16 v[184:187], v[200:203], a[188:191], v[184:187]\n\t"
"ds_read_b128 a[188:191], v255 offset:15360\n\t"
"v_mfma_f32_16x16x32_bf16 v[184:187], a[120:123], a[192:195], v[184:187]\n\t"
"buffer_load_dwordx4 a[36:39], v252, s[8:11], s20 offen offset:2048 sc1\n\t"
"v_mfma_f32_16x16x32_bf16 v[184:187], a[128:131], a[196:199], v[184:187]\n\t"
"buffer_store_dwordx4 a[80:83], v248, s[16:19], s29 offen offset:256 nt\n\t"
"s_waitcnt vmcnt(6)\n\t"
"v_add_u32 v240, 59904, v240\n\t"
"v_add_u32 v241, 59904, v241\n\t"
"s_mov_b32 s20, 245760\n\t"
"v_mfma_f32_16x16x32_bf16 v[188:191], v[200:203], a[192:195], v[188:191]\n\t"
"ds_read_b128 a[192:195], v255 offset:18432\n\t"
"v_mfma_f32_16x16x32_bf16 v[188:191], a[120:123], a[196:199], v[188:191]\n\t"
"v_mfma_f32_16x16x32_bf16 v[188:191], a[128:131], a[200:203], v[188:191]\n\t"
"ds_read_b64_tr_b16 v[236:237], v240 offset:20480\n\t"
"v_mfma_f32_16x16x32_bf16 v[192:195], v[200:203], a[196:199], v[192:195]\n\t"
"buffer_load_dwordx4 v[200:203], v252, s[8:11], s20 offen offset:0 sc1\n\t"
"v_mfma_f32_16x16x32_bf16 v[192:195], a[120:123], a[200:203], v[192:195]\n\t"
"s_waitcnt lgkmcnt(5)\n\t"
"ds_read_b128 a[196:199], v255 offset:21504\n\t"
"s_add_u32 s29, s22, 393600\n\t"
"v_mfma_f32_16x16x32_bf16 v[192:195], a[128:131], a[204:207], v[192:195]\n\t"
"ds_read_b64_tr_b16 v[238:239], v240 offset:22016\n\t"
"Lst17_%=:\n\t"
"v_mfma_f32_16x16x32_bf16 v[0:3], a[140:143], a[168:171], v[0:3]\n\t"
"buffer_load_dwordx4 a[120:123], v252, s[8:11], s20 offen offset:1024 sc1\n\t"
"v_mfma_f32_16x16x32_bf16 v[0:3], v[208:211], a[172:175], v[0:3]\n\t"
"s_waitcnt lgkmcnt(6)\n\t"
"ds_read_b64_tr_b16 v[224:225], v241 offset:20480\n\t"
"v_mfma_f32_16x16x32_bf16 v[0:3], v[216:219], a[176:179], v[0:3]\n\t"
"ds_read_b64_tr_b16 v[226:227], v241 offset:22016\n\t"
"v_mfma_f32_16x16x32_bf16 v[4:7], a[140:143], a[172:175], v[4:7]\n\t"
"s_waitcnt lgkmcnt(7)\n\t"
"ds_read_b128 a[200:203], v255 offset:24576\n\t"
"v_mfma_f32_16x16x32_bf16 v[4:7], v[208:211], a[176:179], v[4:7]\n\t"
"buffer_load_dwordx4 a[128:131], v252, s[8:11], s20 offen offset:2048 sc1\n\t"
"v_mfma_f32_16x16x32_bf16 v[4:7], v[216:219], a[180:183], v[4:7]\n\t"
"ds_read_b128 a[204:207], v255 offset:27648\n\t"
"v_mfma_f32_16x16x32_bf16 v[8:11], a[140:143], a[176:179], v[8:11]\n\t"
"s_waitcnt lgkmcnt(8)\n\t"
"v_cvt_pk_bf16_f32 v0, v0, v1\n\t"
"v_cvt_pk_bf16_f32 v2, v2, v3\n\t"
"v_mfma_f32_16x16x32_bf16 v[8:11], v[208:211], a[180:183], v[8:11]\n\t"
"v_accvgpr_write_b32 a40, v0\n\t"
"v_accvgpr_write_b32 a41, v2\n\t"
"v_mfma_f32_16x16x32_bf16 v[8:11], v[216:219], a[184:187], v[8:11]\n\t"
"v_mfma_f32_16x16x32_bf16 v[12:15], a[140:143], a[180:183], v[12:15]\n\t"
"s_waitcnt vmcnt(7) lgkmcnt(4)\n\t"
"v_cvt_pk_bf16_f32 v4, v4, v5\n\t"
"v_cvt_pk_bf16_f32 v6, v6, v7\n\t"
"v_mfma_f32_16x16x32_bf16 v[12:15], v[208:211], a[184:187], v[12:15]\n\t"
"s_waitcnt lgkmcnt(2)\n\t"
"v_accvgpr_write_b32 a48, v4\n\t"
"v_accvgpr_write_b32 a49, v6\n\t"
"v_mfma_f32_16x16x32_bf16 v[12:15], v[216:219], a[188:191], v[12:15]\n\t"
"buffer_store_dwordx4 a[88:91], v248, s[16:19], s29 offen offset:0 nt\n\t"
"v_mfma_f32_16x16x32_bf16 v[236:239], v[236:239], a[136:139], 0\n\t"
"v_cvt_pk_bf16_f32 v8, v8, v9\n\t"
"v_cvt_pk_bf16_f32 v10, v10, v11\n\t"
"v_mfma_f32_16x16x32_bf16 v[224:227], v[224:227], a[136:139], 0\n\t"
"v_accvgpr_write_b32 a56, v8\n\t"
"v_accvgpr_write_b32 a57, v10\n\t"
"v_mfma_f32_16x16x32_bf16 v[16:19], a[140:143], a[184:187], v[16:19]\n\t"
"v_mfma_f32_16x16x32_bf16 v[16:19], v[208:211], a[188:191], v[16:19]\n\t"
"v_cvt_pk_bf16_f32 v12, v12, v13\n\t"
"v_cvt_pk_bf16_f32 v14, v14, v15\n\t"
"v_mfma_f32_16x16x32_bf16 v[16:19], v[216:219], a[192:195], v[16:19]\n\t"
"ds_read_b64_tr_b16 v[232:233], v240 offset:40448\n\t"
"v_accvgpr_write_b32 a64, v12\n\t"
"v_mfma_f32_16x16x32_bf16 v[20:23], a[140:143], a[188:191], v[20:23]\n\t"
"s_waitcnt lgkmcnt(2)\n\t"
"ds_read_b64_tr_b16 v[234:235], v240 offset:41984\n\t"
"v_cvt_pk_bf16_f32 v236, v236, v237\n\t"
"v_mfma_f32_16x16x32_bf16 v[20:23], v[208:211], a[192:195], v[20:23]\n\t"
"s_mov_b32 s20, 258048\n\t"
"ds_read_b64_tr_b16 v[228:229], v241 offset:40448\n\t"
"v_cvt_pk_bf16_f32 v237, v238, v239\n\t"
"v_mfma_f32_16x16x32_bf16 v[20:23], v[216:219], a[196:199], v[20:23]\n\t"
"ds_read_b64_tr_b16 v[230:231], v241 offset:41984\n\t"
"v_cvt_pk_bf16_f32 v238, v224, v225\n\t"
"v_mfma_f32_16x16x32_bf16 v[24:27], a[140:143], a[192:195], v[24:27]\n\t"
"s_waitcnt lgkmcnt(4)\n\t"
"ds_read_b128 a[32:35], v252 offset:12288\n\t"
"v_cvt_pk_bf16_f32 v239, v226, v227\n\t"
"v_mfma_f32_16x16x32_bf16 v[24:27], v[208:211], a[196:199], v[24:27]\n\t"
"ds_read_b128 a[104:107], v252 offset:13312\n\t"
"v_accvgpr_write_b32 a65, v14\n\t"
"v_mfma_f32_16x16x32_bf16 v[24:27], v[216:219], a[200:203], v[24:27]\n\t"
"buffer_store_dwordx4 a[96:99], v248, s[16:19], s29 offen offset:128 nt\n\t"
"ds_read_b128 a[112:115], v252 offset:14336\n\t"
"v_cvt_pk_bf16_f32 v16, v16, v17\n\t"
"v_cvt_pk_bf16_f32 v18, v18, v19\n\t"
"v_accvgpr_write_b32 a72, v16\n\t"
"v_mfma_f32_16x16x32_bf16 v[28:31], a[140:143], a[196:199], v[28:31]\n\t"
"v_cvt_pk_bf16_f32 v20, v20, v21\n\t"
"v_cvt_pk_bf16_f32 v22, v22, v23\n\t"
"v_mfma_f32_16x16x32_bf16 v[28:31], v[208:211], a[200:203], v[28:31]\n\t"
"v_accvgpr_write_b32 a73, v18\n\t"
"v_accvgpr_write_b32 a80, v20\n\t"
"v_mfma_f32_16x16x32_bf16 v[28:31], v[216:219], a[204:207], v[28:31]\n\t"
"ds_write_b128 v242, v[236:239] offset:30720\n\t"
"v_mfma_f32_16x16x32_bf16 v[32:35], a[4:7], a[168:171], v[32:35]\n\t"
"buffer_load_dwordx4 v[208:211], v252, s[8:11], s20 offen offset:0 sc1\n\t"
"v_mfma_f32_16x16x32_bf16 v[32:35], a[12:15], a[172:175], v[32:35]\n\t"
"buffer_load_dwordx4 v[216:219], v252, s[8:11], s20 offen offset:1024 sc1\n\t"
"v_mfma_f32_16x16x32_bf16 v[32:35], a[208:211], a[176:179], v[32:35]\n\t"
"v_cvt_pk_bf16_f32 v24, v24, v25\n\t"
"v_cvt_pk_bf16_f32 v26, v26, v27\n\t"
"v_mfma_f32_16x16x32_bf16 v[36:39], a[4:7], a[172:175], v[36:39]\n\t"
"s_waitcnt lgkmcnt(6)\n\t"
"v_accvgpr_write_b32 a81, v22\n\t"
"v_cvt_pk_bf16_f32 v28, v28, v29\n\t"
"v_mfma_f32_16x16x32_bf16 v[36:39], a[12:15], a[176:179], v[36:39]\n\t"
"s_waitcnt lgkmcnt(4)\n\t"
"v_cvt_pk_bf16_f32 v30, v30, v31\n\t"
"v_accvgpr_write_b32 a88, v24\n\t"
"v_mfma_f32_16x16x32_bf16 v[36:39], a[208:211], a[180:183], v[36:39]\n\t"
"v_accvgpr_write_b32 a89, v26\n\t"
"v_accvgpr_write_b32 a96, v28\n\t"
"v_mfma_f32_16x16x32_bf16 v[232:235], v[232:235], a[136:139], 0\n\t"
"v_cvt_pk_bf16_f32 v32, v32, v33\n\t"
"v_cvt_pk_bf16_f32 v34, v34, v35\n\t"
"v_mfma_f32_16x16x32_bf16 v[228:231], v[228:231], a[136:139], 0\n\t"
"v_accvgpr_write_b32 a97, v30\n\t"
"v_accvgpr_write_b32 a42, v32\n\t"
"v_mfma_f32_16x16x32_bf16 v[40:43], a[4:7], a[176:179], v[40:43]\n\t"
"v_accvgpr_write_b32 a43, v34\n\t"
"v_mfma_f32_16x16x32_bf16 v[40:43], a[12:15], a[180:183], v[40:43]\n\t"
"v_cvt_pk_bf16_f32 v36, v36, v37\n\t"
"v_cvt_pk_bf16_f32 v38, v38, v39\n\t"
"v_mfma_f32_16x16x32_bf16 v[40:43], a[208:211], a[184:187], v[40:43]\n\t"
"buffer_store_dwordx4 a[108:111], v248, s[16:19], s29 offen offset:256 nt\n\t"
"v_accvgpr_write_b32 a50, v36\n\t"
"v_accvgpr_write_b32 a51, v38\n\t"
"s_add_u32 s29, s22, 590208\n\t"
"v_mfma_f32_16x16x32_bf16 v[44:47], a[4:7], a[180:183], v[44:47]\n\t"
"v_cvt_pk_bf16_f32 v232, v232, v233\n\t"
"v_cvt_pk_bf16_f32 v233, v234, v235\n\t"
"v_mfma_f32_16x16x32_bf16 v[44:47], a[12:15], a[184:187], v[44:47]\n\t"
"v_cvt_pk_bf16_f32 v234, v228, v229\n\t"
"v_mfma_f32_16x16x32_bf16 v[44:47], a[208:211], a[188:191], v[44:47]\n\t"
"s_waitcnt lgkmcnt(1)\n\t"
"s_barrier\n\t"
"v_mfma_f32_16x16x32_bf16 v[48:51], a[4:7], a[184:187], v[48:51]\n\t"
"ds_read_b64_tr_b16 v[224:225], v240 offset:512\n\t"
"v_mfma_f32_16x16x32_bf16 v[48:51], a[12:15], a[188:191], v[48:51]\n\t"
"ds_read_b64_tr_b16 v[226:227], v240 offset:2048\n\t"
"v_cvt_pk_bf16_f32 v235, v230, v231\n\t"
"v_mfma_f32_16x16x32_bf16 v[48:51], a[208:211], a[192:195], v[48:51]\n\t"
"ds_write_b128 v242, v[232:235] offset:32256\n\t"
"v_mfma_f32_16x16x32_bf16 v[52:55], a[4:7], a[188:191], v[52:55]\n\t"
"v_cvt_pk_bf16_f32 v40, v40, v41\n\t"
"v_cvt_pk_bf16_f32 v42, v42, v43\n\t"
"v_mfma_f32_16x16x32_bf16 v[52:55], a[12:15], a[192:195], v[52:55]\n\t"
"ds_read_b64_tr_b16 v[236:237], v241 offset:512\n\t"
"v_mfma_f32_16x16x32_bf16 v[52:55], a[208:211], a[196:199], v[52:55]\n\t"
"ds_read_b64_tr_b16 v[238:239], v241 offset:2048\n\t"
"v_cvt_pk_bf16_f32 v44, v44, v45\n\t"
"v_mfma_f32_16x16x32_bf16 v[56:59], a[4:7], a[192:195], v[56:59]\n\t"
"v_cvt_pk_bf16_f32 v46, v46, v47\n\t"
"v_accvgpr_write_b32 a58, v40\n\t"
"v_mfma_f32_16x16x32_bf16 v[56:59], a[12:15], a[196:199], v[56:59]\n\t"
"v_accvgpr_write_b32 a59, v42\n\t"
"v_cvt_pk_bf16_f32 v48, v48, v49\n\t"
"v_mfma_f32_16x16x32_bf16 v[56:59], a[208:211], a[200:203], v[56:59]\n\t"
"buffer_store_dwordx4 a[116:119], v248, s[16:19], s29 offen offset:0 nt\n\t"
"v_cvt_pk_bf16_f32 v50, v50, v51\n\t"
"v_accvgpr_write_b32 a66, v44\n\t"
"v_accvgpr_write_b32 a67, v46\n\t"
"v_accvgpr_write_b32 a74, v48\n\t"
"v_mfma_f32_16x16x32_bf16 v[60:63], a[4:7], a[196:199], v[60:63]\n\t"
"s_waitcnt lgkmcnt(3)\n\t"
"v_cvt_pk_bf16_f32 v52, v52, v53\n\t"
"v_cvt_pk_bf16_f32 v54, v54, v55\n\t"
"v_mfma_f32_16x16x32_bf16 v[60:63], a[12:15], a[200:203], v[60:63]\n\t"
"s_waitcnt lgkmcnt(0)\n\t"
"v_accvgpr_write_b32 a75, v50\n\t"
"v_accvgpr_write_b32 a82, v52\n\t"
"v_mfma_f32_16x16x32_bf16 v[60:63], a[208:211], a[204:207], v[60:63]\n\t"
"v_accvgpr_write_b32 a83, v54\n\t"
"v_mfma_f32_16x16x32_bf16 v[224:227], v[224:227], a[136:139], 0\n\t"
"buffer_load_dwordx4 a[4:7], v252, s[8:11], s20 offen offset:2048 sc1\n\t"
"s_mov_b32 s20, 270336\n\t"
"v_mfma_f32_16x16x32_bf16 v[236:239], v[236:239], a[136:139], 0\n\t"
"buffer_load_dwordx4 a[12:15], v252, s[8:11], s20 offen offset:0 sc1\n\t"
"v_mfma_f32_16x16x32_bf16 v[64:67], a[212:215], a[168:171], v[64:67]\n\t"
"ds_read_b128 a[168:171], v255 offset:1536\n\t"
"v_subrev_u32 v240, 59904, v240\n\t"
"v_mfma_f32_16x16x32_bf16 v[64:67], a[216:219], a[172:175], v[64:67]\n\t"
"v_subrev_u32 v241, 59904, v241\n\t"
"v_cvt_pk_bf16_f32 v56, v56, v57\n\t"
"v_mfma_f32_16x16x32_bf16 v[64:67], a[220:223], a[176:179], v[64:67]\n\t"
"ds_read_b64_tr_b16 v[228:229], v240 offset:512\n\t"
"v_cvt_pk_bf16_f32 v58, v58, v59\n\t"
"v_mfma_f32_16x16x32_bf16 v[68:71], a[212:215], a[172:175], v[68:71]\n\t"
"ds_read_b64_tr_b16 v[230:231], v240 offset:2048\n\t"
"v_cvt_pk_bf16_f32 v224, v224, v225\n\t"
"v_mfma_f32_16x16x32_bf16 v[68:71], a[216:219], a[176:179], v[68:71]\n\t"
"ds_read_b64_tr_b16 v[232:233], v241 offset:512\n\t"
"v_cvt_pk_bf16_f32 v225, v226, v227\n\t"
"v_mfma_f32_16x16x32_bf16 v[68:71], a[220:223], a[180:183], v[68:71]\n\t"
"buffer_load_dwordx4 a[208:211], v252, s[8:11], s20 offen offset:1024 sc1\n\t"
"v_mfma_f32_16x16x32_bf16 v[72:75], a[212:215], a[176:179], v[72:75]\n\t"
"ds_read_b64_tr_b16 v[234:235], v241 offset:2048\n\t"
"v_cvt_pk_bf16_f32 v226, v236, v237\n\t"
"v_mfma_f32_16x16x32_bf16 v[72:75], a[216:219], a[180:183], v[72:75]\n\t"
"ds_read_b128 a[172:175], v255 offset:4608\n\t"
"v_cvt_pk_bf16_f32 v227, v238, v239\n\t"
"v_mfma_f32_16x16x32_bf16 v[72:75], a[220:223], a[184:187], v[72:75]\n\t"
"buffer_store_dwordx4 a[124:127], v248, s[16:19], s29 offen offset:128 nt\n\t"
"ds_read_b128 a[176:179], v255 offset:7680\n\t"
"v_cvt_pk_bf16_f32 v60, v60, v61\n\t"
"v_cvt_pk_bf16_f32 v62, v62, v63\n\t"
"v_accvgpr_write_b32 a90, v56\n\t"
"v_mfma_f32_16x16x32_bf16 v[76:79], a[212:215], a[180:183], v[76:79]\n\t"
"ds_read_b128 a[180:183], v255 offset:10752\n\t"
"v_accvgpr_write_b32 a91, v58\n\t"
"v_mfma_f32_16x16x32_bf16 v[76:79], a[216:219], a[184:187], v[76:79]\n\t"
"v_accvgpr_write_b32 a98, v60\n\t"
"v_accvgpr_write_b32 a99, v62\n\t"
"v_mfma_f32_16x16x32_bf16 v[76:79], a[220:223], a[188:191], v[76:79]\n\t"
"ds_write_b128 v242, v[224:227] offset:29184\n\t"
"v_mfma_f32_16x16x32_bf16 v[80:83], a[212:215], a[184:187], v[80:83]\n\t"
"v_cvt_pk_bf16_f32 v64, v64, v65\n\t"
"v_cvt_pk_bf16_f32 v66, v66, v67\n\t"
"v_mfma_f32_16x16x32_bf16 v[80:83], a[216:219], a[188:191], v[80:83]\n\t"
"ds_read_b128 a[184:187], v255 offset:13824\n\t"
"v_mfma_f32_16x16x32_bf16 v[80:83], a[220:223], a[192:195], v[80:83]\n\t"
"buffer_store_dwordx4 a[156:159], v248, s[16:19], s29 offen offset:256 nt\n\t"
"v_cvt_pk_bf16_f32 v68, v68, v69\n\t"
"v_cvt_pk_bf16_f32 v70, v70, v71\n\t"
"v_accvgpr_write_b32 a108, v64\n\t"
"v_accvgpr_write_b32 a109, v66\n\t"
"v_mfma_f32_16x16x32_bf16 v[84:87], a[212:215], a[188:191], v[84:87]\n\t"
"s_waitcnt lgkmcnt(7)\n\t"
"ds_read_b128 a[188:191], v255 offset:16896\n\t"
"v_cvt_pk_bf16_f32 v72, v72, v73\n\t"
"v_mfma_f32_16x16x32_bf16 v[84:87], a[216:219], a[192:195], v[84:87]\n\t"
"s_waitcnt lgkmcnt(6)\n\t"
"v_cvt_pk_bf16_f32 v74, v74, v75\n\t"
"v_accvgpr_write_b32 a116, v68\n\t"
"v_mfma_f32_16x16x32_bf16 v[84:87], a[220:223], a[196:199], v[84:87]\n\t"
"v_accvgpr_write_b32 a117, v70\n\t"
"v_cvt_pk_bf16_f32 v76, v76, v77\n\t"
"v_mfma_f32_16x16x32_bf16 v[228:231], v[228:231], a[136:139], 0\n\t"
"v_cvt_pk_bf16_f32 v78, v78, v79\n\t"
"v_accvgpr_write_b32 a124, v72\n\t"
"v_mfma_f32_16x16x32_bf16 v[232:235], v[232:235], a[136:139], 0\n\t"
"v_accvgpr_write_b32 a125, v74\n\t"
"v_cvt_pk_bf16_f32 v80, v80, v81\n\t"
"v_mfma_f32_16x16x32_bf16 v[88:91], a[212:215], a[192:195], v[88:91]\n\t"
"s_add_u32 s29, s22, 786816\n\t"
"ds_read_b128 a[192:195], v255 offset:19968\n\t"
"v_cvt_pk_bf16_f32 v82, v82, v83\n\t"
"v_mfma_f32_16x16x32_bf16 v[88:91], a[216:219], a[196:199], v[88:91]\n\t"
"v_accvgpr_write_b32 a132, v76\n\t"
"v_accvgpr_write_b32 a133, v78\n\t"
"v_mfma_f32_16x16x32_bf16 v[88:91], a[220:223], a[200:203], v[88:91]\n\t"
"ds_read_b64_tr_b16 v[236:237], v240 offset:20480\n\t"
"v_cvt_pk_bf16_f32 v84, v84, v85\n\t"
"v_mfma_f32_16x16x32_bf16 v[92:95], a[212:215], a[196:199], v[92:95]\n\t"
"s_waitcnt lgkmcnt(6)\n\t"
"ds_read_b128 a[196:199], v255 offset:23040\n\t"
"v_cvt_pk_bf16_f32 v228, v228, v229\n\t"
"v_mfma_f32_16x16x32_bf16 v[92:95], a[216:219], a[200:203], v[92:95]\n\t"
"ds_read_b128 a[200:203], v255 offset:26112\n\t"
"v_cvt_pk_bf16_f32 v229, v230, v231\n\t"
"v_mfma_f32_16x16x32_bf16 v[92:95], a[220:223], a[204:207], v[92:95]\n\t"
"ds_read_b128 a[204:207], v255 offset:29184\n\t"
"v_cvt_pk_bf16_f32 v230, v232, v233\n\t"
"v_mfma_f32_16x16x32_bf16 v[100:103], a[224:227], a[168:171], v[100:103]\n\t"
"s_waitcnt lgkmcnt(8)\n\t"
"ds_read_b64_tr_b16 v[238:239], v240 offset:22016\n\t"
"v_cvt_pk_bf16_f32 v231, v234, v235\n\t"
"v_mfma_f32_16x16x32_bf16 v[100:103], a[228:231], a[172:175], v[100:103]\n\t"
"ds_read_b64_tr_b16 v[224:225], v241 offset:20480\n\t"
"v_cvt_pk_bf16_f32 v86, v86, v87\n\t"
"v_mfma_f32_16x16x32_bf16 v[100:103], a[236:239], a[176:179], v[100:103]\n\t"
"buffer_load_dwordx4 a[212:215], v252, s[8:11], s20 offen offset:2048 sc1\n\t"
"s_mov_b32 s20, 282624\n\t"
"v_mfma_f32_16x16x32_bf16 v[104:107], a[224:227], a[172:175], v[104:107]\n\t"
"buffer_load_dwordx4 a[216:219], v252, s[8:11], s20 offen offset:0 sc1\n\t"
"v_mfma_f32_16x16x32_bf16 v[104:107], a[228:231], a[176:179], v[104:107]\n\t"
"s_waitcnt lgkmcnt(8)\n\t"
"ds_read_b64_tr_b16 v[226:227], v241 offset:22016\n\t"
"v_mfma_f32_16x16x32_bf16 v[104:107], a[236:239], a[180:183], v[104:107]\n\t"
"s_waitcnt lgkmcnt(3)\n\t"
"s_barrier\n\t"
"v_mfma_f32_16x16x32_bf16 v[108:111], a[224:227], a[176:179], v[108:111]\n\t"
"v_accvgpr_write_b32 a140, v80\n\t"
"v_mfma_f32_16x16x32_bf16 v[108:111], a[228:231], a[180:183], v[108:111]\n\t"
"buffer_load_dwordx4 a[220:223], v252, s[8:11], s20 offen offset:1024 sc1\n\t"
"v_mfma_f32_16x16x32_bf16 v[108:111], a[236:239], a[184:187], v[108:111]\n\t"
"ds_write_b128 v242, v[228:231] offset:24576\n\t"
"v_mfma_f32_16x16x32_bf16 v[112:115], a[224:227], a[180:183], v[112:115]\n\t"
"s_waitcnt lgkmcnt(3)\n\t"
"v_accvgpr_write_b32 a141, v82\n\t"
"v_accvgpr_write_b32 a148, v84\n\t"
"v_mfma_f32_16x16x32_bf16 v[112:115], a[228:231], a[184:187], v[112:115]\n\t"
"s_waitcnt lgkmcnt(1)\n\t"
"v_accvgpr_write_b32 a149, v86\n\t"
"v_cvt_pk_bf16_f32 v88, v88, v89\n\t"
"v_mfma_f32_16x16x32_bf16 v[112:115], a[236:239], a[188:191], v[112:115]\n\t"
"buffer_store_dwordx4 a[52:55], v248, s[16:19], s29 offen offset:0 nt\n\t"
"ds_write_b64 v250, a[108:109] offset:0\n\t"
"v_cvt_pk_bf16_f32 v90, v90, v91\n\t"
"v_mfma_f32_16x16x32_bf16 v[236:239], v[236:239], a[136:139], 0\n\t"
"ds_write_b128 v249, a[40:43] offset:0\n\t"
"v_mfma_f32_16x16x32_bf16 v[224:227], v[224:227], a[136:139], 0\n\t"
"buffer_store_dwordx4 a[84:87], v248, s[16:19], s29 offen offset:128 nt\n\t"
"v_cvt_pk_bf16_f32 v92, v92, v93\n\t"
"v_cvt_pk_bf16_f32 v94, v94, v95\n\t"
"v_accvgpr_write_b32 a156, v88\n\t"
"v_accvgpr_write_b32 a157, v90\n\t"
"v_mfma_f32_16x16x32_bf16 v[116:119], a[224:227], a[184:187], v[116:119]\n\t"
"v_cvt_pk_bf16_f32 v100, v100, v101\n\t"
"v_cvt_pk_bf16_f32 v102, v102, v103\n\t"
"v_mfma_f32_16x16x32_bf16 v[116:119], a[228:231], a[188:191], v[116:119]\n\t"
"v_accvgpr_write_b32 a164, v92\n\t"
"v_accvgpr_write_b32 a165, v94\n\t"
"v_mfma_f32_16x16x32_bf16 v[116:119], a[236:239], a[192:195], v[116:119]\n\t"
"ds_read_b64_tr_b16 v[232:233], v240 offset:40448\n\t"
"v_cvt_pk_bf16_f32 v104, v104, v105\n\t"
"v_mfma_f32_16x16x32_bf16 v[120:123], a[224:227], a[188:191], v[120:123]\n\t"
"ds_read_b64_tr_b16 v[234:235], v240 offset:41984\n\t"
"v_cvt_pk_bf16_f32 v236, v236, v237\n\t"
"v_mfma_f32_16x16x32_bf16 v[120:123], a[228:231], a[192:195], v[120:123]\n\t"
"ds_read_b64_tr_b16 v[228:229], v241 offset:40448\n\t"
"v_cvt_pk_bf16_f32 v237, v238, v239\n\t"
"v_mfma_f32_16x16x32_bf16 v[120:123], a[236:239], a[196:199], v[120:123]\n\t"
"ds_read_b64_tr_b16 v[230:231], v241 offset:41984\n\t"
"v_cvt_pk_bf16_f32 v238, v224, v225\n\t"
"v_mfma_f32_16x16x32_bf16 v[124:127], a[224:227], a[192:195], v[124:127]\n\t"
"v_cvt_pk_bf16_f32 v239, v226, v227\n\t"
"v_cvt_pk_bf16_f32 v106, v106, v107\n\t"
"v_mfma_f32_16x16x32_bf16 v[124:127], a[228:231], a[196:199], v[124:127]\n\t"
"v_accvgpr_write_b32 a44, v100\n\t"
"v_accvgpr_write_b32 a45, v102\n\t"
"v_mfma_f32_16x16x32_bf16 v[124:127], a[236:239], a[200:203], v[124:127]\n\t"
"ds_write_b128 v242, v[236:239] offset:26112\n\t"
"v_mfma_f32_16x16x32_bf16 v[128:131], a[224:227], a[196:199], v[128:131]\n\t"
"buffer_load_dwordx4 a[224:227], v252, s[8:11], s20 offen offset:2048 sc1\n\t"
"v_mfma_f32_16x16x32_bf16 v[128:131], a[228:231], a[200:203], v[128:131]\n\t"
"s_mov_b32 s20, 516096\n\t"
"v_cvt_pk_bf16_f32 v108, v108, v109\n\t"
"v_cvt_pk_bf16_f32 v110, v110, v111\n\t"
"v_mfma_f32_16x16x32_bf16 v[128:131], a[236:239], a[204:207], v[128:131]\n\t"
"ds_write_b128 v249, a[56:59] offset:12288\n\t"
"v_mfma_f32_16x16x32_bf16 v[132:135], a[240:243], a[168:171], v[132:135]\n\t"
"buffer_load_dwordx4 a[228:231], v252, s[8:11], s20 offen offset:0 sc1\n\t"
"v_mfma_f32_16x16x32_bf16 v[132:135], a[244:247], a[172:175], v[132:135]\n\t"
"v_accvgpr_write_b32 a52, v104\n\t"
"v_accvgpr_write_b32 a53, v106\n\t"
"v_mfma_f32_16x16x32_bf16 v[132:135], a[248:251], a[176:179], v[132:135]\n\t"
"ds_write_b64 v250, a[124:125] offset:12288\n\t"
"v_mfma_f32_16x16x32_bf16 v[136:139], a[240:243], a[172:175], v[136:139]\n\t"
"buffer_load_dwordx4 a[236:239], v252, s[8:11], s20 offen offset:1024 sc1\n\t"
"v_mfma_f32_16x16x32_bf16 v[136:139], a[244:247], a[176:179], v[136:139]\n\t"
"s_waitcnt lgkmcnt(3)\n\t"
"v_cvt_pk_bf16_f32 v112, v112, v113\n\t"
"v_cvt_pk_bf16_f32 v114, v114, v115\n\t"
"v_mfma_f32_16x16x32_bf16 v[136:139], a[248:251], a[180:183], v[136:139]\n\t"
"buffer_store_dwordx4 v[196:199], v248, s[16:19], s29 offen offset:256 nt\n\t"
"ds_write_b64 v250, a[116:117] offset:6144\n\t"
"v_accvgpr_write_b32 a60, v108\n\t"
"v_mfma_f32_16x16x32_bf16 v[232:235], v[232:235], a[136:139], 0\n\t"
"s_add_u32 s29, s22, 983424\n\t"
"ds_write_b128 v249, a[48:51] offset:6144\n\t"
"v_accvgpr_write_b32 a61, v110\n\t"
"v_mfma_f32_16x16x32_bf16 v[228:231], v[228:231], a[136:139], 0\n\t"
"v_accvgpr_write_b32 a68, v112\n\t"
"v_accvgpr_write_b32 a69, v114\n\t"
"v_mfma_f32_16x16x32_bf16 v[140:143], a[240:243], a[176:179], v[140:143]\n\t"
"v_cvt_pk_bf16_f32 v116, v116, v117\n\t"
"v_cvt_pk_bf16_f32 v118, v118, v119\n\t"
"v_mfma_f32_16x16x32_bf16 v[140:143], a[244:247], a[180:183], v[140:143]\n\t"
"v_cvt_pk_bf16_f32 v120, v120, v121\n\t"
"v_cvt_pk_bf16_f32 v122, v122, v123\n\t"
"v_mfma_f32_16x16x32_bf16 v[140:143], a[248:251], a[184:187], v[140:143]\n\t"
"ds_write_b128 v249, a[64:67] offset:18432\n\t"
"v_mfma_f32_16x16x32_bf16 v[144:147], a[240:243], a[180:183], v[144:147]\n\t"
"v_cvt_pk_bf16_f32 v232, v232, v233\n\t"
"v_cvt_pk_bf16_f32 v233, v234, v235\n\t"
"v_mfma_f32_16x16x32_bf16 v[144:147], a[244:247], a[184:187], v[144:147]\n\t"
"v_accvgpr_write_b32 a76, v116\n\t"
"v_accvgpr_write_b32 a77, v118\n\t"
"v_mfma_f32_16x16x32_bf16 v[144:147], a[248:251], a[188:191], v[144:147]\n\t"
"ds_write_b64 v250, a[132:133] offset:18432\n\t"
"v_mfma_f32_16x16x32_bf16 v[148:151], a[240:243], a[184:187], v[148:151]\n\t"
"v_cvt_pk_bf16_f32 v234, v228, v229\n\t"
"v_cvt_pk_bf16_f32 v235, v230, v231\n\t"
"v_mfma_f32_16x16x32_bf16 v[148:151], a[244:247], a[188:191], v[148:151]\n\t"
"v_cvt_pk_bf16_f32 v124, v124, v125\n\t"
"v_cvt_pk_bf16_f32 v126, v126, v127\n\t"
"v_mfma_f32_16x16x32_bf16 v[148:151], a[248:251], a[192:195], v[148:151]\n\t"
"ds_write_b128 v242, v[232:235] offset:27648\n\t"
"v_mfma_f32_16x16x32_bf16 v[152:155], a[240:243], a[188:191], v[152:155]\n\t"
"v_accvgpr_write_b32 a84, v120\n\t"
"v_accvgpr_write_b32 a85, v122\n\t"
"v_mfma_f32_16x16x32_bf16 v[152:155], a[244:247], a[192:195], v[152:155]\n\t"
"v_cvt_pk_bf16_f32 v128, v128, v129\n\t"
"v_cvt_pk_bf16_f32 v130, v130, v131\n\t"
"v_mfma_f32_16x16x32_bf16 v[152:155], a[248:251], a[196:199], v[152:155]\n\t"
"buffer_store_dwordx4 v[204:207], v248, s[16:19], s29 offen offset:0 nt\n\t"
"v_accvgpr_write_b32 a92, v124\n\t"
"v_accvgpr_write_b32 a93, v126\n\t"
"v_cvt_pk_bf16_f32 v132, v132, v133\n\t"
"v_cvt_pk_bf16_f32 v134, v134, v135\n\t"
"v_mfma_f32_16x16x32_bf16 v[156:159], a[240:243], a[192:195], v[156:159]\n\t"
"v_accvgpr_write_b32 a100, v128\n\t"
"v_accvgpr_write_b32 a101, v130\n\t"
"v_mfma_f32_16x16x32_bf16 v[156:159], a[244:247], a[196:199], v[156:159]\n\t"
"v_cvt_pk_bf16_f32 v136, v136, v137\n\t"
"v_cvt_pk_bf16_f32 v138, v138, v139\n\t"
"v_mfma_f32_16x16x32_bf16 v[156:159], a[248:251], a[200:203], v[156:159]\n\t"
"v_accvgpr_write_b32 a46, v132\n\t"
"v_accvgpr_write_b32 a47, v134\n\t"
"v_mfma_f32_16x16x32_bf16 v[160:163], a[240:243], a[196:199], v[160:163]\n\t"
"v_accvgpr_write_b32 a54, v136\n\t"
"v_accvgpr_write_b32 a55, v138\n\t"
"v_mfma_f32_16x16x32_bf16 v[160:163], a[244:247], a[200:203], v[160:163]\n\t"
"v_cvt_pk_bf16_f32 v140, v140, v141\n\t"
"v_cvt_pk_bf16_f32 v142, v142, v143\n\t"
"v_mfma_f32_16x16x32_bf16 v[160:163], a[248:251], a[204:207], v[160:163]\n\t"
"v_cvt_pk_bf16_f32 v144, v144, v145\n\t"
"v_cvt_pk_bf16_f32 v146, v146, v147\n\t"
"v_mfma_f32_16x16x32_bf16 v[164:167], a[252:255], a[168:171], v[164:167]\n\t"
"v_accvgpr_write_b32 a62, v140\n\t"
"v_accvgpr_write_b32 a63, v142\n\t"
"v_mfma_f32_16x16x32_bf16 v[164:167], a[144:147], a[172:175], v[164:167]\n\t"
"v_cvt_pk_bf16_f32 v148, v148, v149\n\t"
"v_cvt_pk_bf16_f32 v150, v150, v151\n\t"
"v_mfma_f32_16x16x32_bf16 v[164:167], a[20:23], a[176:179], v[164:167]\n\t"
"buffer_load_dwordx4 a[240:243], v252, s[8:11], s20 offen offset:2048 sc1\n\t"
"s_mov_b32 s20, 528384\n\t"
"v_mfma_f32_16x16x32_bf16 v[168:171], a[252:255], a[172:175], v[168:171]\n\t"
"buffer_load_dwordx4 a[244:247], v252, s[8:11], s20 offen offset:0 sc1\n\t"
"v_mfma_f32_16x16x32_bf16 v[168:171], a[144:147], a[176:179], v[168:171]\n\t"
"buffer_load_dwordx4 a[248:251], v252, s[8:11], s20 offen offset:1024 sc1\n\t"
"v_mfma_f32_16x16x32_bf16 v[168:171], a[20:23], a[180:183], v[168:171]\n\t"
"buffer_store_dwordx4 v[212:215], v248, s[16:19], s29 offen offset:128 nt\n\t"
"v_accvgpr_write_b32 a70, v144\n\t"
"v_accvgpr_write_b32 a71, v146\n\t"
"v_cvt_pk_bf16_f32 v152, v152, v153\n\t"
"v_cvt_pk_bf16_f32 v154, v154, v155\n\t"
"v_mfma_f32_16x16x32_bf16 v[172:175], a[252:255], a[176:179], v[172:175]\n\t"
"v_accvgpr_write_b32 a78, v148\n\t"
"v_accvgpr_write_b32 a79, v150\n\t"
"v_mfma_f32_16x16x32_bf16 v[172:175], a[144:147], a[180:183], v[172:175]\n\t"
"v_cvt_pk_bf16_f32 v156, v156, v157\n\t"
"v_cvt_pk_bf16_f32 v158, v158, v159\n\t"
"v_mfma_f32_16x16x32_bf16 v[172:175], a[20:23], a[184:187], v[172:175]\n\t"
"v_accvgpr_write_b32 a86, v152\n\t"
"v_accvgpr_write_b32 a87, v154\n\t"
"v_mfma_f32_16x16x32_bf16 v[176:179], a[252:255], a[180:183], v[176:179]\n\t"
"v_cvt_pk_bf16_f32 v160, v160, v161\n\t"
"v_cvt_pk_bf16_f32 v162, v162, v163\n\t"
"v_mfma_f32_16x16x32_bf16 v[176:179], a[144:147], a[184:187], v[176:179]\n\t"
"v_accvgpr_write_b32 a94, v156\n\t"
"v_mfma_f32_16x16x32_bf16 v[176:179], a[20:23], a[188:191], v[176:179]\n\t"
"s_waitcnt lgkmcnt(0)\n\t"
"s_barrier\n\t"
"v_mfma_f32_16x16x32_bf16 v[180:183], a[252:255], a[184:187], v[180:183]\n\t"
"ds_read_b128 a[168:171], v244 offset:24576\n\t"
"v_mfma_f32_16x16x32_bf16 v[180:183], a[144:147], a[188:191], v[180:183]\n\t"
"ds_read_b128 a[172:175], v244 offset:25088\n\t"
"v_accvgpr_write_b32 a95, v158\n\t"
"v_mfma_f32_16x16x32_bf16 v[180:183], a[20:23], a[192:195], v[180:183]\n\t"
"ds_read_b128 a[176:179], v244 offset:25600\n\t"
"v_cvt_pk_bf16_f32 v164, v164, v165\n\t"
"v_mfma_f32_16x16x32_bf16 v[184:187], a[252:255], a[188:191], v[184:187]\n\t"
"ds_read_b128 a[180:183], v244 offset:26112\n\t"
"v_cvt_pk_bf16_f32 v166, v166, v167\n\t"
"v_mfma_f32_16x16x32_bf16 v[184:187], a[144:147], a[192:195], v[184:187]\n\t"
"ds_read_b128 a[184:187], v244 offset:26624\n\t"
"v_accvgpr_write_b32 a102, v160\n\t"
"v_mfma_f32_16x16x32_bf16 v[184:187], a[20:23], a[196:199], v[184:187]\n\t"
"buffer_load_dwordx4 a[48:51], v252, s[8:11], s20 offen offset:2048 sc1\n\t"
"s_mov_b32 s20, 540672\n\t"
"v_mfma_f32_16x16x32_bf16 v[188:191], a[252:255], a[192:195], v[188:191]\n\t"
"ds_read_b128 a[188:191], v244 offset:27136\n\t"
"v_accvgpr_write_b32 a103, v162\n\t"
"v_mfma_f32_16x16x32_bf16 v[188:191], a[144:147], a[196:199], v[188:191]\n\t"
"ds_read_b128 a[192:195], v244 offset:27648\n\t"
"v_cvt_pk_bf16_f32 v168, v168, v169\n\t"
"v_mfma_f32_16x16x32_bf16 v[188:191], a[20:23], a[200:203], v[188:191]\n\t"
"buffer_store_dwordx4 a[0:3], v248, s[16:19], s29 offen offset:256 nt\n\t"
"buffer_load_dwordx4 a[56:59], v252, s[8:11], s20 offen offset:0 sc1\n\t"
"v_cvt_pk_bf16_f32 v170, v170, v171\n\t"
"v_mfma_f32_16x16x32_bf16 v[192:195], a[252:255], a[196:199], v[192:195]\n\t"
"s_waitcnt lgkmcnt(4)\n\t"
"ds_read_b128 a[196:199], v244 offset:28160\n\t"
"v_accvgpr_write_b32 a110, v164\n\t"
"v_mfma_f32_16x16x32_bf16 v[192:195], a[144:147], a[200:203], v[192:195]\n\t"
"ds_read_b128 a[200:203], v244 offset:28672\n\t"
"v_accvgpr_write_b32 a111, v166\n\t"
"v_mfma_f32_16x16x32_bf16 v[192:195], a[20:23], a[204:207], v[192:195]\n\t"
"buffer_load_dwordx4 a[64:67], v252, s[8:11], s20 offen offset:1024 sc1\n\t"
"s_add_u32 s29, s22, 1180032\n\t"
"Lst18_%=:\n\t"
"v_mfma_f32_16x16x32_bf16 v[2:5], v[96:99], a[168:171], 0\n\t"
"buffer_load_dwordx4 a[144:147], v252, s[8:11], s20 offen offset:2048 sc1\n\t"
"v_mfma_f32_16x16x32_bf16 v[2:5], a[232:235], a[172:175], v[2:5]\n\t"
"s_waitcnt lgkmcnt(5)\n\t"
"ds_read_b128 a[204:207], v244 offset:29184\n\t"
"v_cvt_pk_bf16_f32 v172, v172, v173\n\t"
"v_mfma_f32_16x16x32_bf16 v[2:5], a[152:155], a[176:179], v[2:5]\n\t"
"s_mov_b32 s20, 552960\n\t"
"v_cvt_pk_bf16_f32 v174, v174, v175\n\t"
"v_accvgpr_write_b32 a118, v168\n\t"
"v_mfma_f32_16x16x32_bf16 v[6:9], v[96:99], a[172:175], 0\n\t"
"s_waitcnt lgkmcnt(5)\n\t"
"v_accvgpr_write_b32 a119, v170\n\t"
"v_cvt_pk_bf16_f32 v176, v176, v177\n\t"
"v_mfma_f32_16x16x32_bf16 v[6:9], a[232:235], a[176:179], v[6:9]\n\t"
"v_cvt_pk_bf16_f32 v178, v178, v179\n\t"
"v_accvgpr_write_b32 a126, v172\n\t"
"v_mfma_f32_16x16x32_bf16 v[6:9], a[152:155], a[180:183], v[6:9]\n\t"
"buffer_load_dwordx4 a[20:23], v252, s[8:11], s20 offen offset:0 sc1\n\t"
"v_mfma_f32_16x16x32_bf16 v[10:13], v[96:99], a[176:179], 0\n\t"
"buffer_load_dwordx4 a[252:255], v252, s[8:11], s20 offen offset:1024 sc1\n\t"
"v_mfma_f32_16x16x32_bf16 v[10:13], a[232:235], a[180:183], v[10:13]\n\t"
"s_waitcnt lgkmcnt(4)\n\t"
"v_accvgpr_write_b32 a127, v174\n\t"
"v_cvt_pk_bf16_f32 v180, v180, v181\n\t"
"v_mfma_f32_16x16x32_bf16 v[10:13], a[152:155], a[184:187], v[10:13]\n\t"
"buffer_store_dwordx4 a[8:11], v248, s[16:19], s29 offen offset:0 nt\n\t"
"v_cvt_pk_bf16_f32 v182, v182, v183\n\t"
"v_accvgpr_write_b32 a134, v176\n\t"
"v_accvgpr_write_b32 a135, v178\n\t"
"v_cvt_pk_bf16_f32 v184, v184, v185\n\t"
"v_mfma_f32_16x16x32_bf16 v[14:17], v[96:99], a[180:183], 0\n\t"
"s_waitcnt lgkmcnt(3)\n\t"
"v_cvt_pk_bf16_f32 v186, v186, v187\n\t"
"v_accvgpr_write_b32 a142, v180\n\t"
"v_mfma_f32_16x16x32_bf16 v[14:17], a[232:235], a[184:187], v[14:17]\n\t"
"v_accvgpr_write_b32 a143, v182\n\t"
"v_cvt_pk_bf16_f32 v188, v188, v189\n\t"
"v_mfma_f32_16x16x32_bf16 v[14:17], a[152:155], a[188:191], v[14:17]\n\t"
"v_cvt_pk_bf16_f32 v190, v190, v191\n\t"
"v_accvgpr_write_b32 a150, v184\n\t"
"v_mfma_f32_16x16x32_bf16 v[18:21], v[96:99], a[184:187], 0\n\t"
"s_waitcnt lgkmcnt(2)\n\t"
"v_accvgpr_write_b32 a151, v186\n\t"
"v_cvt_pk_bf16_f32 v192, v192, v193\n\t"
"v_mfma_f32_16x16x32_bf16 v[18:21], a[232:235], a[188:191], v[18:21]\n\t"
"v_cvt_pk_bf16_f32 v194, v194, v195\n\t"
"v_accvgpr_write_b32 a158, v188\n\t"
"v_mfma_f32_16x16x32_bf16 v[18:21], a[152:155], a[192:195], v[18:21]\n\t"
"ds_read_b128 v[196:199], v252 offset:0\n\t"
"v_accvgpr_write_b32 a159, v190\n\t"
"v_mfma_f32_16x16x32_bf16 v[22:25], v[96:99], a[188:191], 0\n\t"
"s_waitcnt lgkmcnt(2)\n\t"
"ds_read_b128 a[0:3], v252 offset:12288\n\t"
"v_accvgpr_write_b32 a166, v192\n\t"
"v_mfma_f32_16x16x32_bf16 v[22:25], a[232:235], a[192:195], v[22:25]\n\t"
"ds_read_b128 v[204:207], v252 offset:1024\n\t"
"v_accvgpr_write_b32 a167, v194\n\t"
"v_mfma_f32_16x16x32_bf16 v[22:25], a[152:155], a[196:199], v[22:25]\n\t"
"ds_read_b128 a[8:11], v252 offset:13312\n\t"
"v_mfma_f32_16x16x32_bf16 v[26:29], v[96:99], a[192:195], 0\n\t"
"s_waitcnt lgkmcnt(4)\n\t"
"ds_read_b128 v[212:215], v252 offset:2048\n\t"
"v_mfma_f32_16x16x32_bf16 v[26:29], a[232:235], a[196:199], v[26:29]\n\t"
"ds_read_b128 a[40:43], v252 offset:14336\n\t"
"v_mfma_f32_16x16x32_bf16 v[26:29], a[152:155], a[200:203], v[26:29]\n\t"
"buffer_store_dwordx4 a[16:19], v248, s[16:19], s29 offen offset:128 nt\n\t"
"v_mfma_f32_16x16x32_bf16 v[30:33], v[96:99], a[196:199], 0\n\t"
"s_waitcnt vmcnt(38)\n\t"
"v_mfma_f32_16x16x32_bf16 v[30:33], a[232:235], a[200:203], v[30:33]\n\t"
"buffer_load_dwordx4 a[232:235], v252, s[8:11], s20 offen offset:2048 sc1\n\t"
"v_mfma_f32_16x16x32_bf16 v[30:33], a[152:155], a[204:207], v[30:33]\n\t"
"s_mov_b32 s20, 565248\n\t"
"v_mfma_f32_16x16x32_bf16 v[34:37], a[28:31], a[168:171], 0\n\t"
"v_mfma_f32_16x16x32_bf16 v[34:37], a[160:163], a[172:175], v[34:37]\n\t"
"buffer_load_dwordx4 a[152:155], v252, s[8:11], s20 offen offset:0 sc1\n\t"
"v_mfma_f32_16x16x32_bf16 v[34:37], a[36:39], a[176:179], v[34:37]\n\t"
"buffer_store_dwordx4 a[24:27], v248, s[16:19], s29 offen offset:256 nt\n\t"
"ds_read_b64_tr_b16 v[224:225], v240 offset:1024\n\t"
"ds_read_b64_tr_b16 v[226:227], v240 offset:2560\n\t"
"v_mfma_f32_16x16x32_bf16 v[38:41], a[28:31], a[172:175], 0\n\t"
"v_mfma_f32_16x16x32_bf16 v[38:41], a[160:163], a[176:179], v[38:41]\n\t"
"v_mfma_f32_16x16x32_bf16 v[38:41], a[36:39], a[180:183], v[38:41]\n\t"
"v_mfma_f32_16x16x32_bf16 v[42:45], a[28:31], a[176:179], 0\n\t"
"ds_read_b64_tr_b16 v[236:237], v241 offset:1024\n\t"
"v_mfma_f32_16x16x32_bf16 v[42:45], a[160:163], a[180:183], v[42:45]\n\t"
"ds_read_b64_tr_b16 v[238:239], v241 offset:2560\n\t"
"v_mfma_f32_16x16x32_bf16 v[42:45], a[36:39], a[184:187], v[42:45]\n\t"
"s_add_u32 s29, s22, 1376640\n\t"
"v_mfma_f32_16x16x32_bf16 v[46:49], a[28:31], a[180:183], 0\n\t"
"v_mfma_f32_16x16x32_bf16 v[46:49], a[160:163], a[184:187], v[46:49]\n\t"
"v_mfma_f32_16x16x32_bf16 v[46:49], a[36:39], a[188:191], v[46:49]\n\t"
"s_waitcnt lgkmcnt(4)\n\t"
"s_barrier\n\t"
"v_mfma_f32_16x16x32_bf16 v[50:53], a[28:31], a[184:187], 0\n\t"
"v_mfma_f32_16x16x32_bf16 v[50:53], a[160:163], a[188:191], v[50:53]\n\t"
"v_mfma_f32_16x16x32_bf16 v[50:53], a[36:39], a[192:195], v[50:53]\n\t"
"buffer_store_dwordx4 a[32:35], v248, s[16:19], s29 offen offset:0 nt\n\t"
"ds_write_b64 v250, a[140:141] offset:0\n\t"
"v_mfma_f32_16x16x32_bf16 v[54:57], a[28:31], a[188:191], 0\n\t"
"s_waitcnt lgkmcnt(3)\n\t"
"v_mfma_f32_16x16x32_bf16 v[54:57], a[160:163], a[192:195], v[54:57]\n\t"
"ds_read_b64_tr_b16 v[228:229], v240 offset:20992\n\t"
"v_mfma_f32_16x16x32_bf16 v[54:57], a[36:39], a[196:199], v[54:57]\n\t"
"ds_read_b64_tr_b16 v[230:231], v240 offset:22528\n\t"
"v_mfma_f32_16x16x32_bf16 v[224:227], v[224:227], a[136:139], 0\n\t"
"ds_write_b128 v249, a[72:75] offset:0\n\t"
"v_mfma_f32_16x16x32_bf16 v[58:61], a[28:31], a[192:195], 0\n\t"
"s_waitcnt lgkmcnt(4)\n\t"
"v_mfma_f32_16x16x32_bf16 v[58:61], a[160:163], a[196:199], v[58:61]\n\t"
"ds_read_b64_tr_b16 v[232:233], v241 offset:20992\n\t"
"v_mfma_f32_16x16x32_bf16 v[58:61], a[36:39], a[200:203], v[58:61]\n\t"
"ds_write_b128 v249, a[88:91] offset:12288\n\t"
"v_mfma_f32_16x16x32_bf16 v[236:239], v[236:239], a[136:139], 0\n\t"
"v_mfma_f32_16x16x32_bf16 v[62:65], a[28:31], a[196:199], 0\n\t"
"buffer_load_dwordx4 a[28:31], v252, s[8:11], s20 offen offset:1024 sc1\n\t"
"v_mfma_f32_16x16x32_bf16 v[62:65], a[160:163], a[200:203], v[62:65]\n\t"
"s_waitcnt vmcnt(39)\n\t"
"ds_read_b64_tr_b16 v[234:235], v241 offset:22528\n\t"
"v_cvt_pk_bf16_f32 v224, v224, v225\n\t"
"v_mfma_f32_16x16x32_bf16 v[62:65], a[36:39], a[204:207], v[62:65]\n\t"
"ds_write_b64 v250, a[156:157] offset:12288\n\t"
"v_mfma_f32_16x16x32_bf16 v[66:69], v[200:203], a[168:171], 0\n\t"
"buffer_load_dwordx4 a[160:163], v252, s[8:11], s20 offen offset:2048 sc1\n\t"
"v_mfma_f32_16x16x32_bf16 v[66:69], a[120:123], a[172:175], v[66:69]\n\t"
"s_mov_b32 s20, 577536\n\t"
"ds_read_b128 a[168:171], v244 offset:33792\n\t"
"v_cvt_pk_bf16_f32 v225, v226, v227\n\t"
"v_mfma_f32_16x16x32_bf16 v[66:69], a[128:131], a[176:179], v[66:69]\n\t"
"ds_write_b128 v249, a[80:83] offset:6144\n\t"
"v_mfma_f32_16x16x32_bf16 v[70:73], v[200:203], a[172:175], 0\n\t"
"buffer_load_dwordx4 a[36:39], v252, s[8:11], s20 offen offset:0 sc1\n\t"
"v_mfma_f32_16x16x32_bf16 v[70:73], a[120:123], a[176:179], v[70:73]\n\t"
"s_waitcnt lgkmcnt(7)\n\t"
"ds_read_b128 a[172:175], v244 offset:34304\n\t"
"v_mfma_f32_16x16x32_bf16 v[70:73], a[128:131], a[180:183], v[70:73]\n\t"
"buffer_store_dwordx4 a[104:107], v248, s[16:19], s29 offen offset:128 nt\n\t"
"ds_write_b64 v250, a[148:149] offset:6144\n\t"
"v_cvt_pk_bf16_f32 v226, v236, v237\n\t"
"v_cvt_pk_bf16_f32 v227, v238, v239\n\t"
"v_mfma_f32_16x16x32_bf16 v[228:231], v[228:231], a[136:139], 0\n\t"
"ds_write_b128 v249, a[96:99] offset:18432\n\t"
"v_mfma_f32_16x16x32_bf16 v[74:77], v[200:203], a[176:179], 0\n\t"
"s_waitcnt lgkmcnt(6)\n\t"
"v_mfma_f32_16x16x32_bf16 v[74:77], a[120:123], a[180:183], v[74:77]\n\t"
"ds_read_b128 a[176:179], v244 offset:34816\n\t"
"v_mfma_f32_16x16x32_bf16 v[74:77], a[128:131], a[184:187], v[74:77]\n\t"
"ds_write_b64 v250, a[164:165] offset:18432\n\t"
"v_mfma_f32_16x16x32_bf16 v[232:235], v[232:235], a[136:139], 0\n\t"
"v_mfma_f32_16x16x32_bf16 v[78:81], v[200:203], a[180:183], 0\n\t"
"ds_read_b128 a[180:183], v244 offset:35328\n\t"
"v_cvt_pk_bf16_f32 v228, v228, v229\n\t"
"v_mfma_f32_16x16x32_bf16 v[78:81], a[120:123], a[184:187], v[78:81]\n\t"
"v_cvt_pk_bf16_f32 v229, v230, v231\n\t"
"v_mfma_f32_16x16x32_bf16 v[78:81], a[128:131], a[188:191], v[78:81]\n\t"
"buffer_store_dwordx4 a[112:115], v248, s[16:19], s29 offen offset:256 nt\n\t"
"v_mfma_f32_16x16x32_bf16 v[82:85], v[200:203], a[184:187], 0\n\t"
"ds_read_b128 a[184:187], v244 offset:35840\n\t"
"v_mfma_f32_16x16x32_bf16 v[82:85], a[120:123], a[188:191], v[82:85]\n\t"
"v_cvt_pk_bf16_f32 v230, v232, v233\n\t"
"v_cvt_pk_bf16_f32 v231, v234, v235\n\t"
"v_mfma_f32_16x16x32_bf16 v[82:85], a[128:131], a[192:195], v[82:85]\n\t"
"v_mfma_f32_16x16x32_bf16 v[86:89], v[200:203], a[188:191], 0\n\t"
"ds_read_b128 a[188:191], v244 offset:36352\n\t"
"v_mfma_f32_16x16x32_bf16 v[86:89], a[120:123], a[192:195], v[86:89]\n\t"
"v_mfma_f32_16x16x32_bf16 v[86:89], a[128:131], a[196:199], v[86:89]\n\t"
"v_mfma_f32_16x16x32_bf16 v[90:93], v[200:203], a[192:195], 0\n\t"
"ds_read_b128 a[192:195], v244 offset:36864\n\t"
"v_mfma_f32_16x16x32_bf16 v[90:93], a[120:123], a[196:199], v[90:93]\n\t"
"v_mfma_f32_16x16x32_bf16 v[90:93], a[128:131], a[200:203], v[90:93]\n\t"
"buffer_load_dwordx4 a[112:115], v252, s[8:11], s20 offen offset:1024 sc1\n\t"
"v_mfma_f32_16x16x32_bf16 v[94:97], v[200:203], a[196:199], 0\n\t"
"s_waitcnt vmcnt(37) lgkmcnt(5)\n\t"
"ds_read_b128 a[196:199], v244 offset:37376\n\t"
"v_mfma_f32_16x16x32_bf16 v[94:97], a[120:123], a[200:203], v[94:97]\n\t"
"ds_read_b128 a[200:203], v244 offset:37888\n\t"
"v_mfma_f32_16x16x32_bf16 v[94:97], a[128:131], a[204:207], v[94:97]\n\t"
"ds_read_b128 a[204:207], v244 offset:38400\n\t"
"v_mfma_f32_16x16x32_bf16 v[100:103], v[208:211], a[168:171], 0\n\t"
"buffer_load_dwordx4 v[200:203], v252, s[8:11], s20 offen offset:2048 sc1\n\t"
"v_mfma_f32_16x16x32_bf16 v[100:103], v[216:219], a[172:175], v[100:103]\n\t"
"s_waitcnt lgkmcnt(6)\n\t"
"v_mfma_f32_16x16x32_bf16 v[100:103], a[4:7], a[176:179], v[100:103]\n\t"
"ds_read_b64_tr_b16 v[236:237], v240 offset:40960\n\t"
"v_mfma_f32_16x16x32_bf16 v[104:107], v[208:211], a[172:175], 0\n\t"
"s_waitcnt lgkmcnt(6)\n\t"
"ds_read_b64_tr_b16 v[238:239], v240 offset:42496\n\t"
"v_mfma_f32_16x16x32_bf16 v[104:107], v[216:219], a[176:179], v[104:107]\n\t"
"ds_read_b64_tr_b16 v[232:233], v241 offset:40960\n\t"
"v_mfma_f32_16x16x32_bf16 v[104:107], a[4:7], a[180:183], v[104:107]\n\t"
"s_waitcnt lgkmcnt(3)\n\t"
"s_barrier\n\t"
"v_mfma_f32_16x16x32_bf16 v[108:111], v[208:211], a[176:179], 0\n\t"
"s_add_u32 s29, s23, 0\n\t"
"ds_read_b64_tr_b16 v[234:235], v241 offset:42496\n\t"
"v_mfma_f32_16x16x32_bf16 v[108:111], v[216:219], a[180:183], v[108:111]\n\t"
"v_add_u32 v240, 59904, v240\n\t"
"v_add_u32 v241, 59904, v241\n\t"
"v_mfma_f32_16x16x32_bf16 v[108:111], a[4:7], a[184:187], v[108:111]\n\t"
"ds_write_b128 v242, v[224:227] offset:24576\n\t"
"v_mfma_f32_16x16x32_bf16 v[112:115], v[208:211], a[180:183], 0\n\t"
"v_mfma_f32_16x16x32_bf16 v[112:115], v[216:219], a[184:187], v[112:115]\n\t"
"ds_read_b64_tr_b16 v[224:225], v240 offset:1024\n\t"
"v_mfma_f32_16x16x32_bf16 v[112:115], a[4:7], a[188:191], v[112:115]\n\t"
"ds_write_b128 v242, v[228:231] offset:26112\n\t"
"v_mfma_f32_16x16x32_bf16 v[116:119], v[208:211], a[184:187], 0\n\t"
"v_mfma_f32_16x16x32_bf16 v[116:119], v[216:219], a[188:191], v[116:119]\n\t"
"ds_read_b64_tr_b16 v[226:227], v240 offset:2560\n\t"
"v_mfma_f32_16x16x32_bf16 v[116:119], a[4:7], a[192:195], v[116:119]\n\t"
"buffer_store_dwordx4 v[196:199], v248, s[16:19], s29 offen offset:0 nt\n\t"
"ds_read_b64_tr_b16 v[228:229], v241 offset:1024\n\t"
"ds_read_b64_tr_b16 v[230:231], v241 offset:2560\n\t"
"v_mfma_f32_16x16x32_bf16 v[120:123], v[208:211], a[188:191], 0\n\t"
"s_waitcnt lgkmcnt(8)\n\t"
"v_mfma_f32_16x16x32_bf16 v[120:123], v[216:219], a[192:195], v[120:123]\n\t"
"s_waitcnt lgkmcnt(6)\n\t"
"v_mfma_f32_16x16x32_bf16 v[120:123], a[4:7], a[196:199], v[120:123]\n\t"
"s_mov_b32 s20, 110592\n\t"
"v_mfma_f32_16x16x32_bf16 v[236:239], v[236:239], a[136:139], 0\n\t"
"v_mfma_f32_16x16x32_bf16 v[232:235], v[232:235], a[136:139], 0\n\t"
"v_mfma_f32_16x16x32_bf16 v[124:127], v[208:211], a[192:195], 0\n\t"
"v_mfma_f32_16x16x32_bf16 v[124:127], v[216:219], a[196:199], v[124:127]\n\t"
"v_mfma_f32_16x16x32_bf16 v[124:127], a[4:7], a[200:203], v[124:127]\n\t"
"v_mfma_f32_16x16x32_bf16 v[128:131], v[208:211], a[196:199], 0\n\t"
"buffer_load_dwordx4 v[208:211], v252, s[8:11], s20 offen offset:0 sc1\n\t"
"v_mfma_f32_16x16x32_bf16 v[128:131], v[216:219], a[200:203], v[128:131]\n\t"
"s_waitcnt lgkmcnt(0)\n\t"
"v_cvt_pk_bf16_f32 v236, v236, v237\n\t"
"v_cvt_pk_bf16_f32 v237, v238, v239\n\t"
"v_mfma_f32_16x16x32_bf16 v[128:131], a[4:7], a[204:207], v[128:131]\n\t"
"s_waitcnt vmcnt(35)\n\t"
"v_cvt_pk_bf16_f32 v238, v232, v233\n\t"
"v_cvt_pk_bf16_f32 v239, v234, v235\n\t"
"v_mfma_f32_16x16x32_bf16 v[224:227], v[224:227], a[136:139], 0\n\t"
"ds_write_b128 v242, v[236:239] offset:27648\n\t"
"v_mfma_f32_16x16x32_bf16 v[228:231], v[228:231], a[136:139], 0\n\t"
"buffer_load_dwordx4 v[216:219], v252, s[8:11], s20 offen offset:1024 sc1\n\t"
"v_mfma_f32_16x16x32_bf16 v[132:135], a[12:15], a[168:171], 0\n\t"
"ds_read_b64_tr_b16 v[232:233], v240 offset:20992\n\t"
"v_mfma_f32_16x16x32_bf16 v[132:135], a[208:211], a[172:175], v[132:135]\n\t"
"ds_read_b64_tr_b16 v[234:235], v240 offset:22528\n\t"
"v_mfma_f32_16x16x32_bf16 v[132:135], a[212:215], a[176:179], v[132:135]\n\t"
"buffer_store_dwordx4 v[204:207], v248, s[16:19], s29 offen offset:128 nt\n\t"
"buffer_load_dwordx4 a[4:7], v252, s[8:11], s20 offen offset:2048 sc1\n\t"
"ds_read_b64_tr_b16 v[236:237], v241 offset:20992\n\t"
"v_mfma_f32_16x16x32_bf16 v[136:139], a[12:15], a[172:175], 0\n\t"
"ds_read_b64_tr_b16 v[238:239], v241 offset:22528\n\t"
"v_cvt_pk_bf16_f32 v224, v224, v225\n\t"
"v_mfma_f32_16x16x32_bf16 v[136:139], a[208:211], a[176:179], v[136:139]\n\t"
"v_cvt_pk_bf16_f32 v225, v226, v227\n\t"
"v_cvt_pk_bf16_f32 v226, v228, v229\n\t"
"v_mfma_f32_16x16x32_bf16 v[136:139], a[212:215], a[180:183], v[136:139]\n\t"
"v_cvt_pk_bf16_f32 v227, v230, v231\n\t"
"v_mfma_f32_16x16x32_bf16 v[140:143], a[12:15], a[176:179], 0\n\t"
"ds_read_b64_tr_b16 v[228:229], v240 offset:40960\n\t"
"v_mfma_f32_16x16x32_bf16 v[140:143], a[208:211], a[180:183], v[140:143]\n\t"
"ds_read_b64_tr_b16 v[230:231], v240 offset:42496\n\t"
"v_mfma_f32_16x16x32_bf16 v[140:143], a[212:215], a[184:187], v[140:143]\n\t"
"ds_write_b128 v242, v[224:227] offset:29184\n\t"
"v_mfma_f32_16x16x32_bf16 v[144:147], a[12:15], a[180:183], 0\n\t"
"s_waitcnt lgkmcnt(5)\n\t"
"v_mfma_f32_16x16x32_bf16 v[144:147], a[208:211], a[184:187], v[144:147]\n\t"
"s_waitcnt lgkmcnt(3)\n\t"
"ds_read_b64_tr_b16 v[224:225], v241 offset:40960\n\t"
"v_mfma_f32_16x16x32_bf16 v[144:147], a[212:215], a[188:191], v[144:147]\n\t"
"ds_read_b64_tr_b16 v[226:227], v241 offset:42496\n\t"
"v_mfma_f32_16x16x32_bf16 v[232:235], v[232:235], a[136:139], 0\n\t"
"buffer_store_dwordx4 v[212:215], v248, s[16:19], s29 offen offset:256 nt\n\t"
"v_mfma_f32_16x16x32_bf16 v[236:239], v[236:239], a[136:139], 0\n\t"
"v_mfma_f32_16x16x32_bf16 v[148:151], a[12:15], a[184:187], 0\n\t"
"v_mfma_f32_16x16x32_bf16 v[148:151], a[208:211], a[188:191], v[148:151]\n\t"
"v_mfma_f32_16x16x32_bf16 v[148:151], a[212:215], a[192:195], v[148:151]\n\t"
"s_add_u32 s29, s23, 393216\n\t"
"v_mfma_f32_16x16x32_bf16 v[152:155], a[12:15], a[188:191], 0\n\t"
"s_waitcnt lgkmcnt(3)\n\t"
"v_cvt_pk_bf16_f32 v232, v232, v233\n\t"
"v_cvt_pk_bf16_f32 v233, v234, v235\n\t"
"v_mfma_f32_16x16x32_bf16 v[152:155], a[208:211], a[192:195], v[152:155]\n\t"
"s_waitcnt lgkmcnt(0)\n\t"
"ds_read_b128 v[196:199], v252 offset:0\n\t"
"v_cvt_pk_bf16_f32 v234, v236, v237\n\t"
"v_mfma_f32_16x16x32_bf16 v[152:155], a[212:215], a[196:199], v[152:155]\n\t"
"ds_read_b128 a[16:19], v252 offset:12288\n\t"
"v_cvt_pk_bf16_f32 v235, v238, v239\n\t"
"v_mfma_f32_16x16x32_bf16 v[228:231], v[228:231], a[136:139], 0\n\t"
"s_mov_b32 s20, 122880\n\t"
"ds_write_b128 v242, v[232:235] offset:30720\n\t"
"v_mfma_f32_16x16x32_bf16 v[224:227], v[224:227], a[136:139], 0\n\t"
"v_mfma_f32_16x16x32_bf16 v[156:159], a[12:15], a[192:195], 0\n\t"
"ds_read_b128 v[204:207], v252 offset:1024\n\t"
"v_mfma_f32_16x16x32_bf16 v[156:159], a[208:211], a[196:199], v[156:159]\n\t"
"ds_read_b128 a[24:27], v252 offset:13312\n\t"
"v_mfma_f32_16x16x32_bf16 v[156:159], a[212:215], a[200:203], v[156:159]\n\t"
"buffer_store_dwordx4 a[0:3], v248, s[16:19], s29 offen offset:0 nt\n\t"
"ds_read_b128 v[212:215], v252 offset:2048\n\t"
"ds_read_b128 a[32:35], v252 offset:14336\n\t"
"v_mfma_f32_16x16x32_bf16 v[160:163], a[12:15], a[196:199], 0\n\t"
"s_waitcnt vmcnt(35)\n\t"
"v_cvt_pk_bf16_f32 v228, v228, v229\n\t"
"v_cvt_pk_bf16_f32 v229, v230, v231\n\t"
"v_mfma_f32_16x16x32_bf16 v[160:163], a[208:211], a[200:203], v[160:163]\n\t"
"v_cvt_pk_bf16_f32 v230, v224, v225\n\t"
"v_cvt_pk_bf16_f32 v231, v226, v227\n\t"
"v_mfma_f32_16x16x32_bf16 v[160:163], a[212:215], a[204:207], v[160:163]\n\t"
"ds_write_b128 v242, v[228:231] offset:32256\n\t"
"v_mfma_f32_16x16x32_bf16 v[164:167], a[216:219], a[168:171], 0\n\t"
"buffer_load_dwordx4 a[208:211], v252, s[8:11], s20 offen offset:0 sc1\n\t"
"v_mfma_f32_16x16x32_bf16 v[164:167], a[220:223], a[172:175], v[164:167]\n\t"
"buffer_load_dwordx4 a[212:215], v252, s[8:11], s20 offen offset:1024 sc1\n\t"
"v_mfma_f32_16x16x32_bf16 v[164:167], a[224:227], a[176:179], v[164:167]\n\t"
"v_mfma_f32_16x16x32_bf16 v[168:171], a[216:219], a[172:175], 0\n\t"
"v_mfma_f32_16x16x32_bf16 v[168:171], a[220:223], a[176:179], v[168:171]\n\t"
"v_mfma_f32_16x16x32_bf16 v[168:171], a[224:227], a[180:183], v[168:171]\n\t"
"v_mfma_f32_16x16x32_bf16 v[172:175], a[216:219], a[176:179], 0\n\t"
"buffer_load_dwordx4 v[240:243], v252, s[8:11], s20 offen offset:2048 sc1\n\t"
"v_mfma_f32_16x16x32_bf16 v[172:175], a[220:223], a[180:183], v[172:175]\n\t"
"v_mfma_f32_16x16x32_bf16 v[172:175], a[224:227], a[184:187], v[172:175]\n\t"
"buffer_store_dwordx4 a[8:11], v248, s[16:19], s29 offen offset:128 nt\n\t"
"v_mfma_f32_16x16x32_bf16 v[176:179], a[216:219], a[180:183], 0\n\t"
"v_mfma_f32_16x16x32_bf16 v[176:179], a[220:223], a[184:187], v[176:179]\n\t"
"v_mfma_f32_16x16x32_bf16 v[176:179], a[224:227], a[188:191], v[176:179]\n\t"
"s_waitcnt lgkmcnt(0)\n\t"
"s_barrier\n\t"
"v_mfma_f32_16x16x32_bf16 v[180:183], a[216:219], a[184:187], 0\n\t"
"ds_read_b128 a[168:171], v244 offset:24576\n\t"
"v_mfma_f32_16x16x32_bf16 v[180:183], a[220:223], a[188:191], v[180:183]\n\t"
"ds_read_b128 a[172:175], v244 offset:25088\n\t"
"v_mfma_f32_16x16x32_bf16 v[180:183], a[224:227], a[192:195], v[180:183]\n\t"
"ds_read_b128 a[176:179], v244 offset:25600\n\t"
"v_mfma_f32_16x16x32_bf16 v[184:187], a[216:219], a[188:191], 0\n\t"
"s_mov_b32 s20, 135168\n\t"
"ds_read_b128 a[180:183], v244 offset:26112\n\t"
"v_mfma_f32_16x16x32_bf16 v[184:187], a[220:223], a[192:195], v[184:187]\n\t"
"ds_read_b128 a[184:187], v244 offset:26624\n\t"
"v_mfma_f32_16x16x32_bf16 v[184:187], a[224:227], a[196:199], v[184:187]\n\t"
"ds_read_b128 a[188:191], v244 offset:27136\n\t"
"v_mfma_f32_16x16x32_bf16 v[188:191], a[216:219], a[192:195], 0\n\t"
"ds_read_b128 a[192:195], v244 offset:27648\n\t"
"v_mfma_f32_16x16x32_bf16 v[188:191], a[220:223], a[196:199], v[188:191]\n\t"
"v_mfma_f32_16x16x32_bf16 v[188:191], a[224:227], a[200:203], v[188:191]\n\t"
"ds_write_b128 v249, a[44:47] offset:0\n\t"
"v_mfma_f32_16x16x32_bf16 v[192:195], a[216:219], a[196:199], 0\n\t"
"buffer_load_dwordx4 a[216:219], v252, s[8:11], s20 offen offset:0 sc1\n\t"
"v_mfma_f32_16x16x32_bf16 v[192:195], a[220:223], a[200:203], v[192:195]\n\t"
"s_waitcnt vmcnt(35) lgkmcnt(5)\n\t"
"ds_read_b128 a[196:199], v244 offset:28160\n\t"
"v_mfma_f32_16x16x32_bf16 v[192:195], a[224:227], a[204:207], v[192:195]\n\t"
"ds_write_b128 v249, a[60:63] offset:12288\n\t"
"Lst19_%=:\n\t"
"v_mfma_f32_16x16x32_bf16 v[2:5], a[228:231], a[168:171], v[2:5]\n\t"
"buffer_load_dwordx4 a[220:223], v252, s[8:11], s20 offen offset:1024 sc1\n\t"
"v_mfma_f32_16x16x32_bf16 v[2:5], a[236:239], a[172:175], v[2:5]\n\t"
"s_waitcnt lgkmcnt(6)\n\t"
"ds_read_b128 a[200:203], v244 offset:28672\n\t"
"v_mfma_f32_16x16x32_bf16 v[2:5], a[240:243], a[176:179], v[2:5]\n\t"
"buffer_store_dwordx4 a[40:43], v248, s[16:19], s29 offen offset:256 nt\n\t"
"ds_read_b128 a[204:207], v244 offset:29184\n\t"
"v_mfma_f32_16x16x32_bf16 v[6:9], a[228:231], a[172:175], v[6:9]\n\t"
"s_waitcnt lgkmcnt(7)\n\t"
"s_add_u32 s29, s23, 786432\n\t"
"v_mfma_f32_16x16x32_bf16 v[6:9], a[236:239], a[176:179], v[6:9]\n\t"
"buffer_load_dwordx4 a[224:227], v252, s[8:11], s20 offen offset:2048 sc1\n\t"
"v_mfma_f32_16x16x32_bf16 v[6:9], a[240:243], a[180:183], v[6:9]\n\t"
"ds_write_b64 v250, a[110:111] offset:0\n\t"
"v_mfma_f32_16x16x32_bf16 v[10:13], a[228:231], a[176:179], v[10:13]\n\t"
"s_waitcnt lgkmcnt(7)\n\t"
"v_mfma_f32_16x16x32_bf16 v[10:13], a[236:239], a[180:183], v[10:13]\n\t"
"v_mfma_f32_16x16x32_bf16 v[10:13], a[240:243], a[184:187], v[10:13]\n\t"
"ds_write_b64 v250, a[126:127] offset:12288\n\t"
"v_mfma_f32_16x16x32_bf16 v[14:17], a[228:231], a[180:183], v[14:17]\n\t"
"s_waitcnt lgkmcnt(7)\n\t"
"v_mfma_f32_16x16x32_bf16 v[14:17], a[236:239], a[184:187], v[14:17]\n\t"
"v_mfma_f32_16x16x32_bf16 v[14:17], a[240:243], a[188:191], v[14:17]\n\t"
"buffer_store_dwordx4 v[196:199], v248, s[16:19], s29 offen offset:0 nt\n\t"
"ds_write_b64 v250, a[118:119] offset:6144\n\t"
"v_mfma_f32_16x16x32_bf16 v[18:21], a[228:231], a[184:187], v[18:21]\n\t"
"s_waitcnt lgkmcnt(6)\n\t"
"v_mfma_f32_16x16x32_bf16 v[18:21], a[236:239], a[188:191], v[18:21]\n\t"
"v_mfma_f32_16x16x32_bf16 v[18:21], a[240:243], a[192:195], v[18:21]\n\t"
"ds_write_b128 v249, a[52:55] offset:6144\n\t"
"v_mfma_f32_16x16x32_bf16 v[22:25], a[228:231], a[188:191], v[22:25]\n\t"
"s_waitcnt lgkmcnt(5)\n\t"
"s_mov_b32 s20, 147456\n\t"
"v_mfma_f32_16x16x32_bf16 v[22:25], a[236:239], a[192:195], v[22:25]\n\t"
"v_mfma_f32_16x16x32_bf16 v[22:25], a[240:243], a[196:199], v[22:25]\n\t"
"ds_write_b128 v249, a[68:71] offset:18432\n\t"
"v_mfma_f32_16x16x32_bf16 v[26:29], a[228:231], a[192:195], v[26:29]\n\t"
"s_waitcnt lgkmcnt(5)\n\t"
"v_mfma_f32_16x16x32_bf16 v[26:29], a[236:239], a[196:199], v[26:29]\n\t"
"v_mfma_f32_16x16x32_bf16 v[26:29], a[240:243], a[200:203], v[26:29]\n\t"
"ds_write_b64 v250, a[134:135] offset:18432\n\t"
"v_mfma_f32_16x16x32_bf16 v[30:33], a[228:231], a[196:199], v[30:33]\n\t"
"buffer_load_dwordx4 a[228:231], v252, s[8:11], s20 offen offset:0 sc1\n\t"
"v_mfma_f32_16x16x32_bf16 v[30:33], a[236:239], a[200:203], v[30:33]\n\t"
"s_waitcnt vmcnt(36)\n\t"
"v_mfma_f32_16x16x32_bf16 v[30:33], a[240:243], a[204:207], v[30:33]\n\t"
"buffer_store_dwordx4 v[204:207], v248, s[16:19], s29 offen offset:128 nt\n\t"
"v_mfma_f32_16x16x32_bf16 v[34:37], a[244:247], a[168:171], v[34:37]\n\t"
"v_mfma_f32_16x16x32_bf16 v[34:37], a[248:251], a[172:175], v[34:37]\n\t"
"buffer_load_dwordx4 a[236:239], v252, s[8:11], s20 offen offset:1024 sc1\n\t"
"v_mfma_f32_16x16x32_bf16 v[34:37], a[48:51], a[176:179], v[34:37]\n\t"
"ds_read_b64_tr_b16 v[236:237], v246 offset:512\n\t"
"v_mfma_f32_16x16x32_bf16 v[38:41], a[244:247], a[172:175], v[38:41]\n\t"
"buffer_load_dwordx4 a[240:243], v252, s[8:11], s20 offen offset:2048 sc1\n\t"
"v_mfma_f32_16x16x32_bf16 v[38:41], a[248:251], a[176:179], v[38:41]\n\t"
"ds_read_b64_tr_b16 v[238:239], v246 offset:3584\n\t"
"v_mfma_f32_16x16x32_bf16 v[38:41], a[48:51], a[180:183], v[38:41]\n\t"
"v_mfma_f32_16x16x32_bf16 v[42:45], a[244:247], a[176:179], v[42:45]\n\t"
"v_mfma_f32_16x16x32_bf16 v[42:45], a[248:251], a[180:183], v[42:45]\n\t"
"v_mfma_f32_16x16x32_bf16 v[42:45], a[48:51], a[184:187], v[42:45]\n\t"
"v_mfma_f32_16x16x32_bf16 v[46:49], a[244:247], a[180:183], v[46:49]\n\t"
"ds_read_b64_tr_b16 v[232:233], v246 offset:6656\n\t"
"v_mfma_f32_16x16x32_bf16 v[46:49], a[248:251], a[184:187], v[46:49]\n\t"
"ds_read_b64_tr_b16 v[234:235], v246 offset:9728\n\t"
"v_mfma_f32_16x16x32_bf16 v[46:49], a[48:51], a[188:191], v[46:49]\n\t"
"s_waitcnt lgkmcnt(4)\n\t"
"s_barrier\n\t"
"v_mfma_f32_16x16x32_bf16 v[50:53], a[244:247], a[184:187], v[50:53]\n\t"
"v_mfma_f32_16x16x32_bf16 v[50:53], a[248:251], a[188:191], v[50:53]\n\t"
"v_mfma_f32_16x16x32_bf16 v[50:53], a[48:51], a[192:195], v[50:53]\n\t"
"buffer_store_dwordx4 v[212:215], v248, s[16:19], s29 offen offset:256 nt\n\t"
"v_mfma_f32_16x16x32_bf16 v[54:57], a[244:247], a[188:191], v[54:57]\n\t"
"s_waitcnt lgkmcnt(2)\n\t"
"v_mfma_f32_16x16x32_bf16 v[54:57], a[248:251], a[192:195], v[54:57]\n\t"
"s_mov_b32 s20, 159744\n\t"
"v_mfma_f32_16x16x32_bf16 v[54:57], a[48:51], a[196:199], v[54:57]\n\t"
"v_mfma_f32_16x16x32_bf16 v[236:239], v[236:239], v[220:223], 0\n\t"
"s_add_u32 s29, s23, 1179648\n\t"
"v_mfma_f32_16x16x32_bf16 v[58:61], a[244:247], a[192:195], v[58:61]\n\t"
"ds_read_b64_tr_b16 v[224:225], v246 offset:12800\n\t"
"v_mfma_f32_16x16x32_bf16 v[58:61], a[248:251], a[196:199], v[58:61]\n\t"
"ds_read_b64_tr_b16 v[226:227], v246 offset:15872\n\t"
"v_mfma_f32_16x16x32_bf16 v[58:61], a[48:51], a[200:203], v[58:61]\n\t"
"v_mfma_f32_16x16x32_bf16 v[62:65], a[244:247], a[196:199], v[62:65]\n\t"
"buffer_load_dwordx4 a[244:247], v252, s[8:11], s20 offen offset:0 sc1\n\t"
"v_mfma_f32_16x16x32_bf16 v[62:65], a[248:251], a[200:203], v[62:65]\n\t"
"s_waitcnt vmcnt(37) lgkmcnt(2)\n\t"
"v_cvt_pk_bf16_f32 v236, v236, v237\n\t"
"v_cvt_pk_bf16_f32 v237, v238, v239\n\t"
"v_mfma_f32_16x16x32_bf16 v[62:65], a[48:51], a[204:207], v[62:65]\n\t"
"v_mfma_f32_16x16x32_bf16 v[232:235], v[232:235], v[220:223], 0\n\t"
"buffer_load_dwordx4 a[248:251], v252, s[8:11], s20 offen offset:1024 sc1\n\t"
"v_mfma_f32_16x16x32_bf16 v[66:69], a[56:59], a[168:171], v[66:69]\n\t"
"ds_read_b64_tr_b16 v[228:229], v246 offset:20480\n\t"
"v_mfma_f32_16x16x32_bf16 v[66:69], a[64:67], a[172:175], v[66:69]\n\t"
"ds_read_b64_tr_b16 v[230:231], v246 offset:23552\n\t"
"v_mfma_f32_16x16x32_bf16 v[66:69], a[144:147], a[176:179], v[66:69]\n\t"
"buffer_store_dwordx4 a[16:19], v248, s[16:19], s29 offen offset:0 nt\n\t"
"ds_read_b128 a[168:171], v244 offset:33792\n\t"
"v_mfma_f32_16x16x32_bf16 v[70:73], a[56:59], a[172:175], v[70:73]\n\t"
"ds_read_b128 a[172:175], v244 offset:34304\n\t"
"v_mfma_f32_16x16x32_bf16 v[70:73], a[64:67], a[176:179], v[70:73]\n\t"
"v_cvt_pk_bf16_f32 v232, v232, v233\n\t"
"v_cvt_pk_bf16_f32 v233, v234, v235\n\t"
"v_mfma_f32_16x16x32_bf16 v[70:73], a[144:147], a[180:183], v[70:73]\n\t"
"v_mfma_f32_16x16x32_bf16 v[74:77], a[56:59], a[176:179], v[74:77]\n\t"
"s_waitcnt lgkmcnt(4)\n\t"
"ds_read_b128 a[176:179], v244 offset:34816\n\t"
"v_mfma_f32_16x16x32_bf16 v[74:77], a[64:67], a[180:183], v[74:77]\n\t"
"v_mfma_f32_16x16x32_bf16 v[74:77], a[144:147], a[184:187], v[74:77]\n\t"
"v_mfma_f32_16x16x32_bf16 v[224:227], v[224:227], v[220:223], 0\n\t"
"v_mfma_f32_16x16x32_bf16 v[78:81], a[56:59], a[180:183], v[78:81]\n\t"
"ds_read_b128 a[180:183], v244 offset:35328\n\t"
"v_mfma_f32_16x16x32_bf16 v[78:81], a[64:67], a[184:187], v[78:81]\n\t"
"ds_read_b128 v[196:199], v252 offset:0\n\t"
"v_mfma_f32_16x16x32_bf16 v[78:81], a[144:147], a[188:191], v[78:81]\n\t"
"buffer_store_dwordx4 a[24:27], v248, s[16:19], s29 offen offset:128 nt\n\t"
"ds_read_b128 a[40:43], v252 offset:12288\n\t"
"ds_read_b128 v[204:207], v252 offset:1024\n\t"
"v_mfma_f32_16x16x32_bf16 v[82:85], a[56:59], a[184:187], v[82:85]\n\t"
"s_waitcnt lgkmcnt(7)\n\t"
"ds_read_b128 a[184:187], v244 offset:35840\n\t"
"v_mfma_f32_16x16x32_bf16 v[82:85], a[64:67], a[188:191], v[82:85]\n\t"
"ds_read_b128 a[72:75], v252 offset:13312\n\t"
"v_cvt_pk_bf16_f32 v224, v224, v225\n\t"
"v_mfma_f32_16x16x32_bf16 v[82:85], a[144:147], a[192:195], v[82:85]\n\t"
"ds_read_b128 a[8:11], v252 offset:2048\n\t"
"v_cvt_pk_bf16_f32 v225, v226, v227\n\t"
"v_mfma_f32_16x16x32_bf16 v[228:231], v[228:231], v[220:223], 0\n\t"
"ds_read_b128 a[104:107], v252 offset:14336\n\t"
"v_mfma_f32_16x16x32_bf16 v[86:89], a[56:59], a[188:191], v[86:89]\n\t"
"ds_read_b128 a[188:191], v244 offset:36352\n\t"
"v_mfma_f32_16x16x32_bf16 v[86:89], a[64:67], a[192:195], v[86:89]\n\t"
"v_mfma_f32_16x16x32_bf16 v[86:89], a[144:147], a[196:199], v[86:89]\n\t"
"v_mfma_f32_16x16x32_bf16 v[90:93], a[56:59], a[192:195], v[90:93]\n\t"
"ds_read_b128 a[192:195], v244 offset:36864\n\t"
"v_mfma_f32_16x16x32_bf16 v[90:93], a[64:67], a[196:199], v[90:93]\n\t"
"v_cvt_pk_bf16_f32 v228, v228, v229\n\t"
"v_cvt_pk_bf16_f32 v229, v230, v231\n\t"
"v_mfma_f32_16x16x32_bf16 v[90:93], a[144:147], a[200:203], v[90:93]\n\t"
"v_mfma_f32_16x16x32_bf16 v[94:97], a[56:59], a[196:199], v[94:97]\n\t"
"s_waitcnt vmcnt(35) lgkmcnt(10)\n\t"
"ds_read_b128 a[196:199], v244 offset:37376\n\t"
"v_mfma_f32_16x16x32_bf16 v[94:97], a[64:67], a[200:203], v[94:97]\n\t"
"ds_read_b128 a[200:203], v244 offset:37888\n\t"
"v_mfma_f32_16x16x32_bf16 v[94:97], a[144:147], a[204:207], v[94:97]\n\t"
"ds_read_b128 a[204:207], v244 offset:38400\n\t"
"v_mfma_f32_16x16x32_bf16 v[100:103], a[20:23], a[168:171], v[100:103]\n\t"
"s_waitcnt lgkmcnt(12)\n\t"
"v_mfma_f32_16x16x32_bf16 v[100:103], a[252:255], a[172:175], v[100:103]\n\t"
"v_mfma_f32_16x16x32_bf16 v[100:103], a[232:235], a[176:179], v[100:103]\n\t"
"buffer_store_dwordx4 a[32:35], v248, s[16:19], s29 offen offset:256 nt\n\t"
"v_mfma_f32_16x16x32_bf16 v[104:107], a[20:23], a[172:175], v[104:107]\n\t"
"s_waitcnt lgkmcnt(8)\n\t"
"v_mfma_f32_16x16x32_bf16 v[104:107], a[252:255], a[176:179], v[104:107]\n\t"
"v_mfma_f32_16x16x32_bf16 v[104:107], a[232:235], a[180:183], v[104:107]\n\t"
"s_waitcnt lgkmcnt(0)\n\t"
"s_barrier\n\t"
"v_mfma_f32_16x16x32_bf16 v[108:111], a[20:23], a[176:179], v[108:111]\n\t"
"s_add_u32 s29, s23, 1152\n\t"
"v_fma_f32 v0, v2, s52, v100\n\t"
"v_mfma_f32_16x16x32_bf16 v[108:111], a[252:255], a[180:183], v[108:111]\n\t"
"v_fma_f32 v1, v3, s52, v101\n\t"
"v_mfma_f32_16x16x32_bf16 v[108:111], a[232:235], a[184:187], v[108:111]\n\t"
"ds_write_b64 v245, v[236:237] offset:24576\n\t"
"v_mfma_f32_16x16x32_bf16 v[112:115], a[20:23], a[180:183], v[112:115]\n\t"
"ds_read_b64_tr_b16 v[236:237], v246 offset:26624\n\t"
"v_mfma_f32_16x16x32_bf16 v[112:115], a[252:255], a[184:187], v[112:115]\n\t"
"ds_read_b64_tr_b16 v[238:239], v246 offset:29696\n\t"
"v_mfma_f32_16x16x32_bf16 v[112:115], a[232:235], a[188:191], v[112:115]\n\t"
"ds_write_b64 v245, v[232:233] offset:25088\n\t"
"v_mfma_f32_16x16x32_bf16 v[116:119], a[20:23], a[184:187], v[116:119]\n\t"
"ds_read_b64_tr_b16 v[232:233], v246 offset:32768\n\t"
"v_fma_f32 v212, -v10, s56, -v108\n\t"
"v_mfma_f32_16x16x32_bf16 v[116:119], a[252:255], a[188:191], v[116:119]\n\t"
"ds_read_b64_tr_b16 v[234:235], v246 offset:35840\n\t"
"v_fma_f32 v213, -v11, s56, -v109\n\t"
"v_mfma_f32_16x16x32_bf16 v[116:119], a[232:235], a[192:195], v[116:119]\n\t"
"ds_write_b64 v245, v[224:225] offset:25600\n\t"
"v_mfma_f32_16x16x32_bf16 v[120:123], a[20:23], a[188:191], v[120:123]\n\t"
"s_waitcnt lgkmcnt(4)\n\t"
"ds_read_b64_tr_b16 v[224:225], v246 offset:40448\n\t"
"v_fma_f32 v214, -v12, s56, -v110\n\t"
"v_mfma_f32_16x16x32_bf16 v[120:123], a[252:255], a[192:195], v[120:123]\n\t"
"ds_read_b64_tr_b16 v[226:227], v246 offset:43520\n\t"
"v_fma_f32 v215, -v13, s56, -v111\n\t"
"v_mfma_f32_16x16x32_bf16 v[120:123], a[232:235], a[196:199], v[120:123]\n\t"
"ds_write_b64 v245, v[228:229] offset:26112\n\t"
"v_mfma_f32_16x16x32_bf16 v[236:239], v[236:239], v[220:223], 0\n\t"
"buffer_store_dwordx4 v[196:199], v248, s[16:19], s29 offen offset:0 nt\n\t"
"ds_read_b64_tr_b16 v[228:229], v246 offset:46592\n\t"
"ds_read_b64_tr_b16 v[230:231], v246 offset:49664\n\t"
"v_fma_f32 v98, -v14, s56, -v112\n\t"
"v_mfma_f32_16x16x32_bf16 v[124:127], a[20:23], a[192:195], v[124:127]\n\t"
"s_waitcnt lgkmcnt(6)\n\t"
"v_fma_f32 v99, -v15, s56, -v113\n\t"
"v_accvgpr_write_b32 a0, v98\n\t"
"v_mfma_f32_16x16x32_bf16 v[124:127], a[252:255], a[196:199], v[124:127]\n\t"
"v_fma_f32 v196, -v2, s56, -v100\n\t"
"v_fma_f32 v197, -v3, s56, -v101\n\t"
"v_mfma_f32_16x16x32_bf16 v[124:127], a[232:235], a[200:203], v[124:127]\n\t"
"ds_write_b128 v249, a[76:79] offset:0\n\t"
"v_mfma_f32_16x16x32_bf16 v[232:235], v[232:235], v[220:223], 0\n\t"
"ds_write_b128 v249, a[92:95] offset:12288\n\t"
"v_mfma_f32_16x16x32_bf16 v[128:131], a[20:23], a[196:199], v[128:131]\n\t"
"s_waitcnt lgkmcnt(5)\n\t"
"v_cvt_pk_bf16_f32 v236, v236, v237\n\t"
"v_cvt_pk_bf16_f32 v237, v238, v239\n\t"
"v_mfma_f32_16x16x32_bf16 v[128:131], a[252:255], a[200:203], v[128:131]\n\t"
"s_waitcnt vmcnt(32)\n\t"
"v_fma_f32 v198, -v4, s56, -v102\n\t"
"v_fma_f32 v199, -v5, s56, -v103\n\t"
"v_mfma_f32_16x16x32_bf16 v[128:131], a[232:235], a[204:207], v[128:131]\n\t"
"ds_write_b64 v250, a[142:143] offset:0\n\t"
"v_mfma_f32_16x16x32_bf16 v[224:227], v[224:227], v[220:223], 0\n\t"
"buffer_load_dwordx4 a[252:255], v252, s[8:11], s20 offen offset:2048 sc1\n\t"
"s_mov_b32 s20, 172032\n\t"
"v_mfma_f32_16x16x32_bf16 v[132:135], a[152:155], a[168:171], v[132:135]\n\t"
"buffer_load_dwordx4 a[232:235], v252, s[8:11], s20 offen offset:0 sc1\n\t"
"v_mfma_f32_16x16x32_bf16 v[132:135], a[28:31], a[172:175], v[132:135]\n\t"
"s_waitcnt lgkmcnt(3)\n\t"
"v_cvt_pk_bf16_f32 v232, v232, v233\n\t"
"v_cvt_pk_bf16_f32 v233, v234, v235\n\t"
"v_mfma_f32_16x16x32_bf16 v[132:135], a[160:163], a[176:179], v[132:135]\n\t"
"ds_write_b64 v250, a[158:159] offset:12288\n\t"
"v_mfma_f32_16x16x32_bf16 v[228:231], v[228:231], v[220:223], 0\n\t"
"ds_write_b64 v245, v[236:237] offset:26624\n\t"
"v_mfma_f32_16x16x32_bf16 v[136:139], a[152:155], a[172:175], v[136:139]\n\t"
"ds_read_b64_tr_b16 v[236:237], v246 offset:52736\n\t"
"v_cvt_pk_bf16_f32 v224, v224, v225\n\t"
"v_mfma_f32_16x16x32_bf16 v[136:139], a[28:31], a[176:179], v[136:139]\n\t"
"ds_read_b64_tr_b16 v[238:239], v246 offset:55808\n\t"
"v_cvt_pk_bf16_f32 v225, v226, v227\n\t"
"v_mfma_f32_16x16x32_bf16 v[136:139], a[160:163], a[180:183], v[136:139]\n\t"
"buffer_store_dwordx4 v[204:207], v248, s[16:19], s29 offen offset:128 nt\n\t"
"ds_write_b64 v245, v[232:233] offset:27136\n\t"
"v_fma_f32 v98, -v16, s56, -v114\n\t"
"v_mfma_f32_16x16x32_bf16 v[140:143], a[152:155], a[176:179], v[140:143]\n\t"
"v_fma_f32 v204, -v6, s56, -v104\n\t"
"v_fma_f32 v205, -v7, s56, -v105\n\t"
"v_mfma_f32_16x16x32_bf16 v[140:143], a[28:31], a[180:183], v[140:143]\n\t"
"v_cvt_pk_bf16_f32 v228, v228, v229\n\t"
"v_cvt_pk_bf16_f32 v229, v230, v231\n\t"
"v_mfma_f32_16x16x32_bf16 v[140:143], a[160:163], a[184:187], v[140:143]\n\t"
"ds_write_b64 v245, v[224:225] offset:27648\n\t"
"v_mfma_f32_16x16x32_bf16 v[144:147], a[152:155], a[180:183], v[144:147]\n\t"
"s_waitcnt lgkmcnt(2)\n\t"
"v_fma_f32 v206, -v8, s56, -v106\n\t"
"v_fma_f32 v207, -v9, s56, -v107\n\t"
"v_mfma_f32_16x16x32_bf16 v[144:147], a[28:31], a[184:187], v[144:147]\n\t"
"v_accvgpr_write_b32 a1, v99\n\t"
"v_fma_f32 v99, -v17, s56, -v115\n\t"
"v_mfma_f32_16x16x32_bf16 v[144:147], a[160:163], a[188:191], v[144:147]\n\t"
"ds_write_b128 v249, a[84:87] offset:6144\n\t"
"v_mfma_f32_16x16x32_bf16 v[236:239], v[236:239], v[220:223], 0\n\t"
"buffer_store_dwordx4 a[8:11], v248, s[16:19], s29 offen offset:256 nt\n\t"
"v_accvgpr_write_b32 a2, v98\n\t"
"v_accvgpr_write_b32 a3, v99\n\t"
"v_mfma_f32_16x16x32_bf16 v[148:151], a[152:155], a[184:187], v[148:151]\n\t"
"v_mfma_f32_16x16x32_bf16 v[148:151], a[28:31], a[188:191], v[148:151]\n\t"
"v_mfma_f32_16x16x32_bf16 v[148:151], a[160:163], a[192:195], v[148:151]\n\t"
"ds_write_b128 v249, a[100:103] offset:18432\n\t"
"v_mfma_f32_16x16x32_bf16 v[152:155], a[152:155], a[188:191], v[152:155]\n\t"
"s_add_u32 s29, s23, 394368\n\t"
"v_mfma_f32_16x16x32_bf16 v[152:155], a[28:31], a[192:195], v[152:155]\n\t"
"v_cvt_pk_bf16_f32 v236, v236, v237\n\t"
"v_cvt_pk_bf16_f32 v237, v238, v239\n\t"
"v_mfma_f32_16x16x32_bf16 v[152:155], a[160:163], a[196:199], v[152:155]\n\t"
"ds_write_b64 v245, v[228:229] offset:28160\n\t"
"v_mfma_f32_16x16x32_bf16 v[156:159], a[152:155], a[192:195], v[156:159]\n\t"
"v_fma_f32 v98, -v18, s56, -v116\n\t"
"v_fma_f32 v99, -v19, s56, -v117\n\t"
"v_mfma_f32_16x16x32_bf16 v[156:159], a[28:31], a[196:199], v[156:159]\n\t"
"v_accvgpr_write_b32 a8, v98\n\t"
"v_fma_f32 v98, -v20, s56, -v118\n\t"
"v_mfma_f32_16x16x32_bf16 v[156:159], a[160:163], a[200:203], v[156:159]\n\t"
"ds_write_b64 v250, a[150:151] offset:6144\n\t"
"v_mfma_f32_16x16x32_bf16 v[160:163], a[152:155], a[196:199], v[160:163]\n\t"
"s_waitcnt vmcnt(31)\n\t"
"v_accvgpr_write_b32 a9, v99\n\t"
"v_fma_f32 v99, -v21, s56, -v119\n\t"
"v_mfma_f32_16x16x32_bf16 v[160:163], a[28:31], a[200:203], v[160:163]\n\t"
"v_accvgpr_write_b32 a10, v98\n\t"
"v_accvgpr_write_b32 a11, v99\n\t"
"v_mfma_f32_16x16x32_bf16 v[160:163], a[160:163], a[204:207], v[160:163]\n\t"
"ds_write_b64 v250, a[166:167] offset:18432\n\t"
"v_mfma_f32_16x16x32_bf16 v[164:167], a[36:39], a[168:171], v[164:167]\n\t"
"v_fma_f32 v98, -v22, s56, -v120\n\t"
"v_fma_f32 v99, -v23, s56, -v121\n\t"
"v_mfma_f32_16x16x32_bf16 v[164:167], a[112:115], a[172:175], v[164:167]\n\t"
"v_accvgpr_write_b32 a16, v98\n\t"
"v_mfma_f32_16x16x32_bf16 v[164:167], v[200:203], a[176:179], v[164:167]\n\t"
"ds_write_b64 v245, v[236:237] offset:28672\n\t"
"s_waitcnt lgkmcnt(0)\n\t"
"s_barrier\n\t"
"v_mfma_f32_16x16x32_bf16 v[168:171], a[36:39], a[172:175], v[168:171]\n\t"
"ds_read_b128 a[168:171], v244 offset:24576\n\t"
"v_mfma_f32_16x16x32_bf16 v[168:171], a[112:115], a[176:179], v[168:171]\n\t"
"buffer_load_dwordx4 v[236:239], v252, s[8:11], s20 offen offset:1024 sc1\n\t"
"v_mfma_f32_16x16x32_bf16 v[168:171], v[200:203], a[180:183], v[168:171]\n\t"
"buffer_store_dwordx4 a[40:43], v248, s[16:19], s29 offen offset:0 nt\n\t"
"ds_read_b128 a[172:175], v244 offset:25088\n\t"
"v_fma_f32 v98, -v24, s56, -v122\n\t"
"v_accvgpr_write_b32 a17, v99\n\t"
"v_fma_f32 v99, -v25, s56, -v123\n\t"
"v_mfma_f32_16x16x32_bf16 v[172:175], a[36:39], a[176:179], v[172:175]\n\t"
"ds_read_b128 a[176:179], v244 offset:25600\n\t"
"v_accvgpr_write_b32 a18, v98\n\t"
"v_mfma_f32_16x16x32_bf16 v[172:175], a[112:115], a[180:183], v[172:175]\n\t"
"v_accvgpr_write_b32 a19, v99\n\t"
"v_fma_f32 v98, -v26, s56, -v124\n\t"
"v_mfma_f32_16x16x32_bf16 v[172:175], v[200:203], a[184:187], v[172:175]\n\t"
"v_fma_f32 v99, -v27, s56, -v125\n\t"
"v_accvgpr_write_b32 a24, v98\n\t"
"v_mfma_f32_16x16x32_bf16 v[176:179], a[36:39], a[180:183], v[176:179]\n\t"
"ds_read_b128 a[180:183], v244 offset:26112\n\t"
"v_fma_f32 v98, -v28, s56, -v126\n\t"
"v_mfma_f32_16x16x32_bf16 v[176:179], a[112:115], a[184:187], v[176:179]\n\t"
"v_accvgpr_write_b32 a25, v99\n\t"
"v_fma_f32 v99, -v29, s56, -v127\n\t"
"v_mfma_f32_16x16x32_bf16 v[176:179], v[200:203], a[188:191], v[176:179]\n\t"
"v_accvgpr_write_b32 a26, v98\n\t"
"v_accvgpr_write_b32 a27, v99\n\t"
"v_mfma_f32_16x16x32_bf16 v[180:183], a[36:39], a[184:187], v[180:183]\n\t"
"ds_read_b128 a[184:187], v244 offset:26624\n\t"
"v_fma_f32 v98, -v30, s56, -v128\n\t"
"v_mfma_f32_16x16x32_bf16 v[180:183], a[112:115], a[188:191], v[180:183]\n\t"
"v_fma_f32 v99, -v31, s56, -v129\n\t"
"v_accvgpr_write_b32 a32, v98\n\t"
"v_mfma_f32_16x16x32_bf16 v[180:183], v[200:203], a[192:195], v[180:183]\n\t"
"v_fma_f32 v98, -v32, s56, -v130\n\t"
"v_accvgpr_write_b32 a33, v99\n\t"
"v_mfma_f32_16x16x32_bf16 v[184:187], a[36:39], a[188:191], v[184:187]\n\t"
"ds_read_b128 a[188:191], v244 offset:27136\n\t"
"v_fma_f32 v99, -v33, s56, -v131\n\t"
"v_mfma_f32_16x16x32_bf16 v[184:187], a[112:115], a[192:195], v[184:187]\n\t"
"v_accvgpr_write_b32 a34, v98\n\t"
"v_accvgpr_write_b32 a35, v99\n\t"
"v_mfma_f32_16x16x32_bf16 v[184:187], v[200:203], a[196:199], v[184:187]\n\t"
"buffer_store_dwordx4 a[72:75], v248, s[16:19], s29 offen offset:128 nt\n\t"
"v_fma_f32 v98, -v34, s56, -v132\n\t"
"v_fma_f32 v99, -v35, s56, -v133\n\t"
"v_accvgpr_write_b32 a40, v98\n\t"
"v_fma_f32 v98, -v36, s56, -v134\n\t"
"v_mfma_f32_16x16x32_bf16 v[188:191], a[36:39], a[192:195], v[188:191]\n\t"
"ds_read_b128 a[192:195], v244 offset:27648\n\t"
"v_accvgpr_write_b32 a41, v99\n\t"
"v_mfma_f32_16x16x32_bf16 v[188:191], a[112:115], a[196:199], v[188:191]\n\t"
"ds_read_b128 a[136:139], v252 offset:0\n\t"
"v_fma_f32 v99, -v37, s56, -v135\n\t"
"v_mfma_f32_16x16x32_bf16 v[188:191], v[200:203], a[200:203], v[188:191]\n\t"
"v_accvgpr_write_b32 a42, v98\n\t"
"v_accvgpr_write_b32 a43, v99\n\t"
"v_mfma_f32_16x16x32_bf16 v[192:195], a[36:39], a[196:199], v[192:195]\n\t"
"s_waitcnt vmcnt(29) lgkmcnt(5)\n\t"
"ds_read_b128 a[196:199], v244 offset:28160\n\t"
"v_fma_f32 v98, -v38, s56, -v136\n\t"
"v_mfma_f32_16x16x32_bf16 v[192:195], a[112:115], a[200:203], v[192:195]\n\t"
"ds_read_b128 a[200:203], v244 offset:28672\n\t"
"v_fma_f32 v99, -v39, s56, -v137\n\t"
"v_mfma_f32_16x16x32_bf16 v[192:195], v[200:203], a[204:207], v[192:195]\n\t"
"ds_read_b128 a[204:207], v244 offset:29184\n\t"
"v_accvgpr_write_b32 a48, v98\n\t"
"Lst20_%=:\n\t"
"v_mfma_f32_16x16x32_bf16 v[196:199], v[208:211], a[168:171], v[196:199]\n\t"
"s_waitcnt lgkmcnt(7)\n\t"
"v_xor_b32 v246, 0xc0, v246\n\t"
"v_fma_f32 v98, -v40, s56, -v138\n\t"
"v_mfma_f32_16x16x32_bf16 v[196:199], v[216:219], a[172:175], v[196:199]\n\t"
"v_accvgpr_write_b32 a49, v99\n\t"
"v_fma_f32 v99, -v41, s56, -v139\n\t"
"v_mfma_f32_16x16x32_bf16 v[196:199], a[4:7], a[176:179], v[196:199]\n\t"
"v_accvgpr_write_b32 a50, v98\n\t"
"v_accvgpr_write_b32 a51, v99\n\t"
"v_mfma_f32_16x16x32_bf16 v[204:207], v[208:211], a[172:175], v[204:207]\n\t"
"s_waitcnt lgkmcnt(6)\n\t"
"v_fma_f32 v98, -v42, s56, -v140\n\t"
"v_fma_f32 v99, -v43, s56, -v141\n\t"
"v_mfma_f32_16x16x32_bf16 v[204:207], v[216:219], a[176:179], v[204:207]\n\t"
"v_accvgpr_write_b32 a56, v98\n\t"
"v_fma_f32 v98, -v44, s56, -v142\n\t"
"v_mfma_f32_16x16x32_bf16 v[204:207], a[4:7], a[180:183], v[204:207]\n\t"
"buffer_store_dwordx4 a[104:107], v248, s[16:19], s29 offen offset:256 nt\n\t"
"v_accvgpr_write_b32 a57, v99\n\t"
"v_fma_f32 v99, -v45, s56, -v143\n\t"
"v_accvgpr_write_b32 a58, v98\n\t"
"v_accvgpr_write_b32 a59, v99\n\t"
"v_mfma_f32_16x16x32_bf16 v[212:215], v[208:211], a[176:179], v[212:215]\n\t"
"s_waitcnt lgkmcnt(5)\n\t"
"v_fma_f32 v98, -v46, s56, -v144\n\t"
"v_fma_f32 v99, -v47, s56, -v145\n\t"
"v_mfma_f32_16x16x32_bf16 v[212:215], v[216:219], a[180:183], v[212:215]\n\t"
"s_add_u32 s29, s23, 787584\n\t"
"ds_read_b128 v[200:203], v252 offset:1024\n\t"
"v_accvgpr_write_b32 a64, v98\n\t"
"v_mfma_f32_16x16x32_bf16 v[212:215], a[4:7], a[184:187], v[212:215]\n\t"
"v_fma_f32 v98, -v48, s56, -v146\n\t"
"v_accvgpr_write_b32 a65, v99\n\t"
"v_mfma_f32_16x16x32_bf16 a[0:3], v[208:211], a[180:183], a[0:3]\n\t"
"s_waitcnt lgkmcnt(5)\n\t"
"v_fma_f32 v99, -v49, s56, -v147\n\t"
"v_accvgpr_write_b32 a66, v98\n\t"
"v_mfma_f32_16x16x32_bf16 a[0:3], v[216:219], a[184:187], a[0:3]\n\t"
"s_waitcnt lgkmcnt(4)\n\t"
"v_accvgpr_write_b32 a67, v99\n\t"
"v_fma_f32 v98, -v50, s56, -v148\n\t"
"v_mfma_f32_16x16x32_bf16 a[0:3], a[4:7], a[188:191], a[0:3]\n\t"
"v_fma_f32 v99, -v51, s56, -v149\n\t"
"v_accvgpr_write_b32 a72, v98\n\t"
"v_mfma_f32_16x16x32_bf16 a[8:11], v[208:211], a[184:187], a[8:11]\n\t"
"s_waitcnt lgkmcnt(3)\n\t"
"v_fma_f32 v98, -v52, s56, -v150\n\t"
"v_accvgpr_write_b32 a73, v99\n\t"
"v_mfma_f32_16x16x32_bf16 a[8:11], v[216:219], a[188:191], a[8:11]\n\t"
"v_fma_f32 v99, -v53, s56, -v151\n\t"
"v_accvgpr_write_b32 a74, v98\n\t"
"v_mfma_f32_16x16x32_bf16 a[8:11], a[4:7], a[192:195], a[8:11]\n\t"
"ds_read_b64_tr_b16 v[232:233], v246 offset:512\n\t"
"v_accvgpr_write_b32 a75, v99\n\t"
"v_mfma_f32_16x16x32_bf16 a[16:19], v[208:211], a[188:191], a[16:19]\n\t"
"s_waitcnt lgkmcnt(3)\n\t"
"ds_read_b64_tr_b16 v[234:235], v246 offset:3584\n\t"
"v_fma_f32 v98, -v54, s56, -v152\n\t"
"v_mfma_f32_16x16x32_bf16 a[16:19], v[216:219], a[192:195], a[16:19]\n\t"
"ds_read_b128 a[44:47], v252 offset:12288\n\t"
"v_fma_f32 v99, -v55, s56, -v153\n\t"
"v_mfma_f32_16x16x32_bf16 a[16:19], a[4:7], a[196:199], a[16:19]\n\t"
"v_accvgpr_write_b32 a80, v98\n\t"
"v_fma_f32 v98, -v56, s56, -v154\n\t"
"v_mfma_f32_16x16x32_bf16 a[24:27], v[208:211], a[192:195], a[24:27]\n\t"
"s_waitcnt lgkmcnt(4)\n\t"
"ds_read_b128 a[76:79], v252 offset:13312\n\t"
"v_accvgpr_write_b32 a81, v99\n\t"
"v_mfma_f32_16x16x32_bf16 a[24:27], v[216:219], a[196:199], a[24:27]\n\t"
"v_fma_f32 v99, -v57, s56, -v155\n\t"
"v_accvgpr_write_b32 a82, v98\n\t"
"v_mfma_f32_16x16x32_bf16 a[24:27], a[4:7], a[200:203], a[24:27]\n\t"
"buffer_store_dwordx4 a[136:139], v248, s[16:19], s29 offen offset:0 nt\n\t"
"ds_read_b128 a[12:15], v252 offset:2048\n\t"
"ds_read_b128 a[108:111], v252 offset:14336\n\t"
"v_accvgpr_write_b32 a83, v99\n\t"
"v_mfma_f32_16x16x32_bf16 a[32:35], v[208:211], a[196:199], a[32:35]\n\t"
"s_waitcnt vmcnt(26)\n\t"
"ds_read_b64_tr_b16 v[224:225], v246 offset:6656\n\t"
"v_fma_f32 v98, -v58, s56, -v156\n\t"
"v_mfma_f32_16x16x32_bf16 a[32:35], v[216:219], a[200:203], a[32:35]\n\t"
"s_waitcnt lgkmcnt(7)\n\t"
"ds_read_b64_tr_b16 v[226:227], v246 offset:9728\n\t"
"v_fma_f32 v99, -v59, s56, -v157\n\t"
"v_mfma_f32_16x16x32_bf16 a[32:35], a[4:7], a[204:207], a[32:35]\n\t"
"v_accvgpr_write_b32 a88, v98\n\t"
"v_fma_f32 v98, -v60, s56, -v158\n\t"
"v_mfma_f32_16x16x32_bf16 a[40:43], a[208:211], a[168:171], a[40:43]\n\t"
"v_accvgpr_write_b32 a89, v99\n\t"
"v_fma_f32 v99, -v61, s56, -v159\n\t"
"v_mfma_f32_16x16x32_bf16 a[40:43], a[212:215], a[172:175], a[40:43]\n\t"
"v_accvgpr_write_b32 a90, v98\n\t"
"v_accvgpr_write_b32 a91, v99\n\t"
"v_mfma_f32_16x16x32_bf16 a[40:43], v[240:243], a[176:179], a[40:43]\n\t"
"v_fma_f32 v98, -v62, s56, -v160\n\t"
"v_fma_f32 v99, -v63, s56, -v161\n\t"
"v_mfma_f32_16x16x32_bf16 a[48:51], a[208:211], a[172:175], a[48:51]\n\t"
"s_waitcnt lgkmcnt(6)\n\t"
"v_accvgpr_write_b32 a96, v98\n\t"
"v_fma_f32 v98, -v64, s56, -v162\n\t"
"v_mfma_f32_16x16x32_bf16 a[48:51], a[212:215], a[176:179], a[48:51]\n\t"
"v_accvgpr_write_b32 a97, v99\n\t"
"v_fma_f32 v99, -v65, s56, -v163\n\t"
"v_mfma_f32_16x16x32_bf16 a[48:51], v[240:243], a[180:183], a[48:51]\n\t"
"v_accvgpr_write_b32 a98, v98\n\t"
"v_accvgpr_write_b32 a99, v99\n\t"
"v_mfma_f32_16x16x32_bf16 v[232:235], v[232:235], v[220:223], 0\n\t"
"v_fma_f32 v98, -v66, s56, -v164\n\t"
"v_fma_f32 v99, -v67, s56, -v165\n\t"
"v_mfma_f32_16x16x32_bf16 a[56:59], a[208:211], a[176:179], a[56:59]\n\t"
"ds_read_b64_tr_b16 v[228:229], v246 offset:12800\n\t"
"v_accvgpr_write_b32 a104, v98\n\t"
"v_mfma_f32_16x16x32_bf16 a[56:59], a[212:215], a[180:183], a[56:59]\n\t"
"ds_read_b64_tr_b16 v[230:231], v246 offset:15872\n\t"
"v_fma_f32 v98, -v68, s56, -v166\n\t"
"v_mfma_f32_16x16x32_bf16 a[56:59], v[240:243], a[184:187], a[56:59]\n\t"
"buffer_store_dwordx4 v[200:203], v248, s[16:19], s29 offen offset:128 nt\n\t"
"v_accvgpr_write_b32 a105, v99\n\t"
"v_fma_f32 v99, -v69, s56, -v167\n\t"
"v_accvgpr_write_b32 a106, v98\n\t"
"v_accvgpr_write_b32 a107, v99\n\t"
"v_mfma_f32_16x16x32_bf16 a[64:67], a[208:211], a[180:183], a[64:67]\n\t"
"s_waitcnt lgkmcnt(2)\n\t"
"v_fma_f32 v98, -v70, s56, -v168\n\t"
"v_fma_f32 v99, -v71, s56, -v169\n\t"
"v_mfma_f32_16x16x32_bf16 a[64:67], a[212:215], a[184:187], a[64:67]\n\t"
"v_cvt_pk_bf16_f32 v232, v232, v233\n\t"
"v_cvt_pk_bf16_f32 v233, v234, v235\n\t"
"v_mfma_f32_16x16x32_bf16 a[64:67], v[240:243], a[188:191], a[64:67]\n\t"
"v_accvgpr_write_b32 a112, v98\n\t"
"v_mfma_f32_16x16x32_bf16 v[224:227], v[224:227], v[220:223], 0\n\t"
"s_barrier\n\t"
"v_mfma_f32_16x16x32_bf16 a[72:75], a[208:211], a[184:187], a[72:75]\n\t"
"v_fma_f32 v98, -v72, s56, -v170\n\t"
"v_mfma_f32_16x16x32_bf16 a[72:75], a[212:215], a[188:191], a[72:75]\n\t"
"v_accvgpr_write_b32 a113, v99\n\t"
"v_fma_f32 v99, -v73, s56, -v171\n\t"
"v_mfma_f32_16x16x32_bf16 a[72:75], v[240:243], a[192:195], a[72:75]\n\t"
"v_accvgpr_write_b32 a114, v98\n\t"
"v_accvgpr_write_b32 a115, v99\n\t"
"v_mfma_f32_16x16x32_bf16 a[80:83], a[208:211], a[188:191], a[80:83]\n\t"
"v_fma_f32 v98, -v74, s56, -v172\n\t"
"v_fma_f32 v99, -v75, s56, -v173\n\t"
"v_mfma_f32_16x16x32_bf16 a[80:83], a[212:215], a[192:195], a[80:83]\n\t"
"v_cvt_pk_bf16_f32 v234, v224, v225\n\t"
"v_cvt_pk_bf16_f32 v235, v226, v227\n\t"
"v_mfma_f32_16x16x32_bf16 a[80:83], v[240:243], a[196:199], a[80:83]\n\t"
"ds_read_b64_tr_b16 v[224:225], v246 offset:20480\n\t"
"v_accvgpr_write_b32 a120, v98\n\t"
"v_mfma_f32_16x16x32_bf16 a[88:91], a[208:211], a[192:195], a[88:91]\n\t"
"s_waitcnt lgkmcnt(1)\n\t"
"ds_read_b64_tr_b16 v[226:227], v246 offset:23552\n\t"
"v_fma_f32 v98, -v76, s56, -v174\n\t"
"v_mfma_f32_16x16x32_bf16 a[88:91], a[212:215], a[196:199], a[88:91]\n\t"
"v_accvgpr_write_b32 a121, v99\n\t"
"v_fma_f32 v99, -v77, s56, -v175\n\t"
"v_mfma_f32_16x16x32_bf16 a[88:91], v[240:243], a[200:203], a[88:91]\n\t"
"buffer_store_dwordx4 a[12:15], v248, s[16:19], s29 offen offset:256 nt\n\t"
"v_accvgpr_write_b32 a122, v98\n\t"
"v_accvgpr_write_b32 a123, v99\n\t"
"v_fma_f32 v98, -v78, s56, -v176\n\t"
"v_fma_f32 v99, -v79, s56, -v177\n\t"
"v_mfma_f32_16x16x32_bf16 v[228:231], v[228:231], v[220:223], 0\n\t"
"s_add_u32 s29, s23, 1180800\n\t"
"v_accvgpr_write_b32 a128, v98\n\t"
"v_fma_f32 v98, -v80, s56, -v178\n\t"
"v_mfma_f32_16x16x32_bf16 a[96:99], a[208:211], a[196:199], a[96:99]\n\t"
"s_waitcnt vmcnt(23)\n\t"
"v_accvgpr_write_b32 a129, v99\n\t"
"v_fma_f32 v99, -v81, s56, -v179\n\t"
"v_mfma_f32_16x16x32_bf16 a[96:99], a[212:215], a[200:203], a[96:99]\n\t"
"v_accvgpr_write_b32 a130, v98\n\t"
"v_accvgpr_write_b32 a131, v99\n\t"
"v_mfma_f32_16x16x32_bf16 a[96:99], v[240:243], a[204:207], a[96:99]\n\t"
"v_fma_f32 v98, -v82, s56, -v180\n\t"
"v_fma_f32 v99, -v83, s56, -v181\n\t"
"v_mfma_f32_16x16x32_bf16 a[104:107], a[216:219], a[168:171], a[104:107]\n\t"
"ds_read_b128 a[168:171], v244 offset:33792\n\t"
"v_accvgpr_write_b32 a136, v98\n\t"
"v_mfma_f32_16x16x32_bf16 a[104:107], a[220:223], a[172:175], a[104:107]\n\t"
"v_cvt_pk_bf16_f32 v228, v228, v229\n\t"
"v_cvt_pk_bf16_f32 v229, v230, v231\n\t"
"v_mfma_f32_16x16x32_bf16 a[104:107], a[224:227], a[176:179], a[104:107]\n\t"
"buffer_load_dwordx4 a[208:211], v252, s[8:11], s20 offen offset:2048 sc1\n\t"
"s_mov_b32 s20, 36864\n\t"
"v_mfma_f32_16x16x32_bf16 a[112:115], a[216:219], a[172:175], a[112:115]\n\t"
"buffer_load_dwordx4 v[240:243], v252, s[8:11], s20 offen offset:0 sc1\n\t"
"v_mfma_f32_16x16x32_bf16 a[112:115], a[220:223], a[176:179], a[112:115]\n\t"
"buffer_load_dwordx4 a[212:215], v252, s[8:11], s20 offen offset:1024 sc1\n\t"
"v_mfma_f32_16x16x32_bf16 a[112:115], a[224:227], a[180:183], a[112:115]\n\t"
"s_waitcnt lgkmcnt(1)\n\t"
"ds_read_b128 a[172:175], v244 offset:34304\n\t"
"v_fma_f32 v98, -v84, s56, -v182\n\t"
"v_mfma_f32_16x16x32_bf16 v[224:227], v[224:227], v[220:223], 0\n\t"
"buffer_store_dwordx4 a[44:47], v248, s[16:19], s29 offen offset:0 nt\n\t"
"v_accvgpr_write_b32 a137, v99\n\t"
"v_fma_f32 v99, -v85, s56, -v183\n\t"
"v_accvgpr_write_b32 a138, v98\n\t"
"v_accvgpr_write_b32 a139, v99\n\t"
"v_mfma_f32_16x16x32_bf16 a[120:123], a[216:219], a[176:179], a[120:123]\n\t"
"ds_read_b128 a[176:179], v244 offset:34816\n\t"
"v_fma_f32 v98, -v86, s56, -v184\n\t"
"v_mfma_f32_16x16x32_bf16 a[120:123], a[220:223], a[180:183], a[120:123]\n\t"
"v_fma_f32 v99, -v87, s56, -v185\n\t"
"v_accvgpr_write_b32 a144, v98\n\t"
"v_mfma_f32_16x16x32_bf16 a[120:123], a[224:227], a[184:187], a[120:123]\n\t"
"v_fma_f32 v98, -v88, s56, -v186\n\t"
"v_accvgpr_write_b32 a145, v99\n\t"
"v_mfma_f32_16x16x32_bf16 a[128:131], a[216:219], a[180:183], a[128:131]\n\t"
"ds_read_b128 a[180:183], v244 offset:35328\n\t"
"v_fma_f32 v99, -v89, s56, -v187\n\t"
"v_mfma_f32_16x16x32_bf16 a[128:131], a[220:223], a[184:187], a[128:131]\n\t"
"v_accvgpr_write_b32 a146, v98\n\t"
"v_accvgpr_write_b32 a147, v99\n\t"
"v_mfma_f32_16x16x32_bf16 a[128:131], a[224:227], a[188:191], a[128:131]\n\t"
"v_fma_f32 v98, -v90, s56, -v188\n\t"
"v_fma_f32 v99, -v91, s56, -v189\n\t"
"v_mfma_f32_16x16x32_bf16 a[136:139], a[216:219], a[184:187], a[136:139]\n\t"
"ds_read_b128 a[184:187], v244 offset:35840\n\t"
"v_cvt_pk_bf16_f32 v230, v224, v225\n\t"
"v_mfma_f32_16x16x32_bf16 a[136:139], a[220:223], a[188:191], a[136:139]\n\t"
"v_cvt_pk_bf16_f32 v231, v226, v227\n\t"
"v_accvgpr_write_b32 a152, v98\n\t"
"v_mfma_f32_16x16x32_bf16 a[136:139], a[224:227], a[192:195], a[136:139]\n\t"
"ds_read_b64_tr_b16 v[224:225], v246 offset:26624\n\t"
"v_fma_f32 v98, -v92, s56, -v190\n\t"
"v_mfma_f32_16x16x32_bf16 a[144:147], a[216:219], a[188:191], a[144:147]\n\t"
"ds_read_b64_tr_b16 v[226:227], v246 offset:29696\n\t"
"v_accvgpr_write_b32 a153, v99\n\t"
"v_mfma_f32_16x16x32_bf16 a[144:147], a[220:223], a[192:195], a[144:147]\n\t"
"ds_read_b128 a[188:191], v244 offset:36352\n\t"
"v_fma_f32 v99, -v93, s56, -v191\n\t"
"v_mfma_f32_16x16x32_bf16 a[144:147], a[224:227], a[196:199], a[144:147]\n\t"
"v_accvgpr_write_b32 a154, v98\n\t"
"v_accvgpr_write_b32 a155, v99\n\t"
"v_fma_f32 v98, -v94, s56, -v192\n\t"
"v_fma_f32 v99, -v95, s56, -v193\n\t"
"v_mfma_f32_16x16x32_bf16 a[152:155], a[216:219], a[192:195], a[152:155]\n\t"
"ds_read_b128 a[192:195], v244 offset:36864\n\t"
"v_accvgpr_write_b32 a160, v98\n\t"
"v_mfma_f32_16x16x32_bf16 a[152:155], a[220:223], a[196:199], a[152:155]\n\t"
"v_fma_f32 v98, -v96, s56, -v194\n\t"
"v_accvgpr_write_b32 a161, v99\n\t"
"v_mfma_f32_16x16x32_bf16 a[152:155], a[224:227], a[200:203], a[152:155]\n\t"
"buffer_store_dwordx4 a[76:79], v248, s[16:19], s29 offen offset:128 nt\n\t"
"v_fma_f32 v99, -v97, s56, -v195\n\t"
"v_accvgpr_write_b32 a162, v98\n\t"
"v_accvgpr_write_b32 a163, v99\n\t"
"v_fma_f32 v200, v100, s64, -v2\n\t"
"v_fma_f32 v201, v101, s64, -v3\n\t"
"v_fma_f32 v202, v102, s64, -v4\n\t"
"v_fma_f32 v203, v103, s64, -v5\n\t"
"v_mfma_f32_16x16x32_bf16 a[160:163], a[216:219], a[196:199], a[160:163]\n\t"
"s_waitcnt lgkmcnt(2)\n\t"
"ds_read_b128 a[196:199], v244 offset:37376\n\t"
"v_fma_f32 v208, v104, s64, -v6\n\t"
"v_mfma_f32_16x16x32_bf16 a[160:163], a[220:223], a[200:203], a[160:163]\n\t"
"s_waitcnt vmcnt(23)\n\t"
"ds_read_b128 a[200:203], v244 offset:37888\n\t"
"v_fma_f32 v209, v105, s64, -v7\n\t"
"v_mfma_f32_16x16x32_bf16 a[160:163], a[224:227], a[204:207], a[160:163]\n\t"
"ds_read_b128 a[204:207], v244 offset:38400\n\t"
"v_fma_f32 v210, v106, s64, -v8\n\t"
"v_fma_f32 v211, v107, s64, -v9\n\t"
"v_mfma_f32_16x16x32_bf16 v[224:227], v[224:227], v[220:223], 0\n\t"
"buffer_load_dwordx4 a[216:219], v252, s[8:11], s20 offen offset:2048 sc1\n\t"
"s_mov_b32 s20, 49152\n\t"
"v_mfma_f32_16x16x32_bf16 v[200:203], a[228:231], a[168:171], v[200:203]\n\t"
"buffer_load_dwordx4 a[220:223], v252, s[8:11], s20 offen offset:0 sc1\n\t"
"v_mfma_f32_16x16x32_bf16 v[200:203], a[236:239], a[172:175], v[200:203]\n\t"
"v_fma_f32 v216, v108, s64, -v10\n\t"
"v_fma_f32 v217, v109, s64, -v11\n\t"
"v_mfma_f32_16x16x32_bf16 v[200:203], a[240:243], a[176:179], v[200:203]\n\t"
"v_fma_f32 v218, v110, s64, -v12\n\t"
"v_fma_f32 v219, v111, s64, -v13\n\t"
"v_mfma_f32_16x16x32_bf16 v[208:211], a[228:231], a[172:175], v[208:211]\n\t"
"v_fma_f32 v98, v112, s64, -v14\n\t"
"v_fma_f32 v99, v113, s64, -v15\n\t"
"v_mfma_f32_16x16x32_bf16 v[208:211], a[236:239], a[176:179], v[208:211]\n\t"
"v_accvgpr_write_b32 a4, v98\n\t"
"v_mfma_f32_16x16x32_bf16 v[208:211], a[240:243], a[180:183], v[208:211]\n\t"
"s_waitcnt lgkmcnt(0)\n\t"
"s_barrier\n\t"
"v_mfma_f32_16x16x32_bf16 v[216:219], a[228:231], a[176:179], v[216:219]\n\t"
"v_fma_f32 v98, v114, s64, -v16\n\t"
"v_mfma_f32_16x16x32_bf16 v[216:219], a[236:239], a[180:183], v[216:219]\n\t"
"buffer_load_dwordx4 a[224:227], v252, s[8:11], s20 offen offset:1024 sc1\n\t"
"v_mfma_f32_16x16x32_bf16 v[216:219], a[240:243], a[184:187], v[216:219]\n\t"
"buffer_store_dwordx4 a[108:111], v248, s[16:19], s29 offen offset:256 nt\n\t"
"v_accvgpr_write_b32 a5, v99\n\t"
"v_fma_f32 v99, v115, s64, -v17\n\t"
"v_accvgpr_write_b32 a6, v98\n\t"
"v_accvgpr_write_b32 a7, v99\n\t"
"v_fma_f32 v98, v116, s64, -v18\n\t"
"v_fma_f32 v99, v117, s64, -v19\n\t"
"v_cvt_pk_bf16_f32 v224, v224, v225\n\t"
"v_cvt_pk_bf16_f32 v225, v226, v227\n\t"
"v_mfma_f32_16x16x32_bf16 a[4:7], a[228:231], a[180:183], a[4:7]\n\t"
"v_accvgpr_write_b32 a12, v98\n\t"
"v_fma_f32 v98, v118, s64, -v20\n\t"
"v_mfma_f32_16x16x32_bf16 a[4:7], a[236:239], a[184:187], a[4:7]\n\t"
"v_accvgpr_write_b32 a13, v99\n\t"
"v_fma_f32 v99, v119, s64, -v21\n\t"
"v_mfma_f32_16x16x32_bf16 a[4:7], a[240:243], a[188:191], a[4:7]\n\t"
"ds_write2st64_b64 v245, v[232:233], v[234:235] offset0:48 offset1:49\n\t"
"v_accvgpr_write_b32 a14, v98\n\t"
"v_accvgpr_write_b32 a15, v99\n\t"
"v_fma_f32 v98, v120, s64, -v22\n\t"
"v_fma_f32 v99, v121, s64, -v23\n\t"
"v_mfma_f32_16x16x32_bf16 a[12:15], a[228:231], a[184:187], a[12:15]\n\t"
"ds_read_b64_tr_b16 v[232:233], v246 offset:32768\n\t"
"v_mfma_f32_16x16x32_bf16 a[12:15], a[236:239], a[188:191], a[12:15]\n\t"
"ds_read_b64_tr_b16 v[234:235], v246 offset:35840\n\t"
"v_accvgpr_write_b32 a20, v98\n\t"
"v_mfma_f32_16x16x32_bf16 a[12:15], a[240:243], a[192:195], a[12:15]\n\t"
"ds_write2st64_b64 v245, v[228:229], v[230:231] offset0:50 offset1:51\n\t"
"buffer_load_dwordx4 v[248:251], v252, s[8:11], s20 offen offset:2048 sc1\n\t"
"v_fma_f32 v98, v122, s64, -v24\n\t"
"v_accvgpr_write_b32 a21, v99\n\t"
"v_fma_f32 v99, v123, s64, -v25\n\t"
"v_accvgpr_write_b32 a22, v98\n\t"
"v_accvgpr_write_b32 a23, v99\n\t"
"v_fma_f32 v98, v124, s64, -v26\n\t"
"v_fma_f32 v99, v125, s64, -v27\n\t"
"v_mfma_f32_16x16x32_bf16 a[20:23], a[228:231], a[188:191], a[20:23]\n\t"
"ds_read_b64_tr_b16 v[228:229], v246 offset:40448\n\t"
"v_accvgpr_write_b32 a28, v98\n\t"
"v_mfma_f32_16x16x32_bf16 a[20:23], a[236:239], a[192:195], a[20:23]\n\t"
"ds_read_b64_tr_b16 v[230:231], v246 offset:43520\n\t"
"v_fma_f32 v98, v126, s64, -v28\n\t"
"v_mfma_f32_16x16x32_bf16 a[20:23], a[240:243], a[196:199], a[20:23]\n\t"
"s_waitcnt lgkmcnt(3)\n\t"
"ds_write_b64 v245, v[224:225] offset:26624\n\t"
"v_accvgpr_write_b32 a29, v99\n\t"
"v_fma_f32 v99, v127, s64, -v29\n\t"
"v_accvgpr_write_b32 a30, v98\n\t"
"v_accvgpr_write_b32 a31, v99\n\t"
"v_mfma_f32_16x16x32_bf16 v[232:235], v[232:235], v[220:223], 0\n\t"
"s_mov_b32 s20, 61440\n\t"
"ds_read_b64_tr_b16 v[224:225], v246 offset:46592\n\t"
"v_fma_f32 v98, v128, s64, -v30\n\t"
"v_fma_f32 v99, v129, s64, -v31\n\t"
"v_mfma_f32_16x16x32_bf16 a[28:31], a[228:231], a[192:195], a[28:31]\n\t"
"ds_read_b64_tr_b16 v[226:227], v246 offset:49664\n\t"
"v_accvgpr_write_b32 a36, v98\n\t"
"v_mfma_f32_16x16x32_bf16 a[28:31], a[236:239], a[196:199], a[28:31]\n\t"
"v_fma_f32 v98, v130, s64, -v32\n\t"
"v_accvgpr_write_b32 a37, v99\n\t"
"v_mfma_f32_16x16x32_bf16 a[28:31], a[240:243], a[200:203], a[28:31]\n\t"
"s_waitcnt lgkmcnt(3)\n\t"
"v_fma_f32 v99, v131, s64, -v33\n\t"
"v_accvgpr_write_b32 a38, v98\n\t"
"v_accvgpr_write_b32 a39, v99\n\t"
"v_mfma_f32_16x16x32_bf16 v[228:231], v[228:231], v[220:223], 0\n\t"
"v_fma_f32 v98, v132, s64, -v34\n\t"
"v_fma_f32 v99, v133, s64, -v35\n\t"
"v_mfma_f32_16x16x32_bf16 a[36:39], a[228:231], a[196:199], a[36:39]\n\t"
"v_accvgpr_write_b32 a44, v98\n\t"
"v_fma_f32 v98, v134, s64, -v36\n\t"
"v_mfma_f32_16x16x32_bf16 a[36:39], a[236:239], a[200:203], a[36:39]\n\t"
"s_waitcnt lgkmcnt(0)\n\t"
"v_accvgpr_write_b32 a45, v99\n\t"
"v_fma_f32 v99, v135, s64, -v37\n\t"
"v_mfma_f32_16x16x32_bf16 a[36:39], a[240:243], a[204:207], a[36:39]\n\t"
"v_accvgpr_write_b32 a46, v98\n\t"
"v_accvgpr_write_b32 a47, v99\n\t"
"v_fma_f32 v98, v136, s64, -v38\n\t"
"v_fma_f32 v99, v137, s64, -v39\n\t"
"v_mfma_f32_16x16x32_bf16 v[224:227], v[224:227], v[220:223], 0\n\t"
"s_waitcnt vmcnt(20)\n\t"
"buffer_load_dwordx4 a[228:231], v252, s[8:11], s20 offen offset:0 sc1\n\t"
"v_cvt_pk_bf16_f32 v232, v232, v233\n\t"
"v_cvt_pk_bf16_f32 v233, v234, v235\n\t"
"v_mfma_f32_16x16x32_bf16 a[44:47], a[244:247], a[168:171], a[44:47]\n\t"
"v_accvgpr_write_b32 a52, v98\n\t"
"v_fma_f32 v98, v138, s64, -v40\n\t"
"v_mfma_f32_16x16x32_bf16 a[44:47], a[248:251], a[172:175], a[44:47]\n\t"
"v_accvgpr_write_b32 a53, v99\n\t"
"v_fma_f32 v99, v139, s64, -v41\n\t"
"v_mfma_f32_16x16x32_bf16 a[44:47], a[252:255], a[176:179], a[44:47]\n\t"
"buffer_load_dwordx4 a[236:239], v252, s[8:11], s20 offen offset:1024 sc1\n\t"
"v_accvgpr_write_b32 a54, v98\n\t"
"v_accvgpr_write_b32 a55, v99\n\t"
"v_fma_f32 v98, v140, s64, -v42\n\t"
"v_fma_f32 v99, v141, s64, -v43\n\t"
"v_mfma_f32_16x16x32_bf16 a[52:55], a[244:247], a[172:175], a[52:55]\n\t"
"v_accvgpr_write_b32 a60, v98\n\t"
"v_fma_f32 v98, v142, s64, -v44\n\t"
"v_mfma_f32_16x16x32_bf16 a[52:55], a[248:251], a[176:179], a[52:55]\n\t"
"v_accvgpr_write_b32 a61, v99\n\t"
"v_fma_f32 v99, v143, s64, -v45\n\t"
"v_mfma_f32_16x16x32_bf16 a[52:55], a[252:255], a[180:183], a[52:55]\n\t"
"buffer_load_dwordx4 a[240:243], v252, s[8:11], s20 offen offset:2048 sc1\n\t"
"v_accvgpr_write_b32 a62, v98\n\t"
"v_accvgpr_write_b32 a63, v99\n\t"
"v_cvt_pk_bf16_f32 v234, v228, v229\n\t"
"v_cvt_pk_bf16_f32 v235, v230, v231\n\t"
"v_fma_f32 v98, v144, s64, -v46\n\t"
"v_fma_f32 v99, v145, s64, -v47\n\t"
"v_mfma_f32_16x16x32_bf16 a[60:63], a[244:247], a[176:179], a[60:63]\n\t"
"ds_read_b64_tr_b16 v[228:229], v246 offset:52736\n\t"
"v_accvgpr_write_b32 a68, v98\n\t"
"v_mfma_f32_16x16x32_bf16 a[60:63], a[248:251], a[180:183], a[60:63]\n\t"
"ds_read_b64_tr_b16 v[230:231], v246 offset:55808\n\t"
"v_fma_f32 v98, v146, s64, -v48\n\t"
"v_mfma_f32_16x16x32_bf16 a[60:63], a[252:255], a[184:187], a[60:63]\n\t"
"ds_write2st64_b64 v245, v[232:233], v[234:235] offset0:53 offset1:54\n\t"
"v_accvgpr_write_b32 a69, v99\n\t"
"v_fma_f32 v99, v147, s64, -v49\n\t"
"v_accvgpr_write_b32 a70, v98\n\t"
"v_accvgpr_write_b32 a71, v99\n\t"
"v_fma_f32 v98, v148, s64, -v50\n\t"
"v_fma_f32 v99, v149, s64, -v51\n\t"
"v_cvt_pk_bf16_f32 v224, v224, v225\n\t"
"v_cvt_pk_bf16_f32 v225, v226, v227\n\t"
"v_mfma_f32_16x16x32_bf16 a[68:71], a[244:247], a[180:183], a[68:71]\n\t"
"v_accvgpr_write_b32 a76, v98\n\t"
"v_fma_f32 v98, v150, s64, -v52\n\t"
"v_mfma_f32_16x16x32_bf16 a[68:71], a[248:251], a[184:187], a[68:71]\n\t"
"v_accvgpr_write_b32 a77, v99\n\t"
"v_fma_f32 v99, v151, s64, -v53\n\t"
"v_mfma_f32_16x16x32_bf16 a[68:71], a[252:255], a[188:191], a[68:71]\n\t"
"v_accvgpr_write_b32 a78, v98\n\t"
"v_accvgpr_write_b32 a79, v99\n\t"
"v_fma_f32 v98, v152, s64, -v54\n\t"
"v_fma_f32 v99, v153, s64, -v55\n\t"
"v_mfma_f32_16x16x32_bf16 a[76:79], a[244:247], a[184:187], a[76:79]\n\t"
"s_waitcnt lgkmcnt(1)\n\t"
"v_accvgpr_write_b32 a84, v98\n\t"
"v_fma_f32 v98, v154, s64, -v56\n\t"
"v_mfma_f32_16x16x32_bf16 a[76:79], a[248:251], a[188:191], a[76:79]\n\t"
"v_accvgpr_write_b32 a85, v99\n\t"
"v_fma_f32 v99, v155, s64, -v57\n\t"
"v_mfma_f32_16x16x32_bf16 a[76:79], a[252:255], a[192:195], a[76:79]\n\t"
"v_accvgpr_write_b32 a86, v98\n\t"
"v_accvgpr_write_b32 a87, v99\n\t"
"v_fma_f32 v98, v156, s64, -v58\n\t"
"v_fma_f32 v99, v157, s64, -v59\n\t"
"v_mfma_f32_16x16x32_bf16 v[228:231], v[228:231], v[220:223], 0\n\t"
"v_accvgpr_write_b32 a92, v98\n\t"
"v_fma_f32 v98, v158, s64, -v60\n\t"
"v_mfma_f32_16x16x32_bf16 a[84:87], a[244:247], a[188:191], a[84:87]\n\t"
"s_mov_b32 s20, 73728\n\t"
"v_accvgpr_write_b32 a93, v99\n\t"
"v_fma_f32 v99, v159, s64, -v61\n\t"
"v_mfma_f32_16x16x32_bf16 a[84:87], a[248:251], a[192:195], a[84:87]\n\t"
"v_accvgpr_write_b32 a94, v98\n\t"
"v_accvgpr_write_b32 a95, v99\n\t"
"v_mfma_f32_16x16x32_bf16 a[84:87], a[252:255], a[196:199], a[84:87]\n\t"
"v_fma_f32 v98, v160, s64, -v62\n\t"
"v_fma_f32 v99, v161, s64, -v63\n\t"
"v_mfma_f32_16x16x32_bf16 a[92:95], a[244:247], a[192:195], a[92:95]\n\t"
"v_accvgpr_write_b32 a100, v98\n\t"
"v_fma_f32 v98, v162, s64, -v64\n\t"
"v_mfma_f32_16x16x32_bf16 a[92:95], a[248:251], a[196:199], a[92:95]\n\t"
"v_accvgpr_write_b32 a101, v99\n\t"
"v_fma_f32 v99, v163, s64, -v65\n\t"
"v_mfma_f32_16x16x32_bf16 a[92:95], a[252:255], a[200:203], a[92:95]\n\t"
"v_accvgpr_write_b32 a102, v98\n\t"
"v_accvgpr_write_b32 a103, v99\n\t"
"v_fma_f32 v98, v164, s64, -v66\n\t"
"v_fma_f32 v99, v165, s64, -v67\n\t"
"v_cvt_pk_bf16_f32 v226, v228, v229\n\t"
"v_cvt_pk_bf16_f32 v227, v230, v231\n\t"
"v_mfma_f32_16x16x32_bf16 a[100:103], a[244:247], a[196:199], a[100:103]\n\t"
"v_accvgpr_write_b32 a108, v98\n\t"
"v_fma_f32 v98, v166, s64, -v68\n\t"
"v_mfma_f32_16x16x32_bf16 a[100:103], a[248:251], a[200:203], a[100:103]\n\t"
"v_accvgpr_write_b32 a109, v99\n\t"
"v_fma_f32 v99, v167, s64, -v69\n\t"
"v_mfma_f32_16x16x32_bf16 a[100:103], a[252:255], a[204:207], a[100:103]\n\t"
"ds_write2st64_b64 v245, v[224:225], v[226:227] offset0:55 offset1:56\n\t"
"v_accvgpr_write_b32 a110, v98\n\t"
"v_accvgpr_write_b32 a111, v99\n\t"
"v_fma_f32 v98, v168, s64, -v70\n\t"
"v_fma_f32 v99, v169, s64, -v71\n\t"
"s_waitcnt vmcnt(12)\n\t"
"v_mfma_f32_16x16x32_bf16 a[108:111], a[232:235], a[168:171], a[108:111]\n\t"
"buffer_load_dwordx4 a[244:247], v252, s[8:11], s20 offen offset:0 sc1\n\t"
"v_mfma_f32_16x16x32_bf16 a[108:111], v[236:239], a[172:175], a[108:111]\n\t"
"v_accvgpr_write_b32 a116, v98\n\t"
"v_mfma_f32_16x16x32_bf16 a[108:111], a[208:211], a[176:179], a[108:111]\n\t"
"v_accvgpr_write_b32 a117, v99\n\t"
"v_fma_f32 v98, v170, s64, -v72\n\t"
"v_accvgpr_write_b32 a118, v98\n\t"
"v_fma_f32 v99, v171, s64, -v73\n\t"
"v_accvgpr_write_b32 a119, v99\n\t"
"s_waitcnt lgkmcnt(0)\n\t"
"s_barrier\n\t"
"v_mfma_f32_16x16x32_bf16 a[116:119], a[232:235], a[172:175], a[116:119]\n\t"
"ds_read_b128 a[168:171], v244 offset:24576\n\t"
"v_mfma_f32_16x16x32_bf16 a[116:119], v[236:239], a[176:179], a[116:119]\n\t"
"buffer_load_dwordx4 a[248:251], v252, s[8:11], s20 offen offset:1024 sc1\n\t"
"v_mfma_f32_16x16x32_bf16 a[116:119], a[208:211], a[180:183], a[116:119]\n\t"
"buffer_load_dwordx4 a[252:255], v252, s[8:11], s20 offen offset:2048 sc1\n\t"
"v_fma_f32 v98, v172, s64, -v74\n\t"
"v_fma_f32 v99, v173, s64, -v75\n\t"
"v_accvgpr_write_b32 a124, v98\n\t"
"v_fma_f32 v98, v174, s64, -v76\n\t"
"v_accvgpr_write_b32 a125, v99\n\t"
"v_fma_f32 v99, v175, s64, -v77\n\t"
"v_accvgpr_write_b32 a126, v98\n\t"
"v_accvgpr_write_b32 a127, v99\n\t"
"v_fma_f32 v98, v176, s64, -v78\n\t"
"v_fma_f32 v99, v177, s64, -v79\n\t"
"v_mfma_f32_16x16x32_bf16 a[124:127], a[232:235], a[176:179], a[124:127]\n\t"
"ds_read_b128 a[172:175], v244 offset:25088\n\t"
"v_accvgpr_write_b32 a132, v98\n\t"
"v_mfma_f32_16x16x32_bf16 a[124:127], v[236:239], a[180:183], a[124:127]\n\t"
"ds_read_b128 a[176:179], v244 offset:25600\n\t"
"v_fma_f32 v98, v178, s64, -v80\n\t"
"v_mfma_f32_16x16x32_bf16 a[124:127], a[208:211], a[184:187], a[124:127]\n\t"
"v_accvgpr_write_b32 a133, v99\n\t"
"v_fma_f32 v99, v179, s64, -v81\n\t"
"v_accvgpr_write_b32 a134, v98\n\t"
"v_accvgpr_write_b32 a135, v99\n\t"
"v_fma_f32 v98, v180, s64, -v82\n\t"
"v_fma_f32 v99, v181, s64, -v83\n\t"
"v_mfma_f32_16x16x32_bf16 a[132:135], a[232:235], a[180:183], a[132:135]\n\t"
"ds_read_b128 a[180:183], v244 offset:26112\n\t"
"v_accvgpr_write_b32 a140, v98\n\t"
"v_mfma_f32_16x16x32_bf16 a[132:135], v[236:239], a[184:187], a[132:135]\n\t"
"v_fma_f32 v98, v182, s64, -v84\n\t"
"v_accvgpr_write_b32 a141, v99\n\t"
"v_mfma_f32_16x16x32_bf16 a[132:135], a[208:211], a[188:191], a[132:135]\n\t"
"v_fma_f32 v99, v183, s64, -v85\n\t"
"v_accvgpr_write_b32 a142, v98\n\t"
"v_accvgpr_write_b32 a143, v99\n\t"
"v_fma_f32 v98, v184, s64, -v86\n\t"
"v_fma_f32 v99, v185, s64, -v87\n\t"
"v_mfma_f32_16x16x32_bf16 a[140:143], a[232:235], a[184:187], a[140:143]\n\t"
"ds_read_b128 a[184:187], v244 offset:26624\n\t"
"v_accvgpr_write_b32 a148, v98\n\t"
"v_mfma_f32_16x16x32_bf16 a[140:143], v[236:239], a[188:191], a[140:143]\n\t"
"v_fma_f32 v98, v186, s64, -v88\n\t"
"v_accvgpr_write_b32 a149, v99\n\t"
"v_mfma_f32_16x16x32_bf16 a[140:143], a[208:211], a[192:195], a[140:143]\n\t"
"v_fma_f32 v99, v187, s64, -v89\n\t"
"v_accvgpr_write_b32 a150, v98\n\t"
"v_accvgpr_write_b32 a151, v99\n\t"
"v_fma_f32 v98, v188, s64, -v90\n\t"
"v_fma_f32 v99, v189, s64, -v91\n\t"
"v_mfma_f32_16x16x32_bf16 a[148:151], a[232:235], a[188:191], a[148:151]\n\t"
"s_mov_b32 s20, 86016\n\t"
"ds_read_b128 a[188:191], v244 offset:27136\n\t"
"v_accvgpr_write_b32 a156, v98\n\t"
"v_mfma_f32_16x16x32_bf16 a[148:151], v[236:239], a[192:195], a[148:151]\n\t"
"v_fma_f32 v98, v190, s64, -v92\n\t"
"v_accvgpr_write_b32 a157, v99\n\t"
"v_mfma_f32_16x16x32_bf16 a[148:151], a[208:211], a[196:199], a[148:151]\n\t"
"v_fma_f32 v99, v191, s64, -v93\n\t"
"v_accvgpr_write_b32 a158, v98\n\t"
"v_accvgpr_write_b32 a159, v99\n\t"
"v_fma_f32 v98, v192, s64, -v94\n\t"
"v_fma_f32 v99, v193, s64, -v95\n\t"
"v_mfma_f32_16x16x32_bf16 a[156:159], a[232:235], a[192:195], a[156:159]\n\t"
"ds_read_b128 a[192:195], v244 offset:27648\n\t"
"v_accvgpr_write_b32 a164, v98\n\t"
"v_mfma_f32_16x16x32_bf16 a[156:159], v[236:239], a[196:199], a[156:159]\n\t"
"v_fma_f32 v98, v194, s64, -v96\n\t"
"v_accvgpr_write_b32 a165, v99\n\t"
"v_mfma_f32_16x16x32_bf16 a[156:159], a[208:211], a[200:203], a[156:159]\n\t"
"s_add_u32 m0, s24, -128\n\t"
"s_add_u32 s28, s21, 0\n\t"
"v_fma_f32 v99, v195, s64, -v97\n\t"
"v_accvgpr_write_b32 a166, v98\n\t"
"v_accvgpr_write_b32 a167, v99\n\t"
"s_nop 1\n\t"
"v_mfma_f32_16x16x32_bf16 a[164:167], a[232:235], a[196:199], a[164:167]\n\t"
"buffer_load_dwordx4 a[232:235], v252, s[8:11], s20 offen offset:0 sc1\n\t"
"v_mfma_f32_16x16x32_bf16 a[164:167], v[236:239], a[200:203], a[164:167]\n\t"
"ds_read_b128 a[196:199], v244 offset:28160\n\t"
"v_fma_f32 v100, -v100, s66, v2\n\t"
"v_mfma_f32_16x16x32_bf16 a[164:167], a[208:211], a[204:207], a[164:167]\n\t"
"ds_read_b128 a[200:203], v244 offset:28672\n\t"
"v_fma_f32 v2, v4, s52, v102\n\t"
"v_fma_f32 v101, -v101, s66, v3\n\t"
"v_fma_f32 v3, v5, s52, v103\n\t"
"v_fma_f32 v102, -v102, s66, v4\n\t"
"v_fma_f32 v103, -v103, s66, v5\n\t"
"v_fma_f32 v4, v6, s52, v104\n\t"
"v_fma_f32 v5, v7, s52, v105\n\t"
"Lst21_%=:\n\t"
"s_waitcnt vmcnt(11) lgkmcnt(6)\n\t"
"v_mfma_f32_16x16x32_bf16 v[0:3], v[240:243], a[168:171], v[0:3]\n\t"
"buffer_load_dwordx4 v247, s[12:15], s28 offen offset:128 nt lds\n\t"
"v_mfma_f32_16x16x32_bf16 v[0:3], a[212:215], a[172:175], v[0:3]\n\t"
"s_add_u32 m0, s24, 6016\n\t"
"s_add_u32 s28, s21, 196608\n\t"
"ds_read_b128 a[204:207], v244 offset:29184\n\t"
"v_mfma_f32_16x16x32_bf16 v[0:3], a[216:219], a[176:179], v[0:3]\n\t"
"buffer_load_dwordx4 v247, s[12:15], s28 offen offset:128 nt lds\n\t"
"v_fma_f32 v104, -v104, s66, v6\n\t"
"v_fma_f32 v6, v8, s52, v106\n\t"
"v_fma_f32 v105, -v105, s66, v7\n\t"
"v_fma_f32 v7, v9, s52, v107\n\t"
"v_fma_f32 v106, -v106, s66, v8\n\t"
"v_fma_f32 v107, -v107, s66, v9\n\t"
"v_fma_f32 v8, v10, s52, v108\n\t"
"v_fma_f32 v9, v11, s52, v109\n\t"
"s_waitcnt lgkmcnt(6)\n\t"
"v_mfma_f32_16x16x32_bf16 v[4:7], v[240:243], a[172:175], v[4:7]\n\t"
"s_add_u32 m0, s24, 59776\n\t"
"s_add_u32 s28, s21, 1769472\n\t"
"v_fma_f32 v108, -v108, s66, v10\n\t"
"v_mfma_f32_16x16x32_bf16 v[4:7], a[212:215], a[176:179], v[4:7]\n\t"
"v_fma_f32 v10, v12, s52, v110\n\t"
"v_fma_f32 v109, -v109, s66, v11\n\t"
"v_mfma_f32_16x16x32_bf16 v[4:7], a[216:219], a[180:183], v[4:7]\n\t"
"buffer_load_dwordx4 v247, s[12:15], s28 offen offset:128 nt lds\n\t"
"v_fma_f32 v11, v13, s52, v111\n\t"
"v_fma_f32 v110, -v110, s66, v12\n\t"
"v_fma_f32 v111, -v111, s66, v13\n\t"
"v_fma_f32 v12, v14, s52, v112\n\t"
"v_fma_f32 v13, v15, s52, v113\n\t"
"s_add_u32 m0, s24, 65920\n\t"
"s_add_u32 s28, s21, 1966080\n\t"
"s_waitcnt lgkmcnt(5)\n\t"
"v_mfma_f32_16x16x32_bf16 v[8:11], v[240:243], a[176:179], v[8:11]\n\t"
"buffer_load_dwordx4 v247, s[12:15], s28 offen offset:128 nt lds\n\t"
"v_mfma_f32_16x16x32_bf16 v[8:11], a[212:215], a[180:183], v[8:11]\n\t"
"s_add_u32 m0, s24, 12160\n\t"
"s_add_u32 s28, s21, 393216\n\t"
"v_fma_f32 v112, -v112, s66, v14\n\t"
"v_mfma_f32_16x16x32_bf16 v[8:11], a[216:219], a[184:187], v[8:11]\n\t"
"v_fma_f32 v14, v16, s52, v114\n\t"
"v_fma_f32 v113, -v113, s66, v15\n\t"
"v_fma_f32 v15, v17, s52, v115\n\t"
"v_fma_f32 v114, -v114, s66, v16\n\t"
"v_fma_f32 v115, -v115, s66, v17\n\t"
"v_fma_f32 v16, v18, s52, v116\n\t"
"v_fma_f32 v17, v19, s52, v117\n\t"
"s_waitcnt lgkmcnt(4)\n\t"
"v_mfma_f32_16x16x32_bf16 v[12:15], v[240:243], a[180:183], v[12:15]\n\t"
"buffer_load_dwordx4 v247, s[12:15], s28 offen offset:128 nt lds\n\t"
"v_mfma_f32_16x16x32_bf16 v[12:15], a[212:215], a[184:187], v[12:15]\n\t"
"s_add_u32 m0, s24, 19840\n\t"
"s_add_u32 s28, s21, 589824\n\t"
"v_fma_f32 v116, -v116, s66, v18\n\t"
"v_mfma_f32_16x16x32_bf16 v[12:15], a[216:219], a[188:191], v[12:15]\n\t"
"buffer_load_dwordx4 v247, s[12:15], s28 offen offset:128 nt lds\n\t"
"v_fma_f32 v18, v20, s52, v118\n\t"
"v_fma_f32 v117, -v117, s66, v19\n\t"
"v_fma_f32 v19, v21, s52, v119\n\t"
"v_fma_f32 v118, -v118, s66, v20\n\t"
"v_fma_f32 v119, -v119, s66, v21\n\t"
"v_fma_f32 v20, v22, s52, v120\n\t"
"v_fma_f32 v21, v23, s52, v121\n\t"
"s_waitcnt lgkmcnt(3)\n\t"
"v_mfma_f32_16x16x32_bf16 v[16:19], v[240:243], a[184:187], v[16:19]\n\t"
"v_fma_f32 v120, -v120, s66, v22\n\t"
"v_fma_f32 v22, v24, s52, v122\n\t"
"v_mfma_f32_16x16x32_bf16 v[16:19], a[212:215], a[188:191], v[16:19]\n\t"
"v_fma_f32 v121, -v121, s66, v23\n\t"
"v_fma_f32 v23, v25, s52, v123\n\t"
"v_mfma_f32_16x16x32_bf16 v[16:19], a[216:219], a[192:195], v[16:19]\n\t"
"ds_read_b64_tr_b16 v[232:233], v246 offset:1024\n\t"
"v_fma_f32 v122, -v122, s66, v24\n\t"
"v_fma_f32 v123, -v123, s66, v25\n\t"
"v_fma_f32 v24, v26, s52, v124\n\t"
"v_fma_f32 v25, v27, s52, v125\n\t"
"s_waitcnt lgkmcnt(3)\n\t"
"v_mfma_f32_16x16x32_bf16 v[20:23], v[240:243], a[188:191], v[20:23]\n\t"
"ds_read_b64_tr_b16 v[234:235], v246 offset:4096\n\t"
"v_fma_f32 v124, -v124, s66, v26\n\t"
"v_mfma_f32_16x16x32_bf16 v[20:23], a[212:215], a[192:195], v[20:23]\n\t"
"v_fma_f32 v26, v28, s52, v126\n\t"
"v_fma_f32 v125, -v125, s66, v27\n\t"
"v_mfma_f32_16x16x32_bf16 v[20:23], a[216:219], a[196:199], v[20:23]\n\t"
"v_fma_f32 v27, v29, s52, v127\n\t"
"v_fma_f32 v126, -v126, s66, v28\n\t"
"v_fma_f32 v127, -v127, s66, v29\n\t"
"v_fma_f32 v28, v30, s52, v128\n\t"
"v_fma_f32 v29, v31, s52, v129\n\t"
"s_waitcnt lgkmcnt(3)\n\t"
"v_mfma_f32_16x16x32_bf16 v[24:27], v[240:243], a[192:195], v[24:27]\n\t"
"v_fma_f32 v128, -v128, s66, v30\n\t"
"v_fma_f32 v30, v32, s52, v130\n\t"
"v_mfma_f32_16x16x32_bf16 v[24:27], a[212:215], a[196:199], v[24:27]\n\t"
"v_fma_f32 v129, -v129, s66, v31\n\t"
"v_fma_f32 v31, v33, s52, v131\n\t"
"v_mfma_f32_16x16x32_bf16 v[24:27], a[216:219], a[200:203], v[24:27]\n\t"
"v_fma_f32 v130, -v130, s66, v32\n\t"
"v_fma_f32 v131, -v131, s66, v33\n\t"
"v_fma_f32 v32, v34, s52, v132\n\t"
"v_fma_f32 v33, v35, s52, v133\n\t"
"s_waitcnt lgkmcnt(2)\n\t"
"v_mfma_f32_16x16x32_bf16 v[28:31], v[240:243], a[196:199], v[28:31]\n\t"
"ds_read_b64_tr_b16 v[228:229], v246 offset:7168\n\t"
"v_fma_f32 v132, -v132, s66, v34\n\t"
"v_mfma_f32_16x16x32_bf16 v[28:31], a[212:215], a[200:203], v[28:31]\n\t"
"ds_read_b64_tr_b16 v[230:231], v246 offset:10240\n\t"
"v_fma_f32 v34, v36, s52, v134\n\t"
"v_mfma_f32_16x16x32_bf16 v[28:31], a[216:219], a[204:207], v[28:31]\n\t"
"v_fma_f32 v133, -v133, s66, v35\n\t"
"v_fma_f32 v35, v37, s52, v135\n\t"
"v_fma_f32 v134, -v134, s66, v36\n\t"
"v_fma_f32 v135, -v135, s66, v37\n\t"
"v_fma_f32 v36, v38, s52, v136\n\t"
"v_fma_f32 v37, v39, s52, v137\n\t"
"s_waitcnt vmcnt(13)\n\t"
"v_mfma_f32_16x16x32_bf16 v[32:35], a[220:223], a[168:171], v[32:35]\n\t"
"v_fma_f32 v136, -v136, s66, v38\n\t"
"v_fma_f32 v38, v40, s52, v138\n\t"
"v_mfma_f32_16x16x32_bf16 v[32:35], a[224:227], a[172:175], v[32:35]\n\t"
"v_fma_f32 v137, -v137, s66, v39\n\t"
"v_fma_f32 v39, v41, s52, v139\n\t"
"v_mfma_f32_16x16x32_bf16 v[32:35], v[248:251], a[176:179], v[32:35]\n\t"
"v_fma_f32 v138, -v138, s66, v40\n\t"
"v_fma_f32 v139, -v139, s66, v41\n\t"
"v_mfma_f32_16x16x32_bf16 v[36:39], a[220:223], a[172:175], v[36:39]\n\t"
"s_waitcnt lgkmcnt(2)\n\t"
"v_fma_f32 v40, v42, s52, v140\n\t"
"v_fma_f32 v41, v43, s52, v141\n\t"
"v_mfma_f32_16x16x32_bf16 v[36:39], a[224:227], a[176:179], v[36:39]\n\t"
"v_fma_f32 v140, -v140, s66, v42\n\t"
"v_fma_f32 v42, v44, s52, v142\n\t"
"v_mfma_f32_16x16x32_bf16 v[36:39], v[248:251], a[180:183], v[36:39]\n\t"
"v_fma_f32 v141, -v141, s66, v43\n\t"
"v_fma_f32 v43, v45, s52, v143\n\t"
"v_fma_f32 v142, -v142, s66, v44\n\t"
"v_fma_f32 v143, -v143, s66, v45\n\t"
"v_mfma_f32_16x16x32_bf16 v[232:235], v[232:235], v[220:223], 0\n\t"
"v_fma_f32 v44, v46, s52, v144\n\t"
"v_fma_f32 v45, v47, s52, v145\n\t"
"v_mfma_f32_16x16x32_bf16 v[40:43], a[220:223], a[176:179], v[40:43]\n\t"
"ds_read_b64_tr_b16 v[224:225], v246 offset:13312\n\t"
"v_fma_f32 v144, -v144, s66, v46\n\t"
"v_mfma_f32_16x16x32_bf16 v[40:43], a[224:227], a[180:183], v[40:43]\n\t"
"ds_read_b64_tr_b16 v[226:227], v246 offset:16384\n\t"
"v_fma_f32 v46, v48, s52, v146\n\t"
"v_mfma_f32_16x16x32_bf16 v[40:43], v[248:251], a[184:187], v[40:43]\n\t"
"buffer_load_dwordx4 v[240:243], v252, s[8:11], s20 offen offset:1024 sc1\n\t"
"v_fma_f32 v145, -v145, s66, v47\n\t"
"v_fma_f32 v47, v49, s52, v147\n\t"
"v_fma_f32 v146, -v146, s66, v48\n\t"
"v_fma_f32 v147, -v147, s66, v49\n\t"
"v_mfma_f32_16x16x32_bf16 v[44:47], a[220:223], a[180:183], v[44:47]\n\t"
"s_waitcnt lgkmcnt(2)\n\t"
"v_fma_f32 v48, v50, s52, v148\n\t"
"v_fma_f32 v49, v51, s52, v149\n\t"
"v_mfma_f32_16x16x32_bf16 v[44:47], a[224:227], a[184:187], v[44:47]\n\t"
"v_fma_f32 v148, -v148, s66, v50\n\t"
"v_fma_f32 v50, v52, s52, v150\n\t"
"v_mfma_f32_16x16x32_bf16 v[44:47], v[248:251], a[188:191], v[44:47]\n\t"
"v_fma_f32 v149, -v149, s66, v51\n\t"
"v_fma_f32 v51, v53, s52, v151\n\t"
"v_fma_f32 v150, -v150, s66, v52\n\t"
"v_fma_f32 v151, -v151, s66, v53\n\t"
"v_mfma_f32_16x16x32_bf16 v[228:231], v[228:231], v[220:223], 0\n\t"
"v_fma_f32 v52, v54, s52, v152\n\t"
"v_fma_f32 v53, v55, s52, v153\n\t"
"v_mfma_f32_16x16x32_bf16 v[48:51], a[220:223], a[184:187], v[48:51]\n\t"
"v_fma_f32 v152, -v152, s66, v54\n\t"
"v_fma_f32 v54, v56, s52, v154\n\t"
"v_mfma_f32_16x16x32_bf16 v[48:51], a[224:227], a[188:191], v[48:51]\n\t"
"v_fma_f32 v153, -v153, s66, v55\n\t"
"v_fma_f32 v55, v57, s52, v155\n\t"
"v_mfma_f32_16x16x32_bf16 v[48:51], v[248:251], a[192:195], v[48:51]\n\t"
"v_fma_f32 v154, -v154, s66, v56\n\t"
"v_fma_f32 v155, -v155, s66, v57\n\t"
"v_cvt_pk_bf16_f32 v232, v232, v233\n\t"
"v_cvt_pk_bf16_f32 v233, v234, v235\n\t"
"v_fma_f32 v56, v58, s52, v156\n\t"
"v_fma_f32 v57, v59, s52, v157\n\t"
"v_mfma_f32_16x16x32_bf16 v[52:55], a[220:223], a[188:191], v[52:55]\n\t"
"v_fma_f32 v156, -v156, s66, v58\n\t"
"v_fma_f32 v58, v60, s52, v158\n\t"
"v_mfma_f32_16x16x32_bf16 v[52:55], a[224:227], a[192:195], v[52:55]\n\t"
"v_fma_f32 v157, -v157, s66, v59\n\t"
"v_fma_f32 v59, v61, s52, v159\n\t"
"v_mfma_f32_16x16x32_bf16 v[52:55], v[248:251], a[196:199], v[52:55]\n\t"
"v_fma_f32 v158, -v158, s66, v60\n\t"
"v_fma_f32 v159, -v159, s66, v61\n\t"
"v_mfma_f32_16x16x32_bf16 v[56:59], a[220:223], a[192:195], v[56:59]\n\t"
"s_waitcnt lgkmcnt(0)\n\t"
"v_fma_f32 v60, v62, s52, v160\n\t"
"v_fma_f32 v61, v63, s52, v161\n\t"
"v_mfma_f32_16x16x32_bf16 v[56:59], a[224:227], a[196:199], v[56:59]\n\t"
"v_fma_f32 v160, -v160, s66, v62\n\t"
"v_fma_f32 v62, v64, s52, v162\n\t"
"v_mfma_f32_16x16x32_bf16 v[56:59], v[248:251], a[200:203], v[56:59]\n\t"
"v_fma_f32 v161, -v161, s66, v63\n\t"
"v_fma_f32 v63, v65, s52, v163\n\t"
"v_fma_f32 v162, -v162, s66, v64\n\t"
"v_fma_f32 v163, -v163, s66, v65\n\t"
"v_cvt_pk_bf16_f32 v234, v228, v229\n\t"
"v_cvt_pk_bf16_f32 v235, v230, v231\n\t"
"v_mfma_f32_16x16x32_bf16 v[224:227], v[224:227], v[220:223], 0\n\t"
"ds_read_b64_tr_b16 v[228:229], v246 offset:20992\n\t"
"v_fma_f32 v64, v66, s52, v164\n\t"
"v_fma_f32 v65, v67, s52, v165\n\t"
"v_mfma_f32_16x16x32_bf16 v[60:63], a[220:223], a[196:199], v[60:63]\n\t"
"ds_read_b64_tr_b16 v[230:231], v246 offset:24064\n\t"
"v_fma_f32 v164, -v164, s66, v66\n\t"
"v_mfma_f32_16x16x32_bf16 v[60:63], a[224:227], a[200:203], v[60:63]\n\t"
"v_fma_f32 v66, v68, s52, v166\n\t"
"v_fma_f32 v165, -v165, s66, v67\n\t"
"v_mfma_f32_16x16x32_bf16 v[60:63], v[248:251], a[204:207], v[60:63]\n\t"
"v_fma_f32 v67, v69, s52, v167\n\t"
"v_fma_f32 v166, -v166, s66, v68\n\t"
"v_fma_f32 v167, -v167, s66, v69\n\t"
"v_fma_f32 v68, v70, s52, v168\n\t"
"v_fma_f32 v69, v71, s52, v169\n\t"
"s_waitcnt vmcnt(11)\n\t"
"v_mfma_f32_16x16x32_bf16 v[64:67], a[228:231], a[168:171], v[64:67]\n\t"
"ds_read_b128 a[168:171], v244 offset:33792\n\t"
"v_fma_f32 v168, -v168, s66, v70\n\t"
"v_mfma_f32_16x16x32_bf16 v[64:67], a[236:239], a[172:175], v[64:67]\n\t"
"v_fma_f32 v70, v72, s52, v170\n\t"
"v_fma_f32 v169, -v169, s66, v71\n\t"
"v_mfma_f32_16x16x32_bf16 v[64:67], a[240:243], a[176:179], v[64:67]\n\t"
"v_fma_f32 v71, v73, s52, v171\n\t"
"v_fma_f32 v170, -v170, s66, v72\n\t"
"v_fma_f32 v171, -v171, s66, v73\n\t"
"v_mfma_f32_16x16x32_bf16 v[68:71], a[228:231], a[172:175], v[68:71]\n\t"
"s_waitcnt lgkmcnt(1)\n\t"
"ds_read_b128 a[172:175], v244 offset:34304\n\t"
"v_fma_f32 v72, v74, s52, v172\n\t"
"v_mfma_f32_16x16x32_bf16 v[68:71], a[236:239], a[176:179], v[68:71]\n\t"
"v_fma_f32 v73, v75, s52, v173\n\t"
"v_fma_f32 v172, -v172, s66, v74\n\t"
"v_mfma_f32_16x16x32_bf16 v[68:71], a[240:243], a[180:183], v[68:71]\n\t"
"v_fma_f32 v74, v76, s52, v174\n\t"
"v_fma_f32 v173, -v173, s66, v75\n\t"
"v_fma_f32 v75, v77, s52, v175\n\t"
"v_fma_f32 v174, -v174, s66, v76\n\t"
"v_fma_f32 v175, -v175, s66, v77\n\t"
"v_cvt_pk_bf16_f32 v224, v224, v225\n\t"
"v_cvt_pk_bf16_f32 v225, v226, v227\n\t"
"v_mfma_f32_16x16x32_bf16 v[228:231], v[228:231], v[220:223], 0\n\t"
"buffer_load_dwordx4 a[208:211], v252, s[8:11], s20 offen offset:2048 sc1\n\t"
"v_fma_f32 v76, v78, s52, v176\n\t"
"v_fma_f32 v77, v79, s52, v177\n\t"
"s_mov_b32 s20, 98304\n\t"
"v_mfma_f32_16x16x32_bf16 v[72:75], a[228:231], a[176:179], v[72:75]\n\t"
"buffer_load_dwordx4 v[236:239], v252, s[8:11], s20 offen offset:0 sc1\n\t"
"v_mfma_f32_16x16x32_bf16 v[72:75], a[236:239], a[180:183], v[72:75]\n\t"
"ds_read_b128 a[176:179], v244 offset:34816\n\t"
"v_fma_f32 v176, -v176, s66, v78\n\t"
"v_mfma_f32_16x16x32_bf16 v[72:75], a[240:243], a[184:187], v[72:75]\n\t"
"v_fma_f32 v78, v80, s52, v178\n\t"
"v_fma_f32 v177, -v177, s66, v79\n\t"
"v_fma_f32 v79, v81, s52, v179\n\t"
"v_fma_f32 v178, -v178, s66, v80\n\t"
"v_fma_f32 v179, -v179, s66, v81\n\t"
"v_fma_f32 v80, v82, s52, v180\n\t"
"v_fma_f32 v81, v83, s52, v181\n\t"
"v_mfma_f32_16x16x32_bf16 v[76:79], a[228:231], a[180:183], v[76:79]\n\t"
"ds_read_b128 a[180:183], v244 offset:35328\n\t"
"v_fma_f32 v180, -v180, s66, v82\n\t"
"v_mfma_f32_16x16x32_bf16 v[76:79], a[236:239], a[184:187], v[76:79]\n\t"
"v_fma_f32 v82, v84, s52, v182\n\t"
"v_fma_f32 v181, -v181, s66, v83\n\t"
"v_mfma_f32_16x16x32_bf16 v[76:79], a[240:243], a[188:191], v[76:79]\n\t"
"buffer_load_dwordx4 v[248:251], v252, s[8:11], s20 offen offset:1024 sc1\n\t"
"v_fma_f32 v83, v85, s52, v183\n\t"
"v_fma_f32 v182, -v182, s66, v84\n\t"
"v_fma_f32 v183, -v183, s66, v85\n\t"
"v_fma_f32 v84, v86, s52, v184\n\t"
"v_fma_f32 v85, v87, s52, v185\n\t"
"v_mfma_f32_16x16x32_bf16 v[80:83], a[228:231], a[184:187], v[80:83]\n\t"
"ds_read_b128 a[184:187], v244 offset:35840\n\t"
"v_fma_f32 v184, -v184, s66, v86\n\t"
"v_mfma_f32_16x16x32_bf16 v[80:83], a[236:239], a[188:191], v[80:83]\n\t"
"v_fma_f32 v86, v88, s52, v186\n\t"
"v_fma_f32 v185, -v185, s66, v87\n\t"
"v_mfma_f32_16x16x32_bf16 v[80:83], a[240:243], a[192:195], v[80:83]\n\t"
"buffer_load_dwordx4 a[212:215], v252, s[8:11], s20 offen offset:2048 sc1\n\t"
"v_fma_f32 v87, v89, s52, v187\n\t"
"v_fma_f32 v186, -v186, s66, v88\n\t"
"v_fma_f32 v187, -v187, s66, v89\n\t"
"v_cvt_pk_bf16_f32 v226, v228, v229\n\t"
"v_cvt_pk_bf16_f32 v227, v230, v231\n\t"
"v_fma_f32 v88, v90, s52, v188\n\t"
"v_fma_f32 v89, v91, s52, v189\n\t"
"s_mov_b32 s20, 331776\n\t"
"v_mfma_f32_16x16x32_bf16 v[84:87], a[228:231], a[188:191], v[84:87]\n\t"
"ds_read_b64_tr_b16 v[228:229], v246 offset:27136\n\t"
"v_fma_f32 v188, -v188, s66, v90\n\t"
"v_mfma_f32_16x16x32_bf16 v[84:87], a[236:239], a[192:195], v[84:87]\n\t"
"ds_read_b64_tr_b16 v[230:231], v246 offset:30208\n\t"
"v_fma_f32 v90, v92, s52, v190\n\t"
"v_mfma_f32_16x16x32_bf16 v[84:87], a[240:243], a[196:199], v[84:87]\n\t"
"buffer_load_dwordx4 a[216:219], v252, s[8:11], s20 offen offset:0 sc1\n\t"
"v_fma_f32 v189, -v189, s66, v91\n\t"
"v_fma_f32 v91, v93, s52, v191\n\t"
"v_fma_f32 v190, -v190, s66, v92\n\t"
"v_fma_f32 v191, -v191, s66, v93\n\t"
"v_fma_f32 v92, v94, s52, v192\n\t"
"v_fma_f32 v93, v95, s52, v193\n\t"
"v_mfma_f32_16x16x32_bf16 v[88:91], a[228:231], a[192:195], v[88:91]\n\t"
"ds_read_b128 a[188:191], v244 offset:36352\n\t"
"v_fma_f32 v192, -v192, s66, v94\n\t"
"v_mfma_f32_16x16x32_bf16 v[88:91], a[236:239], a[196:199], v[88:91]\n\t"
"ds_read_b128 a[192:195], v244 offset:36864\n\t"
"v_fma_f32 v94, v96, s52, v194\n\t"
"v_mfma_f32_16x16x32_bf16 v[88:91], a[240:243], a[200:203], v[88:91]\n\t"
"buffer_load_dwordx4 a[220:223], v252, s[8:11], s20 offen offset:1024 sc1\n\t"
"v_fma_f32 v193, -v193, s66, v95\n\t"
"v_fma_f32 v95, v97, s52, v195\n\t"
"v_fma_f32 v194, -v194, s66, v96\n\t"
"v_fma_f32 v195, -v195, s66, v97\n\t"
"v_mfma_f32_16x16x32_bf16 v[92:95], a[228:231], a[196:199], v[92:95]\n\t"
"s_waitcnt lgkmcnt(2)\n\t"
"ds_read_b128 a[196:199], v244 offset:37376\n\t"
"v_mfma_f32_16x16x32_bf16 v[92:95], a[236:239], a[200:203], v[92:95]\n\t"
"buffer_load_dwordx4 v[96:99], v252, s[8:11], s20 offen offset:2048 sc1\n\t"
"v_mfma_f32_16x16x32_bf16 v[92:95], a[240:243], a[204:207], v[92:95]\n\t"
"s_waitcnt vmcnt(15)\n\t"
"ds_read_b128 a[200:203], v244 offset:37888\n\t"
"s_mov_b32 s20, 344064\n\t"
"v_mfma_f32_16x16x32_bf16 v[228:231], v[228:231], v[220:223], 0\n\t"
"ds_read_b128 a[204:207], v244 offset:38400\n\t"
"v_mfma_f32_16x16x32_bf16 v[100:103], a[244:247], a[168:171], v[100:103]\n\t"
"buffer_load_dwordx4 a[224:227], v252, s[8:11], s20 offen offset:0 sc1\n\t"
"v_mfma_f32_16x16x32_bf16 v[100:103], a[248:251], a[172:175], v[100:103]\n\t"
"buffer_load_dwordx4 a[228:231], v252, s[8:11], s20 offen offset:1024 sc1\n\t"
"v_mfma_f32_16x16x32_bf16 v[100:103], a[252:255], a[176:179], v[100:103]\n\t"
"v_mfma_f32_16x16x32_bf16 v[104:107], a[244:247], a[172:175], v[104:107]\n\t"
"v_mfma_f32_16x16x32_bf16 v[104:107], a[248:251], a[176:179], v[104:107]\n\t"
"v_cvt_pk_bf16_f32 v228, v228, v229\n\t"
"v_mfma_f32_16x16x32_bf16 v[104:107], a[252:255], a[180:183], v[104:107]\n\t"
"s_waitcnt lgkmcnt(0)\n\t"
"s_barrier\n\t"
"v_mfma_f32_16x16x32_bf16 v[108:111], a[244:247], a[176:179], v[108:111]\n\t"
"v_cvt_pk_bf16_f32 v229, v230, v231\n\t"
"v_mfma_f32_16x16x32_bf16 v[108:111], a[248:251], a[180:183], v[108:111]\n\t"
"buffer_load_dwordx4 a[236:239], v252, s[8:11], s20 offen offset:2048 sc1\n\t"
"v_mfma_f32_16x16x32_bf16 v[108:111], a[252:255], a[184:187], v[108:111]\n\t"
"s_mov_b32 s20, 356352\n\t"
"ds_write2st64_b64 v245, v[232:233], v[234:235] offset0:48 offset1:49\n\t"
"v_mfma_f32_16x16x32_bf16 v[112:115], a[244:247], a[180:183], v[112:115]\n\t"
"buffer_load_dwordx4 a[240:243], v252, s[8:11], s20 offen offset:0 sc1\n\t"
"v_mfma_f32_16x16x32_bf16 v[112:115], a[248:251], a[184:187], v[112:115]\n\t"
"ds_read_b64_tr_b16 v[232:233], v246 offset:33280\n\t"
"v_mfma_f32_16x16x32_bf16 v[112:115], a[252:255], a[188:191], v[112:115]\n\t"
"ds_write2st64_b64 v245, v[224:225], v[226:227] offset0:50 offset1:51\n\t"
"v_mfma_f32_16x16x32_bf16 v[116:119], a[244:247], a[184:187], v[116:119]\n\t"
"v_mfma_f32_16x16x32_bf16 v[116:119], a[248:251], a[188:191], v[116:119]\n\t"
"ds_read_b64_tr_b16 v[234:235], v246 offset:36352\n\t"
"v_mfma_f32_16x16x32_bf16 v[116:119], a[252:255], a[192:195], v[116:119]\n\t"
"ds_write_b64 v245, v[228:229] offset:26624\n\t"
"v_mfma_f32_16x16x32_bf16 v[120:123], a[244:247], a[188:191], v[120:123]\n\t"
"ds_read_b64_tr_b16 v[224:225], v246 offset:40960\n\t"
"v_mfma_f32_16x16x32_bf16 v[120:123], a[248:251], a[192:195], v[120:123]\n\t"
"ds_read_b64_tr_b16 v[226:227], v246 offset:44032\n\t"
"v_mfma_f32_16x16x32_bf16 v[120:123], a[252:255], a[196:199], v[120:123]\n\t"
"s_waitcnt lgkmcnt(3)\n\t"
"ds_read_b64_tr_b16 v[228:229], v246 offset:47104\n\t"
"v_mfma_f32_16x16x32_bf16 v[232:235], v[232:235], v[220:223], 0\n\t"
"ds_read_b64_tr_b16 v[230:231], v246 offset:50176\n\t"
"v_mfma_f32_16x16x32_bf16 v[124:127], a[244:247], a[192:195], v[124:127]\n\t"
"v_mfma_f32_16x16x32_bf16 v[124:127], a[248:251], a[196:199], v[124:127]\n\t"
"v_mfma_f32_16x16x32_bf16 v[124:127], a[252:255], a[200:203], v[124:127]\n\t"
"s_waitcnt lgkmcnt(2)\n\t"
"v_mfma_f32_16x16x32_bf16 v[224:227], v[224:227], v[220:223], 0\n\t"
"v_mfma_f32_16x16x32_bf16 v[128:131], a[244:247], a[196:199], v[128:131]\n\t"
"buffer_load_dwordx4 a[244:247], v252, s[8:11], s20 offen offset:1024 sc1\n\t"
"v_mfma_f32_16x16x32_bf16 v[128:131], a[248:251], a[200:203], v[128:131]\n\t"
"s_waitcnt vmcnt(11) lgkmcnt(0)\n\t"
"v_cvt_pk_bf16_f32 v232, v232, v233\n\t"
"v_cvt_pk_bf16_f32 v233, v234, v235\n\t"
"v_mfma_f32_16x16x32_bf16 v[128:131], a[252:255], a[204:207], v[128:131]\n\t"
"v_mfma_f32_16x16x32_bf16 v[228:231], v[228:231], v[220:223], 0\n\t"
"buffer_load_dwordx4 a[248:251], v252, s[8:11], s20 offen offset:2048 sc1\n\t"
"v_mfma_f32_16x16x32_bf16 v[132:135], a[232:235], a[168:171], v[132:135]\n\t"
"s_mov_b32 s20, 368640\n\t"
"v_cvt_pk_bf16_f32 v234, v224, v225\n\t"
"v_cvt_pk_bf16_f32 v235, v226, v227\n\t"
"v_mfma_f32_16x16x32_bf16 v[132:135], v[240:243], a[172:175], v[132:135]\n\t"
"ds_read_b64_tr_b16 v[224:225], v246 offset:53248\n\t"
"v_mfma_f32_16x16x32_bf16 v[132:135], a[208:211], a[176:179], v[132:135]\n\t"
"buffer_load_dwordx4 a[252:255], v252, s[8:11], s20 offen offset:0 sc1\n\t"
"v_mfma_f32_16x16x32_bf16 v[136:139], a[232:235], a[172:175], v[136:139]\n\t"
"ds_read_b64_tr_b16 v[226:227], v246 offset:56320\n\t"
"v_mfma_f32_16x16x32_bf16 v[136:139], v[240:243], a[176:179], v[136:139]\n\t"
"v_cvt_pk_bf16_f32 v228, v228, v229\n\t"
"v_cvt_pk_bf16_f32 v229, v230, v231\n\t"
"v_mfma_f32_16x16x32_bf16 v[136:139], a[208:211], a[180:183], v[136:139]\n\t"
"ds_write2st64_b64 v245, v[232:233], v[234:235] offset0:53 offset1:54\n\t"
"v_mfma_f32_16x16x32_bf16 v[140:143], a[232:235], a[176:179], v[140:143]\n\t"
"v_mfma_f32_16x16x32_bf16 v[140:143], v[240:243], a[180:183], v[140:143]\n\t"
"v_mfma_f32_16x16x32_bf16 v[140:143], a[208:211], a[184:187], v[140:143]\n\t"
"v_mfma_f32_16x16x32_bf16 v[144:147], a[232:235], a[180:183], v[144:147]\n\t"
"v_mfma_f32_16x16x32_bf16 v[144:147], v[240:243], a[184:187], v[144:147]\n\t"
"v_mfma_f32_16x16x32_bf16 v[144:147], a[208:211], a[188:191], v[144:147]\n\t"
"v_mfma_f32_16x16x32_bf16 v[148:151], a[232:235], a[184:187], v[148:151]\n\t"
"s_waitcnt lgkmcnt(1)\n\t"
"s_add_u32 m0, s24, 25984\n\t"
"s_add_u32 s28, s21, 786432\n\t"
"v_mfma_f32_16x16x32_bf16 v[148:151], v[240:243], a[188:191], v[148:151]\n\t"
"v_mfma_f32_16x16x32_bf16 v[148:151], a[208:211], a[192:195], v[148:151]\n\t"
"v_mfma_f32_16x16x32_bf16 v[224:227], v[224:227], v[220:223], 0\n\t"
"v_mfma_f32_16x16x32_bf16 v[152:155], a[232:235], a[188:191], v[152:155]\n\t"
"buffer_load_dwordx4 v247, s[12:15], s28 offen offset:128 nt lds\n\t"
"v_mfma_f32_16x16x32_bf16 v[152:155], v[240:243], a[192:195], v[152:155]\n\t"
"s_add_u32 m0, s24, 72064\n\t"
"s_add_u32 s28, s21, 2162688\n\t"
"v_mfma_f32_16x16x32_bf16 v[152:155], a[208:211], a[196:199], v[152:155]\n\t"
"buffer_load_dwordx4 v247, s[12:15], s28 offen offset:128 nt lds\n\t"
"v_mfma_f32_16x16x32_bf16 v[156:159], a[232:235], a[192:195], v[156:159]\n\t"
"s_add_u32 m0, s24, 79744\n\t"
"s_add_u32 s28, s21, 2359296\n\t"
"v_mfma_f32_16x16x32_bf16 v[156:159], v[240:243], a[196:199], v[156:159]\n\t"
"buffer_load_dwordx4 v247, s[12:15], s28 offen offset:128 nt lds\n\t"
"v_mfma_f32_16x16x32_bf16 v[156:159], a[208:211], a[200:203], v[156:159]\n\t"
"s_add_u32 m0, s24, 85888\n\t"
"s_add_u32 s28, s21, 2555904\n\t"
"v_cvt_pk_bf16_f32 v230, v224, v225\n\t"
"v_cvt_pk_bf16_f32 v231, v226, v227\n\t"
"v_mfma_f32_16x16x32_bf16 v[160:163], a[232:235], a[196:199], v[160:163]\n\t"
"s_waitcnt vmcnt(13)\n\t"
"v_mfma_f32_16x16x32_bf16 v[160:163], v[240:243], a[200:203], v[160:163]\n\t"
"buffer_load_dwordx4 v247, s[12:15], s28 offen offset:128 nt lds\n\t"
"v_mfma_f32_16x16x32_bf16 v[160:163], a[208:211], a[204:207], v[160:163]\n\t"
"s_add_u32 m0, s25, -128\n\t"
"ds_write2st64_b64 v245, v[228:229], v[230:231] offset0:55 offset1:56\n\t"
"v_mfma_f32_16x16x32_bf16 v[164:167], v[236:239], a[168:171], v[164:167]\n\t"
"buffer_load_dwordx4 v254, s[12:15], 0 offen offset:128 nt lds\n\t"
"v_mfma_f32_16x16x32_bf16 v[164:167], v[248:251], a[172:175], v[164:167]\n\t"
"s_add_u32 m0, s26, -128\n\t"
"v_mfma_f32_16x16x32_bf16 v[164:167], a[212:215], a[176:179], v[164:167]\n\t"
"s_waitcnt lgkmcnt(0)\n\t"
"s_barrier\n\t"
"v_mfma_f32_16x16x32_bf16 v[168:171], v[236:239], a[172:175], v[168:171]\n\t"
"ds_read_b128 a[168:171], v244 offset:24576\n\t"
"v_mfma_f32_16x16x32_bf16 v[168:171], v[248:251], a[176:179], v[168:171]\n\t"
"ds_read_b128 a[172:175], v244 offset:25088\n\t"
"v_mfma_f32_16x16x32_bf16 v[168:171], a[212:215], a[180:183], v[168:171]\n\t"
"s_mov_b64 exec, s[36:37]\n\t"
"buffer_load_dwordx4 v254, s[12:15], s27 offen offset:128 nt lds\n\t"
"s_mov_b64 exec, -1\n\t"
"v_mfma_f32_16x16x32_bf16 v[172:175], v[236:239], a[176:179], v[172:175]\n\t"
"ds_read_b128 a[176:179], v244 offset:25600\n\t"
"v_mfma_f32_16x16x32_bf16 v[172:175], v[248:251], a[180:183], v[172:175]\n\t"
"v_mfma_f32_16x16x32_bf16 v[172:175], a[212:215], a[184:187], v[172:175]\n\t"
"v_mfma_f32_16x16x32_bf16 v[176:179], v[236:239], a[180:183], v[176:179]\n\t"
"ds_read_b128 a[180:183], v244 offset:26112\n\t"
"v_mfma_f32_16x16x32_bf16 v[176:179], v[248:251], a[184:187], v[176:179]\n\t"
"v_mfma_f32_16x16x32_bf16 v[176:179], a[212:215], a[188:191], v[176:179]\n\t"
"v_mfma_f32_16x16x32_bf16 v[180:183], v[236:239], a[184:187], v[180:183]\n\t"
"ds_read_b128 a[184:187], v244 offset:26624\n\t"
"v_mfma_f32_16x16x32_bf16 v[180:183], v[248:251], a[188:191], v[180:183]\n\t"
"v_mfma_f32_16x16x32_bf16 v[180:183], a[212:215], a[192:195], v[180:183]\n\t"
"v_mfma_f32_16x16x32_bf16 v[184:187], v[236:239], a[188:191], v[184:187]\n\t"
"ds_read_b128 a[188:191], v244 offset:27136\n\t"
"v_mfma_f32_16x16x32_bf16 v[184:187], v[248:251], a[192:195], v[184:187]\n\t"
"v_mfma_f32_16x16x32_bf16 v[184:187], a[212:215], a[196:199], v[184:187]\n\t"
"v_mfma_f32_16x16x32_bf16 v[188:191], v[236:239], a[192:195], v[188:191]\n\t"
"ds_read_b128 a[192:195], v244 offset:27648\n\t"
"v_mfma_f32_16x16x32_bf16 v[188:191], v[248:251], a[196:199], v[188:191]\n\t"
"v_mfma_f32_16x16x32_bf16 v[188:191], a[212:215], a[200:203], v[188:191]\n\t"
"v_mfma_f32_16x16x32_bf16 v[192:195], v[236:239], a[196:199], v[192:195]\n\t"
"s_waitcnt vmcnt(13) lgkmcnt(4)\n\t"
"ds_read_b128 a[196:199], v244 offset:28160\n\t"
"v_mfma_f32_16x16x32_bf16 v[192:195], v[248:251], a[200:203], v[192:195]\n\t"
"ds_read_b128 a[200:203], v244 offset:28672\n\t"
"v_mfma_f32_16x16x32_bf16 v[192:195], a[212:215], a[204:207], v[192:195]\n\t"
"ds_read_b128 a[204:207], v244 offset:29184\n\t"
"Lst22_%=:\n\t"
"v_mfma_f32_16x16x32_bf16 v[0:3], a[216:219], a[168:171], v[0:3]\n\t"
"s_waitcnt lgkmcnt(6)\n\t"
"v_xor_b32 v246, 0xc0, v246\n\t"
"v_mfma_f32_16x16x32_bf16 v[0:3], a[220:223], a[172:175], v[0:3]\n\t"
"v_mfma_f32_16x16x32_bf16 v[0:3], v[96:99], a[176:179], v[0:3]\n\t"
"v_mfma_f32_16x16x32_bf16 v[4:7], a[216:219], a[172:175], v[4:7]\n\t"
"s_waitcnt lgkmcnt(5)\n\t"
"v_mfma_f32_16x16x32_bf16 v[4:7], a[220:223], a[176:179], v[4:7]\n\t"
"v_mfma_f32_16x16x32_bf16 v[4:7], v[96:99], a[180:183], v[4:7]\n\t"
"v_mfma_f32_16x16x32_bf16 v[8:11], a[216:219], a[176:179], v[8:11]\n\t"
"buffer_load_dwordx4 v[240:243], v252, s[8:11], s20 offen offset:1024 sc1\n\t"
"v_mfma_f32_16x16x32_bf16 v[8:11], a[220:223], a[180:183], v[8:11]\n\t"
"s_waitcnt lgkmcnt(4)\n\t"
"v_mfma_f32_16x16x32_bf16 v[8:11], v[96:99], a[184:187], v[8:11]\n\t"
"v_mfma_f32_16x16x32_bf16 v[12:15], a[216:219], a[180:183], v[12:15]\n\t"
"s_waitcnt lgkmcnt(3)\n\t"
"v_mfma_f32_16x16x32_bf16 v[12:15], a[220:223], a[184:187], v[12:15]\n\t"
"v_mfma_f32_16x16x32_bf16 v[12:15], v[96:99], a[188:191], v[12:15]\n\t"
"v_mfma_f32_16x16x32_bf16 v[16:19], a[216:219], a[184:187], v[16:19]\n\t"
"s_waitcnt lgkmcnt(2)\n\t"
"v_mfma_f32_16x16x32_bf16 v[16:19], a[220:223], a[188:191], v[16:19]\n\t"
"v_mfma_f32_16x16x32_bf16 v[16:19], v[96:99], a[192:195], v[16:19]\n\t"
"ds_read_b64_tr_b16 v[232:233], v246 offset:1024\n\t"
"v_mfma_f32_16x16x32_bf16 v[20:23], a[216:219], a[188:191], v[20:23]\n\t"
"s_waitcnt lgkmcnt(2)\n\t"
"ds_read_b64_tr_b16 v[234:235], v246 offset:4096\n\t"
"v_mfma_f32_16x16x32_bf16 v[20:23], a[220:223], a[192:195], v[20:23]\n\t"
"v_mfma_f32_16x16x32_bf16 v[20:23], v[96:99], a[196:199], v[20:23]\n\t"
"v_mfma_f32_16x16x32_bf16 v[24:27], a[216:219], a[192:195], v[24:27]\n\t"
"s_waitcnt lgkmcnt(2)\n\t"
"v_mfma_f32_16x16x32_bf16 v[24:27], a[220:223], a[196:199], v[24:27]\n\t"
"v_mfma_f32_16x16x32_bf16 v[24:27], v[96:99], a[200:203], v[24:27]\n\t"
"v_mfma_f32_16x16x32_bf16 v[28:31], a[216:219], a[196:199], v[28:31]\n\t"
"s_waitcnt vmcnt(11)\n\t"
"ds_read_b64_tr_b16 v[224:225], v246 offset:7168\n\t"
"v_mfma_f32_16x16x32_bf16 v[28:31], a[220:223], a[200:203], v[28:31]\n\t"
"ds_read_b64_tr_b16 v[226:227], v246 offset:10240\n\t"
"v_mfma_f32_16x16x32_bf16 v[28:31], v[96:99], a[204:207], v[28:31]\n\t"
"v_mfma_f32_16x16x32_bf16 v[32:35], a[224:227], a[168:171], v[32:35]\n\t"
"v_mfma_f32_16x16x32_bf16 v[32:35], a[228:231], a[172:175], v[32:35]\n\t"
"v_mfma_f32_16x16x32_bf16 v[32:35], a[236:239], a[176:179], v[32:35]\n\t"
"v_mfma_f32_16x16x32_bf16 v[36:39], a[224:227], a[172:175], v[36:39]\n\t"
"s_waitcnt lgkmcnt(2)\n\t"
"v_mfma_f32_16x16x32_bf16 v[36:39], a[228:231], a[176:179], v[36:39]\n\t"
"v_mfma_f32_16x16x32_bf16 v[36:39], a[236:239], a[180:183], v[36:39]\n\t"
"v_mfma_f32_16x16x32_bf16 v[232:235], v[232:235], v[220:223], 0\n\t"
"v_mfma_f32_16x16x32_bf16 v[40:43], a[224:227], a[176:179], v[40:43]\n\t"
"ds_read_b64_tr_b16 v[228:229], v246 offset:13312\n\t"
"v_mfma_f32_16x16x32_bf16 v[40:43], a[228:231], a[180:183], v[40:43]\n\t"
"ds_read_b64_tr_b16 v[230:231], v246 offset:16384\n\t"
"v_mfma_f32_16x16x32_bf16 v[40:43], a[236:239], a[184:187], v[40:43]\n\t"
"v_mfma_f32_16x16x32_bf16 v[44:47], a[224:227], a[180:183], v[44:47]\n\t"
"s_waitcnt lgkmcnt(2)\n\t"
"v_mfma_f32_16x16x32_bf16 v[44:47], a[228:231], a[184:187], v[44:47]\n\t"
"v_cvt_pk_bf16_f32 v232, v232, v233\n\t"
"v_cvt_pk_bf16_f32 v233, v234, v235\n\t"
"v_mfma_f32_16x16x32_bf16 v[44:47], a[236:239], a[188:191], v[44:47]\n\t"
"v_mfma_f32_16x16x32_bf16 v[224:227], v[224:227], v[220:223], 0\n\t"
"buffer_load_dwordx4 a[208:211], v252, s[8:11], s20 offen offset:2048 sc1\n\t"
"v_mfma_f32_16x16x32_bf16 v[48:51], a[224:227], a[184:187], v[48:51]\n\t"
"s_mov_b32 s20, 380928\n\t"
"v_mfma_f32_16x16x32_bf16 v[48:51], a[228:231], a[188:191], v[48:51]\n\t"
"v_mfma_f32_16x16x32_bf16 v[48:51], a[236:239], a[192:195], v[48:51]\n\t"
"v_mfma_f32_16x16x32_bf16 v[52:55], a[224:227], a[188:191], v[52:55]\n\t"
"buffer_load_dwordx4 v[236:239], v252, s[8:11], s20 offen offset:0 sc1\n\t"
"v_mfma_f32_16x16x32_bf16 v[52:55], a[228:231], a[192:195], v[52:55]\n\t"
"v_cvt_pk_bf16_f32 v234, v224, v225\n\t"
"v_cvt_pk_bf16_f32 v235, v226, v227\n\t"
"v_mfma_f32_16x16x32_bf16 v[52:55], a[236:239], a[196:199], v[52:55]\n\t"
"ds_read_b64_tr_b16 v[224:225], v246 offset:20992\n\t"
"v_mfma_f32_16x16x32_bf16 v[56:59], a[224:227], a[192:195], v[56:59]\n\t"
"s_waitcnt lgkmcnt(1)\n\t"
"ds_read_b64_tr_b16 v[226:227], v246 offset:24064\n\t"
"v_mfma_f32_16x16x32_bf16 v[56:59], a[228:231], a[196:199], v[56:59]\n\t"
"buffer_load_dwordx4 v[96:99], v252, s[8:11], s20 offen offset:1024 sc1\n\t"
"v_mfma_f32_16x16x32_bf16 v[56:59], a[236:239], a[200:203], v[56:59]\n\t"
"v_mfma_f32_16x16x32_bf16 v[228:231], v[228:231], v[220:223], 0\n\t"
"v_mfma_f32_16x16x32_bf16 v[60:63], a[224:227], a[196:199], v[60:63]\n\t"
"s_waitcnt vmcnt(11)\n\t"
"v_mfma_f32_16x16x32_bf16 v[60:63], a[228:231], a[200:203], v[60:63]\n\t"
"buffer_load_dwordx4 v[248:251], v252, s[8:11], s20 offen offset:2048 sc1\n\t"
"v_mfma_f32_16x16x32_bf16 v[60:63], a[236:239], a[204:207], v[60:63]\n\t"
"s_mov_b32 s20, 393216\n\t"
"v_mfma_f32_16x16x32_bf16 v[64:67], a[240:243], a[168:171], v[64:67]\n\t"
"ds_read_b128 a[168:171], v244 offset:33792\n\t"
"v_mfma_f32_16x16x32_bf16 v[64:67], a[244:247], a[172:175], v[64:67]\n\t"
"v_cvt_pk_bf16_f32 v228, v228, v229\n\t"
"v_cvt_pk_bf16_f32 v229, v230, v231\n\t"
"v_mfma_f32_16x16x32_bf16 v[64:67], a[248:251], a[176:179], v[64:67]\n\t"
"v_mfma_f32_16x16x32_bf16 v[68:71], a[240:243], a[172:175], v[68:71]\n\t"
"buffer_load_dwordx4 a[212:215], v252, s[8:11], s20 offen offset:0 sc1\n\t"
"v_mfma_f32_16x16x32_bf16 v[68:71], a[244:247], a[176:179], v[68:71]\n\t"
"s_waitcnt lgkmcnt(1)\n\t"
"ds_read_b128 a[172:175], v244 offset:34304\n\t"
"v_mfma_f32_16x16x32_bf16 v[68:71], a[248:251], a[180:183], v[68:71]\n\t"
"v_mfma_f32_16x16x32_bf16 v[224:227], v[224:227], v[220:223], 0\n\t"
"buffer_load_dwordx4 a[216:219], v252, s[8:11], s20 offen offset:1024 sc1\n\t"
"v_mfma_f32_16x16x32_bf16 v[72:75], a[240:243], a[176:179], v[72:75]\n\t"
"ds_read_b128 a[176:179], v244 offset:34816\n\t"
"v_mfma_f32_16x16x32_bf16 v[72:75], a[244:247], a[180:183], v[72:75]\n\t"
"v_mfma_f32_16x16x32_bf16 v[72:75], a[248:251], a[184:187], v[72:75]\n\t"
"v_mfma_f32_16x16x32_bf16 v[76:79], a[240:243], a[180:183], v[76:79]\n\t"
"buffer_load_dwordx4 a[220:223], v252, s[8:11], s20 offen offset:2048 sc1\n\t"
"v_mfma_f32_16x16x32_bf16 v[76:79], a[244:247], a[184:187], v[76:79]\n\t"
"s_mov_b32 s20, 405504\n\t"
"ds_read_b128 a[180:183], v244 offset:35328\n\t"
"v_cvt_pk_bf16_f32 v230, v224, v225\n\t"
"v_mfma_f32_16x16x32_bf16 v[76:79], a[248:251], a[188:191], v[76:79]\n\t"
"v_cvt_pk_bf16_f32 v231, v226, v227\n\t"
"v_mfma_f32_16x16x32_bf16 v[80:83], a[240:243], a[184:187], v[80:83]\n\t"
"ds_read_b64_tr_b16 v[224:225], v246 offset:27136\n\t"
"v_mfma_f32_16x16x32_bf16 v[80:83], a[244:247], a[188:191], v[80:83]\n\t"
"ds_read_b64_tr_b16 v[226:227], v246 offset:30208\n\t"
"v_mfma_f32_16x16x32_bf16 v[80:83], a[248:251], a[192:195], v[80:83]\n\t"
"buffer_load_dwordx4 a[224:227], v252, s[8:11], s20 offen offset:0 sc1\n\t"
"v_mfma_f32_16x16x32_bf16 v[84:87], a[240:243], a[188:191], v[84:87]\n\t"
"ds_read_b128 a[184:187], v244 offset:35840\n\t"
"v_mfma_f32_16x16x32_bf16 v[84:87], a[244:247], a[192:195], v[84:87]\n\t"
"ds_read_b128 a[188:191], v244 offset:36352\n\t"
"v_mfma_f32_16x16x32_bf16 v[84:87], a[248:251], a[196:199], v[84:87]\n\t"
"buffer_load_dwordx4 a[228:231], v252, s[8:11], s20 offen offset:1024 sc1\n\t"
"v_mfma_f32_16x16x32_bf16 v[88:91], a[240:243], a[192:195], v[88:91]\n\t"
"ds_read_b128 a[192:195], v244 offset:36864\n\t"
"v_mfma_f32_16x16x32_bf16 v[88:91], a[244:247], a[196:199], v[88:91]\n\t"
"v_mfma_f32_16x16x32_bf16 v[88:91], a[248:251], a[200:203], v[88:91]\n\t"
"v_mfma_f32_16x16x32_bf16 v[92:95], a[240:243], a[196:199], v[92:95]\n\t"
"buffer_load_dwordx4 a[232:235], v252, s[8:11], s20 offen offset:2048 sc1\n\t"
"v_mfma_f32_16x16x32_bf16 v[92:95], a[244:247], a[200:203], v[92:95]\n\t"
"s_waitcnt vmcnt(9) lgkmcnt(3)\n\t"
"ds_read_b128 a[196:199], v244 offset:37376\n\t"
"s_mov_b32 s20, 417792\n\t"
"v_mfma_f32_16x16x32_bf16 v[92:95], a[248:251], a[204:207], v[92:95]\n\t"
"ds_read_b128 a[200:203], v244 offset:37888\n\t"
"v_mfma_f32_16x16x32_bf16 v[224:227], v[224:227], v[220:223], 0\n\t"
"ds_read_b128 a[204:207], v244 offset:38400\n\t"
"v_mfma_f32_16x16x32_bf16 v[100:103], a[252:255], a[168:171], v[100:103]\n\t"
"buffer_load_dwordx4 a[236:239], v252, s[8:11], s20 offen offset:0 sc1\n\t"
"v_mfma_f32_16x16x32_bf16 v[100:103], v[240:243], a[172:175], v[100:103]\n\t"
"v_mfma_f32_16x16x32_bf16 v[100:103], a[208:211], a[176:179], v[100:103]\n\t"
"v_mfma_f32_16x16x32_bf16 v[104:107], a[252:255], a[172:175], v[104:107]\n\t"
"buffer_load_dwordx4 a[240:243], v252, s[8:11], s20 offen offset:1024 sc1\n\t"
"v_mfma_f32_16x16x32_bf16 v[104:107], v[240:243], a[176:179], v[104:107]\n\t"
"s_waitcnt lgkmcnt(5)\n\t"
"v_cvt_pk_bf16_f32 v224, v224, v225\n\t"
"v_mfma_f32_16x16x32_bf16 v[104:107], a[208:211], a[180:183], v[104:107]\n\t"
"s_waitcnt lgkmcnt(0)\n\t"
"s_barrier\n\t"
"v_mfma_f32_16x16x32_bf16 v[108:111], a[252:255], a[176:179], v[108:111]\n\t"
"s_add_u32 m0, s24, 32128\n\t"
"s_add_u32 s28, s21, 983040\n\t"
"v_mfma_f32_16x16x32_bf16 v[108:111], v[240:243], a[180:183], v[108:111]\n\t"
"buffer_load_dwordx4 a[244:247], v252, s[8:11], s20 offen offset:2048 sc1\n\t"
"v_mfma_f32_16x16x32_bf16 v[108:111], a[208:211], a[184:187], v[108:111]\n\t"
"s_mov_b32 s20, 430080\n\t"
"ds_write2st64_b64 v245, v[232:233], v[234:235] offset0:48 offset1:49\n\t"
"v_cvt_pk_bf16_f32 v225, v226, v227\n\t"
"v_mfma_f32_16x16x32_bf16 v[112:115], a[252:255], a[180:183], v[112:115]\n\t"
"v_mfma_f32_16x16x32_bf16 v[112:115], v[240:243], a[184:187], v[112:115]\n\t"
"ds_read_b64_tr_b16 v[232:233], v246 offset:33280\n\t"
"v_mfma_f32_16x16x32_bf16 v[112:115], a[208:211], a[188:191], v[112:115]\n\t"
"ds_write2st64_b64 v245, v[228:229], v[230:231] offset0:50 offset1:51\n\t"
"v_mfma_f32_16x16x32_bf16 v[116:119], a[252:255], a[184:187], v[116:119]\n\t"
"buffer_load_dwordx4 v247, s[12:15], s28 offen offset:128 nt lds\n\t"
"v_mfma_f32_16x16x32_bf16 v[116:119], v[240:243], a[188:191], v[116:119]\n\t"
"s_add_u32 m0, s24, 39808\n\t"
"s_add_u32 s28, s21, 1179648\n\t"
"ds_read_b64_tr_b16 v[234:235], v246 offset:36352\n\t"
"v_mfma_f32_16x16x32_bf16 v[116:119], a[208:211], a[192:195], v[116:119]\n\t"
"ds_write_b64 v245, v[224:225] offset:26624\n\t"
"v_mfma_f32_16x16x32_bf16 v[120:123], a[252:255], a[188:191], v[120:123]\n\t"
"buffer_load_dwordx4 a[248:251], v252, s[8:11], s20 offen offset:0 sc1\n\t"
"v_mfma_f32_16x16x32_bf16 v[120:123], v[240:243], a[192:195], v[120:123]\n\t"
"buffer_load_dwordx4 v247, s[12:15], s28 offen offset:128 nt lds\n\t"
"v_mfma_f32_16x16x32_bf16 v[120:123], a[208:211], a[196:199], v[120:123]\n\t"
"s_waitcnt lgkmcnt(1)\n\t"
"ds_read_b64_tr_b16 v[228:229], v246 offset:40960\n\t"
"ds_read_b64_tr_b16 v[230:231], v246 offset:44032\n\t"
"s_add_u32 m0, s24, 45952\n\t"
"s_add_u32 s28, s21, 1376256\n\t"
"v_mfma_f32_16x16x32_bf16 v[232:235], v[232:235], v[220:223], 0\n\t"
"buffer_load_dwordx4 v247, s[12:15], s28 offen offset:128 nt lds\n\t"
"s_add_u32 m0, s24, 52096\n\t"
"s_add_u32 s28, s21, 1572864\n\t"
"v_mfma_f32_16x16x32_bf16 v[124:127], a[252:255], a[192:195], v[124:127]\n\t"
"buffer_load_dwordx4 v247, s[12:15], s28 offen offset:128 nt lds\n\t"
"v_mfma_f32_16x16x32_bf16 v[124:127], v[240:243], a[196:199], v[124:127]\n\t"
"s_add_u32 m0, s24, 92032\n\t"
"s_add_u32 s28, s21, 2752512\n\t"
"ds_read_b64_tr_b16 v[224:225], v246 offset:47104\n\t"
"v_mfma_f32_16x16x32_bf16 v[124:127], a[208:211], a[200:203], v[124:127]\n\t"
"s_waitcnt lgkmcnt(1)\n\t"
"ds_read_b64_tr_b16 v[226:227], v246 offset:50176\n\t"
"v_mfma_f32_16x16x32_bf16 v[228:231], v[228:231], v[220:223], 0\n\t"
"buffer_load_dwordx4 v247, s[12:15], s28 offen offset:128 nt lds\n\t"
"s_add_u32 m0, s24, 99712\n\t"
"s_add_u32 s28, s21, 2949120\n\t"
"v_mfma_f32_16x16x32_bf16 v[128:131], a[252:255], a[196:199], v[128:131]\n\t"
"buffer_load_dwordx4 v247, s[12:15], s28 offen offset:128 nt lds\n\t"
"v_mfma_f32_16x16x32_bf16 v[128:131], v[240:243], a[200:203], v[128:131]\n\t"
"s_waitcnt vmcnt(16)\n\t"
"s_add_u32 m0, s24, 105856\n\t"
"s_add_u32 s28, s21, 3145728\n\t"
"v_mfma_f32_16x16x32_bf16 v[128:131], a[208:211], a[204:207], v[128:131]\n\t"
"s_waitcnt lgkmcnt(0)\n\t"
"buffer_load_dwordx4 v247, s[12:15], s28 offen offset:128 nt lds\n\t"
"s_add_u32 m0, s24, 112000\n\t"
"s_add_u32 s28, s21, 3342336\n\t"
"v_mfma_f32_16x16x32_bf16 v[224:227], v[224:227], v[220:223], 0\n\t"
"buffer_load_dwordx4 v247, s[12:15], s28 offen offset:128 nt lds\n\t"
"v_cvt_pk_bf16_f32 v232, v232, v233\n\t"
"v_cvt_pk_bf16_f32 v233, v234, v235\n\t"
"v_mfma_f32_16x16x32_bf16 v[132:135], v[236:239], a[168:171], v[132:135]\n\t"
"v_cvt_pk_bf16_f32 v234, v228, v229\n\t"
"v_cvt_pk_bf16_f32 v235, v230, v231\n\t"
"v_mfma_f32_16x16x32_bf16 v[132:135], v[96:99], a[172:175], v[132:135]\n\t"
"ds_read_b64_tr_b16 v[228:229], v246 offset:53248\n\t"
"v_mfma_f32_16x16x32_bf16 v[132:135], v[248:251], a[176:179], v[132:135]\n\t"
"ds_write2st64_b64 v245, v[232:233], v[234:235] offset0:53 offset1:54\n\t"
"v_mfma_f32_16x16x32_bf16 v[136:139], v[236:239], a[172:175], v[136:139]\n\t"
"v_mfma_f32_16x16x32_bf16 v[136:139], v[96:99], a[176:179], v[136:139]\n\t"
"ds_read_b64_tr_b16 v[230:231], v246 offset:56320\n\t"
"v_mfma_f32_16x16x32_bf16 v[136:139], v[248:251], a[180:183], v[136:139]\n\t"
"v_cvt_pk_bf16_f32 v224, v224, v225\n\t"
"v_cvt_pk_bf16_f32 v225, v226, v227\n\t"
"v_mfma_f32_16x16x32_bf16 v[140:143], v[236:239], a[176:179], v[140:143]\n\t"
"v_mfma_f32_16x16x32_bf16 v[140:143], v[96:99], a[180:183], v[140:143]\n\t"
"v_mfma_f32_16x16x32_bf16 v[140:143], v[248:251], a[184:187], v[140:143]\n\t"
"v_mfma_f32_16x16x32_bf16 v[144:147], v[236:239], a[180:183], v[144:147]\n\t"
"v_mfma_f32_16x16x32_bf16 v[144:147], v[96:99], a[184:187], v[144:147]\n\t"
"v_mfma_f32_16x16x32_bf16 v[144:147], v[248:251], a[188:191], v[144:147]\n\t"
"v_mfma_f32_16x16x32_bf16 v[148:151], v[236:239], a[184:187], v[148:151]\n\t"
"s_waitcnt lgkmcnt(0)\n\t"
"v_mfma_f32_16x16x32_bf16 v[148:151], v[96:99], a[188:191], v[148:151]\n\t"
"v_mfma_f32_16x16x32_bf16 v[148:151], v[248:251], a[192:195], v[148:151]\n\t"
"v_mfma_f32_16x16x32_bf16 v[228:231], v[228:231], v[220:223], 0\n\t"
"v_mfma_f32_16x16x32_bf16 v[152:155], v[236:239], a[188:191], v[152:155]\n\t"
"v_mfma_f32_16x16x32_bf16 v[152:155], v[96:99], a[192:195], v[152:155]\n\t"
"v_mfma_f32_16x16x32_bf16 v[152:155], v[248:251], a[196:199], v[152:155]\n\t"
"v_mfma_f32_16x16x32_bf16 v[156:159], v[236:239], a[192:195], v[156:159]\n\t"
"v_mfma_f32_16x16x32_bf16 v[156:159], v[96:99], a[196:199], v[156:159]\n\t"
"v_cvt_pk_bf16_f32 v226, v228, v229\n\t"
"v_cvt_pk_bf16_f32 v227, v230, v231\n\t"
"v_mfma_f32_16x16x32_bf16 v[156:159], v[248:251], a[200:203], v[156:159]\n\t"
"ds_write2st64_b64 v245, v[224:225], v[226:227] offset0:55 offset1:56\n\t"
"v_mfma_f32_16x16x32_bf16 v[160:163], v[236:239], a[196:199], v[160:163]\n\t"
"s_waitcnt vmcnt(15)\n\t"
"v_mfma_f32_16x16x32_bf16 v[160:163], v[96:99], a[200:203], v[160:163]\n\t"
"v_mfma_f32_16x16x32_bf16 v[160:163], v[248:251], a[204:207], v[160:163]\n\t"
"v_mfma_f32_16x16x32_bf16 v[164:167], a[212:215], a[168:171], v[164:167]\n\t"
"v_mfma_f32_16x16x32_bf16 v[164:167], a[216:219], a[172:175], v[164:167]\n\t"
"v_mfma_f32_16x16x32_bf16 v[164:167], a[220:223], a[176:179], v[164:167]\n\t"
"s_waitcnt lgkmcnt(0)\n\t"
"s_barrier\n\t"
"v_mfma_f32_16x16x32_bf16 v[168:171], a[212:215], a[172:175], v[168:171]\n\t"
"ds_read_b128 a[168:171], v244 offset:24576\n\t"
"v_mfma_f32_16x16x32_bf16 v[168:171], a[216:219], a[176:179], v[168:171]\n\t"
"ds_read_b128 a[172:175], v244 offset:25088\n\t"
"v_mfma_f32_16x16x32_bf16 v[168:171], a[220:223], a[180:183], v[168:171]\n\t"
"v_mfma_f32_16x16x32_bf16 v[172:175], a[212:215], a[176:179], v[172:175]\n\t"
"ds_read_b128 a[176:179], v244 offset:25600\n\t"
"v_mfma_f32_16x16x32_bf16 v[172:175], a[216:219], a[180:183], v[172:175]\n\t"
"v_mfma_f32_16x16x32_bf16 v[172:175], a[220:223], a[184:187], v[172:175]\n\t"
"v_mfma_f32_16x16x32_bf16 v[176:179], a[212:215], a[180:183], v[176:179]\n\t"
"ds_read_b128 a[180:183], v244 offset:26112\n\t"
"v_mfma_f32_16x16x32_bf16 v[176:179], a[216:219], a[184:187], v[176:179]\n\t"
"v_mfma_f32_16x16x32_bf16 v[176:179], a[220:223], a[188:191], v[176:179]\n\t"
"v_mfma_f32_16x16x32_bf16 v[180:183], a[212:215], a[184:187], v[180:183]\n\t"
"ds_read_b128 a[184:187], v244 offset:26624\n\t"
"v_mfma_f32_16x16x32_bf16 v[180:183], a[216:219], a[188:191], v[180:183]\n\t"
"v_mfma_f32_16x16x32_bf16 v[180:183], a[220:223], a[192:195], v[180:183]\n\t"
"v_mfma_f32_16x16x32_bf16 v[184:187], a[212:215], a[188:191], v[184:187]\n\t"
"ds_read_b128 a[188:191], v244 offset:27136\n\t"
"v_mfma_f32_16x16x32_bf16 v[184:187], a[216:219], a[192:195], v[184:187]\n\t"
"v_mfma_f32_16x16x32_bf16 v[184:187], a[220:223], a[196:199], v[184:187]\n\t"
"v_mfma_f32_16x16x32_bf16 v[188:191], a[212:215], a[192:195], v[188:191]\n\t"
"ds_read_b128 a[192:195], v244 offset:27648\n\t"
"v_mfma_f32_16x16x32_bf16 v[188:191], a[216:219], a[196:199], v[188:191]\n\t"
"v_mfma_f32_16x16x32_bf16 v[188:191], a[220:223], a[200:203], v[188:191]\n\t"
"v_mfma_f32_16x16x32_bf16 v[192:195], a[212:215], a[196:199], v[192:195]\n\t"
"s_waitcnt vmcnt(12) lgkmcnt(4)\n\t"
"ds_read_b128 a[196:199], v244 offset:28160\n\t"
"v_mfma_f32_16x16x32_bf16 v[192:195], a[216:219], a[200:203], v[192:195]\n\t"
"ds_read_b128 a[200:203], v244 offset:28672\n\t"
"v_mfma_f32_16x16x32_bf16 v[192:195], a[220:223], a[204:207], v[192:195]\n\t"
"ds_read_b128 a[204:207], v244 offset:29184\n\t"
"Lst23_%=:\n\t"
"v_mfma_f32_16x16x32_bf16 v[196:199], a[224:227], a[168:171], v[196:199]\n\t"
"s_waitcnt lgkmcnt(6)\n\t"
"v_mfma_f32_16x16x32_bf16 v[196:199], a[228:231], a[172:175], v[196:199]\n\t"
"buffer_load_dwordx4 v[240:243], v252, s[8:11], s20 offen offset:1024 sc1\n\t"
"v_mfma_f32_16x16x32_bf16 v[196:199], a[232:235], a[176:179], v[196:199]\n\t"
"v_mfma_f32_16x16x32_bf16 v[204:207], a[224:227], a[172:175], v[204:207]\n\t"
"s_waitcnt lgkmcnt(5)\n\t"
"v_mfma_f32_16x16x32_bf16 v[204:207], a[228:231], a[176:179], v[204:207]\n\t"
"v_mfma_f32_16x16x32_bf16 v[204:207], a[232:235], a[180:183], v[204:207]\n\t"
"v_mfma_f32_16x16x32_bf16 v[212:215], a[224:227], a[176:179], v[212:215]\n\t"
"s_waitcnt lgkmcnt(4)\n\t"
"v_mfma_f32_16x16x32_bf16 v[212:215], a[228:231], a[180:183], v[212:215]\n\t"
"v_mfma_f32_16x16x32_bf16 v[212:215], a[232:235], a[184:187], v[212:215]\n\t"
"v_mfma_f32_16x16x32_bf16 a[0:3], a[224:227], a[180:183], a[0:3]\n\t"
"s_waitcnt lgkmcnt(3)\n\t"
"v_mfma_f32_16x16x32_bf16 a[0:3], a[228:231], a[184:187], a[0:3]\n\t"
"v_mfma_f32_16x16x32_bf16 a[0:3], a[232:235], a[188:191], a[0:3]\n\t"
"buffer_load_dwordx4 a[208:211], v252, s[8:11], s20 offen offset:2048 sc1\n\t"
"v_mfma_f32_16x16x32_bf16 a[8:11], a[224:227], a[184:187], a[8:11]\n\t"
"s_waitcnt lgkmcnt(2)\n\t"
"s_mov_b32 s20, 442368\n\t"
"v_mfma_f32_16x16x32_bf16 a[8:11], a[228:231], a[188:191], a[8:11]\n\t"
"v_mfma_f32_16x16x32_bf16 a[8:11], a[232:235], a[192:195], a[8:11]\n\t"
"ds_read_b64_tr_b16 v[232:233], v246 offset:0\n\t"
"v_mfma_f32_16x16x32_bf16 a[16:19], a[224:227], a[188:191], a[16:19]\n\t"
"buffer_load_dwordx4 v[236:239], v252, s[8:11], s20 offen offset:0 sc1\n\t"
"v_mfma_f32_16x16x32_bf16 a[16:19], a[228:231], a[192:195], a[16:19]\n\t"
"s_waitcnt lgkmcnt(2)\n\t"
"ds_read_b64_tr_b16 v[234:235], v246 offset:3072\n\t"
"v_mfma_f32_16x16x32_bf16 a[16:19], a[232:235], a[196:199], a[16:19]\n\t"
"v_mfma_f32_16x16x32_bf16 a[24:27], a[224:227], a[192:195], a[24:27]\n\t"
"s_waitcnt lgkmcnt(2)\n\t"
"v_mfma_f32_16x16x32_bf16 a[24:27], a[228:231], a[196:199], a[24:27]\n\t"
"buffer_load_dwordx4 v[96:99], v252, s[8:11], s20 offen offset:1024 sc1\n\t"
"v_mfma_f32_16x16x32_bf16 a[24:27], a[232:235], a[200:203], a[24:27]\n\t"
"v_mfma_f32_16x16x32_bf16 a[32:35], a[224:227], a[196:199], a[32:35]\n\t"
"s_waitcnt vmcnt(13)\n\t"
"ds_read_b64_tr_b16 v[228:229], v246 offset:6144\n\t"
"v_mfma_f32_16x16x32_bf16 a[32:35], a[228:231], a[200:203], a[32:35]\n\t"
"ds_read_b64_tr_b16 v[230:231], v246 offset:9216\n\t"
"v_mfma_f32_16x16x32_bf16 a[32:35], a[232:235], a[204:207], a[32:35]\n\t"
"buffer_load_dwordx4 v[248:251], v252, s[8:11], s20 offen offset:2048 sc1\n\t"
"v_mfma_f32_16x16x32_bf16 a[40:43], a[236:239], a[168:171], a[40:43]\n\t"
"s_mov_b32 s20, 454656\n\t"
"v_mfma_f32_16x16x32_bf16 a[40:43], a[240:243], a[172:175], a[40:43]\n\t"
"v_mfma_f32_16x16x32_bf16 a[40:43], a[244:247], a[176:179], a[40:43]\n\t"
"v_mfma_f32_16x16x32_bf16 a[48:51], a[236:239], a[172:175], a[48:51]\n\t"
"buffer_load_dwordx4 a[212:215], v252, s[8:11], s20 offen offset:0 sc1\n\t"
"v_mfma_f32_16x16x32_bf16 a[48:51], a[240:243], a[176:179], a[48:51]\n\t"
"s_waitcnt lgkmcnt(2)\n\t"
"v_mfma_f32_16x16x32_bf16 a[48:51], a[244:247], a[180:183], a[48:51]\n\t"
"v_mfma_f32_16x16x32_bf16 v[232:235], v[232:235], v[220:223], 0\n\t"
"v_mfma_f32_16x16x32_bf16 a[56:59], a[236:239], a[176:179], a[56:59]\n\t"
"buffer_load_dwordx4 a[216:219], v252, s[8:11], s20 offen offset:1024 sc1\n\t"
"v_mfma_f32_16x16x32_bf16 a[56:59], a[240:243], a[180:183], a[56:59]\n\t"
"ds_read_b64_tr_b16 v[224:225], v246 offset:12288\n\t"
"v_mfma_f32_16x16x32_bf16 a[56:59], a[244:247], a[184:187], a[56:59]\n\t"
"ds_read_b64_tr_b16 v[226:227], v246 offset:15360\n\t"
"v_mfma_f32_16x16x32_bf16 a[64:67], a[236:239], a[180:183], a[64:67]\n\t"
"s_waitcnt lgkmcnt(2)\n\t"
"v_mfma_f32_16x16x32_bf16 a[64:67], a[240:243], a[184:187], a[64:67]\n\t"
"buffer_load_dwordx4 a[220:223], v252, s[8:11], s20 offen offset:2048 sc1\n\t"
"v_mfma_f32_16x16x32_bf16 a[64:67], a[244:247], a[188:191], a[64:67]\n\t"
"s_mov_b32 s20, 466944\n\t"
"v_cvt_pk_bf16_f32 v232, v232, v233\n\t"
"v_cvt_pk_bf16_f32 v233, v234, v235\n\t"
"v_mfma_f32_16x16x32_bf16 v[228:231], v[228:231], v[220:223], 0\n\t"
"v_mfma_f32_16x16x32_bf16 a[72:75], a[236:239], a[184:187], a[72:75]\n\t"
"v_mfma_f32_16x16x32_bf16 a[72:75], a[240:243], a[188:191], a[72:75]\n\t"
"buffer_load_dwordx4 a[224:227], v252, s[8:11], s20 offen offset:0 sc1\n\t"
"v_mfma_f32_16x16x32_bf16 a[72:75], a[244:247], a[192:195], a[72:75]\n\t"
"v_mfma_f32_16x16x32_bf16 a[80:83], a[236:239], a[188:191], a[80:83]\n\t"
"v_mfma_f32_16x16x32_bf16 a[80:83], a[240:243], a[192:195], a[80:83]\n\t"
"v_cvt_pk_bf16_f32 v234, v228, v229\n\t"
"v_cvt_pk_bf16_f32 v235, v230, v231\n\t"
"v_mfma_f32_16x16x32_bf16 a[80:83], a[244:247], a[196:199], a[80:83]\n\t"
"ds_read_b64_tr_b16 v[228:229], v246 offset:19968\n\t"
"v_mfma_f32_16x16x32_bf16 a[88:91], a[236:239], a[192:195], a[88:91]\n\t"
"buffer_load_dwordx4 a[228:231], v252, s[8:11], s20 offen offset:1024 sc1\n\t"
"v_mfma_f32_16x16x32_bf16 a[88:91], a[240:243], a[196:199], a[88:91]\n\t"
"s_waitcnt lgkmcnt(1)\n\t"
"ds_read_b64_tr_b16 v[230:231], v246 offset:23040\n\t"
"v_mfma_f32_16x16x32_bf16 a[88:91], a[244:247], a[200:203], a[88:91]\n\t"
"v_mfma_f32_16x16x32_bf16 v[224:227], v[224:227], v[220:223], 0\n\t"
"buffer_load_dwordx4 a[232:235], v252, s[8:11], s20 offen offset:2048 sc1\n\t"
"v_mfma_f32_16x16x32_bf16 a[96:99], a[236:239], a[196:199], a[96:99]\n\t"
"s_waitcnt vmcnt(9)\n\t"
"s_mov_b32 s20, 700416\n\t"
"v_mfma_f32_16x16x32_bf16 a[96:99], a[240:243], a[200:203], a[96:99]\n\t"
"v_mfma_f32_16x16x32_bf16 a[96:99], a[244:247], a[204:207], a[96:99]\n\t"
"v_mfma_f32_16x16x32_bf16 a[104:107], a[248:251], a[168:171], a[104:107]\n\t"
"buffer_load_dwordx4 a[236:239], v252, s[8:11], s20 offen offset:0 sc1\n\t"
"v_mfma_f32_16x16x32_bf16 a[104:107], v[240:243], a[172:175], a[104:107]\n\t"
"ds_read_b128 a[168:171], v244 offset:33792\n\t"
"v_cvt_pk_bf16_f32 v224, v224, v225\n\t"
"v_mfma_f32_16x16x32_bf16 a[104:107], a[208:211], a[176:179], a[104:107]\n\t"
"v_cvt_pk_bf16_f32 v225, v226, v227\n\t"
"v_mfma_f32_16x16x32_bf16 a[112:115], a[248:251], a[172:175], a[112:115]\n\t"
"buffer_load_dwordx4 a[240:243], v252, s[8:11], s20 offen offset:1024 sc1\n\t"
"v_mfma_f32_16x16x32_bf16 a[112:115], v[240:243], a[176:179], a[112:115]\n\t"
"s_waitcnt lgkmcnt(1)\n\t"
"ds_read_b128 a[172:175], v244 offset:34304\n\t"
"v_mfma_f32_16x16x32_bf16 a[112:115], a[208:211], a[180:183], a[112:115]\n\t"
"v_mfma_f32_16x16x32_bf16 v[228:231], v[228:231], v[220:223], 0\n\t"
"buffer_load_dwordx4 a[244:247], v252, s[8:11], s20 offen offset:2048 sc1\n\t"
"v_mfma_f32_16x16x32_bf16 a[120:123], a[248:251], a[176:179], a[120:123]\n\t"
"s_mov_b32 s20, 712704\n\t"
"ds_read_b128 a[176:179], v244 offset:34816\n\t"
"v_mfma_f32_16x16x32_bf16 a[120:123], v[240:243], a[180:183], a[120:123]\n\t"
"v_mfma_f32_16x16x32_bf16 a[120:123], a[208:211], a[184:187], a[120:123]\n\t"
"buffer_load_dwordx4 a[252:255], v252, s[8:11], s20 offen offset:0 sc1\n\t"
"v_mfma_f32_16x16x32_bf16 a[128:131], a[248:251], a[180:183], a[128:131]\n\t"
"ds_read_b128 a[180:183], v244 offset:35328\n\t"
"v_mfma_f32_16x16x32_bf16 a[128:131], v[240:243], a[184:187], a[128:131]\n\t"
"v_cvt_pk_bf16_f32 v226, v228, v229\n\t"
"v_cvt_pk_bf16_f32 v227, v230, v231\n\t"
"v_mfma_f32_16x16x32_bf16 a[128:131], a[208:211], a[188:191], a[128:131]\n\t"
"ds_read_b64_tr_b16 v[228:229], v246 offset:26112\n\t"
"v_mfma_f32_16x16x32_bf16 a[136:139], a[248:251], a[184:187], a[136:139]\n\t"
"ds_read_b64_tr_b16 v[230:231], v246 offset:29184\n\t"
"v_mfma_f32_16x16x32_bf16 a[136:139], v[240:243], a[188:191], a[136:139]\n\t"
"ds_read_b128 a[184:187], v244 offset:35840\n\t"
"v_mfma_f32_16x16x32_bf16 a[136:139], a[208:211], a[192:195], a[136:139]\n\t"
"v_mfma_f32_16x16x32_bf16 a[144:147], a[248:251], a[188:191], a[144:147]\n\t"
"ds_read_b128 a[188:191], v244 offset:36352\n\t"
"v_mfma_f32_16x16x32_bf16 a[144:147], v[240:243], a[192:195], a[144:147]\n\t"
"v_mfma_f32_16x16x32_bf16 a[144:147], a[208:211], a[196:199], a[144:147]\n\t"
"v_mfma_f32_16x16x32_bf16 a[152:155], a[248:251], a[192:195], a[152:155]\n\t"
"ds_read_b128 a[192:195], v244 offset:36864\n\t"
"v_mfma_f32_16x16x32_bf16 a[152:155], v[240:243], a[196:199], a[152:155]\n\t"
"v_mfma_f32_16x16x32_bf16 a[152:155], a[208:211], a[200:203], a[152:155]\n\t"
"v_mfma_f32_16x16x32_bf16 a[160:163], a[248:251], a[196:199], a[160:163]\n\t"
"buffer_load_dwordx4 a[248:251], v252, s[8:11], s20 offen offset:1024 sc1\n\t"
"v_mfma_f32_16x16x32_bf16 a[160:163], v[240:243], a[200:203], a[160:163]\n\t"
"s_waitcnt vmcnt(11) lgkmcnt(3)\n\t"
"ds_read_b128 a[196:199], v244 offset:37376\n\t"
"v_mfma_f32_16x16x32_bf16 a[160:163], a[208:211], a[204:207], a[160:163]\n\t"
"ds_read_b128 a[200:203], v244 offset:37888\n\t"
"v_mfma_f32_16x16x32_bf16 v[228:231], v[228:231], v[220:223], 0\n\t"
"ds_read_b128 a[204:207], v244 offset:38400\n\t"
"v_mfma_f32_16x16x32_bf16 v[200:203], v[236:239], a[168:171], v[200:203]\n\t"
"v_mfma_f32_16x16x32_bf16 v[200:203], v[96:99], a[172:175], v[200:203]\n\t"
"v_mfma_f32_16x16x32_bf16 v[200:203], v[248:251], a[176:179], v[200:203]\n\t"
"v_mfma_f32_16x16x32_bf16 v[208:211], v[236:239], a[172:175], v[208:211]\n\t"
"s_waitcnt lgkmcnt(5)\n\t"
"v_mfma_f32_16x16x32_bf16 v[208:211], v[96:99], a[176:179], v[208:211]\n\t"
"v_cvt_pk_bf16_f32 v228, v228, v229\n\t"
"v_mfma_f32_16x16x32_bf16 v[208:211], v[248:251], a[180:183], v[208:211]\n\t"
"s_waitcnt lgkmcnt(0)\n\t"
"s_barrier\n\t"
"v_mfma_f32_16x16x32_bf16 v[216:219], v[236:239], a[176:179], v[216:219]\n\t"
"v_cvt_pk_bf16_f32 v229, v230, v231\n\t"
"v_mfma_f32_16x16x32_bf16 v[216:219], v[96:99], a[180:183], v[216:219]\n\t"
"v_mfma_f32_16x16x32_bf16 v[216:219], v[248:251], a[184:187], v[216:219]\n\t"
"ds_write2st64_b64 v245, v[232:233], v[234:235] offset0:48 offset1:49\n\t"
"v_mfma_f32_16x16x32_bf16 a[4:7], v[236:239], a[180:183], a[4:7]\n\t"
"v_mfma_f32_16x16x32_bf16 a[4:7], v[96:99], a[184:187], a[4:7]\n\t"
"ds_read_b64_tr_b16 v[232:233], v246 offset:32256\n\t"
"v_mfma_f32_16x16x32_bf16 a[4:7], v[248:251], a[188:191], a[4:7]\n\t"
"ds_write2st64_b64 v245, v[224:225], v[226:227] offset0:50 offset1:51\n\t"
"v_mfma_f32_16x16x32_bf16 a[12:15], v[236:239], a[184:187], a[12:15]\n\t"
"v_mfma_f32_16x16x32_bf16 a[12:15], v[96:99], a[188:191], a[12:15]\n\t"
"ds_read_b64_tr_b16 v[234:235], v246 offset:35328\n\t"
"v_mfma_f32_16x16x32_bf16 a[12:15], v[248:251], a[192:195], a[12:15]\n\t"
"ds_write_b64 v245, v[228:229] offset:26624\n\t"
"v_mfma_f32_16x16x32_bf16 a[20:23], v[236:239], a[188:191], a[20:23]\n\t"
"ds_read_b64_tr_b16 v[224:225], v246 offset:39936\n\t"
"v_mfma_f32_16x16x32_bf16 a[20:23], v[96:99], a[192:195], a[20:23]\n\t"
"ds_read_b64_tr_b16 v[226:227], v246 offset:43008\n\t"
"v_mfma_f32_16x16x32_bf16 a[20:23], v[248:251], a[196:199], a[20:23]\n\t"
"s_waitcnt lgkmcnt(3)\n\t"
"ds_read_b64_tr_b16 v[228:229], v246 offset:46080\n\t"
"v_mfma_f32_16x16x32_bf16 v[232:235], v[232:235], v[220:223], 0\n\t"
"ds_read_b64_tr_b16 v[230:231], v246 offset:49152\n\t"
"v_mfma_f32_16x16x32_bf16 a[28:31], v[236:239], a[192:195], a[28:31]\n\t"
"global_load_dwordx4 v[240:243], v252, s[48:49] offset:2048\n\t"
"v_mfma_f32_16x16x32_bf16 a[28:31], v[96:99], a[196:199], a[28:31]\n\t"
"global_load_dwordx4 a[208:211], v252, s[46:47] offset:1024\n\t"
"v_mfma_f32_16x16x32_bf16 a[28:31], v[248:251], a[200:203], a[28:31]\n\t"
"s_waitcnt lgkmcnt(2)\n\t"
"v_mfma_f32_16x16x32_bf16 v[224:227], v[224:227], v[220:223], 0\n\t"
"v_mfma_f32_16x16x32_bf16 a[36:39], v[236:239], a[196:199], a[36:39]\n\t"
"s_waitcnt lgkmcnt(0)\n\t"
"v_cvt_pk_bf16_f32 v232, v232, v233\n\t"
"v_cvt_pk_bf16_f32 v233, v234, v235\n\t"
"v_mfma_f32_16x16x32_bf16 a[36:39], v[96:99], a[200:203], a[36:39]\n\t"
"s_waitcnt vmcnt(10)\n\t"
"v_mfma_f32_16x16x32_bf16 a[36:39], v[248:251], a[204:207], a[36:39]\n\t"
"buffer_load_dwordx4 v[96:99], v252, s[8:11], s20 offen offset:2048 sc1\n\t"
"v_mfma_f32_16x16x32_bf16 v[228:231], v[228:231], v[220:223], 0\n\t"
"s_mov_b32 s20, 724992\n\t"
"v_mfma_f32_16x16x32_bf16 a[44:47], a[212:215], a[168:171], a[44:47]\n\t"
"buffer_load_dwordx4 v[248:251], v252, s[8:11], s20 offen offset:0 sc1\n\t"
"v_mfma_f32_16x16x32_bf16 a[44:47], a[216:219], a[172:175], a[44:47]\n\t"
"v_cvt_pk_bf16_f32 v234, v224, v225\n\t"
"v_cvt_pk_bf16_f32 v235, v226, v227\n\t"
"v_mfma_f32_16x16x32_bf16 a[44:47], a[220:223], a[176:179], a[44:47]\n\t"
"ds_write2st64_b64 v245, v[232:233], v[234:235] offset0:53 offset1:54\n\t"
"v_mfma_f32_16x16x32_bf16 a[52:55], a[212:215], a[172:175], a[52:55]\n\t"
"v_mfma_f32_16x16x32_bf16 a[52:55], a[216:219], a[176:179], a[52:55]\n\t"
"ds_read_b64_tr_b16 v[224:225], v246 offset:52224\n\t"
"v_mfma_f32_16x16x32_bf16 a[52:55], a[220:223], a[180:183], a[52:55]\n\t"
"ds_read_b64_tr_b16 v[226:227], v246 offset:55296\n\t"
"v_cvt_pk_bf16_f32 v228, v228, v229\n\t"
"v_mfma_f32_16x16x32_bf16 a[60:63], a[212:215], a[176:179], a[60:63]\n\t"
"v_cvt_pk_bf16_f32 v229, v230, v231\n\t"
"v_mfma_f32_16x16x32_bf16 a[60:63], a[216:219], a[180:183], a[60:63]\n\t"
"v_mfma_f32_16x16x32_bf16 a[60:63], a[220:223], a[184:187], a[60:63]\n\t"
"v_mfma_f32_16x16x32_bf16 a[68:71], a[212:215], a[180:183], a[68:71]\n\t"
"v_mfma_f32_16x16x32_bf16 a[68:71], a[216:219], a[184:187], a[68:71]\n\t"
"v_mfma_f32_16x16x32_bf16 a[68:71], a[220:223], a[188:191], a[68:71]\n\t"
"v_mfma_f32_16x16x32_bf16 a[76:79], a[212:215], a[184:187], a[76:79]\n\t"
"s_waitcnt lgkmcnt(0)\n\t"
"v_mfma_f32_16x16x32_bf16 a[76:79], a[216:219], a[188:191], a[76:79]\n\t"
"v_mfma_f32_16x16x32_bf16 a[76:79], a[220:223], a[192:195], a[76:79]\n\t"
"v_mfma_f32_16x16x32_bf16 v[224:227], v[224:227], v[220:223], 0\n\t"
"v_mfma_f32_16x16x32_bf16 a[84:87], a[212:215], a[188:191], a[84:87]\n\t"
"v_mfma_f32_16x16x32_bf16 a[84:87], a[216:219], a[192:195], a[84:87]\n\t"
"v_mfma_f32_16x16x32_bf16 a[84:87], a[220:223], a[196:199], a[84:87]\n\t"
"v_mfma_f32_16x16x32_bf16 a[92:95], a[212:215], a[192:195], a[92:95]\n\t"
"v_mfma_f32_16x16x32_bf16 a[92:95], a[216:219], a[196:199], a[92:95]\n\t"
"v_cvt_pk_bf16_f32 v230, v224, v225\n\t"
"v_cvt_pk_bf16_f32 v231, v226, v227\n\t"
"v_mfma_f32_16x16x32_bf16 a[92:95], a[220:223], a[200:203], a[92:95]\n\t"
"ds_write2st64_b64 v245, v[228:229], v[230:231] offset0:55 offset1:56\n\t"
"v_mfma_f32_16x16x32_bf16 a[100:103], a[212:215], a[196:199], a[100:103]\n\t"
"buffer_load_dwordx4 a[212:215], v252, s[8:11], s20 offen offset:1024 sc1\n\t"
"v_mfma_f32_16x16x32_bf16 a[100:103], a[216:219], a[200:203], a[100:103]\n\t"
"s_waitcnt vmcnt(10)\n\t"
"v_mfma_f32_16x16x32_bf16 a[100:103], a[220:223], a[204:207], a[100:103]\n\t"
"v_mfma_f32_16x16x32_bf16 a[108:111], a[224:227], a[168:171], a[108:111]\n\t"
"buffer_load_dwordx4 a[216:219], v252, s[8:11], s20 offen offset:2048 sc1\n\t"
"v_mfma_f32_16x16x32_bf16 a[108:111], a[228:231], a[172:175], a[108:111]\n\t"
"s_mov_b32 s20, 737280\n\t"
"v_mfma_f32_16x16x32_bf16 a[108:111], a[232:235], a[176:179], a[108:111]\n\t"
"s_waitcnt lgkmcnt(0)\n\t"
"s_barrier\n\t"
"v_mfma_f32_16x16x32_bf16 a[116:119], a[224:227], a[172:175], a[116:119]\n\t"
"s_waitcnt vmcnt(5)\n\t"
"ds_read_b64_tr_b16 v[236:237], v240 offset:0\n\t"
"v_mfma_f32_16x16x32_bf16 a[116:119], a[228:231], a[176:179], a[116:119]\n\t"
"ds_read_b64_tr_b16 v[238:239], v240 offset:1536\n\t"
"v_mfma_f32_16x16x32_bf16 a[116:119], a[232:235], a[180:183], a[116:119]\n\t"
"ds_read_b64_tr_b16 v[232:233], v241 offset:0\n\t"
"v_mfma_f32_16x16x32_bf16 a[124:127], a[224:227], a[176:179], a[124:127]\n\t"
"ds_read_b64_tr_b16 v[234:235], v241 offset:1536\n\t"
"v_mfma_f32_16x16x32_bf16 a[124:127], a[228:231], a[180:183], a[124:127]\n\t"
"buffer_load_dwordx4 a[220:223], v252, s[8:11], s20 offen offset:0 sc1\n\t"
"v_mfma_f32_16x16x32_bf16 a[124:127], a[232:235], a[184:187], a[124:127]\n\t"
"ds_read_b64_tr_b16 v[224:225], v240 offset:19968\n\t"
"v_mfma_f32_16x16x32_bf16 a[132:135], a[224:227], a[180:183], a[132:135]\n\t"
"s_waitcnt vmcnt(5) lgkmcnt(3)\n\t"
"ds_read_b64_tr_b16 v[226:227], v240 offset:21504\n\t"
"v_mfma_f32_16x16x32_bf16 a[132:135], a[228:231], a[184:187], a[132:135]\n\t"
"ds_read_b64_tr_b16 v[228:229], v241 offset:19968\n\t"
"v_mfma_f32_16x16x32_bf16 a[132:135], a[232:235], a[188:191], a[132:135]\n\t"
"s_waitcnt lgkmcnt(3)\n\t"
"ds_read_b64_tr_b16 v[230:231], v241 offset:21504\n\t"
"v_mfma_f32_16x16x32_bf16 v[236:239], v[236:239], a[208:211], 0\n\t"
"ds_read_b128 a[168:171], v244 offset:24576\n\t"
"v_mfma_f32_16x16x32_bf16 v[232:235], v[232:235], a[208:211], 0\n\t"
"ds_read_b128 a[172:175], v244 offset:25088\n\t"
"v_mfma_f32_16x16x32_bf16 a[140:143], a[224:227], a[184:187], a[140:143]\n\t"
"ds_read_b128 a[176:179], v244 offset:25600\n\t"
"v_mfma_f32_16x16x32_bf16 a[140:143], a[228:231], a[188:191], a[140:143]\n\t"
"ds_read_b128 a[180:183], v244 offset:26112\n\t"
"v_mfma_f32_16x16x32_bf16 a[140:143], a[232:235], a[192:195], a[140:143]\n\t"
"ds_read_b128 a[184:187], v244 offset:26624\n\t"
"v_mfma_f32_16x16x32_bf16 a[148:151], a[224:227], a[188:191], a[148:151]\n\t"
"s_waitcnt lgkmcnt(7)\n\t"
"ds_read_b128 a[188:191], v244 offset:27136\n\t"
"v_cvt_pk_bf16_f32 v236, v236, v237\n\t"
"v_mfma_f32_16x16x32_bf16 a[148:151], a[228:231], a[192:195], a[148:151]\n\t"
"s_waitcnt lgkmcnt(6)\n\t"
"v_cvt_pk_bf16_f32 v237, v238, v239\n\t"
"v_cvt_pk_bf16_f32 v238, v232, v233\n\t"
"v_mfma_f32_16x16x32_bf16 a[148:151], a[232:235], a[196:199], a[148:151]\n\t"
"v_cvt_pk_bf16_f32 v239, v234, v235\n\t"
"v_mfma_f32_16x16x32_bf16 v[224:227], v[224:227], a[208:211], 0\n\t"
"ds_write_b128 v242, v[236:239] offset:0\n\t"
"v_mfma_f32_16x16x32_bf16 v[228:231], v[228:231], a[208:211], 0\n\t"
"v_mfma_f32_16x16x32_bf16 a[156:159], a[224:227], a[192:195], a[156:159]\n\t"
"ds_read_b128 a[192:195], v244 offset:27648\n\t"
"v_mfma_f32_16x16x32_bf16 a[156:159], a[228:231], a[196:199], a[156:159]\n\t"
"ds_read_b64_tr_b16 v[232:233], v240 offset:39936\n\t"
"v_mfma_f32_16x16x32_bf16 a[156:159], a[232:235], a[200:203], a[156:159]\n\t"
"ds_read_b64_tr_b16 v[234:235], v240 offset:41472\n\t"
"v_mfma_f32_16x16x32_bf16 a[164:167], a[224:227], a[196:199], a[164:167]\n\t"
"buffer_load_dwordx4 a[224:227], v252, s[8:11], s20 offen offset:1024 sc1\n\t"
"v_mfma_f32_16x16x32_bf16 a[164:167], a[228:231], a[200:203], a[164:167]\n\t"
"s_waitcnt lgkmcnt(7)\n\t"
"ds_read_b64_tr_b16 v[236:237], v241 offset:39936\n\t"
"v_cvt_pk_bf16_f32 v224, v224, v225\n\t"
"v_mfma_f32_16x16x32_bf16 a[164:167], a[232:235], a[204:207], a[164:167]\n\t"
"ds_read_b64_tr_b16 v[238:239], v241 offset:41472\n\t"
"v_cvt_pk_bf16_f32 v225, v226, v227\n\t"
"Lst24_%=:\n\t"
"v_mfma_f32_16x16x32_bf16 v[196:199], a[236:239], a[168:171], v[196:199]\n\t"
"s_waitcnt lgkmcnt(8)\n\t"
"ds_read_b128 a[196:199], v244 offset:28160\n\t"
"v_cvt_pk_bf16_f32 v226, v228, v229\n\t"
"v_mfma_f32_16x16x32_bf16 v[196:199], a[240:243], a[172:175], v[196:199]\n\t"
"ds_read_b128 a[200:203], v244 offset:28672\n\t"
"v_cvt_pk_bf16_f32 v227, v230, v231\n\t"
"v_mfma_f32_16x16x32_bf16 v[196:199], a[244:247], a[176:179], v[196:199]\n\t"
"ds_write_b128 v242, v[224:227] offset:1536\n\t"
"v_mfma_f32_16x16x32_bf16 v[204:207], a[236:239], a[172:175], v[204:207]\n\t"
"buffer_load_dwordx4 a[228:231], v252, s[8:11], s20 offen offset:2048 sc1\n\t"
"v_mfma_f32_16x16x32_bf16 v[204:207], a[240:243], a[176:179], v[204:207]\n\t"
"s_waitcnt lgkmcnt(10)\n\t"
"ds_read_b128 a[204:207], v244 offset:29184\n\t"
"v_mfma_f32_16x16x32_bf16 v[204:207], a[244:247], a[180:183], v[204:207]\n\t"
"s_mov_b32 s20, 749568\n\t"
"v_add_u32 v240, 59904, v240\n\t"
"v_add_u32 v241, 59904, v241\n\t"
"v_mfma_f32_16x16x32_bf16 v[212:215], a[236:239], a[176:179], v[212:215]\n\t"
"s_waitcnt lgkmcnt(6)\n\t"
"ds_read_b64_tr_b16 v[228:229], v240 offset:0\n\t"
"v_mfma_f32_16x16x32_bf16 v[212:215], a[240:243], a[180:183], v[212:215]\n\t"
"buffer_load_dwordx4 a[232:235], v252, s[8:11], s20 offen offset:0 sc1\n\t"
"v_mfma_f32_16x16x32_bf16 v[212:215], a[244:247], a[184:187], v[212:215]\n\t"
"s_waitcnt lgkmcnt(5)\n\t"
"ds_read_b64_tr_b16 v[230:231], v240 offset:1536\n\t"
"v_mfma_f32_16x16x32_bf16 v[232:235], v[232:235], a[208:211], 0\n\t"
"ds_read_b64_tr_b16 v[224:225], v241 offset:0\n\t"
"v_mfma_f32_16x16x32_bf16 v[236:239], v[236:239], a[208:211], 0\n\t"
"ds_read_b64_tr_b16 v[226:227], v241 offset:1536\n\t"
"v_mfma_f32_16x16x32_bf16 a[0:3], a[236:239], a[180:183], a[0:3]\n\t"
"v_mfma_f32_16x16x32_bf16 a[0:3], a[240:243], a[184:187], a[0:3]\n\t"
"v_mfma_f32_16x16x32_bf16 a[0:3], a[244:247], a[188:191], a[0:3]\n\t"
"v_mfma_f32_16x16x32_bf16 a[8:11], a[236:239], a[184:187], a[8:11]\n\t"
"s_waitcnt lgkmcnt(2)\n\t"
"v_cvt_pk_bf16_f32 v232, v232, v233\n\t"
"v_cvt_pk_bf16_f32 v233, v234, v235\n\t"
"v_mfma_f32_16x16x32_bf16 a[8:11], a[240:243], a[188:191], a[8:11]\n\t"
"s_waitcnt lgkmcnt(0)\n\t"
"v_cvt_pk_bf16_f32 v234, v236, v237\n\t"
"v_cvt_pk_bf16_f32 v235, v238, v239\n\t"
"v_mfma_f32_16x16x32_bf16 a[8:11], a[244:247], a[192:195], a[8:11]\n\t"
"ds_write_b128 v242, v[232:235] offset:3072\n\t"
"v_mfma_f32_16x16x32_bf16 v[228:231], v[228:231], a[208:211], 0\n\t"
"v_mfma_f32_16x16x32_bf16 v[224:227], v[224:227], a[208:211], 0\n\t"
"ds_read_b64_tr_b16 v[236:237], v240 offset:19968\n\t"
"v_mfma_f32_16x16x32_bf16 a[16:19], a[236:239], a[188:191], a[16:19]\n\t"
"ds_read_b64_tr_b16 v[238:239], v240 offset:21504\n\t"
"v_mfma_f32_16x16x32_bf16 a[16:19], a[240:243], a[192:195], a[16:19]\n\t"
"ds_read_b64_tr_b16 v[232:233], v241 offset:19968\n\t"
"v_mfma_f32_16x16x32_bf16 a[16:19], a[244:247], a[196:199], a[16:19]\n\t"
"ds_read_b64_tr_b16 v[234:235], v241 offset:21504\n\t"
"v_mfma_f32_16x16x32_bf16 a[24:27], a[236:239], a[192:195], a[24:27]\n\t"
"v_cvt_pk_bf16_f32 v228, v228, v229\n\t"
"v_cvt_pk_bf16_f32 v229, v230, v231\n\t"
"v_mfma_f32_16x16x32_bf16 a[24:27], a[240:243], a[196:199], a[24:27]\n\t"
"v_cvt_pk_bf16_f32 v230, v224, v225\n\t"
"v_cvt_pk_bf16_f32 v231, v226, v227\n\t"
"v_mfma_f32_16x16x32_bf16 a[24:27], a[244:247], a[200:203], a[24:27]\n\t"
"ds_write_b128 v242, v[228:231] offset:4608\n\t"
"v_mfma_f32_16x16x32_bf16 a[32:35], a[236:239], a[196:199], a[32:35]\n\t"
"buffer_load_dwordx4 a[236:239], v252, s[8:11], s20 offen offset:1024 sc1\n\t"
"v_mfma_f32_16x16x32_bf16 a[32:35], a[240:243], a[200:203], a[32:35]\n\t"
"s_waitcnt vmcnt(8)\n\t"
"ds_read_b64_tr_b16 v[224:225], v240 offset:39936\n\t"
"v_mfma_f32_16x16x32_bf16 a[32:35], a[244:247], a[204:207], a[32:35]\n\t"
"ds_read_b64_tr_b16 v[226:227], v240 offset:41472\n\t"
"v_mfma_f32_16x16x32_bf16 a[40:43], a[252:255], a[168:171], a[40:43]\n\t"
"buffer_load_dwordx4 a[240:243], v252, s[8:11], s20 offen offset:2048 sc1\n\t"
"v_mfma_f32_16x16x32_bf16 a[40:43], a[248:251], a[172:175], a[40:43]\n\t"
"s_mov_b32 s20, 761856\n\t"
"ds_read_b64_tr_b16 v[228:229], v241 offset:39936\n\t"
"v_mfma_f32_16x16x32_bf16 a[40:43], v[96:99], a[176:179], a[40:43]\n\t"
"ds_read_b64_tr_b16 v[230:231], v241 offset:41472\n\t"
"v_mfma_f32_16x16x32_bf16 a[48:51], a[252:255], a[172:175], a[48:51]\n\t"
"buffer_load_dwordx4 a[244:247], v252, s[8:11], s20 offen offset:0 sc1\n\t"
"v_mfma_f32_16x16x32_bf16 a[48:51], a[248:251], a[176:179], a[48:51]\n\t"
"s_waitcnt lgkmcnt(5)\n\t"
"v_mfma_f32_16x16x32_bf16 a[48:51], v[96:99], a[180:183], a[48:51]\n\t"
"v_mfma_f32_16x16x32_bf16 v[236:239], v[236:239], a[208:211], 0\n\t"
"v_mfma_f32_16x16x32_bf16 v[232:235], v[232:235], a[208:211], 0\n\t"
"v_xor_b32 v246, 0xc0, v246\n\t"
"v_mfma_f32_16x16x32_bf16 a[56:59], a[252:255], a[176:179], a[56:59]\n\t"
"v_mfma_f32_16x16x32_bf16 a[56:59], a[248:251], a[180:183], a[56:59]\n\t"
"v_mfma_f32_16x16x32_bf16 a[56:59], v[96:99], a[184:187], a[56:59]\n\t"
"v_mfma_f32_16x16x32_bf16 a[64:67], a[252:255], a[180:183], a[64:67]\n\t"
"s_waitcnt lgkmcnt(2)\n\t"
"v_cvt_pk_bf16_f32 v236, v236, v237\n\t"
"v_cvt_pk_bf16_f32 v237, v238, v239\n\t"
"v_mfma_f32_16x16x32_bf16 a[64:67], a[248:251], a[184:187], a[64:67]\n\t"
"s_waitcnt lgkmcnt(0)\n\t"
"v_cvt_pk_bf16_f32 v238, v232, v233\n\t"
"v_cvt_pk_bf16_f32 v239, v234, v235\n\t"
"v_mfma_f32_16x16x32_bf16 a[64:67], v[96:99], a[188:191], a[64:67]\n\t"
"ds_write_b128 v242, v[236:239] offset:6144\n\t"
"v_mfma_f32_16x16x32_bf16 v[224:227], v[224:227], a[208:211], 0\n\t"
"v_mfma_f32_16x16x32_bf16 v[228:231], v[228:231], a[208:211], 0\n\t"
"v_mfma_f32_16x16x32_bf16 a[72:75], a[252:255], a[184:187], a[72:75]\n\t"
"buffer_load_dwordx4 a[208:211], v252, s[8:11], s20 offen offset:1024 sc1\n\t"
"v_mfma_f32_16x16x32_bf16 a[72:75], a[248:251], a[188:191], a[72:75]\n\t"
"v_mfma_f32_16x16x32_bf16 a[72:75], v[96:99], a[192:195], a[72:75]\n\t"
"v_mfma_f32_16x16x32_bf16 a[80:83], a[252:255], a[188:191], a[80:83]\n\t"
"v_cvt_pk_bf16_f32 v224, v224, v225\n\t"
"v_cvt_pk_bf16_f32 v225, v226, v227\n\t"
"v_mfma_f32_16x16x32_bf16 a[80:83], a[248:251], a[192:195], a[80:83]\n\t"
"v_cvt_pk_bf16_f32 v226, v228, v229\n\t"
"v_cvt_pk_bf16_f32 v227, v230, v231\n\t"
"v_mfma_f32_16x16x32_bf16 a[80:83], v[96:99], a[196:199], a[80:83]\n\t"
"ds_write_b128 v242, v[224:227] offset:7680\n\t"
"v_mfma_f32_16x16x32_bf16 a[88:91], a[252:255], a[192:195], a[88:91]\n\t"
"v_mfma_f32_16x16x32_bf16 a[88:91], a[248:251], a[196:199], a[88:91]\n\t"
"ds_read_b64_tr_b16 v[232:233], v246 offset:0\n\t"
"v_mfma_f32_16x16x32_bf16 a[88:91], v[96:99], a[200:203], a[88:91]\n\t"
"ds_read_b64_tr_b16 v[234:235], v246 offset:3072\n\t"
"v_mfma_f32_16x16x32_bf16 a[96:99], a[252:255], a[196:199], a[96:99]\n\t"
"buffer_load_dwordx4 a[252:255], v252, s[8:11], s20 offen offset:2048 sc1\n\t"
"v_mfma_f32_16x16x32_bf16 a[96:99], a[248:251], a[200:203], a[96:99]\n\t"
"s_waitcnt vmcnt(9)\n\t"
"ds_read_b64_tr_b16 v[236:237], v246 offset:6144\n\t"
"s_mov_b32 s20, 626688\n\t"
"v_mfma_f32_16x16x32_bf16 a[96:99], v[96:99], a[204:207], a[96:99]\n\t"
"ds_read_b64_tr_b16 v[238:239], v246 offset:9216\n\t"
"v_mfma_f32_16x16x32_bf16 a[104:107], v[248:251], a[168:171], a[104:107]\n\t"
"buffer_load_dwordx4 v[240:243], v252, s[8:11], s20 offen offset:0 sc1\n\t"
"v_mfma_f32_16x16x32_bf16 a[104:107], a[212:215], a[172:175], a[104:107]\n\t"
"ds_read_b64_tr_b16 v[228:229], v246 offset:12288\n\t"
"v_mfma_f32_16x16x32_bf16 a[104:107], a[216:219], a[176:179], a[104:107]\n\t"
"ds_read_b64_tr_b16 v[230:231], v246 offset:15360\n\t"
"v_mfma_f32_16x16x32_bf16 a[112:115], v[248:251], a[172:175], a[112:115]\n\t"
"buffer_load_dwordx4 v[96:99], v252, s[8:11], s20 offen offset:1024 sc1\n\t"
"v_mfma_f32_16x16x32_bf16 a[112:115], a[212:215], a[176:179], a[112:115]\n\t"
"ds_read_b128 a[168:171], v244 offset:33792\n\t"
"v_mfma_f32_16x16x32_bf16 a[112:115], a[216:219], a[180:183], a[112:115]\n\t"
"ds_read_b64_tr_b16 v[224:225], v246 offset:19968\n\t"
"v_mfma_f32_16x16x32_bf16 a[120:123], v[248:251], a[176:179], a[120:123]\n\t"
"buffer_load_dwordx4 a[248:251], v252, s[8:11], s20 offen offset:2048 sc1\n\t"
"v_mfma_f32_16x16x32_bf16 a[120:123], a[212:215], a[180:183], a[120:123]\n\t"
"s_waitcnt lgkmcnt(6)\n\t"
"ds_read_b64_tr_b16 v[226:227], v246 offset:23040\n\t"
"v_mfma_f32_16x16x32_bf16 a[120:123], a[216:219], a[184:187], a[120:123]\n\t"
"ds_read_b128 a[172:175], v244 offset:34304\n\t"
"v_mfma_f32_16x16x32_bf16 v[232:235], v[232:235], v[220:223], 0\n\t"
"ds_read_b128 a[176:179], v244 offset:34816\n\t"
"v_mfma_f32_16x16x32_bf16 a[128:131], v[248:251], a[180:183], a[128:131]\n\t"
"s_waitcnt lgkmcnt(7)\n\t"
"ds_read_b128 a[180:183], v244 offset:35328\n\t"
"v_mfma_f32_16x16x32_bf16 a[128:131], a[212:215], a[184:187], a[128:131]\n\t"
"v_mfma_f32_16x16x32_bf16 a[128:131], a[216:219], a[188:191], a[128:131]\n\t"
"v_mfma_f32_16x16x32_bf16 v[236:239], v[236:239], v[220:223], 0\n\t"
"v_mfma_f32_16x16x32_bf16 a[136:139], v[248:251], a[184:187], a[136:139]\n\t"
"s_waitcnt lgkmcnt(6)\n\t"
"ds_read_b128 a[184:187], v244 offset:35840\n\t"
"v_cvt_pk_bf16_f32 v232, v232, v233\n\t"
"v_mfma_f32_16x16x32_bf16 a[136:139], a[212:215], a[188:191], a[136:139]\n\t"
"v_cvt_pk_bf16_f32 v233, v234, v235\n\t"
"v_mfma_f32_16x16x32_bf16 a[136:139], a[216:219], a[192:195], a[136:139]\n\t"
"s_mov_b32 s20, 638976\n\t"
"v_mfma_f32_16x16x32_bf16 v[228:231], v[228:231], v[220:223], 0\n\t"
"v_mfma_f32_16x16x32_bf16 a[144:147], v[248:251], a[188:191], a[144:147]\n\t"
"s_waitcnt lgkmcnt(4)\n\t"
"ds_read_b128 a[188:191], v244 offset:36352\n\t"
"v_cvt_pk_bf16_f32 v234, v236, v237\n\t"
"v_mfma_f32_16x16x32_bf16 a[144:147], a[212:215], a[192:195], a[144:147]\n\t"
"v_cvt_pk_bf16_f32 v235, v238, v239\n\t"
"v_mfma_f32_16x16x32_bf16 a[144:147], a[216:219], a[196:199], a[144:147]\n\t"
"v_mfma_f32_16x16x32_bf16 v[224:227], v[224:227], v[220:223], 0\n\t"
"v_mfma_f32_16x16x32_bf16 a[152:155], v[248:251], a[192:195], a[152:155]\n\t"
"buffer_load_dwordx4 v[236:239], v252, s[8:11], s20 offen offset:0 sc1\n\t"
"v_mfma_f32_16x16x32_bf16 a[152:155], a[212:215], a[196:199], a[152:155]\n\t"
"ds_read_b128 a[192:195], v244 offset:36864\n\t"
"v_cvt_pk_bf16_f32 v228, v228, v229\n\t"
"v_mfma_f32_16x16x32_bf16 a[152:155], a[216:219], a[200:203], a[152:155]\n\t"
"v_cvt_pk_bf16_f32 v229, v230, v231\n\t"
"v_mfma_f32_16x16x32_bf16 a[160:163], v[248:251], a[196:199], a[160:163]\n\t"
"buffer_load_dwordx4 v[248:251], v252, s[8:11], s20 offen offset:1024 sc1\n\t"
"v_mfma_f32_16x16x32_bf16 a[160:163], a[212:215], a[200:203], a[160:163]\n\t"
"s_waitcnt vmcnt(11) lgkmcnt(4)\n\t"
"ds_read_b128 a[196:199], v244 offset:37376\n\t"
"v_cvt_pk_bf16_f32 v230, v224, v225\n\t"
"v_mfma_f32_16x16x32_bf16 a[160:163], a[216:219], a[204:207], a[160:163]\n\t"
"ds_read_b128 a[200:203], v244 offset:37888\n\t"
"v_cvt_pk_bf16_f32 v231, v226, v227\n\t"
"v_mfma_f32_16x16x32_bf16 v[200:203], a[220:223], a[168:171], v[200:203]\n\t"
"ds_read_b64_tr_b16 v[224:225], v246 offset:26112\n\t"
"v_mfma_f32_16x16x32_bf16 v[200:203], a[224:227], a[172:175], v[200:203]\n\t"
"s_waitcnt lgkmcnt(6)\n\t"
"ds_read_b64_tr_b16 v[226:227], v246 offset:29184\n\t"
"v_mfma_f32_16x16x32_bf16 v[200:203], a[228:231], a[176:179], v[200:203]\n\t"
"s_waitcnt lgkmcnt(0)\n\t"
"ds_read_b128 a[204:207], v244 offset:38400\n\t"
"v_mfma_f32_16x16x32_bf16 v[224:227], v[224:227], v[220:223], 0\n\t"
"buffer_load_dwordx4 a[212:215], v252, s[8:11], s20 offen offset:2048 sc1\n\t"
"s_mov_b32 s20, 651264\n\t"
"v_mfma_f32_16x16x32_bf16 v[208:211], a[220:223], a[172:175], v[208:211]\n\t"
"buffer_load_dwordx4 a[216:219], v252, s[8:11], s20 offen offset:0 sc1\n\t"
"v_mfma_f32_16x16x32_bf16 v[208:211], a[224:227], a[176:179], v[208:211]\n\t"
"s_add_u32 m0, s24, 768\n\t"
"s_add_u32 s28, s21, 0\n\t"
"v_mfma_f32_16x16x32_bf16 v[208:211], a[228:231], a[180:183], v[208:211]\n\t"
"s_waitcnt lgkmcnt(0)\n\t"
"s_barrier\n\t"
"v_mfma_f32_16x16x32_bf16 v[216:219], a[220:223], a[176:179], v[216:219]\n\t"
"v_mfma_f32_16x16x32_bf16 v[216:219], a[224:227], a[180:183], v[216:219]\n\t"
"buffer_load_dwordx4 v247, s[12:15], s28 offen offset:256 nt lds\n\t"
"v_mfma_f32_16x16x32_bf16 v[216:219], a[228:231], a[184:187], v[216:219]\n\t"
"s_add_u32 m0, s24, 6912\n\t"
"s_add_u32 s28, s21, 196608\n\t"
"ds_write2st64_b64 v245, v[232:233], v[234:235] offset0:48 offset1:49\n\t"
"v_mfma_f32_16x16x32_bf16 a[4:7], a[220:223], a[180:183], a[4:7]\n\t"
"buffer_load_dwordx4 v247, s[12:15], s28 offen offset:256 nt lds\n\t"
"v_mfma_f32_16x16x32_bf16 a[4:7], a[224:227], a[184:187], a[4:7]\n\t"
"s_add_u32 m0, s24, 13056\n\t"
"s_add_u32 s28, s21, 393216\n\t"
"ds_read_b64_tr_b16 v[232:233], v246 offset:32256\n\t"
"v_mfma_f32_16x16x32_bf16 a[4:7], a[228:231], a[188:191], a[4:7]\n\t"
"ds_write2st64_b64 v245, v[228:229], v[230:231] offset0:50 offset1:51\n\t"
"v_cvt_pk_bf16_f32 v224, v224, v225\n\t"
"v_cvt_pk_bf16_f32 v225, v226, v227\n\t"
"v_mfma_f32_16x16x32_bf16 a[12:15], a[220:223], a[184:187], a[12:15]\n\t"
"v_mfma_f32_16x16x32_bf16 a[12:15], a[224:227], a[188:191], a[12:15]\n\t"
"ds_read_b64_tr_b16 v[234:235], v246 offset:35328\n\t"
"v_mfma_f32_16x16x32_bf16 a[12:15], a[228:231], a[192:195], a[12:15]\n\t"
"buffer_load_dwordx4 v247, s[12:15], s28 offen offset:256 nt lds\n\t"
"s_add_u32 m0, s24, 20736\n\t"
"s_add_u32 s28, s21, 589824\n\t"
"v_mfma_f32_16x16x32_bf16 a[20:23], a[220:223], a[188:191], a[20:23]\n\t"
"buffer_load_dwordx4 v247, s[12:15], s28 offen offset:256 nt lds\n\t"
"v_mfma_f32_16x16x32_bf16 a[20:23], a[224:227], a[192:195], a[20:23]\n\t"
"s_add_u32 m0, s24, 60672\n\t"
"s_add_u32 s28, s21, 1769472\n\t"
"ds_read_b64_tr_b16 v[228:229], v246 offset:39936\n\t"
"v_mfma_f32_16x16x32_bf16 a[20:23], a[228:231], a[196:199], a[20:23]\n\t"
"s_waitcnt lgkmcnt(1)\n\t"
"ds_write_b64 v245, v[224:225] offset:26624\n\t"
"ds_read_b64_tr_b16 v[230:231], v246 offset:43008\n\t"
"v_mfma_f32_16x16x32_bf16 v[232:235], v[232:235], v[220:223], 0\n\t"
"buffer_load_dwordx4 v247, s[12:15], s28 offen offset:256 nt lds\n\t"
"s_add_u32 m0, s24, 66816\n\t"
"s_add_u32 s28, s21, 1966080\n\t"
"v_mfma_f32_16x16x32_bf16 a[28:31], a[220:223], a[192:195], a[28:31]\n\t"
"buffer_load_dwordx4 v247, s[12:15], s28 offen offset:256 nt lds\n\t"
"v_mfma_f32_16x16x32_bf16 a[28:31], a[224:227], a[196:199], a[28:31]\n\t"
"ds_read_b64_tr_b16 v[224:225], v246 offset:46080\n\t"
"v_mfma_f32_16x16x32_bf16 a[28:31], a[228:231], a[200:203], a[28:31]\n\t"
"s_waitcnt lgkmcnt(1)\n\t"
"ds_read_b64_tr_b16 v[226:227], v246 offset:49152\n\t"
"v_mfma_f32_16x16x32_bf16 v[228:231], v[228:231], v[220:223], 0\n\t"
"v_mfma_f32_16x16x32_bf16 a[36:39], a[220:223], a[196:199], a[36:39]\n\t"
"v_cvt_pk_bf16_f32 v232, v232, v233\n\t"
"v_cvt_pk_bf16_f32 v233, v234, v235\n\t"
"v_mfma_f32_16x16x32_bf16 a[36:39], a[224:227], a[200:203], a[36:39]\n\t"
"s_waitcnt vmcnt(16)\n\t"
"v_mfma_f32_16x16x32_bf16 a[36:39], a[228:231], a[204:207], a[36:39]\n\t"
"s_waitcnt lgkmcnt(0)\n\t"
"v_mfma_f32_16x16x32_bf16 v[224:227], v[224:227], v[220:223], 0\n\t"
"v_mfma_f32_16x16x32_bf16 a[44:47], a[232:235], a[168:171], a[44:47]\n\t"
"v_cvt_pk_bf16_f32 v234, v228, v229\n\t"
"v_cvt_pk_bf16_f32 v235, v230, v231\n\t"
"v_mfma_f32_16x16x32_bf16 a[44:47], a[236:239], a[172:175], a[44:47]\n\t"
"ds_read_b64_tr_b16 v[228:229], v246 offset:52224\n\t"
"v_mfma_f32_16x16x32_bf16 a[44:47], a[240:243], a[176:179], a[44:47]\n\t"
"ds_write2st64_b64 v245, v[232:233], v[234:235] offset0:53 offset1:54\n\t"
"v_mfma_f32_16x16x32_bf16 a[52:55], a[232:235], a[172:175], a[52:55]\n\t"
"v_mfma_f32_16x16x32_bf16 a[52:55], a[236:239], a[176:179], a[52:55]\n\t"
"ds_read_b64_tr_b16 v[230:231], v246 offset:55296\n\t"
"v_mfma_f32_16x16x32_bf16 a[52:55], a[240:243], a[180:183], a[52:55]\n\t"
"v_cvt_pk_bf16_f32 v224, v224, v225\n\t"
"v_cvt_pk_bf16_f32 v225, v226, v227\n\t"
"v_mfma_f32_16x16x32_bf16 a[60:63], a[232:235], a[176:179], a[60:63]\n\t"
"v_mfma_f32_16x16x32_bf16 a[60:63], a[236:239], a[180:183], a[60:63]\n\t"
"v_mfma_f32_16x16x32_bf16 a[60:63], a[240:243], a[184:187], a[60:63]\n\t"
"v_mfma_f32_16x16x32_bf16 a[68:71], a[232:235], a[180:183], a[68:71]\n\t"
"v_mfma_f32_16x16x32_bf16 a[68:71], a[236:239], a[184:187], a[68:71]\n\t"
"v_mfma_f32_16x16x32_bf16 a[68:71], a[240:243], a[188:191], a[68:71]\n\t"
"v_mfma_f32_16x16x32_bf16 a[76:79], a[232:235], a[184:187], a[76:79]\n\t"
"s_waitcnt lgkmcnt(0)\n\t"
"v_mfma_f32_16x16x32_bf16 a[76:79], a[236:239], a[188:191], a[76:79]\n\t"
"v_mfma_f32_16x16x32_bf16 a[76:79], a[240:243], a[192:195], a[76:79]\n\t"
"v_mfma_f32_16x16x32_bf16 v[228:231], v[228:231], v[220:223], 0\n\t"
"v_mfma_f32_16x16x32_bf16 a[84:87], a[232:235], a[188:191], a[84:87]\n\t"
"buffer_load_dwordx4 a[220:223], v252, s[8:11], s20 offen offset:1024 sc1\n\t"
"v_mfma_f32_16x16x32_bf16 a[84:87], a[236:239], a[192:195], a[84:87]\n\t"
"v_mfma_f32_16x16x32_bf16 a[84:87], a[240:243], a[196:199], a[84:87]\n\t"
"v_mfma_f32_16x16x32_bf16 a[92:95], a[232:235], a[192:195], a[92:95]\n\t"
"v_mfma_f32_16x16x32_bf16 a[92:95], a[236:239], a[196:199], a[92:95]\n\t"
"v_cvt_pk_bf16_f32 v226, v228, v229\n\t"
"v_cvt_pk_bf16_f32 v227, v230, v231\n\t"
"v_mfma_f32_16x16x32_bf16 a[92:95], a[240:243], a[200:203], a[92:95]\n\t"
"ds_write2st64_b64 v245, v[224:225], v[226:227] offset0:55 offset1:56\n\t"
"v_mfma_f32_16x16x32_bf16 a[100:103], a[232:235], a[196:199], a[100:103]\n\t"
"s_waitcnt vmcnt(14)\n\t"
"v_mfma_f32_16x16x32_bf16 a[100:103], a[236:239], a[200:203], a[100:103]\n\t"
"v_mfma_f32_16x16x32_bf16 a[100:103], a[240:243], a[204:207], a[100:103]\n\t"
"v_mfma_f32_16x16x32_bf16 a[108:111], a[244:247], a[168:171], a[108:111]\n\t"
"v_mfma_f32_16x16x32_bf16 a[108:111], a[208:211], a[172:175], a[108:111]\n\t"
"v_mfma_f32_16x16x32_bf16 a[108:111], a[252:255], a[176:179], a[108:111]\n\t"
"s_waitcnt lgkmcnt(0)\n\t"
"s_barrier\n\t"
"v_mfma_f32_16x16x32_bf16 a[116:119], a[244:247], a[172:175], a[116:119]\n\t"
"ds_read_b128 a[168:171], v244 offset:24576\n\t"
"v_mfma_f32_16x16x32_bf16 a[116:119], a[208:211], a[176:179], a[116:119]\n\t"
"ds_read_b128 a[172:175], v244 offset:25088\n\t"
"v_mfma_f32_16x16x32_bf16 a[116:119], a[252:255], a[180:183], a[116:119]\n\t"
"v_mfma_f32_16x16x32_bf16 a[124:127], a[244:247], a[176:179], a[124:127]\n\t"
"ds_read_b128 a[176:179], v244 offset:25600\n\t"
"v_mfma_f32_16x16x32_bf16 a[124:127], a[208:211], a[180:183], a[124:127]\n\t"
"v_mfma_f32_16x16x32_bf16 a[124:127], a[252:255], a[184:187], a[124:127]\n\t"
"v_mfma_f32_16x16x32_bf16 a[132:135], a[244:247], a[180:183], a[132:135]\n\t"
"ds_read_b128 a[180:183], v244 offset:26112\n\t"
"v_mfma_f32_16x16x32_bf16 a[132:135], a[208:211], a[184:187], a[132:135]\n\t"
"v_mfma_f32_16x16x32_bf16 a[132:135], a[252:255], a[188:191], a[132:135]\n\t"
"v_mfma_f32_16x16x32_bf16 a[140:143], a[244:247], a[184:187], a[140:143]\n\t"
"ds_read_b128 a[184:187], v244 offset:26624\n\t"
"v_mfma_f32_16x16x32_bf16 a[140:143], a[208:211], a[188:191], a[140:143]\n\t"
"v_mfma_f32_16x16x32_bf16 a[140:143], a[252:255], a[192:195], a[140:143]\n\t"
"v_mfma_f32_16x16x32_bf16 a[148:151], a[244:247], a[188:191], a[148:151]\n\t"
"ds_read_b128 a[188:191], v244 offset:27136\n\t"
"v_mfma_f32_16x16x32_bf16 a[148:151], a[208:211], a[192:195], a[148:151]\n\t"
"v_mfma_f32_16x16x32_bf16 a[148:151], a[252:255], a[196:199], a[148:151]\n\t"
"v_mfma_f32_16x16x32_bf16 a[156:159], a[244:247], a[192:195], a[156:159]\n\t"
"ds_read_b128 a[192:195], v244 offset:27648\n\t"
"v_mfma_f32_16x16x32_bf16 a[156:159], a[208:211], a[196:199], a[156:159]\n\t"
"v_mfma_f32_16x16x32_bf16 a[156:159], a[252:255], a[200:203], a[156:159]\n\t"
"v_mfma_f32_16x16x32_bf16 a[164:167], a[244:247], a[196:199], a[164:167]\n\t"
"s_waitcnt vmcnt(11) lgkmcnt(4)\n\t"
"ds_read_b128 a[196:199], v244 offset:28160\n\t"
"v_mfma_f32_16x16x32_bf16 a[164:167], a[208:211], a[200:203], a[164:167]\n\t"
"ds_read_b128 a[200:203], v244 offset:28672\n\t"
"v_mfma_f32_16x16x32_bf16 a[164:167], a[252:255], a[204:207], a[164:167]\n\t"
"ds_read_b128 a[204:207], v244 offset:29184\n\t"
"Lst25_%=:\n\t"
"v_mfma_f32_16x16x32_bf16 v[0:3], v[240:243], a[168:171], v[0:3]\n\t"
"buffer_load_dwordx4 a[208:211], v252, s[8:11], s20 offen offset:2048 sc1\n\t"
"v_mfma_f32_16x16x32_bf16 v[0:3], v[96:99], a[172:175], v[0:3]\n\t"
"s_waitcnt lgkmcnt(6)\n\t"
"s_mov_b32 s20, 663552\n\t"
"v_mfma_f32_16x16x32_bf16 v[0:3], a[248:251], a[176:179], v[0:3]\n\t"
"v_mfma_f32_16x16x32_bf16 v[4:7], v[240:243], a[172:175], v[4:7]\n\t"
"s_waitcnt lgkmcnt(5)\n\t"
"v_mfma_f32_16x16x32_bf16 v[4:7], v[96:99], a[176:179], v[4:7]\n\t"
"buffer_load_dwordx4 a[224:227], v252, s[8:11], s20 offen offset:0 sc1\n\t"
"v_mfma_f32_16x16x32_bf16 v[4:7], a[248:251], a[180:183], v[4:7]\n\t"
"v_mfma_f32_16x16x32_bf16 v[8:11], v[240:243], a[176:179], v[8:11]\n\t"
"s_waitcnt lgkmcnt(4)\n\t"
"v_mfma_f32_16x16x32_bf16 v[8:11], v[96:99], a[180:183], v[8:11]\n\t"
"v_mfma_f32_16x16x32_bf16 v[8:11], a[248:251], a[184:187], v[8:11]\n\t"
"buffer_load_dwordx4 a[228:231], v252, s[8:11], s20 offen offset:1024 sc1\n\t"
"v_mfma_f32_16x16x32_bf16 v[12:15], v[240:243], a[180:183], v[12:15]\n\t"
"s_waitcnt lgkmcnt(3)\n\t"
"v_mfma_f32_16x16x32_bf16 v[12:15], v[96:99], a[184:187], v[12:15]\n\t"
"v_mfma_f32_16x16x32_bf16 v[12:15], a[248:251], a[188:191], v[12:15]\n\t"
"v_mfma_f32_16x16x32_bf16 v[16:19], v[240:243], a[184:187], v[16:19]\n\t"
"buffer_load_dwordx4 a[232:235], v252, s[8:11], s20 offen offset:2048 sc1\n\t"
"v_mfma_f32_16x16x32_bf16 v[16:19], v[96:99], a[188:191], v[16:19]\n\t"
"s_waitcnt lgkmcnt(2)\n\t"
"s_mov_b32 s20, 675840\n\t"
"v_mfma_f32_16x16x32_bf16 v[16:19], a[248:251], a[192:195], v[16:19]\n\t"
"ds_read_b64_tr_b16 v[232:233], v246 offset:512\n\t"
"v_mfma_f32_16x16x32_bf16 v[20:23], v[240:243], a[188:191], v[20:23]\n\t"
"s_waitcnt lgkmcnt(2)\n\t"
"ds_read_b64_tr_b16 v[234:235], v246 offset:3584\n\t"
"v_mfma_f32_16x16x32_bf16 v[20:23], v[96:99], a[192:195], v[20:23]\n\t"
"buffer_load_dwordx4 a[236:239], v252, s[8:11], s20 offen offset:0 sc1\n\t"
"v_mfma_f32_16x16x32_bf16 v[20:23], a[248:251], a[196:199], v[20:23]\n\t"
"v_mfma_f32_16x16x32_bf16 v[24:27], v[240:243], a[192:195], v[24:27]\n\t"
"s_waitcnt lgkmcnt(2)\n\t"
"v_mfma_f32_16x16x32_bf16 v[24:27], v[96:99], a[196:199], v[24:27]\n\t"
"v_mfma_f32_16x16x32_bf16 v[24:27], a[248:251], a[200:203], v[24:27]\n\t"
"buffer_load_dwordx4 a[240:243], v252, s[8:11], s20 offen offset:1024 sc1\n\t"
"v_mfma_f32_16x16x32_bf16 v[28:31], v[240:243], a[196:199], v[28:31]\n\t"
"s_waitcnt vmcnt(14)\n\t"
"ds_read_b64_tr_b16 v[228:229], v246 offset:6656\n\t"
"v_mfma_f32_16x16x32_bf16 v[28:31], v[96:99], a[200:203], v[28:31]\n\t"
"ds_read_b64_tr_b16 v[230:231], v246 offset:9728\n\t"
"v_mfma_f32_16x16x32_bf16 v[28:31], a[248:251], a[204:207], v[28:31]\n\t"
"v_mfma_f32_16x16x32_bf16 v[32:35], v[236:239], a[168:171], v[32:35]\n\t"
"buffer_load_dwordx4 v[240:243], v252, s[8:11], s20 offen offset:2048 sc1\n\t"
"v_mfma_f32_16x16x32_bf16 v[32:35], v[248:251], a[172:175], v[32:35]\n\t"
"s_mov_b32 s20, 688128\n\t"
"v_mfma_f32_16x16x32_bf16 v[32:35], a[212:215], a[176:179], v[32:35]\n\t"
"v_mfma_f32_16x16x32_bf16 v[36:39], v[236:239], a[172:175], v[36:39]\n\t"
"s_waitcnt lgkmcnt(2)\n\t"
"v_mfma_f32_16x16x32_bf16 v[36:39], v[248:251], a[176:179], v[36:39]\n\t"
"buffer_load_dwordx4 v[96:99], v252, s[8:11], s20 offen offset:0 sc1\n\t"
"v_mfma_f32_16x16x32_bf16 v[36:39], a[212:215], a[180:183], v[36:39]\n\t"
"v_mfma_f32_16x16x32_bf16 v[232:235], v[232:235], v[220:223], 0\n\t"
"v_mfma_f32_16x16x32_bf16 v[40:43], v[236:239], a[176:179], v[40:43]\n\t"
"ds_read_b64_tr_b16 v[224:225], v246 offset:12800\n\t"
"v_mfma_f32_16x16x32_bf16 v[40:43], v[248:251], a[180:183], v[40:43]\n\t"
"buffer_load_dwordx4 a[244:247], v252, s[8:11], s20 offen offset:1024 sc1\n\t"
"v_mfma_f32_16x16x32_bf16 v[40:43], a[212:215], a[184:187], v[40:43]\n\t"
"ds_read_b64_tr_b16 v[226:227], v246 offset:15872\n\t"
"v_mfma_f32_16x16x32_bf16 v[44:47], v[236:239], a[180:183], v[44:47]\n\t"
"s_waitcnt lgkmcnt(2)\n\t"
"v_mfma_f32_16x16x32_bf16 v[44:47], v[248:251], a[184:187], v[44:47]\n\t"
"buffer_load_dwordx4 a[248:251], v252, s[8:11], s20 offen offset:2048 sc1\n\t"
"v_mfma_f32_16x16x32_bf16 v[44:47], a[212:215], a[188:191], v[44:47]\n\t"
"s_mov_b32 s20, 921600\n\t"
"v_cvt_pk_bf16_f32 v232, v232, v233\n\t"
"v_cvt_pk_bf16_f32 v233, v234, v235\n\t"
"v_mfma_f32_16x16x32_bf16 v[228:231], v[228:231], v[220:223], 0\n\t"
"v_mfma_f32_16x16x32_bf16 v[48:51], v[236:239], a[184:187], v[48:51]\n\t"
"v_mfma_f32_16x16x32_bf16 v[48:51], v[248:251], a[188:191], v[48:51]\n\t"
"buffer_load_dwordx4 a[252:255], v252, s[8:11], s20 offen offset:0 sc1\n\t"
"v_mfma_f32_16x16x32_bf16 v[48:51], a[212:215], a[192:195], v[48:51]\n\t"
"v_mfma_f32_16x16x32_bf16 v[52:55], v[236:239], a[188:191], v[52:55]\n\t"
"v_mfma_f32_16x16x32_bf16 v[52:55], v[248:251], a[192:195], v[52:55]\n\t"
"v_cvt_pk_bf16_f32 v234, v228, v229\n\t"
"v_cvt_pk_bf16_f32 v235, v230, v231\n\t"
"v_mfma_f32_16x16x32_bf16 v[52:55], a[212:215], a[196:199], v[52:55]\n\t"
"ds_read_b64_tr_b16 v[228:229], v246 offset:20480\n\t"
"v_mfma_f32_16x16x32_bf16 v[56:59], v[236:239], a[192:195], v[56:59]\n\t"
"s_waitcnt lgkmcnt(1)\n\t"
"ds_read_b64_tr_b16 v[230:231], v246 offset:23552\n\t"
"v_mfma_f32_16x16x32_bf16 v[56:59], v[248:251], a[196:199], v[56:59]\n\t"
"v_mfma_f32_16x16x32_bf16 v[56:59], a[212:215], a[200:203], v[56:59]\n\t"
"v_mfma_f32_16x16x32_bf16 v[224:227], v[224:227], v[220:223], 0\n\t"
"v_mfma_f32_16x16x32_bf16 v[60:63], v[236:239], a[196:199], v[60:63]\n\t"
"buffer_load_dwordx4 v[236:239], v252, s[8:11], s20 offen offset:1024 sc1\n\t"
"v_mfma_f32_16x16x32_bf16 v[60:63], v[248:251], a[200:203], v[60:63]\n\t"
"s_waitcnt vmcnt(11)\n\t"
"v_mfma_f32_16x16x32_bf16 v[60:63], a[212:215], a[204:207], v[60:63]\n\t"
"v_mfma_f32_16x16x32_bf16 v[64:67], a[216:219], a[168:171], v[64:67]\n\t"
"buffer_load_dwordx4 v[248:251], v252, s[8:11], s20 offen offset:2048 sc1\n\t"
"v_mfma_f32_16x16x32_bf16 v[64:67], a[220:223], a[172:175], v[64:67]\n\t"
"s_mov_b32 s20, 933888\n\t"
"ds_read_b128 a[168:171], v244 offset:33792\n\t"
"v_cvt_pk_bf16_f32 v224, v224, v225\n\t"
"v_mfma_f32_16x16x32_bf16 v[64:67], a[208:211], a[176:179], v[64:67]\n\t"
"v_cvt_pk_bf16_f32 v225, v226, v227\n\t"
"v_mfma_f32_16x16x32_bf16 v[68:71], a[216:219], a[172:175], v[68:71]\n\t"
"buffer_load_dwordx4 a[212:215], v252, s[8:11], s20 offen offset:0 sc1\n\t"
"v_mfma_f32_16x16x32_bf16 v[68:71], a[220:223], a[176:179], v[68:71]\n\t"
"s_waitcnt lgkmcnt(1)\n\t"
"ds_read_b128 a[172:175], v244 offset:34304\n\t"
"v_mfma_f32_16x16x32_bf16 v[68:71], a[208:211], a[180:183], v[68:71]\n\t"
"v_mfma_f32_16x16x32_bf16 v[228:231], v[228:231], v[220:223], 0\n\t"
"v_mfma_f32_16x16x32_bf16 v[72:75], a[216:219], a[176:179], v[72:75]\n\t"
"ds_read_b128 a[176:179], v244 offset:34816\n\t"
"v_mfma_f32_16x16x32_bf16 v[72:75], a[220:223], a[180:183], v[72:75]\n\t"
"v_mfma_f32_16x16x32_bf16 v[72:75], a[208:211], a[184:187], v[72:75]\n\t"
"v_mfma_f32_16x16x32_bf16 v[76:79], a[216:219], a[180:183], v[76:79]\n\t"
"ds_read_b128 a[180:183], v244 offset:35328\n\t"
"v_mfma_f32_16x16x32_bf16 v[76:79], a[220:223], a[184:187], v[76:79]\n\t"
"v_cvt_pk_bf16_f32 v226, v228, v229\n\t"
"v_cvt_pk_bf16_f32 v227, v230, v231\n\t"
"v_mfma_f32_16x16x32_bf16 v[76:79], a[208:211], a[188:191], v[76:79]\n\t"
"ds_read_b64_tr_b16 v[228:229], v246 offset:26624\n\t"
"v_mfma_f32_16x16x32_bf16 v[80:83], a[216:219], a[184:187], v[80:83]\n\t"
"ds_read_b64_tr_b16 v[230:231], v246 offset:29696\n\t"
"v_mfma_f32_16x16x32_bf16 v[80:83], a[220:223], a[188:191], v[80:83]\n\t"
"ds_read_b128 a[184:187], v244 offset:35840\n\t"
"v_mfma_f32_16x16x32_bf16 v[80:83], a[208:211], a[192:195], v[80:83]\n\t"
"v_mfma_f32_16x16x32_bf16 v[84:87], a[216:219], a[188:191], v[84:87]\n\t"
"ds_read_b128 a[188:191], v244 offset:36352\n\t"
"v_mfma_f32_16x16x32_bf16 v[84:87], a[220:223], a[192:195], v[84:87]\n\t"
"v_mfma_f32_16x16x32_bf16 v[84:87], a[208:211], a[196:199], v[84:87]\n\t"
"v_mfma_f32_16x16x32_bf16 v[88:91], a[216:219], a[192:195], v[88:91]\n\t"
"ds_read_b128 a[192:195], v244 offset:36864\n\t"
"v_mfma_f32_16x16x32_bf16 v[88:91], a[220:223], a[196:199], v[88:91]\n\t"
"v_mfma_f32_16x16x32_bf16 v[88:91], a[208:211], a[200:203], v[88:91]\n\t"
"v_mfma_f32_16x16x32_bf16 v[92:95], a[216:219], a[196:199], v[92:95]\n\t"
"buffer_load_dwordx4 a[216:219], v252, s[8:11], s20 offen offset:1024 sc1\n\t"
"v_mfma_f32_16x16x32_bf16 v[92:95], a[220:223], a[200:203], v[92:95]\n\t"
"s_waitcnt vmcnt(11) lgkmcnt(3)\n\t"
"ds_read_b128 a[196:199], v244 offset:37376\n\t"
"v_mfma_f32_16x16x32_bf16 v[92:95], a[208:211], a[204:207], v[92:95]\n\t"
"ds_read_b128 a[200:203], v244 offset:37888\n\t"
"v_mfma_f32_16x16x32_bf16 v[228:231], v[228:231], v[220:223], 0\n\t"
"ds_read_b128 a[204:207], v244 offset:38400\n\t"
"v_mfma_f32_16x16x32_bf16 v[100:103], a[224:227], a[168:171], v[100:103]\n\t"
"buffer_load_dwordx4 a[208:211], v252, s[8:11], s20 offen offset:2048 sc1\n\t"
"v_mfma_f32_16x16x32_bf16 v[100:103], a[228:231], a[172:175], v[100:103]\n\t"
"s_mov_b32 s20, 946176\n\t"
"v_mfma_f32_16x16x32_bf16 v[100:103], a[232:235], a[176:179], v[100:103]\n\t"
"buffer_load_dwordx4 a[220:223], v252, s[8:11], s20 offen offset:0 sc1\n\t"
"v_mfma_f32_16x16x32_bf16 v[104:107], a[224:227], a[172:175], v[104:107]\n\t"
"s_waitcnt lgkmcnt(5)\n\t"
"v_mfma_f32_16x16x32_bf16 v[104:107], a[228:231], a[176:179], v[104:107]\n\t"
"v_cvt_pk_bf16_f32 v228, v228, v229\n\t"
"v_mfma_f32_16x16x32_bf16 v[104:107], a[232:235], a[180:183], v[104:107]\n\t"
"s_waitcnt lgkmcnt(0)\n\t"
"s_barrier\n\t"
"v_mfma_f32_16x16x32_bf16 v[108:111], a[224:227], a[176:179], v[108:111]\n\t"
"v_cvt_pk_bf16_f32 v229, v230, v231\n\t"
"v_mfma_f32_16x16x32_bf16 v[108:111], a[228:231], a[180:183], v[108:111]\n\t"
"v_mfma_f32_16x16x32_bf16 v[108:111], a[232:235], a[184:187], v[108:111]\n\t"
"ds_write2st64_b64 v245, v[232:233], v[234:235] offset0:48 offset1:49\n\t"
"v_mfma_f32_16x16x32_bf16 v[112:115], a[224:227], a[180:183], v[112:115]\n\t"
"v_mfma_f32_16x16x32_bf16 v[112:115], a[228:231], a[184:187], v[112:115]\n\t"
"ds_read_b64_tr_b16 v[232:233], v246 offset:32768\n\t"
"v_mfma_f32_16x16x32_bf16 v[112:115], a[232:235], a[188:191], v[112:115]\n\t"
"ds_write2st64_b64 v245, v[224:225], v[226:227] offset0:50 offset1:51\n\t"
"v_mfma_f32_16x16x32_bf16 v[116:119], a[224:227], a[184:187], v[116:119]\n\t"
"v_mfma_f32_16x16x32_bf16 v[116:119], a[228:231], a[188:191], v[116:119]\n\t"
"ds_read_b64_tr_b16 v[234:235], v246 offset:35840\n\t"
"v_mfma_f32_16x16x32_bf16 v[116:119], a[232:235], a[192:195], v[116:119]\n\t"
"s_add_u32 m0, s24, 72960\n\t"
"s_add_u32 s28, s21, 2162688\n\t"
"ds_write_b64 v245, v[228:229] offset:26624\n\t"
"v_mfma_f32_16x16x32_bf16 v[120:123], a[224:227], a[188:191], v[120:123]\n\t"
"ds_read_b64_tr_b16 v[224:225], v246 offset:40448\n\t"
"v_mfma_f32_16x16x32_bf16 v[120:123], a[228:231], a[192:195], v[120:123]\n\t"
"ds_read_b64_tr_b16 v[226:227], v246 offset:43520\n\t"
"v_mfma_f32_16x16x32_bf16 v[120:123], a[232:235], a[196:199], v[120:123]\n\t"
"s_waitcnt lgkmcnt(3)\n\t"
"ds_read_b64_tr_b16 v[228:229], v246 offset:46592\n\t"
"v_mfma_f32_16x16x32_bf16 v[232:235], v[232:235], v[220:223], 0\n\t"
"ds_read_b64_tr_b16 v[230:231], v246 offset:49664\n\t"
"v_mfma_f32_16x16x32_bf16 v[124:127], a[224:227], a[192:195], v[124:127]\n\t"
"buffer_load_dwordx4 v247, s[12:15], s28 offen offset:256 nt lds\n\t"
"v_mfma_f32_16x16x32_bf16 v[124:127], a[228:231], a[196:199], v[124:127]\n\t"
"s_add_u32 m0, s24, 80640\n\t"
"s_add_u32 s28, s21, 2359296\n\t"
"v_mfma_f32_16x16x32_bf16 v[124:127], a[232:235], a[200:203], v[124:127]\n\t"
"s_waitcnt lgkmcnt(2)\n\t"
"buffer_load_dwordx4 v247, s[12:15], s28 offen offset:256 nt lds\n\t"
"s_add_u32 m0, s24, 26880\n\t"
"s_add_u32 s28, s21, 786432\n\t"
"v_mfma_f32_16x16x32_bf16 v[224:227], v[224:227], v[220:223], 0\n\t"
"buffer_load_dwordx4 v247, s[12:15], s28 offen offset:256 nt lds\n\t"
"v_mfma_f32_16x16x32_bf16 v[128:131], a[224:227], a[196:199], v[128:131]\n\t"
"s_waitcnt lgkmcnt(0)\n\t"
"s_add_u32 m0, s24, 86784\n\t"
"s_add_u32 s28, s21, 2555904\n\t"
"v_mfma_f32_16x16x32_bf16 v[128:131], a[228:231], a[200:203], v[128:131]\n\t"
"buffer_load_dwordx4 v247, s[12:15], s28 offen offset:256 nt lds\n\t"
"v_mfma_f32_16x16x32_bf16 v[128:131], a[232:235], a[204:207], v[128:131]\n\t"
"s_waitcnt vmcnt(14)\n\t"
"s_add_u32 m0, s24, 33024\n\t"
"s_add_u32 s28, s21, 983040\n\t"
"v_mfma_f32_16x16x32_bf16 v[228:231], v[228:231], v[220:223], 0\n\t"
"buffer_load_dwordx4 v247, s[12:15], s28 offen offset:256 nt lds\n\t"
"v_cvt_pk_bf16_f32 v232, v232, v233\n\t"
"v_cvt_pk_bf16_f32 v233, v234, v235\n\t"
"v_mfma_f32_16x16x32_bf16 v[132:135], a[236:239], a[168:171], v[132:135]\n\t"
"s_add_u32 m0, s24, 40704\n\t"
"s_add_u32 s28, s21, 1179648\n\t"
"v_cvt_pk_bf16_f32 v234, v224, v225\n\t"
"v_mfma_f32_16x16x32_bf16 v[132:135], a[240:243], a[172:175], v[132:135]\n\t"
"v_cvt_pk_bf16_f32 v235, v226, v227\n\t"
"v_mfma_f32_16x16x32_bf16 v[132:135], v[240:243], a[176:179], v[132:135]\n\t"
"buffer_load_dwordx4 v247, s[12:15], s28 offen offset:256 nt lds\n\t"
"v_mfma_f32_16x16x32_bf16 v[136:139], a[236:239], a[172:175], v[136:139]\n\t"
"ds_read_b64_tr_b16 v[224:225], v246 offset:52736\n\t"
"v_mfma_f32_16x16x32_bf16 v[136:139], a[240:243], a[176:179], v[136:139]\n\t"
"ds_read_b64_tr_b16 v[226:227], v246 offset:55808\n\t"
"v_cvt_pk_bf16_f32 v228, v228, v229\n\t"
"v_mfma_f32_16x16x32_bf16 v[136:139], v[240:243], a[180:183], v[136:139]\n\t"
"ds_write2st64_b64 v245, v[232:233], v[234:235] offset0:53 offset1:54\n\t"
"v_mfma_f32_16x16x32_bf16 v[140:143], a[236:239], a[176:179], v[140:143]\n\t"
"v_cvt_pk_bf16_f32 v229, v230, v231\n\t"
"v_mfma_f32_16x16x32_bf16 v[140:143], a[240:243], a[180:183], v[140:143]\n\t"
"v_mfma_f32_16x16x32_bf16 v[140:143], v[240:243], a[184:187], v[140:143]\n\t"
"v_mfma_f32_16x16x32_bf16 v[144:147], a[236:239], a[180:183], v[144:147]\n\t"
"v_mfma_f32_16x16x32_bf16 v[144:147], a[240:243], a[184:187], v[144:147]\n\t"
"v_mfma_f32_16x16x32_bf16 v[144:147], v[240:243], a[188:191], v[144:147]\n\t"
"v_mfma_f32_16x16x32_bf16 v[148:151], a[236:239], a[184:187], v[148:151]\n\t"
"s_waitcnt lgkmcnt(1)\n\t"
"v_mfma_f32_16x16x32_bf16 v[148:151], a[240:243], a[188:191], v[148:151]\n\t"
"v_mfma_f32_16x16x32_bf16 v[148:151], v[240:243], a[192:195], v[148:151]\n\t"
"v_mfma_f32_16x16x32_bf16 v[224:227], v[224:227], v[220:223], 0\n\t"
"v_mfma_f32_16x16x32_bf16 v[152:155], a[236:239], a[188:191], v[152:155]\n\t"
"v_mfma_f32_16x16x32_bf16 v[152:155], a[240:243], a[192:195], v[152:155]\n\t"
"v_mfma_f32_16x16x32_bf16 v[152:155], v[240:243], a[196:199], v[152:155]\n\t"
"v_mfma_f32_16x16x32_bf16 v[156:159], a[236:239], a[192:195], v[156:159]\n\t"
"v_mfma_f32_16x16x32_bf16 v[156:159], a[240:243], a[196:199], v[156:159]\n\t"
"v_cvt_pk_bf16_f32 v230, v224, v225\n\t"
"v_cvt_pk_bf16_f32 v231, v226, v227\n\t"
"v_mfma_f32_16x16x32_bf16 v[156:159], v[240:243], a[200:203], v[156:159]\n\t"
"ds_write2st64_b64 v245, v[228:229], v[230:231] offset0:55 offset1:56\n\t"
"v_mfma_f32_16x16x32_bf16 v[160:163], a[236:239], a[196:199], v[160:163]\n\t"
"s_waitcnt vmcnt(13)\n\t"
"v_mfma_f32_16x16x32_bf16 v[160:163], a[240:243], a[200:203], v[160:163]\n\t"
"v_mfma_f32_16x16x32_bf16 v[160:163], v[240:243], a[204:207], v[160:163]\n\t"
"v_mfma_f32_16x16x32_bf16 v[164:167], v[96:99], a[168:171], v[164:167]\n\t"
"v_mfma_f32_16x16x32_bf16 v[164:167], a[244:247], a[172:175], v[164:167]\n\t"
"v_mfma_f32_16x16x32_bf16 v[164:167], a[248:251], a[176:179], v[164:167]\n\t"
"s_waitcnt lgkmcnt(0)\n\t"
"s_barrier\n\t"
"v_mfma_f32_16x16x32_bf16 v[168:171], v[96:99], a[172:175], v[168:171]\n\t"
"ds_read_b128 a[168:171], v244 offset:24576\n\t"
"v_mfma_f32_16x16x32_bf16 v[168:171], a[244:247], a[176:179], v[168:171]\n\t"
"buffer_load_dwordx4 v[240:243], v252, s[8:11], s20 offen offset:1024 sc1\n\t"
"v_mfma_f32_16x16x32_bf16 v[168:171], a[248:251], a[180:183], v[168:171]\n\t"
"ds_read_b128 a[172:175], v244 offset:25088\n\t"
"v_mfma_f32_16x16x32_bf16 v[172:175], v[96:99], a[176:179], v[172:175]\n\t"
"ds_read_b128 a[176:179], v244 offset:25600\n\t"
"v_mfma_f32_16x16x32_bf16 v[172:175], a[244:247], a[180:183], v[172:175]\n\t"
"v_mfma_f32_16x16x32_bf16 v[172:175], a[248:251], a[184:187], v[172:175]\n\t"
"v_mfma_f32_16x16x32_bf16 v[176:179], v[96:99], a[180:183], v[176:179]\n\t"
"ds_read_b128 a[180:183], v244 offset:26112\n\t"
"v_mfma_f32_16x16x32_bf16 v[176:179], a[244:247], a[184:187], v[176:179]\n\t"
"v_mfma_f32_16x16x32_bf16 v[176:179], a[248:251], a[188:191], v[176:179]\n\t"
"v_mfma_f32_16x16x32_bf16 v[180:183], v[96:99], a[184:187], v[180:183]\n\t"
"ds_read_b128 a[184:187], v244 offset:26624\n\t"
"v_mfma_f32_16x16x32_bf16 v[180:183], a[244:247], a[188:191], v[180:183]\n\t"
"v_mfma_f32_16x16x32_bf16 v[180:183], a[248:251], a[192:195], v[180:183]\n\t"
"v_mfma_f32_16x16x32_bf16 v[184:187], v[96:99], a[188:191], v[184:187]\n\t"
"ds_read_b128 a[188:191], v244 offset:27136\n\t"
"v_mfma_f32_16x16x32_bf16 v[184:187], a[244:247], a[192:195], v[184:187]\n\t"
"v_mfma_f32_16x16x32_bf16 v[184:187], a[248:251], a[196:199], v[184:187]\n\t"
"v_mfma_f32_16x16x32_bf16 v[188:191], v[96:99], a[192:195], v[188:191]\n\t"
"ds_read_b128 a[192:195], v244 offset:27648\n\t"
"v_mfma_f32_16x16x32_bf16 v[188:191], a[244:247], a[196:199], v[188:191]\n\t"
"v_mfma_f32_16x16x32_bf16 v[188:191], a[248:251], a[200:203], v[188:191]\n\t"
"v_mfma_f32_16x16x32_bf16 v[192:195], v[96:99], a[196:199], v[192:195]\n\t"
"s_waitcnt vmcnt(11) lgkmcnt(4)\n\t"
"ds_read_b128 a[196:199], v244 offset:28160\n\t"
"v_mfma_f32_16x16x32_bf16 v[192:195], a[244:247], a[200:203], v[192:195]\n\t"
"ds_read_b128 a[200:203], v244 offset:28672\n\t"
"v_mfma_f32_16x16x32_bf16 v[192:195], a[248:251], a[204:207], v[192:195]\n\t"
"ds_read_b128 a[204:207], v244 offset:29184\n\t"
"Lst26_%=:\n\t"
"v_mfma_f32_16x16x32_bf16 v[0:3], a[252:255], a[168:171], v[0:3]\n\t"
"s_waitcnt lgkmcnt(6)\n\t"
"v_xor_b32 v246, 0xc0, v246\n\t"
"v_mfma_f32_16x16x32_bf16 v[0:3], v[236:239], a[172:175], v[0:3]\n\t"
"s_add_u32 s23, s40, 6291456\n\t"
"s_add_u32 s22, s22, 6291456\n\t"
"v_mfma_f32_16x16x32_bf16 v[0:3], v[248:251], a[176:179], v[0:3]\n\t"
"v_mfma_f32_16x16x32_bf16 v[4:7], a[252:255], a[172:175], v[4:7]\n\t"
"s_waitcnt lgkmcnt(5)\n\t"
"v_mfma_f32_16x16x32_bf16 v[4:7], v[236:239], a[176:179], v[4:7]\n\t"
"v_mfma_f32_16x16x32_bf16 v[4:7], v[248:251], a[180:183], v[4:7]\n\t"
"v_mfma_f32_16x16x32_bf16 v[8:11], a[252:255], a[176:179], v[8:11]\n\t"
"s_waitcnt lgkmcnt(4)\n\t"
"v_mfma_f32_16x16x32_bf16 v[8:11], v[236:239], a[180:183], v[8:11]\n\t"
"v_mfma_f32_16x16x32_bf16 v[8:11], v[248:251], a[184:187], v[8:11]\n\t"
"v_mfma_f32_16x16x32_bf16 v[12:15], a[252:255], a[180:183], v[12:15]\n\t"
"s_waitcnt lgkmcnt(3)\n\t"
"v_mfma_f32_16x16x32_bf16 v[12:15], v[236:239], a[184:187], v[12:15]\n\t"
"v_mfma_f32_16x16x32_bf16 v[12:15], v[248:251], a[188:191], v[12:15]\n\t"
"buffer_load_dwordx4 v[96:99], v252, s[8:11], s20 offen offset:2048 sc1\n\t"
"v_mfma_f32_16x16x32_bf16 v[16:19], a[252:255], a[184:187], v[16:19]\n\t"
"s_waitcnt lgkmcnt(2)\n\t"
"s_mov_b32 s20, 958464\n\t"
"v_mfma_f32_16x16x32_bf16 v[16:19], v[236:239], a[188:191], v[16:19]\n\t"
"v_mfma_f32_16x16x32_bf16 v[16:19], v[248:251], a[192:195], v[16:19]\n\t"
"ds_read_b64_tr_b16 v[232:233], v246 offset:512\n\t"
"v_mfma_f32_16x16x32_bf16 v[20:23], a[252:255], a[188:191], v[20:23]\n\t"
"buffer_load_dwordx4 a[224:227], v252, s[8:11], s20 offen offset:0 sc1\n\t"
"v_mfma_f32_16x16x32_bf16 v[20:23], v[236:239], a[192:195], v[20:23]\n\t"
"s_waitcnt lgkmcnt(2)\n\t"
"ds_read_b64_tr_b16 v[234:235], v246 offset:3584\n\t"
"v_mfma_f32_16x16x32_bf16 v[20:23], v[248:251], a[196:199], v[20:23]\n\t"
"v_mfma_f32_16x16x32_bf16 v[24:27], a[252:255], a[192:195], v[24:27]\n\t"
"buffer_load_dwordx4 a[228:231], v252, s[8:11], s20 offen offset:1024 sc1\n\t"
"v_mfma_f32_16x16x32_bf16 v[24:27], v[236:239], a[196:199], v[24:27]\n\t"
"s_waitcnt lgkmcnt(2)\n\t"
"v_mfma_f32_16x16x32_bf16 v[24:27], v[248:251], a[200:203], v[24:27]\n\t"
"v_mfma_f32_16x16x32_bf16 v[28:31], a[252:255], a[196:199], v[28:31]\n\t"
"s_waitcnt vmcnt(11)\n\t"
"ds_read_b64_tr_b16 v[224:225], v246 offset:6656\n\t"
"v_mfma_f32_16x16x32_bf16 v[28:31], v[236:239], a[200:203], v[28:31]\n\t"
"buffer_load_dwordx4 v[236:239], v252, s[8:11], s20 offen offset:2048 sc1\n\t"
"v_mfma_f32_16x16x32_bf16 v[28:31], v[248:251], a[204:207], v[28:31]\n\t"
"s_mov_b32 s20, 970752\n\t"
"ds_read_b64_tr_b16 v[226:227], v246 offset:9728\n\t"
"v_mfma_f32_16x16x32_bf16 v[32:35], a[212:215], a[168:171], v[32:35]\n\t"
"v_mfma_f32_16x16x32_bf16 v[32:35], a[216:219], a[172:175], v[32:35]\n\t"
"v_mfma_f32_16x16x32_bf16 v[32:35], a[208:211], a[176:179], v[32:35]\n\t"
"buffer_load_dwordx4 v[248:251], v252, s[8:11], s20 offen offset:0 sc1\n\t"
"v_mfma_f32_16x16x32_bf16 v[36:39], a[212:215], a[172:175], v[36:39]\n\t"
"s_waitcnt lgkmcnt(2)\n\t"
"v_mfma_f32_16x16x32_bf16 v[36:39], a[216:219], a[176:179], v[36:39]\n\t"
"v_mfma_f32_16x16x32_bf16 v[36:39], a[208:211], a[180:183], v[36:39]\n\t"
"v_mfma_f32_16x16x32_bf16 v[232:235], v[232:235], v[220:223], 0\n\t"
"buffer_load_dwordx4 a[232:235], v252, s[8:11], s20 offen offset:1024 sc1\n\t"
"v_mfma_f32_16x16x32_bf16 v[40:43], a[212:215], a[176:179], v[40:43]\n\t"
"ds_read_b64_tr_b16 v[228:229], v246 offset:12800\n\t"
"v_mfma_f32_16x16x32_bf16 v[40:43], a[216:219], a[180:183], v[40:43]\n\t"
"ds_read_b64_tr_b16 v[230:231], v246 offset:15872\n\t"
"v_mfma_f32_16x16x32_bf16 v[40:43], a[208:211], a[184:187], v[40:43]\n\t"
"v_mfma_f32_16x16x32_bf16 v[44:47], a[212:215], a[180:183], v[44:47]\n\t"
"buffer_load_dwordx4 a[236:239], v252, s[8:11], s20 offen offset:2048 sc1\n\t"
"v_mfma_f32_16x16x32_bf16 v[44:47], a[216:219], a[184:187], v[44:47]\n\t"
"s_waitcnt lgkmcnt(2)\n\t"
"v_cvt_pk_bf16_f32 v232, v232, v233\n\t"
"v_cvt_pk_bf16_f32 v233, v234, v235\n\t"
"v_mfma_f32_16x16x32_bf16 v[44:47], a[208:211], a[188:191], v[44:47]\n\t"
"s_mov_b32 s20, 983040\n\t"
"v_mfma_f32_16x16x32_bf16 v[224:227], v[224:227], v[220:223], 0\n\t"
"v_mfma_f32_16x16x32_bf16 v[48:51], a[212:215], a[184:187], v[48:51]\n\t"
"buffer_load_dwordx4 a[240:243], v252, s[8:11], s20 offen offset:0 sc1\n\t"
"v_mfma_f32_16x16x32_bf16 v[48:51], a[216:219], a[188:191], v[48:51]\n\t"
"v_mfma_f32_16x16x32_bf16 v[48:51], a[208:211], a[192:195], v[48:51]\n\t"
"v_mfma_f32_16x16x32_bf16 v[52:55], a[212:215], a[188:191], v[52:55]\n\t"
"v_mfma_f32_16x16x32_bf16 v[52:55], a[216:219], a[192:195], v[52:55]\n\t"
"buffer_load_dwordx4 a[244:247], v252, s[8:11], s20 offen offset:1024 sc1\n\t"
"v_mfma_f32_16x16x32_bf16 v[52:55], a[208:211], a[196:199], v[52:55]\n\t"
"v_cvt_pk_bf16_f32 v234, v224, v225\n\t"
"v_cvt_pk_bf16_f32 v235, v226, v227\n\t"
"v_mfma_f32_16x16x32_bf16 v[56:59], a[212:215], a[192:195], v[56:59]\n\t"
"s_waitcnt lgkmcnt(0)\n\t"
"ds_read_b64_tr_b16 v[224:225], v246 offset:20480\n\t"
"v_mfma_f32_16x16x32_bf16 v[56:59], a[216:219], a[196:199], v[56:59]\n\t"
"ds_read_b64_tr_b16 v[226:227], v246 offset:23552\n\t"
"v_mfma_f32_16x16x32_bf16 v[56:59], a[208:211], a[200:203], v[56:59]\n\t"
"buffer_load_dwordx4 a[248:251], v252, s[8:11], s20 offen offset:2048 sc1\n\t"
"v_mfma_f32_16x16x32_bf16 v[228:231], v[228:231], v[220:223], 0\n\t"
"s_mov_b32 s20, 995328\n\t"
"v_mfma_f32_16x16x32_bf16 v[60:63], a[212:215], a[196:199], v[60:63]\n\t"
"s_waitcnt vmcnt(9)\n\t"
"v_mfma_f32_16x16x32_bf16 v[60:63], a[216:219], a[200:203], v[60:63]\n\t"
"v_mfma_f32_16x16x32_bf16 v[60:63], a[208:211], a[204:207], v[60:63]\n\t"
"buffer_load_dwordx4 a[212:215], v252, s[8:11], s20 offen offset:0 sc1\n\t"
"v_mfma_f32_16x16x32_bf16 v[64:67], a[220:223], a[168:171], v[64:67]\n\t"
"ds_read_b128 a[168:171], v244 offset:33792\n\t"
"v_mfma_f32_16x16x32_bf16 v[64:67], v[240:243], a[172:175], v[64:67]\n\t"
"v_cvt_pk_bf16_f32 v228, v228, v229\n\t"
"v_cvt_pk_bf16_f32 v229, v230, v231\n\t"
"v_mfma_f32_16x16x32_bf16 v[64:67], v[96:99], a[176:179], v[64:67]\n\t"
"v_mfma_f32_16x16x32_bf16 v[68:71], a[220:223], a[172:175], v[68:71]\n\t"
"buffer_load_dwordx4 a[216:219], v252, s[8:11], s20 offen offset:1024 sc1\n\t"
"v_mfma_f32_16x16x32_bf16 v[68:71], v[240:243], a[176:179], v[68:71]\n\t"
"s_waitcnt lgkmcnt(1)\n\t"
"ds_read_b128 a[172:175], v244 offset:34304\n\t"
"v_mfma_f32_16x16x32_bf16 v[68:71], v[96:99], a[180:183], v[68:71]\n\t"
"buffer_load_dwordx4 a[252:255], v252, s[8:11], s20 offen offset:2048 sc1\n\t"
"v_mfma_f32_16x16x32_bf16 v[224:227], v[224:227], v[220:223], 0\n\t"
"v_mfma_f32_16x16x32_bf16 v[72:75], a[220:223], a[176:179], v[72:75]\n\t"
"ds_read_b128 a[176:179], v244 offset:34816\n\t"
"v_mfma_f32_16x16x32_bf16 v[72:75], v[240:243], a[180:183], v[72:75]\n\t"
"v_mfma_f32_16x16x32_bf16 v[72:75], v[96:99], a[184:187], v[72:75]\n\t"
"v_mfma_f32_16x16x32_bf16 v[76:79], a[220:223], a[180:183], v[76:79]\n\t"
"ds_read_b128 a[180:183], v244 offset:35328\n\t"
"v_mfma_f32_16x16x32_bf16 v[76:79], v[240:243], a[184:187], v[76:79]\n\t"
"v_cvt_pk_bf16_f32 v230, v224, v225\n\t"
"v_cvt_pk_bf16_f32 v231, v226, v227\n\t"
"v_mfma_f32_16x16x32_bf16 v[76:79], v[96:99], a[188:191], v[76:79]\n\t"
"ds_read_b64_tr_b16 v[224:225], v246 offset:26624\n\t"
"v_mfma_f32_16x16x32_bf16 v[80:83], a[220:223], a[184:187], v[80:83]\n\t"
"ds_read_b64_tr_b16 v[226:227], v246 offset:29696\n\t"
"v_mfma_f32_16x16x32_bf16 v[80:83], v[240:243], a[188:191], v[80:83]\n\t"
"ds_read_b128 a[184:187], v244 offset:35840\n\t"
"v_mfma_f32_16x16x32_bf16 v[80:83], v[96:99], a[192:195], v[80:83]\n\t"
"v_mfma_f32_16x16x32_bf16 v[84:87], a[220:223], a[188:191], v[84:87]\n\t"
"s_mov_b32 s20, 1007616\n\t"
"ds_read_b128 a[188:191], v244 offset:36352\n\t"
"v_mfma_f32_16x16x32_bf16 v[84:87], v[240:243], a[192:195], v[84:87]\n\t"
"v_mfma_f32_16x16x32_bf16 v[84:87], v[96:99], a[196:199], v[84:87]\n\t"
"v_mfma_f32_16x16x32_bf16 v[88:91], a[220:223], a[192:195], v[88:91]\n\t"
"ds_read_b128 a[192:195], v244 offset:36864\n\t"
"v_mfma_f32_16x16x32_bf16 v[88:91], v[240:243], a[196:199], v[88:91]\n\t"
"v_mfma_f32_16x16x32_bf16 v[88:91], v[96:99], a[200:203], v[88:91]\n\t"
"v_mfma_f32_16x16x32_bf16 v[92:95], a[220:223], a[196:199], v[92:95]\n\t"
"buffer_load_dwordx4 a[220:223], v252, s[8:11], s20 offen offset:0 sc1\n\t"
"v_mfma_f32_16x16x32_bf16 v[92:95], v[240:243], a[200:203], v[92:95]\n\t"
"s_waitcnt vmcnt(10) lgkmcnt(3)\n\t"
"ds_read_b128 a[196:199], v244 offset:37376\n\t"
"v_mfma_f32_16x16x32_bf16 v[92:95], v[96:99], a[204:207], v[92:95]\n\t"
"ds_read_b128 a[200:203], v244 offset:37888\n\t"
"v_mfma_f32_16x16x32_bf16 v[224:227], v[224:227], v[220:223], 0\n\t"
"ds_read_b128 a[204:207], v244 offset:38400\n\t"
"v_mfma_f32_16x16x32_bf16 v[100:103], a[224:227], a[168:171], v[100:103]\n\t"
"buffer_load_dwordx4 v[96:99], v252, s[8:11], s20 offen offset:1024 sc1\n\t"
"v_mfma_f32_16x16x32_bf16 v[100:103], a[228:231], a[172:175], v[100:103]\n\t"
"v_mfma_f32_16x16x32_bf16 v[100:103], v[236:239], a[176:179], v[100:103]\n\t"
"v_mfma_f32_16x16x32_bf16 v[104:107], a[224:227], a[172:175], v[104:107]\n\t"
"s_waitcnt lgkmcnt(5)\n\t"
"v_mfma_f32_16x16x32_bf16 v[104:107], a[228:231], a[176:179], v[104:107]\n\t"
"v_cvt_pk_bf16_f32 v224, v224, v225\n\t"
"v_mfma_f32_16x16x32_bf16 v[104:107], v[236:239], a[180:183], v[104:107]\n\t"
"s_waitcnt lgkmcnt(0)\n\t"
"s_barrier\n\t"
"v_mfma_f32_16x16x32_bf16 v[108:111], a[224:227], a[176:179], v[108:111]\n\t"
"v_cvt_pk_bf16_f32 v225, v226, v227\n\t"
"v_mfma_f32_16x16x32_bf16 v[108:111], a[228:231], a[180:183], v[108:111]\n\t"
"v_mfma_f32_16x16x32_bf16 v[108:111], v[236:239], a[184:187], v[108:111]\n\t"
"ds_write2st64_b64 v245, v[232:233], v[234:235] offset0:48 offset1:49\n\t"
"v_mfma_f32_16x16x32_bf16 v[112:115], a[224:227], a[180:183], v[112:115]\n\t"
"v_mfma_f32_16x16x32_bf16 v[112:115], a[228:231], a[184:187], v[112:115]\n\t"
"ds_read_b64_tr_b16 v[232:233], v246 offset:32768\n\t"
"v_mfma_f32_16x16x32_bf16 v[112:115], v[236:239], a[188:191], v[112:115]\n\t"
"ds_write2st64_b64 v245, v[228:229], v[230:231] offset0:50 offset1:51\n\t"
"v_mfma_f32_16x16x32_bf16 v[116:119], a[224:227], a[184:187], v[116:119]\n\t"
"global_load_dwordx4 v[240:243], v252, s[48:49] offset:2048\n\t"
"v_mfma_f32_16x16x32_bf16 v[116:119], a[228:231], a[188:191], v[116:119]\n\t"
"ds_read_b64_tr_b16 v[234:235], v246 offset:35840\n\t"
"v_mfma_f32_16x16x32_bf16 v[116:119], v[236:239], a[192:195], v[116:119]\n\t"
"ds_write_b64 v245, v[224:225] offset:26624\n\t"
"v_mfma_f32_16x16x32_bf16 v[120:123], a[224:227], a[188:191], v[120:123]\n\t"
"ds_read_b64_tr_b16 v[228:229], v246 offset:40448\n\t"
"v_mfma_f32_16x16x32_bf16 v[120:123], a[228:231], a[192:195], v[120:123]\n\t"
"ds_read_b64_tr_b16 v[230:231], v246 offset:43520\n\t"
"v_mfma_f32_16x16x32_bf16 v[120:123], v[236:239], a[196:199], v[120:123]\n\t"
"s_waitcnt lgkmcnt(3)\n\t"
"ds_read_b64_tr_b16 v[224:225], v246 offset:46592\n\t"
"v_mfma_f32_16x16x32_bf16 v[232:235], v[232:235], v[220:223], 0\n\t"
"ds_read_b64_tr_b16 v[226:227], v246 offset:49664\n\t"
"v_mfma_f32_16x16x32_bf16 v[124:127], a[224:227], a[192:195], v[124:127]\n\t"
"global_load_dwordx4 a[208:211], v252, s[46:47] offset:1024\n\t"
"v_mfma_f32_16x16x32_bf16 v[124:127], a[228:231], a[196:199], v[124:127]\n\t"
"v_mfma_f32_16x16x32_bf16 v[124:127], v[236:239], a[200:203], v[124:127]\n\t"
"s_waitcnt lgkmcnt(2)\n\t"
"v_mfma_f32_16x16x32_bf16 v[228:231], v[228:231], v[220:223], 0\n\t"
"v_mfma_f32_16x16x32_bf16 v[128:131], a[224:227], a[196:199], v[128:131]\n\t"
"buffer_load_dwordx4 a[224:227], v252, s[8:11], s20 offen offset:2048 sc1\n\t"
"v_mfma_f32_16x16x32_bf16 v[128:131], a[228:231], a[200:203], v[128:131]\n\t"
"s_waitcnt vmcnt(11) lgkmcnt(0)\n\t"
"s_mov_b32 s20, 1019904\n\t"
"v_cvt_pk_bf16_f32 v232, v232, v233\n\t"
"v_mfma_f32_16x16x32_bf16 v[128:131], v[236:239], a[204:207], v[128:131]\n\t"
"v_cvt_pk_bf16_f32 v233, v234, v235\n\t"
"v_mfma_f32_16x16x32_bf16 v[224:227], v[224:227], v[220:223], 0\n\t"
"buffer_load_dwordx4 a[228:231], v252, s[8:11], s20 offen offset:0 sc1\n\t"
"v_mfma_f32_16x16x32_bf16 v[132:135], v[248:251], a[168:171], v[132:135]\n\t"
"s_add_u32 m0, s24, 46848\n\t"
"s_add_u32 s28, s21, 1376256\n\t"
"v_cvt_pk_bf16_f32 v234, v228, v229\n\t"
"v_mfma_f32_16x16x32_bf16 v[132:135], a[232:235], a[172:175], v[132:135]\n\t"
"v_cvt_pk_bf16_f32 v235, v230, v231\n\t"
"v_mfma_f32_16x16x32_bf16 v[132:135], a[236:239], a[176:179], v[132:135]\n\t"
"ds_write2st64_b64 v245, v[232:233], v[234:235] offset0:53 offset1:54\n\t"
"v_mfma_f32_16x16x32_bf16 v[136:139], v[248:251], a[172:175], v[136:139]\n\t"
"buffer_load_dwordx4 v247, s[12:15], s28 offen offset:256 nt lds\n\t"
"v_mfma_f32_16x16x32_bf16 v[136:139], a[232:235], a[176:179], v[136:139]\n\t"
"s_add_u32 m0, s24, 52992\n\t"
"s_add_u32 s28, s21, 1572864\n\t"
"ds_read_b64_tr_b16 v[228:229], v246 offset:52736\n\t"
"v_mfma_f32_16x16x32_bf16 v[136:139], a[236:239], a[180:183], v[136:139]\n\t"
"buffer_load_dwordx4 v247, s[12:15], s28 offen offset:256 nt lds\n\t"
"v_mfma_f32_16x16x32_bf16 v[140:143], v[248:251], a[176:179], v[140:143]\n\t"
"s_add_u32 m0, s24, 92928\n\t"
"s_add_u32 s28, s21, 2752512\n\t"
"ds_read_b64_tr_b16 v[230:231], v246 offset:55808\n\t"
"v_mfma_f32_16x16x32_bf16 v[140:143], a[232:235], a[180:183], v[140:143]\n\t"
"buffer_load_dwordx4 v247, s[12:15], s28 offen offset:256 nt lds\n\t"
"v_mfma_f32_16x16x32_bf16 v[140:143], a[236:239], a[184:187], v[140:143]\n\t"
"s_add_u32 m0, s24, 100608\n\t"
"s_add_u32 s28, s21, 2949120\n\t"
"v_cvt_pk_bf16_f32 v224, v224, v225\n\t"
"v_cvt_pk_bf16_f32 v225, v226, v227\n\t"
"v_mfma_f32_16x16x32_bf16 v[144:147], v[248:251], a[180:183], v[144:147]\n\t"
"v_mfma_f32_16x16x32_bf16 v[144:147], a[232:235], a[184:187], v[144:147]\n\t"
"buffer_load_dwordx4 v247, s[12:15], s28 offen offset:256 nt lds\n\t"
"v_mfma_f32_16x16x32_bf16 v[144:147], a[236:239], a[188:191], v[144:147]\n\t"
"s_add_u32 m0, s24, 106752\n\t"
"s_add_u32 s28, s21, 3145728\n\t"
"v_mfma_f32_16x16x32_bf16 v[148:151], v[248:251], a[184:187], v[148:151]\n\t"
"buffer_load_dwordx4 v247, s[12:15], s28 offen offset:256 nt lds\n\t"
"v_mfma_f32_16x16x32_bf16 v[148:151], a[232:235], a[188:191], v[148:151]\n\t"
"s_waitcnt lgkmcnt(0)\n\t"
"s_add_u32 m0, s24, 112896\n\t"
"s_add_u32 s28, s21, 3342336\n\t"
"v_mfma_f32_16x16x32_bf16 v[148:151], a[236:239], a[192:195], v[148:151]\n\t"
"buffer_load_dwordx4 v247, s[12:15], s28 offen offset:256 nt lds\n\t"
"s_add_u32 m0, s25, 768\n\t"
"v_mfma_f32_16x16x32_bf16 v[228:231], v[228:231], v[220:223], 0\n\t"
"buffer_load_dwordx4 v254, s[12:15], 0 offen offset:256 nt lds\n\t"
"v_mfma_f32_16x16x32_bf16 v[152:155], v[248:251], a[188:191], v[152:155]\n\t"
"s_add_u32 m0, s26, 768\n\t"
"v_mfma_f32_16x16x32_bf16 v[152:155], a[232:235], a[192:195], v[152:155]\n\t"
"v_mfma_f32_16x16x32_bf16 v[152:155], a[236:239], a[196:199], v[152:155]\n\t"
"s_mov_b64 exec, s[36:37]\n\t"
"buffer_load_dwordx4 v254, s[12:15], s27 offen offset:256 nt lds\n\t"
"s_mov_b64 exec, -1\n\t"
"v_mfma_f32_16x16x32_bf16 v[156:159], v[248:251], a[192:195], v[156:159]\n\t"
"s_waitcnt vmcnt(11)\n\t"
"ds_read_b64_tr_b16 v[236:237], v240 offset:512\n\t"
"v_mfma_f32_16x16x32_bf16 v[156:159], a[232:235], a[196:199], v[156:159]\n\t"
"ds_read_b64_tr_b16 v[238:239], v240 offset:2048\n\t"
"v_cvt_pk_bf16_f32 v226, v228, v229\n\t"
"v_mfma_f32_16x16x32_bf16 v[156:159], a[236:239], a[200:203], v[156:159]\n\t"
"ds_read_b64_tr_b16 v[232:233], v241 offset:512\n\t"
"v_cvt_pk_bf16_f32 v227, v230, v231\n\t"
"v_mfma_f32_16x16x32_bf16 v[160:163], v[248:251], a[196:199], v[160:163]\n\t"
"ds_read_b64_tr_b16 v[234:235], v241 offset:2048\n\t"
"v_mfma_f32_16x16x32_bf16 v[160:163], a[232:235], a[200:203], v[160:163]\n\t"
"v_mfma_f32_16x16x32_bf16 v[160:163], a[236:239], a[204:207], v[160:163]\n\t"
"ds_write2st64_b64 v245, v[224:225], v[226:227] offset0:55 offset1:56\n\t"
"v_mfma_f32_16x16x32_bf16 v[164:167], a[240:243], a[168:171], v[164:167]\n\t"
"v_mfma_f32_16x16x32_bf16 v[164:167], a[244:247], a[172:175], v[164:167]\n\t"
"v_mfma_f32_16x16x32_bf16 v[164:167], a[248:251], a[176:179], v[164:167]\n\t"
"s_waitcnt lgkmcnt(0)\n\t"
"s_barrier\n\t"
"v_mfma_f32_16x16x32_bf16 v[168:171], a[240:243], a[172:175], v[168:171]\n\t"
"ds_read_b64_tr_b16 v[228:229], v240 offset:20480\n\t"
"v_mfma_f32_16x16x32_bf16 v[168:171], a[244:247], a[176:179], v[168:171]\n\t"
"ds_read_b64_tr_b16 v[230:231], v240 offset:22016\n\t"
"v_mfma_f32_16x16x32_bf16 v[168:171], a[248:251], a[180:183], v[168:171]\n\t"
"ds_read_b64_tr_b16 v[224:225], v241 offset:20480\n\t"
"v_mfma_f32_16x16x32_bf16 v[172:175], a[240:243], a[176:179], v[172:175]\n\t"
"s_waitcnt vmcnt(10)\n\t"
"ds_read_b64_tr_b16 v[226:227], v241 offset:22016\n\t"
"v_mfma_f32_16x16x32_bf16 v[172:175], a[244:247], a[180:183], v[172:175]\n\t"
"ds_read_b128 a[168:171], v244 offset:24576\n\t"
"v_mfma_f32_16x16x32_bf16 v[172:175], a[248:251], a[184:187], v[172:175]\n\t"
"ds_read_b128 a[172:175], v244 offset:25088\n\t"
"v_mfma_f32_16x16x32_bf16 v[236:239], v[236:239], a[208:211], 0\n\t"
"ds_read_b128 a[176:179], v244 offset:25600\n\t"
"v_mfma_f32_16x16x32_bf16 v[232:235], v[232:235], a[208:211], 0\n\t"
"v_mfma_f32_16x16x32_bf16 v[176:179], a[240:243], a[180:183], v[176:179]\n\t"
"ds_read_b128 a[180:183], v244 offset:26112\n\t"
"v_mfma_f32_16x16x32_bf16 v[176:179], a[244:247], a[184:187], v[176:179]\n\t"
"v_mfma_f32_16x16x32_bf16 v[176:179], a[248:251], a[188:191], v[176:179]\n\t"
"v_mfma_f32_16x16x32_bf16 v[180:183], a[240:243], a[184:187], v[180:183]\n\t"
"s_waitcnt lgkmcnt(6)\n\t"
"ds_read_b128 a[184:187], v244 offset:26624\n\t"
"v_cvt_pk_bf16_f32 v236, v236, v237\n\t"
"v_mfma_f32_16x16x32_bf16 v[180:183], a[244:247], a[188:191], v[180:183]\n\t"
"s_waitcnt lgkmcnt(5)\n\t"
"v_cvt_pk_bf16_f32 v237, v238, v239\n\t"
"v_cvt_pk_bf16_f32 v238, v232, v233\n\t"
"v_mfma_f32_16x16x32_bf16 v[180:183], a[248:251], a[192:195], v[180:183]\n\t"
"v_cvt_pk_bf16_f32 v239, v234, v235\n\t"
"v_mfma_f32_16x16x32_bf16 v[228:231], v[228:231], a[208:211], 0\n\t"
"v_mfma_f32_16x16x32_bf16 v[224:227], v[224:227], a[208:211], 0\n\t"
"ds_read_b64_tr_b16 v[232:233], v240 offset:40448\n\t"
"v_mfma_f32_16x16x32_bf16 v[184:187], a[240:243], a[188:191], v[184:187]\n\t"
"ds_read_b128 a[188:191], v244 offset:27136\n\t"
"v_mfma_f32_16x16x32_bf16 v[184:187], a[244:247], a[192:195], v[184:187]\n\t"
"ds_read_b64_tr_b16 v[234:235], v240 offset:41984\n\t"
"v_mfma_f32_16x16x32_bf16 v[184:187], a[248:251], a[196:199], v[184:187]\n\t"
"v_mfma_f32_16x16x32_bf16 v[188:191], a[240:243], a[192:195], v[188:191]\n\t"
"ds_read_b128 a[192:195], v244 offset:27648\n\t"
"v_cvt_pk_bf16_f32 v228, v228, v229\n\t"
"v_mfma_f32_16x16x32_bf16 v[188:191], a[244:247], a[196:199], v[188:191]\n\t"
"v_cvt_pk_bf16_f32 v229, v230, v231\n\t"
"v_cvt_pk_bf16_f32 v230, v224, v225\n\t"
"v_mfma_f32_16x16x32_bf16 v[188:191], a[248:251], a[200:203], v[188:191]\n\t"
"v_cvt_pk_bf16_f32 v231, v226, v227\n\t"
"v_mfma_f32_16x16x32_bf16 v[192:195], a[240:243], a[196:199], v[192:195]\n\t"
"s_waitcnt lgkmcnt(6)\n\t"
"ds_read_b64_tr_b16 v[224:225], v241 offset:40448\n\t"
"v_mfma_f32_16x16x32_bf16 v[192:195], a[244:247], a[200:203], v[192:195]\n\t"
"ds_read_b64_tr_b16 v[226:227], v241 offset:41984\n\t"
"v_mfma_f32_16x16x32_bf16 v[192:195], a[248:251], a[204:207], v[192:195]\n\t"
"ds_read_b128 a[196:199], v244 offset:28160\n\t"
"Lst27_%=:\n\t"
"v_mfma_f32_16x16x32_bf16 v[196:199], a[212:215], a[168:171], v[196:199]\n\t"
"s_waitcnt lgkmcnt(8)\n\t"
"ds_read_b128 a[200:203], v244 offset:28672\n\t"
"v_mfma_f32_16x16x32_bf16 v[196:199], a[216:219], a[172:175], v[196:199]\n\t"
"ds_read_b128 a[204:207], v244 offset:29184\n\t"
"v_mfma_f32_16x16x32_bf16 v[196:199], a[252:255], a[176:179], v[196:199]\n\t"
"buffer_load_dwordx4 v[248:251], v252, s[8:11], s20 offen offset:1024 sc1\n\t"
"v_mfma_f32_16x16x32_bf16 v[204:207], a[212:215], a[172:175], v[204:207]\n\t"
"s_waitcnt lgkmcnt(6)\n\t"
"v_mfma_f32_16x16x32_bf16 v[204:207], a[216:219], a[176:179], v[204:207]\n\t"
"s_waitcnt lgkmcnt(3)\n\t"
"v_mfma_f32_16x16x32_bf16 v[204:207], a[252:255], a[180:183], v[204:207]\n\t"
"v_mfma_f32_16x16x32_bf16 v[232:235], v[232:235], a[208:211], 0\n\t"
"v_add_u32 v240, 59904, v240\n\t"
"v_add_u32 v241, 59904, v241\n\t"
"v_mfma_f32_16x16x32_bf16 v[224:227], v[224:227], a[208:211], 0\n\t"
"v_mfma_f32_16x16x32_bf16 v[212:215], a[212:215], a[176:179], v[212:215]\n\t"
"v_mfma_f32_16x16x32_bf16 v[212:215], a[216:219], a[180:183], v[212:215]\n\t"
"v_mfma_f32_16x16x32_bf16 v[212:215], a[252:255], a[184:187], v[212:215]\n\t"
"s_barrier\n\t"
"v_mfma_f32_16x16x32_bf16 a[0:3], a[212:215], a[180:183], a[0:3]\n\t"
"v_cvt_pk_bf16_f32 v232, v232, v233\n\t"
"v_mfma_f32_16x16x32_bf16 a[0:3], a[216:219], a[184:187], a[0:3]\n\t"
"v_cvt_pk_bf16_f32 v233, v234, v235\n\t"
"v_cvt_pk_bf16_f32 v234, v224, v225\n\t"
"v_mfma_f32_16x16x32_bf16 a[0:3], a[252:255], a[188:191], a[0:3]\n\t"
"ds_write_b128 v243, v[236:239] offset:512\n\t"
"v_mfma_f32_16x16x32_bf16 a[8:11], a[212:215], a[184:187], a[8:11]\n\t"
"s_waitcnt lgkmcnt(3)\n\t"
"v_cvt_pk_bf16_f32 v235, v226, v227\n\t"
"v_mfma_f32_16x16x32_bf16 a[8:11], a[216:219], a[188:191], a[8:11]\n\t"
"ds_read_b64_tr_b16 v[236:237], v240 offset:512\n\t"
"v_mfma_f32_16x16x32_bf16 a[8:11], a[252:255], a[192:195], a[8:11]\n\t"
"ds_write_b128 v243, v[228:231] offset:9728\n\t"
"v_mfma_f32_16x16x32_bf16 a[16:19], a[212:215], a[188:191], a[16:19]\n\t"
"buffer_load_dwordx4 a[232:235], v252, s[8:11], s20 offen offset:2048 sc1\n\t"
"v_mfma_f32_16x16x32_bf16 a[16:19], a[216:219], a[192:195], a[16:19]\n\t"
"s_waitcnt lgkmcnt(4)\n\t"
"ds_read_b64_tr_b16 v[238:239], v240 offset:2048\n\t"
"v_mfma_f32_16x16x32_bf16 a[16:19], a[252:255], a[196:199], a[16:19]\n\t"
"s_mov_b32 s20, 1032192\n\t"
"ds_write_b128 v243, v[232:235] offset:18944\n\t"
"v_mfma_f32_16x16x32_bf16 a[24:27], a[212:215], a[192:195], a[24:27]\n\t"
"buffer_load_dwordx4 a[236:239], v252, s[8:11], s20 offen offset:0 sc1\n\t"
"v_mfma_f32_16x16x32_bf16 a[24:27], a[216:219], a[196:199], a[24:27]\n\t"
"s_waitcnt lgkmcnt(5)\n\t"
"ds_read_b64_tr_b16 v[224:225], v241 offset:512\n\t"
"v_mfma_f32_16x16x32_bf16 a[24:27], a[252:255], a[200:203], a[24:27]\n\t"
"ds_read_b64_tr_b16 v[226:227], v241 offset:2048\n\t"
"v_mfma_f32_16x16x32_bf16 a[32:35], a[212:215], a[196:199], a[32:35]\n\t"
"s_waitcnt lgkmcnt(3)\n\t"
"ds_read_b64_tr_b16 v[228:229], v240 offset:20480\n\t"
"v_mfma_f32_16x16x32_bf16 a[32:35], a[216:219], a[200:203], a[32:35]\n\t"
"ds_read_b64_tr_b16 v[230:231], v240 offset:22016\n\t"
"v_mfma_f32_16x16x32_bf16 a[32:35], a[252:255], a[204:207], a[32:35]\n\t"
"s_waitcnt vmcnt(12)\n\t"
"ds_read_b64_tr_b16 v[232:233], v241 offset:20480\n\t"
"v_mfma_f32_16x16x32_bf16 v[236:239], v[236:239], a[208:211], 0\n\t"
"s_waitcnt lgkmcnt(3)\n\t"
"ds_read_b64_tr_b16 v[234:235], v241 offset:22016\n\t"
"v_mfma_f32_16x16x32_bf16 v[224:227], v[224:227], a[208:211], 0\n\t"
"buffer_load_dwordx4 a[212:215], v252, s[8:11], s20 offen offset:1024 sc1\n\t"
"v_mfma_f32_16x16x32_bf16 a[40:43], a[220:223], a[168:171], a[40:43]\n\t"
"buffer_load_dwordx4 a[216:219], v252, s[8:11], s20 offen offset:2048 sc1\n\t"
"v_mfma_f32_16x16x32_bf16 a[40:43], v[96:99], a[172:175], a[40:43]\n\t"
"s_mov_b32 s20, 1044480\n\t"
"v_mfma_f32_16x16x32_bf16 a[40:43], a[224:227], a[176:179], a[40:43]\n\t"
"v_mfma_f32_16x16x32_bf16 a[48:51], a[220:223], a[172:175], a[48:51]\n\t"
"s_waitcnt lgkmcnt(2)\n\t"
"v_cvt_pk_bf16_f32 v236, v236, v237\n\t"
"v_cvt_pk_bf16_f32 v237, v238, v239\n\t"
"v_mfma_f32_16x16x32_bf16 a[48:51], v[96:99], a[176:179], a[48:51]\n\t"
"s_waitcnt lgkmcnt(0)\n\t"
"v_cvt_pk_bf16_f32 v238, v224, v225\n\t"
"v_cvt_pk_bf16_f32 v239, v226, v227\n\t"
"v_mfma_f32_16x16x32_bf16 a[48:51], a[224:227], a[180:183], a[48:51]\n\t"
"ds_write_b128 v243, v[236:239] offset:28160\n\t"
"v_mfma_f32_16x16x32_bf16 v[228:231], v[228:231], a[208:211], 0\n\t"
"buffer_load_dwordx4 a[240:243], v252, s[8:11], s20 offen offset:0 sc1\n\t"
"v_mfma_f32_16x16x32_bf16 v[232:235], v[232:235], a[208:211], 0\n\t"
"ds_read_b64_tr_b16 v[224:225], v240 offset:40448\n\t"
"v_mfma_f32_16x16x32_bf16 a[56:59], a[220:223], a[176:179], a[56:59]\n\t"
"buffer_load_dwordx4 a[244:247], v252, s[8:11], s20 offen offset:1024 sc1\n\t"
"v_mfma_f32_16x16x32_bf16 a[56:59], v[96:99], a[180:183], a[56:59]\n\t"
"ds_read_b64_tr_b16 v[226:227], v240 offset:41984\n\t"
"v_mfma_f32_16x16x32_bf16 a[56:59], a[224:227], a[184:187], a[56:59]\n\t"
"ds_read_b64_tr_b16 v[236:237], v241 offset:40448\n\t"
"v_mfma_f32_16x16x32_bf16 a[64:67], a[220:223], a[180:183], a[64:67]\n\t"
"ds_read_b64_tr_b16 v[238:239], v241 offset:41984\n\t"
"v_cvt_pk_bf16_f32 v228, v228, v229\n\t"
"v_mfma_f32_16x16x32_bf16 a[64:67], v[96:99], a[184:187], a[64:67]\n\t"
"v_cvt_pk_bf16_f32 v229, v230, v231\n\t"
"v_cvt_pk_bf16_f32 v230, v232, v233\n\t"
"v_mfma_f32_16x16x32_bf16 a[64:67], a[224:227], a[188:191], a[64:67]\n\t"
"v_cvt_pk_bf16_f32 v231, v234, v235\n\t"
"v_mfma_f32_16x16x32_bf16 a[72:75], a[220:223], a[184:187], a[72:75]\n\t"
"buffer_load_dwordx4 a[248:251], v252, s[8:11], s20 offen offset:2048 sc1\n\t"
"v_mfma_f32_16x16x32_bf16 a[72:75], v[96:99], a[188:191], a[72:75]\n\t"
"s_mov_b32 s20, 1056768\n\t"
"v_mfma_f32_16x16x32_bf16 a[72:75], a[224:227], a[192:195], a[72:75]\n\t"
"ds_write_b128 v243, v[228:231] offset:37376\n\t"
"v_mfma_f32_16x16x32_bf16 a[80:83], a[220:223], a[188:191], a[80:83]\n\t"
"buffer_load_dwordx4 a[252:255], v252, s[8:11], s20 offen offset:0 sc1\n\t"
"v_mfma_f32_16x16x32_bf16 a[80:83], v[96:99], a[192:195], a[80:83]\n\t"
"v_mfma_f32_16x16x32_bf16 a[80:83], a[224:227], a[196:199], a[80:83]\n\t"
"v_mfma_f32_16x16x32_bf16 a[88:91], a[220:223], a[192:195], a[88:91]\n\t"
"s_waitcnt lgkmcnt(3)\n\t"
"v_mfma_f32_16x16x32_bf16 a[88:91], v[96:99], a[196:199], a[88:91]\n\t"
"s_waitcnt lgkmcnt(1)\n\t"
"v_mfma_f32_16x16x32_bf16 a[88:91], a[224:227], a[200:203], a[88:91]\n\t"
"v_mfma_f32_16x16x32_bf16 v[224:227], v[224:227], a[208:211], 0\n\t"
"v_mfma_f32_16x16x32_bf16 v[236:239], v[236:239], a[208:211], 0\n\t"
"ds_read_b64_tr_b16 v[232:233], v246 offset:1024\n\t"
"v_mfma_f32_16x16x32_bf16 a[96:99], a[220:223], a[196:199], a[96:99]\n\t"
"buffer_load_dwordx4 a[220:223], v252, s[8:11], s20 offen offset:1024 sc1\n\t"
"v_mfma_f32_16x16x32_bf16 a[96:99], v[96:99], a[200:203], a[96:99]\n\t"
"s_waitcnt vmcnt(8)\n\t"
"ds_read_b64_tr_b16 v[234:235], v246 offset:4096\n\t"
"v_mfma_f32_16x16x32_bf16 a[96:99], a[224:227], a[204:207], a[96:99]\n\t"
"v_mfma_f32_16x16x32_bf16 a[104:107], a[228:231], a[168:171], a[104:107]\n\t"
"buffer_load_dwordx4 v[96:99], v252, s[8:11], s20 offen offset:2048 sc1\n\t"
"v_mfma_f32_16x16x32_bf16 a[104:107], v[248:251], a[172:175], a[104:107]\n\t"
"s_mov_b32 s20, 1290240\n\t"
"ds_read_b64_tr_b16 v[228:229], v246 offset:7168\n\t"
"v_cvt_pk_bf16_f32 v224, v224, v225\n\t"
"v_mfma_f32_16x16x32_bf16 a[104:107], a[232:235], a[176:179], a[104:107]\n\t"
"ds_read_b64_tr_b16 v[230:231], v246 offset:10240\n\t"
"v_cvt_pk_bf16_f32 v225, v226, v227\n\t"
"v_mfma_f32_16x16x32_bf16 a[112:115], a[228:231], a[172:175], a[112:115]\n\t"
"ds_read_b128 a[168:171], v244 offset:33792\n\t"
"v_cvt_pk_bf16_f32 v226, v236, v237\n\t"
"v_mfma_f32_16x16x32_bf16 a[112:115], v[248:251], a[176:179], a[112:115]\n\t"
"ds_read_b128 a[172:175], v244 offset:34304\n\t"
"v_cvt_pk_bf16_f32 v227, v238, v239\n\t"
"v_mfma_f32_16x16x32_bf16 a[112:115], a[232:235], a[180:183], a[112:115]\n\t"
"ds_write_b128 v243, v[224:227] offset:46592\n\t"
"v_mfma_f32_16x16x32_bf16 a[120:123], a[228:231], a[176:179], a[120:123]\n\t"
"buffer_load_dwordx4 a[224:227], v252, s[8:11], s20 offen offset:0 sc1\n\t"
"v_mfma_f32_16x16x32_bf16 a[120:123], v[248:251], a[180:183], a[120:123]\n\t"
"ds_read_b64_tr_b16 v[236:237], v246 offset:13312\n\t"
"v_mfma_f32_16x16x32_bf16 a[120:123], a[232:235], a[184:187], a[120:123]\n\t"
"ds_read_b64_tr_b16 v[238:239], v246 offset:16384\n\t"
"v_mfma_f32_16x16x32_bf16 a[128:131], a[228:231], a[180:183], a[128:131]\n\t"
"s_waitcnt lgkmcnt(7)\n\t"
"ds_read_b128 a[176:179], v244 offset:34816\n\t"
"v_mfma_f32_16x16x32_bf16 a[128:131], v[248:251], a[184:187], a[128:131]\n\t"
"ds_read_b64_tr_b16 v[224:225], v246 offset:20992\n\t"
"v_mfma_f32_16x16x32_bf16 a[128:131], a[232:235], a[188:191], a[128:131]\n\t"
"ds_read_b64_tr_b16 v[226:227], v246 offset:24064\n\t"
"v_mfma_f32_16x16x32_bf16 v[232:235], v[232:235], v[220:223], 0\n\t"
"ds_read_b128 a[180:183], v244 offset:35328\n\t"
"v_mfma_f32_16x16x32_bf16 a[136:139], a[228:231], a[184:187], a[136:139]\n\t"
"s_waitcnt lgkmcnt(9)\n\t"
"ds_read_b128 a[184:187], v244 offset:35840\n\t"
"v_mfma_f32_16x16x32_bf16 a[136:139], v[248:251], a[188:191], a[136:139]\n\t"
"v_mfma_f32_16x16x32_bf16 a[136:139], a[232:235], a[192:195], a[136:139]\n\t"
"v_mfma_f32_16x16x32_bf16 v[228:231], v[228:231], v[220:223], 0\n\t"
"v_mfma_f32_16x16x32_bf16 a[144:147], a[228:231], a[188:191], a[144:147]\n\t"
"s_waitcnt lgkmcnt(5)\n\t"
"ds_read_b128 a[188:191], v244 offset:36352\n\t"
"v_cvt_pk_bf16_f32 v232, v232, v233\n\t"
"v_mfma_f32_16x16x32_bf16 a[144:147], v[248:251], a[192:195], a[144:147]\n\t"
"v_cvt_pk_bf16_f32 v233, v234, v235\n\t"
"v_mfma_f32_16x16x32_bf16 a[144:147], a[232:235], a[196:199], a[144:147]\n\t"
"v_mfma_f32_16x16x32_bf16 v[236:239], v[236:239], v[220:223], 0\n\t"
"v_mfma_f32_16x16x32_bf16 a[152:155], a[228:231], a[192:195], a[152:155]\n\t"
"s_waitcnt lgkmcnt(3)\n\t"
"ds_read_b128 a[192:195], v244 offset:36864\n\t"
"v_cvt_pk_bf16_f32 v234, v228, v229\n\t"
"v_mfma_f32_16x16x32_bf16 a[152:155], v[248:251], a[196:199], a[152:155]\n\t"
"v_cvt_pk_bf16_f32 v235, v230, v231\n\t"
"v_mfma_f32_16x16x32_bf16 a[152:155], a[232:235], a[200:203], a[152:155]\n\t"
"ds_read_b64_tr_b16 v[228:229], v246 offset:27136\n\t"
"v_mfma_f32_16x16x32_bf16 v[224:227], v[224:227], v[220:223], 0\n\t"
"ds_read_b64_tr_b16 v[230:231], v246 offset:30208\n\t"
"v_mfma_f32_16x16x32_bf16 a[160:163], a[228:231], a[196:199], a[160:163]\n\t"
"buffer_load_dwordx4 a[228:231], v252, s[8:11], s20 offen offset:1024 sc1\n\t"
"v_mfma_f32_16x16x32_bf16 a[160:163], v[248:251], a[200:203], a[160:163]\n\t"
"s_waitcnt vmcnt(8)\n\t"
"ds_read_b128 a[196:199], v244 offset:37376\n\t"
"v_cvt_pk_bf16_f32 v236, v236, v237\n\t"
"v_mfma_f32_16x16x32_bf16 a[160:163], a[232:235], a[204:207], a[160:163]\n\t"
"ds_read_b128 a[200:203], v244 offset:37888\n\t"
"v_cvt_pk_bf16_f32 v237, v238, v239\n\t"
"v_mfma_f32_16x16x32_bf16 v[200:203], a[236:239], a[168:171], v[200:203]\n\t"
"ds_read_b128 a[204:207], v244 offset:38400\n\t"
"v_mfma_f32_16x16x32_bf16 v[200:203], a[212:215], a[172:175], v[200:203]\n\t"
"buffer_load_dwordx4 v[248:251], v252, s[8:11], s20 offen offset:2048 sc1\n\t"
"v_mfma_f32_16x16x32_bf16 v[200:203], a[216:219], a[176:179], v[200:203]\n\t"
"s_waitcnt lgkmcnt(3)\n\t"
"s_mov_b32 s20, 1302528\n\t"
"v_cvt_pk_bf16_f32 v238, v224, v225\n\t"
"v_mfma_f32_16x16x32_bf16 v[228:231], v[228:231], v[220:223], 0\n\t"
"v_cvt_pk_bf16_f32 v239, v226, v227\n\t"
"v_mfma_f32_16x16x32_bf16 v[208:211], a[236:239], a[172:175], v[208:211]\n\t"
"buffer_load_dwordx4 a[232:235], v252, s[8:11], s20 offen offset:0 sc1\n\t"
"v_mfma_f32_16x16x32_bf16 v[208:211], a[212:215], a[176:179], v[208:211]\n\t"
"v_mfma_f32_16x16x32_bf16 v[208:211], a[216:219], a[180:183], v[208:211]\n\t"
"s_waitcnt lgkmcnt(0)\n\t"
"s_barrier\n\t"
"v_mfma_f32_16x16x32_bf16 v[216:219], a[236:239], a[176:179], v[216:219]\n\t"
"ds_read_b64_tr_b16 v[224:225], v246 offset:33280\n\t"
"v_mfma_f32_16x16x32_bf16 v[216:219], a[212:215], a[180:183], v[216:219]\n\t"
"ds_read_b64_tr_b16 v[226:227], v246 offset:36352\n\t"
"v_cvt_pk_bf16_f32 v228, v228, v229\n\t"
"v_mfma_f32_16x16x32_bf16 v[216:219], a[216:219], a[184:187], v[216:219]\n\t"
"ds_write2st64_b64 v245, v[232:233], v[234:235] offset0:48 offset1:49\n\t"
"v_mfma_f32_16x16x32_bf16 a[4:7], a[236:239], a[180:183], a[4:7]\n\t"
"v_cvt_pk_bf16_f32 v229, v230, v231\n\t"
"v_mfma_f32_16x16x32_bf16 a[4:7], a[212:215], a[184:187], a[4:7]\n\t"
"ds_read_b64_tr_b16 v[232:233], v246 offset:40960\n\t"
"v_mfma_f32_16x16x32_bf16 a[4:7], a[216:219], a[188:191], a[4:7]\n\t"
"ds_write2st64_b64 v245, v[236:237], v[238:239] offset0:50 offset1:51\n\t"
"v_mfma_f32_16x16x32_bf16 a[12:15], a[236:239], a[184:187], a[12:15]\n\t"
"v_mfma_f32_16x16x32_bf16 a[12:15], a[212:215], a[188:191], a[12:15]\n\t"
"ds_read_b64_tr_b16 v[234:235], v246 offset:44032\n\t"
"v_mfma_f32_16x16x32_bf16 a[12:15], a[216:219], a[192:195], a[12:15]\n\t"
"ds_write_b64 v245, v[228:229] offset:26624\n\t"
"v_mfma_f32_16x16x32_bf16 a[20:23], a[236:239], a[188:191], a[20:23]\n\t"
"s_waitcnt lgkmcnt(5)\n\t"
"ds_read_b64_tr_b16 v[228:229], v246 offset:47104\n\t"
"v_mfma_f32_16x16x32_bf16 a[20:23], a[212:215], a[192:195], a[20:23]\n\t"
"ds_read_b64_tr_b16 v[230:231], v246 offset:50176\n\t"
"v_mfma_f32_16x16x32_bf16 a[20:23], a[216:219], a[196:199], a[20:23]\n\t"
"v_mfma_f32_16x16x32_bf16 v[224:227], v[224:227], v[220:223], 0\n\t"
"v_mfma_f32_16x16x32_bf16 a[28:31], a[236:239], a[192:195], a[28:31]\n\t"
"s_waitcnt lgkmcnt(3)\n\t"
"v_mfma_f32_16x16x32_bf16 a[28:31], a[212:215], a[196:199], a[28:31]\n\t"
"v_mfma_f32_16x16x32_bf16 a[28:31], a[216:219], a[200:203], a[28:31]\n\t"
"v_mfma_f32_16x16x32_bf16 v[232:235], v[232:235], v[220:223], 0\n\t"
"v_mfma_f32_16x16x32_bf16 a[36:39], a[236:239], a[196:199], a[36:39]\n\t"
"buffer_load_dwordx4 a[236:239], v252, s[8:11], s20 offen offset:1024 sc1\n\t"
"v_mfma_f32_16x16x32_bf16 a[36:39], a[212:215], a[200:203], a[36:39]\n\t"
"s_waitcnt vmcnt(8) lgkmcnt(0)\n\t"
"v_cvt_pk_bf16_f32 v224, v224, v225\n\t"
"v_cvt_pk_bf16_f32 v225, v226, v227\n\t"
"v_mfma_f32_16x16x32_bf16 a[36:39], a[216:219], a[204:207], a[36:39]\n\t"
"v_mfma_f32_16x16x32_bf16 v[228:231], v[228:231], v[220:223], 0\n\t"
"buffer_load_dwordx4 a[212:215], v252, s[8:11], s20 offen offset:2048 sc1\n\t"
"v_mfma_f32_16x16x32_bf16 a[44:47], a[240:243], a[168:171], a[44:47]\n\t"
"s_mov_b32 s20, 1314816\n\t"
"v_cvt_pk_bf16_f32 v226, v232, v233\n\t"
"v_cvt_pk_bf16_f32 v227, v234, v235\n\t"
"v_mfma_f32_16x16x32_bf16 a[44:47], a[244:247], a[172:175], a[44:47]\n\t"
"ds_read_b64_tr_b16 v[232:233], v246 offset:53248\n\t"
"v_mfma_f32_16x16x32_bf16 a[44:47], a[248:251], a[176:179], a[44:47]\n\t"
"buffer_load_dwordx4 a[216:219], v252, s[8:11], s20 offen offset:0 sc1\n\t"
"v_mfma_f32_16x16x32_bf16 a[52:55], a[240:243], a[172:175], a[52:55]\n\t"
"ds_read_b64_tr_b16 v[234:235], v246 offset:56320\n\t"
"v_mfma_f32_16x16x32_bf16 a[52:55], a[244:247], a[176:179], a[52:55]\n\t"
"v_cvt_pk_bf16_f32 v228, v228, v229\n\t"
"v_cvt_pk_bf16_f32 v229, v230, v231\n\t"
"v_mfma_f32_16x16x32_bf16 a[52:55], a[248:251], a[180:183], a[52:55]\n\t"
"ds_write2st64_b64 v245, v[224:225], v[226:227] offset0:53 offset1:54\n\t"
"v_mfma_f32_16x16x32_bf16 a[60:63], a[240:243], a[176:179], a[60:63]\n\t"
"v_mfma_f32_16x16x32_bf16 a[60:63], a[244:247], a[180:183], a[60:63]\n\t"
"v_add_u32 v243, 55296, v243\n\t"
"v_mfma_f32_16x16x32_bf16 a[60:63], a[248:251], a[184:187], a[60:63]\n\t"
"v_mfma_f32_16x16x32_bf16 a[68:71], a[240:243], a[180:183], a[68:71]\n\t"
"v_mfma_f32_16x16x32_bf16 a[68:71], a[244:247], a[184:187], a[68:71]\n\t"
"v_mfma_f32_16x16x32_bf16 a[68:71], a[248:251], a[188:191], a[68:71]\n\t"
"v_mfma_f32_16x16x32_bf16 a[76:79], a[240:243], a[184:187], a[76:79]\n\t"
"s_waitcnt lgkmcnt(1)\n\t"
"v_mfma_f32_16x16x32_bf16 a[76:79], a[244:247], a[188:191], a[76:79]\n\t"
"v_mfma_f32_16x16x32_bf16 a[76:79], a[248:251], a[192:195], a[76:79]\n\t"
"v_mfma_f32_16x16x32_bf16 v[232:235], v[232:235], v[220:223], 0\n\t"
"v_mfma_f32_16x16x32_bf16 a[84:87], a[240:243], a[188:191], a[84:87]\n\t"
"v_mfma_f32_16x16x32_bf16 a[84:87], a[244:247], a[192:195], a[84:87]\n\t"
"v_mfma_f32_16x16x32_bf16 a[84:87], a[248:251], a[196:199], a[84:87]\n\t"
"v_mfma_f32_16x16x32_bf16 a[92:95], a[240:243], a[192:195], a[92:95]\n\t"
"v_mfma_f32_16x16x32_bf16 a[92:95], a[244:247], a[196:199], a[92:95]\n\t"
"v_cvt_pk_bf16_f32 v230, v232, v233\n\t"
"v_cvt_pk_bf16_f32 v231, v234, v235\n\t"
"v_mfma_f32_16x16x32_bf16 a[92:95], a[248:251], a[200:203], a[92:95]\n\t"
"ds_write2st64_b64 v245, v[228:229], v[230:231] offset0:55 offset1:56\n\t"
"v_mfma_f32_16x16x32_bf16 a[100:103], a[240:243], a[196:199], a[100:103]\n\t"
"buffer_load_dwordx4 a[240:243], v252, s[8:11], s20 offen offset:1024 sc1\n\t"
"v_mfma_f32_16x16x32_bf16 a[100:103], a[244:247], a[200:203], a[100:103]\n\t"
"s_waitcnt vmcnt(8)\n\t"
"v_mfma_f32_16x16x32_bf16 a[100:103], a[248:251], a[204:207], a[100:103]\n\t"
"v_mfma_f32_16x16x32_bf16 a[108:111], a[252:255], a[168:171], a[108:111]\n\t"
"buffer_load_dwordx4 a[244:247], v252, s[8:11], s20 offen offset:2048 sc1\n\t"
"v_mfma_f32_16x16x32_bf16 a[108:111], a[220:223], a[172:175], a[108:111]\n\t"
"s_mov_b32 s20, 1327104\n\t"
"v_mfma_f32_16x16x32_bf16 a[108:111], v[96:99], a[176:179], a[108:111]\n\t"
"s_waitcnt lgkmcnt(0)\n\t"
"s_barrier\n\t"
"v_mfma_f32_16x16x32_bf16 a[116:119], a[252:255], a[172:175], a[116:119]\n\t"
"ds_read_b64_tr_b16 v[236:237], v240 offset:1024\n\t"
"v_mfma_f32_16x16x32_bf16 a[116:119], a[220:223], a[176:179], a[116:119]\n\t"
"ds_read_b64_tr_b16 v[238:239], v240 offset:2560\n\t"
"v_mfma_f32_16x16x32_bf16 a[116:119], v[96:99], a[180:183], a[116:119]\n\t"
"ds_read_b64_tr_b16 v[224:225], v241 offset:1024\n\t"
"v_mfma_f32_16x16x32_bf16 a[124:127], a[252:255], a[176:179], a[124:127]\n\t"
"ds_read_b64_tr_b16 v[226:227], v241 offset:2560\n\t"
"v_mfma_f32_16x16x32_bf16 a[124:127], a[220:223], a[180:183], a[124:127]\n\t"
"buffer_load_dwordx4 a[248:251], v252, s[8:11], s20 offen offset:0 sc1\n\t"
"v_mfma_f32_16x16x32_bf16 a[124:127], v[96:99], a[184:187], a[124:127]\n\t"
"ds_read_b64_tr_b16 v[232:233], v240 offset:20992\n\t"
"v_mfma_f32_16x16x32_bf16 a[132:135], a[252:255], a[180:183], a[132:135]\n\t"
"s_waitcnt lgkmcnt(3)\n\t"
"ds_read_b64_tr_b16 v[234:235], v240 offset:22528\n\t"
"v_mfma_f32_16x16x32_bf16 a[132:135], a[220:223], a[184:187], a[132:135]\n\t"
"ds_read_b64_tr_b16 v[228:229], v241 offset:20992\n\t"
"v_mfma_f32_16x16x32_bf16 a[132:135], v[96:99], a[188:191], a[132:135]\n\t"
"s_waitcnt lgkmcnt(3)\n\t"
"ds_read_b64_tr_b16 v[230:231], v241 offset:22528\n\t"
"v_mfma_f32_16x16x32_bf16 v[236:239], v[236:239], a[208:211], 0\n\t"
"ds_read_b128 a[168:171], v244 offset:24576\n\t"
"v_mfma_f32_16x16x32_bf16 v[224:227], v[224:227], a[208:211], 0\n\t"
"ds_read_b128 a[172:175], v244 offset:25088\n\t"
"v_mfma_f32_16x16x32_bf16 a[140:143], a[252:255], a[184:187], a[140:143]\n\t"
"ds_read_b128 a[176:179], v244 offset:25600\n\t"
"v_mfma_f32_16x16x32_bf16 a[140:143], a[220:223], a[188:191], a[140:143]\n\t"
"ds_read_b128 a[180:183], v244 offset:26112\n\t"
"v_mfma_f32_16x16x32_bf16 a[140:143], v[96:99], a[192:195], a[140:143]\n\t"
"ds_read_b128 a[184:187], v244 offset:26624\n\t"
"v_mfma_f32_16x16x32_bf16 a[148:151], a[252:255], a[188:191], a[148:151]\n\t"
"s_waitcnt lgkmcnt(7)\n\t"
"ds_read_b128 a[188:191], v244 offset:27136\n\t"
"v_cvt_pk_bf16_f32 v236, v236, v237\n\t"
"v_mfma_f32_16x16x32_bf16 a[148:151], a[220:223], a[192:195], a[148:151]\n\t"
"s_waitcnt lgkmcnt(6)\n\t"
"v_cvt_pk_bf16_f32 v237, v238, v239\n\t"
"v_cvt_pk_bf16_f32 v238, v224, v225\n\t"
"v_mfma_f32_16x16x32_bf16 a[148:151], v[96:99], a[196:199], a[148:151]\n\t"
"v_cvt_pk_bf16_f32 v239, v226, v227\n\t"
"v_mfma_f32_16x16x32_bf16 v[232:235], v[232:235], a[208:211], 0\n\t"
"ds_write_b128 v243, v[236:239] offset:28160\n\t"
"v_mfma_f32_16x16x32_bf16 v[228:231], v[228:231], a[208:211], 0\n\t"
"v_mfma_f32_16x16x32_bf16 a[156:159], a[252:255], a[192:195], a[156:159]\n\t"
"ds_read_b128 a[192:195], v244 offset:27648\n\t"
"v_mfma_f32_16x16x32_bf16 a[156:159], a[220:223], a[196:199], a[156:159]\n\t"
"ds_read_b64_tr_b16 v[224:225], v240 offset:40960\n\t"
"v_mfma_f32_16x16x32_bf16 a[156:159], v[96:99], a[200:203], a[156:159]\n\t"
"ds_read_b64_tr_b16 v[226:227], v240 offset:42496\n\t"
"v_mfma_f32_16x16x32_bf16 a[164:167], a[252:255], a[196:199], a[164:167]\n\t"
"buffer_load_dwordx4 a[252:255], v252, s[8:11], s20 offen offset:1024 sc1\n\t"
"v_mfma_f32_16x16x32_bf16 a[164:167], a[220:223], a[200:203], a[164:167]\n\t"
"s_waitcnt vmcnt(8) lgkmcnt(7)\n\t"
"ds_read_b64_tr_b16 v[236:237], v241 offset:40960\n\t"
"v_cvt_pk_bf16_f32 v232, v232, v233\n\t"
"v_mfma_f32_16x16x32_bf16 a[164:167], v[96:99], a[204:207], a[164:167]\n\t"
"ds_read_b64_tr_b16 v[238:239], v241 offset:42496\n\t"
"v_cvt_pk_bf16_f32 v233, v234, v235\n\t"
"Lst28_%=:\n\t"
"v_mfma_f32_16x16x32_bf16 v[196:199], a[224:227], a[168:171], v[196:199]\n\t"
"s_waitcnt lgkmcnt(8)\n\t"
"ds_read_b128 a[196:199], v244 offset:28160\n\t"
"v_cvt_pk_bf16_f32 v234, v228, v229\n\t"
"v_mfma_f32_16x16x32_bf16 v[196:199], a[228:231], a[172:175], v[196:199]\n\t"
"ds_read_b128 a[200:203], v244 offset:28672\n\t"
"v_cvt_pk_bf16_f32 v235, v230, v231\n\t"
"v_mfma_f32_16x16x32_bf16 v[196:199], v[248:251], a[176:179], v[196:199]\n\t"
"ds_write_b128 v243, v[232:235] offset:37376\n\t"
"v_mfma_f32_16x16x32_bf16 v[204:207], a[224:227], a[172:175], v[204:207]\n\t"
"buffer_load_dwordx4 v[96:99], v252, s[8:11], s20 offen offset:2048 sc1\n\t"
"v_mfma_f32_16x16x32_bf16 v[204:207], a[228:231], a[176:179], v[204:207]\n\t"
"s_waitcnt lgkmcnt(10)\n\t"
"ds_read_b128 a[204:207], v244 offset:29184\n\t"
"v_mfma_f32_16x16x32_bf16 v[204:207], v[248:251], a[180:183], v[204:207]\n\t"
"s_mov_b32 s20, 1339392\n\t"
"v_subrev_u32 v240, 59904, v240\n\t"
"v_subrev_u32 v241, 59904, v241\n\t"
"v_mfma_f32_16x16x32_bf16 v[212:215], a[224:227], a[176:179], v[212:215]\n\t"
"s_waitcnt lgkmcnt(6)\n\t"
"ds_read_b64_tr_b16 v[228:229], v240 offset:1024\n\t"
"v_mfma_f32_16x16x32_bf16 v[212:215], a[228:231], a[180:183], v[212:215]\n\t"
"buffer_load_dwordx4 a[220:223], v252, s[8:11], s20 offen offset:0 sc1\n\t"
"v_mfma_f32_16x16x32_bf16 v[212:215], v[248:251], a[184:187], v[212:215]\n\t"
"s_waitcnt lgkmcnt(5)\n\t"
"ds_read_b64_tr_b16 v[230:231], v240 offset:2560\n\t"
"v_mfma_f32_16x16x32_bf16 v[224:227], v[224:227], a[208:211], 0\n\t"
"ds_read_b64_tr_b16 v[232:233], v241 offset:1024\n\t"
"v_mfma_f32_16x16x32_bf16 v[236:239], v[236:239], a[208:211], 0\n\t"
"ds_read_b64_tr_b16 v[234:235], v241 offset:2560\n\t"
"v_mfma_f32_16x16x32_bf16 a[0:3], a[224:227], a[180:183], a[0:3]\n\t"
"v_mfma_f32_16x16x32_bf16 a[0:3], a[228:231], a[184:187], a[0:3]\n\t"
"v_mfma_f32_16x16x32_bf16 a[0:3], v[248:251], a[188:191], a[0:3]\n\t"
"v_mfma_f32_16x16x32_bf16 a[8:11], a[224:227], a[184:187], a[8:11]\n\t"
"s_waitcnt lgkmcnt(2)\n\t"
"v_cvt_pk_bf16_f32 v224, v224, v225\n\t"
"v_cvt_pk_bf16_f32 v225, v226, v227\n\t"
"v_mfma_f32_16x16x32_bf16 a[8:11], a[228:231], a[188:191], a[8:11]\n\t"
"s_waitcnt lgkmcnt(0)\n\t"
"v_cvt_pk_bf16_f32 v226, v236, v237\n\t"
"v_cvt_pk_bf16_f32 v227, v238, v239\n\t"
"v_mfma_f32_16x16x32_bf16 a[8:11], v[248:251], a[192:195], a[8:11]\n\t"
"ds_write_b128 v243, v[224:227] offset:46592\n\t"
"v_mfma_f32_16x16x32_bf16 v[228:231], v[228:231], a[208:211], 0\n\t"
"v_mfma_f32_16x16x32_bf16 v[232:235], v[232:235], a[208:211], 0\n\t"
"ds_read_b64_tr_b16 v[236:237], v240 offset:20992\n\t"
"v_mfma_f32_16x16x32_bf16 a[16:19], a[224:227], a[188:191], a[16:19]\n\t"
"ds_read_b64_tr_b16 v[238:239], v240 offset:22528\n\t"
"v_mfma_f32_16x16x32_bf16 a[16:19], a[228:231], a[192:195], a[16:19]\n\t"
"ds_read_b64_tr_b16 v[224:225], v241 offset:20992\n\t"
"v_mfma_f32_16x16x32_bf16 a[16:19], v[248:251], a[196:199], a[16:19]\n\t"
"ds_read_b64_tr_b16 v[226:227], v241 offset:22528\n\t"
"v_mfma_f32_16x16x32_bf16 a[24:27], a[224:227], a[192:195], a[24:27]\n\t"
"v_cvt_pk_bf16_f32 v228, v228, v229\n\t"
"v_cvt_pk_bf16_f32 v229, v230, v231\n\t"
"v_mfma_f32_16x16x32_bf16 a[24:27], a[228:231], a[196:199], a[24:27]\n\t"
"v_cvt_pk_bf16_f32 v230, v232, v233\n\t"
"v_cvt_pk_bf16_f32 v231, v234, v235\n\t"
"v_mfma_f32_16x16x32_bf16 a[24:27], v[248:251], a[200:203], a[24:27]\n\t"
"ds_write_b128 v243, v[228:231] offset:512\n\t"
"v_mfma_f32_16x16x32_bf16 a[32:35], a[224:227], a[196:199], a[32:35]\n\t"
"buffer_load_dwordx4 a[224:227], v252, s[8:11], s20 offen offset:1024 sc1\n\t"
"v_mfma_f32_16x16x32_bf16 a[32:35], a[228:231], a[200:203], a[32:35]\n\t"
"s_waitcnt vmcnt(8)\n\t"
"ds_read_b64_tr_b16 v[232:233], v240 offset:40960\n\t"
"v_mfma_f32_16x16x32_bf16 a[32:35], v[248:251], a[204:207], a[32:35]\n\t"
"ds_read_b64_tr_b16 v[234:235], v240 offset:42496\n\t"
"v_mfma_f32_16x16x32_bf16 a[40:43], a[232:235], a[168:171], a[40:43]\n\t"
"buffer_load_dwordx4 v[248:251], v252, s[8:11], s20 offen offset:2048 sc1\n\t"
"v_mfma_f32_16x16x32_bf16 a[40:43], a[236:239], a[172:175], a[40:43]\n\t"
"s_mov_b32 s20, 1351680\n\t"
"ds_read_b64_tr_b16 v[228:229], v241 offset:40960\n\t"
"v_mfma_f32_16x16x32_bf16 a[40:43], a[212:215], a[176:179], a[40:43]\n\t"
"ds_read_b64_tr_b16 v[230:231], v241 offset:42496\n\t"
"v_mfma_f32_16x16x32_bf16 a[48:51], a[232:235], a[172:175], a[48:51]\n\t"
"buffer_load_dwordx4 a[228:231], v252, s[8:11], s20 offen offset:0 sc1\n\t"
"v_mfma_f32_16x16x32_bf16 a[48:51], a[236:239], a[176:179], a[48:51]\n\t"
"s_waitcnt lgkmcnt(5)\n\t"
"v_mfma_f32_16x16x32_bf16 a[48:51], a[212:215], a[180:183], a[48:51]\n\t"
"v_mfma_f32_16x16x32_bf16 v[236:239], v[236:239], a[208:211], 0\n\t"
"v_mfma_f32_16x16x32_bf16 v[224:227], v[224:227], a[208:211], 0\n\t"
"v_xor_b32 v246, 0xc0, v246\n\t"
"v_mfma_f32_16x16x32_bf16 a[56:59], a[232:235], a[176:179], a[56:59]\n\t"
"v_mfma_f32_16x16x32_bf16 a[56:59], a[236:239], a[180:183], a[56:59]\n\t"
"v_mfma_f32_16x16x32_bf16 a[56:59], a[212:215], a[184:187], a[56:59]\n\t"
"v_mfma_f32_16x16x32_bf16 a[64:67], a[232:235], a[180:183], a[64:67]\n\t"
"s_waitcnt lgkmcnt(2)\n\t"
"v_cvt_pk_bf16_f32 v236, v236, v237\n\t"
"v_cvt_pk_bf16_f32 v237, v238, v239\n\t"
"v_mfma_f32_16x16x32_bf16 a[64:67], a[236:239], a[184:187], a[64:67]\n\t"
"s_waitcnt lgkmcnt(0)\n\t"
"v_cvt_pk_bf16_f32 v238, v224, v225\n\t"
"v_cvt_pk_bf16_f32 v239, v226, v227\n\t"
"v_mfma_f32_16x16x32_bf16 a[64:67], a[212:215], a[188:191], a[64:67]\n\t"
"ds_write_b128 v243, v[236:239] offset:9728\n\t"
"v_mfma_f32_16x16x32_bf16 v[232:235], v[232:235], a[208:211], 0\n\t"
"v_mfma_f32_16x16x32_bf16 v[228:231], v[228:231], a[208:211], 0\n\t"
"v_mfma_f32_16x16x32_bf16 a[72:75], a[232:235], a[184:187], a[72:75]\n\t"
"v_mfma_f32_16x16x32_bf16 a[72:75], a[236:239], a[188:191], a[72:75]\n\t"
"v_mfma_f32_16x16x32_bf16 a[72:75], a[212:215], a[192:195], a[72:75]\n\t"
"v_mfma_f32_16x16x32_bf16 a[80:83], a[232:235], a[188:191], a[80:83]\n\t"
"v_cvt_pk_bf16_f32 v232, v232, v233\n\t"
"v_cvt_pk_bf16_f32 v233, v234, v235\n\t"
"v_mfma_f32_16x16x32_bf16 a[80:83], a[236:239], a[192:195], a[80:83]\n\t"
"v_cvt_pk_bf16_f32 v234, v228, v229\n\t"
"v_cvt_pk_bf16_f32 v235, v230, v231\n\t"
"v_mfma_f32_16x16x32_bf16 a[80:83], a[212:215], a[196:199], a[80:83]\n\t"
"ds_write_b128 v243, v[232:235] offset:18944\n\t"
"v_mfma_f32_16x16x32_bf16 a[88:91], a[232:235], a[192:195], a[88:91]\n\t"
"v_mfma_f32_16x16x32_bf16 a[88:91], a[236:239], a[196:199], a[88:91]\n\t"
"ds_read_b64_tr_b16 v[224:225], v246 offset:1024\n\t"
"v_mfma_f32_16x16x32_bf16 a[88:91], a[212:215], a[200:203], a[88:91]\n\t"
"ds_read_b64_tr_b16 v[226:227], v246 offset:4096\n\t"
"v_mfma_f32_16x16x32_bf16 a[96:99], a[232:235], a[196:199], a[96:99]\n\t"
"buffer_load_dwordx4 a[232:235], v252, s[8:11], s20 offen offset:1024 sc1\n\t"
"v_mfma_f32_16x16x32_bf16 a[96:99], a[236:239], a[200:203], a[96:99]\n\t"
"s_waitcnt vmcnt(8)\n\t"
"ds_read_b64_tr_b16 v[236:237], v246 offset:7168\n\t"
"v_mfma_f32_16x16x32_bf16 a[96:99], a[212:215], a[204:207], a[96:99]\n\t"
"ds_read_b64_tr_b16 v[238:239], v246 offset:10240\n\t"
"v_mfma_f32_16x16x32_bf16 a[104:107], a[216:219], a[168:171], a[104:107]\n\t"
"buffer_load_dwordx4 a[212:215], v252, s[8:11], s20 offen offset:2048 sc1\n\t"
"v_mfma_f32_16x16x32_bf16 a[104:107], a[240:243], a[172:175], a[104:107]\n\t"
"s_mov_b32 s20, 1216512\n\t"
"ds_read_b64_tr_b16 v[228:229], v246 offset:13312\n\t"
"v_mfma_f32_16x16x32_bf16 a[104:107], a[244:247], a[176:179], a[104:107]\n\t"
"ds_read_b64_tr_b16 v[230:231], v246 offset:16384\n\t"
"v_mfma_f32_16x16x32_bf16 a[112:115], a[216:219], a[172:175], a[112:115]\n\t"
"buffer_load_dwordx4 a[236:239], v252, s[8:11], s20 offen offset:0 sc1\n\t"
"v_mfma_f32_16x16x32_bf16 a[112:115], a[240:243], a[176:179], a[112:115]\n\t"
"ds_read_b128 a[168:171], v244 offset:33792\n\t"
"v_mfma_f32_16x16x32_bf16 a[112:115], a[244:247], a[180:183], a[112:115]\n\t"
"ds_read_b64_tr_b16 v[232:233], v246 offset:20992\n\t"
"v_mfma_f32_16x16x32_bf16 a[120:123], a[216:219], a[176:179], a[120:123]\n\t"
"s_waitcnt lgkmcnt(6)\n\t"
"ds_read_b64_tr_b16 v[234:235], v246 offset:24064\n\t"
"v_mfma_f32_16x16x32_bf16 a[120:123], a[240:243], a[180:183], a[120:123]\n\t"
"ds_read_b128 a[172:175], v244 offset:34304\n\t"
"v_mfma_f32_16x16x32_bf16 a[120:123], a[244:247], a[184:187], a[120:123]\n\t"
"ds_read_b128 a[176:179], v244 offset:34816\n\t"
"v_mfma_f32_16x16x32_bf16 v[224:227], v[224:227], v[220:223], 0\n\t"
"v_mfma_f32_16x16x32_bf16 a[128:131], a[216:219], a[180:183], a[128:131]\n\t"
"s_waitcnt lgkmcnt(7)\n\t"
"ds_read_b128 a[180:183], v244 offset:35328\n\t"
"v_mfma_f32_16x16x32_bf16 a[128:131], a[240:243], a[184:187], a[128:131]\n\t"
"v_mfma_f32_16x16x32_bf16 a[128:131], a[244:247], a[188:191], a[128:131]\n\t"
"v_mfma_f32_16x16x32_bf16 v[236:239], v[236:239], v[220:223], 0\n\t"
"v_mfma_f32_16x16x32_bf16 a[136:139], a[216:219], a[184:187], a[136:139]\n\t"
"s_waitcnt lgkmcnt(6)\n\t"
"ds_read_b128 a[184:187], v244 offset:35840\n\t"
"v_cvt_pk_bf16_f32 v224, v224, v225\n\t"
"v_mfma_f32_16x16x32_bf16 a[136:139], a[240:243], a[188:191], a[136:139]\n\t"
"v_cvt_pk_bf16_f32 v225, v226, v227\n\t"
"v_mfma_f32_16x16x32_bf16 a[136:139], a[244:247], a[192:195], a[136:139]\n\t"
"v_mfma_f32_16x16x32_bf16 v[228:231], v[228:231], v[220:223], 0\n\t"
"v_mfma_f32_16x16x32_bf16 a[144:147], a[216:219], a[188:191], a[144:147]\n\t"
"s_waitcnt lgkmcnt(4)\n\t"
"ds_read_b128 a[188:191], v244 offset:36352\n\t"
"v_cvt_pk_bf16_f32 v226, v236, v237\n\t"
"v_mfma_f32_16x16x32_bf16 a[144:147], a[240:243], a[192:195], a[144:147]\n\t"
"v_cvt_pk_bf16_f32 v227, v238, v239\n\t"
"v_mfma_f32_16x16x32_bf16 a[144:147], a[244:247], a[196:199], a[144:147]\n\t"
"v_mfma_f32_16x16x32_bf16 v[232:235], v[232:235], v[220:223], 0\n\t"
"v_mfma_f32_16x16x32_bf16 a[152:155], a[216:219], a[192:195], a[152:155]\n\t"
"ds_read_b128 a[192:195], v244 offset:36864\n\t"
"v_cvt_pk_bf16_f32 v228, v228, v229\n\t"
"v_mfma_f32_16x16x32_bf16 a[152:155], a[240:243], a[196:199], a[152:155]\n\t"
"v_cvt_pk_bf16_f32 v229, v230, v231\n\t"
"v_mfma_f32_16x16x32_bf16 a[152:155], a[244:247], a[200:203], a[152:155]\n\t"
"v_mfma_f32_16x16x32_bf16 a[160:163], a[216:219], a[196:199], a[160:163]\n\t"
"buffer_load_dwordx4 a[216:219], v252, s[8:11], s20 offen offset:1024 sc1\n\t"
"v_mfma_f32_16x16x32_bf16 a[160:163], a[240:243], a[200:203], a[160:163]\n\t"
"s_waitcnt vmcnt(8) lgkmcnt(4)\n\t"
"ds_read_b128 a[196:199], v244 offset:37376\n\t"
"v_cvt_pk_bf16_f32 v230, v232, v233\n\t"
"v_mfma_f32_16x16x32_bf16 a[160:163], a[244:247], a[204:207], a[160:163]\n\t"
"ds_read_b128 a[200:203], v244 offset:37888\n\t"
"v_cvt_pk_bf16_f32 v231, v234, v235\n\t"
"v_mfma_f32_16x16x32_bf16 v[200:203], a[248:251], a[168:171], v[200:203]\n\t"
"ds_read_b64_tr_b16 v[232:233], v246 offset:27136\n\t"
"v_mfma_f32_16x16x32_bf16 v[200:203], a[252:255], a[172:175], v[200:203]\n\t"
"s_waitcnt lgkmcnt(6)\n\t"
"ds_read_b64_tr_b16 v[234:235], v246 offset:30208\n\t"
"v_mfma_f32_16x16x32_bf16 v[200:203], v[96:99], a[176:179], v[200:203]\n\t"
"s_waitcnt lgkmcnt(0)\n\t"
"ds_read_b128 a[204:207], v244 offset:38400\n\t"
"v_mfma_f32_16x16x32_bf16 v[232:235], v[232:235], v[220:223], 0\n\t"
"buffer_load_dwordx4 a[240:243], v252, s[8:11], s20 offen offset:2048 sc1\n\t"
"s_mov_b32 s20, 1228800\n\t"
"v_mfma_f32_16x16x32_bf16 v[208:211], a[248:251], a[172:175], v[208:211]\n\t"
"buffer_load_dwordx4 a[244:247], v252, s[8:11], s20 offen offset:0 sc1\n\t"
"v_mfma_f32_16x16x32_bf16 v[208:211], a[252:255], a[176:179], v[208:211]\n\t"
"v_mfma_f32_16x16x32_bf16 v[208:211], v[96:99], a[180:183], v[208:211]\n\t"
"s_waitcnt lgkmcnt(0)\n\t"
"s_barrier\n\t"
"v_mfma_f32_16x16x32_bf16 v[216:219], a[248:251], a[176:179], v[216:219]\n\t"
"v_mfma_f32_16x16x32_bf16 v[216:219], a[252:255], a[180:183], v[216:219]\n\t"
"v_cvt_pk_bf16_f32 v232, v232, v233\n\t"
"v_cvt_pk_bf16_f32 v233, v234, v235\n\t"
"v_mfma_f32_16x16x32_bf16 v[216:219], v[96:99], a[184:187], v[216:219]\n\t"
"ds_write2st64_b64 v245, v[224:225], v[226:227] offset0:48 offset1:49\n\t"
"v_mfma_f32_16x16x32_bf16 a[4:7], a[248:251], a[180:183], a[4:7]\n\t"
"v_mfma_f32_16x16x32_bf16 a[4:7], a[252:255], a[184:187], a[4:7]\n\t"
"ds_read_b64_tr_b16 v[224:225], v246 offset:33280\n\t"
"v_mfma_f32_16x16x32_bf16 a[4:7], v[96:99], a[188:191], a[4:7]\n\t"
"ds_write2st64_b64 v245, v[228:229], v[230:231] offset0:50 offset1:51\n\t"
"v_mfma_f32_16x16x32_bf16 a[12:15], a[248:251], a[184:187], a[12:15]\n\t"
"v_mfma_f32_16x16x32_bf16 a[12:15], a[252:255], a[188:191], a[12:15]\n\t"
"ds_read_b64_tr_b16 v[226:227], v246 offset:36352\n\t"
"v_mfma_f32_16x16x32_bf16 a[12:15], v[96:99], a[192:195], a[12:15]\n\t"
"ds_write_b64 v245, v[232:233] offset:26624\n\t"
"v_mfma_f32_16x16x32_bf16 a[20:23], a[248:251], a[188:191], a[20:23]\n\t"
"ds_read_b64_tr_b16 v[228:229], v246 offset:40960\n\t"
"v_mfma_f32_16x16x32_bf16 a[20:23], a[252:255], a[192:195], a[20:23]\n\t"
"ds_read_b64_tr_b16 v[230:231], v246 offset:44032\n\t"
"v_mfma_f32_16x16x32_bf16 a[20:23], v[96:99], a[196:199], a[20:23]\n\t"
"s_waitcnt lgkmcnt(3)\n\t"
"ds_read_b64_tr_b16 v[232:233], v246 offset:47104\n\t"
"v_mfma_f32_16x16x32_bf16 v[224:227], v[224:227], v[220:223], 0\n\t"
"ds_read_b64_tr_b16 v[234:235], v246 offset:50176\n\t"
"v_mfma_f32_16x16x32_bf16 a[28:31], a[248:251], a[192:195], a[28:31]\n\t"
"v_mfma_f32_16x16x32_bf16 a[28:31], a[252:255], a[196:199], a[28:31]\n\t"
"v_mfma_f32_16x16x32_bf16 a[28:31], v[96:99], a[200:203], a[28:31]\n\t"
"s_waitcnt lgkmcnt(2)\n\t"
"v_mfma_f32_16x16x32_bf16 v[228:231], v[228:231], v[220:223], 0\n\t"
"v_mfma_f32_16x16x32_bf16 a[36:39], a[248:251], a[196:199], a[36:39]\n\t"
"buffer_load_dwordx4 a[248:251], v252, s[8:11], s20 offen offset:1024 sc1\n\t"
"v_mfma_f32_16x16x32_bf16 a[36:39], a[252:255], a[200:203], a[36:39]\n\t"
"s_waitcnt vmcnt(8) lgkmcnt(0)\n\t"
"v_cvt_pk_bf16_f32 v224, v224, v225\n\t"
"v_cvt_pk_bf16_f32 v225, v226, v227\n\t"
"v_mfma_f32_16x16x32_bf16 a[36:39], v[96:99], a[204:207], a[36:39]\n\t"
"v_mfma_f32_16x16x32_bf16 v[232:235], v[232:235], v[220:223], 0\n\t"
"buffer_load_dwordx4 v[96:99], v252, s[8:11], s20 offen offset:2048 sc1\n\t"
"v_mfma_f32_16x16x32_bf16 a[44:47], a[220:223], a[168:171], a[44:47]\n\t"
"s_mov_b32 s20, 1241088\n\t"
"v_cvt_pk_bf16_f32 v226, v228, v229\n\t"
"v_cvt_pk_bf16_f32 v227, v230, v231\n\t"
"v_mfma_f32_16x16x32_bf16 a[44:47], a[224:227], a[172:175], a[44:47]\n\t"
"ds_read_b64_tr_b16 v[228:229], v246 offset:53248\n\t"
"v_mfma_f32_16x16x32_bf16 a[44:47], v[248:251], a[176:179], a[44:47]\n\t"
"buffer_load_dwordx4 a[252:255], v252, s[8:11], s20 offen offset:0 sc1\n\t"
"v_mfma_f32_16x16x32_bf16 a[52:55], a[220:223], a[172:175], a[52:55]\n\t"
"ds_read_b64_tr_b16 v[230:231], v246 offset:56320\n\t"
"v_mfma_f32_16x16x32_bf16 a[52:55], a[224:227], a[176:179], a[52:55]\n\t"
"v_cvt_pk_bf16_f32 v232, v232, v233\n\t"
"v_cvt_pk_bf16_f32 v233, v234, v235\n\t"
"v_mfma_f32_16x16x32_bf16 a[52:55], v[248:251], a[180:183], a[52:55]\n\t"
"ds_write2st64_b64 v245, v[224:225], v[226:227] offset0:53 offset1:54\n\t"
"v_mfma_f32_16x16x32_bf16 a[60:63], a[220:223], a[176:179], a[60:63]\n\t"
"v_mfma_f32_16x16x32_bf16 a[60:63], a[224:227], a[180:183], a[60:63]\n\t"
"v_subrev_u32 v243, 55296, v243\n\t"
"v_mfma_f32_16x16x32_bf16 a[60:63], v[248:251], a[184:187], a[60:63]\n\t"
"v_mfma_f32_16x16x32_bf16 a[68:71], a[220:223], a[180:183], a[68:71]\n\t"
"v_mfma_f32_16x16x32_bf16 a[68:71], a[224:227], a[184:187], a[68:71]\n\t"
"v_mfma_f32_16x16x32_bf16 a[68:71], v[248:251], a[188:191], a[68:71]\n\t"
"v_mfma_f32_16x16x32_bf16 a[76:79], a[220:223], a[184:187], a[76:79]\n\t"
"s_waitcnt lgkmcnt(1)\n\t"
"v_mfma_f32_16x16x32_bf16 a[76:79], a[224:227], a[188:191], a[76:79]\n\t"
"v_mfma_f32_16x16x32_bf16 a[76:79], v[248:251], a[192:195], a[76:79]\n\t"
"v_mfma_f32_16x16x32_bf16 v[228:231], v[228:231], v[220:223], 0\n\t"
"v_mfma_f32_16x16x32_bf16 a[84:87], a[220:223], a[188:191], a[84:87]\n\t"
"v_mfma_f32_16x16x32_bf16 a[84:87], a[224:227], a[192:195], a[84:87]\n\t"
"v_mfma_f32_16x16x32_bf16 a[84:87], v[248:251], a[196:199], a[84:87]\n\t"
"v_mfma_f32_16x16x32_bf16 a[92:95], a[220:223], a[192:195], a[92:95]\n\t"
"v_mfma_f32_16x16x32_bf16 a[92:95], a[224:227], a[196:199], a[92:95]\n\t"
"v_cvt_pk_bf16_f32 v234, v228, v229\n\t"
"v_cvt_pk_bf16_f32 v235, v230, v231\n\t"
"v_mfma_f32_16x16x32_bf16 a[92:95], v[248:251], a[200:203], a[92:95]\n\t"
"ds_write2st64_b64 v245, v[232:233], v[234:235] offset0:55 offset1:56\n\t"
"v_mfma_f32_16x16x32_bf16 a[100:103], a[220:223], a[196:199], a[100:103]\n\t"
"buffer_load_dwordx4 a[220:223], v252, s[8:11], s20 offen offset:1024 sc1\n\t"
"v_mfma_f32_16x16x32_bf16 a[100:103], a[224:227], a[200:203], a[100:103]\n\t"
"s_waitcnt vmcnt(8)\n\t"
"ds_read_b64_tr_b16 v[236:237], v240 offset:1536\n\t"
"v_mfma_f32_16x16x32_bf16 a[100:103], v[248:251], a[204:207], a[100:103]\n\t"
"ds_read_b64_tr_b16 v[238:239], v240 offset:3072\n\t"
"v_mfma_f32_16x16x32_bf16 a[108:111], a[228:231], a[168:171], a[108:111]\n\t"
"buffer_load_dwordx4 v[248:251], v252, s[8:11], s20 offen offset:2048 sc1\n\t"
"v_mfma_f32_16x16x32_bf16 a[108:111], a[232:235], a[172:175], a[108:111]\n\t"
"s_mov_b32 s20, 1253376\n\t"
"ds_read_b64_tr_b16 v[224:225], v241 offset:1536\n\t"
"v_mfma_f32_16x16x32_bf16 a[108:111], a[212:215], a[176:179], a[108:111]\n\t"
"s_waitcnt lgkmcnt(3)\n\t"
"s_barrier\n\t"
"v_mfma_f32_16x16x32_bf16 a[116:119], a[228:231], a[172:175], a[116:119]\n\t"
"ds_read_b64_tr_b16 v[226:227], v241 offset:3072\n\t"
"v_mfma_f32_16x16x32_bf16 a[116:119], a[232:235], a[176:179], a[116:119]\n\t"
"buffer_load_dwordx4 a[224:227], v252, s[8:11], s20 offen offset:0 sc1\n\t"
"v_mfma_f32_16x16x32_bf16 a[116:119], a[212:215], a[180:183], a[116:119]\n\t"
"ds_read_b64_tr_b16 v[228:229], v240 offset:21504\n\t"
"v_mfma_f32_16x16x32_bf16 a[124:127], a[228:231], a[176:179], a[124:127]\n\t"
"ds_read_b64_tr_b16 v[230:231], v240 offset:23040\n\t"
"v_mfma_f32_16x16x32_bf16 a[124:127], a[232:235], a[180:183], a[124:127]\n\t"
"ds_read_b64_tr_b16 v[232:233], v241 offset:21504\n\t"
"v_mfma_f32_16x16x32_bf16 a[124:127], a[212:215], a[184:187], a[124:127]\n\t"
"ds_read_b64_tr_b16 v[234:235], v241 offset:23040\n\t"
"v_mfma_f32_16x16x32_bf16 a[132:135], a[228:231], a[180:183], a[132:135]\n\t"
"s_waitcnt lgkmcnt(6)\n\t"
"ds_read_b128 a[168:171], v244 offset:24576\n\t"
"v_mfma_f32_16x16x32_bf16 a[132:135], a[232:235], a[184:187], a[132:135]\n\t"
"s_waitcnt lgkmcnt(5)\n\t"
"ds_read_b128 a[172:175], v244 offset:25088\n\t"
"v_mfma_f32_16x16x32_bf16 a[132:135], a[212:215], a[188:191], a[132:135]\n\t"
"ds_read_b128 a[176:179], v244 offset:25600\n\t"
"v_mfma_f32_16x16x32_bf16 v[236:239], v[236:239], a[208:211], 0\n\t"
"ds_read_b128 a[180:183], v244 offset:26112\n\t"
"v_mfma_f32_16x16x32_bf16 v[224:227], v[224:227], a[208:211], 0\n\t"
"v_mfma_f32_16x16x32_bf16 a[140:143], a[228:231], a[184:187], a[140:143]\n\t"
"ds_read_b128 a[184:187], v244 offset:26624\n\t"
"v_mfma_f32_16x16x32_bf16 a[140:143], a[232:235], a[188:191], a[140:143]\n\t"
"v_mfma_f32_16x16x32_bf16 a[140:143], a[212:215], a[192:195], a[140:143]\n\t"
"v_mfma_f32_16x16x32_bf16 a[148:151], a[228:231], a[188:191], a[148:151]\n\t"
"s_waitcnt lgkmcnt(7)\n\t"
"ds_read_b128 a[188:191], v244 offset:27136\n\t"
"v_cvt_pk_bf16_f32 v236, v236, v237\n\t"
"v_mfma_f32_16x16x32_bf16 a[148:151], a[232:235], a[192:195], a[148:151]\n\t"
"s_waitcnt lgkmcnt(6)\n\t"
"v_cvt_pk_bf16_f32 v237, v238, v239\n\t"
"v_cvt_pk_bf16_f32 v238, v224, v225\n\t"
"v_mfma_f32_16x16x32_bf16 a[148:151], a[212:215], a[196:199], a[148:151]\n\t"
"v_cvt_pk_bf16_f32 v239, v226, v227\n\t"
"v_mfma_f32_16x16x32_bf16 v[228:231], v[228:231], a[208:211], 0\n\t"
"ds_write_b128 v243, v[236:239] offset:1024\n\t"
"v_mfma_f32_16x16x32_bf16 v[232:235], v[232:235], a[208:211], 0\n\t"
"v_mfma_f32_16x16x32_bf16 a[156:159], a[228:231], a[192:195], a[156:159]\n\t"
"ds_read_b128 a[192:195], v244 offset:27648\n\t"
"v_mfma_f32_16x16x32_bf16 a[156:159], a[232:235], a[196:199], a[156:159]\n\t"
"ds_read_b64_tr_b16 v[224:225], v240 offset:41472\n\t"
"v_mfma_f32_16x16x32_bf16 a[156:159], a[212:215], a[200:203], a[156:159]\n\t"
"ds_read_b64_tr_b16 v[226:227], v240 offset:43008\n\t"
"v_mfma_f32_16x16x32_bf16 a[164:167], a[228:231], a[196:199], a[164:167]\n\t"
"buffer_load_dwordx4 a[228:231], v252, s[8:11], s20 offen offset:1024 sc1\n\t"
"v_mfma_f32_16x16x32_bf16 a[164:167], a[232:235], a[200:203], a[164:167]\n\t"
"s_waitcnt vmcnt(8) lgkmcnt(7)\n\t"
"ds_read_b64_tr_b16 v[236:237], v241 offset:41472\n\t"
"v_cvt_pk_bf16_f32 v228, v228, v229\n\t"
"v_mfma_f32_16x16x32_bf16 a[164:167], a[212:215], a[204:207], a[164:167]\n\t"
"ds_read_b64_tr_b16 v[238:239], v241 offset:43008\n\t"
"v_cvt_pk_bf16_f32 v229, v230, v231\n\t"
"Lst29_%=:\n\t"
"v_mfma_f32_16x16x32_bf16 v[0:3], a[236:239], a[168:171], v[0:3]\n\t"
"s_waitcnt lgkmcnt(8)\n\t"
"ds_read_b128 a[196:199], v244 offset:28160\n\t"
"v_cvt_pk_bf16_f32 v230, v232, v233\n\t"
"v_mfma_f32_16x16x32_bf16 v[0:3], a[216:219], a[172:175], v[0:3]\n\t"
"ds_read_b128 a[200:203], v244 offset:28672\n\t"
"v_cvt_pk_bf16_f32 v231, v234, v235\n\t"
"v_mfma_f32_16x16x32_bf16 v[0:3], a[240:243], a[176:179], v[0:3]\n\t"
"ds_write_b128 v243, v[228:231] offset:10240\n\t"
"v_mfma_f32_16x16x32_bf16 v[4:7], a[236:239], a[172:175], v[4:7]\n\t"
"buffer_load_dwordx4 a[212:215], v252, s[8:11], s20 offen offset:2048 sc1\n\t"
"v_mfma_f32_16x16x32_bf16 v[4:7], a[216:219], a[176:179], v[4:7]\n\t"
"s_waitcnt lgkmcnt(10)\n\t"
"ds_read_b128 a[204:207], v244 offset:29184\n\t"
"v_mfma_f32_16x16x32_bf16 v[4:7], a[240:243], a[180:183], v[4:7]\n\t"
"s_mov_b32 s20, 1265664\n\t"
"v_add_u32 v240, 59904, v240\n\t"
"v_add_u32 v241, 59904, v241\n\t"
"v_mfma_f32_16x16x32_bf16 v[8:11], a[236:239], a[176:179], v[8:11]\n\t"
"s_waitcnt lgkmcnt(6)\n\t"
"ds_read_b64_tr_b16 v[232:233], v240 offset:1536\n\t"
"v_mfma_f32_16x16x32_bf16 v[8:11], a[216:219], a[180:183], v[8:11]\n\t"
"buffer_load_dwordx4 a[232:235], v252, s[8:11], s20 offen offset:0 sc1\n\t"
"v_mfma_f32_16x16x32_bf16 v[8:11], a[240:243], a[184:187], v[8:11]\n\t"
"s_waitcnt lgkmcnt(5)\n\t"
"ds_read_b64_tr_b16 v[234:235], v240 offset:3072\n\t"
"v_mfma_f32_16x16x32_bf16 v[224:227], v[224:227], a[208:211], 0\n\t"
"ds_read_b64_tr_b16 v[228:229], v241 offset:1536\n\t"
"v_mfma_f32_16x16x32_bf16 v[236:239], v[236:239], a[208:211], 0\n\t"
"ds_read_b64_tr_b16 v[230:231], v241 offset:3072\n\t"
"v_mfma_f32_16x16x32_bf16 v[12:15], a[236:239], a[180:183], v[12:15]\n\t"
"v_mfma_f32_16x16x32_bf16 v[12:15], a[216:219], a[184:187], v[12:15]\n\t"
"v_mfma_f32_16x16x32_bf16 v[12:15], a[240:243], a[188:191], v[12:15]\n\t"
"v_mfma_f32_16x16x32_bf16 v[16:19], a[236:239], a[184:187], v[16:19]\n\t"
"s_waitcnt lgkmcnt(2)\n\t"
"v_cvt_pk_bf16_f32 v224, v224, v225\n\t"
"v_cvt_pk_bf16_f32 v225, v226, v227\n\t"
"v_mfma_f32_16x16x32_bf16 v[16:19], a[216:219], a[188:191], v[16:19]\n\t"
"s_waitcnt lgkmcnt(0)\n\t"
"v_cvt_pk_bf16_f32 v226, v236, v237\n\t"
"v_cvt_pk_bf16_f32 v227, v238, v239\n\t"
"v_mfma_f32_16x16x32_bf16 v[16:19], a[240:243], a[192:195], v[16:19]\n\t"
"ds_write_b128 v243, v[224:227] offset:19456\n\t"
"v_mfma_f32_16x16x32_bf16 v[232:235], v[232:235], a[208:211], 0\n\t"
"v_mfma_f32_16x16x32_bf16 v[228:231], v[228:231], a[208:211], 0\n\t"
"ds_read_b64_tr_b16 v[236:237], v240 offset:21504\n\t"
"v_mfma_f32_16x16x32_bf16 v[20:23], a[236:239], a[188:191], v[20:23]\n\t"
"ds_read_b64_tr_b16 v[238:239], v240 offset:23040\n\t"
"v_mfma_f32_16x16x32_bf16 v[20:23], a[216:219], a[192:195], v[20:23]\n\t"
"ds_read_b64_tr_b16 v[224:225], v241 offset:21504\n\t"
"v_mfma_f32_16x16x32_bf16 v[20:23], a[240:243], a[196:199], v[20:23]\n\t"
"ds_read_b64_tr_b16 v[226:227], v241 offset:23040\n\t"
"v_mfma_f32_16x16x32_bf16 v[24:27], a[236:239], a[192:195], v[24:27]\n\t"
"v_cvt_pk_bf16_f32 v232, v232, v233\n\t"
"v_cvt_pk_bf16_f32 v233, v234, v235\n\t"
"v_mfma_f32_16x16x32_bf16 v[24:27], a[216:219], a[196:199], v[24:27]\n\t"
"v_cvt_pk_bf16_f32 v234, v228, v229\n\t"
"v_cvt_pk_bf16_f32 v235, v230, v231\n\t"
"v_mfma_f32_16x16x32_bf16 v[24:27], a[240:243], a[200:203], v[24:27]\n\t"
"ds_write_b128 v243, v[232:235] offset:28672\n\t"
"v_mfma_f32_16x16x32_bf16 v[28:31], a[236:239], a[196:199], v[28:31]\n\t"
"buffer_load_dwordx4 a[236:239], v252, s[8:11], s20 offen offset:1024 sc1\n\t"
"v_mfma_f32_16x16x32_bf16 v[28:31], a[216:219], a[200:203], v[28:31]\n\t"
"s_waitcnt vmcnt(8)\n\t"
"ds_read_b64_tr_b16 v[228:229], v240 offset:41472\n\t"
"v_mfma_f32_16x16x32_bf16 v[28:31], a[240:243], a[204:207], v[28:31]\n\t"
"ds_read_b64_tr_b16 v[230:231], v240 offset:43008\n\t"
"v_mfma_f32_16x16x32_bf16 v[32:35], a[244:247], a[168:171], v[32:35]\n\t"
"buffer_load_dwordx4 a[216:219], v252, s[8:11], s20 offen offset:2048 sc1\n\t"
"v_mfma_f32_16x16x32_bf16 v[32:35], a[248:251], a[172:175], v[32:35]\n\t"
"s_mov_b32 s20, 1277952\n\t"
"ds_read_b64_tr_b16 v[232:233], v241 offset:41472\n\t"
"v_mfma_f32_16x16x32_bf16 v[32:35], v[96:99], a[176:179], v[32:35]\n\t"
"ds_read_b64_tr_b16 v[234:235], v241 offset:43008\n\t"
"v_mfma_f32_16x16x32_bf16 v[36:39], a[244:247], a[172:175], v[36:39]\n\t"
"buffer_load_dwordx4 a[240:243], v252, s[8:11], s20 offen offset:0 sc1\n\t"
"v_mfma_f32_16x16x32_bf16 v[36:39], a[248:251], a[176:179], v[36:39]\n\t"
"s_waitcnt lgkmcnt(5)\n\t"
"v_mfma_f32_16x16x32_bf16 v[36:39], v[96:99], a[180:183], v[36:39]\n\t"
"v_mfma_f32_16x16x32_bf16 v[236:239], v[236:239], a[208:211], 0\n\t"
"v_mfma_f32_16x16x32_bf16 v[224:227], v[224:227], a[208:211], 0\n\t"
"v_mfma_f32_16x16x32_bf16 v[40:43], a[244:247], a[176:179], v[40:43]\n\t"
"v_mfma_f32_16x16x32_bf16 v[40:43], a[248:251], a[180:183], v[40:43]\n\t"
"v_mfma_f32_16x16x32_bf16 v[40:43], v[96:99], a[184:187], v[40:43]\n\t"
"v_mfma_f32_16x16x32_bf16 v[44:47], a[244:247], a[180:183], v[44:47]\n\t"
"s_waitcnt lgkmcnt(2)\n\t"
"v_cvt_pk_bf16_f32 v236, v236, v237\n\t"
"v_cvt_pk_bf16_f32 v237, v238, v239\n\t"
"v_mfma_f32_16x16x32_bf16 v[44:47], a[248:251], a[184:187], v[44:47]\n\t"
"s_waitcnt lgkmcnt(0)\n\t"
"v_cvt_pk_bf16_f32 v238, v224, v225\n\t"
"v_cvt_pk_bf16_f32 v239, v226, v227\n\t"
"v_mfma_f32_16x16x32_bf16 v[44:47], v[96:99], a[188:191], v[44:47]\n\t"
"ds_write_b128 v243, v[236:239] offset:37888\n\t"
"v_mfma_f32_16x16x32_bf16 v[228:231], v[228:231], a[208:211], 0\n\t"
"v_mfma_f32_16x16x32_bf16 v[232:235], v[232:235], a[208:211], 0\n\t"
"v_mfma_f32_16x16x32_bf16 v[48:51], a[244:247], a[184:187], v[48:51]\n\t"
"buffer_load_dwordx4 a[208:211], v252, s[8:11], s20 offen offset:1024 sc1\n\t"
"v_mfma_f32_16x16x32_bf16 v[48:51], a[248:251], a[188:191], v[48:51]\n\t"
"v_mfma_f32_16x16x32_bf16 v[48:51], v[96:99], a[192:195], v[48:51]\n\t"
"v_mfma_f32_16x16x32_bf16 v[52:55], a[244:247], a[188:191], v[52:55]\n\t"
"v_cvt_pk_bf16_f32 v228, v228, v229\n\t"
"v_cvt_pk_bf16_f32 v229, v230, v231\n\t"
"v_mfma_f32_16x16x32_bf16 v[52:55], a[248:251], a[192:195], v[52:55]\n\t"
"v_cvt_pk_bf16_f32 v230, v232, v233\n\t"
"v_cvt_pk_bf16_f32 v231, v234, v235\n\t"
"v_mfma_f32_16x16x32_bf16 v[52:55], v[96:99], a[196:199], v[52:55]\n\t"
"ds_write_b128 v243, v[228:231] offset:47104\n\t"
"v_mfma_f32_16x16x32_bf16 v[56:59], a[244:247], a[192:195], v[56:59]\n\t"
"v_mfma_f32_16x16x32_bf16 v[56:59], a[248:251], a[196:199], v[56:59]\n\t"
"ds_read_b64_tr_b16 v[224:225], v246 offset:1536\n\t"
"v_mfma_f32_16x16x32_bf16 v[56:59], v[96:99], a[200:203], v[56:59]\n\t"
"ds_read_b64_tr_b16 v[226:227], v246 offset:4608\n\t"
"v_mfma_f32_16x16x32_bf16 v[60:63], a[244:247], a[196:199], v[60:63]\n\t"
"buffer_load_dwordx4 a[244:247], v252, s[8:11], s20 offen offset:2048 sc1\n\t"
"v_mfma_f32_16x16x32_bf16 v[60:63], a[248:251], a[200:203], v[60:63]\n\t"
"s_waitcnt vmcnt(9)\n\t"
"ds_read_b64_tr_b16 v[236:237], v246 offset:7680\n\t"
"s_mov_b32 s20, 1511424\n\t"
"v_mfma_f32_16x16x32_bf16 v[60:63], v[96:99], a[204:207], v[60:63]\n\t"
"ds_read_b64_tr_b16 v[238:239], v246 offset:10752\n\t"
"v_mfma_f32_16x16x32_bf16 v[64:67], a[252:255], a[168:171], v[64:67]\n\t"
"buffer_load_dwordx4 v[96:99], v252, s[8:11], s20 offen offset:0 sc1\n\t"
"v_mfma_f32_16x16x32_bf16 v[64:67], a[220:223], a[172:175], v[64:67]\n\t"
"ds_read_b64_tr_b16 v[232:233], v246 offset:13824\n\t"
"v_mfma_f32_16x16x32_bf16 v[64:67], v[248:251], a[176:179], v[64:67]\n\t"
"ds_read_b64_tr_b16 v[234:235], v246 offset:16896\n\t"
"v_mfma_f32_16x16x32_bf16 v[68:71], a[252:255], a[172:175], v[68:71]\n\t"
"buffer_load_dwordx4 a[248:251], v252, s[8:11], s20 offen offset:1024 sc1\n\t"
"v_mfma_f32_16x16x32_bf16 v[68:71], a[220:223], a[176:179], v[68:71]\n\t"
"ds_read_b128 a[168:171], v244 offset:33792\n\t"
"v_mfma_f32_16x16x32_bf16 v[68:71], v[248:251], a[180:183], v[68:71]\n\t"
"ds_read_b64_tr_b16 v[228:229], v246 offset:21504\n\t"
"v_mfma_f32_16x16x32_bf16 v[72:75], a[252:255], a[176:179], v[72:75]\n\t"
"buffer_load_dwordx4 v[240:243], v252, s[8:11], s20 offen offset:2048 sc1\n\t"
"v_mfma_f32_16x16x32_bf16 v[72:75], a[220:223], a[180:183], v[72:75]\n\t"
"s_waitcnt lgkmcnt(6)\n\t"
"ds_read_b64_tr_b16 v[230:231], v246 offset:24576\n\t"
"v_mfma_f32_16x16x32_bf16 v[72:75], v[248:251], a[184:187], v[72:75]\n\t"
"ds_read_b128 a[172:175], v244 offset:34304\n\t"
"v_mfma_f32_16x16x32_bf16 v[224:227], v[224:227], v[220:223], 0\n\t"
"ds_read_b128 a[176:179], v244 offset:34816\n\t"
"v_mfma_f32_16x16x32_bf16 v[76:79], a[252:255], a[180:183], v[76:79]\n\t"
"s_waitcnt lgkmcnt(7)\n\t"
"ds_read_b128 a[180:183], v244 offset:35328\n\t"
"v_mfma_f32_16x16x32_bf16 v[76:79], a[220:223], a[184:187], v[76:79]\n\t"
"v_mfma_f32_16x16x32_bf16 v[76:79], v[248:251], a[188:191], v[76:79]\n\t"
"v_mfma_f32_16x16x32_bf16 v[236:239], v[236:239], v[220:223], 0\n\t"
"v_mfma_f32_16x16x32_bf16 v[80:83], a[252:255], a[184:187], v[80:83]\n\t"
"s_waitcnt lgkmcnt(6)\n\t"
"ds_read_b128 a[184:187], v244 offset:35840\n\t"
"v_cvt_pk_bf16_f32 v224, v224, v225\n\t"
"v_mfma_f32_16x16x32_bf16 v[80:83], a[220:223], a[188:191], v[80:83]\n\t"
"v_cvt_pk_bf16_f32 v225, v226, v227\n\t"
"v_mfma_f32_16x16x32_bf16 v[80:83], v[248:251], a[192:195], v[80:83]\n\t"
"s_mov_b32 s20, 1523712\n\t"
"v_mfma_f32_16x16x32_bf16 v[232:235], v[232:235], v[220:223], 0\n\t"
"v_mfma_f32_16x16x32_bf16 v[84:87], a[252:255], a[188:191], v[84:87]\n\t"
"s_waitcnt lgkmcnt(4)\n\t"
"ds_read_b128 a[188:191], v244 offset:36352\n\t"
"v_cvt_pk_bf16_f32 v226, v236, v237\n\t"
"v_mfma_f32_16x16x32_bf16 v[84:87], a[220:223], a[192:195], v[84:87]\n\t"
"v_cvt_pk_bf16_f32 v227, v238, v239\n\t"
"v_mfma_f32_16x16x32_bf16 v[84:87], v[248:251], a[196:199], v[84:87]\n\t"
"v_mfma_f32_16x16x32_bf16 v[228:231], v[228:231], v[220:223], 0\n\t"
"v_mfma_f32_16x16x32_bf16 v[88:91], a[252:255], a[192:195], v[88:91]\n\t"
"buffer_load_dwordx4 v[236:239], v252, s[8:11], s20 offen offset:0 sc1\n\t"
"v_mfma_f32_16x16x32_bf16 v[88:91], a[220:223], a[196:199], v[88:91]\n\t"
"ds_read_b128 a[192:195], v244 offset:36864\n\t"
"v_cvt_pk_bf16_f32 v232, v232, v233\n\t"
"v_mfma_f32_16x16x32_bf16 v[88:91], v[248:251], a[200:203], v[88:91]\n\t"
"v_cvt_pk_bf16_f32 v233, v234, v235\n\t"
"v_mfma_f32_16x16x32_bf16 v[92:95], a[252:255], a[196:199], v[92:95]\n\t"
"buffer_load_dwordx4 a[252:255], v252, s[8:11], s20 offen offset:1024 sc1\n\t"
"v_mfma_f32_16x16x32_bf16 v[92:95], a[220:223], a[200:203], v[92:95]\n\t"
"s_waitcnt vmcnt(11) lgkmcnt(4)\n\t"
"ds_read_b128 a[196:199], v244 offset:37376\n\t"
"v_cvt_pk_bf16_f32 v234, v228, v229\n\t"
"v_mfma_f32_16x16x32_bf16 v[92:95], v[248:251], a[204:207], v[92:95]\n\t"
"ds_read_b128 a[200:203], v244 offset:37888\n\t"
"v_cvt_pk_bf16_f32 v235, v230, v231\n\t"
"v_mfma_f32_16x16x32_bf16 v[100:103], a[224:227], a[168:171], v[100:103]\n\t"
"ds_read_b64_tr_b16 v[228:229], v246 offset:27648\n\t"
"v_mfma_f32_16x16x32_bf16 v[100:103], a[228:231], a[172:175], v[100:103]\n\t"
"s_waitcnt lgkmcnt(6)\n\t"
"ds_read_b64_tr_b16 v[230:231], v246 offset:30720\n\t"
"v_mfma_f32_16x16x32_bf16 v[100:103], a[212:215], a[176:179], v[100:103]\n\t"
"s_waitcnt lgkmcnt(0)\n\t"
"ds_read_b128 a[204:207], v244 offset:38400\n\t"
"v_mfma_f32_16x16x32_bf16 v[228:231], v[228:231], v[220:223], 0\n\t"
"buffer_load_dwordx4 v[248:251], v252, s[8:11], s20 offen offset:2048 sc1\n\t"
"s_mov_b32 s20, 1536000\n\t"
"v_mfma_f32_16x16x32_bf16 v[104:107], a[224:227], a[172:175], v[104:107]\n\t"
"buffer_load_dwordx4 a[220:223], v252, s[8:11], s20 offen offset:0 sc1\n\t"
"v_mfma_f32_16x16x32_bf16 v[104:107], a[228:231], a[176:179], v[104:107]\n\t"
"v_mfma_f32_16x16x32_bf16 v[104:107], a[212:215], a[180:183], v[104:107]\n\t"
"s_waitcnt lgkmcnt(0)\n\t"
"s_barrier\n\t"
"v_mfma_f32_16x16x32_bf16 v[108:111], a[224:227], a[176:179], v[108:111]\n\t"
"v_mfma_f32_16x16x32_bf16 v[108:111], a[228:231], a[180:183], v[108:111]\n\t"
"v_cvt_pk_bf16_f32 v228, v228, v229\n\t"
"v_cvt_pk_bf16_f32 v229, v230, v231\n\t"
"v_mfma_f32_16x16x32_bf16 v[108:111], a[212:215], a[184:187], v[108:111]\n\t"
"ds_write2st64_b64 v245, v[224:225], v[226:227] offset0:48 offset1:49\n\t"
"v_mfma_f32_16x16x32_bf16 v[112:115], a[224:227], a[180:183], v[112:115]\n\t"
"v_mfma_f32_16x16x32_bf16 v[112:115], a[228:231], a[184:187], v[112:115]\n\t"
"ds_read_b64_tr_b16 v[224:225], v246 offset:33792\n\t"
"v_mfma_f32_16x16x32_bf16 v[112:115], a[212:215], a[188:191], v[112:115]\n\t"
"ds_write2st64_b64 v245, v[232:233], v[234:235] offset0:50 offset1:51\n\t"
"v_mfma_f32_16x16x32_bf16 v[116:119], a[224:227], a[184:187], v[116:119]\n\t"
"v_mfma_f32_16x16x32_bf16 v[116:119], a[228:231], a[188:191], v[116:119]\n\t"
"ds_read_b64_tr_b16 v[226:227], v246 offset:36864\n\t"
"v_mfma_f32_16x16x32_bf16 v[116:119], a[212:215], a[192:195], v[116:119]\n\t"
"ds_write_b64 v245, v[228:229] offset:26624\n\t"
"v_mfma_f32_16x16x32_bf16 v[120:123], a[224:227], a[188:191], v[120:123]\n\t"
"ds_read_b64_tr_b16 v[232:233], v246 offset:41472\n\t"
"v_mfma_f32_16x16x32_bf16 v[120:123], a[228:231], a[192:195], v[120:123]\n\t"
"ds_read_b64_tr_b16 v[234:235], v246 offset:44544\n\t"
"v_mfma_f32_16x16x32_bf16 v[120:123], a[212:215], a[196:199], v[120:123]\n\t"
"s_waitcnt lgkmcnt(3)\n\t"
"ds_read_b64_tr_b16 v[228:229], v246 offset:47616\n\t"
"v_mfma_f32_16x16x32_bf16 v[224:227], v[224:227], v[220:223], 0\n\t"
"ds_read_b64_tr_b16 v[230:231], v246 offset:50688\n\t"
"v_mfma_f32_16x16x32_bf16 v[124:127], a[224:227], a[192:195], v[124:127]\n\t"
"v_mfma_f32_16x16x32_bf16 v[124:127], a[228:231], a[196:199], v[124:127]\n\t"
"v_mfma_f32_16x16x32_bf16 v[124:127], a[212:215], a[200:203], v[124:127]\n\t"
"s_waitcnt lgkmcnt(2)\n\t"
"v_mfma_f32_16x16x32_bf16 v[232:235], v[232:235], v[220:223], 0\n\t"
"v_mfma_f32_16x16x32_bf16 v[128:131], a[224:227], a[196:199], v[128:131]\n\t"
"buffer_load_dwordx4 a[224:227], v252, s[8:11], s20 offen offset:1024 sc1\n\t"
"v_mfma_f32_16x16x32_bf16 v[128:131], a[228:231], a[200:203], v[128:131]\n\t"
"s_waitcnt vmcnt(11) lgkmcnt(0)\n\t"
"v_cvt_pk_bf16_f32 v224, v224, v225\n\t"
"v_cvt_pk_bf16_f32 v225, v226, v227\n\t"
"v_mfma_f32_16x16x32_bf16 v[128:131], a[212:215], a[204:207], v[128:131]\n\t"
"v_mfma_f32_16x16x32_bf16 v[228:231], v[228:231], v[220:223], 0\n\t"
"buffer_load_dwordx4 a[212:215], v252, s[8:11], s20 offen offset:2048 sc1\n\t"
"v_mfma_f32_16x16x32_bf16 v[132:135], a[232:235], a[168:171], v[132:135]\n\t"
"s_mov_b32 s20, 1548288\n\t"
"v_cvt_pk_bf16_f32 v226, v232, v233\n\t"
"v_cvt_pk_bf16_f32 v227, v234, v235\n\t"
"v_mfma_f32_16x16x32_bf16 v[132:135], a[236:239], a[172:175], v[132:135]\n\t"
"ds_read_b64_tr_b16 v[232:233], v246 offset:53760\n\t"
"v_mfma_f32_16x16x32_bf16 v[132:135], a[216:219], a[176:179], v[132:135]\n\t"
"buffer_load_dwordx4 a[228:231], v252, s[8:11], s20 offen offset:0 sc1\n\t"
"v_mfma_f32_16x16x32_bf16 v[136:139], a[232:235], a[172:175], v[136:139]\n\t"
"ds_read_b64_tr_b16 v[234:235], v246 offset:56832\n\t"
"v_mfma_f32_16x16x32_bf16 v[136:139], a[236:239], a[176:179], v[136:139]\n\t"
"v_cvt_pk_bf16_f32 v228, v228, v229\n\t"
"v_cvt_pk_bf16_f32 v229, v230, v231\n\t"
"v_mfma_f32_16x16x32_bf16 v[136:139], a[216:219], a[180:183], v[136:139]\n\t"
"ds_write2st64_b64 v245, v[224:225], v[226:227] offset0:53 offset1:54\n\t"
"v_mfma_f32_16x16x32_bf16 v[140:143], a[232:235], a[176:179], v[140:143]\n\t"
"v_mfma_f32_16x16x32_bf16 v[140:143], a[236:239], a[180:183], v[140:143]\n\t"
"v_mfma_f32_16x16x32_bf16 v[140:143], a[216:219], a[184:187], v[140:143]\n\t"
"v_mfma_f32_16x16x32_bf16 v[144:147], a[232:235], a[180:183], v[144:147]\n\t"
"v_mfma_f32_16x16x32_bf16 v[144:147], a[236:239], a[184:187], v[144:147]\n\t"
"v_mfma_f32_16x16x32_bf16 v[144:147], a[216:219], a[188:191], v[144:147]\n\t"
"v_mfma_f32_16x16x32_bf16 v[148:151], a[232:235], a[184:187], v[148:151]\n\t"
"s_waitcnt lgkmcnt(1)\n\t"
"v_mfma_f32_16x16x32_bf16 v[148:151], a[236:239], a[188:191], v[148:151]\n\t"
"v_mfma_f32_16x16x32_bf16 v[148:151], a[216:219], a[192:195], v[148:151]\n\t"
"v_mfma_f32_16x16x32_bf16 v[232:235], v[232:235], v[220:223], 0\n\t"
"v_mfma_f32_16x16x32_bf16 v[152:155], a[232:235], a[188:191], v[152:155]\n\t"
"v_mfma_f32_16x16x32_bf16 v[152:155], a[236:239], a[192:195], v[152:155]\n\t"
"v_mfma_f32_16x16x32_bf16 v[152:155], a[216:219], a[196:199], v[152:155]\n\t"
"v_mfma_f32_16x16x32_bf16 v[156:159], a[232:235], a[192:195], v[156:159]\n\t"
"v_mfma_f32_16x16x32_bf16 v[156:159], a[236:239], a[196:199], v[156:159]\n\t"
"v_cvt_pk_bf16_f32 v230, v232, v233\n\t"
"v_cvt_pk_bf16_f32 v231, v234, v235\n\t"
"v_mfma_f32_16x16x32_bf16 v[156:159], a[216:219], a[200:203], v[156:159]\n\t"
"ds_write2st64_b64 v245, v[228:229], v[230:231] offset0:55 offset1:56\n\t"
"v_mfma_f32_16x16x32_bf16 v[160:163], a[232:235], a[196:199], v[160:163]\n\t"
"buffer_load_dwordx4 a[232:235], v252, s[8:11], s20 offen offset:1024 sc1\n\t"
"v_mfma_f32_16x16x32_bf16 v[160:163], a[236:239], a[200:203], v[160:163]\n\t"
"s_waitcnt vmcnt(11)\n\t"
"v_mfma_f32_16x16x32_bf16 v[160:163], a[216:219], a[204:207], v[160:163]\n\t"
"v_mfma_f32_16x16x32_bf16 v[164:167], a[240:243], a[168:171], v[164:167]\n\t"
"buffer_load_dwordx4 a[216:219], v252, s[8:11], s20 offen offset:2048 sc1\n\t"
"v_mfma_f32_16x16x32_bf16 v[164:167], a[208:211], a[172:175], v[164:167]\n\t"
"s_mov_b32 s20, 1560576\n\t"
"v_mfma_f32_16x16x32_bf16 v[164:167], a[244:247], a[176:179], v[164:167]\n\t"
"s_waitcnt lgkmcnt(0)\n\t"
"s_barrier\n\t"
"v_mfma_f32_16x16x32_bf16 v[168:171], a[240:243], a[172:175], v[168:171]\n\t"
"ds_read_b128 a[168:171], v244 offset:24576\n\t"
"v_mfma_f32_16x16x32_bf16 v[168:171], a[208:211], a[176:179], v[168:171]\n\t"
"buffer_load_dwordx4 a[236:239], v252, s[8:11], s20 offen offset:0 sc1\n\t"
"v_mfma_f32_16x16x32_bf16 v[168:171], a[244:247], a[180:183], v[168:171]\n\t"
"ds_read_b128 a[172:175], v244 offset:25088\n\t"
"v_mfma_f32_16x16x32_bf16 v[172:175], a[240:243], a[176:179], v[172:175]\n\t"
"ds_read_b128 a[176:179], v244 offset:25600\n\t"
"v_mfma_f32_16x16x32_bf16 v[172:175], a[208:211], a[180:183], v[172:175]\n\t"
"v_mfma_f32_16x16x32_bf16 v[172:175], a[244:247], a[184:187], v[172:175]\n\t"
"v_mfma_f32_16x16x32_bf16 v[176:179], a[240:243], a[180:183], v[176:179]\n\t"
"ds_read_b128 a[180:183], v244 offset:26112\n\t"
"v_mfma_f32_16x16x32_bf16 v[176:179], a[208:211], a[184:187], v[176:179]\n\t"
"v_mfma_f32_16x16x32_bf16 v[176:179], a[244:247], a[188:191], v[176:179]\n\t"
"v_mfma_f32_16x16x32_bf16 v[180:183], a[240:243], a[184:187], v[180:183]\n\t"
"ds_read_b128 a[184:187], v244 offset:26624\n\t"
"v_mfma_f32_16x16x32_bf16 v[180:183], a[208:211], a[188:191], v[180:183]\n\t"
"v_mfma_f32_16x16x32_bf16 v[180:183], a[244:247], a[192:195], v[180:183]\n\t"
"v_mfma_f32_16x16x32_bf16 v[184:187], a[240:243], a[188:191], v[184:187]\n\t"
"ds_read_b128 a[188:191], v244 offset:27136\n\t"
"v_mfma_f32_16x16x32_bf16 v[184:187], a[208:211], a[192:195], v[184:187]\n\t"
"v_mfma_f32_16x16x32_bf16 v[184:187], a[244:247], a[196:199], v[184:187]\n\t"
"v_mfma_f32_16x16x32_bf16 v[188:191], a[240:243], a[192:195], v[188:191]\n\t"
"ds_read_b128 a[192:195], v244 offset:27648\n\t"
"v_mfma_f32_16x16x32_bf16 v[188:191], a[208:211], a[196:199], v[188:191]\n\t"
"v_mfma_f32_16x16x32_bf16 v[188:191], a[244:247], a[200:203], v[188:191]\n\t"
"v_mfma_f32_16x16x32_bf16 v[192:195], a[240:243], a[196:199], v[192:195]\n\t"
"buffer_load_dwordx4 a[240:243], v252, s[8:11], s20 offen offset:1024 sc1\n\t"
"v_mfma_f32_16x16x32_bf16 v[192:195], a[208:211], a[200:203], v[192:195]\n\t"
"s_waitcnt vmcnt(11) lgkmcnt(4)\n\t"
"ds_read_b128 a[196:199], v244 offset:28160\n\t"
"v_mfma_f32_16x16x32_bf16 v[192:195], a[244:247], a[204:207], v[192:195]\n\t"
"ds_read_b128 a[200:203], v244 offset:28672\n\t"
"Lst30_%=:\n\t"
"v_mfma_f32_16x16x32_bf16 v[0:3], v[96:99], a[168:171], v[0:3]\n\t"
"buffer_load_dwordx4 a[208:211], v252, s[8:11], s20 offen offset:2048 sc1\n\t"
"v_mfma_f32_16x16x32_bf16 v[0:3], a[248:251], a[172:175], v[0:3]\n\t"
"s_waitcnt lgkmcnt(5)\n\t"
"ds_read_b128 a[204:207], v244 offset:29184\n\t"
"v_xor_b32 v246, 0xc0, v246\n\t"
"v_mfma_f32_16x16x32_bf16 v[0:3], v[240:243], a[176:179], v[0:3]\n\t"
"s_mov_b32 s20, 1572864\n\t"
"v_mfma_f32_16x16x32_bf16 v[4:7], v[96:99], a[172:175], v[4:7]\n\t"
"buffer_load_dwordx4 a[244:247], v252, s[8:11], s20 offen offset:0 sc1\n\t"
"v_mfma_f32_16x16x32_bf16 v[4:7], a[248:251], a[176:179], v[4:7]\n\t"
"s_waitcnt lgkmcnt(5)\n\t"
"v_mfma_f32_16x16x32_bf16 v[4:7], v[240:243], a[180:183], v[4:7]\n\t"
"v_mfma_f32_16x16x32_bf16 v[8:11], v[96:99], a[176:179], v[8:11]\n\t"
"s_waitcnt lgkmcnt(4)\n\t"
"v_mfma_f32_16x16x32_bf16 v[8:11], a[248:251], a[180:183], v[8:11]\n\t"
"v_mfma_f32_16x16x32_bf16 v[8:11], v[240:243], a[184:187], v[8:11]\n\t"
"v_mfma_f32_16x16x32_bf16 v[12:15], v[96:99], a[180:183], v[12:15]\n\t"
"s_waitcnt lgkmcnt(3)\n\t"
"v_mfma_f32_16x16x32_bf16 v[12:15], a[248:251], a[184:187], v[12:15]\n\t"
"v_mfma_f32_16x16x32_bf16 v[12:15], v[240:243], a[188:191], v[12:15]\n\t"
"v_mfma_f32_16x16x32_bf16 v[16:19], v[96:99], a[184:187], v[16:19]\n\t"
"s_waitcnt lgkmcnt(2)\n\t"
"v_mfma_f32_16x16x32_bf16 v[16:19], a[248:251], a[188:191], v[16:19]\n\t"
"v_mfma_f32_16x16x32_bf16 v[16:19], v[240:243], a[192:195], v[16:19]\n\t"
"ds_read_b64_tr_b16 v[224:225], v246 offset:1536\n\t"
"v_mfma_f32_16x16x32_bf16 v[20:23], v[96:99], a[188:191], v[20:23]\n\t"
"s_waitcnt lgkmcnt(2)\n\t"
"ds_read_b64_tr_b16 v[226:227], v246 offset:4608\n\t"
"v_mfma_f32_16x16x32_bf16 v[20:23], a[248:251], a[192:195], v[20:23]\n\t"
"v_mfma_f32_16x16x32_bf16 v[20:23], v[240:243], a[196:199], v[20:23]\n\t"
"v_mfma_f32_16x16x32_bf16 v[24:27], v[96:99], a[192:195], v[24:27]\n\t"
"s_waitcnt lgkmcnt(2)\n\t"
"v_mfma_f32_16x16x32_bf16 v[24:27], a[248:251], a[196:199], v[24:27]\n\t"
"v_mfma_f32_16x16x32_bf16 v[24:27], v[240:243], a[200:203], v[24:27]\n\t"
"v_mfma_f32_16x16x32_bf16 v[28:31], v[96:99], a[196:199], v[28:31]\n\t"
"buffer_load_dwordx4 v[96:99], v252, s[8:11], s20 offen offset:1024 sc1\n\t"
"v_mfma_f32_16x16x32_bf16 v[28:31], a[248:251], a[200:203], v[28:31]\n\t"
"s_waitcnt vmcnt(11)\n\t"
"ds_read_b64_tr_b16 v[232:233], v246 offset:7680\n\t"
"v_mfma_f32_16x16x32_bf16 v[28:31], v[240:243], a[204:207], v[28:31]\n\t"
"ds_read_b64_tr_b16 v[234:235], v246 offset:10752\n\t"
"v_mfma_f32_16x16x32_bf16 v[32:35], v[236:239], a[168:171], v[32:35]\n\t"
"buffer_load_dwordx4 a[248:251], v252, s[8:11], s20 offen offset:2048 sc1\n\t"
"v_mfma_f32_16x16x32_bf16 v[32:35], a[252:255], a[172:175], v[32:35]\n\t"
"s_mov_b32 s20, 1585152\n\t"
"v_mfma_f32_16x16x32_bf16 v[32:35], v[248:251], a[176:179], v[32:35]\n\t"
"v_mfma_f32_16x16x32_bf16 v[36:39], v[236:239], a[172:175], v[36:39]\n\t"
"buffer_load_dwordx4 v[240:243], v252, s[8:11], s20 offen offset:0 sc1\n\t"
"v_mfma_f32_16x16x32_bf16 v[36:39], a[252:255], a[176:179], v[36:39]\n\t"
"s_waitcnt lgkmcnt(2)\n\t"
"v_mfma_f32_16x16x32_bf16 v[36:39], v[248:251], a[180:183], v[36:39]\n\t"
"v_mfma_f32_16x16x32_bf16 v[224:227], v[224:227], v[220:223], 0\n\t"
"v_mfma_f32_16x16x32_bf16 v[40:43], v[236:239], a[176:179], v[40:43]\n\t"
"ds_read_b64_tr_b16 v[228:229], v246 offset:13824\n\t"
"v_mfma_f32_16x16x32_bf16 v[40:43], a[252:255], a[180:183], v[40:43]\n\t"
"ds_read_b64_tr_b16 v[230:231], v246 offset:16896\n\t"
"v_mfma_f32_16x16x32_bf16 v[40:43], v[248:251], a[184:187], v[40:43]\n\t"
"v_mfma_f32_16x16x32_bf16 v[44:47], v[236:239], a[180:183], v[44:47]\n\t"
"s_waitcnt lgkmcnt(2)\n\t"
"v_mfma_f32_16x16x32_bf16 v[44:47], a[252:255], a[184:187], v[44:47]\n\t"
"v_cvt_pk_bf16_f32 v224, v224, v225\n\t"
"v_cvt_pk_bf16_f32 v225, v226, v227\n\t"
"v_mfma_f32_16x16x32_bf16 v[44:47], v[248:251], a[188:191], v[44:47]\n\t"
"v_mfma_f32_16x16x32_bf16 v[232:235], v[232:235], v[220:223], 0\n\t"
"v_mfma_f32_16x16x32_bf16 v[48:51], v[236:239], a[184:187], v[48:51]\n\t"
"v_mfma_f32_16x16x32_bf16 v[48:51], a[252:255], a[188:191], v[48:51]\n\t"
"v_mfma_f32_16x16x32_bf16 v[48:51], v[248:251], a[192:195], v[48:51]\n\t"
"v_mfma_f32_16x16x32_bf16 v[52:55], v[236:239], a[188:191], v[52:55]\n\t"
"v_mfma_f32_16x16x32_bf16 v[52:55], a[252:255], a[192:195], v[52:55]\n\t"
"v_cvt_pk_bf16_f32 v226, v232, v233\n\t"
"v_cvt_pk_bf16_f32 v227, v234, v235\n\t"
"v_mfma_f32_16x16x32_bf16 v[52:55], v[248:251], a[196:199], v[52:55]\n\t"
"ds_read_b64_tr_b16 v[232:233], v246 offset:21504\n\t"
"v_mfma_f32_16x16x32_bf16 v[56:59], v[236:239], a[192:195], v[56:59]\n\t"
"s_waitcnt lgkmcnt(1)\n\t"
"ds_read_b64_tr_b16 v[234:235], v246 offset:24576\n\t"
"v_mfma_f32_16x16x32_bf16 v[56:59], a[252:255], a[196:199], v[56:59]\n\t"
"v_mfma_f32_16x16x32_bf16 v[56:59], v[248:251], a[200:203], v[56:59]\n\t"
"v_mfma_f32_16x16x32_bf16 v[228:231], v[228:231], v[220:223], 0\n\t"
"v_mfma_f32_16x16x32_bf16 v[60:63], v[236:239], a[196:199], v[60:63]\n\t"
"buffer_load_dwordx4 v[236:239], v252, s[8:11], s20 offen offset:1024 sc1\n\t"
"v_mfma_f32_16x16x32_bf16 v[60:63], a[252:255], a[200:203], v[60:63]\n\t"
"s_waitcnt vmcnt(11)\n\t"
"v_mfma_f32_16x16x32_bf16 v[60:63], v[248:251], a[204:207], v[60:63]\n\t"
"v_mfma_f32_16x16x32_bf16 v[64:67], a[220:223], a[168:171], v[64:67]\n\t"
"buffer_load_dwordx4 v[248:251], v252, s[8:11], s20 offen offset:2048 sc1\n\t"
"v_mfma_f32_16x16x32_bf16 v[64:67], a[224:227], a[172:175], v[64:67]\n\t"
"s_mov_b32 s20, 1597440\n\t"
"ds_read_b128 a[168:171], v244 offset:33792\n\t"
"v_cvt_pk_bf16_f32 v228, v228, v229\n\t"
"v_mfma_f32_16x16x32_bf16 v[64:67], a[212:215], a[176:179], v[64:67]\n\t"
"v_cvt_pk_bf16_f32 v229, v230, v231\n\t"
"v_mfma_f32_16x16x32_bf16 v[68:71], a[220:223], a[172:175], v[68:71]\n\t"
"buffer_load_dwordx4 a[252:255], v252, s[8:11], s20 offen offset:0 sc1\n\t"
"v_mfma_f32_16x16x32_bf16 v[68:71], a[224:227], a[176:179], v[68:71]\n\t"
"s_waitcnt lgkmcnt(1)\n\t"
"ds_read_b128 a[172:175], v244 offset:34304\n\t"
"v_mfma_f32_16x16x32_bf16 v[68:71], a[212:215], a[180:183], v[68:71]\n\t"
"v_mfma_f32_16x16x32_bf16 v[232:235], v[232:235], v[220:223], 0\n\t"
"v_mfma_f32_16x16x32_bf16 v[72:75], a[220:223], a[176:179], v[72:75]\n\t"
"ds_read_b128 a[176:179], v244 offset:34816\n\t"
"v_mfma_f32_16x16x32_bf16 v[72:75], a[224:227], a[180:183], v[72:75]\n\t"
"v_mfma_f32_16x16x32_bf16 v[72:75], a[212:215], a[184:187], v[72:75]\n\t"
"v_mfma_f32_16x16x32_bf16 v[76:79], a[220:223], a[180:183], v[76:79]\n\t"
"ds_read_b128 a[180:183], v244 offset:35328\n\t"
"v_mfma_f32_16x16x32_bf16 v[76:79], a[224:227], a[184:187], v[76:79]\n\t"
"v_cvt_pk_bf16_f32 v230, v232, v233\n\t"
"v_cvt_pk_bf16_f32 v231, v234, v235\n\t"
"v_mfma_f32_16x16x32_bf16 v[76:79], a[212:215], a[188:191], v[76:79]\n\t"
"ds_read_b64_tr_b16 v[232:233], v246 offset:27648\n\t"
"v_mfma_f32_16x16x32_bf16 v[80:83], a[220:223], a[184:187], v[80:83]\n\t"
"ds_read_b64_tr_b16 v[234:235], v246 offset:30720\n\t"
"v_mfma_f32_16x16x32_bf16 v[80:83], a[224:227], a[188:191], v[80:83]\n\t"
"ds_read_b128 a[184:187], v244 offset:35840\n\t"
"v_mfma_f32_16x16x32_bf16 v[80:83], a[212:215], a[192:195], v[80:83]\n\t"
"v_mfma_f32_16x16x32_bf16 v[84:87], a[220:223], a[188:191], v[84:87]\n\t"
"ds_read_b128 a[188:191], v244 offset:36352\n\t"
"v_mfma_f32_16x16x32_bf16 v[84:87], a[224:227], a[192:195], v[84:87]\n\t"
"v_mfma_f32_16x16x32_bf16 v[84:87], a[212:215], a[196:199], v[84:87]\n\t"
"v_mfma_f32_16x16x32_bf16 v[88:91], a[220:223], a[192:195], v[88:91]\n\t"
"ds_read_b128 a[192:195], v244 offset:36864\n\t"
"v_mfma_f32_16x16x32_bf16 v[88:91], a[224:227], a[196:199], v[88:91]\n\t"
"v_mfma_f32_16x16x32_bf16 v[88:91], a[212:215], a[200:203], v[88:91]\n\t"
"v_mfma_f32_16x16x32_bf16 v[92:95], a[220:223], a[196:199], v[92:95]\n\t"
"buffer_load_dwordx4 a[220:223], v252, s[8:11], s20 offen offset:1024 sc1\n\t"
"v_mfma_f32_16x16x32_bf16 v[92:95], a[224:227], a[200:203], v[92:95]\n\t"
"s_waitcnt vmcnt(11) lgkmcnt(3)\n\t"
"ds_read_b128 a[196:199], v244 offset:37376\n\t"
"v_mfma_f32_16x16x32_bf16 v[92:95], a[212:215], a[204:207], v[92:95]\n\t"
"ds_read_b128 a[200:203], v244 offset:37888\n\t"
"v_mfma_f32_16x16x32_bf16 v[232:235], v[232:235], v[220:223], 0\n\t"
"ds_read_b128 a[204:207], v244 offset:38400\n\t"
"v_mfma_f32_16x16x32_bf16 v[100:103], a[228:231], a[168:171], v[100:103]\n\t"
"buffer_load_dwordx4 a[212:215], v252, s[8:11], s20 offen offset:2048 sc1\n\t"
"v_mfma_f32_16x16x32_bf16 v[100:103], a[232:235], a[172:175], v[100:103]\n\t"
"s_mov_b32 s20, 1609728\n\t"
"v_mfma_f32_16x16x32_bf16 v[100:103], a[216:219], a[176:179], v[100:103]\n\t"
"buffer_load_dwordx4 a[224:227], v252, s[8:11], s20 offen offset:0 sc1\n\t"
"v_mfma_f32_16x16x32_bf16 v[104:107], a[228:231], a[172:175], v[104:107]\n\t"
"s_waitcnt lgkmcnt(5)\n\t"
"v_mfma_f32_16x16x32_bf16 v[104:107], a[232:235], a[176:179], v[104:107]\n\t"
"v_cvt_pk_bf16_f32 v232, v232, v233\n\t"
"v_mfma_f32_16x16x32_bf16 v[104:107], a[216:219], a[180:183], v[104:107]\n\t"
"s_waitcnt lgkmcnt(0)\n\t"
"s_barrier\n\t"
"v_mfma_f32_16x16x32_bf16 v[108:111], a[228:231], a[176:179], v[108:111]\n\t"
"v_cvt_pk_bf16_f32 v233, v234, v235\n\t"
"v_mfma_f32_16x16x32_bf16 v[108:111], a[232:235], a[180:183], v[108:111]\n\t"
"v_fma_f32 v0, v100, s52, v0\n\t"
"v_fma_f32 v1, v101, s52, v1\n\t"
"v_mfma_f32_16x16x32_bf16 v[108:111], a[216:219], a[184:187], v[108:111]\n\t"
"ds_write2st64_b64 v245, v[224:225], v[226:227] offset0:48 offset1:49\n\t"
"v_mfma_f32_16x16x32_bf16 v[112:115], a[228:231], a[180:183], v[112:115]\n\t"
"v_fma_f32 v2, v102, s52, v2\n\t"
"v_fma_f32 v3, v103, s52, v3\n\t"
"v_mfma_f32_16x16x32_bf16 v[112:115], a[232:235], a[184:187], v[112:115]\n\t"
"ds_read_b64_tr_b16 v[224:225], v246 offset:33792\n\t"
"v_mfma_f32_16x16x32_bf16 v[112:115], a[216:219], a[188:191], v[112:115]\n\t"
"ds_write2st64_b64 v245, v[228:229], v[230:231] offset0:50 offset1:51\n\t"
"v_mfma_f32_16x16x32_bf16 v[116:119], a[228:231], a[184:187], v[116:119]\n\t"
"v_fma_f32 v100, v0, s54, -v100\n\t"
"v_fma_f32 v101, v1, s54, -v101\n\t"
"v_mfma_f32_16x16x32_bf16 v[116:119], a[232:235], a[188:191], v[116:119]\n\t"
"ds_read_b64_tr_b16 v[226:227], v246 offset:36864\n\t"
"v_mfma_f32_16x16x32_bf16 v[116:119], a[216:219], a[192:195], v[116:119]\n\t"
"ds_write_b64 v245, v[232:233] offset:26624\n\t"
"v_mfma_f32_16x16x32_bf16 v[120:123], a[228:231], a[188:191], v[120:123]\n\t"
"ds_read_b64_tr_b16 v[228:229], v246 offset:41472\n\t"
"v_fma_f32 v102, v2, s54, -v102\n\t"
"v_mfma_f32_16x16x32_bf16 v[120:123], a[232:235], a[192:195], v[120:123]\n\t"
"ds_read_b64_tr_b16 v[230:231], v246 offset:44544\n\t"
"v_fma_f32 v103, v3, s54, -v103\n\t"
"v_mfma_f32_16x16x32_bf16 v[120:123], a[216:219], a[196:199], v[120:123]\n\t"
"s_waitcnt lgkmcnt(3)\n\t"
"ds_read_b64_tr_b16 v[232:233], v246 offset:47616\n\t"
"v_fma_f32 v4, v104, s52, v4\n\t"
"v_mfma_f32_16x16x32_bf16 v[224:227], v[224:227], v[220:223], 0\n\t"
"ds_read_b64_tr_b16 v[234:235], v246 offset:50688\n\t"
"v_fma_f32 v5, v105, s52, v5\n\t"
"v_mfma_f32_16x16x32_bf16 v[124:127], a[228:231], a[192:195], v[124:127]\n\t"
"v_fma_f32 v6, v106, s52, v6\n\t"
"v_fma_f32 v7, v107, s52, v7\n\t"
"v_mfma_f32_16x16x32_bf16 v[124:127], a[232:235], a[196:199], v[124:127]\n\t"
"v_fma_f32 v104, v4, s54, -v104\n\t"
"v_fma_f32 v105, v5, s54, -v105\n\t"
"v_mfma_f32_16x16x32_bf16 v[124:127], a[216:219], a[200:203], v[124:127]\n\t"
"s_waitcnt lgkmcnt(2)\n\t"
"v_fma_f32 v106, v6, s54, -v106\n\t"
"v_fma_f32 v107, v7, s54, -v107\n\t"
"v_mfma_f32_16x16x32_bf16 v[228:231], v[228:231], v[220:223], 0\n\t"
"v_fma_f32 v8, v108, s52, v8\n\t"
"v_fma_f32 v9, v109, s52, v9\n\t"
"v_mfma_f32_16x16x32_bf16 v[128:131], a[228:231], a[196:199], v[128:131]\n\t"
"s_waitcnt lgkmcnt(0)\n\t"
"v_cvt_pk_bf16_f32 v224, v224, v225\n\t"
"v_cvt_pk_bf16_f32 v225, v226, v227\n\t"
"v_mfma_f32_16x16x32_bf16 v[128:131], a[232:235], a[200:203], v[128:131]\n\t"
"s_waitcnt vmcnt(10)\n\t"
"v_fma_f32 v10, v110, s52, v10\n\t"
"v_fma_f32 v11, v111, s52, v11\n\t"
"v_mfma_f32_16x16x32_bf16 v[128:131], a[216:219], a[204:207], v[128:131]\n\t"
"v_fma_f32 v108, v8, s54, -v108\n\t"
"v_fma_f32 v109, v9, s54, -v109\n\t"
"v_mfma_f32_16x16x32_bf16 v[232:235], v[232:235], v[220:223], 0\n\t"
"buffer_load_dwordx4 a[228:231], v252, s[8:11], s20 offen offset:1024 sc1\n\t"
"v_mfma_f32_16x16x32_bf16 v[132:135], a[236:239], a[168:171], v[132:135]\n\t"
"buffer_load_dwordx4 a[216:219], v252, s[8:11], s20 offen offset:2048 sc1\n\t"
"v_mfma_f32_16x16x32_bf16 v[132:135], a[240:243], a[172:175], v[132:135]\n\t"
"s_mov_b32 s20, 1622016\n\t"
"v_cvt_pk_bf16_f32 v226, v228, v229\n\t"
"v_cvt_pk_bf16_f32 v227, v230, v231\n\t"
"v_mfma_f32_16x16x32_bf16 v[132:135], a[208:211], a[176:179], v[132:135]\n\t"
"ds_write2st64_b64 v245, v[224:225], v[226:227] offset0:53 offset1:54\n\t"
"v_mfma_f32_16x16x32_bf16 v[136:139], a[236:239], a[172:175], v[136:139]\n\t"
"buffer_load_dwordx4 a[232:235], v252, s[8:11], s20 offen offset:0 sc1\n\t"
"v_mfma_f32_16x16x32_bf16 v[136:139], a[240:243], a[176:179], v[136:139]\n\t"
"ds_read_b64_tr_b16 v[228:229], v246 offset:53760\n\t"
"v_mfma_f32_16x16x32_bf16 v[136:139], a[208:211], a[180:183], v[136:139]\n\t"
"ds_read_b64_tr_b16 v[230:231], v246 offset:56832\n\t"
"v_cvt_pk_bf16_f32 v232, v232, v233\n\t"
"v_mfma_f32_16x16x32_bf16 v[140:143], a[236:239], a[176:179], v[140:143]\n\t"
"v_cvt_pk_bf16_f32 v233, v234, v235\n\t"
"v_fma_f32 v110, v10, s54, -v110\n\t"
"v_mfma_f32_16x16x32_bf16 v[140:143], a[240:243], a[180:183], v[140:143]\n\t"
"v_fma_f32 v111, v11, s54, -v111\n\t"
"v_fma_f32 v12, v112, s52, v12\n\t"
"v_mfma_f32_16x16x32_bf16 v[140:143], a[208:211], a[184:187], v[140:143]\n\t"
"v_fma_f32 v13, v113, s52, v13\n\t"
"v_fma_f32 v14, v114, s52, v14\n\t"
"v_mfma_f32_16x16x32_bf16 v[144:147], a[236:239], a[180:183], v[144:147]\n\t"
"v_fma_f32 v15, v115, s52, v15\n\t"
"v_fma_f32 v112, v12, s54, -v112\n\t"
"v_mfma_f32_16x16x32_bf16 v[144:147], a[240:243], a[184:187], v[144:147]\n\t"
"v_fma_f32 v113, v13, s54, -v113\n\t"
"v_fma_f32 v114, v14, s54, -v114\n\t"
"v_mfma_f32_16x16x32_bf16 v[144:147], a[208:211], a[188:191], v[144:147]\n\t"
"v_fma_f32 v115, v15, s54, -v115\n\t"
"v_fma_f32 v16, v116, s52, v16\n\t"
"v_mfma_f32_16x16x32_bf16 v[148:151], a[236:239], a[184:187], v[148:151]\n\t"
"s_waitcnt lgkmcnt(0)\n\t"
"v_fma_f32 v17, v117, s52, v17\n\t"
"v_fma_f32 v18, v118, s52, v18\n\t"
"v_mfma_f32_16x16x32_bf16 v[148:151], a[240:243], a[188:191], v[148:151]\n\t"
"v_fma_f32 v19, v119, s52, v19\n\t"
"v_fma_f32 v116, v16, s54, -v116\n\t"
"v_mfma_f32_16x16x32_bf16 v[148:151], a[208:211], a[192:195], v[148:151]\n\t"
"v_fma_f32 v117, v17, s54, -v117\n\t"
"v_fma_f32 v118, v18, s54, -v118\n\t"
"v_mfma_f32_16x16x32_bf16 v[228:231], v[228:231], v[220:223], 0\n\t"
"v_fma_f32 v119, v19, s54, -v119\n\t"
"v_fma_f32 v20, v120, s52, v20\n\t"
"v_mfma_f32_16x16x32_bf16 v[152:155], a[236:239], a[188:191], v[152:155]\n\t"
"v_fma_f32 v21, v121, s52, v21\n\t"
"v_fma_f32 v22, v122, s52, v22\n\t"
"v_mfma_f32_16x16x32_bf16 v[152:155], a[240:243], a[192:195], v[152:155]\n\t"
"v_fma_f32 v23, v123, s52, v23\n\t"
"v_fma_f32 v120, v20, s54, -v120\n\t"
"v_mfma_f32_16x16x32_bf16 v[152:155], a[208:211], a[196:199], v[152:155]\n\t"
"v_fma_f32 v121, v21, s54, -v121\n\t"
"v_fma_f32 v122, v22, s54, -v122\n\t"
"v_mfma_f32_16x16x32_bf16 v[156:159], a[236:239], a[192:195], v[156:159]\n\t"
"v_fma_f32 v123, v23, s54, -v123\n\t"
"v_fma_f32 v24, v124, s52, v24\n\t"
"v_mfma_f32_16x16x32_bf16 v[156:159], a[240:243], a[196:199], v[156:159]\n\t"
"v_cvt_pk_bf16_f32 v234, v228, v229\n\t"
"v_cvt_pk_bf16_f32 v235, v230, v231\n\t"
"v_mfma_f32_16x16x32_bf16 v[156:159], a[208:211], a[200:203], v[156:159]\n\t"
"ds_write2st64_b64 v245, v[232:233], v[234:235] offset0:55 offset1:56\n\t"
"v_mfma_f32_16x16x32_bf16 v[160:163], a[236:239], a[196:199], v[160:163]\n\t"
"buffer_load_dwordx4 a[236:239], v252, s[8:11], s20 offen offset:1024 sc1\n\t"
"v_mfma_f32_16x16x32_bf16 v[160:163], a[240:243], a[200:203], v[160:163]\n\t"
"s_waitcnt vmcnt(11)\n\t"
"v_fma_f32 v25, v125, s52, v25\n\t"
"v_fma_f32 v26, v126, s52, v26\n\t"
"v_mfma_f32_16x16x32_bf16 v[160:163], a[208:211], a[204:207], v[160:163]\n\t"
"v_fma_f32 v27, v127, s52, v27\n\t"
"v_fma_f32 v124, v24, s54, -v124\n\t"
"v_mfma_f32_16x16x32_bf16 v[164:167], a[244:247], a[168:171], v[164:167]\n\t"
"v_fma_f32 v125, v25, s54, -v125\n\t"
"v_fma_f32 v126, v26, s54, -v126\n\t"
"v_mfma_f32_16x16x32_bf16 v[164:167], v[96:99], a[172:175], v[164:167]\n\t"
"v_fma_f32 v127, v27, s54, -v127\n\t"
"v_mfma_f32_16x16x32_bf16 v[164:167], a[248:251], a[176:179], v[164:167]\n\t"
"s_waitcnt lgkmcnt(0)\n\t"
"s_barrier\n\t"
"v_mfma_f32_16x16x32_bf16 v[168:171], a[244:247], a[172:175], v[168:171]\n\t"
"ds_read_b128 a[168:171], v244 offset:24576\n\t"
"v_mfma_f32_16x16x32_bf16 v[168:171], v[96:99], a[176:179], v[168:171]\n\t"
"buffer_load_dwordx4 a[208:211], v252, s[8:11], s20 offen offset:2048 sc1\n\t"
"v_mfma_f32_16x16x32_bf16 v[168:171], a[248:251], a[180:183], v[168:171]\n\t"
"s_mov_b32 s20, 1634304\n\t"
"ds_read_b128 a[172:175], v244 offset:25088\n\t"
"v_fma_f32 v28, v128, s52, v28\n\t"
"v_mfma_f32_16x16x32_bf16 v[172:175], a[244:247], a[176:179], v[172:175]\n\t"
"ds_read_b128 a[176:179], v244 offset:25600\n\t"
"v_fma_f32 v29, v129, s52, v29\n\t"
"v_mfma_f32_16x16x32_bf16 v[172:175], v[96:99], a[180:183], v[172:175]\n\t"
"v_fma_f32 v30, v130, s52, v30\n\t"
"v_fma_f32 v31, v131, s52, v31\n\t"
"v_mfma_f32_16x16x32_bf16 v[172:175], a[248:251], a[184:187], v[172:175]\n\t"
"buffer_load_dwordx4 a[240:243], v252, s[8:11], s20 offen offset:0 sc1\n\t"
"v_mfma_f32_16x16x32_bf16 v[176:179], a[244:247], a[180:183], v[176:179]\n\t"
"ds_read_b128 a[180:183], v244 offset:26112\n\t"
"v_fma_f32 v128, v28, s54, -v128\n\t"
"v_mfma_f32_16x16x32_bf16 v[176:179], v[96:99], a[184:187], v[176:179]\n\t"
"v_fma_f32 v129, v29, s54, -v129\n\t"
"v_fma_f32 v130, v30, s54, -v130\n\t"
"v_mfma_f32_16x16x32_bf16 v[176:179], a[248:251], a[188:191], v[176:179]\n\t"
"v_fma_f32 v131, v31, s54, -v131\n\t"
"v_fma_f32 v32, v132, s52, v32\n\t"
"v_mfma_f32_16x16x32_bf16 v[180:183], a[244:247], a[184:187], v[180:183]\n\t"
"ds_read_b128 a[184:187], v244 offset:26624\n\t"
"v_fma_f32 v33, v133, s52, v33\n\t"
"v_mfma_f32_16x16x32_bf16 v[180:183], v[96:99], a[188:191], v[180:183]\n\t"
"v_fma_f32 v34, v134, s52, v34\n\t"
"v_fma_f32 v35, v135, s52, v35\n\t"
"v_mfma_f32_16x16x32_bf16 v[180:183], a[248:251], a[192:195], v[180:183]\n\t"
"v_fma_f32 v132, v32, s54, -v132\n\t"
"v_fma_f32 v133, v33, s54, -v133\n\t"
"v_mfma_f32_16x16x32_bf16 v[184:187], a[244:247], a[188:191], v[184:187]\n\t"
"ds_read_b128 a[188:191], v244 offset:27136\n\t"
"v_fma_f32 v134, v34, s54, -v134\n\t"
"v_mfma_f32_16x16x32_bf16 v[184:187], v[96:99], a[192:195], v[184:187]\n\t"
"v_fma_f32 v135, v35, s54, -v135\n\t"
"v_fma_f32 v36, v136, s52, v36\n\t"
"v_mfma_f32_16x16x32_bf16 v[184:187], a[248:251], a[196:199], v[184:187]\n\t"
"v_fma_f32 v37, v137, s52, v37\n\t"
"v_fma_f32 v38, v138, s52, v38\n\t"
"v_mfma_f32_16x16x32_bf16 v[188:191], a[244:247], a[192:195], v[188:191]\n\t"
"ds_read_b128 a[192:195], v244 offset:27648\n\t"
"v_fma_f32 v39, v139, s52, v39\n\t"
"v_mfma_f32_16x16x32_bf16 v[188:191], v[96:99], a[196:199], v[188:191]\n\t"
"v_fma_f32 v136, v36, s54, -v136\n\t"
"v_fma_f32 v137, v37, s54, -v137\n\t"
"v_mfma_f32_16x16x32_bf16 v[188:191], a[248:251], a[200:203], v[188:191]\n\t"
"v_fma_f32 v138, v38, s54, -v138\n\t"
"v_fma_f32 v139, v39, s54, -v139\n\t"
"v_mfma_f32_16x16x32_bf16 v[192:195], a[244:247], a[196:199], v[192:195]\n\t"
"s_waitcnt vmcnt(10) lgkmcnt(4)\n\t"
"ds_read_b128 a[196:199], v244 offset:28160\n\t"
"v_fma_f32 v40, v140, s52, v40\n\t"
"v_mfma_f32_16x16x32_bf16 v[192:195], v[96:99], a[200:203], v[192:195]\n\t"
"ds_read_b128 a[200:203], v244 offset:28672\n\t"
"v_fma_f32 v41, v141, s52, v41\n\t"
"v_mfma_f32_16x16x32_bf16 v[192:195], a[248:251], a[204:207], v[192:195]\n\t"
"ds_read_b128 a[204:207], v244 offset:29184\n\t"
"v_fma_f32 v42, v142, s52, v42\n\t"
"Lst31_%=:\n\t"
"v_mfma_f32_16x16x32_bf16 v[196:199], v[240:243], a[168:171], v[196:199]\n\t"
"s_waitcnt lgkmcnt(6)\n\t"
"v_fma_f32 v43, v143, s52, v43\n\t"
"v_fma_f32 v140, v40, s54, -v140\n\t"
"v_mfma_f32_16x16x32_bf16 v[196:199], v[236:239], a[172:175], v[196:199]\n\t"
"v_fma_f32 v141, v41, s54, -v141\n\t"
"v_fma_f32 v142, v42, s54, -v142\n\t"
"v_mfma_f32_16x16x32_bf16 v[196:199], v[248:251], a[176:179], v[196:199]\n\t"
"buffer_load_dwordx4 a[244:247], v252, s[8:11], s20 offen offset:1024 sc1\n\t"
"v_mfma_f32_16x16x32_bf16 v[204:207], v[240:243], a[172:175], v[204:207]\n\t"
"buffer_load_dwordx4 a[248:251], v252, s[8:11], s20 offen offset:2048 sc1\n\t"
"v_mfma_f32_16x16x32_bf16 v[204:207], v[236:239], a[176:179], v[204:207]\n\t"
"s_waitcnt lgkmcnt(5)\n\t"
"v_fma_f32 v143, v43, s54, -v143\n\t"
"v_fma_f32 v44, v144, s52, v44\n\t"
"v_mfma_f32_16x16x32_bf16 v[204:207], v[248:251], a[180:183], v[204:207]\n\t"
"v_fma_f32 v45, v145, s52, v45\n\t"
"v_fma_f32 v46, v146, s52, v46\n\t"
"v_mfma_f32_16x16x32_bf16 v[212:215], v[240:243], a[176:179], v[212:215]\n\t"
"s_waitcnt lgkmcnt(4)\n\t"
"v_fma_f32 v47, v147, s52, v47\n\t"
"v_fma_f32 v144, v44, s54, -v144\n\t"
"v_mfma_f32_16x16x32_bf16 v[212:215], v[236:239], a[180:183], v[212:215]\n\t"
"v_fma_f32 v145, v45, s54, -v145\n\t"
"v_fma_f32 v146, v46, s54, -v146\n\t"
"v_mfma_f32_16x16x32_bf16 v[212:215], v[248:251], a[184:187], v[212:215]\n\t"
"v_fma_f32 v147, v47, s54, -v147\n\t"
"v_fma_f32 v48, v148, s52, v48\n\t"
"v_mfma_f32_16x16x32_bf16 a[0:3], v[240:243], a[180:183], a[0:3]\n\t"
"s_waitcnt lgkmcnt(3)\n\t"
"v_fma_f32 v49, v149, s52, v49\n\t"
"v_fma_f32 v50, v150, s52, v50\n\t"
"v_mfma_f32_16x16x32_bf16 a[0:3], v[236:239], a[184:187], a[0:3]\n\t"
"v_fma_f32 v51, v151, s52, v51\n\t"
"v_fma_f32 v148, v48, s54, -v148\n\t"
"v_mfma_f32_16x16x32_bf16 a[0:3], v[248:251], a[188:191], a[0:3]\n\t"
"v_fma_f32 v149, v49, s54, -v149\n\t"
"v_fma_f32 v150, v50, s54, -v150\n\t"
"v_mfma_f32_16x16x32_bf16 a[8:11], v[240:243], a[184:187], a[8:11]\n\t"
"s_waitcnt lgkmcnt(2)\n\t"
"v_fma_f32 v151, v51, s54, -v151\n\t"
"v_fma_f32 v52, v152, s52, v52\n\t"
"v_mfma_f32_16x16x32_bf16 a[8:11], v[236:239], a[188:191], a[8:11]\n\t"
"v_fma_f32 v53, v153, s52, v53\n\t"
"v_fma_f32 v54, v154, s52, v54\n\t"
"v_mfma_f32_16x16x32_bf16 a[8:11], v[248:251], a[192:195], a[8:11]\n\t"
"v_fma_f32 v55, v155, s52, v55\n\t"
"v_fma_f32 v152, v52, s54, -v152\n\t"
"v_mfma_f32_16x16x32_bf16 a[16:19], v[240:243], a[188:191], a[16:19]\n\t"
"s_waitcnt lgkmcnt(1)\n\t"
"v_fma_f32 v153, v53, s54, -v153\n\t"
"v_fma_f32 v154, v54, s54, -v154\n\t"
"v_mfma_f32_16x16x32_bf16 a[16:19], v[236:239], a[192:195], a[16:19]\n\t"
"s_mov_b32 s20, 1646592\n\t"
"v_fma_f32 v155, v55, s54, -v155\n\t"
"v_fma_f32 v56, v156, s52, v56\n\t"
"v_mfma_f32_16x16x32_bf16 a[16:19], v[248:251], a[196:199], a[16:19]\n\t"
"v_fma_f32 v57, v157, s52, v57\n\t"
"v_fma_f32 v58, v158, s52, v58\n\t"
"v_mfma_f32_16x16x32_bf16 a[24:27], v[240:243], a[192:195], a[24:27]\n\t"
"s_waitcnt lgkmcnt(0)\n\t"
"v_fma_f32 v59, v159, s52, v59\n\t"
"v_fma_f32 v156, v56, s54, -v156\n\t"
"v_mfma_f32_16x16x32_bf16 a[24:27], v[236:239], a[196:199], a[24:27]\n\t"
"v_fma_f32 v157, v57, s54, -v157\n\t"
"v_fma_f32 v158, v58, s54, -v158\n\t"
"v_mfma_f32_16x16x32_bf16 a[24:27], v[248:251], a[200:203], a[24:27]\n\t"
"v_fma_f32 v159, v59, s54, -v159\n\t"
"v_fma_f32 v60, v160, s52, v60\n\t"
"v_mfma_f32_16x16x32_bf16 a[32:35], v[240:243], a[196:199], a[32:35]\n\t"
"s_waitcnt vmcnt(9)\n\t"
"v_fma_f32 v61, v161, s52, v61\n\t"
"v_fma_f32 v62, v162, s52, v62\n\t"
"v_mfma_f32_16x16x32_bf16 a[32:35], v[236:239], a[200:203], a[32:35]\n\t"
"v_fma_f32 v63, v163, s52, v63\n\t"
"v_fma_f32 v160, v60, s54, -v160\n\t"
"v_mfma_f32_16x16x32_bf16 a[32:35], v[248:251], a[204:207], a[32:35]\n\t"
"v_fma_f32 v161, v61, s54, -v161\n\t"
"v_fma_f32 v162, v62, s54, -v162\n\t"
"v_mfma_f32_16x16x32_bf16 a[40:43], a[252:255], a[168:171], a[40:43]\n\t"
"v_fma_f32 v163, v63, s54, -v163\n\t"
"v_fma_f32 v64, v164, s52, v64\n\t"
"v_mfma_f32_16x16x32_bf16 a[40:43], a[220:223], a[172:175], a[40:43]\n\t"
"v_fma_f32 v65, v165, s52, v65\n\t"
"v_fma_f32 v66, v166, s52, v66\n\t"
"v_mfma_f32_16x16x32_bf16 a[40:43], a[212:215], a[176:179], a[40:43]\n\t"
"buffer_load_dwordx4 v[240:243], v252, s[8:11], s20 offen offset:0 sc1\n\t"
"v_mfma_f32_16x16x32_bf16 a[48:51], a[252:255], a[172:175], a[48:51]\n\t"
"v_fma_f32 v67, v167, s52, v67\n\t"
"v_fma_f32 v164, v64, s54, -v164\n\t"
"v_mfma_f32_16x16x32_bf16 a[48:51], a[220:223], a[176:179], a[48:51]\n\t"
"v_fma_f32 v165, v65, s54, -v165\n\t"
"v_fma_f32 v166, v66, s54, -v166\n\t"
"v_mfma_f32_16x16x32_bf16 a[48:51], a[212:215], a[180:183], a[48:51]\n\t"
"v_fma_f32 v167, v67, s54, -v167\n\t"
"v_fma_f32 v68, v168, s52, v68\n\t"
"v_mfma_f32_16x16x32_bf16 a[56:59], a[252:255], a[176:179], a[56:59]\n\t"
"v_fma_f32 v69, v169, s52, v69\n\t"
"v_fma_f32 v70, v170, s52, v70\n\t"
"v_mfma_f32_16x16x32_bf16 a[56:59], a[220:223], a[180:183], a[56:59]\n\t"
"v_fma_f32 v71, v171, s52, v71\n\t"
"v_fma_f32 v168, v68, s54, -v168\n\t"
"v_mfma_f32_16x16x32_bf16 a[56:59], a[212:215], a[184:187], a[56:59]\n\t"
"v_fma_f32 v169, v69, s54, -v169\n\t"
"v_fma_f32 v170, v70, s54, -v170\n\t"
"v_mfma_f32_16x16x32_bf16 a[64:67], a[252:255], a[180:183], a[64:67]\n\t"
"v_fma_f32 v171, v71, s54, -v171\n\t"
"v_fma_f32 v72, v172, s52, v72\n\t"
"v_mfma_f32_16x16x32_bf16 a[64:67], a[220:223], a[184:187], a[64:67]\n\t"
"v_fma_f32 v73, v173, s52, v73\n\t"
"v_fma_f32 v74, v174, s52, v74\n\t"
"v_mfma_f32_16x16x32_bf16 a[64:67], a[212:215], a[188:191], a[64:67]\n\t"
"v_fma_f32 v75, v175, s52, v75\n\t"
"v_fma_f32 v172, v72, s54, -v172\n\t"
"v_mfma_f32_16x16x32_bf16 a[72:75], a[252:255], a[184:187], a[72:75]\n\t"
"v_fma_f32 v173, v73, s54, -v173\n\t"
"v_fma_f32 v174, v74, s54, -v174\n\t"
"v_mfma_f32_16x16x32_bf16 a[72:75], a[220:223], a[188:191], a[72:75]\n\t"
"v_fma_f32 v175, v75, s54, -v175\n\t"
"v_fma_f32 v76, v176, s52, v76\n\t"
"v_mfma_f32_16x16x32_bf16 a[72:75], a[212:215], a[192:195], a[72:75]\n\t"
"v_fma_f32 v77, v177, s52, v77\n\t"
"v_fma_f32 v78, v178, s52, v78\n\t"
"v_mfma_f32_16x16x32_bf16 a[80:83], a[252:255], a[188:191], a[80:83]\n\t"
"v_fma_f32 v79, v179, s52, v79\n\t"
"v_fma_f32 v176, v76, s54, -v176\n\t"
"v_mfma_f32_16x16x32_bf16 a[80:83], a[220:223], a[192:195], a[80:83]\n\t"
"v_fma_f32 v177, v77, s54, -v177\n\t"
"v_fma_f32 v178, v78, s54, -v178\n\t"
"v_mfma_f32_16x16x32_bf16 a[80:83], a[212:215], a[196:199], a[80:83]\n\t"
"v_fma_f32 v179, v79, s54, -v179\n\t"
"v_fma_f32 v80, v180, s52, v80\n\t"
"v_mfma_f32_16x16x32_bf16 a[88:91], a[252:255], a[192:195], a[88:91]\n\t"
"v_fma_f32 v81, v181, s52, v81\n\t"
"v_fma_f32 v82, v182, s52, v82\n\t"
"v_mfma_f32_16x16x32_bf16 a[88:91], a[220:223], a[196:199], a[88:91]\n\t"
"v_fma_f32 v83, v183, s52, v83\n\t"
"v_fma_f32 v180, v80, s54, -v180\n\t"
"v_mfma_f32_16x16x32_bf16 a[88:91], a[212:215], a[200:203], a[88:91]\n\t"
"v_fma_f32 v181, v81, s54, -v181\n\t"
"v_fma_f32 v182, v82, s54, -v182\n\t"
"v_mfma_f32_16x16x32_bf16 a[96:99], a[252:255], a[196:199], a[96:99]\n\t"
"s_waitcnt vmcnt(7)\n\t"
"v_fma_f32 v183, v83, s54, -v183\n\t"
"v_fma_f32 v84, v184, s52, v84\n\t"
"v_mfma_f32_16x16x32_bf16 a[96:99], a[220:223], a[200:203], a[96:99]\n\t"
"v_fma_f32 v85, v185, s52, v85\n\t"
"v_fma_f32 v86, v186, s52, v86\n\t"
"v_mfma_f32_16x16x32_bf16 a[96:99], a[212:215], a[204:207], a[96:99]\n\t"
"v_fma_f32 v87, v187, s52, v87\n\t"
"v_fma_f32 v184, v84, s54, -v184\n\t"
"v_mfma_f32_16x16x32_bf16 a[104:107], a[224:227], a[168:171], a[104:107]\n\t"
"ds_read_b128 a[168:171], v244 offset:33792\n\t"
"v_fma_f32 v185, v85, s54, -v185\n\t"
"v_mfma_f32_16x16x32_bf16 a[104:107], a[228:231], a[172:175], a[104:107]\n\t"
"v_fma_f32 v186, v86, s54, -v186\n\t"
"v_fma_f32 v187, v87, s54, -v187\n\t"
"v_mfma_f32_16x16x32_bf16 a[104:107], a[216:219], a[176:179], a[104:107]\n\t"
"buffer_load_dwordx4 a[252:255], v252, s[8:11], s20 offen offset:1024 sc1\n\t"
"v_mfma_f32_16x16x32_bf16 a[112:115], a[224:227], a[172:175], a[112:115]\n\t"
"buffer_load_dwordx4 a[212:215], v252, s[8:11], s20 offen offset:2048 sc1\n\t"
"v_mfma_f32_16x16x32_bf16 a[112:115], a[228:231], a[176:179], a[112:115]\n\t"
"s_mov_b32 s20, 1179648\n\t"
"ds_read_b128 a[172:175], v244 offset:34304\n\t"
"v_fma_f32 v88, v188, s52, v88\n\t"
"v_mfma_f32_16x16x32_bf16 a[112:115], a[216:219], a[180:183], a[112:115]\n\t"
"v_fma_f32 v89, v189, s52, v89\n\t"
"v_fma_f32 v90, v190, s52, v90\n\t"
"v_mfma_f32_16x16x32_bf16 a[120:123], a[224:227], a[176:179], a[120:123]\n\t"
"ds_read_b128 a[176:179], v244 offset:34816\n\t"
"v_fma_f32 v91, v191, s52, v91\n\t"
"v_mfma_f32_16x16x32_bf16 a[120:123], a[228:231], a[180:183], a[120:123]\n\t"
"v_fma_f32 v188, v88, s54, -v188\n\t"
"v_fma_f32 v189, v89, s54, -v189\n\t"
"v_mfma_f32_16x16x32_bf16 a[120:123], a[216:219], a[184:187], a[120:123]\n\t"
"buffer_load_dwordx4 a[220:223], v252, s[8:11], s20 offen offset:0 sc1\n\t"
"v_mfma_f32_16x16x32_bf16 a[128:131], a[224:227], a[180:183], a[128:131]\n\t"
"ds_read_b128 a[180:183], v244 offset:35328\n\t"
"v_fma_f32 v190, v90, s54, -v190\n\t"
"v_mfma_f32_16x16x32_bf16 a[128:131], a[228:231], a[184:187], a[128:131]\n\t"
"v_fma_f32 v191, v91, s54, -v191\n\t"
"v_fma_f32 v92, v192, s52, v92\n\t"
"v_mfma_f32_16x16x32_bf16 a[128:131], a[216:219], a[188:191], a[128:131]\n\t"
"v_fma_f32 v93, v193, s52, v93\n\t"
"v_fma_f32 v94, v194, s52, v94\n\t"
"v_mfma_f32_16x16x32_bf16 a[136:139], a[224:227], a[184:187], a[136:139]\n\t"
"ds_read_b128 a[184:187], v244 offset:35840\n\t"
"v_fma_f32 v95, v195, s52, v95\n\t"
"v_mfma_f32_16x16x32_bf16 a[136:139], a[228:231], a[188:191], a[136:139]\n\t"
"v_fma_f32 v192, v92, s54, -v192\n\t"
"v_fma_f32 v193, v93, s54, -v193\n\t"
"v_mfma_f32_16x16x32_bf16 a[136:139], a[216:219], a[192:195], a[136:139]\n\t"
"v_fma_f32 v194, v94, s54, -v194\n\t"
"v_fma_f32 v195, v95, s54, -v195\n\t"
"v_mfma_f32_16x16x32_bf16 a[144:147], a[224:227], a[188:191], a[144:147]\n\t"
"ds_read_b128 a[188:191], v244 offset:36352\n\t"
"v_mfma_f32_16x16x32_bf16 a[144:147], a[228:231], a[192:195], a[144:147]\n\t"
"v_mfma_f32_16x16x32_bf16 a[144:147], a[216:219], a[196:199], a[144:147]\n\t"
"v_mfma_f32_16x16x32_bf16 a[152:155], a[224:227], a[192:195], a[152:155]\n\t"
"ds_read_b128 a[192:195], v244 offset:36864\n\t"
"v_mfma_f32_16x16x32_bf16 a[152:155], a[228:231], a[196:199], a[152:155]\n\t"
"v_mfma_f32_16x16x32_bf16 a[152:155], a[216:219], a[200:203], a[152:155]\n\t"
"v_mfma_f32_16x16x32_bf16 a[160:163], a[224:227], a[196:199], a[160:163]\n\t"
"buffer_load_dwordx4 a[224:227], v252, s[8:11], s20 offen offset:1024 sc1\n\t"
"v_mfma_f32_16x16x32_bf16 a[160:163], a[228:231], a[200:203], a[160:163]\n\t"
"s_waitcnt vmcnt(8) lgkmcnt(4)\n\t"
"ds_read_b128 a[196:199], v244 offset:37376\n\t"
"v_mfma_f32_16x16x32_bf16 a[160:163], a[216:219], a[204:207], a[160:163]\n\t"
"ds_read_b128 a[200:203], v244 offset:37888\n\t"
"v_mfma_f32_16x16x32_bf16 v[200:203], a[232:235], a[168:171], v[200:203]\n\t"
"s_waitcnt lgkmcnt(5)\n\t"
"ds_read_b128 a[204:207], v244 offset:38400\n\t"
"v_mfma_f32_16x16x32_bf16 v[200:203], a[236:239], a[172:175], v[200:203]\n\t"
"buffer_load_dwordx4 a[216:219], v252, s[8:11], s20 offen offset:2048 sc1\n\t"
"v_mfma_f32_16x16x32_bf16 v[200:203], a[208:211], a[176:179], v[200:203]\n\t"
"s_mov_b32 s20, 1191936\n\t"
"v_mfma_f32_16x16x32_bf16 v[208:211], a[232:235], a[172:175], v[208:211]\n\t"
"buffer_load_dwordx4 a[228:231], v252, s[8:11], s20 offen offset:0 sc1\n\t"
"v_mfma_f32_16x16x32_bf16 v[208:211], a[236:239], a[176:179], v[208:211]\n\t"
"s_waitcnt lgkmcnt(5)\n\t"
"v_mfma_f32_16x16x32_bf16 v[208:211], a[208:211], a[180:183], v[208:211]\n\t"
"s_waitcnt lgkmcnt(0)\n\t"
"s_barrier\n\t"
"v_mfma_f32_16x16x32_bf16 v[216:219], a[232:235], a[176:179], v[216:219]\n\t"
"v_mfma_f32_16x16x32_bf16 v[216:219], a[236:239], a[180:183], v[216:219]\n\t"
"v_mfma_f32_16x16x32_bf16 v[216:219], a[208:211], a[184:187], v[216:219]\n\t"
"v_pk_fma_f32 v[196:197], v[200:201], s[56:57], v[196:197]\n\t"
"v_pk_fma_f32 v[200:201], v[196:197], s[58:59], v[200:201] neg_lo:[0,0,1] neg_hi:[0,0,1]\n\t"
"v_pk_fma_f32 v[228:229], v[0:1], s[60:61], v[196:197]\n\t"
"v_pk_fma_f32 v[0:1], v[196:197], s[60:61], v[0:1]\n\t"
"v_pk_fma_f32 v[196:197], v[100:101], s[62:63], v[200:201]\n\t"
"v_pk_fma_f32 v[100:101], v[200:201], s[62:63], v[100:101]\n\t"
"v_pk_fma_f32 v[198:199], v[202:203], s[56:57], v[198:199]\n\t"
"v_pk_fma_f32 v[202:203], v[198:199], s[58:59], v[202:203] neg_lo:[0,0,1] neg_hi:[0,0,1]\n\t"
"v_pk_fma_f32 v[234:235], v[2:3], s[60:61], v[198:199]\n\t"
"v_pk_fma_f32 v[2:3], v[198:199], s[60:61], v[2:3]\n\t"
"v_pk_fma_f32 v[198:199], v[102:103], s[62:63], v[202:203]\n\t"
"v_pk_fma_f32 v[102:103], v[202:203], s[62:63], v[102:103]\n\t"
"v_mfma_f32_16x16x32_bf16 a[4:7], a[232:235], a[180:183], a[4:7]\n\t"
"v_cvt_pk_bf16_f32 v196, v196, v197\n\t"
"v_cvt_pk_bf16_f32 v200, v228, v229\n\t"
"v_mfma_f32_16x16x32_bf16 a[4:7], a[236:239], a[184:187], a[4:7]\n\t"
"v_cvt_pk_bf16_f32 v197, v198, v199\n\t"
"v_cvt_pk_bf16_f32 v201, v234, v235\n\t"
"v_mfma_f32_16x16x32_bf16 a[4:7], a[208:211], a[188:191], a[4:7]\n\t"
"v_pk_fma_f32 v[204:205], v[208:209], s[56:57], v[204:205]\n\t"
"v_pk_fma_f32 v[208:209], v[204:205], s[58:59], v[208:209] neg_lo:[0,0,1] neg_hi:[0,0,1]\n\t"
"v_pk_fma_f32 v[98:99], v[4:5], s[60:61], v[204:205]\n\t"
"v_pk_fma_f32 v[4:5], v[204:205], s[60:61], v[4:5]\n\t"
"v_pk_fma_f32 v[204:205], v[104:105], s[62:63], v[208:209]\n\t"
"v_pk_fma_f32 v[104:105], v[208:209], s[62:63], v[104:105]\n\t"
"v_mfma_f32_16x16x32_bf16 a[12:15], a[232:235], a[184:187], a[12:15]\n\t"
"v_cvt_pk_bf16_f32 v204, v204, v205\n\t"
"v_cvt_pk_bf16_f32 v208, v98, v99\n\t"
"v_mfma_f32_16x16x32_bf16 a[12:15], a[236:239], a[188:191], a[12:15]\n\t"
"v_mfma_f32_16x16x32_bf16 a[12:15], a[208:211], a[192:195], a[12:15]\n\t"
"v_pk_fma_f32 v[206:207], v[210:211], s[56:57], v[206:207]\n\t"
"v_pk_fma_f32 v[210:211], v[206:207], s[58:59], v[210:211] neg_lo:[0,0,1] neg_hi:[0,0,1]\n\t"
"v_pk_fma_f32 v[228:229], v[6:7], s[60:61], v[206:207]\n\t"
"v_pk_fma_f32 v[6:7], v[206:207], s[60:61], v[6:7]\n\t"
"v_pk_fma_f32 v[206:207], v[106:107], s[62:63], v[210:211]\n\t"
"v_pk_fma_f32 v[106:107], v[210:211], s[62:63], v[106:107]\n\t"
"v_mfma_f32_16x16x32_bf16 a[20:23], a[232:235], a[188:191], a[20:23]\n\t"
"v_cvt_pk_bf16_f32 v205, v206, v207\n\t"
"v_cvt_pk_bf16_f32 v209, v228, v229\n\t"
"v_mfma_f32_16x16x32_bf16 a[20:23], a[236:239], a[192:195], a[20:23]\n\t"
"v_accvgpr_read_b32 v224, a0\n\t"
"v_accvgpr_read_b32 v226, a4\n\t"
"v_mfma_f32_16x16x32_bf16 a[20:23], a[208:211], a[196:199], a[20:23]\n\t"
"v_accvgpr_read_b32 v225, a1\n\t"
"v_accvgpr_read_b32 v227, a5\n\t"
"v_pk_fma_f32 v[212:213], v[216:217], s[56:57], v[212:213]\n\t"
"v_pk_fma_f32 v[216:217], v[212:213], s[58:59], v[216:217] neg_lo:[0,0,1] neg_hi:[0,0,1]\n\t"
"v_pk_fma_f32 v[234:235], v[8:9], s[60:61], v[212:213]\n\t"
"v_pk_fma_f32 v[8:9], v[212:213], s[60:61], v[8:9]\n\t"
"v_pk_fma_f32 v[212:213], v[108:109], s[62:63], v[216:217]\n\t"
"v_pk_fma_f32 v[108:109], v[216:217], s[62:63], v[108:109]\n\t"
"v_mfma_f32_16x16x32_bf16 a[28:31], a[232:235], a[192:195], a[28:31]\n\t"
"v_cvt_pk_bf16_f32 v212, v212, v213\n\t"
"v_cvt_pk_bf16_f32 v216, v234, v235\n\t"
"v_mfma_f32_16x16x32_bf16 a[28:31], a[236:239], a[196:199], a[28:31]\n\t"
"v_accvgpr_read_b32 v230, a2\n\t"
"v_accvgpr_read_b32 v232, a6\n\t"
"v_mfma_f32_16x16x32_bf16 a[28:31], a[208:211], a[200:203], a[28:31]\n\t"
"v_accvgpr_read_b32 v231, a3\n\t"
"v_accvgpr_read_b32 v233, a7\n\t"
"v_pk_fma_f32 v[214:215], v[218:219], s[56:57], v[214:215]\n\t"
"v_pk_fma_f32 v[218:219], v[214:215], s[58:59], v[218:219] neg_lo:[0,0,1] neg_hi:[0,0,1]\n\t"
"v_pk_fma_f32 v[98:99], v[10:11], s[60:61], v[214:215]\n\t"
"v_pk_fma_f32 v[10:11], v[214:215], s[60:61], v[10:11]\n\t"
"v_pk_fma_f32 v[214:215], v[110:111], s[62:63], v[218:219]\n\t"
"v_pk_fma_f32 v[110:111], v[218:219], s[62:63], v[110:111]\n\t"
"v_mfma_f32_16x16x32_bf16 a[36:39], a[232:235], a[196:199], a[36:39]\n\t"
"s_waitcnt vmcnt(7)\n\t"
"v_cvt_pk_bf16_f32 v213, v214, v215\n\t"
"v_cvt_pk_bf16_f32 v217, v98, v99\n\t"
"v_mfma_f32_16x16x32_bf16 a[36:39], a[236:239], a[200:203], a[36:39]\n\t"
"v_accvgpr_read_b32 v236, a8\n\t"
"v_accvgpr_read_b32 v238, a12\n\t"
"v_mfma_f32_16x16x32_bf16 a[36:39], a[208:211], a[204:207], a[36:39]\n\t"
"buffer_load_dwordx4 a[232:235], v252, s[8:11], s20 offen offset:1024 sc1\n\t"
"v_accvgpr_read_b32 v237, a9\n\t"
"v_accvgpr_read_b32 v239, a13\n\t"
"v_pk_fma_f32 v[224:225], v[226:227], s[56:57], v[224:225]\n\t"
"v_pk_fma_f32 v[226:227], v[224:225], s[58:59], v[226:227] neg_lo:[0,0,1] neg_hi:[0,0,1]\n\t"
"v_pk_fma_f32 v[228:229], v[12:13], s[60:61], v[224:225]\n\t"
"v_pk_fma_f32 v[12:13], v[224:225], s[60:61], v[12:13]\n\t"
"v_pk_fma_f32 v[224:225], v[112:113], s[62:63], v[226:227]\n\t"
"v_pk_fma_f32 v[112:113], v[226:227], s[62:63], v[112:113]\n\t"
"v_mfma_f32_16x16x32_bf16 a[44:47], a[240:243], a[168:171], a[44:47]\n\t"
"v_cvt_pk_bf16_f32 v224, v224, v225\n\t"
"v_cvt_pk_bf16_f32 v228, v228, v229\n\t"
"v_mfma_f32_16x16x32_bf16 a[44:47], a[244:247], a[172:175], a[44:47]\n\t"
"v_accvgpr_write_b32 a0, v224\n\t"
"v_accvgpr_write_b32 a4, v228\n\t"
"v_mfma_f32_16x16x32_bf16 a[44:47], a[248:251], a[176:179], a[44:47]\n\t"
"buffer_load_dwordx4 a[208:211], v252, s[8:11], s20 offen offset:2048 sc1\n\t"
"v_accvgpr_read_b32 v224, a10\n\t"
"v_accvgpr_read_b32 v226, a14\n\t"
"s_mov_b32 s20, 1204224\n\t"
"v_pk_fma_f32 v[230:231], v[232:233], s[56:57], v[230:231]\n\t"
"v_pk_fma_f32 v[232:233], v[230:231], s[58:59], v[232:233] neg_lo:[0,0,1] neg_hi:[0,0,1]\n\t"
"v_pk_fma_f32 v[234:235], v[14:15], s[60:61], v[230:231]\n\t"
"v_pk_fma_f32 v[14:15], v[230:231], s[60:61], v[14:15]\n\t"
"v_pk_fma_f32 v[230:231], v[114:115], s[62:63], v[232:233]\n\t"
"v_pk_fma_f32 v[114:115], v[232:233], s[62:63], v[114:115]\n\t"
"v_mfma_f32_16x16x32_bf16 a[52:55], a[240:243], a[172:175], a[52:55]\n\t"
"v_cvt_pk_bf16_f32 v230, v230, v231\n\t"
"v_cvt_pk_bf16_f32 v234, v234, v235\n\t"
"v_mfma_f32_16x16x32_bf16 a[52:55], a[244:247], a[176:179], a[52:55]\n\t"
"v_accvgpr_write_b32 a1, v230\n\t"
"v_accvgpr_write_b32 a5, v234\n\t"
"v_mfma_f32_16x16x32_bf16 a[52:55], a[248:251], a[180:183], a[52:55]\n\t"
"buffer_load_dwordx4 a[236:239], v252, s[8:11], s20 offen offset:0 sc1\n\t"
"v_accvgpr_read_b32 v225, a11\n\t"
"v_accvgpr_read_b32 v227, a15\n\t"
"v_pk_fma_f32 v[236:237], v[238:239], s[56:57], v[236:237]\n\t"
"v_pk_fma_f32 v[238:239], v[236:237], s[58:59], v[238:239] neg_lo:[0,0,1] neg_hi:[0,0,1]\n\t"
"v_pk_fma_f32 v[98:99], v[16:17], s[60:61], v[236:237]\n\t"
"v_pk_fma_f32 v[16:17], v[236:237], s[60:61], v[16:17]\n\t"
"v_pk_fma_f32 v[236:237], v[116:117], s[62:63], v[238:239]\n\t"
"v_pk_fma_f32 v[116:117], v[238:239], s[62:63], v[116:117]\n\t"
"v_mfma_f32_16x16x32_bf16 a[60:63], a[240:243], a[176:179], a[60:63]\n\t"
"v_accvgpr_read_b32 v230, a16\n\t"
"v_accvgpr_read_b32 v232, a20\n\t"
"v_mfma_f32_16x16x32_bf16 a[60:63], a[244:247], a[180:183], a[60:63]\n\t"
"v_accvgpr_read_b32 v231, a17\n\t"
"v_accvgpr_read_b32 v233, a21\n\t"
"v_mfma_f32_16x16x32_bf16 a[60:63], a[248:251], a[184:187], a[60:63]\n\t"
"v_cvt_pk_bf16_f32 v236, v236, v237\n\t"
"v_cvt_pk_bf16_f32 v98, v98, v99\n\t"
"v_accvgpr_write_b32 a8, v236\n\t"
"v_accvgpr_write_b32 a12, v98\n\t"
"v_accvgpr_read_b32 v236, a18\n\t"
"v_pk_fma_f32 v[224:225], v[226:227], s[56:57], v[224:225]\n\t"
"v_pk_fma_f32 v[226:227], v[224:225], s[58:59], v[226:227] neg_lo:[0,0,1] neg_hi:[0,0,1]\n\t"
"v_pk_fma_f32 v[228:229], v[18:19], s[60:61], v[224:225]\n\t"
"v_pk_fma_f32 v[18:19], v[224:225], s[60:61], v[18:19]\n\t"
"v_pk_fma_f32 v[224:225], v[118:119], s[62:63], v[226:227]\n\t"
"v_pk_fma_f32 v[118:119], v[226:227], s[62:63], v[118:119]\n\t"
"v_pk_fma_f32 v[230:231], v[232:233], s[56:57], v[230:231]\n\t"
"v_pk_fma_f32 v[232:233], v[230:231], s[58:59], v[232:233] neg_lo:[0,0,1] neg_hi:[0,0,1]\n\t"
"v_pk_fma_f32 v[234:235], v[20:21], s[60:61], v[230:231]\n\t"
"v_pk_fma_f32 v[20:21], v[230:231], s[60:61], v[20:21]\n\t"
"v_pk_fma_f32 v[230:231], v[120:121], s[62:63], v[232:233]\n\t"
"v_pk_fma_f32 v[120:121], v[232:233], s[62:63], v[120:121]\n\t"
"v_mfma_f32_16x16x32_bf16 a[68:71], a[240:243], a[180:183], a[68:71]\n\t"
"v_accvgpr_read_b32 v238, a22\n\t"
"v_accvgpr_read_b32 v237, a19\n\t"
"v_mfma_f32_16x16x32_bf16 a[68:71], a[244:247], a[184:187], a[68:71]\n\t"
"v_accvgpr_read_b32 v239, a23\n\t"
"v_cvt_pk_bf16_f32 v224, v224, v225\n\t"
"v_mfma_f32_16x16x32_bf16 a[68:71], a[248:251], a[188:191], a[68:71]\n\t"
"v_cvt_pk_bf16_f32 v228, v228, v229\n\t"
"v_cvt_pk_bf16_f32 v230, v230, v231\n\t"
"v_cvt_pk_bf16_f32 v234, v234, v235\n\t"
"v_accvgpr_write_b32 a9, v224\n\t"
"v_accvgpr_write_b32 a13, v228\n\t"
"v_pk_fma_f32 v[236:237], v[238:239], s[56:57], v[236:237]\n\t"
"v_pk_fma_f32 v[238:239], v[236:237], s[58:59], v[238:239] neg_lo:[0,0,1] neg_hi:[0,0,1]\n\t"
"v_pk_fma_f32 v[98:99], v[22:23], s[60:61], v[236:237]\n\t"
"v_pk_fma_f32 v[22:23], v[236:237], s[60:61], v[22:23]\n\t"
"v_pk_fma_f32 v[236:237], v[122:123], s[62:63], v[238:239]\n\t"
"v_pk_fma_f32 v[122:123], v[238:239], s[62:63], v[122:123]\n\t"
"v_mfma_f32_16x16x32_bf16 a[76:79], a[240:243], a[184:187], a[76:79]\n\t"
"v_accvgpr_write_b32 a16, v230\n\t"
"v_accvgpr_write_b32 a20, v234\n\t"
"v_mfma_f32_16x16x32_bf16 a[76:79], a[244:247], a[188:191], a[76:79]\n\t"
"v_accvgpr_read_b32 v224, a24\n\t"
"v_accvgpr_read_b32 v226, a28\n\t"
"v_mfma_f32_16x16x32_bf16 a[76:79], a[248:251], a[192:195], a[76:79]\n\t"
"v_accvgpr_read_b32 v225, a25\n\t"
"v_accvgpr_read_b32 v227, a29\n\t"
"v_cvt_pk_bf16_f32 v236, v236, v237\n\t"
"v_cvt_pk_bf16_f32 v98, v98, v99\n\t"
"v_accvgpr_write_b32 a17, v236\n\t"
"v_pk_fma_f32 v[224:225], v[226:227], s[56:57], v[224:225]\n\t"
"v_pk_fma_f32 v[226:227], v[224:225], s[58:59], v[226:227] neg_lo:[0,0,1] neg_hi:[0,0,1]\n\t"
"v_pk_fma_f32 v[228:229], v[24:25], s[60:61], v[224:225]\n\t"
"v_pk_fma_f32 v[24:25], v[224:225], s[60:61], v[24:25]\n\t"
"v_pk_fma_f32 v[224:225], v[124:125], s[62:63], v[226:227]\n\t"
"v_pk_fma_f32 v[124:125], v[226:227], s[62:63], v[124:125]\n\t"
"v_mfma_f32_16x16x32_bf16 a[84:87], a[240:243], a[188:191], a[84:87]\n\t"
"v_accvgpr_write_b32 a21, v98\n\t"
"v_accvgpr_read_b32 v230, a26\n\t"
"v_mfma_f32_16x16x32_bf16 a[84:87], a[244:247], a[192:195], a[84:87]\n\t"
"v_accvgpr_read_b32 v232, a30\n\t"
"v_accvgpr_read_b32 v231, a27\n\t"
"v_mfma_f32_16x16x32_bf16 a[84:87], a[248:251], a[196:199], a[84:87]\n\t"
"v_accvgpr_read_b32 v233, a31\n\t"
"v_cvt_pk_bf16_f32 v224, v224, v225\n\t"
"v_cvt_pk_bf16_f32 v228, v228, v229\n\t"
"v_accvgpr_write_b32 a24, v224\n\t"
"v_accvgpr_write_b32 a28, v228\n\t"
"v_pk_fma_f32 v[230:231], v[232:233], s[56:57], v[230:231]\n\t"
"v_pk_fma_f32 v[232:233], v[230:231], s[58:59], v[232:233] neg_lo:[0,0,1] neg_hi:[0,0,1]\n\t"
"v_pk_fma_f32 v[234:235], v[26:27], s[60:61], v[230:231]\n\t"
"v_pk_fma_f32 v[26:27], v[230:231], s[60:61], v[26:27]\n\t"
"v_pk_fma_f32 v[230:231], v[126:127], s[62:63], v[232:233]\n\t"
"v_pk_fma_f32 v[126:127], v[232:233], s[62:63], v[126:127]\n\t"
"v_mfma_f32_16x16x32_bf16 a[92:95], a[240:243], a[192:195], a[92:95]\n\t"
"v_accvgpr_read_b32 v236, a32\n\t"
"v_accvgpr_read_b32 v238, a36\n\t"
"v_mfma_f32_16x16x32_bf16 a[92:95], a[244:247], a[196:199], a[92:95]\n\t"
"v_accvgpr_read_b32 v237, a33\n\t"
"v_accvgpr_read_b32 v239, a37\n\t"
"v_mfma_f32_16x16x32_bf16 a[92:95], a[248:251], a[200:203], a[92:95]\n\t"
"v_cvt_pk_bf16_f32 v230, v230, v231\n\t"
"v_cvt_pk_bf16_f32 v234, v234, v235\n\t"
"v_accvgpr_write_b32 a25, v230\n\t"
"v_accvgpr_write_b32 a29, v234\n\t"
"v_accvgpr_read_b32 v224, a34\n\t"
"v_pk_fma_f32 v[236:237], v[238:239], s[56:57], v[236:237]\n\t"
"v_pk_fma_f32 v[238:239], v[236:237], s[58:59], v[238:239] neg_lo:[0,0,1] neg_hi:[0,0,1]\n\t"
"v_pk_fma_f32 v[98:99], v[28:29], s[60:61], v[236:237]\n\t"
"v_pk_fma_f32 v[28:29], v[236:237], s[60:61], v[28:29]\n\t"
"v_pk_fma_f32 v[236:237], v[128:129], s[62:63], v[238:239]\n\t"
"v_pk_fma_f32 v[128:129], v[238:239], s[62:63], v[128:129]\n\t"
"v_mfma_f32_16x16x32_bf16 a[100:103], a[240:243], a[196:199], a[100:103]\n\t"
"s_waitcnt vmcnt(7)\n\t"
"v_accvgpr_read_b32 v226, a38\n\t"
"v_accvgpr_read_b32 v225, a35\n\t"
"v_mfma_f32_16x16x32_bf16 a[100:103], a[244:247], a[200:203], a[100:103]\n\t"
"v_accvgpr_read_b32 v227, a39\n\t"
"v_cvt_pk_bf16_f32 v236, v236, v237\n\t"
"v_mfma_f32_16x16x32_bf16 a[100:103], a[248:251], a[204:207], a[100:103]\n\t"
"buffer_load_dwordx4 a[240:243], v252, s[8:11], s20 offen offset:1024 sc1\n\t"
"v_cvt_pk_bf16_f32 v98, v98, v99\n\t"
"v_accvgpr_write_b32 a32, v236\n\t"
"v_pk_fma_f32 v[224:225], v[226:227], s[56:57], v[224:225]\n\t"
"v_pk_fma_f32 v[226:227], v[224:225], s[58:59], v[226:227] neg_lo:[0,0,1] neg_hi:[0,0,1]\n\t"
"v_pk_fma_f32 v[228:229], v[30:31], s[60:61], v[224:225]\n\t"
"v_pk_fma_f32 v[30:31], v[224:225], s[60:61], v[30:31]\n\t"
"v_pk_fma_f32 v[224:225], v[130:131], s[62:63], v[226:227]\n\t"
"v_pk_fma_f32 v[130:131], v[226:227], s[62:63], v[130:131]\n\t"
"v_mfma_f32_16x16x32_bf16 a[108:111], v[240:243], a[168:171], a[108:111]\n\t"
"ds_read_b128 a[168:171], v255 offset:512\n\t"
"v_accvgpr_write_b32 a36, v98\n\t"
"v_mfma_f32_16x16x32_bf16 a[108:111], a[252:255], a[172:175], a[108:111]\n\t"
"v_accvgpr_read_b32 v230, a40\n\t"
"v_accvgpr_read_b32 v232, a44\n\t"
"v_mfma_f32_16x16x32_bf16 a[108:111], a[212:215], a[176:179], a[108:111]\n\t"
"buffer_load_dwordx4 a[244:247], v252, s[8:11], s20 offen offset:2048 sc1\n\t"
"v_accvgpr_read_b32 v231, a41\n\t"
"v_accvgpr_read_b32 v233, a45\n\t"
"v_cvt_pk_bf16_f32 v224, v224, v225\n\t"
"v_cvt_pk_bf16_f32 v228, v228, v229\n\t"
"s_mov_b32 s20, 1363968\n\t"
"v_pk_fma_f32 v[230:231], v[232:233], s[56:57], v[230:231]\n\t"
"v_pk_fma_f32 v[232:233], v[230:231], s[58:59], v[232:233] neg_lo:[0,0,1] neg_hi:[0,0,1]\n\t"
"v_pk_fma_f32 v[234:235], v[32:33], s[60:61], v[230:231]\n\t"
"v_pk_fma_f32 v[32:33], v[230:231], s[60:61], v[32:33]\n\t"
"v_pk_fma_f32 v[230:231], v[132:133], s[62:63], v[232:233]\n\t"
"v_pk_fma_f32 v[132:133], v[232:233], s[62:63], v[132:133]\n\t"
"v_mfma_f32_16x16x32_bf16 a[116:119], v[240:243], a[172:175], a[116:119]\n\t"
"ds_read_b128 a[172:175], v255 offset:3584\n\t"
"v_accvgpr_write_b32 a33, v224\n\t"
"v_mfma_f32_16x16x32_bf16 a[116:119], a[252:255], a[176:179], a[116:119]\n\t"
"v_accvgpr_write_b32 a37, v228\n\t"
"v_accvgpr_read_b32 v236, a42\n\t"
"v_mfma_f32_16x16x32_bf16 a[116:119], a[212:215], a[180:183], a[116:119]\n\t"
"buffer_load_dwordx4 a[248:251], v252, s[8:11], s20 offen offset:0 sc1\n\t"
"v_accvgpr_read_b32 v238, a46\n\t"
"v_accvgpr_read_b32 v237, a43\n\t"
"v_accvgpr_read_b32 v239, a47\n\t"
"v_pk_fma_f32 v[236:237], v[238:239], s[56:57], v[236:237]\n\t"
"v_pk_fma_f32 v[238:239], v[236:237], s[58:59], v[238:239] neg_lo:[0,0,1] neg_hi:[0,0,1]\n\t"
"v_pk_fma_f32 v[98:99], v[34:35], s[60:61], v[236:237]\n\t"
"v_pk_fma_f32 v[34:35], v[236:237], s[60:61], v[34:35]\n\t"
"v_pk_fma_f32 v[236:237], v[134:135], s[62:63], v[238:239]\n\t"
"v_pk_fma_f32 v[134:135], v[238:239], s[62:63], v[134:135]\n\t"
"v_mfma_f32_16x16x32_bf16 a[124:127], v[240:243], a[176:179], a[124:127]\n\t"
"ds_read_b128 a[176:179], v255 offset:6656\n\t"
"v_cvt_pk_bf16_f32 v198, v230, v231\n\t"
"v_mfma_f32_16x16x32_bf16 a[124:127], a[252:255], a[180:183], a[124:127]\n\t"
"v_cvt_pk_bf16_f32 v202, v234, v235\n\t"
"v_accvgpr_read_b32 v224, a48\n\t"
"v_mfma_f32_16x16x32_bf16 a[124:127], a[212:215], a[184:187], a[124:127]\n\t"
"global_load_dwordx4 v[248:251], v252, s[48:49] offset:0\n\t"
"v_accvgpr_read_b32 v226, a52\n\t"
"v_accvgpr_read_b32 v225, a49\n\t"
"v_accvgpr_read_b32 v227, a53\n\t"
"v_accvgpr_read_b32 v230, a50\n\t"
"v_accvgpr_read_b32 v232, a54\n\t"
"v_accvgpr_read_b32 v231, a51\n\t"
"v_accvgpr_read_b32 v233, a55\n\t"
"v_pk_fma_f32 v[224:225], v[226:227], s[56:57], v[224:225]\n\t"
"v_pk_fma_f32 v[226:227], v[224:225], s[58:59], v[226:227] neg_lo:[0,0,1] neg_hi:[0,0,1]\n\t"
"v_pk_fma_f32 v[228:229], v[36:37], s[60:61], v[224:225]\n\t"
"v_pk_fma_f32 v[36:37], v[224:225], s[60:61], v[36:37]\n\t"
"v_pk_fma_f32 v[224:225], v[136:137], s[62:63], v[226:227]\n\t"
"v_pk_fma_f32 v[136:137], v[226:227], s[62:63], v[136:137]\n\t"
"v_pk_fma_f32 v[230:231], v[232:233], s[56:57], v[230:231]\n\t"
"v_pk_fma_f32 v[232:233], v[230:231], s[58:59], v[232:233] neg_lo:[0,0,1] neg_hi:[0,0,1]\n\t"
"v_pk_fma_f32 v[234:235], v[38:39], s[60:61], v[230:231]\n\t"
"v_pk_fma_f32 v[38:39], v[230:231], s[60:61], v[38:39]\n\t"
"v_pk_fma_f32 v[230:231], v[138:139], s[62:63], v[232:233]\n\t"
"v_pk_fma_f32 v[138:139], v[232:233], s[62:63], v[138:139]\n\t"
"v_mfma_f32_16x16x32_bf16 a[132:135], v[240:243], a[180:183], a[132:135]\n\t"
"ds_read_b128 a[180:183], v255 offset:9728\n\t"
"v_cvt_pk_bf16_f32 v199, v236, v237\n\t"
"v_mfma_f32_16x16x32_bf16 a[132:135], a[252:255], a[184:187], a[132:135]\n\t"
"v_cvt_pk_bf16_f32 v203, v98, v99\n\t"
"v_accvgpr_read_b32 v236, a56\n\t"
"v_mfma_f32_16x16x32_bf16 a[132:135], a[212:215], a[188:191], a[132:135]\n\t"
"buffer_load_dwordx4 a[52:55], v252, s[8:11], s20 offen offset:1024 sc1\n\t"
"v_accvgpr_read_b32 v238, a60\n\t"
"v_accvgpr_read_b32 v237, a57\n\t"
"v_accvgpr_read_b32 v239, a61\n\t"
"v_pk_fma_f32 v[236:237], v[238:239], s[56:57], v[236:237]\n\t"
"v_pk_fma_f32 v[238:239], v[236:237], s[58:59], v[238:239] neg_lo:[0,0,1] neg_hi:[0,0,1]\n\t"
"v_pk_fma_f32 v[98:99], v[40:41], s[60:61], v[236:237]\n\t"
"v_pk_fma_f32 v[40:41], v[236:237], s[60:61], v[40:41]\n\t"
"v_pk_fma_f32 v[236:237], v[140:141], s[62:63], v[238:239]\n\t"
"v_pk_fma_f32 v[140:141], v[238:239], s[62:63], v[140:141]\n\t"
"v_mfma_f32_16x16x32_bf16 a[140:143], v[240:243], a[184:187], a[140:143]\n\t"
"ds_read_b128 a[184:187], v255 offset:12800\n\t"
"v_cvt_pk_bf16_f32 v206, v224, v225\n\t"
"v_mfma_f32_16x16x32_bf16 a[140:143], a[252:255], a[188:191], a[140:143]\n\t"
"v_cvt_pk_bf16_f32 v210, v228, v229\n\t"
"v_cvt_pk_bf16_f32 v207, v230, v231\n\t"
"v_mfma_f32_16x16x32_bf16 a[140:143], a[212:215], a[192:195], a[140:143]\n\t"
"v_accvgpr_read_b32 v224, a58\n\t"
"v_accvgpr_read_b32 v226, a62\n\t"
"v_accvgpr_read_b32 v225, a59\n\t"
"v_accvgpr_read_b32 v227, a63\n\t"
"v_cvt_pk_bf16_f32 v211, v234, v235\n\t"
"v_pk_fma_f32 v[224:225], v[226:227], s[56:57], v[224:225]\n\t"
"v_pk_fma_f32 v[226:227], v[224:225], s[58:59], v[226:227] neg_lo:[0,0,1] neg_hi:[0,0,1]\n\t"
"v_pk_fma_f32 v[228:229], v[42:43], s[60:61], v[224:225]\n\t"
"v_pk_fma_f32 v[42:43], v[224:225], s[60:61], v[42:43]\n\t"
"v_pk_fma_f32 v[224:225], v[142:143], s[62:63], v[226:227]\n\t"
"v_pk_fma_f32 v[142:143], v[226:227], s[62:63], v[142:143]\n\t"
"v_mfma_f32_16x16x32_bf16 a[148:151], v[240:243], a[188:191], a[148:151]\n\t"
"ds_read_b128 a[188:191], v255 offset:15872\n\t"
"v_cvt_pk_bf16_f32 v214, v236, v237\n\t"
"v_mfma_f32_16x16x32_bf16 a[148:151], a[252:255], a[192:195], a[148:151]\n\t"
"v_cvt_pk_bf16_f32 v218, v98, v99\n\t"
"v_accvgpr_read_b32 v230, a64\n\t"
"v_mfma_f32_16x16x32_bf16 a[148:151], a[212:215], a[196:199], a[148:151]\n\t"
"buffer_load_dwordx4 a[56:59], v252, s[8:11], s20 offen offset:2048 sc1\n\t"
"v_accvgpr_read_b32 v232, a68\n\t"
"v_accvgpr_read_b32 v231, a65\n\t"
"v_accvgpr_read_b32 v233, a69\n\t"
"v_pk_fma_f32 v[230:231], v[232:233], s[56:57], v[230:231]\n\t"
"v_pk_fma_f32 v[232:233], v[230:231], s[58:59], v[232:233] neg_lo:[0,0,1] neg_hi:[0,0,1]\n\t"
"v_pk_fma_f32 v[234:235], v[44:45], s[60:61], v[230:231]\n\t"
"v_pk_fma_f32 v[44:45], v[230:231], s[60:61], v[44:45]\n\t"
"v_pk_fma_f32 v[230:231], v[144:145], s[62:63], v[232:233]\n\t"
"v_pk_fma_f32 v[144:145], v[232:233], s[62:63], v[144:145]\n\t"
"v_mfma_f32_16x16x32_bf16 a[156:159], v[240:243], a[192:195], a[156:159]\n\t"
"s_mov_b32 s20, 1376256\n\t"
"ds_read_b128 a[192:195], v255 offset:18944\n\t"
"v_cvt_pk_bf16_f32 v215, v224, v225\n\t"
"v_mfma_f32_16x16x32_bf16 a[156:159], a[252:255], a[196:199], a[156:159]\n\t"
"v_cvt_pk_bf16_f32 v219, v228, v229\n\t"
"v_accvgpr_read_b32 v236, a66\n\t"
"v_mfma_f32_16x16x32_bf16 a[156:159], a[212:215], a[200:203], a[156:159]\n\t"
"buffer_load_dwordx4 a[60:63], v252, s[8:11], s20 offen offset:0 sc1\n\t"
"v_accvgpr_read_b32 v238, a70\n\t"
"v_accvgpr_read_b32 v237, a67\n\t"
"v_accvgpr_read_b32 v239, a71\n\t"
"v_cvt_pk_bf16_f32 v230, v230, v231\n\t"
"v_cvt_pk_bf16_f32 v234, v234, v235\n\t"
"v_pk_fma_f32 v[236:237], v[238:239], s[56:57], v[236:237]\n\t"
"v_pk_fma_f32 v[238:239], v[236:237], s[58:59], v[238:239] neg_lo:[0,0,1] neg_hi:[0,0,1]\n\t"
"v_pk_fma_f32 v[98:99], v[46:47], s[60:61], v[236:237]\n\t"
"v_pk_fma_f32 v[46:47], v[236:237], s[60:61], v[46:47]\n\t"
"v_pk_fma_f32 v[236:237], v[146:147], s[62:63], v[238:239]\n\t"
"v_pk_fma_f32 v[146:147], v[238:239], s[62:63], v[146:147]\n\t"
"v_mfma_f32_16x16x32_bf16 a[164:167], v[240:243], a[196:199], a[164:167]\n\t"
"s_waitcnt vmcnt(11) lgkmcnt(4)\n\t"
"ds_read_b128 a[196:199], v255 offset:22016\n\t"
"v_accvgpr_write_b32 a2, v230\n\t"
"v_mfma_f32_16x16x32_bf16 a[164:167], a[252:255], a[200:203], a[164:167]\n\t"
"ds_read_b128 a[200:203], v255 offset:25088\n\t"
"v_accvgpr_write_b32 a6, v234\n\t"
"v_mfma_f32_16x16x32_bf16 a[164:167], a[212:215], a[204:207], a[164:167]\n\t"
"buffer_load_dwordx4 a[252:255], v252, s[8:11], s20 offen offset:1024 sc1\n\t"
"ds_read_b128 a[204:207], v255 offset:28160\n\t"
"v_accvgpr_read_b32 v224, a72\n\t"
"v_accvgpr_read_b32 v226, a76\n\t"
"v_accvgpr_read_b32 v225, a73\n\t"
"v_accvgpr_read_b32 v227, a77\n\t"
"v_cvt_pk_bf16_f32 v236, v236, v237\n\t"
"v_cvt_pk_bf16_f32 v98, v98, v99\n\t"
"v_pk_fma_f32 v[224:225], v[226:227], s[56:57], v[224:225]\n\t"
"v_pk_fma_f32 v[226:227], v[224:225], s[58:59], v[226:227] neg_lo:[0,0,1] neg_hi:[0,0,1]\n\t"
"v_pk_fma_f32 v[228:229], v[48:49], s[60:61], v[224:225]\n\t"
"v_pk_fma_f32 v[48:49], v[224:225], s[60:61], v[48:49]\n\t"
"v_pk_fma_f32 v[224:225], v[148:149], s[62:63], v[226:227]\n\t"
"v_pk_fma_f32 v[148:149], v[226:227], s[62:63], v[148:149]\n\t"
"Lst32_%=:\n\t"
"v_mfma_f32_16x16x32_bf16 v[0:3], a[220:223], a[168:171], v[0:3]\n\t"
"s_waitcnt lgkmcnt(6)\n\t"
"v_accvgpr_write_b32 a3, v236\n\t"
"v_accvgpr_write_b32 a7, v98\n\t"
"v_mfma_f32_16x16x32_bf16 v[0:3], a[224:227], a[172:175], v[0:3]\n\t"
"v_accvgpr_read_b32 v230, a74\n\t"
"v_accvgpr_read_b32 v232, a78\n\t"
"v_mfma_f32_16x16x32_bf16 v[0:3], a[216:219], a[176:179], v[0:3]\n\t"
"buffer_load_dwordx4 a[212:215], v252, s[8:11], s20 offen offset:2048 sc1\n\t"
"v_accvgpr_read_b32 v231, a75\n\t"
"v_accvgpr_read_b32 v233, a79\n\t"
"v_cvt_pk_bf16_f32 v224, v224, v225\n\t"
"v_cvt_pk_bf16_f32 v228, v228, v229\n\t"
"v_pk_fma_f32 v[230:231], v[232:233], s[56:57], v[230:231]\n\t"
"v_pk_fma_f32 v[232:233], v[230:231], s[58:59], v[232:233] neg_lo:[0,0,1] neg_hi:[0,0,1]\n\t"
"v_pk_fma_f32 v[234:235], v[50:51], s[60:61], v[230:231]\n\t"
"v_pk_fma_f32 v[50:51], v[230:231], s[60:61], v[50:51]\n\t"
"v_pk_fma_f32 v[230:231], v[150:151], s[62:63], v[232:233]\n\t"
"v_pk_fma_f32 v[150:151], v[232:233], s[62:63], v[150:151]\n\t"
"v_mfma_f32_16x16x32_bf16 v[4:7], a[220:223], a[172:175], v[4:7]\n\t"
"s_waitcnt lgkmcnt(5)\n\t"
"s_mov_b32 s20, 1388544\n\t"
"v_accvgpr_write_b32 a10, v224\n\t"
"v_mfma_f32_16x16x32_bf16 v[4:7], a[224:227], a[176:179], v[4:7]\n\t"
"v_accvgpr_write_b32 a14, v228\n\t"
"v_accvgpr_read_b32 v236, a80\n\t"
"v_mfma_f32_16x16x32_bf16 v[4:7], a[216:219], a[180:183], v[4:7]\n\t"
"buffer_load_dwordx4 a[64:67], v252, s[8:11], s20 offen offset:0 sc1\n\t"
"v_accvgpr_read_b32 v238, a84\n\t"
"v_accvgpr_read_b32 v237, a81\n\t"
"v_accvgpr_read_b32 v239, a85\n\t"
"v_cvt_pk_bf16_f32 v230, v230, v231\n\t"
"v_cvt_pk_bf16_f32 v234, v234, v235\n\t"
"v_pk_fma_f32 v[236:237], v[238:239], s[56:57], v[236:237]\n\t"
"v_pk_fma_f32 v[238:239], v[236:237], s[58:59], v[238:239] neg_lo:[0,0,1] neg_hi:[0,0,1]\n\t"
"v_pk_fma_f32 v[98:99], v[52:53], s[60:61], v[236:237]\n\t"
"v_pk_fma_f32 v[52:53], v[236:237], s[60:61], v[52:53]\n\t"
"v_pk_fma_f32 v[236:237], v[152:153], s[62:63], v[238:239]\n\t"
"v_pk_fma_f32 v[152:153], v[238:239], s[62:63], v[152:153]\n\t"
"v_mfma_f32_16x16x32_bf16 v[8:11], a[220:223], a[176:179], v[8:11]\n\t"
"s_waitcnt lgkmcnt(4)\n\t"
"v_accvgpr_write_b32 a11, v230\n\t"
"v_accvgpr_write_b32 a15, v234\n\t"
"v_mfma_f32_16x16x32_bf16 v[8:11], a[224:227], a[180:183], v[8:11]\n\t"
"v_accvgpr_read_b32 v224, a82\n\t"
"v_accvgpr_read_b32 v226, a86\n\t"
"v_mfma_f32_16x16x32_bf16 v[8:11], a[216:219], a[184:187], v[8:11]\n\t"
"buffer_load_dwordx4 a[68:71], v252, s[8:11], s20 offen offset:1024 sc1\n\t"
"v_accvgpr_read_b32 v225, a83\n\t"
"v_accvgpr_read_b32 v227, a87\n\t"
"v_cvt_pk_bf16_f32 v236, v236, v237\n\t"
"v_cvt_pk_bf16_f32 v98, v98, v99\n\t"
"v_pk_fma_f32 v[224:225], v[226:227], s[56:57], v[224:225]\n\t"
"v_pk_fma_f32 v[226:227], v[224:225], s[58:59], v[226:227] neg_lo:[0,0,1] neg_hi:[0,0,1]\n\t"
"v_pk_fma_f32 v[228:229], v[54:55], s[60:61], v[224:225]\n\t"
"v_pk_fma_f32 v[54:55], v[224:225], s[60:61], v[54:55]\n\t"
"v_pk_fma_f32 v[224:225], v[154:155], s[62:63], v[226:227]\n\t"
"v_pk_fma_f32 v[154:155], v[226:227], s[62:63], v[154:155]\n\t"
"v_mfma_f32_16x16x32_bf16 v[12:15], a[220:223], a[180:183], v[12:15]\n\t"
"s_waitcnt lgkmcnt(3)\n\t"
"v_accvgpr_write_b32 a18, v236\n\t"
"v_accvgpr_write_b32 a22, v98\n\t"
"v_mfma_f32_16x16x32_bf16 v[12:15], a[224:227], a[184:187], v[12:15]\n\t"
"v_accvgpr_read_b32 v230, a88\n\t"
"v_accvgpr_read_b32 v232, a92\n\t"
"v_mfma_f32_16x16x32_bf16 v[12:15], a[216:219], a[188:191], v[12:15]\n\t"
"buffer_load_dwordx4 a[72:75], v252, s[8:11], s20 offen offset:2048 sc1\n\t"
"v_accvgpr_read_b32 v231, a89\n\t"
"v_accvgpr_read_b32 v233, a93\n\t"
"v_accvgpr_read_b32 v236, a90\n\t"
"v_accvgpr_read_b32 v238, a94\n\t"
"v_accvgpr_read_b32 v237, a91\n\t"
"v_accvgpr_read_b32 v239, a95\n\t"
"v_cvt_pk_bf16_f32 v224, v224, v225\n\t"
"v_cvt_pk_bf16_f32 v228, v228, v229\n\t"
"v_pk_fma_f32 v[230:231], v[232:233], s[56:57], v[230:231]\n\t"
"v_pk_fma_f32 v[232:233], v[230:231], s[58:59], v[232:233] neg_lo:[0,0,1] neg_hi:[0,0,1]\n\t"
"v_pk_fma_f32 v[234:235], v[56:57], s[60:61], v[230:231]\n\t"
"v_pk_fma_f32 v[56:57], v[230:231], s[60:61], v[56:57]\n\t"
"v_pk_fma_f32 v[230:231], v[156:157], s[62:63], v[232:233]\n\t"
"v_pk_fma_f32 v[156:157], v[232:233], s[62:63], v[156:157]\n\t"
"v_pk_fma_f32 v[236:237], v[238:239], s[56:57], v[236:237]\n\t"
"v_pk_fma_f32 v[238:239], v[236:237], s[58:59], v[238:239] neg_lo:[0,0,1] neg_hi:[0,0,1]\n\t"
"v_pk_fma_f32 v[98:99], v[58:59], s[60:61], v[236:237]\n\t"
"v_pk_fma_f32 v[58:59], v[236:237], s[60:61], v[58:59]\n\t"
"v_pk_fma_f32 v[236:237], v[158:159], s[62:63], v[238:239]\n\t"
"v_pk_fma_f32 v[158:159], v[238:239], s[62:63], v[158:159]\n\t"
"v_mfma_f32_16x16x32_bf16 v[16:19], a[220:223], a[184:187], v[16:19]\n\t"
"s_waitcnt lgkmcnt(2)\n\t"
"s_mov_b32 s20, 589824\n\t"
"v_accvgpr_write_b32 a19, v224\n\t"
"v_mfma_f32_16x16x32_bf16 v[16:19], a[224:227], a[188:191], v[16:19]\n\t"
"v_accvgpr_write_b32 a23, v228\n\t"
"v_accvgpr_read_b32 v224, a96\n\t"
"v_mfma_f32_16x16x32_bf16 v[16:19], a[216:219], a[192:195], v[16:19]\n\t"
"buffer_load_dwordx4 a[76:79], v252, s[8:11], s20 offen offset:0 sc1\n\t"
"v_accvgpr_read_b32 v226, a100\n\t"
"v_accvgpr_read_b32 v225, a97\n\t"
"v_accvgpr_read_b32 v227, a101\n\t"
"v_cvt_pk_bf16_f32 v230, v230, v231\n\t"
"v_cvt_pk_bf16_f32 v234, v234, v235\n\t"
"v_pk_fma_f32 v[224:225], v[226:227], s[56:57], v[224:225]\n\t"
"v_pk_fma_f32 v[226:227], v[224:225], s[58:59], v[226:227] neg_lo:[0,0,1] neg_hi:[0,0,1]\n\t"
"v_pk_fma_f32 v[228:229], v[60:61], s[60:61], v[224:225]\n\t"
"v_pk_fma_f32 v[60:61], v[224:225], s[60:61], v[60:61]\n\t"
"v_pk_fma_f32 v[224:225], v[160:161], s[62:63], v[226:227]\n\t"
"v_pk_fma_f32 v[160:161], v[226:227], s[62:63], v[160:161]\n\t"
"v_mfma_f32_16x16x32_bf16 v[20:23], a[220:223], a[188:191], v[20:23]\n\t"
"s_waitcnt lgkmcnt(1)\n\t"
"v_accvgpr_write_b32 a26, v230\n\t"
"v_cvt_pk_bf16_f32 v236, v236, v237\n\t"
"v_mfma_f32_16x16x32_bf16 v[20:23], a[224:227], a[192:195], v[20:23]\n\t"
"v_accvgpr_write_b32 a30, v234\n\t"
"v_cvt_pk_bf16_f32 v98, v98, v99\n\t"
"v_mfma_f32_16x16x32_bf16 v[20:23], a[216:219], a[196:199], v[20:23]\n\t"
"buffer_load_dwordx4 a[80:83], v252, s[8:11], s20 offen offset:1024 sc1\n\t"
"v_accvgpr_read_b32 v230, a98\n\t"
"v_accvgpr_read_b32 v232, a102\n\t"
"v_accvgpr_read_b32 v231, a99\n\t"
"v_accvgpr_read_b32 v233, a103\n\t"
"v_accvgpr_write_b32 a27, v236\n\t"
"v_accvgpr_write_b32 a31, v98\n\t"
"v_cvt_pk_bf16_f32 v224, v224, v225\n\t"
"v_cvt_pk_bf16_f32 v228, v228, v229\n\t"
"v_pk_fma_f32 v[230:231], v[232:233], s[56:57], v[230:231]\n\t"
"v_pk_fma_f32 v[232:233], v[230:231], s[58:59], v[232:233] neg_lo:[0,0,1] neg_hi:[0,0,1]\n\t"
"v_pk_fma_f32 v[234:235], v[62:63], s[60:61], v[230:231]\n\t"
"v_pk_fma_f32 v[62:63], v[230:231], s[60:61], v[62:63]\n\t"
"v_pk_fma_f32 v[230:231], v[162:163], s[62:63], v[232:233]\n\t"
"v_pk_fma_f32 v[162:163], v[232:233], s[62:63], v[162:163]\n\t"
"v_mfma_f32_16x16x32_bf16 v[24:27], a[220:223], a[192:195], v[24:27]\n\t"
"s_waitcnt lgkmcnt(0)\n\t"
"v_accvgpr_write_b32 a34, v224\n\t"
"v_accvgpr_write_b32 a38, v228\n\t"
"v_mfma_f32_16x16x32_bf16 v[24:27], a[224:227], a[196:199], v[24:27]\n\t"
"v_accvgpr_read_b32 v236, a104\n\t"
"v_accvgpr_read_b32 v238, a108\n\t"
"v_mfma_f32_16x16x32_bf16 v[24:27], a[216:219], a[200:203], v[24:27]\n\t"
"buffer_load_dwordx4 a[84:87], v252, s[8:11], s20 offen offset:2048 sc1\n\t"
"v_accvgpr_read_b32 v237, a105\n\t"
"v_accvgpr_read_b32 v239, a109\n\t"
"v_cvt_pk_bf16_f32 v230, v230, v231\n\t"
"v_cvt_pk_bf16_f32 v234, v234, v235\n\t"
"v_pk_fma_f32 v[236:237], v[238:239], s[56:57], v[236:237]\n\t"
"v_pk_fma_f32 v[238:239], v[236:237], s[58:59], v[238:239] neg_lo:[0,0,1] neg_hi:[0,0,1]\n\t"
"v_pk_fma_f32 v[98:99], v[64:65], s[60:61], v[236:237]\n\t"
"v_pk_fma_f32 v[64:65], v[236:237], s[60:61], v[64:65]\n\t"
"v_pk_fma_f32 v[236:237], v[164:165], s[62:63], v[238:239]\n\t"
"v_pk_fma_f32 v[164:165], v[238:239], s[62:63], v[164:165]\n\t"
"v_mfma_f32_16x16x32_bf16 v[28:31], a[220:223], a[196:199], v[28:31]\n\t"
"s_waitcnt vmcnt(16)\n\t"
"s_mov_b32 s20, 602112\n\t"
"v_accvgpr_write_b32 a35, v230\n\t"
"v_mfma_f32_16x16x32_bf16 v[28:31], a[224:227], a[200:203], v[28:31]\n\t"
"v_accvgpr_write_b32 a39, v234\n\t"
"v_accvgpr_read_b32 v224, a106\n\t"
"v_mfma_f32_16x16x32_bf16 v[28:31], a[216:219], a[204:207], v[28:31]\n\t"
"buffer_load_dwordx4 a[220:223], v252, s[8:11], s20 offen offset:0 sc1\n\t"
"v_accvgpr_read_b32 v226, a110\n\t"
"v_accvgpr_read_b32 v225, a107\n\t"
"v_accvgpr_read_b32 v227, a111\n\t"
"v_cvt_pk_bf16_f32 v236, v236, v237\n\t"
"v_cvt_pk_bf16_f32 v98, v98, v99\n\t"
"v_pk_fma_f32 v[224:225], v[226:227], s[56:57], v[224:225]\n\t"
"v_pk_fma_f32 v[226:227], v[224:225], s[58:59], v[226:227] neg_lo:[0,0,1] neg_hi:[0,0,1]\n\t"
"v_pk_fma_f32 v[228:229], v[66:67], s[60:61], v[224:225]\n\t"
"v_pk_fma_f32 v[66:67], v[224:225], s[60:61], v[66:67]\n\t"
"v_pk_fma_f32 v[224:225], v[166:167], s[62:63], v[226:227]\n\t"
"v_pk_fma_f32 v[166:167], v[226:227], s[62:63], v[166:167]\n\t"
"v_mfma_f32_16x16x32_bf16 v[32:35], a[228:231], a[168:171], v[32:35]\n\t"
"v_accvgpr_write_b32 a104, v236\n\t"
"v_accvgpr_write_b32 a106, v98\n\t"
"v_mfma_f32_16x16x32_bf16 v[32:35], a[232:235], a[172:175], v[32:35]\n\t"
"v_accvgpr_read_b32 v230, a112\n\t"
"v_accvgpr_read_b32 v232, a116\n\t"
"v_mfma_f32_16x16x32_bf16 v[32:35], a[208:211], a[176:179], v[32:35]\n\t"
"buffer_load_dwordx4 a[216:219], v252, s[8:11], s20 offen offset:1024 sc1\n\t"
"v_accvgpr_read_b32 v231, a113\n\t"
"v_accvgpr_read_b32 v233, a117\n\t"
"v_cvt_pk_bf16_f32 v224, v224, v225\n\t"
"v_cvt_pk_bf16_f32 v228, v228, v229\n\t"
"v_pk_fma_f32 v[230:231], v[232:233], s[56:57], v[230:231]\n\t"
"v_pk_fma_f32 v[232:233], v[230:231], s[58:59], v[232:233] neg_lo:[0,0,1] neg_hi:[0,0,1]\n\t"
"v_pk_fma_f32 v[234:235], v[68:69], s[60:61], v[230:231]\n\t"
"v_pk_fma_f32 v[68:69], v[230:231], s[60:61], v[68:69]\n\t"
"v_pk_fma_f32 v[230:231], v[168:169], s[62:63], v[232:233]\n\t"
"v_pk_fma_f32 v[168:169], v[232:233], s[62:63], v[168:169]\n\t"
"v_mfma_f32_16x16x32_bf16 v[36:39], a[228:231], a[172:175], v[36:39]\n\t"
"v_accvgpr_write_b32 a105, v224\n\t"
"v_accvgpr_write_b32 a107, v228\n\t"
"v_mfma_f32_16x16x32_bf16 v[36:39], a[232:235], a[176:179], v[36:39]\n\t"
"v_accvgpr_read_b32 v236, a114\n\t"
"v_accvgpr_read_b32 v238, a118\n\t"
"v_mfma_f32_16x16x32_bf16 v[36:39], a[208:211], a[180:183], v[36:39]\n\t"
"buffer_load_dwordx4 a[224:227], v252, s[8:11], s20 offen offset:2048 sc1\n\t"
"v_accvgpr_read_b32 v237, a115\n\t"
"v_accvgpr_read_b32 v239, a119\n\t"
"v_cvt_pk_bf16_f32 v230, v230, v231\n\t"
"v_cvt_pk_bf16_f32 v234, v234, v235\n\t"
"v_pk_fma_f32 v[236:237], v[238:239], s[56:57], v[236:237]\n\t"
"v_pk_fma_f32 v[238:239], v[236:237], s[58:59], v[238:239] neg_lo:[0,0,1] neg_hi:[0,0,1]\n\t"
"v_pk_fma_f32 v[98:99], v[70:71], s[60:61], v[236:237]\n\t"
"v_pk_fma_f32 v[70:71], v[236:237], s[60:61], v[70:71]\n\t"
"v_pk_fma_f32 v[236:237], v[170:171], s[62:63], v[238:239]\n\t"
"v_pk_fma_f32 v[170:171], v[238:239], s[62:63], v[170:171]\n\t"
"v_mfma_f32_16x16x32_bf16 v[40:43], a[228:231], a[176:179], v[40:43]\n\t"
"s_mov_b32 s20, 614400\n\t"
"v_accvgpr_write_b32 a112, v230\n\t"
"v_accvgpr_write_b32 a114, v234\n\t"
"v_mfma_f32_16x16x32_bf16 v[40:43], a[232:235], a[180:183], v[40:43]\n\t"
"v_accvgpr_read_b32 v224, a120\n\t"
"v_accvgpr_read_b32 v226, a124\n\t"
"v_mfma_f32_16x16x32_bf16 v[40:43], a[208:211], a[184:187], v[40:43]\n\t"
"buffer_load_dwordx4 a[88:91], v252, s[8:11], s20 offen offset:0 sc1\n\t"
"v_accvgpr_read_b32 v225, a121\n\t"
"v_accvgpr_read_b32 v227, a125\n\t"
"v_cvt_pk_bf16_f32 v236, v236, v237\n\t"
"v_cvt_pk_bf16_f32 v98, v98, v99\n\t"
"v_pk_fma_f32 v[224:225], v[226:227], s[56:57], v[224:225]\n\t"
"v_pk_fma_f32 v[226:227], v[224:225], s[58:59], v[226:227] neg_lo:[0,0,1] neg_hi:[0,0,1]\n\t"
"v_pk_fma_f32 v[228:229], v[72:73], s[60:61], v[224:225]\n\t"
"v_pk_fma_f32 v[72:73], v[224:225], s[60:61], v[72:73]\n\t"
"v_pk_fma_f32 v[224:225], v[172:173], s[62:63], v[226:227]\n\t"
"v_pk_fma_f32 v[172:173], v[226:227], s[62:63], v[172:173]\n\t"
"v_mfma_f32_16x16x32_bf16 v[44:47], a[228:231], a[180:183], v[44:47]\n\t"
"v_accvgpr_write_b32 a113, v236\n\t"
"v_accvgpr_write_b32 a115, v98\n\t"
"v_mfma_f32_16x16x32_bf16 v[44:47], a[232:235], a[184:187], v[44:47]\n\t"
"v_accvgpr_read_b32 v230, a122\n\t"
"v_accvgpr_read_b32 v232, a126\n\t"
"v_mfma_f32_16x16x32_bf16 v[44:47], a[208:211], a[188:191], v[44:47]\n\t"
"buffer_load_dwordx4 a[92:95], v252, s[8:11], s20 offen offset:1024 sc1\n\t"
"v_accvgpr_read_b32 v231, a123\n\t"
"v_accvgpr_read_b32 v233, a127\n\t"
"v_accvgpr_read_b32 v236, a128\n\t"
"v_accvgpr_read_b32 v238, a132\n\t"
"v_accvgpr_read_b32 v237, a129\n\t"
"v_accvgpr_read_b32 v239, a133\n\t"
"v_cvt_pk_bf16_f32 v224, v224, v225\n\t"
"v_cvt_pk_bf16_f32 v228, v228, v229\n\t"
"v_pk_fma_f32 v[230:231], v[232:233], s[56:57], v[230:231]\n\t"
"v_pk_fma_f32 v[232:233], v[230:231], s[58:59], v[232:233] neg_lo:[0,0,1] neg_hi:[0,0,1]\n\t"
"v_pk_fma_f32 v[234:235], v[74:75], s[60:61], v[230:231]\n\t"
"v_pk_fma_f32 v[74:75], v[230:231], s[60:61], v[74:75]\n\t"
"v_pk_fma_f32 v[230:231], v[174:175], s[62:63], v[232:233]\n\t"
"v_pk_fma_f32 v[174:175], v[232:233], s[62:63], v[174:175]\n\t"
"v_pk_fma_f32 v[236:237], v[238:239], s[56:57], v[236:237]\n\t"
"v_pk_fma_f32 v[238:239], v[236:237], s[58:59], v[238:239] neg_lo:[0,0,1] neg_hi:[0,0,1]\n\t"
"v_pk_fma_f32 v[98:99], v[76:77], s[60:61], v[236:237]\n\t"
"v_pk_fma_f32 v[76:77], v[236:237], s[60:61], v[76:77]\n\t"
"v_pk_fma_f32 v[236:237], v[176:177], s[62:63], v[238:239]\n\t"
"v_pk_fma_f32 v[176:177], v[238:239], s[62:63], v[176:177]\n\t"
"v_mfma_f32_16x16x32_bf16 v[48:51], a[228:231], a[184:187], v[48:51]\n\t"
"v_accvgpr_write_b32 a120, v224\n\t"
"v_accvgpr_write_b32 a122, v228\n\t"
"v_mfma_f32_16x16x32_bf16 v[48:51], a[232:235], a[188:191], v[48:51]\n\t"
"v_accvgpr_read_b32 v224, a130\n\t"
"v_accvgpr_read_b32 v226, a134\n\t"
"v_mfma_f32_16x16x32_bf16 v[48:51], a[208:211], a[192:195], v[48:51]\n\t"
"buffer_load_dwordx4 a[96:99], v252, s[8:11], s20 offen offset:2048 sc1\n\t"
"v_accvgpr_read_b32 v225, a131\n\t"
"v_accvgpr_read_b32 v227, a135\n\t"
"v_cvt_pk_bf16_f32 v230, v230, v231\n\t"
"v_cvt_pk_bf16_f32 v234, v234, v235\n\t"
"v_cvt_pk_bf16_f32 v236, v236, v237\n\t"
"v_cvt_pk_bf16_f32 v98, v98, v99\n\t"
"v_pk_fma_f32 v[224:225], v[226:227], s[56:57], v[224:225]\n\t"
"v_pk_fma_f32 v[226:227], v[224:225], s[58:59], v[226:227] neg_lo:[0,0,1] neg_hi:[0,0,1]\n\t"
"v_pk_fma_f32 v[228:229], v[78:79], s[60:61], v[224:225]\n\t"
"v_pk_fma_f32 v[78:79], v[224:225], s[60:61], v[78:79]\n\t"
"v_pk_fma_f32 v[224:225], v[178:179], s[62:63], v[226:227]\n\t"
"v_pk_fma_f32 v[178:179], v[226:227], s[62:63], v[178:179]\n\t"
"v_mfma_f32_16x16x32_bf16 v[52:55], a[228:231], a[188:191], v[52:55]\n\t"
"s_mov_b32 s20, 774144\n\t"
"v_accvgpr_write_b32 a121, v230\n\t"
"v_accvgpr_write_b32 a123, v234\n\t"
"v_mfma_f32_16x16x32_bf16 v[52:55], a[232:235], a[192:195], v[52:55]\n\t"
"v_accvgpr_write_b32 a128, v236\n\t"
"v_accvgpr_write_b32 a130, v98\n\t"
"v_mfma_f32_16x16x32_bf16 v[52:55], a[208:211], a[196:199], v[52:55]\n\t"
"s_waitcnt vmcnt(17)\n\t"
"ds_write_b128 v249, v[196:199] offset:18432\n\t"
"v_accvgpr_read_b32 v230, a136\n\t"
"v_accvgpr_read_b32 v232, a140\n\t"
"v_accvgpr_read_b32 v231, a137\n\t"
"v_accvgpr_read_b32 v233, a141\n\t"
"v_cvt_pk_bf16_f32 v224, v224, v225\n\t"
"v_cvt_pk_bf16_f32 v228, v228, v229\n\t"
"v_pk_fma_f32 v[230:231], v[232:233], s[56:57], v[230:231]\n\t"
"v_pk_fma_f32 v[232:233], v[230:231], s[58:59], v[232:233] neg_lo:[0,0,1] neg_hi:[0,0,1]\n\t"
"v_pk_fma_f32 v[234:235], v[80:81], s[60:61], v[230:231]\n\t"
"v_pk_fma_f32 v[80:81], v[230:231], s[60:61], v[80:81]\n\t"
"v_pk_fma_f32 v[230:231], v[180:181], s[62:63], v[232:233]\n\t"
"v_pk_fma_f32 v[180:181], v[232:233], s[62:63], v[180:181]\n\t"
"v_mfma_f32_16x16x32_bf16 v[56:59], a[228:231], a[192:195], v[56:59]\n\t"
"v_accvgpr_write_b32 a129, v224\n\t"
"v_accvgpr_write_b32 a131, v228\n\t"
"v_mfma_f32_16x16x32_bf16 v[56:59], a[232:235], a[196:199], v[56:59]\n\t"
"v_accvgpr_read_b32 v236, a138\n\t"
"v_accvgpr_read_b32 v238, a142\n\t"
"v_mfma_f32_16x16x32_bf16 v[56:59], a[208:211], a[200:203], v[56:59]\n\t"
"buffer_load_dwordx4 a[100:103], v252, s[8:11], s20 offen offset:0 sc1\n\t"
"v_accvgpr_read_b32 v237, a139\n\t"
"v_accvgpr_read_b32 v239, a143\n\t"
"v_cvt_pk_bf16_f32 v230, v230, v231\n\t"
"v_cvt_pk_bf16_f32 v234, v234, v235\n\t"
"v_pk_fma_f32 v[236:237], v[238:239], s[56:57], v[236:237]\n\t"
"v_pk_fma_f32 v[238:239], v[236:237], s[58:59], v[238:239] neg_lo:[0,0,1] neg_hi:[0,0,1]\n\t"
"v_pk_fma_f32 v[98:99], v[82:83], s[60:61], v[236:237]\n\t"
"v_pk_fma_f32 v[82:83], v[236:237], s[60:61], v[82:83]\n\t"
"v_pk_fma_f32 v[236:237], v[182:183], s[62:63], v[238:239]\n\t"
"v_pk_fma_f32 v[182:183], v[238:239], s[62:63], v[182:183]\n\t"
"v_mfma_f32_16x16x32_bf16 v[60:63], a[228:231], a[196:199], v[60:63]\n\t"
"v_accvgpr_write_b32 a136, v230\n\t"
"v_accvgpr_write_b32 a138, v234\n\t"
"v_mfma_f32_16x16x32_bf16 v[60:63], a[232:235], a[200:203], v[60:63]\n\t"
"v_accvgpr_read_b32 v224, a144\n\t"
"v_accvgpr_read_b32 v226, a148\n\t"
"v_mfma_f32_16x16x32_bf16 v[60:63], a[208:211], a[204:207], v[60:63]\n\t"
"buffer_load_dwordx4 a[108:111], v252, s[8:11], s20 offen offset:1024 sc1\n\t"
"v_accvgpr_read_b32 v225, a145\n\t"
"v_accvgpr_read_b32 v227, a149\n\t"
"v_cvt_pk_bf16_f32 v236, v236, v237\n\t"
"v_cvt_pk_bf16_f32 v98, v98, v99\n\t"
"v_pk_fma_f32 v[224:225], v[226:227], s[56:57], v[224:225]\n\t"
"v_pk_fma_f32 v[226:227], v[224:225], s[58:59], v[226:227] neg_lo:[0,0,1] neg_hi:[0,0,1]\n\t"
"v_pk_fma_f32 v[228:229], v[84:85], s[60:61], v[224:225]\n\t"
"v_pk_fma_f32 v[84:85], v[224:225], s[60:61], v[84:85]\n\t"
"v_pk_fma_f32 v[224:225], v[184:185], s[62:63], v[226:227]\n\t"
"v_pk_fma_f32 v[184:185], v[226:227], s[62:63], v[184:185]\n\t"
"v_mfma_f32_16x16x32_bf16 v[64:67], a[236:239], a[168:171], v[64:67]\n\t"
"ds_read_b128 a[168:171], v255 offset:2048\n\t"
"v_accvgpr_write_b32 a137, v236\n\t"
"v_mfma_f32_16x16x32_bf16 v[64:67], a[240:243], a[172:175], v[64:67]\n\t"
"v_accvgpr_write_b32 a139, v98\n\t"
"v_accvgpr_read_b32 v230, a146\n\t"
"v_mfma_f32_16x16x32_bf16 v[64:67], a[244:247], a[176:179], v[64:67]\n\t"
"ds_write_b64 v250, a[104:105] offset:18432\n\t"
"v_accvgpr_read_b32 v232, a150\n\t"
"v_accvgpr_read_b32 v231, a147\n\t"
"v_accvgpr_read_b32 v233, a151\n\t"
"v_cvt_pk_bf16_f32 v224, v224, v225\n\t"
"v_cvt_pk_bf16_f32 v228, v228, v229\n\t"
"v_pk_fma_f32 v[230:231], v[232:233], s[56:57], v[230:231]\n\t"
"v_pk_fma_f32 v[232:233], v[230:231], s[58:59], v[232:233] neg_lo:[0,0,1] neg_hi:[0,0,1]\n\t"
"v_pk_fma_f32 v[234:235], v[86:87], s[60:61], v[230:231]\n\t"
"v_pk_fma_f32 v[86:87], v[230:231], s[60:61], v[86:87]\n\t"
"v_pk_fma_f32 v[230:231], v[186:187], s[62:63], v[232:233]\n\t"
"v_pk_fma_f32 v[186:187], v[232:233], s[62:63], v[186:187]\n\t"
"v_mfma_f32_16x16x32_bf16 v[68:71], a[236:239], a[172:175], v[68:71]\n\t"
"ds_read_b128 a[172:175], v255 offset:5120\n\t"
"v_accvgpr_write_b32 a144, v224\n\t"
"v_mfma_f32_16x16x32_bf16 v[68:71], a[240:243], a[176:179], v[68:71]\n\t"
"v_accvgpr_write_b32 a146, v228\n\t"
"v_accvgpr_read_b32 v236, a152\n\t"
"v_mfma_f32_16x16x32_bf16 v[68:71], a[244:247], a[180:183], v[68:71]\n\t"
"buffer_load_dwordx4 a[228:231], v252, s[8:11], s20 offen offset:2048 sc1\n\t"
"v_accvgpr_read_b32 v238, a156\n\t"
"v_accvgpr_read_b32 v237, a153\n\t"
"v_accvgpr_read_b32 v239, a157\n\t"
"v_cvt_pk_bf16_f32 v230, v230, v231\n\t"
"v_cvt_pk_bf16_f32 v234, v234, v235\n\t"
"s_mov_b32 s20, 786432\n\t"
"v_pk_fma_f32 v[236:237], v[238:239], s[56:57], v[236:237]\n\t"
"v_pk_fma_f32 v[238:239], v[236:237], s[58:59], v[238:239] neg_lo:[0,0,1] neg_hi:[0,0,1]\n\t"
"v_pk_fma_f32 v[98:99], v[88:89], s[60:61], v[236:237]\n\t"
"v_pk_fma_f32 v[88:89], v[236:237], s[60:61], v[88:89]\n\t"
"v_pk_fma_f32 v[236:237], v[188:189], s[62:63], v[238:239]\n\t"
"v_pk_fma_f32 v[188:189], v[238:239], s[62:63], v[188:189]\n\t"
"v_mfma_f32_16x16x32_bf16 v[72:75], a[236:239], a[176:179], v[72:75]\n\t"
"ds_read_b128 a[176:179], v255 offset:8192\n\t"
"v_accvgpr_write_b32 a145, v230\n\t"
"v_mfma_f32_16x16x32_bf16 v[72:75], a[240:243], a[180:183], v[72:75]\n\t"
"v_accvgpr_write_b32 a147, v234\n\t"
"v_accvgpr_read_b32 v224, a154\n\t"
"v_mfma_f32_16x16x32_bf16 v[72:75], a[244:247], a[184:187], v[72:75]\n\t"
"buffer_load_dwordx4 a[208:211], v252, s[8:11], s20 offen offset:0 sc1\n\t"
"v_accvgpr_read_b32 v226, a158\n\t"
"v_accvgpr_read_b32 v225, a155\n\t"
"v_accvgpr_read_b32 v227, a159\n\t"
"v_cvt_pk_bf16_f32 v236, v236, v237\n\t"
"v_cvt_pk_bf16_f32 v98, v98, v99\n\t"
"v_pk_fma_f32 v[224:225], v[226:227], s[56:57], v[224:225]\n\t"
"v_pk_fma_f32 v[226:227], v[224:225], s[58:59], v[226:227] neg_lo:[0,0,1] neg_hi:[0,0,1]\n\t"
"v_pk_fma_f32 v[228:229], v[90:91], s[60:61], v[224:225]\n\t"
"v_pk_fma_f32 v[90:91], v[224:225], s[60:61], v[90:91]\n\t"
"v_pk_fma_f32 v[224:225], v[190:191], s[62:63], v[226:227]\n\t"
"v_pk_fma_f32 v[190:191], v[226:227], s[62:63], v[190:191]\n\t"
"v_mfma_f32_16x16x32_bf16 v[76:79], a[236:239], a[180:183], v[76:79]\n\t"
"ds_read_b128 a[180:183], v255 offset:11264\n\t"
"v_accvgpr_write_b32 a152, v236\n\t"
"v_mfma_f32_16x16x32_bf16 v[76:79], a[240:243], a[184:187], v[76:79]\n\t"
"v_accvgpr_write_b32 a154, v98\n\t"
"v_accvgpr_read_b32 v230, a160\n\t"
"v_mfma_f32_16x16x32_bf16 v[76:79], a[244:247], a[188:191], v[76:79]\n\t"
"buffer_load_dwordx4 a[232:235], v252, s[8:11], s20 offen offset:1024 sc1\n\t"
"v_accvgpr_read_b32 v232, a164\n\t"
"v_accvgpr_read_b32 v231, a161\n\t"
"v_accvgpr_read_b32 v233, a165\n\t"
"v_cvt_pk_bf16_f32 v224, v224, v225\n\t"
"v_cvt_pk_bf16_f32 v228, v228, v229\n\t"
"v_pk_fma_f32 v[230:231], v[232:233], s[56:57], v[230:231]\n\t"
"v_pk_fma_f32 v[232:233], v[230:231], s[58:59], v[232:233] neg_lo:[0,0,1] neg_hi:[0,0,1]\n\t"
"v_pk_fma_f32 v[234:235], v[92:93], s[60:61], v[230:231]\n\t"
"v_pk_fma_f32 v[92:93], v[230:231], s[60:61], v[92:93]\n\t"
"v_pk_fma_f32 v[230:231], v[192:193], s[62:63], v[232:233]\n\t"
"v_pk_fma_f32 v[192:193], v[232:233], s[62:63], v[192:193]\n\t"
"v_mfma_f32_16x16x32_bf16 v[80:83], a[236:239], a[184:187], v[80:83]\n\t"
"ds_read_b128 a[184:187], v255 offset:14336\n\t"
"v_accvgpr_write_b32 a153, v224\n\t"
"v_mfma_f32_16x16x32_bf16 v[80:83], a[240:243], a[188:191], v[80:83]\n\t"
"v_accvgpr_write_b32 a155, v228\n\t"
"v_accvgpr_read_b32 v236, a162\n\t"
"v_mfma_f32_16x16x32_bf16 v[80:83], a[244:247], a[192:195], v[80:83]\n\t"
"buffer_load_dwordx4 a[116:119], v252, s[8:11], s20 offen offset:2048 sc1\n\t"
"v_accvgpr_read_b32 v238, a166\n\t"
"v_accvgpr_read_b32 v237, a163\n\t"
"v_accvgpr_read_b32 v239, a167\n\t"
"v_cvt_pk_bf16_f32 v230, v230, v231\n\t"
"v_cvt_pk_bf16_f32 v234, v234, v235\n\t"
"s_mov_b32 s20, 798720\n\t"
"v_pk_fma_f32 v[236:237], v[238:239], s[56:57], v[236:237]\n\t"
"v_pk_fma_f32 v[238:239], v[236:237], s[58:59], v[238:239] neg_lo:[0,0,1] neg_hi:[0,0,1]\n\t"
"v_pk_fma_f32 v[98:99], v[94:95], s[60:61], v[236:237]\n\t"
"v_pk_fma_f32 v[94:95], v[236:237], s[60:61], v[94:95]\n\t"
"v_pk_fma_f32 v[236:237], v[194:195], s[62:63], v[238:239]\n\t"
"v_pk_fma_f32 v[194:195], v[238:239], s[62:63], v[194:195]\n\t"
"v_mfma_f32_16x16x32_bf16 v[84:87], a[236:239], a[188:191], v[84:87]\n\t"
"ds_read_b128 a[188:191], v255 offset:17408\n\t"
"v_accvgpr_write_b32 a160, v230\n\t"
"v_mfma_f32_16x16x32_bf16 v[84:87], a[240:243], a[192:195], v[84:87]\n\t"
"v_accvgpr_write_b32 a162, v234\n\t"
"v_cvt_pk_bf16_f32 v236, v236, v237\n\t"
"v_mfma_f32_16x16x32_bf16 v[84:87], a[244:247], a[196:199], v[84:87]\n\t"
"buffer_load_dwordx4 a[124:127], v252, s[8:11], s20 offen offset:0 sc1\n\t"
"v_cvt_pk_bf16_f32 v98, v98, v99\n\t"
"v_mfma_f32_16x16x32_bf16 v[88:91], a[236:239], a[192:195], v[88:91]\n\t"
"ds_read_b128 a[192:195], v255 offset:20480\n\t"
"v_accvgpr_write_b32 a161, v236\n\t"
"v_mfma_f32_16x16x32_bf16 v[88:91], a[240:243], a[196:199], v[88:91]\n\t"
"v_accvgpr_write_b32 a163, v98\n\t"
"v_mfma_f32_16x16x32_bf16 v[88:91], a[244:247], a[200:203], v[88:91]\n\t"
"ds_write_b128 v249, v[200:203] offset:24576\n\t"
"buffer_load_dwordx4 a[132:135], v252, s[8:11], s20 offen offset:1024 sc1\n\t"
"v_mfma_f32_16x16x32_bf16 v[92:95], a[236:239], a[196:199], v[92:95]\n\t"
"buffer_load_dwordx4 a[140:143], v252, s[8:11], s20 offen offset:2048 sc1\n\t"
"v_mfma_f32_16x16x32_bf16 v[92:95], a[240:243], a[200:203], v[92:95]\n\t"
"s_waitcnt vmcnt(24) lgkmcnt(5)\n\t"
"ds_read_b128 a[196:199], v255 offset:23552\n\t"
"s_mov_b32 s20, 884736\n\t"
"v_mfma_f32_16x16x32_bf16 v[92:95], a[244:247], a[204:207], v[92:95]\n\t"
"ds_write_b64 v250, a[106:107] offset:24576\n\t"
"v_mfma_f32_16x16x32_bf16 v[100:103], a[248:251], a[168:171], v[100:103]\n\t"
"buffer_load_dwordx4 a[148:151], v252, s[8:11], s20 offen offset:0 sc1\n\t"
"v_mfma_f32_16x16x32_bf16 v[100:103], a[52:55], a[172:175], v[100:103]\n\t"
"s_waitcnt lgkmcnt(6)\n\t"
"ds_read_b128 a[200:203], v255 offset:26624\n\t"
"v_mfma_f32_16x16x32_bf16 v[100:103], a[56:59], a[176:179], v[100:103]\n\t"
"ds_read_b128 a[204:207], v255 offset:29696\n\t"
"v_mfma_f32_16x16x32_bf16 v[104:107], a[248:251], a[172:175], v[104:107]\n\t"
"buffer_load_dwordx4 a[236:239], v252, s[8:11], s20 offen offset:1024 sc1\n\t"
"v_mfma_f32_16x16x32_bf16 v[104:107], a[52:55], a[176:179], v[104:107]\n\t"
"s_waitcnt lgkmcnt(7)\n\t"
"v_mfma_f32_16x16x32_bf16 v[104:107], a[56:59], a[180:183], v[104:107]\n\t"
"s_waitcnt lgkmcnt(0)\n\t"
"s_barrier\n\t"
"v_mfma_f32_16x16x32_bf16 v[108:111], a[248:251], a[176:179], v[108:111]\n\t"
"ds_read_b128 a[40:43], v252 offset:18432\n\t"
"v_mfma_f32_16x16x32_bf16 v[108:111], a[52:55], a[180:183], v[108:111]\n\t"
"buffer_load_dwordx4 a[240:243], v252, s[8:11], s20 offen offset:2048 sc1\n\t"
"v_mfma_f32_16x16x32_bf16 v[108:111], a[56:59], a[184:187], v[108:111]\n\t"
"s_mov_b32 s20, 1474560\n\t"
"ds_read_b128 a[44:47], v252 offset:19456\n\t"
"v_add_u32 v224, 98304, v249\n\t"
"v_mfma_f32_16x16x32_bf16 v[112:115], a[248:251], a[180:183], v[112:115]\n\t"
"s_add_u32 s29, s22, 384\n\t"
"ds_read_b128 a[48:51], v252 offset:20480\n\t"
"v_add_u32 v225, 98304, v250\n\t"
"v_mfma_f32_16x16x32_bf16 v[112:115], a[52:55], a[184:187], v[112:115]\n\t"
"v_add_u32 v226, 98304, v252\n\t"
"v_mfma_f32_16x16x32_bf16 v[112:115], a[56:59], a[188:191], v[112:115]\n\t"
"buffer_load_dwordx4 a[244:247], v252, s[8:11], s20 offen offset:0 sc1\n\t"
"v_mfma_f32_16x16x32_bf16 v[116:119], a[248:251], a[184:187], v[116:119]\n\t"
"buffer_load_dwordx4 a[104:107], v252, s[8:11], s20 offen offset:1024 sc1\n\t"
"v_mfma_f32_16x16x32_bf16 v[116:119], a[52:55], a[188:191], v[116:119]\n\t"
"buffer_load_dwordx4 a[156:159], v252, s[8:11], s20 offen offset:2048 sc1\n\t"
"v_mfma_f32_16x16x32_bf16 v[116:119], a[56:59], a[192:195], v[116:119]\n\t"
"s_mov_b32 s20, 897024\n\t"
"ds_write_b128 v249, v[204:207] offset:30720\n\t"
"s_waitcnt lgkmcnt(3)\n\t"
"v_mfma_f32_16x16x32_bf16 v[120:123], a[248:251], a[188:191], v[120:123]\n\t"
"buffer_load_dwordx4 a[164:167], v252, s[8:11], s20 offen offset:0 sc1\n\t"
"v_mfma_f32_16x16x32_bf16 v[120:123], a[52:55], a[192:195], v[120:123]\n\t"
"buffer_load_dwordx4 v[240:243], v252, s[8:11], s20 offen offset:1024 sc1\n\t"
"v_mfma_f32_16x16x32_bf16 v[120:123], a[56:59], a[196:199], v[120:123]\n\t"
"s_waitcnt lgkmcnt(2)\n\t"
"ds_write_b128 v224, v[212:215] offset:0\n\t"
"v_mfma_f32_16x16x32_bf16 v[124:127], a[248:251], a[192:195], v[124:127]\n\t"
"s_waitcnt lgkmcnt(2)\n\t"
"v_mfma_f32_16x16x32_bf16 v[124:127], a[52:55], a[196:199], v[124:127]\n\t"
"buffer_load_dwordx4 v[96:99], v252, s[8:11], s20 offen offset:2048 sc1\n\t"
"v_mfma_f32_16x16x32_bf16 v[124:127], a[56:59], a[200:203], v[124:127]\n\t"
"s_mov_b32 s20, 1486848\n\t"
"ds_write_b128 v224, a[0:3] offset:12288\n\t"
"v_mfma_f32_16x16x32_bf16 v[128:131], a[248:251], a[196:199], v[128:131]\n\t"
"s_waitcnt vmcnt(30)\n\t"
"v_mfma_f32_16x16x32_bf16 v[128:131], a[52:55], a[200:203], v[128:131]\n\t"
"buffer_load_dwordx4 a[248:251], v252, s[8:11], s20 offen offset:0 sc1\n\t"
"v_mfma_f32_16x16x32_bf16 v[128:131], a[56:59], a[204:207], v[128:131]\n\t"
"ds_write_b128 v224, a[8:11] offset:24576\n\t"
"v_mfma_f32_16x16x32_bf16 v[132:135], a[60:63], a[168:171], v[132:135]\n\t"
"v_mfma_f32_16x16x32_bf16 v[132:135], a[252:255], a[172:175], v[132:135]\n\t"
"buffer_load_dwordx4 a[52:55], v252, s[8:11], s20 offen offset:1024 sc1\n\t"
"v_mfma_f32_16x16x32_bf16 v[132:135], a[212:215], a[176:179], v[132:135]\n\t"
"ds_write_b128 v224, a[16:19] offset:36864\n\t"
"ds_write_b128 v224, a[24:27] offset:49152\n\t"
"buffer_store_dwordx4 a[40:43], v248, s[16:19], s29 offen offset:0 nt\n\t"
"v_mfma_f32_16x16x32_bf16 v[136:139], a[60:63], a[172:175], v[136:139]\n\t"
"v_mfma_f32_16x16x32_bf16 v[136:139], a[252:255], a[176:179], v[136:139]\n\t"
"buffer_load_dwordx4 a[56:59], v252, s[8:11], s20 offen offset:2048 sc1\n\t"
"v_mfma_f32_16x16x32_bf16 v[136:139], a[212:215], a[180:183], v[136:139]\n\t"
"buffer_store_dwordx4 a[44:47], v248, s[16:19], s29 offen offset:128 nt\n\t"
"ds_write_b64 v250, a[112:113] offset:30720\n\t"
"s_mov_b32 s20, 909312\n\t"
"v_mfma_f32_16x16x32_bf16 v[140:143], a[60:63], a[176:179], v[140:143]\n\t"
"v_mfma_f32_16x16x32_bf16 v[140:143], a[252:255], a[180:183], v[140:143]\n\t"
"v_mfma_f32_16x16x32_bf16 v[140:143], a[212:215], a[184:187], v[140:143]\n\t"
"buffer_store_dwordx4 a[48:51], v248, s[16:19], s29 offen offset:256 nt\n\t"
"ds_write_b64 v225, a[120:121] offset:0\n\t"
"v_mfma_f32_16x16x32_bf16 v[144:147], a[60:63], a[180:183], v[144:147]\n\t"
"v_mfma_f32_16x16x32_bf16 v[144:147], a[252:255], a[184:187], v[144:147]\n\t"
"buffer_load_dwordx4 a[48:51], v252, s[8:11], s20 offen offset:0 sc1\n\t"
"v_mfma_f32_16x16x32_bf16 v[144:147], a[212:215], a[188:191], v[144:147]\n\t"
"ds_write_b64 v225, a[128:129] offset:12288\n\t"
"ds_write_b64 v225, a[136:137] offset:24576\n\t"
"ds_write_b64 v225, a[144:145] offset:36864\n\t"
"ds_write_b64 v225, a[152:153] offset:49152\n\t"
"v_mfma_f32_16x16x32_bf16 v[148:151], a[60:63], a[184:187], v[148:151]\n\t"
"buffer_load_dwordx4 v[236:239], v252, s[8:11], s20 offen offset:1024 sc1\n\t"
"v_mfma_f32_16x16x32_bf16 v[148:151], a[252:255], a[188:191], v[148:151]\n\t"
"v_mfma_f32_16x16x32_bf16 v[148:151], a[212:215], a[192:195], v[148:151]\n\t"
"ds_write_b128 v249, v[208:211] offset:36864\n\t"
"v_mfma_f32_16x16x32_bf16 v[152:155], a[60:63], a[188:191], v[152:155]\n\t"
"v_mfma_f32_16x16x32_bf16 v[152:155], a[252:255], a[192:195], v[152:155]\n\t"
"v_mfma_f32_16x16x32_bf16 v[152:155], a[212:215], a[196:199], v[152:155]\n\t"
"ds_write_b128 v224, v[216:219] offset:6144\n\t"
"v_mfma_f32_16x16x32_bf16 v[156:159], a[60:63], a[192:195], v[156:159]\n\t"
"v_mfma_f32_16x16x32_bf16 v[156:159], a[252:255], a[196:199], v[156:159]\n\t"
"v_mfma_f32_16x16x32_bf16 v[156:159], a[212:215], a[200:203], v[156:159]\n\t"
"ds_write_b128 v224, a[4:7] offset:18432\n\t"
"v_mfma_f32_16x16x32_bf16 v[160:163], a[60:63], a[196:199], v[160:163]\n\t"
"buffer_load_dwordx4 a[60:63], v252, s[8:11], s20 offen offset:2048 sc1\n\t"
"v_mfma_f32_16x16x32_bf16 v[160:163], a[252:255], a[200:203], v[160:163]\n\t"
"s_waitcnt vmcnt(36)\n\t"
"s_mov_b32 s20, 1499136\n\t"
"v_mfma_f32_16x16x32_bf16 v[160:163], a[212:215], a[204:207], v[160:163]\n\t"
"ds_write_b128 v224, a[12:15] offset:30720\n\t"
"ds_write_b128 v224, a[20:23] offset:43008\n\t"
"ds_write_b128 v224, a[28:31] offset:55296\n\t"
"v_mfma_f32_16x16x32_bf16 v[164:167], a[64:67], a[168:171], v[164:167]\n\t"
"buffer_load_dwordx4 a[212:215], v252, s[8:11], s20 offen offset:0 sc1\n\t"
"v_mfma_f32_16x16x32_bf16 v[164:167], a[68:71], a[172:175], v[164:167]\n\t"
"ds_read_b128 a[168:171], v244 offset:0\n\t"
"v_mfma_f32_16x16x32_bf16 v[164:167], a[72:75], a[176:179], v[164:167]\n\t"
"ds_write_b64 v250, a[114:115] offset:36864\n\t"
"v_mfma_f32_16x16x32_bf16 v[168:171], a[64:67], a[172:175], v[168:171]\n\t"
"buffer_load_dwordx4 a[252:255], v252, s[8:11], s20 offen offset:1024 sc1\n\t"
"v_mfma_f32_16x16x32_bf16 v[168:171], a[68:71], a[176:179], v[168:171]\n\t"
"ds_read_b128 a[172:175], v244 offset:512\n\t"
"v_mfma_f32_16x16x32_bf16 v[168:171], a[72:75], a[180:183], v[168:171]\n\t"
"ds_write_b64 v225, a[122:123] offset:6144\n\t"
"v_mfma_f32_16x16x32_bf16 v[172:175], a[64:67], a[176:179], v[172:175]\n\t"
"ds_read_b128 a[176:179], v244 offset:1024\n\t"
"v_mfma_f32_16x16x32_bf16 v[172:175], a[68:71], a[180:183], v[172:175]\n\t"
"v_mfma_f32_16x16x32_bf16 v[172:175], a[72:75], a[184:187], v[172:175]\n\t"
"ds_write_b64 v225, a[130:131] offset:18432\n\t"
"ds_write_b64 v225, a[138:139] offset:30720\n\t"
"ds_write_b64 v225, a[146:147] offset:43008\n\t"
"ds_write_b64 v225, a[154:155] offset:55296\n\t"
"v_mfma_f32_16x16x32_bf16 v[176:179], a[64:67], a[180:183], v[176:179]\n\t"
"ds_read_b128 a[180:183], v244 offset:1536\n\t"
"v_mfma_f32_16x16x32_bf16 v[176:179], a[68:71], a[184:187], v[176:179]\n\t"
"v_mfma_f32_16x16x32_bf16 v[176:179], a[72:75], a[188:191], v[176:179]\n\t"
"s_waitcnt lgkmcnt(1)\n\t"
"s_barrier\n\t"
"v_mfma_f32_16x16x32_bf16 v[180:183], a[64:67], a[184:187], v[180:183]\n\t"
"ds_read_b128 v[196:199], v252 offset:30720\n\t"
"v_mfma_f32_16x16x32_bf16 v[180:183], a[68:71], a[188:191], v[180:183]\n\t"
"ds_read_b128 v[200:203], v252 offset:31744\n\t"
"v_mfma_f32_16x16x32_bf16 v[180:183], a[72:75], a[192:195], v[180:183]\n\t"
"ds_read_b128 a[184:187], v244 offset:2048\n\t"
"v_mfma_f32_16x16x32_bf16 v[184:187], a[64:67], a[188:191], v[184:187]\n\t"
"buffer_load_dwordx4 a[28:31], v252, s[8:11], s20 offen offset:2048 sc1\n\t"
"v_mfma_f32_16x16x32_bf16 v[184:187], a[68:71], a[192:195], v[184:187]\n\t"
"s_mov_b32 s20, 1069056\n\t"
"ds_read_b128 a[40:43], v252 offset:32768\n\t"
"s_add_u32 s29, s22, 196992\n\t"
"v_mfma_f32_16x16x32_bf16 v[184:187], a[72:75], a[196:199], v[184:187]\n\t"
"ds_read_b128 a[188:191], v244 offset:2560\n\t"
"v_mfma_f32_16x16x32_bf16 v[188:191], a[64:67], a[192:195], v[188:191]\n\t"
"buffer_load_dwordx4 a[112:115], v252, s[8:11], s20 offen offset:0 sc1\n\t"
"v_mfma_f32_16x16x32_bf16 v[188:191], a[68:71], a[196:199], v[188:191]\n\t"
"s_waitcnt lgkmcnt(4)\n\t"
"ds_read_b128 a[44:47], v226 offset:0\n\t"
"v_mfma_f32_16x16x32_bf16 v[188:191], a[72:75], a[200:203], v[188:191]\n\t"
"ds_read_b128 a[192:195], v244 offset:3072\n\t"
"v_mfma_f32_16x16x32_bf16 v[192:195], a[64:67], a[196:199], v[192:195]\n\t"
"buffer_load_dwordx4 a[120:123], v252, s[8:11], s20 offen offset:1024 sc1\n\t"
"v_mfma_f32_16x16x32_bf16 v[192:195], a[68:71], a[200:203], v[192:195]\n\t"
"s_waitcnt vmcnt(38)\n\t"
"ds_read_b128 v[204:207], v226 offset:1024\n\t"
"s_waitcnt lgkmcnt(6)\n\t"
"v_mfma_f32_16x16x32_bf16 v[192:195], a[72:75], a[204:207], v[192:195]\n\t"
"ds_write_b128 v249, a[32:35] offset:18432\n\t"
"Lst33_%=:\n\t"
"v_mfma_f32_16x16x32_bf16 v[0:3], a[76:79], a[168:171], v[0:3]\n\t"
"buffer_load_dwordx4 a[64:67], v252, s[8:11], s20 offen offset:2048 sc1\n\t"
"v_mfma_f32_16x16x32_bf16 v[0:3], a[80:83], a[172:175], v[0:3]\n\t"
"s_mov_b32 s20, 1658880\n\t"
"ds_read_b128 a[196:199], v244 offset:3584\n\t"
"v_mfma_f32_16x16x32_bf16 v[0:3], a[84:87], a[176:179], v[0:3]\n\t"
"buffer_store_dwordx4 v[196:199], v248, s[16:19], s29 offen offset:0 nt\n\t"
"ds_read_b128 v[208:211], v226 offset:2048\n\t"
"ds_read_b128 a[200:203], v244 offset:4096\n\t"
"s_waitcnt lgkmcnt(8)\n\t"
"v_mfma_f32_16x16x32_bf16 v[4:7], a[76:79], a[172:175], v[4:7]\n\t"
"s_waitcnt lgkmcnt(6)\n\t"
"ds_read_b128 v[212:215], v226 offset:12288\n\t"
"v_mfma_f32_16x16x32_bf16 v[4:7], a[80:83], a[176:179], v[4:7]\n\t"
"buffer_load_dwordx4 a[68:71], v252, s[8:11], s20 offen offset:0 sc1\n\t"
"v_mfma_f32_16x16x32_bf16 v[4:7], a[84:87], a[180:183], v[4:7]\n\t"
"buffer_store_dwordx4 v[200:203], v248, s[16:19], s29 offen offset:128 nt\n\t"
"ds_read_b128 a[204:207], v244 offset:4608\n\t"
"buffer_load_dwordx4 a[72:75], v252, s[8:11], s20 offen offset:1024 sc1\n\t"
"v_mfma_f32_16x16x32_bf16 v[8:11], a[76:79], a[176:179], v[8:11]\n\t"
"s_waitcnt lgkmcnt(6)\n\t"
"ds_read_b128 v[216:219], v226 offset:13312\n\t"
"v_mfma_f32_16x16x32_bf16 v[8:11], a[80:83], a[180:183], v[8:11]\n\t"
"v_mfma_f32_16x16x32_bf16 v[8:11], a[84:87], a[184:187], v[8:11]\n\t"
"ds_write_b64 v250, a[160:161] offset:18432\n\t"
"v_mfma_f32_16x16x32_bf16 v[12:15], a[76:79], a[180:183], v[12:15]\n\t"
"s_waitcnt lgkmcnt(5)\n\t"
"ds_read_b128 v[196:199], v226 offset:14336\n\t"
"v_mfma_f32_16x16x32_bf16 v[12:15], a[80:83], a[184:187], v[12:15]\n\t"
"v_mfma_f32_16x16x32_bf16 v[12:15], a[84:87], a[188:191], v[12:15]\n\t"
"buffer_store_dwordx4 a[40:43], v248, s[16:19], s29 offen offset:256 nt\n\t"
"ds_write_b64 v250, a[162:163] offset:24576\n\t"
"s_add_u32 s29, s22, 393600\n\t"
"v_mfma_f32_16x16x32_bf16 v[16:19], a[76:79], a[184:187], v[16:19]\n\t"
"s_waitcnt lgkmcnt(5)\n\t"
"v_mfma_f32_16x16x32_bf16 v[16:19], a[80:83], a[188:191], v[16:19]\n\t"
"v_mfma_f32_16x16x32_bf16 v[16:19], a[84:87], a[192:195], v[16:19]\n\t"
"buffer_store_dwordx4 a[44:47], v248, s[16:19], s29 offen offset:0 nt\n\t"
"ds_read_b128 v[200:203], v226 offset:24576\n\t"
"ds_read_b128 a[4:7], v226 offset:36864\n\t"
"v_mfma_f32_16x16x32_bf16 v[20:23], a[76:79], a[188:191], v[20:23]\n\t"
"s_waitcnt lgkmcnt(5)\n\t"
"ds_read_b128 a[16:19], v226 offset:49152\n\t"
"v_mfma_f32_16x16x32_bf16 v[20:23], a[80:83], a[192:195], v[20:23]\n\t"
"ds_read_b128 a[0:3], v226 offset:25600\n\t"
"v_mfma_f32_16x16x32_bf16 v[20:23], a[84:87], a[196:199], v[20:23]\n\t"
"buffer_store_dwordx4 v[204:207], v248, s[16:19], s29 offen offset:128 nt\n\t"
"ds_read_b128 a[8:11], v226 offset:37888\n\t"
"ds_read_b128 a[20:23], v226 offset:50176\n\t"
"v_mfma_f32_16x16x32_bf16 v[24:27], a[76:79], a[192:195], v[24:27]\n\t"
"ds_read_b128 v[204:207], v226 offset:26624\n\t"
"v_mfma_f32_16x16x32_bf16 v[24:27], a[80:83], a[196:199], v[24:27]\n\t"
"ds_read_b128 a[12:15], v226 offset:38912\n\t"
"v_mfma_f32_16x16x32_bf16 v[24:27], a[84:87], a[200:203], v[24:27]\n\t"
"buffer_store_dwordx4 v[208:211], v248, s[16:19], s29 offen offset:256 nt\n\t"
"ds_read_b128 a[24:27], v226 offset:51200\n\t"
"s_add_u32 s29, s22, 590208\n\t"
"v_mfma_f32_16x16x32_bf16 v[28:31], a[76:79], a[196:199], v[28:31]\n\t"
"s_waitcnt vmcnt(44)\n\t"
"v_mfma_f32_16x16x32_bf16 v[28:31], a[80:83], a[200:203], v[28:31]\n\t"
"v_mfma_f32_16x16x32_bf16 v[28:31], a[84:87], a[204:207], v[28:31]\n\t"
"ds_write_b128 v249, a[36:39] offset:24576\n\t"
"v_mfma_f32_16x16x32_bf16 v[32:35], a[220:223], a[168:171], v[32:35]\n\t"
"s_waitcnt lgkmcnt(11)\n\t"
"v_mfma_f32_16x16x32_bf16 v[32:35], a[216:219], a[172:175], v[32:35]\n\t"
"buffer_load_dwordx4 a[40:43], v252, s[8:11], s20 offen offset:2048 sc1\n\t"
"v_mfma_f32_16x16x32_bf16 v[32:35], a[224:227], a[176:179], v[32:35]\n\t"
"buffer_store_dwordx4 v[212:215], v248, s[16:19], s29 offen offset:0 nt\n\t"
"v_mfma_f32_16x16x32_bf16 v[36:39], a[220:223], a[172:175], v[36:39]\n\t"
"s_mov_b32 s20, 1081344\n\t"
"v_mfma_f32_16x16x32_bf16 v[36:39], a[216:219], a[176:179], v[36:39]\n\t"
"v_mfma_f32_16x16x32_bf16 v[36:39], a[224:227], a[180:183], v[36:39]\n\t"
"buffer_store_dwordx4 v[216:219], v248, s[16:19], s29 offen offset:128 nt\n\t"
"v_mfma_f32_16x16x32_bf16 v[40:43], a[220:223], a[176:179], v[40:43]\n\t"
"v_mfma_f32_16x16x32_bf16 v[40:43], a[216:219], a[180:183], v[40:43]\n\t"
"v_mfma_f32_16x16x32_bf16 v[40:43], a[224:227], a[184:187], v[40:43]\n\t"
"buffer_store_dwordx4 v[196:199], v248, s[16:19], s29 offen offset:256 nt\n\t"
"s_add_u32 s29, s22, 786816\n\t"
"v_mfma_f32_16x16x32_bf16 v[44:47], a[220:223], a[180:183], v[44:47]\n\t"
"s_waitcnt lgkmcnt(9)\n\t"
"v_mfma_f32_16x16x32_bf16 v[44:47], a[216:219], a[184:187], v[44:47]\n\t"
"v_mfma_f32_16x16x32_bf16 v[44:47], a[224:227], a[188:191], v[44:47]\n\t"
"s_waitcnt lgkmcnt(0)\n\t"
"s_barrier\n\t"
"v_mfma_f32_16x16x32_bf16 v[48:51], a[220:223], a[184:187], v[48:51]\n\t"
"v_mfma_f32_16x16x32_bf16 v[48:51], a[216:219], a[188:191], v[48:51]\n\t"
"buffer_load_dwordx4 a[44:47], v252, s[8:11], s20 offen offset:0 sc1\n\t"
"v_mfma_f32_16x16x32_bf16 v[48:51], a[224:227], a[192:195], v[48:51]\n\t"
"buffer_load_dwordx4 a[32:35], v252, s[8:11], s20 offen offset:1024 sc1\n\t"
"v_mfma_f32_16x16x32_bf16 v[52:55], a[220:223], a[188:191], v[52:55]\n\t"
"v_mfma_f32_16x16x32_bf16 v[52:55], a[216:219], a[192:195], v[52:55]\n\t"
"v_mfma_f32_16x16x32_bf16 v[52:55], a[224:227], a[196:199], v[52:55]\n\t"
"buffer_store_dwordx4 v[200:203], v248, s[16:19], s29 offen offset:0 nt\n\t"
"v_mfma_f32_16x16x32_bf16 v[56:59], a[220:223], a[192:195], v[56:59]\n\t"
"v_mfma_f32_16x16x32_bf16 v[56:59], a[216:219], a[196:199], v[56:59]\n\t"
"buffer_load_dwordx4 a[36:39], v252, s[8:11], s20 offen offset:2048 sc1\n\t"
"v_mfma_f32_16x16x32_bf16 v[56:59], a[224:227], a[200:203], v[56:59]\n\t"
"s_mov_b32 s20, 1671168\n\t"
"v_mfma_f32_16x16x32_bf16 v[60:63], a[220:223], a[196:199], v[60:63]\n\t"
"s_waitcnt vmcnt(49)\n\t"
"v_mfma_f32_16x16x32_bf16 v[60:63], a[216:219], a[200:203], v[60:63]\n\t"
"v_mfma_f32_16x16x32_bf16 v[60:63], a[224:227], a[204:207], v[60:63]\n\t"
"buffer_store_dwordx4 a[0:3], v248, s[16:19], s29 offen offset:128 nt\n\t"
"v_mfma_f32_16x16x32_bf16 v[64:67], a[88:91], a[168:171], v[64:67]\n\t"
"ds_read_b128 a[168:171], v244 offset:9216\n\t"
"v_mfma_f32_16x16x32_bf16 v[64:67], a[92:95], a[172:175], v[64:67]\n\t"
"v_mfma_f32_16x16x32_bf16 v[64:67], a[96:99], a[176:179], v[64:67]\n\t"
"buffer_load_dwordx4 a[216:219], v252, s[8:11], s20 offen offset:0 sc1\n\t"
"v_mfma_f32_16x16x32_bf16 v[68:71], a[88:91], a[172:175], v[68:71]\n\t"
"ds_read_b128 a[172:175], v244 offset:9728\n\t"
"v_mfma_f32_16x16x32_bf16 v[68:71], a[92:95], a[176:179], v[68:71]\n\t"
"v_mfma_f32_16x16x32_bf16 v[68:71], a[96:99], a[180:183], v[68:71]\n\t"
"v_mfma_f32_16x16x32_bf16 v[72:75], a[88:91], a[176:179], v[72:75]\n\t"
"ds_read_b128 a[176:179], v244 offset:10240\n\t"
"v_mfma_f32_16x16x32_bf16 v[72:75], a[92:95], a[180:183], v[72:75]\n\t"
"v_mfma_f32_16x16x32_bf16 v[72:75], a[96:99], a[184:187], v[72:75]\n\t"
"buffer_store_dwordx4 v[204:207], v248, s[16:19], s29 offen offset:256 nt\n\t"
"ds_read_b128 v[196:199], v252 offset:18432\n\t"
"v_mfma_f32_16x16x32_bf16 v[76:79], a[88:91], a[180:183], v[76:79]\n\t"
"s_add_u32 s29, s22, 983424\n\t"
"ds_read_b128 a[180:183], v244 offset:10752\n\t"
"v_mfma_f32_16x16x32_bf16 v[76:79], a[92:95], a[184:187], v[76:79]\n\t"
"buffer_load_dwordx4 a[220:223], v252, s[8:11], s20 offen offset:1024 sc1\n\t"
"v_mfma_f32_16x16x32_bf16 v[76:79], a[96:99], a[188:191], v[76:79]\n\t"
"ds_read_b128 v[200:203], v252 offset:19456\n\t"
"v_mfma_f32_16x16x32_bf16 v[80:83], a[88:91], a[184:187], v[80:83]\n\t"
"ds_read_b128 a[184:187], v244 offset:11264\n\t"
"v_mfma_f32_16x16x32_bf16 v[80:83], a[92:95], a[188:191], v[80:83]\n\t"
"buffer_load_dwordx4 a[224:227], v252, s[8:11], s20 offen offset:2048 sc1\n\t"
"v_mfma_f32_16x16x32_bf16 v[80:83], a[96:99], a[192:195], v[80:83]\n\t"
"ds_read_b128 v[208:211], v252 offset:20480\n\t"
"v_mfma_f32_16x16x32_bf16 v[84:87], a[88:91], a[188:191], v[84:87]\n\t"
"s_mov_b32 s20, 1093632\n\t"
"ds_read_b128 a[188:191], v244 offset:11776\n\t"
"v_mfma_f32_16x16x32_bf16 v[84:87], a[92:95], a[192:195], v[84:87]\n\t"
"v_mfma_f32_16x16x32_bf16 v[84:87], a[96:99], a[196:199], v[84:87]\n\t"
"buffer_store_dwordx4 a[4:7], v248, s[16:19], s29 offen offset:0 nt\n\t"
"v_mfma_f32_16x16x32_bf16 v[88:91], a[88:91], a[192:195], v[88:91]\n\t"
"ds_read_b128 a[192:195], v244 offset:12288\n\t"
"v_mfma_f32_16x16x32_bf16 v[88:91], a[92:95], a[196:199], v[88:91]\n\t"
"v_mfma_f32_16x16x32_bf16 v[88:91], a[96:99], a[200:203], v[88:91]\n\t"
"v_mfma_f32_16x16x32_bf16 v[92:95], a[88:91], a[196:199], v[92:95]\n\t"
"buffer_load_dwordx4 a[76:79], v252, s[8:11], s20 offen offset:0 sc1\n\t"
"v_mfma_f32_16x16x32_bf16 v[92:95], a[92:95], a[200:203], v[92:95]\n\t"
"s_waitcnt vmcnt(53) lgkmcnt(7)\n\t"
"ds_read_b128 a[196:199], v244 offset:12800\n\t"
"v_mfma_f32_16x16x32_bf16 v[92:95], a[96:99], a[204:207], v[92:95]\n\t"
"ds_read_b128 a[200:203], v244 offset:13312\n\t"
"v_mfma_f32_16x16x32_bf16 v[100:103], a[100:103], a[168:171], v[100:103]\n\t"
"s_waitcnt lgkmcnt(7)\n\t"
"ds_read_b128 a[204:207], v244 offset:13824\n\t"
"v_mfma_f32_16x16x32_bf16 v[100:103], a[108:111], a[172:175], v[100:103]\n\t"
"v_mfma_f32_16x16x32_bf16 v[100:103], a[228:231], a[176:179], v[100:103]\n\t"
"buffer_store_dwordx4 a[8:11], v248, s[16:19], s29 offen offset:128 nt\n\t"
"v_mfma_f32_16x16x32_bf16 v[104:107], a[100:103], a[172:175], v[104:107]\n\t"
"s_waitcnt lgkmcnt(6)\n\t"
"v_mfma_f32_16x16x32_bf16 v[104:107], a[108:111], a[176:179], v[104:107]\n\t"
"v_mfma_f32_16x16x32_bf16 v[104:107], a[228:231], a[180:183], v[104:107]\n\t"
"s_waitcnt lgkmcnt(0)\n\t"
"s_barrier\n\t"
"v_mfma_f32_16x16x32_bf16 v[108:111], a[100:103], a[176:179], v[108:111]\n\t"
"v_mfma_f32_16x16x32_bf16 v[108:111], a[108:111], a[180:183], v[108:111]\n\t"
"buffer_load_dwordx4 a[4:7], v252, s[8:11], s20 offen offset:1024 sc1\n\t"
"v_mfma_f32_16x16x32_bf16 v[108:111], a[228:231], a[184:187], v[108:111]\n\t"
"buffer_load_dwordx4 a[80:83], v252, s[8:11], s20 offen offset:2048 sc1\n\t"
"v_mfma_f32_16x16x32_bf16 v[112:115], a[100:103], a[180:183], v[112:115]\n\t"
"s_mov_b32 s20, 1683456\n\t"
"v_mfma_f32_16x16x32_bf16 v[112:115], a[108:111], a[184:187], v[112:115]\n\t"
"v_mfma_f32_16x16x32_bf16 v[112:115], a[228:231], a[188:191], v[112:115]\n\t"
"buffer_store_dwordx4 a[12:15], v248, s[16:19], s29 offen offset:256 nt\n\t"
"v_mfma_f32_16x16x32_bf16 v[116:119], a[100:103], a[184:187], v[116:119]\n\t"
"s_add_u32 s29, s22, 1180032\n\t"
"v_mfma_f32_16x16x32_bf16 v[116:119], a[108:111], a[188:191], v[116:119]\n\t"
"v_mfma_f32_16x16x32_bf16 v[116:119], a[228:231], a[192:195], v[116:119]\n\t"
"buffer_load_dwordx4 a[8:11], v252, s[8:11], s20 offen offset:0 sc1\n\t"
"v_mfma_f32_16x16x32_bf16 v[120:123], a[100:103], a[188:191], v[120:123]\n\t"
"v_mfma_f32_16x16x32_bf16 v[120:123], a[108:111], a[192:195], v[120:123]\n\t"
"v_mfma_f32_16x16x32_bf16 v[120:123], a[228:231], a[196:199], v[120:123]\n\t"
"v_mfma_f32_16x16x32_bf16 v[124:127], a[100:103], a[192:195], v[124:127]\n\t"
"v_mfma_f32_16x16x32_bf16 v[124:127], a[108:111], a[196:199], v[124:127]\n\t"
"v_mfma_f32_16x16x32_bf16 v[124:127], a[228:231], a[200:203], v[124:127]\n\t"
"buffer_store_dwordx4 a[16:19], v248, s[16:19], s29 offen offset:0 nt\n\t"
"v_mfma_f32_16x16x32_bf16 v[128:131], a[100:103], a[196:199], v[128:131]\n\t"
"s_waitcnt vmcnt(56)\n\t"
"v_mfma_f32_16x16x32_bf16 v[128:131], a[108:111], a[200:203], v[128:131]\n\t"
"buffer_load_dwordx4 a[12:15], v252, s[8:11], s20 offen offset:1024 sc1\n\t"
"v_mfma_f32_16x16x32_bf16 v[128:131], a[228:231], a[204:207], v[128:131]\n\t"
"v_mfma_f32_16x16x32_bf16 v[132:135], a[208:211], a[168:171], v[132:135]\n\t"
"v_mfma_f32_16x16x32_bf16 v[132:135], a[232:235], a[172:175], v[132:135]\n\t"
"buffer_load_dwordx4 a[228:231], v252, s[8:11], s20 offen offset:2048 sc1\n\t"
"v_mfma_f32_16x16x32_bf16 v[132:135], a[116:119], a[176:179], v[132:135]\n\t"
"v_mfma_f32_16x16x32_bf16 v[136:139], a[208:211], a[172:175], v[136:139]\n\t"
"v_mfma_f32_16x16x32_bf16 v[136:139], a[232:235], a[176:179], v[136:139]\n\t"
"v_subrev_u32 v255, 55296, v255\n\t"
"v_mfma_f32_16x16x32_bf16 v[136:139], a[116:119], a[180:183], v[136:139]\n\t"
"buffer_store_dwordx4 a[20:23], v248, s[16:19], s29 offen offset:128 nt\n\t"
"v_mfma_f32_16x16x32_bf16 v[140:143], a[208:211], a[176:179], v[140:143]\n\t"
"v_mfma_f32_16x16x32_bf16 v[140:143], a[232:235], a[180:183], v[140:143]\n\t"
"v_mfma_f32_16x16x32_bf16 v[140:143], a[116:119], a[184:187], v[140:143]\n\t"
"v_mfma_f32_16x16x32_bf16 v[144:147], a[208:211], a[180:183], v[144:147]\n\t"
"v_mfma_f32_16x16x32_bf16 v[144:147], a[232:235], a[184:187], v[144:147]\n\t"
"v_mfma_f32_16x16x32_bf16 v[144:147], a[116:119], a[188:191], v[144:147]\n\t"
"v_mfma_f32_16x16x32_bf16 v[148:151], a[208:211], a[184:187], v[148:151]\n\t"
"v_mfma_f32_16x16x32_bf16 v[148:151], a[232:235], a[188:191], v[148:151]\n\t"
"v_mfma_f32_16x16x32_bf16 v[148:151], a[116:119], a[192:195], v[148:151]\n\t"
"buffer_store_dwordx4 a[24:27], v248, s[16:19], s29 offen offset:256 nt\n\t"
"v_mfma_f32_16x16x32_bf16 v[152:155], a[208:211], a[188:191], v[152:155]\n\t"
"s_add_u32 s29, s22, 1376640\n\t"
"v_mfma_f32_16x16x32_bf16 v[152:155], a[232:235], a[192:195], v[152:155]\n\t"
"v_mfma_f32_16x16x32_bf16 v[152:155], a[116:119], a[196:199], v[152:155]\n\t"
"v_mfma_f32_16x16x32_bf16 v[156:159], a[208:211], a[192:195], v[156:159]\n\t"
"v_mfma_f32_16x16x32_bf16 v[156:159], a[232:235], a[196:199], v[156:159]\n\t"
"v_mfma_f32_16x16x32_bf16 v[156:159], a[116:119], a[200:203], v[156:159]\n\t"
"v_mfma_f32_16x16x32_bf16 v[160:163], a[208:211], a[196:199], v[160:163]\n\t"
"s_waitcnt vmcnt(57)\n\t"
"v_mfma_f32_16x16x32_bf16 v[160:163], a[232:235], a[200:203], v[160:163]\n\t"
"v_mfma_f32_16x16x32_bf16 v[160:163], a[116:119], a[204:207], v[160:163]\n\t"
"buffer_store_dwordx4 v[196:199], v248, s[16:19], s29 offen offset:0 nt\n\t"
"v_mfma_f32_16x16x32_bf16 v[164:167], a[124:127], a[168:171], v[164:167]\n\t"
"ds_read_b128 a[168:171], v255 offset:512\n\t"
"v_mfma_f32_16x16x32_bf16 v[164:167], a[132:135], a[172:175], v[164:167]\n\t"
"ds_read_b128 v[204:207], v255 offset:3584\n\t"
"v_mfma_f32_16x16x32_bf16 v[164:167], a[140:143], a[176:179], v[164:167]\n\t"
"ds_read_b128 v[212:215], v255 offset:6656\n\t"
"v_mfma_f32_16x16x32_bf16 v[168:171], a[124:127], a[172:175], v[168:171]\n\t"
"ds_read_b128 a[172:175], v255 offset:1024\n\t"
"v_mfma_f32_16x16x32_bf16 v[168:171], a[132:135], a[176:179], v[168:171]\n\t"
"ds_read_b128 v[216:219], v255 offset:4096\n\t"
"v_mfma_f32_16x16x32_bf16 v[168:171], a[140:143], a[180:183], v[168:171]\n\t"
"ds_read_b128 a[0:3], v255 offset:7168\n\t"
"v_mfma_f32_16x16x32_bf16 v[172:175], a[124:127], a[176:179], v[172:175]\n\t"
"v_mfma_f32_16x16x32_bf16 v[172:175], a[132:135], a[180:183], v[172:175]\n\t"
"v_mfma_f32_16x16x32_bf16 v[172:175], a[140:143], a[184:187], v[172:175]\n\t"
"buffer_store_dwordx4 v[200:203], v248, s[16:19], s29 offen offset:128 nt\n\t"
"v_mfma_f32_16x16x32_bf16 v[176:179], a[124:127], a[180:183], v[176:179]\n\t"
"v_mfma_f32_16x16x32_bf16 v[176:179], a[132:135], a[184:187], v[176:179]\n\t"
"v_mfma_f32_16x16x32_bf16 v[176:179], a[140:143], a[188:191], v[176:179]\n\t"
"s_barrier\n\t"
"v_mfma_f32_16x16x32_bf16 v[180:183], a[124:127], a[184:187], v[180:183]\n\t"
"v_mfma_f32_16x16x32_bf16 v[180:183], a[132:135], a[188:191], v[180:183]\n\t"
"v_mfma_f32_16x16x32_bf16 v[180:183], a[140:143], a[192:195], v[180:183]\n\t"
"v_mfma_f32_16x16x32_bf16 v[184:187], a[124:127], a[188:191], v[184:187]\n\t"
"v_mfma_f32_16x16x32_bf16 v[184:187], a[132:135], a[192:195], v[184:187]\n\t"
"v_mfma_f32_16x16x32_bf16 v[184:187], a[140:143], a[196:199], v[184:187]\n\t"
"buffer_store_dwordx4 v[208:211], v248, s[16:19], s29 offen offset:256 nt\n\t"
"v_mfma_f32_16x16x32_bf16 v[188:191], a[124:127], a[192:195], v[188:191]\n\t"
"ds_read_b128 a[176:179], v255 offset:2048\n\t"
"v_mfma_f32_16x16x32_bf16 v[188:191], a[132:135], a[196:199], v[188:191]\n\t"
"ds_read_b128 a[180:183], v255 offset:5120\n\t"
"v_mfma_f32_16x16x32_bf16 v[188:191], a[140:143], a[200:203], v[188:191]\n\t"
"ds_read_b128 a[184:187], v255 offset:8192\n\t"
"v_mfma_f32_16x16x32_bf16 v[192:195], a[124:127], a[196:199], v[192:195]\n\t"
"s_waitcnt vmcnt(57) lgkmcnt(6)\n\t"
"ds_read_b128 a[188:191], v255 offset:2560\n\t"
"v_mfma_f32_16x16x32_bf16 v[192:195], a[132:135], a[200:203], v[192:195]\n\t"
"ds_read_b128 a[192:195], v255 offset:5632\n\t"
"v_mfma_f32_16x16x32_bf16 v[192:195], a[140:143], a[204:207], v[192:195]\n\t"
"ds_read_b128 a[196:199], v255 offset:8704\n\t"
"Lst34_%=:\n\t"
"v_mfma_f32_16x16x32_bf16 v[0:3], a[148:151], a[168:171], v[0:3]\n\t"
"s_waitcnt vmcnt(54) lgkmcnt(6)\n\t"
"v_mfma_f32_16x16x32_bf16 v[0:3], a[236:239], v[204:207], v[0:3]\n\t"
"v_mfma_f32_16x16x32_bf16 v[0:3], a[240:243], v[212:215], v[0:3]\n\t"
"v_mfma_f32_16x16x32_bf16 v[0:3], a[244:247], a[172:175], v[0:3]\n\t"
"s_waitcnt vmcnt(51)\n\t"
"v_mfma_f32_16x16x32_bf16 v[0:3], a[104:107], v[216:219], v[0:3]\n\t"
"v_mfma_f32_16x16x32_bf16 v[0:3], a[156:159], a[0:3], v[0:3]\n\t"
"v_mfma_f32_16x16x32_bf16 v[32:35], a[164:167], a[168:171], v[32:35]\n\t"
"s_waitcnt vmcnt(47)\n\t"
"v_mfma_f32_16x16x32_bf16 v[32:35], v[240:243], v[204:207], v[32:35]\n\t"
"v_mfma_f32_16x16x32_bf16 v[32:35], v[96:99], v[212:215], v[32:35]\n\t"
"v_mfma_f32_16x16x32_bf16 v[32:35], a[248:251], a[172:175], v[32:35]\n\t"
"s_waitcnt vmcnt(42)\n\t"
"v_cvt_pk_bf16_f32 v0, v0, v1\n\t"
"v_cvt_pk_bf16_f32 v1, v2, v3\n\t"
"v_mfma_f32_16x16x32_bf16 v[32:35], a[52:55], v[216:219], v[32:35]\n\t"
"v_mfma_f32_16x16x32_bf16 v[32:35], a[56:59], a[0:3], v[32:35]\n\t"
"v_mfma_f32_16x16x32_bf16 v[64:67], a[48:51], a[168:171], v[64:67]\n\t"
"s_waitcnt vmcnt(39)\n\t"
"ds_read_b128 a[168:171], v255 offset:9728\n\t"
"v_mfma_f32_16x16x32_bf16 v[64:67], v[236:239], v[204:207], v[64:67]\n\t"
"v_mfma_f32_16x16x32_bf16 v[64:67], a[60:63], v[212:215], v[64:67]\n\t"
"s_barrier\n\t"
"v_mfma_f32_16x16x32_bf16 v[64:67], a[212:215], a[172:175], v[64:67]\n\t"
"s_waitcnt vmcnt(36) lgkmcnt(4)\n\t"
"ds_read_b128 a[172:175], v255 offset:10240\n\t"
"v_mfma_f32_16x16x32_bf16 v[64:67], a[252:255], v[216:219], v[64:67]\n\t"
"v_cvt_pk_bf16_f32 v2, v32, v33\n\t"
"v_cvt_pk_bf16_f32 v3, v34, v35\n\t"
"v_mfma_f32_16x16x32_bf16 v[64:67], a[28:31], a[0:3], v[64:67]\n\t"
"ds_write_b128 v249, v[0:3] offset:18432\n\t"
"v_mfma_f32_16x16x32_bf16 v[100:103], a[112:115], a[176:179], v[100:103]\n\t"
"s_waitcnt vmcnt(27) lgkmcnt(3)\n\t"
"v_mfma_f32_16x16x32_bf16 v[100:103], a[120:123], a[180:183], v[100:103]\n\t"
"v_mfma_f32_16x16x32_bf16 v[100:103], a[64:67], a[184:187], v[100:103]\n\t"
"v_mfma_f32_16x16x32_bf16 v[100:103], a[68:71], a[188:191], v[100:103]\n\t"
"s_waitcnt vmcnt(20)\n\t"
"v_cvt_pk_bf16_f32 v64, v64, v65\n\t"
"v_cvt_pk_bf16_f32 v65, v66, v67\n\t"
"v_mfma_f32_16x16x32_bf16 v[100:103], a[72:75], a[192:195], v[100:103]\n\t"
"v_mfma_f32_16x16x32_bf16 v[100:103], a[40:43], a[196:199], v[100:103]\n\t"
"ds_write_b64 v250, v[64:65] offset:18432\n\t"
"v_mfma_f32_16x16x32_bf16 v[132:135], a[44:47], a[176:179], v[132:135]\n\t"
"s_waitcnt vmcnt(15)\n\t"
"v_mfma_f32_16x16x32_bf16 v[132:135], a[32:35], a[180:183], v[132:135]\n\t"
"v_mfma_f32_16x16x32_bf16 v[132:135], a[36:39], a[184:187], v[132:135]\n\t"
"v_mfma_f32_16x16x32_bf16 v[132:135], a[216:219], a[188:191], v[132:135]\n\t"
"s_waitcnt vmcnt(10)\n\t"
"v_cvt_pk_bf16_f32 v100, v100, v101\n\t"
"v_cvt_pk_bf16_f32 v101, v102, v103\n\t"
"v_mfma_f32_16x16x32_bf16 v[132:135], a[220:223], a[192:195], v[132:135]\n\t"
"v_mfma_f32_16x16x32_bf16 v[132:135], a[224:227], a[196:199], v[132:135]\n\t"
"v_mfma_f32_16x16x32_bf16 v[164:167], a[76:79], a[176:179], v[164:167]\n\t"
"s_waitcnt vmcnt(5)\n\t"
"ds_read_b128 a[176:179], v255 offset:11264\n\t"
"v_mfma_f32_16x16x32_bf16 v[164:167], a[4:7], a[180:183], v[164:167]\n\t"
"v_mfma_f32_16x16x32_bf16 v[164:167], a[80:83], a[184:187], v[164:167]\n\t"
"v_mfma_f32_16x16x32_bf16 v[164:167], a[8:11], a[188:191], v[164:167]\n\t"
"s_waitcnt lgkmcnt(4)\n\t"
"ds_read_b128 a[188:191], v255 offset:11776\n\t"
"v_cvt_pk_bf16_f32 v102, v132, v133\n\t"
"v_mfma_f32_16x16x32_bf16 v[164:167], a[12:15], a[192:195], v[164:167]\n\t"
"v_cvt_pk_bf16_f32 v103, v134, v135\n\t"
"v_mfma_f32_16x16x32_bf16 v[164:167], a[228:231], a[196:199], v[164:167]\n\t"
"ds_write_b128 v249, v[100:103] offset:24576\n\t"
"v_mfma_f32_16x16x32_bf16 v[4:7], a[148:151], v[204:207], v[4:7]\n\t"
"s_waitcnt lgkmcnt(5)\n\t"
"v_mfma_f32_16x16x32_bf16 v[4:7], a[236:239], v[212:215], v[4:7]\n\t"
"v_mfma_f32_16x16x32_bf16 v[4:7], a[240:243], a[168:171], v[4:7]\n\t"
"v_mfma_f32_16x16x32_bf16 v[4:7], a[244:247], v[216:219], v[4:7]\n\t"
"v_cvt_pk_bf16_f32 v164, v164, v165\n\t"
"v_cvt_pk_bf16_f32 v165, v166, v167\n\t"
"v_mfma_f32_16x16x32_bf16 v[4:7], a[104:107], a[0:3], v[4:7]\n\t"
"v_mfma_f32_16x16x32_bf16 v[4:7], a[156:159], a[172:175], v[4:7]\n\t"
"ds_write_b64 v250, v[164:165] offset:24576\n\t"
"v_mfma_f32_16x16x32_bf16 v[36:39], a[164:167], v[204:207], v[36:39]\n\t"
"v_mfma_f32_16x16x32_bf16 v[36:39], v[240:243], v[212:215], v[36:39]\n\t"
"v_mfma_f32_16x16x32_bf16 v[36:39], v[96:99], a[168:171], v[36:39]\n\t"
"v_mfma_f32_16x16x32_bf16 v[36:39], a[248:251], v[216:219], v[36:39]\n\t"
"v_cvt_pk_bf16_f32 v4, v4, v5\n\t"
"v_cvt_pk_bf16_f32 v5, v6, v7\n\t"
"v_mfma_f32_16x16x32_bf16 v[36:39], a[52:55], a[0:3], v[36:39]\n\t"
"v_mfma_f32_16x16x32_bf16 v[36:39], a[56:59], a[172:175], v[36:39]\n\t"
"v_mfma_f32_16x16x32_bf16 v[68:71], a[48:51], v[204:207], v[68:71]\n\t"
"ds_read_b128 a[200:203], v255 offset:12800\n\t"
"v_mfma_f32_16x16x32_bf16 v[68:71], v[236:239], v[212:215], v[68:71]\n\t"
"v_mfma_f32_16x16x32_bf16 v[68:71], a[60:63], a[168:171], v[68:71]\n\t"
"s_waitcnt lgkmcnt(1)\n\t"
"s_barrier\n\t"
"v_mfma_f32_16x16x32_bf16 v[68:71], a[212:215], v[216:219], v[68:71]\n\t"
"ds_read_b128 v[196:199], v252 offset:18432\n\t"
"v_mfma_f32_16x16x32_bf16 v[68:71], a[252:255], a[0:3], v[68:71]\n\t"
"ds_read_b128 v[200:203], v252 offset:19456\n\t"
"v_cvt_pk_bf16_f32 v6, v36, v37\n\t"
"v_mfma_f32_16x16x32_bf16 v[68:71], a[28:31], a[172:175], v[68:71]\n\t"
"ds_read_b128 v[204:207], v252 offset:20480\n\t"
"v_cvt_pk_bf16_f32 v7, v38, v39\n\t"
"v_mfma_f32_16x16x32_bf16 v[104:107], a[112:115], a[180:183], v[104:107]\n\t"
"s_add_u32 s29, s23, 0\n\t"
"ds_read_b128 a[204:207], v255 offset:13312\n\t"
"v_mfma_f32_16x16x32_bf16 v[104:107], a[120:123], a[184:187], v[104:107]\n\t"
"v_mfma_f32_16x16x32_bf16 v[104:107], a[64:67], a[176:179], v[104:107]\n\t"
"ds_write_b128 v249, v[4:7] offset:30720\n\t"
"v_mfma_f32_16x16x32_bf16 v[104:107], a[68:71], a[192:195], v[104:107]\n\t"
"s_waitcnt lgkmcnt(4)\n\t"
"v_cvt_pk_bf16_f32 v68, v68, v69\n\t"
"v_cvt_pk_bf16_f32 v69, v70, v71\n\t"
"v_mfma_f32_16x16x32_bf16 v[104:107], a[72:75], a[196:199], v[104:107]\n\t"
"v_mfma_f32_16x16x32_bf16 v[104:107], a[40:43], a[188:191], v[104:107]\n\t"
"ds_write_b64 v250, v[68:69] offset:30720\n\t"
"v_mfma_f32_16x16x32_bf16 v[136:139], a[44:47], a[180:183], v[136:139]\n\t"
"s_waitcnt lgkmcnt(4)\n\t"
"v_mfma_f32_16x16x32_bf16 v[136:139], a[32:35], a[184:187], v[136:139]\n\t"
"v_mfma_f32_16x16x32_bf16 v[136:139], a[36:39], a[176:179], v[136:139]\n\t"
"buffer_store_dwordx4 v[196:199], v248, s[16:19], s29 offen offset:0 sc1\n\t"
"v_mfma_f32_16x16x32_bf16 v[136:139], a[216:219], a[192:195], v[136:139]\n\t"
"s_waitcnt lgkmcnt(3)\n\t"
"v_cvt_pk_bf16_f32 v104, v104, v105\n\t"
"v_cvt_pk_bf16_f32 v105, v106, v107\n\t"
"v_mfma_f32_16x16x32_bf16 v[136:139], a[220:223], a[196:199], v[136:139]\n\t"
"v_mfma_f32_16x16x32_bf16 v[136:139], a[224:227], a[188:191], v[136:139]\n\t"
"buffer_store_dwordx4 v[200:203], v248, s[16:19], s29 offen offset:128 sc1\n\t"
"v_mfma_f32_16x16x32_bf16 v[168:171], a[76:79], a[180:183], v[168:171]\n\t"
"ds_read_b128 a[180:183], v255 offset:14336\n\t"
"v_mfma_f32_16x16x32_bf16 v[168:171], a[4:7], a[184:187], v[168:171]\n\t"
"v_mfma_f32_16x16x32_bf16 v[168:171], a[80:83], a[176:179], v[168:171]\n\t"
"buffer_store_dwordx4 v[204:207], v248, s[16:19], s29 offen offset:256 sc1\n\t"
"v_mfma_f32_16x16x32_bf16 v[168:171], a[8:11], a[192:195], v[168:171]\n\t"
"ds_read_b128 a[192:195], v255 offset:14848\n\t"
"v_cvt_pk_bf16_f32 v106, v136, v137\n\t"
"v_mfma_f32_16x16x32_bf16 v[168:171], a[12:15], a[196:199], v[168:171]\n\t"
"v_cvt_pk_bf16_f32 v107, v138, v139\n\t"
"v_mfma_f32_16x16x32_bf16 v[168:171], a[228:231], a[188:191], v[168:171]\n\t"
"ds_write_b128 v249, v[104:107] offset:36864\n\t"
"v_mfma_f32_16x16x32_bf16 v[8:11], a[148:151], v[212:215], v[8:11]\n\t"
"s_waitcnt lgkmcnt(5)\n\t"
"v_mfma_f32_16x16x32_bf16 v[8:11], a[236:239], a[168:171], v[8:11]\n\t"
"v_mfma_f32_16x16x32_bf16 v[8:11], a[240:243], a[200:203], v[8:11]\n\t"
"v_mfma_f32_16x16x32_bf16 v[8:11], a[244:247], a[0:3], v[8:11]\n\t"
"v_cvt_pk_bf16_f32 v168, v168, v169\n\t"
"v_cvt_pk_bf16_f32 v169, v170, v171\n\t"
"v_mfma_f32_16x16x32_bf16 v[8:11], a[104:107], a[172:175], v[8:11]\n\t"
"v_mfma_f32_16x16x32_bf16 v[8:11], a[156:159], a[204:207], v[8:11]\n\t"
"ds_write_b64 v250, v[168:169] offset:36864\n\t"
"v_mfma_f32_16x16x32_bf16 v[40:43], a[164:167], v[212:215], v[40:43]\n\t"
"v_mfma_f32_16x16x32_bf16 v[40:43], v[240:243], a[168:171], v[40:43]\n\t"
"v_mfma_f32_16x16x32_bf16 v[40:43], v[96:99], a[200:203], v[40:43]\n\t"
"v_mfma_f32_16x16x32_bf16 v[40:43], a[248:251], a[0:3], v[40:43]\n\t"
"v_cvt_pk_bf16_f32 v8, v8, v9\n\t"
"v_cvt_pk_bf16_f32 v9, v10, v11\n\t"
"v_mfma_f32_16x16x32_bf16 v[40:43], a[52:55], a[172:175], v[40:43]\n\t"
"v_mfma_f32_16x16x32_bf16 v[40:43], a[56:59], a[204:207], v[40:43]\n\t"
"v_mfma_f32_16x16x32_bf16 v[72:75], a[48:51], v[212:215], v[72:75]\n\t"
"ds_read_b128 v[208:211], v255 offset:15872\n\t"
"v_mfma_f32_16x16x32_bf16 v[72:75], v[236:239], a[168:171], v[72:75]\n\t"
"v_mfma_f32_16x16x32_bf16 v[72:75], a[60:63], a[200:203], v[72:75]\n\t"
"s_waitcnt lgkmcnt(1)\n\t"
"s_barrier\n\t"
"v_mfma_f32_16x16x32_bf16 v[72:75], a[212:215], a[0:3], v[72:75]\n\t"
"ds_read_b128 v[196:199], v252 offset:30720\n\t"
"v_mfma_f32_16x16x32_bf16 v[72:75], a[252:255], a[172:175], v[72:75]\n\t"
"ds_read_b128 v[200:203], v252 offset:31744\n\t"
"v_cvt_pk_bf16_f32 v10, v40, v41\n\t"
"v_mfma_f32_16x16x32_bf16 v[72:75], a[28:31], a[204:207], v[72:75]\n\t"
"ds_read_b128 v[204:207], v252 offset:32768\n\t"
"v_cvt_pk_bf16_f32 v11, v42, v43\n\t"
"v_mfma_f32_16x16x32_bf16 v[108:111], a[112:115], a[184:187], v[108:111]\n\t"
"s_add_u32 s29, s23, 196608\n\t"
"ds_read_b128 v[212:215], v255 offset:16384\n\t"
"v_mfma_f32_16x16x32_bf16 v[108:111], a[120:123], a[176:179], v[108:111]\n\t"
"v_mfma_f32_16x16x32_bf16 v[108:111], a[64:67], a[180:183], v[108:111]\n\t"
"ds_write_b128 v249, v[8:11] offset:18432\n\t"
"v_mfma_f32_16x16x32_bf16 v[108:111], a[68:71], a[196:199], v[108:111]\n\t"
"s_waitcnt lgkmcnt(4)\n\t"
"v_cvt_pk_bf16_f32 v72, v72, v73\n\t"
"v_cvt_pk_bf16_f32 v73, v74, v75\n\t"
"v_mfma_f32_16x16x32_bf16 v[108:111], a[72:75], a[188:191], v[108:111]\n\t"
"v_mfma_f32_16x16x32_bf16 v[108:111], a[40:43], a[192:195], v[108:111]\n\t"
"ds_write_b64 v250, v[72:73] offset:18432\n\t"
"v_mfma_f32_16x16x32_bf16 v[140:143], a[44:47], a[184:187], v[140:143]\n\t"
"s_waitcnt lgkmcnt(4)\n\t"
"v_mfma_f32_16x16x32_bf16 v[140:143], a[32:35], a[176:179], v[140:143]\n\t"
"v_mfma_f32_16x16x32_bf16 v[140:143], a[36:39], a[180:183], v[140:143]\n\t"
"buffer_store_dwordx4 v[196:199], v248, s[16:19], s29 offen offset:0 sc1\n\t"
"v_mfma_f32_16x16x32_bf16 v[140:143], a[216:219], a[196:199], v[140:143]\n\t"
"s_waitcnt lgkmcnt(3)\n\t"
"v_cvt_pk_bf16_f32 v108, v108, v109\n\t"
"v_cvt_pk_bf16_f32 v109, v110, v111\n\t"
"v_mfma_f32_16x16x32_bf16 v[140:143], a[220:223], a[188:191], v[140:143]\n\t"
"v_mfma_f32_16x16x32_bf16 v[140:143], a[224:227], a[192:195], v[140:143]\n\t"
"buffer_store_dwordx4 v[200:203], v248, s[16:19], s29 offen offset:128 sc1\n\t"
"v_mfma_f32_16x16x32_bf16 v[172:175], a[76:79], a[184:187], v[172:175]\n\t"
"ds_read_b128 a[184:187], v255 offset:17408\n\t"
"v_mfma_f32_16x16x32_bf16 v[172:175], a[4:7], a[176:179], v[172:175]\n\t"
"v_mfma_f32_16x16x32_bf16 v[172:175], a[80:83], a[180:183], v[172:175]\n\t"
"buffer_store_dwordx4 v[204:207], v248, s[16:19], s29 offen offset:256 sc1\n\t"
"v_mfma_f32_16x16x32_bf16 v[172:175], a[8:11], a[196:199], v[172:175]\n\t"
"ds_read_b128 a[196:199], v255 offset:17920\n\t"
"v_cvt_pk_bf16_f32 v110, v140, v141\n\t"
"v_mfma_f32_16x16x32_bf16 v[172:175], a[12:15], a[188:191], v[172:175]\n\t"
"v_cvt_pk_bf16_f32 v111, v142, v143\n\t"
"v_mfma_f32_16x16x32_bf16 v[172:175], a[228:231], a[192:195], v[172:175]\n\t"
"ds_write_b128 v249, v[108:111] offset:24576\n\t"
"v_mfma_f32_16x16x32_bf16 v[12:15], a[148:151], a[168:171], v[12:15]\n\t"
"s_waitcnt lgkmcnt(5)\n\t"
"v_mfma_f32_16x16x32_bf16 v[12:15], a[236:239], a[200:203], v[12:15]\n\t"
"v_mfma_f32_16x16x32_bf16 v[12:15], a[240:243], v[208:211], v[12:15]\n\t"
"v_mfma_f32_16x16x32_bf16 v[12:15], a[244:247], a[172:175], v[12:15]\n\t"
"v_cvt_pk_bf16_f32 v172, v172, v173\n\t"
"v_cvt_pk_bf16_f32 v173, v174, v175\n\t"
"v_mfma_f32_16x16x32_bf16 v[12:15], a[104:107], a[204:207], v[12:15]\n\t"
"v_mfma_f32_16x16x32_bf16 v[12:15], a[156:159], v[212:215], v[12:15]\n\t"
"ds_write_b64 v250, v[172:173] offset:24576\n\t"
"v_mfma_f32_16x16x32_bf16 v[44:47], a[164:167], a[168:171], v[44:47]\n\t"
"v_mfma_f32_16x16x32_bf16 v[44:47], v[240:243], a[200:203], v[44:47]\n\t"
"v_mfma_f32_16x16x32_bf16 v[44:47], v[96:99], v[208:211], v[44:47]\n\t"
"v_mfma_f32_16x16x32_bf16 v[44:47], a[248:251], a[172:175], v[44:47]\n\t"
"v_cvt_pk_bf16_f32 v12, v12, v13\n\t"
"v_cvt_pk_bf16_f32 v13, v14, v15\n\t"
"v_mfma_f32_16x16x32_bf16 v[44:47], a[52:55], a[204:207], v[44:47]\n\t"
"v_mfma_f32_16x16x32_bf16 v[44:47], a[56:59], v[212:215], v[44:47]\n\t"
"v_mfma_f32_16x16x32_bf16 v[76:79], a[48:51], a[168:171], v[76:79]\n\t"
"ds_read_b128 a[168:171], v255 offset:18944\n\t"
"v_mfma_f32_16x16x32_bf16 v[76:79], v[236:239], a[200:203], v[76:79]\n\t"
"v_mfma_f32_16x16x32_bf16 v[76:79], a[60:63], v[208:211], v[76:79]\n\t"
"s_waitcnt lgkmcnt(1)\n\t"
"s_barrier\n\t"
"v_mfma_f32_16x16x32_bf16 v[76:79], a[212:215], a[172:175], v[76:79]\n\t"
"ds_read_b128 v[196:199], v252 offset:18432\n\t"
"v_mfma_f32_16x16x32_bf16 v[76:79], a[252:255], a[204:207], v[76:79]\n\t"
"ds_read_b128 v[200:203], v252 offset:19456\n\t"
"v_cvt_pk_bf16_f32 v14, v44, v45\n\t"
"v_mfma_f32_16x16x32_bf16 v[76:79], a[28:31], v[212:215], v[76:79]\n\t"
"ds_read_b128 v[204:207], v252 offset:20480\n\t"
"v_cvt_pk_bf16_f32 v15, v46, v47\n\t"
"v_mfma_f32_16x16x32_bf16 v[112:115], a[112:115], a[176:179], v[112:115]\n\t"
"s_add_u32 s29, s23, 393216\n\t"
"ds_read_b128 a[172:175], v255 offset:19456\n\t"
"v_mfma_f32_16x16x32_bf16 v[112:115], a[120:123], a[180:183], v[112:115]\n\t"
"v_mfma_f32_16x16x32_bf16 v[112:115], a[64:67], a[184:187], v[112:115]\n\t"
"ds_write_b128 v249, v[12:15] offset:30720\n\t"
"v_mfma_f32_16x16x32_bf16 v[112:115], a[68:71], a[188:191], v[112:115]\n\t"
"s_waitcnt lgkmcnt(4)\n\t"
"v_cvt_pk_bf16_f32 v76, v76, v77\n\t"
"v_cvt_pk_bf16_f32 v77, v78, v79\n\t"
"v_mfma_f32_16x16x32_bf16 v[112:115], a[72:75], a[192:195], v[112:115]\n\t"
"v_mfma_f32_16x16x32_bf16 v[112:115], a[40:43], a[196:199], v[112:115]\n\t"
"ds_write_b64 v250, v[76:77] offset:30720\n\t"
"v_mfma_f32_16x16x32_bf16 v[144:147], a[44:47], a[176:179], v[144:147]\n\t"
"s_waitcnt lgkmcnt(4)\n\t"
"v_mfma_f32_16x16x32_bf16 v[144:147], a[32:35], a[180:183], v[144:147]\n\t"
"v_mfma_f32_16x16x32_bf16 v[144:147], a[36:39], a[184:187], v[144:147]\n\t"
"buffer_store_dwordx4 v[196:199], v248, s[16:19], s29 offen offset:0 sc1\n\t"
"v_mfma_f32_16x16x32_bf16 v[144:147], a[216:219], a[188:191], v[144:147]\n\t"
"s_waitcnt lgkmcnt(3)\n\t"
"v_cvt_pk_bf16_f32 v112, v112, v113\n\t"
"v_cvt_pk_bf16_f32 v113, v114, v115\n\t"
"v_mfma_f32_16x16x32_bf16 v[144:147], a[220:223], a[192:195], v[144:147]\n\t"
"v_mfma_f32_16x16x32_bf16 v[144:147], a[224:227], a[196:199], v[144:147]\n\t"
"buffer_store_dwordx4 v[200:203], v248, s[16:19], s29 offen offset:128 sc1\n\t"
"v_mfma_f32_16x16x32_bf16 v[176:179], a[76:79], a[176:179], v[176:179]\n\t"
"ds_read_b128 a[176:179], v255 offset:20480\n\t"
"v_mfma_f32_16x16x32_bf16 v[176:179], a[4:7], a[180:183], v[176:179]\n\t"
"v_mfma_f32_16x16x32_bf16 v[176:179], a[80:83], a[184:187], v[176:179]\n\t"
"buffer_store_dwordx4 v[204:207], v248, s[16:19], s29 offen offset:256 sc1\n\t"
"v_mfma_f32_16x16x32_bf16 v[176:179], a[8:11], a[188:191], v[176:179]\n\t"
"ds_read_b128 a[188:191], v255 offset:20992\n\t"
"v_cvt_pk_bf16_f32 v114, v144, v145\n\t"
"v_mfma_f32_16x16x32_bf16 v[176:179], a[12:15], a[192:195], v[176:179]\n\t"
"v_cvt_pk_bf16_f32 v115, v146, v147\n\t"
"v_mfma_f32_16x16x32_bf16 v[176:179], a[228:231], a[196:199], v[176:179]\n\t"
"ds_write_b128 v249, v[112:115] offset:36864\n\t"
"Lst35_%=:\n\t"
"v_mfma_f32_16x16x32_bf16 v[16:19], a[148:151], a[200:203], v[16:19]\n\t"
"s_waitcnt lgkmcnt(5)\n\t"
"v_mfma_f32_16x16x32_bf16 v[16:19], a[236:239], v[208:211], v[16:19]\n\t"
"v_mfma_f32_16x16x32_bf16 v[16:19], a[240:243], a[168:171], v[16:19]\n\t"
"v_mfma_f32_16x16x32_bf16 v[16:19], a[244:247], a[204:207], v[16:19]\n\t"
"v_cvt_pk_bf16_f32 v176, v176, v177\n\t"
"v_cvt_pk_bf16_f32 v177, v178, v179\n\t"
"v_mfma_f32_16x16x32_bf16 v[16:19], a[104:107], v[212:215], v[16:19]\n\t"
"v_mfma_f32_16x16x32_bf16 v[16:19], a[156:159], a[172:175], v[16:19]\n\t"
"ds_write_b64 v250, v[176:177] offset:36864\n\t"
"v_mfma_f32_16x16x32_bf16 v[48:51], a[164:167], a[200:203], v[48:51]\n\t"
"v_mfma_f32_16x16x32_bf16 v[48:51], v[240:243], v[208:211], v[48:51]\n\t"
"v_mfma_f32_16x16x32_bf16 v[48:51], v[96:99], a[168:171], v[48:51]\n\t"
"v_mfma_f32_16x16x32_bf16 v[48:51], a[248:251], a[204:207], v[48:51]\n\t"
"v_cvt_pk_bf16_f32 v16, v16, v17\n\t"
"v_cvt_pk_bf16_f32 v17, v18, v19\n\t"
"v_mfma_f32_16x16x32_bf16 v[48:51], a[52:55], v[212:215], v[48:51]\n\t"
"v_mfma_f32_16x16x32_bf16 v[48:51], a[56:59], a[172:175], v[48:51]\n\t"
"v_mfma_f32_16x16x32_bf16 v[80:83], a[48:51], a[200:203], v[80:83]\n\t"
"ds_read_b128 a[200:203], v255 offset:22016\n\t"
"v_mfma_f32_16x16x32_bf16 v[80:83], v[236:239], v[208:211], v[80:83]\n\t"
"v_mfma_f32_16x16x32_bf16 v[80:83], a[60:63], a[168:171], v[80:83]\n\t"
"s_waitcnt lgkmcnt(1)\n\t"
"s_barrier\n\t"
"v_mfma_f32_16x16x32_bf16 v[80:83], a[212:215], a[204:207], v[80:83]\n\t"
"ds_read_b128 v[196:199], v252 offset:30720\n\t"
"v_mfma_f32_16x16x32_bf16 v[80:83], a[252:255], v[212:215], v[80:83]\n\t"
"ds_read_b128 v[200:203], v252 offset:31744\n\t"
"v_cvt_pk_bf16_f32 v18, v48, v49\n\t"
"v_mfma_f32_16x16x32_bf16 v[80:83], a[28:31], a[172:175], v[80:83]\n\t"
"ds_read_b128 v[204:207], v252 offset:32768\n\t"
"v_cvt_pk_bf16_f32 v19, v50, v51\n\t"
"v_mfma_f32_16x16x32_bf16 v[116:119], a[112:115], a[180:183], v[116:119]\n\t"
"s_add_u32 s29, s23, 589824\n\t"
"ds_read_b128 a[204:207], v255 offset:22528\n\t"
"v_mfma_f32_16x16x32_bf16 v[116:119], a[120:123], a[184:187], v[116:119]\n\t"
"v_mfma_f32_16x16x32_bf16 v[116:119], a[64:67], a[176:179], v[116:119]\n\t"
"ds_write_b128 v249, v[16:19] offset:18432\n\t"
"v_mfma_f32_16x16x32_bf16 v[116:119], a[68:71], a[192:195], v[116:119]\n\t"
"s_waitcnt lgkmcnt(4)\n\t"
"v_cvt_pk_bf16_f32 v80, v80, v81\n\t"
"v_cvt_pk_bf16_f32 v81, v82, v83\n\t"
"v_mfma_f32_16x16x32_bf16 v[116:119], a[72:75], a[196:199], v[116:119]\n\t"
"v_mfma_f32_16x16x32_bf16 v[116:119], a[40:43], a[188:191], v[116:119]\n\t"
"ds_write_b64 v250, v[80:81] offset:18432\n\t"
"v_mfma_f32_16x16x32_bf16 v[148:151], a[44:47], a[180:183], v[148:151]\n\t"
"s_waitcnt lgkmcnt(4)\n\t"
"v_mfma_f32_16x16x32_bf16 v[148:151], a[32:35], a[184:187], v[148:151]\n\t"
"v_mfma_f32_16x16x32_bf16 v[148:151], a[36:39], a[176:179], v[148:151]\n\t"
"buffer_store_dwordx4 v[196:199], v248, s[16:19], s29 offen offset:0 sc1\n\t"
"v_mfma_f32_16x16x32_bf16 v[148:151], a[216:219], a[192:195], v[148:151]\n\t"
"s_waitcnt lgkmcnt(3)\n\t"
"v_cvt_pk_bf16_f32 v116, v116, v117\n\t"
"v_cvt_pk_bf16_f32 v117, v118, v119\n\t"
"v_mfma_f32_16x16x32_bf16 v[148:151], a[220:223], a[196:199], v[148:151]\n\t"
"v_mfma_f32_16x16x32_bf16 v[148:151], a[224:227], a[188:191], v[148:151]\n\t"
"buffer_store_dwordx4 v[200:203], v248, s[16:19], s29 offen offset:128 sc1\n\t"
"v_mfma_f32_16x16x32_bf16 v[180:183], a[76:79], a[180:183], v[180:183]\n\t"
"ds_read_b128 a[180:183], v255 offset:23552\n\t"
"v_mfma_f32_16x16x32_bf16 v[180:183], a[4:7], a[184:187], v[180:183]\n\t"
"v_mfma_f32_16x16x32_bf16 v[180:183], a[80:83], a[176:179], v[180:183]\n\t"
"buffer_store_dwordx4 v[204:207], v248, s[16:19], s29 offen offset:256 sc1\n\t"
"v_mfma_f32_16x16x32_bf16 v[180:183], a[8:11], a[192:195], v[180:183]\n\t"
"ds_read_b128 a[192:195], v255 offset:24064\n\t"
"v_cvt_pk_bf16_f32 v118, v148, v149\n\t"
"v_mfma_f32_16x16x32_bf16 v[180:183], a[12:15], a[196:199], v[180:183]\n\t"
"v_cvt_pk_bf16_f32 v119, v150, v151\n\t"
"v_mfma_f32_16x16x32_bf16 v[180:183], a[228:231], a[188:191], v[180:183]\n\t"
"ds_write_b128 v249, v[116:119] offset:24576\n\t"
"v_mfma_f32_16x16x32_bf16 v[20:23], a[148:151], v[208:211], v[20:23]\n\t"
"s_waitcnt lgkmcnt(5)\n\t"
"v_mfma_f32_16x16x32_bf16 v[20:23], a[236:239], a[168:171], v[20:23]\n\t"
"v_mfma_f32_16x16x32_bf16 v[20:23], a[240:243], a[200:203], v[20:23]\n\t"
"v_mfma_f32_16x16x32_bf16 v[20:23], a[244:247], v[212:215], v[20:23]\n\t"
"v_cvt_pk_bf16_f32 v180, v180, v181\n\t"
"v_cvt_pk_bf16_f32 v181, v182, v183\n\t"
"v_mfma_f32_16x16x32_bf16 v[20:23], a[104:107], a[172:175], v[20:23]\n\t"
"v_mfma_f32_16x16x32_bf16 v[20:23], a[156:159], a[204:207], v[20:23]\n\t"
"ds_write_b64 v250, v[180:181] offset:24576\n\t"
"v_mfma_f32_16x16x32_bf16 v[52:55], a[164:167], v[208:211], v[52:55]\n\t"
"v_mfma_f32_16x16x32_bf16 v[52:55], v[240:243], a[168:171], v[52:55]\n\t"
"v_mfma_f32_16x16x32_bf16 v[52:55], v[96:99], a[200:203], v[52:55]\n\t"
"v_mfma_f32_16x16x32_bf16 v[52:55], a[248:251], v[212:215], v[52:55]\n\t"
"v_cvt_pk_bf16_f32 v20, v20, v21\n\t"
"v_cvt_pk_bf16_f32 v21, v22, v23\n\t"
"v_mfma_f32_16x16x32_bf16 v[52:55], a[52:55], a[172:175], v[52:55]\n\t"
"v_mfma_f32_16x16x32_bf16 v[52:55], a[56:59], a[204:207], v[52:55]\n\t"
"v_mfma_f32_16x16x32_bf16 v[84:87], a[48:51], v[208:211], v[84:87]\n\t"
"ds_read_b128 v[208:211], v255 offset:25088\n\t"
"v_mfma_f32_16x16x32_bf16 v[84:87], v[236:239], a[168:171], v[84:87]\n\t"
"v_mfma_f32_16x16x32_bf16 v[84:87], a[60:63], a[200:203], v[84:87]\n\t"
"s_waitcnt lgkmcnt(1)\n\t"
"s_barrier\n\t"
"v_mfma_f32_16x16x32_bf16 v[84:87], a[212:215], v[212:215], v[84:87]\n\t"
"ds_read_b128 v[196:199], v252 offset:18432\n\t"
"v_mfma_f32_16x16x32_bf16 v[84:87], a[252:255], a[172:175], v[84:87]\n\t"
"ds_read_b128 v[200:203], v252 offset:19456\n\t"
"v_cvt_pk_bf16_f32 v22, v52, v53\n\t"
"v_mfma_f32_16x16x32_bf16 v[84:87], a[28:31], a[204:207], v[84:87]\n\t"
"ds_read_b128 v[204:207], v252 offset:20480\n\t"
"v_cvt_pk_bf16_f32 v23, v54, v55\n\t"
"v_mfma_f32_16x16x32_bf16 v[120:123], a[112:115], a[184:187], v[120:123]\n\t"
"s_add_u32 s29, s23, 786432\n\t"
"ds_read_b128 v[212:215], v255 offset:25600\n\t"
"v_mfma_f32_16x16x32_bf16 v[120:123], a[120:123], a[176:179], v[120:123]\n\t"
"v_mfma_f32_16x16x32_bf16 v[120:123], a[64:67], a[180:183], v[120:123]\n\t"
"ds_write_b128 v249, v[20:23] offset:30720\n\t"
"v_mfma_f32_16x16x32_bf16 v[120:123], a[68:71], a[196:199], v[120:123]\n\t"
"s_waitcnt lgkmcnt(4)\n\t"
"v_cvt_pk_bf16_f32 v84, v84, v85\n\t"
"v_cvt_pk_bf16_f32 v85, v86, v87\n\t"
"v_mfma_f32_16x16x32_bf16 v[120:123], a[72:75], a[188:191], v[120:123]\n\t"
"v_mfma_f32_16x16x32_bf16 v[120:123], a[40:43], a[192:195], v[120:123]\n\t"
"ds_write_b64 v250, v[84:85] offset:30720\n\t"
"v_mfma_f32_16x16x32_bf16 v[152:155], a[44:47], a[184:187], v[152:155]\n\t"
"s_waitcnt lgkmcnt(4)\n\t"
"v_mfma_f32_16x16x32_bf16 v[152:155], a[32:35], a[176:179], v[152:155]\n\t"
"v_mfma_f32_16x16x32_bf16 v[152:155], a[36:39], a[180:183], v[152:155]\n\t"
"buffer_store_dwordx4 v[196:199], v248, s[16:19], s29 offen offset:0 sc1\n\t"
"v_mfma_f32_16x16x32_bf16 v[152:155], a[216:219], a[196:199], v[152:155]\n\t"
"s_waitcnt lgkmcnt(3)\n\t"
"v_cvt_pk_bf16_f32 v120, v120, v121\n\t"
"v_cvt_pk_bf16_f32 v121, v122, v123\n\t"
"v_mfma_f32_16x16x32_bf16 v[152:155], a[220:223], a[188:191], v[152:155]\n\t"
"v_mfma_f32_16x16x32_bf16 v[152:155], a[224:227], a[192:195], v[152:155]\n\t"
"buffer_store_dwordx4 v[200:203], v248, s[16:19], s29 offen offset:128 sc1\n\t"
"v_mfma_f32_16x16x32_bf16 v[184:187], a[76:79], a[184:187], v[184:187]\n\t"
"ds_read_b128 a[184:187], v255 offset:26624\n\t"
"v_mfma_f32_16x16x32_bf16 v[184:187], a[4:7], a[176:179], v[184:187]\n\t"
"v_mfma_f32_16x16x32_bf16 v[184:187], a[80:83], a[180:183], v[184:187]\n\t"
"buffer_store_dwordx4 v[204:207], v248, s[16:19], s29 offen offset:256 sc1\n\t"
"v_mfma_f32_16x16x32_bf16 v[184:187], a[8:11], a[196:199], v[184:187]\n\t"
"ds_read_b128 a[196:199], v255 offset:27136\n\t"
"v_cvt_pk_bf16_f32 v122, v152, v153\n\t"
"v_mfma_f32_16x16x32_bf16 v[184:187], a[12:15], a[188:191], v[184:187]\n\t"
"v_cvt_pk_bf16_f32 v123, v154, v155\n\t"
"v_mfma_f32_16x16x32_bf16 v[184:187], a[228:231], a[192:195], v[184:187]\n\t"
"ds_write_b128 v249, v[120:123] offset:36864\n\t"
"v_mfma_f32_16x16x32_bf16 v[24:27], a[148:151], a[168:171], v[24:27]\n\t"
"s_waitcnt lgkmcnt(5)\n\t"
"v_mfma_f32_16x16x32_bf16 v[24:27], a[236:239], a[200:203], v[24:27]\n\t"
"v_mfma_f32_16x16x32_bf16 v[24:27], a[240:243], v[208:211], v[24:27]\n\t"
"v_mfma_f32_16x16x32_bf16 v[24:27], a[244:247], a[172:175], v[24:27]\n\t"
"v_cvt_pk_bf16_f32 v184, v184, v185\n\t"
"v_cvt_pk_bf16_f32 v185, v186, v187\n\t"
"v_mfma_f32_16x16x32_bf16 v[24:27], a[104:107], a[204:207], v[24:27]\n\t"
"v_mfma_f32_16x16x32_bf16 v[24:27], a[156:159], v[212:215], v[24:27]\n\t"
"ds_write_b64 v250, v[184:185] offset:36864\n\t"
"v_mfma_f32_16x16x32_bf16 v[56:59], a[164:167], a[168:171], v[56:59]\n\t"
"v_mfma_f32_16x16x32_bf16 v[56:59], v[240:243], a[200:203], v[56:59]\n\t"
"v_mfma_f32_16x16x32_bf16 v[56:59], v[96:99], v[208:211], v[56:59]\n\t"
"v_mfma_f32_16x16x32_bf16 v[56:59], a[248:251], a[172:175], v[56:59]\n\t"
"v_cvt_pk_bf16_f32 v24, v24, v25\n\t"
"v_cvt_pk_bf16_f32 v25, v26, v27\n\t"
"v_mfma_f32_16x16x32_bf16 v[56:59], a[52:55], a[204:207], v[56:59]\n\t"
"v_mfma_f32_16x16x32_bf16 v[56:59], a[56:59], v[212:215], v[56:59]\n\t"
"v_mfma_f32_16x16x32_bf16 v[88:91], a[48:51], a[168:171], v[88:91]\n\t"
"ds_read_b128 a[168:171], v255 offset:28160\n\t"
"v_mfma_f32_16x16x32_bf16 v[88:91], v[236:239], a[200:203], v[88:91]\n\t"
"v_mfma_f32_16x16x32_bf16 v[88:91], a[60:63], v[208:211], v[88:91]\n\t"
"s_waitcnt lgkmcnt(1)\n\t"
"s_barrier\n\t"
"v_mfma_f32_16x16x32_bf16 v[88:91], a[212:215], a[172:175], v[88:91]\n\t"
"ds_read_b128 v[196:199], v252 offset:30720\n\t"
"v_mfma_f32_16x16x32_bf16 v[88:91], a[252:255], a[204:207], v[88:91]\n\t"
"ds_read_b128 v[200:203], v252 offset:31744\n\t"
"v_cvt_pk_bf16_f32 v26, v56, v57\n\t"
"v_mfma_f32_16x16x32_bf16 v[88:91], a[28:31], v[212:215], v[88:91]\n\t"
"ds_read_b128 v[204:207], v252 offset:32768\n\t"
"v_cvt_pk_bf16_f32 v27, v58, v59\n\t"
"v_mfma_f32_16x16x32_bf16 v[124:127], a[112:115], a[176:179], v[124:127]\n\t"
"s_add_u32 s29, s23, 983040\n\t"
"ds_read_b128 a[172:175], v255 offset:28672\n\t"
"v_mfma_f32_16x16x32_bf16 v[124:127], a[120:123], a[180:183], v[124:127]\n\t"
"v_mfma_f32_16x16x32_bf16 v[124:127], a[64:67], a[184:187], v[124:127]\n\t"
"ds_write_b128 v249, v[24:27] offset:18432\n\t"
"v_mfma_f32_16x16x32_bf16 v[124:127], a[68:71], a[188:191], v[124:127]\n\t"
"s_waitcnt lgkmcnt(4)\n\t"
"v_cvt_pk_bf16_f32 v88, v88, v89\n\t"
"v_cvt_pk_bf16_f32 v89, v90, v91\n\t"
"v_mfma_f32_16x16x32_bf16 v[124:127], a[72:75], a[192:195], v[124:127]\n\t"
"v_mfma_f32_16x16x32_bf16 v[124:127], a[40:43], a[196:199], v[124:127]\n\t"
"ds_write_b64 v250, v[88:89] offset:18432\n\t"
"v_mfma_f32_16x16x32_bf16 v[156:159], a[44:47], a[176:179], v[156:159]\n\t"
"s_waitcnt lgkmcnt(4)\n\t"
"v_mfma_f32_16x16x32_bf16 v[156:159], a[32:35], a[180:183], v[156:159]\n\t"
"v_mfma_f32_16x16x32_bf16 v[156:159], a[36:39], a[184:187], v[156:159]\n\t"
"buffer_store_dwordx4 v[196:199], v248, s[16:19], s29 offen offset:0 sc1\n\t"
"v_mfma_f32_16x16x32_bf16 v[156:159], a[216:219], a[188:191], v[156:159]\n\t"
"s_waitcnt lgkmcnt(3)\n\t"
"v_cvt_pk_bf16_f32 v124, v124, v125\n\t"
"v_cvt_pk_bf16_f32 v125, v126, v127\n\t"
"v_mfma_f32_16x16x32_bf16 v[156:159], a[220:223], a[192:195], v[156:159]\n\t"
"v_mfma_f32_16x16x32_bf16 v[156:159], a[224:227], a[196:199], v[156:159]\n\t"
"buffer_store_dwordx4 v[200:203], v248, s[16:19], s29 offen offset:128 sc1\n\t"
"v_mfma_f32_16x16x32_bf16 v[188:191], a[76:79], a[176:179], v[188:191]\n\t"
"ds_read_b128 a[176:179], v255 offset:29696\n\t"
"v_mfma_f32_16x16x32_bf16 v[188:191], a[4:7], a[180:183], v[188:191]\n\t"
"v_mfma_f32_16x16x32_bf16 v[188:191], a[80:83], a[184:187], v[188:191]\n\t"
"buffer_store_dwordx4 v[204:207], v248, s[16:19], s29 offen offset:256 sc1\n\t"
"v_mfma_f32_16x16x32_bf16 v[188:191], a[8:11], a[188:191], v[188:191]\n\t"
"ds_read_b128 a[188:191], v255 offset:30208\n\t"
"v_cvt_pk_bf16_f32 v126, v156, v157\n\t"
"v_mfma_f32_16x16x32_bf16 v[188:191], a[12:15], a[192:195], v[188:191]\n\t"
"v_cvt_pk_bf16_f32 v127, v158, v159\n\t"
"v_mfma_f32_16x16x32_bf16 v[188:191], a[228:231], a[196:199], v[188:191]\n\t"
"ds_write_b128 v249, v[124:127] offset:24576\n\t"
"v_mfma_f32_16x16x32_bf16 v[28:31], a[148:151], a[200:203], v[28:31]\n\t"
"s_waitcnt lgkmcnt(5)\n\t"
"v_mfma_f32_16x16x32_bf16 v[28:31], a[236:239], v[208:211], v[28:31]\n\t"
"v_mfma_f32_16x16x32_bf16 v[28:31], a[240:243], a[168:171], v[28:31]\n\t"
"v_mfma_f32_16x16x32_bf16 v[28:31], a[244:247], a[204:207], v[28:31]\n\t"
"v_cvt_pk_bf16_f32 v188, v188, v189\n\t"
"v_cvt_pk_bf16_f32 v189, v190, v191\n\t"
"v_mfma_f32_16x16x32_bf16 v[28:31], a[104:107], v[212:215], v[28:31]\n\t"
"v_mfma_f32_16x16x32_bf16 v[28:31], a[156:159], a[172:175], v[28:31]\n\t"
"ds_write_b64 v250, v[188:189] offset:24576\n\t"
"v_mfma_f32_16x16x32_bf16 v[60:63], a[164:167], a[200:203], v[60:63]\n\t"
"v_mfma_f32_16x16x32_bf16 v[60:63], v[240:243], v[208:211], v[60:63]\n\t"
"v_mfma_f32_16x16x32_bf16 v[60:63], v[96:99], a[168:171], v[60:63]\n\t"
"v_mfma_f32_16x16x32_bf16 v[60:63], a[248:251], a[204:207], v[60:63]\n\t"
"v_cvt_pk_bf16_f32 v28, v28, v29\n\t"
"v_cvt_pk_bf16_f32 v29, v30, v31\n\t"
"v_mfma_f32_16x16x32_bf16 v[60:63], a[52:55], v[212:215], v[60:63]\n\t"
"v_mfma_f32_16x16x32_bf16 v[60:63], a[56:59], a[172:175], v[60:63]\n\t"
"v_mfma_f32_16x16x32_bf16 v[92:95], a[48:51], a[200:203], v[92:95]\n\t"
"v_mfma_f32_16x16x32_bf16 v[92:95], v[236:239], v[208:211], v[92:95]\n\t"
"v_mfma_f32_16x16x32_bf16 v[92:95], a[60:63], a[168:171], v[92:95]\n\t"
"s_waitcnt lgkmcnt(0)\n\t"
"s_barrier\n\t"
"v_mfma_f32_16x16x32_bf16 v[92:95], a[212:215], a[204:207], v[92:95]\n\t"
"ds_read_b128 v[196:199], v252 offset:18432\n\t"
"v_mfma_f32_16x16x32_bf16 v[92:95], a[252:255], v[212:215], v[92:95]\n\t"
"ds_read_b128 v[200:203], v252 offset:19456\n\t"
"v_cvt_pk_bf16_f32 v30, v60, v61\n\t"
"v_mfma_f32_16x16x32_bf16 v[92:95], a[28:31], a[172:175], v[92:95]\n\t"
"ds_read_b128 v[204:207], v252 offset:20480\n\t"
"v_cvt_pk_bf16_f32 v31, v62, v63\n\t"
"v_mfma_f32_16x16x32_bf16 v[128:131], a[112:115], a[180:183], v[128:131]\n\t"
"s_add_u32 s29, s23, 1179648\n\t"
"v_mfma_f32_16x16x32_bf16 v[128:131], a[120:123], a[184:187], v[128:131]\n\t"
"v_mfma_f32_16x16x32_bf16 v[128:131], a[64:67], a[176:179], v[128:131]\n\t"
"ds_write_b128 v249, v[28:31] offset:30720\n\t"
"v_mfma_f32_16x16x32_bf16 v[128:131], a[68:71], a[192:195], v[128:131]\n\t"
"s_waitcnt lgkmcnt(3)\n\t"
"v_cvt_pk_bf16_f32 v92, v92, v93\n\t"
"v_cvt_pk_bf16_f32 v93, v94, v95\n\t"
"v_mfma_f32_16x16x32_bf16 v[128:131], a[72:75], a[196:199], v[128:131]\n\t"
"v_mfma_f32_16x16x32_bf16 v[128:131], a[40:43], a[188:191], v[128:131]\n\t"
"ds_write_b64 v250, v[92:93] offset:30720\n\t"
"v_mfma_f32_16x16x32_bf16 v[160:163], a[44:47], a[180:183], v[160:163]\n\t"
"s_waitcnt lgkmcnt(3)\n\t"
"v_mfma_f32_16x16x32_bf16 v[160:163], a[32:35], a[184:187], v[160:163]\n\t"
"v_mfma_f32_16x16x32_bf16 v[160:163], a[36:39], a[176:179], v[160:163]\n\t"
"buffer_store_dwordx4 v[196:199], v248, s[16:19], s29 offen offset:0 sc1\n\t"
"v_mfma_f32_16x16x32_bf16 v[160:163], a[216:219], a[192:195], v[160:163]\n\t"
"s_waitcnt lgkmcnt(2)\n\t"
"v_cvt_pk_bf16_f32 v128, v128, v129\n\t"
"v_cvt_pk_bf16_f32 v129, v130, v131\n\t"
"v_mfma_f32_16x16x32_bf16 v[160:163], a[220:223], a[196:199], v[160:163]\n\t"
"v_mfma_f32_16x16x32_bf16 v[160:163], a[224:227], a[188:191], v[160:163]\n\t"
"buffer_store_dwordx4 v[200:203], v248, s[16:19], s29 offen offset:128 sc1\n\t"
"v_mfma_f32_16x16x32_bf16 v[192:195], a[76:79], a[180:183], v[192:195]\n\t"
"v_mfma_f32_16x16x32_bf16 v[192:195], a[4:7], a[184:187], v[192:195]\n\t"
"v_mfma_f32_16x16x32_bf16 v[192:195], a[80:83], a[176:179], v[192:195]\n\t"
"buffer_store_dwordx4 v[204:207], v248, s[16:19], s29 offen offset:256 sc1\n\t"
"v_mfma_f32_16x16x32_bf16 v[192:195], a[8:11], a[192:195], v[192:195]\n\t"
"v_cvt_pk_bf16_f32 v130, v160, v161\n\t"
"v_cvt_pk_bf16_f32 v131, v162, v163\n\t"
"v_mfma_f32_16x16x32_bf16 v[192:195], a[12:15], a[196:199], v[192:195]\n\t"
"v_mfma_f32_16x16x32_bf16 v[192:195], a[228:231], a[188:191], v[192:195]\n\t"
"ds_write_b128 v249, v[128:131] offset:36864\n\t"
"s_nop 6\n\t"
"v_cvt_pk_bf16_f32 v192, v192, v193\n\t"
"v_cvt_pk_bf16_f32 v193, v194, v195\n\t"
"ds_write_b64 v250, v[192:193] offset:36864\n\t"
"s_waitcnt lgkmcnt(0)\n\t"
"s_barrier\n\t"
"ds_read_b128 v[196:199], v252 offset:30720\n\t"
"ds_read_b128 v[200:203], v252 offset:31744\n\t"
"ds_read_b128 v[208:211], v252 offset:32768\n\t"
"s_waitcnt lgkmcnt(2)\n\t"
"s_add_u32 s29, s23, 1376256\n\t"
"buffer_store_dwordx4 v[196:199], v248, s[16:19], s29 offen offset:0 sc1\n\t"
"s_waitcnt lgkmcnt(1)\n\t"
"buffer_store_dwordx4 v[200:203], v248, s[16:19], s29 offen offset:128 sc1\n\t"
"s_waitcnt lgkmcnt(0)\n\t"
"buffer_store_dwordx4 v[208:211], v248, s[16:19], s29 offen offset:256 sc1\n\t"
"s_waitcnt vmcnt(0)\n\t"
"Lwino_end:\n\t"
  : "+{v205}"(dummy)
  : "s"((unsigned)pu), "s"((unsigned)(pu >> 32)), "s"((unsigned)px), "s"((unsigned)(px >> 32)), "s"((unsigned)py), "s"((unsigned)(py >> 32)),
    "s"(xbase), "s"(ybase), "s"(w), "s"((unsigned)(size_t)(__attribute__((address_space(3))) char*)smem), "s"(blockIdx.x), "s"((unsigned long long)lane_tab),
    "{v252}"(w * 3072 + lane * 16), "s"(medge), "s"((unsigned)pw), "s"((unsigned)(pw >> 32)), "s"(fso), "s"(fuo), "s"(0), "s"(0), "s"(fs8),
    "{v206}"(fwo), "{v207}"(flw), "{v208}"(fst), "{v209}"((lane >> 5) * 8),
    "{v28}"((8 * w + ((lane >> 2) & 7)) * PXB + (lane >> 5) * 64 + 16 * ((lane & 3) ^ ((lane >> 3) & 2))),
    "{v29}"((lane >> 2) * PXB + (lane & 3) * 16 + w * 64), "{v30}"((lane >> 1) * PXB + (lane & 1) * 16 + 256 + w * 32), "{v31}"(18432 + w * 3072 + lane * 8)
  : "v0", "v1", "v2", "v3", "v4", "v5", "v6", "v7", "v8", "v9", "v10", "v11", "v12", "v13", "v14", "v15", "v16", "v17", "v18", "v19", "v20", "v21", "v22", "v23", "v24", "v25", "v26", "v27", "v32", "v33", "v34", "v35", "v36", "v37", "v38", "v39", "v40", "v41", "v42", "v43", "v44", "v45", "v46", "v47", "v48", "v49", "v50", "v51", "v52", "v53", "v54", "v55", "v56", "v57", "v58", "v59", "v60", "v61", "v62", "v63", "v64", "v65", "v66", "v67", "v68", "v69", "v70", "v71", "v72", "v73", "v74", "v75", "v76", "v77", "v78", "v79", "v80", "v81", "v82", "v83", "v84", "v85", "v86", "v87", "v88", "v89", "v90", "v91", "v92", "v93", "v94", "v95", "v96", "v97", "v98", "v99", "v100", "v101", "v102", "v103", "v104", "v105", "v106", "v107", "v108", "v109", "v110", "v111", "v112", "v113", "v114", "v115", "v116", "v117", "v118", "v119", "v120", "v121", "v122", "v123", "v124", "v125", "v126", "v127", "v128", "v129", "v130", "v131", "v132", "v133", "v134", "v135", "v136", "v137", "v138", "v139", "v140", "v141", "v142", "v143", "v144", "v145", "v146", "v147", "v148", "v149", "v150", "v151", "v152", "v153", "v154", "v155", "v156", "v157", "v158", "v159", "v160", "v161", "v162", "v163", "v164", "v165", "v166", "v167", "v168", "v169", "v170", "v171", "v172", "v173", "v174", "v175", "v176", "v177", "v178", "v179", "v180", "v181", "v182", "v183", "v184", "v185", "v186", "v187", "v188", "v189", "v190", "v191", "v192", "v193", "v194", "v195", "v196", "v197", "v198", "v199", "v200", "v201", "v202", "v203", "v204", "v210", "v211", "v212", "v213", "v214", "v215", "v216", "v217", "v218", "v219", "v220", "v221", "v222", "v223", "v224", "v225", "v226", "v227", "v228", "v229", "v230", "v231", "v232", "v233", "v234", "v235", "v236", "v237", "v238", "v239", "v240", "v241", "v242", "v243", "v244", "v245", "v246", "v247", "v248", "v249", "v250", "v251", "v253", "v254", "v255", "a0", "a1", "a2", "a3", "a4", "a5", "a6", "a7", "a8", "a9", "a10", "a11", "a12", "a13", "a14", "a15", "a16", "a17", "a18", "a19", "a20", "a21", "a22", "a23", "a24", "a25", "a26", "a27", "a28", "a29", "a30", "a31", "a32", "a33", "a34", "a35", "a36", "a37", "a38", "a39", "a40", "a41", "a42", "a43", "a44", "a45", "a46", "a47", "a48", "a49", "a50", "a51", "a52", "a53", "a54", "a55", "a56", "a57", "a58", "a59", "a60", "a61", "a62", "a63", "a64", "a65", "a66", "a67", "a68", "a69", "a70", "a71", "a72", "a73", "a74", "a75", "a76", "a77", "a78", "a79", "a80", "a81", "a82", "a83", "a84", "a85", "a86", "a87", "a88", "a89", "a90", "a91", "a92", "a93", "a94", "a95", "a96", "a97", "a98", "a99", "a100", "a101", "a102", "a103", "a104", "a105", "a106", "a107", "a108", "a109", "a110", "a111", "a112", "a113", "a114", "a115", "a116", "a117", "a118", "a119", "a120", "a121", "a122", "a123", "a124", "a125", "a126", "a127", "a128", "a129", "a130", "a131", "a132", "a133", "a134", "a135", "a136", "a137", "a138", "a139", "a140", "a141", "a142", "a143", "a144", "a145", "a146", "a147", "a148", "a149", "a150", "a151", "a152", "a153", "a154", "a155", "a156", "a157", "a158", "a159", "a160", "a161", "a162", "a163", "a164", "a165", "a166", "a167", "a168", "a169", "a170", "a171", "a172", "a173", "a174", "a175", "a176", "a177", "a178", "a179", "a180", "a181", "a182", "a183", "a184", "a185", "a186", "a187", "a188", "a189", "a190", "a191", "a192", "a193", "a194", "a195", "a196", "a197", "a198", "a199", "a200", "a201", "a202", "a203", "a204", "a205", "a206", "a207", "a208", "a209", "a210", "a211", "a212", "a213", "a214", "a215", "a216", "a217", "a218", "a219", "a220", "a221", "a222", "a223", "a224", "a225", "a226", "a227", "a228", "a229", "a230", "a231", "a232", "a233", "a234", "a235", "a236", "a237", "a238", "a239", "a240", "a241", "a242", "a243", "a244", "a245", "a246", "a247", "a248", "a249", "a250", "a251", "a252", "a253", "a254", "a255", "s8", "s9", "s10", "s11", "s12", "s13", "s14", "s15", "s16", "s17", "s18", "s19", "s20", "s21", "s22", "s23", "s24", "s25", "s26", "s27", "s28", "s29", "s30", "s31", "s32", "s33", "s34", "s35", "s36", "s37", "s38", "s39", "s40", "s41", "s42", "s43", "s44", "s45", "s46", "s47", "s48", "s49", "s50", "s51", "s52", "s53", "s54", "s55", "s56", "s57", "s58", "s59", "s60", "s61", "s62", "s63", "s64", "s65", "s66", "s67", "s68", "s69", "s70", "s71", "s72", "s73", "s74", "s75", "s76", "s77", "scc", "vcc", "memory");
}

extern "C" void kernel_launch(void* const* d_in, const int* in_sizes, int n_in, void* d_out, int out_size, void* d_ws,
                              size_t ws_size, hipStream_t stream) {
  wino_main_kernel<<<256, 256, 0, stream>>>((const char*)d_in[0], (char*)d_ws, (char*)d_out, (const char*)d_in[1]);
}
```

```cpp
#include <hip/hip_runtime.h>
#include <hip/hip_bf16.h>
#include <stdint.h>

#define IMG_W 512
#define CH 192
#define PXB 384
#define ROWB (IMG_W * PXB)
#define U_BYTES 1769472

__device__ const __attribute__((aligned(1024))) unsigned lane_tab[6400] = {
  0xa800,0xa820,0x0,0x3ea80a81,0xa808,0xa828,0x40,0x3ea80a81,0xa810,0xa830,0x90,0x3ea80a81,0xa818,0xa838,0xd0,0x3ea80a81,
  0xa920,0xa900,0x2400,0xbebd0bd1,0xa928,0xa908,0x2440,0xbebd0bd1,0xa930,0xa910,0x2490,0xbebd0bd1,0xa938,0xa918,0x24d0,0xbebd0bd1,
  0xa840,0xa860,0x200,0x3ea80a81,0xa848,0xa868,0x240,0x3ea80a81,0xa850,0xa870,0x290,0x3ea80a81,0xa858,0xa878,0x2d0,0x3ea80a81,
  0xa960,0xa940,0x200,0x3ea80a81,0xa968,0xa948,0x240,0x3ea80a81,0xa970,0xa950,0x290,0x3ea80a81,0xa978,0xa958,0x2d0,0x3ea80a81,
  0xea80,0xeaa0,0x10,0x3ea80a81,0xea88,0xeaa8,0x50,0x3ea80a81,0xea90,0xeab0,0x80,0x3ea80a81,0xea98,0xeab8,0xc0,0x3ea80a81,
  0xa9a0,0xa980,0x2410,0xbebd0bd1,0xa9a8,0xa988,0x2450,0xbebd0bd1,0xa9b0,0xa990,0x2480,0xbebd0bd1,0xa9b8,0xa998,0x24c0,0xbebd0bd1,
  0xc0c0,0xc0e0,0x210,0x3ea80a81,0xc0c8,0xc0e8,0x250,0x3ea80a81,0xc0d0,0xc0f0,0x280,0x3ea80a81,0xc0d8,0xc0f8,0x2c0,0x3ea80a81,
  0xc1e0,0xc1c0,0x210,0x3ea80a81,0xc1e8,0xc1c8,0x250,0x3ea80a81,0xc1f0,0xc1d0,0x280,0x3ea80a81,0xc1f8,0xc1d8,0x2c0,0x3ea80a81,
  0xae00,0xae20,0x20,0x3ea80a81,0xae08,0xae28,0x60,0x3ea80a81,0xae10,0xae30,0xb0,0x3ea80a81,0xae18,0xae38,0xf0,0x3ea80a81,
  0xeb20,0xeb00,0x2420,0xbebd0bd1,0xeb28,0xeb08,0x2460,0xbebd0bd1,0xeb30,0xeb10,0x24b0,0xbebd0bd1,0xeb38,0xeb18,0x24f0,0xbebd0bd1,
  0xc040,0xc060,0x220,0x3ea80a81,0xc048,0xc068,0x260,0x3ea80a81,0xc050,0xc070,0x2b0,0x3ea80a81,0xc058,0xc078,0x2f0,0x3ea80a81,
  0xc160,0xc140,0x220,0x3ea80a81,0xc168,0xc148,0x260,0x3ea80a81,0xc170,0xc150,0x2b0,0x3ea80a81,0xc178,0xc158,0x2f0,0x3ea80a81,
  0xa880,0xa8a0,0x30,0x3ea80a81,0xa888,0xa8a8,0x70,0x3ea80a81,0xa890,0xa8b0,0xa0,0x3ea80a81,0xa898,0xa8b8,0xe0,0x3ea80a81,
  0xa8c0,0xa8e0,0x2430,0xbebd0bd1,0xa8c8,0xa8e8,0x2470,0xbebd0bd1,0xa8d0,0xa8f0,0x24a0,0xbebd0bd1,0xa8d8,0xa8f8,0x24e0,0xbebd0bd1,
  0xa9e0,0xa9c0,0x230,0x3ea80a81,0xa9e8,0xa9c8,0x270,0x3ea80a81,0xa9f0,0xa9d0,0x2a0,0x3ea80a81,0xa9f8,0xa9d8,0x2e0,0x3ea80a81,
  0xae00,0xae20,0x230,0x3ea80a81,0xae08,0xae28,0x270,0x3ea80a81,0xae10,0xae30,0x2a0,0x3ea80a81,0xae18,0xae38,0x2e0,0x3ea80a81,
  0x0,0xc042,0x0,0x0,0x0,0xc0420000,0x0,0x0,0x0,0x0,0x0,0xc042,0x0,0x0,0x0,0xc0420000,
  0x3f903f90,0x0,0x0,0x0,0x3f900000,0x0,0x3f90,0x0,0x0,0x0,0x3f903f90,0x0,0x0,0x0,0x3f900000,0x0,
  0x0,0x0,0x0,0x0,0x0,0x0,0x0,0x0,0x0,0x0,0x0,0x0,0x0,0x0,0x0,0x0,
  0x0,0x0,0x0,0x0,0x0,0x0,0x0,0x0,0x0,0x0,0x0,0x0,0x0,0x0,0x0,0x0,
  0x0,0x0,0x3f90,0x0,0x0,0x0,0x0,0x0,0x0,0x0,0x0,0x0,0x0,0x0,0x0,0x0,
  0x0,0x0,0x0,0x0,0xc0420000,0x0,0x0,0x0,0x0,0x0,0x0,0x0,0x0,0x0,0xc0420000,0x0,
  0x0,0x3f90,0x0,0x0,0x0,0x3f903f90,0x0,0x0,0x0,0x3f900000,0x0,0x3f90,0x0,0x0,0x0,0x3f903f90,
  0x0,0x3f90,0x0,0x0,0x0,0x3f903f90,0x0,0x0,0x0,0x3f900000,0x0,0x3f90,0x0,0x0,0x0,0x3f903f90,
  0x0,0x0,0x0,0x0,0x0,0x0,0x0,0x0,0x0,0x0,0x0,0x0,0x0,0x0,0x0,0x0,
  0x0,0x0,0x0,0x0,0x0,0x0,0x0,0x0,0x0,0x0,0x0,0x0,0x0,0x0,0x3f90,0x0,
  0x0,0xc042,0x3f900000,0x0,0x0,0xc0420000,0x0,0x0,0x0,0x0,0x0,0xc042,0x0,0x0,0x0,0xc0420000,
  0x0,0xc042,0x3f900000,0x0,0x0,0xc0420000,0x0,0x0,0x0,0x0,0x0,0xc042,0x0,0x0,0x0,0xc0420000,
  0x3f900000,0x0,0x0,0x0,0x3f900000,0x3f90,0x0,0x0,0x0,0x3f90,0x3f900000,0x0,0x0,0x0,0x3f900000,0x3f90,
  0xc042,0x0,0x0,0x0,0x0,0x0,0x0,0x0,0x0,0x0,0xc042,0x0,0x0,0x0,0x0,0x0,
  0x0,0x0,0x0,0x0,0x0,0x0,0x0,0x0,0x0,0x0,0x0,0x0,0x0,0x0,0x0,0x0,
  0x0,0x0,0x0,0x0,0x0,0x0,0x0,0x0,0x0,0x0,0x0,0x0,0x0,0x0,0x0,0x0,
  0x0,0x0,0xa800,0x0,0x40,0x40,0xa808,0x10,0x90,0x90,0xa810,0x20,0xd0,0xd0,0xa818,0x30,
  0x120,0x120,0xa840,0x180,0x160,0x160,0xa848,0x190,0x1b0,0x1b0,0xa850,0x1a0,0x1f0,0x1f0,0xa858,0x1b0,
  0x1000,0x2408,0xa880,0x300,0x1040,0x2448,0xa888,0x310,0x1090,0x2498,0xa890,0x320,0x10d0,0x24d8,0xa898,0x330,
  0x1120,0x2528,0xa8c0,0x480,0x1160,0x2568,0xa8c8,0x490,0x11b0,0x25b8,0xa8d0,0x4a0,0x11f0,0x25f8,0xa8d8,0x4b0,
  0x10,0x10,0xa920,0x620,0x50,0x50,0xa928,0x630,0x80,0x80,0xa930,0x600,0xc0,0xc0,0xa938,0x610,
  0x130,0x130,0xa960,0x7a0,0x170,0x170,0xa968,0x7b0,0x1a0,0x1a0,0xa970,0x780,0x1e0,0x1e0,0xa978,0x790,
  0x1010,0x2418,0xa9a0,0x920,0x1050,0x2458,0xa9a8,0x930,0x1080,0x2488,0xa9b0,0x900,0x10c0,0x24c8,0xa9b8,0x910,
  0x1130,0x2538,0xa9e0,0xaa0,0x1170,0x2578,0xa9e8,0xab0,0x11a0,0x25a8,0xa9f0,0xa80,0x11e0,0x25e8,0xa9f8,0xa90,
  0x20,0x20,0xae00,0x40,0x60,0x60,0xae08,0x50,0xb0,0xb0,0xae10,0x60,0xf0,0xf0,0xae18,0x70,
  0x100,0x100,0xae40,0x1c0,0x140,0x140,0xae48,0x1d0,0x190,0x190,0xae50,0x1e0,0x1d0,0x1d0,0xae58,0x1f0,
  0x1020,0x2428,0xae80,0x340,0x1060,0x2468,0xae88,0x350,0x10b0,0x24b8,0xae90,0x360,0x10f0,0x24f8,0xae98,0x370,
  0x1100,0x2508,0xaec0,0x4c0,0x1140,0x2548,0xaec8,0x4d0,0x1190,0x2598,0xaed0,0x4e0,0x11d0,0x25d8,0xaed8,0x4f0,
  0x30,0x30,0xaf20,0x660,0x70,0x70,0xaf28,0x670,0xa0,0xa0,0xaf30,0x640,0xe0,0xe0,0xaf38,0x650,
  0x110,0x110,0xaf60,0x7e0,0x150,0x150,0xaf68,0x7f0,0x180,0x180,0xaf70,0x7c0,0x1c0,0x1c0,0xaf78,0x7d0,
  0x1030,0x2438,0xafa0,0x960,0x1070,0x2478,0xafa8,0x970,0x10a0,0x24a8,0xafb0,0x940,0x10e0,0x24e8,0xafb8,0x950,
  0x1110,0x2518,0xafe0,0xae0,0x1150,0x2558,0xafe8,0xaf0,0x1180,0x2588,0xaff0,0xac0,0x11c0,0x25c8,0xaff8,0xad0,
  0xb400,0xb420,0x120,0x3ea80a81,0xb408,0xb428,0x160,0x3ea80a81,0xb410,0xb430,0x1b0,0x3ea80a81,0xb418,0xb438,0x1f0,0x3ea80a81,
  0xb440,0xb460,0x2520,0xbebd0bd1,0xb448,0xb468,0x2560,0xbebd0bd1,0xb450,0xb470,0x25b0,0xbebd0bd1,0xb458,0xb478,0x25f0,0xbebd0bd1,
  0xcc40,0xcc60,0x320,0x3ea80a81,0xcc48,0xcc68,0x360,0x3ea80a81,0xcc50,0xcc70,0x3b0,0x3ea80a81,0xcc58,0xcc78,0x3f0,0x3ea80a81,
  0xcd60,0xcd40,0x320,0x3ea80a81,0xcd68,0xcd48,0x360,0x3ea80a81,0xcd70,0xcd50,0x3b0,0x3ea80a81,0xcd78,0xcd58,0x3f0,0x3ea80a81,
  0xb5a0,0xb580,0x130,0x3ea80a81,0xb5a8,0xb588,0x170,0x3ea80a81,0xb5b0,0xb590,0x1a0,0x3ea80a81,0xb5b8,0xb598,0x1e0,0x3ea80a81,
  0xccc0,0xcce0,0x2530,0xbebd0bd1,0xccc8,0xcce8,0x2570,0xbebd0bd1,0xccd0,0xccf0,0x25a0,0xbebd0bd1,0xccd8,0xccf8,0x25e0,0xbebd0bd1,
  0xa9e0,0xa9c0,0x330,0x3ea80a81,0xa9e8,0xa9c8,0x370,0x3ea80a81,0xa9f0,0xa9d0,0x3a0,0x3ea80a81,0xa9f8,0xa9d8,0x3e0,0x3ea80a81,
  0xc1e0,0xc1c0,0x330,0x3ea80a81,0xc1e8,0xc1c8,0x370,0x3ea80a81,0xc1f0,0xc1d0,0x3a0,0x3ea80a81,0xc1f8,0xc1d8,0x3e0,0x3ea80a81,
  0xb520,0xb500,0x100,0x3ea80a81,0xb528,0xb508,0x140,0x3ea80a81,0xb530,0xb510,0x190,0x3ea80a81,0xb538,0xb518,0x1d0,0x3ea80a81,
  0xea40,0xea60,0x2500,0xbebd0bd1,0xea48,0xea68,0x2540,0xbebd0bd1,0xea50,0xea70,0x2590,0xbebd0bd1,0xea58,0xea78,0x25d0,0xbebd0bd1,
  0xb560,0xb540,0x300,0x3ea80a81,0xb568,0xb548,0x340,0x3ea80a81,0xb570,0xb550,0x390,0x3ea80a81,0xb578,0xb558,0x3d0,0x3ea80a81,
  0xb480,0xb4a0,0x300,0x3ea80a81,0xb488,0xb4a8,0x340,0x3ea80a81,0xb490,0xb4b0,0x390,0x3ea80a81,0xb498,0xb4b8,0x3d0,0x3ea80a81,
  0xb4c0,0xb4e0,0x110,0x3ea80a81,0xb4c8,0xb4e8,0x150,0x3ea80a81,0xb4d0,0xb4f0,0x180,0x3ea80a81,0xb4d8,0xb4f8,0x1c0,0x3ea80a81,
  0xb5e0,0xb5c0,0x2510,0xbebd0bd1,0xb5e8,0xb5c8,0x2550,0xbebd0bd1,0xb5f0,0xb5d0,0x2580,0xbebd0bd1,0xb5f8,0xb5d8,0x25c0,0xbebd0bd1,
  0xcde0,0xcdc0,0x310,0x3ea80a81,0xcde8,0xcdc8,0x350,0x3ea80a81,0xcdf0,0xcdd0,0x380,0x3ea80a81,0xcdf8,0xcdd8,0x3c0,0x3ea80a81,
  0xb520,0xb500,0x310,0x3ea80a81,0xb528,0xb508,0x350,0x3ea80a81,0xb530,0xb510,0x380,0x3ea80a81,0xb538,0xb518,0x3c0,0x3ea80a81,
  0xc0420000,0x0,0x0,0x0,0x0,0x0,0x0,0x0,0x0,0x0,0xc0420000,0x0,0x0,0x0,0x0,0x0,
  0x3f90,0x0,0x0,0x0,0x0,0x0,0x3f90,0x0,0x0,0x0,0x3f90,0x0,0x0,0x0,0x0,0x0,
  0x0,0xc042,0x0,0x0,0x0,0xc0420000,0x0,0x0,0x0,0x0,0x0,0xc042,0x0,0x0,0x0,0xc0420000,
  0x0,0xc042,0x0,0x0,0x0,0xc0420000,0x0,0x0,0x0,0x0,0x0,0xc042,0x0,0x0,0x0,0xc0420000,
  0x0,0x0,0x0,0x3f90,0x0,0x0,0x0,0x0,0x0,0x0,0x0,0x0,0x0,0x0,0x0,0x0,
  0x0,0x0,0x0,0x0,0xc042,0x0,0x0,0x0,0x0,0x0,0x0,0x0,0x0,0x0,0xc042,0x0,
  0x3f900000,0x0,0x0,0x3f900000,0x3f900000,0x0,0x0,0x0,0x0,0x0,0x3f900000,0x0,0x0,0x0,0x3f900000,0x0,
  0x3f900000,0x0,0x0,0x3f900000,0x3f900000,0x0,0x0,0x0,0x0,0x0,0x3f900000,0x0,0x0,0x0,0x3f900000,0x0,
  0x0,0x0,0x0,0x0,0x0,0xc042,0x0,0x0,0x0,0x0,0x0,0x0,0x0,0x0,0x0,0xc042,
  0x3f90,0xc0420000,0x0,0x0,0x3f90,0x0,0x0,0x0,0x0,0x0,0x3f90,0xc0420000,0x0,0x0,0x3f903f90,0x0,
  0x0,0x0,0x0,0x0,0x0,0x0,0x0,0x0,0x0,0x0,0x0,0x0,0x0,0x0,0x0,0x0,
  0x0,0x0,0x0,0x0,0x0,0x0,0x0,0x0,0x0,0x0,0x0,0x0,0x0,0x0,0x0,0x0,
  0x3f90,0x0,0x0,0x0,0x3f903f90,0x0,0x0,0x0,0x3f900000,0x0,0x3f90,0x0,0x0,0x0,0x3f903f90,0x0,
  0x0,0x0,0x0,0x0,0x0,0x0,0x0,0x0,0x0,0x0,0x0,0x0,0x0,0x0,0x0,0x0,
  0x0,0x0,0x0,0x0,0x0,0x3f90,0x0,0x0,0x0,0x3f90,0x0,0x0,0x0,0x0,0x0,0x3f90,
  0x0,0x0,0x0,0x0,0x0,0x3f90,0x0,0x0,0x0,0x3f90,0x0,0x0,0x0,0x0,0x0,0x3f90,
  0x0,0x8,0xa820,0xc00,0x40,0x48,0xa828,0xc10,0x90,0x98,0xa830,0xc20,0xd0,0xd8,0xa838,0xc30,
  0x120,0x128,0xa860,0xd80,0x160,0x168,0xa868,0xd90,0x1b0,0x1b8,0xa870,0xda0,0x1f0,0x1f8,0xa878,0xdb0,
  0x1000,0x2400,0xa8a0,0xf00,0x1040,0x2440,0xa8a8,0xf10,0x1090,0x2490,0xa8b0,0xf20,0x10d0,0x24d0,0xa8b8,0xf30,
  0x1120,0x2520,0xa8e0,0x1080,0x1160,0x2560,0xa8e8,0x1090,0x11b0,0x25b0,0xa8f0,0x10a0,0x11f0,0x25f0,0xa8f8,0x10b0,
  0x10,0x18,0xa900,0x1220,0x50,0x58,0xa908,0x1230,0x80,0x88,0xa910,0x1200,0xc0,0xc8,0xa918,0x1210,
  0x130,0x138,0xa940,0x13a0,0x170,0x178,0xa948,0x13b0,0x1a0,0x1a8,0xa950,0x1380,0x1e0,0x1e8,0xa958,0x1390,
  0x1010,0x2410,0xa980,0x1520,0x1050,0x2450,0xa988,0x1530,0x1080,0x2480,0xa990,0x1500,0x10c0,0x24c0,0xa998,0x1510,
  0x1130,0x2530,0xa9c0,0x16a0,0x1170,0x2570,0xa9c8,0x16b0,0x11a0,0x25a0,0xa9d0,0x1680,0x11e0,0x25e0,0xa9d8,0x1690,
  0x20,0x28,0xae20,0xc40,0x60,0x68,0xae28,0xc50,0xb0,0xb8,0xae30,0xc60,0xf0,0xf8,0xae38,0xc70,
  0x100,0x108,0xae60,0xdc0,0x140,0x148,0xae68,0xdd0,0x190,0x198,0xae70,0xde0,0x1d0,0x1d8,0xae78,0xdf0,
  0x1020,0x2420,0xaea0,0xf40,0x1060,0x2460,0xaea8,0xf50,0x10b0,0x24b0,0xaeb0,0xf60,0x10f0,0x24f0,0xaeb8,0xf70,
  0x1100,0x2500,0xaee0,0x10c0,0x1140,0x2540,0xaee8,0x10d0,0x1190,0x2590,0xaef0,0x10e0,0x11d0,0x25d0,0xaef8,0x10f0,
  0x30,0x38,0xaf00,0x1260,0x70,0x78,0xaf08,0x1270,0xa0,0xa8,0xaf10,0x1240,0xe0,0xe8,0xaf18,0x1250,
  0x110,0x118,0xaf40,0x13e0,0x150,0x158,0xaf48,0x13f0,0x180,0x188,0xaf50,0x13c0,0x1c0,0x1c8,0xaf58,0x13d0,
  0x1030,0x2430,0xaf80,0x1560,0x1070,0x2470,0xaf88,0x1570,0x10a0,0x24a0,0xaf90,0x1540,0x10e0,0x24e0,0xaf98,0x1550,
  0x1110,0x2510,0xafc0,0x16e0,0x1150,0x2550,0xafc8,0x16f0,0x1180,0x2580,0xafd0,0x16c0,0x11c0,0x25c0,0xafd8,0x16d0,
  0xc000,0xc020,0x2600,0xbebd0bd1,0xc008,0xc028,0x2640,0xbebd0bd1,0xc010,0xc030,0x2690,0xbebd0bd1,0xc018,0xc038,0x26d0,0xbebd0bd1,
  0xd800,0xd820,0x400,0x3ea80a81,0xd808,0xd828,0x440,0x3ea80a81,0xd810,0xd830,0x490,0x3ea80a81,0xd818,0xd838,0x4d0,0x3ea80a81,
  0xc120,0xc100,0x2800,0xbebd0bd1,0xc128,0xc108,0x2840,0xbebd0bd1,0xc130,0xc110,0x2890,0xbebd0bd1,0xc138,0xc118,0x28d0,0xbebd0bd1,
  0xd840,0xd860,0x2800,0xbebd0bd1,0xd848,0xd868,0x2840,0xbebd0bd1,0xd850,0xd870,0x2890,0xbebd0bd1,0xd858,0xd878,0x28d0,0xbebd0bd1,
  0xc080,0xc0a0,0x2610,0xbebd0bd1,0xc088,0xc0a8,0x2650,0xbebd0bd1,0xc090,0xc0b0,0x2680,0xbebd0bd1,0xc098,0xc0b8,0x26c0,0xbebd0bd1,
  0xc1a0,0xc180,0x410,0x3ea80a81,0xc1a8,0xc188,0x450,0x3ea80a81,0xc1b0,0xc190,0x480,0x3ea80a81,0xc1b8,0xc198,0x4c0,0x3ea80a81,
  0xd9a0,0xd980,0x2810,0xbebd0bd1,0xd9a8,0xd988,0x2850,0xbebd0bd1,0xd9b0,0xd990,0x2880,0xbebd0bd1,0xd9b8,0xd998,0x28c0,0xbebd0bd1,
  0xd9e0,0xd9c0,0x2810,0xbebd0bd1,0xd9e8,0xd9c8,0x2850,0xbebd0bd1,0xd9f0,0xd9d0,0x2880,0xbebd0bd1,0xd9f8,0xd9d8,0x28c0,0xbebd0bd1,
  0xc600,0xc620,0x2620,0xbebd0bd1,0xc608,0xc628,0x2660,0xbebd0bd1,0xc610,0xc630,0x26b0,0xbebd0bd1,0xc618,0xc638,0x26f0,0xbebd0bd1,
  0xde00,0xde20,0x420,0x3ea80a81,0xde08,0xde28,0x460,0x3ea80a81,0xde10,0xde30,0x4b0,0x3ea80a81,0xde18,0xde38,0x4f0,0x3ea80a81,
  0xd920,0xd900,0x2820,0xbebd0bd1,0xd928,0xd908,0x2860,0xbebd0bd1,0xd930,0xd910,0x28b0,0xbebd0bd1,0xd938,0xd918,0x28f0,0xbebd0bd1,
  0xd960,0xd940,0x2820,0xbebd0bd1,0xd968,0xd948,0x2860,0xbebd0bd1,0xd970,0xd950,0x28b0,0xbebd0bd1,0xd978,0xd958,0x28f0,0xbebd0bd1,
  0xd880,0xd8a0,0x2630,0xbebd0bd1,0xd888,0xd8a8,0x2670,0xbebd0bd1,0xd890,0xd8b0,0x26a0,0xbebd0bd1,0xd898,0xd8b8,0x26e0,0xbebd0bd1,
  0xeba0,0xeb80,0x430,0x3ea80a81,0xeba8,0xeb88,0x470,0x3ea80a81,0xebb0,0xeb90,0x4a0,0x3ea80a81,0xebb8,0xeb98,0x4e0,0x3ea80a81,
  0xd8c0,0xd8e0,0x2830,0xbebd0bd1,0xd8c8,0xd8e8,0x2870,0xbebd0bd1,0xd8d0,0xd8f0,0x28a0,0xbebd0bd1,0xd8d8,0xd8f8,0x28e0,0xbebd0bd1,
  0xc600,0xc620,0x2830,0xbebd0bd1,0xc608,0xc628,0x2870,0xbebd0bd1,0xc610,0xc630,0x28a0,0xbebd0bd1,0xc618,0xc638,0x28e0,0xbebd0bd1,
  0x3f90,0x3f90,0x0,0x0,0x0,0x3f90,0x3f90,0x0,0x0,0x0,0x3f90,0x3f90,0x0,0x0,0x0,0x3f90,
  0x0,0xc0420000,0x0,0x0,0x0,0x0,0x0,0x0,0x0,0x0,0x0,0xc0420000,0x0,0x0,0x0,0x0,
  0x3f900000,0x0,0x0,0x0,0x0,0x0,0x3f900000,0x0,0x0,0x0,0x3f900000,0x0,0x0,0x0,0x0,0x0,
  0x3f900000,0x0,0x0,0x0,0x0,0x0,0x3f900000,0x0,0x0,0x0,0x3f900000,0x0,0x0,0x0,0x0,0x0,
  0xc042,0x0,0x0,0x0,0xc0420000,0x0,0x0,0x0,0x0,0x0,0xc042,0x0,0x0,0x0,0xc0420000,0x0,
  0x0,0x0,0x0,0x0,0x0,0x3f900000,0x0,0x0,0x0,0x3f900000,0x0,0x0,0x0,0x0,0x0,0x3f900000,
  0x0,0x0,0x0,0x0,0x0,0xc042,0x0,0x0,0x0,0x0,0x0,0x0,0x0,0x0,0x0,0xc042,
  0x0,0x0,0x0,0x0,0x0,0xc042,0x0,0x0,0x0,0x0,0x0,0x0,0x0,0x0,0x0,0xc042,
  0x0,0x0,0x0,0x0,0x0,0x0,0x0,0x0,0x0,0x0,0x0,0x0,0x0,0x0,0x3f90,0x0,
  0x0,0x0,0x0,0x0,0x0,0xc0420000,0x0,0x0,0x0,0x0,0x0,0x0,0x0,0x0,0x0,0xc0420000,
  0x0,0x3f90,0x0,0x0,0x0,0x3f90,0x0,0x0,0x0,0x0,0x0,0x3f90,0x0,0x0,0x3f900000,0x3f90,
  0x0,0x3f90,0x0,0x0,0x0,0x3f90,0x0,0x0,0x0,0x0,0x0,0x3f90,0x0,0x0,0x3f900000,0x3f90,
  0x0,0x0,0x0,0x0,0x0,0x0,0x0,0x0,0x0,0x0,0x0,0x0,0x0,0x0,0x0,0x0,
  0x0,0x3f90,0x3f900000,0x0,0x0,0x3f90,0x0,0x0,0x0,0x0,0x0,0x3f90,0x0,0x0,0x0,0x3f90,
  0xc042,0x0,0x0,0x0,0x0,0x0,0x0,0x0,0x0,0x0,0xc042,0x0,0x0,0x0,0x0,0x0,
  0xc042,0x0,0x0,0x0,0x0,0x0,0x0,0x0,0x0,0x0,0xc042,0x0,0x0,0x0,0x0,0x0,
  0x0,0x1200,0x19200,0x1800,0x40,0x1240,0x19208,0x1810,0x90,0x1290,0x19210,0x1820,0xd0,0x12d0,0x19218,0x1830,
  0x120,0x1320,0x19240,0x1980,0x160,0x1360,0x19248,0x1990,0x1b0,0x13b0,0x19250,0x19a0,0x1f0,0x13f0,0x19258,0x19b0,
  0x1000,0x3608,0x19280,0x1b00,0x1040,0x3648,0x19288,0x1b10,0x1090,0x3698,0x19290,0x1b20,0x10d0,0x36d8,0x19298,0x1b30,
  0x1120,0x3728,0x192c0,0x1c80,0x1160,0x3768,0x192c8,0x1c90,0x11b0,0x37b8,0x192d0,0x1ca0,0x11f0,0x37f8,0x192d8,0x1cb0,
  0x10,0x1210,0x19320,0x1e20,0x50,0x1250,0x19328,0x1e30,0x80,0x1280,0x19330,0x1e00,0xc0,0x12c0,0x19338,0x1e10,
  0x130,0x1330,0x19360,0x1fa0,0x170,0x1370,0x19368,0x1fb0,0x1a0,0x13a0,0x19370,0x1f80,0x1e0,0x13e0,0x19378,0x1f90,
  0x1010,0x3618,0x193a0,0x2120,0x1050,0x3658,0x193a8,0x2130,0x1080,0x3688,0x193b0,0x2100,0x10c0,0x36c8,0x193b8,0x2110,
  0x1130,0x3738,0x193e0,0x22a0,0x1170,0x3778,0x193e8,0x22b0,0x11a0,0x37a8,0x193f0,0x2280,0x11e0,0x37e8,0x193f8,0x2290,
  0x20,0x1220,0x19800,0x1840,0x60,0x1260,0x19808,0x1850,0xb0,0x12b0,0x19810,0x1860,0xf0,0x12f0,0x19818,0x1870,
  0x100,0x1300,0x19840,0x19c0,0x140,0x1340,0x19848,0x19d0,0x190,0x1390,0x19850,0x19e0,0x1d0,0x13d0,0x19858,0x19f0,
  0x1020,0x3628,0x19880,0x1b40,0x1060,0x3668,0x19888,0x1b50,0x10b0,0x36b8,0x19890,0x1b60,0x10f0,0x36f8,0x19898,0x1b70,
  0x1100,0x3708,0x198c0,0x1cc0,0x1140,0x3748,0x198c8,0x1cd0,0x1190,0x3798,0x198d0,0x1ce0,0x11d0,0x37d8,0x198d8,0x1cf0,
  0x30,0x1230,0x19920,0x1e60,0x70,0x1270,0x19928,0x1e70,0xa0,0x12a0,0x19930,0x1e40,0xe0,0x12e0,0x19938,0x1e50,
  0x110,0x1310,0x19960,0x1fe0,0x150,0x1350,0x19968,0x1ff0,0x180,0x1380,0x19970,0x1fc0,0x1c0,0x13c0,0x19978,0x1fd0,
  0x1030,0x3638,0x199a0,0x2160,0x1070,0x3678,0x199a8,0x2170,0x10a0,0x36a8,0x199b0,0x2140,0x10e0,0x36e8,0x199b8,0x2150,
  0x1110,0x3718,0x199e0,0x22e0,0x1150,0x3758,0x199e8,0x22f0,0x1180,0x3788,0x199f0,0x22c0,0x11c0,0x37c8,0x199f8,0x22d0,
  0xcc00,0xcc20,0x2720,0xbebd0bd1,0xcc08,0xcc28,0x2760,0xbebd0bd1,0xcc10,0xcc30,0x27b0,0xbebd0bd1,0xcc18,0xcc38,0x27f0,0xbebd0bd1,
  0xcd20,0xcd00,0x520,0x3ea80a81,0xcd28,0xcd08,0x560,0x3ea80a81,0xcd30,0xcd10,0x5b0,0x3ea80a81,0xcd38,0xcd18,0x5f0,0x3ea80a81,
  0xe440,0xe460,0x2920,0xbebd0bd1,0xe448,0xe468,0x2960,0xbebd0bd1,0xe450,0xe470,0x29b0,0xbebd0bd1,0xe458,0xe478,0x29f0,0xbebd0bd1,
  0xeb60,0xeb40,0x2920,0xbebd0bd1,0xeb68,0xeb48,0x2960,0xbebd0bd1,0xeb70,0xeb50,0x29b0,0xbebd0bd1,0xeb78,0xeb58,0x29f0,0xbebd0bd1,
  0xe480,0xe4a0,0x2730,0xbebd0bd1,0xe488,0xe4a8,0x2770,0xbebd0bd1,0xe490,0xe4b0,0x27a0,0xbebd0bd1,0xe498,0xe4b8,0x27e0,0xbebd0bd1,
  0xe5a0,0xe580,0x530,0x3ea80a81,0xe5a8,0xe588,0x570,0x3ea80a81,0xe5b0,0xe590,0x5a0,0x3ea80a81,0xe5b8,0xe598,0x5e0,0x3ea80a81,
  0xe4c0,0xe4e0,0x2930,0xbebd0bd1,0xe4c8,0xe4e8,0x2970,0xbebd0bd1,0xe4d0,0xe4f0,0x29a0,0xbebd0bd1,0xe4d8,0xe4f8,0x29e0,0xbebd0bd1,
  0xd9e0,0xd9c0,0x2930,0xbebd0bd1,0xd9e8,0xd9c8,0x2970,0xbebd0bd1,0xd9f0,0xd9d0,0x29a0,0xbebd0bd1,0xd9f8,0xd9d8,0x29e0,0xbebd0bd1,
  0xe400,0xe420,0x2700,0xbebd0bd1,0xe408,0xe428,0x2740,0xbebd0bd1,0xe410,0xe430,0x2790,0xbebd0bd1,0xe418,0xe438,0x27d0,0xbebd0bd1,
  0xe520,0xe500,0x500,0x3ea80a81,0xe528,0xe508,0x540,0x3ea80a81,0xe530,0xe510,0x590,0x3ea80a81,0xe538,0xe518,0x5d0,0x3ea80a81,
  0xe560,0xe540,0x2900,0xbebd0bd1,0xe568,0xe548,0x2940,0xbebd0bd1,0xe570,0xe550,0x2990,0xbebd0bd1,0xe578,0xe558,0x29d0,0xbebd0bd1,
  0xcc80,0xcca0,0x2900,0xbebd0bd1,0xcc88,0xcca8,0x2940,0xbebd0bd1,0xcc90,0xccb0,0x2990,0xbebd0bd1,0xcc98,0xccb8,0x29d0,0xbebd0bd1,
  0xcda0,0xcd80,0x2710,0xbebd0bd1,0xcda8,0xcd88,0x2750,0xbebd0bd1,0xcdb0,0xcd90,0x2780,0xbebd0bd1,0xcdb8,0xcd98,0x27c0,0xbebd0bd1,
  0xeac0,0xeae0,0x510,0x3ea80a81,0xeac8,0xeae8,0x550,0x3ea80a81,0xead0,0xeaf0,0x580,0x3ea80a81,0xead8,0xeaf8,0x5c0,0x3ea80a81,
  0xe5e0,0xe5c0,0x2910,0xbebd0bd1,0xe5e8,0xe5c8,0x2950,0xbebd0bd1,0xe5f0,0xe5d0,0x2980,0xbebd0bd1,0xe5f8,0xe5d8,0x29c0,0xbebd0bd1,
  0xe400,0xe420,0x2910,0xbebd0bd1,0xe408,0xe428,0x2950,0xbebd0bd1,0xe410,0xe430,0x2980,0xbebd0bd1,0xe418,0xe438,0x29c0,0xbebd0bd1,
  0x3f903f90,0x0,0x0,0x0,0x3f900000,0x0,0x3f90,0x0,0x0,0x0,0x3f903f90,0x0,0x0,0x0,0x3f900000,0x0,
  0x0,0xc042,0x0,0x0,0x0,0x0,0x0,0x0,0x0,0x0,0x0,0xc042,0x0,0x0,0x0,0x0,
  0x0,0x0,0x0,0x0,0x0,0x0,0x0,0x0,0x0,0x0,0x0,0x0,0x0,0x0,0x0,0x3f900000,
  0x0,0x0,0x0,0x0,0x0,0x0,0x0,0x0,0x0,0x0,0x0,0x0,0x0,0x0,0x0,0x3f900000,
  0x0,0x0,0x0,0x0,0x0,0x0,0x0,0x0,0x0,0x0,0x0,0x0,0x0,0x0,0x0,0x0,
  0x0,0x3f90,0x0,0x3f900000,0x0,0x3f90,0x0,0x0,0x0,0x0,0x0,0x3f90,0x0,0x0,0x0,0x3f90,
  0xc042,0x0,0x0,0x0,0xc0420000,0x0,0x0,0x0,0x0,0x0,0xc042,0x0,0x0,0x0,0xc0420000,0x0,
  0xc042,0x0,0x0,0x0,0xc0420000,0x0,0x0,0x0,0x0,0x0,0xc042,0x0,0x0,0x0,0xc0420000,0x0,
  0x0,0xc0420000,0x0,0x0,0x0,0x0,0x0,0x0,0x0,0x0,0x0,0xc0420000,0x0,0x0,0x0,0x0,
  0x0,0x0,0x0,0x0,0x0,0xc042,0x0,0x0,0x0,0x0,0x0,0x0,0x0,0x0,0x0,0xc042,
  0x3f903f90,0x0,0x0,0x0,0x3f900000,0x0,0x3f90,0x0,0x0,0x0,0x3f903f90,0x0,0x0,0x0,0x3f900000,0x0,
  0x3f903f90,0x0,0x0,0x0,0x3f900000,0x0,0x3f90,0x0,0x0,0x0,0x3f903f90,0x0,0x0,0x0,0x3f900000,0x0,
  0x0,0x0,0x0,0x0,0xc042,0x0,0x0,0x0,0x0,0x0,0x0,0x0,0x0,0x0,0x3f90c042,0x0,
  0x0,0x0,0x0,0x0,0x0,0x3f90,0x0,0x0,0x0,0x3f90,0x0,0x0,0x0,0x0,0x0,0x3f90,
  0x0,0x0,0x0,0x0,0x0,0x0,0x0,0x0,0x0,0x0,0x0,0x0,0x0,0x0,0x0,0x0,
  0x0,0x0,0x0,0x0,0x0,0x0,0x0,0x0,0x0,0x0,0x0,0x0,0x0,0x0,0x0,0x0,
  0x0,0x1208,0x19220,0x2400,0x40,0x1248,0x19228,0x2410,0x90,0x1298,0x19230,0x2420,0xd0,0x12d8,0x19238,0x2430,
  0x120,0x1328,0x19260,0x2580,0x160,0x1368,0x19268,0x2590,0x1b0,0x13b8,0x19270,0x25a0,0x1f0,0x13f8,0x19278,0x25b0,
  0x1000,0x3600,0x192a0,0x2700,0x1040,0x3640,0x192a8,0x2710,0x1090,0x3690,0x192b0,0x2720,0x10d0,0x36d0,0x192b8,0x2730,
  0x1120,0x3720,0x192e0,0x2880,0x1160,0x3760,0x192e8,0x2890,0x11b0,0x37b0,0x192f0,0x28a0,0x11f0,0x37f0,0x192f8,0x28b0,
  0x10,0x1218,0x19300,0x2a20,0x50,0x1258,0x19308,0x2a30,0x80,0x1288,0x19310,0x2a00,0xc0,0x12c8,0x19318,0x2a10,
  0x130,0x1338,0x19340,0x2ba0,0x170,0x1378,0x19348,0x2bb0,0x1a0,0x13a8,0x19350,0x2b80,0x1e0,0x13e8,0x19358,0x2b90,
  0x1010,0x3610,0x19380,0x2d20,0x1050,0x3650,0x19388,0x2d30,0x1080,0x3680,0x19390,0x2d00,0x10c0,0x36c0,0x19398,0x2d10,
  0x1130,0x3730,0x193c0,0x2ea0,0x1170,0x3770,0x193c8,0x2eb0,0x11a0,0x37a0,0x193d0,0x2e80,0x11e0,0x37e0,0x193d8,0x2e90,
  0x20,0x1228,0x19820,0x2440,0x60,0x1268,0x19828,0x2450,0xb0,0x12b8,0x19830,0x2460,0xf0,0x12f8,0x19838,0x2470,
  0x100,0x1308,0x19860,0x25c0,0x140,0x1348,0x19868,0x25d0,0x190,0x1398,0x19870,0x25e0,0x1d0,0x13d8,0x19878,0x25f0,
  0x1020,0x3620,0x198a0,0x2740,0x1060,0x3660,0x198a8,0x2750,0x10b0,0x36b0,0x198b0,0x2760,0x10f0,0x36f0,0x198b8,0x2770,
  0x1100,0x3700,0x198e0,0x28c0,0x1140,0x3740,0x198e8,0x28d0,0x1190,0x3790,0x198f0,0x28e0,0x11d0,0x37d0,0x198f8,0x28f0,
  0x30,0x1238,0x19900,0x2a60,0x70,0x1278,0x19908,0x2a70,0xa0,0x12a8,0x19910,0x2a40,0xe0,0x12e8,0x19918,0x2a50,
  0x110,0x1318,0x19940,0x2be0,0x150,0x1358,0x19948,0x2bf0,0x180,0x1388,0x19950,0x2bc0,0x1c0,0x13c8,0x19958,0x2bd0,
  0x1030,0x3630,0x19980,0x2d60,0x1070,0x3670,0x19988,0x2d70,0x10a0,0x36a0,0x19990,0x2d40,0x10e0,0x36e0,0x19998,0x2d50,
  0x1110,0x3710,0x199c0,0x2ee0,0x1150,0x3750,0x199c8,0x2ef0,0x1180,0x3780,0x199d0,0x2ec0,0x11c0,0x37c0,0x199d8,0x2ed0,
  0x0,0x0,0x800,0x10000000,0x10,0x90,0x890,0x10000000,0x20,0x120,0x920,0x10000000,0x30,0x1b0,0x9b0,0x10000000,
  0x40,0x240,0xa40,0x20003180,0x50,0x2d0,0xad0,0x20003190,0x60,0x360,0xb60,0x200031a0,0x70,0x3f0,0xbf0,0x200031b0,
  0x610,0xc00,0x1400,0x40000000,0x600,0xc90,0x1490,0x40000010,0x630,0xd20,0x1520,0x40000020,0x620,0xdb0,0x15b0,0x40000030,
  0x650,0xe40,0x1640,0x20033180,0x640,0xed0,0x16d0,0x20033190,0x670,0xf60,0x1760,0x200331a0,0x660,0xff0,0x17f0,0x200331b0,
  0xc20,0x10,0x808,0x40030020,0xc30,0x80,0x898,0x40030030,0xc00,0x130,0x928,0x40030000,0xc10,0x1a0,0x9b8,0x40030010,
  0xc60,0x250,0xa48,0x200631a0,0xc70,0x2c0,0xad8,0x200631b0,0xc40,0x370,0xb68,0x20063180,0xc50,0x3e0,0xbf8,0x20063190,
  0x1230,0xc10,0x1408,0x40060020,0x1220,0xc80,0x1498,0x40060030,0x1210,0xd30,0x1528,0x40060000,0x1200,0xda0,0x15b8,0x40060010,
  0x1270,0xe50,0x1648,0x10000000,0x1260,0xec0,0x16d8,0x10000000,0x1250,0xf70,0x1768,0x10000000,0x1240,0xfe0,0x17f8,0x10000000,
  0x1840,0x20,0x810,0x10000000,0x1850,0xb0,0x880,0x10000000,0x1860,0x100,0x930,0x10000000,0x1870,0x190,0x9a0,0x10000000,
  0x1800,0x260,0xa50,0x200031c0,0x1810,0x2f0,0xac0,0x200031d0,0x1820,0x340,0xb70,0x200031e0,0x1830,0x3d0,0xbe0,0x200031f0,
  0x1e50,0xc20,0x1410,0x40000040,0x1e40,0xcb0,0x1480,0x40000050,0x1e70,0xd00,0x1530,0x40000060,0x1e60,0xd90,0x15a0,0x40000070,
  0x1e10,0xe60,0x1650,0x200331c0,0x1e00,0xef0,0x16c0,0x200331d0,0x1e30,0xf40,0x1770,0x200331e0,0x1e20,0xfd0,0x17e0,0x200331f0,
  0x2460,0x30,0x818,0x40030060,0x2470,0xa0,0x888,0x40030070,0x2440,0x110,0x938,0x40030040,0x2450,0x180,0x9a8,0x40030050,
  0x2420,0x270,0xa58,0x200631e0,0x2430,0x2e0,0xac8,0x200631f0,0x2400,0x350,0xb78,0x200631c0,0x2410,0x3c0,0xbe8,0x200631d0,
  0x2a70,0xc30,0x1418,0x40060060,0x2a60,0xca0,0x1488,0x40060070,0x2a50,0xd10,0x1538,0x40060040,0x2a40,0xd80,0x15a8,0x40060050,
  0x2a30,0xe70,0x1658,0x10000000,0x2a20,0xee0,0x16c8,0x10000000,0x2a10,0xf50,0x1778,0x10000000,0x2a00,0xfc0,0x17e8,0x10000000,
  0xbfd8bf90,0x3f403f00,0x0,0x0,0x0,0x0,0x0,0x0,0x0,0x0,0x0,0x0,0x0,0x0,0x0,0x0,
  0x0,0x0,0xbfd8bf90,0x3f403f00,0x0,0x0,0x0,0x0,0x0,0x0,0x0,0x0,0x0,0x0,0x0,0x0,
  0xbfd83f90,0x3f40bf00,0x0,0x0,0x0,0x0,0x0,0x0,0x0,0x0,0x0,0x0,0x0,0x0,0x0,0x0,
  0x0,0x0,0xbfd83f90,0x3f40bf00,0x0,0x0,0x0,0x0,0x0,0x0,0x0,0x0,0x0,0x0,0x0,0x0,
  0x0,0x0,0x0,0x0,0xbfd8bf90,0x3f403f00,0x0,0x0,0x0,0x0,0x0,0x0,0x0,0x0,0x0,0x0,
  0x0,0x0,0x0,0x0,0x0,0x0,0xbfd8bf90,0x3f403f00,0x0,0x0,0x0,0x0,0x0,0x0,0x0,0x0,
  0x0,0x0,0x0,0x0,0xbfd83f90,0x3f40bf00,0x0,0x0,0x0,0x0,0x0,0x0,0x0,0x0,0x0,0x0,
  0x0,0x0,0x0,0x0,0x0,0x0,0xbfd83f90,0x3f40bf00,0x0,0x0,0x0,0x0,0x0,0x0,0x0,0x0,
  0x0,0x0,0x0,0x0,0x0,0x0,0x0,0x0,0xbfd8bf90,0x3f403f00,0x0,0x0,0x0,0x0,0x0,0x0,
  0x0,0x0,0x0,0x0,0x0,0x0,0x0,0x0,0x0,0x0,0xbfd8bf90,0x3f403f00,0x0,0x0,0x0,0x0,
  0x0,0x0,0x0,0x0,0x0,0x0,0x0,0x0,0xbfd83f90,0x3f40bf00,0x0,0x0,0x0,0x0,0x0,0x0,
  0x0,0x0,0x0,0x0,0x0,0x0,0x0,0x0,0x0,0x0,0xbfd83f90,0x3f40bf00,0x0,0x0,0x0,0x0,
  0x0,0x0,0x0,0x0,0x0,0x0,0x0,0x0,0x0,0x0,0x0,0x0,0xbfd8bf90,0x3f403f00,0x0,0x0,
  0x0,0x0,0x0,0x0,0x0,0x0,0x0,0x0,0x0,0x0,0x0,0x0,0x0,0x0,0xbfd8bf90,0x3f403f00,
  0x0,0x0,0x0,0x0,0x0,0x0,0x0,0x0,0x0,0x0,0x0,0x0,0xbfd83f90,0x3f40bf00,0x0,0x0,
  0x0,0x0,0x0,0x0,0x0,0x0,0x0,0x0,0x0,0x0,0x0,0x0,0x0,0x0,0xbfd83f90,0x3f40bf00,
  0xa800,0xa820,0x0,0xa800,0xa808,0xa828,0x40,0xa840,0xa810,0xa830,0x90,0xa890,0xa818,0xa838,0xd0,0xa8d0,
  0xa920,0xa900,0x2400,0xae00,0xa928,0xa908,0x2440,0xae40,0xa930,0xa910,0x2490,0xae90,0xa938,0xa918,0x24d0,0xaed0,
  0xa840,0xa860,0x200,0xb400,0xa848,0xa868,0x240,0xb440,0xa850,0xa870,0x290,0xb490,0xa858,0xa878,0x2d0,0xb4d0,
  0xa960,0xa940,0x200,0xb400,0xa968,0xa948,0x240,0xb440,0xa970,0xa950,0x290,0xb490,0xa978,0xa958,0x2d0,0xb4d0,
  0xea80,0xeaa0,0x10,0xa810,0xea88,0xeaa8,0x50,0xa850,0xea90,0xeab0,0x80,0xa880,0xea98,0xeab8,0xc0,0xa8c0,
  0xa9a0,0xa980,0x2410,0xae10,0xa9a8,0xa988,0x2450,0xae50,0xa9b0,0xa990,0x2480,0xae80,0xa9b8,0xa998,0x24c0,0xaec0,
  0xc0c0,0xc0e0,0x210,0xb410,0xc0c8,0xc0e8,0x250,0xb450,0xc0d0,0xc0f0,0x280,0xb480,0xc0d8,0xc0f8,0x2c0,0xb4c0,
  0xc1e0,0xc1c0,0x210,0xb410,0xc1e8,0xc1c8,0x250,0xb450,0xc1f0,0xc1d0,0x280,0xb480,0xc1f8,0xc1d8,0x2c0,0xb4c0,
  0xae00,0xae20,0x20,0xa820,0xae08,0xae28,0x60,0xa860,0xae10,0xae30,0xb0,0xa8b0,0xae18,0xae38,0xf0,0xa8f0,
  0xeb20,0xeb00,0x2420,0xae20,0xeb28,0xeb08,0x2460,0xae60,0xeb30,0xeb10,0x24b0,0xaeb0,0xeb38,0xeb18,0x24f0,0xaef0,
  0xc040,0xc060,0x220,0xb420,0xc048,0xc068,0x260,0xb460,0xc050,0xc070,0x2b0,0xb4b0,0xc058,0xc078,0x2f0,0xb4f0,
  0xc160,0xc140,0x220,0xb420,0xc168,0xc148,0x260,0xb460,0xc170,0xc150,0x2b0,0xb4b0,0xc178,0xc158,0x2f0,0xb4f0,
  0xa880,0xa8a0,0x30,0xa830,0xa888,0xa8a8,0x70,0xa870,0xa890,0xa8b0,0xa0,0xa8a0,0xa898,0xa8b8,0xe0,0xa8e0,
  0xa8c0,0xa8e0,0x2430,0xae30,0xa8c8,0xa8e8,0x2470,0xae70,0xa8d0,0xa8f0,0x24a0,0xaea0,0xa8d8,0xa8f8,0x24e0,0xaee0,
  0xa9e0,0xa9c0,0x230,0xb430,0xa9e8,0xa9c8,0x270,0xb470,0xa9f0,0xa9d0,0x2a0,0xb4a0,0xa9f8,0xa9d8,0x2e0,0xb4e0,
  0xae00,0xae20,0x230,0xb430,0xae08,0xae28,0x270,0xb470,0xae10,0xae30,0x2a0,0xb4a0,0xae18,0xae38,0x2e0,0xb4e0,
  0x180000,0x40,0x820,0x10000000,0x180010,0xd0,0x8b0,0x10000000,0x180020,0x160,0x900,0x10000000,0x180030,0x1f0,0x990,0x10000000,
  0x180040,0x200,0xa60,0x20093180,0x180050,0x290,0xaf0,0x20093190,0x180060,0x320,0xb40,0x200931a0,0x180070,0x3b0,0xbd0,0x200931b0,
  0x180610,0xc40,0x1420,0x40090000,0x180600,0xcd0,0x14b0,0x40090010,0x180630,0xd60,0x1500,0x40090020,0x180620,0xdf0,0x1590,0x40090030,
  0x180650,0xe00,0x1660,0x200c3180,0x180640,0xe90,0x16f0,0x200c3190,0x180670,0xf20,0x1740,0x200c31a0,0x180660,0xfb0,0x17d0,0x200c31b0,
  0x180c20,0x50,0x828,0x400c0020,0x180c30,0xc0,0x8b8,0x400c0030,0x180c00,0x170,0x908,0x400c0000,0x180c10,0x1e0,0x998,0x400c0010,
  0x180c60,0x210,0xa68,0x200f31a0,0x180c70,0x280,0xaf8,0x200f31b0,0x180c40,0x330,0xb48,0x200f3180,0x180c50,0x3a0,0xbd8,0x200f3190,
  0x181230,0xc50,0x1428,0x400f0020,0x181220,0xcc0,0x14b8,0x400f0030,0x181210,0xd70,0x1508,0x400f0000,0x181200,0xde0,0x1598,0x400f0010,
  0x181270,0xe10,0x1668,0x10000000,0x181260,0xe80,0x16f8,0x10000000,0x181250,0xf30,0x1748,0x10000000,0x181240,0xfa0,0x17d8,0x10000000,
  0x181840,0x60,0x830,0x10000000,0x181850,0xf0,0x8a0,0x10000000,0x181860,0x140,0x910,0x10000000,0x181870,0x1d0,0x980,0x10000000,
  0x181800,0x220,0xa70,0x200931c0,0x181810,0x2b0,0xae0,0x200931d0,0x181820,0x300,0xb50,0x200931e0,0x181830,0x390,0xbc0,0x200931f0,
  0x181e50,0xc60,0x1430,0x40090040,0x181e40,0xcf0,0x14a0,0x40090050,0x181e70,0xd40,0x1510,0x40090060,0x181e60,0xdd0,0x1580,0x40090070,
  0x181e10,0xe20,0x1670,0x200c31c0,0x181e00,0xeb0,0x16e0,0x200c31d0,0x181e30,0xf00,0x1750,0x200c31e0,0x181e20,0xf90,0x17c0,0x200c31f0,
  0x182460,0x70,0x838,0x400c0060,0x182470,0xe0,0x8a8,0x400c0070,0x182440,0x150,0x918,0x400c0040,0x182450,0x1c0,0x988,0x400c0050,
  0x182420,0x230,0xa78,0x200f31e0,0x182430,0x2a0,0xae8,0x200f31f0,0x182400,0x310,0xb58,0x200f31c0,0x182410,0x380,0xbc8,0x200f31d0,
  0x182a70,0xc70,0x1438,0x400f0060,0x182a60,0xce0,0x14a8,0x400f0070,0x182a50,0xd50,0x1518,0x400f0040,0x182a40,0xdc0,0x1588,0x400f0050,
  0x182a30,0xe30,0x1678,0x10000000,0x182a20,0xea0,0x16e8,0x10000000,0x182a10,0xf10,0x1758,0x10000000,0x182a00,0xf80,0x17c8,0x10000000,
  0xbfd8bf90,0x3f403f00,0x0,0x0,0x0,0x0,0x0,0x0,0x0,0x0,0x0,0x0,0x0,0x0,0x0,0x0,
  0x0,0x0,0xbfd8bf90,0x3f403f00,0x0,0x0,0x0,0x0,0x0,0x0,0x0,0x0,0x0,0x0,0x0,0x0,
  0xbfd83f90,0x3f40bf00,0x0,0x0,0x0,0x0,0x0,0x0,0x0,0x0,0x0,0x0,0x0,0x0,0x0,0x0,
  0x0,0x0,0xbfd83f90,0x3f40bf00,0x0,0x0,0x0,0x0,0x0,0x0,0x0,0x0,0x0,0x0,0x0,0x0,
  0x0,0x0,0x0,0x0,0xbfd8bf90,0x3f403f00,0x0,0x0,0x0,0x0,0x0,0x0,0x0,0x0,0x0,0x0,
  0x0,0x0,0x0,0x0,0x0,0x0,0xbfd8bf90,0x3f403f00,0x0,0x0,0x0,0x0,0x0,0x0,0x0,0x0,
  0x0,0x0,0x0,0x0,0xbfd83f90,0x3f40bf00,0x0,0x0,0x0,0x0,0x0,0x0,0x0,0x0,0x0,0x0,
  0x0,0x0,0x0,0x0,0x0,0x0,0xbfd83f90,0x3f40bf00,0x0,0x0,0x0,0x0,0x0,0x0,0x0,0x0,
  0x0,0x0,0x0,0x0,0x0,0x0,0x0,0x0,0xbfd8bf90,0x3f403f00,0x0,0x0,0x0,0x0,0x0,0x0,
  0x0,0x0,0x0,0x0,0x0,0x0,0x0,0x0,0x0,0x0,0xbfd8bf90,0x3f403f00,0x0,0x0,0x0,0x0,
  0x0,0x0,0x0,0x0,0x0,0x0,0x0,0x0,0xbfd83f90,0x3f40bf00,0x0,0x0,0x0,0x0,0x0,0x0,
  0x0,0x0,0x0,0x0,0x0,0x0,0x0,0x0,0x0,0x0,0xbfd83f90,0x3f40bf00,0x0,0x0,0x0,0x0,
  0x0,0x0,0x0,0x0,0x0,0x0,0x0,0x0,0x0,0x0,0x0,0x0,0xbfd8bf90,0x3f403f00,0x0,0x0,
  0x0,0x0,0x0,0x0,0x0,0x0,0x0,0x0,0x0,0x0,0x0,0x0,0x0,0x0,0xbfd8bf90,0x3f403f00,
  0x0,0x0,0x0,0x0,0x0,0x0,0x0,0x0,0x0,0x0,0x0,0x0,0xbfd83f90,0x3f40bf00,0x0,0x0,
  0x0,0x0,0x0,0x0,0x0,0x0,0x0,0x0,0x0,0x0,0x0,0x0,0x0,0x0,0xbfd83f90,0x3f40bf00,
  0xb400,0xb420,0x120,0xa920,0xb408,0xb428,0x160,0xa960,0xb410,0xb430,0x1b0,0xa9b0,0xb418,0xb438,0x1f0,0xa9f0,
  0xb440,0xb460,0x2520,0xaf20,0xb448,0xb468,0x2560,0xaf60,0xb450,0xb470,0x25b0,0xafb0,0xb458,0xb478,0x25f0,0xaff0,
  0xcc40,0xcc60,0x320,0xb520,0xcc48,0xcc68,0x360,0xb560,0xcc50,0xcc70,0x3b0,0xb5b0,0xcc58,0xcc78,0x3f0,0xb5f0,
  0xcd60,0xcd40,0x320,0xb520,0xcd68,0xcd48,0x360,0xb560,0xcd70,0xcd50,0x3b0,0xb5b0,0xcd78,0xcd58,0x3f0,0xb5f0,
  0xb5a0,0xb580,0x130,0xa930,0xb5a8,0xb588,0x170,0xa970,0xb5b0,0xb590,0x1a0,0xa9a0,0xb5b8,0xb598,0x1e0,0xa9e0,
  0xccc0,0xcce0,0x2530,0xaf30,0xccc8,0xcce8,0x2570,0xaf70,0xccd0,0xccf0,0x25a0,0xafa0,0xccd8,0xccf8,0x25e0,0xafe0,
  0xa9e0,0xa9c0,0x330,0xb530,0xa9e8,0xa9c8,0x370,0xb570,0xa9f0,0xa9d0,0x3a0,0xb5a0,0xa9f8,0xa9d8,0x3e0,0xb5e0,
  0xc1e0,0xc1c0,0x330,0xb530,0xc1e8,0xc1c8,0x370,0xb570,0xc1f0,0xc1d0,0x3a0,0xb5a0,0xc1f8,0xc1d8,0x3e0,0xb5e0,
  0xb520,0xb500,0x100,0xa900,0xb528,0xb508,0x140,0xa940,0xb530,0xb510,0x190,0xa990,0xb538,0xb518,0x1d0,0xa9d0,
  0xea40,0xea60,0x2500,0xaf00,0xea48,0xea68,0x2540,0xaf40,0xea50,0xea70,0x2590,0xaf90,0xea58,0xea78,0x25d0,0xafd0,
  0xb560,0xb540,0x300,0xb500,0xb568,0xb548,0x340,0xb540,0xb570,0xb550,0x390,0xb590,0xb578,0xb558,0x3d0,0xb5d0,
  0xb480,0xb4a0,0x300,0xb500,0xb488,0xb4a8,0x340,0xb540,0xb490,0xb4b0,0x390,0xb590,0xb498,0xb4b8,0x3d0,0xb5d0,
  0xb4c0,0xb4e0,0x110,0xa910,0xb4c8,0xb4e8,0x150,0xa950,0xb4d0,0xb4f0,0x180,0xa980,0xb4d8,0xb4f8,0x1c0,0xa9c0,
  0xb5e0,0xb5c0,0x2510,0xaf10,0xb5e8,0xb5c8,0x2550,0xaf50,0xb5f0,0xb5d0,0x2580,0xaf80,0xb5f8,0xb5d8,0x25c0,0xafc0,
  0xcde0,0xcdc0,0x310,0xb510,0xcde8,0xcdc8,0x350,0xb550,0xcdf0,0xcdd0,0x380,0xb580,0xcdf8,0xcdd8,0x3c0,0xb5c0,
  0xb520,0xb500,0x310,0xb510,0xb528,0xb508,0x350,0xb550,0xb530,0xb510,0x380,0xb580,0xb538,0xb518,0x3c0,0xb5c0,
  0x0,0x400,0x840,0x10000000,0x10,0x490,0x8d0,0x10000000,0x20,0x520,0x960,0x10000000,0x30,0x5b0,0x9f0,0x10000000,
  0x40,0x640,0xa00,0x20123180,0x50,0x6d0,0xa90,0x20123190,0x60,0x760,0xb20,0x201231a0,0x70,0x7f0,0xbb0,0x201231b0,
  0x610,0x1000,0x1440,0x40120000,0x600,0x1090,0x14d0,0x40120010,0x630,0x1120,0x1560,0x40120020,0x620,0x11b0,0x15f0,0x40120030,
  0x650,0x1240,0x1600,0x20153180,0x640,0x12d0,0x1690,0x20153190,0x670,0x1360,0x1720,0x201531a0,0x660,0x13f0,0x17b0,0x201531b0,
  0xc20,0x410,0x848,0x40150020,0xc30,0x480,0x8d8,0x40150030,0xc00,0x530,0x968,0x40150000,0xc10,0x5a0,0x9f8,0x40150010,
  0xc60,0x650,0xa08,0x201831a0,0xc70,0x6c0,0xa98,0x201831b0,0xc40,0x770,0xb28,0x20183180,0xc50,0x7e0,0xbb8,0x20183190,
  0x1230,0x1010,0x1448,0x40180020,0x1220,0x1080,0x14d8,0x40180030,0x1210,0x1130,0x1568,0x40180000,0x1200,0x11a0,0x15f8,0x40180010,
  0x1270,0x1250,0x1608,0x10000000,0x1260,0x12c0,0x1698,0x10000000,0x1250,0x1370,0x1728,0x10000000,0x1240,0x13e0,0x17b8,0x10000000,
  0x1840,0x420,0x850,0x10000000,0x1850,0x4b0,0x8c0,0x10000000,0x1860,0x500,0x970,0x10000000,0x1870,0x590,0x9e0,0x10000000,
  0x1800,0x660,0xa10,0x201231c0,0x1810,0x6f0,0xa80,0x201231d0,0x1820,0x740,0xb30,0x201231e0,0x1830,0x7d0,0xba0,0x201231f0,
  0x1e50,0x1020,0x1450,0x40120040,0x1e40,0x10b0,0x14c0,0x40120050,0x1e70,0x1100,0x1570,0x40120060,0x1e60,0x1190,0x15e0,0x40120070,
  0x1e10,0x1260,0x1610,0x201531c0,0x1e00,0x12f0,0x1680,0x201531d0,0x1e30,0x1340,0x1730,0x201531e0,0x1e20,0x13d0,0x17a0,0x201531f0,
  0x2460,0x430,0x858,0x40150060,0x2470,0x4a0,0x8c8,0x40150070,0x2440,0x510,0x978,0x40150040,0x2450,0x580,0x9e8,0x40150050,
  0x2420,0x670,0xa18,0x201831e0,0x2430,0x6e0,0xa88,0x201831f0,0x2400,0x750,0xb38,0x201831c0,0x2410,0x7c0,0xba8,0x201831d0,
  0x2a70,0x1030,0x1458,0x40180060,0x2a60,0x10a0,0x14c8,0x40180070,0x2a50,0x1110,0x1578,0x40180040,0x2a40,0x1180,0x15e8,0x40180050,
  0x2a30,0x1270,0x1618,0x10000000,0x2a20,0x12e0,0x1688,0x10000000,0x2a10,0x1350,0x1738,0x10000000,0x2a00,0x13c0,0x17a8,0x10000000,
  0xbfd8bf90,0x3f403f00,0x0,0x0,0x0,0x0,0x0,0x0,0x0,0x0,0x0,0x0,0x0,0x0,0x0,0x0,
  0x0,0x0,0xbfd8bf90,0x3f403f00,0x0,0x0,0x0,0x0,0x0,0x0,0x0,0x0,0x0,0x0,0x0,0x0,
  0xbfd83f90,0x3f40bf00,0x0,0x0,0x0,0x0,0x0,0x0,0x0,0x0,0x0,0x0,0x0,0x0,0x0,0x0,
  0x0,0x0,0xbfd83f90,0x3f40bf00,0x0,0x0,0x0,0x0,0x0,0x0,0x0,0x0,0x0,0x0,0x0,0x0,
  0x0,0x0,0x0,0x0,0xbfd8bf90,0x3f403f00,0x0,0x0,0x0,0x0,0x0,0x0,0x0,0x0,0x0,0x0,
  0x0,0x0,0x0,0x0,0x0,0x0,0xbfd8bf90,0x3f403f00,0x0,0x0,0x0,0x0,0x0,0x0,0x0,0x0,
  0x0,0x0,0x0,0x0,0xbfd83f90,0x3f40bf00,0x0,0x0,0x0,0x0,0x0,0x0,0x0,0x0,0x0,0x0,
  0x0,0x0,0x0,0x0,0x0,0x0,0xbfd83f90,0x3f40bf00,0x0,0x0,0x0,0x0,0x0,0x0,0x0,0x0,
  0x0,0x0,0x0,0x0,0x0,0x0,0x0,0x0,0xbfd8bf90,0x3f403f00,0x0,0x0,0x0,0x0,0x0,0x0,
  0x0,0x0,0x0,0x0,0x0,0x0,0x0,0x0,0x0,0x0,0xbfd8bf90,0x3f403f00,0x0,0x0,0x0,0x0,
  0x0,0x0,0x0,0x0,0x0,0x0,0x0,0x0,0xbfd83f90,0x3f40bf00,0x0,0x0,0x0,0x0,0x0,0x0,
  0x0,0x0,0x0,0x0,0x0,0x0,0x0,0x0,0x0,0x0,0xbfd83f90,0x3f40bf00,0x0,0x0,0x0,0x0,
  0x0,0x0,0x0,0x0,0x0,0x0,0x0,0x0,0x0,0x0,0x0,0x0,0xbfd8bf90,0x3f403f00,0x0,0x0,
  0x0,0x0,0x0,0x0,0x0,0x0,0x0,0x0,0x0,0x0,0x0,0x0,0x0,0x0,0xbfd8bf90,0x3f403f00,
  0x0,0x0,0x0,0x0,0x0,0x0,0x0,0x0,0x0,0x0,0x0,0x0,0xbfd83f90,0x3f40bf00,0x0,0x0,
  0x0,0x0,0x0,0x0,0x0,0x0,0x0,0x0,0x0,0x0,0x0,0x0,0x0,0x0,0xbfd83f90,0x3f40bf00,
  0xc000,0xc020,0x2600,0xba00,0xc008,0xc028,0x2640,0xba40,0xc010,0xc030,0x2690,0xba90,0xc018,0xc038,0x26d0,0xbad0,
  0xd800,0xd820,0x400,0xc000,0xd808,0xd828,0x440,0xc040,0xd810,0xd830,0x490,0xc090,0xd818,0xd838,0x4d0,0xc0d0,
  0xc120,0xc100,0x2800,0xc600,0xc128,0xc108,0x2840,0xc640,0xc130,0xc110,0x2890,0xc690,0xc138,0xc118,0x28d0,0xc6d0,
  0xd840,0xd860,0x2800,0xc600,0xd848,0xd868,0x2840,0xc640,0xd850,0xd870,0x2890,0xc690,0xd858,0xd878,0x28d0,0xc6d0,
  0xc080,0xc0a0,0x2610,0xba10,0xc088,0xc0a8,0x2650,0xba50,0xc090,0xc0b0,0x2680,0xba80,0xc098,0xc0b8,0x26c0,0xbac0,
  0xc1a0,0xc180,0x410,0xc010,0xc1a8,0xc188,0x450,0xc050,0xc1b0,0xc190,0x480,0xc080,0xc1b8,0xc198,0x4c0,0xc0c0,
  0xd9a0,0xd980,0x2810,0xc610,0xd9a8,0xd988,0x2850,0xc650,0xd9b0,0xd990,0x2880,0xc680,0xd9b8,0xd998,0x28c0,0xc6c0,
  0xd9e0,0xd9c0,0x2810,0xc610,0xd9e8,0xd9c8,0x2850,0xc650,0xd9f0,0xd9d0,0x2880,0xc680,0xd9f8,0xd9d8,0x28c0,0xc6c0,
  0xc600,0xc620,0x2620,0xba20,0xc608,0xc628,0x2660,0xba60,0xc610,0xc630,0x26b0,0xbab0,0xc618,0xc638,0x26f0,0xbaf0,
  0xde00,0xde20,0x420,0xc020,0xde08,0xde28,0x460,0xc060,0xde10,0xde30,0x4b0,0xc0b0,0xde18,0xde38,0x4f0,0xc0f0,
  0xd920,0xd900,0x2820,0xc620,0xd928,0xd908,0x2860,0xc660,0xd930,0xd910,0x28b0,0xc6b0,0xd938,0xd918,0x28f0,0xc6f0,
  0xd960,0xd940,0x2820,0xc620,0xd968,0xd948,0x2860,0xc660,0xd970,0xd950,0x28b0,0xc6b0,0xd978,0xd958,0x28f0,0xc6f0,
  0xd880,0xd8a0,0x2630,0xba30,0xd888,0xd8a8,0x2670,0xba70,0xd890,0xd8b0,0x26a0,0xbaa0,0xd898,0xd8b8,0x26e0,0xbae0,
  0xeba0,0xeb80,0x430,0xc030,0xeba8,0xeb88,0x470,0xc070,0xebb0,0xeb90,0x4a0,0xc0a0,0xebb8,0xeb98,0x4e0,0xc0e0,
  0xd8c0,0xd8e0,0x2830,0xc630,0xd8c8,0xd8e8,0x2870,0xc670,0xd8d0,0xd8f0,0x28a0,0xc6a0,0xd8d8,0xd8f8,0x28e0,0xc6e0,
  0xc600,0xc620,0x2830,0xc630,0xc608,0xc628,0x2870,0xc670,0xc610,0xc630,0x28a0,0xc6a0,0xc618,0xc638,0x28e0,0xc6e0,
  0x180000,0x440,0x860,0x10000000,0x180010,0x4d0,0x8f0,0x10000000,0x180020,0x560,0x940,0x10000000,0x180030,0x5f0,0x9d0,0x10000000,
  0x180040,0x600,0xa20,0x201b3180,0x180050,0x690,0xab0,0x201b3190,0x180060,0x720,0xb00,0x201b31a0,0x180070,0x7b0,0xb90,0x201b31b0,
  0x180610,0x1040,0x1460,0x401b0000,0x180600,0x10d0,0x14f0,0x401b0010,0x180630,0x1160,0x1540,0x401b0020,0x180620,0x11f0,0x15d0,0x401b0030,
  0x180650,0x1200,0x1620,0x201e3180,0x180640,0x1290,0x16b0,0x201e3190,0x180670,0x1320,0x1700,0x201e31a0,0x180660,0x13b0,0x1790,0x201e31b0,
  0x180c20,0x450,0x868,0x401e0020,0x180c30,0x4c0,0x8f8,0x401e0030,0x180c00,0x570,0x948,0x401e0000,0x180c10,0x5e0,0x9d8,0x401e0010,
  0x180c60,0x610,0xa28,0x202131a0,0x180c70,0x680,0xab8,0x202131b0,0x180c40,0x730,0xb08,0x20213180,0x180c50,0x7a0,0xb98,0x20213190,
  0x181230,0x1050,0x1468,0x40210020,0x181220,0x10c0,0x14f8,0x40210030,0x181210,0x1170,0x1548,0x40210000,0x181200,0x11e0,0x15d8,0x40210010,
  0x181270,0x1210,0x1628,0x10000000,0x181260,0x1280,0x16b8,0x10000000,0x181250,0x1330,0x1708,0x10000000,0x181240,0x13a0,0x1798,0x10000000,
  0x181840,0x460,0x870,0x10000000,0x181850,0x4f0,0x8e0,0x10000000,0x181860,0x540,0x950,0x10000000,0x181870,0x5d0,0x9c0,0x10000000,
  0x181800,0x620,0xa30,0x201b31c0,0x181810,0x6b0,0xaa0,0x201b31d0,0x181820,0x700,0xb10,0x201b31e0,0x181830,0x790,0xb80,0x201b31f0,
  0x181e50,0x1060,0x1470,0x401b0040,0x181e40,0x10f0,0x14e0,0x401b0050,0x181e70,0x1140,0x1550,0x401b0060,0x181e60,0x11d0,0x15c0,0x401b0070,
  0x181e10,0x1220,0x1630,0x201e31c0,0x181e00,0x12b0,0x16a0,0x201e31d0,0x181e30,0x1300,0x1710,0x201e31e0,0x181e20,0x1390,0x1780,0x201e31f0,
  0x182460,0x470,0x878,0x401e0060,0x182470,0x4e0,0x8e8,0x401e0070,0x182440,0x550,0x958,0x401e0040,0x182450,0x5c0,0x9c8,0x401e0050,
  0x182420,0x630,0xa38,0x202131e0,0x182430,0x6a0,0xaa8,0x202131f0,0x182400,0x710,0xb18,0x202131c0,0x182410,0x780,0xb88,0x202131d0,
  0x182a70,0x1070,0x1478,0x40210060,0x182a60,0x10e0,0x14e8,0x40210070,0x182a50,0x1150,0x1558,0x40210040,0x182a40,0x11c0,0x15c8,0x40210050,
  0x182a30,0x1230,0x1638,0x10000000,0x182a20,0x12a0,0x16a8,0x10000000,0x182a10,0x1310,0x1718,0x10000000,0x182a00,0x1380,0x1788,0x10000000,
  0xbfd8bf90,0x3f403f00,0x0,0x0,0x0,0x0,0x0,0x0,0x0,0x0,0x0,0x0,0x0,0x0,0x0,0x0,
  0x0,0x0,0xbfd8bf90,0x3f403f00,0x0,0x0,0x0,0x0,0x0,0x0,0x0,0x0,0x0,0x0,0x0,0x0,
  0xbfd83f90,0x3f40bf00,0x0,0x0,0x0,0x0,0x0,0x0,0x0,0x0,0x0,0x0,0x0,0x0,0x0,0x0,
  0x0,0x0,0xbfd83f90,0x3f40bf00,0x0,0x0,0x0,0x0,0x0,0x0,0x0,0x0,0x0,0x0,0x0,0x0,
  0x0,0x0,0x0,0x0,0xbfd8bf90,0x3f403f00,0x0,0x0,0x0,0x0,0x0,0x0,0x0,0x0,0x0,0x0,
  0x0,0x0,0x0,0x0,0x0,0x0,0xbfd8bf90,0x3f403f00,0x0,0x0,0x0,0x0,0x0,0x0,0x0,0x0,
  0x0,0x0,0x0,0x0,0xbfd83f90,0x3f40bf00,0x0,0x0,0x0,0x0,0x0,0x0,0x0,0x0,0x0,0x0,
  0x0,0x0,0x0,0x0,0x0,0x0,0xbfd83f90,0x3f40bf00,0x0,0x0,0x0,0x0,0x0,0x0,0x0,0x0,
  0x0,0x0,0x0,0x0,0x0,0x0,0x0,0x0,0xbfd8bf90,0x3f403f00,0x0,0x0,0x0,0x0,0x0,0x0,
  0x0,0x0,0x0,0x0,0x0,0x0,0x0,0x0,0x0,0x0,0xbfd8bf90,0x3f403f00,0x0,0x0,0x0,0x0,
  0x0,0x0,0x0,0x0,0x0,0x0,0x0,0x0,0xbfd83f90,0x3f40bf00,0x0,0x0,0x0,0x0,0x0,0x0,
  0x0,0x0,0x0,0x0,0x0,0x0,0x0,0x0,0x0,0x0,0xbfd83f90,0x3f40bf00,0x0,0x0,0x0,0x0,
  0x0,0x0,0x0,0x0,0x0,0x0,0x0,0x0,0x0,0x0,0x0,0x0,0xbfd8bf90,0x3f403f00,0x0,0x0,
  0x0,0x0,0x0,0x0,0x0,0x0,0x0,0x0,0x0,0x0,0x0,0x0,0x0,0x0,0xbfd8bf90,0x3f403f00,
  0x0,0x0,0x0,0x0,0x0,0x0,0x0,0x0,0x0,0x0,0x0,0x0,0xbfd83f90,0x3f40bf00,0x0,0x0,
  0x0,0x0,0x0,0x0,0x0,0x0,0x0,0x0,0x0,0x0,0x0,0x0,0x0,0x0,0xbfd83f90,0x3f40bf00,
  0xcc00,0xcc20,0x2720,0xbb20,0xcc08,0xcc28,0x2760,0xbb60,0xcc10,0xcc30,0x27b0,0xbbb0,0xcc18,0xcc38,0x27f0,0xbbf0,
  0xcd20,0xcd00,0x520,0xc120,0xcd28,0xcd08,0x560,0xc160,0xcd30,0xcd10,0x5b0,0xc1b0,0xcd38,0xcd18,0x5f0,0xc1f0,
  0xe440,0xe460,0x2920,0xc720,0xe448,0xe468,0x2960,0xc760,0xe450,0xe470,0x29b0,0xc7b0,0xe458,0xe478,0x29f0,0xc7f0,
  0xeb60,0xeb40,0x2920,0xc720,0xeb68,0xeb48,0x2960,0xc760,0xeb70,0xeb50,0x29b0,0xc7b0,0xeb78,0xeb58,0x29f0,0xc7f0,
  0xe480,0xe4a0,0x2730,0xbb30,0xe488,0xe4a8,0x2770,0xbb70,0xe490,0xe4b0,0x27a0,0xbba0,0xe498,0xe4b8,0x27e0,0xbbe0,
  0xe5a0,0xe580,0x530,0xc130,0xe5a8,0xe588,0x570,0xc170,0xe5b0,0xe590,0x5a0,0xc1a0,0xe5b8,0xe598,0x5e0,0xc1e0,
  0xe4c0,0xe4e0,0x2930,0xc730,0xe4c8,0xe4e8,0x2970,0xc770,0xe4d0,0xe4f0,0x29a0,0xc7a0,0xe4d8,0xe4f8,0x29e0,0xc7e0,
  0xd9e0,0xd9c0,0x2930,0xc730,0xd9e8,0xd9c8,0x2970,0xc770,0xd9f0,0xd9d0,0x29a0,0xc7a0,0xd9f8,0xd9d8,0x29e0,0xc7e0,
  0xe400,0xe420,0x2700,0xbb00,0xe408,0xe428,0x2740,0xbb40,0xe410,0xe430,0x2790,0xbb90,0xe418,0xe438,0x27d0,0xbbd0,
  0xe520,0xe500,0x500,0xc100,0xe528,0xe508,0x540,0xc140,0xe530,0xe510,0x590,0xc190,0xe538,0xe518,0x5d0,0xc1d0,
  0xe560,0xe540,0x2900,0xc700,0xe568,0xe548,0x2940,0xc740,0xe570,0xe550,0x2990,0xc790,0xe578,0xe558,0x29d0,0xc7d0,
  0xcc80,0xcca0,0x2900,0xc700,0xcc88,0xcca8,0x2940,0xc740,0xcc90,0xccb0,0x2990,0xc790,0xcc98,0xccb8,0x29d0,0xc7d0,
  0xcda0,0xcd80,0x2710,0xbb10,0xcda8,0xcd88,0x2750,0xbb50,0xcdb0,0xcd90,0x2780,0xbb80,0xcdb8,0xcd98,0x27c0,0xbbc0,
  0xeac0,0xeae0,0x510,0xc110,0xeac8,0xeae8,0x550,0xc150,0xead0,0xeaf0,0x580,0xc180,0xead8,0xeaf8,0x5c0,0xc1c0,
  0xe5e0,0xe5c0,0x2910,0xc710,0xe5e8,0xe5c8,0x2950,0xc750,0xe5f0,0xe5d0,0x2980,0xc780,0xe5f8,0xe5d8,0x29c0,0xc7c0,
  0xe400,0xe420,0x2910,0xc710,0xe408,0xe428,0x2950,0xc750,0xe410,0xe430,0x2980,0xc780,0xe418,0xe438,0x29c0,0xc7c0,
  0x0,0x4800,0x0,0x0,0x10,0x4802,0x180,0x0,0x20,0x4804,0x300,0x0,0x30,0x4806,0x480,0x0,
  0x40,0x4808,0x1800,0x0,0x50,0x480a,0x1980,0x0,0x60,0x480c,0x1b00,0x0,0x70,0x480e,0x1c80,0x0,
  0x9000,0x4810,0x2,0x0,0x9010,0x4812,0x182,0x0,0x9020,0x4814,0x302,0x0,0x9030,0x4816,0x482,0x0,
  0x9040,0x4818,0x1802,0x0,0x9050,0x481a,0x1982,0x0,0x9060,0x481c,0x1b02,0x0,0x9070,0x481e,0x1c82,0x0,
  0x12000,0x4820,0x4,0x0,0x12010,0x4822,0x184,0x0,0x12020,0x4824,0x304,0x0,0x12030,0x4826,0x484,0x0,
  0x12040,0x4828,0x1804,0x0,0x12050,0x482a,0x1984,0x0,0x12060,0x482c,0x1b04,0x0,0x12070,0x482e,0x1c84,0x0,
  0x1b000,0x4830,0x6,0x0,0x1b010,0x4832,0x186,0x0,0x1b020,0x4834,0x306,0x0,0x1b030,0x4836,0x486,0x0,
  0x1b040,0x4838,0x1806,0x0,0x1b050,0x483a,0x1986,0x0,0x1b060,0x483c,0x1b06,0x0,0x1b070,0x483e,0x1c86,0x0,
  0x24000,0x4840,0x8,0x8,0x24010,0x4842,0x188,0x8,0x24020,0x4844,0x308,0x8,0x24030,0x4846,0x488,0x8,
  0x24040,0x4848,0x1808,0x8,0x24050,0x484a,0x1988,0x8,0x24060,0x484c,0x1b08,0x8,0x24070,0x484e,0x1c88,0x8,
  0x2d000,0x4850,0xa,0x8,0x2d010,0x4852,0x18a,0x8,0x2d020,0x4854,0x30a,0x8,0x2d030,0x4856,0x48a,0x8,
  0x2d040,0x4858,0x180a,0x8,0x2d050,0x485a,0x198a,0x8,0x2d060,0x485c,0x1b0a,0x8,0x2d070,0x485e,0x1c8a,0x8,
  0x36000,0x4860,0xc,0x8,0x36010,0x4862,0x18c,0x8,0x36020,0x4864,0x30c,0x8,0x36030,0x4866,0x48c,0x8,
  0x36040,0x4868,0x180c,0x8,0x36050,0x486a,0x198c,0x8,0x36060,0x486c,0x1b0c,0x8,0x36070,0x486e,0x1c8c,0x8,
  0x3f000,0x4870,0xe,0x8,0x3f010,0x4872,0x18e,0x8,0x3f020,0x4874,0x30e,0x8,0x3f030,0x4876,0x48e,0x8,
  0x3f040,0x4878,0x180e,0x8,0x3f050,0x487a,0x198e,0x8,0x3f060,0x487c,0x1b0e,0x8,0x3f070,0x487e,0x1c8e,0x8,
};

__global__ __launch_bounds__(256, 1) void wino_main_kernel(const char* __restrict__ x, char* __restrict__ Uq, char* __restrict__ out, const char* __restrict__ wt) {
  __shared__ __attribute__((aligned(1024))) char smem[163840];
  asm volatile("s_getpc_b64 s[40:41]\n\tLwino_pc:\n\ts_bfe_u32 s42, %0, 0x50003\n\ts_lshl_b32 s42, s42, 12\n\tv_lshl_add_u32 v203, %1, 4, s42\n\t"
               "v_min_u32 v203, (Lwino_end-Lwino_pc-16)&-16, v203\n\tglobal_load_dwordx4 v[100:103], v203, s[40:41]"
               :: "s"(blockIdx.x), "v"(threadIdx.x) : "s40", "s41", "s42", "v100", "v101", "v102", "v103", "v203", "scc", "memory");
  const int lane = threadIdx.x & 63;
  const int w = __builtin_amdgcn_readfirstlane(threadIdx.x >> 6);
  const int sub = blockIdx.x >> 3, bx = sub & 15, by = 4 * (blockIdx.x & 7) + (sub >> 4);
  const unsigned long long px = (unsigned long long)x, pu = (unsigned long long)Uq, py = (unsigned long long)out, pw = (unsigned long long)wt;
  const int xbase = ((16 * by - 1) * IMG_W + 32 * bx) * PXB;
  const int ybase = (16 * by * IMG_W + 32 * bx) * PXB;
  const unsigned medge = (bx == 0 ? 0xffffffffu : 0xbfffffffu) & (bx == 15 ? 0xffffffffu : 0xdfffffffu);
  unsigned fso, fuo, fs8, fln;
  {
    const unsigned p = blockIdx.x - 1, hb = p & 1, q = (p >> 1) & 127, r = q / 3, kb = q - 3 * r, wv = r & 3, chunk = r >> 2;
    fs8 = 1 - (kb >> 1);
    fso = p < 144 ? chunk * 12288 + hb * 3072 + (w >> 1) * 1536 + (kb < 2 ? kb * 8 : 256) + ((2 * wv + (w & 1)) << (4 + fs8)) : 0x40000000u;
    fuo = p < 144 ? (chunk * 24 + kb) * 12288 + wv * 3072 + hb * 512 + w * 128 : 0x40000000u;
    fln = chunk < 2 ? 64 : 48;
  }
  const unsigned fwo = (((lane & 3) + 4 * (lane & 4)) * CH + 4 * (lane >> 5) + ((lane >> 3) & 3)) * 2;
  const unsigned flw = 18432 + w * 3072 + lane * 2, fst = lane < fln ? (lane >> 3) * 36864 + (lane & 7) * 16 : 0x40000000u;
  unsigned dummy = 0;
  asm volatile(
"s_mov_b32 s52, %15\n\t"
"s_and_b32 s53, %16, 0xffff\n\t"
"s_mov_b32 s54, 663552\n\t"
"s_mov_b32 s55, 0x20000\n\t"
"s_mov_b32 s56, %17\n\t"
"s_mov_b32 s57, %18\n\t"
"s_mov_b32 s74, %21\n\t"
"s_mul_i32 s66, %11, 4\n\t"
"s_mov_b32 s73, %9\n\t"
"s_or_b32 s72, %11, %9\n\t"
"v_mov_b32 v32, 0\n\t"
"v_mov_b32 v33, 0\n\t"
"v_mov_b32 v34, 0\n\t"
"v_mov_b32 v35, 0\n\t"
"v_mov_b32 v36, 0\n\t"
"v_mov_b32 v37, 0\n\t"
"v_mov_b32 v38, 0\n\t"
"v_mov_b32 v39, 0\n\t"
"v_mov_b32 v40, 0\n\t"
"v_mov_b32 v118, %22\n\t"
"v_mov_b32 v42, %23\n\t"
"v_add_u32 v43, 18432, v252\n\t"
"v_mov_b32 v44, %24\n\t"
"v_mov_b32 v119, %25\n\t"
"s_mov_b32 s8, %1\n\t"
"s_and_b32 s9, %2, 0xffff\n\t"
"s_mov_b32 s10, 0x200000\n\t"
"s_mov_b32 s11, 0x20000\n\t"
"s_mov_b32 s12, %3\n\t"
"s_and_b32 s13, %4, 0xffff\n\t"
"s_mov_b32 s14, 100663296\n\t"
"s_mov_b32 s15, 0x20000\n\t"
"s_mov_b32 s16, %5\n\t"
"s_and_b32 s17, %6, 0xffff\n\t"
"s_mov_b32 s18, 100663296\n\t"
"s_mov_b32 s19, 0x20000\n\t"
"s_mov_b32 s21, %7\n\t"
"s_lshr_b32 s60, %9, 1\n\t"
"s_mul_i32 s61, s60, 384\n\t"
"s_add_u32 s22, %8, s61\n\t"
"s_mul_i32 s61, s60, 196608\n\t"
"s_add_u32 s23, %8, s61\n\t"
"s_mul_i32 s24, %9, 1536\n\t"
"s_add_u32 s24, s24, 43008\n\t"
"s_mul_i32 s25, %9, 19968\n\t"
"s_add_u32 s25, s25, 61440\n\t"
"s_mul_i32 s26, s60, 19968\n\t"
"s_add_u32 s26, s26, 141312\n\t"
"s_sub_u32 s61, s60, %9\n\t"
"s_add_u32 s61, s61, 4\n\t"
"s_mul_i32 s27, s61, 589824\n\t"
"s_bitcmp1_b32 %9, 0\n\t"
"s_mov_b32 s36, 0x0fff0000\n\t"
"s_cselect_b32 s36, 0xfff0, s36\n\t"
"s_mov_b32 s37, s36\n\t"
"s_mov_b32 s44, 0x0fff0fff\n\t"
"s_mov_b32 s45, 0x0fff0fff\n\t"
"s_mov_b64 s[46:47], %12\n\t"
"s_add_u32 s48, s46, 12288\n\t"
"s_addc_u32 s49, s47, 0\n\t"
"s_mul_i32 s50, %9, 122880\n\t"
"s_lshl_b32 s51, s60, 30\n\t"
"s_add_u32 s51, s51, s50\n\t"
"s_mov_b32 s40, %14\n\t"
"s_sub_u32 s42, %7, 384\n\t"
"s_mov_b32 s38, 0x3f2aaaab\n\t"
"s_mov_b32 s39, 0x3fc00000\n\t"
"s_mov_b32 s41, 0x3ee38e39\n\t"
"s_mov_b32 s43, 0x40100000\n\t"
"s_nop 4\n\t"
"s_branch Lpro_%=\n\t"
"Lfb_%=:\n\t"
"s_waitcnt vmcnt(0) lgkmcnt(0)\n\t"
"s_nop 15\n\t"
"v_lshrrev_b32 v255, 9, v252\n\t"
"v_lshl_add_u32 v255, v255, 10, v252\n\t"
"v_add_u32 v255, 44032, v255\n\t"
"ds_write_b128 v255, v[32:35] offset:0\n\t"
"ds_write_b128 v255, v[36:39] offset:3072\n\t"
"ds_write_b128 v255, v[40:43] offset:6144\n\t"
"ds_write_b128 v255, v[44:47] offset:9216\n\t"
"ds_write_b128 v255, v[48:51] offset:12288\n\t"
"ds_write_b128 v255, v[52:55] offset:15360\n\t"
"ds_write_b128 v255, v[56:59] offset:18432\n\t"
"ds_write_b128 v255, v[60:63] offset:21504\n\t"
"ds_write_b128 v255, v[64:67] offset:24576\n\t"
"ds_write_b128 v255, v[68:71] offset:27648\n\t"
"ds_write_b128 v255, v[72:75] offset:30720\n\t"
"ds_write_b128 v255, v[76:79] offset:33792\n\t"
"ds_write_b128 v255, v[80:83] offset:36864\n\t"
"ds_write_b128 v255, v[84:87] offset:39936\n\t"
"ds_write_b128 v255, v[88:91] offset:43008\n\t"
"ds_write_b128 v255, v[92:95] offset:46080\n\t"
"ds_write_b128 v255, v[96:99] offset:49152\n\t"
"ds_write_b128 v255, v[100:103] offset:52224\n\t"
"ds_write_b128 v255, v[104:107] offset:55296\n\t"
"ds_write_b128 v255, v[108:111] offset:58368\n\t"
"ds_write_b128 v255, v[112:115] offset:61440\n\t"
"ds_write_b128 v255, v[116:119] offset:64512\n\t"
"s_waitcnt lgkmcnt(0)\n\t"
"s_add_u32 s60, s48, 12288\n\t"
"s_addc_u32 s61, s49, 0\n\t"
"global_load_dwordx4 v[116:119], v252, s[60:61]\n\t"
"v_mov_b32 v32, 0\n\t"
"v_mov_b32 v33, 0\n\t"
"v_mov_b32 v34, 0\n\t"
"v_mov_b32 v35, 0\n\t"
"v_mov_b32 v36, 0\n\t"
"v_mov_b32 v37, 0\n\t"
"v_mov_b32 v38, 0\n\t"
"v_mov_b32 v39, 0\n\t"
"v_mov_b32 v40, 0\n\t"
"v_add_u32 v43, 18432, v252\n\t"
"s_waitcnt vmcnt(0)\n\t"
"v_mov_b32 v44, v116\n\t"
"v_mov_b32 v42, v117\n\t"
"v_xor_b32 v45, 8, v42\n\t"
"s_mov_b32 s69, 0\n\t"
"Lfb1_%=:\n\t"
"s_lshr_b32 s71, s69, 2\n\t"
"s_and_b32 s70, s69, 3\n\t"
"s_and_b32 s60, s71, 1\n\t"
"s_lshr_b32 s61, s71, 1\n\t"
"s_mul_i32 s62, s61, 171\n\t"
"s_lshr_b32 s62, s62, 9\n\t"
"s_mul_i32 s63, s62, 3\n\t"
"s_sub_u32 s61, s61, s63\n\t"
"s_and_b32 s63, s62, 3\n\t"
"s_lshr_b32 s62, s62, 2\n\t"
"s_mul_i32 s56, s62, 12288\n\t"
"s_mul_i32 s64, s60, 3072\n\t"
"s_add_u32 s56, s56, s64\n\t"
"s_lshr_b32 s64, s70, 1\n\t"
"s_mul_i32 s64, s64, 1536\n\t"
"s_add_u32 s56, s56, s64\n\t"
"s_and_b32 s64, s70, 1\n\t"
"s_lshl1_add_u32 s64, s63, s64\n\t"
"s_lshl_b32 s65, s61, 3\n\t"
"s_cmp_lt_u32 s61, 2\n\t"
"s_cselect_b32 s74, 1, 0\n\t"
"s_cselect_b32 s65, s65, 0x100\n\t"
"s_add_u32 s56, s56, s65\n\t"
"s_add_u32 s65, s74, 4\n\t"
"s_lshl_b32 s64, s64, s65\n\t"
"s_add_u32 s56, s56, s64\n\t"
"s_mul_i32 s57, s62, 24\n\t"
"s_add_u32 s57, s57, s61\n\t"
"s_mul_i32 s57, s57, 12288\n\t"
"s_mul_i32 s64, s63, 3072\n\t"
"s_add_u32 s57, s57, s64\n\t"
"s_lshl_b32 s64, s60, 9\n\t"
"s_add_u32 s57, s57, s64\n\t"
"s_lshl_b32 s64, s70, 7\n\t"
"s_add_u32 s57, s57, s64\n\t"
"v_mad_u32_u24 v41, v119, s74, v118\n\t"
"s_add_u32 s60, s56, 0\n\t"
"buffer_load_short_d16_hi v32, v41, s[52:55], s60 offen\n\t"
"s_add_u32 s60, s56, 73728\n\t"
"buffer_load_short_d16_hi v33, v41, s[52:55], s60 offen\n\t"
"s_add_u32 s60, s56, 147456\n\t"
"buffer_load_short_d16_hi v34, v41, s[52:55], s60 offen\n\t"
"s_add_u32 s60, s56, 221184\n\t"
"buffer_load_short_d16_hi v35, v41, s[52:55], s60 offen\n\t"
"s_add_u32 s60, s56, 294912\n\t"
"buffer_load_short_d16_hi v36, v41, s[52:55], s60 offen\n\t"
"s_add_u32 s60, s56, 368640\n\t"
"buffer_load_short_d16_hi v37, v41, s[52:55], s60 offen\n\t"
"s_add_u32 s60, s56, 442368\n\t"
"buffer_load_short_d16_hi v38, v41, s[52:55], s60 offen\n\t"
"s_add_u32 s60, s56, 516096\n\t"
"buffer_load_short_d16_hi v39, v41, s[52:55], s60 offen\n\t"
"s_add_u32 s60, s56, 589824\n\t"
"buffer_load_short_d16_hi v40, v41, s[52:55], s60 offen\n\t"
"s_waitcnt vmcnt(6)\n\t"
"v_fma_f32 v48, v34, s41, v32\n\t"
"v_fma_f32 v51, v34, s43, v32\n\t"
"s_waitcnt vmcnt(3)\n\t"
"v_fma_f32 v49, v37, s41, v35\n\t"
"v_fma_f32 v52, v37, s43, v35\n\t"
"s_waitcnt vmcnt(0)\n\t"
"v_fma_f32 v50, v40, s41, v38\n\t"
"v_fma_f32 v53, v40, s43, v38\n\t"
"v_fma_f32 v54, v33, s38, v48\n\t"
"v_fma_f32 v57, -v33, s38, v48\n\t"
"v_fma_f32 v60, v33, s39, v51\n\t"
"v_fma_f32 v63, -v33, s39, v51\n\t"
"v_fma_f32 v55, v36, s38, v49\n\t"
"v_fma_f32 v58, -v36, s38, v49\n\t"
"v_fma_f32 v61, v36, s39, v52\n\t"
"v_fma_f32 v64, -v36, s39, v52\n\t"
"v_fma_f32 v56, v39, s38, v50\n\t"
"v_fma_f32 v59, -v39, s38, v50\n\t"
"v_fma_f32 v62, v39, s39, v53\n\t"
"v_fma_f32 v65, -v39, s39, v53\n\t"
"v_mul_f32 v70, 0x3f638e39, v32\n\t"
"v_mul_f32 v71, 0x3f638e39, v35\n\t"
"v_mul_f32 v72, 0x3f638e39, v38\n\t"
"v_mul_f32 v73, 0xbf54ad4b, v54\n\t"
"v_mul_f32 v74, 0xbf54ad4b, v55\n\t"
"v_mul_f32 v75, 0xbf54ad4b, v56\n\t"
"v_mul_f32 v76, 0xbefc0fc1, v57\n\t"
"v_mul_f32 v77, 0xbefc0fc1, v58\n\t"
"v_mul_f32 v78, 0xbefc0fc1, v59\n\t"
"v_mul_f32 v79, 0xbe7c0fc1, v60\n\t"
"v_mul_f32 v80, 0xbe7c0fc1, v61\n\t"
"v_mul_f32 v81, 0xbe7c0fc1, v62\n\t"
"v_mul_f32 v82, 0x3ea80a81, v63\n\t"
"v_mul_f32 v83, 0x3ea80a81, v64\n\t"
"v_mul_f32 v84, 0x3ea80a81, v65\n\t"
"v_mul_f32 v85, 0x3f638e39, v34\n\t"
"v_mul_f32 v86, 0x3f638e39, v37\n\t"
"v_mul_f32 v87, 0x3f638e39, v40\n\t"
"v_mul_f32 v88, 0x3ea80a81, v32\n\t"
"v_mul_f32 v89, 0x3ea80a81, v35\n\t"
"v_mul_f32 v90, 0x3ea80a81, v38\n\t"
"v_mul_f32 v91, 0xbebd0bd1, v34\n\t"
"v_mul_f32 v92, 0xbebd0bd1, v37\n\t"
"v_mul_f32 v93, 0xbebd0bd1, v40\n\t"
"v_cvt_pk_bf16_f32 v70, v70, v71\n\t"
"v_cvt_pk_bf16_f32 v72, v72, v73\n\t"
"v_cvt_pk_bf16_f32 v74, v74, v75\n\t"
"v_cvt_pk_bf16_f32 v76, v76, v77\n\t"
"v_cvt_pk_bf16_f32 v78, v78, v79\n\t"
"v_cvt_pk_bf16_f32 v80, v80, v81\n\t"
"v_cvt_pk_bf16_f32 v82, v82, v83\n\t"
"v_cvt_pk_bf16_f32 v84, v84, v85\n\t"
"v_cvt_pk_bf16_f32 v86, v86, v87\n\t"
"v_cvt_pk_bf16_f32 v88, v88, v89\n\t"
"v_cvt_pk_bf16_f32 v90, v90, v91\n\t"
"v_cvt_pk_bf16_f32 v92, v92, v93\n\t"
"ds_write_b16 v42, v70 offset:0\n\t"
"ds_write_b16_d16_hi v42, v70 offset:1024\n\t"
"ds_write_b16 v42, v72 offset:2048\n\t"
"ds_write_b16_d16_hi v42, v72 offset:128\n\t"
"ds_write_b16 v42, v74 offset:1152\n\t"
"ds_write_b16_d16_hi v42, v74 offset:2176\n\t"
"ds_write_b16 v45, v76 offset:256\n\t"
"ds_write_b16_d16_hi v45, v76 offset:1280\n\t"
"ds_write_b16 v45, v78 offset:2304\n\t"
"ds_write_b16_d16_hi v42, v78 offset:384\n\t"
"ds_write_b16 v42, v80 offset:1408\n\t"
"ds_write_b16_d16_hi v42, v80 offset:2432\n\t"
"ds_write_b16 v45, v82 offset:512\n\t"
"ds_write_b16_d16_hi v45, v82 offset:1536\n\t"
"ds_write_b16 v45, v84 offset:2560\n\t"
"ds_write_b16_d16_hi v42, v84 offset:640\n\t"
"ds_write_b16 v42, v86 offset:1664\n\t"
"ds_write_b16_d16_hi v42, v86 offset:2688\n\t"
"ds_write_b16 v42, v88 offset:768\n\t"
"ds_write_b16_d16_hi v42, v88 offset:1792\n\t"
"ds_write_b16 v42, v90 offset:2816\n\t"
"ds_write_b16_d16_hi v42, v90 offset:896\n\t"
"ds_write_b16 v42, v92 offset:1920\n\t"
"ds_write_b16_d16_hi v42, v92 offset:2944\n\t"
"ds_read_b128 v[48:51], v43 offset:0\n\t"
"ds_read_b128 v[52:55], v43 offset:1024\n\t"
"ds_read_b128 v[56:59], v43 offset:2048\n\t"
"s_waitcnt lgkmcnt(2)\n\t"
"buffer_store_dwordx4 v[48:51], v44, s[8:11], s57 offen offset:0 sc1\n\t"
"s_waitcnt lgkmcnt(1)\n\t"
"buffer_store_dwordx4 v[52:55], v44, s[8:11], s57 offen offset:1024 sc1\n\t"
"s_waitcnt lgkmcnt(0)\n\t"
"buffer_store_dwordx4 v[56:59], v44, s[8:11], s57 offen offset:2048 sc1\n\t"
"s_waitcnt vmcnt(0) lgkmcnt(0)\n\t"
"s_add_u32 s69, s69, 1\n\t"
"s_cmp_lt_u32 s69, 576\n\t"
"s_cbranch_scc1 Lfb1_%=\n\t"
"ds_read_b128 v[32:35], v255 offset:0\n\t"
"ds_read_b128 v[36:39], v255 offset:3072\n\t"
"ds_read_b128 v[40:43], v255 offset:6144\n\t"
"ds_read_b128 v[44:47], v255 offset:9216\n\t"
"ds_read_b128 v[48:51], v255 offset:12288\n\t"
"ds_read_b128 v[52:55], v255 offset:15360\n\t"
"ds_read_b128 v[56:59], v255 offset:18432\n\t"
"ds_read_b128 v[60:63], v255 offset:21504\n\t"
"ds_read_b128 v[64:67], v255 offset:24576\n\t"
"ds_read_b128 v[68:71], v255 offset:27648\n\t"
"ds_read_b128 v[72:75], v255 offset:30720\n\t"
"ds_read_b128 v[76:79], v255 offset:33792\n\t"
"ds_read_b128 v[80:83], v255 offset:36864\n\t"
"ds_read_b128 v[84:87], v255 offset:39936\n\t"
"ds_read_b128 v[88:91], v255 offset:43008\n\t"
"ds_read_b128 v[92:95], v255 offset:46080\n\t"
"ds_read_b128 v[96:99], v255 offset:49152\n\t"
"ds_read_b128 v[100:103], v255 offset:52224\n\t"
"ds_read_b128 v[104:107], v255 offset:55296\n\t"
"ds_read_b128 v[108:111], v255 offset:58368\n\t"
"ds_read_b128 v[112:115], v255 offset:61440\n\t"
"ds_read_b128 v[116:119], v255 offset:64512\n\t"
"s_waitcnt lgkmcnt(0)\n\t"
"s_branch Lgo_%=\n\t"
".p2align 12\n\t"
"Lpro_%=:\n\t"
"v_mad_u32_u24 v41, v119, s74, v118\n\t"
"s_add_u32 s60, s56, 0\n\t"
"buffer_load_short_d16_hi v32, v41, s[52:55], s60 offen\n\t"
"s_add_u32 s60, s56, 73728\n\t"
"buffer_load_short_d16_hi v33, v41, s[52:55], s60 offen\n\t"
"s_add_u32 s60, s56, 147456\n\t"
"buffer_load_short_d16_hi v34, v41, s[52:55], s60 offen\n\t"
"s_add_u32 s60, s56, 221184\n\t"
"buffer_load_short_d16_hi v35, v41, s[52:55], s60 offen\n\t"
"s_add_u32 s60, s56, 294912\n\t"
"buffer_load_short_d16_hi v36, v41, s[52:55], s60 offen\n\t"
"s_add_u32 s60, s56, 368640\n\t"
"buffer_load_short_d16_hi v37, v41, s[52:55], s60 offen\n\t"
"s_add_u32 s60, s56, 442368\n\t"
"buffer_load_short_d16_hi v38, v41, s[52:55], s60 offen\n\t"
"s_add_u32 s60, s56, 516096\n\t"
"buffer_load_short_d16_hi v39, v41, s[52:55], s60 offen\n\t"
"s_add_u32 s60, s56, 589824\n\t"
"buffer_load_short_d16_hi v40, v41, s[52:55], s60 offen\n\t"
"s_add_u32 s60, s66, 1867776\n\t"
"buffer_load_dword v46, off, s[8:11], s60 sc1\n\t"
"s_mul_i32 s70, s66, 1\n\t"
"s_add_u32 s70, s70, 1900544\n\t"
"s_mov_b32 s60, s70\n\t"
"buffer_load_dword v47, off, s[8:11], s60 sc1\n\t"
"global_load_dwordx4 v[244:247], v252, s[46:47] offset:2048\n\t"
"global_load_dwordx4 v[248:251], v252, s[48:49] offset:0\n\t"
"global_load_dwordx4 v[240:243], v252, s[46:47] offset:0\n\t"
"global_load_dwordx4 v[196:199], v252, s[46:47] offset:1024\n\t"
"global_load_dwordx4 v[220:223], v252, s[48:49] offset:1024\n\t"
"s_cmp_lg_u32 s72, 0\n\t"
"s_cbranch_scc1 Lnl1_%=\n\t"
"v_mul_u32_u24 v116, 1, v252\n\t"
"v_lshrrev_b32 v116, 2, v116\n\t"
"v_mul_u32_u24 v117, 1, v116\n\t"
"s_mov_b32 s60, 1900544\n\t"
"buffer_load_dword v104, v117, s[8:11], s60 offen sc1\n\t"
"s_mov_b32 s60, 1900800\n\t"
"buffer_load_dword v105, v117, s[8:11], s60 offen sc1\n\t"
"s_mov_b32 s60, 1901056\n\t"
"buffer_load_dword v106, v117, s[8:11], s60 offen sc1\n\t"
"s_mov_b32 s60, 1901312\n\t"
"buffer_load_dword v107, v117, s[8:11], s60 offen sc1\n\t"
"s_waitcnt vmcnt(0)\n\t"
"v_add_u32 v104, 0x10000, v104\n\t"
"v_add_u32 v105, 0x10000, v105\n\t"
"v_add_u32 v106, 0x10000, v106\n\t"
"v_add_u32 v107, 0x10000, v107\n\t"
"v_and_b32 v104, 0xffff0000, v104\n\t"
"v_and_b32 v105, 0xffff0000, v105\n\t"
"v_and_b32 v106, 0xffff0000, v106\n\t"
"v_and_b32 v107, 0xffff0000, v107\n\t"
"s_mov_b32 s60, 1835008\n\t"
"buffer_store_dword v104, v116, s[8:11], s60 offen sc1\n\t"
"s_mov_b32 s60, 1835264\n\t"
"buffer_store_dword v105, v116, s[8:11], s60 offen sc1\n\t"
"s_mov_b32 s60, 1835520\n\t"
"buffer_store_dword v106, v116, s[8:11], s60 offen sc1\n\t"
"s_mov_b32 s60, 1835776\n\t"
"buffer_store_dword v107, v116, s[8:11], s60 offen sc1\n\t"
"Lnl1_%=:\n\t"
"s_add_u32 m0, s24, 59904\n\t"
"s_add_u32 s28, s21, 1769472\n\t"
"buffer_load_dwordx4 v28, s[12:15], s28 offen offset:0 nt lds\n\t"
"s_add_u32 m0, s24, 66048\n\t"
"s_add_u32 s28, s21, 1966080\n\t"
"buffer_load_dwordx4 v28, s[12:15], s28 offen offset:0 nt lds\n\t"
"s_add_u32 m0, s24, 72192\n\t"
"s_add_u32 s28, s21, 2162688\n\t"
"buffer_load_dwordx4 v28, s[12:15], s28 offen offset:0 nt lds\n\t"
"s_add_u32 m0, s24, 79872\n\t"
"s_add_u32 s28, s21, 2359296\n\t"
"buffer_load_dwordx4 v28, s[12:15], s28 offen offset:0 nt lds\n\t"
"s_add_u32 m0, s24, 86016\n\t"
"s_add_u32 s28, s21, 2555904\n\t"
"buffer_load_dwordx4 v28, s[12:15], s28 offen offset:0 nt lds\n\t"
"s_add_u32 m0, s24, 92160\n\t"
"s_add_u32 s28, s21, 2752512\n\t"
"buffer_load_dwordx4 v28, s[12:15], s28 offen offset:0 nt lds\n\t"
"s_add_u32 m0, s24, 19968\n\t"
"s_add_u32 s28, s21, 589824\n\t"
"buffer_load_dwordx4 v28, s[12:15], s28 offen offset:0 nt lds\n\t"
"s_add_u32 m0, s24, 26112\n\t"
"s_add_u32 s28, s21, 786432\n\t"
"buffer_load_dwordx4 v28, s[12:15], s28 offen offset:0 nt lds\n\t"
"v_xor_b32 v45, 8, v42\n\t"
"s_waitcnt vmcnt(21)\n\t"
"v_fma_f32 v48, v34, s41, v32\n\t"
"v_fma_f32 v51, v34, s43, v32\n\t"
"s_waitcnt vmcnt(18)\n\t"
"v_fma_f32 v49, v37, s41, v35\n\t"
"v_fma_f32 v52, v37, s43, v35\n\t"
"s_waitcnt vmcnt(15)\n\t"
"v_fma_f32 v50, v40, s41, v38\n\t"
"v_fma_f32 v53, v40, s43, v38\n\t"
"v_fma_f32 v54, v33, s38, v48\n\t"
"v_fma_f32 v57, -v33, s38, v48\n\t"
"v_fma_f32 v60, v33, s39, v51\n\t"
"v_fma_f32 v63, -v33, s39, v51\n\t"
"v_fma_f32 v55, v36, s38, v49\n\t"
"v_fma_f32 v58, -v36, s38, v49\n\t"
"v_fma_f32 v61, v36, s39, v52\n\t"
"v_fma_f32 v64, -v36, s39, v52\n\t"
"v_fma_f32 v56, v39, s38, v50\n\t"
"v_fma_f32 v59, -v39, s38, v50\n\t"
"v_fma_f32 v62, v39, s39, v53\n\t"
"v_fma_f32 v65, -v39, s39, v53\n\t"
"v_mul_f32 v70, 0x3f638e39, v32\n\t"
"v_mul_f32 v71, 0x3f638e39, v35\n\t"
"v_mul_f32 v72, 0x3f638e39, v38\n\t"
"v_mul_f32 v73, 0xbf54ad4b, v54\n\t"
"v_mul_f32 v74, 0xbf54ad4b, v55\n\t"
"v_mul_f32 v75, 0xbf54ad4b, v56\n\t"
"v_mul_f32 v76, 0xbefc0fc1, v57\n\t"
"v_mul_f32 v77, 0xbefc0fc1, v58\n\t"
"v_mul_f32 v78, 0xbefc0fc1, v59\n\t"
"v_mul_f32 v79, 0xbe7c0fc1, v60\n\t"
"v_mul_f32 v80, 0xbe7c0fc1, v61\n\t"
"v_mul_f32 v81, 0xbe7c0fc1, v62\n\t"
"v_mul_f32 v82, 0x3ea80a81, v63\n\t"
"v_mul_f32 v83, 0x3ea80a81, v64\n\t"
"v_mul_f32 v84, 0x3ea80a81, v65\n\t"
"v_mul_f32 v85, 0x3f638e39, v34\n\t"
"v_mul_f32 v86, 0x3f638e39, v37\n\t"
"v_mul_f32 v87, 0x3f638e39, v40\n\t"
"v_mul_f32 v88, 0x3ea80a81, v32\n\t"
"v_mul_f32 v89, 0x3ea80a81, v35\n\t"
"v_mul_f32 v90, 0x3ea80a81, v38\n\t"
"v_mul_f32 v91, 0xbebd0bd1, v34\n\t"
"v_mul_f32 v92, 0xbebd0bd1, v37\n\t"
"v_mul_f32 v93, 0xbebd0bd1, v40\n\t"
"v_cvt_pk_bf16_f32 v70, v70, v71\n\t"
"v_cvt_pk_bf16_f32 v72, v72, v73\n\t"
"v_cvt_pk_bf16_f32 v74, v74, v75\n\t"
"v_cvt_pk_bf16_f32 v76, v76, v77\n\t"
"v_cvt_pk_bf16_f32 v78, v78, v79\n\t"
"v_cvt_pk_bf16_f32 v80, v80, v81\n\t"
"v_cvt_pk_bf16_f32 v82, v82, v83\n\t"
"v_cvt_pk_bf16_f32 v84, v84, v85\n\t"
"v_cvt_pk_bf16_f32 v86, v86, v87\n\t"
"v_cvt_pk_bf16_f32 v88, v88, v89\n\t"
"v_cvt_pk_bf16_f32 v90, v90, v91\n\t"
"v_cvt_pk_bf16_f32 v92, v92, v93\n\t"
"ds_write_b16 v42, v70 offset:0\n\t"
"ds_write_b16_d16_hi v42, v70 offset:1024\n\t"
"ds_write_b16 v42, v72 offset:2048\n\t"
"ds_write_b16_d16_hi v42, v72 offset:128\n\t"
"ds_write_b16 v42, v74 offset:1152\n\t"
"ds_write_b16_d16_hi v42, v74 offset:2176\n\t"
"ds_write_b16 v45, v76 offset:256\n\t"
"ds_write_b16_d16_hi v45, v76 offset:1280\n\t"
"ds_write_b16 v45, v78 offset:2304\n\t"
"ds_write_b16_d16_hi v42, v78 offset:384\n\t"
"ds_write_b16 v42, v80 offset:1408\n\t"
"ds_write_b16_d16_hi v42, v80 offset:2432\n\t"
"ds_write_b16 v45, v82 offset:512\n\t"
"ds_write_b16_d16_hi v45, v82 offset:1536\n\t"
"ds_write_b16 v45, v84 offset:2560\n\t"
"ds_write_b16_d16_hi v42, v84 offset:640\n\t"
"ds_write_b16 v42, v86 offset:1664\n\t"
"ds_write_b16_d16_hi v42, v86 offset:2688\n\t"
"ds_write_b16 v42, v88 offset:768\n\t"
"ds_write_b16_d16_hi v42, v88 offset:1792\n\t"
"ds_write_b16 v42, v90 offset:2816\n\t"
"ds_write_b16_d16_hi v42, v90 offset:896\n\t"
"ds_write_b16 v42, v92 offset:1920\n\t"
"ds_write_b16_d16_hi v42, v92 offset:2944\n\t"
"ds_read_b128 v[48:51], v43 offset:0\n\t"
"ds_read_b128 v[52:55], v43 offset:1024\n\t"
"ds_read_b128 v[56:59], v43 offset:2048\n\t"
"s_waitcnt lgkmcnt(2)\n\t"
"buffer_store_dwordx4 v[48:51], v44, s[8:11], s57 offen offset:0 sc1\n\t"
"s_waitcnt lgkmcnt(1)\n\t"
"buffer_store_dwordx4 v[52:55], v44, s[8:11], s57 offen offset:1024 sc1\n\t"
"s_waitcnt lgkmcnt(0)\n\t"
"buffer_store_dwordx4 v[56:59], v44, s[8:11], s57 offen offset:2048 sc1\n\t"
"s_waitcnt vmcnt(14)\n\t"
"v_and_b32 v254, s40, v251\n\t"
"v_add_u32 v254, s42, v254\n\t"
"s_lshr_b32 s40, s73, 1\n\t"
"s_mul_i32 s40, s40, 768\n\t"
"s_add_u32 s40, s40, s22\n\t"
"s_mov_b32 s60, 0\n\t"
"buffer_load_dwordx4 a[0:3], v29, s[52:55], s60 offen\n\t"
"s_mov_b32 s60, 6144\n\t"
"buffer_load_dwordx4 a[24:27], v29, s[52:55], s60 offen\n\t"
"s_mov_b32 s60, 0\n\t"
"buffer_load_dwordx4 a[48:51], v30, s[52:55], s60 offen\n\t"
"s_mov_b32 s60, 221184\n\t"
"buffer_load_dwordx4 a[8:11], v29, s[52:55], s60 offen\n\t"
"s_mov_b32 s60, 227328\n\t"
"buffer_load_dwordx4 a[32:35], v29, s[52:55], s60 offen\n\t"
"s_mov_b32 s60, 221184\n\t"
"buffer_load_dwordx4 a[56:59], v30, s[52:55], s60 offen\n\t"
"s_mov_b32 s60, 442368\n\t"
"buffer_load_dwordx4 a[16:19], v29, s[52:55], s60 offen\n\t"
"s_mov_b32 s60, 448512\n\t"
"buffer_load_dwordx4 a[40:43], v29, s[52:55], s60 offen\n\t"
"s_mov_b32 s60, 442368\n\t"
"buffer_load_dwordx4 a[64:67], v30, s[52:55], s60 offen\n\t"
"s_mov_b32 s60, 147456\n\t"
"buffer_load_dwordx4 a[72:75], v29, s[52:55], s60 offen\n\t"
"s_mov_b32 s60, 153600\n\t"
"buffer_load_dwordx4 a[96:99], v29, s[52:55], s60 offen\n\t"
"s_mov_b32 s60, 147456\n\t"
"buffer_load_dwordx4 a[120:123], v30, s[52:55], s60 offen\n\t"
"s_mov_b64 vcc, -1\n\t"
"s_bitcmp1_b32 s73, 1\n\t"
"s_cbranch_scc1 Lvh0_%=\n\t"
"s_bitcmp1_b32 s73, 0\n\t"
"s_cbranch_scc1 Lpg2_%=\n\t"
"s_branch Lpg1_%=\n\t"
"Lvh0_%=:\n\t"
"s_bitcmp1_b32 s73, 0\n\t"
"s_cbranch_scc1 Lpg4_%=\n\t"
"s_branch Lpg3_%=\n\t"
"Lvr0_%=:\n\t"
"s_mov_b64 vcc, 0\n\t"
"s_waitcnt vmcnt(12)\n\t"
"s_barrier\n\t"
"v_readfirstlane_b32 s67, v46\n\t"
"v_readfirstlane_b32 s71, v47\n\t"
"s_add_u32 s67, s67, 1\n\t"
"s_and_b32 s67, s67, 0xffff\n\t"
"s_add_u32 s71, s71, 0x10000\n\t"
"s_and_b32 s71, s71, 0xffff0000\n\t"
"s_or_b32 s71, s71, s67\n\t"
"s_mov_b32 s68, 3000\n\t"
"s_cmp_lg_u32 s73, 0\n\t"
"s_cbranch_scc1 Lnl2_%=\n\t"
"v_mov_b32 v47, s71\n\t"
"s_mov_b64 exec, 1\n\t"
"buffer_store_dword v47, off, s[8:11], s70 sc1\n\t"
"s_mov_b64 exec, -1\n\t"
"Lnl2_%=:\n\t"
"s_mov_b32 s60, 368640\n\t"
"buffer_load_dwordx4 a[80:83], v29, s[52:55], s60 offen\n\t"
"s_mov_b32 s60, 374784\n\t"
"buffer_load_dwordx4 a[104:107], v29, s[52:55], s60 offen\n\t"
"s_mov_b32 s60, 368640\n\t"
"buffer_load_dwordx4 a[128:131], v30, s[52:55], s60 offen\n\t"
"s_waitcnt vmcnt(14)\n\t"
"ds_write_b128 v43, a[0:3] offset:0\n\t"
"s_waitcnt vmcnt(13)\n\t"
"ds_write_b128 v43, a[24:27] offset:1024\n\t"
"s_waitcnt vmcnt(12)\n\t"
"ds_write_b128 v43, a[48:51] offset:2048\n\t"
"ds_read_b64_tr_b16 a[0:1], v43 offset:0\n\t"
"ds_read_b64_tr_b16 a[2:3], v43 offset:1024\n\t"
"ds_read_b64_tr_b16 a[24:25], v43 offset:8\n\t"
"ds_read_b64_tr_b16 a[26:27], v43 offset:1032\n\t"
"ds_read_b64_tr_b16 a[48:49], v31 offset:2048\n\t"
"ds_read_b64_tr_b16 a[50:51], v31 offset:2560\n\t"
"s_mov_b32 s60, 589824\n\t"
"buffer_load_dwordx4 a[88:91], v29, s[52:55], s60 offen\n\t"
"s_mov_b32 s60, 595968\n\t"
"buffer_load_dwordx4 a[112:115], v29, s[52:55], s60 offen\n\t"
"s_mov_b32 s60, 589824\n\t"
"buffer_load_dwordx4 a[136:139], v30, s[52:55], s60 offen\n\t"
"s_waitcnt vmcnt(14)\n\t"
"ds_write_b128 v43, a[8:11] offset:0\n\t"
"s_waitcnt vmcnt(13)\n\t"
"ds_write_b128 v43, a[32:35] offset:1024\n\t"
"s_waitcnt vmcnt(12)\n\t"
"ds_write_b128 v43, a[56:59] offset:2048\n\t"
"ds_read_b64_tr_b16 a[8:9], v43 offset:0\n\t"
"ds_read_b64_tr_b16 a[10:11], v43 offset:1024\n\t"
"ds_read_b64_tr_b16 a[32:33], v43 offset:8\n\t"
"ds_read_b64_tr_b16 a[34:35], v43 offset:1032\n\t"
"ds_read_b64_tr_b16 a[56:57], v31 offset:2048\n\t"
"ds_read_b64_tr_b16 a[58:59], v31 offset:2560\n\t"
"s_add_u32 m0, s25, 0\n\t"
"s_nop 0\n\t"
"buffer_load_dwordx4 v254, s[12:15], 0 offen offset:0 nt lds\n\t"
"s_add_u32 m0, s26, 0\n\t"
"s_mov_b64 exec, s[36:37]\n\t"
"buffer_load_dwordx4 v254, s[12:15], s27 offen offset:0 nt lds\n\t"
"s_mov_b64 exec, -1\n\t"
"s_add_u32 m0, s24, 32256\n\t"
"s_add_u32 s28, s21, 983040\n\t"
"buffer_load_dwordx4 v247, s[12:15], s28 offen offset:0 nt lds\n\t"
"s_waitcnt vmcnt(14)\n\t"
"ds_write_b128 v43, a[16:19] offset:0\n\t"
"s_waitcnt vmcnt(13)\n\t"
"ds_write_b128 v43, a[40:43] offset:1024\n\t"
"s_waitcnt vmcnt(12)\n\t"
"ds_write_b128 v43, a[64:67] offset:2048\n\t"
"ds_read_b64_tr_b16 a[16:17], v43 offset:0\n\t"
"ds_read_b64_tr_b16 a[18:19], v43 offset:1024\n\t"
"ds_read_b64_tr_b16 a[40:41], v43 offset:8\n\t"
"ds_read_b64_tr_b16 a[42:43], v43 offset:1032\n\t"
"ds_read_b64_tr_b16 a[64:65], v31 offset:2048\n\t"
"ds_read_b64_tr_b16 a[66:67], v31 offset:2560\n\t"
"s_add_u32 m0, s24, 0\n\t"
"s_add_u32 s28, s21, 0\n\t"
"buffer_load_dwordx4 v247, s[12:15], s28 offen offset:0 nt lds\n\t"
"s_add_u32 m0, s24, 6144\n\t"
"s_add_u32 s28, s21, 196608\n\t"
"buffer_load_dwordx4 v247, s[12:15], s28 offen offset:0 nt lds\n\t"
"s_add_u32 m0, s24, 12288\n\t"
"s_add_u32 s28, s21, 393216\n\t"
"buffer_load_dwordx4 v247, s[12:15], s28 offen offset:0 nt lds\n\t"
"s_waitcnt vmcnt(14)\n\t"
"ds_write_b128 v43, a[72:75] offset:0\n\t"
"s_waitcnt vmcnt(13)\n\t"
"ds_write_b128 v43, a[96:99] offset:1024\n\t"
"s_waitcnt vmcnt(12)\n\t"
"ds_write_b128 v43, a[120:123] offset:2048\n\t"
"ds_read_b64_tr_b16 a[72:73], v43 offset:0\n\t"
"ds_read_b64_tr_b16 a[74:75], v43 offset:1024\n\t"
"ds_read_b64_tr_b16 a[96:97], v43 offset:8\n\t"
"ds_read_b64_tr_b16 a[98:99], v43 offset:1032\n\t"
"ds_read_b64_tr_b16 a[120:121], v31 offset:2048\n\t"
"ds_read_b64_tr_b16 a[122:123], v31 offset:2560\n\t"
"s_cmp_lg_u32 s66, 0\n\t"
"s_cbranch_scc1 Lld4_%=\n\t"
"s_cmp_lg_u32 s73, 0\n\t"
"s_cbranch_scc1 Lld2_%=\n\t"
"Lld1_%=:\n\t"
"s_mov_b32 s60, 1900544\n\t"
"buffer_load_dword v108, v117, s[8:11], s60 offen sc1\n\t"
"s_mov_b32 s60, 1900800\n\t"
"buffer_load_dword v109, v117, s[8:11], s60 offen sc1\n\t"
"s_mov_b32 s60, 1901056\n\t"
"buffer_load_dword v110, v117, s[8:11], s60 offen sc1\n\t"
"s_mov_b32 s60, 1901312\n\t"
"buffer_load_dword v111, v117, s[8:11], s60 offen sc1\n\t"
"s_waitcnt vmcnt(0)\n\t"
"v_xor_b32 v112, v108, v104\n\t"
"v_xor_b32 v113, v109, v105\n\t"
"v_xor_b32 v114, v110, v106\n\t"
"v_xor_b32 v115, v111, v107\n\t"
"s_nop 1\n\t"
"v_writelane_b32 v112, 0, 0\n\t"
"s_nop 1\n\t"
"v_or3_b32 v112, v112, v113, v114\n\t"
"v_or_b32 v112, v112, v115\n\t"
"v_cmp_lt_u32 vcc, 0xffff, v112\n\t"
"s_nop 4\n\t"
"s_cmp_eq_u64 vcc, 0\n\t"
"s_cbranch_scc1 Lld3_%=\n\t"
"s_sub_u32 s68, s68, 1\n\t"
"s_cmp_lg_u32 s68, 0\n\t"
"s_cbranch_scc1 Lld1_%=\n\t"
"s_branch Lld2_%=\n\t"
"Lld3_%=:\n\t"
"s_mov_b32 s60, 1867776\n\t"
"buffer_store_dword v108, v116, s[8:11], s60 offen sc1\n\t"
"s_mov_b32 s60, 1868032\n\t"
"buffer_store_dword v109, v116, s[8:11], s60 offen sc1\n\t"
"s_mov_b32 s60, 1868288\n\t"
"buffer_store_dword v110, v116, s[8:11], s60 offen sc1\n\t"
"s_mov_b32 s60, 1868544\n\t"
"buffer_store_dword v111, v116, s[8:11], s60 offen sc1\n\t"
"s_mov_b32 s68, -1\n\t"
"Lld2_%=:\n\t"
"s_barrier\n\t"
"Lld4_%=:\n\t"
"s_add_u32 m0, s24, 39936\n\t"
"s_add_u32 s28, s21, 1179648\n\t"
"buffer_load_dwordx4 v247, s[12:15], s28 offen offset:0 nt lds\n\t"
"s_add_u32 m0, s24, 46080\n\t"
"s_add_u32 s28, s21, 1376256\n\t"
"buffer_load_dwordx4 v247, s[12:15], s28 offen offset:0 nt lds\n\t"
"s_add_u32 m0, s24, 52224\n\t"
"s_add_u32 s28, s21, 1572864\n\t"
"buffer_load_dwordx4 v247, s[12:15], s28 offen offset:0 nt lds\n\t"
"s_waitcnt vmcnt(14)\n\t"
"ds_write_b128 v43, a[80:83] offset:0\n\t"
"s_waitcnt vmcnt(13)\n\t"
"ds_write_b128 v43, a[104:107] offset:1024\n\t"
"s_waitcnt vmcnt(12)\n\t"
"ds_write_b128 v43, a[128:131] offset:2048\n\t"
"ds_read_b64_tr_b16 a[80:81], v43 offset:0\n\t"
"ds_read_b64_tr_b16 a[82:83], v43 offset:1024\n\t"
"ds_read_b64_tr_b16 a[104:105], v43 offset:8\n\t"
"ds_read_b64_tr_b16 a[106:107], v43 offset:1032\n\t"
"ds_read_b64_tr_b16 a[128:129], v31 offset:2048\n\t"
"ds_read_b64_tr_b16 a[130:131], v31 offset:2560\n\t"
"s_waitcnt vmcnt(11)\n\t"
"ds_write_b128 v43, a[88:91] offset:0\n\t"
"s_waitcnt vmcnt(10)\n\t"
"ds_write_b128 v43, a[112:115] offset:1024\n\t"
"s_waitcnt vmcnt(9)\n\t"
"ds_write_b128 v43, a[136:139] offset:2048\n\t"
"ds_read_b64_tr_b16 a[88:89], v43 offset:0\n\t"
"ds_read_b64_tr_b16 a[90:91], v43 offset:1024\n\t"
"ds_read_b64_tr_b16 a[112:113], v43 offset:8\n\t"
"ds_read_b64_tr_b16 a[114:115], v43 offset:1032\n\t"
"ds_read_b64_tr_b16 a[136:137], v31 offset:2048\n\t"
"ds_read_b64_tr_b16 a[138:139], v31 offset:2560\n\t"
"s_waitcnt lgkmcnt(0)\n\t"
"v_add_u32 v240, 59904, v240\n\t"
"v_add_u32 v241, 59904, v241\n\t"
"s_mov_b64 vcc, 0\n\t"
"s_waitcnt vmcnt(3)\n\t"
"s_barrier\n\t"
"ds_read_b64_tr_b16 v[120:121], v240 offset:0\n\t"
"ds_read_b64_tr_b16 v[122:123], v240 offset:1536\n\t"
"ds_read_b64_tr_b16 v[124:125], v241 offset:0\n\t"
"ds_read_b64_tr_b16 v[126:127], v241 offset:1536\n\t"
"ds_read_b64_tr_b16 v[128:129], v240 offset:19968\n\t"
"ds_read_b64_tr_b16 v[130:131], v240 offset:21504\n\t"
"ds_read_b64_tr_b16 v[132:133], v241 offset:19968\n\t"
"ds_read_b64_tr_b16 v[134:135], v241 offset:21504\n\t"
"v_subrev_u32 v240, 59904, v240\n\t"
"v_subrev_u32 v241, 59904, v241\n\t"
"ds_read_b64_tr_b16 v[136:137], v240 offset:19968\n\t"
"ds_read_b64_tr_b16 v[138:139], v240 offset:21504\n\t"
"ds_read_b64_tr_b16 v[140:141], v241 offset:19968\n\t"
"ds_read_b64_tr_b16 v[142:143], v241 offset:21504\n\t"
"ds_read_b64_tr_b16 v[144:145], v240 offset:0\n\t"
"ds_read_b64_tr_b16 v[146:147], v240 offset:1536\n\t"
"ds_read_b64_tr_b16 v[148:149], v241 offset:0\n\t"
"ds_read_b64_tr_b16 v[150:151], v241 offset:1536\n\t"
"s_add_u32 m0, s24, 99840\n\t"
"s_add_u32 s28, s21, 2949120\n\t"
"buffer_load_dwordx4 v247, s[12:15], s28 offen offset:0 nt lds\n\t"
"s_add_u32 m0, s24, 105984\n\t"
"s_add_u32 s28, s21, 3145728\n\t"
"buffer_load_dwordx4 v247, s[12:15], s28 offen offset:0 nt lds\n\t"
"s_add_u32 m0, s24, 112128\n\t"
"s_add_u32 s28, s21, 3342336\n\t"
"buffer_load_dwordx4 v247, s[12:15], s28 offen offset:0 nt lds\n\t"
"s_waitcnt lgkmcnt(14)\n\t"
"v_mfma_f32_16x16x32_bf16 v[120:123], v[120:123], v[196:199], 0\n\t"
"s_waitcnt lgkmcnt(12)\n\t"
"v_mfma_f32_16x16x32_bf16 v[124:127], v[124:127], v[196:199], 0\n\t"
"s_waitcnt lgkmcnt(10)\n\t"
"v_mfma_f32_16x16x32_bf16 v[128:131], v[128:131], v[196:199], 0\n\t"
"s_waitcnt lgkmcnt(8)\n\t"
"v_mfma_f32_16x16x32_bf16 v[132:135], v[132:135], v[196:199], 0\n\t"
"s_waitcnt lgkmcnt(6)\n\t"
"v_mfma_f32_16x16x32_bf16 v[136:139], v[136:139], v[196:199], 0\n\t"
"s_waitcnt lgkmcnt(4)\n\t"
"v_mfma_f32_16x16x32_bf16 v[140:143], v[140:143], v[196:199], 0\n\t"
"s_waitcnt lgkmcnt(2)\n\t"
"v_mfma_f32_16x16x32_bf16 v[144:147], v[144:147], v[196:199], 0\n\t"
"s_waitcnt lgkmcnt(0)\n\t"
"v_mfma_f32_16x16x32_bf16 v[148:151], v[148:151], v[196:199], 0\n\t"
"v_pk_mul_f32 v[120:121], v[120:121], v[242:243] op_sel:[0,1] op_sel_hi:[1,1]\n\t"
"v_pk_mul_f32 v[122:123], v[122:123], v[242:243] op_sel:[0,1] op_sel_hi:[1,1]\n\t"
"v_cvt_pk_bf16_f32 v120, v120, v121\n\t"
"v_cvt_pk_bf16_f32 v121, v122, v123\n\t"
"v_pk_mul_f32 v[124:125], v[124:125], v[242:243] op_sel:[0,1] op_sel_hi:[1,1]\n\t"
"v_pk_mul_f32 v[126:127], v[126:127], v[242:243] op_sel:[0,1] op_sel_hi:[1,1]\n\t"
"v_cvt_pk_bf16_f32 v122, v124, v125\n\t"
"v_cvt_pk_bf16_f32 v123, v126, v127\n\t"
"ds_write_b128 v242, v[120:123] offset:29184\n\t"
"v_pk_mul_f32 v[128:129], v[128:129], v[242:243] op_sel:[0,1] op_sel_hi:[1,1]\n\t"
"v_pk_mul_f32 v[130:131], v[130:131], v[242:243] op_sel:[0,1] op_sel_hi:[1,1]\n\t"
"v_cvt_pk_bf16_f32 v128, v128, v129\n\t"
"v_cvt_pk_bf16_f32 v129, v130, v131\n\t"
"v_pk_mul_f32 v[132:133], v[132:133], v[242:243] op_sel:[0,1] op_sel_hi:[1,1]\n\t"
"v_pk_mul_f32 v[134:135], v[134:135], v[242:243] op_sel:[0,1] op_sel_hi:[1,1]\n\t"
"v_cvt_pk_bf16_f32 v130, v132, v133\n\t"
"v_cvt_pk_bf16_f32 v131, v134, v135\n\t"
"ds_write_b128 v242, v[128:131] offset:30720\n\t"
"v_pk_mul_f32 v[136:137], v[136:137], v[242:243] op_sel:[0,1] op_sel_hi:[1,1]\n\t"
"v_pk_mul_f32 v[138:139], v[138:139], v[242:243] op_sel:[0,1] op_sel_hi:[1,1]\n\t"
"v_cvt_pk_bf16_f32 v136, v136, v137\n\t"
"v_cvt_pk_bf16_f32 v137, v138, v139\n\t"
"v_pk_mul_f32 v[140:141], v[140:141], v[242:243] op_sel:[0,1] op_sel_hi:[1,1]\n\t"
"v_pk_mul_f32 v[142:143], v[142:143], v[242:243] op_sel:[0,1] op_sel_hi:[1,1]\n\t"
"v_cvt_pk_bf16_f32 v138, v140, v141\n\t"
"v_cvt_pk_bf16_f32 v139, v142, v143\n\t"
"ds_write_b128 v242, v[136:139] offset:26112\n\t"
"v_pk_mul_f32 v[144:145], v[144:145], v[242:243] op_sel:[0,1] op_sel_hi:[1,1]\n\t"
"v_pk_mul_f32 v[146:147], v[146:147], v[242:243] op_sel:[0,1] op_sel_hi:[1,1]\n\t"
"v_cvt_pk_bf16_f32 v144, v144, v145\n\t"
"v_cvt_pk_bf16_f32 v145, v146, v147\n\t"
"v_pk_mul_f32 v[148:149], v[148:149], v[242:243] op_sel:[0,1] op_sel_hi:[1,1]\n\t"
"v_pk_mul_f32 v[150:151], v[150:151], v[242:243] op_sel:[0,1] op_sel_hi:[1,1]\n\t"
"v_cvt_pk_bf16_f32 v146, v148, v149\n\t"
"v_cvt_pk_bf16_f32 v147, v150, v151\n\t"
"ds_write_b128 v242, v[144:147] offset:24576\n\t"
"s_waitcnt lgkmcnt(0)\n\t"
"s_barrier\n\t"
"ds_read_b128 a[168:171], v244 offset:26112\n\t"
"ds_read_b128 a[172:175], v244 offset:26624\n\t"
"ds_read_b128 a[176:179], v244 offset:27136\n\t"
"ds_read_b128 a[180:183], v244 offset:35328\n\t"
"ds_read_b128 a[184:187], v244 offset:35840\n\t"
"ds_read_b128 a[188:191], v244 offset:36352\n\t"
"ds_read_b128 a[192:195], v244 offset:25600\n\t"
"s_waitcnt lgkmcnt(4)\n\t"
"v_mfma_f32_16x16x32_bf16 v[14:17], a[0:3], a[168:171], 0\n\t"
"ds_read_b64_tr_b16 v[224:225], v240 offset:512\n\t"
"v_mfma_f32_16x16x32_bf16 v[14:17], a[8:11], a[172:175], v[14:17]\n\t"
"ds_read_b64_tr_b16 v[226:227], v240 offset:2048\n\t"
"v_mfma_f32_16x16x32_bf16 v[14:17], a[16:19], a[176:179], v[14:17]\n\t"
"ds_read_b64_tr_b16 v[228:229], v241 offset:512\n\t"
"v_mfma_f32_16x16x32_bf16 v[46:49], a[24:27], a[168:171], 0\n\t"
"ds_read_b64_tr_b16 v[230:231], v241 offset:2048\n\t"
"v_mfma_f32_16x16x32_bf16 v[46:49], a[32:35], a[172:175], v[46:49]\n\t"
"v_mfma_f32_16x16x32_bf16 v[46:49], a[40:43], a[176:179], v[46:49]\n\t"
"v_mfma_f32_16x16x32_bf16 v[78:81], a[48:51], a[168:171], 0\n\t"
"s_waitcnt lgkmcnt(5)\n\t"
"v_mfma_f32_16x16x32_bf16 v[78:81], a[56:59], a[172:175], v[78:81]\n\t"
"ds_read_b64_tr_b16 v[232:233], v240 offset:20480\n\t"
"v_mfma_f32_16x16x32_bf16 v[78:81], a[64:67], a[176:179], v[78:81]\n\t"
"ds_read_b64_tr_b16 v[234:235], v240 offset:22016\n\t"
"v_mfma_f32_16x16x32_bf16 v[112:115], a[72:75], a[180:183], 0\n\t"
"s_waitcnt lgkmcnt(4)\n\t"
"ds_read_b64_tr_b16 v[236:237], v241 offset:20480\n\t"
"v_mfma_f32_16x16x32_bf16 v[112:115], a[80:83], a[184:187], v[112:115]\n\t"
"s_waitcnt lgkmcnt(3)\n\t"
"ds_read_b64_tr_b16 v[238:239], v241 offset:22016\n\t"
"v_mfma_f32_16x16x32_bf16 v[112:115], a[88:91], a[188:191], v[112:115]\n\t"
"ds_read_b128 a[196:199], v244 offset:34816\n\t"
"v_mfma_f32_16x16x32_bf16 v[224:227], v[224:227], v[196:199], 0\n\t"
"v_mfma_f32_16x16x32_bf16 v[228:231], v[228:231], v[196:199], 0\n\t"
"v_mfma_f32_16x16x32_bf16 v[144:147], a[96:99], a[180:183], 0\n\t"
"v_mfma_f32_16x16x32_bf16 v[144:147], a[104:107], a[184:187], v[144:147]\n\t"
"v_mfma_f32_16x16x32_bf16 v[144:147], a[112:115], a[188:191], v[144:147]\n\t"
"v_mfma_f32_16x16x32_bf16 v[176:179], a[120:123], a[180:183], 0\n\t"
"v_cvt_pk_bf16_f32 v224, v224, v225\n\t"
"v_cvt_pk_bf16_f32 v225, v226, v227\n\t"
"v_mfma_f32_16x16x32_bf16 v[176:179], a[128:131], a[184:187], v[176:179]\n\t"
"ds_read_b128 a[176:179], v244 offset:25088\n\t"
"v_cvt_pk_bf16_f32 v226, v228, v229\n\t"
"v_mfma_f32_16x16x32_bf16 v[176:179], a[136:139], a[188:191], v[176:179]\n\t"
"v_cvt_pk_bf16_f32 v227, v230, v231\n\t"
"v_mfma_f32_16x16x32_bf16 v[10:13], a[0:3], a[192:195], 0\n\t"
"s_waitcnt lgkmcnt(4)\n\t"
"v_mfma_f32_16x16x32_bf16 v[10:13], a[8:11], a[168:171], v[10:13]\n\t"
"s_waitcnt lgkmcnt(2)\n\t"
"v_mfma_f32_16x16x32_bf16 v[10:13], a[16:19], a[172:175], v[10:13]\n\t"
"ds_write_b128 v242, v[224:227] offset:0\n\t"
"v_mfma_f32_16x16x32_bf16 v[232:235], v[232:235], v[196:199], 0\n\t"
"v_mfma_f32_16x16x32_bf16 v[236:239], v[236:239], v[196:199], 0\n\t"
"s_waitcnt vmcnt(3)\n\t"
"s_barrier\n\t"
"v_mfma_f32_16x16x32_bf16 v[42:45], a[24:27], a[192:195], 0\n\t"
"ds_read_b64_tr_b16 v[228:229], v240 offset:39936\n\t"
"v_mfma_f32_16x16x32_bf16 v[42:45], a[32:35], a[168:171], v[42:45]\n\t"
"ds_read_b64_tr_b16 v[230:231], v240 offset:41472\n\t"
"v_mfma_f32_16x16x32_bf16 v[42:45], a[40:43], a[172:175], v[42:45]\n\t"
"ds_read_b64_tr_b16 v[224:225], v241 offset:39936\n\t"
"v_mfma_f32_16x16x32_bf16 v[74:77], a[48:51], a[192:195], 0\n\t"
"s_waitcnt lgkmcnt(5)\n\t"
"ds_read_b64_tr_b16 v[226:227], v241 offset:41472\n\t"
"v_cvt_pk_bf16_f32 v232, v232, v233\n\t"
"v_mfma_f32_16x16x32_bf16 v[74:77], a[56:59], a[168:171], v[74:77]\n\t"
"ds_read_b128 a[188:191], v244 offset:34304\n\t"
"v_cvt_pk_bf16_f32 v233, v234, v235\n\t"
"v_mfma_f32_16x16x32_bf16 v[74:77], a[64:67], a[172:175], v[74:77]\n\t"
"v_cvt_pk_bf16_f32 v234, v236, v237\n\t"
"v_cvt_pk_bf16_f32 v235, v238, v239\n\t"
"v_mfma_f32_16x16x32_bf16 v[108:111], a[72:75], a[196:199], 0\n\t"
"s_waitcnt lgkmcnt(3)\n\t"
"v_mfma_f32_16x16x32_bf16 v[108:111], a[80:83], a[180:183], v[108:111]\n\t"
"ds_read_b64_tr_b16 v[236:237], v240 offset:40448\n\t"
"v_mfma_f32_16x16x32_bf16 v[108:111], a[88:91], a[184:187], v[108:111]\n\t"
"s_waitcnt lgkmcnt(2)\n\t"
"ds_write_b128 v242, v[232:235] offset:1536\n\t"
"v_mfma_f32_16x16x32_bf16 v[228:231], v[228:231], v[196:199], 0\n\t"
"v_mfma_f32_16x16x32_bf16 v[224:227], v[224:227], v[196:199], 0\n\t"
"ds_read_b64_tr_b16 v[238:239], v240 offset:41984\n\t"
"v_mfma_f32_16x16x32_bf16 v[140:143], a[96:99], a[196:199], 0\n\t"
"ds_read_b64_tr_b16 v[232:233], v241 offset:40448\n\t"
"v_mfma_f32_16x16x32_bf16 v[140:143], a[104:107], a[180:183], v[140:143]\n\t"
"ds_read_b64_tr_b16 v[234:235], v241 offset:41984\n\t"
"v_mfma_f32_16x16x32_bf16 v[140:143], a[112:115], a[184:187], v[140:143]\n\t"
"v_pk_mul_f32 v[228:229], v[228:229], v[242:243] op_sel:[0,1] op_sel_hi:[1,1]\n\t"
"v_pk_mul_f32 v[230:231], v[230:231], v[242:243] op_sel:[0,1] op_sel_hi:[1,1]\n\t"
"v_mfma_f32_16x16x32_bf16 v[172:175], a[120:123], a[196:199], 0\n\t"
"v_cvt_pk_bf16_f32 v228, v228, v229\n\t"
"v_cvt_pk_bf16_f32 v229, v230, v231\n\t"
"v_mfma_f32_16x16x32_bf16 v[172:175], a[128:131], a[180:183], v[172:175]\n\t"
"v_mfma_f32_16x16x32_bf16 v[172:175], a[136:139], a[184:187], v[172:175]\n\t"
"v_pk_mul_f32 v[224:225], v[224:225], v[242:243] op_sel:[0,1] op_sel_hi:[1,1]\n\t"
"v_pk_mul_f32 v[226:227], v[226:227], v[242:243] op_sel:[0,1] op_sel_hi:[1,1]\n\t"
"v_mfma_f32_16x16x32_bf16 v[6:9], a[0:3], a[176:179], 0\n\t"
"s_waitcnt lgkmcnt(0)\n\t"
"v_cvt_pk_bf16_f32 v230, v224, v225\n\t"
"v_cvt_pk_bf16_f32 v231, v226, v227\n\t"
"v_mfma_f32_16x16x32_bf16 v[6:9], a[8:11], a[192:195], v[6:9]\n\t"
"Lpg1_%=:\n\t"
"s_cbranch_vccnz Lpg5_%=\n\t"
"v_add_u32 v240, 59904, v240\n\t"
"v_add_u32 v241, 59904, v241\n\t"
"v_mfma_f32_16x16x32_bf16 v[6:9], a[16:19], a[168:171], v[6:9]\n\t"
"ds_write_b128 v242, v[228:231] offset:27648\n\t"
"v_mfma_f32_16x16x32_bf16 v[38:41], a[24:27], a[176:179], 0\n\t"
"v_mfma_f32_16x16x32_bf16 v[38:41], a[32:35], a[192:195], v[38:41]\n\t"
"ds_read_b64_tr_b16 v[224:225], v240 offset:512\n\t"
"v_mfma_f32_16x16x32_bf16 v[38:41], a[40:43], a[168:171], v[38:41]\n\t"
"ds_read_b64_tr_b16 v[226:227], v240 offset:2048\n\t"
"v_mfma_f32_16x16x32_bf16 v[236:239], v[236:239], v[196:199], 0\n\t"
"ds_read_b64_tr_b16 v[228:229], v241 offset:512\n\t"
"v_mfma_f32_16x16x32_bf16 v[232:235], v[232:235], v[196:199], 0\n\t"
"ds_read_b64_tr_b16 v[230:231], v241 offset:2048\n\t"
"v_mfma_f32_16x16x32_bf16 v[70:73], a[48:51], a[176:179], 0\n\t"
"v_mfma_f32_16x16x32_bf16 v[70:73], a[56:59], a[192:195], v[70:73]\n\t"
"v_mfma_f32_16x16x32_bf16 v[70:73], a[64:67], a[168:171], v[70:73]\n\t"
"v_mfma_f32_16x16x32_bf16 v[104:107], a[72:75], a[188:191], 0\n\t"
"v_cvt_pk_bf16_f32 v236, v236, v237\n\t"
"v_cvt_pk_bf16_f32 v237, v238, v239\n\t"
"v_mfma_f32_16x16x32_bf16 v[104:107], a[80:83], a[196:199], v[104:107]\n\t"
"v_cvt_pk_bf16_f32 v238, v232, v233\n\t"
"v_mfma_f32_16x16x32_bf16 v[104:107], a[88:91], a[180:183], v[104:107]\n\t"
"s_waitcnt lgkmcnt(4)\n\t"
"s_barrier\n\t"
"v_mfma_f32_16x16x32_bf16 v[136:139], a[96:99], a[188:191], 0\n\t"
"ds_read_b128 a[168:171], v244 offset:24576\n\t"
"v_mfma_f32_16x16x32_bf16 v[136:139], a[104:107], a[196:199], v[136:139]\n\t"
"ds_read_b128 a[172:175], v244 offset:33792\n\t"
"v_cvt_pk_bf16_f32 v239, v234, v235\n\t"
"v_mfma_f32_16x16x32_bf16 v[136:139], a[112:115], a[180:183], v[136:139]\n\t"
"ds_write_b128 v242, v[236:239] offset:3072\n\t"
"v_mfma_f32_16x16x32_bf16 v[168:171], a[120:123], a[188:191], 0\n\t"
"s_waitcnt lgkmcnt(5)\n\t"
"v_mfma_f32_16x16x32_bf16 v[168:171], a[128:131], a[196:199], v[168:171]\n\t"
"s_waitcnt lgkmcnt(3)\n\t"
"ds_read_b128 a[184:187], v244 offset:26624\n\t"
"v_mfma_f32_16x16x32_bf16 v[168:171], a[136:139], a[180:183], v[168:171]\n\t"
"s_waitcnt lgkmcnt(3)\n\t"
"ds_read_b128 a[200:203], v244 offset:27136\n\t"
"v_mfma_f32_16x16x32_bf16 v[224:227], v[224:227], v[196:199], 0\n\t"
"ds_read_b128 a[204:207], v244 offset:27648\n\t"
"v_mfma_f32_16x16x32_bf16 v[228:231], v[228:231], v[196:199], 0\n\t"
"v_mfma_f32_16x16x32_bf16 v[2:5], a[0:3], a[168:171], 0\n\t"
"v_mfma_f32_16x16x32_bf16 v[2:5], a[8:11], a[176:179], v[2:5]\n\t"
"v_mfma_f32_16x16x32_bf16 v[2:5], a[16:19], a[192:195], v[2:5]\n\t"
"s_waitcnt vmcnt(0)\n\t"
"s_barrier\n\t"
"s_add_u32 s60, s66, 1867776\n\t"
"buffer_load_dword v98, off, s[8:11], s60 sc1\n\t"
"v_mfma_f32_16x16x32_bf16 v[34:37], a[24:27], a[168:171], 0\n\t"
"ds_read_b64_tr_b16 v[232:233], v240 offset:39936\n\t"
"v_mfma_f32_16x16x32_bf16 v[34:37], a[32:35], a[176:179], v[34:37]\n\t"
"ds_read_b64_tr_b16 v[234:235], v240 offset:41472\n\t"
"v_cvt_pk_bf16_f32 v224, v224, v225\n\t"
"v_mfma_f32_16x16x32_bf16 v[34:37], a[40:43], a[192:195], v[34:37]\n\t"
"ds_read_b64_tr_b16 v[236:237], v241 offset:39936\n\t"
"v_cvt_pk_bf16_f32 v225, v226, v227\n\t"
"v_mfma_f32_16x16x32_bf16 v[66:69], a[48:51], a[168:171], 0\n\t"
"s_waitcnt lgkmcnt(7)\n\t"
"ds_read_b64_tr_b16 v[238:239], v241 offset:41472\n\t"
"v_cvt_pk_bf16_f32 v226, v228, v229\n\t"
"v_mfma_f32_16x16x32_bf16 v[66:69], a[56:59], a[176:179], v[66:69]\n\t"
"ds_read_b128 a[168:171], v244 offset:35840\n\t"
"v_cvt_pk_bf16_f32 v227, v230, v231\n\t"
"v_mfma_f32_16x16x32_bf16 v[66:69], a[64:67], a[192:195], v[66:69]\n\t"
"ds_write_b128 v242, v[224:227] offset:4608\n\t"
"v_mfma_f32_16x16x32_bf16 v[100:103], a[72:75], a[172:175], 0\n\t"
"s_waitcnt lgkmcnt(4)\n\t"
"v_mfma_f32_16x16x32_bf16 v[100:103], a[80:83], a[188:191], v[100:103]\n\t"
"ds_read_b128 a[176:179], v244 offset:36352\n\t"
"v_mfma_f32_16x16x32_bf16 v[100:103], a[88:91], a[196:199], v[100:103]\n\t"
"s_waitcnt lgkmcnt(3)\n\t"
"ds_read_b128 a[180:183], v244 offset:36864\n\t"
"v_mfma_f32_16x16x32_bf16 v[232:235], v[232:235], v[196:199], 0\n\t"
"ds_read_b64_tr_b16 v[228:229], v240 offset:20480\n\t"
"v_mfma_f32_16x16x32_bf16 v[236:239], v[236:239], v[196:199], 0\n\t"
"ds_read_b64_tr_b16 v[230:231], v240 offset:22016\n\t"
"v_mfma_f32_16x16x32_bf16 v[132:135], a[96:99], a[172:175], 0\n\t"
"ds_read_b64_tr_b16 v[224:225], v241 offset:20480\n\t"
"v_mfma_f32_16x16x32_bf16 v[132:135], a[104:107], a[188:191], v[132:135]\n\t"
"ds_read_b64_tr_b16 v[226:227], v241 offset:22016\n\t"
"v_mfma_f32_16x16x32_bf16 v[132:135], a[112:115], a[196:199], v[132:135]\n\t"
"v_pk_mul_f32 v[232:233], v[232:233], v[242:243] op_sel:[0,1] op_sel_hi:[1,1]\n\t"
"v_pk_mul_f32 v[234:235], v[234:235], v[242:243] op_sel:[0,1] op_sel_hi:[1,1]\n\t"
"v_mfma_f32_16x16x32_bf16 v[164:167], a[120:123], a[172:175], 0\n\t"
"v_cvt_pk_bf16_f32 v232, v232, v233\n\t"
"v_cvt_pk_bf16_f32 v233, v234, v235\n\t"
"v_mfma_f32_16x16x32_bf16 v[164:167], a[128:131], a[188:191], v[164:167]\n\t"
"v_mfma_f32_16x16x32_bf16 v[164:167], a[136:139], a[196:199], v[164:167]\n\t"
"v_pk_mul_f32 v[236:237], v[236:237], v[242:243] op_sel:[0,1] op_sel_hi:[1,1]\n\t"
"v_pk_mul_f32 v[238:239], v[238:239], v[242:243] op_sel:[0,1] op_sel_hi:[1,1]\n\t"
"v_mfma_f32_16x16x32_bf16 v[18:21], a[0:3], a[184:187], 0\n\t"
"v_cvt_pk_bf16_f32 v234, v236, v237\n\t"
"v_cvt_pk_bf16_f32 v235, v238, v239\n\t"
"v_mfma_f32_16x16x32_bf16 v[18:21], a[8:11], a[200:203], v[18:21]\n\t"
"ds_read_b64_tr_b16 v[236:237], v240 offset:40448\n\t"
"v_mfma_f32_16x16x32_bf16 v[18:21], a[16:19], a[204:207], v[18:21]\n\t"
"ds_write_b128 v242, v[232:235] offset:32256\n\t"
"v_mfma_f32_16x16x32_bf16 v[50:53], a[24:27], a[184:187], 0\n\t"
"s_waitcnt lgkmcnt(4)\n\t"
"v_mfma_f32_16x16x32_bf16 v[50:53], a[32:35], a[200:203], v[50:53]\n\t"
"s_waitcnt lgkmcnt(2)\n\t"
"ds_read_b64_tr_b16 v[238:239], v240 offset:41984\n\t"
"v_mfma_f32_16x16x32_bf16 v[50:53], a[40:43], a[204:207], v[50:53]\n\t"
"ds_read_b64_tr_b16 v[232:233], v241 offset:40448\n\t"
"v_mfma_f32_16x16x32_bf16 v[228:231], v[228:231], v[196:199], 0\n\t"
"ds_read_b64_tr_b16 v[234:235], v241 offset:41984\n\t"
"v_mfma_f32_16x16x32_bf16 v[224:227], v[224:227], v[196:199], 0\n\t"
"v_mfma_f32_16x16x32_bf16 v[82:85], a[48:51], a[184:187], 0\n\t"
"v_mfma_f32_16x16x32_bf16 v[82:85], a[56:59], a[200:203], v[82:85]\n\t"
"v_mfma_f32_16x16x32_bf16 v[82:85], a[64:67], a[204:207], v[82:85]\n\t"
"v_mfma_f32_16x16x32_bf16 v[116:119], a[72:75], a[168:171], 0\n\t"
"v_cvt_pk_bf16_f32 v228, v228, v229\n\t"
"v_cvt_pk_bf16_f32 v229, v230, v231\n\t"
"v_mfma_f32_16x16x32_bf16 v[116:119], a[80:83], a[176:179], v[116:119]\n\t"
"v_cvt_pk_bf16_f32 v230, v224, v225\n\t"
"v_mfma_f32_16x16x32_bf16 v[116:119], a[88:91], a[180:183], v[116:119]\n\t"
"s_waitcnt lgkmcnt(3)\n\t"
"s_barrier\n\t"
"v_mfma_f32_16x16x32_bf16 v[148:151], a[96:99], a[168:171], 0\n\t"
"s_waitcnt lgkmcnt(2)\n\t"
"ds_read_b128 a[188:191], v244 offset:28160\n\t"
"v_mfma_f32_16x16x32_bf16 v[148:151], a[104:107], a[176:179], v[148:151]\n\t"
"s_waitcnt lgkmcnt(1)\n\t"
"ds_read_b128 v[204:207], v244 offset:37376\n\t"
"v_cvt_pk_bf16_f32 v231, v226, v227\n\t"
"v_mfma_f32_16x16x32_bf16 v[148:151], a[112:115], a[180:183], v[148:151]\n\t"
"ds_write_b128 v242, v[228:231] offset:6144\n\t"
"v_mfma_f32_16x16x32_bf16 v[236:239], v[236:239], v[196:199], 0\n\t"
"v_mfma_f32_16x16x32_bf16 v[232:235], v[232:235], v[196:199], 0\n\t"
"ds_read_b128 a[192:195], v244 offset:28672\n\t"
"v_mfma_f32_16x16x32_bf16 v[180:183], a[120:123], a[168:171], 0\n\t"
"s_waitcnt lgkmcnt(3)\n\t"
"v_mfma_f32_16x16x32_bf16 v[180:183], a[128:131], a[176:179], v[180:183]\n\t"
"v_mfma_f32_16x16x32_bf16 v[180:183], a[136:139], a[180:183], v[180:183]\n\t"
"v_mfma_f32_16x16x32_bf16 v[22:25], a[0:3], a[200:203], 0\n\t"
"v_cvt_pk_bf16_f32 v236, v236, v237\n\t"
"v_cvt_pk_bf16_f32 v237, v238, v239\n\t"
"v_mfma_f32_16x16x32_bf16 v[22:25], a[8:11], a[204:207], v[22:25]\n\t"
"v_cvt_pk_bf16_f32 v238, v232, v233\n\t"
"v_cvt_pk_bf16_f32 v239, v234, v235\n\t"
"v_mfma_f32_16x16x32_bf16 v[22:25], a[16:19], a[188:191], v[22:25]\n\t"
"ds_write_b128 v242, v[236:239] offset:7680\n\t"
"v_mfma_f32_16x16x32_bf16 v[54:57], a[24:27], a[200:203], 0\n\t"
"v_mfma_f32_16x16x32_bf16 v[54:57], a[32:35], a[204:207], v[54:57]\n\t"
"ds_read_b128 v[196:199], v244 offset:37888\n\t"
"v_mfma_f32_16x16x32_bf16 v[54:57], a[40:43], a[188:191], v[54:57]\n\t"
"v_mfma_f32_16x16x32_bf16 v[86:89], a[48:51], a[200:203], 0\n\t"
"s_waitcnt lgkmcnt(4)\n\t"
"v_mfma_f32_16x16x32_bf16 v[86:89], a[56:59], a[204:207], v[86:89]\n\t"
"v_mfma_f32_16x16x32_bf16 v[86:89], a[64:67], a[188:191], v[86:89]\n\t"
"v_mfma_f32_16x16x32_bf16 v[120:123], a[72:75], a[176:179], 0\n\t"
"s_mov_b32 s20, 516096\n\t"
"v_mfma_f32_16x16x32_bf16 v[120:123], a[80:83], a[180:183], v[120:123]\n\t"
"v_mfma_f32_16x16x32_bf16 v[120:123], a[88:91], v[204:207], v[120:123]\n\t"
"v_mfma_f32_16x16x32_bf16 v[152:155], a[96:99], a[176:179], 0\n\t"
"ds_read_b128 a[196:199], v244 offset:29184\n\t"
"v_mfma_f32_16x16x32_bf16 v[152:155], a[104:107], a[180:183], v[152:155]\n\t"
"v_mfma_f32_16x16x32_bf16 v[152:155], a[112:115], v[204:207], v[152:155]\n\t"
"s_waitcnt lgkmcnt(2)\n\t"
"s_waitcnt vmcnt(0)\n\t"
"s_cmp_lg_u32 s73, 0\n\t"
"s_cbranch_scc1 Lgo_%=\n\t"
"s_cmp_lg_u32 s66, 0\n\t"
"s_cbranch_scc1 Lchk_%=\n\t"
"s_cmp_eq_u32 s68, -1\n\t"
"s_cbranch_scc1 Lgo_%=\n\t"
"s_branch Lfb_%=\n\t"
"Lchk_%=:\n\t"
"s_waitcnt vmcnt(0)\n\t"
"v_readfirstlane_b32 s60, v98\n\t"
"s_and_b32 s60, s60, 0xffff\n\t"
"s_cmp_eq_u32 s60, s67\n\t"
"s_cbranch_scc1 Lgo_%=\n\t"
"s_sub_u32 s68, s68, 1\n\t"
"s_cmp_eq_u32 s68, 0\n\t"
"s_cbranch_scc1 Lfb_%=\n\t"
"s_add_u32 s60, s66, 1835008\n\t"
"buffer_load_dword v99, off, s[8:11], s60 sc1\n\t"
"s_add_u32 s60, s66, 1867776\n\t"
"buffer_load_dword v98, off, s[8:11], s60 sc1\n\t"
"s_waitcnt vmcnt(1)\n\t"
"v_readfirstlane_b32 s60, v99\n\t"
"s_and_b32 s60, s60, 0xffff0000\n\t"
"s_or_b32 s60, s60, s67\n\t"
"s_cmp_eq_u32 s60, s71\n\t"
"s_cbranch_scc1 Lchk_%=\n\t"
"s_mov_b32 s71, s60\n\t"
"v_mov_b32 v99, s71\n\t"
"s_mov_b64 exec, 1\n\t"
"buffer_store_dword v99, off, s[8:11], s70 sc1\n\t"
"s_mov_b64 exec, -1\n\t"
"s_branch Lchk_%=\n\t"
"Lgo_%=:\n\t"
"s_barrier\n\t"
"s_mov_b32 s52, 0x3fd80000\n\t"
"s_mov_b32 s53, s52\n\t"
"s_mov_b32 s54, 0x3e97b426\n\t"
"s_mov_b32 s55, s54\n\t"
"s_mov_b32 s56, 0x3f400000\n\t"
"s_mov_b32 s57, s56\n\t"
"s_mov_b32 s58, 0x3f2aaaab\n\t"
"s_mov_b32 s59, s58\n\t"
"s_mov_b32 s60, 0x3ee38e39\n\t"
"s_mov_b32 s61, s60\n\t"
"s_mov_b32 s62, 0x40100000\n\t"
"s_mov_b32 s63, s62\n\t"
"s_mov_b32 s64, 0x3faaaaab\n\t"
"s_mov_b32 s65, s64\n\t"
"s_mov_b32 s66, 0x3f17b426\n\t"
"s_mov_b32 s67, s66\n\t"
"v_mfma_f32_16x16x32_bf16 v[184:187], a[120:123], a[176:179], 0\n\t"
"v_mfma_f32_16x16x32_bf16 v[184:187], a[128:131], a[180:183], v[184:187]\n\t"
"buffer_load_dwordx4 v[236:239], v252, s[8:11], s20 offen offset:0 sc1\n\t"
"v_mfma_f32_16x16x32_bf16 v[184:187], a[136:139], v[204:207], v[184:187]\n\t"
"v_mfma_f32_16x16x32_bf16 v[26:29], a[0:3], a[204:207], 0\n\t"
"buffer_load_dwordx4 a[144:147], v252, s[8:11], s20 offen offset:1024 sc1\n\t"
"v_mfma_f32_16x16x32_bf16 v[26:29], a[8:11], a[188:191], v[26:29]\n\t"
"v_mfma_f32_16x16x32_bf16 v[26:29], a[16:19], a[192:195], v[26:29]\n\t"
"v_mfma_f32_16x16x32_bf16 v[58:61], a[24:27], a[204:207], 0\n\t"
"buffer_load_dwordx4 a[152:155], v252, s[8:11], s20 offen offset:2048 sc1\n\t"
"v_mfma_f32_16x16x32_bf16 v[58:61], a[32:35], a[188:191], v[58:61]\n\t"
"s_mov_b32 s20, 528384\n\t"
"ds_read_b128 v[212:215], v244 offset:38400\n\t"
"v_mfma_f32_16x16x32_bf16 v[58:61], a[40:43], a[192:195], v[58:61]\n\t"
"v_mfma_f32_16x16x32_bf16 v[90:93], a[48:51], a[204:207], 0\n\t"
"buffer_load_dwordx4 a[160:163], v252, s[8:11], s20 offen offset:0 sc1\n\t"
"v_mfma_f32_16x16x32_bf16 v[90:93], a[56:59], a[188:191], v[90:93]\n\t"
"s_waitcnt lgkmcnt(2)\n\t"
"v_mfma_f32_16x16x32_bf16 v[90:93], a[64:67], a[192:195], v[90:93]\n\t"
"v_mfma_f32_16x16x32_bf16 v[124:127], a[72:75], a[180:183], 0\n\t"
"buffer_load_dwordx4 v[200:203], v252, s[8:11], s20 offen offset:1024 sc1\n\t"
"v_mfma_f32_16x16x32_bf16 v[124:127], a[80:83], v[204:207], v[124:127]\n\t"
"v_mfma_f32_16x16x32_bf16 v[124:127], a[88:91], v[196:199], v[124:127]\n\t"
"v_mfma_f32_16x16x32_bf16 v[156:159], a[96:99], a[180:183], 0\n\t"
"buffer_load_dwordx4 v[208:211], v252, s[8:11], s20 offen offset:2048 sc1\n\t"
"v_mfma_f32_16x16x32_bf16 v[156:159], a[104:107], v[204:207], v[156:159]\n\t"
"s_mov_b32 s20, 540672\n\t"
"v_mfma_f32_16x16x32_bf16 v[156:159], a[112:115], v[196:199], v[156:159]\n\t"
"v_mfma_f32_16x16x32_bf16 v[188:191], a[120:123], a[180:183], 0\n\t"
"buffer_load_dwordx4 v[216:219], v252, s[8:11], s20 offen offset:0 sc1\n\t"
"v_mfma_f32_16x16x32_bf16 v[188:191], a[128:131], v[204:207], v[188:191]\n\t"
"s_waitcnt lgkmcnt(1)\n\t"
"ds_read_b128 a[168:171], v244 offset:0\n\t"
"v_mfma_f32_16x16x32_bf16 v[188:191], a[136:139], v[196:199], v[188:191]\n\t"
"ds_read_b128 a[172:175], v244 offset:512\n\t"
"v_mfma_f32_16x16x32_bf16 v[30:33], a[0:3], a[188:191], 0\n\t"
"buffer_load_dwordx4 a[0:3], v252, s[8:11], s20 offen offset:1024 sc1\n\t"
"v_mfma_f32_16x16x32_bf16 v[30:33], a[8:11], a[192:195], v[30:33]\n\t"
"ds_read_b128 a[176:179], v244 offset:1024\n\t"
"v_mfma_f32_16x16x32_bf16 v[30:33], a[16:19], a[196:199], v[30:33]\n\t"
"ds_read_b128 a[180:183], v244 offset:1536\n\t"
"v_mfma_f32_16x16x32_bf16 v[62:65], a[24:27], a[188:191], 0\n\t"
"buffer_load_dwordx4 a[8:11], v252, s[8:11], s20 offen offset:2048 sc1\n\t"
"v_mfma_f32_16x16x32_bf16 v[62:65], a[32:35], a[192:195], v[62:65]\n\t"
"s_mov_b32 s20, 552960\n\t"
"ds_read_b128 a[184:187], v244 offset:2048\n\t"
"v_mfma_f32_16x16x32_bf16 v[62:65], a[40:43], a[196:199], v[62:65]\n\t"
"v_mfma_f32_16x16x32_bf16 v[94:97], a[48:51], a[188:191], 0\n\t"
"buffer_load_dwordx4 a[16:19], v252, s[8:11], s20 offen offset:0 sc1\n\t"
"v_mfma_f32_16x16x32_bf16 v[94:97], a[56:59], a[192:195], v[94:97]\n\t"
"s_waitcnt lgkmcnt(5)\n\t"
"ds_read_b128 a[188:191], v244 offset:2560\n\t"
"v_mfma_f32_16x16x32_bf16 v[94:97], a[64:67], a[196:199], v[94:97]\n\t"
"v_mfma_f32_16x16x32_bf16 v[128:131], a[72:75], v[204:207], 0\n\t"
"buffer_load_dwordx4 a[24:27], v252, s[8:11], s20 offen offset:1024 sc1\n\t"
"v_mfma_f32_16x16x32_bf16 v[128:131], a[80:83], v[196:199], v[128:131]\n\t"
"ds_read_b128 a[192:195], v244 offset:3072\n\t"
"v_mfma_f32_16x16x32_bf16 v[128:131], a[88:91], v[212:215], v[128:131]\n\t"
"v_mfma_f32_16x16x32_bf16 v[160:163], a[96:99], v[204:207], 0\n\t"
"buffer_load_dwordx4 a[32:35], v252, s[8:11], s20 offen offset:2048 sc1\n\t"
"v_mfma_f32_16x16x32_bf16 v[160:163], a[104:107], v[196:199], v[160:163]\n\t"
"s_mov_b32 s20, 565248\n\t"
"ds_read_b128 a[196:199], v244 offset:3584\n\t"
"v_mfma_f32_16x16x32_bf16 v[160:163], a[112:115], v[212:215], v[160:163]\n\t"
"v_mfma_f32_16x16x32_bf16 v[192:195], a[120:123], v[204:207], 0\n\t"
"buffer_load_dwordx4 a[40:43], v252, s[8:11], s20 offen offset:0 sc1\n\t"
"v_mfma_f32_16x16x32_bf16 v[192:195], a[128:131], v[196:199], v[192:195]\n\t"
"s_waitcnt vmcnt(10) lgkmcnt(5)\n\t"
"ds_read_b128 a[200:203], v244 offset:4096\n\t"
"v_mfma_f32_16x16x32_bf16 v[192:195], a[136:139], v[212:215], v[192:195]\n\t"
"Lst1_%=:\n\t"
"v_mfma_f32_16x16x32_bf16 v[2:5], v[236:239], a[168:171], v[2:5]\n\t"
"buffer_load_dwordx4 a[48:51], v252, s[8:11], s20 offen offset:1024 sc1\n\t"
"v_mfma_f32_16x16x32_bf16 v[2:5], a[144:147], a[172:175], v[2:5]\n\t"
"s_waitcnt lgkmcnt(5)\n\t"
"ds_read_b128 a[204:207], v244 offset:4608\n\t"
"v_mfma_f32_16x16x32_bf16 v[2:5], a[152:155], a[176:179], v[2:5]\n\t"
"v_mfma_f32_16x16x32_bf16 v[6:9], v[236:239], a[172:175], v[6:9]\n\t"
"buffer_load_dwordx4 a[56:59], v252, s[8:11], s20 offen offset:2048 sc1\n\t"
"v_mfma_f32_16x16x32_bf16 v[6:9], a[144:147], a[176:179], v[6:9]\n\t"
"s_waitcnt lgkmcnt(5)\n\t"
"s_mov_b32 s20, 577536\n\t"
"v_mfma_f32_16x16x32_bf16 v[6:9], a[152:155], a[180:183], v[6:9]\n\t"
"v_mfma_f32_16x16x32_bf16 v[10:13], v[236:239], a[176:179], v[10:13]\n\t"
"buffer_load_dwordx4 a[80:83], v252, s[8:11], s20 offen offset:0 sc1\n\t"
"v_mfma_f32_16x16x32_bf16 v[10:13], a[144:147], a[180:183], v[10:13]\n\t"
"s_waitcnt lgkmcnt(4)\n\t"
"v_mfma_f32_16x16x32_bf16 v[10:13], a[152:155], a[184:187], v[10:13]\n\t"
"v_mfma_f32_16x16x32_bf16 v[14:17], v[236:239], a[180:183], v[14:17]\n\t"
"buffer_load_dwordx4 a[88:91], v252, s[8:11], s20 offen offset:1024 sc1\n\t"
"v_mfma_f32_16x16x32_bf16 v[14:17], a[144:147], a[184:187], v[14:17]\n\t"
"s_waitcnt lgkmcnt(3)\n\t"
"v_mfma_f32_16x16x32_bf16 v[14:17], a[152:155], a[188:191], v[14:17]\n\t"
"v_mfma_f32_16x16x32_bf16 v[18:21], v[236:239], a[184:187], v[18:21]\n\t"
"buffer_load_dwordx4 a[96:99], v252, s[8:11], s20 offen offset:2048 sc1\n\t"
"v_mfma_f32_16x16x32_bf16 v[18:21], a[144:147], a[188:191], v[18:21]\n\t"
"s_waitcnt lgkmcnt(2)\n\t"
"s_mov_b32 s20, 110592\n\t"
"v_xor_b32 v246, 0xc0, v246\n\t"
"v_mfma_f32_16x16x32_bf16 v[18:21], a[152:155], a[192:195], v[18:21]\n\t"
"v_mfma_f32_16x16x32_bf16 v[22:25], v[236:239], a[188:191], v[22:25]\n\t"
"buffer_load_dwordx4 a[136:139], v252, s[8:11], s20 offen offset:0 sc1\n\t"
"v_mfma_f32_16x16x32_bf16 v[22:25], a[144:147], a[192:195], v[22:25]\n\t"
"s_waitcnt lgkmcnt(1)\n\t"
"v_mfma_f32_16x16x32_bf16 v[22:25], a[152:155], a[196:199], v[22:25]\n\t"
"v_mfma_f32_16x16x32_bf16 v[26:29], v[236:239], a[192:195], v[26:29]\n\t"
"buffer_load_dwordx4 a[4:7], v252, s[8:11], s20 offen offset:1024 sc1\n\t"
"v_mfma_f32_16x16x32_bf16 v[26:29], a[144:147], a[196:199], v[26:29]\n\t"
"s_waitcnt lgkmcnt(0)\n\t"
"v_mfma_f32_16x16x32_bf16 v[26:29], a[152:155], a[200:203], v[26:29]\n\t"
"v_mfma_f32_16x16x32_bf16 v[30:33], v[236:239], a[196:199], v[30:33]\n\t"
"buffer_load_dwordx4 a[12:15], v252, s[8:11], s20 offen offset:2048 sc1\n\t"
"v_mfma_f32_16x16x32_bf16 v[30:33], a[144:147], a[200:203], v[30:33]\n\t"
"s_waitcnt vmcnt(15)\n\t"
"s_mov_b32 s20, 122880\n\t"
"v_mfma_f32_16x16x32_bf16 v[30:33], a[152:155], a[204:207], v[30:33]\n\t"
"v_mfma_f32_16x16x32_bf16 v[34:37], a[160:163], a[168:171], v[34:37]\n\t"
"buffer_load_dwordx4 a[20:23], v252, s[8:11], s20 offen offset:0 sc1\n\t"
"v_mfma_f32_16x16x32_bf16 v[34:37], v[200:203], a[172:175], v[34:37]\n\t"
"v_mfma_f32_16x16x32_bf16 v[34:37], v[208:211], a[176:179], v[34:37]\n\t"
"ds_read_b64_tr_b16 v[224:225], v246 offset:0\n\t"
"v_mfma_f32_16x16x32_bf16 v[38:41], a[160:163], a[172:175], v[38:41]\n\t"
"buffer_load_dwordx4 a[28:31], v252, s[8:11], s20 offen offset:1024 sc1\n\t"
"v_mfma_f32_16x16x32_bf16 v[38:41], v[200:203], a[176:179], v[38:41]\n\t"
"ds_read_b64_tr_b16 v[226:227], v246 offset:3072\n\t"
"v_mfma_f32_16x16x32_bf16 v[38:41], v[208:211], a[180:183], v[38:41]\n\t"
"v_mfma_f32_16x16x32_bf16 v[42:45], a[160:163], a[176:179], v[42:45]\n\t"
"buffer_load_dwordx4 a[36:39], v252, s[8:11], s20 offen offset:2048 sc1\n\t"
"v_mfma_f32_16x16x32_bf16 v[42:45], v[200:203], a[180:183], v[42:45]\n\t"
"s_mov_b32 s20, 135168\n\t"
"v_mfma_f32_16x16x32_bf16 v[42:45], v[208:211], a[184:187], v[42:45]\n\t"
"v_mfma_f32_16x16x32_bf16 v[46:49], a[160:163], a[180:183], v[46:49]\n\t"
"buffer_load_dwordx4 a[76:79], v252, s[8:11], s20 offen offset:0 sc1\n\t"
"v_mfma_f32_16x16x32_bf16 v[46:49], v[200:203], a[184:187], v[46:49]\n\t"
"ds_read_b64_tr_b16 v[228:229], v246 offset:6144\n\t"
"v_mfma_f32_16x16x32_bf16 v[46:49], v[208:211], a[188:191], v[46:49]\n\t"
"ds_read_b64_tr_b16 v[230:231], v246 offset:9216\n\t"
"v_mfma_f32_16x16x32_bf16 v[50:53], a[160:163], a[184:187], v[50:53]\n\t"
"buffer_load_dwordx4 a[84:87], v252, s[8:11], s20 offen offset:1024 sc1\n\t"
"v_mfma_f32_16x16x32_bf16 v[50:53], v[200:203], a[188:191], v[50:53]\n\t"
"v_mfma_f32_16x16x32_bf16 v[50:53], v[208:211], a[192:195], v[50:53]\n\t"
"v_mfma_f32_16x16x32_bf16 v[54:57], a[160:163], a[188:191], v[54:57]\n\t"
"buffer_load_dwordx4 a[92:95], v252, s[8:11], s20 offen offset:2048 sc1\n\t"
"v_mfma_f32_16x16x32_bf16 v[54:57], v[200:203], a[192:195], v[54:57]\n\t"
"s_waitcnt lgkmcnt(2)\n\t"
"s_mov_b32 s20, 147456\n\t"
"v_mfma_f32_16x16x32_bf16 v[54:57], v[208:211], a[196:199], v[54:57]\n\t"
"v_mfma_f32_16x16x32_bf16 v[224:227], v[224:227], v[220:223], 0\n\t"
"buffer_load_dwordx4 a[148:151], v252, s[8:11], s20 offen offset:0 sc1\n\t"
"v_mfma_f32_16x16x32_bf16 v[58:61], a[160:163], a[192:195], v[58:61]\n\t"
"ds_read_b64_tr_b16 v[232:233], v246 offset:12288\n\t"
"v_mfma_f32_16x16x32_bf16 v[58:61], v[200:203], a[196:199], v[58:61]\n\t"
"ds_read_b64_tr_b16 v[234:235], v246 offset:15360\n\t"
"v_mfma_f32_16x16x32_bf16 v[58:61], v[208:211], a[200:203], v[58:61]\n\t"
"v_mfma_f32_16x16x32_bf16 v[62:65], a[160:163], a[196:199], v[62:65]\n\t"
"buffer_load_dwordx4 a[156:159], v252, s[8:11], s20 offen offset:1024 sc1\n\t"
"v_mfma_f32_16x16x32_bf16 v[62:65], v[200:203], a[200:203], v[62:65]\n\t"
"s_waitcnt vmcnt(20) lgkmcnt(2)\n\t"
"v_cvt_pk_bf16_f32 v224, v224, v225\n\t"
"v_cvt_pk_bf16_f32 v225, v226, v227\n\t"
"v_mfma_f32_16x16x32_bf16 v[62:65], v[208:211], a[204:207], v[62:65]\n\t"
"v_mfma_f32_16x16x32_bf16 v[228:231], v[228:231], v[220:223], 0\n\t"
"v_mfma_f32_16x16x32_bf16 v[66:69], v[216:219], a[168:171], v[66:69]\n\t"
"buffer_load_dwordx4 a[164:167], v252, s[8:11], s20 offen offset:2048 sc1\n\t"
"v_mfma_f32_16x16x32_bf16 v[66:69], a[0:3], a[172:175], v[66:69]\n\t"
"s_mov_b32 s20, 159744\n\t"
"ds_read_b64_tr_b16 v[236:237], v246 offset:19968\n\t"
"v_mfma_f32_16x16x32_bf16 v[66:69], a[8:11], a[176:179], v[66:69]\n\t"
"ds_read_b64_tr_b16 v[238:239], v246 offset:23040\n\t"
"v_mfma_f32_16x16x32_bf16 v[70:73], v[216:219], a[172:175], v[70:73]\n\t"
"ds_read_b128 a[168:171], v244 offset:9216\n\t"
"v_mfma_f32_16x16x32_bf16 v[70:73], a[0:3], a[176:179], v[70:73]\n\t"
"buffer_load_dwordx4 v[240:243], v252, s[8:11], s20 offen offset:0 sc1\n\t"
"v_mfma_f32_16x16x32_bf16 v[70:73], a[8:11], a[180:183], v[70:73]\n\t"
"ds_read_b128 a[172:175], v244 offset:9728\n\t"
"v_cvt_pk_bf16_f32 v228, v228, v229\n\t"
"v_mfma_f32_16x16x32_bf16 v[74:77], v[216:219], a[176:179], v[74:77]\n\t"
"s_waitcnt lgkmcnt(4)\n\t"
"ds_read_b128 a[176:179], v244 offset:10240\n\t"
"v_cvt_pk_bf16_f32 v229, v230, v231\n\t"
"v_mfma_f32_16x16x32_bf16 v[74:77], a[0:3], a[180:183], v[74:77]\n\t"
"v_mfma_f32_16x16x32_bf16 v[74:77], a[8:11], a[184:187], v[74:77]\n\t"
"buffer_load_dwordx4 a[208:211], v252, s[8:11], s20 offen offset:1024 sc1\n\t"
"v_mfma_f32_16x16x32_bf16 v[232:235], v[232:235], v[220:223], 0\n\t"
"v_mfma_f32_16x16x32_bf16 v[78:81], v[216:219], a[180:183], v[78:81]\n\t"
"ds_read_b128 a[180:183], v244 offset:10752\n\t"
"v_mfma_f32_16x16x32_bf16 v[78:81], a[0:3], a[184:187], v[78:81]\n\t"
"v_mfma_f32_16x16x32_bf16 v[78:81], a[8:11], a[188:191], v[78:81]\n\t"
"buffer_load_dwordx4 v[248:251], v252, s[8:11], s20 offen offset:2048 sc1\n\t"
"v_mfma_f32_16x16x32_bf16 v[82:85], v[216:219], a[184:187], v[82:85]\n\t"
"s_waitcnt lgkmcnt(4)\n\t"
"ds_read_b128 a[184:187], v244 offset:11264\n\t"
"s_mov_b32 s20, 172032\n\t"
"v_mfma_f32_16x16x32_bf16 v[82:85], a[0:3], a[188:191], v[82:85]\n\t"
"v_cvt_pk_bf16_f32 v232, v232, v233\n\t"
"v_cvt_pk_bf16_f32 v233, v234, v235\n\t"
"v_mfma_f32_16x16x32_bf16 v[82:85], a[8:11], a[192:195], v[82:85]\n\t"
"v_mfma_f32_16x16x32_bf16 v[236:239], v[236:239], v[220:223], 0\n\t"
"buffer_load_dwordx4 a[212:215], v252, s[8:11], s20 offen offset:0 sc1\n\t"
"v_mfma_f32_16x16x32_bf16 v[86:89], v[216:219], a[188:191], v[86:89]\n\t"
"ds_read_b128 a[188:191], v244 offset:11776\n\t"
"v_mfma_f32_16x16x32_bf16 v[86:89], a[0:3], a[192:195], v[86:89]\n\t"
"v_mfma_f32_16x16x32_bf16 v[86:89], a[8:11], a[196:199], v[86:89]\n\t"
"v_mfma_f32_16x16x32_bf16 v[90:93], v[216:219], a[192:195], v[90:93]\n\t"
"buffer_load_dwordx4 a[216:219], v252, s[8:11], s20 offen offset:1024 sc1\n\t"
"v_mfma_f32_16x16x32_bf16 v[90:93], a[0:3], a[196:199], v[90:93]\n\t"
"ds_read_b128 a[192:195], v244 offset:12288\n\t"
"v_cvt_pk_bf16_f32 v236, v236, v237\n\t"
"v_mfma_f32_16x16x32_bf16 v[90:93], a[8:11], a[200:203], v[90:93]\n\t"
"v_cvt_pk_bf16_f32 v237, v238, v239\n\t"
"v_mfma_f32_16x16x32_bf16 v[94:97], v[216:219], a[196:199], v[94:97]\n\t"
"s_waitcnt vmcnt(23) lgkmcnt(4)\n\t"
"ds_read_b128 a[196:199], v244 offset:12800\n\t"
"v_mfma_f32_16x16x32_bf16 v[94:97], a[0:3], a[200:203], v[94:97]\n\t"
"ds_read_b128 a[200:203], v244 offset:13312\n\t"
"v_mfma_f32_16x16x32_bf16 v[94:97], a[8:11], a[204:207], v[94:97]\n\t"
"ds_read_b128 a[204:207], v244 offset:13824\n\t"
"v_mfma_f32_16x16x32_bf16 v[100:103], a[16:19], a[168:171], v[100:103]\n\t"
"buffer_load_dwordx4 a[220:223], v252, s[8:11], s20 offen offset:2048 sc1\n\t"
"v_mfma_f32_16x16x32_bf16 v[100:103], a[24:27], a[172:175], v[100:103]\n\t"
"s_waitcnt lgkmcnt(6)\n\t"
"s_mov_b32 s20, 36864\n\t"
"v_mfma_f32_16x16x32_bf16 v[100:103], a[32:35], a[176:179], v[100:103]\n\t"
"buffer_load_dwordx4 a[224:227], v252, s[8:11], s20 offen offset:0 sc1\n\t"
"v_mfma_f32_16x16x32_bf16 v[104:107], a[16:19], a[172:175], v[104:107]\n\t"
"s_waitcnt lgkmcnt(5)\n\t"
"v_mfma_f32_16x16x32_bf16 v[104:107], a[24:27], a[176:179], v[104:107]\n\t"
"v_mfma_f32_16x16x32_bf16 v[104:107], a[32:35], a[180:183], v[104:107]\n\t"
"s_waitcnt lgkmcnt(0)\n\t"
"s_barrier\n\t"
"v_mfma_f32_16x16x32_bf16 v[108:111], a[16:19], a[176:179], v[108:111]\n\t"
"v_fma_f32 v196, -v2, s56, -v100\n\t"
"v_mfma_f32_16x16x32_bf16 v[108:111], a[24:27], a[180:183], v[108:111]\n\t"
"buffer_load_dwordx4 a[228:231], v252, s[8:11], s20 offen offset:1024 sc1\n\t"
"v_mfma_f32_16x16x32_bf16 v[108:111], a[32:35], a[184:187], v[108:111]\n\t"
"ds_write_b64 v245, v[224:225] offset:0\n\t"
"v_mfma_f32_16x16x32_bf16 v[112:115], a[16:19], a[180:183], v[112:115]\n\t"
"ds_read_b64_tr_b16 v[224:225], v246 offset:26112\n\t"
"v_fma_f32 v197, -v3, s56, -v101\n\t"
"v_mfma_f32_16x16x32_bf16 v[112:115], a[24:27], a[184:187], v[112:115]\n\t"
"ds_read_b64_tr_b16 v[226:227], v246 offset:29184\n\t"
"v_fma_f32 v198, -v4, s56, -v102\n\t"
"v_mfma_f32_16x16x32_bf16 v[112:115], a[32:35], a[188:191], v[112:115]\n\t"
"ds_write_b64 v245, v[228:229] offset:512\n\t"
"v_mfma_f32_16x16x32_bf16 v[116:119], a[16:19], a[184:187], v[116:119]\n\t"
"buffer_load_dwordx4 a[232:235], v252, s[8:11], s20 offen offset:2048 sc1\n\t"
"v_mfma_f32_16x16x32_bf16 v[116:119], a[24:27], a[188:191], v[116:119]\n\t"
"s_mov_b32 s20, 49152\n\t"
"ds_read_b64_tr_b16 v[228:229], v246 offset:32256\n\t"
"v_fma_f32 v199, -v5, s56, -v103\n\t"
"v_mfma_f32_16x16x32_bf16 v[116:119], a[32:35], a[192:195], v[116:119]\n\t"
"ds_write_b64 v245, v[232:233] offset:1024\n\t"
"v_mfma_f32_16x16x32_bf16 v[120:123], a[16:19], a[188:191], v[120:123]\n\t"
"buffer_load_dwordx4 a[236:239], v252, s[8:11], s20 offen offset:0 sc1\n\t"
"v_mfma_f32_16x16x32_bf16 v[120:123], a[24:27], a[192:195], v[120:123]\n\t"
"s_waitcnt lgkmcnt(3)\n\t"
"ds_read_b64_tr_b16 v[230:231], v246 offset:35328\n\t"
"v_fma_f32 v204, -v6, s56, -v104\n\t"
"v_mfma_f32_16x16x32_bf16 v[120:123], a[32:35], a[196:199], v[120:123]\n\t"
"ds_write_b64 v245, v[236:237] offset:1536\n\t"
"v_mfma_f32_16x16x32_bf16 v[224:227], v[224:227], v[220:223], 0\n\t"
"buffer_load_dwordx4 a[240:243], v252, s[8:11], s20 offen offset:1024 sc1\n\t"
"v_mfma_f32_16x16x32_bf16 v[124:127], a[16:19], a[192:195], v[124:127]\n\t"
"ds_read_b64_tr_b16 v[232:233], v246 offset:39936\n\t"
"v_fma_f32 v205, -v7, s56, -v105\n\t"
"v_mfma_f32_16x16x32_bf16 v[124:127], a[24:27], a[196:199], v[124:127]\n\t"
"ds_read_b64_tr_b16 v[234:235], v246 offset:43008\n\t"
"v_fma_f32 v206, -v8, s56, -v106\n\t"
"v_mfma_f32_16x16x32_bf16 v[124:127], a[32:35], a[200:203], v[124:127]\n\t"
"s_waitcnt lgkmcnt(3)\n\t"
"buffer_load_dwordx4 a[244:247], v252, s[8:11], s20 offen offset:2048 sc1\n\t"
"s_mov_b32 s20, 61440\n\t"
"v_mfma_f32_16x16x32_bf16 v[228:231], v[228:231], v[220:223], 0\n\t"
"ds_read_b64_tr_b16 v[236:237], v246 offset:46080\n\t"
"v_fma_f32 v207, -v9, s56, -v107\n\t"
"v_mfma_f32_16x16x32_bf16 v[128:131], a[16:19], a[196:199], v[128:131]\n\t"
"ds_read_b64_tr_b16 v[238:239], v246 offset:49152\n\t"
"v_cvt_pk_bf16_f32 v224, v224, v225\n\t"
"v_mfma_f32_16x16x32_bf16 v[128:131], a[24:27], a[200:203], v[128:131]\n\t"
"s_waitcnt vmcnt(27)\n\t"
"v_cvt_pk_bf16_f32 v225, v226, v227\n\t"
"v_fma_f32 v212, -v10, s56, -v108\n\t"
"v_mfma_f32_16x16x32_bf16 v[128:131], a[32:35], a[204:207], v[128:131]\n\t"
"s_waitcnt lgkmcnt(2)\n\t"
"buffer_load_dwordx4 a[248:251], v252, s[8:11], s20 offen offset:0 sc1\n\t"
"v_mfma_f32_16x16x32_bf16 v[232:235], v[232:235], v[220:223], 0\n\t"
"buffer_load_dwordx4 a[252:255], v252, s[8:11], s20 offen offset:1024 sc1\n\t"
"v_mfma_f32_16x16x32_bf16 v[132:135], a[40:43], a[168:171], v[132:135]\n\t"
"v_cvt_pk_bf16_f32 v228, v228, v229\n\t"
"v_cvt_pk_bf16_f32 v229, v230, v231\n\t"
"v_mfma_f32_16x16x32_bf16 v[132:135], a[48:51], a[172:175], v[132:135]\n\t"
"s_waitcnt lgkmcnt(0)\n\t"
"v_fma_f32 v213, -v11, s56, -v109\n\t"
"v_fma_f32 v214, -v12, s56, -v110\n\t"
"v_mfma_f32_16x16x32_bf16 v[132:135], a[56:59], a[176:179], v[132:135]\n\t"
"ds_write_b64 v245, v[224:225] offset:2048\n\t"
"v_mfma_f32_16x16x32_bf16 v[236:239], v[236:239], v[220:223], 0\n\t"
"ds_read_b64_tr_b16 v[224:225], v246 offset:52224\n\t"
"v_fma_f32 v215, -v13, s56, -v111\n\t"
"v_mfma_f32_16x16x32_bf16 v[136:139], a[40:43], a[172:175], v[136:139]\n\t"
"ds_read_b64_tr_b16 v[226:227], v246 offset:55296\n\t"
"v_cvt_pk_bf16_f32 v232, v232, v233\n\t"
"v_mfma_f32_16x16x32_bf16 v[136:139], a[48:51], a[176:179], v[136:139]\n\t"
"v_cvt_pk_bf16_f32 v233, v234, v235\n\t"
"v_fma_f32 v98, -v14, s56, -v112\n\t"
"v_mfma_f32_16x16x32_bf16 v[136:139], a[56:59], a[180:183], v[136:139]\n\t"
"ds_write_b64 v245, v[228:229] offset:2560\n\t"
"v_mfma_f32_16x16x32_bf16 v[140:143], a[40:43], a[176:179], v[140:143]\n\t"
"v_fma_f32 v99, -v15, s56, -v113\n\t"
"v_accvgpr_write_b32 a0, v98\n\t"
"v_mfma_f32_16x16x32_bf16 v[140:143], a[48:51], a[180:183], v[140:143]\n\t"
"v_cvt_pk_bf16_f32 v236, v236, v237\n\t"
"v_cvt_pk_bf16_f32 v237, v238, v239\n\t"
"v_mfma_f32_16x16x32_bf16 v[140:143], a[56:59], a[184:187], v[140:143]\n\t"
"ds_write_b64 v245, v[232:233] offset:3072\n\t"
"v_mfma_f32_16x16x32_bf16 v[144:147], a[40:43], a[180:183], v[144:147]\n\t"
"s_waitcnt lgkmcnt(2)\n\t"
"v_fma_f32 v98, -v16, s56, -v114\n\t"
"v_accvgpr_write_b32 a1, v99\n\t"
"v_mfma_f32_16x16x32_bf16 v[144:147], a[48:51], a[184:187], v[144:147]\n\t"
"v_fma_f32 v99, -v17, s56, -v115\n\t"
"v_accvgpr_write_b32 a2, v98\n\t"
"v_mfma_f32_16x16x32_bf16 v[144:147], a[56:59], a[188:191], v[144:147]\n\t"
"ds_write_b64 v245, v[236:237] offset:3584\n\t"
"v_mfma_f32_16x16x32_bf16 v[224:227], v[224:227], v[220:223], 0\n\t"
"v_accvgpr_write_b32 a3, v99\n\t"
"v_fma_f32 v98, -v18, s56, -v116\n\t"
"v_mfma_f32_16x16x32_bf16 v[148:151], a[40:43], a[184:187], v[148:151]\n\t"
"v_fma_f32 v99, -v19, s56, -v117\n\t"
"v_accvgpr_write_b32 a8, v98\n\t"
"v_mfma_f32_16x16x32_bf16 v[148:151], a[48:51], a[188:191], v[148:151]\n\t"
"v_fma_f32 v98, -v20, s56, -v118\n\t"
"v_accvgpr_write_b32 a9, v99\n\t"
"v_mfma_f32_16x16x32_bf16 v[148:151], a[56:59], a[192:195], v[148:151]\n\t"
"v_fma_f32 v99, -v21, s56, -v119\n\t"
"v_accvgpr_write_b32 a10, v98\n\t"
"v_mfma_f32_16x16x32_bf16 v[152:155], a[40:43], a[188:191], v[152:155]\n\t"
"v_accvgpr_write_b32 a11, v99\n\t"
"v_fma_f32 v98, -v22, s56, -v120\n\t"
"v_mfma_f32_16x16x32_bf16 v[152:155], a[48:51], a[192:195], v[152:155]\n\t"
"s_waitcnt vmcnt(26)\n\t"
"v_cvt_pk_bf16_f32 v224, v224, v225\n\t"
"v_cvt_pk_bf16_f32 v225, v226, v227\n\t"
"v_mfma_f32_16x16x32_bf16 v[152:155], a[56:59], a[196:199], v[152:155]\n\t"
"Lpg2_%=:\n\t"
"s_cbranch_vccnz Lpg6_%=\n\t"
"buffer_load_dwordx4 v[236:239], v252, s[8:11], s20 offen offset:2048 sc1\n\t"
"v_mfma_f32_16x16x32_bf16 v[156:159], a[40:43], a[192:195], v[156:159]\n\t"
"v_fma_f32 v99, -v23, s56, -v121\n\t"
"v_accvgpr_write_b32 a16, v98\n\t"
"v_mfma_f32_16x16x32_bf16 v[156:159], a[48:51], a[196:199], v[156:159]\n\t"
"v_fma_f32 v98, -v24, s56, -v122\n\t"
"v_accvgpr_write_b32 a17, v99\n\t"
"v_mfma_f32_16x16x32_bf16 v[156:159], a[56:59], a[200:203], v[156:159]\n\t"
"ds_write_b64 v245, v[224:225] offset:4096\n\t"
"v_mfma_f32_16x16x32_bf16 v[160:163], a[40:43], a[196:199], v[160:163]\n\t"
"v_fma_f32 v99, -v25, s56, -v123\n\t"
"v_accvgpr_write_b32 a18, v98\n\t"
"v_mfma_f32_16x16x32_bf16 v[160:163], a[48:51], a[200:203], v[160:163]\n\t"
"v_accvgpr_write_b32 a19, v99\n\t"
"v_fma_f32 v98, -v26, s56, -v124\n\t"
"v_mfma_f32_16x16x32_bf16 v[160:163], a[56:59], a[204:207], v[160:163]\n\t"
"v_fma_f32 v99, -v27, s56, -v125\n\t"
"v_accvgpr_write_b32 a24, v98\n\t"
"v_mfma_f32_16x16x32_bf16 v[164:167], a[80:83], a[168:171], v[164:167]\n\t"
"v_fma_f32 v98, -v28, s56, -v126\n\t"
"v_accvgpr_write_b32 a25, v99\n\t"
"v_mfma_f32_16x16x32_bf16 v[164:167], a[88:91], a[172:175], v[164:167]\n\t"
"v_fma_f32 v99, -v29, s56, -v127\n\t"
"v_mfma_f32_16x16x32_bf16 v[164:167], a[96:99], a[176:179], v[164:167]\n\t"
"s_waitcnt lgkmcnt(0)\n\t"
"s_barrier\n\t"
"v_mfma_f32_16x16x32_bf16 v[168:171], a[80:83], a[172:175], v[168:171]\n\t"
"ds_read_b128 a[168:171], v244 offset:0\n\t"
"v_mfma_f32_16x16x32_bf16 v[168:171], a[88:91], a[176:179], v[168:171]\n\t"
"ds_read_b128 a[172:175], v244 offset:512\n\t"
"v_accvgpr_write_b32 a26, v98\n\t"
"v_mfma_f32_16x16x32_bf16 v[168:171], a[96:99], a[180:183], v[168:171]\n\t"
"v_accvgpr_write_b32 a27, v99\n\t"
"v_fma_f32 v98, -v30, s56, -v128\n\t"
"v_mfma_f32_16x16x32_bf16 v[172:175], a[80:83], a[176:179], v[172:175]\n\t"
"ds_read_b128 a[176:179], v244 offset:1024\n\t"
"v_fma_f32 v99, -v31, s56, -v129\n\t"
"v_mfma_f32_16x16x32_bf16 v[172:175], a[88:91], a[180:183], v[172:175]\n\t"
"v_accvgpr_write_b32 a32, v98\n\t"
"v_fma_f32 v98, -v32, s56, -v130\n\t"
"v_mfma_f32_16x16x32_bf16 v[172:175], a[96:99], a[184:187], v[172:175]\n\t"
"v_accvgpr_write_b32 a33, v99\n\t"
"v_fma_f32 v99, -v33, s56, -v131\n\t"
"v_mfma_f32_16x16x32_bf16 v[176:179], a[80:83], a[180:183], v[176:179]\n\t"
"ds_read_b128 a[180:183], v244 offset:1536\n\t"
"v_accvgpr_write_b32 a34, v98\n\t"
"v_mfma_f32_16x16x32_bf16 v[176:179], a[88:91], a[184:187], v[176:179]\n\t"
"v_accvgpr_write_b32 a35, v99\n\t"
"v_fma_f32 v98, -v34, s56, -v132\n\t"
"v_mfma_f32_16x16x32_bf16 v[176:179], a[96:99], a[188:191], v[176:179]\n\t"
"v_fma_f32 v99, -v35, s56, -v133\n\t"
"v_accvgpr_write_b32 a40, v98\n\t"
"v_mfma_f32_16x16x32_bf16 v[180:183], a[80:83], a[184:187], v[180:183]\n\t"
"ds_read_b128 a[184:187], v244 offset:2048\n\t"
"v_fma_f32 v98, -v36, s56, -v134\n\t"
"v_mfma_f32_16x16x32_bf16 v[180:183], a[88:91], a[188:191], v[180:183]\n\t"
"v_accvgpr_write_b32 a41, v99\n\t"
"v_fma_f32 v99, -v37, s56, -v135\n\t"
"v_mfma_f32_16x16x32_bf16 v[180:183], a[96:99], a[192:195], v[180:183]\n\t"
"v_accvgpr_write_b32 a42, v98\n\t"
"v_accvgpr_write_b32 a43, v99\n\t"
"v_mfma_f32_16x16x32_bf16 v[184:187], a[80:83], a[188:191], v[184:187]\n\t"
"ds_read_b128 a[188:191], v244 offset:2560\n\t"
"v_fma_f32 v98, -v38, s56, -v136\n\t"
"v_mfma_f32_16x16x32_bf16 v[184:187], a[88:91], a[192:195], v[184:187]\n\t"
"v_fma_f32 v99, -v39, s56, -v137\n\t"
"v_accvgpr_write_b32 a48, v98\n\t"
"v_mfma_f32_16x16x32_bf16 v[184:187], a[96:99], a[196:199], v[184:187]\n\t"
"v_fma_f32 v98, -v40, s56, -v138\n\t"
"v_accvgpr_write_b32 a49, v99\n\t"
"v_mfma_f32_16x16x32_bf16 v[188:191], a[80:83], a[192:195], v[188:191]\n\t"
"ds_read_b128 a[192:195], v244 offset:3072\n\t"
"v_fma_f32 v99, -v41, s56, -v139\n\t"
"v_mfma_f32_16x16x32_bf16 v[188:191], a[88:91], a[196:199], v[188:191]\n\t"
"v_accvgpr_write_b32 a50, v98\n\t"
"v_accvgpr_write_b32 a51, v99\n\t"
"v_mfma_f32_16x16x32_bf16 v[188:191], a[96:99], a[200:203], v[188:191]\n\t"
"v_fma_f32 v98, -v42, s56, -v140\n\t"
"v_fma_f32 v99, -v43, s56, -v141\n\t"
"v_mfma_f32_16x16x32_bf16 v[192:195], a[80:83], a[196:199], v[192:195]\n\t"
"s_waitcnt vmcnt(24) lgkmcnt(4)\n\t"
"ds_read_b128 a[196:199], v244 offset:3584\n\t"
"v_accvgpr_write_b32 a56, v98\n\t"
"v_mfma_f32_16x16x32_bf16 v[192:195], a[88:91], a[200:203], v[192:195]\n\t"
"ds_read_b128 a[200:203], v244 offset:4096\n\t"
"v_fma_f32 v98, -v44, s56, -v142\n\t"
"v_mfma_f32_16x16x32_bf16 v[192:195], a[96:99], a[204:207], v[192:195]\n\t"
"ds_read_b128 a[204:207], v244 offset:4608\n\t"
"v_accvgpr_write_b32 a57, v99\n\t"
"Lst2_%=:\n\t"
"v_mfma_f32_16x16x32_bf16 v[196:199], a[136:139], a[168:171], v[196:199]\n\t"
"s_waitcnt lgkmcnt(6)\n\t"
"v_xor_b32 v246, 0xc0, v246\n\t"
"v_fma_f32 v99, -v45, s56, -v143\n\t"
"v_mfma_f32_16x16x32_bf16 v[196:199], a[4:7], a[172:175], v[196:199]\n\t"
"v_accvgpr_write_b32 a58, v98\n\t"
"v_accvgpr_write_b32 a59, v99\n\t"
"v_mfma_f32_16x16x32_bf16 v[196:199], a[12:15], a[176:179], v[196:199]\n\t"
"v_fma_f32 v98, -v46, s56, -v144\n\t"
"v_fma_f32 v99, -v47, s56, -v145\n\t"
"v_mfma_f32_16x16x32_bf16 v[204:207], a[136:139], a[172:175], v[204:207]\n\t"
"s_waitcnt lgkmcnt(5)\n\t"
"v_accvgpr_write_b32 a64, v98\n\t"
"v_fma_f32 v98, -v48, s56, -v146\n\t"
"v_mfma_f32_16x16x32_bf16 v[204:207], a[4:7], a[176:179], v[204:207]\n\t"
"v_accvgpr_write_b32 a65, v99\n\t"
"v_fma_f32 v99, -v49, s56, -v147\n\t"
"v_mfma_f32_16x16x32_bf16 v[204:207], a[12:15], a[180:183], v[204:207]\n\t"
"v_accvgpr_write_b32 a66, v98\n\t"
"v_accvgpr_write_b32 a67, v99\n\t"
"v_mfma_f32_16x16x32_bf16 v[212:215], a[136:139], a[176:179], v[212:215]\n\t"
"s_waitcnt lgkmcnt(4)\n\t"
"v_fma_f32 v98, -v50, s56, -v148\n\t"
"v_fma_f32 v99, -v51, s56, -v149\n\t"
"v_mfma_f32_16x16x32_bf16 v[212:215], a[4:7], a[180:183], v[212:215]\n\t"
"v_accvgpr_write_b32 a72, v98\n\t"
"v_fma_f32 v98, -v52, s56, -v150\n\t"
"v_mfma_f32_16x16x32_bf16 v[212:215], a[12:15], a[184:187], v[212:215]\n\t"
"v_accvgpr_write_b32 a73, v99\n\t"
"v_fma_f32 v99, -v53, s56, -v151\n\t"
"v_mfma_f32_16x16x32_bf16 a[0:3], a[136:139], a[180:183], a[0:3]\n\t"
"s_waitcnt lgkmcnt(3)\n\t"
"v_accvgpr_write_b32 a74, v98\n\t"
"v_accvgpr_write_b32 a75, v99\n\t"
"v_mfma_f32_16x16x32_bf16 a[0:3], a[4:7], a[184:187], a[0:3]\n\t"
"v_fma_f32 v98, -v54, s56, -v152\n\t"
"v_fma_f32 v99, -v55, s56, -v153\n\t"
"v_mfma_f32_16x16x32_bf16 a[0:3], a[12:15], a[188:191], a[0:3]\n\t"
"v_accvgpr_write_b32 a80, v98\n\t"
"v_fma_f32 v98, -v56, s56, -v154\n\t"
"v_mfma_f32_16x16x32_bf16 a[8:11], a[136:139], a[184:187], a[8:11]\n\t"
"s_waitcnt lgkmcnt(2)\n\t"
"v_accvgpr_write_b32 a81, v99\n\t"
"v_fma_f32 v99, -v57, s56, -v155\n\t"
"v_mfma_f32_16x16x32_bf16 a[8:11], a[4:7], a[188:191], a[8:11]\n\t"
"v_accvgpr_write_b32 a82, v98\n\t"
"v_accvgpr_write_b32 a83, v99\n\t"
"v_mfma_f32_16x16x32_bf16 a[8:11], a[12:15], a[192:195], a[8:11]\n\t"
"ds_read_b64_tr_b16 v[228:229], v246 offset:0\n\t"
"v_fma_f32 v98, -v58, s56, -v156\n\t"
"v_mfma_f32_16x16x32_bf16 a[16:19], a[136:139], a[188:191], a[16:19]\n\t"
"s_waitcnt lgkmcnt(2)\n\t"
"ds_read_b64_tr_b16 v[230:231], v246 offset:3072\n\t"
"v_fma_f32 v99, -v59, s56, -v157\n\t"
"v_mfma_f32_16x16x32_bf16 a[16:19], a[4:7], a[192:195], a[16:19]\n\t"
"v_accvgpr_write_b32 a88, v98\n\t"
"v_fma_f32 v98, -v60, s56, -v158\n\t"
"v_mfma_f32_16x16x32_bf16 a[16:19], a[12:15], a[196:199], a[16:19]\n\t"
"v_accvgpr_write_b32 a89, v99\n\t"
"v_fma_f32 v99, -v61, s56, -v159\n\t"
"v_mfma_f32_16x16x32_bf16 a[24:27], a[136:139], a[192:195], a[24:27]\n\t"
"s_waitcnt lgkmcnt(2)\n\t"
"v_accvgpr_write_b32 a90, v98\n\t"
"v_accvgpr_write_b32 a91, v99\n\t"
"v_mfma_f32_16x16x32_bf16 a[24:27], a[4:7], a[196:199], a[24:27]\n\t"
"v_fma_f32 v98, -v62, s56, -v160\n\t"
"v_fma_f32 v99, -v63, s56, -v161\n\t"
"v_mfma_f32_16x16x32_bf16 a[24:27], a[12:15], a[200:203], a[24:27]\n\t"
"v_accvgpr_write_b32 a96, v98\n\t"
"v_fma_f32 v98, -v64, s56, -v162\n\t"
"v_mfma_f32_16x16x32_bf16 a[32:35], a[136:139], a[196:199], a[32:35]\n\t"
"s_waitcnt vmcnt(21)\n\t"
"ds_read_b64_tr_b16 v[232:233], v246 offset:6144\n\t"
"v_accvgpr_write_b32 a97, v99\n\t"
"v_mfma_f32_16x16x32_bf16 a[32:35], a[4:7], a[200:203], a[32:35]\n\t"
"ds_read_b64_tr_b16 v[234:235], v246 offset:9216\n\t"
"v_fma_f32 v99, -v65, s56, -v163\n\t"
"v_mfma_f32_16x16x32_bf16 a[32:35], a[12:15], a[204:207], a[32:35]\n\t"
"v_accvgpr_write_b32 a98, v98\n\t"
"v_accvgpr_write_b32 a99, v99\n\t"
"v_mfma_f32_16x16x32_bf16 a[40:43], a[20:23], a[168:171], a[40:43]\n\t"
"v_fma_f32 v98, -v66, s56, -v164\n\t"
"v_fma_f32 v99, -v67, s56, -v165\n\t"
"v_mfma_f32_16x16x32_bf16 a[40:43], a[28:31], a[172:175], a[40:43]\n\t"
"v_accvgpr_write_b32 a104, v98\n\t"
"v_fma_f32 v98, -v68, s56, -v166\n\t"
"v_mfma_f32_16x16x32_bf16 a[40:43], a[36:39], a[176:179], a[40:43]\n\t"
"v_accvgpr_write_b32 a105, v99\n\t"
"v_fma_f32 v99, -v69, s56, -v167\n\t"
"v_mfma_f32_16x16x32_bf16 a[48:51], a[20:23], a[172:175], a[48:51]\n\t"
"s_waitcnt lgkmcnt(2)\n\t"
"v_accvgpr_write_b32 a106, v98\n\t"
"v_accvgpr_write_b32 a107, v99\n\t"
"v_mfma_f32_16x16x32_bf16 a[48:51], a[28:31], a[176:179], a[48:51]\n\t"
"v_fma_f32 v98, -v70, s56, -v168\n\t"
"v_fma_f32 v99, -v71, s56, -v169\n\t"
"v_mfma_f32_16x16x32_bf16 a[48:51], a[36:39], a[180:183], a[48:51]\n\t"
"v_accvgpr_write_b32 a112, v98\n\t"
"v_fma_f32 v98, -v72, s56, -v170\n\t"
"v_mfma_f32_16x16x32_bf16 v[228:231], v[228:231], v[220:223], 0\n\t"
"v_accvgpr_write_b32 a113, v99\n\t"
"v_fma_f32 v99, -v73, s56, -v171\n\t"
"v_mfma_f32_16x16x32_bf16 a[56:59], a[20:23], a[176:179], a[56:59]\n\t"
"ds_read_b64_tr_b16 v[224:225], v246 offset:12288\n\t"
"v_accvgpr_write_b32 a114, v98\n\t"
"v_mfma_f32_16x16x32_bf16 a[56:59], a[28:31], a[180:183], a[56:59]\n\t"
"ds_read_b64_tr_b16 v[226:227], v246 offset:15360\n\t"
"v_accvgpr_write_b32 a115, v99\n\t"
"v_mfma_f32_16x16x32_bf16 a[56:59], a[36:39], a[184:187], a[56:59]\n\t"
"v_fma_f32 v98, -v74, s56, -v172\n\t"
"v_fma_f32 v99, -v75, s56, -v173\n\t"
"v_mfma_f32_16x16x32_bf16 a[64:67], a[20:23], a[180:183], a[64:67]\n\t"
"s_waitcnt lgkmcnt(2)\n\t"
"v_accvgpr_write_b32 a120, v98\n\t"
"v_fma_f32 v98, -v76, s56, -v174\n\t"
"v_mfma_f32_16x16x32_bf16 a[64:67], a[28:31], a[184:187], a[64:67]\n\t"
"v_cvt_pk_bf16_f32 v228, v228, v229\n\t"
"v_cvt_pk_bf16_f32 v229, v230, v231\n\t"
"v_mfma_f32_16x16x32_bf16 a[64:67], a[36:39], a[188:191], a[64:67]\n\t"
"v_accvgpr_write_b32 a121, v99\n\t"
"v_fma_f32 v99, -v77, s56, -v175\n\t"
"v_mfma_f32_16x16x32_bf16 v[232:235], v[232:235], v[220:223], 0\n\t"
"v_accvgpr_write_b32 a122, v98\n\t"
"v_accvgpr_write_b32 a123, v99\n\t"
"v_mfma_f32_16x16x32_bf16 a[72:75], a[20:23], a[184:187], a[72:75]\n\t"
"v_fma_f32 v98, -v78, s56, -v176\n\t"
"v_fma_f32 v99, -v79, s56, -v177\n\t"
"v_mfma_f32_16x16x32_bf16 a[72:75], a[28:31], a[188:191], a[72:75]\n\t"
"v_accvgpr_write_b32 a128, v98\n\t"
"v_fma_f32 v98, -v80, s56, -v178\n\t"
"v_mfma_f32_16x16x32_bf16 a[72:75], a[36:39], a[192:195], a[72:75]\n\t"
"v_accvgpr_write_b32 a129, v99\n\t"
"v_fma_f32 v99, -v81, s56, -v179\n\t"
"v_mfma_f32_16x16x32_bf16 a[80:83], a[20:23], a[188:191], a[80:83]\n\t"
"v_accvgpr_write_b32 a130, v98\n\t"
"v_accvgpr_write_b32 a131, v99\n\t"
"v_mfma_f32_16x16x32_bf16 a[80:83], a[28:31], a[192:195], a[80:83]\n\t"
"v_cvt_pk_bf16_f32 v230, v232, v233\n\t"
"v_cvt_pk_bf16_f32 v231, v234, v235\n\t"
"v_mfma_f32_16x16x32_bf16 a[80:83], a[36:39], a[196:199], a[80:83]\n\t"
"ds_read_b64_tr_b16 v[232:233], v246 offset:19968\n\t"
"v_fma_f32 v98, -v82, s56, -v180\n\t"
"v_mfma_f32_16x16x32_bf16 a[88:91], a[20:23], a[192:195], a[88:91]\n\t"
"s_waitcnt lgkmcnt(1)\n\t"
"ds_read_b64_tr_b16 v[234:235], v246 offset:23040\n\t"
"v_fma_f32 v99, -v83, s56, -v181\n\t"
"v_mfma_f32_16x16x32_bf16 a[88:91], a[28:31], a[196:199], a[88:91]\n\t"
"v_accvgpr_write_b32 a136, v98\n\t"
"v_fma_f32 v98, -v84, s56, -v182\n\t"
"v_mfma_f32_16x16x32_bf16 a[88:91], a[36:39], a[200:203], a[88:91]\n\t"
"v_accvgpr_write_b32 a137, v99\n\t"
"v_fma_f32 v99, -v85, s56, -v183\n\t"
"v_mfma_f32_16x16x32_bf16 v[224:227], v[224:227], v[220:223], 0\n\t"
"v_accvgpr_write_b32 a138, v98\n\t"
"v_accvgpr_write_b32 a139, v99\n\t"
"v_mfma_f32_16x16x32_bf16 a[96:99], a[20:23], a[196:199], a[96:99]\n\t"
"s_waitcnt vmcnt(18)\n\t"
"v_fma_f32 v98, -v86, s56, -v184\n\t"
"v_fma_f32 v99, -v87, s56, -v185\n\t"
"v_mfma_f32_16x16x32_bf16 a[96:99], a[28:31], a[200:203], a[96:99]\n\t"
"v_accvgpr_write_b32 a144, v98\n\t"
"v_fma_f32 v98, -v88, s56, -v186\n\t"
"v_mfma_f32_16x16x32_bf16 a[96:99], a[36:39], a[204:207], a[96:99]\n\t"
"v_accvgpr_write_b32 a145, v99\n\t"
"v_fma_f32 v99, -v89, s56, -v187\n\t"
"v_mfma_f32_16x16x32_bf16 a[104:107], a[76:79], a[168:171], a[104:107]\n\t"
"ds_read_b128 a[168:171], v244 offset:9216\n\t"
"v_accvgpr_write_b32 a146, v98\n\t"
"v_mfma_f32_16x16x32_bf16 a[104:107], a[84:87], a[172:175], a[104:107]\n\t"
"v_cvt_pk_bf16_f32 v224, v224, v225\n\t"
"v_cvt_pk_bf16_f32 v225, v226, v227\n\t"
"v_mfma_f32_16x16x32_bf16 a[104:107], a[92:95], a[176:179], a[104:107]\n\t"
"v_accvgpr_write_b32 a147, v99\n\t"
"v_fma_f32 v98, -v90, s56, -v188\n\t"
"v_mfma_f32_16x16x32_bf16 a[112:115], a[76:79], a[172:175], a[112:115]\n\t"
"s_waitcnt lgkmcnt(1)\n\t"
"ds_read_b128 a[172:175], v244 offset:9728\n\t"
"v_fma_f32 v99, -v91, s56, -v189\n\t"
"v_mfma_f32_16x16x32_bf16 a[112:115], a[84:87], a[176:179], a[112:115]\n\t"
"v_accvgpr_write_b32 a152, v98\n\t"
"v_fma_f32 v98, -v92, s56, -v190\n\t"
"v_mfma_f32_16x16x32_bf16 a[112:115], a[92:95], a[180:183], a[112:115]\n\t"
"v_accvgpr_write_b32 a153, v99\n\t"
"v_fma_f32 v99, -v93, s56, -v191\n\t"
"v_mfma_f32_16x16x32_bf16 v[232:235], v[232:235], v[220:223], 0\n\t"
"v_accvgpr_write_b32 a154, v98\n\t"
"v_accvgpr_write_b32 a155, v99\n\t"
"v_mfma_f32_16x16x32_bf16 a[120:123], a[76:79], a[176:179], a[120:123]\n\t"
"ds_read_b128 a[176:179], v244 offset:10240\n\t"
"v_fma_f32 v98, -v94, s56, -v192\n\t"
"v_mfma_f32_16x16x32_bf16 a[120:123], a[84:87], a[180:183], a[120:123]\n\t"
"v_fma_f32 v99, -v95, s56, -v193\n\t"
"v_accvgpr_write_b32 a160, v98\n\t"
"v_mfma_f32_16x16x32_bf16 a[120:123], a[92:95], a[184:187], a[120:123]\n\t"
"v_fma_f32 v98, -v96, s56, -v194\n\t"
"v_accvgpr_write_b32 a161, v99\n\t"
"v_mfma_f32_16x16x32_bf16 a[128:131], a[76:79], a[180:183], a[128:131]\n\t"
"ds_read_b128 a[180:183], v244 offset:10752\n\t"
"v_fma_f32 v99, -v97, s56, -v195\n\t"
"v_mfma_f32_16x16x32_bf16 a[128:131], a[84:87], a[184:187], a[128:131]\n\t"
"v_cvt_pk_bf16_f32 v226, v232, v233\n\t"
"v_cvt_pk_bf16_f32 v227, v234, v235\n\t"
"v_mfma_f32_16x16x32_bf16 a[128:131], a[92:95], a[188:191], a[128:131]\n\t"
"ds_read_b64_tr_b16 v[232:233], v246 offset:26112\n\t"
"v_accvgpr_write_b32 a162, v98\n\t"
"v_mfma_f32_16x16x32_bf16 a[136:139], a[76:79], a[184:187], a[136:139]\n\t"
"ds_read_b64_tr_b16 v[234:235], v246 offset:29184\n\t"
"v_accvgpr_write_b32 a163, v99\n\t"
"v_mfma_f32_16x16x32_bf16 a[136:139], a[84:87], a[188:191], a[136:139]\n\t"
"ds_read_b128 a[184:187], v244 offset:11264\n\t"
"v_fma_f32 v200, v100, s64, -v2\n\t"
"v_mfma_f32_16x16x32_bf16 a[136:139], a[92:95], a[192:195], a[136:139]\n\t"
"v_fma_f32 v201, v101, s64, -v3\n\t"
"v_fma_f32 v202, v102, s64, -v4\n\t"
"v_mfma_f32_16x16x32_bf16 a[144:147], a[76:79], a[188:191], a[144:147]\n\t"
"ds_read_b128 a[188:191], v244 offset:11776\n\t"
"v_fma_f32 v203, v103, s64, -v5\n\t"
"v_mfma_f32_16x16x32_bf16 a[144:147], a[84:87], a[192:195], a[144:147]\n\t"
"v_fma_f32 v208, v104, s64, -v6\n\t"
"v_fma_f32 v209, v105, s64, -v7\n\t"
"v_mfma_f32_16x16x32_bf16 a[144:147], a[92:95], a[196:199], a[144:147]\n\t"
"v_fma_f32 v210, v106, s64, -v8\n\t"
"v_fma_f32 v211, v107, s64, -v9\n\t"
"v_mfma_f32_16x16x32_bf16 a[152:155], a[76:79], a[192:195], a[152:155]\n\t"
"ds_read_b128 a[192:195], v244 offset:12288\n\t"
"v_fma_f32 v216, v108, s64, -v10\n\t"
"v_mfma_f32_16x16x32_bf16 a[152:155], a[84:87], a[196:199], a[152:155]\n\t"
"v_fma_f32 v217, v109, s64, -v11\n\t"
"v_fma_f32 v218, v110, s64, -v12\n\t"
"v_mfma_f32_16x16x32_bf16 a[152:155], a[92:95], a[200:203], a[152:155]\n\t"
"v_fma_f32 v219, v111, s64, -v13\n\t"
"v_fma_f32 v98, v112, s64, -v14\n\t"
"v_mfma_f32_16x16x32_bf16 a[160:163], a[76:79], a[196:199], a[160:163]\n\t"
"s_waitcnt lgkmcnt(3)\n\t"
"ds_read_b128 a[196:199], v244 offset:12800\n\t"
"v_fma_f32 v99, v113, s64, -v15\n\t"
"v_mfma_f32_16x16x32_bf16 a[160:163], a[84:87], a[200:203], a[160:163]\n\t"
"s_waitcnt vmcnt(15)\n\t"
"ds_read_b128 a[200:203], v244 offset:13312\n\t"
"v_accvgpr_write_b32 a4, v98\n\t"
"v_mfma_f32_16x16x32_bf16 a[160:163], a[92:95], a[204:207], a[160:163]\n\t"
"ds_read_b128 a[204:207], v244 offset:13824\n\t"
"v_fma_f32 v98, v114, s64, -v16\n\t"
"v_mfma_f32_16x16x32_bf16 v[232:235], v[232:235], v[220:223], 0\n\t"
"v_accvgpr_write_b32 a5, v99\n\t"
"v_fma_f32 v99, v115, s64, -v17\n\t"
"v_mfma_f32_16x16x32_bf16 v[200:203], a[148:151], a[168:171], v[200:203]\n\t"
"v_accvgpr_write_b32 a6, v98\n\t"
"v_accvgpr_write_b32 a7, v99\n\t"
"v_mfma_f32_16x16x32_bf16 v[200:203], a[156:159], a[172:175], v[200:203]\n\t"
"v_fma_f32 v98, v116, s64, -v18\n\t"
"v_fma_f32 v99, v117, s64, -v19\n\t"
"v_mfma_f32_16x16x32_bf16 v[200:203], a[164:167], a[176:179], v[200:203]\n\t"
"s_add_u32 m0, s24, 896\n\t"
"s_add_u32 s28, s21, 0\n\t"
"v_accvgpr_write_b32 a12, v98\n\t"
"v_mfma_f32_16x16x32_bf16 v[208:211], a[148:151], a[172:175], v[208:211]\n\t"
"s_waitcnt lgkmcnt(5)\n\t"
"v_fma_f32 v98, v118, s64, -v20\n\t"
"v_accvgpr_write_b32 a13, v99\n\t"
"v_mfma_f32_16x16x32_bf16 v[208:211], a[156:159], a[176:179], v[208:211]\n\t"
"v_fma_f32 v99, v119, s64, -v21\n\t"
"v_mfma_f32_16x16x32_bf16 v[208:211], a[164:167], a[180:183], v[208:211]\n\t"
"s_waitcnt lgkmcnt(0)\n\t"
"s_barrier\n\t"
"v_mfma_f32_16x16x32_bf16 v[216:219], a[148:151], a[176:179], v[216:219]\n\t"
"v_cvt_pk_bf16_f32 v232, v232, v233\n\t"
"v_mfma_f32_16x16x32_bf16 v[216:219], a[156:159], a[180:183], v[216:219]\n\t"
"buffer_load_dwordx4 v247, s[12:15], s28 offen offset:128 nt lds\n\t"
"v_mfma_f32_16x16x32_bf16 v[216:219], a[164:167], a[184:187], v[216:219]\n\t"
"s_add_u32 m0, s24, 7040\n\t"
"s_add_u32 s28, s21, 196608\n\t"
"ds_write2st64_b64 v245, v[228:229], v[230:231] offset0:0 offset1:1\n\t"
"v_cvt_pk_bf16_f32 v233, v234, v235\n\t"
"v_mfma_f32_16x16x32_bf16 a[4:7], a[148:151], a[180:183], a[4:7]\n\t"
"v_accvgpr_write_b32 a14, v98\n\t"
"v_accvgpr_write_b32 a15, v99\n\t"
"v_mfma_f32_16x16x32_bf16 a[4:7], a[156:159], a[184:187], a[4:7]\n\t"
"ds_read_b64_tr_b16 v[228:229], v246 offset:32256\n\t"
"v_fma_f32 v98, v120, s64, -v22\n\t"
"v_mfma_f32_16x16x32_bf16 a[4:7], a[164:167], a[188:191], a[4:7]\n\t"
"buffer_load_dwordx4 v247, s[12:15], s28 offen offset:128 nt lds\n\t"
"v_fma_f32 v99, v121, s64, -v23\n\t"
"s_add_u32 m0, s24, 13184\n\t"
"s_add_u32 s28, s21, 393216\n\t"
"v_mfma_f32_16x16x32_bf16 a[12:15], a[148:151], a[184:187], a[12:15]\n\t"
"ds_read_b64_tr_b16 v[230:231], v246 offset:35328\n\t"
"v_accvgpr_write_b32 a20, v98\n\t"
"v_mfma_f32_16x16x32_bf16 a[12:15], a[156:159], a[188:191], a[12:15]\n\t"
"v_fma_f32 v98, v122, s64, -v24\n\t"
"v_accvgpr_write_b32 a21, v99\n\t"
"v_mfma_f32_16x16x32_bf16 a[12:15], a[164:167], a[192:195], a[12:15]\n\t"
"ds_write2st64_b64 v245, v[224:225], v[226:227] offset0:2 offset1:3\n\t"
"buffer_load_dwordx4 v247, s[12:15], s28 offen offset:128 nt lds\n\t"
"v_fma_f32 v99, v123, s64, -v25\n\t"
"v_accvgpr_write_b32 a22, v98\n\t"
"v_accvgpr_write_b32 a23, v99\n\t"
"v_fma_f32 v98, v124, s64, -v26\n\t"
"v_fma_f32 v99, v125, s64, -v27\n\t"
"s_add_u32 m0, s24, 20864\n\t"
"s_add_u32 s28, s21, 589824\n\t"
"v_mfma_f32_16x16x32_bf16 a[20:23], a[148:151], a[188:191], a[20:23]\n\t"
"buffer_load_dwordx4 v247, s[12:15], s28 offen offset:128 nt lds\n\t"
"v_mfma_f32_16x16x32_bf16 a[20:23], a[156:159], a[192:195], a[20:23]\n\t"
"s_add_u32 m0, s24, 60800\n\t"
"s_add_u32 s28, s21, 1769472\n\t"
"ds_read_b64_tr_b16 v[224:225], v246 offset:39936\n\t"
"v_mfma_f32_16x16x32_bf16 a[20:23], a[164:167], a[196:199], a[20:23]\n\t"
"s_waitcnt lgkmcnt(2)\n\t"
"ds_write_b64 v245, v[232:233] offset:2048\n\t"
"ds_read_b64_tr_b16 v[226:227], v246 offset:43008\n\t"
"v_accvgpr_write_b32 a28, v98\n\t"
"v_fma_f32 v98, v126, s64, -v28\n\t"
"v_accvgpr_write_b32 a29, v99\n\t"
"v_fma_f32 v99, v127, s64, -v29\n\t"
"v_accvgpr_write_b32 a30, v98\n\t"
"v_accvgpr_write_b32 a31, v99\n\t"
"v_mfma_f32_16x16x32_bf16 v[228:231], v[228:231], v[220:223], 0\n\t"
"buffer_load_dwordx4 v247, s[12:15], s28 offen offset:128 nt lds\n\t"
"v_fma_f32 v98, v128, s64, -v30\n\t"
"v_fma_f32 v99, v129, s64, -v31\n\t"
"s_add_u32 m0, s24, 66944\n\t"
"s_add_u32 s28, s21, 1966080\n\t"
"v_mfma_f32_16x16x32_bf16 a[28:31], a[148:151], a[192:195], a[28:31]\n\t"
"buffer_load_dwordx4 v247, s[12:15], s28 offen offset:128 nt lds\n\t"
"v_mfma_f32_16x16x32_bf16 a[28:31], a[156:159], a[196:199], a[28:31]\n\t"
"ds_read_b64_tr_b16 v[232:233], v246 offset:46080\n\t"
"v_accvgpr_write_b32 a36, v98\n\t"
"v_mfma_f32_16x16x32_bf16 a[28:31], a[164:167], a[200:203], a[28:31]\n\t"
"s_waitcnt lgkmcnt(1)\n\t"
"ds_read_b64_tr_b16 v[234:235], v246 offset:49152\n\t"
"v_fma_f32 v98, v130, s64, -v32\n\t"
"v_accvgpr_write_b32 a37, v99\n\t"
"v_fma_f32 v99, v131, s64, -v33\n\t"
"v_accvgpr_write_b32 a38, v98\n\t"
"v_accvgpr_write_b32 a39, v99\n\t"
"v_mfma_f32_16x16x32_bf16 v[224:227], v[224:227], v[220:223], 0\n\t"
"v_fma_f32 v98, v132, s64, -v34\n\t"
"v_fma_f32 v99, v133, s64, -v35\n\t"
"v_mfma_f32_16x16x32_bf16 a[36:39], a[148:151], a[196:199], a[36:39]\n\t"
"v_accvgpr_write_b32 a44, v98\n\t"
"v_fma_f32 v98, v134, s64, -v36\n\t"
"v_mfma_f32_16x16x32_bf16 a[36:39], a[156:159], a[200:203], a[36:39]\n\t"
"v_accvgpr_write_b32 a45, v99\n\t"
"v_fma_f32 v99, v135, s64, -v37\n\t"
"v_mfma_f32_16x16x32_bf16 a[36:39], a[164:167], a[204:207], a[36:39]\n\t"
"s_waitcnt lgkmcnt(0)\n\t"
"v_accvgpr_write_b32 a46, v98\n\t"
"v_accvgpr_write_b32 a47, v99\n\t"
"v_fma_f32 v98, v136, s64, -v38\n\t"
"v_fma_f32 v99, v137, s64, -v39\n\t"
"v_mfma_f32_16x16x32_bf16 v[232:235], v[232:235], v[220:223], 0\n\t"
"s_waitcnt vmcnt(18)\n\t"
"v_cvt_pk_bf16_f32 v228, v228, v229\n\t"
"v_cvt_pk_bf16_f32 v229, v230, v231\n\t"
"v_mfma_f32_16x16x32_bf16 a[44:47], v[240:243], a[168:171], a[44:47]\n\t"
"v_accvgpr_write_b32 a52, v98\n\t"
"v_fma_f32 v98, v138, s64, -v40\n\t"
"v_mfma_f32_16x16x32_bf16 a[44:47], a[208:211], a[172:175], a[44:47]\n\t"
"v_accvgpr_write_b32 a53, v99\n\t"
"v_fma_f32 v99, v139, s64, -v41\n\t"
"v_mfma_f32_16x16x32_bf16 a[44:47], v[248:251], a[176:179], a[44:47]\n\t"
"v_accvgpr_write_b32 a54, v98\n\t"
"v_accvgpr_write_b32 a55, v99\n\t"
"v_fma_f32 v98, v140, s64, -v42\n\t"
"v_fma_f32 v99, v141, s64, -v43\n\t"
"v_mfma_f32_16x16x32_bf16 a[52:55], v[240:243], a[172:175], a[52:55]\n\t"
"v_accvgpr_write_b32 a60, v98\n\t"
"v_fma_f32 v98, v142, s64, -v44\n\t"
"v_mfma_f32_16x16x32_bf16 a[52:55], a[208:211], a[176:179], a[52:55]\n\t"
"v_accvgpr_write_b32 a61, v99\n\t"
"v_fma_f32 v99, v143, s64, -v45\n\t"
"v_mfma_f32_16x16x32_bf16 a[52:55], v[248:251], a[180:183], a[52:55]\n\t"
"v_accvgpr_write_b32 a62, v98\n\t"
"v_accvgpr_write_b32 a63, v99\n\t"
"v_cvt_pk_bf16_f32 v230, v224, v225\n\t"
"v_cvt_pk_bf16_f32 v231, v226, v227\n\t"
"v_fma_f32 v98, v144, s64, -v46\n\t"
"v_fma_f32 v99, v145, s64, -v47\n\t"
"v_mfma_f32_16x16x32_bf16 a[60:63], v[240:243], a[176:179], a[60:63]\n\t"
"ds_read_b64_tr_b16 v[224:225], v246 offset:52224\n\t"
"v_accvgpr_write_b32 a68, v98\n\t"
"v_mfma_f32_16x16x32_bf16 a[60:63], a[208:211], a[180:183], a[60:63]\n\t"
"ds_read_b64_tr_b16 v[226:227], v246 offset:55296\n\t"
"v_fma_f32 v98, v146, s64, -v48\n\t"
"v_mfma_f32_16x16x32_bf16 a[60:63], v[248:251], a[184:187], a[60:63]\n\t"
"ds_write2st64_b64 v245, v[228:229], v[230:231] offset0:5 offset1:6\n\t"
"v_accvgpr_write_b32 a69, v99\n\t"
"v_fma_f32 v99, v147, s64, -v49\n\t"
"v_accvgpr_write_b32 a70, v98\n\t"
"v_accvgpr_write_b32 a71, v99\n\t"
"v_fma_f32 v98, v148, s64, -v50\n\t"
"v_fma_f32 v99, v149, s64, -v51\n\t"
"v_cvt_pk_bf16_f32 v232, v232, v233\n\t"
"v_cvt_pk_bf16_f32 v233, v234, v235\n\t"
"v_mfma_f32_16x16x32_bf16 a[68:71], v[240:243], a[180:183], a[68:71]\n\t"
"v_accvgpr_write_b32 a76, v98\n\t"
"v_fma_f32 v98, v150, s64, -v52\n\t"
"v_mfma_f32_16x16x32_bf16 a[68:71], a[208:211], a[184:187], a[68:71]\n\t"
"v_accvgpr_write_b32 a77, v99\n\t"
"v_fma_f32 v99, v151, s64, -v53\n\t"
"v_mfma_f32_16x16x32_bf16 a[68:71], v[248:251], a[188:191], a[68:71]\n\t"
"v_accvgpr_write_b32 a78, v98\n\t"
"v_accvgpr_write_b32 a79, v99\n\t"
"v_fma_f32 v98, v152, s64, -v54\n\t"
"v_fma_f32 v99, v153, s64, -v55\n\t"
"v_mfma_f32_16x16x32_bf16 a[76:79], v[240:243], a[184:187], a[76:79]\n\t"
"s_waitcnt lgkmcnt(1)\n\t"
"v_accvgpr_write_b32 a84, v98\n\t"
"v_fma_f32 v98, v154, s64, -v56\n\t"
"v_mfma_f32_16x16x32_bf16 a[76:79], a[208:211], a[188:191], a[76:79]\n\t"
"v_accvgpr_write_b32 a85, v99\n\t"
"v_fma_f32 v99, v155, s64, -v57\n\t"
"v_mfma_f32_16x16x32_bf16 a[76:79], v[248:251], a[192:195], a[76:79]\n\t"
"v_accvgpr_write_b32 a86, v98\n\t"
"v_accvgpr_write_b32 a87, v99\n\t"
"v_fma_f32 v98, v156, s64, -v58\n\t"
"v_fma_f32 v99, v157, s64, -v59\n\t"
"v_mfma_f32_16x16x32_bf16 v[224:227], v[224:227], v[220:223], 0\n\t"
"v_accvgpr_write_b32 a92, v98\n\t"
"v_fma_f32 v98, v158, s64, -v60\n\t"
"v_mfma_f32_16x16x32_bf16 a[84:87], v[240:243], a[188:191], a[84:87]\n\t"
"s_mov_b32 s20, 73728\n\t"
"v_accvgpr_write_b32 a93, v99\n\t"
"v_fma_f32 v99, v159, s64, -v61\n\t"
"v_mfma_f32_16x16x32_bf16 a[84:87], a[208:211], a[192:195], a[84:87]\n\t"
"v_accvgpr_write_b32 a94, v98\n\t"
"v_accvgpr_write_b32 a95, v99\n\t"
"v_mfma_f32_16x16x32_bf16 a[84:87], v[248:251], a[196:199], a[84:87]\n\t"
"v_fma_f32 v98, v160, s64, -v62\n\t"
"v_fma_f32 v99, v161, s64, -v63\n\t"
"v_mfma_f32_16x16x32_bf16 a[92:95], v[240:243], a[192:195], a[92:95]\n\t"
"v_accvgpr_write_b32 a100, v98\n\t"
"v_fma_f32 v98, v162, s64, -v64\n\t"
"v_mfma_f32_16x16x32_bf16 a[92:95], a[208:211], a[196:199], a[92:95]\n\t"
"v_accvgpr_write_b32 a101, v99\n\t"
"v_fma_f32 v99, v163, s64, -v65\n\t"
"v_mfma_f32_16x16x32_bf16 a[92:95], v[248:251], a[200:203], a[92:95]\n\t"
"Lpg3_%=:\n\t"
"s_cbranch_vccnz Lpg7_%=\n\t"
"v_accvgpr_write_b32 a102, v98\n\t"
"v_accvgpr_write_b32 a103, v99\n\t"
"v_fma_f32 v98, v164, s64, -v66\n\t"
"v_fma_f32 v99, v165, s64, -v67\n\t"
"v_cvt_pk_bf16_f32 v234, v224, v225\n\t"
"v_cvt_pk_bf16_f32 v235, v226, v227\n\t"
"v_mfma_f32_16x16x32_bf16 a[100:103], v[240:243], a[196:199], a[100:103]\n\t"
"v_accvgpr_write_b32 a108, v98\n\t"
"v_fma_f32 v98, v166, s64, -v68\n\t"
"v_mfma_f32_16x16x32_bf16 a[100:103], a[208:211], a[200:203], a[100:103]\n\t"
"v_accvgpr_write_b32 a109, v99\n\t"
"v_fma_f32 v99, v167, s64, -v69\n\t"
"v_mfma_f32_16x16x32_bf16 a[100:103], v[248:251], a[204:207], a[100:103]\n\t"
"ds_write2st64_b64 v245, v[232:233], v[234:235] offset0:7 offset1:8\n\t"
"v_accvgpr_write_b32 a110, v98\n\t"
"v_accvgpr_write_b32 a111, v99\n\t"
"v_fma_f32 v98, v168, s64, -v70\n\t"
"v_fma_f32 v99, v169, s64, -v71\n\t"
"s_waitcnt vmcnt(15)\n\t"
"v_mfma_f32_16x16x32_bf16 a[108:111], a[212:215], a[168:171], a[108:111]\n\t"
"buffer_load_dwordx4 v[240:243], v252, s[8:11], s20 offen offset:0 sc1\n\t"
"v_mfma_f32_16x16x32_bf16 a[108:111], a[216:219], a[172:175], a[108:111]\n\t"
"v_accvgpr_write_b32 a116, v98\n\t"
"v_mfma_f32_16x16x32_bf16 a[108:111], a[220:223], a[176:179], a[108:111]\n\t"
"v_accvgpr_write_b32 a117, v99\n\t"
"v_fma_f32 v98, v170, s64, -v72\n\t"
"v_accvgpr_write_b32 a118, v98\n\t"
"v_fma_f32 v99, v171, s64, -v73\n\t"
"v_accvgpr_write_b32 a119, v99\n\t"
"s_waitcnt lgkmcnt(0)\n\t"
"s_barrier\n\t"
"v_mfma_f32_16x16x32_bf16 a[116:119], a[212:215], a[172:175], a[116:119]\n\t"
"ds_read_b128 a[168:171], v244 offset:0\n\t"
"v_mfma_f32_16x16x32_bf16 a[116:119], a[216:219], a[176:179], a[116:119]\n\t"
"buffer_load_dwordx4 a[208:211], v252, s[8:11], s20 offen offset:1024 sc1\n\t"
"v_mfma_f32_16x16x32_bf16 a[116:119], a[220:223], a[180:183], a[116:119]\n\t"
"ds_read_b128 a[172:175], v244 offset:512\n\t"
"v_fma_f32 v98, v172, s64, -v74\n\t"
"v_fma_f32 v99, v173, s64, -v75\n\t"
"v_accvgpr_write_b32 a124, v98\n\t"
"v_fma_f32 v98, v174, s64, -v76\n\t"
"v_accvgpr_write_b32 a125, v99\n\t"
"v_fma_f32 v99, v175, s64, -v77\n\t"
"v_accvgpr_write_b32 a126, v98\n\t"
"v_accvgpr_write_b32 a127, v99\n\t"
"v_fma_f32 v98, v176, s64, -v78\n\t"
"v_fma_f32 v99, v177, s64, -v79\n\t"
"v_mfma_f32_16x16x32_bf16 a[124:127], a[212:215], a[176:179], a[124:127]\n\t"
"ds_read_b128 a[176:179], v244 offset:1024\n\t"
"v_accvgpr_write_b32 a132, v98\n\t"
"v_mfma_f32_16x16x32_bf16 a[124:127], a[216:219], a[180:183], a[124:127]\n\t"
"v_fma_f32 v98, v178, s64, -v80\n\t"
"v_accvgpr_write_b32 a133, v99\n\t"
"v_mfma_f32_16x16x32_bf16 a[124:127], a[220:223], a[184:187], a[124:127]\n\t"
"v_fma_f32 v99, v179, s64, -v81\n\t"
"v_accvgpr_write_b32 a134, v98\n\t"
"v_accvgpr_write_b32 a135, v99\n\t"
"v_fma_f32 v98, v180, s64, -v82\n\t"
"v_fma_f32 v99, v181, s64, -v83\n\t"
"v_mfma_f32_16x16x32_bf16 a[132:135], a[212:215], a[180:183], a[132:135]\n\t"
"ds_read_b128 a[180:183], v244 offset:1536\n\t"
"v_accvgpr_write_b32 a140, v98\n\t"
"v_mfma_f32_16x16x32_bf16 a[132:135], a[216:219], a[184:187], a[132:135]\n\t"
"v_fma_f32 v98, v182, s64, -v84\n\t"
"v_accvgpr_write_b32 a141, v99\n\t"
"v_mfma_f32_16x16x32_bf16 a[132:135], a[220:223], a[188:191], a[132:135]\n\t"
"buffer_load_dwordx4 v[248:251], v252, s[8:11], s20 offen offset:2048 sc1\n\t"
"v_fma_f32 v99, v183, s64, -v85\n\t"
"v_accvgpr_write_b32 a142, v98\n\t"
"v_accvgpr_write_b32 a143, v99\n\t"
"v_fma_f32 v98, v184, s64, -v86\n\t"
"v_fma_f32 v99, v185, s64, -v87\n\t"
"v_mfma_f32_16x16x32_bf16 a[140:143], a[212:215], a[184:187], a[140:143]\n\t"
"ds_read_b128 a[184:187], v244 offset:2048\n\t"
"v_accvgpr_write_b32 a148, v98\n\t"
"v_mfma_f32_16x16x32_bf16 a[140:143], a[216:219], a[188:191], a[140:143]\n\t"
"v_fma_f32 v98, v186, s64, -v88\n\t"
"v_accvgpr_write_b32 a149, v99\n\t"
"v_mfma_f32_16x16x32_bf16 a[140:143], a[220:223], a[192:195], a[140:143]\n\t"
"v_fma_f32 v99, v187, s64, -v89\n\t"
"v_accvgpr_write_b32 a150, v98\n\t"
"v_accvgpr_write_b32 a151, v99\n\t"
"v_fma_f32 v98, v188, s64, -v90\n\t"
"v_fma_f32 v99, v189, s64, -v91\n\t"
"v_mfma_f32_16x16x32_bf16 a[148:151], a[212:215], a[188:191], a[148:151]\n\t"
"s_mov_b32 s20, 86016\n\t"
"ds_read_b128 a[188:191], v244 offset:2560\n\t"
"v_accvgpr_write_b32 a156, v98\n\t"
"v_mfma_f32_16x16x32_bf16 a[148:151], a[216:219], a[192:195], a[148:151]\n\t"
"v_fma_f32 v98, v190, s64, -v92\n\t"
"v_accvgpr_write_b32 a157, v99\n\t"
"v_mfma_f32_16x16x32_bf16 a[148:151], a[220:223], a[196:199], a[148:151]\n\t"
"v_fma_f32 v99, v191, s64, -v93\n\t"
"v_accvgpr_write_b32 a158, v98\n\t"
"v_accvgpr_write_b32 a159, v99\n\t"
"v_fma_f32 v98, v192, s64, -v94\n\t"
"v_fma_f32 v99, v193, s64, -v95\n\t"
"v_mfma_f32_16x16x32_bf16 a[156:159], a[212:215], a[192:195], a[156:159]\n\t"
"ds_read_b128 a[192:195], v244 offset:3072\n\t"
"v_accvgpr_write_b32 a164, v98\n\t"
"v_mfma_f32_16x16x32_bf16 a[156:159], a[216:219], a[196:199], a[156:159]\n\t"
"v_fma_f32 v98, v194, s64, -v96\n\t"
"v_accvgpr_write_b32 a165, v99\n\t"
"v_mfma_f32_16x16x32_bf16 a[156:159], a[220:223], a[200:203], a[156:159]\n\t"
"v_fma_f32 v99, v195, s64, -v97\n\t"
"v_accvgpr_write_b32 a166, v98\n\t"
"v_accvgpr_write_b32 a167, v99\n\t"
"v_fma_f32 v0, v2, s52, v100\n\t"
"v_fma_f32 v1, v3, s52, v101\n\t"
"v_mfma_f32_16x16x32_bf16 a[164:167], a[212:215], a[196:199], a[164:167]\n\t"
"ds_read_b128 a[196:199], v244 offset:3584\n\t"
"v_fma_f32 v100, -v100, s66, v2\n\t"
"v_mfma_f32_16x16x32_bf16 a[164:167], a[216:219], a[200:203], a[164:167]\n\t"
"ds_read_b128 a[200:203], v244 offset:4096\n\t"
"v_fma_f32 v2, v4, s52, v102\n\t"
"v_mfma_f32_16x16x32_bf16 a[164:167], a[220:223], a[204:207], a[164:167]\n\t"
"ds_read_b128 a[204:207], v244 offset:4608\n\t"
"v_fma_f32 v101, -v101, s66, v3\n\t"
"v_fma_f32 v3, v5, s52, v103\n\t"
"v_fma_f32 v102, -v102, s66, v4\n\t"
"v_fma_f32 v103, -v103, s66, v5\n\t"
"v_fma_f32 v4, v6, s52, v104\n\t"
"v_fma_f32 v5, v7, s52, v105\n\t"
"Lst3_%=:\n\t"
"s_waitcnt vmcnt(15) lgkmcnt(7)\n\t"
"v_mfma_f32_16x16x32_bf16 v[0:3], a[224:227], a[168:171], v[0:3]\n\t"
"buffer_load_dwordx4 a[212:215], v252, s[8:11], s20 offen offset:0 sc1\n\t"
"v_mfma_f32_16x16x32_bf16 v[0:3], a[228:231], a[172:175], v[0:3]\n\t"
"buffer_load_dwordx4 a[216:219], v252, s[8:11], s20 offen offset:1024 sc1\n\t"
"v_mfma_f32_16x16x32_bf16 v[0:3], a[232:235], a[176:179], v[0:3]\n\t"
"v_fma_f32 v104, -v104, s66, v6\n\t"
"v_fma_f32 v6, v8, s52, v106\n\t"
"v_fma_f32 v105, -v105, s66, v7\n\t"
"v_fma_f32 v7, v9, s52, v107\n\t"
"v_fma_f32 v106, -v106, s66, v8\n\t"
"v_fma_f32 v107, -v107, s66, v9\n\t"
"v_fma_f32 v8, v10, s52, v108\n\t"
"v_fma_f32 v9, v11, s52, v109\n\t"
"s_waitcnt lgkmcnt(6)\n\t"
"v_mfma_f32_16x16x32_bf16 v[4:7], a[224:227], a[172:175], v[4:7]\n\t"
"buffer_load_dwordx4 a[220:223], v252, s[8:11], s20 offen offset:2048 sc1\n\t"
"v_mfma_f32_16x16x32_bf16 v[4:7], a[228:231], a[176:179], v[4:7]\n\t"
"v_fma_f32 v108, -v108, s66, v10\n\t"
"v_fma_f32 v10, v12, s52, v110\n\t"
"v_mfma_f32_16x16x32_bf16 v[4:7], a[232:235], a[180:183], v[4:7]\n\t"
"v_fma_f32 v109, -v109, s66, v11\n\t"
"v_fma_f32 v11, v13, s52, v111\n\t"
"v_fma_f32 v110, -v110, s66, v12\n\t"
"v_fma_f32 v111, -v111, s66, v13\n\t"
"v_fma_f32 v12, v14, s52, v112\n\t"
"v_fma_f32 v13, v15, s52, v113\n\t"
"s_waitcnt lgkmcnt(5)\n\t"
"v_mfma_f32_16x16x32_bf16 v[8:11], a[224:227], a[176:179], v[8:11]\n\t"
"v_fma_f32 v112, -v112, s66, v14\n\t"
"v_fma_f32 v14, v16, s52, v114\n\t"
"v_mfma_f32_16x16x32_bf16 v[8:11], a[228:231], a[180:183], v[8:11]\n\t"
"v_fma_f32 v113, -v113, s66, v15\n\t"
"v_fma_f32 v15, v17, s52, v115\n\t"
"v_mfma_f32_16x16x32_bf16 v[8:11], a[232:235], a[184:187], v[8:11]\n\t"
"v_fma_f32 v114, -v114, s66, v16\n\t"
"v_fma_f32 v115, -v115, s66, v17\n\t"
"v_fma_f32 v16, v18, s52, v116\n\t"
"v_fma_f32 v17, v19, s52, v117\n\t"
"s_waitcnt lgkmcnt(4)\n\t"
"v_mfma_f32_16x16x32_bf16 v[12:15], a[224:227], a[180:183], v[12:15]\n\t"
"v_fma_f32 v116, -v116, s66, v18\n\t"
"v_fma_f32 v18, v20, s52, v118\n\t"
"v_mfma_f32_16x16x32_bf16 v[12:15], a[228:231], a[184:187], v[12:15]\n\t"
"v_fma_f32 v117, -v117, s66, v19\n\t"
"v_fma_f32 v19, v21, s52, v119\n\t"
"v_mfma_f32_16x16x32_bf16 v[12:15], a[232:235], a[188:191], v[12:15]\n\t"
"v_fma_f32 v118, -v118, s66, v20\n\t"
"v_fma_f32 v119, -v119, s66, v21\n\t"
"v_fma_f32 v20, v22, s52, v120\n\t"
"v_fma_f32 v21, v23, s52, v121\n\t"
"s_waitcnt lgkmcnt(3)\n\t"
"v_mfma_f32_16x16x32_bf16 v[16:19], a[224:227], a[184:187], v[16:19]\n\t"
"v_fma_f32 v120, -v120, s66, v22\n\t"
"v_fma_f32 v22, v24, s52, v122\n\t"
"v_mfma_f32_16x16x32_bf16 v[16:19], a[228:231], a[188:191], v[16:19]\n\t"
"v_fma_f32 v121, -v121, s66, v23\n\t"
"v_fma_f32 v23, v25, s52, v123\n\t"
"v_mfma_f32_16x16x32_bf16 v[16:19], a[232:235], a[192:195], v[16:19]\n\t"
"ds_read_b64_tr_b16 v[228:229], v246 offset:512\n\t"
"v_fma_f32 v122, -v122, s66, v24\n\t"
"v_fma_f32 v123, -v123, s66, v25\n\t"
"v_fma_f32 v24, v26, s52, v124\n\t"
"v_fma_f32 v25, v27, s52, v125\n\t"
"s_waitcnt lgkmcnt(3)\n\t"
"v_mfma_f32_16x16x32_bf16 v[20:23], a[224:227], a[188:191], v[20:23]\n\t"
"s_mov_b32 s20, 98304\n\t"
"ds_read_b64_tr_b16 v[230:231], v246 offset:3584\n\t"
"v_fma_f32 v124, -v124, s66, v26\n\t"
"v_mfma_f32_16x16x32_bf16 v[20:23], a[228:231], a[192:195], v[20:23]\n\t"
"v_fma_f32 v26, v28, s52, v126\n\t"
"v_fma_f32 v125, -v125, s66, v27\n\t"
"v_mfma_f32_16x16x32_bf16 v[20:23], a[232:235], a[196:199], v[20:23]\n\t"
"v_fma_f32 v27, v29, s52, v127\n\t"
"v_fma_f32 v126, -v126, s66, v28\n\t"
"v_fma_f32 v127, -v127, s66, v29\n\t"
"v_fma_f32 v28, v30, s52, v128\n\t"
"v_fma_f32 v29, v31, s52, v129\n\t"
"s_waitcnt lgkmcnt(3)\n\t"
"v_mfma_f32_16x16x32_bf16 v[24:27], a[224:227], a[192:195], v[24:27]\n\t"
"v_fma_f32 v128, -v128, s66, v30\n\t"
"v_fma_f32 v30, v32, s52, v130\n\t"
"v_mfma_f32_16x16x32_bf16 v[24:27], a[228:231], a[196:199], v[24:27]\n\t"
"v_fma_f32 v129, -v129, s66, v31\n\t"
"v_fma_f32 v31, v33, s52, v131\n\t"
"v_mfma_f32_16x16x32_bf16 v[24:27], a[232:235], a[200:203], v[24:27]\n\t"
"v_fma_f32 v130, -v130, s66, v32\n\t"
"v_fma_f32 v131, -v131, s66, v33\n\t"
"v_fma_f32 v32, v34, s52, v132\n\t"
"v_fma_f32 v33, v35, s52, v133\n\t"
"s_waitcnt lgkmcnt(2)\n\t"
"v_mfma_f32_16x16x32_bf16 v[28:31], a[224:227], a[196:199], v[28:31]\n\t"
"buffer_load_dwordx4 a[224:227], v252, s[8:11], s20 offen offset:0 sc1\n\t"
"v_mfma_f32_16x16x32_bf16 v[28:31], a[228:231], a[200:203], v[28:31]\n\t"
"ds_read_b64_tr_b16 v[224:225], v246 offset:6656\n\t"
"v_fma_f32 v132, -v132, s66, v34\n\t"
"v_mfma_f32_16x16x32_bf16 v[28:31], a[232:235], a[204:207], v[28:31]\n\t"
"ds_read_b64_tr_b16 v[226:227], v246 offset:9728\n\t"
"v_fma_f32 v34, v36, s52, v134\n\t"
"v_fma_f32 v133, -v133, s66, v35\n\t"
"v_fma_f32 v35, v37, s52, v135\n\t"
"v_fma_f32 v134, -v134, s66, v36\n\t"
"v_fma_f32 v135, -v135, s66, v37\n\t"
"v_fma_f32 v36, v38, s52, v136\n\t"
"v_fma_f32 v37, v39, s52, v137\n\t"
"s_waitcnt vmcnt(16)\n\t"
"v_mfma_f32_16x16x32_bf16 v[32:35], a[236:239], a[168:171], v[32:35]\n\t"
"buffer_load_dwordx4 a[228:231], v252, s[8:11], s20 offen offset:1024 sc1\n\t"
"v_mfma_f32_16x16x32_bf16 v[32:35], a[240:243], a[172:175], v[32:35]\n\t"
"v_fma_f32 v136, -v136, s66, v38\n\t"
"v_fma_f32 v38, v40, s52, v138\n\t"
"v_mfma_f32_16x16x32_bf16 v[32:35], a[244:247], a[176:179], v[32:35]\n\t"
"v_fma_f32 v137, -v137, s66, v39\n\t"
"v_fma_f32 v39, v41, s52, v139\n\t"
"v_fma_f32 v138, -v138, s66, v40\n\t"
"v_fma_f32 v139, -v139, s66, v41\n\t"
"v_mfma_f32_16x16x32_bf16 v[36:39], a[236:239], a[172:175], v[36:39]\n\t"
"s_waitcnt lgkmcnt(2)\n\t"
"v_fma_f32 v40, v42, s52, v140\n\t"
"v_fma_f32 v41, v43, s52, v141\n\t"
"v_mfma_f32_16x16x32_bf16 v[36:39], a[240:243], a[176:179], v[36:39]\n\t"
"v_fma_f32 v140, -v140, s66, v42\n\t"
"v_fma_f32 v42, v44, s52, v142\n\t"
"v_mfma_f32_16x16x32_bf16 v[36:39], a[244:247], a[180:183], v[36:39]\n\t"
"v_fma_f32 v141, -v141, s66, v43\n\t"
"v_fma_f32 v43, v45, s52, v143\n\t"
"v_fma_f32 v142, -v142, s66, v44\n\t"
"v_fma_f32 v143, -v143, s66, v45\n\t"
"v_mfma_f32_16x16x32_bf16 v[228:231], v[228:231], v[220:223], 0\n\t"
"buffer_load_dwordx4 a[232:235], v252, s[8:11], s20 offen offset:2048 sc1\n\t"
"v_fma_f32 v44, v46, s52, v144\n\t"
"v_fma_f32 v45, v47, s52, v145\n\t"
"v_mfma_f32_16x16x32_bf16 v[40:43], a[236:239], a[176:179], v[40:43]\n\t"
"ds_read_b64_tr_b16 v[232:233], v246 offset:12800\n\t"
"v_fma_f32 v144, -v144, s66, v46\n\t"
"v_mfma_f32_16x16x32_bf16 v[40:43], a[240:243], a[180:183], v[40:43]\n\t"
"ds_read_b64_tr_b16 v[234:235], v246 offset:15872\n\t"
"v_fma_f32 v46, v48, s52, v146\n\t"
"v_mfma_f32_16x16x32_bf16 v[40:43], a[244:247], a[184:187], v[40:43]\n\t"
"v_fma_f32 v145, -v145, s66, v47\n\t"
"v_fma_f32 v47, v49, s52, v147\n\t"
"v_fma_f32 v146, -v146, s66, v48\n\t"
"v_fma_f32 v147, -v147, s66, v49\n\t"
"v_mfma_f32_16x16x32_bf16 v[44:47], a[236:239], a[180:183], v[44:47]\n\t"
"s_waitcnt lgkmcnt(2)\n\t"
"v_fma_f32 v48, v50, s52, v148\n\t"
"v_fma_f32 v49, v51, s52, v149\n\t"
"v_mfma_f32_16x16x32_bf16 v[44:47], a[240:243], a[184:187], v[44:47]\n\t"
"v_fma_f32 v148, -v148, s66, v50\n\t"
"v_fma_f32 v50, v52, s52, v150\n\t"
"v_mfma_f32_16x16x32_bf16 v[44:47], a[244:247], a[188:191], v[44:47]\n\t"
"v_fma_f32 v149, -v149, s66, v51\n\t"
"v_fma_f32 v51, v53, s52, v151\n\t"
"v_fma_f32 v150, -v150, s66, v52\n\t"
"v_fma_f32 v151, -v151, s66, v53\n\t"
"v_mfma_f32_16x16x32_bf16 v[224:227], v[224:227], v[220:223], 0\n\t"
"v_fma_f32 v52, v54, s52, v152\n\t"
"v_fma_f32 v53, v55, s52, v153\n\t"
"v_mfma_f32_16x16x32_bf16 v[48:51], a[236:239], a[184:187], v[48:51]\n\t"
"v_fma_f32 v152, -v152, s66, v54\n\t"
"v_fma_f32 v54, v56, s52, v154\n\t"
"v_mfma_f32_16x16x32_bf16 v[48:51], a[240:243], a[188:191], v[48:51]\n\t"
"v_fma_f32 v153, -v153, s66, v55\n\t"
"v_fma_f32 v55, v57, s52, v155\n\t"
"v_mfma_f32_16x16x32_bf16 v[48:51], a[244:247], a[192:195], v[48:51]\n\t"
"v_fma_f32 v154, -v154, s66, v56\n\t"
"v_fma_f32 v155, -v155, s66, v57\n\t"
"v_cvt_pk_bf16_f32 v228, v228, v229\n\t"
"v_cvt_pk_bf16_f32 v229, v230, v231\n\t"
"v_fma_f32 v56, v58, s52, v156\n\t"
"v_fma_f32 v57, v59, s52, v157\n\t"
"v_mfma_f32_16x16x32_bf16 v[52:55], a[236:239], a[188:191], v[52:55]\n\t"
"v_fma_f32 v156, -v156, s66, v58\n\t"
"v_fma_f32 v58, v60, s52, v158\n\t"
"v_mfma_f32_16x16x32_bf16 v[52:55], a[240:243], a[192:195], v[52:55]\n\t"
"s_mov_b32 s20, 331776\n\t"
"v_fma_f32 v157, -v157, s66, v59\n\t"
"v_fma_f32 v59, v61, s52, v159\n\t"
"v_mfma_f32_16x16x32_bf16 v[52:55], a[244:247], a[196:199], v[52:55]\n\t"
"v_fma_f32 v158, -v158, s66, v60\n\t"
"v_fma_f32 v159, -v159, s66, v61\n\t"
"v_mfma_f32_16x16x32_bf16 v[56:59], a[236:239], a[192:195], v[56:59]\n\t"
"s_waitcnt lgkmcnt(0)\n\t"
"v_fma_f32 v60, v62, s52, v160\n\t"
"v_fma_f32 v61, v63, s52, v161\n\t"
"v_mfma_f32_16x16x32_bf16 v[56:59], a[240:243], a[196:199], v[56:59]\n\t"
"v_fma_f32 v160, -v160, s66, v62\n\t"
"v_fma_f32 v62, v64, s52, v162\n\t"
"v_mfma_f32_16x16x32_bf16 v[56:59], a[244:247], a[200:203], v[56:59]\n\t"
"v_fma_f32 v161, -v161, s66, v63\n\t"
"v_fma_f32 v63, v65, s52, v163\n\t"
"v_fma_f32 v162, -v162, s66, v64\n\t"
"v_fma_f32 v163, -v163, s66, v65\n\t"
"v_cvt_pk_bf16_f32 v230, v224, v225\n\t"
"v_cvt_pk_bf16_f32 v231, v226, v227\n\t"
"v_mfma_f32_16x16x32_bf16 v[232:235], v[232:235], v[220:223], 0\n\t"
"ds_read_b64_tr_b16 v[224:225], v246 offset:20480\n\t"
"v_fma_f32 v64, v66, s52, v164\n\t"
"v_fma_f32 v65, v67, s52, v165\n\t"
"v_mfma_f32_16x16x32_bf16 v[60:63], a[236:239], a[196:199], v[60:63]\n\t"
"ds_read_b64_tr_b16 v[226:227], v246 offset:23552\n\t"
"v_fma_f32 v164, -v164, s66, v66\n\t"
"v_mfma_f32_16x16x32_bf16 v[60:63], a[240:243], a[200:203], v[60:63]\n\t"
"v_fma_f32 v66, v68, s52, v166\n\t"
"v_fma_f32 v165, -v165, s66, v67\n\t"
"v_mfma_f32_16x16x32_bf16 v[60:63], a[244:247], a[204:207], v[60:63]\n\t"
"v_fma_f32 v67, v69, s52, v167\n\t"
"v_fma_f32 v166, -v166, s66, v68\n\t"
"v_fma_f32 v167, -v167, s66, v69\n\t"
"v_fma_f32 v68, v70, s52, v168\n\t"
"v_fma_f32 v69, v71, s52, v169\n\t"
"s_waitcnt vmcnt(15)\n\t"
"v_mfma_f32_16x16x32_bf16 v[64:67], a[248:251], a[168:171], v[64:67]\n\t"
"buffer_load_dwordx4 a[236:239], v252, s[8:11], s20 offen offset:0 sc1\n\t"
"v_mfma_f32_16x16x32_bf16 v[64:67], a[252:255], a[172:175], v[64:67]\n\t"
"buffer_load_dwordx4 a[240:243], v252, s[8:11], s20 offen offset:1024 sc1\n\t"
"v_mfma_f32_16x16x32_bf16 v[64:67], v[236:239], a[176:179], v[64:67]\n\t"
"ds_read_b128 a[168:171], v244 offset:9216\n\t"
"v_fma_f32 v168, -v168, s66, v70\n\t"
"v_fma_f32 v70, v72, s52, v170\n\t"
"v_fma_f32 v169, -v169, s66, v71\n\t"
"v_fma_f32 v71, v73, s52, v171\n\t"
"v_fma_f32 v170, -v170, s66, v72\n\t"
"v_fma_f32 v171, -v171, s66, v73\n\t"
"v_mfma_f32_16x16x32_bf16 v[68:71], a[248:251], a[172:175], v[68:71]\n\t"
"s_waitcnt lgkmcnt(1)\n\t"
"ds_read_b128 a[172:175], v244 offset:9728\n\t"
"v_fma_f32 v72, v74, s52, v172\n\t"
"v_mfma_f32_16x16x32_bf16 v[68:71], a[252:255], a[176:179], v[68:71]\n\t"
"v_fma_f32 v73, v75, s52, v173\n\t"
"v_fma_f32 v172, -v172, s66, v74\n\t"
"v_mfma_f32_16x16x32_bf16 v[68:71], v[236:239], a[180:183], v[68:71]\n\t"
"v_fma_f32 v74, v76, s52, v174\n\t"
"v_fma_f32 v173, -v173, s66, v75\n\t"
"v_fma_f32 v75, v77, s52, v175\n\t"
"v_fma_f32 v174, -v174, s66, v76\n\t"
"v_fma_f32 v175, -v175, s66, v77\n\t"
"v_cvt_pk_bf16_f32 v232, v232, v233\n\t"
"v_cvt_pk_bf16_f32 v233, v234, v235\n\t"
"v_mfma_f32_16x16x32_bf16 v[224:227], v[224:227], v[220:223], 0\n\t"
"buffer_load_dwordx4 a[244:247], v252, s[8:11], s20 offen offset:2048 sc1\n\t"
"v_fma_f32 v76, v78, s52, v176\n\t"
"v_fma_f32 v77, v79, s52, v177\n\t"
"v_mfma_f32_16x16x32_bf16 v[72:75], a[248:251], a[176:179], v[72:75]\n\t"
"ds_read_b128 a[176:179], v244 offset:10240\n\t"
"v_fma_f32 v176, -v176, s66, v78\n\t"
"v_mfma_f32_16x16x32_bf16 v[72:75], a[252:255], a[180:183], v[72:75]\n\t"
"v_fma_f32 v78, v80, s52, v178\n\t"
"v_fma_f32 v177, -v177, s66, v79\n\t"
"v_mfma_f32_16x16x32_bf16 v[72:75], v[236:239], a[184:187], v[72:75]\n\t"
"v_fma_f32 v79, v81, s52, v179\n\t"
"v_fma_f32 v178, -v178, s66, v80\n\t"
"v_fma_f32 v179, -v179, s66, v81\n\t"
"v_fma_f32 v80, v82, s52, v180\n\t"
"v_fma_f32 v81, v83, s52, v181\n\t"
"v_mfma_f32_16x16x32_bf16 v[76:79], a[248:251], a[180:183], v[76:79]\n\t"
"ds_read_b128 a[180:183], v244 offset:10752\n\t"
"v_fma_f32 v180, -v180, s66, v82\n\t"
"v_mfma_f32_16x16x32_bf16 v[76:79], a[252:255], a[184:187], v[76:79]\n\t"
"v_fma_f32 v82, v84, s52, v182\n\t"
"v_fma_f32 v181, -v181, s66, v83\n\t"
"v_mfma_f32_16x16x32_bf16 v[76:79], v[236:239], a[188:191], v[76:79]\n\t"
"v_fma_f32 v83, v85, s52, v183\n\t"
"v_fma_f32 v182, -v182, s66, v84\n\t"
"v_fma_f32 v183, -v183, s66, v85\n\t"
"v_fma_f32 v84, v86, s52, v184\n\t"
"v_fma_f32 v85, v87, s52, v185\n\t"
"v_mfma_f32_16x16x32_bf16 v[80:83], a[248:251], a[184:187], v[80:83]\n\t"
"ds_read_b128 a[184:187], v244 offset:11264\n\t"
"v_fma_f32 v184, -v184, s66, v86\n\t"
"v_mfma_f32_16x16x32_bf16 v[80:83], a[252:255], a[188:191], v[80:83]\n\t"
"s_mov_b32 s20, 344064\n\t"
"v_fma_f32 v86, v88, s52, v186\n\t"
"v_fma_f32 v185, -v185, s66, v87\n\t"
"v_mfma_f32_16x16x32_bf16 v[80:83], v[236:239], a[192:195], v[80:83]\n\t"
"v_fma_f32 v87, v89, s52, v187\n\t"
"v_fma_f32 v186, -v186, s66, v88\n\t"
"v_fma_f32 v187, -v187, s66, v89\n\t"
"v_cvt_pk_bf16_f32 v234, v224, v225\n\t"
"v_cvt_pk_bf16_f32 v235, v226, v227\n\t"
"v_fma_f32 v88, v90, s52, v188\n\t"
"v_fma_f32 v89, v91, s52, v189\n\t"
"v_mfma_f32_16x16x32_bf16 v[84:87], a[248:251], a[188:191], v[84:87]\n\t"
"ds_read_b64_tr_b16 v[224:225], v246 offset:26624\n\t"
"v_fma_f32 v188, -v188, s66, v90\n\t"
"v_mfma_f32_16x16x32_bf16 v[84:87], a[252:255], a[192:195], v[84:87]\n\t"
"ds_read_b64_tr_b16 v[226:227], v246 offset:29696\n\t"
"v_fma_f32 v90, v92, s52, v190\n\t"
"v_mfma_f32_16x16x32_bf16 v[84:87], v[236:239], a[196:199], v[84:87]\n\t"
"ds_read_b128 a[188:191], v244 offset:11776\n\t"
"v_fma_f32 v189, -v189, s66, v91\n\t"
"v_fma_f32 v91, v93, s52, v191\n\t"
"v_fma_f32 v190, -v190, s66, v92\n\t"
"v_fma_f32 v191, -v191, s66, v93\n\t"
"v_fma_f32 v92, v94, s52, v192\n\t"
"v_fma_f32 v93, v95, s52, v193\n\t"
"v_mfma_f32_16x16x32_bf16 v[88:91], a[248:251], a[192:195], v[88:91]\n\t"
"ds_read_b128 a[192:195], v244 offset:12288\n\t"
"v_fma_f32 v192, -v192, s66, v94\n\t"
"v_mfma_f32_16x16x32_bf16 v[88:91], a[252:255], a[196:199], v[88:91]\n\t"
"v_fma_f32 v94, v96, s52, v194\n\t"
"v_fma_f32 v193, -v193, s66, v95\n\t"
"v_mfma_f32_16x16x32_bf16 v[88:91], v[236:239], a[200:203], v[88:91]\n\t"
"v_fma_f32 v95, v97, s52, v195\n\t"
"v_fma_f32 v194, -v194, s66, v96\n\t"
"v_fma_f32 v195, -v195, s66, v97\n\t"
"v_mfma_f32_16x16x32_bf16 v[92:95], a[248:251], a[196:199], v[92:95]\n\t"
"s_waitcnt lgkmcnt(2)\n\t"
"ds_read_b128 a[196:199], v244 offset:12800\n\t"
"v_mfma_f32_16x16x32_bf16 v[92:95], a[252:255], a[200:203], v[92:95]\n\t"
"buffer_load_dwordx4 v[96:99], v252, s[8:11], s20 offen offset:0 sc1\n\t"
"v_mfma_f32_16x16x32_bf16 v[92:95], v[236:239], a[204:207], v[92:95]\n\t"
"buffer_load_dwordx4 a[248:251], v252, s[8:11], s20 offen offset:1024 sc1\n\t"
"v_mfma_f32_16x16x32_bf16 v[224:227], v[224:227], v[220:223], 0\n\t"
"s_waitcnt vmcnt(11)\n\t"
"ds_read_b128 a[200:203], v244 offset:13312\n\t"
"v_mfma_f32_16x16x32_bf16 v[100:103], v[240:243], a[168:171], v[100:103]\n\t"
"ds_read_b128 a[204:207], v244 offset:13824\n\t"
"v_mfma_f32_16x16x32_bf16 v[100:103], a[208:211], a[172:175], v[100:103]\n\t"
"buffer_load_dwordx4 v[236:239], v252, s[8:11], s20 offen offset:2048 sc1\n\t"
"v_mfma_f32_16x16x32_bf16 v[100:103], v[248:251], a[176:179], v[100:103]\n\t"
"s_mov_b32 s20, 356352\n\t"
"v_mfma_f32_16x16x32_bf16 v[104:107], v[240:243], a[172:175], v[104:107]\n\t"
"buffer_load_dwordx4 a[252:255], v252, s[8:11], s20 offen offset:0 sc1\n\t"
"v_mfma_f32_16x16x32_bf16 v[104:107], a[208:211], a[176:179], v[104:107]\n\t"
"v_cvt_pk_bf16_f32 v224, v224, v225\n\t"
"v_mfma_f32_16x16x32_bf16 v[104:107], v[248:251], a[180:183], v[104:107]\n\t"
"s_waitcnt lgkmcnt(0)\n\t"
"s_barrier\n\t"
"v_mfma_f32_16x16x32_bf16 v[108:111], v[240:243], a[176:179], v[108:111]\n\t"
"v_cvt_pk_bf16_f32 v225, v226, v227\n\t"
"v_mfma_f32_16x16x32_bf16 v[108:111], a[208:211], a[180:183], v[108:111]\n\t"
"v_mfma_f32_16x16x32_bf16 v[108:111], v[248:251], a[184:187], v[108:111]\n\t"
"ds_write2st64_b64 v245, v[228:229], v[230:231] offset0:0 offset1:1\n\t"
"v_mfma_f32_16x16x32_bf16 v[112:115], v[240:243], a[180:183], v[112:115]\n\t"
"v_mfma_f32_16x16x32_bf16 v[112:115], a[208:211], a[184:187], v[112:115]\n\t"
"ds_read_b64_tr_b16 v[228:229], v246 offset:32768\n\t"
"v_mfma_f32_16x16x32_bf16 v[112:115], v[248:251], a[188:191], v[112:115]\n\t"
"ds_write2st64_b64 v245, v[232:233], v[234:235] offset0:2 offset1:3\n\t"
"v_mfma_f32_16x16x32_bf16 v[116:119], v[240:243], a[184:187], v[116:119]\n\t"
"v_mfma_f32_16x16x32_bf16 v[116:119], a[208:211], a[188:191], v[116:119]\n\t"
"ds_read_b64_tr_b16 v[230:231], v246 offset:35840\n\t"
"v_mfma_f32_16x16x32_bf16 v[116:119], v[248:251], a[192:195], v[116:119]\n\t"
"s_add_u32 m0, s24, 73088\n\t"
"s_add_u32 s28, s21, 2162688\n\t"
"ds_write_b64 v245, v[224:225] offset:2048\n\t"
"v_mfma_f32_16x16x32_bf16 v[120:123], v[240:243], a[188:191], v[120:123]\n\t"
"ds_read_b64_tr_b16 v[232:233], v246 offset:40448\n\t"
"v_mfma_f32_16x16x32_bf16 v[120:123], a[208:211], a[192:195], v[120:123]\n\t"
"ds_read_b64_tr_b16 v[234:235], v246 offset:43520\n\t"
"v_mfma_f32_16x16x32_bf16 v[120:123], v[248:251], a[196:199], v[120:123]\n\t"
"s_waitcnt lgkmcnt(3)\n\t"
"ds_read_b64_tr_b16 v[224:225], v246 offset:46592\n\t"
"v_mfma_f32_16x16x32_bf16 v[228:231], v[228:231], v[220:223], 0\n\t"
"ds_read_b64_tr_b16 v[226:227], v246 offset:49664\n\t"
"v_mfma_f32_16x16x32_bf16 v[124:127], v[240:243], a[192:195], v[124:127]\n\t"
"buffer_load_dwordx4 v247, s[12:15], s28 offen offset:128 nt lds\n\t"
"v_mfma_f32_16x16x32_bf16 v[124:127], a[208:211], a[196:199], v[124:127]\n\t"
"s_add_u32 m0, s24, 80768\n\t"
"s_add_u32 s28, s21, 2359296\n\t"
"v_mfma_f32_16x16x32_bf16 v[124:127], v[248:251], a[200:203], v[124:127]\n\t"
"s_waitcnt lgkmcnt(2)\n\t"
"buffer_load_dwordx4 v247, s[12:15], s28 offen offset:128 nt lds\n\t"
"s_add_u32 m0, s24, 27008\n\t"
"s_add_u32 s28, s21, 786432\n\t"
"v_mfma_f32_16x16x32_bf16 v[232:235], v[232:235], v[220:223], 0\n\t"
"buffer_load_dwordx4 v247, s[12:15], s28 offen offset:128 nt lds\n\t"
"v_mfma_f32_16x16x32_bf16 v[128:131], v[240:243], a[196:199], v[128:131]\n\t"
"s_waitcnt lgkmcnt(0)\n\t"
"s_add_u32 m0, s24, 86912\n\t"
"s_add_u32 s28, s21, 2555904\n\t"
"v_mfma_f32_16x16x32_bf16 v[128:131], a[208:211], a[200:203], v[128:131]\n\t"
"buffer_load_dwordx4 v247, s[12:15], s28 offen offset:128 nt lds\n\t"
"v_mfma_f32_16x16x32_bf16 v[128:131], v[248:251], a[204:207], v[128:131]\n\t"
"s_waitcnt vmcnt(14)\n\t"
"s_add_u32 m0, s24, 33152\n\t"
"s_add_u32 s28, s21, 983040\n\t"
"v_mfma_f32_16x16x32_bf16 v[224:227], v[224:227], v[220:223], 0\n\t"
"buffer_load_dwordx4 v247, s[12:15], s28 offen offset:128 nt lds\n\t"
"v_cvt_pk_bf16_f32 v228, v228, v229\n\t"
"v_cvt_pk_bf16_f32 v229, v230, v231\n\t"
"v_mfma_f32_16x16x32_bf16 v[132:135], a[212:215], a[168:171], v[132:135]\n\t"
"s_add_u32 m0, s24, 40832\n\t"
"s_add_u32 s28, s21, 1179648\n\t"
"v_cvt_pk_bf16_f32 v230, v232, v233\n\t"
"v_mfma_f32_16x16x32_bf16 v[132:135], a[216:219], a[172:175], v[132:135]\n\t"
"v_cvt_pk_bf16_f32 v231, v234, v235\n\t"
"v_mfma_f32_16x16x32_bf16 v[132:135], a[220:223], a[176:179], v[132:135]\n\t"
"buffer_load_dwordx4 v247, s[12:15], s28 offen offset:128 nt lds\n\t"
"v_mfma_f32_16x16x32_bf16 v[136:139], a[212:215], a[172:175], v[136:139]\n\t"
"ds_read_b64_tr_b16 v[232:233], v246 offset:52736\n\t"
"v_mfma_f32_16x16x32_bf16 v[136:139], a[216:219], a[176:179], v[136:139]\n\t"
"ds_read_b64_tr_b16 v[234:235], v246 offset:55808\n\t"
"v_cvt_pk_bf16_f32 v224, v224, v225\n\t"
"v_mfma_f32_16x16x32_bf16 v[136:139], a[220:223], a[180:183], v[136:139]\n\t"
"ds_write2st64_b64 v245, v[228:229], v[230:231] offset0:5 offset1:6\n\t"
"v_mfma_f32_16x16x32_bf16 v[140:143], a[212:215], a[176:179], v[140:143]\n\t"
"v_cvt_pk_bf16_f32 v225, v226, v227\n\t"
"v_mfma_f32_16x16x32_bf16 v[140:143], a[216:219], a[180:183], v[140:143]\n\t"
"Lpg4_%=:\n\t"
"s_cbranch_vccnz Lpg8_%=\n\t"
"v_mfma_f32_16x16x32_bf16 v[140:143], a[220:223], a[184:187], v[140:143]\n\t"
"v_mfma_f32_16x16x32_bf16 v[144:147], a[212:215], a[180:183], v[144:147]\n\t"
"v_mfma_f32_16x16x32_bf16 v[144:147], a[216:219], a[184:187], v[144:147]\n\t"
"v_mfma_f32_16x16x32_bf16 v[144:147], a[220:223], a[188:191], v[144:147]\n\t"
"v_mfma_f32_16x16x32_bf16 v[148:151], a[212:215], a[184:187], v[148:151]\n\t"
"s_waitcnt lgkmcnt(1)\n\t"
"v_mfma_f32_16x16x32_bf16 v[148:151], a[216:219], a[188:191], v[148:151]\n\t"
"v_mfma_f32_16x16x32_bf16 v[148:151], a[220:223], a[192:195], v[148:151]\n\t"
"v_mfma_f32_16x16x32_bf16 v[232:235], v[232:235], v[220:223], 0\n\t"
"v_mfma_f32_16x16x32_bf16 v[152:155], a[212:215], a[188:191], v[152:155]\n\t"
"v_mfma_f32_16x16x32_bf16 v[152:155], a[216:219], a[192:195], v[152:155]\n\t"
"v_mfma_f32_16x16x32_bf16 v[152:155], a[220:223], a[196:199], v[152:155]\n\t"
"v_mfma_f32_16x16x32_bf16 v[156:159], a[212:215], a[192:195], v[156:159]\n\t"
"v_mfma_f32_16x16x32_bf16 v[156:159], a[216:219], a[196:199], v[156:159]\n\t"
"v_cvt_pk_bf16_f32 v226, v232, v233\n\t"
"v_cvt_pk_bf16_f32 v227, v234, v235\n\t"
"v_mfma_f32_16x16x32_bf16 v[156:159], a[220:223], a[200:203], v[156:159]\n\t"
"ds_write2st64_b64 v245, v[224:225], v[226:227] offset0:7 offset1:8\n\t"
"v_mfma_f32_16x16x32_bf16 v[160:163], a[212:215], a[196:199], v[160:163]\n\t"
"s_waitcnt vmcnt(13)\n\t"
"v_mfma_f32_16x16x32_bf16 v[160:163], a[216:219], a[200:203], v[160:163]\n\t"
"v_mfma_f32_16x16x32_bf16 v[160:163], a[220:223], a[204:207], v[160:163]\n\t"
"v_mfma_f32_16x16x32_bf16 v[164:167], a[224:227], a[168:171], v[164:167]\n\t"
"v_mfma_f32_16x16x32_bf16 v[164:167], a[228:231], a[172:175], v[164:167]\n\t"
"v_mfma_f32_16x16x32_bf16 v[164:167], a[232:235], a[176:179], v[164:167]\n\t"
"s_waitcnt lgkmcnt(0)\n\t"
"s_barrier\n\t"
"v_mfma_f32_16x16x32_bf16 v[168:171], a[224:227], a[172:175], v[168:171]\n\t"
"ds_read_b128 a[168:171], v244 offset:0\n\t"
"v_mfma_f32_16x16x32_bf16 v[168:171], a[228:231], a[176:179], v[168:171]\n\t"
"buffer_load_dwordx4 v[240:243], v252, s[8:11], s20 offen offset:1024 sc1\n\t"
"v_mfma_f32_16x16x32_bf16 v[168:171], a[232:235], a[180:183], v[168:171]\n\t"
"ds_read_b128 a[172:175], v244 offset:512\n\t"
"v_mfma_f32_16x16x32_bf16 v[172:175], a[224:227], a[176:179], v[172:175]\n\t"
"ds_read_b128 a[176:179], v244 offset:1024\n\t"
"v_mfma_f32_16x16x32_bf16 v[172:175], a[228:231], a[180:183], v[172:175]\n\t"
"v_mfma_f32_16x16x32_bf16 v[172:175], a[232:235], a[184:187], v[172:175]\n\t"
"v_mfma_f32_16x16x32_bf16 v[176:179], a[224:227], a[180:183], v[176:179]\n\t"
"ds_read_b128 a[180:183], v244 offset:1536\n\t"
"v_mfma_f32_16x16x32_bf16 v[176:179], a[228:231], a[184:187], v[176:179]\n\t"
"v_mfma_f32_16x16x32_bf16 v[176:179], a[232:235], a[188:191], v[176:179]\n\t"
"v_mfma_f32_16x16x32_bf16 v[180:183], a[224:227], a[184:187], v[180:183]\n\t"
"ds_read_b128 a[184:187], v244 offset:2048\n\t"
"v_mfma_f32_16x16x32_bf16 v[180:183], a[228:231], a[188:191], v[180:183]\n\t"
"v_mfma_f32_16x16x32_bf16 v[180:183], a[232:235], a[192:195], v[180:183]\n\t"
"v_mfma_f32_16x16x32_bf16 v[184:187], a[224:227], a[188:191], v[184:187]\n\t"
"ds_read_b128 a[188:191], v244 offset:2560\n\t"
"v_mfma_f32_16x16x32_bf16 v[184:187], a[228:231], a[192:195], v[184:187]\n\t"
"v_mfma_f32_16x16x32_bf16 v[184:187], a[232:235], a[196:199], v[184:187]\n\t"
"v_mfma_f32_16x16x32_bf16 v[188:191], a[224:227], a[192:195], v[188:191]\n\t"
"ds_read_b128 a[192:195], v244 offset:3072\n\t"
"v_mfma_f32_16x16x32_bf16 v[188:191], a[228:231], a[196:199], v[188:191]\n\t"
"v_mfma_f32_16x16x32_bf16 v[188:191], a[232:235], a[200:203], v[188:191]\n\t"
"v_mfma_f32_16x16x32_bf16 v[192:195], a[224:227], a[196:199], v[192:195]\n\t"
"s_waitcnt vmcnt(11) lgkmcnt(4)\n\t"
"ds_read_b128 a[196:199], v244 offset:3584\n\t"
"v_mfma_f32_16x16x32_bf16 v[192:195], a[228:231], a[200:203], v[192:195]\n\t"
"ds_read_b128 a[200:203], v244 offset:4096\n\t"
"v_mfma_f32_16x16x32_bf16 v[192:195], a[232:235], a[204:207], v[192:195]\n\t"
"ds_read_b128 a[204:207], v244 offset:4608\n\t"
"Lst4_%=:\n\t"
"v_mfma_f32_16x16x32_bf16 v[0:3], a[236:239], a[168:171], v[0:3]\n\t"
"s_waitcnt lgkmcnt(6)\n\t"
"v_xor_b32 v246, 0xc0, v246\n\t"
"v_mfma_f32_16x16x32_bf16 v[0:3], a[240:243], a[172:175], v[0:3]\n\t"
"v_mfma_f32_16x16x32_bf16 v[0:3], a[244:247], a[176:179], v[0:3]\n\t"
"v_mfma_f32_16x16x32_bf16 v[4:7], a[236:239], a[172:175], v[4:7]\n\t"
"s_waitcnt lgkmcnt(5)\n\t"
"v_mfma_f32_16x16x32_bf16 v[4:7], a[240:243], a[176:179], v[4:7]\n\t"
"v_mfma_f32_16x16x32_bf16 v[4:7], a[244:247], a[180:183], v[4:7]\n\t"
"v_mfma_f32_16x16x32_bf16 v[8:11], a[236:239], a[176:179], v[8:11]\n\t"
"s_waitcnt lgkmcnt(4)\n\t"
"v_mfma_f32_16x16x32_bf16 v[8:11], a[240:243], a[180:183], v[8:11]\n\t"
"v_mfma_f32_16x16x32_bf16 v[8:11], a[244:247], a[184:187], v[8:11]\n\t"
"v_mfma_f32_16x16x32_bf16 v[12:15], a[236:239], a[180:183], v[12:15]\n\t"
"s_waitcnt lgkmcnt(3)\n\t"
"v_mfma_f32_16x16x32_bf16 v[12:15], a[240:243], a[184:187], v[12:15]\n\t"
"v_mfma_f32_16x16x32_bf16 v[12:15], a[244:247], a[188:191], v[12:15]\n\t"
"buffer_load_dwordx4 a[208:211], v252, s[8:11], s20 offen offset:2048 sc1\n\t"
"v_mfma_f32_16x16x32_bf16 v[16:19], a[236:239], a[184:187], v[16:19]\n\t"
"s_waitcnt lgkmcnt(2)\n\t"
"s_mov_b32 s20, 368640\n\t"
"v_mfma_f32_16x16x32_bf16 v[16:19], a[240:243], a[188:191], v[16:19]\n\t"
"v_mfma_f32_16x16x32_bf16 v[16:19], a[244:247], a[192:195], v[16:19]\n\t"
"ds_read_b64_tr_b16 v[228:229], v246 offset:512\n\t"
"v_mfma_f32_16x16x32_bf16 v[20:23], a[236:239], a[188:191], v[20:23]\n\t"
"buffer_load_dwordx4 v[248:251], v252, s[8:11], s20 offen offset:0 sc1\n\t"
"v_mfma_f32_16x16x32_bf16 v[20:23], a[240:243], a[192:195], v[20:23]\n\t"
"s_waitcnt lgkmcnt(2)\n\t"
"ds_read_b64_tr_b16 v[230:231], v246 offset:3584\n\t"
"v_mfma_f32_16x16x32_bf16 v[20:23], a[244:247], a[196:199], v[20:23]\n\t"
"v_mfma_f32_16x16x32_bf16 v[24:27], a[236:239], a[192:195], v[24:27]\n\t"
"buffer_load_dwordx4 a[212:215], v252, s[8:11], s20 offen offset:1024 sc1\n\t"
"v_mfma_f32_16x16x32_bf16 v[24:27], a[240:243], a[196:199], v[24:27]\n\t"
"s_waitcnt lgkmcnt(2)\n\t"
"v_mfma_f32_16x16x32_bf16 v[24:27], a[244:247], a[200:203], v[24:27]\n\t"
"v_mfma_f32_16x16x32_bf16 v[28:31], a[236:239], a[196:199], v[28:31]\n\t"
"s_waitcnt vmcnt(11)\n\t"
"ds_read_b64_tr_b16 v[232:233], v246 offset:6656\n\t"
"v_mfma_f32_16x16x32_bf16 v[28:31], a[240:243], a[200:203], v[28:31]\n\t"
"buffer_load_dwordx4 a[216:219], v252, s[8:11], s20 offen offset:2048 sc1\n\t"
"v_mfma_f32_16x16x32_bf16 v[28:31], a[244:247], a[204:207], v[28:31]\n\t"
"s_mov_b32 s20, 380928\n\t"
"ds_read_b64_tr_b16 v[234:235], v246 offset:9728\n\t"
"v_mfma_f32_16x16x32_bf16 v[32:35], v[96:99], a[168:171], v[32:35]\n\t"
"v_mfma_f32_16x16x32_bf16 v[32:35], a[248:251], a[172:175], v[32:35]\n\t"
"v_mfma_f32_16x16x32_bf16 v[32:35], v[236:239], a[176:179], v[32:35]\n\t"
"buffer_load_dwordx4 a[220:223], v252, s[8:11], s20 offen offset:0 sc1\n\t"
"v_mfma_f32_16x16x32_bf16 v[36:39], v[96:99], a[172:175], v[36:39]\n\t"
"s_waitcnt lgkmcnt(2)\n\t"
"v_mfma_f32_16x16x32_bf16 v[36:39], a[248:251], a[176:179], v[36:39]\n\t"
"v_mfma_f32_16x16x32_bf16 v[36:39], v[236:239], a[180:183], v[36:39]\n\t"
"v_mfma_f32_16x16x32_bf16 v[228:231], v[228:231], v[220:223], 0\n\t"
"buffer_load_dwordx4 a[224:227], v252, s[8:11], s20 offen offset:1024 sc1\n\t"
"v_mfma_f32_16x16x32_bf16 v[40:43], v[96:99], a[176:179], v[40:43]\n\t"
"ds_read_b64_tr_b16 v[224:225], v246 offset:12800\n\t"
"v_mfma_f32_16x16x32_bf16 v[40:43], a[248:251], a[180:183], v[40:43]\n\t"
"ds_read_b64_tr_b16 v[226:227], v246 offset:15872\n\t"
"v_mfma_f32_16x16x32_bf16 v[40:43], v[236:239], a[184:187], v[40:43]\n\t"
"v_mfma_f32_16x16x32_bf16 v[44:47], v[96:99], a[180:183], v[44:47]\n\t"
"buffer_load_dwordx4 a[228:231], v252, s[8:11], s20 offen offset:2048 sc1\n\t"
"v_mfma_f32_16x16x32_bf16 v[44:47], a[248:251], a[184:187], v[44:47]\n\t"
"s_waitcnt lgkmcnt(2)\n\t"
"v_cvt_pk_bf16_f32 v228, v228, v229\n\t"
"v_cvt_pk_bf16_f32 v229, v230, v231\n\t"
"v_mfma_f32_16x16x32_bf16 v[44:47], v[236:239], a[188:191], v[44:47]\n\t"
"s_mov_b32 s20, 393216\n\t"
"v_mfma_f32_16x16x32_bf16 v[232:235], v[232:235], v[220:223], 0\n\t"
"v_mfma_f32_16x16x32_bf16 v[48:51], v[96:99], a[184:187], v[48:51]\n\t"
"buffer_load_dwordx4 a[232:235], v252, s[8:11], s20 offen offset:0 sc1\n\t"
"v_mfma_f32_16x16x32_bf16 v[48:51], a[248:251], a[188:191], v[48:51]\n\t"
"v_mfma_f32_16x16x32_bf16 v[48:51], v[236:239], a[192:195], v[48:51]\n\t"
"v_mfma_f32_16x16x32_bf16 v[52:55], v[96:99], a[188:191], v[52:55]\n\t"
"v_mfma_f32_16x16x32_bf16 v[52:55], a[248:251], a[192:195], v[52:55]\n\t"
"buffer_load_dwordx4 a[236:239], v252, s[8:11], s20 offen offset:1024 sc1\n\t"
"v_mfma_f32_16x16x32_bf16 v[52:55], v[236:239], a[196:199], v[52:55]\n\t"
"v_cvt_pk_bf16_f32 v230, v232, v233\n\t"
"v_cvt_pk_bf16_f32 v231, v234, v235\n\t"
"v_mfma_f32_16x16x32_bf16 v[56:59], v[96:99], a[192:195], v[56:59]\n\t"
"s_waitcnt lgkmcnt(0)\n\t"
"ds_read_b64_tr_b16 v[232:233], v246 offset:20480\n\t"
"v_mfma_f32_16x16x32_bf16 v[56:59], a[248:251], a[196:199], v[56:59]\n\t"
"ds_read_b64_tr_b16 v[234:235], v246 offset:23552\n\t"
"v_mfma_f32_16x16x32_bf16 v[56:59], v[236:239], a[200:203], v[56:59]\n\t"
"buffer_load_dwordx4 a[240:243], v252, s[8:11], s20 offen offset:2048 sc1\n\t"
"v_mfma_f32_16x16x32_bf16 v[224:227], v[224:227], v[220:223], 0\n\t"
"s_mov_b32 s20, 405504\n\t"
"v_mfma_f32_16x16x32_bf16 v[60:63], v[96:99], a[196:199], v[60:63]\n\t"
"s_waitcnt vmcnt(9)\n\t"
"v_mfma_f32_16x16x32_bf16 v[60:63], a[248:251], a[200:203], v[60:63]\n\t"
"v_mfma_f32_16x16x32_bf16 v[60:63], v[236:239], a[204:207], v[60:63]\n\t"
"buffer_load_dwordx4 v[236:239], v252, s[8:11], s20 offen offset:0 sc1\n\t"
"v_mfma_f32_16x16x32_bf16 v[64:67], a[252:255], a[168:171], v[64:67]\n\t"
"ds_read_b128 a[168:171], v244 offset:9216\n\t"
"v_mfma_f32_16x16x32_bf16 v[64:67], v[240:243], a[172:175], v[64:67]\n\t"
"v_cvt_pk_bf16_f32 v224, v224, v225\n\t"
"v_cvt_pk_bf16_f32 v225, v226, v227\n\t"
"v_mfma_f32_16x16x32_bf16 v[64:67], a[208:211], a[176:179], v[64:67]\n\t"
"v_mfma_f32_16x16x32_bf16 v[68:71], a[252:255], a[172:175], v[68:71]\n\t"
"buffer_load_dwordx4 v[96:99], v252, s[8:11], s20 offen offset:1024 sc1\n\t"
"v_mfma_f32_16x16x32_bf16 v[68:71], v[240:243], a[176:179], v[68:71]\n\t"
"s_waitcnt lgkmcnt(1)\n\t"
"ds_read_b128 a[172:175], v244 offset:9728\n\t"
"v_mfma_f32_16x16x32_bf16 v[68:71], a[208:211], a[180:183], v[68:71]\n\t"
"buffer_load_dwordx4 a[244:247], v252, s[8:11], s20 offen offset:2048 sc1\n\t"
"v_mfma_f32_16x16x32_bf16 v[232:235], v[232:235], v[220:223], 0\n\t"
"s_mov_b32 s20, 417792\n\t"
"v_mfma_f32_16x16x32_bf16 v[72:75], a[252:255], a[176:179], v[72:75]\n\t"
"ds_read_b128 a[176:179], v244 offset:10240\n\t"
"v_mfma_f32_16x16x32_bf16 v[72:75], v[240:243], a[180:183], v[72:75]\n\t"
"buffer_load_dwordx4 a[248:251], v252, s[8:11], s20 offen offset:0 sc1\n\t"
"v_mfma_f32_16x16x32_bf16 v[72:75], a[208:211], a[184:187], v[72:75]\n\t"
"v_mfma_f32_16x16x32_bf16 v[76:79], a[252:255], a[180:183], v[76:79]\n\t"
"ds_read_b128 a[180:183], v244 offset:10752\n\t"
"v_mfma_f32_16x16x32_bf16 v[76:79], v[240:243], a[184:187], v[76:79]\n\t"
"v_cvt_pk_bf16_f32 v226, v232, v233\n\t"
"v_cvt_pk_bf16_f32 v227, v234, v235\n\t"
"v_mfma_f32_16x16x32_bf16 v[76:79], a[208:211], a[188:191], v[76:79]\n\t"
"ds_read_b64_tr_b16 v[232:233], v246 offset:26624\n\t"
"v_mfma_f32_16x16x32_bf16 v[80:83], a[252:255], a[184:187], v[80:83]\n\t"
"ds_read_b64_tr_b16 v[234:235], v246 offset:29696\n\t"
"v_mfma_f32_16x16x32_bf16 v[80:83], v[240:243], a[188:191], v[80:83]\n\t"
"ds_read_b128 a[184:187], v244 offset:11264\n\t"
"v_mfma_f32_16x16x32_bf16 v[80:83], a[208:211], a[192:195], v[80:83]\n\t"
"v_mfma_f32_16x16x32_bf16 v[84:87], a[252:255], a[188:191], v[84:87]\n\t"
"ds_read_b128 a[188:191], v244 offset:11776\n\t"
"v_mfma_f32_16x16x32_bf16 v[84:87], v[240:243], a[192:195], v[84:87]\n\t"
"v_mfma_f32_16x16x32_bf16 v[84:87], a[208:211], a[196:199], v[84:87]\n\t"
"v_mfma_f32_16x16x32_bf16 v[88:91], a[252:255], a[192:195], v[88:91]\n\t"
"ds_read_b128 a[192:195], v244 offset:12288\n\t"
"v_mfma_f32_16x16x32_bf16 v[88:91], v[240:243], a[196:199], v[88:91]\n\t"
"v_mfma_f32_16x16x32_bf16 v[88:91], a[208:211], a[200:203], v[88:91]\n\t"
"v_mfma_f32_16x16x32_bf16 v[92:95], a[252:255], a[196:199], v[92:95]\n\t"
"buffer_load_dwordx4 a[252:255], v252, s[8:11], s20 offen offset:1024 sc1\n\t"
"v_mfma_f32_16x16x32_bf16 v[92:95], v[240:243], a[200:203], v[92:95]\n\t"
"s_waitcnt vmcnt(11) lgkmcnt(3)\n\t"
"ds_read_b128 a[196:199], v244 offset:12800\n\t"
"v_mfma_f32_16x16x32_bf16 v[92:95], a[208:211], a[204:207], v[92:95]\n\t"
"ds_read_b128 a[200:203], v244 offset:13312\n\t"
"v_mfma_f32_16x16x32_bf16 v[232:235], v[232:235], v[220:223], 0\n\t"
"ds_read_b128 a[204:207], v244 offset:13824\n\t"
"v_mfma_f32_16x16x32_bf16 v[100:103], v[248:251], a[168:171], v[100:103]\n\t"
"buffer_load_dwordx4 v[240:243], v252, s[8:11], s20 offen offset:2048 sc1\n\t"
"v_mfma_f32_16x16x32_bf16 v[100:103], a[212:215], a[172:175], v[100:103]\n\t"
"s_mov_b32 s20, 430080\n\t"
"v_mfma_f32_16x16x32_bf16 v[100:103], a[216:219], a[176:179], v[100:103]\n\t"
"buffer_load_dwordx4 a[208:211], v252, s[8:11], s20 offen offset:0 sc1\n\t"
"v_mfma_f32_16x16x32_bf16 v[104:107], v[248:251], a[172:175], v[104:107]\n\t"
"s_waitcnt lgkmcnt(5)\n\t"
"v_mfma_f32_16x16x32_bf16 v[104:107], a[212:215], a[176:179], v[104:107]\n\t"
"v_cvt_pk_bf16_f32 v232, v232, v233\n\t"
"v_mfma_f32_16x16x32_bf16 v[104:107], a[216:219], a[180:183], v[104:107]\n\t"
"s_waitcnt lgkmcnt(0)\n\t"
"s_barrier\n\t"
"v_mfma_f32_16x16x32_bf16 v[108:111], v[248:251], a[176:179], v[108:111]\n\t"
"v_cvt_pk_bf16_f32 v233, v234, v235\n\t"
"v_mfma_f32_16x16x32_bf16 v[108:111], a[212:215], a[180:183], v[108:111]\n\t"
"v_mfma_f32_16x16x32_bf16 v[108:111], a[216:219], a[184:187], v[108:111]\n\t"
"ds_write2st64_b64 v245, v[228:229], v[230:231] offset0:0 offset1:1\n\t"
"v_mfma_f32_16x16x32_bf16 v[112:115], v[248:251], a[180:183], v[112:115]\n\t"
"v_mfma_f32_16x16x32_bf16 v[112:115], a[212:215], a[184:187], v[112:115]\n\t"
"ds_read_b64_tr_b16 v[228:229], v246 offset:32768\n\t"
"v_mfma_f32_16x16x32_bf16 v[112:115], a[216:219], a[188:191], v[112:115]\n\t"
"ds_write2st64_b64 v245, v[224:225], v[226:227] offset0:2 offset1:3\n\t"
"v_mfma_f32_16x16x32_bf16 v[116:119], v[248:251], a[184:187], v[116:119]\n\t"
"v_mfma_f32_16x16x32_bf16 v[116:119], a[212:215], a[188:191], v[116:119]\n\t"
"ds_read_b64_tr_b16 v[230:231], v246 offset:35840\n\t"
"v_mfma_f32_16x16x32_bf16 v[116:119], a[216:219], a[192:195], v[116:119]\n\t"
"ds_write_b64 v245, v[232:233] offset:2048\n\t"
"v_mfma_f32_16x16x32_bf16 v[120:123], v[248:251], a[188:191], v[120:123]\n\t"
"ds_read_b64_tr_b16 v[224:225], v246 offset:40448\n\t"
"v_mfma_f32_16x16x32_bf16 v[120:123], a[212:215], a[192:195], v[120:123]\n\t"
"ds_read_b64_tr_b16 v[226:227], v246 offset:43520\n\t"
"v_mfma_f32_16x16x32_bf16 v[120:123], a[216:219], a[196:199], v[120:123]\n\t"
"s_waitcnt lgkmcnt(3)\n\t"
"ds_read_b64_tr_b16 v[232:233], v246 offset:46592\n\t"
"v_mfma_f32_16x16x32_bf16 v[228:231], v[228:231], v[220:223], 0\n\t"
"ds_read_b64_tr_b16 v[234:235], v246 offset:49664\n\t"
"v_mfma_f32_16x16x32_bf16 v[124:127], v[248:251], a[192:195], v[124:127]\n\t"
"v_mfma_f32_16x16x32_bf16 v[124:127], a[212:215], a[196:199], v[124:127]\n\t"
"v_mfma_f32_16x16x32_bf16 v[124:127], a[216:219], a[200:203], v[124:127]\n\t"
"s_waitcnt lgkmcnt(2)\n\t"
"v_mfma_f32_16x16x32_bf16 v[224:227], v[224:227], v[220:223], 0\n\t"
"v_mfma_f32_16x16x32_bf16 v[128:131], v[248:251], a[196:199], v[128:131]\n\t"
"buffer_load_dwordx4 v[248:251], v252, s[8:11], s20 offen offset:1024 sc1\n\t"
"v_mfma_f32_16x16x32_bf16 v[128:131], a[212:215], a[200:203], v[128:131]\n\t"
"s_waitcnt vmcnt(11) lgkmcnt(0)\n\t"
"v_cvt_pk_bf16_f32 v228, v228, v229\n\t"
"v_cvt_pk_bf16_f32 v229, v230, v231\n\t"
"v_mfma_f32_16x16x32_bf16 v[128:131], a[216:219], a[204:207], v[128:131]\n\t"
"v_mfma_f32_16x16x32_bf16 v[232:235], v[232:235], v[220:223], 0\n\t"
"buffer_load_dwordx4 a[212:215], v252, s[8:11], s20 offen offset:2048 sc1\n\t"
"v_mfma_f32_16x16x32_bf16 v[132:135], a[220:223], a[168:171], v[132:135]\n\t"
"s_mov_b32 s20, 442368\n\t"
"v_cvt_pk_bf16_f32 v230, v224, v225\n\t"
"v_cvt_pk_bf16_f32 v231, v226, v227\n\t"
"v_mfma_f32_16x16x32_bf16 v[132:135], a[224:227], a[172:175], v[132:135]\n\t"
"s_add_u32 m0, s24, 46976\n\t"
"s_add_u32 s28, s21, 1376256\n\t"
"ds_read_b64_tr_b16 v[224:225], v246 offset:52736\n\t"
"v_mfma_f32_16x16x32_bf16 v[132:135], a[228:231], a[176:179], v[132:135]\n\t"
"buffer_load_dwordx4 a[216:219], v252, s[8:11], s20 offen offset:0 sc1\n\t"
"v_mfma_f32_16x16x32_bf16 v[136:139], a[220:223], a[172:175], v[136:139]\n\t"
"buffer_load_dwordx4 v247, s[12:15], s28 offen offset:128 nt lds\n\t"
"v_mfma_f32_16x16x32_bf16 v[136:139], a[224:227], a[176:179], v[136:139]\n\t"
"s_add_u32 m0, s24, 53120\n\t"
"s_add_u32 s28, s21, 1572864\n\t"
"ds_read_b64_tr_b16 v[226:227], v246 offset:55808\n\t"
"v_mfma_f32_16x16x32_bf16 v[136:139], a[228:231], a[180:183], v[136:139]\n\t"
"buffer_load_dwordx4 v247, s[12:15], s28 offen offset:128 nt lds\n\t"
"v_mfma_f32_16x16x32_bf16 v[140:143], a[220:223], a[176:179], v[140:143]\n\t"
"s_add_u32 m0, s24, 93056\n\t"
"s_add_u32 s28, s21, 2752512\n\t"
"v_cvt_pk_bf16_f32 v232, v232, v233\n\t"
"v_mfma_f32_16x16x32_bf16 v[140:143], a[224:227], a[180:183], v[140:143]\n\t"
"v_cvt_pk_bf16_f32 v233, v234, v235\n\t"
"v_mfma_f32_16x16x32_bf16 v[140:143], a[228:231], a[184:187], v[140:143]\n\t"
"ds_write2st64_b64 v245, v[228:229], v[230:231] offset0:5 offset1:6\n\t"
"v_mfma_f32_16x16x32_bf16 v[144:147], a[220:223], a[180:183], v[144:147]\n\t"
"buffer_load_dwordx4 v247, s[12:15], s28 offen offset:128 nt lds\n\t"
"v_mfma_f32_16x16x32_bf16 v[144:147], a[224:227], a[184:187], v[144:147]\n\t"
"s_add_u32 m0, s24, 100736\n\t"
"s_add_u32 s28, s21, 2949120\n\t"
"v_mfma_f32_16x16x32_bf16 v[144:147], a[228:231], a[188:191], v[144:147]\n\t"
"buffer_load_dwordx4 v247, s[12:15], s28 offen offset:128 nt lds\n\t"
"s_add_u32 m0, s24, 106880\n\t"
"s_add_u32 s28, s21, 3145728\n\t"
"v_mfma_f32_16x16x32_bf16 v[148:151], a[220:223], a[184:187], v[148:151]\n\t"
"buffer_load_dwordx4 v247, s[12:15], s28 offen offset:128 nt lds\n\t"
"v_mfma_f32_16x16x32_bf16 v[148:151], a[224:227], a[188:191], v[148:151]\n\t"
"s_waitcnt lgkmcnt(1)\n\t"
"s_add_u32 m0, s24, 113024\n\t"
"s_add_u32 s28, s21, 3342336\n\t"
"v_mfma_f32_16x16x32_bf16 v[148:151], a[228:231], a[192:195], v[148:151]\n\t"
"buffer_load_dwordx4 v247, s[12:15], s28 offen offset:128 nt lds\n\t"
"s_add_u32 m0, s25, 896\n\t"
"v_mfma_f32_16x16x32_bf16 v[224:227], v[224:227], v[220:223], 0\n\t"
"buffer_load_dwordx4 v254, s[12:15], 0 offen offset:128 nt lds\n\t"
"v_mfma_f32_16x16x32_bf16 v[152:155], a[220:223], a[188:191], v[152:155]\n\t"
"s_add_u32 m0, s26, 896\n\t"
"v_mfma_f32_16x16x32_bf16 v[152:155], a[224:227], a[192:195], v[152:155]\n\t"
"v_mfma_f32_16x16x32_bf16 v[152:155], a[228:231], a[196:199], v[152:155]\n\t"
"s_mov_b64 exec, s[36:37]\n\t"
"buffer_load_dwordx4 v254, s[12:15], s27 offen offset:128 nt lds\n\t"
"s_mov_b64 exec, -1\n\t"
"v_mfma_f32_16x16x32_bf16 v[156:159], a[220:223], a[192:195], v[156:159]\n\t"
"v_mfma_f32_16x16x32_bf16 v[156:159], a[224:227], a[196:199], v[156:159]\n\t"
"v_cvt_pk_bf16_f32 v234, v224, v225\n\t"
"v_cvt_pk_bf16_f32 v235, v226, v227\n\t"
"v_mfma_f32_16x16x32_bf16 v[156:159], a[228:231], a[200:203], v[156:159]\n\t"
"ds_write2st64_b64 v245, v[232:233], v[234:235] offset0:7 offset1:8\n\t"
"v_mfma_f32_16x16x32_bf16 v[160:163], a[220:223], a[196:199], v[160:163]\n\t"
"s_waitcnt vmcnt(18)\n\t"
"v_mfma_f32_16x16x32_bf16 v[160:163], a[224:227], a[200:203], v[160:163]\n\t"
"v_mfma_f32_16x16x32_bf16 v[160:163], a[228:231], a[204:207], v[160:163]\n\t"
"v_mfma_f32_16x16x32_bf16 v[164:167], a[232:235], a[168:171], v[164:167]\n\t"
"v_mfma_f32_16x16x32_bf16 v[164:167], a[236:239], a[172:175], v[164:167]\n\t"
"v_mfma_f32_16x16x32_bf16 v[164:167], a[240:243], a[176:179], v[164:167]\n\t"
"s_waitcnt lgkmcnt(0)\n\t"
"s_barrier\n\t"
"v_mfma_f32_16x16x32_bf16 v[168:171], a[232:235], a[172:175], v[168:171]\n\t"
"ds_read_b128 a[168:171], v244 offset:0\n\t"
"v_mfma_f32_16x16x32_bf16 v[168:171], a[236:239], a[176:179], v[168:171]\n\t"
"ds_read_b128 a[172:175], v244 offset:512\n\t"
"v_mfma_f32_16x16x32_bf16 v[168:171], a[240:243], a[180:183], v[168:171]\n\t"
"v_mfma_f32_16x16x32_bf16 v[172:175], a[232:235], a[176:179], v[172:175]\n\t"
"ds_read_b128 a[176:179], v244 offset:1024\n\t"
"v_mfma_f32_16x16x32_bf16 v[172:175], a[236:239], a[180:183], v[172:175]\n\t"
"v_mfma_f32_16x16x32_bf16 v[172:175], a[240:243], a[184:187], v[172:175]\n\t"
"v_mfma_f32_16x16x32_bf16 v[176:179], a[232:235], a[180:183], v[176:179]\n\t"
"ds_read_b128 a[180:183], v244 offset:1536\n\t"
"v_mfma_f32_16x16x32_bf16 v[176:179], a[236:239], a[184:187], v[176:179]\n\t"
"v_mfma_f32_16x16x32_bf16 v[176:179], a[240:243], a[188:191], v[176:179]\n\t"
"v_mfma_f32_16x16x32_bf16 v[180:183], a[232:235], a[184:187], v[180:183]\n\t"
"ds_read_b128 a[184:187], v244 offset:2048\n\t"
"v_mfma_f32_16x16x32_bf16 v[180:183], a[236:239], a[188:191], v[180:183]\n\t"
"v_mfma_f32_16x16x32_bf16 v[180:183], a[240:243], a[192:195], v[180:183]\n\t"
"v_mfma_f32_16x16x32_bf16 v[184:187], a[232:235], a[188:191], v[184:187]\n\t"
"ds_read_b128 a[188:191], v244 offset:2560\n\t"
"v_mfma_f32_16x16x32_bf16 v[184:187], a[236:239], a[192:195], v[184:187]\n\t"
"v_mfma_f32_16x16x32_bf16 v[184:187], a[240:243], a[196:199], v[184:187]\n\t"
"v_mfma_f32_16x16x32_bf16 v[188:191], a[232:235], a[192:195], v[188:191]\n\t"
"ds_read_b128 a[192:195], v244 offset:3072\n\t"
"v_mfma_f32_16x16x32_bf16 v[188:191], a[236:239], a[196:199], v[188:191]\n\t"
"v_mfma_f32_16x16x32_bf16 v[188:191], a[240:243], a[200:203], v[188:191]\n\t"
"v_mfma_f32_16x16x32_bf16 v[192:195], a[232:235], a[196:199], v[192:195]\n\t"
"s_waitcnt vmcnt(15) lgkmcnt(4)\n\t"
"ds_read_b128 a[196:199], v244 offset:3584\n\t"
"v_mfma_f32_16x16x32_bf16 v[192:195], a[236:239], a[200:203], v[192:195]\n\t"
"ds_read_b128 a[200:203], v244 offset:4096\n\t"
"v_mfma_f32_16x16x32_bf16 v[192:195], a[240:243], a[204:207], v[192:195]\n\t"
"ds_read_b128 a[204:207], v244 offset:4608\n\t"
"Lst5_%=:\n\t"
"v_mfma_f32_16x16x32_bf16 v[196:199], v[236:239], a[168:171], v[196:199]\n\t"
"s_waitcnt lgkmcnt(6)\n\t"
"v_mfma_f32_16x16x32_bf16 v[196:199], v[96:99], a[172:175], v[196:199]\n\t"
"v_mfma_f32_16x16x32_bf16 v[196:199], a[244:247], a[176:179], v[196:199]\n\t"
"v_mfma_f32_16x16x32_bf16 v[204:207], v[236:239], a[172:175], v[204:207]\n\t"
"s_waitcnt lgkmcnt(5)\n\t"
"v_mfma_f32_16x16x32_bf16 v[204:207], v[96:99], a[176:179], v[204:207]\n\t"
"v_mfma_f32_16x16x32_bf16 v[204:207], a[244:247], a[180:183], v[204:207]\n\t"
"v_mfma_f32_16x16x32_bf16 v[212:215], v[236:239], a[176:179], v[212:215]\n\t"
"s_waitcnt lgkmcnt(4)\n\t"
"v_mfma_f32_16x16x32_bf16 v[212:215], v[96:99], a[180:183], v[212:215]\n\t"
"v_mfma_f32_16x16x32_bf16 v[212:215], a[244:247], a[184:187], v[212:215]\n\t"
"v_mfma_f32_16x16x32_bf16 a[0:3], v[236:239], a[180:183], a[0:3]\n\t"
"s_waitcnt lgkmcnt(3)\n\t"
"v_mfma_f32_16x16x32_bf16 a[0:3], v[96:99], a[184:187], a[0:3]\n\t"
"buffer_load_dwordx4 a[220:223], v252, s[8:11], s20 offen offset:1024 sc1\n\t"
"v_mfma_f32_16x16x32_bf16 a[0:3], a[244:247], a[188:191], a[0:3]\n\t"
"v_mfma_f32_16x16x32_bf16 a[8:11], v[236:239], a[184:187], a[8:11]\n\t"
"s_waitcnt lgkmcnt(2)\n\t"
"v_mfma_f32_16x16x32_bf16 a[8:11], v[96:99], a[188:191], a[8:11]\n\t"
"v_mfma_f32_16x16x32_bf16 a[8:11], a[244:247], a[192:195], a[8:11]\n\t"
"ds_read_b64_tr_b16 v[228:229], v246 offset:1024\n\t"
"v_mfma_f32_16x16x32_bf16 a[16:19], v[236:239], a[188:191], a[16:19]\n\t"
"s_waitcnt lgkmcnt(2)\n\t"
"ds_read_b64_tr_b16 v[230:231], v246 offset:4096\n\t"
"v_mfma_f32_16x16x32_bf16 a[16:19], v[96:99], a[192:195], a[16:19]\n\t"
"v_mfma_f32_16x16x32_bf16 a[16:19], a[244:247], a[196:199], a[16:19]\n\t"
"v_mfma_f32_16x16x32_bf16 a[24:27], v[236:239], a[192:195], a[24:27]\n\t"
"s_waitcnt lgkmcnt(2)\n\t"
"v_mfma_f32_16x16x32_bf16 a[24:27], v[96:99], a[196:199], a[24:27]\n\t"
"v_mfma_f32_16x16x32_bf16 a[24:27], a[244:247], a[200:203], a[24:27]\n\t"
"v_mfma_f32_16x16x32_bf16 a[32:35], v[236:239], a[196:199], a[32:35]\n\t"
"s_waitcnt vmcnt(13)\n\t"
"ds_read_b64_tr_b16 v[224:225], v246 offset:7168\n\t"
"v_mfma_f32_16x16x32_bf16 a[32:35], v[96:99], a[200:203], a[32:35]\n\t"
"ds_read_b64_tr_b16 v[226:227], v246 offset:10240\n\t"
"v_mfma_f32_16x16x32_bf16 a[32:35], a[244:247], a[204:207], a[32:35]\n\t"
"v_mfma_f32_16x16x32_bf16 a[40:43], a[248:251], a[168:171], a[40:43]\n\t"
"v_mfma_f32_16x16x32_bf16 a[40:43], a[252:255], a[172:175], a[40:43]\n\t"
"v_mfma_f32_16x16x32_bf16 a[40:43], v[240:243], a[176:179], a[40:43]\n\t"
"v_mfma_f32_16x16x32_bf16 a[48:51], a[248:251], a[172:175], a[48:51]\n\t"
"s_waitcnt lgkmcnt(2)\n\t"
"v_mfma_f32_16x16x32_bf16 a[48:51], a[252:255], a[176:179], a[48:51]\n\t"
"v_mfma_f32_16x16x32_bf16 a[48:51], v[240:243], a[180:183], a[48:51]\n\t"
"v_mfma_f32_16x16x32_bf16 v[228:231], v[228:231], v[220:223], 0\n\t"
"v_mfma_f32_16x16x32_bf16 a[56:59], a[248:251], a[176:179], a[56:59]\n\t"
"ds_read_b64_tr_b16 v[232:233], v246 offset:13312\n\t"
"v_mfma_f32_16x16x32_bf16 a[56:59], a[252:255], a[180:183], a[56:59]\n\t"
"ds_read_b64_tr_b16 v[234:235], v246 offset:16384\n\t"
"v_mfma_f32_16x16x32_bf16 a[56:59], v[240:243], a[184:187], a[56:59]\n\t"
"v_mfma_f32_16x16x32_bf16 a[64:67], a[248:251], a[180:183], a[64:67]\n\t"
"s_waitcnt lgkmcnt(2)\n\t"
"v_mfma_f32_16x16x32_bf16 a[64:67], a[252:255], a[184:187], a[64:67]\n\t"
"v_cvt_pk_bf16_f32 v228, v228, v229\n\t"
"v_cvt_pk_bf16_f32 v229, v230, v231\n\t"
"v_mfma_f32_16x16x32_bf16 a[64:67], v[240:243], a[188:191], a[64:67]\n\t"
"v_mfma_f32_16x16x32_bf16 v[224:227], v[224:227], v[220:223], 0\n\t"
"buffer_load_dwordx4 v[236:239], v252, s[8:11], s20 offen offset:2048 sc1\n\t"
"v_mfma_f32_16x16x32_bf16 a[72:75], a[248:251], a[184:187], a[72:75]\n\t"
"s_mov_b32 s20, 454656\n\t"
"v_mfma_f32_16x16x32_bf16 a[72:75], a[252:255], a[188:191], a[72:75]\n\t"
"v_mfma_f32_16x16x32_bf16 a[72:75], v[240:243], a[192:195], a[72:75]\n\t"
"v_mfma_f32_16x16x32_bf16 a[80:83], a[248:251], a[188:191], a[80:83]\n\t"
"buffer_load_dwordx4 v[96:99], v252, s[8:11], s20 offen offset:0 sc1\n\t"
"v_mfma_f32_16x16x32_bf16 a[80:83], a[252:255], a[192:195], a[80:83]\n\t"
"v_cvt_pk_bf16_f32 v230, v224, v225\n\t"
"v_cvt_pk_bf16_f32 v231, v226, v227\n\t"
"v_mfma_f32_16x16x32_bf16 a[80:83], v[240:243], a[196:199], a[80:83]\n\t"
"ds_read_b64_tr_b16 v[224:225], v246 offset:20992\n\t"
"v_mfma_f32_16x16x32_bf16 a[88:91], a[248:251], a[192:195], a[88:91]\n\t"
"s_waitcnt lgkmcnt(1)\n\t"
"ds_read_b64_tr_b16 v[226:227], v246 offset:24064\n\t"
"v_mfma_f32_16x16x32_bf16 a[88:91], a[252:255], a[196:199], a[88:91]\n\t"
"buffer_load_dwordx4 a[224:227], v252, s[8:11], s20 offen offset:1024 sc1\n\t"
"v_mfma_f32_16x16x32_bf16 a[88:91], v[240:243], a[200:203], a[88:91]\n\t"
"v_mfma_f32_16x16x32_bf16 v[232:235], v[232:235], v[220:223], 0\n\t"
"v_mfma_f32_16x16x32_bf16 a[96:99], a[248:251], a[196:199], a[96:99]\n\t"
"s_waitcnt vmcnt(13)\n\t"
"v_mfma_f32_16x16x32_bf16 a[96:99], a[252:255], a[200:203], a[96:99]\n\t"
"buffer_load_dwordx4 a[228:231], v252, s[8:11], s20 offen offset:2048 sc1\n\t"
"v_mfma_f32_16x16x32_bf16 a[96:99], v[240:243], a[204:207], a[96:99]\n\t"
"s_mov_b32 s20, 466944\n\t"
"v_mfma_f32_16x16x32_bf16 a[104:107], a[208:211], a[168:171], a[104:107]\n\t"
"ds_read_b128 a[168:171], v244 offset:9216\n\t"
"v_mfma_f32_16x16x32_bf16 a[104:107], v[248:251], a[172:175], a[104:107]\n\t"
"v_cvt_pk_bf16_f32 v232, v232, v233\n\t"
"v_cvt_pk_bf16_f32 v233, v234, v235\n\t"
"v_mfma_f32_16x16x32_bf16 a[104:107], a[212:215], a[176:179], a[104:107]\n\t"
"v_mfma_f32_16x16x32_bf16 a[112:115], a[208:211], a[172:175], a[112:115]\n\t"
"buffer_load_dwordx4 a[232:235], v252, s[8:11], s20 offen offset:0 sc1\n\t"
"v_mfma_f32_16x16x32_bf16 a[112:115], v[248:251], a[176:179], a[112:115]\n\t"
"s_waitcnt lgkmcnt(1)\n\t"
"ds_read_b128 a[172:175], v244 offset:9728\n\t"
"v_mfma_f32_16x16x32_bf16 a[112:115], a[212:215], a[180:183], a[112:115]\n\t"
"v_mfma_f32_16x16x32_bf16 v[224:227], v[224:227], v[220:223], 0\n\t"
"buffer_load_dwordx4 a[236:239], v252, s[8:11], s20 offen offset:1024 sc1\n\t"
"v_mfma_f32_16x16x32_bf16 a[120:123], a[208:211], a[176:179], a[120:123]\n\t"
"ds_read_b128 a[176:179], v244 offset:10240\n\t"
"v_mfma_f32_16x16x32_bf16 a[120:123], v[248:251], a[180:183], a[120:123]\n\t"
"v_mfma_f32_16x16x32_bf16 a[120:123], a[212:215], a[184:187], a[120:123]\n\t"
"v_mfma_f32_16x16x32_bf16 a[128:131], a[208:211], a[180:183], a[128:131]\n\t"
"buffer_load_dwordx4 a[240:243], v252, s[8:11], s20 offen offset:2048 sc1\n\t"
"v_mfma_f32_16x16x32_bf16 a[128:131], v[248:251], a[184:187], a[128:131]\n\t"
"s_mov_b32 s20, 700416\n\t"
"ds_read_b128 a[180:183], v244 offset:10752\n\t"
"v_cvt_pk_bf16_f32 v234, v224, v225\n\t"
"v_mfma_f32_16x16x32_bf16 a[128:131], a[212:215], a[188:191], a[128:131]\n\t"
"v_cvt_pk_bf16_f32 v235, v226, v227\n\t"
"v_mfma_f32_16x16x32_bf16 a[136:139], a[208:211], a[184:187], a[136:139]\n\t"
"ds_read_b64_tr_b16 v[224:225], v246 offset:27136\n\t"
"v_mfma_f32_16x16x32_bf16 a[136:139], v[248:251], a[188:191], a[136:139]\n\t"
"ds_read_b64_tr_b16 v[226:227], v246 offset:30208\n\t"
"v_mfma_f32_16x16x32_bf16 a[136:139], a[212:215], a[192:195], a[136:139]\n\t"
"buffer_load_dwordx4 a[244:247], v252, s[8:11], s20 offen offset:0 sc1\n\t"
"v_mfma_f32_16x16x32_bf16 a[144:147], a[208:211], a[188:191], a[144:147]\n\t"
"ds_read_b128 a[184:187], v244 offset:11264\n\t"
"v_mfma_f32_16x16x32_bf16 a[144:147], v[248:251], a[192:195], a[144:147]\n\t"
"ds_read_b128 a[188:191], v244 offset:11776\n\t"
"v_mfma_f32_16x16x32_bf16 a[144:147], a[212:215], a[196:199], a[144:147]\n\t"
"buffer_load_dwordx4 a[248:251], v252, s[8:11], s20 offen offset:1024 sc1\n\t"
"v_mfma_f32_16x16x32_bf16 a[152:155], a[208:211], a[192:195], a[152:155]\n\t"
"ds_read_b128 a[192:195], v244 offset:12288\n\t"
"v_mfma_f32_16x16x32_bf16 a[152:155], v[248:251], a[196:199], a[152:155]\n\t"
"v_mfma_f32_16x16x32_bf16 a[152:155], a[212:215], a[200:203], a[152:155]\n\t"
"v_mfma_f32_16x16x32_bf16 a[160:163], a[208:211], a[196:199], a[160:163]\n\t"
"buffer_load_dwordx4 a[252:255], v252, s[8:11], s20 offen offset:2048 sc1\n\t"
"v_mfma_f32_16x16x32_bf16 a[160:163], v[248:251], a[200:203], a[160:163]\n\t"
"s_waitcnt vmcnt(9) lgkmcnt(3)\n\t"
"ds_read_b128 a[196:199], v244 offset:12800\n\t"
"s_mov_b32 s20, 712704\n\t"
"v_mfma_f32_16x16x32_bf16 a[160:163], a[212:215], a[204:207], a[160:163]\n\t"
"ds_read_b128 a[200:203], v244 offset:13312\n\t"
"v_mfma_f32_16x16x32_bf16 v[224:227], v[224:227], v[220:223], 0\n\t"
"ds_read_b128 a[204:207], v244 offset:13824\n\t"
"v_mfma_f32_16x16x32_bf16 v[200:203], a[216:219], a[168:171], v[200:203]\n\t"
"buffer_load_dwordx4 v[248:251], v252, s[8:11], s20 offen offset:0 sc1\n\t"
"v_mfma_f32_16x16x32_bf16 v[200:203], a[220:223], a[172:175], v[200:203]\n\t"
"v_mfma_f32_16x16x32_bf16 v[200:203], v[236:239], a[176:179], v[200:203]\n\t"
"v_mfma_f32_16x16x32_bf16 v[208:211], a[216:219], a[172:175], v[208:211]\n\t"
"buffer_load_dwordx4 a[212:215], v252, s[8:11], s20 offen offset:1024 sc1\n\t"
"v_mfma_f32_16x16x32_bf16 v[208:211], a[220:223], a[176:179], v[208:211]\n\t"
"s_waitcnt lgkmcnt(5)\n\t"
"v_cvt_pk_bf16_f32 v224, v224, v225\n\t"
"v_mfma_f32_16x16x32_bf16 v[208:211], v[236:239], a[180:183], v[208:211]\n\t"
"s_waitcnt lgkmcnt(0)\n\t"
"s_barrier\n\t"
"v_mfma_f32_16x16x32_bf16 v[216:219], a[216:219], a[176:179], v[216:219]\n\t"
"v_cvt_pk_bf16_f32 v225, v226, v227\n\t"
"v_mfma_f32_16x16x32_bf16 v[216:219], a[220:223], a[180:183], v[216:219]\n\t"
"v_mfma_f32_16x16x32_bf16 v[216:219], v[236:239], a[184:187], v[216:219]\n\t"
"ds_write2st64_b64 v245, v[228:229], v[230:231] offset0:0 offset1:1\n\t"
"v_mfma_f32_16x16x32_bf16 a[4:7], a[216:219], a[180:183], a[4:7]\n\t"
"v_mfma_f32_16x16x32_bf16 a[4:7], a[220:223], a[184:187], a[4:7]\n\t"
"ds_read_b64_tr_b16 v[228:229], v246 offset:33280\n\t"
"v_mfma_f32_16x16x32_bf16 a[4:7], v[236:239], a[188:191], a[4:7]\n\t"
"ds_write2st64_b64 v245, v[232:233], v[234:235] offset0:2 offset1:3\n\t"
"v_mfma_f32_16x16x32_bf16 a[12:15], a[216:219], a[184:187], a[12:15]\n\t"
"v_mfma_f32_16x16x32_bf16 a[12:15], a[220:223], a[188:191], a[12:15]\n\t"
"ds_read_b64_tr_b16 v[230:231], v246 offset:36352\n\t"
"v_mfma_f32_16x16x32_bf16 a[12:15], v[236:239], a[192:195], a[12:15]\n\t"
"ds_write_b64 v245, v[224:225] offset:2048\n\t"
"v_mfma_f32_16x16x32_bf16 a[20:23], a[216:219], a[188:191], a[20:23]\n\t"
"ds_read_b64_tr_b16 v[232:233], v246 offset:40960\n\t"
"v_mfma_f32_16x16x32_bf16 a[20:23], a[220:223], a[192:195], a[20:23]\n\t"
"ds_read_b64_tr_b16 v[234:235], v246 offset:44032\n\t"
"v_mfma_f32_16x16x32_bf16 a[20:23], v[236:239], a[196:199], a[20:23]\n\t"
"s_waitcnt lgkmcnt(3)\n\t"
"ds_read_b64_tr_b16 v[224:225], v246 offset:47104\n\t"
"v_mfma_f32_16x16x32_bf16 v[228:231], v[228:231], v[220:223], 0\n\t"
"ds_read_b64_tr_b16 v[226:227], v246 offset:50176\n\t"
"v_mfma_f32_16x16x32_bf16 a[28:31], a[216:219], a[192:195], a[28:31]\n\t"
"global_load_dwordx4 v[240:243], v252, s[48:49] offset:2048\n\t"
"v_mfma_f32_16x16x32_bf16 a[28:31], a[220:223], a[196:199], a[28:31]\n\t"
"global_load_dwordx4 a[208:211], v252, s[46:47] offset:1024\n\t"
"v_mfma_f32_16x16x32_bf16 a[28:31], v[236:239], a[200:203], a[28:31]\n\t"
"s_waitcnt lgkmcnt(2)\n\t"
"v_mfma_f32_16x16x32_bf16 v[232:235], v[232:235], v[220:223], 0\n\t"
"v_mfma_f32_16x16x32_bf16 a[36:39], a[216:219], a[196:199], a[36:39]\n\t"
"buffer_load_dwordx4 a[216:219], v252, s[8:11], s20 offen offset:2048 sc1\n\t"
"v_mfma_f32_16x16x32_bf16 a[36:39], a[220:223], a[200:203], a[36:39]\n\t"
"s_waitcnt vmcnt(11) lgkmcnt(0)\n\t"
"s_mov_b32 s20, 724992\n\t"
"v_cvt_pk_bf16_f32 v228, v228, v229\n\t"
"v_mfma_f32_16x16x32_bf16 a[36:39], v[236:239], a[204:207], a[36:39]\n\t"
"v_cvt_pk_bf16_f32 v229, v230, v231\n\t"
"v_mfma_f32_16x16x32_bf16 v[224:227], v[224:227], v[220:223], 0\n\t"
"buffer_load_dwordx4 a[220:223], v252, s[8:11], s20 offen offset:0 sc1\n\t"
"v_mfma_f32_16x16x32_bf16 a[44:47], v[96:99], a[168:171], a[44:47]\n\t"
"Lpg5_%=:\n\t"
"s_cbranch_vccnz Lpg9_%=\n\t"
"v_cvt_pk_bf16_f32 v230, v232, v233\n\t"
"v_cvt_pk_bf16_f32 v231, v234, v235\n\t"
"v_mfma_f32_16x16x32_bf16 a[44:47], a[224:227], a[172:175], a[44:47]\n\t"
"ds_read_b64_tr_b16 v[232:233], v246 offset:53248\n\t"
"v_mfma_f32_16x16x32_bf16 a[44:47], a[228:231], a[176:179], a[44:47]\n\t"
"ds_write2st64_b64 v245, v[228:229], v[230:231] offset0:5 offset1:6\n\t"
"v_mfma_f32_16x16x32_bf16 a[52:55], v[96:99], a[172:175], a[52:55]\n\t"
"v_mfma_f32_16x16x32_bf16 a[52:55], a[224:227], a[176:179], a[52:55]\n\t"
"ds_read_b64_tr_b16 v[234:235], v246 offset:56320\n\t"
"v_mfma_f32_16x16x32_bf16 a[52:55], a[228:231], a[180:183], a[52:55]\n\t"
"v_cvt_pk_bf16_f32 v224, v224, v225\n\t"
"v_cvt_pk_bf16_f32 v225, v226, v227\n\t"
"v_mfma_f32_16x16x32_bf16 a[60:63], v[96:99], a[176:179], a[60:63]\n\t"
"v_mfma_f32_16x16x32_bf16 a[60:63], a[224:227], a[180:183], a[60:63]\n\t"
"v_mfma_f32_16x16x32_bf16 a[60:63], a[228:231], a[184:187], a[60:63]\n\t"
"v_mfma_f32_16x16x32_bf16 a[68:71], v[96:99], a[180:183], a[68:71]\n\t"
"v_mfma_f32_16x16x32_bf16 a[68:71], a[224:227], a[184:187], a[68:71]\n\t"
"v_mfma_f32_16x16x32_bf16 a[68:71], a[228:231], a[188:191], a[68:71]\n\t"
"v_mfma_f32_16x16x32_bf16 a[76:79], v[96:99], a[184:187], a[76:79]\n\t"
"s_waitcnt lgkmcnt(0)\n\t"
"v_mfma_f32_16x16x32_bf16 a[76:79], a[224:227], a[188:191], a[76:79]\n\t"
"v_mfma_f32_16x16x32_bf16 a[76:79], a[228:231], a[192:195], a[76:79]\n\t"
"v_mfma_f32_16x16x32_bf16 v[232:235], v[232:235], v[220:223], 0\n\t"
"v_mfma_f32_16x16x32_bf16 a[84:87], v[96:99], a[188:191], a[84:87]\n\t"
"v_mfma_f32_16x16x32_bf16 a[84:87], a[224:227], a[192:195], a[84:87]\n\t"
"v_mfma_f32_16x16x32_bf16 a[84:87], a[228:231], a[196:199], a[84:87]\n\t"
"v_mfma_f32_16x16x32_bf16 a[92:95], v[96:99], a[192:195], a[92:95]\n\t"
"v_mfma_f32_16x16x32_bf16 a[92:95], a[224:227], a[196:199], a[92:95]\n\t"
"v_cvt_pk_bf16_f32 v226, v232, v233\n\t"
"v_cvt_pk_bf16_f32 v227, v234, v235\n\t"
"v_mfma_f32_16x16x32_bf16 a[92:95], a[228:231], a[200:203], a[92:95]\n\t"
"ds_write2st64_b64 v245, v[224:225], v[226:227] offset0:7 offset1:8\n\t"
"v_mfma_f32_16x16x32_bf16 a[100:103], v[96:99], a[196:199], a[100:103]\n\t"
"buffer_load_dwordx4 v[96:99], v252, s[8:11], s20 offen offset:1024 sc1\n\t"
"v_mfma_f32_16x16x32_bf16 a[100:103], a[224:227], a[200:203], a[100:103]\n\t"
"s_waitcnt vmcnt(10)\n\t"
"v_mfma_f32_16x16x32_bf16 a[100:103], a[228:231], a[204:207], a[100:103]\n\t"
"v_mfma_f32_16x16x32_bf16 a[108:111], a[232:235], a[168:171], a[108:111]\n\t"
"buffer_load_dwordx4 a[224:227], v252, s[8:11], s20 offen offset:2048 sc1\n\t"
"v_mfma_f32_16x16x32_bf16 a[108:111], a[236:239], a[172:175], a[108:111]\n\t"
"s_mov_b32 s20, 737280\n\t"
"v_mfma_f32_16x16x32_bf16 a[108:111], a[240:243], a[176:179], a[108:111]\n\t"
"s_waitcnt lgkmcnt(0)\n\t"
"s_barrier\n\t"
"v_mfma_f32_16x16x32_bf16 a[116:119], a[232:235], a[172:175], a[116:119]\n\t"
"s_waitcnt vmcnt(5)\n\t"
"ds_read_b64_tr_b16 v[236:237], v240 offset:1024\n\t"
"v_mfma_f32_16x16x32_bf16 a[116:119], a[236:239], a[176:179], a[116:119]\n\t"
"ds_read_b64_tr_b16 v[238:239], v240 offset:2560\n\t"
"v_mfma_f32_16x16x32_bf16 a[116:119], a[240:243], a[180:183], a[116:119]\n\t"
"ds_read_b64_tr_b16 v[228:229], v241 offset:1024\n\t"
"v_mfma_f32_16x16x32_bf16 a[124:127], a[232:235], a[176:179], a[124:127]\n\t"
"ds_read_b64_tr_b16 v[230:231], v241 offset:2560\n\t"
"v_mfma_f32_16x16x32_bf16 a[124:127], a[236:239], a[180:183], a[124:127]\n\t"
"buffer_load_dwordx4 a[228:231], v252, s[8:11], s20 offen offset:0 sc1\n\t"
"v_mfma_f32_16x16x32_bf16 a[124:127], a[240:243], a[184:187], a[124:127]\n\t"
"ds_read_b64_tr_b16 v[232:233], v240 offset:20992\n\t"
"v_mfma_f32_16x16x32_bf16 a[132:135], a[232:235], a[180:183], a[132:135]\n\t"
"s_waitcnt vmcnt(5) lgkmcnt(3)\n\t"
"ds_read_b64_tr_b16 v[234:235], v240 offset:22528\n\t"
"v_mfma_f32_16x16x32_bf16 a[132:135], a[236:239], a[184:187], a[132:135]\n\t"
"ds_read_b64_tr_b16 v[224:225], v241 offset:20992\n\t"
"v_mfma_f32_16x16x32_bf16 a[132:135], a[240:243], a[188:191], a[132:135]\n\t"
"s_waitcnt lgkmcnt(3)\n\t"
"ds_read_b64_tr_b16 v[226:227], v241 offset:22528\n\t"
"v_mfma_f32_16x16x32_bf16 v[236:239], v[236:239], a[208:211], 0\n\t"
"ds_read_b128 a[168:171], v244 offset:0\n\t"
"v_mfma_f32_16x16x32_bf16 v[228:231], v[228:231], a[208:211], 0\n\t"
"ds_read_b128 a[172:175], v244 offset:512\n\t"
"v_mfma_f32_16x16x32_bf16 a[140:143], a[232:235], a[184:187], a[140:143]\n\t"
"ds_read_b128 a[176:179], v244 offset:1024\n\t"
"v_mfma_f32_16x16x32_bf16 a[140:143], a[236:239], a[188:191], a[140:143]\n\t"
"ds_read_b128 a[180:183], v244 offset:1536\n\t"
"v_mfma_f32_16x16x32_bf16 a[140:143], a[240:243], a[192:195], a[140:143]\n\t"
"ds_read_b128 a[184:187], v244 offset:2048\n\t"
"v_mfma_f32_16x16x32_bf16 a[148:151], a[232:235], a[188:191], a[148:151]\n\t"
"s_waitcnt lgkmcnt(7)\n\t"
"ds_read_b128 a[188:191], v244 offset:2560\n\t"
"v_cvt_pk_bf16_f32 v236, v236, v237\n\t"
"v_mfma_f32_16x16x32_bf16 a[148:151], a[236:239], a[192:195], a[148:151]\n\t"
"s_waitcnt lgkmcnt(6)\n\t"
"v_cvt_pk_bf16_f32 v237, v238, v239\n\t"
"v_cvt_pk_bf16_f32 v238, v228, v229\n\t"
"v_mfma_f32_16x16x32_bf16 a[148:151], a[240:243], a[196:199], a[148:151]\n\t"
"v_cvt_pk_bf16_f32 v239, v230, v231\n\t"
"v_mfma_f32_16x16x32_bf16 v[232:235], v[232:235], a[208:211], 0\n\t"
"ds_write_b128 v242, v[236:239] offset:24576\n\t"
"v_mfma_f32_16x16x32_bf16 v[224:227], v[224:227], a[208:211], 0\n\t"
"v_mfma_f32_16x16x32_bf16 a[156:159], a[232:235], a[192:195], a[156:159]\n\t"
"ds_read_b128 a[192:195], v244 offset:3072\n\t"
"v_mfma_f32_16x16x32_bf16 a[156:159], a[236:239], a[196:199], a[156:159]\n\t"
"ds_read_b64_tr_b16 v[228:229], v240 offset:40960\n\t"
"v_mfma_f32_16x16x32_bf16 a[156:159], a[240:243], a[200:203], a[156:159]\n\t"
"ds_read_b64_tr_b16 v[230:231], v240 offset:42496\n\t"
"v_mfma_f32_16x16x32_bf16 a[164:167], a[232:235], a[196:199], a[164:167]\n\t"
"buffer_load_dwordx4 a[232:235], v252, s[8:11], s20 offen offset:1024 sc1\n\t"
"v_mfma_f32_16x16x32_bf16 a[164:167], a[236:239], a[200:203], a[164:167]\n\t"
"s_waitcnt lgkmcnt(7)\n\t"
"ds_read_b64_tr_b16 v[236:237], v241 offset:40960\n\t"
"v_cvt_pk_bf16_f32 v232, v232, v233\n\t"
"v_mfma_f32_16x16x32_bf16 a[164:167], a[240:243], a[204:207], a[164:167]\n\t"
"ds_read_b64_tr_b16 v[238:239], v241 offset:42496\n\t"
"v_cvt_pk_bf16_f32 v233, v234, v235\n\t"
"Lst6_%=:\n\t"
"v_mfma_f32_16x16x32_bf16 v[196:199], a[244:247], a[168:171], v[196:199]\n\t"
"s_waitcnt lgkmcnt(8)\n\t"
"ds_read_b128 a[196:199], v244 offset:3584\n\t"
"v_cvt_pk_bf16_f32 v234, v224, v225\n\t"
"v_mfma_f32_16x16x32_bf16 v[196:199], a[248:251], a[172:175], v[196:199]\n\t"
"ds_read_b128 a[200:203], v244 offset:4096\n\t"
"v_cvt_pk_bf16_f32 v235, v226, v227\n\t"
"v_mfma_f32_16x16x32_bf16 v[196:199], a[252:255], a[176:179], v[196:199]\n\t"
"ds_write_b128 v242, v[232:235] offset:26112\n\t"
"v_mfma_f32_16x16x32_bf16 v[204:207], a[244:247], a[172:175], v[204:207]\n\t"
"buffer_load_dwordx4 a[236:239], v252, s[8:11], s20 offen offset:2048 sc1\n\t"
"v_mfma_f32_16x16x32_bf16 v[204:207], a[248:251], a[176:179], v[204:207]\n\t"
"s_waitcnt lgkmcnt(10)\n\t"
"ds_read_b128 a[204:207], v244 offset:4608\n\t"
"v_mfma_f32_16x16x32_bf16 v[204:207], a[252:255], a[180:183], v[204:207]\n\t"
"s_mov_b32 s20, 749568\n\t"
"v_add_u32 v240, 59904, v240\n\t"
"v_add_u32 v241, 59904, v241\n\t"
"v_mfma_f32_16x16x32_bf16 v[212:215], a[244:247], a[176:179], v[212:215]\n\t"
"s_waitcnt lgkmcnt(6)\n\t"
"ds_read_b64_tr_b16 v[224:225], v240 offset:1024\n\t"
"v_mfma_f32_16x16x32_bf16 v[212:215], a[248:251], a[180:183], v[212:215]\n\t"
"buffer_load_dwordx4 a[240:243], v252, s[8:11], s20 offen offset:0 sc1\n\t"
"v_mfma_f32_16x16x32_bf16 v[212:215], a[252:255], a[184:187], v[212:215]\n\t"
"s_waitcnt lgkmcnt(5)\n\t"
"ds_read_b64_tr_b16 v[226:227], v240 offset:2560\n\t"
"v_mfma_f32_16x16x32_bf16 v[228:231], v[228:231], a[208:211], 0\n\t"
"ds_read_b64_tr_b16 v[232:233], v241 offset:1024\n\t"
"v_mfma_f32_16x16x32_bf16 v[236:239], v[236:239], a[208:211], 0\n\t"
"ds_read_b64_tr_b16 v[234:235], v241 offset:2560\n\t"
"v_mfma_f32_16x16x32_bf16 a[0:3], a[244:247], a[180:183], a[0:3]\n\t"
"v_mfma_f32_16x16x32_bf16 a[0:3], a[248:251], a[184:187], a[0:3]\n\t"
"v_mfma_f32_16x16x32_bf16 a[0:3], a[252:255], a[188:191], a[0:3]\n\t"
"v_mfma_f32_16x16x32_bf16 a[8:11], a[244:247], a[184:187], a[8:11]\n\t"
"s_waitcnt lgkmcnt(2)\n\t"
"v_cvt_pk_bf16_f32 v228, v228, v229\n\t"
"v_cvt_pk_bf16_f32 v229, v230, v231\n\t"
"v_mfma_f32_16x16x32_bf16 a[8:11], a[248:251], a[188:191], a[8:11]\n\t"
"s_waitcnt lgkmcnt(0)\n\t"
"v_cvt_pk_bf16_f32 v230, v236, v237\n\t"
"v_cvt_pk_bf16_f32 v231, v238, v239\n\t"
"v_mfma_f32_16x16x32_bf16 a[8:11], a[252:255], a[192:195], a[8:11]\n\t"
"ds_write_b128 v242, v[228:231] offset:27648\n\t"
"v_mfma_f32_16x16x32_bf16 v[224:227], v[224:227], a[208:211], 0\n\t"
"v_mfma_f32_16x16x32_bf16 v[232:235], v[232:235], a[208:211], 0\n\t"
"ds_read_b64_tr_b16 v[236:237], v240 offset:20992\n\t"
"v_mfma_f32_16x16x32_bf16 a[16:19], a[244:247], a[188:191], a[16:19]\n\t"
"ds_read_b64_tr_b16 v[238:239], v240 offset:22528\n\t"
"v_mfma_f32_16x16x32_bf16 a[16:19], a[248:251], a[192:195], a[16:19]\n\t"
"ds_read_b64_tr_b16 v[228:229], v241 offset:20992\n\t"
"v_mfma_f32_16x16x32_bf16 a[16:19], a[252:255], a[196:199], a[16:19]\n\t"
"ds_read_b64_tr_b16 v[230:231], v241 offset:22528\n\t"
"v_mfma_f32_16x16x32_bf16 a[24:27], a[244:247], a[192:195], a[24:27]\n\t"
"v_cvt_pk_bf16_f32 v224, v224, v225\n\t"
"v_cvt_pk_bf16_f32 v225, v226, v227\n\t"
"v_mfma_f32_16x16x32_bf16 a[24:27], a[248:251], a[196:199], a[24:27]\n\t"
"v_cvt_pk_bf16_f32 v226, v232, v233\n\t"
"v_cvt_pk_bf16_f32 v227, v234, v235\n\t"
"v_mfma_f32_16x16x32_bf16 a[24:27], a[252:255], a[200:203], a[24:27]\n\t"
"ds_write_b128 v242, v[224:227] offset:29184\n\t"
"v_mfma_f32_16x16x32_bf16 a[32:35], a[244:247], a[196:199], a[32:35]\n\t"
"buffer_load_dwordx4 a[244:247], v252, s[8:11], s20 offen offset:1024 sc1\n\t"
"v_mfma_f32_16x16x32_bf16 a[32:35], a[248:251], a[200:203], a[32:35]\n\t"
"s_waitcnt vmcnt(8)\n\t"
"ds_read_b64_tr_b16 v[232:233], v240 offset:40960\n\t"
"v_mfma_f32_16x16x32_bf16 a[32:35], a[252:255], a[204:207], a[32:35]\n\t"
"ds_read_b64_tr_b16 v[234:235], v240 offset:42496\n\t"
"v_mfma_f32_16x16x32_bf16 a[40:43], v[248:251], a[168:171], a[40:43]\n\t"
"buffer_load_dwordx4 a[248:251], v252, s[8:11], s20 offen offset:2048 sc1\n\t"
"v_mfma_f32_16x16x32_bf16 a[40:43], a[212:215], a[172:175], a[40:43]\n\t"
"s_mov_b32 s20, 761856\n\t"
"ds_read_b64_tr_b16 v[224:225], v241 offset:40960\n\t"
"v_mfma_f32_16x16x32_bf16 a[40:43], a[216:219], a[176:179], a[40:43]\n\t"
"ds_read_b64_tr_b16 v[226:227], v241 offset:42496\n\t"
"v_mfma_f32_16x16x32_bf16 a[48:51], v[248:251], a[172:175], a[48:51]\n\t"
"buffer_load_dwordx4 a[252:255], v252, s[8:11], s20 offen offset:0 sc1\n\t"
"v_mfma_f32_16x16x32_bf16 a[48:51], a[212:215], a[176:179], a[48:51]\n\t"
"s_waitcnt lgkmcnt(5)\n\t"
"v_mfma_f32_16x16x32_bf16 a[48:51], a[216:219], a[180:183], a[48:51]\n\t"
"v_mfma_f32_16x16x32_bf16 v[236:239], v[236:239], a[208:211], 0\n\t"
"v_mfma_f32_16x16x32_bf16 v[228:231], v[228:231], a[208:211], 0\n\t"
"v_xor_b32 v246, 0xc0, v246\n\t"
"v_mfma_f32_16x16x32_bf16 a[56:59], v[248:251], a[176:179], a[56:59]\n\t"
"v_mfma_f32_16x16x32_bf16 a[56:59], a[212:215], a[180:183], a[56:59]\n\t"
"v_mfma_f32_16x16x32_bf16 a[56:59], a[216:219], a[184:187], a[56:59]\n\t"
"v_mfma_f32_16x16x32_bf16 a[64:67], v[248:251], a[180:183], a[64:67]\n\t"
"s_waitcnt lgkmcnt(2)\n\t"
"v_cvt_pk_bf16_f32 v236, v236, v237\n\t"
"v_cvt_pk_bf16_f32 v237, v238, v239\n\t"
"v_mfma_f32_16x16x32_bf16 a[64:67], a[212:215], a[184:187], a[64:67]\n\t"
"s_waitcnt lgkmcnt(0)\n\t"
"v_cvt_pk_bf16_f32 v238, v228, v229\n\t"
"v_cvt_pk_bf16_f32 v239, v230, v231\n\t"
"v_mfma_f32_16x16x32_bf16 a[64:67], a[216:219], a[188:191], a[64:67]\n\t"
"ds_write_b128 v242, v[236:239] offset:30720\n\t"
"v_mfma_f32_16x16x32_bf16 v[232:235], v[232:235], a[208:211], 0\n\t"
"v_mfma_f32_16x16x32_bf16 v[224:227], v[224:227], a[208:211], 0\n\t"
"v_mfma_f32_16x16x32_bf16 a[72:75], v[248:251], a[184:187], a[72:75]\n\t"
"buffer_load_dwordx4 a[208:211], v252, s[8:11], s20 offen offset:1024 sc1\n\t"
"v_mfma_f32_16x16x32_bf16 a[72:75], a[212:215], a[188:191], a[72:75]\n\t"
"v_mfma_f32_16x16x32_bf16 a[72:75], a[216:219], a[192:195], a[72:75]\n\t"
"v_mfma_f32_16x16x32_bf16 a[80:83], v[248:251], a[188:191], a[80:83]\n\t"
"v_cvt_pk_bf16_f32 v232, v232, v233\n\t"
"v_cvt_pk_bf16_f32 v233, v234, v235\n\t"
"v_mfma_f32_16x16x32_bf16 a[80:83], a[212:215], a[192:195], a[80:83]\n\t"
"v_cvt_pk_bf16_f32 v234, v224, v225\n\t"
"v_cvt_pk_bf16_f32 v235, v226, v227\n\t"
"v_mfma_f32_16x16x32_bf16 a[80:83], a[216:219], a[196:199], a[80:83]\n\t"
"ds_write_b128 v242, v[232:235] offset:32256\n\t"
"v_mfma_f32_16x16x32_bf16 a[88:91], v[248:251], a[192:195], a[88:91]\n\t"
"v_mfma_f32_16x16x32_bf16 a[88:91], a[212:215], a[196:199], a[88:91]\n\t"
"ds_read_b64_tr_b16 v[228:229], v246 offset:1024\n\t"
"v_mfma_f32_16x16x32_bf16 a[88:91], a[216:219], a[200:203], a[88:91]\n\t"
"ds_read_b64_tr_b16 v[230:231], v246 offset:4096\n\t"
"v_mfma_f32_16x16x32_bf16 a[96:99], v[248:251], a[196:199], a[96:99]\n\t"
"buffer_load_dwordx4 v[248:251], v252, s[8:11], s20 offen offset:2048 sc1\n\t"
"v_mfma_f32_16x16x32_bf16 a[96:99], a[212:215], a[200:203], a[96:99]\n\t"
"s_waitcnt vmcnt(9)\n\t"
"ds_read_b64_tr_b16 v[236:237], v246 offset:7168\n\t"
"s_mov_b32 s20, 626688\n\t"
"v_mfma_f32_16x16x32_bf16 a[96:99], a[216:219], a[204:207], a[96:99]\n\t"
"ds_read_b64_tr_b16 v[238:239], v246 offset:10240\n\t"
"v_mfma_f32_16x16x32_bf16 a[104:107], a[220:223], a[168:171], a[104:107]\n\t"
"buffer_load_dwordx4 v[240:243], v252, s[8:11], s20 offen offset:0 sc1\n\t"
"v_mfma_f32_16x16x32_bf16 a[104:107], v[96:99], a[172:175], a[104:107]\n\t"
"ds_read_b64_tr_b16 v[224:225], v246 offset:13312\n\t"
"v_mfma_f32_16x16x32_bf16 a[104:107], a[224:227], a[176:179], a[104:107]\n\t"
"ds_read_b64_tr_b16 v[226:227], v246 offset:16384\n\t"
"v_mfma_f32_16x16x32_bf16 a[112:115], a[220:223], a[172:175], a[112:115]\n\t"
"buffer_load_dwordx4 a[212:215], v252, s[8:11], s20 offen offset:1024 sc1\n\t"
"v_mfma_f32_16x16x32_bf16 a[112:115], v[96:99], a[176:179], a[112:115]\n\t"
"ds_read_b128 a[168:171], v244 offset:9216\n\t"
"v_mfma_f32_16x16x32_bf16 a[112:115], a[224:227], a[180:183], a[112:115]\n\t"
"ds_read_b64_tr_b16 v[232:233], v246 offset:20992\n\t"
"v_mfma_f32_16x16x32_bf16 a[120:123], a[220:223], a[176:179], a[120:123]\n\t"
"buffer_load_dwordx4 a[216:219], v252, s[8:11], s20 offen offset:2048 sc1\n\t"
"v_mfma_f32_16x16x32_bf16 a[120:123], v[96:99], a[180:183], a[120:123]\n\t"
"s_waitcnt lgkmcnt(6)\n\t"
"ds_read_b64_tr_b16 v[234:235], v246 offset:24064\n\t"
"v_mfma_f32_16x16x32_bf16 a[120:123], a[224:227], a[184:187], a[120:123]\n\t"
"ds_read_b128 a[172:175], v244 offset:9728\n\t"
"v_mfma_f32_16x16x32_bf16 v[228:231], v[228:231], v[220:223], 0\n\t"
"ds_read_b128 a[176:179], v244 offset:10240\n\t"
"v_mfma_f32_16x16x32_bf16 a[128:131], a[220:223], a[180:183], a[128:131]\n\t"
"s_waitcnt lgkmcnt(7)\n\t"
"ds_read_b128 a[180:183], v244 offset:10752\n\t"
"v_mfma_f32_16x16x32_bf16 a[128:131], v[96:99], a[184:187], a[128:131]\n\t"
"v_mfma_f32_16x16x32_bf16 a[128:131], a[224:227], a[188:191], a[128:131]\n\t"
"v_mfma_f32_16x16x32_bf16 v[236:239], v[236:239], v[220:223], 0\n\t"
"v_mfma_f32_16x16x32_bf16 a[136:139], a[220:223], a[184:187], a[136:139]\n\t"
"s_waitcnt lgkmcnt(6)\n\t"
"ds_read_b128 a[184:187], v244 offset:11264\n\t"
"v_cvt_pk_bf16_f32 v228, v228, v229\n\t"
"v_mfma_f32_16x16x32_bf16 a[136:139], v[96:99], a[188:191], a[136:139]\n\t"
"v_cvt_pk_bf16_f32 v229, v230, v231\n\t"
"v_mfma_f32_16x16x32_bf16 a[136:139], a[224:227], a[192:195], a[136:139]\n\t"
"s_mov_b32 s20, 638976\n\t"
"v_mfma_f32_16x16x32_bf16 v[224:227], v[224:227], v[220:223], 0\n\t"
"v_mfma_f32_16x16x32_bf16 a[144:147], a[220:223], a[188:191], a[144:147]\n\t"
"s_waitcnt lgkmcnt(4)\n\t"
"ds_read_b128 a[188:191], v244 offset:11776\n\t"
"v_cvt_pk_bf16_f32 v230, v236, v237\n\t"
"v_mfma_f32_16x16x32_bf16 a[144:147], v[96:99], a[192:195], a[144:147]\n\t"
"v_cvt_pk_bf16_f32 v231, v238, v239\n\t"
"v_mfma_f32_16x16x32_bf16 a[144:147], a[224:227], a[196:199], a[144:147]\n\t"
"v_mfma_f32_16x16x32_bf16 v[232:235], v[232:235], v[220:223], 0\n\t"
"v_mfma_f32_16x16x32_bf16 a[152:155], a[220:223], a[192:195], a[152:155]\n\t"
"buffer_load_dwordx4 v[236:239], v252, s[8:11], s20 offen offset:0 sc1\n\t"
"v_mfma_f32_16x16x32_bf16 a[152:155], v[96:99], a[196:199], a[152:155]\n\t"
"ds_read_b128 a[192:195], v244 offset:12288\n\t"
"v_cvt_pk_bf16_f32 v224, v224, v225\n\t"
"v_mfma_f32_16x16x32_bf16 a[152:155], a[224:227], a[200:203], a[152:155]\n\t"
"v_cvt_pk_bf16_f32 v225, v226, v227\n\t"
"v_mfma_f32_16x16x32_bf16 a[160:163], a[220:223], a[196:199], a[160:163]\n\t"
"buffer_load_dwordx4 a[220:223], v252, s[8:11], s20 offen offset:1024 sc1\n\t"
"v_mfma_f32_16x16x32_bf16 a[160:163], v[96:99], a[200:203], a[160:163]\n\t"
"s_waitcnt vmcnt(11) lgkmcnt(4)\n\t"
"ds_read_b128 a[196:199], v244 offset:12800\n\t"
"v_cvt_pk_bf16_f32 v226, v232, v233\n\t"
"v_mfma_f32_16x16x32_bf16 a[160:163], a[224:227], a[204:207], a[160:163]\n\t"
"ds_read_b128 a[200:203], v244 offset:13312\n\t"
"v_cvt_pk_bf16_f32 v227, v234, v235\n\t"
"v_mfma_f32_16x16x32_bf16 v[200:203], a[228:231], a[168:171], v[200:203]\n\t"
"ds_read_b64_tr_b16 v[232:233], v246 offset:27136\n\t"
"v_mfma_f32_16x16x32_bf16 v[200:203], a[232:235], a[172:175], v[200:203]\n\t"
"s_waitcnt lgkmcnt(6)\n\t"
"ds_read_b64_tr_b16 v[234:235], v246 offset:30208\n\t"
"v_mfma_f32_16x16x32_bf16 v[200:203], a[236:239], a[176:179], v[200:203]\n\t"
"s_waitcnt lgkmcnt(0)\n\t"
"ds_read_b128 a[204:207], v244 offset:13824\n\t"
"v_mfma_f32_16x16x32_bf16 v[232:235], v[232:235], v[220:223], 0\n\t"
"buffer_load_dwordx4 v[96:99], v252, s[8:11], s20 offen offset:2048 sc1\n\t"
"s_mov_b32 s20, 651264\n\t"
"v_mfma_f32_16x16x32_bf16 v[208:211], a[228:231], a[172:175], v[208:211]\n\t"
"buffer_load_dwordx4 a[224:227], v252, s[8:11], s20 offen offset:0 sc1\n\t"
"v_mfma_f32_16x16x32_bf16 v[208:211], a[232:235], a[176:179], v[208:211]\n\t"
"s_add_u32 m0, s24, 256\n\t"
"s_add_u32 s28, s21, 0\n\t"
"v_mfma_f32_16x16x32_bf16 v[208:211], a[236:239], a[180:183], v[208:211]\n\t"
"s_waitcnt lgkmcnt(0)\n\t"
"s_barrier\n\t"
"v_mfma_f32_16x16x32_bf16 v[216:219], a[228:231], a[176:179], v[216:219]\n\t"
"v_mfma_f32_16x16x32_bf16 v[216:219], a[232:235], a[180:183], v[216:219]\n\t"
"buffer_load_dwordx4 v247, s[12:15], s28 offen offset:256 nt lds\n\t"
"v_mfma_f32_16x16x32_bf16 v[216:219], a[236:239], a[184:187], v[216:219]\n\t"
"s_add_u32 m0, s24, 6400\n\t"
"s_add_u32 s28, s21, 196608\n\t"
"ds_write2st64_b64 v245, v[228:229], v[230:231] offset0:0 offset1:1\n\t"
"v_mfma_f32_16x16x32_bf16 a[4:7], a[228:231], a[180:183], a[4:7]\n\t"
"buffer_load_dwordx4 v247, s[12:15], s28 offen offset:256 nt lds\n\t"
"v_mfma_f32_16x16x32_bf16 a[4:7], a[232:235], a[184:187], a[4:7]\n\t"
"s_add_u32 m0, s24, 12544\n\t"
"s_add_u32 s28, s21, 393216\n\t"
"ds_read_b64_tr_b16 v[228:229], v246 offset:33280\n\t"
"v_mfma_f32_16x16x32_bf16 a[4:7], a[236:239], a[188:191], a[4:7]\n\t"
"ds_write2st64_b64 v245, v[224:225], v[226:227] offset0:2 offset1:3\n\t"
"v_cvt_pk_bf16_f32 v232, v232, v233\n\t"
"v_cvt_pk_bf16_f32 v233, v234, v235\n\t"
"v_mfma_f32_16x16x32_bf16 a[12:15], a[228:231], a[184:187], a[12:15]\n\t"
"v_mfma_f32_16x16x32_bf16 a[12:15], a[232:235], a[188:191], a[12:15]\n\t"
"ds_read_b64_tr_b16 v[230:231], v246 offset:36352\n\t"
"v_mfma_f32_16x16x32_bf16 a[12:15], a[236:239], a[192:195], a[12:15]\n\t"
"buffer_load_dwordx4 v247, s[12:15], s28 offen offset:256 nt lds\n\t"
"s_add_u32 m0, s24, 20224\n\t"
"s_add_u32 s28, s21, 589824\n\t"
"v_mfma_f32_16x16x32_bf16 a[20:23], a[228:231], a[188:191], a[20:23]\n\t"
"buffer_load_dwordx4 v247, s[12:15], s28 offen offset:256 nt lds\n\t"
"v_mfma_f32_16x16x32_bf16 a[20:23], a[232:235], a[192:195], a[20:23]\n\t"
"s_add_u32 m0, s24, 26368\n\t"
"s_add_u32 s28, s21, 786432\n\t"
"ds_read_b64_tr_b16 v[224:225], v246 offset:40960\n\t"
"v_mfma_f32_16x16x32_bf16 a[20:23], a[236:239], a[196:199], a[20:23]\n\t"
"s_waitcnt lgkmcnt(1)\n\t"
"ds_write_b64 v245, v[232:233] offset:2048\n\t"
"ds_read_b64_tr_b16 v[226:227], v246 offset:44032\n\t"
"v_mfma_f32_16x16x32_bf16 v[228:231], v[228:231], v[220:223], 0\n\t"
"buffer_load_dwordx4 v247, s[12:15], s28 offen offset:256 nt lds\n\t"
"s_add_u32 m0, s24, 60160\n\t"
"s_add_u32 s28, s21, 1769472\n\t"
"v_mfma_f32_16x16x32_bf16 a[28:31], a[228:231], a[192:195], a[28:31]\n\t"
"buffer_load_dwordx4 v247, s[12:15], s28 offen offset:256 nt lds\n\t"
"v_mfma_f32_16x16x32_bf16 a[28:31], a[232:235], a[196:199], a[28:31]\n\t"
"ds_read_b64_tr_b16 v[232:233], v246 offset:47104\n\t"
"v_mfma_f32_16x16x32_bf16 a[28:31], a[236:239], a[200:203], a[28:31]\n\t"
"s_waitcnt lgkmcnt(1)\n\t"
"ds_read_b64_tr_b16 v[234:235], v246 offset:50176\n\t"
"v_mfma_f32_16x16x32_bf16 v[224:227], v[224:227], v[220:223], 0\n\t"
"v_mfma_f32_16x16x32_bf16 a[36:39], a[228:231], a[196:199], a[36:39]\n\t"
"v_cvt_pk_bf16_f32 v228, v228, v229\n\t"
"v_cvt_pk_bf16_f32 v229, v230, v231\n\t"
"v_mfma_f32_16x16x32_bf16 a[36:39], a[232:235], a[200:203], a[36:39]\n\t"
"s_waitcnt vmcnt(16)\n\t"
"v_mfma_f32_16x16x32_bf16 a[36:39], a[236:239], a[204:207], a[36:39]\n\t"
"s_waitcnt lgkmcnt(0)\n\t"
"v_mfma_f32_16x16x32_bf16 v[232:235], v[232:235], v[220:223], 0\n\t"
"v_mfma_f32_16x16x32_bf16 a[44:47], a[240:243], a[168:171], a[44:47]\n\t"
"v_cvt_pk_bf16_f32 v230, v224, v225\n\t"
"v_cvt_pk_bf16_f32 v231, v226, v227\n\t"
"v_mfma_f32_16x16x32_bf16 a[44:47], a[244:247], a[172:175], a[44:47]\n\t"
"ds_read_b64_tr_b16 v[224:225], v246 offset:53248\n\t"
"v_mfma_f32_16x16x32_bf16 a[44:47], a[248:251], a[176:179], a[44:47]\n\t"
"ds_write2st64_b64 v245, v[228:229], v[230:231] offset0:5 offset1:6\n\t"
"v_mfma_f32_16x16x32_bf16 a[52:55], a[240:243], a[172:175], a[52:55]\n\t"
"v_mfma_f32_16x16x32_bf16 a[52:55], a[244:247], a[176:179], a[52:55]\n\t"
"ds_read_b64_tr_b16 v[226:227], v246 offset:56320\n\t"
"v_mfma_f32_16x16x32_bf16 a[52:55], a[248:251], a[180:183], a[52:55]\n\t"
"v_cvt_pk_bf16_f32 v232, v232, v233\n\t"
"v_cvt_pk_bf16_f32 v233, v234, v235\n\t"
"v_mfma_f32_16x16x32_bf16 a[60:63], a[240:243], a[176:179], a[60:63]\n\t"
"v_mfma_f32_16x16x32_bf16 a[60:63], a[244:247], a[180:183], a[60:63]\n\t"
"v_mfma_f32_16x16x32_bf16 a[60:63], a[248:251], a[184:187], a[60:63]\n\t"
"v_mfma_f32_16x16x32_bf16 a[68:71], a[240:243], a[180:183], a[68:71]\n\t"
"v_mfma_f32_16x16x32_bf16 a[68:71], a[244:247], a[184:187], a[68:71]\n\t"
"v_mfma_f32_16x16x32_bf16 a[68:71], a[248:251], a[188:191], a[68:71]\n\t"
"v_mfma_f32_16x16x32_bf16 a[76:79], a[240:243], a[184:187], a[76:79]\n\t"
"s_waitcnt lgkmcnt(0)\n\t"
"v_mfma_f32_16x16x32_bf16 a[76:79], a[244:247], a[188:191], a[76:79]\n\t"
"v_mfma_f32_16x16x32_bf16 a[76:79], a[248:251], a[192:195], a[76:79]\n\t"
"v_mfma_f32_16x16x32_bf16 v[224:227], v[224:227], v[220:223], 0\n\t"
"v_mfma_f32_16x16x32_bf16 a[84:87], a[240:243], a[188:191], a[84:87]\n\t"
"v_mfma_f32_16x16x32_bf16 a[84:87], a[244:247], a[192:195], a[84:87]\n\t"
"buffer_load_dwordx4 a[228:231], v252, s[8:11], s20 offen offset:1024 sc1\n\t"
"v_mfma_f32_16x16x32_bf16 a[84:87], a[248:251], a[196:199], a[84:87]\n\t"
"v_mfma_f32_16x16x32_bf16 a[92:95], a[240:243], a[192:195], a[92:95]\n\t"
"v_mfma_f32_16x16x32_bf16 a[92:95], a[244:247], a[196:199], a[92:95]\n\t"
"v_cvt_pk_bf16_f32 v234, v224, v225\n\t"
"v_cvt_pk_bf16_f32 v235, v226, v227\n\t"
"v_mfma_f32_16x16x32_bf16 a[92:95], a[248:251], a[200:203], a[92:95]\n\t"
"ds_write2st64_b64 v245, v[232:233], v[234:235] offset0:7 offset1:8\n\t"
"v_mfma_f32_16x16x32_bf16 a[100:103], a[240:243], a[196:199], a[100:103]\n\t"
"s_waitcnt vmcnt(14)\n\t"
"v_mfma_f32_16x16x32_bf16 a[100:103], a[244:247], a[200:203], a[100:103]\n\t"
"v_mfma_f32_16x16x32_bf16 a[100:103], a[248:251], a[204:207], a[100:103]\n\t"
"v_mfma_f32_16x16x32_bf16 a[108:111], a[252:255], a[168:171], a[108:111]\n\t"
"v_mfma_f32_16x16x32_bf16 a[108:111], a[208:211], a[172:175], a[108:111]\n\t"
"v_mfma_f32_16x16x32_bf16 a[108:111], v[248:251], a[176:179], a[108:111]\n\t"
"s_waitcnt lgkmcnt(0)\n\t"
"s_barrier\n\t"
"v_mfma_f32_16x16x32_bf16 a[116:119], a[252:255], a[172:175], a[116:119]\n\t"
"ds_read_b128 a[168:171], v244 offset:0\n\t"
"v_mfma_f32_16x16x32_bf16 a[116:119], a[208:211], a[176:179], a[116:119]\n\t"
"ds_read_b128 a[172:175], v244 offset:512\n\t"
"v_mfma_f32_16x16x32_bf16 a[116:119], v[248:251], a[180:183], a[116:119]\n\t"
"v_mfma_f32_16x16x32_bf16 a[124:127], a[252:255], a[176:179], a[124:127]\n\t"
"ds_read_b128 a[176:179], v244 offset:1024\n\t"
"v_mfma_f32_16x16x32_bf16 a[124:127], a[208:211], a[180:183], a[124:127]\n\t"
"v_mfma_f32_16x16x32_bf16 a[124:127], v[248:251], a[184:187], a[124:127]\n\t"
"v_mfma_f32_16x16x32_bf16 a[132:135], a[252:255], a[180:183], a[132:135]\n\t"
"ds_read_b128 a[180:183], v244 offset:1536\n\t"
"v_mfma_f32_16x16x32_bf16 a[132:135], a[208:211], a[184:187], a[132:135]\n\t"
"v_mfma_f32_16x16x32_bf16 a[132:135], v[248:251], a[188:191], a[132:135]\n\t"
"v_mfma_f32_16x16x32_bf16 a[140:143], a[252:255], a[184:187], a[140:143]\n\t"
"ds_read_b128 a[184:187], v244 offset:2048\n\t"
"v_mfma_f32_16x16x32_bf16 a[140:143], a[208:211], a[188:191], a[140:143]\n\t"
"v_mfma_f32_16x16x32_bf16 a[140:143], v[248:251], a[192:195], a[140:143]\n\t"
"v_mfma_f32_16x16x32_bf16 a[148:151], a[252:255], a[188:191], a[148:151]\n\t"
"ds_read_b128 a[188:191], v244 offset:2560\n\t"
"v_mfma_f32_16x16x32_bf16 a[148:151], a[208:211], a[192:195], a[148:151]\n\t"
"v_mfma_f32_16x16x32_bf16 a[148:151], v[248:251], a[196:199], a[148:151]\n\t"
"v_mfma_f32_16x16x32_bf16 a[156:159], a[252:255], a[192:195], a[156:159]\n\t"
"ds_read_b128 a[192:195], v244 offset:3072\n\t"
"v_mfma_f32_16x16x32_bf16 a[156:159], a[208:211], a[196:199], a[156:159]\n\t"
"v_mfma_f32_16x16x32_bf16 a[156:159], v[248:251], a[200:203], a[156:159]\n\t"
"v_mfma_f32_16x16x32_bf16 a[164:167], a[252:255], a[196:199], a[164:167]\n\t"
"s_waitcnt vmcnt(11) lgkmcnt(4)\n\t"
"ds_read_b128 a[196:199], v244 offset:3584\n\t"
"v_mfma_f32_16x16x32_bf16 a[164:167], a[208:211], a[200:203], a[164:167]\n\t"
"ds_read_b128 a[200:203], v244 offset:4096\n\t"
"v_mfma_f32_16x16x32_bf16 a[164:167], v[248:251], a[204:207], a[164:167]\n\t"
"ds_read_b128 a[204:207], v244 offset:4608\n\t"
"Lst7_%=:\n\t"
"v_mfma_f32_16x16x32_bf16 v[0:3], v[240:243], a[168:171], v[0:3]\n\t"
"buffer_load_dwordx4 a[208:211], v252, s[8:11], s20 offen offset:2048 sc1\n\t"
"v_mfma_f32_16x16x32_bf16 v[0:3], a[212:215], a[172:175], v[0:3]\n\t"
"s_waitcnt lgkmcnt(6)\n\t"
"s_mov_b32 s20, 663552\n\t"
"v_mfma_f32_16x16x32_bf16 v[0:3], a[216:219], a[176:179], v[0:3]\n\t"
"v_mfma_f32_16x16x32_bf16 v[4:7], v[240:243], a[172:175], v[4:7]\n\t"
"s_waitcnt lgkmcnt(5)\n\t"
"v_mfma_f32_16x16x32_bf16 v[4:7], a[212:215], a[176:179], v[4:7]\n\t"
"buffer_load_dwordx4 v[248:251], v252, s[8:11], s20 offen offset:0 sc1\n\t"
"v_mfma_f32_16x16x32_bf16 v[4:7], a[216:219], a[180:183], v[4:7]\n\t"
"v_mfma_f32_16x16x32_bf16 v[8:11], v[240:243], a[176:179], v[8:11]\n\t"
"s_waitcnt lgkmcnt(4)\n\t"
"v_mfma_f32_16x16x32_bf16 v[8:11], a[212:215], a[180:183], v[8:11]\n\t"
"v_mfma_f32_16x16x32_bf16 v[8:11], a[216:219], a[184:187], v[8:11]\n\t"
"buffer_load_dwordx4 a[232:235], v252, s[8:11], s20 offen offset:1024 sc1\n\t"
"v_mfma_f32_16x16x32_bf16 v[12:15], v[240:243], a[180:183], v[12:15]\n\t"
"s_waitcnt lgkmcnt(3)\n\t"
"v_mfma_f32_16x16x32_bf16 v[12:15], a[212:215], a[184:187], v[12:15]\n\t"
"v_mfma_f32_16x16x32_bf16 v[12:15], a[216:219], a[188:191], v[12:15]\n\t"
"v_mfma_f32_16x16x32_bf16 v[16:19], v[240:243], a[184:187], v[16:19]\n\t"
"buffer_load_dwordx4 a[236:239], v252, s[8:11], s20 offen offset:2048 sc1\n\t"
"v_mfma_f32_16x16x32_bf16 v[16:19], a[212:215], a[188:191], v[16:19]\n\t"
"s_waitcnt lgkmcnt(2)\n\t"
"s_mov_b32 s20, 675840\n\t"
"v_mfma_f32_16x16x32_bf16 v[16:19], a[216:219], a[192:195], v[16:19]\n\t"
"ds_read_b64_tr_b16 v[228:229], v246 offset:1536\n\t"
"v_mfma_f32_16x16x32_bf16 v[20:23], v[240:243], a[188:191], v[20:23]\n\t"
"s_waitcnt lgkmcnt(2)\n\t"
"ds_read_b64_tr_b16 v[230:231], v246 offset:4608\n\t"
"v_mfma_f32_16x16x32_bf16 v[20:23], a[212:215], a[192:195], v[20:23]\n\t"
"buffer_load_dwordx4 a[240:243], v252, s[8:11], s20 offen offset:0 sc1\n\t"
"v_mfma_f32_16x16x32_bf16 v[20:23], a[216:219], a[196:199], v[20:23]\n\t"
"v_mfma_f32_16x16x32_bf16 v[24:27], v[240:243], a[192:195], v[24:27]\n\t"
"s_waitcnt lgkmcnt(2)\n\t"
"v_mfma_f32_16x16x32_bf16 v[24:27], a[212:215], a[196:199], v[24:27]\n\t"
"v_mfma_f32_16x16x32_bf16 v[24:27], a[216:219], a[200:203], v[24:27]\n\t"
"buffer_load_dwordx4 a[244:247], v252, s[8:11], s20 offen offset:1024 sc1\n\t"
"v_mfma_f32_16x16x32_bf16 v[28:31], v[240:243], a[196:199], v[28:31]\n\t"
"s_waitcnt vmcnt(14)\n\t"
"ds_read_b64_tr_b16 v[224:225], v246 offset:7680\n\t"
"v_mfma_f32_16x16x32_bf16 v[28:31], a[212:215], a[200:203], v[28:31]\n\t"
"ds_read_b64_tr_b16 v[226:227], v246 offset:10752\n\t"
"v_mfma_f32_16x16x32_bf16 v[28:31], a[216:219], a[204:207], v[28:31]\n\t"
"v_mfma_f32_16x16x32_bf16 v[32:35], v[236:239], a[168:171], v[32:35]\n\t"
"buffer_load_dwordx4 v[240:243], v252, s[8:11], s20 offen offset:2048 sc1\n\t"
"v_mfma_f32_16x16x32_bf16 v[32:35], a[220:223], a[172:175], v[32:35]\n\t"
"s_mov_b32 s20, 688128\n\t"
"v_mfma_f32_16x16x32_bf16 v[32:35], v[96:99], a[176:179], v[32:35]\n\t"
"v_mfma_f32_16x16x32_bf16 v[36:39], v[236:239], a[172:175], v[36:39]\n\t"
"s_waitcnt lgkmcnt(2)\n\t"
"v_mfma_f32_16x16x32_bf16 v[36:39], a[220:223], a[176:179], v[36:39]\n\t"
"buffer_load_dwordx4 a[212:215], v252, s[8:11], s20 offen offset:0 sc1\n\t"
"v_mfma_f32_16x16x32_bf16 v[36:39], v[96:99], a[180:183], v[36:39]\n\t"
"v_mfma_f32_16x16x32_bf16 v[228:231], v[228:231], v[220:223], 0\n\t"
"v_mfma_f32_16x16x32_bf16 v[40:43], v[236:239], a[176:179], v[40:43]\n\t"
"ds_read_b64_tr_b16 v[232:233], v246 offset:13824\n\t"
"v_mfma_f32_16x16x32_bf16 v[40:43], a[220:223], a[180:183], v[40:43]\n\t"
"buffer_load_dwordx4 a[216:219], v252, s[8:11], s20 offen offset:1024 sc1\n\t"
"v_mfma_f32_16x16x32_bf16 v[40:43], v[96:99], a[184:187], v[40:43]\n\t"
"ds_read_b64_tr_b16 v[234:235], v246 offset:16896\n\t"
"v_mfma_f32_16x16x32_bf16 v[44:47], v[236:239], a[180:183], v[44:47]\n\t"
"s_waitcnt lgkmcnt(2)\n\t"
"v_mfma_f32_16x16x32_bf16 v[44:47], a[220:223], a[184:187], v[44:47]\n\t"
"buffer_load_dwordx4 a[248:251], v252, s[8:11], s20 offen offset:2048 sc1\n\t"
"v_mfma_f32_16x16x32_bf16 v[44:47], v[96:99], a[188:191], v[44:47]\n\t"
"s_mov_b32 s20, 921600\n\t"
"v_cvt_pk_bf16_f32 v228, v228, v229\n\t"
"v_cvt_pk_bf16_f32 v229, v230, v231\n\t"
"v_mfma_f32_16x16x32_bf16 v[224:227], v[224:227], v[220:223], 0\n\t"
"v_mfma_f32_16x16x32_bf16 v[48:51], v[236:239], a[184:187], v[48:51]\n\t"
"v_mfma_f32_16x16x32_bf16 v[48:51], a[220:223], a[188:191], v[48:51]\n\t"
"buffer_load_dwordx4 a[252:255], v252, s[8:11], s20 offen offset:0 sc1\n\t"
"v_mfma_f32_16x16x32_bf16 v[48:51], v[96:99], a[192:195], v[48:51]\n\t"
"v_mfma_f32_16x16x32_bf16 v[52:55], v[236:239], a[188:191], v[52:55]\n\t"
"v_mfma_f32_16x16x32_bf16 v[52:55], a[220:223], a[192:195], v[52:55]\n\t"
"v_cvt_pk_bf16_f32 v230, v224, v225\n\t"
"v_cvt_pk_bf16_f32 v231, v226, v227\n\t"
"v_mfma_f32_16x16x32_bf16 v[52:55], v[96:99], a[196:199], v[52:55]\n\t"
"ds_read_b64_tr_b16 v[224:225], v246 offset:21504\n\t"
"v_mfma_f32_16x16x32_bf16 v[56:59], v[236:239], a[192:195], v[56:59]\n\t"
"s_waitcnt lgkmcnt(1)\n\t"
"ds_read_b64_tr_b16 v[226:227], v246 offset:24576\n\t"
"v_mfma_f32_16x16x32_bf16 v[56:59], a[220:223], a[196:199], v[56:59]\n\t"
"v_mfma_f32_16x16x32_bf16 v[56:59], v[96:99], a[200:203], v[56:59]\n\t"
"v_mfma_f32_16x16x32_bf16 v[232:235], v[232:235], v[220:223], 0\n\t"
"v_mfma_f32_16x16x32_bf16 v[60:63], v[236:239], a[196:199], v[60:63]\n\t"
"buffer_load_dwordx4 v[236:239], v252, s[8:11], s20 offen offset:1024 sc1\n\t"
"v_mfma_f32_16x16x32_bf16 v[60:63], a[220:223], a[200:203], v[60:63]\n\t"
"s_waitcnt vmcnt(11)\n\t"
"v_mfma_f32_16x16x32_bf16 v[60:63], v[96:99], a[204:207], v[60:63]\n\t"
"v_mfma_f32_16x16x32_bf16 v[64:67], a[224:227], a[168:171], v[64:67]\n\t"
"buffer_load_dwordx4 v[96:99], v252, s[8:11], s20 offen offset:2048 sc1\n\t"
"v_mfma_f32_16x16x32_bf16 v[64:67], a[228:231], a[172:175], v[64:67]\n\t"
"s_mov_b32 s20, 933888\n\t"
"ds_read_b128 a[168:171], v244 offset:9216\n\t"
"v_cvt_pk_bf16_f32 v232, v232, v233\n\t"
"v_mfma_f32_16x16x32_bf16 v[64:67], a[208:211], a[176:179], v[64:67]\n\t"
"s_waitcnt lgkmcnt(1)\n\t"
"v_cvt_pk_bf16_f32 v233, v234, v235\n\t"
"v_mfma_f32_16x16x32_bf16 v[68:71], a[224:227], a[172:175], v[68:71]\n\t"
"buffer_load_dwordx4 a[220:223], v252, s[8:11], s20 offen offset:0 sc1\n\t"
"v_mfma_f32_16x16x32_bf16 v[68:71], a[228:231], a[176:179], v[68:71]\n\t"
"ds_read_b128 a[172:175], v244 offset:9728\n\t"
"v_mfma_f32_16x16x32_bf16 v[68:71], a[208:211], a[180:183], v[68:71]\n\t"
"Lpg6_%=:\n\t"
"s_cbranch_vccnz Lpg10_%=\n\t"
"v_mfma_f32_16x16x32_bf16 v[224:227], v[224:227], v[220:223], 0\n\t"
"v_mfma_f32_16x16x32_bf16 v[72:75], a[224:227], a[176:179], v[72:75]\n\t"
"ds_read_b128 a[176:179], v244 offset:10240\n\t"
"v_mfma_f32_16x16x32_bf16 v[72:75], a[228:231], a[180:183], v[72:75]\n\t"
"v_mfma_f32_16x16x32_bf16 v[72:75], a[208:211], a[184:187], v[72:75]\n\t"
"v_mfma_f32_16x16x32_bf16 v[76:79], a[224:227], a[180:183], v[76:79]\n\t"
"ds_read_b128 a[180:183], v244 offset:10752\n\t"
"v_mfma_f32_16x16x32_bf16 v[76:79], a[228:231], a[184:187], v[76:79]\n\t"
"v_cvt_pk_bf16_f32 v234, v224, v225\n\t"
"v_cvt_pk_bf16_f32 v235, v226, v227\n\t"
"v_mfma_f32_16x16x32_bf16 v[76:79], a[208:211], a[188:191], v[76:79]\n\t"
"ds_read_b64_tr_b16 v[224:225], v246 offset:27648\n\t"
"v_mfma_f32_16x16x32_bf16 v[80:83], a[224:227], a[184:187], v[80:83]\n\t"
"ds_read_b64_tr_b16 v[226:227], v246 offset:30720\n\t"
"v_mfma_f32_16x16x32_bf16 v[80:83], a[228:231], a[188:191], v[80:83]\n\t"
"ds_read_b128 a[184:187], v244 offset:11264\n\t"
"v_mfma_f32_16x16x32_bf16 v[80:83], a[208:211], a[192:195], v[80:83]\n\t"
"v_mfma_f32_16x16x32_bf16 v[84:87], a[224:227], a[188:191], v[84:87]\n\t"
"ds_read_b128 a[188:191], v244 offset:11776\n\t"
"v_mfma_f32_16x16x32_bf16 v[84:87], a[228:231], a[192:195], v[84:87]\n\t"
"v_mfma_f32_16x16x32_bf16 v[84:87], a[208:211], a[196:199], v[84:87]\n\t"
"v_mfma_f32_16x16x32_bf16 v[88:91], a[224:227], a[192:195], v[88:91]\n\t"
"ds_read_b128 a[192:195], v244 offset:12288\n\t"
"v_mfma_f32_16x16x32_bf16 v[88:91], a[228:231], a[196:199], v[88:91]\n\t"
"v_mfma_f32_16x16x32_bf16 v[88:91], a[208:211], a[200:203], v[88:91]\n\t"
"v_mfma_f32_16x16x32_bf16 v[92:95], a[224:227], a[196:199], v[92:95]\n\t"
"buffer_load_dwordx4 a[224:227], v252, s[8:11], s20 offen offset:1024 sc1\n\t"
"v_mfma_f32_16x16x32_bf16 v[92:95], a[228:231], a[200:203], v[92:95]\n\t"
"s_waitcnt vmcnt(11) lgkmcnt(3)\n\t"
"ds_read_b128 a[196:199], v244 offset:12800\n\t"
"v_mfma_f32_16x16x32_bf16 v[92:95], a[208:211], a[204:207], v[92:95]\n\t"
"ds_read_b128 a[200:203], v244 offset:13312\n\t"
"v_mfma_f32_16x16x32_bf16 v[224:227], v[224:227], v[220:223], 0\n\t"
"ds_read_b128 a[204:207], v244 offset:13824\n\t"
"v_mfma_f32_16x16x32_bf16 v[100:103], v[248:251], a[168:171], v[100:103]\n\t"
"buffer_load_dwordx4 a[208:211], v252, s[8:11], s20 offen offset:2048 sc1\n\t"
"v_mfma_f32_16x16x32_bf16 v[100:103], a[232:235], a[172:175], v[100:103]\n\t"
"s_mov_b32 s20, 946176\n\t"
"v_mfma_f32_16x16x32_bf16 v[100:103], a[236:239], a[176:179], v[100:103]\n\t"
"buffer_load_dwordx4 a[228:231], v252, s[8:11], s20 offen offset:0 sc1\n\t"
"v_mfma_f32_16x16x32_bf16 v[104:107], v[248:251], a[172:175], v[104:107]\n\t"
"s_waitcnt lgkmcnt(5)\n\t"
"v_mfma_f32_16x16x32_bf16 v[104:107], a[232:235], a[176:179], v[104:107]\n\t"
"v_cvt_pk_bf16_f32 v224, v224, v225\n\t"
"v_mfma_f32_16x16x32_bf16 v[104:107], a[236:239], a[180:183], v[104:107]\n\t"
"s_waitcnt lgkmcnt(0)\n\t"
"s_barrier\n\t"
"v_mfma_f32_16x16x32_bf16 v[108:111], v[248:251], a[176:179], v[108:111]\n\t"
"v_cvt_pk_bf16_f32 v225, v226, v227\n\t"
"v_mfma_f32_16x16x32_bf16 v[108:111], a[232:235], a[180:183], v[108:111]\n\t"
"v_mfma_f32_16x16x32_bf16 v[108:111], a[236:239], a[184:187], v[108:111]\n\t"
"ds_write2st64_b64 v245, v[228:229], v[230:231] offset0:0 offset1:1\n\t"
"v_mfma_f32_16x16x32_bf16 v[112:115], v[248:251], a[180:183], v[112:115]\n\t"
"v_mfma_f32_16x16x32_bf16 v[112:115], a[232:235], a[184:187], v[112:115]\n\t"
"ds_read_b64_tr_b16 v[228:229], v246 offset:33792\n\t"
"v_mfma_f32_16x16x32_bf16 v[112:115], a[236:239], a[188:191], v[112:115]\n\t"
"ds_write2st64_b64 v245, v[232:233], v[234:235] offset0:2 offset1:3\n\t"
"v_mfma_f32_16x16x32_bf16 v[116:119], v[248:251], a[184:187], v[116:119]\n\t"
"v_mfma_f32_16x16x32_bf16 v[116:119], a[232:235], a[188:191], v[116:119]\n\t"
"ds_read_b64_tr_b16 v[230:231], v246 offset:36864\n\t"
"v_mfma_f32_16x16x32_bf16 v[116:119], a[236:239], a[192:195], v[116:119]\n\t"
"s_add_u32 m0, s24, 66304\n\t"
"s_add_u32 s28, s21, 1966080\n\t"
"ds_write_b64 v245, v[224:225] offset:2048\n\t"
"v_mfma_f32_16x16x32_bf16 v[120:123], v[248:251], a[188:191], v[120:123]\n\t"
"ds_read_b64_tr_b16 v[232:233], v246 offset:41472\n\t"
"v_mfma_f32_16x16x32_bf16 v[120:123], a[232:235], a[192:195], v[120:123]\n\t"
"ds_read_b64_tr_b16 v[234:235], v246 offset:44544\n\t"
"v_mfma_f32_16x16x32_bf16 v[120:123], a[236:239], a[196:199], v[120:123]\n\t"
"s_waitcnt lgkmcnt(3)\n\t"
"ds_read_b64_tr_b16 v[224:225], v246 offset:47616\n\t"
"v_mfma_f32_16x16x32_bf16 v[228:231], v[228:231], v[220:223], 0\n\t"
"ds_read_b64_tr_b16 v[226:227], v246 offset:50688\n\t"
"v_mfma_f32_16x16x32_bf16 v[124:127], v[248:251], a[192:195], v[124:127]\n\t"
"buffer_load_dwordx4 v247, s[12:15], s28 offen offset:256 nt lds\n\t"
"v_mfma_f32_16x16x32_bf16 v[124:127], a[232:235], a[196:199], v[124:127]\n\t"
"s_add_u32 m0, s24, 72448\n\t"
"s_add_u32 s28, s21, 2162688\n\t"
"v_mfma_f32_16x16x32_bf16 v[124:127], a[236:239], a[200:203], v[124:127]\n\t"
"s_waitcnt lgkmcnt(2)\n\t"
"buffer_load_dwordx4 v247, s[12:15], s28 offen offset:256 nt lds\n\t"
"s_add_u32 m0, s24, 80128\n\t"
"s_add_u32 s28, s21, 2359296\n\t"
"v_mfma_f32_16x16x32_bf16 v[232:235], v[232:235], v[220:223], 0\n\t"
"buffer_load_dwordx4 v247, s[12:15], s28 offen offset:256 nt lds\n\t"
"v_mfma_f32_16x16x32_bf16 v[128:131], v[248:251], a[196:199], v[128:131]\n\t"
"s_waitcnt lgkmcnt(0)\n\t"
"s_add_u32 m0, s24, 86272\n\t"
"s_add_u32 s28, s21, 2555904\n\t"
"v_mfma_f32_16x16x32_bf16 v[128:131], a[232:235], a[200:203], v[128:131]\n\t"
"buffer_load_dwordx4 v247, s[12:15], s28 offen offset:256 nt lds\n\t"
"v_mfma_f32_16x16x32_bf16 v[128:131], a[236:239], a[204:207], v[128:131]\n\t"
"s_waitcnt vmcnt(14)\n\t"
"s_add_u32 m0, s24, 32512\n\t"
"s_add_u32 s28, s21, 983040\n\t"
"v_mfma_f32_16x16x32_bf16 v[224:227], v[224:227], v[220:223], 0\n\t"
"buffer_load_dwordx4 v247, s[12:15], s28 offen offset:256 nt lds\n\t"
"v_cvt_pk_bf16_f32 v228, v228, v229\n\t"
"v_cvt_pk_bf16_f32 v229, v230, v231\n\t"
"v_mfma_f32_16x16x32_bf16 v[132:135], a[240:243], a[168:171], v[132:135]\n\t"
"s_add_u32 m0, s24, 40192\n\t"
"s_add_u32 s28, s21, 1179648\n\t"
"v_cvt_pk_bf16_f32 v230, v232, v233\n\t"
"v_mfma_f32_16x16x32_bf16 v[132:135], a[244:247], a[172:175], v[132:135]\n\t"
"v_cvt_pk_bf16_f32 v231, v234, v235\n\t"
"v_mfma_f32_16x16x32_bf16 v[132:135], v[240:243], a[176:179], v[132:135]\n\t"
"buffer_load_dwordx4 v247, s[12:15], s28 offen offset:256 nt lds\n\t"
"v_mfma_f32_16x16x32_bf16 v[136:139], a[240:243], a[172:175], v[136:139]\n\t"
"ds_read_b64_tr_b16 v[232:233], v246 offset:53760\n\t"
"v_mfma_f32_16x16x32_bf16 v[136:139], a[244:247], a[176:179], v[136:139]\n\t"
"ds_read_b64_tr_b16 v[234:235], v246 offset:56832\n\t"
"v_cvt_pk_bf16_f32 v224, v224, v225\n\t"
"v_mfma_f32_16x16x32_bf16 v[136:139], v[240:243], a[180:183], v[136:139]\n\t"
"ds_write2st64_b64 v245, v[228:229], v[230:231] offset0:5 offset1:6\n\t"
"v_mfma_f32_16x16x32_bf16 v[140:143], a[240:243], a[176:179], v[140:143]\n\t"
"v_cvt_pk_bf16_f32 v225, v226, v227\n\t"
"v_mfma_f32_16x16x32_bf16 v[140:143], a[244:247], a[180:183], v[140:143]\n\t"
"v_mfma_f32_16x16x32_bf16 v[140:143], v[240:243], a[184:187], v[140:143]\n\t"
"v_mfma_f32_16x16x32_bf16 v[144:147], a[240:243], a[180:183], v[144:147]\n\t"
"v_mfma_f32_16x16x32_bf16 v[144:147], a[244:247], a[184:187], v[144:147]\n\t"
"v_mfma_f32_16x16x32_bf16 v[144:147], v[240:243], a[188:191], v[144:147]\n\t"
"v_mfma_f32_16x16x32_bf16 v[148:151], a[240:243], a[184:187], v[148:151]\n\t"
"s_waitcnt lgkmcnt(1)\n\t"
"v_mfma_f32_16x16x32_bf16 v[148:151], a[244:247], a[188:191], v[148:151]\n\t"
"v_mfma_f32_16x16x32_bf16 v[148:151], v[240:243], a[192:195], v[148:151]\n\t"
"v_mfma_f32_16x16x32_bf16 v[232:235], v[232:235], v[220:223], 0\n\t"
"v_mfma_f32_16x16x32_bf16 v[152:155], a[240:243], a[188:191], v[152:155]\n\t"
"v_mfma_f32_16x16x32_bf16 v[152:155], a[244:247], a[192:195], v[152:155]\n\t"
"v_mfma_f32_16x16x32_bf16 v[152:155], v[240:243], a[196:199], v[152:155]\n\t"
"v_mfma_f32_16x16x32_bf16 v[156:159], a[240:243], a[192:195], v[156:159]\n\t"
"v_mfma_f32_16x16x32_bf16 v[156:159], a[244:247], a[196:199], v[156:159]\n\t"
"v_cvt_pk_bf16_f32 v226, v232, v233\n\t"
"v_cvt_pk_bf16_f32 v227, v234, v235\n\t"
"v_mfma_f32_16x16x32_bf16 v[156:159], v[240:243], a[200:203], v[156:159]\n\t"
"ds_write2st64_b64 v245, v[224:225], v[226:227] offset0:7 offset1:8\n\t"
"v_mfma_f32_16x16x32_bf16 v[160:163], a[240:243], a[196:199], v[160:163]\n\t"
"s_waitcnt vmcnt(13)\n\t"
"v_mfma_f32_16x16x32_bf16 v[160:163], a[244:247], a[200:203], v[160:163]\n\t"
"v_mfma_f32_16x16x32_bf16 v[160:163], v[240:243], a[204:207], v[160:163]\n\t"
"v_mfma_f32_16x16x32_bf16 v[164:167], a[212:215], a[168:171], v[164:167]\n\t"
"v_mfma_f32_16x16x32_bf16 v[164:167], a[216:219], a[172:175], v[164:167]\n\t"
"v_mfma_f32_16x16x32_bf16 v[164:167], a[248:251], a[176:179], v[164:167]\n\t"
"s_waitcnt lgkmcnt(0)\n\t"
"s_barrier\n\t"
"v_mfma_f32_16x16x32_bf16 v[168:171], a[212:215], a[172:175], v[168:171]\n\t"
"ds_read_b128 a[168:171], v244 offset:0\n\t"
"v_mfma_f32_16x16x32_bf16 v[168:171], a[216:219], a[176:179], v[168:171]\n\t"
"buffer_load_dwordx4 v[240:243], v252, s[8:11], s20 offen offset:1024 sc1\n\t"
"v_mfma_f32_16x16x32_bf16 v[168:171], a[248:251], a[180:183], v[168:171]\n\t"
"ds_read_b128 a[172:175], v244 offset:512\n\t"
"v_mfma_f32_16x16x32_bf16 v[172:175], a[212:215], a[176:179], v[172:175]\n\t"
"ds_read_b128 a[176:179], v244 offset:1024\n\t"
"v_mfma_f32_16x16x32_bf16 v[172:175], a[216:219], a[180:183], v[172:175]\n\t"
"v_mfma_f32_16x16x32_bf16 v[172:175], a[248:251], a[184:187], v[172:175]\n\t"
"v_mfma_f32_16x16x32_bf16 v[176:179], a[212:215], a[180:183], v[176:179]\n\t"
"ds_read_b128 a[180:183], v244 offset:1536\n\t"
"v_mfma_f32_16x16x32_bf16 v[176:179], a[216:219], a[184:187], v[176:179]\n\t"
"v_mfma_f32_16x16x32_bf16 v[176:179], a[248:251], a[188:191], v[176:179]\n\t"
"v_mfma_f32_16x16x32_bf16 v[180:183], a[212:215], a[184:187], v[180:183]\n\t"
"ds_read_b128 a[184:187], v244 offset:2048\n\t"
"v_mfma_f32_16x16x32_bf16 v[180:183], a[216:219], a[188:191], v[180:183]\n\t"
"v_mfma_f32_16x16x32_bf16 v[180:183], a[248:251], a[192:195], v[180:183]\n\t"
"v_mfma_f32_16x16x32_bf16 v[184:187], a[212:215], a[188:191], v[184:187]\n\t"
"ds_read_b128 a[188:191], v244 offset:2560\n\t"
"v_mfma_f32_16x16x32_bf16 v[184:187], a[216:219], a[192:195], v[184:187]\n\t"
"v_mfma_f32_16x16x32_bf16 v[184:187], a[248:251], a[196:199], v[184:187]\n\t"
"v_mfma_f32_16x16x32_bf16 v[188:191], a[212:215], a[192:195], v[188:191]\n\t"
"ds_read_b128 a[192:195], v244 offset:3072\n\t"
"v_mfma_f32_16x16x32_bf16 v[188:191], a[216:219], a[196:199], v[188:191]\n\t"
"v_mfma_f32_16x16x32_bf16 v[188:191], a[248:251], a[200:203], v[188:191]\n\t"
"v_mfma_f32_16x16x32_bf16 v[192:195], a[212:215], a[196:199], v[192:195]\n\t"
"s_waitcnt vmcnt(11) lgkmcnt(4)\n\t"
"ds_read_b128 a[196:199], v244 offset:3584\n\t"
"v_mfma_f32_16x16x32_bf16 v[192:195], a[216:219], a[200:203], v[192:195]\n\t"
"ds_read_b128 a[200:203], v244 offset:4096\n\t"
"v_mfma_f32_16x16x32_bf16 v[192:195], a[248:251], a[204:207], v[192:195]\n\t"
"ds_read_b128 a[204:207], v244 offset:4608\n\t"
"Lst8_%=:\n\t"
"v_mfma_f32_16x16x32_bf16 v[0:3], a[252:255], a[168:171], v[0:3]\n\t"
"s_mov_b64 vcc, -1\n\t"
"s_bitcmp1_b32 s73, 1\n\t"
"s_cbranch_scc1 Lvh1_%=\n\t"
"s_bitcmp1_b32 s73, 0\n\t"
"s_cbranch_scc1 Lpg14_%=\n\t"
"s_branch Lpg13_%=\n\t"
"Lvh1_%=:\n\t"
"s_bitcmp1_b32 s73, 0\n\t"
"s_cbranch_scc1 Lpg16_%=\n\t"
"s_branch Lpg15_%=\n\t"
"Lvr1_%=:\n\t"
"s_mov_b64 vcc, 0\n\t"
"s_waitcnt lgkmcnt(6)\n\t"
"v_xor_b32 v246, 0xc0, v246\n\t"
"v_mfma_f32_16x16x32_bf16 v[0:3], v[236:239], a[172:175], v[0:3]\n\t"
"v_mfma_f32_16x16x32_bf16 v[0:3], v[96:99], a[176:179], v[0:3]\n\t"
"v_mfma_f32_16x16x32_bf16 v[4:7], a[252:255], a[172:175], v[4:7]\n\t"
"s_waitcnt lgkmcnt(5)\n\t"
"v_mfma_f32_16x16x32_bf16 v[4:7], v[236:239], a[176:179], v[4:7]\n\t"
"v_mfma_f32_16x16x32_bf16 v[4:7], v[96:99], a[180:183], v[4:7]\n\t"
"v_mfma_f32_16x16x32_bf16 v[8:11], a[252:255], a[176:179], v[8:11]\n\t"
"s_waitcnt lgkmcnt(4)\n\t"
"v_mfma_f32_16x16x32_bf16 v[8:11], v[236:239], a[180:183], v[8:11]\n\t"
"v_mfma_f32_16x16x32_bf16 v[8:11], v[96:99], a[184:187], v[8:11]\n\t"
"v_mfma_f32_16x16x32_bf16 v[12:15], a[252:255], a[180:183], v[12:15]\n\t"
"s_waitcnt lgkmcnt(3)\n\t"
"v_mfma_f32_16x16x32_bf16 v[12:15], v[236:239], a[184:187], v[12:15]\n\t"
"v_mfma_f32_16x16x32_bf16 v[12:15], v[96:99], a[188:191], v[12:15]\n\t"
"buffer_load_dwordx4 v[248:251], v252, s[8:11], s20 offen offset:2048 sc1\n\t"
"v_mfma_f32_16x16x32_bf16 v[16:19], a[252:255], a[184:187], v[16:19]\n\t"
"s_waitcnt lgkmcnt(2)\n\t"
"s_mov_b32 s20, 958464\n\t"
"v_mfma_f32_16x16x32_bf16 v[16:19], v[236:239], a[188:191], v[16:19]\n\t"
"v_mfma_f32_16x16x32_bf16 v[16:19], v[96:99], a[192:195], v[16:19]\n\t"
"ds_read_b64_tr_b16 v[228:229], v246 offset:1536\n\t"
"v_mfma_f32_16x16x32_bf16 v[20:23], a[252:255], a[188:191], v[20:23]\n\t"
"buffer_load_dwordx4 a[212:215], v252, s[8:11], s20 offen offset:0 sc1\n\t"
"v_mfma_f32_16x16x32_bf16 v[20:23], v[236:239], a[192:195], v[20:23]\n\t"
"s_waitcnt lgkmcnt(2)\n\t"
"ds_read_b64_tr_b16 v[230:231], v246 offset:4608\n\t"
"v_mfma_f32_16x16x32_bf16 v[20:23], v[96:99], a[196:199], v[20:23]\n\t"
"v_mfma_f32_16x16x32_bf16 v[24:27], a[252:255], a[192:195], v[24:27]\n\t"
"buffer_load_dwordx4 a[216:219], v252, s[8:11], s20 offen offset:1024 sc1\n\t"
"v_mfma_f32_16x16x32_bf16 v[24:27], v[236:239], a[196:199], v[24:27]\n\t"
"s_waitcnt lgkmcnt(2)\n\t"
"v_mfma_f32_16x16x32_bf16 v[24:27], v[96:99], a[200:203], v[24:27]\n\t"
"v_mfma_f32_16x16x32_bf16 v[28:31], a[252:255], a[196:199], v[28:31]\n\t"
"s_waitcnt vmcnt(11)\n\t"
"ds_read_b64_tr_b16 v[232:233], v246 offset:7680\n\t"
"v_mfma_f32_16x16x32_bf16 v[28:31], v[236:239], a[200:203], v[28:31]\n\t"
"buffer_load_dwordx4 v[236:239], v252, s[8:11], s20 offen offset:2048 sc1\n\t"
"v_mfma_f32_16x16x32_bf16 v[28:31], v[96:99], a[204:207], v[28:31]\n\t"
"s_mov_b32 s20, 970752\n\t"
"ds_read_b64_tr_b16 v[234:235], v246 offset:10752\n\t"
"v_mfma_f32_16x16x32_bf16 v[32:35], a[220:223], a[168:171], v[32:35]\n\t"
"v_mfma_f32_16x16x32_bf16 v[32:35], a[224:227], a[172:175], v[32:35]\n\t"
"v_mfma_f32_16x16x32_bf16 v[32:35], a[208:211], a[176:179], v[32:35]\n\t"
"buffer_load_dwordx4 v[96:99], v252, s[8:11], s20 offen offset:0 sc1\n\t"
"v_mfma_f32_16x16x32_bf16 v[36:39], a[220:223], a[172:175], v[36:39]\n\t"
"s_waitcnt lgkmcnt(2)\n\t"
"v_mfma_f32_16x16x32_bf16 v[36:39], a[224:227], a[176:179], v[36:39]\n\t"
"v_mfma_f32_16x16x32_bf16 v[36:39], a[208:211], a[180:183], v[36:39]\n\t"
"v_mfma_f32_16x16x32_bf16 v[228:231], v[228:231], v[220:223], 0\n\t"
"buffer_load_dwordx4 a[232:235], v252, s[8:11], s20 offen offset:1024 sc1\n\t"
"v_mfma_f32_16x16x32_bf16 v[40:43], a[220:223], a[176:179], v[40:43]\n\t"
"ds_read_b64_tr_b16 v[224:225], v246 offset:13824\n\t"
"v_mfma_f32_16x16x32_bf16 v[40:43], a[224:227], a[180:183], v[40:43]\n\t"
"ds_read_b64_tr_b16 v[226:227], v246 offset:16896\n\t"
"v_mfma_f32_16x16x32_bf16 v[40:43], a[208:211], a[184:187], v[40:43]\n\t"
"v_mfma_f32_16x16x32_bf16 v[44:47], a[220:223], a[180:183], v[44:47]\n\t"
"buffer_load_dwordx4 a[236:239], v252, s[8:11], s20 offen offset:2048 sc1\n\t"
"v_mfma_f32_16x16x32_bf16 v[44:47], a[224:227], a[184:187], v[44:47]\n\t"
"s_waitcnt lgkmcnt(2)\n\t"
"v_cvt_pk_bf16_f32 v228, v228, v229\n\t"
"v_cvt_pk_bf16_f32 v229, v230, v231\n\t"
"v_mfma_f32_16x16x32_bf16 v[44:47], a[208:211], a[188:191], v[44:47]\n\t"
"s_mov_b32 s20, 983040\n\t"
"v_mfma_f32_16x16x32_bf16 v[232:235], v[232:235], v[220:223], 0\n\t"
"v_mfma_f32_16x16x32_bf16 v[48:51], a[220:223], a[184:187], v[48:51]\n\t"
"buffer_load_dwordx4 a[240:243], v252, s[8:11], s20 offen offset:0 sc1\n\t"
"v_mfma_f32_16x16x32_bf16 v[48:51], a[224:227], a[188:191], v[48:51]\n\t"
"v_mfma_f32_16x16x32_bf16 v[48:51], a[208:211], a[192:195], v[48:51]\n\t"
"v_mfma_f32_16x16x32_bf16 v[52:55], a[220:223], a[188:191], v[52:55]\n\t"
"v_mfma_f32_16x16x32_bf16 v[52:55], a[224:227], a[192:195], v[52:55]\n\t"
"buffer_load_dwordx4 a[244:247], v252, s[8:11], s20 offen offset:1024 sc1\n\t"
"v_mfma_f32_16x16x32_bf16 v[52:55], a[208:211], a[196:199], v[52:55]\n\t"
"v_cvt_pk_bf16_f32 v230, v232, v233\n\t"
"v_cvt_pk_bf16_f32 v231, v234, v235\n\t"
"v_mfma_f32_16x16x32_bf16 v[56:59], a[220:223], a[192:195], v[56:59]\n\t"
"s_waitcnt lgkmcnt(0)\n\t"
"ds_read_b64_tr_b16 v[232:233], v246 offset:21504\n\t"
"v_mfma_f32_16x16x32_bf16 v[56:59], a[224:227], a[196:199], v[56:59]\n\t"
"ds_read_b64_tr_b16 v[234:235], v246 offset:24576\n\t"
"v_mfma_f32_16x16x32_bf16 v[56:59], a[208:211], a[200:203], v[56:59]\n\t"
"buffer_load_dwordx4 a[248:251], v252, s[8:11], s20 offen offset:2048 sc1\n\t"
"v_mfma_f32_16x16x32_bf16 v[224:227], v[224:227], v[220:223], 0\n\t"
"s_mov_b32 s20, 995328\n\t"
"v_mfma_f32_16x16x32_bf16 v[60:63], a[220:223], a[196:199], v[60:63]\n\t"
"s_waitcnt vmcnt(9)\n\t"
"v_mfma_f32_16x16x32_bf16 v[60:63], a[224:227], a[200:203], v[60:63]\n\t"
"v_mfma_f32_16x16x32_bf16 v[60:63], a[208:211], a[204:207], v[60:63]\n\t"
"buffer_load_dwordx4 a[220:223], v252, s[8:11], s20 offen offset:0 sc1\n\t"
"v_mfma_f32_16x16x32_bf16 v[64:67], a[228:231], a[168:171], v[64:67]\n\t"
"ds_read_b128 a[168:171], v244 offset:9216\n\t"
"v_mfma_f32_16x16x32_bf16 v[64:67], v[240:243], a[172:175], v[64:67]\n\t"
"v_cvt_pk_bf16_f32 v224, v224, v225\n\t"
"v_cvt_pk_bf16_f32 v225, v226, v227\n\t"
"v_mfma_f32_16x16x32_bf16 v[64:67], v[248:251], a[176:179], v[64:67]\n\t"
"v_mfma_f32_16x16x32_bf16 v[68:71], a[228:231], a[172:175], v[68:71]\n\t"
"buffer_load_dwordx4 a[224:227], v252, s[8:11], s20 offen offset:1024 sc1\n\t"
"v_mfma_f32_16x16x32_bf16 v[68:71], v[240:243], a[176:179], v[68:71]\n\t"
"s_waitcnt lgkmcnt(1)\n\t"
"ds_read_b128 a[172:175], v244 offset:9728\n\t"
"v_mfma_f32_16x16x32_bf16 v[68:71], v[248:251], a[180:183], v[68:71]\n\t"
"buffer_load_dwordx4 a[252:255], v252, s[8:11], s20 offen offset:2048 sc1\n\t"
"v_mfma_f32_16x16x32_bf16 v[232:235], v[232:235], v[220:223], 0\n\t"
"v_mfma_f32_16x16x32_bf16 v[72:75], a[228:231], a[176:179], v[72:75]\n\t"
"ds_read_b128 a[176:179], v244 offset:10240\n\t"
"v_mfma_f32_16x16x32_bf16 v[72:75], v[240:243], a[180:183], v[72:75]\n\t"
"v_mfma_f32_16x16x32_bf16 v[72:75], v[248:251], a[184:187], v[72:75]\n\t"
"v_mfma_f32_16x16x32_bf16 v[76:79], a[228:231], a[180:183], v[76:79]\n\t"
"ds_read_b128 a[180:183], v244 offset:10752\n\t"
"v_mfma_f32_16x16x32_bf16 v[76:79], v[240:243], a[184:187], v[76:79]\n\t"
"v_cvt_pk_bf16_f32 v226, v232, v233\n\t"
"v_cvt_pk_bf16_f32 v227, v234, v235\n\t"
"v_mfma_f32_16x16x32_bf16 v[76:79], v[248:251], a[188:191], v[76:79]\n\t"
"ds_read_b64_tr_b16 v[232:233], v246 offset:27648\n\t"
"v_mfma_f32_16x16x32_bf16 v[80:83], a[228:231], a[184:187], v[80:83]\n\t"
"ds_read_b64_tr_b16 v[234:235], v246 offset:30720\n\t"
"v_mfma_f32_16x16x32_bf16 v[80:83], v[240:243], a[188:191], v[80:83]\n\t"
"ds_read_b128 a[184:187], v244 offset:11264\n\t"
"v_mfma_f32_16x16x32_bf16 v[80:83], v[248:251], a[192:195], v[80:83]\n\t"
"v_mfma_f32_16x16x32_bf16 v[84:87], a[228:231], a[188:191], v[84:87]\n\t"
"s_mov_b32 s20, 1007616\n\t"
"ds_read_b128 a[188:191], v244 offset:11776\n\t"
"v_mfma_f32_16x16x32_bf16 v[84:87], v[240:243], a[192:195], v[84:87]\n\t"
"v_mfma_f32_16x16x32_bf16 v[84:87], v[248:251], a[196:199], v[84:87]\n\t"
"v_mfma_f32_16x16x32_bf16 v[88:91], a[228:231], a[192:195], v[88:91]\n\t"
"ds_read_b128 a[192:195], v244 offset:12288\n\t"
"v_mfma_f32_16x16x32_bf16 v[88:91], v[240:243], a[196:199], v[88:91]\n\t"
"v_mfma_f32_16x16x32_bf16 v[88:91], v[248:251], a[200:203], v[88:91]\n\t"
"v_mfma_f32_16x16x32_bf16 v[92:95], a[228:231], a[196:199], v[92:95]\n\t"
"buffer_load_dwordx4 a[228:231], v252, s[8:11], s20 offen offset:0 sc1\n\t"
"v_mfma_f32_16x16x32_bf16 v[92:95], v[240:243], a[200:203], v[92:95]\n\t"
"s_waitcnt vmcnt(10) lgkmcnt(3)\n\t"
"ds_read_b128 a[196:199], v244 offset:12800\n\t"
"v_mfma_f32_16x16x32_bf16 v[92:95], v[248:251], a[204:207], v[92:95]\n\t"
"ds_read_b128 a[200:203], v244 offset:13312\n\t"
"v_mfma_f32_16x16x32_bf16 v[232:235], v[232:235], v[220:223], 0\n\t"
"ds_read_b128 a[204:207], v244 offset:13824\n\t"
"v_mfma_f32_16x16x32_bf16 v[100:103], a[212:215], a[168:171], v[100:103]\n\t"
"buffer_load_dwordx4 v[248:251], v252, s[8:11], s20 offen offset:1024 sc1\n\t"
"v_mfma_f32_16x16x32_bf16 v[100:103], a[216:219], a[172:175], v[100:103]\n\t"
"v_mfma_f32_16x16x32_bf16 v[100:103], v[236:239], a[176:179], v[100:103]\n\t"
"v_mfma_f32_16x16x32_bf16 v[104:107], a[212:215], a[172:175], v[104:107]\n\t"
"s_waitcnt lgkmcnt(5)\n\t"
"v_mfma_f32_16x16x32_bf16 v[104:107], a[216:219], a[176:179], v[104:107]\n\t"
"v_cvt_pk_bf16_f32 v232, v232, v233\n\t"
"v_mfma_f32_16x16x32_bf16 v[104:107], v[236:239], a[180:183], v[104:107]\n\t"
"s_waitcnt lgkmcnt(0)\n\t"
"s_barrier\n\t"
"v_mfma_f32_16x16x32_bf16 v[108:111], a[212:215], a[176:179], v[108:111]\n\t"
"v_cvt_pk_bf16_f32 v233, v234, v235\n\t"
"v_mfma_f32_16x16x32_bf16 v[108:111], a[216:219], a[180:183], v[108:111]\n\t"
"v_mfma_f32_16x16x32_bf16 v[108:111], v[236:239], a[184:187], v[108:111]\n\t"
"ds_write2st64_b64 v245, v[228:229], v[230:231] offset0:0 offset1:1\n\t"
"v_mfma_f32_16x16x32_bf16 v[112:115], a[212:215], a[180:183], v[112:115]\n\t"
"v_mfma_f32_16x16x32_bf16 v[112:115], a[216:219], a[184:187], v[112:115]\n\t"
"ds_read_b64_tr_b16 v[228:229], v246 offset:33792\n\t"
"v_mfma_f32_16x16x32_bf16 v[112:115], v[236:239], a[188:191], v[112:115]\n\t"
"ds_write2st64_b64 v245, v[224:225], v[226:227] offset0:2 offset1:3\n\t"
"v_mfma_f32_16x16x32_bf16 v[116:119], a[212:215], a[184:187], v[116:119]\n\t"
"global_load_dwordx4 v[240:243], v252, s[48:49] offset:2048\n\t"
"v_mfma_f32_16x16x32_bf16 v[116:119], a[216:219], a[188:191], v[116:119]\n\t"
"ds_read_b64_tr_b16 v[230:231], v246 offset:36864\n\t"
"v_mfma_f32_16x16x32_bf16 v[116:119], v[236:239], a[192:195], v[116:119]\n\t"
"ds_write_b64 v245, v[232:233] offset:2048\n\t"
"v_mfma_f32_16x16x32_bf16 v[120:123], a[212:215], a[188:191], v[120:123]\n\t"
"ds_read_b64_tr_b16 v[224:225], v246 offset:41472\n\t"
"v_mfma_f32_16x16x32_bf16 v[120:123], a[216:219], a[192:195], v[120:123]\n\t"
"ds_read_b64_tr_b16 v[226:227], v246 offset:44544\n\t"
"v_mfma_f32_16x16x32_bf16 v[120:123], v[236:239], a[196:199], v[120:123]\n\t"
"s_waitcnt lgkmcnt(3)\n\t"
"ds_read_b64_tr_b16 v[232:233], v246 offset:47616\n\t"
"v_mfma_f32_16x16x32_bf16 v[228:231], v[228:231], v[220:223], 0\n\t"
"ds_read_b64_tr_b16 v[234:235], v246 offset:50688\n\t"
"v_mfma_f32_16x16x32_bf16 v[124:127], a[212:215], a[192:195], v[124:127]\n\t"
"global_load_dwordx4 a[208:211], v252, s[46:47] offset:1024\n\t"
"v_mfma_f32_16x16x32_bf16 v[124:127], a[216:219], a[196:199], v[124:127]\n\t"
"v_mfma_f32_16x16x32_bf16 v[124:127], v[236:239], a[200:203], v[124:127]\n\t"
"s_waitcnt lgkmcnt(2)\n\t"
"v_mfma_f32_16x16x32_bf16 v[224:227], v[224:227], v[220:223], 0\n\t"
"v_mfma_f32_16x16x32_bf16 v[128:131], a[212:215], a[196:199], v[128:131]\n\t"
"buffer_load_dwordx4 a[212:215], v252, s[8:11], s20 offen offset:2048 sc1\n\t"
"v_mfma_f32_16x16x32_bf16 v[128:131], a[216:219], a[200:203], v[128:131]\n\t"
"s_waitcnt vmcnt(11) lgkmcnt(0)\n\t"
"s_mov_b32 s20, 1019904\n\t"
"v_cvt_pk_bf16_f32 v228, v228, v229\n\t"
"v_mfma_f32_16x16x32_bf16 v[128:131], v[236:239], a[204:207], v[128:131]\n\t"
"v_cvt_pk_bf16_f32 v229, v230, v231\n\t"
"v_mfma_f32_16x16x32_bf16 v[232:235], v[232:235], v[220:223], 0\n\t"
"buffer_load_dwordx4 a[216:219], v252, s[8:11], s20 offen offset:0 sc1\n\t"
"v_mfma_f32_16x16x32_bf16 v[132:135], v[96:99], a[168:171], v[132:135]\n\t"
"s_add_u32 m0, s24, 46336\n\t"
"s_add_u32 s28, s21, 1376256\n\t"
"v_cvt_pk_bf16_f32 v230, v224, v225\n\t"
"v_mfma_f32_16x16x32_bf16 v[132:135], a[232:235], a[172:175], v[132:135]\n\t"
"v_cvt_pk_bf16_f32 v231, v226, v227\n\t"
"v_mfma_f32_16x16x32_bf16 v[132:135], a[236:239], a[176:179], v[132:135]\n\t"
"ds_write2st64_b64 v245, v[228:229], v[230:231] offset0:5 offset1:6\n\t"
"v_mfma_f32_16x16x32_bf16 v[136:139], v[96:99], a[172:175], v[136:139]\n\t"
"buffer_load_dwordx4 v247, s[12:15], s28 offen offset:256 nt lds\n\t"
"v_mfma_f32_16x16x32_bf16 v[136:139], a[232:235], a[176:179], v[136:139]\n\t"
"s_add_u32 m0, s24, 52480\n\t"
"s_add_u32 s28, s21, 1572864\n\t"
"ds_read_b64_tr_b16 v[224:225], v246 offset:53760\n\t"
"v_mfma_f32_16x16x32_bf16 v[136:139], a[236:239], a[180:183], v[136:139]\n\t"
"buffer_load_dwordx4 v247, s[12:15], s28 offen offset:256 nt lds\n\t"
"v_mfma_f32_16x16x32_bf16 v[140:143], v[96:99], a[176:179], v[140:143]\n\t"
"s_add_u32 m0, s24, 92416\n\t"
"s_add_u32 s28, s21, 2752512\n\t"
"ds_read_b64_tr_b16 v[226:227], v246 offset:56832\n\t"
"v_mfma_f32_16x16x32_bf16 v[140:143], a[232:235], a[180:183], v[140:143]\n\t"
"buffer_load_dwordx4 v247, s[12:15], s28 offen offset:256 nt lds\n\t"
"v_mfma_f32_16x16x32_bf16 v[140:143], a[236:239], a[184:187], v[140:143]\n\t"
"s_add_u32 m0, s24, 100096\n\t"
"s_add_u32 s28, s21, 2949120\n\t"
"v_cvt_pk_bf16_f32 v232, v232, v233\n\t"
"v_cvt_pk_bf16_f32 v233, v234, v235\n\t"
"v_mfma_f32_16x16x32_bf16 v[144:147], v[96:99], a[180:183], v[144:147]\n\t"
"v_mfma_f32_16x16x32_bf16 v[144:147], a[232:235], a[184:187], v[144:147]\n\t"
"buffer_load_dwordx4 v247, s[12:15], s28 offen offset:256 nt lds\n\t"
"v_mfma_f32_16x16x32_bf16 v[144:147], a[236:239], a[188:191], v[144:147]\n\t"
"s_add_u32 m0, s24, 106240\n\t"
"s_add_u32 s28, s21, 3145728\n\t"
"v_mfma_f32_16x16x32_bf16 v[148:151], v[96:99], a[184:187], v[148:151]\n\t"
"buffer_load_dwordx4 v247, s[12:15], s28 offen offset:256 nt lds\n\t"
"v_mfma_f32_16x16x32_bf16 v[148:151], a[232:235], a[188:191], v[148:151]\n\t"
"s_waitcnt lgkmcnt(0)\n\t"
"s_add_u32 m0, s24, 112384\n\t"
"s_add_u32 s28, s21, 3342336\n\t"
"v_mfma_f32_16x16x32_bf16 v[148:151], a[236:239], a[192:195], v[148:151]\n\t"
"buffer_load_dwordx4 v247, s[12:15], s28 offen offset:256 nt lds\n\t"
"s_add_u32 m0, s25, 256\n\t"
"v_mfma_f32_16x16x32_bf16 v[224:227], v[224:227], v[220:223], 0\n\t"
"buffer_load_dwordx4 v254, s[12:15], 0 offen offset:256 nt lds\n\t"
"v_mfma_f32_16x16x32_bf16 v[152:155], v[96:99], a[188:191], v[152:155]\n\t"
"s_add_u32 m0, s26, 256\n\t"
"v_mfma_f32_16x16x32_bf16 v[152:155], a[232:235], a[192:195], v[152:155]\n\t"
"v_mfma_f32_16x16x32_bf16 v[152:155], a[236:239], a[196:199], v[152:155]\n\t"
"s_mov_b64 exec, s[36:37]\n\t"
"buffer_load_dwordx4 v254, s[12:15], s27 offen offset:256 nt lds\n\t"
"s_mov_b64 exec, -1\n\t"
"v_mfma_f32_16x16x32_bf16 v[156:159], v[96:99], a[192:195], v[156:159]\n\t"
"s_waitcnt vmcnt(11)\n\t"
"ds_read_b64_tr_b16 v[236:237], v240 offset:1536\n\t"
"v_mfma_f32_16x16x32_bf16 v[156:159], a[232:235], a[196:199], v[156:159]\n\t"
"ds_read_b64_tr_b16 v[238:239], v240 offset:3072\n\t"
"v_cvt_pk_bf16_f32 v234, v224, v225\n\t"
"v_mfma_f32_16x16x32_bf16 v[156:159], a[236:239], a[200:203], v[156:159]\n\t"
"ds_read_b64_tr_b16 v[228:229], v241 offset:1536\n\t"
"v_cvt_pk_bf16_f32 v235, v226, v227\n\t"
"v_mfma_f32_16x16x32_bf16 v[160:163], v[96:99], a[196:199], v[160:163]\n\t"
"ds_read_b64_tr_b16 v[230:231], v241 offset:3072\n\t"
"v_mfma_f32_16x16x32_bf16 v[160:163], a[232:235], a[200:203], v[160:163]\n\t"
"v_mfma_f32_16x16x32_bf16 v[160:163], a[236:239], a[204:207], v[160:163]\n\t"
"ds_write2st64_b64 v245, v[232:233], v[234:235] offset0:7 offset1:8\n\t"
"v_mfma_f32_16x16x32_bf16 v[164:167], a[240:243], a[168:171], v[164:167]\n\t"
"v_mfma_f32_16x16x32_bf16 v[164:167], a[244:247], a[172:175], v[164:167]\n\t"
"v_mfma_f32_16x16x32_bf16 v[164:167], a[248:251], a[176:179], v[164:167]\n\t"
"s_waitcnt lgkmcnt(0)\n\t"
"s_barrier\n\t"
"v_mfma_f32_16x16x32_bf16 v[168:171], a[240:243], a[172:175], v[168:171]\n\t"
"ds_read_b64_tr_b16 v[224:225], v240 offset:21504\n\t"
"v_mfma_f32_16x16x32_bf16 v[168:171], a[244:247], a[176:179], v[168:171]\n\t"
"ds_read_b64_tr_b16 v[226:227], v240 offset:23040\n\t"
"v_mfma_f32_16x16x32_bf16 v[168:171], a[248:251], a[180:183], v[168:171]\n\t"
"ds_read_b64_tr_b16 v[232:233], v241 offset:21504\n\t"
"v_mfma_f32_16x16x32_bf16 v[172:175], a[240:243], a[176:179], v[172:175]\n\t"
"s_waitcnt vmcnt(10)\n\t"
"ds_read_b64_tr_b16 v[234:235], v241 offset:23040\n\t"
"v_mfma_f32_16x16x32_bf16 v[172:175], a[244:247], a[180:183], v[172:175]\n\t"
"ds_read_b128 a[168:171], v244 offset:0\n\t"
"v_mfma_f32_16x16x32_bf16 v[172:175], a[248:251], a[184:187], v[172:175]\n\t"
"ds_read_b128 a[172:175], v244 offset:512\n\t"
"v_mfma_f32_16x16x32_bf16 v[236:239], v[236:239], a[208:211], 0\n\t"
"ds_read_b128 a[176:179], v244 offset:1024\n\t"
"v_mfma_f32_16x16x32_bf16 v[228:231], v[228:231], a[208:211], 0\n\t"
"v_mfma_f32_16x16x32_bf16 v[176:179], a[240:243], a[180:183], v[176:179]\n\t"
"ds_read_b128 a[180:183], v244 offset:1536\n\t"
"v_mfma_f32_16x16x32_bf16 v[176:179], a[244:247], a[184:187], v[176:179]\n\t"
"v_mfma_f32_16x16x32_bf16 v[176:179], a[248:251], a[188:191], v[176:179]\n\t"
"v_mfma_f32_16x16x32_bf16 v[180:183], a[240:243], a[184:187], v[180:183]\n\t"
"s_waitcnt lgkmcnt(6)\n\t"
"ds_read_b128 a[184:187], v244 offset:2048\n\t"
"v_cvt_pk_bf16_f32 v236, v236, v237\n\t"
"v_mfma_f32_16x16x32_bf16 v[180:183], a[244:247], a[188:191], v[180:183]\n\t"
"s_waitcnt lgkmcnt(5)\n\t"
"v_cvt_pk_bf16_f32 v237, v238, v239\n\t"
"v_cvt_pk_bf16_f32 v238, v228, v229\n\t"
"v_mfma_f32_16x16x32_bf16 v[180:183], a[248:251], a[192:195], v[180:183]\n\t"
"v_cvt_pk_bf16_f32 v239, v230, v231\n\t"
"v_mfma_f32_16x16x32_bf16 v[224:227], v[224:227], a[208:211], 0\n\t"
"v_mfma_f32_16x16x32_bf16 v[232:235], v[232:235], a[208:211], 0\n\t"
"ds_read_b64_tr_b16 v[228:229], v240 offset:41472\n\t"
"v_mfma_f32_16x16x32_bf16 v[184:187], a[240:243], a[188:191], v[184:187]\n\t"
"ds_read_b128 a[188:191], v244 offset:2560\n\t"
"v_mfma_f32_16x16x32_bf16 v[184:187], a[244:247], a[192:195], v[184:187]\n\t"
"ds_read_b64_tr_b16 v[230:231], v240 offset:43008\n\t"
"v_mfma_f32_16x16x32_bf16 v[184:187], a[248:251], a[196:199], v[184:187]\n\t"
"v_mfma_f32_16x16x32_bf16 v[188:191], a[240:243], a[192:195], v[188:191]\n\t"
"ds_read_b128 a[192:195], v244 offset:3072\n\t"
"v_cvt_pk_bf16_f32 v224, v224, v225\n\t"
"v_mfma_f32_16x16x32_bf16 v[188:191], a[244:247], a[196:199], v[188:191]\n\t"
"v_cvt_pk_bf16_f32 v225, v226, v227\n\t"
"v_cvt_pk_bf16_f32 v226, v232, v233\n\t"
"v_mfma_f32_16x16x32_bf16 v[188:191], a[248:251], a[200:203], v[188:191]\n\t"
"v_cvt_pk_bf16_f32 v227, v234, v235\n\t"
"v_mfma_f32_16x16x32_bf16 v[192:195], a[240:243], a[196:199], v[192:195]\n\t"
"s_waitcnt lgkmcnt(6)\n\t"
"ds_read_b64_tr_b16 v[232:233], v241 offset:41472\n\t"
"v_mfma_f32_16x16x32_bf16 v[192:195], a[244:247], a[200:203], v[192:195]\n\t"
"ds_read_b64_tr_b16 v[234:235], v241 offset:43008\n\t"
"v_mfma_f32_16x16x32_bf16 v[192:195], a[248:251], a[204:207], v[192:195]\n\t"
"ds_read_b128 a[196:199], v244 offset:3584\n\t"
"Lst9_%=:\n\t"
"v_mfma_f32_16x16x32_bf16 v[196:199], a[220:223], a[168:171], v[196:199]\n\t"
"s_mov_b64 vcc, -1\n\t"
"s_bitcmp1_b32 s73, 1\n\t"
"s_cbranch_scc1 Lvh2_%=\n\t"
"s_bitcmp1_b32 s73, 0\n\t"
"s_cbranch_scc1 Lpg18_%=\n\t"
"s_branch Lpg17_%=\n\t"
"Lvh2_%=:\n\t"
"s_bitcmp1_b32 s73, 0\n\t"
"s_cbranch_scc1 Lpg20_%=\n\t"
"s_branch Lpg19_%=\n\t"
"Lvr2_%=:\n\t"
"s_mov_b64 vcc, 0\n\t"
"s_waitcnt lgkmcnt(8)\n\t"
"ds_read_b128 a[200:203], v244 offset:4096\n\t"
"v_mfma_f32_16x16x32_bf16 v[196:199], a[224:227], a[172:175], v[196:199]\n\t"
"ds_read_b128 a[204:207], v244 offset:4608\n\t"
"v_mfma_f32_16x16x32_bf16 v[196:199], a[252:255], a[176:179], v[196:199]\n\t"
"buffer_load_dwordx4 v[96:99], v252, s[8:11], s20 offen offset:1024 sc1\n\t"
"v_mfma_f32_16x16x32_bf16 v[204:207], a[220:223], a[172:175], v[204:207]\n\t"
"s_waitcnt lgkmcnt(6)\n\t"
"v_mfma_f32_16x16x32_bf16 v[204:207], a[224:227], a[176:179], v[204:207]\n\t"
"s_waitcnt lgkmcnt(3)\n\t"
"v_mfma_f32_16x16x32_bf16 v[204:207], a[252:255], a[180:183], v[204:207]\n\t"
"v_mfma_f32_16x16x32_bf16 v[228:231], v[228:231], a[208:211], 0\n\t"
"v_add_u32 v240, 59904, v240\n\t"
"v_add_u32 v241, 59904, v241\n\t"
"v_mfma_f32_16x16x32_bf16 v[232:235], v[232:235], a[208:211], 0\n\t"
"v_mfma_f32_16x16x32_bf16 v[212:215], a[220:223], a[176:179], v[212:215]\n\t"
"v_mfma_f32_16x16x32_bf16 v[212:215], a[224:227], a[180:183], v[212:215]\n\t"
"v_mfma_f32_16x16x32_bf16 v[212:215], a[252:255], a[184:187], v[212:215]\n\t"
"s_barrier\n\t"
"v_mfma_f32_16x16x32_bf16 a[0:3], a[220:223], a[180:183], a[0:3]\n\t"
"v_cvt_pk_bf16_f32 v228, v228, v229\n\t"
"v_mfma_f32_16x16x32_bf16 a[0:3], a[224:227], a[184:187], a[0:3]\n\t"
"v_cvt_pk_bf16_f32 v229, v230, v231\n\t"
"v_cvt_pk_bf16_f32 v230, v232, v233\n\t"
"v_mfma_f32_16x16x32_bf16 a[0:3], a[252:255], a[188:191], a[0:3]\n\t"
"ds_write_b128 v243, v[236:239] offset:0\n\t"
"v_mfma_f32_16x16x32_bf16 a[8:11], a[220:223], a[184:187], a[8:11]\n\t"
"s_waitcnt lgkmcnt(1)\n\t"
"v_cvt_pk_bf16_f32 v231, v234, v235\n\t"
"v_mfma_f32_16x16x32_bf16 a[8:11], a[224:227], a[188:191], a[8:11]\n\t"
"ds_read_b64_tr_b16 v[236:237], v240 offset:1536\n\t"
"v_mfma_f32_16x16x32_bf16 a[8:11], a[252:255], a[192:195], a[8:11]\n\t"
"Lpg7_%=:\n\t"
"s_cbranch_vccnz Lpg11_%=\n\t"
"ds_write_b128 v243, v[224:227] offset:9216\n\t"
"v_mfma_f32_16x16x32_bf16 a[16:19], a[220:223], a[188:191], a[16:19]\n\t"
"buffer_load_dwordx4 a[232:235], v252, s[8:11], s20 offen offset:2048 sc1\n\t"
"v_mfma_f32_16x16x32_bf16 a[16:19], a[224:227], a[192:195], a[16:19]\n\t"
"s_mov_b32 s20, 1032192\n\t"
"ds_read_b64_tr_b16 v[238:239], v240 offset:3072\n\t"
"v_mfma_f32_16x16x32_bf16 a[16:19], a[252:255], a[196:199], a[16:19]\n\t"
"ds_write_b128 v243, v[228:231] offset:18432\n\t"
"v_mfma_f32_16x16x32_bf16 a[24:27], a[220:223], a[192:195], a[24:27]\n\t"
"buffer_load_dwordx4 a[236:239], v252, s[8:11], s20 offen offset:0 sc1\n\t"
"v_mfma_f32_16x16x32_bf16 a[24:27], a[224:227], a[196:199], a[24:27]\n\t"
"ds_read_b64_tr_b16 v[224:225], v241 offset:1536\n\t"
"v_mfma_f32_16x16x32_bf16 a[24:27], a[252:255], a[200:203], a[24:27]\n\t"
"ds_read_b64_tr_b16 v[226:227], v241 offset:3072\n\t"
"v_mfma_f32_16x16x32_bf16 a[32:35], a[220:223], a[196:199], a[32:35]\n\t"
"s_waitcnt lgkmcnt(3)\n\t"
"ds_read_b64_tr_b16 v[232:233], v240 offset:21504\n\t"
"v_mfma_f32_16x16x32_bf16 a[32:35], a[224:227], a[200:203], a[32:35]\n\t"
"ds_read_b64_tr_b16 v[234:235], v240 offset:23040\n\t"
"v_mfma_f32_16x16x32_bf16 a[32:35], a[252:255], a[204:207], a[32:35]\n\t"
"s_waitcnt vmcnt(12)\n\t"
"ds_read_b64_tr_b16 v[228:229], v241 offset:21504\n\t"
"v_mfma_f32_16x16x32_bf16 v[236:239], v[236:239], a[208:211], 0\n\t"
"s_waitcnt lgkmcnt(3)\n\t"
"ds_read_b64_tr_b16 v[230:231], v241 offset:23040\n\t"
"v_mfma_f32_16x16x32_bf16 v[224:227], v[224:227], a[208:211], 0\n\t"
"buffer_load_dwordx4 a[220:223], v252, s[8:11], s20 offen offset:1024 sc1\n\t"
"v_mfma_f32_16x16x32_bf16 a[40:43], a[228:231], a[168:171], a[40:43]\n\t"
"buffer_load_dwordx4 a[224:227], v252, s[8:11], s20 offen offset:2048 sc1\n\t"
"v_mfma_f32_16x16x32_bf16 a[40:43], v[248:251], a[172:175], a[40:43]\n\t"
"s_mov_b32 s20, 1044480\n\t"
"v_mfma_f32_16x16x32_bf16 a[40:43], a[212:215], a[176:179], a[40:43]\n\t"
"v_mfma_f32_16x16x32_bf16 a[48:51], a[228:231], a[172:175], a[48:51]\n\t"
"s_waitcnt lgkmcnt(2)\n\t"
"v_cvt_pk_bf16_f32 v236, v236, v237\n\t"
"v_cvt_pk_bf16_f32 v237, v238, v239\n\t"
"v_mfma_f32_16x16x32_bf16 a[48:51], v[248:251], a[176:179], a[48:51]\n\t"
"s_waitcnt lgkmcnt(0)\n\t"
"v_cvt_pk_bf16_f32 v238, v224, v225\n\t"
"v_cvt_pk_bf16_f32 v239, v226, v227\n\t"
"v_mfma_f32_16x16x32_bf16 a[48:51], a[212:215], a[180:183], a[48:51]\n\t"
"ds_write_b128 v243, v[236:239] offset:27648\n\t"
"v_mfma_f32_16x16x32_bf16 v[232:235], v[232:235], a[208:211], 0\n\t"
"buffer_load_dwordx4 a[240:243], v252, s[8:11], s20 offen offset:0 sc1\n\t"
"v_mfma_f32_16x16x32_bf16 v[228:231], v[228:231], a[208:211], 0\n\t"
"ds_read_b64_tr_b16 v[224:225], v240 offset:41472\n\t"
"v_mfma_f32_16x16x32_bf16 a[56:59], a[228:231], a[176:179], a[56:59]\n\t"
"buffer_load_dwordx4 a[244:247], v252, s[8:11], s20 offen offset:1024 sc1\n\t"
"v_mfma_f32_16x16x32_bf16 a[56:59], v[248:251], a[180:183], a[56:59]\n\t"
"ds_read_b64_tr_b16 v[226:227], v240 offset:43008\n\t"
"v_mfma_f32_16x16x32_bf16 a[56:59], a[212:215], a[184:187], a[56:59]\n\t"
"ds_read_b64_tr_b16 v[236:237], v241 offset:41472\n\t"
"v_mfma_f32_16x16x32_bf16 a[64:67], a[228:231], a[180:183], a[64:67]\n\t"
"ds_read_b64_tr_b16 v[238:239], v241 offset:43008\n\t"
"v_cvt_pk_bf16_f32 v232, v232, v233\n\t"
"v_mfma_f32_16x16x32_bf16 a[64:67], v[248:251], a[184:187], a[64:67]\n\t"
"v_cvt_pk_bf16_f32 v233, v234, v235\n\t"
"v_cvt_pk_bf16_f32 v234, v228, v229\n\t"
"v_mfma_f32_16x16x32_bf16 a[64:67], a[212:215], a[188:191], a[64:67]\n\t"
"v_cvt_pk_bf16_f32 v235, v230, v231\n\t"
"v_mfma_f32_16x16x32_bf16 a[72:75], a[228:231], a[184:187], a[72:75]\n\t"
"buffer_load_dwordx4 a[248:251], v252, s[8:11], s20 offen offset:2048 sc1\n\t"
"v_mfma_f32_16x16x32_bf16 a[72:75], v[248:251], a[188:191], a[72:75]\n\t"
"s_mov_b32 s20, 1056768\n\t"
"v_mfma_f32_16x16x32_bf16 a[72:75], a[212:215], a[192:195], a[72:75]\n\t"
"ds_write_b128 v243, v[232:235] offset:36864\n\t"
"v_mfma_f32_16x16x32_bf16 a[80:83], a[228:231], a[188:191], a[80:83]\n\t"
"buffer_load_dwordx4 a[252:255], v252, s[8:11], s20 offen offset:0 sc1\n\t"
"v_mfma_f32_16x16x32_bf16 a[80:83], v[248:251], a[192:195], a[80:83]\n\t"
"v_mfma_f32_16x16x32_bf16 a[80:83], a[212:215], a[196:199], a[80:83]\n\t"
"v_mfma_f32_16x16x32_bf16 a[88:91], a[228:231], a[192:195], a[88:91]\n\t"
"s_waitcnt lgkmcnt(3)\n\t"
"v_mfma_f32_16x16x32_bf16 a[88:91], v[248:251], a[196:199], a[88:91]\n\t"
"s_waitcnt lgkmcnt(1)\n\t"
"v_mfma_f32_16x16x32_bf16 a[88:91], a[212:215], a[200:203], a[88:91]\n\t"
"v_mfma_f32_16x16x32_bf16 v[224:227], v[224:227], a[208:211], 0\n\t"
"v_mfma_f32_16x16x32_bf16 v[236:239], v[236:239], a[208:211], 0\n\t"
"ds_read_b64_tr_b16 v[228:229], v246 offset:512\n\t"
"v_mfma_f32_16x16x32_bf16 a[96:99], a[228:231], a[196:199], a[96:99]\n\t"
"buffer_load_dwordx4 a[228:231], v252, s[8:11], s20 offen offset:1024 sc1\n\t"
"v_mfma_f32_16x16x32_bf16 a[96:99], v[248:251], a[200:203], a[96:99]\n\t"
"s_waitcnt vmcnt(8)\n\t"
"ds_read_b64_tr_b16 v[230:231], v246 offset:3584\n\t"
"v_mfma_f32_16x16x32_bf16 a[96:99], a[212:215], a[204:207], a[96:99]\n\t"
"v_mfma_f32_16x16x32_bf16 a[104:107], a[216:219], a[168:171], a[104:107]\n\t"
"buffer_load_dwordx4 v[248:251], v252, s[8:11], s20 offen offset:2048 sc1\n\t"
"v_mfma_f32_16x16x32_bf16 a[104:107], v[96:99], a[172:175], a[104:107]\n\t"
"s_mov_b32 s20, 1290240\n\t"
"ds_read_b64_tr_b16 v[232:233], v246 offset:6656\n\t"
"v_cvt_pk_bf16_f32 v224, v224, v225\n\t"
"v_mfma_f32_16x16x32_bf16 a[104:107], a[232:235], a[176:179], a[104:107]\n\t"
"ds_read_b64_tr_b16 v[234:235], v246 offset:9728\n\t"
"v_cvt_pk_bf16_f32 v225, v226, v227\n\t"
"v_mfma_f32_16x16x32_bf16 a[112:115], a[216:219], a[172:175], a[112:115]\n\t"
"ds_read_b128 a[168:171], v244 offset:9216\n\t"
"v_cvt_pk_bf16_f32 v226, v236, v237\n\t"
"v_mfma_f32_16x16x32_bf16 a[112:115], v[96:99], a[176:179], a[112:115]\n\t"
"ds_read_b128 a[172:175], v244 offset:9728\n\t"
"v_cvt_pk_bf16_f32 v227, v238, v239\n\t"
"v_mfma_f32_16x16x32_bf16 a[112:115], a[232:235], a[180:183], a[112:115]\n\t"
"ds_write_b128 v243, v[224:227] offset:46080\n\t"
"v_mfma_f32_16x16x32_bf16 a[120:123], a[216:219], a[176:179], a[120:123]\n\t"
"buffer_load_dwordx4 a[212:215], v252, s[8:11], s20 offen offset:0 sc1\n\t"
"v_mfma_f32_16x16x32_bf16 a[120:123], v[96:99], a[180:183], a[120:123]\n\t"
"ds_read_b64_tr_b16 v[236:237], v246 offset:12800\n\t"
"v_mfma_f32_16x16x32_bf16 a[120:123], a[232:235], a[184:187], a[120:123]\n\t"
"ds_read_b64_tr_b16 v[238:239], v246 offset:15872\n\t"
"v_mfma_f32_16x16x32_bf16 a[128:131], a[216:219], a[180:183], a[128:131]\n\t"
"s_waitcnt lgkmcnt(7)\n\t"
"ds_read_b128 a[176:179], v244 offset:10240\n\t"
"v_mfma_f32_16x16x32_bf16 a[128:131], v[96:99], a[184:187], a[128:131]\n\t"
"ds_read_b64_tr_b16 v[224:225], v246 offset:20480\n\t"
"v_mfma_f32_16x16x32_bf16 a[128:131], a[232:235], a[188:191], a[128:131]\n\t"
"ds_read_b64_tr_b16 v[226:227], v246 offset:23552\n\t"
"v_mfma_f32_16x16x32_bf16 v[228:231], v[228:231], v[220:223], 0\n\t"
"ds_read_b128 a[180:183], v244 offset:10752\n\t"
"v_mfma_f32_16x16x32_bf16 a[136:139], a[216:219], a[184:187], a[136:139]\n\t"
"s_waitcnt lgkmcnt(9)\n\t"
"ds_read_b128 a[184:187], v244 offset:11264\n\t"
"v_mfma_f32_16x16x32_bf16 a[136:139], v[96:99], a[188:191], a[136:139]\n\t"
"v_mfma_f32_16x16x32_bf16 a[136:139], a[232:235], a[192:195], a[136:139]\n\t"
"v_mfma_f32_16x16x32_bf16 v[232:235], v[232:235], v[220:223], 0\n\t"
"v_mfma_f32_16x16x32_bf16 a[144:147], a[216:219], a[188:191], a[144:147]\n\t"
"s_waitcnt lgkmcnt(5)\n\t"
"ds_read_b128 a[188:191], v244 offset:11776\n\t"
"v_cvt_pk_bf16_f32 v228, v228, v229\n\t"
"v_mfma_f32_16x16x32_bf16 a[144:147], v[96:99], a[192:195], a[144:147]\n\t"
"v_cvt_pk_bf16_f32 v229, v230, v231\n\t"
"v_mfma_f32_16x16x32_bf16 a[144:147], a[232:235], a[196:199], a[144:147]\n\t"
"v_mfma_f32_16x16x32_bf16 v[236:239], v[236:239], v[220:223], 0\n\t"
"v_mfma_f32_16x16x32_bf16 a[152:155], a[216:219], a[192:195], a[152:155]\n\t"
"s_waitcnt lgkmcnt(3)\n\t"
"ds_read_b128 a[192:195], v244 offset:12288\n\t"
"v_cvt_pk_bf16_f32 v230, v232, v233\n\t"
"v_mfma_f32_16x16x32_bf16 a[152:155], v[96:99], a[196:199], a[152:155]\n\t"
"v_cvt_pk_bf16_f32 v231, v234, v235\n\t"
"v_mfma_f32_16x16x32_bf16 a[152:155], a[232:235], a[200:203], a[152:155]\n\t"
"ds_read_b64_tr_b16 v[232:233], v246 offset:26624\n\t"
"v_mfma_f32_16x16x32_bf16 v[224:227], v[224:227], v[220:223], 0\n\t"
"ds_read_b64_tr_b16 v[234:235], v246 offset:29696\n\t"
"v_mfma_f32_16x16x32_bf16 a[160:163], a[216:219], a[196:199], a[160:163]\n\t"
"buffer_load_dwordx4 a[216:219], v252, s[8:11], s20 offen offset:1024 sc1\n\t"
"v_mfma_f32_16x16x32_bf16 a[160:163], v[96:99], a[200:203], a[160:163]\n\t"
"s_waitcnt vmcnt(8)\n\t"
"ds_read_b128 a[196:199], v244 offset:12800\n\t"
"v_cvt_pk_bf16_f32 v236, v236, v237\n\t"
"v_mfma_f32_16x16x32_bf16 a[160:163], a[232:235], a[204:207], a[160:163]\n\t"
"ds_read_b128 a[200:203], v244 offset:13312\n\t"
"v_cvt_pk_bf16_f32 v237, v238, v239\n\t"
"v_mfma_f32_16x16x32_bf16 v[200:203], a[236:239], a[168:171], v[200:203]\n\t"
"ds_read_b128 a[204:207], v244 offset:13824\n\t"
"v_mfma_f32_16x16x32_bf16 v[200:203], a[220:223], a[172:175], v[200:203]\n\t"
"buffer_load_dwordx4 v[96:99], v252, s[8:11], s20 offen offset:2048 sc1\n\t"
"v_mfma_f32_16x16x32_bf16 v[200:203], a[224:227], a[176:179], v[200:203]\n\t"
"s_waitcnt lgkmcnt(3)\n\t"
"s_mov_b32 s20, 1302528\n\t"
"v_cvt_pk_bf16_f32 v238, v224, v225\n\t"
"v_mfma_f32_16x16x32_bf16 v[232:235], v[232:235], v[220:223], 0\n\t"
"v_cvt_pk_bf16_f32 v239, v226, v227\n\t"
"v_mfma_f32_16x16x32_bf16 v[208:211], a[236:239], a[172:175], v[208:211]\n\t"
"buffer_load_dwordx4 a[232:235], v252, s[8:11], s20 offen offset:0 sc1\n\t"
"v_mfma_f32_16x16x32_bf16 v[208:211], a[220:223], a[176:179], v[208:211]\n\t"
"v_mfma_f32_16x16x32_bf16 v[208:211], a[224:227], a[180:183], v[208:211]\n\t"
"s_waitcnt lgkmcnt(0)\n\t"
"s_barrier\n\t"
"v_mfma_f32_16x16x32_bf16 v[216:219], a[236:239], a[176:179], v[216:219]\n\t"
"ds_read_b64_tr_b16 v[224:225], v246 offset:32768\n\t"
"v_mfma_f32_16x16x32_bf16 v[216:219], a[220:223], a[180:183], v[216:219]\n\t"
"ds_read_b64_tr_b16 v[226:227], v246 offset:35840\n\t"
"v_cvt_pk_bf16_f32 v232, v232, v233\n\t"
"v_mfma_f32_16x16x32_bf16 v[216:219], a[224:227], a[184:187], v[216:219]\n\t"
"ds_write2st64_b64 v245, v[228:229], v[230:231] offset0:0 offset1:1\n\t"
"v_mfma_f32_16x16x32_bf16 a[4:7], a[236:239], a[180:183], a[4:7]\n\t"
"v_cvt_pk_bf16_f32 v233, v234, v235\n\t"
"v_mfma_f32_16x16x32_bf16 a[4:7], a[220:223], a[184:187], a[4:7]\n\t"
"ds_read_b64_tr_b16 v[228:229], v246 offset:40448\n\t"
"v_mfma_f32_16x16x32_bf16 a[4:7], a[224:227], a[188:191], a[4:7]\n\t"
"ds_write2st64_b64 v245, v[236:237], v[238:239] offset0:2 offset1:3\n\t"
"v_mfma_f32_16x16x32_bf16 a[12:15], a[236:239], a[184:187], a[12:15]\n\t"
"v_mfma_f32_16x16x32_bf16 a[12:15], a[220:223], a[188:191], a[12:15]\n\t"
"ds_read_b64_tr_b16 v[230:231], v246 offset:43520\n\t"
"v_mfma_f32_16x16x32_bf16 a[12:15], a[224:227], a[192:195], a[12:15]\n\t"
"ds_write_b64 v245, v[232:233] offset:2048\n\t"
"v_mfma_f32_16x16x32_bf16 a[20:23], a[236:239], a[188:191], a[20:23]\n\t"
"s_waitcnt lgkmcnt(5)\n\t"
"ds_read_b64_tr_b16 v[232:233], v246 offset:46592\n\t"
"v_mfma_f32_16x16x32_bf16 a[20:23], a[220:223], a[192:195], a[20:23]\n\t"
"ds_read_b64_tr_b16 v[234:235], v246 offset:49664\n\t"
"v_mfma_f32_16x16x32_bf16 a[20:23], a[224:227], a[196:199], a[20:23]\n\t"
"v_mfma_f32_16x16x32_bf16 v[224:227], v[224:227], v[220:223], 0\n\t"
"v_mfma_f32_16x16x32_bf16 a[28:31], a[236:239], a[192:195], a[28:31]\n\t"
"s_waitcnt lgkmcnt(3)\n\t"
"v_mfma_f32_16x16x32_bf16 a[28:31], a[220:223], a[196:199], a[28:31]\n\t"
"v_mfma_f32_16x16x32_bf16 a[28:31], a[224:227], a[200:203], a[28:31]\n\t"
"v_mfma_f32_16x16x32_bf16 v[228:231], v[228:231], v[220:223], 0\n\t"
"v_mfma_f32_16x16x32_bf16 a[36:39], a[236:239], a[196:199], a[36:39]\n\t"
"buffer_load_dwordx4 a[236:239], v252, s[8:11], s20 offen offset:1024 sc1\n\t"
"v_mfma_f32_16x16x32_bf16 a[36:39], a[220:223], a[200:203], a[36:39]\n\t"
"s_waitcnt vmcnt(8) lgkmcnt(0)\n\t"
"v_cvt_pk_bf16_f32 v224, v224, v225\n\t"
"v_cvt_pk_bf16_f32 v225, v226, v227\n\t"
"v_mfma_f32_16x16x32_bf16 a[36:39], a[224:227], a[204:207], a[36:39]\n\t"
"v_mfma_f32_16x16x32_bf16 v[232:235], v[232:235], v[220:223], 0\n\t"
"buffer_load_dwordx4 a[220:223], v252, s[8:11], s20 offen offset:2048 sc1\n\t"
"v_mfma_f32_16x16x32_bf16 a[44:47], a[240:243], a[168:171], a[44:47]\n\t"
"s_mov_b32 s20, 1314816\n\t"
"v_cvt_pk_bf16_f32 v226, v228, v229\n\t"
"v_cvt_pk_bf16_f32 v227, v230, v231\n\t"
"v_mfma_f32_16x16x32_bf16 a[44:47], a[244:247], a[172:175], a[44:47]\n\t"
"ds_read_b64_tr_b16 v[228:229], v246 offset:52736\n\t"
"v_mfma_f32_16x16x32_bf16 a[44:47], a[248:251], a[176:179], a[44:47]\n\t"
"buffer_load_dwordx4 a[224:227], v252, s[8:11], s20 offen offset:0 sc1\n\t"
"v_mfma_f32_16x16x32_bf16 a[52:55], a[240:243], a[172:175], a[52:55]\n\t"
"ds_read_b64_tr_b16 v[230:231], v246 offset:55808\n\t"
"v_mfma_f32_16x16x32_bf16 a[52:55], a[244:247], a[176:179], a[52:55]\n\t"
"v_cvt_pk_bf16_f32 v232, v232, v233\n\t"
"v_cvt_pk_bf16_f32 v233, v234, v235\n\t"
"v_mfma_f32_16x16x32_bf16 a[52:55], a[248:251], a[180:183], a[52:55]\n\t"
"ds_write2st64_b64 v245, v[224:225], v[226:227] offset0:5 offset1:6\n\t"
"v_mfma_f32_16x16x32_bf16 a[60:63], a[240:243], a[176:179], a[60:63]\n\t"
"v_mfma_f32_16x16x32_bf16 a[60:63], a[244:247], a[180:183], a[60:63]\n\t"
"v_add_u32 v243, 55296, v243\n\t"
"v_mfma_f32_16x16x32_bf16 a[60:63], a[248:251], a[184:187], a[60:63]\n\t"
"v_mfma_f32_16x16x32_bf16 a[68:71], a[240:243], a[180:183], a[68:71]\n\t"
"v_mfma_f32_16x16x32_bf16 a[68:71], a[244:247], a[184:187], a[68:71]\n\t"
"v_mfma_f32_16x16x32_bf16 a[68:71], a[248:251], a[188:191], a[68:71]\n\t"
"v_mfma_f32_16x16x32_bf16 a[76:79], a[240:243], a[184:187], a[76:79]\n\t"
"s_waitcnt lgkmcnt(1)\n\t"
"v_mfma_f32_16x16x32_bf16 a[76:79], a[244:247], a[188:191], a[76:79]\n\t"
"v_mfma_f32_16x16x32_bf16 a[76:79], a[248:251], a[192:195], a[76:79]\n\t"
"v_mfma_f32_16x16x32_bf16 v[228:231], v[228:231], v[220:223], 0\n\t"
"v_mfma_f32_16x16x32_bf16 a[84:87], a[240:243], a[188:191], a[84:87]\n\t"
"v_mfma_f32_16x16x32_bf16 a[84:87], a[244:247], a[192:195], a[84:87]\n\t"
"v_mfma_f32_16x16x32_bf16 a[84:87], a[248:251], a[196:199], a[84:87]\n\t"
"v_mfma_f32_16x16x32_bf16 a[92:95], a[240:243], a[192:195], a[92:95]\n\t"
"v_mfma_f32_16x16x32_bf16 a[92:95], a[244:247], a[196:199], a[92:95]\n\t"
"v_cvt_pk_bf16_f32 v234, v228, v229\n\t"
"v_cvt_pk_bf16_f32 v235, v230, v231\n\t"
"v_mfma_f32_16x16x32_bf16 a[92:95], a[248:251], a[200:203], a[92:95]\n\t"
"ds_write2st64_b64 v245, v[232:233], v[234:235] offset0:7 offset1:8\n\t"
"v_mfma_f32_16x16x32_bf16 a[100:103], a[240:243], a[196:199], a[100:103]\n\t"
"buffer_load_dwordx4 a[240:243], v252, s[8:11], s20 offen offset:1024 sc1\n\t"
"v_mfma_f32_16x16x32_bf16 a[100:103], a[244:247], a[200:203], a[100:103]\n\t"
"s_waitcnt vmcnt(8)\n\t"
"v_mfma_f32_16x16x32_bf16 a[100:103], a[248:251], a[204:207], a[100:103]\n\t"
"v_mfma_f32_16x16x32_bf16 a[108:111], a[252:255], a[168:171], a[108:111]\n\t"
"buffer_load_dwordx4 a[244:247], v252, s[8:11], s20 offen offset:2048 sc1\n\t"
"v_mfma_f32_16x16x32_bf16 a[108:111], a[228:231], a[172:175], a[108:111]\n\t"
"s_mov_b32 s20, 1327104\n\t"
"v_mfma_f32_16x16x32_bf16 a[108:111], v[248:251], a[176:179], a[108:111]\n\t"
"s_waitcnt lgkmcnt(0)\n\t"
"s_barrier\n\t"
"v_mfma_f32_16x16x32_bf16 a[116:119], a[252:255], a[172:175], a[116:119]\n\t"
"ds_read_b64_tr_b16 v[236:237], v240 offset:512\n\t"
"v_mfma_f32_16x16x32_bf16 a[116:119], a[228:231], a[176:179], a[116:119]\n\t"
"ds_read_b64_tr_b16 v[238:239], v240 offset:2048\n\t"
"v_mfma_f32_16x16x32_bf16 a[116:119], v[248:251], a[180:183], a[116:119]\n\t"
"ds_read_b64_tr_b16 v[224:225], v241 offset:512\n\t"
"v_mfma_f32_16x16x32_bf16 a[124:127], a[252:255], a[176:179], a[124:127]\n\t"
"ds_read_b64_tr_b16 v[226:227], v241 offset:2048\n\t"
"v_mfma_f32_16x16x32_bf16 a[124:127], a[228:231], a[180:183], a[124:127]\n\t"
"buffer_load_dwordx4 a[248:251], v252, s[8:11], s20 offen offset:0 sc1\n\t"
"v_mfma_f32_16x16x32_bf16 a[124:127], v[248:251], a[184:187], a[124:127]\n\t"
"ds_read_b64_tr_b16 v[228:229], v240 offset:20480\n\t"
"v_mfma_f32_16x16x32_bf16 a[132:135], a[252:255], a[180:183], a[132:135]\n\t"
"s_waitcnt lgkmcnt(3)\n\t"
"ds_read_b64_tr_b16 v[230:231], v240 offset:22016\n\t"
"v_mfma_f32_16x16x32_bf16 a[132:135], a[228:231], a[184:187], a[132:135]\n\t"
"ds_read_b64_tr_b16 v[232:233], v241 offset:20480\n\t"
"v_mfma_f32_16x16x32_bf16 a[132:135], v[248:251], a[188:191], a[132:135]\n\t"
"s_waitcnt lgkmcnt(3)\n\t"
"ds_read_b64_tr_b16 v[234:235], v241 offset:22016\n\t"
"v_mfma_f32_16x16x32_bf16 v[236:239], v[236:239], a[208:211], 0\n\t"
"ds_read_b128 a[168:171], v244 offset:0\n\t"
"v_mfma_f32_16x16x32_bf16 v[224:227], v[224:227], a[208:211], 0\n\t"
"ds_read_b128 a[172:175], v244 offset:512\n\t"
"v_mfma_f32_16x16x32_bf16 a[140:143], a[252:255], a[184:187], a[140:143]\n\t"
"ds_read_b128 a[176:179], v244 offset:1024\n\t"
"v_mfma_f32_16x16x32_bf16 a[140:143], a[228:231], a[188:191], a[140:143]\n\t"
"ds_read_b128 a[180:183], v244 offset:1536\n\t"
"v_mfma_f32_16x16x32_bf16 a[140:143], v[248:251], a[192:195], a[140:143]\n\t"
"ds_read_b128 a[184:187], v244 offset:2048\n\t"
"v_mfma_f32_16x16x32_bf16 a[148:151], a[252:255], a[188:191], a[148:151]\n\t"
"s_waitcnt lgkmcnt(7)\n\t"
"ds_read_b128 a[188:191], v244 offset:2560\n\t"
"v_cvt_pk_bf16_f32 v236, v236, v237\n\t"
"v_mfma_f32_16x16x32_bf16 a[148:151], a[228:231], a[192:195], a[148:151]\n\t"
"s_waitcnt lgkmcnt(6)\n\t"
"v_cvt_pk_bf16_f32 v237, v238, v239\n\t"
"v_cvt_pk_bf16_f32 v238, v224, v225\n\t"
"v_mfma_f32_16x16x32_bf16 a[148:151], v[248:251], a[196:199], a[148:151]\n\t"
"v_cvt_pk_bf16_f32 v239, v226, v227\n\t"
"v_mfma_f32_16x16x32_bf16 v[228:231], v[228:231], a[208:211], 0\n\t"
"ds_write_b128 v243, v[236:239] offset:27648\n\t"
"v_mfma_f32_16x16x32_bf16 v[232:235], v[232:235], a[208:211], 0\n\t"
"v_mfma_f32_16x16x32_bf16 a[156:159], a[252:255], a[192:195], a[156:159]\n\t"
"ds_read_b128 a[192:195], v244 offset:3072\n\t"
"v_mfma_f32_16x16x32_bf16 a[156:159], a[228:231], a[196:199], a[156:159]\n\t"
"ds_read_b64_tr_b16 v[224:225], v240 offset:40448\n\t"
"v_mfma_f32_16x16x32_bf16 a[156:159], v[248:251], a[200:203], a[156:159]\n\t"
"ds_read_b64_tr_b16 v[226:227], v240 offset:41984\n\t"
"v_mfma_f32_16x16x32_bf16 a[164:167], a[252:255], a[196:199], a[164:167]\n\t"
"buffer_load_dwordx4 a[252:255], v252, s[8:11], s20 offen offset:1024 sc1\n\t"
"v_mfma_f32_16x16x32_bf16 a[164:167], a[228:231], a[200:203], a[164:167]\n\t"
"s_waitcnt vmcnt(8) lgkmcnt(7)\n\t"
"ds_read_b64_tr_b16 v[236:237], v241 offset:40448\n\t"
"v_cvt_pk_bf16_f32 v228, v228, v229\n\t"
"v_mfma_f32_16x16x32_bf16 a[164:167], v[248:251], a[204:207], a[164:167]\n\t"
"ds_read_b64_tr_b16 v[238:239], v241 offset:41984\n\t"
"v_cvt_pk_bf16_f32 v229, v230, v231\n\t"
"Lst10_%=:\n\t"
"v_mfma_f32_16x16x32_bf16 v[196:199], a[212:215], a[168:171], v[196:199]\n\t"
"s_waitcnt lgkmcnt(8)\n\t"
"ds_read_b128 a[196:199], v244 offset:3584\n\t"
"v_cvt_pk_bf16_f32 v230, v232, v233\n\t"
"v_mfma_f32_16x16x32_bf16 v[196:199], a[216:219], a[172:175], v[196:199]\n\t"
"ds_read_b128 a[200:203], v244 offset:4096\n\t"
"v_cvt_pk_bf16_f32 v231, v234, v235\n\t"
"v_mfma_f32_16x16x32_bf16 v[196:199], v[96:99], a[176:179], v[196:199]\n\t"
"ds_write_b128 v243, v[228:231] offset:36864\n\t"
"v_mfma_f32_16x16x32_bf16 v[204:207], a[212:215], a[172:175], v[204:207]\n\t"
"buffer_load_dwordx4 v[248:251], v252, s[8:11], s20 offen offset:2048 sc1\n\t"
"v_mfma_f32_16x16x32_bf16 v[204:207], a[216:219], a[176:179], v[204:207]\n\t"
"s_waitcnt lgkmcnt(10)\n\t"
"ds_read_b128 a[204:207], v244 offset:4608\n\t"
"v_mfma_f32_16x16x32_bf16 v[204:207], v[96:99], a[180:183], v[204:207]\n\t"
"s_mov_b32 s20, 1339392\n\t"
"v_subrev_u32 v240, 59904, v240\n\t"
"v_subrev_u32 v241, 59904, v241\n\t"
"v_mfma_f32_16x16x32_bf16 v[212:215], a[212:215], a[176:179], v[212:215]\n\t"
"s_waitcnt lgkmcnt(6)\n\t"
"ds_read_b64_tr_b16 v[232:233], v240 offset:512\n\t"
"v_mfma_f32_16x16x32_bf16 v[212:215], a[216:219], a[180:183], v[212:215]\n\t"
"buffer_load_dwordx4 a[228:231], v252, s[8:11], s20 offen offset:0 sc1\n\t"
"v_mfma_f32_16x16x32_bf16 v[212:215], v[96:99], a[184:187], v[212:215]\n\t"
"s_waitcnt lgkmcnt(5)\n\t"
"ds_read_b64_tr_b16 v[234:235], v240 offset:2048\n\t"
"v_mfma_f32_16x16x32_bf16 v[224:227], v[224:227], a[208:211], 0\n\t"
"ds_read_b64_tr_b16 v[228:229], v241 offset:512\n\t"
"v_mfma_f32_16x16x32_bf16 v[236:239], v[236:239], a[208:211], 0\n\t"
"ds_read_b64_tr_b16 v[230:231], v241 offset:2048\n\t"
"v_mfma_f32_16x16x32_bf16 a[0:3], a[212:215], a[180:183], a[0:3]\n\t"
"v_mfma_f32_16x16x32_bf16 a[0:3], a[216:219], a[184:187], a[0:3]\n\t"
"v_mfma_f32_16x16x32_bf16 a[0:3], v[96:99], a[188:191], a[0:3]\n\t"
"v_mfma_f32_16x16x32_bf16 a[8:11], a[212:215], a[184:187], a[8:11]\n\t"
"s_waitcnt lgkmcnt(2)\n\t"
"v_cvt_pk_bf16_f32 v224, v224, v225\n\t"
"v_cvt_pk_bf16_f32 v225, v226, v227\n\t"
"v_mfma_f32_16x16x32_bf16 a[8:11], a[216:219], a[188:191], a[8:11]\n\t"
"s_waitcnt lgkmcnt(0)\n\t"
"v_cvt_pk_bf16_f32 v226, v236, v237\n\t"
"v_cvt_pk_bf16_f32 v227, v238, v239\n\t"
"v_mfma_f32_16x16x32_bf16 a[8:11], v[96:99], a[192:195], a[8:11]\n\t"
"ds_write_b128 v243, v[224:227] offset:46080\n\t"
"v_mfma_f32_16x16x32_bf16 v[232:235], v[232:235], a[208:211], 0\n\t"
"v_mfma_f32_16x16x32_bf16 v[228:231], v[228:231], a[208:211], 0\n\t"
"ds_read_b64_tr_b16 v[236:237], v240 offset:20480\n\t"
"v_mfma_f32_16x16x32_bf16 a[16:19], a[212:215], a[188:191], a[16:19]\n\t"
"ds_read_b64_tr_b16 v[238:239], v240 offset:22016\n\t"
"v_mfma_f32_16x16x32_bf16 a[16:19], a[216:219], a[192:195], a[16:19]\n\t"
"ds_read_b64_tr_b16 v[224:225], v241 offset:20480\n\t"
"v_mfma_f32_16x16x32_bf16 a[16:19], v[96:99], a[196:199], a[16:19]\n\t"
"ds_read_b64_tr_b16 v[226:227], v241 offset:22016\n\t"
"v_mfma_f32_16x16x32_bf16 a[24:27], a[212:215], a[192:195], a[24:27]\n\t"
"v_cvt_pk_bf16_f32 v232, v232, v233\n\t"
"v_cvt_pk_bf16_f32 v233, v234, v235\n\t"
"v_mfma_f32_16x16x32_bf16 a[24:27], a[216:219], a[196:199], a[24:27]\n\t"
"v_cvt_pk_bf16_f32 v234, v228, v229\n\t"
"v_cvt_pk_bf16_f32 v235, v230, v231\n\t"
"v_mfma_f32_16x16x32_bf16 a[24:27], v[96:99], a[200:203], a[24:27]\n\t"
"ds_write_b128 v243, v[232:235] offset:0\n\t"
"v_mfma_f32_16x16x32_bf16 a[32:35], a[212:215], a[196:199], a[32:35]\n\t"
"buffer_load_dwordx4 a[212:215], v252, s[8:11], s20 offen offset:1024 sc1\n\t"
"v_mfma_f32_16x16x32_bf16 a[32:35], a[216:219], a[200:203], a[32:35]\n\t"
"s_waitcnt vmcnt(8)\n\t"
"ds_read_b64_tr_b16 v[228:229], v240 offset:40448\n\t"
"v_mfma_f32_16x16x32_bf16 a[32:35], v[96:99], a[204:207], a[32:35]\n\t"
"ds_read_b64_tr_b16 v[230:231], v240 offset:41984\n\t"
"v_mfma_f32_16x16x32_bf16 a[40:43], a[232:235], a[168:171], a[40:43]\n\t"
"buffer_load_dwordx4 v[96:99], v252, s[8:11], s20 offen offset:2048 sc1\n\t"
"v_mfma_f32_16x16x32_bf16 a[40:43], a[236:239], a[172:175], a[40:43]\n\t"
"s_mov_b32 s20, 1351680\n\t"
"ds_read_b64_tr_b16 v[232:233], v241 offset:40448\n\t"
"v_mfma_f32_16x16x32_bf16 a[40:43], a[220:223], a[176:179], a[40:43]\n\t"
"ds_read_b64_tr_b16 v[234:235], v241 offset:41984\n\t"
"v_mfma_f32_16x16x32_bf16 a[48:51], a[232:235], a[172:175], a[48:51]\n\t"
"buffer_load_dwordx4 a[216:219], v252, s[8:11], s20 offen offset:0 sc1\n\t"
"v_mfma_f32_16x16x32_bf16 a[48:51], a[236:239], a[176:179], a[48:51]\n\t"
"s_waitcnt lgkmcnt(5)\n\t"
"v_mfma_f32_16x16x32_bf16 a[48:51], a[220:223], a[180:183], a[48:51]\n\t"
"v_mfma_f32_16x16x32_bf16 v[236:239], v[236:239], a[208:211], 0\n\t"
"v_mfma_f32_16x16x32_bf16 v[224:227], v[224:227], a[208:211], 0\n\t"
"v_xor_b32 v246, 0xc0, v246\n\t"
"v_mfma_f32_16x16x32_bf16 a[56:59], a[232:235], a[176:179], a[56:59]\n\t"
"v_mfma_f32_16x16x32_bf16 a[56:59], a[236:239], a[180:183], a[56:59]\n\t"
"v_mfma_f32_16x16x32_bf16 a[56:59], a[220:223], a[184:187], a[56:59]\n\t"
"v_mfma_f32_16x16x32_bf16 a[64:67], a[232:235], a[180:183], a[64:67]\n\t"
"s_waitcnt lgkmcnt(2)\n\t"
"v_cvt_pk_bf16_f32 v236, v236, v237\n\t"
"v_cvt_pk_bf16_f32 v237, v238, v239\n\t"
"v_mfma_f32_16x16x32_bf16 a[64:67], a[236:239], a[184:187], a[64:67]\n\t"
"s_waitcnt lgkmcnt(0)\n\t"
"v_cvt_pk_bf16_f32 v238, v224, v225\n\t"
"v_cvt_pk_bf16_f32 v239, v226, v227\n\t"
"v_mfma_f32_16x16x32_bf16 a[64:67], a[220:223], a[188:191], a[64:67]\n\t"
"ds_write_b128 v243, v[236:239] offset:9216\n\t"
"v_mfma_f32_16x16x32_bf16 v[228:231], v[228:231], a[208:211], 0\n\t"
"v_mfma_f32_16x16x32_bf16 v[232:235], v[232:235], a[208:211], 0\n\t"
"v_mfma_f32_16x16x32_bf16 a[72:75], a[232:235], a[184:187], a[72:75]\n\t"
"v_mfma_f32_16x16x32_bf16 a[72:75], a[236:239], a[188:191], a[72:75]\n\t"
"v_mfma_f32_16x16x32_bf16 a[72:75], a[220:223], a[192:195], a[72:75]\n\t"
"v_mfma_f32_16x16x32_bf16 a[80:83], a[232:235], a[188:191], a[80:83]\n\t"
"v_cvt_pk_bf16_f32 v228, v228, v229\n\t"
"v_cvt_pk_bf16_f32 v229, v230, v231\n\t"
"v_mfma_f32_16x16x32_bf16 a[80:83], a[236:239], a[192:195], a[80:83]\n\t"
"v_cvt_pk_bf16_f32 v230, v232, v233\n\t"
"v_cvt_pk_bf16_f32 v231, v234, v235\n\t"
"v_mfma_f32_16x16x32_bf16 a[80:83], a[220:223], a[196:199], a[80:83]\n\t"
"ds_write_b128 v243, v[228:231] offset:18432\n\t"
"v_mfma_f32_16x16x32_bf16 a[88:91], a[232:235], a[192:195], a[88:91]\n\t"
"v_mfma_f32_16x16x32_bf16 a[88:91], a[236:239], a[196:199], a[88:91]\n\t"
"ds_read_b64_tr_b16 v[224:225], v246 offset:512\n\t"
"v_mfma_f32_16x16x32_bf16 a[88:91], a[220:223], a[200:203], a[88:91]\n\t"
"ds_read_b64_tr_b16 v[226:227], v246 offset:3584\n\t"
"v_mfma_f32_16x16x32_bf16 a[96:99], a[232:235], a[196:199], a[96:99]\n\t"
"buffer_load_dwordx4 a[232:235], v252, s[8:11], s20 offen offset:1024 sc1\n\t"
"v_mfma_f32_16x16x32_bf16 a[96:99], a[236:239], a[200:203], a[96:99]\n\t"
"s_waitcnt vmcnt(8)\n\t"
"ds_read_b64_tr_b16 v[236:237], v246 offset:6656\n\t"
"v_mfma_f32_16x16x32_bf16 a[96:99], a[220:223], a[204:207], a[96:99]\n\t"
"ds_read_b64_tr_b16 v[238:239], v246 offset:9728\n\t"
"v_mfma_f32_16x16x32_bf16 a[104:107], a[224:227], a[168:171], a[104:107]\n\t"
"buffer_load_dwordx4 a[220:223], v252, s[8:11], s20 offen offset:2048 sc1\n\t"
"v_mfma_f32_16x16x32_bf16 a[104:107], a[240:243], a[172:175], a[104:107]\n\t"
"s_mov_b32 s20, 1216512\n\t"
"ds_read_b64_tr_b16 v[232:233], v246 offset:12800\n\t"
"v_mfma_f32_16x16x32_bf16 a[104:107], a[244:247], a[176:179], a[104:107]\n\t"
"ds_read_b64_tr_b16 v[234:235], v246 offset:15872\n\t"
"v_mfma_f32_16x16x32_bf16 a[112:115], a[224:227], a[172:175], a[112:115]\n\t"
"buffer_load_dwordx4 a[236:239], v252, s[8:11], s20 offen offset:0 sc1\n\t"
"v_mfma_f32_16x16x32_bf16 a[112:115], a[240:243], a[176:179], a[112:115]\n\t"
"ds_read_b128 a[168:171], v244 offset:9216\n\t"
"v_mfma_f32_16x16x32_bf16 a[112:115], a[244:247], a[180:183], a[112:115]\n\t"
"ds_read_b64_tr_b16 v[228:229], v246 offset:20480\n\t"
"v_mfma_f32_16x16x32_bf16 a[120:123], a[224:227], a[176:179], a[120:123]\n\t"
"s_waitcnt lgkmcnt(6)\n\t"
"ds_read_b64_tr_b16 v[230:231], v246 offset:23552\n\t"
"v_mfma_f32_16x16x32_bf16 a[120:123], a[240:243], a[180:183], a[120:123]\n\t"
"ds_read_b128 a[172:175], v244 offset:9728\n\t"
"v_mfma_f32_16x16x32_bf16 a[120:123], a[244:247], a[184:187], a[120:123]\n\t"
"ds_read_b128 a[176:179], v244 offset:10240\n\t"
"v_mfma_f32_16x16x32_bf16 v[224:227], v[224:227], v[220:223], 0\n\t"
"v_mfma_f32_16x16x32_bf16 a[128:131], a[224:227], a[180:183], a[128:131]\n\t"
"s_waitcnt lgkmcnt(7)\n\t"
"ds_read_b128 a[180:183], v244 offset:10752\n\t"
"v_mfma_f32_16x16x32_bf16 a[128:131], a[240:243], a[184:187], a[128:131]\n\t"
"v_mfma_f32_16x16x32_bf16 a[128:131], a[244:247], a[188:191], a[128:131]\n\t"
"v_mfma_f32_16x16x32_bf16 v[236:239], v[236:239], v[220:223], 0\n\t"
"v_mfma_f32_16x16x32_bf16 a[136:139], a[224:227], a[184:187], a[136:139]\n\t"
"s_waitcnt lgkmcnt(6)\n\t"
"ds_read_b128 a[184:187], v244 offset:11264\n\t"
"v_cvt_pk_bf16_f32 v224, v224, v225\n\t"
"v_mfma_f32_16x16x32_bf16 a[136:139], a[240:243], a[188:191], a[136:139]\n\t"
"v_cvt_pk_bf16_f32 v225, v226, v227\n\t"
"v_mfma_f32_16x16x32_bf16 a[136:139], a[244:247], a[192:195], a[136:139]\n\t"
"v_mfma_f32_16x16x32_bf16 v[232:235], v[232:235], v[220:223], 0\n\t"
"v_mfma_f32_16x16x32_bf16 a[144:147], a[224:227], a[188:191], a[144:147]\n\t"
"s_waitcnt lgkmcnt(4)\n\t"
"ds_read_b128 a[188:191], v244 offset:11776\n\t"
"v_cvt_pk_bf16_f32 v226, v236, v237\n\t"
"v_mfma_f32_16x16x32_bf16 a[144:147], a[240:243], a[192:195], a[144:147]\n\t"
"v_cvt_pk_bf16_f32 v227, v238, v239\n\t"
"v_mfma_f32_16x16x32_bf16 a[144:147], a[244:247], a[196:199], a[144:147]\n\t"
"v_mfma_f32_16x16x32_bf16 v[228:231], v[228:231], v[220:223], 0\n\t"
"v_mfma_f32_16x16x32_bf16 a[152:155], a[224:227], a[192:195], a[152:155]\n\t"
"ds_read_b128 a[192:195], v244 offset:12288\n\t"
"v_cvt_pk_bf16_f32 v232, v232, v233\n\t"
"v_mfma_f32_16x16x32_bf16 a[152:155], a[240:243], a[196:199], a[152:155]\n\t"
"v_cvt_pk_bf16_f32 v233, v234, v235\n\t"
"v_mfma_f32_16x16x32_bf16 a[152:155], a[244:247], a[200:203], a[152:155]\n\t"
"v_mfma_f32_16x16x32_bf16 a[160:163], a[224:227], a[196:199], a[160:163]\n\t"
"buffer_load_dwordx4 a[224:227], v252, s[8:11], s20 offen offset:1024 sc1\n\t"
"v_mfma_f32_16x16x32_bf16 a[160:163], a[240:243], a[200:203], a[160:163]\n\t"
"s_waitcnt vmcnt(8) lgkmcnt(4)\n\t"
"ds_read_b128 a[196:199], v244 offset:12800\n\t"
"v_cvt_pk_bf16_f32 v234, v228, v229\n\t"
"v_mfma_f32_16x16x32_bf16 a[160:163], a[244:247], a[204:207], a[160:163]\n\t"
"ds_read_b128 a[200:203], v244 offset:13312\n\t"
"v_cvt_pk_bf16_f32 v235, v230, v231\n\t"
"v_mfma_f32_16x16x32_bf16 v[200:203], a[248:251], a[168:171], v[200:203]\n\t"
"ds_read_b64_tr_b16 v[228:229], v246 offset:26624\n\t"
"v_mfma_f32_16x16x32_bf16 v[200:203], a[252:255], a[172:175], v[200:203]\n\t"
"s_waitcnt lgkmcnt(6)\n\t"
"ds_read_b64_tr_b16 v[230:231], v246 offset:29696\n\t"
"v_mfma_f32_16x16x32_bf16 v[200:203], v[248:251], a[176:179], v[200:203]\n\t"
"s_waitcnt lgkmcnt(0)\n\t"
"ds_read_b128 a[204:207], v244 offset:13824\n\t"
"v_mfma_f32_16x16x32_bf16 v[228:231], v[228:231], v[220:223], 0\n\t"
"buffer_load_dwordx4 a[240:243], v252, s[8:11], s20 offen offset:2048 sc1\n\t"
"s_mov_b32 s20, 1228800\n\t"
"v_mfma_f32_16x16x32_bf16 v[208:211], a[248:251], a[172:175], v[208:211]\n\t"
"buffer_load_dwordx4 a[244:247], v252, s[8:11], s20 offen offset:0 sc1\n\t"
"v_mfma_f32_16x16x32_bf16 v[208:211], a[252:255], a[176:179], v[208:211]\n\t"
"v_mfma_f32_16x16x32_bf16 v[208:211], v[248:251], a[180:183], v[208:211]\n\t"
"s_waitcnt lgkmcnt(0)\n\t"
"s_barrier\n\t"
"v_mfma_f32_16x16x32_bf16 v[216:219], a[248:251], a[176:179], v[216:219]\n\t"
"v_mfma_f32_16x16x32_bf16 v[216:219], a[252:255], a[180:183], v[216:219]\n\t"
"v_cvt_pk_bf16_f32 v228, v228, v229\n\t"
"v_cvt_pk_bf16_f32 v229, v230, v231\n\t"
"v_mfma_f32_16x16x32_bf16 v[216:219], v[248:251], a[184:187], v[216:219]\n\t"
"ds_write2st64_b64 v245, v[224:225], v[226:227] offset0:0 offset1:1\n\t"
"v_mfma_f32_16x16x32_bf16 a[4:7], a[248:251], a[180:183], a[4:7]\n\t"
"v_mfma_f32_16x16x32_bf16 a[4:7], a[252:255], a[184:187], a[4:7]\n\t"
"ds_read_b64_tr_b16 v[224:225], v246 offset:32768\n\t"
"v_mfma_f32_16x16x32_bf16 a[4:7], v[248:251], a[188:191], a[4:7]\n\t"
"ds_write2st64_b64 v245, v[232:233], v[234:235] offset0:2 offset1:3\n\t"
"v_mfma_f32_16x16x32_bf16 a[12:15], a[248:251], a[184:187], a[12:15]\n\t"
"v_mfma_f32_16x16x32_bf16 a[12:15], a[252:255], a[188:191], a[12:15]\n\t"
"ds_read_b64_tr_b16 v[226:227], v246 offset:35840\n\t"
"v_mfma_f32_16x16x32_bf16 a[12:15], v[248:251], a[192:195], a[12:15]\n\t"
"ds_write_b64 v245, v[228:229] offset:2048\n\t"
"v_mfma_f32_16x16x32_bf16 a[20:23], a[248:251], a[188:191], a[20:23]\n\t"
"ds_read_b64_tr_b16 v[232:233], v246 offset:40448\n\t"
"v_mfma_f32_16x16x32_bf16 a[20:23], a[252:255], a[192:195], a[20:23]\n\t"
"ds_read_b64_tr_b16 v[234:235], v246 offset:43520\n\t"
"v_mfma_f32_16x16x32_bf16 a[20:23], v[248:251], a[196:199], a[20:23]\n\t"
"s_waitcnt lgkmcnt(3)\n\t"
"ds_read_b64_tr_b16 v[228:229], v246 offset:46592\n\t"
"v_mfma_f32_16x16x32_bf16 v[224:227], v[224:227], v[220:223], 0\n\t"
"ds_read_b64_tr_b16 v[230:231], v246 offset:49664\n\t"
"v_mfma_f32_16x16x32_bf16 a[28:31], a[248:251], a[192:195], a[28:31]\n\t"
"s_waitcnt vmcnt(7)\n\t"
"v_mfma_f32_16x16x32_bf16 a[28:31], a[252:255], a[196:199], a[28:31]\n\t"
"v_mfma_f32_16x16x32_bf16 a[28:31], v[248:251], a[200:203], a[28:31]\n\t"
"s_waitcnt lgkmcnt(2)\n\t"
"v_mfma_f32_16x16x32_bf16 v[232:235], v[232:235], v[220:223], 0\n\t"
"v_mfma_f32_16x16x32_bf16 a[36:39], a[248:251], a[196:199], a[36:39]\n\t"
"Lpg8_%=:\n\t"
"s_cbranch_vccnz Lpg12_%=\n\t"
"s_waitcnt lgkmcnt(0)\n\t"
"v_cvt_pk_bf16_f32 v224, v224, v225\n\t"
"v_mfma_f32_16x16x32_bf16 a[36:39], a[252:255], a[200:203], a[36:39]\n\t"
"v_cvt_pk_bf16_f32 v225, v226, v227\n\t"
"v_mfma_f32_16x16x32_bf16 a[36:39], v[248:251], a[204:207], a[36:39]\n\t"
"buffer_load_dwordx4 a[248:251], v252, s[8:11], s20 offen offset:1024 sc1\n\t"
"v_mfma_f32_16x16x32_bf16 v[228:231], v[228:231], v[220:223], 0\n\t"
"buffer_load_dwordx4 v[248:251], v252, s[8:11], s20 offen offset:2048 sc1\n\t"
"v_mfma_f32_16x16x32_bf16 a[44:47], a[228:231], a[168:171], a[44:47]\n\t"
"s_mov_b32 s20, 1241088\n\t"
"v_cvt_pk_bf16_f32 v226, v232, v233\n\t"
"v_cvt_pk_bf16_f32 v227, v234, v235\n\t"
"v_mfma_f32_16x16x32_bf16 a[44:47], a[212:215], a[172:175], a[44:47]\n\t"
"ds_read_b64_tr_b16 v[232:233], v246 offset:52736\n\t"
"v_mfma_f32_16x16x32_bf16 a[44:47], v[96:99], a[176:179], a[44:47]\n\t"
"buffer_load_dwordx4 a[252:255], v252, s[8:11], s20 offen offset:0 sc1\n\t"
"v_mfma_f32_16x16x32_bf16 a[52:55], a[228:231], a[172:175], a[52:55]\n\t"
"ds_read_b64_tr_b16 v[234:235], v246 offset:55808\n\t"
"v_mfma_f32_16x16x32_bf16 a[52:55], a[212:215], a[176:179], a[52:55]\n\t"
"v_cvt_pk_bf16_f32 v228, v228, v229\n\t"
"v_cvt_pk_bf16_f32 v229, v230, v231\n\t"
"v_mfma_f32_16x16x32_bf16 a[52:55], v[96:99], a[180:183], a[52:55]\n\t"
"ds_write2st64_b64 v245, v[224:225], v[226:227] offset0:5 offset1:6\n\t"
"v_mfma_f32_16x16x32_bf16 a[60:63], a[228:231], a[176:179], a[60:63]\n\t"
"v_mfma_f32_16x16x32_bf16 a[60:63], a[212:215], a[180:183], a[60:63]\n\t"
"v_subrev_u32 v243, 55296, v243\n\t"
"v_mfma_f32_16x16x32_bf16 a[60:63], v[96:99], a[184:187], a[60:63]\n\t"
"v_mfma_f32_16x16x32_bf16 a[68:71], a[228:231], a[180:183], a[68:71]\n\t"
"v_mfma_f32_16x16x32_bf16 a[68:71], a[212:215], a[184:187], a[68:71]\n\t"
"v_mfma_f32_16x16x32_bf16 a[68:71], v[96:99], a[188:191], a[68:71]\n\t"
"v_mfma_f32_16x16x32_bf16 a[76:79], a[228:231], a[184:187], a[76:79]\n\t"
"s_waitcnt lgkmcnt(1)\n\t"
"v_mfma_f32_16x16x32_bf16 a[76:79], a[212:215], a[188:191], a[76:79]\n\t"
"v_mfma_f32_16x16x32_bf16 a[76:79], v[96:99], a[192:195], a[76:79]\n\t"
"v_mfma_f32_16x16x32_bf16 v[232:235], v[232:235], v[220:223], 0\n\t"
"v_mfma_f32_16x16x32_bf16 a[84:87], a[228:231], a[188:191], a[84:87]\n\t"
"v_mfma_f32_16x16x32_bf16 a[84:87], a[212:215], a[192:195], a[84:87]\n\t"
"v_mfma_f32_16x16x32_bf16 a[84:87], v[96:99], a[196:199], a[84:87]\n\t"
"v_mfma_f32_16x16x32_bf16 a[92:95], a[228:231], a[192:195], a[92:95]\n\t"
"v_mfma_f32_16x16x32_bf16 a[92:95], a[212:215], a[196:199], a[92:95]\n\t"
"v_cvt_pk_bf16_f32 v230, v232, v233\n\t"
"v_cvt_pk_bf16_f32 v231, v234, v235\n\t"
"v_mfma_f32_16x16x32_bf16 a[92:95], v[96:99], a[200:203], a[92:95]\n\t"
"ds_write2st64_b64 v245, v[228:229], v[230:231] offset0:7 offset1:8\n\t"
"v_mfma_f32_16x16x32_bf16 a[100:103], a[228:231], a[196:199], a[100:103]\n\t"
"buffer_load_dwordx4 a[228:231], v252, s[8:11], s20 offen offset:1024 sc1\n\t"
"v_mfma_f32_16x16x32_bf16 a[100:103], a[212:215], a[200:203], a[100:103]\n\t"
"s_waitcnt vmcnt(8)\n\t"
"ds_read_b64_tr_b16 v[236:237], v240 offset:1024\n\t"
"v_mfma_f32_16x16x32_bf16 a[100:103], v[96:99], a[204:207], a[100:103]\n\t"
"ds_read_b64_tr_b16 v[238:239], v240 offset:2560\n\t"
"v_mfma_f32_16x16x32_bf16 a[108:111], a[216:219], a[168:171], a[108:111]\n\t"
"buffer_load_dwordx4 v[96:99], v252, s[8:11], s20 offen offset:2048 sc1\n\t"
"v_mfma_f32_16x16x32_bf16 a[108:111], a[232:235], a[172:175], a[108:111]\n\t"
"s_mov_b32 s20, 1253376\n\t"
"ds_read_b64_tr_b16 v[224:225], v241 offset:1024\n\t"
"v_mfma_f32_16x16x32_bf16 a[108:111], a[220:223], a[176:179], a[108:111]\n\t"
"s_waitcnt lgkmcnt(3)\n\t"
"s_barrier\n\t"
"v_mfma_f32_16x16x32_bf16 a[116:119], a[216:219], a[172:175], a[116:119]\n\t"
"ds_read_b64_tr_b16 v[226:227], v241 offset:2560\n\t"
"v_mfma_f32_16x16x32_bf16 a[116:119], a[232:235], a[176:179], a[116:119]\n\t"
"buffer_load_dwordx4 a[212:215], v252, s[8:11], s20 offen offset:0 sc1\n\t"
"v_mfma_f32_16x16x32_bf16 a[116:119], a[220:223], a[180:183], a[116:119]\n\t"
"ds_read_b64_tr_b16 v[232:233], v240 offset:20992\n\t"
"v_mfma_f32_16x16x32_bf16 a[124:127], a[216:219], a[176:179], a[124:127]\n\t"
"ds_read_b64_tr_b16 v[234:235], v240 offset:22528\n\t"
"v_mfma_f32_16x16x32_bf16 a[124:127], a[232:235], a[180:183], a[124:127]\n\t"
"ds_read_b64_tr_b16 v[228:229], v241 offset:20992\n\t"
"v_mfma_f32_16x16x32_bf16 a[124:127], a[220:223], a[184:187], a[124:127]\n\t"
"ds_read_b64_tr_b16 v[230:231], v241 offset:22528\n\t"
"v_mfma_f32_16x16x32_bf16 a[132:135], a[216:219], a[180:183], a[132:135]\n\t"
"s_waitcnt lgkmcnt(6)\n\t"
"ds_read_b128 a[168:171], v244 offset:0\n\t"
"v_mfma_f32_16x16x32_bf16 a[132:135], a[232:235], a[184:187], a[132:135]\n\t"
"s_waitcnt lgkmcnt(5)\n\t"
"ds_read_b128 a[172:175], v244 offset:512\n\t"
"v_mfma_f32_16x16x32_bf16 a[132:135], a[220:223], a[188:191], a[132:135]\n\t"
"ds_read_b128 a[176:179], v244 offset:1024\n\t"
"v_mfma_f32_16x16x32_bf16 v[236:239], v[236:239], a[208:211], 0\n\t"
"ds_read_b128 a[180:183], v244 offset:1536\n\t"
"v_mfma_f32_16x16x32_bf16 v[224:227], v[224:227], a[208:211], 0\n\t"
"v_mfma_f32_16x16x32_bf16 a[140:143], a[216:219], a[184:187], a[140:143]\n\t"
"ds_read_b128 a[184:187], v244 offset:2048\n\t"
"v_mfma_f32_16x16x32_bf16 a[140:143], a[232:235], a[188:191], a[140:143]\n\t"
"v_mfma_f32_16x16x32_bf16 a[140:143], a[220:223], a[192:195], a[140:143]\n\t"
"v_mfma_f32_16x16x32_bf16 a[148:151], a[216:219], a[188:191], a[148:151]\n\t"
"s_waitcnt lgkmcnt(7)\n\t"
"ds_read_b128 a[188:191], v244 offset:2560\n\t"
"v_cvt_pk_bf16_f32 v236, v236, v237\n\t"
"v_mfma_f32_16x16x32_bf16 a[148:151], a[232:235], a[192:195], a[148:151]\n\t"
"s_waitcnt lgkmcnt(6)\n\t"
"v_cvt_pk_bf16_f32 v237, v238, v239\n\t"
"v_cvt_pk_bf16_f32 v238, v224, v225\n\t"
"v_mfma_f32_16x16x32_bf16 a[148:151], a[220:223], a[196:199], a[148:151]\n\t"
"v_cvt_pk_bf16_f32 v239, v226, v227\n\t"
"v_mfma_f32_16x16x32_bf16 v[232:235], v[232:235], a[208:211], 0\n\t"
"ds_write_b128 v243, v[236:239] offset:512\n\t"
"v_mfma_f32_16x16x32_bf16 v[228:231], v[228:231], a[208:211], 0\n\t"
"v_mfma_f32_16x16x32_bf16 a[156:159], a[216:219], a[192:195], a[156:159]\n\t"
"ds_read_b128 a[192:195], v244 offset:3072\n\t"
"v_mfma_f32_16x16x32_bf16 a[156:159], a[232:235], a[196:199], a[156:159]\n\t"
"ds_read_b64_tr_b16 v[224:225], v240 offset:40960\n\t"
"v_mfma_f32_16x16x32_bf16 a[156:159], a[220:223], a[200:203], a[156:159]\n\t"
"ds_read_b64_tr_b16 v[226:227], v240 offset:42496\n\t"
"v_mfma_f32_16x16x32_bf16 a[164:167], a[216:219], a[196:199], a[164:167]\n\t"
"buffer_load_dwordx4 a[216:219], v252, s[8:11], s20 offen offset:1024 sc1\n\t"
"v_mfma_f32_16x16x32_bf16 a[164:167], a[232:235], a[200:203], a[164:167]\n\t"
"s_waitcnt vmcnt(8) lgkmcnt(7)\n\t"
"ds_read_b64_tr_b16 v[236:237], v241 offset:40960\n\t"
"v_cvt_pk_bf16_f32 v232, v232, v233\n\t"
"v_mfma_f32_16x16x32_bf16 a[164:167], a[220:223], a[204:207], a[164:167]\n\t"
"ds_read_b64_tr_b16 v[238:239], v241 offset:42496\n\t"
"v_cvt_pk_bf16_f32 v233, v234, v235\n\t"
"Lst11_%=:\n\t"
"v_mfma_f32_16x16x32_bf16 v[0:3], a[236:239], a[168:171], v[0:3]\n\t"
"s_waitcnt lgkmcnt(8)\n\t"
"ds_read_b128 a[196:199], v244 offset:3584\n\t"
"v_cvt_pk_bf16_f32 v234, v228, v229\n\t"
"v_mfma_f32_16x16x32_bf16 v[0:3], a[224:227], a[172:175], v[0:3]\n\t"
"ds_read_b128 a[200:203], v244 offset:4096\n\t"
"v_cvt_pk_bf16_f32 v235, v230, v231\n\t"
"v_mfma_f32_16x16x32_bf16 v[0:3], a[240:243], a[176:179], v[0:3]\n\t"
"ds_write_b128 v243, v[232:235] offset:9728\n\t"
"v_mfma_f32_16x16x32_bf16 v[4:7], a[236:239], a[172:175], v[4:7]\n\t"
"buffer_load_dwordx4 a[220:223], v252, s[8:11], s20 offen offset:2048 sc1\n\t"
"v_mfma_f32_16x16x32_bf16 v[4:7], a[224:227], a[176:179], v[4:7]\n\t"
"s_waitcnt lgkmcnt(10)\n\t"
"ds_read_b128 a[204:207], v244 offset:4608\n\t"
"v_mfma_f32_16x16x32_bf16 v[4:7], a[240:243], a[180:183], v[4:7]\n\t"
"s_mov_b32 s20, 1265664\n\t"
"v_add_u32 v240, 59904, v240\n\t"
"v_add_u32 v241, 59904, v241\n\t"
"v_mfma_f32_16x16x32_bf16 v[8:11], a[236:239], a[176:179], v[8:11]\n\t"
"s_waitcnt lgkmcnt(6)\n\t"
"ds_read_b64_tr_b16 v[228:229], v240 offset:1024\n\t"
"v_mfma_f32_16x16x32_bf16 v[8:11], a[224:227], a[180:183], v[8:11]\n\t"
"buffer_load_dwordx4 a[232:235], v252, s[8:11], s20 offen offset:0 sc1\n\t"
"v_mfma_f32_16x16x32_bf16 v[8:11], a[240:243], a[184:187], v[8:11]\n\t"
"s_waitcnt lgkmcnt(5)\n\t"
"ds_read_b64_tr_b16 v[230:231], v240 offset:2560\n\t"
"v_mfma_f32_16x16x32_bf16 v[224:227], v[224:227], a[208:211], 0\n\t"
"ds_read_b64_tr_b16 v[232:233], v241 offset:1024\n\t"
"v_mfma_f32_16x16x32_bf16 v[236:239], v[236:239], a[208:211], 0\n\t"
"ds_read_b64_tr_b16 v[234:235], v241 offset:2560\n\t"
"v_mfma_f32_16x16x32_bf16 v[12:15], a[236:239], a[180:183], v[12:15]\n\t"
"v_mfma_f32_16x16x32_bf16 v[12:15], a[224:227], a[184:187], v[12:15]\n\t"
"v_mfma_f32_16x16x32_bf16 v[12:15], a[240:243], a[188:191], v[12:15]\n\t"
"v_mfma_f32_16x16x32_bf16 v[16:19], a[236:239], a[184:187], v[16:19]\n\t"
"s_waitcnt lgkmcnt(2)\n\t"
"v_cvt_pk_bf16_f32 v224, v224, v225\n\t"
"v_cvt_pk_bf16_f32 v225, v226, v227\n\t"
"v_mfma_f32_16x16x32_bf16 v[16:19], a[224:227], a[188:191], v[16:19]\n\t"
"s_waitcnt lgkmcnt(0)\n\t"
"v_cvt_pk_bf16_f32 v226, v236, v237\n\t"
"v_cvt_pk_bf16_f32 v227, v238, v239\n\t"
"v_mfma_f32_16x16x32_bf16 v[16:19], a[240:243], a[192:195], v[16:19]\n\t"
"ds_write_b128 v243, v[224:227] offset:18944\n\t"
"v_mfma_f32_16x16x32_bf16 v[228:231], v[228:231], a[208:211], 0\n\t"
"v_mfma_f32_16x16x32_bf16 v[232:235], v[232:235], a[208:211], 0\n\t"
"ds_read_b64_tr_b16 v[236:237], v240 offset:20992\n\t"
"v_mfma_f32_16x16x32_bf16 v[20:23], a[236:239], a[188:191], v[20:23]\n\t"
"ds_read_b64_tr_b16 v[238:239], v240 offset:22528\n\t"
"v_mfma_f32_16x16x32_bf16 v[20:23], a[224:227], a[192:195], v[20:23]\n\t"
"ds_read_b64_tr_b16 v[224:225], v241 offset:20992\n\t"
"v_mfma_f32_16x16x32_bf16 v[20:23], a[240:243], a[196:199], v[20:23]\n\t"
"ds_read_b64_tr_b16 v[226:227], v241 offset:22528\n\t"
"v_mfma_f32_16x16x32_bf16 v[24:27], a[236:239], a[192:195], v[24:27]\n\t"
"v_cvt_pk_bf16_f32 v228, v228, v229\n\t"
"v_cvt_pk_bf16_f32 v229, v230, v231\n\t"
"v_mfma_f32_16x16x32_bf16 v[24:27], a[224:227], a[196:199], v[24:27]\n\t"
"v_cvt_pk_bf16_f32 v230, v232, v233\n\t"
"v_cvt_pk_bf16_f32 v231, v234, v235\n\t"
"v_mfma_f32_16x16x32_bf16 v[24:27], a[240:243], a[200:203], v[24:27]\n\t"
"ds_write_b128 v243, v[228:231] offset:28160\n\t"
"v_mfma_f32_16x16x32_bf16 v[28:31], a[236:239], a[196:199], v[28:31]\n\t"
"buffer_load_dwordx4 a[236:239], v252, s[8:11], s20 offen offset:1024 sc1\n\t"
"v_mfma_f32_16x16x32_bf16 v[28:31], a[224:227], a[200:203], v[28:31]\n\t"
"s_waitcnt vmcnt(8)\n\t"
"ds_read_b64_tr_b16 v[232:233], v240 offset:40960\n\t"
"v_mfma_f32_16x16x32_bf16 v[28:31], a[240:243], a[204:207], v[28:31]\n\t"
"ds_read_b64_tr_b16 v[234:235], v240 offset:42496\n\t"
"v_mfma_f32_16x16x32_bf16 v[32:35], a[244:247], a[168:171], v[32:35]\n\t"
"buffer_load_dwordx4 a[224:227], v252, s[8:11], s20 offen offset:2048 sc1\n\t"
"v_mfma_f32_16x16x32_bf16 v[32:35], a[248:251], a[172:175], v[32:35]\n\t"
"s_mov_b32 s20, 1277952\n\t"
"ds_read_b64_tr_b16 v[228:229], v241 offset:40960\n\t"
"v_mfma_f32_16x16x32_bf16 v[32:35], v[248:251], a[176:179], v[32:35]\n\t"
"ds_read_b64_tr_b16 v[230:231], v241 offset:42496\n\t"
"v_mfma_f32_16x16x32_bf16 v[36:39], a[244:247], a[172:175], v[36:39]\n\t"
"buffer_load_dwordx4 a[240:243], v252, s[8:11], s20 offen offset:0 sc1\n\t"
"v_mfma_f32_16x16x32_bf16 v[36:39], a[248:251], a[176:179], v[36:39]\n\t"
"s_waitcnt lgkmcnt(5)\n\t"
"v_mfma_f32_16x16x32_bf16 v[36:39], v[248:251], a[180:183], v[36:39]\n\t"
"v_mfma_f32_16x16x32_bf16 v[236:239], v[236:239], a[208:211], 0\n\t"
"v_mfma_f32_16x16x32_bf16 v[224:227], v[224:227], a[208:211], 0\n\t"
"v_mfma_f32_16x16x32_bf16 v[40:43], a[244:247], a[176:179], v[40:43]\n\t"
"v_mfma_f32_16x16x32_bf16 v[40:43], a[248:251], a[180:183], v[40:43]\n\t"
"v_mfma_f32_16x16x32_bf16 v[40:43], v[248:251], a[184:187], v[40:43]\n\t"
"v_mfma_f32_16x16x32_bf16 v[44:47], a[244:247], a[180:183], v[44:47]\n\t"
"s_waitcnt lgkmcnt(2)\n\t"
"v_cvt_pk_bf16_f32 v236, v236, v237\n\t"
"v_cvt_pk_bf16_f32 v237, v238, v239\n\t"
"v_mfma_f32_16x16x32_bf16 v[44:47], a[248:251], a[184:187], v[44:47]\n\t"
"s_waitcnt lgkmcnt(0)\n\t"
"v_cvt_pk_bf16_f32 v238, v224, v225\n\t"
"v_cvt_pk_bf16_f32 v239, v226, v227\n\t"
"v_mfma_f32_16x16x32_bf16 v[44:47], v[248:251], a[188:191], v[44:47]\n\t"
"ds_write_b128 v243, v[236:239] offset:37376\n\t"
"v_mfma_f32_16x16x32_bf16 v[232:235], v[232:235], a[208:211], 0\n\t"
"v_mfma_f32_16x16x32_bf16 v[228:231], v[228:231], a[208:211], 0\n\t"
"v_mfma_f32_16x16x32_bf16 v[48:51], a[244:247], a[184:187], v[48:51]\n\t"
"buffer_load_dwordx4 a[208:211], v252, s[8:11], s20 offen offset:1024 sc1\n\t"
"v_mfma_f32_16x16x32_bf16 v[48:51], a[248:251], a[188:191], v[48:51]\n\t"
"v_mfma_f32_16x16x32_bf16 v[48:51], v[248:251], a[192:195], v[48:51]\n\t"
"v_mfma_f32_16x16x32_bf16 v[52:55], a[244:247], a[188:191], v[52:55]\n\t"
"v_cvt_pk_bf16_f32 v232, v232, v233\n\t"
"v_cvt_pk_bf16_f32 v233, v234, v235\n\t"
"v_mfma_f32_16x16x32_bf16 v[52:55], a[248:251], a[192:195], v[52:55]\n\t"
"v_cvt_pk_bf16_f32 v234, v228, v229\n\t"
"v_cvt_pk_bf16_f32 v235, v230, v231\n\t"
"v_mfma_f32_16x16x32_bf16 v[52:55], v[248:251], a[196:199], v[52:55]\n\t"
"ds_write_b128 v243, v[232:235] offset:46592\n\t"
"v_mfma_f32_16x16x32_bf16 v[56:59], a[244:247], a[192:195], v[56:59]\n\t"
"v_mfma_f32_16x16x32_bf16 v[56:59], a[248:251], a[196:199], v[56:59]\n\t"
"ds_read_b64_tr_b16 v[224:225], v246 offset:1024\n\t"
"v_mfma_f32_16x16x32_bf16 v[56:59], v[248:251], a[200:203], v[56:59]\n\t"
"ds_read_b64_tr_b16 v[226:227], v246 offset:4096\n\t"
"v_mfma_f32_16x16x32_bf16 v[60:63], a[244:247], a[196:199], v[60:63]\n\t"
"buffer_load_dwordx4 a[244:247], v252, s[8:11], s20 offen offset:2048 sc1\n\t"
"v_mfma_f32_16x16x32_bf16 v[60:63], a[248:251], a[200:203], v[60:63]\n\t"
"s_waitcnt vmcnt(9)\n\t"
"ds_read_b64_tr_b16 v[236:237], v246 offset:7168\n\t"
"s_mov_b32 s20, 1511424\n\t"
"v_mfma_f32_16x16x32_bf16 v[60:63], v[248:251], a[204:207], v[60:63]\n\t"
"ds_read_b64_tr_b16 v[238:239], v246 offset:10240\n\t"
"v_mfma_f32_16x16x32_bf16 v[64:67], a[252:255], a[168:171], v[64:67]\n\t"
"buffer_load_dwordx4 v[248:251], v252, s[8:11], s20 offen offset:0 sc1\n\t"
"v_mfma_f32_16x16x32_bf16 v[64:67], a[228:231], a[172:175], v[64:67]\n\t"
"ds_read_b64_tr_b16 v[228:229], v246 offset:13312\n\t"
"v_mfma_f32_16x16x32_bf16 v[64:67], v[96:99], a[176:179], v[64:67]\n\t"
"ds_read_b64_tr_b16 v[230:231], v246 offset:16384\n\t"
"v_mfma_f32_16x16x32_bf16 v[68:71], a[252:255], a[172:175], v[68:71]\n\t"
"buffer_load_dwordx4 v[240:243], v252, s[8:11], s20 offen offset:1024 sc1\n\t"
"v_mfma_f32_16x16x32_bf16 v[68:71], a[228:231], a[176:179], v[68:71]\n\t"
"ds_read_b128 a[168:171], v244 offset:9216\n\t"
"v_mfma_f32_16x16x32_bf16 v[68:71], v[96:99], a[180:183], v[68:71]\n\t"
"ds_read_b64_tr_b16 v[232:233], v246 offset:20992\n\t"
"v_mfma_f32_16x16x32_bf16 v[72:75], a[252:255], a[176:179], v[72:75]\n\t"
"buffer_load_dwordx4 a[248:251], v252, s[8:11], s20 offen offset:2048 sc1\n\t"
"v_mfma_f32_16x16x32_bf16 v[72:75], a[228:231], a[180:183], v[72:75]\n\t"
"s_waitcnt lgkmcnt(6)\n\t"
"ds_read_b64_tr_b16 v[234:235], v246 offset:24064\n\t"
"v_mfma_f32_16x16x32_bf16 v[72:75], v[96:99], a[184:187], v[72:75]\n\t"
"ds_read_b128 a[172:175], v244 offset:9728\n\t"
"v_mfma_f32_16x16x32_bf16 v[224:227], v[224:227], v[220:223], 0\n\t"
"ds_read_b128 a[176:179], v244 offset:10240\n\t"
"v_mfma_f32_16x16x32_bf16 v[76:79], a[252:255], a[180:183], v[76:79]\n\t"
"s_waitcnt lgkmcnt(7)\n\t"
"ds_read_b128 a[180:183], v244 offset:10752\n\t"
"v_mfma_f32_16x16x32_bf16 v[76:79], a[228:231], a[184:187], v[76:79]\n\t"
"v_mfma_f32_16x16x32_bf16 v[76:79], v[96:99], a[188:191], v[76:79]\n\t"
"v_mfma_f32_16x16x32_bf16 v[236:239], v[236:239], v[220:223], 0\n\t"
"v_mfma_f32_16x16x32_bf16 v[80:83], a[252:255], a[184:187], v[80:83]\n\t"
"s_waitcnt lgkmcnt(6)\n\t"
"ds_read_b128 a[184:187], v244 offset:11264\n\t"
"v_cvt_pk_bf16_f32 v224, v224, v225\n\t"
"v_mfma_f32_16x16x32_bf16 v[80:83], a[228:231], a[188:191], v[80:83]\n\t"
"v_cvt_pk_bf16_f32 v225, v226, v227\n\t"
"v_mfma_f32_16x16x32_bf16 v[80:83], v[96:99], a[192:195], v[80:83]\n\t"
"s_mov_b32 s20, 1523712\n\t"
"v_mfma_f32_16x16x32_bf16 v[228:231], v[228:231], v[220:223], 0\n\t"
"v_mfma_f32_16x16x32_bf16 v[84:87], a[252:255], a[188:191], v[84:87]\n\t"
"s_waitcnt lgkmcnt(4)\n\t"
"ds_read_b128 a[188:191], v244 offset:11776\n\t"
"v_cvt_pk_bf16_f32 v226, v236, v237\n\t"
"v_mfma_f32_16x16x32_bf16 v[84:87], a[228:231], a[192:195], v[84:87]\n\t"
"v_cvt_pk_bf16_f32 v227, v238, v239\n\t"
"v_mfma_f32_16x16x32_bf16 v[84:87], v[96:99], a[196:199], v[84:87]\n\t"
"v_mfma_f32_16x16x32_bf16 v[232:235], v[232:235], v[220:223], 0\n\t"
"v_mfma_f32_16x16x32_bf16 v[88:91], a[252:255], a[192:195], v[88:91]\n\t"
"buffer_load_dwordx4 v[236:239], v252, s[8:11], s20 offen offset:0 sc1\n\t"
"v_mfma_f32_16x16x32_bf16 v[88:91], a[228:231], a[196:199], v[88:91]\n\t"
"ds_read_b128 a[192:195], v244 offset:12288\n\t"
"v_cvt_pk_bf16_f32 v228, v228, v229\n\t"
"v_mfma_f32_16x16x32_bf16 v[88:91], v[96:99], a[200:203], v[88:91]\n\t"
"v_cvt_pk_bf16_f32 v229, v230, v231\n\t"
"v_mfma_f32_16x16x32_bf16 v[92:95], a[252:255], a[196:199], v[92:95]\n\t"
"buffer_load_dwordx4 a[252:255], v252, s[8:11], s20 offen offset:1024 sc1\n\t"
"v_mfma_f32_16x16x32_bf16 v[92:95], a[228:231], a[200:203], v[92:95]\n\t"
"s_waitcnt vmcnt(11) lgkmcnt(4)\n\t"
"ds_read_b128 a[196:199], v244 offset:12800\n\t"
"v_cvt_pk_bf16_f32 v230, v232, v233\n\t"
"v_mfma_f32_16x16x32_bf16 v[92:95], v[96:99], a[204:207], v[92:95]\n\t"
"ds_read_b128 a[200:203], v244 offset:13312\n\t"
"v_cvt_pk_bf16_f32 v231, v234, v235\n\t"
"v_mfma_f32_16x16x32_bf16 v[100:103], a[212:215], a[168:171], v[100:103]\n\t"
"ds_read_b64_tr_b16 v[232:233], v246 offset:27136\n\t"
"v_mfma_f32_16x16x32_bf16 v[100:103], a[216:219], a[172:175], v[100:103]\n\t"
"s_waitcnt lgkmcnt(6)\n\t"
"ds_read_b64_tr_b16 v[234:235], v246 offset:30208\n\t"
"v_mfma_f32_16x16x32_bf16 v[100:103], a[220:223], a[176:179], v[100:103]\n\t"
"s_waitcnt lgkmcnt(0)\n\t"
"ds_read_b128 a[204:207], v244 offset:13824\n\t"
"v_mfma_f32_16x16x32_bf16 v[232:235], v[232:235], v[220:223], 0\n\t"
"buffer_load_dwordx4 v[96:99], v252, s[8:11], s20 offen offset:2048 sc1\n\t"
"s_mov_b32 s20, 1536000\n\t"
"v_mfma_f32_16x16x32_bf16 v[104:107], a[212:215], a[172:175], v[104:107]\n\t"
"buffer_load_dwordx4 a[228:231], v252, s[8:11], s20 offen offset:0 sc1\n\t"
"v_mfma_f32_16x16x32_bf16 v[104:107], a[216:219], a[176:179], v[104:107]\n\t"
"v_mfma_f32_16x16x32_bf16 v[104:107], a[220:223], a[180:183], v[104:107]\n\t"
"s_waitcnt lgkmcnt(0)\n\t"
"s_barrier\n\t"
"v_mfma_f32_16x16x32_bf16 v[108:111], a[212:215], a[176:179], v[108:111]\n\t"
"v_mfma_f32_16x16x32_bf16 v[108:111], a[216:219], a[180:183], v[108:111]\n\t"
"v_cvt_pk_bf16_f32 v232, v232, v233\n\t"
"v_cvt_pk_bf16_f32 v233, v234, v235\n\t"
"v_mfma_f32_16x16x32_bf16 v[108:111], a[220:223], a[184:187], v[108:111]\n\t"
"ds_write2st64_b64 v245, v[224:225], v[226:227] offset0:0 offset1:1\n\t"
"v_mfma_f32_16x16x32_bf16 v[112:115], a[212:215], a[180:183], v[112:115]\n\t"
"v_mfma_f32_16x16x32_bf16 v[112:115], a[216:219], a[184:187], v[112:115]\n\t"
"ds_read_b64_tr_b16 v[224:225], v246 offset:33280\n\t"
"v_mfma_f32_16x16x32_bf16 v[112:115], a[220:223], a[188:191], v[112:115]\n\t"
"ds_write2st64_b64 v245, v[228:229], v[230:231] offset0:2 offset1:3\n\t"
"v_mfma_f32_16x16x32_bf16 v[116:119], a[212:215], a[184:187], v[116:119]\n\t"
"v_mfma_f32_16x16x32_bf16 v[116:119], a[216:219], a[188:191], v[116:119]\n\t"
"ds_read_b64_tr_b16 v[226:227], v246 offset:36352\n\t"
"v_mfma_f32_16x16x32_bf16 v[116:119], a[220:223], a[192:195], v[116:119]\n\t"
"ds_write_b64 v245, v[232:233] offset:2048\n\t"
"v_mfma_f32_16x16x32_bf16 v[120:123], a[212:215], a[188:191], v[120:123]\n\t"
"ds_read_b64_tr_b16 v[228:229], v246 offset:40960\n\t"
"v_mfma_f32_16x16x32_bf16 v[120:123], a[216:219], a[192:195], v[120:123]\n\t"
"ds_read_b64_tr_b16 v[230:231], v246 offset:44032\n\t"
"v_mfma_f32_16x16x32_bf16 v[120:123], a[220:223], a[196:199], v[120:123]\n\t"
"s_waitcnt lgkmcnt(3)\n\t"
"ds_read_b64_tr_b16 v[232:233], v246 offset:47104\n\t"
"v_mfma_f32_16x16x32_bf16 v[224:227], v[224:227], v[220:223], 0\n\t"
"ds_read_b64_tr_b16 v[234:235], v246 offset:50176\n\t"
"v_mfma_f32_16x16x32_bf16 v[124:127], a[212:215], a[192:195], v[124:127]\n\t"
"v_mfma_f32_16x16x32_bf16 v[124:127], a[216:219], a[196:199], v[124:127]\n\t"
"v_mfma_f32_16x16x32_bf16 v[124:127], a[220:223], a[200:203], v[124:127]\n\t"
"s_waitcnt lgkmcnt(2)\n\t"
"v_mfma_f32_16x16x32_bf16 v[228:231], v[228:231], v[220:223], 0\n\t"
"v_mfma_f32_16x16x32_bf16 v[128:131], a[212:215], a[196:199], v[128:131]\n\t"
"buffer_load_dwordx4 a[212:215], v252, s[8:11], s20 offen offset:1024 sc1\n\t"
"v_mfma_f32_16x16x32_bf16 v[128:131], a[216:219], a[200:203], v[128:131]\n\t"
"s_waitcnt vmcnt(11) lgkmcnt(0)\n\t"
"v_cvt_pk_bf16_f32 v224, v224, v225\n\t"
"v_cvt_pk_bf16_f32 v225, v226, v227\n\t"
"v_mfma_f32_16x16x32_bf16 v[128:131], a[220:223], a[204:207], v[128:131]\n\t"
"v_mfma_f32_16x16x32_bf16 v[232:235], v[232:235], v[220:223], 0\n\t"
"buffer_load_dwordx4 a[216:219], v252, s[8:11], s20 offen offset:2048 sc1\n\t"
"v_mfma_f32_16x16x32_bf16 v[132:135], a[232:235], a[168:171], v[132:135]\n\t"
"s_mov_b32 s20, 1548288\n\t"
"v_cvt_pk_bf16_f32 v226, v228, v229\n\t"
"v_cvt_pk_bf16_f32 v227, v230, v231\n\t"
"v_mfma_f32_16x16x32_bf16 v[132:135], a[236:239], a[172:175], v[132:135]\n\t"
"ds_read_b64_tr_b16 v[228:229], v246 offset:53248\n\t"
"v_mfma_f32_16x16x32_bf16 v[132:135], a[224:227], a[176:179], v[132:135]\n\t"
"buffer_load_dwordx4 a[220:223], v252, s[8:11], s20 offen offset:0 sc1\n\t"
"v_mfma_f32_16x16x32_bf16 v[136:139], a[232:235], a[172:175], v[136:139]\n\t"
"ds_read_b64_tr_b16 v[230:231], v246 offset:56320\n\t"
"v_mfma_f32_16x16x32_bf16 v[136:139], a[236:239], a[176:179], v[136:139]\n\t"
"v_cvt_pk_bf16_f32 v232, v232, v233\n\t"
"v_cvt_pk_bf16_f32 v233, v234, v235\n\t"
"v_mfma_f32_16x16x32_bf16 v[136:139], a[224:227], a[180:183], v[136:139]\n\t"
"ds_write2st64_b64 v245, v[224:225], v[226:227] offset0:5 offset1:6\n\t"
"v_mfma_f32_16x16x32_bf16 v[140:143], a[232:235], a[176:179], v[140:143]\n\t"
"v_mfma_f32_16x16x32_bf16 v[140:143], a[236:239], a[180:183], v[140:143]\n\t"
"v_mfma_f32_16x16x32_bf16 v[140:143], a[224:227], a[184:187], v[140:143]\n\t"
"v_mfma_f32_16x16x32_bf16 v[144:147], a[232:235], a[180:183], v[144:147]\n\t"
"v_mfma_f32_16x16x32_bf16 v[144:147], a[236:239], a[184:187], v[144:147]\n\t"
"v_mfma_f32_16x16x32_bf16 v[144:147], a[224:227], a[188:191], v[144:147]\n\t"
"v_mfma_f32_16x16x32_bf16 v[148:151], a[232:235], a[184:187], v[148:151]\n\t"
"s_waitcnt lgkmcnt(1)\n\t"
"v_mfma_f32_16x16x32_bf16 v[148:151], a[236:239], a[188:191], v[148:151]\n\t"
"v_mfma_f32_16x16x32_bf16 v[148:151], a[224:227], a[192:195], v[148:151]\n\t"
"v_mfma_f32_16x16x32_bf16 v[228:231], v[228:231], v[220:223], 0\n\t"
"v_mfma_f32_16x16x32_bf16 v[152:155], a[232:235], a[188:191], v[152:155]\n\t"
"v_mfma_f32_16x16x32_bf16 v[152:155], a[236:239], a[192:195], v[152:155]\n\t"
"v_mfma_f32_16x16x32_bf16 v[152:155], a[224:227], a[196:199], v[152:155]\n\t"
"v_mfma_f32_16x16x32_bf16 v[156:159], a[232:235], a[192:195], v[156:159]\n\t"
"v_mfma_f32_16x16x32_bf16 v[156:159], a[236:239], a[196:199], v[156:159]\n\t"
"v_cvt_pk_bf16_f32 v234, v228, v229\n\t"
"v_cvt_pk_bf16_f32 v235, v230, v231\n\t"
"v_mfma_f32_16x16x32_bf16 v[156:159], a[224:227], a[200:203], v[156:159]\n\t"
"ds_write2st64_b64 v245, v[232:233], v[234:235] offset0:7 offset1:8\n\t"
"v_mfma_f32_16x16x32_bf16 v[160:163], a[232:235], a[196:199], v[160:163]\n\t"
"buffer_load_dwordx4 a[232:235], v252, s[8:11], s20 offen offset:1024 sc1\n\t"
"v_mfma_f32_16x16x32_bf16 v[160:163], a[236:239], a[200:203], v[160:163]\n\t"
"s_waitcnt vmcnt(11)\n\t"
"v_mfma_f32_16x16x32_bf16 v[160:163], a[224:227], a[204:207], v[160:163]\n\t"
"v_mfma_f32_16x16x32_bf16 v[164:167], a[240:243], a[168:171], v[164:167]\n\t"
"buffer_load_dwordx4 a[224:227], v252, s[8:11], s20 offen offset:2048 sc1\n\t"
"v_mfma_f32_16x16x32_bf16 v[164:167], a[208:211], a[172:175], v[164:167]\n\t"
"s_mov_b32 s20, 1560576\n\t"
"v_mfma_f32_16x16x32_bf16 v[164:167], a[244:247], a[176:179], v[164:167]\n\t"
"s_waitcnt lgkmcnt(0)\n\t"
"s_barrier\n\t"
"v_mfma_f32_16x16x32_bf16 v[168:171], a[240:243], a[172:175], v[168:171]\n\t"
"ds_read_b128 a[168:171], v244 offset:0\n\t"
"v_mfma_f32_16x16x32_bf16 v[168:171], a[208:211], a[176:179], v[168:171]\n\t"
"buffer_load_dwordx4 a[236:239], v252, s[8:11], s20 offen offset:0 sc1\n\t"
"v_mfma_f32_16x16x32_bf16 v[168:171], a[244:247], a[180:183], v[168:171]\n\t"
"ds_read_b128 a[172:175], v244 offset:512\n\t"
"v_mfma_f32_16x16x32_bf16 v[172:175], a[240:243], a[176:179], v[172:175]\n\t"
"ds_read_b128 a[176:179], v244 offset:1024\n\t"
"v_mfma_f32_16x16x32_bf16 v[172:175], a[208:211], a[180:183], v[172:175]\n\t"
"v_mfma_f32_16x16x32_bf16 v[172:175], a[244:247], a[184:187], v[172:175]\n\t"
"v_mfma_f32_16x16x32_bf16 v[176:179], a[240:243], a[180:183], v[176:179]\n\t"
"ds_read_b128 a[180:183], v244 offset:1536\n\t"
"v_mfma_f32_16x16x32_bf16 v[176:179], a[208:211], a[184:187], v[176:179]\n\t"
"v_mfma_f32_16x16x32_bf16 v[176:179], a[244:247], a[188:191], v[176:179]\n\t"
"v_mfma_f32_16x16x32_bf16 v[180:183], a[240:243], a[184:187], v[180:183]\n\t"
"ds_read_b128 a[184:187], v244 offset:2048\n\t"
"v_mfma_f32_16x16x32_bf16 v[180:183], a[208:211], a[188:191], v[180:183]\n\t"
"v_mfma_f32_16x16x32_bf16 v[180:183], a[244:247], a[192:195], v[180:183]\n\t"
"v_mfma_f32_16x16x32_bf16 v[184:187], a[240:243], a[188:191], v[184:187]\n\t"
"ds_read_b128 a[188:191], v244 offset:2560\n\t"
"v_mfma_f32_16x16x32_bf16 v[184:187], a[208:211], a[192:195], v[184:187]\n\t"
"v_mfma_f32_16x16x32_bf16 v[184:187], a[244:247], a[196:199], v[184:187]\n\t"
"v_mfma_f32_16x16x32_bf16 v[188:191], a[240:243], a[192:195], v[188:191]\n\t"
"ds_read_b128 a[192:195], v244 offset:3072\n\t"
"v_mfma_f32_16x16x32_bf16 v[188:191], a[208:211], a[196:199], v[188:191]\n\t"
"v_mfma_f32_16x16x32_bf16 v[188:191], a[244:247], a[200:203], v[188:191]\n\t"
"v_mfma_f32_16x16x32_bf16 v[192:195], a[240:243], a[196:199], v[192:195]\n\t"
"buffer_load_dwordx4 a[240:243], v252, s[8:11], s20 offen offset:1024 sc1\n\t"
"v_mfma_f32_16x16x32_bf16 v[192:195], a[208:211], a[200:203], v[192:195]\n\t"
"s_waitcnt vmcnt(11) lgkmcnt(4)\n\t"
"ds_read_b128 a[196:199], v244 offset:3584\n\t"
"v_mfma_f32_16x16x32_bf16 v[192:195], a[244:247], a[204:207], v[192:195]\n\t"
"ds_read_b128 a[200:203], v244 offset:4096\n\t"
"Lst12_%=:\n\t"
"v_mfma_f32_16x16x32_bf16 v[0:3], v[248:251], a[168:171], v[0:3]\n\t"
"buffer_load_dwordx4 a[208:211], v252, s[8:11], s20 offen offset:2048 sc1\n\t"
"v_mfma_f32_16x16x32_bf16 v[0:3], v[240:243], a[172:175], v[0:3]\n\t"
"s_waitcnt lgkmcnt(5)\n\t"
"ds_read_b128 a[204:207], v244 offset:4608\n\t"
"v_xor_b32 v246, 0xc0, v246\n\t"
"v_mfma_f32_16x16x32_bf16 v[0:3], a[248:251], a[176:179], v[0:3]\n\t"
"s_mov_b32 s20, 1572864\n\t"
"v_mfma_f32_16x16x32_bf16 v[4:7], v[248:251], a[172:175], v[4:7]\n\t"
"buffer_load_dwordx4 a[244:247], v252, s[8:11], s20 offen offset:0 sc1\n\t"
"v_mfma_f32_16x16x32_bf16 v[4:7], v[240:243], a[176:179], v[4:7]\n\t"
"s_waitcnt lgkmcnt(5)\n\t"
"v_mfma_f32_16x16x32_bf16 v[4:7], a[248:251], a[180:183], v[4:7]\n\t"
"v_mfma_f32_16x16x32_bf16 v[8:11], v[248:251], a[176:179], v[8:11]\n\t"
"s_waitcnt lgkmcnt(4)\n\t"
"v_mfma_f32_16x16x32_bf16 v[8:11], v[240:243], a[180:183], v[8:11]\n\t"
"v_mfma_f32_16x16x32_bf16 v[8:11], a[248:251], a[184:187], v[8:11]\n\t"
"v_mfma_f32_16x16x32_bf16 v[12:15], v[248:251], a[180:183], v[12:15]\n\t"
"s_waitcnt lgkmcnt(3)\n\t"
"v_mfma_f32_16x16x32_bf16 v[12:15], v[240:243], a[184:187], v[12:15]\n\t"
"v_mfma_f32_16x16x32_bf16 v[12:15], a[248:251], a[188:191], v[12:15]\n\t"
"v_mfma_f32_16x16x32_bf16 v[16:19], v[248:251], a[184:187], v[16:19]\n\t"
"s_waitcnt lgkmcnt(2)\n\t"
"v_mfma_f32_16x16x32_bf16 v[16:19], v[240:243], a[188:191], v[16:19]\n\t"
"v_mfma_f32_16x16x32_bf16 v[16:19], a[248:251], a[192:195], v[16:19]\n\t"
"ds_read_b64_tr_b16 v[224:225], v246 offset:1024\n\t"
"v_mfma_f32_16x16x32_bf16 v[20:23], v[248:251], a[188:191], v[20:23]\n\t"
"s_waitcnt lgkmcnt(2)\n\t"
"ds_read_b64_tr_b16 v[226:227], v246 offset:4096\n\t"
"v_mfma_f32_16x16x32_bf16 v[20:23], v[240:243], a[192:195], v[20:23]\n\t"
"v_mfma_f32_16x16x32_bf16 v[20:23], a[248:251], a[196:199], v[20:23]\n\t"
"v_mfma_f32_16x16x32_bf16 v[24:27], v[248:251], a[192:195], v[24:27]\n\t"
"s_waitcnt lgkmcnt(2)\n\t"
"v_mfma_f32_16x16x32_bf16 v[24:27], v[240:243], a[196:199], v[24:27]\n\t"
"v_mfma_f32_16x16x32_bf16 v[24:27], a[248:251], a[200:203], v[24:27]\n\t"
"v_mfma_f32_16x16x32_bf16 v[28:31], v[248:251], a[196:199], v[28:31]\n\t"
"buffer_load_dwordx4 v[248:251], v252, s[8:11], s20 offen offset:1024 sc1\n\t"
"v_mfma_f32_16x16x32_bf16 v[28:31], v[240:243], a[200:203], v[28:31]\n\t"
"s_waitcnt vmcnt(11)\n\t"
"ds_read_b64_tr_b16 v[228:229], v246 offset:7168\n\t"
"v_mfma_f32_16x16x32_bf16 v[28:31], a[248:251], a[204:207], v[28:31]\n\t"
"s_add_u32 s21, s21, 6291456\n\t"
"v_add_u32 v254, 6291456, v254\n\t"
"ds_read_b64_tr_b16 v[230:231], v246 offset:10240\n\t"
"v_mfma_f32_16x16x32_bf16 v[32:35], v[236:239], a[168:171], v[32:35]\n\t"
"buffer_load_dwordx4 v[240:243], v252, s[8:11], s20 offen offset:2048 sc1\n\t"
"v_mfma_f32_16x16x32_bf16 v[32:35], a[252:255], a[172:175], v[32:35]\n\t"
"s_mov_b32 s20, 1585152\n\t"
"v_mfma_f32_16x16x32_bf16 v[32:35], v[96:99], a[176:179], v[32:35]\n\t"
"v_mfma_f32_16x16x32_bf16 v[36:39], v[236:239], a[172:175], v[36:39]\n\t"
"buffer_load_dwordx4 a[248:251], v252, s[8:11], s20 offen offset:0 sc1\n\t"
"v_mfma_f32_16x16x32_bf16 v[36:39], a[252:255], a[176:179], v[36:39]\n\t"
"s_waitcnt lgkmcnt(2)\n\t"
"v_mfma_f32_16x16x32_bf16 v[36:39], v[96:99], a[180:183], v[36:39]\n\t"
"v_mfma_f32_16x16x32_bf16 v[224:227], v[224:227], v[220:223], 0\n\t"
"v_mfma_f32_16x16x32_bf16 v[40:43], v[236:239], a[176:179], v[40:43]\n\t"
"ds_read_b64_tr_b16 v[232:233], v246 offset:13312\n\t"
"v_mfma_f32_16x16x32_bf16 v[40:43], a[252:255], a[180:183], v[40:43]\n\t"
"ds_read_b64_tr_b16 v[234:235], v246 offset:16384\n\t"
"v_mfma_f32_16x16x32_bf16 v[40:43], v[96:99], a[184:187], v[40:43]\n\t"
"v_mfma_f32_16x16x32_bf16 v[44:47], v[236:239], a[180:183], v[44:47]\n\t"
"s_waitcnt lgkmcnt(2)\n\t"
"v_mfma_f32_16x16x32_bf16 v[44:47], a[252:255], a[184:187], v[44:47]\n\t"
"v_cvt_pk_bf16_f32 v224, v224, v225\n\t"
"v_cvt_pk_bf16_f32 v225, v226, v227\n\t"
"v_mfma_f32_16x16x32_bf16 v[44:47], v[96:99], a[188:191], v[44:47]\n\t"
"v_mfma_f32_16x16x32_bf16 v[228:231], v[228:231], v[220:223], 0\n\t"
"v_mfma_f32_16x16x32_bf16 v[48:51], v[236:239], a[184:187], v[48:51]\n\t"
"v_mfma_f32_16x16x32_bf16 v[48:51], a[252:255], a[188:191], v[48:51]\n\t"
"v_mfma_f32_16x16x32_bf16 v[48:51], v[96:99], a[192:195], v[48:51]\n\t"
"v_mfma_f32_16x16x32_bf16 v[52:55], v[236:239], a[188:191], v[52:55]\n\t"
"v_mfma_f32_16x16x32_bf16 v[52:55], a[252:255], a[192:195], v[52:55]\n\t"
"v_cvt_pk_bf16_f32 v226, v228, v229\n\t"
"v_cvt_pk_bf16_f32 v227, v230, v231\n\t"
"v_mfma_f32_16x16x32_bf16 v[52:55], v[96:99], a[196:199], v[52:55]\n\t"
"ds_read_b64_tr_b16 v[228:229], v246 offset:20992\n\t"
"v_mfma_f32_16x16x32_bf16 v[56:59], v[236:239], a[192:195], v[56:59]\n\t"
"s_waitcnt lgkmcnt(1)\n\t"
"ds_read_b64_tr_b16 v[230:231], v246 offset:24064\n\t"
"v_mfma_f32_16x16x32_bf16 v[56:59], a[252:255], a[196:199], v[56:59]\n\t"
"v_mfma_f32_16x16x32_bf16 v[56:59], v[96:99], a[200:203], v[56:59]\n\t"
"v_mfma_f32_16x16x32_bf16 v[232:235], v[232:235], v[220:223], 0\n\t"
"v_mfma_f32_16x16x32_bf16 v[60:63], v[236:239], a[196:199], v[60:63]\n\t"
"buffer_load_dwordx4 v[236:239], v252, s[8:11], s20 offen offset:1024 sc1\n\t"
"v_mfma_f32_16x16x32_bf16 v[60:63], a[252:255], a[200:203], v[60:63]\n\t"
"s_waitcnt vmcnt(11)\n\t"
"v_mfma_f32_16x16x32_bf16 v[60:63], v[96:99], a[204:207], v[60:63]\n\t"
"v_mfma_f32_16x16x32_bf16 v[64:67], a[228:231], a[168:171], v[64:67]\n\t"
"buffer_load_dwordx4 v[96:99], v252, s[8:11], s20 offen offset:2048 sc1\n\t"
"v_mfma_f32_16x16x32_bf16 v[64:67], a[212:215], a[172:175], v[64:67]\n\t"
"s_mov_b32 s20, 1597440\n\t"
"ds_read_b128 a[168:171], v244 offset:9216\n\t"
"v_cvt_pk_bf16_f32 v232, v232, v233\n\t"
"v_mfma_f32_16x16x32_bf16 v[64:67], a[216:219], a[176:179], v[64:67]\n\t"
"v_cvt_pk_bf16_f32 v233, v234, v235\n\t"
"v_mfma_f32_16x16x32_bf16 v[68:71], a[228:231], a[172:175], v[68:71]\n\t"
"buffer_load_dwordx4 a[252:255], v252, s[8:11], s20 offen offset:0 sc1\n\t"
"v_mfma_f32_16x16x32_bf16 v[68:71], a[212:215], a[176:179], v[68:71]\n\t"
"s_waitcnt lgkmcnt(1)\n\t"
"ds_read_b128 a[172:175], v244 offset:9728\n\t"
"v_mfma_f32_16x16x32_bf16 v[68:71], a[216:219], a[180:183], v[68:71]\n\t"
"v_mfma_f32_16x16x32_bf16 v[228:231], v[228:231], v[220:223], 0\n\t"
"v_mfma_f32_16x16x32_bf16 v[72:75], a[228:231], a[176:179], v[72:75]\n\t"
"ds_read_b128 a[176:179], v244 offset:10240\n\t"
"v_mfma_f32_16x16x32_bf16 v[72:75], a[212:215], a[180:183], v[72:75]\n\t"
"v_mfma_f32_16x16x32_bf16 v[72:75], a[216:219], a[184:187], v[72:75]\n\t"
"Lpg9_%=:\n\t"
"s_cbranch_vccnz Lvr0_%=\n\t"
"v_mfma_f32_16x16x32_bf16 v[76:79], a[228:231], a[180:183], v[76:79]\n\t"
"ds_read_b128 a[180:183], v244 offset:10752\n\t"
"v_mfma_f32_16x16x32_bf16 v[76:79], a[212:215], a[184:187], v[76:79]\n\t"
"v_cvt_pk_bf16_f32 v234, v228, v229\n\t"
"v_cvt_pk_bf16_f32 v235, v230, v231\n\t"
"v_mfma_f32_16x16x32_bf16 v[76:79], a[216:219], a[188:191], v[76:79]\n\t"
"ds_read_b64_tr_b16 v[228:229], v246 offset:27136\n\t"
"v_mfma_f32_16x16x32_bf16 v[80:83], a[228:231], a[184:187], v[80:83]\n\t"
"ds_read_b64_tr_b16 v[230:231], v246 offset:30208\n\t"
"v_mfma_f32_16x16x32_bf16 v[80:83], a[212:215], a[188:191], v[80:83]\n\t"
"ds_read_b128 a[184:187], v244 offset:11264\n\t"
"v_mfma_f32_16x16x32_bf16 v[80:83], a[216:219], a[192:195], v[80:83]\n\t"
"v_mfma_f32_16x16x32_bf16 v[84:87], a[228:231], a[188:191], v[84:87]\n\t"
"ds_read_b128 a[188:191], v244 offset:11776\n\t"
"v_mfma_f32_16x16x32_bf16 v[84:87], a[212:215], a[192:195], v[84:87]\n\t"
"v_mfma_f32_16x16x32_bf16 v[84:87], a[216:219], a[196:199], v[84:87]\n\t"
"v_mfma_f32_16x16x32_bf16 v[88:91], a[228:231], a[192:195], v[88:91]\n\t"
"ds_read_b128 a[192:195], v244 offset:12288\n\t"
"v_mfma_f32_16x16x32_bf16 v[88:91], a[212:215], a[196:199], v[88:91]\n\t"
"v_mfma_f32_16x16x32_bf16 v[88:91], a[216:219], a[200:203], v[88:91]\n\t"
"v_mfma_f32_16x16x32_bf16 v[92:95], a[228:231], a[196:199], v[92:95]\n\t"
"buffer_load_dwordx4 a[228:231], v252, s[8:11], s20 offen offset:1024 sc1\n\t"
"v_mfma_f32_16x16x32_bf16 v[92:95], a[212:215], a[200:203], v[92:95]\n\t"
"s_waitcnt vmcnt(11) lgkmcnt(3)\n\t"
"ds_read_b128 a[196:199], v244 offset:12800\n\t"
"v_mfma_f32_16x16x32_bf16 v[92:95], a[216:219], a[204:207], v[92:95]\n\t"
"ds_read_b128 a[200:203], v244 offset:13312\n\t"
"v_mfma_f32_16x16x32_bf16 v[228:231], v[228:231], v[220:223], 0\n\t"
"ds_read_b128 a[204:207], v244 offset:13824\n\t"
"v_mfma_f32_16x16x32_bf16 v[100:103], a[220:223], a[168:171], v[100:103]\n\t"
"buffer_load_dwordx4 a[212:215], v252, s[8:11], s20 offen offset:2048 sc1\n\t"
"v_mfma_f32_16x16x32_bf16 v[100:103], a[232:235], a[172:175], v[100:103]\n\t"
"s_mov_b32 s20, 1609728\n\t"
"v_mfma_f32_16x16x32_bf16 v[100:103], a[224:227], a[176:179], v[100:103]\n\t"
"buffer_load_dwordx4 a[216:219], v252, s[8:11], s20 offen offset:0 sc1\n\t"
"v_mfma_f32_16x16x32_bf16 v[104:107], a[220:223], a[172:175], v[104:107]\n\t"
"s_waitcnt lgkmcnt(5)\n\t"
"s_add_u32 m0, s26, 512\n\t"
"v_mfma_f32_16x16x32_bf16 v[104:107], a[232:235], a[176:179], v[104:107]\n\t"
"v_cvt_pk_bf16_f32 v228, v228, v229\n\t"
"v_mfma_f32_16x16x32_bf16 v[104:107], a[224:227], a[180:183], v[104:107]\n\t"
"s_waitcnt lgkmcnt(0)\n\t"
"s_barrier\n\t"
"v_mfma_f32_16x16x32_bf16 v[108:111], a[220:223], a[176:179], v[108:111]\n\t"
"v_cvt_pk_bf16_f32 v229, v230, v231\n\t"
"v_mfma_f32_16x16x32_bf16 v[108:111], a[232:235], a[180:183], v[108:111]\n\t"
"v_fma_f32 v0, v100, s52, v0\n\t"
"v_fma_f32 v1, v101, s52, v1\n\t"
"v_mfma_f32_16x16x32_bf16 v[108:111], a[224:227], a[184:187], v[108:111]\n\t"
"s_mov_b64 exec, s[36:37]\n\t"
"buffer_load_dwordx4 v254, s[12:15], s27 offen offset:0 nt lds\n\t"
"s_mov_b64 exec, -1\n\t"
"s_add_u32 m0, s24, 80384\n\t"
"s_add_u32 s28, s21, 2359296\n\t"
"v_mfma_f32_16x16x32_bf16 v[112:115], a[220:223], a[180:183], v[112:115]\n\t"
"buffer_load_dwordx4 v247, s[12:15], s28 offen offset:0 nt lds\n\t"
"v_mfma_f32_16x16x32_bf16 v[112:115], a[232:235], a[184:187], v[112:115]\n\t"
"s_add_u32 m0, s24, 86528\n\t"
"s_add_u32 s28, s21, 2555904\n\t"
"v_fma_f32 v2, v102, s52, v2\n\t"
"v_mfma_f32_16x16x32_bf16 v[112:115], a[224:227], a[188:191], v[112:115]\n\t"
"ds_write2st64_b64 v245, v[224:225], v[226:227] offset0:0 offset1:1\n\t"
"v_mfma_f32_16x16x32_bf16 v[116:119], a[220:223], a[184:187], v[116:119]\n\t"
"buffer_load_dwordx4 v247, s[12:15], s28 offen offset:0 nt lds\n\t"
"v_mfma_f32_16x16x32_bf16 v[116:119], a[232:235], a[188:191], v[116:119]\n\t"
"s_add_u32 m0, s24, 60416\n\t"
"s_add_u32 s28, s21, 1769472\n\t"
"ds_read_b64_tr_b16 v[224:225], v246 offset:33280\n\t"
"v_mfma_f32_16x16x32_bf16 v[116:119], a[224:227], a[192:195], v[116:119]\n\t"
"ds_write2st64_b64 v245, v[232:233], v[234:235] offset0:2 offset1:3\n\t"
"ds_read_b64_tr_b16 v[226:227], v246 offset:36352\n\t"
"v_mfma_f32_16x16x32_bf16 v[120:123], a[220:223], a[188:191], v[120:123]\n\t"
"buffer_load_dwordx4 v247, s[12:15], s28 offen offset:0 nt lds\n\t"
"v_mfma_f32_16x16x32_bf16 v[120:123], a[232:235], a[192:195], v[120:123]\n\t"
"s_add_u32 m0, s24, 66560\n\t"
"s_add_u32 s28, s21, 1966080\n\t"
"ds_read_b64_tr_b16 v[232:233], v246 offset:40960\n\t"
"v_mfma_f32_16x16x32_bf16 v[120:123], a[224:227], a[196:199], v[120:123]\n\t"
"s_waitcnt lgkmcnt(1)\n\t"
"ds_write_b64 v245, v[228:229] offset:2048\n\t"
"ds_read_b64_tr_b16 v[234:235], v246 offset:44032\n\t"
"v_mfma_f32_16x16x32_bf16 v[224:227], v[224:227], v[220:223], 0\n\t"
"buffer_load_dwordx4 v247, s[12:15], s28 offen offset:0 nt lds\n\t"
"v_mfma_f32_16x16x32_bf16 v[124:127], a[220:223], a[192:195], v[124:127]\n\t"
"ds_read_b64_tr_b16 v[228:229], v246 offset:47104\n\t"
"v_fma_f32 v3, v103, s52, v3\n\t"
"v_mfma_f32_16x16x32_bf16 v[124:127], a[232:235], a[196:199], v[124:127]\n\t"
"ds_read_b64_tr_b16 v[230:231], v246 offset:50176\n\t"
"v_fma_f32 v100, v0, s54, -v100\n\t"
"v_mfma_f32_16x16x32_bf16 v[124:127], a[224:227], a[200:203], v[124:127]\n\t"
"s_waitcnt lgkmcnt(2)\n\t"
"v_fma_f32 v101, v1, s54, -v101\n\t"
"v_fma_f32 v102, v2, s54, -v102\n\t"
"v_mfma_f32_16x16x32_bf16 v[232:235], v[232:235], v[220:223], 0\n\t"
"v_fma_f32 v103, v3, s54, -v103\n\t"
"v_fma_f32 v4, v104, s52, v4\n\t"
"v_mfma_f32_16x16x32_bf16 v[128:131], a[220:223], a[196:199], v[128:131]\n\t"
"v_cvt_pk_bf16_f32 v224, v224, v225\n\t"
"v_cvt_pk_bf16_f32 v225, v226, v227\n\t"
"v_mfma_f32_16x16x32_bf16 v[128:131], a[232:235], a[200:203], v[128:131]\n\t"
"s_waitcnt vmcnt(15)\n\t"
"v_fma_f32 v5, v105, s52, v5\n\t"
"v_fma_f32 v6, v106, s52, v6\n\t"
"v_mfma_f32_16x16x32_bf16 v[128:131], a[224:227], a[204:207], v[128:131]\n\t"
"s_waitcnt lgkmcnt(0)\n\t"
"v_fma_f32 v7, v107, s52, v7\n\t"
"v_fma_f32 v104, v4, s54, -v104\n\t"
"v_mfma_f32_16x16x32_bf16 v[228:231], v[228:231], v[220:223], 0\n\t"
"v_fma_f32 v105, v5, s54, -v105\n\t"
"v_fma_f32 v106, v6, s54, -v106\n\t"
"v_mfma_f32_16x16x32_bf16 v[132:135], a[236:239], a[168:171], v[132:135]\n\t"
"v_cvt_pk_bf16_f32 v226, v232, v233\n\t"
"v_cvt_pk_bf16_f32 v227, v234, v235\n\t"
"v_mfma_f32_16x16x32_bf16 v[132:135], a[240:243], a[172:175], v[132:135]\n\t"
"ds_read_b64_tr_b16 v[232:233], v246 offset:53248\n\t"
"v_fma_f32 v107, v7, s54, -v107\n\t"
"v_mfma_f32_16x16x32_bf16 v[132:135], a[208:211], a[176:179], v[132:135]\n\t"
"ds_write2st64_b64 v245, v[224:225], v[226:227] offset0:5 offset1:6\n\t"
"v_mfma_f32_16x16x32_bf16 v[136:139], a[236:239], a[172:175], v[136:139]\n\t"
"v_fma_f32 v8, v108, s52, v8\n\t"
"v_fma_f32 v9, v109, s52, v9\n\t"
"v_mfma_f32_16x16x32_bf16 v[136:139], a[240:243], a[176:179], v[136:139]\n\t"
"ds_read_b64_tr_b16 v[234:235], v246 offset:56320\n\t"
"v_mfma_f32_16x16x32_bf16 v[136:139], a[208:211], a[180:183], v[136:139]\n\t"
"v_cvt_pk_bf16_f32 v228, v228, v229\n\t"
"v_cvt_pk_bf16_f32 v229, v230, v231\n\t"
"v_mfma_f32_16x16x32_bf16 v[140:143], a[236:239], a[176:179], v[140:143]\n\t"
"v_fma_f32 v10, v110, s52, v10\n\t"
"v_fma_f32 v11, v111, s52, v11\n\t"
"v_mfma_f32_16x16x32_bf16 v[140:143], a[240:243], a[180:183], v[140:143]\n\t"
"v_fma_f32 v108, v8, s54, -v108\n\t"
"v_fma_f32 v109, v9, s54, -v109\n\t"
"v_mfma_f32_16x16x32_bf16 v[140:143], a[208:211], a[184:187], v[140:143]\n\t"
"v_fma_f32 v110, v10, s54, -v110\n\t"
"v_fma_f32 v111, v11, s54, -v111\n\t"
"v_mfma_f32_16x16x32_bf16 v[144:147], a[236:239], a[180:183], v[144:147]\n\t"
"v_fma_f32 v12, v112, s52, v12\n\t"
"v_fma_f32 v13, v113, s52, v13\n\t"
"v_mfma_f32_16x16x32_bf16 v[144:147], a[240:243], a[184:187], v[144:147]\n\t"
"v_fma_f32 v14, v114, s52, v14\n\t"
"v_fma_f32 v15, v115, s52, v15\n\t"
"v_mfma_f32_16x16x32_bf16 v[144:147], a[208:211], a[188:191], v[144:147]\n\t"
"buffer_load_dwordx4 a[220:223], v252, s[8:11], s20 offen offset:1024 sc1\n\t"
"v_mfma_f32_16x16x32_bf16 v[148:151], a[236:239], a[184:187], v[148:151]\n\t"
"s_waitcnt lgkmcnt(0)\n\t"
"v_fma_f32 v112, v12, s54, -v112\n\t"
"v_fma_f32 v113, v13, s54, -v113\n\t"
"v_mfma_f32_16x16x32_bf16 v[148:151], a[240:243], a[188:191], v[148:151]\n\t"
"v_fma_f32 v114, v14, s54, -v114\n\t"
"v_fma_f32 v115, v15, s54, -v115\n\t"
"v_mfma_f32_16x16x32_bf16 v[148:151], a[208:211], a[192:195], v[148:151]\n\t"
"v_fma_f32 v16, v116, s52, v16\n\t"
"v_fma_f32 v17, v117, s52, v17\n\t"
"v_mfma_f32_16x16x32_bf16 v[232:235], v[232:235], v[220:223], 0\n\t"
"v_fma_f32 v18, v118, s52, v18\n\t"
"v_fma_f32 v19, v119, s52, v19\n\t"
"v_mfma_f32_16x16x32_bf16 v[152:155], a[236:239], a[188:191], v[152:155]\n\t"
"v_fma_f32 v116, v16, s54, -v116\n\t"
"v_fma_f32 v117, v17, s54, -v117\n\t"
"v_mfma_f32_16x16x32_bf16 v[152:155], a[240:243], a[192:195], v[152:155]\n\t"
"v_fma_f32 v118, v18, s54, -v118\n\t"
"v_fma_f32 v119, v19, s54, -v119\n\t"
"v_mfma_f32_16x16x32_bf16 v[152:155], a[208:211], a[196:199], v[152:155]\n\t"
"v_fma_f32 v20, v120, s52, v20\n\t"
"v_fma_f32 v21, v121, s52, v21\n\t"
"v_mfma_f32_16x16x32_bf16 v[156:159], a[236:239], a[192:195], v[156:159]\n\t"
"v_fma_f32 v22, v122, s52, v22\n\t"
"v_fma_f32 v23, v123, s52, v23\n\t"
"v_mfma_f32_16x16x32_bf16 v[156:159], a[240:243], a[196:199], v[156:159]\n\t"
"v_cvt_pk_bf16_f32 v230, v232, v233\n\t"
"v_cvt_pk_bf16_f32 v231, v234, v235\n\t"
"v_mfma_f32_16x16x32_bf16 v[156:159], a[208:211], a[200:203], v[156:159]\n\t"
"ds_write2st64_b64 v245, v[228:229], v[230:231] offset0:7 offset1:8\n\t"
"v_mfma_f32_16x16x32_bf16 v[160:163], a[236:239], a[196:199], v[160:163]\n\t"
"s_waitcnt vmcnt(13)\n\t"
"v_fma_f32 v120, v20, s54, -v120\n\t"
"v_fma_f32 v121, v21, s54, -v121\n\t"
"v_mfma_f32_16x16x32_bf16 v[160:163], a[240:243], a[200:203], v[160:163]\n\t"
"v_fma_f32 v122, v22, s54, -v122\n\t"
"v_fma_f32 v123, v23, s54, -v123\n\t"
"v_mfma_f32_16x16x32_bf16 v[160:163], a[208:211], a[204:207], v[160:163]\n\t"
"v_fma_f32 v24, v124, s52, v24\n\t"
"v_fma_f32 v25, v125, s52, v25\n\t"
"v_mfma_f32_16x16x32_bf16 v[164:167], a[244:247], a[168:171], v[164:167]\n\t"
"v_fma_f32 v26, v126, s52, v26\n\t"
"v_fma_f32 v27, v127, s52, v27\n\t"
"v_mfma_f32_16x16x32_bf16 v[164:167], v[248:251], a[172:175], v[164:167]\n\t"
"v_fma_f32 v124, v24, s54, -v124\n\t"
"v_mfma_f32_16x16x32_bf16 v[164:167], v[240:243], a[176:179], v[164:167]\n\t"
"s_waitcnt lgkmcnt(0)\n\t"
"s_barrier\n\t"
"v_mfma_f32_16x16x32_bf16 v[168:171], a[244:247], a[172:175], v[168:171]\n\t"
"ds_read_b128 a[168:171], v244 offset:0\n\t"
"v_mfma_f32_16x16x32_bf16 v[168:171], v[248:251], a[176:179], v[168:171]\n\t"
"ds_read_b128 a[172:175], v244 offset:512\n\t"
"v_fma_f32 v125, v25, s54, -v125\n\t"
"v_mfma_f32_16x16x32_bf16 v[168:171], v[240:243], a[180:183], v[168:171]\n\t"
"v_fma_f32 v126, v26, s54, -v126\n\t"
"v_fma_f32 v127, v27, s54, -v127\n\t"
"v_mfma_f32_16x16x32_bf16 v[172:175], a[244:247], a[176:179], v[172:175]\n\t"
"ds_read_b128 a[176:179], v244 offset:1024\n\t"
"v_fma_f32 v28, v128, s52, v28\n\t"
"v_mfma_f32_16x16x32_bf16 v[172:175], v[248:251], a[180:183], v[172:175]\n\t"
"v_fma_f32 v29, v129, s52, v29\n\t"
"v_fma_f32 v30, v130, s52, v30\n\t"
"v_mfma_f32_16x16x32_bf16 v[172:175], v[240:243], a[184:187], v[172:175]\n\t"
"v_fma_f32 v31, v131, s52, v31\n\t"
"v_fma_f32 v128, v28, s54, -v128\n\t"
"v_mfma_f32_16x16x32_bf16 v[176:179], a[244:247], a[180:183], v[176:179]\n\t"
"ds_read_b128 a[180:183], v244 offset:1536\n\t"
"v_fma_f32 v129, v29, s54, -v129\n\t"
"v_mfma_f32_16x16x32_bf16 v[176:179], v[248:251], a[184:187], v[176:179]\n\t"
"v_fma_f32 v130, v30, s54, -v130\n\t"
"v_fma_f32 v131, v31, s54, -v131\n\t"
"v_mfma_f32_16x16x32_bf16 v[176:179], v[240:243], a[188:191], v[176:179]\n\t"
"v_fma_f32 v32, v132, s52, v32\n\t"
"v_fma_f32 v33, v133, s52, v33\n\t"
"v_mfma_f32_16x16x32_bf16 v[180:183], a[244:247], a[184:187], v[180:183]\n\t"
"ds_read_b128 a[184:187], v244 offset:2048\n\t"
"v_fma_f32 v34, v134, s52, v34\n\t"
"v_mfma_f32_16x16x32_bf16 v[180:183], v[248:251], a[188:191], v[180:183]\n\t"
"v_fma_f32 v35, v135, s52, v35\n\t"
"v_fma_f32 v132, v32, s54, -v132\n\t"
"v_mfma_f32_16x16x32_bf16 v[180:183], v[240:243], a[192:195], v[180:183]\n\t"
"v_fma_f32 v133, v33, s54, -v133\n\t"
"v_fma_f32 v134, v34, s54, -v134\n\t"
"v_mfma_f32_16x16x32_bf16 v[184:187], a[244:247], a[188:191], v[184:187]\n\t"
"ds_read_b128 a[188:191], v244 offset:2560\n\t"
"v_fma_f32 v135, v35, s54, -v135\n\t"
"v_mfma_f32_16x16x32_bf16 v[184:187], v[248:251], a[192:195], v[184:187]\n\t"
"v_fma_f32 v36, v136, s52, v36\n\t"
"v_fma_f32 v37, v137, s52, v37\n\t"
"v_mfma_f32_16x16x32_bf16 v[184:187], v[240:243], a[196:199], v[184:187]\n\t"
"v_fma_f32 v38, v138, s52, v38\n\t"
"v_fma_f32 v39, v139, s52, v39\n\t"
"v_mfma_f32_16x16x32_bf16 v[188:191], a[244:247], a[192:195], v[188:191]\n\t"
"ds_read_b128 a[192:195], v244 offset:3072\n\t"
"v_fma_f32 v136, v36, s54, -v136\n\t"
"v_mfma_f32_16x16x32_bf16 v[188:191], v[248:251], a[196:199], v[188:191]\n\t"
"v_fma_f32 v137, v37, s54, -v137\n\t"
"v_fma_f32 v138, v38, s54, -v138\n\t"
"v_mfma_f32_16x16x32_bf16 v[188:191], v[240:243], a[200:203], v[188:191]\n\t"
"buffer_load_dwordx4 a[208:211], v252, s[8:11], s20 offen offset:2048 sc1\n\t"
"s_mov_b32 s20, 1622016\n\t"
"v_mfma_f32_16x16x32_bf16 v[192:195], a[244:247], a[196:199], v[192:195]\n\t"
"buffer_load_dwordx4 a[224:227], v252, s[8:11], s20 offen offset:0 sc1\n\t"
"v_mfma_f32_16x16x32_bf16 v[192:195], v[248:251], a[200:203], v[192:195]\n\t"
"s_waitcnt vmcnt(12) lgkmcnt(4)\n\t"
"ds_read_b128 a[196:199], v244 offset:3584\n\t"
"v_fma_f32 v139, v39, s54, -v139\n\t"
"v_mfma_f32_16x16x32_bf16 v[192:195], v[240:243], a[204:207], v[192:195]\n\t"
"ds_read_b128 a[200:203], v244 offset:4096\n\t"
"v_fma_f32 v40, v140, s52, v40\n\t"
"Lst13_%=:\n\t"
"v_mfma_f32_16x16x32_bf16 v[196:199], a[248:251], a[168:171], v[196:199]\n\t"
"s_waitcnt lgkmcnt(5)\n\t"
"ds_read_b128 a[204:207], v244 offset:4608\n\t"
"v_fma_f32 v41, v141, s52, v41\n\t"
"v_mfma_f32_16x16x32_bf16 v[196:199], v[236:239], a[172:175], v[196:199]\n\t"
"v_fma_f32 v42, v142, s52, v42\n\t"
"v_fma_f32 v43, v143, s52, v43\n\t"
"v_mfma_f32_16x16x32_bf16 v[196:199], v[96:99], a[176:179], v[196:199]\n\t"
"buffer_load_dwordx4 v[248:251], v252, s[8:11], s20 offen offset:1024 sc1\n\t"
"v_mfma_f32_16x16x32_bf16 v[204:207], a[248:251], a[172:175], v[204:207]\n\t"
"buffer_load_dwordx4 v[240:243], v252, s[8:11], s20 offen offset:2048 sc1\n\t"
"v_mfma_f32_16x16x32_bf16 v[204:207], v[236:239], a[176:179], v[204:207]\n\t"
"s_waitcnt lgkmcnt(5)\n\t"
"s_mov_b32 s20, 1634304\n\t"
"v_fma_f32 v140, v40, s54, -v140\n\t"
"v_mfma_f32_16x16x32_bf16 v[204:207], v[96:99], a[180:183], v[204:207]\n\t"
"v_fma_f32 v141, v41, s54, -v141\n\t"
"v_fma_f32 v142, v42, s54, -v142\n\t"
"v_mfma_f32_16x16x32_bf16 v[212:215], a[248:251], a[176:179], v[212:215]\n\t"
"s_waitcnt lgkmcnt(4)\n\t"
"v_fma_f32 v143, v43, s54, -v143\n\t"
"v_fma_f32 v44, v144, s52, v44\n\t"
"v_mfma_f32_16x16x32_bf16 v[212:215], v[236:239], a[180:183], v[212:215]\n\t"
"v_fma_f32 v45, v145, s52, v45\n\t"
"v_fma_f32 v46, v146, s52, v46\n\t"
"v_mfma_f32_16x16x32_bf16 v[212:215], v[96:99], a[184:187], v[212:215]\n\t"
"v_fma_f32 v47, v147, s52, v47\n\t"
"v_fma_f32 v144, v44, s54, -v144\n\t"
"v_mfma_f32_16x16x32_bf16 a[0:3], a[248:251], a[180:183], a[0:3]\n\t"
"s_waitcnt lgkmcnt(3)\n\t"
"v_fma_f32 v145, v45, s54, -v145\n\t"
"v_fma_f32 v146, v46, s54, -v146\n\t"
"v_mfma_f32_16x16x32_bf16 a[0:3], v[236:239], a[184:187], a[0:3]\n\t"
"v_fma_f32 v147, v47, s54, -v147\n\t"
"v_fma_f32 v48, v148, s52, v48\n\t"
"v_mfma_f32_16x16x32_bf16 a[0:3], v[96:99], a[188:191], a[0:3]\n\t"
"buffer_load_dwordx4 a[232:235], v252, s[8:11], s20 offen offset:0 sc1\n\t"
"v_mfma_f32_16x16x32_bf16 a[8:11], a[248:251], a[184:187], a[8:11]\n\t"
"buffer_load_dwordx4 a[236:239], v252, s[8:11], s20 offen offset:1024 sc1\n\t"
"v_mfma_f32_16x16x32_bf16 a[8:11], v[236:239], a[188:191], a[8:11]\n\t"
"s_waitcnt lgkmcnt(2)\n\t"
"s_add_u32 m0, s24, 72704\n\t"
"s_add_u32 s28, s21, 2162688\n\t"
"v_mfma_f32_16x16x32_bf16 a[8:11], v[96:99], a[192:195], a[8:11]\n\t"
"buffer_load_dwordx4 a[240:243], v252, s[8:11], s20 offen offset:2048 sc1\n\t"
"v_mfma_f32_16x16x32_bf16 a[16:19], a[248:251], a[188:191], a[16:19]\n\t"
"buffer_load_dwordx4 v247, s[12:15], s28 offen offset:0 nt lds\n\t"
"v_mfma_f32_16x16x32_bf16 a[16:19], v[236:239], a[192:195], a[16:19]\n\t"
"s_waitcnt lgkmcnt(1)\n\t"
"s_add_u32 m0, s24, 92672\n\t"
"s_add_u32 s28, s21, 2752512\n\t"
"v_mfma_f32_16x16x32_bf16 a[16:19], v[96:99], a[196:199], a[16:19]\n\t"
"buffer_load_dwordx4 v247, s[12:15], s28 offen offset:0 nt lds\n\t"
"v_mfma_f32_16x16x32_bf16 a[24:27], a[248:251], a[192:195], a[24:27]\n\t"
"s_waitcnt lgkmcnt(0)\n\t"
"s_add_u32 m0, s24, 100352\n\t"
"s_add_u32 s28, s21, 2949120\n\t"
"v_mfma_f32_16x16x32_bf16 a[24:27], v[236:239], a[196:199], a[24:27]\n\t"
"buffer_load_dwordx4 v247, s[12:15], s28 offen offset:0 nt lds\n\t"
"v_mfma_f32_16x16x32_bf16 a[24:27], v[96:99], a[200:203], a[24:27]\n\t"
"s_add_u32 m0, s24, 106496\n\t"
"s_add_u32 s28, s21, 3145728\n\t"
"v_fma_f32 v49, v149, s52, v49\n\t"
"v_mfma_f32_16x16x32_bf16 a[32:35], a[248:251], a[196:199], a[32:35]\n\t"
"s_waitcnt vmcnt(17)\n\t"
"v_fma_f32 v50, v150, s52, v50\n\t"
"v_fma_f32 v51, v151, s52, v51\n\t"
"v_mfma_f32_16x16x32_bf16 a[32:35], v[236:239], a[200:203], a[32:35]\n\t"
"v_fma_f32 v148, v48, s54, -v148\n\t"
"v_fma_f32 v149, v49, s54, -v149\n\t"
"v_mfma_f32_16x16x32_bf16 a[32:35], v[96:99], a[204:207], a[32:35]\n\t"
"buffer_load_dwordx4 v247, s[12:15], s28 offen offset:0 nt lds\n\t"
"s_add_u32 m0, s24, 112640\n\t"
"s_add_u32 s28, s21, 3342336\n\t"
"v_mfma_f32_16x16x32_bf16 a[40:43], a[252:255], a[168:171], a[40:43]\n\t"
"buffer_load_dwordx4 v247, s[12:15], s28 offen offset:0 nt lds\n\t"
"v_mfma_f32_16x16x32_bf16 a[40:43], a[228:231], a[172:175], a[40:43]\n\t"
"v_fma_f32 v150, v50, s54, -v150\n\t"
"v_fma_f32 v151, v51, s54, -v151\n\t"
"v_mfma_f32_16x16x32_bf16 a[40:43], a[212:215], a[176:179], a[40:43]\n\t"
"v_fma_f32 v52, v152, s52, v52\n\t"
"v_fma_f32 v53, v153, s52, v53\n\t"
"v_mfma_f32_16x16x32_bf16 a[48:51], a[252:255], a[172:175], a[48:51]\n\t"
"v_fma_f32 v54, v154, s52, v54\n\t"
"v_fma_f32 v55, v155, s52, v55\n\t"
"v_mfma_f32_16x16x32_bf16 a[48:51], a[228:231], a[176:179], a[48:51]\n\t"
"v_fma_f32 v152, v52, s54, -v152\n\t"
"v_fma_f32 v153, v53, s54, -v153\n\t"
"v_mfma_f32_16x16x32_bf16 a[48:51], a[212:215], a[180:183], a[48:51]\n\t"
"v_fma_f32 v154, v54, s54, -v154\n\t"
"v_fma_f32 v155, v55, s54, -v155\n\t"
"v_mfma_f32_16x16x32_bf16 a[56:59], a[252:255], a[176:179], a[56:59]\n\t"
"v_fma_f32 v56, v156, s52, v56\n\t"
"v_fma_f32 v57, v157, s52, v57\n\t"
"v_mfma_f32_16x16x32_bf16 a[56:59], a[228:231], a[180:183], a[56:59]\n\t"
"v_fma_f32 v58, v158, s52, v58\n\t"
"v_fma_f32 v59, v159, s52, v59\n\t"
"v_mfma_f32_16x16x32_bf16 a[56:59], a[212:215], a[184:187], a[56:59]\n\t"
"v_fma_f32 v156, v56, s54, -v156\n\t"
"v_fma_f32 v157, v57, s54, -v157\n\t"
"v_mfma_f32_16x16x32_bf16 a[64:67], a[252:255], a[180:183], a[64:67]\n\t"
"v_fma_f32 v158, v58, s54, -v158\n\t"
"v_fma_f32 v159, v59, s54, -v159\n\t"
"v_mfma_f32_16x16x32_bf16 a[64:67], a[228:231], a[184:187], a[64:67]\n\t"
"v_fma_f32 v60, v160, s52, v60\n\t"
"v_fma_f32 v61, v161, s52, v61\n\t"
"v_mfma_f32_16x16x32_bf16 a[64:67], a[212:215], a[188:191], a[64:67]\n\t"
"v_fma_f32 v62, v162, s52, v62\n\t"
"v_fma_f32 v63, v163, s52, v63\n\t"
"v_mfma_f32_16x16x32_bf16 a[72:75], a[252:255], a[184:187], a[72:75]\n\t"
"v_fma_f32 v160, v60, s54, -v160\n\t"
"v_fma_f32 v161, v61, s54, -v161\n\t"
"v_mfma_f32_16x16x32_bf16 a[72:75], a[228:231], a[188:191], a[72:75]\n\t"
"v_fma_f32 v162, v62, s54, -v162\n\t"
"v_fma_f32 v163, v63, s54, -v163\n\t"
"v_mfma_f32_16x16x32_bf16 a[72:75], a[212:215], a[192:195], a[72:75]\n\t"
"v_fma_f32 v64, v164, s52, v64\n\t"
"v_fma_f32 v65, v165, s52, v65\n\t"
"v_mfma_f32_16x16x32_bf16 a[80:83], a[252:255], a[188:191], a[80:83]\n\t"
"v_fma_f32 v66, v166, s52, v66\n\t"
"v_fma_f32 v67, v167, s52, v67\n\t"
"v_mfma_f32_16x16x32_bf16 a[80:83], a[228:231], a[192:195], a[80:83]\n\t"
"v_fma_f32 v164, v64, s54, -v164\n\t"
"v_fma_f32 v165, v65, s54, -v165\n\t"
"v_mfma_f32_16x16x32_bf16 a[80:83], a[212:215], a[196:199], a[80:83]\n\t"
"v_fma_f32 v166, v66, s54, -v166\n\t"
"v_fma_f32 v167, v67, s54, -v167\n\t"
"v_mfma_f32_16x16x32_bf16 a[88:91], a[252:255], a[192:195], a[88:91]\n\t"
"v_fma_f32 v68, v168, s52, v68\n\t"
"v_fma_f32 v69, v169, s52, v69\n\t"
"v_mfma_f32_16x16x32_bf16 a[88:91], a[228:231], a[196:199], a[88:91]\n\t"
"v_fma_f32 v70, v170, s52, v70\n\t"
"v_fma_f32 v71, v171, s52, v71\n\t"
"v_mfma_f32_16x16x32_bf16 a[88:91], a[212:215], a[200:203], a[88:91]\n\t"
"v_fma_f32 v168, v68, s54, -v168\n\t"
"v_fma_f32 v169, v69, s54, -v169\n\t"
"v_mfma_f32_16x16x32_bf16 a[96:99], a[252:255], a[196:199], a[96:99]\n\t"
"s_waitcnt vmcnt(11)\n\t"
"v_fma_f32 v170, v70, s54, -v170\n\t"
"v_fma_f32 v171, v71, s54, -v171\n\t"
"v_mfma_f32_16x16x32_bf16 a[96:99], a[228:231], a[200:203], a[96:99]\n\t"
"v_fma_f32 v72, v172, s52, v72\n\t"
"v_fma_f32 v73, v173, s52, v73\n\t"
"v_mfma_f32_16x16x32_bf16 a[96:99], a[212:215], a[204:207], a[96:99]\n\t"
"v_fma_f32 v74, v174, s52, v74\n\t"
"v_fma_f32 v75, v175, s52, v75\n\t"
"v_mfma_f32_16x16x32_bf16 a[104:107], a[216:219], a[168:171], a[104:107]\n\t"
"ds_read_b128 a[168:171], v244 offset:9216\n\t"
"v_fma_f32 v172, v72, s54, -v172\n\t"
"v_mfma_f32_16x16x32_bf16 a[104:107], a[220:223], a[172:175], a[104:107]\n\t"
"v_fma_f32 v173, v73, s54, -v173\n\t"
"v_fma_f32 v174, v74, s54, -v174\n\t"
"v_mfma_f32_16x16x32_bf16 a[104:107], a[208:211], a[176:179], a[104:107]\n\t"
"v_fma_f32 v175, v75, s54, -v175\n\t"
"v_fma_f32 v76, v176, s52, v76\n\t"
"v_mfma_f32_16x16x32_bf16 a[112:115], a[216:219], a[172:175], a[112:115]\n\t"
"ds_read_b128 a[172:175], v244 offset:9728\n\t"
"v_fma_f32 v77, v177, s52, v77\n\t"
"v_mfma_f32_16x16x32_bf16 a[112:115], a[220:223], a[176:179], a[112:115]\n\t"
"v_fma_f32 v78, v178, s52, v78\n\t"
"v_fma_f32 v79, v179, s52, v79\n\t"
"v_mfma_f32_16x16x32_bf16 a[112:115], a[208:211], a[180:183], a[112:115]\n\t"
"v_and_b32 v255, 4096, v244\n\t"
"v_mad_u32_u24 v255, v255, 5, v244\n\t"
"v_mfma_f32_16x16x32_bf16 a[120:123], a[216:219], a[176:179], a[120:123]\n\t"
"ds_read_b128 a[176:179], v244 offset:10240\n\t"
"v_add_u32 v255, 43008, v255\n\t"
"v_mfma_f32_16x16x32_bf16 a[120:123], a[220:223], a[180:183], a[120:123]\n\t"
"v_fma_f32 v176, v76, s54, -v176\n\t"
"v_fma_f32 v177, v77, s54, -v177\n\t"
"v_mfma_f32_16x16x32_bf16 a[120:123], a[208:211], a[184:187], a[120:123]\n\t"
"v_fma_f32 v178, v78, s54, -v178\n\t"
"v_fma_f32 v179, v79, s54, -v179\n\t"
"v_mfma_f32_16x16x32_bf16 a[128:131], a[216:219], a[180:183], a[128:131]\n\t"
"ds_read_b128 a[180:183], v244 offset:10752\n\t"
"v_fma_f32 v80, v180, s52, v80\n\t"
"v_mfma_f32_16x16x32_bf16 a[128:131], a[220:223], a[184:187], a[128:131]\n\t"
"v_fma_f32 v81, v181, s52, v81\n\t"
"v_fma_f32 v82, v182, s52, v82\n\t"
"v_mfma_f32_16x16x32_bf16 a[128:131], a[208:211], a[188:191], a[128:131]\n\t"
"v_fma_f32 v83, v183, s52, v83\n\t"
"v_fma_f32 v180, v80, s54, -v180\n\t"
"v_mfma_f32_16x16x32_bf16 a[136:139], a[216:219], a[184:187], a[136:139]\n\t"
"ds_read_b128 a[184:187], v244 offset:11264\n\t"
"v_fma_f32 v181, v81, s54, -v181\n\t"
"v_mfma_f32_16x16x32_bf16 a[136:139], a[220:223], a[188:191], a[136:139]\n\t"
"v_fma_f32 v182, v82, s54, -v182\n\t"
"v_fma_f32 v183, v83, s54, -v183\n\t"
"v_mfma_f32_16x16x32_bf16 a[136:139], a[208:211], a[192:195], a[136:139]\n\t"
"v_fma_f32 v84, v184, s52, v84\n\t"
"v_fma_f32 v85, v185, s52, v85\n\t"
"v_mfma_f32_16x16x32_bf16 a[144:147], a[216:219], a[188:191], a[144:147]\n\t"
"ds_read_b128 a[188:191], v244 offset:11776\n\t"
"v_fma_f32 v86, v186, s52, v86\n\t"
"v_mfma_f32_16x16x32_bf16 a[144:147], a[220:223], a[192:195], a[144:147]\n\t"
"v_fma_f32 v87, v187, s52, v87\n\t"
"v_fma_f32 v184, v84, s54, -v184\n\t"
"v_mfma_f32_16x16x32_bf16 a[144:147], a[208:211], a[196:199], a[144:147]\n\t"
"v_fma_f32 v185, v85, s54, -v185\n\t"
"v_fma_f32 v186, v86, s54, -v186\n\t"
"v_mfma_f32_16x16x32_bf16 a[152:155], a[216:219], a[192:195], a[152:155]\n\t"
"s_mov_b32 s20, 1646592\n\t"
"ds_read_b128 a[192:195], v244 offset:12288\n\t"
"v_fma_f32 v187, v87, s54, -v187\n\t"
"v_mfma_f32_16x16x32_bf16 a[152:155], a[220:223], a[196:199], a[152:155]\n\t"
"v_fma_f32 v88, v188, s52, v88\n\t"
"v_fma_f32 v89, v189, s52, v89\n\t"
"v_mfma_f32_16x16x32_bf16 a[152:155], a[208:211], a[200:203], a[152:155]\n\t"
"v_fma_f32 v90, v190, s52, v90\n\t"
"v_fma_f32 v91, v191, s52, v91\n\t"
"v_mfma_f32_16x16x32_bf16 a[160:163], a[216:219], a[196:199], a[160:163]\n\t"
"s_waitcnt vmcnt(8) lgkmcnt(4)\n\t"
"ds_read_b128 a[196:199], v244 offset:12800\n\t"
"v_fma_f32 v188, v88, s54, -v188\n\t"
"v_mfma_f32_16x16x32_bf16 a[160:163], a[220:223], a[200:203], a[160:163]\n\t"
"ds_read_b128 a[200:203], v244 offset:13312\n\t"
"v_fma_f32 v189, v89, s54, -v189\n\t"
"v_mfma_f32_16x16x32_bf16 a[160:163], a[208:211], a[204:207], a[160:163]\n\t"
"ds_read_b128 a[204:207], v244 offset:13824\n\t"
"v_fma_f32 v190, v90, s54, -v190\n\t"
"v_fma_f32 v191, v91, s54, -v191\n\t"
"v_mfma_f32_16x16x32_bf16 v[200:203], a[224:227], a[168:171], v[200:203]\n\t"
"s_waitcnt lgkmcnt(6)\n\t"
"v_fma_f32 v92, v192, s52, v92\n\t"
"v_fma_f32 v93, v193, s52, v93\n\t"
"v_mfma_f32_16x16x32_bf16 v[200:203], v[248:251], a[172:175], v[200:203]\n\t"
"v_fma_f32 v94, v194, s52, v94\n\t"
"v_fma_f32 v95, v195, s52, v95\n\t"
"v_mfma_f32_16x16x32_bf16 v[200:203], v[240:243], a[176:179], v[200:203]\n\t"
"v_fma_f32 v192, v92, s54, -v192\n\t"
"v_fma_f32 v193, v93, s54, -v193\n\t"
"v_fma_f32 v194, v94, s54, -v194\n\t"
"v_fma_f32 v195, v95, s54, -v195\n\t"
"v_mfma_f32_16x16x32_bf16 v[208:211], a[224:227], a[172:175], v[208:211]\n\t"
"s_waitcnt lgkmcnt(5)\n\t"
"v_mfma_f32_16x16x32_bf16 v[208:211], v[248:251], a[176:179], v[208:211]\n\t"
"v_mfma_f32_16x16x32_bf16 v[208:211], v[240:243], a[180:183], v[208:211]\n\t"
"s_waitcnt lgkmcnt(0)\n\t"
"s_barrier\n\t"
"v_mfma_f32_16x16x32_bf16 v[216:219], a[224:227], a[176:179], v[216:219]\n\t"
"v_mfma_f32_16x16x32_bf16 v[216:219], v[248:251], a[180:183], v[216:219]\n\t"
"buffer_load_dwordx4 a[208:211], v252, s[8:11], s20 offen offset:0 sc1\n\t"
"v_mfma_f32_16x16x32_bf16 v[216:219], v[240:243], a[184:187], v[216:219]\n\t"
"buffer_load_dwordx4 a[212:215], v252, s[8:11], s20 offen offset:1024 sc1\n\t"
"v_pk_fma_f32 v[196:197], v[200:201], s[56:57], v[196:197]\n\t"
"v_pk_fma_f32 v[200:201], v[196:197], s[58:59], v[200:201] neg_lo:[0,0,1] neg_hi:[0,0,1]\n\t"
"v_pk_fma_f32 v[228:229], v[0:1], s[60:61], v[196:197]\n\t"
"v_pk_fma_f32 v[0:1], v[196:197], s[60:61], v[0:1]\n\t"
"v_pk_fma_f32 v[196:197], v[100:101], s[62:63], v[200:201]\n\t"
"v_pk_fma_f32 v[100:101], v[200:201], s[62:63], v[100:101]\n\t"
"v_pk_fma_f32 v[198:199], v[202:203], s[56:57], v[198:199]\n\t"
"v_pk_fma_f32 v[202:203], v[198:199], s[58:59], v[202:203] neg_lo:[0,0,1] neg_hi:[0,0,1]\n\t"
"v_pk_fma_f32 v[234:235], v[2:3], s[60:61], v[198:199]\n\t"
"v_pk_fma_f32 v[2:3], v[198:199], s[60:61], v[2:3]\n\t"
"v_pk_fma_f32 v[198:199], v[102:103], s[62:63], v[202:203]\n\t"
"v_pk_fma_f32 v[102:103], v[202:203], s[62:63], v[102:103]\n\t"
"v_mfma_f32_16x16x32_bf16 a[4:7], a[224:227], a[180:183], a[4:7]\n\t"
"v_cvt_pk_bf16_f32 v196, v196, v197\n\t"
"v_cvt_pk_bf16_f32 v200, v228, v229\n\t"
"v_mfma_f32_16x16x32_bf16 a[4:7], v[248:251], a[184:187], a[4:7]\n\t"
"v_cvt_pk_bf16_f32 v197, v198, v199\n\t"
"v_cvt_pk_bf16_f32 v201, v234, v235\n\t"
"v_mfma_f32_16x16x32_bf16 a[4:7], v[240:243], a[188:191], a[4:7]\n\t"
"buffer_load_dwordx4 a[216:219], v252, s[8:11], s20 offen offset:2048 sc1\n\t"
"s_mov_b32 s20, 1474560\n\t"
"v_pk_fma_f32 v[204:205], v[208:209], s[56:57], v[204:205]\n\t"
"v_pk_fma_f32 v[208:209], v[204:205], s[58:59], v[208:209] neg_lo:[0,0,1] neg_hi:[0,0,1]\n\t"
"v_pk_fma_f32 v[98:99], v[4:5], s[60:61], v[204:205]\n\t"
"v_pk_fma_f32 v[4:5], v[204:205], s[60:61], v[4:5]\n\t"
"v_pk_fma_f32 v[204:205], v[104:105], s[62:63], v[208:209]\n\t"
"v_pk_fma_f32 v[104:105], v[208:209], s[62:63], v[104:105]\n\t"
"v_mfma_f32_16x16x32_bf16 a[12:15], a[224:227], a[184:187], a[12:15]\n\t"
"v_cvt_pk_bf16_f32 v204, v204, v205\n\t"
"v_cvt_pk_bf16_f32 v208, v98, v99\n\t"
"v_mfma_f32_16x16x32_bf16 a[12:15], v[248:251], a[188:191], a[12:15]\n\t"
"Lpg10_%=:\n\t"
"s_cbranch_vccnz Lvr0_%=\n\t"
"v_mfma_f32_16x16x32_bf16 a[12:15], v[240:243], a[192:195], a[12:15]\n\t"
"buffer_load_dwordx4 a[220:223], v252, s[8:11], s20 offen offset:0 sc1\n\t"
"v_pk_fma_f32 v[206:207], v[210:211], s[56:57], v[206:207]\n\t"
"v_pk_fma_f32 v[210:211], v[206:207], s[58:59], v[210:211] neg_lo:[0,0,1] neg_hi:[0,0,1]\n\t"
"v_pk_fma_f32 v[228:229], v[6:7], s[60:61], v[206:207]\n\t"
"v_pk_fma_f32 v[6:7], v[206:207], s[60:61], v[6:7]\n\t"
"v_pk_fma_f32 v[206:207], v[106:107], s[62:63], v[210:211]\n\t"
"v_pk_fma_f32 v[106:107], v[210:211], s[62:63], v[106:107]\n\t"
"v_mfma_f32_16x16x32_bf16 a[20:23], a[224:227], a[188:191], a[20:23]\n\t"
"v_cvt_pk_bf16_f32 v205, v206, v207\n\t"
"v_cvt_pk_bf16_f32 v209, v228, v229\n\t"
"v_mfma_f32_16x16x32_bf16 a[20:23], v[248:251], a[192:195], a[20:23]\n\t"
"v_accvgpr_read_b32 v224, a0\n\t"
"v_accvgpr_read_b32 v226, a4\n\t"
"v_mfma_f32_16x16x32_bf16 a[20:23], v[240:243], a[196:199], a[20:23]\n\t"
"buffer_load_dwordx4 a[228:231], v252, s[8:11], s20 offen offset:1024 sc1\n\t"
"v_accvgpr_read_b32 v225, a1\n\t"
"v_accvgpr_read_b32 v227, a5\n\t"
"v_pk_fma_f32 v[212:213], v[216:217], s[56:57], v[212:213]\n\t"
"v_pk_fma_f32 v[216:217], v[212:213], s[58:59], v[216:217] neg_lo:[0,0,1] neg_hi:[0,0,1]\n\t"
"v_pk_fma_f32 v[234:235], v[8:9], s[60:61], v[212:213]\n\t"
"v_pk_fma_f32 v[8:9], v[212:213], s[60:61], v[8:9]\n\t"
"v_pk_fma_f32 v[212:213], v[108:109], s[62:63], v[216:217]\n\t"
"v_pk_fma_f32 v[108:109], v[216:217], s[62:63], v[108:109]\n\t"
"v_mfma_f32_16x16x32_bf16 a[28:31], a[224:227], a[192:195], a[28:31]\n\t"
"v_cvt_pk_bf16_f32 v212, v212, v213\n\t"
"v_cvt_pk_bf16_f32 v216, v234, v235\n\t"
"v_mfma_f32_16x16x32_bf16 a[28:31], v[248:251], a[196:199], a[28:31]\n\t"
"v_accvgpr_read_b32 v230, a2\n\t"
"v_accvgpr_read_b32 v232, a6\n\t"
"v_mfma_f32_16x16x32_bf16 a[28:31], v[240:243], a[200:203], a[28:31]\n\t"
"buffer_load_dwordx4 a[244:247], v252, s[8:11], s20 offen offset:2048 sc1\n\t"
"v_accvgpr_read_b32 v231, a3\n\t"
"v_accvgpr_read_b32 v233, a7\n\t"
"v_pk_fma_f32 v[214:215], v[218:219], s[56:57], v[214:215]\n\t"
"v_pk_fma_f32 v[218:219], v[214:215], s[58:59], v[218:219] neg_lo:[0,0,1] neg_hi:[0,0,1]\n\t"
"v_pk_fma_f32 v[98:99], v[10:11], s[60:61], v[214:215]\n\t"
"v_pk_fma_f32 v[10:11], v[214:215], s[60:61], v[10:11]\n\t"
"v_pk_fma_f32 v[214:215], v[110:111], s[62:63], v[218:219]\n\t"
"v_pk_fma_f32 v[110:111], v[218:219], s[62:63], v[110:111]\n\t"
"v_mfma_f32_16x16x32_bf16 a[36:39], a[224:227], a[196:199], a[36:39]\n\t"
"s_waitcnt vmcnt(11)\n\t"
"s_mov_b32 s20, 1486848\n\t"
"v_cvt_pk_bf16_f32 v213, v214, v215\n\t"
"v_mfma_f32_16x16x32_bf16 a[36:39], v[248:251], a[200:203], a[36:39]\n\t"
"v_cvt_pk_bf16_f32 v217, v98, v99\n\t"
"v_accvgpr_read_b32 v236, a8\n\t"
"v_mfma_f32_16x16x32_bf16 a[36:39], v[240:243], a[204:207], a[36:39]\n\t"
"buffer_load_dwordx4 v[240:243], v252, s[8:11], s20 offen offset:0 sc1\n\t"
"v_accvgpr_read_b32 v238, a12\n\t"
"v_accvgpr_read_b32 v237, a9\n\t"
"v_pk_fma_f32 v[224:225], v[226:227], s[56:57], v[224:225]\n\t"
"v_pk_fma_f32 v[226:227], v[224:225], s[58:59], v[226:227] neg_lo:[0,0,1] neg_hi:[0,0,1]\n\t"
"v_pk_fma_f32 v[228:229], v[12:13], s[60:61], v[224:225]\n\t"
"v_pk_fma_f32 v[12:13], v[224:225], s[60:61], v[12:13]\n\t"
"v_pk_fma_f32 v[224:225], v[112:113], s[62:63], v[226:227]\n\t"
"v_pk_fma_f32 v[112:113], v[226:227], s[62:63], v[112:113]\n\t"
"v_mfma_f32_16x16x32_bf16 a[44:47], a[232:235], a[168:171], a[44:47]\n\t"
"v_cvt_pk_bf16_f32 v224, v224, v225\n\t"
"v_cvt_pk_bf16_f32 v228, v228, v229\n\t"
"v_mfma_f32_16x16x32_bf16 a[44:47], a[236:239], a[172:175], a[44:47]\n\t"
"v_accvgpr_write_b32 a0, v224\n\t"
"v_accvgpr_write_b32 a4, v228\n\t"
"v_mfma_f32_16x16x32_bf16 a[44:47], a[240:243], a[176:179], a[44:47]\n\t"
"buffer_load_dwordx4 a[224:227], v252, s[8:11], s20 offen offset:1024 sc1\n\t"
"v_accvgpr_read_b32 v239, a13\n\t"
"v_accvgpr_read_b32 v224, a10\n\t"
"v_pk_fma_f32 v[230:231], v[232:233], s[56:57], v[230:231]\n\t"
"v_pk_fma_f32 v[232:233], v[230:231], s[58:59], v[232:233] neg_lo:[0,0,1] neg_hi:[0,0,1]\n\t"
"v_pk_fma_f32 v[234:235], v[14:15], s[60:61], v[230:231]\n\t"
"v_pk_fma_f32 v[14:15], v[230:231], s[60:61], v[14:15]\n\t"
"v_pk_fma_f32 v[230:231], v[114:115], s[62:63], v[232:233]\n\t"
"v_pk_fma_f32 v[114:115], v[232:233], s[62:63], v[114:115]\n\t"
"v_mfma_f32_16x16x32_bf16 a[52:55], a[232:235], a[172:175], a[52:55]\n\t"
"v_cvt_pk_bf16_f32 v230, v230, v231\n\t"
"v_cvt_pk_bf16_f32 v234, v234, v235\n\t"
"v_mfma_f32_16x16x32_bf16 a[52:55], a[236:239], a[176:179], a[52:55]\n\t"
"v_accvgpr_write_b32 a1, v230\n\t"
"v_accvgpr_write_b32 a5, v234\n\t"
"v_mfma_f32_16x16x32_bf16 a[52:55], a[240:243], a[180:183], a[52:55]\n\t"
"buffer_load_dwordx4 a[248:251], v252, s[8:11], s20 offen offset:2048 sc1\n\t"
"v_accvgpr_read_b32 v226, a14\n\t"
"v_accvgpr_read_b32 v225, a11\n\t"
"v_pk_fma_f32 v[236:237], v[238:239], s[56:57], v[236:237]\n\t"
"v_pk_fma_f32 v[238:239], v[236:237], s[58:59], v[238:239] neg_lo:[0,0,1] neg_hi:[0,0,1]\n\t"
"v_pk_fma_f32 v[98:99], v[16:17], s[60:61], v[236:237]\n\t"
"v_pk_fma_f32 v[16:17], v[236:237], s[60:61], v[16:17]\n\t"
"v_pk_fma_f32 v[236:237], v[116:117], s[62:63], v[238:239]\n\t"
"v_pk_fma_f32 v[116:117], v[238:239], s[62:63], v[116:117]\n\t"
"v_mfma_f32_16x16x32_bf16 a[60:63], a[232:235], a[176:179], a[60:63]\n\t"
"s_mov_b32 s20, 1499136\n\t"
"v_accvgpr_read_b32 v227, a15\n\t"
"v_accvgpr_read_b32 v230, a16\n\t"
"v_mfma_f32_16x16x32_bf16 a[60:63], a[236:239], a[180:183], a[60:63]\n\t"
"v_accvgpr_read_b32 v232, a20\n\t"
"v_accvgpr_read_b32 v231, a17\n\t"
"v_mfma_f32_16x16x32_bf16 a[60:63], a[240:243], a[184:187], a[60:63]\n\t"
"buffer_load_dwordx4 a[252:255], v252, s[8:11], s20 offen offset:0 sc1\n\t"
"v_accvgpr_read_b32 v233, a21\n\t"
"v_cvt_pk_bf16_f32 v236, v236, v237\n\t"
"v_cvt_pk_bf16_f32 v98, v98, v99\n\t"
"v_pk_fma_f32 v[224:225], v[226:227], s[56:57], v[224:225]\n\t"
"v_pk_fma_f32 v[226:227], v[224:225], s[58:59], v[226:227] neg_lo:[0,0,1] neg_hi:[0,0,1]\n\t"
"v_pk_fma_f32 v[228:229], v[18:19], s[60:61], v[224:225]\n\t"
"v_pk_fma_f32 v[18:19], v[224:225], s[60:61], v[18:19]\n\t"
"v_pk_fma_f32 v[224:225], v[118:119], s[62:63], v[226:227]\n\t"
"v_pk_fma_f32 v[118:119], v[226:227], s[62:63], v[118:119]\n\t"
"v_pk_fma_f32 v[230:231], v[232:233], s[56:57], v[230:231]\n\t"
"v_pk_fma_f32 v[232:233], v[230:231], s[58:59], v[232:233] neg_lo:[0,0,1] neg_hi:[0,0,1]\n\t"
"v_pk_fma_f32 v[234:235], v[20:21], s[60:61], v[230:231]\n\t"
"v_pk_fma_f32 v[20:21], v[230:231], s[60:61], v[20:21]\n\t"
"v_pk_fma_f32 v[230:231], v[120:121], s[62:63], v[232:233]\n\t"
"v_pk_fma_f32 v[120:121], v[232:233], s[62:63], v[120:121]\n\t"
"v_mfma_f32_16x16x32_bf16 a[68:71], a[232:235], a[180:183], a[68:71]\n\t"
"v_accvgpr_write_b32 a8, v236\n\t"
"v_accvgpr_write_b32 a12, v98\n\t"
"v_mfma_f32_16x16x32_bf16 a[68:71], a[236:239], a[184:187], a[68:71]\n\t"
"v_accvgpr_read_b32 v236, a18\n\t"
"v_accvgpr_read_b32 v238, a22\n\t"
"v_mfma_f32_16x16x32_bf16 a[68:71], a[240:243], a[188:191], a[68:71]\n\t"
"v_accvgpr_read_b32 v237, a19\n\t"
"v_accvgpr_read_b32 v239, a23\n\t"
"v_cvt_pk_bf16_f32 v224, v224, v225\n\t"
"v_cvt_pk_bf16_f32 v228, v228, v229\n\t"
"v_cvt_pk_bf16_f32 v230, v230, v231\n\t"
"v_cvt_pk_bf16_f32 v234, v234, v235\n\t"
"v_pk_fma_f32 v[236:237], v[238:239], s[56:57], v[236:237]\n\t"
"v_pk_fma_f32 v[238:239], v[236:237], s[58:59], v[238:239] neg_lo:[0,0,1] neg_hi:[0,0,1]\n\t"
"v_pk_fma_f32 v[98:99], v[22:23], s[60:61], v[236:237]\n\t"
"v_pk_fma_f32 v[22:23], v[236:237], s[60:61], v[22:23]\n\t"
"v_pk_fma_f32 v[236:237], v[122:123], s[62:63], v[238:239]\n\t"
"v_pk_fma_f32 v[122:123], v[238:239], s[62:63], v[122:123]\n\t"
"v_mfma_f32_16x16x32_bf16 a[76:79], a[232:235], a[184:187], a[76:79]\n\t"
"v_accvgpr_write_b32 a9, v224\n\t"
"v_accvgpr_write_b32 a13, v228\n\t"
"v_mfma_f32_16x16x32_bf16 a[76:79], a[236:239], a[188:191], a[76:79]\n\t"
"v_accvgpr_write_b32 a16, v230\n\t"
"v_accvgpr_write_b32 a20, v234\n\t"
"v_mfma_f32_16x16x32_bf16 a[76:79], a[240:243], a[192:195], a[76:79]\n\t"
"v_accvgpr_read_b32 v224, a24\n\t"
"v_accvgpr_read_b32 v226, a28\n\t"
"v_accvgpr_read_b32 v225, a25\n\t"
"v_accvgpr_read_b32 v227, a29\n\t"
"v_cvt_pk_bf16_f32 v236, v236, v237\n\t"
"v_cvt_pk_bf16_f32 v98, v98, v99\n\t"
"v_pk_fma_f32 v[224:225], v[226:227], s[56:57], v[224:225]\n\t"
"v_pk_fma_f32 v[226:227], v[224:225], s[58:59], v[226:227] neg_lo:[0,0,1] neg_hi:[0,0,1]\n\t"
"v_pk_fma_f32 v[228:229], v[24:25], s[60:61], v[224:225]\n\t"
"v_pk_fma_f32 v[24:25], v[224:225], s[60:61], v[24:25]\n\t"
"v_pk_fma_f32 v[224:225], v[124:125], s[62:63], v[226:227]\n\t"
"v_pk_fma_f32 v[124:125], v[226:227], s[62:63], v[124:125]\n\t"
"v_mfma_f32_16x16x32_bf16 a[84:87], a[232:235], a[188:191], a[84:87]\n\t"
"v_accvgpr_write_b32 a17, v236\n\t"
"v_accvgpr_write_b32 a21, v98\n\t"
"v_mfma_f32_16x16x32_bf16 a[84:87], a[236:239], a[192:195], a[84:87]\n\t"
"v_accvgpr_read_b32 v230, a26\n\t"
"v_accvgpr_read_b32 v232, a30\n\t"
"v_mfma_f32_16x16x32_bf16 a[84:87], a[240:243], a[196:199], a[84:87]\n\t"
"v_accvgpr_read_b32 v231, a27\n\t"
"v_accvgpr_read_b32 v233, a31\n\t"
"v_cvt_pk_bf16_f32 v224, v224, v225\n\t"
"v_cvt_pk_bf16_f32 v228, v228, v229\n\t"
"v_accvgpr_write_b32 a24, v224\n\t"
"v_pk_fma_f32 v[230:231], v[232:233], s[56:57], v[230:231]\n\t"
"v_pk_fma_f32 v[232:233], v[230:231], s[58:59], v[232:233] neg_lo:[0,0,1] neg_hi:[0,0,1]\n\t"
"v_pk_fma_f32 v[234:235], v[26:27], s[60:61], v[230:231]\n\t"
"v_pk_fma_f32 v[26:27], v[230:231], s[60:61], v[26:27]\n\t"
"v_pk_fma_f32 v[230:231], v[126:127], s[62:63], v[232:233]\n\t"
"v_pk_fma_f32 v[126:127], v[232:233], s[62:63], v[126:127]\n\t"
"v_mfma_f32_16x16x32_bf16 a[92:95], a[232:235], a[192:195], a[92:95]\n\t"
"v_accvgpr_write_b32 a28, v228\n\t"
"v_accvgpr_read_b32 v236, a32\n\t"
"v_mfma_f32_16x16x32_bf16 a[92:95], a[236:239], a[196:199], a[92:95]\n\t"
"v_accvgpr_read_b32 v238, a36\n\t"
"v_accvgpr_read_b32 v237, a33\n\t"
"v_mfma_f32_16x16x32_bf16 a[92:95], a[240:243], a[200:203], a[92:95]\n\t"
"v_accvgpr_read_b32 v239, a37\n\t"
"v_cvt_pk_bf16_f32 v230, v230, v231\n\t"
"v_cvt_pk_bf16_f32 v234, v234, v235\n\t"
"v_accvgpr_write_b32 a25, v230\n\t"
"v_accvgpr_write_b32 a29, v234\n\t"
"v_pk_fma_f32 v[236:237], v[238:239], s[56:57], v[236:237]\n\t"
"v_pk_fma_f32 v[238:239], v[236:237], s[58:59], v[238:239] neg_lo:[0,0,1] neg_hi:[0,0,1]\n\t"
"v_pk_fma_f32 v[98:99], v[28:29], s[60:61], v[236:237]\n\t"
"v_pk_fma_f32 v[28:29], v[236:237], s[60:61], v[28:29]\n\t"
"v_pk_fma_f32 v[236:237], v[128:129], s[62:63], v[238:239]\n\t"
"v_pk_fma_f32 v[128:129], v[238:239], s[62:63], v[128:129]\n\t"
"v_mfma_f32_16x16x32_bf16 a[100:103], a[232:235], a[196:199], a[100:103]\n\t"
"s_waitcnt vmcnt(7)\n\t"
"v_accvgpr_read_b32 v224, a34\n\t"
"v_accvgpr_read_b32 v226, a38\n\t"
"v_mfma_f32_16x16x32_bf16 a[100:103], a[236:239], a[200:203], a[100:103]\n\t"
"v_accvgpr_read_b32 v225, a35\n\t"
"v_accvgpr_read_b32 v227, a39\n\t"
"v_mfma_f32_16x16x32_bf16 a[100:103], a[240:243], a[204:207], a[100:103]\n\t"
"buffer_load_dwordx4 a[232:235], v252, s[8:11], s20 offen offset:1024 sc1\n\t"
"v_cvt_pk_bf16_f32 v236, v236, v237\n\t"
"v_cvt_pk_bf16_f32 v98, v98, v99\n\t"
"v_pk_fma_f32 v[224:225], v[226:227], s[56:57], v[224:225]\n\t"
"v_pk_fma_f32 v[226:227], v[224:225], s[58:59], v[226:227] neg_lo:[0,0,1] neg_hi:[0,0,1]\n\t"
"v_pk_fma_f32 v[228:229], v[30:31], s[60:61], v[224:225]\n\t"
"v_pk_fma_f32 v[30:31], v[224:225], s[60:61], v[30:31]\n\t"
"v_pk_fma_f32 v[224:225], v[130:131], s[62:63], v[226:227]\n\t"
"v_pk_fma_f32 v[130:131], v[226:227], s[62:63], v[130:131]\n\t"
"v_mfma_f32_16x16x32_bf16 a[108:111], a[208:211], a[168:171], a[108:111]\n\t"
"ds_read_b128 a[168:171], v255 offset:512\n\t"
"v_accvgpr_write_b32 a32, v236\n\t"
"v_mfma_f32_16x16x32_bf16 a[108:111], a[212:215], a[172:175], a[108:111]\n\t"
"v_accvgpr_write_b32 a36, v98\n\t"
"v_accvgpr_read_b32 v230, a40\n\t"
"v_mfma_f32_16x16x32_bf16 a[108:111], a[216:219], a[176:179], a[108:111]\n\t"
"buffer_load_dwordx4 a[236:239], v252, s[8:11], s20 offen offset:2048 sc1\n\t"
"v_accvgpr_read_b32 v232, a44\n\t"
"v_accvgpr_read_b32 v231, a41\n\t"
"v_accvgpr_read_b32 v233, a45\n\t"
"v_cvt_pk_bf16_f32 v224, v224, v225\n\t"
"v_cvt_pk_bf16_f32 v228, v228, v229\n\t"
"s_mov_b32 s20, 1658880\n\t"
"v_pk_fma_f32 v[230:231], v[232:233], s[56:57], v[230:231]\n\t"
"v_pk_fma_f32 v[232:233], v[230:231], s[58:59], v[232:233] neg_lo:[0,0,1] neg_hi:[0,0,1]\n\t"
"v_pk_fma_f32 v[234:235], v[32:33], s[60:61], v[230:231]\n\t"
"v_pk_fma_f32 v[32:33], v[230:231], s[60:61], v[32:33]\n\t"
"v_pk_fma_f32 v[230:231], v[132:133], s[62:63], v[232:233]\n\t"
"v_pk_fma_f32 v[132:133], v[232:233], s[62:63], v[132:133]\n\t"
"v_mfma_f32_16x16x32_bf16 a[116:119], a[208:211], a[172:175], a[116:119]\n\t"
"ds_read_b128 a[172:175], v255 offset:3584\n\t"
"v_accvgpr_write_b32 a33, v224\n\t"
"v_mfma_f32_16x16x32_bf16 a[116:119], a[212:215], a[176:179], a[116:119]\n\t"
"v_accvgpr_write_b32 a37, v228\n\t"
"v_accvgpr_read_b32 v236, a42\n\t"
"v_mfma_f32_16x16x32_bf16 a[116:119], a[216:219], a[180:183], a[116:119]\n\t"
"buffer_load_dwordx4 a[240:243], v252, s[8:11], s20 offen offset:0 sc1\n\t"
"v_accvgpr_read_b32 v238, a46\n\t"
"v_accvgpr_read_b32 v237, a43\n\t"
"v_accvgpr_read_b32 v239, a47\n\t"
"v_pk_fma_f32 v[236:237], v[238:239], s[56:57], v[236:237]\n\t"
"v_pk_fma_f32 v[238:239], v[236:237], s[58:59], v[238:239] neg_lo:[0,0,1] neg_hi:[0,0,1]\n\t"
"v_pk_fma_f32 v[98:99], v[34:35], s[60:61], v[236:237]\n\t"
"v_pk_fma_f32 v[34:35], v[236:237], s[60:61], v[34:35]\n\t"
"v_pk_fma_f32 v[236:237], v[134:135], s[62:63], v[238:239]\n\t"
"v_pk_fma_f32 v[134:135], v[238:239], s[62:63], v[134:135]\n\t"
"v_mfma_f32_16x16x32_bf16 a[124:127], a[208:211], a[176:179], a[124:127]\n\t"
"ds_read_b128 a[176:179], v255 offset:6656\n\t"
"v_cvt_pk_bf16_f32 v198, v230, v231\n\t"
"v_mfma_f32_16x16x32_bf16 a[124:127], a[212:215], a[180:183], a[124:127]\n\t"
"v_cvt_pk_bf16_f32 v202, v234, v235\n\t"
"v_accvgpr_read_b32 v224, a48\n\t"
"v_mfma_f32_16x16x32_bf16 a[124:127], a[216:219], a[184:187], a[124:127]\n\t"
"global_load_dwordx4 v[248:251], v252, s[48:49] offset:0\n\t"
"v_accvgpr_read_b32 v226, a52\n\t"
"v_accvgpr_read_b32 v225, a49\n\t"
"v_accvgpr_read_b32 v227, a53\n\t"
"v_accvgpr_read_b32 v230, a50\n\t"
"v_accvgpr_read_b32 v232, a54\n\t"
"v_accvgpr_read_b32 v231, a51\n\t"
"v_accvgpr_read_b32 v233, a55\n\t"
"v_pk_fma_f32 v[224:225], v[226:227], s[56:57], v[224:225]\n\t"
"v_pk_fma_f32 v[226:227], v[224:225], s[58:59], v[226:227] neg_lo:[0,0,1] neg_hi:[0,0,1]\n\t"
"v_pk_fma_f32 v[228:229], v[36:37], s[60:61], v[224:225]\n\t"
"v_pk_fma_f32 v[36:37], v[224:225], s[60:61], v[36:37]\n\t"
"v_pk_fma_f32 v[224:225], v[136:137], s[62:63], v[226:227]\n\t"
"v_pk_fma_f32 v[136:137], v[226:227], s[62:63], v[136:137]\n\t"
"v_pk_fma_f32 v[230:231], v[232:233], s[56:57], v[230:231]\n\t"
"v_pk_fma_f32 v[232:233], v[230:231], s[58:59], v[232:233] neg_lo:[0,0,1] neg_hi:[0,0,1]\n\t"
"v_pk_fma_f32 v[234:235], v[38:39], s[60:61], v[230:231]\n\t"
"v_pk_fma_f32 v[38:39], v[230:231], s[60:61], v[38:39]\n\t"
"v_pk_fma_f32 v[230:231], v[138:139], s[62:63], v[232:233]\n\t"
"v_pk_fma_f32 v[138:139], v[232:233], s[62:63], v[138:139]\n\t"
"v_mfma_f32_16x16x32_bf16 a[132:135], a[208:211], a[180:183], a[132:135]\n\t"
"ds_read_b128 a[180:183], v255 offset:9728\n\t"
"v_cvt_pk_bf16_f32 v199, v236, v237\n\t"
"v_mfma_f32_16x16x32_bf16 a[132:135], a[212:215], a[184:187], a[132:135]\n\t"
"v_cvt_pk_bf16_f32 v203, v98, v99\n\t"
"v_accvgpr_read_b32 v236, a56\n\t"
"v_mfma_f32_16x16x32_bf16 a[132:135], a[216:219], a[188:191], a[132:135]\n\t"
"buffer_load_dwordx4 a[40:43], v252, s[8:11], s20 offen offset:1024 sc1\n\t"
"v_accvgpr_read_b32 v238, a60\n\t"
"v_accvgpr_read_b32 v237, a57\n\t"
"v_accvgpr_read_b32 v239, a61\n\t"
"v_pk_fma_f32 v[236:237], v[238:239], s[56:57], v[236:237]\n\t"
"v_pk_fma_f32 v[238:239], v[236:237], s[58:59], v[238:239] neg_lo:[0,0,1] neg_hi:[0,0,1]\n\t"
"v_pk_fma_f32 v[98:99], v[40:41], s[60:61], v[236:237]\n\t"
"v_pk_fma_f32 v[40:41], v[236:237], s[60:61], v[40:41]\n\t"
"v_pk_fma_f32 v[236:237], v[140:141], s[62:63], v[238:239]\n\t"
"v_pk_fma_f32 v[140:141], v[238:239], s[62:63], v[140:141]\n\t"
"v_mfma_f32_16x16x32_bf16 a[140:143], a[208:211], a[184:187], a[140:143]\n\t"
"ds_read_b128 a[184:187], v255 offset:12800\n\t"
"v_cvt_pk_bf16_f32 v206, v224, v225\n\t"
"v_mfma_f32_16x16x32_bf16 a[140:143], a[212:215], a[188:191], a[140:143]\n\t"
"v_cvt_pk_bf16_f32 v210, v228, v229\n\t"
"v_cvt_pk_bf16_f32 v207, v230, v231\n\t"
"v_mfma_f32_16x16x32_bf16 a[140:143], a[216:219], a[192:195], a[140:143]\n\t"
"buffer_load_dwordx4 a[48:51], v252, s[8:11], s20 offen offset:2048 sc1\n\t"
"v_accvgpr_read_b32 v224, a58\n\t"
"v_accvgpr_read_b32 v226, a62\n\t"
"v_accvgpr_read_b32 v225, a59\n\t"
"v_accvgpr_read_b32 v227, a63\n\t"
"v_cvt_pk_bf16_f32 v211, v234, v235\n\t"
"s_mov_b32 s20, 1671168\n\t"
"v_pk_fma_f32 v[224:225], v[226:227], s[56:57], v[224:225]\n\t"
"v_pk_fma_f32 v[226:227], v[224:225], s[58:59], v[226:227] neg_lo:[0,0,1] neg_hi:[0,0,1]\n\t"
"v_pk_fma_f32 v[228:229], v[42:43], s[60:61], v[224:225]\n\t"
"v_pk_fma_f32 v[42:43], v[224:225], s[60:61], v[42:43]\n\t"
"v_pk_fma_f32 v[224:225], v[142:143], s[62:63], v[226:227]\n\t"
"v_pk_fma_f32 v[142:143], v[226:227], s[62:63], v[142:143]\n\t"
"v_mfma_f32_16x16x32_bf16 a[148:151], a[208:211], a[188:191], a[148:151]\n\t"
"ds_read_b128 a[188:191], v255 offset:15872\n\t"
"v_cvt_pk_bf16_f32 v214, v236, v237\n\t"
"v_mfma_f32_16x16x32_bf16 a[148:151], a[212:215], a[192:195], a[148:151]\n\t"
"v_cvt_pk_bf16_f32 v218, v98, v99\n\t"
"v_accvgpr_read_b32 v230, a64\n\t"
"v_mfma_f32_16x16x32_bf16 a[148:151], a[216:219], a[196:199], a[148:151]\n\t"
"buffer_load_dwordx4 a[52:55], v252, s[8:11], s20 offen offset:0 sc1\n\t"
"v_accvgpr_read_b32 v232, a68\n\t"
"v_accvgpr_read_b32 v231, a65\n\t"
"v_accvgpr_read_b32 v233, a69\n\t"
"v_pk_fma_f32 v[230:231], v[232:233], s[56:57], v[230:231]\n\t"
"v_pk_fma_f32 v[232:233], v[230:231], s[58:59], v[232:233] neg_lo:[0,0,1] neg_hi:[0,0,1]\n\t"
"v_pk_fma_f32 v[234:235], v[44:45], s[60:61], v[230:231]\n\t"
"v_pk_fma_f32 v[44:45], v[230:231], s[60:61], v[44:45]\n\t"
"v_pk_fma_f32 v[230:231], v[144:145], s[62:63], v[232:233]\n\t"
"v_pk_fma_f32 v[144:145], v[232:233], s[62:63], v[144:145]\n\t"
"v_mfma_f32_16x16x32_bf16 a[156:159], a[208:211], a[192:195], a[156:159]\n\t"
"ds_read_b128 a[192:195], v255 offset:18944\n\t"
"v_cvt_pk_bf16_f32 v215, v224, v225\n\t"
"v_mfma_f32_16x16x32_bf16 a[156:159], a[212:215], a[196:199], a[156:159]\n\t"
"v_cvt_pk_bf16_f32 v219, v228, v229\n\t"
"v_accvgpr_read_b32 v236, a66\n\t"
"v_mfma_f32_16x16x32_bf16 a[156:159], a[216:219], a[200:203], a[156:159]\n\t"
"buffer_load_dwordx4 a[44:47], v252, s[8:11], s20 offen offset:1024 sc1\n\t"
"v_accvgpr_read_b32 v238, a70\n\t"
"v_accvgpr_read_b32 v237, a67\n\t"
"v_accvgpr_read_b32 v239, a71\n\t"
"v_cvt_pk_bf16_f32 v230, v230, v231\n\t"
"v_cvt_pk_bf16_f32 v234, v234, v235\n\t"
"v_pk_fma_f32 v[236:237], v[238:239], s[56:57], v[236:237]\n\t"
"v_pk_fma_f32 v[238:239], v[236:237], s[58:59], v[238:239] neg_lo:[0,0,1] neg_hi:[0,0,1]\n\t"
"v_pk_fma_f32 v[98:99], v[46:47], s[60:61], v[236:237]\n\t"
"v_pk_fma_f32 v[46:47], v[236:237], s[60:61], v[46:47]\n\t"
"v_pk_fma_f32 v[236:237], v[146:147], s[62:63], v[238:239]\n\t"
"v_pk_fma_f32 v[146:147], v[238:239], s[62:63], v[146:147]\n\t"
"v_mfma_f32_16x16x32_bf16 a[164:167], a[208:211], a[196:199], a[164:167]\n\t"
"s_waitcnt vmcnt(12) lgkmcnt(4)\n\t"
"ds_read_b128 a[196:199], v255 offset:22016\n\t"
"v_accvgpr_write_b32 a2, v230\n\t"
"v_mfma_f32_16x16x32_bf16 a[164:167], a[212:215], a[200:203], a[164:167]\n\t"
"ds_read_b128 a[200:203], v255 offset:25088\n\t"
"v_accvgpr_write_b32 a6, v234\n\t"
"v_mfma_f32_16x16x32_bf16 a[164:167], a[216:219], a[204:207], a[164:167]\n\t"
"buffer_load_dwordx4 a[60:63], v252, s[8:11], s20 offen offset:2048 sc1\n\t"
"ds_read_b128 a[204:207], v255 offset:28160\n\t"
"v_accvgpr_read_b32 v224, a72\n\t"
"v_accvgpr_read_b32 v226, a76\n\t"
"v_accvgpr_read_b32 v225, a73\n\t"
"v_accvgpr_read_b32 v227, a77\n\t"
"v_cvt_pk_bf16_f32 v236, v236, v237\n\t"
"v_cvt_pk_bf16_f32 v98, v98, v99\n\t"
"s_mov_b32 s20, 1683456\n\t"
"v_pk_fma_f32 v[224:225], v[226:227], s[56:57], v[224:225]\n\t"
"v_pk_fma_f32 v[226:227], v[224:225], s[58:59], v[226:227] neg_lo:[0,0,1] neg_hi:[0,0,1]\n\t"
"v_pk_fma_f32 v[228:229], v[48:49], s[60:61], v[224:225]\n\t"
"v_pk_fma_f32 v[48:49], v[224:225], s[60:61], v[48:49]\n\t"
"v_pk_fma_f32 v[224:225], v[148:149], s[62:63], v[226:227]\n\t"
"v_pk_fma_f32 v[148:149], v[226:227], s[62:63], v[148:149]\n\t"
"Lst14_%=:\n\t"
"v_mfma_f32_16x16x32_bf16 v[0:3], a[220:223], a[168:171], v[0:3]\n\t"
"s_waitcnt lgkmcnt(6)\n\t"
"v_accvgpr_write_b32 a3, v236\n\t"
"v_accvgpr_write_b32 a7, v98\n\t"
"v_mfma_f32_16x16x32_bf16 v[0:3], a[228:231], a[172:175], v[0:3]\n\t"
"v_accvgpr_read_b32 v230, a74\n\t"
"v_accvgpr_read_b32 v232, a78\n\t"
"v_mfma_f32_16x16x32_bf16 v[0:3], a[244:247], a[176:179], v[0:3]\n\t"
"buffer_load_dwordx4 a[64:67], v252, s[8:11], s20 offen offset:0 sc1\n\t"
"v_accvgpr_read_b32 v231, a75\n\t"
"v_accvgpr_read_b32 v233, a79\n\t"
"v_cvt_pk_bf16_f32 v224, v224, v225\n\t"
"v_cvt_pk_bf16_f32 v228, v228, v229\n\t"
"v_pk_fma_f32 v[230:231], v[232:233], s[56:57], v[230:231]\n\t"
"v_pk_fma_f32 v[232:233], v[230:231], s[58:59], v[232:233] neg_lo:[0,0,1] neg_hi:[0,0,1]\n\t"
"v_pk_fma_f32 v[234:235], v[50:51], s[60:61], v[230:231]\n\t"
"v_pk_fma_f32 v[50:51], v[230:231], s[60:61], v[50:51]\n\t"
"v_pk_fma_f32 v[230:231], v[150:151], s[62:63], v[232:233]\n\t"
"v_pk_fma_f32 v[150:151], v[232:233], s[62:63], v[150:151]\n\t"
"v_mfma_f32_16x16x32_bf16 v[4:7], a[220:223], a[172:175], v[4:7]\n\t"
"s_waitcnt lgkmcnt(5)\n\t"
"v_accvgpr_write_b32 a10, v224\n\t"
"v_accvgpr_write_b32 a14, v228\n\t"
"v_mfma_f32_16x16x32_bf16 v[4:7], a[228:231], a[176:179], v[4:7]\n\t"
"v_accvgpr_read_b32 v236, a80\n\t"
"v_accvgpr_read_b32 v238, a84\n\t"
"v_mfma_f32_16x16x32_bf16 v[4:7], a[244:247], a[180:183], v[4:7]\n\t"
"buffer_load_dwordx4 a[68:71], v252, s[8:11], s20 offen offset:1024 sc1\n\t"
"v_accvgpr_read_b32 v237, a81\n\t"
"v_accvgpr_read_b32 v239, a85\n\t"
"v_cvt_pk_bf16_f32 v230, v230, v231\n\t"
"v_cvt_pk_bf16_f32 v234, v234, v235\n\t"
"v_pk_fma_f32 v[236:237], v[238:239], s[56:57], v[236:237]\n\t"
"v_pk_fma_f32 v[238:239], v[236:237], s[58:59], v[238:239] neg_lo:[0,0,1] neg_hi:[0,0,1]\n\t"
"v_pk_fma_f32 v[98:99], v[52:53], s[60:61], v[236:237]\n\t"
"v_pk_fma_f32 v[52:53], v[236:237], s[60:61], v[52:53]\n\t"
"v_pk_fma_f32 v[236:237], v[152:153], s[62:63], v[238:239]\n\t"
"v_pk_fma_f32 v[152:153], v[238:239], s[62:63], v[152:153]\n\t"
"v_mfma_f32_16x16x32_bf16 v[8:11], a[220:223], a[176:179], v[8:11]\n\t"
"s_waitcnt lgkmcnt(4)\n\t"
"v_accvgpr_write_b32 a11, v230\n\t"
"v_accvgpr_write_b32 a15, v234\n\t"
"v_mfma_f32_16x16x32_bf16 v[8:11], a[228:231], a[180:183], v[8:11]\n\t"
"v_accvgpr_read_b32 v224, a82\n\t"
"v_accvgpr_read_b32 v226, a86\n\t"
"v_mfma_f32_16x16x32_bf16 v[8:11], a[244:247], a[184:187], v[8:11]\n\t"
"buffer_load_dwordx4 a[72:75], v252, s[8:11], s20 offen offset:2048 sc1\n\t"
"v_accvgpr_read_b32 v225, a83\n\t"
"v_accvgpr_read_b32 v227, a87\n\t"
"v_cvt_pk_bf16_f32 v236, v236, v237\n\t"
"v_cvt_pk_bf16_f32 v98, v98, v99\n\t"
"s_mov_b32 s20, 589824\n\t"
"v_pk_fma_f32 v[224:225], v[226:227], s[56:57], v[224:225]\n\t"
"v_pk_fma_f32 v[226:227], v[224:225], s[58:59], v[226:227] neg_lo:[0,0,1] neg_hi:[0,0,1]\n\t"
"v_pk_fma_f32 v[228:229], v[54:55], s[60:61], v[224:225]\n\t"
"v_pk_fma_f32 v[54:55], v[224:225], s[60:61], v[54:55]\n\t"
"v_pk_fma_f32 v[224:225], v[154:155], s[62:63], v[226:227]\n\t"
"v_pk_fma_f32 v[154:155], v[226:227], s[62:63], v[154:155]\n\t"
"v_mfma_f32_16x16x32_bf16 v[12:15], a[220:223], a[180:183], v[12:15]\n\t"
"s_waitcnt lgkmcnt(3)\n\t"
"v_accvgpr_write_b32 a18, v236\n\t"
"v_accvgpr_write_b32 a22, v98\n\t"
"v_mfma_f32_16x16x32_bf16 v[12:15], a[228:231], a[184:187], v[12:15]\n\t"
"v_accvgpr_read_b32 v230, a88\n\t"
"v_accvgpr_read_b32 v232, a92\n\t"
"v_mfma_f32_16x16x32_bf16 v[12:15], a[244:247], a[188:191], v[12:15]\n\t"
"buffer_load_dwordx4 a[76:79], v252, s[8:11], s20 offen offset:0 sc1\n\t"
"v_accvgpr_read_b32 v231, a89\n\t"
"v_accvgpr_read_b32 v233, a93\n\t"
"v_accvgpr_read_b32 v236, a90\n\t"
"v_accvgpr_read_b32 v238, a94\n\t"
"v_accvgpr_read_b32 v237, a91\n\t"
"v_accvgpr_read_b32 v239, a95\n\t"
"v_cvt_pk_bf16_f32 v224, v224, v225\n\t"
"v_cvt_pk_bf16_f32 v228, v228, v229\n\t"
"v_pk_fma_f32 v[230:231], v[232:233], s[56:57], v[230:231]\n\t"
"v_pk_fma_f32 v[232:233], v[230:231], s[58:59], v[232:233] neg_lo:[0,0,1] neg_hi:[0,0,1]\n\t"
"v_pk_fma_f32 v[234:235], v[56:57], s[60:61], v[230:231]\n\t"
"v_pk_fma_f32 v[56:57], v[230:231], s[60:61], v[56:57]\n\t"
"v_pk_fma_f32 v[230:231], v[156:157], s[62:63], v[232:233]\n\t"
"v_pk_fma_f32 v[156:157], v[232:233], s[62:63], v[156:157]\n\t"
"v_pk_fma_f32 v[236:237], v[238:239], s[56:57], v[236:237]\n\t"
"v_pk_fma_f32 v[238:239], v[236:237], s[58:59], v[238:239] neg_lo:[0,0,1] neg_hi:[0,0,1]\n\t"
"v_pk_fma_f32 v[98:99], v[58:59], s[60:61], v[236:237]\n\t"
"v_pk_fma_f32 v[58:59], v[236:237], s[60:61], v[58:59]\n\t"
"v_pk_fma_f32 v[236:237], v[158:159], s[62:63], v[238:239]\n\t"
"v_pk_fma_f32 v[158:159], v[238:239], s[62:63], v[158:159]\n\t"
"v_mfma_f32_16x16x32_bf16 v[16:19], a[220:223], a[184:187], v[16:19]\n\t"
"s_waitcnt lgkmcnt(2)\n\t"
"v_accvgpr_write_b32 a19, v224\n\t"
"v_accvgpr_write_b32 a23, v228\n\t"
"v_mfma_f32_16x16x32_bf16 v[16:19], a[228:231], a[188:191], v[16:19]\n\t"
"v_accvgpr_read_b32 v224, a96\n\t"
"v_accvgpr_read_b32 v226, a100\n\t"
"v_mfma_f32_16x16x32_bf16 v[16:19], a[244:247], a[192:195], v[16:19]\n\t"
"buffer_load_dwordx4 a[84:87], v252, s[8:11], s20 offen offset:1024 sc1\n\t"
"v_accvgpr_read_b32 v225, a97\n\t"
"v_accvgpr_read_b32 v227, a101\n\t"
"v_cvt_pk_bf16_f32 v230, v230, v231\n\t"
"v_cvt_pk_bf16_f32 v234, v234, v235\n\t"
"v_pk_fma_f32 v[224:225], v[226:227], s[56:57], v[224:225]\n\t"
"v_pk_fma_f32 v[226:227], v[224:225], s[58:59], v[226:227] neg_lo:[0,0,1] neg_hi:[0,0,1]\n\t"
"v_pk_fma_f32 v[228:229], v[60:61], s[60:61], v[224:225]\n\t"
"v_pk_fma_f32 v[60:61], v[224:225], s[60:61], v[60:61]\n\t"
"v_pk_fma_f32 v[224:225], v[160:161], s[62:63], v[226:227]\n\t"
"v_pk_fma_f32 v[160:161], v[226:227], s[62:63], v[160:161]\n\t"
"v_mfma_f32_16x16x32_bf16 v[20:23], a[220:223], a[188:191], v[20:23]\n\t"
"s_waitcnt lgkmcnt(1)\n\t"
"v_accvgpr_write_b32 a26, v230\n\t"
"v_cvt_pk_bf16_f32 v236, v236, v237\n\t"
"v_mfma_f32_16x16x32_bf16 v[20:23], a[228:231], a[192:195], v[20:23]\n\t"
"v_accvgpr_write_b32 a30, v234\n\t"
"v_cvt_pk_bf16_f32 v98, v98, v99\n\t"
"v_mfma_f32_16x16x32_bf16 v[20:23], a[244:247], a[196:199], v[20:23]\n\t"
"buffer_load_dwordx4 a[88:91], v252, s[8:11], s20 offen offset:2048 sc1\n\t"
"v_accvgpr_read_b32 v230, a98\n\t"
"v_accvgpr_read_b32 v232, a102\n\t"
"v_accvgpr_read_b32 v231, a99\n\t"
"v_accvgpr_read_b32 v233, a103\n\t"
"v_accvgpr_write_b32 a27, v236\n\t"
"v_accvgpr_write_b32 a31, v98\n\t"
"v_cvt_pk_bf16_f32 v224, v224, v225\n\t"
"v_cvt_pk_bf16_f32 v228, v228, v229\n\t"
"s_mov_b32 s20, 602112\n\t"
"v_pk_fma_f32 v[230:231], v[232:233], s[56:57], v[230:231]\n\t"
"v_pk_fma_f32 v[232:233], v[230:231], s[58:59], v[232:233] neg_lo:[0,0,1] neg_hi:[0,0,1]\n\t"
"v_pk_fma_f32 v[234:235], v[62:63], s[60:61], v[230:231]\n\t"
"v_pk_fma_f32 v[62:63], v[230:231], s[60:61], v[62:63]\n\t"
"v_pk_fma_f32 v[230:231], v[162:163], s[62:63], v[232:233]\n\t"
"v_pk_fma_f32 v[162:163], v[232:233], s[62:63], v[162:163]\n\t"
"v_mfma_f32_16x16x32_bf16 v[24:27], a[220:223], a[192:195], v[24:27]\n\t"
"s_waitcnt lgkmcnt(0)\n\t"
"v_accvgpr_write_b32 a34, v224\n\t"
"v_accvgpr_write_b32 a38, v228\n\t"
"v_mfma_f32_16x16x32_bf16 v[24:27], a[228:231], a[196:199], v[24:27]\n\t"
"v_accvgpr_read_b32 v236, a104\n\t"
"v_accvgpr_read_b32 v238, a108\n\t"
"v_mfma_f32_16x16x32_bf16 v[24:27], a[244:247], a[200:203], v[24:27]\n\t"
"buffer_load_dwordx4 a[92:95], v252, s[8:11], s20 offen offset:0 sc1\n\t"
"v_accvgpr_read_b32 v237, a105\n\t"
"v_accvgpr_read_b32 v239, a109\n\t"
"v_cvt_pk_bf16_f32 v230, v230, v231\n\t"
"v_cvt_pk_bf16_f32 v234, v234, v235\n\t"
"v_pk_fma_f32 v[236:237], v[238:239], s[56:57], v[236:237]\n\t"
"v_pk_fma_f32 v[238:239], v[236:237], s[58:59], v[238:239] neg_lo:[0,0,1] neg_hi:[0,0,1]\n\t"
"v_pk_fma_f32 v[98:99], v[64:65], s[60:61], v[236:237]\n\t"
"v_pk_fma_f32 v[64:65], v[236:237], s[60:61], v[64:65]\n\t"
"v_pk_fma_f32 v[236:237], v[164:165], s[62:63], v[238:239]\n\t"
"v_pk_fma_f32 v[164:165], v[238:239], s[62:63], v[164:165]\n\t"
"v_mfma_f32_16x16x32_bf16 v[28:31], a[220:223], a[196:199], v[28:31]\n\t"
"s_waitcnt vmcnt(17)\n\t"
"v_accvgpr_write_b32 a35, v230\n\t"
"v_accvgpr_write_b32 a39, v234\n\t"
"v_mfma_f32_16x16x32_bf16 v[28:31], a[228:231], a[200:203], v[28:31]\n\t"
"Lpg11_%=:\n\t"
"s_cbranch_vccnz Lvr0_%=\n\t"
"v_accvgpr_read_b32 v224, a106\n\t"
"v_accvgpr_read_b32 v226, a110\n\t"
"v_mfma_f32_16x16x32_bf16 v[28:31], a[244:247], a[204:207], v[28:31]\n\t"
"buffer_load_dwordx4 a[96:99], v252, s[8:11], s20 offen offset:1024 sc1\n\t"
"v_accvgpr_read_b32 v225, a107\n\t"
"v_accvgpr_read_b32 v227, a111\n\t"
"v_cvt_pk_bf16_f32 v236, v236, v237\n\t"
"v_cvt_pk_bf16_f32 v98, v98, v99\n\t"
"v_pk_fma_f32 v[224:225], v[226:227], s[56:57], v[224:225]\n\t"
"v_pk_fma_f32 v[226:227], v[224:225], s[58:59], v[226:227] neg_lo:[0,0,1] neg_hi:[0,0,1]\n\t"
"v_pk_fma_f32 v[228:229], v[66:67], s[60:61], v[224:225]\n\t"
"v_pk_fma_f32 v[66:67], v[224:225], s[60:61], v[66:67]\n\t"
"v_pk_fma_f32 v[224:225], v[166:167], s[62:63], v[226:227]\n\t"
"v_pk_fma_f32 v[166:167], v[226:227], s[62:63], v[166:167]\n\t"
"v_mfma_f32_16x16x32_bf16 v[32:35], v[240:243], a[168:171], v[32:35]\n\t"
"v_accvgpr_write_b32 a104, v236\n\t"
"v_accvgpr_write_b32 a106, v98\n\t"
"v_mfma_f32_16x16x32_bf16 v[32:35], a[224:227], a[172:175], v[32:35]\n\t"
"v_accvgpr_read_b32 v230, a112\n\t"
"v_accvgpr_read_b32 v232, a116\n\t"
"v_mfma_f32_16x16x32_bf16 v[32:35], a[248:251], a[176:179], v[32:35]\n\t"
"buffer_load_dwordx4 a[100:103], v252, s[8:11], s20 offen offset:2048 sc1\n\t"
"v_accvgpr_read_b32 v231, a113\n\t"
"v_accvgpr_read_b32 v233, a117\n\t"
"v_cvt_pk_bf16_f32 v224, v224, v225\n\t"
"v_cvt_pk_bf16_f32 v228, v228, v229\n\t"
"s_mov_b32 s20, 614400\n\t"
"v_pk_fma_f32 v[230:231], v[232:233], s[56:57], v[230:231]\n\t"
"v_pk_fma_f32 v[232:233], v[230:231], s[58:59], v[232:233] neg_lo:[0,0,1] neg_hi:[0,0,1]\n\t"
"v_pk_fma_f32 v[234:235], v[68:69], s[60:61], v[230:231]\n\t"
"v_pk_fma_f32 v[68:69], v[230:231], s[60:61], v[68:69]\n\t"
"v_pk_fma_f32 v[230:231], v[168:169], s[62:63], v[232:233]\n\t"
"v_pk_fma_f32 v[168:169], v[232:233], s[62:63], v[168:169]\n\t"
"v_mfma_f32_16x16x32_bf16 v[36:39], v[240:243], a[172:175], v[36:39]\n\t"
"v_accvgpr_write_b32 a105, v224\n\t"
"v_accvgpr_write_b32 a107, v228\n\t"
"v_mfma_f32_16x16x32_bf16 v[36:39], a[224:227], a[176:179], v[36:39]\n\t"
"v_accvgpr_read_b32 v236, a114\n\t"
"v_accvgpr_read_b32 v238, a118\n\t"
"v_mfma_f32_16x16x32_bf16 v[36:39], a[248:251], a[180:183], v[36:39]\n\t"
"buffer_load_dwordx4 a[208:211], v252, s[8:11], s20 offen offset:0 sc1\n\t"
"v_accvgpr_read_b32 v237, a115\n\t"
"v_accvgpr_read_b32 v239, a119\n\t"
"v_cvt_pk_bf16_f32 v230, v230, v231\n\t"
"v_cvt_pk_bf16_f32 v234, v234, v235\n\t"
"v_pk_fma_f32 v[236:237], v[238:239], s[56:57], v[236:237]\n\t"
"v_pk_fma_f32 v[238:239], v[236:237], s[58:59], v[238:239] neg_lo:[0,0,1] neg_hi:[0,0,1]\n\t"
"v_pk_fma_f32 v[98:99], v[70:71], s[60:61], v[236:237]\n\t"
"v_pk_fma_f32 v[70:71], v[236:237], s[60:61], v[70:71]\n\t"
"v_pk_fma_f32 v[236:237], v[170:171], s[62:63], v[238:239]\n\t"
"v_pk_fma_f32 v[170:171], v[238:239], s[62:63], v[170:171]\n\t"
"v_mfma_f32_16x16x32_bf16 v[40:43], v[240:243], a[176:179], v[40:43]\n\t"
"v_accvgpr_write_b32 a112, v230\n\t"
"v_accvgpr_write_b32 a114, v234\n\t"
"v_mfma_f32_16x16x32_bf16 v[40:43], a[224:227], a[180:183], v[40:43]\n\t"
"v_accvgpr_read_b32 v224, a120\n\t"
"v_accvgpr_read_b32 v226, a124\n\t"
"v_mfma_f32_16x16x32_bf16 v[40:43], a[248:251], a[184:187], v[40:43]\n\t"
"buffer_load_dwordx4 a[212:215], v252, s[8:11], s20 offen offset:1024 sc1\n\t"
"v_accvgpr_read_b32 v225, a121\n\t"
"v_accvgpr_read_b32 v227, a125\n\t"
"v_cvt_pk_bf16_f32 v236, v236, v237\n\t"
"v_cvt_pk_bf16_f32 v98, v98, v99\n\t"
"v_pk_fma_f32 v[224:225], v[226:227], s[56:57], v[224:225]\n\t"
"v_pk_fma_f32 v[226:227], v[224:225], s[58:59], v[226:227] neg_lo:[0,0,1] neg_hi:[0,0,1]\n\t"
"v_pk_fma_f32 v[228:229], v[72:73], s[60:61], v[224:225]\n\t"
"v_pk_fma_f32 v[72:73], v[224:225], s[60:61], v[72:73]\n\t"
"v_pk_fma_f32 v[224:225], v[172:173], s[62:63], v[226:227]\n\t"
"v_pk_fma_f32 v[172:173], v[226:227], s[62:63], v[172:173]\n\t"
"v_mfma_f32_16x16x32_bf16 v[44:47], v[240:243], a[180:183], v[44:47]\n\t"
"v_accvgpr_write_b32 a113, v236\n\t"
"v_accvgpr_write_b32 a115, v98\n\t"
"v_mfma_f32_16x16x32_bf16 v[44:47], a[224:227], a[184:187], v[44:47]\n\t"
"v_accvgpr_read_b32 v230, a122\n\t"
"v_accvgpr_read_b32 v232, a126\n\t"
"v_mfma_f32_16x16x32_bf16 v[44:47], a[248:251], a[188:191], v[44:47]\n\t"
"buffer_load_dwordx4 a[116:119], v252, s[8:11], s20 offen offset:2048 sc1\n\t"
"v_accvgpr_read_b32 v231, a123\n\t"
"v_accvgpr_read_b32 v233, a127\n\t"
"v_accvgpr_read_b32 v236, a128\n\t"
"v_accvgpr_read_b32 v238, a132\n\t"
"v_accvgpr_read_b32 v237, a129\n\t"
"v_accvgpr_read_b32 v239, a133\n\t"
"v_cvt_pk_bf16_f32 v224, v224, v225\n\t"
"v_cvt_pk_bf16_f32 v228, v228, v229\n\t"
"s_mov_b32 s20, 774144\n\t"
"v_pk_fma_f32 v[230:231], v[232:233], s[56:57], v[230:231]\n\t"
"v_pk_fma_f32 v[232:233], v[230:231], s[58:59], v[232:233] neg_lo:[0,0,1] neg_hi:[0,0,1]\n\t"
"v_pk_fma_f32 v[234:235], v[74:75], s[60:61], v[230:231]\n\t"
"v_pk_fma_f32 v[74:75], v[230:231], s[60:61], v[74:75]\n\t"
"v_pk_fma_f32 v[230:231], v[174:175], s[62:63], v[232:233]\n\t"
"v_pk_fma_f32 v[174:175], v[232:233], s[62:63], v[174:175]\n\t"
"v_pk_fma_f32 v[236:237], v[238:239], s[56:57], v[236:237]\n\t"
"v_pk_fma_f32 v[238:239], v[236:237], s[58:59], v[238:239] neg_lo:[0,0,1] neg_hi:[0,0,1]\n\t"
"v_pk_fma_f32 v[98:99], v[76:77], s[60:61], v[236:237]\n\t"
"v_pk_fma_f32 v[76:77], v[236:237], s[60:61], v[76:77]\n\t"
"v_pk_fma_f32 v[236:237], v[176:177], s[62:63], v[238:239]\n\t"
"v_pk_fma_f32 v[176:177], v[238:239], s[62:63], v[176:177]\n\t"
"v_mfma_f32_16x16x32_bf16 v[48:51], v[240:243], a[184:187], v[48:51]\n\t"
"v_accvgpr_write_b32 a120, v224\n\t"
"v_accvgpr_write_b32 a122, v228\n\t"
"v_mfma_f32_16x16x32_bf16 v[48:51], a[224:227], a[188:191], v[48:51]\n\t"
"v_accvgpr_read_b32 v224, a130\n\t"
"v_accvgpr_read_b32 v226, a134\n\t"
"v_mfma_f32_16x16x32_bf16 v[48:51], a[248:251], a[192:195], v[48:51]\n\t"
"buffer_load_dwordx4 a[216:219], v252, s[8:11], s20 offen offset:0 sc1\n\t"
"v_accvgpr_read_b32 v225, a131\n\t"
"v_accvgpr_read_b32 v227, a135\n\t"
"v_cvt_pk_bf16_f32 v230, v230, v231\n\t"
"v_cvt_pk_bf16_f32 v234, v234, v235\n\t"
"v_cvt_pk_bf16_f32 v236, v236, v237\n\t"
"v_cvt_pk_bf16_f32 v98, v98, v99\n\t"
"v_pk_fma_f32 v[224:225], v[226:227], s[56:57], v[224:225]\n\t"
"v_pk_fma_f32 v[226:227], v[224:225], s[58:59], v[226:227] neg_lo:[0,0,1] neg_hi:[0,0,1]\n\t"
"v_pk_fma_f32 v[228:229], v[78:79], s[60:61], v[224:225]\n\t"
"v_pk_fma_f32 v[78:79], v[224:225], s[60:61], v[78:79]\n\t"
"v_pk_fma_f32 v[224:225], v[178:179], s[62:63], v[226:227]\n\t"
"v_pk_fma_f32 v[178:179], v[226:227], s[62:63], v[178:179]\n\t"
"v_mfma_f32_16x16x32_bf16 v[52:55], v[240:243], a[188:191], v[52:55]\n\t"
"v_accvgpr_write_b32 a121, v230\n\t"
"v_accvgpr_write_b32 a123, v234\n\t"
"v_mfma_f32_16x16x32_bf16 v[52:55], a[224:227], a[192:195], v[52:55]\n\t"
"v_accvgpr_write_b32 a128, v236\n\t"
"v_accvgpr_write_b32 a130, v98\n\t"
"v_mfma_f32_16x16x32_bf16 v[52:55], a[248:251], a[196:199], v[52:55]\n\t"
"buffer_load_dwordx4 a[124:127], v252, s[8:11], s20 offen offset:1024 sc1\n\t"
"v_accvgpr_read_b32 v230, a136\n\t"
"v_accvgpr_read_b32 v232, a140\n\t"
"v_accvgpr_read_b32 v231, a137\n\t"
"v_accvgpr_read_b32 v233, a141\n\t"
"v_cvt_pk_bf16_f32 v224, v224, v225\n\t"
"v_cvt_pk_bf16_f32 v228, v228, v229\n\t"
"v_pk_fma_f32 v[230:231], v[232:233], s[56:57], v[230:231]\n\t"
"v_pk_fma_f32 v[232:233], v[230:231], s[58:59], v[232:233] neg_lo:[0,0,1] neg_hi:[0,0,1]\n\t"
"v_pk_fma_f32 v[234:235], v[80:81], s[60:61], v[230:231]\n\t"
"v_pk_fma_f32 v[80:81], v[230:231], s[60:61], v[80:81]\n\t"
"v_pk_fma_f32 v[230:231], v[180:181], s[62:63], v[232:233]\n\t"
"v_pk_fma_f32 v[180:181], v[232:233], s[62:63], v[180:181]\n\t"
"v_mfma_f32_16x16x32_bf16 v[56:59], v[240:243], a[192:195], v[56:59]\n\t"
"v_accvgpr_write_b32 a129, v224\n\t"
"v_accvgpr_write_b32 a131, v228\n\t"
"v_mfma_f32_16x16x32_bf16 v[56:59], a[224:227], a[196:199], v[56:59]\n\t"
"v_accvgpr_read_b32 v236, a138\n\t"
"v_accvgpr_read_b32 v238, a142\n\t"
"v_mfma_f32_16x16x32_bf16 v[56:59], a[248:251], a[200:203], v[56:59]\n\t"
"s_waitcnt vmcnt(19)\n\t"
"ds_write_b128 v249, v[196:199] offset:0\n\t"
"v_accvgpr_read_b32 v237, a139\n\t"
"v_accvgpr_read_b32 v239, a143\n\t"
"v_cvt_pk_bf16_f32 v230, v230, v231\n\t"
"v_cvt_pk_bf16_f32 v234, v234, v235\n\t"
"v_pk_fma_f32 v[236:237], v[238:239], s[56:57], v[236:237]\n\t"
"v_pk_fma_f32 v[238:239], v[236:237], s[58:59], v[238:239] neg_lo:[0,0,1] neg_hi:[0,0,1]\n\t"
"v_pk_fma_f32 v[98:99], v[82:83], s[60:61], v[236:237]\n\t"
"v_pk_fma_f32 v[82:83], v[236:237], s[60:61], v[82:83]\n\t"
"v_pk_fma_f32 v[236:237], v[182:183], s[62:63], v[238:239]\n\t"
"v_pk_fma_f32 v[182:183], v[238:239], s[62:63], v[182:183]\n\t"
"v_mfma_f32_16x16x32_bf16 v[60:63], v[240:243], a[196:199], v[60:63]\n\t"
"v_accvgpr_write_b32 a136, v230\n\t"
"v_accvgpr_write_b32 a138, v234\n\t"
"v_mfma_f32_16x16x32_bf16 v[60:63], a[224:227], a[200:203], v[60:63]\n\t"
"v_accvgpr_read_b32 v224, a144\n\t"
"v_accvgpr_read_b32 v226, a148\n\t"
"v_mfma_f32_16x16x32_bf16 v[60:63], a[248:251], a[204:207], v[60:63]\n\t"
"buffer_load_dwordx4 a[132:135], v252, s[8:11], s20 offen offset:2048 sc1\n\t"
"v_accvgpr_read_b32 v225, a145\n\t"
"v_accvgpr_read_b32 v227, a149\n\t"
"v_cvt_pk_bf16_f32 v236, v236, v237\n\t"
"v_cvt_pk_bf16_f32 v98, v98, v99\n\t"
"s_mov_b32 s20, 786432\n\t"
"v_pk_fma_f32 v[224:225], v[226:227], s[56:57], v[224:225]\n\t"
"v_pk_fma_f32 v[226:227], v[224:225], s[58:59], v[226:227] neg_lo:[0,0,1] neg_hi:[0,0,1]\n\t"
"v_pk_fma_f32 v[228:229], v[84:85], s[60:61], v[224:225]\n\t"
"v_pk_fma_f32 v[84:85], v[224:225], s[60:61], v[84:85]\n\t"
"v_pk_fma_f32 v[224:225], v[184:185], s[62:63], v[226:227]\n\t"
"v_pk_fma_f32 v[184:185], v[226:227], s[62:63], v[184:185]\n\t"
"v_mfma_f32_16x16x32_bf16 v[64:67], a[252:255], a[168:171], v[64:67]\n\t"
"ds_read_b128 a[168:171], v255 offset:2048\n\t"
"v_accvgpr_write_b32 a137, v236\n\t"
"v_mfma_f32_16x16x32_bf16 v[64:67], a[232:235], a[172:175], v[64:67]\n\t"
"v_accvgpr_write_b32 a139, v98\n\t"
"v_accvgpr_read_b32 v230, a146\n\t"
"v_mfma_f32_16x16x32_bf16 v[64:67], a[236:239], a[176:179], v[64:67]\n\t"
"buffer_load_dwordx4 a[220:223], v252, s[8:11], s20 offen offset:0 sc1\n\t"
"v_accvgpr_read_b32 v232, a150\n\t"
"v_accvgpr_read_b32 v231, a147\n\t"
"v_accvgpr_read_b32 v233, a151\n\t"
"v_cvt_pk_bf16_f32 v224, v224, v225\n\t"
"v_cvt_pk_bf16_f32 v228, v228, v229\n\t"
"v_pk_fma_f32 v[230:231], v[232:233], s[56:57], v[230:231]\n\t"
"v_pk_fma_f32 v[232:233], v[230:231], s[58:59], v[232:233] neg_lo:[0,0,1] neg_hi:[0,0,1]\n\t"
"v_pk_fma_f32 v[234:235], v[86:87], s[60:61], v[230:231]\n\t"
"v_pk_fma_f32 v[86:87], v[230:231], s[60:61], v[86:87]\n\t"
"v_pk_fma_f32 v[230:231], v[186:187], s[62:63], v[232:233]\n\t"
"v_pk_fma_f32 v[186:187], v[232:233], s[62:63], v[186:187]\n\t"
"v_mfma_f32_16x16x32_bf16 v[68:71], a[252:255], a[172:175], v[68:71]\n\t"
"ds_read_b128 a[172:175], v255 offset:5120\n\t"
"v_accvgpr_write_b32 a144, v224\n\t"
"v_mfma_f32_16x16x32_bf16 v[68:71], a[232:235], a[176:179], v[68:71]\n\t"
"v_accvgpr_write_b32 a146, v228\n\t"
"v_accvgpr_read_b32 v236, a152\n\t"
"v_mfma_f32_16x16x32_bf16 v[68:71], a[236:239], a[180:183], v[68:71]\n\t"
"ds_write_b64 v250, a[104:105] offset:0\n\t"
"v_accvgpr_read_b32 v238, a156\n\t"
"v_accvgpr_read_b32 v237, a153\n\t"
"v_accvgpr_read_b32 v239, a157\n\t"
"v_cvt_pk_bf16_f32 v230, v230, v231\n\t"
"v_cvt_pk_bf16_f32 v234, v234, v235\n\t"
"v_pk_fma_f32 v[236:237], v[238:239], s[56:57], v[236:237]\n\t"
"v_pk_fma_f32 v[238:239], v[236:237], s[58:59], v[238:239] neg_lo:[0,0,1] neg_hi:[0,0,1]\n\t"
"v_pk_fma_f32 v[98:99], v[88:89], s[60:61], v[236:237]\n\t"
"v_pk_fma_f32 v[88:89], v[236:237], s[60:61], v[88:89]\n\t"
"v_pk_fma_f32 v[236:237], v[188:189], s[62:63], v[238:239]\n\t"
"v_pk_fma_f32 v[188:189], v[238:239], s[62:63], v[188:189]\n\t"
"v_mfma_f32_16x16x32_bf16 v[72:75], a[252:255], a[176:179], v[72:75]\n\t"
"ds_read_b128 a[176:179], v255 offset:8192\n\t"
"v_accvgpr_write_b32 a145, v230\n\t"
"v_mfma_f32_16x16x32_bf16 v[72:75], a[232:235], a[180:183], v[72:75]\n\t"
"v_accvgpr_write_b32 a147, v234\n\t"
"v_accvgpr_read_b32 v224, a154\n\t"
"v_mfma_f32_16x16x32_bf16 v[72:75], a[236:239], a[184:187], v[72:75]\n\t"
"buffer_load_dwordx4 v[240:243], v252, s[8:11], s20 offen offset:1024 sc1\n\t"
"v_accvgpr_read_b32 v226, a158\n\t"
"v_accvgpr_read_b32 v225, a155\n\t"
"v_accvgpr_read_b32 v227, a159\n\t"
"v_cvt_pk_bf16_f32 v236, v236, v237\n\t"
"v_cvt_pk_bf16_f32 v98, v98, v99\n\t"
"v_pk_fma_f32 v[224:225], v[226:227], s[56:57], v[224:225]\n\t"
"v_pk_fma_f32 v[226:227], v[224:225], s[58:59], v[226:227] neg_lo:[0,0,1] neg_hi:[0,0,1]\n\t"
"v_pk_fma_f32 v[228:229], v[90:91], s[60:61], v[224:225]\n\t"
"v_pk_fma_f32 v[90:91], v[224:225], s[60:61], v[90:91]\n\t"
"v_pk_fma_f32 v[224:225], v[190:191], s[62:63], v[226:227]\n\t"
"v_pk_fma_f32 v[190:191], v[226:227], s[62:63], v[190:191]\n\t"
"v_mfma_f32_16x16x32_bf16 v[76:79], a[252:255], a[180:183], v[76:79]\n\t"
"ds_read_b128 a[180:183], v255 offset:11264\n\t"
"v_accvgpr_write_b32 a152, v236\n\t"
"v_mfma_f32_16x16x32_bf16 v[76:79], a[232:235], a[184:187], v[76:79]\n\t"
"v_accvgpr_write_b32 a154, v98\n\t"
"v_accvgpr_read_b32 v230, a160\n\t"
"v_mfma_f32_16x16x32_bf16 v[76:79], a[236:239], a[188:191], v[76:79]\n\t"
"buffer_load_dwordx4 a[140:143], v252, s[8:11], s20 offen offset:2048 sc1\n\t"
"v_accvgpr_read_b32 v232, a164\n\t"
"v_accvgpr_read_b32 v231, a161\n\t"
"v_accvgpr_read_b32 v233, a165\n\t"
"v_cvt_pk_bf16_f32 v224, v224, v225\n\t"
"v_cvt_pk_bf16_f32 v228, v228, v229\n\t"
"s_mov_b32 s20, 798720\n\t"
"v_pk_fma_f32 v[230:231], v[232:233], s[56:57], v[230:231]\n\t"
"v_pk_fma_f32 v[232:233], v[230:231], s[58:59], v[232:233] neg_lo:[0,0,1] neg_hi:[0,0,1]\n\t"
"v_pk_fma_f32 v[234:235], v[92:93], s[60:61], v[230:231]\n\t"
"v_pk_fma_f32 v[92:93], v[230:231], s[60:61], v[92:93]\n\t"
"v_pk_fma_f32 v[230:231], v[192:193], s[62:63], v[232:233]\n\t"
"v_pk_fma_f32 v[192:193], v[232:233], s[62:63], v[192:193]\n\t"
"v_mfma_f32_16x16x32_bf16 v[80:83], a[252:255], a[184:187], v[80:83]\n\t"
"ds_read_b128 a[184:187], v255 offset:14336\n\t"
"v_accvgpr_write_b32 a153, v224\n\t"
"v_mfma_f32_16x16x32_bf16 v[80:83], a[232:235], a[188:191], v[80:83]\n\t"
"v_accvgpr_write_b32 a155, v228\n\t"
"v_accvgpr_read_b32 v236, a162\n\t"
"v_mfma_f32_16x16x32_bf16 v[80:83], a[236:239], a[192:195], v[80:83]\n\t"
"buffer_load_dwordx4 a[148:151], v252, s[8:11], s20 offen offset:0 sc1\n\t"
"v_accvgpr_read_b32 v238, a166\n\t"
"v_accvgpr_read_b32 v237, a163\n\t"
"v_accvgpr_read_b32 v239, a167\n\t"
"v_cvt_pk_bf16_f32 v230, v230, v231\n\t"
"v_cvt_pk_bf16_f32 v234, v234, v235\n\t"
"v_pk_fma_f32 v[236:237], v[238:239], s[56:57], v[236:237]\n\t"
"v_pk_fma_f32 v[238:239], v[236:237], s[58:59], v[238:239] neg_lo:[0,0,1] neg_hi:[0,0,1]\n\t"
"v_pk_fma_f32 v[98:99], v[94:95], s[60:61], v[236:237]\n\t"
"v_pk_fma_f32 v[94:95], v[236:237], s[60:61], v[94:95]\n\t"
"v_pk_fma_f32 v[236:237], v[194:195], s[62:63], v[238:239]\n\t"
"v_pk_fma_f32 v[194:195], v[238:239], s[62:63], v[194:195]\n\t"
"v_mfma_f32_16x16x32_bf16 v[84:87], a[252:255], a[188:191], v[84:87]\n\t"
"ds_read_b128 a[188:191], v255 offset:17408\n\t"
"v_accvgpr_write_b32 a160, v230\n\t"
"v_mfma_f32_16x16x32_bf16 v[84:87], a[232:235], a[192:195], v[84:87]\n\t"
"v_accvgpr_write_b32 a162, v234\n\t"
"v_cvt_pk_bf16_f32 v236, v236, v237\n\t"
"v_mfma_f32_16x16x32_bf16 v[84:87], a[236:239], a[196:199], v[84:87]\n\t"
"buffer_load_dwordx4 a[224:227], v252, s[8:11], s20 offen offset:1024 sc1\n\t"
"v_cvt_pk_bf16_f32 v98, v98, v99\n\t"
"v_mfma_f32_16x16x32_bf16 v[88:91], a[252:255], a[192:195], v[88:91]\n\t"
"ds_read_b128 a[192:195], v255 offset:20480\n\t"
"v_accvgpr_write_b32 a161, v236\n\t"
"v_mfma_f32_16x16x32_bf16 v[88:91], a[232:235], a[196:199], v[88:91]\n\t"
"v_accvgpr_write_b32 a163, v98\n\t"
"v_mfma_f32_16x16x32_bf16 v[88:91], a[236:239], a[200:203], v[88:91]\n\t"
"buffer_load_dwordx4 a[228:231], v252, s[8:11], s20 offen offset:2048 sc1\n\t"
"ds_write_b128 v249, v[200:203] offset:6144\n\t"
"s_mov_b32 s20, 884736\n\t"
"v_mfma_f32_16x16x32_bf16 v[92:95], a[252:255], a[196:199], v[92:95]\n\t"
"buffer_load_dwordx4 a[244:247], v252, s[8:11], s20 offen offset:0 sc1\n\t"
"v_mfma_f32_16x16x32_bf16 v[92:95], a[232:235], a[200:203], v[92:95]\n\t"
"buffer_load_dwordx4 a[164:167], v252, s[8:11], s20 offen offset:1024 sc1\n\t"
"v_mfma_f32_16x16x32_bf16 v[92:95], a[236:239], a[204:207], v[92:95]\n\t"
"s_waitcnt vmcnt(26) lgkmcnt(5)\n\t"
"ds_write_b64 v250, a[106:107] offset:6144\n\t"
"v_mfma_f32_16x16x32_bf16 v[100:103], a[240:243], a[168:171], v[100:103]\n\t"
"buffer_load_dwordx4 a[248:251], v252, s[8:11], s20 offen offset:2048 sc1\n\t"
"v_mfma_f32_16x16x32_bf16 v[100:103], a[40:43], a[172:175], v[100:103]\n\t"
"s_waitcnt lgkmcnt(5)\n\t"
"ds_read_b128 a[196:199], v255 offset:23552\n\t"
"s_mov_b32 s20, 897024\n\t"
"v_mfma_f32_16x16x32_bf16 v[100:103], a[48:51], a[176:179], v[100:103]\n\t"
"s_add_u32 m0, s25, 512\n\t"
"ds_read_b128 a[200:203], v255 offset:26624\n\t"
"v_mfma_f32_16x16x32_bf16 v[104:107], a[240:243], a[172:175], v[104:107]\n\t"
"s_waitcnt lgkmcnt(6)\n\t"
"ds_read_b128 a[204:207], v255 offset:29696\n\t"
"v_mfma_f32_16x16x32_bf16 v[104:107], a[40:43], a[176:179], v[104:107]\n\t"
"v_mfma_f32_16x16x32_bf16 v[104:107], a[48:51], a[180:183], v[104:107]\n\t"
"s_waitcnt lgkmcnt(0)\n\t"
"s_barrier\n\t"
"v_mfma_f32_16x16x32_bf16 v[108:111], a[240:243], a[176:179], v[108:111]\n\t"
"v_mfma_f32_16x16x32_bf16 v[108:111], a[40:43], a[180:183], v[108:111]\n\t"
"buffer_load_dwordx4 v[236:239], v252, s[8:11], s20 offen offset:0 sc1\n\t"
"v_mfma_f32_16x16x32_bf16 v[108:111], a[48:51], a[184:187], v[108:111]\n\t"
"buffer_load_dwordx4 v[96:99], v252, s[8:11], s20 offen offset:1024 sc1\n\t"
"v_mfma_f32_16x16x32_bf16 v[112:115], a[240:243], a[180:183], v[112:115]\n\t"
"buffer_load_dwordx4 v254, s[12:15], 0 offen offset:0 nt lds\n\t"
"v_mfma_f32_16x16x32_bf16 v[112:115], a[40:43], a[184:187], v[112:115]\n\t"
"buffer_load_dwordx4 a[232:235], v252, s[8:11], s20 offen offset:2048 sc1\n\t"
"v_mfma_f32_16x16x32_bf16 v[112:115], a[48:51], a[188:191], v[112:115]\n\t"
"s_add_u32 m0, s24, 512\n\t"
"s_add_u32 s28, s21, 0\n\t"
"ds_write_b128 v249, v[204:207] offset:12288\n\t"
"v_mfma_f32_16x16x32_bf16 v[116:119], a[240:243], a[184:187], v[116:119]\n\t"
"buffer_load_dwordx4 v247, s[12:15], s28 offen offset:0 nt lds\n\t"
"v_mfma_f32_16x16x32_bf16 v[116:119], a[40:43], a[188:191], v[116:119]\n\t"
"s_add_u32 m0, s24, 6656\n\t"
"s_add_u32 s28, s21, 196608\n\t"
"v_mfma_f32_16x16x32_bf16 v[116:119], a[48:51], a[192:195], v[116:119]\n\t"
"buffer_load_dwordx4 v247, s[12:15], s28 offen offset:0 nt lds\n\t"
"s_add_u32 m0, s24, 12800\n\t"
"s_add_u32 s28, s21, 393216\n\t"
"v_mfma_f32_16x16x32_bf16 v[120:123], a[240:243], a[188:191], v[120:123]\n\t"
"buffer_load_dwordx4 v247, s[12:15], s28 offen offset:0 nt lds\n\t"
"v_mfma_f32_16x16x32_bf16 v[120:123], a[40:43], a[192:195], v[120:123]\n\t"
"s_add_u32 m0, s24, 20480\n\t"
"s_add_u32 s28, s21, 589824\n\t"
"v_mfma_f32_16x16x32_bf16 v[120:123], a[48:51], a[196:199], v[120:123]\n\t"
"buffer_load_dwordx4 v247, s[12:15], s28 offen offset:0 nt lds\n\t"
"v_mfma_f32_16x16x32_bf16 v[124:127], a[240:243], a[192:195], v[124:127]\n\t"
"v_mfma_f32_16x16x32_bf16 v[124:127], a[40:43], a[196:199], v[124:127]\n\t"
"v_mfma_f32_16x16x32_bf16 v[124:127], a[48:51], a[200:203], v[124:127]\n\t"
"ds_write_b64 v250, a[112:113] offset:12288\n\t"
"v_mfma_f32_16x16x32_bf16 v[128:131], a[240:243], a[196:199], v[128:131]\n\t"
"s_waitcnt vmcnt(32)\n\t"
"v_mfma_f32_16x16x32_bf16 v[128:131], a[40:43], a[200:203], v[128:131]\n\t"
"v_mfma_f32_16x16x32_bf16 v[128:131], a[48:51], a[204:207], v[128:131]\n\t"
"ds_write_b128 v249, v[208:211] offset:18432\n\t"
"v_mfma_f32_16x16x32_bf16 v[132:135], a[52:55], a[168:171], v[132:135]\n\t"
"v_mfma_f32_16x16x32_bf16 v[132:135], a[44:47], a[172:175], v[132:135]\n\t"
"v_mfma_f32_16x16x32_bf16 v[132:135], a[60:63], a[176:179], v[132:135]\n\t"
"ds_write_b64 v250, a[114:115] offset:18432\n\t"
"v_mfma_f32_16x16x32_bf16 v[136:139], a[52:55], a[172:175], v[136:139]\n\t"
"v_mfma_f32_16x16x32_bf16 v[136:139], a[44:47], a[176:179], v[136:139]\n\t"
"v_mfma_f32_16x16x32_bf16 v[136:139], a[60:63], a[180:183], v[136:139]\n\t"
"v_mfma_f32_16x16x32_bf16 v[140:143], a[52:55], a[176:179], v[140:143]\n\t"
"v_mfma_f32_16x16x32_bf16 v[140:143], a[44:47], a[180:183], v[140:143]\n\t"
"v_mfma_f32_16x16x32_bf16 v[140:143], a[60:63], a[184:187], v[140:143]\n\t"
"s_mov_b32 s20, 909312\n\t"
"v_mfma_f32_16x16x32_bf16 v[144:147], a[52:55], a[180:183], v[144:147]\n\t"
"v_mfma_f32_16x16x32_bf16 v[144:147], a[44:47], a[184:187], v[144:147]\n\t"
"v_mfma_f32_16x16x32_bf16 v[144:147], a[60:63], a[188:191], v[144:147]\n\t"
"v_mfma_f32_16x16x32_bf16 v[148:151], a[52:55], a[184:187], v[148:151]\n\t"
"v_mfma_f32_16x16x32_bf16 v[148:151], a[44:47], a[188:191], v[148:151]\n\t"
"v_mfma_f32_16x16x32_bf16 v[148:151], a[60:63], a[192:195], v[148:151]\n\t"
"buffer_load_dwordx4 a[104:107], v252, s[8:11], s20 offen offset:0 sc1\n\t"
"v_mfma_f32_16x16x32_bf16 v[152:155], a[52:55], a[188:191], v[152:155]\n\t"
"ds_read_b128 a[40:43], v252 offset:0\n\t"
"v_mfma_f32_16x16x32_bf16 v[152:155], a[44:47], a[192:195], v[152:155]\n\t"
"ds_read_b128 a[48:51], v252 offset:1024\n\t"
"v_mfma_f32_16x16x32_bf16 v[152:155], a[60:63], a[196:199], v[152:155]\n\t"
"v_mfma_f32_16x16x32_bf16 v[156:159], a[52:55], a[192:195], v[156:159]\n\t"
"s_add_u32 m0, s24, 26624\n\t"
"s_add_u32 s28, s21, 786432\n\t"
"ds_read_b128 a[56:59], v252 offset:2048\n\t"
"v_mfma_f32_16x16x32_bf16 v[156:159], a[44:47], a[196:199], v[156:159]\n\t"
"v_mfma_f32_16x16x32_bf16 v[156:159], a[60:63], a[200:203], v[156:159]\n\t"
"v_mfma_f32_16x16x32_bf16 v[160:163], a[52:55], a[196:199], v[160:163]\n\t"
"s_waitcnt vmcnt(30)\n\t"
"v_mfma_f32_16x16x32_bf16 v[160:163], a[44:47], a[200:203], v[160:163]\n\t"
"buffer_load_dwordx4 v247, s[12:15], s28 offen offset:0 nt lds\n\t"
"v_mfma_f32_16x16x32_bf16 v[160:163], a[60:63], a[204:207], v[160:163]\n\t"
"s_add_u32 m0, s24, 32768\n\t"
"s_add_u32 s28, s21, 983040\n\t"
"v_mfma_f32_16x16x32_bf16 v[164:167], a[64:67], a[168:171], v[164:167]\n\t"
"buffer_load_dwordx4 v247, s[12:15], s28 offen offset:0 nt lds\n\t"
"v_mfma_f32_16x16x32_bf16 v[164:167], a[68:71], a[172:175], v[164:167]\n\t"
"s_add_u32 m0, s24, 40448\n\t"
"s_add_u32 s28, s21, 1179648\n\t"
"ds_read_b128 a[168:171], v244 offset:24576\n\t"
"v_mfma_f32_16x16x32_bf16 v[164:167], a[72:75], a[176:179], v[164:167]\n\t"
"buffer_load_dwordx4 v247, s[12:15], s28 offen offset:0 nt lds\n\t"
"v_mfma_f32_16x16x32_bf16 v[168:171], a[64:67], a[172:175], v[168:171]\n\t"
"s_add_u32 m0, s24, 46592\n\t"
"s_add_u32 s28, s21, 1376256\n\t"
"ds_read_b128 a[172:175], v244 offset:25088\n\t"
"v_mfma_f32_16x16x32_bf16 v[168:171], a[68:71], a[176:179], v[168:171]\n\t"
"buffer_load_dwordx4 v247, s[12:15], s28 offen offset:0 nt lds\n\t"
"v_mfma_f32_16x16x32_bf16 v[168:171], a[72:75], a[180:183], v[168:171]\n\t"
"s_add_u32 m0, s24, 52736\n\t"
"s_add_u32 s28, s21, 1572864\n\t"
"v_mfma_f32_16x16x32_bf16 v[172:175], a[64:67], a[176:179], v[172:175]\n\t"
"buffer_load_dwordx4 v247, s[12:15], s28 offen offset:0 nt lds\n\t"
"v_mfma_f32_16x16x32_bf16 v[172:175], a[68:71], a[180:183], v[172:175]\n\t"
"ds_read_b128 a[176:179], v244 offset:25600\n\t"
"v_mfma_f32_16x16x32_bf16 v[172:175], a[72:75], a[184:187], v[172:175]\n\t"
"v_mfma_f32_16x16x32_bf16 v[176:179], a[64:67], a[180:183], v[176:179]\n\t"
"ds_read_b128 a[180:183], v244 offset:26112\n\t"
"v_mfma_f32_16x16x32_bf16 v[176:179], a[68:71], a[184:187], v[176:179]\n\t"
"v_mfma_f32_16x16x32_bf16 v[176:179], a[72:75], a[188:191], v[176:179]\n\t"
"s_waitcnt lgkmcnt(4)\n\t"
"s_barrier\n\t"
"v_mfma_f32_16x16x32_bf16 v[180:183], a[64:67], a[184:187], v[180:183]\n\t"
"ds_read_b128 a[184:187], v244 offset:26624\n\t"
"v_mfma_f32_16x16x32_bf16 v[180:183], a[68:71], a[188:191], v[180:183]\n\t"
"v_mfma_f32_16x16x32_bf16 v[180:183], a[72:75], a[192:195], v[180:183]\n\t"
"ds_write_b128 v249, v[212:215] offset:0\n\t"
"v_mfma_f32_16x16x32_bf16 v[184:187], a[64:67], a[188:191], v[184:187]\n\t"
"v_mfma_f32_16x16x32_bf16 v[184:187], a[68:71], a[192:195], v[184:187]\n\t"
"ds_read_b128 a[188:191], v244 offset:27136\n\t"
"v_mfma_f32_16x16x32_bf16 v[184:187], a[72:75], a[196:199], v[184:187]\n\t"
"ds_write_b64 v250, a[120:121] offset:0\n\t"
"v_mfma_f32_16x16x32_bf16 v[188:191], a[64:67], a[192:195], v[188:191]\n\t"
"ds_read_b128 a[192:195], v244 offset:27648\n\t"
"v_mfma_f32_16x16x32_bf16 v[188:191], a[68:71], a[196:199], v[188:191]\n\t"
"v_mfma_f32_16x16x32_bf16 v[188:191], a[72:75], a[200:203], v[188:191]\n\t"
"ds_write_b128 v249, v[216:219] offset:6144\n\t"
"v_mfma_f32_16x16x32_bf16 v[192:195], a[64:67], a[196:199], v[192:195]\n\t"
"s_waitcnt vmcnt(32) lgkmcnt(7)\n\t"
"v_mfma_f32_16x16x32_bf16 v[192:195], a[68:71], a[200:203], v[192:195]\n\t"
"ds_read_b128 a[196:199], v244 offset:28160\n\t"
"v_mfma_f32_16x16x32_bf16 v[192:195], a[72:75], a[204:207], v[192:195]\n\t"
"ds_read_b128 a[200:203], v244 offset:28672\n\t"
"Lst15_%=:\n\t"
"v_mfma_f32_16x16x32_bf16 v[0:3], a[76:79], a[168:171], v[0:3]\n\t"
"s_waitcnt lgkmcnt(8)\n\t"
"ds_read_b128 a[204:207], v244 offset:29184\n\t"
"v_mfma_f32_16x16x32_bf16 v[0:3], a[84:87], a[172:175], v[0:3]\n\t"
"v_mfma_f32_16x16x32_bf16 v[0:3], a[88:91], a[176:179], v[0:3]\n\t"
"ds_write_b64 v250, a[122:123] offset:6144\n\t"
"v_mfma_f32_16x16x32_bf16 v[4:7], a[76:79], a[172:175], v[4:7]\n\t"
"s_waitcnt lgkmcnt(9)\n\t"
"v_mfma_f32_16x16x32_bf16 v[4:7], a[84:87], a[176:179], v[4:7]\n\t"
"v_mfma_f32_16x16x32_bf16 v[4:7], a[88:91], a[180:183], v[4:7]\n\t"
"v_mfma_f32_16x16x32_bf16 v[8:11], a[76:79], a[176:179], v[8:11]\n\t"
"s_waitcnt lgkmcnt(7)\n\t"
"v_mfma_f32_16x16x32_bf16 v[8:11], a[84:87], a[180:183], v[8:11]\n\t"
"v_mfma_f32_16x16x32_bf16 v[8:11], a[88:91], a[184:187], v[8:11]\n\t"
"v_mfma_f32_16x16x32_bf16 v[12:15], a[76:79], a[180:183], v[12:15]\n\t"
"s_waitcnt lgkmcnt(5)\n\t"
"v_mfma_f32_16x16x32_bf16 v[12:15], a[84:87], a[184:187], v[12:15]\n\t"
"v_mfma_f32_16x16x32_bf16 v[12:15], a[88:91], a[188:191], v[12:15]\n\t"
"v_mfma_f32_16x16x32_bf16 v[16:19], a[76:79], a[184:187], v[16:19]\n\t"
"s_waitcnt lgkmcnt(3)\n\t"
"v_mfma_f32_16x16x32_bf16 v[16:19], a[84:87], a[188:191], v[16:19]\n\t"
"v_mfma_f32_16x16x32_bf16 v[16:19], a[88:91], a[192:195], v[16:19]\n\t"
"ds_read_b128 a[64:67], v252 offset:12288\n\t"
"v_mfma_f32_16x16x32_bf16 v[20:23], a[76:79], a[188:191], v[20:23]\n\t"
"s_waitcnt lgkmcnt(3)\n\t"
"v_mfma_f32_16x16x32_bf16 v[20:23], a[84:87], a[192:195], v[20:23]\n\t"
"ds_read_b128 a[72:75], v252 offset:13312\n\t"
"v_mfma_f32_16x16x32_bf16 v[20:23], a[88:91], a[196:199], v[20:23]\n\t"
"v_mfma_f32_16x16x32_bf16 v[24:27], a[76:79], a[192:195], v[24:27]\n\t"
"s_waitcnt lgkmcnt(3)\n\t"
"ds_read_b128 a[80:83], v252 offset:14336\n\t"
"v_mfma_f32_16x16x32_bf16 v[24:27], a[84:87], a[196:199], v[24:27]\n\t"
"v_mfma_f32_16x16x32_bf16 v[24:27], a[88:91], a[200:203], v[24:27]\n\t"
"v_mfma_f32_16x16x32_bf16 v[28:31], a[76:79], a[196:199], v[28:31]\n\t"
"s_waitcnt vmcnt(29)\n\t"
"v_mfma_f32_16x16x32_bf16 v[28:31], a[84:87], a[200:203], v[28:31]\n\t"
"v_mfma_f32_16x16x32_bf16 v[28:31], a[88:91], a[204:207], v[28:31]\n\t"
"v_mfma_f32_16x16x32_bf16 v[32:35], a[92:95], a[168:171], v[32:35]\n\t"
"v_mfma_f32_16x16x32_bf16 v[32:35], a[96:99], a[172:175], v[32:35]\n\t"
"v_mfma_f32_16x16x32_bf16 v[32:35], a[100:103], a[176:179], v[32:35]\n\t"
"v_mfma_f32_16x16x32_bf16 v[36:39], a[92:95], a[172:175], v[36:39]\n\t"
"v_mfma_f32_16x16x32_bf16 v[36:39], a[96:99], a[176:179], v[36:39]\n\t"
"v_mfma_f32_16x16x32_bf16 v[36:39], a[100:103], a[180:183], v[36:39]\n\t"
"v_mfma_f32_16x16x32_bf16 v[40:43], a[92:95], a[176:179], v[40:43]\n\t"
"v_mfma_f32_16x16x32_bf16 v[40:43], a[96:99], a[180:183], v[40:43]\n\t"
"v_mfma_f32_16x16x32_bf16 v[40:43], a[100:103], a[184:187], v[40:43]\n\t"
"v_mfma_f32_16x16x32_bf16 v[44:47], a[92:95], a[180:183], v[44:47]\n\t"
"v_mfma_f32_16x16x32_bf16 v[44:47], a[96:99], a[184:187], v[44:47]\n\t"
"v_mfma_f32_16x16x32_bf16 v[44:47], a[100:103], a[188:191], v[44:47]\n\t"
"s_waitcnt lgkmcnt(0)\n\t"
"s_barrier\n\t"
"v_mfma_f32_16x16x32_bf16 v[48:51], a[92:95], a[184:187], v[48:51]\n\t"
"v_mfma_f32_16x16x32_bf16 v[48:51], a[96:99], a[188:191], v[48:51]\n\t"
"v_mfma_f32_16x16x32_bf16 v[48:51], a[100:103], a[192:195], v[48:51]\n\t"
"ds_write_b128 v249, a[0:3] offset:12288\n\t"
"v_mfma_f32_16x16x32_bf16 v[52:55], a[92:95], a[188:191], v[52:55]\n\t"
"v_mfma_f32_16x16x32_bf16 v[52:55], a[96:99], a[192:195], v[52:55]\n\t"
"v_mfma_f32_16x16x32_bf16 v[52:55], a[100:103], a[196:199], v[52:55]\n\t"
"ds_write_b64 v250, a[128:129] offset:12288\n\t"
"v_mfma_f32_16x16x32_bf16 v[56:59], a[92:95], a[192:195], v[56:59]\n\t"
"v_mfma_f32_16x16x32_bf16 v[56:59], a[96:99], a[196:199], v[56:59]\n\t"
"v_mfma_f32_16x16x32_bf16 v[56:59], a[100:103], a[200:203], v[56:59]\n\t"
"ds_write_b128 v249, a[4:7] offset:18432\n\t"
"v_mfma_f32_16x16x32_bf16 v[60:63], a[92:95], a[196:199], v[60:63]\n\t"
"s_waitcnt vmcnt(26)\n\t"
"v_mfma_f32_16x16x32_bf16 v[60:63], a[96:99], a[200:203], v[60:63]\n\t"
"v_mfma_f32_16x16x32_bf16 v[60:63], a[100:103], a[204:207], v[60:63]\n\t"
"ds_write_b64 v250, a[130:131] offset:18432\n\t"
"v_mfma_f32_16x16x32_bf16 v[64:67], a[208:211], a[168:171], v[64:67]\n\t"
"ds_read_b128 a[168:171], v244 offset:33792\n\t"
"v_mfma_f32_16x16x32_bf16 v[64:67], a[212:215], a[172:175], v[64:67]\n\t"
"v_mfma_f32_16x16x32_bf16 v[64:67], a[116:119], a[176:179], v[64:67]\n\t"
"buffer_load_dwordx4 a[112:115], v252, s[8:11], s20 offen offset:1024 sc1\n\t"
"v_mfma_f32_16x16x32_bf16 v[68:71], a[208:211], a[172:175], v[68:71]\n\t"
"ds_read_b128 a[172:175], v244 offset:34304\n\t"
"v_mfma_f32_16x16x32_bf16 v[68:71], a[212:215], a[176:179], v[68:71]\n\t"
"v_mfma_f32_16x16x32_bf16 v[68:71], a[116:119], a[180:183], v[68:71]\n\t"
"v_mfma_f32_16x16x32_bf16 v[72:75], a[208:211], a[176:179], v[72:75]\n\t"
"ds_read_b128 a[176:179], v244 offset:34816\n\t"
"v_mfma_f32_16x16x32_bf16 v[72:75], a[212:215], a[180:183], v[72:75]\n\t"
"v_mfma_f32_16x16x32_bf16 v[72:75], a[116:119], a[184:187], v[72:75]\n\t"
"ds_read_b128 a[88:91], v252 offset:0\n\t"
"v_mfma_f32_16x16x32_bf16 v[76:79], a[208:211], a[180:183], v[76:79]\n\t"
"ds_read_b128 a[180:183], v244 offset:35328\n\t"
"v_mfma_f32_16x16x32_bf16 v[76:79], a[212:215], a[184:187], v[76:79]\n\t"
"Lpg12_%=:\n\t"
"s_cbranch_vccnz Lvr0_%=\n\t"
"ds_read_b128 a[96:99], v252 offset:1024\n\t"
"v_mfma_f32_16x16x32_bf16 v[76:79], a[116:119], a[188:191], v[76:79]\n\t"
"v_mfma_f32_16x16x32_bf16 v[80:83], a[208:211], a[184:187], v[80:83]\n\t"
"ds_read_b128 a[184:187], v244 offset:35840\n\t"
"v_mfma_f32_16x16x32_bf16 v[80:83], a[212:215], a[188:191], v[80:83]\n\t"
"ds_read_b128 a[108:111], v252 offset:2048\n\t"
"v_mfma_f32_16x16x32_bf16 v[80:83], a[116:119], a[192:195], v[80:83]\n\t"
"v_mfma_f32_16x16x32_bf16 v[84:87], a[208:211], a[188:191], v[84:87]\n\t"
"ds_read_b128 a[188:191], v244 offset:36352\n\t"
"v_mfma_f32_16x16x32_bf16 v[84:87], a[212:215], a[192:195], v[84:87]\n\t"
"v_mfma_f32_16x16x32_bf16 v[84:87], a[116:119], a[196:199], v[84:87]\n\t"
"v_mfma_f32_16x16x32_bf16 v[88:91], a[208:211], a[192:195], v[88:91]\n\t"
"ds_read_b128 a[192:195], v244 offset:36864\n\t"
"v_mfma_f32_16x16x32_bf16 v[88:91], a[212:215], a[196:199], v[88:91]\n\t"
"v_mfma_f32_16x16x32_bf16 v[88:91], a[116:119], a[200:203], v[88:91]\n\t"
"v_mfma_f32_16x16x32_bf16 v[92:95], a[208:211], a[196:199], v[92:95]\n\t"
"s_waitcnt vmcnt(24) lgkmcnt(7)\n\t"
"ds_read_b128 a[196:199], v244 offset:37376\n\t"
"v_mfma_f32_16x16x32_bf16 v[92:95], a[212:215], a[200:203], v[92:95]\n\t"
"ds_read_b128 a[200:203], v244 offset:37888\n\t"
"v_mfma_f32_16x16x32_bf16 v[92:95], a[116:119], a[204:207], v[92:95]\n\t"
"ds_read_b128 a[204:207], v244 offset:38400\n\t"
"v_mfma_f32_16x16x32_bf16 v[100:103], a[216:219], a[168:171], v[100:103]\n\t"
"s_waitcnt lgkmcnt(8)\n\t"
"v_mfma_f32_16x16x32_bf16 v[100:103], a[124:127], a[172:175], v[100:103]\n\t"
"v_mfma_f32_16x16x32_bf16 v[100:103], a[132:135], a[176:179], v[100:103]\n\t"
"v_mfma_f32_16x16x32_bf16 v[104:107], a[216:219], a[172:175], v[104:107]\n\t"
"s_waitcnt lgkmcnt(6)\n\t"
"v_mfma_f32_16x16x32_bf16 v[104:107], a[124:127], a[176:179], v[104:107]\n\t"
"v_mfma_f32_16x16x32_bf16 v[104:107], a[132:135], a[180:183], v[104:107]\n\t"
"s_waitcnt lgkmcnt(0)\n\t"
"s_barrier\n\t"
"v_mfma_f32_16x16x32_bf16 v[108:111], a[216:219], a[176:179], v[108:111]\n\t"
"v_mfma_f32_16x16x32_bf16 v[108:111], a[124:127], a[180:183], v[108:111]\n\t"
"v_mfma_f32_16x16x32_bf16 v[108:111], a[132:135], a[184:187], v[108:111]\n\t"
"ds_write_b128 v249, a[8:11] offset:0\n\t"
"v_mfma_f32_16x16x32_bf16 v[112:115], a[216:219], a[180:183], v[112:115]\n\t"
"v_mfma_f32_16x16x32_bf16 v[112:115], a[124:127], a[184:187], v[112:115]\n\t"
"buffer_load_dwordx4 a[44:47], v252, s[8:11], s20 offen offset:2048 sc1\n\t"
"v_mfma_f32_16x16x32_bf16 v[112:115], a[132:135], a[188:191], v[112:115]\n\t"
"s_mov_b32 s20, 1069056\n\t"
"ds_write_b64 v250, a[136:137] offset:0\n\t"
"v_mfma_f32_16x16x32_bf16 v[116:119], a[216:219], a[184:187], v[116:119]\n\t"
"v_mfma_f32_16x16x32_bf16 v[116:119], a[124:127], a[188:191], v[116:119]\n\t"
"v_mfma_f32_16x16x32_bf16 v[116:119], a[132:135], a[192:195], v[116:119]\n\t"
"buffer_load_dwordx4 a[60:63], v252, s[8:11], s20 offen offset:0 sc1\n\t"
"v_mfma_f32_16x16x32_bf16 v[120:123], a[216:219], a[188:191], v[120:123]\n\t"
"v_mfma_f32_16x16x32_bf16 v[120:123], a[124:127], a[192:195], v[120:123]\n\t"
"v_mfma_f32_16x16x32_bf16 v[120:123], a[132:135], a[196:199], v[120:123]\n\t"
"ds_write_b128 v249, a[12:15] offset:6144\n\t"
"v_mfma_f32_16x16x32_bf16 v[124:127], a[216:219], a[192:195], v[124:127]\n\t"
"buffer_load_dwordx4 a[68:71], v252, s[8:11], s20 offen offset:1024 sc1\n\t"
"v_mfma_f32_16x16x32_bf16 v[124:127], a[124:127], a[196:199], v[124:127]\n\t"
"v_mfma_f32_16x16x32_bf16 v[124:127], a[132:135], a[200:203], v[124:127]\n\t"
"ds_write_b64 v250, a[138:139] offset:6144\n\t"
"v_mfma_f32_16x16x32_bf16 v[128:131], a[216:219], a[196:199], v[128:131]\n\t"
"s_waitcnt vmcnt(24)\n\t"
"v_mfma_f32_16x16x32_bf16 v[128:131], a[124:127], a[200:203], v[128:131]\n\t"
"buffer_load_dwordx4 a[76:79], v252, s[8:11], s20 offen offset:2048 sc1\n\t"
"v_mfma_f32_16x16x32_bf16 v[128:131], a[132:135], a[204:207], v[128:131]\n\t"
"s_mov_b32 s20, 1081344\n\t"
"v_mfma_f32_16x16x32_bf16 v[132:135], a[220:223], a[168:171], v[132:135]\n\t"
"v_mfma_f32_16x16x32_bf16 v[132:135], v[240:243], a[172:175], v[132:135]\n\t"
"v_mfma_f32_16x16x32_bf16 v[132:135], a[140:143], a[176:179], v[132:135]\n\t"
"buffer_load_dwordx4 a[132:135], v252, s[8:11], s20 offen offset:0 sc1\n\t"
"v_mfma_f32_16x16x32_bf16 v[136:139], a[220:223], a[172:175], v[136:139]\n\t"
"v_mfma_f32_16x16x32_bf16 v[136:139], v[240:243], a[176:179], v[136:139]\n\t"
"v_mfma_f32_16x16x32_bf16 v[136:139], a[140:143], a[180:183], v[136:139]\n\t"
"v_mfma_f32_16x16x32_bf16 v[140:143], a[220:223], a[176:179], v[140:143]\n\t"
"buffer_load_dwordx4 a[92:95], v252, s[8:11], s20 offen offset:1024 sc1\n\t"
"v_mfma_f32_16x16x32_bf16 v[140:143], v[240:243], a[180:183], v[140:143]\n\t"
"v_mfma_f32_16x16x32_bf16 v[140:143], a[140:143], a[184:187], v[140:143]\n\t"
"v_mfma_f32_16x16x32_bf16 v[144:147], a[220:223], a[180:183], v[144:147]\n\t"
"v_mfma_f32_16x16x32_bf16 v[144:147], v[240:243], a[184:187], v[144:147]\n\t"
"buffer_load_dwordx4 a[100:103], v252, s[8:11], s20 offen offset:2048 sc1\n\t"
"v_mfma_f32_16x16x32_bf16 v[144:147], a[140:143], a[188:191], v[144:147]\n\t"
"s_mov_b32 s20, 1093632\n\t"
"v_mfma_f32_16x16x32_bf16 v[148:151], a[220:223], a[184:187], v[148:151]\n\t"
"v_mfma_f32_16x16x32_bf16 v[148:151], v[240:243], a[188:191], v[148:151]\n\t"
"v_mfma_f32_16x16x32_bf16 v[148:151], a[140:143], a[192:195], v[148:151]\n\t"
"buffer_load_dwordx4 v[200:203], v252, s[8:11], s20 offen offset:0 sc1\n\t"
"v_mfma_f32_16x16x32_bf16 v[152:155], a[220:223], a[188:191], v[152:155]\n\t"
"ds_read_b128 a[116:119], v252 offset:12288\n\t"
"v_mfma_f32_16x16x32_bf16 v[152:155], v[240:243], a[192:195], v[152:155]\n\t"
"ds_read_b128 a[124:127], v252 offset:13312\n\t"
"v_mfma_f32_16x16x32_bf16 v[152:155], a[140:143], a[196:199], v[152:155]\n\t"
"v_mfma_f32_16x16x32_bf16 v[156:159], a[220:223], a[192:195], v[156:159]\n\t"
"buffer_load_dwordx4 a[120:123], v252, s[8:11], s20 offen offset:1024 sc1\n\t"
"v_mfma_f32_16x16x32_bf16 v[156:159], v[240:243], a[196:199], v[156:159]\n\t"
"ds_read_b128 a[156:159], v252 offset:14336\n\t"
"v_mfma_f32_16x16x32_bf16 v[156:159], a[140:143], a[200:203], v[156:159]\n\t"
"v_mfma_f32_16x16x32_bf16 v[160:163], a[220:223], a[196:199], v[160:163]\n\t"
"s_waitcnt vmcnt(27)\n\t"
"v_mfma_f32_16x16x32_bf16 v[160:163], v[240:243], a[200:203], v[160:163]\n\t"
"buffer_load_dwordx4 a[128:131], v252, s[8:11], s20 offen offset:2048 sc1\n\t"
"v_mfma_f32_16x16x32_bf16 v[160:163], a[140:143], a[204:207], v[160:163]\n\t"
"s_mov_b32 s20, 1179648\n\t"
"v_mfma_f32_16x16x32_bf16 v[164:167], a[148:151], a[168:171], v[164:167]\n\t"
"ds_read_b128 a[168:171], v255 offset:0\n\t"
"v_mfma_f32_16x16x32_bf16 v[164:167], a[224:227], a[172:175], v[164:167]\n\t"
"v_mfma_f32_16x16x32_bf16 v[164:167], a[228:231], a[176:179], v[164:167]\n\t"
"buffer_load_dwordx4 a[140:143], v252, s[8:11], s20 offen offset:0 sc1\n\t"
"v_mfma_f32_16x16x32_bf16 v[168:171], a[148:151], a[172:175], v[168:171]\n\t"
"ds_read_b128 a[172:175], v255 offset:3072\n\t"
"v_mfma_f32_16x16x32_bf16 v[168:171], a[224:227], a[176:179], v[168:171]\n\t"
"v_mfma_f32_16x16x32_bf16 v[168:171], a[228:231], a[180:183], v[168:171]\n\t"
"v_mfma_f32_16x16x32_bf16 v[172:175], a[148:151], a[176:179], v[172:175]\n\t"
"buffer_load_dwordx4 v[208:211], v252, s[8:11], s20 offen offset:1024 sc1\n\t"
"v_mfma_f32_16x16x32_bf16 v[172:175], a[224:227], a[180:183], v[172:175]\n\t"
"ds_read_b128 a[176:179], v255 offset:6144\n\t"
"v_mfma_f32_16x16x32_bf16 v[172:175], a[228:231], a[184:187], v[172:175]\n\t"
"v_mfma_f32_16x16x32_bf16 v[176:179], a[148:151], a[180:183], v[176:179]\n\t"
"buffer_load_dwordx4 v[216:219], v252, s[8:11], s20 offen offset:2048 sc1\n\t"
"v_mfma_f32_16x16x32_bf16 v[176:179], a[224:227], a[184:187], v[176:179]\n\t"
"s_mov_b32 s20, 1191936\n\t"
"ds_read_b128 a[180:183], v255 offset:9216\n\t"
"v_mfma_f32_16x16x32_bf16 v[176:179], a[228:231], a[188:191], v[176:179]\n\t"
"s_waitcnt lgkmcnt(4)\n\t"
"s_barrier\n\t"
"v_mfma_f32_16x16x32_bf16 v[180:183], a[148:151], a[184:187], v[180:183]\n\t"
"ds_read_b128 a[184:187], v255 offset:12288\n\t"
"v_mfma_f32_16x16x32_bf16 v[180:183], a[224:227], a[188:191], v[180:183]\n\t"
"buffer_load_dwordx4 a[4:7], v252, s[8:11], s20 offen offset:0 sc1\n\t"
"v_mfma_f32_16x16x32_bf16 v[180:183], a[228:231], a[192:195], v[180:183]\n\t"
"ds_write_b128 v249, a[16:19] offset:12288\n\t"
"v_mfma_f32_16x16x32_bf16 v[184:187], a[148:151], a[188:191], v[184:187]\n\t"
"buffer_load_dwordx4 a[12:15], v252, s[8:11], s20 offen offset:1024 sc1\n\t"
"v_mfma_f32_16x16x32_bf16 v[184:187], a[224:227], a[192:195], v[184:187]\n\t"
"ds_read_b128 a[188:191], v255 offset:15360\n\t"
"v_mfma_f32_16x16x32_bf16 v[184:187], a[228:231], a[196:199], v[184:187]\n\t"
"ds_write_b64 v250, a[144:145] offset:12288\n\t"
"v_mfma_f32_16x16x32_bf16 v[188:191], a[148:151], a[192:195], v[188:191]\n\t"
"ds_read_b128 a[192:195], v255 offset:18432\n\t"
"v_mfma_f32_16x16x32_bf16 v[188:191], a[224:227], a[196:199], v[188:191]\n\t"
"buffer_load_dwordx4 a[208:211], v252, s[8:11], s20 offen offset:2048 sc1\n\t"
"v_mfma_f32_16x16x32_bf16 v[188:191], a[228:231], a[200:203], v[188:191]\n\t"
"s_mov_b32 s20, 1204224\n\t"
"ds_write_b128 v249, a[20:23] offset:18432\n\t"
"v_mfma_f32_16x16x32_bf16 v[192:195], a[148:151], a[196:199], v[192:195]\n\t"
"s_waitcnt vmcnt(31) lgkmcnt(7)\n\t"
"v_mfma_f32_16x16x32_bf16 v[192:195], a[224:227], a[200:203], v[192:195]\n\t"
"ds_read_b128 a[196:199], v255 offset:21504\n\t"
"v_mfma_f32_16x16x32_bf16 v[192:195], a[228:231], a[204:207], v[192:195]\n\t"
"buffer_load_dwordx4 a[212:215], v252, s[8:11], s20 offen offset:0 sc1\n\t"
"Lst16_%=:\n\t"
"v_mfma_f32_16x16x32_bf16 v[0:3], a[244:247], a[168:171], v[0:3]\n\t"
"s_waitcnt lgkmcnt(7)\n\t"
"ds_read_b128 a[200:203], v255 offset:24576\n\t"
"v_mfma_f32_16x16x32_bf16 v[0:3], a[164:167], a[172:175], v[0:3]\n\t"
"ds_read_b128 a[204:207], v255 offset:27648\n\t"
"v_mfma_f32_16x16x32_bf16 v[0:3], a[248:251], a[176:179], v[0:3]\n\t"
"ds_write_b64 v250, a[146:147] offset:18432\n\t"
"v_mfma_f32_16x16x32_bf16 v[4:7], a[244:247], a[172:175], v[4:7]\n\t"
"buffer_load_dwordx4 a[216:219], v252, s[8:11], s20 offen offset:1024 sc1\n\t"
"v_mfma_f32_16x16x32_bf16 v[4:7], a[164:167], a[176:179], v[4:7]\n\t"
"s_waitcnt lgkmcnt(9)\n\t"
"v_mfma_f32_16x16x32_bf16 v[4:7], a[248:251], a[180:183], v[4:7]\n\t"
"v_mfma_f32_16x16x32_bf16 v[8:11], a[244:247], a[176:179], v[8:11]\n\t"
"s_waitcnt lgkmcnt(7)\n\t"
"v_mfma_f32_16x16x32_bf16 v[8:11], a[164:167], a[180:183], v[8:11]\n\t"
"buffer_load_dwordx4 a[220:223], v252, s[8:11], s20 offen offset:2048 sc1\n\t"
"v_mfma_f32_16x16x32_bf16 v[8:11], a[248:251], a[184:187], v[8:11]\n\t"
"s_mov_b32 s20, 1363968\n\t"
"v_mfma_f32_16x16x32_bf16 v[12:15], a[244:247], a[180:183], v[12:15]\n\t"
"s_waitcnt lgkmcnt(5)\n\t"
"v_mfma_f32_16x16x32_bf16 v[12:15], a[164:167], a[184:187], v[12:15]\n\t"
"v_mfma_f32_16x16x32_bf16 v[12:15], a[248:251], a[188:191], v[12:15]\n\t"
"buffer_load_dwordx4 a[224:227], v252, s[8:11], s20 offen offset:0 sc1\n\t"
"v_mfma_f32_16x16x32_bf16 v[16:19], a[244:247], a[184:187], v[16:19]\n\t"
"s_waitcnt lgkmcnt(3)\n\t"
"v_mfma_f32_16x16x32_bf16 v[16:19], a[164:167], a[188:191], v[16:19]\n\t"
"v_mfma_f32_16x16x32_bf16 v[16:19], a[248:251], a[192:195], v[16:19]\n\t"
"ds_read_b128 a[52:55], v252 offset:0\n\t"
"v_mfma_f32_16x16x32_bf16 v[20:23], a[244:247], a[188:191], v[20:23]\n\t"
"buffer_load_dwordx4 a[228:231], v252, s[8:11], s20 offen offset:1024 sc1\n\t"
"v_mfma_f32_16x16x32_bf16 v[20:23], a[164:167], a[192:195], v[20:23]\n\t"
"s_waitcnt lgkmcnt(3)\n\t"
"ds_read_b128 a[84:87], v252 offset:1024\n\t"
"v_mfma_f32_16x16x32_bf16 v[20:23], a[248:251], a[196:199], v[20:23]\n\t"
"v_mfma_f32_16x16x32_bf16 v[24:27], a[244:247], a[192:195], v[24:27]\n\t"
"s_waitcnt lgkmcnt(3)\n\t"
"ds_read_b128 v[196:199], v252 offset:2048\n\t"
"v_mfma_f32_16x16x32_bf16 v[24:27], a[164:167], a[196:199], v[24:27]\n\t"
"buffer_load_dwordx4 a[236:239], v252, s[8:11], s20 offen offset:2048 sc1\n\t"
"v_mfma_f32_16x16x32_bf16 v[24:27], a[248:251], a[200:203], v[24:27]\n\t"
"s_mov_b32 s20, 1376256\n\t"
"v_mfma_f32_16x16x32_bf16 v[28:31], a[244:247], a[196:199], v[28:31]\n\t"
"s_waitcnt vmcnt(33)\n\t"
"v_mfma_f32_16x16x32_bf16 v[28:31], a[164:167], a[200:203], v[28:31]\n\t"
"v_mfma_f32_16x16x32_bf16 v[28:31], a[248:251], a[204:207], v[28:31]\n\t"
"buffer_load_dwordx4 a[240:243], v252, s[8:11], s20 offen offset:0 sc1\n\t"
"v_mfma_f32_16x16x32_bf16 v[32:35], v[236:239], a[168:171], v[32:35]\n\t"
"v_mfma_f32_16x16x32_bf16 v[32:35], v[96:99], a[172:175], v[32:35]\n\t"
"v_mfma_f32_16x16x32_bf16 v[32:35], a[232:235], a[176:179], v[32:35]\n\t"
"buffer_load_dwordx4 a[244:247], v252, s[8:11], s20 offen offset:1024 sc1\n\t"
"v_mfma_f32_16x16x32_bf16 v[36:39], v[236:239], a[172:175], v[36:39]\n\t"
"v_mfma_f32_16x16x32_bf16 v[36:39], v[96:99], a[176:179], v[36:39]\n\t"
"v_mfma_f32_16x16x32_bf16 v[36:39], a[232:235], a[180:183], v[36:39]\n\t"
"v_mfma_f32_16x16x32_bf16 v[40:43], v[236:239], a[176:179], v[40:43]\n\t"
"buffer_load_dwordx4 a[248:251], v252, s[8:11], s20 offen offset:2048 sc1\n\t"
"v_mfma_f32_16x16x32_bf16 v[40:43], v[96:99], a[180:183], v[40:43]\n\t"
"s_mov_b32 s20, 1388544\n\t"
"v_mfma_f32_16x16x32_bf16 v[40:43], a[232:235], a[184:187], v[40:43]\n\t"
"v_mfma_f32_16x16x32_bf16 v[44:47], v[236:239], a[180:183], v[44:47]\n\t"
"v_mfma_f32_16x16x32_bf16 v[44:47], v[96:99], a[184:187], v[44:47]\n\t"
"v_mfma_f32_16x16x32_bf16 v[44:47], a[232:235], a[188:191], v[44:47]\n\t"
"s_waitcnt lgkmcnt(0)\n\t"
"s_barrier\n\t"
"v_mfma_f32_16x16x32_bf16 v[48:51], v[236:239], a[184:187], v[48:51]\n\t"
"v_mfma_f32_16x16x32_bf16 v[48:51], v[96:99], a[188:191], v[48:51]\n\t"
"buffer_load_dwordx4 a[252:255], v252, s[8:11], s20 offen offset:0 sc1\n\t"
"v_mfma_f32_16x16x32_bf16 v[48:51], a[232:235], a[192:195], v[48:51]\n\t"
"buffer_load_dwordx4 a[144:147], v252, s[8:11], s20 offen offset:1024 sc1\n\t"
"v_mfma_f32_16x16x32_bf16 v[52:55], v[236:239], a[188:191], v[52:55]\n\t"
"v_mfma_f32_16x16x32_bf16 v[52:55], v[96:99], a[192:195], v[52:55]\n\t"
"buffer_load_dwordx4 a[20:23], v252, s[8:11], s20 offen offset:2048 sc1\n\t"
"v_mfma_f32_16x16x32_bf16 v[52:55], a[232:235], a[196:199], v[52:55]\n\t"
"s_mov_b32 s20, 221184\n\t"
"ds_write_b128 v249, a[24:27] offset:0\n\t"
"v_mfma_f32_16x16x32_bf16 v[56:59], v[236:239], a[192:195], v[56:59]\n\t"
"v_mfma_f32_16x16x32_bf16 v[56:59], v[96:99], a[196:199], v[56:59]\n\t"
"v_mfma_f32_16x16x32_bf16 v[56:59], a[232:235], a[200:203], v[56:59]\n\t"
"ds_write_b64 v250, a[152:153] offset:0\n\t"
"v_mfma_f32_16x16x32_bf16 v[60:63], v[236:239], a[196:199], v[60:63]\n\t"
"s_waitcnt vmcnt(27)\n\t"
"v_mfma_f32_16x16x32_bf16 v[60:63], v[96:99], a[200:203], v[60:63]\n\t"
"buffer_load_dwordx4 v[96:99], v252, s[8:11], s20 offen offset:0 sc1\n\t"
"v_mfma_f32_16x16x32_bf16 v[60:63], a[232:235], a[204:207], v[60:63]\n\t"
"ds_write_b128 v249, a[28:31] offset:6144\n\t"
"v_mfma_f32_16x16x32_bf16 v[64:67], a[104:107], a[168:171], v[64:67]\n\t"
"v_mfma_f32_16x16x32_bf16 v[64:67], a[112:115], a[172:175], v[64:67]\n\t"
"buffer_load_dwordx4 a[232:235], v252, s[8:11], s20 offen offset:1024 sc1\n\t"
"v_mfma_f32_16x16x32_bf16 v[64:67], a[44:47], a[176:179], v[64:67]\n\t"
"ds_read_b128 a[168:171], v255 offset:1536\n\t"
"v_mfma_f32_16x16x32_bf16 v[68:71], a[104:107], a[172:175], v[68:71]\n\t"
"ds_read_b128 a[172:175], v255 offset:4608\n\t"
"v_mfma_f32_16x16x32_bf16 v[68:71], a[112:115], a[176:179], v[68:71]\n\t"
"v_mfma_f32_16x16x32_bf16 v[68:71], a[44:47], a[180:183], v[68:71]\n\t"
"ds_write_b64 v250, a[154:155] offset:6144\n\t"
"v_mfma_f32_16x16x32_bf16 v[72:75], a[104:107], a[176:179], v[72:75]\n\t"
"ds_read_b128 a[176:179], v255 offset:7680\n\t"
"v_mfma_f32_16x16x32_bf16 v[72:75], a[112:115], a[180:183], v[72:75]\n\t"
"v_mfma_f32_16x16x32_bf16 v[72:75], a[44:47], a[184:187], v[72:75]\n\t"
"ds_read_b128 v[204:207], v252 offset:12288\n\t"
"v_mfma_f32_16x16x32_bf16 v[76:79], a[104:107], a[180:183], v[76:79]\n\t"
"ds_read_b128 a[180:183], v255 offset:10752\n\t"
"v_mfma_f32_16x16x32_bf16 v[76:79], a[112:115], a[184:187], v[76:79]\n\t"
"ds_read_b128 v[212:215], v252 offset:13312\n\t"
"v_mfma_f32_16x16x32_bf16 v[76:79], a[44:47], a[188:191], v[76:79]\n\t"
"v_mfma_f32_16x16x32_bf16 v[80:83], a[104:107], a[184:187], v[80:83]\n\t"
"ds_read_b128 a[184:187], v255 offset:13824\n\t"
"v_mfma_f32_16x16x32_bf16 v[80:83], a[112:115], a[188:191], v[80:83]\n\t"
"ds_read_b128 a[0:3], v252 offset:14336\n\t"
"v_mfma_f32_16x16x32_bf16 v[80:83], a[44:47], a[192:195], v[80:83]\n\t"
"v_mfma_f32_16x16x32_bf16 v[84:87], a[104:107], a[188:191], v[84:87]\n\t"
"ds_read_b128 a[188:191], v255 offset:16896\n\t"
"v_mfma_f32_16x16x32_bf16 v[84:87], a[112:115], a[192:195], v[84:87]\n\t"
"v_mfma_f32_16x16x32_bf16 v[84:87], a[44:47], a[196:199], v[84:87]\n\t"
"v_mfma_f32_16x16x32_bf16 v[88:91], a[104:107], a[192:195], v[88:91]\n\t"
"s_add_u32 s29, s22, 384\n\t"
"ds_read_b128 a[192:195], v255 offset:19968\n\t"
"v_mfma_f32_16x16x32_bf16 v[88:91], a[112:115], a[196:199], v[88:91]\n\t"
"v_mfma_f32_16x16x32_bf16 v[88:91], a[44:47], a[200:203], v[88:91]\n\t"
"v_mfma_f32_16x16x32_bf16 v[92:95], a[104:107], a[196:199], v[92:95]\n\t"
"s_waitcnt vmcnt(26) lgkmcnt(7)\n\t"
"ds_read_b128 a[196:199], v255 offset:23040\n\t"
"v_mfma_f32_16x16x32_bf16 v[92:95], a[112:115], a[200:203], v[92:95]\n\t"
"ds_read_b128 a[200:203], v255 offset:26112\n\t"
"v_mfma_f32_16x16x32_bf16 v[92:95], a[44:47], a[204:207], v[92:95]\n\t"
"ds_read_b128 a[204:207], v255 offset:29184\n\t"
"v_mfma_f32_16x16x32_bf16 v[100:103], a[60:63], a[168:171], v[100:103]\n\t"
"s_waitcnt lgkmcnt(8)\n\t"
"v_mfma_f32_16x16x32_bf16 v[100:103], a[68:71], a[172:175], v[100:103]\n\t"
"v_mfma_f32_16x16x32_bf16 v[100:103], a[76:79], a[176:179], v[100:103]\n\t"
"buffer_store_dwordx4 a[40:43], v248, s[16:19], s29 offen offset:0 nt\n\t"
"v_mfma_f32_16x16x32_bf16 v[104:107], a[60:63], a[172:175], v[104:107]\n\t"
"s_waitcnt lgkmcnt(6)\n\t"
"v_mfma_f32_16x16x32_bf16 v[104:107], a[68:71], a[176:179], v[104:107]\n\t"
"v_mfma_f32_16x16x32_bf16 v[104:107], a[76:79], a[180:183], v[104:107]\n\t"
"s_waitcnt lgkmcnt(0)\n\t"
"s_barrier\n\t"
"v_mfma_f32_16x16x32_bf16 v[108:111], a[60:63], a[176:179], v[108:111]\n\t"
"v_mfma_f32_16x16x32_bf16 v[108:111], a[68:71], a[180:183], v[108:111]\n\t"
"buffer_load_dwordx4 a[152:155], v252, s[8:11], s20 offen offset:2048 sc1\n\t"
"v_mfma_f32_16x16x32_bf16 v[108:111], a[76:79], a[184:187], v[108:111]\n\t"
"s_mov_b32 s20, 233472\n\t"
"ds_write_b128 v249, a[32:35] offset:12288\n\t"
"v_mfma_f32_16x16x32_bf16 v[112:115], a[60:63], a[180:183], v[112:115]\n\t"
"v_mfma_f32_16x16x32_bf16 v[112:115], a[68:71], a[184:187], v[112:115]\n\t"
"buffer_load_dwordx4 a[28:31], v252, s[8:11], s20 offen offset:0 sc1\n\t"
"v_mfma_f32_16x16x32_bf16 v[112:115], a[76:79], a[188:191], v[112:115]\n\t"
"ds_write_b64 v250, a[160:161] offset:12288\n\t"
"v_mfma_f32_16x16x32_bf16 v[116:119], a[60:63], a[184:187], v[116:119]\n\t"
"v_mfma_f32_16x16x32_bf16 v[116:119], a[68:71], a[188:191], v[116:119]\n\t"
"v_mfma_f32_16x16x32_bf16 v[116:119], a[76:79], a[192:195], v[116:119]\n\t"
"buffer_store_dwordx4 a[48:51], v248, s[16:19], s29 offen offset:128 nt\n\t"
"ds_write_b64 v250, a[162:163] offset:18432\n\t"
"v_mfma_f32_16x16x32_bf16 v[120:123], a[60:63], a[188:191], v[120:123]\n\t"
"v_mfma_f32_16x16x32_bf16 v[120:123], a[68:71], a[192:195], v[120:123]\n\t"
"v_mfma_f32_16x16x32_bf16 v[120:123], a[76:79], a[196:199], v[120:123]\n\t"
"ds_write_b128 v249, a[36:39] offset:18432\n\t"
"v_mfma_f32_16x16x32_bf16 v[124:127], a[60:63], a[192:195], v[124:127]\n\t"
"v_mfma_f32_16x16x32_bf16 v[124:127], a[68:71], a[196:199], v[124:127]\n\t"
"v_mfma_f32_16x16x32_bf16 v[124:127], a[76:79], a[200:203], v[124:127]\n\t"
"v_mfma_f32_16x16x32_bf16 v[128:131], a[60:63], a[196:199], v[128:131]\n\t"
"s_waitcnt vmcnt(27)\n\t"
"v_mfma_f32_16x16x32_bf16 v[128:131], a[68:71], a[200:203], v[128:131]\n\t"
"v_mfma_f32_16x16x32_bf16 v[128:131], a[76:79], a[204:207], v[128:131]\n\t"
"v_mfma_f32_16x16x32_bf16 v[132:135], a[132:135], a[168:171], v[132:135]\n\t"
"v_mfma_f32_16x16x32_bf16 v[132:135], a[92:95], a[172:175], v[132:135]\n\t"
"v_mfma_f32_16x16x32_bf16 v[132:135], a[100:103], a[176:179], v[132:135]\n\t"
"buffer_store_dwordx4 a[56:59], v248, s[16:19], s29 offen offset:256 nt\n\t"
"v_mfma_f32_16x16x32_bf16 v[136:139], a[132:135], a[172:175], v[136:139]\n\t"
"v_mfma_f32_16x16x32_bf16 v[136:139], a[92:95], a[176:179], v[136:139]\n\t"
"v_add_u32 v255, 55296, v255\n\t"
"v_mfma_f32_16x16x32_bf16 v[136:139], a[100:103], a[180:183], v[136:139]\n\t"
"v_mfma_f32_16x16x32_bf16 v[140:143], a[132:135], a[176:179], v[140:143]\n\t"
"v_mfma_f32_16x16x32_bf16 v[140:143], a[92:95], a[180:183], v[140:143]\n\t"
"v_mfma_f32_16x16x32_bf16 v[140:143], a[100:103], a[184:187], v[140:143]\n\t"
"s_add_u32 s29, s22, 196992\n\t"
"v_mfma_f32_16x16x32_bf16 v[144:147], a[132:135], a[180:183], v[144:147]\n\t"
"v_mfma_f32_16x16x32_bf16 v[144:147], a[92:95], a[184:187], v[144:147]\n\t"
"global_load_dwordx4 v[240:243], v252, s[48:49] offset:2048\n\t"
"v_mfma_f32_16x16x32_bf16 v[144:147], a[100:103], a[188:191], v[144:147]\n\t"
"v_mfma_f32_16x16x32_bf16 v[148:151], a[132:135], a[184:187], v[148:151]\n\t"
"v_mfma_f32_16x16x32_bf16 v[148:151], a[92:95], a[188:191], v[148:151]\n\t"
"v_mfma_f32_16x16x32_bf16 v[148:151], a[100:103], a[192:195], v[148:151]\n\t"
"buffer_store_dwordx4 a[64:67], v248, s[16:19], s29 offen offset:0 nt\n\t"
"ds_read_b128 a[8:11], v252 offset:0\n\t"
"v_mfma_f32_16x16x32_bf16 v[152:155], a[132:135], a[188:191], v[152:155]\n\t"
"v_mfma_f32_16x16x32_bf16 v[152:155], a[92:95], a[192:195], v[152:155]\n\t"
"global_load_dwordx4 a[136:139], v252, s[46:47] offset:1024\n\t"
"v_mfma_f32_16x16x32_bf16 v[152:155], a[100:103], a[196:199], v[152:155]\n\t"
"ds_read_b128 a[16:19], v252 offset:1024\n\t"
"v_mfma_f32_16x16x32_bf16 v[156:159], a[132:135], a[192:195], v[156:159]\n\t"
"ds_read_b128 a[24:27], v252 offset:2048\n\t"
"v_mfma_f32_16x16x32_bf16 v[156:159], a[92:95], a[196:199], v[156:159]\n\t"
"v_mfma_f32_16x16x32_bf16 v[156:159], a[100:103], a[200:203], v[156:159]\n\t"
"v_mfma_f32_16x16x32_bf16 v[160:163], a[132:135], a[196:199], v[160:163]\n\t"
"s_waitcnt vmcnt(28)\n\t"
"v_mfma_f32_16x16x32_bf16 v[160:163], a[92:95], a[200:203], v[160:163]\n\t"
"v_mfma_f32_16x16x32_bf16 v[160:163], a[100:103], a[204:207], v[160:163]\n\t"
"v_mfma_f32_16x16x32_bf16 v[164:167], v[200:203], a[168:171], v[164:167]\n\t"
"ds_read_b128 a[168:171], v255 offset:0\n\t"
"v_mfma_f32_16x16x32_bf16 v[164:167], a[120:123], a[172:175], v[164:167]\n\t"
"v_mfma_f32_16x16x32_bf16 v[164:167], a[128:131], a[176:179], v[164:167]\n\t"
"buffer_store_dwordx4 a[72:75], v248, s[16:19], s29 offen offset:128 nt\n\t"
"v_mfma_f32_16x16x32_bf16 v[168:171], v[200:203], a[172:175], v[168:171]\n\t"
"ds_read_b128 a[172:175], v255 offset:3072\n\t"
"v_mfma_f32_16x16x32_bf16 v[168:171], a[120:123], a[176:179], v[168:171]\n\t"
"v_mfma_f32_16x16x32_bf16 v[168:171], a[128:131], a[180:183], v[168:171]\n\t"
"v_mfma_f32_16x16x32_bf16 v[172:175], v[200:203], a[176:179], v[172:175]\n\t"
"ds_read_b128 a[176:179], v255 offset:6144\n\t"
"v_mfma_f32_16x16x32_bf16 v[172:175], a[120:123], a[180:183], v[172:175]\n\t"
"v_mfma_f32_16x16x32_bf16 v[172:175], a[128:131], a[184:187], v[172:175]\n\t"
"v_mfma_f32_16x16x32_bf16 v[176:179], v[200:203], a[180:183], v[176:179]\n\t"
"ds_read_b128 a[180:183], v255 offset:9216\n\t"
"v_mfma_f32_16x16x32_bf16 v[176:179], a[120:123], a[184:187], v[176:179]\n\t"
"v_mfma_f32_16x16x32_bf16 v[176:179], a[128:131], a[188:191], v[176:179]\n\t"
"s_waitcnt lgkmcnt(4)\n\t"
"s_barrier\n\t"
"v_mfma_f32_16x16x32_bf16 v[180:183], v[200:203], a[184:187], v[180:183]\n\t"
"ds_read_b128 a[184:187], v255 offset:12288\n\t"
"v_mfma_f32_16x16x32_bf16 v[180:183], a[120:123], a[188:191], v[180:183]\n\t"
"buffer_load_dwordx4 a[160:163], v252, s[8:11], s20 offen offset:1024 sc1\n\t"
"v_mfma_f32_16x16x32_bf16 v[180:183], a[128:131], a[192:195], v[180:183]\n\t"
"v_mfma_f32_16x16x32_bf16 v[184:187], v[200:203], a[188:191], v[184:187]\n\t"
"ds_read_b128 a[188:191], v255 offset:15360\n\t"
"v_mfma_f32_16x16x32_bf16 v[184:187], a[120:123], a[192:195], v[184:187]\n\t"
"buffer_load_dwordx4 a[36:39], v252, s[8:11], s20 offen offset:2048 sc1\n\t"
"v_mfma_f32_16x16x32_bf16 v[184:187], a[128:131], a[196:199], v[184:187]\n\t"
"buffer_store_dwordx4 a[80:83], v248, s[16:19], s29 offen offset:256 nt\n\t"
"s_waitcnt vmcnt(6)\n\t"
"v_add_u32 v240, 59904, v240\n\t"
"v_add_u32 v241, 59904, v241\n\t"
"s_mov_b32 s20, 245760\n\t"
"v_mfma_f32_16x16x32_bf16 v[188:191], v[200:203], a[192:195], v[188:191]\n\t"
"ds_read_b128 a[192:195], v255 offset:18432\n\t"
"v_mfma_f32_16x16x32_bf16 v[188:191], a[120:123], a[196:199], v[188:191]\n\t"
"v_mfma_f32_16x16x32_bf16 v[188:191], a[128:131], a[200:203], v[188:191]\n\t"
"ds_read_b64_tr_b16 v[236:237], v240 offset:20480\n\t"
"v_mfma_f32_16x16x32_bf16 v[192:195], v[200:203], a[196:199], v[192:195]\n\t"
"buffer_load_dwordx4 v[200:203], v252, s[8:11], s20 offen offset:0 sc1\n\t"
"v_mfma_f32_16x16x32_bf16 v[192:195], a[120:123], a[200:203], v[192:195]\n\t"
"s_waitcnt lgkmcnt(5)\n\t"
"ds_read_b128 a[196:199], v255 offset:21504\n\t"
"s_add_u32 s29, s22, 393600\n\t"
"v_mfma_f32_16x16x32_bf16 v[192:195], a[128:131], a[204:207], v[192:195]\n\t"
"ds_read_b64_tr_b16 v[238:239], v240 offset:22016\n\t"
"Lst17_%=:\n\t"
"v_mfma_f32_16x16x32_bf16 v[0:3], a[140:143], a[168:171], v[0:3]\n\t"
"buffer_load_dwordx4 a[120:123], v252, s[8:11], s20 offen offset:1024 sc1\n\t"
"v_mfma_f32_16x16x32_bf16 v[0:3], v[208:211], a[172:175], v[0:3]\n\t"
"s_waitcnt lgkmcnt(6)\n\t"
"ds_read_b64_tr_b16 v[224:225], v241 offset:20480\n\t"
"v_mfma_f32_16x16x32_bf16 v[0:3], v[216:219], a[176:179], v[0:3]\n\t"
"ds_read_b64_tr_b16 v[226:227], v241 offset:22016\n\t"
"v_mfma_f32_16x16x32_bf16 v[4:7], a[140:143], a[172:175], v[4:7]\n\t"
"s_waitcnt lgkmcnt(7)\n\t"
"ds_read_b128 a[200:203], v255 offset:24576\n\t"
"v_mfma_f32_16x16x32_bf16 v[4:7], v[208:211], a[176:179], v[4:7]\n\t"
"buffer_load_dwordx4 a[128:131], v252, s[8:11], s20 offen offset:2048 sc1\n\t"
"v_mfma_f32_16x16x32_bf16 v[4:7], v[216:219], a[180:183], v[4:7]\n\t"
"ds_read_b128 a[204:207], v255 offset:27648\n\t"
"v_mfma_f32_16x16x32_bf16 v[8:11], a[140:143], a[176:179], v[8:11]\n\t"
"s_waitcnt lgkmcnt(8)\n\t"
"v_cvt_pk_bf16_f32 v0, v0, v1\n\t"
"v_cvt_pk_bf16_f32 v2, v2, v3\n\t"
"v_mfma_f32_16x16x32_bf16 v[8:11], v[208:211], a[180:183], v[8:11]\n\t"
"v_accvgpr_write_b32 a40, v0\n\t"
"v_accvgpr_write_b32 a41, v2\n\t"
"v_mfma_f32_16x16x32_bf16 v[8:11], v[216:219], a[184:187], v[8:11]\n\t"
"v_mfma_f32_16x16x32_bf16 v[12:15], a[140:143], a[180:183], v[12:15]\n\t"
"s_waitcnt vmcnt(7) lgkmcnt(4)\n\t"
"v_cvt_pk_bf16_f32 v4, v4, v5\n\t"
"v_cvt_pk_bf16_f32 v6, v6, v7\n\t"
"v_mfma_f32_16x16x32_bf16 v[12:15], v[208:211], a[184:187], v[12:15]\n\t"
"s_waitcnt lgkmcnt(2)\n\t"
"v_accvgpr_write_b32 a48, v4\n\t"
"v_accvgpr_write_b32 a49, v6\n\t"
"v_mfma_f32_16x16x32_bf16 v[12:15], v[216:219], a[188:191], v[12:15]\n\t"
"buffer_store_dwordx4 a[88:91], v248, s[16:19], s29 offen offset:0 nt\n\t"
"v_mfma_f32_16x16x32_bf16 v[236:239], v[236:239], a[136:139], 0\n\t"
"v_cvt_pk_bf16_f32 v8, v8, v9\n\t"
"v_cvt_pk_bf16_f32 v10, v10, v11\n\t"
"v_mfma_f32_16x16x32_bf16 v[224:227], v[224:227], a[136:139], 0\n\t"
"v_accvgpr_write_b32 a56, v8\n\t"
"v_accvgpr_write_b32 a57, v10\n\t"
"v_mfma_f32_16x16x32_bf16 v[16:19], a[140:143], a[184:187], v[16:19]\n\t"
"v_mfma_f32_16x16x32_bf16 v[16:19], v[208:211], a[188:191], v[16:19]\n\t"
"v_cvt_pk_bf16_f32 v12, v12, v13\n\t"
"v_cvt_pk_bf16_f32 v14, v14, v15\n\t"
"v_mfma_f32_16x16x32_bf16 v[16:19], v[216:219], a[192:195], v[16:19]\n\t"
"ds_read_b64_tr_b16 v[232:233], v240 offset:40448\n\t"
"v_accvgpr_write_b32 a64, v12\n\t"
"v_mfma_f32_16x16x32_bf16 v[20:23], a[140:143], a[188:191], v[20:23]\n\t"
"s_waitcnt lgkmcnt(2)\n\t"
"ds_read_b64_tr_b16 v[234:235], v240 offset:41984\n\t"
"v_cvt_pk_bf16_f32 v236, v236, v237\n\t"
"v_mfma_f32_16x16x32_bf16 v[20:23], v[208:211], a[192:195], v[20:23]\n\t"
"s_mov_b32 s20, 258048\n\t"
"ds_read_b64_tr_b16 v[228:229], v241 offset:40448\n\t"
"v_cvt_pk_bf16_f32 v237, v238, v239\n\t"
"v_mfma_f32_16x16x32_bf16 v[20:23], v[216:219], a[196:199], v[20:23]\n\t"
"ds_read_b64_tr_b16 v[230:231], v241 offset:41984\n\t"
"v_cvt_pk_bf16_f32 v238, v224, v225\n\t"
"v_mfma_f32_16x16x32_bf16 v[24:27], a[140:143], a[192:195], v[24:27]\n\t"
"s_waitcnt lgkmcnt(4)\n\t"
"ds_read_b128 a[32:35], v252 offset:12288\n\t"
"v_cvt_pk_bf16_f32 v239, v226, v227\n\t"
"v_mfma_f32_16x16x32_bf16 v[24:27], v[208:211], a[196:199], v[24:27]\n\t"
"ds_read_b128 a[104:107], v252 offset:13312\n\t"
"v_accvgpr_write_b32 a65, v14\n\t"
"v_mfma_f32_16x16x32_bf16 v[24:27], v[216:219], a[200:203], v[24:27]\n\t"
"buffer_store_dwordx4 a[96:99], v248, s[16:19], s29 offen offset:128 nt\n\t"
"ds_read_b128 a[112:115], v252 offset:14336\n\t"
"v_cvt_pk_bf16_f32 v16, v16, v17\n\t"
"v_cvt_pk_bf16_f32 v18, v18, v19\n\t"
"v_accvgpr_write_b32 a72, v16\n\t"
"v_mfma_f32_16x16x32_bf16 v[28:31], a[140:143], a[196:199], v[28:31]\n\t"
"v_cvt_pk_bf16_f32 v20, v20, v21\n\t"
"v_cvt_pk_bf16_f32 v22, v22, v23\n\t"
"v_mfma_f32_16x16x32_bf16 v[28:31], v[208:211], a[200:203], v[28:31]\n\t"
"v_accvgpr_write_b32 a73, v18\n\t"
"v_accvgpr_write_b32 a80, v20\n\t"
"v_mfma_f32_16x16x32_bf16 v[28:31], v[216:219], a[204:207], v[28:31]\n\t"
"ds_write_b128 v242, v[236:239] offset:30720\n\t"
"v_mfma_f32_16x16x32_bf16 v[32:35], a[4:7], a[168:171], v[32:35]\n\t"
"buffer_load_dwordx4 v[208:211], v252, s[8:11], s20 offen offset:0 sc1\n\t"
"v_mfma_f32_16x16x32_bf16 v[32:35], a[12:15], a[172:175], v[32:35]\n\t"
"buffer_load_dwordx4 v[216:219], v252, s[8:11], s20 offen offset:1024 sc1\n\t"
"v_mfma_f32_16x16x32_bf16 v[32:35], a[208:211], a[176:179], v[32:35]\n\t"
"v_cvt_pk_bf16_f32 v24, v24, v25\n\t"
"v_cvt_pk_bf16_f32 v26, v26, v27\n\t"
"v_mfma_f32_16x16x32_bf16 v[36:39], a[4:7], a[172:175], v[36:39]\n\t"
"s_waitcnt lgkmcnt(6)\n\t"
"v_accvgpr_write_b32 a81, v22\n\t"
"v_cvt_pk_bf16_f32 v28, v28, v29\n\t"
"v_mfma_f32_16x16x32_bf16 v[36:39], a[12:15], a[176:179], v[36:39]\n\t"
"s_waitcnt lgkmcnt(4)\n\t"
"v_cvt_pk_bf16_f32 v30, v30, v31\n\t"
"v_accvgpr_write_b32 a88, v24\n\t"
"v_mfma_f32_16x16x32_bf16 v[36:39], a[208:211], a[180:183], v[36:39]\n\t"
"v_accvgpr_write_b32 a89, v26\n\t"
"v_accvgpr_write_b32 a96, v28\n\t"
"v_mfma_f32_16x16x32_bf16 v[232:235], v[232:235], a[136:139], 0\n\t"
"v_cvt_pk_bf16_f32 v32, v32, v33\n\t"
"v_cvt_pk_bf16_f32 v34, v34, v35\n\t"
"v_mfma_f32_16x16x32_bf16 v[228:231], v[228:231], a[136:139], 0\n\t"
"v_accvgpr_write_b32 a97, v30\n\t"
"v_accvgpr_write_b32 a42, v32\n\t"
"v_mfma_f32_16x16x32_bf16 v[40:43], a[4:7], a[176:179], v[40:43]\n\t"
"v_accvgpr_write_b32 a43, v34\n\t"
"v_mfma_f32_16x16x32_bf16 v[40:43], a[12:15], a[180:183], v[40:43]\n\t"
"v_cvt_pk_bf16_f32 v36, v36, v37\n\t"
"v_cvt_pk_bf16_f32 v38, v38, v39\n\t"
"v_mfma_f32_16x16x32_bf16 v[40:43], a[208:211], a[184:187], v[40:43]\n\t"
"buffer_store_dwordx4 a[108:111], v248, s[16:19], s29 offen offset:256 nt\n\t"
"v_accvgpr_write_b32 a50, v36\n\t"
"v_accvgpr_write_b32 a51, v38\n\t"
"s_add_u32 s29, s22, 590208\n\t"
"v_mfma_f32_16x16x32_bf16 v[44:47], a[4:7], a[180:183], v[44:47]\n\t"
"v_cvt_pk_bf16_f32 v232, v232, v233\n\t"
"v_cvt_pk_bf16_f32 v233, v234, v235\n\t"
"v_mfma_f32_16x16x32_bf16 v[44:47], a[12:15], a[184:187], v[44:47]\n\t"
"v_cvt_pk_bf16_f32 v234, v228, v229\n\t"
"v_mfma_f32_16x16x32_bf16 v[44:47], a[208:211], a[188:191], v[44:47]\n\t"
"s_waitcnt lgkmcnt(1)\n\t"
"s_barrier\n\t"
"v_mfma_f32_16x16x32_bf16 v[48:51], a[4:7], a[184:187], v[48:51]\n\t"
"ds_read_b64_tr_b16 v[224:225], v240 offset:512\n\t"
"v_mfma_f32_16x16x32_bf16 v[48:51], a[12:15], a[188:191], v[48:51]\n\t"
"ds_read_b64_tr_b16 v[226:227], v240 offset:2048\n\t"
"v_cvt_pk_bf16_f32 v235, v230, v231\n\t"
"v_mfma_f32_16x16x32_bf16 v[48:51], a[208:211], a[192:195], v[48:51]\n\t"
"ds_write_b128 v242, v[232:235] offset:32256\n\t"
"v_mfma_f32_16x16x32_bf16 v[52:55], a[4:7], a[188:191], v[52:55]\n\t"
"v_cvt_pk_bf16_f32 v40, v40, v41\n\t"
"v_cvt_pk_bf16_f32 v42, v42, v43\n\t"
"v_mfma_f32_16x16x32_bf16 v[52:55], a[12:15], a[192:195], v[52:55]\n\t"
"ds_read_b64_tr_b16 v[236:237], v241 offset:512\n\t"
"v_mfma_f32_16x16x32_bf16 v[52:55], a[208:211], a[196:199], v[52:55]\n\t"
"ds_read_b64_tr_b16 v[238:239], v241 offset:2048\n\t"
"v_cvt_pk_bf16_f32 v44, v44, v45\n\t"
"v_mfma_f32_16x16x32_bf16 v[56:59], a[4:7], a[192:195], v[56:59]\n\t"
"v_cvt_pk_bf16_f32 v46, v46, v47\n\t"
"v_accvgpr_write_b32 a58, v40\n\t"
"v_mfma_f32_16x16x32_bf16 v[56:59], a[12:15], a[196:199], v[56:59]\n\t"
"v_accvgpr_write_b32 a59, v42\n\t"
"v_cvt_pk_bf16_f32 v48, v48, v49\n\t"
"v_mfma_f32_16x16x32_bf16 v[56:59], a[208:211], a[200:203], v[56:59]\n\t"
"buffer_store_dwordx4 a[116:119], v248, s[16:19], s29 offen offset:0 nt\n\t"
"v_cvt_pk_bf16_f32 v50, v50, v51\n\t"
"v_accvgpr_write_b32 a66, v44\n\t"
"v_accvgpr_write_b32 a67, v46\n\t"
"v_accvgpr_write_b32 a74, v48\n\t"
"v_mfma_f32_16x16x32_bf16 v[60:63], a[4:7], a[196:199], v[60:63]\n\t"
"Lpg13_%=:\n\t"
"s_cbranch_vccnz Lvr1_%=\n\t"
"s_waitcnt lgkmcnt(3)\n\t"
"v_cvt_pk_bf16_f32 v52, v52, v53\n\t"
"v_mfma_f32_16x16x32_bf16 v[60:63], a[12:15], a[200:203], v[60:63]\n\t"
"s_waitcnt lgkmcnt(0)\n\t"
"v_cvt_pk_bf16_f32 v54, v54, v55\n\t"
"v_accvgpr_write_b32 a75, v50\n\t"
"v_mfma_f32_16x16x32_bf16 v[60:63], a[208:211], a[204:207], v[60:63]\n\t"
"v_accvgpr_write_b32 a82, v52\n\t"
"v_accvgpr_write_b32 a83, v54\n\t"
"v_mfma_f32_16x16x32_bf16 v[224:227], v[224:227], a[136:139], 0\n\t"
"buffer_load_dwordx4 a[4:7], v252, s[8:11], s20 offen offset:2048 sc1\n\t"
"s_mov_b32 s20, 270336\n\t"
"v_mfma_f32_16x16x32_bf16 v[236:239], v[236:239], a[136:139], 0\n\t"
"buffer_load_dwordx4 a[12:15], v252, s[8:11], s20 offen offset:0 sc1\n\t"
"v_mfma_f32_16x16x32_bf16 v[64:67], a[212:215], a[168:171], v[64:67]\n\t"
"ds_read_b128 a[168:171], v255 offset:1536\n\t"
"v_subrev_u32 v240, 59904, v240\n\t"
"v_mfma_f32_16x16x32_bf16 v[64:67], a[216:219], a[172:175], v[64:67]\n\t"
"v_subrev_u32 v241, 59904, v241\n\t"
"v_cvt_pk_bf16_f32 v56, v56, v57\n\t"
"v_mfma_f32_16x16x32_bf16 v[64:67], a[220:223], a[176:179], v[64:67]\n\t"
"ds_read_b64_tr_b16 v[228:229], v240 offset:512\n\t"
"v_cvt_pk_bf16_f32 v58, v58, v59\n\t"
"v_mfma_f32_16x16x32_bf16 v[68:71], a[212:215], a[172:175], v[68:71]\n\t"
"ds_read_b64_tr_b16 v[230:231], v240 offset:2048\n\t"
"v_cvt_pk_bf16_f32 v224, v224, v225\n\t"
"v_mfma_f32_16x16x32_bf16 v[68:71], a[216:219], a[176:179], v[68:71]\n\t"
"ds_read_b64_tr_b16 v[232:233], v241 offset:512\n\t"
"v_cvt_pk_bf16_f32 v225, v226, v227\n\t"
"v_mfma_f32_16x16x32_bf16 v[68:71], a[220:223], a[180:183], v[68:71]\n\t"
"buffer_load_dwordx4 a[208:211], v252, s[8:11], s20 offen offset:1024 sc1\n\t"
"v_mfma_f32_16x16x32_bf16 v[72:75], a[212:215], a[176:179], v[72:75]\n\t"
"ds_read_b64_tr_b16 v[234:235], v241 offset:2048\n\t"
"v_cvt_pk_bf16_f32 v226, v236, v237\n\t"
"v_mfma_f32_16x16x32_bf16 v[72:75], a[216:219], a[180:183], v[72:75]\n\t"
"ds_read_b128 a[172:175], v255 offset:4608\n\t"
"v_cvt_pk_bf16_f32 v227, v238, v239\n\t"
"v_mfma_f32_16x16x32_bf16 v[72:75], a[220:223], a[184:187], v[72:75]\n\t"
"buffer_store_dwordx4 a[124:127], v248, s[16:19], s29 offen offset:128 nt\n\t"
"ds_read_b128 a[176:179], v255 offset:7680\n\t"
"v_cvt_pk_bf16_f32 v60, v60, v61\n\t"
"v_cvt_pk_bf16_f32 v62, v62, v63\n\t"
"v_accvgpr_write_b32 a90, v56\n\t"
"v_mfma_f32_16x16x32_bf16 v[76:79], a[212:215], a[180:183], v[76:79]\n\t"
"ds_read_b128 a[180:183], v255 offset:10752\n\t"
"v_accvgpr_write_b32 a91, v58\n\t"
"v_mfma_f32_16x16x32_bf16 v[76:79], a[216:219], a[184:187], v[76:79]\n\t"
"v_accvgpr_write_b32 a98, v60\n\t"
"v_accvgpr_write_b32 a99, v62\n\t"
"v_mfma_f32_16x16x32_bf16 v[76:79], a[220:223], a[188:191], v[76:79]\n\t"
"ds_write_b128 v242, v[224:227] offset:29184\n\t"
"v_mfma_f32_16x16x32_bf16 v[80:83], a[212:215], a[184:187], v[80:83]\n\t"
"v_cvt_pk_bf16_f32 v64, v64, v65\n\t"
"v_cvt_pk_bf16_f32 v66, v66, v67\n\t"
"v_mfma_f32_16x16x32_bf16 v[80:83], a[216:219], a[188:191], v[80:83]\n\t"
"ds_read_b128 a[184:187], v255 offset:13824\n\t"
"v_mfma_f32_16x16x32_bf16 v[80:83], a[220:223], a[192:195], v[80:83]\n\t"
"buffer_store_dwordx4 a[156:159], v248, s[16:19], s29 offen offset:256 nt\n\t"
"v_cvt_pk_bf16_f32 v68, v68, v69\n\t"
"v_cvt_pk_bf16_f32 v70, v70, v71\n\t"
"v_accvgpr_write_b32 a108, v64\n\t"
"v_accvgpr_write_b32 a109, v66\n\t"
"v_mfma_f32_16x16x32_bf16 v[84:87], a[212:215], a[188:191], v[84:87]\n\t"
"s_waitcnt lgkmcnt(7)\n\t"
"ds_read_b128 a[188:191], v255 offset:16896\n\t"
"v_cvt_pk_bf16_f32 v72, v72, v73\n\t"
"v_mfma_f32_16x16x32_bf16 v[84:87], a[216:219], a[192:195], v[84:87]\n\t"
"s_waitcnt lgkmcnt(6)\n\t"
"v_cvt_pk_bf16_f32 v74, v74, v75\n\t"
"v_accvgpr_write_b32 a116, v68\n\t"
"v_mfma_f32_16x16x32_bf16 v[84:87], a[220:223], a[196:199], v[84:87]\n\t"
"v_accvgpr_write_b32 a117, v70\n\t"
"v_cvt_pk_bf16_f32 v76, v76, v77\n\t"
"v_mfma_f32_16x16x32_bf16 v[228:231], v[228:231], a[136:139], 0\n\t"
"v_cvt_pk_bf16_f32 v78, v78, v79\n\t"
"v_accvgpr_write_b32 a124, v72\n\t"
"v_mfma_f32_16x16x32_bf16 v[232:235], v[232:235], a[136:139], 0\n\t"
"v_accvgpr_write_b32 a125, v74\n\t"
"v_cvt_pk_bf16_f32 v80, v80, v81\n\t"
"v_mfma_f32_16x16x32_bf16 v[88:91], a[212:215], a[192:195], v[88:91]\n\t"
"s_add_u32 s29, s22, 786816\n\t"
"ds_read_b128 a[192:195], v255 offset:19968\n\t"
"v_cvt_pk_bf16_f32 v82, v82, v83\n\t"
"v_mfma_f32_16x16x32_bf16 v[88:91], a[216:219], a[196:199], v[88:91]\n\t"
"v_accvgpr_write_b32 a132, v76\n\t"
"v_accvgpr_write_b32 a133, v78\n\t"
"v_mfma_f32_16x16x32_bf16 v[88:91], a[220:223], a[200:203], v[88:91]\n\t"
"ds_read_b64_tr_b16 v[236:237], v240 offset:20480\n\t"
"v_cvt_pk_bf16_f32 v84, v84, v85\n\t"
"v_mfma_f32_16x16x32_bf16 v[92:95], a[212:215], a[196:199], v[92:95]\n\t"
"s_waitcnt lgkmcnt(6)\n\t"
"ds_read_b128 a[196:199], v255 offset:23040\n\t"
"v_cvt_pk_bf16_f32 v228, v228, v229\n\t"
"v_mfma_f32_16x16x32_bf16 v[92:95], a[216:219], a[200:203], v[92:95]\n\t"
"ds_read_b128 a[200:203], v255 offset:26112\n\t"
"v_cvt_pk_bf16_f32 v229, v230, v231\n\t"
"v_mfma_f32_16x16x32_bf16 v[92:95], a[220:223], a[204:207], v[92:95]\n\t"
"ds_read_b128 a[204:207], v255 offset:29184\n\t"
"v_cvt_pk_bf16_f32 v230, v232, v233\n\t"
"v_mfma_f32_16x16x32_bf16 v[100:103], a[224:227], a[168:171], v[100:103]\n\t"
"s_waitcnt lgkmcnt(8)\n\t"
"ds_read_b64_tr_b16 v[238:239], v240 offset:22016\n\t"
"v_cvt_pk_bf16_f32 v231, v234, v235\n\t"
"v_mfma_f32_16x16x32_bf16 v[100:103], a[228:231], a[172:175], v[100:103]\n\t"
"ds_read_b64_tr_b16 v[224:225], v241 offset:20480\n\t"
"v_cvt_pk_bf16_f32 v86, v86, v87\n\t"
"v_mfma_f32_16x16x32_bf16 v[100:103], a[236:239], a[176:179], v[100:103]\n\t"
"buffer_load_dwordx4 a[212:215], v252, s[8:11], s20 offen offset:2048 sc1\n\t"
"s_mov_b32 s20, 282624\n\t"
"v_mfma_f32_16x16x32_bf16 v[104:107], a[224:227], a[172:175], v[104:107]\n\t"
"buffer_load_dwordx4 a[216:219], v252, s[8:11], s20 offen offset:0 sc1\n\t"
"v_mfma_f32_16x16x32_bf16 v[104:107], a[228:231], a[176:179], v[104:107]\n\t"
"s_waitcnt lgkmcnt(8)\n\t"
"ds_read_b64_tr_b16 v[226:227], v241 offset:22016\n\t"
"v_mfma_f32_16x16x32_bf16 v[104:107], a[236:239], a[180:183], v[104:107]\n\t"
"s_waitcnt lgkmcnt(3)\n\t"
"s_barrier\n\t"
"v_mfma_f32_16x16x32_bf16 v[108:111], a[224:227], a[176:179], v[108:111]\n\t"
"v_accvgpr_write_b32 a140, v80\n\t"
"v_mfma_f32_16x16x32_bf16 v[108:111], a[228:231], a[180:183], v[108:111]\n\t"
"buffer_load_dwordx4 a[220:223], v252, s[8:11], s20 offen offset:1024 sc1\n\t"
"v_mfma_f32_16x16x32_bf16 v[108:111], a[236:239], a[184:187], v[108:111]\n\t"
"ds_write_b128 v242, v[228:231] offset:24576\n\t"
"v_mfma_f32_16x16x32_bf16 v[112:115], a[224:227], a[180:183], v[112:115]\n\t"
"s_waitcnt lgkmcnt(3)\n\t"
"v_accvgpr_write_b32 a141, v82\n\t"
"v_accvgpr_write_b32 a148, v84\n\t"
"v_mfma_f32_16x16x32_bf16 v[112:115], a[228:231], a[184:187], v[112:115]\n\t"
"s_waitcnt lgkmcnt(1)\n\t"
"v_accvgpr_write_b32 a149, v86\n\t"
"v_cvt_pk_bf16_f32 v88, v88, v89\n\t"
"v_mfma_f32_16x16x32_bf16 v[112:115], a[236:239], a[188:191], v[112:115]\n\t"
"buffer_store_dwordx4 a[52:55], v248, s[16:19], s29 offen offset:0 nt\n\t"
"ds_write_b64 v250, a[108:109] offset:0\n\t"
"v_cvt_pk_bf16_f32 v90, v90, v91\n\t"
"v_mfma_f32_16x16x32_bf16 v[236:239], v[236:239], a[136:139], 0\n\t"
"ds_write_b128 v249, a[40:43] offset:0\n\t"
"v_mfma_f32_16x16x32_bf16 v[224:227], v[224:227], a[136:139], 0\n\t"
"buffer_store_dwordx4 a[84:87], v248, s[16:19], s29 offen offset:128 nt\n\t"
"v_cvt_pk_bf16_f32 v92, v92, v93\n\t"
"v_cvt_pk_bf16_f32 v94, v94, v95\n\t"
"v_accvgpr_write_b32 a156, v88\n\t"
"v_accvgpr_write_b32 a157, v90\n\t"
"v_mfma_f32_16x16x32_bf16 v[116:119], a[224:227], a[184:187], v[116:119]\n\t"
"v_cvt_pk_bf16_f32 v100, v100, v101\n\t"
"v_cvt_pk_bf16_f32 v102, v102, v103\n\t"
"v_mfma_f32_16x16x32_bf16 v[116:119], a[228:231], a[188:191], v[116:119]\n\t"
"v_accvgpr_write_b32 a164, v92\n\t"
"v_accvgpr_write_b32 a165, v94\n\t"
"v_mfma_f32_16x16x32_bf16 v[116:119], a[236:239], a[192:195], v[116:119]\n\t"
"ds_read_b64_tr_b16 v[232:233], v240 offset:40448\n\t"
"v_cvt_pk_bf16_f32 v104, v104, v105\n\t"
"v_mfma_f32_16x16x32_bf16 v[120:123], a[224:227], a[188:191], v[120:123]\n\t"
"ds_read_b64_tr_b16 v[234:235], v240 offset:41984\n\t"
"v_cvt_pk_bf16_f32 v236, v236, v237\n\t"
"v_mfma_f32_16x16x32_bf16 v[120:123], a[228:231], a[192:195], v[120:123]\n\t"
"ds_read_b64_tr_b16 v[228:229], v241 offset:40448\n\t"
"v_cvt_pk_bf16_f32 v237, v238, v239\n\t"
"v_mfma_f32_16x16x32_bf16 v[120:123], a[236:239], a[196:199], v[120:123]\n\t"
"ds_read_b64_tr_b16 v[230:231], v241 offset:41984\n\t"
"v_cvt_pk_bf16_f32 v238, v224, v225\n\t"
"v_mfma_f32_16x16x32_bf16 v[124:127], a[224:227], a[192:195], v[124:127]\n\t"
"v_cvt_pk_bf16_f32 v239, v226, v227\n\t"
"v_cvt_pk_bf16_f32 v106, v106, v107\n\t"
"v_mfma_f32_16x16x32_bf16 v[124:127], a[228:231], a[196:199], v[124:127]\n\t"
"v_accvgpr_write_b32 a44, v100\n\t"
"v_accvgpr_write_b32 a45, v102\n\t"
"v_mfma_f32_16x16x32_bf16 v[124:127], a[236:239], a[200:203], v[124:127]\n\t"
"ds_write_b128 v242, v[236:239] offset:26112\n\t"
"v_mfma_f32_16x16x32_bf16 v[128:131], a[224:227], a[196:199], v[128:131]\n\t"
"buffer_load_dwordx4 a[224:227], v252, s[8:11], s20 offen offset:2048 sc1\n\t"
"v_mfma_f32_16x16x32_bf16 v[128:131], a[228:231], a[200:203], v[128:131]\n\t"
"s_mov_b32 s20, 516096\n\t"
"v_cvt_pk_bf16_f32 v108, v108, v109\n\t"
"v_cvt_pk_bf16_f32 v110, v110, v111\n\t"
"v_mfma_f32_16x16x32_bf16 v[128:131], a[236:239], a[204:207], v[128:131]\n\t"
"ds_write_b128 v249, a[56:59] offset:12288\n\t"
"v_mfma_f32_16x16x32_bf16 v[132:135], a[240:243], a[168:171], v[132:135]\n\t"
"buffer_load_dwordx4 a[228:231], v252, s[8:11], s20 offen offset:0 sc1\n\t"
"v_mfma_f32_16x16x32_bf16 v[132:135], a[244:247], a[172:175], v[132:135]\n\t"
"v_accvgpr_write_b32 a52, v104\n\t"
"v_accvgpr_write_b32 a53, v106\n\t"
"v_mfma_f32_16x16x32_bf16 v[132:135], a[248:251], a[176:179], v[132:135]\n\t"
"ds_write_b64 v250, a[124:125] offset:12288\n\t"
"v_mfma_f32_16x16x32_bf16 v[136:139], a[240:243], a[172:175], v[136:139]\n\t"
"buffer_load_dwordx4 a[236:239], v252, s[8:11], s20 offen offset:1024 sc1\n\t"
"v_mfma_f32_16x16x32_bf16 v[136:139], a[244:247], a[176:179], v[136:139]\n\t"
"s_waitcnt lgkmcnt(3)\n\t"
"v_cvt_pk_bf16_f32 v112, v112, v113\n\t"
"v_cvt_pk_bf16_f32 v114, v114, v115\n\t"
"v_mfma_f32_16x16x32_bf16 v[136:139], a[248:251], a[180:183], v[136:139]\n\t"
"buffer_store_dwordx4 v[196:199], v248, s[16:19], s29 offen offset:256 nt\n\t"
"ds_write_b64 v250, a[116:117] offset:6144\n\t"
"v_accvgpr_write_b32 a60, v108\n\t"
"v_mfma_f32_16x16x32_bf16 v[232:235], v[232:235], a[136:139], 0\n\t"
"s_add_u32 s29, s22, 983424\n\t"
"ds_write_b128 v249, a[48:51] offset:6144\n\t"
"v_accvgpr_write_b32 a61, v110\n\t"
"v_mfma_f32_16x16x32_bf16 v[228:231], v[228:231], a[136:139], 0\n\t"
"v_accvgpr_write_b32 a68, v112\n\t"
"v_accvgpr_write_b32 a69, v114\n\t"
"v_mfma_f32_16x16x32_bf16 v[140:143], a[240:243], a[176:179], v[140:143]\n\t"
"v_cvt_pk_bf16_f32 v116, v116, v117\n\t"
"v_cvt_pk_bf16_f32 v118, v118, v119\n\t"
"v_mfma_f32_16x16x32_bf16 v[140:143], a[244:247], a[180:183], v[140:143]\n\t"
"v_cvt_pk_bf16_f32 v120, v120, v121\n\t"
"v_cvt_pk_bf16_f32 v122, v122, v123\n\t"
"v_mfma_f32_16x16x32_bf16 v[140:143], a[248:251], a[184:187], v[140:143]\n\t"
"ds_write_b128 v249, a[64:67] offset:18432\n\t"
"v_mfma_f32_16x16x32_bf16 v[144:147], a[240:243], a[180:183], v[144:147]\n\t"
"v_cvt_pk_bf16_f32 v232, v232, v233\n\t"
"v_cvt_pk_bf16_f32 v233, v234, v235\n\t"
"v_mfma_f32_16x16x32_bf16 v[144:147], a[244:247], a[184:187], v[144:147]\n\t"
"v_accvgpr_write_b32 a76, v116\n\t"
"v_accvgpr_write_b32 a77, v118\n\t"
"v_mfma_f32_16x16x32_bf16 v[144:147], a[248:251], a[188:191], v[144:147]\n\t"
"ds_write_b64 v250, a[132:133] offset:18432\n\t"
"v_mfma_f32_16x16x32_bf16 v[148:151], a[240:243], a[184:187], v[148:151]\n\t"
"v_cvt_pk_bf16_f32 v234, v228, v229\n\t"
"v_cvt_pk_bf16_f32 v235, v230, v231\n\t"
"v_mfma_f32_16x16x32_bf16 v[148:151], a[244:247], a[188:191], v[148:151]\n\t"
"v_cvt_pk_bf16_f32 v124, v124, v125\n\t"
"v_cvt_pk_bf16_f32 v126, v126, v127\n\t"
"v_mfma_f32_16x16x32_bf16 v[148:151], a[248:251], a[192:195], v[148:151]\n\t"
"ds_write_b128 v242, v[232:235] offset:27648\n\t"
"v_mfma_f32_16x16x32_bf16 v[152:155], a[240:243], a[188:191], v[152:155]\n\t"
"v_accvgpr_write_b32 a84, v120\n\t"
"v_accvgpr_write_b32 a85, v122\n\t"
"v_mfma_f32_16x16x32_bf16 v[152:155], a[244:247], a[192:195], v[152:155]\n\t"
"v_cvt_pk_bf16_f32 v128, v128, v129\n\t"
"v_cvt_pk_bf16_f32 v130, v130, v131\n\t"
"v_mfma_f32_16x16x32_bf16 v[152:155], a[248:251], a[196:199], v[152:155]\n\t"
"buffer_store_dwordx4 v[204:207], v248, s[16:19], s29 offen offset:0 nt\n\t"
"v_accvgpr_write_b32 a92, v124\n\t"
"v_accvgpr_write_b32 a93, v126\n\t"
"v_cvt_pk_bf16_f32 v132, v132, v133\n\t"
"v_cvt_pk_bf16_f32 v134, v134, v135\n\t"
"v_mfma_f32_16x16x32_bf16 v[156:159], a[240:243], a[192:195], v[156:159]\n\t"
"v_accvgpr_write_b32 a100, v128\n\t"
"v_accvgpr_write_b32 a101, v130\n\t"
"v_mfma_f32_16x16x32_bf16 v[156:159], a[244:247], a[196:199], v[156:159]\n\t"
"v_cvt_pk_bf16_f32 v136, v136, v137\n\t"
"v_cvt_pk_bf16_f32 v138, v138, v139\n\t"
"v_mfma_f32_16x16x32_bf16 v[156:159], a[248:251], a[200:203], v[156:159]\n\t"
"v_accvgpr_write_b32 a46, v132\n\t"
"v_accvgpr_write_b32 a47, v134\n\t"
"v_mfma_f32_16x16x32_bf16 v[160:163], a[240:243], a[196:199], v[160:163]\n\t"
"v_accvgpr_write_b32 a54, v136\n\t"
"v_accvgpr_write_b32 a55, v138\n\t"
"v_mfma_f32_16x16x32_bf16 v[160:163], a[244:247], a[200:203], v[160:163]\n\t"
"v_cvt_pk_bf16_f32 v140, v140, v141\n\t"
"v_cvt_pk_bf16_f32 v142, v142, v143\n\t"
"v_mfma_f32_16x16x32_bf16 v[160:163], a[248:251], a[204:207], v[160:163]\n\t"
"v_cvt_pk_bf16_f32 v144, v144, v145\n\t"
"v_cvt_pk_bf16_f32 v146, v146, v147\n\t"
"v_mfma_f32_16x16x32_bf16 v[164:167], a[252:255], a[168:171], v[164:167]\n\t"
"v_accvgpr_write_b32 a62, v140\n\t"
"v_accvgpr_write_b32 a63, v142\n\t"
"v_mfma_f32_16x16x32_bf16 v[164:167], a[144:147], a[172:175], v[164:167]\n\t"
"v_cvt_pk_bf16_f32 v148, v148, v149\n\t"
"v_cvt_pk_bf16_f32 v150, v150, v151\n\t"
"v_mfma_f32_16x16x32_bf16 v[164:167], a[20:23], a[176:179], v[164:167]\n\t"
"buffer_load_dwordx4 a[240:243], v252, s[8:11], s20 offen offset:2048 sc1\n\t"
"s_mov_b32 s20, 528384\n\t"
"v_mfma_f32_16x16x32_bf16 v[168:171], a[252:255], a[172:175], v[168:171]\n\t"
"buffer_load_dwordx4 a[244:247], v252, s[8:11], s20 offen offset:0 sc1\n\t"
"v_mfma_f32_16x16x32_bf16 v[168:171], a[144:147], a[176:179], v[168:171]\n\t"
"buffer_load_dwordx4 a[248:251], v252, s[8:11], s20 offen offset:1024 sc1\n\t"
"v_mfma_f32_16x16x32_bf16 v[168:171], a[20:23], a[180:183], v[168:171]\n\t"
"buffer_store_dwordx4 v[212:215], v248, s[16:19], s29 offen offset:128 nt\n\t"
"v_accvgpr_write_b32 a70, v144\n\t"
"v_accvgpr_write_b32 a71, v146\n\t"
"v_cvt_pk_bf16_f32 v152, v152, v153\n\t"
"v_cvt_pk_bf16_f32 v154, v154, v155\n\t"
"v_mfma_f32_16x16x32_bf16 v[172:175], a[252:255], a[176:179], v[172:175]\n\t"
"v_accvgpr_write_b32 a78, v148\n\t"
"v_accvgpr_write_b32 a79, v150\n\t"
"v_mfma_f32_16x16x32_bf16 v[172:175], a[144:147], a[180:183], v[172:175]\n\t"
"v_cvt_pk_bf16_f32 v156, v156, v157\n\t"
"v_cvt_pk_bf16_f32 v158, v158, v159\n\t"
"v_mfma_f32_16x16x32_bf16 v[172:175], a[20:23], a[184:187], v[172:175]\n\t"
"v_accvgpr_write_b32 a86, v152\n\t"
"v_accvgpr_write_b32 a87, v154\n\t"
"v_mfma_f32_16x16x32_bf16 v[176:179], a[252:255], a[180:183], v[176:179]\n\t"
"v_cvt_pk_bf16_f32 v160, v160, v161\n\t"
"v_cvt_pk_bf16_f32 v162, v162, v163\n\t"
"v_mfma_f32_16x16x32_bf16 v[176:179], a[144:147], a[184:187], v[176:179]\n\t"
"v_accvgpr_write_b32 a94, v156\n\t"
"v_mfma_f32_16x16x32_bf16 v[176:179], a[20:23], a[188:191], v[176:179]\n\t"
"s_waitcnt lgkmcnt(0)\n\t"
"s_barrier\n\t"
"v_mfma_f32_16x16x32_bf16 v[180:183], a[252:255], a[184:187], v[180:183]\n\t"
"ds_read_b128 a[168:171], v244 offset:24576\n\t"
"v_mfma_f32_16x16x32_bf16 v[180:183], a[144:147], a[188:191], v[180:183]\n\t"
"ds_read_b128 a[172:175], v244 offset:25088\n\t"
"v_accvgpr_write_b32 a95, v158\n\t"
"v_mfma_f32_16x16x32_bf16 v[180:183], a[20:23], a[192:195], v[180:183]\n\t"
"ds_read_b128 a[176:179], v244 offset:25600\n\t"
"v_cvt_pk_bf16_f32 v164, v164, v165\n\t"
"v_mfma_f32_16x16x32_bf16 v[184:187], a[252:255], a[188:191], v[184:187]\n\t"
"ds_read_b128 a[180:183], v244 offset:26112\n\t"
"v_cvt_pk_bf16_f32 v166, v166, v167\n\t"
"v_mfma_f32_16x16x32_bf16 v[184:187], a[144:147], a[192:195], v[184:187]\n\t"
"ds_read_b128 a[184:187], v244 offset:26624\n\t"
"v_accvgpr_write_b32 a102, v160\n\t"
"v_mfma_f32_16x16x32_bf16 v[184:187], a[20:23], a[196:199], v[184:187]\n\t"
"buffer_load_dwordx4 a[48:51], v252, s[8:11], s20 offen offset:2048 sc1\n\t"
"s_mov_b32 s20, 540672\n\t"
"v_mfma_f32_16x16x32_bf16 v[188:191], a[252:255], a[192:195], v[188:191]\n\t"
"ds_read_b128 a[188:191], v244 offset:27136\n\t"
"v_accvgpr_write_b32 a103, v162\n\t"
"v_mfma_f32_16x16x32_bf16 v[188:191], a[144:147], a[196:199], v[188:191]\n\t"
"ds_read_b128 a[192:195], v244 offset:27648\n\t"
"v_cvt_pk_bf16_f32 v168, v168, v169\n\t"
"v_mfma_f32_16x16x32_bf16 v[188:191], a[20:23], a[200:203], v[188:191]\n\t"
"buffer_store_dwordx4 a[0:3], v248, s[16:19], s29 offen offset:256 nt\n\t"
"buffer_load_dwordx4 a[56:59], v252, s[8:11], s20 offen offset:0 sc1\n\t"
"v_cvt_pk_bf16_f32 v170, v170, v171\n\t"
"v_mfma_f32_16x16x32_bf16 v[192:195], a[252:255], a[196:199], v[192:195]\n\t"
"s_waitcnt lgkmcnt(4)\n\t"
"ds_read_b128 a[196:199], v244 offset:28160\n\t"
"v_accvgpr_write_b32 a110, v164\n\t"
"v_mfma_f32_16x16x32_bf16 v[192:195], a[144:147], a[200:203], v[192:195]\n\t"
"ds_read_b128 a[200:203], v244 offset:28672\n\t"
"v_accvgpr_write_b32 a111, v166\n\t"
"v_mfma_f32_16x16x32_bf16 v[192:195], a[20:23], a[204:207], v[192:195]\n\t"
"buffer_load_dwordx4 a[64:67], v252, s[8:11], s20 offen offset:1024 sc1\n\t"
"s_add_u32 s29, s22, 1180032\n\t"
"Lst18_%=:\n\t"
"v_mfma_f32_16x16x32_bf16 v[2:5], v[96:99], a[168:171], 0\n\t"
"buffer_load_dwordx4 a[144:147], v252, s[8:11], s20 offen offset:2048 sc1\n\t"
"v_mfma_f32_16x16x32_bf16 v[2:5], a[232:235], a[172:175], v[2:5]\n\t"
"s_waitcnt lgkmcnt(5)\n\t"
"ds_read_b128 a[204:207], v244 offset:29184\n\t"
"v_cvt_pk_bf16_f32 v172, v172, v173\n\t"
"v_mfma_f32_16x16x32_bf16 v[2:5], a[152:155], a[176:179], v[2:5]\n\t"
"s_mov_b32 s20, 552960\n\t"
"v_cvt_pk_bf16_f32 v174, v174, v175\n\t"
"v_accvgpr_write_b32 a118, v168\n\t"
"v_mfma_f32_16x16x32_bf16 v[6:9], v[96:99], a[172:175], 0\n\t"
"s_waitcnt lgkmcnt(5)\n\t"
"v_accvgpr_write_b32 a119, v170\n\t"
"v_cvt_pk_bf16_f32 v176, v176, v177\n\t"
"v_mfma_f32_16x16x32_bf16 v[6:9], a[232:235], a[176:179], v[6:9]\n\t"
"v_cvt_pk_bf16_f32 v178, v178, v179\n\t"
"v_accvgpr_write_b32 a126, v172\n\t"
"v_mfma_f32_16x16x32_bf16 v[6:9], a[152:155], a[180:183], v[6:9]\n\t"
"buffer_load_dwordx4 a[20:23], v252, s[8:11], s20 offen offset:0 sc1\n\t"
"v_mfma_f32_16x16x32_bf16 v[10:13], v[96:99], a[176:179], 0\n\t"
"buffer_load_dwordx4 a[252:255], v252, s[8:11], s20 offen offset:1024 sc1\n\t"
"v_mfma_f32_16x16x32_bf16 v[10:13], a[232:235], a[180:183], v[10:13]\n\t"
"s_waitcnt lgkmcnt(4)\n\t"
"v_accvgpr_write_b32 a127, v174\n\t"
"v_cvt_pk_bf16_f32 v180, v180, v181\n\t"
"v_mfma_f32_16x16x32_bf16 v[10:13], a[152:155], a[184:187], v[10:13]\n\t"
"buffer_store_dwordx4 a[8:11], v248, s[16:19], s29 offen offset:0 nt\n\t"
"v_cvt_pk_bf16_f32 v182, v182, v183\n\t"
"v_accvgpr_write_b32 a134, v176\n\t"
"v_accvgpr_write_b32 a135, v178\n\t"
"v_cvt_pk_bf16_f32 v184, v184, v185\n\t"
"v_mfma_f32_16x16x32_bf16 v[14:17], v[96:99], a[180:183], 0\n\t"
"s_waitcnt lgkmcnt(3)\n\t"
"v_cvt_pk_bf16_f32 v186, v186, v187\n\t"
"v_accvgpr_write_b32 a142, v180\n\t"
"v_mfma_f32_16x16x32_bf16 v[14:17], a[232:235], a[184:187], v[14:17]\n\t"
"v_accvgpr_write_b32 a143, v182\n\t"
"v_cvt_pk_bf16_f32 v188, v188, v189\n\t"
"v_mfma_f32_16x16x32_bf16 v[14:17], a[152:155], a[188:191], v[14:17]\n\t"
"v_cvt_pk_bf16_f32 v190, v190, v191\n\t"
"v_accvgpr_write_b32 a150, v184\n\t"
"v_mfma_f32_16x16x32_bf16 v[18:21], v[96:99], a[184:187], 0\n\t"
"s_waitcnt lgkmcnt(2)\n\t"
"v_accvgpr_write_b32 a151, v186\n\t"
"v_cvt_pk_bf16_f32 v192, v192, v193\n\t"
"v_mfma_f32_16x16x32_bf16 v[18:21], a[232:235], a[188:191], v[18:21]\n\t"
"v_cvt_pk_bf16_f32 v194, v194, v195\n\t"
"v_accvgpr_write_b32 a158, v188\n\t"
"v_mfma_f32_16x16x32_bf16 v[18:21], a[152:155], a[192:195], v[18:21]\n\t"
"ds_read_b128 v[196:199], v252 offset:0\n\t"
"v_accvgpr_write_b32 a159, v190\n\t"
"v_mfma_f32_16x16x32_bf16 v[22:25], v[96:99], a[188:191], 0\n\t"
"s_waitcnt lgkmcnt(2)\n\t"
"ds_read_b128 a[0:3], v252 offset:12288\n\t"
"v_accvgpr_write_b32 a166, v192\n\t"
"v_mfma_f32_16x16x32_bf16 v[22:25], a[232:235], a[192:195], v[22:25]\n\t"
"ds_read_b128 v[204:207], v252 offset:1024\n\t"
"v_accvgpr_write_b32 a167, v194\n\t"
"v_mfma_f32_16x16x32_bf16 v[22:25], a[152:155], a[196:199], v[22:25]\n\t"
"ds_read_b128 a[8:11], v252 offset:13312\n\t"
"v_mfma_f32_16x16x32_bf16 v[26:29], v[96:99], a[192:195], 0\n\t"
"s_waitcnt lgkmcnt(4)\n\t"
"ds_read_b128 v[212:215], v252 offset:2048\n\t"
"v_mfma_f32_16x16x32_bf16 v[26:29], a[232:235], a[196:199], v[26:29]\n\t"
"ds_read_b128 a[40:43], v252 offset:14336\n\t"
"v_mfma_f32_16x16x32_bf16 v[26:29], a[152:155], a[200:203], v[26:29]\n\t"
"buffer_store_dwordx4 a[16:19], v248, s[16:19], s29 offen offset:128 nt\n\t"
"v_mfma_f32_16x16x32_bf16 v[30:33], v[96:99], a[196:199], 0\n\t"
"s_waitcnt vmcnt(38)\n\t"
"v_mfma_f32_16x16x32_bf16 v[30:33], a[232:235], a[200:203], v[30:33]\n\t"
"buffer_load_dwordx4 a[232:235], v252, s[8:11], s20 offen offset:2048 sc1\n\t"
"v_mfma_f32_16x16x32_bf16 v[30:33], a[152:155], a[204:207], v[30:33]\n\t"
"s_mov_b32 s20, 565248\n\t"
"v_mfma_f32_16x16x32_bf16 v[34:37], a[28:31], a[168:171], 0\n\t"
"v_mfma_f32_16x16x32_bf16 v[34:37], a[160:163], a[172:175], v[34:37]\n\t"
"buffer_load_dwordx4 a[152:155], v252, s[8:11], s20 offen offset:0 sc1\n\t"
"v_mfma_f32_16x16x32_bf16 v[34:37], a[36:39], a[176:179], v[34:37]\n\t"
"buffer_store_dwordx4 a[24:27], v248, s[16:19], s29 offen offset:256 nt\n\t"
"ds_read_b64_tr_b16 v[224:225], v240 offset:1024\n\t"
"ds_read_b64_tr_b16 v[226:227], v240 offset:2560\n\t"
"v_mfma_f32_16x16x32_bf16 v[38:41], a[28:31], a[172:175], 0\n\t"
"v_mfma_f32_16x16x32_bf16 v[38:41], a[160:163], a[176:179], v[38:41]\n\t"
"v_mfma_f32_16x16x32_bf16 v[38:41], a[36:39], a[180:183], v[38:41]\n\t"
"v_mfma_f32_16x16x32_bf16 v[42:45], a[28:31], a[176:179], 0\n\t"
"ds_read_b64_tr_b16 v[236:237], v241 offset:1024\n\t"
"v_mfma_f32_16x16x32_bf16 v[42:45], a[160:163], a[180:183], v[42:45]\n\t"
"ds_read_b64_tr_b16 v[238:239], v241 offset:2560\n\t"
"v_mfma_f32_16x16x32_bf16 v[42:45], a[36:39], a[184:187], v[42:45]\n\t"
"s_add_u32 s29, s22, 1376640\n\t"
"v_mfma_f32_16x16x32_bf16 v[46:49], a[28:31], a[180:183], 0\n\t"
"v_mfma_f32_16x16x32_bf16 v[46:49], a[160:163], a[184:187], v[46:49]\n\t"
"v_mfma_f32_16x16x32_bf16 v[46:49], a[36:39], a[188:191], v[46:49]\n\t"
"s_waitcnt lgkmcnt(4)\n\t"
"s_barrier\n\t"
"v_mfma_f32_16x16x32_bf16 v[50:53], a[28:31], a[184:187], 0\n\t"
"v_mfma_f32_16x16x32_bf16 v[50:53], a[160:163], a[188:191], v[50:53]\n\t"
"v_mfma_f32_16x16x32_bf16 v[50:53], a[36:39], a[192:195], v[50:53]\n\t"
"buffer_store_dwordx4 a[32:35], v248, s[16:19], s29 offen offset:0 nt\n\t"
"ds_write_b64 v250, a[140:141] offset:0\n\t"
"v_mfma_f32_16x16x32_bf16 v[54:57], a[28:31], a[188:191], 0\n\t"
"s_waitcnt lgkmcnt(3)\n\t"
"v_mfma_f32_16x16x32_bf16 v[54:57], a[160:163], a[192:195], v[54:57]\n\t"
"ds_read_b64_tr_b16 v[228:229], v240 offset:20992\n\t"
"v_mfma_f32_16x16x32_bf16 v[54:57], a[36:39], a[196:199], v[54:57]\n\t"
"ds_read_b64_tr_b16 v[230:231], v240 offset:22528\n\t"
"v_mfma_f32_16x16x32_bf16 v[224:227], v[224:227], a[136:139], 0\n\t"
"ds_write_b128 v249, a[72:75] offset:0\n\t"
"v_mfma_f32_16x16x32_bf16 v[58:61], a[28:31], a[192:195], 0\n\t"
"s_waitcnt lgkmcnt(4)\n\t"
"v_mfma_f32_16x16x32_bf16 v[58:61], a[160:163], a[196:199], v[58:61]\n\t"
"ds_read_b64_tr_b16 v[232:233], v241 offset:20992\n\t"
"v_mfma_f32_16x16x32_bf16 v[58:61], a[36:39], a[200:203], v[58:61]\n\t"
"ds_write_b128 v249, a[88:91] offset:12288\n\t"
"v_mfma_f32_16x16x32_bf16 v[236:239], v[236:239], a[136:139], 0\n\t"
"v_mfma_f32_16x16x32_bf16 v[62:65], a[28:31], a[196:199], 0\n\t"
"buffer_load_dwordx4 a[28:31], v252, s[8:11], s20 offen offset:1024 sc1\n\t"
"v_mfma_f32_16x16x32_bf16 v[62:65], a[160:163], a[200:203], v[62:65]\n\t"
"s_waitcnt vmcnt(39)\n\t"
"ds_read_b64_tr_b16 v[234:235], v241 offset:22528\n\t"
"v_cvt_pk_bf16_f32 v224, v224, v225\n\t"
"v_mfma_f32_16x16x32_bf16 v[62:65], a[36:39], a[204:207], v[62:65]\n\t"
"ds_write_b64 v250, a[156:157] offset:12288\n\t"
"v_mfma_f32_16x16x32_bf16 v[66:69], v[200:203], a[168:171], 0\n\t"
"buffer_load_dwordx4 a[160:163], v252, s[8:11], s20 offen offset:2048 sc1\n\t"
"v_mfma_f32_16x16x32_bf16 v[66:69], a[120:123], a[172:175], v[66:69]\n\t"
"s_mov_b32 s20, 577536\n\t"
"ds_read_b128 a[168:171], v244 offset:33792\n\t"
"v_cvt_pk_bf16_f32 v225, v226, v227\n\t"
"v_mfma_f32_16x16x32_bf16 v[66:69], a[128:131], a[176:179], v[66:69]\n\t"
"ds_write_b128 v249, a[80:83] offset:6144\n\t"
"v_mfma_f32_16x16x32_bf16 v[70:73], v[200:203], a[172:175], 0\n\t"
"buffer_load_dwordx4 a[36:39], v252, s[8:11], s20 offen offset:0 sc1\n\t"
"v_mfma_f32_16x16x32_bf16 v[70:73], a[120:123], a[176:179], v[70:73]\n\t"
"s_waitcnt lgkmcnt(7)\n\t"
"ds_read_b128 a[172:175], v244 offset:34304\n\t"
"v_mfma_f32_16x16x32_bf16 v[70:73], a[128:131], a[180:183], v[70:73]\n\t"
"buffer_store_dwordx4 a[104:107], v248, s[16:19], s29 offen offset:128 nt\n\t"
"ds_write_b64 v250, a[148:149] offset:6144\n\t"
"v_cvt_pk_bf16_f32 v226, v236, v237\n\t"
"v_cvt_pk_bf16_f32 v227, v238, v239\n\t"
"v_mfma_f32_16x16x32_bf16 v[228:231], v[228:231], a[136:139], 0\n\t"
"ds_write_b128 v249, a[96:99] offset:18432\n\t"
"v_mfma_f32_16x16x32_bf16 v[74:77], v[200:203], a[176:179], 0\n\t"
"s_waitcnt lgkmcnt(6)\n\t"
"v_mfma_f32_16x16x32_bf16 v[74:77], a[120:123], a[180:183], v[74:77]\n\t"
"ds_read_b128 a[176:179], v244 offset:34816\n\t"
"v_mfma_f32_16x16x32_bf16 v[74:77], a[128:131], a[184:187], v[74:77]\n\t"
"ds_write_b64 v250, a[164:165] offset:18432\n\t"
"v_mfma_f32_16x16x32_bf16 v[232:235], v[232:235], a[136:139], 0\n\t"
"v_mfma_f32_16x16x32_bf16 v[78:81], v[200:203], a[180:183], 0\n\t"
"ds_read_b128 a[180:183], v244 offset:35328\n\t"
"v_cvt_pk_bf16_f32 v228, v228, v229\n\t"
"v_mfma_f32_16x16x32_bf16 v[78:81], a[120:123], a[184:187], v[78:81]\n\t"
"v_cvt_pk_bf16_f32 v229, v230, v231\n\t"
"v_mfma_f32_16x16x32_bf16 v[78:81], a[128:131], a[188:191], v[78:81]\n\t"
"buffer_store_dwordx4 a[112:115], v248, s[16:19], s29 offen offset:256 nt\n\t"
"v_mfma_f32_16x16x32_bf16 v[82:85], v[200:203], a[184:187], 0\n\t"
"ds_read_b128 a[184:187], v244 offset:35840\n\t"
"v_mfma_f32_16x16x32_bf16 v[82:85], a[120:123], a[188:191], v[82:85]\n\t"
"v_cvt_pk_bf16_f32 v230, v232, v233\n\t"
"v_cvt_pk_bf16_f32 v231, v234, v235\n\t"
"v_mfma_f32_16x16x32_bf16 v[82:85], a[128:131], a[192:195], v[82:85]\n\t"
"v_mfma_f32_16x16x32_bf16 v[86:89], v[200:203], a[188:191], 0\n\t"
"ds_read_b128 a[188:191], v244 offset:36352\n\t"
"v_mfma_f32_16x16x32_bf16 v[86:89], a[120:123], a[192:195], v[86:89]\n\t"
"v_mfma_f32_16x16x32_bf16 v[86:89], a[128:131], a[196:199], v[86:89]\n\t"
"v_mfma_f32_16x16x32_bf16 v[90:93], v[200:203], a[192:195], 0\n\t"
"ds_read_b128 a[192:195], v244 offset:36864\n\t"
"v_mfma_f32_16x16x32_bf16 v[90:93], a[120:123], a[196:199], v[90:93]\n\t"
"v_mfma_f32_16x16x32_bf16 v[90:93], a[128:131], a[200:203], v[90:93]\n\t"
"buffer_load_dwordx4 a[112:115], v252, s[8:11], s20 offen offset:1024 sc1\n\t"
"v_mfma_f32_16x16x32_bf16 v[94:97], v[200:203], a[196:199], 0\n\t"
"s_waitcnt vmcnt(37) lgkmcnt(5)\n\t"
"ds_read_b128 a[196:199], v244 offset:37376\n\t"
"v_mfma_f32_16x16x32_bf16 v[94:97], a[120:123], a[200:203], v[94:97]\n\t"
"ds_read_b128 a[200:203], v244 offset:37888\n\t"
"v_mfma_f32_16x16x32_bf16 v[94:97], a[128:131], a[204:207], v[94:97]\n\t"
"ds_read_b128 a[204:207], v244 offset:38400\n\t"
"v_mfma_f32_16x16x32_bf16 v[100:103], v[208:211], a[168:171], 0\n\t"
"buffer_load_dwordx4 v[200:203], v252, s[8:11], s20 offen offset:2048 sc1\n\t"
"v_mfma_f32_16x16x32_bf16 v[100:103], v[216:219], a[172:175], v[100:103]\n\t"
"s_waitcnt lgkmcnt(6)\n\t"
"v_mfma_f32_16x16x32_bf16 v[100:103], a[4:7], a[176:179], v[100:103]\n\t"
"ds_read_b64_tr_b16 v[236:237], v240 offset:40960\n\t"
"v_mfma_f32_16x16x32_bf16 v[104:107], v[208:211], a[172:175], 0\n\t"
"s_waitcnt lgkmcnt(6)\n\t"
"ds_read_b64_tr_b16 v[238:239], v240 offset:42496\n\t"
"v_mfma_f32_16x16x32_bf16 v[104:107], v[216:219], a[176:179], v[104:107]\n\t"
"ds_read_b64_tr_b16 v[232:233], v241 offset:40960\n\t"
"v_mfma_f32_16x16x32_bf16 v[104:107], a[4:7], a[180:183], v[104:107]\n\t"
"s_waitcnt lgkmcnt(3)\n\t"
"s_barrier\n\t"
"v_mfma_f32_16x16x32_bf16 v[108:111], v[208:211], a[176:179], 0\n\t"
"s_add_u32 s29, s23, 0\n\t"
"ds_read_b64_tr_b16 v[234:235], v241 offset:42496\n\t"
"v_mfma_f32_16x16x32_bf16 v[108:111], v[216:219], a[180:183], v[108:111]\n\t"
"v_add_u32 v240, 59904, v240\n\t"
"v_add_u32 v241, 59904, v241\n\t"
"v_mfma_f32_16x16x32_bf16 v[108:111], a[4:7], a[184:187], v[108:111]\n\t"
"Lpg14_%=:\n\t"
"s_cbranch_vccnz Lvr1_%=\n\t"
"ds_write_b128 v242, v[224:227] offset:24576\n\t"
"v_mfma_f32_16x16x32_bf16 v[112:115], v[208:211], a[180:183], 0\n\t"
"v_mfma_f32_16x16x32_bf16 v[112:115], v[216:219], a[184:187], v[112:115]\n\t"
"ds_read_b64_tr_b16 v[224:225], v240 offset:1024\n\t"
"v_mfma_f32_16x16x32_bf16 v[112:115], a[4:7], a[188:191], v[112:115]\n\t"
"ds_write_b128 v242, v[228:231] offset:26112\n\t"
"v_mfma_f32_16x16x32_bf16 v[116:119], v[208:211], a[184:187], 0\n\t"
"v_mfma_f32_16x16x32_bf16 v[116:119], v[216:219], a[188:191], v[116:119]\n\t"
"ds_read_b64_tr_b16 v[226:227], v240 offset:2560\n\t"
"v_mfma_f32_16x16x32_bf16 v[116:119], a[4:7], a[192:195], v[116:119]\n\t"
"buffer_store_dwordx4 v[196:199], v248, s[16:19], s29 offen offset:0 nt\n\t"
"ds_read_b64_tr_b16 v[228:229], v241 offset:1024\n\t"
"ds_read_b64_tr_b16 v[230:231], v241 offset:2560\n\t"
"v_mfma_f32_16x16x32_bf16 v[120:123], v[208:211], a[188:191], 0\n\t"
"s_waitcnt lgkmcnt(8)\n\t"
"v_mfma_f32_16x16x32_bf16 v[120:123], v[216:219], a[192:195], v[120:123]\n\t"
"s_waitcnt lgkmcnt(6)\n\t"
"v_mfma_f32_16x16x32_bf16 v[120:123], a[4:7], a[196:199], v[120:123]\n\t"
"s_mov_b32 s20, 110592\n\t"
"v_mfma_f32_16x16x32_bf16 v[236:239], v[236:239], a[136:139], 0\n\t"
"v_mfma_f32_16x16x32_bf16 v[232:235], v[232:235], a[136:139], 0\n\t"
"v_mfma_f32_16x16x32_bf16 v[124:127], v[208:211], a[192:195], 0\n\t"
"v_mfma_f32_16x16x32_bf16 v[124:127], v[216:219], a[196:199], v[124:127]\n\t"
"v_mfma_f32_16x16x32_bf16 v[124:127], a[4:7], a[200:203], v[124:127]\n\t"
"v_mfma_f32_16x16x32_bf16 v[128:131], v[208:211], a[196:199], 0\n\t"
"buffer_load_dwordx4 v[208:211], v252, s[8:11], s20 offen offset:0 sc1\n\t"
"v_mfma_f32_16x16x32_bf16 v[128:131], v[216:219], a[200:203], v[128:131]\n\t"
"s_waitcnt lgkmcnt(0)\n\t"
"v_cvt_pk_bf16_f32 v236, v236, v237\n\t"
"v_cvt_pk_bf16_f32 v237, v238, v239\n\t"
"v_mfma_f32_16x16x32_bf16 v[128:131], a[4:7], a[204:207], v[128:131]\n\t"
"s_waitcnt vmcnt(35)\n\t"
"v_cvt_pk_bf16_f32 v238, v232, v233\n\t"
"v_cvt_pk_bf16_f32 v239, v234, v235\n\t"
"v_mfma_f32_16x16x32_bf16 v[224:227], v[224:227], a[136:139], 0\n\t"
"ds_write_b128 v242, v[236:239] offset:27648\n\t"
"v_mfma_f32_16x16x32_bf16 v[228:231], v[228:231], a[136:139], 0\n\t"
"buffer_load_dwordx4 v[216:219], v252, s[8:11], s20 offen offset:1024 sc1\n\t"
"v_mfma_f32_16x16x32_bf16 v[132:135], a[12:15], a[168:171], 0\n\t"
"ds_read_b64_tr_b16 v[232:233], v240 offset:20992\n\t"
"v_mfma_f32_16x16x32_bf16 v[132:135], a[208:211], a[172:175], v[132:135]\n\t"
"ds_read_b64_tr_b16 v[234:235], v240 offset:22528\n\t"
"v_mfma_f32_16x16x32_bf16 v[132:135], a[212:215], a[176:179], v[132:135]\n\t"
"buffer_store_dwordx4 v[204:207], v248, s[16:19], s29 offen offset:128 nt\n\t"
"buffer_load_dwordx4 a[4:7], v252, s[8:11], s20 offen offset:2048 sc1\n\t"
"ds_read_b64_tr_b16 v[236:237], v241 offset:20992\n\t"
"v_mfma_f32_16x16x32_bf16 v[136:139], a[12:15], a[172:175], 0\n\t"
"ds_read_b64_tr_b16 v[238:239], v241 offset:22528\n\t"
"v_cvt_pk_bf16_f32 v224, v224, v225\n\t"
"v_mfma_f32_16x16x32_bf16 v[136:139], a[208:211], a[176:179], v[136:139]\n\t"
"v_cvt_pk_bf16_f32 v225, v226, v227\n\t"
"v_cvt_pk_bf16_f32 v226, v228, v229\n\t"
"v_mfma_f32_16x16x32_bf16 v[136:139], a[212:215], a[180:183], v[136:139]\n\t"
"v_cvt_pk_bf16_f32 v227, v230, v231\n\t"
"v_mfma_f32_16x16x32_bf16 v[140:143], a[12:15], a[176:179], 0\n\t"
"ds_read_b64_tr_b16 v[228:229], v240 offset:40960\n\t"
"v_mfma_f32_16x16x32_bf16 v[140:143], a[208:211], a[180:183], v[140:143]\n\t"
"ds_read_b64_tr_b16 v[230:231], v240 offset:42496\n\t"
"v_mfma_f32_16x16x32_bf16 v[140:143], a[212:215], a[184:187], v[140:143]\n\t"
"ds_write_b128 v242, v[224:227] offset:29184\n\t"
"v_mfma_f32_16x16x32_bf16 v[144:147], a[12:15], a[180:183], 0\n\t"
"s_waitcnt lgkmcnt(5)\n\t"
"v_mfma_f32_16x16x32_bf16 v[144:147], a[208:211], a[184:187], v[144:147]\n\t"
"s_waitcnt lgkmcnt(3)\n\t"
"ds_read_b64_tr_b16 v[224:225], v241 offset:40960\n\t"
"v_mfma_f32_16x16x32_bf16 v[144:147], a[212:215], a[188:191], v[144:147]\n\t"
"ds_read_b64_tr_b16 v[226:227], v241 offset:42496\n\t"
"v_mfma_f32_16x16x32_bf16 v[232:235], v[232:235], a[136:139], 0\n\t"
"buffer_store_dwordx4 v[212:215], v248, s[16:19], s29 offen offset:256 nt\n\t"
"v_mfma_f32_16x16x32_bf16 v[236:239], v[236:239], a[136:139], 0\n\t"
"v_mfma_f32_16x16x32_bf16 v[148:151], a[12:15], a[184:187], 0\n\t"
"v_mfma_f32_16x16x32_bf16 v[148:151], a[208:211], a[188:191], v[148:151]\n\t"
"v_mfma_f32_16x16x32_bf16 v[148:151], a[212:215], a[192:195], v[148:151]\n\t"
"s_add_u32 s29, s23, 393216\n\t"
"v_mfma_f32_16x16x32_bf16 v[152:155], a[12:15], a[188:191], 0\n\t"
"s_waitcnt lgkmcnt(3)\n\t"
"v_cvt_pk_bf16_f32 v232, v232, v233\n\t"
"v_cvt_pk_bf16_f32 v233, v234, v235\n\t"
"v_mfma_f32_16x16x32_bf16 v[152:155], a[208:211], a[192:195], v[152:155]\n\t"
"s_waitcnt lgkmcnt(0)\n\t"
"ds_read_b128 v[196:199], v252 offset:0\n\t"
"v_cvt_pk_bf16_f32 v234, v236, v237\n\t"
"v_mfma_f32_16x16x32_bf16 v[152:155], a[212:215], a[196:199], v[152:155]\n\t"
"ds_read_b128 a[16:19], v252 offset:12288\n\t"
"v_cvt_pk_bf16_f32 v235, v238, v239\n\t"
"v_mfma_f32_16x16x32_bf16 v[228:231], v[228:231], a[136:139], 0\n\t"
"s_mov_b32 s20, 122880\n\t"
"ds_write_b128 v242, v[232:235] offset:30720\n\t"
"v_mfma_f32_16x16x32_bf16 v[224:227], v[224:227], a[136:139], 0\n\t"
"v_mfma_f32_16x16x32_bf16 v[156:159], a[12:15], a[192:195], 0\n\t"
"ds_read_b128 v[204:207], v252 offset:1024\n\t"
"v_mfma_f32_16x16x32_bf16 v[156:159], a[208:211], a[196:199], v[156:159]\n\t"
"ds_read_b128 a[24:27], v252 offset:13312\n\t"
"v_mfma_f32_16x16x32_bf16 v[156:159], a[212:215], a[200:203], v[156:159]\n\t"
"buffer_store_dwordx4 a[0:3], v248, s[16:19], s29 offen offset:0 nt\n\t"
"ds_read_b128 v[212:215], v252 offset:2048\n\t"
"ds_read_b128 a[32:35], v252 offset:14336\n\t"
"v_mfma_f32_16x16x32_bf16 v[160:163], a[12:15], a[196:199], 0\n\t"
"s_waitcnt vmcnt(35)\n\t"
"v_cvt_pk_bf16_f32 v228, v228, v229\n\t"
"v_cvt_pk_bf16_f32 v229, v230, v231\n\t"
"v_mfma_f32_16x16x32_bf16 v[160:163], a[208:211], a[200:203], v[160:163]\n\t"
"v_cvt_pk_bf16_f32 v230, v224, v225\n\t"
"v_cvt_pk_bf16_f32 v231, v226, v227\n\t"
"v_mfma_f32_16x16x32_bf16 v[160:163], a[212:215], a[204:207], v[160:163]\n\t"
"ds_write_b128 v242, v[228:231] offset:32256\n\t"
"v_mfma_f32_16x16x32_bf16 v[164:167], a[216:219], a[168:171], 0\n\t"
"buffer_load_dwordx4 a[208:211], v252, s[8:11], s20 offen offset:0 sc1\n\t"
"v_mfma_f32_16x16x32_bf16 v[164:167], a[220:223], a[172:175], v[164:167]\n\t"
"buffer_load_dwordx4 a[212:215], v252, s[8:11], s20 offen offset:1024 sc1\n\t"
"v_mfma_f32_16x16x32_bf16 v[164:167], a[224:227], a[176:179], v[164:167]\n\t"
"v_mfma_f32_16x16x32_bf16 v[168:171], a[216:219], a[172:175], 0\n\t"
"v_mfma_f32_16x16x32_bf16 v[168:171], a[220:223], a[176:179], v[168:171]\n\t"
"v_mfma_f32_16x16x32_bf16 v[168:171], a[224:227], a[180:183], v[168:171]\n\t"
"v_mfma_f32_16x16x32_bf16 v[172:175], a[216:219], a[176:179], 0\n\t"
"buffer_load_dwordx4 v[240:243], v252, s[8:11], s20 offen offset:2048 sc1\n\t"
"v_mfma_f32_16x16x32_bf16 v[172:175], a[220:223], a[180:183], v[172:175]\n\t"
"v_mfma_f32_16x16x32_bf16 v[172:175], a[224:227], a[184:187], v[172:175]\n\t"
"buffer_store_dwordx4 a[8:11], v248, s[16:19], s29 offen offset:128 nt\n\t"
"v_mfma_f32_16x16x32_bf16 v[176:179], a[216:219], a[180:183], 0\n\t"
"v_mfma_f32_16x16x32_bf16 v[176:179], a[220:223], a[184:187], v[176:179]\n\t"
"v_mfma_f32_16x16x32_bf16 v[176:179], a[224:227], a[188:191], v[176:179]\n\t"
"s_waitcnt lgkmcnt(0)\n\t"
"s_barrier\n\t"
"v_mfma_f32_16x16x32_bf16 v[180:183], a[216:219], a[184:187], 0\n\t"
"ds_read_b128 a[168:171], v244 offset:24576\n\t"
"v_mfma_f32_16x16x32_bf16 v[180:183], a[220:223], a[188:191], v[180:183]\n\t"
"ds_read_b128 a[172:175], v244 offset:25088\n\t"
"v_mfma_f32_16x16x32_bf16 v[180:183], a[224:227], a[192:195], v[180:183]\n\t"
"ds_read_b128 a[176:179], v244 offset:25600\n\t"
"v_mfma_f32_16x16x32_bf16 v[184:187], a[216:219], a[188:191], 0\n\t"
"s_mov_b32 s20, 135168\n\t"
"ds_read_b128 a[180:183], v244 offset:26112\n\t"
"v_mfma_f32_16x16x32_bf16 v[184:187], a[220:223], a[192:195], v[184:187]\n\t"
"ds_read_b128 a[184:187], v244 offset:26624\n\t"
"v_mfma_f32_16x16x32_bf16 v[184:187], a[224:227], a[196:199], v[184:187]\n\t"
"ds_read_b128 a[188:191], v244 offset:27136\n\t"
"v_mfma_f32_16x16x32_bf16 v[188:191], a[216:219], a[192:195], 0\n\t"
"ds_read_b128 a[192:195], v244 offset:27648\n\t"
"v_mfma_f32_16x16x32_bf16 v[188:191], a[220:223], a[196:199], v[188:191]\n\t"
"v_mfma_f32_16x16x32_bf16 v[188:191], a[224:227], a[200:203], v[188:191]\n\t"
"ds_write_b128 v249, a[44:47] offset:0\n\t"
"v_mfma_f32_16x16x32_bf16 v[192:195], a[216:219], a[196:199], 0\n\t"
"buffer_load_dwordx4 a[216:219], v252, s[8:11], s20 offen offset:0 sc1\n\t"
"v_mfma_f32_16x16x32_bf16 v[192:195], a[220:223], a[200:203], v[192:195]\n\t"
"s_waitcnt vmcnt(35) lgkmcnt(5)\n\t"
"ds_read_b128 a[196:199], v244 offset:28160\n\t"
"v_mfma_f32_16x16x32_bf16 v[192:195], a[224:227], a[204:207], v[192:195]\n\t"
"ds_write_b128 v249, a[60:63] offset:12288\n\t"
"Lst19_%=:\n\t"
"v_mfma_f32_16x16x32_bf16 v[2:5], a[228:231], a[168:171], v[2:5]\n\t"
"buffer_load_dwordx4 a[220:223], v252, s[8:11], s20 offen offset:1024 sc1\n\t"
"v_mfma_f32_16x16x32_bf16 v[2:5], a[236:239], a[172:175], v[2:5]\n\t"
"s_waitcnt lgkmcnt(6)\n\t"
"ds_read_b128 a[200:203], v244 offset:28672\n\t"
"v_mfma_f32_16x16x32_bf16 v[2:5], a[240:243], a[176:179], v[2:5]\n\t"
"buffer_store_dwordx4 a[40:43], v248, s[16:19], s29 offen offset:256 nt\n\t"
"ds_read_b128 a[204:207], v244 offset:29184\n\t"
"v_mfma_f32_16x16x32_bf16 v[6:9], a[228:231], a[172:175], v[6:9]\n\t"
"s_waitcnt lgkmcnt(7)\n\t"
"s_add_u32 s29, s23, 786432\n\t"
"v_mfma_f32_16x16x32_bf16 v[6:9], a[236:239], a[176:179], v[6:9]\n\t"
"buffer_load_dwordx4 a[224:227], v252, s[8:11], s20 offen offset:2048 sc1\n\t"
"v_mfma_f32_16x16x32_bf16 v[6:9], a[240:243], a[180:183], v[6:9]\n\t"
"ds_write_b64 v250, a[110:111] offset:0\n\t"
"v_mfma_f32_16x16x32_bf16 v[10:13], a[228:231], a[176:179], v[10:13]\n\t"
"s_waitcnt lgkmcnt(7)\n\t"
"v_mfma_f32_16x16x32_bf16 v[10:13], a[236:239], a[180:183], v[10:13]\n\t"
"v_mfma_f32_16x16x32_bf16 v[10:13], a[240:243], a[184:187], v[10:13]\n\t"
"ds_write_b64 v250, a[126:127] offset:12288\n\t"
"v_mfma_f32_16x16x32_bf16 v[14:17], a[228:231], a[180:183], v[14:17]\n\t"
"s_waitcnt lgkmcnt(7)\n\t"
"v_mfma_f32_16x16x32_bf16 v[14:17], a[236:239], a[184:187], v[14:17]\n\t"
"v_mfma_f32_16x16x32_bf16 v[14:17], a[240:243], a[188:191], v[14:17]\n\t"
"buffer_store_dwordx4 v[196:199], v248, s[16:19], s29 offen offset:0 nt\n\t"
"ds_write_b64 v250, a[118:119] offset:6144\n\t"
"v_mfma_f32_16x16x32_bf16 v[18:21], a[228:231], a[184:187], v[18:21]\n\t"
"s_waitcnt lgkmcnt(6)\n\t"
"v_mfma_f32_16x16x32_bf16 v[18:21], a[236:239], a[188:191], v[18:21]\n\t"
"v_mfma_f32_16x16x32_bf16 v[18:21], a[240:243], a[192:195], v[18:21]\n\t"
"ds_write_b128 v249, a[52:55] offset:6144\n\t"
"v_mfma_f32_16x16x32_bf16 v[22:25], a[228:231], a[188:191], v[22:25]\n\t"
"s_waitcnt lgkmcnt(5)\n\t"
"s_mov_b32 s20, 147456\n\t"
"v_mfma_f32_16x16x32_bf16 v[22:25], a[236:239], a[192:195], v[22:25]\n\t"
"v_mfma_f32_16x16x32_bf16 v[22:25], a[240:243], a[196:199], v[22:25]\n\t"
"ds_write_b128 v249, a[68:71] offset:18432\n\t"
"v_mfma_f32_16x16x32_bf16 v[26:29], a[228:231], a[192:195], v[26:29]\n\t"
"s_waitcnt lgkmcnt(5)\n\t"
"v_mfma_f32_16x16x32_bf16 v[26:29], a[236:239], a[196:199], v[26:29]\n\t"
"v_mfma_f32_16x16x32_bf16 v[26:29], a[240:243], a[200:203], v[26:29]\n\t"
"ds_write_b64 v250, a[134:135] offset:18432\n\t"
"v_mfma_f32_16x16x32_bf16 v[30:33], a[228:231], a[196:199], v[30:33]\n\t"
"buffer_load_dwordx4 a[228:231], v252, s[8:11], s20 offen offset:0 sc1\n\t"
"v_mfma_f32_16x16x32_bf16 v[30:33], a[236:239], a[200:203], v[30:33]\n\t"
"s_waitcnt vmcnt(36)\n\t"
"v_mfma_f32_16x16x32_bf16 v[30:33], a[240:243], a[204:207], v[30:33]\n\t"
"buffer_store_dwordx4 v[204:207], v248, s[16:19], s29 offen offset:128 nt\n\t"
"v_mfma_f32_16x16x32_bf16 v[34:37], a[244:247], a[168:171], v[34:37]\n\t"
"v_mfma_f32_16x16x32_bf16 v[34:37], a[248:251], a[172:175], v[34:37]\n\t"
"buffer_load_dwordx4 a[236:239], v252, s[8:11], s20 offen offset:1024 sc1\n\t"
"v_mfma_f32_16x16x32_bf16 v[34:37], a[48:51], a[176:179], v[34:37]\n\t"
"ds_read_b64_tr_b16 v[236:237], v246 offset:512\n\t"
"v_mfma_f32_16x16x32_bf16 v[38:41], a[244:247], a[172:175], v[38:41]\n\t"
"buffer_load_dwordx4 a[240:243], v252, s[8:11], s20 offen offset:2048 sc1\n\t"
"v_mfma_f32_16x16x32_bf16 v[38:41], a[248:251], a[176:179], v[38:41]\n\t"
"ds_read_b64_tr_b16 v[238:239], v246 offset:3584\n\t"
"v_mfma_f32_16x16x32_bf16 v[38:41], a[48:51], a[180:183], v[38:41]\n\t"
"v_mfma_f32_16x16x32_bf16 v[42:45], a[244:247], a[176:179], v[42:45]\n\t"
"v_mfma_f32_16x16x32_bf16 v[42:45], a[248:251], a[180:183], v[42:45]\n\t"
"v_mfma_f32_16x16x32_bf16 v[42:45], a[48:51], a[184:187], v[42:45]\n\t"
"v_mfma_f32_16x16x32_bf16 v[46:49], a[244:247], a[180:183], v[46:49]\n\t"
"ds_read_b64_tr_b16 v[232:233], v246 offset:6656\n\t"
"v_mfma_f32_16x16x32_bf16 v[46:49], a[248:251], a[184:187], v[46:49]\n\t"
"ds_read_b64_tr_b16 v[234:235], v246 offset:9728\n\t"
"v_mfma_f32_16x16x32_bf16 v[46:49], a[48:51], a[188:191], v[46:49]\n\t"
"s_waitcnt lgkmcnt(4)\n\t"
"s_barrier\n\t"
"v_mfma_f32_16x16x32_bf16 v[50:53], a[244:247], a[184:187], v[50:53]\n\t"
"v_mfma_f32_16x16x32_bf16 v[50:53], a[248:251], a[188:191], v[50:53]\n\t"
"v_mfma_f32_16x16x32_bf16 v[50:53], a[48:51], a[192:195], v[50:53]\n\t"
"buffer_store_dwordx4 v[212:215], v248, s[16:19], s29 offen offset:256 nt\n\t"
"v_mfma_f32_16x16x32_bf16 v[54:57], a[244:247], a[188:191], v[54:57]\n\t"
"s_waitcnt lgkmcnt(2)\n\t"
"v_mfma_f32_16x16x32_bf16 v[54:57], a[248:251], a[192:195], v[54:57]\n\t"
"s_mov_b32 s20, 159744\n\t"
"v_mfma_f32_16x16x32_bf16 v[54:57], a[48:51], a[196:199], v[54:57]\n\t"
"v_mfma_f32_16x16x32_bf16 v[236:239], v[236:239], v[220:223], 0\n\t"
"s_add_u32 s29, s23, 1179648\n\t"
"v_mfma_f32_16x16x32_bf16 v[58:61], a[244:247], a[192:195], v[58:61]\n\t"
"ds_read_b64_tr_b16 v[224:225], v246 offset:12800\n\t"
"v_mfma_f32_16x16x32_bf16 v[58:61], a[248:251], a[196:199], v[58:61]\n\t"
"ds_read_b64_tr_b16 v[226:227], v246 offset:15872\n\t"
"v_mfma_f32_16x16x32_bf16 v[58:61], a[48:51], a[200:203], v[58:61]\n\t"
"v_mfma_f32_16x16x32_bf16 v[62:65], a[244:247], a[196:199], v[62:65]\n\t"
"buffer_load_dwordx4 a[244:247], v252, s[8:11], s20 offen offset:0 sc1\n\t"
"v_mfma_f32_16x16x32_bf16 v[62:65], a[248:251], a[200:203], v[62:65]\n\t"
"s_waitcnt vmcnt(37) lgkmcnt(2)\n\t"
"v_cvt_pk_bf16_f32 v236, v236, v237\n\t"
"v_cvt_pk_bf16_f32 v237, v238, v239\n\t"
"v_mfma_f32_16x16x32_bf16 v[62:65], a[48:51], a[204:207], v[62:65]\n\t"
"v_mfma_f32_16x16x32_bf16 v[232:235], v[232:235], v[220:223], 0\n\t"
"buffer_load_dwordx4 a[248:251], v252, s[8:11], s20 offen offset:1024 sc1\n\t"
"v_mfma_f32_16x16x32_bf16 v[66:69], a[56:59], a[168:171], v[66:69]\n\t"
"ds_read_b64_tr_b16 v[228:229], v246 offset:20480\n\t"
"v_mfma_f32_16x16x32_bf16 v[66:69], a[64:67], a[172:175], v[66:69]\n\t"
"ds_read_b64_tr_b16 v[230:231], v246 offset:23552\n\t"
"v_mfma_f32_16x16x32_bf16 v[66:69], a[144:147], a[176:179], v[66:69]\n\t"
"buffer_store_dwordx4 a[16:19], v248, s[16:19], s29 offen offset:0 nt\n\t"
"ds_read_b128 a[168:171], v244 offset:33792\n\t"
"v_mfma_f32_16x16x32_bf16 v[70:73], a[56:59], a[172:175], v[70:73]\n\t"
"ds_read_b128 a[172:175], v244 offset:34304\n\t"
"v_mfma_f32_16x16x32_bf16 v[70:73], a[64:67], a[176:179], v[70:73]\n\t"
"v_cvt_pk_bf16_f32 v232, v232, v233\n\t"
"v_cvt_pk_bf16_f32 v233, v234, v235\n\t"
"v_mfma_f32_16x16x32_bf16 v[70:73], a[144:147], a[180:183], v[70:73]\n\t"
"v_mfma_f32_16x16x32_bf16 v[74:77], a[56:59], a[176:179], v[74:77]\n\t"
"s_waitcnt lgkmcnt(4)\n\t"
"ds_read_b128 a[176:179], v244 offset:34816\n\t"
"v_mfma_f32_16x16x32_bf16 v[74:77], a[64:67], a[180:183], v[74:77]\n\t"
"v_mfma_f32_16x16x32_bf16 v[74:77], a[144:147], a[184:187], v[74:77]\n\t"
"v_mfma_f32_16x16x32_bf16 v[224:227], v[224:227], v[220:223], 0\n\t"
"v_mfma_f32_16x16x32_bf16 v[78:81], a[56:59], a[180:183], v[78:81]\n\t"
"ds_read_b128 a[180:183], v244 offset:35328\n\t"
"v_mfma_f32_16x16x32_bf16 v[78:81], a[64:67], a[184:187], v[78:81]\n\t"
"ds_read_b128 v[196:199], v252 offset:0\n\t"
"v_mfma_f32_16x16x32_bf16 v[78:81], a[144:147], a[188:191], v[78:81]\n\t"
"buffer_store_dwordx4 a[24:27], v248, s[16:19], s29 offen offset:128 nt\n\t"
"ds_read_b128 a[40:43], v252 offset:12288\n\t"
"ds_read_b128 v[204:207], v252 offset:1024\n\t"
"v_mfma_f32_16x16x32_bf16 v[82:85], a[56:59], a[184:187], v[82:85]\n\t"
"s_waitcnt lgkmcnt(7)\n\t"
"ds_read_b128 a[184:187], v244 offset:35840\n\t"
"v_mfma_f32_16x16x32_bf16 v[82:85], a[64:67], a[188:191], v[82:85]\n\t"
"ds_read_b128 a[72:75], v252 offset:13312\n\t"
"v_cvt_pk_bf16_f32 v224, v224, v225\n\t"
"v_mfma_f32_16x16x32_bf16 v[82:85], a[144:147], a[192:195], v[82:85]\n\t"
"ds_read_b128 a[8:11], v252 offset:2048\n\t"
"v_cvt_pk_bf16_f32 v225, v226, v227\n\t"
"v_mfma_f32_16x16x32_bf16 v[228:231], v[228:231], v[220:223], 0\n\t"
"ds_read_b128 a[104:107], v252 offset:14336\n\t"
"v_mfma_f32_16x16x32_bf16 v[86:89], a[56:59], a[188:191], v[86:89]\n\t"
"ds_read_b128 a[188:191], v244 offset:36352\n\t"
"v_mfma_f32_16x16x32_bf16 v[86:89], a[64:67], a[192:195], v[86:89]\n\t"
"v_mfma_f32_16x16x32_bf16 v[86:89], a[144:147], a[196:199], v[86:89]\n\t"
"v_mfma_f32_16x16x32_bf16 v[90:93], a[56:59], a[192:195], v[90:93]\n\t"
"ds_read_b128 a[192:195], v244 offset:36864\n\t"
"v_mfma_f32_16x16x32_bf16 v[90:93], a[64:67], a[196:199], v[90:93]\n\t"
"v_cvt_pk_bf16_f32 v228, v228, v229\n\t"
"v_cvt_pk_bf16_f32 v229, v230, v231\n\t"
"v_mfma_f32_16x16x32_bf16 v[90:93], a[144:147], a[200:203], v[90:93]\n\t"
"v_mfma_f32_16x16x32_bf16 v[94:97], a[56:59], a[196:199], v[94:97]\n\t"
"s_waitcnt vmcnt(35) lgkmcnt(10)\n\t"
"ds_read_b128 a[196:199], v244 offset:37376\n\t"
"v_mfma_f32_16x16x32_bf16 v[94:97], a[64:67], a[200:203], v[94:97]\n\t"
"ds_read_b128 a[200:203], v244 offset:37888\n\t"
"v_mfma_f32_16x16x32_bf16 v[94:97], a[144:147], a[204:207], v[94:97]\n\t"
"ds_read_b128 a[204:207], v244 offset:38400\n\t"
"v_mfma_f32_16x16x32_bf16 v[100:103], a[20:23], a[168:171], v[100:103]\n\t"
"s_waitcnt lgkmcnt(12)\n\t"
"v_mfma_f32_16x16x32_bf16 v[100:103], a[252:255], a[172:175], v[100:103]\n\t"
"v_mfma_f32_16x16x32_bf16 v[100:103], a[232:235], a[176:179], v[100:103]\n\t"
"buffer_store_dwordx4 a[32:35], v248, s[16:19], s29 offen offset:256 nt\n\t"
"v_mfma_f32_16x16x32_bf16 v[104:107], a[20:23], a[172:175], v[104:107]\n\t"
"s_waitcnt lgkmcnt(8)\n\t"
"v_mfma_f32_16x16x32_bf16 v[104:107], a[252:255], a[176:179], v[104:107]\n\t"
"v_mfma_f32_16x16x32_bf16 v[104:107], a[232:235], a[180:183], v[104:107]\n\t"
"s_waitcnt lgkmcnt(0)\n\t"
"s_barrier\n\t"
"v_mfma_f32_16x16x32_bf16 v[108:111], a[20:23], a[176:179], v[108:111]\n\t"
"s_add_u32 s29, s23, 1152\n\t"
"v_fma_f32 v0, v2, s52, v100\n\t"
"v_mfma_f32_16x16x32_bf16 v[108:111], a[252:255], a[180:183], v[108:111]\n\t"
"v_fma_f32 v1, v3, s52, v101\n\t"
"v_mfma_f32_16x16x32_bf16 v[108:111], a[232:235], a[184:187], v[108:111]\n\t"
"ds_write_b64 v245, v[236:237] offset:24576\n\t"
"v_mfma_f32_16x16x32_bf16 v[112:115], a[20:23], a[180:183], v[112:115]\n\t"
"ds_read_b64_tr_b16 v[236:237], v246 offset:26624\n\t"
"v_mfma_f32_16x16x32_bf16 v[112:115], a[252:255], a[184:187], v[112:115]\n\t"
"ds_read_b64_tr_b16 v[238:239], v246 offset:29696\n\t"
"v_mfma_f32_16x16x32_bf16 v[112:115], a[232:235], a[188:191], v[112:115]\n\t"
"ds_write_b64 v245, v[232:233] offset:25088\n\t"
"v_mfma_f32_16x16x32_bf16 v[116:119], a[20:23], a[184:187], v[116:119]\n\t"
"ds_read_b64_tr_b16 v[232:233], v246 offset:32768\n\t"
"v_fma_f32 v212, -v10, s56, -v108\n\t"
"v_mfma_f32_16x16x32_bf16 v[116:119], a[252:255], a[188:191], v[116:119]\n\t"
"ds_read_b64_tr_b16 v[234:235], v246 offset:35840\n\t"
"v_fma_f32 v213, -v11, s56, -v109\n\t"
"v_mfma_f32_16x16x32_bf16 v[116:119], a[232:235], a[192:195], v[116:119]\n\t"
"ds_write_b64 v245, v[224:225] offset:25600\n\t"
"v_mfma_f32_16x16x32_bf16 v[120:123], a[20:23], a[188:191], v[120:123]\n\t"
"s_waitcnt lgkmcnt(4)\n\t"
"ds_read_b64_tr_b16 v[224:225], v246 offset:40448\n\t"
"v_fma_f32 v214, -v12, s56, -v110\n\t"
"v_mfma_f32_16x16x32_bf16 v[120:123], a[252:255], a[192:195], v[120:123]\n\t"
"ds_read_b64_tr_b16 v[226:227], v246 offset:43520\n\t"
"v_fma_f32 v215, -v13, s56, -v111\n\t"
"v_mfma_f32_16x16x32_bf16 v[120:123], a[232:235], a[196:199], v[120:123]\n\t"
"ds_write_b64 v245, v[228:229] offset:26112\n\t"
"v_mfma_f32_16x16x32_bf16 v[236:239], v[236:239], v[220:223], 0\n\t"
"buffer_store_dwordx4 v[196:199], v248, s[16:19], s29 offen offset:0 nt\n\t"
"ds_read_b64_tr_b16 v[228:229], v246 offset:46592\n\t"
"ds_read_b64_tr_b16 v[230:231], v246 offset:49664\n\t"
"v_fma_f32 v98, -v14, s56, -v112\n\t"
"v_mfma_f32_16x16x32_bf16 v[124:127], a[20:23], a[192:195], v[124:127]\n\t"
"s_waitcnt lgkmcnt(6)\n\t"
"v_fma_f32 v99, -v15, s56, -v113\n\t"
"v_accvgpr_write_b32 a0, v98\n\t"
"v_mfma_f32_16x16x32_bf16 v[124:127], a[252:255], a[196:199], v[124:127]\n\t"
"v_fma_f32 v196, -v2, s56, -v100\n\t"
"v_fma_f32 v197, -v3, s56, -v101\n\t"
"v_mfma_f32_16x16x32_bf16 v[124:127], a[232:235], a[200:203], v[124:127]\n\t"
"ds_write_b128 v249, a[76:79] offset:0\n\t"
"v_mfma_f32_16x16x32_bf16 v[232:235], v[232:235], v[220:223], 0\n\t"
"ds_write_b128 v249, a[92:95] offset:12288\n\t"
"v_mfma_f32_16x16x32_bf16 v[128:131], a[20:23], a[196:199], v[128:131]\n\t"
"s_waitcnt lgkmcnt(5)\n\t"
"v_cvt_pk_bf16_f32 v236, v236, v237\n\t"
"v_cvt_pk_bf16_f32 v237, v238, v239\n\t"
"v_mfma_f32_16x16x32_bf16 v[128:131], a[252:255], a[200:203], v[128:131]\n\t"
"s_waitcnt vmcnt(32)\n\t"
"v_fma_f32 v198, -v4, s56, -v102\n\t"
"v_fma_f32 v199, -v5, s56, -v103\n\t"
"v_mfma_f32_16x16x32_bf16 v[128:131], a[232:235], a[204:207], v[128:131]\n\t"
"ds_write_b64 v250, a[142:143] offset:0\n\t"
"v_mfma_f32_16x16x32_bf16 v[224:227], v[224:227], v[220:223], 0\n\t"
"buffer_load_dwordx4 a[252:255], v252, s[8:11], s20 offen offset:2048 sc1\n\t"
"s_mov_b32 s20, 172032\n\t"
"v_mfma_f32_16x16x32_bf16 v[132:135], a[152:155], a[168:171], v[132:135]\n\t"
"buffer_load_dwordx4 a[232:235], v252, s[8:11], s20 offen offset:0 sc1\n\t"
"v_mfma_f32_16x16x32_bf16 v[132:135], a[28:31], a[172:175], v[132:135]\n\t"
"s_waitcnt lgkmcnt(3)\n\t"
"v_cvt_pk_bf16_f32 v232, v232, v233\n\t"
"v_cvt_pk_bf16_f32 v233, v234, v235\n\t"
"v_mfma_f32_16x16x32_bf16 v[132:135], a[160:163], a[176:179], v[132:135]\n\t"
"ds_write_b64 v250, a[158:159] offset:12288\n\t"
"v_mfma_f32_16x16x32_bf16 v[228:231], v[228:231], v[220:223], 0\n\t"
"ds_write_b64 v245, v[236:237] offset:26624\n\t"
"v_mfma_f32_16x16x32_bf16 v[136:139], a[152:155], a[172:175], v[136:139]\n\t"
"ds_read_b64_tr_b16 v[236:237], v246 offset:52736\n\t"
"v_cvt_pk_bf16_f32 v224, v224, v225\n\t"
"v_mfma_f32_16x16x32_bf16 v[136:139], a[28:31], a[176:179], v[136:139]\n\t"
"ds_read_b64_tr_b16 v[238:239], v246 offset:55808\n\t"
"v_cvt_pk_bf16_f32 v225, v226, v227\n\t"
"v_mfma_f32_16x16x32_bf16 v[136:139], a[160:163], a[180:183], v[136:139]\n\t"
"buffer_store_dwordx4 v[204:207], v248, s[16:19], s29 offen offset:128 nt\n\t"
"ds_write_b64 v245, v[232:233] offset:27136\n\t"
"v_fma_f32 v98, -v16, s56, -v114\n\t"
"v_mfma_f32_16x16x32_bf16 v[140:143], a[152:155], a[176:179], v[140:143]\n\t"
"v_fma_f32 v204, -v6, s56, -v104\n\t"
"v_fma_f32 v205, -v7, s56, -v105\n\t"
"v_mfma_f32_16x16x32_bf16 v[140:143], a[28:31], a[180:183], v[140:143]\n\t"
"v_cvt_pk_bf16_f32 v228, v228, v229\n\t"
"v_cvt_pk_bf16_f32 v229, v230, v231\n\t"
"v_mfma_f32_16x16x32_bf16 v[140:143], a[160:163], a[184:187], v[140:143]\n\t"
"ds_write_b64 v245, v[224:225] offset:27648\n\t"
"v_mfma_f32_16x16x32_bf16 v[144:147], a[152:155], a[180:183], v[144:147]\n\t"
"s_waitcnt lgkmcnt(2)\n\t"
"v_fma_f32 v206, -v8, s56, -v106\n\t"
"v_fma_f32 v207, -v9, s56, -v107\n\t"
"v_mfma_f32_16x16x32_bf16 v[144:147], a[28:31], a[184:187], v[144:147]\n\t"
"v_accvgpr_write_b32 a1, v99\n\t"
"v_fma_f32 v99, -v17, s56, -v115\n\t"
"v_mfma_f32_16x16x32_bf16 v[144:147], a[160:163], a[188:191], v[144:147]\n\t"
"ds_write_b128 v249, a[84:87] offset:6144\n\t"
"v_mfma_f32_16x16x32_bf16 v[236:239], v[236:239], v[220:223], 0\n\t"
"buffer_store_dwordx4 a[8:11], v248, s[16:19], s29 offen offset:256 nt\n\t"
"v_accvgpr_write_b32 a2, v98\n\t"
"v_accvgpr_write_b32 a3, v99\n\t"
"v_mfma_f32_16x16x32_bf16 v[148:151], a[152:155], a[184:187], v[148:151]\n\t"
"v_mfma_f32_16x16x32_bf16 v[148:151], a[28:31], a[188:191], v[148:151]\n\t"
"v_mfma_f32_16x16x32_bf16 v[148:151], a[160:163], a[192:195], v[148:151]\n\t"
"ds_write_b128 v249, a[100:103] offset:18432\n\t"
"v_mfma_f32_16x16x32_bf16 v[152:155], a[152:155], a[188:191], v[152:155]\n\t"
"s_add_u32 s29, s23, 394368\n\t"
"v_mfma_f32_16x16x32_bf16 v[152:155], a[28:31], a[192:195], v[152:155]\n\t"
"v_cvt_pk_bf16_f32 v236, v236, v237\n\t"
"v_cvt_pk_bf16_f32 v237, v238, v239\n\t"
"v_mfma_f32_16x16x32_bf16 v[152:155], a[160:163], a[196:199], v[152:155]\n\t"
"ds_write_b64 v245, v[228:229] offset:28160\n\t"
"v_mfma_f32_16x16x32_bf16 v[156:159], a[152:155], a[192:195], v[156:159]\n\t"
"v_fma_f32 v98, -v18, s56, -v116\n\t"
"v_fma_f32 v99, -v19, s56, -v117\n\t"
"v_mfma_f32_16x16x32_bf16 v[156:159], a[28:31], a[196:199], v[156:159]\n\t"
"v_accvgpr_write_b32 a8, v98\n\t"
"v_fma_f32 v98, -v20, s56, -v118\n\t"
"v_mfma_f32_16x16x32_bf16 v[156:159], a[160:163], a[200:203], v[156:159]\n\t"
"ds_write_b64 v250, a[150:151] offset:6144\n\t"
"v_mfma_f32_16x16x32_bf16 v[160:163], a[152:155], a[196:199], v[160:163]\n\t"
"s_waitcnt vmcnt(31)\n\t"
"v_accvgpr_write_b32 a9, v99\n\t"
"v_fma_f32 v99, -v21, s56, -v119\n\t"
"v_mfma_f32_16x16x32_bf16 v[160:163], a[28:31], a[200:203], v[160:163]\n\t"
"v_accvgpr_write_b32 a10, v98\n\t"
"v_accvgpr_write_b32 a11, v99\n\t"
"v_mfma_f32_16x16x32_bf16 v[160:163], a[160:163], a[204:207], v[160:163]\n\t"
"ds_write_b64 v250, a[166:167] offset:18432\n\t"
"v_mfma_f32_16x16x32_bf16 v[164:167], a[36:39], a[168:171], v[164:167]\n\t"
"v_fma_f32 v98, -v22, s56, -v120\n\t"
"v_fma_f32 v99, -v23, s56, -v121\n\t"
"v_mfma_f32_16x16x32_bf16 v[164:167], a[112:115], a[172:175], v[164:167]\n\t"
"v_accvgpr_write_b32 a16, v98\n\t"
"v_mfma_f32_16x16x32_bf16 v[164:167], v[200:203], a[176:179], v[164:167]\n\t"
"ds_write_b64 v245, v[236:237] offset:28672\n\t"
"s_waitcnt lgkmcnt(0)\n\t"
"s_barrier\n\t"
"v_mfma_f32_16x16x32_bf16 v[168:171], a[36:39], a[172:175], v[168:171]\n\t"
"ds_read_b128 a[168:171], v244 offset:24576\n\t"
"v_mfma_f32_16x16x32_bf16 v[168:171], a[112:115], a[176:179], v[168:171]\n\t"
"buffer_load_dwordx4 v[236:239], v252, s[8:11], s20 offen offset:1024 sc1\n\t"
"v_mfma_f32_16x16x32_bf16 v[168:171], v[200:203], a[180:183], v[168:171]\n\t"
"buffer_store_dwordx4 a[40:43], v248, s[16:19], s29 offen offset:0 nt\n\t"
"ds_read_b128 a[172:175], v244 offset:25088\n\t"
"v_fma_f32 v98, -v24, s56, -v122\n\t"
"v_accvgpr_write_b32 a17, v99\n\t"
"v_fma_f32 v99, -v25, s56, -v123\n\t"
"v_mfma_f32_16x16x32_bf16 v[172:175], a[36:39], a[176:179], v[172:175]\n\t"
"ds_read_b128 a[176:179], v244 offset:25600\n\t"
"v_accvgpr_write_b32 a18, v98\n\t"
"v_mfma_f32_16x16x32_bf16 v[172:175], a[112:115], a[180:183], v[172:175]\n\t"
"v_accvgpr_write_b32 a19, v99\n\t"
"v_fma_f32 v98, -v26, s56, -v124\n\t"
"v_mfma_f32_16x16x32_bf16 v[172:175], v[200:203], a[184:187], v[172:175]\n\t"
"v_fma_f32 v99, -v27, s56, -v125\n\t"
"v_accvgpr_write_b32 a24, v98\n\t"
"v_mfma_f32_16x16x32_bf16 v[176:179], a[36:39], a[180:183], v[176:179]\n\t"
"ds_read_b128 a[180:183], v244 offset:26112\n\t"
"v_fma_f32 v98, -v28, s56, -v126\n\t"
"v_mfma_f32_16x16x32_bf16 v[176:179], a[112:115], a[184:187], v[176:179]\n\t"
"v_accvgpr_write_b32 a25, v99\n\t"
"v_fma_f32 v99, -v29, s56, -v127\n\t"
"v_mfma_f32_16x16x32_bf16 v[176:179], v[200:203], a[188:191], v[176:179]\n\t"
"v_accvgpr_write_b32 a26, v98\n\t"
"v_accvgpr_write_b32 a27, v99\n\t"
"v_mfma_f32_16x16x32_bf16 v[180:183], a[36:39], a[184:187], v[180:183]\n\t"
"ds_read_b128 a[184:187], v244 offset:26624\n\t"
"v_fma_f32 v98, -v30, s56, -v128\n\t"
"v_mfma_f32_16x16x32_bf16 v[180:183], a[112:115], a[188:191], v[180:183]\n\t"
"v_fma_f32 v99, -v31, s56, -v129\n\t"
"v_accvgpr_write_b32 a32, v98\n\t"
"v_mfma_f32_16x16x32_bf16 v[180:183], v[200:203], a[192:195], v[180:183]\n\t"
"v_fma_f32 v98, -v32, s56, -v130\n\t"
"v_accvgpr_write_b32 a33, v99\n\t"
"v_mfma_f32_16x16x32_bf16 v[184:187], a[36:39], a[188:191], v[184:187]\n\t"
"ds_read_b128 a[188:191], v244 offset:27136\n\t"
"v_fma_f32 v99, -v33, s56, -v131\n\t"
"v_mfma_f32_16x16x32_bf16 v[184:187], a[112:115], a[192:195], v[184:187]\n\t"
"v_accvgpr_write_b32 a34, v98\n\t"
"v_accvgpr_write_b32 a35, v99\n\t"
"v_mfma_f32_16x16x32_bf16 v[184:187], v[200:203], a[196:199], v[184:187]\n\t"
"buffer_store_dwordx4 a[72:75], v248, s[16:19], s29 offen offset:128 nt\n\t"
"v_fma_f32 v98, -v34, s56, -v132\n\t"
"v_fma_f32 v99, -v35, s56, -v133\n\t"
"v_accvgpr_write_b32 a40, v98\n\t"
"v_fma_f32 v98, -v36, s56, -v134\n\t"
"v_mfma_f32_16x16x32_bf16 v[188:191], a[36:39], a[192:195], v[188:191]\n\t"
"ds_read_b128 a[192:195], v244 offset:27648\n\t"
"v_accvgpr_write_b32 a41, v99\n\t"
"v_mfma_f32_16x16x32_bf16 v[188:191], a[112:115], a[196:199], v[188:191]\n\t"
"ds_read_b128 a[136:139], v252 offset:0\n\t"
"v_fma_f32 v99, -v37, s56, -v135\n\t"
"v_mfma_f32_16x16x32_bf16 v[188:191], v[200:203], a[200:203], v[188:191]\n\t"
"v_accvgpr_write_b32 a42, v98\n\t"
"v_accvgpr_write_b32 a43, v99\n\t"
"v_mfma_f32_16x16x32_bf16 v[192:195], a[36:39], a[196:199], v[192:195]\n\t"
"s_waitcnt vmcnt(29) lgkmcnt(5)\n\t"
"ds_read_b128 a[196:199], v244 offset:28160\n\t"
"v_fma_f32 v98, -v38, s56, -v136\n\t"
"v_mfma_f32_16x16x32_bf16 v[192:195], a[112:115], a[200:203], v[192:195]\n\t"
"ds_read_b128 a[200:203], v244 offset:28672\n\t"
"v_fma_f32 v99, -v39, s56, -v137\n\t"
"v_mfma_f32_16x16x32_bf16 v[192:195], v[200:203], a[204:207], v[192:195]\n\t"
"ds_read_b128 a[204:207], v244 offset:29184\n\t"
"v_accvgpr_write_b32 a48, v98\n\t"
"Lst20_%=:\n\t"
"v_mfma_f32_16x16x32_bf16 v[196:199], v[208:211], a[168:171], v[196:199]\n\t"
"s_waitcnt lgkmcnt(7)\n\t"
"v_xor_b32 v246, 0xc0, v246\n\t"
"v_fma_f32 v98, -v40, s56, -v138\n\t"
"v_mfma_f32_16x16x32_bf16 v[196:199], v[216:219], a[172:175], v[196:199]\n\t"
"v_accvgpr_write_b32 a49, v99\n\t"
"v_fma_f32 v99, -v41, s56, -v139\n\t"
"v_mfma_f32_16x16x32_bf16 v[196:199], a[4:7], a[176:179], v[196:199]\n\t"
"v_accvgpr_write_b32 a50, v98\n\t"
"v_accvgpr_write_b32 a51, v99\n\t"
"v_mfma_f32_16x16x32_bf16 v[204:207], v[208:211], a[172:175], v[204:207]\n\t"
"s_waitcnt lgkmcnt(6)\n\t"
"v_fma_f32 v98, -v42, s56, -v140\n\t"
"v_fma_f32 v99, -v43, s56, -v141\n\t"
"v_mfma_f32_16x16x32_bf16 v[204:207], v[216:219], a[176:179], v[204:207]\n\t"
"s_waitcnt lgkmcnt(4)\n\t"
"v_accvgpr_write_b32 a56, v98\n\t"
"v_fma_f32 v98, -v44, s56, -v142\n\t"
"v_mfma_f32_16x16x32_bf16 v[204:207], a[4:7], a[180:183], v[204:207]\n\t"
"buffer_store_dwordx4 a[104:107], v248, s[16:19], s29 offen offset:256 nt\n\t"
"v_accvgpr_write_b32 a57, v99\n\t"
"v_fma_f32 v99, -v45, s56, -v143\n\t"
"v_accvgpr_write_b32 a58, v98\n\t"
"v_accvgpr_write_b32 a59, v99\n\t"
"v_mfma_f32_16x16x32_bf16 v[212:215], v[208:211], a[176:179], v[212:215]\n\t"
"Lpg15_%=:\n\t"
"s_cbranch_vccnz Lvr1_%=\n\t"
"v_fma_f32 v98, -v46, s56, -v144\n\t"
"v_fma_f32 v99, -v47, s56, -v145\n\t"
"v_mfma_f32_16x16x32_bf16 v[212:215], v[216:219], a[180:183], v[212:215]\n\t"
"s_add_u32 s29, s23, 787584\n\t"
"ds_read_b128 v[200:203], v252 offset:1024\n\t"
"v_accvgpr_write_b32 a64, v98\n\t"
"v_mfma_f32_16x16x32_bf16 v[212:215], a[4:7], a[184:187], v[212:215]\n\t"
"v_fma_f32 v98, -v48, s56, -v146\n\t"
"v_accvgpr_write_b32 a65, v99\n\t"
"v_mfma_f32_16x16x32_bf16 a[0:3], v[208:211], a[180:183], a[0:3]\n\t"
"s_waitcnt lgkmcnt(4)\n\t"
"v_fma_f32 v99, -v49, s56, -v147\n\t"
"v_accvgpr_write_b32 a66, v98\n\t"
"v_mfma_f32_16x16x32_bf16 a[0:3], v[216:219], a[184:187], a[0:3]\n\t"
"v_accvgpr_write_b32 a67, v99\n\t"
"v_fma_f32 v98, -v50, s56, -v148\n\t"
"v_mfma_f32_16x16x32_bf16 a[0:3], a[4:7], a[188:191], a[0:3]\n\t"
"v_fma_f32 v99, -v51, s56, -v149\n\t"
"v_accvgpr_write_b32 a72, v98\n\t"
"v_mfma_f32_16x16x32_bf16 a[8:11], v[208:211], a[184:187], a[8:11]\n\t"
"s_waitcnt lgkmcnt(3)\n\t"
"v_fma_f32 v98, -v52, s56, -v150\n\t"
"v_accvgpr_write_b32 a73, v99\n\t"
"v_mfma_f32_16x16x32_bf16 a[8:11], v[216:219], a[188:191], a[8:11]\n\t"
"v_fma_f32 v99, -v53, s56, -v151\n\t"
"v_accvgpr_write_b32 a74, v98\n\t"
"v_mfma_f32_16x16x32_bf16 a[8:11], a[4:7], a[192:195], a[8:11]\n\t"
"ds_read_b64_tr_b16 v[232:233], v246 offset:512\n\t"
"v_accvgpr_write_b32 a75, v99\n\t"
"v_mfma_f32_16x16x32_bf16 a[16:19], v[208:211], a[188:191], a[16:19]\n\t"
"s_waitcnt lgkmcnt(3)\n\t"
"ds_read_b64_tr_b16 v[234:235], v246 offset:3584\n\t"
"v_fma_f32 v98, -v54, s56, -v152\n\t"
"v_mfma_f32_16x16x32_bf16 a[16:19], v[216:219], a[192:195], a[16:19]\n\t"
"ds_read_b128 a[44:47], v252 offset:12288\n\t"
"v_fma_f32 v99, -v55, s56, -v153\n\t"
"v_mfma_f32_16x16x32_bf16 a[16:19], a[4:7], a[196:199], a[16:19]\n\t"
"v_accvgpr_write_b32 a80, v98\n\t"
"v_fma_f32 v98, -v56, s56, -v154\n\t"
"v_mfma_f32_16x16x32_bf16 a[24:27], v[208:211], a[192:195], a[24:27]\n\t"
"s_waitcnt lgkmcnt(4)\n\t"
"ds_read_b128 a[76:79], v252 offset:13312\n\t"
"v_accvgpr_write_b32 a81, v99\n\t"
"v_mfma_f32_16x16x32_bf16 a[24:27], v[216:219], a[196:199], a[24:27]\n\t"
"v_fma_f32 v99, -v57, s56, -v155\n\t"
"v_accvgpr_write_b32 a82, v98\n\t"
"v_mfma_f32_16x16x32_bf16 a[24:27], a[4:7], a[200:203], a[24:27]\n\t"
"buffer_store_dwordx4 a[136:139], v248, s[16:19], s29 offen offset:0 nt\n\t"
"ds_read_b128 a[12:15], v252 offset:2048\n\t"
"ds_read_b128 a[108:111], v252 offset:14336\n\t"
"v_accvgpr_write_b32 a83, v99\n\t"
"v_mfma_f32_16x16x32_bf16 a[32:35], v[208:211], a[196:199], a[32:35]\n\t"
"s_waitcnt vmcnt(26)\n\t"
"ds_read_b64_tr_b16 v[224:225], v246 offset:6656\n\t"
"v_fma_f32 v98, -v58, s56, -v156\n\t"
"v_mfma_f32_16x16x32_bf16 a[32:35], v[216:219], a[200:203], a[32:35]\n\t"
"s_waitcnt lgkmcnt(7)\n\t"
"ds_read_b64_tr_b16 v[226:227], v246 offset:9728\n\t"
"v_fma_f32 v99, -v59, s56, -v157\n\t"
"v_mfma_f32_16x16x32_bf16 a[32:35], a[4:7], a[204:207], a[32:35]\n\t"
"v_accvgpr_write_b32 a88, v98\n\t"
"v_fma_f32 v98, -v60, s56, -v158\n\t"
"v_mfma_f32_16x16x32_bf16 a[40:43], a[208:211], a[168:171], a[40:43]\n\t"
"v_accvgpr_write_b32 a89, v99\n\t"
"v_fma_f32 v99, -v61, s56, -v159\n\t"
"v_mfma_f32_16x16x32_bf16 a[40:43], a[212:215], a[172:175], a[40:43]\n\t"
"v_accvgpr_write_b32 a90, v98\n\t"
"v_accvgpr_write_b32 a91, v99\n\t"
"v_mfma_f32_16x16x32_bf16 a[40:43], v[240:243], a[176:179], a[40:43]\n\t"
"v_fma_f32 v98, -v62, s56, -v160\n\t"
"v_fma_f32 v99, -v63, s56, -v161\n\t"
"v_mfma_f32_16x16x32_bf16 a[48:51], a[208:211], a[172:175], a[48:51]\n\t"
"s_waitcnt lgkmcnt(6)\n\t"
"v_accvgpr_write_b32 a96, v98\n\t"
"v_fma_f32 v98, -v64, s56, -v162\n\t"
"v_mfma_f32_16x16x32_bf16 a[48:51], a[212:215], a[176:179], a[48:51]\n\t"
"v_accvgpr_write_b32 a97, v99\n\t"
"v_fma_f32 v99, -v65, s56, -v163\n\t"
"v_mfma_f32_16x16x32_bf16 a[48:51], v[240:243], a[180:183], a[48:51]\n\t"
"v_accvgpr_write_b32 a98, v98\n\t"
"v_accvgpr_write_b32 a99, v99\n\t"
"v_mfma_f32_16x16x32_bf16 v[232:235], v[232:235], v[220:223], 0\n\t"
"v_fma_f32 v98, -v66, s56, -v164\n\t"
"v_fma_f32 v99, -v67, s56, -v165\n\t"
"v_mfma_f32_16x16x32_bf16 a[56:59], a[208:211], a[176:179], a[56:59]\n\t"
"ds_read_b64_tr_b16 v[228:229], v246 offset:12800\n\t"
"v_accvgpr_write_b32 a104, v98\n\t"
"v_mfma_f32_16x16x32_bf16 a[56:59], a[212:215], a[180:183], a[56:59]\n\t"
"ds_read_b64_tr_b16 v[230:231], v246 offset:15872\n\t"
"v_fma_f32 v98, -v68, s56, -v166\n\t"
"v_mfma_f32_16x16x32_bf16 a[56:59], v[240:243], a[184:187], a[56:59]\n\t"
"buffer_store_dwordx4 v[200:203], v248, s[16:19], s29 offen offset:128 nt\n\t"
"v_accvgpr_write_b32 a105, v99\n\t"
"v_fma_f32 v99, -v69, s56, -v167\n\t"
"v_accvgpr_write_b32 a106, v98\n\t"
"v_accvgpr_write_b32 a107, v99\n\t"
"v_mfma_f32_16x16x32_bf16 a[64:67], a[208:211], a[180:183], a[64:67]\n\t"
"s_waitcnt lgkmcnt(2)\n\t"
"v_fma_f32 v98, -v70, s56, -v168\n\t"
"v_fma_f32 v99, -v71, s56, -v169\n\t"
"v_mfma_f32_16x16x32_bf16 a[64:67], a[212:215], a[184:187], a[64:67]\n\t"
"v_cvt_pk_bf16_f32 v232, v232, v233\n\t"
"v_cvt_pk_bf16_f32 v233, v234, v235\n\t"
"v_mfma_f32_16x16x32_bf16 a[64:67], v[240:243], a[188:191], a[64:67]\n\t"
"v_accvgpr_write_b32 a112, v98\n\t"
"v_mfma_f32_16x16x32_bf16 v[224:227], v[224:227], v[220:223], 0\n\t"
"s_barrier\n\t"
"v_mfma_f32_16x16x32_bf16 a[72:75], a[208:211], a[184:187], a[72:75]\n\t"
"v_fma_f32 v98, -v72, s56, -v170\n\t"
"v_mfma_f32_16x16x32_bf16 a[72:75], a[212:215], a[188:191], a[72:75]\n\t"
"v_accvgpr_write_b32 a113, v99\n\t"
"v_fma_f32 v99, -v73, s56, -v171\n\t"
"v_mfma_f32_16x16x32_bf16 a[72:75], v[240:243], a[192:195], a[72:75]\n\t"
"v_accvgpr_write_b32 a114, v98\n\t"
"v_accvgpr_write_b32 a115, v99\n\t"
"v_mfma_f32_16x16x32_bf16 a[80:83], a[208:211], a[188:191], a[80:83]\n\t"
"v_fma_f32 v98, -v74, s56, -v172\n\t"
"v_fma_f32 v99, -v75, s56, -v173\n\t"
"v_mfma_f32_16x16x32_bf16 a[80:83], a[212:215], a[192:195], a[80:83]\n\t"
"v_cvt_pk_bf16_f32 v234, v224, v225\n\t"
"v_cvt_pk_bf16_f32 v235, v226, v227\n\t"
"v_mfma_f32_16x16x32_bf16 a[80:83], v[240:243], a[196:199], a[80:83]\n\t"
"ds_read_b64_tr_b16 v[224:225], v246 offset:20480\n\t"
"v_accvgpr_write_b32 a120, v98\n\t"
"v_mfma_f32_16x16x32_bf16 a[88:91], a[208:211], a[192:195], a[88:91]\n\t"
"s_waitcnt lgkmcnt(1)\n\t"
"ds_read_b64_tr_b16 v[226:227], v246 offset:23552\n\t"
"v_fma_f32 v98, -v76, s56, -v174\n\t"
"v_mfma_f32_16x16x32_bf16 a[88:91], a[212:215], a[196:199], a[88:91]\n\t"
"v_accvgpr_write_b32 a121, v99\n\t"
"v_fma_f32 v99, -v77, s56, -v175\n\t"
"v_mfma_f32_16x16x32_bf16 a[88:91], v[240:243], a[200:203], a[88:91]\n\t"
"buffer_store_dwordx4 a[12:15], v248, s[16:19], s29 offen offset:256 nt\n\t"
"v_accvgpr_write_b32 a122, v98\n\t"
"v_accvgpr_write_b32 a123, v99\n\t"
"v_fma_f32 v98, -v78, s56, -v176\n\t"
"v_fma_f32 v99, -v79, s56, -v177\n\t"
"v_mfma_f32_16x16x32_bf16 v[228:231], v[228:231], v[220:223], 0\n\t"
"s_add_u32 s29, s23, 1180800\n\t"
"v_accvgpr_write_b32 a128, v98\n\t"
"v_fma_f32 v98, -v80, s56, -v178\n\t"
"v_mfma_f32_16x16x32_bf16 a[96:99], a[208:211], a[196:199], a[96:99]\n\t"
"s_waitcnt vmcnt(23)\n\t"
"v_accvgpr_write_b32 a129, v99\n\t"
"v_fma_f32 v99, -v81, s56, -v179\n\t"
"v_mfma_f32_16x16x32_bf16 a[96:99], a[212:215], a[200:203], a[96:99]\n\t"
"v_accvgpr_write_b32 a130, v98\n\t"
"v_accvgpr_write_b32 a131, v99\n\t"
"v_mfma_f32_16x16x32_bf16 a[96:99], v[240:243], a[204:207], a[96:99]\n\t"
"v_fma_f32 v98, -v82, s56, -v180\n\t"
"v_fma_f32 v99, -v83, s56, -v181\n\t"
"v_mfma_f32_16x16x32_bf16 a[104:107], a[216:219], a[168:171], a[104:107]\n\t"
"ds_read_b128 a[168:171], v244 offset:33792\n\t"
"v_accvgpr_write_b32 a136, v98\n\t"
"v_mfma_f32_16x16x32_bf16 a[104:107], a[220:223], a[172:175], a[104:107]\n\t"
"v_cvt_pk_bf16_f32 v228, v228, v229\n\t"
"v_cvt_pk_bf16_f32 v229, v230, v231\n\t"
"v_mfma_f32_16x16x32_bf16 a[104:107], a[224:227], a[176:179], a[104:107]\n\t"
"buffer_load_dwordx4 a[208:211], v252, s[8:11], s20 offen offset:2048 sc1\n\t"
"s_mov_b32 s20, 36864\n\t"
"v_mfma_f32_16x16x32_bf16 a[112:115], a[216:219], a[172:175], a[112:115]\n\t"
"buffer_load_dwordx4 v[240:243], v252, s[8:11], s20 offen offset:0 sc1\n\t"
"v_mfma_f32_16x16x32_bf16 a[112:115], a[220:223], a[176:179], a[112:115]\n\t"
"buffer_load_dwordx4 a[212:215], v252, s[8:11], s20 offen offset:1024 sc1\n\t"
"v_mfma_f32_16x16x32_bf16 a[112:115], a[224:227], a[180:183], a[112:115]\n\t"
"s_waitcnt lgkmcnt(1)\n\t"
"ds_read_b128 a[172:175], v244 offset:34304\n\t"
"v_fma_f32 v98, -v84, s56, -v182\n\t"
"v_mfma_f32_16x16x32_bf16 v[224:227], v[224:227], v[220:223], 0\n\t"
"buffer_store_dwordx4 a[44:47], v248, s[16:19], s29 offen offset:0 nt\n\t"
"v_accvgpr_write_b32 a137, v99\n\t"
"v_fma_f32 v99, -v85, s56, -v183\n\t"
"v_accvgpr_write_b32 a138, v98\n\t"
"v_accvgpr_write_b32 a139, v99\n\t"
"v_mfma_f32_16x16x32_bf16 a[120:123], a[216:219], a[176:179], a[120:123]\n\t"
"ds_read_b128 a[176:179], v244 offset:34816\n\t"
"v_fma_f32 v98, -v86, s56, -v184\n\t"
"v_mfma_f32_16x16x32_bf16 a[120:123], a[220:223], a[180:183], a[120:123]\n\t"
"v_fma_f32 v99, -v87, s56, -v185\n\t"
"v_accvgpr_write_b32 a144, v98\n\t"
"v_mfma_f32_16x16x32_bf16 a[120:123], a[224:227], a[184:187], a[120:123]\n\t"
"v_fma_f32 v98, -v88, s56, -v186\n\t"
"v_accvgpr_write_b32 a145, v99\n\t"
"v_mfma_f32_16x16x32_bf16 a[128:131], a[216:219], a[180:183], a[128:131]\n\t"
"ds_read_b128 a[180:183], v244 offset:35328\n\t"
"v_fma_f32 v99, -v89, s56, -v187\n\t"
"v_mfma_f32_16x16x32_bf16 a[128:131], a[220:223], a[184:187], a[128:131]\n\t"
"v_accvgpr_write_b32 a146, v98\n\t"
"v_accvgpr_write_b32 a147, v99\n\t"
"v_mfma_f32_16x16x32_bf16 a[128:131], a[224:227], a[188:191], a[128:131]\n\t"
"v_fma_f32 v98, -v90, s56, -v188\n\t"
"v_fma_f32 v99, -v91, s56, -v189\n\t"
"v_mfma_f32_16x16x32_bf16 a[136:139], a[216:219], a[184:187], a[136:139]\n\t"
"ds_read_b128 a[184:187], v244 offset:35840\n\t"
"v_cvt_pk_bf16_f32 v230, v224, v225\n\t"
"v_mfma_f32_16x16x32_bf16 a[136:139], a[220:223], a[188:191], a[136:139]\n\t"
"v_cvt_pk_bf16_f32 v231, v226, v227\n\t"
"v_accvgpr_write_b32 a152, v98\n\t"
"v_mfma_f32_16x16x32_bf16 a[136:139], a[224:227], a[192:195], a[136:139]\n\t"
"ds_read_b64_tr_b16 v[224:225], v246 offset:26624\n\t"
"v_fma_f32 v98, -v92, s56, -v190\n\t"
"v_mfma_f32_16x16x32_bf16 a[144:147], a[216:219], a[188:191], a[144:147]\n\t"
"ds_read_b64_tr_b16 v[226:227], v246 offset:29696\n\t"
"v_accvgpr_write_b32 a153, v99\n\t"
"v_mfma_f32_16x16x32_bf16 a[144:147], a[220:223], a[192:195], a[144:147]\n\t"
"ds_read_b128 a[188:191], v244 offset:36352\n\t"
"v_fma_f32 v99, -v93, s56, -v191\n\t"
"v_mfma_f32_16x16x32_bf16 a[144:147], a[224:227], a[196:199], a[144:147]\n\t"
"v_accvgpr_write_b32 a154, v98\n\t"
"v_accvgpr_write_b32 a155, v99\n\t"
"v_fma_f32 v98, -v94, s56, -v192\n\t"
"v_fma_f32 v99, -v95, s56, -v193\n\t"
"v_mfma_f32_16x16x32_bf16 a[152:155], a[216:219], a[192:195], a[152:155]\n\t"
"ds_read_b128 a[192:195], v244 offset:36864\n\t"
"v_accvgpr_write_b32 a160, v98\n\t"
"v_mfma_f32_16x16x32_bf16 a[152:155], a[220:223], a[196:199], a[152:155]\n\t"
"v_fma_f32 v98, -v96, s56, -v194\n\t"
"v_accvgpr_write_b32 a161, v99\n\t"
"v_mfma_f32_16x16x32_bf16 a[152:155], a[224:227], a[200:203], a[152:155]\n\t"
"buffer_store_dwordx4 a[76:79], v248, s[16:19], s29 offen offset:128 nt\n\t"
"v_fma_f32 v99, -v97, s56, -v195\n\t"
"v_accvgpr_write_b32 a162, v98\n\t"
"v_accvgpr_write_b32 a163, v99\n\t"
"v_fma_f32 v200, v100, s64, -v2\n\t"
"v_fma_f32 v201, v101, s64, -v3\n\t"
"v_fma_f32 v202, v102, s64, -v4\n\t"
"v_fma_f32 v203, v103, s64, -v5\n\t"
"v_mfma_f32_16x16x32_bf16 a[160:163], a[216:219], a[196:199], a[160:163]\n\t"
"s_waitcnt lgkmcnt(2)\n\t"
"ds_read_b128 a[196:199], v244 offset:37376\n\t"
"v_fma_f32 v208, v104, s64, -v6\n\t"
"v_mfma_f32_16x16x32_bf16 a[160:163], a[220:223], a[200:203], a[160:163]\n\t"
"s_waitcnt vmcnt(23)\n\t"
"ds_read_b128 a[200:203], v244 offset:37888\n\t"
"v_fma_f32 v209, v105, s64, -v7\n\t"
"v_mfma_f32_16x16x32_bf16 a[160:163], a[224:227], a[204:207], a[160:163]\n\t"
"ds_read_b128 a[204:207], v244 offset:38400\n\t"
"v_fma_f32 v210, v106, s64, -v8\n\t"
"v_fma_f32 v211, v107, s64, -v9\n\t"
"v_mfma_f32_16x16x32_bf16 v[224:227], v[224:227], v[220:223], 0\n\t"
"buffer_load_dwordx4 a[216:219], v252, s[8:11], s20 offen offset:2048 sc1\n\t"
"s_mov_b32 s20, 49152\n\t"
"v_mfma_f32_16x16x32_bf16 v[200:203], a[228:231], a[168:171], v[200:203]\n\t"
"buffer_load_dwordx4 a[220:223], v252, s[8:11], s20 offen offset:0 sc1\n\t"
"v_mfma_f32_16x16x32_bf16 v[200:203], a[236:239], a[172:175], v[200:203]\n\t"
"v_fma_f32 v216, v108, s64, -v10\n\t"
"v_fma_f32 v217, v109, s64, -v11\n\t"
"v_mfma_f32_16x16x32_bf16 v[200:203], a[240:243], a[176:179], v[200:203]\n\t"
"v_fma_f32 v218, v110, s64, -v12\n\t"
"v_fma_f32 v219, v111, s64, -v13\n\t"
"v_mfma_f32_16x16x32_bf16 v[208:211], a[228:231], a[172:175], v[208:211]\n\t"
"v_fma_f32 v98, v112, s64, -v14\n\t"
"v_fma_f32 v99, v113, s64, -v15\n\t"
"v_mfma_f32_16x16x32_bf16 v[208:211], a[236:239], a[176:179], v[208:211]\n\t"
"v_accvgpr_write_b32 a4, v98\n\t"
"v_mfma_f32_16x16x32_bf16 v[208:211], a[240:243], a[180:183], v[208:211]\n\t"
"s_waitcnt lgkmcnt(0)\n\t"
"s_barrier\n\t"
"v_mfma_f32_16x16x32_bf16 v[216:219], a[228:231], a[176:179], v[216:219]\n\t"
"v_fma_f32 v98, v114, s64, -v16\n\t"
"v_mfma_f32_16x16x32_bf16 v[216:219], a[236:239], a[180:183], v[216:219]\n\t"
"buffer_load_dwordx4 a[224:227], v252, s[8:11], s20 offen offset:1024 sc1\n\t"
"v_mfma_f32_16x16x32_bf16 v[216:219], a[240:243], a[184:187], v[216:219]\n\t"
"buffer_store_dwordx4 a[108:111], v248, s[16:19], s29 offen offset:256 nt\n\t"
"v_accvgpr_write_b32 a5, v99\n\t"
"v_fma_f32 v99, v115, s64, -v17\n\t"
"v_accvgpr_write_b32 a6, v98\n\t"
"v_accvgpr_write_b32 a7, v99\n\t"
"v_fma_f32 v98, v116, s64, -v18\n\t"
"v_fma_f32 v99, v117, s64, -v19\n\t"
"v_cvt_pk_bf16_f32 v224, v224, v225\n\t"
"v_cvt_pk_bf16_f32 v225, v226, v227\n\t"
"v_mfma_f32_16x16x32_bf16 a[4:7], a[228:231], a[180:183], a[4:7]\n\t"
"v_accvgpr_write_b32 a12, v98\n\t"
"v_fma_f32 v98, v118, s64, -v20\n\t"
"v_mfma_f32_16x16x32_bf16 a[4:7], a[236:239], a[184:187], a[4:7]\n\t"
"v_accvgpr_write_b32 a13, v99\n\t"
"v_fma_f32 v99, v119, s64, -v21\n\t"
"v_mfma_f32_16x16x32_bf16 a[4:7], a[240:243], a[188:191], a[4:7]\n\t"
"ds_write2st64_b64 v245, v[232:233], v[234:235] offset0:48 offset1:49\n\t"
"v_accvgpr_write_b32 a14, v98\n\t"
"v_accvgpr_write_b32 a15, v99\n\t"
"v_fma_f32 v98, v120, s64, -v22\n\t"
"v_fma_f32 v99, v121, s64, -v23\n\t"
"v_mfma_f32_16x16x32_bf16 a[12:15], a[228:231], a[184:187], a[12:15]\n\t"
"ds_read_b64_tr_b16 v[232:233], v246 offset:32768\n\t"
"v_mfma_f32_16x16x32_bf16 a[12:15], a[236:239], a[188:191], a[12:15]\n\t"
"ds_read_b64_tr_b16 v[234:235], v246 offset:35840\n\t"
"v_accvgpr_write_b32 a20, v98\n\t"
"v_mfma_f32_16x16x32_bf16 a[12:15], a[240:243], a[192:195], a[12:15]\n\t"
"ds_write2st64_b64 v245, v[228:229], v[230:231] offset0:50 offset1:51\n\t"
"buffer_load_dwordx4 v[248:251], v252, s[8:11], s20 offen offset:2048 sc1\n\t"
"v_fma_f32 v98, v122, s64, -v24\n\t"
"v_accvgpr_write_b32 a21, v99\n\t"
"v_fma_f32 v99, v123, s64, -v25\n\t"
"v_accvgpr_write_b32 a22, v98\n\t"
"v_accvgpr_write_b32 a23, v99\n\t"
"v_fma_f32 v98, v124, s64, -v26\n\t"
"v_fma_f32 v99, v125, s64, -v27\n\t"
"v_mfma_f32_16x16x32_bf16 a[20:23], a[228:231], a[188:191], a[20:23]\n\t"
"ds_read_b64_tr_b16 v[228:229], v246 offset:40448\n\t"
"v_accvgpr_write_b32 a28, v98\n\t"
"v_mfma_f32_16x16x32_bf16 a[20:23], a[236:239], a[192:195], a[20:23]\n\t"
"ds_read_b64_tr_b16 v[230:231], v246 offset:43520\n\t"
"v_fma_f32 v98, v126, s64, -v28\n\t"
"v_mfma_f32_16x16x32_bf16 a[20:23], a[240:243], a[196:199], a[20:23]\n\t"
"s_waitcnt lgkmcnt(3)\n\t"
"ds_write_b64 v245, v[224:225] offset:26624\n\t"
"v_accvgpr_write_b32 a29, v99\n\t"
"v_fma_f32 v99, v127, s64, -v29\n\t"
"v_accvgpr_write_b32 a30, v98\n\t"
"v_accvgpr_write_b32 a31, v99\n\t"
"v_mfma_f32_16x16x32_bf16 v[232:235], v[232:235], v[220:223], 0\n\t"
"s_mov_b32 s20, 61440\n\t"
"ds_read_b64_tr_b16 v[224:225], v246 offset:46592\n\t"
"v_fma_f32 v98, v128, s64, -v30\n\t"
"v_fma_f32 v99, v129, s64, -v31\n\t"
"v_mfma_f32_16x16x32_bf16 a[28:31], a[228:231], a[192:195], a[28:31]\n\t"
"ds_read_b64_tr_b16 v[226:227], v246 offset:49664\n\t"
"v_accvgpr_write_b32 a36, v98\n\t"
"v_mfma_f32_16x16x32_bf16 a[28:31], a[236:239], a[196:199], a[28:31]\n\t"
"v_fma_f32 v98, v130, s64, -v32\n\t"
"v_accvgpr_write_b32 a37, v99\n\t"
"v_mfma_f32_16x16x32_bf16 a[28:31], a[240:243], a[200:203], a[28:31]\n\t"
"s_waitcnt lgkmcnt(3)\n\t"
"v_fma_f32 v99, v131, s64, -v33\n\t"
"v_accvgpr_write_b32 a38, v98\n\t"
"v_accvgpr_write_b32 a39, v99\n\t"
"v_mfma_f32_16x16x32_bf16 v[228:231], v[228:231], v[220:223], 0\n\t"
"v_fma_f32 v98, v132, s64, -v34\n\t"
"v_fma_f32 v99, v133, s64, -v35\n\t"
"v_mfma_f32_16x16x32_bf16 a[36:39], a[228:231], a[196:199], a[36:39]\n\t"
"v_accvgpr_write_b32 a44, v98\n\t"
"v_fma_f32 v98, v134, s64, -v36\n\t"
"v_mfma_f32_16x16x32_bf16 a[36:39], a[236:239], a[200:203], a[36:39]\n\t"
"s_waitcnt lgkmcnt(0)\n\t"
"v_accvgpr_write_b32 a45, v99\n\t"
"v_fma_f32 v99, v135, s64, -v37\n\t"
"v_mfma_f32_16x16x32_bf16 a[36:39], a[240:243], a[204:207], a[36:39]\n\t"
"v_accvgpr_write_b32 a46, v98\n\t"
"v_accvgpr_write_b32 a47, v99\n\t"
"v_fma_f32 v98, v136, s64, -v38\n\t"
"v_fma_f32 v99, v137, s64, -v39\n\t"
"v_mfma_f32_16x16x32_bf16 v[224:227], v[224:227], v[220:223], 0\n\t"
"s_waitcnt vmcnt(20)\n\t"
"buffer_load_dwordx4 a[228:231], v252, s[8:11], s20 offen offset:0 sc1\n\t"
"v_cvt_pk_bf16_f32 v232, v232, v233\n\t"
"v_cvt_pk_bf16_f32 v233, v234, v235\n\t"
"v_mfma_f32_16x16x32_bf16 a[44:47], a[244:247], a[168:171], a[44:47]\n\t"
"v_accvgpr_write_b32 a52, v98\n\t"
"v_fma_f32 v98, v138, s64, -v40\n\t"
"v_mfma_f32_16x16x32_bf16 a[44:47], a[248:251], a[172:175], a[44:47]\n\t"
"v_accvgpr_write_b32 a53, v99\n\t"
"v_fma_f32 v99, v139, s64, -v41\n\t"
"v_mfma_f32_16x16x32_bf16 a[44:47], a[252:255], a[176:179], a[44:47]\n\t"
"buffer_load_dwordx4 a[236:239], v252, s[8:11], s20 offen offset:1024 sc1\n\t"
"v_accvgpr_write_b32 a54, v98\n\t"
"v_accvgpr_write_b32 a55, v99\n\t"
"v_fma_f32 v98, v140, s64, -v42\n\t"
"v_fma_f32 v99, v141, s64, -v43\n\t"
"v_mfma_f32_16x16x32_bf16 a[52:55], a[244:247], a[172:175], a[52:55]\n\t"
"v_accvgpr_write_b32 a60, v98\n\t"
"v_fma_f32 v98, v142, s64, -v44\n\t"
"v_mfma_f32_16x16x32_bf16 a[52:55], a[248:251], a[176:179], a[52:55]\n\t"
"v_accvgpr_write_b32 a61, v99\n\t"
"v_fma_f32 v99, v143, s64, -v45\n\t"
"v_mfma_f32_16x16x32_bf16 a[52:55], a[252:255], a[180:183], a[52:55]\n\t"
"buffer_load_dwordx4 a[240:243], v252, s[8:11], s20 offen offset:2048 sc1\n\t"
"v_accvgpr_write_b32 a62, v98\n\t"
"v_accvgpr_write_b32 a63, v99\n\t"
"v_cvt_pk_bf16_f32 v234, v228, v229\n\t"
"v_cvt_pk_bf16_f32 v235, v230, v231\n\t"
"v_fma_f32 v98, v144, s64, -v46\n\t"
"v_fma_f32 v99, v145, s64, -v47\n\t"
"v_mfma_f32_16x16x32_bf16 a[60:63], a[244:247], a[176:179], a[60:63]\n\t"
"ds_read_b64_tr_b16 v[228:229], v246 offset:52736\n\t"
"v_accvgpr_write_b32 a68, v98\n\t"
"v_mfma_f32_16x16x32_bf16 a[60:63], a[248:251], a[180:183], a[60:63]\n\t"
"ds_read_b64_tr_b16 v[230:231], v246 offset:55808\n\t"
"v_fma_f32 v98, v146, s64, -v48\n\t"
"v_mfma_f32_16x16x32_bf16 a[60:63], a[252:255], a[184:187], a[60:63]\n\t"
"ds_write2st64_b64 v245, v[232:233], v[234:235] offset0:53 offset1:54\n\t"
"v_accvgpr_write_b32 a69, v99\n\t"
"v_fma_f32 v99, v147, s64, -v49\n\t"
"v_accvgpr_write_b32 a70, v98\n\t"
"v_accvgpr_write_b32 a71, v99\n\t"
"v_fma_f32 v98, v148, s64, -v50\n\t"
"v_fma_f32 v99, v149, s64, -v51\n\t"
"v_cvt_pk_bf16_f32 v224, v224, v225\n\t"
"v_cvt_pk_bf16_f32 v225, v226, v227\n\t"
"v_mfma_f32_16x16x32_bf16 a[68:71], a[244:247], a[180:183], a[68:71]\n\t"
"v_accvgpr_write_b32 a76, v98\n\t"
"v_fma_f32 v98, v150, s64, -v52\n\t"
"v_mfma_f32_16x16x32_bf16 a[68:71], a[248:251], a[184:187], a[68:71]\n\t"
"v_accvgpr_write_b32 a77, v99\n\t"
"v_fma_f32 v99, v151, s64, -v53\n\t"
"v_mfma_f32_16x16x32_bf16 a[68:71], a[252:255], a[188:191], a[68:71]\n\t"
"v_accvgpr_write_b32 a78, v98\n\t"
"v_accvgpr_write_b32 a79, v99\n\t"
"v_fma_f32 v98, v152, s64, -v54\n\t"
"v_fma_f32 v99, v153, s64, -v55\n\t"
"v_mfma_f32_16x16x32_bf16 a[76:79], a[244:247], a[184:187], a[76:79]\n\t"
"s_waitcnt lgkmcnt(1)\n\t"
"v_accvgpr_write_b32 a84, v98\n\t"
"v_fma_f32 v98, v154, s64, -v56\n\t"
"v_mfma_f32_16x16x32_bf16 a[76:79], a[248:251], a[188:191], a[76:79]\n\t"
"v_accvgpr_write_b32 a85, v99\n\t"
"v_fma_f32 v99, v155, s64, -v57\n\t"
"v_mfma_f32_16x16x32_bf16 a[76:79], a[252:255], a[192:195], a[76:79]\n\t"
"v_accvgpr_write_b32 a86, v98\n\t"
"v_accvgpr_write_b32 a87, v99\n\t"
"v_fma_f32 v98, v156, s64, -v58\n\t"
"v_fma_f32 v99, v157, s64, -v59\n\t"
"v_mfma_f32_16x16x32_bf16 v[228:231], v[228:231], v[220:223], 0\n\t"
"v_accvgpr_write_b32 a92, v98\n\t"
"v_fma_f32 v98, v158, s64, -v60\n\t"
"v_mfma_f32_16x16x32_bf16 a[84:87], a[244:247], a[188:191], a[84:87]\n\t"
"s_mov_b32 s20, 73728\n\t"
"v_accvgpr_write_b32 a93, v99\n\t"
"v_fma_f32 v99, v159, s64, -v61\n\t"
"v_mfma_f32_16x16x32_bf16 a[84:87], a[248:251], a[192:195], a[84:87]\n\t"
"v_accvgpr_write_b32 a94, v98\n\t"
"v_accvgpr_write_b32 a95, v99\n\t"
"v_mfma_f32_16x16x32_bf16 a[84:87], a[252:255], a[196:199], a[84:87]\n\t"
"v_fma_f32 v98, v160, s64, -v62\n\t"
"v_fma_f32 v99, v161, s64, -v63\n\t"
"v_mfma_f32_16x16x32_bf16 a[92:95], a[244:247], a[192:195], a[92:95]\n\t"
"v_accvgpr_write_b32 a100, v98\n\t"
"v_fma_f32 v98, v162, s64, -v64\n\t"
"v_mfma_f32_16x16x32_bf16 a[92:95], a[248:251], a[196:199], a[92:95]\n\t"
"v_accvgpr_write_b32 a101, v99\n\t"
"v_fma_f32 v99, v163, s64, -v65\n\t"
"v_mfma_f32_16x16x32_bf16 a[92:95], a[252:255], a[200:203], a[92:95]\n\t"
"v_accvgpr_write_b32 a102, v98\n\t"
"v_accvgpr_write_b32 a103, v99\n\t"
"v_fma_f32 v98, v164, s64, -v66\n\t"
"v_fma_f32 v99, v165, s64, -v67\n\t"
"v_cvt_pk_bf16_f32 v226, v228, v229\n\t"
"v_cvt_pk_bf16_f32 v227, v230, v231\n\t"
"v_mfma_f32_16x16x32_bf16 a[100:103], a[244:247], a[196:199], a[100:103]\n\t"
"v_accvgpr_write_b32 a108, v98\n\t"
"v_fma_f32 v98, v166, s64, -v68\n\t"
"v_mfma_f32_16x16x32_bf16 a[100:103], a[248:251], a[200:203], a[100:103]\n\t"
"v_accvgpr_write_b32 a109, v99\n\t"
"v_fma_f32 v99, v167, s64, -v69\n\t"
"v_mfma_f32_16x16x32_bf16 a[100:103], a[252:255], a[204:207], a[100:103]\n\t"
"ds_write2st64_b64 v245, v[224:225], v[226:227] offset0:55 offset1:56\n\t"
"v_accvgpr_write_b32 a110, v98\n\t"
"v_accvgpr_write_b32 a111, v99\n\t"
"v_fma_f32 v98, v168, s64, -v70\n\t"
"v_fma_f32 v99, v169, s64, -v71\n\t"
"s_waitcnt vmcnt(12)\n\t"
"v_mfma_f32_16x16x32_bf16 a[108:111], a[232:235], a[168:171], a[108:111]\n\t"
"buffer_load_dwordx4 a[244:247], v252, s[8:11], s20 offen offset:0 sc1\n\t"
"v_mfma_f32_16x16x32_bf16 a[108:111], v[236:239], a[172:175], a[108:111]\n\t"
"v_accvgpr_write_b32 a116, v98\n\t"
"v_mfma_f32_16x16x32_bf16 a[108:111], a[208:211], a[176:179], a[108:111]\n\t"
"v_accvgpr_write_b32 a117, v99\n\t"
"v_fma_f32 v98, v170, s64, -v72\n\t"
"v_accvgpr_write_b32 a118, v98\n\t"
"v_fma_f32 v99, v171, s64, -v73\n\t"
"v_accvgpr_write_b32 a119, v99\n\t"
"s_waitcnt lgkmcnt(0)\n\t"
"s_barrier\n\t"
"v_mfma_f32_16x16x32_bf16 a[116:119], a[232:235], a[172:175], a[116:119]\n\t"
"ds_read_b128 a[168:171], v244 offset:24576\n\t"
"v_mfma_f32_16x16x32_bf16 a[116:119], v[236:239], a[176:179], a[116:119]\n\t"
"buffer_load_dwordx4 a[248:251], v252, s[8:11], s20 offen offset:1024 sc1\n\t"
"v_mfma_f32_16x16x32_bf16 a[116:119], a[208:211], a[180:183], a[116:119]\n\t"
"buffer_load_dwordx4 a[252:255], v252, s[8:11], s20 offen offset:2048 sc1\n\t"
"v_fma_f32 v98, v172, s64, -v74\n\t"
"v_fma_f32 v99, v173, s64, -v75\n\t"
"v_accvgpr_write_b32 a124, v98\n\t"
"v_fma_f32 v98, v174, s64, -v76\n\t"
"v_accvgpr_write_b32 a125, v99\n\t"
"v_fma_f32 v99, v175, s64, -v77\n\t"
"v_accvgpr_write_b32 a126, v98\n\t"
"v_accvgpr_write_b32 a127, v99\n\t"
"v_fma_f32 v98, v176, s64, -v78\n\t"
"v_fma_f32 v99, v177, s64, -v79\n\t"
"v_mfma_f32_16x16x32_bf16 a[124:127], a[232:235], a[176:179], a[124:127]\n\t"
"ds_read_b128 a[172:175], v244 offset:25088\n\t"
"v_accvgpr_write_b32 a132, v98\n\t"
"v_mfma_f32_16x16x32_bf16 a[124:127], v[236:239], a[180:183], a[124:127]\n\t"
"ds_read_b128 a[176:179], v244 offset:25600\n\t"
"v_fma_f32 v98, v178, s64, -v80\n\t"
"v_mfma_f32_16x16x32_bf16 a[124:127], a[208:211], a[184:187], a[124:127]\n\t"
"v_accvgpr_write_b32 a133, v99\n\t"
"v_fma_f32 v99, v179, s64, -v81\n\t"
"v_accvgpr_write_b32 a134, v98\n\t"
"v_accvgpr_write_b32 a135, v99\n\t"
"v_fma_f32 v98, v180, s64, -v82\n\t"
"v_fma_f32 v99, v181, s64, -v83\n\t"
"v_mfma_f32_16x16x32_bf16 a[132:135], a[232:235], a[180:183], a[132:135]\n\t"
"ds_read_b128 a[180:183], v244 offset:26112\n\t"
"v_accvgpr_write_b32 a140, v98\n\t"
"v_mfma_f32_16x16x32_bf16 a[132:135], v[236:239], a[184:187], a[132:135]\n\t"
"v_fma_f32 v98, v182, s64, -v84\n\t"
"v_accvgpr_write_b32 a141, v99\n\t"
"v_mfma_f32_16x16x32_bf16 a[132:135], a[208:211], a[188:191], a[132:135]\n\t"
"v_fma_f32 v99, v183, s64, -v85\n\t"
"v_accvgpr_write_b32 a142, v98\n\t"
"v_accvgpr_write_b32 a143, v99\n\t"
"v_fma_f32 v98, v184, s64, -v86\n\t"
"v_fma_f32 v99, v185, s64, -v87\n\t"
"v_mfma_f32_16x16x32_bf16 a[140:143], a[232:235], a[184:187], a[140:143]\n\t"
"ds_read_b128 a[184:187], v244 offset:26624\n\t"
"v_accvgpr_write_b32 a148, v98\n\t"
"v_mfma_f32_16x16x32_bf16 a[140:143], v[236:239], a[188:191], a[140:143]\n\t"
"v_fma_f32 v98, v186, s64, -v88\n\t"
"v_accvgpr_write_b32 a149, v99\n\t"
"v_mfma_f32_16x16x32_bf16 a[140:143], a[208:211], a[192:195], a[140:143]\n\t"
"v_fma_f32 v99, v187, s64, -v89\n\t"
"v_accvgpr_write_b32 a150, v98\n\t"
"v_accvgpr_write_b32 a151, v99\n\t"
"v_fma_f32 v98, v188, s64, -v90\n\t"
"v_fma_f32 v99, v189, s64, -v91\n\t"
"v_mfma_f32_16x16x32_bf16 a[148:151], a[232:235], a[188:191], a[148:151]\n\t"
"s_mov_b32 s20, 86016\n\t"
"ds_read_b128 a[188:191], v244 offset:27136\n\t"
"v_accvgpr_write_b32 a156, v98\n\t"
"v_mfma_f32_16x16x32_bf16 a[148:151], v[236:239], a[192:195], a[148:151]\n\t"
"Lpg16_%=:\n\t"
"s_cbranch_vccnz Lvr1_%=\n\t"
"v_fma_f32 v98, v190, s64, -v92\n\t"
"v_accvgpr_write_b32 a157, v99\n\t"
"v_mfma_f32_16x16x32_bf16 a[148:151], a[208:211], a[196:199], a[148:151]\n\t"
"v_fma_f32 v99, v191, s64, -v93\n\t"
"v_accvgpr_write_b32 a158, v98\n\t"
"v_accvgpr_write_b32 a159, v99\n\t"
"v_fma_f32 v98, v192, s64, -v94\n\t"
"v_fma_f32 v99, v193, s64, -v95\n\t"
"v_mfma_f32_16x16x32_bf16 a[156:159], a[232:235], a[192:195], a[156:159]\n\t"
"ds_read_b128 a[192:195], v244 offset:27648\n\t"
"v_accvgpr_write_b32 a164, v98\n\t"
"v_mfma_f32_16x16x32_bf16 a[156:159], v[236:239], a[196:199], a[156:159]\n\t"
"v_fma_f32 v98, v194, s64, -v96\n\t"
"v_accvgpr_write_b32 a165, v99\n\t"
"v_mfma_f32_16x16x32_bf16 a[156:159], a[208:211], a[200:203], a[156:159]\n\t"
"s_add_u32 m0, s24, -128\n\t"
"s_add_u32 s28, s21, 0\n\t"
"v_fma_f32 v99, v195, s64, -v97\n\t"
"v_accvgpr_write_b32 a166, v98\n\t"
"v_accvgpr_write_b32 a167, v99\n\t"
"s_nop 1\n\t"
"v_mfma_f32_16x16x32_bf16 a[164:167], a[232:235], a[196:199], a[164:167]\n\t"
"buffer_load_dwordx4 a[232:235], v252, s[8:11], s20 offen offset:0 sc1\n\t"
"v_mfma_f32_16x16x32_bf16 a[164:167], v[236:239], a[200:203], a[164:167]\n\t"
"ds_read_b128 a[196:199], v244 offset:28160\n\t"
"v_fma_f32 v100, -v100, s66, v2\n\t"
"v_mfma_f32_16x16x32_bf16 a[164:167], a[208:211], a[204:207], a[164:167]\n\t"
"ds_read_b128 a[200:203], v244 offset:28672\n\t"
"v_fma_f32 v2, v4, s52, v102\n\t"
"v_fma_f32 v101, -v101, s66, v3\n\t"
"v_fma_f32 v3, v5, s52, v103\n\t"
"v_fma_f32 v102, -v102, s66, v4\n\t"
"v_fma_f32 v103, -v103, s66, v5\n\t"
"v_fma_f32 v4, v6, s52, v104\n\t"
"v_fma_f32 v5, v7, s52, v105\n\t"
"Lst21_%=:\n\t"
"s_waitcnt vmcnt(11) lgkmcnt(6)\n\t"
"v_mfma_f32_16x16x32_bf16 v[0:3], v[240:243], a[168:171], v[0:3]\n\t"
"buffer_load_dwordx4 v247, s[12:15], s28 offen offset:128 nt lds\n\t"
"v_mfma_f32_16x16x32_bf16 v[0:3], a[212:215], a[172:175], v[0:3]\n\t"
"s_add_u32 m0, s24, 6016\n\t"
"s_add_u32 s28, s21, 196608\n\t"
"ds_read_b128 a[204:207], v244 offset:29184\n\t"
"v_mfma_f32_16x16x32_bf16 v[0:3], a[216:219], a[176:179], v[0:3]\n\t"
"buffer_load_dwordx4 v247, s[12:15], s28 offen offset:128 nt lds\n\t"
"v_fma_f32 v104, -v104, s66, v6\n\t"
"v_fma_f32 v6, v8, s52, v106\n\t"
"v_fma_f32 v105, -v105, s66, v7\n\t"
"v_fma_f32 v7, v9, s52, v107\n\t"
"v_fma_f32 v106, -v106, s66, v8\n\t"
"v_fma_f32 v107, -v107, s66, v9\n\t"
"v_fma_f32 v8, v10, s52, v108\n\t"
"v_fma_f32 v9, v11, s52, v109\n\t"
"s_waitcnt lgkmcnt(6)\n\t"
"v_mfma_f32_16x16x32_bf16 v[4:7], v[240:243], a[172:175], v[4:7]\n\t"
"s_add_u32 m0, s24, 59776\n\t"
"s_add_u32 s28, s21, 1769472\n\t"
"v_fma_f32 v108, -v108, s66, v10\n\t"
"v_mfma_f32_16x16x32_bf16 v[4:7], a[212:215], a[176:179], v[4:7]\n\t"
"v_fma_f32 v10, v12, s52, v110\n\t"
"v_fma_f32 v109, -v109, s66, v11\n\t"
"v_mfma_f32_16x16x32_bf16 v[4:7], a[216:219], a[180:183], v[4:7]\n\t"
"buffer_load_dwordx4 v247, s[12:15], s28 offen offset:128 nt lds\n\t"
"v_fma_f32 v11, v13, s52, v111\n\t"
"v_fma_f32 v110, -v110, s66, v12\n\t"
"v_fma_f32 v111, -v111, s66, v13\n\t"
"v_fma_f32 v12, v14, s52, v112\n\t"
"v_fma_f32 v13, v15, s52, v113\n\t"
"s_add_u32 m0, s24, 65920\n\t"
"s_add_u32 s28, s21, 1966080\n\t"
"s_waitcnt lgkmcnt(5)\n\t"
"v_mfma_f32_16x16x32_bf16 v[8:11], v[240:243], a[176:179], v[8:11]\n\t"
"buffer_load_dwordx4 v247, s[12:15], s28 offen offset:128 nt lds\n\t"
"v_mfma_f32_16x16x32_bf16 v[8:11], a[212:215], a[180:183], v[8:11]\n\t"
"s_add_u32 m0, s24, 12160\n\t"
"s_add_u32 s28, s21, 393216\n\t"
"v_fma_f32 v112, -v112, s66, v14\n\t"
"v_mfma_f32_16x16x32_bf16 v[8:11], a[216:219], a[184:187], v[8:11]\n\t"
"v_fma_f32 v14, v16, s52, v114\n\t"
"v_fma_f32 v113, -v113, s66, v15\n\t"
"v_fma_f32 v15, v17, s52, v115\n\t"
"v_fma_f32 v114, -v114, s66, v16\n\t"
"v_fma_f32 v115, -v115, s66, v17\n\t"
"v_fma_f32 v16, v18, s52, v116\n\t"
"v_fma_f32 v17, v19, s52, v117\n\t"
"s_waitcnt lgkmcnt(4)\n\t"
"v_mfma_f32_16x16x32_bf16 v[12:15], v[240:243], a[180:183], v[12:15]\n\t"
"buffer_load_dwordx4 v247, s[12:15], s28 offen offset:128 nt lds\n\t"
"v_mfma_f32_16x16x32_bf16 v[12:15], a[212:215], a[184:187], v[12:15]\n\t"
"s_add_u32 m0, s24, 19840\n\t"
"s_add_u32 s28, s21, 589824\n\t"
"v_fma_f32 v116, -v116, s66, v18\n\t"
"v_mfma_f32_16x16x32_bf16 v[12:15], a[216:219], a[188:191], v[12:15]\n\t"
"buffer_load_dwordx4 v247, s[12:15], s28 offen offset:128 nt lds\n\t"
"v_fma_f32 v18, v20, s52, v118\n\t"
"v_fma_f32 v117, -v117, s66, v19\n\t"
"v_fma_f32 v19, v21, s52, v119\n\t"
"v_fma_f32 v118, -v118, s66, v20\n\t"
"v_fma_f32 v119, -v119, s66, v21\n\t"
"v_fma_f32 v20, v22, s52, v120\n\t"
"v_fma_f32 v21, v23, s52, v121\n\t"
"s_waitcnt lgkmcnt(3)\n\t"
"v_mfma_f32_16x16x32_bf16 v[16:19], v[240:243], a[184:187], v[16:19]\n\t"
"v_fma_f32 v120, -v120, s66, v22\n\t"
"v_fma_f32 v22, v24, s52, v122\n\t"
"v_mfma_f32_16x16x32_bf16 v[16:19], a[212:215], a[188:191], v[16:19]\n\t"
"v_fma_f32 v121, -v121, s66, v23\n\t"
"v_fma_f32 v23, v25, s52, v123\n\t"
"v_mfma_f32_16x16x32_bf16 v[16:19], a[216:219], a[192:195], v[16:19]\n\t"
"ds_read_b64_tr_b16 v[232:233], v246 offset:1024\n\t"
"v_fma_f32 v122, -v122, s66, v24\n\t"
"v_fma_f32 v123, -v123, s66, v25\n\t"
"v_fma_f32 v24, v26, s52, v124\n\t"
"v_fma_f32 v25, v27, s52, v125\n\t"
"s_waitcnt lgkmcnt(3)\n\t"
"v_mfma_f32_16x16x32_bf16 v[20:23], v[240:243], a[188:191], v[20:23]\n\t"
"ds_read_b64_tr_b16 v[234:235], v246 offset:4096\n\t"
"v_fma_f32 v124, -v124, s66, v26\n\t"
"v_mfma_f32_16x16x32_bf16 v[20:23], a[212:215], a[192:195], v[20:23]\n\t"
"v_fma_f32 v26, v28, s52, v126\n\t"
"v_fma_f32 v125, -v125, s66, v27\n\t"
"v_mfma_f32_16x16x32_bf16 v[20:23], a[216:219], a[196:199], v[20:23]\n\t"
"v_fma_f32 v27, v29, s52, v127\n\t"
"v_fma_f32 v126, -v126, s66, v28\n\t"
"v_fma_f32 v127, -v127, s66, v29\n\t"
"v_fma_f32 v28, v30, s52, v128\n\t"
"v_fma_f32 v29, v31, s52, v129\n\t"
"s_waitcnt lgkmcnt(3)\n\t"
"v_mfma_f32_16x16x32_bf16 v[24:27], v[240:243], a[192:195], v[24:27]\n\t"
"v_fma_f32 v128, -v128, s66, v30\n\t"
"v_fma_f32 v30, v32, s52, v130\n\t"
"v_mfma_f32_16x16x32_bf16 v[24:27], a[212:215], a[196:199], v[24:27]\n\t"
"v_fma_f32 v129, -v129, s66, v31\n\t"
"v_fma_f32 v31, v33, s52, v131\n\t"
"v_mfma_f32_16x16x32_bf16 v[24:27], a[216:219], a[200:203], v[24:27]\n\t"
"v_fma_f32 v130, -v130, s66, v32\n\t"
"v_fma_f32 v131, -v131, s66, v33\n\t"
"v_fma_f32 v32, v34, s52, v132\n\t"
"v_fma_f32 v33, v35, s52, v133\n\t"
"s_waitcnt lgkmcnt(2)\n\t"
"v_mfma_f32_16x16x32_bf16 v[28:31], v[240:243], a[196:199], v[28:31]\n\t"
"ds_read_b64_tr_b16 v[228:229], v246 offset:7168\n\t"
"v_fma_f32 v132, -v132, s66, v34\n\t"
"v_mfma_f32_16x16x32_bf16 v[28:31], a[212:215], a[200:203], v[28:31]\n\t"
"ds_read_b64_tr_b16 v[230:231], v246 offset:10240\n\t"
"v_fma_f32 v34, v36, s52, v134\n\t"
"v_mfma_f32_16x16x32_bf16 v[28:31], a[216:219], a[204:207], v[28:31]\n\t"
"v_fma_f32 v133, -v133, s66, v35\n\t"
"v_fma_f32 v35, v37, s52, v135\n\t"
"v_fma_f32 v134, -v134, s66, v36\n\t"
"v_fma_f32 v135, -v135, s66, v37\n\t"
"v_fma_f32 v36, v38, s52, v136\n\t"
"v_fma_f32 v37, v39, s52, v137\n\t"
"s_waitcnt vmcnt(13)\n\t"
"v_mfma_f32_16x16x32_bf16 v[32:35], a[220:223], a[168:171], v[32:35]\n\t"
"v_fma_f32 v136, -v136, s66, v38\n\t"
"v_fma_f32 v38, v40, s52, v138\n\t"
"v_mfma_f32_16x16x32_bf16 v[32:35], a[224:227], a[172:175], v[32:35]\n\t"
"v_fma_f32 v137, -v137, s66, v39\n\t"
"v_fma_f32 v39, v41, s52, v139\n\t"
"v_mfma_f32_16x16x32_bf16 v[32:35], v[248:251], a[176:179], v[32:35]\n\t"
"v_fma_f32 v138, -v138, s66, v40\n\t"
"v_fma_f32 v139, -v139, s66, v41\n\t"
"v_mfma_f32_16x16x32_bf16 v[36:39], a[220:223], a[172:175], v[36:39]\n\t"
"s_waitcnt lgkmcnt(2)\n\t"
"v_fma_f32 v40, v42, s52, v140\n\t"
"v_fma_f32 v41, v43, s52, v141\n\t"
"v_mfma_f32_16x16x32_bf16 v[36:39], a[224:227], a[176:179], v[36:39]\n\t"
"v_fma_f32 v140, -v140, s66, v42\n\t"
"v_fma_f32 v42, v44, s52, v142\n\t"
"v_mfma_f32_16x16x32_bf16 v[36:39], v[248:251], a[180:183], v[36:39]\n\t"
"v_fma_f32 v141, -v141, s66, v43\n\t"
"v_fma_f32 v43, v45, s52, v143\n\t"
"v_fma_f32 v142, -v142, s66, v44\n\t"
"v_fma_f32 v143, -v143, s66, v45\n\t"
"v_mfma_f32_16x16x32_bf16 v[232:235], v[232:235], v[220:223], 0\n\t"
"v_fma_f32 v44, v46, s52, v144\n\t"
"v_fma_f32 v45, v47, s52, v145\n\t"
"v_mfma_f32_16x16x32_bf16 v[40:43], a[220:223], a[176:179], v[40:43]\n\t"
"ds_read_b64_tr_b16 v[224:225], v246 offset:13312\n\t"
"v_fma_f32 v144, -v144, s66, v46\n\t"
"v_mfma_f32_16x16x32_bf16 v[40:43], a[224:227], a[180:183], v[40:43]\n\t"
"ds_read_b64_tr_b16 v[226:227], v246 offset:16384\n\t"
"v_fma_f32 v46, v48, s52, v146\n\t"
"v_mfma_f32_16x16x32_bf16 v[40:43], v[248:251], a[184:187], v[40:43]\n\t"
"buffer_load_dwordx4 v[240:243], v252, s[8:11], s20 offen offset:1024 sc1\n\t"
"v_fma_f32 v145, -v145, s66, v47\n\t"
"v_fma_f32 v47, v49, s52, v147\n\t"
"v_fma_f32 v146, -v146, s66, v48\n\t"
"v_fma_f32 v147, -v147, s66, v49\n\t"
"v_mfma_f32_16x16x32_bf16 v[44:47], a[220:223], a[180:183], v[44:47]\n\t"
"s_waitcnt lgkmcnt(2)\n\t"
"v_fma_f32 v48, v50, s52, v148\n\t"
"v_fma_f32 v49, v51, s52, v149\n\t"
"v_mfma_f32_16x16x32_bf16 v[44:47], a[224:227], a[184:187], v[44:47]\n\t"
"v_fma_f32 v148, -v148, s66, v50\n\t"
"v_fma_f32 v50, v52, s52, v150\n\t"
"v_mfma_f32_16x16x32_bf16 v[44:47], v[248:251], a[188:191], v[44:47]\n\t"
"v_fma_f32 v149, -v149, s66, v51\n\t"
"v_fma_f32 v51, v53, s52, v151\n\t"
"v_fma_f32 v150, -v150, s66, v52\n\t"
"v_fma_f32 v151, -v151, s66, v53\n\t"
"v_mfma_f32_16x16x32_bf16 v[228:231], v[228:231], v[220:223], 0\n\t"
"v_fma_f32 v52, v54, s52, v152\n\t"
"v_fma_f32 v53, v55, s52, v153\n\t"
"v_mfma_f32_16x16x32_bf16 v[48:51], a[220:223], a[184:187], v[48:51]\n\t"
"v_fma_f32 v152, -v152, s66, v54\n\t"
"v_fma_f32 v54, v56, s52, v154\n\t"
"v_mfma_f32_16x16x32_bf16 v[48:51], a[224:227], a[188:191], v[48:51]\n\t"
"v_fma_f32 v153, -v153, s66, v55\n\t"
"v_fma_f32 v55, v57, s52, v155\n\t"
"v_mfma_f32_16x16x32_bf16 v[48:51], v[248:251], a[192:195], v[48:51]\n\t"
"v_fma_f32 v154, -v154, s66, v56\n\t"
"v_fma_f32 v155, -v155, s66, v57\n\t"
"v_cvt_pk_bf16_f32 v232, v232, v233\n\t"
"v_cvt_pk_bf16_f32 v233, v234, v235\n\t"
"v_fma_f32 v56, v58, s52, v156\n\t"
"v_fma_f32 v57, v59, s52, v157\n\t"
"v_mfma_f32_16x16x32_bf16 v[52:55], a[220:223], a[188:191], v[52:55]\n\t"
"v_fma_f32 v156, -v156, s66, v58\n\t"
"v_fma_f32 v58, v60, s52, v158\n\t"
"v_mfma_f32_16x16x32_bf16 v[52:55], a[224:227], a[192:195], v[52:55]\n\t"
"v_fma_f32 v157, -v157, s66, v59\n\t"
"v_fma_f32 v59, v61, s52, v159\n\t"
"v_mfma_f32_16x16x32_bf16 v[52:55], v[248:251], a[196:199], v[52:55]\n\t"
"v_fma_f32 v158, -v158, s66, v60\n\t"
"v_fma_f32 v159, -v159, s66, v61\n\t"
"v_mfma_f32_16x16x32_bf16 v[56:59], a[220:223], a[192:195], v[56:59]\n\t"
"s_waitcnt lgkmcnt(0)\n\t"
"v_fma_f32 v60, v62, s52, v160\n\t"
"v_fma_f32 v61, v63, s52, v161\n\t"
"v_mfma_f32_16x16x32_bf16 v[56:59], a[224:227], a[196:199], v[56:59]\n\t"
"v_fma_f32 v160, -v160, s66, v62\n\t"
"v_fma_f32 v62, v64, s52, v162\n\t"
"v_mfma_f32_16x16x32_bf16 v[56:59], v[248:251], a[200:203], v[56:59]\n\t"
"v_fma_f32 v161, -v161, s66, v63\n\t"
"v_fma_f32 v63, v65, s52, v163\n\t"
"v_fma_f32 v162, -v162, s66, v64\n\t"
"v_fma_f32 v163, -v163, s66, v65\n\t"
"v_cvt_pk_bf16_f32 v234, v228, v229\n\t"
"v_cvt_pk_bf16_f32 v235, v230, v231\n\t"
"v_mfma_f32_16x16x32_bf16 v[224:227], v[224:227], v[220:223], 0\n\t"
"ds_read_b64_tr_b16 v[228:229], v246 offset:20992\n\t"
"v_fma_f32 v64, v66, s52, v164\n\t"
"v_fma_f32 v65, v67, s52, v165\n\t"
"v_mfma_f32_16x16x32_bf16 v[60:63], a[220:223], a[196:199], v[60:63]\n\t"
"ds_read_b64_tr_b16 v[230:231], v246 offset:24064\n\t"
"v_fma_f32 v164, -v164, s66, v66\n\t"
"v_mfma_f32_16x16x32_bf16 v[60:63], a[224:227], a[200:203], v[60:63]\n\t"
"v_fma_f32 v66, v68, s52, v166\n\t"
"v_fma_f32 v165, -v165, s66, v67\n\t"
"v_mfma_f32_16x16x32_bf16 v[60:63], v[248:251], a[204:207], v[60:63]\n\t"
"v_fma_f32 v67, v69, s52, v167\n\t"
"v_fma_f32 v166, -v166, s66, v68\n\t"
"v_fma_f32 v167, -v167, s66, v69\n\t"
"v_fma_f32 v68, v70, s52, v168\n\t"
"v_fma_f32 v69, v71, s52, v169\n\t"
"s_waitcnt vmcnt(11)\n\t"
"v_mfma_f32_16x16x32_bf16 v[64:67], a[228:231], a[168:171], v[64:67]\n\t"
"ds_read_b128 a[168:171], v244 offset:33792\n\t"
"v_fma_f32 v168, -v168, s66, v70\n\t"
"v_mfma_f32_16x16x32_bf16 v[64:67], a[236:239], a[172:175], v[64:67]\n\t"
"v_fma_f32 v70, v72, s52, v170\n\t"
"v_fma_f32 v169, -v169, s66, v71\n\t"
"v_mfma_f32_16x16x32_bf16 v[64:67], a[240:243], a[176:179], v[64:67]\n\t"
"v_fma_f32 v71, v73, s52, v171\n\t"
"v_fma_f32 v170, -v170, s66, v72\n\t"
"v_fma_f32 v171, -v171, s66, v73\n\t"
"v_mfma_f32_16x16x32_bf16 v[68:71], a[228:231], a[172:175], v[68:71]\n\t"
"s_waitcnt lgkmcnt(1)\n\t"
"ds_read_b128 a[172:175], v244 offset:34304\n\t"
"v_fma_f32 v72, v74, s52, v172\n\t"
"v_mfma_f32_16x16x32_bf16 v[68:71], a[236:239], a[176:179], v[68:71]\n\t"
"v_fma_f32 v73, v75, s52, v173\n\t"
"v_fma_f32 v172, -v172, s66, v74\n\t"
"v_mfma_f32_16x16x32_bf16 v[68:71], a[240:243], a[180:183], v[68:71]\n\t"
"v_fma_f32 v74, v76, s52, v174\n\t"
"v_fma_f32 v173, -v173, s66, v75\n\t"
"v_fma_f32 v75, v77, s52, v175\n\t"
"v_fma_f32 v174, -v174, s66, v76\n\t"
"v_fma_f32 v175, -v175, s66, v77\n\t"
"v_cvt_pk_bf16_f32 v224, v224, v225\n\t"
"v_cvt_pk_bf16_f32 v225, v226, v227\n\t"
"v_mfma_f32_16x16x32_bf16 v[228:231], v[228:231], v[220:223], 0\n\t"
"buffer_load_dwordx4 a[208:211], v252, s[8:11], s20 offen offset:2048 sc1\n\t"
"v_fma_f32 v76, v78, s52, v176\n\t"
"v_fma_f32 v77, v79, s52, v177\n\t"
"s_mov_b32 s20, 98304\n\t"
"v_mfma_f32_16x16x32_bf16 v[72:75], a[228:231], a[176:179], v[72:75]\n\t"
"buffer_load_dwordx4 v[236:239], v252, s[8:11], s20 offen offset:0 sc1\n\t"
"v_mfma_f32_16x16x32_bf16 v[72:75], a[236:239], a[180:183], v[72:75]\n\t"
"ds_read_b128 a[176:179], v244 offset:34816\n\t"
"v_fma_f32 v176, -v176, s66, v78\n\t"
"v_mfma_f32_16x16x32_bf16 v[72:75], a[240:243], a[184:187], v[72:75]\n\t"
"v_fma_f32 v78, v80, s52, v178\n\t"
"v_fma_f32 v177, -v177, s66, v79\n\t"
"v_fma_f32 v79, v81, s52, v179\n\t"
"v_fma_f32 v178, -v178, s66, v80\n\t"
"v_fma_f32 v179, -v179, s66, v81\n\t"
"v_fma_f32 v80, v82, s52, v180\n\t"
"v_fma_f32 v81, v83, s52, v181\n\t"
"v_mfma_f32_16x16x32_bf16 v[76:79], a[228:231], a[180:183], v[76:79]\n\t"
"ds_read_b128 a[180:183], v244 offset:35328\n\t"
"v_fma_f32 v180, -v180, s66, v82\n\t"
"v_mfma_f32_16x16x32_bf16 v[76:79], a[236:239], a[184:187], v[76:79]\n\t"
"v_fma_f32 v82, v84, s52, v182\n\t"
"v_fma_f32 v181, -v181, s66, v83\n\t"
"v_mfma_f32_16x16x32_bf16 v[76:79], a[240:243], a[188:191], v[76:79]\n\t"
"buffer_load_dwordx4 v[248:251], v252, s[8:11], s20 offen offset:1024 sc1\n\t"
"v_fma_f32 v83, v85, s52, v183\n\t"
"v_fma_f32 v182, -v182, s66, v84\n\t"
"v_fma_f32 v183, -v183, s66, v85\n\t"
"v_fma_f32 v84, v86, s52, v184\n\t"
"v_fma_f32 v85, v87, s52, v185\n\t"
"v_mfma_f32_16x16x32_bf16 v[80:83], a[228:231], a[184:187], v[80:83]\n\t"
"ds_read_b128 a[184:187], v244 offset:35840\n\t"
"v_fma_f32 v184, -v184, s66, v86\n\t"
"v_mfma_f32_16x16x32_bf16 v[80:83], a[236:239], a[188:191], v[80:83]\n\t"
"v_fma_f32 v86, v88, s52, v186\n\t"
"v_fma_f32 v185, -v185, s66, v87\n\t"
"v_mfma_f32_16x16x32_bf16 v[80:83], a[240:243], a[192:195], v[80:83]\n\t"
"buffer_load_dwordx4 a[212:215], v252, s[8:11], s20 offen offset:2048 sc1\n\t"
"v_fma_f32 v87, v89, s52, v187\n\t"
"v_fma_f32 v186, -v186, s66, v88\n\t"
"v_fma_f32 v187, -v187, s66, v89\n\t"
"v_cvt_pk_bf16_f32 v226, v228, v229\n\t"
"v_cvt_pk_bf16_f32 v227, v230, v231\n\t"
"v_fma_f32 v88, v90, s52, v188\n\t"
"v_fma_f32 v89, v91, s52, v189\n\t"
"s_mov_b32 s20, 331776\n\t"
"v_mfma_f32_16x16x32_bf16 v[84:87], a[228:231], a[188:191], v[84:87]\n\t"
"ds_read_b64_tr_b16 v[228:229], v246 offset:27136\n\t"
"v_fma_f32 v188, -v188, s66, v90\n\t"
"v_mfma_f32_16x16x32_bf16 v[84:87], a[236:239], a[192:195], v[84:87]\n\t"
"ds_read_b64_tr_b16 v[230:231], v246 offset:30208\n\t"
"v_fma_f32 v90, v92, s52, v190\n\t"
"v_mfma_f32_16x16x32_bf16 v[84:87], a[240:243], a[196:199], v[84:87]\n\t"
"buffer_load_dwordx4 a[216:219], v252, s[8:11], s20 offen offset:0 sc1\n\t"
"v_fma_f32 v189, -v189, s66, v91\n\t"
"v_fma_f32 v91, v93, s52, v191\n\t"
"v_fma_f32 v190, -v190, s66, v92\n\t"
"v_fma_f32 v191, -v191, s66, v93\n\t"
"v_fma_f32 v92, v94, s52, v192\n\t"
"v_fma_f32 v93, v95, s52, v193\n\t"
"v_mfma_f32_16x16x32_bf16 v[88:91], a[228:231], a[192:195], v[88:91]\n\t"
"ds_read_b128 a[188:191], v244 offset:36352\n\t"
"v_fma_f32 v192, -v192, s66, v94\n\t"
"v_mfma_f32_16x16x32_bf16 v[88:91], a[236:239], a[196:199], v[88:91]\n\t"
"ds_read_b128 a[192:195], v244 offset:36864\n\t"
"v_fma_f32 v94, v96, s52, v194\n\t"
"v_mfma_f32_16x16x32_bf16 v[88:91], a[240:243], a[200:203], v[88:91]\n\t"
"buffer_load_dwordx4 a[220:223], v252, s[8:11], s20 offen offset:1024 sc1\n\t"
"v_fma_f32 v193, -v193, s66, v95\n\t"
"v_fma_f32 v95, v97, s52, v195\n\t"
"v_fma_f32 v194, -v194, s66, v96\n\t"
"v_fma_f32 v195, -v195, s66, v97\n\t"
"v_mfma_f32_16x16x32_bf16 v[92:95], a[228:231], a[196:199], v[92:95]\n\t"
"s_waitcnt lgkmcnt(2)\n\t"
"ds_read_b128 a[196:199], v244 offset:37376\n\t"
"v_mfma_f32_16x16x32_bf16 v[92:95], a[236:239], a[200:203], v[92:95]\n\t"
"buffer_load_dwordx4 v[96:99], v252, s[8:11], s20 offen offset:2048 sc1\n\t"
"v_mfma_f32_16x16x32_bf16 v[92:95], a[240:243], a[204:207], v[92:95]\n\t"
"s_waitcnt vmcnt(15)\n\t"
"ds_read_b128 a[200:203], v244 offset:37888\n\t"
"s_mov_b32 s20, 344064\n\t"
"v_mfma_f32_16x16x32_bf16 v[228:231], v[228:231], v[220:223], 0\n\t"
"ds_read_b128 a[204:207], v244 offset:38400\n\t"
"v_mfma_f32_16x16x32_bf16 v[100:103], a[244:247], a[168:171], v[100:103]\n\t"
"buffer_load_dwordx4 a[224:227], v252, s[8:11], s20 offen offset:0 sc1\n\t"
"v_mfma_f32_16x16x32_bf16 v[100:103], a[248:251], a[172:175], v[100:103]\n\t"
"buffer_load_dwordx4 a[228:231], v252, s[8:11], s20 offen offset:1024 sc1\n\t"
"v_mfma_f32_16x16x32_bf16 v[100:103], a[252:255], a[176:179], v[100:103]\n\t"
"v_mfma_f32_16x16x32_bf16 v[104:107], a[244:247], a[172:175], v[104:107]\n\t"
"v_mfma_f32_16x16x32_bf16 v[104:107], a[248:251], a[176:179], v[104:107]\n\t"
"v_cvt_pk_bf16_f32 v228, v228, v229\n\t"
"v_mfma_f32_16x16x32_bf16 v[104:107], a[252:255], a[180:183], v[104:107]\n\t"
"s_waitcnt lgkmcnt(0)\n\t"
"s_barrier\n\t"
"v_mfma_f32_16x16x32_bf16 v[108:111], a[244:247], a[176:179], v[108:111]\n\t"
"v_cvt_pk_bf16_f32 v229, v230, v231\n\t"
"v_mfma_f32_16x16x32_bf16 v[108:111], a[248:251], a[180:183], v[108:111]\n\t"
"buffer_load_dwordx4 a[236:239], v252, s[8:11], s20 offen offset:2048 sc1\n\t"
"v_mfma_f32_16x16x32_bf16 v[108:111], a[252:255], a[184:187], v[108:111]\n\t"
"s_mov_b32 s20, 356352\n\t"
"ds_write2st64_b64 v245, v[232:233], v[234:235] offset0:48 offset1:49\n\t"
"v_mfma_f32_16x16x32_bf16 v[112:115], a[244:247], a[180:183], v[112:115]\n\t"
"buffer_load_dwordx4 a[240:243], v252, s[8:11], s20 offen offset:0 sc1\n\t"
"v_mfma_f32_16x16x32_bf16 v[112:115], a[248:251], a[184:187], v[112:115]\n\t"
"ds_read_b64_tr_b16 v[232:233], v246 offset:33280\n\t"
"v_mfma_f32_16x16x32_bf16 v[112:115], a[252:255], a[188:191], v[112:115]\n\t"
"ds_write2st64_b64 v245, v[224:225], v[226:227] offset0:50 offset1:51\n\t"
"v_mfma_f32_16x16x32_bf16 v[116:119], a[244:247], a[184:187], v[116:119]\n\t"
"v_mfma_f32_16x16x32_bf16 v[116:119], a[248:251], a[188:191], v[116:119]\n\t"
"ds_read_b64_tr_b16 v[234:235], v246 offset:36352\n\t"
"v_mfma_f32_16x16x32_bf16 v[116:119], a[252:255], a[192:195], v[116:119]\n\t"
"ds_write_b64 v245, v[228:229] offset:26624\n\t"
"v_mfma_f32_16x16x32_bf16 v[120:123], a[244:247], a[188:191], v[120:123]\n\t"
"ds_read_b64_tr_b16 v[224:225], v246 offset:40960\n\t"
"v_mfma_f32_16x16x32_bf16 v[120:123], a[248:251], a[192:195], v[120:123]\n\t"
"ds_read_b64_tr_b16 v[226:227], v246 offset:44032\n\t"
"v_mfma_f32_16x16x32_bf16 v[120:123], a[252:255], a[196:199], v[120:123]\n\t"
"s_waitcnt lgkmcnt(3)\n\t"
"ds_read_b64_tr_b16 v[228:229], v246 offset:47104\n\t"
"v_mfma_f32_16x16x32_bf16 v[232:235], v[232:235], v[220:223], 0\n\t"
"ds_read_b64_tr_b16 v[230:231], v246 offset:50176\n\t"
"v_mfma_f32_16x16x32_bf16 v[124:127], a[244:247], a[192:195], v[124:127]\n\t"
"v_mfma_f32_16x16x32_bf16 v[124:127], a[248:251], a[196:199], v[124:127]\n\t"
"v_mfma_f32_16x16x32_bf16 v[124:127], a[252:255], a[200:203], v[124:127]\n\t"
"s_waitcnt lgkmcnt(2)\n\t"
"v_mfma_f32_16x16x32_bf16 v[224:227], v[224:227], v[220:223], 0\n\t"
"v_mfma_f32_16x16x32_bf16 v[128:131], a[244:247], a[196:199], v[128:131]\n\t"
"buffer_load_dwordx4 a[244:247], v252, s[8:11], s20 offen offset:1024 sc1\n\t"
"v_mfma_f32_16x16x32_bf16 v[128:131], a[248:251], a[200:203], v[128:131]\n\t"
"s_waitcnt vmcnt(11) lgkmcnt(0)\n\t"
"v_cvt_pk_bf16_f32 v232, v232, v233\n\t"
"v_cvt_pk_bf16_f32 v233, v234, v235\n\t"
"v_mfma_f32_16x16x32_bf16 v[128:131], a[252:255], a[204:207], v[128:131]\n\t"
"v_mfma_f32_16x16x32_bf16 v[228:231], v[228:231], v[220:223], 0\n\t"
"buffer_load_dwordx4 a[248:251], v252, s[8:11], s20 offen offset:2048 sc1\n\t"
"v_mfma_f32_16x16x32_bf16 v[132:135], a[232:235], a[168:171], v[132:135]\n\t"
"s_mov_b32 s20, 368640\n\t"
"v_cvt_pk_bf16_f32 v234, v224, v225\n\t"
"v_cvt_pk_bf16_f32 v235, v226, v227\n\t"
"v_mfma_f32_16x16x32_bf16 v[132:135], v[240:243], a[172:175], v[132:135]\n\t"
"ds_read_b64_tr_b16 v[224:225], v246 offset:53248\n\t"
"v_mfma_f32_16x16x32_bf16 v[132:135], a[208:211], a[176:179], v[132:135]\n\t"
"buffer_load_dwordx4 a[252:255], v252, s[8:11], s20 offen offset:0 sc1\n\t"
"v_mfma_f32_16x16x32_bf16 v[136:139], a[232:235], a[172:175], v[136:139]\n\t"
"ds_read_b64_tr_b16 v[226:227], v246 offset:56320\n\t"
"v_mfma_f32_16x16x32_bf16 v[136:139], v[240:243], a[176:179], v[136:139]\n\t"
"v_cvt_pk_bf16_f32 v228, v228, v229\n\t"
"v_cvt_pk_bf16_f32 v229, v230, v231\n\t"
"v_mfma_f32_16x16x32_bf16 v[136:139], a[208:211], a[180:183], v[136:139]\n\t"
"ds_write2st64_b64 v245, v[232:233], v[234:235] offset0:53 offset1:54\n\t"
"v_mfma_f32_16x16x32_bf16 v[140:143], a[232:235], a[176:179], v[140:143]\n\t"
"v_mfma_f32_16x16x32_bf16 v[140:143], v[240:243], a[180:183], v[140:143]\n\t"
"v_mfma_f32_16x16x32_bf16 v[140:143], a[208:211], a[184:187], v[140:143]\n\t"
"v_mfma_f32_16x16x32_bf16 v[144:147], a[232:235], a[180:183], v[144:147]\n\t"
"v_mfma_f32_16x16x32_bf16 v[144:147], v[240:243], a[184:187], v[144:147]\n\t"
"v_mfma_f32_16x16x32_bf16 v[144:147], a[208:211], a[188:191], v[144:147]\n\t"
"v_mfma_f32_16x16x32_bf16 v[148:151], a[232:235], a[184:187], v[148:151]\n\t"
"s_waitcnt lgkmcnt(1)\n\t"
"s_add_u32 m0, s24, 25984\n\t"
"s_add_u32 s28, s21, 786432\n\t"
"v_mfma_f32_16x16x32_bf16 v[148:151], v[240:243], a[188:191], v[148:151]\n\t"
"v_mfma_f32_16x16x32_bf16 v[148:151], a[208:211], a[192:195], v[148:151]\n\t"
"v_mfma_f32_16x16x32_bf16 v[224:227], v[224:227], v[220:223], 0\n\t"
"v_mfma_f32_16x16x32_bf16 v[152:155], a[232:235], a[188:191], v[152:155]\n\t"
"buffer_load_dwordx4 v247, s[12:15], s28 offen offset:128 nt lds\n\t"
"v_mfma_f32_16x16x32_bf16 v[152:155], v[240:243], a[192:195], v[152:155]\n\t"
"s_add_u32 m0, s24, 72064\n\t"
"s_add_u32 s28, s21, 2162688\n\t"
"v_mfma_f32_16x16x32_bf16 v[152:155], a[208:211], a[196:199], v[152:155]\n\t"
"buffer_load_dwordx4 v247, s[12:15], s28 offen offset:128 nt lds\n\t"
"v_mfma_f32_16x16x32_bf16 v[156:159], a[232:235], a[192:195], v[156:159]\n\t"
"s_add_u32 m0, s24, 79744\n\t"
"s_add_u32 s28, s21, 2359296\n\t"
"v_mfma_f32_16x16x32_bf16 v[156:159], v[240:243], a[196:199], v[156:159]\n\t"
"buffer_load_dwordx4 v247, s[12:15], s28 offen offset:128 nt lds\n\t"
"v_mfma_f32_16x16x32_bf16 v[156:159], a[208:211], a[200:203], v[156:159]\n\t"
"s_add_u32 m0, s24, 85888\n\t"
"s_add_u32 s28, s21, 2555904\n\t"
"v_cvt_pk_bf16_f32 v230, v224, v225\n\t"
"v_cvt_pk_bf16_f32 v231, v226, v227\n\t"
"v_mfma_f32_16x16x32_bf16 v[160:163], a[232:235], a[196:199], v[160:163]\n\t"
"s_waitcnt vmcnt(13)\n\t"
"v_mfma_f32_16x16x32_bf16 v[160:163], v[240:243], a[200:203], v[160:163]\n\t"
"buffer_load_dwordx4 v247, s[12:15], s28 offen offset:128 nt lds\n\t"
"v_mfma_f32_16x16x32_bf16 v[160:163], a[208:211], a[204:207], v[160:163]\n\t"
"s_add_u32 m0, s25, -128\n\t"
"ds_write2st64_b64 v245, v[228:229], v[230:231] offset0:55 offset1:56\n\t"
"v_mfma_f32_16x16x32_bf16 v[164:167], v[236:239], a[168:171], v[164:167]\n\t"
"buffer_load_dwordx4 v254, s[12:15], 0 offen offset:128 nt lds\n\t"
"v_mfma_f32_16x16x32_bf16 v[164:167], v[248:251], a[172:175], v[164:167]\n\t"
"s_add_u32 m0, s26, -128\n\t"
"v_mfma_f32_16x16x32_bf16 v[164:167], a[212:215], a[176:179], v[164:167]\n\t"
"s_waitcnt lgkmcnt(0)\n\t"
"s_barrier\n\t"
"v_mfma_f32_16x16x32_bf16 v[168:171], v[236:239], a[172:175], v[168:171]\n\t"
"ds_read_b128 a[168:171], v244 offset:24576\n\t"
"v_mfma_f32_16x16x32_bf16 v[168:171], v[248:251], a[176:179], v[168:171]\n\t"
"ds_read_b128 a[172:175], v244 offset:25088\n\t"
"v_mfma_f32_16x16x32_bf16 v[168:171], a[212:215], a[180:183], v[168:171]\n\t"
"s_mov_b64 exec, s[36:37]\n\t"
"buffer_load_dwordx4 v254, s[12:15], s27 offen offset:128 nt lds\n\t"
"s_mov_b64 exec, -1\n\t"
"v_mfma_f32_16x16x32_bf16 v[172:175], v[236:239], a[176:179], v[172:175]\n\t"
"ds_read_b128 a[176:179], v244 offset:25600\n\t"
"v_mfma_f32_16x16x32_bf16 v[172:175], v[248:251], a[180:183], v[172:175]\n\t"
"v_mfma_f32_16x16x32_bf16 v[172:175], a[212:215], a[184:187], v[172:175]\n\t"
"v_mfma_f32_16x16x32_bf16 v[176:179], v[236:239], a[180:183], v[176:179]\n\t"
"ds_read_b128 a[180:183], v244 offset:26112\n\t"
"v_mfma_f32_16x16x32_bf16 v[176:179], v[248:251], a[184:187], v[176:179]\n\t"
"v_mfma_f32_16x16x32_bf16 v[176:179], a[212:215], a[188:191], v[176:179]\n\t"
"v_mfma_f32_16x16x32_bf16 v[180:183], v[236:239], a[184:187], v[180:183]\n\t"
"ds_read_b128 a[184:187], v244 offset:26624\n\t"
"v_mfma_f32_16x16x32_bf16 v[180:183], v[248:251], a[188:191], v[180:183]\n\t"
"v_mfma_f32_16x16x32_bf16 v[180:183], a[212:215], a[192:195], v[180:183]\n\t"
"v_mfma_f32_16x16x32_bf16 v[184:187], v[236:239], a[188:191], v[184:187]\n\t"
"ds_read_b128 a[188:191], v244 offset:27136\n\t"
"v_mfma_f32_16x16x32_bf16 v[184:187], v[248:251], a[192:195], v[184:187]\n\t"
"v_mfma_f32_16x16x32_bf16 v[184:187], a[212:215], a[196:199], v[184:187]\n\t"
"v_mfma_f32_16x16x32_bf16 v[188:191], v[236:239], a[192:195], v[188:191]\n\t"
"ds_read_b128 a[192:195], v244 offset:27648\n\t"
"v_mfma_f32_16x16x32_bf16 v[188:191], v[248:251], a[196:199], v[188:191]\n\t"
"v_mfma_f32_16x16x32_bf16 v[188:191], a[212:215], a[200:203], v[188:191]\n\t"
"v_mfma_f32_16x16x32_bf16 v[192:195], v[236:239], a[196:199], v[192:195]\n\t"
"s_waitcnt vmcnt(13) lgkmcnt(4)\n\t"
"ds_read_b128 a[196:199], v244 offset:28160\n\t"
"v_mfma_f32_16x16x32_bf16 v[192:195], v[248:251], a[200:203], v[192:195]\n\t"
"ds_read_b128 a[200:203], v244 offset:28672\n\t"
"v_mfma_f32_16x16x32_bf16 v[192:195], a[212:215], a[204:207], v[192:195]\n\t"
"ds_read_b128 a[204:207], v244 offset:29184\n\t"
"Lst22_%=:\n\t"
"v_mfma_f32_16x16x32_bf16 v[0:3], a[216:219], a[168:171], v[0:3]\n\t"
"s_waitcnt lgkmcnt(6)\n\t"
"v_xor_b32 v246, 0xc0, v246\n\t"
"v_mfma_f32_16x16x32_bf16 v[0:3], a[220:223], a[172:175], v[0:3]\n\t"
"v_mfma_f32_16x16x32_bf16 v[0:3], v[96:99], a[176:179], v[0:3]\n\t"
"v_mfma_f32_16x16x32_bf16 v[4:7], a[216:219], a[172:175], v[4:7]\n\t"
"s_waitcnt lgkmcnt(5)\n\t"
"v_mfma_f32_16x16x32_bf16 v[4:7], a[220:223], a[176:179], v[4:7]\n\t"
"s_waitcnt lgkmcnt(3)\n\t"
"v_mfma_f32_16x16x32_bf16 v[4:7], v[96:99], a[180:183], v[4:7]\n\t"
"s_waitcnt lgkmcnt(2)\n\t"
"v_mfma_f32_16x16x32_bf16 v[8:11], a[216:219], a[176:179], v[8:11]\n\t"
"buffer_load_dwordx4 v[240:243], v252, s[8:11], s20 offen offset:1024 sc1\n\t"
"v_mfma_f32_16x16x32_bf16 v[8:11], a[220:223], a[180:183], v[8:11]\n\t"
"Lpg17_%=:\n\t"
"s_cbranch_vccnz Lvr2_%=\n\t"
"v_mfma_f32_16x16x32_bf16 v[8:11], v[96:99], a[184:187], v[8:11]\n\t"
"v_mfma_f32_16x16x32_bf16 v[12:15], a[216:219], a[180:183], v[12:15]\n\t"
"v_mfma_f32_16x16x32_bf16 v[12:15], a[220:223], a[184:187], v[12:15]\n\t"
"v_mfma_f32_16x16x32_bf16 v[12:15], v[96:99], a[188:191], v[12:15]\n\t"
"v_mfma_f32_16x16x32_bf16 v[16:19], a[216:219], a[184:187], v[16:19]\n\t"
"v_mfma_f32_16x16x32_bf16 v[16:19], a[220:223], a[188:191], v[16:19]\n\t"
"v_mfma_f32_16x16x32_bf16 v[16:19], v[96:99], a[192:195], v[16:19]\n\t"
"ds_read_b64_tr_b16 v[232:233], v246 offset:1024\n\t"
"v_mfma_f32_16x16x32_bf16 v[20:23], a[216:219], a[188:191], v[20:23]\n\t"
"s_waitcnt lgkmcnt(2)\n\t"
"ds_read_b64_tr_b16 v[234:235], v246 offset:4096\n\t"
"v_mfma_f32_16x16x32_bf16 v[20:23], a[220:223], a[192:195], v[20:23]\n\t"
"v_mfma_f32_16x16x32_bf16 v[20:23], v[96:99], a[196:199], v[20:23]\n\t"
"v_mfma_f32_16x16x32_bf16 v[24:27], a[216:219], a[192:195], v[24:27]\n\t"
"s_waitcnt lgkmcnt(2)\n\t"
"v_mfma_f32_16x16x32_bf16 v[24:27], a[220:223], a[196:199], v[24:27]\n\t"
"v_mfma_f32_16x16x32_bf16 v[24:27], v[96:99], a[200:203], v[24:27]\n\t"
"v_mfma_f32_16x16x32_bf16 v[28:31], a[216:219], a[196:199], v[28:31]\n\t"
"s_waitcnt vmcnt(11)\n\t"
"ds_read_b64_tr_b16 v[224:225], v246 offset:7168\n\t"
"v_mfma_f32_16x16x32_bf16 v[28:31], a[220:223], a[200:203], v[28:31]\n\t"
"ds_read_b64_tr_b16 v[226:227], v246 offset:10240\n\t"
"v_mfma_f32_16x16x32_bf16 v[28:31], v[96:99], a[204:207], v[28:31]\n\t"
"v_mfma_f32_16x16x32_bf16 v[32:35], a[224:227], a[168:171], v[32:35]\n\t"
"v_mfma_f32_16x16x32_bf16 v[32:35], a[228:231], a[172:175], v[32:35]\n\t"
"v_mfma_f32_16x16x32_bf16 v[32:35], a[236:239], a[176:179], v[32:35]\n\t"
"v_mfma_f32_16x16x32_bf16 v[36:39], a[224:227], a[172:175], v[36:39]\n\t"
"s_waitcnt lgkmcnt(2)\n\t"
"v_mfma_f32_16x16x32_bf16 v[36:39], a[228:231], a[176:179], v[36:39]\n\t"
"v_mfma_f32_16x16x32_bf16 v[36:39], a[236:239], a[180:183], v[36:39]\n\t"
"v_mfma_f32_16x16x32_bf16 v[232:235], v[232:235], v[220:223], 0\n\t"
"v_mfma_f32_16x16x32_bf16 v[40:43], a[224:227], a[176:179], v[40:43]\n\t"
"ds_read_b64_tr_b16 v[228:229], v246 offset:13312\n\t"
"v_mfma_f32_16x16x32_bf16 v[40:43], a[228:231], a[180:183], v[40:43]\n\t"
"ds_read_b64_tr_b16 v[230:231], v246 offset:16384\n\t"
"v_mfma_f32_16x16x32_bf16 v[40:43], a[236:239], a[184:187], v[40:43]\n\t"
"v_mfma_f32_16x16x32_bf16 v[44:47], a[224:227], a[180:183], v[44:47]\n\t"
"s_waitcnt lgkmcnt(2)\n\t"
"v_mfma_f32_16x16x32_bf16 v[44:47], a[228:231], a[184:187], v[44:47]\n\t"
"v_cvt_pk_bf16_f32 v232, v232, v233\n\t"
"v_cvt_pk_bf16_f32 v233, v234, v235\n\t"
"v_mfma_f32_16x16x32_bf16 v[44:47], a[236:239], a[188:191], v[44:47]\n\t"
"v_mfma_f32_16x16x32_bf16 v[224:227], v[224:227], v[220:223], 0\n\t"
"buffer_load_dwordx4 a[208:211], v252, s[8:11], s20 offen offset:2048 sc1\n\t"
"v_mfma_f32_16x16x32_bf16 v[48:51], a[224:227], a[184:187], v[48:51]\n\t"
"s_mov_b32 s20, 380928\n\t"
"v_mfma_f32_16x16x32_bf16 v[48:51], a[228:231], a[188:191], v[48:51]\n\t"
"v_mfma_f32_16x16x32_bf16 v[48:51], a[236:239], a[192:195], v[48:51]\n\t"
"v_mfma_f32_16x16x32_bf16 v[52:55], a[224:227], a[188:191], v[52:55]\n\t"
"buffer_load_dwordx4 v[236:239], v252, s[8:11], s20 offen offset:0 sc1\n\t"
"v_mfma_f32_16x16x32_bf16 v[52:55], a[228:231], a[192:195], v[52:55]\n\t"
"v_cvt_pk_bf16_f32 v234, v224, v225\n\t"
"v_cvt_pk_bf16_f32 v235, v226, v227\n\t"
"v_mfma_f32_16x16x32_bf16 v[52:55], a[236:239], a[196:199], v[52:55]\n\t"
"ds_read_b64_tr_b16 v[224:225], v246 offset:20992\n\t"
"v_mfma_f32_16x16x32_bf16 v[56:59], a[224:227], a[192:195], v[56:59]\n\t"
"s_waitcnt lgkmcnt(1)\n\t"
"ds_read_b64_tr_b16 v[226:227], v246 offset:24064\n\t"
"v_mfma_f32_16x16x32_bf16 v[56:59], a[228:231], a[196:199], v[56:59]\n\t"
"buffer_load_dwordx4 v[96:99], v252, s[8:11], s20 offen offset:1024 sc1\n\t"
"v_mfma_f32_16x16x32_bf16 v[56:59], a[236:239], a[200:203], v[56:59]\n\t"
"v_mfma_f32_16x16x32_bf16 v[228:231], v[228:231], v[220:223], 0\n\t"
"v_mfma_f32_16x16x32_bf16 v[60:63], a[224:227], a[196:199], v[60:63]\n\t"
"s_waitcnt vmcnt(11)\n\t"
"v_mfma_f32_16x16x32_bf16 v[60:63], a[228:231], a[200:203], v[60:63]\n\t"
"buffer_load_dwordx4 v[248:251], v252, s[8:11], s20 offen offset:2048 sc1\n\t"
"v_mfma_f32_16x16x32_bf16 v[60:63], a[236:239], a[204:207], v[60:63]\n\t"
"s_mov_b32 s20, 393216\n\t"
"v_mfma_f32_16x16x32_bf16 v[64:67], a[240:243], a[168:171], v[64:67]\n\t"
"ds_read_b128 a[168:171], v244 offset:33792\n\t"
"v_mfma_f32_16x16x32_bf16 v[64:67], a[244:247], a[172:175], v[64:67]\n\t"
"v_cvt_pk_bf16_f32 v228, v228, v229\n\t"
"v_cvt_pk_bf16_f32 v229, v230, v231\n\t"
"v_mfma_f32_16x16x32_bf16 v[64:67], a[248:251], a[176:179], v[64:67]\n\t"
"v_mfma_f32_16x16x32_bf16 v[68:71], a[240:243], a[172:175], v[68:71]\n\t"
"buffer_load_dwordx4 a[212:215], v252, s[8:11], s20 offen offset:0 sc1\n\t"
"v_mfma_f32_16x16x32_bf16 v[68:71], a[244:247], a[176:179], v[68:71]\n\t"
"s_waitcnt lgkmcnt(1)\n\t"
"ds_read_b128 a[172:175], v244 offset:34304\n\t"
"v_mfma_f32_16x16x32_bf16 v[68:71], a[248:251], a[180:183], v[68:71]\n\t"
"v_mfma_f32_16x16x32_bf16 v[224:227], v[224:227], v[220:223], 0\n\t"
"buffer_load_dwordx4 a[216:219], v252, s[8:11], s20 offen offset:1024 sc1\n\t"
"v_mfma_f32_16x16x32_bf16 v[72:75], a[240:243], a[176:179], v[72:75]\n\t"
"ds_read_b128 a[176:179], v244 offset:34816\n\t"
"v_mfma_f32_16x16x32_bf16 v[72:75], a[244:247], a[180:183], v[72:75]\n\t"
"v_mfma_f32_16x16x32_bf16 v[72:75], a[248:251], a[184:187], v[72:75]\n\t"
"v_mfma_f32_16x16x32_bf16 v[76:79], a[240:243], a[180:183], v[76:79]\n\t"
"buffer_load_dwordx4 a[220:223], v252, s[8:11], s20 offen offset:2048 sc1\n\t"
"v_mfma_f32_16x16x32_bf16 v[76:79], a[244:247], a[184:187], v[76:79]\n\t"
"s_mov_b32 s20, 405504\n\t"
"ds_read_b128 a[180:183], v244 offset:35328\n\t"
"v_cvt_pk_bf16_f32 v230, v224, v225\n\t"
"v_mfma_f32_16x16x32_bf16 v[76:79], a[248:251], a[188:191], v[76:79]\n\t"
"v_cvt_pk_bf16_f32 v231, v226, v227\n\t"
"v_mfma_f32_16x16x32_bf16 v[80:83], a[240:243], a[184:187], v[80:83]\n\t"
"ds_read_b64_tr_b16 v[224:225], v246 offset:27136\n\t"
"v_mfma_f32_16x16x32_bf16 v[80:83], a[244:247], a[188:191], v[80:83]\n\t"
"ds_read_b64_tr_b16 v[226:227], v246 offset:30208\n\t"
"v_mfma_f32_16x16x32_bf16 v[80:83], a[248:251], a[192:195], v[80:83]\n\t"
"buffer_load_dwordx4 a[224:227], v252, s[8:11], s20 offen offset:0 sc1\n\t"
"v_mfma_f32_16x16x32_bf16 v[84:87], a[240:243], a[188:191], v[84:87]\n\t"
"ds_read_b128 a[184:187], v244 offset:35840\n\t"
"v_mfma_f32_16x16x32_bf16 v[84:87], a[244:247], a[192:195], v[84:87]\n\t"
"ds_read_b128 a[188:191], v244 offset:36352\n\t"
"v_mfma_f32_16x16x32_bf16 v[84:87], a[248:251], a[196:199], v[84:87]\n\t"
"buffer_load_dwordx4 a[228:231], v252, s[8:11], s20 offen offset:1024 sc1\n\t"
"v_mfma_f32_16x16x32_bf16 v[88:91], a[240:243], a[192:195], v[88:91]\n\t"
"ds_read_b128 a[192:195], v244 offset:36864\n\t"
"v_mfma_f32_16x16x32_bf16 v[88:91], a[244:247], a[196:199], v[88:91]\n\t"
"v_mfma_f32_16x16x32_bf16 v[88:91], a[248:251], a[200:203], v[88:91]\n\t"
"v_mfma_f32_16x16x32_bf16 v[92:95], a[240:243], a[196:199], v[92:95]\n\t"
"buffer_load_dwordx4 a[232:235], v252, s[8:11], s20 offen offset:2048 sc1\n\t"
"v_mfma_f32_16x16x32_bf16 v[92:95], a[244:247], a[200:203], v[92:95]\n\t"
"s_waitcnt vmcnt(9) lgkmcnt(3)\n\t"
"ds_read_b128 a[196:199], v244 offset:37376\n\t"
"s_mov_b32 s20, 417792\n\t"
"v_mfma_f32_16x16x32_bf16 v[92:95], a[248:251], a[204:207], v[92:95]\n\t"
"ds_read_b128 a[200:203], v244 offset:37888\n\t"
"v_mfma_f32_16x16x32_bf16 v[224:227], v[224:227], v[220:223], 0\n\t"
"ds_read_b128 a[204:207], v244 offset:38400\n\t"
"v_mfma_f32_16x16x32_bf16 v[100:103], a[252:255], a[168:171], v[100:103]\n\t"
"buffer_load_dwordx4 a[236:239], v252, s[8:11], s20 offen offset:0 sc1\n\t"
"v_mfma_f32_16x16x32_bf16 v[100:103], v[240:243], a[172:175], v[100:103]\n\t"
"v_mfma_f32_16x16x32_bf16 v[100:103], a[208:211], a[176:179], v[100:103]\n\t"
"v_mfma_f32_16x16x32_bf16 v[104:107], a[252:255], a[172:175], v[104:107]\n\t"
"buffer_load_dwordx4 a[240:243], v252, s[8:11], s20 offen offset:1024 sc1\n\t"
"v_mfma_f32_16x16x32_bf16 v[104:107], v[240:243], a[176:179], v[104:107]\n\t"
"s_waitcnt lgkmcnt(5)\n\t"
"v_cvt_pk_bf16_f32 v224, v224, v225\n\t"
"v_mfma_f32_16x16x32_bf16 v[104:107], a[208:211], a[180:183], v[104:107]\n\t"
"s_waitcnt lgkmcnt(0)\n\t"
"s_barrier\n\t"
"v_mfma_f32_16x16x32_bf16 v[108:111], a[252:255], a[176:179], v[108:111]\n\t"
"s_add_u32 m0, s24, 32128\n\t"
"s_add_u32 s28, s21, 983040\n\t"
"v_mfma_f32_16x16x32_bf16 v[108:111], v[240:243], a[180:183], v[108:111]\n\t"
"buffer_load_dwordx4 a[244:247], v252, s[8:11], s20 offen offset:2048 sc1\n\t"
"v_mfma_f32_16x16x32_bf16 v[108:111], a[208:211], a[184:187], v[108:111]\n\t"
"s_mov_b32 s20, 430080\n\t"
"ds_write2st64_b64 v245, v[232:233], v[234:235] offset0:48 offset1:49\n\t"
"v_cvt_pk_bf16_f32 v225, v226, v227\n\t"
"v_mfma_f32_16x16x32_bf16 v[112:115], a[252:255], a[180:183], v[112:115]\n\t"
"v_mfma_f32_16x16x32_bf16 v[112:115], v[240:243], a[184:187], v[112:115]\n\t"
"ds_read_b64_tr_b16 v[232:233], v246 offset:33280\n\t"
"v_mfma_f32_16x16x32_bf16 v[112:115], a[208:211], a[188:191], v[112:115]\n\t"
"ds_write2st64_b64 v245, v[228:229], v[230:231] offset0:50 offset1:51\n\t"
"v_mfma_f32_16x16x32_bf16 v[116:119], a[252:255], a[184:187], v[116:119]\n\t"
"buffer_load_dwordx4 v247, s[12:15], s28 offen offset:128 nt lds\n\t"
"v_mfma_f32_16x16x32_bf16 v[116:119], v[240:243], a[188:191], v[116:119]\n\t"
"s_add_u32 m0, s24, 39808\n\t"
"s_add_u32 s28, s21, 1179648\n\t"
"ds_read_b64_tr_b16 v[234:235], v246 offset:36352\n\t"
"v_mfma_f32_16x16x32_bf16 v[116:119], a[208:211], a[192:195], v[116:119]\n\t"
"ds_write_b64 v245, v[224:225] offset:26624\n\t"
"v_mfma_f32_16x16x32_bf16 v[120:123], a[252:255], a[188:191], v[120:123]\n\t"
"buffer_load_dwordx4 a[248:251], v252, s[8:11], s20 offen offset:0 sc1\n\t"
"v_mfma_f32_16x16x32_bf16 v[120:123], v[240:243], a[192:195], v[120:123]\n\t"
"buffer_load_dwordx4 v247, s[12:15], s28 offen offset:128 nt lds\n\t"
"v_mfma_f32_16x16x32_bf16 v[120:123], a[208:211], a[196:199], v[120:123]\n\t"
"s_waitcnt lgkmcnt(1)\n\t"
"ds_read_b64_tr_b16 v[228:229], v246 offset:40960\n\t"
"ds_read_b64_tr_b16 v[230:231], v246 offset:44032\n\t"
"s_add_u32 m0, s24, 45952\n\t"
"s_add_u32 s28, s21, 1376256\n\t"
"v_mfma_f32_16x16x32_bf16 v[232:235], v[232:235], v[220:223], 0\n\t"
"buffer_load_dwordx4 v247, s[12:15], s28 offen offset:128 nt lds\n\t"
"s_add_u32 m0, s24, 52096\n\t"
"s_add_u32 s28, s21, 1572864\n\t"
"v_mfma_f32_16x16x32_bf16 v[124:127], a[252:255], a[192:195], v[124:127]\n\t"
"buffer_load_dwordx4 v247, s[12:15], s28 offen offset:128 nt lds\n\t"
"v_mfma_f32_16x16x32_bf16 v[124:127], v[240:243], a[196:199], v[124:127]\n\t"
"s_add_u32 m0, s24, 92032\n\t"
"s_add_u32 s28, s21, 2752512\n\t"
"ds_read_b64_tr_b16 v[224:225], v246 offset:47104\n\t"
"v_mfma_f32_16x16x32_bf16 v[124:127], a[208:211], a[200:203], v[124:127]\n\t"
"s_waitcnt lgkmcnt(1)\n\t"
"ds_read_b64_tr_b16 v[226:227], v246 offset:50176\n\t"
"v_mfma_f32_16x16x32_bf16 v[228:231], v[228:231], v[220:223], 0\n\t"
"buffer_load_dwordx4 v247, s[12:15], s28 offen offset:128 nt lds\n\t"
"s_add_u32 m0, s24, 99712\n\t"
"s_add_u32 s28, s21, 2949120\n\t"
"v_mfma_f32_16x16x32_bf16 v[128:131], a[252:255], a[196:199], v[128:131]\n\t"
"buffer_load_dwordx4 v247, s[12:15], s28 offen offset:128 nt lds\n\t"
"v_mfma_f32_16x16x32_bf16 v[128:131], v[240:243], a[200:203], v[128:131]\n\t"
"s_waitcnt vmcnt(16)\n\t"
"s_add_u32 m0, s24, 105856\n\t"
"s_add_u32 s28, s21, 3145728\n\t"
"v_mfma_f32_16x16x32_bf16 v[128:131], a[208:211], a[204:207], v[128:131]\n\t"
"s_waitcnt lgkmcnt(0)\n\t"
"buffer_load_dwordx4 v247, s[12:15], s28 offen offset:128 nt lds\n\t"
"s_add_u32 m0, s24, 112000\n\t"
"s_add_u32 s28, s21, 3342336\n\t"
"v_mfma_f32_16x16x32_bf16 v[224:227], v[224:227], v[220:223], 0\n\t"
"buffer_load_dwordx4 v247, s[12:15], s28 offen offset:128 nt lds\n\t"
"v_cvt_pk_bf16_f32 v232, v232, v233\n\t"
"v_cvt_pk_bf16_f32 v233, v234, v235\n\t"
"v_mfma_f32_16x16x32_bf16 v[132:135], v[236:239], a[168:171], v[132:135]\n\t"
"v_cvt_pk_bf16_f32 v234, v228, v229\n\t"
"v_cvt_pk_bf16_f32 v235, v230, v231\n\t"
"v_mfma_f32_16x16x32_bf16 v[132:135], v[96:99], a[172:175], v[132:135]\n\t"
"ds_read_b64_tr_b16 v[228:229], v246 offset:53248\n\t"
"v_mfma_f32_16x16x32_bf16 v[132:135], v[248:251], a[176:179], v[132:135]\n\t"
"ds_write2st64_b64 v245, v[232:233], v[234:235] offset0:53 offset1:54\n\t"
"v_mfma_f32_16x16x32_bf16 v[136:139], v[236:239], a[172:175], v[136:139]\n\t"
"v_mfma_f32_16x16x32_bf16 v[136:139], v[96:99], a[176:179], v[136:139]\n\t"
"ds_read_b64_tr_b16 v[230:231], v246 offset:56320\n\t"
"v_mfma_f32_16x16x32_bf16 v[136:139], v[248:251], a[180:183], v[136:139]\n\t"
"v_cvt_pk_bf16_f32 v224, v224, v225\n\t"
"v_cvt_pk_bf16_f32 v225, v226, v227\n\t"
"v_mfma_f32_16x16x32_bf16 v[140:143], v[236:239], a[176:179], v[140:143]\n\t"
"v_mfma_f32_16x16x32_bf16 v[140:143], v[96:99], a[180:183], v[140:143]\n\t"
"v_mfma_f32_16x16x32_bf16 v[140:143], v[248:251], a[184:187], v[140:143]\n\t"
"v_mfma_f32_16x16x32_bf16 v[144:147], v[236:239], a[180:183], v[144:147]\n\t"
"v_mfma_f32_16x16x32_bf16 v[144:147], v[96:99], a[184:187], v[144:147]\n\t"
"v_mfma_f32_16x16x32_bf16 v[144:147], v[248:251], a[188:191], v[144:147]\n\t"
"v_mfma_f32_16x16x32_bf16 v[148:151], v[236:239], a[184:187], v[148:151]\n\t"
"s_waitcnt lgkmcnt(0)\n\t"
"v_mfma_f32_16x16x32_bf16 v[148:151], v[96:99], a[188:191], v[148:151]\n\t"
"v_mfma_f32_16x16x32_bf16 v[148:151], v[248:251], a[192:195], v[148:151]\n\t"
"v_mfma_f32_16x16x32_bf16 v[228:231], v[228:231], v[220:223], 0\n\t"
"v_mfma_f32_16x16x32_bf16 v[152:155], v[236:239], a[188:191], v[152:155]\n\t"
"v_mfma_f32_16x16x32_bf16 v[152:155], v[96:99], a[192:195], v[152:155]\n\t"
"v_mfma_f32_16x16x32_bf16 v[152:155], v[248:251], a[196:199], v[152:155]\n\t"
"v_mfma_f32_16x16x32_bf16 v[156:159], v[236:239], a[192:195], v[156:159]\n\t"
"v_mfma_f32_16x16x32_bf16 v[156:159], v[96:99], a[196:199], v[156:159]\n\t"
"v_cvt_pk_bf16_f32 v226, v228, v229\n\t"
"v_cvt_pk_bf16_f32 v227, v230, v231\n\t"
"v_mfma_f32_16x16x32_bf16 v[156:159], v[248:251], a[200:203], v[156:159]\n\t"
"ds_write2st64_b64 v245, v[224:225], v[226:227] offset0:55 offset1:56\n\t"
"v_mfma_f32_16x16x32_bf16 v[160:163], v[236:239], a[196:199], v[160:163]\n\t"
"s_waitcnt vmcnt(15)\n\t"
"v_mfma_f32_16x16x32_bf16 v[160:163], v[96:99], a[200:203], v[160:163]\n\t"
"v_mfma_f32_16x16x32_bf16 v[160:163], v[248:251], a[204:207], v[160:163]\n\t"
"v_mfma_f32_16x16x32_bf16 v[164:167], a[212:215], a[168:171], v[164:167]\n\t"
"v_mfma_f32_16x16x32_bf16 v[164:167], a[216:219], a[172:175], v[164:167]\n\t"
"v_mfma_f32_16x16x32_bf16 v[164:167], a[220:223], a[176:179], v[164:167]\n\t"
"s_waitcnt lgkmcnt(0)\n\t"
"s_barrier\n\t"
"v_mfma_f32_16x16x32_bf16 v[168:171], a[212:215], a[172:175], v[168:171]\n\t"
"ds_read_b128 a[168:171], v244 offset:24576\n\t"
"v_mfma_f32_16x16x32_bf16 v[168:171], a[216:219], a[176:179], v[168:171]\n\t"
"ds_read_b128 a[172:175], v244 offset:25088\n\t"
"v_mfma_f32_16x16x32_bf16 v[168:171], a[220:223], a[180:183], v[168:171]\n\t"
"v_mfma_f32_16x16x32_bf16 v[172:175], a[212:215], a[176:179], v[172:175]\n\t"
"ds_read_b128 a[176:179], v244 offset:25600\n\t"
"v_mfma_f32_16x16x32_bf16 v[172:175], a[216:219], a[180:183], v[172:175]\n\t"
"v_mfma_f32_16x16x32_bf16 v[172:175], a[220:223], a[184:187], v[172:175]\n\t"
"v_mfma_f32_16x16x32_bf16 v[176:179], a[212:215], a[180:183], v[176:179]\n\t"
"ds_read_b128 a[180:183], v244 offset:26112\n\t"
"v_mfma_f32_16x16x32_bf16 v[176:179], a[216:219], a[184:187], v[176:179]\n\t"
"v_mfma_f32_16x16x32_bf16 v[176:179], a[220:223], a[188:191], v[176:179]\n\t"
"v_mfma_f32_16x16x32_bf16 v[180:183], a[212:215], a[184:187], v[180:183]\n\t"
"ds_read_b128 a[184:187], v244 offset:26624\n\t"
"v_mfma_f32_16x16x32_bf16 v[180:183], a[216:219], a[188:191], v[180:183]\n\t"
"v_mfma_f32_16x16x32_bf16 v[180:183], a[220:223], a[192:195], v[180:183]\n\t"
"v_mfma_f32_16x16x32_bf16 v[184:187], a[212:215], a[188:191], v[184:187]\n\t"
"ds_read_b128 a[188:191], v244 offset:27136\n\t"
"v_mfma_f32_16x16x32_bf16 v[184:187], a[216:219], a[192:195], v[184:187]\n\t"
"v_mfma_f32_16x16x32_bf16 v[184:187], a[220:223], a[196:199], v[184:187]\n\t"
"v_mfma_f32_16x16x32_bf16 v[188:191], a[212:215], a[192:195], v[188:191]\n\t"
"ds_read_b128 a[192:195], v244 offset:27648\n\t"
"v_mfma_f32_16x16x32_bf16 v[188:191], a[216:219], a[196:199], v[188:191]\n\t"
"v_mfma_f32_16x16x32_bf16 v[188:191], a[220:223], a[200:203], v[188:191]\n\t"
"v_mfma_f32_16x16x32_bf16 v[192:195], a[212:215], a[196:199], v[192:195]\n\t"
"s_waitcnt vmcnt(12) lgkmcnt(4)\n\t"
"ds_read_b128 a[196:199], v244 offset:28160\n\t"
"v_mfma_f32_16x16x32_bf16 v[192:195], a[216:219], a[200:203], v[192:195]\n\t"
"ds_read_b128 a[200:203], v244 offset:28672\n\t"
"v_mfma_f32_16x16x32_bf16 v[192:195], a[220:223], a[204:207], v[192:195]\n\t"
"ds_read_b128 a[204:207], v244 offset:29184\n\t"
"Lst23_%=:\n\t"
"v_mfma_f32_16x16x32_bf16 v[196:199], a[224:227], a[168:171], v[196:199]\n\t"
"s_mov_b64 vcc, -1\n\t"
"s_bitcmp1_b32 s73, 1\n\t"
"s_cbranch_scc1 Lvh3_%=\n\t"
"s_bitcmp1_b32 s73, 0\n\t"
"s_cbranch_scc1 Lpg22_%=\n\t"
"s_branch Lpg21_%=\n\t"
"Lvh3_%=:\n\t"
"s_bitcmp1_b32 s73, 0\n\t"
"s_cbranch_scc1 Lpg24_%=\n\t"
"s_branch Lpg23_%=\n\t"
"Lvr3_%=:\n\t"
"s_mov_b64 vcc, 0\n\t"
"s_waitcnt lgkmcnt(6)\n\t"
"v_mfma_f32_16x16x32_bf16 v[196:199], a[228:231], a[172:175], v[196:199]\n\t"
"buffer_load_dwordx4 v[240:243], v252, s[8:11], s20 offen offset:1024 sc1\n\t"
"v_mfma_f32_16x16x32_bf16 v[196:199], a[232:235], a[176:179], v[196:199]\n\t"
"v_mfma_f32_16x16x32_bf16 v[204:207], a[224:227], a[172:175], v[204:207]\n\t"
"s_waitcnt lgkmcnt(5)\n\t"
"v_mfma_f32_16x16x32_bf16 v[204:207], a[228:231], a[176:179], v[204:207]\n\t"
"v_mfma_f32_16x16x32_bf16 v[204:207], a[232:235], a[180:183], v[204:207]\n\t"
"v_mfma_f32_16x16x32_bf16 v[212:215], a[224:227], a[176:179], v[212:215]\n\t"
"s_waitcnt lgkmcnt(4)\n\t"
"v_mfma_f32_16x16x32_bf16 v[212:215], a[228:231], a[180:183], v[212:215]\n\t"
"v_mfma_f32_16x16x32_bf16 v[212:215], a[232:235], a[184:187], v[212:215]\n\t"
"v_mfma_f32_16x16x32_bf16 a[0:3], a[224:227], a[180:183], a[0:3]\n\t"
"s_waitcnt lgkmcnt(3)\n\t"
"v_mfma_f32_16x16x32_bf16 a[0:3], a[228:231], a[184:187], a[0:3]\n\t"
"v_mfma_f32_16x16x32_bf16 a[0:3], a[232:235], a[188:191], a[0:3]\n\t"
"buffer_load_dwordx4 a[208:211], v252, s[8:11], s20 offen offset:2048 sc1\n\t"
"v_mfma_f32_16x16x32_bf16 a[8:11], a[224:227], a[184:187], a[8:11]\n\t"
"s_waitcnt lgkmcnt(2)\n\t"
"s_mov_b32 s20, 442368\n\t"
"v_mfma_f32_16x16x32_bf16 a[8:11], a[228:231], a[188:191], a[8:11]\n\t"
"v_mfma_f32_16x16x32_bf16 a[8:11], a[232:235], a[192:195], a[8:11]\n\t"
"ds_read_b64_tr_b16 v[232:233], v246 offset:0\n\t"
"v_mfma_f32_16x16x32_bf16 a[16:19], a[224:227], a[188:191], a[16:19]\n\t"
"buffer_load_dwordx4 v[236:239], v252, s[8:11], s20 offen offset:0 sc1\n\t"
"v_mfma_f32_16x16x32_bf16 a[16:19], a[228:231], a[192:195], a[16:19]\n\t"
"s_waitcnt lgkmcnt(2)\n\t"
"ds_read_b64_tr_b16 v[234:235], v246 offset:3072\n\t"
"v_mfma_f32_16x16x32_bf16 a[16:19], a[232:235], a[196:199], a[16:19]\n\t"
"v_mfma_f32_16x16x32_bf16 a[24:27], a[224:227], a[192:195], a[24:27]\n\t"
"s_waitcnt lgkmcnt(2)\n\t"
"v_mfma_f32_16x16x32_bf16 a[24:27], a[228:231], a[196:199], a[24:27]\n\t"
"buffer_load_dwordx4 v[96:99], v252, s[8:11], s20 offen offset:1024 sc1\n\t"
"v_mfma_f32_16x16x32_bf16 a[24:27], a[232:235], a[200:203], a[24:27]\n\t"
"v_mfma_f32_16x16x32_bf16 a[32:35], a[224:227], a[196:199], a[32:35]\n\t"
"s_waitcnt vmcnt(13)\n\t"
"ds_read_b64_tr_b16 v[228:229], v246 offset:6144\n\t"
"v_mfma_f32_16x16x32_bf16 a[32:35], a[228:231], a[200:203], a[32:35]\n\t"
"ds_read_b64_tr_b16 v[230:231], v246 offset:9216\n\t"
"v_mfma_f32_16x16x32_bf16 a[32:35], a[232:235], a[204:207], a[32:35]\n\t"
"buffer_load_dwordx4 v[248:251], v252, s[8:11], s20 offen offset:2048 sc1\n\t"
"v_mfma_f32_16x16x32_bf16 a[40:43], a[236:239], a[168:171], a[40:43]\n\t"
"s_mov_b32 s20, 454656\n\t"
"v_mfma_f32_16x16x32_bf16 a[40:43], a[240:243], a[172:175], a[40:43]\n\t"
"v_mfma_f32_16x16x32_bf16 a[40:43], a[244:247], a[176:179], a[40:43]\n\t"
"v_mfma_f32_16x16x32_bf16 a[48:51], a[236:239], a[172:175], a[48:51]\n\t"
"buffer_load_dwordx4 a[212:215], v252, s[8:11], s20 offen offset:0 sc1\n\t"
"v_mfma_f32_16x16x32_bf16 a[48:51], a[240:243], a[176:179], a[48:51]\n\t"
"s_waitcnt lgkmcnt(2)\n\t"
"v_mfma_f32_16x16x32_bf16 a[48:51], a[244:247], a[180:183], a[48:51]\n\t"
"v_mfma_f32_16x16x32_bf16 v[232:235], v[232:235], v[220:223], 0\n\t"
"v_mfma_f32_16x16x32_bf16 a[56:59], a[236:239], a[176:179], a[56:59]\n\t"
"buffer_load_dwordx4 a[216:219], v252, s[8:11], s20 offen offset:1024 sc1\n\t"
"v_mfma_f32_16x16x32_bf16 a[56:59], a[240:243], a[180:183], a[56:59]\n\t"
"ds_read_b64_tr_b16 v[224:225], v246 offset:12288\n\t"
"v_mfma_f32_16x16x32_bf16 a[56:59], a[244:247], a[184:187], a[56:59]\n\t"
"ds_read_b64_tr_b16 v[226:227], v246 offset:15360\n\t"
"v_mfma_f32_16x16x32_bf16 a[64:67], a[236:239], a[180:183], a[64:67]\n\t"
"s_waitcnt lgkmcnt(2)\n\t"
"v_mfma_f32_16x16x32_bf16 a[64:67], a[240:243], a[184:187], a[64:67]\n\t"
"buffer_load_dwordx4 a[220:223], v252, s[8:11], s20 offen offset:2048 sc1\n\t"
"v_mfma_f32_16x16x32_bf16 a[64:67], a[244:247], a[188:191], a[64:67]\n\t"
"s_mov_b32 s20, 466944\n\t"
"v_cvt_pk_bf16_f32 v232, v232, v233\n\t"
"v_cvt_pk_bf16_f32 v233, v234, v235\n\t"
"v_mfma_f32_16x16x32_bf16 v[228:231], v[228:231], v[220:223], 0\n\t"
"v_mfma_f32_16x16x32_bf16 a[72:75], a[236:239], a[184:187], a[72:75]\n\t"
"v_mfma_f32_16x16x32_bf16 a[72:75], a[240:243], a[188:191], a[72:75]\n\t"
"buffer_load_dwordx4 a[224:227], v252, s[8:11], s20 offen offset:0 sc1\n\t"
"v_mfma_f32_16x16x32_bf16 a[72:75], a[244:247], a[192:195], a[72:75]\n\t"
"v_mfma_f32_16x16x32_bf16 a[80:83], a[236:239], a[188:191], a[80:83]\n\t"
"v_mfma_f32_16x16x32_bf16 a[80:83], a[240:243], a[192:195], a[80:83]\n\t"
"v_cvt_pk_bf16_f32 v234, v228, v229\n\t"
"v_cvt_pk_bf16_f32 v235, v230, v231\n\t"
"v_mfma_f32_16x16x32_bf16 a[80:83], a[244:247], a[196:199], a[80:83]\n\t"
"ds_read_b64_tr_b16 v[228:229], v246 offset:19968\n\t"
"v_mfma_f32_16x16x32_bf16 a[88:91], a[236:239], a[192:195], a[88:91]\n\t"
"buffer_load_dwordx4 a[228:231], v252, s[8:11], s20 offen offset:1024 sc1\n\t"
"v_mfma_f32_16x16x32_bf16 a[88:91], a[240:243], a[196:199], a[88:91]\n\t"
"s_waitcnt lgkmcnt(1)\n\t"
"ds_read_b64_tr_b16 v[230:231], v246 offset:23040\n\t"
"v_mfma_f32_16x16x32_bf16 a[88:91], a[244:247], a[200:203], a[88:91]\n\t"
"v_mfma_f32_16x16x32_bf16 v[224:227], v[224:227], v[220:223], 0\n\t"
"buffer_load_dwordx4 a[232:235], v252, s[8:11], s20 offen offset:2048 sc1\n\t"
"v_mfma_f32_16x16x32_bf16 a[96:99], a[236:239], a[196:199], a[96:99]\n\t"
"s_waitcnt vmcnt(9)\n\t"
"s_mov_b32 s20, 700416\n\t"
"v_mfma_f32_16x16x32_bf16 a[96:99], a[240:243], a[200:203], a[96:99]\n\t"
"v_mfma_f32_16x16x32_bf16 a[96:99], a[244:247], a[204:207], a[96:99]\n\t"
"v_mfma_f32_16x16x32_bf16 a[104:107], a[248:251], a[168:171], a[104:107]\n\t"
"buffer_load_dwordx4 a[236:239], v252, s[8:11], s20 offen offset:0 sc1\n\t"
"v_mfma_f32_16x16x32_bf16 a[104:107], v[240:243], a[172:175], a[104:107]\n\t"
"ds_read_b128 a[168:171], v244 offset:33792\n\t"
"v_cvt_pk_bf16_f32 v224, v224, v225\n\t"
"v_mfma_f32_16x16x32_bf16 a[104:107], a[208:211], a[176:179], a[104:107]\n\t"
"v_cvt_pk_bf16_f32 v225, v226, v227\n\t"
"v_mfma_f32_16x16x32_bf16 a[112:115], a[248:251], a[172:175], a[112:115]\n\t"
"buffer_load_dwordx4 a[240:243], v252, s[8:11], s20 offen offset:1024 sc1\n\t"
"v_mfma_f32_16x16x32_bf16 a[112:115], v[240:243], a[176:179], a[112:115]\n\t"
"s_waitcnt lgkmcnt(1)\n\t"
"ds_read_b128 a[172:175], v244 offset:34304\n\t"
"v_mfma_f32_16x16x32_bf16 a[112:115], a[208:211], a[180:183], a[112:115]\n\t"
"v_mfma_f32_16x16x32_bf16 v[228:231], v[228:231], v[220:223], 0\n\t"
"buffer_load_dwordx4 a[244:247], v252, s[8:11], s20 offen offset:2048 sc1\n\t"
"v_mfma_f32_16x16x32_bf16 a[120:123], a[248:251], a[176:179], a[120:123]\n\t"
"s_mov_b32 s20, 712704\n\t"
"ds_read_b128 a[176:179], v244 offset:34816\n\t"
"v_mfma_f32_16x16x32_bf16 a[120:123], v[240:243], a[180:183], a[120:123]\n\t"
"v_mfma_f32_16x16x32_bf16 a[120:123], a[208:211], a[184:187], a[120:123]\n\t"
"buffer_load_dwordx4 a[252:255], v252, s[8:11], s20 offen offset:0 sc1\n\t"
"v_mfma_f32_16x16x32_bf16 a[128:131], a[248:251], a[180:183], a[128:131]\n\t"
"ds_read_b128 a[180:183], v244 offset:35328\n\t"
"v_mfma_f32_16x16x32_bf16 a[128:131], v[240:243], a[184:187], a[128:131]\n\t"
"v_cvt_pk_bf16_f32 v226, v228, v229\n\t"
"v_cvt_pk_bf16_f32 v227, v230, v231\n\t"
"v_mfma_f32_16x16x32_bf16 a[128:131], a[208:211], a[188:191], a[128:131]\n\t"
"ds_read_b64_tr_b16 v[228:229], v246 offset:26112\n\t"
"v_mfma_f32_16x16x32_bf16 a[136:139], a[248:251], a[184:187], a[136:139]\n\t"
"ds_read_b64_tr_b16 v[230:231], v246 offset:29184\n\t"
"v_mfma_f32_16x16x32_bf16 a[136:139], v[240:243], a[188:191], a[136:139]\n\t"
"ds_read_b128 a[184:187], v244 offset:35840\n\t"
"v_mfma_f32_16x16x32_bf16 a[136:139], a[208:211], a[192:195], a[136:139]\n\t"
"v_mfma_f32_16x16x32_bf16 a[144:147], a[248:251], a[188:191], a[144:147]\n\t"
"ds_read_b128 a[188:191], v244 offset:36352\n\t"
"v_mfma_f32_16x16x32_bf16 a[144:147], v[240:243], a[192:195], a[144:147]\n\t"
"v_mfma_f32_16x16x32_bf16 a[144:147], a[208:211], a[196:199], a[144:147]\n\t"
"v_mfma_f32_16x16x32_bf16 a[152:155], a[248:251], a[192:195], a[152:155]\n\t"
"ds_read_b128 a[192:195], v244 offset:36864\n\t"
"v_mfma_f32_16x16x32_bf16 a[152:155], v[240:243], a[196:199], a[152:155]\n\t"
"v_mfma_f32_16x16x32_bf16 a[152:155], a[208:211], a[200:203], a[152:155]\n\t"
"v_mfma_f32_16x16x32_bf16 a[160:163], a[248:251], a[196:199], a[160:163]\n\t"
"buffer_load_dwordx4 a[248:251], v252, s[8:11], s20 offen offset:1024 sc1\n\t"
"v_mfma_f32_16x16x32_bf16 a[160:163], v[240:243], a[200:203], a[160:163]\n\t"
"s_waitcnt vmcnt(11) lgkmcnt(3)\n\t"
"ds_read_b128 a[196:199], v244 offset:37376\n\t"
"v_mfma_f32_16x16x32_bf16 a[160:163], a[208:211], a[204:207], a[160:163]\n\t"
"ds_read_b128 a[200:203], v244 offset:37888\n\t"
"v_mfma_f32_16x16x32_bf16 v[228:231], v[228:231], v[220:223], 0\n\t"
"ds_read_b128 a[204:207], v244 offset:38400\n\t"
"v_mfma_f32_16x16x32_bf16 v[200:203], v[236:239], a[168:171], v[200:203]\n\t"
"v_mfma_f32_16x16x32_bf16 v[200:203], v[96:99], a[172:175], v[200:203]\n\t"
"v_mfma_f32_16x16x32_bf16 v[200:203], v[248:251], a[176:179], v[200:203]\n\t"
"v_mfma_f32_16x16x32_bf16 v[208:211], v[236:239], a[172:175], v[208:211]\n\t"
"s_waitcnt lgkmcnt(5)\n\t"
"v_mfma_f32_16x16x32_bf16 v[208:211], v[96:99], a[176:179], v[208:211]\n\t"
"v_cvt_pk_bf16_f32 v228, v228, v229\n\t"
"v_mfma_f32_16x16x32_bf16 v[208:211], v[248:251], a[180:183], v[208:211]\n\t"
"s_waitcnt lgkmcnt(0)\n\t"
"s_barrier\n\t"
"v_mfma_f32_16x16x32_bf16 v[216:219], v[236:239], a[176:179], v[216:219]\n\t"
"v_cvt_pk_bf16_f32 v229, v230, v231\n\t"
"v_mfma_f32_16x16x32_bf16 v[216:219], v[96:99], a[180:183], v[216:219]\n\t"
"v_mfma_f32_16x16x32_bf16 v[216:219], v[248:251], a[184:187], v[216:219]\n\t"
"ds_write2st64_b64 v245, v[232:233], v[234:235] offset0:48 offset1:49\n\t"
"v_mfma_f32_16x16x32_bf16 a[4:7], v[236:239], a[180:183], a[4:7]\n\t"
"v_mfma_f32_16x16x32_bf16 a[4:7], v[96:99], a[184:187], a[4:7]\n\t"
"ds_read_b64_tr_b16 v[232:233], v246 offset:32256\n\t"
"v_mfma_f32_16x16x32_bf16 a[4:7], v[248:251], a[188:191], a[4:7]\n\t"
"ds_write2st64_b64 v245, v[224:225], v[226:227] offset0:50 offset1:51\n\t"
"v_mfma_f32_16x16x32_bf16 a[12:15], v[236:239], a[184:187], a[12:15]\n\t"
"v_mfma_f32_16x16x32_bf16 a[12:15], v[96:99], a[188:191], a[12:15]\n\t"
"ds_read_b64_tr_b16 v[234:235], v246 offset:35328\n\t"
"v_mfma_f32_16x16x32_bf16 a[12:15], v[248:251], a[192:195], a[12:15]\n\t"
"ds_write_b64 v245, v[228:229] offset:26624\n\t"
"v_mfma_f32_16x16x32_bf16 a[20:23], v[236:239], a[188:191], a[20:23]\n\t"
"ds_read_b64_tr_b16 v[224:225], v246 offset:39936\n\t"
"v_mfma_f32_16x16x32_bf16 a[20:23], v[96:99], a[192:195], a[20:23]\n\t"
"ds_read_b64_tr_b16 v[226:227], v246 offset:43008\n\t"
"v_mfma_f32_16x16x32_bf16 a[20:23], v[248:251], a[196:199], a[20:23]\n\t"
"s_waitcnt lgkmcnt(3)\n\t"
"ds_read_b64_tr_b16 v[228:229], v246 offset:46080\n\t"
"v_mfma_f32_16x16x32_bf16 v[232:235], v[232:235], v[220:223], 0\n\t"
"ds_read_b64_tr_b16 v[230:231], v246 offset:49152\n\t"
"v_mfma_f32_16x16x32_bf16 a[28:31], v[236:239], a[192:195], a[28:31]\n\t"
"global_load_dwordx4 v[240:243], v252, s[48:49] offset:2048\n\t"
"v_mfma_f32_16x16x32_bf16 a[28:31], v[96:99], a[196:199], a[28:31]\n\t"
"global_load_dwordx4 a[208:211], v252, s[46:47] offset:1024\n\t"
"v_mfma_f32_16x16x32_bf16 a[28:31], v[248:251], a[200:203], a[28:31]\n\t"
"s_waitcnt lgkmcnt(2)\n\t"
"v_mfma_f32_16x16x32_bf16 v[224:227], v[224:227], v[220:223], 0\n\t"
"v_mfma_f32_16x16x32_bf16 a[36:39], v[236:239], a[196:199], a[36:39]\n\t"
"s_waitcnt lgkmcnt(0)\n\t"
"v_cvt_pk_bf16_f32 v232, v232, v233\n\t"
"v_cvt_pk_bf16_f32 v233, v234, v235\n\t"
"v_mfma_f32_16x16x32_bf16 a[36:39], v[96:99], a[200:203], a[36:39]\n\t"
"s_waitcnt vmcnt(10)\n\t"
"v_mfma_f32_16x16x32_bf16 a[36:39], v[248:251], a[204:207], a[36:39]\n\t"
"buffer_load_dwordx4 v[96:99], v252, s[8:11], s20 offen offset:2048 sc1\n\t"
"v_mfma_f32_16x16x32_bf16 v[228:231], v[228:231], v[220:223], 0\n\t"
"s_mov_b32 s20, 724992\n\t"
"v_mfma_f32_16x16x32_bf16 a[44:47], a[212:215], a[168:171], a[44:47]\n\t"
"buffer_load_dwordx4 v[248:251], v252, s[8:11], s20 offen offset:0 sc1\n\t"
"v_mfma_f32_16x16x32_bf16 a[44:47], a[216:219], a[172:175], a[44:47]\n\t"
"v_cvt_pk_bf16_f32 v234, v224, v225\n\t"
"v_cvt_pk_bf16_f32 v235, v226, v227\n\t"
"v_mfma_f32_16x16x32_bf16 a[44:47], a[220:223], a[176:179], a[44:47]\n\t"
"ds_write2st64_b64 v245, v[232:233], v[234:235] offset0:53 offset1:54\n\t"
"v_mfma_f32_16x16x32_bf16 a[52:55], a[212:215], a[172:175], a[52:55]\n\t"
"v_mfma_f32_16x16x32_bf16 a[52:55], a[216:219], a[176:179], a[52:55]\n\t"
"ds_read_b64_tr_b16 v[224:225], v246 offset:52224\n\t"
"v_mfma_f32_16x16x32_bf16 a[52:55], a[220:223], a[180:183], a[52:55]\n\t"
"ds_read_b64_tr_b16 v[226:227], v246 offset:55296\n\t"
"v_cvt_pk_bf16_f32 v228, v228, v229\n\t"
"v_mfma_f32_16x16x32_bf16 a[60:63], a[212:215], a[176:179], a[60:63]\n\t"
"v_cvt_pk_bf16_f32 v229, v230, v231\n\t"
"v_mfma_f32_16x16x32_bf16 a[60:63], a[216:219], a[180:183], a[60:63]\n\t"
"v_mfma_f32_16x16x32_bf16 a[60:63], a[220:223], a[184:187], a[60:63]\n\t"
"v_mfma_f32_16x16x32_bf16 a[68:71], a[212:215], a[180:183], a[68:71]\n\t"
"v_mfma_f32_16x16x32_bf16 a[68:71], a[216:219], a[184:187], a[68:71]\n\t"
"v_mfma_f32_16x16x32_bf16 a[68:71], a[220:223], a[188:191], a[68:71]\n\t"
"v_mfma_f32_16x16x32_bf16 a[76:79], a[212:215], a[184:187], a[76:79]\n\t"
"s_waitcnt lgkmcnt(0)\n\t"
"v_mfma_f32_16x16x32_bf16 a[76:79], a[216:219], a[188:191], a[76:79]\n\t"
"v_mfma_f32_16x16x32_bf16 a[76:79], a[220:223], a[192:195], a[76:79]\n\t"
"v_mfma_f32_16x16x32_bf16 v[224:227], v[224:227], v[220:223], 0\n\t"
"v_mfma_f32_16x16x32_bf16 a[84:87], a[212:215], a[188:191], a[84:87]\n\t"
"v_mfma_f32_16x16x32_bf16 a[84:87], a[216:219], a[192:195], a[84:87]\n\t"
"v_mfma_f32_16x16x32_bf16 a[84:87], a[220:223], a[196:199], a[84:87]\n\t"
"v_mfma_f32_16x16x32_bf16 a[92:95], a[212:215], a[192:195], a[92:95]\n\t"
"v_mfma_f32_16x16x32_bf16 a[92:95], a[216:219], a[196:199], a[92:95]\n\t"
"v_cvt_pk_bf16_f32 v230, v224, v225\n\t"
"v_cvt_pk_bf16_f32 v231, v226, v227\n\t"
"v_mfma_f32_16x16x32_bf16 a[92:95], a[220:223], a[200:203], a[92:95]\n\t"
"ds_write2st64_b64 v245, v[228:229], v[230:231] offset0:55 offset1:56\n\t"
"v_mfma_f32_16x16x32_bf16 a[100:103], a[212:215], a[196:199], a[100:103]\n\t"
"buffer_load_dwordx4 a[212:215], v252, s[8:11], s20 offen offset:1024 sc1\n\t"
"v_mfma_f32_16x16x32_bf16 a[100:103], a[216:219], a[200:203], a[100:103]\n\t"
"s_waitcnt vmcnt(10)\n\t"
"v_mfma_f32_16x16x32_bf16 a[100:103], a[220:223], a[204:207], a[100:103]\n\t"
"v_mfma_f32_16x16x32_bf16 a[108:111], a[224:227], a[168:171], a[108:111]\n\t"
"buffer_load_dwordx4 a[216:219], v252, s[8:11], s20 offen offset:2048 sc1\n\t"
"v_mfma_f32_16x16x32_bf16 a[108:111], a[228:231], a[172:175], a[108:111]\n\t"
"s_mov_b32 s20, 737280\n\t"
"v_mfma_f32_16x16x32_bf16 a[108:111], a[232:235], a[176:179], a[108:111]\n\t"
"s_waitcnt lgkmcnt(0)\n\t"
"s_barrier\n\t"
"v_mfma_f32_16x16x32_bf16 a[116:119], a[224:227], a[172:175], a[116:119]\n\t"
"s_waitcnt vmcnt(5)\n\t"
"ds_read_b64_tr_b16 v[236:237], v240 offset:0\n\t"
"v_mfma_f32_16x16x32_bf16 a[116:119], a[228:231], a[176:179], a[116:119]\n\t"
"ds_read_b64_tr_b16 v[238:239], v240 offset:1536\n\t"
"v_mfma_f32_16x16x32_bf16 a[116:119], a[232:235], a[180:183], a[116:119]\n\t"
"ds_read_b64_tr_b16 v[232:233], v241 offset:0\n\t"
"v_mfma_f32_16x16x32_bf16 a[124:127], a[224:227], a[176:179], a[124:127]\n\t"
"ds_read_b64_tr_b16 v[234:235], v241 offset:1536\n\t"
"v_mfma_f32_16x16x32_bf16 a[124:127], a[228:231], a[180:183], a[124:127]\n\t"
"buffer_load_dwordx4 a[220:223], v252, s[8:11], s20 offen offset:0 sc1\n\t"
"v_mfma_f32_16x16x32_bf16 a[124:127], a[232:235], a[184:187], a[124:127]\n\t"
"ds_read_b64_tr_b16 v[224:225], v240 offset:19968\n\t"
"v_mfma_f32_16x16x32_bf16 a[132:135], a[224:227], a[180:183], a[132:135]\n\t"
"s_waitcnt vmcnt(5) lgkmcnt(3)\n\t"
"ds_read_b64_tr_b16 v[226:227], v240 offset:21504\n\t"
"v_mfma_f32_16x16x32_bf16 a[132:135], a[228:231], a[184:187], a[132:135]\n\t"
"ds_read_b64_tr_b16 v[228:229], v241 offset:19968\n\t"
"v_mfma_f32_16x16x32_bf16 a[132:135], a[232:235], a[188:191], a[132:135]\n\t"
"s_waitcnt lgkmcnt(3)\n\t"
"ds_read_b64_tr_b16 v[230:231], v241 offset:21504\n\t"
"v_mfma_f32_16x16x32_bf16 v[236:239], v[236:239], a[208:211], 0\n\t"
"ds_read_b128 a[168:171], v244 offset:24576\n\t"
"v_mfma_f32_16x16x32_bf16 v[232:235], v[232:235], a[208:211], 0\n\t"
"Lpg18_%=:\n\t"
"s_cbranch_vccnz Lvr2_%=\n\t"
"ds_read_b128 a[172:175], v244 offset:25088\n\t"
"v_mfma_f32_16x16x32_bf16 a[140:143], a[224:227], a[184:187], a[140:143]\n\t"
"ds_read_b128 a[176:179], v244 offset:25600\n\t"
"v_mfma_f32_16x16x32_bf16 a[140:143], a[228:231], a[188:191], a[140:143]\n\t"
"ds_read_b128 a[180:183], v244 offset:26112\n\t"
"v_mfma_f32_16x16x32_bf16 a[140:143], a[232:235], a[192:195], a[140:143]\n\t"
"ds_read_b128 a[184:187], v244 offset:26624\n\t"
"v_mfma_f32_16x16x32_bf16 a[148:151], a[224:227], a[188:191], a[148:151]\n\t"
"s_waitcnt lgkmcnt(7)\n\t"
"ds_read_b128 a[188:191], v244 offset:27136\n\t"
"v_cvt_pk_bf16_f32 v236, v236, v237\n\t"
"v_mfma_f32_16x16x32_bf16 a[148:151], a[228:231], a[192:195], a[148:151]\n\t"
"s_waitcnt lgkmcnt(6)\n\t"
"v_cvt_pk_bf16_f32 v237, v238, v239\n\t"
"v_cvt_pk_bf16_f32 v238, v232, v233\n\t"
"v_mfma_f32_16x16x32_bf16 a[148:151], a[232:235], a[196:199], a[148:151]\n\t"
"v_cvt_pk_bf16_f32 v239, v234, v235\n\t"
"v_mfma_f32_16x16x32_bf16 v[224:227], v[224:227], a[208:211], 0\n\t"
"ds_write_b128 v242, v[236:239] offset:0\n\t"
"v_mfma_f32_16x16x32_bf16 v[228:231], v[228:231], a[208:211], 0\n\t"
"v_mfma_f32_16x16x32_bf16 a[156:159], a[224:227], a[192:195], a[156:159]\n\t"
"ds_read_b128 a[192:195], v244 offset:27648\n\t"
"v_mfma_f32_16x16x32_bf16 a[156:159], a[228:231], a[196:199], a[156:159]\n\t"
"ds_read_b64_tr_b16 v[232:233], v240 offset:39936\n\t"
"v_mfma_f32_16x16x32_bf16 a[156:159], a[232:235], a[200:203], a[156:159]\n\t"
"ds_read_b64_tr_b16 v[234:235], v240 offset:41472\n\t"
"v_mfma_f32_16x16x32_bf16 a[164:167], a[224:227], a[196:199], a[164:167]\n\t"
"buffer_load_dwordx4 a[224:227], v252, s[8:11], s20 offen offset:1024 sc1\n\t"
"v_mfma_f32_16x16x32_bf16 a[164:167], a[228:231], a[200:203], a[164:167]\n\t"
"s_waitcnt lgkmcnt(7)\n\t"
"ds_read_b64_tr_b16 v[236:237], v241 offset:39936\n\t"
"v_cvt_pk_bf16_f32 v224, v224, v225\n\t"
"v_mfma_f32_16x16x32_bf16 a[164:167], a[232:235], a[204:207], a[164:167]\n\t"
"ds_read_b64_tr_b16 v[238:239], v241 offset:41472\n\t"
"v_cvt_pk_bf16_f32 v225, v226, v227\n\t"
"Lst24_%=:\n\t"
"v_mfma_f32_16x16x32_bf16 v[196:199], a[236:239], a[168:171], v[196:199]\n\t"
"s_waitcnt lgkmcnt(8)\n\t"
"ds_read_b128 a[196:199], v244 offset:28160\n\t"
"v_cvt_pk_bf16_f32 v226, v228, v229\n\t"
"v_mfma_f32_16x16x32_bf16 v[196:199], a[240:243], a[172:175], v[196:199]\n\t"
"ds_read_b128 a[200:203], v244 offset:28672\n\t"
"v_cvt_pk_bf16_f32 v227, v230, v231\n\t"
"v_mfma_f32_16x16x32_bf16 v[196:199], a[244:247], a[176:179], v[196:199]\n\t"
"ds_write_b128 v242, v[224:227] offset:1536\n\t"
"v_mfma_f32_16x16x32_bf16 v[204:207], a[236:239], a[172:175], v[204:207]\n\t"
"buffer_load_dwordx4 a[228:231], v252, s[8:11], s20 offen offset:2048 sc1\n\t"
"v_mfma_f32_16x16x32_bf16 v[204:207], a[240:243], a[176:179], v[204:207]\n\t"
"s_waitcnt lgkmcnt(10)\n\t"
"ds_read_b128 a[204:207], v244 offset:29184\n\t"
"v_mfma_f32_16x16x32_bf16 v[204:207], a[244:247], a[180:183], v[204:207]\n\t"
"s_mov_b32 s20, 749568\n\t"
"v_add_u32 v240, 59904, v240\n\t"
"v_add_u32 v241, 59904, v241\n\t"
"v_mfma_f32_16x16x32_bf16 v[212:215], a[236:239], a[176:179], v[212:215]\n\t"
"s_waitcnt lgkmcnt(6)\n\t"
"ds_read_b64_tr_b16 v[228:229], v240 offset:0\n\t"
"v_mfma_f32_16x16x32_bf16 v[212:215], a[240:243], a[180:183], v[212:215]\n\t"
"buffer_load_dwordx4 a[232:235], v252, s[8:11], s20 offen offset:0 sc1\n\t"
"v_mfma_f32_16x16x32_bf16 v[212:215], a[244:247], a[184:187], v[212:215]\n\t"
"s_waitcnt lgkmcnt(5)\n\t"
"ds_read_b64_tr_b16 v[230:231], v240 offset:1536\n\t"
"v_mfma_f32_16x16x32_bf16 v[232:235], v[232:235], a[208:211], 0\n\t"
"ds_read_b64_tr_b16 v[224:225], v241 offset:0\n\t"
"v_mfma_f32_16x16x32_bf16 v[236:239], v[236:239], a[208:211], 0\n\t"
"ds_read_b64_tr_b16 v[226:227], v241 offset:1536\n\t"
"v_mfma_f32_16x16x32_bf16 a[0:3], a[236:239], a[180:183], a[0:3]\n\t"
"v_mfma_f32_16x16x32_bf16 a[0:3], a[240:243], a[184:187], a[0:3]\n\t"
"v_mfma_f32_16x16x32_bf16 a[0:3], a[244:247], a[188:191], a[0:3]\n\t"
"v_mfma_f32_16x16x32_bf16 a[8:11], a[236:239], a[184:187], a[8:11]\n\t"
"s_waitcnt lgkmcnt(2)\n\t"
"v_cvt_pk_bf16_f32 v232, v232, v233\n\t"
"v_cvt_pk_bf16_f32 v233, v234, v235\n\t"
"v_mfma_f32_16x16x32_bf16 a[8:11], a[240:243], a[188:191], a[8:11]\n\t"
"s_waitcnt lgkmcnt(0)\n\t"
"v_cvt_pk_bf16_f32 v234, v236, v237\n\t"
"v_cvt_pk_bf16_f32 v235, v238, v239\n\t"
"v_mfma_f32_16x16x32_bf16 a[8:11], a[244:247], a[192:195], a[8:11]\n\t"
"ds_write_b128 v242, v[232:235] offset:3072\n\t"
"v_mfma_f32_16x16x32_bf16 v[228:231], v[228:231], a[208:211], 0\n\t"
"v_mfma_f32_16x16x32_bf16 v[224:227], v[224:227], a[208:211], 0\n\t"
"ds_read_b64_tr_b16 v[236:237], v240 offset:19968\n\t"
"v_mfma_f32_16x16x32_bf16 a[16:19], a[236:239], a[188:191], a[16:19]\n\t"
"ds_read_b64_tr_b16 v[238:239], v240 offset:21504\n\t"
"v_mfma_f32_16x16x32_bf16 a[16:19], a[240:243], a[192:195], a[16:19]\n\t"
"ds_read_b64_tr_b16 v[232:233], v241 offset:19968\n\t"
"v_mfma_f32_16x16x32_bf16 a[16:19], a[244:247], a[196:199], a[16:19]\n\t"
"ds_read_b64_tr_b16 v[234:235], v241 offset:21504\n\t"
"v_mfma_f32_16x16x32_bf16 a[24:27], a[236:239], a[192:195], a[24:27]\n\t"
"v_cvt_pk_bf16_f32 v228, v228, v229\n\t"
"v_cvt_pk_bf16_f32 v229, v230, v231\n\t"
"v_mfma_f32_16x16x32_bf16 a[24:27], a[240:243], a[196:199], a[24:27]\n\t"
"v_cvt_pk_bf16_f32 v230, v224, v225\n\t"
"v_cvt_pk_bf16_f32 v231, v226, v227\n\t"
"v_mfma_f32_16x16x32_bf16 a[24:27], a[244:247], a[200:203], a[24:27]\n\t"
"ds_write_b128 v242, v[228:231] offset:4608\n\t"
"v_mfma_f32_16x16x32_bf16 a[32:35], a[236:239], a[196:199], a[32:35]\n\t"
"buffer_load_dwordx4 a[236:239], v252, s[8:11], s20 offen offset:1024 sc1\n\t"
"v_mfma_f32_16x16x32_bf16 a[32:35], a[240:243], a[200:203], a[32:35]\n\t"
"s_waitcnt vmcnt(8)\n\t"
"ds_read_b64_tr_b16 v[224:225], v240 offset:39936\n\t"
"v_mfma_f32_16x16x32_bf16 a[32:35], a[244:247], a[204:207], a[32:35]\n\t"
"ds_read_b64_tr_b16 v[226:227], v240 offset:41472\n\t"
"v_mfma_f32_16x16x32_bf16 a[40:43], a[252:255], a[168:171], a[40:43]\n\t"
"buffer_load_dwordx4 a[240:243], v252, s[8:11], s20 offen offset:2048 sc1\n\t"
"v_mfma_f32_16x16x32_bf16 a[40:43], a[248:251], a[172:175], a[40:43]\n\t"
"s_mov_b32 s20, 761856\n\t"
"ds_read_b64_tr_b16 v[228:229], v241 offset:39936\n\t"
"v_mfma_f32_16x16x32_bf16 a[40:43], v[96:99], a[176:179], a[40:43]\n\t"
"ds_read_b64_tr_b16 v[230:231], v241 offset:41472\n\t"
"v_mfma_f32_16x16x32_bf16 a[48:51], a[252:255], a[172:175], a[48:51]\n\t"
"buffer_load_dwordx4 a[244:247], v252, s[8:11], s20 offen offset:0 sc1\n\t"
"v_mfma_f32_16x16x32_bf16 a[48:51], a[248:251], a[176:179], a[48:51]\n\t"
"s_waitcnt lgkmcnt(5)\n\t"
"v_mfma_f32_16x16x32_bf16 a[48:51], v[96:99], a[180:183], a[48:51]\n\t"
"v_mfma_f32_16x16x32_bf16 v[236:239], v[236:239], a[208:211], 0\n\t"
"v_mfma_f32_16x16x32_bf16 v[232:235], v[232:235], a[208:211], 0\n\t"
"v_xor_b32 v246, 0xc0, v246\n\t"
"v_mfma_f32_16x16x32_bf16 a[56:59], a[252:255], a[176:179], a[56:59]\n\t"
"v_mfma_f32_16x16x32_bf16 a[56:59], a[248:251], a[180:183], a[56:59]\n\t"
"v_mfma_f32_16x16x32_bf16 a[56:59], v[96:99], a[184:187], a[56:59]\n\t"
"v_mfma_f32_16x16x32_bf16 a[64:67], a[252:255], a[180:183], a[64:67]\n\t"
"s_waitcnt lgkmcnt(2)\n\t"
"v_cvt_pk_bf16_f32 v236, v236, v237\n\t"
"v_cvt_pk_bf16_f32 v237, v238, v239\n\t"
"v_mfma_f32_16x16x32_bf16 a[64:67], a[248:251], a[184:187], a[64:67]\n\t"
"s_waitcnt lgkmcnt(0)\n\t"
"v_cvt_pk_bf16_f32 v238, v232, v233\n\t"
"v_cvt_pk_bf16_f32 v239, v234, v235\n\t"
"v_mfma_f32_16x16x32_bf16 a[64:67], v[96:99], a[188:191], a[64:67]\n\t"
"ds_write_b128 v242, v[236:239] offset:6144\n\t"
"v_mfma_f32_16x16x32_bf16 v[224:227], v[224:227], a[208:211], 0\n\t"
"v_mfma_f32_16x16x32_bf16 v[228:231], v[228:231], a[208:211], 0\n\t"
"v_mfma_f32_16x16x32_bf16 a[72:75], a[252:255], a[184:187], a[72:75]\n\t"
"buffer_load_dwordx4 a[208:211], v252, s[8:11], s20 offen offset:1024 sc1\n\t"
"v_mfma_f32_16x16x32_bf16 a[72:75], a[248:251], a[188:191], a[72:75]\n\t"
"v_mfma_f32_16x16x32_bf16 a[72:75], v[96:99], a[192:195], a[72:75]\n\t"
"v_mfma_f32_16x16x32_bf16 a[80:83], a[252:255], a[188:191], a[80:83]\n\t"
"v_cvt_pk_bf16_f32 v224, v224, v225\n\t"
"v_cvt_pk_bf16_f32 v225, v226, v227\n\t"
"v_mfma_f32_16x16x32_bf16 a[80:83], a[248:251], a[192:195], a[80:83]\n\t"
"v_cvt_pk_bf16_f32 v226, v228, v229\n\t"
"v_cvt_pk_bf16_f32 v227, v230, v231\n\t"
"v_mfma_f32_16x16x32_bf16 a[80:83], v[96:99], a[196:199], a[80:83]\n\t"
"ds_write_b128 v242, v[224:227] offset:7680\n\t"
"v_mfma_f32_16x16x32_bf16 a[88:91], a[252:255], a[192:195], a[88:91]\n\t"
"v_mfma_f32_16x16x32_bf16 a[88:91], a[248:251], a[196:199], a[88:91]\n\t"
"ds_read_b64_tr_b16 v[232:233], v246 offset:0\n\t"
"v_mfma_f32_16x16x32_bf16 a[88:91], v[96:99], a[200:203], a[88:91]\n\t"
"ds_read_b64_tr_b16 v[234:235], v246 offset:3072\n\t"
"v_mfma_f32_16x16x32_bf16 a[96:99], a[252:255], a[196:199], a[96:99]\n\t"
"buffer_load_dwordx4 a[252:255], v252, s[8:11], s20 offen offset:2048 sc1\n\t"
"v_mfma_f32_16x16x32_bf16 a[96:99], a[248:251], a[200:203], a[96:99]\n\t"
"s_waitcnt vmcnt(9)\n\t"
"ds_read_b64_tr_b16 v[236:237], v246 offset:6144\n\t"
"s_mov_b32 s20, 626688\n\t"
"v_mfma_f32_16x16x32_bf16 a[96:99], v[96:99], a[204:207], a[96:99]\n\t"
"ds_read_b64_tr_b16 v[238:239], v246 offset:9216\n\t"
"v_mfma_f32_16x16x32_bf16 a[104:107], v[248:251], a[168:171], a[104:107]\n\t"
"buffer_load_dwordx4 v[240:243], v252, s[8:11], s20 offen offset:0 sc1\n\t"
"v_mfma_f32_16x16x32_bf16 a[104:107], a[212:215], a[172:175], a[104:107]\n\t"
"ds_read_b64_tr_b16 v[228:229], v246 offset:12288\n\t"
"v_mfma_f32_16x16x32_bf16 a[104:107], a[216:219], a[176:179], a[104:107]\n\t"
"ds_read_b64_tr_b16 v[230:231], v246 offset:15360\n\t"
"v_mfma_f32_16x16x32_bf16 a[112:115], v[248:251], a[172:175], a[112:115]\n\t"
"buffer_load_dwordx4 v[96:99], v252, s[8:11], s20 offen offset:1024 sc1\n\t"
"v_mfma_f32_16x16x32_bf16 a[112:115], a[212:215], a[176:179], a[112:115]\n\t"
"ds_read_b128 a[168:171], v244 offset:33792\n\t"
"v_mfma_f32_16x16x32_bf16 a[112:115], a[216:219], a[180:183], a[112:115]\n\t"
"ds_read_b64_tr_b16 v[224:225], v246 offset:19968\n\t"
"v_mfma_f32_16x16x32_bf16 a[120:123], v[248:251], a[176:179], a[120:123]\n\t"
"buffer_load_dwordx4 a[248:251], v252, s[8:11], s20 offen offset:2048 sc1\n\t"
"v_mfma_f32_16x16x32_bf16 a[120:123], a[212:215], a[180:183], a[120:123]\n\t"
"s_waitcnt lgkmcnt(6)\n\t"
"ds_read_b64_tr_b16 v[226:227], v246 offset:23040\n\t"
"v_mfma_f32_16x16x32_bf16 a[120:123], a[216:219], a[184:187], a[120:123]\n\t"
"ds_read_b128 a[172:175], v244 offset:34304\n\t"
"v_mfma_f32_16x16x32_bf16 v[232:235], v[232:235], v[220:223], 0\n\t"
"ds_read_b128 a[176:179], v244 offset:34816\n\t"
"v_mfma_f32_16x16x32_bf16 a[128:131], v[248:251], a[180:183], a[128:131]\n\t"
"s_waitcnt lgkmcnt(7)\n\t"
"ds_read_b128 a[180:183], v244 offset:35328\n\t"
"v_mfma_f32_16x16x32_bf16 a[128:131], a[212:215], a[184:187], a[128:131]\n\t"
"v_mfma_f32_16x16x32_bf16 a[128:131], a[216:219], a[188:191], a[128:131]\n\t"
"v_mfma_f32_16x16x32_bf16 v[236:239], v[236:239], v[220:223], 0\n\t"
"v_mfma_f32_16x16x32_bf16 a[136:139], v[248:251], a[184:187], a[136:139]\n\t"
"s_waitcnt lgkmcnt(6)\n\t"
"ds_read_b128 a[184:187], v244 offset:35840\n\t"
"v_cvt_pk_bf16_f32 v232, v232, v233\n\t"
"v_mfma_f32_16x16x32_bf16 a[136:139], a[212:215], a[188:191], a[136:139]\n\t"
"v_cvt_pk_bf16_f32 v233, v234, v235\n\t"
"v_mfma_f32_16x16x32_bf16 a[136:139], a[216:219], a[192:195], a[136:139]\n\t"
"s_mov_b32 s20, 638976\n\t"
"v_mfma_f32_16x16x32_bf16 v[228:231], v[228:231], v[220:223], 0\n\t"
"v_mfma_f32_16x16x32_bf16 a[144:147], v[248:251], a[188:191], a[144:147]\n\t"
"s_waitcnt lgkmcnt(4)\n\t"
"ds_read_b128 a[188:191], v244 offset:36352\n\t"
"v_cvt_pk_bf16_f32 v234, v236, v237\n\t"
"v_mfma_f32_16x16x32_bf16 a[144:147], a[212:215], a[192:195], a[144:147]\n\t"
"v_cvt_pk_bf16_f32 v235, v238, v239\n\t"
"v_mfma_f32_16x16x32_bf16 a[144:147], a[216:219], a[196:199], a[144:147]\n\t"
"v_mfma_f32_16x16x32_bf16 v[224:227], v[224:227], v[220:223], 0\n\t"
"v_mfma_f32_16x16x32_bf16 a[152:155], v[248:251], a[192:195], a[152:155]\n\t"
"buffer_load_dwordx4 v[236:239], v252, s[8:11], s20 offen offset:0 sc1\n\t"
"v_mfma_f32_16x16x32_bf16 a[152:155], a[212:215], a[196:199], a[152:155]\n\t"
"ds_read_b128 a[192:195], v244 offset:36864\n\t"
"v_cvt_pk_bf16_f32 v228, v228, v229\n\t"
"v_mfma_f32_16x16x32_bf16 a[152:155], a[216:219], a[200:203], a[152:155]\n\t"
"v_cvt_pk_bf16_f32 v229, v230, v231\n\t"
"v_mfma_f32_16x16x32_bf16 a[160:163], v[248:251], a[196:199], a[160:163]\n\t"
"buffer_load_dwordx4 v[248:251], v252, s[8:11], s20 offen offset:1024 sc1\n\t"
"v_mfma_f32_16x16x32_bf16 a[160:163], a[212:215], a[200:203], a[160:163]\n\t"
"s_waitcnt vmcnt(11) lgkmcnt(4)\n\t"
"ds_read_b128 a[196:199], v244 offset:37376\n\t"
"v_cvt_pk_bf16_f32 v230, v224, v225\n\t"
"v_mfma_f32_16x16x32_bf16 a[160:163], a[216:219], a[204:207], a[160:163]\n\t"
"ds_read_b128 a[200:203], v244 offset:37888\n\t"
"v_cvt_pk_bf16_f32 v231, v226, v227\n\t"
"v_mfma_f32_16x16x32_bf16 v[200:203], a[220:223], a[168:171], v[200:203]\n\t"
"ds_read_b64_tr_b16 v[224:225], v246 offset:26112\n\t"
"v_mfma_f32_16x16x32_bf16 v[200:203], a[224:227], a[172:175], v[200:203]\n\t"
"s_waitcnt lgkmcnt(6)\n\t"
"ds_read_b64_tr_b16 v[226:227], v246 offset:29184\n\t"
"v_mfma_f32_16x16x32_bf16 v[200:203], a[228:231], a[176:179], v[200:203]\n\t"
"s_waitcnt lgkmcnt(0)\n\t"
"ds_read_b128 a[204:207], v244 offset:38400\n\t"
"v_mfma_f32_16x16x32_bf16 v[224:227], v[224:227], v[220:223], 0\n\t"
"buffer_load_dwordx4 a[212:215], v252, s[8:11], s20 offen offset:2048 sc1\n\t"
"s_mov_b32 s20, 651264\n\t"
"v_mfma_f32_16x16x32_bf16 v[208:211], a[220:223], a[172:175], v[208:211]\n\t"
"buffer_load_dwordx4 a[216:219], v252, s[8:11], s20 offen offset:0 sc1\n\t"
"v_mfma_f32_16x16x32_bf16 v[208:211], a[224:227], a[176:179], v[208:211]\n\t"
"s_add_u32 m0, s24, 768\n\t"
"s_add_u32 s28, s21, 0\n\t"
"v_mfma_f32_16x16x32_bf16 v[208:211], a[228:231], a[180:183], v[208:211]\n\t"
"s_waitcnt lgkmcnt(0)\n\t"
"s_barrier\n\t"
"v_mfma_f32_16x16x32_bf16 v[216:219], a[220:223], a[176:179], v[216:219]\n\t"
"v_mfma_f32_16x16x32_bf16 v[216:219], a[224:227], a[180:183], v[216:219]\n\t"
"buffer_load_dwordx4 v247, s[12:15], s28 offen offset:256 nt lds\n\t"
"v_mfma_f32_16x16x32_bf16 v[216:219], a[228:231], a[184:187], v[216:219]\n\t"
"s_add_u32 m0, s24, 6912\n\t"
"s_add_u32 s28, s21, 196608\n\t"
"ds_write2st64_b64 v245, v[232:233], v[234:235] offset0:48 offset1:49\n\t"
"v_mfma_f32_16x16x32_bf16 a[4:7], a[220:223], a[180:183], a[4:7]\n\t"
"buffer_load_dwordx4 v247, s[12:15], s28 offen offset:256 nt lds\n\t"
"v_mfma_f32_16x16x32_bf16 a[4:7], a[224:227], a[184:187], a[4:7]\n\t"
"s_add_u32 m0, s24, 13056\n\t"
"s_add_u32 s28, s21, 393216\n\t"
"ds_read_b64_tr_b16 v[232:233], v246 offset:32256\n\t"
"v_mfma_f32_16x16x32_bf16 a[4:7], a[228:231], a[188:191], a[4:7]\n\t"
"ds_write2st64_b64 v245, v[228:229], v[230:231] offset0:50 offset1:51\n\t"
"v_cvt_pk_bf16_f32 v224, v224, v225\n\t"
"v_cvt_pk_bf16_f32 v225, v226, v227\n\t"
"v_mfma_f32_16x16x32_bf16 a[12:15], a[220:223], a[184:187], a[12:15]\n\t"
"v_mfma_f32_16x16x32_bf16 a[12:15], a[224:227], a[188:191], a[12:15]\n\t"
"ds_read_b64_tr_b16 v[234:235], v246 offset:35328\n\t"
"v_mfma_f32_16x16x32_bf16 a[12:15], a[228:231], a[192:195], a[12:15]\n\t"
"buffer_load_dwordx4 v247, s[12:15], s28 offen offset:256 nt lds\n\t"
"s_add_u32 m0, s24, 20736\n\t"
"s_add_u32 s28, s21, 589824\n\t"
"v_mfma_f32_16x16x32_bf16 a[20:23], a[220:223], a[188:191], a[20:23]\n\t"
"buffer_load_dwordx4 v247, s[12:15], s28 offen offset:256 nt lds\n\t"
"v_mfma_f32_16x16x32_bf16 a[20:23], a[224:227], a[192:195], a[20:23]\n\t"
"s_add_u32 m0, s24, 60672\n\t"
"s_add_u32 s28, s21, 1769472\n\t"
"ds_read_b64_tr_b16 v[228:229], v246 offset:39936\n\t"
"v_mfma_f32_16x16x32_bf16 a[20:23], a[228:231], a[196:199], a[20:23]\n\t"
"s_waitcnt lgkmcnt(1)\n\t"
"ds_write_b64 v245, v[224:225] offset:26624\n\t"
"ds_read_b64_tr_b16 v[230:231], v246 offset:43008\n\t"
"v_mfma_f32_16x16x32_bf16 v[232:235], v[232:235], v[220:223], 0\n\t"
"buffer_load_dwordx4 v247, s[12:15], s28 offen offset:256 nt lds\n\t"
"s_add_u32 m0, s24, 66816\n\t"
"s_add_u32 s28, s21, 1966080\n\t"
"v_mfma_f32_16x16x32_bf16 a[28:31], a[220:223], a[192:195], a[28:31]\n\t"
"buffer_load_dwordx4 v247, s[12:15], s28 offen offset:256 nt lds\n\t"
"v_mfma_f32_16x16x32_bf16 a[28:31], a[224:227], a[196:199], a[28:31]\n\t"
"ds_read_b64_tr_b16 v[224:225], v246 offset:46080\n\t"
"v_mfma_f32_16x16x32_bf16 a[28:31], a[228:231], a[200:203], a[28:31]\n\t"
"s_waitcnt lgkmcnt(1)\n\t"
"ds_read_b64_tr_b16 v[226:227], v246 offset:49152\n\t"
"v_mfma_f32_16x16x32_bf16 v[228:231], v[228:231], v[220:223], 0\n\t"
"v_mfma_f32_16x16x32_bf16 a[36:39], a[220:223], a[196:199], a[36:39]\n\t"
"v_cvt_pk_bf16_f32 v232, v232, v233\n\t"
"v_cvt_pk_bf16_f32 v233, v234, v235\n\t"
"v_mfma_f32_16x16x32_bf16 a[36:39], a[224:227], a[200:203], a[36:39]\n\t"
"s_waitcnt vmcnt(16)\n\t"
"v_mfma_f32_16x16x32_bf16 a[36:39], a[228:231], a[204:207], a[36:39]\n\t"
"s_waitcnt lgkmcnt(0)\n\t"
"v_mfma_f32_16x16x32_bf16 v[224:227], v[224:227], v[220:223], 0\n\t"
"v_mfma_f32_16x16x32_bf16 a[44:47], a[232:235], a[168:171], a[44:47]\n\t"
"v_cvt_pk_bf16_f32 v234, v228, v229\n\t"
"v_cvt_pk_bf16_f32 v235, v230, v231\n\t"
"v_mfma_f32_16x16x32_bf16 a[44:47], a[236:239], a[172:175], a[44:47]\n\t"
"ds_read_b64_tr_b16 v[228:229], v246 offset:52224\n\t"
"v_mfma_f32_16x16x32_bf16 a[44:47], a[240:243], a[176:179], a[44:47]\n\t"
"ds_write2st64_b64 v245, v[232:233], v[234:235] offset0:53 offset1:54\n\t"
"v_mfma_f32_16x16x32_bf16 a[52:55], a[232:235], a[172:175], a[52:55]\n\t"
"v_mfma_f32_16x16x32_bf16 a[52:55], a[236:239], a[176:179], a[52:55]\n\t"
"ds_read_b64_tr_b16 v[230:231], v246 offset:55296\n\t"
"v_mfma_f32_16x16x32_bf16 a[52:55], a[240:243], a[180:183], a[52:55]\n\t"
"v_cvt_pk_bf16_f32 v224, v224, v225\n\t"
"v_cvt_pk_bf16_f32 v225, v226, v227\n\t"
"v_mfma_f32_16x16x32_bf16 a[60:63], a[232:235], a[176:179], a[60:63]\n\t"
"v_mfma_f32_16x16x32_bf16 a[60:63], a[236:239], a[180:183], a[60:63]\n\t"
"v_mfma_f32_16x16x32_bf16 a[60:63], a[240:243], a[184:187], a[60:63]\n\t"
"v_mfma_f32_16x16x32_bf16 a[68:71], a[232:235], a[180:183], a[68:71]\n\t"
"v_mfma_f32_16x16x32_bf16 a[68:71], a[236:239], a[184:187], a[68:71]\n\t"
"v_mfma_f32_16x16x32_bf16 a[68:71], a[240:243], a[188:191], a[68:71]\n\t"
"v_mfma_f32_16x16x32_bf16 a[76:79], a[232:235], a[184:187], a[76:79]\n\t"
"s_waitcnt lgkmcnt(0)\n\t"
"v_mfma_f32_16x16x32_bf16 a[76:79], a[236:239], a[188:191], a[76:79]\n\t"
"v_mfma_f32_16x16x32_bf16 a[76:79], a[240:243], a[192:195], a[76:79]\n\t"
"v_mfma_f32_16x16x32_bf16 v[228:231], v[228:231], v[220:223], 0\n\t"
"v_mfma_f32_16x16x32_bf16 a[84:87], a[232:235], a[188:191], a[84:87]\n\t"
"buffer_load_dwordx4 a[220:223], v252, s[8:11], s20 offen offset:1024 sc1\n\t"
"v_mfma_f32_16x16x32_bf16 a[84:87], a[236:239], a[192:195], a[84:87]\n\t"
"v_mfma_f32_16x16x32_bf16 a[84:87], a[240:243], a[196:199], a[84:87]\n\t"
"v_mfma_f32_16x16x32_bf16 a[92:95], a[232:235], a[192:195], a[92:95]\n\t"
"v_mfma_f32_16x16x32_bf16 a[92:95], a[236:239], a[196:199], a[92:95]\n\t"
"v_cvt_pk_bf16_f32 v226, v228, v229\n\t"
"v_cvt_pk_bf16_f32 v227, v230, v231\n\t"
"v_mfma_f32_16x16x32_bf16 a[92:95], a[240:243], a[200:203], a[92:95]\n\t"
"ds_write2st64_b64 v245, v[224:225], v[226:227] offset0:55 offset1:56\n\t"
"v_mfma_f32_16x16x32_bf16 a[100:103], a[232:235], a[196:199], a[100:103]\n\t"
"s_waitcnt vmcnt(14)\n\t"
"v_mfma_f32_16x16x32_bf16 a[100:103], a[236:239], a[200:203], a[100:103]\n\t"
"v_mfma_f32_16x16x32_bf16 a[100:103], a[240:243], a[204:207], a[100:103]\n\t"
"v_mfma_f32_16x16x32_bf16 a[108:111], a[244:247], a[168:171], a[108:111]\n\t"
"v_mfma_f32_16x16x32_bf16 a[108:111], a[208:211], a[172:175], a[108:111]\n\t"
"v_mfma_f32_16x16x32_bf16 a[108:111], a[252:255], a[176:179], a[108:111]\n\t"
"s_waitcnt lgkmcnt(0)\n\t"
"s_barrier\n\t"
"v_mfma_f32_16x16x32_bf16 a[116:119], a[244:247], a[172:175], a[116:119]\n\t"
"ds_read_b128 a[168:171], v244 offset:24576\n\t"
"v_mfma_f32_16x16x32_bf16 a[116:119], a[208:211], a[176:179], a[116:119]\n\t"
"ds_read_b128 a[172:175], v244 offset:25088\n\t"
"v_mfma_f32_16x16x32_bf16 a[116:119], a[252:255], a[180:183], a[116:119]\n\t"
"v_mfma_f32_16x16x32_bf16 a[124:127], a[244:247], a[176:179], a[124:127]\n\t"
"ds_read_b128 a[176:179], v244 offset:25600\n\t"
"v_mfma_f32_16x16x32_bf16 a[124:127], a[208:211], a[180:183], a[124:127]\n\t"
"v_mfma_f32_16x16x32_bf16 a[124:127], a[252:255], a[184:187], a[124:127]\n\t"
"v_mfma_f32_16x16x32_bf16 a[132:135], a[244:247], a[180:183], a[132:135]\n\t"
"ds_read_b128 a[180:183], v244 offset:26112\n\t"
"v_mfma_f32_16x16x32_bf16 a[132:135], a[208:211], a[184:187], a[132:135]\n\t"
"v_mfma_f32_16x16x32_bf16 a[132:135], a[252:255], a[188:191], a[132:135]\n\t"
"v_mfma_f32_16x16x32_bf16 a[140:143], a[244:247], a[184:187], a[140:143]\n\t"
"ds_read_b128 a[184:187], v244 offset:26624\n\t"
"v_mfma_f32_16x16x32_bf16 a[140:143], a[208:211], a[188:191], a[140:143]\n\t"
"v_mfma_f32_16x16x32_bf16 a[140:143], a[252:255], a[192:195], a[140:143]\n\t"
"v_mfma_f32_16x16x32_bf16 a[148:151], a[244:247], a[188:191], a[148:151]\n\t"
"ds_read_b128 a[188:191], v244 offset:27136\n\t"
"v_mfma_f32_16x16x32_bf16 a[148:151], a[208:211], a[192:195], a[148:151]\n\t"
"v_mfma_f32_16x16x32_bf16 a[148:151], a[252:255], a[196:199], a[148:151]\n\t"
"v_mfma_f32_16x16x32_bf16 a[156:159], a[244:247], a[192:195], a[156:159]\n\t"
"ds_read_b128 a[192:195], v244 offset:27648\n\t"
"v_mfma_f32_16x16x32_bf16 a[156:159], a[208:211], a[196:199], a[156:159]\n\t"
"v_mfma_f32_16x16x32_bf16 a[156:159], a[252:255], a[200:203], a[156:159]\n\t"
"v_mfma_f32_16x16x32_bf16 a[164:167], a[244:247], a[196:199], a[164:167]\n\t"
"s_waitcnt vmcnt(11) lgkmcnt(4)\n\t"
"ds_read_b128 a[196:199], v244 offset:28160\n\t"
"v_mfma_f32_16x16x32_bf16 a[164:167], a[208:211], a[200:203], a[164:167]\n\t"
"ds_read_b128 a[200:203], v244 offset:28672\n\t"
"v_mfma_f32_16x16x32_bf16 a[164:167], a[252:255], a[204:207], a[164:167]\n\t"
"ds_read_b128 a[204:207], v244 offset:29184\n\t"
"Lst25_%=:\n\t"
"v_mfma_f32_16x16x32_bf16 v[0:3], v[240:243], a[168:171], v[0:3]\n\t"
"buffer_load_dwordx4 a[208:211], v252, s[8:11], s20 offen offset:2048 sc1\n\t"
"v_mfma_f32_16x16x32_bf16 v[0:3], v[96:99], a[172:175], v[0:3]\n\t"
"s_waitcnt lgkmcnt(6)\n\t"
"s_mov_b32 s20, 663552\n\t"
"v_mfma_f32_16x16x32_bf16 v[0:3], a[248:251], a[176:179], v[0:3]\n\t"
"v_mfma_f32_16x16x32_bf16 v[4:7], v[240:243], a[172:175], v[4:7]\n\t"
"s_waitcnt lgkmcnt(5)\n\t"
"v_mfma_f32_16x16x32_bf16 v[4:7], v[96:99], a[176:179], v[4:7]\n\t"
"buffer_load_dwordx4 a[224:227], v252, s[8:11], s20 offen offset:0 sc1\n\t"
"v_mfma_f32_16x16x32_bf16 v[4:7], a[248:251], a[180:183], v[4:7]\n\t"
"v_mfma_f32_16x16x32_bf16 v[8:11], v[240:243], a[176:179], v[8:11]\n\t"
"s_waitcnt lgkmcnt(4)\n\t"
"v_mfma_f32_16x16x32_bf16 v[8:11], v[96:99], a[180:183], v[8:11]\n\t"
"v_mfma_f32_16x16x32_bf16 v[8:11], a[248:251], a[184:187], v[8:11]\n\t"
"buffer_load_dwordx4 a[228:231], v252, s[8:11], s20 offen offset:1024 sc1\n\t"
"v_mfma_f32_16x16x32_bf16 v[12:15], v[240:243], a[180:183], v[12:15]\n\t"
"s_waitcnt lgkmcnt(3)\n\t"
"v_mfma_f32_16x16x32_bf16 v[12:15], v[96:99], a[184:187], v[12:15]\n\t"
"v_mfma_f32_16x16x32_bf16 v[12:15], a[248:251], a[188:191], v[12:15]\n\t"
"v_mfma_f32_16x16x32_bf16 v[16:19], v[240:243], a[184:187], v[16:19]\n\t"
"buffer_load_dwordx4 a[232:235], v252, s[8:11], s20 offen offset:2048 sc1\n\t"
"v_mfma_f32_16x16x32_bf16 v[16:19], v[96:99], a[188:191], v[16:19]\n\t"
"s_waitcnt lgkmcnt(2)\n\t"
"s_mov_b32 s20, 675840\n\t"
"v_mfma_f32_16x16x32_bf16 v[16:19], a[248:251], a[192:195], v[16:19]\n\t"
"ds_read_b64_tr_b16 v[232:233], v246 offset:512\n\t"
"v_mfma_f32_16x16x32_bf16 v[20:23], v[240:243], a[188:191], v[20:23]\n\t"
"s_waitcnt lgkmcnt(2)\n\t"
"ds_read_b64_tr_b16 v[234:235], v246 offset:3584\n\t"
"v_mfma_f32_16x16x32_bf16 v[20:23], v[96:99], a[192:195], v[20:23]\n\t"
"buffer_load_dwordx4 a[236:239], v252, s[8:11], s20 offen offset:0 sc1\n\t"
"v_mfma_f32_16x16x32_bf16 v[20:23], a[248:251], a[196:199], v[20:23]\n\t"
"v_mfma_f32_16x16x32_bf16 v[24:27], v[240:243], a[192:195], v[24:27]\n\t"
"s_waitcnt lgkmcnt(2)\n\t"
"v_mfma_f32_16x16x32_bf16 v[24:27], v[96:99], a[196:199], v[24:27]\n\t"
"v_mfma_f32_16x16x32_bf16 v[24:27], a[248:251], a[200:203], v[24:27]\n\t"
"buffer_load_dwordx4 a[240:243], v252, s[8:11], s20 offen offset:1024 sc1\n\t"
"v_mfma_f32_16x16x32_bf16 v[28:31], v[240:243], a[196:199], v[28:31]\n\t"
"s_waitcnt vmcnt(14)\n\t"
"ds_read_b64_tr_b16 v[228:229], v246 offset:6656\n\t"
"v_mfma_f32_16x16x32_bf16 v[28:31], v[96:99], a[200:203], v[28:31]\n\t"
"ds_read_b64_tr_b16 v[230:231], v246 offset:9728\n\t"
"v_mfma_f32_16x16x32_bf16 v[28:31], a[248:251], a[204:207], v[28:31]\n\t"
"v_mfma_f32_16x16x32_bf16 v[32:35], v[236:239], a[168:171], v[32:35]\n\t"
"buffer_load_dwordx4 v[240:243], v252, s[8:11], s20 offen offset:2048 sc1\n\t"
"v_mfma_f32_16x16x32_bf16 v[32:35], v[248:251], a[172:175], v[32:35]\n\t"
"s_mov_b32 s20, 688128\n\t"
"v_mfma_f32_16x16x32_bf16 v[32:35], a[212:215], a[176:179], v[32:35]\n\t"
"v_mfma_f32_16x16x32_bf16 v[36:39], v[236:239], a[172:175], v[36:39]\n\t"
"s_waitcnt lgkmcnt(2)\n\t"
"v_mfma_f32_16x16x32_bf16 v[36:39], v[248:251], a[176:179], v[36:39]\n\t"
"buffer_load_dwordx4 v[96:99], v252, s[8:11], s20 offen offset:0 sc1\n\t"
"v_mfma_f32_16x16x32_bf16 v[36:39], a[212:215], a[180:183], v[36:39]\n\t"
"v_mfma_f32_16x16x32_bf16 v[232:235], v[232:235], v[220:223], 0\n\t"
"v_mfma_f32_16x16x32_bf16 v[40:43], v[236:239], a[176:179], v[40:43]\n\t"
"ds_read_b64_tr_b16 v[224:225], v246 offset:12800\n\t"
"v_mfma_f32_16x16x32_bf16 v[40:43], v[248:251], a[180:183], v[40:43]\n\t"
"buffer_load_dwordx4 a[244:247], v252, s[8:11], s20 offen offset:1024 sc1\n\t"
"v_mfma_f32_16x16x32_bf16 v[40:43], a[212:215], a[184:187], v[40:43]\n\t"
"ds_read_b64_tr_b16 v[226:227], v246 offset:15872\n\t"
"v_mfma_f32_16x16x32_bf16 v[44:47], v[236:239], a[180:183], v[44:47]\n\t"
"s_waitcnt lgkmcnt(2)\n\t"
"v_mfma_f32_16x16x32_bf16 v[44:47], v[248:251], a[184:187], v[44:47]\n\t"
"buffer_load_dwordx4 a[248:251], v252, s[8:11], s20 offen offset:2048 sc1\n\t"
"v_mfma_f32_16x16x32_bf16 v[44:47], a[212:215], a[188:191], v[44:47]\n\t"
"s_mov_b32 s20, 921600\n\t"
"v_cvt_pk_bf16_f32 v232, v232, v233\n\t"
"v_cvt_pk_bf16_f32 v233, v234, v235\n\t"
"v_mfma_f32_16x16x32_bf16 v[228:231], v[228:231], v[220:223], 0\n\t"
"v_mfma_f32_16x16x32_bf16 v[48:51], v[236:239], a[184:187], v[48:51]\n\t"
"v_mfma_f32_16x16x32_bf16 v[48:51], v[248:251], a[188:191], v[48:51]\n\t"
"buffer_load_dwordx4 a[252:255], v252, s[8:11], s20 offen offset:0 sc1\n\t"
"v_mfma_f32_16x16x32_bf16 v[48:51], a[212:215], a[192:195], v[48:51]\n\t"
"v_mfma_f32_16x16x32_bf16 v[52:55], v[236:239], a[188:191], v[52:55]\n\t"
"v_mfma_f32_16x16x32_bf16 v[52:55], v[248:251], a[192:195], v[52:55]\n\t"
"v_cvt_pk_bf16_f32 v234, v228, v229\n\t"
"v_cvt_pk_bf16_f32 v235, v230, v231\n\t"
"v_mfma_f32_16x16x32_bf16 v[52:55], a[212:215], a[196:199], v[52:55]\n\t"
"ds_read_b64_tr_b16 v[228:229], v246 offset:20480\n\t"
"v_mfma_f32_16x16x32_bf16 v[56:59], v[236:239], a[192:195], v[56:59]\n\t"
"s_waitcnt lgkmcnt(1)\n\t"
"ds_read_b64_tr_b16 v[230:231], v246 offset:23552\n\t"
"v_mfma_f32_16x16x32_bf16 v[56:59], v[248:251], a[196:199], v[56:59]\n\t"
"v_mfma_f32_16x16x32_bf16 v[56:59], a[212:215], a[200:203], v[56:59]\n\t"
"v_mfma_f32_16x16x32_bf16 v[224:227], v[224:227], v[220:223], 0\n\t"
"v_mfma_f32_16x16x32_bf16 v[60:63], v[236:239], a[196:199], v[60:63]\n\t"
"buffer_load_dwordx4 v[236:239], v252, s[8:11], s20 offen offset:1024 sc1\n\t"
"v_mfma_f32_16x16x32_bf16 v[60:63], v[248:251], a[200:203], v[60:63]\n\t"
"s_waitcnt vmcnt(11)\n\t"
"v_mfma_f32_16x16x32_bf16 v[60:63], a[212:215], a[204:207], v[60:63]\n\t"
"v_mfma_f32_16x16x32_bf16 v[64:67], a[216:219], a[168:171], v[64:67]\n\t"
"buffer_load_dwordx4 v[248:251], v252, s[8:11], s20 offen offset:2048 sc1\n\t"
"v_mfma_f32_16x16x32_bf16 v[64:67], a[220:223], a[172:175], v[64:67]\n\t"
"s_mov_b32 s20, 933888\n\t"
"ds_read_b128 a[168:171], v244 offset:33792\n\t"
"v_cvt_pk_bf16_f32 v224, v224, v225\n\t"
"v_mfma_f32_16x16x32_bf16 v[64:67], a[208:211], a[176:179], v[64:67]\n\t"
"v_cvt_pk_bf16_f32 v225, v226, v227\n\t"
"v_mfma_f32_16x16x32_bf16 v[68:71], a[216:219], a[172:175], v[68:71]\n\t"
"buffer_load_dwordx4 a[212:215], v252, s[8:11], s20 offen offset:0 sc1\n\t"
"v_mfma_f32_16x16x32_bf16 v[68:71], a[220:223], a[176:179], v[68:71]\n\t"
"s_waitcnt lgkmcnt(1)\n\t"
"ds_read_b128 a[172:175], v244 offset:34304\n\t"
"v_mfma_f32_16x16x32_bf16 v[68:71], a[208:211], a[180:183], v[68:71]\n\t"
"v_mfma_f32_16x16x32_bf16 v[228:231], v[228:231], v[220:223], 0\n\t"
"v_mfma_f32_16x16x32_bf16 v[72:75], a[216:219], a[176:179], v[72:75]\n\t"
"ds_read_b128 a[176:179], v244 offset:34816\n\t"
"v_mfma_f32_16x16x32_bf16 v[72:75], a[220:223], a[180:183], v[72:75]\n\t"
"v_mfma_f32_16x16x32_bf16 v[72:75], a[208:211], a[184:187], v[72:75]\n\t"
"v_mfma_f32_16x16x32_bf16 v[76:79], a[216:219], a[180:183], v[76:79]\n\t"
"ds_read_b128 a[180:183], v244 offset:35328\n\t"
"v_mfma_f32_16x16x32_bf16 v[76:79], a[220:223], a[184:187], v[76:79]\n\t"
"v_cvt_pk_bf16_f32 v226, v228, v229\n\t"
"v_cvt_pk_bf16_f32 v227, v230, v231\n\t"
"v_mfma_f32_16x16x32_bf16 v[76:79], a[208:211], a[188:191], v[76:79]\n\t"
"ds_read_b64_tr_b16 v[228:229], v246 offset:26624\n\t"
"v_mfma_f32_16x16x32_bf16 v[80:83], a[216:219], a[184:187], v[80:83]\n\t"
"ds_read_b64_tr_b16 v[230:231], v246 offset:29696\n\t"
"v_mfma_f32_16x16x32_bf16 v[80:83], a[220:223], a[188:191], v[80:83]\n\t"
"ds_read_b128 a[184:187], v244 offset:35840\n\t"
"v_mfma_f32_16x16x32_bf16 v[80:83], a[208:211], a[192:195], v[80:83]\n\t"
"v_mfma_f32_16x16x32_bf16 v[84:87], a[216:219], a[188:191], v[84:87]\n\t"
"ds_read_b128 a[188:191], v244 offset:36352\n\t"
"v_mfma_f32_16x16x32_bf16 v[84:87], a[220:223], a[192:195], v[84:87]\n\t"
"v_mfma_f32_16x16x32_bf16 v[84:87], a[208:211], a[196:199], v[84:87]\n\t"
"v_mfma_f32_16x16x32_bf16 v[88:91], a[216:219], a[192:195], v[88:91]\n\t"
"ds_read_b128 a[192:195], v244 offset:36864\n\t"
"v_mfma_f32_16x16x32_bf16 v[88:91], a[220:223], a[196:199], v[88:91]\n\t"
"v_mfma_f32_16x16x32_bf16 v[88:91], a[208:211], a[200:203], v[88:91]\n\t"
"v_mfma_f32_16x16x32_bf16 v[92:95], a[216:219], a[196:199], v[92:95]\n\t"
"buffer_load_dwordx4 a[216:219], v252, s[8:11], s20 offen offset:1024 sc1\n\t"
"v_mfma_f32_16x16x32_bf16 v[92:95], a[220:223], a[200:203], v[92:95]\n\t"
"s_waitcnt vmcnt(11) lgkmcnt(3)\n\t"
"ds_read_b128 a[196:199], v244 offset:37376\n\t"
"v_mfma_f32_16x16x32_bf16 v[92:95], a[208:211], a[204:207], v[92:95]\n\t"
"ds_read_b128 a[200:203], v244 offset:37888\n\t"
"v_mfma_f32_16x16x32_bf16 v[228:231], v[228:231], v[220:223], 0\n\t"
"ds_read_b128 a[204:207], v244 offset:38400\n\t"
"v_mfma_f32_16x16x32_bf16 v[100:103], a[224:227], a[168:171], v[100:103]\n\t"
"buffer_load_dwordx4 a[208:211], v252, s[8:11], s20 offen offset:2048 sc1\n\t"
"v_mfma_f32_16x16x32_bf16 v[100:103], a[228:231], a[172:175], v[100:103]\n\t"
"s_mov_b32 s20, 946176\n\t"
"v_mfma_f32_16x16x32_bf16 v[100:103], a[232:235], a[176:179], v[100:103]\n\t"
"buffer_load_dwordx4 a[220:223], v252, s[8:11], s20 offen offset:0 sc1\n\t"
"v_mfma_f32_16x16x32_bf16 v[104:107], a[224:227], a[172:175], v[104:107]\n\t"
"s_waitcnt lgkmcnt(5)\n\t"
"v_mfma_f32_16x16x32_bf16 v[104:107], a[228:231], a[176:179], v[104:107]\n\t"
"v_cvt_pk_bf16_f32 v228, v228, v229\n\t"
"v_mfma_f32_16x16x32_bf16 v[104:107], a[232:235], a[180:183], v[104:107]\n\t"
"s_waitcnt lgkmcnt(0)\n\t"
"s_barrier\n\t"
"v_mfma_f32_16x16x32_bf16 v[108:111], a[224:227], a[176:179], v[108:111]\n\t"
"v_cvt_pk_bf16_f32 v229, v230, v231\n\t"
"v_mfma_f32_16x16x32_bf16 v[108:111], a[228:231], a[180:183], v[108:111]\n\t"
"v_mfma_f32_16x16x32_bf16 v[108:111], a[232:235], a[184:187], v[108:111]\n\t"
"ds_write2st64_b64 v245, v[232:233], v[234:235] offset0:48 offset1:49\n\t"
"v_mfma_f32_16x16x32_bf16 v[112:115], a[224:227], a[180:183], v[112:115]\n\t"
"v_mfma_f32_16x16x32_bf16 v[112:115], a[228:231], a[184:187], v[112:115]\n\t"
"ds_read_b64_tr_b16 v[232:233], v246 offset:32768\n\t"
"v_mfma_f32_16x16x32_bf16 v[112:115], a[232:235], a[188:191], v[112:115]\n\t"
"ds_write2st64_b64 v245, v[224:225], v[226:227] offset0:50 offset1:51\n\t"
"v_mfma_f32_16x16x32_bf16 v[116:119], a[224:227], a[184:187], v[116:119]\n\t"
"v_mfma_f32_16x16x32_bf16 v[116:119], a[228:231], a[188:191], v[116:119]\n\t"
"ds_read_b64_tr_b16 v[234:235], v246 offset:35840\n\t"
"v_mfma_f32_16x16x32_bf16 v[116:119], a[232:235], a[192:195], v[116:119]\n\t"
"s_add_u32 m0, s24, 72960\n\t"
"s_add_u32 s28, s21, 2162688\n\t"
"ds_write_b64 v245, v[228:229] offset:26624\n\t"
"v_mfma_f32_16x16x32_bf16 v[120:123], a[224:227], a[188:191], v[120:123]\n\t"
"ds_read_b64_tr_b16 v[224:225], v246 offset:40448\n\t"
"v_mfma_f32_16x16x32_bf16 v[120:123], a[228:231], a[192:195], v[120:123]\n\t"
"Lpg19_%=:\n\t"
"s_cbranch_vccnz Lvr2_%=\n\t"
"ds_read_b64_tr_b16 v[226:227], v246 offset:43520\n\t"
"v_mfma_f32_16x16x32_bf16 v[120:123], a[232:235], a[196:199], v[120:123]\n\t"
"s_waitcnt lgkmcnt(3)\n\t"
"ds_read_b64_tr_b16 v[228:229], v246 offset:46592\n\t"
"v_mfma_f32_16x16x32_bf16 v[232:235], v[232:235], v[220:223], 0\n\t"
"ds_read_b64_tr_b16 v[230:231], v246 offset:49664\n\t"
"v_mfma_f32_16x16x32_bf16 v[124:127], a[224:227], a[192:195], v[124:127]\n\t"
"buffer_load_dwordx4 v247, s[12:15], s28 offen offset:256 nt lds\n\t"
"v_mfma_f32_16x16x32_bf16 v[124:127], a[228:231], a[196:199], v[124:127]\n\t"
"s_add_u32 m0, s24, 80640\n\t"
"s_add_u32 s28, s21, 2359296\n\t"
"v_mfma_f32_16x16x32_bf16 v[124:127], a[232:235], a[200:203], v[124:127]\n\t"
"s_waitcnt lgkmcnt(2)\n\t"
"buffer_load_dwordx4 v247, s[12:15], s28 offen offset:256 nt lds\n\t"
"s_add_u32 m0, s24, 26880\n\t"
"s_add_u32 s28, s21, 786432\n\t"
"v_mfma_f32_16x16x32_bf16 v[224:227], v[224:227], v[220:223], 0\n\t"
"buffer_load_dwordx4 v247, s[12:15], s28 offen offset:256 nt lds\n\t"
"v_mfma_f32_16x16x32_bf16 v[128:131], a[224:227], a[196:199], v[128:131]\n\t"
"s_waitcnt lgkmcnt(0)\n\t"
"s_add_u32 m0, s24, 86784\n\t"
"s_add_u32 s28, s21, 2555904\n\t"
"v_mfma_f32_16x16x32_bf16 v[128:131], a[228:231], a[200:203], v[128:131]\n\t"
"buffer_load_dwordx4 v247, s[12:15], s28 offen offset:256 nt lds\n\t"
"v_mfma_f32_16x16x32_bf16 v[128:131], a[232:235], a[204:207], v[128:131]\n\t"
"s_waitcnt vmcnt(14)\n\t"
"s_add_u32 m0, s24, 33024\n\t"
"s_add_u32 s28, s21, 983040\n\t"
"v_mfma_f32_16x16x32_bf16 v[228:231], v[228:231], v[220:223], 0\n\t"
"buffer_load_dwordx4 v247, s[12:15], s28 offen offset:256 nt lds\n\t"
"v_cvt_pk_bf16_f32 v232, v232, v233\n\t"
"v_cvt_pk_bf16_f32 v233, v234, v235\n\t"
"v_mfma_f32_16x16x32_bf16 v[132:135], a[236:239], a[168:171], v[132:135]\n\t"
"s_add_u32 m0, s24, 40704\n\t"
"s_add_u32 s28, s21, 1179648\n\t"
"v_cvt_pk_bf16_f32 v234, v224, v225\n\t"
"v_mfma_f32_16x16x32_bf16 v[132:135], a[240:243], a[172:175], v[132:135]\n\t"
"v_cvt_pk_bf16_f32 v235, v226, v227\n\t"
"v_mfma_f32_16x16x32_bf16 v[132:135], v[240:243], a[176:179], v[132:135]\n\t"
"buffer_load_dwordx4 v247, s[12:15], s28 offen offset:256 nt lds\n\t"
"v_mfma_f32_16x16x32_bf16 v[136:139], a[236:239], a[172:175], v[136:139]\n\t"
"ds_read_b64_tr_b16 v[224:225], v246 offset:52736\n\t"
"v_mfma_f32_16x16x32_bf16 v[136:139], a[240:243], a[176:179], v[136:139]\n\t"
"ds_read_b64_tr_b16 v[226:227], v246 offset:55808\n\t"
"v_cvt_pk_bf16_f32 v228, v228, v229\n\t"
"v_mfma_f32_16x16x32_bf16 v[136:139], v[240:243], a[180:183], v[136:139]\n\t"
"ds_write2st64_b64 v245, v[232:233], v[234:235] offset0:53 offset1:54\n\t"
"v_mfma_f32_16x16x32_bf16 v[140:143], a[236:239], a[176:179], v[140:143]\n\t"
"v_cvt_pk_bf16_f32 v229, v230, v231\n\t"
"v_mfma_f32_16x16x32_bf16 v[140:143], a[240:243], a[180:183], v[140:143]\n\t"
"v_mfma_f32_16x16x32_bf16 v[140:143], v[240:243], a[184:187], v[140:143]\n\t"
"v_mfma_f32_16x16x32_bf16 v[144:147], a[236:239], a[180:183], v[144:147]\n\t"
"v_mfma_f32_16x16x32_bf16 v[144:147], a[240:243], a[184:187], v[144:147]\n\t"
"v_mfma_f32_16x16x32_bf16 v[144:147], v[240:243], a[188:191], v[144:147]\n\t"
"v_mfma_f32_16x16x32_bf16 v[148:151], a[236:239], a[184:187], v[148:151]\n\t"
"s_waitcnt lgkmcnt(1)\n\t"
"v_mfma_f32_16x16x32_bf16 v[148:151], a[240:243], a[188:191], v[148:151]\n\t"
"v_mfma_f32_16x16x32_bf16 v[148:151], v[240:243], a[192:195], v[148:151]\n\t"
"v_mfma_f32_16x16x32_bf16 v[224:227], v[224:227], v[220:223], 0\n\t"
"v_mfma_f32_16x16x32_bf16 v[152:155], a[236:239], a[188:191], v[152:155]\n\t"
"v_mfma_f32_16x16x32_bf16 v[152:155], a[240:243], a[192:195], v[152:155]\n\t"
"v_mfma_f32_16x16x32_bf16 v[152:155], v[240:243], a[196:199], v[152:155]\n\t"
"v_mfma_f32_16x16x32_bf16 v[156:159], a[236:239], a[192:195], v[156:159]\n\t"
"v_mfma_f32_16x16x32_bf16 v[156:159], a[240:243], a[196:199], v[156:159]\n\t"
"v_cvt_pk_bf16_f32 v230, v224, v225\n\t"
"v_cvt_pk_bf16_f32 v231, v226, v227\n\t"
"v_mfma_f32_16x16x32_bf16 v[156:159], v[240:243], a[200:203], v[156:159]\n\t"
"ds_write2st64_b64 v245, v[228:229], v[230:231] offset0:55 offset1:56\n\t"
"v_mfma_f32_16x16x32_bf16 v[160:163], a[236:239], a[196:199], v[160:163]\n\t"
"s_waitcnt vmcnt(13)\n\t"
"v_mfma_f32_16x16x32_bf16 v[160:163], a[240:243], a[200:203], v[160:163]\n\t"
"v_mfma_f32_16x16x32_bf16 v[160:163], v[240:243], a[204:207], v[160:163]\n\t"
"v_mfma_f32_16x16x32_bf16 v[164:167], v[96:99], a[168:171], v[164:167]\n\t"
"v_mfma_f32_16x16x32_bf16 v[164:167], a[244:247], a[172:175], v[164:167]\n\t"
"v_mfma_f32_16x16x32_bf16 v[164:167], a[248:251], a[176:179], v[164:167]\n\t"
"s_waitcnt lgkmcnt(0)\n\t"
"s_barrier\n\t"
"v_mfma_f32_16x16x32_bf16 v[168:171], v[96:99], a[172:175], v[168:171]\n\t"
"ds_read_b128 a[168:171], v244 offset:24576\n\t"
"v_mfma_f32_16x16x32_bf16 v[168:171], a[244:247], a[176:179], v[168:171]\n\t"
"buffer_load_dwordx4 v[240:243], v252, s[8:11], s20 offen offset:1024 sc1\n\t"
"v_mfma_f32_16x16x32_bf16 v[168:171], a[248:251], a[180:183], v[168:171]\n\t"
"ds_read_b128 a[172:175], v244 offset:25088\n\t"
"v_mfma_f32_16x16x32_bf16 v[172:175], v[96:99], a[176:179], v[172:175]\n\t"
"ds_read_b128 a[176:179], v244 offset:25600\n\t"
"v_mfma_f32_16x16x32_bf16 v[172:175], a[244:247], a[180:183], v[172:175]\n\t"
"v_mfma_f32_16x16x32_bf16 v[172:175], a[248:251], a[184:187], v[172:175]\n\t"
"v_mfma_f32_16x16x32_bf16 v[176:179], v[96:99], a[180:183], v[176:179]\n\t"
"ds_read_b128 a[180:183], v244 offset:26112\n\t"
"v_mfma_f32_16x16x32_bf16 v[176:179], a[244:247], a[184:187], v[176:179]\n\t"
"v_mfma_f32_16x16x32_bf16 v[176:179], a[248:251], a[188:191], v[176:179]\n\t"
"v_mfma_f32_16x16x32_bf16 v[180:183], v[96:99], a[184:187], v[180:183]\n\t"
"ds_read_b128 a[184:187], v244 offset:26624\n\t"
"v_mfma_f32_16x16x32_bf16 v[180:183], a[244:247], a[188:191], v[180:183]\n\t"
"v_mfma_f32_16x16x32_bf16 v[180:183], a[248:251], a[192:195], v[180:183]\n\t"
"v_mfma_f32_16x16x32_bf16 v[184:187], v[96:99], a[188:191], v[184:187]\n\t"
"ds_read_b128 a[188:191], v244 offset:27136\n\t"
"v_mfma_f32_16x16x32_bf16 v[184:187], a[244:247], a[192:195], v[184:187]\n\t"
"v_mfma_f32_16x16x32_bf16 v[184:187], a[248:251], a[196:199], v[184:187]\n\t"
"v_mfma_f32_16x16x32_bf16 v[188:191], v[96:99], a[192:195], v[188:191]\n\t"
"ds_read_b128 a[192:195], v244 offset:27648\n\t"
"v_mfma_f32_16x16x32_bf16 v[188:191], a[244:247], a[196:199], v[188:191]\n\t"
"v_mfma_f32_16x16x32_bf16 v[188:191], a[248:251], a[200:203], v[188:191]\n\t"
"v_mfma_f32_16x16x32_bf16 v[192:195], v[96:99], a[196:199], v[192:195]\n\t"
"s_waitcnt vmcnt(11) lgkmcnt(4)\n\t"
"ds_read_b128 a[196:199], v244 offset:28160\n\t"
"v_mfma_f32_16x16x32_bf16 v[192:195], a[244:247], a[200:203], v[192:195]\n\t"
"ds_read_b128 a[200:203], v244 offset:28672\n\t"
"v_mfma_f32_16x16x32_bf16 v[192:195], a[248:251], a[204:207], v[192:195]\n\t"
"ds_read_b128 a[204:207], v244 offset:29184\n\t"
"Lst26_%=:\n\t"
"v_mfma_f32_16x16x32_bf16 v[0:3], a[252:255], a[168:171], v[0:3]\n\t"
"s_waitcnt lgkmcnt(6)\n\t"
"v_xor_b32 v246, 0xc0, v246\n\t"
"v_mfma_f32_16x16x32_bf16 v[0:3], v[236:239], a[172:175], v[0:3]\n\t"
"s_add_u32 s23, s40, 6291456\n\t"
"s_add_u32 s22, s22, 6291456\n\t"
"v_mfma_f32_16x16x32_bf16 v[0:3], v[248:251], a[176:179], v[0:3]\n\t"
"v_mfma_f32_16x16x32_bf16 v[4:7], a[252:255], a[172:175], v[4:7]\n\t"
"s_waitcnt lgkmcnt(5)\n\t"
"v_mfma_f32_16x16x32_bf16 v[4:7], v[236:239], a[176:179], v[4:7]\n\t"
"v_mfma_f32_16x16x32_bf16 v[4:7], v[248:251], a[180:183], v[4:7]\n\t"
"v_mfma_f32_16x16x32_bf16 v[8:11], a[252:255], a[176:179], v[8:11]\n\t"
"s_waitcnt lgkmcnt(4)\n\t"
"v_mfma_f32_16x16x32_bf16 v[8:11], v[236:239], a[180:183], v[8:11]\n\t"
"v_mfma_f32_16x16x32_bf16 v[8:11], v[248:251], a[184:187], v[8:11]\n\t"
"v_mfma_f32_16x16x32_bf16 v[12:15], a[252:255], a[180:183], v[12:15]\n\t"
"s_waitcnt lgkmcnt(3)\n\t"
"v_mfma_f32_16x16x32_bf16 v[12:15], v[236:239], a[184:187], v[12:15]\n\t"
"v_mfma_f32_16x16x32_bf16 v[12:15], v[248:251], a[188:191], v[12:15]\n\t"
"buffer_load_dwordx4 v[96:99], v252, s[8:11], s20 offen offset:2048 sc1\n\t"
"v_mfma_f32_16x16x32_bf16 v[16:19], a[252:255], a[184:187], v[16:19]\n\t"
"s_waitcnt lgkmcnt(2)\n\t"
"s_mov_b32 s20, 958464\n\t"
"v_mfma_f32_16x16x32_bf16 v[16:19], v[236:239], a[188:191], v[16:19]\n\t"
"v_mfma_f32_16x16x32_bf16 v[16:19], v[248:251], a[192:195], v[16:19]\n\t"
"ds_read_b64_tr_b16 v[232:233], v246 offset:512\n\t"
"v_mfma_f32_16x16x32_bf16 v[20:23], a[252:255], a[188:191], v[20:23]\n\t"
"buffer_load_dwordx4 a[224:227], v252, s[8:11], s20 offen offset:0 sc1\n\t"
"v_mfma_f32_16x16x32_bf16 v[20:23], v[236:239], a[192:195], v[20:23]\n\t"
"s_waitcnt lgkmcnt(2)\n\t"
"ds_read_b64_tr_b16 v[234:235], v246 offset:3584\n\t"
"v_mfma_f32_16x16x32_bf16 v[20:23], v[248:251], a[196:199], v[20:23]\n\t"
"v_mfma_f32_16x16x32_bf16 v[24:27], a[252:255], a[192:195], v[24:27]\n\t"
"buffer_load_dwordx4 a[228:231], v252, s[8:11], s20 offen offset:1024 sc1\n\t"
"v_mfma_f32_16x16x32_bf16 v[24:27], v[236:239], a[196:199], v[24:27]\n\t"
"s_waitcnt lgkmcnt(2)\n\t"
"v_mfma_f32_16x16x32_bf16 v[24:27], v[248:251], a[200:203], v[24:27]\n\t"
"v_mfma_f32_16x16x32_bf16 v[28:31], a[252:255], a[196:199], v[28:31]\n\t"
"s_waitcnt vmcnt(11)\n\t"
"ds_read_b64_tr_b16 v[224:225], v246 offset:6656\n\t"
"v_mfma_f32_16x16x32_bf16 v[28:31], v[236:239], a[200:203], v[28:31]\n\t"
"buffer_load_dwordx4 v[236:239], v252, s[8:11], s20 offen offset:2048 sc1\n\t"
"v_mfma_f32_16x16x32_bf16 v[28:31], v[248:251], a[204:207], v[28:31]\n\t"
"s_mov_b32 s20, 970752\n\t"
"ds_read_b64_tr_b16 v[226:227], v246 offset:9728\n\t"
"v_mfma_f32_16x16x32_bf16 v[32:35], a[212:215], a[168:171], v[32:35]\n\t"
"v_mfma_f32_16x16x32_bf16 v[32:35], a[216:219], a[172:175], v[32:35]\n\t"
"v_mfma_f32_16x16x32_bf16 v[32:35], a[208:211], a[176:179], v[32:35]\n\t"
"buffer_load_dwordx4 v[248:251], v252, s[8:11], s20 offen offset:0 sc1\n\t"
"v_mfma_f32_16x16x32_bf16 v[36:39], a[212:215], a[172:175], v[36:39]\n\t"
"s_waitcnt lgkmcnt(2)\n\t"
"v_mfma_f32_16x16x32_bf16 v[36:39], a[216:219], a[176:179], v[36:39]\n\t"
"v_mfma_f32_16x16x32_bf16 v[36:39], a[208:211], a[180:183], v[36:39]\n\t"
"v_mfma_f32_16x16x32_bf16 v[232:235], v[232:235], v[220:223], 0\n\t"
"buffer_load_dwordx4 a[232:235], v252, s[8:11], s20 offen offset:1024 sc1\n\t"
"v_mfma_f32_16x16x32_bf16 v[40:43], a[212:215], a[176:179], v[40:43]\n\t"
"ds_read_b64_tr_b16 v[228:229], v246 offset:12800\n\t"
"v_mfma_f32_16x16x32_bf16 v[40:43], a[216:219], a[180:183], v[40:43]\n\t"
"ds_read_b64_tr_b16 v[230:231], v246 offset:15872\n\t"
"v_mfma_f32_16x16x32_bf16 v[40:43], a[208:211], a[184:187], v[40:43]\n\t"
"v_mfma_f32_16x16x32_bf16 v[44:47], a[212:215], a[180:183], v[44:47]\n\t"
"buffer_load_dwordx4 a[236:239], v252, s[8:11], s20 offen offset:2048 sc1\n\t"
"v_mfma_f32_16x16x32_bf16 v[44:47], a[216:219], a[184:187], v[44:47]\n\t"
"s_waitcnt lgkmcnt(2)\n\t"
"v_cvt_pk_bf16_f32 v232, v232, v233\n\t"
"v_cvt_pk_bf16_f32 v233, v234, v235\n\t"
"v_mfma_f32_16x16x32_bf16 v[44:47], a[208:211], a[188:191], v[44:47]\n\t"
"s_mov_b32 s20, 983040\n\t"
"v_mfma_f32_16x16x32_bf16 v[224:227], v[224:227], v[220:223], 0\n\t"
"v_mfma_f32_16x16x32_bf16 v[48:51], a[212:215], a[184:187], v[48:51]\n\t"
"buffer_load_dwordx4 a[240:243], v252, s[8:11], s20 offen offset:0 sc1\n\t"
"v_mfma_f32_16x16x32_bf16 v[48:51], a[216:219], a[188:191], v[48:51]\n\t"
"v_mfma_f32_16x16x32_bf16 v[48:51], a[208:211], a[192:195], v[48:51]\n\t"
"v_mfma_f32_16x16x32_bf16 v[52:55], a[212:215], a[188:191], v[52:55]\n\t"
"v_mfma_f32_16x16x32_bf16 v[52:55], a[216:219], a[192:195], v[52:55]\n\t"
"buffer_load_dwordx4 a[244:247], v252, s[8:11], s20 offen offset:1024 sc1\n\t"
"v_mfma_f32_16x16x32_bf16 v[52:55], a[208:211], a[196:199], v[52:55]\n\t"
"v_cvt_pk_bf16_f32 v234, v224, v225\n\t"
"v_cvt_pk_bf16_f32 v235, v226, v227\n\t"
"v_mfma_f32_16x16x32_bf16 v[56:59], a[212:215], a[192:195], v[56:59]\n\t"
"s_waitcnt lgkmcnt(0)\n\t"
"ds_read_b64_tr_b16 v[224:225], v246 offset:20480\n\t"
"v_mfma_f32_16x16x32_bf16 v[56:59], a[216:219], a[196:199], v[56:59]\n\t"
"ds_read_b64_tr_b16 v[226:227], v246 offset:23552\n\t"
"v_mfma_f32_16x16x32_bf16 v[56:59], a[208:211], a[200:203], v[56:59]\n\t"
"buffer_load_dwordx4 a[248:251], v252, s[8:11], s20 offen offset:2048 sc1\n\t"
"v_mfma_f32_16x16x32_bf16 v[228:231], v[228:231], v[220:223], 0\n\t"
"s_mov_b32 s20, 995328\n\t"
"v_mfma_f32_16x16x32_bf16 v[60:63], a[212:215], a[196:199], v[60:63]\n\t"
"s_waitcnt vmcnt(9)\n\t"
"v_mfma_f32_16x16x32_bf16 v[60:63], a[216:219], a[200:203], v[60:63]\n\t"
"v_mfma_f32_16x16x32_bf16 v[60:63], a[208:211], a[204:207], v[60:63]\n\t"
"buffer_load_dwordx4 a[212:215], v252, s[8:11], s20 offen offset:0 sc1\n\t"
"v_mfma_f32_16x16x32_bf16 v[64:67], a[220:223], a[168:171], v[64:67]\n\t"
"ds_read_b128 a[168:171], v244 offset:33792\n\t"
"v_mfma_f32_16x16x32_bf16 v[64:67], v[240:243], a[172:175], v[64:67]\n\t"
"v_cvt_pk_bf16_f32 v228, v228, v229\n\t"
"v_cvt_pk_bf16_f32 v229, v230, v231\n\t"
"v_mfma_f32_16x16x32_bf16 v[64:67], v[96:99], a[176:179], v[64:67]\n\t"
"v_mfma_f32_16x16x32_bf16 v[68:71], a[220:223], a[172:175], v[68:71]\n\t"
"buffer_load_dwordx4 a[216:219], v252, s[8:11], s20 offen offset:1024 sc1\n\t"
"v_mfma_f32_16x16x32_bf16 v[68:71], v[240:243], a[176:179], v[68:71]\n\t"
"s_waitcnt lgkmcnt(1)\n\t"
"ds_read_b128 a[172:175], v244 offset:34304\n\t"
"v_mfma_f32_16x16x32_bf16 v[68:71], v[96:99], a[180:183], v[68:71]\n\t"
"buffer_load_dwordx4 a[252:255], v252, s[8:11], s20 offen offset:2048 sc1\n\t"
"v_mfma_f32_16x16x32_bf16 v[224:227], v[224:227], v[220:223], 0\n\t"
"v_mfma_f32_16x16x32_bf16 v[72:75], a[220:223], a[176:179], v[72:75]\n\t"
"ds_read_b128 a[176:179], v244 offset:34816\n\t"
"v_mfma_f32_16x16x32_bf16 v[72:75], v[240:243], a[180:183], v[72:75]\n\t"
"v_mfma_f32_16x16x32_bf16 v[72:75], v[96:99], a[184:187], v[72:75]\n\t"
"v_mfma_f32_16x16x32_bf16 v[76:79], a[220:223], a[180:183], v[76:79]\n\t"
"ds_read_b128 a[180:183], v244 offset:35328\n\t"
"v_mfma_f32_16x16x32_bf16 v[76:79], v[240:243], a[184:187], v[76:79]\n\t"
"v_cvt_pk_bf16_f32 v230, v224, v225\n\t"
"v_cvt_pk_bf16_f32 v231, v226, v227\n\t"
"v_mfma_f32_16x16x32_bf16 v[76:79], v[96:99], a[188:191], v[76:79]\n\t"
"ds_read_b64_tr_b16 v[224:225], v246 offset:26624\n\t"
"v_mfma_f32_16x16x32_bf16 v[80:83], a[220:223], a[184:187], v[80:83]\n\t"
"ds_read_b64_tr_b16 v[226:227], v246 offset:29696\n\t"
"v_mfma_f32_16x16x32_bf16 v[80:83], v[240:243], a[188:191], v[80:83]\n\t"
"ds_read_b128 a[184:187], v244 offset:35840\n\t"
"v_mfma_f32_16x16x32_bf16 v[80:83], v[96:99], a[192:195], v[80:83]\n\t"
"v_mfma_f32_16x16x32_bf16 v[84:87], a[220:223], a[188:191], v[84:87]\n\t"
"s_mov_b32 s20, 1007616\n\t"
"ds_read_b128 a[188:191], v244 offset:36352\n\t"
"v_mfma_f32_16x16x32_bf16 v[84:87], v[240:243], a[192:195], v[84:87]\n\t"
"v_mfma_f32_16x16x32_bf16 v[84:87], v[96:99], a[196:199], v[84:87]\n\t"
"v_mfma_f32_16x16x32_bf16 v[88:91], a[220:223], a[192:195], v[88:91]\n\t"
"ds_read_b128 a[192:195], v244 offset:36864\n\t"
"v_mfma_f32_16x16x32_bf16 v[88:91], v[240:243], a[196:199], v[88:91]\n\t"
"v_mfma_f32_16x16x32_bf16 v[88:91], v[96:99], a[200:203], v[88:91]\n\t"
"v_mfma_f32_16x16x32_bf16 v[92:95], a[220:223], a[196:199], v[92:95]\n\t"
"buffer_load_dwordx4 a[220:223], v252, s[8:11], s20 offen offset:0 sc1\n\t"
"v_mfma_f32_16x16x32_bf16 v[92:95], v[240:243], a[200:203], v[92:95]\n\t"
"s_waitcnt vmcnt(10) lgkmcnt(3)\n\t"
"ds_read_b128 a[196:199], v244 offset:37376\n\t"
"v_mfma_f32_16x16x32_bf16 v[92:95], v[96:99], a[204:207], v[92:95]\n\t"
"ds_read_b128 a[200:203], v244 offset:37888\n\t"
"v_mfma_f32_16x16x32_bf16 v[224:227], v[224:227], v[220:223], 0\n\t"
"ds_read_b128 a[204:207], v244 offset:38400\n\t"
"v_mfma_f32_16x16x32_bf16 v[100:103], a[224:227], a[168:171], v[100:103]\n\t"
"buffer_load_dwordx4 v[96:99], v252, s[8:11], s20 offen offset:1024 sc1\n\t"
"v_mfma_f32_16x16x32_bf16 v[100:103], a[228:231], a[172:175], v[100:103]\n\t"
"v_mfma_f32_16x16x32_bf16 v[100:103], v[236:239], a[176:179], v[100:103]\n\t"
"v_mfma_f32_16x16x32_bf16 v[104:107], a[224:227], a[172:175], v[104:107]\n\t"
"s_waitcnt lgkmcnt(5)\n\t"
"v_mfma_f32_16x16x32_bf16 v[104:107], a[228:231], a[176:179], v[104:107]\n\t"
"v_cvt_pk_bf16_f32 v224, v224, v225\n\t"
"v_mfma_f32_16x16x32_bf16 v[104:107], v[236:239], a[180:183], v[104:107]\n\t"
"s_waitcnt lgkmcnt(0)\n\t"
"s_barrier\n\t"
"v_mfma_f32_16x16x32_bf16 v[108:111], a[224:227], a[176:179], v[108:111]\n\t"
"v_cvt_pk_bf16_f32 v225, v226, v227\n\t"
"v_mfma_f32_16x16x32_bf16 v[108:111], a[228:231], a[180:183], v[108:111]\n\t"
"v_mfma_f32_16x16x32_bf16 v[108:111], v[236:239], a[184:187], v[108:111]\n\t"
"ds_write2st64_b64 v245, v[232:233], v[234:235] offset0:48 offset1:49\n\t"
"v_mfma_f32_16x16x32_bf16 v[112:115], a[224:227], a[180:183], v[112:115]\n\t"
"v_mfma_f32_16x16x32_bf16 v[112:115], a[228:231], a[184:187], v[112:115]\n\t"
"ds_read_b64_tr_b16 v[232:233], v246 offset:32768\n\t"
"v_mfma_f32_16x16x32_bf16 v[112:115], v[236:239], a[188:191], v[112:115]\n\t"
"ds_write2st64_b64 v245, v[228:229], v[230:231] offset0:50 offset1:51\n\t"
"v_mfma_f32_16x16x32_bf16 v[116:119], a[224:227], a[184:187], v[116:119]\n\t"
"global_load_dwordx4 v[240:243], v252, s[48:49] offset:2048\n\t"
"v_mfma_f32_16x16x32_bf16 v[116:119], a[228:231], a[188:191], v[116:119]\n\t"
"ds_read_b64_tr_b16 v[234:235], v246 offset:35840\n\t"
"v_mfma_f32_16x16x32_bf16 v[116:119], v[236:239], a[192:195], v[116:119]\n\t"
"ds_write_b64 v245, v[224:225] offset:26624\n\t"
"v_mfma_f32_16x16x32_bf16 v[120:123], a[224:227], a[188:191], v[120:123]\n\t"
"ds_read_b64_tr_b16 v[228:229], v246 offset:40448\n\t"
"v_mfma_f32_16x16x32_bf16 v[120:123], a[228:231], a[192:195], v[120:123]\n\t"
"ds_read_b64_tr_b16 v[230:231], v246 offset:43520\n\t"
"v_mfma_f32_16x16x32_bf16 v[120:123], v[236:239], a[196:199], v[120:123]\n\t"
"s_waitcnt lgkmcnt(3)\n\t"
"ds_read_b64_tr_b16 v[224:225], v246 offset:46592\n\t"
"v_mfma_f32_16x16x32_bf16 v[232:235], v[232:235], v[220:223], 0\n\t"
"ds_read_b64_tr_b16 v[226:227], v246 offset:49664\n\t"
"v_mfma_f32_16x16x32_bf16 v[124:127], a[224:227], a[192:195], v[124:127]\n\t"
"global_load_dwordx4 a[208:211], v252, s[46:47] offset:1024\n\t"
"v_mfma_f32_16x16x32_bf16 v[124:127], a[228:231], a[196:199], v[124:127]\n\t"
"v_mfma_f32_16x16x32_bf16 v[124:127], v[236:239], a[200:203], v[124:127]\n\t"
"s_waitcnt lgkmcnt(2)\n\t"
"v_mfma_f32_16x16x32_bf16 v[228:231], v[228:231], v[220:223], 0\n\t"
"v_mfma_f32_16x16x32_bf16 v[128:131], a[224:227], a[196:199], v[128:131]\n\t"
"buffer_load_dwordx4 a[224:227], v252, s[8:11], s20 offen offset:2048 sc1\n\t"
"v_mfma_f32_16x16x32_bf16 v[128:131], a[228:231], a[200:203], v[128:131]\n\t"
"s_waitcnt vmcnt(11) lgkmcnt(0)\n\t"
"s_mov_b32 s20, 1019904\n\t"
"v_cvt_pk_bf16_f32 v232, v232, v233\n\t"
"v_mfma_f32_16x16x32_bf16 v[128:131], v[236:239], a[204:207], v[128:131]\n\t"
"v_cvt_pk_bf16_f32 v233, v234, v235\n\t"
"v_mfma_f32_16x16x32_bf16 v[224:227], v[224:227], v[220:223], 0\n\t"
"buffer_load_dwordx4 a[228:231], v252, s[8:11], s20 offen offset:0 sc1\n\t"
"v_mfma_f32_16x16x32_bf16 v[132:135], v[248:251], a[168:171], v[132:135]\n\t"
"s_add_u32 m0, s24, 46848\n\t"
"s_add_u32 s28, s21, 1376256\n\t"
"v_cvt_pk_bf16_f32 v234, v228, v229\n\t"
"v_mfma_f32_16x16x32_bf16 v[132:135], a[232:235], a[172:175], v[132:135]\n\t"
"v_cvt_pk_bf16_f32 v235, v230, v231\n\t"
"v_mfma_f32_16x16x32_bf16 v[132:135], a[236:239], a[176:179], v[132:135]\n\t"
"ds_write2st64_b64 v245, v[232:233], v[234:235] offset0:53 offset1:54\n\t"
"v_mfma_f32_16x16x32_bf16 v[136:139], v[248:251], a[172:175], v[136:139]\n\t"
"buffer_load_dwordx4 v247, s[12:15], s28 offen offset:256 nt lds\n\t"
"v_mfma_f32_16x16x32_bf16 v[136:139], a[232:235], a[176:179], v[136:139]\n\t"
"s_add_u32 m0, s24, 52992\n\t"
"s_add_u32 s28, s21, 1572864\n\t"
"ds_read_b64_tr_b16 v[228:229], v246 offset:52736\n\t"
"v_mfma_f32_16x16x32_bf16 v[136:139], a[236:239], a[180:183], v[136:139]\n\t"
"buffer_load_dwordx4 v247, s[12:15], s28 offen offset:256 nt lds\n\t"
"v_mfma_f32_16x16x32_bf16 v[140:143], v[248:251], a[176:179], v[140:143]\n\t"
"s_add_u32 m0, s24, 92928\n\t"
"s_add_u32 s28, s21, 2752512\n\t"
"ds_read_b64_tr_b16 v[230:231], v246 offset:55808\n\t"
"v_mfma_f32_16x16x32_bf16 v[140:143], a[232:235], a[180:183], v[140:143]\n\t"
"buffer_load_dwordx4 v247, s[12:15], s28 offen offset:256 nt lds\n\t"
"v_mfma_f32_16x16x32_bf16 v[140:143], a[236:239], a[184:187], v[140:143]\n\t"
"s_add_u32 m0, s24, 100608\n\t"
"s_add_u32 s28, s21, 2949120\n\t"
"v_cvt_pk_bf16_f32 v224, v224, v225\n\t"
"v_cvt_pk_bf16_f32 v225, v226, v227\n\t"
"v_mfma_f32_16x16x32_bf16 v[144:147], v[248:251], a[180:183], v[144:147]\n\t"
"v_mfma_f32_16x16x32_bf16 v[144:147], a[232:235], a[184:187], v[144:147]\n\t"
"buffer_load_dwordx4 v247, s[12:15], s28 offen offset:256 nt lds\n\t"
"v_mfma_f32_16x16x32_bf16 v[144:147], a[236:239], a[188:191], v[144:147]\n\t"
"s_add_u32 m0, s24, 106752\n\t"
"s_add_u32 s28, s21, 3145728\n\t"
"v_mfma_f32_16x16x32_bf16 v[148:151], v[248:251], a[184:187], v[148:151]\n\t"
"buffer_load_dwordx4 v247, s[12:15], s28 offen offset:256 nt lds\n\t"
"v_mfma_f32_16x16x32_bf16 v[148:151], a[232:235], a[188:191], v[148:151]\n\t"
"s_waitcnt lgkmcnt(0)\n\t"
"s_add_u32 m0, s24, 112896\n\t"
"s_add_u32 s28, s21, 3342336\n\t"
"v_mfma_f32_16x16x32_bf16 v[148:151], a[236:239], a[192:195], v[148:151]\n\t"
"buffer_load_dwordx4 v247, s[12:15], s28 offen offset:256 nt lds\n\t"
"s_add_u32 m0, s25, 768\n\t"
"v_mfma_f32_16x16x32_bf16 v[228:231], v[228:231], v[220:223], 0\n\t"
"buffer_load_dwordx4 v254, s[12:15], 0 offen offset:256 nt lds\n\t"
"v_mfma_f32_16x16x32_bf16 v[152:155], v[248:251], a[188:191], v[152:155]\n\t"
"s_add_u32 m0, s26, 768\n\t"
"v_mfma_f32_16x16x32_bf16 v[152:155], a[232:235], a[192:195], v[152:155]\n\t"
"v_mfma_f32_16x16x32_bf16 v[152:155], a[236:239], a[196:199], v[152:155]\n\t"
"s_mov_b64 exec, s[36:37]\n\t"
"buffer_load_dwordx4 v254, s[12:15], s27 offen offset:256 nt lds\n\t"
"s_mov_b64 exec, -1\n\t"
"v_mfma_f32_16x16x32_bf16 v[156:159], v[248:251], a[192:195], v[156:159]\n\t"
"s_waitcnt vmcnt(11)\n\t"
"ds_read_b64_tr_b16 v[236:237], v240 offset:512\n\t"
"v_mfma_f32_16x16x32_bf16 v[156:159], a[232:235], a[196:199], v[156:159]\n\t"
"ds_read_b64_tr_b16 v[238:239], v240 offset:2048\n\t"
"v_cvt_pk_bf16_f32 v226, v228, v229\n\t"
"v_mfma_f32_16x16x32_bf16 v[156:159], a[236:239], a[200:203], v[156:159]\n\t"
"ds_read_b64_tr_b16 v[232:233], v241 offset:512\n\t"
"v_cvt_pk_bf16_f32 v227, v230, v231\n\t"
"v_mfma_f32_16x16x32_bf16 v[160:163], v[248:251], a[196:199], v[160:163]\n\t"
"ds_read_b64_tr_b16 v[234:235], v241 offset:2048\n\t"
"v_mfma_f32_16x16x32_bf16 v[160:163], a[232:235], a[200:203], v[160:163]\n\t"
"v_mfma_f32_16x16x32_bf16 v[160:163], a[236:239], a[204:207], v[160:163]\n\t"
"ds_write2st64_b64 v245, v[224:225], v[226:227] offset0:55 offset1:56\n\t"
"v_mfma_f32_16x16x32_bf16 v[164:167], a[240:243], a[168:171], v[164:167]\n\t"
"v_mfma_f32_16x16x32_bf16 v[164:167], a[244:247], a[172:175], v[164:167]\n\t"
"v_mfma_f32_16x16x32_bf16 v[164:167], a[248:251], a[176:179], v[164:167]\n\t"
"s_waitcnt lgkmcnt(0)\n\t"
"s_barrier\n\t"
"v_mfma_f32_16x16x32_bf16 v[168:171], a[240:243], a[172:175], v[168:171]\n\t"
"ds_read_b64_tr_b16 v[228:229], v240 offset:20480\n\t"
"v_mfma_f32_16x16x32_bf16 v[168:171], a[244:247], a[176:179], v[168:171]\n\t"
"ds_read_b64_tr_b16 v[230:231], v240 offset:22016\n\t"
"v_mfma_f32_16x16x32_bf16 v[168:171], a[248:251], a[180:183], v[168:171]\n\t"
"ds_read_b64_tr_b16 v[224:225], v241 offset:20480\n\t"
"v_mfma_f32_16x16x32_bf16 v[172:175], a[240:243], a[176:179], v[172:175]\n\t"
"s_waitcnt vmcnt(10)\n\t"
"ds_read_b64_tr_b16 v[226:227], v241 offset:22016\n\t"
"v_mfma_f32_16x16x32_bf16 v[172:175], a[244:247], a[180:183], v[172:175]\n\t"
"ds_read_b128 a[168:171], v244 offset:24576\n\t"
"v_mfma_f32_16x16x32_bf16 v[172:175], a[248:251], a[184:187], v[172:175]\n\t"
"ds_read_b128 a[172:175], v244 offset:25088\n\t"
"v_mfma_f32_16x16x32_bf16 v[236:239], v[236:239], a[208:211], 0\n\t"
"ds_read_b128 a[176:179], v244 offset:25600\n\t"
"v_mfma_f32_16x16x32_bf16 v[232:235], v[232:235], a[208:211], 0\n\t"
"v_mfma_f32_16x16x32_bf16 v[176:179], a[240:243], a[180:183], v[176:179]\n\t"
"ds_read_b128 a[180:183], v244 offset:26112\n\t"
"v_mfma_f32_16x16x32_bf16 v[176:179], a[244:247], a[184:187], v[176:179]\n\t"
"v_mfma_f32_16x16x32_bf16 v[176:179], a[248:251], a[188:191], v[176:179]\n\t"
"v_mfma_f32_16x16x32_bf16 v[180:183], a[240:243], a[184:187], v[180:183]\n\t"
"s_waitcnt lgkmcnt(6)\n\t"
"ds_read_b128 a[184:187], v244 offset:26624\n\t"
"v_cvt_pk_bf16_f32 v236, v236, v237\n\t"
"v_mfma_f32_16x16x32_bf16 v[180:183], a[244:247], a[188:191], v[180:183]\n\t"
"s_waitcnt lgkmcnt(5)\n\t"
"v_cvt_pk_bf16_f32 v237, v238, v239\n\t"
"v_cvt_pk_bf16_f32 v238, v232, v233\n\t"
"v_mfma_f32_16x16x32_bf16 v[180:183], a[248:251], a[192:195], v[180:183]\n\t"
"v_cvt_pk_bf16_f32 v239, v234, v235\n\t"
"v_mfma_f32_16x16x32_bf16 v[228:231], v[228:231], a[208:211], 0\n\t"
"v_mfma_f32_16x16x32_bf16 v[224:227], v[224:227], a[208:211], 0\n\t"
"ds_read_b64_tr_b16 v[232:233], v240 offset:40448\n\t"
"v_mfma_f32_16x16x32_bf16 v[184:187], a[240:243], a[188:191], v[184:187]\n\t"
"ds_read_b128 a[188:191], v244 offset:27136\n\t"
"v_mfma_f32_16x16x32_bf16 v[184:187], a[244:247], a[192:195], v[184:187]\n\t"
"ds_read_b64_tr_b16 v[234:235], v240 offset:41984\n\t"
"v_mfma_f32_16x16x32_bf16 v[184:187], a[248:251], a[196:199], v[184:187]\n\t"
"v_mfma_f32_16x16x32_bf16 v[188:191], a[240:243], a[192:195], v[188:191]\n\t"
"ds_read_b128 a[192:195], v244 offset:27648\n\t"
"v_cvt_pk_bf16_f32 v228, v228, v229\n\t"
"v_mfma_f32_16x16x32_bf16 v[188:191], a[244:247], a[196:199], v[188:191]\n\t"
"v_cvt_pk_bf16_f32 v229, v230, v231\n\t"
"v_cvt_pk_bf16_f32 v230, v224, v225\n\t"
"v_mfma_f32_16x16x32_bf16 v[188:191], a[248:251], a[200:203], v[188:191]\n\t"
"v_cvt_pk_bf16_f32 v231, v226, v227\n\t"
"v_mfma_f32_16x16x32_bf16 v[192:195], a[240:243], a[196:199], v[192:195]\n\t"
"s_waitcnt lgkmcnt(6)\n\t"
"ds_read_b64_tr_b16 v[224:225], v241 offset:40448\n\t"
"v_mfma_f32_16x16x32_bf16 v[192:195], a[244:247], a[200:203], v[192:195]\n\t"
"ds_read_b64_tr_b16 v[226:227], v241 offset:41984\n\t"
"v_mfma_f32_16x16x32_bf16 v[192:195], a[248:251], a[204:207], v[192:195]\n\t"
"ds_read_b128 a[196:199], v244 offset:28160\n\t"
"Lst27_%=:\n\t"
"v_mfma_f32_16x16x32_bf16 v[196:199], a[212:215], a[168:171], v[196:199]\n\t"
"s_mov_b64 vcc, -1\n\t"
"s_bitcmp1_b32 s73, 1\n\t"
"s_cbranch_scc1 Lvh4_%=\n\t"
"s_bitcmp1_b32 s73, 0\n\t"
"s_cbranch_scc1 Lpg26_%=\n\t"
"s_branch Lpg25_%=\n\t"
"Lvh4_%=:\n\t"
"s_bitcmp1_b32 s73, 0\n\t"
"s_cbranch_scc1 Lvr4_%=\n\t"
"s_branch Lvr4_%=\n\t"
"Lvr4_%=:\n\t"
"s_mov_b64 vcc, 0\n\t"
"s_waitcnt lgkmcnt(8)\n\t"
"ds_read_b128 a[200:203], v244 offset:28672\n\t"
"v_mfma_f32_16x16x32_bf16 v[196:199], a[216:219], a[172:175], v[196:199]\n\t"
"ds_read_b128 a[204:207], v244 offset:29184\n\t"
"v_mfma_f32_16x16x32_bf16 v[196:199], a[252:255], a[176:179], v[196:199]\n\t"
"buffer_load_dwordx4 v[248:251], v252, s[8:11], s20 offen offset:1024 sc1\n\t"
"v_mfma_f32_16x16x32_bf16 v[204:207], a[212:215], a[172:175], v[204:207]\n\t"
"s_waitcnt lgkmcnt(6)\n\t"
"v_mfma_f32_16x16x32_bf16 v[204:207], a[216:219], a[176:179], v[204:207]\n\t"
"s_waitcnt lgkmcnt(3)\n\t"
"v_mfma_f32_16x16x32_bf16 v[204:207], a[252:255], a[180:183], v[204:207]\n\t"
"v_mfma_f32_16x16x32_bf16 v[232:235], v[232:235], a[208:211], 0\n\t"
"v_add_u32 v240, 59904, v240\n\t"
"v_add_u32 v241, 59904, v241\n\t"
"v_mfma_f32_16x16x32_bf16 v[224:227], v[224:227], a[208:211], 0\n\t"
"v_mfma_f32_16x16x32_bf16 v[212:215], a[212:215], a[176:179], v[212:215]\n\t"
"v_mfma_f32_16x16x32_bf16 v[212:215], a[216:219], a[180:183], v[212:215]\n\t"
"v_mfma_f32_16x16x32_bf16 v[212:215], a[252:255], a[184:187], v[212:215]\n\t"
"s_barrier\n\t"
"v_mfma_f32_16x16x32_bf16 a[0:3], a[212:215], a[180:183], a[0:3]\n\t"
"v_cvt_pk_bf16_f32 v232, v232, v233\n\t"
"v_mfma_f32_16x16x32_bf16 a[0:3], a[216:219], a[184:187], a[0:3]\n\t"
"v_cvt_pk_bf16_f32 v233, v234, v235\n\t"
"v_cvt_pk_bf16_f32 v234, v224, v225\n\t"
"v_mfma_f32_16x16x32_bf16 a[0:3], a[252:255], a[188:191], a[0:3]\n\t"
"ds_write_b128 v243, v[236:239] offset:512\n\t"
"v_mfma_f32_16x16x32_bf16 a[8:11], a[212:215], a[184:187], a[8:11]\n\t"
"s_waitcnt lgkmcnt(3)\n\t"
"v_cvt_pk_bf16_f32 v235, v226, v227\n\t"
"v_mfma_f32_16x16x32_bf16 a[8:11], a[216:219], a[188:191], a[8:11]\n\t"
"ds_read_b64_tr_b16 v[236:237], v240 offset:512\n\t"
"v_mfma_f32_16x16x32_bf16 a[8:11], a[252:255], a[192:195], a[8:11]\n\t"
"ds_write_b128 v243, v[228:231] offset:9728\n\t"
"v_mfma_f32_16x16x32_bf16 a[16:19], a[212:215], a[188:191], a[16:19]\n\t"
"buffer_load_dwordx4 a[232:235], v252, s[8:11], s20 offen offset:2048 sc1\n\t"
"v_mfma_f32_16x16x32_bf16 a[16:19], a[216:219], a[192:195], a[16:19]\n\t"
"s_waitcnt lgkmcnt(4)\n\t"
"ds_read_b64_tr_b16 v[238:239], v240 offset:2048\n\t"
"v_mfma_f32_16x16x32_bf16 a[16:19], a[252:255], a[196:199], a[16:19]\n\t"
"s_mov_b32 s20, 1032192\n\t"
"ds_write_b128 v243, v[232:235] offset:18944\n\t"
"v_mfma_f32_16x16x32_bf16 a[24:27], a[212:215], a[192:195], a[24:27]\n\t"
"buffer_load_dwordx4 a[236:239], v252, s[8:11], s20 offen offset:0 sc1\n\t"
"v_mfma_f32_16x16x32_bf16 a[24:27], a[216:219], a[196:199], a[24:27]\n\t"
"s_waitcnt lgkmcnt(5)\n\t"
"ds_read_b64_tr_b16 v[224:225], v241 offset:512\n\t"
"v_mfma_f32_16x16x32_bf16 a[24:27], a[252:255], a[200:203], a[24:27]\n\t"
"ds_read_b64_tr_b16 v[226:227], v241 offset:2048\n\t"
"v_mfma_f32_16x16x32_bf16 a[32:35], a[212:215], a[196:199], a[32:35]\n\t"
"s_waitcnt lgkmcnt(3)\n\t"
"ds_read_b64_tr_b16 v[228:229], v240 offset:20480\n\t"
"v_mfma_f32_16x16x32_bf16 a[32:35], a[216:219], a[200:203], a[32:35]\n\t"
"ds_read_b64_tr_b16 v[230:231], v240 offset:22016\n\t"
"v_mfma_f32_16x16x32_bf16 a[32:35], a[252:255], a[204:207], a[32:35]\n\t"
"s_waitcnt vmcnt(12)\n\t"
"ds_read_b64_tr_b16 v[232:233], v241 offset:20480\n\t"
"v_mfma_f32_16x16x32_bf16 v[236:239], v[236:239], a[208:211], 0\n\t"
"s_waitcnt lgkmcnt(3)\n\t"
"ds_read_b64_tr_b16 v[234:235], v241 offset:22016\n\t"
"v_mfma_f32_16x16x32_bf16 v[224:227], v[224:227], a[208:211], 0\n\t"
"buffer_load_dwordx4 a[212:215], v252, s[8:11], s20 offen offset:1024 sc1\n\t"
"v_mfma_f32_16x16x32_bf16 a[40:43], a[220:223], a[168:171], a[40:43]\n\t"
"buffer_load_dwordx4 a[216:219], v252, s[8:11], s20 offen offset:2048 sc1\n\t"
"v_mfma_f32_16x16x32_bf16 a[40:43], v[96:99], a[172:175], a[40:43]\n\t"
"s_mov_b32 s20, 1044480\n\t"
"v_mfma_f32_16x16x32_bf16 a[40:43], a[224:227], a[176:179], a[40:43]\n\t"
"v_mfma_f32_16x16x32_bf16 a[48:51], a[220:223], a[172:175], a[48:51]\n\t"
"s_waitcnt lgkmcnt(2)\n\t"
"v_cvt_pk_bf16_f32 v236, v236, v237\n\t"
"v_cvt_pk_bf16_f32 v237, v238, v239\n\t"
"v_mfma_f32_16x16x32_bf16 a[48:51], v[96:99], a[176:179], a[48:51]\n\t"
"s_waitcnt lgkmcnt(0)\n\t"
"v_cvt_pk_bf16_f32 v238, v224, v225\n\t"
"v_cvt_pk_bf16_f32 v239, v226, v227\n\t"
"v_mfma_f32_16x16x32_bf16 a[48:51], a[224:227], a[180:183], a[48:51]\n\t"
"ds_write_b128 v243, v[236:239] offset:28160\n\t"
"v_mfma_f32_16x16x32_bf16 v[228:231], v[228:231], a[208:211], 0\n\t"
"buffer_load_dwordx4 a[240:243], v252, s[8:11], s20 offen offset:0 sc1\n\t"
"v_mfma_f32_16x16x32_bf16 v[232:235], v[232:235], a[208:211], 0\n\t"
"ds_read_b64_tr_b16 v[224:225], v240 offset:40448\n\t"
"v_mfma_f32_16x16x32_bf16 a[56:59], a[220:223], a[176:179], a[56:59]\n\t"
"buffer_load_dwordx4 a[244:247], v252, s[8:11], s20 offen offset:1024 sc1\n\t"
"v_mfma_f32_16x16x32_bf16 a[56:59], v[96:99], a[180:183], a[56:59]\n\t"
"ds_read_b64_tr_b16 v[226:227], v240 offset:41984\n\t"
"v_mfma_f32_16x16x32_bf16 a[56:59], a[224:227], a[184:187], a[56:59]\n\t"
"ds_read_b64_tr_b16 v[236:237], v241 offset:40448\n\t"
"v_mfma_f32_16x16x32_bf16 a[64:67], a[220:223], a[180:183], a[64:67]\n\t"
"ds_read_b64_tr_b16 v[238:239], v241 offset:41984\n\t"
"v_cvt_pk_bf16_f32 v228, v228, v229\n\t"
"v_mfma_f32_16x16x32_bf16 a[64:67], v[96:99], a[184:187], a[64:67]\n\t"
"v_cvt_pk_bf16_f32 v229, v230, v231\n\t"
"v_cvt_pk_bf16_f32 v230, v232, v233\n\t"
"v_mfma_f32_16x16x32_bf16 a[64:67], a[224:227], a[188:191], a[64:67]\n\t"
"v_cvt_pk_bf16_f32 v231, v234, v235\n\t"
"v_mfma_f32_16x16x32_bf16 a[72:75], a[220:223], a[184:187], a[72:75]\n\t"
"buffer_load_dwordx4 a[248:251], v252, s[8:11], s20 offen offset:2048 sc1\n\t"
"v_mfma_f32_16x16x32_bf16 a[72:75], v[96:99], a[188:191], a[72:75]\n\t"
"s_mov_b32 s20, 1056768\n\t"
"v_mfma_f32_16x16x32_bf16 a[72:75], a[224:227], a[192:195], a[72:75]\n\t"
"s_waitcnt lgkmcnt(0)\n\t"
"ds_write_b128 v243, v[228:231] offset:37376\n\t"
"v_mfma_f32_16x16x32_bf16 a[80:83], a[220:223], a[188:191], a[80:83]\n\t"
"buffer_load_dwordx4 a[252:255], v252, s[8:11], s20 offen offset:0 sc1\n\t"
"v_mfma_f32_16x16x32_bf16 a[80:83], v[96:99], a[192:195], a[80:83]\n\t"
"Lpg20_%=:\n\t"
"s_cbranch_vccnz Lvr2_%=\n\t"
"v_mfma_f32_16x16x32_bf16 a[80:83], a[224:227], a[196:199], a[80:83]\n\t"
"v_mfma_f32_16x16x32_bf16 a[88:91], a[220:223], a[192:195], a[88:91]\n\t"
"v_mfma_f32_16x16x32_bf16 a[88:91], v[96:99], a[196:199], a[88:91]\n\t"
"v_mfma_f32_16x16x32_bf16 a[88:91], a[224:227], a[200:203], a[88:91]\n\t"
"v_mfma_f32_16x16x32_bf16 v[224:227], v[224:227], a[208:211], 0\n\t"
"v_mfma_f32_16x16x32_bf16 v[236:239], v[236:239], a[208:211], 0\n\t"
"ds_read_b64_tr_b16 v[232:233], v246 offset:1024\n\t"
"v_mfma_f32_16x16x32_bf16 a[96:99], a[220:223], a[196:199], a[96:99]\n\t"
"buffer_load_dwordx4 a[220:223], v252, s[8:11], s20 offen offset:1024 sc1\n\t"
"v_mfma_f32_16x16x32_bf16 a[96:99], v[96:99], a[200:203], a[96:99]\n\t"
"s_waitcnt vmcnt(8)\n\t"
"ds_read_b64_tr_b16 v[234:235], v246 offset:4096\n\t"
"v_mfma_f32_16x16x32_bf16 a[96:99], a[224:227], a[204:207], a[96:99]\n\t"
"v_mfma_f32_16x16x32_bf16 a[104:107], a[228:231], a[168:171], a[104:107]\n\t"
"buffer_load_dwordx4 v[96:99], v252, s[8:11], s20 offen offset:2048 sc1\n\t"
"v_mfma_f32_16x16x32_bf16 a[104:107], v[248:251], a[172:175], a[104:107]\n\t"
"s_mov_b32 s20, 1290240\n\t"
"ds_read_b64_tr_b16 v[228:229], v246 offset:7168\n\t"
"v_cvt_pk_bf16_f32 v224, v224, v225\n\t"
"v_mfma_f32_16x16x32_bf16 a[104:107], a[232:235], a[176:179], a[104:107]\n\t"
"ds_read_b64_tr_b16 v[230:231], v246 offset:10240\n\t"
"v_cvt_pk_bf16_f32 v225, v226, v227\n\t"
"v_mfma_f32_16x16x32_bf16 a[112:115], a[228:231], a[172:175], a[112:115]\n\t"
"ds_read_b128 a[168:171], v244 offset:33792\n\t"
"v_cvt_pk_bf16_f32 v226, v236, v237\n\t"
"v_mfma_f32_16x16x32_bf16 a[112:115], v[248:251], a[176:179], a[112:115]\n\t"
"ds_read_b128 a[172:175], v244 offset:34304\n\t"
"v_cvt_pk_bf16_f32 v227, v238, v239\n\t"
"v_mfma_f32_16x16x32_bf16 a[112:115], a[232:235], a[180:183], a[112:115]\n\t"
"ds_write_b128 v243, v[224:227] offset:46592\n\t"
"v_mfma_f32_16x16x32_bf16 a[120:123], a[228:231], a[176:179], a[120:123]\n\t"
"buffer_load_dwordx4 a[224:227], v252, s[8:11], s20 offen offset:0 sc1\n\t"
"v_mfma_f32_16x16x32_bf16 a[120:123], v[248:251], a[180:183], a[120:123]\n\t"
"ds_read_b64_tr_b16 v[236:237], v246 offset:13312\n\t"
"v_mfma_f32_16x16x32_bf16 a[120:123], a[232:235], a[184:187], a[120:123]\n\t"
"ds_read_b64_tr_b16 v[238:239], v246 offset:16384\n\t"
"v_mfma_f32_16x16x32_bf16 a[128:131], a[228:231], a[180:183], a[128:131]\n\t"
"s_waitcnt lgkmcnt(7)\n\t"
"ds_read_b128 a[176:179], v244 offset:34816\n\t"
"v_mfma_f32_16x16x32_bf16 a[128:131], v[248:251], a[184:187], a[128:131]\n\t"
"ds_read_b64_tr_b16 v[224:225], v246 offset:20992\n\t"
"v_mfma_f32_16x16x32_bf16 a[128:131], a[232:235], a[188:191], a[128:131]\n\t"
"ds_read_b64_tr_b16 v[226:227], v246 offset:24064\n\t"
"v_mfma_f32_16x16x32_bf16 v[232:235], v[232:235], v[220:223], 0\n\t"
"ds_read_b128 a[180:183], v244 offset:35328\n\t"
"v_mfma_f32_16x16x32_bf16 a[136:139], a[228:231], a[184:187], a[136:139]\n\t"
"s_waitcnt lgkmcnt(9)\n\t"
"ds_read_b128 a[184:187], v244 offset:35840\n\t"
"v_mfma_f32_16x16x32_bf16 a[136:139], v[248:251], a[188:191], a[136:139]\n\t"
"v_mfma_f32_16x16x32_bf16 a[136:139], a[232:235], a[192:195], a[136:139]\n\t"
"v_mfma_f32_16x16x32_bf16 v[228:231], v[228:231], v[220:223], 0\n\t"
"v_mfma_f32_16x16x32_bf16 a[144:147], a[228:231], a[188:191], a[144:147]\n\t"
"s_waitcnt lgkmcnt(5)\n\t"
"ds_read_b128 a[188:191], v244 offset:36352\n\t"
"v_cvt_pk_bf16_f32 v232, v232, v233\n\t"
"v_mfma_f32_16x16x32_bf16 a[144:147], v[248:251], a[192:195], a[144:147]\n\t"
"v_cvt_pk_bf16_f32 v233, v234, v235\n\t"
"v_mfma_f32_16x16x32_bf16 a[144:147], a[232:235], a[196:199], a[144:147]\n\t"
"v_mfma_f32_16x16x32_bf16 v[236:239], v[236:239], v[220:223], 0\n\t"
"v_mfma_f32_16x16x32_bf16 a[152:155], a[228:231], a[192:195], a[152:155]\n\t"
"s_waitcnt lgkmcnt(3)\n\t"
"ds_read_b128 a[192:195], v244 offset:36864\n\t"
"v_cvt_pk_bf16_f32 v234, v228, v229\n\t"
"v_mfma_f32_16x16x32_bf16 a[152:155], v[248:251], a[196:199], a[152:155]\n\t"
"v_cvt_pk_bf16_f32 v235, v230, v231\n\t"
"v_mfma_f32_16x16x32_bf16 a[152:155], a[232:235], a[200:203], a[152:155]\n\t"
"ds_read_b64_tr_b16 v[228:229], v246 offset:27136\n\t"
"v_mfma_f32_16x16x32_bf16 v[224:227], v[224:227], v[220:223], 0\n\t"
"ds_read_b64_tr_b16 v[230:231], v246 offset:30208\n\t"
"v_mfma_f32_16x16x32_bf16 a[160:163], a[228:231], a[196:199], a[160:163]\n\t"
"buffer_load_dwordx4 a[228:231], v252, s[8:11], s20 offen offset:1024 sc1\n\t"
"v_mfma_f32_16x16x32_bf16 a[160:163], v[248:251], a[200:203], a[160:163]\n\t"
"s_waitcnt vmcnt(8)\n\t"
"ds_read_b128 a[196:199], v244 offset:37376\n\t"
"v_cvt_pk_bf16_f32 v236, v236, v237\n\t"
"v_mfma_f32_16x16x32_bf16 a[160:163], a[232:235], a[204:207], a[160:163]\n\t"
"ds_read_b128 a[200:203], v244 offset:37888\n\t"
"v_cvt_pk_bf16_f32 v237, v238, v239\n\t"
"v_mfma_f32_16x16x32_bf16 v[200:203], a[236:239], a[168:171], v[200:203]\n\t"
"ds_read_b128 a[204:207], v244 offset:38400\n\t"
"v_mfma_f32_16x16x32_bf16 v[200:203], a[212:215], a[172:175], v[200:203]\n\t"
"buffer_load_dwordx4 v[248:251], v252, s[8:11], s20 offen offset:2048 sc1\n\t"
"v_mfma_f32_16x16x32_bf16 v[200:203], a[216:219], a[176:179], v[200:203]\n\t"
"s_waitcnt lgkmcnt(3)\n\t"
"s_mov_b32 s20, 1302528\n\t"
"v_cvt_pk_bf16_f32 v238, v224, v225\n\t"
"v_mfma_f32_16x16x32_bf16 v[228:231], v[228:231], v[220:223], 0\n\t"
"v_cvt_pk_bf16_f32 v239, v226, v227\n\t"
"v_mfma_f32_16x16x32_bf16 v[208:211], a[236:239], a[172:175], v[208:211]\n\t"
"buffer_load_dwordx4 a[232:235], v252, s[8:11], s20 offen offset:0 sc1\n\t"
"v_mfma_f32_16x16x32_bf16 v[208:211], a[212:215], a[176:179], v[208:211]\n\t"
"v_mfma_f32_16x16x32_bf16 v[208:211], a[216:219], a[180:183], v[208:211]\n\t"
"s_waitcnt lgkmcnt(0)\n\t"
"s_barrier\n\t"
"v_mfma_f32_16x16x32_bf16 v[216:219], a[236:239], a[176:179], v[216:219]\n\t"
"ds_read_b64_tr_b16 v[224:225], v246 offset:33280\n\t"
"v_mfma_f32_16x16x32_bf16 v[216:219], a[212:215], a[180:183], v[216:219]\n\t"
"ds_read_b64_tr_b16 v[226:227], v246 offset:36352\n\t"
"v_cvt_pk_bf16_f32 v228, v228, v229\n\t"
"v_mfma_f32_16x16x32_bf16 v[216:219], a[216:219], a[184:187], v[216:219]\n\t"
"ds_write2st64_b64 v245, v[232:233], v[234:235] offset0:48 offset1:49\n\t"
"v_mfma_f32_16x16x32_bf16 a[4:7], a[236:239], a[180:183], a[4:7]\n\t"
"v_cvt_pk_bf16_f32 v229, v230, v231\n\t"
"v_mfma_f32_16x16x32_bf16 a[4:7], a[212:215], a[184:187], a[4:7]\n\t"
"ds_read_b64_tr_b16 v[232:233], v246 offset:40960\n\t"
"v_mfma_f32_16x16x32_bf16 a[4:7], a[216:219], a[188:191], a[4:7]\n\t"
"ds_write2st64_b64 v245, v[236:237], v[238:239] offset0:50 offset1:51\n\t"
"v_mfma_f32_16x16x32_bf16 a[12:15], a[236:239], a[184:187], a[12:15]\n\t"
"v_mfma_f32_16x16x32_bf16 a[12:15], a[212:215], a[188:191], a[12:15]\n\t"
"ds_read_b64_tr_b16 v[234:235], v246 offset:44032\n\t"
"v_mfma_f32_16x16x32_bf16 a[12:15], a[216:219], a[192:195], a[12:15]\n\t"
"ds_write_b64 v245, v[228:229] offset:26624\n\t"
"v_mfma_f32_16x16x32_bf16 a[20:23], a[236:239], a[188:191], a[20:23]\n\t"
"s_waitcnt lgkmcnt(5)\n\t"
"ds_read_b64_tr_b16 v[228:229], v246 offset:47104\n\t"
"v_mfma_f32_16x16x32_bf16 a[20:23], a[212:215], a[192:195], a[20:23]\n\t"
"ds_read_b64_tr_b16 v[230:231], v246 offset:50176\n\t"
"v_mfma_f32_16x16x32_bf16 a[20:23], a[216:219], a[196:199], a[20:23]\n\t"
"v_mfma_f32_16x16x32_bf16 v[224:227], v[224:227], v[220:223], 0\n\t"
"v_mfma_f32_16x16x32_bf16 a[28:31], a[236:239], a[192:195], a[28:31]\n\t"
"s_waitcnt lgkmcnt(3)\n\t"
"v_mfma_f32_16x16x32_bf16 a[28:31], a[212:215], a[196:199], a[28:31]\n\t"
"v_mfma_f32_16x16x32_bf16 a[28:31], a[216:219], a[200:203], a[28:31]\n\t"
"v_mfma_f32_16x16x32_bf16 v[232:235], v[232:235], v[220:223], 0\n\t"
"v_mfma_f32_16x16x32_bf16 a[36:39], a[236:239], a[196:199], a[36:39]\n\t"
"buffer_load_dwordx4 a[236:239], v252, s[8:11], s20 offen offset:1024 sc1\n\t"
"v_mfma_f32_16x16x32_bf16 a[36:39], a[212:215], a[200:203], a[36:39]\n\t"
"s_waitcnt vmcnt(8) lgkmcnt(0)\n\t"
"v_cvt_pk_bf16_f32 v224, v224, v225\n\t"
"v_cvt_pk_bf16_f32 v225, v226, v227\n\t"
"v_mfma_f32_16x16x32_bf16 a[36:39], a[216:219], a[204:207], a[36:39]\n\t"
"v_mfma_f32_16x16x32_bf16 v[228:231], v[228:231], v[220:223], 0\n\t"
"buffer_load_dwordx4 a[212:215], v252, s[8:11], s20 offen offset:2048 sc1\n\t"
"v_mfma_f32_16x16x32_bf16 a[44:47], a[240:243], a[168:171], a[44:47]\n\t"
"s_mov_b32 s20, 1314816\n\t"
"v_cvt_pk_bf16_f32 v226, v232, v233\n\t"
"v_cvt_pk_bf16_f32 v227, v234, v235\n\t"
"v_mfma_f32_16x16x32_bf16 a[44:47], a[244:247], a[172:175], a[44:47]\n\t"
"ds_read_b64_tr_b16 v[232:233], v246 offset:53248\n\t"
"v_mfma_f32_16x16x32_bf16 a[44:47], a[248:251], a[176:179], a[44:47]\n\t"
"buffer_load_dwordx4 a[216:219], v252, s[8:11], s20 offen offset:0 sc1\n\t"
"v_mfma_f32_16x16x32_bf16 a[52:55], a[240:243], a[172:175], a[52:55]\n\t"
"ds_read_b64_tr_b16 v[234:235], v246 offset:56320\n\t"
"v_mfma_f32_16x16x32_bf16 a[52:55], a[244:247], a[176:179], a[52:55]\n\t"
"v_cvt_pk_bf16_f32 v228, v228, v229\n\t"
"v_cvt_pk_bf16_f32 v229, v230, v231\n\t"
"v_mfma_f32_16x16x32_bf16 a[52:55], a[248:251], a[180:183], a[52:55]\n\t"
"ds_write2st64_b64 v245, v[224:225], v[226:227] offset0:53 offset1:54\n\t"
"v_mfma_f32_16x16x32_bf16 a[60:63], a[240:243], a[176:179], a[60:63]\n\t"
"v_mfma_f32_16x16x32_bf16 a[60:63], a[244:247], a[180:183], a[60:63]\n\t"
"v_add_u32 v243, 55296, v243\n\t"
"v_mfma_f32_16x16x32_bf16 a[60:63], a[248:251], a[184:187], a[60:63]\n\t"
"v_mfma_f32_16x16x32_bf16 a[68:71], a[240:243], a[180:183], a[68:71]\n\t"
"v_mfma_f32_16x16x32_bf16 a[68:71], a[244:247], a[184:187], a[68:71]\n\t"
"v_mfma_f32_16x16x32_bf16 a[68:71], a[248:251], a[188:191], a[68:71]\n\t"
"v_mfma_f32_16x16x32_bf16 a[76:79], a[240:243], a[184:187], a[76:79]\n\t"
"s_waitcnt lgkmcnt(1)\n\t"
"v_mfma_f32_16x16x32_bf16 a[76:79], a[244:247], a[188:191], a[76:79]\n\t"
"v_mfma_f32_16x16x32_bf16 a[76:79], a[248:251], a[192:195], a[76:79]\n\t"
"v_mfma_f32_16x16x32_bf16 v[232:235], v[232:235], v[220:223], 0\n\t"
"v_mfma_f32_16x16x32_bf16 a[84:87], a[240:243], a[188:191], a[84:87]\n\t"
"v_mfma_f32_16x16x32_bf16 a[84:87], a[244:247], a[192:195], a[84:87]\n\t"
"v_mfma_f32_16x16x32_bf16 a[84:87], a[248:251], a[196:199], a[84:87]\n\t"
"v_mfma_f32_16x16x32_bf16 a[92:95], a[240:243], a[192:195], a[92:95]\n\t"
"v_mfma_f32_16x16x32_bf16 a[92:95], a[244:247], a[196:199], a[92:95]\n\t"
"v_cvt_pk_bf16_f32 v230, v232, v233\n\t"
"v_cvt_pk_bf16_f32 v231, v234, v235\n\t"
"v_mfma_f32_16x16x32_bf16 a[92:95], a[248:251], a[200:203], a[92:95]\n\t"
"ds_write2st64_b64 v245, v[228:229], v[230:231] offset0:55 offset1:56\n\t"
"v_mfma_f32_16x16x32_bf16 a[100:103], a[240:243], a[196:199], a[100:103]\n\t"
"buffer_load_dwordx4 a[240:243], v252, s[8:11], s20 offen offset:1024 sc1\n\t"
"v_mfma_f32_16x16x32_bf16 a[100:103], a[244:247], a[200:203], a[100:103]\n\t"
"s_waitcnt vmcnt(8)\n\t"
"v_mfma_f32_16x16x32_bf16 a[100:103], a[248:251], a[204:207], a[100:103]\n\t"
"v_mfma_f32_16x16x32_bf16 a[108:111], a[252:255], a[168:171], a[108:111]\n\t"
"buffer_load_dwordx4 a[244:247], v252, s[8:11], s20 offen offset:2048 sc1\n\t"
"v_mfma_f32_16x16x32_bf16 a[108:111], a[220:223], a[172:175], a[108:111]\n\t"
"s_mov_b32 s20, 1327104\n\t"
"v_mfma_f32_16x16x32_bf16 a[108:111], v[96:99], a[176:179], a[108:111]\n\t"
"s_waitcnt lgkmcnt(0)\n\t"
"s_barrier\n\t"
"v_mfma_f32_16x16x32_bf16 a[116:119], a[252:255], a[172:175], a[116:119]\n\t"
"ds_read_b64_tr_b16 v[236:237], v240 offset:1024\n\t"
"v_mfma_f32_16x16x32_bf16 a[116:119], a[220:223], a[176:179], a[116:119]\n\t"
"ds_read_b64_tr_b16 v[238:239], v240 offset:2560\n\t"
"v_mfma_f32_16x16x32_bf16 a[116:119], v[96:99], a[180:183], a[116:119]\n\t"
"ds_read_b64_tr_b16 v[224:225], v241 offset:1024\n\t"
"v_mfma_f32_16x16x32_bf16 a[124:127], a[252:255], a[176:179], a[124:127]\n\t"
"ds_read_b64_tr_b16 v[226:227], v241 offset:2560\n\t"
"v_mfma_f32_16x16x32_bf16 a[124:127], a[220:223], a[180:183], a[124:127]\n\t"
"buffer_load_dwordx4 a[248:251], v252, s[8:11], s20 offen offset:0 sc1\n\t"
"v_mfma_f32_16x16x32_bf16 a[124:127], v[96:99], a[184:187], a[124:127]\n\t"
"ds_read_b64_tr_b16 v[232:233], v240 offset:20992\n\t"
"v_mfma_f32_16x16x32_bf16 a[132:135], a[252:255], a[180:183], a[132:135]\n\t"
"s_waitcnt lgkmcnt(3)\n\t"
"ds_read_b64_tr_b16 v[234:235], v240 offset:22528\n\t"
"v_mfma_f32_16x16x32_bf16 a[132:135], a[220:223], a[184:187], a[132:135]\n\t"
"ds_read_b64_tr_b16 v[228:229], v241 offset:20992\n\t"
"v_mfma_f32_16x16x32_bf16 a[132:135], v[96:99], a[188:191], a[132:135]\n\t"
"s_waitcnt lgkmcnt(3)\n\t"
"ds_read_b64_tr_b16 v[230:231], v241 offset:22528\n\t"
"v_mfma_f32_16x16x32_bf16 v[236:239], v[236:239], a[208:211], 0\n\t"
"ds_read_b128 a[168:171], v244 offset:24576\n\t"
"v_mfma_f32_16x16x32_bf16 v[224:227], v[224:227], a[208:211], 0\n\t"
"ds_read_b128 a[172:175], v244 offset:25088\n\t"
"v_mfma_f32_16x16x32_bf16 a[140:143], a[252:255], a[184:187], a[140:143]\n\t"
"ds_read_b128 a[176:179], v244 offset:25600\n\t"
"v_mfma_f32_16x16x32_bf16 a[140:143], a[220:223], a[188:191], a[140:143]\n\t"
"ds_read_b128 a[180:183], v244 offset:26112\n\t"
"v_mfma_f32_16x16x32_bf16 a[140:143], v[96:99], a[192:195], a[140:143]\n\t"
"ds_read_b128 a[184:187], v244 offset:26624\n\t"
"v_mfma_f32_16x16x32_bf16 a[148:151], a[252:255], a[188:191], a[148:151]\n\t"
"s_waitcnt lgkmcnt(7)\n\t"
"ds_read_b128 a[188:191], v244 offset:27136\n\t"
"v_cvt_pk_bf16_f32 v236, v236, v237\n\t"
"v_mfma_f32_16x16x32_bf16 a[148:151], a[220:223], a[192:195], a[148:151]\n\t"
"s_waitcnt lgkmcnt(6)\n\t"
"v_cvt_pk_bf16_f32 v237, v238, v239\n\t"
"v_cvt_pk_bf16_f32 v238, v224, v225\n\t"
"v_mfma_f32_16x16x32_bf16 a[148:151], v[96:99], a[196:199], a[148:151]\n\t"
"v_cvt_pk_bf16_f32 v239, v226, v227\n\t"
"v_mfma_f32_16x16x32_bf16 v[232:235], v[232:235], a[208:211], 0\n\t"
"ds_write_b128 v243, v[236:239] offset:28160\n\t"
"v_mfma_f32_16x16x32_bf16 v[228:231], v[228:231], a[208:211], 0\n\t"
"v_mfma_f32_16x16x32_bf16 a[156:159], a[252:255], a[192:195], a[156:159]\n\t"
"ds_read_b128 a[192:195], v244 offset:27648\n\t"
"v_mfma_f32_16x16x32_bf16 a[156:159], a[220:223], a[196:199], a[156:159]\n\t"
"ds_read_b64_tr_b16 v[224:225], v240 offset:40960\n\t"
"v_mfma_f32_16x16x32_bf16 a[156:159], v[96:99], a[200:203], a[156:159]\n\t"
"ds_read_b64_tr_b16 v[226:227], v240 offset:42496\n\t"
"v_mfma_f32_16x16x32_bf16 a[164:167], a[252:255], a[196:199], a[164:167]\n\t"
"buffer_load_dwordx4 a[252:255], v252, s[8:11], s20 offen offset:1024 sc1\n\t"
"v_mfma_f32_16x16x32_bf16 a[164:167], a[220:223], a[200:203], a[164:167]\n\t"
"s_waitcnt vmcnt(8) lgkmcnt(7)\n\t"
"ds_read_b64_tr_b16 v[236:237], v241 offset:40960\n\t"
"v_cvt_pk_bf16_f32 v232, v232, v233\n\t"
"v_mfma_f32_16x16x32_bf16 a[164:167], v[96:99], a[204:207], a[164:167]\n\t"
"ds_read_b64_tr_b16 v[238:239], v241 offset:42496\n\t"
"v_cvt_pk_bf16_f32 v233, v234, v235\n\t"
"Lst28_%=:\n\t"
"v_mfma_f32_16x16x32_bf16 v[196:199], a[224:227], a[168:171], v[196:199]\n\t"
"s_waitcnt lgkmcnt(8)\n\t"
"ds_read_b128 a[196:199], v244 offset:28160\n\t"
"v_cvt_pk_bf16_f32 v234, v228, v229\n\t"
"v_mfma_f32_16x16x32_bf16 v[196:199], a[228:231], a[172:175], v[196:199]\n\t"
"ds_read_b128 a[200:203], v244 offset:28672\n\t"
"v_cvt_pk_bf16_f32 v235, v230, v231\n\t"
"v_mfma_f32_16x16x32_bf16 v[196:199], v[248:251], a[176:179], v[196:199]\n\t"
"ds_write_b128 v243, v[232:235] offset:37376\n\t"
"v_mfma_f32_16x16x32_bf16 v[204:207], a[224:227], a[172:175], v[204:207]\n\t"
"buffer_load_dwordx4 v[96:99], v252, s[8:11], s20 offen offset:2048 sc1\n\t"
"v_mfma_f32_16x16x32_bf16 v[204:207], a[228:231], a[176:179], v[204:207]\n\t"
"s_waitcnt lgkmcnt(10)\n\t"
"ds_read_b128 a[204:207], v244 offset:29184\n\t"
"v_mfma_f32_16x16x32_bf16 v[204:207], v[248:251], a[180:183], v[204:207]\n\t"
"s_mov_b32 s20, 1339392\n\t"
"v_subrev_u32 v240, 59904, v240\n\t"
"v_subrev_u32 v241, 59904, v241\n\t"
"v_mfma_f32_16x16x32_bf16 v[212:215], a[224:227], a[176:179], v[212:215]\n\t"
"s_waitcnt lgkmcnt(6)\n\t"
"ds_read_b64_tr_b16 v[228:229], v240 offset:1024\n\t"
"v_mfma_f32_16x16x32_bf16 v[212:215], a[228:231], a[180:183], v[212:215]\n\t"
"buffer_load_dwordx4 a[220:223], v252, s[8:11], s20 offen offset:0 sc1\n\t"
"v_mfma_f32_16x16x32_bf16 v[212:215], v[248:251], a[184:187], v[212:215]\n\t"
"s_waitcnt lgkmcnt(5)\n\t"
"ds_read_b64_tr_b16 v[230:231], v240 offset:2560\n\t"
"v_mfma_f32_16x16x32_bf16 v[224:227], v[224:227], a[208:211], 0\n\t"
"ds_read_b64_tr_b16 v[232:233], v241 offset:1024\n\t"
"v_mfma_f32_16x16x32_bf16 v[236:239], v[236:239], a[208:211], 0\n\t"
"ds_read_b64_tr_b16 v[234:235], v241 offset:2560\n\t"
"v_mfma_f32_16x16x32_bf16 a[0:3], a[224:227], a[180:183], a[0:3]\n\t"
"v_mfma_f32_16x16x32_bf16 a[0:3], a[228:231], a[184:187], a[0:3]\n\t"
"v_mfma_f32_16x16x32_bf16 a[0:3], v[248:251], a[188:191], a[0:3]\n\t"
"v_mfma_f32_16x16x32_bf16 a[8:11], a[224:227], a[184:187], a[8:11]\n\t"
"s_waitcnt lgkmcnt(2)\n\t"
"v_cvt_pk_bf16_f32 v224, v224, v225\n\t"
"v_cvt_pk_bf16_f32 v225, v226, v227\n\t"
"v_mfma_f32_16x16x32_bf16 a[8:11], a[228:231], a[188:191], a[8:11]\n\t"
"s_waitcnt lgkmcnt(0)\n\t"
"v_cvt_pk_bf16_f32 v226, v236, v237\n\t"
"v_cvt_pk_bf16_f32 v227, v238, v239\n\t"
"v_mfma_f32_16x16x32_bf16 a[8:11], v[248:251], a[192:195], a[8:11]\n\t"
"ds_write_b128 v243, v[224:227] offset:46592\n\t"
"v_mfma_f32_16x16x32_bf16 v[228:231], v[228:231], a[208:211], 0\n\t"
"v_mfma_f32_16x16x32_bf16 v[232:235], v[232:235], a[208:211], 0\n\t"
"ds_read_b64_tr_b16 v[236:237], v240 offset:20992\n\t"
"v_mfma_f32_16x16x32_bf16 a[16:19], a[224:227], a[188:191], a[16:19]\n\t"
"ds_read_b64_tr_b16 v[238:239], v240 offset:22528\n\t"
"v_mfma_f32_16x16x32_bf16 a[16:19], a[228:231], a[192:195], a[16:19]\n\t"
"ds_read_b64_tr_b16 v[224:225], v241 offset:20992\n\t"
"v_mfma_f32_16x16x32_bf16 a[16:19], v[248:251], a[196:199], a[16:19]\n\t"
"ds_read_b64_tr_b16 v[226:227], v241 offset:22528\n\t"
"v_mfma_f32_16x16x32_bf16 a[24:27], a[224:227], a[192:195], a[24:27]\n\t"
"v_cvt_pk_bf16_f32 v228, v228, v229\n\t"
"v_cvt_pk_bf16_f32 v229, v230, v231\n\t"
"v_mfma_f32_16x16x32_bf16 a[24:27], a[228:231], a[196:199], a[24:27]\n\t"
"v_cvt_pk_bf16_f32 v230, v232, v233\n\t"
"v_cvt_pk_bf16_f32 v231, v234, v235\n\t"
"v_mfma_f32_16x16x32_bf16 a[24:27], v[248:251], a[200:203], a[24:27]\n\t"
"ds_write_b128 v243, v[228:231] offset:512\n\t"
"v_mfma_f32_16x16x32_bf16 a[32:35], a[224:227], a[196:199], a[32:35]\n\t"
"buffer_load_dwordx4 a[224:227], v252, s[8:11], s20 offen offset:1024 sc1\n\t"
"v_mfma_f32_16x16x32_bf16 a[32:35], a[228:231], a[200:203], a[32:35]\n\t"
"s_waitcnt vmcnt(8)\n\t"
"ds_read_b64_tr_b16 v[232:233], v240 offset:40960\n\t"
"v_mfma_f32_16x16x32_bf16 a[32:35], v[248:251], a[204:207], a[32:35]\n\t"
"ds_read_b64_tr_b16 v[234:235], v240 offset:42496\n\t"
"v_mfma_f32_16x16x32_bf16 a[40:43], a[232:235], a[168:171], a[40:43]\n\t"
"buffer_load_dwordx4 v[248:251], v252, s[8:11], s20 offen offset:2048 sc1\n\t"
"v_mfma_f32_16x16x32_bf16 a[40:43], a[236:239], a[172:175], a[40:43]\n\t"
"s_mov_b32 s20, 1351680\n\t"
"ds_read_b64_tr_b16 v[228:229], v241 offset:40960\n\t"
"v_mfma_f32_16x16x32_bf16 a[40:43], a[212:215], a[176:179], a[40:43]\n\t"
"ds_read_b64_tr_b16 v[230:231], v241 offset:42496\n\t"
"v_mfma_f32_16x16x32_bf16 a[48:51], a[232:235], a[172:175], a[48:51]\n\t"
"buffer_load_dwordx4 a[228:231], v252, s[8:11], s20 offen offset:0 sc1\n\t"
"v_mfma_f32_16x16x32_bf16 a[48:51], a[236:239], a[176:179], a[48:51]\n\t"
"s_waitcnt lgkmcnt(5)\n\t"
"v_mfma_f32_16x16x32_bf16 a[48:51], a[212:215], a[180:183], a[48:51]\n\t"
"v_mfma_f32_16x16x32_bf16 v[236:239], v[236:239], a[208:211], 0\n\t"
"v_mfma_f32_16x16x32_bf16 v[224:227], v[224:227], a[208:211], 0\n\t"
"v_xor_b32 v246, 0xc0, v246\n\t"
"v_mfma_f32_16x16x32_bf16 a[56:59], a[232:235], a[176:179], a[56:59]\n\t"
"v_mfma_f32_16x16x32_bf16 a[56:59], a[236:239], a[180:183], a[56:59]\n\t"
"v_mfma_f32_16x16x32_bf16 a[56:59], a[212:215], a[184:187], a[56:59]\n\t"
"v_mfma_f32_16x16x32_bf16 a[64:67], a[232:235], a[180:183], a[64:67]\n\t"
"s_waitcnt lgkmcnt(2)\n\t"
"v_cvt_pk_bf16_f32 v236, v236, v237\n\t"
"v_cvt_pk_bf16_f32 v237, v238, v239\n\t"
"v_mfma_f32_16x16x32_bf16 a[64:67], a[236:239], a[184:187], a[64:67]\n\t"
"s_waitcnt lgkmcnt(0)\n\t"
"v_cvt_pk_bf16_f32 v238, v224, v225\n\t"
"v_cvt_pk_bf16_f32 v239, v226, v227\n\t"
"v_mfma_f32_16x16x32_bf16 a[64:67], a[212:215], a[188:191], a[64:67]\n\t"
"ds_write_b128 v243, v[236:239] offset:9728\n\t"
"v_mfma_f32_16x16x32_bf16 v[232:235], v[232:235], a[208:211], 0\n\t"
"v_mfma_f32_16x16x32_bf16 v[228:231], v[228:231], a[208:211], 0\n\t"
"v_mfma_f32_16x16x32_bf16 a[72:75], a[232:235], a[184:187], a[72:75]\n\t"
"v_mfma_f32_16x16x32_bf16 a[72:75], a[236:239], a[188:191], a[72:75]\n\t"
"v_mfma_f32_16x16x32_bf16 a[72:75], a[212:215], a[192:195], a[72:75]\n\t"
"v_mfma_f32_16x16x32_bf16 a[80:83], a[232:235], a[188:191], a[80:83]\n\t"
"v_cvt_pk_bf16_f32 v232, v232, v233\n\t"
"v_cvt_pk_bf16_f32 v233, v234, v235\n\t"
"v_mfma_f32_16x16x32_bf16 a[80:83], a[236:239], a[192:195], a[80:83]\n\t"
"v_cvt_pk_bf16_f32 v234, v228, v229\n\t"
"v_cvt_pk_bf16_f32 v235, v230, v231\n\t"
"v_mfma_f32_16x16x32_bf16 a[80:83], a[212:215], a[196:199], a[80:83]\n\t"
"ds_write_b128 v243, v[232:235] offset:18944\n\t"
"v_mfma_f32_16x16x32_bf16 a[88:91], a[232:235], a[192:195], a[88:91]\n\t"
"v_mfma_f32_16x16x32_bf16 a[88:91], a[236:239], a[196:199], a[88:91]\n\t"
"ds_read_b64_tr_b16 v[224:225], v246 offset:1024\n\t"
"v_mfma_f32_16x16x32_bf16 a[88:91], a[212:215], a[200:203], a[88:91]\n\t"
"ds_read_b64_tr_b16 v[226:227], v246 offset:4096\n\t"
"v_mfma_f32_16x16x32_bf16 a[96:99], a[232:235], a[196:199], a[96:99]\n\t"
"buffer_load_dwordx4 a[232:235], v252, s[8:11], s20 offen offset:1024 sc1\n\t"
"v_mfma_f32_16x16x32_bf16 a[96:99], a[236:239], a[200:203], a[96:99]\n\t"
"s_waitcnt vmcnt(8)\n\t"
"ds_read_b64_tr_b16 v[236:237], v246 offset:7168\n\t"
"v_mfma_f32_16x16x32_bf16 a[96:99], a[212:215], a[204:207], a[96:99]\n\t"
"ds_read_b64_tr_b16 v[238:239], v246 offset:10240\n\t"
"v_mfma_f32_16x16x32_bf16 a[104:107], a[216:219], a[168:171], a[104:107]\n\t"
"buffer_load_dwordx4 a[212:215], v252, s[8:11], s20 offen offset:2048 sc1\n\t"
"v_mfma_f32_16x16x32_bf16 a[104:107], a[240:243], a[172:175], a[104:107]\n\t"
"s_mov_b32 s20, 1216512\n\t"
"ds_read_b64_tr_b16 v[228:229], v246 offset:13312\n\t"
"v_mfma_f32_16x16x32_bf16 a[104:107], a[244:247], a[176:179], a[104:107]\n\t"
"ds_read_b64_tr_b16 v[230:231], v246 offset:16384\n\t"
"v_mfma_f32_16x16x32_bf16 a[112:115], a[216:219], a[172:175], a[112:115]\n\t"
"buffer_load_dwordx4 a[236:239], v252, s[8:11], s20 offen offset:0 sc1\n\t"
"v_mfma_f32_16x16x32_bf16 a[112:115], a[240:243], a[176:179], a[112:115]\n\t"
"ds_read_b128 a[168:171], v244 offset:33792\n\t"
"v_mfma_f32_16x16x32_bf16 a[112:115], a[244:247], a[180:183], a[112:115]\n\t"
"ds_read_b64_tr_b16 v[232:233], v246 offset:20992\n\t"
"v_mfma_f32_16x16x32_bf16 a[120:123], a[216:219], a[176:179], a[120:123]\n\t"
"s_waitcnt lgkmcnt(6)\n\t"
"ds_read_b64_tr_b16 v[234:235], v246 offset:24064\n\t"
"v_mfma_f32_16x16x32_bf16 a[120:123], a[240:243], a[180:183], a[120:123]\n\t"
"ds_read_b128 a[172:175], v244 offset:34304\n\t"
"v_mfma_f32_16x16x32_bf16 a[120:123], a[244:247], a[184:187], a[120:123]\n\t"
"ds_read_b128 a[176:179], v244 offset:34816\n\t"
"v_mfma_f32_16x16x32_bf16 v[224:227], v[224:227], v[220:223], 0\n\t"
"v_mfma_f32_16x16x32_bf16 a[128:131], a[216:219], a[180:183], a[128:131]\n\t"
"s_waitcnt lgkmcnt(7)\n\t"
"ds_read_b128 a[180:183], v244 offset:35328\n\t"
"v_mfma_f32_16x16x32_bf16 a[128:131], a[240:243], a[184:187], a[128:131]\n\t"
"v_mfma_f32_16x16x32_bf16 a[128:131], a[244:247], a[188:191], a[128:131]\n\t"
"v_mfma_f32_16x16x32_bf16 v[236:239], v[236:239], v[220:223], 0\n\t"
"v_mfma_f32_16x16x32_bf16 a[136:139], a[216:219], a[184:187], a[136:139]\n\t"
"s_waitcnt lgkmcnt(6)\n\t"
"ds_read_b128 a[184:187], v244 offset:35840\n\t"
"v_cvt_pk_bf16_f32 v224, v224, v225\n\t"
"v_mfma_f32_16x16x32_bf16 a[136:139], a[240:243], a[188:191], a[136:139]\n\t"
"v_cvt_pk_bf16_f32 v225, v226, v227\n\t"
"v_mfma_f32_16x16x32_bf16 a[136:139], a[244:247], a[192:195], a[136:139]\n\t"
"v_mfma_f32_16x16x32_bf16 v[228:231], v[228:231], v[220:223], 0\n\t"
"v_mfma_f32_16x16x32_bf16 a[144:147], a[216:219], a[188:191], a[144:147]\n\t"
"s_waitcnt lgkmcnt(4)\n\t"
"ds_read_b128 a[188:191], v244 offset:36352\n\t"
"v_cvt_pk_bf16_f32 v226, v236, v237\n\t"
"v_mfma_f32_16x16x32_bf16 a[144:147], a[240:243], a[192:195], a[144:147]\n\t"
"v_cvt_pk_bf16_f32 v227, v238, v239\n\t"
"v_mfma_f32_16x16x32_bf16 a[144:147], a[244:247], a[196:199], a[144:147]\n\t"
"v_mfma_f32_16x16x32_bf16 v[232:235], v[232:235], v[220:223], 0\n\t"
"v_mfma_f32_16x16x32_bf16 a[152:155], a[216:219], a[192:195], a[152:155]\n\t"
"ds_read_b128 a[192:195], v244 offset:36864\n\t"
"v_cvt_pk_bf16_f32 v228, v228, v229\n\t"
"v_mfma_f32_16x16x32_bf16 a[152:155], a[240:243], a[196:199], a[152:155]\n\t"
"v_cvt_pk_bf16_f32 v229, v230, v231\n\t"
"v_mfma_f32_16x16x32_bf16 a[152:155], a[244:247], a[200:203], a[152:155]\n\t"
"v_mfma_f32_16x16x32_bf16 a[160:163], a[216:219], a[196:199], a[160:163]\n\t"
"buffer_load_dwordx4 a[216:219], v252, s[8:11], s20 offen offset:1024 sc1\n\t"
"v_mfma_f32_16x16x32_bf16 a[160:163], a[240:243], a[200:203], a[160:163]\n\t"
"s_waitcnt vmcnt(8) lgkmcnt(4)\n\t"
"ds_read_b128 a[196:199], v244 offset:37376\n\t"
"v_cvt_pk_bf16_f32 v230, v232, v233\n\t"
"v_mfma_f32_16x16x32_bf16 a[160:163], a[244:247], a[204:207], a[160:163]\n\t"
"ds_read_b128 a[200:203], v244 offset:37888\n\t"
"v_cvt_pk_bf16_f32 v231, v234, v235\n\t"
"v_mfma_f32_16x16x32_bf16 v[200:203], a[248:251], a[168:171], v[200:203]\n\t"
"ds_read_b64_tr_b16 v[232:233], v246 offset:27136\n\t"
"v_mfma_f32_16x16x32_bf16 v[200:203], a[252:255], a[172:175], v[200:203]\n\t"
"s_waitcnt lgkmcnt(6)\n\t"
"ds_read_b64_tr_b16 v[234:235], v246 offset:30208\n\t"
"v_mfma_f32_16x16x32_bf16 v[200:203], v[96:99], a[176:179], v[200:203]\n\t"
"s_waitcnt lgkmcnt(0)\n\t"
"ds_read_b128 a[204:207], v244 offset:38400\n\t"
"v_mfma_f32_16x16x32_bf16 v[232:235], v[232:235], v[220:223], 0\n\t"
"buffer_load_dwordx4 a[240:243], v252, s[8:11], s20 offen offset:2048 sc1\n\t"
"s_mov_b32 s20, 1228800\n\t"
"v_mfma_f32_16x16x32_bf16 v[208:211], a[248:251], a[172:175], v[208:211]\n\t"
"buffer_load_dwordx4 a[244:247], v252, s[8:11], s20 offen offset:0 sc1\n\t"
"v_mfma_f32_16x16x32_bf16 v[208:211], a[252:255], a[176:179], v[208:211]\n\t"
"v_mfma_f32_16x16x32_bf16 v[208:211], v[96:99], a[180:183], v[208:211]\n\t"
"s_waitcnt lgkmcnt(0)\n\t"
"s_barrier\n\t"
"v_mfma_f32_16x16x32_bf16 v[216:219], a[248:251], a[176:179], v[216:219]\n\t"
"v_mfma_f32_16x16x32_bf16 v[216:219], a[252:255], a[180:183], v[216:219]\n\t"
"v_cvt_pk_bf16_f32 v232, v232, v233\n\t"
"v_cvt_pk_bf16_f32 v233, v234, v235\n\t"
"v_mfma_f32_16x16x32_bf16 v[216:219], v[96:99], a[184:187], v[216:219]\n\t"
"ds_write2st64_b64 v245, v[224:225], v[226:227] offset0:48 offset1:49\n\t"
"v_mfma_f32_16x16x32_bf16 a[4:7], a[248:251], a[180:183], a[4:7]\n\t"
"v_mfma_f32_16x16x32_bf16 a[4:7], a[252:255], a[184:187], a[4:7]\n\t"
"ds_read_b64_tr_b16 v[224:225], v246 offset:33280\n\t"
"v_mfma_f32_16x16x32_bf16 a[4:7], v[96:99], a[188:191], a[4:7]\n\t"
"ds_write2st64_b64 v245, v[228:229], v[230:231] offset0:50 offset1:51\n\t"
"v_mfma_f32_16x16x32_bf16 a[12:15], a[248:251], a[184:187], a[12:15]\n\t"
"v_mfma_f32_16x16x32_bf16 a[12:15], a[252:255], a[188:191], a[12:15]\n\t"
"ds_read_b64_tr_b16 v[226:227], v246 offset:36352\n\t"
"v_mfma_f32_16x16x32_bf16 a[12:15], v[96:99], a[192:195], a[12:15]\n\t"
"ds_write_b64 v245, v[232:233] offset:26624\n\t"
"v_mfma_f32_16x16x32_bf16 a[20:23], a[248:251], a[188:191], a[20:23]\n\t"
"ds_read_b64_tr_b16 v[228:229], v246 offset:40960\n\t"
"v_mfma_f32_16x16x32_bf16 a[20:23], a[252:255], a[192:195], a[20:23]\n\t"
"ds_read_b64_tr_b16 v[230:231], v246 offset:44032\n\t"
"v_mfma_f32_16x16x32_bf16 a[20:23], v[96:99], a[196:199], a[20:23]\n\t"
"s_waitcnt lgkmcnt(3)\n\t"
"ds_read_b64_tr_b16 v[232:233], v246 offset:47104\n\t"
"v_mfma_f32_16x16x32_bf16 v[224:227], v[224:227], v[220:223], 0\n\t"
"ds_read_b64_tr_b16 v[234:235], v246 offset:50176\n\t"
"v_mfma_f32_16x16x32_bf16 a[28:31], a[248:251], a[192:195], a[28:31]\n\t"
"v_mfma_f32_16x16x32_bf16 a[28:31], a[252:255], a[196:199], a[28:31]\n\t"
"v_mfma_f32_16x16x32_bf16 a[28:31], v[96:99], a[200:203], a[28:31]\n\t"
"s_waitcnt lgkmcnt(2)\n\t"
"v_mfma_f32_16x16x32_bf16 v[228:231], v[228:231], v[220:223], 0\n\t"
"v_mfma_f32_16x16x32_bf16 a[36:39], a[248:251], a[196:199], a[36:39]\n\t"
"buffer_load_dwordx4 a[248:251], v252, s[8:11], s20 offen offset:1024 sc1\n\t"
"v_mfma_f32_16x16x32_bf16 a[36:39], a[252:255], a[200:203], a[36:39]\n\t"
"s_waitcnt vmcnt(8) lgkmcnt(0)\n\t"
"v_cvt_pk_bf16_f32 v224, v224, v225\n\t"
"v_cvt_pk_bf16_f32 v225, v226, v227\n\t"
"v_mfma_f32_16x16x32_bf16 a[36:39], v[96:99], a[204:207], a[36:39]\n\t"
"v_mfma_f32_16x16x32_bf16 v[232:235], v[232:235], v[220:223], 0\n\t"
"buffer_load_dwordx4 v[96:99], v252, s[8:11], s20 offen offset:2048 sc1\n\t"
"v_mfma_f32_16x16x32_bf16 a[44:47], a[220:223], a[168:171], a[44:47]\n\t"
"s_mov_b32 s20, 1241088\n\t"
"v_cvt_pk_bf16_f32 v226, v228, v229\n\t"
"v_cvt_pk_bf16_f32 v227, v230, v231\n\t"
"v_mfma_f32_16x16x32_bf16 a[44:47], a[224:227], a[172:175], a[44:47]\n\t"
"ds_read_b64_tr_b16 v[228:229], v246 offset:53248\n\t"
"v_mfma_f32_16x16x32_bf16 a[44:47], v[248:251], a[176:179], a[44:47]\n\t"
"buffer_load_dwordx4 a[252:255], v252, s[8:11], s20 offen offset:0 sc1\n\t"
"v_mfma_f32_16x16x32_bf16 a[52:55], a[220:223], a[172:175], a[52:55]\n\t"
"ds_read_b64_tr_b16 v[230:231], v246 offset:56320\n\t"
"v_mfma_f32_16x16x32_bf16 a[52:55], a[224:227], a[176:179], a[52:55]\n\t"
"v_cvt_pk_bf16_f32 v232, v232, v233\n\t"
"v_cvt_pk_bf16_f32 v233, v234, v235\n\t"
"v_mfma_f32_16x16x32_bf16 a[52:55], v[248:251], a[180:183], a[52:55]\n\t"
"ds_write2st64_b64 v245, v[224:225], v[226:227] offset0:53 offset1:54\n\t"
"v_mfma_f32_16x16x32_bf16 a[60:63], a[220:223], a[176:179], a[60:63]\n\t"
"v_mfma_f32_16x16x32_bf16 a[60:63], a[224:227], a[180:183], a[60:63]\n\t"
"v_subrev_u32 v243, 55296, v243\n\t"
"v_mfma_f32_16x16x32_bf16 a[60:63], v[248:251], a[184:187], a[60:63]\n\t"
"v_mfma_f32_16x16x32_bf16 a[68:71], a[220:223], a[180:183], a[68:71]\n\t"
"v_mfma_f32_16x16x32_bf16 a[68:71], a[224:227], a[184:187], a[68:71]\n\t"
"v_mfma_f32_16x16x32_bf16 a[68:71], v[248:251], a[188:191], a[68:71]\n\t"
"v_mfma_f32_16x16x32_bf16 a[76:79], a[220:223], a[184:187], a[76:79]\n\t"
"s_waitcnt lgkmcnt(1)\n\t"
"v_mfma_f32_16x16x32_bf16 a[76:79], a[224:227], a[188:191], a[76:79]\n\t"
"v_mfma_f32_16x16x32_bf16 a[76:79], v[248:251], a[192:195], a[76:79]\n\t"
"v_mfma_f32_16x16x32_bf16 v[228:231], v[228:231], v[220:223], 0\n\t"
"v_mfma_f32_16x16x32_bf16 a[84:87], a[220:223], a[188:191], a[84:87]\n\t"
"v_mfma_f32_16x16x32_bf16 a[84:87], a[224:227], a[192:195], a[84:87]\n\t"
"v_mfma_f32_16x16x32_bf16 a[84:87], v[248:251], a[196:199], a[84:87]\n\t"
"v_mfma_f32_16x16x32_bf16 a[92:95], a[220:223], a[192:195], a[92:95]\n\t"
"v_mfma_f32_16x16x32_bf16 a[92:95], a[224:227], a[196:199], a[92:95]\n\t"
"v_cvt_pk_bf16_f32 v234, v228, v229\n\t"
"v_cvt_pk_bf16_f32 v235, v230, v231\n\t"
"v_mfma_f32_16x16x32_bf16 a[92:95], v[248:251], a[200:203], a[92:95]\n\t"
"ds_write2st64_b64 v245, v[232:233], v[234:235] offset0:55 offset1:56\n\t"
"v_mfma_f32_16x16x32_bf16 a[100:103], a[220:223], a[196:199], a[100:103]\n\t"
"buffer_load_dwordx4 a[220:223], v252, s[8:11], s20 offen offset:1024 sc1\n\t"
"v_mfma_f32_16x16x32_bf16 a[100:103], a[224:227], a[200:203], a[100:103]\n\t"
"s_waitcnt vmcnt(8)\n\t"
"ds_read_b64_tr_b16 v[236:237], v240 offset:1536\n\t"
"v_mfma_f32_16x16x32_bf16 a[100:103], v[248:251], a[204:207], a[100:103]\n\t"
"ds_read_b64_tr_b16 v[238:239], v240 offset:3072\n\t"
"v_mfma_f32_16x16x32_bf16 a[108:111], a[228:231], a[168:171], a[108:111]\n\t"
"buffer_load_dwordx4 v[248:251], v252, s[8:11], s20 offen offset:2048 sc1\n\t"
"v_mfma_f32_16x16x32_bf16 a[108:111], a[232:235], a[172:175], a[108:111]\n\t"
"s_mov_b32 s20, 1253376\n\t"
"ds_read_b64_tr_b16 v[224:225], v241 offset:1536\n\t"
"v_mfma_f32_16x16x32_bf16 a[108:111], a[212:215], a[176:179], a[108:111]\n\t"
"s_waitcnt lgkmcnt(3)\n\t"
"s_barrier\n\t"
"v_mfma_f32_16x16x32_bf16 a[116:119], a[228:231], a[172:175], a[116:119]\n\t"
"ds_read_b64_tr_b16 v[226:227], v241 offset:3072\n\t"
"v_mfma_f32_16x16x32_bf16 a[116:119], a[232:235], a[176:179], a[116:119]\n\t"
"buffer_load_dwordx4 a[224:227], v252, s[8:11], s20 offen offset:0 sc1\n\t"
"v_mfma_f32_16x16x32_bf16 a[116:119], a[212:215], a[180:183], a[116:119]\n\t"
"ds_read_b64_tr_b16 v[228:229], v240 offset:21504\n\t"
"v_mfma_f32_16x16x32_bf16 a[124:127], a[228:231], a[176:179], a[124:127]\n\t"
"s_waitcnt lgkmcnt(3)\n\t"
"ds_read_b64_tr_b16 v[230:231], v240 offset:23040\n\t"
"v_mfma_f32_16x16x32_bf16 a[124:127], a[232:235], a[180:183], a[124:127]\n\t"
"ds_read_b64_tr_b16 v[232:233], v241 offset:21504\n\t"
"v_mfma_f32_16x16x32_bf16 a[124:127], a[212:215], a[184:187], a[124:127]\n\t"
"Lpg21_%=:\n\t"
"s_cbranch_vccnz Lvr3_%=\n\t"
"ds_read_b64_tr_b16 v[234:235], v241 offset:23040\n\t"
"v_mfma_f32_16x16x32_bf16 a[132:135], a[228:231], a[180:183], a[132:135]\n\t"
"ds_read_b128 a[168:171], v244 offset:24576\n\t"
"v_mfma_f32_16x16x32_bf16 a[132:135], a[232:235], a[184:187], a[132:135]\n\t"
"s_waitcnt lgkmcnt(5)\n\t"
"ds_read_b128 a[172:175], v244 offset:25088\n\t"
"v_mfma_f32_16x16x32_bf16 a[132:135], a[212:215], a[188:191], a[132:135]\n\t"
"ds_read_b128 a[176:179], v244 offset:25600\n\t"
"v_mfma_f32_16x16x32_bf16 v[236:239], v[236:239], a[208:211], 0\n\t"
"ds_read_b128 a[180:183], v244 offset:26112\n\t"
"v_mfma_f32_16x16x32_bf16 v[224:227], v[224:227], a[208:211], 0\n\t"
"v_mfma_f32_16x16x32_bf16 a[140:143], a[228:231], a[184:187], a[140:143]\n\t"
"ds_read_b128 a[184:187], v244 offset:26624\n\t"
"v_mfma_f32_16x16x32_bf16 a[140:143], a[232:235], a[188:191], a[140:143]\n\t"
"v_mfma_f32_16x16x32_bf16 a[140:143], a[212:215], a[192:195], a[140:143]\n\t"
"v_mfma_f32_16x16x32_bf16 a[148:151], a[228:231], a[188:191], a[148:151]\n\t"
"s_waitcnt lgkmcnt(7)\n\t"
"ds_read_b128 a[188:191], v244 offset:27136\n\t"
"v_cvt_pk_bf16_f32 v236, v236, v237\n\t"
"v_mfma_f32_16x16x32_bf16 a[148:151], a[232:235], a[192:195], a[148:151]\n\t"
"s_waitcnt lgkmcnt(6)\n\t"
"v_cvt_pk_bf16_f32 v237, v238, v239\n\t"
"v_cvt_pk_bf16_f32 v238, v224, v225\n\t"
"v_mfma_f32_16x16x32_bf16 a[148:151], a[212:215], a[196:199], a[148:151]\n\t"
"v_cvt_pk_bf16_f32 v239, v226, v227\n\t"
"v_mfma_f32_16x16x32_bf16 v[228:231], v[228:231], a[208:211], 0\n\t"
"ds_write_b128 v243, v[236:239] offset:1024\n\t"
"v_mfma_f32_16x16x32_bf16 v[232:235], v[232:235], a[208:211], 0\n\t"
"v_mfma_f32_16x16x32_bf16 a[156:159], a[228:231], a[192:195], a[156:159]\n\t"
"ds_read_b128 a[192:195], v244 offset:27648\n\t"
"v_mfma_f32_16x16x32_bf16 a[156:159], a[232:235], a[196:199], a[156:159]\n\t"
"ds_read_b64_tr_b16 v[224:225], v240 offset:41472\n\t"
"v_mfma_f32_16x16x32_bf16 a[156:159], a[212:215], a[200:203], a[156:159]\n\t"
"ds_read_b64_tr_b16 v[226:227], v240 offset:43008\n\t"
"v_mfma_f32_16x16x32_bf16 a[164:167], a[228:231], a[196:199], a[164:167]\n\t"
"buffer_load_dwordx4 a[228:231], v252, s[8:11], s20 offen offset:1024 sc1\n\t"
"v_mfma_f32_16x16x32_bf16 a[164:167], a[232:235], a[200:203], a[164:167]\n\t"
"s_waitcnt vmcnt(8) lgkmcnt(7)\n\t"
"ds_read_b64_tr_b16 v[236:237], v241 offset:41472\n\t"
"v_cvt_pk_bf16_f32 v228, v228, v229\n\t"
"v_mfma_f32_16x16x32_bf16 a[164:167], a[212:215], a[204:207], a[164:167]\n\t"
"ds_read_b64_tr_b16 v[238:239], v241 offset:43008\n\t"
"v_cvt_pk_bf16_f32 v229, v230, v231\n\t"
"Lst29_%=:\n\t"
"v_mfma_f32_16x16x32_bf16 v[0:3], a[236:239], a[168:171], v[0:3]\n\t"
"s_waitcnt lgkmcnt(8)\n\t"
"ds_read_b128 a[196:199], v244 offset:28160\n\t"
"v_cvt_pk_bf16_f32 v230, v232, v233\n\t"
"v_mfma_f32_16x16x32_bf16 v[0:3], a[216:219], a[172:175], v[0:3]\n\t"
"ds_read_b128 a[200:203], v244 offset:28672\n\t"
"v_cvt_pk_bf16_f32 v231, v234, v235\n\t"
"v_mfma_f32_16x16x32_bf16 v[0:3], a[240:243], a[176:179], v[0:3]\n\t"
"ds_write_b128 v243, v[228:231] offset:10240\n\t"
"v_mfma_f32_16x16x32_bf16 v[4:7], a[236:239], a[172:175], v[4:7]\n\t"
"buffer_load_dwordx4 a[212:215], v252, s[8:11], s20 offen offset:2048 sc1\n\t"
"v_mfma_f32_16x16x32_bf16 v[4:7], a[216:219], a[176:179], v[4:7]\n\t"
"s_waitcnt lgkmcnt(10)\n\t"
"ds_read_b128 a[204:207], v244 offset:29184\n\t"
"v_mfma_f32_16x16x32_bf16 v[4:7], a[240:243], a[180:183], v[4:7]\n\t"
"s_mov_b32 s20, 1265664\n\t"
"v_add_u32 v240, 59904, v240\n\t"
"v_add_u32 v241, 59904, v241\n\t"
"v_mfma_f32_16x16x32_bf16 v[8:11], a[236:239], a[176:179], v[8:11]\n\t"
"s_waitcnt lgkmcnt(6)\n\t"
"ds_read_b64_tr_b16 v[232:233], v240 offset:1536\n\t"
"v_mfma_f32_16x16x32_bf16 v[8:11], a[216:219], a[180:183], v[8:11]\n\t"
"buffer_load_dwordx4 a[232:235], v252, s[8:11], s20 offen offset:0 sc1\n\t"
"v_mfma_f32_16x16x32_bf16 v[8:11], a[240:243], a[184:187], v[8:11]\n\t"
"s_waitcnt lgkmcnt(5)\n\t"
"ds_read_b64_tr_b16 v[234:235], v240 offset:3072\n\t"
"v_mfma_f32_16x16x32_bf16 v[224:227], v[224:227], a[208:211], 0\n\t"
"ds_read_b64_tr_b16 v[228:229], v241 offset:1536\n\t"
"v_mfma_f32_16x16x32_bf16 v[236:239], v[236:239], a[208:211], 0\n\t"
"ds_read_b64_tr_b16 v[230:231], v241 offset:3072\n\t"
"v_mfma_f32_16x16x32_bf16 v[12:15], a[236:239], a[180:183], v[12:15]\n\t"
"v_mfma_f32_16x16x32_bf16 v[12:15], a[216:219], a[184:187], v[12:15]\n\t"
"v_mfma_f32_16x16x32_bf16 v[12:15], a[240:243], a[188:191], v[12:15]\n\t"
"v_mfma_f32_16x16x32_bf16 v[16:19], a[236:239], a[184:187], v[16:19]\n\t"
"s_waitcnt lgkmcnt(2)\n\t"
"v_cvt_pk_bf16_f32 v224, v224, v225\n\t"
"v_cvt_pk_bf16_f32 v225, v226, v227\n\t"
"v_mfma_f32_16x16x32_bf16 v[16:19], a[216:219], a[188:191], v[16:19]\n\t"
"s_waitcnt lgkmcnt(0)\n\t"
"v_cvt_pk_bf16_f32 v226, v236, v237\n\t"
"v_cvt_pk_bf16_f32 v227, v238, v239\n\t"
"v_mfma_f32_16x16x32_bf16 v[16:19], a[240:243], a[192:195], v[16:19]\n\t"
"ds_write_b128 v243, v[224:227] offset:19456\n\t"
"v_mfma_f32_16x16x32_bf16 v[232:235], v[232:235], a[208:211], 0\n\t"
"v_mfma_f32_16x16x32_bf16 v[228:231], v[228:231], a[208:211], 0\n\t"
"ds_read_b64_tr_b16 v[236:237], v240 offset:21504\n\t"
"v_mfma_f32_16x16x32_bf16 v[20:23], a[236:239], a[188:191], v[20:23]\n\t"
"ds_read_b64_tr_b16 v[238:239], v240 offset:23040\n\t"
"v_mfma_f32_16x16x32_bf16 v[20:23], a[216:219], a[192:195], v[20:23]\n\t"
"ds_read_b64_tr_b16 v[224:225], v241 offset:21504\n\t"
"v_mfma_f32_16x16x32_bf16 v[20:23], a[240:243], a[196:199], v[20:23]\n\t"
"ds_read_b64_tr_b16 v[226:227], v241 offset:23040\n\t"
"v_mfma_f32_16x16x32_bf16 v[24:27], a[236:239], a[192:195], v[24:27]\n\t"
"v_cvt_pk_bf16_f32 v232, v232, v233\n\t"
"v_cvt_pk_bf16_f32 v233, v234, v235\n\t"
"v_mfma_f32_16x16x32_bf16 v[24:27], a[216:219], a[196:199], v[24:27]\n\t"
"v_cvt_pk_bf16_f32 v234, v228, v229\n\t"
"v_cvt_pk_bf16_f32 v235, v230, v231\n\t"
"v_mfma_f32_16x16x32_bf16 v[24:27], a[240:243], a[200:203], v[24:27]\n\t"
"ds_write_b128 v243, v[232:235] offset:28672\n\t"
"v_mfma_f32_16x16x32_bf16 v[28:31], a[236:239], a[196:199], v[28:31]\n\t"
"buffer_load_dwordx4 a[236:239], v252, s[8:11], s20 offen offset:1024 sc1\n\t"
"v_mfma_f32_16x16x32_bf16 v[28:31], a[216:219], a[200:203], v[28:31]\n\t"
"s_waitcnt vmcnt(8)\n\t"
"ds_read_b64_tr_b16 v[228:229], v240 offset:41472\n\t"
"v_mfma_f32_16x16x32_bf16 v[28:31], a[240:243], a[204:207], v[28:31]\n\t"
"ds_read_b64_tr_b16 v[230:231], v240 offset:43008\n\t"
"v_mfma_f32_16x16x32_bf16 v[32:35], a[244:247], a[168:171], v[32:35]\n\t"
"buffer_load_dwordx4 a[216:219], v252, s[8:11], s20 offen offset:2048 sc1\n\t"
"v_mfma_f32_16x16x32_bf16 v[32:35], a[248:251], a[172:175], v[32:35]\n\t"
"s_mov_b32 s20, 1277952\n\t"
"ds_read_b64_tr_b16 v[232:233], v241 offset:41472\n\t"
"v_mfma_f32_16x16x32_bf16 v[32:35], v[96:99], a[176:179], v[32:35]\n\t"
"ds_read_b64_tr_b16 v[234:235], v241 offset:43008\n\t"
"v_mfma_f32_16x16x32_bf16 v[36:39], a[244:247], a[172:175], v[36:39]\n\t"
"buffer_load_dwordx4 a[240:243], v252, s[8:11], s20 offen offset:0 sc1\n\t"
"v_mfma_f32_16x16x32_bf16 v[36:39], a[248:251], a[176:179], v[36:39]\n\t"
"s_waitcnt lgkmcnt(5)\n\t"
"v_mfma_f32_16x16x32_bf16 v[36:39], v[96:99], a[180:183], v[36:39]\n\t"
"v_mfma_f32_16x16x32_bf16 v[236:239], v[236:239], a[208:211], 0\n\t"
"v_mfma_f32_16x16x32_bf16 v[224:227], v[224:227], a[208:211], 0\n\t"
"v_mfma_f32_16x16x32_bf16 v[40:43], a[244:247], a[176:179], v[40:43]\n\t"
"v_mfma_f32_16x16x32_bf16 v[40:43], a[248:251], a[180:183], v[40:43]\n\t"
"v_mfma_f32_16x16x32_bf16 v[40:43], v[96:99], a[184:187], v[40:43]\n\t"
"v_mfma_f32_16x16x32_bf16 v[44:47], a[244:247], a[180:183], v[44:47]\n\t"
"s_waitcnt lgkmcnt(2)\n\t"
"v_cvt_pk_bf16_f32 v236, v236, v237\n\t"
"v_cvt_pk_bf16_f32 v237, v238, v239\n\t"
"v_mfma_f32_16x16x32_bf16 v[44:47], a[248:251], a[184:187], v[44:47]\n\t"
"s_waitcnt lgkmcnt(0)\n\t"
"v_cvt_pk_bf16_f32 v238, v224, v225\n\t"
"v_cvt_pk_bf16_f32 v239, v226, v227\n\t"
"v_mfma_f32_16x16x32_bf16 v[44:47], v[96:99], a[188:191], v[44:47]\n\t"
"ds_write_b128 v243, v[236:239] offset:37888\n\t"
"v_mfma_f32_16x16x32_bf16 v[228:231], v[228:231], a[208:211], 0\n\t"
"v_mfma_f32_16x16x32_bf16 v[232:235], v[232:235], a[208:211], 0\n\t"
"v_mfma_f32_16x16x32_bf16 v[48:51], a[244:247], a[184:187], v[48:51]\n\t"
"buffer_load_dwordx4 a[208:211], v252, s[8:11], s20 offen offset:1024 sc1\n\t"
"v_mfma_f32_16x16x32_bf16 v[48:51], a[248:251], a[188:191], v[48:51]\n\t"
"v_mfma_f32_16x16x32_bf16 v[48:51], v[96:99], a[192:195], v[48:51]\n\t"
"v_mfma_f32_16x16x32_bf16 v[52:55], a[244:247], a[188:191], v[52:55]\n\t"
"v_cvt_pk_bf16_f32 v228, v228, v229\n\t"
"v_cvt_pk_bf16_f32 v229, v230, v231\n\t"
"v_mfma_f32_16x16x32_bf16 v[52:55], a[248:251], a[192:195], v[52:55]\n\t"
"v_cvt_pk_bf16_f32 v230, v232, v233\n\t"
"v_cvt_pk_bf16_f32 v231, v234, v235\n\t"
"v_mfma_f32_16x16x32_bf16 v[52:55], v[96:99], a[196:199], v[52:55]\n\t"
"ds_write_b128 v243, v[228:231] offset:47104\n\t"
"v_mfma_f32_16x16x32_bf16 v[56:59], a[244:247], a[192:195], v[56:59]\n\t"
"v_mfma_f32_16x16x32_bf16 v[56:59], a[248:251], a[196:199], v[56:59]\n\t"
"ds_read_b64_tr_b16 v[224:225], v246 offset:1536\n\t"
"v_mfma_f32_16x16x32_bf16 v[56:59], v[96:99], a[200:203], v[56:59]\n\t"
"ds_read_b64_tr_b16 v[226:227], v246 offset:4608\n\t"
"v_mfma_f32_16x16x32_bf16 v[60:63], a[244:247], a[196:199], v[60:63]\n\t"
"buffer_load_dwordx4 a[244:247], v252, s[8:11], s20 offen offset:2048 sc1\n\t"
"v_mfma_f32_16x16x32_bf16 v[60:63], a[248:251], a[200:203], v[60:63]\n\t"
"s_waitcnt vmcnt(9)\n\t"
"ds_read_b64_tr_b16 v[236:237], v246 offset:7680\n\t"
"s_mov_b32 s20, 1511424\n\t"
"v_mfma_f32_16x16x32_bf16 v[60:63], v[96:99], a[204:207], v[60:63]\n\t"
"ds_read_b64_tr_b16 v[238:239], v246 offset:10752\n\t"
"v_mfma_f32_16x16x32_bf16 v[64:67], a[252:255], a[168:171], v[64:67]\n\t"
"buffer_load_dwordx4 v[96:99], v252, s[8:11], s20 offen offset:0 sc1\n\t"
"v_mfma_f32_16x16x32_bf16 v[64:67], a[220:223], a[172:175], v[64:67]\n\t"
"ds_read_b64_tr_b16 v[232:233], v246 offset:13824\n\t"
"v_mfma_f32_16x16x32_bf16 v[64:67], v[248:251], a[176:179], v[64:67]\n\t"
"ds_read_b64_tr_b16 v[234:235], v246 offset:16896\n\t"
"v_mfma_f32_16x16x32_bf16 v[68:71], a[252:255], a[172:175], v[68:71]\n\t"
"buffer_load_dwordx4 a[248:251], v252, s[8:11], s20 offen offset:1024 sc1\n\t"
"v_mfma_f32_16x16x32_bf16 v[68:71], a[220:223], a[176:179], v[68:71]\n\t"
"ds_read_b128 a[168:171], v244 offset:33792\n\t"
"v_mfma_f32_16x16x32_bf16 v[68:71], v[248:251], a[180:183], v[68:71]\n\t"
"ds_read_b64_tr_b16 v[228:229], v246 offset:21504\n\t"
"v_mfma_f32_16x16x32_bf16 v[72:75], a[252:255], a[176:179], v[72:75]\n\t"
"buffer_load_dwordx4 v[240:243], v252, s[8:11], s20 offen offset:2048 sc1\n\t"
"v_mfma_f32_16x16x32_bf16 v[72:75], a[220:223], a[180:183], v[72:75]\n\t"
"s_waitcnt lgkmcnt(6)\n\t"
"ds_read_b64_tr_b16 v[230:231], v246 offset:24576\n\t"
"v_mfma_f32_16x16x32_bf16 v[72:75], v[248:251], a[184:187], v[72:75]\n\t"
"ds_read_b128 a[172:175], v244 offset:34304\n\t"
"v_mfma_f32_16x16x32_bf16 v[224:227], v[224:227], v[220:223], 0\n\t"
"ds_read_b128 a[176:179], v244 offset:34816\n\t"
"v_mfma_f32_16x16x32_bf16 v[76:79], a[252:255], a[180:183], v[76:79]\n\t"
"s_waitcnt lgkmcnt(7)\n\t"
"ds_read_b128 a[180:183], v244 offset:35328\n\t"
"v_mfma_f32_16x16x32_bf16 v[76:79], a[220:223], a[184:187], v[76:79]\n\t"
"v_mfma_f32_16x16x32_bf16 v[76:79], v[248:251], a[188:191], v[76:79]\n\t"
"v_mfma_f32_16x16x32_bf16 v[236:239], v[236:239], v[220:223], 0\n\t"
"v_mfma_f32_16x16x32_bf16 v[80:83], a[252:255], a[184:187], v[80:83]\n\t"
"s_waitcnt lgkmcnt(6)\n\t"
"ds_read_b128 a[184:187], v244 offset:35840\n\t"
"v_cvt_pk_bf16_f32 v224, v224, v225\n\t"
"v_mfma_f32_16x16x32_bf16 v[80:83], a[220:223], a[188:191], v[80:83]\n\t"
"v_cvt_pk_bf16_f32 v225, v226, v227\n\t"
"v_mfma_f32_16x16x32_bf16 v[80:83], v[248:251], a[192:195], v[80:83]\n\t"
"s_mov_b32 s20, 1523712\n\t"
"v_mfma_f32_16x16x32_bf16 v[232:235], v[232:235], v[220:223], 0\n\t"
"v_mfma_f32_16x16x32_bf16 v[84:87], a[252:255], a[188:191], v[84:87]\n\t"
"s_waitcnt lgkmcnt(4)\n\t"
"ds_read_b128 a[188:191], v244 offset:36352\n\t"
"v_cvt_pk_bf16_f32 v226, v236, v237\n\t"
"v_mfma_f32_16x16x32_bf16 v[84:87], a[220:223], a[192:195], v[84:87]\n\t"
"v_cvt_pk_bf16_f32 v227, v238, v239\n\t"
"v_mfma_f32_16x16x32_bf16 v[84:87], v[248:251], a[196:199], v[84:87]\n\t"
"v_mfma_f32_16x16x32_bf16 v[228:231], v[228:231], v[220:223], 0\n\t"
"v_mfma_f32_16x16x32_bf16 v[88:91], a[252:255], a[192:195], v[88:91]\n\t"
"buffer_load_dwordx4 v[236:239], v252, s[8:11], s20 offen offset:0 sc1\n\t"
"v_mfma_f32_16x16x32_bf16 v[88:91], a[220:223], a[196:199], v[88:91]\n\t"
"ds_read_b128 a[192:195], v244 offset:36864\n\t"
"v_cvt_pk_bf16_f32 v232, v232, v233\n\t"
"v_mfma_f32_16x16x32_bf16 v[88:91], v[248:251], a[200:203], v[88:91]\n\t"
"v_cvt_pk_bf16_f32 v233, v234, v235\n\t"
"v_mfma_f32_16x16x32_bf16 v[92:95], a[252:255], a[196:199], v[92:95]\n\t"
"buffer_load_dwordx4 a[252:255], v252, s[8:11], s20 offen offset:1024 sc1\n\t"
"v_mfma_f32_16x16x32_bf16 v[92:95], a[220:223], a[200:203], v[92:95]\n\t"
"s_waitcnt vmcnt(11) lgkmcnt(4)\n\t"
"ds_read_b128 a[196:199], v244 offset:37376\n\t"
"v_cvt_pk_bf16_f32 v234, v228, v229\n\t"
"v_mfma_f32_16x16x32_bf16 v[92:95], v[248:251], a[204:207], v[92:95]\n\t"
"ds_read_b128 a[200:203], v244 offset:37888\n\t"
"v_cvt_pk_bf16_f32 v235, v230, v231\n\t"
"v_mfma_f32_16x16x32_bf16 v[100:103], a[224:227], a[168:171], v[100:103]\n\t"
"ds_read_b64_tr_b16 v[228:229], v246 offset:27648\n\t"
"v_mfma_f32_16x16x32_bf16 v[100:103], a[228:231], a[172:175], v[100:103]\n\t"
"s_waitcnt lgkmcnt(6)\n\t"
"ds_read_b64_tr_b16 v[230:231], v246 offset:30720\n\t"
"v_mfma_f32_16x16x32_bf16 v[100:103], a[212:215], a[176:179], v[100:103]\n\t"
"s_waitcnt lgkmcnt(0)\n\t"
"ds_read_b128 a[204:207], v244 offset:38400\n\t"
"v_mfma_f32_16x16x32_bf16 v[228:231], v[228:231], v[220:223], 0\n\t"
"buffer_load_dwordx4 v[248:251], v252, s[8:11], s20 offen offset:2048 sc1\n\t"
"s_mov_b32 s20, 1536000\n\t"
"v_mfma_f32_16x16x32_bf16 v[104:107], a[224:227], a[172:175], v[104:107]\n\t"
"buffer_load_dwordx4 a[220:223], v252, s[8:11], s20 offen offset:0 sc1\n\t"
"v_mfma_f32_16x16x32_bf16 v[104:107], a[228:231], a[176:179], v[104:107]\n\t"
"v_mfma_f32_16x16x32_bf16 v[104:107], a[212:215], a[180:183], v[104:107]\n\t"
"s_waitcnt lgkmcnt(0)\n\t"
"s_barrier\n\t"
"v_mfma_f32_16x16x32_bf16 v[108:111], a[224:227], a[176:179], v[108:111]\n\t"
"v_mfma_f32_16x16x32_bf16 v[108:111], a[228:231], a[180:183], v[108:111]\n\t"
"v_cvt_pk_bf16_f32 v228, v228, v229\n\t"
"v_cvt_pk_bf16_f32 v229, v230, v231\n\t"
"v_mfma_f32_16x16x32_bf16 v[108:111], a[212:215], a[184:187], v[108:111]\n\t"
"ds_write2st64_b64 v245, v[224:225], v[226:227] offset0:48 offset1:49\n\t"
"v_mfma_f32_16x16x32_bf16 v[112:115], a[224:227], a[180:183], v[112:115]\n\t"
"v_mfma_f32_16x16x32_bf16 v[112:115], a[228:231], a[184:187], v[112:115]\n\t"
"ds_read_b64_tr_b16 v[224:225], v246 offset:33792\n\t"
"v_mfma_f32_16x16x32_bf16 v[112:115], a[212:215], a[188:191], v[112:115]\n\t"
"ds_write2st64_b64 v245, v[232:233], v[234:235] offset0:50 offset1:51\n\t"
"v_mfma_f32_16x16x32_bf16 v[116:119], a[224:227], a[184:187], v[116:119]\n\t"
"v_mfma_f32_16x16x32_bf16 v[116:119], a[228:231], a[188:191], v[116:119]\n\t"
"ds_read_b64_tr_b16 v[226:227], v246 offset:36864\n\t"
"v_mfma_f32_16x16x32_bf16 v[116:119], a[212:215], a[192:195], v[116:119]\n\t"
"ds_write_b64 v245, v[228:229] offset:26624\n\t"
"v_mfma_f32_16x16x32_bf16 v[120:123], a[224:227], a[188:191], v[120:123]\n\t"
"ds_read_b64_tr_b16 v[232:233], v246 offset:41472\n\t"
"v_mfma_f32_16x16x32_bf16 v[120:123], a[228:231], a[192:195], v[120:123]\n\t"
"ds_read_b64_tr_b16 v[234:235], v246 offset:44544\n\t"
"v_mfma_f32_16x16x32_bf16 v[120:123], a[212:215], a[196:199], v[120:123]\n\t"
"s_waitcnt lgkmcnt(3)\n\t"
"ds_read_b64_tr_b16 v[228:229], v246 offset:47616\n\t"
"v_mfma_f32_16x16x32_bf16 v[224:227], v[224:227], v[220:223], 0\n\t"
"ds_read_b64_tr_b16 v[230:231], v246 offset:50688\n\t"
"v_mfma_f32_16x16x32_bf16 v[124:127], a[224:227], a[192:195], v[124:127]\n\t"
"v_mfma_f32_16x16x32_bf16 v[124:127], a[228:231], a[196:199], v[124:127]\n\t"
"v_mfma_f32_16x16x32_bf16 v[124:127], a[212:215], a[200:203], v[124:127]\n\t"
"s_waitcnt lgkmcnt(2)\n\t"
"v_mfma_f32_16x16x32_bf16 v[232:235], v[232:235], v[220:223], 0\n\t"
"v_mfma_f32_16x16x32_bf16 v[128:131], a[224:227], a[196:199], v[128:131]\n\t"
"buffer_load_dwordx4 a[224:227], v252, s[8:11], s20 offen offset:1024 sc1\n\t"
"v_mfma_f32_16x16x32_bf16 v[128:131], a[228:231], a[200:203], v[128:131]\n\t"
"s_waitcnt vmcnt(11) lgkmcnt(0)\n\t"
"v_cvt_pk_bf16_f32 v224, v224, v225\n\t"
"v_cvt_pk_bf16_f32 v225, v226, v227\n\t"
"v_mfma_f32_16x16x32_bf16 v[128:131], a[212:215], a[204:207], v[128:131]\n\t"
"v_mfma_f32_16x16x32_bf16 v[228:231], v[228:231], v[220:223], 0\n\t"
"buffer_load_dwordx4 a[212:215], v252, s[8:11], s20 offen offset:2048 sc1\n\t"
"v_mfma_f32_16x16x32_bf16 v[132:135], a[232:235], a[168:171], v[132:135]\n\t"
"s_mov_b32 s20, 1548288\n\t"
"v_cvt_pk_bf16_f32 v226, v232, v233\n\t"
"v_cvt_pk_bf16_f32 v227, v234, v235\n\t"
"v_mfma_f32_16x16x32_bf16 v[132:135], a[236:239], a[172:175], v[132:135]\n\t"
"ds_read_b64_tr_b16 v[232:233], v246 offset:53760\n\t"
"v_mfma_f32_16x16x32_bf16 v[132:135], a[216:219], a[176:179], v[132:135]\n\t"
"buffer_load_dwordx4 a[228:231], v252, s[8:11], s20 offen offset:0 sc1\n\t"
"v_mfma_f32_16x16x32_bf16 v[136:139], a[232:235], a[172:175], v[136:139]\n\t"
"ds_read_b64_tr_b16 v[234:235], v246 offset:56832\n\t"
"v_mfma_f32_16x16x32_bf16 v[136:139], a[236:239], a[176:179], v[136:139]\n\t"
"v_cvt_pk_bf16_f32 v228, v228, v229\n\t"
"v_cvt_pk_bf16_f32 v229, v230, v231\n\t"
"v_mfma_f32_16x16x32_bf16 v[136:139], a[216:219], a[180:183], v[136:139]\n\t"
"ds_write2st64_b64 v245, v[224:225], v[226:227] offset0:53 offset1:54\n\t"
"v_mfma_f32_16x16x32_bf16 v[140:143], a[232:235], a[176:179], v[140:143]\n\t"
"v_mfma_f32_16x16x32_bf16 v[140:143], a[236:239], a[180:183], v[140:143]\n\t"
"v_mfma_f32_16x16x32_bf16 v[140:143], a[216:219], a[184:187], v[140:143]\n\t"
"v_mfma_f32_16x16x32_bf16 v[144:147], a[232:235], a[180:183], v[144:147]\n\t"
"v_mfma_f32_16x16x32_bf16 v[144:147], a[236:239], a[184:187], v[144:147]\n\t"
"v_mfma_f32_16x16x32_bf16 v[144:147], a[216:219], a[188:191], v[144:147]\n\t"
"v_mfma_f32_16x16x32_bf16 v[148:151], a[232:235], a[184:187], v[148:151]\n\t"
"s_waitcnt lgkmcnt(1)\n\t"
"v_mfma_f32_16x16x32_bf16 v[148:151], a[236:239], a[188:191], v[148:151]\n\t"
"v_mfma_f32_16x16x32_bf16 v[148:151], a[216:219], a[192:195], v[148:151]\n\t"
"v_mfma_f32_16x16x32_bf16 v[232:235], v[232:235], v[220:223], 0\n\t"
"v_mfma_f32_16x16x32_bf16 v[152:155], a[232:235], a[188:191], v[152:155]\n\t"
"v_mfma_f32_16x16x32_bf16 v[152:155], a[236:239], a[192:195], v[152:155]\n\t"
"v_mfma_f32_16x16x32_bf16 v[152:155], a[216:219], a[196:199], v[152:155]\n\t"
"v_mfma_f32_16x16x32_bf16 v[156:159], a[232:235], a[192:195], v[156:159]\n\t"
"v_mfma_f32_16x16x32_bf16 v[156:159], a[236:239], a[196:199], v[156:159]\n\t"
"v_cvt_pk_bf16_f32 v230, v232, v233\n\t"
"v_cvt_pk_bf16_f32 v231, v234, v235\n\t"
"v_mfma_f32_16x16x32_bf16 v[156:159], a[216:219], a[200:203], v[156:159]\n\t"
"ds_write2st64_b64 v245, v[228:229], v[230:231] offset0:55 offset1:56\n\t"
"v_mfma_f32_16x16x32_bf16 v[160:163], a[232:235], a[196:199], v[160:163]\n\t"
"buffer_load_dwordx4 a[232:235], v252, s[8:11], s20 offen offset:1024 sc1\n\t"
"v_mfma_f32_16x16x32_bf16 v[160:163], a[236:239], a[200:203], v[160:163]\n\t"
"s_waitcnt vmcnt(11)\n\t"
"v_mfma_f32_16x16x32_bf16 v[160:163], a[216:219], a[204:207], v[160:163]\n\t"
"v_mfma_f32_16x16x32_bf16 v[164:167], a[240:243], a[168:171], v[164:167]\n\t"
"buffer_load_dwordx4 a[216:219], v252, s[8:11], s20 offen offset:2048 sc1\n\t"
"v_mfma_f32_16x16x32_bf16 v[164:167], a[208:211], a[172:175], v[164:167]\n\t"
"s_mov_b32 s20, 1560576\n\t"
"v_mfma_f32_16x16x32_bf16 v[164:167], a[244:247], a[176:179], v[164:167]\n\t"
"s_waitcnt lgkmcnt(0)\n\t"
"s_barrier\n\t"
"v_mfma_f32_16x16x32_bf16 v[168:171], a[240:243], a[172:175], v[168:171]\n\t"
"ds_read_b128 a[168:171], v244 offset:24576\n\t"
"v_mfma_f32_16x16x32_bf16 v[168:171], a[208:211], a[176:179], v[168:171]\n\t"
"buffer_load_dwordx4 a[236:239], v252, s[8:11], s20 offen offset:0 sc1\n\t"
"v_mfma_f32_16x16x32_bf16 v[168:171], a[244:247], a[180:183], v[168:171]\n\t"
"ds_read_b128 a[172:175], v244 offset:25088\n\t"
"v_mfma_f32_16x16x32_bf16 v[172:175], a[240:243], a[176:179], v[172:175]\n\t"
"ds_read_b128 a[176:179], v244 offset:25600\n\t"
"v_mfma_f32_16x16x32_bf16 v[172:175], a[208:211], a[180:183], v[172:175]\n\t"
"v_mfma_f32_16x16x32_bf16 v[172:175], a[244:247], a[184:187], v[172:175]\n\t"
"v_mfma_f32_16x16x32_bf16 v[176:179], a[240:243], a[180:183], v[176:179]\n\t"
"ds_read_b128 a[180:183], v244 offset:26112\n\t"
"v_mfma_f32_16x16x32_bf16 v[176:179], a[208:211], a[184:187], v[176:179]\n\t"
"v_mfma_f32_16x16x32_bf16 v[176:179], a[244:247], a[188:191], v[176:179]\n\t"
"v_mfma_f32_16x16x32_bf16 v[180:183], a[240:243], a[184:187], v[180:183]\n\t"
"ds_read_b128 a[184:187], v244 offset:26624\n\t"
"v_mfma_f32_16x16x32_bf16 v[180:183], a[208:211], a[188:191], v[180:183]\n\t"
"v_mfma_f32_16x16x32_bf16 v[180:183], a[244:247], a[192:195], v[180:183]\n\t"
"v_mfma_f32_16x16x32_bf16 v[184:187], a[240:243], a[188:191], v[184:187]\n\t"
"ds_read_b128 a[188:191], v244 offset:27136\n\t"
"v_mfma_f32_16x16x32_bf16 v[184:187], a[208:211], a[192:195], v[184:187]\n\t"
"v_mfma_f32_16x16x32_bf16 v[184:187], a[244:247], a[196:199], v[184:187]\n\t"
"v_mfma_f32_16x16x32_bf16 v[188:191], a[240:243], a[192:195], v[188:191]\n\t"
"ds_read_b128 a[192:195], v244 offset:27648\n\t"
"v_mfma_f32_16x16x32_bf16 v[188:191], a[208:211], a[196:199], v[188:191]\n\t"
"v_mfma_f32_16x16x32_bf16 v[188:191], a[244:247], a[200:203], v[188:191]\n\t"
"v_mfma_f32_16x16x32_bf16 v[192:195], a[240:243], a[196:199], v[192:195]\n\t"
"buffer_load_dwordx4 a[240:243], v252, s[8:11], s20 offen offset:1024 sc1\n\t"
"v_mfma_f32_16x16x32_bf16 v[192:195], a[208:211], a[200:203], v[192:195]\n\t"
"s_waitcnt vmcnt(11) lgkmcnt(4)\n\t"
"ds_read_b128 a[196:199], v244 offset:28160\n\t"
"v_mfma_f32_16x16x32_bf16 v[192:195], a[244:247], a[204:207], v[192:195]\n\t"
"ds_read_b128 a[200:203], v244 offset:28672\n\t"
"Lst30_%=:\n\t"
"v_mfma_f32_16x16x32_bf16 v[0:3], v[96:99], a[168:171], v[0:3]\n\t"
"buffer_load_dwordx4 a[208:211], v252, s[8:11], s20 offen offset:2048 sc1\n\t"
"v_mfma_f32_16x16x32_bf16 v[0:3], a[248:251], a[172:175], v[0:3]\n\t"
"s_waitcnt lgkmcnt(5)\n\t"
"ds_read_b128 a[204:207], v244 offset:29184\n\t"
"v_xor_b32 v246, 0xc0, v246\n\t"
"v_mfma_f32_16x16x32_bf16 v[0:3], v[240:243], a[176:179], v[0:3]\n\t"
"s_mov_b32 s20, 1572864\n\t"
"v_mfma_f32_16x16x32_bf16 v[4:7], v[96:99], a[172:175], v[4:7]\n\t"
"buffer_load_dwordx4 a[244:247], v252, s[8:11], s20 offen offset:0 sc1\n\t"
"v_mfma_f32_16x16x32_bf16 v[4:7], a[248:251], a[176:179], v[4:7]\n\t"
"s_waitcnt lgkmcnt(5)\n\t"
"v_mfma_f32_16x16x32_bf16 v[4:7], v[240:243], a[180:183], v[4:7]\n\t"
"v_mfma_f32_16x16x32_bf16 v[8:11], v[96:99], a[176:179], v[8:11]\n\t"
"s_waitcnt lgkmcnt(4)\n\t"
"v_mfma_f32_16x16x32_bf16 v[8:11], a[248:251], a[180:183], v[8:11]\n\t"
"v_mfma_f32_16x16x32_bf16 v[8:11], v[240:243], a[184:187], v[8:11]\n\t"
"v_mfma_f32_16x16x32_bf16 v[12:15], v[96:99], a[180:183], v[12:15]\n\t"
"s_waitcnt lgkmcnt(3)\n\t"
"v_mfma_f32_16x16x32_bf16 v[12:15], a[248:251], a[184:187], v[12:15]\n\t"
"v_mfma_f32_16x16x32_bf16 v[12:15], v[240:243], a[188:191], v[12:15]\n\t"
"v_mfma_f32_16x16x32_bf16 v[16:19], v[96:99], a[184:187], v[16:19]\n\t"
"s_waitcnt lgkmcnt(2)\n\t"
"v_mfma_f32_16x16x32_bf16 v[16:19], a[248:251], a[188:191], v[16:19]\n\t"
"v_mfma_f32_16x16x32_bf16 v[16:19], v[240:243], a[192:195], v[16:19]\n\t"
"ds_read_b64_tr_b16 v[224:225], v246 offset:1536\n\t"
"v_mfma_f32_16x16x32_bf16 v[20:23], v[96:99], a[188:191], v[20:23]\n\t"
"s_waitcnt lgkmcnt(2)\n\t"
"ds_read_b64_tr_b16 v[226:227], v246 offset:4608\n\t"
"v_mfma_f32_16x16x32_bf16 v[20:23], a[248:251], a[192:195], v[20:23]\n\t"
"v_mfma_f32_16x16x32_bf16 v[20:23], v[240:243], a[196:199], v[20:23]\n\t"
"v_mfma_f32_16x16x32_bf16 v[24:27], v[96:99], a[192:195], v[24:27]\n\t"
"s_waitcnt lgkmcnt(2)\n\t"
"v_mfma_f32_16x16x32_bf16 v[24:27], a[248:251], a[196:199], v[24:27]\n\t"
"v_mfma_f32_16x16x32_bf16 v[24:27], v[240:243], a[200:203], v[24:27]\n\t"
"v_mfma_f32_16x16x32_bf16 v[28:31], v[96:99], a[196:199], v[28:31]\n\t"
"buffer_load_dwordx4 v[96:99], v252, s[8:11], s20 offen offset:1024 sc1\n\t"
"v_mfma_f32_16x16x32_bf16 v[28:31], a[248:251], a[200:203], v[28:31]\n\t"
"s_waitcnt vmcnt(11)\n\t"
"ds_read_b64_tr_b16 v[232:233], v246 offset:7680\n\t"
"v_mfma_f32_16x16x32_bf16 v[28:31], v[240:243], a[204:207], v[28:31]\n\t"
"ds_read_b64_tr_b16 v[234:235], v246 offset:10752\n\t"
"v_mfma_f32_16x16x32_bf16 v[32:35], v[236:239], a[168:171], v[32:35]\n\t"
"buffer_load_dwordx4 a[248:251], v252, s[8:11], s20 offen offset:2048 sc1\n\t"
"v_mfma_f32_16x16x32_bf16 v[32:35], a[252:255], a[172:175], v[32:35]\n\t"
"s_mov_b32 s20, 1585152\n\t"
"v_mfma_f32_16x16x32_bf16 v[32:35], v[248:251], a[176:179], v[32:35]\n\t"
"v_mfma_f32_16x16x32_bf16 v[36:39], v[236:239], a[172:175], v[36:39]\n\t"
"buffer_load_dwordx4 v[240:243], v252, s[8:11], s20 offen offset:0 sc1\n\t"
"v_mfma_f32_16x16x32_bf16 v[36:39], a[252:255], a[176:179], v[36:39]\n\t"
"s_waitcnt lgkmcnt(2)\n\t"
"v_mfma_f32_16x16x32_bf16 v[36:39], v[248:251], a[180:183], v[36:39]\n\t"
"v_mfma_f32_16x16x32_bf16 v[224:227], v[224:227], v[220:223], 0\n\t"
"v_mfma_f32_16x16x32_bf16 v[40:43], v[236:239], a[176:179], v[40:43]\n\t"
"ds_read_b64_tr_b16 v[228:229], v246 offset:13824\n\t"
"v_mfma_f32_16x16x32_bf16 v[40:43], a[252:255], a[180:183], v[40:43]\n\t"
"ds_read_b64_tr_b16 v[230:231], v246 offset:16896\n\t"
"v_mfma_f32_16x16x32_bf16 v[40:43], v[248:251], a[184:187], v[40:43]\n\t"
"v_mfma_f32_16x16x32_bf16 v[44:47], v[236:239], a[180:183], v[44:47]\n\t"
"s_waitcnt lgkmcnt(2)\n\t"
"v_mfma_f32_16x16x32_bf16 v[44:47], a[252:255], a[184:187], v[44:47]\n\t"
"v_cvt_pk_bf16_f32 v224, v224, v225\n\t"
"v_cvt_pk_bf16_f32 v225, v226, v227\n\t"
"v_mfma_f32_16x16x32_bf16 v[44:47], v[248:251], a[188:191], v[44:47]\n\t"
"v_mfma_f32_16x16x32_bf16 v[232:235], v[232:235], v[220:223], 0\n\t"
"v_mfma_f32_16x16x32_bf16 v[48:51], v[236:239], a[184:187], v[48:51]\n\t"
"v_mfma_f32_16x16x32_bf16 v[48:51], a[252:255], a[188:191], v[48:51]\n\t"
"v_mfma_f32_16x16x32_bf16 v[48:51], v[248:251], a[192:195], v[48:51]\n\t"
"v_mfma_f32_16x16x32_bf16 v[52:55], v[236:239], a[188:191], v[52:55]\n\t"
"v_mfma_f32_16x16x32_bf16 v[52:55], a[252:255], a[192:195], v[52:55]\n\t"
"v_cvt_pk_bf16_f32 v226, v232, v233\n\t"
"v_cvt_pk_bf16_f32 v227, v234, v235\n\t"
"v_mfma_f32_16x16x32_bf16 v[52:55], v[248:251], a[196:199], v[52:55]\n\t"
"ds_read_b64_tr_b16 v[232:233], v246 offset:21504\n\t"
"v_mfma_f32_16x16x32_bf16 v[56:59], v[236:239], a[192:195], v[56:59]\n\t"
"s_waitcnt lgkmcnt(1)\n\t"
"ds_read_b64_tr_b16 v[234:235], v246 offset:24576\n\t"
"v_mfma_f32_16x16x32_bf16 v[56:59], a[252:255], a[196:199], v[56:59]\n\t"
"v_mfma_f32_16x16x32_bf16 v[56:59], v[248:251], a[200:203], v[56:59]\n\t"
"v_mfma_f32_16x16x32_bf16 v[228:231], v[228:231], v[220:223], 0\n\t"
"v_mfma_f32_16x16x32_bf16 v[60:63], v[236:239], a[196:199], v[60:63]\n\t"
"buffer_load_dwordx4 v[236:239], v252, s[8:11], s20 offen offset:1024 sc1\n\t"
"v_mfma_f32_16x16x32_bf16 v[60:63], a[252:255], a[200:203], v[60:63]\n\t"
"s_waitcnt vmcnt(11)\n\t"
"v_mfma_f32_16x16x32_bf16 v[60:63], v[248:251], a[204:207], v[60:63]\n\t"
"v_mfma_f32_16x16x32_bf16 v[64:67], a[220:223], a[168:171], v[64:67]\n\t"
"buffer_load_dwordx4 v[248:251], v252, s[8:11], s20 offen offset:2048 sc1\n\t"
"v_mfma_f32_16x16x32_bf16 v[64:67], a[224:227], a[172:175], v[64:67]\n\t"
"s_mov_b32 s20, 1597440\n\t"
"ds_read_b128 a[168:171], v244 offset:33792\n\t"
"v_cvt_pk_bf16_f32 v228, v228, v229\n\t"
"v_mfma_f32_16x16x32_bf16 v[64:67], a[212:215], a[176:179], v[64:67]\n\t"
"v_cvt_pk_bf16_f32 v229, v230, v231\n\t"
"v_mfma_f32_16x16x32_bf16 v[68:71], a[220:223], a[172:175], v[68:71]\n\t"
"buffer_load_dwordx4 a[252:255], v252, s[8:11], s20 offen offset:0 sc1\n\t"
"v_mfma_f32_16x16x32_bf16 v[68:71], a[224:227], a[176:179], v[68:71]\n\t"
"s_waitcnt lgkmcnt(1)\n\t"
"ds_read_b128 a[172:175], v244 offset:34304\n\t"
"v_mfma_f32_16x16x32_bf16 v[68:71], a[212:215], a[180:183], v[68:71]\n\t"
"v_mfma_f32_16x16x32_bf16 v[232:235], v[232:235], v[220:223], 0\n\t"
"v_mfma_f32_16x16x32_bf16 v[72:75], a[220:223], a[176:179], v[72:75]\n\t"
"ds_read_b128 a[176:179], v244 offset:34816\n\t"
"v_mfma_f32_16x16x32_bf16 v[72:75], a[224:227], a[180:183], v[72:75]\n\t"
"v_mfma_f32_16x16x32_bf16 v[72:75], a[212:215], a[184:187], v[72:75]\n\t"
"v_mfma_f32_16x16x32_bf16 v[76:79], a[220:223], a[180:183], v[76:79]\n\t"
"ds_read_b128 a[180:183], v244 offset:35328\n\t"
"v_mfma_f32_16x16x32_bf16 v[76:79], a[224:227], a[184:187], v[76:79]\n\t"
"v_cvt_pk_bf16_f32 v230, v232, v233\n\t"
"v_cvt_pk_bf16_f32 v231, v234, v235\n\t"
"v_mfma_f32_16x16x32_bf16 v[76:79], a[212:215], a[188:191], v[76:79]\n\t"
"ds_read_b64_tr_b16 v[232:233], v246 offset:27648\n\t"
"v_mfma_f32_16x16x32_bf16 v[80:83], a[220:223], a[184:187], v[80:83]\n\t"
"ds_read_b64_tr_b16 v[234:235], v246 offset:30720\n\t"
"v_mfma_f32_16x16x32_bf16 v[80:83], a[224:227], a[188:191], v[80:83]\n\t"
"ds_read_b128 a[184:187], v244 offset:35840\n\t"
"v_mfma_f32_16x16x32_bf16 v[80:83], a[212:215], a[192:195], v[80:83]\n\t"
"v_mfma_f32_16x16x32_bf16 v[84:87], a[220:223], a[188:191], v[84:87]\n\t"
"ds_read_b128 a[188:191], v244 offset:36352\n\t"
"v_mfma_f32_16x16x32_bf16 v[84:87], a[224:227], a[192:195], v[84:87]\n\t"
"v_mfma_f32_16x16x32_bf16 v[84:87], a[212:215], a[196:199], v[84:87]\n\t"
"v_mfma_f32_16x16x32_bf16 v[88:91], a[220:223], a[192:195], v[88:91]\n\t"
"ds_read_b128 a[192:195], v244 offset:36864\n\t"
"v_mfma_f32_16x16x32_bf16 v[88:91], a[224:227], a[196:199], v[88:91]\n\t"
"v_mfma_f32_16x16x32_bf16 v[88:91], a[212:215], a[200:203], v[88:91]\n\t"
"v_mfma_f32_16x16x32_bf16 v[92:95], a[220:223], a[196:199], v[92:95]\n\t"
"buffer_load_dwordx4 a[220:223], v252, s[8:11], s20 offen offset:1024 sc1\n\t"
"v_mfma_f32_16x16x32_bf16 v[92:95], a[224:227], a[200:203], v[92:95]\n\t"
"s_waitcnt vmcnt(11) lgkmcnt(3)\n\t"
"ds_read_b128 a[196:199], v244 offset:37376\n\t"
"v_mfma_f32_16x16x32_bf16 v[92:95], a[212:215], a[204:207], v[92:95]\n\t"
"ds_read_b128 a[200:203], v244 offset:37888\n\t"
"v_mfma_f32_16x16x32_bf16 v[232:235], v[232:235], v[220:223], 0\n\t"
"ds_read_b128 a[204:207], v244 offset:38400\n\t"
"v_mfma_f32_16x16x32_bf16 v[100:103], a[228:231], a[168:171], v[100:103]\n\t"
"buffer_load_dwordx4 a[212:215], v252, s[8:11], s20 offen offset:2048 sc1\n\t"
"v_mfma_f32_16x16x32_bf16 v[100:103], a[232:235], a[172:175], v[100:103]\n\t"
"s_mov_b32 s20, 1609728\n\t"
"v_mfma_f32_16x16x32_bf16 v[100:103], a[216:219], a[176:179], v[100:103]\n\t"
"buffer_load_dwordx4 a[224:227], v252, s[8:11], s20 offen offset:0 sc1\n\t"
"v_mfma_f32_16x16x32_bf16 v[104:107], a[228:231], a[172:175], v[104:107]\n\t"
"s_waitcnt lgkmcnt(5)\n\t"
"v_mfma_f32_16x16x32_bf16 v[104:107], a[232:235], a[176:179], v[104:107]\n\t"
"v_cvt_pk_bf16_f32 v232, v232, v233\n\t"
"v_mfma_f32_16x16x32_bf16 v[104:107], a[216:219], a[180:183], v[104:107]\n\t"
"s_waitcnt lgkmcnt(0)\n\t"
"s_barrier\n\t"
"v_mfma_f32_16x16x32_bf16 v[108:111], a[228:231], a[176:179], v[108:111]\n\t"
"v_cvt_pk_bf16_f32 v233, v234, v235\n\t"
"v_mfma_f32_16x16x32_bf16 v[108:111], a[232:235], a[180:183], v[108:111]\n\t"
"v_fma_f32 v0, v100, s52, v0\n\t"
"v_fma_f32 v1, v101, s52, v1\n\t"
"v_mfma_f32_16x16x32_bf16 v[108:111], a[216:219], a[184:187], v[108:111]\n\t"
"ds_write2st64_b64 v245, v[224:225], v[226:227] offset0:48 offset1:49\n\t"
"v_mfma_f32_16x16x32_bf16 v[112:115], a[228:231], a[180:183], v[112:115]\n\t"
"v_fma_f32 v2, v102, s52, v2\n\t"
"v_fma_f32 v3, v103, s52, v3\n\t"
"v_mfma_f32_16x16x32_bf16 v[112:115], a[232:235], a[184:187], v[112:115]\n\t"
"ds_read_b64_tr_b16 v[224:225], v246 offset:33792\n\t"
"v_mfma_f32_16x16x32_bf16 v[112:115], a[216:219], a[188:191], v[112:115]\n\t"
"ds_write2st64_b64 v245, v[228:229], v[230:231] offset0:50 offset1:51\n\t"
"v_mfma_f32_16x16x32_bf16 v[116:119], a[228:231], a[184:187], v[116:119]\n\t"
"v_fma_f32 v100, v0, s54, -v100\n\t"
"v_fma_f32 v101, v1, s54, -v101\n\t"
"v_mfma_f32_16x16x32_bf16 v[116:119], a[232:235], a[188:191], v[116:119]\n\t"
"ds_read_b64_tr_b16 v[226:227], v246 offset:36864\n\t"
"v_mfma_f32_16x16x32_bf16 v[116:119], a[216:219], a[192:195], v[116:119]\n\t"
"ds_write_b64 v245, v[232:233] offset:26624\n\t"
"v_mfma_f32_16x16x32_bf16 v[120:123], a[228:231], a[188:191], v[120:123]\n\t"
"ds_read_b64_tr_b16 v[228:229], v246 offset:41472\n\t"
"v_fma_f32 v102, v2, s54, -v102\n\t"
"v_mfma_f32_16x16x32_bf16 v[120:123], a[232:235], a[192:195], v[120:123]\n\t"
"ds_read_b64_tr_b16 v[230:231], v246 offset:44544\n\t"
"v_fma_f32 v103, v3, s54, -v103\n\t"
"v_mfma_f32_16x16x32_bf16 v[120:123], a[216:219], a[196:199], v[120:123]\n\t"
"s_waitcnt vmcnt(10) lgkmcnt(3)\n\t"
"ds_read_b64_tr_b16 v[232:233], v246 offset:47616\n\t"
"v_fma_f32 v4, v104, s52, v4\n\t"
"v_mfma_f32_16x16x32_bf16 v[224:227], v[224:227], v[220:223], 0\n\t"
"Lpg22_%=:\n\t"
"s_cbranch_vccnz Lvr3_%=\n\t"
"ds_read_b64_tr_b16 v[234:235], v246 offset:50688\n\t"
"v_fma_f32 v5, v105, s52, v5\n\t"
"v_mfma_f32_16x16x32_bf16 v[124:127], a[228:231], a[192:195], v[124:127]\n\t"
"v_fma_f32 v6, v106, s52, v6\n\t"
"v_fma_f32 v7, v107, s52, v7\n\t"
"v_mfma_f32_16x16x32_bf16 v[124:127], a[232:235], a[196:199], v[124:127]\n\t"
"v_fma_f32 v104, v4, s54, -v104\n\t"
"v_fma_f32 v105, v5, s54, -v105\n\t"
"v_mfma_f32_16x16x32_bf16 v[124:127], a[216:219], a[200:203], v[124:127]\n\t"
"s_waitcnt lgkmcnt(2)\n\t"
"v_fma_f32 v106, v6, s54, -v106\n\t"
"v_fma_f32 v107, v7, s54, -v107\n\t"
"v_mfma_f32_16x16x32_bf16 v[228:231], v[228:231], v[220:223], 0\n\t"
"v_fma_f32 v8, v108, s52, v8\n\t"
"v_fma_f32 v9, v109, s52, v9\n\t"
"v_mfma_f32_16x16x32_bf16 v[128:131], a[228:231], a[196:199], v[128:131]\n\t"
"s_waitcnt lgkmcnt(0)\n\t"
"v_cvt_pk_bf16_f32 v224, v224, v225\n\t"
"v_cvt_pk_bf16_f32 v225, v226, v227\n\t"
"v_mfma_f32_16x16x32_bf16 v[128:131], a[232:235], a[200:203], v[128:131]\n\t"
"v_fma_f32 v10, v110, s52, v10\n\t"
"v_fma_f32 v11, v111, s52, v11\n\t"
"v_mfma_f32_16x16x32_bf16 v[128:131], a[216:219], a[204:207], v[128:131]\n\t"
"v_fma_f32 v108, v8, s54, -v108\n\t"
"v_fma_f32 v109, v9, s54, -v109\n\t"
"v_mfma_f32_16x16x32_bf16 v[232:235], v[232:235], v[220:223], 0\n\t"
"buffer_load_dwordx4 a[228:231], v252, s[8:11], s20 offen offset:1024 sc1\n\t"
"v_mfma_f32_16x16x32_bf16 v[132:135], a[236:239], a[168:171], v[132:135]\n\t"
"buffer_load_dwordx4 a[216:219], v252, s[8:11], s20 offen offset:2048 sc1\n\t"
"v_mfma_f32_16x16x32_bf16 v[132:135], a[240:243], a[172:175], v[132:135]\n\t"
"s_mov_b32 s20, 1622016\n\t"
"v_cvt_pk_bf16_f32 v226, v228, v229\n\t"
"v_cvt_pk_bf16_f32 v227, v230, v231\n\t"
"v_mfma_f32_16x16x32_bf16 v[132:135], a[208:211], a[176:179], v[132:135]\n\t"
"ds_write2st64_b64 v245, v[224:225], v[226:227] offset0:53 offset1:54\n\t"
"v_mfma_f32_16x16x32_bf16 v[136:139], a[236:239], a[172:175], v[136:139]\n\t"
"buffer_load_dwordx4 a[232:235], v252, s[8:11], s20 offen offset:0 sc1\n\t"
"v_mfma_f32_16x16x32_bf16 v[136:139], a[240:243], a[176:179], v[136:139]\n\t"
"ds_read_b64_tr_b16 v[228:229], v246 offset:53760\n\t"
"v_mfma_f32_16x16x32_bf16 v[136:139], a[208:211], a[180:183], v[136:139]\n\t"
"ds_read_b64_tr_b16 v[230:231], v246 offset:56832\n\t"
"v_cvt_pk_bf16_f32 v232, v232, v233\n\t"
"v_mfma_f32_16x16x32_bf16 v[140:143], a[236:239], a[176:179], v[140:143]\n\t"
"v_cvt_pk_bf16_f32 v233, v234, v235\n\t"
"v_fma_f32 v110, v10, s54, -v110\n\t"
"v_mfma_f32_16x16x32_bf16 v[140:143], a[240:243], a[180:183], v[140:143]\n\t"
"v_fma_f32 v111, v11, s54, -v111\n\t"
"v_fma_f32 v12, v112, s52, v12\n\t"
"v_mfma_f32_16x16x32_bf16 v[140:143], a[208:211], a[184:187], v[140:143]\n\t"
"v_fma_f32 v13, v113, s52, v13\n\t"
"v_fma_f32 v14, v114, s52, v14\n\t"
"v_mfma_f32_16x16x32_bf16 v[144:147], a[236:239], a[180:183], v[144:147]\n\t"
"v_fma_f32 v15, v115, s52, v15\n\t"
"v_fma_f32 v112, v12, s54, -v112\n\t"
"v_mfma_f32_16x16x32_bf16 v[144:147], a[240:243], a[184:187], v[144:147]\n\t"
"v_fma_f32 v113, v13, s54, -v113\n\t"
"v_fma_f32 v114, v14, s54, -v114\n\t"
"v_mfma_f32_16x16x32_bf16 v[144:147], a[208:211], a[188:191], v[144:147]\n\t"
"v_fma_f32 v115, v15, s54, -v115\n\t"
"v_fma_f32 v16, v116, s52, v16\n\t"
"v_mfma_f32_16x16x32_bf16 v[148:151], a[236:239], a[184:187], v[148:151]\n\t"
"s_waitcnt lgkmcnt(0)\n\t"
"v_fma_f32 v17, v117, s52, v17\n\t"
"v_fma_f32 v18, v118, s52, v18\n\t"
"v_mfma_f32_16x16x32_bf16 v[148:151], a[240:243], a[188:191], v[148:151]\n\t"
"v_fma_f32 v19, v119, s52, v19\n\t"
"v_fma_f32 v116, v16, s54, -v116\n\t"
"v_mfma_f32_16x16x32_bf16 v[148:151], a[208:211], a[192:195], v[148:151]\n\t"
"v_fma_f32 v117, v17, s54, -v117\n\t"
"v_fma_f32 v118, v18, s54, -v118\n\t"
"v_mfma_f32_16x16x32_bf16 v[228:231], v[228:231], v[220:223], 0\n\t"
"v_fma_f32 v119, v19, s54, -v119\n\t"
"v_fma_f32 v20, v120, s52, v20\n\t"
"v_mfma_f32_16x16x32_bf16 v[152:155], a[236:239], a[188:191], v[152:155]\n\t"
"v_fma_f32 v21, v121, s52, v21\n\t"
"v_fma_f32 v22, v122, s52, v22\n\t"
"v_mfma_f32_16x16x32_bf16 v[152:155], a[240:243], a[192:195], v[152:155]\n\t"
"v_fma_f32 v23, v123, s52, v23\n\t"
"v_fma_f32 v120, v20, s54, -v120\n\t"
"v_mfma_f32_16x16x32_bf16 v[152:155], a[208:211], a[196:199], v[152:155]\n\t"
"v_fma_f32 v121, v21, s54, -v121\n\t"
"v_fma_f32 v122, v22, s54, -v122\n\t"
"v_mfma_f32_16x16x32_bf16 v[156:159], a[236:239], a[192:195], v[156:159]\n\t"
"v_fma_f32 v123, v23, s54, -v123\n\t"
"v_fma_f32 v24, v124, s52, v24\n\t"
"v_mfma_f32_16x16x32_bf16 v[156:159], a[240:243], a[196:199], v[156:159]\n\t"
"v_cvt_pk_bf16_f32 v234, v228, v229\n\t"
"v_cvt_pk_bf16_f32 v235, v230, v231\n\t"
"v_mfma_f32_16x16x32_bf16 v[156:159], a[208:211], a[200:203], v[156:159]\n\t"
"ds_write2st64_b64 v245, v[232:233], v[234:235] offset0:55 offset1:56\n\t"
"v_mfma_f32_16x16x32_bf16 v[160:163], a[236:239], a[196:199], v[160:163]\n\t"
"buffer_load_dwordx4 a[236:239], v252, s[8:11], s20 offen offset:1024 sc1\n\t"
"v_mfma_f32_16x16x32_bf16 v[160:163], a[240:243], a[200:203], v[160:163]\n\t"
"s_waitcnt vmcnt(11)\n\t"
"v_fma_f32 v25, v125, s52, v25\n\t"
"v_fma_f32 v26, v126, s52, v26\n\t"
"v_mfma_f32_16x16x32_bf16 v[160:163], a[208:211], a[204:207], v[160:163]\n\t"
"v_fma_f32 v27, v127, s52, v27\n\t"
"v_fma_f32 v124, v24, s54, -v124\n\t"
"v_mfma_f32_16x16x32_bf16 v[164:167], a[244:247], a[168:171], v[164:167]\n\t"
"v_fma_f32 v125, v25, s54, -v125\n\t"
"v_fma_f32 v126, v26, s54, -v126\n\t"
"v_mfma_f32_16x16x32_bf16 v[164:167], v[96:99], a[172:175], v[164:167]\n\t"
"v_fma_f32 v127, v27, s54, -v127\n\t"
"v_mfma_f32_16x16x32_bf16 v[164:167], a[248:251], a[176:179], v[164:167]\n\t"
"s_waitcnt lgkmcnt(0)\n\t"
"s_barrier\n\t"
"v_mfma_f32_16x16x32_bf16 v[168:171], a[244:247], a[172:175], v[168:171]\n\t"
"ds_read_b128 a[168:171], v244 offset:24576\n\t"
"v_mfma_f32_16x16x32_bf16 v[168:171], v[96:99], a[176:179], v[168:171]\n\t"
"buffer_load_dwordx4 a[208:211], v252, s[8:11], s20 offen offset:2048 sc1\n\t"
"v_mfma_f32_16x16x32_bf16 v[168:171], a[248:251], a[180:183], v[168:171]\n\t"
"s_mov_b32 s20, 1634304\n\t"
"ds_read_b128 a[172:175], v244 offset:25088\n\t"
"v_fma_f32 v28, v128, s52, v28\n\t"
"v_mfma_f32_16x16x32_bf16 v[172:175], a[244:247], a[176:179], v[172:175]\n\t"
"ds_read_b128 a[176:179], v244 offset:25600\n\t"
"v_fma_f32 v29, v129, s52, v29\n\t"
"v_mfma_f32_16x16x32_bf16 v[172:175], v[96:99], a[180:183], v[172:175]\n\t"
"v_fma_f32 v30, v130, s52, v30\n\t"
"v_fma_f32 v31, v131, s52, v31\n\t"
"v_mfma_f32_16x16x32_bf16 v[172:175], a[248:251], a[184:187], v[172:175]\n\t"
"buffer_load_dwordx4 a[240:243], v252, s[8:11], s20 offen offset:0 sc1\n\t"
"v_mfma_f32_16x16x32_bf16 v[176:179], a[244:247], a[180:183], v[176:179]\n\t"
"ds_read_b128 a[180:183], v244 offset:26112\n\t"
"v_fma_f32 v128, v28, s54, -v128\n\t"
"v_mfma_f32_16x16x32_bf16 v[176:179], v[96:99], a[184:187], v[176:179]\n\t"
"v_fma_f32 v129, v29, s54, -v129\n\t"
"v_fma_f32 v130, v30, s54, -v130\n\t"
"v_mfma_f32_16x16x32_bf16 v[176:179], a[248:251], a[188:191], v[176:179]\n\t"
"v_fma_f32 v131, v31, s54, -v131\n\t"
"v_fma_f32 v32, v132, s52, v32\n\t"
"v_mfma_f32_16x16x32_bf16 v[180:183], a[244:247], a[184:187], v[180:183]\n\t"
"ds_read_b128 a[184:187], v244 offset:26624\n\t"
"v_fma_f32 v33, v133, s52, v33\n\t"
"v_mfma_f32_16x16x32_bf16 v[180:183], v[96:99], a[188:191], v[180:183]\n\t"
"v_fma_f32 v34, v134, s52, v34\n\t"
"v_fma_f32 v35, v135, s52, v35\n\t"
"v_mfma_f32_16x16x32_bf16 v[180:183], a[248:251], a[192:195], v[180:183]\n\t"
"v_fma_f32 v132, v32, s54, -v132\n\t"
"v_fma_f32 v133, v33, s54, -v133\n\t"
"v_mfma_f32_16x16x32_bf16 v[184:187], a[244:247], a[188:191], v[184:187]\n\t"
"ds_read_b128 a[188:191], v244 offset:27136\n\t"
"v_fma_f32 v134, v34, s54, -v134\n\t"
"v_mfma_f32_16x16x32_bf16 v[184:187], v[96:99], a[192:195], v[184:187]\n\t"
"v_fma_f32 v135, v35, s54, -v135\n\t"
"v_fma_f32 v36, v136, s52, v36\n\t"
"v_mfma_f32_16x16x32_bf16 v[184:187], a[248:251], a[196:199], v[184:187]\n\t"
"v_fma_f32 v37, v137, s52, v37\n\t"
"v_fma_f32 v38, v138, s52, v38\n\t"
"v_mfma_f32_16x16x32_bf16 v[188:191], a[244:247], a[192:195], v[188:191]\n\t"
"ds_read_b128 a[192:195], v244 offset:27648\n\t"
"v_fma_f32 v39, v139, s52, v39\n\t"
"v_mfma_f32_16x16x32_bf16 v[188:191], v[96:99], a[196:199], v[188:191]\n\t"
"v_fma_f32 v136, v36, s54, -v136\n\t"
"v_fma_f32 v137, v37, s54, -v137\n\t"
"v_mfma_f32_16x16x32_bf16 v[188:191], a[248:251], a[200:203], v[188:191]\n\t"
"v_fma_f32 v138, v38, s54, -v138\n\t"
"v_fma_f32 v139, v39, s54, -v139\n\t"
"v_mfma_f32_16x16x32_bf16 v[192:195], a[244:247], a[196:199], v[192:195]\n\t"
"s_waitcnt vmcnt(10) lgkmcnt(4)\n\t"
"ds_read_b128 a[196:199], v244 offset:28160\n\t"
"v_fma_f32 v40, v140, s52, v40\n\t"
"v_mfma_f32_16x16x32_bf16 v[192:195], v[96:99], a[200:203], v[192:195]\n\t"
"ds_read_b128 a[200:203], v244 offset:28672\n\t"
"v_fma_f32 v41, v141, s52, v41\n\t"
"v_mfma_f32_16x16x32_bf16 v[192:195], a[248:251], a[204:207], v[192:195]\n\t"
"ds_read_b128 a[204:207], v244 offset:29184\n\t"
"v_fma_f32 v42, v142, s52, v42\n\t"
"Lst31_%=:\n\t"
"v_mfma_f32_16x16x32_bf16 v[196:199], v[240:243], a[168:171], v[196:199]\n\t"
"s_waitcnt lgkmcnt(6)\n\t"
"v_fma_f32 v43, v143, s52, v43\n\t"
"v_fma_f32 v140, v40, s54, -v140\n\t"
"v_mfma_f32_16x16x32_bf16 v[196:199], v[236:239], a[172:175], v[196:199]\n\t"
"v_fma_f32 v141, v41, s54, -v141\n\t"
"v_fma_f32 v142, v42, s54, -v142\n\t"
"v_mfma_f32_16x16x32_bf16 v[196:199], v[248:251], a[176:179], v[196:199]\n\t"
"buffer_load_dwordx4 a[244:247], v252, s[8:11], s20 offen offset:1024 sc1\n\t"
"v_mfma_f32_16x16x32_bf16 v[204:207], v[240:243], a[172:175], v[204:207]\n\t"
"buffer_load_dwordx4 a[248:251], v252, s[8:11], s20 offen offset:2048 sc1\n\t"
"v_mfma_f32_16x16x32_bf16 v[204:207], v[236:239], a[176:179], v[204:207]\n\t"
"s_waitcnt lgkmcnt(5)\n\t"
"v_fma_f32 v143, v43, s54, -v143\n\t"
"v_fma_f32 v44, v144, s52, v44\n\t"
"v_mfma_f32_16x16x32_bf16 v[204:207], v[248:251], a[180:183], v[204:207]\n\t"
"v_fma_f32 v45, v145, s52, v45\n\t"
"v_fma_f32 v46, v146, s52, v46\n\t"
"v_mfma_f32_16x16x32_bf16 v[212:215], v[240:243], a[176:179], v[212:215]\n\t"
"s_waitcnt lgkmcnt(4)\n\t"
"v_fma_f32 v47, v147, s52, v47\n\t"
"v_fma_f32 v144, v44, s54, -v144\n\t"
"v_mfma_f32_16x16x32_bf16 v[212:215], v[236:239], a[180:183], v[212:215]\n\t"
"v_fma_f32 v145, v45, s54, -v145\n\t"
"v_fma_f32 v146, v46, s54, -v146\n\t"
"v_mfma_f32_16x16x32_bf16 v[212:215], v[248:251], a[184:187], v[212:215]\n\t"
"v_fma_f32 v147, v47, s54, -v147\n\t"
"v_fma_f32 v48, v148, s52, v48\n\t"
"v_mfma_f32_16x16x32_bf16 a[0:3], v[240:243], a[180:183], a[0:3]\n\t"
"s_waitcnt lgkmcnt(3)\n\t"
"v_fma_f32 v49, v149, s52, v49\n\t"
"v_fma_f32 v50, v150, s52, v50\n\t"
"v_mfma_f32_16x16x32_bf16 a[0:3], v[236:239], a[184:187], a[0:3]\n\t"
"v_fma_f32 v51, v151, s52, v51\n\t"
"v_fma_f32 v148, v48, s54, -v148\n\t"
"v_mfma_f32_16x16x32_bf16 a[0:3], v[248:251], a[188:191], a[0:3]\n\t"
"v_fma_f32 v149, v49, s54, -v149\n\t"
"v_fma_f32 v150, v50, s54, -v150\n\t"
"v_mfma_f32_16x16x32_bf16 a[8:11], v[240:243], a[184:187], a[8:11]\n\t"
"s_waitcnt lgkmcnt(2)\n\t"
"v_fma_f32 v151, v51, s54, -v151\n\t"
"v_fma_f32 v52, v152, s52, v52\n\t"
"v_mfma_f32_16x16x32_bf16 a[8:11], v[236:239], a[188:191], a[8:11]\n\t"
"v_fma_f32 v53, v153, s52, v53\n\t"
"v_fma_f32 v54, v154, s52, v54\n\t"
"v_mfma_f32_16x16x32_bf16 a[8:11], v[248:251], a[192:195], a[8:11]\n\t"
"v_fma_f32 v55, v155, s52, v55\n\t"
"v_fma_f32 v152, v52, s54, -v152\n\t"
"v_mfma_f32_16x16x32_bf16 a[16:19], v[240:243], a[188:191], a[16:19]\n\t"
"s_waitcnt lgkmcnt(1)\n\t"
"v_fma_f32 v153, v53, s54, -v153\n\t"
"v_fma_f32 v154, v54, s54, -v154\n\t"
"v_mfma_f32_16x16x32_bf16 a[16:19], v[236:239], a[192:195], a[16:19]\n\t"
"s_mov_b32 s20, 1646592\n\t"
"v_fma_f32 v155, v55, s54, -v155\n\t"
"v_fma_f32 v56, v156, s52, v56\n\t"
"v_mfma_f32_16x16x32_bf16 a[16:19], v[248:251], a[196:199], a[16:19]\n\t"
"v_fma_f32 v57, v157, s52, v57\n\t"
"v_fma_f32 v58, v158, s52, v58\n\t"
"v_mfma_f32_16x16x32_bf16 a[24:27], v[240:243], a[192:195], a[24:27]\n\t"
"s_waitcnt lgkmcnt(0)\n\t"
"v_fma_f32 v59, v159, s52, v59\n\t"
"v_fma_f32 v156, v56, s54, -v156\n\t"
"v_mfma_f32_16x16x32_bf16 a[24:27], v[236:239], a[196:199], a[24:27]\n\t"
"v_fma_f32 v157, v57, s54, -v157\n\t"
"v_fma_f32 v158, v58, s54, -v158\n\t"
"v_mfma_f32_16x16x32_bf16 a[24:27], v[248:251], a[200:203], a[24:27]\n\t"
"v_fma_f32 v159, v59, s54, -v159\n\t"
"v_fma_f32 v60, v160, s52, v60\n\t"
"v_mfma_f32_16x16x32_bf16 a[32:35], v[240:243], a[196:199], a[32:35]\n\t"
"s_waitcnt vmcnt(9)\n\t"
"v_fma_f32 v61, v161, s52, v61\n\t"
"v_fma_f32 v62, v162, s52, v62\n\t"
"v_mfma_f32_16x16x32_bf16 a[32:35], v[236:239], a[200:203], a[32:35]\n\t"
"v_fma_f32 v63, v163, s52, v63\n\t"
"v_fma_f32 v160, v60, s54, -v160\n\t"
"v_mfma_f32_16x16x32_bf16 a[32:35], v[248:251], a[204:207], a[32:35]\n\t"
"v_fma_f32 v161, v61, s54, -v161\n\t"
"v_fma_f32 v162, v62, s54, -v162\n\t"
"v_mfma_f32_16x16x32_bf16 a[40:43], a[252:255], a[168:171], a[40:43]\n\t"
"v_fma_f32 v163, v63, s54, -v163\n\t"
"v_fma_f32 v64, v164, s52, v64\n\t"
"v_mfma_f32_16x16x32_bf16 a[40:43], a[220:223], a[172:175], a[40:43]\n\t"
"v_fma_f32 v65, v165, s52, v65\n\t"
"v_fma_f32 v66, v166, s52, v66\n\t"
"v_mfma_f32_16x16x32_bf16 a[40:43], a[212:215], a[176:179], a[40:43]\n\t"
"buffer_load_dwordx4 v[240:243], v252, s[8:11], s20 offen offset:0 sc1\n\t"
"v_mfma_f32_16x16x32_bf16 a[48:51], a[252:255], a[172:175], a[48:51]\n\t"
"v_fma_f32 v67, v167, s52, v67\n\t"
"v_fma_f32 v164, v64, s54, -v164\n\t"
"v_mfma_f32_16x16x32_bf16 a[48:51], a[220:223], a[176:179], a[48:51]\n\t"
"v_fma_f32 v165, v65, s54, -v165\n\t"
"v_fma_f32 v166, v66, s54, -v166\n\t"
"v_mfma_f32_16x16x32_bf16 a[48:51], a[212:215], a[180:183], a[48:51]\n\t"
"v_fma_f32 v167, v67, s54, -v167\n\t"
"v_fma_f32 v68, v168, s52, v68\n\t"
"v_mfma_f32_16x16x32_bf16 a[56:59], a[252:255], a[176:179], a[56:59]\n\t"
"v_fma_f32 v69, v169, s52, v69\n\t"
"v_fma_f32 v70, v170, s52, v70\n\t"
"v_mfma_f32_16x16x32_bf16 a[56:59], a[220:223], a[180:183], a[56:59]\n\t"
"v_fma_f32 v71, v171, s52, v71\n\t"
"v_fma_f32 v168, v68, s54, -v168\n\t"
"v_mfma_f32_16x16x32_bf16 a[56:59], a[212:215], a[184:187], a[56:59]\n\t"
"v_fma_f32 v169, v69, s54, -v169\n\t"
"v_fma_f32 v170, v70, s54, -v170\n\t"
"v_mfma_f32_16x16x32_bf16 a[64:67], a[252:255], a[180:183], a[64:67]\n\t"
"v_fma_f32 v171, v71, s54, -v171\n\t"
"v_fma_f32 v72, v172, s52, v72\n\t"
"v_mfma_f32_16x16x32_bf16 a[64:67], a[220:223], a[184:187], a[64:67]\n\t"
"v_fma_f32 v73, v173, s52, v73\n\t"
"v_fma_f32 v74, v174, s52, v74\n\t"
"v_mfma_f32_16x16x32_bf16 a[64:67], a[212:215], a[188:191], a[64:67]\n\t"
"v_fma_f32 v75, v175, s52, v75\n\t"
"v_fma_f32 v172, v72, s54, -v172\n\t"
"v_mfma_f32_16x16x32_bf16 a[72:75], a[252:255], a[184:187], a[72:75]\n\t"
"v_fma_f32 v173, v73, s54, -v173\n\t"
"v_fma_f32 v174, v74, s54, -v174\n\t"
"v_mfma_f32_16x16x32_bf16 a[72:75], a[220:223], a[188:191], a[72:75]\n\t"
"v_fma_f32 v175, v75, s54, -v175\n\t"
"v_fma_f32 v76, v176, s52, v76\n\t"
"v_mfma_f32_16x16x32_bf16 a[72:75], a[212:215], a[192:195], a[72:75]\n\t"
"v_fma_f32 v77, v177, s52, v77\n\t"
"v_fma_f32 v78, v178, s52, v78\n\t"
"v_mfma_f32_16x16x32_bf16 a[80:83], a[252:255], a[188:191], a[80:83]\n\t"
"v_fma_f32 v79, v179, s52, v79\n\t"
"v_fma_f32 v176, v76, s54, -v176\n\t"
"v_mfma_f32_16x16x32_bf16 a[80:83], a[220:223], a[192:195], a[80:83]\n\t"
"v_fma_f32 v177, v77, s54, -v177\n\t"
"v_fma_f32 v178, v78, s54, -v178\n\t"
"v_mfma_f32_16x16x32_bf16 a[80:83], a[212:215], a[196:199], a[80:83]\n\t"
"v_fma_f32 v179, v79, s54, -v179\n\t"
"v_fma_f32 v80, v180, s52, v80\n\t"
"v_mfma_f32_16x16x32_bf16 a[88:91], a[252:255], a[192:195], a[88:91]\n\t"
"v_fma_f32 v81, v181, s52, v81\n\t"
"v_fma_f32 v82, v182, s52, v82\n\t"
"v_mfma_f32_16x16x32_bf16 a[88:91], a[220:223], a[196:199], a[88:91]\n\t"
"v_fma_f32 v83, v183, s52, v83\n\t"
"v_fma_f32 v180, v80, s54, -v180\n\t"
"v_mfma_f32_16x16x32_bf16 a[88:91], a[212:215], a[200:203], a[88:91]\n\t"
"v_fma_f32 v181, v81, s54, -v181\n\t"
"v_fma_f32 v182, v82, s54, -v182\n\t"
"v_mfma_f32_16x16x32_bf16 a[96:99], a[252:255], a[196:199], a[96:99]\n\t"
"s_waitcnt vmcnt(7)\n\t"
"v_fma_f32 v183, v83, s54, -v183\n\t"
"v_fma_f32 v84, v184, s52, v84\n\t"
"v_mfma_f32_16x16x32_bf16 a[96:99], a[220:223], a[200:203], a[96:99]\n\t"
"v_fma_f32 v85, v185, s52, v85\n\t"
"v_fma_f32 v86, v186, s52, v86\n\t"
"v_mfma_f32_16x16x32_bf16 a[96:99], a[212:215], a[204:207], a[96:99]\n\t"
"v_fma_f32 v87, v187, s52, v87\n\t"
"v_fma_f32 v184, v84, s54, -v184\n\t"
"v_mfma_f32_16x16x32_bf16 a[104:107], a[224:227], a[168:171], a[104:107]\n\t"
"ds_read_b128 a[168:171], v244 offset:33792\n\t"
"v_fma_f32 v185, v85, s54, -v185\n\t"
"v_mfma_f32_16x16x32_bf16 a[104:107], a[228:231], a[172:175], a[104:107]\n\t"
"v_fma_f32 v186, v86, s54, -v186\n\t"
"v_fma_f32 v187, v87, s54, -v187\n\t"
"v_mfma_f32_16x16x32_bf16 a[104:107], a[216:219], a[176:179], a[104:107]\n\t"
"buffer_load_dwordx4 a[252:255], v252, s[8:11], s20 offen offset:1024 sc1\n\t"
"v_mfma_f32_16x16x32_bf16 a[112:115], a[224:227], a[172:175], a[112:115]\n\t"
"buffer_load_dwordx4 a[212:215], v252, s[8:11], s20 offen offset:2048 sc1\n\t"
"v_mfma_f32_16x16x32_bf16 a[112:115], a[228:231], a[176:179], a[112:115]\n\t"
"s_mov_b32 s20, 1179648\n\t"
"ds_read_b128 a[172:175], v244 offset:34304\n\t"
"v_fma_f32 v88, v188, s52, v88\n\t"
"v_mfma_f32_16x16x32_bf16 a[112:115], a[216:219], a[180:183], a[112:115]\n\t"
"v_fma_f32 v89, v189, s52, v89\n\t"
"v_fma_f32 v90, v190, s52, v90\n\t"
"v_mfma_f32_16x16x32_bf16 a[120:123], a[224:227], a[176:179], a[120:123]\n\t"
"ds_read_b128 a[176:179], v244 offset:34816\n\t"
"v_fma_f32 v91, v191, s52, v91\n\t"
"v_mfma_f32_16x16x32_bf16 a[120:123], a[228:231], a[180:183], a[120:123]\n\t"
"v_fma_f32 v188, v88, s54, -v188\n\t"
"v_fma_f32 v189, v89, s54, -v189\n\t"
"v_mfma_f32_16x16x32_bf16 a[120:123], a[216:219], a[184:187], a[120:123]\n\t"
"buffer_load_dwordx4 a[220:223], v252, s[8:11], s20 offen offset:0 sc1\n\t"
"v_mfma_f32_16x16x32_bf16 a[128:131], a[224:227], a[180:183], a[128:131]\n\t"
"ds_read_b128 a[180:183], v244 offset:35328\n\t"
"v_fma_f32 v190, v90, s54, -v190\n\t"
"v_mfma_f32_16x16x32_bf16 a[128:131], a[228:231], a[184:187], a[128:131]\n\t"
"v_fma_f32 v191, v91, s54, -v191\n\t"
"v_fma_f32 v92, v192, s52, v92\n\t"
"v_mfma_f32_16x16x32_bf16 a[128:131], a[216:219], a[188:191], a[128:131]\n\t"
"v_fma_f32 v93, v193, s52, v93\n\t"
"v_fma_f32 v94, v194, s52, v94\n\t"
"v_mfma_f32_16x16x32_bf16 a[136:139], a[224:227], a[184:187], a[136:139]\n\t"
"ds_read_b128 a[184:187], v244 offset:35840\n\t"
"v_fma_f32 v95, v195, s52, v95\n\t"
"v_mfma_f32_16x16x32_bf16 a[136:139], a[228:231], a[188:191], a[136:139]\n\t"
"v_fma_f32 v192, v92, s54, -v192\n\t"
"v_fma_f32 v193, v93, s54, -v193\n\t"
"v_mfma_f32_16x16x32_bf16 a[136:139], a[216:219], a[192:195], a[136:139]\n\t"
"v_fma_f32 v194, v94, s54, -v194\n\t"
"v_fma_f32 v195, v95, s54, -v195\n\t"
"v_mfma_f32_16x16x32_bf16 a[144:147], a[224:227], a[188:191], a[144:147]\n\t"
"ds_read_b128 a[188:191], v244 offset:36352\n\t"
"v_mfma_f32_16x16x32_bf16 a[144:147], a[228:231], a[192:195], a[144:147]\n\t"
"v_mfma_f32_16x16x32_bf16 a[144:147], a[216:219], a[196:199], a[144:147]\n\t"
"v_mfma_f32_16x16x32_bf16 a[152:155], a[224:227], a[192:195], a[152:155]\n\t"
"ds_read_b128 a[192:195], v244 offset:36864\n\t"
"v_mfma_f32_16x16x32_bf16 a[152:155], a[228:231], a[196:199], a[152:155]\n\t"
"v_mfma_f32_16x16x32_bf16 a[152:155], a[216:219], a[200:203], a[152:155]\n\t"
"v_mfma_f32_16x16x32_bf16 a[160:163], a[224:227], a[196:199], a[160:163]\n\t"
"buffer_load_dwordx4 a[224:227], v252, s[8:11], s20 offen offset:1024 sc1\n\t"
"v_mfma_f32_16x16x32_bf16 a[160:163], a[228:231], a[200:203], a[160:163]\n\t"
"s_waitcnt vmcnt(8) lgkmcnt(4)\n\t"
"ds_read_b128 a[196:199], v244 offset:37376\n\t"
"v_mfma_f32_16x16x32_bf16 a[160:163], a[216:219], a[204:207], a[160:163]\n\t"
"ds_read_b128 a[200:203], v244 offset:37888\n\t"
"v_mfma_f32_16x16x32_bf16 v[200:203], a[232:235], a[168:171], v[200:203]\n\t"
"s_waitcnt lgkmcnt(5)\n\t"
"ds_read_b128 a[204:207], v244 offset:38400\n\t"
"v_mfma_f32_16x16x32_bf16 v[200:203], a[236:239], a[172:175], v[200:203]\n\t"
"buffer_load_dwordx4 a[216:219], v252, s[8:11], s20 offen offset:2048 sc1\n\t"
"v_mfma_f32_16x16x32_bf16 v[200:203], a[208:211], a[176:179], v[200:203]\n\t"
"s_mov_b32 s20, 1191936\n\t"
"v_mfma_f32_16x16x32_bf16 v[208:211], a[232:235], a[172:175], v[208:211]\n\t"
"buffer_load_dwordx4 a[228:231], v252, s[8:11], s20 offen offset:0 sc1\n\t"
"v_mfma_f32_16x16x32_bf16 v[208:211], a[236:239], a[176:179], v[208:211]\n\t"
"s_waitcnt lgkmcnt(5)\n\t"
"v_mfma_f32_16x16x32_bf16 v[208:211], a[208:211], a[180:183], v[208:211]\n\t"
"s_waitcnt lgkmcnt(0)\n\t"
"s_barrier\n\t"
"v_mfma_f32_16x16x32_bf16 v[216:219], a[232:235], a[176:179], v[216:219]\n\t"
"v_mfma_f32_16x16x32_bf16 v[216:219], a[236:239], a[180:183], v[216:219]\n\t"
"v_mfma_f32_16x16x32_bf16 v[216:219], a[208:211], a[184:187], v[216:219]\n\t"
"v_pk_fma_f32 v[196:197], v[200:201], s[56:57], v[196:197]\n\t"
"v_pk_fma_f32 v[200:201], v[196:197], s[58:59], v[200:201] neg_lo:[0,0,1] neg_hi:[0,0,1]\n\t"
"v_pk_fma_f32 v[228:229], v[0:1], s[60:61], v[196:197]\n\t"
"v_pk_fma_f32 v[0:1], v[196:197], s[60:61], v[0:1]\n\t"
"v_pk_fma_f32 v[196:197], v[100:101], s[62:63], v[200:201]\n\t"
"v_pk_fma_f32 v[100:101], v[200:201], s[62:63], v[100:101]\n\t"
"v_pk_fma_f32 v[198:199], v[202:203], s[56:57], v[198:199]\n\t"
"v_pk_fma_f32 v[202:203], v[198:199], s[58:59], v[202:203] neg_lo:[0,0,1] neg_hi:[0,0,1]\n\t"
"v_pk_fma_f32 v[234:235], v[2:3], s[60:61], v[198:199]\n\t"
"v_pk_fma_f32 v[2:3], v[198:199], s[60:61], v[2:3]\n\t"
"v_pk_fma_f32 v[198:199], v[102:103], s[62:63], v[202:203]\n\t"
"v_pk_fma_f32 v[102:103], v[202:203], s[62:63], v[102:103]\n\t"
"v_mfma_f32_16x16x32_bf16 a[4:7], a[232:235], a[180:183], a[4:7]\n\t"
"v_cvt_pk_bf16_f32 v196, v196, v197\n\t"
"v_cvt_pk_bf16_f32 v200, v228, v229\n\t"
"v_mfma_f32_16x16x32_bf16 a[4:7], a[236:239], a[184:187], a[4:7]\n\t"
"v_cvt_pk_bf16_f32 v197, v198, v199\n\t"
"v_cvt_pk_bf16_f32 v201, v234, v235\n\t"
"v_mfma_f32_16x16x32_bf16 a[4:7], a[208:211], a[188:191], a[4:7]\n\t"
"v_pk_fma_f32 v[204:205], v[208:209], s[56:57], v[204:205]\n\t"
"v_pk_fma_f32 v[208:209], v[204:205], s[58:59], v[208:209] neg_lo:[0,0,1] neg_hi:[0,0,1]\n\t"
"v_pk_fma_f32 v[98:99], v[4:5], s[60:61], v[204:205]\n\t"
"v_pk_fma_f32 v[4:5], v[204:205], s[60:61], v[4:5]\n\t"
"v_pk_fma_f32 v[204:205], v[104:105], s[62:63], v[208:209]\n\t"
"v_pk_fma_f32 v[104:105], v[208:209], s[62:63], v[104:105]\n\t"
"v_mfma_f32_16x16x32_bf16 a[12:15], a[232:235], a[184:187], a[12:15]\n\t"
"v_cvt_pk_bf16_f32 v204, v204, v205\n\t"
"v_cvt_pk_bf16_f32 v208, v98, v99\n\t"
"v_mfma_f32_16x16x32_bf16 a[12:15], a[236:239], a[188:191], a[12:15]\n\t"
"v_mfma_f32_16x16x32_bf16 a[12:15], a[208:211], a[192:195], a[12:15]\n\t"
"v_pk_fma_f32 v[206:207], v[210:211], s[56:57], v[206:207]\n\t"
"v_pk_fma_f32 v[210:211], v[206:207], s[58:59], v[210:211] neg_lo:[0,0,1] neg_hi:[0,0,1]\n\t"
"v_pk_fma_f32 v[228:229], v[6:7], s[60:61], v[206:207]\n\t"
"v_pk_fma_f32 v[6:7], v[206:207], s[60:61], v[6:7]\n\t"
"v_pk_fma_f32 v[206:207], v[106:107], s[62:63], v[210:211]\n\t"
"v_pk_fma_f32 v[106:107], v[210:211], s[62:63], v[106:107]\n\t"
"v_mfma_f32_16x16x32_bf16 a[20:23], a[232:235], a[188:191], a[20:23]\n\t"
"v_cvt_pk_bf16_f32 v205, v206, v207\n\t"
"v_cvt_pk_bf16_f32 v209, v228, v229\n\t"
"v_mfma_f32_16x16x32_bf16 a[20:23], a[236:239], a[192:195], a[20:23]\n\t"
"v_accvgpr_read_b32 v224, a0\n\t"
"v_accvgpr_read_b32 v226, a4\n\t"
"v_mfma_f32_16x16x32_bf16 a[20:23], a[208:211], a[196:199], a[20:23]\n\t"
"v_accvgpr_read_b32 v225, a1\n\t"
"v_accvgpr_read_b32 v227, a5\n\t"
"v_pk_fma_f32 v[212:213], v[216:217], s[56:57], v[212:213]\n\t"
"v_pk_fma_f32 v[216:217], v[212:213], s[58:59], v[216:217] neg_lo:[0,0,1] neg_hi:[0,0,1]\n\t"
"v_pk_fma_f32 v[234:235], v[8:9], s[60:61], v[212:213]\n\t"
"v_pk_fma_f32 v[8:9], v[212:213], s[60:61], v[8:9]\n\t"
"v_pk_fma_f32 v[212:213], v[108:109], s[62:63], v[216:217]\n\t"
"v_pk_fma_f32 v[108:109], v[216:217], s[62:63], v[108:109]\n\t"
"v_mfma_f32_16x16x32_bf16 a[28:31], a[232:235], a[192:195], a[28:31]\n\t"
"v_cvt_pk_bf16_f32 v212, v212, v213\n\t"
"v_cvt_pk_bf16_f32 v216, v234, v235\n\t"
"v_mfma_f32_16x16x32_bf16 a[28:31], a[236:239], a[196:199], a[28:31]\n\t"
"v_accvgpr_read_b32 v230, a2\n\t"
"v_accvgpr_read_b32 v232, a6\n\t"
"v_mfma_f32_16x16x32_bf16 a[28:31], a[208:211], a[200:203], a[28:31]\n\t"
"v_accvgpr_read_b32 v231, a3\n\t"
"v_accvgpr_read_b32 v233, a7\n\t"
"v_pk_fma_f32 v[214:215], v[218:219], s[56:57], v[214:215]\n\t"
"v_pk_fma_f32 v[218:219], v[214:215], s[58:59], v[218:219] neg_lo:[0,0,1] neg_hi:[0,0,1]\n\t"
"v_pk_fma_f32 v[98:99], v[10:11], s[60:61], v[214:215]\n\t"
"v_pk_fma_f32 v[10:11], v[214:215], s[60:61], v[10:11]\n\t"
"v_pk_fma_f32 v[214:215], v[110:111], s[62:63], v[218:219]\n\t"
"v_pk_fma_f32 v[110:111], v[218:219], s[62:63], v[110:111]\n\t"
"v_mfma_f32_16x16x32_bf16 a[36:39], a[232:235], a[196:199], a[36:39]\n\t"
"s_waitcnt vmcnt(7)\n\t"
"v_cvt_pk_bf16_f32 v213, v214, v215\n\t"
"v_cvt_pk_bf16_f32 v217, v98, v99\n\t"
"v_mfma_f32_16x16x32_bf16 a[36:39], a[236:239], a[200:203], a[36:39]\n\t"
"v_accvgpr_read_b32 v236, a8\n\t"
"v_accvgpr_read_b32 v238, a12\n\t"
"v_mfma_f32_16x16x32_bf16 a[36:39], a[208:211], a[204:207], a[36:39]\n\t"
"buffer_load_dwordx4 a[232:235], v252, s[8:11], s20 offen offset:1024 sc1\n\t"
"v_accvgpr_read_b32 v237, a9\n\t"
"v_accvgpr_read_b32 v239, a13\n\t"
"v_pk_fma_f32 v[224:225], v[226:227], s[56:57], v[224:225]\n\t"
"v_pk_fma_f32 v[226:227], v[224:225], s[58:59], v[226:227] neg_lo:[0,0,1] neg_hi:[0,0,1]\n\t"
"v_pk_fma_f32 v[228:229], v[12:13], s[60:61], v[224:225]\n\t"
"v_pk_fma_f32 v[12:13], v[224:225], s[60:61], v[12:13]\n\t"
"v_pk_fma_f32 v[224:225], v[112:113], s[62:63], v[226:227]\n\t"
"v_pk_fma_f32 v[112:113], v[226:227], s[62:63], v[112:113]\n\t"
"v_mfma_f32_16x16x32_bf16 a[44:47], a[240:243], a[168:171], a[44:47]\n\t"
"v_cvt_pk_bf16_f32 v224, v224, v225\n\t"
"v_cvt_pk_bf16_f32 v228, v228, v229\n\t"
"v_mfma_f32_16x16x32_bf16 a[44:47], a[244:247], a[172:175], a[44:47]\n\t"
"v_accvgpr_write_b32 a0, v224\n\t"
"v_accvgpr_write_b32 a4, v228\n\t"
"v_mfma_f32_16x16x32_bf16 a[44:47], a[248:251], a[176:179], a[44:47]\n\t"
"buffer_load_dwordx4 a[208:211], v252, s[8:11], s20 offen offset:2048 sc1\n\t"
"v_accvgpr_read_b32 v224, a10\n\t"
"v_accvgpr_read_b32 v226, a14\n\t"
"s_mov_b32 s20, 1204224\n\t"
"v_pk_fma_f32 v[230:231], v[232:233], s[56:57], v[230:231]\n\t"
"v_pk_fma_f32 v[232:233], v[230:231], s[58:59], v[232:233] neg_lo:[0,0,1] neg_hi:[0,0,1]\n\t"
"v_pk_fma_f32 v[234:235], v[14:15], s[60:61], v[230:231]\n\t"
"v_pk_fma_f32 v[14:15], v[230:231], s[60:61], v[14:15]\n\t"
"v_pk_fma_f32 v[230:231], v[114:115], s[62:63], v[232:233]\n\t"
"v_pk_fma_f32 v[114:115], v[232:233], s[62:63], v[114:115]\n\t"
"v_mfma_f32_16x16x32_bf16 a[52:55], a[240:243], a[172:175], a[52:55]\n\t"
"v_cvt_pk_bf16_f32 v230, v230, v231\n\t"
"v_cvt_pk_bf16_f32 v234, v234, v235\n\t"
"v_mfma_f32_16x16x32_bf16 a[52:55], a[244:247], a[176:179], a[52:55]\n\t"
"v_accvgpr_write_b32 a1, v230\n\t"
"v_accvgpr_write_b32 a5, v234\n\t"
"v_mfma_f32_16x16x32_bf16 a[52:55], a[248:251], a[180:183], a[52:55]\n\t"
"buffer_load_dwordx4 a[236:239], v252, s[8:11], s20 offen offset:0 sc1\n\t"
"v_accvgpr_read_b32 v225, a11\n\t"
"v_accvgpr_read_b32 v227, a15\n\t"
"v_pk_fma_f32 v[236:237], v[238:239], s[56:57], v[236:237]\n\t"
"v_pk_fma_f32 v[238:239], v[236:237], s[58:59], v[238:239] neg_lo:[0,0,1] neg_hi:[0,0,1]\n\t"
"v_pk_fma_f32 v[98:99], v[16:17], s[60:61], v[236:237]\n\t"
"v_pk_fma_f32 v[16:17], v[236:237], s[60:61], v[16:17]\n\t"
"v_pk_fma_f32 v[236:237], v[116:117], s[62:63], v[238:239]\n\t"
"v_pk_fma_f32 v[116:117], v[238:239], s[62:63], v[116:117]\n\t"
"v_mfma_f32_16x16x32_bf16 a[60:63], a[240:243], a[176:179], a[60:63]\n\t"
"v_accvgpr_read_b32 v230, a16\n\t"
"v_accvgpr_read_b32 v232, a20\n\t"
"v_mfma_f32_16x16x32_bf16 a[60:63], a[244:247], a[180:183], a[60:63]\n\t"
"v_accvgpr_read_b32 v231, a17\n\t"
"v_accvgpr_read_b32 v233, a21\n\t"
"v_mfma_f32_16x16x32_bf16 a[60:63], a[248:251], a[184:187], a[60:63]\n\t"
"v_cvt_pk_bf16_f32 v236, v236, v237\n\t"
"v_cvt_pk_bf16_f32 v98, v98, v99\n\t"
"v_accvgpr_write_b32 a8, v236\n\t"
"v_accvgpr_write_b32 a12, v98\n\t"
"v_accvgpr_read_b32 v236, a18\n\t"
"Lpg23_%=:\n\t"
"s_cbranch_vccnz Lvr3_%=\n\t"
"v_pk_fma_f32 v[224:225], v[226:227], s[56:57], v[224:225]\n\t"
"v_pk_fma_f32 v[226:227], v[224:225], s[58:59], v[226:227] neg_lo:[0,0,1] neg_hi:[0,0,1]\n\t"
"v_pk_fma_f32 v[228:229], v[18:19], s[60:61], v[224:225]\n\t"
"v_pk_fma_f32 v[18:19], v[224:225], s[60:61], v[18:19]\n\t"
"v_pk_fma_f32 v[224:225], v[118:119], s[62:63], v[226:227]\n\t"
"v_pk_fma_f32 v[118:119], v[226:227], s[62:63], v[118:119]\n\t"
"v_pk_fma_f32 v[230:231], v[232:233], s[56:57], v[230:231]\n\t"
"v_pk_fma_f32 v[232:233], v[230:231], s[58:59], v[232:233] neg_lo:[0,0,1] neg_hi:[0,0,1]\n\t"
"v_pk_fma_f32 v[234:235], v[20:21], s[60:61], v[230:231]\n\t"
"v_pk_fma_f32 v[20:21], v[230:231], s[60:61], v[20:21]\n\t"
"v_pk_fma_f32 v[230:231], v[120:121], s[62:63], v[232:233]\n\t"
"v_pk_fma_f32 v[120:121], v[232:233], s[62:63], v[120:121]\n\t"
"v_mfma_f32_16x16x32_bf16 a[68:71], a[240:243], a[180:183], a[68:71]\n\t"
"v_accvgpr_read_b32 v238, a22\n\t"
"v_accvgpr_read_b32 v237, a19\n\t"
"v_mfma_f32_16x16x32_bf16 a[68:71], a[244:247], a[184:187], a[68:71]\n\t"
"v_accvgpr_read_b32 v239, a23\n\t"
"v_cvt_pk_bf16_f32 v224, v224, v225\n\t"
"v_mfma_f32_16x16x32_bf16 a[68:71], a[248:251], a[188:191], a[68:71]\n\t"
"v_cvt_pk_bf16_f32 v228, v228, v229\n\t"
"v_cvt_pk_bf16_f32 v230, v230, v231\n\t"
"v_cvt_pk_bf16_f32 v234, v234, v235\n\t"
"v_accvgpr_write_b32 a9, v224\n\t"
"v_accvgpr_write_b32 a13, v228\n\t"
"v_pk_fma_f32 v[236:237], v[238:239], s[56:57], v[236:237]\n\t"
"v_pk_fma_f32 v[238:239], v[236:237], s[58:59], v[238:239] neg_lo:[0,0,1] neg_hi:[0,0,1]\n\t"
"v_pk_fma_f32 v[98:99], v[22:23], s[60:61], v[236:237]\n\t"
"v_pk_fma_f32 v[22:23], v[236:237], s[60:61], v[22:23]\n\t"
"v_pk_fma_f32 v[236:237], v[122:123], s[62:63], v[238:239]\n\t"
"v_pk_fma_f32 v[122:123], v[238:239], s[62:63], v[122:123]\n\t"
"v_mfma_f32_16x16x32_bf16 a[76:79], a[240:243], a[184:187], a[76:79]\n\t"
"v_accvgpr_write_b32 a16, v230\n\t"
"v_accvgpr_write_b32 a20, v234\n\t"
"v_mfma_f32_16x16x32_bf16 a[76:79], a[244:247], a[188:191], a[76:79]\n\t"
"v_accvgpr_read_b32 v224, a24\n\t"
"v_accvgpr_read_b32 v226, a28\n\t"
"v_mfma_f32_16x16x32_bf16 a[76:79], a[248:251], a[192:195], a[76:79]\n\t"
"v_accvgpr_read_b32 v225, a25\n\t"
"v_accvgpr_read_b32 v227, a29\n\t"
"v_cvt_pk_bf16_f32 v236, v236, v237\n\t"
"v_cvt_pk_bf16_f32 v98, v98, v99\n\t"
"v_accvgpr_write_b32 a17, v236\n\t"
"v_pk_fma_f32 v[224:225], v[226:227], s[56:57], v[224:225]\n\t"
"v_pk_fma_f32 v[226:227], v[224:225], s[58:59], v[226:227] neg_lo:[0,0,1] neg_hi:[0,0,1]\n\t"
"v_pk_fma_f32 v[228:229], v[24:25], s[60:61], v[224:225]\n\t"
"v_pk_fma_f32 v[24:25], v[224:225], s[60:61], v[24:25]\n\t"
"v_pk_fma_f32 v[224:225], v[124:125], s[62:63], v[226:227]\n\t"
"v_pk_fma_f32 v[124:125], v[226:227], s[62:63], v[124:125]\n\t"
"v_mfma_f32_16x16x32_bf16 a[84:87], a[240:243], a[188:191], a[84:87]\n\t"
"v_accvgpr_write_b32 a21, v98\n\t"
"v_accvgpr_read_b32 v230, a26\n\t"
"v_mfma_f32_16x16x32_bf16 a[84:87], a[244:247], a[192:195], a[84:87]\n\t"
"v_accvgpr_read_b32 v232, a30\n\t"
"v_accvgpr_read_b32 v231, a27\n\t"
"v_mfma_f32_16x16x32_bf16 a[84:87], a[248:251], a[196:199], a[84:87]\n\t"
"v_accvgpr_read_b32 v233, a31\n\t"
"v_cvt_pk_bf16_f32 v224, v224, v225\n\t"
"v_cvt_pk_bf16_f32 v228, v228, v229\n\t"
"v_accvgpr_write_b32 a24, v224\n\t"
"v_accvgpr_write_b32 a28, v228\n\t"
"v_pk_fma_f32 v[230:231], v[232:233], s[56:57], v[230:231]\n\t"
"v_pk_fma_f32 v[232:233], v[230:231], s[58:59], v[232:233] neg_lo:[0,0,1] neg_hi:[0,0,1]\n\t"
"v_pk_fma_f32 v[234:235], v[26:27], s[60:61], v[230:231]\n\t"
"v_pk_fma_f32 v[26:27], v[230:231], s[60:61], v[26:27]\n\t"
"v_pk_fma_f32 v[230:231], v[126:127], s[62:63], v[232:233]\n\t"
"v_pk_fma_f32 v[126:127], v[232:233], s[62:63], v[126:127]\n\t"
"v_mfma_f32_16x16x32_bf16 a[92:95], a[240:243], a[192:195], a[92:95]\n\t"
"v_accvgpr_read_b32 v236, a32\n\t"
"v_accvgpr_read_b32 v238, a36\n\t"
"v_mfma_f32_16x16x32_bf16 a[92:95], a[244:247], a[196:199], a[92:95]\n\t"
"v_accvgpr_read_b32 v237, a33\n\t"
"v_accvgpr_read_b32 v239, a37\n\t"
"v_mfma_f32_16x16x32_bf16 a[92:95], a[248:251], a[200:203], a[92:95]\n\t"
"v_cvt_pk_bf16_f32 v230, v230, v231\n\t"
"v_cvt_pk_bf16_f32 v234, v234, v235\n\t"
"v_accvgpr_write_b32 a25, v230\n\t"
"v_accvgpr_write_b32 a29, v234\n\t"
"v_accvgpr_read_b32 v224, a34\n\t"
"v_pk_fma_f32 v[236:237], v[238:239], s[56:57], v[236:237]\n\t"
"v_pk_fma_f32 v[238:239], v[236:237], s[58:59], v[238:239] neg_lo:[0,0,1] neg_hi:[0,0,1]\n\t"
"v_pk_fma_f32 v[98:99], v[28:29], s[60:61], v[236:237]\n\t"
"v_pk_fma_f32 v[28:29], v[236:237], s[60:61], v[28:29]\n\t"
"v_pk_fma_f32 v[236:237], v[128:129], s[62:63], v[238:239]\n\t"
"v_pk_fma_f32 v[128:129], v[238:239], s[62:63], v[128:129]\n\t"
"v_mfma_f32_16x16x32_bf16 a[100:103], a[240:243], a[196:199], a[100:103]\n\t"
"s_waitcnt vmcnt(7)\n\t"
"v_accvgpr_read_b32 v226, a38\n\t"
"v_accvgpr_read_b32 v225, a35\n\t"
"v_mfma_f32_16x16x32_bf16 a[100:103], a[244:247], a[200:203], a[100:103]\n\t"
"v_accvgpr_read_b32 v227, a39\n\t"
"v_cvt_pk_bf16_f32 v236, v236, v237\n\t"
"v_mfma_f32_16x16x32_bf16 a[100:103], a[248:251], a[204:207], a[100:103]\n\t"
"buffer_load_dwordx4 a[240:243], v252, s[8:11], s20 offen offset:1024 sc1\n\t"
"v_cvt_pk_bf16_f32 v98, v98, v99\n\t"
"v_accvgpr_write_b32 a32, v236\n\t"
"v_pk_fma_f32 v[224:225], v[226:227], s[56:57], v[224:225]\n\t"
"v_pk_fma_f32 v[226:227], v[224:225], s[58:59], v[226:227] neg_lo:[0,0,1] neg_hi:[0,0,1]\n\t"
"v_pk_fma_f32 v[228:229], v[30:31], s[60:61], v[224:225]\n\t"
"v_pk_fma_f32 v[30:31], v[224:225], s[60:61], v[30:31]\n\t"
"v_pk_fma_f32 v[224:225], v[130:131], s[62:63], v[226:227]\n\t"
"v_pk_fma_f32 v[130:131], v[226:227], s[62:63], v[130:131]\n\t"
"v_mfma_f32_16x16x32_bf16 a[108:111], v[240:243], a[168:171], a[108:111]\n\t"
"ds_read_b128 a[168:171], v255 offset:512\n\t"
"v_accvgpr_write_b32 a36, v98\n\t"
"v_mfma_f32_16x16x32_bf16 a[108:111], a[252:255], a[172:175], a[108:111]\n\t"
"v_accvgpr_read_b32 v230, a40\n\t"
"v_accvgpr_read_b32 v232, a44\n\t"
"v_mfma_f32_16x16x32_bf16 a[108:111], a[212:215], a[176:179], a[108:111]\n\t"
"buffer_load_dwordx4 a[244:247], v252, s[8:11], s20 offen offset:2048 sc1\n\t"
"v_accvgpr_read_b32 v231, a41\n\t"
"v_accvgpr_read_b32 v233, a45\n\t"
"v_cvt_pk_bf16_f32 v224, v224, v225\n\t"
"v_cvt_pk_bf16_f32 v228, v228, v229\n\t"
"s_mov_b32 s20, 1363968\n\t"
"v_pk_fma_f32 v[230:231], v[232:233], s[56:57], v[230:231]\n\t"
"v_pk_fma_f32 v[232:233], v[230:231], s[58:59], v[232:233] neg_lo:[0,0,1] neg_hi:[0,0,1]\n\t"
"v_pk_fma_f32 v[234:235], v[32:33], s[60:61], v[230:231]\n\t"
"v_pk_fma_f32 v[32:33], v[230:231], s[60:61], v[32:33]\n\t"
"v_pk_fma_f32 v[230:231], v[132:133], s[62:63], v[232:233]\n\t"
"v_pk_fma_f32 v[132:133], v[232:233], s[62:63], v[132:133]\n\t"
"v_mfma_f32_16x16x32_bf16 a[116:119], v[240:243], a[172:175], a[116:119]\n\t"
"ds_read_b128 a[172:175], v255 offset:3584\n\t"
"v_accvgpr_write_b32 a33, v224\n\t"
"v_mfma_f32_16x16x32_bf16 a[116:119], a[252:255], a[176:179], a[116:119]\n\t"
"v_accvgpr_write_b32 a37, v228\n\t"
"v_accvgpr_read_b32 v236, a42\n\t"
"v_mfma_f32_16x16x32_bf16 a[116:119], a[212:215], a[180:183], a[116:119]\n\t"
"buffer_load_dwordx4 a[248:251], v252, s[8:11], s20 offen offset:0 sc1\n\t"
"v_accvgpr_read_b32 v238, a46\n\t"
"v_accvgpr_read_b32 v237, a43\n\t"
"v_accvgpr_read_b32 v239, a47\n\t"
"v_pk_fma_f32 v[236:237], v[238:239], s[56:57], v[236:237]\n\t"
"v_pk_fma_f32 v[238:239], v[236:237], s[58:59], v[238:239] neg_lo:[0,0,1] neg_hi:[0,0,1]\n\t"
"v_pk_fma_f32 v[98:99], v[34:35], s[60:61], v[236:237]\n\t"
"v_pk_fma_f32 v[34:35], v[236:237], s[60:61], v[34:35]\n\t"
"v_pk_fma_f32 v[236:237], v[134:135], s[62:63], v[238:239]\n\t"
"v_pk_fma_f32 v[134:135], v[238:239], s[62:63], v[134:135]\n\t"
"v_mfma_f32_16x16x32_bf16 a[124:127], v[240:243], a[176:179], a[124:127]\n\t"
"ds_read_b128 a[176:179], v255 offset:6656\n\t"
"v_cvt_pk_bf16_f32 v198, v230, v231\n\t"
"v_mfma_f32_16x16x32_bf16 a[124:127], a[252:255], a[180:183], a[124:127]\n\t"
"v_cvt_pk_bf16_f32 v202, v234, v235\n\t"
"v_accvgpr_read_b32 v224, a48\n\t"
"v_mfma_f32_16x16x32_bf16 a[124:127], a[212:215], a[184:187], a[124:127]\n\t"
"global_load_dwordx4 v[248:251], v252, s[48:49] offset:0\n\t"
"v_accvgpr_read_b32 v226, a52\n\t"
"v_accvgpr_read_b32 v225, a49\n\t"
"v_accvgpr_read_b32 v227, a53\n\t"
"v_accvgpr_read_b32 v230, a50\n\t"
"v_accvgpr_read_b32 v232, a54\n\t"
"v_accvgpr_read_b32 v231, a51\n\t"
"v_accvgpr_read_b32 v233, a55\n\t"
"v_pk_fma_f32 v[224:225], v[226:227], s[56:57], v[224:225]\n\t"
"v_pk_fma_f32 v[226:227], v[224:225], s[58:59], v[226:227] neg_lo:[0,0,1] neg_hi:[0,0,1]\n\t"
"v_pk_fma_f32 v[228:229], v[36:37], s[60:61], v[224:225]\n\t"
"v_pk_fma_f32 v[36:37], v[224:225], s[60:61], v[36:37]\n\t"
"v_pk_fma_f32 v[224:225], v[136:137], s[62:63], v[226:227]\n\t"
"v_pk_fma_f32 v[136:137], v[226:227], s[62:63], v[136:137]\n\t"
"v_pk_fma_f32 v[230:231], v[232:233], s[56:57], v[230:231]\n\t"
"v_pk_fma_f32 v[232:233], v[230:231], s[58:59], v[232:233] neg_lo:[0,0,1] neg_hi:[0,0,1]\n\t"
"v_pk_fma_f32 v[234:235], v[38:39], s[60:61], v[230:231]\n\t"
"v_pk_fma_f32 v[38:39], v[230:231], s[60:61], v[38:39]\n\t"
"v_pk_fma_f32 v[230:231], v[138:139], s[62:63], v[232:233]\n\t"
"v_pk_fma_f32 v[138:139], v[232:233], s[62:63], v[138:139]\n\t"
"v_mfma_f32_16x16x32_bf16 a[132:135], v[240:243], a[180:183], a[132:135]\n\t"
"ds_read_b128 a[180:183], v255 offset:9728\n\t"
"v_cvt_pk_bf16_f32 v199, v236, v237\n\t"
"v_mfma_f32_16x16x32_bf16 a[132:135], a[252:255], a[184:187], a[132:135]\n\t"
"v_cvt_pk_bf16_f32 v203, v98, v99\n\t"
"v_accvgpr_read_b32 v236, a56\n\t"
"v_mfma_f32_16x16x32_bf16 a[132:135], a[212:215], a[188:191], a[132:135]\n\t"
"buffer_load_dwordx4 a[52:55], v252, s[8:11], s20 offen offset:1024 sc1\n\t"
"v_accvgpr_read_b32 v238, a60\n\t"
"v_accvgpr_read_b32 v237, a57\n\t"
"v_accvgpr_read_b32 v239, a61\n\t"
"v_pk_fma_f32 v[236:237], v[238:239], s[56:57], v[236:237]\n\t"
"v_pk_fma_f32 v[238:239], v[236:237], s[58:59], v[238:239] neg_lo:[0,0,1] neg_hi:[0,0,1]\n\t"
"v_pk_fma_f32 v[98:99], v[40:41], s[60:61], v[236:237]\n\t"
"v_pk_fma_f32 v[40:41], v[236:237], s[60:61], v[40:41]\n\t"
"v_pk_fma_f32 v[236:237], v[140:141], s[62:63], v[238:239]\n\t"
"v_pk_fma_f32 v[140:141], v[238:239], s[62:63], v[140:141]\n\t"
"v_mfma_f32_16x16x32_bf16 a[140:143], v[240:243], a[184:187], a[140:143]\n\t"
"ds_read_b128 a[184:187], v255 offset:12800\n\t"
"v_cvt_pk_bf16_f32 v206, v224, v225\n\t"
"v_mfma_f32_16x16x32_bf16 a[140:143], a[252:255], a[188:191], a[140:143]\n\t"
"v_cvt_pk_bf16_f32 v210, v228, v229\n\t"
"v_cvt_pk_bf16_f32 v207, v230, v231\n\t"
"v_mfma_f32_16x16x32_bf16 a[140:143], a[212:215], a[192:195], a[140:143]\n\t"
"v_accvgpr_read_b32 v224, a58\n\t"
"v_accvgpr_read_b32 v226, a62\n\t"
"v_accvgpr_read_b32 v225, a59\n\t"
"v_accvgpr_read_b32 v227, a63\n\t"
"v_cvt_pk_bf16_f32 v211, v234, v235\n\t"
"v_pk_fma_f32 v[224:225], v[226:227], s[56:57], v[224:225]\n\t"
"v_pk_fma_f32 v[226:227], v[224:225], s[58:59], v[226:227] neg_lo:[0,0,1] neg_hi:[0,0,1]\n\t"
"v_pk_fma_f32 v[228:229], v[42:43], s[60:61], v[224:225]\n\t"
"v_pk_fma_f32 v[42:43], v[224:225], s[60:61], v[42:43]\n\t"
"v_pk_fma_f32 v[224:225], v[142:143], s[62:63], v[226:227]\n\t"
"v_pk_fma_f32 v[142:143], v[226:227], s[62:63], v[142:143]\n\t"
"v_mfma_f32_16x16x32_bf16 a[148:151], v[240:243], a[188:191], a[148:151]\n\t"
"ds_read_b128 a[188:191], v255 offset:15872\n\t"
"v_cvt_pk_bf16_f32 v214, v236, v237\n\t"
"v_mfma_f32_16x16x32_bf16 a[148:151], a[252:255], a[192:195], a[148:151]\n\t"
"v_cvt_pk_bf16_f32 v218, v98, v99\n\t"
"v_accvgpr_read_b32 v230, a64\n\t"
"v_mfma_f32_16x16x32_bf16 a[148:151], a[212:215], a[196:199], a[148:151]\n\t"
"buffer_load_dwordx4 a[56:59], v252, s[8:11], s20 offen offset:2048 sc1\n\t"
"v_accvgpr_read_b32 v232, a68\n\t"
"v_accvgpr_read_b32 v231, a65\n\t"
"v_accvgpr_read_b32 v233, a69\n\t"
"v_pk_fma_f32 v[230:231], v[232:233], s[56:57], v[230:231]\n\t"
"v_pk_fma_f32 v[232:233], v[230:231], s[58:59], v[232:233] neg_lo:[0,0,1] neg_hi:[0,0,1]\n\t"
"v_pk_fma_f32 v[234:235], v[44:45], s[60:61], v[230:231]\n\t"
"v_pk_fma_f32 v[44:45], v[230:231], s[60:61], v[44:45]\n\t"
"v_pk_fma_f32 v[230:231], v[144:145], s[62:63], v[232:233]\n\t"
"v_pk_fma_f32 v[144:145], v[232:233], s[62:63], v[144:145]\n\t"
"v_mfma_f32_16x16x32_bf16 a[156:159], v[240:243], a[192:195], a[156:159]\n\t"
"s_mov_b32 s20, 1376256\n\t"
"ds_read_b128 a[192:195], v255 offset:18944\n\t"
"v_cvt_pk_bf16_f32 v215, v224, v225\n\t"
"v_mfma_f32_16x16x32_bf16 a[156:159], a[252:255], a[196:199], a[156:159]\n\t"
"v_cvt_pk_bf16_f32 v219, v228, v229\n\t"
"v_accvgpr_read_b32 v236, a66\n\t"
"v_mfma_f32_16x16x32_bf16 a[156:159], a[212:215], a[200:203], a[156:159]\n\t"
"buffer_load_dwordx4 a[60:63], v252, s[8:11], s20 offen offset:0 sc1\n\t"
"v_accvgpr_read_b32 v238, a70\n\t"
"v_accvgpr_read_b32 v237, a67\n\t"
"v_accvgpr_read_b32 v239, a71\n\t"
"v_cvt_pk_bf16_f32 v230, v230, v231\n\t"
"v_cvt_pk_bf16_f32 v234, v234, v235\n\t"
"v_pk_fma_f32 v[236:237], v[238:239], s[56:57], v[236:237]\n\t"
"v_pk_fma_f32 v[238:239], v[236:237], s[58:59], v[238:239] neg_lo:[0,0,1] neg_hi:[0,0,1]\n\t"
"v_pk_fma_f32 v[98:99], v[46:47], s[60:61], v[236:237]\n\t"
"v_pk_fma_f32 v[46:47], v[236:237], s[60:61], v[46:47]\n\t"
"v_pk_fma_f32 v[236:237], v[146:147], s[62:63], v[238:239]\n\t"
"v_pk_fma_f32 v[146:147], v[238:239], s[62:63], v[146:147]\n\t"
"v_mfma_f32_16x16x32_bf16 a[164:167], v[240:243], a[196:199], a[164:167]\n\t"
"s_waitcnt vmcnt(11) lgkmcnt(4)\n\t"
"ds_read_b128 a[196:199], v255 offset:22016\n\t"
"v_accvgpr_write_b32 a2, v230\n\t"
"v_mfma_f32_16x16x32_bf16 a[164:167], a[252:255], a[200:203], a[164:167]\n\t"
"ds_read_b128 a[200:203], v255 offset:25088\n\t"
"v_accvgpr_write_b32 a6, v234\n\t"
"v_mfma_f32_16x16x32_bf16 a[164:167], a[212:215], a[204:207], a[164:167]\n\t"
"buffer_load_dwordx4 a[252:255], v252, s[8:11], s20 offen offset:1024 sc1\n\t"
"ds_read_b128 a[204:207], v255 offset:28160\n\t"
"v_accvgpr_read_b32 v224, a72\n\t"
"v_accvgpr_read_b32 v226, a76\n\t"
"v_accvgpr_read_b32 v225, a73\n\t"
"v_accvgpr_read_b32 v227, a77\n\t"
"v_cvt_pk_bf16_f32 v236, v236, v237\n\t"
"v_cvt_pk_bf16_f32 v98, v98, v99\n\t"
"v_pk_fma_f32 v[224:225], v[226:227], s[56:57], v[224:225]\n\t"
"v_pk_fma_f32 v[226:227], v[224:225], s[58:59], v[226:227] neg_lo:[0,0,1] neg_hi:[0,0,1]\n\t"
"v_pk_fma_f32 v[228:229], v[48:49], s[60:61], v[224:225]\n\t"
"v_pk_fma_f32 v[48:49], v[224:225], s[60:61], v[48:49]\n\t"
"v_pk_fma_f32 v[224:225], v[148:149], s[62:63], v[226:227]\n\t"
"v_pk_fma_f32 v[148:149], v[226:227], s[62:63], v[148:149]\n\t"
"Lst32_%=:\n\t"
"v_mfma_f32_16x16x32_bf16 v[0:3], a[220:223], a[168:171], v[0:3]\n\t"
"s_waitcnt lgkmcnt(6)\n\t"
"v_accvgpr_write_b32 a3, v236\n\t"
"v_accvgpr_write_b32 a7, v98\n\t"
"v_mfma_f32_16x16x32_bf16 v[0:3], a[224:227], a[172:175], v[0:3]\n\t"
"v_accvgpr_read_b32 v230, a74\n\t"
"v_accvgpr_read_b32 v232, a78\n\t"
"v_mfma_f32_16x16x32_bf16 v[0:3], a[216:219], a[176:179], v[0:3]\n\t"
"buffer_load_dwordx4 a[212:215], v252, s[8:11], s20 offen offset:2048 sc1\n\t"
"v_accvgpr_read_b32 v231, a75\n\t"
"v_accvgpr_read_b32 v233, a79\n\t"
"v_cvt_pk_bf16_f32 v224, v224, v225\n\t"
"v_cvt_pk_bf16_f32 v228, v228, v229\n\t"
"v_pk_fma_f32 v[230:231], v[232:233], s[56:57], v[230:231]\n\t"
"v_pk_fma_f32 v[232:233], v[230:231], s[58:59], v[232:233] neg_lo:[0,0,1] neg_hi:[0,0,1]\n\t"
"v_pk_fma_f32 v[234:235], v[50:51], s[60:61], v[230:231]\n\t"
"v_pk_fma_f32 v[50:51], v[230:231], s[60:61], v[50:51]\n\t"
"v_pk_fma_f32 v[230:231], v[150:151], s[62:63], v[232:233]\n\t"
"v_pk_fma_f32 v[150:151], v[232:233], s[62:63], v[150:151]\n\t"
"v_mfma_f32_16x16x32_bf16 v[4:7], a[220:223], a[172:175], v[4:7]\n\t"
"s_waitcnt lgkmcnt(5)\n\t"
"s_mov_b32 s20, 1388544\n\t"
"v_accvgpr_write_b32 a10, v224\n\t"
"v_mfma_f32_16x16x32_bf16 v[4:7], a[224:227], a[176:179], v[4:7]\n\t"
"v_accvgpr_write_b32 a14, v228\n\t"
"v_accvgpr_read_b32 v236, a80\n\t"
"v_mfma_f32_16x16x32_bf16 v[4:7], a[216:219], a[180:183], v[4:7]\n\t"
"buffer_load_dwordx4 a[64:67], v252, s[8:11], s20 offen offset:0 sc1\n\t"
"v_accvgpr_read_b32 v238, a84\n\t"
"v_accvgpr_read_b32 v237, a81\n\t"
"v_accvgpr_read_b32 v239, a85\n\t"
"v_cvt_pk_bf16_f32 v230, v230, v231\n\t"
"v_cvt_pk_bf16_f32 v234, v234, v235\n\t"
"v_pk_fma_f32 v[236:237], v[238:239], s[56:57], v[236:237]\n\t"
"v_pk_fma_f32 v[238:239], v[236:237], s[58:59], v[238:239] neg_lo:[0,0,1] neg_hi:[0,0,1]\n\t"
"v_pk_fma_f32 v[98:99], v[52:53], s[60:61], v[236:237]\n\t"
"v_pk_fma_f32 v[52:53], v[236:237], s[60:61], v[52:53]\n\t"
"v_pk_fma_f32 v[236:237], v[152:153], s[62:63], v[238:239]\n\t"
"v_pk_fma_f32 v[152:153], v[238:239], s[62:63], v[152:153]\n\t"
"v_mfma_f32_16x16x32_bf16 v[8:11], a[220:223], a[176:179], v[8:11]\n\t"
"s_waitcnt lgkmcnt(4)\n\t"
"v_accvgpr_write_b32 a11, v230\n\t"
"v_accvgpr_write_b32 a15, v234\n\t"
"v_mfma_f32_16x16x32_bf16 v[8:11], a[224:227], a[180:183], v[8:11]\n\t"
"v_accvgpr_read_b32 v224, a82\n\t"
"v_accvgpr_read_b32 v226, a86\n\t"
"v_mfma_f32_16x16x32_bf16 v[8:11], a[216:219], a[184:187], v[8:11]\n\t"
"buffer_load_dwordx4 a[68:71], v252, s[8:11], s20 offen offset:1024 sc1\n\t"
"v_accvgpr_read_b32 v225, a83\n\t"
"v_accvgpr_read_b32 v227, a87\n\t"
"v_cvt_pk_bf16_f32 v236, v236, v237\n\t"
"v_cvt_pk_bf16_f32 v98, v98, v99\n\t"
"v_pk_fma_f32 v[224:225], v[226:227], s[56:57], v[224:225]\n\t"
"v_pk_fma_f32 v[226:227], v[224:225], s[58:59], v[226:227] neg_lo:[0,0,1] neg_hi:[0,0,1]\n\t"
"v_pk_fma_f32 v[228:229], v[54:55], s[60:61], v[224:225]\n\t"
"v_pk_fma_f32 v[54:55], v[224:225], s[60:61], v[54:55]\n\t"
"v_pk_fma_f32 v[224:225], v[154:155], s[62:63], v[226:227]\n\t"
"v_pk_fma_f32 v[154:155], v[226:227], s[62:63], v[154:155]\n\t"
"v_mfma_f32_16x16x32_bf16 v[12:15], a[220:223], a[180:183], v[12:15]\n\t"
"s_waitcnt lgkmcnt(3)\n\t"
"v_accvgpr_write_b32 a18, v236\n\t"
"v_accvgpr_write_b32 a22, v98\n\t"
"v_mfma_f32_16x16x32_bf16 v[12:15], a[224:227], a[184:187], v[12:15]\n\t"
"v_accvgpr_read_b32 v230, a88\n\t"
"v_accvgpr_read_b32 v232, a92\n\t"
"v_mfma_f32_16x16x32_bf16 v[12:15], a[216:219], a[188:191], v[12:15]\n\t"
"buffer_load_dwordx4 a[72:75], v252, s[8:11], s20 offen offset:2048 sc1\n\t"
"v_accvgpr_read_b32 v231, a89\n\t"
"v_accvgpr_read_b32 v233, a93\n\t"
"v_accvgpr_read_b32 v236, a90\n\t"
"v_accvgpr_read_b32 v238, a94\n\t"
"v_accvgpr_read_b32 v237, a91\n\t"
"v_accvgpr_read_b32 v239, a95\n\t"
"v_cvt_pk_bf16_f32 v224, v224, v225\n\t"
"v_cvt_pk_bf16_f32 v228, v228, v229\n\t"
"v_pk_fma_f32 v[230:231], v[232:233], s[56:57], v[230:231]\n\t"
"v_pk_fma_f32 v[232:233], v[230:231], s[58:59], v[232:233] neg_lo:[0,0,1] neg_hi:[0,0,1]\n\t"
"v_pk_fma_f32 v[234:235], v[56:57], s[60:61], v[230:231]\n\t"
"v_pk_fma_f32 v[56:57], v[230:231], s[60:61], v[56:57]\n\t"
"v_pk_fma_f32 v[230:231], v[156:157], s[62:63], v[232:233]\n\t"
"v_pk_fma_f32 v[156:157], v[232:233], s[62:63], v[156:157]\n\t"
"v_pk_fma_f32 v[236:237], v[238:239], s[56:57], v[236:237]\n\t"
"v_pk_fma_f32 v[238:239], v[236:237], s[58:59], v[238:239] neg_lo:[0,0,1] neg_hi:[0,0,1]\n\t"
"v_pk_fma_f32 v[98:99], v[58:59], s[60:61], v[236:237]\n\t"
"v_pk_fma_f32 v[58:59], v[236:237], s[60:61], v[58:59]\n\t"
"v_pk_fma_f32 v[236:237], v[158:159], s[62:63], v[238:239]\n\t"
"v_pk_fma_f32 v[158:159], v[238:239], s[62:63], v[158:159]\n\t"
"v_mfma_f32_16x16x32_bf16 v[16:19], a[220:223], a[184:187], v[16:19]\n\t"
"s_waitcnt lgkmcnt(2)\n\t"
"s_mov_b32 s20, 589824\n\t"
"v_accvgpr_write_b32 a19, v224\n\t"
"v_mfma_f32_16x16x32_bf16 v[16:19], a[224:227], a[188:191], v[16:19]\n\t"
"v_accvgpr_write_b32 a23, v228\n\t"
"v_accvgpr_read_b32 v224, a96\n\t"
"v_mfma_f32_16x16x32_bf16 v[16:19], a[216:219], a[192:195], v[16:19]\n\t"
"buffer_load_dwordx4 a[76:79], v252, s[8:11], s20 offen offset:0 sc1\n\t"
"v_accvgpr_read_b32 v226, a100\n\t"
"v_accvgpr_read_b32 v225, a97\n\t"
"v_accvgpr_read_b32 v227, a101\n\t"
"v_cvt_pk_bf16_f32 v230, v230, v231\n\t"
"v_cvt_pk_bf16_f32 v234, v234, v235\n\t"
"v_pk_fma_f32 v[224:225], v[226:227], s[56:57], v[224:225]\n\t"
"v_pk_fma_f32 v[226:227], v[224:225], s[58:59], v[226:227] neg_lo:[0,0,1] neg_hi:[0,0,1]\n\t"
"v_pk_fma_f32 v[228:229], v[60:61], s[60:61], v[224:225]\n\t"
"v_pk_fma_f32 v[60:61], v[224:225], s[60:61], v[60:61]\n\t"
"v_pk_fma_f32 v[224:225], v[160:161], s[62:63], v[226:227]\n\t"
"v_pk_fma_f32 v[160:161], v[226:227], s[62:63], v[160:161]\n\t"
"v_mfma_f32_16x16x32_bf16 v[20:23], a[220:223], a[188:191], v[20:23]\n\t"
"s_waitcnt lgkmcnt(1)\n\t"
"v_accvgpr_write_b32 a26, v230\n\t"
"v_cvt_pk_bf16_f32 v236, v236, v237\n\t"
"v_mfma_f32_16x16x32_bf16 v[20:23], a[224:227], a[192:195], v[20:23]\n\t"
"v_accvgpr_write_b32 a30, v234\n\t"
"v_cvt_pk_bf16_f32 v98, v98, v99\n\t"
"v_mfma_f32_16x16x32_bf16 v[20:23], a[216:219], a[196:199], v[20:23]\n\t"
"buffer_load_dwordx4 a[80:83], v252, s[8:11], s20 offen offset:1024 sc1\n\t"
"v_accvgpr_read_b32 v230, a98\n\t"
"v_accvgpr_read_b32 v232, a102\n\t"
"v_accvgpr_read_b32 v231, a99\n\t"
"v_accvgpr_read_b32 v233, a103\n\t"
"v_accvgpr_write_b32 a27, v236\n\t"
"v_accvgpr_write_b32 a31, v98\n\t"
"v_cvt_pk_bf16_f32 v224, v224, v225\n\t"
"v_cvt_pk_bf16_f32 v228, v228, v229\n\t"
"v_pk_fma_f32 v[230:231], v[232:233], s[56:57], v[230:231]\n\t"
"v_pk_fma_f32 v[232:233], v[230:231], s[58:59], v[232:233] neg_lo:[0,0,1] neg_hi:[0,0,1]\n\t"
"v_pk_fma_f32 v[234:235], v[62:63], s[60:61], v[230:231]\n\t"
"v_pk_fma_f32 v[62:63], v[230:231], s[60:61], v[62:63]\n\t"
"v_pk_fma_f32 v[230:231], v[162:163], s[62:63], v[232:233]\n\t"
"v_pk_fma_f32 v[162:163], v[232:233], s[62:63], v[162:163]\n\t"
"v_mfma_f32_16x16x32_bf16 v[24:27], a[220:223], a[192:195], v[24:27]\n\t"
"s_waitcnt lgkmcnt(0)\n\t"
"v_accvgpr_write_b32 a34, v224\n\t"
"v_accvgpr_write_b32 a38, v228\n\t"
"v_mfma_f32_16x16x32_bf16 v[24:27], a[224:227], a[196:199], v[24:27]\n\t"
"v_accvgpr_read_b32 v236, a104\n\t"
"v_accvgpr_read_b32 v238, a108\n\t"
"v_mfma_f32_16x16x32_bf16 v[24:27], a[216:219], a[200:203], v[24:27]\n\t"
"buffer_load_dwordx4 a[84:87], v252, s[8:11], s20 offen offset:2048 sc1\n\t"
"v_accvgpr_read_b32 v237, a105\n\t"
"v_accvgpr_read_b32 v239, a109\n\t"
"v_cvt_pk_bf16_f32 v230, v230, v231\n\t"
"v_cvt_pk_bf16_f32 v234, v234, v235\n\t"
"v_pk_fma_f32 v[236:237], v[238:239], s[56:57], v[236:237]\n\t"
"v_pk_fma_f32 v[238:239], v[236:237], s[58:59], v[238:239] neg_lo:[0,0,1] neg_hi:[0,0,1]\n\t"
"v_pk_fma_f32 v[98:99], v[64:65], s[60:61], v[236:237]\n\t"
"v_pk_fma_f32 v[64:65], v[236:237], s[60:61], v[64:65]\n\t"
"v_pk_fma_f32 v[236:237], v[164:165], s[62:63], v[238:239]\n\t"
"v_pk_fma_f32 v[164:165], v[238:239], s[62:63], v[164:165]\n\t"
"v_mfma_f32_16x16x32_bf16 v[28:31], a[220:223], a[196:199], v[28:31]\n\t"
"s_waitcnt vmcnt(16)\n\t"
"s_mov_b32 s20, 602112\n\t"
"v_accvgpr_write_b32 a35, v230\n\t"
"v_mfma_f32_16x16x32_bf16 v[28:31], a[224:227], a[200:203], v[28:31]\n\t"
"v_accvgpr_write_b32 a39, v234\n\t"
"v_accvgpr_read_b32 v224, a106\n\t"
"v_mfma_f32_16x16x32_bf16 v[28:31], a[216:219], a[204:207], v[28:31]\n\t"
"buffer_load_dwordx4 a[220:223], v252, s[8:11], s20 offen offset:0 sc1\n\t"
"v_accvgpr_read_b32 v226, a110\n\t"
"v_accvgpr_read_b32 v225, a107\n\t"
"v_accvgpr_read_b32 v227, a111\n\t"
"v_cvt_pk_bf16_f32 v236, v236, v237\n\t"
"v_cvt_pk_bf16_f32 v98, v98, v99\n\t"
"v_pk_fma_f32 v[224:225], v[226:227], s[56:57], v[224:225]\n\t"
"v_pk_fma_f32 v[226:227], v[224:225], s[58:59], v[226:227] neg_lo:[0,0,1] neg_hi:[0,0,1]\n\t"
"v_pk_fma_f32 v[228:229], v[66:67], s[60:61], v[224:225]\n\t"
"v_pk_fma_f32 v[66:67], v[224:225], s[60:61], v[66:67]\n\t"
"v_pk_fma_f32 v[224:225], v[166:167], s[62:63], v[226:227]\n\t"
"v_pk_fma_f32 v[166:167], v[226:227], s[62:63], v[166:167]\n\t"
"v_mfma_f32_16x16x32_bf16 v[32:35], a[228:231], a[168:171], v[32:35]\n\t"
"v_accvgpr_write_b32 a104, v236\n\t"
"v_accvgpr_write_b32 a106, v98\n\t"
"v_mfma_f32_16x16x32_bf16 v[32:35], a[232:235], a[172:175], v[32:35]\n\t"
"v_accvgpr_read_b32 v230, a112\n\t"
"v_accvgpr_read_b32 v232, a116\n\t"
"v_mfma_f32_16x16x32_bf16 v[32:35], a[208:211], a[176:179], v[32:35]\n\t"
"buffer_load_dwordx4 a[216:219], v252, s[8:11], s20 offen offset:1024 sc1\n\t"
"v_accvgpr_read_b32 v231, a113\n\t"
"v_accvgpr_read_b32 v233, a117\n\t"
"v_cvt_pk_bf16_f32 v224, v224, v225\n\t"
"v_cvt_pk_bf16_f32 v228, v228, v229\n\t"
"v_pk_fma_f32 v[230:231], v[232:233], s[56:57], v[230:231]\n\t"
"v_pk_fma_f32 v[232:233], v[230:231], s[58:59], v[232:233] neg_lo:[0,0,1] neg_hi:[0,0,1]\n\t"
"v_pk_fma_f32 v[234:235], v[68:69], s[60:61], v[230:231]\n\t"
"v_pk_fma_f32 v[68:69], v[230:231], s[60:61], v[68:69]\n\t"
"v_pk_fma_f32 v[230:231], v[168:169], s[62:63], v[232:233]\n\t"
"v_pk_fma_f32 v[168:169], v[232:233], s[62:63], v[168:169]\n\t"
"v_mfma_f32_16x16x32_bf16 v[36:39], a[228:231], a[172:175], v[36:39]\n\t"
"v_accvgpr_write_b32 a105, v224\n\t"
"v_accvgpr_write_b32 a107, v228\n\t"
"v_mfma_f32_16x16x32_bf16 v[36:39], a[232:235], a[176:179], v[36:39]\n\t"
"v_accvgpr_read_b32 v236, a114\n\t"
"v_accvgpr_read_b32 v238, a118\n\t"
"v_mfma_f32_16x16x32_bf16 v[36:39], a[208:211], a[180:183], v[36:39]\n\t"
"buffer_load_dwordx4 a[224:227], v252, s[8:11], s20 offen offset:2048 sc1\n\t"
"v_accvgpr_read_b32 v237, a115\n\t"
"v_accvgpr_read_b32 v239, a119\n\t"
"v_cvt_pk_bf16_f32 v230, v230, v231\n\t"
"v_cvt_pk_bf16_f32 v234, v234, v235\n\t"
"v_pk_fma_f32 v[236:237], v[238:239], s[56:57], v[236:237]\n\t"
"v_pk_fma_f32 v[238:239], v[236:237], s[58:59], v[238:239] neg_lo:[0,0,1] neg_hi:[0,0,1]\n\t"
"v_pk_fma_f32 v[98:99], v[70:71], s[60:61], v[236:237]\n\t"
"v_pk_fma_f32 v[70:71], v[236:237], s[60:61], v[70:71]\n\t"
"v_pk_fma_f32 v[236:237], v[170:171], s[62:63], v[238:239]\n\t"
"v_pk_fma_f32 v[170:171], v[238:239], s[62:63], v[170:171]\n\t"
"v_mfma_f32_16x16x32_bf16 v[40:43], a[228:231], a[176:179], v[40:43]\n\t"
"s_mov_b32 s20, 614400\n\t"
"v_accvgpr_write_b32 a112, v230\n\t"
"v_accvgpr_write_b32 a114, v234\n\t"
"v_mfma_f32_16x16x32_bf16 v[40:43], a[232:235], a[180:183], v[40:43]\n\t"
"v_accvgpr_read_b32 v224, a120\n\t"
"v_accvgpr_read_b32 v226, a124\n\t"
"v_mfma_f32_16x16x32_bf16 v[40:43], a[208:211], a[184:187], v[40:43]\n\t"
"buffer_load_dwordx4 a[88:91], v252, s[8:11], s20 offen offset:0 sc1\n\t"
"v_accvgpr_read_b32 v225, a121\n\t"
"v_accvgpr_read_b32 v227, a125\n\t"
"v_cvt_pk_bf16_f32 v236, v236, v237\n\t"
"v_cvt_pk_bf16_f32 v98, v98, v99\n\t"
"v_pk_fma_f32 v[224:225], v[226:227], s[56:57], v[224:225]\n\t"
"v_pk_fma_f32 v[226:227], v[224:225], s[58:59], v[226:227] neg_lo:[0,0,1] neg_hi:[0,0,1]\n\t"
"v_pk_fma_f32 v[228:229], v[72:73], s[60:61], v[224:225]\n\t"
"v_pk_fma_f32 v[72:73], v[224:225], s[60:61], v[72:73]\n\t"
"v_pk_fma_f32 v[224:225], v[172:173], s[62:63], v[226:227]\n\t"
"v_pk_fma_f32 v[172:173], v[226:227], s[62:63], v[172:173]\n\t"
"v_mfma_f32_16x16x32_bf16 v[44:47], a[228:231], a[180:183], v[44:47]\n\t"
"v_accvgpr_write_b32 a113, v236\n\t"
"v_accvgpr_write_b32 a115, v98\n\t"
"v_mfma_f32_16x16x32_bf16 v[44:47], a[232:235], a[184:187], v[44:47]\n\t"
"v_accvgpr_read_b32 v230, a122\n\t"
"v_accvgpr_read_b32 v232, a126\n\t"
"v_mfma_f32_16x16x32_bf16 v[44:47], a[208:211], a[188:191], v[44:47]\n\t"
"buffer_load_dwordx4 a[92:95], v252, s[8:11], s20 offen offset:1024 sc1\n\t"
"v_accvgpr_read_b32 v231, a123\n\t"
"v_accvgpr_read_b32 v233, a127\n\t"
"v_accvgpr_read_b32 v236, a128\n\t"
"v_accvgpr_read_b32 v238, a132\n\t"
"v_accvgpr_read_b32 v237, a129\n\t"
"v_accvgpr_read_b32 v239, a133\n\t"
"v_cvt_pk_bf16_f32 v224, v224, v225\n\t"
"v_cvt_pk_bf16_f32 v228, v228, v229\n\t"
"v_pk_fma_f32 v[230:231], v[232:233], s[56:57], v[230:231]\n\t"
"v_pk_fma_f32 v[232:233], v[230:231], s[58:59], v[232:233] neg_lo:[0,0,1] neg_hi:[0,0,1]\n\t"
"v_pk_fma_f32 v[234:235], v[74:75], s[60:61], v[230:231]\n\t"
"v_pk_fma_f32 v[74:75], v[230:231], s[60:61], v[74:75]\n\t"
"v_pk_fma_f32 v[230:231], v[174:175], s[62:63], v[232:233]\n\t"
"v_pk_fma_f32 v[174:175], v[232:233], s[62:63], v[174:175]\n\t"
"v_pk_fma_f32 v[236:237], v[238:239], s[56:57], v[236:237]\n\t"
"v_pk_fma_f32 v[238:239], v[236:237], s[58:59], v[238:239] neg_lo:[0,0,1] neg_hi:[0,0,1]\n\t"
"v_pk_fma_f32 v[98:99], v[76:77], s[60:61], v[236:237]\n\t"
"v_pk_fma_f32 v[76:77], v[236:237], s[60:61], v[76:77]\n\t"
"v_pk_fma_f32 v[236:237], v[176:177], s[62:63], v[238:239]\n\t"
"v_pk_fma_f32 v[176:177], v[238:239], s[62:63], v[176:177]\n\t"
"v_mfma_f32_16x16x32_bf16 v[48:51], a[228:231], a[184:187], v[48:51]\n\t"
"v_accvgpr_write_b32 a120, v224\n\t"
"v_accvgpr_write_b32 a122, v228\n\t"
"v_mfma_f32_16x16x32_bf16 v[48:51], a[232:235], a[188:191], v[48:51]\n\t"
"v_accvgpr_read_b32 v224, a130\n\t"
"v_accvgpr_read_b32 v226, a134\n\t"
"v_mfma_f32_16x16x32_bf16 v[48:51], a[208:211], a[192:195], v[48:51]\n\t"
"Lpg24_%=:\n\t"
"s_cbranch_vccnz Lvr3_%=\n\t"
"buffer_load_dwordx4 a[96:99], v252, s[8:11], s20 offen offset:2048 sc1\n\t"
"v_accvgpr_read_b32 v225, a131\n\t"
"v_accvgpr_read_b32 v227, a135\n\t"
"v_cvt_pk_bf16_f32 v230, v230, v231\n\t"
"v_cvt_pk_bf16_f32 v234, v234, v235\n\t"
"v_cvt_pk_bf16_f32 v236, v236, v237\n\t"
"v_cvt_pk_bf16_f32 v98, v98, v99\n\t"
"v_pk_fma_f32 v[224:225], v[226:227], s[56:57], v[224:225]\n\t"
"v_pk_fma_f32 v[226:227], v[224:225], s[58:59], v[226:227] neg_lo:[0,0,1] neg_hi:[0,0,1]\n\t"
"v_pk_fma_f32 v[228:229], v[78:79], s[60:61], v[224:225]\n\t"
"v_pk_fma_f32 v[78:79], v[224:225], s[60:61], v[78:79]\n\t"
"v_pk_fma_f32 v[224:225], v[178:179], s[62:63], v[226:227]\n\t"
"v_pk_fma_f32 v[178:179], v[226:227], s[62:63], v[178:179]\n\t"
"v_mfma_f32_16x16x32_bf16 v[52:55], a[228:231], a[188:191], v[52:55]\n\t"
"s_mov_b32 s20, 774144\n\t"
"v_accvgpr_write_b32 a121, v230\n\t"
"v_accvgpr_write_b32 a123, v234\n\t"
"v_mfma_f32_16x16x32_bf16 v[52:55], a[232:235], a[192:195], v[52:55]\n\t"
"v_accvgpr_write_b32 a128, v236\n\t"
"v_accvgpr_write_b32 a130, v98\n\t"
"v_mfma_f32_16x16x32_bf16 v[52:55], a[208:211], a[196:199], v[52:55]\n\t"
"s_waitcnt vmcnt(17)\n\t"
"ds_write_b128 v249, v[196:199] offset:18432\n\t"
"v_accvgpr_read_b32 v230, a136\n\t"
"v_accvgpr_read_b32 v232, a140\n\t"
"v_accvgpr_read_b32 v231, a137\n\t"
"v_accvgpr_read_b32 v233, a141\n\t"
"v_cvt_pk_bf16_f32 v224, v224, v225\n\t"
"v_cvt_pk_bf16_f32 v228, v228, v229\n\t"
"v_pk_fma_f32 v[230:231], v[232:233], s[56:57], v[230:231]\n\t"
"v_pk_fma_f32 v[232:233], v[230:231], s[58:59], v[232:233] neg_lo:[0,0,1] neg_hi:[0,0,1]\n\t"
"v_pk_fma_f32 v[234:235], v[80:81], s[60:61], v[230:231]\n\t"
"v_pk_fma_f32 v[80:81], v[230:231], s[60:61], v[80:81]\n\t"
"v_pk_fma_f32 v[230:231], v[180:181], s[62:63], v[232:233]\n\t"
"v_pk_fma_f32 v[180:181], v[232:233], s[62:63], v[180:181]\n\t"
"v_mfma_f32_16x16x32_bf16 v[56:59], a[228:231], a[192:195], v[56:59]\n\t"
"v_accvgpr_write_b32 a129, v224\n\t"
"v_accvgpr_write_b32 a131, v228\n\t"
"v_mfma_f32_16x16x32_bf16 v[56:59], a[232:235], a[196:199], v[56:59]\n\t"
"v_accvgpr_read_b32 v236, a138\n\t"
"v_accvgpr_read_b32 v238, a142\n\t"
"v_mfma_f32_16x16x32_bf16 v[56:59], a[208:211], a[200:203], v[56:59]\n\t"
"buffer_load_dwordx4 a[100:103], v252, s[8:11], s20 offen offset:0 sc1\n\t"
"v_accvgpr_read_b32 v237, a139\n\t"
"v_accvgpr_read_b32 v239, a143\n\t"
"v_cvt_pk_bf16_f32 v230, v230, v231\n\t"
"v_cvt_pk_bf16_f32 v234, v234, v235\n\t"
"v_pk_fma_f32 v[236:237], v[238:239], s[56:57], v[236:237]\n\t"
"v_pk_fma_f32 v[238:239], v[236:237], s[58:59], v[238:239] neg_lo:[0,0,1] neg_hi:[0,0,1]\n\t"
"v_pk_fma_f32 v[98:99], v[82:83], s[60:61], v[236:237]\n\t"
"v_pk_fma_f32 v[82:83], v[236:237], s[60:61], v[82:83]\n\t"
"v_pk_fma_f32 v[236:237], v[182:183], s[62:63], v[238:239]\n\t"
"v_pk_fma_f32 v[182:183], v[238:239], s[62:63], v[182:183]\n\t"
"v_mfma_f32_16x16x32_bf16 v[60:63], a[228:231], a[196:199], v[60:63]\n\t"
"v_accvgpr_write_b32 a136, v230\n\t"
"v_accvgpr_write_b32 a138, v234\n\t"
"v_mfma_f32_16x16x32_bf16 v[60:63], a[232:235], a[200:203], v[60:63]\n\t"
"v_accvgpr_read_b32 v224, a144\n\t"
"v_accvgpr_read_b32 v226, a148\n\t"
"v_mfma_f32_16x16x32_bf16 v[60:63], a[208:211], a[204:207], v[60:63]\n\t"
"buffer_load_dwordx4 a[108:111], v252, s[8:11], s20 offen offset:1024 sc1\n\t"
"v_accvgpr_read_b32 v225, a145\n\t"
"v_accvgpr_read_b32 v227, a149\n\t"
"v_cvt_pk_bf16_f32 v236, v236, v237\n\t"
"v_cvt_pk_bf16_f32 v98, v98, v99\n\t"
"v_pk_fma_f32 v[224:225], v[226:227], s[56:57], v[224:225]\n\t"
"v_pk_fma_f32 v[226:227], v[224:225], s[58:59], v[226:227] neg_lo:[0,0,1] neg_hi:[0,0,1]\n\t"
"v_pk_fma_f32 v[228:229], v[84:85], s[60:61], v[224:225]\n\t"
"v_pk_fma_f32 v[84:85], v[224:225], s[60:61], v[84:85]\n\t"
"v_pk_fma_f32 v[224:225], v[184:185], s[62:63], v[226:227]\n\t"
"v_pk_fma_f32 v[184:185], v[226:227], s[62:63], v[184:185]\n\t"
"v_mfma_f32_16x16x32_bf16 v[64:67], a[236:239], a[168:171], v[64:67]\n\t"
"ds_read_b128 a[168:171], v255 offset:2048\n\t"
"v_accvgpr_write_b32 a137, v236\n\t"
"v_mfma_f32_16x16x32_bf16 v[64:67], a[240:243], a[172:175], v[64:67]\n\t"
"v_accvgpr_write_b32 a139, v98\n\t"
"v_accvgpr_read_b32 v230, a146\n\t"
"v_mfma_f32_16x16x32_bf16 v[64:67], a[244:247], a[176:179], v[64:67]\n\t"
"ds_write_b64 v250, a[104:105] offset:18432\n\t"
"v_accvgpr_read_b32 v232, a150\n\t"
"v_accvgpr_read_b32 v231, a147\n\t"
"v_accvgpr_read_b32 v233, a151\n\t"
"v_cvt_pk_bf16_f32 v224, v224, v225\n\t"
"v_cvt_pk_bf16_f32 v228, v228, v229\n\t"
"v_pk_fma_f32 v[230:231], v[232:233], s[56:57], v[230:231]\n\t"
"v_pk_fma_f32 v[232:233], v[230:231], s[58:59], v[232:233] neg_lo:[0,0,1] neg_hi:[0,0,1]\n\t"
"v_pk_fma_f32 v[234:235], v[86:87], s[60:61], v[230:231]\n\t"
"v_pk_fma_f32 v[86:87], v[230:231], s[60:61], v[86:87]\n\t"
"v_pk_fma_f32 v[230:231], v[186:187], s[62:63], v[232:233]\n\t"
"v_pk_fma_f32 v[186:187], v[232:233], s[62:63], v[186:187]\n\t"
"v_mfma_f32_16x16x32_bf16 v[68:71], a[236:239], a[172:175], v[68:71]\n\t"
"ds_read_b128 a[172:175], v255 offset:5120\n\t"
"v_accvgpr_write_b32 a144, v224\n\t"
"v_mfma_f32_16x16x32_bf16 v[68:71], a[240:243], a[176:179], v[68:71]\n\t"
"v_accvgpr_write_b32 a146, v228\n\t"
"v_accvgpr_read_b32 v236, a152\n\t"
"v_mfma_f32_16x16x32_bf16 v[68:71], a[244:247], a[180:183], v[68:71]\n\t"
"buffer_load_dwordx4 a[228:231], v252, s[8:11], s20 offen offset:2048 sc1\n\t"
"v_accvgpr_read_b32 v238, a156\n\t"
"v_accvgpr_read_b32 v237, a153\n\t"
"v_accvgpr_read_b32 v239, a157\n\t"
"v_cvt_pk_bf16_f32 v230, v230, v231\n\t"
"v_cvt_pk_bf16_f32 v234, v234, v235\n\t"
"s_mov_b32 s20, 786432\n\t"
"v_pk_fma_f32 v[236:237], v[238:239], s[56:57], v[236:237]\n\t"
"v_pk_fma_f32 v[238:239], v[236:237], s[58:59], v[238:239] neg_lo:[0,0,1] neg_hi:[0,0,1]\n\t"
"v_pk_fma_f32 v[98:99], v[88:89], s[60:61], v[236:237]\n\t"
"v_pk_fma_f32 v[88:89], v[236:237], s[60:61], v[88:89]\n\t"
"v_pk_fma_f32 v[236:237], v[188:189], s[62:63], v[238:239]\n\t"
"v_pk_fma_f32 v[188:189], v[238:239], s[62:63], v[188:189]\n\t"
"v_mfma_f32_16x16x32_bf16 v[72:75], a[236:239], a[176:179], v[72:75]\n\t"
"ds_read_b128 a[176:179], v255 offset:8192\n\t"
"v_accvgpr_write_b32 a145, v230\n\t"
"v_mfma_f32_16x16x32_bf16 v[72:75], a[240:243], a[180:183], v[72:75]\n\t"
"v_accvgpr_write_b32 a147, v234\n\t"
"v_accvgpr_read_b32 v224, a154\n\t"
"v_mfma_f32_16x16x32_bf16 v[72:75], a[244:247], a[184:187], v[72:75]\n\t"
"buffer_load_dwordx4 a[208:211], v252, s[8:11], s20 offen offset:0 sc1\n\t"
"v_accvgpr_read_b32 v226, a158\n\t"
"v_accvgpr_read_b32 v225, a155\n\t"
"v_accvgpr_read_b32 v227, a159\n\t"
"v_cvt_pk_bf16_f32 v236, v236, v237\n\t"
"v_cvt_pk_bf16_f32 v98, v98, v99\n\t"
"v_pk_fma_f32 v[224:225], v[226:227], s[56:57], v[224:225]\n\t"
"v_pk_fma_f32 v[226:227], v[224:225], s[58:59], v[226:227] neg_lo:[0,0,1] neg_hi:[0,0,1]\n\t"
"v_pk_fma_f32 v[228:229], v[90:91], s[60:61], v[224:225]\n\t"
"v_pk_fma_f32 v[90:91], v[224:225], s[60:61], v[90:91]\n\t"
"v_pk_fma_f32 v[224:225], v[190:191], s[62:63], v[226:227]\n\t"
"v_pk_fma_f32 v[190:191], v[226:227], s[62:63], v[190:191]\n\t"
"v_mfma_f32_16x16x32_bf16 v[76:79], a[236:239], a[180:183], v[76:79]\n\t"
"ds_read_b128 a[180:183], v255 offset:11264\n\t"
"v_accvgpr_write_b32 a152, v236\n\t"
"v_mfma_f32_16x16x32_bf16 v[76:79], a[240:243], a[184:187], v[76:79]\n\t"
"v_accvgpr_write_b32 a154, v98\n\t"
"v_accvgpr_read_b32 v230, a160\n\t"
"v_mfma_f32_16x16x32_bf16 v[76:79], a[244:247], a[188:191], v[76:79]\n\t"
"buffer_load_dwordx4 a[232:235], v252, s[8:11], s20 offen offset:1024 sc1\n\t"
"v_accvgpr_read_b32 v232, a164\n\t"
"v_accvgpr_read_b32 v231, a161\n\t"
"v_accvgpr_read_b32 v233, a165\n\t"
"v_cvt_pk_bf16_f32 v224, v224, v225\n\t"
"v_cvt_pk_bf16_f32 v228, v228, v229\n\t"
"v_pk_fma_f32 v[230:231], v[232:233], s[56:57], v[230:231]\n\t"
"v_pk_fma_f32 v[232:233], v[230:231], s[58:59], v[232:233] neg_lo:[0,0,1] neg_hi:[0,0,1]\n\t"
"v_pk_fma_f32 v[234:235], v[92:93], s[60:61], v[230:231]\n\t"
"v_pk_fma_f32 v[92:93], v[230:231], s[60:61], v[92:93]\n\t"
"v_pk_fma_f32 v[230:231], v[192:193], s[62:63], v[232:233]\n\t"
"v_pk_fma_f32 v[192:193], v[232:233], s[62:63], v[192:193]\n\t"
"v_mfma_f32_16x16x32_bf16 v[80:83], a[236:239], a[184:187], v[80:83]\n\t"
"ds_read_b128 a[184:187], v255 offset:14336\n\t"
"v_accvgpr_write_b32 a153, v224\n\t"
"v_mfma_f32_16x16x32_bf16 v[80:83], a[240:243], a[188:191], v[80:83]\n\t"
"v_accvgpr_write_b32 a155, v228\n\t"
"v_accvgpr_read_b32 v236, a162\n\t"
"v_mfma_f32_16x16x32_bf16 v[80:83], a[244:247], a[192:195], v[80:83]\n\t"
"buffer_load_dwordx4 a[116:119], v252, s[8:11], s20 offen offset:2048 sc1\n\t"
"v_accvgpr_read_b32 v238, a166\n\t"
"v_accvgpr_read_b32 v237, a163\n\t"
"v_accvgpr_read_b32 v239, a167\n\t"
"v_cvt_pk_bf16_f32 v230, v230, v231\n\t"
"v_cvt_pk_bf16_f32 v234, v234, v235\n\t"
"s_mov_b32 s20, 798720\n\t"
"v_pk_fma_f32 v[236:237], v[238:239], s[56:57], v[236:237]\n\t"
"v_pk_fma_f32 v[238:239], v[236:237], s[58:59], v[238:239] neg_lo:[0,0,1] neg_hi:[0,0,1]\n\t"
"v_pk_fma_f32 v[98:99], v[94:95], s[60:61], v[236:237]\n\t"
"v_pk_fma_f32 v[94:95], v[236:237], s[60:61], v[94:95]\n\t"
"v_pk_fma_f32 v[236:237], v[194:195], s[62:63], v[238:239]\n\t"
"v_pk_fma_f32 v[194:195], v[238:239], s[62:63], v[194:195]\n\t"
"v_mfma_f32_16x16x32_bf16 v[84:87], a[236:239], a[188:191], v[84:87]\n\t"
"ds_read_b128 a[188:191], v255 offset:17408\n\t"
"v_accvgpr_write_b32 a160, v230\n\t"
"v_mfma_f32_16x16x32_bf16 v[84:87], a[240:243], a[192:195], v[84:87]\n\t"
"v_accvgpr_write_b32 a162, v234\n\t"
"v_cvt_pk_bf16_f32 v236, v236, v237\n\t"
"v_mfma_f32_16x16x32_bf16 v[84:87], a[244:247], a[196:199], v[84:87]\n\t"
"buffer_load_dwordx4 a[124:127], v252, s[8:11], s20 offen offset:0 sc1\n\t"
"v_cvt_pk_bf16_f32 v98, v98, v99\n\t"
"v_mfma_f32_16x16x32_bf16 v[88:91], a[236:239], a[192:195], v[88:91]\n\t"
"ds_read_b128 a[192:195], v255 offset:20480\n\t"
"v_accvgpr_write_b32 a161, v236\n\t"
"v_mfma_f32_16x16x32_bf16 v[88:91], a[240:243], a[196:199], v[88:91]\n\t"
"v_accvgpr_write_b32 a163, v98\n\t"
"v_mfma_f32_16x16x32_bf16 v[88:91], a[244:247], a[200:203], v[88:91]\n\t"
"ds_write_b128 v249, v[200:203] offset:24576\n\t"
"buffer_load_dwordx4 a[132:135], v252, s[8:11], s20 offen offset:1024 sc1\n\t"
"v_mfma_f32_16x16x32_bf16 v[92:95], a[236:239], a[196:199], v[92:95]\n\t"
"buffer_load_dwordx4 a[140:143], v252, s[8:11], s20 offen offset:2048 sc1\n\t"
"v_mfma_f32_16x16x32_bf16 v[92:95], a[240:243], a[200:203], v[92:95]\n\t"
"s_waitcnt vmcnt(24) lgkmcnt(5)\n\t"
"ds_read_b128 a[196:199], v255 offset:23552\n\t"
"s_mov_b32 s20, 884736\n\t"
"v_mfma_f32_16x16x32_bf16 v[92:95], a[244:247], a[204:207], v[92:95]\n\t"
"ds_write_b64 v250, a[106:107] offset:24576\n\t"
"v_mfma_f32_16x16x32_bf16 v[100:103], a[248:251], a[168:171], v[100:103]\n\t"
"buffer_load_dwordx4 a[148:151], v252, s[8:11], s20 offen offset:0 sc1\n\t"
"v_mfma_f32_16x16x32_bf16 v[100:103], a[52:55], a[172:175], v[100:103]\n\t"
"s_waitcnt lgkmcnt(6)\n\t"
"ds_read_b128 a[200:203], v255 offset:26624\n\t"
"v_mfma_f32_16x16x32_bf16 v[100:103], a[56:59], a[176:179], v[100:103]\n\t"
"ds_read_b128 a[204:207], v255 offset:29696\n\t"
"v_mfma_f32_16x16x32_bf16 v[104:107], a[248:251], a[172:175], v[104:107]\n\t"
"buffer_load_dwordx4 a[236:239], v252, s[8:11], s20 offen offset:1024 sc1\n\t"
"v_mfma_f32_16x16x32_bf16 v[104:107], a[52:55], a[176:179], v[104:107]\n\t"
"s_waitcnt lgkmcnt(7)\n\t"
"v_mfma_f32_16x16x32_bf16 v[104:107], a[56:59], a[180:183], v[104:107]\n\t"
"s_waitcnt lgkmcnt(0)\n\t"
"s_barrier\n\t"
"v_mfma_f32_16x16x32_bf16 v[108:111], a[248:251], a[176:179], v[108:111]\n\t"
"ds_read_b128 a[40:43], v252 offset:18432\n\t"
"v_mfma_f32_16x16x32_bf16 v[108:111], a[52:55], a[180:183], v[108:111]\n\t"
"buffer_load_dwordx4 a[240:243], v252, s[8:11], s20 offen offset:2048 sc1\n\t"
"v_mfma_f32_16x16x32_bf16 v[108:111], a[56:59], a[184:187], v[108:111]\n\t"
"s_mov_b32 s20, 1474560\n\t"
"ds_read_b128 a[44:47], v252 offset:19456\n\t"
"v_add_u32 v224, 98304, v249\n\t"
"v_mfma_f32_16x16x32_bf16 v[112:115], a[248:251], a[180:183], v[112:115]\n\t"
"s_add_u32 s29, s22, 384\n\t"
"ds_read_b128 a[48:51], v252 offset:20480\n\t"
"v_add_u32 v225, 98304, v250\n\t"
"v_mfma_f32_16x16x32_bf16 v[112:115], a[52:55], a[184:187], v[112:115]\n\t"
"v_add_u32 v226, 98304, v252\n\t"
"v_mfma_f32_16x16x32_bf16 v[112:115], a[56:59], a[188:191], v[112:115]\n\t"
"buffer_load_dwordx4 a[244:247], v252, s[8:11], s20 offen offset:0 sc1\n\t"
"v_mfma_f32_16x16x32_bf16 v[116:119], a[248:251], a[184:187], v[116:119]\n\t"
"buffer_load_dwordx4 a[104:107], v252, s[8:11], s20 offen offset:1024 sc1\n\t"
"v_mfma_f32_16x16x32_bf16 v[116:119], a[52:55], a[188:191], v[116:119]\n\t"
"buffer_load_dwordx4 a[156:159], v252, s[8:11], s20 offen offset:2048 sc1\n\t"
"v_mfma_f32_16x16x32_bf16 v[116:119], a[56:59], a[192:195], v[116:119]\n\t"
"s_mov_b32 s20, 897024\n\t"
"ds_write_b128 v249, v[204:207] offset:30720\n\t"
"s_waitcnt lgkmcnt(3)\n\t"
"v_mfma_f32_16x16x32_bf16 v[120:123], a[248:251], a[188:191], v[120:123]\n\t"
"buffer_load_dwordx4 a[164:167], v252, s[8:11], s20 offen offset:0 sc1\n\t"
"v_mfma_f32_16x16x32_bf16 v[120:123], a[52:55], a[192:195], v[120:123]\n\t"
"buffer_load_dwordx4 v[240:243], v252, s[8:11], s20 offen offset:1024 sc1\n\t"
"v_mfma_f32_16x16x32_bf16 v[120:123], a[56:59], a[196:199], v[120:123]\n\t"
"s_waitcnt lgkmcnt(2)\n\t"
"ds_write_b128 v224, v[212:215] offset:0\n\t"
"v_mfma_f32_16x16x32_bf16 v[124:127], a[248:251], a[192:195], v[124:127]\n\t"
"s_waitcnt lgkmcnt(2)\n\t"
"v_mfma_f32_16x16x32_bf16 v[124:127], a[52:55], a[196:199], v[124:127]\n\t"
"buffer_load_dwordx4 v[96:99], v252, s[8:11], s20 offen offset:2048 sc1\n\t"
"v_mfma_f32_16x16x32_bf16 v[124:127], a[56:59], a[200:203], v[124:127]\n\t"
"s_mov_b32 s20, 1486848\n\t"
"ds_write_b128 v224, a[0:3] offset:12288\n\t"
"v_mfma_f32_16x16x32_bf16 v[128:131], a[248:251], a[196:199], v[128:131]\n\t"
"s_waitcnt vmcnt(30)\n\t"
"v_mfma_f32_16x16x32_bf16 v[128:131], a[52:55], a[200:203], v[128:131]\n\t"
"buffer_load_dwordx4 a[248:251], v252, s[8:11], s20 offen offset:0 sc1\n\t"
"v_mfma_f32_16x16x32_bf16 v[128:131], a[56:59], a[204:207], v[128:131]\n\t"
"ds_write_b128 v224, a[8:11] offset:24576\n\t"
"v_mfma_f32_16x16x32_bf16 v[132:135], a[60:63], a[168:171], v[132:135]\n\t"
"v_mfma_f32_16x16x32_bf16 v[132:135], a[252:255], a[172:175], v[132:135]\n\t"
"buffer_load_dwordx4 a[52:55], v252, s[8:11], s20 offen offset:1024 sc1\n\t"
"v_mfma_f32_16x16x32_bf16 v[132:135], a[212:215], a[176:179], v[132:135]\n\t"
"ds_write_b128 v224, a[16:19] offset:36864\n\t"
"ds_write_b128 v224, a[24:27] offset:49152\n\t"
"buffer_store_dwordx4 a[40:43], v248, s[16:19], s29 offen offset:0 nt\n\t"
"v_mfma_f32_16x16x32_bf16 v[136:139], a[60:63], a[172:175], v[136:139]\n\t"
"v_mfma_f32_16x16x32_bf16 v[136:139], a[252:255], a[176:179], v[136:139]\n\t"
"buffer_load_dwordx4 a[56:59], v252, s[8:11], s20 offen offset:2048 sc1\n\t"
"v_mfma_f32_16x16x32_bf16 v[136:139], a[212:215], a[180:183], v[136:139]\n\t"
"buffer_store_dwordx4 a[44:47], v248, s[16:19], s29 offen offset:128 nt\n\t"
"ds_write_b64 v250, a[112:113] offset:30720\n\t"
"s_mov_b32 s20, 909312\n\t"
"v_mfma_f32_16x16x32_bf16 v[140:143], a[60:63], a[176:179], v[140:143]\n\t"
"v_mfma_f32_16x16x32_bf16 v[140:143], a[252:255], a[180:183], v[140:143]\n\t"
"v_mfma_f32_16x16x32_bf16 v[140:143], a[212:215], a[184:187], v[140:143]\n\t"
"buffer_store_dwordx4 a[48:51], v248, s[16:19], s29 offen offset:256 nt\n\t"
"ds_write_b64 v225, a[120:121] offset:0\n\t"
"v_mfma_f32_16x16x32_bf16 v[144:147], a[60:63], a[180:183], v[144:147]\n\t"
"v_mfma_f32_16x16x32_bf16 v[144:147], a[252:255], a[184:187], v[144:147]\n\t"
"buffer_load_dwordx4 a[48:51], v252, s[8:11], s20 offen offset:0 sc1\n\t"
"v_mfma_f32_16x16x32_bf16 v[144:147], a[212:215], a[188:191], v[144:147]\n\t"
"ds_write_b64 v225, a[128:129] offset:12288\n\t"
"ds_write_b64 v225, a[136:137] offset:24576\n\t"
"ds_write_b64 v225, a[144:145] offset:36864\n\t"
"ds_write_b64 v225, a[152:153] offset:49152\n\t"
"v_mfma_f32_16x16x32_bf16 v[148:151], a[60:63], a[184:187], v[148:151]\n\t"
"buffer_load_dwordx4 v[236:239], v252, s[8:11], s20 offen offset:1024 sc1\n\t"
"v_mfma_f32_16x16x32_bf16 v[148:151], a[252:255], a[188:191], v[148:151]\n\t"
"v_mfma_f32_16x16x32_bf16 v[148:151], a[212:215], a[192:195], v[148:151]\n\t"
"ds_write_b128 v249, v[208:211] offset:36864\n\t"
"v_mfma_f32_16x16x32_bf16 v[152:155], a[60:63], a[188:191], v[152:155]\n\t"
"v_mfma_f32_16x16x32_bf16 v[152:155], a[252:255], a[192:195], v[152:155]\n\t"
"v_mfma_f32_16x16x32_bf16 v[152:155], a[212:215], a[196:199], v[152:155]\n\t"
"ds_write_b128 v224, v[216:219] offset:6144\n\t"
"v_mfma_f32_16x16x32_bf16 v[156:159], a[60:63], a[192:195], v[156:159]\n\t"
"v_mfma_f32_16x16x32_bf16 v[156:159], a[252:255], a[196:199], v[156:159]\n\t"
"v_mfma_f32_16x16x32_bf16 v[156:159], a[212:215], a[200:203], v[156:159]\n\t"
"ds_write_b128 v224, a[4:7] offset:18432\n\t"
"v_mfma_f32_16x16x32_bf16 v[160:163], a[60:63], a[196:199], v[160:163]\n\t"
"buffer_load_dwordx4 a[60:63], v252, s[8:11], s20 offen offset:2048 sc1\n\t"
"v_mfma_f32_16x16x32_bf16 v[160:163], a[252:255], a[200:203], v[160:163]\n\t"
"s_waitcnt vmcnt(36)\n\t"
"s_mov_b32 s20, 1499136\n\t"
"v_mfma_f32_16x16x32_bf16 v[160:163], a[212:215], a[204:207], v[160:163]\n\t"
"ds_write_b128 v224, a[12:15] offset:30720\n\t"
"ds_write_b128 v224, a[20:23] offset:43008\n\t"
"ds_write_b128 v224, a[28:31] offset:55296\n\t"
"v_mfma_f32_16x16x32_bf16 v[164:167], a[64:67], a[168:171], v[164:167]\n\t"
"buffer_load_dwordx4 a[212:215], v252, s[8:11], s20 offen offset:0 sc1\n\t"
"v_mfma_f32_16x16x32_bf16 v[164:167], a[68:71], a[172:175], v[164:167]\n\t"
"ds_read_b128 a[168:171], v244 offset:0\n\t"
"v_mfma_f32_16x16x32_bf16 v[164:167], a[72:75], a[176:179], v[164:167]\n\t"
"ds_write_b64 v250, a[114:115] offset:36864\n\t"
"v_mfma_f32_16x16x32_bf16 v[168:171], a[64:67], a[172:175], v[168:171]\n\t"
"buffer_load_dwordx4 a[252:255], v252, s[8:11], s20 offen offset:1024 sc1\n\t"
"v_mfma_f32_16x16x32_bf16 v[168:171], a[68:71], a[176:179], v[168:171]\n\t"
"ds_read_b128 a[172:175], v244 offset:512\n\t"
"v_mfma_f32_16x16x32_bf16 v[168:171], a[72:75], a[180:183], v[168:171]\n\t"
"ds_write_b64 v225, a[122:123] offset:6144\n\t"
"v_mfma_f32_16x16x32_bf16 v[172:175], a[64:67], a[176:179], v[172:175]\n\t"
"ds_read_b128 a[176:179], v244 offset:1024\n\t"
"v_mfma_f32_16x16x32_bf16 v[172:175], a[68:71], a[180:183], v[172:175]\n\t"
"v_mfma_f32_16x16x32_bf16 v[172:175], a[72:75], a[184:187], v[172:175]\n\t"
"ds_write_b64 v225, a[130:131] offset:18432\n\t"
"ds_write_b64 v225, a[138:139] offset:30720\n\t"
"ds_write_b64 v225, a[146:147] offset:43008\n\t"
"ds_write_b64 v225, a[154:155] offset:55296\n\t"
"v_mfma_f32_16x16x32_bf16 v[176:179], a[64:67], a[180:183], v[176:179]\n\t"
"ds_read_b128 a[180:183], v244 offset:1536\n\t"
"v_mfma_f32_16x16x32_bf16 v[176:179], a[68:71], a[184:187], v[176:179]\n\t"
"v_mfma_f32_16x16x32_bf16 v[176:179], a[72:75], a[188:191], v[176:179]\n\t"
"s_waitcnt lgkmcnt(1)\n\t"
"s_barrier\n\t"
"v_mfma_f32_16x16x32_bf16 v[180:183], a[64:67], a[184:187], v[180:183]\n\t"
"ds_read_b128 v[196:199], v252 offset:30720\n\t"
"v_mfma_f32_16x16x32_bf16 v[180:183], a[68:71], a[188:191], v[180:183]\n\t"
"ds_read_b128 v[200:203], v252 offset:31744\n\t"
"v_mfma_f32_16x16x32_bf16 v[180:183], a[72:75], a[192:195], v[180:183]\n\t"
"ds_read_b128 a[184:187], v244 offset:2048\n\t"
"v_mfma_f32_16x16x32_bf16 v[184:187], a[64:67], a[188:191], v[184:187]\n\t"
"buffer_load_dwordx4 a[28:31], v252, s[8:11], s20 offen offset:2048 sc1\n\t"
"v_mfma_f32_16x16x32_bf16 v[184:187], a[68:71], a[192:195], v[184:187]\n\t"
"s_mov_b32 s20, 1069056\n\t"
"ds_read_b128 a[40:43], v252 offset:32768\n\t"
"s_add_u32 s29, s22, 196992\n\t"
"v_mfma_f32_16x16x32_bf16 v[184:187], a[72:75], a[196:199], v[184:187]\n\t"
"ds_read_b128 a[188:191], v244 offset:2560\n\t"
"v_mfma_f32_16x16x32_bf16 v[188:191], a[64:67], a[192:195], v[188:191]\n\t"
"buffer_load_dwordx4 a[112:115], v252, s[8:11], s20 offen offset:0 sc1\n\t"
"v_mfma_f32_16x16x32_bf16 v[188:191], a[68:71], a[196:199], v[188:191]\n\t"
"s_waitcnt lgkmcnt(4)\n\t"
"ds_read_b128 a[44:47], v226 offset:0\n\t"
"v_mfma_f32_16x16x32_bf16 v[188:191], a[72:75], a[200:203], v[188:191]\n\t"
"ds_read_b128 a[192:195], v244 offset:3072\n\t"
"v_mfma_f32_16x16x32_bf16 v[192:195], a[64:67], a[196:199], v[192:195]\n\t"
"buffer_load_dwordx4 a[120:123], v252, s[8:11], s20 offen offset:1024 sc1\n\t"
"v_mfma_f32_16x16x32_bf16 v[192:195], a[68:71], a[200:203], v[192:195]\n\t"
"s_waitcnt vmcnt(38)\n\t"
"ds_read_b128 v[204:207], v226 offset:1024\n\t"
"s_waitcnt lgkmcnt(6)\n\t"
"v_mfma_f32_16x16x32_bf16 v[192:195], a[72:75], a[204:207], v[192:195]\n\t"
"ds_write_b128 v249, a[32:35] offset:18432\n\t"
"Lst33_%=:\n\t"
"v_mfma_f32_16x16x32_bf16 v[0:3], a[76:79], a[168:171], v[0:3]\n\t"
"buffer_load_dwordx4 a[64:67], v252, s[8:11], s20 offen offset:2048 sc1\n\t"
"v_mfma_f32_16x16x32_bf16 v[0:3], a[80:83], a[172:175], v[0:3]\n\t"
"s_mov_b32 s20, 1658880\n\t"
"ds_read_b128 a[196:199], v244 offset:3584\n\t"
"v_mfma_f32_16x16x32_bf16 v[0:3], a[84:87], a[176:179], v[0:3]\n\t"
"buffer_store_dwordx4 v[196:199], v248, s[16:19], s29 offen offset:0 nt\n\t"
"ds_read_b128 v[208:211], v226 offset:2048\n\t"
"ds_read_b128 a[200:203], v244 offset:4096\n\t"
"s_waitcnt lgkmcnt(8)\n\t"
"v_mfma_f32_16x16x32_bf16 v[4:7], a[76:79], a[172:175], v[4:7]\n\t"
"s_waitcnt lgkmcnt(6)\n\t"
"ds_read_b128 v[212:215], v226 offset:12288\n\t"
"v_mfma_f32_16x16x32_bf16 v[4:7], a[80:83], a[176:179], v[4:7]\n\t"
"buffer_load_dwordx4 a[68:71], v252, s[8:11], s20 offen offset:0 sc1\n\t"
"v_mfma_f32_16x16x32_bf16 v[4:7], a[84:87], a[180:183], v[4:7]\n\t"
"buffer_store_dwordx4 v[200:203], v248, s[16:19], s29 offen offset:128 nt\n\t"
"ds_read_b128 a[204:207], v244 offset:4608\n\t"
"buffer_load_dwordx4 a[72:75], v252, s[8:11], s20 offen offset:1024 sc1\n\t"
"v_mfma_f32_16x16x32_bf16 v[8:11], a[76:79], a[176:179], v[8:11]\n\t"
"s_waitcnt lgkmcnt(6)\n\t"
"ds_read_b128 v[216:219], v226 offset:13312\n\t"
"v_mfma_f32_16x16x32_bf16 v[8:11], a[80:83], a[180:183], v[8:11]\n\t"
"v_mfma_f32_16x16x32_bf16 v[8:11], a[84:87], a[184:187], v[8:11]\n\t"
"ds_write_b64 v250, a[160:161] offset:18432\n\t"
"v_mfma_f32_16x16x32_bf16 v[12:15], a[76:79], a[180:183], v[12:15]\n\t"
"s_waitcnt lgkmcnt(5)\n\t"
"ds_read_b128 v[196:199], v226 offset:14336\n\t"
"v_mfma_f32_16x16x32_bf16 v[12:15], a[80:83], a[184:187], v[12:15]\n\t"
"v_mfma_f32_16x16x32_bf16 v[12:15], a[84:87], a[188:191], v[12:15]\n\t"
"buffer_store_dwordx4 a[40:43], v248, s[16:19], s29 offen offset:256 nt\n\t"
"ds_write_b64 v250, a[162:163] offset:24576\n\t"
"s_add_u32 s29, s22, 393600\n\t"
"v_mfma_f32_16x16x32_bf16 v[16:19], a[76:79], a[184:187], v[16:19]\n\t"
"s_waitcnt lgkmcnt(5)\n\t"
"v_mfma_f32_16x16x32_bf16 v[16:19], a[80:83], a[188:191], v[16:19]\n\t"
"v_mfma_f32_16x16x32_bf16 v[16:19], a[84:87], a[192:195], v[16:19]\n\t"
"buffer_store_dwordx4 a[44:47], v248, s[16:19], s29 offen offset:0 nt\n\t"
"ds_read_b128 v[200:203], v226 offset:24576\n\t"
"ds_read_b128 a[4:7], v226 offset:36864\n\t"
"v_mfma_f32_16x16x32_bf16 v[20:23], a[76:79], a[188:191], v[20:23]\n\t"
"s_waitcnt lgkmcnt(5)\n\t"
"ds_read_b128 a[16:19], v226 offset:49152\n\t"
"v_mfma_f32_16x16x32_bf16 v[20:23], a[80:83], a[192:195], v[20:23]\n\t"
"ds_read_b128 a[0:3], v226 offset:25600\n\t"
"v_mfma_f32_16x16x32_bf16 v[20:23], a[84:87], a[196:199], v[20:23]\n\t"
"buffer_store_dwordx4 v[204:207], v248, s[16:19], s29 offen offset:128 nt\n\t"
"ds_read_b128 a[8:11], v226 offset:37888\n\t"
"ds_read_b128 a[20:23], v226 offset:50176\n\t"
"v_mfma_f32_16x16x32_bf16 v[24:27], a[76:79], a[192:195], v[24:27]\n\t"
"ds_read_b128 v[204:207], v226 offset:26624\n\t"
"v_mfma_f32_16x16x32_bf16 v[24:27], a[80:83], a[196:199], v[24:27]\n\t"
"ds_read_b128 a[12:15], v226 offset:38912\n\t"
"v_mfma_f32_16x16x32_bf16 v[24:27], a[84:87], a[200:203], v[24:27]\n\t"
"buffer_store_dwordx4 v[208:211], v248, s[16:19], s29 offen offset:256 nt\n\t"
"ds_read_b128 a[24:27], v226 offset:51200\n\t"
"s_add_u32 s29, s22, 590208\n\t"
"v_mfma_f32_16x16x32_bf16 v[28:31], a[76:79], a[196:199], v[28:31]\n\t"
"s_waitcnt vmcnt(44)\n\t"
"v_mfma_f32_16x16x32_bf16 v[28:31], a[80:83], a[200:203], v[28:31]\n\t"
"v_mfma_f32_16x16x32_bf16 v[28:31], a[84:87], a[204:207], v[28:31]\n\t"
"ds_write_b128 v249, a[36:39] offset:24576\n\t"
"v_mfma_f32_16x16x32_bf16 v[32:35], a[220:223], a[168:171], v[32:35]\n\t"
"s_waitcnt lgkmcnt(11)\n\t"
"v_mfma_f32_16x16x32_bf16 v[32:35], a[216:219], a[172:175], v[32:35]\n\t"
"buffer_load_dwordx4 a[40:43], v252, s[8:11], s20 offen offset:2048 sc1\n\t"
"v_mfma_f32_16x16x32_bf16 v[32:35], a[224:227], a[176:179], v[32:35]\n\t"
"buffer_store_dwordx4 v[212:215], v248, s[16:19], s29 offen offset:0 nt\n\t"
"v_mfma_f32_16x16x32_bf16 v[36:39], a[220:223], a[172:175], v[36:39]\n\t"
"s_mov_b32 s20, 1081344\n\t"
"v_mfma_f32_16x16x32_bf16 v[36:39], a[216:219], a[176:179], v[36:39]\n\t"
"v_mfma_f32_16x16x32_bf16 v[36:39], a[224:227], a[180:183], v[36:39]\n\t"
"buffer_store_dwordx4 v[216:219], v248, s[16:19], s29 offen offset:128 nt\n\t"
"v_mfma_f32_16x16x32_bf16 v[40:43], a[220:223], a[176:179], v[40:43]\n\t"
"v_mfma_f32_16x16x32_bf16 v[40:43], a[216:219], a[180:183], v[40:43]\n\t"
"v_mfma_f32_16x16x32_bf16 v[40:43], a[224:227], a[184:187], v[40:43]\n\t"
"buffer_store_dwordx4 v[196:199], v248, s[16:19], s29 offen offset:256 nt\n\t"
"s_add_u32 s29, s22, 786816\n\t"
"v_mfma_f32_16x16x32_bf16 v[44:47], a[220:223], a[180:183], v[44:47]\n\t"
"s_waitcnt lgkmcnt(9)\n\t"
"v_mfma_f32_16x16x32_bf16 v[44:47], a[216:219], a[184:187], v[44:47]\n\t"
"v_mfma_f32_16x16x32_bf16 v[44:47], a[224:227], a[188:191], v[44:47]\n\t"
"s_waitcnt lgkmcnt(0)\n\t"
"s_barrier\n\t"
"v_mfma_f32_16x16x32_bf16 v[48:51], a[220:223], a[184:187], v[48:51]\n\t"
"v_mfma_f32_16x16x32_bf16 v[48:51], a[216:219], a[188:191], v[48:51]\n\t"
"buffer_load_dwordx4 a[44:47], v252, s[8:11], s20 offen offset:0 sc1\n\t"
"v_mfma_f32_16x16x32_bf16 v[48:51], a[224:227], a[192:195], v[48:51]\n\t"
"buffer_load_dwordx4 a[32:35], v252, s[8:11], s20 offen offset:1024 sc1\n\t"
"v_mfma_f32_16x16x32_bf16 v[52:55], a[220:223], a[188:191], v[52:55]\n\t"
"v_mfma_f32_16x16x32_bf16 v[52:55], a[216:219], a[192:195], v[52:55]\n\t"
"v_mfma_f32_16x16x32_bf16 v[52:55], a[224:227], a[196:199], v[52:55]\n\t"
"buffer_store_dwordx4 v[200:203], v248, s[16:19], s29 offen offset:0 nt\n\t"
"v_mfma_f32_16x16x32_bf16 v[56:59], a[220:223], a[192:195], v[56:59]\n\t"
"v_mfma_f32_16x16x32_bf16 v[56:59], a[216:219], a[196:199], v[56:59]\n\t"
"buffer_load_dwordx4 a[36:39], v252, s[8:11], s20 offen offset:2048 sc1\n\t"
"v_mfma_f32_16x16x32_bf16 v[56:59], a[224:227], a[200:203], v[56:59]\n\t"
"s_mov_b32 s20, 1671168\n\t"
"v_mfma_f32_16x16x32_bf16 v[60:63], a[220:223], a[196:199], v[60:63]\n\t"
"s_waitcnt vmcnt(49)\n\t"
"v_mfma_f32_16x16x32_bf16 v[60:63], a[216:219], a[200:203], v[60:63]\n\t"
"v_mfma_f32_16x16x32_bf16 v[60:63], a[224:227], a[204:207], v[60:63]\n\t"
"buffer_store_dwordx4 a[0:3], v248, s[16:19], s29 offen offset:128 nt\n\t"
"v_mfma_f32_16x16x32_bf16 v[64:67], a[88:91], a[168:171], v[64:67]\n\t"
"ds_read_b128 a[168:171], v244 offset:9216\n\t"
"v_mfma_f32_16x16x32_bf16 v[64:67], a[92:95], a[172:175], v[64:67]\n\t"
"v_mfma_f32_16x16x32_bf16 v[64:67], a[96:99], a[176:179], v[64:67]\n\t"
"buffer_load_dwordx4 a[216:219], v252, s[8:11], s20 offen offset:0 sc1\n\t"
"v_mfma_f32_16x16x32_bf16 v[68:71], a[88:91], a[172:175], v[68:71]\n\t"
"ds_read_b128 a[172:175], v244 offset:9728\n\t"
"v_mfma_f32_16x16x32_bf16 v[68:71], a[92:95], a[176:179], v[68:71]\n\t"
"v_mfma_f32_16x16x32_bf16 v[68:71], a[96:99], a[180:183], v[68:71]\n\t"
"v_mfma_f32_16x16x32_bf16 v[72:75], a[88:91], a[176:179], v[72:75]\n\t"
"ds_read_b128 a[176:179], v244 offset:10240\n\t"
"v_mfma_f32_16x16x32_bf16 v[72:75], a[92:95], a[180:183], v[72:75]\n\t"
"v_mfma_f32_16x16x32_bf16 v[72:75], a[96:99], a[184:187], v[72:75]\n\t"
"buffer_store_dwordx4 v[204:207], v248, s[16:19], s29 offen offset:256 nt\n\t"
"ds_read_b128 v[196:199], v252 offset:18432\n\t"
"v_mfma_f32_16x16x32_bf16 v[76:79], a[88:91], a[180:183], v[76:79]\n\t"
"s_add_u32 s29, s22, 983424\n\t"
"ds_read_b128 a[180:183], v244 offset:10752\n\t"
"v_mfma_f32_16x16x32_bf16 v[76:79], a[92:95], a[184:187], v[76:79]\n\t"
"buffer_load_dwordx4 a[220:223], v252, s[8:11], s20 offen offset:1024 sc1\n\t"
"v_mfma_f32_16x16x32_bf16 v[76:79], a[96:99], a[188:191], v[76:79]\n\t"
"ds_read_b128 v[200:203], v252 offset:19456\n\t"
"v_mfma_f32_16x16x32_bf16 v[80:83], a[88:91], a[184:187], v[80:83]\n\t"
"ds_read_b128 a[184:187], v244 offset:11264\n\t"
"v_mfma_f32_16x16x32_bf16 v[80:83], a[92:95], a[188:191], v[80:83]\n\t"
"buffer_load_dwordx4 a[224:227], v252, s[8:11], s20 offen offset:2048 sc1\n\t"
"v_mfma_f32_16x16x32_bf16 v[80:83], a[96:99], a[192:195], v[80:83]\n\t"
"ds_read_b128 v[208:211], v252 offset:20480\n\t"
"v_mfma_f32_16x16x32_bf16 v[84:87], a[88:91], a[188:191], v[84:87]\n\t"
"s_mov_b32 s20, 1093632\n\t"
"ds_read_b128 a[188:191], v244 offset:11776\n\t"
"v_mfma_f32_16x16x32_bf16 v[84:87], a[92:95], a[192:195], v[84:87]\n\t"
"v_mfma_f32_16x16x32_bf16 v[84:87], a[96:99], a[196:199], v[84:87]\n\t"
"buffer_store_dwordx4 a[4:7], v248, s[16:19], s29 offen offset:0 nt\n\t"
"v_mfma_f32_16x16x32_bf16 v[88:91], a[88:91], a[192:195], v[88:91]\n\t"
"ds_read_b128 a[192:195], v244 offset:12288\n\t"
"v_mfma_f32_16x16x32_bf16 v[88:91], a[92:95], a[196:199], v[88:91]\n\t"
"v_mfma_f32_16x16x32_bf16 v[88:91], a[96:99], a[200:203], v[88:91]\n\t"
"v_mfma_f32_16x16x32_bf16 v[92:95], a[88:91], a[196:199], v[92:95]\n\t"
"buffer_load_dwordx4 a[76:79], v252, s[8:11], s20 offen offset:0 sc1\n\t"
"v_mfma_f32_16x16x32_bf16 v[92:95], a[92:95], a[200:203], v[92:95]\n\t"
"s_waitcnt vmcnt(53) lgkmcnt(7)\n\t"
"ds_read_b128 a[196:199], v244 offset:12800\n\t"
"v_mfma_f32_16x16x32_bf16 v[92:95], a[96:99], a[204:207], v[92:95]\n\t"
"ds_read_b128 a[200:203], v244 offset:13312\n\t"
"v_mfma_f32_16x16x32_bf16 v[100:103], a[100:103], a[168:171], v[100:103]\n\t"
"s_waitcnt lgkmcnt(7)\n\t"
"ds_read_b128 a[204:207], v244 offset:13824\n\t"
"v_mfma_f32_16x16x32_bf16 v[100:103], a[108:111], a[172:175], v[100:103]\n\t"
"v_mfma_f32_16x16x32_bf16 v[100:103], a[228:231], a[176:179], v[100:103]\n\t"
"buffer_store_dwordx4 a[8:11], v248, s[16:19], s29 offen offset:128 nt\n\t"
"v_mfma_f32_16x16x32_bf16 v[104:107], a[100:103], a[172:175], v[104:107]\n\t"
"s_waitcnt lgkmcnt(6)\n\t"
"v_mfma_f32_16x16x32_bf16 v[104:107], a[108:111], a[176:179], v[104:107]\n\t"
"v_mfma_f32_16x16x32_bf16 v[104:107], a[228:231], a[180:183], v[104:107]\n\t"
"s_waitcnt lgkmcnt(0)\n\t"
"s_barrier\n\t"
"v_mfma_f32_16x16x32_bf16 v[108:111], a[100:103], a[176:179], v[108:111]\n\t"
"v_mfma_f32_16x16x32_bf16 v[108:111], a[108:111], a[180:183], v[108:111]\n\t"
"buffer_load_dwordx4 a[4:7], v252, s[8:11], s20 offen offset:1024 sc1\n\t"
"v_mfma_f32_16x16x32_bf16 v[108:111], a[228:231], a[184:187], v[108:111]\n\t"
"buffer_load_dwordx4 a[80:83], v252, s[8:11], s20 offen offset:2048 sc1\n\t"
"v_mfma_f32_16x16x32_bf16 v[112:115], a[100:103], a[180:183], v[112:115]\n\t"
"s_mov_b32 s20, 1683456\n\t"
"v_mfma_f32_16x16x32_bf16 v[112:115], a[108:111], a[184:187], v[112:115]\n\t"
"v_mfma_f32_16x16x32_bf16 v[112:115], a[228:231], a[188:191], v[112:115]\n\t"
"buffer_store_dwordx4 a[12:15], v248, s[16:19], s29 offen offset:256 nt\n\t"
"v_mfma_f32_16x16x32_bf16 v[116:119], a[100:103], a[184:187], v[116:119]\n\t"
"Lpg25_%=:\n\t"
"s_cbranch_vccnz Lvr4_%=\n\t"
"s_add_u32 s29, s22, 1180032\n\t"
"v_mfma_f32_16x16x32_bf16 v[116:119], a[108:111], a[188:191], v[116:119]\n\t"
"v_mfma_f32_16x16x32_bf16 v[116:119], a[228:231], a[192:195], v[116:119]\n\t"
"buffer_load_dwordx4 a[8:11], v252, s[8:11], s20 offen offset:0 sc1\n\t"
"v_mfma_f32_16x16x32_bf16 v[120:123], a[100:103], a[188:191], v[120:123]\n\t"
"v_mfma_f32_16x16x32_bf16 v[120:123], a[108:111], a[192:195], v[120:123]\n\t"
"v_mfma_f32_16x16x32_bf16 v[120:123], a[228:231], a[196:199], v[120:123]\n\t"
"v_mfma_f32_16x16x32_bf16 v[124:127], a[100:103], a[192:195], v[124:127]\n\t"
"v_mfma_f32_16x16x32_bf16 v[124:127], a[108:111], a[196:199], v[124:127]\n\t"
"v_mfma_f32_16x16x32_bf16 v[124:127], a[228:231], a[200:203], v[124:127]\n\t"
"buffer_store_dwordx4 a[16:19], v248, s[16:19], s29 offen offset:0 nt\n\t"
"v_mfma_f32_16x16x32_bf16 v[128:131], a[100:103], a[196:199], v[128:131]\n\t"
"s_waitcnt vmcnt(56)\n\t"
"v_mfma_f32_16x16x32_bf16 v[128:131], a[108:111], a[200:203], v[128:131]\n\t"
"buffer_load_dwordx4 a[12:15], v252, s[8:11], s20 offen offset:1024 sc1\n\t"
"v_mfma_f32_16x16x32_bf16 v[128:131], a[228:231], a[204:207], v[128:131]\n\t"
"v_mfma_f32_16x16x32_bf16 v[132:135], a[208:211], a[168:171], v[132:135]\n\t"
"v_mfma_f32_16x16x32_bf16 v[132:135], a[232:235], a[172:175], v[132:135]\n\t"
"buffer_load_dwordx4 a[228:231], v252, s[8:11], s20 offen offset:2048 sc1\n\t"
"v_mfma_f32_16x16x32_bf16 v[132:135], a[116:119], a[176:179], v[132:135]\n\t"
"v_mfma_f32_16x16x32_bf16 v[136:139], a[208:211], a[172:175], v[136:139]\n\t"
"v_mfma_f32_16x16x32_bf16 v[136:139], a[232:235], a[176:179], v[136:139]\n\t"
"v_subrev_u32 v255, 55296, v255\n\t"
"v_mfma_f32_16x16x32_bf16 v[136:139], a[116:119], a[180:183], v[136:139]\n\t"
"buffer_store_dwordx4 a[20:23], v248, s[16:19], s29 offen offset:128 nt\n\t"
"v_mfma_f32_16x16x32_bf16 v[140:143], a[208:211], a[176:179], v[140:143]\n\t"
"v_mfma_f32_16x16x32_bf16 v[140:143], a[232:235], a[180:183], v[140:143]\n\t"
"v_mfma_f32_16x16x32_bf16 v[140:143], a[116:119], a[184:187], v[140:143]\n\t"
"v_mfma_f32_16x16x32_bf16 v[144:147], a[208:211], a[180:183], v[144:147]\n\t"
"v_mfma_f32_16x16x32_bf16 v[144:147], a[232:235], a[184:187], v[144:147]\n\t"
"v_mfma_f32_16x16x32_bf16 v[144:147], a[116:119], a[188:191], v[144:147]\n\t"
"v_mfma_f32_16x16x32_bf16 v[148:151], a[208:211], a[184:187], v[148:151]\n\t"
"v_mfma_f32_16x16x32_bf16 v[148:151], a[232:235], a[188:191], v[148:151]\n\t"
"v_mfma_f32_16x16x32_bf16 v[148:151], a[116:119], a[192:195], v[148:151]\n\t"
"buffer_store_dwordx4 a[24:27], v248, s[16:19], s29 offen offset:256 nt\n\t"
"v_mfma_f32_16x16x32_bf16 v[152:155], a[208:211], a[188:191], v[152:155]\n\t"
"s_add_u32 s29, s22, 1376640\n\t"
"v_mfma_f32_16x16x32_bf16 v[152:155], a[232:235], a[192:195], v[152:155]\n\t"
"v_mfma_f32_16x16x32_bf16 v[152:155], a[116:119], a[196:199], v[152:155]\n\t"
"v_mfma_f32_16x16x32_bf16 v[156:159], a[208:211], a[192:195], v[156:159]\n\t"
"v_mfma_f32_16x16x32_bf16 v[156:159], a[232:235], a[196:199], v[156:159]\n\t"
"v_mfma_f32_16x16x32_bf16 v[156:159], a[116:119], a[200:203], v[156:159]\n\t"
"v_mfma_f32_16x16x32_bf16 v[160:163], a[208:211], a[196:199], v[160:163]\n\t"
"s_waitcnt vmcnt(57)\n\t"
"v_mfma_f32_16x16x32_bf16 v[160:163], a[232:235], a[200:203], v[160:163]\n\t"
"v_mfma_f32_16x16x32_bf16 v[160:163], a[116:119], a[204:207], v[160:163]\n\t"
"buffer_store_dwordx4 v[196:199], v248, s[16:19], s29 offen offset:0 nt\n\t"
"v_mfma_f32_16x16x32_bf16 v[164:167], a[124:127], a[168:171], v[164:167]\n\t"
"ds_read_b128 a[168:171], v255 offset:512\n\t"
"v_mfma_f32_16x16x32_bf16 v[164:167], a[132:135], a[172:175], v[164:167]\n\t"
"ds_read_b128 v[204:207], v255 offset:3584\n\t"
"v_mfma_f32_16x16x32_bf16 v[164:167], a[140:143], a[176:179], v[164:167]\n\t"
"ds_read_b128 v[212:215], v255 offset:6656\n\t"
"v_mfma_f32_16x16x32_bf16 v[168:171], a[124:127], a[172:175], v[168:171]\n\t"
"ds_read_b128 a[172:175], v255 offset:1024\n\t"
"v_mfma_f32_16x16x32_bf16 v[168:171], a[132:135], a[176:179], v[168:171]\n\t"
"ds_read_b128 v[216:219], v255 offset:4096\n\t"
"v_mfma_f32_16x16x32_bf16 v[168:171], a[140:143], a[180:183], v[168:171]\n\t"
"ds_read_b128 a[0:3], v255 offset:7168\n\t"
"v_mfma_f32_16x16x32_bf16 v[172:175], a[124:127], a[176:179], v[172:175]\n\t"
"v_mfma_f32_16x16x32_bf16 v[172:175], a[132:135], a[180:183], v[172:175]\n\t"
"v_mfma_f32_16x16x32_bf16 v[172:175], a[140:143], a[184:187], v[172:175]\n\t"
"buffer_store_dwordx4 v[200:203], v248, s[16:19], s29 offen offset:128 nt\n\t"
"v_mfma_f32_16x16x32_bf16 v[176:179], a[124:127], a[180:183], v[176:179]\n\t"
"v_mfma_f32_16x16x32_bf16 v[176:179], a[132:135], a[184:187], v[176:179]\n\t"
"v_mfma_f32_16x16x32_bf16 v[176:179], a[140:143], a[188:191], v[176:179]\n\t"
"s_barrier\n\t"
"v_mfma_f32_16x16x32_bf16 v[180:183], a[124:127], a[184:187], v[180:183]\n\t"
"v_mfma_f32_16x16x32_bf16 v[180:183], a[132:135], a[188:191], v[180:183]\n\t"
"v_mfma_f32_16x16x32_bf16 v[180:183], a[140:143], a[192:195], v[180:183]\n\t"
"v_mfma_f32_16x16x32_bf16 v[184:187], a[124:127], a[188:191], v[184:187]\n\t"
"v_mfma_f32_16x16x32_bf16 v[184:187], a[132:135], a[192:195], v[184:187]\n\t"
"v_mfma_f32_16x16x32_bf16 v[184:187], a[140:143], a[196:199], v[184:187]\n\t"
"buffer_store_dwordx4 v[208:211], v248, s[16:19], s29 offen offset:256 nt\n\t"
"v_mfma_f32_16x16x32_bf16 v[188:191], a[124:127], a[192:195], v[188:191]\n\t"
"ds_read_b128 a[176:179], v255 offset:2048\n\t"
"v_mfma_f32_16x16x32_bf16 v[188:191], a[132:135], a[196:199], v[188:191]\n\t"
"ds_read_b128 a[180:183], v255 offset:5120\n\t"
"v_mfma_f32_16x16x32_bf16 v[188:191], a[140:143], a[200:203], v[188:191]\n\t"
"ds_read_b128 a[184:187], v255 offset:8192\n\t"
"v_mfma_f32_16x16x32_bf16 v[192:195], a[124:127], a[196:199], v[192:195]\n\t"
"s_waitcnt vmcnt(57) lgkmcnt(6)\n\t"
"ds_read_b128 a[188:191], v255 offset:2560\n\t"
"v_mfma_f32_16x16x32_bf16 v[192:195], a[132:135], a[200:203], v[192:195]\n\t"
"ds_read_b128 a[192:195], v255 offset:5632\n\t"
"v_mfma_f32_16x16x32_bf16 v[192:195], a[140:143], a[204:207], v[192:195]\n\t"
"ds_read_b128 a[196:199], v255 offset:8704\n\t"
"Lst34_%=:\n\t"
"v_mfma_f32_16x16x32_bf16 v[0:3], a[148:151], a[168:171], v[0:3]\n\t"
"s_waitcnt vmcnt(54) lgkmcnt(6)\n\t"
"v_mfma_f32_16x16x32_bf16 v[0:3], a[236:239], v[204:207], v[0:3]\n\t"
"v_mfma_f32_16x16x32_bf16 v[0:3], a[240:243], v[212:215], v[0:3]\n\t"
"v_mfma_f32_16x16x32_bf16 v[0:3], a[244:247], a[172:175], v[0:3]\n\t"
"s_waitcnt vmcnt(51)\n\t"
"v_mfma_f32_16x16x32_bf16 v[0:3], a[104:107], v[216:219], v[0:3]\n\t"
"v_mfma_f32_16x16x32_bf16 v[0:3], a[156:159], a[0:3], v[0:3]\n\t"
"v_mfma_f32_16x16x32_bf16 v[32:35], a[164:167], a[168:171], v[32:35]\n\t"
"s_waitcnt vmcnt(47)\n\t"
"v_mfma_f32_16x16x32_bf16 v[32:35], v[240:243], v[204:207], v[32:35]\n\t"
"v_mfma_f32_16x16x32_bf16 v[32:35], v[96:99], v[212:215], v[32:35]\n\t"
"v_mfma_f32_16x16x32_bf16 v[32:35], a[248:251], a[172:175], v[32:35]\n\t"
"s_waitcnt vmcnt(42)\n\t"
"v_cvt_pk_bf16_f32 v0, v0, v1\n\t"
"v_cvt_pk_bf16_f32 v1, v2, v3\n\t"
"v_mfma_f32_16x16x32_bf16 v[32:35], a[52:55], v[216:219], v[32:35]\n\t"
"v_mfma_f32_16x16x32_bf16 v[32:35], a[56:59], a[0:3], v[32:35]\n\t"
"v_mfma_f32_16x16x32_bf16 v[64:67], a[48:51], a[168:171], v[64:67]\n\t"
"s_waitcnt vmcnt(39)\n\t"
"ds_read_b128 a[168:171], v255 offset:9728\n\t"
"v_mfma_f32_16x16x32_bf16 v[64:67], v[236:239], v[204:207], v[64:67]\n\t"
"v_mfma_f32_16x16x32_bf16 v[64:67], a[60:63], v[212:215], v[64:67]\n\t"
"s_barrier\n\t"
"v_mfma_f32_16x16x32_bf16 v[64:67], a[212:215], a[172:175], v[64:67]\n\t"
"s_waitcnt vmcnt(36) lgkmcnt(4)\n\t"
"ds_read_b128 a[172:175], v255 offset:10240\n\t"
"v_mfma_f32_16x16x32_bf16 v[64:67], a[252:255], v[216:219], v[64:67]\n\t"
"v_cvt_pk_bf16_f32 v2, v32, v33\n\t"
"v_cvt_pk_bf16_f32 v3, v34, v35\n\t"
"v_mfma_f32_16x16x32_bf16 v[64:67], a[28:31], a[0:3], v[64:67]\n\t"
"ds_write_b128 v249, v[0:3] offset:18432\n\t"
"v_mfma_f32_16x16x32_bf16 v[100:103], a[112:115], a[176:179], v[100:103]\n\t"
"s_waitcnt vmcnt(27) lgkmcnt(3)\n\t"
"v_mfma_f32_16x16x32_bf16 v[100:103], a[120:123], a[180:183], v[100:103]\n\t"
"v_mfma_f32_16x16x32_bf16 v[100:103], a[64:67], a[184:187], v[100:103]\n\t"
"v_mfma_f32_16x16x32_bf16 v[100:103], a[68:71], a[188:191], v[100:103]\n\t"
"s_waitcnt vmcnt(20)\n\t"
"v_cvt_pk_bf16_f32 v64, v64, v65\n\t"
"v_cvt_pk_bf16_f32 v65, v66, v67\n\t"
"v_mfma_f32_16x16x32_bf16 v[100:103], a[72:75], a[192:195], v[100:103]\n\t"
"v_mfma_f32_16x16x32_bf16 v[100:103], a[40:43], a[196:199], v[100:103]\n\t"
"ds_write_b64 v250, v[64:65] offset:18432\n\t"
"v_mfma_f32_16x16x32_bf16 v[132:135], a[44:47], a[176:179], v[132:135]\n\t"
"s_waitcnt vmcnt(15)\n\t"
"v_mfma_f32_16x16x32_bf16 v[132:135], a[32:35], a[180:183], v[132:135]\n\t"
"v_mfma_f32_16x16x32_bf16 v[132:135], a[36:39], a[184:187], v[132:135]\n\t"
"v_mfma_f32_16x16x32_bf16 v[132:135], a[216:219], a[188:191], v[132:135]\n\t"
"s_waitcnt vmcnt(10)\n\t"
"v_cvt_pk_bf16_f32 v100, v100, v101\n\t"
"v_cvt_pk_bf16_f32 v101, v102, v103\n\t"
"v_mfma_f32_16x16x32_bf16 v[132:135], a[220:223], a[192:195], v[132:135]\n\t"
"v_mfma_f32_16x16x32_bf16 v[132:135], a[224:227], a[196:199], v[132:135]\n\t"
"v_mfma_f32_16x16x32_bf16 v[164:167], a[76:79], a[176:179], v[164:167]\n\t"
"s_waitcnt vmcnt(5)\n\t"
"ds_read_b128 a[176:179], v255 offset:11264\n\t"
"v_mfma_f32_16x16x32_bf16 v[164:167], a[4:7], a[180:183], v[164:167]\n\t"
"v_mfma_f32_16x16x32_bf16 v[164:167], a[80:83], a[184:187], v[164:167]\n\t"
"v_mfma_f32_16x16x32_bf16 v[164:167], a[8:11], a[188:191], v[164:167]\n\t"
"s_waitcnt lgkmcnt(4)\n\t"
"ds_read_b128 a[188:191], v255 offset:11776\n\t"
"v_cvt_pk_bf16_f32 v102, v132, v133\n\t"
"v_mfma_f32_16x16x32_bf16 v[164:167], a[12:15], a[192:195], v[164:167]\n\t"
"v_cvt_pk_bf16_f32 v103, v134, v135\n\t"
"v_mfma_f32_16x16x32_bf16 v[164:167], a[228:231], a[196:199], v[164:167]\n\t"
"ds_write_b128 v249, v[100:103] offset:24576\n\t"
"v_mfma_f32_16x16x32_bf16 v[4:7], a[148:151], v[204:207], v[4:7]\n\t"
"s_waitcnt lgkmcnt(5)\n\t"
"v_mfma_f32_16x16x32_bf16 v[4:7], a[236:239], v[212:215], v[4:7]\n\t"
"v_mfma_f32_16x16x32_bf16 v[4:7], a[240:243], a[168:171], v[4:7]\n\t"
"v_mfma_f32_16x16x32_bf16 v[4:7], a[244:247], v[216:219], v[4:7]\n\t"
"v_cvt_pk_bf16_f32 v164, v164, v165\n\t"
"v_cvt_pk_bf16_f32 v165, v166, v167\n\t"
"v_mfma_f32_16x16x32_bf16 v[4:7], a[104:107], a[0:3], v[4:7]\n\t"
"v_mfma_f32_16x16x32_bf16 v[4:7], a[156:159], a[172:175], v[4:7]\n\t"
"ds_write_b64 v250, v[164:165] offset:24576\n\t"
"v_mfma_f32_16x16x32_bf16 v[36:39], a[164:167], v[204:207], v[36:39]\n\t"
"v_mfma_f32_16x16x32_bf16 v[36:39], v[240:243], v[212:215], v[36:39]\n\t"
"v_mfma_f32_16x16x32_bf16 v[36:39], v[96:99], a[168:171], v[36:39]\n\t"
"v_mfma_f32_16x16x32_bf16 v[36:39], a[248:251], v[216:219], v[36:39]\n\t"
"v_cvt_pk_bf16_f32 v4, v4, v5\n\t"
"v_cvt_pk_bf16_f32 v5, v6, v7\n\t"
"v_mfma_f32_16x16x32_bf16 v[36:39], a[52:55], a[0:3], v[36:39]\n\t"
"v_mfma_f32_16x16x32_bf16 v[36:39], a[56:59], a[172:175], v[36:39]\n\t"
"v_mfma_f32_16x16x32_bf16 v[68:71], a[48:51], v[204:207], v[68:71]\n\t"
"ds_read_b128 a[200:203], v255 offset:12800\n\t"
"v_mfma_f32_16x16x32_bf16 v[68:71], v[236:239], v[212:215], v[68:71]\n\t"
"v_mfma_f32_16x16x32_bf16 v[68:71], a[60:63], a[168:171], v[68:71]\n\t"
"s_waitcnt lgkmcnt(1)\n\t"
"s_barrier\n\t"
"v_mfma_f32_16x16x32_bf16 v[68:71], a[212:215], v[216:219], v[68:71]\n\t"
"ds_read_b128 v[196:199], v252 offset:18432\n\t"
"v_mfma_f32_16x16x32_bf16 v[68:71], a[252:255], a[0:3], v[68:71]\n\t"
"ds_read_b128 v[200:203], v252 offset:19456\n\t"
"v_cvt_pk_bf16_f32 v6, v36, v37\n\t"
"v_mfma_f32_16x16x32_bf16 v[68:71], a[28:31], a[172:175], v[68:71]\n\t"
"ds_read_b128 v[204:207], v252 offset:20480\n\t"
"v_cvt_pk_bf16_f32 v7, v38, v39\n\t"
"v_mfma_f32_16x16x32_bf16 v[104:107], a[112:115], a[180:183], v[104:107]\n\t"
"s_add_u32 s29, s23, 0\n\t"
"ds_read_b128 a[204:207], v255 offset:13312\n\t"
"v_mfma_f32_16x16x32_bf16 v[104:107], a[120:123], a[184:187], v[104:107]\n\t"
"v_mfma_f32_16x16x32_bf16 v[104:107], a[64:67], a[176:179], v[104:107]\n\t"
"ds_write_b128 v249, v[4:7] offset:30720\n\t"
"v_mfma_f32_16x16x32_bf16 v[104:107], a[68:71], a[192:195], v[104:107]\n\t"
"s_waitcnt lgkmcnt(4)\n\t"
"v_cvt_pk_bf16_f32 v68, v68, v69\n\t"
"v_cvt_pk_bf16_f32 v69, v70, v71\n\t"
"v_mfma_f32_16x16x32_bf16 v[104:107], a[72:75], a[196:199], v[104:107]\n\t"
"v_mfma_f32_16x16x32_bf16 v[104:107], a[40:43], a[188:191], v[104:107]\n\t"
"ds_write_b64 v250, v[68:69] offset:30720\n\t"
"v_mfma_f32_16x16x32_bf16 v[136:139], a[44:47], a[180:183], v[136:139]\n\t"
"s_waitcnt lgkmcnt(4)\n\t"
"v_mfma_f32_16x16x32_bf16 v[136:139], a[32:35], a[184:187], v[136:139]\n\t"
"v_mfma_f32_16x16x32_bf16 v[136:139], a[36:39], a[176:179], v[136:139]\n\t"
"buffer_store_dwordx4 v[196:199], v248, s[16:19], s29 offen offset:0 sc1\n\t"
"v_mfma_f32_16x16x32_bf16 v[136:139], a[216:219], a[192:195], v[136:139]\n\t"
"s_waitcnt lgkmcnt(3)\n\t"
"v_cvt_pk_bf16_f32 v104, v104, v105\n\t"
"v_cvt_pk_bf16_f32 v105, v106, v107\n\t"
"v_mfma_f32_16x16x32_bf16 v[136:139], a[220:223], a[196:199], v[136:139]\n\t"
"v_mfma_f32_16x16x32_bf16 v[136:139], a[224:227], a[188:191], v[136:139]\n\t"
"buffer_store_dwordx4 v[200:203], v248, s[16:19], s29 offen offset:128 sc1\n\t"
"v_mfma_f32_16x16x32_bf16 v[168:171], a[76:79], a[180:183], v[168:171]\n\t"
"ds_read_b128 a[180:183], v255 offset:14336\n\t"
"v_mfma_f32_16x16x32_bf16 v[168:171], a[4:7], a[184:187], v[168:171]\n\t"
"v_mfma_f32_16x16x32_bf16 v[168:171], a[80:83], a[176:179], v[168:171]\n\t"
"buffer_store_dwordx4 v[204:207], v248, s[16:19], s29 offen offset:256 sc1\n\t"
"v_mfma_f32_16x16x32_bf16 v[168:171], a[8:11], a[192:195], v[168:171]\n\t"
"ds_read_b128 a[192:195], v255 offset:14848\n\t"
"v_cvt_pk_bf16_f32 v106, v136, v137\n\t"
"v_mfma_f32_16x16x32_bf16 v[168:171], a[12:15], a[196:199], v[168:171]\n\t"
"v_cvt_pk_bf16_f32 v107, v138, v139\n\t"
"v_mfma_f32_16x16x32_bf16 v[168:171], a[228:231], a[188:191], v[168:171]\n\t"
"ds_write_b128 v249, v[104:107] offset:36864\n\t"
"v_mfma_f32_16x16x32_bf16 v[8:11], a[148:151], v[212:215], v[8:11]\n\t"
"s_waitcnt lgkmcnt(5)\n\t"
"v_mfma_f32_16x16x32_bf16 v[8:11], a[236:239], a[168:171], v[8:11]\n\t"
"v_mfma_f32_16x16x32_bf16 v[8:11], a[240:243], a[200:203], v[8:11]\n\t"
"v_mfma_f32_16x16x32_bf16 v[8:11], a[244:247], a[0:3], v[8:11]\n\t"
"v_cvt_pk_bf16_f32 v168, v168, v169\n\t"
"v_cvt_pk_bf16_f32 v169, v170, v171\n\t"
"v_mfma_f32_16x16x32_bf16 v[8:11], a[104:107], a[172:175], v[8:11]\n\t"
"v_mfma_f32_16x16x32_bf16 v[8:11], a[156:159], a[204:207], v[8:11]\n\t"
"ds_write_b64 v250, v[168:169] offset:36864\n\t"
"v_mfma_f32_16x16x32_bf16 v[40:43], a[164:167], v[212:215], v[40:43]\n\t"
"v_mfma_f32_16x16x32_bf16 v[40:43], v[240:243], a[168:171], v[40:43]\n\t"
"v_mfma_f32_16x16x32_bf16 v[40:43], v[96:99], a[200:203], v[40:43]\n\t"
"v_mfma_f32_16x16x32_bf16 v[40:43], a[248:251], a[0:3], v[40:43]\n\t"
"v_cvt_pk_bf16_f32 v8, v8, v9\n\t"
"v_cvt_pk_bf16_f32 v9, v10, v11\n\t"
"v_mfma_f32_16x16x32_bf16 v[40:43], a[52:55], a[172:175], v[40:43]\n\t"
"v_mfma_f32_16x16x32_bf16 v[40:43], a[56:59], a[204:207], v[40:43]\n\t"
"v_mfma_f32_16x16x32_bf16 v[72:75], a[48:51], v[212:215], v[72:75]\n\t"
"ds_read_b128 v[208:211], v255 offset:15872\n\t"
"v_mfma_f32_16x16x32_bf16 v[72:75], v[236:239], a[168:171], v[72:75]\n\t"
"v_mfma_f32_16x16x32_bf16 v[72:75], a[60:63], a[200:203], v[72:75]\n\t"
"s_waitcnt lgkmcnt(1)\n\t"
"s_barrier\n\t"
"v_mfma_f32_16x16x32_bf16 v[72:75], a[212:215], a[0:3], v[72:75]\n\t"
"ds_read_b128 v[196:199], v252 offset:30720\n\t"
"v_mfma_f32_16x16x32_bf16 v[72:75], a[252:255], a[172:175], v[72:75]\n\t"
"ds_read_b128 v[200:203], v252 offset:31744\n\t"
"v_cvt_pk_bf16_f32 v10, v40, v41\n\t"
"v_mfma_f32_16x16x32_bf16 v[72:75], a[28:31], a[204:207], v[72:75]\n\t"
"ds_read_b128 v[204:207], v252 offset:32768\n\t"
"v_cvt_pk_bf16_f32 v11, v42, v43\n\t"
"v_mfma_f32_16x16x32_bf16 v[108:111], a[112:115], a[184:187], v[108:111]\n\t"
"s_add_u32 s29, s23, 196608\n\t"
"ds_read_b128 v[212:215], v255 offset:16384\n\t"
"v_mfma_f32_16x16x32_bf16 v[108:111], a[120:123], a[176:179], v[108:111]\n\t"
"v_mfma_f32_16x16x32_bf16 v[108:111], a[64:67], a[180:183], v[108:111]\n\t"
"ds_write_b128 v249, v[8:11] offset:18432\n\t"
"v_mfma_f32_16x16x32_bf16 v[108:111], a[68:71], a[196:199], v[108:111]\n\t"
"s_waitcnt lgkmcnt(4)\n\t"
"v_cvt_pk_bf16_f32 v72, v72, v73\n\t"
"v_cvt_pk_bf16_f32 v73, v74, v75\n\t"
"v_mfma_f32_16x16x32_bf16 v[108:111], a[72:75], a[188:191], v[108:111]\n\t"
"v_mfma_f32_16x16x32_bf16 v[108:111], a[40:43], a[192:195], v[108:111]\n\t"
"ds_write_b64 v250, v[72:73] offset:18432\n\t"
"v_mfma_f32_16x16x32_bf16 v[140:143], a[44:47], a[184:187], v[140:143]\n\t"
"s_waitcnt lgkmcnt(4)\n\t"
"v_mfma_f32_16x16x32_bf16 v[140:143], a[32:35], a[176:179], v[140:143]\n\t"
"v_mfma_f32_16x16x32_bf16 v[140:143], a[36:39], a[180:183], v[140:143]\n\t"
"buffer_store_dwordx4 v[196:199], v248, s[16:19], s29 offen offset:0 sc1\n\t"
"v_mfma_f32_16x16x32_bf16 v[140:143], a[216:219], a[196:199], v[140:143]\n\t"
"s_waitcnt lgkmcnt(3)\n\t"
"v_cvt_pk_bf16_f32 v108, v108, v109\n\t"
"v_cvt_pk_bf16_f32 v109, v110, v111\n\t"
"v_mfma_f32_16x16x32_bf16 v[140:143], a[220:223], a[188:191], v[140:143]\n\t"
"v_mfma_f32_16x16x32_bf16 v[140:143], a[224:227], a[192:195], v[140:143]\n\t"
"buffer_store_dwordx4 v[200:203], v248, s[16:19], s29 offen offset:128 sc1\n\t"
"v_mfma_f32_16x16x32_bf16 v[172:175], a[76:79], a[184:187], v[172:175]\n\t"
"ds_read_b128 a[184:187], v255 offset:17408\n\t"
"v_mfma_f32_16x16x32_bf16 v[172:175], a[4:7], a[176:179], v[172:175]\n\t"
"v_mfma_f32_16x16x32_bf16 v[172:175], a[80:83], a[180:183], v[172:175]\n\t"
"buffer_store_dwordx4 v[204:207], v248, s[16:19], s29 offen offset:256 sc1\n\t"
"v_mfma_f32_16x16x32_bf16 v[172:175], a[8:11], a[196:199], v[172:175]\n\t"
"ds_read_b128 a[196:199], v255 offset:17920\n\t"
"v_cvt_pk_bf16_f32 v110, v140, v141\n\t"
"v_mfma_f32_16x16x32_bf16 v[172:175], a[12:15], a[188:191], v[172:175]\n\t"
"v_cvt_pk_bf16_f32 v111, v142, v143\n\t"
"v_mfma_f32_16x16x32_bf16 v[172:175], a[228:231], a[192:195], v[172:175]\n\t"
"ds_write_b128 v249, v[108:111] offset:24576\n\t"
"v_mfma_f32_16x16x32_bf16 v[12:15], a[148:151], a[168:171], v[12:15]\n\t"
"s_waitcnt lgkmcnt(5)\n\t"
"v_mfma_f32_16x16x32_bf16 v[12:15], a[236:239], a[200:203], v[12:15]\n\t"
"v_mfma_f32_16x16x32_bf16 v[12:15], a[240:243], v[208:211], v[12:15]\n\t"
"v_mfma_f32_16x16x32_bf16 v[12:15], a[244:247], a[172:175], v[12:15]\n\t"
"v_cvt_pk_bf16_f32 v172, v172, v173\n\t"
"v_cvt_pk_bf16_f32 v173, v174, v175\n\t"
"v_mfma_f32_16x16x32_bf16 v[12:15], a[104:107], a[204:207], v[12:15]\n\t"
"v_mfma_f32_16x16x32_bf16 v[12:15], a[156:159], v[212:215], v[12:15]\n\t"
"ds_write_b64 v250, v[172:173] offset:24576\n\t"
"v_mfma_f32_16x16x32_bf16 v[44:47], a[164:167], a[168:171], v[44:47]\n\t"
"v_mfma_f32_16x16x32_bf16 v[44:47], v[240:243], a[200:203], v[44:47]\n\t"
"v_mfma_f32_16x16x32_bf16 v[44:47], v[96:99], v[208:211], v[44:47]\n\t"
"v_mfma_f32_16x16x32_bf16 v[44:47], a[248:251], a[172:175], v[44:47]\n\t"
"v_cvt_pk_bf16_f32 v12, v12, v13\n\t"
"v_cvt_pk_bf16_f32 v13, v14, v15\n\t"
"v_mfma_f32_16x16x32_bf16 v[44:47], a[52:55], a[204:207], v[44:47]\n\t"
"v_mfma_f32_16x16x32_bf16 v[44:47], a[56:59], v[212:215], v[44:47]\n\t"
"v_mfma_f32_16x16x32_bf16 v[76:79], a[48:51], a[168:171], v[76:79]\n\t"
"ds_read_b128 a[168:171], v255 offset:18944\n\t"
"v_mfma_f32_16x16x32_bf16 v[76:79], v[236:239], a[200:203], v[76:79]\n\t"
"v_mfma_f32_16x16x32_bf16 v[76:79], a[60:63], v[208:211], v[76:79]\n\t"
"s_waitcnt lgkmcnt(1)\n\t"
"s_barrier\n\t"
"v_mfma_f32_16x16x32_bf16 v[76:79], a[212:215], a[172:175], v[76:79]\n\t"
"ds_read_b128 v[196:199], v252 offset:18432\n\t"
"v_mfma_f32_16x16x32_bf16 v[76:79], a[252:255], a[204:207], v[76:79]\n\t"
"ds_read_b128 v[200:203], v252 offset:19456\n\t"
"v_cvt_pk_bf16_f32 v14, v44, v45\n\t"
"v_mfma_f32_16x16x32_bf16 v[76:79], a[28:31], v[212:215], v[76:79]\n\t"
"ds_read_b128 v[204:207], v252 offset:20480\n\t"
"v_cvt_pk_bf16_f32 v15, v46, v47\n\t"
"v_mfma_f32_16x16x32_bf16 v[112:115], a[112:115], a[176:179], v[112:115]\n\t"
"s_add_u32 s29, s23, 393216\n\t"
"ds_read_b128 a[172:175], v255 offset:19456\n\t"
"v_mfma_f32_16x16x32_bf16 v[112:115], a[120:123], a[180:183], v[112:115]\n\t"
"v_mfma_f32_16x16x32_bf16 v[112:115], a[64:67], a[184:187], v[112:115]\n\t"
"ds_write_b128 v249, v[12:15] offset:30720\n\t"
"v_mfma_f32_16x16x32_bf16 v[112:115], a[68:71], a[188:191], v[112:115]\n\t"
"s_waitcnt lgkmcnt(4)\n\t"
"v_cvt_pk_bf16_f32 v76, v76, v77\n\t"
"v_cvt_pk_bf16_f32 v77, v78, v79\n\t"
"v_mfma_f32_16x16x32_bf16 v[112:115], a[72:75], a[192:195], v[112:115]\n\t"
"v_mfma_f32_16x16x32_bf16 v[112:115], a[40:43], a[196:199], v[112:115]\n\t"
"ds_write_b64 v250, v[76:77] offset:30720\n\t"
"v_mfma_f32_16x16x32_bf16 v[144:147], a[44:47], a[176:179], v[144:147]\n\t"
"s_waitcnt lgkmcnt(4)\n\t"
"v_mfma_f32_16x16x32_bf16 v[144:147], a[32:35], a[180:183], v[144:147]\n\t"
"v_mfma_f32_16x16x32_bf16 v[144:147], a[36:39], a[184:187], v[144:147]\n\t"
"buffer_store_dwordx4 v[196:199], v248, s[16:19], s29 offen offset:0 sc1\n\t"
"v_mfma_f32_16x16x32_bf16 v[144:147], a[216:219], a[188:191], v[144:147]\n\t"
"s_waitcnt lgkmcnt(3)\n\t"
"v_cvt_pk_bf16_f32 v112, v112, v113\n\t"
"v_cvt_pk_bf16_f32 v113, v114, v115\n\t"
"v_mfma_f32_16x16x32_bf16 v[144:147], a[220:223], a[192:195], v[144:147]\n\t"
"v_mfma_f32_16x16x32_bf16 v[144:147], a[224:227], a[196:199], v[144:147]\n\t"
"buffer_store_dwordx4 v[200:203], v248, s[16:19], s29 offen offset:128 sc1\n\t"
"v_mfma_f32_16x16x32_bf16 v[176:179], a[76:79], a[176:179], v[176:179]\n\t"
"ds_read_b128 a[176:179], v255 offset:20480\n\t"
"v_mfma_f32_16x16x32_bf16 v[176:179], a[4:7], a[180:183], v[176:179]\n\t"
"v_mfma_f32_16x16x32_bf16 v[176:179], a[80:83], a[184:187], v[176:179]\n\t"
"buffer_store_dwordx4 v[204:207], v248, s[16:19], s29 offen offset:256 sc1\n\t"
"v_mfma_f32_16x16x32_bf16 v[176:179], a[8:11], a[188:191], v[176:179]\n\t"
"ds_read_b128 a[188:191], v255 offset:20992\n\t"
"v_cvt_pk_bf16_f32 v114, v144, v145\n\t"
"v_mfma_f32_16x16x32_bf16 v[176:179], a[12:15], a[192:195], v[176:179]\n\t"
"v_cvt_pk_bf16_f32 v115, v146, v147\n\t"
"v_mfma_f32_16x16x32_bf16 v[176:179], a[228:231], a[196:199], v[176:179]\n\t"
"ds_write_b128 v249, v[112:115] offset:36864\n\t"
"Lst35_%=:\n\t"
"v_mfma_f32_16x16x32_bf16 v[16:19], a[148:151], a[200:203], v[16:19]\n\t"
"s_waitcnt lgkmcnt(5)\n\t"
"v_mfma_f32_16x16x32_bf16 v[16:19], a[236:239], v[208:211], v[16:19]\n\t"
"v_mfma_f32_16x16x32_bf16 v[16:19], a[240:243], a[168:171], v[16:19]\n\t"
"v_mfma_f32_16x16x32_bf16 v[16:19], a[244:247], a[204:207], v[16:19]\n\t"
"v_cvt_pk_bf16_f32 v176, v176, v177\n\t"
"v_cvt_pk_bf16_f32 v177, v178, v179\n\t"
"v_mfma_f32_16x16x32_bf16 v[16:19], a[104:107], v[212:215], v[16:19]\n\t"
"v_mfma_f32_16x16x32_bf16 v[16:19], a[156:159], a[172:175], v[16:19]\n\t"
"ds_write_b64 v250, v[176:177] offset:36864\n\t"
"v_mfma_f32_16x16x32_bf16 v[48:51], a[164:167], a[200:203], v[48:51]\n\t"
"v_mfma_f32_16x16x32_bf16 v[48:51], v[240:243], v[208:211], v[48:51]\n\t"
"v_mfma_f32_16x16x32_bf16 v[48:51], v[96:99], a[168:171], v[48:51]\n\t"
"v_mfma_f32_16x16x32_bf16 v[48:51], a[248:251], a[204:207], v[48:51]\n\t"
"v_cvt_pk_bf16_f32 v16, v16, v17\n\t"
"v_cvt_pk_bf16_f32 v17, v18, v19\n\t"
"v_mfma_f32_16x16x32_bf16 v[48:51], a[52:55], v[212:215], v[48:51]\n\t"
"v_mfma_f32_16x16x32_bf16 v[48:51], a[56:59], a[172:175], v[48:51]\n\t"
"v_mfma_f32_16x16x32_bf16 v[80:83], a[48:51], a[200:203], v[80:83]\n\t"
"ds_read_b128 a[200:203], v255 offset:22016\n\t"
"v_mfma_f32_16x16x32_bf16 v[80:83], v[236:239], v[208:211], v[80:83]\n\t"
"v_mfma_f32_16x16x32_bf16 v[80:83], a[60:63], a[168:171], v[80:83]\n\t"
"s_waitcnt lgkmcnt(1)\n\t"
"s_barrier\n\t"
"v_mfma_f32_16x16x32_bf16 v[80:83], a[212:215], a[204:207], v[80:83]\n\t"
"ds_read_b128 v[196:199], v252 offset:30720\n\t"
"v_mfma_f32_16x16x32_bf16 v[80:83], a[252:255], v[212:215], v[80:83]\n\t"
"ds_read_b128 v[200:203], v252 offset:31744\n\t"
"v_cvt_pk_bf16_f32 v18, v48, v49\n\t"
"v_mfma_f32_16x16x32_bf16 v[80:83], a[28:31], a[172:175], v[80:83]\n\t"
"ds_read_b128 v[204:207], v252 offset:32768\n\t"
"v_cvt_pk_bf16_f32 v19, v50, v51\n\t"
"v_mfma_f32_16x16x32_bf16 v[116:119], a[112:115], a[180:183], v[116:119]\n\t"
"s_add_u32 s29, s23, 589824\n\t"
"ds_read_b128 a[204:207], v255 offset:22528\n\t"
"v_mfma_f32_16x16x32_bf16 v[116:119], a[120:123], a[184:187], v[116:119]\n\t"
"v_mfma_f32_16x16x32_bf16 v[116:119], a[64:67], a[176:179], v[116:119]\n\t"
"ds_write_b128 v249, v[16:19] offset:18432\n\t"
"v_mfma_f32_16x16x32_bf16 v[116:119], a[68:71], a[192:195], v[116:119]\n\t"
"s_waitcnt lgkmcnt(4)\n\t"
"v_cvt_pk_bf16_f32 v80, v80, v81\n\t"
"v_cvt_pk_bf16_f32 v81, v82, v83\n\t"
"v_mfma_f32_16x16x32_bf16 v[116:119], a[72:75], a[196:199], v[116:119]\n\t"
"v_mfma_f32_16x16x32_bf16 v[116:119], a[40:43], a[188:191], v[116:119]\n\t"
"ds_write_b64 v250, v[80:81] offset:18432\n\t"
"v_mfma_f32_16x16x32_bf16 v[148:151], a[44:47], a[180:183], v[148:151]\n\t"
"s_waitcnt lgkmcnt(4)\n\t"
"v_mfma_f32_16x16x32_bf16 v[148:151], a[32:35], a[184:187], v[148:151]\n\t"
"v_mfma_f32_16x16x32_bf16 v[148:151], a[36:39], a[176:179], v[148:151]\n\t"
"buffer_store_dwordx4 v[196:199], v248, s[16:19], s29 offen offset:0 sc1\n\t"
"v_mfma_f32_16x16x32_bf16 v[148:151], a[216:219], a[192:195], v[148:151]\n\t"
"s_waitcnt lgkmcnt(3)\n\t"
"v_cvt_pk_bf16_f32 v116, v116, v117\n\t"
"v_cvt_pk_bf16_f32 v117, v118, v119\n\t"
"v_mfma_f32_16x16x32_bf16 v[148:151], a[220:223], a[196:199], v[148:151]\n\t"
"v_mfma_f32_16x16x32_bf16 v[148:151], a[224:227], a[188:191], v[148:151]\n\t"
"buffer_store_dwordx4 v[200:203], v248, s[16:19], s29 offen offset:128 sc1\n\t"
"v_mfma_f32_16x16x32_bf16 v[180:183], a[76:79], a[180:183], v[180:183]\n\t"
"ds_read_b128 a[180:183], v255 offset:23552\n\t"
"v_mfma_f32_16x16x32_bf16 v[180:183], a[4:7], a[184:187], v[180:183]\n\t"
"v_mfma_f32_16x16x32_bf16 v[180:183], a[80:83], a[176:179], v[180:183]\n\t"
"buffer_store_dwordx4 v[204:207], v248, s[16:19], s29 offen offset:256 sc1\n\t"
"v_mfma_f32_16x16x32_bf16 v[180:183], a[8:11], a[192:195], v[180:183]\n\t"
"ds_read_b128 a[192:195], v255 offset:24064\n\t"
"v_cvt_pk_bf16_f32 v118, v148, v149\n\t"
"v_mfma_f32_16x16x32_bf16 v[180:183], a[12:15], a[196:199], v[180:183]\n\t"
"v_cvt_pk_bf16_f32 v119, v150, v151\n\t"
"v_mfma_f32_16x16x32_bf16 v[180:183], a[228:231], a[188:191], v[180:183]\n\t"
"ds_write_b128 v249, v[116:119] offset:24576\n\t"
"v_mfma_f32_16x16x32_bf16 v[20:23], a[148:151], v[208:211], v[20:23]\n\t"
"s_waitcnt lgkmcnt(5)\n\t"
"v_mfma_f32_16x16x32_bf16 v[20:23], a[236:239], a[168:171], v[20:23]\n\t"
"v_mfma_f32_16x16x32_bf16 v[20:23], a[240:243], a[200:203], v[20:23]\n\t"
"v_mfma_f32_16x16x32_bf16 v[20:23], a[244:247], v[212:215], v[20:23]\n\t"
"v_cvt_pk_bf16_f32 v180, v180, v181\n\t"
"v_cvt_pk_bf16_f32 v181, v182, v183\n\t"
"v_mfma_f32_16x16x32_bf16 v[20:23], a[104:107], a[172:175], v[20:23]\n\t"
"v_mfma_f32_16x16x32_bf16 v[20:23], a[156:159], a[204:207], v[20:23]\n\t"
"ds_write_b64 v250, v[180:181] offset:24576\n\t"
"v_mfma_f32_16x16x32_bf16 v[52:55], a[164:167], v[208:211], v[52:55]\n\t"
"v_mfma_f32_16x16x32_bf16 v[52:55], v[240:243], a[168:171], v[52:55]\n\t"
"v_mfma_f32_16x16x32_bf16 v[52:55], v[96:99], a[200:203], v[52:55]\n\t"
"v_mfma_f32_16x16x32_bf16 v[52:55], a[248:251], v[212:215], v[52:55]\n\t"
"v_cvt_pk_bf16_f32 v20, v20, v21\n\t"
"v_cvt_pk_bf16_f32 v21, v22, v23\n\t"
"v_mfma_f32_16x16x32_bf16 v[52:55], a[52:55], a[172:175], v[52:55]\n\t"
"v_mfma_f32_16x16x32_bf16 v[52:55], a[56:59], a[204:207], v[52:55]\n\t"
"v_mfma_f32_16x16x32_bf16 v[84:87], a[48:51], v[208:211], v[84:87]\n\t"
"ds_read_b128 v[208:211], v255 offset:25088\n\t"
"v_mfma_f32_16x16x32_bf16 v[84:87], v[236:239], a[168:171], v[84:87]\n\t"
"v_mfma_f32_16x16x32_bf16 v[84:87], a[60:63], a[200:203], v[84:87]\n\t"
"s_waitcnt lgkmcnt(1)\n\t"
"s_barrier\n\t"
"v_mfma_f32_16x16x32_bf16 v[84:87], a[212:215], v[212:215], v[84:87]\n\t"
"ds_read_b128 v[196:199], v252 offset:18432\n\t"
"v_mfma_f32_16x16x32_bf16 v[84:87], a[252:255], a[172:175], v[84:87]\n\t"
"ds_read_b128 v[200:203], v252 offset:19456\n\t"
"v_cvt_pk_bf16_f32 v22, v52, v53\n\t"
"v_mfma_f32_16x16x32_bf16 v[84:87], a[28:31], a[204:207], v[84:87]\n\t"
"ds_read_b128 v[204:207], v252 offset:20480\n\t"
"v_cvt_pk_bf16_f32 v23, v54, v55\n\t"
"v_mfma_f32_16x16x32_bf16 v[120:123], a[112:115], a[184:187], v[120:123]\n\t"
"s_add_u32 s29, s23, 786432\n\t"
"ds_read_b128 v[212:215], v255 offset:25600\n\t"
"v_mfma_f32_16x16x32_bf16 v[120:123], a[120:123], a[176:179], v[120:123]\n\t"
"v_mfma_f32_16x16x32_bf16 v[120:123], a[64:67], a[180:183], v[120:123]\n\t"
"ds_write_b128 v249, v[20:23] offset:30720\n\t"
"v_mfma_f32_16x16x32_bf16 v[120:123], a[68:71], a[196:199], v[120:123]\n\t"
"s_waitcnt lgkmcnt(4)\n\t"
"v_cvt_pk_bf16_f32 v84, v84, v85\n\t"
"v_cvt_pk_bf16_f32 v85, v86, v87\n\t"
"v_mfma_f32_16x16x32_bf16 v[120:123], a[72:75], a[188:191], v[120:123]\n\t"
"v_mfma_f32_16x16x32_bf16 v[120:123], a[40:43], a[192:195], v[120:123]\n\t"
"ds_write_b64 v250, v[84:85] offset:30720\n\t"
"v_mfma_f32_16x16x32_bf16 v[152:155], a[44:47], a[184:187], v[152:155]\n\t"
"s_waitcnt lgkmcnt(4)\n\t"
"v_mfma_f32_16x16x32_bf16 v[152:155], a[32:35], a[176:179], v[152:155]\n\t"
"v_mfma_f32_16x16x32_bf16 v[152:155], a[36:39], a[180:183], v[152:155]\n\t"
"buffer_store_dwordx4 v[196:199], v248, s[16:19], s29 offen offset:0 sc1\n\t"
"v_mfma_f32_16x16x32_bf16 v[152:155], a[216:219], a[196:199], v[152:155]\n\t"
"s_waitcnt lgkmcnt(3)\n\t"
"v_cvt_pk_bf16_f32 v120, v120, v121\n\t"
"v_cvt_pk_bf16_f32 v121, v122, v123\n\t"
"v_mfma_f32_16x16x32_bf16 v[152:155], a[220:223], a[188:191], v[152:155]\n\t"
"v_mfma_f32_16x16x32_bf16 v[152:155], a[224:227], a[192:195], v[152:155]\n\t"
"buffer_store_dwordx4 v[200:203], v248, s[16:19], s29 offen offset:128 sc1\n\t"
"v_mfma_f32_16x16x32_bf16 v[184:187], a[76:79], a[184:187], v[184:187]\n\t"
"ds_read_b128 a[184:187], v255 offset:26624\n\t"
"v_mfma_f32_16x16x32_bf16 v[184:187], a[4:7], a[176:179], v[184:187]\n\t"
"v_mfma_f32_16x16x32_bf16 v[184:187], a[80:83], a[180:183], v[184:187]\n\t"
"buffer_store_dwordx4 v[204:207], v248, s[16:19], s29 offen offset:256 sc1\n\t"
"v_mfma_f32_16x16x32_bf16 v[184:187], a[8:11], a[196:199], v[184:187]\n\t"
"ds_read_b128 a[196:199], v255 offset:27136\n\t"
"v_cvt_pk_bf16_f32 v122, v152, v153\n\t"
"v_mfma_f32_16x16x32_bf16 v[184:187], a[12:15], a[188:191], v[184:187]\n\t"
"v_cvt_pk_bf16_f32 v123, v154, v155\n\t"
"v_mfma_f32_16x16x32_bf16 v[184:187], a[228:231], a[192:195], v[184:187]\n\t"
"ds_write_b128 v249, v[120:123] offset:36864\n\t"
"v_mfma_f32_16x16x32_bf16 v[24:27], a[148:151], a[168:171], v[24:27]\n\t"
"s_waitcnt lgkmcnt(5)\n\t"
"v_mfma_f32_16x16x32_bf16 v[24:27], a[236:239], a[200:203], v[24:27]\n\t"
"v_mfma_f32_16x16x32_bf16 v[24:27], a[240:243], v[208:211], v[24:27]\n\t"
"v_mfma_f32_16x16x32_bf16 v[24:27], a[244:247], a[172:175], v[24:27]\n\t"
"v_cvt_pk_bf16_f32 v184, v184, v185\n\t"
"v_cvt_pk_bf16_f32 v185, v186, v187\n\t"
"v_mfma_f32_16x16x32_bf16 v[24:27], a[104:107], a[204:207], v[24:27]\n\t"
"v_mfma_f32_16x16x32_bf16 v[24:27], a[156:159], v[212:215], v[24:27]\n\t"
"ds_write_b64 v250, v[184:185] offset:36864\n\t"
"v_mfma_f32_16x16x32_bf16 v[56:59], a[164:167], a[168:171], v[56:59]\n\t"
"v_mfma_f32_16x16x32_bf16 v[56:59], v[240:243], a[200:203], v[56:59]\n\t"
"v_mfma_f32_16x16x32_bf16 v[56:59], v[96:99], v[208:211], v[56:59]\n\t"
"v_mfma_f32_16x16x32_bf16 v[56:59], a[248:251], a[172:175], v[56:59]\n\t"
"v_cvt_pk_bf16_f32 v24, v24, v25\n\t"
"v_cvt_pk_bf16_f32 v25, v26, v27\n\t"
"v_mfma_f32_16x16x32_bf16 v[56:59], a[52:55], a[204:207], v[56:59]\n\t"
"v_mfma_f32_16x16x32_bf16 v[56:59], a[56:59], v[212:215], v[56:59]\n\t"
"v_mfma_f32_16x16x32_bf16 v[88:91], a[48:51], a[168:171], v[88:91]\n\t"
"ds_read_b128 a[168:171], v255 offset:28160\n\t"
"v_mfma_f32_16x16x32_bf16 v[88:91], v[236:239], a[200:203], v[88:91]\n\t"
"v_mfma_f32_16x16x32_bf16 v[88:91], a[60:63], v[208:211], v[88:91]\n\t"
"s_waitcnt lgkmcnt(1)\n\t"
"s_barrier\n\t"
"v_mfma_f32_16x16x32_bf16 v[88:91], a[212:215], a[172:175], v[88:91]\n\t"
"ds_read_b128 v[196:199], v252 offset:30720\n\t"
"v_mfma_f32_16x16x32_bf16 v[88:91], a[252:255], a[204:207], v[88:91]\n\t"
"ds_read_b128 v[200:203], v252 offset:31744\n\t"
"v_cvt_pk_bf16_f32 v26, v56, v57\n\t"
"v_mfma_f32_16x16x32_bf16 v[88:91], a[28:31], v[212:215], v[88:91]\n\t"
"ds_read_b128 v[204:207], v252 offset:32768\n\t"
"v_cvt_pk_bf16_f32 v27, v58, v59\n\t"
"v_mfma_f32_16x16x32_bf16 v[124:127], a[112:115], a[176:179], v[124:127]\n\t"
"s_add_u32 s29, s23, 983040\n\t"
"ds_read_b128 a[172:175], v255 offset:28672\n\t"
"v_mfma_f32_16x16x32_bf16 v[124:127], a[120:123], a[180:183], v[124:127]\n\t"
"v_mfma_f32_16x16x32_bf16 v[124:127], a[64:67], a[184:187], v[124:127]\n\t"
"ds_write_b128 v249, v[24:27] offset:18432\n\t"
"v_mfma_f32_16x16x32_bf16 v[124:127], a[68:71], a[188:191], v[124:127]\n\t"
"Lpg26_%=:\n\t"
"s_cbranch_vccnz Lvr4_%=\n\t"
"v_cvt_pk_bf16_f32 v88, v88, v89\n\t"
"v_cvt_pk_bf16_f32 v89, v90, v91\n\t"
"v_mfma_f32_16x16x32_bf16 v[124:127], a[72:75], a[192:195], v[124:127]\n\t"
"s_waitcnt lgkmcnt(4)\n\t"
"v_mfma_f32_16x16x32_bf16 v[124:127], a[40:43], a[196:199], v[124:127]\n\t"
"ds_write_b64 v250, v[88:89] offset:18432\n\t"
"v_mfma_f32_16x16x32_bf16 v[156:159], a[44:47], a[176:179], v[156:159]\n\t"
"s_waitcnt lgkmcnt(4)\n\t"
"v_mfma_f32_16x16x32_bf16 v[156:159], a[32:35], a[180:183], v[156:159]\n\t"
"v_mfma_f32_16x16x32_bf16 v[156:159], a[36:39], a[184:187], v[156:159]\n\t"
"buffer_store_dwordx4 v[196:199], v248, s[16:19], s29 offen offset:0 sc1\n\t"
"v_mfma_f32_16x16x32_bf16 v[156:159], a[216:219], a[188:191], v[156:159]\n\t"
"s_waitcnt lgkmcnt(3)\n\t"
"v_cvt_pk_bf16_f32 v124, v124, v125\n\t"
"v_cvt_pk_bf16_f32 v125, v126, v127\n\t"
"v_mfma_f32_16x16x32_bf16 v[156:159], a[220:223], a[192:195], v[156:159]\n\t"
"v_mfma_f32_16x16x32_bf16 v[156:159], a[224:227], a[196:199], v[156:159]\n\t"
"buffer_store_dwordx4 v[200:203], v248, s[16:19], s29 offen offset:128 sc1\n\t"
"v_mfma_f32_16x16x32_bf16 v[188:191], a[76:79], a[176:179], v[188:191]\n\t"
"ds_read_b128 a[176:179], v255 offset:29696\n\t"
"v_mfma_f32_16x16x32_bf16 v[188:191], a[4:7], a[180:183], v[188:191]\n\t"
"v_mfma_f32_16x16x32_bf16 v[188:191], a[80:83], a[184:187], v[188:191]\n\t"
"buffer_store_dwordx4 v[204:207], v248, s[16:19], s29 offen offset:256 sc1\n\t"
"v_mfma_f32_16x16x32_bf16 v[188:191], a[8:11], a[188:191], v[188:191]\n\t"
"ds_read_b128 a[188:191], v255 offset:30208\n\t"
"v_cvt_pk_bf16_f32 v126, v156, v157\n\t"
"v_mfma_f32_16x16x32_bf16 v[188:191], a[12:15], a[192:195], v[188:191]\n\t"
"v_cvt_pk_bf16_f32 v127, v158, v159\n\t"
"v_mfma_f32_16x16x32_bf16 v[188:191], a[228:231], a[196:199], v[188:191]\n\t"
"ds_write_b128 v249, v[124:127] offset:24576\n\t"
"v_mfma_f32_16x16x32_bf16 v[28:31], a[148:151], a[200:203], v[28:31]\n\t"
"s_waitcnt lgkmcnt(5)\n\t"
"v_mfma_f32_16x16x32_bf16 v[28:31], a[236:239], v[208:211], v[28:31]\n\t"
"v_mfma_f32_16x16x32_bf16 v[28:31], a[240:243], a[168:171], v[28:31]\n\t"
"v_mfma_f32_16x16x32_bf16 v[28:31], a[244:247], a[204:207], v[28:31]\n\t"
"v_cvt_pk_bf16_f32 v188, v188, v189\n\t"
"v_cvt_pk_bf16_f32 v189, v190, v191\n\t"
"v_mfma_f32_16x16x32_bf16 v[28:31], a[104:107], v[212:215], v[28:31]\n\t"
"v_mfma_f32_16x16x32_bf16 v[28:31], a[156:159], a[172:175], v[28:31]\n\t"
"ds_write_b64 v250, v[188:189] offset:24576\n\t"
"v_mfma_f32_16x16x32_bf16 v[60:63], a[164:167], a[200:203], v[60:63]\n\t"
"v_mfma_f32_16x16x32_bf16 v[60:63], v[240:243], v[208:211], v[60:63]\n\t"
"v_mfma_f32_16x16x32_bf16 v[60:63], v[96:99], a[168:171], v[60:63]\n\t"
"v_mfma_f32_16x16x32_bf16 v[60:63], a[248:251], a[204:207], v[60:63]\n\t"
"v_cvt_pk_bf16_f32 v28, v28, v29\n\t"
"v_cvt_pk_bf16_f32 v29, v30, v31\n\t"
"v_mfma_f32_16x16x32_bf16 v[60:63], a[52:55], v[212:215], v[60:63]\n\t"
"v_mfma_f32_16x16x32_bf16 v[60:63], a[56:59], a[172:175], v[60:63]\n\t"
"v_mfma_f32_16x16x32_bf16 v[92:95], a[48:51], a[200:203], v[92:95]\n\t"
"v_mfma_f32_16x16x32_bf16 v[92:95], v[236:239], v[208:211], v[92:95]\n\t"
"v_mfma_f32_16x16x32_bf16 v[92:95], a[60:63], a[168:171], v[92:95]\n\t"
"s_waitcnt lgkmcnt(0)\n\t"
"s_barrier\n\t"
"v_mfma_f32_16x16x32_bf16 v[92:95], a[212:215], a[204:207], v[92:95]\n\t"
"ds_read_b128 v[196:199], v252 offset:18432\n\t"
"v_mfma_f32_16x16x32_bf16 v[92:95], a[252:255], v[212:215], v[92:95]\n\t"
"ds_read_b128 v[200:203], v252 offset:19456\n\t"
"v_cvt_pk_bf16_f32 v30, v60, v61\n\t"
"v_mfma_f32_16x16x32_bf16 v[92:95], a[28:31], a[172:175], v[92:95]\n\t"
"ds_read_b128 v[204:207], v252 offset:20480\n\t"
"v_cvt_pk_bf16_f32 v31, v62, v63\n\t"
"v_mfma_f32_16x16x32_bf16 v[128:131], a[112:115], a[180:183], v[128:131]\n\t"
"s_add_u32 s29, s23, 1179648\n\t"
"v_mfma_f32_16x16x32_bf16 v[128:131], a[120:123], a[184:187], v[128:131]\n\t"
"v_mfma_f32_16x16x32_bf16 v[128:131], a[64:67], a[176:179], v[128:131]\n\t"
"ds_write_b128 v249, v[28:31] offset:30720\n\t"
"v_mfma_f32_16x16x32_bf16 v[128:131], a[68:71], a[192:195], v[128:131]\n\t"
"s_waitcnt lgkmcnt(3)\n\t"
"v_cvt_pk_bf16_f32 v92, v92, v93\n\t"
"v_cvt_pk_bf16_f32 v93, v94, v95\n\t"
"v_mfma_f32_16x16x32_bf16 v[128:131], a[72:75], a[196:199], v[128:131]\n\t"
"v_mfma_f32_16x16x32_bf16 v[128:131], a[40:43], a[188:191], v[128:131]\n\t"
"ds_write_b64 v250, v[92:93] offset:30720\n\t"
"v_mfma_f32_16x16x32_bf16 v[160:163], a[44:47], a[180:183], v[160:163]\n\t"
"s_waitcnt lgkmcnt(3)\n\t"
"v_mfma_f32_16x16x32_bf16 v[160:163], a[32:35], a[184:187], v[160:163]\n\t"
"v_mfma_f32_16x16x32_bf16 v[160:163], a[36:39], a[176:179], v[160:163]\n\t"
"buffer_store_dwordx4 v[196:199], v248, s[16:19], s29 offen offset:0 sc1\n\t"
"v_mfma_f32_16x16x32_bf16 v[160:163], a[216:219], a[192:195], v[160:163]\n\t"
"s_waitcnt lgkmcnt(2)\n\t"
"v_cvt_pk_bf16_f32 v128, v128, v129\n\t"
"v_cvt_pk_bf16_f32 v129, v130, v131\n\t"
"v_mfma_f32_16x16x32_bf16 v[160:163], a[220:223], a[196:199], v[160:163]\n\t"
"v_mfma_f32_16x16x32_bf16 v[160:163], a[224:227], a[188:191], v[160:163]\n\t"
"buffer_store_dwordx4 v[200:203], v248, s[16:19], s29 offen offset:128 sc1\n\t"
"v_mfma_f32_16x16x32_bf16 v[192:195], a[76:79], a[180:183], v[192:195]\n\t"
"v_mfma_f32_16x16x32_bf16 v[192:195], a[4:7], a[184:187], v[192:195]\n\t"
"v_mfma_f32_16x16x32_bf16 v[192:195], a[80:83], a[176:179], v[192:195]\n\t"
"buffer_store_dwordx4 v[204:207], v248, s[16:19], s29 offen offset:256 sc1\n\t"
"v_mfma_f32_16x16x32_bf16 v[192:195], a[8:11], a[192:195], v[192:195]\n\t"
"v_cvt_pk_bf16_f32 v130, v160, v161\n\t"
"v_cvt_pk_bf16_f32 v131, v162, v163\n\t"
"v_mfma_f32_16x16x32_bf16 v[192:195], a[12:15], a[196:199], v[192:195]\n\t"
"v_mfma_f32_16x16x32_bf16 v[192:195], a[228:231], a[188:191], v[192:195]\n\t"
"ds_write_b128 v249, v[128:131] offset:36864\n\t"
"s_nop 6\n\t"
"v_cvt_pk_bf16_f32 v192, v192, v193\n\t"
"v_cvt_pk_bf16_f32 v193, v194, v195\n\t"
"ds_write_b64 v250, v[192:193] offset:36864\n\t"
"s_waitcnt lgkmcnt(0)\n\t"
"s_barrier\n\t"
"ds_read_b128 v[196:199], v252 offset:30720\n\t"
"ds_read_b128 v[200:203], v252 offset:31744\n\t"
"ds_read_b128 v[208:211], v252 offset:32768\n\t"
"s_waitcnt lgkmcnt(2)\n\t"
"s_add_u32 s29, s23, 1376256\n\t"
"buffer_store_dwordx4 v[196:199], v248, s[16:19], s29 offen offset:0 sc1\n\t"
"s_waitcnt lgkmcnt(1)\n\t"
"buffer_store_dwordx4 v[200:203], v248, s[16:19], s29 offen offset:128 sc1\n\t"
"s_waitcnt lgkmcnt(0)\n\t"
"buffer_store_dwordx4 v[208:211], v248, s[16:19], s29 offen offset:256 sc1\n\t"
"s_waitcnt vmcnt(0)\n\t"
"Lwino_end:\n\t"
  : "+{v205}"(dummy)
  : "s"((unsigned)pu), "s"((unsigned)(pu >> 32)), "s"((unsigned)px), "s"((unsigned)(px >> 32)), "s"((unsigned)py), "s"((unsigned)(py >> 32)),
    "s"(xbase), "s"(ybase), "s"(w), "s"((unsigned)(size_t)(__attribute__((address_space(3))) char*)smem), "s"(blockIdx.x), "s"((unsigned long long)lane_tab),
    "{v252}"(w * 3072 + lane * 16), "s"(medge), "s"((unsigned)pw), "s"((unsigned)(pw >> 32)), "s"(fso), "s"(fuo), "s"(0), "s"(0), "s"(fs8),
    "{v206}"(fwo), "{v207}"(flw), "{v208}"(fst), "{v209}"((lane >> 5) * 8),
    "{v28}"((8 * w + ((lane >> 2) & 7)) * PXB + (lane >> 5) * 64 + 16 * ((lane & 3) ^ ((lane >> 3) & 2))),
    "{v29}"((lane >> 2) * PXB + (lane & 3) * 16 + w * 64), "{v30}"((lane >> 1) * PXB + (lane & 1) * 16 + 256 + w * 32), "{v31}"(18432 + w * 3072 + lane * 8)
  : "v0", "v1", "v2", "v3", "v4", "v5", "v6", "v7", "v8", "v9", "v10", "v11", "v12", "v13", "v14", "v15", "v16", "v17", "v18", "v19", "v20", "v21", "v22", "v23", "v24", "v25", "v26", "v27", "v32", "v33", "v34", "v35", "v36", "v37", "v38", "v39", "v40", "v41", "v42", "v43", "v44", "v45", "v46", "v47", "v48", "v49", "v50", "v51", "v52", "v53", "v54", "v55", "v56", "v57", "v58", "v59", "v60", "v61", "v62", "v63", "v64", "v65", "v66", "v67", "v68", "v69", "v70", "v71", "v72", "v73", "v74", "v75", "v76", "v77", "v78", "v79", "v80", "v81", "v82", "v83", "v84", "v85", "v86", "v87", "v88", "v89", "v90", "v91", "v92", "v93", "v94", "v95", "v96", "v97", "v98", "v99", "v100", "v101", "v102", "v103", "v104", "v105", "v106", "v107", "v108", "v109", "v110", "v111", "v112", "v113", "v114", "v115", "v116", "v117", "v118", "v119", "v120", "v121", "v122", "v123", "v124", "v125", "v126", "v127", "v128", "v129", "v130", "v131", "v132", "v133", "v134", "v135", "v136", "v137", "v138", "v139", "v140", "v141", "v142", "v143", "v144", "v145", "v146", "v147", "v148", "v149", "v150", "v151", "v152", "v153", "v154", "v155", "v156", "v157", "v158", "v159", "v160", "v161", "v162", "v163", "v164", "v165", "v166", "v167", "v168", "v169", "v170", "v171", "v172", "v173", "v174", "v175", "v176", "v177", "v178", "v179", "v180", "v181", "v182", "v183", "v184", "v185", "v186", "v187", "v188", "v189", "v190", "v191", "v192", "v193", "v194", "v195", "v196", "v197", "v198", "v199", "v200", "v201", "v202", "v203", "v204", "v210", "v211", "v212", "v213", "v214", "v215", "v216", "v217", "v218", "v219", "v220", "v221", "v222", "v223", "v224", "v225", "v226", "v227", "v228", "v229", "v230", "v231", "v232", "v233", "v234", "v235", "v236", "v237", "v238", "v239", "v240", "v241", "v242", "v243", "v244", "v245", "v246", "v247", "v248", "v249", "v250", "v251", "v253", "v254", "v255", "a0", "a1", "a2", "a3", "a4", "a5", "a6", "a7", "a8", "a9", "a10", "a11", "a12", "a13", "a14", "a15", "a16", "a17", "a18", "a19", "a20", "a21", "a22", "a23", "a24", "a25", "a26", "a27", "a28", "a29", "a30", "a31", "a32", "a33", "a34", "a35", "a36", "a37", "a38", "a39", "a40", "a41", "a42", "a43", "a44", "a45", "a46", "a47", "a48", "a49", "a50", "a51", "a52", "a53", "a54", "a55", "a56", "a57", "a58", "a59", "a60", "a61", "a62", "a63", "a64", "a65", "a66", "a67", "a68", "a69", "a70", "a71", "a72", "a73", "a74", "a75", "a76", "a77", "a78", "a79", "a80", "a81", "a82", "a83", "a84", "a85", "a86", "a87", "a88", "a89", "a90", "a91", "a92", "a93", "a94", "a95", "a96", "a97", "a98", "a99", "a100", "a101", "a102", "a103", "a104", "a105", "a106", "a107", "a108", "a109", "a110", "a111", "a112", "a113", "a114", "a115", "a116", "a117", "a118", "a119", "a120", "a121", "a122", "a123", "a124", "a125", "a126", "a127", "a128", "a129", "a130", "a131", "a132", "a133", "a134", "a135", "a136", "a137", "a138", "a139", "a140", "a141", "a142", "a143", "a144", "a145", "a146", "a147", "a148", "a149", "a150", "a151", "a152", "a153", "a154", "a155", "a156", "a157", "a158", "a159", "a160", "a161", "a162", "a163", "a164", "a165", "a166", "a167", "a168", "a169", "a170", "a171", "a172", "a173", "a174", "a175", "a176", "a177", "a178", "a179", "a180", "a181", "a182", "a183", "a184", "a185", "a186", "a187", "a188", "a189", "a190", "a191", "a192", "a193", "a194", "a195", "a196", "a197", "a198", "a199", "a200", "a201", "a202", "a203", "a204", "a205", "a206", "a207", "a208", "a209", "a210", "a211", "a212", "a213", "a214", "a215", "a216", "a217", "a218", "a219", "a220", "a221", "a222", "a223", "a224", "a225", "a226", "a227", "a228", "a229", "a230", "a231", "a232", "a233", "a234", "a235", "a236", "a237", "a238", "a239", "a240", "a241", "a242", "a243", "a244", "a245", "a246", "a247", "a248", "a249", "a250", "a251", "a252", "a253", "a254", "a255", "s8", "s9", "s10", "s11", "s12", "s13", "s14", "s15", "s16", "s17", "s18", "s19", "s20", "s21", "s22", "s23", "s24", "s25", "s26", "s27", "s28", "s29", "s30", "s31", "s32", "s33", "s34", "s35", "s36", "s37", "s38", "s39", "s40", "s41", "s42", "s43", "s44", "s45", "s46", "s47", "s48", "s49", "s50", "s51", "s52", "s53", "s54", "s55", "s56", "s57", "s58", "s59", "s60", "s61", "s62", "s63", "s64", "s65", "s66", "s67", "s68", "s69", "s70", "s71", "s72", "s73", "s74", "s75", "s76", "s77", "scc", "vcc", "memory");
}

extern "C" void kernel_launch(void* const* d_in, const int* in_sizes, int n_in, void* d_out, int out_size, void* d_ws,
                              size_t ws_size, hipStream_t stream) {
  wino_main_kernel<<<256, 256, 0, stream>>>((const char*)d_in[0], (char*)d_ws, (char*)d_out, (const char*)d_in[1]);
}
```

```cpp
#include <hip/hip_runtime.h>
#include <hip/hip_bf16.h>
#include <stdint.h>

#define IMG_W 512
#define CH 192
#define PXB 384
#define ROWB (IMG_W * PXB)
#define U_BYTES 1769472

__device__ const __attribute__((aligned(1024))) unsigned lane_tab[6144] = {
  0x0,0x0,0x0,0x0,0x0,0x0,0x0,0x0,0x0,0x0,0x0,0x0,0x0,0x0,0x0,0x0,
  0x0,0x0,0x0,0x0,0x0,0x0,0x0,0x0,0x0,0x0,0x0,0x0,0x0,0x0,0x0,0x0,
  0x0,0x0,0x0,0x0,0x0,0x0,0x0,0x0,0x0,0x0,0x0,0x0,0x0,0x0,0x0,0x0,
  0x0,0x0,0x0,0x0,0x0,0x0,0x0,0x0,0x0,0x0,0x0,0x0,0x0,0x0,0x0,0x0,
  0x0,0x0,0x0,0x0,0x0,0x0,0x0,0x0,0x0,0x0,0x0,0x0,0x0,0x0,0x0,0x0,
  0x0,0x0,0x0,0x0,0x0,0x0,0x0,0x0,0x0,0x0,0x0,0x0,0x0,0x0,0x0,0x0,
  0x0,0x0,0x0,0x0,0x0,0x0,0x0,0x0,0x0,0x0,0x0,0x0,0x0,0x0,0x0,0x0,
  0x0,0x0,0x0,0x0,0x0,0x0,0x0,0x0,0x0,0x0,0x0,0x0,0x0,0x0,0x0,0x0,
  0x0,0x0,0x0,0x0,0x0,0x0,0x0,0x0,0x0,0x0,0x0,0x0,0x0,0x0,0x0,0x0,
  0x0,0x0,0x0,0x0,0x0,0x0,0x0,0x0,0x0,0x0,0x0,0x0,0x0,0x0,0x0,0x0,
  0x0,0x0,0x0,0x0,0x0,0x0,0x0,0x0,0x0,0x0,0x0,0x0,0x0,0x0,0x0,0x0,
  0x0,0x0,0x0,0x0,0x0,0x0,0x0,0x0,0x0,0x0,0x0,0x0,0x0,0x0,0x0,0x0,
  0x0,0x0,0x0,0x0,0x0,0x0,0x0,0x0,0x0,0x0,0x0,0x0,0x0,0x0,0x0,0x0,
  0x0,0x0,0x0,0x0,0x0,0x0,0x0,0x0,0x0,0x0,0x0,0x0,0x0,0x0,0x0,0x0,
  0x0,0x0,0x0,0x0,0x0,0x0,0x0,0x0,0x0,0x0,0x0,0x0,0x0,0x0,0x0,0x0,
  0x0,0x0,0x0,0x0,0x0,0x0,0x0,0x0,0x0,0x0,0x0,0x0,0x0,0x0,0x0,0x0,
  0x0,0xc042,0x0,0x0,0x0,0xc0420000,0x0,0x0,0x0,0x0,0x0,0xc042,0x0,0x0,0x0,0xc0420000,
  0x3f903f90,0x0,0x0,0x0,0x3f900000,0x0,0x3f90,0x0,0x0,0x0,0x3f903f90,0x0,0x0,0x0,0x3f900000,0x0,
  0x0,0x0,0x0,0x0,0x0,0x0,0x0,0x0,0x0,0x0,0x0,0x0,0x0,0x0,0x0,0x0,
  0x0,0x0,0x0,0x0,0x0,0x0,0x0,0x0,0x0,0x0,0x0,0x0,0x0,0x0,0x0,0x0,
  0x0,0x0,0x3f90,0x0,0x0,0x0,0x0,0x0,0x0,0x0,0x0,0x0,0x0,0x0,0x0,0x0,
  0x0,0x0,0x0,0x0,0xc0420000,0x0,0x0,0x0,0x0,0x0,0x0,0x0,0x0,0x0,0xc0420000,0x0,
  0x0,0x3f90,0x0,0x0,0x0,0x3f903f90,0x0,0x0,0x0,0x3f900000,0x0,0x3f90,0x0,0x0,0x0,0x3f903f90,
  0x0,0x3f90,0x0,0x0,0x0,0x3f903f90,0x0,0x0,0x0,0x3f900000,0x0,0x3f90,0x0,0x0,0x0,0x3f903f90,
  0x0,0x0,0x0,0x0,0x0,0x0,0x0,0x0,0x0,0x0,0x0,0x0,0x0,0x0,0x0,0x0,
  0x0,0x0,0x0,0x0,0x0,0x0,0x0,0x0,0x0,0x0,0x0,0x0,0x0,0x0,0x3f90,0x0,
  0x0,0xc042,0x3f900000,0x0,0x0,0xc0420000,0x0,0x0,0x0,0x0,0x0,0xc042,0x0,0x0,0x0,0xc0420000,
  0x0,0xc042,0x3f900000,0x0,0x0,0xc0420000,0x0,0x0,0x0,0x0,0x0,0xc042,0x0,0x0,0x0,0xc0420000,
  0x3f900000,0x0,0x0,0x0,0x3f900000,0x3f90,0x0,0x0,0x0,0x3f90,0x3f900000,0x0,0x0,0x0,0x3f900000,0x3f90,
  0xc042,0x0,0x0,0x0,0x0,0x0,0x0,0x0,0x0,0x0,0xc042,0x0,0x0,0x0,0x0,0x0,
  0x0,0x0,0x0,0x0,0x0,0x0,0x0,0x0,0x0,0x0,0x0,0x0,0x0,0x0,0x0,0x0,
  0x0,0x0,0x0,0x0,0x0,0x0,0x0,0x0,0x0,0x0,0x0,0x0,0x0,0x0,0x0,0x0,
  0xa8000000,0x0,0xa800,0x60,0xa8080040,0x400040,0xa808,0x600000,0xa8100090,0x900090,0xa810,0x6000,0xa81800d0,0xd000d0,0xa818,0x60000000,
  0xa9200120,0x24000120,0xa840,0x40004,0xa9280160,0x24400160,0xa848,0x40400,0xa93001b0,0x249001b0,0xa850,0x4000400,0xa93801f0,0x24d001f0,0xa858,0x4000000,
  0xa8401000,0x2002408,0xa880,0x0,0xa8481040,0x2402448,0xa888,0x0,0xa8501090,0x2902498,0xa890,0x0,0xa85810d0,0x2d024d8,0xa898,0x0,
  0xa9601120,0x2002528,0xa8c0,0x0,0xa9681160,0x2402568,0xa8c8,0x0,0xa97011b0,0x29025b8,0xa8d0,0x0,0xa97811f0,0x2d025f8,0xa8d8,0x0,
  0xea800010,0x100010,0xa920,0x400,0xea880050,0x500050,0xa928,0x0,0xea900080,0x800080,0xa930,0x0,0xea9800c0,0xc000c0,0xa938,0x0,
  0xa9a00130,0x24100130,0xa960,0x0,0xa9a80170,0x24500170,0xa968,0x60000,0xa9b001a0,0x248001a0,0xa970,0x0,0xa9b801e0,0x24c001e0,0xa978,0x6000000,
  0xc0c01010,0x2102418,0xa9a0,0x40,0xc0c81050,0x2502458,0xa9a8,0x400040,0xc0d01080,0x2802488,0xa9b0,0x404000,0xc0d810c0,0x2c024c8,0xa9b8,0x40004000,
  0xc1e01130,0x2102538,0xa9e0,0x40,0xc1e81170,0x2502578,0xa9e8,0x400040,0xc1f011a0,0x28025a8,0xa9f0,0x404000,0xc1f811e0,0x2c025e8,0xa9f8,0x40004000,
  0xae000020,0x200020,0xae00,0x0,0xae080060,0x600060,0xae08,0x0,0xae1000b0,0xb000b0,0xae10,0x0,0xae1800f0,0xf000f0,0xae18,0x0,
  0xeb200100,0x24200100,0xae40,0x0,0xeb280140,0x24600140,0xae48,0x0,0xeb300190,0x24b00190,0xae50,0x0,0xeb3801d0,0x24f001d0,0xae58,0x400,
  0xc0401020,0x2202428,0xae80,0x4000060,0xc0481060,0x2602468,0xae88,0x600000,0xc05010b0,0x2b024b8,0xae90,0x6000,0xc05810f0,0x2f024f8,0xae98,0x60000000,
  0xc1601100,0x2202508,0xaec0,0x4000060,0xc1681140,0x2602548,0xaec8,0x600000,0xc1701190,0x2b02598,0xaed0,0x6000,0xc17811d0,0x2f025d8,0xaed8,0x60000000,
  0xa8800030,0x300030,0xaf20,0x40000,0xa8880070,0x700070,0xaf28,0x40040,0xa89000a0,0xa000a0,0xaf30,0x4000040,0xa89800e0,0xe000e0,0xaf38,0x4004000,
  0xa8c00110,0x24300110,0xaf60,0x6,0xa8c80150,0x24700150,0xaf68,0x0,0xa8d00180,0x24a00180,0xaf70,0x600,0xa8d801c0,0x24e001c0,0xaf78,0x0,
  0xa9e01030,0x2302438,0xafa0,0x0,0xa9e81070,0x2702478,0xafa8,0x0,0xa9f010a0,0x2a024a8,0xafb0,0x0,0xa9f810e0,0x2e024e8,0xafb8,0x0,
  0xae001110,0x2302518,0xafe0,0x0,0xae081150,0x2702558,0xafe8,0x0,0xae101180,0x2a02588,0xaff0,0x0,0xae1811c0,0x2e025c8,0xaff8,0x0,
  0x0,0x0,0x0,0x0,0x0,0x0,0x0,0x0,0x0,0x0,0x0,0x0,0x0,0x0,0x0,0x0,
  0x0,0x0,0x0,0x0,0x0,0x0,0x0,0x0,0x0,0x0,0x0,0x0,0x0,0x0,0x0,0x0,
  0x0,0x0,0x0,0x0,0x0,0x0,0x0,0x0,0x0,0x0,0x0,0x0,0x0,0x0,0x0,0x0,
  0x0,0x0,0x0,0x0,0x0,0x0,0x0,0x0,0x0,0x0,0x0,0x0,0x0,0x0,0x0,0x0,
  0x0,0x0,0x0,0x0,0x0,0x0,0x0,0x0,0x0,0x0,0x0,0x0,0x0,0x0,0x0,0x0,
  0x0,0x0,0x0,0x0,0x0,0x0,0x0,0x0,0x0,0x0,0x0,0x0,0x0,0x0,0x0,0x0,
  0x0,0x0,0x0,0x0,0x0,0x0,0x0,0x0,0x0,0x0,0x0,0x0,0x0,0x0,0x0,0x0,
  0x0,0x0,0x0,0x0,0x0,0x0,0x0,0x0,0x0,0x0,0x0,0x0,0x0,0x0,0x0,0x0,
  0x0,0x0,0x0,0x0,0x0,0x0,0x0,0x0,0x0,0x0,0x0,0x0,0x0,0x0,0x0,0x0,
  0x0,0x0,0x0,0x0,0x0,0x0,0x0,0x0,0x0,0x0,0x0,0x0,0x0,0x0,0x0,0x0,
  0x0,0x0,0x0,0x0,0x0,0x0,0x0,0x0,0x0,0x0,0x0,0x0,0x0,0x0,0x0,0x0,
  0x0,0x0,0x0,0x0,0x0,0x0,0x0,0x0,0x0,0x0,0x0,0x0,0x0,0x0,0x0,0x0,
  0x0,0x0,0x0,0x0,0x0,0x0,0x0,0x0,0x0,0x0,0x0,0x0,0x0,0x0,0x0,0x0,
  0x0,0x0,0x0,0x0,0x0,0x0,0x0,0x0,0x0,0x0,0x0,0x0,0x0,0x0,0x0,0x0,
  0x0,0x0,0x0,0x0,0x0,0x0,0x0,0x0,0x0,0x0,0x0,0x0,0x0,0x0,0x0,0x0,
  0x0,0x0,0x0,0x0,0x0,0x0,0x0,0x0,0x0,0x0,0x0,0x0,0x0,0x0,0x0,0x0,
  0xc0420000,0x0,0x0,0x0,0x0,0x0,0x0,0x0,0x0,0x0,0xc0420000,0x0,0x0,0x0,0x0,0x0,
  0x3f90,0x0,0x0,0x0,0x0,0x0,0x3f90,0x0,0x0,0x0,0x3f90,0x0,0x0,0x0,0x0,0x0,
  0x0,0xc042,0x0,0x0,0x0,0xc0420000,0x0,0x0,0x0,0x0,0x0,0xc042,0x0,0x0,0x0,0xc0420000,
  0x0,0xc042,0x0,0x0,0x0,0xc0420000,0x0,0x0,0x0,0x0,0x0,0xc042,0x0,0x0,0x0,0xc0420000,
  0x0,0x0,0x0,0x3f90,0x0,0x0,0x0,0x0,0x0,0x0,0x0,0x0,0x0,0x0,0x0,0x0,
  0x0,0x0,0x0,0x0,0xc042,0x0,0x0,0x0,0x0,0x0,0x0,0x0,0x0,0x0,0xc042,0x0,
  0x3f900000,0x0,0x0,0x3f900000,0x3f900000,0x0,0x0,0x0,0x0,0x0,0x3f900000,0x0,0x0,0x0,0x3f900000,0x0,
  0x3f900000,0x0,0x0,0x3f900000,0x3f900000,0x0,0x0,0x0,0x0,0x0,0x3f900000,0x0,0x0,0x0,0x3f900000,0x0,
  0x0,0x0,0x0,0x0,0x0,0xc042,0x0,0x0,0x0,0x0,0x0,0x0,0x0,0x0,0x0,0xc042,
  0x3f90,0xc0420000,0x0,0x0,0x3f90,0x0,0x0,0x0,0x0,0x0,0x3f90,0xc0420000,0x0,0x0,0x3f903f90,0x0,
  0x0,0x0,0x0,0x0,0x0,0x0,0x0,0x0,0x0,0x0,0x0,0x0,0x0,0x0,0x0,0x0,
  0x0,0x0,0x0,0x0,0x0,0x0,0x0,0x0,0x0,0x0,0x0,0x0,0x0,0x0,0x0,0x0,
  0x3f90,0x0,0x0,0x0,0x3f903f90,0x0,0x0,0x0,0x3f900000,0x0,0x3f90,0x0,0x0,0x0,0x3f903f90,0x0,
  0x0,0x0,0x0,0x0,0x0,0x0,0x0,0x0,0x0,0x0,0x0,0x0,0x0,0x0,0x0,0x0,
  0x0,0x0,0x0,0x0,0x0,0x3f90,0x0,0x0,0x0,0x3f90,0x0,0x0,0x0,0x0,0x0,0x3f90,
  0x0,0x0,0x0,0x0,0x0,0x3f90,0x0,0x0,0x0,0x3f90,0x0,0x0,0x0,0x0,0x0,0x3f90,
  0xb4000000,0x1200008,0xa820,0x60000,0xb4080040,0x1600048,0xa828,0x0,0xb4100090,0x1b00098,0xa830,0x6000000,0xb41800d0,0x1f000d8,0xa838,0x0,
  0xb4400120,0x25200128,0xa860,0x4,0xb4480160,0x25600168,0xa868,0x400,0xb45001b0,0x25b001b8,0xa870,0x400,0xb45801f0,0x25f001f8,0xa878,0x0,
  0xcc401000,0x3202400,0xa8a0,0x60,0xcc481040,0x3602440,0xa8a8,0x600000,0xcc501090,0x3b02490,0xa8b0,0x6000,0xcc5810d0,0x3f024d0,0xa8b8,0x60000000,
  0xcd601120,0x3202520,0xa8e0,0x60,0xcd681160,0x3602560,0xa8e8,0x600000,0xcd7011b0,0x3b025b0,0xa8f0,0x6000,0xcd7811f0,0x3f025f0,0xa8f8,0x60000000,
  0xb5a00010,0x1300018,0xa900,0x4000,0xb5a80050,0x1700058,0xa908,0x0,0xb5b00080,0x1a00088,0xa910,0x0,0xb5b800c0,0x1e000c8,0xa918,0x0,
  0xccc00130,0x25300138,0xa940,0x0,0xccc80170,0x25700178,0xa948,0x6,0xccd001a0,0x25a001a8,0xa950,0x0,0xccd801e0,0x25e001e8,0xa958,0x600,
  0xa9e01010,0x3302410,0xa980,0x40040000,0xa9e81050,0x3702450,0xa988,0x40000,0xa9f01080,0x3a02480,0xa990,0x4000000,0xa9f810c0,0x3e024c0,0xa998,0x4000000,
  0xc1e01130,0x3302530,0xa9c0,0x40040000,0xc1e81170,0x3702570,0xa9c8,0x40000,0xc1f011a0,0x3a025a0,0xa9d0,0x4000000,0xc1f811e0,0x3e025e0,0xa9d8,0x4000000,
  0xb5200020,0x1000028,0xae20,0x0,0xb5280060,0x1400068,0xae28,0x60,0xb53000b0,0x19000b8,0xae30,0x0,0xb53800f0,0x1d000f8,0xae38,0x6000,
  0xea400100,0x25000108,0xae60,0x600004,0xea480140,0x25400148,0xae68,0x4,0xea500190,0x25900198,0xae70,0x60000400,0xea5801d0,0x25d001d8,0xae78,0x4000400,
  0xb5601020,0x3002420,0xaea0,0x0,0xb5681060,0x3402460,0xaea8,0x0,0xb57010b0,0x39024b0,0xaeb0,0x0,0xb57810f0,0x3d024f0,0xaeb8,0x0,
  0xb4801100,0x3002500,0xaee0,0x0,0xb4881140,0x3402540,0xaee8,0x0,0xb4901190,0x3902590,0xaef0,0x0,0xb49811d0,0x3d025d0,0xaef8,0x0,
  0xb4c00030,0x1100038,0xaf00,0x4,0xb4c80070,0x1500078,0xaf08,0x40004,0xb4d000a0,0x18000a8,0xaf10,0x40400,0xb4d800e0,0x1c000e8,0xaf18,0x4000400,
  0xb5e00110,0x25100118,0xaf40,0x0,0xb5e80150,0x25500158,0xaf48,0x0,0xb5f00180,0x25800188,0xaf50,0x0,0xb5f801c0,0x25c001c8,0xaf58,0x0,
  0xcde01030,0x3102430,0xaf80,0x0,0xcde81070,0x3502470,0xaf88,0x40,0xcdf010a0,0x38024a0,0xaf90,0x40,0xcdf810e0,0x3c024e0,0xaf98,0x4000,
  0xb5201110,0x3102510,0xafc0,0x0,0xb5281150,0x3502550,0xafc8,0x40,0xb5301180,0x3802580,0xafd0,0x40,0xb53811c0,0x3c025c0,0xafd8,0x4000,
  0x0,0x0,0x0,0x0,0x0,0x0,0x0,0x0,0x0,0x0,0x0,0x0,0x0,0x0,0x0,0x0,
  0x0,0x0,0x0,0x0,0x0,0x0,0x0,0x0,0x0,0x0,0x0,0x0,0x0,0x0,0x0,0x0,
  0x0,0x0,0x0,0x0,0x0,0x0,0x0,0x0,0x0,0x0,0x0,0x0,0x0,0x0,0x0,0x0,
  0x0,0x0,0x0,0x0,0x0,0x0,0x0,0x0,0x0,0x0,0x0,0x0,0x0,0x0,0x0,0x0,
  0x0,0x0,0x0,0x0,0x0,0x0,0x0,0x0,0x0,0x0,0x0,0x0,0x0,0x0,0x0,0x0,
  0x0,0x0,0x0,0x0,0x0,0x0,0x0,0x0,0x0,0x0,0x0,0x0,0x0,0x0,0x0,0x0,
  0x0,0x0,0x0,0x0,0x0,0x0,0x0,0x0,0x0,0x0,0x0,0x0,0x0,0x0,0x0,0x0,
  0x0,0x0,0x0,0x0,0x0,0x0,0x0,0x0,0x0,0x0,0x0,0x0,0x0,0x0,0x0,0x0,
  0x0,0x0,0x0,0x0,0x0,0x0,0x0,0x0,0x0,0x0,0x0,0x0,0x0,0x0,0x0,0x0,
  0x0,0x0,0x0,0x0,0x0,0x0,0x0,0x0,0x0,0x0,0x0,0x0,0x0,0x0,0x0,0x0,
  0x0,0x0,0x0,0x0,0x0,0x0,0x0,0x0,0x0,0x0,0x0,0x0,0x0,0x0,0x0,0x0,
  0x0,0x0,0x0,0x0,0x0,0x0,0x0,0x0,0x0,0x0,0x0,0x0,0x0,0x0,0x0,0x0,
  0x0,0x0,0x0,0x0,0x0,0x0,0x0,0x0,0x0,0x0,0x0,0x0,0x0,0x0,0x0,0x0,
  0x0,0x0,0x0,0x0,0x0,0x0,0x0,0x0,0x0,0x0,0x0,0x0,0x0,0x0,0x0,0x0,
  0x0,0x0,0x0,0x0,0x0,0x0,0x0,0x0,0x0,0x0,0x0,0x0,0x0,0x0,0x0,0x0,
  0x0,0x0,0x0,0x0,0x0,0x0,0x0,0x0,0x0,0x0,0x0,0x0,0x0,0x0,0x0,0x0,
  0x3f90,0x3f90,0x0,0x0,0x0,0x3f90,0x3f90,0x0,0x0,0x0,0x3f90,0x3f90,0x0,0x0,0x0,0x3f90,
  0x0,0xc0420000,0x0,0x0,0x0,0x0,0x0,0x0,0x0,0x0,0x0,0xc0420000,0x0,0x0,0x0,0x0,
  0x3f900000,0x0,0x0,0x0,0x0,0x0,0x3f900000,0x0,0x0,0x0,0x3f900000,0x0,0x0,0x0,0x0,0x0,
  0x3f900000,0x0,0x0,0x0,0x0,0x0,0x3f900000,0x0,0x0,0x0,0x3f900000,0x0,0x0,0x0,0x0,0x0,
  0xc042,0x0,0x0,0x0,0xc0420000,0x0,0x0,0x0,0x0,0x0,0xc042,0x0,0x0,0x0,0xc0420000,0x0,
  0x0,0x0,0x0,0x0,0x0,0x3f900000,0x0,0x0,0x0,0x3f900000,0x0,0x0,0x0,0x0,0x0,0x3f900000,
  0x0,0x0,0x0,0x0,0x0,0xc042,0x0,0x0,0x0,0x0,0x0,0x0,0x0,0x0,0x0,0xc042,
  0x0,0x0,0x0,0x0,0x0,0xc042,0x0,0x0,0x0,0x0,0x0,0x0,0x0,0x0,0x0,0xc042,
  0x0,0x0,0x0,0x0,0x0,0x0,0x0,0x0,0x0,0x0,0x0,0x0,0x0,0x0,0x3f90,0x0,
  0x0,0x0,0x0,0x0,0x0,0xc0420000,0x0,0x0,0x0,0x0,0x0,0x0,0x0,0x0,0x0,0xc0420000,
  0x0,0x3f90,0x0,0x0,0x0,0x3f90,0x0,0x0,0x0,0x0,0x0,0x3f90,0x0,0x0,0x3f900000,0x3f90,
  0x0,0x3f90,0x0,0x0,0x0,0x3f90,0x0,0x0,0x0,0x0,0x0,0x3f90,0x0,0x0,0x3f900000,0x3f90,
  0x0,0x0,0x0,0x0,0x0,0x0,0x0,0x0,0x0,0x0,0x0,0x0,0x0,0x0,0x0,0x0,
  0x0,0x3f90,0x3f900000,0x0,0x0,0x3f90,0x0,0x0,0x0,0x0,0x0,0x3f90,0x0,0x0,0x0,0x3f90,
  0xc042,0x0,0x0,0x0,0x0,0x0,0x0,0x0,0x0,0x0,0xc042,0x0,0x0,0x0,0x0,0x0,
  0xc042,0x0,0x0,0x0,0x0,0x0,0x0,0x0,0x0,0x0,0xc042,0x0,0x0,0x0,0x0,0x0,
  0xc0000000,0x26001200,0x19200,0x44,0xc0080040,0x26401240,0x19208,0x440,0xc0100090,0x26901290,0x19210,0x4400,0xc01800d0,0x26d012d0,0x19218,0x4000,
  0xd8000120,0x4001320,0x19240,0x600000,0xd8080160,0x4401360,0x19248,0x0,0xd81001b0,0x49013b0,0x19250,0x60000000,0xd81801f0,0x4d013f0,0x19258,0x0,
  0xc1201000,0x28003608,0x19280,0x40000,0xc1281040,0x28403648,0x19288,0x4000000,0xc1301090,0x28903698,0x19290,0x4000000,0xc13810d0,0x28d036d8,0x19298,0x0,
  0xd8401120,0x28003728,0x192c0,0x40000,0xd8481160,0x28403768,0x192c8,0x4000000,0xd85011b0,0x289037b8,0x192d0,0x4000000,0xd85811f0,0x28d037f8,0x192d8,0x0,
  0xc0800010,0x26101210,0x19320,0x6,0xc0880050,0x26501250,0x19328,0x60000,0xc0900080,0x26801280,0x19330,0x600,0xc09800c0,0x26c012c0,0x19338,0x6000000,
  0xc1a00130,0x4101330,0x19360,0x0,0xc1a80170,0x4501370,0x19368,0x400000,0xc1b001a0,0x48013a0,0x19370,0x400000,0xc1b801e0,0x4c013e0,0x19378,0x40000000,
  0xd9a01010,0x28103618,0x193a0,0x0,0xd9a81050,0x28503658,0x193a8,0x60,0xd9b01080,0x28803688,0x193b0,0x0,0xd9b810c0,0x28c036c8,0x193b8,0x6000,
  0xd9e01130,0x28103738,0x193e0,0x0,0xd9e81170,0x28503778,0x193e8,0x60,0xd9f011a0,0x288037a8,0x193f0,0x0,0xd9f811e0,0x28c037e8,0x193f8,0x6000,
  0xc6000020,0x26201220,0x19800,0x0,0xc6080060,0x26601260,0x19808,0x0,0xc61000b0,0x26b012b0,0x19810,0x0,0xc61800f0,0x26f012f0,0x19818,0x400,
  0xde000100,0x4201300,0x19840,0x0,0xde080140,0x4601340,0x19848,0x600000,0xde100190,0x4b01390,0x19850,0x0,0xde1801d0,0x4f013d0,0x19858,0x60000000,
  0xd9201020,0x28203628,0x19880,0x40,0xd9281060,0x28603668,0x19888,0x40,0xd93010b0,0x28b036b8,0x19890,0x4000,0xd93810f0,0x28f036f8,0x19898,0x4004000,
  0xd9601100,0x28203708,0x198c0,0x40,0xd9681140,0x28603748,0x198c8,0x40,0xd9701190,0x28b03798,0x198d0,0x4000,0xd97811d0,0x28f037d8,0x198d8,0x4004000,
  0xd8800030,0x26301230,0x19920,0x0,0xd8880070,0x26701270,0x19928,0x0,0xd89000a0,0x26a012a0,0x19930,0x0,0xd89800e0,0x26e012e0,0x19938,0x0,
  0xeba00110,0x4301310,0x19960,0x4000040,0xeba80150,0x4701350,0x19968,0x40,0xebb00180,0x4a01380,0x19970,0x4000,0xebb801c0,0x4e013c0,0x19978,0x4000,
  0xd8c01030,0x28303638,0x199a0,0x6,0xd8c81070,0x28703678,0x199a8,0x0,0xd8d010a0,0x28a036a8,0x199b0,0x600,0xd8d810e0,0x28e036e8,0x199b8,0x0,
  0xc6001110,0x28303718,0x199e0,0x6,0xc6081150,0x28703758,0x199e8,0x0,0xc6101180,0x28a03788,0x199f0,0x600,0xc61811c0,0x28e037c8,0x199f8,0x0,
  0x0,0x0,0x0,0x0,0x0,0x0,0x0,0x0,0x0,0x0,0x0,0x0,0x0,0x0,0x0,0x0,
  0x0,0x0,0x0,0x0,0x0,0x0,0x0,0x0,0x0,0x0,0x0,0x0,0x0,0x0,0x0,0x0,
  0x0,0x0,0x0,0x0,0x0,0x0,0x0,0x0,0x0,0x0,0x0,0x0,0x0,0x0,0x0,0x0,
  0x0,0x0,0x0,0x0,0x0,0x0,0x0,0x0,0x0,0x0,0x0,0x0,0x0,0x0,0x0,0x0,
  0x0,0x0,0x0,0x0,0x0,0x0,0x0,0x0,0x0,0x0,0x0,0x0,0x0,0x0,0x0,0x0,
  0x0,0x0,0x0,0x0,0x0,0x0,0x0,0x0,0x0,0x0,0x0,0x0,0x0,0x0,0x0,0x0,
  0x0,0x0,0x0,0x0,0x0,0x0,0x0,0x0,0x0,0x0,0x0,0x0,0x0,0x0,0x0,0x0,
  0x0,0x0,0x0,0x0,0x0,0x0,0x0,0x0,0x0,0x0,0x0,0x0,0x0,0x0,0x0,0x0,
  0x0,0x0,0x0,0x0,0x0,0x0,0x0,0x0,0x0,0x0,0x0,0x0,0x0,0x0,0x0,0x0,
  0x0,0x0,0x0,0x0,0x0,0x0,0x0,0x0,0x0,0x0,0x0,0x0,0x0,0x0,0x0,0x0,
  0x0,0x0,0x0,0x0,0x0,0x0,0x0,0x0,0x0,0x0,0x0,0x0,0x0,0x0,0x0,0x0,
  0x0,0x0,0x0,0x0,0x0,0x0,0x0,0x0,0x0,0x0,0x0,0x0,0x0,0x0,0x0,0x0,
  0x0,0x0,0x0,0x0,0x0,0x0,0x0,0x0,0x0,0x0,0x0,0x0,0x0,0x0,0x0,0x0,
  0x0,0x0,0x0,0x0,0x0,0x0,0x0,0x0,0x0,0x0,0x0,0x0,0x0,0x0,0x0,0x0,
  0x0,0x0,0x0,0x0,0x0,0x0,0x0,0x0,0x0,0x0,0x0,0x0,0x0,0x0,0x0,0x0,
  0x0,0x0,0x0,0x0,0x0,0x0,0x0,0x0,0x0,0x0,0x0,0x0,0x0,0x0,0x0,0x0,
  0x3f903f90,0x0,0x0,0x0,0x3f900000,0x0,0x3f90,0x0,0x0,0x0,0x3f903f90,0x0,0x0,0x0,0x3f900000,0x0,
  0x0,0xc042,0x0,0x0,0x0,0x0,0x0,0x0,0x0,0x0,0x0,0xc042,0x0,0x0,0x0,0x0,
  0x0,0x0,0x0,0x0,0x0,0x0,0x0,0x0,0x0,0x0,0x0,0x0,0x0,0x0,0x0,0x3f900000,
  0x0,0x0,0x0,0x0,0x0,0x0,0x0,0x0,0x0,0x0,0x0,0x0,0x0,0x0,0x0,0x3f900000,
  0x0,0x0,0x0,0x0,0x0,0x0,0x0,0x0,0x0,0x0,0x0,0x0,0x0,0x0,0x0,0x0,
  0x0,0x3f90,0x0,0x3f900000,0x0,0x3f90,0x0,0x0,0x0,0x0,0x0,0x3f90,0x0,0x0,0x0,0x3f90,
  0xc042,0x0,0x0,0x0,0xc0420000,0x0,0x0,0x0,0x0,0x0,0xc042,0x0,0x0,0x0,0xc0420000,0x0,
  0xc042,0x0,0x0,0x0,0xc0420000,0x0,0x0,0x0,0x0,0x0,0xc042,0x0,0x0,0x0,0xc0420000,0x0,
  0x0,0xc0420000,0x0,0x0,0x0,0x0,0x0,0x0,0x0,0x0,0x0,0xc0420000,0x0,0x0,0x0,0x0,
  0x0,0x0,0x0,0x0,0x0,0xc042,0x0,0x0,0x0,0x0,0x0,0x0,0x0,0x0,0x0,0xc042,
  0x3f903f90,0x0,0x0,0x0,0x3f900000,0x0,0x3f90,0x0,0x0,0x0,0x3f903f90,0x0,0x0,0x0,0x3f900000,0x0,
  0x3f903f90,0x0,0x0,0x0,0x3f900000,0x0,0x3f90,0x0,0x0,0x0,0x3f903f90,0x0,0x0,0x0,0x3f900000,0x0,
  0x0,0x0,0x0,0x0,0xc042,0x0,0x0,0x0,0x0,0x0,0x0,0x0,0x0,0x0,0x3f90c042,0x0,
  0x0,0x0,0x0,0x0,0x0,0x3f90,0x0,0x0,0x0,0x3f90,0x0,0x0,0x0,0x0,0x0,0x3f90,
  0x0,0x0,0x0,0x0,0x0,0x0,0x0,0x0,0x0,0x0,0x0,0x0,0x0,0x0,0x0,0x0,
  0x0,0x0,0x0,0x0,0x0,0x0,0x0,0x0,0x0,0x0,0x0,0x0,0x0,0x0,0x0,0x0,
  0xcc000000,0x27201208,0x19220,0x40004,0xcc080040,0x27601248,0x19228,0x40400,0xcc100090,0x27b01298,0x19230,0x4000400,0xcc1800d0,0x27f012d8,0x19238,0x4000000,
  0xcd200120,0x5201328,0x19260,0x60,0xcd280160,0x5601368,0x19268,0x0,0xcd3001b0,0x5b013b8,0x19270,0x6000,0xcd3801f0,0x5f013f8,0x19278,0x0,
  0xe4401000,0x29203600,0x192a0,0x0,0xe4481040,0x29603640,0x192a8,0x0,0xe4501090,0x29b03690,0x192b0,0x0,0xe45810d0,0x29f036d0,0x192b8,0x40000000,
  0xeb601120,0x29203720,0x192e0,0x0,0xeb681160,0x29603760,0x192e8,0x0,0xeb7011b0,0x29b037b0,0x192f0,0x0,0xeb7811f0,0x29f037f0,0x192f8,0x40000000,
  0xe4800010,0x27301218,0x19300,0x0,0xe4880050,0x27701258,0x19308,0x0,0xe4900080,0x27a01288,0x19310,0x0,0xe49800c0,0x27e012c8,0x19318,0x0,
  0xe5a00130,0x5301338,0x19340,0x40000040,0xe5a80170,0x5701378,0x19348,0x40,0xe5b001a0,0x5a013a8,0x19350,0x4000,0xe5b801e0,0x5e013e8,0x19358,0x4000,
  0xe4c01010,0x29303610,0x19380,0x6,0xe4c81050,0x29703650,0x19388,0x60000,0xe4d01080,0x29a03680,0x19390,0x600,0xe4d810c0,0x29e036c0,0x19398,0x6000000,
  0xd9e01130,0x29303730,0x193c0,0x6,0xd9e81170,0x29703770,0x193c8,0x60000,0xd9f011a0,0x29a037a0,0x193d0,0x600,0xd9f811e0,0x29e037e0,0x193d8,0x6000000,
  0xe4000020,0x27001228,0x19820,0x600000,0xe4080060,0x27401268,0x19828,0x0,0xe41000b0,0x279012b8,0x19830,0x60000000,0xe41800f0,0x27d012f8,0x19838,0x0,
  0xe5200100,0x5001308,0x19860,0x0,0xe5280140,0x5401348,0x19868,0x60,0xe5300190,0x5901398,0x19870,0x0,0xe53801d0,0x5d013d8,0x19878,0x6000,
  0xe5601020,0x29003620,0x198a0,0x40004,0xe5681060,0x29403660,0x198a8,0x40400,0xe57010b0,0x299036b0,0x198b0,0x4000400,0xe57810f0,0x29d036f0,0x198b8,0x4000000,
  0xcc801100,0x29003700,0x198e0,0x40004,0xcc881140,0x29403740,0x198e8,0x40400,0xcc901190,0x29903790,0x198f0,0x4000400,0xcc9811d0,0x29d037d0,0x198f8,0x4000000,
  0xcda00030,0x27101238,0x19900,0x0,0xcda80070,0x27501278,0x19908,0x6,0xcdb000a0,0x278012a8,0x19910,0x0,0xcdb800e0,0x27c012e8,0x19918,0x4000600,
  0xeac00110,0x5101318,0x19940,0x0,0xeac80150,0x5501358,0x19948,0x40,0xead00180,0x5801388,0x19950,0x40,0xead801c0,0x5c013c8,0x19958,0x4000,
  0xe5e01030,0x29103630,0x19980,0x0,0xe5e81070,0x29503670,0x19988,0x0,0xe5f010a0,0x298036a0,0x19990,0x0,0xe5f810e0,0x29c036e0,0x19998,0x0,
  0xe4001110,0x29103710,0x199c0,0x0,0xe4081150,0x29503750,0x199c8,0x0,0xe4101180,0x29803780,0x199d0,0x0,0xe41811c0,0x29c037c0,0x199d8,0x0,
  0x0,0x0,0x800,0x10000000,0x10,0x90,0x890,0x10000000,0x20,0x120,0x920,0x10000000,0x30,0x1b0,0x9b0,0x10000000,
  0x40,0x240,0xa40,0x20003180,0x50,0x2d0,0xad0,0x20003190,0x60,0x360,0xb60,0x200031a0,0x70,0x3f0,0xbf0,0x200031b0,
  0x610,0xc00,0x1400,0x40000000,0x600,0xc90,0x1490,0x40000010,0x630,0xd20,0x1520,0x40000020,0x620,0xdb0,0x15b0,0x40000030,
  0x650,0xe40,0x1640,0x20033180,0x640,0xed0,0x16d0,0x20033190,0x670,0xf60,0x1760,0x200331a0,0x660,0xff0,0x17f0,0x200331b0,
  0xc20,0x10,0x808,0x40030020,0xc30,0x80,0x898,0x40030030,0xc00,0x130,0x928,0x40030000,0xc10,0x1a0,0x9b8,0x40030010,
  0xc60,0x250,0xa48,0x200631a0,0xc70,0x2c0,0xad8,0x200631b0,0xc40,0x370,0xb68,0x20063180,0xc50,0x3e0,0xbf8,0x20063190,
  0x1230,0xc10,0x1408,0x40060020,0x1220,0xc80,0x1498,0x40060030,0x1210,0xd30,0x1528,0x40060000,0x1200,0xda0,0x15b8,0x40060010,
  0x1270,0xe50,0x1648,0x10000000,0x1260,0xec0,0x16d8,0x10000000,0x1250,0xf70,0x1768,0x10000000,0x1240,0xfe0,0x17f8,0x10000000,
  0x1840,0x20,0x810,0x10000000,0x1850,0xb0,0x880,0x10000000,0x1860,0x100,0x930,0x10000000,0x1870,0x190,0x9a0,0x10000000,
  0x1800,0x260,0xa50,0x200031c0,0x1810,0x2f0,0xac0,0x200031d0,0x1820,0x340,0xb70,0x200031e0,0x1830,0x3d0,0xbe0,0x200031f0,
  0x1e50,0xc20,0x1410,0x40000040,0x1e40,0xcb0,0x1480,0x40000050,0x1e70,0xd00,0x1530,0x40000060,0x1e60,0xd90,0x15a0,0x40000070,
  0x1e10,0xe60,0x1650,0x200331c0,0x1e00,0xef0,0x16c0,0x200331d0,0x1e30,0xf40,0x1770,0x200331e0,0x1e20,0xfd0,0x17e0,0x200331f0,
  0x2460,0x30,0x818,0x40030060,0x2470,0xa0,0x888,0x40030070,0x2440,0x110,0x938,0x40030040,0x2450,0x180,0x9a8,0x40030050,
  0x2420,0x270,0xa58,0x200631e0,0x2430,0x2e0,0xac8,0x200631f0,0x2400,0x350,0xb78,0x200631c0,0x2410,0x3c0,0xbe8,0x200631d0,
  0x2a70,0xc30,0x1418,0x40060060,0x2a60,0xca0,0x1488,0x40060070,0x2a50,0xd10,0x1538,0x40060040,0x2a40,0xd80,0x15a8,0x40060050,
  0x2a30,0xe70,0x1658,0x10000000,0x2a20,0xee0,0x16c8,0x10000000,0x2a10,0xf50,0x1778,0x10000000,0x2a00,0xfc0,0x17e8,0x10000000,
  0xbfd8bf90,0x3f403f00,0x0,0x0,0x0,0x0,0x0,0x0,0x0,0x0,0x0,0x0,0x0,0x0,0x0,0x0,
  0x0,0x0,0xbfd8bf90,0x3f403f00,0x0,0x0,0x0,0x0,0x0,0x0,0x0,0x0,0x0,0x0,0x0,0x0,
  0xbfd83f90,0x3f40bf00,0x0,0x0,0x0,0x0,0x0,0x0,0x0,0x0,0x0,0x0,0x0,0x0,0x0,0x0,
  0x0,0x0,0xbfd83f90,0x3f40bf00,0x0,0x0,0x0,0x0,0x0,0x0,0x0,0x0,0x0,0x0,0x0,0x0,
  0x0,0x0,0x0,0x0,0xbfd8bf90,0x3f403f00,0x0,0x0,0x0,0x0,0x0,0x0,0x0,0x0,0x0,0x0,
  0x0,0x0,0x0,0x0,0x0,0x0,0xbfd8bf90,0x3f403f00,0x0,0x0,0x0,0x0,0x0,0x0,0x0,0x0,
  0x0,0x0,0x0,0x0,0xbfd83f90,0x3f40bf00,0x0,0x0,0x0,0x0,0x0,0x0,0x0,0x0,0x0,0x0,
  0x0,0x0,0x0,0x0,0x0,0x0,0xbfd83f90,0x3f40bf00,0x0,0x0,0x0,0x0,0x0,0x0,0x0,0x0,
  0x0,0x0,0x0,0x0,0x0,0x0,0x0,0x0,0xbfd8bf90,0x3f403f00,0x0,0x0,0x0,0x0,0x0,0x0,
  0x0,0x0,0x0,0x0,0x0,0x0,0x0,0x0,0x0,0x0,0xbfd8bf90,0x3f403f00,0x0,0x0,0x0,0x0,
  0x0,0x0,0x0,0x0,0x0,0x0,0x0,0x0,0xbfd83f90,0x3f40bf00,0x0,0x0,0x0,0x0,0x0,0x0,
  0x0,0x0,0x0,0x0,0x0,0x0,0x0,0x0,0x0,0x0,0xbfd83f90,0x3f40bf00,0x0,0x0,0x0,0x0,
  0x0,0x0,0x0,0x0,0x0,0x0,0x0,0x0,0x0,0x0,0x0,0x0,0xbfd8bf90,0x3f403f00,0x0,0x0,
  0x0,0x0,0x0,0x0,0x0,0x0,0x0,0x0,0x0,0x0,0x0,0x0,0x0,0x0,0xbfd8bf90,0x3f403f00,
  0x0,0x0,0x0,0x0,0x0,0x0,0x0,0x0,0x0,0x0,0x0,0x0,0xbfd83f90,0x3f40bf00,0x0,0x0,
  0x0,0x0,0x0,0x0,0x0,0x0,0x0,0x0,0x0,0x0,0x0,0x0,0x0,0x0,0xbfd83f90,0x3f40bf00,
  0xa800,0xa820,0x0,0xa800,0xa808,0xa828,0x40,0xa840,0xa810,0xa830,0x90,0xa890,0xa818,0xa838,0xd0,0xa8d0,
  0xa920,0xa900,0x2400,0xae00,0xa928,0xa908,0x2440,0xae40,0xa930,0xa910,0x2490,0xae90,0xa938,0xa918,0x24d0,0xaed0,
  0xa840,0xa860,0x200,0xb400,0xa848,0xa868,0x240,0xb440,0xa850,0xa870,0x290,0xb490,0xa858,0xa878,0x2d0,0xb4d0,
  0xa960,0xa940,0x200,0xb400,0xa968,0xa948,0x240,0xb440,0xa970,0xa950,0x290,0xb490,0xa978,0xa958,0x2d0,0xb4d0,
  0xea80,0xeaa0,0x10,0xa810,0xea88,0xeaa8,0x50,0xa850,0xea90,0xeab0,0x80,0xa880,0xea98,0xeab8,0xc0,0xa8c0,
  0xa9a0,0xa980,0x2410,0xae10,0xa9a8,0xa988,0x2450,0xae50,0xa9b0,0xa990,0x2480,0xae80,0xa9b8,0xa998,0x24c0,0xaec0,
  0xc0c0,0xc0e0,0x210,0xb410,0xc0c8,0xc0e8,0x250,0xb450,0xc0d0,0xc0f0,0x280,0xb480,0xc0d8,0xc0f8,0x2c0,0xb4c0,
  0xc1e0,0xc1c0,0x210,0xb410,0xc1e8,0xc1c8,0x250,0xb450,0xc1f0,0xc1d0,0x280,0xb480,0xc1f8,0xc1d8,0x2c0,0xb4c0,
  0xae00,0xae20,0x20,0xa820,0xae08,0xae28,0x60,0xa860,0xae10,0xae30,0xb0,0xa8b0,0xae18,0xae38,0xf0,0xa8f0,
  0xeb20,0xeb00,0x2420,0xae20,0xeb28,0xeb08,0x2460,0xae60,0xeb30,0xeb10,0x24b0,0xaeb0,0xeb38,0xeb18,0x24f0,0xaef0,
  0xc040,0xc060,0x220,0xb420,0xc048,0xc068,0x260,0xb460,0xc050,0xc070,0x2b0,0xb4b0,0xc058,0xc078,0x2f0,0xb4f0,
  0xc160,0xc140,0x220,0xb420,0xc168,0xc148,0x260,0xb460,0xc170,0xc150,0x2b0,0xb4b0,0xc178,0xc158,0x2f0,0xb4f0,
  0xa880,0xa8a0,0x30,0xa830,0xa888,0xa8a8,0x70,0xa870,0xa890,0xa8b0,0xa0,0xa8a0,0xa898,0xa8b8,0xe0,0xa8e0,
  0xa8c0,0xa8e0,0x2430,0xae30,0xa8c8,0xa8e8,0x2470,0xae70,0xa8d0,0xa8f0,0x24a0,0xaea0,0xa8d8,0xa8f8,0x24e0,0xaee0,
  0xa9e0,0xa9c0,0x230,0xb430,0xa9e8,0xa9c8,0x270,0xb470,0xa9f0,0xa9d0,0x2a0,0xb4a0,0xa9f8,0xa9d8,0x2e0,0xb4e0,
  0xae00,0xae20,0x230,0xb430,0xae08,0xae28,0x270,0xb470,0xae10,0xae30,0x2a0,0xb4a0,0xae18,0xae38,0x2e0,0xb4e0,
  0x180000,0x40,0x820,0x10000000,0x180010,0xd0,0x8b0,0x10000000,0x180020,0x160,0x900,0x10000000,0x180030,0x1f0,0x990,0x10000000,
  0x180040,0x200,0xa60,0x20093180,0x180050,0x290,0xaf0,0x20093190,0x180060,0x320,0xb40,0x200931a0,0x180070,0x3b0,0xbd0,0x200931b0,
  0x180610,0xc40,0x1420,0x40090000,0x180600,0xcd0,0x14b0,0x40090010,0x180630,0xd60,0x1500,0x40090020,0x180620,0xdf0,0x1590,0x40090030,
  0x180650,0xe00,0x1660,0x200c3180,0x180640,0xe90,0x16f0,0x200c3190,0x180670,0xf20,0x1740,0x200c31a0,0x180660,0xfb0,0x17d0,0x200c31b0,
  0x180c20,0x50,0x828,0x400c0020,0x180c30,0xc0,0x8b8,0x400c0030,0x180c00,0x170,0x908,0x400c0000,0x180c10,0x1e0,0x998,0x400c0010,
  0x180c60,0x210,0xa68,0x200f31a0,0x180c70,0x280,0xaf8,0x200f31b0,0x180c40,0x330,0xb48,0x200f3180,0x180c50,0x3a0,0xbd8,0x200f3190,
  0x181230,0xc50,0x1428,0x400f0020,0x181220,0xcc0,0x14b8,0x400f0030,0x181210,0xd70,0x1508,0x400f0000,0x181200,0xde0,0x1598,0x400f0010,
  0x181270,0xe10,0x1668,0x10000000,0x181260,0xe80,0x16f8,0x10000000,0x181250,0xf30,0x1748,0x10000000,0x181240,0xfa0,0x17d8,0x10000000,
  0x181840,0x60,0x830,0x10000000,0x181850,0xf0,0x8a0,0x10000000,0x181860,0x140,0x910,0x10000000,0x181870,0x1d0,0x980,0x10000000,
  0x181800,0x220,0xa70,0x200931c0,0x181810,0x2b0,0xae0,0x200931d0,0x181820,0x300,0xb50,0x200931e0,0x181830,0x390,0xbc0,0x200931f0,
  0x181e50,0xc60,0x1430,0x40090040,0x181e40,0xcf0,0x14a0,0x40090050,0x181e70,0xd40,0x1510,0x40090060,0x181e60,0xdd0,0x1580,0x40090070,
  0x181e10,0xe20,0x1670,0x200c31c0,0x181e00,0xeb0,0x16e0,0x200c31d0,0x181e30,0xf00,0x1750,0x200c31e0,0x181e20,0xf90,0x17c0,0x200c31f0,
  0x182460,0x70,0x838,0x400c0060,0x182470,0xe0,0x8a8,0x400c0070,0x182440,0x150,0x918,0x400c0040,0x182450,0x1c0,0x988,0x400c0050,
  0x182420,0x230,0xa78,0x200f31e0,0x182430,0x2a0,0xae8,0x200f31f0,0x182400,0x310,0xb58,0x200f31c0,0x182410,0x380,0xbc8,0x200f31d0,
  0x182a70,0xc70,0x1438,0x400f0060,0x182a60,0xce0,0x14a8,0x400f0070,0x182a50,0xd50,0x1518,0x400f0040,0x182a40,0xdc0,0x1588,0x400f0050,
  0x182a30,0xe30,0x1678,0x10000000,0x182a20,0xea0,0x16e8,0x10000000,0x182a10,0xf10,0x1758,0x10000000,0x182a00,0xf80,0x17c8,0x10000000,
  0xbfd8bf90,0x3f403f00,0x0,0x0,0x0,0x0,0x0,0x0,0x0,0x0,0x0,0x0,0x0,0x0,0x0,0x0,
  0x0,0x0,0xbfd8bf90,0x3f403f00,0x0,0x0,0x0,0x0,0x0,0x0,0x0,0x0,0x0,0x0,0x0,0x0,
  0xbfd83f90,0x3f40bf00,0x0,0x0,0x0,0x0,0x0,0x0,0x0,0x0,0x0,0x0,0x0,0x0,0x0,0x0,
  0x0,0x0,0xbfd83f90,0x3f40bf00,0x0,0x0,0x0,0x0,0x0,0x0,0x0,0x0,0x0,0x0,0x0,0x0,
  0x0,0x0,0x0,0x0,0xbfd8bf90,0x3f403f00,0x0,0x0,0x0,0x0,0x0,0x0,0x0,0x0,0x0,0x0,
  0x0,0x0,0x0,0x0,0x0,0x0,0xbfd8bf90,0x3f403f00,0x0,0x0,0x0,0x0,0x0,0x0,0x0,0x0,
  0x0,0x0,0x0,0x0,0xbfd83f90,0x3f40bf00,0x0,0x0,0x0,0x0,0x0,0x0,0x0,0x0,0x0,0x0,
  0x0,0x0,0x0,0x0,0x0,0x0,0xbfd83f90,0x3f40bf00,0x0,0x0,0x0,0x0,0x0,0x0,0x0,0x0,
  0x0,0x0,0x0,0x0,0x0,0x0,0x0,0x0,0xbfd8bf90,0x3f403f00,0x0,0x0,0x0,0x0,0x0,0x0,
  0x0,0x0,0x0,0x0,0x0,0x0,0x0,0x0,0x0,0x0,0xbfd8bf90,0x3f403f00,0x0,0x0,0x0,0x0,
  0x0,0x0,0x0,0x0,0x0,0x0,0x0,0x0,0xbfd83f90,0x3f40bf00,0x0,0x0,0x0,0x0,0x0,0x0,
  0x0,0x0,0x0,0x0,0x0,0x0,0x0,0x0,0x0,0x0,0xbfd83f90,0x3f40bf00,0x0,0x0,0x0,0x0,
  0x0,0x0,0x0,0x0,0x0,0x0,0x0,0x0,0x0,0x0,0x0,0x0,0xbfd8bf90,0x3f403f00,0x0,0x0,
  0x0,0x0,0x0,0x0,0x0,0x0,0x0,0x0,0x0,0x0,0x0,0x0,0x0,0x0,0xbfd8bf90,0x3f403f00,
  0x0,0x0,0x0,0x0,0x0,0x0,0x0,0x0,0x0,0x0,0x0,0x0,0xbfd83f90,0x3f40bf00,0x0,0x0,
  0x0,0x0,0x0,0x0,0x0,0x0,0x0,0x0,0x0,0x0,0x0,0x0,0x0,0x0,0xbfd83f90,0x3f40bf00,
  0xb400,0xb420,0x120,0xa920,0xb408,0xb428,0x160,0xa960,0xb410,0xb430,0x1b0,0xa9b0,0xb418,0xb438,0x1f0,0xa9f0,
  0xb440,0xb460,0x2520,0xaf20,0xb448,0xb468,0x2560,0xaf60,0xb450,0xb470,0x25b0,0xafb0,0xb458,0xb478,0x25f0,0xaff0,
  0xcc40,0xcc60,0x320,0xb520,0xcc48,0xcc68,0x360,0xb560,0xcc50,0xcc70,0x3b0,0xb5b0,0xcc58,0xcc78,0x3f0,0xb5f0,
  0xcd60,0xcd40,0x320,0xb520,0xcd68,0xcd48,0x360,0xb560,0xcd70,0xcd50,0x3b0,0xb5b0,0xcd78,0xcd58,0x3f0,0xb5f0,
  0xb5a0,0xb580,0x130,0xa930,0xb5a8,0xb588,0x170,0xa970,0xb5b0,0xb590,0x1a0,0xa9a0,0xb5b8,0xb598,0x1e0,0xa9e0,
  0xccc0,0xcce0,0x2530,0xaf30,0xccc8,0xcce8,0x2570,0xaf70,0xccd0,0xccf0,0x25a0,0xafa0,0xccd8,0xccf8,0x25e0,0xafe0,
  0xa9e0,0xa9c0,0x330,0xb530,0xa9e8,0xa9c8,0x370,0xb570,0xa9f0,0xa9d0,0x3a0,0xb5a0,0xa9f8,0xa9d8,0x3e0,0xb5e0,
  0xc1e0,0xc1c0,0x330,0xb530,0xc1e8,0xc1c8,0x370,0xb570,0xc1f0,0xc1d0,0x3a0,0xb5a0,0xc1f8,0xc1d8,0x3e0,0xb5e0,
  0xb520,0xb500,0x100,0xa900,0xb528,0xb508,0x140,0xa940,0xb530,0xb510,0x190,0xa990,0xb538,0xb518,0x1d0,0xa9d0,
  0xea40,0xea60,0x2500,0xaf00,0xea48,0xea68,0x2540,0xaf40,0xea50,0xea70,0x2590,0xaf90,0xea58,0xea78,0x25d0,0xafd0,
  0xb560,0xb540,0x300,0xb500,0xb568,0xb548,0x340,0xb540,0xb570,0xb550,0x390,0xb590,0xb578,0xb558,0x3d0,0xb5d0,
  0xb480,0xb4a0,0x300,0xb500,0xb488,0xb4a8,0x340,0xb540,0xb490,0xb4b0,0x390,0xb590,0xb498,0xb4b8,0x3d0,0xb5d0,
  0xb4c0,0xb4e0,0x110,0xa910,0xb4c8,0xb4e8,0x150,0xa950,0xb4d0,0xb4f0,0x180,0xa980,0xb4d8,0xb4f8,0x1c0,0xa9c0,
  0xb5e0,0xb5c0,0x2510,0xaf10,0xb5e8,0xb5c8,0x2550,0xaf50,0xb5f0,0xb5d0,0x2580,0xaf80,0xb5f8,0xb5d8,0x25c0,0xafc0,
  0xcde0,0xcdc0,0x310,0xb510,0xcde8,0xcdc8,0x350,0xb550,0xcdf0,0xcdd0,0x380,0xb580,0xcdf8,0xcdd8,0x3c0,0xb5c0,
  0xb520,0xb500,0x310,0xb510,0xb528,0xb508,0x350,0xb550,0xb530,0xb510,0x380,0xb580,0xb538,0xb518,0x3c0,0xb5c0,
  0x0,0x400,0x840,0x10000000,0x10,0x490,0x8d0,0x10000000,0x20,0x520,0x960,0x10000000,0x30,0x5b0,0x9f0,0x10000000,
  0x40,0x640,0xa00,0x20123180,0x50,0x6d0,0xa90,0x20123190,0x60,0x760,0xb20,0x201231a0,0x70,0x7f0,0xbb0,0x201231b0,
  0x610,0x1000,0x1440,0x40120000,0x600,0x1090,0x14d0,0x40120010,0x630,0x1120,0x1560,0x40120020,0x620,0x11b0,0x15f0,0x40120030,
  0x650,0x1240,0x1600,0x20153180,0x640,0x12d0,0x1690,0x20153190,0x670,0x1360,0x1720,0x201531a0,0x660,0x13f0,0x17b0,0x201531b0,
  0xc20,0x410,0x848,0x40150020,0xc30,0x480,0x8d8,0x40150030,0xc00,0x530,0x968,0x40150000,0xc10,0x5a0,0x9f8,0x40150010,
  0xc60,0x650,0xa08,0x201831a0,0xc70,0x6c0,0xa98,0x201831b0,0xc40,0x770,0xb28,0x20183180,0xc50,0x7e0,0xbb8,0x20183190,
  0x1230,0x1010,0x1448,0x40180020,0x1220,0x1080,0x14d8,0x40180030,0x1210,0x1130,0x1568,0x40180000,0x1200,0x11a0,0x15f8,0x40180010,
  0x1270,0x1250,0x1608,0x10000000,0x1260,0x12c0,0x1698,0x10000000,0x1250,0x1370,0x1728,0x10000000,0x1240,0x13e0,0x17b8,0x10000000,
  0x1840,0x420,0x850,0x10000000,0x1850,0x4b0,0x8c0,0x10000000,0x1860,0x500,0x970,0x10000000,0x1870,0x590,0x9e0,0x10000000,
  0x1800,0x660,0xa10,0x201231c0,0x1810,0x6f0,0xa80,0x201231d0,0x1820,0x740,0xb30,0x201231e0,0x1830,0x7d0,0xba0,0x201231f0,
  0x1e50,0x1020,0x1450,0x40120040,0x1e40,0x10b0,0x14c0,0x40120050,0x1e70,0x1100,0x1570,0x40120060,0x1e60,0x1190,0x15e0,0x40120070,
  0x1e10,0x1260,0x1610,0x201531c0,0x1e00,0x12f0,0x1680,0x201531d0,0x1e30,0x1340,0x1730,0x201531e0,0x1e20,0x13d0,0x17a0,0x201531f0,
  0x2460,0x430,0x858,0x40150060,0x2470,0x4a0,0x8c8,0x40150070,0x2440,0x510,0x978,0x40150040,0x2450,0x580,0x9e8,0x40150050,
  0x2420,0x670,0xa18,0x201831e0,0x2430,0x6e0,0xa88,0x201831f0,0x2400,0x750,0xb38,0x201831c0,0x2410,0x7c0,0xba8,0x201831d0,
  0x2a70,0x1030,0x1458,0x40180060,0x2a60,0x10a0,0x14c8,0x40180070,0x2a50,0x1110,0x1578,0x40180040,0x2a40,0x1180,0x15e8,0x40180050,
  0x2a30,0x1270,0x1618,0x10000000,0x2a20,0x12e0,0x1688,0x10000000,0x2a10,0x1350,0x1738,0x10000000,0x2a00,0x13c0,0x17a8,0x10000000,
  0xbfd8bf90,0x3f403f00,0x0,0x0,0x0,0x0,0x0,0x0,0x0,0x0,0x0,0x0,0x0,0x0,0x0,0x0,
  0x0,0x0,0xbfd8bf90,0x3f403f00,0x0,0x0,0x0,0x0,0x0,0x0,0x0,0x0,0x0,0x0,0x0,0x0,
  0xbfd83f90,0x3f40bf00,0x0,0x0,0x0,0x0,0x0,0x0,0x0,0x0,0x0,0x0,0x0,0x0,0x0,0x0,
  0x0,0x0,0xbfd83f90,0x3f40bf00,0x0,0x0,0x0,0x0,0x0,0x0,0x0,0x0,0x0,0x0,0x0,0x0,
  0x0,0x0,0x0,0x0,0xbfd8bf90,0x3f403f00,0x0,0x0,0x0,0x0,0x0,0x0,0x0,0x0,0x0,0x0,
  0x0,0x0,0x0,0x0,0x0,0x0,0xbfd8bf90,0x3f403f00,0x0,0x0,0x0,0x0,0x0,0x0,0x0,0x0,
  0x0,0x0,0x0,0x0,0xbfd83f90,0x3f40bf00,0x0,0x0,0x0,0x0,0x0,0x0,0x0,0x0,0x0,0x0,
  0x0,0x0,0x0,0x0,0x0,0x0,0xbfd83f90,0x3f40bf00,0x0,0x0,0x0,0x0,0x0,0x0,0x0,0x0,
  0x0,0x0,0x0,0x0,0x0,0x0,0x0,0x0,0xbfd8bf90,0x3f403f00,0x0,0x0,0x0,0x0,0x0,0x0,
  0x0,0x0,0x0,0x0,0x0,0x0,0x0,0x0,0x0,0x0,0xbfd8bf90,0x3f403f00,0x0,0x0,0x0,0x0,
  0x0,0x0,0x0,0x0,0x0,0x0,0x0,0x0,0xbfd83f90,0x3f40bf00,0x0,0x0,0x0,0x0,0x0,0x0,
  0x0,0x0,0x0,0x0,0x0,0x0,0x0,0x0,0x0,0x0,0xbfd83f90,0x3f40bf00,0x0,0x0,0x0,0x0,
  0x0,0x0,0x0,0x0,0x0,0x0,0x0,0x0,0x0,0x0,0x0,0x0,0xbfd8bf90,0x3f403f00,0x0,0x0,
  0x0,0x0,0x0,0x0,0x0,0x0,0x0,0x0,0x0,0x0,0x0,0x0,0x0,0x0,0xbfd8bf90,0x3f403f00,
  0x0,0x0,0x0,0x0,0x0,0x0,0x0,0x0,0x0,0x0,0x0,0x0,0xbfd83f90,0x3f40bf00,0x0,0x0,
  0x0,0x0,0x0,0x0,0x0,0x0,0x0,0x0,0x0,0x0,0x0,0x0,0x0,0x0,0xbfd83f90,0x3f40bf00,
  0xc000,0xc020,0x2600,0xba00,0xc008,0xc028,0x2640,0xba40,0xc010,0xc030,0x2690,0xba90,0xc018,0xc038,0x26d0,0xbad0,
  0xd800,0xd820,0x400,0xc000,0xd808,0xd828,0x440,0xc040,0xd810,0xd830,0x490,0xc090,0xd818,0xd838,0x4d0,0xc0d0,
  0xc120,0xc100,0x2800,0xc600,0xc128,0xc108,0x2840,0xc640,0xc130,0xc110,0x2890,0xc690,0xc138,0xc118,0x28d0,0xc6d0,
  0xd840,0xd860,0x2800,0xc600,0xd848,0xd868,0x2840,0xc640,0xd850,0xd870,0x2890,0xc690,0xd858,0xd878,0x28d0,0xc6d0,
  0xc080,0xc0a0,0x2610,0xba10,0xc088,0xc0a8,0x2650,0xba50,0xc090,0xc0b0,0x2680,0xba80,0xc098,0xc0b8,0x26c0,0xbac0,
  0xc1a0,0xc180,0x410,0xc010,0xc1a8,0xc188,0x450,0xc050,0xc1b0,0xc190,0x480,0xc080,0xc1b8,0xc198,0x4c0,0xc0c0,
  0xd9a0,0xd980,0x2810,0xc610,0xd9a8,0xd988,0x2850,0xc650,0xd9b0,0xd990,0x2880,0xc680,0xd9b8,0xd998,0x28c0,0xc6c0,
  0xd9e0,0xd9c0,0x2810,0xc610,0xd9e8,0xd9c8,0x2850,0xc650,0xd9f0,0xd9d0,0x2880,0xc680,0xd9f8,0xd9d8,0x28c0,0xc6c0,
  0xc600,0xc620,0x2620,0xba20,0xc608,0xc628,0x2660,0xba60,0xc610,0xc630,0x26b0,0xbab0,0xc618,0xc638,0x26f0,0xbaf0,
  0xde00,0xde20,0x420,0xc020,0xde08,0xde28,0x460,0xc060,0xde10,0xde30,0x4b0,0xc0b0,0xde18,0xde38,0x4f0,0xc0f0,
  0xd920,0xd900,0x2820,0xc620,0xd928,0xd908,0x2860,0xc660,0xd930,0xd910,0x28b0,0xc6b0,0xd938,0xd918,0x28f0,0xc6f0,
  0xd960,0xd940,0x2820,0xc620,0xd968,0xd948,0x2860,0xc660,0xd970,0xd950,0x28b0,0xc6b0,0xd978,0xd958,0x28f0,0xc6f0,
  0xd880,0xd8a0,0x2630,0xba30,0xd888,0xd8a8,0x2670,0xba70,0xd890,0xd8b0,0x26a0,0xbaa0,0xd898,0xd8b8,0x26e0,0xbae0,
  0xeba0,0xeb80,0x430,0xc030,0xeba8,0xeb88,0x470,0xc070,0xebb0,0xeb90,0x4a0,0xc0a0,0xebb8,0xeb98,0x4e0,0xc0e0,
  0xd8c0,0xd8e0,0x2830,0xc630,0xd8c8,0xd8e8,0x2870,0xc670,0xd8d0,0xd8f0,0x28a0,0xc6a0,0xd8d8,0xd8f8,0x28e0,0xc6e0,
  0xc600,0xc620,0x2830,0xc630,0xc608,0xc628,0x2870,0xc670,0xc610,0xc630,0x28a0,0xc6a0,0xc618,0xc638,0x28e0,0xc6e0,
  0x180000,0x440,0x860,0x10000000,0x180010,0x4d0,0x8f0,0x10000000,0x180020,0x560,0x940,0x10000000,0x180030,0x5f0,0x9d0,0x10000000,
  0x180040,0x600,0xa20,0x201b3180,0x180050,0x690,0xab0,0x201b3190,0x180060,0x720,0xb00,0x201b31a0,0x180070,0x7b0,0xb90,0x201b31b0,
  0x180610,0x1040,0x1460,0x401b0000,0x180600,0x10d0,0x14f0,0x401b0010,0x180630,0x1160,0x1540,0x401b0020,0x180620,0x11f0,0x15d0,0x401b0030,
  0x180650,0x1200,0x1620,0x201e3180,0x180640,0x1290,0x16b0,0x201e3190,0x180670,0x1320,0x1700,0x201e31a0,0x180660,0x13b0,0x1790,0x201e31b0,
  0x180c20,0x450,0x868,0x401e0020,0x180c30,0x4c0,0x8f8,0x401e0030,0x180c00,0x570,0x948,0x401e0000,0x180c10,0x5e0,0x9d8,0x401e0010,
  0x180c60,0x610,0xa28,0x202131a0,0x180c70,0x680,0xab8,0x202131b0,0x180c40,0x730,0xb08,0x20213180,0x180c50,0x7a0,0xb98,0x20213190,
  0x181230,0x1050,0x1468,0x40210020,0x181220,0x10c0,0x14f8,0x40210030,0x181210,0x1170,0x1548,0x40210000,0x181200,0x11e0,0x15d8,0x40210010,
  0x181270,0x1210,0x1628,0x10000000,0x181260,0x1280,0x16b8,0x10000000,0x181250,0x1330,0x1708,0x10000000,0x181240,0x13a0,0x1798,0x10000000,
  0x181840,0x460,0x870,0x10000000,0x181850,0x4f0,0x8e0,0x10000000,0x181860,0x540,0x950,0x10000000,0x181870,0x5d0,0x9c0,0x10000000,
  0x181800,0x620,0xa30,0x201b31c0,0x181810,0x6b0,0xaa0,0x201b31d0,0x181820,0x700,0xb10,0x201b31e0,0x181830,0x790,0xb80,0x201b31f0,
  0x181e50,0x1060,0x1470,0x401b0040,0x181e40,0x10f0,0x14e0,0x401b0050,0x181e70,0x1140,0x1550,0x401b0060,0x181e60,0x11d0,0x15c0,0x401b0070,
  0x181e10,0x1220,0x1630,0x201e31c0,0x181e00,0x12b0,0x16a0,0x201e31d0,0x181e30,0x1300,0x1710,0x201e31e0,0x181e20,0x1390,0x1780,0x201e31f0,
  0x182460,0x470,0x878,0x401e0060,0x182470,0x4e0,0x8e8,0x401e0070,0x182440,0x550,0x958,0x401e0040,0x182450,0x5c0,0x9c8,0x401e0050,
  0x182420,0x630,0xa38,0x202131e0,0x182430,0x6a0,0xaa8,0x202131f0,0x182400,0x710,0xb18,0x202131c0,0x182410,0x780,0xb88,0x202131d0,
  0x182a70,0x1070,0x1478,0x40210060,0x182a60,0x10e0,0x14e8,0x40210070,0x182a50,0x1150,0x1558,0x40210040,0x182a40,0x11c0,0x15c8,0x40210050,
  0x182a30,0x1230,0x1638,0x10000000,0x182a20,0x12a0,0x16a8,0x10000000,0x182a10,0x1310,0x1718,0x10000000,0x182a00,0x1380,0x1788,0x10000000,
  0xbfd8bf90,0x3f403f00,0x0,0x0,0x0,0x0,0x0,0x0,0x0,0x0,0x0,0x0,0x0,0x0,0x0,0x0,
  0x0,0x0,0xbfd8bf90,0x3f403f00,0x0,0x0,0x0,0x0,0x0,0x0,0x0,0x0,0x0,0x0,0x0,0x0,
  0xbfd83f90,0x3f40bf00,0x0,0x0,0x0,0x0,0x0,0x0,0x0,0x0,0x0,0x0,0x0,0x0,0x0,0x0,
  0x0,0x0,0xbfd83f90,0x3f40bf00,0x0,0x0,0x0,0x0,0x0,0x0,0x0,0x0,0x0,0x0,0x0,0x0,
  0x0,0x0,0x0,0x0,0xbfd8bf90,0x3f403f00,0x0,0x0,0x0,0x0,0x0,0x0,0x0,0x0,0x0,0x0,
  0x0,0x0,0x0,0x0,0x0,0x0,0xbfd8bf90,0x3f403f00,0x0,0x0,0x0,0x0,0x0,0x0,0x0,0x0,
  0x0,0x0,0x0,0x0,0xbfd83f90,0x3f40bf00,0x0,0x0,0x0,0x0,0x0,0x0,0x0,0x0,0x0,0x0,
  0x0,0x0,0x0,0x0,0x0,0x0,0xbfd83f90,0x3f40bf00,0x0,0x0,0x0,0x0,0x0,0x0,0x0,0x0,
  0x0,0x0,0x0,0x0,0x0,0x0,0x0,0x0,0xbfd8bf90,0x3f403f00,0x0,0x0,0x0,0x0,0x0,0x0,
  0x0,0x0,0x0,0x0,0x0,0x0,0x0,0x0,0x0,0x0,0xbfd8bf90,0x3f403f00,0x0,0x0,0x0,0x0,
  0x0,0x0,0x0,0x0,0x0,0x0,0x0,0x0,0xbfd83f90,0x3f40bf00,0x0,0x0,0x0,0x0,0x0,0x0,
  0x0,0x0,0x0,0x0,0x0,0x0,0x0,0x0,0x0,0x0,0xbfd83f90,0x3f40bf00,0x0,0x0,0x0,0x0,
  0x0,0x0,0x0,0x0,0x0,0x0,0x0,0x0,0x0,0x0,0x0,0x0,0xbfd8bf90,0x3f403f00,0x0,0x0,
  0x0,0x0,0x0,0x0,0x0,0x0,0x0,0x0,0x0,0x0,0x0,0x0,0x0,0x0,0xbfd8bf90,0x3f403f00,
  0x0,0x0,0x0,0x0,0x0,0x0,0x0,0x0,0x0,0x0,0x0,0x0,0xbfd83f90,0x3f40bf00,0x0,0x0,
  0x0,0x0,0x0,0x0,0x0,0x0,0x0,0x0,0x0,0x0,0x0,0x0,0x0,0x0,0xbfd83f90,0x3f40bf00,
  0xcc00,0xcc20,0x2720,0xbb20,0xcc08,0xcc28,0x2760,0xbb60,0xcc10,0xcc30,0x27b0,0xbbb0,0xcc18,0xcc38,0x27f0,0xbbf0,
  0xcd20,0xcd00,0x520,0xc120,0xcd28,0xcd08,0x560,0xc160,0xcd30,0xcd10,0x5b0,0xc1b0,0xcd38,0xcd18,0x5f0,0xc1f0,
  0xe440,0xe460,0x2920,0xc720,0xe448,0xe468,0x2960,0xc760,0xe450,0xe470,0x29b0,0xc7b0,0xe458,0xe478,0x29f0,0xc7f0,
  0xeb60,0xeb40,0x2920,0xc720,0xeb68,0xeb48,0x2960,0xc760,0xeb70,0xeb50,0x29b0,0xc7b0,0xeb78,0xeb58,0x29f0,0xc7f0,
  0xe480,0xe4a0,0x2730,0xbb30,0xe488,0xe4a8,0x2770,0xbb70,0xe490,0xe4b0,0x27a0,0xbba0,0xe498,0xe4b8,0x27e0,0xbbe0,
  0xe5a0,0xe580,0x530,0xc130,0xe5a8,0xe588,0x570,0xc170,0xe5b0,0xe590,0x5a0,0xc1a0,0xe5b8,0xe598,0x5e0,0xc1e0,
  0xe4c0,0xe4e0,0x2930,0xc730,0xe4c8,0xe4e8,0x2970,0xc770,0xe4d0,0xe4f0,0x29a0,0xc7a0,0xe4d8,0xe4f8,0x29e0,0xc7e0,
  0xd9e0,0xd9c0,0x2930,0xc730,0xd9e8,0xd9c8,0x2970,0xc770,0xd9f0,0xd9d0,0x29a0,0xc7a0,0xd9f8,0xd9d8,0x29e0,0xc7e0,
  0xe400,0xe420,0x2700,0xbb00,0xe408,0xe428,0x2740,0xbb40,0xe410,0xe430,0x2790,0xbb90,0xe418,0xe438,0x27d0,0xbbd0,
  0xe520,0xe500,0x500,0xc100,0xe528,0xe508,0x540,0xc140,0xe530,0xe510,0x590,0xc190,0xe538,0xe518,0x5d0,0xc1d0,
  0xe560,0xe540,0x2900,0xc700,0xe568,0xe548,0x2940,0xc740,0xe570,0xe550,0x2990,0xc790,0xe578,0xe558,0x29d0,0xc7d0,
  0xcc80,0xcca0,0x2900,0xc700,0xcc88,0xcca8,0x2940,0xc740,0xcc90,0xccb0,0x2990,0xc790,0xcc98,0xccb8,0x29d0,0xc7d0,
  0xcda0,0xcd80,0x2710,0xbb10,0xcda8,0xcd88,0x2750,0xbb50,0xcdb0,0xcd90,0x2780,0xbb80,0xcdb8,0xcd98,0x27c0,0xbbc0,
  0xeac0,0xeae0,0x510,0xc110,0xeac8,0xeae8,0x550,0xc150,0xead0,0xeaf0,0x580,0xc180,0xead8,0xeaf8,0x5c0,0xc1c0,
  0xe5e0,0xe5c0,0x2910,0xc710,0xe5e8,0xe5c8,0x2950,0xc750,0xe5f0,0xe5d0,0x2980,0xc780,0xe5f8,0xe5d8,0x29c0,0xc7c0,
  0xe400,0xe420,0x2910,0xc710,0xe408,0xe428,0x2950,0xc750,0xe410,0xe430,0x2980,0xc780,0xe418,0xe438,0x29c0,0xc7c0,
};

__global__ __launch_bounds__(256, 1) void wino_main_kernel(const char* __restrict__ x, char* __restrict__ Uq, char* __restrict__ out, const char* __restrict__ wt) {
  __shared__ __attribute__((aligned(1024))) char smem[163840];
  unsigned tid = threadIdx.x;
  asm volatile(
"s_load_dwordx2 s[12:13], %1, 0\n\t"
"s_load_dwordx2 s[52:53], %1, 24\n\t"
"s_load_dwordx2 s[8:9], %1, 8\n\t"
"s_load_dwordx2 s[16:17], %1, 16\n\t"
"s_getpc_b64 s[40:41]\n\t"
"Lwino_pc:\n\t"
"s_bfe_u32 s42, %2, 0x50003\n\t"
"s_lshl_b32 s42, s42, 12\n\t"
"v_lshl_add_u32 v203, v205, 4, s42\n\t"
"v_min_u32 v203, (Lwino_end-Lwino_pc-16)&-16, v203\n\t"
"global_load_dwordx4 v[100:103], v203, s[40:41]\n\t"
"v_readfirstlane_b32 s73, v205\n\t"
"s_lshl2_add_u32 s69, s73, %2\n\t"
"s_and_b32 s60, %2, 7\n\t"
"s_lshr_b32 s61, %2, 7\n\t"
"s_lshl2_add_u32 s60, s60, s61\n\t"
"s_bfe_u32 s61, %2, 0x40003\n\t"
"s_lshl_b32 s60, s60, 8\n\t"
"s_add_u32 s60, s60, s61\n\t"
"s_sub_u32 s60, s60, 16\n\t"
"s_mul_i32 s21, s60, 12288\n\t"
"s_lshr_b32 s73, s73, 6\n\t"
"s_add_u32 s24, s73, 28\n\t"
"s_mulk_i32 s24, 1536\n\t"
"v_bfe_u32 v1, v205, 2, 3\n\t"
"v_lshrrev_b32 v2, 3, v205\n\t"
"v_and_or_b32 v1, v2, 24, v1\n\t"
"v_and_b32 v3, 6, v2\n\t"
"v_and_b32 v4, 3, v205\n\t"
"v_xor_b32 v4, v3, v4\n\t"
"v_mul_u32_u24 v1, 384, v1\n\t"
"v_lshl_add_u32 v28, v4, 4, v1\n\t"
"s_mov_b32 s14, 100663296\n\t"
"s_mov_b32 s15, 0x20000\n\t"
"s_mov_b32 s54, 663552\n\t"
"s_mov_b32 s55, s15\n\t"
"s_mov_b32 s10, 0x200000\n\t"
"s_mov_b32 s11, s15\n\t"
"s_lshl_b32 s60, s73, 11\n\t"
"v_lshl_add_u32 v252, v205, 4, s60\n\t"
"s_mov_b64 s[46:47], %3\n\t"
"s_add_u32 s48, s46, 12288\n\t"
"s_addc_u32 s49, s47, 0\n\t"
"global_load_dwordx4 v[244:247], v252, s[46:47] offset:2048\n\t"
"s_and_b32 s60, s69, 3\n\t"
"s_lshr_b32 s61, s69, 2\n\t"
"s_mul_i32 s62, s61, 171\n\t"
"s_lshr_b32 s62, s62, 9\n\t"
"s_mul_i32 s63, s62, 3\n\t"
"s_sub_u32 s61, s61, s63\n\t"
"s_mul_i32 s63, s62, 43\n\t"
"s_lshr_b32 s63, s63, 7\n\t"
"s_mul_i32 s64, s63, 3\n\t"
"s_sub_u32 s62, s62, s64\n\t"
"s_mul_i32 s74, s62, 18\n\t"
"s_add_u32 s74, s74, s63\n\t"
"s_lshl3_add_u32 s74, s74, s60\n\t"
"s_mul_i32 s74, s74, 12\n\t"
"s_add_u32 s74, s74, s61\n\t"
"s_lshl_b32 s74, s74, 7\n\t"
"v_bfe_u32 v96, v205, 3, 2\n\t"
"v_and_b32 v97, 32, v205\n\t"
"v_lshl_or_b32 v96, v96, 1, v97\n\t"
"v_mul_u32_u24 v96, 192, v96\n\t"
"v_and_b32 v97, 7, v205\n\t"
"v_lshl_add_u32 v96, v97, 4, v96\n\t"
"v_and_b32 v4, 7, v205\n\t"
"v_xor_b32 v4, v3, v4\n\t"
"v_and_b32 v2, 7, v2\n\t"
"v_mul_u32_u24 v2, 384, v2\n\t"
"v_lshl_add_u32 v29, v4, 4, v2\n\t"
"s_sub_u32 s58, s73, 1\n\t"
"s_lshl_b32 s59, s58, 10\n\t"
"s_lshl_b32 s58, s58, 7\n\t"
"s_waitcnt lgkmcnt(0)\n\t"
"s_and_b32 s13, s13, 0xffff\n\t"
"s_and_b32 s53, s53, 0xffff\n\t"
"s_and_b32 s9, s9, 0xffff\n\t"
"s_cmp_lg_u32 s73, 0\n\t"
"s_cbranch_scc1 Lpb_%=\n\t"
"s_cmp_lt_u32 s69, 216\n\t"
"s_cbranch_scc0 Lnf1_%=\n\t"
"s_mov_b32 m0, 95232\n\t"
"s_add_u32 s64, s74, 0\n\t"
"buffer_load_dwordx4 v96, s[52:55], s64 offen lds\n\t"
"s_mov_b32 m0, 96256\n\t"
"s_add_u32 s64, s74, 73728\n\t"
"buffer_load_dwordx4 v96, s[52:55], s64 offen lds\n\t"
"s_mov_b32 m0, 97280\n\t"
"s_add_u32 s64, s74, 147456\n\t"
"buffer_load_dwordx4 v96, s[52:55], s64 offen lds\n\t"
"Lnf1_%=:\n\t"
"s_mov_b32 s72, 1900544\n\t"
"s_lshl2_add_u32 s70, %2, s72\n\t"
"s_mov_b32 s68, 3000\n\t"
"buffer_load_dwordx4 v[220:223], v252, s[8:11], s72 offen sc1\n\t"
"s_branch Lpa_%=\n\t"
"Lpb_%=:\n\t"
"s_mul_i32 s64, s58, 13056\n\t"
"s_add_u32 s64, s64, s21\n\t"
"s_add_u32 s65, s64, 0\n\t"
"s_max_i32 s65, s65, 0\n\t"
"s_min_i32 s65, s65, 100663232\n\t"
"s_load_dword s76, s[12:13], s65\n\t"
"s_add_u32 m0, s59, 24576\n\t"
"s_add_u32 s57, s58, 0\n\t"
"buffer_load_dwordx4 v29, s[52:55], s57 offen offset:0 lds\n\t"
"buffer_load_dwordx4 v29, s[52:55], s57 offen offset:3072 lds\n\t"
"s_add_u32 m0, s59, 30720\n\t"
"s_add_u32 s57, s58, 6144\n\t"
"buffer_load_dwordx4 v29, s[52:55], s57 offen offset:0 lds\n\t"
"buffer_load_dwordx4 v29, s[52:55], s57 offen offset:3072 lds\n\t"
"s_add_u32 m0, s59, 36864\n\t"
"s_add_u32 s57, s58, 221184\n\t"
"buffer_load_dwordx4 v29, s[52:55], s57 offen offset:0 lds\n\t"
"buffer_load_dwordx4 v29, s[52:55], s57 offen offset:3072 lds\n\t"
"s_add_u32 m0, s59, 82944\n\t"
"s_add_u32 s57, s58, 227328\n\t"
"buffer_load_dwordx4 v29, s[52:55], s57 offen offset:0 lds\n\t"
"buffer_load_dwordx4 v29, s[52:55], s57 offen offset:3072 lds\n\t"
"s_add_u32 m0, s59, 89088\n\t"
"s_add_u32 s57, s58, 442368\n\t"
"buffer_load_dwordx4 v29, s[52:55], s57 offen offset:0 lds\n\t"
"buffer_load_dwordx4 v29, s[52:55], s57 offen offset:3072 lds\n\t"
"s_add_u32 m0, s59, 142848\n\t"
"s_add_u32 s57, s58, 448512\n\t"
"buffer_load_dwordx4 v29, s[52:55], s57 offen offset:0 lds\n\t"
"buffer_load_dwordx4 v29, s[52:55], s57 offen offset:3072 lds\n\t"
"s_branch Lpro_%=\n\t"
"Lpa_%=:\n\t"
"s_mul_i32 s75, s63, 288\n\t"
"s_add_u32 s75, s75, s62\n\t"
"s_lshl2_add_u32 s75, s75, s60\n\t"
"s_cmp_lt_u32 s61, 2\n\t"
"s_cselect_b64 vcc, -1, 0\n\t"
"s_cselect_b32 s61, s61, 4\n\t"
"s_mulk_i32 s61, 24\n\t"
"s_add_u32 s75, s75, s61\n\t"
"s_lshl_b32 s75, s75, 8\n\t"
"v_and_b32 v99, 12, v205\n\t"
"v_and_b32 v60, 35, v205\n\t"
"v_lshl_or_b32 v60, v99, 1, v60\n\t"
"v_bfe_u32 v99, v205, 4, 1\n\t"
"v_lshl_or_b32 v60, v99, 2, v60\n\t"
"v_cndmask_b32 v60, v205, v60, vcc\n\t"
"v_lshlrev_b32 v60, 1, v60\n\t"
"v_add_u32 v60, 95232, v60\n\t"
"v_lshrrev_b32 v97, 5, v205\n\t"
"v_lshl_or_b32 v97, v99, 2, v97\n\t"
"v_lshrrev_b32 v99, 4, v205\n\t"
"v_cndmask_b32 v97, v99, v97, vcc\n\t"
"v_mul_u32_u24 v97, 3072, v97\n\t"
"v_and_b32 v99, 15, v205\n\t"
"v_lshl_add_u32 v97, v99, 4, v97\n\t"
"s_cmp_lt_u32 s63, 2\n\t"
"s_mov_b32 s56, 0x3f2aaaab\n\t"
"s_mov_b32 s57, 0x3fc00000\n\t"
"s_mov_b32 s58, 0x3ee38e39\n\t"
"s_mov_b32 s59, 0x40100000\n\t"
"s_mov_b32 s62, 0xbf54ad4b\n\t"
"s_mov_b32 s63, 0xbefc0fc1\n\t"
"s_mov_b32 s64, 0xbe7c0fc1\n\t"
"s_mov_b32 s65, 0x3ea80a81\n\t"
"s_mov_b32 s38, 0x3f638e39\n\t"
"s_mov_b32 s39, 0x3f638e39\n\t"
"s_cselect_b32 s38, 0x3ea80a81, s38\n\t"
"s_cselect_b32 s39, 0xbebd0bd1, s39\n\t"
"v_mov_b64 v[32:33], 0\n\t"
"v_mov_b64 v[34:35], 0\n\t"
"v_mov_b64 v[36:37], 0\n\t"
"v_mov_b64 v[38:39], 0\n\t"
"v_mov_b64 v[40:41], 0\n\t"
"v_mov_b64 v[42:43], 0\n\t"
"v_mov_b64 v[44:45], 0\n\t"
"v_mov_b64 v[46:47], 0\n\t"
"v_mov_b64 v[48:49], 0\n\t"
"v_mov_b64 v[50:51], 0\n\t"
"v_mov_b64 v[52:53], 0\n\t"
"v_mov_b64 v[54:55], 0\n\t"
"s_mul_i32 s25, s73, 19968\n\t"
"s_add_u32 s25, s25, 61440\n\t"
"s_bfe_u32 s41, %2, 0x40003\n\t"
"s_cmp_eq_u32 s41, 0\n\t"
"s_cselect_b32 s40, -1, 0xbfffffff\n\t"
"s_cmp_eq_u32 s41, 15\n\t"
"s_cselect_b32 s41, -1, 0xdfffffff\n\t"
"s_and_b32 s40, s40, s41\n\t"
"s_sub_u32 s42, s21, 384\n\t"
"s_mul_i32 s41, s73, 589824\n\t"
"s_add_u32 s42, s42, s41\n\t"
"s_and_b32 s41, s40, 536883584\n\t"
"s_and_b32 s40, s40, 2.0\n\t"
"v_bfe_u32 v26, v205, 2, 3\n\t"
"v_add_u32 v27, -1, v26\n\t"
"v_cmp_gt_u32 vcc, 6, v27\n\t"
"v_lshrrev_b32 v27, 1, v27\n\t"
"v_mul_u32_u24 v27, 196608, v27\n\t"
"v_and_b32 v20, 0x70, v28\n\t"
"v_add3_u32 v27, v27, v20, s42\n\t"
"v_bfe_i32 v20, v205, 2, 1\n\t"
"v_mov_b32 v21, s40\n\t"
"v_bfi_b32 v20, v20, s41, v21\n\t"
"v_add_u32 v27, v27, v20\n\t"
"v_bfrev_b32 v20, 8\n\t"
"v_cndmask_b32 v254, v20, v27, vcc\n\t"
"v_bfe_u32 v26, v205, 2, 3\n\t"
"v_bfe_u32 v27, v205, 5, 1\n\t"
"v_mul_u32_u24 v27, 3072, v27\n\t"
"v_lshl_add_u32 v27, v26, 7, v27\n\t"
"v_and_b32 v26, 6, v26\n\t"
"v_lshrrev_b32 v20, 4, v205\n\t"
"v_and_b32 v20, 4, v20\n\t"
"v_and_or_b32 v20, v205, 3, v20\n\t"
"v_xor_b32 v20, v20, v26\n\t"
"v_lshl_add_u32 v20, v20, 4, v27\n\t"
"v_lshrrev_b32 v21, 7, v205\n\t"
"v_lshl_add_u32 v20, v21, 10, v20\n\t"
"v_lshrrev_b32 v21, 5, v205\n\t"
"v_and_b32 v21, 6, v21\n\t"
"v_bfe_u32 v22, v205, 1, 1\n\t"
"v_or_b32 v21, v21, v22\n\t"
"v_xor_b32 v21, v21, v26\n\t"
"v_lshl_add_u32 v21, v21, 4, v27\n\t"
"v_and_b32 v22, 1, v205\n\t"
"v_lshl_add_u32 v21, v22, 3, v21\n\t"
"v_add_u32 v22, 142848, v20\n\t"
"v_add_u32 v23, 142848, v21\n\t"
"v_add_u32 v24, 82944, v20\n\t"
"v_add_u32 v25, 82944, v21\n\t"
"s_and_b32 s17, s17, 0xffff\n\t"
"s_mov_b32 s18, 100663296\n\t"
"s_mov_b32 s19, 0x20000\n\t"
"s_lshr_b32 s41, s73, 1\n\t"
"s_add_u32 s42, s21, 196608\n\t"
"s_mul_i32 s43, s41, 384\n\t"
"s_add_u32 s22, s42, s43\n\t"
"s_lshl1_add_u32 s40, s43, s22\n\t"
"s_mul_i32 s43, s41, 196608\n\t"
"s_add_u32 s23, s42, s43\n\t"
"s_mov_b32 s44, 0x0fff0fff\n\t"
"s_mov_b32 s45, 0x0fff0fff\n\t"
"s_mul_i32 s26, s41, 19968\n\t"
"s_add_u32 s26, s26, 141312\n\t"
"s_sub_u32 s43, s41, s73\n\t"
"s_add_u32 s43, s43, 4\n\t"
"s_mul_i32 s27, s43, 589824\n\t"
"s_bitcmp1_b32 s73, 0\n\t"
"s_mov_b32 s36, 0x0fff0000\n\t"
"s_cselect_b32 s36, 0xfff0, s36\n\t"
"s_mov_b32 s37, s36\n\t"
"s_cmp_lt_u32 s69, 216\n\t"
"s_cbranch_scc0 Lnf3_%=\n\t"
"s_waitcnt vmcnt(1)\n\t"
"ds_read_u16_d16_hi v32, v60 offset:0\n\t"
"ds_read_u16_d16_hi v33, v60 offset:128\n\t"
"ds_read_u16_d16_hi v40, v60 offset:1024\n\t"
"ds_read_u16_d16_hi v41, v60 offset:1152\n\t"
"ds_read_u16_d16_hi v48, v60 offset:2048\n\t"
"ds_read_u16_d16_hi v49, v60 offset:2176\n\t"
"ds_read_u16_d16_hi v34, v60 offset:256\n\t"
"ds_read_u16_d16_hi v35, v60 offset:384\n\t"
"ds_read_u16_d16_hi v42, v60 offset:1280\n\t"
"ds_read_u16_d16_hi v43, v60 offset:1408\n\t"
"ds_read_u16_d16_hi v50, v60 offset:2304\n\t"
"ds_read_u16_d16_hi v51, v60 offset:2432\n\t"
"ds_read_u16_d16_hi v36, v60 offset:512\n\t"
"ds_read_u16_d16_hi v37, v60 offset:640\n\t"
"ds_read_u16_d16_hi v44, v60 offset:1536\n\t"
"ds_read_u16_d16_hi v45, v60 offset:1664\n\t"
"ds_read_u16_d16_hi v52, v60 offset:2560\n\t"
"ds_read_u16_d16_hi v53, v60 offset:2688\n\t"
"ds_read_u16_d16_hi v38, v60 offset:768\n\t"
"ds_read_u16_d16_hi v39, v60 offset:896\n\t"
"ds_read_u16_d16_hi v46, v60 offset:1792\n\t"
"ds_read_u16_d16_hi v47, v60 offset:1920\n\t"
"ds_read_u16_d16_hi v54, v60 offset:2816\n\t"
"ds_read_u16_d16_hi v55, v60 offset:2944\n\t"
"s_waitcnt lgkmcnt(15)\n\t"
"v_pk_fma_f32 v[56:57], v[48:49], s[58:59], v[32:33] op_sel:[0,0,0] op_sel_hi:[1,0,1]\n\t"
"v_pk_fma_f32 v[58:59], v[48:49], s[58:59], v[32:33] op_sel:[0,1,0] op_sel_hi:[1,1,1]\n\t"
"v_pk_mul_f32 v[104:105], v[32:33], s[38:39] op_sel:[0,0] op_sel_hi:[1,0]\n\t"
"v_pk_mul_f32 v[114:115], v[48:49], s[38:39] op_sel:[0,1] op_sel_hi:[1,1]\n\t"
"v_pk_fma_f32 v[106:107], v[40:41], s[56:57], v[56:57] op_sel:[0,0,0] op_sel_hi:[1,0,1]\n\t"
"v_pk_fma_f32 v[108:109], v[40:41], s[56:57], v[56:57] op_sel:[0,0,0] op_sel_hi:[1,0,1] neg_lo:[1,0,0] neg_hi:[1,0,0]\n\t"
"v_pk_fma_f32 v[110:111], v[40:41], s[56:57], v[58:59] op_sel:[0,1,0] op_sel_hi:[1,1,1]\n\t"
"v_pk_fma_f32 v[112:113], v[40:41], s[56:57], v[58:59] op_sel:[0,1,0] op_sel_hi:[1,1,1] neg_lo:[1,0,0] neg_hi:[1,0,0]\n\t"
"v_pk_mul_f32 v[106:107], v[106:107], s[62:63] op_sel:[0,0] op_sel_hi:[1,0]\n\t"
"v_pk_mul_f32 v[108:109], v[108:109], s[62:63] op_sel:[0,1] op_sel_hi:[1,1]\n\t"
"v_pk_mul_f32 v[110:111], v[110:111], s[64:65] op_sel:[0,0] op_sel_hi:[1,0]\n\t"
"v_pk_mul_f32 v[112:113], v[112:113], s[64:65] op_sel:[0,1] op_sel_hi:[1,1]\n\t"
"v_cvt_pk_bf16_f32 v64, v104, v105\n\t"
"v_cvt_pk_bf16_f32 v84, v114, v115\n\t"
"v_cvt_pk_bf16_f32 v68, v106, v107\n\t"
"v_cvt_pk_bf16_f32 v74, v108, v109\n\t"
"v_cvt_pk_bf16_f32 v76, v110, v111\n\t"
"v_cvt_pk_bf16_f32 v82, v112, v113\n\t"
"s_waitcnt lgkmcnt(12)\n\t"
"v_pk_fma_f32 v[56:57], v[50:51], s[58:59], v[34:35] op_sel:[0,0,0] op_sel_hi:[1,0,1]\n\t"
"v_pk_fma_f32 v[58:59], v[50:51], s[58:59], v[34:35] op_sel:[0,1,0] op_sel_hi:[1,1,1]\n\t"
"v_pk_mul_f32 v[104:105], v[34:35], s[38:39] op_sel:[0,0] op_sel_hi:[1,0]\n\t"
"v_pk_mul_f32 v[114:115], v[50:51], s[38:39] op_sel:[0,1] op_sel_hi:[1,1]\n\t"
"v_pk_fma_f32 v[106:107], v[42:43], s[56:57], v[56:57] op_sel:[0,0,0] op_sel_hi:[1,0,1]\n\t"
"v_pk_fma_f32 v[108:109], v[42:43], s[56:57], v[56:57] op_sel:[0,0,0] op_sel_hi:[1,0,1] neg_lo:[1,0,0] neg_hi:[1,0,0]\n\t"
"v_pk_fma_f32 v[110:111], v[42:43], s[56:57], v[58:59] op_sel:[0,1,0] op_sel_hi:[1,1,1]\n\t"
"v_pk_fma_f32 v[112:113], v[42:43], s[56:57], v[58:59] op_sel:[0,1,0] op_sel_hi:[1,1,1] neg_lo:[1,0,0] neg_hi:[1,0,0]\n\t"
"v_pk_mul_f32 v[106:107], v[106:107], s[62:63] op_sel:[0,0] op_sel_hi:[1,0]\n\t"
"v_pk_mul_f32 v[108:109], v[108:109], s[62:63] op_sel:[0,1] op_sel_hi:[1,1]\n\t"
"v_pk_mul_f32 v[110:111], v[110:111], s[64:65] op_sel:[0,0] op_sel_hi:[1,0]\n\t"
"v_pk_mul_f32 v[112:113], v[112:113], s[64:65] op_sel:[0,1] op_sel_hi:[1,1]\n\t"
"v_cvt_pk_bf16_f32 v65, v104, v105\n\t"
"v_cvt_pk_bf16_f32 v85, v114, v115\n\t"
"v_cvt_pk_bf16_f32 v69, v106, v107\n\t"
"v_cvt_pk_bf16_f32 v75, v108, v109\n\t"
"v_cvt_pk_bf16_f32 v77, v110, v111\n\t"
"v_cvt_pk_bf16_f32 v83, v112, v113\n\t"
"s_waitcnt lgkmcnt(6)\n\t"
"v_pk_fma_f32 v[56:57], v[52:53], s[58:59], v[36:37] op_sel:[0,0,0] op_sel_hi:[1,0,1]\n\t"
"v_pk_fma_f32 v[58:59], v[52:53], s[58:59], v[36:37] op_sel:[0,1,0] op_sel_hi:[1,1,1]\n\t"
"v_pk_mul_f32 v[104:105], v[36:37], s[38:39] op_sel:[0,0] op_sel_hi:[1,0]\n\t"
"v_pk_mul_f32 v[114:115], v[52:53], s[38:39] op_sel:[0,1] op_sel_hi:[1,1]\n\t"
"v_pk_fma_f32 v[106:107], v[44:45], s[56:57], v[56:57] op_sel:[0,0,0] op_sel_hi:[1,0,1]\n\t"
"v_pk_fma_f32 v[108:109], v[44:45], s[56:57], v[56:57] op_sel:[0,0,0] op_sel_hi:[1,0,1] neg_lo:[1,0,0] neg_hi:[1,0,0]\n\t"
"v_pk_fma_f32 v[110:111], v[44:45], s[56:57], v[58:59] op_sel:[0,1,0] op_sel_hi:[1,1,1]\n\t"
"v_pk_fma_f32 v[112:113], v[44:45], s[56:57], v[58:59] op_sel:[0,1,0] op_sel_hi:[1,1,1] neg_lo:[1,0,0] neg_hi:[1,0,0]\n\t"
"v_pk_mul_f32 v[106:107], v[106:107], s[62:63] op_sel:[0,0] op_sel_hi:[1,0]\n\t"
"v_pk_mul_f32 v[108:109], v[108:109], s[62:63] op_sel:[0,1] op_sel_hi:[1,1]\n\t"
"v_pk_mul_f32 v[110:111], v[110:111], s[64:65] op_sel:[0,0] op_sel_hi:[1,0]\n\t"
"v_pk_mul_f32 v[112:113], v[112:113], s[64:65] op_sel:[0,1] op_sel_hi:[1,1]\n\t"
"v_cvt_pk_bf16_f32 v66, v104, v105\n\t"
"v_cvt_pk_bf16_f32 v86, v114, v115\n\t"
"v_cvt_pk_bf16_f32 v70, v106, v107\n\t"
"v_cvt_pk_bf16_f32 v72, v108, v109\n\t"
"v_cvt_pk_bf16_f32 v78, v110, v111\n\t"
"v_cvt_pk_bf16_f32 v80, v112, v113\n\t"
"s_waitcnt lgkmcnt(0)\n\t"
"v_pk_fma_f32 v[56:57], v[54:55], s[58:59], v[38:39] op_sel:[0,0,0] op_sel_hi:[1,0,1]\n\t"
"v_pk_fma_f32 v[58:59], v[54:55], s[58:59], v[38:39] op_sel:[0,1,0] op_sel_hi:[1,1,1]\n\t"
"v_pk_mul_f32 v[104:105], v[38:39], s[38:39] op_sel:[0,0] op_sel_hi:[1,0]\n\t"
"v_pk_mul_f32 v[114:115], v[54:55], s[38:39] op_sel:[0,1] op_sel_hi:[1,1]\n\t"
"v_pk_fma_f32 v[106:107], v[46:47], s[56:57], v[56:57] op_sel:[0,0,0] op_sel_hi:[1,0,1]\n\t"
"v_pk_fma_f32 v[108:109], v[46:47], s[56:57], v[56:57] op_sel:[0,0,0] op_sel_hi:[1,0,1] neg_lo:[1,0,0] neg_hi:[1,0,0]\n\t"
"v_pk_fma_f32 v[110:111], v[46:47], s[56:57], v[58:59] op_sel:[0,1,0] op_sel_hi:[1,1,1]\n\t"
"v_pk_fma_f32 v[112:113], v[46:47], s[56:57], v[58:59] op_sel:[0,1,0] op_sel_hi:[1,1,1] neg_lo:[1,0,0] neg_hi:[1,0,0]\n\t"
"v_pk_mul_f32 v[106:107], v[106:107], s[62:63] op_sel:[0,0] op_sel_hi:[1,0]\n\t"
"v_pk_mul_f32 v[108:109], v[108:109], s[62:63] op_sel:[0,1] op_sel_hi:[1,1]\n\t"
"v_pk_mul_f32 v[110:111], v[110:111], s[64:65] op_sel:[0,0] op_sel_hi:[1,0]\n\t"
"v_pk_mul_f32 v[112:113], v[112:113], s[64:65] op_sel:[0,1] op_sel_hi:[1,1]\n\t"
"v_cvt_pk_bf16_f32 v67, v104, v105\n\t"
"v_cvt_pk_bf16_f32 v87, v114, v115\n\t"
"v_cvt_pk_bf16_f32 v71, v106, v107\n\t"
"v_cvt_pk_bf16_f32 v73, v108, v109\n\t"
"v_cvt_pk_bf16_f32 v79, v110, v111\n\t"
"v_cvt_pk_bf16_f32 v81, v112, v113\n\t"
"s_add_u32 s60, s75, 0\n\t"
"buffer_store_dwordx4 v[64:67], v97, s[8:11], s60 offen sc1\n\t"
"s_add_u32 s60, s75, 184320\n\t"
"buffer_store_dwordx4 v[84:87], v97, s[8:11], s60 offen sc1\n\t"
"s_add_u32 s60, s75, 36864\n\t"
"buffer_store_dwordx4 v[68:71], v97, s[8:11], s60 offen sc1\n\t"
"s_add_u32 s60, s75, 73728\n\t"
"buffer_store_dwordx4 v[72:75], v97, s[8:11], s60 offen sc1\n\t"
"s_add_u32 s60, s75, 110592\n\t"
"buffer_store_dwordx4 v[76:79], v97, s[8:11], s60 offen sc1\n\t"
"s_add_u32 s60, s75, 147456\n\t"
"buffer_store_dwordx4 v[80:83], v97, s[8:11], s60 offen sc1\n\t"
"s_waitcnt vmcnt(6)\n\t"
"s_branch Lnf4_%=\n\t"
"Lnf3_%=:\n\t"
"s_waitcnt vmcnt(0)\n\t"
"Lnf4_%=:\n\t"
"s_lshr_b32 s60, %2, 2\n\t"
"v_readlane_b32 s61, v220, s60\n\t"
"v_readlane_b32 s62, v221, s60\n\t"
"v_readlane_b32 s63, v222, s60\n\t"
"v_readlane_b32 s64, v223, s60\n\t"
"s_bitcmp1_b32 %2, 0\n\t"
"s_cselect_b32 s61, s62, s61\n\t"
"s_cselect_b32 s63, s64, s63\n\t"
"s_bitcmp1_b32 %2, 1\n\t"
"s_cselect_b32 s71, s63, s61\n\t"
"s_branch Ljoin_%=\n\t"
"Lfb_%=:\n\t"
"s_waitcnt vmcnt(0) lgkmcnt(0)\n\t"
"s_nop 15\n\t"
"v_lshrrev_b32 v255, 9, v252\n\t"
"v_lshl_add_u32 v255, v255, 10, v252\n\t"
"v_add_u32 v255, 44032, v255\n\t"
"ds_write_b128 v255, v[32:35] offset:0\n\t"
"ds_write_b128 v255, v[36:39] offset:3072\n\t"
"ds_write_b128 v255, v[40:43] offset:6144\n\t"
"ds_write_b128 v255, v[44:47] offset:9216\n\t"
"ds_write_b128 v255, v[48:51] offset:12288\n\t"
"ds_write_b128 v255, v[52:55] offset:15360\n\t"
"ds_write_b128 v255, v[56:59] offset:18432\n\t"
"ds_write_b128 v255, v[60:63] offset:21504\n\t"
"ds_write_b128 v255, v[64:67] offset:24576\n\t"
"ds_write_b128 v255, v[68:71] offset:27648\n\t"
"ds_write_b128 v255, v[72:75] offset:30720\n\t"
"ds_write_b128 v255, v[76:79] offset:33792\n\t"
"ds_write_b128 v255, v[80:83] offset:36864\n\t"
"ds_write_b128 v255, v[84:87] offset:39936\n\t"
"ds_write_b128 v255, v[88:91] offset:43008\n\t"
"ds_write_b128 v255, v[92:95] offset:46080\n\t"
"ds_write_b128 v255, v[96:99] offset:49152\n\t"
"ds_write_b128 v255, v[100:103] offset:52224\n\t"
"ds_write_b128 v255, v[104:107] offset:55296\n\t"
"ds_write_b128 v255, v[108:111] offset:58368\n\t"
"ds_write_b128 v255, v[112:115] offset:61440\n\t"
"ds_write_b128 v255, v[116:119] offset:64512\n\t"
"s_waitcnt lgkmcnt(0)\n\t"
"v_mov_b64 v[32:33], 0\n\t"
"v_mov_b64 v[34:35], 0\n\t"
"v_mov_b64 v[36:37], 0\n\t"
"v_mov_b64 v[38:39], 0\n\t"
"v_mov_b64 v[40:41], 0\n\t"
"v_mov_b64 v[42:43], 0\n\t"
"v_mov_b64 v[44:45], 0\n\t"
"v_mov_b64 v[46:47], 0\n\t"
"v_mov_b64 v[48:49], 0\n\t"
"v_mov_b64 v[50:51], 0\n\t"
"v_mov_b64 v[52:53], 0\n\t"
"v_mov_b64 v[54:55], 0\n\t"
"v_lshrrev_b32 v98, 4, v252\n\t"
"v_bfe_u32 v96, v98, 3, 2\n\t"
"v_and_b32 v97, 32, v98\n\t"
"v_lshl_or_b32 v96, v96, 1, v97\n\t"
"v_mul_u32_u24 v96, 192, v96\n\t"
"v_and_b32 v97, 7, v98\n\t"
"v_lshl_add_u32 v96, v97, 4, v96\n\t"
"s_mov_b32 s69, 0\n\t"
"Lfb1_%=:\n\t"
"s_and_b32 s60, s69, 3\n\t"
"s_lshr_b32 s61, s69, 2\n\t"
"s_mul_i32 s62, s61, 171\n\t"
"s_lshr_b32 s62, s62, 9\n\t"
"s_mul_i32 s63, s62, 3\n\t"
"s_sub_u32 s61, s61, s63\n\t"
"s_mul_i32 s63, s62, 43\n\t"
"s_lshr_b32 s63, s63, 7\n\t"
"s_mul_i32 s64, s63, 3\n\t"
"s_sub_u32 s62, s62, s64\n\t"
"s_mul_i32 s74, s62, 18\n\t"
"s_add_u32 s74, s74, s63\n\t"
"s_lshl3_add_u32 s74, s74, s60\n\t"
"s_mul_i32 s74, s74, 12\n\t"
"s_add_u32 s74, s74, s61\n\t"
"s_lshl_b32 s74, s74, 7\n\t"
"s_mov_b32 m0, 18432\n\t"
"s_add_u32 s64, s74, 0\n\t"
"buffer_load_dwordx4 v96, s[52:55], s64 offen lds\n\t"
"s_mov_b32 m0, 19456\n\t"
"s_add_u32 s64, s74, 73728\n\t"
"buffer_load_dwordx4 v96, s[52:55], s64 offen lds\n\t"
"s_mov_b32 m0, 20480\n\t"
"s_add_u32 s64, s74, 147456\n\t"
"buffer_load_dwordx4 v96, s[52:55], s64 offen lds\n\t"
"s_mul_i32 s75, s63, 288\n\t"
"s_add_u32 s75, s75, s62\n\t"
"s_lshl2_add_u32 s75, s75, s60\n\t"
"s_cmp_lt_u32 s61, 2\n\t"
"s_cselect_b64 vcc, -1, 0\n\t"
"s_cselect_b32 s61, s61, 4\n\t"
"s_mulk_i32 s61, 24\n\t"
"s_add_u32 s75, s75, s61\n\t"
"s_lshl_b32 s75, s75, 8\n\t"
"v_and_b32 v99, 12, v98\n\t"
"v_and_b32 v60, 35, v98\n\t"
"v_lshl_or_b32 v60, v99, 1, v60\n\t"
"v_bfe_u32 v99, v98, 4, 1\n\t"
"v_lshl_or_b32 v60, v99, 2, v60\n\t"
"v_cndmask_b32 v60, v98, v60, vcc\n\t"
"v_lshlrev_b32 v60, 1, v60\n\t"
"v_add_u32 v60, 18432, v60\n\t"
"v_lshrrev_b32 v97, 5, v98\n\t"
"v_lshl_or_b32 v97, v99, 2, v97\n\t"
"v_lshrrev_b32 v99, 4, v98\n\t"
"v_cndmask_b32 v97, v99, v97, vcc\n\t"
"v_mul_u32_u24 v97, 3072, v97\n\t"
"v_and_b32 v99, 15, v98\n\t"
"v_lshl_add_u32 v97, v99, 4, v97\n\t"
"s_cmp_lt_u32 s63, 2\n\t"
"s_mov_b32 s56, 0x3f2aaaab\n\t"
"s_mov_b32 s57, 0x3fc00000\n\t"
"s_mov_b32 s58, 0x3ee38e39\n\t"
"s_mov_b32 s59, 0x40100000\n\t"
"s_mov_b32 s62, 0xbf54ad4b\n\t"
"s_mov_b32 s63, 0xbefc0fc1\n\t"
"s_mov_b32 s64, 0xbe7c0fc1\n\t"
"s_mov_b32 s65, 0x3ea80a81\n\t"
"s_mov_b32 s38, 0x3f638e39\n\t"
"s_mov_b32 s39, 0x3f638e39\n\t"
"s_cselect_b32 s38, 0x3ea80a81, s38\n\t"
"s_cselect_b32 s39, 0xbebd0bd1, s39\n\t"
"s_waitcnt vmcnt(0)\n\t"
"ds_read_u16_d16_hi v32, v60 offset:0\n\t"
"ds_read_u16_d16_hi v33, v60 offset:128\n\t"
"ds_read_u16_d16_hi v40, v60 offset:1024\n\t"
"ds_read_u16_d16_hi v41, v60 offset:1152\n\t"
"ds_read_u16_d16_hi v48, v60 offset:2048\n\t"
"ds_read_u16_d16_hi v49, v60 offset:2176\n\t"
"ds_read_u16_d16_hi v34, v60 offset:256\n\t"
"ds_read_u16_d16_hi v35, v60 offset:384\n\t"
"ds_read_u16_d16_hi v42, v60 offset:1280\n\t"
"ds_read_u16_d16_hi v43, v60 offset:1408\n\t"
"ds_read_u16_d16_hi v50, v60 offset:2304\n\t"
"ds_read_u16_d16_hi v51, v60 offset:2432\n\t"
"ds_read_u16_d16_hi v36, v60 offset:512\n\t"
"ds_read_u16_d16_hi v37, v60 offset:640\n\t"
"ds_read_u16_d16_hi v44, v60 offset:1536\n\t"
"ds_read_u16_d16_hi v45, v60 offset:1664\n\t"
"ds_read_u16_d16_hi v52, v60 offset:2560\n\t"
"ds_read_u16_d16_hi v53, v60 offset:2688\n\t"
"ds_read_u16_d16_hi v38, v60 offset:768\n\t"
"ds_read_u16_d16_hi v39, v60 offset:896\n\t"
"ds_read_u16_d16_hi v46, v60 offset:1792\n\t"
"ds_read_u16_d16_hi v47, v60 offset:1920\n\t"
"ds_read_u16_d16_hi v54, v60 offset:2816\n\t"
"ds_read_u16_d16_hi v55, v60 offset:2944\n\t"
"s_waitcnt lgkmcnt(15)\n\t"
"v_pk_fma_f32 v[56:57], v[48:49], s[58:59], v[32:33] op_sel:[0,0,0] op_sel_hi:[1,0,1]\n\t"
"v_pk_fma_f32 v[58:59], v[48:49], s[58:59], v[32:33] op_sel:[0,1,0] op_sel_hi:[1,1,1]\n\t"
"v_pk_mul_f32 v[104:105], v[32:33], s[38:39] op_sel:[0,0] op_sel_hi:[1,0]\n\t"
"v_pk_mul_f32 v[114:115], v[48:49], s[38:39] op_sel:[0,1] op_sel_hi:[1,1]\n\t"
"v_pk_fma_f32 v[106:107], v[40:41], s[56:57], v[56:57] op_sel:[0,0,0] op_sel_hi:[1,0,1]\n\t"
"v_pk_fma_f32 v[108:109], v[40:41], s[56:57], v[56:57] op_sel:[0,0,0] op_sel_hi:[1,0,1] neg_lo:[1,0,0] neg_hi:[1,0,0]\n\t"
"v_pk_fma_f32 v[110:111], v[40:41], s[56:57], v[58:59] op_sel:[0,1,0] op_sel_hi:[1,1,1]\n\t"
"v_pk_fma_f32 v[112:113], v[40:41], s[56:57], v[58:59] op_sel:[0,1,0] op_sel_hi:[1,1,1] neg_lo:[1,0,0] neg_hi:[1,0,0]\n\t"
"v_pk_mul_f32 v[106:107], v[106:107], s[62:63] op_sel:[0,0] op_sel_hi:[1,0]\n\t"
"v_pk_mul_f32 v[108:109], v[108:109], s[62:63] op_sel:[0,1] op_sel_hi:[1,1]\n\t"
"v_pk_mul_f32 v[110:111], v[110:111], s[64:65] op_sel:[0,0] op_sel_hi:[1,0]\n\t"
"v_pk_mul_f32 v[112:113], v[112:113], s[64:65] op_sel:[0,1] op_sel_hi:[1,1]\n\t"
"v_cvt_pk_bf16_f32 v64, v104, v105\n\t"
"v_cvt_pk_bf16_f32 v84, v114, v115\n\t"
"v_cvt_pk_bf16_f32 v68, v106, v107\n\t"
"v_cvt_pk_bf16_f32 v74, v108, v109\n\t"
"v_cvt_pk_bf16_f32 v76, v110, v111\n\t"
"v_cvt_pk_bf16_f32 v82, v112, v113\n\t"
"s_waitcnt lgkmcnt(12)\n\t"
"v_pk_fma_f32 v[56:57], v[50:51], s[58:59], v[34:35] op_sel:[0,0,0] op_sel_hi:[1,0,1]\n\t"
"v_pk_fma_f32 v[58:59], v[50:51], s[58:59], v[34:35] op_sel:[0,1,0] op_sel_hi:[1,1,1]\n\t"
"v_pk_mul_f32 v[104:105], v[34:35], s[38:39] op_sel:[0,0] op_sel_hi:[1,0]\n\t"
"v_pk_mul_f32 v[114:115], v[50:51], s[38:39] op_sel:[0,1] op_sel_hi:[1,1]\n\t"
"v_pk_fma_f32 v[106:107], v[42:43], s[56:57], v[56:57] op_sel:[0,0,0] op_sel_hi:[1,0,1]\n\t"
"v_pk_fma_f32 v[108:109], v[42:43], s[56:57], v[56:57] op_sel:[0,0,0] op_sel_hi:[1,0,1] neg_lo:[1,0,0] neg_hi:[1,0,0]\n\t"
"v_pk_fma_f32 v[110:111], v[42:43], s[56:57], v[58:59] op_sel:[0,1,0] op_sel_hi:[1,1,1]\n\t"
"v_pk_fma_f32 v[112:113], v[42:43], s[56:57], v[58:59] op_sel:[0,1,0] op_sel_hi:[1,1,1] neg_lo:[1,0,0] neg_hi:[1,0,0]\n\t"
"v_pk_mul_f32 v[106:107], v[106:107], s[62:63] op_sel:[0,0] op_sel_hi:[1,0]\n\t"
"v_pk_mul_f32 v[108:109], v[108:109], s[62:63] op_sel:[0,1] op_sel_hi:[1,1]\n\t"
"v_pk_mul_f32 v[110:111], v[110:111], s[64:65] op_sel:[0,0] op_sel_hi:[1,0]\n\t"
"v_pk_mul_f32 v[112:113], v[112:113], s[64:65] op_sel:[0,1] op_sel_hi:[1,1]\n\t"
"v_cvt_pk_bf16_f32 v65, v104, v105\n\t"
"v_cvt_pk_bf16_f32 v85, v114, v115\n\t"
"v_cvt_pk_bf16_f32 v69, v106, v107\n\t"
"v_cvt_pk_bf16_f32 v75, v108, v109\n\t"
"v_cvt_pk_bf16_f32 v77, v110, v111\n\t"
"v_cvt_pk_bf16_f32 v83, v112, v113\n\t"
"s_waitcnt lgkmcnt(6)\n\t"
"v_pk_fma_f32 v[56:57], v[52:53], s[58:59], v[36:37] op_sel:[0,0,0] op_sel_hi:[1,0,1]\n\t"
"v_pk_fma_f32 v[58:59], v[52:53], s[58:59], v[36:37] op_sel:[0,1,0] op_sel_hi:[1,1,1]\n\t"
"v_pk_mul_f32 v[104:105], v[36:37], s[38:39] op_sel:[0,0] op_sel_hi:[1,0]\n\t"
"v_pk_mul_f32 v[114:115], v[52:53], s[38:39] op_sel:[0,1] op_sel_hi:[1,1]\n\t"
"v_pk_fma_f32 v[106:107], v[44:45], s[56:57], v[56:57] op_sel:[0,0,0] op_sel_hi:[1,0,1]\n\t"
"v_pk_fma_f32 v[108:109], v[44:45], s[56:57], v[56:57] op_sel:[0,0,0] op_sel_hi:[1,0,1] neg_lo:[1,0,0] neg_hi:[1,0,0]\n\t"
"v_pk_fma_f32 v[110:111], v[44:45], s[56:57], v[58:59] op_sel:[0,1,0] op_sel_hi:[1,1,1]\n\t"
"v_pk_fma_f32 v[112:113], v[44:45], s[56:57], v[58:59] op_sel:[0,1,0] op_sel_hi:[1,1,1] neg_lo:[1,0,0] neg_hi:[1,0,0]\n\t"
"v_pk_mul_f32 v[106:107], v[106:107], s[62:63] op_sel:[0,0] op_sel_hi:[1,0]\n\t"
"v_pk_mul_f32 v[108:109], v[108:109], s[62:63] op_sel:[0,1] op_sel_hi:[1,1]\n\t"
"v_pk_mul_f32 v[110:111], v[110:111], s[64:65] op_sel:[0,0] op_sel_hi:[1,0]\n\t"
"v_pk_mul_f32 v[112:113], v[112:113], s[64:65] op_sel:[0,1] op_sel_hi:[1,1]\n\t"
"v_cvt_pk_bf16_f32 v66, v104, v105\n\t"
"v_cvt_pk_bf16_f32 v86, v114, v115\n\t"
"v_cvt_pk_bf16_f32 v70, v106, v107\n\t"
"v_cvt_pk_bf16_f32 v72, v108, v109\n\t"
"v_cvt_pk_bf16_f32 v78, v110, v111\n\t"
"v_cvt_pk_bf16_f32 v80, v112, v113\n\t"
"s_waitcnt lgkmcnt(0)\n\t"
"v_pk_fma_f32 v[56:57], v[54:55], s[58:59], v[38:39] op_sel:[0,0,0] op_sel_hi:[1,0,1]\n\t"
"v_pk_fma_f32 v[58:59], v[54:55], s[58:59], v[38:39] op_sel:[0,1,0] op_sel_hi:[1,1,1]\n\t"
"v_pk_mul_f32 v[104:105], v[38:39], s[38:39] op_sel:[0,0] op_sel_hi:[1,0]\n\t"
"v_pk_mul_f32 v[114:115], v[54:55], s[38:39] op_sel:[0,1] op_sel_hi:[1,1]\n\t"
"v_pk_fma_f32 v[106:107], v[46:47], s[56:57], v[56:57] op_sel:[0,0,0] op_sel_hi:[1,0,1]\n\t"
"v_pk_fma_f32 v[108:109], v[46:47], s[56:57], v[56:57] op_sel:[0,0,0] op_sel_hi:[1,0,1] neg_lo:[1,0,0] neg_hi:[1,0,0]\n\t"
"v_pk_fma_f32 v[110:111], v[46:47], s[56:57], v[58:59] op_sel:[0,1,0] op_sel_hi:[1,1,1]\n\t"
"v_pk_fma_f32 v[112:113], v[46:47], s[56:57], v[58:59] op_sel:[0,1,0] op_sel_hi:[1,1,1] neg_lo:[1,0,0] neg_hi:[1,0,0]\n\t"
"v_pk_mul_f32 v[106:107], v[106:107], s[62:63] op_sel:[0,0] op_sel_hi:[1,0]\n\t"
"v_pk_mul_f32 v[108:109], v[108:109], s[62:63] op_sel:[0,1] op_sel_hi:[1,1]\n\t"
"v_pk_mul_f32 v[110:111], v[110:111], s[64:65] op_sel:[0,0] op_sel_hi:[1,0]\n\t"
"v_pk_mul_f32 v[112:113], v[112:113], s[64:65] op_sel:[0,1] op_sel_hi:[1,1]\n\t"
"v_cvt_pk_bf16_f32 v67, v104, v105\n\t"
"v_cvt_pk_bf16_f32 v87, v114, v115\n\t"
"v_cvt_pk_bf16_f32 v71, v106, v107\n\t"
"v_cvt_pk_bf16_f32 v73, v108, v109\n\t"
"v_cvt_pk_bf16_f32 v79, v110, v111\n\t"
"v_cvt_pk_bf16_f32 v81, v112, v113\n\t"
"s_add_u32 s60, s75, 0\n\t"
"buffer_store_dwordx4 v[64:67], v97, s[8:11], s60 offen sc1\n\t"
"s_add_u32 s60, s75, 184320\n\t"
"buffer_store_dwordx4 v[84:87], v97, s[8:11], s60 offen sc1\n\t"
"s_add_u32 s60, s75, 36864\n\t"
"buffer_store_dwordx4 v[68:71], v97, s[8:11], s60 offen sc1\n\t"
"s_add_u32 s60, s75, 73728\n\t"
"buffer_store_dwordx4 v[72:75], v97, s[8:11], s60 offen sc1\n\t"
"s_add_u32 s60, s75, 110592\n\t"
"buffer_store_dwordx4 v[76:79], v97, s[8:11], s60 offen sc1\n\t"
"s_add_u32 s60, s75, 147456\n\t"
"buffer_store_dwordx4 v[80:83], v97, s[8:11], s60 offen sc1\n\t"
"s_waitcnt vmcnt(0)\n\t"
"s_add_u32 s69, s69, 1\n\t"
"s_cmp_lt_u32 s69, 216\n\t"
"s_cbranch_scc1 Lfb1_%=\n\t"
"s_mov_b32 s69, %2\n\t"
"s_mov_b64 vcc, 0\n\t"
"ds_read_b128 v[32:35], v255 offset:0\n\t"
"ds_read_b128 v[36:39], v255 offset:3072\n\t"
"ds_read_b128 v[40:43], v255 offset:6144\n\t"
"ds_read_b128 v[44:47], v255 offset:9216\n\t"
"ds_read_b128 v[48:51], v255 offset:12288\n\t"
"ds_read_b128 v[52:55], v255 offset:15360\n\t"
"ds_read_b128 v[56:59], v255 offset:18432\n\t"
"ds_read_b128 v[60:63], v255 offset:21504\n\t"
"ds_read_b128 v[64:67], v255 offset:24576\n\t"
"ds_read_b128 v[68:71], v255 offset:27648\n\t"
"ds_read_b128 v[72:75], v255 offset:30720\n\t"
"ds_read_b128 v[76:79], v255 offset:33792\n\t"
"ds_read_b128 v[80:83], v255 offset:36864\n\t"
"ds_read_b128 v[84:87], v255 offset:39936\n\t"
"ds_read_b128 v[88:91], v255 offset:43008\n\t"
"ds_read_b128 v[92:95], v255 offset:46080\n\t"
"ds_read_b128 v[96:99], v255 offset:49152\n\t"
"ds_read_b128 v[100:103], v255 offset:52224\n\t"
"ds_read_b128 v[104:107], v255 offset:55296\n\t"
"ds_read_b128 v[108:111], v255 offset:58368\n\t"
"ds_read_b128 v[112:115], v255 offset:61440\n\t"
"ds_read_b128 v[116:119], v255 offset:64512\n\t"
"s_waitcnt lgkmcnt(0)\n\t"
"s_branch Lgo_%=\n\t"
".p2align 12\n\t"
"Lpro_%=:\n\t"
"s_mul_i32 s25, s73, 19968\n\t"
"s_add_u32 s25, s25, 61440\n\t"
"s_bfe_u32 s41, %2, 0x40003\n\t"
"s_cmp_eq_u32 s41, 0\n\t"
"s_cselect_b32 s40, -1, 0xbfffffff\n\t"
"s_cmp_eq_u32 s41, 15\n\t"
"s_cselect_b32 s41, -1, 0xdfffffff\n\t"
"s_and_b32 s40, s40, s41\n\t"
"s_sub_u32 s42, s21, 384\n\t"
"s_mul_i32 s41, s73, 589824\n\t"
"s_add_u32 s42, s42, s41\n\t"
"s_and_b32 s41, s40, 536883584\n\t"
"s_and_b32 s40, s40, 2.0\n\t"
"v_bfe_u32 v26, v205, 2, 3\n\t"
"v_add_u32 v27, -1, v26\n\t"
"v_cmp_gt_u32 vcc, 6, v27\n\t"
"v_lshrrev_b32 v27, 1, v27\n\t"
"v_mul_u32_u24 v27, 196608, v27\n\t"
"v_and_b32 v20, 0x70, v28\n\t"
"v_add3_u32 v27, v27, v20, s42\n\t"
"v_bfe_i32 v20, v205, 2, 1\n\t"
"v_mov_b32 v21, s40\n\t"
"v_bfi_b32 v20, v20, s41, v21\n\t"
"v_add_u32 v27, v27, v20\n\t"
"v_bfrev_b32 v20, 8\n\t"
"v_cndmask_b32 v254, v20, v27, vcc\n\t"
"v_bfe_u32 v26, v205, 2, 3\n\t"
"v_bfe_u32 v27, v205, 5, 1\n\t"
"v_mul_u32_u24 v27, 3072, v27\n\t"
"v_lshl_add_u32 v27, v26, 7, v27\n\t"
"v_and_b32 v26, 6, v26\n\t"
"v_lshrrev_b32 v20, 4, v205\n\t"
"v_and_b32 v20, 4, v20\n\t"
"v_and_or_b32 v20, v205, 3, v20\n\t"
"v_xor_b32 v20, v20, v26\n\t"
"v_lshl_add_u32 v20, v20, 4, v27\n\t"
"v_lshrrev_b32 v21, 7, v205\n\t"
"v_lshl_add_u32 v20, v21, 10, v20\n\t"
"v_lshrrev_b32 v21, 5, v205\n\t"
"v_and_b32 v21, 6, v21\n\t"
"v_bfe_u32 v22, v205, 1, 1\n\t"
"v_or_b32 v21, v21, v22\n\t"
"v_xor_b32 v21, v21, v26\n\t"
"v_lshl_add_u32 v21, v21, 4, v27\n\t"
"v_and_b32 v22, 1, v205\n\t"
"v_lshl_add_u32 v21, v22, 3, v21\n\t"
"v_add_u32 v22, 142848, v20\n\t"
"v_add_u32 v23, 142848, v21\n\t"
"v_add_u32 v24, 82944, v20\n\t"
"v_add_u32 v25, 82944, v21\n\t"
"s_and_b32 s17, s17, 0xffff\n\t"
"s_mov_b32 s18, 100663296\n\t"
"s_mov_b32 s19, 0x20000\n\t"
"s_lshr_b32 s41, s73, 1\n\t"
"s_add_u32 s42, s21, 196608\n\t"
"s_mul_i32 s43, s41, 384\n\t"
"s_add_u32 s22, s42, s43\n\t"
"s_lshl1_add_u32 s40, s43, s22\n\t"
"s_mul_i32 s43, s41, 196608\n\t"
"s_add_u32 s23, s42, s43\n\t"
"s_mov_b32 s44, 0x0fff0fff\n\t"
"s_mov_b32 s45, 0x0fff0fff\n\t"
"s_mul_i32 s26, s41, 19968\n\t"
"s_add_u32 s26, s26, 141312\n\t"
"s_sub_u32 s43, s41, s73\n\t"
"s_add_u32 s43, s43, 4\n\t"
"s_mul_i32 s27, s43, 589824\n\t"
"s_bitcmp1_b32 s73, 0\n\t"
"s_mov_b32 s36, 0x0fff0000\n\t"
"s_cselect_b32 s36, 0xfff0, s36\n\t"
"s_mov_b32 s37, s36\n\t"
"s_add_u32 m0, s59, 148992\n\t"
"s_add_u32 s57, s58, 147456\n\t"
"buffer_load_dwordx4 v29, s[52:55], s57 offen offset:0 lds\n\t"
"buffer_load_dwordx4 v29, s[52:55], s57 offen offset:3072 lds\n\t"
"s_add_u32 m0, s59, 155136\n\t"
"s_add_u32 s57, s58, 153600\n\t"
"buffer_load_dwordx4 v29, s[52:55], s57 offen offset:0 lds\n\t"
"buffer_load_dwordx4 v29, s[52:55], s57 offen offset:3072 lds\n\t"
"s_add_u32 m0, s59, 0\n\t"
"s_add_u32 s57, s58, 368640\n\t"
"buffer_load_dwordx4 v29, s[52:55], s57 offen offset:0 lds\n\t"
"buffer_load_dwordx4 v29, s[52:55], s57 offen offset:3072 lds\n\t"
"s_add_u32 m0, s59, 6144\n\t"
"s_add_u32 s57, s58, 374784\n\t"
"buffer_load_dwordx4 v29, s[52:55], s57 offen offset:0 lds\n\t"
"buffer_load_dwordx4 v29, s[52:55], s57 offen offset:3072 lds\n\t"
"s_add_u32 m0, s59, 12288\n\t"
"s_add_u32 s57, s58, 589824\n\t"
"buffer_load_dwordx4 v29, s[52:55], s57 offen offset:0 lds\n\t"
"buffer_load_dwordx4 v29, s[52:55], s57 offen offset:3072 lds\n\t"
"s_add_u32 m0, s59, 18432\n\t"
"s_add_u32 s57, s58, 595968\n\t"
"buffer_load_dwordx4 v29, s[52:55], s57 offen offset:0 lds\n\t"
"buffer_load_dwordx4 v29, s[52:55], s57 offen offset:3072 lds\n\t"
"Ljoin_%=:\n\t"
"s_barrier\n\t"
"s_cmp_eq_u32 s73, 0\n\t"
"s_cbranch_scc1 Lsk0_%=\n\t"
"s_add_u32 m0, s25, 0\n\t"
"s_nop 0\n\t"
"buffer_load_dwordx4 v254, s[12:15], 0 offen nt lds\n\t"
"s_cmp_eq_u32 s73, 3\n\t"
"s_cselect_b32 s62, -1, 1\n\t"
"s_mul_i32 s63, s62, 59904\n\t"
"s_mul_i32 s62, s62, 1769472\n\t"
"v_add_u32 v19, s62, v254\n\t"
"s_add_u32 m0, s25, s63\n\t"
"s_nop 0\n\t"
"buffer_load_dwordx4 v19, s[12:15], 0 offen nt lds\n\t"
"s_add_u32 m0, s24, 59904\n\t"
"s_add_u32 s28, s21, 1769472\n\t"
"buffer_load_dwordx4 v28, s[12:15], s28 offen offset:0 nt lds\n\t"
"s_add_u32 m0, s24, 66048\n\t"
"s_add_u32 s28, s21, 1966080\n\t"
"buffer_load_dwordx4 v28, s[12:15], s28 offen offset:0 nt lds\n\t"
"s_add_u32 m0, s24, 72192\n\t"
"s_add_u32 s28, s21, 2162688\n\t"
"buffer_load_dwordx4 v28, s[12:15], s28 offen offset:0 nt lds\n\t"
"s_mul_i32 s60, s59, 6\n\t"
"s_mul_i32 s61, s59, 192\n\t"
"s_add_u32 s61, s61, s21\n\t"
"s_mul_i32 s62, s73, 3072\n\t"
"v_subrev_u32 v30, s62, v28\n\t"
"s_add_u32 m0, s60, 102912\n\t"
"s_add_u32 s28, s61, 1769472\n\t"
"buffer_load_dwordx4 v30, s[12:15], s28 offen nt lds\n\t"
"s_add_u32 m0, s24, 79872\n\t"
"s_add_u32 s28, s21, 2359296\n\t"
"buffer_load_dwordx4 v28, s[12:15], s28 offen offset:0 nt lds\n\t"
"s_add_u32 m0, s24, 86016\n\t"
"s_add_u32 s28, s21, 2555904\n\t"
"buffer_load_dwordx4 v28, s[12:15], s28 offen offset:0 nt lds\n\t"
"s_add_u32 m0, s24, 92160\n\t"
"s_add_u32 s28, s21, 2752512\n\t"
"buffer_load_dwordx4 v28, s[12:15], s28 offen offset:0 nt lds\n\t"
"s_add_u32 m0, s60, 122880\n\t"
"s_add_u32 s28, s61, 2359296\n\t"
"buffer_load_dwordx4 v30, s[12:15], s28 offen nt lds\n\t"
"s_waitcnt vmcnt(10)\n\t"
"Lsk0_%=:\n\t"
"s_cmp_lt_u32 s69, 216\n\t"
"s_cbranch_scc0 Lna1_%=\n\t"
"s_waitcnt vmcnt(0)\n\t"
"s_add_u32 s71, s71, 1\n\t"
"v_mov_b32 v31, s71\n\t"
"s_mov_b64 exec, 1\n\t"
"buffer_store_dword v31, off, s[8:11], s70 sc1\n\t"
"s_mov_b64 exec, -1\n\t"
"Lna1_%=:\n\t"
"s_barrier\n\t"
"ds_read_b64_tr_b16 a[0:1], v20 offset:24576\n\t"
"ds_read_b64_tr_b16 a[2:3], v20 offset:30720\n\t"
"ds_read_b64_tr_b16 a[24:25], v20 offset:24584\n\t"
"ds_read_b64_tr_b16 a[26:27], v20 offset:30728\n\t"
"ds_read_b64_tr_b16 a[48:49], v21 offset:26624\n\t"
"ds_read_b64_tr_b16 a[50:51], v21 offset:32768\n\t"
"ds_read_b64_tr_b16 a[8:9], v20 offset:36864\n\t"
"ds_read_b64_tr_b16 a[10:11], v24 offset:0\n\t"
"ds_read_b64_tr_b16 a[32:33], v20 offset:36872\n\t"
"ds_read_b64_tr_b16 a[34:35], v24 offset:8\n\t"
"ds_read_b64_tr_b16 a[56:57], v21 offset:38912\n\t"
"ds_read_b64_tr_b16 a[58:59], v25 offset:2048\n\t"
"ds_read_b64_tr_b16 a[16:17], v24 offset:6144\n\t"
"ds_read_b64_tr_b16 a[18:19], v22 offset:0\n\t"
"ds_read_b64_tr_b16 a[40:41], v24 offset:6152\n\t"
"ds_read_b64_tr_b16 a[42:43], v22 offset:8\n\t"
"ds_read_b64_tr_b16 a[64:65], v25 offset:8192\n\t"
"ds_read_b64_tr_b16 a[66:67], v23 offset:2048\n\t"
"ds_read_b64_tr_b16 a[72:73], v22 offset:6144\n\t"
"ds_read_b64_tr_b16 a[74:75], v22 offset:12288\n\t"
"ds_read_b64_tr_b16 a[96:97], v22 offset:6152\n\t"
"ds_read_b64_tr_b16 a[98:99], v22 offset:12296\n\t"
"ds_read_b64_tr_b16 a[120:121], v23 offset:8192\n\t"
"ds_read_b64_tr_b16 a[122:123], v23 offset:14336\n\t"
"ds_read_b64_tr_b16 a[80:81], v20 offset:0\n\t"
"ds_read_b64_tr_b16 a[82:83], v20 offset:6144\n\t"
"ds_read_b64_tr_b16 a[104:105], v20 offset:8\n\t"
"ds_read_b64_tr_b16 a[106:107], v20 offset:6152\n\t"
"ds_read_b64_tr_b16 a[128:129], v21 offset:2048\n\t"
"ds_read_b64_tr_b16 a[130:131], v21 offset:8192\n\t"
"ds_read_b64_tr_b16 a[88:89], v20 offset:12288\n\t"
"ds_read_b64_tr_b16 a[90:91], v20 offset:18432\n\t"
"ds_read_b64_tr_b16 a[112:113], v20 offset:12296\n\t"
"ds_read_b64_tr_b16 a[114:115], v20 offset:18440\n\t"
"ds_read_b64_tr_b16 a[136:137], v21 offset:14336\n\t"
"ds_read_b64_tr_b16 a[138:139], v21 offset:20480\n\t"
"s_mov_b32 s41, 0xc0423f90\n\t"
"s_movk_i32 s43, 0x101\n\t"
"v_mov_b32 v6, 0x01000100\n\t"
"v_mov_b32 v7, 0xbebd0bd1\n\t"
"v_lshrrev_b32 v240, 16, v244\n\t"
"v_lshrrev_b32 v242, 16, v245\n\t"
"v_xor_b32 v241, 32, v240\n\t"
"v_and_b32 v244, 0xffff, v244\n\t"
"v_and_b32 v245, 0xffff, v245\n\t"
"v_cmp_le_u32 vcc, 9216, v242\n\t"
"v_mov_b32 v243, 0x3ea80a81\n\t"
"v_cndmask_b32 v243, v243, v7, vcc\n\t"
"v_and_b32 v8, 0xf000f, v247\n\t"
"v_mad_u32_u24 v8, v8, s43, v6\n\t"
"v_perm_b32 v196, s41, 0, v8\n\t"
"v_lshrrev_b32 v9, 4, v247\n\t"
"v_and_b32 v9, 0xf000f, v9\n\t"
"v_mad_u32_u24 v9, v9, s43, v6\n\t"
"v_perm_b32 v197, s41, 0, v9\n\t"
"v_lshrrev_b32 v10, 8, v247\n\t"
"v_and_b32 v10, 0xf000f, v10\n\t"
"v_mad_u32_u24 v10, v10, s43, v6\n\t"
"v_perm_b32 v198, s41, 0, v10\n\t"
"v_lshrrev_b32 v11, 12, v247\n\t"
"v_and_b32 v11, 0xf000f, v11\n\t"
"v_mad_u32_u24 v11, v11, s43, v6\n\t"
"v_perm_b32 v199, s41, 0, v11\n\t"
"v_mov_b32 v247, v28\n\t"
"s_cmp_eq_u32 s73, 0\n\t"
"s_cbranch_scc1 Lsk1_%=\n\t"
"s_add_u32 m0, s24, 19968\n\t"
"s_add_u32 s28, s21, 589824\n\t"
"buffer_load_dwordx4 v28, s[12:15], s28 offen offset:0 nt lds\n\t"
"s_add_u32 m0, s24, 26112\n\t"
"s_add_u32 s28, s21, 786432\n\t"
"buffer_load_dwordx4 v28, s[12:15], s28 offen offset:0 nt lds\n\t"
"s_add_u32 m0, s24, 32256\n\t"
"s_add_u32 s28, s21, 983040\n\t"
"buffer_load_dwordx4 v28, s[12:15], s28 offen offset:0 nt lds\n\t"
"s_add_u32 m0, s60, 62976\n\t"
"s_add_u32 s28, s61, 589824\n\t"
"buffer_load_dwordx4 v30, s[12:15], s28 offen nt lds\n\t"
"s_add_u32 m0, s24, 0\n\t"
"s_add_u32 s28, s21, 0\n\t"
"buffer_load_dwordx4 v28, s[12:15], s28 offen offset:0 nt lds\n\t"
"s_add_u32 m0, s24, 6144\n\t"
"s_add_u32 s28, s21, 196608\n\t"
"buffer_load_dwordx4 v28, s[12:15], s28 offen offset:0 nt lds\n\t"
"s_add_u32 m0, s24, 12288\n\t"
"s_add_u32 s28, s21, 393216\n\t"
"buffer_load_dwordx4 v28, s[12:15], s28 offen offset:0 nt lds\n\t"
"s_add_u32 m0, s60, 43008\n\t"
"s_add_u32 s28, s61, 0\n\t"
"buffer_load_dwordx4 v30, s[12:15], s28 offen nt lds\n\t"
"Lsk1_%=:\n\t"
"s_waitcnt lgkmcnt(0)\n\t"
"s_mov_b64 vcc, -1\n\t"
"s_bitcmp1_b32 s73, 1\n\t"
"s_cbranch_scc1 Lvh0_%=\n\t"
"s_bitcmp1_b32 s73, 0\n\t"
"s_cbranch_scc1 Lpg2_%=\n\t"
"s_branch Lpg1_%=\n\t"
"Lvh0_%=:\n\t"
"s_bitcmp1_b32 s73, 0\n\t"
"s_cbranch_scc1 Lpg4_%=\n\t"
"s_branch Lpg3_%=\n\t"
"Lvr0_%=:\n\t"
"s_mov_b64 vcc, 0\n\t"
"v_add_u32 v240, 59904, v240\n\t"
"v_add_u32 v241, 59904, v241\n\t"
"s_cmp_eq_u32 s73, 0\n\t"
"s_cbranch_scc1 Lsk2_%=\n\t"
"s_waitcnt vmcnt(8)\n\t"
"Lsk2_%=:\n\t"
"s_barrier\n\t"
"ds_read_b64_tr_b16 v[120:121], v240 offset:0\n\t"
"ds_read_b64_tr_b16 v[122:123], v240 offset:1536\n\t"
"ds_read_b64_tr_b16 v[124:125], v241 offset:0\n\t"
"ds_read_b64_tr_b16 v[126:127], v241 offset:1536\n\t"
"ds_read_b64_tr_b16 v[128:129], v240 offset:19968\n\t"
"ds_read_b64_tr_b16 v[130:131], v240 offset:21504\n\t"
"ds_read_b64_tr_b16 v[132:133], v241 offset:19968\n\t"
"ds_read_b64_tr_b16 v[134:135], v241 offset:21504\n\t"
"s_cmp_eq_u32 s73, 0\n\t"
"s_cbranch_scc1 Lsk3_%=\n\t"
"s_add_u32 m0, s24, 39936\n\t"
"s_add_u32 s28, s21, 1179648\n\t"
"buffer_load_dwordx4 v28, s[12:15], s28 offen offset:0 nt lds\n\t"
"s_add_u32 m0, s24, 46080\n\t"
"s_add_u32 s28, s21, 1376256\n\t"
"buffer_load_dwordx4 v28, s[12:15], s28 offen offset:0 nt lds\n\t"
"s_add_u32 m0, s24, 52224\n\t"
"s_add_u32 s28, s21, 1572864\n\t"
"buffer_load_dwordx4 v28, s[12:15], s28 offen offset:0 nt lds\n\t"
"s_add_u32 m0, s60, 82944\n\t"
"s_add_u32 s28, s61, 1179648\n\t"
"buffer_load_dwordx4 v30, s[12:15], s28 offen nt lds\n\t"
"Lsk3_%=:\n\t"
"s_waitcnt lgkmcnt(6)\n\t"
"v_mfma_f32_16x16x32_bf16 v[120:123], v[120:123], v[196:199], 0\n\t"
"s_waitcnt lgkmcnt(4)\n\t"
"v_mfma_f32_16x16x32_bf16 v[124:127], v[124:127], v[196:199], 0\n\t"
"s_waitcnt lgkmcnt(2)\n\t"
"v_mfma_f32_16x16x32_bf16 v[128:131], v[128:131], v[196:199], 0\n\t"
"s_waitcnt lgkmcnt(0)\n\t"
"v_mfma_f32_16x16x32_bf16 v[132:135], v[132:135], v[196:199], 0\n\t"
"v_pk_mul_f32 v[120:121], v[120:121], v[242:243] op_sel:[0,1] op_sel_hi:[1,1]\n\t"
"v_pk_mul_f32 v[122:123], v[122:123], v[242:243] op_sel:[0,1] op_sel_hi:[1,1]\n\t"
"v_cvt_pk_bf16_f32 v120, v120, v121\n\t"
"v_cvt_pk_bf16_f32 v121, v122, v123\n\t"
"v_pk_mul_f32 v[124:125], v[124:125], v[242:243] op_sel:[0,1] op_sel_hi:[1,1]\n\t"
"v_pk_mul_f32 v[126:127], v[126:127], v[242:243] op_sel:[0,1] op_sel_hi:[1,1]\n\t"
"v_cvt_pk_bf16_f32 v122, v124, v125\n\t"
"v_cvt_pk_bf16_f32 v123, v126, v127\n\t"
"ds_write_b128 v242, v[120:123] offset:29184\n\t"
"v_pk_mul_f32 v[128:129], v[128:129], v[242:243] op_sel:[0,1] op_sel_hi:[1,1]\n\t"
"v_pk_mul_f32 v[130:131], v[130:131], v[242:243] op_sel:[0,1] op_sel_hi:[1,1]\n\t"
"v_cvt_pk_bf16_f32 v128, v128, v129\n\t"
"v_cvt_pk_bf16_f32 v129, v130, v131\n\t"
"v_pk_mul_f32 v[132:133], v[132:133], v[242:243] op_sel:[0,1] op_sel_hi:[1,1]\n\t"
"v_pk_mul_f32 v[134:135], v[134:135], v[242:243] op_sel:[0,1] op_sel_hi:[1,1]\n\t"
"v_cvt_pk_bf16_f32 v130, v132, v133\n\t"
"v_cvt_pk_bf16_f32 v131, v134, v135\n\t"
"ds_write_b128 v242, v[128:131] offset:30720\n\t"
"s_waitcnt lgkmcnt(0)\n\t"
"v_subrev_u32 v240, 59904, v240\n\t"
"v_subrev_u32 v241, 59904, v241\n\t"
"s_cmp_eq_u32 s73, 0\n\t"
"s_cbranch_scc1 Lsk4_%=\n\t"
"s_waitcnt vmcnt(4)\n\t"
"Lsk4_%=:\n\t"
"s_barrier\n\t"
"ds_read_b64_tr_b16 v[136:137], v240 offset:19968\n\t"
"ds_read_b64_tr_b16 v[138:139], v240 offset:21504\n\t"
"ds_read_b64_tr_b16 v[140:141], v241 offset:19968\n\t"
"ds_read_b64_tr_b16 v[142:143], v241 offset:21504\n\t"
"ds_read_b64_tr_b16 v[144:145], v240 offset:0\n\t"
"ds_read_b64_tr_b16 v[146:147], v240 offset:1536\n\t"
"ds_read_b64_tr_b16 v[148:149], v241 offset:0\n\t"
"ds_read_b64_tr_b16 v[150:151], v241 offset:1536\n\t"
"s_cmp_eq_u32 s73, 0\n\t"
"s_cbranch_scc1 Lsk5_%=\n\t"
"s_add_u32 m0, s24, 99840\n\t"
"s_add_u32 s28, s21, 2949120\n\t"
"buffer_load_dwordx4 v28, s[12:15], s28 offen offset:0 nt lds\n\t"
"s_add_u32 m0, s24, 105984\n\t"
"s_add_u32 s28, s21, 3145728\n\t"
"buffer_load_dwordx4 v28, s[12:15], s28 offen offset:0 nt lds\n\t"
"s_add_u32 m0, s24, 112128\n\t"
"s_add_u32 s28, s21, 3342336\n\t"
"buffer_load_dwordx4 v28, s[12:15], s28 offen offset:0 nt lds\n\t"
"s_add_u32 m0, s60, 142848\n\t"
"s_add_u32 s28, s61, 2949120\n\t"
"buffer_load_dwordx4 v30, s[12:15], s28 offen nt lds\n\t"
"Lsk5_%=:\n\t"
"s_waitcnt lgkmcnt(6)\n\t"
"v_mfma_f32_16x16x32_bf16 v[136:139], v[136:139], v[196:199], 0\n\t"
"s_waitcnt lgkmcnt(4)\n\t"
"v_mfma_f32_16x16x32_bf16 v[140:143], v[140:143], v[196:199], 0\n\t"
"s_waitcnt lgkmcnt(2)\n\t"
"v_mfma_f32_16x16x32_bf16 v[144:147], v[144:147], v[196:199], 0\n\t"
"s_waitcnt lgkmcnt(0)\n\t"
"v_mfma_f32_16x16x32_bf16 v[148:151], v[148:151], v[196:199], 0\n\t"
"v_pk_mul_f32 v[136:137], v[136:137], v[242:243] op_sel:[0,1] op_sel_hi:[1,1]\n\t"
"v_pk_mul_f32 v[138:139], v[138:139], v[242:243] op_sel:[0,1] op_sel_hi:[1,1]\n\t"
"v_cvt_pk_bf16_f32 v136, v136, v137\n\t"
"v_cvt_pk_bf16_f32 v137, v138, v139\n\t"
"v_pk_mul_f32 v[140:141], v[140:141], v[242:243] op_sel:[0,1] op_sel_hi:[1,1]\n\t"
"v_pk_mul_f32 v[142:143], v[142:143], v[242:243] op_sel:[0,1] op_sel_hi:[1,1]\n\t"
"v_cvt_pk_bf16_f32 v138, v140, v141\n\t"
"v_cvt_pk_bf16_f32 v139, v142, v143\n\t"
"ds_write_b128 v242, v[136:139] offset:26112\n\t"
"v_pk_mul_f32 v[144:145], v[144:145], v[242:243] op_sel:[0,1] op_sel_hi:[1,1]\n\t"
"v_pk_mul_f32 v[146:147], v[146:147], v[242:243] op_sel:[0,1] op_sel_hi:[1,1]\n\t"
"v_cvt_pk_bf16_f32 v144, v144, v145\n\t"
"v_cvt_pk_bf16_f32 v145, v146, v147\n\t"
"v_pk_mul_f32 v[148:149], v[148:149], v[242:243] op_sel:[0,1] op_sel_hi:[1,1]\n\t"
"v_pk_mul_f32 v[150:151], v[150:151], v[242:243] op_sel:[0,1] op_sel_hi:[1,1]\n\t"
"v_cvt_pk_bf16_f32 v146, v148, v149\n\t"
"v_cvt_pk_bf16_f32 v147, v150, v151\n\t"
"ds_write_b128 v242, v[144:147] offset:24576\n\t"
"s_mov_b64 vcc, 0\n\t"
"s_waitcnt lgkmcnt(0)\n\t"
"s_barrier\n\t"
"ds_read_b128 a[168:171], v244 offset:26112\n\t"
"ds_read_b128 a[172:175], v244 offset:26624\n\t"
"ds_read_b128 a[176:179], v244 offset:27136\n\t"
"ds_read_b128 a[180:183], v244 offset:35328\n\t"
"ds_read_b128 a[184:187], v244 offset:35840\n\t"
"ds_read_b128 a[188:191], v244 offset:36352\n\t"
"ds_read_b128 a[192:195], v244 offset:25600\n\t"
"s_waitcnt lgkmcnt(4)\n\t"
"v_mfma_f32_16x16x32_bf16 v[14:17], a[0:3], a[168:171], 0\n\t"
"ds_read_b64_tr_b16 v[224:225], v240 offset:512\n\t"
"v_mfma_f32_16x16x32_bf16 v[14:17], a[8:11], a[172:175], v[14:17]\n\t"
"ds_read_b64_tr_b16 v[226:227], v240 offset:2048\n\t"
"v_mfma_f32_16x16x32_bf16 v[14:17], a[16:19], a[176:179], v[14:17]\n\t"
"ds_read_b64_tr_b16 v[228:229], v241 offset:512\n\t"
"v_mfma_f32_16x16x32_bf16 v[46:49], a[24:27], a[168:171], 0\n\t"
"ds_read_b64_tr_b16 v[230:231], v241 offset:2048\n\t"
"v_mfma_f32_16x16x32_bf16 v[46:49], a[32:35], a[172:175], v[46:49]\n\t"
"v_mfma_f32_16x16x32_bf16 v[46:49], a[40:43], a[176:179], v[46:49]\n\t"
"v_mfma_f32_16x16x32_bf16 v[78:81], a[48:51], a[168:171], 0\n\t"
"s_waitcnt lgkmcnt(5)\n\t"
"v_mfma_f32_16x16x32_bf16 v[78:81], a[56:59], a[172:175], v[78:81]\n\t"
"ds_read_b64_tr_b16 v[232:233], v240 offset:20480\n\t"
"v_mfma_f32_16x16x32_bf16 v[78:81], a[64:67], a[176:179], v[78:81]\n\t"
"s_cmp_lg_u32 s73, 0\n\t"
"s_cbranch_scc1 Lnp_%=\n\t"
"buffer_load_dwordx4 v[248:251], v252, s[8:11], s72 offen sc1\n\t"
"Lnp_%=:\n\t"
"ds_read_b64_tr_b16 v[234:235], v240 offset:22016\n\t"
"v_mfma_f32_16x16x32_bf16 v[112:115], a[72:75], a[180:183], 0\n\t"
"s_waitcnt lgkmcnt(4)\n\t"
"ds_read_b64_tr_b16 v[236:237], v241 offset:20480\n\t"
"v_mfma_f32_16x16x32_bf16 v[112:115], a[80:83], a[184:187], v[112:115]\n\t"
"s_waitcnt lgkmcnt(3)\n\t"
"ds_read_b64_tr_b16 v[238:239], v241 offset:22016\n\t"
"v_mfma_f32_16x16x32_bf16 v[112:115], a[88:91], a[188:191], v[112:115]\n\t"
"ds_read_b128 a[196:199], v244 offset:34816\n\t"
"v_mfma_f32_16x16x32_bf16 v[224:227], v[224:227], v[196:199], 0\n\t"
"v_mfma_f32_16x16x32_bf16 v[228:231], v[228:231], v[196:199], 0\n\t"
"v_mfma_f32_16x16x32_bf16 v[144:147], a[96:99], a[180:183], 0\n\t"
"v_mfma_f32_16x16x32_bf16 v[144:147], a[104:107], a[184:187], v[144:147]\n\t"
"v_mfma_f32_16x16x32_bf16 v[144:147], a[112:115], a[188:191], v[144:147]\n\t"
"v_mfma_f32_16x16x32_bf16 v[176:179], a[120:123], a[180:183], 0\n\t"
"v_cvt_pk_bf16_f32 v224, v224, v225\n\t"
"v_cvt_pk_bf16_f32 v225, v226, v227\n\t"
"v_mfma_f32_16x16x32_bf16 v[176:179], a[128:131], a[184:187], v[176:179]\n\t"
"ds_read_b128 a[176:179], v244 offset:25088\n\t"
"v_cvt_pk_bf16_f32 v226, v228, v229\n\t"
"v_mfma_f32_16x16x32_bf16 v[176:179], a[136:139], a[188:191], v[176:179]\n\t"
"v_cvt_pk_bf16_f32 v227, v230, v231\n\t"
"v_mfma_f32_16x16x32_bf16 v[10:13], a[0:3], a[192:195], 0\n\t"
"s_waitcnt lgkmcnt(4)\n\t"
"v_mfma_f32_16x16x32_bf16 v[10:13], a[8:11], a[168:171], v[10:13]\n\t"
"s_waitcnt lgkmcnt(2)\n\t"
"v_mfma_f32_16x16x32_bf16 v[10:13], a[16:19], a[172:175], v[10:13]\n\t"
"v_mfma_f32_16x16x32_bf16 v[232:235], v[232:235], v[196:199], 0\n\t"
"v_mfma_f32_16x16x32_bf16 v[236:239], v[236:239], v[196:199], 0\n\t"
"s_waitcnt vmcnt(4)\n\t"
"s_barrier\n\t"
"v_mfma_f32_16x16x32_bf16 v[42:45], a[24:27], a[192:195], 0\n\t"
"ds_read_b64_tr_b16 v[228:229], v240 offset:39936\n\t"
"v_mfma_f32_16x16x32_bf16 v[42:45], a[32:35], a[168:171], v[42:45]\n\t"
"ds_read_b64_tr_b16 v[230:231], v240 offset:41472\n\t"
"v_mfma_f32_16x16x32_bf16 v[42:45], a[40:43], a[172:175], v[42:45]\n\t"
"ds_write_b128 v242, v[224:227] offset:0\n\t"
"v_mfma_f32_16x16x32_bf16 v[74:77], a[48:51], a[192:195], 0\n\t"
"s_waitcnt lgkmcnt(4)\n\t"
"v_cvt_pk_bf16_f32 v232, v232, v233\n\t"
"v_cvt_pk_bf16_f32 v233, v234, v235\n\t"
"v_mfma_f32_16x16x32_bf16 v[74:77], a[56:59], a[168:171], v[74:77]\n\t"
"ds_read_b64_tr_b16 v[224:225], v241 offset:39936\n\t"
"v_mfma_f32_16x16x32_bf16 v[74:77], a[64:67], a[172:175], v[74:77]\n\t"
"ds_read_b64_tr_b16 v[226:227], v241 offset:41472\n\t"
"v_cvt_pk_bf16_f32 v234, v236, v237\n\t"
"v_mfma_f32_16x16x32_bf16 v[108:111], a[72:75], a[196:199], 0\n\t"
"s_waitcnt lgkmcnt(3)\n\t"
"ds_read_b128 a[188:191], v244 offset:34304\n\t"
"v_cvt_pk_bf16_f32 v235, v238, v239\n\t"
"v_mfma_f32_16x16x32_bf16 v[108:111], a[80:83], a[180:183], v[108:111]\n\t"
"ds_read_b64_tr_b16 v[236:237], v240 offset:40448\n\t"
"v_mfma_f32_16x16x32_bf16 v[108:111], a[88:91], a[184:187], v[108:111]\n\t"
"ds_write_b128 v242, v[232:235] offset:1536\n\t"
"v_mfma_f32_16x16x32_bf16 v[228:231], v[228:231], v[196:199], 0\n\t"
"s_waitcnt lgkmcnt(3)\n\t"
"v_mfma_f32_16x16x32_bf16 v[224:227], v[224:227], v[196:199], 0\n\t"
"ds_read_b64_tr_b16 v[238:239], v240 offset:41984\n\t"
"v_mfma_f32_16x16x32_bf16 v[140:143], a[96:99], a[196:199], 0\n\t"
"ds_read_b64_tr_b16 v[232:233], v241 offset:40448\n\t"
"v_mfma_f32_16x16x32_bf16 v[140:143], a[104:107], a[180:183], v[140:143]\n\t"
"ds_read_b64_tr_b16 v[234:235], v241 offset:41984\n\t"
"v_mfma_f32_16x16x32_bf16 v[140:143], a[112:115], a[184:187], v[140:143]\n\t"
"v_pk_mul_f32 v[228:229], v[228:229], v[242:243] op_sel:[0,1] op_sel_hi:[1,1]\n\t"
"v_pk_mul_f32 v[230:231], v[230:231], v[242:243] op_sel:[0,1] op_sel_hi:[1,1]\n\t"
"v_mfma_f32_16x16x32_bf16 v[172:175], a[120:123], a[196:199], 0\n\t"
"v_cvt_pk_bf16_f32 v228, v228, v229\n\t"
"v_cvt_pk_bf16_f32 v229, v230, v231\n\t"
"v_mfma_f32_16x16x32_bf16 v[172:175], a[128:131], a[180:183], v[172:175]\n\t"
"v_mfma_f32_16x16x32_bf16 v[172:175], a[136:139], a[184:187], v[172:175]\n\t"
"v_pk_mul_f32 v[224:225], v[224:225], v[242:243] op_sel:[0,1] op_sel_hi:[1,1]\n\t"
"v_pk_mul_f32 v[226:227], v[226:227], v[242:243] op_sel:[0,1] op_sel_hi:[1,1]\n\t"
"v_mfma_f32_16x16x32_bf16 v[6:9], a[0:3], a[176:179], 0\n\t"
"v_cvt_pk_bf16_f32 v230, v224, v225\n\t"
"v_cvt_pk_bf16_f32 v231, v226, v227\n\t"
"v_mfma_f32_16x16x32_bf16 v[6:9], a[8:11], a[192:195], v[6:9]\n\t"
"v_add_u32 v240, 59904, v240\n\t"
"v_add_u32 v241, 59904, v241\n\t"
"v_mfma_f32_16x16x32_bf16 v[6:9], a[16:19], a[168:171], v[6:9]\n\t"
"ds_write_b128 v242, v[228:231] offset:27648\n\t"
"v_mfma_f32_16x16x32_bf16 v[38:41], a[24:27], a[176:179], 0\n\t"
"s_waitcnt lgkmcnt(3)\n\t"
"v_mfma_f32_16x16x32_bf16 v[38:41], a[32:35], a[192:195], v[38:41]\n\t"
"s_waitcnt lgkmcnt(1)\n\t"
"ds_read_b64_tr_b16 v[224:225], v240 offset:512\n\t"
"v_mfma_f32_16x16x32_bf16 v[38:41], a[40:43], a[168:171], v[38:41]\n\t"
"ds_read_b64_tr_b16 v[226:227], v240 offset:2048\n\t"
"v_mfma_f32_16x16x32_bf16 v[236:239], v[236:239], v[196:199], 0\n\t"
"ds_read_b64_tr_b16 v[228:229], v241 offset:512\n\t"
"v_mfma_f32_16x16x32_bf16 v[232:235], v[232:235], v[196:199], 0\n\t"
"ds_read_b64_tr_b16 v[230:231], v241 offset:2048\n\t"
"v_mfma_f32_16x16x32_bf16 v[70:73], a[48:51], a[176:179], 0\n\t"
"s_mov_b32 s20, 294912\n\t"
"v_mfma_f32_16x16x32_bf16 v[70:73], a[56:59], a[192:195], v[70:73]\n\t"
"v_mfma_f32_16x16x32_bf16 v[70:73], a[64:67], a[168:171], v[70:73]\n\t"
"v_mfma_f32_16x16x32_bf16 v[104:107], a[72:75], a[188:191], 0\n\t"
"v_cvt_pk_bf16_f32 v236, v236, v237\n\t"
"v_cvt_pk_bf16_f32 v237, v238, v239\n\t"
"v_mfma_f32_16x16x32_bf16 v[104:107], a[80:83], a[196:199], v[104:107]\n\t"
"v_cvt_pk_bf16_f32 v238, v232, v233\n\t"
"v_mfma_f32_16x16x32_bf16 v[104:107], a[88:91], a[180:183], v[104:107]\n\t"
"s_waitcnt lgkmcnt(4)\n\t"
"s_cmp_lg_u32 s73, 0\n\t"
"s_cbranch_scc1 Lgo_%=\n\t"
"Lchk_%=:\n\t"
"s_waitcnt vmcnt(0)\n\t"
"v_cmpx_ne_u32_e64 s[60:61], v220, v248\n\t"
"v_cmpx_ne_u32_e64 s[60:61], v221, v249\n\t"
"v_cmpx_ne_u32_e64 s[60:61], v222, v250\n\t"
"v_cmpx_ne_u32_e64 s[60:61], v223, v251\n\t"
"s_not_b64 s[60:61], s[60:61]\n\t"
"s_mov_b64 exec, -1\n\t"
"s_lshl_b64 s[60:61], s[60:61], 10\n\t"
"s_cbranch_scc0 Lgo_%=\n\t"
"s_sub_u32 s68, s68, 1\n\t"
"s_cbranch_scc1 Lfb_%=\n\t"
"buffer_load_dwordx4 v[248:251], v252, s[8:11], s72 offen sc1\n\t"
"s_branch Lchk_%=\n\t"
"Lgo_%=:\n\t"
"s_barrier\n\t"
"v_mfma_f32_16x16x32_bf16 v[136:139], a[96:99], a[188:191], 0\n\t"
"s_mov_b32 s52, 0x3fd80000\n\t"
"ds_read_b128 a[168:171], v244 offset:24576\n\t"
"v_mfma_f32_16x16x32_bf16 v[136:139], a[104:107], a[196:199], v[136:139]\n\t"
"s_mov_b32 s53, s52\n\t"
"ds_read_b128 a[172:175], v244 offset:33792\n\t"
"v_cvt_pk_bf16_f32 v239, v234, v235\n\t"
"v_mfma_f32_16x16x32_bf16 v[136:139], a[112:115], a[180:183], v[136:139]\n\t"
"s_mov_b32 s54, 0x3e97b426\n\t"
"ds_write_b128 v242, v[236:239] offset:3072\n\t"
"v_mfma_f32_16x16x32_bf16 v[168:171], a[120:123], a[188:191], 0\n\t"
"buffer_load_dwordx4 a[144:147], v252, s[8:11], s20 offen offset:0 sc1\n\t"
"v_mfma_f32_16x16x32_bf16 v[168:171], a[128:131], a[196:199], v[168:171]\n\t"
"buffer_load_dwordx4 a[152:155], v252, s[8:11], s20 offen offset:1024 sc1\n\t"
"v_mfma_f32_16x16x32_bf16 v[168:171], a[136:139], a[180:183], v[168:171]\n\t"
"s_waitcnt lgkmcnt(2)\n\t"
"ds_read_b128 a[184:187], v244 offset:26624\n\t"
"s_mov_b32 s55, s54\n\t"
"v_mfma_f32_16x16x32_bf16 v[224:227], v[224:227], v[196:199], 0\n\t"
"buffer_load_dwordx4 a[160:163], v252, s[8:11], s20 offen offset:2048 sc1\n\t"
"v_mfma_f32_16x16x32_bf16 v[228:231], v[228:231], v[196:199], 0\n\t"
"s_mov_b32 s20, 307200\n\t"
"ds_read_b128 a[200:203], v244 offset:27136\n\t"
"s_mov_b32 s56, 0x3f400000\n\t"
"v_mfma_f32_16x16x32_bf16 v[2:5], a[0:3], a[168:171], 0\n\t"
"s_mov_b32 s57, s56\n\t"
"ds_read_b128 a[204:207], v244 offset:27648\n\t"
"s_mov_b32 s58, 0x3f2aaaab\n\t"
"v_mfma_f32_16x16x32_bf16 v[2:5], a[8:11], a[176:179], v[2:5]\n\t"
"s_mov_b32 s59, s58\n\t"
"s_mov_b32 s60, 0x3ee38e39\n\t"
"v_mfma_f32_16x16x32_bf16 v[2:5], a[16:19], a[192:195], v[2:5]\n\t"
"s_waitcnt vmcnt(3)\n\t"
"s_barrier\n\t"
"v_mfma_f32_16x16x32_bf16 v[34:37], a[24:27], a[168:171], 0\n\t"
"s_mov_b32 s61, s60\n\t"
"ds_read_b64_tr_b16 v[232:233], v240 offset:39936\n\t"
"v_mfma_f32_16x16x32_bf16 v[34:37], a[32:35], a[176:179], v[34:37]\n\t"
"s_mov_b32 s62, 0x40100000\n\t"
"ds_read_b64_tr_b16 v[234:235], v240 offset:41472\n\t"
"v_cvt_pk_bf16_f32 v224, v224, v225\n\t"
"v_mfma_f32_16x16x32_bf16 v[34:37], a[40:43], a[192:195], v[34:37]\n\t"
"buffer_load_dwordx4 v[200:203], v252, s[8:11], s20 offen offset:0 sc1\n\t"
"v_mfma_f32_16x16x32_bf16 v[66:69], a[48:51], a[168:171], 0\n\t"
"s_waitcnt lgkmcnt(6)\n\t"
"ds_read_b64_tr_b16 v[236:237], v241 offset:39936\n\t"
"v_cvt_pk_bf16_f32 v225, v226, v227\n\t"
"v_mfma_f32_16x16x32_bf16 v[66:69], a[56:59], a[176:179], v[66:69]\n\t"
"s_mov_b32 s63, s62\n\t"
"ds_read_b64_tr_b16 v[238:239], v241 offset:41472\n\t"
"v_cvt_pk_bf16_f32 v226, v228, v229\n\t"
"v_mfma_f32_16x16x32_bf16 v[66:69], a[64:67], a[192:195], v[66:69]\n\t"
"buffer_load_dwordx4 v[208:211], v252, s[8:11], s20 offen offset:1024 sc1\n\t"
"v_cvt_pk_bf16_f32 v227, v230, v231\n\t"
"v_mfma_f32_16x16x32_bf16 v[100:103], a[72:75], a[172:175], 0\n\t"
"s_waitcnt lgkmcnt(2)\n\t"
"ds_read_b128 a[168:171], v244 offset:35840\n\t"
"s_mov_b32 s64, 0x3faaaaab\n\t"
"v_mfma_f32_16x16x32_bf16 v[100:103], a[80:83], a[188:191], v[100:103]\n\t"
"buffer_load_dwordx4 v[216:219], v252, s[8:11], s20 offen offset:2048 sc1\n\t"
"v_mfma_f32_16x16x32_bf16 v[100:103], a[88:91], a[196:199], v[100:103]\n\t"
"s_mov_b32 s20, 319488\n\t"
"ds_write_b128 v242, v[224:227] offset:4608\n\t"
"v_mfma_f32_16x16x32_bf16 v[232:235], v[232:235], v[196:199], 0\n\t"
"s_waitcnt lgkmcnt(2)\n\t"
"s_mov_b32 s65, s64\n\t"
"s_mov_b32 s66, 0x3f17b426\n\t"
"v_mfma_f32_16x16x32_bf16 v[236:239], v[236:239], v[196:199], 0\n\t"
"buffer_load_dwordx4 a[4:7], v252, s[8:11], s20 offen offset:0 sc1\n\t"
"v_mfma_f32_16x16x32_bf16 v[132:135], a[96:99], a[172:175], 0\n\t"
"buffer_load_dwordx4 a[12:15], v252, s[8:11], s20 offen offset:1024 sc1\n\t"
"v_mfma_f32_16x16x32_bf16 v[132:135], a[104:107], a[188:191], v[132:135]\n\t"
"s_mov_b32 s67, s66\n\t"
"ds_read_b128 a[176:179], v244 offset:36352\n\t"
"v_mfma_f32_16x16x32_bf16 v[132:135], a[112:115], a[196:199], v[132:135]\n\t"
"ds_read_b128 a[180:183], v244 offset:36864\n\t"
"ds_read_b64_tr_b16 v[228:229], v240 offset:20480\n\t"
"ds_read_b64_tr_b16 v[230:231], v240 offset:22016\n\t"
"v_pk_mul_f32 v[232:233], v[232:233], v[242:243] op_sel:[0,1] op_sel_hi:[1,1]\n\t"
"v_pk_mul_f32 v[234:235], v[234:235], v[242:243] op_sel:[0,1] op_sel_hi:[1,1]\n\t"
"v_mfma_f32_16x16x32_bf16 v[164:167], a[120:123], a[172:175], 0\n\t"
"Lpg1_%=:\n\t"
"s_cbranch_vccnz Lpg5_%=\n\t"
"ds_read_b64_tr_b16 v[224:225], v241 offset:20480\n\t"
"v_cvt_pk_bf16_f32 v232, v232, v233\n\t"
"v_mfma_f32_16x16x32_bf16 v[164:167], a[128:131], a[188:191], v[164:167]\n\t"
"ds_read_b64_tr_b16 v[226:227], v241 offset:22016\n\t"
"v_cvt_pk_bf16_f32 v233, v234, v235\n\t"
"v_mfma_f32_16x16x32_bf16 v[164:167], a[136:139], a[196:199], v[164:167]\n\t"
"buffer_load_dwordx4 a[20:23], v252, s[8:11], s20 offen offset:2048 sc1\n\t"
"s_mov_b32 s20, 479232\n\t"
"v_pk_mul_f32 v[236:237], v[236:237], v[242:243] op_sel:[0,1] op_sel_hi:[1,1]\n\t"
"v_pk_mul_f32 v[238:239], v[238:239], v[242:243] op_sel:[0,1] op_sel_hi:[1,1]\n\t"
"v_mfma_f32_16x16x32_bf16 v[18:21], a[0:3], a[184:187], 0\n\t"
"v_cvt_pk_bf16_f32 v234, v236, v237\n\t"
"v_cvt_pk_bf16_f32 v235, v238, v239\n\t"
"v_mfma_f32_16x16x32_bf16 v[18:21], a[8:11], a[200:203], v[18:21]\n\t"
"ds_read_b64_tr_b16 v[236:237], v240 offset:40448\n\t"
"v_mfma_f32_16x16x32_bf16 v[18:21], a[16:19], a[204:207], v[18:21]\n\t"
"buffer_load_dwordx4 a[28:31], v252, s[8:11], s20 offen offset:0 sc1\n\t"
"v_mfma_f32_16x16x32_bf16 v[50:53], a[24:27], a[184:187], 0\n\t"
"buffer_load_dwordx4 a[36:39], v252, s[8:11], s20 offen offset:1024 sc1\n\t"
"v_mfma_f32_16x16x32_bf16 v[50:53], a[32:35], a[200:203], v[50:53]\n\t"
"s_waitcnt lgkmcnt(1)\n\t"
"ds_read_b64_tr_b16 v[238:239], v240 offset:41984\n\t"
"v_mfma_f32_16x16x32_bf16 v[50:53], a[40:43], a[204:207], v[50:53]\n\t"
"ds_write_b128 v242, v[232:235] offset:32256\n\t"
"v_mfma_f32_16x16x32_bf16 v[228:231], v[228:231], v[196:199], 0\n\t"
"buffer_load_dwordx4 a[44:47], v252, s[8:11], s20 offen offset:2048 sc1\n\t"
"v_mfma_f32_16x16x32_bf16 v[224:227], v[224:227], v[196:199], 0\n\t"
"s_mov_b32 s20, 491520\n\t"
"ds_read_b64_tr_b16 v[232:233], v241 offset:40448\n\t"
"v_mfma_f32_16x16x32_bf16 v[82:85], a[48:51], a[184:187], 0\n\t"
"ds_read_b64_tr_b16 v[234:235], v241 offset:41984\n\t"
"v_mfma_f32_16x16x32_bf16 v[82:85], a[56:59], a[200:203], v[82:85]\n\t"
"buffer_load_dwordx4 a[52:55], v252, s[8:11], s20 offen offset:0 sc1\n\t"
"v_mfma_f32_16x16x32_bf16 v[82:85], a[64:67], a[204:207], v[82:85]\n\t"
"v_mfma_f32_16x16x32_bf16 v[116:119], a[72:75], a[168:171], 0\n\t"
"buffer_load_dwordx4 a[60:63], v252, s[8:11], s20 offen offset:1024 sc1\n\t"
"v_mfma_f32_16x16x32_bf16 v[116:119], a[80:83], a[176:179], v[116:119]\n\t"
"v_cvt_pk_bf16_f32 v228, v228, v229\n\t"
"v_mfma_f32_16x16x32_bf16 v[116:119], a[88:91], a[180:183], v[116:119]\n\t"
"s_waitcnt lgkmcnt(2)\n\t"
"s_barrier\n\t"
"v_mfma_f32_16x16x32_bf16 v[148:151], a[96:99], a[168:171], 0\n\t"
"ds_read_b128 a[188:191], v244 offset:28160\n\t"
"v_mfma_f32_16x16x32_bf16 v[148:151], a[104:107], a[176:179], v[148:151]\n\t"
"s_waitcnt lgkmcnt(1)\n\t"
"ds_read_b128 v[204:207], v244 offset:37376\n\t"
"v_cvt_pk_bf16_f32 v229, v230, v231\n\t"
"v_mfma_f32_16x16x32_bf16 v[148:151], a[112:115], a[180:183], v[148:151]\n\t"
"ds_read_b128 a[192:195], v244 offset:28672\n\t"
"v_cvt_pk_bf16_f32 v230, v224, v225\n\t"
"v_cvt_pk_bf16_f32 v231, v226, v227\n\t"
"v_mfma_f32_16x16x32_bf16 v[236:239], v[236:239], v[196:199], 0\n\t"
"buffer_load_dwordx4 a[68:71], v252, s[8:11], s20 offen offset:2048 sc1\n\t"
"s_mov_b32 s20, 503808\n\t"
"v_mfma_f32_16x16x32_bf16 v[232:235], v[232:235], v[196:199], 0\n\t"
"ds_write_b128 v242, v[228:231] offset:6144\n\t"
"v_mfma_f32_16x16x32_bf16 v[180:183], a[120:123], a[168:171], 0\n\t"
"buffer_load_dwordx4 a[76:79], v252, s[8:11], s20 offen offset:0 sc1\n\t"
"v_mfma_f32_16x16x32_bf16 v[180:183], a[128:131], a[176:179], v[180:183]\n\t"
"buffer_load_dwordx4 a[84:87], v252, s[8:11], s20 offen offset:1024 sc1\n\t"
"v_mfma_f32_16x16x32_bf16 v[180:183], a[136:139], a[180:183], v[180:183]\n\t"
"s_waitcnt lgkmcnt(3)\n\t"
"v_mfma_f32_16x16x32_bf16 v[22:25], a[0:3], a[200:203], 0\n\t"
"v_cvt_pk_bf16_f32 v236, v236, v237\n\t"
"v_cvt_pk_bf16_f32 v237, v238, v239\n\t"
"v_mfma_f32_16x16x32_bf16 v[22:25], a[8:11], a[204:207], v[22:25]\n\t"
"v_cvt_pk_bf16_f32 v238, v232, v233\n\t"
"v_cvt_pk_bf16_f32 v239, v234, v235\n\t"
"v_mfma_f32_16x16x32_bf16 v[22:25], a[16:19], a[188:191], v[22:25]\n\t"
"ds_write_b128 v242, v[236:239] offset:7680\n\t"
"v_mfma_f32_16x16x32_bf16 v[54:57], a[24:27], a[200:203], 0\n\t"
"buffer_load_dwordx4 a[92:95], v252, s[8:11], s20 offen offset:2048 sc1\n\t"
"v_mfma_f32_16x16x32_bf16 v[54:57], a[32:35], a[204:207], v[54:57]\n\t"
"s_mov_b32 s20, 110592\n\t"
"ds_read_b128 v[196:199], v244 offset:37888\n\t"
"v_mfma_f32_16x16x32_bf16 v[54:57], a[40:43], a[188:191], v[54:57]\n\t"
"buffer_load_dwordx4 a[100:103], v252, s[8:11], s20 offen offset:0 sc1\n\t"
"v_mfma_f32_16x16x32_bf16 v[86:89], a[48:51], a[200:203], 0\n\t"
"s_waitcnt lgkmcnt(4)\n\t"
"v_mfma_f32_16x16x32_bf16 v[86:89], a[56:59], a[204:207], v[86:89]\n\t"
"buffer_load_dwordx4 a[108:111], v252, s[8:11], s20 offen offset:1024 sc1\n\t"
"v_mfma_f32_16x16x32_bf16 v[86:89], a[64:67], a[188:191], v[86:89]\n\t"
"v_mfma_f32_16x16x32_bf16 v[120:123], a[72:75], a[176:179], 0\n\t"
"v_mfma_f32_16x16x32_bf16 v[120:123], a[80:83], a[180:183], v[120:123]\n\t"
"buffer_load_dwordx4 a[116:119], v252, s[8:11], s20 offen offset:2048 sc1\n\t"
"v_mfma_f32_16x16x32_bf16 v[120:123], a[88:91], v[204:207], v[120:123]\n\t"
"s_mov_b32 s20, 122880\n\t"
"v_mfma_f32_16x16x32_bf16 v[152:155], a[96:99], a[176:179], 0\n\t"
"ds_read_b128 a[196:199], v244 offset:29184\n\t"
"v_mfma_f32_16x16x32_bf16 v[152:155], a[104:107], a[180:183], v[152:155]\n\t"
"v_mfma_f32_16x16x32_bf16 v[152:155], a[112:115], v[204:207], v[152:155]\n\t"
"s_waitcnt lgkmcnt(2)\n\t"
"s_barrier\n\t"
"v_mfma_f32_16x16x32_bf16 v[184:187], a[120:123], a[176:179], 0\n\t"
"v_mfma_f32_16x16x32_bf16 v[184:187], a[128:131], a[180:183], v[184:187]\n\t"
"buffer_load_dwordx4 a[124:127], v252, s[8:11], s20 offen offset:0 sc1\n\t"
"v_mfma_f32_16x16x32_bf16 v[184:187], a[136:139], v[204:207], v[184:187]\n\t"
"buffer_load_dwordx4 a[132:135], v252, s[8:11], s20 offen offset:1024 sc1\n\t"
"v_mfma_f32_16x16x32_bf16 v[26:29], a[0:3], a[204:207], 0\n\t"
"v_mfma_f32_16x16x32_bf16 v[26:29], a[8:11], a[188:191], v[26:29]\n\t"
"buffer_load_dwordx4 a[140:143], v252, s[8:11], s20 offen offset:2048 sc1\n\t"
"v_mfma_f32_16x16x32_bf16 v[26:29], a[16:19], a[192:195], v[26:29]\n\t"
"s_mov_b32 s20, 135168\n\t"
"v_mfma_f32_16x16x32_bf16 v[58:61], a[24:27], a[204:207], 0\n\t"
"ds_read_b128 v[212:215], v244 offset:38400\n\t"
"v_mfma_f32_16x16x32_bf16 v[58:61], a[32:35], a[188:191], v[58:61]\n\t"
"buffer_load_dwordx4 a[148:151], v252, s[8:11], s20 offen offset:0 sc1\n\t"
"v_mfma_f32_16x16x32_bf16 v[58:61], a[40:43], a[192:195], v[58:61]\n\t"
"v_mfma_f32_16x16x32_bf16 v[90:93], a[48:51], a[204:207], 0\n\t"
"s_waitcnt lgkmcnt(2)\n\t"
"v_mfma_f32_16x16x32_bf16 v[90:93], a[56:59], a[188:191], v[90:93]\n\t"
"buffer_load_dwordx4 a[156:159], v252, s[8:11], s20 offen offset:1024 sc1\n\t"
"v_mfma_f32_16x16x32_bf16 v[90:93], a[64:67], a[192:195], v[90:93]\n\t"
"v_mfma_f32_16x16x32_bf16 v[124:127], a[72:75], a[180:183], 0\n\t"
"v_mfma_f32_16x16x32_bf16 v[124:127], a[80:83], v[204:207], v[124:127]\n\t"
"buffer_load_dwordx4 a[164:167], v252, s[8:11], s20 offen offset:2048 sc1\n\t"
"v_mfma_f32_16x16x32_bf16 v[124:127], a[88:91], v[196:199], v[124:127]\n\t"
"s_mov_b32 s20, 147456\n\t"
"v_mfma_f32_16x16x32_bf16 v[156:159], a[96:99], a[180:183], 0\n\t"
"v_mfma_f32_16x16x32_bf16 v[156:159], a[104:107], v[204:207], v[156:159]\n\t"
"buffer_load_dwordx4 v[240:243], v252, s[8:11], s20 offen offset:0 sc1\n\t"
"v_mfma_f32_16x16x32_bf16 v[156:159], a[112:115], v[196:199], v[156:159]\n\t"
"v_mfma_f32_16x16x32_bf16 v[188:191], a[120:123], a[180:183], 0\n\t"
"s_waitcnt lgkmcnt(1)\n\t"
"v_mfma_f32_16x16x32_bf16 v[188:191], a[128:131], v[204:207], v[188:191]\n\t"
"buffer_load_dwordx4 a[208:211], v252, s[8:11], s20 offen offset:1024 sc1\n\t"
"v_mfma_f32_16x16x32_bf16 v[188:191], a[136:139], v[196:199], v[188:191]\n\t"
"ds_read_b128 a[168:171], v244 offset:0\n\t"
"v_mfma_f32_16x16x32_bf16 v[30:33], a[0:3], a[188:191], 0\n\t"
"ds_read_b128 a[172:175], v244 offset:512\n\t"
"v_mfma_f32_16x16x32_bf16 v[30:33], a[8:11], a[192:195], v[30:33]\n\t"
"ds_read_b128 a[176:179], v244 offset:1024\n\t"
"v_mfma_f32_16x16x32_bf16 v[30:33], a[16:19], a[196:199], v[30:33]\n\t"
"buffer_load_dwordx4 v[248:251], v252, s[8:11], s20 offen offset:2048 sc1\n\t"
"v_mfma_f32_16x16x32_bf16 v[62:65], a[24:27], a[188:191], 0\n\t"
"s_mov_b32 s20, 159744\n\t"
"ds_read_b128 a[180:183], v244 offset:1536\n\t"
"v_mfma_f32_16x16x32_bf16 v[62:65], a[32:35], a[192:195], v[62:65]\n\t"
"buffer_load_dwordx4 a[212:215], v252, s[8:11], s20 offen offset:0 sc1\n\t"
"v_mfma_f32_16x16x32_bf16 v[62:65], a[40:43], a[196:199], v[62:65]\n\t"
"ds_read_b128 a[184:187], v244 offset:2048\n\t"
"v_mfma_f32_16x16x32_bf16 v[94:97], a[48:51], a[188:191], 0\n\t"
"global_load_dwordx4 v[220:223], v252, s[48:49] offset:1024\n\t"
"v_mfma_f32_16x16x32_bf16 v[94:97], a[56:59], a[192:195], v[94:97]\n\t"
"buffer_load_dwordx4 a[216:219], v252, s[8:11], s20 offen offset:1024 sc1\n\t"
"v_mfma_f32_16x16x32_bf16 v[94:97], a[64:67], a[196:199], v[94:97]\n\t"
"s_waitcnt lgkmcnt(5)\n\t"
"ds_read_b128 a[188:191], v244 offset:2560\n\t"
"v_mfma_f32_16x16x32_bf16 v[128:131], a[72:75], v[204:207], 0\n\t"
"v_mfma_f32_16x16x32_bf16 v[128:131], a[80:83], v[196:199], v[128:131]\n\t"
"buffer_load_dwordx4 a[220:223], v252, s[8:11], s20 offen offset:2048 sc1\n\t"
"v_mfma_f32_16x16x32_bf16 v[128:131], a[88:91], v[212:215], v[128:131]\n\t"
"s_mov_b32 s20, 172032\n\t"
"ds_read_b128 a[192:195], v244 offset:3072\n\t"
"v_mfma_f32_16x16x32_bf16 v[160:163], a[96:99], v[204:207], 0\n\t"
"v_mfma_f32_16x16x32_bf16 v[160:163], a[104:107], v[196:199], v[160:163]\n\t"
"buffer_load_dwordx4 a[224:227], v252, s[8:11], s20 offen offset:0 sc1\n\t"
"v_mfma_f32_16x16x32_bf16 v[160:163], a[112:115], v[212:215], v[160:163]\n\t"
"ds_read_b128 a[196:199], v244 offset:3584\n\t"
"v_mfma_f32_16x16x32_bf16 v[192:195], a[120:123], v[204:207], 0\n\t"
"s_waitcnt vmcnt(32) lgkmcnt(5)\n\t"
"v_mfma_f32_16x16x32_bf16 v[192:195], a[128:131], v[196:199], v[192:195]\n\t"
"buffer_load_dwordx4 a[228:231], v252, s[8:11], s20 offen offset:1024 sc1\n\t"
"v_mfma_f32_16x16x32_bf16 v[192:195], a[136:139], v[212:215], v[192:195]\n\t"
"ds_read_b128 a[200:203], v244 offset:4096\n\t"
"Lst1_%=:\n\t"
"v_mfma_f32_16x16x32_bf16 v[2:5], a[144:147], a[168:171], v[2:5]\n\t"
"s_waitcnt lgkmcnt(5)\n\t"
"v_mfma_f32_16x16x32_bf16 v[2:5], a[152:155], a[172:175], v[2:5]\n\t"
"ds_read_b128 a[204:207], v244 offset:4608\n\t"
"v_mfma_f32_16x16x32_bf16 v[2:5], a[160:163], a[176:179], v[2:5]\n\t"
"buffer_load_dwordx4 a[232:235], v252, s[8:11], s20 offen offset:2048 sc1\n\t"
"v_mfma_f32_16x16x32_bf16 v[6:9], a[144:147], a[172:175], v[6:9]\n\t"
"s_waitcnt lgkmcnt(5)\n\t"
"s_mov_b32 s20, 36864\n\t"
"v_mfma_f32_16x16x32_bf16 v[6:9], a[152:155], a[176:179], v[6:9]\n\t"
"v_mfma_f32_16x16x32_bf16 v[6:9], a[160:163], a[180:183], v[6:9]\n\t"
"v_mfma_f32_16x16x32_bf16 v[10:13], a[144:147], a[176:179], v[10:13]\n\t"
"buffer_load_dwordx4 a[236:239], v252, s[8:11], s20 offen offset:0 sc1\n\t"
"v_mfma_f32_16x16x32_bf16 v[10:13], a[152:155], a[180:183], v[10:13]\n\t"
"s_waitcnt lgkmcnt(4)\n\t"
"v_mfma_f32_16x16x32_bf16 v[10:13], a[160:163], a[184:187], v[10:13]\n\t"
"v_mfma_f32_16x16x32_bf16 v[14:17], a[144:147], a[180:183], v[14:17]\n\t"
"s_waitcnt lgkmcnt(3)\n\t"
"v_mfma_f32_16x16x32_bf16 v[14:17], a[152:155], a[184:187], v[14:17]\n\t"
"buffer_load_dwordx4 a[240:243], v252, s[8:11], s20 offen offset:1024 sc1\n\t"
"v_mfma_f32_16x16x32_bf16 v[14:17], a[160:163], a[188:191], v[14:17]\n\t"
"v_mfma_f32_16x16x32_bf16 v[18:21], a[144:147], a[184:187], v[18:21]\n\t"
"s_waitcnt lgkmcnt(2)\n\t"
"v_xor_b32 v246, 0xc0, v246\n\t"
"v_mfma_f32_16x16x32_bf16 v[18:21], a[152:155], a[188:191], v[18:21]\n\t"
"v_mfma_f32_16x16x32_bf16 v[18:21], a[160:163], a[192:195], v[18:21]\n\t"
"buffer_load_dwordx4 a[244:247], v252, s[8:11], s20 offen offset:2048 sc1\n\t"
"v_mfma_f32_16x16x32_bf16 v[22:25], a[144:147], a[188:191], v[22:25]\n\t"
"s_waitcnt lgkmcnt(1)\n\t"
"s_mov_b32 s20, 49152\n\t"
"v_mfma_f32_16x16x32_bf16 v[22:25], a[152:155], a[192:195], v[22:25]\n\t"
"v_mfma_f32_16x16x32_bf16 v[22:25], a[160:163], a[196:199], v[22:25]\n\t"
"v_mfma_f32_16x16x32_bf16 v[26:29], a[144:147], a[192:195], v[26:29]\n\t"
"buffer_load_dwordx4 a[248:251], v252, s[8:11], s20 offen offset:0 sc1\n\t"
"v_mfma_f32_16x16x32_bf16 v[26:29], a[152:155], a[196:199], v[26:29]\n\t"
"s_waitcnt lgkmcnt(0)\n\t"
"v_mfma_f32_16x16x32_bf16 v[26:29], a[160:163], a[200:203], v[26:29]\n\t"
"v_mfma_f32_16x16x32_bf16 v[30:33], a[144:147], a[196:199], v[30:33]\n\t"
"s_waitcnt vmcnt(35)\n\t"
"v_mfma_f32_16x16x32_bf16 v[30:33], a[152:155], a[200:203], v[30:33]\n\t"
"buffer_load_dwordx4 a[252:255], v252, s[8:11], s20 offen offset:1024 sc1\n\t"
"v_mfma_f32_16x16x32_bf16 v[30:33], a[160:163], a[204:207], v[30:33]\n\t"
"v_mfma_f32_16x16x32_bf16 v[34:37], v[200:203], a[168:171], v[34:37]\n\t"
"v_mfma_f32_16x16x32_bf16 v[34:37], v[208:211], a[172:175], v[34:37]\n\t"
"v_mfma_f32_16x16x32_bf16 v[34:37], v[216:219], a[176:179], v[34:37]\n\t"
"ds_read_b64_tr_b16 v[224:225], v246 offset:0\n\t"
"v_mfma_f32_16x16x32_bf16 v[38:41], v[200:203], a[172:175], v[38:41]\n\t"
"ds_read_b64_tr_b16 v[226:227], v246 offset:3072\n\t"
"v_mfma_f32_16x16x32_bf16 v[38:41], v[208:211], a[176:179], v[38:41]\n\t"
"v_mfma_f32_16x16x32_bf16 v[38:41], v[216:219], a[180:183], v[38:41]\n\t"
"v_mfma_f32_16x16x32_bf16 v[42:45], v[200:203], a[176:179], v[42:45]\n\t"
"v_mfma_f32_16x16x32_bf16 v[42:45], v[208:211], a[180:183], v[42:45]\n\t"
"v_mfma_f32_16x16x32_bf16 v[42:45], v[216:219], a[184:187], v[42:45]\n\t"
"v_mfma_f32_16x16x32_bf16 v[46:49], v[200:203], a[180:183], v[46:49]\n\t"
"ds_read_b64_tr_b16 v[228:229], v246 offset:6144\n\t"
"v_mfma_f32_16x16x32_bf16 v[46:49], v[208:211], a[184:187], v[46:49]\n\t"
"ds_read_b64_tr_b16 v[230:231], v246 offset:9216\n\t"
"v_mfma_f32_16x16x32_bf16 v[46:49], v[216:219], a[188:191], v[46:49]\n\t"
"v_mfma_f32_16x16x32_bf16 v[50:53], v[200:203], a[184:187], v[50:53]\n\t"
"v_mfma_f32_16x16x32_bf16 v[50:53], v[208:211], a[188:191], v[50:53]\n\t"
"v_mfma_f32_16x16x32_bf16 v[50:53], v[216:219], a[192:195], v[50:53]\n\t"
"v_mfma_f32_16x16x32_bf16 v[54:57], v[200:203], a[188:191], v[54:57]\n\t"
"s_waitcnt vmcnt(10) lgkmcnt(2)\n\t"
"v_mfma_f32_16x16x32_bf16 v[54:57], v[208:211], a[192:195], v[54:57]\n\t"
"v_mfma_f32_16x16x32_bf16 v[54:57], v[216:219], a[196:199], v[54:57]\n\t"
"v_mfma_f32_16x16x32_bf16 v[224:227], v[224:227], v[220:223], 0\n\t"
"v_mfma_f32_16x16x32_bf16 v[58:61], v[200:203], a[192:195], v[58:61]\n\t"
"ds_read_b64_tr_b16 v[232:233], v246 offset:12288\n\t"
"v_mfma_f32_16x16x32_bf16 v[58:61], v[208:211], a[196:199], v[58:61]\n\t"
"ds_read_b64_tr_b16 v[234:235], v246 offset:15360\n\t"
"v_mfma_f32_16x16x32_bf16 v[58:61], v[216:219], a[200:203], v[58:61]\n\t"
"v_mfma_f32_16x16x32_bf16 v[62:65], v[200:203], a[196:199], v[62:65]\n\t"
"s_waitcnt lgkmcnt(2)\n\t"
"v_mfma_f32_16x16x32_bf16 v[62:65], v[208:211], a[200:203], v[62:65]\n\t"
"v_cvt_pk_bf16_f32 v224, v224, v225\n\t"
"v_cvt_pk_bf16_f32 v225, v226, v227\n\t"
"v_mfma_f32_16x16x32_bf16 v[62:65], v[216:219], a[204:207], v[62:65]\n\t"
"v_mfma_f32_16x16x32_bf16 v[228:231], v[228:231], v[220:223], 0\n\t"
"v_mfma_f32_16x16x32_bf16 v[66:69], a[4:7], a[168:171], v[66:69]\n\t"
"ds_read_b64_tr_b16 v[236:237], v246 offset:19968\n\t"
"v_mfma_f32_16x16x32_bf16 v[66:69], a[12:15], a[172:175], v[66:69]\n\t"
"ds_read_b64_tr_b16 v[238:239], v246 offset:23040\n\t"
"v_mfma_f32_16x16x32_bf16 v[66:69], a[20:23], a[176:179], v[66:69]\n\t"
"ds_read_b128 a[168:171], v244 offset:9216\n\t"
"v_mfma_f32_16x16x32_bf16 v[70:73], a[4:7], a[172:175], v[70:73]\n\t"
"ds_read_b128 a[172:175], v244 offset:9728\n\t"
"v_mfma_f32_16x16x32_bf16 v[70:73], a[12:15], a[176:179], v[70:73]\n\t"
"v_cvt_pk_bf16_f32 v228, v228, v229\n\t"
"v_cvt_pk_bf16_f32 v229, v230, v231\n\t"
"v_mfma_f32_16x16x32_bf16 v[70:73], a[20:23], a[180:183], v[70:73]\n\t"
"v_mfma_f32_16x16x32_bf16 v[74:77], a[4:7], a[176:179], v[74:77]\n\t"
"s_waitcnt lgkmcnt(4)\n\t"
"ds_read_b128 a[176:179], v244 offset:10240\n\t"
"v_mfma_f32_16x16x32_bf16 v[74:77], a[12:15], a[180:183], v[74:77]\n\t"
"v_mfma_f32_16x16x32_bf16 v[74:77], a[20:23], a[184:187], v[74:77]\n\t"
"v_mfma_f32_16x16x32_bf16 v[232:235], v[232:235], v[220:223], 0\n\t"
"v_mfma_f32_16x16x32_bf16 v[78:81], a[4:7], a[180:183], v[78:81]\n\t"
"ds_read_b128 a[180:183], v244 offset:10752\n\t"
"v_mfma_f32_16x16x32_bf16 v[78:81], a[12:15], a[184:187], v[78:81]\n\t"
"v_mfma_f32_16x16x32_bf16 v[78:81], a[20:23], a[188:191], v[78:81]\n\t"
"v_mfma_f32_16x16x32_bf16 v[82:85], a[4:7], a[184:187], v[82:85]\n\t"
"s_waitcnt lgkmcnt(4)\n\t"
"ds_read_b128 a[184:187], v244 offset:11264\n\t"
"v_mfma_f32_16x16x32_bf16 v[82:85], a[12:15], a[188:191], v[82:85]\n\t"
"v_cvt_pk_bf16_f32 v232, v232, v233\n\t"
"v_cvt_pk_bf16_f32 v233, v234, v235\n\t"
"v_mfma_f32_16x16x32_bf16 v[82:85], a[20:23], a[192:195], v[82:85]\n\t"
"v_mfma_f32_16x16x32_bf16 v[236:239], v[236:239], v[220:223], 0\n\t"
"v_mfma_f32_16x16x32_bf16 v[86:89], a[4:7], a[188:191], v[86:89]\n\t"
"ds_read_b128 a[188:191], v244 offset:11776\n\t"
"v_mfma_f32_16x16x32_bf16 v[86:89], a[12:15], a[192:195], v[86:89]\n\t"
"v_mfma_f32_16x16x32_bf16 v[86:89], a[20:23], a[196:199], v[86:89]\n\t"
"v_mfma_f32_16x16x32_bf16 v[90:93], a[4:7], a[192:195], v[90:93]\n\t"
"ds_read_b128 a[192:195], v244 offset:12288\n\t"
"v_mfma_f32_16x16x32_bf16 v[90:93], a[12:15], a[196:199], v[90:93]\n\t"
"v_cvt_pk_bf16_f32 v236, v236, v237\n\t"
"v_cvt_pk_bf16_f32 v237, v238, v239\n\t"
"v_mfma_f32_16x16x32_bf16 v[90:93], a[20:23], a[200:203], v[90:93]\n\t"
"v_mfma_f32_16x16x32_bf16 v[94:97], a[4:7], a[196:199], v[94:97]\n\t"
"s_waitcnt lgkmcnt(4)\n\t"
"ds_read_b128 a[196:199], v244 offset:12800\n\t"
"v_mfma_f32_16x16x32_bf16 v[94:97], a[12:15], a[200:203], v[94:97]\n\t"
"ds_read_b128 a[200:203], v244 offset:13312\n\t"
"v_mfma_f32_16x16x32_bf16 v[94:97], a[20:23], a[204:207], v[94:97]\n\t"
"ds_read_b128 a[204:207], v244 offset:13824\n\t"
"v_mfma_f32_16x16x32_bf16 v[100:103], a[28:31], a[168:171], v[100:103]\n\t"
"s_waitcnt lgkmcnt(6)\n\t"
"v_mfma_f32_16x16x32_bf16 v[100:103], a[36:39], a[172:175], v[100:103]\n\t"
"v_mfma_f32_16x16x32_bf16 v[100:103], a[44:47], a[176:179], v[100:103]\n\t"
"v_mfma_f32_16x16x32_bf16 v[104:107], a[28:31], a[172:175], v[104:107]\n\t"
"s_waitcnt lgkmcnt(5)\n\t"
"v_mfma_f32_16x16x32_bf16 v[104:107], a[36:39], a[176:179], v[104:107]\n\t"
"v_mfma_f32_16x16x32_bf16 v[104:107], a[44:47], a[180:183], v[104:107]\n\t"
"s_waitcnt lgkmcnt(0)\n\t"
"s_barrier\n\t"
"v_mfma_f32_16x16x32_bf16 v[108:111], a[28:31], a[176:179], v[108:111]\n\t"
"v_fma_f32 v196, -v2, s56, -v100\n\t"
"v_mfma_f32_16x16x32_bf16 v[108:111], a[36:39], a[180:183], v[108:111]\n\t"
"v_fma_f32 v197, -v3, s56, -v101\n\t"
"v_fma_f32 v198, -v4, s56, -v102\n\t"
"v_mfma_f32_16x16x32_bf16 v[108:111], a[44:47], a[184:187], v[108:111]\n\t"
"ds_write_b64 v245, v[224:225] offset:0\n\t"
"v_mfma_f32_16x16x32_bf16 v[112:115], a[28:31], a[180:183], v[112:115]\n\t"
"ds_read_b64_tr_b16 v[224:225], v246 offset:26112\n\t"
"v_fma_f32 v199, -v5, s56, -v103\n\t"
"v_mfma_f32_16x16x32_bf16 v[112:115], a[36:39], a[184:187], v[112:115]\n\t"
"ds_read_b64_tr_b16 v[226:227], v246 offset:29184\n\t"
"v_fma_f32 v204, -v6, s56, -v104\n\t"
"v_mfma_f32_16x16x32_bf16 v[112:115], a[44:47], a[188:191], v[112:115]\n\t"
"ds_write_b64 v245, v[228:229] offset:512\n\t"
"v_mfma_f32_16x16x32_bf16 v[116:119], a[28:31], a[184:187], v[116:119]\n\t"
"ds_read_b64_tr_b16 v[228:229], v246 offset:32256\n\t"
"v_fma_f32 v205, -v7, s56, -v105\n\t"
"v_mfma_f32_16x16x32_bf16 v[116:119], a[36:39], a[188:191], v[116:119]\n\t"
"ds_read_b64_tr_b16 v[230:231], v246 offset:35328\n\t"
"v_fma_f32 v206, -v8, s56, -v106\n\t"
"v_mfma_f32_16x16x32_bf16 v[116:119], a[44:47], a[192:195], v[116:119]\n\t"
"ds_write_b64 v245, v[232:233] offset:1024\n\t"
"v_mfma_f32_16x16x32_bf16 v[120:123], a[28:31], a[188:191], v[120:123]\n\t"
"s_waitcnt lgkmcnt(4)\n\t"
"ds_read_b64_tr_b16 v[232:233], v246 offset:39936\n\t"
"v_fma_f32 v207, -v9, s56, -v107\n\t"
"v_mfma_f32_16x16x32_bf16 v[120:123], a[36:39], a[192:195], v[120:123]\n\t"
"ds_read_b64_tr_b16 v[234:235], v246 offset:43008\n\t"
"v_fma_f32 v212, -v10, s56, -v108\n\t"
"v_mfma_f32_16x16x32_bf16 v[120:123], a[44:47], a[196:199], v[120:123]\n\t"
"ds_write_b64 v245, v[236:237] offset:1536\n\t"
"v_mfma_f32_16x16x32_bf16 v[224:227], v[224:227], v[220:223], 0\n\t"
"ds_read_b64_tr_b16 v[236:237], v246 offset:46080\n\t"
"v_fma_f32 v213, -v11, s56, -v109\n\t"
"v_mfma_f32_16x16x32_bf16 v[124:127], a[28:31], a[192:195], v[124:127]\n\t"
"s_waitcnt lgkmcnt(5)\n\t"
"ds_read_b64_tr_b16 v[238:239], v246 offset:49152\n\t"
"v_fma_f32 v214, -v12, s56, -v110\n\t"
"v_mfma_f32_16x16x32_bf16 v[124:127], a[36:39], a[196:199], v[124:127]\n\t"
"v_fma_f32 v215, -v13, s56, -v111\n\t"
"v_fma_f32 v98, -v14, s56, -v112\n\t"
"v_mfma_f32_16x16x32_bf16 v[124:127], a[44:47], a[200:203], v[124:127]\n\t"
"v_fma_f32 v99, -v15, s56, -v113\n\t"
"v_accvgpr_write_b32 a0, v98\n\t"
"v_mfma_f32_16x16x32_bf16 v[228:231], v[228:231], v[220:223], 0\n\t"
"v_fma_f32 v98, -v16, s56, -v114\n\t"
"v_accvgpr_write_b32 a1, v99\n\t"
"v_mfma_f32_16x16x32_bf16 v[128:131], a[28:31], a[196:199], v[128:131]\n\t"
"s_waitcnt lgkmcnt(3)\n\t"
"v_cvt_pk_bf16_f32 v224, v224, v225\n\t"
"v_cvt_pk_bf16_f32 v225, v226, v227\n\t"
"v_mfma_f32_16x16x32_bf16 v[128:131], a[36:39], a[200:203], v[128:131]\n\t"
"v_fma_f32 v99, -v17, s56, -v115\n\t"
"v_accvgpr_write_b32 a2, v98\n\t"
"v_mfma_f32_16x16x32_bf16 v[128:131], a[44:47], a[204:207], v[128:131]\n\t"
"ds_write_b64 v245, v[224:225] offset:2048\n\t"
"v_mfma_f32_16x16x32_bf16 v[232:235], v[232:235], v[220:223], 0\n\t"
"ds_read_b64_tr_b16 v[224:225], v246 offset:52224\n\t"
"v_accvgpr_write_b32 a3, v99\n\t"
"v_mfma_f32_16x16x32_bf16 v[132:135], a[52:55], a[168:171], v[132:135]\n\t"
"s_waitcnt lgkmcnt(2)\n\t"
"ds_read_b64_tr_b16 v[226:227], v246 offset:55296\n\t"
"v_cvt_pk_bf16_f32 v228, v228, v229\n\t"
"v_mfma_f32_16x16x32_bf16 v[132:135], a[60:63], a[172:175], v[132:135]\n\t"
"v_cvt_pk_bf16_f32 v229, v230, v231\n\t"
"v_fma_f32 v98, -v18, s56, -v116\n\t"
"v_mfma_f32_16x16x32_bf16 v[132:135], a[68:71], a[176:179], v[132:135]\n\t"
"ds_write_b64 v245, v[228:229] offset:2560\n\t"
"v_mfma_f32_16x16x32_bf16 v[236:239], v[236:239], v[220:223], 0\n\t"
"v_fma_f32 v99, -v19, s56, -v117\n\t"
"v_accvgpr_write_b32 a8, v98\n\t"
"v_mfma_f32_16x16x32_bf16 v[136:139], a[52:55], a[172:175], v[136:139]\n\t"
"v_cvt_pk_bf16_f32 v232, v232, v233\n\t"
"v_cvt_pk_bf16_f32 v233, v234, v235\n\t"
"v_mfma_f32_16x16x32_bf16 v[136:139], a[60:63], a[176:179], v[136:139]\n\t"
"v_fma_f32 v98, -v20, s56, -v118\n\t"
"v_accvgpr_write_b32 a9, v99\n\t"
"v_mfma_f32_16x16x32_bf16 v[136:139], a[68:71], a[180:183], v[136:139]\n\t"
"ds_write_b64 v245, v[232:233] offset:3072\n\t"
"v_mfma_f32_16x16x32_bf16 v[140:143], a[52:55], a[176:179], v[140:143]\n\t"
"v_fma_f32 v99, -v21, s56, -v119\n\t"
"v_accvgpr_write_b32 a10, v98\n\t"
"v_mfma_f32_16x16x32_bf16 v[140:143], a[60:63], a[180:183], v[140:143]\n\t"
"v_cvt_pk_bf16_f32 v236, v236, v237\n\t"
"v_cvt_pk_bf16_f32 v237, v238, v239\n\t"
"v_mfma_f32_16x16x32_bf16 v[140:143], a[68:71], a[184:187], v[140:143]\n\t"
"ds_write_b64 v245, v[236:237] offset:3584\n\t"
"v_mfma_f32_16x16x32_bf16 v[144:147], a[52:55], a[180:183], v[144:147]\n\t"
"s_waitcnt lgkmcnt(3)\n\t"
"v_accvgpr_write_b32 a11, v99\n\t"
"v_fma_f32 v98, -v22, s56, -v120\n\t"
"v_mfma_f32_16x16x32_bf16 v[144:147], a[60:63], a[184:187], v[144:147]\n\t"
"v_fma_f32 v99, -v23, s56, -v121\n\t"
"v_accvgpr_write_b32 a16, v98\n\t"
"v_mfma_f32_16x16x32_bf16 v[144:147], a[68:71], a[188:191], v[144:147]\n\t"
"v_fma_f32 v98, -v24, s56, -v122\n\t"
"v_accvgpr_write_b32 a17, v99\n\t"
"v_mfma_f32_16x16x32_bf16 v[224:227], v[224:227], v[220:223], 0\n\t"
"v_fma_f32 v99, -v25, s56, -v123\n\t"
"v_accvgpr_write_b32 a18, v98\n\t"
"v_mfma_f32_16x16x32_bf16 v[148:151], a[52:55], a[184:187], v[148:151]\n\t"
"v_accvgpr_write_b32 a19, v99\n\t"
"v_fma_f32 v98, -v26, s56, -v124\n\t"
"v_mfma_f32_16x16x32_bf16 v[148:151], a[60:63], a[188:191], v[148:151]\n\t"
"v_fma_f32 v99, -v27, s56, -v125\n\t"
"v_accvgpr_write_b32 a24, v98\n\t"
"v_mfma_f32_16x16x32_bf16 v[148:151], a[68:71], a[192:195], v[148:151]\n\t"
"v_fma_f32 v98, -v28, s56, -v126\n\t"
"v_accvgpr_write_b32 a25, v99\n\t"
"v_mfma_f32_16x16x32_bf16 v[152:155], a[52:55], a[188:191], v[152:155]\n\t"
"v_fma_f32 v99, -v29, s56, -v127\n\t"
"v_accvgpr_write_b32 a26, v98\n\t"
"v_mfma_f32_16x16x32_bf16 v[152:155], a[60:63], a[192:195], v[152:155]\n\t"
"v_cvt_pk_bf16_f32 v224, v224, v225\n\t"
"v_cvt_pk_bf16_f32 v225, v226, v227\n\t"
"v_mfma_f32_16x16x32_bf16 v[152:155], a[68:71], a[196:199], v[152:155]\n\t"
"buffer_load_dwordx4 v[236:239], v252, s[8:11], s20 offen offset:2048 sc1\n\t"
"v_mfma_f32_16x16x32_bf16 v[156:159], a[52:55], a[192:195], v[156:159]\n\t"
"v_accvgpr_write_b32 a27, v99\n\t"
"v_fma_f32 v98, -v30, s56, -v128\n\t"
"v_mfma_f32_16x16x32_bf16 v[156:159], a[60:63], a[196:199], v[156:159]\n\t"
"v_fma_f32 v99, -v31, s56, -v129\n\t"
"v_accvgpr_write_b32 a32, v98\n\t"
"v_mfma_f32_16x16x32_bf16 v[156:159], a[68:71], a[200:203], v[156:159]\n\t"
"ds_write_b64 v245, v[224:225] offset:4096\n\t"
"v_mfma_f32_16x16x32_bf16 v[160:163], a[52:55], a[196:199], v[160:163]\n\t"
"v_fma_f32 v98, -v32, s56, -v130\n\t"
"v_accvgpr_write_b32 a33, v99\n\t"
"v_mfma_f32_16x16x32_bf16 v[160:163], a[60:63], a[200:203], v[160:163]\n\t"
"v_fma_f32 v99, -v33, s56, -v131\n\t"
"v_accvgpr_write_b32 a34, v98\n\t"
"v_mfma_f32_16x16x32_bf16 v[160:163], a[68:71], a[204:207], v[160:163]\n\t"
"v_accvgpr_write_b32 a35, v99\n\t"
"v_fma_f32 v98, -v34, s56, -v132\n\t"
"v_mfma_f32_16x16x32_bf16 v[164:167], a[76:79], a[168:171], v[164:167]\n\t"
"v_fma_f32 v99, -v35, s56, -v133\n\t"
"v_accvgpr_write_b32 a40, v98\n\t"
"v_mfma_f32_16x16x32_bf16 v[164:167], a[84:87], a[172:175], v[164:167]\n\t"
"v_fma_f32 v98, -v36, s56, -v134\n\t"
"v_mfma_f32_16x16x32_bf16 v[164:167], a[92:95], a[176:179], v[164:167]\n\t"
"s_waitcnt lgkmcnt(0)\n\t"
"s_barrier\n\t"
"v_mfma_f32_16x16x32_bf16 v[168:171], a[76:79], a[172:175], v[168:171]\n\t"
"ds_read_b128 a[168:171], v244 offset:0\n\t"
"v_mfma_f32_16x16x32_bf16 v[168:171], a[84:87], a[176:179], v[168:171]\n\t"
"ds_read_b128 a[172:175], v244 offset:512\n\t"
"v_accvgpr_write_b32 a41, v99\n\t"
"v_mfma_f32_16x16x32_bf16 v[168:171], a[92:95], a[180:183], v[168:171]\n\t"
"v_fma_f32 v99, -v37, s56, -v135\n\t"
"v_accvgpr_write_b32 a42, v98\n\t"
"v_mfma_f32_16x16x32_bf16 v[172:175], a[76:79], a[176:179], v[172:175]\n\t"
"ds_read_b128 a[176:179], v244 offset:1024\n\t"
"v_accvgpr_write_b32 a43, v99\n\t"
"v_mfma_f32_16x16x32_bf16 v[172:175], a[84:87], a[180:183], v[172:175]\n\t"
"v_fma_f32 v98, -v38, s56, -v136\n\t"
"v_fma_f32 v99, -v39, s56, -v137\n\t"
"v_mfma_f32_16x16x32_bf16 v[172:175], a[92:95], a[184:187], v[172:175]\n\t"
"v_accvgpr_write_b32 a48, v98\n\t"
"v_fma_f32 v98, -v40, s56, -v138\n\t"
"v_mfma_f32_16x16x32_bf16 v[176:179], a[76:79], a[180:183], v[176:179]\n\t"
"ds_read_b128 a[180:183], v244 offset:1536\n\t"
"v_accvgpr_write_b32 a49, v99\n\t"
"v_mfma_f32_16x16x32_bf16 v[176:179], a[84:87], a[184:187], v[176:179]\n\t"
"v_fma_f32 v99, -v41, s56, -v139\n\t"
"v_accvgpr_write_b32 a50, v98\n\t"
"v_mfma_f32_16x16x32_bf16 v[176:179], a[92:95], a[188:191], v[176:179]\n\t"
"v_accvgpr_write_b32 a51, v99\n\t"
"v_fma_f32 v98, -v42, s56, -v140\n\t"
"v_mfma_f32_16x16x32_bf16 v[180:183], a[76:79], a[184:187], v[180:183]\n\t"
"ds_read_b128 a[184:187], v244 offset:2048\n\t"
"v_fma_f32 v99, -v43, s56, -v141\n\t"
"v_mfma_f32_16x16x32_bf16 v[180:183], a[84:87], a[188:191], v[180:183]\n\t"
"v_accvgpr_write_b32 a56, v98\n\t"
"v_fma_f32 v98, -v44, s56, -v142\n\t"
"v_mfma_f32_16x16x32_bf16 v[180:183], a[92:95], a[192:195], v[180:183]\n\t"
"v_accvgpr_write_b32 a57, v99\n\t"
"v_fma_f32 v99, -v45, s56, -v143\n\t"
"v_mfma_f32_16x16x32_bf16 v[184:187], a[76:79], a[188:191], v[184:187]\n\t"
"ds_read_b128 a[188:191], v244 offset:2560\n\t"
"v_accvgpr_write_b32 a58, v98\n\t"
"v_mfma_f32_16x16x32_bf16 v[184:187], a[84:87], a[192:195], v[184:187]\n\t"
"v_accvgpr_write_b32 a59, v99\n\t"
"v_fma_f32 v98, -v46, s56, -v144\n\t"
"v_mfma_f32_16x16x32_bf16 v[184:187], a[92:95], a[196:199], v[184:187]\n\t"
"v_fma_f32 v99, -v47, s56, -v145\n\t"
"v_accvgpr_write_b32 a64, v98\n\t"
"v_mfma_f32_16x16x32_bf16 v[188:191], a[76:79], a[192:195], v[188:191]\n\t"
"ds_read_b128 a[192:195], v244 offset:3072\n\t"
"v_fma_f32 v98, -v48, s56, -v146\n\t"
"v_mfma_f32_16x16x32_bf16 v[188:191], a[84:87], a[196:199], v[188:191]\n\t"
"v_accvgpr_write_b32 a65, v99\n\t"
"v_fma_f32 v99, -v49, s56, -v147\n\t"
"v_mfma_f32_16x16x32_bf16 v[188:191], a[92:95], a[200:203], v[188:191]\n\t"
"v_accvgpr_write_b32 a66, v98\n\t"
"v_accvgpr_write_b32 a67, v99\n\t"
"v_mfma_f32_16x16x32_bf16 v[192:195], a[76:79], a[196:199], v[192:195]\n\t"
"s_waitcnt lgkmcnt(4)\n\t"
"ds_read_b128 a[196:199], v244 offset:3584\n\t"
"v_fma_f32 v98, -v50, s56, -v148\n\t"
"v_mfma_f32_16x16x32_bf16 v[192:195], a[84:87], a[200:203], v[192:195]\n\t"
"ds_read_b128 a[200:203], v244 offset:4096\n\t"
"v_fma_f32 v99, -v51, s56, -v149\n\t"
"v_mfma_f32_16x16x32_bf16 v[192:195], a[92:95], a[204:207], v[192:195]\n\t"
"ds_read_b128 a[204:207], v244 offset:4608\n\t"
"v_accvgpr_write_b32 a72, v98\n\t"
"Lst2_%=:\n\t"
"v_mfma_f32_16x16x32_bf16 v[196:199], a[100:103], a[168:171], v[196:199]\n\t"
"s_waitcnt lgkmcnt(6)\n\t"
"v_xor_b32 v246, 0xc0, v246\n\t"
"v_fma_f32 v98, -v52, s56, -v150\n\t"
"v_mfma_f32_16x16x32_bf16 v[196:199], a[108:111], a[172:175], v[196:199]\n\t"
"v_accvgpr_write_b32 a73, v99\n\t"
"v_fma_f32 v99, -v53, s56, -v151\n\t"
"v_mfma_f32_16x16x32_bf16 v[196:199], a[116:119], a[176:179], v[196:199]\n\t"
"v_accvgpr_write_b32 a74, v98\n\t"
"v_accvgpr_write_b32 a75, v99\n\t"
"v_mfma_f32_16x16x32_bf16 v[204:207], a[100:103], a[172:175], v[204:207]\n\t"
"s_waitcnt lgkmcnt(5)\n\t"
"v_fma_f32 v98, -v54, s56, -v152\n\t"
"v_fma_f32 v99, -v55, s56, -v153\n\t"
"v_mfma_f32_16x16x32_bf16 v[204:207], a[108:111], a[176:179], v[204:207]\n\t"
"v_accvgpr_write_b32 a80, v98\n\t"
"v_fma_f32 v98, -v56, s56, -v154\n\t"
"v_mfma_f32_16x16x32_bf16 v[204:207], a[116:119], a[180:183], v[204:207]\n\t"
"s_waitcnt lgkmcnt(2)\n\t"
"v_accvgpr_write_b32 a81, v99\n\t"
"v_fma_f32 v99, -v57, s56, -v155\n\t"
"v_mfma_f32_16x16x32_bf16 v[212:215], a[100:103], a[176:179], v[212:215]\n\t"
"v_accvgpr_write_b32 a82, v98\n\t"
"v_accvgpr_write_b32 a83, v99\n\t"
"v_mfma_f32_16x16x32_bf16 v[212:215], a[108:111], a[180:183], v[212:215]\n\t"
"Lpg2_%=:\n\t"
"s_cbranch_vccnz Lpg6_%=\n\t"
"v_fma_f32 v98, -v58, s56, -v156\n\t"
"v_fma_f32 v99, -v59, s56, -v157\n\t"
"v_mfma_f32_16x16x32_bf16 v[212:215], a[116:119], a[184:187], v[212:215]\n\t"
"v_accvgpr_write_b32 a88, v98\n\t"
"v_fma_f32 v98, -v60, s56, -v158\n\t"
"v_mfma_f32_16x16x32_bf16 a[0:3], a[100:103], a[180:183], a[0:3]\n\t"
"v_accvgpr_write_b32 a89, v99\n\t"
"v_fma_f32 v99, -v61, s56, -v159\n\t"
"v_mfma_f32_16x16x32_bf16 a[0:3], a[108:111], a[184:187], a[0:3]\n\t"
"v_accvgpr_write_b32 a90, v98\n\t"
"v_accvgpr_write_b32 a91, v99\n\t"
"v_mfma_f32_16x16x32_bf16 a[0:3], a[116:119], a[188:191], a[0:3]\n\t"
"v_fma_f32 v98, -v62, s56, -v160\n\t"
"v_fma_f32 v99, -v63, s56, -v161\n\t"
"v_mfma_f32_16x16x32_bf16 a[8:11], a[100:103], a[184:187], a[8:11]\n\t"
"v_accvgpr_write_b32 a96, v98\n\t"
"v_fma_f32 v98, -v64, s56, -v162\n\t"
"v_mfma_f32_16x16x32_bf16 a[8:11], a[108:111], a[188:191], a[8:11]\n\t"
"v_accvgpr_write_b32 a97, v99\n\t"
"v_fma_f32 v99, -v65, s56, -v163\n\t"
"v_mfma_f32_16x16x32_bf16 a[8:11], a[116:119], a[192:195], a[8:11]\n\t"
"ds_read_b64_tr_b16 v[228:229], v246 offset:0\n\t"
"v_accvgpr_write_b32 a98, v98\n\t"
"v_mfma_f32_16x16x32_bf16 a[16:19], a[100:103], a[188:191], a[16:19]\n\t"
"s_waitcnt lgkmcnt(2)\n\t"
"ds_read_b64_tr_b16 v[230:231], v246 offset:3072\n\t"
"v_accvgpr_write_b32 a99, v99\n\t"
"v_mfma_f32_16x16x32_bf16 a[16:19], a[108:111], a[192:195], a[16:19]\n\t"
"v_fma_f32 v98, -v66, s56, -v164\n\t"
"v_fma_f32 v99, -v67, s56, -v165\n\t"
"v_mfma_f32_16x16x32_bf16 a[16:19], a[116:119], a[196:199], a[16:19]\n\t"
"v_accvgpr_write_b32 a104, v98\n\t"
"v_fma_f32 v98, -v68, s56, -v166\n\t"
"v_mfma_f32_16x16x32_bf16 a[24:27], a[100:103], a[192:195], a[24:27]\n\t"
"s_waitcnt lgkmcnt(2)\n\t"
"v_accvgpr_write_b32 a105, v99\n\t"
"v_fma_f32 v99, -v69, s56, -v167\n\t"
"v_mfma_f32_16x16x32_bf16 a[24:27], a[108:111], a[196:199], a[24:27]\n\t"
"v_accvgpr_write_b32 a106, v98\n\t"
"v_accvgpr_write_b32 a107, v99\n\t"
"v_mfma_f32_16x16x32_bf16 a[24:27], a[116:119], a[200:203], a[24:27]\n\t"
"v_fma_f32 v98, -v70, s56, -v168\n\t"
"v_fma_f32 v99, -v71, s56, -v169\n\t"
"v_mfma_f32_16x16x32_bf16 a[32:35], a[100:103], a[196:199], a[32:35]\n\t"
"ds_read_b64_tr_b16 v[232:233], v246 offset:6144\n\t"
"v_accvgpr_write_b32 a112, v98\n\t"
"v_mfma_f32_16x16x32_bf16 a[32:35], a[108:111], a[200:203], a[32:35]\n\t"
"ds_read_b64_tr_b16 v[234:235], v246 offset:9216\n\t"
"v_fma_f32 v98, -v72, s56, -v170\n\t"
"v_mfma_f32_16x16x32_bf16 a[32:35], a[116:119], a[204:207], a[32:35]\n\t"
"v_accvgpr_write_b32 a113, v99\n\t"
"v_fma_f32 v99, -v73, s56, -v171\n\t"
"v_mfma_f32_16x16x32_bf16 a[40:43], a[124:127], a[168:171], a[40:43]\n\t"
"v_accvgpr_write_b32 a114, v98\n\t"
"v_accvgpr_write_b32 a115, v99\n\t"
"v_mfma_f32_16x16x32_bf16 a[40:43], a[132:135], a[172:175], a[40:43]\n\t"
"v_fma_f32 v98, -v74, s56, -v172\n\t"
"v_fma_f32 v99, -v75, s56, -v173\n\t"
"v_mfma_f32_16x16x32_bf16 a[40:43], a[140:143], a[176:179], a[40:43]\n\t"
"v_accvgpr_write_b32 a120, v98\n\t"
"v_fma_f32 v98, -v76, s56, -v174\n\t"
"v_mfma_f32_16x16x32_bf16 a[48:51], a[124:127], a[172:175], a[48:51]\n\t"
"s_waitcnt lgkmcnt(2)\n\t"
"v_accvgpr_write_b32 a121, v99\n\t"
"v_fma_f32 v99, -v77, s56, -v175\n\t"
"v_mfma_f32_16x16x32_bf16 a[48:51], a[132:135], a[176:179], a[48:51]\n\t"
"v_accvgpr_write_b32 a122, v98\n\t"
"v_accvgpr_write_b32 a123, v99\n\t"
"v_mfma_f32_16x16x32_bf16 a[48:51], a[140:143], a[180:183], a[48:51]\n\t"
"v_fma_f32 v98, -v78, s56, -v176\n\t"
"v_fma_f32 v99, -v79, s56, -v177\n\t"
"v_mfma_f32_16x16x32_bf16 v[228:231], v[228:231], v[220:223], 0\n\t"
"v_accvgpr_write_b32 a128, v98\n\t"
"v_fma_f32 v98, -v80, s56, -v178\n\t"
"v_mfma_f32_16x16x32_bf16 a[56:59], a[124:127], a[176:179], a[56:59]\n\t"
"ds_read_b64_tr_b16 v[224:225], v246 offset:12288\n\t"
"v_accvgpr_write_b32 a129, v99\n\t"
"v_mfma_f32_16x16x32_bf16 a[56:59], a[132:135], a[180:183], a[56:59]\n\t"
"ds_read_b64_tr_b16 v[226:227], v246 offset:15360\n\t"
"v_fma_f32 v99, -v81, s56, -v179\n\t"
"v_mfma_f32_16x16x32_bf16 a[56:59], a[140:143], a[184:187], a[56:59]\n\t"
"v_accvgpr_write_b32 a130, v98\n\t"
"v_accvgpr_write_b32 a131, v99\n\t"
"v_mfma_f32_16x16x32_bf16 a[64:67], a[124:127], a[180:183], a[64:67]\n\t"
"s_waitcnt lgkmcnt(2)\n\t"
"v_fma_f32 v98, -v82, s56, -v180\n\t"
"v_fma_f32 v99, -v83, s56, -v181\n\t"
"v_mfma_f32_16x16x32_bf16 a[64:67], a[132:135], a[184:187], a[64:67]\n\t"
"v_cvt_pk_bf16_f32 v228, v228, v229\n\t"
"v_cvt_pk_bf16_f32 v229, v230, v231\n\t"
"v_mfma_f32_16x16x32_bf16 a[64:67], a[140:143], a[188:191], a[64:67]\n\t"
"v_accvgpr_write_b32 a136, v98\n\t"
"v_fma_f32 v98, -v84, s56, -v182\n\t"
"v_mfma_f32_16x16x32_bf16 v[232:235], v[232:235], v[220:223], 0\n\t"
"v_accvgpr_write_b32 a137, v99\n\t"
"v_fma_f32 v99, -v85, s56, -v183\n\t"
"v_mfma_f32_16x16x32_bf16 a[72:75], a[124:127], a[184:187], a[72:75]\n\t"
"v_accvgpr_write_b32 a138, v98\n\t"
"v_accvgpr_write_b32 a139, v99\n\t"
"v_mfma_f32_16x16x32_bf16 a[72:75], a[132:135], a[188:191], a[72:75]\n\t"
"v_fma_f32 v98, -v86, s56, -v184\n\t"
"v_fma_f32 v99, -v87, s56, -v185\n\t"
"v_mfma_f32_16x16x32_bf16 a[72:75], a[140:143], a[192:195], a[72:75]\n\t"
"v_accvgpr_write_b32 a144, v98\n\t"
"v_fma_f32 v98, -v88, s56, -v186\n\t"
"v_mfma_f32_16x16x32_bf16 a[80:83], a[124:127], a[188:191], a[80:83]\n\t"
"v_accvgpr_write_b32 a145, v99\n\t"
"v_fma_f32 v99, -v89, s56, -v187\n\t"
"v_mfma_f32_16x16x32_bf16 a[80:83], a[132:135], a[192:195], a[80:83]\n\t"
"v_cvt_pk_bf16_f32 v230, v232, v233\n\t"
"v_cvt_pk_bf16_f32 v231, v234, v235\n\t"
"v_mfma_f32_16x16x32_bf16 a[80:83], a[140:143], a[196:199], a[80:83]\n\t"
"ds_read_b64_tr_b16 v[232:233], v246 offset:19968\n\t"
"v_accvgpr_write_b32 a146, v98\n\t"
"v_mfma_f32_16x16x32_bf16 a[88:91], a[124:127], a[192:195], a[88:91]\n\t"
"s_waitcnt lgkmcnt(1)\n\t"
"ds_read_b64_tr_b16 v[234:235], v246 offset:23040\n\t"
"v_accvgpr_write_b32 a147, v99\n\t"
"v_mfma_f32_16x16x32_bf16 a[88:91], a[132:135], a[196:199], a[88:91]\n\t"
"v_fma_f32 v98, -v90, s56, -v188\n\t"
"v_fma_f32 v99, -v91, s56, -v189\n\t"
"v_mfma_f32_16x16x32_bf16 a[88:91], a[140:143], a[200:203], a[88:91]\n\t"
"v_accvgpr_write_b32 a152, v98\n\t"
"v_fma_f32 v98, -v92, s56, -v190\n\t"
"v_mfma_f32_16x16x32_bf16 v[224:227], v[224:227], v[220:223], 0\n\t"
"v_accvgpr_write_b32 a153, v99\n\t"
"v_fma_f32 v99, -v93, s56, -v191\n\t"
"v_mfma_f32_16x16x32_bf16 a[96:99], a[124:127], a[196:199], a[96:99]\n\t"
"v_accvgpr_write_b32 a154, v98\n\t"
"v_accvgpr_write_b32 a155, v99\n\t"
"v_mfma_f32_16x16x32_bf16 a[96:99], a[132:135], a[200:203], a[96:99]\n\t"
"v_fma_f32 v98, -v94, s56, -v192\n\t"
"v_fma_f32 v99, -v95, s56, -v193\n\t"
"v_mfma_f32_16x16x32_bf16 a[96:99], a[140:143], a[204:207], a[96:99]\n\t"
"v_accvgpr_write_b32 a160, v98\n\t"
"v_fma_f32 v98, -v96, s56, -v194\n\t"
"v_mfma_f32_16x16x32_bf16 a[104:107], a[148:151], a[168:171], a[104:107]\n\t"
"ds_read_b128 a[168:171], v244 offset:9216\n\t"
"v_accvgpr_write_b32 a161, v99\n\t"
"v_mfma_f32_16x16x32_bf16 a[104:107], a[156:159], a[172:175], a[104:107]\n\t"
"v_cvt_pk_bf16_f32 v224, v224, v225\n\t"
"v_cvt_pk_bf16_f32 v225, v226, v227\n\t"
"v_mfma_f32_16x16x32_bf16 a[104:107], a[164:167], a[176:179], a[104:107]\n\t"
"v_fma_f32 v99, -v97, s56, -v195\n\t"
"v_accvgpr_write_b32 a162, v98\n\t"
"v_mfma_f32_16x16x32_bf16 a[112:115], a[148:151], a[172:175], a[112:115]\n\t"
"s_waitcnt lgkmcnt(1)\n\t"
"ds_read_b128 a[172:175], v244 offset:9728\n\t"
"v_accvgpr_write_b32 a163, v99\n\t"
"v_mfma_f32_16x16x32_bf16 a[112:115], a[156:159], a[176:179], a[112:115]\n\t"
"v_fma_f32 v200, v100, s64, -v2\n\t"
"v_fma_f32 v201, v101, s64, -v3\n\t"
"v_mfma_f32_16x16x32_bf16 a[112:115], a[164:167], a[180:183], a[112:115]\n\t"
"v_fma_f32 v202, v102, s64, -v4\n\t"
"v_fma_f32 v203, v103, s64, -v5\n\t"
"v_mfma_f32_16x16x32_bf16 v[232:235], v[232:235], v[220:223], 0\n\t"
"v_fma_f32 v208, v104, s64, -v6\n\t"
"v_fma_f32 v209, v105, s64, -v7\n\t"
"v_mfma_f32_16x16x32_bf16 a[120:123], a[148:151], a[176:179], a[120:123]\n\t"
"ds_read_b128 a[176:179], v244 offset:10240\n\t"
"v_fma_f32 v210, v106, s64, -v8\n\t"
"v_mfma_f32_16x16x32_bf16 a[120:123], a[156:159], a[180:183], a[120:123]\n\t"
"v_fma_f32 v211, v107, s64, -v9\n\t"
"v_fma_f32 v216, v108, s64, -v10\n\t"
"v_mfma_f32_16x16x32_bf16 a[120:123], a[164:167], a[184:187], a[120:123]\n\t"
"v_fma_f32 v217, v109, s64, -v11\n\t"
"v_fma_f32 v218, v110, s64, -v12\n\t"
"v_mfma_f32_16x16x32_bf16 a[128:131], a[148:151], a[180:183], a[128:131]\n\t"
"ds_read_b128 a[180:183], v244 offset:10752\n\t"
"v_fma_f32 v219, v111, s64, -v13\n\t"
"v_mfma_f32_16x16x32_bf16 a[128:131], a[156:159], a[184:187], a[128:131]\n\t"
"v_cvt_pk_bf16_f32 v226, v232, v233\n\t"
"v_cvt_pk_bf16_f32 v227, v234, v235\n\t"
"v_mfma_f32_16x16x32_bf16 a[128:131], a[164:167], a[188:191], a[128:131]\n\t"
"ds_read_b64_tr_b16 v[232:233], v246 offset:26112\n\t"
"v_fma_f32 v98, v112, s64, -v14\n\t"
"v_mfma_f32_16x16x32_bf16 a[136:139], a[148:151], a[184:187], a[136:139]\n\t"
"ds_read_b64_tr_b16 v[234:235], v246 offset:29184\n\t"
"v_fma_f32 v99, v113, s64, -v15\n\t"
"v_mfma_f32_16x16x32_bf16 a[136:139], a[156:159], a[188:191], a[136:139]\n\t"
"ds_read_b128 a[184:187], v244 offset:11264\n\t"
"v_accvgpr_write_b32 a4, v98\n\t"
"v_mfma_f32_16x16x32_bf16 a[136:139], a[164:167], a[192:195], a[136:139]\n\t"
"v_fma_f32 v98, v114, s64, -v16\n\t"
"v_accvgpr_write_b32 a5, v99\n\t"
"v_mfma_f32_16x16x32_bf16 a[144:147], a[148:151], a[188:191], a[144:147]\n\t"
"ds_read_b128 a[188:191], v244 offset:11776\n\t"
"v_fma_f32 v99, v115, s64, -v17\n\t"
"v_mfma_f32_16x16x32_bf16 a[144:147], a[156:159], a[192:195], a[144:147]\n\t"
"v_accvgpr_write_b32 a6, v98\n\t"
"v_accvgpr_write_b32 a7, v99\n\t"
"v_mfma_f32_16x16x32_bf16 a[144:147], a[164:167], a[196:199], a[144:147]\n\t"
"v_fma_f32 v98, v116, s64, -v18\n\t"
"v_fma_f32 v99, v117, s64, -v19\n\t"
"v_mfma_f32_16x16x32_bf16 a[152:155], a[148:151], a[192:195], a[152:155]\n\t"
"ds_read_b128 a[192:195], v244 offset:12288\n\t"
"v_accvgpr_write_b32 a12, v98\n\t"
"v_mfma_f32_16x16x32_bf16 a[152:155], a[156:159], a[196:199], a[152:155]\n\t"
"v_fma_f32 v98, v118, s64, -v20\n\t"
"v_accvgpr_write_b32 a13, v99\n\t"
"v_mfma_f32_16x16x32_bf16 a[152:155], a[164:167], a[200:203], a[152:155]\n\t"
"v_fma_f32 v99, v119, s64, -v21\n\t"
"v_accvgpr_write_b32 a14, v98\n\t"
"v_mfma_f32_16x16x32_bf16 a[160:163], a[148:151], a[196:199], a[160:163]\n\t"
"s_waitcnt lgkmcnt(3)\n\t"
"ds_read_b128 a[196:199], v244 offset:12800\n\t"
"v_accvgpr_write_b32 a15, v99\n\t"
"v_mfma_f32_16x16x32_bf16 a[160:163], a[156:159], a[200:203], a[160:163]\n\t"
"ds_read_b128 a[200:203], v244 offset:13312\n\t"
"v_fma_f32 v98, v120, s64, -v22\n\t"
"v_mfma_f32_16x16x32_bf16 a[160:163], a[164:167], a[204:207], a[160:163]\n\t"
"ds_read_b128 a[204:207], v244 offset:13824\n\t"
"v_fma_f32 v99, v121, s64, -v23\n\t"
"v_mfma_f32_16x16x32_bf16 v[232:235], v[232:235], v[220:223], 0\n\t"
"v_accvgpr_write_b32 a20, v98\n\t"
"v_fma_f32 v98, v122, s64, -v24\n\t"
"v_mfma_f32_16x16x32_bf16 v[200:203], v[240:243], a[168:171], v[200:203]\n\t"
"v_accvgpr_write_b32 a21, v99\n\t"
"v_fma_f32 v99, v123, s64, -v25\n\t"
"v_mfma_f32_16x16x32_bf16 v[200:203], a[208:211], a[172:175], v[200:203]\n\t"
"v_accvgpr_write_b32 a22, v98\n\t"
"v_accvgpr_write_b32 a23, v99\n\t"
"v_mfma_f32_16x16x32_bf16 v[200:203], v[248:251], a[176:179], v[200:203]\n\t"
"s_add_u32 m0, s24, 896\n\t"
"s_add_u32 s28, s21, 0\n\t"
"v_fma_f32 v98, v124, s64, -v26\n\t"
"v_mfma_f32_16x16x32_bf16 v[208:211], v[240:243], a[172:175], v[208:211]\n\t"
"s_waitcnt lgkmcnt(5)\n\t"
"v_fma_f32 v99, v125, s64, -v27\n\t"
"v_accvgpr_write_b32 a28, v98\n\t"
"v_mfma_f32_16x16x32_bf16 v[208:211], a[208:211], a[176:179], v[208:211]\n\t"
"v_cvt_pk_bf16_f32 v232, v232, v233\n\t"
"v_mfma_f32_16x16x32_bf16 v[208:211], v[248:251], a[180:183], v[208:211]\n\t"
"s_waitcnt lgkmcnt(0)\n\t"
"s_barrier\n\t"
"v_mfma_f32_16x16x32_bf16 v[216:219], v[240:243], a[176:179], v[216:219]\n\t"
"v_cvt_pk_bf16_f32 v233, v234, v235\n\t"
"v_mfma_f32_16x16x32_bf16 v[216:219], a[208:211], a[180:183], v[216:219]\n\t"
"buffer_load_dwordx4 v247, s[12:15], s28 offen offset:128 nt lds\n\t"
"v_mfma_f32_16x16x32_bf16 v[216:219], v[248:251], a[184:187], v[216:219]\n\t"
"s_add_u32 m0, s24, 7040\n\t"
"s_add_u32 s28, s21, 196608\n\t"
"ds_write2st64_b64 v245, v[228:229], v[230:231] offset0:0 offset1:1\n\t"
"v_mfma_f32_16x16x32_bf16 a[4:7], v[240:243], a[180:183], a[4:7]\n\t"
"buffer_load_dwordx4 v247, s[12:15], s28 offen offset:128 nt lds\n\t"
"v_mfma_f32_16x16x32_bf16 a[4:7], a[208:211], a[184:187], a[4:7]\n\t"
"s_add_u32 m0, s24, 13184\n\t"
"s_add_u32 s28, s21, 393216\n\t"
"ds_read_b64_tr_b16 v[228:229], v246 offset:32256\n\t"
"v_mfma_f32_16x16x32_bf16 a[4:7], v[248:251], a[188:191], a[4:7]\n\t"
"ds_write2st64_b64 v245, v[224:225], v[226:227] offset0:2 offset1:3\n\t"
"v_mfma_f32_16x16x32_bf16 a[12:15], v[240:243], a[184:187], a[12:15]\n\t"
"buffer_load_dwordx4 v247, s[12:15], s28 offen offset:128 nt lds\n\t"
"v_mfma_f32_16x16x32_bf16 a[12:15], a[208:211], a[188:191], a[12:15]\n\t"
"s_add_u32 m0, s24, 20864\n\t"
"s_add_u32 s28, s21, 589824\n\t"
"ds_read_b64_tr_b16 v[230:231], v246 offset:35328\n\t"
"v_mfma_f32_16x16x32_bf16 a[12:15], v[248:251], a[192:195], a[12:15]\n\t"
"ds_write_b64 v245, v[232:233] offset:2048\n\t"
"v_fma_f32 v98, v126, s64, -v28\n\t"
"v_mfma_f32_16x16x32_bf16 a[20:23], v[240:243], a[188:191], a[20:23]\n\t"
"ds_read_b64_tr_b16 v[224:225], v246 offset:39936\n\t"
"v_accvgpr_write_b32 a29, v99\n\t"
"v_mfma_f32_16x16x32_bf16 a[20:23], a[208:211], a[192:195], a[20:23]\n\t"
"ds_read_b64_tr_b16 v[226:227], v246 offset:43008\n\t"
"v_fma_f32 v99, v127, s64, -v29\n\t"
"v_mfma_f32_16x16x32_bf16 a[20:23], v[248:251], a[196:199], a[20:23]\n\t"
"s_waitcnt lgkmcnt(3)\n\t"
"buffer_load_dwordx4 v247, s[12:15], s28 offen offset:128 nt lds\n\t"
"v_accvgpr_write_b32 a30, v98\n\t"
"v_accvgpr_write_b32 a31, v99\n\t"
"s_add_u32 m0, s24, 60800\n\t"
"s_add_u32 s28, s21, 1769472\n\t"
"v_mfma_f32_16x16x32_bf16 v[228:231], v[228:231], v[220:223], 0\n\t"
"buffer_load_dwordx4 v247, s[12:15], s28 offen offset:128 nt lds\n\t"
"v_fma_f32 v98, v128, s64, -v30\n\t"
"v_fma_f32 v99, v129, s64, -v31\n\t"
"s_add_u32 m0, s24, 66944\n\t"
"s_add_u32 s28, s21, 1966080\n\t"
"v_mfma_f32_16x16x32_bf16 a[28:31], v[240:243], a[192:195], a[28:31]\n\t"
"buffer_load_dwordx4 v247, s[12:15], s28 offen offset:128 nt lds\n\t"
"v_mfma_f32_16x16x32_bf16 a[28:31], a[208:211], a[196:199], a[28:31]\n\t"
"ds_read_b64_tr_b16 v[232:233], v246 offset:46080\n\t"
"v_accvgpr_write_b32 a36, v98\n\t"
"v_mfma_f32_16x16x32_bf16 a[28:31], v[248:251], a[200:203], a[28:31]\n\t"
"s_waitcnt lgkmcnt(1)\n\t"
"ds_read_b64_tr_b16 v[234:235], v246 offset:49152\n\t"
"v_fma_f32 v98, v130, s64, -v32\n\t"
"v_accvgpr_write_b32 a37, v99\n\t"
"v_fma_f32 v99, v131, s64, -v33\n\t"
"v_accvgpr_write_b32 a38, v98\n\t"
"v_accvgpr_write_b32 a39, v99\n\t"
"v_mfma_f32_16x16x32_bf16 v[224:227], v[224:227], v[220:223], 0\n\t"
"v_fma_f32 v98, v132, s64, -v34\n\t"
"v_fma_f32 v99, v133, s64, -v35\n\t"
"v_mfma_f32_16x16x32_bf16 a[36:39], v[240:243], a[196:199], a[36:39]\n\t"
"v_accvgpr_write_b32 a44, v98\n\t"
"v_fma_f32 v98, v134, s64, -v36\n\t"
"v_mfma_f32_16x16x32_bf16 a[36:39], a[208:211], a[200:203], a[36:39]\n\t"
"v_accvgpr_write_b32 a45, v99\n\t"
"v_fma_f32 v99, v135, s64, -v37\n\t"
"v_mfma_f32_16x16x32_bf16 a[36:39], v[248:251], a[204:207], a[36:39]\n\t"
"s_waitcnt lgkmcnt(0)\n\t"
"v_accvgpr_write_b32 a46, v98\n\t"
"v_accvgpr_write_b32 a47, v99\n\t"
"v_fma_f32 v98, v136, s64, -v38\n\t"
"v_fma_f32 v99, v137, s64, -v39\n\t"
"v_mfma_f32_16x16x32_bf16 v[232:235], v[232:235], v[220:223], 0\n\t"
"s_waitcnt vmcnt(15)\n\t"
"v_cvt_pk_bf16_f32 v228, v228, v229\n\t"
"v_cvt_pk_bf16_f32 v229, v230, v231\n\t"
"v_mfma_f32_16x16x32_bf16 a[44:47], a[212:215], a[168:171], a[44:47]\n\t"
"s_mov_b32 s20, 61440\n\t"
"v_accvgpr_write_b32 a52, v98\n\t"
"v_fma_f32 v98, v138, s64, -v40\n\t"
"v_mfma_f32_16x16x32_bf16 a[44:47], a[216:219], a[172:175], a[44:47]\n\t"
"v_accvgpr_write_b32 a53, v99\n\t"
"v_fma_f32 v99, v139, s64, -v41\n\t"
"v_mfma_f32_16x16x32_bf16 a[44:47], a[220:223], a[176:179], a[44:47]\n\t"
"v_accvgpr_write_b32 a54, v98\n\t"
"v_accvgpr_write_b32 a55, v99\n\t"
"v_fma_f32 v98, v140, s64, -v42\n\t"
"v_fma_f32 v99, v141, s64, -v43\n\t"
"v_mfma_f32_16x16x32_bf16 a[52:55], a[212:215], a[172:175], a[52:55]\n\t"
"v_accvgpr_write_b32 a60, v98\n\t"
"v_fma_f32 v98, v142, s64, -v44\n\t"
"v_mfma_f32_16x16x32_bf16 a[52:55], a[216:219], a[176:179], a[52:55]\n\t"
"v_accvgpr_write_b32 a61, v99\n\t"
"v_fma_f32 v99, v143, s64, -v45\n\t"
"v_mfma_f32_16x16x32_bf16 a[52:55], a[220:223], a[180:183], a[52:55]\n\t"
"v_accvgpr_write_b32 a62, v98\n\t"
"v_accvgpr_write_b32 a63, v99\n\t"
"v_cvt_pk_bf16_f32 v230, v224, v225\n\t"
"v_cvt_pk_bf16_f32 v231, v226, v227\n\t"
"v_fma_f32 v98, v144, s64, -v46\n\t"
"v_fma_f32 v99, v145, s64, -v47\n\t"
"v_mfma_f32_16x16x32_bf16 a[60:63], a[212:215], a[176:179], a[60:63]\n\t"
"ds_read_b64_tr_b16 v[224:225], v246 offset:52224\n\t"
"v_accvgpr_write_b32 a68, v98\n\t"
"v_mfma_f32_16x16x32_bf16 a[60:63], a[216:219], a[180:183], a[60:63]\n\t"
"ds_read_b64_tr_b16 v[226:227], v246 offset:55296\n\t"
"v_fma_f32 v98, v146, s64, -v48\n\t"
"v_mfma_f32_16x16x32_bf16 a[60:63], a[220:223], a[184:187], a[60:63]\n\t"
"ds_write2st64_b64 v245, v[228:229], v[230:231] offset0:5 offset1:6\n\t"
"v_accvgpr_write_b32 a69, v99\n\t"
"v_fma_f32 v99, v147, s64, -v49\n\t"
"v_accvgpr_write_b32 a70, v98\n\t"
"v_accvgpr_write_b32 a71, v99\n\t"
"v_fma_f32 v98, v148, s64, -v50\n\t"
"v_fma_f32 v99, v149, s64, -v51\n\t"
"v_cvt_pk_bf16_f32 v232, v232, v233\n\t"
"v_cvt_pk_bf16_f32 v233, v234, v235\n\t"
"v_mfma_f32_16x16x32_bf16 a[68:71], a[212:215], a[180:183], a[68:71]\n\t"
"v_accvgpr_write_b32 a76, v98\n\t"
"v_fma_f32 v98, v150, s64, -v52\n\t"
"v_mfma_f32_16x16x32_bf16 a[68:71], a[216:219], a[184:187], a[68:71]\n\t"
"v_accvgpr_write_b32 a77, v99\n\t"
"v_fma_f32 v99, v151, s64, -v53\n\t"
"v_mfma_f32_16x16x32_bf16 a[68:71], a[220:223], a[188:191], a[68:71]\n\t"
"buffer_load_dwordx4 v[240:243], v252, s[8:11], s20 offen offset:0 sc1\n\t"
"v_accvgpr_write_b32 a78, v98\n\t"
"v_accvgpr_write_b32 a79, v99\n\t"
"v_fma_f32 v98, v152, s64, -v54\n\t"
"v_fma_f32 v99, v153, s64, -v55\n\t"
"v_mfma_f32_16x16x32_bf16 a[76:79], a[212:215], a[184:187], a[76:79]\n\t"
"s_waitcnt lgkmcnt(1)\n\t"
"v_accvgpr_write_b32 a84, v98\n\t"
"v_fma_f32 v98, v154, s64, -v56\n\t"
"v_mfma_f32_16x16x32_bf16 a[76:79], a[216:219], a[188:191], a[76:79]\n\t"
"v_accvgpr_write_b32 a85, v99\n\t"
"v_fma_f32 v99, v155, s64, -v57\n\t"
"v_mfma_f32_16x16x32_bf16 a[76:79], a[220:223], a[192:195], a[76:79]\n\t"
"v_accvgpr_write_b32 a86, v98\n\t"
"v_accvgpr_write_b32 a87, v99\n\t"
"v_fma_f32 v98, v156, s64, -v58\n\t"
"v_fma_f32 v99, v157, s64, -v59\n\t"
"v_mfma_f32_16x16x32_bf16 v[224:227], v[224:227], v[220:223], 0\n\t"
"v_accvgpr_write_b32 a92, v98\n\t"
"v_fma_f32 v98, v158, s64, -v60\n\t"
"v_mfma_f32_16x16x32_bf16 a[84:87], a[212:215], a[188:191], a[84:87]\n\t"
"v_accvgpr_write_b32 a93, v99\n\t"
"v_fma_f32 v99, v159, s64, -v61\n\t"
"v_mfma_f32_16x16x32_bf16 a[84:87], a[216:219], a[192:195], a[84:87]\n\t"
"v_accvgpr_write_b32 a94, v98\n\t"
"v_accvgpr_write_b32 a95, v99\n\t"
"v_mfma_f32_16x16x32_bf16 a[84:87], a[220:223], a[196:199], a[84:87]\n\t"
"v_fma_f32 v98, v160, s64, -v62\n\t"
"v_fma_f32 v99, v161, s64, -v63\n\t"
"v_mfma_f32_16x16x32_bf16 a[92:95], a[212:215], a[192:195], a[92:95]\n\t"
"v_accvgpr_write_b32 a100, v98\n\t"
"v_fma_f32 v98, v162, s64, -v64\n\t"
"v_mfma_f32_16x16x32_bf16 a[92:95], a[216:219], a[196:199], a[92:95]\n\t"
"v_accvgpr_write_b32 a101, v99\n\t"
"v_fma_f32 v99, v163, s64, -v65\n\t"
"v_mfma_f32_16x16x32_bf16 a[92:95], a[220:223], a[200:203], a[92:95]\n\t"
"v_accvgpr_write_b32 a102, v98\n\t"
"v_accvgpr_write_b32 a103, v99\n\t"
"v_fma_f32 v98, v164, s64, -v66\n\t"
"v_fma_f32 v99, v165, s64, -v67\n\t"
"v_cvt_pk_bf16_f32 v234, v224, v225\n\t"
"v_cvt_pk_bf16_f32 v235, v226, v227\n\t"
"v_mfma_f32_16x16x32_bf16 a[100:103], a[212:215], a[196:199], a[100:103]\n\t"
"v_accvgpr_write_b32 a108, v98\n\t"
"v_fma_f32 v98, v166, s64, -v68\n\t"
"v_mfma_f32_16x16x32_bf16 a[100:103], a[216:219], a[200:203], a[100:103]\n\t"
"v_accvgpr_write_b32 a109, v99\n\t"
"v_fma_f32 v99, v167, s64, -v69\n\t"
"v_mfma_f32_16x16x32_bf16 a[100:103], a[220:223], a[204:207], a[100:103]\n\t"
"ds_write2st64_b64 v245, v[232:233], v[234:235] offset0:7 offset1:8\n\t"
"v_accvgpr_write_b32 a110, v98\n\t"
"v_accvgpr_write_b32 a111, v99\n\t"
"v_fma_f32 v98, v168, s64, -v70\n\t"
"v_fma_f32 v99, v169, s64, -v71\n\t"
"s_waitcnt vmcnt(13)\n\t"
"v_mfma_f32_16x16x32_bf16 a[108:111], a[224:227], a[168:171], a[108:111]\n\t"
"v_accvgpr_write_b32 a116, v98\n\t"
"v_fma_f32 v98, v170, s64, -v72\n\t"
"v_mfma_f32_16x16x32_bf16 a[108:111], a[228:231], a[172:175], a[108:111]\n\t"
"v_accvgpr_write_b32 a117, v99\n\t"
"v_mfma_f32_16x16x32_bf16 a[108:111], a[232:235], a[176:179], a[108:111]\n\t"
"v_accvgpr_write_b32 a118, v98\n\t"
"v_fma_f32 v99, v171, s64, -v73\n\t"
"v_accvgpr_write_b32 a119, v99\n\t"
"s_waitcnt lgkmcnt(0)\n\t"
"s_barrier\n\t"
"v_mfma_f32_16x16x32_bf16 a[116:119], a[224:227], a[172:175], a[116:119]\n\t"
"ds_read_b128 a[168:171], v244 offset:0\n\t"
"v_mfma_f32_16x16x32_bf16 a[116:119], a[228:231], a[176:179], a[116:119]\n\t"
"buffer_load_dwordx4 a[208:211], v252, s[8:11], s20 offen offset:1024 sc1\n\t"
"v_mfma_f32_16x16x32_bf16 a[116:119], a[232:235], a[180:183], a[116:119]\n\t"
"ds_read_b128 a[172:175], v244 offset:512\n\t"
"v_fma_f32 v98, v172, s64, -v74\n\t"
"v_fma_f32 v99, v173, s64, -v75\n\t"
"v_accvgpr_write_b32 a124, v98\n\t"
"v_fma_f32 v98, v174, s64, -v76\n\t"
"v_accvgpr_write_b32 a125, v99\n\t"
"v_fma_f32 v99, v175, s64, -v77\n\t"
"v_accvgpr_write_b32 a126, v98\n\t"
"v_accvgpr_write_b32 a127, v99\n\t"
"v_fma_f32 v98, v176, s64, -v78\n\t"
"v_fma_f32 v99, v177, s64, -v79\n\t"
"v_mfma_f32_16x16x32_bf16 a[124:127], a[224:227], a[176:179], a[124:127]\n\t"
"ds_read_b128 a[176:179], v244 offset:1024\n\t"
"v_accvgpr_write_b32 a132, v98\n\t"
"v_mfma_f32_16x16x32_bf16 a[124:127], a[228:231], a[180:183], a[124:127]\n\t"
"v_fma_f32 v98, v178, s64, -v80\n\t"
"v_accvgpr_write_b32 a133, v99\n\t"
"v_mfma_f32_16x16x32_bf16 a[124:127], a[232:235], a[184:187], a[124:127]\n\t"
"v_fma_f32 v99, v179, s64, -v81\n\t"
"v_accvgpr_write_b32 a134, v98\n\t"
"v_accvgpr_write_b32 a135, v99\n\t"
"v_fma_f32 v98, v180, s64, -v82\n\t"
"v_fma_f32 v99, v181, s64, -v83\n\t"
"v_mfma_f32_16x16x32_bf16 a[132:135], a[224:227], a[180:183], a[132:135]\n\t"
"ds_read_b128 a[180:183], v244 offset:1536\n\t"
"v_accvgpr_write_b32 a140, v98\n\t"
"v_mfma_f32_16x16x32_bf16 a[132:135], a[228:231], a[184:187], a[132:135]\n\t"
"v_fma_f32 v98, v182, s64, -v84\n\t"
"v_accvgpr_write_b32 a141, v99\n\t"
"v_mfma_f32_16x16x32_bf16 a[132:135], a[232:235], a[188:191], a[132:135]\n\t"
"buffer_load_dwordx4 v[248:251], v252, s[8:11], s20 offen offset:2048 sc1\n\t"
"v_fma_f32 v99, v183, s64, -v85\n\t"
"v_accvgpr_write_b32 a142, v98\n\t"
"v_accvgpr_write_b32 a143, v99\n\t"
"v_fma_f32 v98, v184, s64, -v86\n\t"
"v_fma_f32 v99, v185, s64, -v87\n\t"
"s_mov_b32 s20, 73728\n\t"
"v_mfma_f32_16x16x32_bf16 a[140:143], a[224:227], a[184:187], a[140:143]\n\t"
"buffer_load_dwordx4 a[212:215], v252, s[8:11], s20 offen offset:0 sc1\n\t"
"v_mfma_f32_16x16x32_bf16 a[140:143], a[228:231], a[188:191], a[140:143]\n\t"
"ds_read_b128 a[184:187], v244 offset:2048\n\t"
"v_accvgpr_write_b32 a148, v98\n\t"
"v_mfma_f32_16x16x32_bf16 a[140:143], a[232:235], a[192:195], a[140:143]\n\t"
"v_fma_f32 v98, v186, s64, -v88\n\t"
"v_accvgpr_write_b32 a149, v99\n\t"
"v_fma_f32 v99, v187, s64, -v89\n\t"
"v_accvgpr_write_b32 a150, v98\n\t"
"v_accvgpr_write_b32 a151, v99\n\t"
"v_fma_f32 v98, v188, s64, -v90\n\t"
"v_fma_f32 v99, v189, s64, -v91\n\t"
"v_mfma_f32_16x16x32_bf16 a[148:151], a[224:227], a[188:191], a[148:151]\n\t"
"ds_read_b128 a[188:191], v244 offset:2560\n\t"
"v_accvgpr_write_b32 a156, v98\n\t"
"v_mfma_f32_16x16x32_bf16 a[148:151], a[228:231], a[192:195], a[148:151]\n\t"
"v_fma_f32 v98, v190, s64, -v92\n\t"
"v_accvgpr_write_b32 a157, v99\n\t"
"v_mfma_f32_16x16x32_bf16 a[148:151], a[232:235], a[196:199], a[148:151]\n\t"
"buffer_load_dwordx4 a[216:219], v252, s[8:11], s20 offen offset:1024 sc1\n\t"
"v_fma_f32 v99, v191, s64, -v93\n\t"
"v_accvgpr_write_b32 a158, v98\n\t"
"v_accvgpr_write_b32 a159, v99\n\t"
"v_fma_f32 v98, v192, s64, -v94\n\t"
"v_fma_f32 v99, v193, s64, -v95\n\t"
"v_mfma_f32_16x16x32_bf16 a[156:159], a[224:227], a[192:195], a[156:159]\n\t"
"ds_read_b128 a[192:195], v244 offset:3072\n\t"
"v_accvgpr_write_b32 a164, v98\n\t"
"v_mfma_f32_16x16x32_bf16 a[156:159], a[228:231], a[196:199], a[156:159]\n\t"
"v_fma_f32 v98, v194, s64, -v96\n\t"
"v_accvgpr_write_b32 a165, v99\n\t"
"v_mfma_f32_16x16x32_bf16 a[156:159], a[232:235], a[200:203], a[156:159]\n\t"
"buffer_load_dwordx4 a[220:223], v252, s[8:11], s20 offen offset:2048 sc1\n\t"
"v_fma_f32 v99, v195, s64, -v97\n\t"
"v_accvgpr_write_b32 a166, v98\n\t"
"v_accvgpr_write_b32 a167, v99\n\t"
"v_fma_f32 v0, v2, s52, v100\n\t"
"v_fma_f32 v1, v3, s52, v101\n\t"
"s_mov_b32 s20, 86016\n\t"
"v_mfma_f32_16x16x32_bf16 a[164:167], a[224:227], a[196:199], a[164:167]\n\t"
"buffer_load_dwordx4 a[224:227], v252, s[8:11], s20 offen offset:0 sc1\n\t"
"v_mfma_f32_16x16x32_bf16 a[164:167], a[228:231], a[200:203], a[164:167]\n\t"
"ds_read_b128 a[196:199], v244 offset:3584\n\t"
"v_fma_f32 v100, -v100, s66, v2\n\t"
"v_mfma_f32_16x16x32_bf16 a[164:167], a[232:235], a[204:207], a[164:167]\n\t"
"ds_read_b128 a[200:203], v244 offset:4096\n\t"
"v_fma_f32 v2, v4, s52, v102\n\t"
"v_fma_f32 v101, -v101, s66, v3\n\t"
"v_fma_f32 v3, v5, s52, v103\n\t"
"v_fma_f32 v102, -v102, s66, v4\n\t"
"v_fma_f32 v103, -v103, s66, v5\n\t"
"v_fma_f32 v4, v6, s52, v104\n\t"
"v_fma_f32 v5, v7, s52, v105\n\t"
"Lst3_%=:\n\t"
"s_waitcnt vmcnt(16) lgkmcnt(6)\n\t"
"v_mfma_f32_16x16x32_bf16 v[0:3], a[236:239], a[168:171], v[0:3]\n\t"
"Lpg3_%=:\n\t"
"s_cbranch_vccnz Lpg7_%=\n\t"
"ds_read_b128 a[204:207], v244 offset:4608\n\t"
"v_fma_f32 v104, -v104, s66, v6\n\t"
"v_mfma_f32_16x16x32_bf16 v[0:3], a[240:243], a[172:175], v[0:3]\n\t"
"v_fma_f32 v6, v8, s52, v106\n\t"
"v_fma_f32 v105, -v105, s66, v7\n\t"
"v_mfma_f32_16x16x32_bf16 v[0:3], a[244:247], a[176:179], v[0:3]\n\t"
"v_fma_f32 v7, v9, s52, v107\n\t"
"v_fma_f32 v106, -v106, s66, v8\n\t"
"v_fma_f32 v107, -v107, s66, v9\n\t"
"v_fma_f32 v8, v10, s52, v108\n\t"
"v_fma_f32 v9, v11, s52, v109\n\t"
"s_waitcnt lgkmcnt(6)\n\t"
"v_mfma_f32_16x16x32_bf16 v[4:7], a[236:239], a[172:175], v[4:7]\n\t"
"buffer_load_dwordx4 a[228:231], v252, s[8:11], s20 offen offset:1024 sc1\n\t"
"v_mfma_f32_16x16x32_bf16 v[4:7], a[240:243], a[176:179], v[4:7]\n\t"
"buffer_load_dwordx4 a[232:235], v252, s[8:11], s20 offen offset:2048 sc1\n\t"
"v_mfma_f32_16x16x32_bf16 v[4:7], a[244:247], a[180:183], v[4:7]\n\t"
"v_fma_f32 v108, -v108, s66, v10\n\t"
"v_fma_f32 v10, v12, s52, v110\n\t"
"v_fma_f32 v109, -v109, s66, v11\n\t"
"v_fma_f32 v11, v13, s52, v111\n\t"
"v_fma_f32 v110, -v110, s66, v12\n\t"
"v_fma_f32 v111, -v111, s66, v13\n\t"
"v_fma_f32 v12, v14, s52, v112\n\t"
"v_fma_f32 v13, v15, s52, v113\n\t"
"s_waitcnt lgkmcnt(5)\n\t"
"v_mfma_f32_16x16x32_bf16 v[8:11], a[236:239], a[176:179], v[8:11]\n\t"
"v_fma_f32 v112, -v112, s66, v14\n\t"
"v_fma_f32 v14, v16, s52, v114\n\t"
"v_mfma_f32_16x16x32_bf16 v[8:11], a[240:243], a[180:183], v[8:11]\n\t"
"v_fma_f32 v113, -v113, s66, v15\n\t"
"v_fma_f32 v15, v17, s52, v115\n\t"
"v_mfma_f32_16x16x32_bf16 v[8:11], a[244:247], a[184:187], v[8:11]\n\t"
"v_fma_f32 v114, -v114, s66, v16\n\t"
"v_fma_f32 v115, -v115, s66, v17\n\t"
"v_fma_f32 v16, v18, s52, v116\n\t"
"v_fma_f32 v17, v19, s52, v117\n\t"
"s_waitcnt lgkmcnt(4)\n\t"
"v_mfma_f32_16x16x32_bf16 v[12:15], a[236:239], a[180:183], v[12:15]\n\t"
"v_fma_f32 v116, -v116, s66, v18\n\t"
"v_fma_f32 v18, v20, s52, v118\n\t"
"v_mfma_f32_16x16x32_bf16 v[12:15], a[240:243], a[184:187], v[12:15]\n\t"
"v_fma_f32 v117, -v117, s66, v19\n\t"
"v_fma_f32 v19, v21, s52, v119\n\t"
"v_mfma_f32_16x16x32_bf16 v[12:15], a[244:247], a[188:191], v[12:15]\n\t"
"v_fma_f32 v118, -v118, s66, v20\n\t"
"v_fma_f32 v119, -v119, s66, v21\n\t"
"v_fma_f32 v20, v22, s52, v120\n\t"
"v_fma_f32 v21, v23, s52, v121\n\t"
"s_waitcnt lgkmcnt(3)\n\t"
"v_mfma_f32_16x16x32_bf16 v[16:19], a[236:239], a[184:187], v[16:19]\n\t"
"v_fma_f32 v120, -v120, s66, v22\n\t"
"v_fma_f32 v22, v24, s52, v122\n\t"
"v_mfma_f32_16x16x32_bf16 v[16:19], a[240:243], a[188:191], v[16:19]\n\t"
"v_fma_f32 v121, -v121, s66, v23\n\t"
"v_fma_f32 v23, v25, s52, v123\n\t"
"v_mfma_f32_16x16x32_bf16 v[16:19], a[244:247], a[192:195], v[16:19]\n\t"
"ds_read_b64_tr_b16 v[228:229], v246 offset:512\n\t"
"v_fma_f32 v122, -v122, s66, v24\n\t"
"v_fma_f32 v123, -v123, s66, v25\n\t"
"v_fma_f32 v24, v26, s52, v124\n\t"
"v_fma_f32 v25, v27, s52, v125\n\t"
"s_waitcnt lgkmcnt(3)\n\t"
"v_mfma_f32_16x16x32_bf16 v[20:23], a[236:239], a[188:191], v[20:23]\n\t"
"s_mov_b32 s20, 98304\n\t"
"ds_read_b64_tr_b16 v[230:231], v246 offset:3584\n\t"
"v_fma_f32 v124, -v124, s66, v26\n\t"
"v_mfma_f32_16x16x32_bf16 v[20:23], a[240:243], a[192:195], v[20:23]\n\t"
"v_fma_f32 v26, v28, s52, v126\n\t"
"v_fma_f32 v125, -v125, s66, v27\n\t"
"v_mfma_f32_16x16x32_bf16 v[20:23], a[244:247], a[196:199], v[20:23]\n\t"
"v_fma_f32 v27, v29, s52, v127\n\t"
"v_fma_f32 v126, -v126, s66, v28\n\t"
"v_fma_f32 v127, -v127, s66, v29\n\t"
"v_fma_f32 v28, v30, s52, v128\n\t"
"v_fma_f32 v29, v31, s52, v129\n\t"
"s_waitcnt lgkmcnt(3)\n\t"
"v_mfma_f32_16x16x32_bf16 v[24:27], a[236:239], a[192:195], v[24:27]\n\t"
"v_fma_f32 v128, -v128, s66, v30\n\t"
"v_fma_f32 v30, v32, s52, v130\n\t"
"v_mfma_f32_16x16x32_bf16 v[24:27], a[240:243], a[196:199], v[24:27]\n\t"
"v_fma_f32 v129, -v129, s66, v31\n\t"
"v_fma_f32 v31, v33, s52, v131\n\t"
"v_mfma_f32_16x16x32_bf16 v[24:27], a[244:247], a[200:203], v[24:27]\n\t"
"v_fma_f32 v130, -v130, s66, v32\n\t"
"v_fma_f32 v131, -v131, s66, v33\n\t"
"v_fma_f32 v32, v34, s52, v132\n\t"
"v_fma_f32 v33, v35, s52, v133\n\t"
"s_waitcnt lgkmcnt(2)\n\t"
"v_mfma_f32_16x16x32_bf16 v[28:31], a[236:239], a[196:199], v[28:31]\n\t"
"buffer_load_dwordx4 a[236:239], v252, s[8:11], s20 offen offset:0 sc1\n\t"
"v_mfma_f32_16x16x32_bf16 v[28:31], a[240:243], a[200:203], v[28:31]\n\t"
"ds_read_b64_tr_b16 v[224:225], v246 offset:6656\n\t"
"v_fma_f32 v132, -v132, s66, v34\n\t"
"v_mfma_f32_16x16x32_bf16 v[28:31], a[244:247], a[204:207], v[28:31]\n\t"
"ds_read_b64_tr_b16 v[226:227], v246 offset:9728\n\t"
"v_fma_f32 v34, v36, s52, v134\n\t"
"v_fma_f32 v133, -v133, s66, v35\n\t"
"v_fma_f32 v35, v37, s52, v135\n\t"
"v_fma_f32 v134, -v134, s66, v36\n\t"
"v_fma_f32 v135, -v135, s66, v37\n\t"
"v_fma_f32 v36, v38, s52, v136\n\t"
"v_fma_f32 v37, v39, s52, v137\n\t"
"s_waitcnt vmcnt(16)\n\t"
"v_mfma_f32_16x16x32_bf16 v[32:35], a[248:251], a[168:171], v[32:35]\n\t"
"buffer_load_dwordx4 a[240:243], v252, s[8:11], s20 offen offset:1024 sc1\n\t"
"v_mfma_f32_16x16x32_bf16 v[32:35], a[252:255], a[172:175], v[32:35]\n\t"
"v_fma_f32 v136, -v136, s66, v38\n\t"
"v_fma_f32 v38, v40, s52, v138\n\t"
"v_mfma_f32_16x16x32_bf16 v[32:35], v[236:239], a[176:179], v[32:35]\n\t"
"v_fma_f32 v137, -v137, s66, v39\n\t"
"v_fma_f32 v39, v41, s52, v139\n\t"
"v_fma_f32 v138, -v138, s66, v40\n\t"
"v_fma_f32 v139, -v139, s66, v41\n\t"
"v_mfma_f32_16x16x32_bf16 v[36:39], a[248:251], a[172:175], v[36:39]\n\t"
"s_waitcnt lgkmcnt(2)\n\t"
"v_fma_f32 v40, v42, s52, v140\n\t"
"v_fma_f32 v41, v43, s52, v141\n\t"
"v_mfma_f32_16x16x32_bf16 v[36:39], a[252:255], a[176:179], v[36:39]\n\t"
"v_fma_f32 v140, -v140, s66, v42\n\t"
"v_fma_f32 v42, v44, s52, v142\n\t"
"v_mfma_f32_16x16x32_bf16 v[36:39], v[236:239], a[180:183], v[36:39]\n\t"
"v_fma_f32 v141, -v141, s66, v43\n\t"
"v_fma_f32 v43, v45, s52, v143\n\t"
"v_fma_f32 v142, -v142, s66, v44\n\t"
"v_fma_f32 v143, -v143, s66, v45\n\t"
"v_mfma_f32_16x16x32_bf16 v[228:231], v[228:231], v[220:223], 0\n\t"
"buffer_load_dwordx4 a[244:247], v252, s[8:11], s20 offen offset:2048 sc1\n\t"
"v_fma_f32 v44, v46, s52, v144\n\t"
"v_fma_f32 v45, v47, s52, v145\n\t"
"v_mfma_f32_16x16x32_bf16 v[40:43], a[248:251], a[176:179], v[40:43]\n\t"
"ds_read_b64_tr_b16 v[232:233], v246 offset:12800\n\t"
"v_fma_f32 v144, -v144, s66, v46\n\t"
"v_mfma_f32_16x16x32_bf16 v[40:43], a[252:255], a[180:183], v[40:43]\n\t"
"ds_read_b64_tr_b16 v[234:235], v246 offset:15872\n\t"
"v_fma_f32 v46, v48, s52, v146\n\t"
"v_mfma_f32_16x16x32_bf16 v[40:43], v[236:239], a[184:187], v[40:43]\n\t"
"v_fma_f32 v145, -v145, s66, v47\n\t"
"v_fma_f32 v47, v49, s52, v147\n\t"
"v_fma_f32 v146, -v146, s66, v48\n\t"
"v_fma_f32 v147, -v147, s66, v49\n\t"
"v_mfma_f32_16x16x32_bf16 v[44:47], a[248:251], a[180:183], v[44:47]\n\t"
"s_waitcnt lgkmcnt(2)\n\t"
"v_fma_f32 v48, v50, s52, v148\n\t"
"v_fma_f32 v49, v51, s52, v149\n\t"
"v_mfma_f32_16x16x32_bf16 v[44:47], a[252:255], a[184:187], v[44:47]\n\t"
"v_fma_f32 v148, -v148, s66, v50\n\t"
"v_fma_f32 v50, v52, s52, v150\n\t"
"v_mfma_f32_16x16x32_bf16 v[44:47], v[236:239], a[188:191], v[44:47]\n\t"
"v_fma_f32 v149, -v149, s66, v51\n\t"
"v_fma_f32 v51, v53, s52, v151\n\t"
"v_fma_f32 v150, -v150, s66, v52\n\t"
"v_fma_f32 v151, -v151, s66, v53\n\t"
"v_mfma_f32_16x16x32_bf16 v[224:227], v[224:227], v[220:223], 0\n\t"
"v_fma_f32 v52, v54, s52, v152\n\t"
"v_fma_f32 v53, v55, s52, v153\n\t"
"v_mfma_f32_16x16x32_bf16 v[48:51], a[248:251], a[184:187], v[48:51]\n\t"
"v_fma_f32 v152, -v152, s66, v54\n\t"
"v_fma_f32 v54, v56, s52, v154\n\t"
"v_mfma_f32_16x16x32_bf16 v[48:51], a[252:255], a[188:191], v[48:51]\n\t"
"v_fma_f32 v153, -v153, s66, v55\n\t"
"v_fma_f32 v55, v57, s52, v155\n\t"
"v_mfma_f32_16x16x32_bf16 v[48:51], v[236:239], a[192:195], v[48:51]\n\t"
"v_fma_f32 v154, -v154, s66, v56\n\t"
"v_fma_f32 v155, -v155, s66, v57\n\t"
"v_cvt_pk_bf16_f32 v228, v228, v229\n\t"
"v_cvt_pk_bf16_f32 v229, v230, v231\n\t"
"v_fma_f32 v56, v58, s52, v156\n\t"
"v_fma_f32 v57, v59, s52, v157\n\t"
"v_mfma_f32_16x16x32_bf16 v[52:55], a[248:251], a[188:191], v[52:55]\n\t"
"v_fma_f32 v156, -v156, s66, v58\n\t"
"v_fma_f32 v58, v60, s52, v158\n\t"
"v_mfma_f32_16x16x32_bf16 v[52:55], a[252:255], a[192:195], v[52:55]\n\t"
"s_mov_b32 s20, 331776\n\t"
"v_fma_f32 v157, -v157, s66, v59\n\t"
"v_fma_f32 v59, v61, s52, v159\n\t"
"v_mfma_f32_16x16x32_bf16 v[52:55], v[236:239], a[196:199], v[52:55]\n\t"
"v_fma_f32 v158, -v158, s66, v60\n\t"
"v_fma_f32 v159, -v159, s66, v61\n\t"
"v_mfma_f32_16x16x32_bf16 v[56:59], a[248:251], a[192:195], v[56:59]\n\t"
"s_waitcnt lgkmcnt(0)\n\t"
"v_fma_f32 v60, v62, s52, v160\n\t"
"v_fma_f32 v61, v63, s52, v161\n\t"
"v_mfma_f32_16x16x32_bf16 v[56:59], a[252:255], a[196:199], v[56:59]\n\t"
"v_fma_f32 v160, -v160, s66, v62\n\t"
"v_fma_f32 v62, v64, s52, v162\n\t"
"v_mfma_f32_16x16x32_bf16 v[56:59], v[236:239], a[200:203], v[56:59]\n\t"
"v_fma_f32 v161, -v161, s66, v63\n\t"
"v_fma_f32 v63, v65, s52, v163\n\t"
"v_fma_f32 v162, -v162, s66, v64\n\t"
"v_fma_f32 v163, -v163, s66, v65\n\t"
"v_cvt_pk_bf16_f32 v230, v224, v225\n\t"
"v_cvt_pk_bf16_f32 v231, v226, v227\n\t"
"v_mfma_f32_16x16x32_bf16 v[232:235], v[232:235], v[220:223], 0\n\t"
"ds_read_b64_tr_b16 v[224:225], v246 offset:20480\n\t"
"v_fma_f32 v64, v66, s52, v164\n\t"
"v_fma_f32 v65, v67, s52, v165\n\t"
"v_mfma_f32_16x16x32_bf16 v[60:63], a[248:251], a[196:199], v[60:63]\n\t"
"ds_read_b64_tr_b16 v[226:227], v246 offset:23552\n\t"
"v_fma_f32 v164, -v164, s66, v66\n\t"
"v_mfma_f32_16x16x32_bf16 v[60:63], a[252:255], a[200:203], v[60:63]\n\t"
"v_fma_f32 v66, v68, s52, v166\n\t"
"v_fma_f32 v165, -v165, s66, v67\n\t"
"v_mfma_f32_16x16x32_bf16 v[60:63], v[236:239], a[204:207], v[60:63]\n\t"
"v_fma_f32 v67, v69, s52, v167\n\t"
"v_fma_f32 v166, -v166, s66, v68\n\t"
"v_fma_f32 v167, -v167, s66, v69\n\t"
"v_fma_f32 v68, v70, s52, v168\n\t"
"v_fma_f32 v69, v71, s52, v169\n\t"
"s_waitcnt vmcnt(9)\n\t"
"v_mfma_f32_16x16x32_bf16 v[64:67], v[240:243], a[168:171], v[64:67]\n\t"
"buffer_load_dwordx4 a[248:251], v252, s[8:11], s20 offen offset:0 sc1\n\t"
"v_mfma_f32_16x16x32_bf16 v[64:67], a[208:211], a[172:175], v[64:67]\n\t"
"buffer_load_dwordx4 v[236:239], v252, s[8:11], s20 offen offset:1024 sc1\n\t"
"v_mfma_f32_16x16x32_bf16 v[64:67], v[248:251], a[176:179], v[64:67]\n\t"
"ds_read_b128 a[168:171], v244 offset:9216\n\t"
"v_fma_f32 v168, -v168, s66, v70\n\t"
"v_fma_f32 v70, v72, s52, v170\n\t"
"v_fma_f32 v169, -v169, s66, v71\n\t"
"v_fma_f32 v71, v73, s52, v171\n\t"
"v_fma_f32 v170, -v170, s66, v72\n\t"
"v_fma_f32 v171, -v171, s66, v73\n\t"
"v_mfma_f32_16x16x32_bf16 v[68:71], v[240:243], a[172:175], v[68:71]\n\t"
"s_waitcnt lgkmcnt(1)\n\t"
"ds_read_b128 a[172:175], v244 offset:9728\n\t"
"v_fma_f32 v72, v74, s52, v172\n\t"
"v_mfma_f32_16x16x32_bf16 v[68:71], a[208:211], a[176:179], v[68:71]\n\t"
"v_fma_f32 v73, v75, s52, v173\n\t"
"v_fma_f32 v172, -v172, s66, v74\n\t"
"v_mfma_f32_16x16x32_bf16 v[68:71], v[248:251], a[180:183], v[68:71]\n\t"
"v_fma_f32 v74, v76, s52, v174\n\t"
"v_fma_f32 v173, -v173, s66, v75\n\t"
"v_fma_f32 v75, v77, s52, v175\n\t"
"v_fma_f32 v174, -v174, s66, v76\n\t"
"v_fma_f32 v175, -v175, s66, v77\n\t"
"v_cvt_pk_bf16_f32 v232, v232, v233\n\t"
"v_cvt_pk_bf16_f32 v233, v234, v235\n\t"
"v_mfma_f32_16x16x32_bf16 v[224:227], v[224:227], v[220:223], 0\n\t"
"buffer_load_dwordx4 a[252:255], v252, s[8:11], s20 offen offset:2048 sc1\n\t"
"v_fma_f32 v76, v78, s52, v176\n\t"
"v_fma_f32 v77, v79, s52, v177\n\t"
"v_mfma_f32_16x16x32_bf16 v[72:75], v[240:243], a[176:179], v[72:75]\n\t"
"ds_read_b128 a[176:179], v244 offset:10240\n\t"
"v_fma_f32 v176, -v176, s66, v78\n\t"
"v_mfma_f32_16x16x32_bf16 v[72:75], a[208:211], a[180:183], v[72:75]\n\t"
"v_fma_f32 v78, v80, s52, v178\n\t"
"v_fma_f32 v177, -v177, s66, v79\n\t"
"v_mfma_f32_16x16x32_bf16 v[72:75], v[248:251], a[184:187], v[72:75]\n\t"
"v_fma_f32 v79, v81, s52, v179\n\t"
"v_fma_f32 v178, -v178, s66, v80\n\t"
"v_fma_f32 v179, -v179, s66, v81\n\t"
"v_fma_f32 v80, v82, s52, v180\n\t"
"v_fma_f32 v81, v83, s52, v181\n\t"
"v_mfma_f32_16x16x32_bf16 v[76:79], v[240:243], a[180:183], v[76:79]\n\t"
"ds_read_b128 a[180:183], v244 offset:10752\n\t"
"v_fma_f32 v180, -v180, s66, v82\n\t"
"v_mfma_f32_16x16x32_bf16 v[76:79], a[208:211], a[184:187], v[76:79]\n\t"
"v_fma_f32 v82, v84, s52, v182\n\t"
"v_fma_f32 v181, -v181, s66, v83\n\t"
"v_mfma_f32_16x16x32_bf16 v[76:79], v[248:251], a[188:191], v[76:79]\n\t"
"v_fma_f32 v83, v85, s52, v183\n\t"
"v_fma_f32 v182, -v182, s66, v84\n\t"
"v_fma_f32 v183, -v183, s66, v85\n\t"
"v_fma_f32 v84, v86, s52, v184\n\t"
"v_fma_f32 v85, v87, s52, v185\n\t"
"v_mfma_f32_16x16x32_bf16 v[80:83], v[240:243], a[184:187], v[80:83]\n\t"
"ds_read_b128 a[184:187], v244 offset:11264\n\t"
"v_fma_f32 v184, -v184, s66, v86\n\t"
"v_mfma_f32_16x16x32_bf16 v[80:83], a[208:211], a[188:191], v[80:83]\n\t"
"s_mov_b32 s20, 344064\n\t"
"v_fma_f32 v86, v88, s52, v186\n\t"
"v_fma_f32 v185, -v185, s66, v87\n\t"
"v_mfma_f32_16x16x32_bf16 v[80:83], v[248:251], a[192:195], v[80:83]\n\t"
"v_fma_f32 v87, v89, s52, v187\n\t"
"v_fma_f32 v186, -v186, s66, v88\n\t"
"v_fma_f32 v187, -v187, s66, v89\n\t"
"v_cvt_pk_bf16_f32 v234, v224, v225\n\t"
"v_cvt_pk_bf16_f32 v235, v226, v227\n\t"
"v_fma_f32 v88, v90, s52, v188\n\t"
"v_fma_f32 v89, v91, s52, v189\n\t"
"v_mfma_f32_16x16x32_bf16 v[84:87], v[240:243], a[188:191], v[84:87]\n\t"
"ds_read_b64_tr_b16 v[224:225], v246 offset:26624\n\t"
"v_fma_f32 v188, -v188, s66, v90\n\t"
"v_mfma_f32_16x16x32_bf16 v[84:87], a[208:211], a[192:195], v[84:87]\n\t"
"ds_read_b64_tr_b16 v[226:227], v246 offset:29696\n\t"
"v_fma_f32 v90, v92, s52, v190\n\t"
"v_mfma_f32_16x16x32_bf16 v[84:87], v[248:251], a[196:199], v[84:87]\n\t"
"ds_read_b128 a[188:191], v244 offset:11776\n\t"
"v_fma_f32 v189, -v189, s66, v91\n\t"
"v_fma_f32 v91, v93, s52, v191\n\t"
"v_fma_f32 v190, -v190, s66, v92\n\t"
"v_fma_f32 v191, -v191, s66, v93\n\t"
"v_fma_f32 v92, v94, s52, v192\n\t"
"v_fma_f32 v93, v95, s52, v193\n\t"
"v_mfma_f32_16x16x32_bf16 v[88:91], v[240:243], a[192:195], v[88:91]\n\t"
"ds_read_b128 a[192:195], v244 offset:12288\n\t"
"v_fma_f32 v192, -v192, s66, v94\n\t"
"v_mfma_f32_16x16x32_bf16 v[88:91], a[208:211], a[196:199], v[88:91]\n\t"
"v_fma_f32 v94, v96, s52, v194\n\t"
"v_fma_f32 v193, -v193, s66, v95\n\t"
"v_mfma_f32_16x16x32_bf16 v[88:91], v[248:251], a[200:203], v[88:91]\n\t"
"v_fma_f32 v95, v97, s52, v195\n\t"
"v_fma_f32 v194, -v194, s66, v96\n\t"
"v_fma_f32 v195, -v195, s66, v97\n\t"
"v_mfma_f32_16x16x32_bf16 v[92:95], v[240:243], a[196:199], v[92:95]\n\t"
"s_waitcnt lgkmcnt(2)\n\t"
"ds_read_b128 a[196:199], v244 offset:12800\n\t"
"v_mfma_f32_16x16x32_bf16 v[92:95], a[208:211], a[200:203], v[92:95]\n\t"
"buffer_load_dwordx4 v[96:99], v252, s[8:11], s20 offen offset:0 sc1\n\t"
"v_mfma_f32_16x16x32_bf16 v[92:95], v[248:251], a[204:207], v[92:95]\n\t"
"buffer_load_dwordx4 v[240:243], v252, s[8:11], s20 offen offset:1024 sc1\n\t"
"v_mfma_f32_16x16x32_bf16 v[224:227], v[224:227], v[220:223], 0\n\t"
"s_waitcnt vmcnt(11)\n\t"
"ds_read_b128 a[200:203], v244 offset:13312\n\t"
"v_mfma_f32_16x16x32_bf16 v[100:103], a[212:215], a[168:171], v[100:103]\n\t"
"ds_read_b128 a[204:207], v244 offset:13824\n\t"
"v_mfma_f32_16x16x32_bf16 v[100:103], a[216:219], a[172:175], v[100:103]\n\t"
"buffer_load_dwordx4 a[208:211], v252, s[8:11], s20 offen offset:2048 sc1\n\t"
"v_mfma_f32_16x16x32_bf16 v[100:103], a[220:223], a[176:179], v[100:103]\n\t"
"s_mov_b32 s20, 356352\n\t"
"v_mfma_f32_16x16x32_bf16 v[104:107], a[212:215], a[172:175], v[104:107]\n\t"
"buffer_load_dwordx4 v[248:251], v252, s[8:11], s20 offen offset:0 sc1\n\t"
"v_mfma_f32_16x16x32_bf16 v[104:107], a[216:219], a[176:179], v[104:107]\n\t"
"v_cvt_pk_bf16_f32 v224, v224, v225\n\t"
"v_mfma_f32_16x16x32_bf16 v[104:107], a[220:223], a[180:183], v[104:107]\n\t"
"s_waitcnt lgkmcnt(0)\n\t"
"s_barrier\n\t"
"v_mfma_f32_16x16x32_bf16 v[108:111], a[212:215], a[176:179], v[108:111]\n\t"
"v_cvt_pk_bf16_f32 v225, v226, v227\n\t"
"v_mfma_f32_16x16x32_bf16 v[108:111], a[216:219], a[180:183], v[108:111]\n\t"
"v_mfma_f32_16x16x32_bf16 v[108:111], a[220:223], a[184:187], v[108:111]\n\t"
"ds_write2st64_b64 v245, v[228:229], v[230:231] offset0:0 offset1:1\n\t"
"v_mfma_f32_16x16x32_bf16 v[112:115], a[212:215], a[180:183], v[112:115]\n\t"
"v_mfma_f32_16x16x32_bf16 v[112:115], a[216:219], a[184:187], v[112:115]\n\t"
"ds_read_b64_tr_b16 v[228:229], v246 offset:32768\n\t"
"v_mfma_f32_16x16x32_bf16 v[112:115], a[220:223], a[188:191], v[112:115]\n\t"
"ds_write2st64_b64 v245, v[232:233], v[234:235] offset0:2 offset1:3\n\t"
"v_mfma_f32_16x16x32_bf16 v[116:119], a[212:215], a[184:187], v[116:119]\n\t"
"v_mfma_f32_16x16x32_bf16 v[116:119], a[216:219], a[188:191], v[116:119]\n\t"
"ds_read_b64_tr_b16 v[230:231], v246 offset:35840\n\t"
"v_mfma_f32_16x16x32_bf16 v[116:119], a[220:223], a[192:195], v[116:119]\n\t"
"s_add_u32 m0, s24, 73088\n\t"
"s_add_u32 s28, s21, 2162688\n\t"
"ds_write_b64 v245, v[224:225] offset:2048\n\t"
"v_mfma_f32_16x16x32_bf16 v[120:123], a[212:215], a[188:191], v[120:123]\n\t"
"ds_read_b64_tr_b16 v[232:233], v246 offset:40448\n\t"
"v_mfma_f32_16x16x32_bf16 v[120:123], a[216:219], a[192:195], v[120:123]\n\t"
"ds_read_b64_tr_b16 v[234:235], v246 offset:43520\n\t"
"v_mfma_f32_16x16x32_bf16 v[120:123], a[220:223], a[196:199], v[120:123]\n\t"
"s_waitcnt lgkmcnt(3)\n\t"
"ds_read_b64_tr_b16 v[224:225], v246 offset:46592\n\t"
"v_mfma_f32_16x16x32_bf16 v[228:231], v[228:231], v[220:223], 0\n\t"
"ds_read_b64_tr_b16 v[226:227], v246 offset:49664\n\t"
"v_mfma_f32_16x16x32_bf16 v[124:127], a[212:215], a[192:195], v[124:127]\n\t"
"buffer_load_dwordx4 v247, s[12:15], s28 offen offset:128 nt lds\n\t"
"v_mfma_f32_16x16x32_bf16 v[124:127], a[216:219], a[196:199], v[124:127]\n\t"
"s_add_u32 m0, s24, 80768\n\t"
"s_add_u32 s28, s21, 2359296\n\t"
"v_mfma_f32_16x16x32_bf16 v[124:127], a[220:223], a[200:203], v[124:127]\n\t"
"s_waitcnt lgkmcnt(2)\n\t"
"buffer_load_dwordx4 v247, s[12:15], s28 offen offset:128 nt lds\n\t"
"s_add_u32 m0, s24, 27008\n\t"
"s_add_u32 s28, s21, 786432\n\t"
"v_mfma_f32_16x16x32_bf16 v[232:235], v[232:235], v[220:223], 0\n\t"
"buffer_load_dwordx4 v247, s[12:15], s28 offen offset:128 nt lds\n\t"
"v_mfma_f32_16x16x32_bf16 v[128:131], a[212:215], a[196:199], v[128:131]\n\t"
"s_waitcnt lgkmcnt(0)\n\t"
"s_add_u32 m0, s24, 86912\n\t"
"s_add_u32 s28, s21, 2555904\n\t"
"v_mfma_f32_16x16x32_bf16 v[128:131], a[216:219], a[200:203], v[128:131]\n\t"
"buffer_load_dwordx4 v247, s[12:15], s28 offen offset:128 nt lds\n\t"
"v_mfma_f32_16x16x32_bf16 v[128:131], a[220:223], a[204:207], v[128:131]\n\t"
"s_waitcnt vmcnt(14)\n\t"
"s_add_u32 m0, s24, 33152\n\t"
"s_add_u32 s28, s21, 983040\n\t"
"v_mfma_f32_16x16x32_bf16 v[224:227], v[224:227], v[220:223], 0\n\t"
"buffer_load_dwordx4 v247, s[12:15], s28 offen offset:128 nt lds\n\t"
"v_cvt_pk_bf16_f32 v228, v228, v229\n\t"
"v_cvt_pk_bf16_f32 v229, v230, v231\n\t"
"v_mfma_f32_16x16x32_bf16 v[132:135], a[224:227], a[168:171], v[132:135]\n\t"
"s_add_u32 m0, s24, 40832\n\t"
"s_add_u32 s28, s21, 1179648\n\t"
"v_cvt_pk_bf16_f32 v230, v232, v233\n\t"
"v_mfma_f32_16x16x32_bf16 v[132:135], a[228:231], a[172:175], v[132:135]\n\t"
"v_cvt_pk_bf16_f32 v231, v234, v235\n\t"
"v_mfma_f32_16x16x32_bf16 v[132:135], a[232:235], a[176:179], v[132:135]\n\t"
"buffer_load_dwordx4 v247, s[12:15], s28 offen offset:128 nt lds\n\t"
"v_mfma_f32_16x16x32_bf16 v[136:139], a[224:227], a[172:175], v[136:139]\n\t"
"ds_read_b64_tr_b16 v[232:233], v246 offset:52736\n\t"
"v_mfma_f32_16x16x32_bf16 v[136:139], a[228:231], a[176:179], v[136:139]\n\t"
"ds_read_b64_tr_b16 v[234:235], v246 offset:55808\n\t"
"v_cvt_pk_bf16_f32 v224, v224, v225\n\t"
"v_mfma_f32_16x16x32_bf16 v[136:139], a[232:235], a[180:183], v[136:139]\n\t"
"ds_write2st64_b64 v245, v[228:229], v[230:231] offset0:5 offset1:6\n\t"
"v_mfma_f32_16x16x32_bf16 v[140:143], a[224:227], a[176:179], v[140:143]\n\t"
"v_cvt_pk_bf16_f32 v225, v226, v227\n\t"
"v_mfma_f32_16x16x32_bf16 v[140:143], a[228:231], a[180:183], v[140:143]\n\t"
"v_mfma_f32_16x16x32_bf16 v[140:143], a[232:235], a[184:187], v[140:143]\n\t"
"v_mfma_f32_16x16x32_bf16 v[144:147], a[224:227], a[180:183], v[144:147]\n\t"
"v_mfma_f32_16x16x32_bf16 v[144:147], a[228:231], a[184:187], v[144:147]\n\t"
"v_mfma_f32_16x16x32_bf16 v[144:147], a[232:235], a[188:191], v[144:147]\n\t"
"v_mfma_f32_16x16x32_bf16 v[148:151], a[224:227], a[184:187], v[148:151]\n\t"
"s_waitcnt lgkmcnt(1)\n\t"
"v_mfma_f32_16x16x32_bf16 v[148:151], a[228:231], a[188:191], v[148:151]\n\t"
"v_mfma_f32_16x16x32_bf16 v[148:151], a[232:235], a[192:195], v[148:151]\n\t"
"v_mfma_f32_16x16x32_bf16 v[232:235], v[232:235], v[220:223], 0\n\t"
"v_mfma_f32_16x16x32_bf16 v[152:155], a[224:227], a[188:191], v[152:155]\n\t"
"v_mfma_f32_16x16x32_bf16 v[152:155], a[228:231], a[192:195], v[152:155]\n\t"
"v_mfma_f32_16x16x32_bf16 v[152:155], a[232:235], a[196:199], v[152:155]\n\t"
"v_mfma_f32_16x16x32_bf16 v[156:159], a[224:227], a[192:195], v[156:159]\n\t"
"v_mfma_f32_16x16x32_bf16 v[156:159], a[228:231], a[196:199], v[156:159]\n\t"
"v_cvt_pk_bf16_f32 v226, v232, v233\n\t"
"v_cvt_pk_bf16_f32 v227, v234, v235\n\t"
"v_mfma_f32_16x16x32_bf16 v[156:159], a[232:235], a[200:203], v[156:159]\n\t"
"ds_write2st64_b64 v245, v[224:225], v[226:227] offset0:7 offset1:8\n\t"
"v_mfma_f32_16x16x32_bf16 v[160:163], a[224:227], a[196:199], v[160:163]\n\t"
"s_waitcnt vmcnt(13)\n\t"
"v_mfma_f32_16x16x32_bf16 v[160:163], a[228:231], a[200:203], v[160:163]\n\t"
"v_mfma_f32_16x16x32_bf16 v[160:163], a[232:235], a[204:207], v[160:163]\n\t"
"v_mfma_f32_16x16x32_bf16 v[164:167], a[236:239], a[168:171], v[164:167]\n\t"
"v_mfma_f32_16x16x32_bf16 v[164:167], a[240:243], a[172:175], v[164:167]\n\t"
"v_mfma_f32_16x16x32_bf16 v[164:167], a[244:247], a[176:179], v[164:167]\n\t"
"s_waitcnt lgkmcnt(0)\n\t"
"s_barrier\n\t"
"v_mfma_f32_16x16x32_bf16 v[168:171], a[236:239], a[172:175], v[168:171]\n\t"
"ds_read_b128 a[168:171], v244 offset:0\n\t"
"v_mfma_f32_16x16x32_bf16 v[168:171], a[240:243], a[176:179], v[168:171]\n\t"
"buffer_load_dwordx4 a[212:215], v252, s[8:11], s20 offen offset:1024 sc1\n\t"
"v_mfma_f32_16x16x32_bf16 v[168:171], a[244:247], a[180:183], v[168:171]\n\t"
"ds_read_b128 a[172:175], v244 offset:512\n\t"
"v_mfma_f32_16x16x32_bf16 v[172:175], a[236:239], a[176:179], v[172:175]\n\t"
"ds_read_b128 a[176:179], v244 offset:1024\n\t"
"v_mfma_f32_16x16x32_bf16 v[172:175], a[240:243], a[180:183], v[172:175]\n\t"
"v_mfma_f32_16x16x32_bf16 v[172:175], a[244:247], a[184:187], v[172:175]\n\t"
"v_mfma_f32_16x16x32_bf16 v[176:179], a[236:239], a[180:183], v[176:179]\n\t"
"ds_read_b128 a[180:183], v244 offset:1536\n\t"
"v_mfma_f32_16x16x32_bf16 v[176:179], a[240:243], a[184:187], v[176:179]\n\t"
"v_mfma_f32_16x16x32_bf16 v[176:179], a[244:247], a[188:191], v[176:179]\n\t"
"v_mfma_f32_16x16x32_bf16 v[180:183], a[236:239], a[184:187], v[180:183]\n\t"
"ds_read_b128 a[184:187], v244 offset:2048\n\t"
"v_mfma_f32_16x16x32_bf16 v[180:183], a[240:243], a[188:191], v[180:183]\n\t"
"v_mfma_f32_16x16x32_bf16 v[180:183], a[244:247], a[192:195], v[180:183]\n\t"
"v_mfma_f32_16x16x32_bf16 v[184:187], a[236:239], a[188:191], v[184:187]\n\t"
"ds_read_b128 a[188:191], v244 offset:2560\n\t"
"v_mfma_f32_16x16x32_bf16 v[184:187], a[240:243], a[192:195], v[184:187]\n\t"
"v_mfma_f32_16x16x32_bf16 v[184:187], a[244:247], a[196:199], v[184:187]\n\t"
"v_mfma_f32_16x16x32_bf16 v[188:191], a[236:239], a[192:195], v[188:191]\n\t"
"ds_read_b128 a[192:195], v244 offset:3072\n\t"
"v_mfma_f32_16x16x32_bf16 v[188:191], a[240:243], a[196:199], v[188:191]\n\t"
"v_mfma_f32_16x16x32_bf16 v[188:191], a[244:247], a[200:203], v[188:191]\n\t"
"v_mfma_f32_16x16x32_bf16 v[192:195], a[236:239], a[196:199], v[192:195]\n\t"
"s_waitcnt vmcnt(11) lgkmcnt(4)\n\t"
"ds_read_b128 a[196:199], v244 offset:3584\n\t"
"v_mfma_f32_16x16x32_bf16 v[192:195], a[240:243], a[200:203], v[192:195]\n\t"
"ds_read_b128 a[200:203], v244 offset:4096\n\t"
"v_mfma_f32_16x16x32_bf16 v[192:195], a[244:247], a[204:207], v[192:195]\n\t"
"ds_read_b128 a[204:207], v244 offset:4608\n\t"
"Lst4_%=:\n\t"
"v_mfma_f32_16x16x32_bf16 v[0:3], a[248:251], a[168:171], v[0:3]\n\t"
"s_waitcnt lgkmcnt(6)\n\t"
"v_xor_b32 v246, 0xc0, v246\n\t"
"v_mfma_f32_16x16x32_bf16 v[0:3], v[236:239], a[172:175], v[0:3]\n\t"
"v_mfma_f32_16x16x32_bf16 v[0:3], a[252:255], a[176:179], v[0:3]\n\t"
"v_mfma_f32_16x16x32_bf16 v[4:7], a[248:251], a[172:175], v[4:7]\n\t"
"s_waitcnt lgkmcnt(5)\n\t"
"v_mfma_f32_16x16x32_bf16 v[4:7], v[236:239], a[176:179], v[4:7]\n\t"
"v_mfma_f32_16x16x32_bf16 v[4:7], a[252:255], a[180:183], v[4:7]\n\t"
"v_mfma_f32_16x16x32_bf16 v[8:11], a[248:251], a[176:179], v[8:11]\n\t"
"s_waitcnt lgkmcnt(4)\n\t"
"v_mfma_f32_16x16x32_bf16 v[8:11], v[236:239], a[180:183], v[8:11]\n\t"
"v_mfma_f32_16x16x32_bf16 v[8:11], a[252:255], a[184:187], v[8:11]\n\t"
"v_mfma_f32_16x16x32_bf16 v[12:15], a[248:251], a[180:183], v[12:15]\n\t"
"s_waitcnt lgkmcnt(3)\n\t"
"v_mfma_f32_16x16x32_bf16 v[12:15], v[236:239], a[184:187], v[12:15]\n\t"
"v_mfma_f32_16x16x32_bf16 v[12:15], a[252:255], a[188:191], v[12:15]\n\t"
"buffer_load_dwordx4 a[216:219], v252, s[8:11], s20 offen offset:2048 sc1\n\t"
"v_mfma_f32_16x16x32_bf16 v[16:19], a[248:251], a[184:187], v[16:19]\n\t"
"s_waitcnt lgkmcnt(2)\n\t"
"s_mov_b32 s20, 368640\n\t"
"v_mfma_f32_16x16x32_bf16 v[16:19], v[236:239], a[188:191], v[16:19]\n\t"
"v_mfma_f32_16x16x32_bf16 v[16:19], a[252:255], a[192:195], v[16:19]\n\t"
"ds_read_b64_tr_b16 v[228:229], v246 offset:512\n\t"
"v_mfma_f32_16x16x32_bf16 v[20:23], a[248:251], a[188:191], v[20:23]\n\t"
"buffer_load_dwordx4 a[220:223], v252, s[8:11], s20 offen offset:0 sc1\n\t"
"v_mfma_f32_16x16x32_bf16 v[20:23], v[236:239], a[192:195], v[20:23]\n\t"
"s_waitcnt lgkmcnt(2)\n\t"
"ds_read_b64_tr_b16 v[230:231], v246 offset:3584\n\t"
"v_mfma_f32_16x16x32_bf16 v[20:23], a[252:255], a[196:199], v[20:23]\n\t"
"v_mfma_f32_16x16x32_bf16 v[24:27], a[248:251], a[192:195], v[24:27]\n\t"
"buffer_load_dwordx4 a[224:227], v252, s[8:11], s20 offen offset:1024 sc1\n\t"
"v_mfma_f32_16x16x32_bf16 v[24:27], v[236:239], a[196:199], v[24:27]\n\t"
"s_waitcnt lgkmcnt(2)\n\t"
"v_mfma_f32_16x16x32_bf16 v[24:27], a[252:255], a[200:203], v[24:27]\n\t"
"v_mfma_f32_16x16x32_bf16 v[28:31], a[248:251], a[196:199], v[28:31]\n\t"
"s_waitcnt vmcnt(11)\n\t"
"ds_read_b64_tr_b16 v[232:233], v246 offset:6656\n\t"
"v_mfma_f32_16x16x32_bf16 v[28:31], v[236:239], a[200:203], v[28:31]\n\t"
"buffer_load_dwordx4 v[236:239], v252, s[8:11], s20 offen offset:2048 sc1\n\t"
"v_mfma_f32_16x16x32_bf16 v[28:31], a[252:255], a[204:207], v[28:31]\n\t"
"s_mov_b32 s20, 380928\n\t"
"ds_read_b64_tr_b16 v[234:235], v246 offset:9728\n\t"
"v_mfma_f32_16x16x32_bf16 v[32:35], v[96:99], a[168:171], v[32:35]\n\t"
"v_mfma_f32_16x16x32_bf16 v[32:35], v[240:243], a[172:175], v[32:35]\n\t"
"v_mfma_f32_16x16x32_bf16 v[32:35], a[208:211], a[176:179], v[32:35]\n\t"
"buffer_load_dwordx4 a[228:231], v252, s[8:11], s20 offen offset:0 sc1\n\t"
"v_mfma_f32_16x16x32_bf16 v[36:39], v[96:99], a[172:175], v[36:39]\n\t"
"s_waitcnt lgkmcnt(2)\n\t"
"v_mfma_f32_16x16x32_bf16 v[36:39], v[240:243], a[176:179], v[36:39]\n\t"
"v_mfma_f32_16x16x32_bf16 v[36:39], a[208:211], a[180:183], v[36:39]\n\t"
"v_mfma_f32_16x16x32_bf16 v[228:231], v[228:231], v[220:223], 0\n\t"
"buffer_load_dwordx4 a[232:235], v252, s[8:11], s20 offen offset:1024 sc1\n\t"
"v_mfma_f32_16x16x32_bf16 v[40:43], v[96:99], a[176:179], v[40:43]\n\t"
"ds_read_b64_tr_b16 v[224:225], v246 offset:12800\n\t"
"v_mfma_f32_16x16x32_bf16 v[40:43], v[240:243], a[180:183], v[40:43]\n\t"
"ds_read_b64_tr_b16 v[226:227], v246 offset:15872\n\t"
"v_mfma_f32_16x16x32_bf16 v[40:43], a[208:211], a[184:187], v[40:43]\n\t"
"v_mfma_f32_16x16x32_bf16 v[44:47], v[96:99], a[180:183], v[44:47]\n\t"
"buffer_load_dwordx4 a[236:239], v252, s[8:11], s20 offen offset:2048 sc1\n\t"
"v_mfma_f32_16x16x32_bf16 v[44:47], v[240:243], a[184:187], v[44:47]\n\t"
"s_waitcnt lgkmcnt(2)\n\t"
"v_cvt_pk_bf16_f32 v228, v228, v229\n\t"
"v_cvt_pk_bf16_f32 v229, v230, v231\n\t"
"v_mfma_f32_16x16x32_bf16 v[44:47], a[208:211], a[188:191], v[44:47]\n\t"
"s_mov_b32 s20, 393216\n\t"
"v_mfma_f32_16x16x32_bf16 v[232:235], v[232:235], v[220:223], 0\n\t"
"s_waitcnt lgkmcnt(0)\n\t"
"v_mfma_f32_16x16x32_bf16 v[48:51], v[96:99], a[184:187], v[48:51]\n\t"
"Lpg4_%=:\n\t"
"s_cbranch_vccnz Lpg8_%=\n\t"
"v_mfma_f32_16x16x32_bf16 v[48:51], v[240:243], a[188:191], v[48:51]\n\t"
"buffer_load_dwordx4 a[240:243], v252, s[8:11], s20 offen offset:0 sc1\n\t"
"v_mfma_f32_16x16x32_bf16 v[48:51], a[208:211], a[192:195], v[48:51]\n\t"
"v_mfma_f32_16x16x32_bf16 v[52:55], v[96:99], a[188:191], v[52:55]\n\t"
"v_mfma_f32_16x16x32_bf16 v[52:55], v[240:243], a[192:195], v[52:55]\n\t"
"buffer_load_dwordx4 a[244:247], v252, s[8:11], s20 offen offset:1024 sc1\n\t"
"v_mfma_f32_16x16x32_bf16 v[52:55], a[208:211], a[196:199], v[52:55]\n\t"
"v_cvt_pk_bf16_f32 v230, v232, v233\n\t"
"v_cvt_pk_bf16_f32 v231, v234, v235\n\t"
"v_mfma_f32_16x16x32_bf16 v[56:59], v[96:99], a[192:195], v[56:59]\n\t"
"ds_read_b64_tr_b16 v[232:233], v246 offset:20480\n\t"
"v_mfma_f32_16x16x32_bf16 v[56:59], v[240:243], a[196:199], v[56:59]\n\t"
"ds_read_b64_tr_b16 v[234:235], v246 offset:23552\n\t"
"v_mfma_f32_16x16x32_bf16 v[56:59], a[208:211], a[200:203], v[56:59]\n\t"
"buffer_load_dwordx4 a[248:251], v252, s[8:11], s20 offen offset:2048 sc1\n\t"
"v_mfma_f32_16x16x32_bf16 v[224:227], v[224:227], v[220:223], 0\n\t"
"s_mov_b32 s20, 405504\n\t"
"v_mfma_f32_16x16x32_bf16 v[60:63], v[96:99], a[196:199], v[60:63]\n\t"
"s_waitcnt vmcnt(9)\n\t"
"v_mfma_f32_16x16x32_bf16 v[60:63], v[240:243], a[200:203], v[60:63]\n\t"
"v_mfma_f32_16x16x32_bf16 v[60:63], a[208:211], a[204:207], v[60:63]\n\t"
"buffer_load_dwordx4 v[240:243], v252, s[8:11], s20 offen offset:0 sc1\n\t"
"v_mfma_f32_16x16x32_bf16 v[64:67], v[248:251], a[168:171], v[64:67]\n\t"
"ds_read_b128 a[168:171], v244 offset:9216\n\t"
"v_mfma_f32_16x16x32_bf16 v[64:67], a[212:215], a[172:175], v[64:67]\n\t"
"v_cvt_pk_bf16_f32 v224, v224, v225\n\t"
"v_cvt_pk_bf16_f32 v225, v226, v227\n\t"
"v_mfma_f32_16x16x32_bf16 v[64:67], a[216:219], a[176:179], v[64:67]\n\t"
"v_mfma_f32_16x16x32_bf16 v[68:71], v[248:251], a[172:175], v[68:71]\n\t"
"buffer_load_dwordx4 a[208:211], v252, s[8:11], s20 offen offset:1024 sc1\n\t"
"v_mfma_f32_16x16x32_bf16 v[68:71], a[212:215], a[176:179], v[68:71]\n\t"
"s_waitcnt lgkmcnt(1)\n\t"
"ds_read_b128 a[172:175], v244 offset:9728\n\t"
"v_mfma_f32_16x16x32_bf16 v[68:71], a[216:219], a[180:183], v[68:71]\n\t"
"buffer_load_dwordx4 v[96:99], v252, s[8:11], s20 offen offset:2048 sc1\n\t"
"v_mfma_f32_16x16x32_bf16 v[232:235], v[232:235], v[220:223], 0\n\t"
"s_mov_b32 s20, 417792\n\t"
"v_mfma_f32_16x16x32_bf16 v[72:75], v[248:251], a[176:179], v[72:75]\n\t"
"ds_read_b128 a[176:179], v244 offset:10240\n\t"
"v_mfma_f32_16x16x32_bf16 v[72:75], a[212:215], a[180:183], v[72:75]\n\t"
"buffer_load_dwordx4 a[252:255], v252, s[8:11], s20 offen offset:0 sc1\n\t"
"v_mfma_f32_16x16x32_bf16 v[72:75], a[216:219], a[184:187], v[72:75]\n\t"
"v_mfma_f32_16x16x32_bf16 v[76:79], v[248:251], a[180:183], v[76:79]\n\t"
"ds_read_b128 a[180:183], v244 offset:10752\n\t"
"v_mfma_f32_16x16x32_bf16 v[76:79], a[212:215], a[184:187], v[76:79]\n\t"
"v_cvt_pk_bf16_f32 v226, v232, v233\n\t"
"v_cvt_pk_bf16_f32 v227, v234, v235\n\t"
"v_mfma_f32_16x16x32_bf16 v[76:79], a[216:219], a[188:191], v[76:79]\n\t"
"ds_read_b64_tr_b16 v[232:233], v246 offset:26624\n\t"
"v_mfma_f32_16x16x32_bf16 v[80:83], v[248:251], a[184:187], v[80:83]\n\t"
"ds_read_b64_tr_b16 v[234:235], v246 offset:29696\n\t"
"v_mfma_f32_16x16x32_bf16 v[80:83], a[212:215], a[188:191], v[80:83]\n\t"
"ds_read_b128 a[184:187], v244 offset:11264\n\t"
"v_mfma_f32_16x16x32_bf16 v[80:83], a[216:219], a[192:195], v[80:83]\n\t"
"v_mfma_f32_16x16x32_bf16 v[84:87], v[248:251], a[188:191], v[84:87]\n\t"
"ds_read_b128 a[188:191], v244 offset:11776\n\t"
"v_mfma_f32_16x16x32_bf16 v[84:87], a[212:215], a[192:195], v[84:87]\n\t"
"v_mfma_f32_16x16x32_bf16 v[84:87], a[216:219], a[196:199], v[84:87]\n\t"
"v_mfma_f32_16x16x32_bf16 v[88:91], v[248:251], a[192:195], v[88:91]\n\t"
"ds_read_b128 a[192:195], v244 offset:12288\n\t"
"v_mfma_f32_16x16x32_bf16 v[88:91], a[212:215], a[196:199], v[88:91]\n\t"
"v_mfma_f32_16x16x32_bf16 v[88:91], a[216:219], a[200:203], v[88:91]\n\t"
"v_mfma_f32_16x16x32_bf16 v[92:95], v[248:251], a[196:199], v[92:95]\n\t"
"buffer_load_dwordx4 v[248:251], v252, s[8:11], s20 offen offset:1024 sc1\n\t"
"v_mfma_f32_16x16x32_bf16 v[92:95], a[212:215], a[200:203], v[92:95]\n\t"
"s_waitcnt vmcnt(11) lgkmcnt(3)\n\t"
"ds_read_b128 a[196:199], v244 offset:12800\n\t"
"v_mfma_f32_16x16x32_bf16 v[92:95], a[216:219], a[204:207], v[92:95]\n\t"
"ds_read_b128 a[200:203], v244 offset:13312\n\t"
"v_mfma_f32_16x16x32_bf16 v[232:235], v[232:235], v[220:223], 0\n\t"
"ds_read_b128 a[204:207], v244 offset:13824\n\t"
"v_mfma_f32_16x16x32_bf16 v[100:103], a[220:223], a[168:171], v[100:103]\n\t"
"buffer_load_dwordx4 a[212:215], v252, s[8:11], s20 offen offset:2048 sc1\n\t"
"v_mfma_f32_16x16x32_bf16 v[100:103], a[224:227], a[172:175], v[100:103]\n\t"
"s_mov_b32 s20, 430080\n\t"
"v_mfma_f32_16x16x32_bf16 v[100:103], v[236:239], a[176:179], v[100:103]\n\t"
"buffer_load_dwordx4 a[216:219], v252, s[8:11], s20 offen offset:0 sc1\n\t"
"v_mfma_f32_16x16x32_bf16 v[104:107], a[220:223], a[172:175], v[104:107]\n\t"
"s_waitcnt lgkmcnt(5)\n\t"
"v_mfma_f32_16x16x32_bf16 v[104:107], a[224:227], a[176:179], v[104:107]\n\t"
"v_cvt_pk_bf16_f32 v232, v232, v233\n\t"
"v_mfma_f32_16x16x32_bf16 v[104:107], v[236:239], a[180:183], v[104:107]\n\t"
"s_waitcnt lgkmcnt(0)\n\t"
"s_barrier\n\t"
"v_mfma_f32_16x16x32_bf16 v[108:111], a[220:223], a[176:179], v[108:111]\n\t"
"v_cvt_pk_bf16_f32 v233, v234, v235\n\t"
"v_mfma_f32_16x16x32_bf16 v[108:111], a[224:227], a[180:183], v[108:111]\n\t"
"v_mfma_f32_16x16x32_bf16 v[108:111], v[236:239], a[184:187], v[108:111]\n\t"
"ds_write2st64_b64 v245, v[228:229], v[230:231] offset0:0 offset1:1\n\t"
"v_mfma_f32_16x16x32_bf16 v[112:115], a[220:223], a[180:183], v[112:115]\n\t"
"v_mfma_f32_16x16x32_bf16 v[112:115], a[224:227], a[184:187], v[112:115]\n\t"
"ds_read_b64_tr_b16 v[228:229], v246 offset:32768\n\t"
"v_mfma_f32_16x16x32_bf16 v[112:115], v[236:239], a[188:191], v[112:115]\n\t"
"ds_write2st64_b64 v245, v[224:225], v[226:227] offset0:2 offset1:3\n\t"
"v_mfma_f32_16x16x32_bf16 v[116:119], a[220:223], a[184:187], v[116:119]\n\t"
"v_mfma_f32_16x16x32_bf16 v[116:119], a[224:227], a[188:191], v[116:119]\n\t"
"ds_read_b64_tr_b16 v[230:231], v246 offset:35840\n\t"
"v_mfma_f32_16x16x32_bf16 v[116:119], v[236:239], a[192:195], v[116:119]\n\t"
"ds_write_b64 v245, v[232:233] offset:2048\n\t"
"v_mfma_f32_16x16x32_bf16 v[120:123], a[220:223], a[188:191], v[120:123]\n\t"
"ds_read_b64_tr_b16 v[224:225], v246 offset:40448\n\t"
"v_mfma_f32_16x16x32_bf16 v[120:123], a[224:227], a[192:195], v[120:123]\n\t"
"ds_read_b64_tr_b16 v[226:227], v246 offset:43520\n\t"
"v_mfma_f32_16x16x32_bf16 v[120:123], v[236:239], a[196:199], v[120:123]\n\t"
"s_waitcnt lgkmcnt(3)\n\t"
"ds_read_b64_tr_b16 v[232:233], v246 offset:46592\n\t"
"v_mfma_f32_16x16x32_bf16 v[228:231], v[228:231], v[220:223], 0\n\t"
"ds_read_b64_tr_b16 v[234:235], v246 offset:49664\n\t"
"v_mfma_f32_16x16x32_bf16 v[124:127], a[220:223], a[192:195], v[124:127]\n\t"
"v_mfma_f32_16x16x32_bf16 v[124:127], a[224:227], a[196:199], v[124:127]\n\t"
"v_mfma_f32_16x16x32_bf16 v[124:127], v[236:239], a[200:203], v[124:127]\n\t"
"s_waitcnt lgkmcnt(2)\n\t"
"v_mfma_f32_16x16x32_bf16 v[224:227], v[224:227], v[220:223], 0\n\t"
"v_mfma_f32_16x16x32_bf16 v[128:131], a[220:223], a[196:199], v[128:131]\n\t"
"buffer_load_dwordx4 a[220:223], v252, s[8:11], s20 offen offset:1024 sc1\n\t"
"v_mfma_f32_16x16x32_bf16 v[128:131], a[224:227], a[200:203], v[128:131]\n\t"
"s_waitcnt vmcnt(11) lgkmcnt(0)\n\t"
"v_cvt_pk_bf16_f32 v228, v228, v229\n\t"
"v_cvt_pk_bf16_f32 v229, v230, v231\n\t"
"v_mfma_f32_16x16x32_bf16 v[128:131], v[236:239], a[204:207], v[128:131]\n\t"
"v_mfma_f32_16x16x32_bf16 v[232:235], v[232:235], v[220:223], 0\n\t"
"buffer_load_dwordx4 v[236:239], v252, s[8:11], s20 offen offset:2048 sc1\n\t"
"v_mfma_f32_16x16x32_bf16 v[132:135], a[228:231], a[168:171], v[132:135]\n\t"
"s_mov_b32 s20, 442368\n\t"
"v_cvt_pk_bf16_f32 v230, v224, v225\n\t"
"v_cvt_pk_bf16_f32 v231, v226, v227\n\t"
"v_mfma_f32_16x16x32_bf16 v[132:135], a[232:235], a[172:175], v[132:135]\n\t"
"s_add_u32 m0, s24, 46976\n\t"
"s_add_u32 s28, s21, 1376256\n\t"
"ds_read_b64_tr_b16 v[224:225], v246 offset:52736\n\t"
"v_mfma_f32_16x16x32_bf16 v[132:135], a[236:239], a[176:179], v[132:135]\n\t"
"buffer_load_dwordx4 a[224:227], v252, s[8:11], s20 offen offset:0 sc1\n\t"
"v_mfma_f32_16x16x32_bf16 v[136:139], a[228:231], a[172:175], v[136:139]\n\t"
"buffer_load_dwordx4 v247, s[12:15], s28 offen offset:128 nt lds\n\t"
"v_mfma_f32_16x16x32_bf16 v[136:139], a[232:235], a[176:179], v[136:139]\n\t"
"s_add_u32 m0, s24, 53120\n\t"
"s_add_u32 s28, s21, 1572864\n\t"
"ds_read_b64_tr_b16 v[226:227], v246 offset:55808\n\t"
"v_mfma_f32_16x16x32_bf16 v[136:139], a[236:239], a[180:183], v[136:139]\n\t"
"buffer_load_dwordx4 v247, s[12:15], s28 offen offset:128 nt lds\n\t"
"v_mfma_f32_16x16x32_bf16 v[140:143], a[228:231], a[176:179], v[140:143]\n\t"
"s_add_u32 m0, s24, 93056\n\t"
"s_add_u32 s28, s21, 2752512\n\t"
"v_cvt_pk_bf16_f32 v232, v232, v233\n\t"
"v_mfma_f32_16x16x32_bf16 v[140:143], a[232:235], a[180:183], v[140:143]\n\t"
"v_cvt_pk_bf16_f32 v233, v234, v235\n\t"
"v_mfma_f32_16x16x32_bf16 v[140:143], a[236:239], a[184:187], v[140:143]\n\t"
"ds_write2st64_b64 v245, v[228:229], v[230:231] offset0:5 offset1:6\n\t"
"v_mfma_f32_16x16x32_bf16 v[144:147], a[228:231], a[180:183], v[144:147]\n\t"
"buffer_load_dwordx4 v247, s[12:15], s28 offen offset:128 nt lds\n\t"
"v_mfma_f32_16x16x32_bf16 v[144:147], a[232:235], a[184:187], v[144:147]\n\t"
"s_add_u32 m0, s24, 100736\n\t"
"s_add_u32 s28, s21, 2949120\n\t"
"v_mfma_f32_16x16x32_bf16 v[144:147], a[236:239], a[188:191], v[144:147]\n\t"
"buffer_load_dwordx4 v247, s[12:15], s28 offen offset:128 nt lds\n\t"
"s_add_u32 m0, s24, 106880\n\t"
"s_add_u32 s28, s21, 3145728\n\t"
"v_mfma_f32_16x16x32_bf16 v[148:151], a[228:231], a[184:187], v[148:151]\n\t"
"buffer_load_dwordx4 v247, s[12:15], s28 offen offset:128 nt lds\n\t"
"v_mfma_f32_16x16x32_bf16 v[148:151], a[232:235], a[188:191], v[148:151]\n\t"
"s_waitcnt lgkmcnt(1)\n\t"
"s_add_u32 m0, s24, 113024\n\t"
"s_add_u32 s28, s21, 3342336\n\t"
"v_mfma_f32_16x16x32_bf16 v[148:151], a[236:239], a[192:195], v[148:151]\n\t"
"buffer_load_dwordx4 v247, s[12:15], s28 offen offset:128 nt lds\n\t"
"s_add_u32 m0, s25, 896\n\t"
"v_mfma_f32_16x16x32_bf16 v[224:227], v[224:227], v[220:223], 0\n\t"
"buffer_load_dwordx4 v254, s[12:15], 0 offen offset:128 nt lds\n\t"
"v_mfma_f32_16x16x32_bf16 v[152:155], a[228:231], a[188:191], v[152:155]\n\t"
"s_add_u32 m0, s26, 896\n\t"
"v_mfma_f32_16x16x32_bf16 v[152:155], a[232:235], a[192:195], v[152:155]\n\t"
"v_mfma_f32_16x16x32_bf16 v[152:155], a[236:239], a[196:199], v[152:155]\n\t"
"s_mov_b64 exec, s[36:37]\n\t"
"buffer_load_dwordx4 v254, s[12:15], s27 offen offset:128 nt lds\n\t"
"s_mov_b64 exec, -1\n\t"
"v_mfma_f32_16x16x32_bf16 v[156:159], a[228:231], a[192:195], v[156:159]\n\t"
"v_mfma_f32_16x16x32_bf16 v[156:159], a[232:235], a[196:199], v[156:159]\n\t"
"v_cvt_pk_bf16_f32 v234, v224, v225\n\t"
"v_cvt_pk_bf16_f32 v235, v226, v227\n\t"
"v_mfma_f32_16x16x32_bf16 v[156:159], a[236:239], a[200:203], v[156:159]\n\t"
"ds_write2st64_b64 v245, v[232:233], v[234:235] offset0:7 offset1:8\n\t"
"v_mfma_f32_16x16x32_bf16 v[160:163], a[228:231], a[196:199], v[160:163]\n\t"
"s_waitcnt vmcnt(18)\n\t"
"v_mfma_f32_16x16x32_bf16 v[160:163], a[232:235], a[200:203], v[160:163]\n\t"
"v_mfma_f32_16x16x32_bf16 v[160:163], a[236:239], a[204:207], v[160:163]\n\t"
"v_mfma_f32_16x16x32_bf16 v[164:167], a[240:243], a[168:171], v[164:167]\n\t"
"v_mfma_f32_16x16x32_bf16 v[164:167], a[244:247], a[172:175], v[164:167]\n\t"
"v_mfma_f32_16x16x32_bf16 v[164:167], a[248:251], a[176:179], v[164:167]\n\t"
"s_waitcnt lgkmcnt(0)\n\t"
"s_barrier\n\t"
"v_mfma_f32_16x16x32_bf16 v[168:171], a[240:243], a[172:175], v[168:171]\n\t"
"ds_read_b128 a[168:171], v244 offset:0\n\t"
"v_mfma_f32_16x16x32_bf16 v[168:171], a[244:247], a[176:179], v[168:171]\n\t"
"ds_read_b128 a[172:175], v244 offset:512\n\t"
"v_mfma_f32_16x16x32_bf16 v[168:171], a[248:251], a[180:183], v[168:171]\n\t"
"v_mfma_f32_16x16x32_bf16 v[172:175], a[240:243], a[176:179], v[172:175]\n\t"
"ds_read_b128 a[176:179], v244 offset:1024\n\t"
"v_mfma_f32_16x16x32_bf16 v[172:175], a[244:247], a[180:183], v[172:175]\n\t"
"v_mfma_f32_16x16x32_bf16 v[172:175], a[248:251], a[184:187], v[172:175]\n\t"
"v_mfma_f32_16x16x32_bf16 v[176:179], a[240:243], a[180:183], v[176:179]\n\t"
"ds_read_b128 a[180:183], v244 offset:1536\n\t"
"v_mfma_f32_16x16x32_bf16 v[176:179], a[244:247], a[184:187], v[176:179]\n\t"
"v_mfma_f32_16x16x32_bf16 v[176:179], a[248:251], a[188:191], v[176:179]\n\t"
"v_mfma_f32_16x16x32_bf16 v[180:183], a[240:243], a[184:187], v[180:183]\n\t"
"ds_read_b128 a[184:187], v244 offset:2048\n\t"
"v_mfma_f32_16x16x32_bf16 v[180:183], a[244:247], a[188:191], v[180:183]\n\t"
"v_mfma_f32_16x16x32_bf16 v[180:183], a[248:251], a[192:195], v[180:183]\n\t"
"v_mfma_f32_16x16x32_bf16 v[184:187], a[240:243], a[188:191], v[184:187]\n\t"
"ds_read_b128 a[188:191], v244 offset:2560\n\t"
"v_mfma_f32_16x16x32_bf16 v[184:187], a[244:247], a[192:195], v[184:187]\n\t"
"v_mfma_f32_16x16x32_bf16 v[184:187], a[248:251], a[196:199], v[184:187]\n\t"
"v_mfma_f32_16x16x32_bf16 v[188:191], a[240:243], a[192:195], v[188:191]\n\t"
"ds_read_b128 a[192:195], v244 offset:3072\n\t"
"v_mfma_f32_16x16x32_bf16 v[188:191], a[244:247], a[196:199], v[188:191]\n\t"
"v_mfma_f32_16x16x32_bf16 v[188:191], a[248:251], a[200:203], v[188:191]\n\t"
"v_mfma_f32_16x16x32_bf16 v[192:195], a[240:243], a[196:199], v[192:195]\n\t"
"s_waitcnt vmcnt(15) lgkmcnt(4)\n\t"
"ds_read_b128 a[196:199], v244 offset:3584\n\t"
"v_mfma_f32_16x16x32_bf16 v[192:195], a[244:247], a[200:203], v[192:195]\n\t"
"ds_read_b128 a[200:203], v244 offset:4096\n\t"
"v_mfma_f32_16x16x32_bf16 v[192:195], a[248:251], a[204:207], v[192:195]\n\t"
"ds_read_b128 a[204:207], v244 offset:4608\n\t"
"Lst5_%=:\n\t"
"v_mfma_f32_16x16x32_bf16 v[196:199], v[240:243], a[168:171], v[196:199]\n\t"
"s_waitcnt lgkmcnt(6)\n\t"
"v_mfma_f32_16x16x32_bf16 v[196:199], a[208:211], a[172:175], v[196:199]\n\t"
"v_mfma_f32_16x16x32_bf16 v[196:199], v[96:99], a[176:179], v[196:199]\n\t"
"v_mfma_f32_16x16x32_bf16 v[204:207], v[240:243], a[172:175], v[204:207]\n\t"
"s_waitcnt lgkmcnt(5)\n\t"
"v_mfma_f32_16x16x32_bf16 v[204:207], a[208:211], a[176:179], v[204:207]\n\t"
"v_mfma_f32_16x16x32_bf16 v[204:207], v[96:99], a[180:183], v[204:207]\n\t"
"v_mfma_f32_16x16x32_bf16 v[212:215], v[240:243], a[176:179], v[212:215]\n\t"
"s_waitcnt lgkmcnt(4)\n\t"
"v_mfma_f32_16x16x32_bf16 v[212:215], a[208:211], a[180:183], v[212:215]\n\t"
"v_mfma_f32_16x16x32_bf16 v[212:215], v[96:99], a[184:187], v[212:215]\n\t"
"v_mfma_f32_16x16x32_bf16 a[0:3], v[240:243], a[180:183], a[0:3]\n\t"
"s_waitcnt lgkmcnt(3)\n\t"
"v_mfma_f32_16x16x32_bf16 a[0:3], a[208:211], a[184:187], a[0:3]\n\t"
"buffer_load_dwordx4 a[228:231], v252, s[8:11], s20 offen offset:1024 sc1\n\t"
"v_mfma_f32_16x16x32_bf16 a[0:3], v[96:99], a[188:191], a[0:3]\n\t"
"v_mfma_f32_16x16x32_bf16 a[8:11], v[240:243], a[184:187], a[8:11]\n\t"
"s_waitcnt lgkmcnt(2)\n\t"
"v_mfma_f32_16x16x32_bf16 a[8:11], a[208:211], a[188:191], a[8:11]\n\t"
"v_mfma_f32_16x16x32_bf16 a[8:11], v[96:99], a[192:195], a[8:11]\n\t"
"ds_read_b64_tr_b16 v[228:229], v246 offset:1024\n\t"
"v_mfma_f32_16x16x32_bf16 a[16:19], v[240:243], a[188:191], a[16:19]\n\t"
"s_waitcnt lgkmcnt(2)\n\t"
"ds_read_b64_tr_b16 v[230:231], v246 offset:4096\n\t"
"v_mfma_f32_16x16x32_bf16 a[16:19], a[208:211], a[192:195], a[16:19]\n\t"
"v_mfma_f32_16x16x32_bf16 a[16:19], v[96:99], a[196:199], a[16:19]\n\t"
"v_mfma_f32_16x16x32_bf16 a[24:27], v[240:243], a[192:195], a[24:27]\n\t"
"s_waitcnt lgkmcnt(2)\n\t"
"v_mfma_f32_16x16x32_bf16 a[24:27], a[208:211], a[196:199], a[24:27]\n\t"
"v_mfma_f32_16x16x32_bf16 a[24:27], v[96:99], a[200:203], a[24:27]\n\t"
"v_mfma_f32_16x16x32_bf16 a[32:35], v[240:243], a[196:199], a[32:35]\n\t"
"s_waitcnt vmcnt(13)\n\t"
"ds_read_b64_tr_b16 v[224:225], v246 offset:7168\n\t"
"v_mfma_f32_16x16x32_bf16 a[32:35], a[208:211], a[200:203], a[32:35]\n\t"
"ds_read_b64_tr_b16 v[226:227], v246 offset:10240\n\t"
"v_mfma_f32_16x16x32_bf16 a[32:35], v[96:99], a[204:207], a[32:35]\n\t"
"v_mfma_f32_16x16x32_bf16 a[40:43], a[252:255], a[168:171], a[40:43]\n\t"
"v_mfma_f32_16x16x32_bf16 a[40:43], v[248:251], a[172:175], a[40:43]\n\t"
"v_mfma_f32_16x16x32_bf16 a[40:43], a[212:215], a[176:179], a[40:43]\n\t"
"v_mfma_f32_16x16x32_bf16 a[48:51], a[252:255], a[172:175], a[48:51]\n\t"
"s_waitcnt lgkmcnt(2)\n\t"
"v_mfma_f32_16x16x32_bf16 a[48:51], v[248:251], a[176:179], a[48:51]\n\t"
"v_mfma_f32_16x16x32_bf16 a[48:51], a[212:215], a[180:183], a[48:51]\n\t"
"v_mfma_f32_16x16x32_bf16 v[228:231], v[228:231], v[220:223], 0\n\t"
"v_mfma_f32_16x16x32_bf16 a[56:59], a[252:255], a[176:179], a[56:59]\n\t"
"ds_read_b64_tr_b16 v[232:233], v246 offset:13312\n\t"
"v_mfma_f32_16x16x32_bf16 a[56:59], v[248:251], a[180:183], a[56:59]\n\t"
"ds_read_b64_tr_b16 v[234:235], v246 offset:16384\n\t"
"v_mfma_f32_16x16x32_bf16 a[56:59], a[212:215], a[184:187], a[56:59]\n\t"
"v_mfma_f32_16x16x32_bf16 a[64:67], a[252:255], a[180:183], a[64:67]\n\t"
"s_waitcnt lgkmcnt(2)\n\t"
"v_mfma_f32_16x16x32_bf16 a[64:67], v[248:251], a[184:187], a[64:67]\n\t"
"v_cvt_pk_bf16_f32 v228, v228, v229\n\t"
"v_cvt_pk_bf16_f32 v229, v230, v231\n\t"
"v_mfma_f32_16x16x32_bf16 a[64:67], a[212:215], a[188:191], a[64:67]\n\t"
"v_mfma_f32_16x16x32_bf16 v[224:227], v[224:227], v[220:223], 0\n\t"
"buffer_load_dwordx4 v[96:99], v252, s[8:11], s20 offen offset:2048 sc1\n\t"
"v_mfma_f32_16x16x32_bf16 a[72:75], a[252:255], a[184:187], a[72:75]\n\t"
"s_mov_b32 s20, 454656\n\t"
"v_mfma_f32_16x16x32_bf16 a[72:75], v[248:251], a[188:191], a[72:75]\n\t"
"v_mfma_f32_16x16x32_bf16 a[72:75], a[212:215], a[192:195], a[72:75]\n\t"
"v_mfma_f32_16x16x32_bf16 a[80:83], a[252:255], a[188:191], a[80:83]\n\t"
"buffer_load_dwordx4 a[232:235], v252, s[8:11], s20 offen offset:0 sc1\n\t"
"v_mfma_f32_16x16x32_bf16 a[80:83], v[248:251], a[192:195], a[80:83]\n\t"
"v_cvt_pk_bf16_f32 v230, v224, v225\n\t"
"v_cvt_pk_bf16_f32 v231, v226, v227\n\t"
"v_mfma_f32_16x16x32_bf16 a[80:83], a[212:215], a[196:199], a[80:83]\n\t"
"ds_read_b64_tr_b16 v[224:225], v246 offset:20992\n\t"
"v_mfma_f32_16x16x32_bf16 a[88:91], a[252:255], a[192:195], a[88:91]\n\t"
"s_waitcnt lgkmcnt(1)\n\t"
"ds_read_b64_tr_b16 v[226:227], v246 offset:24064\n\t"
"v_mfma_f32_16x16x32_bf16 a[88:91], v[248:251], a[196:199], a[88:91]\n\t"
"buffer_load_dwordx4 a[236:239], v252, s[8:11], s20 offen offset:1024 sc1\n\t"
"v_mfma_f32_16x16x32_bf16 a[88:91], a[212:215], a[200:203], a[88:91]\n\t"
"v_mfma_f32_16x16x32_bf16 v[232:235], v[232:235], v[220:223], 0\n\t"
"v_mfma_f32_16x16x32_bf16 a[96:99], a[252:255], a[196:199], a[96:99]\n\t"
"s_waitcnt vmcnt(13)\n\t"
"v_mfma_f32_16x16x32_bf16 a[96:99], v[248:251], a[200:203], a[96:99]\n\t"
"buffer_load_dwordx4 v[248:251], v252, s[8:11], s20 offen offset:2048 sc1\n\t"
"v_mfma_f32_16x16x32_bf16 a[96:99], a[212:215], a[204:207], a[96:99]\n\t"
"s_mov_b32 s20, 466944\n\t"
"v_mfma_f32_16x16x32_bf16 a[104:107], a[216:219], a[168:171], a[104:107]\n\t"
"ds_read_b128 a[168:171], v244 offset:9216\n\t"
"v_mfma_f32_16x16x32_bf16 a[104:107], a[220:223], a[172:175], a[104:107]\n\t"
"v_cvt_pk_bf16_f32 v232, v232, v233\n\t"
"v_cvt_pk_bf16_f32 v233, v234, v235\n\t"
"v_mfma_f32_16x16x32_bf16 a[104:107], v[236:239], a[176:179], a[104:107]\n\t"
"v_mfma_f32_16x16x32_bf16 a[112:115], a[216:219], a[172:175], a[112:115]\n\t"
"buffer_load_dwordx4 a[212:215], v252, s[8:11], s20 offen offset:0 sc1\n\t"
"v_mfma_f32_16x16x32_bf16 a[112:115], a[220:223], a[176:179], a[112:115]\n\t"
"s_waitcnt lgkmcnt(1)\n\t"
"ds_read_b128 a[172:175], v244 offset:9728\n\t"
"v_mfma_f32_16x16x32_bf16 a[112:115], v[236:239], a[180:183], a[112:115]\n\t"
"v_mfma_f32_16x16x32_bf16 v[224:227], v[224:227], v[220:223], 0\n\t"
"buffer_load_dwordx4 a[240:243], v252, s[8:11], s20 offen offset:1024 sc1\n\t"
"v_mfma_f32_16x16x32_bf16 a[120:123], a[216:219], a[176:179], a[120:123]\n\t"
"ds_read_b128 a[176:179], v244 offset:10240\n\t"
"v_mfma_f32_16x16x32_bf16 a[120:123], a[220:223], a[180:183], a[120:123]\n\t"
"v_mfma_f32_16x16x32_bf16 a[120:123], v[236:239], a[184:187], a[120:123]\n\t"
"v_mfma_f32_16x16x32_bf16 a[128:131], a[216:219], a[180:183], a[128:131]\n\t"
"buffer_load_dwordx4 a[244:247], v252, s[8:11], s20 offen offset:2048 sc1\n\t"
"v_mfma_f32_16x16x32_bf16 a[128:131], a[220:223], a[184:187], a[128:131]\n\t"
"s_mov_b32 s20, 700416\n\t"
"ds_read_b128 a[180:183], v244 offset:10752\n\t"
"v_cvt_pk_bf16_f32 v234, v224, v225\n\t"
"v_mfma_f32_16x16x32_bf16 a[128:131], v[236:239], a[188:191], a[128:131]\n\t"
"v_cvt_pk_bf16_f32 v235, v226, v227\n\t"
"v_mfma_f32_16x16x32_bf16 a[136:139], a[216:219], a[184:187], a[136:139]\n\t"
"ds_read_b64_tr_b16 v[224:225], v246 offset:27136\n\t"
"v_mfma_f32_16x16x32_bf16 a[136:139], a[220:223], a[188:191], a[136:139]\n\t"
"ds_read_b64_tr_b16 v[226:227], v246 offset:30208\n\t"
"v_mfma_f32_16x16x32_bf16 a[136:139], v[236:239], a[192:195], a[136:139]\n\t"
"buffer_load_dwordx4 a[248:251], v252, s[8:11], s20 offen offset:0 sc1\n\t"
"v_mfma_f32_16x16x32_bf16 a[144:147], a[216:219], a[188:191], a[144:147]\n\t"
"ds_read_b128 a[184:187], v244 offset:11264\n\t"
"v_mfma_f32_16x16x32_bf16 a[144:147], a[220:223], a[192:195], a[144:147]\n\t"
"ds_read_b128 a[188:191], v244 offset:11776\n\t"
"v_mfma_f32_16x16x32_bf16 a[144:147], v[236:239], a[196:199], a[144:147]\n\t"
"buffer_load_dwordx4 a[252:255], v252, s[8:11], s20 offen offset:1024 sc1\n\t"
"v_mfma_f32_16x16x32_bf16 a[152:155], a[216:219], a[192:195], a[152:155]\n\t"
"ds_read_b128 a[192:195], v244 offset:12288\n\t"
"v_mfma_f32_16x16x32_bf16 a[152:155], a[220:223], a[196:199], a[152:155]\n\t"
"v_mfma_f32_16x16x32_bf16 a[152:155], v[236:239], a[200:203], a[152:155]\n\t"
"v_mfma_f32_16x16x32_bf16 a[160:163], a[216:219], a[196:199], a[160:163]\n\t"
"buffer_load_dwordx4 a[216:219], v252, s[8:11], s20 offen offset:2048 sc1\n\t"
"v_mfma_f32_16x16x32_bf16 a[160:163], a[220:223], a[200:203], a[160:163]\n\t"
"s_waitcnt vmcnt(9) lgkmcnt(3)\n\t"
"ds_read_b128 a[196:199], v244 offset:12800\n\t"
"s_mov_b32 s20, 712704\n\t"
"v_mfma_f32_16x16x32_bf16 a[160:163], v[236:239], a[204:207], a[160:163]\n\t"
"ds_read_b128 a[200:203], v244 offset:13312\n\t"
"v_mfma_f32_16x16x32_bf16 v[224:227], v[224:227], v[220:223], 0\n\t"
"ds_read_b128 a[204:207], v244 offset:13824\n\t"
"v_mfma_f32_16x16x32_bf16 v[200:203], a[224:227], a[168:171], v[200:203]\n\t"
"buffer_load_dwordx4 a[220:223], v252, s[8:11], s20 offen offset:0 sc1\n\t"
"v_mfma_f32_16x16x32_bf16 v[200:203], a[228:231], a[172:175], v[200:203]\n\t"
"v_mfma_f32_16x16x32_bf16 v[200:203], v[96:99], a[176:179], v[200:203]\n\t"
"v_mfma_f32_16x16x32_bf16 v[208:211], a[224:227], a[172:175], v[208:211]\n\t"
"s_waitcnt lgkmcnt(5)\n\t"
"v_mfma_f32_16x16x32_bf16 v[208:211], a[228:231], a[176:179], v[208:211]\n\t"
"v_cvt_pk_bf16_f32 v224, v224, v225\n\t"
"v_mfma_f32_16x16x32_bf16 v[208:211], v[96:99], a[180:183], v[208:211]\n\t"
"s_waitcnt lgkmcnt(0)\n\t"
"s_barrier\n\t"
"v_mfma_f32_16x16x32_bf16 v[216:219], a[224:227], a[176:179], v[216:219]\n\t"
"v_cvt_pk_bf16_f32 v225, v226, v227\n\t"
"v_mfma_f32_16x16x32_bf16 v[216:219], a[228:231], a[180:183], v[216:219]\n\t"
"v_mfma_f32_16x16x32_bf16 v[216:219], v[96:99], a[184:187], v[216:219]\n\t"
"ds_write2st64_b64 v245, v[228:229], v[230:231] offset0:0 offset1:1\n\t"
"v_mfma_f32_16x16x32_bf16 a[4:7], a[224:227], a[180:183], a[4:7]\n\t"
"v_mfma_f32_16x16x32_bf16 a[4:7], a[228:231], a[184:187], a[4:7]\n\t"
"ds_read_b64_tr_b16 v[228:229], v246 offset:33280\n\t"
"v_mfma_f32_16x16x32_bf16 a[4:7], v[96:99], a[188:191], a[4:7]\n\t"
"ds_write2st64_b64 v245, v[232:233], v[234:235] offset0:2 offset1:3\n\t"
"v_mfma_f32_16x16x32_bf16 a[12:15], a[224:227], a[184:187], a[12:15]\n\t"
"v_mfma_f32_16x16x32_bf16 a[12:15], a[228:231], a[188:191], a[12:15]\n\t"
"ds_read_b64_tr_b16 v[230:231], v246 offset:36352\n\t"
"v_mfma_f32_16x16x32_bf16 a[12:15], v[96:99], a[192:195], a[12:15]\n\t"
"ds_write_b64 v245, v[224:225] offset:2048\n\t"
"v_mfma_f32_16x16x32_bf16 a[20:23], a[224:227], a[188:191], a[20:23]\n\t"
"ds_read_b64_tr_b16 v[232:233], v246 offset:40960\n\t"
"v_mfma_f32_16x16x32_bf16 a[20:23], a[228:231], a[192:195], a[20:23]\n\t"
"ds_read_b64_tr_b16 v[234:235], v246 offset:44032\n\t"
"v_mfma_f32_16x16x32_bf16 a[20:23], v[96:99], a[196:199], a[20:23]\n\t"
"s_waitcnt lgkmcnt(3)\n\t"
"ds_read_b64_tr_b16 v[224:225], v246 offset:47104\n\t"
"v_mfma_f32_16x16x32_bf16 v[228:231], v[228:231], v[220:223], 0\n\t"
"ds_read_b64_tr_b16 v[226:227], v246 offset:50176\n\t"
"v_mfma_f32_16x16x32_bf16 a[28:31], a[224:227], a[192:195], a[28:31]\n\t"
"global_load_dwordx4 v[240:243], v252, s[48:49] offset:2048\n\t"
"v_mfma_f32_16x16x32_bf16 a[28:31], a[228:231], a[196:199], a[28:31]\n\t"
"global_load_dwordx4 a[208:211], v252, s[46:47] offset:1024\n\t"
"v_mfma_f32_16x16x32_bf16 a[28:31], v[96:99], a[200:203], a[28:31]\n\t"
"s_waitcnt lgkmcnt(2)\n\t"
"v_mfma_f32_16x16x32_bf16 v[232:235], v[232:235], v[220:223], 0\n\t"
"v_mfma_f32_16x16x32_bf16 a[36:39], a[224:227], a[196:199], a[36:39]\n\t"
"buffer_load_dwordx4 a[224:227], v252, s[8:11], s20 offen offset:1024 sc1\n\t"
"v_mfma_f32_16x16x32_bf16 a[36:39], a[228:231], a[200:203], a[36:39]\n\t"
"s_waitcnt vmcnt(10) lgkmcnt(0)\n\t"
"v_cvt_pk_bf16_f32 v228, v228, v229\n\t"
"v_cvt_pk_bf16_f32 v229, v230, v231\n\t"
"v_mfma_f32_16x16x32_bf16 a[36:39], v[96:99], a[204:207], a[36:39]\n\t"
"v_mfma_f32_16x16x32_bf16 v[224:227], v[224:227], v[220:223], 0\n\t"
"buffer_load_dwordx4 v[96:99], v252, s[8:11], s20 offen offset:2048 sc1\n\t"
"v_mfma_f32_16x16x32_bf16 a[44:47], a[232:235], a[168:171], a[44:47]\n\t"
"s_mov_b32 s20, 724992\n\t"
"v_cvt_pk_bf16_f32 v230, v232, v233\n\t"
"v_cvt_pk_bf16_f32 v231, v234, v235\n\t"
"v_mfma_f32_16x16x32_bf16 a[44:47], a[236:239], a[172:175], a[44:47]\n\t"
"ds_read_b64_tr_b16 v[232:233], v246 offset:53248\n\t"
"v_mfma_f32_16x16x32_bf16 a[44:47], v[248:251], a[176:179], a[44:47]\n\t"
"buffer_load_dwordx4 a[228:231], v252, s[8:11], s20 offen offset:0 sc1\n\t"
"v_mfma_f32_16x16x32_bf16 a[52:55], a[232:235], a[172:175], a[52:55]\n\t"
"ds_read_b64_tr_b16 v[234:235], v246 offset:56320\n\t"
"v_mfma_f32_16x16x32_bf16 a[52:55], a[236:239], a[176:179], a[52:55]\n\t"
"v_cvt_pk_bf16_f32 v224, v224, v225\n\t"
"v_cvt_pk_bf16_f32 v225, v226, v227\n\t"
"v_mfma_f32_16x16x32_bf16 a[52:55], v[248:251], a[180:183], a[52:55]\n\t"
"ds_write2st64_b64 v245, v[228:229], v[230:231] offset0:5 offset1:6\n\t"
"v_mfma_f32_16x16x32_bf16 a[60:63], a[232:235], a[176:179], a[60:63]\n\t"
"v_mfma_f32_16x16x32_bf16 a[60:63], a[236:239], a[180:183], a[60:63]\n\t"
"v_mfma_f32_16x16x32_bf16 a[60:63], v[248:251], a[184:187], a[60:63]\n\t"
"v_mfma_f32_16x16x32_bf16 a[68:71], a[232:235], a[180:183], a[68:71]\n\t"
"v_mfma_f32_16x16x32_bf16 a[68:71], a[236:239], a[184:187], a[68:71]\n\t"
"v_mfma_f32_16x16x32_bf16 a[68:71], v[248:251], a[188:191], a[68:71]\n\t"
"v_mfma_f32_16x16x32_bf16 a[76:79], a[232:235], a[184:187], a[76:79]\n\t"
"s_waitcnt lgkmcnt(1)\n\t"
"v_mfma_f32_16x16x32_bf16 a[76:79], a[236:239], a[188:191], a[76:79]\n\t"
"v_mfma_f32_16x16x32_bf16 a[76:79], v[248:251], a[192:195], a[76:79]\n\t"
"v_mfma_f32_16x16x32_bf16 v[232:235], v[232:235], v[220:223], 0\n\t"
"v_mfma_f32_16x16x32_bf16 a[84:87], a[232:235], a[188:191], a[84:87]\n\t"
"v_mfma_f32_16x16x32_bf16 a[84:87], a[236:239], a[192:195], a[84:87]\n\t"
"v_mfma_f32_16x16x32_bf16 a[84:87], v[248:251], a[196:199], a[84:87]\n\t"
"v_mfma_f32_16x16x32_bf16 a[92:95], a[232:235], a[192:195], a[92:95]\n\t"
"v_mfma_f32_16x16x32_bf16 a[92:95], a[236:239], a[196:199], a[92:95]\n\t"
"v_cvt_pk_bf16_f32 v226, v232, v233\n\t"
"v_cvt_pk_bf16_f32 v227, v234, v235\n\t"
"v_mfma_f32_16x16x32_bf16 a[92:95], v[248:251], a[200:203], a[92:95]\n\t"
"ds_write2st64_b64 v245, v[224:225], v[226:227] offset0:7 offset1:8\n\t"
"v_mfma_f32_16x16x32_bf16 a[100:103], a[232:235], a[196:199], a[100:103]\n\t"
"buffer_load_dwordx4 a[232:235], v252, s[8:11], s20 offen offset:1024 sc1\n\t"
"v_mfma_f32_16x16x32_bf16 a[100:103], a[236:239], a[200:203], a[100:103]\n\t"
"s_waitcnt vmcnt(10)\n\t"
"v_mfma_f32_16x16x32_bf16 a[100:103], v[248:251], a[204:207], a[100:103]\n\t"
"v_mfma_f32_16x16x32_bf16 a[108:111], a[212:215], a[168:171], a[108:111]\n\t"
"buffer_load_dwordx4 v[248:251], v252, s[8:11], s20 offen offset:2048 sc1\n\t"
"v_mfma_f32_16x16x32_bf16 a[108:111], a[240:243], a[172:175], a[108:111]\n\t"
"s_mov_b32 s20, 737280\n\t"
"v_mfma_f32_16x16x32_bf16 a[108:111], a[244:247], a[176:179], a[108:111]\n\t"
"s_waitcnt lgkmcnt(0)\n\t"
"s_barrier\n\t"
"v_mfma_f32_16x16x32_bf16 a[116:119], a[212:215], a[172:175], a[116:119]\n\t"
"s_waitcnt vmcnt(6)\n\t"
"ds_read_b64_tr_b16 v[236:237], v240 offset:1024\n\t"
"v_mfma_f32_16x16x32_bf16 a[116:119], a[240:243], a[176:179], a[116:119]\n\t"
"ds_read_b64_tr_b16 v[238:239], v240 offset:2560\n\t"
"v_mfma_f32_16x16x32_bf16 a[116:119], a[244:247], a[180:183], a[116:119]\n\t"
"ds_read_b64_tr_b16 v[228:229], v241 offset:1024\n\t"
"v_mfma_f32_16x16x32_bf16 a[124:127], a[212:215], a[176:179], a[124:127]\n\t"
"ds_read_b64_tr_b16 v[230:231], v241 offset:2560\n\t"
"v_mfma_f32_16x16x32_bf16 a[124:127], a[240:243], a[180:183], a[124:127]\n\t"
"buffer_load_dwordx4 a[236:239], v252, s[8:11], s20 offen offset:0 sc1\n\t"
"v_mfma_f32_16x16x32_bf16 a[124:127], a[244:247], a[184:187], a[124:127]\n\t"
"ds_read_b64_tr_b16 v[232:233], v240 offset:20992\n\t"
"v_mfma_f32_16x16x32_bf16 a[132:135], a[212:215], a[180:183], a[132:135]\n\t"
"s_waitcnt vmcnt(6) lgkmcnt(3)\n\t"
"ds_read_b64_tr_b16 v[234:235], v240 offset:22528\n\t"
"v_mfma_f32_16x16x32_bf16 a[132:135], a[240:243], a[184:187], a[132:135]\n\t"
"ds_read_b64_tr_b16 v[224:225], v241 offset:20992\n\t"
"v_mfma_f32_16x16x32_bf16 a[132:135], a[244:247], a[188:191], a[132:135]\n\t"
"s_waitcnt lgkmcnt(3)\n\t"
"ds_read_b64_tr_b16 v[226:227], v241 offset:22528\n\t"
"v_mfma_f32_16x16x32_bf16 v[236:239], v[236:239], a[208:211], 0\n\t"
"ds_read_b128 a[168:171], v244 offset:0\n\t"
"v_mfma_f32_16x16x32_bf16 v[228:231], v[228:231], a[208:211], 0\n\t"
"ds_read_b128 a[172:175], v244 offset:512\n\t"
"v_mfma_f32_16x16x32_bf16 a[140:143], a[212:215], a[184:187], a[140:143]\n\t"
"ds_read_b128 a[176:179], v244 offset:1024\n\t"
"v_mfma_f32_16x16x32_bf16 a[140:143], a[240:243], a[188:191], a[140:143]\n\t"
"ds_read_b128 a[180:183], v244 offset:1536\n\t"
"v_mfma_f32_16x16x32_bf16 a[140:143], a[244:247], a[192:195], a[140:143]\n\t"
"ds_read_b128 a[184:187], v244 offset:2048\n\t"
"v_mfma_f32_16x16x32_bf16 a[148:151], a[212:215], a[188:191], a[148:151]\n\t"
"s_waitcnt lgkmcnt(7)\n\t"
"ds_read_b128 a[188:191], v244 offset:2560\n\t"
"v_cvt_pk_bf16_f32 v236, v236, v237\n\t"
"v_mfma_f32_16x16x32_bf16 a[148:151], a[240:243], a[192:195], a[148:151]\n\t"
"s_waitcnt lgkmcnt(6)\n\t"
"v_cvt_pk_bf16_f32 v237, v238, v239\n\t"
"v_cvt_pk_bf16_f32 v238, v228, v229\n\t"
"v_mfma_f32_16x16x32_bf16 a[148:151], a[244:247], a[196:199], a[148:151]\n\t"
"v_cvt_pk_bf16_f32 v239, v230, v231\n\t"
"v_mfma_f32_16x16x32_bf16 v[232:235], v[232:235], a[208:211], 0\n\t"
"ds_write_b128 v242, v[236:239] offset:24576\n\t"
"v_mfma_f32_16x16x32_bf16 v[224:227], v[224:227], a[208:211], 0\n\t"
"v_mfma_f32_16x16x32_bf16 a[156:159], a[212:215], a[192:195], a[156:159]\n\t"
"ds_read_b128 a[192:195], v244 offset:3072\n\t"
"v_mfma_f32_16x16x32_bf16 a[156:159], a[240:243], a[196:199], a[156:159]\n\t"
"ds_read_b64_tr_b16 v[228:229], v240 offset:40960\n\t"
"v_mfma_f32_16x16x32_bf16 a[156:159], a[244:247], a[200:203], a[156:159]\n\t"
"ds_read_b64_tr_b16 v[230:231], v240 offset:42496\n\t"
"v_mfma_f32_16x16x32_bf16 a[164:167], a[212:215], a[196:199], a[164:167]\n\t"
"buffer_load_dwordx4 a[212:215], v252, s[8:11], s20 offen offset:1024 sc1\n\t"
"v_mfma_f32_16x16x32_bf16 a[164:167], a[240:243], a[200:203], a[164:167]\n\t"
"s_waitcnt lgkmcnt(7)\n\t"
"ds_read_b64_tr_b16 v[236:237], v241 offset:40960\n\t"
"v_cvt_pk_bf16_f32 v232, v232, v233\n\t"
"v_mfma_f32_16x16x32_bf16 a[164:167], a[244:247], a[204:207], a[164:167]\n\t"
"ds_read_b64_tr_b16 v[238:239], v241 offset:42496\n\t"
"v_cvt_pk_bf16_f32 v233, v234, v235\n\t"
"Lst6_%=:\n\t"
"v_mfma_f32_16x16x32_bf16 v[196:199], a[248:251], a[168:171], v[196:199]\n\t"
"s_waitcnt lgkmcnt(8)\n\t"
"ds_read_b128 a[196:199], v244 offset:3584\n\t"
"v_cvt_pk_bf16_f32 v234, v224, v225\n\t"
"v_mfma_f32_16x16x32_bf16 v[196:199], a[252:255], a[172:175], v[196:199]\n\t"
"ds_read_b128 a[200:203], v244 offset:4096\n\t"
"v_cvt_pk_bf16_f32 v235, v226, v227\n\t"
"v_mfma_f32_16x16x32_bf16 v[196:199], a[216:219], a[176:179], v[196:199]\n\t"
"ds_write_b128 v242, v[232:235] offset:26112\n\t"
"v_mfma_f32_16x16x32_bf16 v[204:207], a[248:251], a[172:175], v[204:207]\n\t"
"buffer_load_dwordx4 a[240:243], v252, s[8:11], s20 offen offset:2048 sc1\n\t"
"v_mfma_f32_16x16x32_bf16 v[204:207], a[252:255], a[176:179], v[204:207]\n\t"
"s_waitcnt lgkmcnt(10)\n\t"
"ds_read_b128 a[204:207], v244 offset:4608\n\t"
"v_mfma_f32_16x16x32_bf16 v[204:207], a[216:219], a[180:183], v[204:207]\n\t"
"s_mov_b32 s20, 749568\n\t"
"v_add_u32 v240, 59904, v240\n\t"
"v_add_u32 v241, 59904, v241\n\t"
"v_mfma_f32_16x16x32_bf16 v[212:215], a[248:251], a[176:179], v[212:215]\n\t"
"s_waitcnt lgkmcnt(4)\n\t"
"ds_read_b64_tr_b16 v[224:225], v240 offset:1024\n\t"
"v_mfma_f32_16x16x32_bf16 v[212:215], a[252:255], a[180:183], v[212:215]\n\t"
"buffer_load_dwordx4 a[244:247], v252, s[8:11], s20 offen offset:0 sc1\n\t"
"v_mfma_f32_16x16x32_bf16 v[212:215], a[216:219], a[184:187], v[212:215]\n\t"
"Lpg5_%=:\n\t"
"s_cbranch_vccnz Lpg9_%=\n\t"
"ds_read_b64_tr_b16 v[226:227], v240 offset:2560\n\t"
"v_mfma_f32_16x16x32_bf16 v[228:231], v[228:231], a[208:211], 0\n\t"
"ds_read_b64_tr_b16 v[232:233], v241 offset:1024\n\t"
"v_mfma_f32_16x16x32_bf16 v[236:239], v[236:239], a[208:211], 0\n\t"
"ds_read_b64_tr_b16 v[234:235], v241 offset:2560\n\t"
"v_mfma_f32_16x16x32_bf16 a[0:3], a[248:251], a[180:183], a[0:3]\n\t"
"v_mfma_f32_16x16x32_bf16 a[0:3], a[252:255], a[184:187], a[0:3]\n\t"
"v_mfma_f32_16x16x32_bf16 a[0:3], a[216:219], a[188:191], a[0:3]\n\t"
"v_mfma_f32_16x16x32_bf16 a[8:11], a[248:251], a[184:187], a[8:11]\n\t"
"s_waitcnt lgkmcnt(2)\n\t"
"v_cvt_pk_bf16_f32 v228, v228, v229\n\t"
"v_cvt_pk_bf16_f32 v229, v230, v231\n\t"
"v_mfma_f32_16x16x32_bf16 a[8:11], a[252:255], a[188:191], a[8:11]\n\t"
"s_waitcnt lgkmcnt(0)\n\t"
"v_cvt_pk_bf16_f32 v230, v236, v237\n\t"
"v_cvt_pk_bf16_f32 v231, v238, v239\n\t"
"v_mfma_f32_16x16x32_bf16 a[8:11], a[216:219], a[192:195], a[8:11]\n\t"
"ds_write_b128 v242, v[228:231] offset:27648\n\t"
"v_mfma_f32_16x16x32_bf16 v[224:227], v[224:227], a[208:211], 0\n\t"
"v_mfma_f32_16x16x32_bf16 v[232:235], v[232:235], a[208:211], 0\n\t"
"ds_read_b64_tr_b16 v[236:237], v240 offset:20992\n\t"
"v_mfma_f32_16x16x32_bf16 a[16:19], a[248:251], a[188:191], a[16:19]\n\t"
"ds_read_b64_tr_b16 v[238:239], v240 offset:22528\n\t"
"v_mfma_f32_16x16x32_bf16 a[16:19], a[252:255], a[192:195], a[16:19]\n\t"
"ds_read_b64_tr_b16 v[228:229], v241 offset:20992\n\t"
"v_mfma_f32_16x16x32_bf16 a[16:19], a[216:219], a[196:199], a[16:19]\n\t"
"ds_read_b64_tr_b16 v[230:231], v241 offset:22528\n\t"
"v_mfma_f32_16x16x32_bf16 a[24:27], a[248:251], a[192:195], a[24:27]\n\t"
"v_cvt_pk_bf16_f32 v224, v224, v225\n\t"
"v_cvt_pk_bf16_f32 v225, v226, v227\n\t"
"v_mfma_f32_16x16x32_bf16 a[24:27], a[252:255], a[196:199], a[24:27]\n\t"
"v_cvt_pk_bf16_f32 v226, v232, v233\n\t"
"v_cvt_pk_bf16_f32 v227, v234, v235\n\t"
"v_mfma_f32_16x16x32_bf16 a[24:27], a[216:219], a[200:203], a[24:27]\n\t"
"ds_write_b128 v242, v[224:227] offset:29184\n\t"
"v_mfma_f32_16x16x32_bf16 a[32:35], a[248:251], a[196:199], a[32:35]\n\t"
"buffer_load_dwordx4 a[248:251], v252, s[8:11], s20 offen offset:1024 sc1\n\t"
"v_mfma_f32_16x16x32_bf16 a[32:35], a[252:255], a[200:203], a[32:35]\n\t"
"s_waitcnt vmcnt(8)\n\t"
"ds_read_b64_tr_b16 v[232:233], v240 offset:40960\n\t"
"v_mfma_f32_16x16x32_bf16 a[32:35], a[216:219], a[204:207], a[32:35]\n\t"
"ds_read_b64_tr_b16 v[234:235], v240 offset:42496\n\t"
"v_mfma_f32_16x16x32_bf16 a[40:43], a[220:223], a[168:171], a[40:43]\n\t"
"buffer_load_dwordx4 a[216:219], v252, s[8:11], s20 offen offset:2048 sc1\n\t"
"v_mfma_f32_16x16x32_bf16 a[40:43], a[224:227], a[172:175], a[40:43]\n\t"
"s_mov_b32 s20, 761856\n\t"
"ds_read_b64_tr_b16 v[224:225], v241 offset:40960\n\t"
"v_mfma_f32_16x16x32_bf16 a[40:43], v[96:99], a[176:179], a[40:43]\n\t"
"ds_read_b64_tr_b16 v[226:227], v241 offset:42496\n\t"
"v_mfma_f32_16x16x32_bf16 a[48:51], a[220:223], a[172:175], a[48:51]\n\t"
"buffer_load_dwordx4 a[252:255], v252, s[8:11], s20 offen offset:0 sc1\n\t"
"v_mfma_f32_16x16x32_bf16 a[48:51], a[224:227], a[176:179], a[48:51]\n\t"
"s_waitcnt lgkmcnt(5)\n\t"
"v_mfma_f32_16x16x32_bf16 a[48:51], v[96:99], a[180:183], a[48:51]\n\t"
"v_mfma_f32_16x16x32_bf16 v[236:239], v[236:239], a[208:211], 0\n\t"
"v_mfma_f32_16x16x32_bf16 v[228:231], v[228:231], a[208:211], 0\n\t"
"v_xor_b32 v246, 0xc0, v246\n\t"
"v_mfma_f32_16x16x32_bf16 a[56:59], a[220:223], a[176:179], a[56:59]\n\t"
"v_mfma_f32_16x16x32_bf16 a[56:59], a[224:227], a[180:183], a[56:59]\n\t"
"v_mfma_f32_16x16x32_bf16 a[56:59], v[96:99], a[184:187], a[56:59]\n\t"
"v_mfma_f32_16x16x32_bf16 a[64:67], a[220:223], a[180:183], a[64:67]\n\t"
"s_waitcnt lgkmcnt(2)\n\t"
"v_cvt_pk_bf16_f32 v236, v236, v237\n\t"
"v_cvt_pk_bf16_f32 v237, v238, v239\n\t"
"v_mfma_f32_16x16x32_bf16 a[64:67], a[224:227], a[184:187], a[64:67]\n\t"
"s_waitcnt lgkmcnt(0)\n\t"
"v_cvt_pk_bf16_f32 v238, v228, v229\n\t"
"v_cvt_pk_bf16_f32 v239, v230, v231\n\t"
"v_mfma_f32_16x16x32_bf16 a[64:67], v[96:99], a[188:191], a[64:67]\n\t"
"ds_write_b128 v242, v[236:239] offset:30720\n\t"
"v_mfma_f32_16x16x32_bf16 v[232:235], v[232:235], a[208:211], 0\n\t"
"v_mfma_f32_16x16x32_bf16 v[224:227], v[224:227], a[208:211], 0\n\t"
"v_mfma_f32_16x16x32_bf16 a[72:75], a[220:223], a[184:187], a[72:75]\n\t"
"buffer_load_dwordx4 a[208:211], v252, s[8:11], s20 offen offset:1024 sc1\n\t"
"v_mfma_f32_16x16x32_bf16 a[72:75], a[224:227], a[188:191], a[72:75]\n\t"
"v_mfma_f32_16x16x32_bf16 a[72:75], v[96:99], a[192:195], a[72:75]\n\t"
"v_mfma_f32_16x16x32_bf16 a[80:83], a[220:223], a[188:191], a[80:83]\n\t"
"v_cvt_pk_bf16_f32 v232, v232, v233\n\t"
"v_cvt_pk_bf16_f32 v233, v234, v235\n\t"
"v_mfma_f32_16x16x32_bf16 a[80:83], a[224:227], a[192:195], a[80:83]\n\t"
"v_cvt_pk_bf16_f32 v234, v224, v225\n\t"
"v_cvt_pk_bf16_f32 v235, v226, v227\n\t"
"v_mfma_f32_16x16x32_bf16 a[80:83], v[96:99], a[196:199], a[80:83]\n\t"
"ds_write_b128 v242, v[232:235] offset:32256\n\t"
"v_mfma_f32_16x16x32_bf16 a[88:91], a[220:223], a[192:195], a[88:91]\n\t"
"v_mfma_f32_16x16x32_bf16 a[88:91], a[224:227], a[196:199], a[88:91]\n\t"
"ds_read_b64_tr_b16 v[228:229], v246 offset:1024\n\t"
"v_mfma_f32_16x16x32_bf16 a[88:91], v[96:99], a[200:203], a[88:91]\n\t"
"ds_read_b64_tr_b16 v[230:231], v246 offset:4096\n\t"
"v_mfma_f32_16x16x32_bf16 a[96:99], a[220:223], a[196:199], a[96:99]\n\t"
"buffer_load_dwordx4 a[220:223], v252, s[8:11], s20 offen offset:2048 sc1\n\t"
"v_mfma_f32_16x16x32_bf16 a[96:99], a[224:227], a[200:203], a[96:99]\n\t"
"s_waitcnt vmcnt(9)\n\t"
"ds_read_b64_tr_b16 v[236:237], v246 offset:7168\n\t"
"s_mov_b32 s20, 626688\n\t"
"v_mfma_f32_16x16x32_bf16 a[96:99], v[96:99], a[204:207], a[96:99]\n\t"
"ds_read_b64_tr_b16 v[238:239], v246 offset:10240\n\t"
"v_mfma_f32_16x16x32_bf16 a[104:107], a[228:231], a[168:171], a[104:107]\n\t"
"buffer_load_dwordx4 v[240:243], v252, s[8:11], s20 offen offset:0 sc1\n\t"
"v_mfma_f32_16x16x32_bf16 a[104:107], a[232:235], a[172:175], a[104:107]\n\t"
"ds_read_b64_tr_b16 v[224:225], v246 offset:13312\n\t"
"v_mfma_f32_16x16x32_bf16 a[104:107], v[248:251], a[176:179], a[104:107]\n\t"
"ds_read_b64_tr_b16 v[226:227], v246 offset:16384\n\t"
"v_mfma_f32_16x16x32_bf16 a[112:115], a[228:231], a[172:175], a[112:115]\n\t"
"buffer_load_dwordx4 v[96:99], v252, s[8:11], s20 offen offset:1024 sc1\n\t"
"v_mfma_f32_16x16x32_bf16 a[112:115], a[232:235], a[176:179], a[112:115]\n\t"
"ds_read_b128 a[168:171], v244 offset:9216\n\t"
"v_mfma_f32_16x16x32_bf16 a[112:115], v[248:251], a[180:183], a[112:115]\n\t"
"ds_read_b64_tr_b16 v[232:233], v246 offset:20992\n\t"
"v_mfma_f32_16x16x32_bf16 a[120:123], a[228:231], a[176:179], a[120:123]\n\t"
"buffer_load_dwordx4 a[224:227], v252, s[8:11], s20 offen offset:2048 sc1\n\t"
"v_mfma_f32_16x16x32_bf16 a[120:123], a[232:235], a[180:183], a[120:123]\n\t"
"s_waitcnt lgkmcnt(6)\n\t"
"ds_read_b64_tr_b16 v[234:235], v246 offset:24064\n\t"
"v_mfma_f32_16x16x32_bf16 a[120:123], v[248:251], a[184:187], a[120:123]\n\t"
"ds_read_b128 a[172:175], v244 offset:9728\n\t"
"v_mfma_f32_16x16x32_bf16 v[228:231], v[228:231], v[220:223], 0\n\t"
"ds_read_b128 a[176:179], v244 offset:10240\n\t"
"v_mfma_f32_16x16x32_bf16 a[128:131], a[228:231], a[180:183], a[128:131]\n\t"
"s_waitcnt lgkmcnt(7)\n\t"
"ds_read_b128 a[180:183], v244 offset:10752\n\t"
"v_mfma_f32_16x16x32_bf16 a[128:131], a[232:235], a[184:187], a[128:131]\n\t"
"v_mfma_f32_16x16x32_bf16 a[128:131], v[248:251], a[188:191], a[128:131]\n\t"
"v_mfma_f32_16x16x32_bf16 v[236:239], v[236:239], v[220:223], 0\n\t"
"v_mfma_f32_16x16x32_bf16 a[136:139], a[228:231], a[184:187], a[136:139]\n\t"
"s_waitcnt lgkmcnt(6)\n\t"
"ds_read_b128 a[184:187], v244 offset:11264\n\t"
"v_cvt_pk_bf16_f32 v228, v228, v229\n\t"
"v_mfma_f32_16x16x32_bf16 a[136:139], a[232:235], a[188:191], a[136:139]\n\t"
"v_cvt_pk_bf16_f32 v229, v230, v231\n\t"
"v_mfma_f32_16x16x32_bf16 a[136:139], v[248:251], a[192:195], a[136:139]\n\t"
"s_mov_b32 s20, 638976\n\t"
"v_mfma_f32_16x16x32_bf16 v[224:227], v[224:227], v[220:223], 0\n\t"
"v_mfma_f32_16x16x32_bf16 a[144:147], a[228:231], a[188:191], a[144:147]\n\t"
"s_waitcnt lgkmcnt(4)\n\t"
"ds_read_b128 a[188:191], v244 offset:11776\n\t"
"v_cvt_pk_bf16_f32 v230, v236, v237\n\t"
"v_mfma_f32_16x16x32_bf16 a[144:147], a[232:235], a[192:195], a[144:147]\n\t"
"v_cvt_pk_bf16_f32 v231, v238, v239\n\t"
"v_mfma_f32_16x16x32_bf16 a[144:147], v[248:251], a[196:199], a[144:147]\n\t"
"v_mfma_f32_16x16x32_bf16 v[232:235], v[232:235], v[220:223], 0\n\t"
"v_mfma_f32_16x16x32_bf16 a[152:155], a[228:231], a[192:195], a[152:155]\n\t"
"buffer_load_dwordx4 v[236:239], v252, s[8:11], s20 offen offset:0 sc1\n\t"
"v_mfma_f32_16x16x32_bf16 a[152:155], a[232:235], a[196:199], a[152:155]\n\t"
"ds_read_b128 a[192:195], v244 offset:12288\n\t"
"v_cvt_pk_bf16_f32 v224, v224, v225\n\t"
"v_mfma_f32_16x16x32_bf16 a[152:155], v[248:251], a[200:203], a[152:155]\n\t"
"v_cvt_pk_bf16_f32 v225, v226, v227\n\t"
"v_mfma_f32_16x16x32_bf16 a[160:163], a[228:231], a[196:199], a[160:163]\n\t"
"buffer_load_dwordx4 a[228:231], v252, s[8:11], s20 offen offset:1024 sc1\n\t"
"v_mfma_f32_16x16x32_bf16 a[160:163], a[232:235], a[200:203], a[160:163]\n\t"
"s_waitcnt vmcnt(11) lgkmcnt(4)\n\t"
"ds_read_b128 a[196:199], v244 offset:12800\n\t"
"v_cvt_pk_bf16_f32 v226, v232, v233\n\t"
"v_mfma_f32_16x16x32_bf16 a[160:163], v[248:251], a[204:207], a[160:163]\n\t"
"ds_read_b128 a[200:203], v244 offset:13312\n\t"
"v_cvt_pk_bf16_f32 v227, v234, v235\n\t"
"v_mfma_f32_16x16x32_bf16 v[200:203], a[236:239], a[168:171], v[200:203]\n\t"
"ds_read_b64_tr_b16 v[232:233], v246 offset:27136\n\t"
"v_mfma_f32_16x16x32_bf16 v[200:203], a[212:215], a[172:175], v[200:203]\n\t"
"s_waitcnt lgkmcnt(6)\n\t"
"ds_read_b64_tr_b16 v[234:235], v246 offset:30208\n\t"
"v_mfma_f32_16x16x32_bf16 v[200:203], a[240:243], a[176:179], v[200:203]\n\t"
"s_waitcnt lgkmcnt(0)\n\t"
"ds_read_b128 a[204:207], v244 offset:13824\n\t"
"v_mfma_f32_16x16x32_bf16 v[232:235], v[232:235], v[220:223], 0\n\t"
"buffer_load_dwordx4 v[248:251], v252, s[8:11], s20 offen offset:2048 sc1\n\t"
"s_mov_b32 s20, 651264\n\t"
"v_mfma_f32_16x16x32_bf16 v[208:211], a[236:239], a[172:175], v[208:211]\n\t"
"buffer_load_dwordx4 a[232:235], v252, s[8:11], s20 offen offset:0 sc1\n\t"
"v_mfma_f32_16x16x32_bf16 v[208:211], a[212:215], a[176:179], v[208:211]\n\t"
"s_add_u32 m0, s24, 256\n\t"
"s_add_u32 s28, s21, 0\n\t"
"v_mfma_f32_16x16x32_bf16 v[208:211], a[240:243], a[180:183], v[208:211]\n\t"
"s_waitcnt lgkmcnt(0)\n\t"
"s_barrier\n\t"
"v_mfma_f32_16x16x32_bf16 v[216:219], a[236:239], a[176:179], v[216:219]\n\t"
"v_mfma_f32_16x16x32_bf16 v[216:219], a[212:215], a[180:183], v[216:219]\n\t"
"buffer_load_dwordx4 v247, s[12:15], s28 offen offset:256 nt lds\n\t"
"v_mfma_f32_16x16x32_bf16 v[216:219], a[240:243], a[184:187], v[216:219]\n\t"
"s_add_u32 m0, s24, 6400\n\t"
"s_add_u32 s28, s21, 196608\n\t"
"ds_write2st64_b64 v245, v[228:229], v[230:231] offset0:0 offset1:1\n\t"
"v_mfma_f32_16x16x32_bf16 a[4:7], a[236:239], a[180:183], a[4:7]\n\t"
"buffer_load_dwordx4 v247, s[12:15], s28 offen offset:256 nt lds\n\t"
"v_mfma_f32_16x16x32_bf16 a[4:7], a[212:215], a[184:187], a[4:7]\n\t"
"s_add_u32 m0, s24, 12544\n\t"
"s_add_u32 s28, s21, 393216\n\t"
"ds_read_b64_tr_b16 v[228:229], v246 offset:33280\n\t"
"v_mfma_f32_16x16x32_bf16 a[4:7], a[240:243], a[188:191], a[4:7]\n\t"
"ds_write2st64_b64 v245, v[224:225], v[226:227] offset0:2 offset1:3\n\t"
"v_cvt_pk_bf16_f32 v232, v232, v233\n\t"
"v_cvt_pk_bf16_f32 v233, v234, v235\n\t"
"v_mfma_f32_16x16x32_bf16 a[12:15], a[236:239], a[184:187], a[12:15]\n\t"
"v_mfma_f32_16x16x32_bf16 a[12:15], a[212:215], a[188:191], a[12:15]\n\t"
"ds_read_b64_tr_b16 v[230:231], v246 offset:36352\n\t"
"v_mfma_f32_16x16x32_bf16 a[12:15], a[240:243], a[192:195], a[12:15]\n\t"
"buffer_load_dwordx4 v247, s[12:15], s28 offen offset:256 nt lds\n\t"
"s_add_u32 m0, s24, 20224\n\t"
"s_add_u32 s28, s21, 589824\n\t"
"v_mfma_f32_16x16x32_bf16 a[20:23], a[236:239], a[188:191], a[20:23]\n\t"
"buffer_load_dwordx4 v247, s[12:15], s28 offen offset:256 nt lds\n\t"
"v_mfma_f32_16x16x32_bf16 a[20:23], a[212:215], a[192:195], a[20:23]\n\t"
"s_add_u32 m0, s24, 26368\n\t"
"s_add_u32 s28, s21, 786432\n\t"
"ds_read_b64_tr_b16 v[224:225], v246 offset:40960\n\t"
"v_mfma_f32_16x16x32_bf16 a[20:23], a[240:243], a[196:199], a[20:23]\n\t"
"s_waitcnt lgkmcnt(1)\n\t"
"ds_write_b64 v245, v[232:233] offset:2048\n\t"
"ds_read_b64_tr_b16 v[226:227], v246 offset:44032\n\t"
"v_mfma_f32_16x16x32_bf16 v[228:231], v[228:231], v[220:223], 0\n\t"
"buffer_load_dwordx4 v247, s[12:15], s28 offen offset:256 nt lds\n\t"
"s_add_u32 m0, s24, 60160\n\t"
"s_add_u32 s28, s21, 1769472\n\t"
"v_mfma_f32_16x16x32_bf16 a[28:31], a[236:239], a[192:195], a[28:31]\n\t"
"buffer_load_dwordx4 v247, s[12:15], s28 offen offset:256 nt lds\n\t"
"v_mfma_f32_16x16x32_bf16 a[28:31], a[212:215], a[196:199], a[28:31]\n\t"
"ds_read_b64_tr_b16 v[232:233], v246 offset:47104\n\t"
"v_mfma_f32_16x16x32_bf16 a[28:31], a[240:243], a[200:203], a[28:31]\n\t"
"s_waitcnt lgkmcnt(1)\n\t"
"ds_read_b64_tr_b16 v[234:235], v246 offset:50176\n\t"
"v_mfma_f32_16x16x32_bf16 v[224:227], v[224:227], v[220:223], 0\n\t"
"v_mfma_f32_16x16x32_bf16 a[36:39], a[236:239], a[196:199], a[36:39]\n\t"
"v_cvt_pk_bf16_f32 v228, v228, v229\n\t"
"v_cvt_pk_bf16_f32 v229, v230, v231\n\t"
"v_mfma_f32_16x16x32_bf16 a[36:39], a[212:215], a[200:203], a[36:39]\n\t"
"s_waitcnt vmcnt(16)\n\t"
"v_mfma_f32_16x16x32_bf16 a[36:39], a[240:243], a[204:207], a[36:39]\n\t"
"s_waitcnt lgkmcnt(0)\n\t"
"v_mfma_f32_16x16x32_bf16 v[232:235], v[232:235], v[220:223], 0\n\t"
"v_mfma_f32_16x16x32_bf16 a[44:47], a[244:247], a[168:171], a[44:47]\n\t"
"v_cvt_pk_bf16_f32 v230, v224, v225\n\t"
"v_cvt_pk_bf16_f32 v231, v226, v227\n\t"
"v_mfma_f32_16x16x32_bf16 a[44:47], a[248:251], a[172:175], a[44:47]\n\t"
"ds_read_b64_tr_b16 v[224:225], v246 offset:53248\n\t"
"v_mfma_f32_16x16x32_bf16 a[44:47], a[216:219], a[176:179], a[44:47]\n\t"
"ds_write2st64_b64 v245, v[228:229], v[230:231] offset0:5 offset1:6\n\t"
"v_mfma_f32_16x16x32_bf16 a[52:55], a[244:247], a[172:175], a[52:55]\n\t"
"v_mfma_f32_16x16x32_bf16 a[52:55], a[248:251], a[176:179], a[52:55]\n\t"
"ds_read_b64_tr_b16 v[226:227], v246 offset:56320\n\t"
"v_mfma_f32_16x16x32_bf16 a[52:55], a[216:219], a[180:183], a[52:55]\n\t"
"v_cvt_pk_bf16_f32 v232, v232, v233\n\t"
"v_cvt_pk_bf16_f32 v233, v234, v235\n\t"
"v_mfma_f32_16x16x32_bf16 a[60:63], a[244:247], a[176:179], a[60:63]\n\t"
"v_mfma_f32_16x16x32_bf16 a[60:63], a[248:251], a[180:183], a[60:63]\n\t"
"v_mfma_f32_16x16x32_bf16 a[60:63], a[216:219], a[184:187], a[60:63]\n\t"
"v_mfma_f32_16x16x32_bf16 a[68:71], a[244:247], a[180:183], a[68:71]\n\t"
"v_mfma_f32_16x16x32_bf16 a[68:71], a[248:251], a[184:187], a[68:71]\n\t"
"v_mfma_f32_16x16x32_bf16 a[68:71], a[216:219], a[188:191], a[68:71]\n\t"
"v_mfma_f32_16x16x32_bf16 a[76:79], a[244:247], a[184:187], a[76:79]\n\t"
"s_waitcnt lgkmcnt(0)\n\t"
"v_mfma_f32_16x16x32_bf16 a[76:79], a[248:251], a[188:191], a[76:79]\n\t"
"v_mfma_f32_16x16x32_bf16 a[76:79], a[216:219], a[192:195], a[76:79]\n\t"
"v_mfma_f32_16x16x32_bf16 v[224:227], v[224:227], v[220:223], 0\n\t"
"v_mfma_f32_16x16x32_bf16 a[84:87], a[244:247], a[188:191], a[84:87]\n\t"
"v_mfma_f32_16x16x32_bf16 a[84:87], a[248:251], a[192:195], a[84:87]\n\t"
"buffer_load_dwordx4 a[212:215], v252, s[8:11], s20 offen offset:1024 sc1\n\t"
"v_mfma_f32_16x16x32_bf16 a[84:87], a[216:219], a[196:199], a[84:87]\n\t"
"v_mfma_f32_16x16x32_bf16 a[92:95], a[244:247], a[192:195], a[92:95]\n\t"
"v_mfma_f32_16x16x32_bf16 a[92:95], a[248:251], a[196:199], a[92:95]\n\t"
"v_cvt_pk_bf16_f32 v234, v224, v225\n\t"
"v_cvt_pk_bf16_f32 v235, v226, v227\n\t"
"v_mfma_f32_16x16x32_bf16 a[92:95], a[216:219], a[200:203], a[92:95]\n\t"
"ds_write2st64_b64 v245, v[232:233], v[234:235] offset0:7 offset1:8\n\t"
"v_mfma_f32_16x16x32_bf16 a[100:103], a[244:247], a[196:199], a[100:103]\n\t"
"s_waitcnt vmcnt(14)\n\t"
"v_mfma_f32_16x16x32_bf16 a[100:103], a[248:251], a[200:203], a[100:103]\n\t"
"v_mfma_f32_16x16x32_bf16 a[100:103], a[216:219], a[204:207], a[100:103]\n\t"
"v_mfma_f32_16x16x32_bf16 a[108:111], a[252:255], a[168:171], a[108:111]\n\t"
"v_mfma_f32_16x16x32_bf16 a[108:111], a[208:211], a[172:175], a[108:111]\n\t"
"v_mfma_f32_16x16x32_bf16 a[108:111], a[220:223], a[176:179], a[108:111]\n\t"
"s_waitcnt lgkmcnt(0)\n\t"
"s_barrier\n\t"
"v_mfma_f32_16x16x32_bf16 a[116:119], a[252:255], a[172:175], a[116:119]\n\t"
"ds_read_b128 a[168:171], v244 offset:0\n\t"
"v_mfma_f32_16x16x32_bf16 a[116:119], a[208:211], a[176:179], a[116:119]\n\t"
"ds_read_b128 a[172:175], v244 offset:512\n\t"
"v_mfma_f32_16x16x32_bf16 a[116:119], a[220:223], a[180:183], a[116:119]\n\t"
"v_mfma_f32_16x16x32_bf16 a[124:127], a[252:255], a[176:179], a[124:127]\n\t"
"ds_read_b128 a[176:179], v244 offset:1024\n\t"
"v_mfma_f32_16x16x32_bf16 a[124:127], a[208:211], a[180:183], a[124:127]\n\t"
"v_mfma_f32_16x16x32_bf16 a[124:127], a[220:223], a[184:187], a[124:127]\n\t"
"v_mfma_f32_16x16x32_bf16 a[132:135], a[252:255], a[180:183], a[132:135]\n\t"
"ds_read_b128 a[180:183], v244 offset:1536\n\t"
"v_mfma_f32_16x16x32_bf16 a[132:135], a[208:211], a[184:187], a[132:135]\n\t"
"v_mfma_f32_16x16x32_bf16 a[132:135], a[220:223], a[188:191], a[132:135]\n\t"
"v_mfma_f32_16x16x32_bf16 a[140:143], a[252:255], a[184:187], a[140:143]\n\t"
"ds_read_b128 a[184:187], v244 offset:2048\n\t"
"v_mfma_f32_16x16x32_bf16 a[140:143], a[208:211], a[188:191], a[140:143]\n\t"
"v_mfma_f32_16x16x32_bf16 a[140:143], a[220:223], a[192:195], a[140:143]\n\t"
"v_mfma_f32_16x16x32_bf16 a[148:151], a[252:255], a[188:191], a[148:151]\n\t"
"ds_read_b128 a[188:191], v244 offset:2560\n\t"
"v_mfma_f32_16x16x32_bf16 a[148:151], a[208:211], a[192:195], a[148:151]\n\t"
"v_mfma_f32_16x16x32_bf16 a[148:151], a[220:223], a[196:199], a[148:151]\n\t"
"v_mfma_f32_16x16x32_bf16 a[156:159], a[252:255], a[192:195], a[156:159]\n\t"
"ds_read_b128 a[192:195], v244 offset:3072\n\t"
"v_mfma_f32_16x16x32_bf16 a[156:159], a[208:211], a[196:199], a[156:159]\n\t"
"v_mfma_f32_16x16x32_bf16 a[156:159], a[220:223], a[200:203], a[156:159]\n\t"
"v_mfma_f32_16x16x32_bf16 a[164:167], a[252:255], a[196:199], a[164:167]\n\t"
"s_waitcnt vmcnt(11) lgkmcnt(4)\n\t"
"ds_read_b128 a[196:199], v244 offset:3584\n\t"
"v_mfma_f32_16x16x32_bf16 a[164:167], a[208:211], a[200:203], a[164:167]\n\t"
"ds_read_b128 a[200:203], v244 offset:4096\n\t"
"v_mfma_f32_16x16x32_bf16 a[164:167], a[220:223], a[204:207], a[164:167]\n\t"
"ds_read_b128 a[204:207], v244 offset:4608\n\t"
"Lst7_%=:\n\t"
"v_mfma_f32_16x16x32_bf16 v[0:3], v[240:243], a[168:171], v[0:3]\n\t"
"buffer_load_dwordx4 a[208:211], v252, s[8:11], s20 offen offset:2048 sc1\n\t"
"v_mfma_f32_16x16x32_bf16 v[0:3], v[96:99], a[172:175], v[0:3]\n\t"
"s_waitcnt lgkmcnt(6)\n\t"
"s_mov_b32 s20, 663552\n\t"
"v_mfma_f32_16x16x32_bf16 v[0:3], a[224:227], a[176:179], v[0:3]\n\t"
"v_mfma_f32_16x16x32_bf16 v[4:7], v[240:243], a[172:175], v[4:7]\n\t"
"s_waitcnt lgkmcnt(5)\n\t"
"v_mfma_f32_16x16x32_bf16 v[4:7], v[96:99], a[176:179], v[4:7]\n\t"
"buffer_load_dwordx4 a[216:219], v252, s[8:11], s20 offen offset:0 sc1\n\t"
"v_mfma_f32_16x16x32_bf16 v[4:7], a[224:227], a[180:183], v[4:7]\n\t"
"v_mfma_f32_16x16x32_bf16 v[8:11], v[240:243], a[176:179], v[8:11]\n\t"
"s_waitcnt lgkmcnt(4)\n\t"
"v_mfma_f32_16x16x32_bf16 v[8:11], v[96:99], a[180:183], v[8:11]\n\t"
"v_mfma_f32_16x16x32_bf16 v[8:11], a[224:227], a[184:187], v[8:11]\n\t"
"buffer_load_dwordx4 a[220:223], v252, s[8:11], s20 offen offset:1024 sc1\n\t"
"v_mfma_f32_16x16x32_bf16 v[12:15], v[240:243], a[180:183], v[12:15]\n\t"
"s_waitcnt lgkmcnt(3)\n\t"
"v_mfma_f32_16x16x32_bf16 v[12:15], v[96:99], a[184:187], v[12:15]\n\t"
"v_mfma_f32_16x16x32_bf16 v[12:15], a[224:227], a[188:191], v[12:15]\n\t"
"v_mfma_f32_16x16x32_bf16 v[16:19], v[240:243], a[184:187], v[16:19]\n\t"
"buffer_load_dwordx4 a[236:239], v252, s[8:11], s20 offen offset:2048 sc1\n\t"
"v_mfma_f32_16x16x32_bf16 v[16:19], v[96:99], a[188:191], v[16:19]\n\t"
"s_waitcnt lgkmcnt(2)\n\t"
"s_mov_b32 s20, 675840\n\t"
"v_mfma_f32_16x16x32_bf16 v[16:19], a[224:227], a[192:195], v[16:19]\n\t"
"ds_read_b64_tr_b16 v[228:229], v246 offset:1536\n\t"
"v_mfma_f32_16x16x32_bf16 v[20:23], v[240:243], a[188:191], v[20:23]\n\t"
"s_waitcnt lgkmcnt(2)\n\t"
"ds_read_b64_tr_b16 v[230:231], v246 offset:4608\n\t"
"v_mfma_f32_16x16x32_bf16 v[20:23], v[96:99], a[192:195], v[20:23]\n\t"
"buffer_load_dwordx4 a[240:243], v252, s[8:11], s20 offen offset:0 sc1\n\t"
"v_mfma_f32_16x16x32_bf16 v[20:23], a[224:227], a[196:199], v[20:23]\n\t"
"v_mfma_f32_16x16x32_bf16 v[24:27], v[240:243], a[192:195], v[24:27]\n\t"
"s_waitcnt lgkmcnt(2)\n\t"
"v_mfma_f32_16x16x32_bf16 v[24:27], v[96:99], a[196:199], v[24:27]\n\t"
"v_mfma_f32_16x16x32_bf16 v[24:27], a[224:227], a[200:203], v[24:27]\n\t"
"buffer_load_dwordx4 a[244:247], v252, s[8:11], s20 offen offset:1024 sc1\n\t"
"v_mfma_f32_16x16x32_bf16 v[28:31], v[240:243], a[196:199], v[28:31]\n\t"
"s_waitcnt vmcnt(14)\n\t"
"ds_read_b64_tr_b16 v[224:225], v246 offset:7680\n\t"
"v_mfma_f32_16x16x32_bf16 v[28:31], v[96:99], a[200:203], v[28:31]\n\t"
"ds_read_b64_tr_b16 v[226:227], v246 offset:10752\n\t"
"v_mfma_f32_16x16x32_bf16 v[28:31], a[224:227], a[204:207], v[28:31]\n\t"
"v_mfma_f32_16x16x32_bf16 v[32:35], v[236:239], a[168:171], v[32:35]\n\t"
"buffer_load_dwordx4 v[240:243], v252, s[8:11], s20 offen offset:2048 sc1\n\t"
"v_mfma_f32_16x16x32_bf16 v[32:35], a[228:231], a[172:175], v[32:35]\n\t"
"s_mov_b32 s20, 688128\n\t"
"v_mfma_f32_16x16x32_bf16 v[32:35], v[248:251], a[176:179], v[32:35]\n\t"
"v_mfma_f32_16x16x32_bf16 v[36:39], v[236:239], a[172:175], v[36:39]\n\t"
"s_waitcnt lgkmcnt(2)\n\t"
"v_mfma_f32_16x16x32_bf16 v[36:39], a[228:231], a[176:179], v[36:39]\n\t"
"buffer_load_dwordx4 v[96:99], v252, s[8:11], s20 offen offset:0 sc1\n\t"
"v_mfma_f32_16x16x32_bf16 v[36:39], v[248:251], a[180:183], v[36:39]\n\t"
"v_mfma_f32_16x16x32_bf16 v[228:231], v[228:231], v[220:223], 0\n\t"
"v_mfma_f32_16x16x32_bf16 v[40:43], v[236:239], a[176:179], v[40:43]\n\t"
"ds_read_b64_tr_b16 v[232:233], v246 offset:13824\n\t"
"v_mfma_f32_16x16x32_bf16 v[40:43], a[228:231], a[180:183], v[40:43]\n\t"
"buffer_load_dwordx4 a[224:227], v252, s[8:11], s20 offen offset:1024 sc1\n\t"
"v_mfma_f32_16x16x32_bf16 v[40:43], v[248:251], a[184:187], v[40:43]\n\t"
"ds_read_b64_tr_b16 v[234:235], v246 offset:16896\n\t"
"v_mfma_f32_16x16x32_bf16 v[44:47], v[236:239], a[180:183], v[44:47]\n\t"
"s_waitcnt lgkmcnt(2)\n\t"
"v_mfma_f32_16x16x32_bf16 v[44:47], a[228:231], a[184:187], v[44:47]\n\t"
"buffer_load_dwordx4 a[248:251], v252, s[8:11], s20 offen offset:2048 sc1\n\t"
"v_mfma_f32_16x16x32_bf16 v[44:47], v[248:251], a[188:191], v[44:47]\n\t"
"s_mov_b32 s20, 921600\n\t"
"v_cvt_pk_bf16_f32 v228, v228, v229\n\t"
"v_cvt_pk_bf16_f32 v229, v230, v231\n\t"
"v_mfma_f32_16x16x32_bf16 v[224:227], v[224:227], v[220:223], 0\n\t"
"v_mfma_f32_16x16x32_bf16 v[48:51], v[236:239], a[184:187], v[48:51]\n\t"
"v_mfma_f32_16x16x32_bf16 v[48:51], a[228:231], a[188:191], v[48:51]\n\t"
"buffer_load_dwordx4 a[252:255], v252, s[8:11], s20 offen offset:0 sc1\n\t"
"v_mfma_f32_16x16x32_bf16 v[48:51], v[248:251], a[192:195], v[48:51]\n\t"
"v_mfma_f32_16x16x32_bf16 v[52:55], v[236:239], a[188:191], v[52:55]\n\t"
"v_mfma_f32_16x16x32_bf16 v[52:55], a[228:231], a[192:195], v[52:55]\n\t"
"v_cvt_pk_bf16_f32 v230, v224, v225\n\t"
"v_cvt_pk_bf16_f32 v231, v226, v227\n\t"
"v_mfma_f32_16x16x32_bf16 v[52:55], v[248:251], a[196:199], v[52:55]\n\t"
"ds_read_b64_tr_b16 v[224:225], v246 offset:21504\n\t"
"v_mfma_f32_16x16x32_bf16 v[56:59], v[236:239], a[192:195], v[56:59]\n\t"
"s_waitcnt lgkmcnt(1)\n\t"
"ds_read_b64_tr_b16 v[226:227], v246 offset:24576\n\t"
"v_mfma_f32_16x16x32_bf16 v[56:59], a[228:231], a[196:199], v[56:59]\n\t"
"v_mfma_f32_16x16x32_bf16 v[56:59], v[248:251], a[200:203], v[56:59]\n\t"
"v_mfma_f32_16x16x32_bf16 v[232:235], v[232:235], v[220:223], 0\n\t"
"v_mfma_f32_16x16x32_bf16 v[60:63], v[236:239], a[196:199], v[60:63]\n\t"
"buffer_load_dwordx4 v[236:239], v252, s[8:11], s20 offen offset:1024 sc1\n\t"
"v_mfma_f32_16x16x32_bf16 v[60:63], a[228:231], a[200:203], v[60:63]\n\t"
"s_waitcnt vmcnt(11)\n\t"
"v_mfma_f32_16x16x32_bf16 v[60:63], v[248:251], a[204:207], v[60:63]\n\t"
"v_mfma_f32_16x16x32_bf16 v[64:67], a[232:235], a[168:171], v[64:67]\n\t"
"buffer_load_dwordx4 v[248:251], v252, s[8:11], s20 offen offset:2048 sc1\n\t"
"v_mfma_f32_16x16x32_bf16 v[64:67], a[212:215], a[172:175], v[64:67]\n\t"
"s_mov_b32 s20, 933888\n\t"
"ds_read_b128 a[168:171], v244 offset:9216\n\t"
"v_cvt_pk_bf16_f32 v232, v232, v233\n\t"
"v_mfma_f32_16x16x32_bf16 v[64:67], a[208:211], a[176:179], v[64:67]\n\t"
"v_cvt_pk_bf16_f32 v233, v234, v235\n\t"
"v_mfma_f32_16x16x32_bf16 v[68:71], a[232:235], a[172:175], v[68:71]\n\t"
"buffer_load_dwordx4 a[228:231], v252, s[8:11], s20 offen offset:0 sc1\n\t"
"v_mfma_f32_16x16x32_bf16 v[68:71], a[212:215], a[176:179], v[68:71]\n\t"
"s_waitcnt lgkmcnt(1)\n\t"
"ds_read_b128 a[172:175], v244 offset:9728\n\t"
"v_mfma_f32_16x16x32_bf16 v[68:71], a[208:211], a[180:183], v[68:71]\n\t"
"v_mfma_f32_16x16x32_bf16 v[224:227], v[224:227], v[220:223], 0\n\t"
"v_mfma_f32_16x16x32_bf16 v[72:75], a[232:235], a[176:179], v[72:75]\n\t"
"ds_read_b128 a[176:179], v244 offset:10240\n\t"
"v_mfma_f32_16x16x32_bf16 v[72:75], a[212:215], a[180:183], v[72:75]\n\t"
"v_mfma_f32_16x16x32_bf16 v[72:75], a[208:211], a[184:187], v[72:75]\n\t"
"v_mfma_f32_16x16x32_bf16 v[76:79], a[232:235], a[180:183], v[76:79]\n\t"
"ds_read_b128 a[180:183], v244 offset:10752\n\t"
"v_mfma_f32_16x16x32_bf16 v[76:79], a[212:215], a[184:187], v[76:79]\n\t"
"v_cvt_pk_bf16_f32 v234, v224, v225\n\t"
"v_cvt_pk_bf16_f32 v235, v226, v227\n\t"
"v_mfma_f32_16x16x32_bf16 v[76:79], a[208:211], a[188:191], v[76:79]\n\t"
"ds_read_b64_tr_b16 v[224:225], v246 offset:27648\n\t"
"v_mfma_f32_16x16x32_bf16 v[80:83], a[232:235], a[184:187], v[80:83]\n\t"
"ds_read_b64_tr_b16 v[226:227], v246 offset:30720\n\t"
"v_mfma_f32_16x16x32_bf16 v[80:83], a[212:215], a[188:191], v[80:83]\n\t"
"ds_read_b128 a[184:187], v244 offset:11264\n\t"
"v_mfma_f32_16x16x32_bf16 v[80:83], a[208:211], a[192:195], v[80:83]\n\t"
"v_mfma_f32_16x16x32_bf16 v[84:87], a[232:235], a[188:191], v[84:87]\n\t"
"ds_read_b128 a[188:191], v244 offset:11776\n\t"
"v_mfma_f32_16x16x32_bf16 v[84:87], a[212:215], a[192:195], v[84:87]\n\t"
"v_mfma_f32_16x16x32_bf16 v[84:87], a[208:211], a[196:199], v[84:87]\n\t"
"v_mfma_f32_16x16x32_bf16 v[88:91], a[232:235], a[192:195], v[88:91]\n\t"
"ds_read_b128 a[192:195], v244 offset:12288\n\t"
"v_mfma_f32_16x16x32_bf16 v[88:91], a[212:215], a[196:199], v[88:91]\n\t"
"v_mfma_f32_16x16x32_bf16 v[88:91], a[208:211], a[200:203], v[88:91]\n\t"
"v_mfma_f32_16x16x32_bf16 v[92:95], a[232:235], a[196:199], v[92:95]\n\t"
"buffer_load_dwordx4 a[232:235], v252, s[8:11], s20 offen offset:1024 sc1\n\t"
"v_mfma_f32_16x16x32_bf16 v[92:95], a[212:215], a[200:203], v[92:95]\n\t"
"s_waitcnt vmcnt(11) lgkmcnt(3)\n\t"
"ds_read_b128 a[196:199], v244 offset:12800\n\t"
"v_mfma_f32_16x16x32_bf16 v[92:95], a[208:211], a[204:207], v[92:95]\n\t"
"ds_read_b128 a[200:203], v244 offset:13312\n\t"
"v_mfma_f32_16x16x32_bf16 v[224:227], v[224:227], v[220:223], 0\n\t"
"ds_read_b128 a[204:207], v244 offset:13824\n\t"
"v_mfma_f32_16x16x32_bf16 v[100:103], a[216:219], a[168:171], v[100:103]\n\t"
"buffer_load_dwordx4 a[208:211], v252, s[8:11], s20 offen offset:2048 sc1\n\t"
"v_mfma_f32_16x16x32_bf16 v[100:103], a[220:223], a[172:175], v[100:103]\n\t"
"s_mov_b32 s20, 946176\n\t"
"v_mfma_f32_16x16x32_bf16 v[100:103], a[236:239], a[176:179], v[100:103]\n\t"
"buffer_load_dwordx4 a[212:215], v252, s[8:11], s20 offen offset:0 sc1\n\t"
"v_mfma_f32_16x16x32_bf16 v[104:107], a[216:219], a[172:175], v[104:107]\n\t"
"s_waitcnt lgkmcnt(5)\n\t"
"v_mfma_f32_16x16x32_bf16 v[104:107], a[220:223], a[176:179], v[104:107]\n\t"
"v_cvt_pk_bf16_f32 v224, v224, v225\n\t"
"v_mfma_f32_16x16x32_bf16 v[104:107], a[236:239], a[180:183], v[104:107]\n\t"
"s_waitcnt lgkmcnt(0)\n\t"
"s_barrier\n\t"
"v_mfma_f32_16x16x32_bf16 v[108:111], a[216:219], a[176:179], v[108:111]\n\t"
"v_cvt_pk_bf16_f32 v225, v226, v227\n\t"
"v_mfma_f32_16x16x32_bf16 v[108:111], a[220:223], a[180:183], v[108:111]\n\t"
"v_mfma_f32_16x16x32_bf16 v[108:111], a[236:239], a[184:187], v[108:111]\n\t"
"ds_write2st64_b64 v245, v[228:229], v[230:231] offset0:0 offset1:1\n\t"
"v_mfma_f32_16x16x32_bf16 v[112:115], a[216:219], a[180:183], v[112:115]\n\t"
"v_mfma_f32_16x16x32_bf16 v[112:115], a[220:223], a[184:187], v[112:115]\n\t"
"ds_read_b64_tr_b16 v[228:229], v246 offset:33792\n\t"
"v_mfma_f32_16x16x32_bf16 v[112:115], a[236:239], a[188:191], v[112:115]\n\t"
"ds_write2st64_b64 v245, v[232:233], v[234:235] offset0:2 offset1:3\n\t"
"v_mfma_f32_16x16x32_bf16 v[116:119], a[216:219], a[184:187], v[116:119]\n\t"
"v_mfma_f32_16x16x32_bf16 v[116:119], a[220:223], a[188:191], v[116:119]\n\t"
"ds_read_b64_tr_b16 v[230:231], v246 offset:36864\n\t"
"v_mfma_f32_16x16x32_bf16 v[116:119], a[236:239], a[192:195], v[116:119]\n\t"
"s_add_u32 m0, s24, 66304\n\t"
"s_add_u32 s28, s21, 1966080\n\t"
"ds_write_b64 v245, v[224:225] offset:2048\n\t"
"v_mfma_f32_16x16x32_bf16 v[120:123], a[216:219], a[188:191], v[120:123]\n\t"
"ds_read_b64_tr_b16 v[232:233], v246 offset:41472\n\t"
"v_mfma_f32_16x16x32_bf16 v[120:123], a[220:223], a[192:195], v[120:123]\n\t"
"ds_read_b64_tr_b16 v[234:235], v246 offset:44544\n\t"
"v_mfma_f32_16x16x32_bf16 v[120:123], a[236:239], a[196:199], v[120:123]\n\t"
"s_waitcnt lgkmcnt(3)\n\t"
"ds_read_b64_tr_b16 v[224:225], v246 offset:47616\n\t"
"v_mfma_f32_16x16x32_bf16 v[228:231], v[228:231], v[220:223], 0\n\t"
"ds_read_b64_tr_b16 v[226:227], v246 offset:50688\n\t"
"v_mfma_f32_16x16x32_bf16 v[124:127], a[216:219], a[192:195], v[124:127]\n\t"
"buffer_load_dwordx4 v247, s[12:15], s28 offen offset:256 nt lds\n\t"
"v_mfma_f32_16x16x32_bf16 v[124:127], a[220:223], a[196:199], v[124:127]\n\t"
"s_add_u32 m0, s24, 72448\n\t"
"s_add_u32 s28, s21, 2162688\n\t"
"v_mfma_f32_16x16x32_bf16 v[124:127], a[236:239], a[200:203], v[124:127]\n\t"
"s_waitcnt lgkmcnt(2)\n\t"
"buffer_load_dwordx4 v247, s[12:15], s28 offen offset:256 nt lds\n\t"
"s_add_u32 m0, s24, 80128\n\t"
"s_add_u32 s28, s21, 2359296\n\t"
"v_mfma_f32_16x16x32_bf16 v[232:235], v[232:235], v[220:223], 0\n\t"
"buffer_load_dwordx4 v247, s[12:15], s28 offen offset:256 nt lds\n\t"
"v_mfma_f32_16x16x32_bf16 v[128:131], a[216:219], a[196:199], v[128:131]\n\t"
"s_waitcnt lgkmcnt(0)\n\t"
"s_add_u32 m0, s24, 86272\n\t"
"s_add_u32 s28, s21, 2555904\n\t"
"v_mfma_f32_16x16x32_bf16 v[128:131], a[220:223], a[200:203], v[128:131]\n\t"
"buffer_load_dwordx4 v247, s[12:15], s28 offen offset:256 nt lds\n\t"
"v_mfma_f32_16x16x32_bf16 v[128:131], a[236:239], a[204:207], v[128:131]\n\t"
"s_waitcnt vmcnt(14)\n\t"
"s_add_u32 m0, s24, 32512\n\t"
"s_add_u32 s28, s21, 983040\n\t"
"v_mfma_f32_16x16x32_bf16 v[224:227], v[224:227], v[220:223], 0\n\t"
"buffer_load_dwordx4 v247, s[12:15], s28 offen offset:256 nt lds\n\t"
"v_cvt_pk_bf16_f32 v228, v228, v229\n\t"
"v_cvt_pk_bf16_f32 v229, v230, v231\n\t"
"v_mfma_f32_16x16x32_bf16 v[132:135], a[240:243], a[168:171], v[132:135]\n\t"
"s_add_u32 m0, s24, 40192\n\t"
"s_add_u32 s28, s21, 1179648\n\t"
"v_cvt_pk_bf16_f32 v230, v232, v233\n\t"
"v_mfma_f32_16x16x32_bf16 v[132:135], a[244:247], a[172:175], v[132:135]\n\t"
"v_cvt_pk_bf16_f32 v231, v234, v235\n\t"
"v_mfma_f32_16x16x32_bf16 v[132:135], v[240:243], a[176:179], v[132:135]\n\t"
"buffer_load_dwordx4 v247, s[12:15], s28 offen offset:256 nt lds\n\t"
"v_mfma_f32_16x16x32_bf16 v[136:139], a[240:243], a[172:175], v[136:139]\n\t"
"ds_read_b64_tr_b16 v[232:233], v246 offset:53760\n\t"
"v_mfma_f32_16x16x32_bf16 v[136:139], a[244:247], a[176:179], v[136:139]\n\t"
"ds_read_b64_tr_b16 v[234:235], v246 offset:56832\n\t"
"v_cvt_pk_bf16_f32 v224, v224, v225\n\t"
"v_mfma_f32_16x16x32_bf16 v[136:139], v[240:243], a[180:183], v[136:139]\n\t"
"ds_write2st64_b64 v245, v[228:229], v[230:231] offset0:5 offset1:6\n\t"
"v_mfma_f32_16x16x32_bf16 v[140:143], a[240:243], a[176:179], v[140:143]\n\t"
"v_cvt_pk_bf16_f32 v225, v226, v227\n\t"
"v_mfma_f32_16x16x32_bf16 v[140:143], a[244:247], a[180:183], v[140:143]\n\t"
"v_mfma_f32_16x16x32_bf16 v[140:143], v[240:243], a[184:187], v[140:143]\n\t"
"v_mfma_f32_16x16x32_bf16 v[144:147], a[240:243], a[180:183], v[144:147]\n\t"
"s_waitcnt lgkmcnt(1)\n\t"
"v_mfma_f32_16x16x32_bf16 v[144:147], a[244:247], a[184:187], v[144:147]\n\t"
"v_mfma_f32_16x16x32_bf16 v[144:147], v[240:243], a[188:191], v[144:147]\n\t"
"v_mfma_f32_16x16x32_bf16 v[148:151], a[240:243], a[184:187], v[148:151]\n\t"
"v_mfma_f32_16x16x32_bf16 v[148:151], a[244:247], a[188:191], v[148:151]\n\t"
"v_mfma_f32_16x16x32_bf16 v[148:151], v[240:243], a[192:195], v[148:151]\n\t"
"Lpg6_%=:\n\t"
"s_cbranch_vccnz Lpg10_%=\n\t"
"v_mfma_f32_16x16x32_bf16 v[232:235], v[232:235], v[220:223], 0\n\t"
"v_mfma_f32_16x16x32_bf16 v[152:155], a[240:243], a[188:191], v[152:155]\n\t"
"v_mfma_f32_16x16x32_bf16 v[152:155], a[244:247], a[192:195], v[152:155]\n\t"
"v_mfma_f32_16x16x32_bf16 v[152:155], v[240:243], a[196:199], v[152:155]\n\t"
"v_mfma_f32_16x16x32_bf16 v[156:159], a[240:243], a[192:195], v[156:159]\n\t"
"v_mfma_f32_16x16x32_bf16 v[156:159], a[244:247], a[196:199], v[156:159]\n\t"
"v_cvt_pk_bf16_f32 v226, v232, v233\n\t"
"v_cvt_pk_bf16_f32 v227, v234, v235\n\t"
"v_mfma_f32_16x16x32_bf16 v[156:159], v[240:243], a[200:203], v[156:159]\n\t"
"ds_write2st64_b64 v245, v[224:225], v[226:227] offset0:7 offset1:8\n\t"
"v_mfma_f32_16x16x32_bf16 v[160:163], a[240:243], a[196:199], v[160:163]\n\t"
"s_waitcnt vmcnt(13)\n\t"
"v_mfma_f32_16x16x32_bf16 v[160:163], a[244:247], a[200:203], v[160:163]\n\t"
"v_mfma_f32_16x16x32_bf16 v[160:163], v[240:243], a[204:207], v[160:163]\n\t"
"v_mfma_f32_16x16x32_bf16 v[164:167], v[96:99], a[168:171], v[164:167]\n\t"
"v_mfma_f32_16x16x32_bf16 v[164:167], a[224:227], a[172:175], v[164:167]\n\t"
"v_mfma_f32_16x16x32_bf16 v[164:167], a[248:251], a[176:179], v[164:167]\n\t"
"s_waitcnt lgkmcnt(0)\n\t"
"s_barrier\n\t"
"v_mfma_f32_16x16x32_bf16 v[168:171], v[96:99], a[172:175], v[168:171]\n\t"
"ds_read_b128 a[168:171], v244 offset:0\n\t"
"v_mfma_f32_16x16x32_bf16 v[168:171], a[224:227], a[176:179], v[168:171]\n\t"
"buffer_load_dwordx4 v[240:243], v252, s[8:11], s20 offen offset:1024 sc1\n\t"
"v_mfma_f32_16x16x32_bf16 v[168:171], a[248:251], a[180:183], v[168:171]\n\t"
"ds_read_b128 a[172:175], v244 offset:512\n\t"
"v_mfma_f32_16x16x32_bf16 v[172:175], v[96:99], a[176:179], v[172:175]\n\t"
"ds_read_b128 a[176:179], v244 offset:1024\n\t"
"v_mfma_f32_16x16x32_bf16 v[172:175], a[224:227], a[180:183], v[172:175]\n\t"
"v_mfma_f32_16x16x32_bf16 v[172:175], a[248:251], a[184:187], v[172:175]\n\t"
"v_mfma_f32_16x16x32_bf16 v[176:179], v[96:99], a[180:183], v[176:179]\n\t"
"ds_read_b128 a[180:183], v244 offset:1536\n\t"
"v_mfma_f32_16x16x32_bf16 v[176:179], a[224:227], a[184:187], v[176:179]\n\t"
"v_mfma_f32_16x16x32_bf16 v[176:179], a[248:251], a[188:191], v[176:179]\n\t"
"v_mfma_f32_16x16x32_bf16 v[180:183], v[96:99], a[184:187], v[180:183]\n\t"
"ds_read_b128 a[184:187], v244 offset:2048\n\t"
"v_mfma_f32_16x16x32_bf16 v[180:183], a[224:227], a[188:191], v[180:183]\n\t"
"v_mfma_f32_16x16x32_bf16 v[180:183], a[248:251], a[192:195], v[180:183]\n\t"
"v_mfma_f32_16x16x32_bf16 v[184:187], v[96:99], a[188:191], v[184:187]\n\t"
"ds_read_b128 a[188:191], v244 offset:2560\n\t"
"v_mfma_f32_16x16x32_bf16 v[184:187], a[224:227], a[192:195], v[184:187]\n\t"
"v_mfma_f32_16x16x32_bf16 v[184:187], a[248:251], a[196:199], v[184:187]\n\t"
"v_mfma_f32_16x16x32_bf16 v[188:191], v[96:99], a[192:195], v[188:191]\n\t"
"ds_read_b128 a[192:195], v244 offset:3072\n\t"
"v_mfma_f32_16x16x32_bf16 v[188:191], a[224:227], a[196:199], v[188:191]\n\t"
"v_mfma_f32_16x16x32_bf16 v[188:191], a[248:251], a[200:203], v[188:191]\n\t"
"v_mfma_f32_16x16x32_bf16 v[192:195], v[96:99], a[196:199], v[192:195]\n\t"
"s_waitcnt vmcnt(11) lgkmcnt(4)\n\t"
"ds_read_b128 a[196:199], v244 offset:3584\n\t"
"v_mfma_f32_16x16x32_bf16 v[192:195], a[224:227], a[200:203], v[192:195]\n\t"
"ds_read_b128 a[200:203], v244 offset:4096\n\t"
"v_mfma_f32_16x16x32_bf16 v[192:195], a[248:251], a[204:207], v[192:195]\n\t"
"ds_read_b128 a[204:207], v244 offset:4608\n\t"
"Lst8_%=:\n\t"
"v_mfma_f32_16x16x32_bf16 v[0:3], a[252:255], a[168:171], v[0:3]\n\t"
"s_mov_b64 vcc, -1\n\t"
"s_bitcmp1_b32 s73, 1\n\t"
"s_cbranch_scc1 Lvh1_%=\n\t"
"s_bitcmp1_b32 s73, 0\n\t"
"s_cbranch_scc1 Lpg14_%=\n\t"
"s_branch Lpg13_%=\n\t"
"Lvh1_%=:\n\t"
"s_bitcmp1_b32 s73, 0\n\t"
"s_cbranch_scc1 Lpg16_%=\n\t"
"s_branch Lpg15_%=\n\t"
"Lvr1_%=:\n\t"
"s_mov_b64 vcc, 0\n\t"
"s_waitcnt lgkmcnt(6)\n\t"
"v_xor_b32 v246, 0xc0, v246\n\t"
"v_mfma_f32_16x16x32_bf16 v[0:3], v[236:239], a[172:175], v[0:3]\n\t"
"v_mfma_f32_16x16x32_bf16 v[0:3], v[248:251], a[176:179], v[0:3]\n\t"
"v_mfma_f32_16x16x32_bf16 v[4:7], a[252:255], a[172:175], v[4:7]\n\t"
"s_waitcnt lgkmcnt(5)\n\t"
"v_mfma_f32_16x16x32_bf16 v[4:7], v[236:239], a[176:179], v[4:7]\n\t"
"v_mfma_f32_16x16x32_bf16 v[4:7], v[248:251], a[180:183], v[4:7]\n\t"
"v_mfma_f32_16x16x32_bf16 v[8:11], a[252:255], a[176:179], v[8:11]\n\t"
"s_waitcnt lgkmcnt(4)\n\t"
"v_mfma_f32_16x16x32_bf16 v[8:11], v[236:239], a[180:183], v[8:11]\n\t"
"v_mfma_f32_16x16x32_bf16 v[8:11], v[248:251], a[184:187], v[8:11]\n\t"
"v_mfma_f32_16x16x32_bf16 v[12:15], a[252:255], a[180:183], v[12:15]\n\t"
"s_waitcnt lgkmcnt(3)\n\t"
"v_mfma_f32_16x16x32_bf16 v[12:15], v[236:239], a[184:187], v[12:15]\n\t"
"v_mfma_f32_16x16x32_bf16 v[12:15], v[248:251], a[188:191], v[12:15]\n\t"
"buffer_load_dwordx4 v[96:99], v252, s[8:11], s20 offen offset:2048 sc1\n\t"
"v_mfma_f32_16x16x32_bf16 v[16:19], a[252:255], a[184:187], v[16:19]\n\t"
"s_waitcnt lgkmcnt(2)\n\t"
"s_mov_b32 s20, 958464\n\t"
"v_mfma_f32_16x16x32_bf16 v[16:19], v[236:239], a[188:191], v[16:19]\n\t"
"v_mfma_f32_16x16x32_bf16 v[16:19], v[248:251], a[192:195], v[16:19]\n\t"
"ds_read_b64_tr_b16 v[228:229], v246 offset:1536\n\t"
"v_mfma_f32_16x16x32_bf16 v[20:23], a[252:255], a[188:191], v[20:23]\n\t"
"buffer_load_dwordx4 a[216:219], v252, s[8:11], s20 offen offset:0 sc1\n\t"
"v_mfma_f32_16x16x32_bf16 v[20:23], v[236:239], a[192:195], v[20:23]\n\t"
"s_waitcnt lgkmcnt(2)\n\t"
"ds_read_b64_tr_b16 v[230:231], v246 offset:4608\n\t"
"v_mfma_f32_16x16x32_bf16 v[20:23], v[248:251], a[196:199], v[20:23]\n\t"
"v_mfma_f32_16x16x32_bf16 v[24:27], a[252:255], a[192:195], v[24:27]\n\t"
"buffer_load_dwordx4 a[220:223], v252, s[8:11], s20 offen offset:1024 sc1\n\t"
"v_mfma_f32_16x16x32_bf16 v[24:27], v[236:239], a[196:199], v[24:27]\n\t"
"s_waitcnt lgkmcnt(2)\n\t"
"v_mfma_f32_16x16x32_bf16 v[24:27], v[248:251], a[200:203], v[24:27]\n\t"
"v_mfma_f32_16x16x32_bf16 v[28:31], a[252:255], a[196:199], v[28:31]\n\t"
"s_waitcnt vmcnt(11)\n\t"
"ds_read_b64_tr_b16 v[232:233], v246 offset:7680\n\t"
"v_mfma_f32_16x16x32_bf16 v[28:31], v[236:239], a[200:203], v[28:31]\n\t"
"buffer_load_dwordx4 v[236:239], v252, s[8:11], s20 offen offset:2048 sc1\n\t"
"v_mfma_f32_16x16x32_bf16 v[28:31], v[248:251], a[204:207], v[28:31]\n\t"
"s_mov_b32 s20, 970752\n\t"
"ds_read_b64_tr_b16 v[234:235], v246 offset:10752\n\t"
"v_mfma_f32_16x16x32_bf16 v[32:35], a[228:231], a[168:171], v[32:35]\n\t"
"v_mfma_f32_16x16x32_bf16 v[32:35], a[232:235], a[172:175], v[32:35]\n\t"
"v_mfma_f32_16x16x32_bf16 v[32:35], a[208:211], a[176:179], v[32:35]\n\t"
"buffer_load_dwordx4 v[248:251], v252, s[8:11], s20 offen offset:0 sc1\n\t"
"v_mfma_f32_16x16x32_bf16 v[36:39], a[228:231], a[172:175], v[36:39]\n\t"
"s_waitcnt lgkmcnt(2)\n\t"
"v_mfma_f32_16x16x32_bf16 v[36:39], a[232:235], a[176:179], v[36:39]\n\t"
"v_mfma_f32_16x16x32_bf16 v[36:39], a[208:211], a[180:183], v[36:39]\n\t"
"v_mfma_f32_16x16x32_bf16 v[228:231], v[228:231], v[220:223], 0\n\t"
"buffer_load_dwordx4 a[224:227], v252, s[8:11], s20 offen offset:1024 sc1\n\t"
"v_mfma_f32_16x16x32_bf16 v[40:43], a[228:231], a[176:179], v[40:43]\n\t"
"ds_read_b64_tr_b16 v[224:225], v246 offset:13824\n\t"
"v_mfma_f32_16x16x32_bf16 v[40:43], a[232:235], a[180:183], v[40:43]\n\t"
"ds_read_b64_tr_b16 v[226:227], v246 offset:16896\n\t"
"v_mfma_f32_16x16x32_bf16 v[40:43], a[208:211], a[184:187], v[40:43]\n\t"
"v_mfma_f32_16x16x32_bf16 v[44:47], a[228:231], a[180:183], v[44:47]\n\t"
"buffer_load_dwordx4 a[236:239], v252, s[8:11], s20 offen offset:2048 sc1\n\t"
"v_mfma_f32_16x16x32_bf16 v[44:47], a[232:235], a[184:187], v[44:47]\n\t"
"s_waitcnt lgkmcnt(2)\n\t"
"v_cvt_pk_bf16_f32 v228, v228, v229\n\t"
"v_cvt_pk_bf16_f32 v229, v230, v231\n\t"
"v_mfma_f32_16x16x32_bf16 v[44:47], a[208:211], a[188:191], v[44:47]\n\t"
"s_mov_b32 s20, 983040\n\t"
"v_mfma_f32_16x16x32_bf16 v[232:235], v[232:235], v[220:223], 0\n\t"
"v_mfma_f32_16x16x32_bf16 v[48:51], a[228:231], a[184:187], v[48:51]\n\t"
"buffer_load_dwordx4 a[240:243], v252, s[8:11], s20 offen offset:0 sc1\n\t"
"v_mfma_f32_16x16x32_bf16 v[48:51], a[232:235], a[188:191], v[48:51]\n\t"
"v_mfma_f32_16x16x32_bf16 v[48:51], a[208:211], a[192:195], v[48:51]\n\t"
"v_mfma_f32_16x16x32_bf16 v[52:55], a[228:231], a[188:191], v[52:55]\n\t"
"v_mfma_f32_16x16x32_bf16 v[52:55], a[232:235], a[192:195], v[52:55]\n\t"
"buffer_load_dwordx4 a[244:247], v252, s[8:11], s20 offen offset:1024 sc1\n\t"
"v_mfma_f32_16x16x32_bf16 v[52:55], a[208:211], a[196:199], v[52:55]\n\t"
"v_cvt_pk_bf16_f32 v230, v232, v233\n\t"
"v_cvt_pk_bf16_f32 v231, v234, v235\n\t"
"v_mfma_f32_16x16x32_bf16 v[56:59], a[228:231], a[192:195], v[56:59]\n\t"
"s_waitcnt lgkmcnt(0)\n\t"
"ds_read_b64_tr_b16 v[232:233], v246 offset:21504\n\t"
"v_mfma_f32_16x16x32_bf16 v[56:59], a[232:235], a[196:199], v[56:59]\n\t"
"ds_read_b64_tr_b16 v[234:235], v246 offset:24576\n\t"
"v_mfma_f32_16x16x32_bf16 v[56:59], a[208:211], a[200:203], v[56:59]\n\t"
"buffer_load_dwordx4 a[248:251], v252, s[8:11], s20 offen offset:2048 sc1\n\t"
"v_mfma_f32_16x16x32_bf16 v[224:227], v[224:227], v[220:223], 0\n\t"
"s_mov_b32 s20, 995328\n\t"
"v_mfma_f32_16x16x32_bf16 v[60:63], a[228:231], a[196:199], v[60:63]\n\t"
"s_waitcnt vmcnt(9)\n\t"
"v_mfma_f32_16x16x32_bf16 v[60:63], a[232:235], a[200:203], v[60:63]\n\t"
"v_mfma_f32_16x16x32_bf16 v[60:63], a[208:211], a[204:207], v[60:63]\n\t"
"buffer_load_dwordx4 a[228:231], v252, s[8:11], s20 offen offset:0 sc1\n\t"
"v_mfma_f32_16x16x32_bf16 v[64:67], a[212:215], a[168:171], v[64:67]\n\t"
"ds_read_b128 a[168:171], v244 offset:9216\n\t"
"v_mfma_f32_16x16x32_bf16 v[64:67], v[240:243], a[172:175], v[64:67]\n\t"
"v_cvt_pk_bf16_f32 v224, v224, v225\n\t"
"v_cvt_pk_bf16_f32 v225, v226, v227\n\t"
"v_mfma_f32_16x16x32_bf16 v[64:67], v[96:99], a[176:179], v[64:67]\n\t"
"v_mfma_f32_16x16x32_bf16 v[68:71], a[212:215], a[172:175], v[68:71]\n\t"
"buffer_load_dwordx4 a[232:235], v252, s[8:11], s20 offen offset:1024 sc1\n\t"
"v_mfma_f32_16x16x32_bf16 v[68:71], v[240:243], a[176:179], v[68:71]\n\t"
"s_waitcnt lgkmcnt(1)\n\t"
"ds_read_b128 a[172:175], v244 offset:9728\n\t"
"v_mfma_f32_16x16x32_bf16 v[68:71], v[96:99], a[180:183], v[68:71]\n\t"
"buffer_load_dwordx4 a[252:255], v252, s[8:11], s20 offen offset:2048 sc1\n\t"
"v_mfma_f32_16x16x32_bf16 v[232:235], v[232:235], v[220:223], 0\n\t"
"v_mfma_f32_16x16x32_bf16 v[72:75], a[212:215], a[176:179], v[72:75]\n\t"
"ds_read_b128 a[176:179], v244 offset:10240\n\t"
"v_mfma_f32_16x16x32_bf16 v[72:75], v[240:243], a[180:183], v[72:75]\n\t"
"v_mfma_f32_16x16x32_bf16 v[72:75], v[96:99], a[184:187], v[72:75]\n\t"
"v_mfma_f32_16x16x32_bf16 v[76:79], a[212:215], a[180:183], v[76:79]\n\t"
"ds_read_b128 a[180:183], v244 offset:10752\n\t"
"v_mfma_f32_16x16x32_bf16 v[76:79], v[240:243], a[184:187], v[76:79]\n\t"
"v_cvt_pk_bf16_f32 v226, v232, v233\n\t"
"v_cvt_pk_bf16_f32 v227, v234, v235\n\t"
"v_mfma_f32_16x16x32_bf16 v[76:79], v[96:99], a[188:191], v[76:79]\n\t"
"ds_read_b64_tr_b16 v[232:233], v246 offset:27648\n\t"
"v_mfma_f32_16x16x32_bf16 v[80:83], a[212:215], a[184:187], v[80:83]\n\t"
"ds_read_b64_tr_b16 v[234:235], v246 offset:30720\n\t"
"v_mfma_f32_16x16x32_bf16 v[80:83], v[240:243], a[188:191], v[80:83]\n\t"
"ds_read_b128 a[184:187], v244 offset:11264\n\t"
"v_mfma_f32_16x16x32_bf16 v[80:83], v[96:99], a[192:195], v[80:83]\n\t"
"v_mfma_f32_16x16x32_bf16 v[84:87], a[212:215], a[188:191], v[84:87]\n\t"
"s_mov_b32 s20, 1007616\n\t"
"ds_read_b128 a[188:191], v244 offset:11776\n\t"
"v_mfma_f32_16x16x32_bf16 v[84:87], v[240:243], a[192:195], v[84:87]\n\t"
"v_mfma_f32_16x16x32_bf16 v[84:87], v[96:99], a[196:199], v[84:87]\n\t"
"v_mfma_f32_16x16x32_bf16 v[88:91], a[212:215], a[192:195], v[88:91]\n\t"
"ds_read_b128 a[192:195], v244 offset:12288\n\t"
"v_mfma_f32_16x16x32_bf16 v[88:91], v[240:243], a[196:199], v[88:91]\n\t"
"v_mfma_f32_16x16x32_bf16 v[88:91], v[96:99], a[200:203], v[88:91]\n\t"
"v_mfma_f32_16x16x32_bf16 v[92:95], a[212:215], a[196:199], v[92:95]\n\t"
"buffer_load_dwordx4 a[212:215], v252, s[8:11], s20 offen offset:0 sc1\n\t"
"v_mfma_f32_16x16x32_bf16 v[92:95], v[240:243], a[200:203], v[92:95]\n\t"
"s_waitcnt vmcnt(10) lgkmcnt(3)\n\t"
"ds_read_b128 a[196:199], v244 offset:12800\n\t"
"v_mfma_f32_16x16x32_bf16 v[92:95], v[96:99], a[204:207], v[92:95]\n\t"
"ds_read_b128 a[200:203], v244 offset:13312\n\t"
"v_mfma_f32_16x16x32_bf16 v[232:235], v[232:235], v[220:223], 0\n\t"
"ds_read_b128 a[204:207], v244 offset:13824\n\t"
"v_mfma_f32_16x16x32_bf16 v[100:103], a[216:219], a[168:171], v[100:103]\n\t"
"buffer_load_dwordx4 v[96:99], v252, s[8:11], s20 offen offset:1024 sc1\n\t"
"v_mfma_f32_16x16x32_bf16 v[100:103], a[220:223], a[172:175], v[100:103]\n\t"
"v_mfma_f32_16x16x32_bf16 v[100:103], v[236:239], a[176:179], v[100:103]\n\t"
"v_mfma_f32_16x16x32_bf16 v[104:107], a[216:219], a[172:175], v[104:107]\n\t"
"s_waitcnt lgkmcnt(5)\n\t"
"v_mfma_f32_16x16x32_bf16 v[104:107], a[220:223], a[176:179], v[104:107]\n\t"
"v_cvt_pk_bf16_f32 v232, v232, v233\n\t"
"v_mfma_f32_16x16x32_bf16 v[104:107], v[236:239], a[180:183], v[104:107]\n\t"
"s_waitcnt lgkmcnt(0)\n\t"
"s_barrier\n\t"
"v_mfma_f32_16x16x32_bf16 v[108:111], a[216:219], a[176:179], v[108:111]\n\t"
"v_cvt_pk_bf16_f32 v233, v234, v235\n\t"
"v_mfma_f32_16x16x32_bf16 v[108:111], a[220:223], a[180:183], v[108:111]\n\t"
"v_mfma_f32_16x16x32_bf16 v[108:111], v[236:239], a[184:187], v[108:111]\n\t"
"ds_write2st64_b64 v245, v[228:229], v[230:231] offset0:0 offset1:1\n\t"
"v_mfma_f32_16x16x32_bf16 v[112:115], a[216:219], a[180:183], v[112:115]\n\t"
"v_mfma_f32_16x16x32_bf16 v[112:115], a[220:223], a[184:187], v[112:115]\n\t"
"ds_read_b64_tr_b16 v[228:229], v246 offset:33792\n\t"
"v_mfma_f32_16x16x32_bf16 v[112:115], v[236:239], a[188:191], v[112:115]\n\t"
"ds_write2st64_b64 v245, v[224:225], v[226:227] offset0:2 offset1:3\n\t"
"v_mfma_f32_16x16x32_bf16 v[116:119], a[216:219], a[184:187], v[116:119]\n\t"
"global_load_dwordx4 v[240:243], v252, s[48:49] offset:2048\n\t"
"v_mfma_f32_16x16x32_bf16 v[116:119], a[220:223], a[188:191], v[116:119]\n\t"
"ds_read_b64_tr_b16 v[230:231], v246 offset:36864\n\t"
"v_mfma_f32_16x16x32_bf16 v[116:119], v[236:239], a[192:195], v[116:119]\n\t"
"ds_write_b64 v245, v[232:233] offset:2048\n\t"
"v_mfma_f32_16x16x32_bf16 v[120:123], a[216:219], a[188:191], v[120:123]\n\t"
"ds_read_b64_tr_b16 v[224:225], v246 offset:41472\n\t"
"v_mfma_f32_16x16x32_bf16 v[120:123], a[220:223], a[192:195], v[120:123]\n\t"
"ds_read_b64_tr_b16 v[226:227], v246 offset:44544\n\t"
"v_mfma_f32_16x16x32_bf16 v[120:123], v[236:239], a[196:199], v[120:123]\n\t"
"s_waitcnt lgkmcnt(3)\n\t"
"ds_read_b64_tr_b16 v[232:233], v246 offset:47616\n\t"
"v_mfma_f32_16x16x32_bf16 v[228:231], v[228:231], v[220:223], 0\n\t"
"ds_read_b64_tr_b16 v[234:235], v246 offset:50688\n\t"
"v_mfma_f32_16x16x32_bf16 v[124:127], a[216:219], a[192:195], v[124:127]\n\t"
"global_load_dwordx4 a[208:211], v252, s[46:47] offset:1024\n\t"
"v_mfma_f32_16x16x32_bf16 v[124:127], a[220:223], a[196:199], v[124:127]\n\t"
"v_mfma_f32_16x16x32_bf16 v[124:127], v[236:239], a[200:203], v[124:127]\n\t"
"s_waitcnt lgkmcnt(2)\n\t"
"v_mfma_f32_16x16x32_bf16 v[224:227], v[224:227], v[220:223], 0\n\t"
"v_mfma_f32_16x16x32_bf16 v[128:131], a[216:219], a[196:199], v[128:131]\n\t"
"buffer_load_dwordx4 a[216:219], v252, s[8:11], s20 offen offset:2048 sc1\n\t"
"v_mfma_f32_16x16x32_bf16 v[128:131], a[220:223], a[200:203], v[128:131]\n\t"
"s_waitcnt vmcnt(11) lgkmcnt(0)\n\t"
"s_mov_b32 s20, 1019904\n\t"
"v_cvt_pk_bf16_f32 v228, v228, v229\n\t"
"v_mfma_f32_16x16x32_bf16 v[128:131], v[236:239], a[204:207], v[128:131]\n\t"
"v_cvt_pk_bf16_f32 v229, v230, v231\n\t"
"v_mfma_f32_16x16x32_bf16 v[232:235], v[232:235], v[220:223], 0\n\t"
"buffer_load_dwordx4 a[220:223], v252, s[8:11], s20 offen offset:0 sc1\n\t"
"v_mfma_f32_16x16x32_bf16 v[132:135], v[248:251], a[168:171], v[132:135]\n\t"
"s_add_u32 m0, s24, 46336\n\t"
"s_add_u32 s28, s21, 1376256\n\t"
"v_cvt_pk_bf16_f32 v230, v224, v225\n\t"
"v_mfma_f32_16x16x32_bf16 v[132:135], a[224:227], a[172:175], v[132:135]\n\t"
"v_cvt_pk_bf16_f32 v231, v226, v227\n\t"
"v_mfma_f32_16x16x32_bf16 v[132:135], a[236:239], a[176:179], v[132:135]\n\t"
"ds_write2st64_b64 v245, v[228:229], v[230:231] offset0:5 offset1:6\n\t"
"v_mfma_f32_16x16x32_bf16 v[136:139], v[248:251], a[172:175], v[136:139]\n\t"
"buffer_load_dwordx4 v247, s[12:15], s28 offen offset:256 nt lds\n\t"
"v_mfma_f32_16x16x32_bf16 v[136:139], a[224:227], a[176:179], v[136:139]\n\t"
"s_add_u32 m0, s24, 52480\n\t"
"s_add_u32 s28, s21, 1572864\n\t"
"ds_read_b64_tr_b16 v[224:225], v246 offset:53760\n\t"
"v_mfma_f32_16x16x32_bf16 v[136:139], a[236:239], a[180:183], v[136:139]\n\t"
"buffer_load_dwordx4 v247, s[12:15], s28 offen offset:256 nt lds\n\t"
"v_mfma_f32_16x16x32_bf16 v[140:143], v[248:251], a[176:179], v[140:143]\n\t"
"s_add_u32 m0, s24, 92416\n\t"
"s_add_u32 s28, s21, 2752512\n\t"
"ds_read_b64_tr_b16 v[226:227], v246 offset:56832\n\t"
"v_mfma_f32_16x16x32_bf16 v[140:143], a[224:227], a[180:183], v[140:143]\n\t"
"buffer_load_dwordx4 v247, s[12:15], s28 offen offset:256 nt lds\n\t"
"v_mfma_f32_16x16x32_bf16 v[140:143], a[236:239], a[184:187], v[140:143]\n\t"
"s_add_u32 m0, s24, 100096\n\t"
"s_add_u32 s28, s21, 2949120\n\t"
"v_cvt_pk_bf16_f32 v232, v232, v233\n\t"
"v_cvt_pk_bf16_f32 v233, v234, v235\n\t"
"v_mfma_f32_16x16x32_bf16 v[144:147], v[248:251], a[180:183], v[144:147]\n\t"
"v_mfma_f32_16x16x32_bf16 v[144:147], a[224:227], a[184:187], v[144:147]\n\t"
"buffer_load_dwordx4 v247, s[12:15], s28 offen offset:256 nt lds\n\t"
"v_mfma_f32_16x16x32_bf16 v[144:147], a[236:239], a[188:191], v[144:147]\n\t"
"s_add_u32 m0, s24, 106240\n\t"
"s_add_u32 s28, s21, 3145728\n\t"
"v_mfma_f32_16x16x32_bf16 v[148:151], v[248:251], a[184:187], v[148:151]\n\t"
"buffer_load_dwordx4 v247, s[12:15], s28 offen offset:256 nt lds\n\t"
"v_mfma_f32_16x16x32_bf16 v[148:151], a[224:227], a[188:191], v[148:151]\n\t"
"s_waitcnt lgkmcnt(0)\n\t"
"s_add_u32 m0, s24, 112384\n\t"
"s_add_u32 s28, s21, 3342336\n\t"
"v_mfma_f32_16x16x32_bf16 v[148:151], a[236:239], a[192:195], v[148:151]\n\t"
"buffer_load_dwordx4 v247, s[12:15], s28 offen offset:256 nt lds\n\t"
"s_add_u32 m0, s25, 256\n\t"
"v_mfma_f32_16x16x32_bf16 v[224:227], v[224:227], v[220:223], 0\n\t"
"buffer_load_dwordx4 v254, s[12:15], 0 offen offset:256 nt lds\n\t"
"v_mfma_f32_16x16x32_bf16 v[152:155], v[248:251], a[188:191], v[152:155]\n\t"
"s_add_u32 m0, s26, 256\n\t"
"v_mfma_f32_16x16x32_bf16 v[152:155], a[224:227], a[192:195], v[152:155]\n\t"
"v_mfma_f32_16x16x32_bf16 v[152:155], a[236:239], a[196:199], v[152:155]\n\t"
"s_mov_b64 exec, s[36:37]\n\t"
"buffer_load_dwordx4 v254, s[12:15], s27 offen offset:256 nt lds\n\t"
"s_mov_b64 exec, -1\n\t"
"v_mfma_f32_16x16x32_bf16 v[156:159], v[248:251], a[192:195], v[156:159]\n\t"
"s_waitcnt vmcnt(11)\n\t"
"ds_read_b64_tr_b16 v[236:237], v240 offset:1536\n\t"
"v_mfma_f32_16x16x32_bf16 v[156:159], a[224:227], a[196:199], v[156:159]\n\t"
"ds_read_b64_tr_b16 v[238:239], v240 offset:3072\n\t"
"v_cvt_pk_bf16_f32 v234, v224, v225\n\t"
"v_mfma_f32_16x16x32_bf16 v[156:159], a[236:239], a[200:203], v[156:159]\n\t"
"ds_read_b64_tr_b16 v[228:229], v241 offset:1536\n\t"
"v_cvt_pk_bf16_f32 v235, v226, v227\n\t"
"v_mfma_f32_16x16x32_bf16 v[160:163], v[248:251], a[196:199], v[160:163]\n\t"
"ds_read_b64_tr_b16 v[230:231], v241 offset:3072\n\t"
"v_mfma_f32_16x16x32_bf16 v[160:163], a[224:227], a[200:203], v[160:163]\n\t"
"v_mfma_f32_16x16x32_bf16 v[160:163], a[236:239], a[204:207], v[160:163]\n\t"
"ds_write2st64_b64 v245, v[232:233], v[234:235] offset0:7 offset1:8\n\t"
"v_mfma_f32_16x16x32_bf16 v[164:167], a[240:243], a[168:171], v[164:167]\n\t"
"v_mfma_f32_16x16x32_bf16 v[164:167], a[244:247], a[172:175], v[164:167]\n\t"
"v_mfma_f32_16x16x32_bf16 v[164:167], a[248:251], a[176:179], v[164:167]\n\t"
"s_waitcnt lgkmcnt(0)\n\t"
"s_barrier\n\t"
"v_mfma_f32_16x16x32_bf16 v[168:171], a[240:243], a[172:175], v[168:171]\n\t"
"ds_read_b64_tr_b16 v[224:225], v240 offset:21504\n\t"
"v_mfma_f32_16x16x32_bf16 v[168:171], a[244:247], a[176:179], v[168:171]\n\t"
"ds_read_b64_tr_b16 v[226:227], v240 offset:23040\n\t"
"v_mfma_f32_16x16x32_bf16 v[168:171], a[248:251], a[180:183], v[168:171]\n\t"
"ds_read_b64_tr_b16 v[232:233], v241 offset:21504\n\t"
"v_mfma_f32_16x16x32_bf16 v[172:175], a[240:243], a[176:179], v[172:175]\n\t"
"s_waitcnt vmcnt(10)\n\t"
"ds_read_b64_tr_b16 v[234:235], v241 offset:23040\n\t"
"v_mfma_f32_16x16x32_bf16 v[172:175], a[244:247], a[180:183], v[172:175]\n\t"
"ds_read_b128 a[168:171], v244 offset:0\n\t"
"v_mfma_f32_16x16x32_bf16 v[172:175], a[248:251], a[184:187], v[172:175]\n\t"
"ds_read_b128 a[172:175], v244 offset:512\n\t"
"v_mfma_f32_16x16x32_bf16 v[236:239], v[236:239], a[208:211], 0\n\t"
"ds_read_b128 a[176:179], v244 offset:1024\n\t"
"v_mfma_f32_16x16x32_bf16 v[228:231], v[228:231], a[208:211], 0\n\t"
"v_mfma_f32_16x16x32_bf16 v[176:179], a[240:243], a[180:183], v[176:179]\n\t"
"ds_read_b128 a[180:183], v244 offset:1536\n\t"
"v_mfma_f32_16x16x32_bf16 v[176:179], a[244:247], a[184:187], v[176:179]\n\t"
"v_mfma_f32_16x16x32_bf16 v[176:179], a[248:251], a[188:191], v[176:179]\n\t"
"v_mfma_f32_16x16x32_bf16 v[180:183], a[240:243], a[184:187], v[180:183]\n\t"
"s_waitcnt lgkmcnt(6)\n\t"
"ds_read_b128 a[184:187], v244 offset:2048\n\t"
"v_cvt_pk_bf16_f32 v236, v236, v237\n\t"
"v_mfma_f32_16x16x32_bf16 v[180:183], a[244:247], a[188:191], v[180:183]\n\t"
"s_waitcnt lgkmcnt(5)\n\t"
"v_cvt_pk_bf16_f32 v237, v238, v239\n\t"
"v_cvt_pk_bf16_f32 v238, v228, v229\n\t"
"v_mfma_f32_16x16x32_bf16 v[180:183], a[248:251], a[192:195], v[180:183]\n\t"
"v_cvt_pk_bf16_f32 v239, v230, v231\n\t"
"v_mfma_f32_16x16x32_bf16 v[224:227], v[224:227], a[208:211], 0\n\t"
"v_mfma_f32_16x16x32_bf16 v[232:235], v[232:235], a[208:211], 0\n\t"
"ds_read_b64_tr_b16 v[228:229], v240 offset:41472\n\t"
"v_mfma_f32_16x16x32_bf16 v[184:187], a[240:243], a[188:191], v[184:187]\n\t"
"ds_read_b128 a[188:191], v244 offset:2560\n\t"
"v_mfma_f32_16x16x32_bf16 v[184:187], a[244:247], a[192:195], v[184:187]\n\t"
"ds_read_b64_tr_b16 v[230:231], v240 offset:43008\n\t"
"v_mfma_f32_16x16x32_bf16 v[184:187], a[248:251], a[196:199], v[184:187]\n\t"
"v_mfma_f32_16x16x32_bf16 v[188:191], a[240:243], a[192:195], v[188:191]\n\t"
"ds_read_b128 a[192:195], v244 offset:3072\n\t"
"v_cvt_pk_bf16_f32 v224, v224, v225\n\t"
"v_mfma_f32_16x16x32_bf16 v[188:191], a[244:247], a[196:199], v[188:191]\n\t"
"v_cvt_pk_bf16_f32 v225, v226, v227\n\t"
"v_cvt_pk_bf16_f32 v226, v232, v233\n\t"
"v_mfma_f32_16x16x32_bf16 v[188:191], a[248:251], a[200:203], v[188:191]\n\t"
"v_cvt_pk_bf16_f32 v227, v234, v235\n\t"
"v_mfma_f32_16x16x32_bf16 v[192:195], a[240:243], a[196:199], v[192:195]\n\t"
"s_waitcnt lgkmcnt(6)\n\t"
"ds_read_b64_tr_b16 v[232:233], v241 offset:41472\n\t"
"v_mfma_f32_16x16x32_bf16 v[192:195], a[244:247], a[200:203], v[192:195]\n\t"
"ds_read_b64_tr_b16 v[234:235], v241 offset:43008\n\t"
"v_mfma_f32_16x16x32_bf16 v[192:195], a[248:251], a[204:207], v[192:195]\n\t"
"ds_read_b128 a[196:199], v244 offset:3584\n\t"
"Lst9_%=:\n\t"
"v_mfma_f32_16x16x32_bf16 v[196:199], a[228:231], a[168:171], v[196:199]\n\t"
"s_mov_b64 vcc, -1\n\t"
"s_bitcmp1_b32 s73, 1\n\t"
"s_cbranch_scc1 Lvh2_%=\n\t"
"s_bitcmp1_b32 s73, 0\n\t"
"s_cbranch_scc1 Lpg18_%=\n\t"
"s_branch Lpg17_%=\n\t"
"Lvh2_%=:\n\t"
"s_bitcmp1_b32 s73, 0\n\t"
"s_cbranch_scc1 Lpg20_%=\n\t"
"s_branch Lpg19_%=\n\t"
"Lvr2_%=:\n\t"
"s_mov_b64 vcc, 0\n\t"
"s_waitcnt lgkmcnt(8)\n\t"
"ds_read_b128 a[200:203], v244 offset:4096\n\t"
"v_mfma_f32_16x16x32_bf16 v[196:199], a[232:235], a[172:175], v[196:199]\n\t"
"ds_read_b128 a[204:207], v244 offset:4608\n\t"
"v_mfma_f32_16x16x32_bf16 v[196:199], a[252:255], a[176:179], v[196:199]\n\t"
"buffer_load_dwordx4 v[248:251], v252, s[8:11], s20 offen offset:1024 sc1\n\t"
"v_mfma_f32_16x16x32_bf16 v[204:207], a[228:231], a[172:175], v[204:207]\n\t"
"s_waitcnt lgkmcnt(6)\n\t"
"v_mfma_f32_16x16x32_bf16 v[204:207], a[232:235], a[176:179], v[204:207]\n\t"
"s_waitcnt lgkmcnt(3)\n\t"
"v_mfma_f32_16x16x32_bf16 v[204:207], a[252:255], a[180:183], v[204:207]\n\t"
"v_mfma_f32_16x16x32_bf16 v[228:231], v[228:231], a[208:211], 0\n\t"
"v_add_u32 v240, 59904, v240\n\t"
"v_add_u32 v241, 59904, v241\n\t"
"v_mfma_f32_16x16x32_bf16 v[232:235], v[232:235], a[208:211], 0\n\t"
"v_mfma_f32_16x16x32_bf16 v[212:215], a[228:231], a[176:179], v[212:215]\n\t"
"v_mfma_f32_16x16x32_bf16 v[212:215], a[232:235], a[180:183], v[212:215]\n\t"
"v_mfma_f32_16x16x32_bf16 v[212:215], a[252:255], a[184:187], v[212:215]\n\t"
"s_barrier\n\t"
"v_mfma_f32_16x16x32_bf16 a[0:3], a[228:231], a[180:183], a[0:3]\n\t"
"v_cvt_pk_bf16_f32 v228, v228, v229\n\t"
"v_mfma_f32_16x16x32_bf16 a[0:3], a[232:235], a[184:187], a[0:3]\n\t"
"v_cvt_pk_bf16_f32 v229, v230, v231\n\t"
"v_cvt_pk_bf16_f32 v230, v232, v233\n\t"
"v_mfma_f32_16x16x32_bf16 a[0:3], a[252:255], a[188:191], a[0:3]\n\t"
"ds_write_b128 v243, v[236:239] offset:0\n\t"
"v_mfma_f32_16x16x32_bf16 a[8:11], a[228:231], a[184:187], a[8:11]\n\t"
"s_waitcnt lgkmcnt(3)\n\t"
"v_cvt_pk_bf16_f32 v231, v234, v235\n\t"
"v_mfma_f32_16x16x32_bf16 a[8:11], a[232:235], a[188:191], a[8:11]\n\t"
"ds_read_b64_tr_b16 v[236:237], v240 offset:1536\n\t"
"v_mfma_f32_16x16x32_bf16 a[8:11], a[252:255], a[192:195], a[8:11]\n\t"
"ds_write_b128 v243, v[224:227] offset:9216\n\t"
"v_mfma_f32_16x16x32_bf16 a[16:19], a[228:231], a[188:191], a[16:19]\n\t"
"buffer_load_dwordx4 a[224:227], v252, s[8:11], s20 offen offset:2048 sc1\n\t"
"v_mfma_f32_16x16x32_bf16 a[16:19], a[232:235], a[192:195], a[16:19]\n\t"
"s_waitcnt lgkmcnt(4)\n\t"
"ds_read_b64_tr_b16 v[238:239], v240 offset:3072\n\t"
"v_mfma_f32_16x16x32_bf16 a[16:19], a[252:255], a[196:199], a[16:19]\n\t"
"s_mov_b32 s20, 1032192\n\t"
"ds_write_b128 v243, v[228:231] offset:18432\n\t"
"v_mfma_f32_16x16x32_bf16 a[24:27], a[228:231], a[192:195], a[24:27]\n\t"
"buffer_load_dwordx4 a[236:239], v252, s[8:11], s20 offen offset:0 sc1\n\t"
"v_mfma_f32_16x16x32_bf16 a[24:27], a[232:235], a[196:199], a[24:27]\n\t"
"s_waitcnt lgkmcnt(5)\n\t"
"ds_read_b64_tr_b16 v[224:225], v241 offset:1536\n\t"
"v_mfma_f32_16x16x32_bf16 a[24:27], a[252:255], a[200:203], a[24:27]\n\t"
"ds_read_b64_tr_b16 v[226:227], v241 offset:3072\n\t"
"v_mfma_f32_16x16x32_bf16 a[32:35], a[228:231], a[196:199], a[32:35]\n\t"
"s_waitcnt lgkmcnt(3)\n\t"
"ds_read_b64_tr_b16 v[232:233], v240 offset:21504\n\t"
"v_mfma_f32_16x16x32_bf16 a[32:35], a[232:235], a[200:203], a[32:35]\n\t"
"ds_read_b64_tr_b16 v[234:235], v240 offset:23040\n\t"
"v_mfma_f32_16x16x32_bf16 a[32:35], a[252:255], a[204:207], a[32:35]\n\t"
"s_waitcnt vmcnt(12)\n\t"
"ds_read_b64_tr_b16 v[228:229], v241 offset:21504\n\t"
"v_mfma_f32_16x16x32_bf16 v[236:239], v[236:239], a[208:211], 0\n\t"
"s_waitcnt lgkmcnt(3)\n\t"
"ds_read_b64_tr_b16 v[230:231], v241 offset:23040\n\t"
"v_mfma_f32_16x16x32_bf16 v[224:227], v[224:227], a[208:211], 0\n\t"
"buffer_load_dwordx4 a[228:231], v252, s[8:11], s20 offen offset:1024 sc1\n\t"
"v_mfma_f32_16x16x32_bf16 a[40:43], a[212:215], a[168:171], a[40:43]\n\t"
"buffer_load_dwordx4 a[232:235], v252, s[8:11], s20 offen offset:2048 sc1\n\t"
"v_mfma_f32_16x16x32_bf16 a[40:43], v[96:99], a[172:175], a[40:43]\n\t"
"s_mov_b32 s20, 1044480\n\t"
"v_mfma_f32_16x16x32_bf16 a[40:43], a[216:219], a[176:179], a[40:43]\n\t"
"v_mfma_f32_16x16x32_bf16 a[48:51], a[212:215], a[172:175], a[48:51]\n\t"
"s_waitcnt lgkmcnt(2)\n\t"
"v_cvt_pk_bf16_f32 v236, v236, v237\n\t"
"v_cvt_pk_bf16_f32 v237, v238, v239\n\t"
"v_mfma_f32_16x16x32_bf16 a[48:51], v[96:99], a[176:179], a[48:51]\n\t"
"s_waitcnt lgkmcnt(0)\n\t"
"v_cvt_pk_bf16_f32 v238, v224, v225\n\t"
"v_cvt_pk_bf16_f32 v239, v226, v227\n\t"
"v_mfma_f32_16x16x32_bf16 a[48:51], a[216:219], a[180:183], a[48:51]\n\t"
"ds_write_b128 v243, v[236:239] offset:27648\n\t"
"v_mfma_f32_16x16x32_bf16 v[232:235], v[232:235], a[208:211], 0\n\t"
"buffer_load_dwordx4 a[240:243], v252, s[8:11], s20 offen offset:0 sc1\n\t"
"v_mfma_f32_16x16x32_bf16 v[228:231], v[228:231], a[208:211], 0\n\t"
"ds_read_b64_tr_b16 v[224:225], v240 offset:41472\n\t"
"v_mfma_f32_16x16x32_bf16 a[56:59], a[212:215], a[176:179], a[56:59]\n\t"
"buffer_load_dwordx4 a[244:247], v252, s[8:11], s20 offen offset:1024 sc1\n\t"
"v_mfma_f32_16x16x32_bf16 a[56:59], v[96:99], a[180:183], a[56:59]\n\t"
"ds_read_b64_tr_b16 v[226:227], v240 offset:43008\n\t"
"v_mfma_f32_16x16x32_bf16 a[56:59], a[216:219], a[184:187], a[56:59]\n\t"
"ds_read_b64_tr_b16 v[236:237], v241 offset:41472\n\t"
"v_mfma_f32_16x16x32_bf16 a[64:67], a[212:215], a[180:183], a[64:67]\n\t"
"ds_read_b64_tr_b16 v[238:239], v241 offset:43008\n\t"
"v_cvt_pk_bf16_f32 v232, v232, v233\n\t"
"v_mfma_f32_16x16x32_bf16 a[64:67], v[96:99], a[184:187], a[64:67]\n\t"
"v_cvt_pk_bf16_f32 v233, v234, v235\n\t"
"v_cvt_pk_bf16_f32 v234, v228, v229\n\t"
"v_mfma_f32_16x16x32_bf16 a[64:67], a[216:219], a[188:191], a[64:67]\n\t"
"v_cvt_pk_bf16_f32 v235, v230, v231\n\t"
"v_mfma_f32_16x16x32_bf16 a[72:75], a[212:215], a[184:187], a[72:75]\n\t"
"buffer_load_dwordx4 a[248:251], v252, s[8:11], s20 offen offset:2048 sc1\n\t"
"v_mfma_f32_16x16x32_bf16 a[72:75], v[96:99], a[188:191], a[72:75]\n\t"
"s_mov_b32 s20, 1056768\n\t"
"v_mfma_f32_16x16x32_bf16 a[72:75], a[216:219], a[192:195], a[72:75]\n\t"
"ds_write_b128 v243, v[232:235] offset:36864\n\t"
"v_mfma_f32_16x16x32_bf16 a[80:83], a[212:215], a[188:191], a[80:83]\n\t"
"buffer_load_dwordx4 a[252:255], v252, s[8:11], s20 offen offset:0 sc1\n\t"
"v_mfma_f32_16x16x32_bf16 a[80:83], v[96:99], a[192:195], a[80:83]\n\t"
"v_mfma_f32_16x16x32_bf16 a[80:83], a[216:219], a[196:199], a[80:83]\n\t"
"v_mfma_f32_16x16x32_bf16 a[88:91], a[212:215], a[192:195], a[88:91]\n\t"
"s_waitcnt lgkmcnt(3)\n\t"
"v_mfma_f32_16x16x32_bf16 a[88:91], v[96:99], a[196:199], a[88:91]\n\t"
"s_waitcnt lgkmcnt(1)\n\t"
"v_mfma_f32_16x16x32_bf16 a[88:91], a[216:219], a[200:203], a[88:91]\n\t"
"v_mfma_f32_16x16x32_bf16 v[224:227], v[224:227], a[208:211], 0\n\t"
"v_mfma_f32_16x16x32_bf16 v[236:239], v[236:239], a[208:211], 0\n\t"
"ds_read_b64_tr_b16 v[228:229], v246 offset:512\n\t"
"v_mfma_f32_16x16x32_bf16 a[96:99], a[212:215], a[196:199], a[96:99]\n\t"
"buffer_load_dwordx4 a[212:215], v252, s[8:11], s20 offen offset:1024 sc1\n\t"
"v_mfma_f32_16x16x32_bf16 a[96:99], v[96:99], a[200:203], a[96:99]\n\t"
"s_waitcnt vmcnt(8)\n\t"
"ds_read_b64_tr_b16 v[230:231], v246 offset:3584\n\t"
"v_mfma_f32_16x16x32_bf16 a[96:99], a[216:219], a[204:207], a[96:99]\n\t"
"v_mfma_f32_16x16x32_bf16 a[104:107], a[220:223], a[168:171], a[104:107]\n\t"
"buffer_load_dwordx4 v[96:99], v252, s[8:11], s20 offen offset:2048 sc1\n\t"
"v_mfma_f32_16x16x32_bf16 a[104:107], v[248:251], a[172:175], a[104:107]\n\t"
"s_mov_b32 s20, 1290240\n\t"
"ds_read_b64_tr_b16 v[232:233], v246 offset:6656\n\t"
"v_cvt_pk_bf16_f32 v224, v224, v225\n\t"
"v_mfma_f32_16x16x32_bf16 a[104:107], a[224:227], a[176:179], a[104:107]\n\t"
"ds_read_b64_tr_b16 v[234:235], v246 offset:9728\n\t"
"v_cvt_pk_bf16_f32 v225, v226, v227\n\t"
"v_mfma_f32_16x16x32_bf16 a[112:115], a[220:223], a[172:175], a[112:115]\n\t"
"ds_read_b128 a[168:171], v244 offset:9216\n\t"
"v_cvt_pk_bf16_f32 v226, v236, v237\n\t"
"v_mfma_f32_16x16x32_bf16 a[112:115], v[248:251], a[176:179], a[112:115]\n\t"
"ds_read_b128 a[172:175], v244 offset:9728\n\t"
"v_cvt_pk_bf16_f32 v227, v238, v239\n\t"
"v_mfma_f32_16x16x32_bf16 a[112:115], a[224:227], a[180:183], a[112:115]\n\t"
"ds_write_b128 v243, v[224:227] offset:46080\n\t"
"v_mfma_f32_16x16x32_bf16 a[120:123], a[220:223], a[176:179], a[120:123]\n\t"
"buffer_load_dwordx4 a[216:219], v252, s[8:11], s20 offen offset:0 sc1\n\t"
"v_mfma_f32_16x16x32_bf16 a[120:123], v[248:251], a[180:183], a[120:123]\n\t"
"ds_read_b64_tr_b16 v[236:237], v246 offset:12800\n\t"
"v_mfma_f32_16x16x32_bf16 a[120:123], a[224:227], a[184:187], a[120:123]\n\t"
"ds_read_b64_tr_b16 v[238:239], v246 offset:15872\n\t"
"v_mfma_f32_16x16x32_bf16 a[128:131], a[220:223], a[180:183], a[128:131]\n\t"
"s_waitcnt lgkmcnt(7)\n\t"
"ds_read_b128 a[176:179], v244 offset:10240\n\t"
"v_mfma_f32_16x16x32_bf16 a[128:131], v[248:251], a[184:187], a[128:131]\n\t"
"ds_read_b64_tr_b16 v[224:225], v246 offset:20480\n\t"
"v_mfma_f32_16x16x32_bf16 a[128:131], a[224:227], a[188:191], a[128:131]\n\t"
"ds_read_b64_tr_b16 v[226:227], v246 offset:23552\n\t"
"v_mfma_f32_16x16x32_bf16 v[228:231], v[228:231], v[220:223], 0\n\t"
"ds_read_b128 a[180:183], v244 offset:10752\n\t"
"v_mfma_f32_16x16x32_bf16 a[136:139], a[220:223], a[184:187], a[136:139]\n\t"
"s_waitcnt lgkmcnt(9)\n\t"
"ds_read_b128 a[184:187], v244 offset:11264\n\t"
"v_mfma_f32_16x16x32_bf16 a[136:139], v[248:251], a[188:191], a[136:139]\n\t"
"v_mfma_f32_16x16x32_bf16 a[136:139], a[224:227], a[192:195], a[136:139]\n\t"
"s_waitcnt lgkmcnt(5)\n\t"
"v_mfma_f32_16x16x32_bf16 v[232:235], v[232:235], v[220:223], 0\n\t"
"s_waitcnt lgkmcnt(2)\n\t"
"v_mfma_f32_16x16x32_bf16 a[144:147], a[220:223], a[188:191], a[144:147]\n\t"
"ds_read_b128 a[188:191], v244 offset:11776\n\t"
"v_cvt_pk_bf16_f32 v228, v228, v229\n\t"
"v_mfma_f32_16x16x32_bf16 a[144:147], v[248:251], a[192:195], a[144:147]\n\t"
"Lpg7_%=:\n\t"
"s_cbranch_vccnz Lpg11_%=\n\t"
"v_cvt_pk_bf16_f32 v229, v230, v231\n\t"
"v_mfma_f32_16x16x32_bf16 a[144:147], a[224:227], a[196:199], a[144:147]\n\t"
"v_mfma_f32_16x16x32_bf16 v[236:239], v[236:239], v[220:223], 0\n\t"
"v_mfma_f32_16x16x32_bf16 a[152:155], a[220:223], a[192:195], a[152:155]\n\t"
"ds_read_b128 a[192:195], v244 offset:12288\n\t"
"v_cvt_pk_bf16_f32 v230, v232, v233\n\t"
"v_mfma_f32_16x16x32_bf16 a[152:155], v[248:251], a[196:199], a[152:155]\n\t"
"v_cvt_pk_bf16_f32 v231, v234, v235\n\t"
"v_mfma_f32_16x16x32_bf16 a[152:155], a[224:227], a[200:203], a[152:155]\n\t"
"ds_read_b64_tr_b16 v[232:233], v246 offset:26624\n\t"
"v_mfma_f32_16x16x32_bf16 v[224:227], v[224:227], v[220:223], 0\n\t"
"ds_read_b64_tr_b16 v[234:235], v246 offset:29696\n\t"
"v_mfma_f32_16x16x32_bf16 a[160:163], a[220:223], a[196:199], a[160:163]\n\t"
"buffer_load_dwordx4 a[220:223], v252, s[8:11], s20 offen offset:1024 sc1\n\t"
"v_mfma_f32_16x16x32_bf16 a[160:163], v[248:251], a[200:203], a[160:163]\n\t"
"s_waitcnt vmcnt(8)\n\t"
"ds_read_b128 a[196:199], v244 offset:12800\n\t"
"v_cvt_pk_bf16_f32 v236, v236, v237\n\t"
"v_mfma_f32_16x16x32_bf16 a[160:163], a[224:227], a[204:207], a[160:163]\n\t"
"ds_read_b128 a[200:203], v244 offset:13312\n\t"
"v_cvt_pk_bf16_f32 v237, v238, v239\n\t"
"v_mfma_f32_16x16x32_bf16 v[200:203], a[236:239], a[168:171], v[200:203]\n\t"
"ds_read_b128 a[204:207], v244 offset:13824\n\t"
"v_mfma_f32_16x16x32_bf16 v[200:203], a[228:231], a[172:175], v[200:203]\n\t"
"buffer_load_dwordx4 v[248:251], v252, s[8:11], s20 offen offset:2048 sc1\n\t"
"v_mfma_f32_16x16x32_bf16 v[200:203], a[232:235], a[176:179], v[200:203]\n\t"
"s_waitcnt lgkmcnt(3)\n\t"
"s_mov_b32 s20, 1302528\n\t"
"v_cvt_pk_bf16_f32 v238, v224, v225\n\t"
"v_mfma_f32_16x16x32_bf16 v[232:235], v[232:235], v[220:223], 0\n\t"
"v_cvt_pk_bf16_f32 v239, v226, v227\n\t"
"v_mfma_f32_16x16x32_bf16 v[208:211], a[236:239], a[172:175], v[208:211]\n\t"
"buffer_load_dwordx4 a[224:227], v252, s[8:11], s20 offen offset:0 sc1\n\t"
"v_mfma_f32_16x16x32_bf16 v[208:211], a[228:231], a[176:179], v[208:211]\n\t"
"v_mfma_f32_16x16x32_bf16 v[208:211], a[232:235], a[180:183], v[208:211]\n\t"
"s_waitcnt lgkmcnt(0)\n\t"
"s_barrier\n\t"
"v_mfma_f32_16x16x32_bf16 v[216:219], a[236:239], a[176:179], v[216:219]\n\t"
"ds_read_b64_tr_b16 v[224:225], v246 offset:32768\n\t"
"v_mfma_f32_16x16x32_bf16 v[216:219], a[228:231], a[180:183], v[216:219]\n\t"
"ds_read_b64_tr_b16 v[226:227], v246 offset:35840\n\t"
"v_cvt_pk_bf16_f32 v232, v232, v233\n\t"
"v_mfma_f32_16x16x32_bf16 v[216:219], a[232:235], a[184:187], v[216:219]\n\t"
"ds_write2st64_b64 v245, v[228:229], v[230:231] offset0:0 offset1:1\n\t"
"v_mfma_f32_16x16x32_bf16 a[4:7], a[236:239], a[180:183], a[4:7]\n\t"
"v_cvt_pk_bf16_f32 v233, v234, v235\n\t"
"v_mfma_f32_16x16x32_bf16 a[4:7], a[228:231], a[184:187], a[4:7]\n\t"
"ds_read_b64_tr_b16 v[228:229], v246 offset:40448\n\t"
"v_mfma_f32_16x16x32_bf16 a[4:7], a[232:235], a[188:191], a[4:7]\n\t"
"ds_write2st64_b64 v245, v[236:237], v[238:239] offset0:2 offset1:3\n\t"
"v_mfma_f32_16x16x32_bf16 a[12:15], a[236:239], a[184:187], a[12:15]\n\t"
"v_mfma_f32_16x16x32_bf16 a[12:15], a[228:231], a[188:191], a[12:15]\n\t"
"ds_read_b64_tr_b16 v[230:231], v246 offset:43520\n\t"
"v_mfma_f32_16x16x32_bf16 a[12:15], a[232:235], a[192:195], a[12:15]\n\t"
"ds_write_b64 v245, v[232:233] offset:2048\n\t"
"v_mfma_f32_16x16x32_bf16 a[20:23], a[236:239], a[188:191], a[20:23]\n\t"
"s_waitcnt lgkmcnt(5)\n\t"
"ds_read_b64_tr_b16 v[232:233], v246 offset:46592\n\t"
"v_mfma_f32_16x16x32_bf16 a[20:23], a[228:231], a[192:195], a[20:23]\n\t"
"ds_read_b64_tr_b16 v[234:235], v246 offset:49664\n\t"
"v_mfma_f32_16x16x32_bf16 a[20:23], a[232:235], a[196:199], a[20:23]\n\t"
"v_mfma_f32_16x16x32_bf16 v[224:227], v[224:227], v[220:223], 0\n\t"
"v_mfma_f32_16x16x32_bf16 a[28:31], a[236:239], a[192:195], a[28:31]\n\t"
"s_waitcnt lgkmcnt(3)\n\t"
"v_mfma_f32_16x16x32_bf16 a[28:31], a[228:231], a[196:199], a[28:31]\n\t"
"v_mfma_f32_16x16x32_bf16 a[28:31], a[232:235], a[200:203], a[28:31]\n\t"
"v_mfma_f32_16x16x32_bf16 v[228:231], v[228:231], v[220:223], 0\n\t"
"v_mfma_f32_16x16x32_bf16 a[36:39], a[236:239], a[196:199], a[36:39]\n\t"
"buffer_load_dwordx4 a[236:239], v252, s[8:11], s20 offen offset:1024 sc1\n\t"
"v_mfma_f32_16x16x32_bf16 a[36:39], a[228:231], a[200:203], a[36:39]\n\t"
"s_waitcnt vmcnt(8) lgkmcnt(0)\n\t"
"v_cvt_pk_bf16_f32 v224, v224, v225\n\t"
"v_cvt_pk_bf16_f32 v225, v226, v227\n\t"
"v_mfma_f32_16x16x32_bf16 a[36:39], a[232:235], a[204:207], a[36:39]\n\t"
"v_mfma_f32_16x16x32_bf16 v[232:235], v[232:235], v[220:223], 0\n\t"
"buffer_load_dwordx4 a[228:231], v252, s[8:11], s20 offen offset:2048 sc1\n\t"
"v_mfma_f32_16x16x32_bf16 a[44:47], a[240:243], a[168:171], a[44:47]\n\t"
"s_mov_b32 s20, 1314816\n\t"
"v_cvt_pk_bf16_f32 v226, v228, v229\n\t"
"v_cvt_pk_bf16_f32 v227, v230, v231\n\t"
"v_mfma_f32_16x16x32_bf16 a[44:47], a[244:247], a[172:175], a[44:47]\n\t"
"ds_read_b64_tr_b16 v[228:229], v246 offset:52736\n\t"
"v_mfma_f32_16x16x32_bf16 a[44:47], a[248:251], a[176:179], a[44:47]\n\t"
"buffer_load_dwordx4 a[232:235], v252, s[8:11], s20 offen offset:0 sc1\n\t"
"v_mfma_f32_16x16x32_bf16 a[52:55], a[240:243], a[172:175], a[52:55]\n\t"
"ds_read_b64_tr_b16 v[230:231], v246 offset:55808\n\t"
"v_mfma_f32_16x16x32_bf16 a[52:55], a[244:247], a[176:179], a[52:55]\n\t"
"v_cvt_pk_bf16_f32 v232, v232, v233\n\t"
"v_cvt_pk_bf16_f32 v233, v234, v235\n\t"
"v_mfma_f32_16x16x32_bf16 a[52:55], a[248:251], a[180:183], a[52:55]\n\t"
"ds_write2st64_b64 v245, v[224:225], v[226:227] offset0:5 offset1:6\n\t"
"v_mfma_f32_16x16x32_bf16 a[60:63], a[240:243], a[176:179], a[60:63]\n\t"
"v_mfma_f32_16x16x32_bf16 a[60:63], a[244:247], a[180:183], a[60:63]\n\t"
"v_add_u32 v243, 55296, v243\n\t"
"v_mfma_f32_16x16x32_bf16 a[60:63], a[248:251], a[184:187], a[60:63]\n\t"
"v_mfma_f32_16x16x32_bf16 a[68:71], a[240:243], a[180:183], a[68:71]\n\t"
"v_mfma_f32_16x16x32_bf16 a[68:71], a[244:247], a[184:187], a[68:71]\n\t"
"v_mfma_f32_16x16x32_bf16 a[68:71], a[248:251], a[188:191], a[68:71]\n\t"
"v_mfma_f32_16x16x32_bf16 a[76:79], a[240:243], a[184:187], a[76:79]\n\t"
"s_waitcnt lgkmcnt(1)\n\t"
"v_mfma_f32_16x16x32_bf16 a[76:79], a[244:247], a[188:191], a[76:79]\n\t"
"v_mfma_f32_16x16x32_bf16 a[76:79], a[248:251], a[192:195], a[76:79]\n\t"
"v_mfma_f32_16x16x32_bf16 v[228:231], v[228:231], v[220:223], 0\n\t"
"v_mfma_f32_16x16x32_bf16 a[84:87], a[240:243], a[188:191], a[84:87]\n\t"
"v_mfma_f32_16x16x32_bf16 a[84:87], a[244:247], a[192:195], a[84:87]\n\t"
"v_mfma_f32_16x16x32_bf16 a[84:87], a[248:251], a[196:199], a[84:87]\n\t"
"v_mfma_f32_16x16x32_bf16 a[92:95], a[240:243], a[192:195], a[92:95]\n\t"
"v_mfma_f32_16x16x32_bf16 a[92:95], a[244:247], a[196:199], a[92:95]\n\t"
"v_cvt_pk_bf16_f32 v234, v228, v229\n\t"
"v_cvt_pk_bf16_f32 v235, v230, v231\n\t"
"v_mfma_f32_16x16x32_bf16 a[92:95], a[248:251], a[200:203], a[92:95]\n\t"
"ds_write2st64_b64 v245, v[232:233], v[234:235] offset0:7 offset1:8\n\t"
"v_mfma_f32_16x16x32_bf16 a[100:103], a[240:243], a[196:199], a[100:103]\n\t"
"buffer_load_dwordx4 a[240:243], v252, s[8:11], s20 offen offset:1024 sc1\n\t"
"v_mfma_f32_16x16x32_bf16 a[100:103], a[244:247], a[200:203], a[100:103]\n\t"
"s_waitcnt vmcnt(8)\n\t"
"v_mfma_f32_16x16x32_bf16 a[100:103], a[248:251], a[204:207], a[100:103]\n\t"
"v_mfma_f32_16x16x32_bf16 a[108:111], a[252:255], a[168:171], a[108:111]\n\t"
"buffer_load_dwordx4 a[244:247], v252, s[8:11], s20 offen offset:2048 sc1\n\t"
"v_mfma_f32_16x16x32_bf16 a[108:111], a[212:215], a[172:175], a[108:111]\n\t"
"s_mov_b32 s20, 1327104\n\t"
"v_mfma_f32_16x16x32_bf16 a[108:111], v[96:99], a[176:179], a[108:111]\n\t"
"s_waitcnt lgkmcnt(0)\n\t"
"s_barrier\n\t"
"v_mfma_f32_16x16x32_bf16 a[116:119], a[252:255], a[172:175], a[116:119]\n\t"
"ds_read_b64_tr_b16 v[236:237], v240 offset:512\n\t"
"v_mfma_f32_16x16x32_bf16 a[116:119], a[212:215], a[176:179], a[116:119]\n\t"
"ds_read_b64_tr_b16 v[238:239], v240 offset:2048\n\t"
"v_mfma_f32_16x16x32_bf16 a[116:119], v[96:99], a[180:183], a[116:119]\n\t"
"ds_read_b64_tr_b16 v[224:225], v241 offset:512\n\t"
"v_mfma_f32_16x16x32_bf16 a[124:127], a[252:255], a[176:179], a[124:127]\n\t"
"ds_read_b64_tr_b16 v[226:227], v241 offset:2048\n\t"
"v_mfma_f32_16x16x32_bf16 a[124:127], a[212:215], a[180:183], a[124:127]\n\t"
"buffer_load_dwordx4 a[248:251], v252, s[8:11], s20 offen offset:0 sc1\n\t"
"v_mfma_f32_16x16x32_bf16 a[124:127], v[96:99], a[184:187], a[124:127]\n\t"
"ds_read_b64_tr_b16 v[228:229], v240 offset:20480\n\t"
"v_mfma_f32_16x16x32_bf16 a[132:135], a[252:255], a[180:183], a[132:135]\n\t"
"s_waitcnt lgkmcnt(3)\n\t"
"ds_read_b64_tr_b16 v[230:231], v240 offset:22016\n\t"
"v_mfma_f32_16x16x32_bf16 a[132:135], a[212:215], a[184:187], a[132:135]\n\t"
"ds_read_b64_tr_b16 v[232:233], v241 offset:20480\n\t"
"v_mfma_f32_16x16x32_bf16 a[132:135], v[96:99], a[188:191], a[132:135]\n\t"
"s_waitcnt lgkmcnt(3)\n\t"
"ds_read_b64_tr_b16 v[234:235], v241 offset:22016\n\t"
"v_mfma_f32_16x16x32_bf16 v[236:239], v[236:239], a[208:211], 0\n\t"
"ds_read_b128 a[168:171], v244 offset:0\n\t"
"v_mfma_f32_16x16x32_bf16 v[224:227], v[224:227], a[208:211], 0\n\t"
"ds_read_b128 a[172:175], v244 offset:512\n\t"
"v_mfma_f32_16x16x32_bf16 a[140:143], a[252:255], a[184:187], a[140:143]\n\t"
"ds_read_b128 a[176:179], v244 offset:1024\n\t"
"v_mfma_f32_16x16x32_bf16 a[140:143], a[212:215], a[188:191], a[140:143]\n\t"
"ds_read_b128 a[180:183], v244 offset:1536\n\t"
"v_mfma_f32_16x16x32_bf16 a[140:143], v[96:99], a[192:195], a[140:143]\n\t"
"ds_read_b128 a[184:187], v244 offset:2048\n\t"
"v_mfma_f32_16x16x32_bf16 a[148:151], a[252:255], a[188:191], a[148:151]\n\t"
"s_waitcnt lgkmcnt(7)\n\t"
"ds_read_b128 a[188:191], v244 offset:2560\n\t"
"v_cvt_pk_bf16_f32 v236, v236, v237\n\t"
"v_mfma_f32_16x16x32_bf16 a[148:151], a[212:215], a[192:195], a[148:151]\n\t"
"s_waitcnt lgkmcnt(6)\n\t"
"v_cvt_pk_bf16_f32 v237, v238, v239\n\t"
"v_cvt_pk_bf16_f32 v238, v224, v225\n\t"
"v_mfma_f32_16x16x32_bf16 a[148:151], v[96:99], a[196:199], a[148:151]\n\t"
"v_cvt_pk_bf16_f32 v239, v226, v227\n\t"
"v_mfma_f32_16x16x32_bf16 v[228:231], v[228:231], a[208:211], 0\n\t"
"ds_write_b128 v243, v[236:239] offset:27648\n\t"
"v_mfma_f32_16x16x32_bf16 v[232:235], v[232:235], a[208:211], 0\n\t"
"v_mfma_f32_16x16x32_bf16 a[156:159], a[252:255], a[192:195], a[156:159]\n\t"
"ds_read_b128 a[192:195], v244 offset:3072\n\t"
"v_mfma_f32_16x16x32_bf16 a[156:159], a[212:215], a[196:199], a[156:159]\n\t"
"ds_read_b64_tr_b16 v[224:225], v240 offset:40448\n\t"
"v_mfma_f32_16x16x32_bf16 a[156:159], v[96:99], a[200:203], a[156:159]\n\t"
"ds_read_b64_tr_b16 v[226:227], v240 offset:41984\n\t"
"v_mfma_f32_16x16x32_bf16 a[164:167], a[252:255], a[196:199], a[164:167]\n\t"
"buffer_load_dwordx4 a[252:255], v252, s[8:11], s20 offen offset:1024 sc1\n\t"
"v_mfma_f32_16x16x32_bf16 a[164:167], a[212:215], a[200:203], a[164:167]\n\t"
"s_waitcnt vmcnt(8) lgkmcnt(7)\n\t"
"ds_read_b64_tr_b16 v[236:237], v241 offset:40448\n\t"
"v_cvt_pk_bf16_f32 v228, v228, v229\n\t"
"v_mfma_f32_16x16x32_bf16 a[164:167], v[96:99], a[204:207], a[164:167]\n\t"
"ds_read_b64_tr_b16 v[238:239], v241 offset:41984\n\t"
"v_cvt_pk_bf16_f32 v229, v230, v231\n\t"
"Lst10_%=:\n\t"
"v_mfma_f32_16x16x32_bf16 v[196:199], a[216:219], a[168:171], v[196:199]\n\t"
"s_waitcnt lgkmcnt(8)\n\t"
"ds_read_b128 a[196:199], v244 offset:3584\n\t"
"v_cvt_pk_bf16_f32 v230, v232, v233\n\t"
"v_mfma_f32_16x16x32_bf16 v[196:199], a[220:223], a[172:175], v[196:199]\n\t"
"ds_read_b128 a[200:203], v244 offset:4096\n\t"
"v_cvt_pk_bf16_f32 v231, v234, v235\n\t"
"v_mfma_f32_16x16x32_bf16 v[196:199], v[248:251], a[176:179], v[196:199]\n\t"
"ds_write_b128 v243, v[228:231] offset:36864\n\t"
"v_mfma_f32_16x16x32_bf16 v[204:207], a[216:219], a[172:175], v[204:207]\n\t"
"buffer_load_dwordx4 v[96:99], v252, s[8:11], s20 offen offset:2048 sc1\n\t"
"v_mfma_f32_16x16x32_bf16 v[204:207], a[220:223], a[176:179], v[204:207]\n\t"
"s_waitcnt lgkmcnt(10)\n\t"
"ds_read_b128 a[204:207], v244 offset:4608\n\t"
"v_mfma_f32_16x16x32_bf16 v[204:207], v[248:251], a[180:183], v[204:207]\n\t"
"s_mov_b32 s20, 1339392\n\t"
"v_subrev_u32 v240, 59904, v240\n\t"
"v_subrev_u32 v241, 59904, v241\n\t"
"v_mfma_f32_16x16x32_bf16 v[212:215], a[216:219], a[176:179], v[212:215]\n\t"
"s_waitcnt lgkmcnt(6)\n\t"
"ds_read_b64_tr_b16 v[232:233], v240 offset:512\n\t"
"v_mfma_f32_16x16x32_bf16 v[212:215], a[220:223], a[180:183], v[212:215]\n\t"
"buffer_load_dwordx4 a[212:215], v252, s[8:11], s20 offen offset:0 sc1\n\t"
"v_mfma_f32_16x16x32_bf16 v[212:215], v[248:251], a[184:187], v[212:215]\n\t"
"s_waitcnt lgkmcnt(5)\n\t"
"ds_read_b64_tr_b16 v[234:235], v240 offset:2048\n\t"
"v_mfma_f32_16x16x32_bf16 v[224:227], v[224:227], a[208:211], 0\n\t"
"ds_read_b64_tr_b16 v[228:229], v241 offset:512\n\t"
"v_mfma_f32_16x16x32_bf16 v[236:239], v[236:239], a[208:211], 0\n\t"
"ds_read_b64_tr_b16 v[230:231], v241 offset:2048\n\t"
"v_mfma_f32_16x16x32_bf16 a[0:3], a[216:219], a[180:183], a[0:3]\n\t"
"v_mfma_f32_16x16x32_bf16 a[0:3], a[220:223], a[184:187], a[0:3]\n\t"
"v_mfma_f32_16x16x32_bf16 a[0:3], v[248:251], a[188:191], a[0:3]\n\t"
"v_mfma_f32_16x16x32_bf16 a[8:11], a[216:219], a[184:187], a[8:11]\n\t"
"s_waitcnt lgkmcnt(2)\n\t"
"v_cvt_pk_bf16_f32 v224, v224, v225\n\t"
"v_cvt_pk_bf16_f32 v225, v226, v227\n\t"
"v_mfma_f32_16x16x32_bf16 a[8:11], a[220:223], a[188:191], a[8:11]\n\t"
"s_waitcnt lgkmcnt(0)\n\t"
"v_cvt_pk_bf16_f32 v226, v236, v237\n\t"
"v_cvt_pk_bf16_f32 v227, v238, v239\n\t"
"v_mfma_f32_16x16x32_bf16 a[8:11], v[248:251], a[192:195], a[8:11]\n\t"
"ds_write_b128 v243, v[224:227] offset:46080\n\t"
"v_mfma_f32_16x16x32_bf16 v[232:235], v[232:235], a[208:211], 0\n\t"
"v_mfma_f32_16x16x32_bf16 v[228:231], v[228:231], a[208:211], 0\n\t"
"ds_read_b64_tr_b16 v[236:237], v240 offset:20480\n\t"
"v_mfma_f32_16x16x32_bf16 a[16:19], a[216:219], a[188:191], a[16:19]\n\t"
"ds_read_b64_tr_b16 v[238:239], v240 offset:22016\n\t"
"v_mfma_f32_16x16x32_bf16 a[16:19], a[220:223], a[192:195], a[16:19]\n\t"
"ds_read_b64_tr_b16 v[224:225], v241 offset:20480\n\t"
"v_mfma_f32_16x16x32_bf16 a[16:19], v[248:251], a[196:199], a[16:19]\n\t"
"ds_read_b64_tr_b16 v[226:227], v241 offset:22016\n\t"
"v_mfma_f32_16x16x32_bf16 a[24:27], a[216:219], a[192:195], a[24:27]\n\t"
"v_cvt_pk_bf16_f32 v232, v232, v233\n\t"
"v_cvt_pk_bf16_f32 v233, v234, v235\n\t"
"v_mfma_f32_16x16x32_bf16 a[24:27], a[220:223], a[196:199], a[24:27]\n\t"
"v_cvt_pk_bf16_f32 v234, v228, v229\n\t"
"v_cvt_pk_bf16_f32 v235, v230, v231\n\t"
"v_mfma_f32_16x16x32_bf16 a[24:27], v[248:251], a[200:203], a[24:27]\n\t"
"ds_write_b128 v243, v[232:235] offset:0\n\t"
"v_mfma_f32_16x16x32_bf16 a[32:35], a[216:219], a[196:199], a[32:35]\n\t"
"buffer_load_dwordx4 a[216:219], v252, s[8:11], s20 offen offset:1024 sc1\n\t"
"v_mfma_f32_16x16x32_bf16 a[32:35], a[220:223], a[200:203], a[32:35]\n\t"
"s_waitcnt vmcnt(8)\n\t"
"ds_read_b64_tr_b16 v[228:229], v240 offset:40448\n\t"
"v_mfma_f32_16x16x32_bf16 a[32:35], v[248:251], a[204:207], a[32:35]\n\t"
"ds_read_b64_tr_b16 v[230:231], v240 offset:41984\n\t"
"v_mfma_f32_16x16x32_bf16 a[40:43], a[224:227], a[168:171], a[40:43]\n\t"
"buffer_load_dwordx4 v[248:251], v252, s[8:11], s20 offen offset:2048 sc1\n\t"
"v_mfma_f32_16x16x32_bf16 a[40:43], a[236:239], a[172:175], a[40:43]\n\t"
"s_mov_b32 s20, 1351680\n\t"
"ds_read_b64_tr_b16 v[232:233], v241 offset:40448\n\t"
"v_mfma_f32_16x16x32_bf16 a[40:43], a[228:231], a[176:179], a[40:43]\n\t"
"ds_read_b64_tr_b16 v[234:235], v241 offset:41984\n\t"
"v_mfma_f32_16x16x32_bf16 a[48:51], a[224:227], a[172:175], a[48:51]\n\t"
"buffer_load_dwordx4 a[220:223], v252, s[8:11], s20 offen offset:0 sc1\n\t"
"v_mfma_f32_16x16x32_bf16 a[48:51], a[236:239], a[176:179], a[48:51]\n\t"
"s_waitcnt lgkmcnt(5)\n\t"
"v_mfma_f32_16x16x32_bf16 a[48:51], a[228:231], a[180:183], a[48:51]\n\t"
"v_mfma_f32_16x16x32_bf16 v[236:239], v[236:239], a[208:211], 0\n\t"
"v_mfma_f32_16x16x32_bf16 v[224:227], v[224:227], a[208:211], 0\n\t"
"v_xor_b32 v246, 0xc0, v246\n\t"
"v_mfma_f32_16x16x32_bf16 a[56:59], a[224:227], a[176:179], a[56:59]\n\t"
"v_mfma_f32_16x16x32_bf16 a[56:59], a[236:239], a[180:183], a[56:59]\n\t"
"v_mfma_f32_16x16x32_bf16 a[56:59], a[228:231], a[184:187], a[56:59]\n\t"
"v_mfma_f32_16x16x32_bf16 a[64:67], a[224:227], a[180:183], a[64:67]\n\t"
"s_waitcnt lgkmcnt(2)\n\t"
"v_cvt_pk_bf16_f32 v236, v236, v237\n\t"
"v_cvt_pk_bf16_f32 v237, v238, v239\n\t"
"v_mfma_f32_16x16x32_bf16 a[64:67], a[236:239], a[184:187], a[64:67]\n\t"
"s_waitcnt lgkmcnt(0)\n\t"
"v_cvt_pk_bf16_f32 v238, v224, v225\n\t"
"v_cvt_pk_bf16_f32 v239, v226, v227\n\t"
"v_mfma_f32_16x16x32_bf16 a[64:67], a[228:231], a[188:191], a[64:67]\n\t"
"ds_write_b128 v243, v[236:239] offset:9216\n\t"
"v_mfma_f32_16x16x32_bf16 v[228:231], v[228:231], a[208:211], 0\n\t"
"v_mfma_f32_16x16x32_bf16 v[232:235], v[232:235], a[208:211], 0\n\t"
"v_mfma_f32_16x16x32_bf16 a[72:75], a[224:227], a[184:187], a[72:75]\n\t"
"v_mfma_f32_16x16x32_bf16 a[72:75], a[236:239], a[188:191], a[72:75]\n\t"
"v_mfma_f32_16x16x32_bf16 a[72:75], a[228:231], a[192:195], a[72:75]\n\t"
"v_mfma_f32_16x16x32_bf16 a[80:83], a[224:227], a[188:191], a[80:83]\n\t"
"v_cvt_pk_bf16_f32 v228, v228, v229\n\t"
"v_cvt_pk_bf16_f32 v229, v230, v231\n\t"
"v_mfma_f32_16x16x32_bf16 a[80:83], a[236:239], a[192:195], a[80:83]\n\t"
"v_cvt_pk_bf16_f32 v230, v232, v233\n\t"
"v_cvt_pk_bf16_f32 v231, v234, v235\n\t"
"v_mfma_f32_16x16x32_bf16 a[80:83], a[228:231], a[196:199], a[80:83]\n\t"
"ds_write_b128 v243, v[228:231] offset:18432\n\t"
"v_mfma_f32_16x16x32_bf16 a[88:91], a[224:227], a[192:195], a[88:91]\n\t"
"v_mfma_f32_16x16x32_bf16 a[88:91], a[236:239], a[196:199], a[88:91]\n\t"
"ds_read_b64_tr_b16 v[224:225], v246 offset:512\n\t"
"v_mfma_f32_16x16x32_bf16 a[88:91], a[228:231], a[200:203], a[88:91]\n\t"
"ds_read_b64_tr_b16 v[226:227], v246 offset:3584\n\t"
"v_mfma_f32_16x16x32_bf16 a[96:99], a[224:227], a[196:199], a[96:99]\n\t"
"buffer_load_dwordx4 a[224:227], v252, s[8:11], s20 offen offset:1024 sc1\n\t"
"v_mfma_f32_16x16x32_bf16 a[96:99], a[236:239], a[200:203], a[96:99]\n\t"
"s_waitcnt vmcnt(8)\n\t"
"ds_read_b64_tr_b16 v[236:237], v246 offset:6656\n\t"
"v_mfma_f32_16x16x32_bf16 a[96:99], a[228:231], a[204:207], a[96:99]\n\t"
"ds_read_b64_tr_b16 v[238:239], v246 offset:9728\n\t"
"v_mfma_f32_16x16x32_bf16 a[104:107], a[232:235], a[168:171], a[104:107]\n\t"
"buffer_load_dwordx4 a[228:231], v252, s[8:11], s20 offen offset:2048 sc1\n\t"
"v_mfma_f32_16x16x32_bf16 a[104:107], a[240:243], a[172:175], a[104:107]\n\t"
"s_mov_b32 s20, 1216512\n\t"
"ds_read_b64_tr_b16 v[232:233], v246 offset:12800\n\t"
"v_mfma_f32_16x16x32_bf16 a[104:107], a[244:247], a[176:179], a[104:107]\n\t"
"ds_read_b64_tr_b16 v[234:235], v246 offset:15872\n\t"
"v_mfma_f32_16x16x32_bf16 a[112:115], a[232:235], a[172:175], a[112:115]\n\t"
"buffer_load_dwordx4 a[236:239], v252, s[8:11], s20 offen offset:0 sc1\n\t"
"v_mfma_f32_16x16x32_bf16 a[112:115], a[240:243], a[176:179], a[112:115]\n\t"
"ds_read_b128 a[168:171], v244 offset:9216\n\t"
"v_mfma_f32_16x16x32_bf16 a[112:115], a[244:247], a[180:183], a[112:115]\n\t"
"ds_read_b64_tr_b16 v[228:229], v246 offset:20480\n\t"
"v_mfma_f32_16x16x32_bf16 a[120:123], a[232:235], a[176:179], a[120:123]\n\t"
"s_waitcnt lgkmcnt(6)\n\t"
"ds_read_b64_tr_b16 v[230:231], v246 offset:23552\n\t"
"v_mfma_f32_16x16x32_bf16 a[120:123], a[240:243], a[180:183], a[120:123]\n\t"
"ds_read_b128 a[172:175], v244 offset:9728\n\t"
"v_mfma_f32_16x16x32_bf16 a[120:123], a[244:247], a[184:187], a[120:123]\n\t"
"ds_read_b128 a[176:179], v244 offset:10240\n\t"
"v_mfma_f32_16x16x32_bf16 v[224:227], v[224:227], v[220:223], 0\n\t"
"v_mfma_f32_16x16x32_bf16 a[128:131], a[232:235], a[180:183], a[128:131]\n\t"
"s_waitcnt lgkmcnt(7)\n\t"
"ds_read_b128 a[180:183], v244 offset:10752\n\t"
"v_mfma_f32_16x16x32_bf16 a[128:131], a[240:243], a[184:187], a[128:131]\n\t"
"v_mfma_f32_16x16x32_bf16 a[128:131], a[244:247], a[188:191], a[128:131]\n\t"
"v_mfma_f32_16x16x32_bf16 v[236:239], v[236:239], v[220:223], 0\n\t"
"v_mfma_f32_16x16x32_bf16 a[136:139], a[232:235], a[184:187], a[136:139]\n\t"
"s_waitcnt lgkmcnt(6)\n\t"
"ds_read_b128 a[184:187], v244 offset:11264\n\t"
"v_cvt_pk_bf16_f32 v224, v224, v225\n\t"
"v_mfma_f32_16x16x32_bf16 a[136:139], a[240:243], a[188:191], a[136:139]\n\t"
"v_cvt_pk_bf16_f32 v225, v226, v227\n\t"
"v_mfma_f32_16x16x32_bf16 a[136:139], a[244:247], a[192:195], a[136:139]\n\t"
"v_mfma_f32_16x16x32_bf16 v[232:235], v[232:235], v[220:223], 0\n\t"
"v_mfma_f32_16x16x32_bf16 a[144:147], a[232:235], a[188:191], a[144:147]\n\t"
"s_waitcnt lgkmcnt(4)\n\t"
"ds_read_b128 a[188:191], v244 offset:11776\n\t"
"v_cvt_pk_bf16_f32 v226, v236, v237\n\t"
"v_mfma_f32_16x16x32_bf16 a[144:147], a[240:243], a[192:195], a[144:147]\n\t"
"v_cvt_pk_bf16_f32 v227, v238, v239\n\t"
"v_mfma_f32_16x16x32_bf16 a[144:147], a[244:247], a[196:199], a[144:147]\n\t"
"v_mfma_f32_16x16x32_bf16 v[228:231], v[228:231], v[220:223], 0\n\t"
"v_mfma_f32_16x16x32_bf16 a[152:155], a[232:235], a[192:195], a[152:155]\n\t"
"ds_read_b128 a[192:195], v244 offset:12288\n\t"
"v_cvt_pk_bf16_f32 v232, v232, v233\n\t"
"v_mfma_f32_16x16x32_bf16 a[152:155], a[240:243], a[196:199], a[152:155]\n\t"
"v_cvt_pk_bf16_f32 v233, v234, v235\n\t"
"v_mfma_f32_16x16x32_bf16 a[152:155], a[244:247], a[200:203], a[152:155]\n\t"
"v_mfma_f32_16x16x32_bf16 a[160:163], a[232:235], a[196:199], a[160:163]\n\t"
"buffer_load_dwordx4 a[232:235], v252, s[8:11], s20 offen offset:1024 sc1\n\t"
"v_mfma_f32_16x16x32_bf16 a[160:163], a[240:243], a[200:203], a[160:163]\n\t"
"s_waitcnt vmcnt(8) lgkmcnt(4)\n\t"
"ds_read_b128 a[196:199], v244 offset:12800\n\t"
"v_cvt_pk_bf16_f32 v234, v228, v229\n\t"
"v_mfma_f32_16x16x32_bf16 a[160:163], a[244:247], a[204:207], a[160:163]\n\t"
"ds_read_b128 a[200:203], v244 offset:13312\n\t"
"v_cvt_pk_bf16_f32 v235, v230, v231\n\t"
"v_mfma_f32_16x16x32_bf16 v[200:203], a[248:251], a[168:171], v[200:203]\n\t"
"ds_read_b64_tr_b16 v[228:229], v246 offset:26624\n\t"
"v_mfma_f32_16x16x32_bf16 v[200:203], a[252:255], a[172:175], v[200:203]\n\t"
"s_waitcnt lgkmcnt(6)\n\t"
"ds_read_b64_tr_b16 v[230:231], v246 offset:29696\n\t"
"v_mfma_f32_16x16x32_bf16 v[200:203], v[96:99], a[176:179], v[200:203]\n\t"
"s_waitcnt lgkmcnt(0)\n\t"
"ds_read_b128 a[204:207], v244 offset:13824\n\t"
"v_mfma_f32_16x16x32_bf16 v[228:231], v[228:231], v[220:223], 0\n\t"
"buffer_load_dwordx4 a[240:243], v252, s[8:11], s20 offen offset:2048 sc1\n\t"
"s_mov_b32 s20, 1228800\n\t"
"v_mfma_f32_16x16x32_bf16 v[208:211], a[248:251], a[172:175], v[208:211]\n\t"
"buffer_load_dwordx4 a[244:247], v252, s[8:11], s20 offen offset:0 sc1\n\t"
"v_mfma_f32_16x16x32_bf16 v[208:211], a[252:255], a[176:179], v[208:211]\n\t"
"v_mfma_f32_16x16x32_bf16 v[208:211], v[96:99], a[180:183], v[208:211]\n\t"
"s_waitcnt lgkmcnt(0)\n\t"
"s_barrier\n\t"
"v_mfma_f32_16x16x32_bf16 v[216:219], a[248:251], a[176:179], v[216:219]\n\t"
"v_mfma_f32_16x16x32_bf16 v[216:219], a[252:255], a[180:183], v[216:219]\n\t"
"v_cvt_pk_bf16_f32 v228, v228, v229\n\t"
"v_cvt_pk_bf16_f32 v229, v230, v231\n\t"
"v_mfma_f32_16x16x32_bf16 v[216:219], v[96:99], a[184:187], v[216:219]\n\t"
"ds_write2st64_b64 v245, v[224:225], v[226:227] offset0:0 offset1:1\n\t"
"v_mfma_f32_16x16x32_bf16 a[4:7], a[248:251], a[180:183], a[4:7]\n\t"
"v_mfma_f32_16x16x32_bf16 a[4:7], a[252:255], a[184:187], a[4:7]\n\t"
"ds_read_b64_tr_b16 v[224:225], v246 offset:32768\n\t"
"v_mfma_f32_16x16x32_bf16 a[4:7], v[96:99], a[188:191], a[4:7]\n\t"
"ds_write2st64_b64 v245, v[232:233], v[234:235] offset0:2 offset1:3\n\t"
"v_mfma_f32_16x16x32_bf16 a[12:15], a[248:251], a[184:187], a[12:15]\n\t"
"v_mfma_f32_16x16x32_bf16 a[12:15], a[252:255], a[188:191], a[12:15]\n\t"
"ds_read_b64_tr_b16 v[226:227], v246 offset:35840\n\t"
"v_mfma_f32_16x16x32_bf16 a[12:15], v[96:99], a[192:195], a[12:15]\n\t"
"ds_write_b64 v245, v[228:229] offset:2048\n\t"
"v_mfma_f32_16x16x32_bf16 a[20:23], a[248:251], a[188:191], a[20:23]\n\t"
"ds_read_b64_tr_b16 v[232:233], v246 offset:40448\n\t"
"v_mfma_f32_16x16x32_bf16 a[20:23], a[252:255], a[192:195], a[20:23]\n\t"
"ds_read_b64_tr_b16 v[234:235], v246 offset:43520\n\t"
"v_mfma_f32_16x16x32_bf16 a[20:23], v[96:99], a[196:199], a[20:23]\n\t"
"s_waitcnt lgkmcnt(3)\n\t"
"ds_read_b64_tr_b16 v[228:229], v246 offset:46592\n\t"
"v_mfma_f32_16x16x32_bf16 v[224:227], v[224:227], v[220:223], 0\n\t"
"ds_read_b64_tr_b16 v[230:231], v246 offset:49664\n\t"
"v_mfma_f32_16x16x32_bf16 a[28:31], a[248:251], a[192:195], a[28:31]\n\t"
"v_mfma_f32_16x16x32_bf16 a[28:31], a[252:255], a[196:199], a[28:31]\n\t"
"v_mfma_f32_16x16x32_bf16 a[28:31], v[96:99], a[200:203], a[28:31]\n\t"
"s_waitcnt lgkmcnt(2)\n\t"
"v_mfma_f32_16x16x32_bf16 v[232:235], v[232:235], v[220:223], 0\n\t"
"v_mfma_f32_16x16x32_bf16 a[36:39], a[248:251], a[196:199], a[36:39]\n\t"
"buffer_load_dwordx4 a[248:251], v252, s[8:11], s20 offen offset:1024 sc1\n\t"
"v_mfma_f32_16x16x32_bf16 a[36:39], a[252:255], a[200:203], a[36:39]\n\t"
"s_waitcnt vmcnt(8) lgkmcnt(0)\n\t"
"v_cvt_pk_bf16_f32 v224, v224, v225\n\t"
"v_cvt_pk_bf16_f32 v225, v226, v227\n\t"
"v_mfma_f32_16x16x32_bf16 a[36:39], v[96:99], a[204:207], a[36:39]\n\t"
"v_mfma_f32_16x16x32_bf16 v[228:231], v[228:231], v[220:223], 0\n\t"
"buffer_load_dwordx4 v[96:99], v252, s[8:11], s20 offen offset:2048 sc1\n\t"
"v_mfma_f32_16x16x32_bf16 a[44:47], a[212:215], a[168:171], a[44:47]\n\t"
"s_mov_b32 s20, 1241088\n\t"
"v_cvt_pk_bf16_f32 v226, v232, v233\n\t"
"v_cvt_pk_bf16_f32 v227, v234, v235\n\t"
"v_mfma_f32_16x16x32_bf16 a[44:47], a[216:219], a[172:175], a[44:47]\n\t"
"ds_read_b64_tr_b16 v[232:233], v246 offset:52736\n\t"
"v_mfma_f32_16x16x32_bf16 a[44:47], v[248:251], a[176:179], a[44:47]\n\t"
"buffer_load_dwordx4 a[252:255], v252, s[8:11], s20 offen offset:0 sc1\n\t"
"v_mfma_f32_16x16x32_bf16 a[52:55], a[212:215], a[172:175], a[52:55]\n\t"
"ds_read_b64_tr_b16 v[234:235], v246 offset:55808\n\t"
"v_mfma_f32_16x16x32_bf16 a[52:55], a[216:219], a[176:179], a[52:55]\n\t"
"v_cvt_pk_bf16_f32 v228, v228, v229\n\t"
"v_cvt_pk_bf16_f32 v229, v230, v231\n\t"
"v_mfma_f32_16x16x32_bf16 a[52:55], v[248:251], a[180:183], a[52:55]\n\t"
"ds_write2st64_b64 v245, v[224:225], v[226:227] offset0:5 offset1:6\n\t"
"v_mfma_f32_16x16x32_bf16 a[60:63], a[212:215], a[176:179], a[60:63]\n\t"
"v_mfma_f32_16x16x32_bf16 a[60:63], a[216:219], a[180:183], a[60:63]\n\t"
"v_subrev_u32 v243, 55296, v243\n\t"
"v_mfma_f32_16x16x32_bf16 a[60:63], v[248:251], a[184:187], a[60:63]\n\t"
"v_mfma_f32_16x16x32_bf16 a[68:71], a[212:215], a[180:183], a[68:71]\n\t"
"v_mfma_f32_16x16x32_bf16 a[68:71], a[216:219], a[184:187], a[68:71]\n\t"
"v_mfma_f32_16x16x32_bf16 a[68:71], v[248:251], a[188:191], a[68:71]\n\t"
"v_mfma_f32_16x16x32_bf16 a[76:79], a[212:215], a[184:187], a[76:79]\n\t"
"s_waitcnt lgkmcnt(1)\n\t"
"v_mfma_f32_16x16x32_bf16 a[76:79], a[216:219], a[188:191], a[76:79]\n\t"
"v_mfma_f32_16x16x32_bf16 a[76:79], v[248:251], a[192:195], a[76:79]\n\t"
"v_mfma_f32_16x16x32_bf16 v[232:235], v[232:235], v[220:223], 0\n\t"
"v_mfma_f32_16x16x32_bf16 a[84:87], a[212:215], a[188:191], a[84:87]\n\t"
"v_mfma_f32_16x16x32_bf16 a[84:87], a[216:219], a[192:195], a[84:87]\n\t"
"v_mfma_f32_16x16x32_bf16 a[84:87], v[248:251], a[196:199], a[84:87]\n\t"
"v_mfma_f32_16x16x32_bf16 a[92:95], a[212:215], a[192:195], a[92:95]\n\t"
"v_mfma_f32_16x16x32_bf16 a[92:95], a[216:219], a[196:199], a[92:95]\n\t"
"v_cvt_pk_bf16_f32 v230, v232, v233\n\t"
"v_cvt_pk_bf16_f32 v231, v234, v235\n\t"
"v_mfma_f32_16x16x32_bf16 a[92:95], v[248:251], a[200:203], a[92:95]\n\t"
"ds_write2st64_b64 v245, v[228:229], v[230:231] offset0:7 offset1:8\n\t"
"v_mfma_f32_16x16x32_bf16 a[100:103], a[212:215], a[196:199], a[100:103]\n\t"
"buffer_load_dwordx4 a[212:215], v252, s[8:11], s20 offen offset:1024 sc1\n\t"
"v_mfma_f32_16x16x32_bf16 a[100:103], a[216:219], a[200:203], a[100:103]\n\t"
"s_waitcnt vmcnt(8)\n\t"
"ds_read_b64_tr_b16 v[236:237], v240 offset:1024\n\t"
"v_mfma_f32_16x16x32_bf16 a[100:103], v[248:251], a[204:207], a[100:103]\n\t"
"ds_read_b64_tr_b16 v[238:239], v240 offset:2560\n\t"
"v_mfma_f32_16x16x32_bf16 a[108:111], a[220:223], a[168:171], a[108:111]\n\t"
"buffer_load_dwordx4 v[248:251], v252, s[8:11], s20 offen offset:2048 sc1\n\t"
"v_mfma_f32_16x16x32_bf16 a[108:111], a[224:227], a[172:175], a[108:111]\n\t"
"s_mov_b32 s20, 1253376\n\t"
"ds_read_b64_tr_b16 v[224:225], v241 offset:1024\n\t"
"v_mfma_f32_16x16x32_bf16 a[108:111], a[228:231], a[176:179], a[108:111]\n\t"
"s_waitcnt lgkmcnt(3)\n\t"
"s_barrier\n\t"
"v_mfma_f32_16x16x32_bf16 a[116:119], a[220:223], a[172:175], a[116:119]\n\t"
"ds_read_b64_tr_b16 v[226:227], v241 offset:2560\n\t"
"v_mfma_f32_16x16x32_bf16 a[116:119], a[224:227], a[176:179], a[116:119]\n\t"
"buffer_load_dwordx4 a[216:219], v252, s[8:11], s20 offen offset:0 sc1\n\t"
"v_mfma_f32_16x16x32_bf16 a[116:119], a[228:231], a[180:183], a[116:119]\n\t"
"ds_read_b64_tr_b16 v[232:233], v240 offset:20992\n\t"
"v_mfma_f32_16x16x32_bf16 a[124:127], a[220:223], a[176:179], a[124:127]\n\t"
"ds_read_b64_tr_b16 v[234:235], v240 offset:22528\n\t"
"v_mfma_f32_16x16x32_bf16 a[124:127], a[224:227], a[180:183], a[124:127]\n\t"
"ds_read_b64_tr_b16 v[228:229], v241 offset:20992\n\t"
"v_mfma_f32_16x16x32_bf16 a[124:127], a[228:231], a[184:187], a[124:127]\n\t"
"ds_read_b64_tr_b16 v[230:231], v241 offset:22528\n\t"
"v_mfma_f32_16x16x32_bf16 a[132:135], a[220:223], a[180:183], a[132:135]\n\t"
"s_waitcnt lgkmcnt(6)\n\t"
"ds_read_b128 a[168:171], v244 offset:0\n\t"
"v_mfma_f32_16x16x32_bf16 a[132:135], a[224:227], a[184:187], a[132:135]\n\t"
"s_waitcnt lgkmcnt(5)\n\t"
"ds_read_b128 a[172:175], v244 offset:512\n\t"
"v_mfma_f32_16x16x32_bf16 a[132:135], a[228:231], a[188:191], a[132:135]\n\t"
"ds_read_b128 a[176:179], v244 offset:1024\n\t"
"v_mfma_f32_16x16x32_bf16 v[236:239], v[236:239], a[208:211], 0\n\t"
"ds_read_b128 a[180:183], v244 offset:1536\n\t"
"v_mfma_f32_16x16x32_bf16 v[224:227], v[224:227], a[208:211], 0\n\t"
"v_mfma_f32_16x16x32_bf16 a[140:143], a[220:223], a[184:187], a[140:143]\n\t"
"ds_read_b128 a[184:187], v244 offset:2048\n\t"
"v_mfma_f32_16x16x32_bf16 a[140:143], a[224:227], a[188:191], a[140:143]\n\t"
"v_mfma_f32_16x16x32_bf16 a[140:143], a[228:231], a[192:195], a[140:143]\n\t"
"v_mfma_f32_16x16x32_bf16 a[148:151], a[220:223], a[188:191], a[148:151]\n\t"
"s_waitcnt lgkmcnt(7)\n\t"
"ds_read_b128 a[188:191], v244 offset:2560\n\t"
"v_cvt_pk_bf16_f32 v236, v236, v237\n\t"
"v_mfma_f32_16x16x32_bf16 a[148:151], a[224:227], a[192:195], a[148:151]\n\t"
"s_waitcnt lgkmcnt(6)\n\t"
"v_cvt_pk_bf16_f32 v237, v238, v239\n\t"
"v_cvt_pk_bf16_f32 v238, v224, v225\n\t"
"v_mfma_f32_16x16x32_bf16 a[148:151], a[228:231], a[196:199], a[148:151]\n\t"
"v_cvt_pk_bf16_f32 v239, v226, v227\n\t"
"v_mfma_f32_16x16x32_bf16 v[232:235], v[232:235], a[208:211], 0\n\t"
"ds_write_b128 v243, v[236:239] offset:512\n\t"
"v_mfma_f32_16x16x32_bf16 v[228:231], v[228:231], a[208:211], 0\n\t"
"v_mfma_f32_16x16x32_bf16 a[156:159], a[220:223], a[192:195], a[156:159]\n\t"
"ds_read_b128 a[192:195], v244 offset:3072\n\t"
"v_mfma_f32_16x16x32_bf16 a[156:159], a[224:227], a[196:199], a[156:159]\n\t"
"ds_read_b64_tr_b16 v[224:225], v240 offset:40960\n\t"
"v_mfma_f32_16x16x32_bf16 a[156:159], a[228:231], a[200:203], a[156:159]\n\t"
"ds_read_b64_tr_b16 v[226:227], v240 offset:42496\n\t"
"v_mfma_f32_16x16x32_bf16 a[164:167], a[220:223], a[196:199], a[164:167]\n\t"
"buffer_load_dwordx4 a[220:223], v252, s[8:11], s20 offen offset:1024 sc1\n\t"
"v_mfma_f32_16x16x32_bf16 a[164:167], a[224:227], a[200:203], a[164:167]\n\t"
"s_waitcnt vmcnt(8) lgkmcnt(7)\n\t"
"ds_read_b64_tr_b16 v[236:237], v241 offset:40960\n\t"
"v_cvt_pk_bf16_f32 v232, v232, v233\n\t"
"v_mfma_f32_16x16x32_bf16 a[164:167], a[228:231], a[204:207], a[164:167]\n\t"
"ds_read_b64_tr_b16 v[238:239], v241 offset:42496\n\t"
"v_cvt_pk_bf16_f32 v233, v234, v235\n\t"
"Lst11_%=:\n\t"
"v_mfma_f32_16x16x32_bf16 v[0:3], a[236:239], a[168:171], v[0:3]\n\t"
"s_waitcnt lgkmcnt(8)\n\t"
"ds_read_b128 a[196:199], v244 offset:3584\n\t"
"v_cvt_pk_bf16_f32 v234, v228, v229\n\t"
"v_mfma_f32_16x16x32_bf16 v[0:3], a[232:235], a[172:175], v[0:3]\n\t"
"ds_read_b128 a[200:203], v244 offset:4096\n\t"
"v_cvt_pk_bf16_f32 v235, v230, v231\n\t"
"v_mfma_f32_16x16x32_bf16 v[0:3], a[240:243], a[176:179], v[0:3]\n\t"
"s_waitcnt lgkmcnt(4)\n\t"
"ds_write_b128 v243, v[232:235] offset:9728\n\t"
"v_mfma_f32_16x16x32_bf16 v[4:7], a[236:239], a[172:175], v[4:7]\n\t"
"buffer_load_dwordx4 a[224:227], v252, s[8:11], s20 offen offset:2048 sc1\n\t"
"v_mfma_f32_16x16x32_bf16 v[4:7], a[232:235], a[176:179], v[4:7]\n\t"
"Lpg8_%=:\n\t"
"s_cbranch_vccnz Lpg12_%=\n\t"
"ds_read_b128 a[204:207], v244 offset:4608\n\t"
"v_mfma_f32_16x16x32_bf16 v[4:7], a[240:243], a[180:183], v[4:7]\n\t"
"s_mov_b32 s20, 1265664\n\t"
"v_add_u32 v240, 59904, v240\n\t"
"v_add_u32 v241, 59904, v241\n\t"
"v_mfma_f32_16x16x32_bf16 v[8:11], a[236:239], a[176:179], v[8:11]\n\t"
"ds_read_b64_tr_b16 v[228:229], v240 offset:1024\n\t"
"v_mfma_f32_16x16x32_bf16 v[8:11], a[232:235], a[180:183], v[8:11]\n\t"
"buffer_load_dwordx4 a[228:231], v252, s[8:11], s20 offen offset:0 sc1\n\t"
"v_mfma_f32_16x16x32_bf16 v[8:11], a[240:243], a[184:187], v[8:11]\n\t"
"s_waitcnt lgkmcnt(5)\n\t"
"ds_read_b64_tr_b16 v[230:231], v240 offset:2560\n\t"
"v_mfma_f32_16x16x32_bf16 v[224:227], v[224:227], a[208:211], 0\n\t"
"ds_read_b64_tr_b16 v[232:233], v241 offset:1024\n\t"
"v_mfma_f32_16x16x32_bf16 v[236:239], v[236:239], a[208:211], 0\n\t"
"ds_read_b64_tr_b16 v[234:235], v241 offset:2560\n\t"
"v_mfma_f32_16x16x32_bf16 v[12:15], a[236:239], a[180:183], v[12:15]\n\t"
"v_mfma_f32_16x16x32_bf16 v[12:15], a[232:235], a[184:187], v[12:15]\n\t"
"v_mfma_f32_16x16x32_bf16 v[12:15], a[240:243], a[188:191], v[12:15]\n\t"
"v_mfma_f32_16x16x32_bf16 v[16:19], a[236:239], a[184:187], v[16:19]\n\t"
"s_waitcnt lgkmcnt(2)\n\t"
"v_cvt_pk_bf16_f32 v224, v224, v225\n\t"
"v_cvt_pk_bf16_f32 v225, v226, v227\n\t"
"v_mfma_f32_16x16x32_bf16 v[16:19], a[232:235], a[188:191], v[16:19]\n\t"
"s_waitcnt lgkmcnt(0)\n\t"
"v_cvt_pk_bf16_f32 v226, v236, v237\n\t"
"v_cvt_pk_bf16_f32 v227, v238, v239\n\t"
"v_mfma_f32_16x16x32_bf16 v[16:19], a[240:243], a[192:195], v[16:19]\n\t"
"ds_write_b128 v243, v[224:227] offset:18944\n\t"
"v_mfma_f32_16x16x32_bf16 v[228:231], v[228:231], a[208:211], 0\n\t"
"v_mfma_f32_16x16x32_bf16 v[232:235], v[232:235], a[208:211], 0\n\t"
"ds_read_b64_tr_b16 v[236:237], v240 offset:20992\n\t"
"v_mfma_f32_16x16x32_bf16 v[20:23], a[236:239], a[188:191], v[20:23]\n\t"
"ds_read_b64_tr_b16 v[238:239], v240 offset:22528\n\t"
"v_mfma_f32_16x16x32_bf16 v[20:23], a[232:235], a[192:195], v[20:23]\n\t"
"ds_read_b64_tr_b16 v[224:225], v241 offset:20992\n\t"
"v_mfma_f32_16x16x32_bf16 v[20:23], a[240:243], a[196:199], v[20:23]\n\t"
"ds_read_b64_tr_b16 v[226:227], v241 offset:22528\n\t"
"v_mfma_f32_16x16x32_bf16 v[24:27], a[236:239], a[192:195], v[24:27]\n\t"
"v_cvt_pk_bf16_f32 v228, v228, v229\n\t"
"v_cvt_pk_bf16_f32 v229, v230, v231\n\t"
"v_mfma_f32_16x16x32_bf16 v[24:27], a[232:235], a[196:199], v[24:27]\n\t"
"v_cvt_pk_bf16_f32 v230, v232, v233\n\t"
"v_cvt_pk_bf16_f32 v231, v234, v235\n\t"
"v_mfma_f32_16x16x32_bf16 v[24:27], a[240:243], a[200:203], v[24:27]\n\t"
"ds_write_b128 v243, v[228:231] offset:28160\n\t"
"v_mfma_f32_16x16x32_bf16 v[28:31], a[236:239], a[196:199], v[28:31]\n\t"
"buffer_load_dwordx4 a[236:239], v252, s[8:11], s20 offen offset:1024 sc1\n\t"
"v_mfma_f32_16x16x32_bf16 v[28:31], a[232:235], a[200:203], v[28:31]\n\t"
"s_waitcnt vmcnt(8)\n\t"
"ds_read_b64_tr_b16 v[232:233], v240 offset:40960\n\t"
"v_mfma_f32_16x16x32_bf16 v[28:31], a[240:243], a[204:207], v[28:31]\n\t"
"ds_read_b64_tr_b16 v[234:235], v240 offset:42496\n\t"
"v_mfma_f32_16x16x32_bf16 v[32:35], a[244:247], a[168:171], v[32:35]\n\t"
"buffer_load_dwordx4 a[232:235], v252, s[8:11], s20 offen offset:2048 sc1\n\t"
"v_mfma_f32_16x16x32_bf16 v[32:35], a[248:251], a[172:175], v[32:35]\n\t"
"s_mov_b32 s20, 1277952\n\t"
"ds_read_b64_tr_b16 v[228:229], v241 offset:40960\n\t"
"v_mfma_f32_16x16x32_bf16 v[32:35], v[96:99], a[176:179], v[32:35]\n\t"
"ds_read_b64_tr_b16 v[230:231], v241 offset:42496\n\t"
"v_mfma_f32_16x16x32_bf16 v[36:39], a[244:247], a[172:175], v[36:39]\n\t"
"buffer_load_dwordx4 a[240:243], v252, s[8:11], s20 offen offset:0 sc1\n\t"
"v_mfma_f32_16x16x32_bf16 v[36:39], a[248:251], a[176:179], v[36:39]\n\t"
"s_waitcnt lgkmcnt(5)\n\t"
"v_mfma_f32_16x16x32_bf16 v[36:39], v[96:99], a[180:183], v[36:39]\n\t"
"v_mfma_f32_16x16x32_bf16 v[236:239], v[236:239], a[208:211], 0\n\t"
"v_mfma_f32_16x16x32_bf16 v[224:227], v[224:227], a[208:211], 0\n\t"
"v_mfma_f32_16x16x32_bf16 v[40:43], a[244:247], a[176:179], v[40:43]\n\t"
"v_mfma_f32_16x16x32_bf16 v[40:43], a[248:251], a[180:183], v[40:43]\n\t"
"v_mfma_f32_16x16x32_bf16 v[40:43], v[96:99], a[184:187], v[40:43]\n\t"
"v_mfma_f32_16x16x32_bf16 v[44:47], a[244:247], a[180:183], v[44:47]\n\t"
"s_waitcnt lgkmcnt(2)\n\t"
"v_cvt_pk_bf16_f32 v236, v236, v237\n\t"
"v_cvt_pk_bf16_f32 v237, v238, v239\n\t"
"v_mfma_f32_16x16x32_bf16 v[44:47], a[248:251], a[184:187], v[44:47]\n\t"
"s_waitcnt lgkmcnt(0)\n\t"
"v_cvt_pk_bf16_f32 v238, v224, v225\n\t"
"v_cvt_pk_bf16_f32 v239, v226, v227\n\t"
"v_mfma_f32_16x16x32_bf16 v[44:47], v[96:99], a[188:191], v[44:47]\n\t"
"ds_write_b128 v243, v[236:239] offset:37376\n\t"
"v_mfma_f32_16x16x32_bf16 v[232:235], v[232:235], a[208:211], 0\n\t"
"v_mfma_f32_16x16x32_bf16 v[228:231], v[228:231], a[208:211], 0\n\t"
"v_mfma_f32_16x16x32_bf16 v[48:51], a[244:247], a[184:187], v[48:51]\n\t"
"buffer_load_dwordx4 a[208:211], v252, s[8:11], s20 offen offset:1024 sc1\n\t"
"v_mfma_f32_16x16x32_bf16 v[48:51], a[248:251], a[188:191], v[48:51]\n\t"
"v_mfma_f32_16x16x32_bf16 v[48:51], v[96:99], a[192:195], v[48:51]\n\t"
"v_mfma_f32_16x16x32_bf16 v[52:55], a[244:247], a[188:191], v[52:55]\n\t"
"v_cvt_pk_bf16_f32 v232, v232, v233\n\t"
"v_cvt_pk_bf16_f32 v233, v234, v235\n\t"
"v_mfma_f32_16x16x32_bf16 v[52:55], a[248:251], a[192:195], v[52:55]\n\t"
"v_cvt_pk_bf16_f32 v234, v228, v229\n\t"
"v_cvt_pk_bf16_f32 v235, v230, v231\n\t"
"v_mfma_f32_16x16x32_bf16 v[52:55], v[96:99], a[196:199], v[52:55]\n\t"
"ds_write_b128 v243, v[232:235] offset:46592\n\t"
"v_mfma_f32_16x16x32_bf16 v[56:59], a[244:247], a[192:195], v[56:59]\n\t"
"v_mfma_f32_16x16x32_bf16 v[56:59], a[248:251], a[196:199], v[56:59]\n\t"
"ds_read_b64_tr_b16 v[224:225], v246 offset:1024\n\t"
"v_mfma_f32_16x16x32_bf16 v[56:59], v[96:99], a[200:203], v[56:59]\n\t"
"ds_read_b64_tr_b16 v[226:227], v246 offset:4096\n\t"
"v_mfma_f32_16x16x32_bf16 v[60:63], a[244:247], a[196:199], v[60:63]\n\t"
"buffer_load_dwordx4 a[244:247], v252, s[8:11], s20 offen offset:2048 sc1\n\t"
"v_mfma_f32_16x16x32_bf16 v[60:63], a[248:251], a[200:203], v[60:63]\n\t"
"s_waitcnt vmcnt(9)\n\t"
"ds_read_b64_tr_b16 v[236:237], v246 offset:7168\n\t"
"s_mov_b32 s20, 1511424\n\t"
"v_mfma_f32_16x16x32_bf16 v[60:63], v[96:99], a[204:207], v[60:63]\n\t"
"ds_read_b64_tr_b16 v[238:239], v246 offset:10240\n\t"
"v_mfma_f32_16x16x32_bf16 v[64:67], a[252:255], a[168:171], v[64:67]\n\t"
"buffer_load_dwordx4 v[96:99], v252, s[8:11], s20 offen offset:0 sc1\n\t"
"v_mfma_f32_16x16x32_bf16 v[64:67], a[212:215], a[172:175], v[64:67]\n\t"
"ds_read_b64_tr_b16 v[228:229], v246 offset:13312\n\t"
"v_mfma_f32_16x16x32_bf16 v[64:67], v[248:251], a[176:179], v[64:67]\n\t"
"ds_read_b64_tr_b16 v[230:231], v246 offset:16384\n\t"
"v_mfma_f32_16x16x32_bf16 v[68:71], a[252:255], a[172:175], v[68:71]\n\t"
"buffer_load_dwordx4 v[240:243], v252, s[8:11], s20 offen offset:1024 sc1\n\t"
"v_mfma_f32_16x16x32_bf16 v[68:71], a[212:215], a[176:179], v[68:71]\n\t"
"ds_read_b128 a[168:171], v244 offset:9216\n\t"
"v_mfma_f32_16x16x32_bf16 v[68:71], v[248:251], a[180:183], v[68:71]\n\t"
"ds_read_b64_tr_b16 v[232:233], v246 offset:20992\n\t"
"v_mfma_f32_16x16x32_bf16 v[72:75], a[252:255], a[176:179], v[72:75]\n\t"
"buffer_load_dwordx4 a[248:251], v252, s[8:11], s20 offen offset:2048 sc1\n\t"
"v_mfma_f32_16x16x32_bf16 v[72:75], a[212:215], a[180:183], v[72:75]\n\t"
"s_waitcnt lgkmcnt(6)\n\t"
"ds_read_b64_tr_b16 v[234:235], v246 offset:24064\n\t"
"v_mfma_f32_16x16x32_bf16 v[72:75], v[248:251], a[184:187], v[72:75]\n\t"
"ds_read_b128 a[172:175], v244 offset:9728\n\t"
"v_mfma_f32_16x16x32_bf16 v[224:227], v[224:227], v[220:223], 0\n\t"
"ds_read_b128 a[176:179], v244 offset:10240\n\t"
"v_mfma_f32_16x16x32_bf16 v[76:79], a[252:255], a[180:183], v[76:79]\n\t"
"s_waitcnt lgkmcnt(7)\n\t"
"ds_read_b128 a[180:183], v244 offset:10752\n\t"
"v_mfma_f32_16x16x32_bf16 v[76:79], a[212:215], a[184:187], v[76:79]\n\t"
"v_mfma_f32_16x16x32_bf16 v[76:79], v[248:251], a[188:191], v[76:79]\n\t"
"v_mfma_f32_16x16x32_bf16 v[236:239], v[236:239], v[220:223], 0\n\t"
"v_mfma_f32_16x16x32_bf16 v[80:83], a[252:255], a[184:187], v[80:83]\n\t"
"s_waitcnt lgkmcnt(6)\n\t"
"ds_read_b128 a[184:187], v244 offset:11264\n\t"
"v_cvt_pk_bf16_f32 v224, v224, v225\n\t"
"v_mfma_f32_16x16x32_bf16 v[80:83], a[212:215], a[188:191], v[80:83]\n\t"
"v_cvt_pk_bf16_f32 v225, v226, v227\n\t"
"v_mfma_f32_16x16x32_bf16 v[80:83], v[248:251], a[192:195], v[80:83]\n\t"
"s_mov_b32 s20, 1523712\n\t"
"v_mfma_f32_16x16x32_bf16 v[228:231], v[228:231], v[220:223], 0\n\t"
"v_mfma_f32_16x16x32_bf16 v[84:87], a[252:255], a[188:191], v[84:87]\n\t"
"s_waitcnt lgkmcnt(4)\n\t"
"ds_read_b128 a[188:191], v244 offset:11776\n\t"
"v_cvt_pk_bf16_f32 v226, v236, v237\n\t"
"v_mfma_f32_16x16x32_bf16 v[84:87], a[212:215], a[192:195], v[84:87]\n\t"
"v_cvt_pk_bf16_f32 v227, v238, v239\n\t"
"v_mfma_f32_16x16x32_bf16 v[84:87], v[248:251], a[196:199], v[84:87]\n\t"
"v_mfma_f32_16x16x32_bf16 v[232:235], v[232:235], v[220:223], 0\n\t"
"v_mfma_f32_16x16x32_bf16 v[88:91], a[252:255], a[192:195], v[88:91]\n\t"
"buffer_load_dwordx4 v[236:239], v252, s[8:11], s20 offen offset:0 sc1\n\t"
"v_mfma_f32_16x16x32_bf16 v[88:91], a[212:215], a[196:199], v[88:91]\n\t"
"ds_read_b128 a[192:195], v244 offset:12288\n\t"
"v_cvt_pk_bf16_f32 v228, v228, v229\n\t"
"v_mfma_f32_16x16x32_bf16 v[88:91], v[248:251], a[200:203], v[88:91]\n\t"
"v_cvt_pk_bf16_f32 v229, v230, v231\n\t"
"v_mfma_f32_16x16x32_bf16 v[92:95], a[252:255], a[196:199], v[92:95]\n\t"
"buffer_load_dwordx4 a[252:255], v252, s[8:11], s20 offen offset:1024 sc1\n\t"
"v_mfma_f32_16x16x32_bf16 v[92:95], a[212:215], a[200:203], v[92:95]\n\t"
"s_waitcnt vmcnt(11) lgkmcnt(4)\n\t"
"ds_read_b128 a[196:199], v244 offset:12800\n\t"
"v_cvt_pk_bf16_f32 v230, v232, v233\n\t"
"v_mfma_f32_16x16x32_bf16 v[92:95], v[248:251], a[204:207], v[92:95]\n\t"
"ds_read_b128 a[200:203], v244 offset:13312\n\t"
"v_cvt_pk_bf16_f32 v231, v234, v235\n\t"
"v_mfma_f32_16x16x32_bf16 v[100:103], a[216:219], a[168:171], v[100:103]\n\t"
"ds_read_b64_tr_b16 v[232:233], v246 offset:27136\n\t"
"v_mfma_f32_16x16x32_bf16 v[100:103], a[220:223], a[172:175], v[100:103]\n\t"
"s_waitcnt lgkmcnt(6)\n\t"
"ds_read_b64_tr_b16 v[234:235], v246 offset:30208\n\t"
"v_mfma_f32_16x16x32_bf16 v[100:103], a[224:227], a[176:179], v[100:103]\n\t"
"s_waitcnt lgkmcnt(0)\n\t"
"ds_read_b128 a[204:207], v244 offset:13824\n\t"
"v_mfma_f32_16x16x32_bf16 v[232:235], v[232:235], v[220:223], 0\n\t"
"buffer_load_dwordx4 v[248:251], v252, s[8:11], s20 offen offset:2048 sc1\n\t"
"s_mov_b32 s20, 1536000\n\t"
"v_mfma_f32_16x16x32_bf16 v[104:107], a[216:219], a[172:175], v[104:107]\n\t"
"buffer_load_dwordx4 a[212:215], v252, s[8:11], s20 offen offset:0 sc1\n\t"
"v_mfma_f32_16x16x32_bf16 v[104:107], a[220:223], a[176:179], v[104:107]\n\t"
"v_mfma_f32_16x16x32_bf16 v[104:107], a[224:227], a[180:183], v[104:107]\n\t"
"s_waitcnt lgkmcnt(0)\n\t"
"s_barrier\n\t"
"v_mfma_f32_16x16x32_bf16 v[108:111], a[216:219], a[176:179], v[108:111]\n\t"
"v_mfma_f32_16x16x32_bf16 v[108:111], a[220:223], a[180:183], v[108:111]\n\t"
"v_cvt_pk_bf16_f32 v232, v232, v233\n\t"
"v_cvt_pk_bf16_f32 v233, v234, v235\n\t"
"v_mfma_f32_16x16x32_bf16 v[108:111], a[224:227], a[184:187], v[108:111]\n\t"
"ds_write2st64_b64 v245, v[224:225], v[226:227] offset0:0 offset1:1\n\t"
"v_mfma_f32_16x16x32_bf16 v[112:115], a[216:219], a[180:183], v[112:115]\n\t"
"v_mfma_f32_16x16x32_bf16 v[112:115], a[220:223], a[184:187], v[112:115]\n\t"
"ds_read_b64_tr_b16 v[224:225], v246 offset:33280\n\t"
"v_mfma_f32_16x16x32_bf16 v[112:115], a[224:227], a[188:191], v[112:115]\n\t"
"ds_write2st64_b64 v245, v[228:229], v[230:231] offset0:2 offset1:3\n\t"
"v_mfma_f32_16x16x32_bf16 v[116:119], a[216:219], a[184:187], v[116:119]\n\t"
"v_mfma_f32_16x16x32_bf16 v[116:119], a[220:223], a[188:191], v[116:119]\n\t"
"ds_read_b64_tr_b16 v[226:227], v246 offset:36352\n\t"
"v_mfma_f32_16x16x32_bf16 v[116:119], a[224:227], a[192:195], v[116:119]\n\t"
"ds_write_b64 v245, v[232:233] offset:2048\n\t"
"v_mfma_f32_16x16x32_bf16 v[120:123], a[216:219], a[188:191], v[120:123]\n\t"
"ds_read_b64_tr_b16 v[228:229], v246 offset:40960\n\t"
"v_mfma_f32_16x16x32_bf16 v[120:123], a[220:223], a[192:195], v[120:123]\n\t"
"ds_read_b64_tr_b16 v[230:231], v246 offset:44032\n\t"
"v_mfma_f32_16x16x32_bf16 v[120:123], a[224:227], a[196:199], v[120:123]\n\t"
"s_waitcnt lgkmcnt(3)\n\t"
"ds_read_b64_tr_b16 v[232:233], v246 offset:47104\n\t"
"v_mfma_f32_16x16x32_bf16 v[224:227], v[224:227], v[220:223], 0\n\t"
"ds_read_b64_tr_b16 v[234:235], v246 offset:50176\n\t"
"v_mfma_f32_16x16x32_bf16 v[124:127], a[216:219], a[192:195], v[124:127]\n\t"
"v_mfma_f32_16x16x32_bf16 v[124:127], a[220:223], a[196:199], v[124:127]\n\t"
"v_mfma_f32_16x16x32_bf16 v[124:127], a[224:227], a[200:203], v[124:127]\n\t"
"s_waitcnt lgkmcnt(2)\n\t"
"v_mfma_f32_16x16x32_bf16 v[228:231], v[228:231], v[220:223], 0\n\t"
"v_mfma_f32_16x16x32_bf16 v[128:131], a[216:219], a[196:199], v[128:131]\n\t"
"buffer_load_dwordx4 a[216:219], v252, s[8:11], s20 offen offset:1024 sc1\n\t"
"v_mfma_f32_16x16x32_bf16 v[128:131], a[220:223], a[200:203], v[128:131]\n\t"
"s_waitcnt vmcnt(11) lgkmcnt(0)\n\t"
"v_cvt_pk_bf16_f32 v224, v224, v225\n\t"
"v_cvt_pk_bf16_f32 v225, v226, v227\n\t"
"v_mfma_f32_16x16x32_bf16 v[128:131], a[224:227], a[204:207], v[128:131]\n\t"
"v_mfma_f32_16x16x32_bf16 v[232:235], v[232:235], v[220:223], 0\n\t"
"buffer_load_dwordx4 a[220:223], v252, s[8:11], s20 offen offset:2048 sc1\n\t"
"v_mfma_f32_16x16x32_bf16 v[132:135], a[228:231], a[168:171], v[132:135]\n\t"
"s_mov_b32 s20, 1548288\n\t"
"v_cvt_pk_bf16_f32 v226, v228, v229\n\t"
"v_cvt_pk_bf16_f32 v227, v230, v231\n\t"
"v_mfma_f32_16x16x32_bf16 v[132:135], a[236:239], a[172:175], v[132:135]\n\t"
"ds_read_b64_tr_b16 v[228:229], v246 offset:53248\n\t"
"v_mfma_f32_16x16x32_bf16 v[132:135], a[232:235], a[176:179], v[132:135]\n\t"
"buffer_load_dwordx4 a[224:227], v252, s[8:11], s20 offen offset:0 sc1\n\t"
"v_mfma_f32_16x16x32_bf16 v[136:139], a[228:231], a[172:175], v[136:139]\n\t"
"ds_read_b64_tr_b16 v[230:231], v246 offset:56320\n\t"
"v_mfma_f32_16x16x32_bf16 v[136:139], a[236:239], a[176:179], v[136:139]\n\t"
"v_cvt_pk_bf16_f32 v232, v232, v233\n\t"
"v_cvt_pk_bf16_f32 v233, v234, v235\n\t"
"v_mfma_f32_16x16x32_bf16 v[136:139], a[232:235], a[180:183], v[136:139]\n\t"
"ds_write2st64_b64 v245, v[224:225], v[226:227] offset0:5 offset1:6\n\t"
"v_mfma_f32_16x16x32_bf16 v[140:143], a[228:231], a[176:179], v[140:143]\n\t"
"v_mfma_f32_16x16x32_bf16 v[140:143], a[236:239], a[180:183], v[140:143]\n\t"
"v_mfma_f32_16x16x32_bf16 v[140:143], a[232:235], a[184:187], v[140:143]\n\t"
"v_mfma_f32_16x16x32_bf16 v[144:147], a[228:231], a[180:183], v[144:147]\n\t"
"v_mfma_f32_16x16x32_bf16 v[144:147], a[236:239], a[184:187], v[144:147]\n\t"
"v_mfma_f32_16x16x32_bf16 v[144:147], a[232:235], a[188:191], v[144:147]\n\t"
"v_mfma_f32_16x16x32_bf16 v[148:151], a[228:231], a[184:187], v[148:151]\n\t"
"s_waitcnt lgkmcnt(1)\n\t"
"v_mfma_f32_16x16x32_bf16 v[148:151], a[236:239], a[188:191], v[148:151]\n\t"
"v_mfma_f32_16x16x32_bf16 v[148:151], a[232:235], a[192:195], v[148:151]\n\t"
"v_mfma_f32_16x16x32_bf16 v[228:231], v[228:231], v[220:223], 0\n\t"
"v_mfma_f32_16x16x32_bf16 v[152:155], a[228:231], a[188:191], v[152:155]\n\t"
"v_mfma_f32_16x16x32_bf16 v[152:155], a[236:239], a[192:195], v[152:155]\n\t"
"v_mfma_f32_16x16x32_bf16 v[152:155], a[232:235], a[196:199], v[152:155]\n\t"
"v_mfma_f32_16x16x32_bf16 v[156:159], a[228:231], a[192:195], v[156:159]\n\t"
"v_mfma_f32_16x16x32_bf16 v[156:159], a[236:239], a[196:199], v[156:159]\n\t"
"v_cvt_pk_bf16_f32 v234, v228, v229\n\t"
"v_cvt_pk_bf16_f32 v235, v230, v231\n\t"
"v_mfma_f32_16x16x32_bf16 v[156:159], a[232:235], a[200:203], v[156:159]\n\t"
"ds_write2st64_b64 v245, v[232:233], v[234:235] offset0:7 offset1:8\n\t"
"v_mfma_f32_16x16x32_bf16 v[160:163], a[228:231], a[196:199], v[160:163]\n\t"
"buffer_load_dwordx4 a[228:231], v252, s[8:11], s20 offen offset:1024 sc1\n\t"
"v_mfma_f32_16x16x32_bf16 v[160:163], a[236:239], a[200:203], v[160:163]\n\t"
"s_waitcnt vmcnt(11)\n\t"
"v_mfma_f32_16x16x32_bf16 v[160:163], a[232:235], a[204:207], v[160:163]\n\t"
"v_mfma_f32_16x16x32_bf16 v[164:167], a[240:243], a[168:171], v[164:167]\n\t"
"buffer_load_dwordx4 a[232:235], v252, s[8:11], s20 offen offset:2048 sc1\n\t"
"v_mfma_f32_16x16x32_bf16 v[164:167], a[208:211], a[172:175], v[164:167]\n\t"
"s_mov_b32 s20, 1560576\n\t"
"v_mfma_f32_16x16x32_bf16 v[164:167], a[244:247], a[176:179], v[164:167]\n\t"
"s_waitcnt lgkmcnt(0)\n\t"
"s_barrier\n\t"
"v_mfma_f32_16x16x32_bf16 v[168:171], a[240:243], a[172:175], v[168:171]\n\t"
"ds_read_b128 a[168:171], v244 offset:0\n\t"
"v_mfma_f32_16x16x32_bf16 v[168:171], a[208:211], a[176:179], v[168:171]\n\t"
"buffer_load_dwordx4 a[236:239], v252, s[8:11], s20 offen offset:0 sc1\n\t"
"v_mfma_f32_16x16x32_bf16 v[168:171], a[244:247], a[180:183], v[168:171]\n\t"
"ds_read_b128 a[172:175], v244 offset:512\n\t"
"v_mfma_f32_16x16x32_bf16 v[172:175], a[240:243], a[176:179], v[172:175]\n\t"
"ds_read_b128 a[176:179], v244 offset:1024\n\t"
"v_mfma_f32_16x16x32_bf16 v[172:175], a[208:211], a[180:183], v[172:175]\n\t"
"v_mfma_f32_16x16x32_bf16 v[172:175], a[244:247], a[184:187], v[172:175]\n\t"
"v_mfma_f32_16x16x32_bf16 v[176:179], a[240:243], a[180:183], v[176:179]\n\t"
"ds_read_b128 a[180:183], v244 offset:1536\n\t"
"v_mfma_f32_16x16x32_bf16 v[176:179], a[208:211], a[184:187], v[176:179]\n\t"
"v_mfma_f32_16x16x32_bf16 v[176:179], a[244:247], a[188:191], v[176:179]\n\t"
"v_mfma_f32_16x16x32_bf16 v[180:183], a[240:243], a[184:187], v[180:183]\n\t"
"ds_read_b128 a[184:187], v244 offset:2048\n\t"
"v_mfma_f32_16x16x32_bf16 v[180:183], a[208:211], a[188:191], v[180:183]\n\t"
"v_mfma_f32_16x16x32_bf16 v[180:183], a[244:247], a[192:195], v[180:183]\n\t"
"v_mfma_f32_16x16x32_bf16 v[184:187], a[240:243], a[188:191], v[184:187]\n\t"
"ds_read_b128 a[188:191], v244 offset:2560\n\t"
"v_mfma_f32_16x16x32_bf16 v[184:187], a[208:211], a[192:195], v[184:187]\n\t"
"v_mfma_f32_16x16x32_bf16 v[184:187], a[244:247], a[196:199], v[184:187]\n\t"
"v_mfma_f32_16x16x32_bf16 v[188:191], a[240:243], a[192:195], v[188:191]\n\t"
"ds_read_b128 a[192:195], v244 offset:3072\n\t"
"v_mfma_f32_16x16x32_bf16 v[188:191], a[208:211], a[196:199], v[188:191]\n\t"
"v_mfma_f32_16x16x32_bf16 v[188:191], a[244:247], a[200:203], v[188:191]\n\t"
"v_mfma_f32_16x16x32_bf16 v[192:195], a[240:243], a[196:199], v[192:195]\n\t"
"buffer_load_dwordx4 a[240:243], v252, s[8:11], s20 offen offset:1024 sc1\n\t"
"v_mfma_f32_16x16x32_bf16 v[192:195], a[208:211], a[200:203], v[192:195]\n\t"
"s_waitcnt vmcnt(11) lgkmcnt(4)\n\t"
"ds_read_b128 a[196:199], v244 offset:3584\n\t"
"v_mfma_f32_16x16x32_bf16 v[192:195], a[244:247], a[204:207], v[192:195]\n\t"
"ds_read_b128 a[200:203], v244 offset:4096\n\t"
"Lst12_%=:\n\t"
"v_mfma_f32_16x16x32_bf16 v[0:3], v[96:99], a[168:171], v[0:3]\n\t"
"buffer_load_dwordx4 a[208:211], v252, s[8:11], s20 offen offset:2048 sc1\n\t"
"v_mfma_f32_16x16x32_bf16 v[0:3], v[240:243], a[172:175], v[0:3]\n\t"
"s_waitcnt lgkmcnt(5)\n\t"
"ds_read_b128 a[204:207], v244 offset:4608\n\t"
"v_xor_b32 v246, 0xc0, v246\n\t"
"v_mfma_f32_16x16x32_bf16 v[0:3], a[248:251], a[176:179], v[0:3]\n\t"
"s_mov_b32 s20, 1572864\n\t"
"v_mfma_f32_16x16x32_bf16 v[4:7], v[96:99], a[172:175], v[4:7]\n\t"
"buffer_load_dwordx4 a[244:247], v252, s[8:11], s20 offen offset:0 sc1\n\t"
"v_mfma_f32_16x16x32_bf16 v[4:7], v[240:243], a[176:179], v[4:7]\n\t"
"s_waitcnt lgkmcnt(5)\n\t"
"v_mfma_f32_16x16x32_bf16 v[4:7], a[248:251], a[180:183], v[4:7]\n\t"
"v_mfma_f32_16x16x32_bf16 v[8:11], v[96:99], a[176:179], v[8:11]\n\t"
"s_waitcnt lgkmcnt(4)\n\t"
"v_mfma_f32_16x16x32_bf16 v[8:11], v[240:243], a[180:183], v[8:11]\n\t"
"v_mfma_f32_16x16x32_bf16 v[8:11], a[248:251], a[184:187], v[8:11]\n\t"
"v_mfma_f32_16x16x32_bf16 v[12:15], v[96:99], a[180:183], v[12:15]\n\t"
"s_waitcnt lgkmcnt(3)\n\t"
"v_mfma_f32_16x16x32_bf16 v[12:15], v[240:243], a[184:187], v[12:15]\n\t"
"v_mfma_f32_16x16x32_bf16 v[12:15], a[248:251], a[188:191], v[12:15]\n\t"
"v_mfma_f32_16x16x32_bf16 v[16:19], v[96:99], a[184:187], v[16:19]\n\t"
"s_waitcnt lgkmcnt(2)\n\t"
"v_mfma_f32_16x16x32_bf16 v[16:19], v[240:243], a[188:191], v[16:19]\n\t"
"v_mfma_f32_16x16x32_bf16 v[16:19], a[248:251], a[192:195], v[16:19]\n\t"
"ds_read_b64_tr_b16 v[224:225], v246 offset:1024\n\t"
"v_mfma_f32_16x16x32_bf16 v[20:23], v[96:99], a[188:191], v[20:23]\n\t"
"s_waitcnt lgkmcnt(2)\n\t"
"ds_read_b64_tr_b16 v[226:227], v246 offset:4096\n\t"
"v_mfma_f32_16x16x32_bf16 v[20:23], v[240:243], a[192:195], v[20:23]\n\t"
"v_mfma_f32_16x16x32_bf16 v[20:23], a[248:251], a[196:199], v[20:23]\n\t"
"v_mfma_f32_16x16x32_bf16 v[24:27], v[96:99], a[192:195], v[24:27]\n\t"
"s_waitcnt lgkmcnt(2)\n\t"
"v_mfma_f32_16x16x32_bf16 v[24:27], v[240:243], a[196:199], v[24:27]\n\t"
"v_mfma_f32_16x16x32_bf16 v[24:27], a[248:251], a[200:203], v[24:27]\n\t"
"v_mfma_f32_16x16x32_bf16 v[28:31], v[96:99], a[196:199], v[28:31]\n\t"
"buffer_load_dwordx4 v[96:99], v252, s[8:11], s20 offen offset:1024 sc1\n\t"
"v_mfma_f32_16x16x32_bf16 v[28:31], v[240:243], a[200:203], v[28:31]\n\t"
"s_waitcnt vmcnt(11)\n\t"
"ds_read_b64_tr_b16 v[228:229], v246 offset:7168\n\t"
"v_mfma_f32_16x16x32_bf16 v[28:31], a[248:251], a[204:207], v[28:31]\n\t"
"s_add_u32 s21, s21, 6291456\n\t"
"v_add_u32 v254, 6291456, v254\n\t"
"ds_read_b64_tr_b16 v[230:231], v246 offset:10240\n\t"
"v_mfma_f32_16x16x32_bf16 v[32:35], v[236:239], a[168:171], v[32:35]\n\t"
"buffer_load_dwordx4 v[240:243], v252, s[8:11], s20 offen offset:2048 sc1\n\t"
"v_mfma_f32_16x16x32_bf16 v[32:35], a[252:255], a[172:175], v[32:35]\n\t"
"s_mov_b32 s20, 1585152\n\t"
"v_mfma_f32_16x16x32_bf16 v[32:35], v[248:251], a[176:179], v[32:35]\n\t"
"v_mfma_f32_16x16x32_bf16 v[36:39], v[236:239], a[172:175], v[36:39]\n\t"
"buffer_load_dwordx4 a[248:251], v252, s[8:11], s20 offen offset:0 sc1\n\t"
"v_mfma_f32_16x16x32_bf16 v[36:39], a[252:255], a[176:179], v[36:39]\n\t"
"s_waitcnt lgkmcnt(2)\n\t"
"v_mfma_f32_16x16x32_bf16 v[36:39], v[248:251], a[180:183], v[36:39]\n\t"
"v_mfma_f32_16x16x32_bf16 v[224:227], v[224:227], v[220:223], 0\n\t"
"v_mfma_f32_16x16x32_bf16 v[40:43], v[236:239], a[176:179], v[40:43]\n\t"
"ds_read_b64_tr_b16 v[232:233], v246 offset:13312\n\t"
"v_mfma_f32_16x16x32_bf16 v[40:43], a[252:255], a[180:183], v[40:43]\n\t"
"ds_read_b64_tr_b16 v[234:235], v246 offset:16384\n\t"
"v_mfma_f32_16x16x32_bf16 v[40:43], v[248:251], a[184:187], v[40:43]\n\t"
"v_mfma_f32_16x16x32_bf16 v[44:47], v[236:239], a[180:183], v[44:47]\n\t"
"s_waitcnt lgkmcnt(2)\n\t"
"v_mfma_f32_16x16x32_bf16 v[44:47], a[252:255], a[184:187], v[44:47]\n\t"
"v_cvt_pk_bf16_f32 v224, v224, v225\n\t"
"v_cvt_pk_bf16_f32 v225, v226, v227\n\t"
"v_mfma_f32_16x16x32_bf16 v[44:47], v[248:251], a[188:191], v[44:47]\n\t"
"v_mfma_f32_16x16x32_bf16 v[228:231], v[228:231], v[220:223], 0\n\t"
"v_mfma_f32_16x16x32_bf16 v[48:51], v[236:239], a[184:187], v[48:51]\n\t"
"v_mfma_f32_16x16x32_bf16 v[48:51], a[252:255], a[188:191], v[48:51]\n\t"
"v_mfma_f32_16x16x32_bf16 v[48:51], v[248:251], a[192:195], v[48:51]\n\t"
"v_mfma_f32_16x16x32_bf16 v[52:55], v[236:239], a[188:191], v[52:55]\n\t"
"v_mfma_f32_16x16x32_bf16 v[52:55], a[252:255], a[192:195], v[52:55]\n\t"
"v_cvt_pk_bf16_f32 v226, v228, v229\n\t"
"v_cvt_pk_bf16_f32 v227, v230, v231\n\t"
"v_mfma_f32_16x16x32_bf16 v[52:55], v[248:251], a[196:199], v[52:55]\n\t"
"ds_read_b64_tr_b16 v[228:229], v246 offset:20992\n\t"
"v_mfma_f32_16x16x32_bf16 v[56:59], v[236:239], a[192:195], v[56:59]\n\t"
"s_waitcnt lgkmcnt(1)\n\t"
"ds_read_b64_tr_b16 v[230:231], v246 offset:24064\n\t"
"v_mfma_f32_16x16x32_bf16 v[56:59], a[252:255], a[196:199], v[56:59]\n\t"
"v_mfma_f32_16x16x32_bf16 v[56:59], v[248:251], a[200:203], v[56:59]\n\t"
"v_mfma_f32_16x16x32_bf16 v[232:235], v[232:235], v[220:223], 0\n\t"
"v_mfma_f32_16x16x32_bf16 v[60:63], v[236:239], a[196:199], v[60:63]\n\t"
"buffer_load_dwordx4 v[236:239], v252, s[8:11], s20 offen offset:1024 sc1\n\t"
"v_mfma_f32_16x16x32_bf16 v[60:63], a[252:255], a[200:203], v[60:63]\n\t"
"s_waitcnt vmcnt(11)\n\t"
"v_mfma_f32_16x16x32_bf16 v[60:63], v[248:251], a[204:207], v[60:63]\n\t"
"v_mfma_f32_16x16x32_bf16 v[64:67], a[212:215], a[168:171], v[64:67]\n\t"
"buffer_load_dwordx4 v[248:251], v252, s[8:11], s20 offen offset:2048 sc1\n\t"
"v_mfma_f32_16x16x32_bf16 v[64:67], a[216:219], a[172:175], v[64:67]\n\t"
"s_mov_b32 s20, 1597440\n\t"
"ds_read_b128 a[168:171], v244 offset:9216\n\t"
"v_cvt_pk_bf16_f32 v232, v232, v233\n\t"
"v_mfma_f32_16x16x32_bf16 v[64:67], a[220:223], a[176:179], v[64:67]\n\t"
"v_cvt_pk_bf16_f32 v233, v234, v235\n\t"
"v_mfma_f32_16x16x32_bf16 v[68:71], a[212:215], a[172:175], v[68:71]\n\t"
"buffer_load_dwordx4 a[252:255], v252, s[8:11], s20 offen offset:0 sc1\n\t"
"v_mfma_f32_16x16x32_bf16 v[68:71], a[216:219], a[176:179], v[68:71]\n\t"
"s_waitcnt lgkmcnt(1)\n\t"
"ds_read_b128 a[172:175], v244 offset:9728\n\t"
"v_mfma_f32_16x16x32_bf16 v[68:71], a[220:223], a[180:183], v[68:71]\n\t"
"v_mfma_f32_16x16x32_bf16 v[228:231], v[228:231], v[220:223], 0\n\t"
"v_mfma_f32_16x16x32_bf16 v[72:75], a[212:215], a[176:179], v[72:75]\n\t"
"ds_read_b128 a[176:179], v244 offset:10240\n\t"
"v_mfma_f32_16x16x32_bf16 v[72:75], a[216:219], a[180:183], v[72:75]\n\t"
"v_mfma_f32_16x16x32_bf16 v[72:75], a[220:223], a[184:187], v[72:75]\n\t"
"v_mfma_f32_16x16x32_bf16 v[76:79], a[212:215], a[180:183], v[76:79]\n\t"
"ds_read_b128 a[180:183], v244 offset:10752\n\t"
"v_mfma_f32_16x16x32_bf16 v[76:79], a[216:219], a[184:187], v[76:79]\n\t"
"v_cvt_pk_bf16_f32 v234, v228, v229\n\t"
"v_cvt_pk_bf16_f32 v235, v230, v231\n\t"
"v_mfma_f32_16x16x32_bf16 v[76:79], a[220:223], a[188:191], v[76:79]\n\t"
"ds_read_b64_tr_b16 v[228:229], v246 offset:27136\n\t"
"v_mfma_f32_16x16x32_bf16 v[80:83], a[212:215], a[184:187], v[80:83]\n\t"
"ds_read_b64_tr_b16 v[230:231], v246 offset:30208\n\t"
"v_mfma_f32_16x16x32_bf16 v[80:83], a[216:219], a[188:191], v[80:83]\n\t"
"ds_read_b128 a[184:187], v244 offset:11264\n\t"
"v_mfma_f32_16x16x32_bf16 v[80:83], a[220:223], a[192:195], v[80:83]\n\t"
"v_mfma_f32_16x16x32_bf16 v[84:87], a[212:215], a[188:191], v[84:87]\n\t"
"ds_read_b128 a[188:191], v244 offset:11776\n\t"
"v_mfma_f32_16x16x32_bf16 v[84:87], a[216:219], a[192:195], v[84:87]\n\t"
"v_mfma_f32_16x16x32_bf16 v[84:87], a[220:223], a[196:199], v[84:87]\n\t"
"v_mfma_f32_16x16x32_bf16 v[88:91], a[212:215], a[192:195], v[88:91]\n\t"
"ds_read_b128 a[192:195], v244 offset:12288\n\t"
"v_mfma_f32_16x16x32_bf16 v[88:91], a[216:219], a[196:199], v[88:91]\n\t"
"v_mfma_f32_16x16x32_bf16 v[88:91], a[220:223], a[200:203], v[88:91]\n\t"
"v_mfma_f32_16x16x32_bf16 v[92:95], a[212:215], a[196:199], v[92:95]\n\t"
"buffer_load_dwordx4 a[212:215], v252, s[8:11], s20 offen offset:1024 sc1\n\t"
"v_mfma_f32_16x16x32_bf16 v[92:95], a[216:219], a[200:203], v[92:95]\n\t"
"s_waitcnt vmcnt(11) lgkmcnt(3)\n\t"
"ds_read_b128 a[196:199], v244 offset:12800\n\t"
"v_mfma_f32_16x16x32_bf16 v[92:95], a[220:223], a[204:207], v[92:95]\n\t"
"ds_read_b128 a[200:203], v244 offset:13312\n\t"
"v_mfma_f32_16x16x32_bf16 v[228:231], v[228:231], v[220:223], 0\n\t"
"ds_read_b128 a[204:207], v244 offset:13824\n\t"
"v_mfma_f32_16x16x32_bf16 v[100:103], a[224:227], a[168:171], v[100:103]\n\t"
"buffer_load_dwordx4 a[216:219], v252, s[8:11], s20 offen offset:2048 sc1\n\t"
"v_mfma_f32_16x16x32_bf16 v[100:103], a[228:231], a[172:175], v[100:103]\n\t"
"s_mov_b32 s20, 1609728\n\t"
"v_mfma_f32_16x16x32_bf16 v[100:103], a[232:235], a[176:179], v[100:103]\n\t"
"buffer_load_dwordx4 a[220:223], v252, s[8:11], s20 offen offset:0 sc1\n\t"
"v_mfma_f32_16x16x32_bf16 v[104:107], a[224:227], a[172:175], v[104:107]\n\t"
"s_waitcnt lgkmcnt(5)\n\t"
"s_add_u32 m0, s26, 512\n\t"
"v_mfma_f32_16x16x32_bf16 v[104:107], a[228:231], a[176:179], v[104:107]\n\t"
"v_cvt_pk_bf16_f32 v228, v228, v229\n\t"
"v_mfma_f32_16x16x32_bf16 v[104:107], a[232:235], a[180:183], v[104:107]\n\t"
"s_waitcnt lgkmcnt(0)\n\t"
"s_barrier\n\t"
"v_mfma_f32_16x16x32_bf16 v[108:111], a[224:227], a[176:179], v[108:111]\n\t"
"v_cvt_pk_bf16_f32 v229, v230, v231\n\t"
"v_mfma_f32_16x16x32_bf16 v[108:111], a[228:231], a[180:183], v[108:111]\n\t"
"v_fma_f32 v0, v100, s52, v0\n\t"
"v_fma_f32 v1, v101, s52, v1\n\t"
"v_mfma_f32_16x16x32_bf16 v[108:111], a[232:235], a[184:187], v[108:111]\n\t"
"s_mov_b64 exec, s[36:37]\n\t"
"buffer_load_dwordx4 v254, s[12:15], s27 offen offset:0 nt lds\n\t"
"s_mov_b64 exec, -1\n\t"
"s_add_u32 m0, s24, 80384\n\t"
"s_add_u32 s28, s21, 2359296\n\t"
"v_mfma_f32_16x16x32_bf16 v[112:115], a[224:227], a[180:183], v[112:115]\n\t"
"buffer_load_dwordx4 v247, s[12:15], s28 offen offset:0 nt lds\n\t"
"v_mfma_f32_16x16x32_bf16 v[112:115], a[228:231], a[184:187], v[112:115]\n\t"
"s_add_u32 m0, s24, 86528\n\t"
"s_add_u32 s28, s21, 2555904\n\t"
"v_fma_f32 v2, v102, s52, v2\n\t"
"v_mfma_f32_16x16x32_bf16 v[112:115], a[232:235], a[188:191], v[112:115]\n\t"
"ds_write2st64_b64 v245, v[224:225], v[226:227] offset0:0 offset1:1\n\t"
"v_mfma_f32_16x16x32_bf16 v[116:119], a[224:227], a[184:187], v[116:119]\n\t"
"buffer_load_dwordx4 v247, s[12:15], s28 offen offset:0 nt lds\n\t"
"v_mfma_f32_16x16x32_bf16 v[116:119], a[228:231], a[188:191], v[116:119]\n\t"
"s_add_u32 m0, s24, 60416\n\t"
"s_add_u32 s28, s21, 1769472\n\t"
"ds_read_b64_tr_b16 v[224:225], v246 offset:33280\n\t"
"v_mfma_f32_16x16x32_bf16 v[116:119], a[232:235], a[192:195], v[116:119]\n\t"
"ds_write2st64_b64 v245, v[232:233], v[234:235] offset0:2 offset1:3\n\t"
"ds_read_b64_tr_b16 v[226:227], v246 offset:36352\n\t"
"v_mfma_f32_16x16x32_bf16 v[120:123], a[224:227], a[188:191], v[120:123]\n\t"
"buffer_load_dwordx4 v247, s[12:15], s28 offen offset:0 nt lds\n\t"
"v_mfma_f32_16x16x32_bf16 v[120:123], a[228:231], a[192:195], v[120:123]\n\t"
"s_add_u32 m0, s24, 66560\n\t"
"s_add_u32 s28, s21, 1966080\n\t"
"ds_read_b64_tr_b16 v[232:233], v246 offset:40960\n\t"
"v_mfma_f32_16x16x32_bf16 v[120:123], a[232:235], a[196:199], v[120:123]\n\t"
"s_waitcnt lgkmcnt(1)\n\t"
"ds_write_b64 v245, v[228:229] offset:2048\n\t"
"ds_read_b64_tr_b16 v[234:235], v246 offset:44032\n\t"
"v_mfma_f32_16x16x32_bf16 v[224:227], v[224:227], v[220:223], 0\n\t"
"buffer_load_dwordx4 v247, s[12:15], s28 offen offset:0 nt lds\n\t"
"v_mfma_f32_16x16x32_bf16 v[124:127], a[224:227], a[192:195], v[124:127]\n\t"
"ds_read_b64_tr_b16 v[228:229], v246 offset:47104\n\t"
"v_fma_f32 v3, v103, s52, v3\n\t"
"v_mfma_f32_16x16x32_bf16 v[124:127], a[228:231], a[196:199], v[124:127]\n\t"
"ds_read_b64_tr_b16 v[230:231], v246 offset:50176\n\t"
"v_fma_f32 v100, v0, s54, -v100\n\t"
"v_mfma_f32_16x16x32_bf16 v[124:127], a[232:235], a[200:203], v[124:127]\n\t"
"s_waitcnt lgkmcnt(2)\n\t"
"v_fma_f32 v101, v1, s54, -v101\n\t"
"v_fma_f32 v102, v2, s54, -v102\n\t"
"v_mfma_f32_16x16x32_bf16 v[232:235], v[232:235], v[220:223], 0\n\t"
"v_fma_f32 v103, v3, s54, -v103\n\t"
"v_fma_f32 v4, v104, s52, v4\n\t"
"v_mfma_f32_16x16x32_bf16 v[128:131], a[224:227], a[196:199], v[128:131]\n\t"
"v_cvt_pk_bf16_f32 v224, v224, v225\n\t"
"v_cvt_pk_bf16_f32 v225, v226, v227\n\t"
"v_mfma_f32_16x16x32_bf16 v[128:131], a[228:231], a[200:203], v[128:131]\n\t"
"s_waitcnt vmcnt(15)\n\t"
"v_fma_f32 v5, v105, s52, v5\n\t"
"v_fma_f32 v6, v106, s52, v6\n\t"
"v_mfma_f32_16x16x32_bf16 v[128:131], a[232:235], a[204:207], v[128:131]\n\t"
"s_waitcnt lgkmcnt(0)\n\t"
"v_fma_f32 v7, v107, s52, v7\n\t"
"v_fma_f32 v104, v4, s54, -v104\n\t"
"v_mfma_f32_16x16x32_bf16 v[228:231], v[228:231], v[220:223], 0\n\t"
"v_fma_f32 v105, v5, s54, -v105\n\t"
"v_fma_f32 v106, v6, s54, -v106\n\t"
"v_mfma_f32_16x16x32_bf16 v[132:135], a[236:239], a[168:171], v[132:135]\n\t"
"v_cvt_pk_bf16_f32 v226, v232, v233\n\t"
"v_cvt_pk_bf16_f32 v227, v234, v235\n\t"
"v_mfma_f32_16x16x32_bf16 v[132:135], a[240:243], a[172:175], v[132:135]\n\t"
"ds_read_b64_tr_b16 v[232:233], v246 offset:53248\n\t"
"v_fma_f32 v107, v7, s54, -v107\n\t"
"v_mfma_f32_16x16x32_bf16 v[132:135], a[208:211], a[176:179], v[132:135]\n\t"
"ds_write2st64_b64 v245, v[224:225], v[226:227] offset0:5 offset1:6\n\t"
"v_mfma_f32_16x16x32_bf16 v[136:139], a[236:239], a[172:175], v[136:139]\n\t"
"v_fma_f32 v8, v108, s52, v8\n\t"
"v_fma_f32 v9, v109, s52, v9\n\t"
"v_mfma_f32_16x16x32_bf16 v[136:139], a[240:243], a[176:179], v[136:139]\n\t"
"ds_read_b64_tr_b16 v[234:235], v246 offset:56320\n\t"
"v_mfma_f32_16x16x32_bf16 v[136:139], a[208:211], a[180:183], v[136:139]\n\t"
"v_cvt_pk_bf16_f32 v228, v228, v229\n\t"
"v_cvt_pk_bf16_f32 v229, v230, v231\n\t"
"v_mfma_f32_16x16x32_bf16 v[140:143], a[236:239], a[176:179], v[140:143]\n\t"
"Lpg9_%=:\n\t"
"s_cbranch_vccnz Lvr0_%=\n\t"
"v_fma_f32 v10, v110, s52, v10\n\t"
"v_fma_f32 v11, v111, s52, v11\n\t"
"v_mfma_f32_16x16x32_bf16 v[140:143], a[240:243], a[180:183], v[140:143]\n\t"
"v_fma_f32 v108, v8, s54, -v108\n\t"
"v_fma_f32 v109, v9, s54, -v109\n\t"
"v_mfma_f32_16x16x32_bf16 v[140:143], a[208:211], a[184:187], v[140:143]\n\t"
"v_fma_f32 v110, v10, s54, -v110\n\t"
"v_fma_f32 v111, v11, s54, -v111\n\t"
"v_mfma_f32_16x16x32_bf16 v[144:147], a[236:239], a[180:183], v[144:147]\n\t"
"v_fma_f32 v12, v112, s52, v12\n\t"
"v_fma_f32 v13, v113, s52, v13\n\t"
"v_mfma_f32_16x16x32_bf16 v[144:147], a[240:243], a[184:187], v[144:147]\n\t"
"v_fma_f32 v14, v114, s52, v14\n\t"
"v_fma_f32 v15, v115, s52, v15\n\t"
"v_mfma_f32_16x16x32_bf16 v[144:147], a[208:211], a[188:191], v[144:147]\n\t"
"buffer_load_dwordx4 a[224:227], v252, s[8:11], s20 offen offset:1024 sc1\n\t"
"v_mfma_f32_16x16x32_bf16 v[148:151], a[236:239], a[184:187], v[148:151]\n\t"
"s_waitcnt lgkmcnt(0)\n\t"
"v_fma_f32 v112, v12, s54, -v112\n\t"
"v_fma_f32 v113, v13, s54, -v113\n\t"
"v_mfma_f32_16x16x32_bf16 v[148:151], a[240:243], a[188:191], v[148:151]\n\t"
"v_fma_f32 v114, v14, s54, -v114\n\t"
"v_fma_f32 v115, v15, s54, -v115\n\t"
"v_mfma_f32_16x16x32_bf16 v[148:151], a[208:211], a[192:195], v[148:151]\n\t"
"v_fma_f32 v16, v116, s52, v16\n\t"
"v_fma_f32 v17, v117, s52, v17\n\t"
"v_mfma_f32_16x16x32_bf16 v[232:235], v[232:235], v[220:223], 0\n\t"
"v_fma_f32 v18, v118, s52, v18\n\t"
"v_fma_f32 v19, v119, s52, v19\n\t"
"v_mfma_f32_16x16x32_bf16 v[152:155], a[236:239], a[188:191], v[152:155]\n\t"
"v_fma_f32 v116, v16, s54, -v116\n\t"
"v_fma_f32 v117, v17, s54, -v117\n\t"
"v_mfma_f32_16x16x32_bf16 v[152:155], a[240:243], a[192:195], v[152:155]\n\t"
"v_fma_f32 v118, v18, s54, -v118\n\t"
"v_fma_f32 v119, v19, s54, -v119\n\t"
"v_mfma_f32_16x16x32_bf16 v[152:155], a[208:211], a[196:199], v[152:155]\n\t"
"v_fma_f32 v20, v120, s52, v20\n\t"
"v_fma_f32 v21, v121, s52, v21\n\t"
"v_mfma_f32_16x16x32_bf16 v[156:159], a[236:239], a[192:195], v[156:159]\n\t"
"v_fma_f32 v22, v122, s52, v22\n\t"
"v_fma_f32 v23, v123, s52, v23\n\t"
"v_mfma_f32_16x16x32_bf16 v[156:159], a[240:243], a[196:199], v[156:159]\n\t"
"v_cvt_pk_bf16_f32 v230, v232, v233\n\t"
"v_cvt_pk_bf16_f32 v231, v234, v235\n\t"
"v_mfma_f32_16x16x32_bf16 v[156:159], a[208:211], a[200:203], v[156:159]\n\t"
"ds_write2st64_b64 v245, v[228:229], v[230:231] offset0:7 offset1:8\n\t"
"v_mfma_f32_16x16x32_bf16 v[160:163], a[236:239], a[196:199], v[160:163]\n\t"
"s_waitcnt vmcnt(13)\n\t"
"v_fma_f32 v120, v20, s54, -v120\n\t"
"v_fma_f32 v121, v21, s54, -v121\n\t"
"v_mfma_f32_16x16x32_bf16 v[160:163], a[240:243], a[200:203], v[160:163]\n\t"
"v_fma_f32 v122, v22, s54, -v122\n\t"
"v_fma_f32 v123, v23, s54, -v123\n\t"
"v_mfma_f32_16x16x32_bf16 v[160:163], a[208:211], a[204:207], v[160:163]\n\t"
"v_fma_f32 v24, v124, s52, v24\n\t"
"v_fma_f32 v25, v125, s52, v25\n\t"
"v_mfma_f32_16x16x32_bf16 v[164:167], a[244:247], a[168:171], v[164:167]\n\t"
"v_fma_f32 v26, v126, s52, v26\n\t"
"v_fma_f32 v27, v127, s52, v27\n\t"
"v_mfma_f32_16x16x32_bf16 v[164:167], v[96:99], a[172:175], v[164:167]\n\t"
"v_fma_f32 v124, v24, s54, -v124\n\t"
"v_mfma_f32_16x16x32_bf16 v[164:167], v[240:243], a[176:179], v[164:167]\n\t"
"s_waitcnt lgkmcnt(0)\n\t"
"s_barrier\n\t"
"v_mfma_f32_16x16x32_bf16 v[168:171], a[244:247], a[172:175], v[168:171]\n\t"
"ds_read_b128 a[168:171], v244 offset:0\n\t"
"v_mfma_f32_16x16x32_bf16 v[168:171], v[96:99], a[176:179], v[168:171]\n\t"
"ds_read_b128 a[172:175], v244 offset:512\n\t"
"v_fma_f32 v125, v25, s54, -v125\n\t"
"v_mfma_f32_16x16x32_bf16 v[168:171], v[240:243], a[180:183], v[168:171]\n\t"
"v_fma_f32 v126, v26, s54, -v126\n\t"
"v_fma_f32 v127, v27, s54, -v127\n\t"
"v_mfma_f32_16x16x32_bf16 v[172:175], a[244:247], a[176:179], v[172:175]\n\t"
"ds_read_b128 a[176:179], v244 offset:1024\n\t"
"v_fma_f32 v28, v128, s52, v28\n\t"
"v_mfma_f32_16x16x32_bf16 v[172:175], v[96:99], a[180:183], v[172:175]\n\t"
"v_fma_f32 v29, v129, s52, v29\n\t"
"v_fma_f32 v30, v130, s52, v30\n\t"
"v_mfma_f32_16x16x32_bf16 v[172:175], v[240:243], a[184:187], v[172:175]\n\t"
"v_fma_f32 v31, v131, s52, v31\n\t"
"v_fma_f32 v128, v28, s54, -v128\n\t"
"v_mfma_f32_16x16x32_bf16 v[176:179], a[244:247], a[180:183], v[176:179]\n\t"
"ds_read_b128 a[180:183], v244 offset:1536\n\t"
"v_fma_f32 v129, v29, s54, -v129\n\t"
"v_mfma_f32_16x16x32_bf16 v[176:179], v[96:99], a[184:187], v[176:179]\n\t"
"v_fma_f32 v130, v30, s54, -v130\n\t"
"v_fma_f32 v131, v31, s54, -v131\n\t"
"v_mfma_f32_16x16x32_bf16 v[176:179], v[240:243], a[188:191], v[176:179]\n\t"
"v_fma_f32 v32, v132, s52, v32\n\t"
"v_fma_f32 v33, v133, s52, v33\n\t"
"v_mfma_f32_16x16x32_bf16 v[180:183], a[244:247], a[184:187], v[180:183]\n\t"
"ds_read_b128 a[184:187], v244 offset:2048\n\t"
"v_fma_f32 v34, v134, s52, v34\n\t"
"v_mfma_f32_16x16x32_bf16 v[180:183], v[96:99], a[188:191], v[180:183]\n\t"
"v_fma_f32 v35, v135, s52, v35\n\t"
"v_fma_f32 v132, v32, s54, -v132\n\t"
"v_mfma_f32_16x16x32_bf16 v[180:183], v[240:243], a[192:195], v[180:183]\n\t"
"v_fma_f32 v133, v33, s54, -v133\n\t"
"v_fma_f32 v134, v34, s54, -v134\n\t"
"v_mfma_f32_16x16x32_bf16 v[184:187], a[244:247], a[188:191], v[184:187]\n\t"
"ds_read_b128 a[188:191], v244 offset:2560\n\t"
"v_fma_f32 v135, v35, s54, -v135\n\t"
"v_mfma_f32_16x16x32_bf16 v[184:187], v[96:99], a[192:195], v[184:187]\n\t"
"v_fma_f32 v36, v136, s52, v36\n\t"
"v_fma_f32 v37, v137, s52, v37\n\t"
"v_mfma_f32_16x16x32_bf16 v[184:187], v[240:243], a[196:199], v[184:187]\n\t"
"v_fma_f32 v38, v138, s52, v38\n\t"
"v_fma_f32 v39, v139, s52, v39\n\t"
"v_mfma_f32_16x16x32_bf16 v[188:191], a[244:247], a[192:195], v[188:191]\n\t"
"ds_read_b128 a[192:195], v244 offset:3072\n\t"
"v_fma_f32 v136, v36, s54, -v136\n\t"
"v_mfma_f32_16x16x32_bf16 v[188:191], v[96:99], a[196:199], v[188:191]\n\t"
"v_fma_f32 v137, v37, s54, -v137\n\t"
"v_fma_f32 v138, v38, s54, -v138\n\t"
"v_mfma_f32_16x16x32_bf16 v[188:191], v[240:243], a[200:203], v[188:191]\n\t"
"buffer_load_dwordx4 a[208:211], v252, s[8:11], s20 offen offset:2048 sc1\n\t"
"s_mov_b32 s20, 1622016\n\t"
"v_mfma_f32_16x16x32_bf16 v[192:195], a[244:247], a[196:199], v[192:195]\n\t"
"buffer_load_dwordx4 a[228:231], v252, s[8:11], s20 offen offset:0 sc1\n\t"
"v_mfma_f32_16x16x32_bf16 v[192:195], v[96:99], a[200:203], v[192:195]\n\t"
"s_waitcnt vmcnt(12) lgkmcnt(4)\n\t"
"ds_read_b128 a[196:199], v244 offset:3584\n\t"
"v_fma_f32 v139, v39, s54, -v139\n\t"
"v_mfma_f32_16x16x32_bf16 v[192:195], v[240:243], a[204:207], v[192:195]\n\t"
"ds_read_b128 a[200:203], v244 offset:4096\n\t"
"v_fma_f32 v40, v140, s52, v40\n\t"
"Lst13_%=:\n\t"
"v_mfma_f32_16x16x32_bf16 v[196:199], a[248:251], a[168:171], v[196:199]\n\t"
"s_waitcnt lgkmcnt(5)\n\t"
"ds_read_b128 a[204:207], v244 offset:4608\n\t"
"v_fma_f32 v41, v141, s52, v41\n\t"
"v_mfma_f32_16x16x32_bf16 v[196:199], v[236:239], a[172:175], v[196:199]\n\t"
"v_fma_f32 v42, v142, s52, v42\n\t"
"v_fma_f32 v43, v143, s52, v43\n\t"
"v_mfma_f32_16x16x32_bf16 v[196:199], v[248:251], a[176:179], v[196:199]\n\t"
"buffer_load_dwordx4 v[240:243], v252, s[8:11], s20 offen offset:1024 sc1\n\t"
"v_mfma_f32_16x16x32_bf16 v[204:207], a[248:251], a[172:175], v[204:207]\n\t"
"buffer_load_dwordx4 a[232:235], v252, s[8:11], s20 offen offset:2048 sc1\n\t"
"v_mfma_f32_16x16x32_bf16 v[204:207], v[236:239], a[176:179], v[204:207]\n\t"
"s_waitcnt lgkmcnt(5)\n\t"
"s_mov_b32 s20, 1634304\n\t"
"v_fma_f32 v140, v40, s54, -v140\n\t"
"v_mfma_f32_16x16x32_bf16 v[204:207], v[248:251], a[180:183], v[204:207]\n\t"
"v_fma_f32 v141, v41, s54, -v141\n\t"
"v_fma_f32 v142, v42, s54, -v142\n\t"
"v_mfma_f32_16x16x32_bf16 v[212:215], a[248:251], a[176:179], v[212:215]\n\t"
"s_waitcnt lgkmcnt(4)\n\t"
"v_fma_f32 v143, v43, s54, -v143\n\t"
"v_fma_f32 v44, v144, s52, v44\n\t"
"v_mfma_f32_16x16x32_bf16 v[212:215], v[236:239], a[180:183], v[212:215]\n\t"
"v_fma_f32 v45, v145, s52, v45\n\t"
"v_fma_f32 v46, v146, s52, v46\n\t"
"v_mfma_f32_16x16x32_bf16 v[212:215], v[248:251], a[184:187], v[212:215]\n\t"
"v_fma_f32 v47, v147, s52, v47\n\t"
"v_fma_f32 v144, v44, s54, -v144\n\t"
"v_mfma_f32_16x16x32_bf16 a[0:3], a[248:251], a[180:183], a[0:3]\n\t"
"s_waitcnt lgkmcnt(3)\n\t"
"v_fma_f32 v145, v45, s54, -v145\n\t"
"v_fma_f32 v146, v46, s54, -v146\n\t"
"v_mfma_f32_16x16x32_bf16 a[0:3], v[236:239], a[184:187], a[0:3]\n\t"
"v_fma_f32 v147, v47, s54, -v147\n\t"
"v_fma_f32 v48, v148, s52, v48\n\t"
"v_mfma_f32_16x16x32_bf16 a[0:3], v[248:251], a[188:191], a[0:3]\n\t"
"buffer_load_dwordx4 a[236:239], v252, s[8:11], s20 offen offset:0 sc1\n\t"
"v_mfma_f32_16x16x32_bf16 a[8:11], a[248:251], a[184:187], a[8:11]\n\t"
"buffer_load_dwordx4 a[240:243], v252, s[8:11], s20 offen offset:1024 sc1\n\t"
"v_mfma_f32_16x16x32_bf16 a[8:11], v[236:239], a[188:191], a[8:11]\n\t"
"s_waitcnt lgkmcnt(2)\n\t"
"s_add_u32 m0, s24, 72704\n\t"
"s_add_u32 s28, s21, 2162688\n\t"
"v_mfma_f32_16x16x32_bf16 a[8:11], v[248:251], a[192:195], a[8:11]\n\t"
"buffer_load_dwordx4 a[244:247], v252, s[8:11], s20 offen offset:2048 sc1\n\t"
"v_mfma_f32_16x16x32_bf16 a[16:19], a[248:251], a[188:191], a[16:19]\n\t"
"buffer_load_dwordx4 v247, s[12:15], s28 offen offset:0 nt lds\n\t"
"v_mfma_f32_16x16x32_bf16 a[16:19], v[236:239], a[192:195], a[16:19]\n\t"
"s_waitcnt lgkmcnt(1)\n\t"
"s_add_u32 m0, s24, 92672\n\t"
"s_add_u32 s28, s21, 2752512\n\t"
"v_mfma_f32_16x16x32_bf16 a[16:19], v[248:251], a[196:199], a[16:19]\n\t"
"buffer_load_dwordx4 v247, s[12:15], s28 offen offset:0 nt lds\n\t"
"v_mfma_f32_16x16x32_bf16 a[24:27], a[248:251], a[192:195], a[24:27]\n\t"
"s_waitcnt lgkmcnt(0)\n\t"
"s_add_u32 m0, s24, 100352\n\t"
"s_add_u32 s28, s21, 2949120\n\t"
"v_mfma_f32_16x16x32_bf16 a[24:27], v[236:239], a[196:199], a[24:27]\n\t"
"buffer_load_dwordx4 v247, s[12:15], s28 offen offset:0 nt lds\n\t"
"v_mfma_f32_16x16x32_bf16 a[24:27], v[248:251], a[200:203], a[24:27]\n\t"
"s_add_u32 m0, s24, 106496\n\t"
"s_add_u32 s28, s21, 3145728\n\t"
"v_fma_f32 v49, v149, s52, v49\n\t"
"v_mfma_f32_16x16x32_bf16 a[32:35], a[248:251], a[196:199], a[32:35]\n\t"
"s_waitcnt vmcnt(17)\n\t"
"v_fma_f32 v50, v150, s52, v50\n\t"
"v_fma_f32 v51, v151, s52, v51\n\t"
"v_mfma_f32_16x16x32_bf16 a[32:35], v[236:239], a[200:203], a[32:35]\n\t"
"v_fma_f32 v148, v48, s54, -v148\n\t"
"v_fma_f32 v149, v49, s54, -v149\n\t"
"v_mfma_f32_16x16x32_bf16 a[32:35], v[248:251], a[204:207], a[32:35]\n\t"
"buffer_load_dwordx4 v247, s[12:15], s28 offen offset:0 nt lds\n\t"
"s_add_u32 m0, s24, 112640\n\t"
"s_add_u32 s28, s21, 3342336\n\t"
"v_mfma_f32_16x16x32_bf16 a[40:43], a[252:255], a[168:171], a[40:43]\n\t"
"buffer_load_dwordx4 v247, s[12:15], s28 offen offset:0 nt lds\n\t"
"v_mfma_f32_16x16x32_bf16 a[40:43], a[212:215], a[172:175], a[40:43]\n\t"
"v_fma_f32 v150, v50, s54, -v150\n\t"
"v_fma_f32 v151, v51, s54, -v151\n\t"
"v_mfma_f32_16x16x32_bf16 a[40:43], a[216:219], a[176:179], a[40:43]\n\t"
"v_fma_f32 v52, v152, s52, v52\n\t"
"v_fma_f32 v53, v153, s52, v53\n\t"
"v_mfma_f32_16x16x32_bf16 a[48:51], a[252:255], a[172:175], a[48:51]\n\t"
"v_fma_f32 v54, v154, s52, v54\n\t"
"v_fma_f32 v55, v155, s52, v55\n\t"
"v_mfma_f32_16x16x32_bf16 a[48:51], a[212:215], a[176:179], a[48:51]\n\t"
"v_fma_f32 v152, v52, s54, -v152\n\t"
"v_fma_f32 v153, v53, s54, -v153\n\t"
"v_mfma_f32_16x16x32_bf16 a[48:51], a[216:219], a[180:183], a[48:51]\n\t"
"v_fma_f32 v154, v54, s54, -v154\n\t"
"v_fma_f32 v155, v55, s54, -v155\n\t"
"v_mfma_f32_16x16x32_bf16 a[56:59], a[252:255], a[176:179], a[56:59]\n\t"
"v_fma_f32 v56, v156, s52, v56\n\t"
"v_fma_f32 v57, v157, s52, v57\n\t"
"v_mfma_f32_16x16x32_bf16 a[56:59], a[212:215], a[180:183], a[56:59]\n\t"
"v_fma_f32 v58, v158, s52, v58\n\t"
"v_fma_f32 v59, v159, s52, v59\n\t"
"v_mfma_f32_16x16x32_bf16 a[56:59], a[216:219], a[184:187], a[56:59]\n\t"
"v_fma_f32 v156, v56, s54, -v156\n\t"
"v_fma_f32 v157, v57, s54, -v157\n\t"
"v_mfma_f32_16x16x32_bf16 a[64:67], a[252:255], a[180:183], a[64:67]\n\t"
"v_fma_f32 v158, v58, s54, -v158\n\t"
"v_fma_f32 v159, v59, s54, -v159\n\t"
"v_mfma_f32_16x16x32_bf16 a[64:67], a[212:215], a[184:187], a[64:67]\n\t"
"v_fma_f32 v60, v160, s52, v60\n\t"
"v_fma_f32 v61, v161, s52, v61\n\t"
"v_mfma_f32_16x16x32_bf16 a[64:67], a[216:219], a[188:191], a[64:67]\n\t"
"v_fma_f32 v62, v162, s52, v62\n\t"
"v_fma_f32 v63, v163, s52, v63\n\t"
"v_mfma_f32_16x16x32_bf16 a[72:75], a[252:255], a[184:187], a[72:75]\n\t"
"v_fma_f32 v160, v60, s54, -v160\n\t"
"v_fma_f32 v161, v61, s54, -v161\n\t"
"v_mfma_f32_16x16x32_bf16 a[72:75], a[212:215], a[188:191], a[72:75]\n\t"
"v_fma_f32 v162, v62, s54, -v162\n\t"
"v_fma_f32 v163, v63, s54, -v163\n\t"
"v_mfma_f32_16x16x32_bf16 a[72:75], a[216:219], a[192:195], a[72:75]\n\t"
"v_fma_f32 v64, v164, s52, v64\n\t"
"v_fma_f32 v65, v165, s52, v65\n\t"
"v_mfma_f32_16x16x32_bf16 a[80:83], a[252:255], a[188:191], a[80:83]\n\t"
"v_fma_f32 v66, v166, s52, v66\n\t"
"v_fma_f32 v67, v167, s52, v67\n\t"
"v_mfma_f32_16x16x32_bf16 a[80:83], a[212:215], a[192:195], a[80:83]\n\t"
"v_fma_f32 v164, v64, s54, -v164\n\t"
"v_fma_f32 v165, v65, s54, -v165\n\t"
"v_mfma_f32_16x16x32_bf16 a[80:83], a[216:219], a[196:199], a[80:83]\n\t"
"v_fma_f32 v166, v66, s54, -v166\n\t"
"v_fma_f32 v167, v67, s54, -v167\n\t"
"v_mfma_f32_16x16x32_bf16 a[88:91], a[252:255], a[192:195], a[88:91]\n\t"
"v_fma_f32 v68, v168, s52, v68\n\t"
"v_fma_f32 v69, v169, s52, v69\n\t"
"v_mfma_f32_16x16x32_bf16 a[88:91], a[212:215], a[196:199], a[88:91]\n\t"
"v_fma_f32 v70, v170, s52, v70\n\t"
"v_fma_f32 v71, v171, s52, v71\n\t"
"v_mfma_f32_16x16x32_bf16 a[88:91], a[216:219], a[200:203], a[88:91]\n\t"
"v_fma_f32 v168, v68, s54, -v168\n\t"
"v_fma_f32 v169, v69, s54, -v169\n\t"
"v_mfma_f32_16x16x32_bf16 a[96:99], a[252:255], a[196:199], a[96:99]\n\t"
"s_waitcnt vmcnt(11)\n\t"
"v_fma_f32 v170, v70, s54, -v170\n\t"
"v_fma_f32 v171, v71, s54, -v171\n\t"
"v_mfma_f32_16x16x32_bf16 a[96:99], a[212:215], a[200:203], a[96:99]\n\t"
"v_fma_f32 v72, v172, s52, v72\n\t"
"v_fma_f32 v73, v173, s52, v73\n\t"
"v_mfma_f32_16x16x32_bf16 a[96:99], a[216:219], a[204:207], a[96:99]\n\t"
"v_fma_f32 v74, v174, s52, v74\n\t"
"v_fma_f32 v75, v175, s52, v75\n\t"
"v_mfma_f32_16x16x32_bf16 a[104:107], a[220:223], a[168:171], a[104:107]\n\t"
"ds_read_b128 a[168:171], v244 offset:9216\n\t"
"v_fma_f32 v172, v72, s54, -v172\n\t"
"v_mfma_f32_16x16x32_bf16 a[104:107], a[224:227], a[172:175], a[104:107]\n\t"
"v_fma_f32 v173, v73, s54, -v173\n\t"
"v_fma_f32 v174, v74, s54, -v174\n\t"
"v_mfma_f32_16x16x32_bf16 a[104:107], a[208:211], a[176:179], a[104:107]\n\t"
"v_fma_f32 v175, v75, s54, -v175\n\t"
"v_fma_f32 v76, v176, s52, v76\n\t"
"v_mfma_f32_16x16x32_bf16 a[112:115], a[220:223], a[172:175], a[112:115]\n\t"
"ds_read_b128 a[172:175], v244 offset:9728\n\t"
"v_fma_f32 v77, v177, s52, v77\n\t"
"v_mfma_f32_16x16x32_bf16 a[112:115], a[224:227], a[176:179], a[112:115]\n\t"
"v_fma_f32 v78, v178, s52, v78\n\t"
"v_fma_f32 v79, v179, s52, v79\n\t"
"v_mfma_f32_16x16x32_bf16 a[112:115], a[208:211], a[180:183], a[112:115]\n\t"
"v_and_b32 v255, 4096, v244\n\t"
"v_mad_u32_u24 v255, v255, 5, v244\n\t"
"v_mfma_f32_16x16x32_bf16 a[120:123], a[220:223], a[176:179], a[120:123]\n\t"
"ds_read_b128 a[176:179], v244 offset:10240\n\t"
"v_add_u32 v255, 43008, v255\n\t"
"v_mfma_f32_16x16x32_bf16 a[120:123], a[224:227], a[180:183], a[120:123]\n\t"
"v_fma_f32 v176, v76, s54, -v176\n\t"
"v_fma_f32 v177, v77, s54, -v177\n\t"
"v_mfma_f32_16x16x32_bf16 a[120:123], a[208:211], a[184:187], a[120:123]\n\t"
"v_fma_f32 v178, v78, s54, -v178\n\t"
"v_fma_f32 v179, v79, s54, -v179\n\t"
"v_mfma_f32_16x16x32_bf16 a[128:131], a[220:223], a[180:183], a[128:131]\n\t"
"ds_read_b128 a[180:183], v244 offset:10752\n\t"
"v_fma_f32 v80, v180, s52, v80\n\t"
"v_mfma_f32_16x16x32_bf16 a[128:131], a[224:227], a[184:187], a[128:131]\n\t"
"v_fma_f32 v81, v181, s52, v81\n\t"
"v_fma_f32 v82, v182, s52, v82\n\t"
"v_mfma_f32_16x16x32_bf16 a[128:131], a[208:211], a[188:191], a[128:131]\n\t"
"v_fma_f32 v83, v183, s52, v83\n\t"
"v_fma_f32 v180, v80, s54, -v180\n\t"
"v_mfma_f32_16x16x32_bf16 a[136:139], a[220:223], a[184:187], a[136:139]\n\t"
"ds_read_b128 a[184:187], v244 offset:11264\n\t"
"v_fma_f32 v181, v81, s54, -v181\n\t"
"v_mfma_f32_16x16x32_bf16 a[136:139], a[224:227], a[188:191], a[136:139]\n\t"
"v_fma_f32 v182, v82, s54, -v182\n\t"
"v_fma_f32 v183, v83, s54, -v183\n\t"
"v_mfma_f32_16x16x32_bf16 a[136:139], a[208:211], a[192:195], a[136:139]\n\t"
"v_fma_f32 v84, v184, s52, v84\n\t"
"v_fma_f32 v85, v185, s52, v85\n\t"
"v_mfma_f32_16x16x32_bf16 a[144:147], a[220:223], a[188:191], a[144:147]\n\t"
"ds_read_b128 a[188:191], v244 offset:11776\n\t"
"v_fma_f32 v86, v186, s52, v86\n\t"
"v_mfma_f32_16x16x32_bf16 a[144:147], a[224:227], a[192:195], a[144:147]\n\t"
"v_fma_f32 v87, v187, s52, v87\n\t"
"v_fma_f32 v184, v84, s54, -v184\n\t"
"v_mfma_f32_16x16x32_bf16 a[144:147], a[208:211], a[196:199], a[144:147]\n\t"
"v_fma_f32 v185, v85, s54, -v185\n\t"
"v_fma_f32 v186, v86, s54, -v186\n\t"
"v_mfma_f32_16x16x32_bf16 a[152:155], a[220:223], a[192:195], a[152:155]\n\t"
"s_mov_b32 s20, 1646592\n\t"
"ds_read_b128 a[192:195], v244 offset:12288\n\t"
"v_fma_f32 v187, v87, s54, -v187\n\t"
"v_mfma_f32_16x16x32_bf16 a[152:155], a[224:227], a[196:199], a[152:155]\n\t"
"v_fma_f32 v88, v188, s52, v88\n\t"
"v_fma_f32 v89, v189, s52, v89\n\t"
"v_mfma_f32_16x16x32_bf16 a[152:155], a[208:211], a[200:203], a[152:155]\n\t"
"v_fma_f32 v90, v190, s52, v90\n\t"
"v_fma_f32 v91, v191, s52, v91\n\t"
"v_mfma_f32_16x16x32_bf16 a[160:163], a[220:223], a[196:199], a[160:163]\n\t"
"s_waitcnt vmcnt(8) lgkmcnt(4)\n\t"
"ds_read_b128 a[196:199], v244 offset:12800\n\t"
"v_fma_f32 v188, v88, s54, -v188\n\t"
"v_mfma_f32_16x16x32_bf16 a[160:163], a[224:227], a[200:203], a[160:163]\n\t"
"ds_read_b128 a[200:203], v244 offset:13312\n\t"
"v_fma_f32 v189, v89, s54, -v189\n\t"
"v_mfma_f32_16x16x32_bf16 a[160:163], a[208:211], a[204:207], a[160:163]\n\t"
"ds_read_b128 a[204:207], v244 offset:13824\n\t"
"v_fma_f32 v190, v90, s54, -v190\n\t"
"v_fma_f32 v191, v91, s54, -v191\n\t"
"v_mfma_f32_16x16x32_bf16 v[200:203], a[228:231], a[168:171], v[200:203]\n\t"
"s_waitcnt lgkmcnt(6)\n\t"
"v_fma_f32 v92, v192, s52, v92\n\t"
"v_fma_f32 v93, v193, s52, v93\n\t"
"v_mfma_f32_16x16x32_bf16 v[200:203], v[240:243], a[172:175], v[200:203]\n\t"
"v_fma_f32 v94, v194, s52, v94\n\t"
"v_fma_f32 v95, v195, s52, v95\n\t"
"v_mfma_f32_16x16x32_bf16 v[200:203], a[232:235], a[176:179], v[200:203]\n\t"
"v_fma_f32 v192, v92, s54, -v192\n\t"
"v_fma_f32 v193, v93, s54, -v193\n\t"
"v_fma_f32 v194, v94, s54, -v194\n\t"
"v_fma_f32 v195, v95, s54, -v195\n\t"
"v_mfma_f32_16x16x32_bf16 v[208:211], a[228:231], a[172:175], v[208:211]\n\t"
"s_waitcnt lgkmcnt(5)\n\t"
"v_mfma_f32_16x16x32_bf16 v[208:211], v[240:243], a[176:179], v[208:211]\n\t"
"v_mfma_f32_16x16x32_bf16 v[208:211], a[232:235], a[180:183], v[208:211]\n\t"
"s_waitcnt lgkmcnt(0)\n\t"
"s_barrier\n\t"
"v_mfma_f32_16x16x32_bf16 v[216:219], a[228:231], a[176:179], v[216:219]\n\t"
"v_mfma_f32_16x16x32_bf16 v[216:219], v[240:243], a[180:183], v[216:219]\n\t"
"buffer_load_dwordx4 a[208:211], v252, s[8:11], s20 offen offset:0 sc1\n\t"
"v_mfma_f32_16x16x32_bf16 v[216:219], a[232:235], a[184:187], v[216:219]\n\t"
"buffer_load_dwordx4 a[212:215], v252, s[8:11], s20 offen offset:1024 sc1\n\t"
"v_pk_fma_f32 v[196:197], v[200:201], s[56:57], v[196:197]\n\t"
"v_pk_fma_f32 v[200:201], v[196:197], s[58:59], v[200:201] neg_lo:[0,0,1] neg_hi:[0,0,1]\n\t"
"v_pk_fma_f32 v[228:229], v[0:1], s[60:61], v[196:197]\n\t"
"v_pk_fma_f32 v[0:1], v[196:197], s[60:61], v[0:1]\n\t"
"v_pk_fma_f32 v[196:197], v[100:101], s[62:63], v[200:201]\n\t"
"v_pk_fma_f32 v[100:101], v[200:201], s[62:63], v[100:101]\n\t"
"v_pk_fma_f32 v[198:199], v[202:203], s[56:57], v[198:199]\n\t"
"v_pk_fma_f32 v[202:203], v[198:199], s[58:59], v[202:203] neg_lo:[0,0,1] neg_hi:[0,0,1]\n\t"
"v_pk_fma_f32 v[234:235], v[2:3], s[60:61], v[198:199]\n\t"
"v_pk_fma_f32 v[2:3], v[198:199], s[60:61], v[2:3]\n\t"
"v_pk_fma_f32 v[198:199], v[102:103], s[62:63], v[202:203]\n\t"
"v_pk_fma_f32 v[102:103], v[202:203], s[62:63], v[102:103]\n\t"
"v_mfma_f32_16x16x32_bf16 a[4:7], a[228:231], a[180:183], a[4:7]\n\t"
"v_cvt_pk_bf16_f32 v196, v196, v197\n\t"
"v_cvt_pk_bf16_f32 v200, v228, v229\n\t"
"v_mfma_f32_16x16x32_bf16 a[4:7], v[240:243], a[184:187], a[4:7]\n\t"
"v_cvt_pk_bf16_f32 v197, v198, v199\n\t"
"v_cvt_pk_bf16_f32 v201, v234, v235\n\t"
"v_mfma_f32_16x16x32_bf16 a[4:7], a[232:235], a[188:191], a[4:7]\n\t"
"buffer_load_dwordx4 a[216:219], v252, s[8:11], s20 offen offset:2048 sc1\n\t"
"s_mov_b32 s20, 1474560\n\t"
"v_pk_fma_f32 v[204:205], v[208:209], s[56:57], v[204:205]\n\t"
"v_pk_fma_f32 v[208:209], v[204:205], s[58:59], v[208:209] neg_lo:[0,0,1] neg_hi:[0,0,1]\n\t"
"v_pk_fma_f32 v[98:99], v[4:5], s[60:61], v[204:205]\n\t"
"v_pk_fma_f32 v[4:5], v[204:205], s[60:61], v[4:5]\n\t"
"v_pk_fma_f32 v[204:205], v[104:105], s[62:63], v[208:209]\n\t"
"v_pk_fma_f32 v[104:105], v[208:209], s[62:63], v[104:105]\n\t"
"v_mfma_f32_16x16x32_bf16 a[12:15], a[228:231], a[184:187], a[12:15]\n\t"
"v_cvt_pk_bf16_f32 v204, v204, v205\n\t"
"v_cvt_pk_bf16_f32 v208, v98, v99\n\t"
"v_mfma_f32_16x16x32_bf16 a[12:15], v[240:243], a[188:191], a[12:15]\n\t"
"v_mfma_f32_16x16x32_bf16 a[12:15], a[232:235], a[192:195], a[12:15]\n\t"
"buffer_load_dwordx4 a[220:223], v252, s[8:11], s20 offen offset:0 sc1\n\t"
"v_pk_fma_f32 v[206:207], v[210:211], s[56:57], v[206:207]\n\t"
"v_pk_fma_f32 v[210:211], v[206:207], s[58:59], v[210:211] neg_lo:[0,0,1] neg_hi:[0,0,1]\n\t"
"v_pk_fma_f32 v[228:229], v[6:7], s[60:61], v[206:207]\n\t"
"v_pk_fma_f32 v[6:7], v[206:207], s[60:61], v[6:7]\n\t"
"v_pk_fma_f32 v[206:207], v[106:107], s[62:63], v[210:211]\n\t"
"v_pk_fma_f32 v[106:107], v[210:211], s[62:63], v[106:107]\n\t"
"v_mfma_f32_16x16x32_bf16 a[20:23], a[228:231], a[188:191], a[20:23]\n\t"
"v_cvt_pk_bf16_f32 v205, v206, v207\n\t"
"v_cvt_pk_bf16_f32 v209, v228, v229\n\t"
"v_mfma_f32_16x16x32_bf16 a[20:23], v[240:243], a[192:195], a[20:23]\n\t"
"v_accvgpr_read_b32 v224, a0\n\t"
"v_accvgpr_read_b32 v226, a4\n\t"
"v_mfma_f32_16x16x32_bf16 a[20:23], a[232:235], a[196:199], a[20:23]\n\t"
"buffer_load_dwordx4 a[224:227], v252, s[8:11], s20 offen offset:1024 sc1\n\t"
"v_accvgpr_read_b32 v225, a1\n\t"
"v_accvgpr_read_b32 v227, a5\n\t"
"v_pk_fma_f32 v[212:213], v[216:217], s[56:57], v[212:213]\n\t"
"v_pk_fma_f32 v[216:217], v[212:213], s[58:59], v[216:217] neg_lo:[0,0,1] neg_hi:[0,0,1]\n\t"
"v_pk_fma_f32 v[234:235], v[8:9], s[60:61], v[212:213]\n\t"
"v_pk_fma_f32 v[8:9], v[212:213], s[60:61], v[8:9]\n\t"
"v_pk_fma_f32 v[212:213], v[108:109], s[62:63], v[216:217]\n\t"
"v_pk_fma_f32 v[108:109], v[216:217], s[62:63], v[108:109]\n\t"
"v_mfma_f32_16x16x32_bf16 a[28:31], a[228:231], a[192:195], a[28:31]\n\t"
"v_cvt_pk_bf16_f32 v212, v212, v213\n\t"
"v_cvt_pk_bf16_f32 v216, v234, v235\n\t"
"v_mfma_f32_16x16x32_bf16 a[28:31], v[240:243], a[196:199], a[28:31]\n\t"
"v_accvgpr_read_b32 v230, a2\n\t"
"v_accvgpr_read_b32 v232, a6\n\t"
"v_mfma_f32_16x16x32_bf16 a[28:31], a[232:235], a[200:203], a[28:31]\n\t"
"buffer_load_dwordx4 a[248:251], v252, s[8:11], s20 offen offset:2048 sc1\n\t"
"v_accvgpr_read_b32 v231, a3\n\t"
"v_accvgpr_read_b32 v233, a7\n\t"
"v_pk_fma_f32 v[214:215], v[218:219], s[56:57], v[214:215]\n\t"
"v_pk_fma_f32 v[218:219], v[214:215], s[58:59], v[218:219] neg_lo:[0,0,1] neg_hi:[0,0,1]\n\t"
"v_pk_fma_f32 v[98:99], v[10:11], s[60:61], v[214:215]\n\t"
"v_pk_fma_f32 v[10:11], v[214:215], s[60:61], v[10:11]\n\t"
"v_pk_fma_f32 v[214:215], v[110:111], s[62:63], v[218:219]\n\t"
"v_pk_fma_f32 v[110:111], v[218:219], s[62:63], v[110:111]\n\t"
"v_mfma_f32_16x16x32_bf16 a[36:39], a[228:231], a[196:199], a[36:39]\n\t"
"s_waitcnt vmcnt(11)\n\t"
"s_mov_b32 s20, 1486848\n\t"
"v_cvt_pk_bf16_f32 v213, v214, v215\n\t"
"v_mfma_f32_16x16x32_bf16 a[36:39], v[240:243], a[200:203], a[36:39]\n\t"
"v_cvt_pk_bf16_f32 v217, v98, v99\n\t"
"v_accvgpr_read_b32 v236, a8\n\t"
"v_mfma_f32_16x16x32_bf16 a[36:39], a[232:235], a[204:207], a[36:39]\n\t"
"buffer_load_dwordx4 v[240:243], v252, s[8:11], s20 offen offset:0 sc1\n\t"
"v_accvgpr_read_b32 v238, a12\n\t"
"v_accvgpr_read_b32 v237, a9\n\t"
"v_pk_fma_f32 v[224:225], v[226:227], s[56:57], v[224:225]\n\t"
"v_pk_fma_f32 v[226:227], v[224:225], s[58:59], v[226:227] neg_lo:[0,0,1] neg_hi:[0,0,1]\n\t"
"v_pk_fma_f32 v[228:229], v[12:13], s[60:61], v[224:225]\n\t"
"v_pk_fma_f32 v[12:13], v[224:225], s[60:61], v[12:13]\n\t"
"v_pk_fma_f32 v[224:225], v[112:113], s[62:63], v[226:227]\n\t"
"v_pk_fma_f32 v[112:113], v[226:227], s[62:63], v[112:113]\n\t"
"v_mfma_f32_16x16x32_bf16 a[44:47], a[236:239], a[168:171], a[44:47]\n\t"
"v_cvt_pk_bf16_f32 v224, v224, v225\n\t"
"v_cvt_pk_bf16_f32 v228, v228, v229\n\t"
"v_mfma_f32_16x16x32_bf16 a[44:47], a[240:243], a[172:175], a[44:47]\n\t"
"v_accvgpr_write_b32 a0, v224\n\t"
"v_accvgpr_write_b32 a4, v228\n\t"
"v_mfma_f32_16x16x32_bf16 a[44:47], a[244:247], a[176:179], a[44:47]\n\t"
"buffer_load_dwordx4 a[228:231], v252, s[8:11], s20 offen offset:1024 sc1\n\t"
"v_accvgpr_read_b32 v239, a13\n\t"
"v_accvgpr_read_b32 v224, a10\n\t"
"v_pk_fma_f32 v[230:231], v[232:233], s[56:57], v[230:231]\n\t"
"v_pk_fma_f32 v[232:233], v[230:231], s[58:59], v[232:233] neg_lo:[0,0,1] neg_hi:[0,0,1]\n\t"
"v_pk_fma_f32 v[234:235], v[14:15], s[60:61], v[230:231]\n\t"
"v_pk_fma_f32 v[14:15], v[230:231], s[60:61], v[14:15]\n\t"
"v_pk_fma_f32 v[230:231], v[114:115], s[62:63], v[232:233]\n\t"
"v_pk_fma_f32 v[114:115], v[232:233], s[62:63], v[114:115]\n\t"
"v_mfma_f32_16x16x32_bf16 a[52:55], a[236:239], a[172:175], a[52:55]\n\t"
"v_cvt_pk_bf16_f32 v230, v230, v231\n\t"
"v_cvt_pk_bf16_f32 v234, v234, v235\n\t"
"v_mfma_f32_16x16x32_bf16 a[52:55], a[240:243], a[176:179], a[52:55]\n\t"
"v_accvgpr_write_b32 a1, v230\n\t"
"v_accvgpr_write_b32 a5, v234\n\t"
"v_mfma_f32_16x16x32_bf16 a[52:55], a[244:247], a[180:183], a[52:55]\n\t"
"buffer_load_dwordx4 a[232:235], v252, s[8:11], s20 offen offset:2048 sc1\n\t"
"v_accvgpr_read_b32 v226, a14\n\t"
"v_accvgpr_read_b32 v225, a11\n\t"
"v_pk_fma_f32 v[236:237], v[238:239], s[56:57], v[236:237]\n\t"
"v_pk_fma_f32 v[238:239], v[236:237], s[58:59], v[238:239] neg_lo:[0,0,1] neg_hi:[0,0,1]\n\t"
"v_pk_fma_f32 v[98:99], v[16:17], s[60:61], v[236:237]\n\t"
"v_pk_fma_f32 v[16:17], v[236:237], s[60:61], v[16:17]\n\t"
"v_pk_fma_f32 v[236:237], v[116:117], s[62:63], v[238:239]\n\t"
"v_pk_fma_f32 v[116:117], v[238:239], s[62:63], v[116:117]\n\t"
"v_mfma_f32_16x16x32_bf16 a[60:63], a[236:239], a[176:179], a[60:63]\n\t"
"s_mov_b32 s20, 1499136\n\t"
"v_accvgpr_read_b32 v227, a15\n\t"
"v_accvgpr_read_b32 v230, a16\n\t"
"v_mfma_f32_16x16x32_bf16 a[60:63], a[240:243], a[180:183], a[60:63]\n\t"
"v_accvgpr_read_b32 v232, a20\n\t"
"v_accvgpr_read_b32 v231, a17\n\t"
"v_mfma_f32_16x16x32_bf16 a[60:63], a[244:247], a[184:187], a[60:63]\n\t"
"buffer_load_dwordx4 a[252:255], v252, s[8:11], s20 offen offset:0 sc1\n\t"
"v_accvgpr_read_b32 v233, a21\n\t"
"v_cvt_pk_bf16_f32 v236, v236, v237\n\t"
"v_cvt_pk_bf16_f32 v98, v98, v99\n\t"
"v_pk_fma_f32 v[224:225], v[226:227], s[56:57], v[224:225]\n\t"
"v_pk_fma_f32 v[226:227], v[224:225], s[58:59], v[226:227] neg_lo:[0,0,1] neg_hi:[0,0,1]\n\t"
"v_pk_fma_f32 v[228:229], v[18:19], s[60:61], v[224:225]\n\t"
"v_pk_fma_f32 v[18:19], v[224:225], s[60:61], v[18:19]\n\t"
"v_pk_fma_f32 v[224:225], v[118:119], s[62:63], v[226:227]\n\t"
"v_pk_fma_f32 v[118:119], v[226:227], s[62:63], v[118:119]\n\t"
"v_pk_fma_f32 v[230:231], v[232:233], s[56:57], v[230:231]\n\t"
"v_pk_fma_f32 v[232:233], v[230:231], s[58:59], v[232:233] neg_lo:[0,0,1] neg_hi:[0,0,1]\n\t"
"v_pk_fma_f32 v[234:235], v[20:21], s[60:61], v[230:231]\n\t"
"v_pk_fma_f32 v[20:21], v[230:231], s[60:61], v[20:21]\n\t"
"v_pk_fma_f32 v[230:231], v[120:121], s[62:63], v[232:233]\n\t"
"v_pk_fma_f32 v[120:121], v[232:233], s[62:63], v[120:121]\n\t"
"v_mfma_f32_16x16x32_bf16 a[68:71], a[236:239], a[180:183], a[68:71]\n\t"
"v_accvgpr_write_b32 a8, v236\n\t"
"v_accvgpr_write_b32 a12, v98\n\t"
"v_mfma_f32_16x16x32_bf16 a[68:71], a[240:243], a[184:187], a[68:71]\n\t"
"v_accvgpr_read_b32 v236, a18\n\t"
"v_accvgpr_read_b32 v238, a22\n\t"
"v_mfma_f32_16x16x32_bf16 a[68:71], a[244:247], a[188:191], a[68:71]\n\t"
"Lpg10_%=:\n\t"
"s_cbranch_vccnz Lvr0_%=\n\t"
"v_accvgpr_read_b32 v237, a19\n\t"
"v_accvgpr_read_b32 v239, a23\n\t"
"v_cvt_pk_bf16_f32 v224, v224, v225\n\t"
"v_cvt_pk_bf16_f32 v228, v228, v229\n\t"
"v_cvt_pk_bf16_f32 v230, v230, v231\n\t"
"v_cvt_pk_bf16_f32 v234, v234, v235\n\t"
"v_pk_fma_f32 v[236:237], v[238:239], s[56:57], v[236:237]\n\t"
"v_pk_fma_f32 v[238:239], v[236:237], s[58:59], v[238:239] neg_lo:[0,0,1] neg_hi:[0,0,1]\n\t"
"v_pk_fma_f32 v[98:99], v[22:23], s[60:61], v[236:237]\n\t"
"v_pk_fma_f32 v[22:23], v[236:237], s[60:61], v[22:23]\n\t"
"v_pk_fma_f32 v[236:237], v[122:123], s[62:63], v[238:239]\n\t"
"v_pk_fma_f32 v[122:123], v[238:239], s[62:63], v[122:123]\n\t"
"v_mfma_f32_16x16x32_bf16 a[76:79], a[236:239], a[184:187], a[76:79]\n\t"
"v_accvgpr_write_b32 a9, v224\n\t"
"v_accvgpr_write_b32 a13, v228\n\t"
"v_mfma_f32_16x16x32_bf16 a[76:79], a[240:243], a[188:191], a[76:79]\n\t"
"v_accvgpr_write_b32 a16, v230\n\t"
"v_accvgpr_write_b32 a20, v234\n\t"
"v_mfma_f32_16x16x32_bf16 a[76:79], a[244:247], a[192:195], a[76:79]\n\t"
"v_accvgpr_read_b32 v224, a24\n\t"
"v_accvgpr_read_b32 v226, a28\n\t"
"v_accvgpr_read_b32 v225, a25\n\t"
"v_accvgpr_read_b32 v227, a29\n\t"
"v_cvt_pk_bf16_f32 v236, v236, v237\n\t"
"v_cvt_pk_bf16_f32 v98, v98, v99\n\t"
"v_pk_fma_f32 v[224:225], v[226:227], s[56:57], v[224:225]\n\t"
"v_pk_fma_f32 v[226:227], v[224:225], s[58:59], v[226:227] neg_lo:[0,0,1] neg_hi:[0,0,1]\n\t"
"v_pk_fma_f32 v[228:229], v[24:25], s[60:61], v[224:225]\n\t"
"v_pk_fma_f32 v[24:25], v[224:225], s[60:61], v[24:25]\n\t"
"v_pk_fma_f32 v[224:225], v[124:125], s[62:63], v[226:227]\n\t"
"v_pk_fma_f32 v[124:125], v[226:227], s[62:63], v[124:125]\n\t"
"v_mfma_f32_16x16x32_bf16 a[84:87], a[236:239], a[188:191], a[84:87]\n\t"
"v_accvgpr_write_b32 a17, v236\n\t"
"v_accvgpr_write_b32 a21, v98\n\t"
"v_mfma_f32_16x16x32_bf16 a[84:87], a[240:243], a[192:195], a[84:87]\n\t"
"v_accvgpr_read_b32 v230, a26\n\t"
"v_accvgpr_read_b32 v232, a30\n\t"
"v_mfma_f32_16x16x32_bf16 a[84:87], a[244:247], a[196:199], a[84:87]\n\t"
"v_accvgpr_read_b32 v231, a27\n\t"
"v_accvgpr_read_b32 v233, a31\n\t"
"v_cvt_pk_bf16_f32 v224, v224, v225\n\t"
"v_cvt_pk_bf16_f32 v228, v228, v229\n\t"
"v_accvgpr_write_b32 a24, v224\n\t"
"v_pk_fma_f32 v[230:231], v[232:233], s[56:57], v[230:231]\n\t"
"v_pk_fma_f32 v[232:233], v[230:231], s[58:59], v[232:233] neg_lo:[0,0,1] neg_hi:[0,0,1]\n\t"
"v_pk_fma_f32 v[234:235], v[26:27], s[60:61], v[230:231]\n\t"
"v_pk_fma_f32 v[26:27], v[230:231], s[60:61], v[26:27]\n\t"
"v_pk_fma_f32 v[230:231], v[126:127], s[62:63], v[232:233]\n\t"
"v_pk_fma_f32 v[126:127], v[232:233], s[62:63], v[126:127]\n\t"
"v_mfma_f32_16x16x32_bf16 a[92:95], a[236:239], a[192:195], a[92:95]\n\t"
"v_accvgpr_write_b32 a28, v228\n\t"
"v_accvgpr_read_b32 v236, a32\n\t"
"v_mfma_f32_16x16x32_bf16 a[92:95], a[240:243], a[196:199], a[92:95]\n\t"
"v_accvgpr_read_b32 v238, a36\n\t"
"v_accvgpr_read_b32 v237, a33\n\t"
"v_mfma_f32_16x16x32_bf16 a[92:95], a[244:247], a[200:203], a[92:95]\n\t"
"v_accvgpr_read_b32 v239, a37\n\t"
"v_cvt_pk_bf16_f32 v230, v230, v231\n\t"
"v_cvt_pk_bf16_f32 v234, v234, v235\n\t"
"v_accvgpr_write_b32 a25, v230\n\t"
"v_accvgpr_write_b32 a29, v234\n\t"
"v_pk_fma_f32 v[236:237], v[238:239], s[56:57], v[236:237]\n\t"
"v_pk_fma_f32 v[238:239], v[236:237], s[58:59], v[238:239] neg_lo:[0,0,1] neg_hi:[0,0,1]\n\t"
"v_pk_fma_f32 v[98:99], v[28:29], s[60:61], v[236:237]\n\t"
"v_pk_fma_f32 v[28:29], v[236:237], s[60:61], v[28:29]\n\t"
"v_pk_fma_f32 v[236:237], v[128:129], s[62:63], v[238:239]\n\t"
"v_pk_fma_f32 v[128:129], v[238:239], s[62:63], v[128:129]\n\t"
"v_mfma_f32_16x16x32_bf16 a[100:103], a[236:239], a[196:199], a[100:103]\n\t"
"s_waitcnt vmcnt(7)\n\t"
"v_accvgpr_read_b32 v224, a34\n\t"
"v_accvgpr_read_b32 v226, a38\n\t"
"v_mfma_f32_16x16x32_bf16 a[100:103], a[240:243], a[200:203], a[100:103]\n\t"
"v_accvgpr_read_b32 v225, a35\n\t"
"v_accvgpr_read_b32 v227, a39\n\t"
"v_mfma_f32_16x16x32_bf16 a[100:103], a[244:247], a[204:207], a[100:103]\n\t"
"buffer_load_dwordx4 a[236:239], v252, s[8:11], s20 offen offset:1024 sc1\n\t"
"v_cvt_pk_bf16_f32 v236, v236, v237\n\t"
"v_cvt_pk_bf16_f32 v98, v98, v99\n\t"
"v_pk_fma_f32 v[224:225], v[226:227], s[56:57], v[224:225]\n\t"
"v_pk_fma_f32 v[226:227], v[224:225], s[58:59], v[226:227] neg_lo:[0,0,1] neg_hi:[0,0,1]\n\t"
"v_pk_fma_f32 v[228:229], v[30:31], s[60:61], v[224:225]\n\t"
"v_pk_fma_f32 v[30:31], v[224:225], s[60:61], v[30:31]\n\t"
"v_pk_fma_f32 v[224:225], v[130:131], s[62:63], v[226:227]\n\t"
"v_pk_fma_f32 v[130:131], v[226:227], s[62:63], v[130:131]\n\t"
"v_mfma_f32_16x16x32_bf16 a[108:111], a[208:211], a[168:171], a[108:111]\n\t"
"ds_read_b128 a[168:171], v255 offset:512\n\t"
"v_accvgpr_write_b32 a32, v236\n\t"
"v_mfma_f32_16x16x32_bf16 a[108:111], a[212:215], a[172:175], a[108:111]\n\t"
"v_accvgpr_write_b32 a36, v98\n\t"
"v_accvgpr_read_b32 v230, a40\n\t"
"v_mfma_f32_16x16x32_bf16 a[108:111], a[216:219], a[176:179], a[108:111]\n\t"
"buffer_load_dwordx4 a[240:243], v252, s[8:11], s20 offen offset:2048 sc1\n\t"
"v_accvgpr_read_b32 v232, a44\n\t"
"v_accvgpr_read_b32 v231, a41\n\t"
"v_accvgpr_read_b32 v233, a45\n\t"
"v_cvt_pk_bf16_f32 v224, v224, v225\n\t"
"v_cvt_pk_bf16_f32 v228, v228, v229\n\t"
"s_mov_b32 s20, 1658880\n\t"
"v_pk_fma_f32 v[230:231], v[232:233], s[56:57], v[230:231]\n\t"
"v_pk_fma_f32 v[232:233], v[230:231], s[58:59], v[232:233] neg_lo:[0,0,1] neg_hi:[0,0,1]\n\t"
"v_pk_fma_f32 v[234:235], v[32:33], s[60:61], v[230:231]\n\t"
"v_pk_fma_f32 v[32:33], v[230:231], s[60:61], v[32:33]\n\t"
"v_pk_fma_f32 v[230:231], v[132:133], s[62:63], v[232:233]\n\t"
"v_pk_fma_f32 v[132:133], v[232:233], s[62:63], v[132:133]\n\t"
"v_mfma_f32_16x16x32_bf16 a[116:119], a[208:211], a[172:175], a[116:119]\n\t"
"ds_read_b128 a[172:175], v255 offset:3584\n\t"
"v_accvgpr_write_b32 a33, v224\n\t"
"v_mfma_f32_16x16x32_bf16 a[116:119], a[212:215], a[176:179], a[116:119]\n\t"
"v_accvgpr_write_b32 a37, v228\n\t"
"v_accvgpr_read_b32 v236, a42\n\t"
"v_mfma_f32_16x16x32_bf16 a[116:119], a[216:219], a[180:183], a[116:119]\n\t"
"buffer_load_dwordx4 a[244:247], v252, s[8:11], s20 offen offset:0 sc1\n\t"
"v_accvgpr_read_b32 v238, a46\n\t"
"v_accvgpr_read_b32 v237, a43\n\t"
"v_accvgpr_read_b32 v239, a47\n\t"
"v_pk_fma_f32 v[236:237], v[238:239], s[56:57], v[236:237]\n\t"
"v_pk_fma_f32 v[238:239], v[236:237], s[58:59], v[238:239] neg_lo:[0,0,1] neg_hi:[0,0,1]\n\t"
"v_pk_fma_f32 v[98:99], v[34:35], s[60:61], v[236:237]\n\t"
"v_pk_fma_f32 v[34:35], v[236:237], s[60:61], v[34:35]\n\t"
"v_pk_fma_f32 v[236:237], v[134:135], s[62:63], v[238:239]\n\t"
"v_pk_fma_f32 v[134:135], v[238:239], s[62:63], v[134:135]\n\t"
"v_mfma_f32_16x16x32_bf16 a[124:127], a[208:211], a[176:179], a[124:127]\n\t"
"ds_read_b128 a[176:179], v255 offset:6656\n\t"
"v_cvt_pk_bf16_f32 v198, v230, v231\n\t"
"v_mfma_f32_16x16x32_bf16 a[124:127], a[212:215], a[180:183], a[124:127]\n\t"
"v_cvt_pk_bf16_f32 v202, v234, v235\n\t"
"v_accvgpr_read_b32 v224, a48\n\t"
"v_mfma_f32_16x16x32_bf16 a[124:127], a[216:219], a[184:187], a[124:127]\n\t"
"global_load_dwordx4 v[248:251], v252, s[48:49] offset:0\n\t"
"v_accvgpr_read_b32 v226, a52\n\t"
"v_accvgpr_read_b32 v225, a49\n\t"
"v_accvgpr_read_b32 v227, a53\n\t"
"v_accvgpr_read_b32 v230, a50\n\t"
"v_accvgpr_read_b32 v232, a54\n\t"
"v_accvgpr_read_b32 v231, a51\n\t"
"v_accvgpr_read_b32 v233, a55\n\t"
"v_pk_fma_f32 v[224:225], v[226:227], s[56:57], v[224:225]\n\t"
"v_pk_fma_f32 v[226:227], v[224:225], s[58:59], v[226:227] neg_lo:[0,0,1] neg_hi:[0,0,1]\n\t"
"v_pk_fma_f32 v[228:229], v[36:37], s[60:61], v[224:225]\n\t"
"v_pk_fma_f32 v[36:37], v[224:225], s[60:61], v[36:37]\n\t"
"v_pk_fma_f32 v[224:225], v[136:137], s[62:63], v[226:227]\n\t"
"v_pk_fma_f32 v[136:137], v[226:227], s[62:63], v[136:137]\n\t"
"v_pk_fma_f32 v[230:231], v[232:233], s[56:57], v[230:231]\n\t"
"v_pk_fma_f32 v[232:233], v[230:231], s[58:59], v[232:233] neg_lo:[0,0,1] neg_hi:[0,0,1]\n\t"
"v_pk_fma_f32 v[234:235], v[38:39], s[60:61], v[230:231]\n\t"
"v_pk_fma_f32 v[38:39], v[230:231], s[60:61], v[38:39]\n\t"
"v_pk_fma_f32 v[230:231], v[138:139], s[62:63], v[232:233]\n\t"
"v_pk_fma_f32 v[138:139], v[232:233], s[62:63], v[138:139]\n\t"
"v_mfma_f32_16x16x32_bf16 a[132:135], a[208:211], a[180:183], a[132:135]\n\t"
"ds_read_b128 a[180:183], v255 offset:9728\n\t"
"v_cvt_pk_bf16_f32 v199, v236, v237\n\t"
"v_mfma_f32_16x16x32_bf16 a[132:135], a[212:215], a[184:187], a[132:135]\n\t"
"v_cvt_pk_bf16_f32 v203, v98, v99\n\t"
"v_accvgpr_read_b32 v236, a56\n\t"
"v_mfma_f32_16x16x32_bf16 a[132:135], a[216:219], a[188:191], a[132:135]\n\t"
"buffer_load_dwordx4 a[40:43], v252, s[8:11], s20 offen offset:1024 sc1\n\t"
"v_accvgpr_read_b32 v238, a60\n\t"
"v_accvgpr_read_b32 v237, a57\n\t"
"v_accvgpr_read_b32 v239, a61\n\t"
"v_pk_fma_f32 v[236:237], v[238:239], s[56:57], v[236:237]\n\t"
"v_pk_fma_f32 v[238:239], v[236:237], s[58:59], v[238:239] neg_lo:[0,0,1] neg_hi:[0,0,1]\n\t"
"v_pk_fma_f32 v[98:99], v[40:41], s[60:61], v[236:237]\n\t"
"v_pk_fma_f32 v[40:41], v[236:237], s[60:61], v[40:41]\n\t"
"v_pk_fma_f32 v[236:237], v[140:141], s[62:63], v[238:239]\n\t"
"v_pk_fma_f32 v[140:141], v[238:239], s[62:63], v[140:141]\n\t"
"v_mfma_f32_16x16x32_bf16 a[140:143], a[208:211], a[184:187], a[140:143]\n\t"
"ds_read_b128 a[184:187], v255 offset:12800\n\t"
"v_cvt_pk_bf16_f32 v206, v224, v225\n\t"
"v_mfma_f32_16x16x32_bf16 a[140:143], a[212:215], a[188:191], a[140:143]\n\t"
"v_cvt_pk_bf16_f32 v210, v228, v229\n\t"
"v_cvt_pk_bf16_f32 v207, v230, v231\n\t"
"v_mfma_f32_16x16x32_bf16 a[140:143], a[216:219], a[192:195], a[140:143]\n\t"
"buffer_load_dwordx4 a[48:51], v252, s[8:11], s20 offen offset:2048 sc1\n\t"
"v_accvgpr_read_b32 v224, a58\n\t"
"v_accvgpr_read_b32 v226, a62\n\t"
"v_accvgpr_read_b32 v225, a59\n\t"
"v_accvgpr_read_b32 v227, a63\n\t"
"v_cvt_pk_bf16_f32 v211, v234, v235\n\t"
"s_mov_b32 s20, 1671168\n\t"
"v_pk_fma_f32 v[224:225], v[226:227], s[56:57], v[224:225]\n\t"
"v_pk_fma_f32 v[226:227], v[224:225], s[58:59], v[226:227] neg_lo:[0,0,1] neg_hi:[0,0,1]\n\t"
"v_pk_fma_f32 v[228:229], v[42:43], s[60:61], v[224:225]\n\t"
"v_pk_fma_f32 v[42:43], v[224:225], s[60:61], v[42:43]\n\t"
"v_pk_fma_f32 v[224:225], v[142:143], s[62:63], v[226:227]\n\t"
"v_pk_fma_f32 v[142:143], v[226:227], s[62:63], v[142:143]\n\t"
"v_mfma_f32_16x16x32_bf16 a[148:151], a[208:211], a[188:191], a[148:151]\n\t"
"ds_read_b128 a[188:191], v255 offset:15872\n\t"
"v_cvt_pk_bf16_f32 v214, v236, v237\n\t"
"v_mfma_f32_16x16x32_bf16 a[148:151], a[212:215], a[192:195], a[148:151]\n\t"
"v_cvt_pk_bf16_f32 v218, v98, v99\n\t"
"v_accvgpr_read_b32 v230, a64\n\t"
"v_mfma_f32_16x16x32_bf16 a[148:151], a[216:219], a[196:199], a[148:151]\n\t"
"buffer_load_dwordx4 a[52:55], v252, s[8:11], s20 offen offset:0 sc1\n\t"
"v_accvgpr_read_b32 v232, a68\n\t"
"v_accvgpr_read_b32 v231, a65\n\t"
"v_accvgpr_read_b32 v233, a69\n\t"
"v_pk_fma_f32 v[230:231], v[232:233], s[56:57], v[230:231]\n\t"
"v_pk_fma_f32 v[232:233], v[230:231], s[58:59], v[232:233] neg_lo:[0,0,1] neg_hi:[0,0,1]\n\t"
"v_pk_fma_f32 v[234:235], v[44:45], s[60:61], v[230:231]\n\t"
"v_pk_fma_f32 v[44:45], v[230:231], s[60:61], v[44:45]\n\t"
"v_pk_fma_f32 v[230:231], v[144:145], s[62:63], v[232:233]\n\t"
"v_pk_fma_f32 v[144:145], v[232:233], s[62:63], v[144:145]\n\t"
"v_mfma_f32_16x16x32_bf16 a[156:159], a[208:211], a[192:195], a[156:159]\n\t"
"ds_read_b128 a[192:195], v255 offset:18944\n\t"
"v_cvt_pk_bf16_f32 v215, v224, v225\n\t"
"v_mfma_f32_16x16x32_bf16 a[156:159], a[212:215], a[196:199], a[156:159]\n\t"
"v_cvt_pk_bf16_f32 v219, v228, v229\n\t"
"v_accvgpr_read_b32 v236, a66\n\t"
"v_mfma_f32_16x16x32_bf16 a[156:159], a[216:219], a[200:203], a[156:159]\n\t"
"buffer_load_dwordx4 a[44:47], v252, s[8:11], s20 offen offset:1024 sc1\n\t"
"v_accvgpr_read_b32 v238, a70\n\t"
"v_accvgpr_read_b32 v237, a67\n\t"
"v_accvgpr_read_b32 v239, a71\n\t"
"v_cvt_pk_bf16_f32 v230, v230, v231\n\t"
"v_cvt_pk_bf16_f32 v234, v234, v235\n\t"
"v_pk_fma_f32 v[236:237], v[238:239], s[56:57], v[236:237]\n\t"
"v_pk_fma_f32 v[238:239], v[236:237], s[58:59], v[238:239] neg_lo:[0,0,1] neg_hi:[0,0,1]\n\t"
"v_pk_fma_f32 v[98:99], v[46:47], s[60:61], v[236:237]\n\t"
"v_pk_fma_f32 v[46:47], v[236:237], s[60:61], v[46:47]\n\t"
"v_pk_fma_f32 v[236:237], v[146:147], s[62:63], v[238:239]\n\t"
"v_pk_fma_f32 v[146:147], v[238:239], s[62:63], v[146:147]\n\t"
"v_mfma_f32_16x16x32_bf16 a[164:167], a[208:211], a[196:199], a[164:167]\n\t"
"s_waitcnt vmcnt(12) lgkmcnt(4)\n\t"
"ds_read_b128 a[196:199], v255 offset:22016\n\t"
"v_accvgpr_write_b32 a2, v230\n\t"
"v_mfma_f32_16x16x32_bf16 a[164:167], a[212:215], a[200:203], a[164:167]\n\t"
"ds_read_b128 a[200:203], v255 offset:25088\n\t"
"v_accvgpr_write_b32 a6, v234\n\t"
"v_mfma_f32_16x16x32_bf16 a[164:167], a[216:219], a[204:207], a[164:167]\n\t"
"buffer_load_dwordx4 a[60:63], v252, s[8:11], s20 offen offset:2048 sc1\n\t"
"ds_read_b128 a[204:207], v255 offset:28160\n\t"
"v_accvgpr_read_b32 v224, a72\n\t"
"v_accvgpr_read_b32 v226, a76\n\t"
"v_accvgpr_read_b32 v225, a73\n\t"
"v_accvgpr_read_b32 v227, a77\n\t"
"v_cvt_pk_bf16_f32 v236, v236, v237\n\t"
"v_cvt_pk_bf16_f32 v98, v98, v99\n\t"
"s_mov_b32 s20, 1683456\n\t"
"v_pk_fma_f32 v[224:225], v[226:227], s[56:57], v[224:225]\n\t"
"v_pk_fma_f32 v[226:227], v[224:225], s[58:59], v[226:227] neg_lo:[0,0,1] neg_hi:[0,0,1]\n\t"
"v_pk_fma_f32 v[228:229], v[48:49], s[60:61], v[224:225]\n\t"
"v_pk_fma_f32 v[48:49], v[224:225], s[60:61], v[48:49]\n\t"
"v_pk_fma_f32 v[224:225], v[148:149], s[62:63], v[226:227]\n\t"
"v_pk_fma_f32 v[148:149], v[226:227], s[62:63], v[148:149]\n\t"
"Lst14_%=:\n\t"
"v_mfma_f32_16x16x32_bf16 v[0:3], a[220:223], a[168:171], v[0:3]\n\t"
"s_waitcnt lgkmcnt(6)\n\t"
"v_accvgpr_write_b32 a3, v236\n\t"
"v_accvgpr_write_b32 a7, v98\n\t"
"v_mfma_f32_16x16x32_bf16 v[0:3], a[224:227], a[172:175], v[0:3]\n\t"
"v_accvgpr_read_b32 v230, a74\n\t"
"v_accvgpr_read_b32 v232, a78\n\t"
"v_mfma_f32_16x16x32_bf16 v[0:3], a[248:251], a[176:179], v[0:3]\n\t"
"buffer_load_dwordx4 a[64:67], v252, s[8:11], s20 offen offset:0 sc1\n\t"
"v_accvgpr_read_b32 v231, a75\n\t"
"v_accvgpr_read_b32 v233, a79\n\t"
"v_cvt_pk_bf16_f32 v224, v224, v225\n\t"
"v_cvt_pk_bf16_f32 v228, v228, v229\n\t"
"v_pk_fma_f32 v[230:231], v[232:233], s[56:57], v[230:231]\n\t"
"v_pk_fma_f32 v[232:233], v[230:231], s[58:59], v[232:233] neg_lo:[0,0,1] neg_hi:[0,0,1]\n\t"
"v_pk_fma_f32 v[234:235], v[50:51], s[60:61], v[230:231]\n\t"
"v_pk_fma_f32 v[50:51], v[230:231], s[60:61], v[50:51]\n\t"
"v_pk_fma_f32 v[230:231], v[150:151], s[62:63], v[232:233]\n\t"
"v_pk_fma_f32 v[150:151], v[232:233], s[62:63], v[150:151]\n\t"
"v_mfma_f32_16x16x32_bf16 v[4:7], a[220:223], a[172:175], v[4:7]\n\t"
"s_waitcnt lgkmcnt(5)\n\t"
"v_accvgpr_write_b32 a10, v224\n\t"
"v_accvgpr_write_b32 a14, v228\n\t"
"v_mfma_f32_16x16x32_bf16 v[4:7], a[224:227], a[176:179], v[4:7]\n\t"
"v_accvgpr_read_b32 v236, a80\n\t"
"v_accvgpr_read_b32 v238, a84\n\t"
"v_mfma_f32_16x16x32_bf16 v[4:7], a[248:251], a[180:183], v[4:7]\n\t"
"buffer_load_dwordx4 a[68:71], v252, s[8:11], s20 offen offset:1024 sc1\n\t"
"v_accvgpr_read_b32 v237, a81\n\t"
"v_accvgpr_read_b32 v239, a85\n\t"
"v_cvt_pk_bf16_f32 v230, v230, v231\n\t"
"v_cvt_pk_bf16_f32 v234, v234, v235\n\t"
"v_pk_fma_f32 v[236:237], v[238:239], s[56:57], v[236:237]\n\t"
"v_pk_fma_f32 v[238:239], v[236:237], s[58:59], v[238:239] neg_lo:[0,0,1] neg_hi:[0,0,1]\n\t"
"v_pk_fma_f32 v[98:99], v[52:53], s[60:61], v[236:237]\n\t"
"v_pk_fma_f32 v[52:53], v[236:237], s[60:61], v[52:53]\n\t"
"v_pk_fma_f32 v[236:237], v[152:153], s[62:63], v[238:239]\n\t"
"v_pk_fma_f32 v[152:153], v[238:239], s[62:63], v[152:153]\n\t"
"v_mfma_f32_16x16x32_bf16 v[8:11], a[220:223], a[176:179], v[8:11]\n\t"
"s_waitcnt lgkmcnt(4)\n\t"
"v_accvgpr_write_b32 a11, v230\n\t"
"v_accvgpr_write_b32 a15, v234\n\t"
"v_mfma_f32_16x16x32_bf16 v[8:11], a[224:227], a[180:183], v[8:11]\n\t"
"v_accvgpr_read_b32 v224, a82\n\t"
"v_accvgpr_read_b32 v226, a86\n\t"
"v_mfma_f32_16x16x32_bf16 v[8:11], a[248:251], a[184:187], v[8:11]\n\t"
"buffer_load_dwordx4 a[72:75], v252, s[8:11], s20 offen offset:2048 sc1\n\t"
"v_accvgpr_read_b32 v225, a83\n\t"
"v_accvgpr_read_b32 v227, a87\n\t"
"v_cvt_pk_bf16_f32 v236, v236, v237\n\t"
"v_cvt_pk_bf16_f32 v98, v98, v99\n\t"
"s_mov_b32 s20, 589824\n\t"
"v_pk_fma_f32 v[224:225], v[226:227], s[56:57], v[224:225]\n\t"
"v_pk_fma_f32 v[226:227], v[224:225], s[58:59], v[226:227] neg_lo:[0,0,1] neg_hi:[0,0,1]\n\t"
"v_pk_fma_f32 v[228:229], v[54:55], s[60:61], v[224:225]\n\t"
"v_pk_fma_f32 v[54:55], v[224:225], s[60:61], v[54:55]\n\t"
"v_pk_fma_f32 v[224:225], v[154:155], s[62:63], v[226:227]\n\t"
"v_pk_fma_f32 v[154:155], v[226:227], s[62:63], v[154:155]\n\t"
"v_mfma_f32_16x16x32_bf16 v[12:15], a[220:223], a[180:183], v[12:15]\n\t"
"s_waitcnt lgkmcnt(3)\n\t"
"v_accvgpr_write_b32 a18, v236\n\t"
"v_accvgpr_write_b32 a22, v98\n\t"
"v_mfma_f32_16x16x32_bf16 v[12:15], a[224:227], a[184:187], v[12:15]\n\t"
"v_accvgpr_read_b32 v230, a88\n\t"
"v_accvgpr_read_b32 v232, a92\n\t"
"v_mfma_f32_16x16x32_bf16 v[12:15], a[248:251], a[188:191], v[12:15]\n\t"
"buffer_load_dwordx4 a[76:79], v252, s[8:11], s20 offen offset:0 sc1\n\t"
"v_accvgpr_read_b32 v231, a89\n\t"
"v_accvgpr_read_b32 v233, a93\n\t"
"v_accvgpr_read_b32 v236, a90\n\t"
"v_accvgpr_read_b32 v238, a94\n\t"
"v_accvgpr_read_b32 v237, a91\n\t"
"v_accvgpr_read_b32 v239, a95\n\t"
"v_cvt_pk_bf16_f32 v224, v224, v225\n\t"
"v_cvt_pk_bf16_f32 v228, v228, v229\n\t"
"v_pk_fma_f32 v[230:231], v[232:233], s[56:57], v[230:231]\n\t"
"v_pk_fma_f32 v[232:233], v[230:231], s[58:59], v[232:233] neg_lo:[0,0,1] neg_hi:[0,0,1]\n\t"
"v_pk_fma_f32 v[234:235], v[56:57], s[60:61], v[230:231]\n\t"
"v_pk_fma_f32 v[56:57], v[230:231], s[60:61], v[56:57]\n\t"
"v_pk_fma_f32 v[230:231], v[156:157], s[62:63], v[232:233]\n\t"
"v_pk_fma_f32 v[156:157], v[232:233], s[62:63], v[156:157]\n\t"
"v_pk_fma_f32 v[236:237], v[238:239], s[56:57], v[236:237]\n\t"
"v_pk_fma_f32 v[238:239], v[236:237], s[58:59], v[238:239] neg_lo:[0,0,1] neg_hi:[0,0,1]\n\t"
"v_pk_fma_f32 v[98:99], v[58:59], s[60:61], v[236:237]\n\t"
"v_pk_fma_f32 v[58:59], v[236:237], s[60:61], v[58:59]\n\t"
"v_pk_fma_f32 v[236:237], v[158:159], s[62:63], v[238:239]\n\t"
"v_pk_fma_f32 v[158:159], v[238:239], s[62:63], v[158:159]\n\t"
"v_mfma_f32_16x16x32_bf16 v[16:19], a[220:223], a[184:187], v[16:19]\n\t"
"s_waitcnt lgkmcnt(2)\n\t"
"v_accvgpr_write_b32 a19, v224\n\t"
"v_accvgpr_write_b32 a23, v228\n\t"
"v_mfma_f32_16x16x32_bf16 v[16:19], a[224:227], a[188:191], v[16:19]\n\t"
"v_accvgpr_read_b32 v224, a96\n\t"
"v_accvgpr_read_b32 v226, a100\n\t"
"v_mfma_f32_16x16x32_bf16 v[16:19], a[248:251], a[192:195], v[16:19]\n\t"
"buffer_load_dwordx4 a[84:87], v252, s[8:11], s20 offen offset:1024 sc1\n\t"
"v_accvgpr_read_b32 v225, a97\n\t"
"v_accvgpr_read_b32 v227, a101\n\t"
"v_cvt_pk_bf16_f32 v230, v230, v231\n\t"
"v_cvt_pk_bf16_f32 v234, v234, v235\n\t"
"v_pk_fma_f32 v[224:225], v[226:227], s[56:57], v[224:225]\n\t"
"v_pk_fma_f32 v[226:227], v[224:225], s[58:59], v[226:227] neg_lo:[0,0,1] neg_hi:[0,0,1]\n\t"
"v_pk_fma_f32 v[228:229], v[60:61], s[60:61], v[224:225]\n\t"
"v_pk_fma_f32 v[60:61], v[224:225], s[60:61], v[60:61]\n\t"
"v_pk_fma_f32 v[224:225], v[160:161], s[62:63], v[226:227]\n\t"
"v_pk_fma_f32 v[160:161], v[226:227], s[62:63], v[160:161]\n\t"
"v_mfma_f32_16x16x32_bf16 v[20:23], a[220:223], a[188:191], v[20:23]\n\t"
"s_waitcnt lgkmcnt(1)\n\t"
"v_accvgpr_write_b32 a26, v230\n\t"
"v_cvt_pk_bf16_f32 v236, v236, v237\n\t"
"v_mfma_f32_16x16x32_bf16 v[20:23], a[224:227], a[192:195], v[20:23]\n\t"
"v_accvgpr_write_b32 a30, v234\n\t"
"v_cvt_pk_bf16_f32 v98, v98, v99\n\t"
"v_mfma_f32_16x16x32_bf16 v[20:23], a[248:251], a[196:199], v[20:23]\n\t"
"buffer_load_dwordx4 a[88:91], v252, s[8:11], s20 offen offset:2048 sc1\n\t"
"v_accvgpr_read_b32 v230, a98\n\t"
"v_accvgpr_read_b32 v232, a102\n\t"
"v_accvgpr_read_b32 v231, a99\n\t"
"v_accvgpr_read_b32 v233, a103\n\t"
"v_accvgpr_write_b32 a27, v236\n\t"
"v_accvgpr_write_b32 a31, v98\n\t"
"v_cvt_pk_bf16_f32 v224, v224, v225\n\t"
"v_cvt_pk_bf16_f32 v228, v228, v229\n\t"
"s_mov_b32 s20, 602112\n\t"
"v_pk_fma_f32 v[230:231], v[232:233], s[56:57], v[230:231]\n\t"
"v_pk_fma_f32 v[232:233], v[230:231], s[58:59], v[232:233] neg_lo:[0,0,1] neg_hi:[0,0,1]\n\t"
"v_pk_fma_f32 v[234:235], v[62:63], s[60:61], v[230:231]\n\t"
"v_pk_fma_f32 v[62:63], v[230:231], s[60:61], v[62:63]\n\t"
"v_pk_fma_f32 v[230:231], v[162:163], s[62:63], v[232:233]\n\t"
"v_pk_fma_f32 v[162:163], v[232:233], s[62:63], v[162:163]\n\t"
"v_mfma_f32_16x16x32_bf16 v[24:27], a[220:223], a[192:195], v[24:27]\n\t"
"s_waitcnt lgkmcnt(0)\n\t"
"v_accvgpr_write_b32 a34, v224\n\t"
"v_accvgpr_write_b32 a38, v228\n\t"
"v_mfma_f32_16x16x32_bf16 v[24:27], a[224:227], a[196:199], v[24:27]\n\t"
"v_accvgpr_read_b32 v236, a104\n\t"
"v_accvgpr_read_b32 v238, a108\n\t"
"v_mfma_f32_16x16x32_bf16 v[24:27], a[248:251], a[200:203], v[24:27]\n\t"
"buffer_load_dwordx4 a[92:95], v252, s[8:11], s20 offen offset:0 sc1\n\t"
"v_accvgpr_read_b32 v237, a105\n\t"
"v_accvgpr_read_b32 v239, a109\n\t"
"v_cvt_pk_bf16_f32 v230, v230, v231\n\t"
"v_cvt_pk_bf16_f32 v234, v234, v235\n\t"
"v_pk_fma_f32 v[236:237], v[238:239], s[56:57], v[236:237]\n\t"
"v_pk_fma_f32 v[238:239], v[236:237], s[58:59], v[238:239] neg_lo:[0,0,1] neg_hi:[0,0,1]\n\t"
"v_pk_fma_f32 v[98:99], v[64:65], s[60:61], v[236:237]\n\t"
"v_pk_fma_f32 v[64:65], v[236:237], s[60:61], v[64:65]\n\t"
"v_pk_fma_f32 v[236:237], v[164:165], s[62:63], v[238:239]\n\t"
"v_pk_fma_f32 v[164:165], v[238:239], s[62:63], v[164:165]\n\t"
"v_mfma_f32_16x16x32_bf16 v[28:31], a[220:223], a[196:199], v[28:31]\n\t"
"s_waitcnt vmcnt(17)\n\t"
"v_accvgpr_write_b32 a35, v230\n\t"
"v_accvgpr_write_b32 a39, v234\n\t"
"v_mfma_f32_16x16x32_bf16 v[28:31], a[224:227], a[200:203], v[28:31]\n\t"
"v_accvgpr_read_b32 v224, a106\n\t"
"v_accvgpr_read_b32 v226, a110\n\t"
"v_mfma_f32_16x16x32_bf16 v[28:31], a[248:251], a[204:207], v[28:31]\n\t"
"buffer_load_dwordx4 a[96:99], v252, s[8:11], s20 offen offset:1024 sc1\n\t"
"v_accvgpr_read_b32 v225, a107\n\t"
"v_accvgpr_read_b32 v227, a111\n\t"
"v_cvt_pk_bf16_f32 v236, v236, v237\n\t"
"v_cvt_pk_bf16_f32 v98, v98, v99\n\t"
"v_pk_fma_f32 v[224:225], v[226:227], s[56:57], v[224:225]\n\t"
"v_pk_fma_f32 v[226:227], v[224:225], s[58:59], v[226:227] neg_lo:[0,0,1] neg_hi:[0,0,1]\n\t"
"v_pk_fma_f32 v[228:229], v[66:67], s[60:61], v[224:225]\n\t"
"v_pk_fma_f32 v[66:67], v[224:225], s[60:61], v[66:67]\n\t"
"v_pk_fma_f32 v[224:225], v[166:167], s[62:63], v[226:227]\n\t"
"v_pk_fma_f32 v[166:167], v[226:227], s[62:63], v[166:167]\n\t"
"v_mfma_f32_16x16x32_bf16 v[32:35], v[240:243], a[168:171], v[32:35]\n\t"
"v_accvgpr_write_b32 a104, v236\n\t"
"v_accvgpr_write_b32 a106, v98\n\t"
"v_mfma_f32_16x16x32_bf16 v[32:35], a[228:231], a[172:175], v[32:35]\n\t"
"v_accvgpr_read_b32 v230, a112\n\t"
"v_accvgpr_read_b32 v232, a116\n\t"
"v_mfma_f32_16x16x32_bf16 v[32:35], a[232:235], a[176:179], v[32:35]\n\t"
"buffer_load_dwordx4 a[100:103], v252, s[8:11], s20 offen offset:2048 sc1\n\t"
"v_accvgpr_read_b32 v231, a113\n\t"
"v_accvgpr_read_b32 v233, a117\n\t"
"v_cvt_pk_bf16_f32 v224, v224, v225\n\t"
"v_cvt_pk_bf16_f32 v228, v228, v229\n\t"
"s_mov_b32 s20, 614400\n\t"
"v_pk_fma_f32 v[230:231], v[232:233], s[56:57], v[230:231]\n\t"
"v_pk_fma_f32 v[232:233], v[230:231], s[58:59], v[232:233] neg_lo:[0,0,1] neg_hi:[0,0,1]\n\t"
"v_pk_fma_f32 v[234:235], v[68:69], s[60:61], v[230:231]\n\t"
"v_pk_fma_f32 v[68:69], v[230:231], s[60:61], v[68:69]\n\t"
"v_pk_fma_f32 v[230:231], v[168:169], s[62:63], v[232:233]\n\t"
"v_pk_fma_f32 v[168:169], v[232:233], s[62:63], v[168:169]\n\t"
"v_mfma_f32_16x16x32_bf16 v[36:39], v[240:243], a[172:175], v[36:39]\n\t"
"v_accvgpr_write_b32 a105, v224\n\t"
"v_accvgpr_write_b32 a107, v228\n\t"
"v_mfma_f32_16x16x32_bf16 v[36:39], a[228:231], a[176:179], v[36:39]\n\t"
"v_accvgpr_read_b32 v236, a114\n\t"
"v_accvgpr_read_b32 v238, a118\n\t"
"v_mfma_f32_16x16x32_bf16 v[36:39], a[232:235], a[180:183], v[36:39]\n\t"
"buffer_load_dwordx4 a[208:211], v252, s[8:11], s20 offen offset:0 sc1\n\t"
"v_accvgpr_read_b32 v237, a115\n\t"
"v_accvgpr_read_b32 v239, a119\n\t"
"v_cvt_pk_bf16_f32 v230, v230, v231\n\t"
"v_cvt_pk_bf16_f32 v234, v234, v235\n\t"
"v_pk_fma_f32 v[236:237], v[238:239], s[56:57], v[236:237]\n\t"
"v_pk_fma_f32 v[238:239], v[236:237], s[58:59], v[238:239] neg_lo:[0,0,1] neg_hi:[0,0,1]\n\t"
"v_pk_fma_f32 v[98:99], v[70:71], s[60:61], v[236:237]\n\t"
"v_pk_fma_f32 v[70:71], v[236:237], s[60:61], v[70:71]\n\t"
"v_pk_fma_f32 v[236:237], v[170:171], s[62:63], v[238:239]\n\t"
"v_pk_fma_f32 v[170:171], v[238:239], s[62:63], v[170:171]\n\t"
"v_mfma_f32_16x16x32_bf16 v[40:43], v[240:243], a[176:179], v[40:43]\n\t"
"v_accvgpr_write_b32 a112, v230\n\t"
"v_accvgpr_write_b32 a114, v234\n\t"
"v_mfma_f32_16x16x32_bf16 v[40:43], a[228:231], a[180:183], v[40:43]\n\t"
"v_accvgpr_read_b32 v224, a120\n\t"
"v_accvgpr_read_b32 v226, a124\n\t"
"v_mfma_f32_16x16x32_bf16 v[40:43], a[232:235], a[184:187], v[40:43]\n\t"
"buffer_load_dwordx4 a[212:215], v252, s[8:11], s20 offen offset:1024 sc1\n\t"
"v_accvgpr_read_b32 v225, a121\n\t"
"v_accvgpr_read_b32 v227, a125\n\t"
"v_cvt_pk_bf16_f32 v236, v236, v237\n\t"
"v_cvt_pk_bf16_f32 v98, v98, v99\n\t"
"v_pk_fma_f32 v[224:225], v[226:227], s[56:57], v[224:225]\n\t"
"v_pk_fma_f32 v[226:227], v[224:225], s[58:59], v[226:227] neg_lo:[0,0,1] neg_hi:[0,0,1]\n\t"
"v_pk_fma_f32 v[228:229], v[72:73], s[60:61], v[224:225]\n\t"
"v_pk_fma_f32 v[72:73], v[224:225], s[60:61], v[72:73]\n\t"
"v_pk_fma_f32 v[224:225], v[172:173], s[62:63], v[226:227]\n\t"
"v_pk_fma_f32 v[172:173], v[226:227], s[62:63], v[172:173]\n\t"
"v_mfma_f32_16x16x32_bf16 v[44:47], v[240:243], a[180:183], v[44:47]\n\t"
"v_accvgpr_write_b32 a113, v236\n\t"
"v_accvgpr_write_b32 a115, v98\n\t"
"v_mfma_f32_16x16x32_bf16 v[44:47], a[228:231], a[184:187], v[44:47]\n\t"
"v_accvgpr_read_b32 v230, a122\n\t"
"v_accvgpr_read_b32 v232, a126\n\t"
"v_mfma_f32_16x16x32_bf16 v[44:47], a[232:235], a[188:191], v[44:47]\n\t"
"buffer_load_dwordx4 a[116:119], v252, s[8:11], s20 offen offset:2048 sc1\n\t"
"v_accvgpr_read_b32 v231, a123\n\t"
"v_accvgpr_read_b32 v233, a127\n\t"
"v_accvgpr_read_b32 v236, a128\n\t"
"v_accvgpr_read_b32 v238, a132\n\t"
"v_accvgpr_read_b32 v237, a129\n\t"
"v_accvgpr_read_b32 v239, a133\n\t"
"v_cvt_pk_bf16_f32 v224, v224, v225\n\t"
"v_cvt_pk_bf16_f32 v228, v228, v229\n\t"
"s_mov_b32 s20, 774144\n\t"
"v_pk_fma_f32 v[230:231], v[232:233], s[56:57], v[230:231]\n\t"
"v_pk_fma_f32 v[232:233], v[230:231], s[58:59], v[232:233] neg_lo:[0,0,1] neg_hi:[0,0,1]\n\t"
"v_pk_fma_f32 v[234:235], v[74:75], s[60:61], v[230:231]\n\t"
"v_pk_fma_f32 v[74:75], v[230:231], s[60:61], v[74:75]\n\t"
"v_pk_fma_f32 v[230:231], v[174:175], s[62:63], v[232:233]\n\t"
"v_pk_fma_f32 v[174:175], v[232:233], s[62:63], v[174:175]\n\t"
"v_pk_fma_f32 v[236:237], v[238:239], s[56:57], v[236:237]\n\t"
"v_pk_fma_f32 v[238:239], v[236:237], s[58:59], v[238:239] neg_lo:[0,0,1] neg_hi:[0,0,1]\n\t"
"v_pk_fma_f32 v[98:99], v[76:77], s[60:61], v[236:237]\n\t"
"v_pk_fma_f32 v[76:77], v[236:237], s[60:61], v[76:77]\n\t"
"v_pk_fma_f32 v[236:237], v[176:177], s[62:63], v[238:239]\n\t"
"v_pk_fma_f32 v[176:177], v[238:239], s[62:63], v[176:177]\n\t"
"v_mfma_f32_16x16x32_bf16 v[48:51], v[240:243], a[184:187], v[48:51]\n\t"
"v_accvgpr_write_b32 a120, v224\n\t"
"v_accvgpr_write_b32 a122, v228\n\t"
"v_mfma_f32_16x16x32_bf16 v[48:51], a[228:231], a[188:191], v[48:51]\n\t"
"v_accvgpr_read_b32 v224, a130\n\t"
"v_accvgpr_read_b32 v226, a134\n\t"
"v_mfma_f32_16x16x32_bf16 v[48:51], a[232:235], a[192:195], v[48:51]\n\t"
"buffer_load_dwordx4 a[216:219], v252, s[8:11], s20 offen offset:0 sc1\n\t"
"v_accvgpr_read_b32 v225, a131\n\t"
"v_accvgpr_read_b32 v227, a135\n\t"
"v_cvt_pk_bf16_f32 v230, v230, v231\n\t"
"v_cvt_pk_bf16_f32 v234, v234, v235\n\t"
"v_cvt_pk_bf16_f32 v236, v236, v237\n\t"
"v_cvt_pk_bf16_f32 v98, v98, v99\n\t"
"v_pk_fma_f32 v[224:225], v[226:227], s[56:57], v[224:225]\n\t"
"v_pk_fma_f32 v[226:227], v[224:225], s[58:59], v[226:227] neg_lo:[0,0,1] neg_hi:[0,0,1]\n\t"
"v_pk_fma_f32 v[228:229], v[78:79], s[60:61], v[224:225]\n\t"
"v_pk_fma_f32 v[78:79], v[224:225], s[60:61], v[78:79]\n\t"
"v_pk_fma_f32 v[224:225], v[178:179], s[62:63], v[226:227]\n\t"
"v_pk_fma_f32 v[178:179], v[226:227], s[62:63], v[178:179]\n\t"
"v_mfma_f32_16x16x32_bf16 v[52:55], v[240:243], a[188:191], v[52:55]\n\t"
"Lpg11_%=:\n\t"
"s_cbranch_vccnz Lvr0_%=\n\t"
"v_accvgpr_write_b32 a121, v230\n\t"
"v_accvgpr_write_b32 a123, v234\n\t"
"v_mfma_f32_16x16x32_bf16 v[52:55], a[228:231], a[192:195], v[52:55]\n\t"
"v_accvgpr_write_b32 a128, v236\n\t"
"v_accvgpr_write_b32 a130, v98\n\t"
"v_mfma_f32_16x16x32_bf16 v[52:55], a[232:235], a[196:199], v[52:55]\n\t"
"buffer_load_dwordx4 a[124:127], v252, s[8:11], s20 offen offset:1024 sc1\n\t"
"v_accvgpr_read_b32 v230, a136\n\t"
"v_accvgpr_read_b32 v232, a140\n\t"
"v_accvgpr_read_b32 v231, a137\n\t"
"v_accvgpr_read_b32 v233, a141\n\t"
"v_cvt_pk_bf16_f32 v224, v224, v225\n\t"
"v_cvt_pk_bf16_f32 v228, v228, v229\n\t"
"v_pk_fma_f32 v[230:231], v[232:233], s[56:57], v[230:231]\n\t"
"v_pk_fma_f32 v[232:233], v[230:231], s[58:59], v[232:233] neg_lo:[0,0,1] neg_hi:[0,0,1]\n\t"
"v_pk_fma_f32 v[234:235], v[80:81], s[60:61], v[230:231]\n\t"
"v_pk_fma_f32 v[80:81], v[230:231], s[60:61], v[80:81]\n\t"
"v_pk_fma_f32 v[230:231], v[180:181], s[62:63], v[232:233]\n\t"
"v_pk_fma_f32 v[180:181], v[232:233], s[62:63], v[180:181]\n\t"
"v_mfma_f32_16x16x32_bf16 v[56:59], v[240:243], a[192:195], v[56:59]\n\t"
"v_accvgpr_write_b32 a129, v224\n\t"
"v_accvgpr_write_b32 a131, v228\n\t"
"v_mfma_f32_16x16x32_bf16 v[56:59], a[228:231], a[196:199], v[56:59]\n\t"
"v_accvgpr_read_b32 v236, a138\n\t"
"v_accvgpr_read_b32 v238, a142\n\t"
"v_mfma_f32_16x16x32_bf16 v[56:59], a[232:235], a[200:203], v[56:59]\n\t"
"s_waitcnt vmcnt(19)\n\t"
"ds_write_b128 v249, v[196:199] offset:0\n\t"
"v_accvgpr_read_b32 v237, a139\n\t"
"v_accvgpr_read_b32 v239, a143\n\t"
"v_cvt_pk_bf16_f32 v230, v230, v231\n\t"
"v_cvt_pk_bf16_f32 v234, v234, v235\n\t"
"v_pk_fma_f32 v[236:237], v[238:239], s[56:57], v[236:237]\n\t"
"v_pk_fma_f32 v[238:239], v[236:237], s[58:59], v[238:239] neg_lo:[0,0,1] neg_hi:[0,0,1]\n\t"
"v_pk_fma_f32 v[98:99], v[82:83], s[60:61], v[236:237]\n\t"
"v_pk_fma_f32 v[82:83], v[236:237], s[60:61], v[82:83]\n\t"
"v_pk_fma_f32 v[236:237], v[182:183], s[62:63], v[238:239]\n\t"
"v_pk_fma_f32 v[182:183], v[238:239], s[62:63], v[182:183]\n\t"
"v_mfma_f32_16x16x32_bf16 v[60:63], v[240:243], a[196:199], v[60:63]\n\t"
"v_accvgpr_write_b32 a136, v230\n\t"
"v_accvgpr_write_b32 a138, v234\n\t"
"v_mfma_f32_16x16x32_bf16 v[60:63], a[228:231], a[200:203], v[60:63]\n\t"
"v_accvgpr_read_b32 v224, a144\n\t"
"v_accvgpr_read_b32 v226, a148\n\t"
"v_mfma_f32_16x16x32_bf16 v[60:63], a[232:235], a[204:207], v[60:63]\n\t"
"buffer_load_dwordx4 a[132:135], v252, s[8:11], s20 offen offset:2048 sc1\n\t"
"v_accvgpr_read_b32 v225, a145\n\t"
"v_accvgpr_read_b32 v227, a149\n\t"
"v_cvt_pk_bf16_f32 v236, v236, v237\n\t"
"v_cvt_pk_bf16_f32 v98, v98, v99\n\t"
"s_mov_b32 s20, 786432\n\t"
"v_pk_fma_f32 v[224:225], v[226:227], s[56:57], v[224:225]\n\t"
"v_pk_fma_f32 v[226:227], v[224:225], s[58:59], v[226:227] neg_lo:[0,0,1] neg_hi:[0,0,1]\n\t"
"v_pk_fma_f32 v[228:229], v[84:85], s[60:61], v[224:225]\n\t"
"v_pk_fma_f32 v[84:85], v[224:225], s[60:61], v[84:85]\n\t"
"v_pk_fma_f32 v[224:225], v[184:185], s[62:63], v[226:227]\n\t"
"v_pk_fma_f32 v[184:185], v[226:227], s[62:63], v[184:185]\n\t"
"v_mfma_f32_16x16x32_bf16 v[64:67], a[252:255], a[168:171], v[64:67]\n\t"
"ds_read_b128 a[168:171], v255 offset:2048\n\t"
"v_accvgpr_write_b32 a137, v236\n\t"
"v_mfma_f32_16x16x32_bf16 v[64:67], a[236:239], a[172:175], v[64:67]\n\t"
"v_accvgpr_write_b32 a139, v98\n\t"
"v_accvgpr_read_b32 v230, a146\n\t"
"v_mfma_f32_16x16x32_bf16 v[64:67], a[240:243], a[176:179], v[64:67]\n\t"
"buffer_load_dwordx4 a[220:223], v252, s[8:11], s20 offen offset:0 sc1\n\t"
"v_accvgpr_read_b32 v232, a150\n\t"
"v_accvgpr_read_b32 v231, a147\n\t"
"v_accvgpr_read_b32 v233, a151\n\t"
"v_cvt_pk_bf16_f32 v224, v224, v225\n\t"
"v_cvt_pk_bf16_f32 v228, v228, v229\n\t"
"v_pk_fma_f32 v[230:231], v[232:233], s[56:57], v[230:231]\n\t"
"v_pk_fma_f32 v[232:233], v[230:231], s[58:59], v[232:233] neg_lo:[0,0,1] neg_hi:[0,0,1]\n\t"
"v_pk_fma_f32 v[234:235], v[86:87], s[60:61], v[230:231]\n\t"
"v_pk_fma_f32 v[86:87], v[230:231], s[60:61], v[86:87]\n\t"
"v_pk_fma_f32 v[230:231], v[186:187], s[62:63], v[232:233]\n\t"
"v_pk_fma_f32 v[186:187], v[232:233], s[62:63], v[186:187]\n\t"
"v_mfma_f32_16x16x32_bf16 v[68:71], a[252:255], a[172:175], v[68:71]\n\t"
"ds_read_b128 a[172:175], v255 offset:5120\n\t"
"v_accvgpr_write_b32 a144, v224\n\t"
"v_mfma_f32_16x16x32_bf16 v[68:71], a[236:239], a[176:179], v[68:71]\n\t"
"v_accvgpr_write_b32 a146, v228\n\t"
"v_accvgpr_read_b32 v236, a152\n\t"
"v_mfma_f32_16x16x32_bf16 v[68:71], a[240:243], a[180:183], v[68:71]\n\t"
"ds_write_b64 v250, a[104:105] offset:0\n\t"
"v_accvgpr_read_b32 v238, a156\n\t"
"v_accvgpr_read_b32 v237, a153\n\t"
"v_accvgpr_read_b32 v239, a157\n\t"
"v_cvt_pk_bf16_f32 v230, v230, v231\n\t"
"v_cvt_pk_bf16_f32 v234, v234, v235\n\t"
"v_pk_fma_f32 v[236:237], v[238:239], s[56:57], v[236:237]\n\t"
"v_pk_fma_f32 v[238:239], v[236:237], s[58:59], v[238:239] neg_lo:[0,0,1] neg_hi:[0,0,1]\n\t"
"v_pk_fma_f32 v[98:99], v[88:89], s[60:61], v[236:237]\n\t"
"v_pk_fma_f32 v[88:89], v[236:237], s[60:61], v[88:89]\n\t"
"v_pk_fma_f32 v[236:237], v[188:189], s[62:63], v[238:239]\n\t"
"v_pk_fma_f32 v[188:189], v[238:239], s[62:63], v[188:189]\n\t"
"v_mfma_f32_16x16x32_bf16 v[72:75], a[252:255], a[176:179], v[72:75]\n\t"
"ds_read_b128 a[176:179], v255 offset:8192\n\t"
"v_accvgpr_write_b32 a145, v230\n\t"
"v_mfma_f32_16x16x32_bf16 v[72:75], a[236:239], a[180:183], v[72:75]\n\t"
"v_accvgpr_write_b32 a147, v234\n\t"
"v_accvgpr_read_b32 v224, a154\n\t"
"v_mfma_f32_16x16x32_bf16 v[72:75], a[240:243], a[184:187], v[72:75]\n\t"
"buffer_load_dwordx4 v[240:243], v252, s[8:11], s20 offen offset:1024 sc1\n\t"
"v_accvgpr_read_b32 v226, a158\n\t"
"v_accvgpr_read_b32 v225, a155\n\t"
"v_accvgpr_read_b32 v227, a159\n\t"
"v_cvt_pk_bf16_f32 v236, v236, v237\n\t"
"v_cvt_pk_bf16_f32 v98, v98, v99\n\t"
"v_pk_fma_f32 v[224:225], v[226:227], s[56:57], v[224:225]\n\t"
"v_pk_fma_f32 v[226:227], v[224:225], s[58:59], v[226:227] neg_lo:[0,0,1] neg_hi:[0,0,1]\n\t"
"v_pk_fma_f32 v[228:229], v[90:91], s[60:61], v[224:225]\n\t"
"v_pk_fma_f32 v[90:91], v[224:225], s[60:61], v[90:91]\n\t"
"v_pk_fma_f32 v[224:225], v[190:191], s[62:63], v[226:227]\n\t"
"v_pk_fma_f32 v[190:191], v[226:227], s[62:63], v[190:191]\n\t"
"v_mfma_f32_16x16x32_bf16 v[76:79], a[252:255], a[180:183], v[76:79]\n\t"
"ds_read_b128 a[180:183], v255 offset:11264\n\t"
"v_accvgpr_write_b32 a152, v236\n\t"
"v_mfma_f32_16x16x32_bf16 v[76:79], a[236:239], a[184:187], v[76:79]\n\t"
"v_accvgpr_write_b32 a154, v98\n\t"
"v_accvgpr_read_b32 v230, a160\n\t"
"v_mfma_f32_16x16x32_bf16 v[76:79], a[240:243], a[188:191], v[76:79]\n\t"
"buffer_load_dwordx4 a[140:143], v252, s[8:11], s20 offen offset:2048 sc1\n\t"
"v_accvgpr_read_b32 v232, a164\n\t"
"v_accvgpr_read_b32 v231, a161\n\t"
"v_accvgpr_read_b32 v233, a165\n\t"
"v_cvt_pk_bf16_f32 v224, v224, v225\n\t"
"v_cvt_pk_bf16_f32 v228, v228, v229\n\t"
"s_mov_b32 s20, 798720\n\t"
"v_pk_fma_f32 v[230:231], v[232:233], s[56:57], v[230:231]\n\t"
"v_pk_fma_f32 v[232:233], v[230:231], s[58:59], v[232:233] neg_lo:[0,0,1] neg_hi:[0,0,1]\n\t"
"v_pk_fma_f32 v[234:235], v[92:93], s[60:61], v[230:231]\n\t"
"v_pk_fma_f32 v[92:93], v[230:231], s[60:61], v[92:93]\n\t"
"v_pk_fma_f32 v[230:231], v[192:193], s[62:63], v[232:233]\n\t"
"v_pk_fma_f32 v[192:193], v[232:233], s[62:63], v[192:193]\n\t"
"v_mfma_f32_16x16x32_bf16 v[80:83], a[252:255], a[184:187], v[80:83]\n\t"
"ds_read_b128 a[184:187], v255 offset:14336\n\t"
"v_accvgpr_write_b32 a153, v224\n\t"
"v_mfma_f32_16x16x32_bf16 v[80:83], a[236:239], a[188:191], v[80:83]\n\t"
"v_accvgpr_write_b32 a155, v228\n\t"
"v_accvgpr_read_b32 v236, a162\n\t"
"v_mfma_f32_16x16x32_bf16 v[80:83], a[240:243], a[192:195], v[80:83]\n\t"
"buffer_load_dwordx4 a[148:151], v252, s[8:11], s20 offen offset:0 sc1\n\t"
"v_accvgpr_read_b32 v238, a166\n\t"
"v_accvgpr_read_b32 v237, a163\n\t"
"v_accvgpr_read_b32 v239, a167\n\t"
"v_cvt_pk_bf16_f32 v230, v230, v231\n\t"
"v_cvt_pk_bf16_f32 v234, v234, v235\n\t"
"v_pk_fma_f32 v[236:237], v[238:239], s[56:57], v[236:237]\n\t"
"v_pk_fma_f32 v[238:239], v[236:237], s[58:59], v[238:239] neg_lo:[0,0,1] neg_hi:[0,0,1]\n\t"
"v_pk_fma_f32 v[98:99], v[94:95], s[60:61], v[236:237]\n\t"
"v_pk_fma_f32 v[94:95], v[236:237], s[60:61], v[94:95]\n\t"
"v_pk_fma_f32 v[236:237], v[194:195], s[62:63], v[238:239]\n\t"
"v_pk_fma_f32 v[194:195], v[238:239], s[62:63], v[194:195]\n\t"
"v_mfma_f32_16x16x32_bf16 v[84:87], a[252:255], a[188:191], v[84:87]\n\t"
"ds_read_b128 a[188:191], v255 offset:17408\n\t"
"v_accvgpr_write_b32 a160, v230\n\t"
"v_mfma_f32_16x16x32_bf16 v[84:87], a[236:239], a[192:195], v[84:87]\n\t"
"v_accvgpr_write_b32 a162, v234\n\t"
"v_cvt_pk_bf16_f32 v236, v236, v237\n\t"
"v_mfma_f32_16x16x32_bf16 v[84:87], a[240:243], a[196:199], v[84:87]\n\t"
"buffer_load_dwordx4 a[224:227], v252, s[8:11], s20 offen offset:1024 sc1\n\t"
"v_cvt_pk_bf16_f32 v98, v98, v99\n\t"
"v_mfma_f32_16x16x32_bf16 v[88:91], a[252:255], a[192:195], v[88:91]\n\t"
"ds_read_b128 a[192:195], v255 offset:20480\n\t"
"v_accvgpr_write_b32 a161, v236\n\t"
"v_mfma_f32_16x16x32_bf16 v[88:91], a[236:239], a[196:199], v[88:91]\n\t"
"v_accvgpr_write_b32 a163, v98\n\t"
"v_mfma_f32_16x16x32_bf16 v[88:91], a[240:243], a[200:203], v[88:91]\n\t"
"buffer_load_dwordx4 a[228:231], v252, s[8:11], s20 offen offset:2048 sc1\n\t"
"ds_write_b128 v249, v[200:203] offset:6144\n\t"
"s_mov_b32 s20, 884736\n\t"
"v_mfma_f32_16x16x32_bf16 v[92:95], a[252:255], a[196:199], v[92:95]\n\t"
"buffer_load_dwordx4 a[232:235], v252, s[8:11], s20 offen offset:0 sc1\n\t"
"v_mfma_f32_16x16x32_bf16 v[92:95], a[236:239], a[200:203], v[92:95]\n\t"
"buffer_load_dwordx4 a[164:167], v252, s[8:11], s20 offen offset:1024 sc1\n\t"
"v_mfma_f32_16x16x32_bf16 v[92:95], a[240:243], a[204:207], v[92:95]\n\t"
"s_waitcnt vmcnt(26) lgkmcnt(5)\n\t"
"ds_write_b64 v250, a[106:107] offset:6144\n\t"
"v_mfma_f32_16x16x32_bf16 v[100:103], a[244:247], a[168:171], v[100:103]\n\t"
"buffer_load_dwordx4 a[248:251], v252, s[8:11], s20 offen offset:2048 sc1\n\t"
"v_mfma_f32_16x16x32_bf16 v[100:103], a[40:43], a[172:175], v[100:103]\n\t"
"s_waitcnt lgkmcnt(5)\n\t"
"ds_read_b128 a[196:199], v255 offset:23552\n\t"
"s_mov_b32 s20, 897024\n\t"
"v_mfma_f32_16x16x32_bf16 v[100:103], a[48:51], a[176:179], v[100:103]\n\t"
"s_add_u32 m0, s25, 512\n\t"
"ds_read_b128 a[200:203], v255 offset:26624\n\t"
"v_mfma_f32_16x16x32_bf16 v[104:107], a[244:247], a[172:175], v[104:107]\n\t"
"s_waitcnt lgkmcnt(6)\n\t"
"ds_read_b128 a[204:207], v255 offset:29696\n\t"
"v_mfma_f32_16x16x32_bf16 v[104:107], a[40:43], a[176:179], v[104:107]\n\t"
"v_mfma_f32_16x16x32_bf16 v[104:107], a[48:51], a[180:183], v[104:107]\n\t"
"s_waitcnt lgkmcnt(0)\n\t"
"s_barrier\n\t"
"v_mfma_f32_16x16x32_bf16 v[108:111], a[244:247], a[176:179], v[108:111]\n\t"
"v_mfma_f32_16x16x32_bf16 v[108:111], a[40:43], a[180:183], v[108:111]\n\t"
"buffer_load_dwordx4 v[236:239], v252, s[8:11], s20 offen offset:0 sc1\n\t"
"v_mfma_f32_16x16x32_bf16 v[108:111], a[48:51], a[184:187], v[108:111]\n\t"
"buffer_load_dwordx4 v[96:99], v252, s[8:11], s20 offen offset:1024 sc1\n\t"
"v_mfma_f32_16x16x32_bf16 v[112:115], a[244:247], a[180:183], v[112:115]\n\t"
"buffer_load_dwordx4 v254, s[12:15], 0 offen offset:0 nt lds\n\t"
"v_mfma_f32_16x16x32_bf16 v[112:115], a[40:43], a[184:187], v[112:115]\n\t"
"buffer_load_dwordx4 a[236:239], v252, s[8:11], s20 offen offset:2048 sc1\n\t"
"v_mfma_f32_16x16x32_bf16 v[112:115], a[48:51], a[188:191], v[112:115]\n\t"
"s_add_u32 m0, s24, 512\n\t"
"s_add_u32 s28, s21, 0\n\t"
"ds_write_b128 v249, v[204:207] offset:12288\n\t"
"v_mfma_f32_16x16x32_bf16 v[116:119], a[244:247], a[184:187], v[116:119]\n\t"
"buffer_load_dwordx4 v247, s[12:15], s28 offen offset:0 nt lds\n\t"
"v_mfma_f32_16x16x32_bf16 v[116:119], a[40:43], a[188:191], v[116:119]\n\t"
"s_add_u32 m0, s24, 6656\n\t"
"s_add_u32 s28, s21, 196608\n\t"
"v_mfma_f32_16x16x32_bf16 v[116:119], a[48:51], a[192:195], v[116:119]\n\t"
"buffer_load_dwordx4 v247, s[12:15], s28 offen offset:0 nt lds\n\t"
"s_add_u32 m0, s24, 12800\n\t"
"s_add_u32 s28, s21, 393216\n\t"
"v_mfma_f32_16x16x32_bf16 v[120:123], a[244:247], a[188:191], v[120:123]\n\t"
"buffer_load_dwordx4 v247, s[12:15], s28 offen offset:0 nt lds\n\t"
"v_mfma_f32_16x16x32_bf16 v[120:123], a[40:43], a[192:195], v[120:123]\n\t"
"s_add_u32 m0, s24, 20480\n\t"
"s_add_u32 s28, s21, 589824\n\t"
"v_mfma_f32_16x16x32_bf16 v[120:123], a[48:51], a[196:199], v[120:123]\n\t"
"buffer_load_dwordx4 v247, s[12:15], s28 offen offset:0 nt lds\n\t"
"v_mfma_f32_16x16x32_bf16 v[124:127], a[244:247], a[192:195], v[124:127]\n\t"
"v_mfma_f32_16x16x32_bf16 v[124:127], a[40:43], a[196:199], v[124:127]\n\t"
"v_mfma_f32_16x16x32_bf16 v[124:127], a[48:51], a[200:203], v[124:127]\n\t"
"ds_write_b64 v250, a[112:113] offset:12288\n\t"
"v_mfma_f32_16x16x32_bf16 v[128:131], a[244:247], a[196:199], v[128:131]\n\t"
"s_waitcnt vmcnt(32)\n\t"
"v_mfma_f32_16x16x32_bf16 v[128:131], a[40:43], a[200:203], v[128:131]\n\t"
"v_mfma_f32_16x16x32_bf16 v[128:131], a[48:51], a[204:207], v[128:131]\n\t"
"ds_write_b128 v249, v[208:211] offset:18432\n\t"
"v_mfma_f32_16x16x32_bf16 v[132:135], a[52:55], a[168:171], v[132:135]\n\t"
"v_mfma_f32_16x16x32_bf16 v[132:135], a[44:47], a[172:175], v[132:135]\n\t"
"v_mfma_f32_16x16x32_bf16 v[132:135], a[60:63], a[176:179], v[132:135]\n\t"
"ds_write_b64 v250, a[114:115] offset:18432\n\t"
"v_mfma_f32_16x16x32_bf16 v[136:139], a[52:55], a[172:175], v[136:139]\n\t"
"v_mfma_f32_16x16x32_bf16 v[136:139], a[44:47], a[176:179], v[136:139]\n\t"
"v_mfma_f32_16x16x32_bf16 v[136:139], a[60:63], a[180:183], v[136:139]\n\t"
"v_mfma_f32_16x16x32_bf16 v[140:143], a[52:55], a[176:179], v[140:143]\n\t"
"v_mfma_f32_16x16x32_bf16 v[140:143], a[44:47], a[180:183], v[140:143]\n\t"
"v_mfma_f32_16x16x32_bf16 v[140:143], a[60:63], a[184:187], v[140:143]\n\t"
"s_mov_b32 s20, 909312\n\t"
"v_mfma_f32_16x16x32_bf16 v[144:147], a[52:55], a[180:183], v[144:147]\n\t"
"v_mfma_f32_16x16x32_bf16 v[144:147], a[44:47], a[184:187], v[144:147]\n\t"
"v_mfma_f32_16x16x32_bf16 v[144:147], a[60:63], a[188:191], v[144:147]\n\t"
"v_mfma_f32_16x16x32_bf16 v[148:151], a[52:55], a[184:187], v[148:151]\n\t"
"v_mfma_f32_16x16x32_bf16 v[148:151], a[44:47], a[188:191], v[148:151]\n\t"
"v_mfma_f32_16x16x32_bf16 v[148:151], a[60:63], a[192:195], v[148:151]\n\t"
"buffer_load_dwordx4 a[104:107], v252, s[8:11], s20 offen offset:0 sc1\n\t"
"v_mfma_f32_16x16x32_bf16 v[152:155], a[52:55], a[188:191], v[152:155]\n\t"
"ds_read_b128 a[40:43], v252 offset:0\n\t"
"v_mfma_f32_16x16x32_bf16 v[152:155], a[44:47], a[192:195], v[152:155]\n\t"
"ds_read_b128 a[48:51], v252 offset:1024\n\t"
"v_mfma_f32_16x16x32_bf16 v[152:155], a[60:63], a[196:199], v[152:155]\n\t"
"v_mfma_f32_16x16x32_bf16 v[156:159], a[52:55], a[192:195], v[156:159]\n\t"
"s_add_u32 m0, s24, 26624\n\t"
"s_add_u32 s28, s21, 786432\n\t"
"ds_read_b128 a[56:59], v252 offset:2048\n\t"
"v_mfma_f32_16x16x32_bf16 v[156:159], a[44:47], a[196:199], v[156:159]\n\t"
"v_mfma_f32_16x16x32_bf16 v[156:159], a[60:63], a[200:203], v[156:159]\n\t"
"v_mfma_f32_16x16x32_bf16 v[160:163], a[52:55], a[196:199], v[160:163]\n\t"
"s_waitcnt vmcnt(30)\n\t"
"v_mfma_f32_16x16x32_bf16 v[160:163], a[44:47], a[200:203], v[160:163]\n\t"
"buffer_load_dwordx4 v247, s[12:15], s28 offen offset:0 nt lds\n\t"
"v_mfma_f32_16x16x32_bf16 v[160:163], a[60:63], a[204:207], v[160:163]\n\t"
"s_add_u32 m0, s24, 32768\n\t"
"s_add_u32 s28, s21, 983040\n\t"
"v_mfma_f32_16x16x32_bf16 v[164:167], a[64:67], a[168:171], v[164:167]\n\t"
"buffer_load_dwordx4 v247, s[12:15], s28 offen offset:0 nt lds\n\t"
"v_mfma_f32_16x16x32_bf16 v[164:167], a[68:71], a[172:175], v[164:167]\n\t"
"s_add_u32 m0, s24, 40448\n\t"
"s_add_u32 s28, s21, 1179648\n\t"
"ds_read_b128 a[168:171], v244 offset:24576\n\t"
"v_mfma_f32_16x16x32_bf16 v[164:167], a[72:75], a[176:179], v[164:167]\n\t"
"buffer_load_dwordx4 v247, s[12:15], s28 offen offset:0 nt lds\n\t"
"v_mfma_f32_16x16x32_bf16 v[168:171], a[64:67], a[172:175], v[168:171]\n\t"
"s_add_u32 m0, s24, 46592\n\t"
"s_add_u32 s28, s21, 1376256\n\t"
"ds_read_b128 a[172:175], v244 offset:25088\n\t"
"v_mfma_f32_16x16x32_bf16 v[168:171], a[68:71], a[176:179], v[168:171]\n\t"
"buffer_load_dwordx4 v247, s[12:15], s28 offen offset:0 nt lds\n\t"
"v_mfma_f32_16x16x32_bf16 v[168:171], a[72:75], a[180:183], v[168:171]\n\t"
"s_add_u32 m0, s24, 52736\n\t"
"s_add_u32 s28, s21, 1572864\n\t"
"v_mfma_f32_16x16x32_bf16 v[172:175], a[64:67], a[176:179], v[172:175]\n\t"
"buffer_load_dwordx4 v247, s[12:15], s28 offen offset:0 nt lds\n\t"
"v_mfma_f32_16x16x32_bf16 v[172:175], a[68:71], a[180:183], v[172:175]\n\t"
"ds_read_b128 a[176:179], v244 offset:25600\n\t"
"v_mfma_f32_16x16x32_bf16 v[172:175], a[72:75], a[184:187], v[172:175]\n\t"
"v_mfma_f32_16x16x32_bf16 v[176:179], a[64:67], a[180:183], v[176:179]\n\t"
"ds_read_b128 a[180:183], v244 offset:26112\n\t"
"v_mfma_f32_16x16x32_bf16 v[176:179], a[68:71], a[184:187], v[176:179]\n\t"
"v_mfma_f32_16x16x32_bf16 v[176:179], a[72:75], a[188:191], v[176:179]\n\t"
"s_waitcnt lgkmcnt(4)\n\t"
"s_barrier\n\t"
"v_mfma_f32_16x16x32_bf16 v[180:183], a[64:67], a[184:187], v[180:183]\n\t"
"ds_read_b128 a[184:187], v244 offset:26624\n\t"
"v_mfma_f32_16x16x32_bf16 v[180:183], a[68:71], a[188:191], v[180:183]\n\t"
"v_mfma_f32_16x16x32_bf16 v[180:183], a[72:75], a[192:195], v[180:183]\n\t"
"ds_write_b128 v249, v[212:215] offset:0\n\t"
"v_mfma_f32_16x16x32_bf16 v[184:187], a[64:67], a[188:191], v[184:187]\n\t"
"v_mfma_f32_16x16x32_bf16 v[184:187], a[68:71], a[192:195], v[184:187]\n\t"
"ds_read_b128 a[188:191], v244 offset:27136\n\t"
"v_mfma_f32_16x16x32_bf16 v[184:187], a[72:75], a[196:199], v[184:187]\n\t"
"ds_write_b64 v250, a[120:121] offset:0\n\t"
"v_mfma_f32_16x16x32_bf16 v[188:191], a[64:67], a[192:195], v[188:191]\n\t"
"ds_read_b128 a[192:195], v244 offset:27648\n\t"
"v_mfma_f32_16x16x32_bf16 v[188:191], a[68:71], a[196:199], v[188:191]\n\t"
"v_mfma_f32_16x16x32_bf16 v[188:191], a[72:75], a[200:203], v[188:191]\n\t"
"ds_write_b128 v249, v[216:219] offset:6144\n\t"
"v_mfma_f32_16x16x32_bf16 v[192:195], a[64:67], a[196:199], v[192:195]\n\t"
"s_waitcnt vmcnt(32) lgkmcnt(7)\n\t"
"v_mfma_f32_16x16x32_bf16 v[192:195], a[68:71], a[200:203], v[192:195]\n\t"
"ds_read_b128 a[196:199], v244 offset:28160\n\t"
"v_mfma_f32_16x16x32_bf16 v[192:195], a[72:75], a[204:207], v[192:195]\n\t"
"ds_read_b128 a[200:203], v244 offset:28672\n\t"
"Lst15_%=:\n\t"
"v_mfma_f32_16x16x32_bf16 v[0:3], a[76:79], a[168:171], v[0:3]\n\t"
"s_waitcnt lgkmcnt(8)\n\t"
"ds_read_b128 a[204:207], v244 offset:29184\n\t"
"v_mfma_f32_16x16x32_bf16 v[0:3], a[84:87], a[172:175], v[0:3]\n\t"
"v_mfma_f32_16x16x32_bf16 v[0:3], a[88:91], a[176:179], v[0:3]\n\t"
"ds_write_b64 v250, a[122:123] offset:6144\n\t"
"v_mfma_f32_16x16x32_bf16 v[4:7], a[76:79], a[172:175], v[4:7]\n\t"
"s_waitcnt lgkmcnt(9)\n\t"
"v_mfma_f32_16x16x32_bf16 v[4:7], a[84:87], a[176:179], v[4:7]\n\t"
"v_mfma_f32_16x16x32_bf16 v[4:7], a[88:91], a[180:183], v[4:7]\n\t"
"v_mfma_f32_16x16x32_bf16 v[8:11], a[76:79], a[176:179], v[8:11]\n\t"
"s_waitcnt lgkmcnt(7)\n\t"
"v_mfma_f32_16x16x32_bf16 v[8:11], a[84:87], a[180:183], v[8:11]\n\t"
"v_mfma_f32_16x16x32_bf16 v[8:11], a[88:91], a[184:187], v[8:11]\n\t"
"v_mfma_f32_16x16x32_bf16 v[12:15], a[76:79], a[180:183], v[12:15]\n\t"
"s_waitcnt lgkmcnt(5)\n\t"
"v_mfma_f32_16x16x32_bf16 v[12:15], a[84:87], a[184:187], v[12:15]\n\t"
"v_mfma_f32_16x16x32_bf16 v[12:15], a[88:91], a[188:191], v[12:15]\n\t"
"v_mfma_f32_16x16x32_bf16 v[16:19], a[76:79], a[184:187], v[16:19]\n\t"
"s_waitcnt lgkmcnt(3)\n\t"
"v_mfma_f32_16x16x32_bf16 v[16:19], a[84:87], a[188:191], v[16:19]\n\t"
"v_mfma_f32_16x16x32_bf16 v[16:19], a[88:91], a[192:195], v[16:19]\n\t"
"ds_read_b128 a[64:67], v252 offset:12288\n\t"
"v_mfma_f32_16x16x32_bf16 v[20:23], a[76:79], a[188:191], v[20:23]\n\t"
"s_waitcnt lgkmcnt(3)\n\t"
"v_mfma_f32_16x16x32_bf16 v[20:23], a[84:87], a[192:195], v[20:23]\n\t"
"ds_read_b128 a[72:75], v252 offset:13312\n\t"
"v_mfma_f32_16x16x32_bf16 v[20:23], a[88:91], a[196:199], v[20:23]\n\t"
"v_mfma_f32_16x16x32_bf16 v[24:27], a[76:79], a[192:195], v[24:27]\n\t"
"s_waitcnt lgkmcnt(3)\n\t"
"ds_read_b128 a[80:83], v252 offset:14336\n\t"
"v_mfma_f32_16x16x32_bf16 v[24:27], a[84:87], a[196:199], v[24:27]\n\t"
"v_mfma_f32_16x16x32_bf16 v[24:27], a[88:91], a[200:203], v[24:27]\n\t"
"v_mfma_f32_16x16x32_bf16 v[28:31], a[76:79], a[196:199], v[28:31]\n\t"
"s_waitcnt vmcnt(29)\n\t"
"v_mfma_f32_16x16x32_bf16 v[28:31], a[84:87], a[200:203], v[28:31]\n\t"
"v_mfma_f32_16x16x32_bf16 v[28:31], a[88:91], a[204:207], v[28:31]\n\t"
"v_mfma_f32_16x16x32_bf16 v[32:35], a[92:95], a[168:171], v[32:35]\n\t"
"v_mfma_f32_16x16x32_bf16 v[32:35], a[96:99], a[172:175], v[32:35]\n\t"
"v_mfma_f32_16x16x32_bf16 v[32:35], a[100:103], a[176:179], v[32:35]\n\t"
"v_mfma_f32_16x16x32_bf16 v[36:39], a[92:95], a[172:175], v[36:39]\n\t"
"v_mfma_f32_16x16x32_bf16 v[36:39], a[96:99], a[176:179], v[36:39]\n\t"
"v_mfma_f32_16x16x32_bf16 v[36:39], a[100:103], a[180:183], v[36:39]\n\t"
"v_mfma_f32_16x16x32_bf16 v[40:43], a[92:95], a[176:179], v[40:43]\n\t"
"v_mfma_f32_16x16x32_bf16 v[40:43], a[96:99], a[180:183], v[40:43]\n\t"
"v_mfma_f32_16x16x32_bf16 v[40:43], a[100:103], a[184:187], v[40:43]\n\t"
"v_mfma_f32_16x16x32_bf16 v[44:47], a[92:95], a[180:183], v[44:47]\n\t"
"v_mfma_f32_16x16x32_bf16 v[44:47], a[96:99], a[184:187], v[44:47]\n\t"
"v_mfma_f32_16x16x32_bf16 v[44:47], a[100:103], a[188:191], v[44:47]\n\t"
"s_waitcnt lgkmcnt(0)\n\t"
"s_barrier\n\t"
"v_mfma_f32_16x16x32_bf16 v[48:51], a[92:95], a[184:187], v[48:51]\n\t"
"v_mfma_f32_16x16x32_bf16 v[48:51], a[96:99], a[188:191], v[48:51]\n\t"
"v_mfma_f32_16x16x32_bf16 v[48:51], a[100:103], a[192:195], v[48:51]\n\t"
"ds_write_b128 v249, a[0:3] offset:12288\n\t"
"v_mfma_f32_16x16x32_bf16 v[52:55], a[92:95], a[188:191], v[52:55]\n\t"
"v_mfma_f32_16x16x32_bf16 v[52:55], a[96:99], a[192:195], v[52:55]\n\t"
"v_mfma_f32_16x16x32_bf16 v[52:55], a[100:103], a[196:199], v[52:55]\n\t"
"ds_write_b64 v250, a[128:129] offset:12288\n\t"
"v_mfma_f32_16x16x32_bf16 v[56:59], a[92:95], a[192:195], v[56:59]\n\t"
"v_mfma_f32_16x16x32_bf16 v[56:59], a[96:99], a[196:199], v[56:59]\n\t"
"v_mfma_f32_16x16x32_bf16 v[56:59], a[100:103], a[200:203], v[56:59]\n\t"
"ds_write_b128 v249, a[4:7] offset:18432\n\t"
"v_mfma_f32_16x16x32_bf16 v[60:63], a[92:95], a[196:199], v[60:63]\n\t"
"s_waitcnt vmcnt(26)\n\t"
"v_mfma_f32_16x16x32_bf16 v[60:63], a[96:99], a[200:203], v[60:63]\n\t"
"v_mfma_f32_16x16x32_bf16 v[60:63], a[100:103], a[204:207], v[60:63]\n\t"
"ds_write_b64 v250, a[130:131] offset:18432\n\t"
"v_mfma_f32_16x16x32_bf16 v[64:67], a[208:211], a[168:171], v[64:67]\n\t"
"ds_read_b128 a[168:171], v244 offset:33792\n\t"
"v_mfma_f32_16x16x32_bf16 v[64:67], a[212:215], a[172:175], v[64:67]\n\t"
"v_mfma_f32_16x16x32_bf16 v[64:67], a[116:119], a[176:179], v[64:67]\n\t"
"buffer_load_dwordx4 a[112:115], v252, s[8:11], s20 offen offset:1024 sc1\n\t"
"v_mfma_f32_16x16x32_bf16 v[68:71], a[208:211], a[172:175], v[68:71]\n\t"
"ds_read_b128 a[172:175], v244 offset:34304\n\t"
"v_mfma_f32_16x16x32_bf16 v[68:71], a[212:215], a[176:179], v[68:71]\n\t"
"v_mfma_f32_16x16x32_bf16 v[68:71], a[116:119], a[180:183], v[68:71]\n\t"
"v_mfma_f32_16x16x32_bf16 v[72:75], a[208:211], a[176:179], v[72:75]\n\t"
"ds_read_b128 a[176:179], v244 offset:34816\n\t"
"v_mfma_f32_16x16x32_bf16 v[72:75], a[212:215], a[180:183], v[72:75]\n\t"
"v_mfma_f32_16x16x32_bf16 v[72:75], a[116:119], a[184:187], v[72:75]\n\t"
"ds_read_b128 a[88:91], v252 offset:0\n\t"
"v_mfma_f32_16x16x32_bf16 v[76:79], a[208:211], a[180:183], v[76:79]\n\t"
"ds_read_b128 a[180:183], v244 offset:35328\n\t"
"v_mfma_f32_16x16x32_bf16 v[76:79], a[212:215], a[184:187], v[76:79]\n\t"
"ds_read_b128 a[96:99], v252 offset:1024\n\t"
"v_mfma_f32_16x16x32_bf16 v[76:79], a[116:119], a[188:191], v[76:79]\n\t"
"v_mfma_f32_16x16x32_bf16 v[80:83], a[208:211], a[184:187], v[80:83]\n\t"
"ds_read_b128 a[184:187], v244 offset:35840\n\t"
"v_mfma_f32_16x16x32_bf16 v[80:83], a[212:215], a[188:191], v[80:83]\n\t"
"ds_read_b128 a[108:111], v252 offset:2048\n\t"
"v_mfma_f32_16x16x32_bf16 v[80:83], a[116:119], a[192:195], v[80:83]\n\t"
"v_mfma_f32_16x16x32_bf16 v[84:87], a[208:211], a[188:191], v[84:87]\n\t"
"ds_read_b128 a[188:191], v244 offset:36352\n\t"
"v_mfma_f32_16x16x32_bf16 v[84:87], a[212:215], a[192:195], v[84:87]\n\t"
"v_mfma_f32_16x16x32_bf16 v[84:87], a[116:119], a[196:199], v[84:87]\n\t"
"v_mfma_f32_16x16x32_bf16 v[88:91], a[208:211], a[192:195], v[88:91]\n\t"
"ds_read_b128 a[192:195], v244 offset:36864\n\t"
"v_mfma_f32_16x16x32_bf16 v[88:91], a[212:215], a[196:199], v[88:91]\n\t"
"v_mfma_f32_16x16x32_bf16 v[88:91], a[116:119], a[200:203], v[88:91]\n\t"
"v_mfma_f32_16x16x32_bf16 v[92:95], a[208:211], a[196:199], v[92:95]\n\t"
"s_waitcnt vmcnt(24) lgkmcnt(7)\n\t"
"ds_read_b128 a[196:199], v244 offset:37376\n\t"
"v_mfma_f32_16x16x32_bf16 v[92:95], a[212:215], a[200:203], v[92:95]\n\t"
"ds_read_b128 a[200:203], v244 offset:37888\n\t"
"v_mfma_f32_16x16x32_bf16 v[92:95], a[116:119], a[204:207], v[92:95]\n\t"
"ds_read_b128 a[204:207], v244 offset:38400\n\t"
"v_mfma_f32_16x16x32_bf16 v[100:103], a[216:219], a[168:171], v[100:103]\n\t"
"s_waitcnt lgkmcnt(8)\n\t"
"v_mfma_f32_16x16x32_bf16 v[100:103], a[124:127], a[172:175], v[100:103]\n\t"
"v_mfma_f32_16x16x32_bf16 v[100:103], a[132:135], a[176:179], v[100:103]\n\t"
"v_mfma_f32_16x16x32_bf16 v[104:107], a[216:219], a[172:175], v[104:107]\n\t"
"s_waitcnt lgkmcnt(6)\n\t"
"v_mfma_f32_16x16x32_bf16 v[104:107], a[124:127], a[176:179], v[104:107]\n\t"
"v_mfma_f32_16x16x32_bf16 v[104:107], a[132:135], a[180:183], v[104:107]\n\t"
"s_waitcnt lgkmcnt(0)\n\t"
"s_barrier\n\t"
"v_mfma_f32_16x16x32_bf16 v[108:111], a[216:219], a[176:179], v[108:111]\n\t"
"v_mfma_f32_16x16x32_bf16 v[108:111], a[124:127], a[180:183], v[108:111]\n\t"
"v_mfma_f32_16x16x32_bf16 v[108:111], a[132:135], a[184:187], v[108:111]\n\t"
"ds_write_b128 v249, a[8:11] offset:0\n\t"
"v_mfma_f32_16x16x32_bf16 v[112:115], a[216:219], a[180:183], v[112:115]\n\t"
"v_mfma_f32_16x16x32_bf16 v[112:115], a[124:127], a[184:187], v[112:115]\n\t"
"buffer_load_dwordx4 a[44:47], v252, s[8:11], s20 offen offset:2048 sc1\n\t"
"v_mfma_f32_16x16x32_bf16 v[112:115], a[132:135], a[188:191], v[112:115]\n\t"
"s_mov_b32 s20, 1069056\n\t"
"ds_write_b64 v250, a[136:137] offset:0\n\t"
"v_mfma_f32_16x16x32_bf16 v[116:119], a[216:219], a[184:187], v[116:119]\n\t"
"v_mfma_f32_16x16x32_bf16 v[116:119], a[124:127], a[188:191], v[116:119]\n\t"
"v_mfma_f32_16x16x32_bf16 v[116:119], a[132:135], a[192:195], v[116:119]\n\t"
"buffer_load_dwordx4 a[60:63], v252, s[8:11], s20 offen offset:0 sc1\n\t"
"v_mfma_f32_16x16x32_bf16 v[120:123], a[216:219], a[188:191], v[120:123]\n\t"
"v_mfma_f32_16x16x32_bf16 v[120:123], a[124:127], a[192:195], v[120:123]\n\t"
"v_mfma_f32_16x16x32_bf16 v[120:123], a[132:135], a[196:199], v[120:123]\n\t"
"ds_write_b128 v249, a[12:15] offset:6144\n\t"
"v_mfma_f32_16x16x32_bf16 v[124:127], a[216:219], a[192:195], v[124:127]\n\t"
"buffer_load_dwordx4 a[68:71], v252, s[8:11], s20 offen offset:1024 sc1\n\t"
"v_mfma_f32_16x16x32_bf16 v[124:127], a[124:127], a[196:199], v[124:127]\n\t"
"v_mfma_f32_16x16x32_bf16 v[124:127], a[132:135], a[200:203], v[124:127]\n\t"
"ds_write_b64 v250, a[138:139] offset:6144\n\t"
"v_mfma_f32_16x16x32_bf16 v[128:131], a[216:219], a[196:199], v[128:131]\n\t"
"s_waitcnt vmcnt(24)\n\t"
"v_mfma_f32_16x16x32_bf16 v[128:131], a[124:127], a[200:203], v[128:131]\n\t"
"buffer_load_dwordx4 a[76:79], v252, s[8:11], s20 offen offset:2048 sc1\n\t"
"v_mfma_f32_16x16x32_bf16 v[128:131], a[132:135], a[204:207], v[128:131]\n\t"
"s_mov_b32 s20, 1081344\n\t"
"v_mfma_f32_16x16x32_bf16 v[132:135], a[220:223], a[168:171], v[132:135]\n\t"
"v_mfma_f32_16x16x32_bf16 v[132:135], v[240:243], a[172:175], v[132:135]\n\t"
"v_mfma_f32_16x16x32_bf16 v[132:135], a[140:143], a[176:179], v[132:135]\n\t"
"buffer_load_dwordx4 a[132:135], v252, s[8:11], s20 offen offset:0 sc1\n\t"
"v_mfma_f32_16x16x32_bf16 v[136:139], a[220:223], a[172:175], v[136:139]\n\t"
"v_mfma_f32_16x16x32_bf16 v[136:139], v[240:243], a[176:179], v[136:139]\n\t"
"v_mfma_f32_16x16x32_bf16 v[136:139], a[140:143], a[180:183], v[136:139]\n\t"
"v_mfma_f32_16x16x32_bf16 v[140:143], a[220:223], a[176:179], v[140:143]\n\t"
"buffer_load_dwordx4 a[92:95], v252, s[8:11], s20 offen offset:1024 sc1\n\t"
"v_mfma_f32_16x16x32_bf16 v[140:143], v[240:243], a[180:183], v[140:143]\n\t"
"v_mfma_f32_16x16x32_bf16 v[140:143], a[140:143], a[184:187], v[140:143]\n\t"
"v_mfma_f32_16x16x32_bf16 v[144:147], a[220:223], a[180:183], v[144:147]\n\t"
"v_mfma_f32_16x16x32_bf16 v[144:147], v[240:243], a[184:187], v[144:147]\n\t"
"buffer_load_dwordx4 a[100:103], v252, s[8:11], s20 offen offset:2048 sc1\n\t"
"v_mfma_f32_16x16x32_bf16 v[144:147], a[140:143], a[188:191], v[144:147]\n\t"
"s_mov_b32 s20, 1093632\n\t"
"v_mfma_f32_16x16x32_bf16 v[148:151], a[220:223], a[184:187], v[148:151]\n\t"
"v_mfma_f32_16x16x32_bf16 v[148:151], v[240:243], a[188:191], v[148:151]\n\t"
"v_mfma_f32_16x16x32_bf16 v[148:151], a[140:143], a[192:195], v[148:151]\n\t"
"buffer_load_dwordx4 v[200:203], v252, s[8:11], s20 offen offset:0 sc1\n\t"
"v_mfma_f32_16x16x32_bf16 v[152:155], a[220:223], a[188:191], v[152:155]\n\t"
"ds_read_b128 a[116:119], v252 offset:12288\n\t"
"v_mfma_f32_16x16x32_bf16 v[152:155], v[240:243], a[192:195], v[152:155]\n\t"
"ds_read_b128 a[124:127], v252 offset:13312\n\t"
"v_mfma_f32_16x16x32_bf16 v[152:155], a[140:143], a[196:199], v[152:155]\n\t"
"v_mfma_f32_16x16x32_bf16 v[156:159], a[220:223], a[192:195], v[156:159]\n\t"
"buffer_load_dwordx4 a[120:123], v252, s[8:11], s20 offen offset:1024 sc1\n\t"
"v_mfma_f32_16x16x32_bf16 v[156:159], v[240:243], a[196:199], v[156:159]\n\t"
"ds_read_b128 a[156:159], v252 offset:14336\n\t"
"v_mfma_f32_16x16x32_bf16 v[156:159], a[140:143], a[200:203], v[156:159]\n\t"
"v_mfma_f32_16x16x32_bf16 v[160:163], a[220:223], a[196:199], v[160:163]\n\t"
"s_waitcnt vmcnt(27)\n\t"
"v_mfma_f32_16x16x32_bf16 v[160:163], v[240:243], a[200:203], v[160:163]\n\t"
"buffer_load_dwordx4 a[128:131], v252, s[8:11], s20 offen offset:2048 sc1\n\t"
"v_mfma_f32_16x16x32_bf16 v[160:163], a[140:143], a[204:207], v[160:163]\n\t"
"s_mov_b32 s20, 1179648\n\t"
"v_mfma_f32_16x16x32_bf16 v[164:167], a[148:151], a[168:171], v[164:167]\n\t"
"ds_read_b128 a[168:171], v255 offset:0\n\t"
"v_mfma_f32_16x16x32_bf16 v[164:167], a[224:227], a[172:175], v[164:167]\n\t"
"v_mfma_f32_16x16x32_bf16 v[164:167], a[228:231], a[176:179], v[164:167]\n\t"
"buffer_load_dwordx4 a[140:143], v252, s[8:11], s20 offen offset:0 sc1\n\t"
"v_mfma_f32_16x16x32_bf16 v[168:171], a[148:151], a[172:175], v[168:171]\n\t"
"ds_read_b128 a[172:175], v255 offset:3072\n\t"
"v_mfma_f32_16x16x32_bf16 v[168:171], a[224:227], a[176:179], v[168:171]\n\t"
"v_mfma_f32_16x16x32_bf16 v[168:171], a[228:231], a[180:183], v[168:171]\n\t"
"v_mfma_f32_16x16x32_bf16 v[172:175], a[148:151], a[176:179], v[172:175]\n\t"
"buffer_load_dwordx4 v[208:211], v252, s[8:11], s20 offen offset:1024 sc1\n\t"
"v_mfma_f32_16x16x32_bf16 v[172:175], a[224:227], a[180:183], v[172:175]\n\t"
"ds_read_b128 a[176:179], v255 offset:6144\n\t"
"v_mfma_f32_16x16x32_bf16 v[172:175], a[228:231], a[184:187], v[172:175]\n\t"
"v_mfma_f32_16x16x32_bf16 v[176:179], a[148:151], a[180:183], v[176:179]\n\t"
"buffer_load_dwordx4 v[216:219], v252, s[8:11], s20 offen offset:2048 sc1\n\t"
"v_mfma_f32_16x16x32_bf16 v[176:179], a[224:227], a[184:187], v[176:179]\n\t"
"s_mov_b32 s20, 1191936\n\t"
"ds_read_b128 a[180:183], v255 offset:9216\n\t"
"v_mfma_f32_16x16x32_bf16 v[176:179], a[228:231], a[188:191], v[176:179]\n\t"
"s_waitcnt lgkmcnt(4)\n\t"
"s_barrier\n\t"
"v_mfma_f32_16x16x32_bf16 v[180:183], a[148:151], a[184:187], v[180:183]\n\t"
"ds_read_b128 a[184:187], v255 offset:12288\n\t"
"v_mfma_f32_16x16x32_bf16 v[180:183], a[224:227], a[188:191], v[180:183]\n\t"
"buffer_load_dwordx4 a[4:7], v252, s[8:11], s20 offen offset:0 sc1\n\t"
"v_mfma_f32_16x16x32_bf16 v[180:183], a[228:231], a[192:195], v[180:183]\n\t"
"s_waitcnt vmcnt(29) lgkmcnt(2)\n\t"
"ds_write_b128 v249, a[16:19] offset:12288\n\t"
"v_mfma_f32_16x16x32_bf16 v[184:187], a[148:151], a[188:191], v[184:187]\n\t"
"Lpg12_%=:\n\t"
"s_cbranch_vccnz Lvr0_%=\n\t"
"v_mfma_f32_16x16x32_bf16 v[184:187], a[224:227], a[192:195], v[184:187]\n\t"
"buffer_load_dwordx4 a[12:15], v252, s[8:11], s20 offen offset:1024 sc1\n\t"
"v_mfma_f32_16x16x32_bf16 v[184:187], a[228:231], a[196:199], v[184:187]\n\t"
"ds_read_b128 a[188:191], v255 offset:15360\n\t"
"v_mfma_f32_16x16x32_bf16 v[188:191], a[148:151], a[192:195], v[188:191]\n\t"
"ds_read_b128 a[192:195], v255 offset:18432\n\t"
"v_mfma_f32_16x16x32_bf16 v[188:191], a[224:227], a[196:199], v[188:191]\n\t"
"buffer_load_dwordx4 a[208:211], v252, s[8:11], s20 offen offset:2048 sc1\n\t"
"v_mfma_f32_16x16x32_bf16 v[188:191], a[228:231], a[200:203], v[188:191]\n\t"
"s_mov_b32 s20, 1204224\n\t"
"ds_write_b64 v250, a[144:145] offset:12288\n\t"
"v_mfma_f32_16x16x32_bf16 v[192:195], a[148:151], a[196:199], v[192:195]\n\t"
"ds_read_b128 a[196:199], v255 offset:21504\n\t"
"v_mfma_f32_16x16x32_bf16 v[192:195], a[224:227], a[200:203], v[192:195]\n\t"
"ds_read_b128 a[200:203], v255 offset:24576\n\t"
"v_mfma_f32_16x16x32_bf16 v[192:195], a[228:231], a[204:207], v[192:195]\n\t"
"buffer_load_dwordx4 a[212:215], v252, s[8:11], s20 offen offset:0 sc1\n\t"
"Lst16_%=:\n\t"
"v_mfma_f32_16x16x32_bf16 v[0:3], a[232:235], a[168:171], v[0:3]\n\t"
"s_waitcnt lgkmcnt(7)\n\t"
"ds_read_b128 a[204:207], v255 offset:27648\n\t"
"v_mfma_f32_16x16x32_bf16 v[0:3], a[164:167], a[172:175], v[0:3]\n\t"
"v_mfma_f32_16x16x32_bf16 v[0:3], a[248:251], a[176:179], v[0:3]\n\t"
"ds_write_b128 v249, a[20:23] offset:18432\n\t"
"v_mfma_f32_16x16x32_bf16 v[4:7], a[232:235], a[172:175], v[4:7]\n\t"
"buffer_load_dwordx4 a[216:219], v252, s[8:11], s20 offen offset:1024 sc1\n\t"
"v_mfma_f32_16x16x32_bf16 v[4:7], a[164:167], a[176:179], v[4:7]\n\t"
"s_waitcnt lgkmcnt(8)\n\t"
"v_mfma_f32_16x16x32_bf16 v[4:7], a[248:251], a[180:183], v[4:7]\n\t"
"ds_write_b64 v250, a[146:147] offset:18432\n\t"
"v_mfma_f32_16x16x32_bf16 v[8:11], a[232:235], a[176:179], v[8:11]\n\t"
"s_waitcnt lgkmcnt(7)\n\t"
"v_mfma_f32_16x16x32_bf16 v[8:11], a[164:167], a[180:183], v[8:11]\n\t"
"buffer_load_dwordx4 a[220:223], v252, s[8:11], s20 offen offset:2048 sc1\n\t"
"v_mfma_f32_16x16x32_bf16 v[8:11], a[248:251], a[184:187], v[8:11]\n\t"
"s_mov_b32 s20, 1363968\n\t"
"v_mfma_f32_16x16x32_bf16 v[12:15], a[232:235], a[180:183], v[12:15]\n\t"
"s_waitcnt lgkmcnt(6)\n\t"
"v_mfma_f32_16x16x32_bf16 v[12:15], a[164:167], a[184:187], v[12:15]\n\t"
"v_mfma_f32_16x16x32_bf16 v[12:15], a[248:251], a[188:191], v[12:15]\n\t"
"buffer_load_dwordx4 a[224:227], v252, s[8:11], s20 offen offset:0 sc1\n\t"
"v_mfma_f32_16x16x32_bf16 v[16:19], a[232:235], a[184:187], v[16:19]\n\t"
"s_waitcnt lgkmcnt(4)\n\t"
"v_mfma_f32_16x16x32_bf16 v[16:19], a[164:167], a[188:191], v[16:19]\n\t"
"v_mfma_f32_16x16x32_bf16 v[16:19], a[248:251], a[192:195], v[16:19]\n\t"
"ds_read_b128 a[52:55], v252 offset:0\n\t"
"v_mfma_f32_16x16x32_bf16 v[20:23], a[232:235], a[188:191], v[20:23]\n\t"
"buffer_load_dwordx4 a[228:231], v252, s[8:11], s20 offen offset:1024 sc1\n\t"
"v_mfma_f32_16x16x32_bf16 v[20:23], a[164:167], a[192:195], v[20:23]\n\t"
"s_waitcnt lgkmcnt(4)\n\t"
"ds_read_b128 a[84:87], v252 offset:1024\n\t"
"v_mfma_f32_16x16x32_bf16 v[20:23], a[248:251], a[196:199], v[20:23]\n\t"
"v_mfma_f32_16x16x32_bf16 v[24:27], a[232:235], a[192:195], v[24:27]\n\t"
"s_waitcnt lgkmcnt(4)\n\t"
"ds_read_b128 v[196:199], v252 offset:2048\n\t"
"v_mfma_f32_16x16x32_bf16 v[24:27], a[164:167], a[196:199], v[24:27]\n\t"
"buffer_load_dwordx4 a[240:243], v252, s[8:11], s20 offen offset:2048 sc1\n\t"
"v_mfma_f32_16x16x32_bf16 v[24:27], a[248:251], a[200:203], v[24:27]\n\t"
"s_mov_b32 s20, 1376256\n\t"
"v_mfma_f32_16x16x32_bf16 v[28:31], a[232:235], a[196:199], v[28:31]\n\t"
"s_waitcnt vmcnt(33)\n\t"
"v_mfma_f32_16x16x32_bf16 v[28:31], a[164:167], a[200:203], v[28:31]\n\t"
"v_mfma_f32_16x16x32_bf16 v[28:31], a[248:251], a[204:207], v[28:31]\n\t"
"buffer_load_dwordx4 a[232:235], v252, s[8:11], s20 offen offset:0 sc1\n\t"
"v_mfma_f32_16x16x32_bf16 v[32:35], v[236:239], a[168:171], v[32:35]\n\t"
"v_mfma_f32_16x16x32_bf16 v[32:35], v[96:99], a[172:175], v[32:35]\n\t"
"v_mfma_f32_16x16x32_bf16 v[32:35], a[236:239], a[176:179], v[32:35]\n\t"
"buffer_load_dwordx4 a[244:247], v252, s[8:11], s20 offen offset:1024 sc1\n\t"
"v_mfma_f32_16x16x32_bf16 v[36:39], v[236:239], a[172:175], v[36:39]\n\t"
"v_mfma_f32_16x16x32_bf16 v[36:39], v[96:99], a[176:179], v[36:39]\n\t"
"v_mfma_f32_16x16x32_bf16 v[36:39], a[236:239], a[180:183], v[36:39]\n\t"
"v_mfma_f32_16x16x32_bf16 v[40:43], v[236:239], a[176:179], v[40:43]\n\t"
"buffer_load_dwordx4 a[248:251], v252, s[8:11], s20 offen offset:2048 sc1\n\t"
"v_mfma_f32_16x16x32_bf16 v[40:43], v[96:99], a[180:183], v[40:43]\n\t"
"s_mov_b32 s20, 1388544\n\t"
"v_mfma_f32_16x16x32_bf16 v[40:43], a[236:239], a[184:187], v[40:43]\n\t"
"v_mfma_f32_16x16x32_bf16 v[44:47], v[236:239], a[180:183], v[44:47]\n\t"
"v_mfma_f32_16x16x32_bf16 v[44:47], v[96:99], a[184:187], v[44:47]\n\t"
"v_mfma_f32_16x16x32_bf16 v[44:47], a[236:239], a[188:191], v[44:47]\n\t"
"s_waitcnt lgkmcnt(0)\n\t"
"s_barrier\n\t"
"v_mfma_f32_16x16x32_bf16 v[48:51], v[236:239], a[184:187], v[48:51]\n\t"
"v_mfma_f32_16x16x32_bf16 v[48:51], v[96:99], a[188:191], v[48:51]\n\t"
"buffer_load_dwordx4 a[252:255], v252, s[8:11], s20 offen offset:0 sc1\n\t"
"v_mfma_f32_16x16x32_bf16 v[48:51], a[236:239], a[192:195], v[48:51]\n\t"
"buffer_load_dwordx4 a[144:147], v252, s[8:11], s20 offen offset:1024 sc1\n\t"
"v_mfma_f32_16x16x32_bf16 v[52:55], v[236:239], a[188:191], v[52:55]\n\t"
"v_mfma_f32_16x16x32_bf16 v[52:55], v[96:99], a[192:195], v[52:55]\n\t"
"buffer_load_dwordx4 a[20:23], v252, s[8:11], s20 offen offset:2048 sc1\n\t"
"v_mfma_f32_16x16x32_bf16 v[52:55], a[236:239], a[196:199], v[52:55]\n\t"
"s_mov_b32 s20, 0\n\t"
"ds_write_b128 v249, a[24:27] offset:0\n\t"
"v_mfma_f32_16x16x32_bf16 v[56:59], v[236:239], a[192:195], v[56:59]\n\t"
"v_mfma_f32_16x16x32_bf16 v[56:59], v[96:99], a[196:199], v[56:59]\n\t"
"v_mfma_f32_16x16x32_bf16 v[56:59], a[236:239], a[200:203], v[56:59]\n\t"
"ds_write_b64 v250, a[152:153] offset:0\n\t"
"v_mfma_f32_16x16x32_bf16 v[60:63], v[236:239], a[196:199], v[60:63]\n\t"
"s_waitcnt vmcnt(27)\n\t"
"v_mfma_f32_16x16x32_bf16 v[60:63], v[96:99], a[200:203], v[60:63]\n\t"
"buffer_load_dwordx4 v[96:99], v252, s[8:11], s20 offen offset:0 sc1\n\t"
"v_mfma_f32_16x16x32_bf16 v[60:63], a[236:239], a[204:207], v[60:63]\n\t"
"ds_write_b128 v249, a[28:31] offset:6144\n\t"
"v_mfma_f32_16x16x32_bf16 v[64:67], a[104:107], a[168:171], v[64:67]\n\t"
"v_mfma_f32_16x16x32_bf16 v[64:67], a[112:115], a[172:175], v[64:67]\n\t"
"buffer_load_dwordx4 a[236:239], v252, s[8:11], s20 offen offset:1024 sc1\n\t"
"v_mfma_f32_16x16x32_bf16 v[64:67], a[44:47], a[176:179], v[64:67]\n\t"
"ds_read_b128 a[168:171], v255 offset:1536\n\t"
"v_mfma_f32_16x16x32_bf16 v[68:71], a[104:107], a[172:175], v[68:71]\n\t"
"ds_read_b128 a[172:175], v255 offset:4608\n\t"
"v_mfma_f32_16x16x32_bf16 v[68:71], a[112:115], a[176:179], v[68:71]\n\t"
"v_mfma_f32_16x16x32_bf16 v[68:71], a[44:47], a[180:183], v[68:71]\n\t"
"ds_write_b64 v250, a[154:155] offset:6144\n\t"
"v_mfma_f32_16x16x32_bf16 v[72:75], a[104:107], a[176:179], v[72:75]\n\t"
"ds_read_b128 a[176:179], v255 offset:7680\n\t"
"v_mfma_f32_16x16x32_bf16 v[72:75], a[112:115], a[180:183], v[72:75]\n\t"
"v_mfma_f32_16x16x32_bf16 v[72:75], a[44:47], a[184:187], v[72:75]\n\t"
"ds_read_b128 v[204:207], v252 offset:12288\n\t"
"v_mfma_f32_16x16x32_bf16 v[76:79], a[104:107], a[180:183], v[76:79]\n\t"
"ds_read_b128 a[180:183], v255 offset:10752\n\t"
"v_mfma_f32_16x16x32_bf16 v[76:79], a[112:115], a[184:187], v[76:79]\n\t"
"ds_read_b128 v[212:215], v252 offset:13312\n\t"
"v_mfma_f32_16x16x32_bf16 v[76:79], a[44:47], a[188:191], v[76:79]\n\t"
"v_mfma_f32_16x16x32_bf16 v[80:83], a[104:107], a[184:187], v[80:83]\n\t"
"ds_read_b128 a[184:187], v255 offset:13824\n\t"
"v_mfma_f32_16x16x32_bf16 v[80:83], a[112:115], a[188:191], v[80:83]\n\t"
"ds_read_b128 a[0:3], v252 offset:14336\n\t"
"v_mfma_f32_16x16x32_bf16 v[80:83], a[44:47], a[192:195], v[80:83]\n\t"
"v_mfma_f32_16x16x32_bf16 v[84:87], a[104:107], a[188:191], v[84:87]\n\t"
"ds_read_b128 a[188:191], v255 offset:16896\n\t"
"v_mfma_f32_16x16x32_bf16 v[84:87], a[112:115], a[192:195], v[84:87]\n\t"
"v_mfma_f32_16x16x32_bf16 v[84:87], a[44:47], a[196:199], v[84:87]\n\t"
"v_mfma_f32_16x16x32_bf16 v[88:91], a[104:107], a[192:195], v[88:91]\n\t"
"s_add_u32 s29, s22, 384\n\t"
"ds_read_b128 a[192:195], v255 offset:19968\n\t"
"v_mfma_f32_16x16x32_bf16 v[88:91], a[112:115], a[196:199], v[88:91]\n\t"
"v_mfma_f32_16x16x32_bf16 v[88:91], a[44:47], a[200:203], v[88:91]\n\t"
"v_mfma_f32_16x16x32_bf16 v[92:95], a[104:107], a[196:199], v[92:95]\n\t"
"s_waitcnt vmcnt(26) lgkmcnt(7)\n\t"
"ds_read_b128 a[196:199], v255 offset:23040\n\t"
"v_mfma_f32_16x16x32_bf16 v[92:95], a[112:115], a[200:203], v[92:95]\n\t"
"ds_read_b128 a[200:203], v255 offset:26112\n\t"
"v_mfma_f32_16x16x32_bf16 v[92:95], a[44:47], a[204:207], v[92:95]\n\t"
"ds_read_b128 a[204:207], v255 offset:29184\n\t"
"v_mfma_f32_16x16x32_bf16 v[100:103], a[60:63], a[168:171], v[100:103]\n\t"
"s_waitcnt lgkmcnt(8)\n\t"
"v_mfma_f32_16x16x32_bf16 v[100:103], a[68:71], a[172:175], v[100:103]\n\t"
"v_mfma_f32_16x16x32_bf16 v[100:103], a[76:79], a[176:179], v[100:103]\n\t"
"buffer_store_dwordx4 a[40:43], v248, s[16:19], s29 offen offset:0 nt\n\t"
"v_mfma_f32_16x16x32_bf16 v[104:107], a[60:63], a[172:175], v[104:107]\n\t"
"s_waitcnt lgkmcnt(6)\n\t"
"v_mfma_f32_16x16x32_bf16 v[104:107], a[68:71], a[176:179], v[104:107]\n\t"
"v_mfma_f32_16x16x32_bf16 v[104:107], a[76:79], a[180:183], v[104:107]\n\t"
"s_waitcnt lgkmcnt(0)\n\t"
"s_barrier\n\t"
"v_mfma_f32_16x16x32_bf16 v[108:111], a[60:63], a[176:179], v[108:111]\n\t"
"v_mfma_f32_16x16x32_bf16 v[108:111], a[68:71], a[180:183], v[108:111]\n\t"
"buffer_load_dwordx4 a[152:155], v252, s[8:11], s20 offen offset:2048 sc1\n\t"
"v_mfma_f32_16x16x32_bf16 v[108:111], a[76:79], a[184:187], v[108:111]\n\t"
"s_mov_b32 s20, 12288\n\t"
"ds_write_b128 v249, a[32:35] offset:12288\n\t"
"v_mfma_f32_16x16x32_bf16 v[112:115], a[60:63], a[180:183], v[112:115]\n\t"
"v_mfma_f32_16x16x32_bf16 v[112:115], a[68:71], a[184:187], v[112:115]\n\t"
"buffer_load_dwordx4 a[28:31], v252, s[8:11], s20 offen offset:0 sc1\n\t"
"v_mfma_f32_16x16x32_bf16 v[112:115], a[76:79], a[188:191], v[112:115]\n\t"
"ds_write_b64 v250, a[160:161] offset:12288\n\t"
"v_mfma_f32_16x16x32_bf16 v[116:119], a[60:63], a[184:187], v[116:119]\n\t"
"v_mfma_f32_16x16x32_bf16 v[116:119], a[68:71], a[188:191], v[116:119]\n\t"
"v_mfma_f32_16x16x32_bf16 v[116:119], a[76:79], a[192:195], v[116:119]\n\t"
"buffer_store_dwordx4 a[48:51], v248, s[16:19], s29 offen offset:128 nt\n\t"
"ds_write_b64 v250, a[162:163] offset:18432\n\t"
"v_mfma_f32_16x16x32_bf16 v[120:123], a[60:63], a[188:191], v[120:123]\n\t"
"v_mfma_f32_16x16x32_bf16 v[120:123], a[68:71], a[192:195], v[120:123]\n\t"
"v_mfma_f32_16x16x32_bf16 v[120:123], a[76:79], a[196:199], v[120:123]\n\t"
"ds_write_b128 v249, a[36:39] offset:18432\n\t"
"v_mfma_f32_16x16x32_bf16 v[124:127], a[60:63], a[192:195], v[124:127]\n\t"
"v_mfma_f32_16x16x32_bf16 v[124:127], a[68:71], a[196:199], v[124:127]\n\t"
"v_mfma_f32_16x16x32_bf16 v[124:127], a[76:79], a[200:203], v[124:127]\n\t"
"v_mfma_f32_16x16x32_bf16 v[128:131], a[60:63], a[196:199], v[128:131]\n\t"
"s_waitcnt vmcnt(27)\n\t"
"v_mfma_f32_16x16x32_bf16 v[128:131], a[68:71], a[200:203], v[128:131]\n\t"
"v_mfma_f32_16x16x32_bf16 v[128:131], a[76:79], a[204:207], v[128:131]\n\t"
"v_mfma_f32_16x16x32_bf16 v[132:135], a[132:135], a[168:171], v[132:135]\n\t"
"v_mfma_f32_16x16x32_bf16 v[132:135], a[92:95], a[172:175], v[132:135]\n\t"
"v_mfma_f32_16x16x32_bf16 v[132:135], a[100:103], a[176:179], v[132:135]\n\t"
"buffer_store_dwordx4 a[56:59], v248, s[16:19], s29 offen offset:256 nt\n\t"
"v_mfma_f32_16x16x32_bf16 v[136:139], a[132:135], a[172:175], v[136:139]\n\t"
"v_mfma_f32_16x16x32_bf16 v[136:139], a[92:95], a[176:179], v[136:139]\n\t"
"v_add_u32 v255, 55296, v255\n\t"
"v_mfma_f32_16x16x32_bf16 v[136:139], a[100:103], a[180:183], v[136:139]\n\t"
"v_mfma_f32_16x16x32_bf16 v[140:143], a[132:135], a[176:179], v[140:143]\n\t"
"v_mfma_f32_16x16x32_bf16 v[140:143], a[92:95], a[180:183], v[140:143]\n\t"
"v_mfma_f32_16x16x32_bf16 v[140:143], a[100:103], a[184:187], v[140:143]\n\t"
"s_add_u32 s29, s22, 196992\n\t"
"v_mfma_f32_16x16x32_bf16 v[144:147], a[132:135], a[180:183], v[144:147]\n\t"
"v_mfma_f32_16x16x32_bf16 v[144:147], a[92:95], a[184:187], v[144:147]\n\t"
"global_load_dwordx4 v[240:243], v252, s[48:49] offset:2048\n\t"
"v_mfma_f32_16x16x32_bf16 v[144:147], a[100:103], a[188:191], v[144:147]\n\t"
"v_mfma_f32_16x16x32_bf16 v[148:151], a[132:135], a[184:187], v[148:151]\n\t"
"v_mfma_f32_16x16x32_bf16 v[148:151], a[92:95], a[188:191], v[148:151]\n\t"
"v_mfma_f32_16x16x32_bf16 v[148:151], a[100:103], a[192:195], v[148:151]\n\t"
"buffer_store_dwordx4 a[64:67], v248, s[16:19], s29 offen offset:0 nt\n\t"
"ds_read_b128 a[8:11], v252 offset:0\n\t"
"v_mfma_f32_16x16x32_bf16 v[152:155], a[132:135], a[188:191], v[152:155]\n\t"
"v_mfma_f32_16x16x32_bf16 v[152:155], a[92:95], a[192:195], v[152:155]\n\t"
"global_load_dwordx4 a[136:139], v252, s[46:47] offset:1024\n\t"
"v_mfma_f32_16x16x32_bf16 v[152:155], a[100:103], a[196:199], v[152:155]\n\t"
"ds_read_b128 a[16:19], v252 offset:1024\n\t"
"v_mfma_f32_16x16x32_bf16 v[156:159], a[132:135], a[192:195], v[156:159]\n\t"
"ds_read_b128 a[24:27], v252 offset:2048\n\t"
"v_mfma_f32_16x16x32_bf16 v[156:159], a[92:95], a[196:199], v[156:159]\n\t"
"v_mfma_f32_16x16x32_bf16 v[156:159], a[100:103], a[200:203], v[156:159]\n\t"
"v_mfma_f32_16x16x32_bf16 v[160:163], a[132:135], a[196:199], v[160:163]\n\t"
"s_waitcnt vmcnt(28)\n\t"
"v_mfma_f32_16x16x32_bf16 v[160:163], a[92:95], a[200:203], v[160:163]\n\t"
"v_mfma_f32_16x16x32_bf16 v[160:163], a[100:103], a[204:207], v[160:163]\n\t"
"v_mfma_f32_16x16x32_bf16 v[164:167], v[200:203], a[168:171], v[164:167]\n\t"
"ds_read_b128 a[168:171], v255 offset:0\n\t"
"v_mfma_f32_16x16x32_bf16 v[164:167], a[120:123], a[172:175], v[164:167]\n\t"
"v_mfma_f32_16x16x32_bf16 v[164:167], a[128:131], a[176:179], v[164:167]\n\t"
"buffer_store_dwordx4 a[72:75], v248, s[16:19], s29 offen offset:128 nt\n\t"
"v_mfma_f32_16x16x32_bf16 v[168:171], v[200:203], a[172:175], v[168:171]\n\t"
"ds_read_b128 a[172:175], v255 offset:3072\n\t"
"v_mfma_f32_16x16x32_bf16 v[168:171], a[120:123], a[176:179], v[168:171]\n\t"
"v_mfma_f32_16x16x32_bf16 v[168:171], a[128:131], a[180:183], v[168:171]\n\t"
"v_mfma_f32_16x16x32_bf16 v[172:175], v[200:203], a[176:179], v[172:175]\n\t"
"ds_read_b128 a[176:179], v255 offset:6144\n\t"
"v_mfma_f32_16x16x32_bf16 v[172:175], a[120:123], a[180:183], v[172:175]\n\t"
"v_mfma_f32_16x16x32_bf16 v[172:175], a[128:131], a[184:187], v[172:175]\n\t"
"v_mfma_f32_16x16x32_bf16 v[176:179], v[200:203], a[180:183], v[176:179]\n\t"
"ds_read_b128 a[180:183], v255 offset:9216\n\t"
"v_mfma_f32_16x16x32_bf16 v[176:179], a[120:123], a[184:187], v[176:179]\n\t"
"v_mfma_f32_16x16x32_bf16 v[176:179], a[128:131], a[188:191], v[176:179]\n\t"
"s_waitcnt lgkmcnt(4)\n\t"
"s_barrier\n\t"
"v_mfma_f32_16x16x32_bf16 v[180:183], v[200:203], a[184:187], v[180:183]\n\t"
"ds_read_b128 a[184:187], v255 offset:12288\n\t"
"v_mfma_f32_16x16x32_bf16 v[180:183], a[120:123], a[188:191], v[180:183]\n\t"
"buffer_load_dwordx4 a[160:163], v252, s[8:11], s20 offen offset:1024 sc1\n\t"
"v_mfma_f32_16x16x32_bf16 v[180:183], a[128:131], a[192:195], v[180:183]\n\t"
"v_mfma_f32_16x16x32_bf16 v[184:187], v[200:203], a[188:191], v[184:187]\n\t"
"ds_read_b128 a[188:191], v255 offset:15360\n\t"
"v_mfma_f32_16x16x32_bf16 v[184:187], a[120:123], a[192:195], v[184:187]\n\t"
"buffer_load_dwordx4 a[36:39], v252, s[8:11], s20 offen offset:2048 sc1\n\t"
"v_mfma_f32_16x16x32_bf16 v[184:187], a[128:131], a[196:199], v[184:187]\n\t"
"buffer_store_dwordx4 a[80:83], v248, s[16:19], s29 offen offset:256 nt\n\t"
"s_waitcnt vmcnt(6)\n\t"
"v_add_u32 v240, 59904, v240\n\t"
"v_add_u32 v241, 59904, v241\n\t"
"s_mov_b32 s20, 24576\n\t"
"v_mfma_f32_16x16x32_bf16 v[188:191], v[200:203], a[192:195], v[188:191]\n\t"
"ds_read_b128 a[192:195], v255 offset:18432\n\t"
"v_mfma_f32_16x16x32_bf16 v[188:191], a[120:123], a[196:199], v[188:191]\n\t"
"v_mfma_f32_16x16x32_bf16 v[188:191], a[128:131], a[200:203], v[188:191]\n\t"
"ds_read_b64_tr_b16 v[236:237], v240 offset:20480\n\t"
"v_mfma_f32_16x16x32_bf16 v[192:195], v[200:203], a[196:199], v[192:195]\n\t"
"buffer_load_dwordx4 v[200:203], v252, s[8:11], s20 offen offset:0 sc1\n\t"
"v_mfma_f32_16x16x32_bf16 v[192:195], a[120:123], a[200:203], v[192:195]\n\t"
"s_waitcnt lgkmcnt(5)\n\t"
"ds_read_b128 a[196:199], v255 offset:21504\n\t"
"s_add_u32 s29, s22, 393600\n\t"
"v_mfma_f32_16x16x32_bf16 v[192:195], a[128:131], a[204:207], v[192:195]\n\t"
"ds_read_b64_tr_b16 v[238:239], v240 offset:22016\n\t"
"Lst17_%=:\n\t"
"v_mfma_f32_16x16x32_bf16 v[0:3], a[140:143], a[168:171], v[0:3]\n\t"
"buffer_load_dwordx4 a[120:123], v252, s[8:11], s20 offen offset:1024 sc1\n\t"
"v_mfma_f32_16x16x32_bf16 v[0:3], v[208:211], a[172:175], v[0:3]\n\t"
"s_waitcnt lgkmcnt(6)\n\t"
"ds_read_b64_tr_b16 v[224:225], v241 offset:20480\n\t"
"v_mfma_f32_16x16x32_bf16 v[0:3], v[216:219], a[176:179], v[0:3]\n\t"
"ds_read_b64_tr_b16 v[226:227], v241 offset:22016\n\t"
"v_mfma_f32_16x16x32_bf16 v[4:7], a[140:143], a[172:175], v[4:7]\n\t"
"s_waitcnt lgkmcnt(7)\n\t"
"ds_read_b128 a[200:203], v255 offset:24576\n\t"
"v_mfma_f32_16x16x32_bf16 v[4:7], v[208:211], a[176:179], v[4:7]\n\t"
"buffer_load_dwordx4 a[128:131], v252, s[8:11], s20 offen offset:2048 sc1\n\t"
"v_mfma_f32_16x16x32_bf16 v[4:7], v[216:219], a[180:183], v[4:7]\n\t"
"ds_read_b128 a[204:207], v255 offset:27648\n\t"
"v_mfma_f32_16x16x32_bf16 v[8:11], a[140:143], a[176:179], v[8:11]\n\t"
"s_waitcnt lgkmcnt(8)\n\t"
"v_cvt_pk_bf16_f32 v0, v0, v1\n\t"
"v_cvt_pk_bf16_f32 v2, v2, v3\n\t"
"v_mfma_f32_16x16x32_bf16 v[8:11], v[208:211], a[180:183], v[8:11]\n\t"
"v_accvgpr_write_b32 a40, v0\n\t"
"v_accvgpr_write_b32 a41, v2\n\t"
"v_mfma_f32_16x16x32_bf16 v[8:11], v[216:219], a[184:187], v[8:11]\n\t"
"v_mfma_f32_16x16x32_bf16 v[12:15], a[140:143], a[180:183], v[12:15]\n\t"
"s_waitcnt vmcnt(7) lgkmcnt(4)\n\t"
"v_cvt_pk_bf16_f32 v4, v4, v5\n\t"
"v_cvt_pk_bf16_f32 v6, v6, v7\n\t"
"v_mfma_f32_16x16x32_bf16 v[12:15], v[208:211], a[184:187], v[12:15]\n\t"
"s_waitcnt lgkmcnt(2)\n\t"
"v_accvgpr_write_b32 a48, v4\n\t"
"v_accvgpr_write_b32 a49, v6\n\t"
"v_mfma_f32_16x16x32_bf16 v[12:15], v[216:219], a[188:191], v[12:15]\n\t"
"buffer_store_dwordx4 a[88:91], v248, s[16:19], s29 offen offset:0 nt\n\t"
"v_mfma_f32_16x16x32_bf16 v[236:239], v[236:239], a[136:139], 0\n\t"
"v_cvt_pk_bf16_f32 v8, v8, v9\n\t"
"v_cvt_pk_bf16_f32 v10, v10, v11\n\t"
"v_mfma_f32_16x16x32_bf16 v[224:227], v[224:227], a[136:139], 0\n\t"
"v_accvgpr_write_b32 a56, v8\n\t"
"v_accvgpr_write_b32 a57, v10\n\t"
"v_mfma_f32_16x16x32_bf16 v[16:19], a[140:143], a[184:187], v[16:19]\n\t"
"v_mfma_f32_16x16x32_bf16 v[16:19], v[208:211], a[188:191], v[16:19]\n\t"
"v_cvt_pk_bf16_f32 v12, v12, v13\n\t"
"v_cvt_pk_bf16_f32 v14, v14, v15\n\t"
"v_mfma_f32_16x16x32_bf16 v[16:19], v[216:219], a[192:195], v[16:19]\n\t"
"ds_read_b64_tr_b16 v[232:233], v240 offset:40448\n\t"
"v_accvgpr_write_b32 a64, v12\n\t"
"v_mfma_f32_16x16x32_bf16 v[20:23], a[140:143], a[188:191], v[20:23]\n\t"
"s_waitcnt lgkmcnt(2)\n\t"
"ds_read_b64_tr_b16 v[234:235], v240 offset:41984\n\t"
"v_cvt_pk_bf16_f32 v236, v236, v237\n\t"
"v_mfma_f32_16x16x32_bf16 v[20:23], v[208:211], a[192:195], v[20:23]\n\t"
"s_mov_b32 s20, 184320\n\t"
"ds_read_b64_tr_b16 v[228:229], v241 offset:40448\n\t"
"v_cvt_pk_bf16_f32 v237, v238, v239\n\t"
"v_mfma_f32_16x16x32_bf16 v[20:23], v[216:219], a[196:199], v[20:23]\n\t"
"ds_read_b64_tr_b16 v[230:231], v241 offset:41984\n\t"
"v_cvt_pk_bf16_f32 v238, v224, v225\n\t"
"v_mfma_f32_16x16x32_bf16 v[24:27], a[140:143], a[192:195], v[24:27]\n\t"
"s_waitcnt lgkmcnt(4)\n\t"
"ds_read_b128 a[32:35], v252 offset:12288\n\t"
"v_cvt_pk_bf16_f32 v239, v226, v227\n\t"
"v_mfma_f32_16x16x32_bf16 v[24:27], v[208:211], a[196:199], v[24:27]\n\t"
"ds_read_b128 a[104:107], v252 offset:13312\n\t"
"v_accvgpr_write_b32 a65, v14\n\t"
"v_mfma_f32_16x16x32_bf16 v[24:27], v[216:219], a[200:203], v[24:27]\n\t"
"buffer_store_dwordx4 a[96:99], v248, s[16:19], s29 offen offset:128 nt\n\t"
"ds_read_b128 a[112:115], v252 offset:14336\n\t"
"v_cvt_pk_bf16_f32 v16, v16, v17\n\t"
"v_cvt_pk_bf16_f32 v18, v18, v19\n\t"
"v_accvgpr_write_b32 a72, v16\n\t"
"v_mfma_f32_16x16x32_bf16 v[28:31], a[140:143], a[196:199], v[28:31]\n\t"
"v_cvt_pk_bf16_f32 v20, v20, v21\n\t"
"v_cvt_pk_bf16_f32 v22, v22, v23\n\t"
"v_mfma_f32_16x16x32_bf16 v[28:31], v[208:211], a[200:203], v[28:31]\n\t"
"v_accvgpr_write_b32 a73, v18\n\t"
"v_accvgpr_write_b32 a80, v20\n\t"
"v_mfma_f32_16x16x32_bf16 v[28:31], v[216:219], a[204:207], v[28:31]\n\t"
"ds_write_b128 v242, v[236:239] offset:30720\n\t"
"v_mfma_f32_16x16x32_bf16 v[32:35], a[4:7], a[168:171], v[32:35]\n\t"
"buffer_load_dwordx4 v[208:211], v252, s[8:11], s20 offen offset:0 sc1\n\t"
"v_mfma_f32_16x16x32_bf16 v[32:35], a[12:15], a[172:175], v[32:35]\n\t"
"buffer_load_dwordx4 v[216:219], v252, s[8:11], s20 offen offset:1024 sc1\n\t"
"v_mfma_f32_16x16x32_bf16 v[32:35], a[208:211], a[176:179], v[32:35]\n\t"
"v_cvt_pk_bf16_f32 v24, v24, v25\n\t"
"v_cvt_pk_bf16_f32 v26, v26, v27\n\t"
"v_mfma_f32_16x16x32_bf16 v[36:39], a[4:7], a[172:175], v[36:39]\n\t"
"s_waitcnt lgkmcnt(6)\n\t"
"v_accvgpr_write_b32 a81, v22\n\t"
"v_cvt_pk_bf16_f32 v28, v28, v29\n\t"
"v_mfma_f32_16x16x32_bf16 v[36:39], a[12:15], a[176:179], v[36:39]\n\t"
"s_waitcnt lgkmcnt(4)\n\t"
"v_cvt_pk_bf16_f32 v30, v30, v31\n\t"
"v_accvgpr_write_b32 a88, v24\n\t"
"v_mfma_f32_16x16x32_bf16 v[36:39], a[208:211], a[180:183], v[36:39]\n\t"
"v_accvgpr_write_b32 a89, v26\n\t"
"v_accvgpr_write_b32 a96, v28\n\t"
"v_mfma_f32_16x16x32_bf16 v[232:235], v[232:235], a[136:139], 0\n\t"
"v_cvt_pk_bf16_f32 v32, v32, v33\n\t"
"v_cvt_pk_bf16_f32 v34, v34, v35\n\t"
"v_mfma_f32_16x16x32_bf16 v[228:231], v[228:231], a[136:139], 0\n\t"
"v_accvgpr_write_b32 a97, v30\n\t"
"v_accvgpr_write_b32 a42, v32\n\t"
"v_mfma_f32_16x16x32_bf16 v[40:43], a[4:7], a[176:179], v[40:43]\n\t"
"v_accvgpr_write_b32 a43, v34\n\t"
"v_mfma_f32_16x16x32_bf16 v[40:43], a[12:15], a[180:183], v[40:43]\n\t"
"v_cvt_pk_bf16_f32 v36, v36, v37\n\t"
"v_cvt_pk_bf16_f32 v38, v38, v39\n\t"
"v_mfma_f32_16x16x32_bf16 v[40:43], a[208:211], a[184:187], v[40:43]\n\t"
"buffer_store_dwordx4 a[108:111], v248, s[16:19], s29 offen offset:256 nt\n\t"
"v_accvgpr_write_b32 a50, v36\n\t"
"v_accvgpr_write_b32 a51, v38\n\t"
"s_add_u32 s29, s22, 590208\n\t"
"v_mfma_f32_16x16x32_bf16 v[44:47], a[4:7], a[180:183], v[44:47]\n\t"
"v_cvt_pk_bf16_f32 v232, v232, v233\n\t"
"v_cvt_pk_bf16_f32 v233, v234, v235\n\t"
"v_mfma_f32_16x16x32_bf16 v[44:47], a[12:15], a[184:187], v[44:47]\n\t"
"v_cvt_pk_bf16_f32 v234, v228, v229\n\t"
"v_mfma_f32_16x16x32_bf16 v[44:47], a[208:211], a[188:191], v[44:47]\n\t"
"s_waitcnt lgkmcnt(1)\n\t"
"s_barrier\n\t"
"v_mfma_f32_16x16x32_bf16 v[48:51], a[4:7], a[184:187], v[48:51]\n\t"
"ds_read_b64_tr_b16 v[224:225], v240 offset:512\n\t"
"v_mfma_f32_16x16x32_bf16 v[48:51], a[12:15], a[188:191], v[48:51]\n\t"
"ds_read_b64_tr_b16 v[226:227], v240 offset:2048\n\t"
"v_cvt_pk_bf16_f32 v235, v230, v231\n\t"
"v_mfma_f32_16x16x32_bf16 v[48:51], a[208:211], a[192:195], v[48:51]\n\t"
"ds_write_b128 v242, v[232:235] offset:32256\n\t"
"v_mfma_f32_16x16x32_bf16 v[52:55], a[4:7], a[188:191], v[52:55]\n\t"
"v_cvt_pk_bf16_f32 v40, v40, v41\n\t"
"v_cvt_pk_bf16_f32 v42, v42, v43\n\t"
"v_mfma_f32_16x16x32_bf16 v[52:55], a[12:15], a[192:195], v[52:55]\n\t"
"ds_read_b64_tr_b16 v[236:237], v241 offset:512\n\t"
"v_mfma_f32_16x16x32_bf16 v[52:55], a[208:211], a[196:199], v[52:55]\n\t"
"ds_read_b64_tr_b16 v[238:239], v241 offset:2048\n\t"
"v_cvt_pk_bf16_f32 v44, v44, v45\n\t"
"v_mfma_f32_16x16x32_bf16 v[56:59], a[4:7], a[192:195], v[56:59]\n\t"
"v_cvt_pk_bf16_f32 v46, v46, v47\n\t"
"v_accvgpr_write_b32 a58, v40\n\t"
"v_mfma_f32_16x16x32_bf16 v[56:59], a[12:15], a[196:199], v[56:59]\n\t"
"v_accvgpr_write_b32 a59, v42\n\t"
"v_cvt_pk_bf16_f32 v48, v48, v49\n\t"
"v_mfma_f32_16x16x32_bf16 v[56:59], a[208:211], a[200:203], v[56:59]\n\t"
"buffer_store_dwordx4 a[116:119], v248, s[16:19], s29 offen offset:0 nt\n\t"
"v_cvt_pk_bf16_f32 v50, v50, v51\n\t"
"v_accvgpr_write_b32 a66, v44\n\t"
"v_accvgpr_write_b32 a67, v46\n\t"
"v_accvgpr_write_b32 a74, v48\n\t"
"v_mfma_f32_16x16x32_bf16 v[60:63], a[4:7], a[196:199], v[60:63]\n\t"
"s_waitcnt lgkmcnt(3)\n\t"
"v_cvt_pk_bf16_f32 v52, v52, v53\n\t"
"v_cvt_pk_bf16_f32 v54, v54, v55\n\t"
"v_mfma_f32_16x16x32_bf16 v[60:63], a[12:15], a[200:203], v[60:63]\n\t"
"s_waitcnt lgkmcnt(0)\n\t"
"v_accvgpr_write_b32 a75, v50\n\t"
"v_accvgpr_write_b32 a82, v52\n\t"
"v_mfma_f32_16x16x32_bf16 v[60:63], a[208:211], a[204:207], v[60:63]\n\t"
"v_accvgpr_write_b32 a83, v54\n\t"
"v_mfma_f32_16x16x32_bf16 v[224:227], v[224:227], a[136:139], 0\n\t"
"buffer_load_dwordx4 a[4:7], v252, s[8:11], s20 offen offset:2048 sc1\n\t"
"s_mov_b32 s20, 196608\n\t"
"v_mfma_f32_16x16x32_bf16 v[236:239], v[236:239], a[136:139], 0\n\t"
"buffer_load_dwordx4 a[12:15], v252, s[8:11], s20 offen offset:0 sc1\n\t"
"v_mfma_f32_16x16x32_bf16 v[64:67], a[212:215], a[168:171], v[64:67]\n\t"
"ds_read_b128 a[168:171], v255 offset:1536\n\t"
"v_subrev_u32 v240, 59904, v240\n\t"
"v_mfma_f32_16x16x32_bf16 v[64:67], a[216:219], a[172:175], v[64:67]\n\t"
"v_subrev_u32 v241, 59904, v241\n\t"
"v_cvt_pk_bf16_f32 v56, v56, v57\n\t"
"v_mfma_f32_16x16x32_bf16 v[64:67], a[220:223], a[176:179], v[64:67]\n\t"
"ds_read_b64_tr_b16 v[228:229], v240 offset:512\n\t"
"v_cvt_pk_bf16_f32 v58, v58, v59\n\t"
"v_mfma_f32_16x16x32_bf16 v[68:71], a[212:215], a[172:175], v[68:71]\n\t"
"ds_read_b64_tr_b16 v[230:231], v240 offset:2048\n\t"
"v_cvt_pk_bf16_f32 v224, v224, v225\n\t"
"v_mfma_f32_16x16x32_bf16 v[68:71], a[216:219], a[176:179], v[68:71]\n\t"
"ds_read_b64_tr_b16 v[232:233], v241 offset:512\n\t"
"v_cvt_pk_bf16_f32 v225, v226, v227\n\t"
"v_mfma_f32_16x16x32_bf16 v[68:71], a[220:223], a[180:183], v[68:71]\n\t"
"buffer_load_dwordx4 a[208:211], v252, s[8:11], s20 offen offset:1024 sc1\n\t"
"v_mfma_f32_16x16x32_bf16 v[72:75], a[212:215], a[176:179], v[72:75]\n\t"
"ds_read_b64_tr_b16 v[234:235], v241 offset:2048\n\t"
"v_cvt_pk_bf16_f32 v226, v236, v237\n\t"
"v_mfma_f32_16x16x32_bf16 v[72:75], a[216:219], a[180:183], v[72:75]\n\t"
"ds_read_b128 a[172:175], v255 offset:4608\n\t"
"v_cvt_pk_bf16_f32 v227, v238, v239\n\t"
"v_mfma_f32_16x16x32_bf16 v[72:75], a[220:223], a[184:187], v[72:75]\n\t"
"buffer_store_dwordx4 a[124:127], v248, s[16:19], s29 offen offset:128 nt\n\t"
"ds_read_b128 a[176:179], v255 offset:7680\n\t"
"v_cvt_pk_bf16_f32 v60, v60, v61\n\t"
"v_cvt_pk_bf16_f32 v62, v62, v63\n\t"
"v_accvgpr_write_b32 a90, v56\n\t"
"v_mfma_f32_16x16x32_bf16 v[76:79], a[212:215], a[180:183], v[76:79]\n\t"
"ds_read_b128 a[180:183], v255 offset:10752\n\t"
"v_accvgpr_write_b32 a91, v58\n\t"
"v_mfma_f32_16x16x32_bf16 v[76:79], a[216:219], a[184:187], v[76:79]\n\t"
"v_accvgpr_write_b32 a98, v60\n\t"
"v_accvgpr_write_b32 a99, v62\n\t"
"v_mfma_f32_16x16x32_bf16 v[76:79], a[220:223], a[188:191], v[76:79]\n\t"
"ds_write_b128 v242, v[224:227] offset:29184\n\t"
"v_mfma_f32_16x16x32_bf16 v[80:83], a[212:215], a[184:187], v[80:83]\n\t"
"v_cvt_pk_bf16_f32 v64, v64, v65\n\t"
"v_cvt_pk_bf16_f32 v66, v66, v67\n\t"
"v_mfma_f32_16x16x32_bf16 v[80:83], a[216:219], a[188:191], v[80:83]\n\t"
"ds_read_b128 a[184:187], v255 offset:13824\n\t"
"v_mfma_f32_16x16x32_bf16 v[80:83], a[220:223], a[192:195], v[80:83]\n\t"
"buffer_store_dwordx4 a[156:159], v248, s[16:19], s29 offen offset:256 nt\n\t"
"v_cvt_pk_bf16_f32 v68, v68, v69\n\t"
"v_cvt_pk_bf16_f32 v70, v70, v71\n\t"
"v_accvgpr_write_b32 a108, v64\n\t"
"v_accvgpr_write_b32 a109, v66\n\t"
"v_mfma_f32_16x16x32_bf16 v[84:87], a[212:215], a[188:191], v[84:87]\n\t"
"s_waitcnt lgkmcnt(7)\n\t"
"ds_read_b128 a[188:191], v255 offset:16896\n\t"
"v_cvt_pk_bf16_f32 v72, v72, v73\n\t"
"v_mfma_f32_16x16x32_bf16 v[84:87], a[216:219], a[192:195], v[84:87]\n\t"
"s_waitcnt lgkmcnt(6)\n\t"
"v_cvt_pk_bf16_f32 v74, v74, v75\n\t"
"v_accvgpr_write_b32 a116, v68\n\t"
"v_mfma_f32_16x16x32_bf16 v[84:87], a[220:223], a[196:199], v[84:87]\n\t"
"v_accvgpr_write_b32 a117, v70\n\t"
"v_cvt_pk_bf16_f32 v76, v76, v77\n\t"
"v_mfma_f32_16x16x32_bf16 v[228:231], v[228:231], a[136:139], 0\n\t"
"v_cvt_pk_bf16_f32 v78, v78, v79\n\t"
"v_accvgpr_write_b32 a124, v72\n\t"
"v_mfma_f32_16x16x32_bf16 v[232:235], v[232:235], a[136:139], 0\n\t"
"v_accvgpr_write_b32 a125, v74\n\t"
"v_cvt_pk_bf16_f32 v80, v80, v81\n\t"
"v_mfma_f32_16x16x32_bf16 v[88:91], a[212:215], a[192:195], v[88:91]\n\t"
"s_add_u32 s29, s22, 786816\n\t"
"ds_read_b128 a[192:195], v255 offset:19968\n\t"
"v_cvt_pk_bf16_f32 v82, v82, v83\n\t"
"v_mfma_f32_16x16x32_bf16 v[88:91], a[216:219], a[196:199], v[88:91]\n\t"
"v_accvgpr_write_b32 a132, v76\n\t"
"v_accvgpr_write_b32 a133, v78\n\t"
"v_mfma_f32_16x16x32_bf16 v[88:91], a[220:223], a[200:203], v[88:91]\n\t"
"ds_read_b64_tr_b16 v[236:237], v240 offset:20480\n\t"
"v_cvt_pk_bf16_f32 v84, v84, v85\n\t"
"v_mfma_f32_16x16x32_bf16 v[92:95], a[212:215], a[196:199], v[92:95]\n\t"
"s_waitcnt lgkmcnt(6)\n\t"
"ds_read_b128 a[196:199], v255 offset:23040\n\t"
"v_cvt_pk_bf16_f32 v228, v228, v229\n\t"
"v_mfma_f32_16x16x32_bf16 v[92:95], a[216:219], a[200:203], v[92:95]\n\t"
"ds_read_b128 a[200:203], v255 offset:26112\n\t"
"v_cvt_pk_bf16_f32 v229, v230, v231\n\t"
"v_mfma_f32_16x16x32_bf16 v[92:95], a[220:223], a[204:207], v[92:95]\n\t"
"ds_read_b128 a[204:207], v255 offset:29184\n\t"
"v_cvt_pk_bf16_f32 v230, v232, v233\n\t"
"v_mfma_f32_16x16x32_bf16 v[100:103], a[224:227], a[168:171], v[100:103]\n\t"
"s_waitcnt lgkmcnt(8)\n\t"
"ds_read_b64_tr_b16 v[238:239], v240 offset:22016\n\t"
"v_cvt_pk_bf16_f32 v231, v234, v235\n\t"
"v_mfma_f32_16x16x32_bf16 v[100:103], a[228:231], a[172:175], v[100:103]\n\t"
"ds_read_b64_tr_b16 v[224:225], v241 offset:20480\n\t"
"v_cvt_pk_bf16_f32 v86, v86, v87\n\t"
"v_mfma_f32_16x16x32_bf16 v[100:103], a[240:243], a[176:179], v[100:103]\n\t"
"buffer_load_dwordx4 a[212:215], v252, s[8:11], s20 offen offset:2048 sc1\n\t"
"s_mov_b32 s20, 208896\n\t"
"v_mfma_f32_16x16x32_bf16 v[104:107], a[224:227], a[172:175], v[104:107]\n\t"
"buffer_load_dwordx4 a[216:219], v252, s[8:11], s20 offen offset:0 sc1\n\t"
"v_mfma_f32_16x16x32_bf16 v[104:107], a[228:231], a[176:179], v[104:107]\n\t"
"s_waitcnt lgkmcnt(8)\n\t"
"ds_read_b64_tr_b16 v[226:227], v241 offset:22016\n\t"
"v_mfma_f32_16x16x32_bf16 v[104:107], a[240:243], a[180:183], v[104:107]\n\t"
"s_waitcnt lgkmcnt(3)\n\t"
"s_barrier\n\t"
"v_mfma_f32_16x16x32_bf16 v[108:111], a[224:227], a[176:179], v[108:111]\n\t"
"v_accvgpr_write_b32 a140, v80\n\t"
"v_mfma_f32_16x16x32_bf16 v[108:111], a[228:231], a[180:183], v[108:111]\n\t"
"buffer_load_dwordx4 a[220:223], v252, s[8:11], s20 offen offset:1024 sc1\n\t"
"v_mfma_f32_16x16x32_bf16 v[108:111], a[240:243], a[184:187], v[108:111]\n\t"
"s_waitcnt lgkmcnt(2)\n\t"
"ds_write_b128 v242, v[228:231] offset:24576\n\t"
"v_mfma_f32_16x16x32_bf16 v[112:115], a[224:227], a[180:183], v[112:115]\n\t"
"Lpg13_%=:\n\t"
"s_cbranch_vccnz Lvr1_%=\n\t"
"v_accvgpr_write_b32 a141, v82\n\t"
"v_accvgpr_write_b32 a148, v84\n\t"
"v_mfma_f32_16x16x32_bf16 v[112:115], a[228:231], a[184:187], v[112:115]\n\t"
"s_waitcnt lgkmcnt(1)\n\t"
"v_accvgpr_write_b32 a149, v86\n\t"
"v_cvt_pk_bf16_f32 v88, v88, v89\n\t"
"v_mfma_f32_16x16x32_bf16 v[112:115], a[240:243], a[188:191], v[112:115]\n\t"
"buffer_store_dwordx4 a[52:55], v248, s[16:19], s29 offen offset:0 nt\n\t"
"ds_write_b64 v250, a[108:109] offset:0\n\t"
"v_cvt_pk_bf16_f32 v90, v90, v91\n\t"
"v_mfma_f32_16x16x32_bf16 v[236:239], v[236:239], a[136:139], 0\n\t"
"ds_write_b128 v249, a[40:43] offset:0\n\t"
"v_mfma_f32_16x16x32_bf16 v[224:227], v[224:227], a[136:139], 0\n\t"
"buffer_store_dwordx4 a[84:87], v248, s[16:19], s29 offen offset:128 nt\n\t"
"v_cvt_pk_bf16_f32 v92, v92, v93\n\t"
"v_cvt_pk_bf16_f32 v94, v94, v95\n\t"
"v_accvgpr_write_b32 a156, v88\n\t"
"v_accvgpr_write_b32 a157, v90\n\t"
"v_mfma_f32_16x16x32_bf16 v[116:119], a[224:227], a[184:187], v[116:119]\n\t"
"v_cvt_pk_bf16_f32 v100, v100, v101\n\t"
"v_cvt_pk_bf16_f32 v102, v102, v103\n\t"
"v_mfma_f32_16x16x32_bf16 v[116:119], a[228:231], a[188:191], v[116:119]\n\t"
"v_accvgpr_write_b32 a164, v92\n\t"
"v_accvgpr_write_b32 a165, v94\n\t"
"v_mfma_f32_16x16x32_bf16 v[116:119], a[240:243], a[192:195], v[116:119]\n\t"
"ds_read_b64_tr_b16 v[232:233], v240 offset:40448\n\t"
"v_cvt_pk_bf16_f32 v104, v104, v105\n\t"
"v_mfma_f32_16x16x32_bf16 v[120:123], a[224:227], a[188:191], v[120:123]\n\t"
"ds_read_b64_tr_b16 v[234:235], v240 offset:41984\n\t"
"v_cvt_pk_bf16_f32 v236, v236, v237\n\t"
"v_mfma_f32_16x16x32_bf16 v[120:123], a[228:231], a[192:195], v[120:123]\n\t"
"ds_read_b64_tr_b16 v[228:229], v241 offset:40448\n\t"
"v_cvt_pk_bf16_f32 v237, v238, v239\n\t"
"v_mfma_f32_16x16x32_bf16 v[120:123], a[240:243], a[196:199], v[120:123]\n\t"
"ds_read_b64_tr_b16 v[230:231], v241 offset:41984\n\t"
"v_cvt_pk_bf16_f32 v238, v224, v225\n\t"
"v_mfma_f32_16x16x32_bf16 v[124:127], a[224:227], a[192:195], v[124:127]\n\t"
"v_cvt_pk_bf16_f32 v239, v226, v227\n\t"
"v_cvt_pk_bf16_f32 v106, v106, v107\n\t"
"v_mfma_f32_16x16x32_bf16 v[124:127], a[228:231], a[196:199], v[124:127]\n\t"
"v_accvgpr_write_b32 a44, v100\n\t"
"v_accvgpr_write_b32 a45, v102\n\t"
"v_mfma_f32_16x16x32_bf16 v[124:127], a[240:243], a[200:203], v[124:127]\n\t"
"ds_write_b128 v242, v[236:239] offset:26112\n\t"
"v_mfma_f32_16x16x32_bf16 v[128:131], a[224:227], a[196:199], v[128:131]\n\t"
"buffer_load_dwordx4 a[224:227], v252, s[8:11], s20 offen offset:2048 sc1\n\t"
"v_mfma_f32_16x16x32_bf16 v[128:131], a[228:231], a[200:203], v[128:131]\n\t"
"s_mov_b32 s20, 294912\n\t"
"v_cvt_pk_bf16_f32 v108, v108, v109\n\t"
"v_cvt_pk_bf16_f32 v110, v110, v111\n\t"
"v_mfma_f32_16x16x32_bf16 v[128:131], a[240:243], a[204:207], v[128:131]\n\t"
"ds_write_b128 v249, a[56:59] offset:12288\n\t"
"v_mfma_f32_16x16x32_bf16 v[132:135], a[232:235], a[168:171], v[132:135]\n\t"
"buffer_load_dwordx4 a[228:231], v252, s[8:11], s20 offen offset:0 sc1\n\t"
"v_mfma_f32_16x16x32_bf16 v[132:135], a[244:247], a[172:175], v[132:135]\n\t"
"v_accvgpr_write_b32 a52, v104\n\t"
"v_accvgpr_write_b32 a53, v106\n\t"
"v_mfma_f32_16x16x32_bf16 v[132:135], a[248:251], a[176:179], v[132:135]\n\t"
"ds_write_b64 v250, a[124:125] offset:12288\n\t"
"v_mfma_f32_16x16x32_bf16 v[136:139], a[232:235], a[172:175], v[136:139]\n\t"
"buffer_load_dwordx4 a[240:243], v252, s[8:11], s20 offen offset:1024 sc1\n\t"
"v_mfma_f32_16x16x32_bf16 v[136:139], a[244:247], a[176:179], v[136:139]\n\t"
"s_waitcnt lgkmcnt(3)\n\t"
"v_cvt_pk_bf16_f32 v112, v112, v113\n\t"
"v_cvt_pk_bf16_f32 v114, v114, v115\n\t"
"v_mfma_f32_16x16x32_bf16 v[136:139], a[248:251], a[180:183], v[136:139]\n\t"
"buffer_store_dwordx4 v[196:199], v248, s[16:19], s29 offen offset:256 nt\n\t"
"ds_write_b64 v250, a[116:117] offset:6144\n\t"
"v_accvgpr_write_b32 a60, v108\n\t"
"v_mfma_f32_16x16x32_bf16 v[232:235], v[232:235], a[136:139], 0\n\t"
"s_add_u32 s29, s22, 983424\n\t"
"ds_write_b128 v249, a[48:51] offset:6144\n\t"
"v_accvgpr_write_b32 a61, v110\n\t"
"v_mfma_f32_16x16x32_bf16 v[228:231], v[228:231], a[136:139], 0\n\t"
"v_accvgpr_write_b32 a68, v112\n\t"
"v_accvgpr_write_b32 a69, v114\n\t"
"v_mfma_f32_16x16x32_bf16 v[140:143], a[232:235], a[176:179], v[140:143]\n\t"
"v_cvt_pk_bf16_f32 v116, v116, v117\n\t"
"v_cvt_pk_bf16_f32 v118, v118, v119\n\t"
"v_mfma_f32_16x16x32_bf16 v[140:143], a[244:247], a[180:183], v[140:143]\n\t"
"v_cvt_pk_bf16_f32 v120, v120, v121\n\t"
"v_cvt_pk_bf16_f32 v122, v122, v123\n\t"
"v_mfma_f32_16x16x32_bf16 v[140:143], a[248:251], a[184:187], v[140:143]\n\t"
"ds_write_b128 v249, a[64:67] offset:18432\n\t"
"v_mfma_f32_16x16x32_bf16 v[144:147], a[232:235], a[180:183], v[144:147]\n\t"
"v_cvt_pk_bf16_f32 v232, v232, v233\n\t"
"v_cvt_pk_bf16_f32 v233, v234, v235\n\t"
"v_mfma_f32_16x16x32_bf16 v[144:147], a[244:247], a[184:187], v[144:147]\n\t"
"v_accvgpr_write_b32 a76, v116\n\t"
"v_accvgpr_write_b32 a77, v118\n\t"
"v_mfma_f32_16x16x32_bf16 v[144:147], a[248:251], a[188:191], v[144:147]\n\t"
"ds_write_b64 v250, a[132:133] offset:18432\n\t"
"v_mfma_f32_16x16x32_bf16 v[148:151], a[232:235], a[184:187], v[148:151]\n\t"
"v_cvt_pk_bf16_f32 v234, v228, v229\n\t"
"v_cvt_pk_bf16_f32 v235, v230, v231\n\t"
"v_mfma_f32_16x16x32_bf16 v[148:151], a[244:247], a[188:191], v[148:151]\n\t"
"v_cvt_pk_bf16_f32 v124, v124, v125\n\t"
"v_cvt_pk_bf16_f32 v126, v126, v127\n\t"
"v_mfma_f32_16x16x32_bf16 v[148:151], a[248:251], a[192:195], v[148:151]\n\t"
"ds_write_b128 v242, v[232:235] offset:27648\n\t"
"v_mfma_f32_16x16x32_bf16 v[152:155], a[232:235], a[188:191], v[152:155]\n\t"
"v_accvgpr_write_b32 a84, v120\n\t"
"v_accvgpr_write_b32 a85, v122\n\t"
"v_mfma_f32_16x16x32_bf16 v[152:155], a[244:247], a[192:195], v[152:155]\n\t"
"v_cvt_pk_bf16_f32 v128, v128, v129\n\t"
"v_cvt_pk_bf16_f32 v130, v130, v131\n\t"
"v_mfma_f32_16x16x32_bf16 v[152:155], a[248:251], a[196:199], v[152:155]\n\t"
"buffer_store_dwordx4 v[204:207], v248, s[16:19], s29 offen offset:0 nt\n\t"
"v_accvgpr_write_b32 a92, v124\n\t"
"v_accvgpr_write_b32 a93, v126\n\t"
"v_cvt_pk_bf16_f32 v132, v132, v133\n\t"
"v_cvt_pk_bf16_f32 v134, v134, v135\n\t"
"v_mfma_f32_16x16x32_bf16 v[156:159], a[232:235], a[192:195], v[156:159]\n\t"
"v_accvgpr_write_b32 a100, v128\n\t"
"v_accvgpr_write_b32 a101, v130\n\t"
"v_mfma_f32_16x16x32_bf16 v[156:159], a[244:247], a[196:199], v[156:159]\n\t"
"v_cvt_pk_bf16_f32 v136, v136, v137\n\t"
"v_cvt_pk_bf16_f32 v138, v138, v139\n\t"
"v_mfma_f32_16x16x32_bf16 v[156:159], a[248:251], a[200:203], v[156:159]\n\t"
"v_accvgpr_write_b32 a46, v132\n\t"
"v_accvgpr_write_b32 a47, v134\n\t"
"v_mfma_f32_16x16x32_bf16 v[160:163], a[232:235], a[196:199], v[160:163]\n\t"
"v_accvgpr_write_b32 a54, v136\n\t"
"v_accvgpr_write_b32 a55, v138\n\t"
"v_mfma_f32_16x16x32_bf16 v[160:163], a[244:247], a[200:203], v[160:163]\n\t"
"v_cvt_pk_bf16_f32 v140, v140, v141\n\t"
"v_cvt_pk_bf16_f32 v142, v142, v143\n\t"
"v_mfma_f32_16x16x32_bf16 v[160:163], a[248:251], a[204:207], v[160:163]\n\t"
"v_cvt_pk_bf16_f32 v144, v144, v145\n\t"
"v_cvt_pk_bf16_f32 v146, v146, v147\n\t"
"v_mfma_f32_16x16x32_bf16 v[164:167], a[252:255], a[168:171], v[164:167]\n\t"
"v_accvgpr_write_b32 a62, v140\n\t"
"v_accvgpr_write_b32 a63, v142\n\t"
"v_mfma_f32_16x16x32_bf16 v[164:167], a[144:147], a[172:175], v[164:167]\n\t"
"v_cvt_pk_bf16_f32 v148, v148, v149\n\t"
"v_cvt_pk_bf16_f32 v150, v150, v151\n\t"
"v_mfma_f32_16x16x32_bf16 v[164:167], a[20:23], a[176:179], v[164:167]\n\t"
"buffer_load_dwordx4 a[232:235], v252, s[8:11], s20 offen offset:2048 sc1\n\t"
"s_mov_b32 s20, 307200\n\t"
"v_mfma_f32_16x16x32_bf16 v[168:171], a[252:255], a[172:175], v[168:171]\n\t"
"buffer_load_dwordx4 a[244:247], v252, s[8:11], s20 offen offset:0 sc1\n\t"
"v_mfma_f32_16x16x32_bf16 v[168:171], a[144:147], a[176:179], v[168:171]\n\t"
"buffer_load_dwordx4 a[248:251], v252, s[8:11], s20 offen offset:1024 sc1\n\t"
"v_mfma_f32_16x16x32_bf16 v[168:171], a[20:23], a[180:183], v[168:171]\n\t"
"buffer_store_dwordx4 v[212:215], v248, s[16:19], s29 offen offset:128 nt\n\t"
"v_accvgpr_write_b32 a70, v144\n\t"
"v_accvgpr_write_b32 a71, v146\n\t"
"v_cvt_pk_bf16_f32 v152, v152, v153\n\t"
"v_cvt_pk_bf16_f32 v154, v154, v155\n\t"
"v_mfma_f32_16x16x32_bf16 v[172:175], a[252:255], a[176:179], v[172:175]\n\t"
"v_accvgpr_write_b32 a78, v148\n\t"
"v_accvgpr_write_b32 a79, v150\n\t"
"v_mfma_f32_16x16x32_bf16 v[172:175], a[144:147], a[180:183], v[172:175]\n\t"
"v_cvt_pk_bf16_f32 v156, v156, v157\n\t"
"v_cvt_pk_bf16_f32 v158, v158, v159\n\t"
"v_mfma_f32_16x16x32_bf16 v[172:175], a[20:23], a[184:187], v[172:175]\n\t"
"v_accvgpr_write_b32 a86, v152\n\t"
"v_accvgpr_write_b32 a87, v154\n\t"
"v_mfma_f32_16x16x32_bf16 v[176:179], a[252:255], a[180:183], v[176:179]\n\t"
"v_cvt_pk_bf16_f32 v160, v160, v161\n\t"
"v_cvt_pk_bf16_f32 v162, v162, v163\n\t"
"v_mfma_f32_16x16x32_bf16 v[176:179], a[144:147], a[184:187], v[176:179]\n\t"
"v_accvgpr_write_b32 a94, v156\n\t"
"v_mfma_f32_16x16x32_bf16 v[176:179], a[20:23], a[188:191], v[176:179]\n\t"
"s_waitcnt lgkmcnt(0)\n\t"
"s_barrier\n\t"
"v_mfma_f32_16x16x32_bf16 v[180:183], a[252:255], a[184:187], v[180:183]\n\t"
"ds_read_b128 a[168:171], v244 offset:24576\n\t"
"v_mfma_f32_16x16x32_bf16 v[180:183], a[144:147], a[188:191], v[180:183]\n\t"
"ds_read_b128 a[172:175], v244 offset:25088\n\t"
"v_accvgpr_write_b32 a95, v158\n\t"
"v_mfma_f32_16x16x32_bf16 v[180:183], a[20:23], a[192:195], v[180:183]\n\t"
"ds_read_b128 a[176:179], v244 offset:25600\n\t"
"v_cvt_pk_bf16_f32 v164, v164, v165\n\t"
"v_mfma_f32_16x16x32_bf16 v[184:187], a[252:255], a[188:191], v[184:187]\n\t"
"ds_read_b128 a[180:183], v244 offset:26112\n\t"
"v_cvt_pk_bf16_f32 v166, v166, v167\n\t"
"v_mfma_f32_16x16x32_bf16 v[184:187], a[144:147], a[192:195], v[184:187]\n\t"
"ds_read_b128 a[184:187], v244 offset:26624\n\t"
"v_accvgpr_write_b32 a102, v160\n\t"
"v_mfma_f32_16x16x32_bf16 v[184:187], a[20:23], a[196:199], v[184:187]\n\t"
"buffer_load_dwordx4 a[48:51], v252, s[8:11], s20 offen offset:2048 sc1\n\t"
"s_mov_b32 s20, 319488\n\t"
"v_mfma_f32_16x16x32_bf16 v[188:191], a[252:255], a[192:195], v[188:191]\n\t"
"ds_read_b128 a[188:191], v244 offset:27136\n\t"
"v_accvgpr_write_b32 a103, v162\n\t"
"v_mfma_f32_16x16x32_bf16 v[188:191], a[144:147], a[196:199], v[188:191]\n\t"
"ds_read_b128 a[192:195], v244 offset:27648\n\t"
"v_cvt_pk_bf16_f32 v168, v168, v169\n\t"
"v_mfma_f32_16x16x32_bf16 v[188:191], a[20:23], a[200:203], v[188:191]\n\t"
"buffer_store_dwordx4 a[0:3], v248, s[16:19], s29 offen offset:256 nt\n\t"
"buffer_load_dwordx4 a[56:59], v252, s[8:11], s20 offen offset:0 sc1\n\t"
"v_cvt_pk_bf16_f32 v170, v170, v171\n\t"
"v_mfma_f32_16x16x32_bf16 v[192:195], a[252:255], a[196:199], v[192:195]\n\t"
"s_waitcnt lgkmcnt(4)\n\t"
"ds_read_b128 a[196:199], v244 offset:28160\n\t"
"v_accvgpr_write_b32 a110, v164\n\t"
"v_mfma_f32_16x16x32_bf16 v[192:195], a[144:147], a[200:203], v[192:195]\n\t"
"ds_read_b128 a[200:203], v244 offset:28672\n\t"
"v_accvgpr_write_b32 a111, v166\n\t"
"v_mfma_f32_16x16x32_bf16 v[192:195], a[20:23], a[204:207], v[192:195]\n\t"
"buffer_load_dwordx4 a[64:67], v252, s[8:11], s20 offen offset:1024 sc1\n\t"
"s_add_u32 s29, s22, 1180032\n\t"
"Lst18_%=:\n\t"
"v_mfma_f32_16x16x32_bf16 v[2:5], v[96:99], a[168:171], 0\n\t"
"buffer_load_dwordx4 a[144:147], v252, s[8:11], s20 offen offset:2048 sc1\n\t"
"v_mfma_f32_16x16x32_bf16 v[2:5], a[236:239], a[172:175], v[2:5]\n\t"
"s_waitcnt lgkmcnt(5)\n\t"
"ds_read_b128 a[204:207], v244 offset:29184\n\t"
"v_cvt_pk_bf16_f32 v172, v172, v173\n\t"
"v_mfma_f32_16x16x32_bf16 v[2:5], a[152:155], a[176:179], v[2:5]\n\t"
"s_mov_b32 s20, 479232\n\t"
"v_cvt_pk_bf16_f32 v174, v174, v175\n\t"
"v_accvgpr_write_b32 a118, v168\n\t"
"v_mfma_f32_16x16x32_bf16 v[6:9], v[96:99], a[172:175], 0\n\t"
"s_waitcnt lgkmcnt(5)\n\t"
"v_accvgpr_write_b32 a119, v170\n\t"
"v_cvt_pk_bf16_f32 v176, v176, v177\n\t"
"v_mfma_f32_16x16x32_bf16 v[6:9], a[236:239], a[176:179], v[6:9]\n\t"
"v_cvt_pk_bf16_f32 v178, v178, v179\n\t"
"v_accvgpr_write_b32 a126, v172\n\t"
"v_mfma_f32_16x16x32_bf16 v[6:9], a[152:155], a[180:183], v[6:9]\n\t"
"buffer_load_dwordx4 a[20:23], v252, s[8:11], s20 offen offset:0 sc1\n\t"
"v_mfma_f32_16x16x32_bf16 v[10:13], v[96:99], a[176:179], 0\n\t"
"buffer_load_dwordx4 a[252:255], v252, s[8:11], s20 offen offset:1024 sc1\n\t"
"v_mfma_f32_16x16x32_bf16 v[10:13], a[236:239], a[180:183], v[10:13]\n\t"
"s_waitcnt lgkmcnt(4)\n\t"
"v_accvgpr_write_b32 a127, v174\n\t"
"v_cvt_pk_bf16_f32 v180, v180, v181\n\t"
"v_mfma_f32_16x16x32_bf16 v[10:13], a[152:155], a[184:187], v[10:13]\n\t"
"buffer_store_dwordx4 a[8:11], v248, s[16:19], s29 offen offset:0 nt\n\t"
"v_cvt_pk_bf16_f32 v182, v182, v183\n\t"
"v_accvgpr_write_b32 a134, v176\n\t"
"v_accvgpr_write_b32 a135, v178\n\t"
"v_cvt_pk_bf16_f32 v184, v184, v185\n\t"
"v_mfma_f32_16x16x32_bf16 v[14:17], v[96:99], a[180:183], 0\n\t"
"s_waitcnt lgkmcnt(3)\n\t"
"v_cvt_pk_bf16_f32 v186, v186, v187\n\t"
"v_accvgpr_write_b32 a142, v180\n\t"
"v_mfma_f32_16x16x32_bf16 v[14:17], a[236:239], a[184:187], v[14:17]\n\t"
"v_accvgpr_write_b32 a143, v182\n\t"
"v_cvt_pk_bf16_f32 v188, v188, v189\n\t"
"v_mfma_f32_16x16x32_bf16 v[14:17], a[152:155], a[188:191], v[14:17]\n\t"
"v_cvt_pk_bf16_f32 v190, v190, v191\n\t"
"v_accvgpr_write_b32 a150, v184\n\t"
"v_mfma_f32_16x16x32_bf16 v[18:21], v[96:99], a[184:187], 0\n\t"
"s_waitcnt lgkmcnt(2)\n\t"
"v_accvgpr_write_b32 a151, v186\n\t"
"v_cvt_pk_bf16_f32 v192, v192, v193\n\t"
"v_mfma_f32_16x16x32_bf16 v[18:21], a[236:239], a[188:191], v[18:21]\n\t"
"v_cvt_pk_bf16_f32 v194, v194, v195\n\t"
"v_accvgpr_write_b32 a158, v188\n\t"
"v_mfma_f32_16x16x32_bf16 v[18:21], a[152:155], a[192:195], v[18:21]\n\t"
"ds_read_b128 v[196:199], v252 offset:0\n\t"
"v_accvgpr_write_b32 a159, v190\n\t"
"v_mfma_f32_16x16x32_bf16 v[22:25], v[96:99], a[188:191], 0\n\t"
"s_waitcnt lgkmcnt(2)\n\t"
"ds_read_b128 a[0:3], v252 offset:12288\n\t"
"v_accvgpr_write_b32 a166, v192\n\t"
"v_mfma_f32_16x16x32_bf16 v[22:25], a[236:239], a[192:195], v[22:25]\n\t"
"ds_read_b128 v[204:207], v252 offset:1024\n\t"
"v_accvgpr_write_b32 a167, v194\n\t"
"v_mfma_f32_16x16x32_bf16 v[22:25], a[152:155], a[196:199], v[22:25]\n\t"
"ds_read_b128 a[8:11], v252 offset:13312\n\t"
"v_mfma_f32_16x16x32_bf16 v[26:29], v[96:99], a[192:195], 0\n\t"
"s_waitcnt lgkmcnt(4)\n\t"
"ds_read_b128 v[212:215], v252 offset:2048\n\t"
"v_mfma_f32_16x16x32_bf16 v[26:29], a[236:239], a[196:199], v[26:29]\n\t"
"ds_read_b128 a[40:43], v252 offset:14336\n\t"
"v_mfma_f32_16x16x32_bf16 v[26:29], a[152:155], a[200:203], v[26:29]\n\t"
"buffer_store_dwordx4 a[16:19], v248, s[16:19], s29 offen offset:128 nt\n\t"
"v_mfma_f32_16x16x32_bf16 v[30:33], v[96:99], a[196:199], 0\n\t"
"s_waitcnt vmcnt(38)\n\t"
"v_mfma_f32_16x16x32_bf16 v[30:33], a[236:239], a[200:203], v[30:33]\n\t"
"buffer_load_dwordx4 a[236:239], v252, s[8:11], s20 offen offset:2048 sc1\n\t"
"v_mfma_f32_16x16x32_bf16 v[30:33], a[152:155], a[204:207], v[30:33]\n\t"
"s_mov_b32 s20, 491520\n\t"
"v_mfma_f32_16x16x32_bf16 v[34:37], a[28:31], a[168:171], 0\n\t"
"v_mfma_f32_16x16x32_bf16 v[34:37], a[160:163], a[172:175], v[34:37]\n\t"
"buffer_load_dwordx4 a[152:155], v252, s[8:11], s20 offen offset:0 sc1\n\t"
"v_mfma_f32_16x16x32_bf16 v[34:37], a[36:39], a[176:179], v[34:37]\n\t"
"buffer_store_dwordx4 a[24:27], v248, s[16:19], s29 offen offset:256 nt\n\t"
"ds_read_b64_tr_b16 v[224:225], v240 offset:1024\n\t"
"ds_read_b64_tr_b16 v[226:227], v240 offset:2560\n\t"
"v_mfma_f32_16x16x32_bf16 v[38:41], a[28:31], a[172:175], 0\n\t"
"v_mfma_f32_16x16x32_bf16 v[38:41], a[160:163], a[176:179], v[38:41]\n\t"
"v_mfma_f32_16x16x32_bf16 v[38:41], a[36:39], a[180:183], v[38:41]\n\t"
"v_mfma_f32_16x16x32_bf16 v[42:45], a[28:31], a[176:179], 0\n\t"
"ds_read_b64_tr_b16 v[236:237], v241 offset:1024\n\t"
"v_mfma_f32_16x16x32_bf16 v[42:45], a[160:163], a[180:183], v[42:45]\n\t"
"ds_read_b64_tr_b16 v[238:239], v241 offset:2560\n\t"
"v_mfma_f32_16x16x32_bf16 v[42:45], a[36:39], a[184:187], v[42:45]\n\t"
"s_add_u32 s29, s22, 1376640\n\t"
"v_mfma_f32_16x16x32_bf16 v[46:49], a[28:31], a[180:183], 0\n\t"
"v_mfma_f32_16x16x32_bf16 v[46:49], a[160:163], a[184:187], v[46:49]\n\t"
"v_mfma_f32_16x16x32_bf16 v[46:49], a[36:39], a[188:191], v[46:49]\n\t"
"s_waitcnt lgkmcnt(4)\n\t"
"s_barrier\n\t"
"v_mfma_f32_16x16x32_bf16 v[50:53], a[28:31], a[184:187], 0\n\t"
"v_mfma_f32_16x16x32_bf16 v[50:53], a[160:163], a[188:191], v[50:53]\n\t"
"v_mfma_f32_16x16x32_bf16 v[50:53], a[36:39], a[192:195], v[50:53]\n\t"
"buffer_store_dwordx4 a[32:35], v248, s[16:19], s29 offen offset:0 nt\n\t"
"ds_write_b64 v250, a[140:141] offset:0\n\t"
"v_mfma_f32_16x16x32_bf16 v[54:57], a[28:31], a[188:191], 0\n\t"
"s_waitcnt lgkmcnt(3)\n\t"
"v_mfma_f32_16x16x32_bf16 v[54:57], a[160:163], a[192:195], v[54:57]\n\t"
"ds_read_b64_tr_b16 v[228:229], v240 offset:20992\n\t"
"v_mfma_f32_16x16x32_bf16 v[54:57], a[36:39], a[196:199], v[54:57]\n\t"
"ds_read_b64_tr_b16 v[230:231], v240 offset:22528\n\t"
"v_mfma_f32_16x16x32_bf16 v[224:227], v[224:227], a[136:139], 0\n\t"
"ds_write_b128 v249, a[72:75] offset:0\n\t"
"v_mfma_f32_16x16x32_bf16 v[58:61], a[28:31], a[192:195], 0\n\t"
"s_waitcnt lgkmcnt(4)\n\t"
"v_mfma_f32_16x16x32_bf16 v[58:61], a[160:163], a[196:199], v[58:61]\n\t"
"ds_read_b64_tr_b16 v[232:233], v241 offset:20992\n\t"
"v_mfma_f32_16x16x32_bf16 v[58:61], a[36:39], a[200:203], v[58:61]\n\t"
"ds_write_b128 v249, a[88:91] offset:12288\n\t"
"v_mfma_f32_16x16x32_bf16 v[236:239], v[236:239], a[136:139], 0\n\t"
"v_mfma_f32_16x16x32_bf16 v[62:65], a[28:31], a[196:199], 0\n\t"
"buffer_load_dwordx4 a[28:31], v252, s[8:11], s20 offen offset:1024 sc1\n\t"
"v_mfma_f32_16x16x32_bf16 v[62:65], a[160:163], a[200:203], v[62:65]\n\t"
"s_waitcnt vmcnt(39)\n\t"
"ds_read_b64_tr_b16 v[234:235], v241 offset:22528\n\t"
"v_cvt_pk_bf16_f32 v224, v224, v225\n\t"
"v_mfma_f32_16x16x32_bf16 v[62:65], a[36:39], a[204:207], v[62:65]\n\t"
"ds_write_b64 v250, a[156:157] offset:12288\n\t"
"v_mfma_f32_16x16x32_bf16 v[66:69], v[200:203], a[168:171], 0\n\t"
"buffer_load_dwordx4 a[160:163], v252, s[8:11], s20 offen offset:2048 sc1\n\t"
"v_mfma_f32_16x16x32_bf16 v[66:69], a[120:123], a[172:175], v[66:69]\n\t"
"s_mov_b32 s20, 503808\n\t"
"ds_read_b128 a[168:171], v244 offset:33792\n\t"
"v_cvt_pk_bf16_f32 v225, v226, v227\n\t"
"v_mfma_f32_16x16x32_bf16 v[66:69], a[128:131], a[176:179], v[66:69]\n\t"
"ds_write_b128 v249, a[80:83] offset:6144\n\t"
"v_mfma_f32_16x16x32_bf16 v[70:73], v[200:203], a[172:175], 0\n\t"
"buffer_load_dwordx4 a[36:39], v252, s[8:11], s20 offen offset:0 sc1\n\t"
"v_mfma_f32_16x16x32_bf16 v[70:73], a[120:123], a[176:179], v[70:73]\n\t"
"s_waitcnt lgkmcnt(7)\n\t"
"ds_read_b128 a[172:175], v244 offset:34304\n\t"
"v_mfma_f32_16x16x32_bf16 v[70:73], a[128:131], a[180:183], v[70:73]\n\t"
"buffer_store_dwordx4 a[104:107], v248, s[16:19], s29 offen offset:128 nt\n\t"
"ds_write_b64 v250, a[148:149] offset:6144\n\t"
"v_cvt_pk_bf16_f32 v226, v236, v237\n\t"
"v_cvt_pk_bf16_f32 v227, v238, v239\n\t"
"v_mfma_f32_16x16x32_bf16 v[228:231], v[228:231], a[136:139], 0\n\t"
"ds_write_b128 v249, a[96:99] offset:18432\n\t"
"v_mfma_f32_16x16x32_bf16 v[74:77], v[200:203], a[176:179], 0\n\t"
"s_waitcnt lgkmcnt(6)\n\t"
"v_mfma_f32_16x16x32_bf16 v[74:77], a[120:123], a[180:183], v[74:77]\n\t"
"ds_read_b128 a[176:179], v244 offset:34816\n\t"
"v_mfma_f32_16x16x32_bf16 v[74:77], a[128:131], a[184:187], v[74:77]\n\t"
"ds_write_b64 v250, a[164:165] offset:18432\n\t"
"v_mfma_f32_16x16x32_bf16 v[232:235], v[232:235], a[136:139], 0\n\t"
"v_mfma_f32_16x16x32_bf16 v[78:81], v[200:203], a[180:183], 0\n\t"
"ds_read_b128 a[180:183], v244 offset:35328\n\t"
"v_cvt_pk_bf16_f32 v228, v228, v229\n\t"
"v_mfma_f32_16x16x32_bf16 v[78:81], a[120:123], a[184:187], v[78:81]\n\t"
"v_cvt_pk_bf16_f32 v229, v230, v231\n\t"
"v_mfma_f32_16x16x32_bf16 v[78:81], a[128:131], a[188:191], v[78:81]\n\t"
"buffer_store_dwordx4 a[112:115], v248, s[16:19], s29 offen offset:256 nt\n\t"
"v_mfma_f32_16x16x32_bf16 v[82:85], v[200:203], a[184:187], 0\n\t"
"ds_read_b128 a[184:187], v244 offset:35840\n\t"
"v_mfma_f32_16x16x32_bf16 v[82:85], a[120:123], a[188:191], v[82:85]\n\t"
"v_cvt_pk_bf16_f32 v230, v232, v233\n\t"
"v_cvt_pk_bf16_f32 v231, v234, v235\n\t"
"v_mfma_f32_16x16x32_bf16 v[82:85], a[128:131], a[192:195], v[82:85]\n\t"
"v_mfma_f32_16x16x32_bf16 v[86:89], v[200:203], a[188:191], 0\n\t"
"ds_read_b128 a[188:191], v244 offset:36352\n\t"
"v_mfma_f32_16x16x32_bf16 v[86:89], a[120:123], a[192:195], v[86:89]\n\t"
"v_mfma_f32_16x16x32_bf16 v[86:89], a[128:131], a[196:199], v[86:89]\n\t"
"v_mfma_f32_16x16x32_bf16 v[90:93], v[200:203], a[192:195], 0\n\t"
"ds_read_b128 a[192:195], v244 offset:36864\n\t"
"v_mfma_f32_16x16x32_bf16 v[90:93], a[120:123], a[196:199], v[90:93]\n\t"
"v_mfma_f32_16x16x32_bf16 v[90:93], a[128:131], a[200:203], v[90:93]\n\t"
"buffer_load_dwordx4 a[112:115], v252, s[8:11], s20 offen offset:1024 sc1\n\t"
"v_mfma_f32_16x16x32_bf16 v[94:97], v[200:203], a[196:199], 0\n\t"
"s_waitcnt vmcnt(37) lgkmcnt(5)\n\t"
"ds_read_b128 a[196:199], v244 offset:37376\n\t"
"v_mfma_f32_16x16x32_bf16 v[94:97], a[120:123], a[200:203], v[94:97]\n\t"
"ds_read_b128 a[200:203], v244 offset:37888\n\t"
"v_mfma_f32_16x16x32_bf16 v[94:97], a[128:131], a[204:207], v[94:97]\n\t"
"ds_read_b128 a[204:207], v244 offset:38400\n\t"
"v_mfma_f32_16x16x32_bf16 v[100:103], v[208:211], a[168:171], 0\n\t"
"buffer_load_dwordx4 v[200:203], v252, s[8:11], s20 offen offset:2048 sc1\n\t"
"v_mfma_f32_16x16x32_bf16 v[100:103], v[216:219], a[172:175], v[100:103]\n\t"
"s_waitcnt lgkmcnt(6)\n\t"
"v_mfma_f32_16x16x32_bf16 v[100:103], a[4:7], a[176:179], v[100:103]\n\t"
"ds_read_b64_tr_b16 v[236:237], v240 offset:40960\n\t"
"v_mfma_f32_16x16x32_bf16 v[104:107], v[208:211], a[172:175], 0\n\t"
"s_waitcnt lgkmcnt(6)\n\t"
"ds_read_b64_tr_b16 v[238:239], v240 offset:42496\n\t"
"v_mfma_f32_16x16x32_bf16 v[104:107], v[216:219], a[176:179], v[104:107]\n\t"
"ds_read_b64_tr_b16 v[232:233], v241 offset:40960\n\t"
"v_mfma_f32_16x16x32_bf16 v[104:107], a[4:7], a[180:183], v[104:107]\n\t"
"s_waitcnt lgkmcnt(3)\n\t"
"s_barrier\n\t"
"v_mfma_f32_16x16x32_bf16 v[108:111], v[208:211], a[176:179], 0\n\t"
"s_add_u32 s29, s23, 0\n\t"
"ds_read_b64_tr_b16 v[234:235], v241 offset:42496\n\t"
"v_mfma_f32_16x16x32_bf16 v[108:111], v[216:219], a[180:183], v[108:111]\n\t"
"v_add_u32 v240, 59904, v240\n\t"
"v_add_u32 v241, 59904, v241\n\t"
"v_mfma_f32_16x16x32_bf16 v[108:111], a[4:7], a[184:187], v[108:111]\n\t"
"ds_write_b128 v242, v[224:227] offset:24576\n\t"
"v_mfma_f32_16x16x32_bf16 v[112:115], v[208:211], a[180:183], 0\n\t"
"v_mfma_f32_16x16x32_bf16 v[112:115], v[216:219], a[184:187], v[112:115]\n\t"
"ds_read_b64_tr_b16 v[224:225], v240 offset:1024\n\t"
"v_mfma_f32_16x16x32_bf16 v[112:115], a[4:7], a[188:191], v[112:115]\n\t"
"ds_write_b128 v242, v[228:231] offset:26112\n\t"
"v_mfma_f32_16x16x32_bf16 v[116:119], v[208:211], a[184:187], 0\n\t"
"v_mfma_f32_16x16x32_bf16 v[116:119], v[216:219], a[188:191], v[116:119]\n\t"
"ds_read_b64_tr_b16 v[226:227], v240 offset:2560\n\t"
"v_mfma_f32_16x16x32_bf16 v[116:119], a[4:7], a[192:195], v[116:119]\n\t"
"buffer_store_dwordx4 v[196:199], v248, s[16:19], s29 offen offset:0 nt\n\t"
"ds_read_b64_tr_b16 v[228:229], v241 offset:1024\n\t"
"ds_read_b64_tr_b16 v[230:231], v241 offset:2560\n\t"
"v_mfma_f32_16x16x32_bf16 v[120:123], v[208:211], a[188:191], 0\n\t"
"s_waitcnt lgkmcnt(8)\n\t"
"v_mfma_f32_16x16x32_bf16 v[120:123], v[216:219], a[192:195], v[120:123]\n\t"
"s_waitcnt lgkmcnt(6)\n\t"
"v_mfma_f32_16x16x32_bf16 v[120:123], a[4:7], a[196:199], v[120:123]\n\t"
"s_mov_b32 s20, 110592\n\t"
"v_mfma_f32_16x16x32_bf16 v[236:239], v[236:239], a[136:139], 0\n\t"
"v_mfma_f32_16x16x32_bf16 v[232:235], v[232:235], a[136:139], 0\n\t"
"v_mfma_f32_16x16x32_bf16 v[124:127], v[208:211], a[192:195], 0\n\t"
"v_mfma_f32_16x16x32_bf16 v[124:127], v[216:219], a[196:199], v[124:127]\n\t"
"v_mfma_f32_16x16x32_bf16 v[124:127], a[4:7], a[200:203], v[124:127]\n\t"
"v_mfma_f32_16x16x32_bf16 v[128:131], v[208:211], a[196:199], 0\n\t"
"buffer_load_dwordx4 v[208:211], v252, s[8:11], s20 offen offset:0 sc1\n\t"
"v_mfma_f32_16x16x32_bf16 v[128:131], v[216:219], a[200:203], v[128:131]\n\t"
"s_waitcnt lgkmcnt(0)\n\t"
"v_cvt_pk_bf16_f32 v236, v236, v237\n\t"
"v_cvt_pk_bf16_f32 v237, v238, v239\n\t"
"v_mfma_f32_16x16x32_bf16 v[128:131], a[4:7], a[204:207], v[128:131]\n\t"
"s_waitcnt vmcnt(35)\n\t"
"v_cvt_pk_bf16_f32 v238, v232, v233\n\t"
"v_cvt_pk_bf16_f32 v239, v234, v235\n\t"
"v_mfma_f32_16x16x32_bf16 v[224:227], v[224:227], a[136:139], 0\n\t"
"ds_write_b128 v242, v[236:239] offset:27648\n\t"
"v_mfma_f32_16x16x32_bf16 v[228:231], v[228:231], a[136:139], 0\n\t"
"buffer_load_dwordx4 v[216:219], v252, s[8:11], s20 offen offset:1024 sc1\n\t"
"v_mfma_f32_16x16x32_bf16 v[132:135], a[12:15], a[168:171], 0\n\t"
"ds_read_b64_tr_b16 v[232:233], v240 offset:20992\n\t"
"v_mfma_f32_16x16x32_bf16 v[132:135], a[208:211], a[172:175], v[132:135]\n\t"
"ds_read_b64_tr_b16 v[234:235], v240 offset:22528\n\t"
"v_mfma_f32_16x16x32_bf16 v[132:135], a[212:215], a[176:179], v[132:135]\n\t"
"buffer_store_dwordx4 v[204:207], v248, s[16:19], s29 offen offset:128 nt\n\t"
"buffer_load_dwordx4 a[4:7], v252, s[8:11], s20 offen offset:2048 sc1\n\t"
"ds_read_b64_tr_b16 v[236:237], v241 offset:20992\n\t"
"v_mfma_f32_16x16x32_bf16 v[136:139], a[12:15], a[172:175], 0\n\t"
"ds_read_b64_tr_b16 v[238:239], v241 offset:22528\n\t"
"v_cvt_pk_bf16_f32 v224, v224, v225\n\t"
"v_mfma_f32_16x16x32_bf16 v[136:139], a[208:211], a[176:179], v[136:139]\n\t"
"v_cvt_pk_bf16_f32 v225, v226, v227\n\t"
"v_cvt_pk_bf16_f32 v226, v228, v229\n\t"
"v_mfma_f32_16x16x32_bf16 v[136:139], a[212:215], a[180:183], v[136:139]\n\t"
"v_cvt_pk_bf16_f32 v227, v230, v231\n\t"
"v_mfma_f32_16x16x32_bf16 v[140:143], a[12:15], a[176:179], 0\n\t"
"ds_read_b64_tr_b16 v[228:229], v240 offset:40960\n\t"
"v_mfma_f32_16x16x32_bf16 v[140:143], a[208:211], a[180:183], v[140:143]\n\t"
"ds_read_b64_tr_b16 v[230:231], v240 offset:42496\n\t"
"v_mfma_f32_16x16x32_bf16 v[140:143], a[212:215], a[184:187], v[140:143]\n\t"
"ds_write_b128 v242, v[224:227] offset:29184\n\t"
"v_mfma_f32_16x16x32_bf16 v[144:147], a[12:15], a[180:183], 0\n\t"
"s_waitcnt lgkmcnt(5)\n\t"
"v_mfma_f32_16x16x32_bf16 v[144:147], a[208:211], a[184:187], v[144:147]\n\t"
"s_waitcnt lgkmcnt(3)\n\t"
"ds_read_b64_tr_b16 v[224:225], v241 offset:40960\n\t"
"v_mfma_f32_16x16x32_bf16 v[144:147], a[212:215], a[188:191], v[144:147]\n\t"
"ds_read_b64_tr_b16 v[226:227], v241 offset:42496\n\t"
"v_mfma_f32_16x16x32_bf16 v[232:235], v[232:235], a[136:139], 0\n\t"
"buffer_store_dwordx4 v[212:215], v248, s[16:19], s29 offen offset:256 nt\n\t"
"v_mfma_f32_16x16x32_bf16 v[236:239], v[236:239], a[136:139], 0\n\t"
"v_mfma_f32_16x16x32_bf16 v[148:151], a[12:15], a[184:187], 0\n\t"
"v_mfma_f32_16x16x32_bf16 v[148:151], a[208:211], a[188:191], v[148:151]\n\t"
"v_mfma_f32_16x16x32_bf16 v[148:151], a[212:215], a[192:195], v[148:151]\n\t"
"s_add_u32 s29, s23, 393216\n\t"
"v_mfma_f32_16x16x32_bf16 v[152:155], a[12:15], a[188:191], 0\n\t"
"s_waitcnt lgkmcnt(3)\n\t"
"v_cvt_pk_bf16_f32 v232, v232, v233\n\t"
"v_cvt_pk_bf16_f32 v233, v234, v235\n\t"
"v_mfma_f32_16x16x32_bf16 v[152:155], a[208:211], a[192:195], v[152:155]\n\t"
"s_waitcnt lgkmcnt(0)\n\t"
"ds_read_b128 v[196:199], v252 offset:0\n\t"
"v_cvt_pk_bf16_f32 v234, v236, v237\n\t"
"v_mfma_f32_16x16x32_bf16 v[152:155], a[212:215], a[196:199], v[152:155]\n\t"
"ds_read_b128 a[16:19], v252 offset:12288\n\t"
"v_cvt_pk_bf16_f32 v235, v238, v239\n\t"
"v_mfma_f32_16x16x32_bf16 v[228:231], v[228:231], a[136:139], 0\n\t"
"s_mov_b32 s20, 122880\n\t"
"ds_write_b128 v242, v[232:235] offset:30720\n\t"
"v_mfma_f32_16x16x32_bf16 v[224:227], v[224:227], a[136:139], 0\n\t"
"v_mfma_f32_16x16x32_bf16 v[156:159], a[12:15], a[192:195], 0\n\t"
"ds_read_b128 v[204:207], v252 offset:1024\n\t"
"v_mfma_f32_16x16x32_bf16 v[156:159], a[208:211], a[196:199], v[156:159]\n\t"
"ds_read_b128 a[24:27], v252 offset:13312\n\t"
"v_mfma_f32_16x16x32_bf16 v[156:159], a[212:215], a[200:203], v[156:159]\n\t"
"buffer_store_dwordx4 a[0:3], v248, s[16:19], s29 offen offset:0 nt\n\t"
"ds_read_b128 v[212:215], v252 offset:2048\n\t"
"ds_read_b128 a[32:35], v252 offset:14336\n\t"
"v_mfma_f32_16x16x32_bf16 v[160:163], a[12:15], a[196:199], 0\n\t"
"s_waitcnt vmcnt(35)\n\t"
"v_cvt_pk_bf16_f32 v228, v228, v229\n\t"
"v_cvt_pk_bf16_f32 v229, v230, v231\n\t"
"v_mfma_f32_16x16x32_bf16 v[160:163], a[208:211], a[200:203], v[160:163]\n\t"
"v_cvt_pk_bf16_f32 v230, v224, v225\n\t"
"v_cvt_pk_bf16_f32 v231, v226, v227\n\t"
"v_mfma_f32_16x16x32_bf16 v[160:163], a[212:215], a[204:207], v[160:163]\n\t"
"ds_write_b128 v242, v[228:231] offset:32256\n\t"
"v_mfma_f32_16x16x32_bf16 v[164:167], a[216:219], a[168:171], 0\n\t"
"buffer_load_dwordx4 a[208:211], v252, s[8:11], s20 offen offset:0 sc1\n\t"
"v_mfma_f32_16x16x32_bf16 v[164:167], a[220:223], a[172:175], v[164:167]\n\t"
"buffer_load_dwordx4 a[212:215], v252, s[8:11], s20 offen offset:1024 sc1\n\t"
"v_mfma_f32_16x16x32_bf16 v[164:167], a[224:227], a[176:179], v[164:167]\n\t"
"v_mfma_f32_16x16x32_bf16 v[168:171], a[216:219], a[172:175], 0\n\t"
"v_mfma_f32_16x16x32_bf16 v[168:171], a[220:223], a[176:179], v[168:171]\n\t"
"v_mfma_f32_16x16x32_bf16 v[168:171], a[224:227], a[180:183], v[168:171]\n\t"
"v_mfma_f32_16x16x32_bf16 v[172:175], a[216:219], a[176:179], 0\n\t"
"buffer_load_dwordx4 v[240:243], v252, s[8:11], s20 offen offset:2048 sc1\n\t"
"v_mfma_f32_16x16x32_bf16 v[172:175], a[220:223], a[180:183], v[172:175]\n\t"
"v_mfma_f32_16x16x32_bf16 v[172:175], a[224:227], a[184:187], v[172:175]\n\t"
"buffer_store_dwordx4 a[8:11], v248, s[16:19], s29 offen offset:128 nt\n\t"
"v_mfma_f32_16x16x32_bf16 v[176:179], a[216:219], a[180:183], 0\n\t"
"v_mfma_f32_16x16x32_bf16 v[176:179], a[220:223], a[184:187], v[176:179]\n\t"
"v_mfma_f32_16x16x32_bf16 v[176:179], a[224:227], a[188:191], v[176:179]\n\t"
"s_waitcnt lgkmcnt(0)\n\t"
"s_barrier\n\t"
"v_mfma_f32_16x16x32_bf16 v[180:183], a[216:219], a[184:187], 0\n\t"
"ds_read_b128 a[168:171], v244 offset:24576\n\t"
"v_mfma_f32_16x16x32_bf16 v[180:183], a[220:223], a[188:191], v[180:183]\n\t"
"Lpg14_%=:\n\t"
"s_cbranch_vccnz Lvr1_%=\n\t"
"ds_read_b128 a[172:175], v244 offset:25088\n\t"
"v_mfma_f32_16x16x32_bf16 v[180:183], a[224:227], a[192:195], v[180:183]\n\t"
"ds_read_b128 a[176:179], v244 offset:25600\n\t"
"v_mfma_f32_16x16x32_bf16 v[184:187], a[216:219], a[188:191], 0\n\t"
"s_mov_b32 s20, 135168\n\t"
"ds_read_b128 a[180:183], v244 offset:26112\n\t"
"v_mfma_f32_16x16x32_bf16 v[184:187], a[220:223], a[192:195], v[184:187]\n\t"
"ds_read_b128 a[184:187], v244 offset:26624\n\t"
"v_mfma_f32_16x16x32_bf16 v[184:187], a[224:227], a[196:199], v[184:187]\n\t"
"ds_read_b128 a[188:191], v244 offset:27136\n\t"
"v_mfma_f32_16x16x32_bf16 v[188:191], a[216:219], a[192:195], 0\n\t"
"ds_read_b128 a[192:195], v244 offset:27648\n\t"
"v_mfma_f32_16x16x32_bf16 v[188:191], a[220:223], a[196:199], v[188:191]\n\t"
"v_mfma_f32_16x16x32_bf16 v[188:191], a[224:227], a[200:203], v[188:191]\n\t"
"ds_write_b128 v249, a[44:47] offset:0\n\t"
"v_mfma_f32_16x16x32_bf16 v[192:195], a[216:219], a[196:199], 0\n\t"
"buffer_load_dwordx4 a[216:219], v252, s[8:11], s20 offen offset:0 sc1\n\t"
"v_mfma_f32_16x16x32_bf16 v[192:195], a[220:223], a[200:203], v[192:195]\n\t"
"s_waitcnt vmcnt(35) lgkmcnt(5)\n\t"
"ds_read_b128 a[196:199], v244 offset:28160\n\t"
"v_mfma_f32_16x16x32_bf16 v[192:195], a[224:227], a[204:207], v[192:195]\n\t"
"ds_write_b128 v249, a[60:63] offset:12288\n\t"
"Lst19_%=:\n\t"
"v_mfma_f32_16x16x32_bf16 v[2:5], a[228:231], a[168:171], v[2:5]\n\t"
"buffer_load_dwordx4 a[220:223], v252, s[8:11], s20 offen offset:1024 sc1\n\t"
"v_mfma_f32_16x16x32_bf16 v[2:5], a[240:243], a[172:175], v[2:5]\n\t"
"s_waitcnt lgkmcnt(6)\n\t"
"ds_read_b128 a[200:203], v244 offset:28672\n\t"
"v_mfma_f32_16x16x32_bf16 v[2:5], a[232:235], a[176:179], v[2:5]\n\t"
"buffer_store_dwordx4 a[40:43], v248, s[16:19], s29 offen offset:256 nt\n\t"
"ds_read_b128 a[204:207], v244 offset:29184\n\t"
"v_mfma_f32_16x16x32_bf16 v[6:9], a[228:231], a[172:175], v[6:9]\n\t"
"s_waitcnt lgkmcnt(7)\n\t"
"s_add_u32 s29, s23, 786432\n\t"
"v_mfma_f32_16x16x32_bf16 v[6:9], a[240:243], a[176:179], v[6:9]\n\t"
"buffer_load_dwordx4 a[224:227], v252, s[8:11], s20 offen offset:2048 sc1\n\t"
"v_mfma_f32_16x16x32_bf16 v[6:9], a[232:235], a[180:183], v[6:9]\n\t"
"ds_write_b64 v250, a[110:111] offset:0\n\t"
"v_mfma_f32_16x16x32_bf16 v[10:13], a[228:231], a[176:179], v[10:13]\n\t"
"s_waitcnt lgkmcnt(7)\n\t"
"v_mfma_f32_16x16x32_bf16 v[10:13], a[240:243], a[180:183], v[10:13]\n\t"
"v_mfma_f32_16x16x32_bf16 v[10:13], a[232:235], a[184:187], v[10:13]\n\t"
"ds_write_b64 v250, a[126:127] offset:12288\n\t"
"v_mfma_f32_16x16x32_bf16 v[14:17], a[228:231], a[180:183], v[14:17]\n\t"
"s_waitcnt lgkmcnt(7)\n\t"
"v_mfma_f32_16x16x32_bf16 v[14:17], a[240:243], a[184:187], v[14:17]\n\t"
"v_mfma_f32_16x16x32_bf16 v[14:17], a[232:235], a[188:191], v[14:17]\n\t"
"buffer_store_dwordx4 v[196:199], v248, s[16:19], s29 offen offset:0 nt\n\t"
"ds_write_b64 v250, a[118:119] offset:6144\n\t"
"v_mfma_f32_16x16x32_bf16 v[18:21], a[228:231], a[184:187], v[18:21]\n\t"
"s_waitcnt lgkmcnt(6)\n\t"
"v_mfma_f32_16x16x32_bf16 v[18:21], a[240:243], a[188:191], v[18:21]\n\t"
"v_mfma_f32_16x16x32_bf16 v[18:21], a[232:235], a[192:195], v[18:21]\n\t"
"ds_write_b128 v249, a[52:55] offset:6144\n\t"
"v_mfma_f32_16x16x32_bf16 v[22:25], a[228:231], a[188:191], v[22:25]\n\t"
"s_waitcnt lgkmcnt(5)\n\t"
"s_mov_b32 s20, 147456\n\t"
"v_mfma_f32_16x16x32_bf16 v[22:25], a[240:243], a[192:195], v[22:25]\n\t"
"v_mfma_f32_16x16x32_bf16 v[22:25], a[232:235], a[196:199], v[22:25]\n\t"
"ds_write_b128 v249, a[68:71] offset:18432\n\t"
"v_mfma_f32_16x16x32_bf16 v[26:29], a[228:231], a[192:195], v[26:29]\n\t"
"s_waitcnt lgkmcnt(5)\n\t"
"v_mfma_f32_16x16x32_bf16 v[26:29], a[240:243], a[196:199], v[26:29]\n\t"
"v_mfma_f32_16x16x32_bf16 v[26:29], a[232:235], a[200:203], v[26:29]\n\t"
"ds_write_b64 v250, a[134:135] offset:18432\n\t"
"v_mfma_f32_16x16x32_bf16 v[30:33], a[228:231], a[196:199], v[30:33]\n\t"
"buffer_load_dwordx4 a[228:231], v252, s[8:11], s20 offen offset:0 sc1\n\t"
"v_mfma_f32_16x16x32_bf16 v[30:33], a[240:243], a[200:203], v[30:33]\n\t"
"s_waitcnt vmcnt(36)\n\t"
"v_mfma_f32_16x16x32_bf16 v[30:33], a[232:235], a[204:207], v[30:33]\n\t"
"buffer_store_dwordx4 v[204:207], v248, s[16:19], s29 offen offset:128 nt\n\t"
"v_mfma_f32_16x16x32_bf16 v[34:37], a[244:247], a[168:171], v[34:37]\n\t"
"v_mfma_f32_16x16x32_bf16 v[34:37], a[248:251], a[172:175], v[34:37]\n\t"
"buffer_load_dwordx4 a[232:235], v252, s[8:11], s20 offen offset:1024 sc1\n\t"
"v_mfma_f32_16x16x32_bf16 v[34:37], a[48:51], a[176:179], v[34:37]\n\t"
"ds_read_b64_tr_b16 v[236:237], v246 offset:512\n\t"
"v_mfma_f32_16x16x32_bf16 v[38:41], a[244:247], a[172:175], v[38:41]\n\t"
"buffer_load_dwordx4 a[240:243], v252, s[8:11], s20 offen offset:2048 sc1\n\t"
"v_mfma_f32_16x16x32_bf16 v[38:41], a[248:251], a[176:179], v[38:41]\n\t"
"ds_read_b64_tr_b16 v[238:239], v246 offset:3584\n\t"
"v_mfma_f32_16x16x32_bf16 v[38:41], a[48:51], a[180:183], v[38:41]\n\t"
"v_mfma_f32_16x16x32_bf16 v[42:45], a[244:247], a[176:179], v[42:45]\n\t"
"v_mfma_f32_16x16x32_bf16 v[42:45], a[248:251], a[180:183], v[42:45]\n\t"
"v_mfma_f32_16x16x32_bf16 v[42:45], a[48:51], a[184:187], v[42:45]\n\t"
"v_mfma_f32_16x16x32_bf16 v[46:49], a[244:247], a[180:183], v[46:49]\n\t"
"ds_read_b64_tr_b16 v[232:233], v246 offset:6656\n\t"
"v_mfma_f32_16x16x32_bf16 v[46:49], a[248:251], a[184:187], v[46:49]\n\t"
"ds_read_b64_tr_b16 v[234:235], v246 offset:9728\n\t"
"v_mfma_f32_16x16x32_bf16 v[46:49], a[48:51], a[188:191], v[46:49]\n\t"
"s_waitcnt lgkmcnt(4)\n\t"
"s_barrier\n\t"
"v_mfma_f32_16x16x32_bf16 v[50:53], a[244:247], a[184:187], v[50:53]\n\t"
"v_mfma_f32_16x16x32_bf16 v[50:53], a[248:251], a[188:191], v[50:53]\n\t"
"v_mfma_f32_16x16x32_bf16 v[50:53], a[48:51], a[192:195], v[50:53]\n\t"
"buffer_store_dwordx4 v[212:215], v248, s[16:19], s29 offen offset:256 nt\n\t"
"v_mfma_f32_16x16x32_bf16 v[54:57], a[244:247], a[188:191], v[54:57]\n\t"
"s_waitcnt lgkmcnt(2)\n\t"
"v_mfma_f32_16x16x32_bf16 v[54:57], a[248:251], a[192:195], v[54:57]\n\t"
"s_mov_b32 s20, 159744\n\t"
"v_mfma_f32_16x16x32_bf16 v[54:57], a[48:51], a[196:199], v[54:57]\n\t"
"v_mfma_f32_16x16x32_bf16 v[236:239], v[236:239], v[220:223], 0\n\t"
"s_add_u32 s29, s23, 1179648\n\t"
"v_mfma_f32_16x16x32_bf16 v[58:61], a[244:247], a[192:195], v[58:61]\n\t"
"ds_read_b64_tr_b16 v[224:225], v246 offset:12800\n\t"
"v_mfma_f32_16x16x32_bf16 v[58:61], a[248:251], a[196:199], v[58:61]\n\t"
"ds_read_b64_tr_b16 v[226:227], v246 offset:15872\n\t"
"v_mfma_f32_16x16x32_bf16 v[58:61], a[48:51], a[200:203], v[58:61]\n\t"
"v_mfma_f32_16x16x32_bf16 v[62:65], a[244:247], a[196:199], v[62:65]\n\t"
"buffer_load_dwordx4 a[244:247], v252, s[8:11], s20 offen offset:0 sc1\n\t"
"v_mfma_f32_16x16x32_bf16 v[62:65], a[248:251], a[200:203], v[62:65]\n\t"
"s_waitcnt vmcnt(37) lgkmcnt(2)\n\t"
"v_cvt_pk_bf16_f32 v236, v236, v237\n\t"
"v_cvt_pk_bf16_f32 v237, v238, v239\n\t"
"v_mfma_f32_16x16x32_bf16 v[62:65], a[48:51], a[204:207], v[62:65]\n\t"
"v_mfma_f32_16x16x32_bf16 v[232:235], v[232:235], v[220:223], 0\n\t"
"buffer_load_dwordx4 a[248:251], v252, s[8:11], s20 offen offset:1024 sc1\n\t"
"v_mfma_f32_16x16x32_bf16 v[66:69], a[56:59], a[168:171], v[66:69]\n\t"
"ds_read_b64_tr_b16 v[228:229], v246 offset:20480\n\t"
"v_mfma_f32_16x16x32_bf16 v[66:69], a[64:67], a[172:175], v[66:69]\n\t"
"ds_read_b64_tr_b16 v[230:231], v246 offset:23552\n\t"
"v_mfma_f32_16x16x32_bf16 v[66:69], a[144:147], a[176:179], v[66:69]\n\t"
"buffer_store_dwordx4 a[16:19], v248, s[16:19], s29 offen offset:0 nt\n\t"
"ds_read_b128 a[168:171], v244 offset:33792\n\t"
"v_mfma_f32_16x16x32_bf16 v[70:73], a[56:59], a[172:175], v[70:73]\n\t"
"ds_read_b128 a[172:175], v244 offset:34304\n\t"
"v_mfma_f32_16x16x32_bf16 v[70:73], a[64:67], a[176:179], v[70:73]\n\t"
"v_cvt_pk_bf16_f32 v232, v232, v233\n\t"
"v_cvt_pk_bf16_f32 v233, v234, v235\n\t"
"v_mfma_f32_16x16x32_bf16 v[70:73], a[144:147], a[180:183], v[70:73]\n\t"
"v_mfma_f32_16x16x32_bf16 v[74:77], a[56:59], a[176:179], v[74:77]\n\t"
"s_waitcnt lgkmcnt(4)\n\t"
"ds_read_b128 a[176:179], v244 offset:34816\n\t"
"v_mfma_f32_16x16x32_bf16 v[74:77], a[64:67], a[180:183], v[74:77]\n\t"
"v_mfma_f32_16x16x32_bf16 v[74:77], a[144:147], a[184:187], v[74:77]\n\t"
"v_mfma_f32_16x16x32_bf16 v[224:227], v[224:227], v[220:223], 0\n\t"
"v_mfma_f32_16x16x32_bf16 v[78:81], a[56:59], a[180:183], v[78:81]\n\t"
"ds_read_b128 a[180:183], v244 offset:35328\n\t"
"v_mfma_f32_16x16x32_bf16 v[78:81], a[64:67], a[184:187], v[78:81]\n\t"
"ds_read_b128 v[196:199], v252 offset:0\n\t"
"v_mfma_f32_16x16x32_bf16 v[78:81], a[144:147], a[188:191], v[78:81]\n\t"
"buffer_store_dwordx4 a[24:27], v248, s[16:19], s29 offen offset:128 nt\n\t"
"ds_read_b128 a[40:43], v252 offset:12288\n\t"
"ds_read_b128 v[204:207], v252 offset:1024\n\t"
"v_mfma_f32_16x16x32_bf16 v[82:85], a[56:59], a[184:187], v[82:85]\n\t"
"s_waitcnt lgkmcnt(7)\n\t"
"ds_read_b128 a[184:187], v244 offset:35840\n\t"
"v_mfma_f32_16x16x32_bf16 v[82:85], a[64:67], a[188:191], v[82:85]\n\t"
"ds_read_b128 a[72:75], v252 offset:13312\n\t"
"v_cvt_pk_bf16_f32 v224, v224, v225\n\t"
"v_mfma_f32_16x16x32_bf16 v[82:85], a[144:147], a[192:195], v[82:85]\n\t"
"ds_read_b128 a[8:11], v252 offset:2048\n\t"
"v_cvt_pk_bf16_f32 v225, v226, v227\n\t"
"v_mfma_f32_16x16x32_bf16 v[228:231], v[228:231], v[220:223], 0\n\t"
"ds_read_b128 a[104:107], v252 offset:14336\n\t"
"v_mfma_f32_16x16x32_bf16 v[86:89], a[56:59], a[188:191], v[86:89]\n\t"
"ds_read_b128 a[188:191], v244 offset:36352\n\t"
"v_mfma_f32_16x16x32_bf16 v[86:89], a[64:67], a[192:195], v[86:89]\n\t"
"v_mfma_f32_16x16x32_bf16 v[86:89], a[144:147], a[196:199], v[86:89]\n\t"
"v_mfma_f32_16x16x32_bf16 v[90:93], a[56:59], a[192:195], v[90:93]\n\t"
"ds_read_b128 a[192:195], v244 offset:36864\n\t"
"v_mfma_f32_16x16x32_bf16 v[90:93], a[64:67], a[196:199], v[90:93]\n\t"
"v_cvt_pk_bf16_f32 v228, v228, v229\n\t"
"v_cvt_pk_bf16_f32 v229, v230, v231\n\t"
"v_mfma_f32_16x16x32_bf16 v[90:93], a[144:147], a[200:203], v[90:93]\n\t"
"v_mfma_f32_16x16x32_bf16 v[94:97], a[56:59], a[196:199], v[94:97]\n\t"
"s_waitcnt vmcnt(35) lgkmcnt(10)\n\t"
"ds_read_b128 a[196:199], v244 offset:37376\n\t"
"v_mfma_f32_16x16x32_bf16 v[94:97], a[64:67], a[200:203], v[94:97]\n\t"
"ds_read_b128 a[200:203], v244 offset:37888\n\t"
"v_mfma_f32_16x16x32_bf16 v[94:97], a[144:147], a[204:207], v[94:97]\n\t"
"ds_read_b128 a[204:207], v244 offset:38400\n\t"
"v_mfma_f32_16x16x32_bf16 v[100:103], a[20:23], a[168:171], v[100:103]\n\t"
"s_waitcnt lgkmcnt(12)\n\t"
"v_mfma_f32_16x16x32_bf16 v[100:103], a[252:255], a[172:175], v[100:103]\n\t"
"v_mfma_f32_16x16x32_bf16 v[100:103], a[236:239], a[176:179], v[100:103]\n\t"
"buffer_store_dwordx4 a[32:35], v248, s[16:19], s29 offen offset:256 nt\n\t"
"v_mfma_f32_16x16x32_bf16 v[104:107], a[20:23], a[172:175], v[104:107]\n\t"
"s_waitcnt lgkmcnt(8)\n\t"
"v_mfma_f32_16x16x32_bf16 v[104:107], a[252:255], a[176:179], v[104:107]\n\t"
"v_mfma_f32_16x16x32_bf16 v[104:107], a[236:239], a[180:183], v[104:107]\n\t"
"s_waitcnt lgkmcnt(0)\n\t"
"s_barrier\n\t"
"v_mfma_f32_16x16x32_bf16 v[108:111], a[20:23], a[176:179], v[108:111]\n\t"
"s_add_u32 s29, s23, 1152\n\t"
"v_fma_f32 v0, v2, s52, v100\n\t"
"v_mfma_f32_16x16x32_bf16 v[108:111], a[252:255], a[180:183], v[108:111]\n\t"
"v_fma_f32 v1, v3, s52, v101\n\t"
"v_mfma_f32_16x16x32_bf16 v[108:111], a[236:239], a[184:187], v[108:111]\n\t"
"ds_write_b64 v245, v[236:237] offset:24576\n\t"
"v_mfma_f32_16x16x32_bf16 v[112:115], a[20:23], a[180:183], v[112:115]\n\t"
"ds_read_b64_tr_b16 v[236:237], v246 offset:26624\n\t"
"v_mfma_f32_16x16x32_bf16 v[112:115], a[252:255], a[184:187], v[112:115]\n\t"
"ds_read_b64_tr_b16 v[238:239], v246 offset:29696\n\t"
"v_mfma_f32_16x16x32_bf16 v[112:115], a[236:239], a[188:191], v[112:115]\n\t"
"ds_write_b64 v245, v[232:233] offset:25088\n\t"
"v_mfma_f32_16x16x32_bf16 v[116:119], a[20:23], a[184:187], v[116:119]\n\t"
"ds_read_b64_tr_b16 v[232:233], v246 offset:32768\n\t"
"v_fma_f32 v212, -v10, s56, -v108\n\t"
"v_mfma_f32_16x16x32_bf16 v[116:119], a[252:255], a[188:191], v[116:119]\n\t"
"ds_read_b64_tr_b16 v[234:235], v246 offset:35840\n\t"
"v_fma_f32 v213, -v11, s56, -v109\n\t"
"v_mfma_f32_16x16x32_bf16 v[116:119], a[236:239], a[192:195], v[116:119]\n\t"
"ds_write_b64 v245, v[224:225] offset:25600\n\t"
"v_mfma_f32_16x16x32_bf16 v[120:123], a[20:23], a[188:191], v[120:123]\n\t"
"s_waitcnt lgkmcnt(4)\n\t"
"ds_read_b64_tr_b16 v[224:225], v246 offset:40448\n\t"
"v_fma_f32 v214, -v12, s56, -v110\n\t"
"v_mfma_f32_16x16x32_bf16 v[120:123], a[252:255], a[192:195], v[120:123]\n\t"
"ds_read_b64_tr_b16 v[226:227], v246 offset:43520\n\t"
"v_fma_f32 v215, -v13, s56, -v111\n\t"
"v_mfma_f32_16x16x32_bf16 v[120:123], a[236:239], a[196:199], v[120:123]\n\t"
"ds_write_b64 v245, v[228:229] offset:26112\n\t"
"v_mfma_f32_16x16x32_bf16 v[236:239], v[236:239], v[220:223], 0\n\t"
"buffer_store_dwordx4 v[196:199], v248, s[16:19], s29 offen offset:0 nt\n\t"
"ds_read_b64_tr_b16 v[228:229], v246 offset:46592\n\t"
"ds_read_b64_tr_b16 v[230:231], v246 offset:49664\n\t"
"v_fma_f32 v98, -v14, s56, -v112\n\t"
"v_mfma_f32_16x16x32_bf16 v[124:127], a[20:23], a[192:195], v[124:127]\n\t"
"s_waitcnt lgkmcnt(6)\n\t"
"v_fma_f32 v99, -v15, s56, -v113\n\t"
"v_accvgpr_write_b32 a0, v98\n\t"
"v_mfma_f32_16x16x32_bf16 v[124:127], a[252:255], a[196:199], v[124:127]\n\t"
"v_fma_f32 v196, -v2, s56, -v100\n\t"
"v_fma_f32 v197, -v3, s56, -v101\n\t"
"v_mfma_f32_16x16x32_bf16 v[124:127], a[236:239], a[200:203], v[124:127]\n\t"
"ds_write_b128 v249, a[76:79] offset:0\n\t"
"v_mfma_f32_16x16x32_bf16 v[232:235], v[232:235], v[220:223], 0\n\t"
"ds_write_b128 v249, a[92:95] offset:12288\n\t"
"v_mfma_f32_16x16x32_bf16 v[128:131], a[20:23], a[196:199], v[128:131]\n\t"
"s_waitcnt lgkmcnt(5)\n\t"
"v_cvt_pk_bf16_f32 v236, v236, v237\n\t"
"v_cvt_pk_bf16_f32 v237, v238, v239\n\t"
"v_mfma_f32_16x16x32_bf16 v[128:131], a[252:255], a[200:203], v[128:131]\n\t"
"s_waitcnt vmcnt(32)\n\t"
"v_fma_f32 v198, -v4, s56, -v102\n\t"
"v_fma_f32 v199, -v5, s56, -v103\n\t"
"v_mfma_f32_16x16x32_bf16 v[128:131], a[236:239], a[204:207], v[128:131]\n\t"
"ds_write_b64 v250, a[142:143] offset:0\n\t"
"v_mfma_f32_16x16x32_bf16 v[224:227], v[224:227], v[220:223], 0\n\t"
"buffer_load_dwordx4 a[252:255], v252, s[8:11], s20 offen offset:2048 sc1\n\t"
"s_mov_b32 s20, 172032\n\t"
"v_mfma_f32_16x16x32_bf16 v[132:135], a[152:155], a[168:171], v[132:135]\n\t"
"buffer_load_dwordx4 a[236:239], v252, s[8:11], s20 offen offset:0 sc1\n\t"
"v_mfma_f32_16x16x32_bf16 v[132:135], a[28:31], a[172:175], v[132:135]\n\t"
"s_waitcnt lgkmcnt(3)\n\t"
"v_cvt_pk_bf16_f32 v232, v232, v233\n\t"
"v_cvt_pk_bf16_f32 v233, v234, v235\n\t"
"v_mfma_f32_16x16x32_bf16 v[132:135], a[160:163], a[176:179], v[132:135]\n\t"
"ds_write_b64 v250, a[158:159] offset:12288\n\t"
"v_mfma_f32_16x16x32_bf16 v[228:231], v[228:231], v[220:223], 0\n\t"
"ds_write_b64 v245, v[236:237] offset:26624\n\t"
"v_mfma_f32_16x16x32_bf16 v[136:139], a[152:155], a[172:175], v[136:139]\n\t"
"ds_read_b64_tr_b16 v[236:237], v246 offset:52736\n\t"
"v_cvt_pk_bf16_f32 v224, v224, v225\n\t"
"v_mfma_f32_16x16x32_bf16 v[136:139], a[28:31], a[176:179], v[136:139]\n\t"
"ds_read_b64_tr_b16 v[238:239], v246 offset:55808\n\t"
"v_cvt_pk_bf16_f32 v225, v226, v227\n\t"
"v_mfma_f32_16x16x32_bf16 v[136:139], a[160:163], a[180:183], v[136:139]\n\t"
"buffer_store_dwordx4 v[204:207], v248, s[16:19], s29 offen offset:128 nt\n\t"
"ds_write_b64 v245, v[232:233] offset:27136\n\t"
"v_fma_f32 v98, -v16, s56, -v114\n\t"
"v_mfma_f32_16x16x32_bf16 v[140:143], a[152:155], a[176:179], v[140:143]\n\t"
"v_fma_f32 v204, -v6, s56, -v104\n\t"
"v_fma_f32 v205, -v7, s56, -v105\n\t"
"v_mfma_f32_16x16x32_bf16 v[140:143], a[28:31], a[180:183], v[140:143]\n\t"
"v_cvt_pk_bf16_f32 v228, v228, v229\n\t"
"v_cvt_pk_bf16_f32 v229, v230, v231\n\t"
"v_mfma_f32_16x16x32_bf16 v[140:143], a[160:163], a[184:187], v[140:143]\n\t"
"ds_write_b64 v245, v[224:225] offset:27648\n\t"
"v_mfma_f32_16x16x32_bf16 v[144:147], a[152:155], a[180:183], v[144:147]\n\t"
"s_waitcnt lgkmcnt(2)\n\t"
"v_fma_f32 v206, -v8, s56, -v106\n\t"
"v_fma_f32 v207, -v9, s56, -v107\n\t"
"v_mfma_f32_16x16x32_bf16 v[144:147], a[28:31], a[184:187], v[144:147]\n\t"
"v_accvgpr_write_b32 a1, v99\n\t"
"v_fma_f32 v99, -v17, s56, -v115\n\t"
"v_mfma_f32_16x16x32_bf16 v[144:147], a[160:163], a[188:191], v[144:147]\n\t"
"ds_write_b128 v249, a[84:87] offset:6144\n\t"
"v_mfma_f32_16x16x32_bf16 v[236:239], v[236:239], v[220:223], 0\n\t"
"buffer_store_dwordx4 a[8:11], v248, s[16:19], s29 offen offset:256 nt\n\t"
"v_accvgpr_write_b32 a2, v98\n\t"
"v_accvgpr_write_b32 a3, v99\n\t"
"v_mfma_f32_16x16x32_bf16 v[148:151], a[152:155], a[184:187], v[148:151]\n\t"
"v_mfma_f32_16x16x32_bf16 v[148:151], a[28:31], a[188:191], v[148:151]\n\t"
"v_mfma_f32_16x16x32_bf16 v[148:151], a[160:163], a[192:195], v[148:151]\n\t"
"ds_write_b128 v249, a[100:103] offset:18432\n\t"
"v_mfma_f32_16x16x32_bf16 v[152:155], a[152:155], a[188:191], v[152:155]\n\t"
"s_add_u32 s29, s23, 394368\n\t"
"v_mfma_f32_16x16x32_bf16 v[152:155], a[28:31], a[192:195], v[152:155]\n\t"
"v_cvt_pk_bf16_f32 v236, v236, v237\n\t"
"v_cvt_pk_bf16_f32 v237, v238, v239\n\t"
"v_mfma_f32_16x16x32_bf16 v[152:155], a[160:163], a[196:199], v[152:155]\n\t"
"ds_write_b64 v245, v[228:229] offset:28160\n\t"
"v_mfma_f32_16x16x32_bf16 v[156:159], a[152:155], a[192:195], v[156:159]\n\t"
"v_fma_f32 v98, -v18, s56, -v116\n\t"
"v_fma_f32 v99, -v19, s56, -v117\n\t"
"v_mfma_f32_16x16x32_bf16 v[156:159], a[28:31], a[196:199], v[156:159]\n\t"
"v_accvgpr_write_b32 a8, v98\n\t"
"v_fma_f32 v98, -v20, s56, -v118\n\t"
"v_mfma_f32_16x16x32_bf16 v[156:159], a[160:163], a[200:203], v[156:159]\n\t"
"ds_write_b64 v250, a[150:151] offset:6144\n\t"
"v_mfma_f32_16x16x32_bf16 v[160:163], a[152:155], a[196:199], v[160:163]\n\t"
"s_waitcnt vmcnt(31)\n\t"
"v_accvgpr_write_b32 a9, v99\n\t"
"v_fma_f32 v99, -v21, s56, -v119\n\t"
"v_mfma_f32_16x16x32_bf16 v[160:163], a[28:31], a[200:203], v[160:163]\n\t"
"v_accvgpr_write_b32 a10, v98\n\t"
"v_accvgpr_write_b32 a11, v99\n\t"
"v_mfma_f32_16x16x32_bf16 v[160:163], a[160:163], a[204:207], v[160:163]\n\t"
"ds_write_b64 v250, a[166:167] offset:18432\n\t"
"v_mfma_f32_16x16x32_bf16 v[164:167], a[36:39], a[168:171], v[164:167]\n\t"
"v_fma_f32 v98, -v22, s56, -v120\n\t"
"v_fma_f32 v99, -v23, s56, -v121\n\t"
"v_mfma_f32_16x16x32_bf16 v[164:167], a[112:115], a[172:175], v[164:167]\n\t"
"v_accvgpr_write_b32 a16, v98\n\t"
"v_mfma_f32_16x16x32_bf16 v[164:167], v[200:203], a[176:179], v[164:167]\n\t"
"ds_write_b64 v245, v[236:237] offset:28672\n\t"
"s_waitcnt lgkmcnt(0)\n\t"
"s_barrier\n\t"
"v_mfma_f32_16x16x32_bf16 v[168:171], a[36:39], a[172:175], v[168:171]\n\t"
"ds_read_b128 a[168:171], v244 offset:24576\n\t"
"v_mfma_f32_16x16x32_bf16 v[168:171], a[112:115], a[176:179], v[168:171]\n\t"
"buffer_load_dwordx4 v[236:239], v252, s[8:11], s20 offen offset:1024 sc1\n\t"
"v_mfma_f32_16x16x32_bf16 v[168:171], v[200:203], a[180:183], v[168:171]\n\t"
"buffer_store_dwordx4 a[40:43], v248, s[16:19], s29 offen offset:0 nt\n\t"
"ds_read_b128 a[172:175], v244 offset:25088\n\t"
"v_fma_f32 v98, -v24, s56, -v122\n\t"
"v_accvgpr_write_b32 a17, v99\n\t"
"v_fma_f32 v99, -v25, s56, -v123\n\t"
"v_mfma_f32_16x16x32_bf16 v[172:175], a[36:39], a[176:179], v[172:175]\n\t"
"ds_read_b128 a[176:179], v244 offset:25600\n\t"
"v_accvgpr_write_b32 a18, v98\n\t"
"v_mfma_f32_16x16x32_bf16 v[172:175], a[112:115], a[180:183], v[172:175]\n\t"
"v_accvgpr_write_b32 a19, v99\n\t"
"v_fma_f32 v98, -v26, s56, -v124\n\t"
"v_mfma_f32_16x16x32_bf16 v[172:175], v[200:203], a[184:187], v[172:175]\n\t"
"v_fma_f32 v99, -v27, s56, -v125\n\t"
"v_accvgpr_write_b32 a24, v98\n\t"
"v_mfma_f32_16x16x32_bf16 v[176:179], a[36:39], a[180:183], v[176:179]\n\t"
"ds_read_b128 a[180:183], v244 offset:26112\n\t"
"v_fma_f32 v98, -v28, s56, -v126\n\t"
"v_mfma_f32_16x16x32_bf16 v[176:179], a[112:115], a[184:187], v[176:179]\n\t"
"v_accvgpr_write_b32 a25, v99\n\t"
"v_fma_f32 v99, -v29, s56, -v127\n\t"
"v_mfma_f32_16x16x32_bf16 v[176:179], v[200:203], a[188:191], v[176:179]\n\t"
"v_accvgpr_write_b32 a26, v98\n\t"
"v_accvgpr_write_b32 a27, v99\n\t"
"v_mfma_f32_16x16x32_bf16 v[180:183], a[36:39], a[184:187], v[180:183]\n\t"
"ds_read_b128 a[184:187], v244 offset:26624\n\t"
"v_fma_f32 v98, -v30, s56, -v128\n\t"
"v_mfma_f32_16x16x32_bf16 v[180:183], a[112:115], a[188:191], v[180:183]\n\t"
"v_fma_f32 v99, -v31, s56, -v129\n\t"
"v_accvgpr_write_b32 a32, v98\n\t"
"v_mfma_f32_16x16x32_bf16 v[180:183], v[200:203], a[192:195], v[180:183]\n\t"
"v_fma_f32 v98, -v32, s56, -v130\n\t"
"v_accvgpr_write_b32 a33, v99\n\t"
"v_mfma_f32_16x16x32_bf16 v[184:187], a[36:39], a[188:191], v[184:187]\n\t"
"ds_read_b128 a[188:191], v244 offset:27136\n\t"
"v_fma_f32 v99, -v33, s56, -v131\n\t"
"v_mfma_f32_16x16x32_bf16 v[184:187], a[112:115], a[192:195], v[184:187]\n\t"
"v_accvgpr_write_b32 a34, v98\n\t"
"v_accvgpr_write_b32 a35, v99\n\t"
"v_mfma_f32_16x16x32_bf16 v[184:187], v[200:203], a[196:199], v[184:187]\n\t"
"buffer_store_dwordx4 a[72:75], v248, s[16:19], s29 offen offset:128 nt\n\t"
"v_fma_f32 v98, -v34, s56, -v132\n\t"
"v_fma_f32 v99, -v35, s56, -v133\n\t"
"v_accvgpr_write_b32 a40, v98\n\t"
"v_fma_f32 v98, -v36, s56, -v134\n\t"
"v_mfma_f32_16x16x32_bf16 v[188:191], a[36:39], a[192:195], v[188:191]\n\t"
"ds_read_b128 a[192:195], v244 offset:27648\n\t"
"v_accvgpr_write_b32 a41, v99\n\t"
"v_mfma_f32_16x16x32_bf16 v[188:191], a[112:115], a[196:199], v[188:191]\n\t"
"ds_read_b128 a[136:139], v252 offset:0\n\t"
"v_fma_f32 v99, -v37, s56, -v135\n\t"
"v_mfma_f32_16x16x32_bf16 v[188:191], v[200:203], a[200:203], v[188:191]\n\t"
"v_accvgpr_write_b32 a42, v98\n\t"
"v_accvgpr_write_b32 a43, v99\n\t"
"v_mfma_f32_16x16x32_bf16 v[192:195], a[36:39], a[196:199], v[192:195]\n\t"
"s_waitcnt vmcnt(29) lgkmcnt(5)\n\t"
"ds_read_b128 a[196:199], v244 offset:28160\n\t"
"v_fma_f32 v98, -v38, s56, -v136\n\t"
"v_mfma_f32_16x16x32_bf16 v[192:195], a[112:115], a[200:203], v[192:195]\n\t"
"ds_read_b128 a[200:203], v244 offset:28672\n\t"
"v_fma_f32 v99, -v39, s56, -v137\n\t"
"v_mfma_f32_16x16x32_bf16 v[192:195], v[200:203], a[204:207], v[192:195]\n\t"
"ds_read_b128 a[204:207], v244 offset:29184\n\t"
"v_accvgpr_write_b32 a48, v98\n\t"
"Lst20_%=:\n\t"
"v_mfma_f32_16x16x32_bf16 v[196:199], v[208:211], a[168:171], v[196:199]\n\t"
"s_waitcnt lgkmcnt(7)\n\t"
"v_xor_b32 v246, 0xc0, v246\n\t"
"v_fma_f32 v98, -v40, s56, -v138\n\t"
"v_mfma_f32_16x16x32_bf16 v[196:199], v[216:219], a[172:175], v[196:199]\n\t"
"v_accvgpr_write_b32 a49, v99\n\t"
"v_fma_f32 v99, -v41, s56, -v139\n\t"
"v_mfma_f32_16x16x32_bf16 v[196:199], a[4:7], a[176:179], v[196:199]\n\t"
"v_accvgpr_write_b32 a50, v98\n\t"
"v_accvgpr_write_b32 a51, v99\n\t"
"v_mfma_f32_16x16x32_bf16 v[204:207], v[208:211], a[172:175], v[204:207]\n\t"
"s_waitcnt lgkmcnt(6)\n\t"
"v_fma_f32 v98, -v42, s56, -v140\n\t"
"v_fma_f32 v99, -v43, s56, -v141\n\t"
"v_mfma_f32_16x16x32_bf16 v[204:207], v[216:219], a[176:179], v[204:207]\n\t"
"v_accvgpr_write_b32 a56, v98\n\t"
"v_fma_f32 v98, -v44, s56, -v142\n\t"
"v_mfma_f32_16x16x32_bf16 v[204:207], a[4:7], a[180:183], v[204:207]\n\t"
"buffer_store_dwordx4 a[104:107], v248, s[16:19], s29 offen offset:256 nt\n\t"
"v_accvgpr_write_b32 a57, v99\n\t"
"v_fma_f32 v99, -v45, s56, -v143\n\t"
"v_accvgpr_write_b32 a58, v98\n\t"
"v_accvgpr_write_b32 a59, v99\n\t"
"v_mfma_f32_16x16x32_bf16 v[212:215], v[208:211], a[176:179], v[212:215]\n\t"
"s_waitcnt lgkmcnt(5)\n\t"
"v_fma_f32 v98, -v46, s56, -v144\n\t"
"v_fma_f32 v99, -v47, s56, -v145\n\t"
"v_mfma_f32_16x16x32_bf16 v[212:215], v[216:219], a[180:183], v[212:215]\n\t"
"s_add_u32 s29, s23, 787584\n\t"
"ds_read_b128 v[200:203], v252 offset:1024\n\t"
"v_accvgpr_write_b32 a64, v98\n\t"
"v_mfma_f32_16x16x32_bf16 v[212:215], a[4:7], a[184:187], v[212:215]\n\t"
"v_fma_f32 v98, -v48, s56, -v146\n\t"
"v_accvgpr_write_b32 a65, v99\n\t"
"v_mfma_f32_16x16x32_bf16 a[0:3], v[208:211], a[180:183], a[0:3]\n\t"
"s_waitcnt lgkmcnt(5)\n\t"
"v_fma_f32 v99, -v49, s56, -v147\n\t"
"v_accvgpr_write_b32 a66, v98\n\t"
"v_mfma_f32_16x16x32_bf16 a[0:3], v[216:219], a[184:187], a[0:3]\n\t"
"s_waitcnt lgkmcnt(4)\n\t"
"v_accvgpr_write_b32 a67, v99\n\t"
"v_fma_f32 v98, -v50, s56, -v148\n\t"
"v_mfma_f32_16x16x32_bf16 a[0:3], a[4:7], a[188:191], a[0:3]\n\t"
"v_fma_f32 v99, -v51, s56, -v149\n\t"
"v_accvgpr_write_b32 a72, v98\n\t"
"v_mfma_f32_16x16x32_bf16 a[8:11], v[208:211], a[184:187], a[8:11]\n\t"
"s_waitcnt lgkmcnt(3)\n\t"
"v_fma_f32 v98, -v52, s56, -v150\n\t"
"v_accvgpr_write_b32 a73, v99\n\t"
"v_mfma_f32_16x16x32_bf16 a[8:11], v[216:219], a[188:191], a[8:11]\n\t"
"v_fma_f32 v99, -v53, s56, -v151\n\t"
"v_accvgpr_write_b32 a74, v98\n\t"
"v_mfma_f32_16x16x32_bf16 a[8:11], a[4:7], a[192:195], a[8:11]\n\t"
"ds_read_b64_tr_b16 v[232:233], v246 offset:512\n\t"
"v_accvgpr_write_b32 a75, v99\n\t"
"v_mfma_f32_16x16x32_bf16 a[16:19], v[208:211], a[188:191], a[16:19]\n\t"
"s_waitcnt lgkmcnt(3)\n\t"
"ds_read_b64_tr_b16 v[234:235], v246 offset:3584\n\t"
"v_fma_f32 v98, -v54, s56, -v152\n\t"
"v_mfma_f32_16x16x32_bf16 a[16:19], v[216:219], a[192:195], a[16:19]\n\t"
"ds_read_b128 a[44:47], v252 offset:12288\n\t"
"v_fma_f32 v99, -v55, s56, -v153\n\t"
"v_mfma_f32_16x16x32_bf16 a[16:19], a[4:7], a[196:199], a[16:19]\n\t"
"v_accvgpr_write_b32 a80, v98\n\t"
"v_fma_f32 v98, -v56, s56, -v154\n\t"
"v_mfma_f32_16x16x32_bf16 a[24:27], v[208:211], a[192:195], a[24:27]\n\t"
"s_waitcnt lgkmcnt(4)\n\t"
"ds_read_b128 a[76:79], v252 offset:13312\n\t"
"v_accvgpr_write_b32 a81, v99\n\t"
"v_mfma_f32_16x16x32_bf16 a[24:27], v[216:219], a[196:199], a[24:27]\n\t"
"v_fma_f32 v99, -v57, s56, -v155\n\t"
"v_accvgpr_write_b32 a82, v98\n\t"
"v_mfma_f32_16x16x32_bf16 a[24:27], a[4:7], a[200:203], a[24:27]\n\t"
"buffer_store_dwordx4 a[136:139], v248, s[16:19], s29 offen offset:0 nt\n\t"
"ds_read_b128 a[12:15], v252 offset:2048\n\t"
"ds_read_b128 a[108:111], v252 offset:14336\n\t"
"v_accvgpr_write_b32 a83, v99\n\t"
"v_mfma_f32_16x16x32_bf16 a[32:35], v[208:211], a[196:199], a[32:35]\n\t"
"s_waitcnt vmcnt(26)\n\t"
"ds_read_b64_tr_b16 v[224:225], v246 offset:6656\n\t"
"v_fma_f32 v98, -v58, s56, -v156\n\t"
"v_mfma_f32_16x16x32_bf16 a[32:35], v[216:219], a[200:203], a[32:35]\n\t"
"s_waitcnt lgkmcnt(7)\n\t"
"ds_read_b64_tr_b16 v[226:227], v246 offset:9728\n\t"
"v_fma_f32 v99, -v59, s56, -v157\n\t"
"v_mfma_f32_16x16x32_bf16 a[32:35], a[4:7], a[204:207], a[32:35]\n\t"
"v_accvgpr_write_b32 a88, v98\n\t"
"v_fma_f32 v98, -v60, s56, -v158\n\t"
"v_mfma_f32_16x16x32_bf16 a[40:43], a[208:211], a[168:171], a[40:43]\n\t"
"v_accvgpr_write_b32 a89, v99\n\t"
"v_fma_f32 v99, -v61, s56, -v159\n\t"
"v_mfma_f32_16x16x32_bf16 a[40:43], a[212:215], a[172:175], a[40:43]\n\t"
"v_accvgpr_write_b32 a90, v98\n\t"
"v_accvgpr_write_b32 a91, v99\n\t"
"v_mfma_f32_16x16x32_bf16 a[40:43], v[240:243], a[176:179], a[40:43]\n\t"
"v_fma_f32 v98, -v62, s56, -v160\n\t"
"v_fma_f32 v99, -v63, s56, -v161\n\t"
"v_mfma_f32_16x16x32_bf16 a[48:51], a[208:211], a[172:175], a[48:51]\n\t"
"s_waitcnt lgkmcnt(6)\n\t"
"v_accvgpr_write_b32 a96, v98\n\t"
"v_fma_f32 v98, -v64, s56, -v162\n\t"
"v_mfma_f32_16x16x32_bf16 a[48:51], a[212:215], a[176:179], a[48:51]\n\t"
"v_accvgpr_write_b32 a97, v99\n\t"
"v_fma_f32 v99, -v65, s56, -v163\n\t"
"v_mfma_f32_16x16x32_bf16 a[48:51], v[240:243], a[180:183], a[48:51]\n\t"
"v_accvgpr_write_b32 a98, v98\n\t"
"v_accvgpr_write_b32 a99, v99\n\t"
"v_mfma_f32_16x16x32_bf16 v[232:235], v[232:235], v[220:223], 0\n\t"
"v_fma_f32 v98, -v66, s56, -v164\n\t"
"v_fma_f32 v99, -v67, s56, -v165\n\t"
"v_mfma_f32_16x16x32_bf16 a[56:59], a[208:211], a[176:179], a[56:59]\n\t"
"ds_read_b64_tr_b16 v[228:229], v246 offset:12800\n\t"
"v_accvgpr_write_b32 a104, v98\n\t"
"v_mfma_f32_16x16x32_bf16 a[56:59], a[212:215], a[180:183], a[56:59]\n\t"
"ds_read_b64_tr_b16 v[230:231], v246 offset:15872\n\t"
"v_fma_f32 v98, -v68, s56, -v166\n\t"
"v_mfma_f32_16x16x32_bf16 a[56:59], v[240:243], a[184:187], a[56:59]\n\t"
"buffer_store_dwordx4 v[200:203], v248, s[16:19], s29 offen offset:128 nt\n\t"
"v_accvgpr_write_b32 a105, v99\n\t"
"v_fma_f32 v99, -v69, s56, -v167\n\t"
"v_accvgpr_write_b32 a106, v98\n\t"
"v_accvgpr_write_b32 a107, v99\n\t"
"v_mfma_f32_16x16x32_bf16 a[64:67], a[208:211], a[180:183], a[64:67]\n\t"
"s_waitcnt lgkmcnt(2)\n\t"
"v_fma_f32 v98, -v70, s56, -v168\n\t"
"v_fma_f32 v99, -v71, s56, -v169\n\t"
"v_mfma_f32_16x16x32_bf16 a[64:67], a[212:215], a[184:187], a[64:67]\n\t"
"v_cvt_pk_bf16_f32 v232, v232, v233\n\t"
"v_cvt_pk_bf16_f32 v233, v234, v235\n\t"
"v_mfma_f32_16x16x32_bf16 a[64:67], v[240:243], a[188:191], a[64:67]\n\t"
"v_accvgpr_write_b32 a112, v98\n\t"
"v_mfma_f32_16x16x32_bf16 v[224:227], v[224:227], v[220:223], 0\n\t"
"s_barrier\n\t"
"v_mfma_f32_16x16x32_bf16 a[72:75], a[208:211], a[184:187], a[72:75]\n\t"
"v_fma_f32 v98, -v72, s56, -v170\n\t"
"v_mfma_f32_16x16x32_bf16 a[72:75], a[212:215], a[188:191], a[72:75]\n\t"
"v_accvgpr_write_b32 a113, v99\n\t"
"v_fma_f32 v99, -v73, s56, -v171\n\t"
"v_mfma_f32_16x16x32_bf16 a[72:75], v[240:243], a[192:195], a[72:75]\n\t"
"v_accvgpr_write_b32 a114, v98\n\t"
"v_accvgpr_write_b32 a115, v99\n\t"
"v_mfma_f32_16x16x32_bf16 a[80:83], a[208:211], a[188:191], a[80:83]\n\t"
"s_waitcnt vmcnt(22) lgkmcnt(0)\n\t"
"v_fma_f32 v98, -v74, s56, -v172\n\t"
"v_fma_f32 v99, -v75, s56, -v173\n\t"
"v_mfma_f32_16x16x32_bf16 a[80:83], a[212:215], a[192:195], a[80:83]\n\t"
"v_cvt_pk_bf16_f32 v234, v224, v225\n\t"
"v_cvt_pk_bf16_f32 v235, v226, v227\n\t"
"v_mfma_f32_16x16x32_bf16 a[80:83], v[240:243], a[196:199], a[80:83]\n\t"
"Lpg15_%=:\n\t"
"s_cbranch_vccnz Lvr1_%=\n\t"
"ds_read_b64_tr_b16 v[224:225], v246 offset:20480\n\t"
"v_accvgpr_write_b32 a120, v98\n\t"
"v_mfma_f32_16x16x32_bf16 a[88:91], a[208:211], a[192:195], a[88:91]\n\t"
"ds_read_b64_tr_b16 v[226:227], v246 offset:23552\n\t"
"v_fma_f32 v98, -v76, s56, -v174\n\t"
"v_mfma_f32_16x16x32_bf16 a[88:91], a[212:215], a[196:199], a[88:91]\n\t"
"v_accvgpr_write_b32 a121, v99\n\t"
"v_fma_f32 v99, -v77, s56, -v175\n\t"
"v_mfma_f32_16x16x32_bf16 a[88:91], v[240:243], a[200:203], a[88:91]\n\t"
"buffer_store_dwordx4 a[12:15], v248, s[16:19], s29 offen offset:256 nt\n\t"
"v_accvgpr_write_b32 a122, v98\n\t"
"v_accvgpr_write_b32 a123, v99\n\t"
"v_fma_f32 v98, -v78, s56, -v176\n\t"
"v_fma_f32 v99, -v79, s56, -v177\n\t"
"v_mfma_f32_16x16x32_bf16 v[228:231], v[228:231], v[220:223], 0\n\t"
"s_add_u32 s29, s23, 1180800\n\t"
"v_accvgpr_write_b32 a128, v98\n\t"
"v_fma_f32 v98, -v80, s56, -v178\n\t"
"v_mfma_f32_16x16x32_bf16 a[96:99], a[208:211], a[196:199], a[96:99]\n\t"
"v_accvgpr_write_b32 a129, v99\n\t"
"v_fma_f32 v99, -v81, s56, -v179\n\t"
"v_mfma_f32_16x16x32_bf16 a[96:99], a[212:215], a[200:203], a[96:99]\n\t"
"v_accvgpr_write_b32 a130, v98\n\t"
"v_accvgpr_write_b32 a131, v99\n\t"
"v_mfma_f32_16x16x32_bf16 a[96:99], v[240:243], a[204:207], a[96:99]\n\t"
"v_fma_f32 v98, -v82, s56, -v180\n\t"
"v_fma_f32 v99, -v83, s56, -v181\n\t"
"v_mfma_f32_16x16x32_bf16 a[104:107], a[216:219], a[168:171], a[104:107]\n\t"
"ds_read_b128 a[168:171], v244 offset:33792\n\t"
"v_accvgpr_write_b32 a136, v98\n\t"
"v_mfma_f32_16x16x32_bf16 a[104:107], a[220:223], a[172:175], a[104:107]\n\t"
"v_cvt_pk_bf16_f32 v228, v228, v229\n\t"
"v_cvt_pk_bf16_f32 v229, v230, v231\n\t"
"v_mfma_f32_16x16x32_bf16 a[104:107], a[224:227], a[176:179], a[104:107]\n\t"
"buffer_load_dwordx4 a[208:211], v252, s[8:11], s20 offen offset:2048 sc1\n\t"
"s_mov_b32 s20, 36864\n\t"
"v_mfma_f32_16x16x32_bf16 a[112:115], a[216:219], a[172:175], a[112:115]\n\t"
"buffer_load_dwordx4 v[240:243], v252, s[8:11], s20 offen offset:0 sc1\n\t"
"v_mfma_f32_16x16x32_bf16 a[112:115], a[220:223], a[176:179], a[112:115]\n\t"
"buffer_load_dwordx4 a[212:215], v252, s[8:11], s20 offen offset:1024 sc1\n\t"
"v_mfma_f32_16x16x32_bf16 a[112:115], a[224:227], a[180:183], a[112:115]\n\t"
"s_waitcnt lgkmcnt(1)\n\t"
"ds_read_b128 a[172:175], v244 offset:34304\n\t"
"v_fma_f32 v98, -v84, s56, -v182\n\t"
"v_mfma_f32_16x16x32_bf16 v[224:227], v[224:227], v[220:223], 0\n\t"
"buffer_store_dwordx4 a[44:47], v248, s[16:19], s29 offen offset:0 nt\n\t"
"v_accvgpr_write_b32 a137, v99\n\t"
"v_fma_f32 v99, -v85, s56, -v183\n\t"
"v_accvgpr_write_b32 a138, v98\n\t"
"v_accvgpr_write_b32 a139, v99\n\t"
"v_mfma_f32_16x16x32_bf16 a[120:123], a[216:219], a[176:179], a[120:123]\n\t"
"ds_read_b128 a[176:179], v244 offset:34816\n\t"
"v_fma_f32 v98, -v86, s56, -v184\n\t"
"v_mfma_f32_16x16x32_bf16 a[120:123], a[220:223], a[180:183], a[120:123]\n\t"
"v_fma_f32 v99, -v87, s56, -v185\n\t"
"v_accvgpr_write_b32 a144, v98\n\t"
"v_mfma_f32_16x16x32_bf16 a[120:123], a[224:227], a[184:187], a[120:123]\n\t"
"v_fma_f32 v98, -v88, s56, -v186\n\t"
"v_accvgpr_write_b32 a145, v99\n\t"
"v_mfma_f32_16x16x32_bf16 a[128:131], a[216:219], a[180:183], a[128:131]\n\t"
"ds_read_b128 a[180:183], v244 offset:35328\n\t"
"v_fma_f32 v99, -v89, s56, -v187\n\t"
"v_mfma_f32_16x16x32_bf16 a[128:131], a[220:223], a[184:187], a[128:131]\n\t"
"v_accvgpr_write_b32 a146, v98\n\t"
"v_accvgpr_write_b32 a147, v99\n\t"
"v_mfma_f32_16x16x32_bf16 a[128:131], a[224:227], a[188:191], a[128:131]\n\t"
"v_fma_f32 v98, -v90, s56, -v188\n\t"
"v_fma_f32 v99, -v91, s56, -v189\n\t"
"v_mfma_f32_16x16x32_bf16 a[136:139], a[216:219], a[184:187], a[136:139]\n\t"
"ds_read_b128 a[184:187], v244 offset:35840\n\t"
"v_cvt_pk_bf16_f32 v230, v224, v225\n\t"
"v_mfma_f32_16x16x32_bf16 a[136:139], a[220:223], a[188:191], a[136:139]\n\t"
"v_cvt_pk_bf16_f32 v231, v226, v227\n\t"
"v_accvgpr_write_b32 a152, v98\n\t"
"v_mfma_f32_16x16x32_bf16 a[136:139], a[224:227], a[192:195], a[136:139]\n\t"
"ds_read_b64_tr_b16 v[224:225], v246 offset:26624\n\t"
"v_fma_f32 v98, -v92, s56, -v190\n\t"
"v_mfma_f32_16x16x32_bf16 a[144:147], a[216:219], a[188:191], a[144:147]\n\t"
"ds_read_b64_tr_b16 v[226:227], v246 offset:29696\n\t"
"v_accvgpr_write_b32 a153, v99\n\t"
"v_mfma_f32_16x16x32_bf16 a[144:147], a[220:223], a[192:195], a[144:147]\n\t"
"ds_read_b128 a[188:191], v244 offset:36352\n\t"
"v_fma_f32 v99, -v93, s56, -v191\n\t"
"v_mfma_f32_16x16x32_bf16 a[144:147], a[224:227], a[196:199], a[144:147]\n\t"
"v_accvgpr_write_b32 a154, v98\n\t"
"v_accvgpr_write_b32 a155, v99\n\t"
"v_fma_f32 v98, -v94, s56, -v192\n\t"
"v_fma_f32 v99, -v95, s56, -v193\n\t"
"v_mfma_f32_16x16x32_bf16 a[152:155], a[216:219], a[192:195], a[152:155]\n\t"
"ds_read_b128 a[192:195], v244 offset:36864\n\t"
"v_accvgpr_write_b32 a160, v98\n\t"
"v_mfma_f32_16x16x32_bf16 a[152:155], a[220:223], a[196:199], a[152:155]\n\t"
"v_fma_f32 v98, -v96, s56, -v194\n\t"
"v_accvgpr_write_b32 a161, v99\n\t"
"v_mfma_f32_16x16x32_bf16 a[152:155], a[224:227], a[200:203], a[152:155]\n\t"
"buffer_store_dwordx4 a[76:79], v248, s[16:19], s29 offen offset:128 nt\n\t"
"v_fma_f32 v99, -v97, s56, -v195\n\t"
"v_accvgpr_write_b32 a162, v98\n\t"
"v_accvgpr_write_b32 a163, v99\n\t"
"v_fma_f32 v200, v100, s64, -v2\n\t"
"v_fma_f32 v201, v101, s64, -v3\n\t"
"v_fma_f32 v202, v102, s64, -v4\n\t"
"v_fma_f32 v203, v103, s64, -v5\n\t"
"v_mfma_f32_16x16x32_bf16 a[160:163], a[216:219], a[196:199], a[160:163]\n\t"
"s_waitcnt lgkmcnt(2)\n\t"
"ds_read_b128 a[196:199], v244 offset:37376\n\t"
"v_fma_f32 v208, v104, s64, -v6\n\t"
"v_mfma_f32_16x16x32_bf16 a[160:163], a[220:223], a[200:203], a[160:163]\n\t"
"s_waitcnt vmcnt(23)\n\t"
"ds_read_b128 a[200:203], v244 offset:37888\n\t"
"v_fma_f32 v209, v105, s64, -v7\n\t"
"v_mfma_f32_16x16x32_bf16 a[160:163], a[224:227], a[204:207], a[160:163]\n\t"
"ds_read_b128 a[204:207], v244 offset:38400\n\t"
"v_fma_f32 v210, v106, s64, -v8\n\t"
"v_fma_f32 v211, v107, s64, -v9\n\t"
"v_mfma_f32_16x16x32_bf16 v[224:227], v[224:227], v[220:223], 0\n\t"
"buffer_load_dwordx4 a[216:219], v252, s[8:11], s20 offen offset:2048 sc1\n\t"
"s_mov_b32 s20, 49152\n\t"
"v_mfma_f32_16x16x32_bf16 v[200:203], a[228:231], a[168:171], v[200:203]\n\t"
"buffer_load_dwordx4 a[220:223], v252, s[8:11], s20 offen offset:0 sc1\n\t"
"v_mfma_f32_16x16x32_bf16 v[200:203], a[232:235], a[172:175], v[200:203]\n\t"
"v_fma_f32 v216, v108, s64, -v10\n\t"
"v_fma_f32 v217, v109, s64, -v11\n\t"
"v_mfma_f32_16x16x32_bf16 v[200:203], a[240:243], a[176:179], v[200:203]\n\t"
"v_fma_f32 v218, v110, s64, -v12\n\t"
"v_fma_f32 v219, v111, s64, -v13\n\t"
"v_mfma_f32_16x16x32_bf16 v[208:211], a[228:231], a[172:175], v[208:211]\n\t"
"v_fma_f32 v98, v112, s64, -v14\n\t"
"v_fma_f32 v99, v113, s64, -v15\n\t"
"v_mfma_f32_16x16x32_bf16 v[208:211], a[232:235], a[176:179], v[208:211]\n\t"
"v_accvgpr_write_b32 a4, v98\n\t"
"v_mfma_f32_16x16x32_bf16 v[208:211], a[240:243], a[180:183], v[208:211]\n\t"
"s_waitcnt lgkmcnt(0)\n\t"
"s_barrier\n\t"
"v_mfma_f32_16x16x32_bf16 v[216:219], a[228:231], a[176:179], v[216:219]\n\t"
"v_fma_f32 v98, v114, s64, -v16\n\t"
"v_mfma_f32_16x16x32_bf16 v[216:219], a[232:235], a[180:183], v[216:219]\n\t"
"buffer_load_dwordx4 a[224:227], v252, s[8:11], s20 offen offset:1024 sc1\n\t"
"v_mfma_f32_16x16x32_bf16 v[216:219], a[240:243], a[184:187], v[216:219]\n\t"
"buffer_store_dwordx4 a[108:111], v248, s[16:19], s29 offen offset:256 nt\n\t"
"v_accvgpr_write_b32 a5, v99\n\t"
"v_fma_f32 v99, v115, s64, -v17\n\t"
"v_accvgpr_write_b32 a6, v98\n\t"
"v_accvgpr_write_b32 a7, v99\n\t"
"v_fma_f32 v98, v116, s64, -v18\n\t"
"v_fma_f32 v99, v117, s64, -v19\n\t"
"v_cvt_pk_bf16_f32 v224, v224, v225\n\t"
"v_cvt_pk_bf16_f32 v225, v226, v227\n\t"
"v_mfma_f32_16x16x32_bf16 a[4:7], a[228:231], a[180:183], a[4:7]\n\t"
"v_accvgpr_write_b32 a12, v98\n\t"
"v_fma_f32 v98, v118, s64, -v20\n\t"
"v_mfma_f32_16x16x32_bf16 a[4:7], a[232:235], a[184:187], a[4:7]\n\t"
"v_accvgpr_write_b32 a13, v99\n\t"
"v_fma_f32 v99, v119, s64, -v21\n\t"
"v_mfma_f32_16x16x32_bf16 a[4:7], a[240:243], a[188:191], a[4:7]\n\t"
"ds_write2st64_b64 v245, v[232:233], v[234:235] offset0:48 offset1:49\n\t"
"v_accvgpr_write_b32 a14, v98\n\t"
"v_accvgpr_write_b32 a15, v99\n\t"
"v_fma_f32 v98, v120, s64, -v22\n\t"
"v_fma_f32 v99, v121, s64, -v23\n\t"
"v_mfma_f32_16x16x32_bf16 a[12:15], a[228:231], a[184:187], a[12:15]\n\t"
"ds_read_b64_tr_b16 v[232:233], v246 offset:32768\n\t"
"v_mfma_f32_16x16x32_bf16 a[12:15], a[232:235], a[188:191], a[12:15]\n\t"
"ds_read_b64_tr_b16 v[234:235], v246 offset:35840\n\t"
"v_accvgpr_write_b32 a20, v98\n\t"
"v_mfma_f32_16x16x32_bf16 a[12:15], a[240:243], a[192:195], a[12:15]\n\t"
"ds_write2st64_b64 v245, v[228:229], v[230:231] offset0:50 offset1:51\n\t"
"buffer_load_dwordx4 v[248:251], v252, s[8:11], s20 offen offset:2048 sc1\n\t"
"v_fma_f32 v98, v122, s64, -v24\n\t"
"v_accvgpr_write_b32 a21, v99\n\t"
"v_fma_f32 v99, v123, s64, -v25\n\t"
"v_accvgpr_write_b32 a22, v98\n\t"
"v_accvgpr_write_b32 a23, v99\n\t"
"v_fma_f32 v98, v124, s64, -v26\n\t"
"v_fma_f32 v99, v125, s64, -v27\n\t"
"v_mfma_f32_16x16x32_bf16 a[20:23], a[228:231], a[188:191], a[20:23]\n\t"
"ds_read_b64_tr_b16 v[228:229], v246 offset:40448\n\t"
"v_accvgpr_write_b32 a28, v98\n\t"
"v_mfma_f32_16x16x32_bf16 a[20:23], a[232:235], a[192:195], a[20:23]\n\t"
"ds_read_b64_tr_b16 v[230:231], v246 offset:43520\n\t"
"v_fma_f32 v98, v126, s64, -v28\n\t"
"v_mfma_f32_16x16x32_bf16 a[20:23], a[240:243], a[196:199], a[20:23]\n\t"
"s_waitcnt lgkmcnt(3)\n\t"
"ds_write_b64 v245, v[224:225] offset:26624\n\t"
"v_accvgpr_write_b32 a29, v99\n\t"
"v_fma_f32 v99, v127, s64, -v29\n\t"
"v_accvgpr_write_b32 a30, v98\n\t"
"v_accvgpr_write_b32 a31, v99\n\t"
"v_mfma_f32_16x16x32_bf16 v[232:235], v[232:235], v[220:223], 0\n\t"
"s_mov_b32 s20, 61440\n\t"
"ds_read_b64_tr_b16 v[224:225], v246 offset:46592\n\t"
"v_fma_f32 v98, v128, s64, -v30\n\t"
"v_fma_f32 v99, v129, s64, -v31\n\t"
"v_mfma_f32_16x16x32_bf16 a[28:31], a[228:231], a[192:195], a[28:31]\n\t"
"ds_read_b64_tr_b16 v[226:227], v246 offset:49664\n\t"
"v_accvgpr_write_b32 a36, v98\n\t"
"v_mfma_f32_16x16x32_bf16 a[28:31], a[232:235], a[196:199], a[28:31]\n\t"
"v_fma_f32 v98, v130, s64, -v32\n\t"
"v_accvgpr_write_b32 a37, v99\n\t"
"v_mfma_f32_16x16x32_bf16 a[28:31], a[240:243], a[200:203], a[28:31]\n\t"
"s_waitcnt lgkmcnt(3)\n\t"
"v_fma_f32 v99, v131, s64, -v33\n\t"
"v_accvgpr_write_b32 a38, v98\n\t"
"v_accvgpr_write_b32 a39, v99\n\t"
"v_mfma_f32_16x16x32_bf16 v[228:231], v[228:231], v[220:223], 0\n\t"
"v_fma_f32 v98, v132, s64, -v34\n\t"
"v_fma_f32 v99, v133, s64, -v35\n\t"
"v_mfma_f32_16x16x32_bf16 a[36:39], a[228:231], a[196:199], a[36:39]\n\t"
"v_accvgpr_write_b32 a44, v98\n\t"
"v_fma_f32 v98, v134, s64, -v36\n\t"
"v_mfma_f32_16x16x32_bf16 a[36:39], a[232:235], a[200:203], a[36:39]\n\t"
"s_waitcnt lgkmcnt(0)\n\t"
"v_accvgpr_write_b32 a45, v99\n\t"
"v_fma_f32 v99, v135, s64, -v37\n\t"
"v_mfma_f32_16x16x32_bf16 a[36:39], a[240:243], a[204:207], a[36:39]\n\t"
"v_accvgpr_write_b32 a46, v98\n\t"
"v_accvgpr_write_b32 a47, v99\n\t"
"v_fma_f32 v98, v136, s64, -v38\n\t"
"v_fma_f32 v99, v137, s64, -v39\n\t"
"v_mfma_f32_16x16x32_bf16 v[224:227], v[224:227], v[220:223], 0\n\t"
"s_waitcnt vmcnt(20)\n\t"
"buffer_load_dwordx4 a[228:231], v252, s[8:11], s20 offen offset:0 sc1\n\t"
"v_cvt_pk_bf16_f32 v232, v232, v233\n\t"
"v_cvt_pk_bf16_f32 v233, v234, v235\n\t"
"v_mfma_f32_16x16x32_bf16 a[44:47], a[244:247], a[168:171], a[44:47]\n\t"
"v_accvgpr_write_b32 a52, v98\n\t"
"v_fma_f32 v98, v138, s64, -v40\n\t"
"v_mfma_f32_16x16x32_bf16 a[44:47], a[248:251], a[172:175], a[44:47]\n\t"
"v_accvgpr_write_b32 a53, v99\n\t"
"v_fma_f32 v99, v139, s64, -v41\n\t"
"v_mfma_f32_16x16x32_bf16 a[44:47], a[252:255], a[176:179], a[44:47]\n\t"
"buffer_load_dwordx4 a[232:235], v252, s[8:11], s20 offen offset:1024 sc1\n\t"
"v_accvgpr_write_b32 a54, v98\n\t"
"v_accvgpr_write_b32 a55, v99\n\t"
"v_fma_f32 v98, v140, s64, -v42\n\t"
"v_fma_f32 v99, v141, s64, -v43\n\t"
"v_mfma_f32_16x16x32_bf16 a[52:55], a[244:247], a[172:175], a[52:55]\n\t"
"v_accvgpr_write_b32 a60, v98\n\t"
"v_fma_f32 v98, v142, s64, -v44\n\t"
"v_mfma_f32_16x16x32_bf16 a[52:55], a[248:251], a[176:179], a[52:55]\n\t"
"v_accvgpr_write_b32 a61, v99\n\t"
"v_fma_f32 v99, v143, s64, -v45\n\t"
"v_mfma_f32_16x16x32_bf16 a[52:55], a[252:255], a[180:183], a[52:55]\n\t"
"buffer_load_dwordx4 a[240:243], v252, s[8:11], s20 offen offset:2048 sc1\n\t"
"v_accvgpr_write_b32 a62, v98\n\t"
"v_accvgpr_write_b32 a63, v99\n\t"
"v_cvt_pk_bf16_f32 v234, v228, v229\n\t"
"v_cvt_pk_bf16_f32 v235, v230, v231\n\t"
"v_fma_f32 v98, v144, s64, -v46\n\t"
"v_fma_f32 v99, v145, s64, -v47\n\t"
"v_mfma_f32_16x16x32_bf16 a[60:63], a[244:247], a[176:179], a[60:63]\n\t"
"ds_read_b64_tr_b16 v[228:229], v246 offset:52736\n\t"
"v_accvgpr_write_b32 a68, v98\n\t"
"v_mfma_f32_16x16x32_bf16 a[60:63], a[248:251], a[180:183], a[60:63]\n\t"
"ds_read_b64_tr_b16 v[230:231], v246 offset:55808\n\t"
"v_fma_f32 v98, v146, s64, -v48\n\t"
"v_mfma_f32_16x16x32_bf16 a[60:63], a[252:255], a[184:187], a[60:63]\n\t"
"ds_write2st64_b64 v245, v[232:233], v[234:235] offset0:53 offset1:54\n\t"
"v_accvgpr_write_b32 a69, v99\n\t"
"v_fma_f32 v99, v147, s64, -v49\n\t"
"v_accvgpr_write_b32 a70, v98\n\t"
"v_accvgpr_write_b32 a71, v99\n\t"
"v_fma_f32 v98, v148, s64, -v50\n\t"
"v_fma_f32 v99, v149, s64, -v51\n\t"
"v_cvt_pk_bf16_f32 v224, v224, v225\n\t"
"v_cvt_pk_bf16_f32 v225, v226, v227\n\t"
"v_mfma_f32_16x16x32_bf16 a[68:71], a[244:247], a[180:183], a[68:71]\n\t"
"v_accvgpr_write_b32 a76, v98\n\t"
"v_fma_f32 v98, v150, s64, -v52\n\t"
"v_mfma_f32_16x16x32_bf16 a[68:71], a[248:251], a[184:187], a[68:71]\n\t"
"v_accvgpr_write_b32 a77, v99\n\t"
"v_fma_f32 v99, v151, s64, -v53\n\t"
"v_mfma_f32_16x16x32_bf16 a[68:71], a[252:255], a[188:191], a[68:71]\n\t"
"v_accvgpr_write_b32 a78, v98\n\t"
"v_accvgpr_write_b32 a79, v99\n\t"
"v_fma_f32 v98, v152, s64, -v54\n\t"
"v_fma_f32 v99, v153, s64, -v55\n\t"
"v_mfma_f32_16x16x32_bf16 a[76:79], a[244:247], a[184:187], a[76:79]\n\t"
"s_waitcnt lgkmcnt(1)\n\t"
"v_accvgpr_write_b32 a84, v98\n\t"
"v_fma_f32 v98, v154, s64, -v56\n\t"
"v_mfma_f32_16x16x32_bf16 a[76:79], a[248:251], a[188:191], a[76:79]\n\t"
"v_accvgpr_write_b32 a85, v99\n\t"
"v_fma_f32 v99, v155, s64, -v57\n\t"
"v_mfma_f32_16x16x32_bf16 a[76:79], a[252:255], a[192:195], a[76:79]\n\t"
"v_accvgpr_write_b32 a86, v98\n\t"
"v_accvgpr_write_b32 a87, v99\n\t"
"v_fma_f32 v98, v156, s64, -v58\n\t"
"v_fma_f32 v99, v157, s64, -v59\n\t"
"v_mfma_f32_16x16x32_bf16 v[228:231], v[228:231], v[220:223], 0\n\t"
"v_accvgpr_write_b32 a92, v98\n\t"
"v_fma_f32 v98, v158, s64, -v60\n\t"
"v_mfma_f32_16x16x32_bf16 a[84:87], a[244:247], a[188:191], a[84:87]\n\t"
"s_mov_b32 s20, 73728\n\t"
"v_accvgpr_write_b32 a93, v99\n\t"
"v_fma_f32 v99, v159, s64, -v61\n\t"
"v_mfma_f32_16x16x32_bf16 a[84:87], a[248:251], a[192:195], a[84:87]\n\t"
"v_accvgpr_write_b32 a94, v98\n\t"
"v_accvgpr_write_b32 a95, v99\n\t"
"v_mfma_f32_16x16x32_bf16 a[84:87], a[252:255], a[196:199], a[84:87]\n\t"
"v_fma_f32 v98, v160, s64, -v62\n\t"
"v_fma_f32 v99, v161, s64, -v63\n\t"
"v_mfma_f32_16x16x32_bf16 a[92:95], a[244:247], a[192:195], a[92:95]\n\t"
"v_accvgpr_write_b32 a100, v98\n\t"
"v_fma_f32 v98, v162, s64, -v64\n\t"
"v_mfma_f32_16x16x32_bf16 a[92:95], a[248:251], a[196:199], a[92:95]\n\t"
"v_accvgpr_write_b32 a101, v99\n\t"
"v_fma_f32 v99, v163, s64, -v65\n\t"
"v_mfma_f32_16x16x32_bf16 a[92:95], a[252:255], a[200:203], a[92:95]\n\t"
"v_accvgpr_write_b32 a102, v98\n\t"
"v_accvgpr_write_b32 a103, v99\n\t"
"v_fma_f32 v98, v164, s64, -v66\n\t"
"v_fma_f32 v99, v165, s64, -v67\n\t"
"v_cvt_pk_bf16_f32 v226, v228, v229\n\t"
"v_cvt_pk_bf16_f32 v227, v230, v231\n\t"
"v_mfma_f32_16x16x32_bf16 a[100:103], a[244:247], a[196:199], a[100:103]\n\t"
"v_accvgpr_write_b32 a108, v98\n\t"
"v_fma_f32 v98, v166, s64, -v68\n\t"
"v_mfma_f32_16x16x32_bf16 a[100:103], a[248:251], a[200:203], a[100:103]\n\t"
"v_accvgpr_write_b32 a109, v99\n\t"
"v_fma_f32 v99, v167, s64, -v69\n\t"
"v_mfma_f32_16x16x32_bf16 a[100:103], a[252:255], a[204:207], a[100:103]\n\t"
"ds_write2st64_b64 v245, v[224:225], v[226:227] offset0:55 offset1:56\n\t"
"v_accvgpr_write_b32 a110, v98\n\t"
"v_accvgpr_write_b32 a111, v99\n\t"
"v_fma_f32 v98, v168, s64, -v70\n\t"
"v_fma_f32 v99, v169, s64, -v71\n\t"
"s_waitcnt vmcnt(12)\n\t"
"v_mfma_f32_16x16x32_bf16 a[108:111], a[236:239], a[168:171], a[108:111]\n\t"
"buffer_load_dwordx4 a[244:247], v252, s[8:11], s20 offen offset:0 sc1\n\t"
"v_mfma_f32_16x16x32_bf16 a[108:111], v[236:239], a[172:175], a[108:111]\n\t"
"v_accvgpr_write_b32 a116, v98\n\t"
"v_mfma_f32_16x16x32_bf16 a[108:111], a[208:211], a[176:179], a[108:111]\n\t"
"v_accvgpr_write_b32 a117, v99\n\t"
"v_fma_f32 v98, v170, s64, -v72\n\t"
"v_accvgpr_write_b32 a118, v98\n\t"
"v_fma_f32 v99, v171, s64, -v73\n\t"
"v_accvgpr_write_b32 a119, v99\n\t"
"s_waitcnt lgkmcnt(0)\n\t"
"s_barrier\n\t"
"v_mfma_f32_16x16x32_bf16 a[116:119], a[236:239], a[172:175], a[116:119]\n\t"
"ds_read_b128 a[168:171], v244 offset:24576\n\t"
"v_mfma_f32_16x16x32_bf16 a[116:119], v[236:239], a[176:179], a[116:119]\n\t"
"buffer_load_dwordx4 a[248:251], v252, s[8:11], s20 offen offset:1024 sc1\n\t"
"v_mfma_f32_16x16x32_bf16 a[116:119], a[208:211], a[180:183], a[116:119]\n\t"
"buffer_load_dwordx4 a[252:255], v252, s[8:11], s20 offen offset:2048 sc1\n\t"
"v_fma_f32 v98, v172, s64, -v74\n\t"
"v_fma_f32 v99, v173, s64, -v75\n\t"
"v_accvgpr_write_b32 a124, v98\n\t"
"v_fma_f32 v98, v174, s64, -v76\n\t"
"v_accvgpr_write_b32 a125, v99\n\t"
"v_fma_f32 v99, v175, s64, -v77\n\t"
"v_accvgpr_write_b32 a126, v98\n\t"
"v_accvgpr_write_b32 a127, v99\n\t"
"v_fma_f32 v98, v176, s64, -v78\n\t"
"v_fma_f32 v99, v177, s64, -v79\n\t"
"v_mfma_f32_16x16x32_bf16 a[124:127], a[236:239], a[176:179], a[124:127]\n\t"
"ds_read_b128 a[172:175], v244 offset:25088\n\t"
"v_accvgpr_write_b32 a132, v98\n\t"
"v_mfma_f32_16x16x32_bf16 a[124:127], v[236:239], a[180:183], a[124:127]\n\t"
"ds_read_b128 a[176:179], v244 offset:25600\n\t"
"v_fma_f32 v98, v178, s64, -v80\n\t"
"v_mfma_f32_16x16x32_bf16 a[124:127], a[208:211], a[184:187], a[124:127]\n\t"
"v_accvgpr_write_b32 a133, v99\n\t"
"v_fma_f32 v99, v179, s64, -v81\n\t"
"v_accvgpr_write_b32 a134, v98\n\t"
"v_accvgpr_write_b32 a135, v99\n\t"
"v_fma_f32 v98, v180, s64, -v82\n\t"
"v_fma_f32 v99, v181, s64, -v83\n\t"
"v_mfma_f32_16x16x32_bf16 a[132:135], a[236:239], a[180:183], a[132:135]\n\t"
"ds_read_b128 a[180:183], v244 offset:26112\n\t"
"v_accvgpr_write_b32 a140, v98\n\t"
"v_mfma_f32_16x16x32_bf16 a[132:135], v[236:239], a[184:187], a[132:135]\n\t"
"v_fma_f32 v98, v182, s64, -v84\n\t"
"v_accvgpr_write_b32 a141, v99\n\t"
"v_mfma_f32_16x16x32_bf16 a[132:135], a[208:211], a[188:191], a[132:135]\n\t"
"v_fma_f32 v99, v183, s64, -v85\n\t"
"v_accvgpr_write_b32 a142, v98\n\t"
"v_accvgpr_write_b32 a143, v99\n\t"
"v_fma_f32 v98, v184, s64, -v86\n\t"
"v_fma_f32 v99, v185, s64, -v87\n\t"
"v_mfma_f32_16x16x32_bf16 a[140:143], a[236:239], a[184:187], a[140:143]\n\t"
"ds_read_b128 a[184:187], v244 offset:26624\n\t"
"v_accvgpr_write_b32 a148, v98\n\t"
"v_mfma_f32_16x16x32_bf16 a[140:143], v[236:239], a[188:191], a[140:143]\n\t"
"v_fma_f32 v98, v186, s64, -v88\n\t"
"v_accvgpr_write_b32 a149, v99\n\t"
"v_mfma_f32_16x16x32_bf16 a[140:143], a[208:211], a[192:195], a[140:143]\n\t"
"v_fma_f32 v99, v187, s64, -v89\n\t"
"v_accvgpr_write_b32 a150, v98\n\t"
"v_accvgpr_write_b32 a151, v99\n\t"
"v_fma_f32 v98, v188, s64, -v90\n\t"
"v_fma_f32 v99, v189, s64, -v91\n\t"
"v_mfma_f32_16x16x32_bf16 a[148:151], a[236:239], a[188:191], a[148:151]\n\t"
"s_mov_b32 s20, 86016\n\t"
"ds_read_b128 a[188:191], v244 offset:27136\n\t"
"v_accvgpr_write_b32 a156, v98\n\t"
"v_mfma_f32_16x16x32_bf16 a[148:151], v[236:239], a[192:195], a[148:151]\n\t"
"v_fma_f32 v98, v190, s64, -v92\n\t"
"v_accvgpr_write_b32 a157, v99\n\t"
"v_mfma_f32_16x16x32_bf16 a[148:151], a[208:211], a[196:199], a[148:151]\n\t"
"v_fma_f32 v99, v191, s64, -v93\n\t"
"v_accvgpr_write_b32 a158, v98\n\t"
"v_accvgpr_write_b32 a159, v99\n\t"
"v_fma_f32 v98, v192, s64, -v94\n\t"
"v_fma_f32 v99, v193, s64, -v95\n\t"
"v_mfma_f32_16x16x32_bf16 a[156:159], a[236:239], a[192:195], a[156:159]\n\t"
"ds_read_b128 a[192:195], v244 offset:27648\n\t"
"v_accvgpr_write_b32 a164, v98\n\t"
"v_mfma_f32_16x16x32_bf16 a[156:159], v[236:239], a[196:199], a[156:159]\n\t"
"v_fma_f32 v98, v194, s64, -v96\n\t"
"v_accvgpr_write_b32 a165, v99\n\t"
"v_mfma_f32_16x16x32_bf16 a[156:159], a[208:211], a[200:203], a[156:159]\n\t"
"s_add_u32 m0, s24, -128\n\t"
"s_add_u32 s28, s21, 0\n\t"
"v_fma_f32 v99, v195, s64, -v97\n\t"
"v_accvgpr_write_b32 a166, v98\n\t"
"v_accvgpr_write_b32 a167, v99\n\t"
"s_nop 1\n\t"
"v_mfma_f32_16x16x32_bf16 a[164:167], a[236:239], a[196:199], a[164:167]\n\t"
"buffer_load_dwordx4 a[236:239], v252, s[8:11], s20 offen offset:0 sc1\n\t"
"v_mfma_f32_16x16x32_bf16 a[164:167], v[236:239], a[200:203], a[164:167]\n\t"
"ds_read_b128 a[196:199], v244 offset:28160\n\t"
"v_fma_f32 v100, -v100, s66, v2\n\t"
"v_mfma_f32_16x16x32_bf16 a[164:167], a[208:211], a[204:207], a[164:167]\n\t"
"ds_read_b128 a[200:203], v244 offset:28672\n\t"
"v_fma_f32 v2, v4, s52, v102\n\t"
"v_fma_f32 v101, -v101, s66, v3\n\t"
"v_fma_f32 v3, v5, s52, v103\n\t"
"v_fma_f32 v102, -v102, s66, v4\n\t"
"v_fma_f32 v103, -v103, s66, v5\n\t"
"v_fma_f32 v4, v6, s52, v104\n\t"
"v_fma_f32 v5, v7, s52, v105\n\t"
"Lst21_%=:\n\t"
"s_waitcnt vmcnt(11) lgkmcnt(6)\n\t"
"v_mfma_f32_16x16x32_bf16 v[0:3], v[240:243], a[168:171], v[0:3]\n\t"
"buffer_load_dwordx4 v247, s[12:15], s28 offen offset:128 nt lds\n\t"
"v_mfma_f32_16x16x32_bf16 v[0:3], a[212:215], a[172:175], v[0:3]\n\t"
"s_add_u32 m0, s24, 6016\n\t"
"s_add_u32 s28, s21, 196608\n\t"
"ds_read_b128 a[204:207], v244 offset:29184\n\t"
"v_mfma_f32_16x16x32_bf16 v[0:3], a[216:219], a[176:179], v[0:3]\n\t"
"buffer_load_dwordx4 v247, s[12:15], s28 offen offset:128 nt lds\n\t"
"v_fma_f32 v104, -v104, s66, v6\n\t"
"v_fma_f32 v6, v8, s52, v106\n\t"
"v_fma_f32 v105, -v105, s66, v7\n\t"
"v_fma_f32 v7, v9, s52, v107\n\t"
"v_fma_f32 v106, -v106, s66, v8\n\t"
"v_fma_f32 v107, -v107, s66, v9\n\t"
"v_fma_f32 v8, v10, s52, v108\n\t"
"v_fma_f32 v9, v11, s52, v109\n\t"
"s_waitcnt lgkmcnt(6)\n\t"
"v_mfma_f32_16x16x32_bf16 v[4:7], v[240:243], a[172:175], v[4:7]\n\t"
"s_add_u32 m0, s24, 59776\n\t"
"s_add_u32 s28, s21, 1769472\n\t"
"v_fma_f32 v108, -v108, s66, v10\n\t"
"v_mfma_f32_16x16x32_bf16 v[4:7], a[212:215], a[176:179], v[4:7]\n\t"
"v_fma_f32 v10, v12, s52, v110\n\t"
"v_fma_f32 v109, -v109, s66, v11\n\t"
"v_mfma_f32_16x16x32_bf16 v[4:7], a[216:219], a[180:183], v[4:7]\n\t"
"buffer_load_dwordx4 v247, s[12:15], s28 offen offset:128 nt lds\n\t"
"v_fma_f32 v11, v13, s52, v111\n\t"
"v_fma_f32 v110, -v110, s66, v12\n\t"
"v_fma_f32 v111, -v111, s66, v13\n\t"
"v_fma_f32 v12, v14, s52, v112\n\t"
"v_fma_f32 v13, v15, s52, v113\n\t"
"s_add_u32 m0, s24, 65920\n\t"
"s_add_u32 s28, s21, 1966080\n\t"
"s_waitcnt lgkmcnt(5)\n\t"
"v_mfma_f32_16x16x32_bf16 v[8:11], v[240:243], a[176:179], v[8:11]\n\t"
"buffer_load_dwordx4 v247, s[12:15], s28 offen offset:128 nt lds\n\t"
"v_mfma_f32_16x16x32_bf16 v[8:11], a[212:215], a[180:183], v[8:11]\n\t"
"s_add_u32 m0, s24, 12160\n\t"
"s_add_u32 s28, s21, 393216\n\t"
"v_fma_f32 v112, -v112, s66, v14\n\t"
"v_mfma_f32_16x16x32_bf16 v[8:11], a[216:219], a[184:187], v[8:11]\n\t"
"v_fma_f32 v14, v16, s52, v114\n\t"
"v_fma_f32 v113, -v113, s66, v15\n\t"
"v_fma_f32 v15, v17, s52, v115\n\t"
"v_fma_f32 v114, -v114, s66, v16\n\t"
"v_fma_f32 v115, -v115, s66, v17\n\t"
"v_fma_f32 v16, v18, s52, v116\n\t"
"v_fma_f32 v17, v19, s52, v117\n\t"
"s_waitcnt lgkmcnt(4)\n\t"
"v_mfma_f32_16x16x32_bf16 v[12:15], v[240:243], a[180:183], v[12:15]\n\t"
"buffer_load_dwordx4 v247, s[12:15], s28 offen offset:128 nt lds\n\t"
"v_mfma_f32_16x16x32_bf16 v[12:15], a[212:215], a[184:187], v[12:15]\n\t"
"s_add_u32 m0, s24, 19840\n\t"
"s_add_u32 s28, s21, 589824\n\t"
"v_fma_f32 v116, -v116, s66, v18\n\t"
"v_mfma_f32_16x16x32_bf16 v[12:15], a[216:219], a[188:191], v[12:15]\n\t"
"buffer_load_dwordx4 v247, s[12:15], s28 offen offset:128 nt lds\n\t"
"v_fma_f32 v18, v20, s52, v118\n\t"
"v_fma_f32 v117, -v117, s66, v19\n\t"
"v_fma_f32 v19, v21, s52, v119\n\t"
"v_fma_f32 v118, -v118, s66, v20\n\t"
"v_fma_f32 v119, -v119, s66, v21\n\t"
"v_fma_f32 v20, v22, s52, v120\n\t"
"v_fma_f32 v21, v23, s52, v121\n\t"
"s_waitcnt lgkmcnt(3)\n\t"
"v_mfma_f32_16x16x32_bf16 v[16:19], v[240:243], a[184:187], v[16:19]\n\t"
"v_fma_f32 v120, -v120, s66, v22\n\t"
"v_fma_f32 v22, v24, s52, v122\n\t"
"v_mfma_f32_16x16x32_bf16 v[16:19], a[212:215], a[188:191], v[16:19]\n\t"
"v_fma_f32 v121, -v121, s66, v23\n\t"
"v_fma_f32 v23, v25, s52, v123\n\t"
"v_mfma_f32_16x16x32_bf16 v[16:19], a[216:219], a[192:195], v[16:19]\n\t"
"ds_read_b64_tr_b16 v[232:233], v246 offset:1024\n\t"
"v_fma_f32 v122, -v122, s66, v24\n\t"
"v_fma_f32 v123, -v123, s66, v25\n\t"
"v_fma_f32 v24, v26, s52, v124\n\t"
"v_fma_f32 v25, v27, s52, v125\n\t"
"s_waitcnt lgkmcnt(3)\n\t"
"v_mfma_f32_16x16x32_bf16 v[20:23], v[240:243], a[188:191], v[20:23]\n\t"
"ds_read_b64_tr_b16 v[234:235], v246 offset:4096\n\t"
"v_fma_f32 v124, -v124, s66, v26\n\t"
"v_mfma_f32_16x16x32_bf16 v[20:23], a[212:215], a[192:195], v[20:23]\n\t"
"v_fma_f32 v26, v28, s52, v126\n\t"
"v_fma_f32 v125, -v125, s66, v27\n\t"
"v_mfma_f32_16x16x32_bf16 v[20:23], a[216:219], a[196:199], v[20:23]\n\t"
"v_fma_f32 v27, v29, s52, v127\n\t"
"v_fma_f32 v126, -v126, s66, v28\n\t"
"v_fma_f32 v127, -v127, s66, v29\n\t"
"v_fma_f32 v28, v30, s52, v128\n\t"
"v_fma_f32 v29, v31, s52, v129\n\t"
"s_waitcnt lgkmcnt(3)\n\t"
"v_mfma_f32_16x16x32_bf16 v[24:27], v[240:243], a[192:195], v[24:27]\n\t"
"Lpg16_%=:\n\t"
"s_cbranch_vccnz Lvr1_%=\n\t"
"v_fma_f32 v128, -v128, s66, v30\n\t"
"v_fma_f32 v30, v32, s52, v130\n\t"
"v_mfma_f32_16x16x32_bf16 v[24:27], a[212:215], a[196:199], v[24:27]\n\t"
"v_fma_f32 v129, -v129, s66, v31\n\t"
"v_fma_f32 v31, v33, s52, v131\n\t"
"v_mfma_f32_16x16x32_bf16 v[24:27], a[216:219], a[200:203], v[24:27]\n\t"
"v_fma_f32 v130, -v130, s66, v32\n\t"
"v_fma_f32 v131, -v131, s66, v33\n\t"
"v_fma_f32 v32, v34, s52, v132\n\t"
"v_fma_f32 v33, v35, s52, v133\n\t"
"s_waitcnt lgkmcnt(2)\n\t"
"v_mfma_f32_16x16x32_bf16 v[28:31], v[240:243], a[196:199], v[28:31]\n\t"
"ds_read_b64_tr_b16 v[228:229], v246 offset:7168\n\t"
"v_fma_f32 v132, -v132, s66, v34\n\t"
"v_mfma_f32_16x16x32_bf16 v[28:31], a[212:215], a[200:203], v[28:31]\n\t"
"ds_read_b64_tr_b16 v[230:231], v246 offset:10240\n\t"
"v_fma_f32 v34, v36, s52, v134\n\t"
"v_mfma_f32_16x16x32_bf16 v[28:31], a[216:219], a[204:207], v[28:31]\n\t"
"v_fma_f32 v133, -v133, s66, v35\n\t"
"v_fma_f32 v35, v37, s52, v135\n\t"
"v_fma_f32 v134, -v134, s66, v36\n\t"
"v_fma_f32 v135, -v135, s66, v37\n\t"
"v_fma_f32 v36, v38, s52, v136\n\t"
"v_fma_f32 v37, v39, s52, v137\n\t"
"s_waitcnt vmcnt(13)\n\t"
"v_mfma_f32_16x16x32_bf16 v[32:35], a[220:223], a[168:171], v[32:35]\n\t"
"v_fma_f32 v136, -v136, s66, v38\n\t"
"v_fma_f32 v38, v40, s52, v138\n\t"
"v_mfma_f32_16x16x32_bf16 v[32:35], a[224:227], a[172:175], v[32:35]\n\t"
"v_fma_f32 v137, -v137, s66, v39\n\t"
"v_fma_f32 v39, v41, s52, v139\n\t"
"v_mfma_f32_16x16x32_bf16 v[32:35], v[248:251], a[176:179], v[32:35]\n\t"
"v_fma_f32 v138, -v138, s66, v40\n\t"
"v_fma_f32 v139, -v139, s66, v41\n\t"
"v_mfma_f32_16x16x32_bf16 v[36:39], a[220:223], a[172:175], v[36:39]\n\t"
"s_waitcnt lgkmcnt(2)\n\t"
"v_fma_f32 v40, v42, s52, v140\n\t"
"v_fma_f32 v41, v43, s52, v141\n\t"
"v_mfma_f32_16x16x32_bf16 v[36:39], a[224:227], a[176:179], v[36:39]\n\t"
"v_fma_f32 v140, -v140, s66, v42\n\t"
"v_fma_f32 v42, v44, s52, v142\n\t"
"v_mfma_f32_16x16x32_bf16 v[36:39], v[248:251], a[180:183], v[36:39]\n\t"
"v_fma_f32 v141, -v141, s66, v43\n\t"
"v_fma_f32 v43, v45, s52, v143\n\t"
"v_fma_f32 v142, -v142, s66, v44\n\t"
"v_fma_f32 v143, -v143, s66, v45\n\t"
"v_mfma_f32_16x16x32_bf16 v[232:235], v[232:235], v[220:223], 0\n\t"
"v_fma_f32 v44, v46, s52, v144\n\t"
"v_fma_f32 v45, v47, s52, v145\n\t"
"v_mfma_f32_16x16x32_bf16 v[40:43], a[220:223], a[176:179], v[40:43]\n\t"
"ds_read_b64_tr_b16 v[224:225], v246 offset:13312\n\t"
"v_fma_f32 v144, -v144, s66, v46\n\t"
"v_mfma_f32_16x16x32_bf16 v[40:43], a[224:227], a[180:183], v[40:43]\n\t"
"ds_read_b64_tr_b16 v[226:227], v246 offset:16384\n\t"
"v_fma_f32 v46, v48, s52, v146\n\t"
"v_mfma_f32_16x16x32_bf16 v[40:43], v[248:251], a[184:187], v[40:43]\n\t"
"buffer_load_dwordx4 v[240:243], v252, s[8:11], s20 offen offset:1024 sc1\n\t"
"v_fma_f32 v145, -v145, s66, v47\n\t"
"v_fma_f32 v47, v49, s52, v147\n\t"
"v_fma_f32 v146, -v146, s66, v48\n\t"
"v_fma_f32 v147, -v147, s66, v49\n\t"
"v_mfma_f32_16x16x32_bf16 v[44:47], a[220:223], a[180:183], v[44:47]\n\t"
"s_waitcnt lgkmcnt(2)\n\t"
"v_fma_f32 v48, v50, s52, v148\n\t"
"v_fma_f32 v49, v51, s52, v149\n\t"
"v_mfma_f32_16x16x32_bf16 v[44:47], a[224:227], a[184:187], v[44:47]\n\t"
"v_fma_f32 v148, -v148, s66, v50\n\t"
"v_fma_f32 v50, v52, s52, v150\n\t"
"v_mfma_f32_16x16x32_bf16 v[44:47], v[248:251], a[188:191], v[44:47]\n\t"
"v_fma_f32 v149, -v149, s66, v51\n\t"
"v_fma_f32 v51, v53, s52, v151\n\t"
"v_fma_f32 v150, -v150, s66, v52\n\t"
"v_fma_f32 v151, -v151, s66, v53\n\t"
"v_mfma_f32_16x16x32_bf16 v[228:231], v[228:231], v[220:223], 0\n\t"
"v_fma_f32 v52, v54, s52, v152\n\t"
"v_fma_f32 v53, v55, s52, v153\n\t"
"v_mfma_f32_16x16x32_bf16 v[48:51], a[220:223], a[184:187], v[48:51]\n\t"
"v_fma_f32 v152, -v152, s66, v54\n\t"
"v_fma_f32 v54, v56, s52, v154\n\t"
"v_mfma_f32_16x16x32_bf16 v[48:51], a[224:227], a[188:191], v[48:51]\n\t"
"v_fma_f32 v153, -v153, s66, v55\n\t"
"v_fma_f32 v55, v57, s52, v155\n\t"
"v_mfma_f32_16x16x32_bf16 v[48:51], v[248:251], a[192:195], v[48:51]\n\t"
"v_fma_f32 v154, -v154, s66, v56\n\t"
"v_fma_f32 v155, -v155, s66, v57\n\t"
"v_cvt_pk_bf16_f32 v232, v232, v233\n\t"
"v_cvt_pk_bf16_f32 v233, v234, v235\n\t"
"v_fma_f32 v56, v58, s52, v156\n\t"
"v_fma_f32 v57, v59, s52, v157\n\t"
"v_mfma_f32_16x16x32_bf16 v[52:55], a[220:223], a[188:191], v[52:55]\n\t"
"v_fma_f32 v156, -v156, s66, v58\n\t"
"v_fma_f32 v58, v60, s52, v158\n\t"
"v_mfma_f32_16x16x32_bf16 v[52:55], a[224:227], a[192:195], v[52:55]\n\t"
"v_fma_f32 v157, -v157, s66, v59\n\t"
"v_fma_f32 v59, v61, s52, v159\n\t"
"v_mfma_f32_16x16x32_bf16 v[52:55], v[248:251], a[196:199], v[52:55]\n\t"
"v_fma_f32 v158, -v158, s66, v60\n\t"
"v_fma_f32 v159, -v159, s66, v61\n\t"
"v_mfma_f32_16x16x32_bf16 v[56:59], a[220:223], a[192:195], v[56:59]\n\t"
"s_waitcnt lgkmcnt(0)\n\t"
"v_fma_f32 v60, v62, s52, v160\n\t"
"v_fma_f32 v61, v63, s52, v161\n\t"
"v_mfma_f32_16x16x32_bf16 v[56:59], a[224:227], a[196:199], v[56:59]\n\t"
"v_fma_f32 v160, -v160, s66, v62\n\t"
"v_fma_f32 v62, v64, s52, v162\n\t"
"v_mfma_f32_16x16x32_bf16 v[56:59], v[248:251], a[200:203], v[56:59]\n\t"
"v_fma_f32 v161, -v161, s66, v63\n\t"
"v_fma_f32 v63, v65, s52, v163\n\t"
"v_fma_f32 v162, -v162, s66, v64\n\t"
"v_fma_f32 v163, -v163, s66, v65\n\t"
"v_cvt_pk_bf16_f32 v234, v228, v229\n\t"
"v_cvt_pk_bf16_f32 v235, v230, v231\n\t"
"v_mfma_f32_16x16x32_bf16 v[224:227], v[224:227], v[220:223], 0\n\t"
"ds_read_b64_tr_b16 v[228:229], v246 offset:20992\n\t"
"v_fma_f32 v64, v66, s52, v164\n\t"
"v_fma_f32 v65, v67, s52, v165\n\t"
"v_mfma_f32_16x16x32_bf16 v[60:63], a[220:223], a[196:199], v[60:63]\n\t"
"ds_read_b64_tr_b16 v[230:231], v246 offset:24064\n\t"
"v_fma_f32 v164, -v164, s66, v66\n\t"
"v_mfma_f32_16x16x32_bf16 v[60:63], a[224:227], a[200:203], v[60:63]\n\t"
"v_fma_f32 v66, v68, s52, v166\n\t"
"v_fma_f32 v165, -v165, s66, v67\n\t"
"v_mfma_f32_16x16x32_bf16 v[60:63], v[248:251], a[204:207], v[60:63]\n\t"
"v_fma_f32 v67, v69, s52, v167\n\t"
"v_fma_f32 v166, -v166, s66, v68\n\t"
"v_fma_f32 v167, -v167, s66, v69\n\t"
"v_fma_f32 v68, v70, s52, v168\n\t"
"v_fma_f32 v69, v71, s52, v169\n\t"
"s_waitcnt vmcnt(11)\n\t"
"v_mfma_f32_16x16x32_bf16 v[64:67], a[228:231], a[168:171], v[64:67]\n\t"
"ds_read_b128 a[168:171], v244 offset:33792\n\t"
"v_fma_f32 v168, -v168, s66, v70\n\t"
"v_mfma_f32_16x16x32_bf16 v[64:67], a[232:235], a[172:175], v[64:67]\n\t"
"v_fma_f32 v70, v72, s52, v170\n\t"
"v_fma_f32 v169, -v169, s66, v71\n\t"
"v_mfma_f32_16x16x32_bf16 v[64:67], a[240:243], a[176:179], v[64:67]\n\t"
"v_fma_f32 v71, v73, s52, v171\n\t"
"v_fma_f32 v170, -v170, s66, v72\n\t"
"v_fma_f32 v171, -v171, s66, v73\n\t"
"v_mfma_f32_16x16x32_bf16 v[68:71], a[228:231], a[172:175], v[68:71]\n\t"
"s_waitcnt lgkmcnt(1)\n\t"
"ds_read_b128 a[172:175], v244 offset:34304\n\t"
"v_fma_f32 v72, v74, s52, v172\n\t"
"v_mfma_f32_16x16x32_bf16 v[68:71], a[232:235], a[176:179], v[68:71]\n\t"
"v_fma_f32 v73, v75, s52, v173\n\t"
"v_fma_f32 v172, -v172, s66, v74\n\t"
"v_mfma_f32_16x16x32_bf16 v[68:71], a[240:243], a[180:183], v[68:71]\n\t"
"v_fma_f32 v74, v76, s52, v174\n\t"
"v_fma_f32 v173, -v173, s66, v75\n\t"
"v_fma_f32 v75, v77, s52, v175\n\t"
"v_fma_f32 v174, -v174, s66, v76\n\t"
"v_fma_f32 v175, -v175, s66, v77\n\t"
"v_cvt_pk_bf16_f32 v224, v224, v225\n\t"
"v_cvt_pk_bf16_f32 v225, v226, v227\n\t"
"v_mfma_f32_16x16x32_bf16 v[228:231], v[228:231], v[220:223], 0\n\t"
"buffer_load_dwordx4 a[208:211], v252, s[8:11], s20 offen offset:2048 sc1\n\t"
"v_fma_f32 v76, v78, s52, v176\n\t"
"v_fma_f32 v77, v79, s52, v177\n\t"
"s_mov_b32 s20, 98304\n\t"
"v_mfma_f32_16x16x32_bf16 v[72:75], a[228:231], a[176:179], v[72:75]\n\t"
"buffer_load_dwordx4 v[236:239], v252, s[8:11], s20 offen offset:0 sc1\n\t"
"v_mfma_f32_16x16x32_bf16 v[72:75], a[232:235], a[180:183], v[72:75]\n\t"
"ds_read_b128 a[176:179], v244 offset:34816\n\t"
"v_fma_f32 v176, -v176, s66, v78\n\t"
"v_mfma_f32_16x16x32_bf16 v[72:75], a[240:243], a[184:187], v[72:75]\n\t"
"v_fma_f32 v78, v80, s52, v178\n\t"
"v_fma_f32 v177, -v177, s66, v79\n\t"
"v_fma_f32 v79, v81, s52, v179\n\t"
"v_fma_f32 v178, -v178, s66, v80\n\t"
"v_fma_f32 v179, -v179, s66, v81\n\t"
"v_fma_f32 v80, v82, s52, v180\n\t"
"v_fma_f32 v81, v83, s52, v181\n\t"
"v_mfma_f32_16x16x32_bf16 v[76:79], a[228:231], a[180:183], v[76:79]\n\t"
"ds_read_b128 a[180:183], v244 offset:35328\n\t"
"v_fma_f32 v180, -v180, s66, v82\n\t"
"v_mfma_f32_16x16x32_bf16 v[76:79], a[232:235], a[184:187], v[76:79]\n\t"
"v_fma_f32 v82, v84, s52, v182\n\t"
"v_fma_f32 v181, -v181, s66, v83\n\t"
"v_mfma_f32_16x16x32_bf16 v[76:79], a[240:243], a[188:191], v[76:79]\n\t"
"buffer_load_dwordx4 v[248:251], v252, s[8:11], s20 offen offset:1024 sc1\n\t"
"v_fma_f32 v83, v85, s52, v183\n\t"
"v_fma_f32 v182, -v182, s66, v84\n\t"
"v_fma_f32 v183, -v183, s66, v85\n\t"
"v_fma_f32 v84, v86, s52, v184\n\t"
"v_fma_f32 v85, v87, s52, v185\n\t"
"v_mfma_f32_16x16x32_bf16 v[80:83], a[228:231], a[184:187], v[80:83]\n\t"
"ds_read_b128 a[184:187], v244 offset:35840\n\t"
"v_fma_f32 v184, -v184, s66, v86\n\t"
"v_mfma_f32_16x16x32_bf16 v[80:83], a[232:235], a[188:191], v[80:83]\n\t"
"v_fma_f32 v86, v88, s52, v186\n\t"
"v_fma_f32 v185, -v185, s66, v87\n\t"
"v_mfma_f32_16x16x32_bf16 v[80:83], a[240:243], a[192:195], v[80:83]\n\t"
"buffer_load_dwordx4 a[212:215], v252, s[8:11], s20 offen offset:2048 sc1\n\t"
"v_fma_f32 v87, v89, s52, v187\n\t"
"v_fma_f32 v186, -v186, s66, v88\n\t"
"v_fma_f32 v187, -v187, s66, v89\n\t"
"v_cvt_pk_bf16_f32 v226, v228, v229\n\t"
"v_cvt_pk_bf16_f32 v227, v230, v231\n\t"
"v_fma_f32 v88, v90, s52, v188\n\t"
"v_fma_f32 v89, v91, s52, v189\n\t"
"s_mov_b32 s20, 331776\n\t"
"v_mfma_f32_16x16x32_bf16 v[84:87], a[228:231], a[188:191], v[84:87]\n\t"
"ds_read_b64_tr_b16 v[228:229], v246 offset:27136\n\t"
"v_fma_f32 v188, -v188, s66, v90\n\t"
"v_mfma_f32_16x16x32_bf16 v[84:87], a[232:235], a[192:195], v[84:87]\n\t"
"ds_read_b64_tr_b16 v[230:231], v246 offset:30208\n\t"
"v_fma_f32 v90, v92, s52, v190\n\t"
"v_mfma_f32_16x16x32_bf16 v[84:87], a[240:243], a[196:199], v[84:87]\n\t"
"buffer_load_dwordx4 a[216:219], v252, s[8:11], s20 offen offset:0 sc1\n\t"
"v_fma_f32 v189, -v189, s66, v91\n\t"
"v_fma_f32 v91, v93, s52, v191\n\t"
"v_fma_f32 v190, -v190, s66, v92\n\t"
"v_fma_f32 v191, -v191, s66, v93\n\t"
"v_fma_f32 v92, v94, s52, v192\n\t"
"v_fma_f32 v93, v95, s52, v193\n\t"
"v_mfma_f32_16x16x32_bf16 v[88:91], a[228:231], a[192:195], v[88:91]\n\t"
"ds_read_b128 a[188:191], v244 offset:36352\n\t"
"v_fma_f32 v192, -v192, s66, v94\n\t"
"v_mfma_f32_16x16x32_bf16 v[88:91], a[232:235], a[196:199], v[88:91]\n\t"
"ds_read_b128 a[192:195], v244 offset:36864\n\t"
"v_fma_f32 v94, v96, s52, v194\n\t"
"v_mfma_f32_16x16x32_bf16 v[88:91], a[240:243], a[200:203], v[88:91]\n\t"
"buffer_load_dwordx4 a[220:223], v252, s[8:11], s20 offen offset:1024 sc1\n\t"
"v_fma_f32 v193, -v193, s66, v95\n\t"
"v_fma_f32 v95, v97, s52, v195\n\t"
"v_fma_f32 v194, -v194, s66, v96\n\t"
"v_fma_f32 v195, -v195, s66, v97\n\t"
"v_mfma_f32_16x16x32_bf16 v[92:95], a[228:231], a[196:199], v[92:95]\n\t"
"s_waitcnt lgkmcnt(2)\n\t"
"ds_read_b128 a[196:199], v244 offset:37376\n\t"
"v_mfma_f32_16x16x32_bf16 v[92:95], a[232:235], a[200:203], v[92:95]\n\t"
"buffer_load_dwordx4 v[96:99], v252, s[8:11], s20 offen offset:2048 sc1\n\t"
"v_mfma_f32_16x16x32_bf16 v[92:95], a[240:243], a[204:207], v[92:95]\n\t"
"s_waitcnt vmcnt(15)\n\t"
"ds_read_b128 a[200:203], v244 offset:37888\n\t"
"s_mov_b32 s20, 344064\n\t"
"v_mfma_f32_16x16x32_bf16 v[228:231], v[228:231], v[220:223], 0\n\t"
"ds_read_b128 a[204:207], v244 offset:38400\n\t"
"v_mfma_f32_16x16x32_bf16 v[100:103], a[244:247], a[168:171], v[100:103]\n\t"
"buffer_load_dwordx4 a[224:227], v252, s[8:11], s20 offen offset:0 sc1\n\t"
"v_mfma_f32_16x16x32_bf16 v[100:103], a[248:251], a[172:175], v[100:103]\n\t"
"buffer_load_dwordx4 a[228:231], v252, s[8:11], s20 offen offset:1024 sc1\n\t"
"v_mfma_f32_16x16x32_bf16 v[100:103], a[252:255], a[176:179], v[100:103]\n\t"
"v_mfma_f32_16x16x32_bf16 v[104:107], a[244:247], a[172:175], v[104:107]\n\t"
"v_mfma_f32_16x16x32_bf16 v[104:107], a[248:251], a[176:179], v[104:107]\n\t"
"v_cvt_pk_bf16_f32 v228, v228, v229\n\t"
"v_mfma_f32_16x16x32_bf16 v[104:107], a[252:255], a[180:183], v[104:107]\n\t"
"s_waitcnt lgkmcnt(0)\n\t"
"s_barrier\n\t"
"v_mfma_f32_16x16x32_bf16 v[108:111], a[244:247], a[176:179], v[108:111]\n\t"
"v_cvt_pk_bf16_f32 v229, v230, v231\n\t"
"v_mfma_f32_16x16x32_bf16 v[108:111], a[248:251], a[180:183], v[108:111]\n\t"
"buffer_load_dwordx4 a[232:235], v252, s[8:11], s20 offen offset:2048 sc1\n\t"
"v_mfma_f32_16x16x32_bf16 v[108:111], a[252:255], a[184:187], v[108:111]\n\t"
"s_mov_b32 s20, 356352\n\t"
"ds_write2st64_b64 v245, v[232:233], v[234:235] offset0:48 offset1:49\n\t"
"v_mfma_f32_16x16x32_bf16 v[112:115], a[244:247], a[180:183], v[112:115]\n\t"
"buffer_load_dwordx4 a[240:243], v252, s[8:11], s20 offen offset:0 sc1\n\t"
"v_mfma_f32_16x16x32_bf16 v[112:115], a[248:251], a[184:187], v[112:115]\n\t"
"ds_read_b64_tr_b16 v[232:233], v246 offset:33280\n\t"
"v_mfma_f32_16x16x32_bf16 v[112:115], a[252:255], a[188:191], v[112:115]\n\t"
"ds_write2st64_b64 v245, v[224:225], v[226:227] offset0:50 offset1:51\n\t"
"v_mfma_f32_16x16x32_bf16 v[116:119], a[244:247], a[184:187], v[116:119]\n\t"
"v_mfma_f32_16x16x32_bf16 v[116:119], a[248:251], a[188:191], v[116:119]\n\t"
"ds_read_b64_tr_b16 v[234:235], v246 offset:36352\n\t"
"v_mfma_f32_16x16x32_bf16 v[116:119], a[252:255], a[192:195], v[116:119]\n\t"
"ds_write_b64 v245, v[228:229] offset:26624\n\t"
"v_mfma_f32_16x16x32_bf16 v[120:123], a[244:247], a[188:191], v[120:123]\n\t"
"ds_read_b64_tr_b16 v[224:225], v246 offset:40960\n\t"
"v_mfma_f32_16x16x32_bf16 v[120:123], a[248:251], a[192:195], v[120:123]\n\t"
"ds_read_b64_tr_b16 v[226:227], v246 offset:44032\n\t"
"v_mfma_f32_16x16x32_bf16 v[120:123], a[252:255], a[196:199], v[120:123]\n\t"
"s_waitcnt lgkmcnt(3)\n\t"
"ds_read_b64_tr_b16 v[228:229], v246 offset:47104\n\t"
"v_mfma_f32_16x16x32_bf16 v[232:235], v[232:235], v[220:223], 0\n\t"
"ds_read_b64_tr_b16 v[230:231], v246 offset:50176\n\t"
"v_mfma_f32_16x16x32_bf16 v[124:127], a[244:247], a[192:195], v[124:127]\n\t"
"v_mfma_f32_16x16x32_bf16 v[124:127], a[248:251], a[196:199], v[124:127]\n\t"
"v_mfma_f32_16x16x32_bf16 v[124:127], a[252:255], a[200:203], v[124:127]\n\t"
"s_waitcnt lgkmcnt(2)\n\t"
"v_mfma_f32_16x16x32_bf16 v[224:227], v[224:227], v[220:223], 0\n\t"
"v_mfma_f32_16x16x32_bf16 v[128:131], a[244:247], a[196:199], v[128:131]\n\t"
"buffer_load_dwordx4 a[244:247], v252, s[8:11], s20 offen offset:1024 sc1\n\t"
"v_mfma_f32_16x16x32_bf16 v[128:131], a[248:251], a[200:203], v[128:131]\n\t"
"s_waitcnt vmcnt(11) lgkmcnt(0)\n\t"
"v_cvt_pk_bf16_f32 v232, v232, v233\n\t"
"v_cvt_pk_bf16_f32 v233, v234, v235\n\t"
"v_mfma_f32_16x16x32_bf16 v[128:131], a[252:255], a[204:207], v[128:131]\n\t"
"v_mfma_f32_16x16x32_bf16 v[228:231], v[228:231], v[220:223], 0\n\t"
"buffer_load_dwordx4 a[248:251], v252, s[8:11], s20 offen offset:2048 sc1\n\t"
"v_mfma_f32_16x16x32_bf16 v[132:135], a[236:239], a[168:171], v[132:135]\n\t"
"s_mov_b32 s20, 368640\n\t"
"v_cvt_pk_bf16_f32 v234, v224, v225\n\t"
"v_cvt_pk_bf16_f32 v235, v226, v227\n\t"
"v_mfma_f32_16x16x32_bf16 v[132:135], v[240:243], a[172:175], v[132:135]\n\t"
"ds_read_b64_tr_b16 v[224:225], v246 offset:53248\n\t"
"v_mfma_f32_16x16x32_bf16 v[132:135], a[208:211], a[176:179], v[132:135]\n\t"
"buffer_load_dwordx4 a[252:255], v252, s[8:11], s20 offen offset:0 sc1\n\t"
"v_mfma_f32_16x16x32_bf16 v[136:139], a[236:239], a[172:175], v[136:139]\n\t"
"ds_read_b64_tr_b16 v[226:227], v246 offset:56320\n\t"
"v_mfma_f32_16x16x32_bf16 v[136:139], v[240:243], a[176:179], v[136:139]\n\t"
"v_cvt_pk_bf16_f32 v228, v228, v229\n\t"
"v_cvt_pk_bf16_f32 v229, v230, v231\n\t"
"v_mfma_f32_16x16x32_bf16 v[136:139], a[208:211], a[180:183], v[136:139]\n\t"
"ds_write2st64_b64 v245, v[232:233], v[234:235] offset0:53 offset1:54\n\t"
"v_mfma_f32_16x16x32_bf16 v[140:143], a[236:239], a[176:179], v[140:143]\n\t"
"v_mfma_f32_16x16x32_bf16 v[140:143], v[240:243], a[180:183], v[140:143]\n\t"
"v_mfma_f32_16x16x32_bf16 v[140:143], a[208:211], a[184:187], v[140:143]\n\t"
"v_mfma_f32_16x16x32_bf16 v[144:147], a[236:239], a[180:183], v[144:147]\n\t"
"v_mfma_f32_16x16x32_bf16 v[144:147], v[240:243], a[184:187], v[144:147]\n\t"
"v_mfma_f32_16x16x32_bf16 v[144:147], a[208:211], a[188:191], v[144:147]\n\t"
"v_mfma_f32_16x16x32_bf16 v[148:151], a[236:239], a[184:187], v[148:151]\n\t"
"s_waitcnt lgkmcnt(1)\n\t"
"s_add_u32 m0, s24, 25984\n\t"
"s_add_u32 s28, s21, 786432\n\t"
"v_mfma_f32_16x16x32_bf16 v[148:151], v[240:243], a[188:191], v[148:151]\n\t"
"v_mfma_f32_16x16x32_bf16 v[148:151], a[208:211], a[192:195], v[148:151]\n\t"
"v_mfma_f32_16x16x32_bf16 v[224:227], v[224:227], v[220:223], 0\n\t"
"v_mfma_f32_16x16x32_bf16 v[152:155], a[236:239], a[188:191], v[152:155]\n\t"
"buffer_load_dwordx4 v247, s[12:15], s28 offen offset:128 nt lds\n\t"
"v_mfma_f32_16x16x32_bf16 v[152:155], v[240:243], a[192:195], v[152:155]\n\t"
"s_add_u32 m0, s24, 72064\n\t"
"s_add_u32 s28, s21, 2162688\n\t"
"v_mfma_f32_16x16x32_bf16 v[152:155], a[208:211], a[196:199], v[152:155]\n\t"
"buffer_load_dwordx4 v247, s[12:15], s28 offen offset:128 nt lds\n\t"
"v_mfma_f32_16x16x32_bf16 v[156:159], a[236:239], a[192:195], v[156:159]\n\t"
"s_add_u32 m0, s24, 79744\n\t"
"s_add_u32 s28, s21, 2359296\n\t"
"v_mfma_f32_16x16x32_bf16 v[156:159], v[240:243], a[196:199], v[156:159]\n\t"
"buffer_load_dwordx4 v247, s[12:15], s28 offen offset:128 nt lds\n\t"
"v_mfma_f32_16x16x32_bf16 v[156:159], a[208:211], a[200:203], v[156:159]\n\t"
"s_add_u32 m0, s24, 85888\n\t"
"s_add_u32 s28, s21, 2555904\n\t"
"v_cvt_pk_bf16_f32 v230, v224, v225\n\t"
"v_cvt_pk_bf16_f32 v231, v226, v227\n\t"
"v_mfma_f32_16x16x32_bf16 v[160:163], a[236:239], a[196:199], v[160:163]\n\t"
"s_waitcnt vmcnt(13)\n\t"
"v_mfma_f32_16x16x32_bf16 v[160:163], v[240:243], a[200:203], v[160:163]\n\t"
"buffer_load_dwordx4 v247, s[12:15], s28 offen offset:128 nt lds\n\t"
"v_mfma_f32_16x16x32_bf16 v[160:163], a[208:211], a[204:207], v[160:163]\n\t"
"s_add_u32 m0, s25, -128\n\t"
"ds_write2st64_b64 v245, v[228:229], v[230:231] offset0:55 offset1:56\n\t"
"v_mfma_f32_16x16x32_bf16 v[164:167], v[236:239], a[168:171], v[164:167]\n\t"
"buffer_load_dwordx4 v254, s[12:15], 0 offen offset:128 nt lds\n\t"
"v_mfma_f32_16x16x32_bf16 v[164:167], v[248:251], a[172:175], v[164:167]\n\t"
"s_add_u32 m0, s26, -128\n\t"
"v_mfma_f32_16x16x32_bf16 v[164:167], a[212:215], a[176:179], v[164:167]\n\t"
"s_waitcnt lgkmcnt(0)\n\t"
"s_barrier\n\t"
"v_mfma_f32_16x16x32_bf16 v[168:171], v[236:239], a[172:175], v[168:171]\n\t"
"ds_read_b128 a[168:171], v244 offset:24576\n\t"
"v_mfma_f32_16x16x32_bf16 v[168:171], v[248:251], a[176:179], v[168:171]\n\t"
"ds_read_b128 a[172:175], v244 offset:25088\n\t"
"v_mfma_f32_16x16x32_bf16 v[168:171], a[212:215], a[180:183], v[168:171]\n\t"
"s_mov_b64 exec, s[36:37]\n\t"
"buffer_load_dwordx4 v254, s[12:15], s27 offen offset:128 nt lds\n\t"
"s_mov_b64 exec, -1\n\t"
"v_mfma_f32_16x16x32_bf16 v[172:175], v[236:239], a[176:179], v[172:175]\n\t"
"ds_read_b128 a[176:179], v244 offset:25600\n\t"
"v_mfma_f32_16x16x32_bf16 v[172:175], v[248:251], a[180:183], v[172:175]\n\t"
"v_mfma_f32_16x16x32_bf16 v[172:175], a[212:215], a[184:187], v[172:175]\n\t"
"v_mfma_f32_16x16x32_bf16 v[176:179], v[236:239], a[180:183], v[176:179]\n\t"
"ds_read_b128 a[180:183], v244 offset:26112\n\t"
"v_mfma_f32_16x16x32_bf16 v[176:179], v[248:251], a[184:187], v[176:179]\n\t"
"v_mfma_f32_16x16x32_bf16 v[176:179], a[212:215], a[188:191], v[176:179]\n\t"
"v_mfma_f32_16x16x32_bf16 v[180:183], v[236:239], a[184:187], v[180:183]\n\t"
"ds_read_b128 a[184:187], v244 offset:26624\n\t"
"v_mfma_f32_16x16x32_bf16 v[180:183], v[248:251], a[188:191], v[180:183]\n\t"
"v_mfma_f32_16x16x32_bf16 v[180:183], a[212:215], a[192:195], v[180:183]\n\t"
"v_mfma_f32_16x16x32_bf16 v[184:187], v[236:239], a[188:191], v[184:187]\n\t"
"ds_read_b128 a[188:191], v244 offset:27136\n\t"
"v_mfma_f32_16x16x32_bf16 v[184:187], v[248:251], a[192:195], v[184:187]\n\t"
"v_mfma_f32_16x16x32_bf16 v[184:187], a[212:215], a[196:199], v[184:187]\n\t"
"v_mfma_f32_16x16x32_bf16 v[188:191], v[236:239], a[192:195], v[188:191]\n\t"
"ds_read_b128 a[192:195], v244 offset:27648\n\t"
"v_mfma_f32_16x16x32_bf16 v[188:191], v[248:251], a[196:199], v[188:191]\n\t"
"v_mfma_f32_16x16x32_bf16 v[188:191], a[212:215], a[200:203], v[188:191]\n\t"
"v_mfma_f32_16x16x32_bf16 v[192:195], v[236:239], a[196:199], v[192:195]\n\t"
"s_waitcnt vmcnt(13) lgkmcnt(4)\n\t"
"ds_read_b128 a[196:199], v244 offset:28160\n\t"
"v_mfma_f32_16x16x32_bf16 v[192:195], v[248:251], a[200:203], v[192:195]\n\t"
"ds_read_b128 a[200:203], v244 offset:28672\n\t"
"v_mfma_f32_16x16x32_bf16 v[192:195], a[212:215], a[204:207], v[192:195]\n\t"
"ds_read_b128 a[204:207], v244 offset:29184\n\t"
"Lst22_%=:\n\t"
"v_mfma_f32_16x16x32_bf16 v[0:3], a[216:219], a[168:171], v[0:3]\n\t"
"s_waitcnt lgkmcnt(6)\n\t"
"v_xor_b32 v246, 0xc0, v246\n\t"
"v_mfma_f32_16x16x32_bf16 v[0:3], a[220:223], a[172:175], v[0:3]\n\t"
"v_mfma_f32_16x16x32_bf16 v[0:3], v[96:99], a[176:179], v[0:3]\n\t"
"v_mfma_f32_16x16x32_bf16 v[4:7], a[216:219], a[172:175], v[4:7]\n\t"
"s_waitcnt lgkmcnt(5)\n\t"
"v_mfma_f32_16x16x32_bf16 v[4:7], a[220:223], a[176:179], v[4:7]\n\t"
"v_mfma_f32_16x16x32_bf16 v[4:7], v[96:99], a[180:183], v[4:7]\n\t"
"v_mfma_f32_16x16x32_bf16 v[8:11], a[216:219], a[176:179], v[8:11]\n\t"
"buffer_load_dwordx4 v[240:243], v252, s[8:11], s20 offen offset:1024 sc1\n\t"
"v_mfma_f32_16x16x32_bf16 v[8:11], a[220:223], a[180:183], v[8:11]\n\t"
"s_waitcnt lgkmcnt(4)\n\t"
"v_mfma_f32_16x16x32_bf16 v[8:11], v[96:99], a[184:187], v[8:11]\n\t"
"v_mfma_f32_16x16x32_bf16 v[12:15], a[216:219], a[180:183], v[12:15]\n\t"
"s_waitcnt lgkmcnt(3)\n\t"
"v_mfma_f32_16x16x32_bf16 v[12:15], a[220:223], a[184:187], v[12:15]\n\t"
"v_mfma_f32_16x16x32_bf16 v[12:15], v[96:99], a[188:191], v[12:15]\n\t"
"v_mfma_f32_16x16x32_bf16 v[16:19], a[216:219], a[184:187], v[16:19]\n\t"
"s_waitcnt lgkmcnt(2)\n\t"
"v_mfma_f32_16x16x32_bf16 v[16:19], a[220:223], a[188:191], v[16:19]\n\t"
"v_mfma_f32_16x16x32_bf16 v[16:19], v[96:99], a[192:195], v[16:19]\n\t"
"ds_read_b64_tr_b16 v[232:233], v246 offset:1024\n\t"
"v_mfma_f32_16x16x32_bf16 v[20:23], a[216:219], a[188:191], v[20:23]\n\t"
"s_waitcnt lgkmcnt(2)\n\t"
"ds_read_b64_tr_b16 v[234:235], v246 offset:4096\n\t"
"v_mfma_f32_16x16x32_bf16 v[20:23], a[220:223], a[192:195], v[20:23]\n\t"
"v_mfma_f32_16x16x32_bf16 v[20:23], v[96:99], a[196:199], v[20:23]\n\t"
"v_mfma_f32_16x16x32_bf16 v[24:27], a[216:219], a[192:195], v[24:27]\n\t"
"s_waitcnt lgkmcnt(2)\n\t"
"v_mfma_f32_16x16x32_bf16 v[24:27], a[220:223], a[196:199], v[24:27]\n\t"
"v_mfma_f32_16x16x32_bf16 v[24:27], v[96:99], a[200:203], v[24:27]\n\t"
"v_mfma_f32_16x16x32_bf16 v[28:31], a[216:219], a[196:199], v[28:31]\n\t"
"s_waitcnt vmcnt(11)\n\t"
"ds_read_b64_tr_b16 v[224:225], v246 offset:7168\n\t"
"v_mfma_f32_16x16x32_bf16 v[28:31], a[220:223], a[200:203], v[28:31]\n\t"
"ds_read_b64_tr_b16 v[226:227], v246 offset:10240\n\t"
"v_mfma_f32_16x16x32_bf16 v[28:31], v[96:99], a[204:207], v[28:31]\n\t"
"v_mfma_f32_16x16x32_bf16 v[32:35], a[224:227], a[168:171], v[32:35]\n\t"
"v_mfma_f32_16x16x32_bf16 v[32:35], a[228:231], a[172:175], v[32:35]\n\t"
"v_mfma_f32_16x16x32_bf16 v[32:35], a[232:235], a[176:179], v[32:35]\n\t"
"v_mfma_f32_16x16x32_bf16 v[36:39], a[224:227], a[172:175], v[36:39]\n\t"
"s_waitcnt lgkmcnt(2)\n\t"
"v_mfma_f32_16x16x32_bf16 v[36:39], a[228:231], a[176:179], v[36:39]\n\t"
"v_mfma_f32_16x16x32_bf16 v[36:39], a[232:235], a[180:183], v[36:39]\n\t"
"v_mfma_f32_16x16x32_bf16 v[232:235], v[232:235], v[220:223], 0\n\t"
"v_mfma_f32_16x16x32_bf16 v[40:43], a[224:227], a[176:179], v[40:43]\n\t"
"ds_read_b64_tr_b16 v[228:229], v246 offset:13312\n\t"
"v_mfma_f32_16x16x32_bf16 v[40:43], a[228:231], a[180:183], v[40:43]\n\t"
"ds_read_b64_tr_b16 v[230:231], v246 offset:16384\n\t"
"v_mfma_f32_16x16x32_bf16 v[40:43], a[232:235], a[184:187], v[40:43]\n\t"
"v_mfma_f32_16x16x32_bf16 v[44:47], a[224:227], a[180:183], v[44:47]\n\t"
"s_waitcnt lgkmcnt(2)\n\t"
"v_mfma_f32_16x16x32_bf16 v[44:47], a[228:231], a[184:187], v[44:47]\n\t"
"v_cvt_pk_bf16_f32 v232, v232, v233\n\t"
"v_cvt_pk_bf16_f32 v233, v234, v235\n\t"
"v_mfma_f32_16x16x32_bf16 v[44:47], a[232:235], a[188:191], v[44:47]\n\t"
"v_mfma_f32_16x16x32_bf16 v[224:227], v[224:227], v[220:223], 0\n\t"
"buffer_load_dwordx4 a[208:211], v252, s[8:11], s20 offen offset:2048 sc1\n\t"
"v_mfma_f32_16x16x32_bf16 v[48:51], a[224:227], a[184:187], v[48:51]\n\t"
"s_mov_b32 s20, 380928\n\t"
"v_mfma_f32_16x16x32_bf16 v[48:51], a[228:231], a[188:191], v[48:51]\n\t"
"v_mfma_f32_16x16x32_bf16 v[48:51], a[232:235], a[192:195], v[48:51]\n\t"
"v_mfma_f32_16x16x32_bf16 v[52:55], a[224:227], a[188:191], v[52:55]\n\t"
"buffer_load_dwordx4 v[236:239], v252, s[8:11], s20 offen offset:0 sc1\n\t"
"v_mfma_f32_16x16x32_bf16 v[52:55], a[228:231], a[192:195], v[52:55]\n\t"
"v_cvt_pk_bf16_f32 v234, v224, v225\n\t"
"v_cvt_pk_bf16_f32 v235, v226, v227\n\t"
"v_mfma_f32_16x16x32_bf16 v[52:55], a[232:235], a[196:199], v[52:55]\n\t"
"ds_read_b64_tr_b16 v[224:225], v246 offset:20992\n\t"
"v_mfma_f32_16x16x32_bf16 v[56:59], a[224:227], a[192:195], v[56:59]\n\t"
"s_waitcnt lgkmcnt(1)\n\t"
"ds_read_b64_tr_b16 v[226:227], v246 offset:24064\n\t"
"v_mfma_f32_16x16x32_bf16 v[56:59], a[228:231], a[196:199], v[56:59]\n\t"
"buffer_load_dwordx4 v[96:99], v252, s[8:11], s20 offen offset:1024 sc1\n\t"
"v_mfma_f32_16x16x32_bf16 v[56:59], a[232:235], a[200:203], v[56:59]\n\t"
"v_mfma_f32_16x16x32_bf16 v[228:231], v[228:231], v[220:223], 0\n\t"
"v_mfma_f32_16x16x32_bf16 v[60:63], a[224:227], a[196:199], v[60:63]\n\t"
"s_waitcnt vmcnt(11)\n\t"
"v_mfma_f32_16x16x32_bf16 v[60:63], a[228:231], a[200:203], v[60:63]\n\t"
"buffer_load_dwordx4 v[248:251], v252, s[8:11], s20 offen offset:2048 sc1\n\t"
"v_mfma_f32_16x16x32_bf16 v[60:63], a[232:235], a[204:207], v[60:63]\n\t"
"s_mov_b32 s20, 393216\n\t"
"v_mfma_f32_16x16x32_bf16 v[64:67], a[240:243], a[168:171], v[64:67]\n\t"
"ds_read_b128 a[168:171], v244 offset:33792\n\t"
"v_mfma_f32_16x16x32_bf16 v[64:67], a[244:247], a[172:175], v[64:67]\n\t"
"v_cvt_pk_bf16_f32 v228, v228, v229\n\t"
"v_cvt_pk_bf16_f32 v229, v230, v231\n\t"
"v_mfma_f32_16x16x32_bf16 v[64:67], a[248:251], a[176:179], v[64:67]\n\t"
"v_mfma_f32_16x16x32_bf16 v[68:71], a[240:243], a[172:175], v[68:71]\n\t"
"buffer_load_dwordx4 a[212:215], v252, s[8:11], s20 offen offset:0 sc1\n\t"
"v_mfma_f32_16x16x32_bf16 v[68:71], a[244:247], a[176:179], v[68:71]\n\t"
"s_waitcnt lgkmcnt(1)\n\t"
"ds_read_b128 a[172:175], v244 offset:34304\n\t"
"v_mfma_f32_16x16x32_bf16 v[68:71], a[248:251], a[180:183], v[68:71]\n\t"
"v_mfma_f32_16x16x32_bf16 v[224:227], v[224:227], v[220:223], 0\n\t"
"buffer_load_dwordx4 a[216:219], v252, s[8:11], s20 offen offset:1024 sc1\n\t"
"v_mfma_f32_16x16x32_bf16 v[72:75], a[240:243], a[176:179], v[72:75]\n\t"
"ds_read_b128 a[176:179], v244 offset:34816\n\t"
"v_mfma_f32_16x16x32_bf16 v[72:75], a[244:247], a[180:183], v[72:75]\n\t"
"v_mfma_f32_16x16x32_bf16 v[72:75], a[248:251], a[184:187], v[72:75]\n\t"
"v_mfma_f32_16x16x32_bf16 v[76:79], a[240:243], a[180:183], v[76:79]\n\t"
"buffer_load_dwordx4 a[220:223], v252, s[8:11], s20 offen offset:2048 sc1\n\t"
"v_mfma_f32_16x16x32_bf16 v[76:79], a[244:247], a[184:187], v[76:79]\n\t"
"s_mov_b32 s20, 405504\n\t"
"ds_read_b128 a[180:183], v244 offset:35328\n\t"
"v_cvt_pk_bf16_f32 v230, v224, v225\n\t"
"v_mfma_f32_16x16x32_bf16 v[76:79], a[248:251], a[188:191], v[76:79]\n\t"
"v_cvt_pk_bf16_f32 v231, v226, v227\n\t"
"v_mfma_f32_16x16x32_bf16 v[80:83], a[240:243], a[184:187], v[80:83]\n\t"
"ds_read_b64_tr_b16 v[224:225], v246 offset:27136\n\t"
"v_mfma_f32_16x16x32_bf16 v[80:83], a[244:247], a[188:191], v[80:83]\n\t"
"ds_read_b64_tr_b16 v[226:227], v246 offset:30208\n\t"
"v_mfma_f32_16x16x32_bf16 v[80:83], a[248:251], a[192:195], v[80:83]\n\t"
"buffer_load_dwordx4 a[224:227], v252, s[8:11], s20 offen offset:0 sc1\n\t"
"v_mfma_f32_16x16x32_bf16 v[84:87], a[240:243], a[188:191], v[84:87]\n\t"
"ds_read_b128 a[184:187], v244 offset:35840\n\t"
"v_mfma_f32_16x16x32_bf16 v[84:87], a[244:247], a[192:195], v[84:87]\n\t"
"ds_read_b128 a[188:191], v244 offset:36352\n\t"
"v_mfma_f32_16x16x32_bf16 v[84:87], a[248:251], a[196:199], v[84:87]\n\t"
"buffer_load_dwordx4 a[228:231], v252, s[8:11], s20 offen offset:1024 sc1\n\t"
"v_mfma_f32_16x16x32_bf16 v[88:91], a[240:243], a[192:195], v[88:91]\n\t"
"ds_read_b128 a[192:195], v244 offset:36864\n\t"
"v_mfma_f32_16x16x32_bf16 v[88:91], a[244:247], a[196:199], v[88:91]\n\t"
"v_mfma_f32_16x16x32_bf16 v[88:91], a[248:251], a[200:203], v[88:91]\n\t"
"v_mfma_f32_16x16x32_bf16 v[92:95], a[240:243], a[196:199], v[92:95]\n\t"
"buffer_load_dwordx4 a[232:235], v252, s[8:11], s20 offen offset:2048 sc1\n\t"
"v_mfma_f32_16x16x32_bf16 v[92:95], a[244:247], a[200:203], v[92:95]\n\t"
"s_waitcnt vmcnt(9) lgkmcnt(3)\n\t"
"ds_read_b128 a[196:199], v244 offset:37376\n\t"
"s_mov_b32 s20, 417792\n\t"
"v_mfma_f32_16x16x32_bf16 v[92:95], a[248:251], a[204:207], v[92:95]\n\t"
"ds_read_b128 a[200:203], v244 offset:37888\n\t"
"v_mfma_f32_16x16x32_bf16 v[224:227], v[224:227], v[220:223], 0\n\t"
"ds_read_b128 a[204:207], v244 offset:38400\n\t"
"v_mfma_f32_16x16x32_bf16 v[100:103], a[252:255], a[168:171], v[100:103]\n\t"
"buffer_load_dwordx4 a[236:239], v252, s[8:11], s20 offen offset:0 sc1\n\t"
"v_mfma_f32_16x16x32_bf16 v[100:103], v[240:243], a[172:175], v[100:103]\n\t"
"s_waitcnt lgkmcnt(4)\n\t"
"v_mfma_f32_16x16x32_bf16 v[100:103], a[208:211], a[176:179], v[100:103]\n\t"
"s_waitcnt lgkmcnt(3)\n\t"
"v_mfma_f32_16x16x32_bf16 v[104:107], a[252:255], a[172:175], v[104:107]\n\t"
"Lpg17_%=:\n\t"
"s_cbranch_vccnz Lvr2_%=\n\t"
"v_mfma_f32_16x16x32_bf16 v[104:107], v[240:243], a[176:179], v[104:107]\n\t"
"v_cvt_pk_bf16_f32 v224, v224, v225\n\t"
"v_mfma_f32_16x16x32_bf16 v[104:107], a[208:211], a[180:183], v[104:107]\n\t"
"s_waitcnt lgkmcnt(0)\n\t"
"s_barrier\n\t"
"v_mfma_f32_16x16x32_bf16 v[108:111], a[252:255], a[176:179], v[108:111]\n\t"
"s_add_u32 m0, s24, 32128\n\t"
"s_add_u32 s28, s21, 983040\n\t"
"v_mfma_f32_16x16x32_bf16 v[108:111], v[240:243], a[180:183], v[108:111]\n\t"
"buffer_load_dwordx4 a[240:243], v252, s[8:11], s20 offen offset:1024 sc1\n\t"
"v_mfma_f32_16x16x32_bf16 v[108:111], a[208:211], a[184:187], v[108:111]\n\t"
"ds_write2st64_b64 v245, v[232:233], v[234:235] offset0:48 offset1:49\n\t"
"v_cvt_pk_bf16_f32 v225, v226, v227\n\t"
"v_mfma_f32_16x16x32_bf16 v[112:115], a[252:255], a[180:183], v[112:115]\n\t"
"v_mfma_f32_16x16x32_bf16 v[112:115], v[240:243], a[184:187], v[112:115]\n\t"
"buffer_load_dwordx4 a[244:247], v252, s[8:11], s20 offen offset:2048 sc1\n\t"
"v_mfma_f32_16x16x32_bf16 v[112:115], a[208:211], a[188:191], v[112:115]\n\t"
"s_mov_b32 s20, 430080\n\t"
"ds_write2st64_b64 v245, v[228:229], v[230:231] offset0:50 offset1:51\n\t"
"v_mfma_f32_16x16x32_bf16 v[116:119], a[252:255], a[184:187], v[116:119]\n\t"
"buffer_load_dwordx4 v247, s[12:15], s28 offen offset:128 nt lds\n\t"
"v_mfma_f32_16x16x32_bf16 v[116:119], v[240:243], a[188:191], v[116:119]\n\t"
"s_add_u32 m0, s24, 39808\n\t"
"s_add_u32 s28, s21, 1179648\n\t"
"ds_read_b64_tr_b16 v[232:233], v246 offset:33280\n\t"
"v_mfma_f32_16x16x32_bf16 v[116:119], a[208:211], a[192:195], v[116:119]\n\t"
"ds_read_b64_tr_b16 v[234:235], v246 offset:36352\n\t"
"v_mfma_f32_16x16x32_bf16 v[120:123], a[252:255], a[188:191], v[120:123]\n\t"
"buffer_load_dwordx4 a[248:251], v252, s[8:11], s20 offen offset:0 sc1\n\t"
"v_mfma_f32_16x16x32_bf16 v[120:123], v[240:243], a[192:195], v[120:123]\n\t"
"buffer_load_dwordx4 v247, s[12:15], s28 offen offset:128 nt lds\n\t"
"v_mfma_f32_16x16x32_bf16 v[120:123], a[208:211], a[196:199], v[120:123]\n\t"
"s_waitcnt lgkmcnt(0)\n\t"
"ds_write_b64 v245, v[224:225] offset:26624\n\t"
"ds_read_b64_tr_b16 v[228:229], v246 offset:40960\n\t"
"ds_read_b64_tr_b16 v[230:231], v246 offset:44032\n\t"
"s_add_u32 m0, s24, 45952\n\t"
"s_add_u32 s28, s21, 1376256\n\t"
"v_mfma_f32_16x16x32_bf16 v[232:235], v[232:235], v[220:223], 0\n\t"
"buffer_load_dwordx4 v247, s[12:15], s28 offen offset:128 nt lds\n\t"
"s_add_u32 m0, s24, 52096\n\t"
"s_add_u32 s28, s21, 1572864\n\t"
"v_mfma_f32_16x16x32_bf16 v[124:127], a[252:255], a[192:195], v[124:127]\n\t"
"buffer_load_dwordx4 v247, s[12:15], s28 offen offset:128 nt lds\n\t"
"v_mfma_f32_16x16x32_bf16 v[124:127], v[240:243], a[196:199], v[124:127]\n\t"
"s_add_u32 m0, s24, 92032\n\t"
"s_add_u32 s28, s21, 2752512\n\t"
"ds_read_b64_tr_b16 v[224:225], v246 offset:47104\n\t"
"v_mfma_f32_16x16x32_bf16 v[124:127], a[208:211], a[200:203], v[124:127]\n\t"
"s_waitcnt lgkmcnt(1)\n\t"
"ds_read_b64_tr_b16 v[226:227], v246 offset:50176\n\t"
"v_mfma_f32_16x16x32_bf16 v[228:231], v[228:231], v[220:223], 0\n\t"
"buffer_load_dwordx4 v247, s[12:15], s28 offen offset:128 nt lds\n\t"
"s_add_u32 m0, s24, 99712\n\t"
"s_add_u32 s28, s21, 2949120\n\t"
"v_mfma_f32_16x16x32_bf16 v[128:131], a[252:255], a[196:199], v[128:131]\n\t"
"buffer_load_dwordx4 v247, s[12:15], s28 offen offset:128 nt lds\n\t"
"v_mfma_f32_16x16x32_bf16 v[128:131], v[240:243], a[200:203], v[128:131]\n\t"
"s_waitcnt vmcnt(16)\n\t"
"s_add_u32 m0, s24, 105856\n\t"
"s_add_u32 s28, s21, 3145728\n\t"
"v_mfma_f32_16x16x32_bf16 v[128:131], a[208:211], a[204:207], v[128:131]\n\t"
"s_waitcnt lgkmcnt(0)\n\t"
"buffer_load_dwordx4 v247, s[12:15], s28 offen offset:128 nt lds\n\t"
"s_add_u32 m0, s24, 112000\n\t"
"s_add_u32 s28, s21, 3342336\n\t"
"v_mfma_f32_16x16x32_bf16 v[224:227], v[224:227], v[220:223], 0\n\t"
"buffer_load_dwordx4 v247, s[12:15], s28 offen offset:128 nt lds\n\t"
"v_cvt_pk_bf16_f32 v232, v232, v233\n\t"
"v_cvt_pk_bf16_f32 v233, v234, v235\n\t"
"v_mfma_f32_16x16x32_bf16 v[132:135], v[236:239], a[168:171], v[132:135]\n\t"
"v_cvt_pk_bf16_f32 v234, v228, v229\n\t"
"v_cvt_pk_bf16_f32 v235, v230, v231\n\t"
"v_mfma_f32_16x16x32_bf16 v[132:135], v[96:99], a[172:175], v[132:135]\n\t"
"ds_read_b64_tr_b16 v[228:229], v246 offset:53248\n\t"
"v_mfma_f32_16x16x32_bf16 v[132:135], v[248:251], a[176:179], v[132:135]\n\t"
"ds_write2st64_b64 v245, v[232:233], v[234:235] offset0:53 offset1:54\n\t"
"v_mfma_f32_16x16x32_bf16 v[136:139], v[236:239], a[172:175], v[136:139]\n\t"
"v_mfma_f32_16x16x32_bf16 v[136:139], v[96:99], a[176:179], v[136:139]\n\t"
"ds_read_b64_tr_b16 v[230:231], v246 offset:56320\n\t"
"v_mfma_f32_16x16x32_bf16 v[136:139], v[248:251], a[180:183], v[136:139]\n\t"
"v_cvt_pk_bf16_f32 v224, v224, v225\n\t"
"v_cvt_pk_bf16_f32 v225, v226, v227\n\t"
"v_mfma_f32_16x16x32_bf16 v[140:143], v[236:239], a[176:179], v[140:143]\n\t"
"v_mfma_f32_16x16x32_bf16 v[140:143], v[96:99], a[180:183], v[140:143]\n\t"
"v_mfma_f32_16x16x32_bf16 v[140:143], v[248:251], a[184:187], v[140:143]\n\t"
"v_mfma_f32_16x16x32_bf16 v[144:147], v[236:239], a[180:183], v[144:147]\n\t"
"v_mfma_f32_16x16x32_bf16 v[144:147], v[96:99], a[184:187], v[144:147]\n\t"
"v_mfma_f32_16x16x32_bf16 v[144:147], v[248:251], a[188:191], v[144:147]\n\t"
"v_mfma_f32_16x16x32_bf16 v[148:151], v[236:239], a[184:187], v[148:151]\n\t"
"s_waitcnt lgkmcnt(0)\n\t"
"v_mfma_f32_16x16x32_bf16 v[148:151], v[96:99], a[188:191], v[148:151]\n\t"
"v_mfma_f32_16x16x32_bf16 v[148:151], v[248:251], a[192:195], v[148:151]\n\t"
"v_mfma_f32_16x16x32_bf16 v[228:231], v[228:231], v[220:223], 0\n\t"
"v_mfma_f32_16x16x32_bf16 v[152:155], v[236:239], a[188:191], v[152:155]\n\t"
"v_mfma_f32_16x16x32_bf16 v[152:155], v[96:99], a[192:195], v[152:155]\n\t"
"v_mfma_f32_16x16x32_bf16 v[152:155], v[248:251], a[196:199], v[152:155]\n\t"
"v_mfma_f32_16x16x32_bf16 v[156:159], v[236:239], a[192:195], v[156:159]\n\t"
"v_mfma_f32_16x16x32_bf16 v[156:159], v[96:99], a[196:199], v[156:159]\n\t"
"v_cvt_pk_bf16_f32 v226, v228, v229\n\t"
"v_cvt_pk_bf16_f32 v227, v230, v231\n\t"
"v_mfma_f32_16x16x32_bf16 v[156:159], v[248:251], a[200:203], v[156:159]\n\t"
"ds_write2st64_b64 v245, v[224:225], v[226:227] offset0:55 offset1:56\n\t"
"v_mfma_f32_16x16x32_bf16 v[160:163], v[236:239], a[196:199], v[160:163]\n\t"
"s_waitcnt vmcnt(15)\n\t"
"v_mfma_f32_16x16x32_bf16 v[160:163], v[96:99], a[200:203], v[160:163]\n\t"
"v_mfma_f32_16x16x32_bf16 v[160:163], v[248:251], a[204:207], v[160:163]\n\t"
"v_mfma_f32_16x16x32_bf16 v[164:167], a[212:215], a[168:171], v[164:167]\n\t"
"v_mfma_f32_16x16x32_bf16 v[164:167], a[216:219], a[172:175], v[164:167]\n\t"
"v_mfma_f32_16x16x32_bf16 v[164:167], a[220:223], a[176:179], v[164:167]\n\t"
"s_waitcnt lgkmcnt(0)\n\t"
"s_barrier\n\t"
"v_mfma_f32_16x16x32_bf16 v[168:171], a[212:215], a[172:175], v[168:171]\n\t"
"ds_read_b128 a[168:171], v244 offset:24576\n\t"
"v_mfma_f32_16x16x32_bf16 v[168:171], a[216:219], a[176:179], v[168:171]\n\t"
"ds_read_b128 a[172:175], v244 offset:25088\n\t"
"v_mfma_f32_16x16x32_bf16 v[168:171], a[220:223], a[180:183], v[168:171]\n\t"
"v_mfma_f32_16x16x32_bf16 v[172:175], a[212:215], a[176:179], v[172:175]\n\t"
"ds_read_b128 a[176:179], v244 offset:25600\n\t"
"v_mfma_f32_16x16x32_bf16 v[172:175], a[216:219], a[180:183], v[172:175]\n\t"
"v_mfma_f32_16x16x32_bf16 v[172:175], a[220:223], a[184:187], v[172:175]\n\t"
"v_mfma_f32_16x16x32_bf16 v[176:179], a[212:215], a[180:183], v[176:179]\n\t"
"ds_read_b128 a[180:183], v244 offset:26112\n\t"
"v_mfma_f32_16x16x32_bf16 v[176:179], a[216:219], a[184:187], v[176:179]\n\t"
"v_mfma_f32_16x16x32_bf16 v[176:179], a[220:223], a[188:191], v[176:179]\n\t"
"v_mfma_f32_16x16x32_bf16 v[180:183], a[212:215], a[184:187], v[180:183]\n\t"
"ds_read_b128 a[184:187], v244 offset:26624\n\t"
"v_mfma_f32_16x16x32_bf16 v[180:183], a[216:219], a[188:191], v[180:183]\n\t"
"v_mfma_f32_16x16x32_bf16 v[180:183], a[220:223], a[192:195], v[180:183]\n\t"
"v_mfma_f32_16x16x32_bf16 v[184:187], a[212:215], a[188:191], v[184:187]\n\t"
"ds_read_b128 a[188:191], v244 offset:27136\n\t"
"v_mfma_f32_16x16x32_bf16 v[184:187], a[216:219], a[192:195], v[184:187]\n\t"
"v_mfma_f32_16x16x32_bf16 v[184:187], a[220:223], a[196:199], v[184:187]\n\t"
"v_mfma_f32_16x16x32_bf16 v[188:191], a[212:215], a[192:195], v[188:191]\n\t"
"ds_read_b128 a[192:195], v244 offset:27648\n\t"
"v_mfma_f32_16x16x32_bf16 v[188:191], a[216:219], a[196:199], v[188:191]\n\t"
"v_mfma_f32_16x16x32_bf16 v[188:191], a[220:223], a[200:203], v[188:191]\n\t"
"v_mfma_f32_16x16x32_bf16 v[192:195], a[212:215], a[196:199], v[192:195]\n\t"
"s_waitcnt vmcnt(12) lgkmcnt(4)\n\t"
"ds_read_b128 a[196:199], v244 offset:28160\n\t"
"v_mfma_f32_16x16x32_bf16 v[192:195], a[216:219], a[200:203], v[192:195]\n\t"
"ds_read_b128 a[200:203], v244 offset:28672\n\t"
"v_mfma_f32_16x16x32_bf16 v[192:195], a[220:223], a[204:207], v[192:195]\n\t"
"ds_read_b128 a[204:207], v244 offset:29184\n\t"
"Lst23_%=:\n\t"
"v_mfma_f32_16x16x32_bf16 v[196:199], a[224:227], a[168:171], v[196:199]\n\t"
"s_mov_b64 vcc, -1\n\t"
"s_bitcmp1_b32 s73, 1\n\t"
"s_cbranch_scc1 Lvh3_%=\n\t"
"s_bitcmp1_b32 s73, 0\n\t"
"s_cbranch_scc1 Lpg22_%=\n\t"
"s_branch Lpg21_%=\n\t"
"Lvh3_%=:\n\t"
"s_bitcmp1_b32 s73, 0\n\t"
"s_cbranch_scc1 Lpg24_%=\n\t"
"s_branch Lpg23_%=\n\t"
"Lvr3_%=:\n\t"
"s_mov_b64 vcc, 0\n\t"
"s_waitcnt lgkmcnt(6)\n\t"
"v_mfma_f32_16x16x32_bf16 v[196:199], a[228:231], a[172:175], v[196:199]\n\t"
"buffer_load_dwordx4 v[240:243], v252, s[8:11], s20 offen offset:1024 sc1\n\t"
"v_mfma_f32_16x16x32_bf16 v[196:199], a[232:235], a[176:179], v[196:199]\n\t"
"v_mfma_f32_16x16x32_bf16 v[204:207], a[224:227], a[172:175], v[204:207]\n\t"
"s_waitcnt lgkmcnt(5)\n\t"
"v_mfma_f32_16x16x32_bf16 v[204:207], a[228:231], a[176:179], v[204:207]\n\t"
"v_mfma_f32_16x16x32_bf16 v[204:207], a[232:235], a[180:183], v[204:207]\n\t"
"v_mfma_f32_16x16x32_bf16 v[212:215], a[224:227], a[176:179], v[212:215]\n\t"
"s_waitcnt lgkmcnt(4)\n\t"
"v_mfma_f32_16x16x32_bf16 v[212:215], a[228:231], a[180:183], v[212:215]\n\t"
"v_mfma_f32_16x16x32_bf16 v[212:215], a[232:235], a[184:187], v[212:215]\n\t"
"v_mfma_f32_16x16x32_bf16 a[0:3], a[224:227], a[180:183], a[0:3]\n\t"
"s_waitcnt lgkmcnt(3)\n\t"
"v_mfma_f32_16x16x32_bf16 a[0:3], a[228:231], a[184:187], a[0:3]\n\t"
"v_mfma_f32_16x16x32_bf16 a[0:3], a[232:235], a[188:191], a[0:3]\n\t"
"buffer_load_dwordx4 a[208:211], v252, s[8:11], s20 offen offset:2048 sc1\n\t"
"v_mfma_f32_16x16x32_bf16 a[8:11], a[224:227], a[184:187], a[8:11]\n\t"
"s_waitcnt lgkmcnt(2)\n\t"
"s_mov_b32 s20, 442368\n\t"
"v_mfma_f32_16x16x32_bf16 a[8:11], a[228:231], a[188:191], a[8:11]\n\t"
"v_mfma_f32_16x16x32_bf16 a[8:11], a[232:235], a[192:195], a[8:11]\n\t"
"ds_read_b64_tr_b16 v[232:233], v246 offset:0\n\t"
"v_mfma_f32_16x16x32_bf16 a[16:19], a[224:227], a[188:191], a[16:19]\n\t"
"buffer_load_dwordx4 v[236:239], v252, s[8:11], s20 offen offset:0 sc1\n\t"
"v_mfma_f32_16x16x32_bf16 a[16:19], a[228:231], a[192:195], a[16:19]\n\t"
"s_waitcnt lgkmcnt(2)\n\t"
"ds_read_b64_tr_b16 v[234:235], v246 offset:3072\n\t"
"v_mfma_f32_16x16x32_bf16 a[16:19], a[232:235], a[196:199], a[16:19]\n\t"
"v_mfma_f32_16x16x32_bf16 a[24:27], a[224:227], a[192:195], a[24:27]\n\t"
"s_waitcnt lgkmcnt(2)\n\t"
"v_mfma_f32_16x16x32_bf16 a[24:27], a[228:231], a[196:199], a[24:27]\n\t"
"buffer_load_dwordx4 v[96:99], v252, s[8:11], s20 offen offset:1024 sc1\n\t"
"v_mfma_f32_16x16x32_bf16 a[24:27], a[232:235], a[200:203], a[24:27]\n\t"
"v_mfma_f32_16x16x32_bf16 a[32:35], a[224:227], a[196:199], a[32:35]\n\t"
"s_waitcnt vmcnt(13)\n\t"
"ds_read_b64_tr_b16 v[228:229], v246 offset:6144\n\t"
"v_mfma_f32_16x16x32_bf16 a[32:35], a[228:231], a[200:203], a[32:35]\n\t"
"ds_read_b64_tr_b16 v[230:231], v246 offset:9216\n\t"
"v_mfma_f32_16x16x32_bf16 a[32:35], a[232:235], a[204:207], a[32:35]\n\t"
"buffer_load_dwordx4 v[248:251], v252, s[8:11], s20 offen offset:2048 sc1\n\t"
"v_mfma_f32_16x16x32_bf16 a[40:43], a[236:239], a[168:171], a[40:43]\n\t"
"s_mov_b32 s20, 454656\n\t"
"v_mfma_f32_16x16x32_bf16 a[40:43], a[240:243], a[172:175], a[40:43]\n\t"
"v_mfma_f32_16x16x32_bf16 a[40:43], a[244:247], a[176:179], a[40:43]\n\t"
"v_mfma_f32_16x16x32_bf16 a[48:51], a[236:239], a[172:175], a[48:51]\n\t"
"buffer_load_dwordx4 a[212:215], v252, s[8:11], s20 offen offset:0 sc1\n\t"
"v_mfma_f32_16x16x32_bf16 a[48:51], a[240:243], a[176:179], a[48:51]\n\t"
"s_waitcnt lgkmcnt(2)\n\t"
"v_mfma_f32_16x16x32_bf16 a[48:51], a[244:247], a[180:183], a[48:51]\n\t"
"v_mfma_f32_16x16x32_bf16 v[232:235], v[232:235], v[220:223], 0\n\t"
"v_mfma_f32_16x16x32_bf16 a[56:59], a[236:239], a[176:179], a[56:59]\n\t"
"buffer_load_dwordx4 a[216:219], v252, s[8:11], s20 offen offset:1024 sc1\n\t"
"v_mfma_f32_16x16x32_bf16 a[56:59], a[240:243], a[180:183], a[56:59]\n\t"
"ds_read_b64_tr_b16 v[224:225], v246 offset:12288\n\t"
"v_mfma_f32_16x16x32_bf16 a[56:59], a[244:247], a[184:187], a[56:59]\n\t"
"ds_read_b64_tr_b16 v[226:227], v246 offset:15360\n\t"
"v_mfma_f32_16x16x32_bf16 a[64:67], a[236:239], a[180:183], a[64:67]\n\t"
"s_waitcnt lgkmcnt(2)\n\t"
"v_mfma_f32_16x16x32_bf16 a[64:67], a[240:243], a[184:187], a[64:67]\n\t"
"buffer_load_dwordx4 a[220:223], v252, s[8:11], s20 offen offset:2048 sc1\n\t"
"v_mfma_f32_16x16x32_bf16 a[64:67], a[244:247], a[188:191], a[64:67]\n\t"
"s_mov_b32 s20, 466944\n\t"
"v_cvt_pk_bf16_f32 v232, v232, v233\n\t"
"v_cvt_pk_bf16_f32 v233, v234, v235\n\t"
"v_mfma_f32_16x16x32_bf16 v[228:231], v[228:231], v[220:223], 0\n\t"
"v_mfma_f32_16x16x32_bf16 a[72:75], a[236:239], a[184:187], a[72:75]\n\t"
"v_mfma_f32_16x16x32_bf16 a[72:75], a[240:243], a[188:191], a[72:75]\n\t"
"buffer_load_dwordx4 a[224:227], v252, s[8:11], s20 offen offset:0 sc1\n\t"
"v_mfma_f32_16x16x32_bf16 a[72:75], a[244:247], a[192:195], a[72:75]\n\t"
"v_mfma_f32_16x16x32_bf16 a[80:83], a[236:239], a[188:191], a[80:83]\n\t"
"v_mfma_f32_16x16x32_bf16 a[80:83], a[240:243], a[192:195], a[80:83]\n\t"
"v_cvt_pk_bf16_f32 v234, v228, v229\n\t"
"v_cvt_pk_bf16_f32 v235, v230, v231\n\t"
"v_mfma_f32_16x16x32_bf16 a[80:83], a[244:247], a[196:199], a[80:83]\n\t"
"ds_read_b64_tr_b16 v[228:229], v246 offset:19968\n\t"
"v_mfma_f32_16x16x32_bf16 a[88:91], a[236:239], a[192:195], a[88:91]\n\t"
"buffer_load_dwordx4 a[228:231], v252, s[8:11], s20 offen offset:1024 sc1\n\t"
"v_mfma_f32_16x16x32_bf16 a[88:91], a[240:243], a[196:199], a[88:91]\n\t"
"s_waitcnt lgkmcnt(1)\n\t"
"ds_read_b64_tr_b16 v[230:231], v246 offset:23040\n\t"
"v_mfma_f32_16x16x32_bf16 a[88:91], a[244:247], a[200:203], a[88:91]\n\t"
"v_mfma_f32_16x16x32_bf16 v[224:227], v[224:227], v[220:223], 0\n\t"
"buffer_load_dwordx4 a[232:235], v252, s[8:11], s20 offen offset:2048 sc1\n\t"
"v_mfma_f32_16x16x32_bf16 a[96:99], a[236:239], a[196:199], a[96:99]\n\t"
"s_waitcnt vmcnt(9)\n\t"
"s_mov_b32 s20, 700416\n\t"
"v_mfma_f32_16x16x32_bf16 a[96:99], a[240:243], a[200:203], a[96:99]\n\t"
"v_mfma_f32_16x16x32_bf16 a[96:99], a[244:247], a[204:207], a[96:99]\n\t"
"v_mfma_f32_16x16x32_bf16 a[104:107], a[248:251], a[168:171], a[104:107]\n\t"
"buffer_load_dwordx4 a[236:239], v252, s[8:11], s20 offen offset:0 sc1\n\t"
"v_mfma_f32_16x16x32_bf16 a[104:107], v[240:243], a[172:175], a[104:107]\n\t"
"ds_read_b128 a[168:171], v244 offset:33792\n\t"
"v_cvt_pk_bf16_f32 v224, v224, v225\n\t"
"v_mfma_f32_16x16x32_bf16 a[104:107], a[208:211], a[176:179], a[104:107]\n\t"
"v_cvt_pk_bf16_f32 v225, v226, v227\n\t"
"v_mfma_f32_16x16x32_bf16 a[112:115], a[248:251], a[172:175], a[112:115]\n\t"
"buffer_load_dwordx4 a[240:243], v252, s[8:11], s20 offen offset:1024 sc1\n\t"
"v_mfma_f32_16x16x32_bf16 a[112:115], v[240:243], a[176:179], a[112:115]\n\t"
"s_waitcnt lgkmcnt(1)\n\t"
"ds_read_b128 a[172:175], v244 offset:34304\n\t"
"v_mfma_f32_16x16x32_bf16 a[112:115], a[208:211], a[180:183], a[112:115]\n\t"
"v_mfma_f32_16x16x32_bf16 v[228:231], v[228:231], v[220:223], 0\n\t"
"buffer_load_dwordx4 a[244:247], v252, s[8:11], s20 offen offset:2048 sc1\n\t"
"v_mfma_f32_16x16x32_bf16 a[120:123], a[248:251], a[176:179], a[120:123]\n\t"
"s_mov_b32 s20, 712704\n\t"
"ds_read_b128 a[176:179], v244 offset:34816\n\t"
"v_mfma_f32_16x16x32_bf16 a[120:123], v[240:243], a[180:183], a[120:123]\n\t"
"v_mfma_f32_16x16x32_bf16 a[120:123], a[208:211], a[184:187], a[120:123]\n\t"
"buffer_load_dwordx4 a[252:255], v252, s[8:11], s20 offen offset:0 sc1\n\t"
"v_mfma_f32_16x16x32_bf16 a[128:131], a[248:251], a[180:183], a[128:131]\n\t"
"ds_read_b128 a[180:183], v244 offset:35328\n\t"
"v_mfma_f32_16x16x32_bf16 a[128:131], v[240:243], a[184:187], a[128:131]\n\t"
"v_cvt_pk_bf16_f32 v226, v228, v229\n\t"
"v_cvt_pk_bf16_f32 v227, v230, v231\n\t"
"v_mfma_f32_16x16x32_bf16 a[128:131], a[208:211], a[188:191], a[128:131]\n\t"
"ds_read_b64_tr_b16 v[228:229], v246 offset:26112\n\t"
"v_mfma_f32_16x16x32_bf16 a[136:139], a[248:251], a[184:187], a[136:139]\n\t"
"ds_read_b64_tr_b16 v[230:231], v246 offset:29184\n\t"
"v_mfma_f32_16x16x32_bf16 a[136:139], v[240:243], a[188:191], a[136:139]\n\t"
"ds_read_b128 a[184:187], v244 offset:35840\n\t"
"v_mfma_f32_16x16x32_bf16 a[136:139], a[208:211], a[192:195], a[136:139]\n\t"
"v_mfma_f32_16x16x32_bf16 a[144:147], a[248:251], a[188:191], a[144:147]\n\t"
"ds_read_b128 a[188:191], v244 offset:36352\n\t"
"v_mfma_f32_16x16x32_bf16 a[144:147], v[240:243], a[192:195], a[144:147]\n\t"
"v_mfma_f32_16x16x32_bf16 a[144:147], a[208:211], a[196:199], a[144:147]\n\t"
"v_mfma_f32_16x16x32_bf16 a[152:155], a[248:251], a[192:195], a[152:155]\n\t"
"ds_read_b128 a[192:195], v244 offset:36864\n\t"
"v_mfma_f32_16x16x32_bf16 a[152:155], v[240:243], a[196:199], a[152:155]\n\t"
"v_mfma_f32_16x16x32_bf16 a[152:155], a[208:211], a[200:203], a[152:155]\n\t"
"v_mfma_f32_16x16x32_bf16 a[160:163], a[248:251], a[196:199], a[160:163]\n\t"
"buffer_load_dwordx4 a[248:251], v252, s[8:11], s20 offen offset:1024 sc1\n\t"
"v_mfma_f32_16x16x32_bf16 a[160:163], v[240:243], a[200:203], a[160:163]\n\t"
"s_waitcnt vmcnt(11) lgkmcnt(3)\n\t"
"ds_read_b128 a[196:199], v244 offset:37376\n\t"
"v_mfma_f32_16x16x32_bf16 a[160:163], a[208:211], a[204:207], a[160:163]\n\t"
"ds_read_b128 a[200:203], v244 offset:37888\n\t"
"v_mfma_f32_16x16x32_bf16 v[228:231], v[228:231], v[220:223], 0\n\t"
"ds_read_b128 a[204:207], v244 offset:38400\n\t"
"v_mfma_f32_16x16x32_bf16 v[200:203], v[236:239], a[168:171], v[200:203]\n\t"
"v_mfma_f32_16x16x32_bf16 v[200:203], v[96:99], a[172:175], v[200:203]\n\t"
"v_mfma_f32_16x16x32_bf16 v[200:203], v[248:251], a[176:179], v[200:203]\n\t"
"v_mfma_f32_16x16x32_bf16 v[208:211], v[236:239], a[172:175], v[208:211]\n\t"
"s_waitcnt lgkmcnt(5)\n\t"
"v_mfma_f32_16x16x32_bf16 v[208:211], v[96:99], a[176:179], v[208:211]\n\t"
"v_cvt_pk_bf16_f32 v228, v228, v229\n\t"
"v_mfma_f32_16x16x32_bf16 v[208:211], v[248:251], a[180:183], v[208:211]\n\t"
"s_waitcnt lgkmcnt(0)\n\t"
"s_barrier\n\t"
"v_mfma_f32_16x16x32_bf16 v[216:219], v[236:239], a[176:179], v[216:219]\n\t"
"v_cvt_pk_bf16_f32 v229, v230, v231\n\t"
"v_mfma_f32_16x16x32_bf16 v[216:219], v[96:99], a[180:183], v[216:219]\n\t"
"v_mfma_f32_16x16x32_bf16 v[216:219], v[248:251], a[184:187], v[216:219]\n\t"
"ds_write2st64_b64 v245, v[232:233], v[234:235] offset0:48 offset1:49\n\t"
"v_mfma_f32_16x16x32_bf16 a[4:7], v[236:239], a[180:183], a[4:7]\n\t"
"v_mfma_f32_16x16x32_bf16 a[4:7], v[96:99], a[184:187], a[4:7]\n\t"
"ds_read_b64_tr_b16 v[232:233], v246 offset:32256\n\t"
"v_mfma_f32_16x16x32_bf16 a[4:7], v[248:251], a[188:191], a[4:7]\n\t"
"ds_write2st64_b64 v245, v[224:225], v[226:227] offset0:50 offset1:51\n\t"
"v_mfma_f32_16x16x32_bf16 a[12:15], v[236:239], a[184:187], a[12:15]\n\t"
"v_mfma_f32_16x16x32_bf16 a[12:15], v[96:99], a[188:191], a[12:15]\n\t"
"ds_read_b64_tr_b16 v[234:235], v246 offset:35328\n\t"
"v_mfma_f32_16x16x32_bf16 a[12:15], v[248:251], a[192:195], a[12:15]\n\t"
"ds_write_b64 v245, v[228:229] offset:26624\n\t"
"v_mfma_f32_16x16x32_bf16 a[20:23], v[236:239], a[188:191], a[20:23]\n\t"
"ds_read_b64_tr_b16 v[224:225], v246 offset:39936\n\t"
"v_mfma_f32_16x16x32_bf16 a[20:23], v[96:99], a[192:195], a[20:23]\n\t"
"ds_read_b64_tr_b16 v[226:227], v246 offset:43008\n\t"
"v_mfma_f32_16x16x32_bf16 a[20:23], v[248:251], a[196:199], a[20:23]\n\t"
"s_waitcnt lgkmcnt(3)\n\t"
"ds_read_b64_tr_b16 v[228:229], v246 offset:46080\n\t"
"v_mfma_f32_16x16x32_bf16 v[232:235], v[232:235], v[220:223], 0\n\t"
"ds_read_b64_tr_b16 v[230:231], v246 offset:49152\n\t"
"v_mfma_f32_16x16x32_bf16 a[28:31], v[236:239], a[192:195], a[28:31]\n\t"
"global_load_dwordx4 v[240:243], v252, s[48:49] offset:2048\n\t"
"v_mfma_f32_16x16x32_bf16 a[28:31], v[96:99], a[196:199], a[28:31]\n\t"
"global_load_dwordx4 a[208:211], v252, s[46:47] offset:1024\n\t"
"v_mfma_f32_16x16x32_bf16 a[28:31], v[248:251], a[200:203], a[28:31]\n\t"
"s_waitcnt lgkmcnt(2)\n\t"
"v_mfma_f32_16x16x32_bf16 v[224:227], v[224:227], v[220:223], 0\n\t"
"v_mfma_f32_16x16x32_bf16 a[36:39], v[236:239], a[196:199], a[36:39]\n\t"
"s_waitcnt lgkmcnt(0)\n\t"
"v_cvt_pk_bf16_f32 v232, v232, v233\n\t"
"v_cvt_pk_bf16_f32 v233, v234, v235\n\t"
"v_mfma_f32_16x16x32_bf16 a[36:39], v[96:99], a[200:203], a[36:39]\n\t"
"s_waitcnt vmcnt(10)\n\t"
"v_mfma_f32_16x16x32_bf16 a[36:39], v[248:251], a[204:207], a[36:39]\n\t"
"buffer_load_dwordx4 v[96:99], v252, s[8:11], s20 offen offset:2048 sc1\n\t"
"v_mfma_f32_16x16x32_bf16 v[228:231], v[228:231], v[220:223], 0\n\t"
"s_mov_b32 s20, 724992\n\t"
"v_mfma_f32_16x16x32_bf16 a[44:47], a[212:215], a[168:171], a[44:47]\n\t"
"buffer_load_dwordx4 v[248:251], v252, s[8:11], s20 offen offset:0 sc1\n\t"
"v_mfma_f32_16x16x32_bf16 a[44:47], a[216:219], a[172:175], a[44:47]\n\t"
"v_cvt_pk_bf16_f32 v234, v224, v225\n\t"
"v_cvt_pk_bf16_f32 v235, v226, v227\n\t"
"v_mfma_f32_16x16x32_bf16 a[44:47], a[220:223], a[176:179], a[44:47]\n\t"
"ds_write2st64_b64 v245, v[232:233], v[234:235] offset0:53 offset1:54\n\t"
"v_mfma_f32_16x16x32_bf16 a[52:55], a[212:215], a[172:175], a[52:55]\n\t"
"v_mfma_f32_16x16x32_bf16 a[52:55], a[216:219], a[176:179], a[52:55]\n\t"
"ds_read_b64_tr_b16 v[224:225], v246 offset:52224\n\t"
"v_mfma_f32_16x16x32_bf16 a[52:55], a[220:223], a[180:183], a[52:55]\n\t"
"ds_read_b64_tr_b16 v[226:227], v246 offset:55296\n\t"
"v_cvt_pk_bf16_f32 v228, v228, v229\n\t"
"v_mfma_f32_16x16x32_bf16 a[60:63], a[212:215], a[176:179], a[60:63]\n\t"
"v_cvt_pk_bf16_f32 v229, v230, v231\n\t"
"v_mfma_f32_16x16x32_bf16 a[60:63], a[216:219], a[180:183], a[60:63]\n\t"
"v_mfma_f32_16x16x32_bf16 a[60:63], a[220:223], a[184:187], a[60:63]\n\t"
"v_mfma_f32_16x16x32_bf16 a[68:71], a[212:215], a[180:183], a[68:71]\n\t"
"v_mfma_f32_16x16x32_bf16 a[68:71], a[216:219], a[184:187], a[68:71]\n\t"
"v_mfma_f32_16x16x32_bf16 a[68:71], a[220:223], a[188:191], a[68:71]\n\t"
"v_mfma_f32_16x16x32_bf16 a[76:79], a[212:215], a[184:187], a[76:79]\n\t"
"s_waitcnt lgkmcnt(0)\n\t"
"v_mfma_f32_16x16x32_bf16 a[76:79], a[216:219], a[188:191], a[76:79]\n\t"
"v_mfma_f32_16x16x32_bf16 a[76:79], a[220:223], a[192:195], a[76:79]\n\t"
"v_mfma_f32_16x16x32_bf16 v[224:227], v[224:227], v[220:223], 0\n\t"
"v_mfma_f32_16x16x32_bf16 a[84:87], a[212:215], a[188:191], a[84:87]\n\t"
"v_mfma_f32_16x16x32_bf16 a[84:87], a[216:219], a[192:195], a[84:87]\n\t"
"v_mfma_f32_16x16x32_bf16 a[84:87], a[220:223], a[196:199], a[84:87]\n\t"
"v_mfma_f32_16x16x32_bf16 a[92:95], a[212:215], a[192:195], a[92:95]\n\t"
"v_mfma_f32_16x16x32_bf16 a[92:95], a[216:219], a[196:199], a[92:95]\n\t"
"v_cvt_pk_bf16_f32 v230, v224, v225\n\t"
"v_cvt_pk_bf16_f32 v231, v226, v227\n\t"
"v_mfma_f32_16x16x32_bf16 a[92:95], a[220:223], a[200:203], a[92:95]\n\t"
"ds_write2st64_b64 v245, v[228:229], v[230:231] offset0:55 offset1:56\n\t"
"v_mfma_f32_16x16x32_bf16 a[100:103], a[212:215], a[196:199], a[100:103]\n\t"
"buffer_load_dwordx4 a[212:215], v252, s[8:11], s20 offen offset:1024 sc1\n\t"
"v_mfma_f32_16x16x32_bf16 a[100:103], a[216:219], a[200:203], a[100:103]\n\t"
"s_waitcnt vmcnt(10)\n\t"
"v_mfma_f32_16x16x32_bf16 a[100:103], a[220:223], a[204:207], a[100:103]\n\t"
"v_mfma_f32_16x16x32_bf16 a[108:111], a[224:227], a[168:171], a[108:111]\n\t"
"buffer_load_dwordx4 a[216:219], v252, s[8:11], s20 offen offset:2048 sc1\n\t"
"v_mfma_f32_16x16x32_bf16 a[108:111], a[228:231], a[172:175], a[108:111]\n\t"
"s_mov_b32 s20, 737280\n\t"
"v_mfma_f32_16x16x32_bf16 a[108:111], a[232:235], a[176:179], a[108:111]\n\t"
"s_waitcnt lgkmcnt(0)\n\t"
"s_barrier\n\t"
"v_mfma_f32_16x16x32_bf16 a[116:119], a[224:227], a[172:175], a[116:119]\n\t"
"s_waitcnt vmcnt(5)\n\t"
"ds_read_b64_tr_b16 v[236:237], v240 offset:0\n\t"
"v_mfma_f32_16x16x32_bf16 a[116:119], a[228:231], a[176:179], a[116:119]\n\t"
"ds_read_b64_tr_b16 v[238:239], v240 offset:1536\n\t"
"v_mfma_f32_16x16x32_bf16 a[116:119], a[232:235], a[180:183], a[116:119]\n\t"
"ds_read_b64_tr_b16 v[232:233], v241 offset:0\n\t"
"v_mfma_f32_16x16x32_bf16 a[124:127], a[224:227], a[176:179], a[124:127]\n\t"
"ds_read_b64_tr_b16 v[234:235], v241 offset:1536\n\t"
"v_mfma_f32_16x16x32_bf16 a[124:127], a[228:231], a[180:183], a[124:127]\n\t"
"buffer_load_dwordx4 a[220:223], v252, s[8:11], s20 offen offset:0 sc1\n\t"
"v_mfma_f32_16x16x32_bf16 a[124:127], a[232:235], a[184:187], a[124:127]\n\t"
"ds_read_b64_tr_b16 v[224:225], v240 offset:19968\n\t"
"v_mfma_f32_16x16x32_bf16 a[132:135], a[224:227], a[180:183], a[132:135]\n\t"
"s_waitcnt vmcnt(5) lgkmcnt(3)\n\t"
"ds_read_b64_tr_b16 v[226:227], v240 offset:21504\n\t"
"v_mfma_f32_16x16x32_bf16 a[132:135], a[228:231], a[184:187], a[132:135]\n\t"
"ds_read_b64_tr_b16 v[228:229], v241 offset:19968\n\t"
"v_mfma_f32_16x16x32_bf16 a[132:135], a[232:235], a[188:191], a[132:135]\n\t"
"s_waitcnt lgkmcnt(3)\n\t"
"ds_read_b64_tr_b16 v[230:231], v241 offset:21504\n\t"
"v_mfma_f32_16x16x32_bf16 v[236:239], v[236:239], a[208:211], 0\n\t"
"ds_read_b128 a[168:171], v244 offset:24576\n\t"
"v_mfma_f32_16x16x32_bf16 v[232:235], v[232:235], a[208:211], 0\n\t"
"ds_read_b128 a[172:175], v244 offset:25088\n\t"
"v_mfma_f32_16x16x32_bf16 a[140:143], a[224:227], a[184:187], a[140:143]\n\t"
"ds_read_b128 a[176:179], v244 offset:25600\n\t"
"v_mfma_f32_16x16x32_bf16 a[140:143], a[228:231], a[188:191], a[140:143]\n\t"
"ds_read_b128 a[180:183], v244 offset:26112\n\t"
"v_mfma_f32_16x16x32_bf16 a[140:143], a[232:235], a[192:195], a[140:143]\n\t"
"ds_read_b128 a[184:187], v244 offset:26624\n\t"
"v_mfma_f32_16x16x32_bf16 a[148:151], a[224:227], a[188:191], a[148:151]\n\t"
"s_waitcnt lgkmcnt(7)\n\t"
"ds_read_b128 a[188:191], v244 offset:27136\n\t"
"v_cvt_pk_bf16_f32 v236, v236, v237\n\t"
"v_mfma_f32_16x16x32_bf16 a[148:151], a[228:231], a[192:195], a[148:151]\n\t"
"s_waitcnt lgkmcnt(6)\n\t"
"v_cvt_pk_bf16_f32 v237, v238, v239\n\t"
"v_cvt_pk_bf16_f32 v238, v232, v233\n\t"
"v_mfma_f32_16x16x32_bf16 a[148:151], a[232:235], a[196:199], a[148:151]\n\t"
"v_cvt_pk_bf16_f32 v239, v234, v235\n\t"
"v_mfma_f32_16x16x32_bf16 v[224:227], v[224:227], a[208:211], 0\n\t"
"ds_write_b128 v242, v[236:239] offset:0\n\t"
"v_mfma_f32_16x16x32_bf16 v[228:231], v[228:231], a[208:211], 0\n\t"
"v_mfma_f32_16x16x32_bf16 a[156:159], a[224:227], a[192:195], a[156:159]\n\t"
"ds_read_b128 a[192:195], v244 offset:27648\n\t"
"v_mfma_f32_16x16x32_bf16 a[156:159], a[228:231], a[196:199], a[156:159]\n\t"
"ds_read_b64_tr_b16 v[232:233], v240 offset:39936\n\t"
"v_mfma_f32_16x16x32_bf16 a[156:159], a[232:235], a[200:203], a[156:159]\n\t"
"ds_read_b64_tr_b16 v[234:235], v240 offset:41472\n\t"
"v_mfma_f32_16x16x32_bf16 a[164:167], a[224:227], a[196:199], a[164:167]\n\t"
"buffer_load_dwordx4 a[224:227], v252, s[8:11], s20 offen offset:1024 sc1\n\t"
"v_mfma_f32_16x16x32_bf16 a[164:167], a[228:231], a[200:203], a[164:167]\n\t"
"s_waitcnt lgkmcnt(7)\n\t"
"ds_read_b64_tr_b16 v[236:237], v241 offset:39936\n\t"
"v_cvt_pk_bf16_f32 v224, v224, v225\n\t"
"v_mfma_f32_16x16x32_bf16 a[164:167], a[232:235], a[204:207], a[164:167]\n\t"
"ds_read_b64_tr_b16 v[238:239], v241 offset:41472\n\t"
"v_cvt_pk_bf16_f32 v225, v226, v227\n\t"
"Lst24_%=:\n\t"
"v_mfma_f32_16x16x32_bf16 v[196:199], a[236:239], a[168:171], v[196:199]\n\t"
"s_waitcnt lgkmcnt(8)\n\t"
"ds_read_b128 a[196:199], v244 offset:28160\n\t"
"v_cvt_pk_bf16_f32 v226, v228, v229\n\t"
"v_mfma_f32_16x16x32_bf16 v[196:199], a[240:243], a[172:175], v[196:199]\n\t"
"ds_read_b128 a[200:203], v244 offset:28672\n\t"
"v_cvt_pk_bf16_f32 v227, v230, v231\n\t"
"v_mfma_f32_16x16x32_bf16 v[196:199], a[244:247], a[176:179], v[196:199]\n\t"
"ds_write_b128 v242, v[224:227] offset:1536\n\t"
"v_mfma_f32_16x16x32_bf16 v[204:207], a[236:239], a[172:175], v[204:207]\n\t"
"buffer_load_dwordx4 a[228:231], v252, s[8:11], s20 offen offset:2048 sc1\n\t"
"v_mfma_f32_16x16x32_bf16 v[204:207], a[240:243], a[176:179], v[204:207]\n\t"
"s_waitcnt lgkmcnt(10)\n\t"
"ds_read_b128 a[204:207], v244 offset:29184\n\t"
"v_mfma_f32_16x16x32_bf16 v[204:207], a[244:247], a[180:183], v[204:207]\n\t"
"s_mov_b32 s20, 749568\n\t"
"v_add_u32 v240, 59904, v240\n\t"
"v_add_u32 v241, 59904, v241\n\t"
"v_mfma_f32_16x16x32_bf16 v[212:215], a[236:239], a[176:179], v[212:215]\n\t"
"s_waitcnt lgkmcnt(6)\n\t"
"ds_read_b64_tr_b16 v[228:229], v240 offset:0\n\t"
"v_mfma_f32_16x16x32_bf16 v[212:215], a[240:243], a[180:183], v[212:215]\n\t"
"buffer_load_dwordx4 a[232:235], v252, s[8:11], s20 offen offset:0 sc1\n\t"
"v_mfma_f32_16x16x32_bf16 v[212:215], a[244:247], a[184:187], v[212:215]\n\t"
"s_waitcnt lgkmcnt(5)\n\t"
"ds_read_b64_tr_b16 v[230:231], v240 offset:1536\n\t"
"v_mfma_f32_16x16x32_bf16 v[232:235], v[232:235], a[208:211], 0\n\t"
"ds_read_b64_tr_b16 v[224:225], v241 offset:0\n\t"
"v_mfma_f32_16x16x32_bf16 v[236:239], v[236:239], a[208:211], 0\n\t"
"ds_read_b64_tr_b16 v[226:227], v241 offset:1536\n\t"
"v_mfma_f32_16x16x32_bf16 a[0:3], a[236:239], a[180:183], a[0:3]\n\t"
"v_mfma_f32_16x16x32_bf16 a[0:3], a[240:243], a[184:187], a[0:3]\n\t"
"v_mfma_f32_16x16x32_bf16 a[0:3], a[244:247], a[188:191], a[0:3]\n\t"
"v_mfma_f32_16x16x32_bf16 a[8:11], a[236:239], a[184:187], a[8:11]\n\t"
"s_waitcnt lgkmcnt(2)\n\t"
"v_cvt_pk_bf16_f32 v232, v232, v233\n\t"
"v_cvt_pk_bf16_f32 v233, v234, v235\n\t"
"v_mfma_f32_16x16x32_bf16 a[8:11], a[240:243], a[188:191], a[8:11]\n\t"
"s_waitcnt lgkmcnt(0)\n\t"
"v_cvt_pk_bf16_f32 v234, v236, v237\n\t"
"v_cvt_pk_bf16_f32 v235, v238, v239\n\t"
"v_mfma_f32_16x16x32_bf16 a[8:11], a[244:247], a[192:195], a[8:11]\n\t"
"ds_write_b128 v242, v[232:235] offset:3072\n\t"
"v_mfma_f32_16x16x32_bf16 v[228:231], v[228:231], a[208:211], 0\n\t"
"v_mfma_f32_16x16x32_bf16 v[224:227], v[224:227], a[208:211], 0\n\t"
"ds_read_b64_tr_b16 v[236:237], v240 offset:19968\n\t"
"v_mfma_f32_16x16x32_bf16 a[16:19], a[236:239], a[188:191], a[16:19]\n\t"
"ds_read_b64_tr_b16 v[238:239], v240 offset:21504\n\t"
"v_mfma_f32_16x16x32_bf16 a[16:19], a[240:243], a[192:195], a[16:19]\n\t"
"ds_read_b64_tr_b16 v[232:233], v241 offset:19968\n\t"
"v_mfma_f32_16x16x32_bf16 a[16:19], a[244:247], a[196:199], a[16:19]\n\t"
"ds_read_b64_tr_b16 v[234:235], v241 offset:21504\n\t"
"v_mfma_f32_16x16x32_bf16 a[24:27], a[236:239], a[192:195], a[24:27]\n\t"
"v_cvt_pk_bf16_f32 v228, v228, v229\n\t"
"v_cvt_pk_bf16_f32 v229, v230, v231\n\t"
"v_mfma_f32_16x16x32_bf16 a[24:27], a[240:243], a[196:199], a[24:27]\n\t"
"v_cvt_pk_bf16_f32 v230, v224, v225\n\t"
"v_cvt_pk_bf16_f32 v231, v226, v227\n\t"
"v_mfma_f32_16x16x32_bf16 a[24:27], a[244:247], a[200:203], a[24:27]\n\t"
"ds_write_b128 v242, v[228:231] offset:4608\n\t"
"v_mfma_f32_16x16x32_bf16 a[32:35], a[236:239], a[196:199], a[32:35]\n\t"
"buffer_load_dwordx4 a[236:239], v252, s[8:11], s20 offen offset:1024 sc1\n\t"
"v_mfma_f32_16x16x32_bf16 a[32:35], a[240:243], a[200:203], a[32:35]\n\t"
"s_waitcnt vmcnt(8)\n\t"
"ds_read_b64_tr_b16 v[224:225], v240 offset:39936\n\t"
"v_mfma_f32_16x16x32_bf16 a[32:35], a[244:247], a[204:207], a[32:35]\n\t"
"ds_read_b64_tr_b16 v[226:227], v240 offset:41472\n\t"
"v_mfma_f32_16x16x32_bf16 a[40:43], a[252:255], a[168:171], a[40:43]\n\t"
"buffer_load_dwordx4 a[240:243], v252, s[8:11], s20 offen offset:2048 sc1\n\t"
"v_mfma_f32_16x16x32_bf16 a[40:43], a[248:251], a[172:175], a[40:43]\n\t"
"s_mov_b32 s20, 761856\n\t"
"ds_read_b64_tr_b16 v[228:229], v241 offset:39936\n\t"
"v_mfma_f32_16x16x32_bf16 a[40:43], v[96:99], a[176:179], a[40:43]\n\t"
"ds_read_b64_tr_b16 v[230:231], v241 offset:41472\n\t"
"v_mfma_f32_16x16x32_bf16 a[48:51], a[252:255], a[172:175], a[48:51]\n\t"
"buffer_load_dwordx4 a[244:247], v252, s[8:11], s20 offen offset:0 sc1\n\t"
"v_mfma_f32_16x16x32_bf16 a[48:51], a[248:251], a[176:179], a[48:51]\n\t"
"s_waitcnt lgkmcnt(5)\n\t"
"v_mfma_f32_16x16x32_bf16 a[48:51], v[96:99], a[180:183], a[48:51]\n\t"
"v_mfma_f32_16x16x32_bf16 v[236:239], v[236:239], a[208:211], 0\n\t"
"v_mfma_f32_16x16x32_bf16 v[232:235], v[232:235], a[208:211], 0\n\t"
"v_xor_b32 v246, 0xc0, v246\n\t"
"v_mfma_f32_16x16x32_bf16 a[56:59], a[252:255], a[176:179], a[56:59]\n\t"
"v_mfma_f32_16x16x32_bf16 a[56:59], a[248:251], a[180:183], a[56:59]\n\t"
"v_mfma_f32_16x16x32_bf16 a[56:59], v[96:99], a[184:187], a[56:59]\n\t"
"v_mfma_f32_16x16x32_bf16 a[64:67], a[252:255], a[180:183], a[64:67]\n\t"
"s_waitcnt lgkmcnt(0)\n\t"
"v_cvt_pk_bf16_f32 v236, v236, v237\n\t"
"v_cvt_pk_bf16_f32 v237, v238, v239\n\t"
"v_mfma_f32_16x16x32_bf16 a[64:67], a[248:251], a[184:187], a[64:67]\n\t"
"v_cvt_pk_bf16_f32 v238, v232, v233\n\t"
"v_cvt_pk_bf16_f32 v239, v234, v235\n\t"
"v_mfma_f32_16x16x32_bf16 a[64:67], v[96:99], a[188:191], a[64:67]\n\t"
"Lpg18_%=:\n\t"
"s_cbranch_vccnz Lvr2_%=\n\t"
"ds_write_b128 v242, v[236:239] offset:6144\n\t"
"v_mfma_f32_16x16x32_bf16 v[224:227], v[224:227], a[208:211], 0\n\t"
"v_mfma_f32_16x16x32_bf16 v[228:231], v[228:231], a[208:211], 0\n\t"
"v_mfma_f32_16x16x32_bf16 a[72:75], a[252:255], a[184:187], a[72:75]\n\t"
"buffer_load_dwordx4 a[208:211], v252, s[8:11], s20 offen offset:1024 sc1\n\t"
"v_mfma_f32_16x16x32_bf16 a[72:75], a[248:251], a[188:191], a[72:75]\n\t"
"v_mfma_f32_16x16x32_bf16 a[72:75], v[96:99], a[192:195], a[72:75]\n\t"
"v_mfma_f32_16x16x32_bf16 a[80:83], a[252:255], a[188:191], a[80:83]\n\t"
"v_cvt_pk_bf16_f32 v224, v224, v225\n\t"
"v_cvt_pk_bf16_f32 v225, v226, v227\n\t"
"v_mfma_f32_16x16x32_bf16 a[80:83], a[248:251], a[192:195], a[80:83]\n\t"
"v_cvt_pk_bf16_f32 v226, v228, v229\n\t"
"v_cvt_pk_bf16_f32 v227, v230, v231\n\t"
"v_mfma_f32_16x16x32_bf16 a[80:83], v[96:99], a[196:199], a[80:83]\n\t"
"ds_write_b128 v242, v[224:227] offset:7680\n\t"
"v_mfma_f32_16x16x32_bf16 a[88:91], a[252:255], a[192:195], a[88:91]\n\t"
"v_mfma_f32_16x16x32_bf16 a[88:91], a[248:251], a[196:199], a[88:91]\n\t"
"ds_read_b64_tr_b16 v[232:233], v246 offset:0\n\t"
"v_mfma_f32_16x16x32_bf16 a[88:91], v[96:99], a[200:203], a[88:91]\n\t"
"ds_read_b64_tr_b16 v[234:235], v246 offset:3072\n\t"
"v_mfma_f32_16x16x32_bf16 a[96:99], a[252:255], a[196:199], a[96:99]\n\t"
"buffer_load_dwordx4 a[252:255], v252, s[8:11], s20 offen offset:2048 sc1\n\t"
"v_mfma_f32_16x16x32_bf16 a[96:99], a[248:251], a[200:203], a[96:99]\n\t"
"s_waitcnt vmcnt(9)\n\t"
"ds_read_b64_tr_b16 v[236:237], v246 offset:6144\n\t"
"s_mov_b32 s20, 626688\n\t"
"v_mfma_f32_16x16x32_bf16 a[96:99], v[96:99], a[204:207], a[96:99]\n\t"
"ds_read_b64_tr_b16 v[238:239], v246 offset:9216\n\t"
"v_mfma_f32_16x16x32_bf16 a[104:107], v[248:251], a[168:171], a[104:107]\n\t"
"buffer_load_dwordx4 v[240:243], v252, s[8:11], s20 offen offset:0 sc1\n\t"
"v_mfma_f32_16x16x32_bf16 a[104:107], a[212:215], a[172:175], a[104:107]\n\t"
"ds_read_b64_tr_b16 v[228:229], v246 offset:12288\n\t"
"v_mfma_f32_16x16x32_bf16 a[104:107], a[216:219], a[176:179], a[104:107]\n\t"
"ds_read_b64_tr_b16 v[230:231], v246 offset:15360\n\t"
"v_mfma_f32_16x16x32_bf16 a[112:115], v[248:251], a[172:175], a[112:115]\n\t"
"buffer_load_dwordx4 v[96:99], v252, s[8:11], s20 offen offset:1024 sc1\n\t"
"v_mfma_f32_16x16x32_bf16 a[112:115], a[212:215], a[176:179], a[112:115]\n\t"
"ds_read_b128 a[168:171], v244 offset:33792\n\t"
"v_mfma_f32_16x16x32_bf16 a[112:115], a[216:219], a[180:183], a[112:115]\n\t"
"ds_read_b64_tr_b16 v[224:225], v246 offset:19968\n\t"
"v_mfma_f32_16x16x32_bf16 a[120:123], v[248:251], a[176:179], a[120:123]\n\t"
"buffer_load_dwordx4 a[248:251], v252, s[8:11], s20 offen offset:2048 sc1\n\t"
"v_mfma_f32_16x16x32_bf16 a[120:123], a[212:215], a[180:183], a[120:123]\n\t"
"s_waitcnt lgkmcnt(6)\n\t"
"ds_read_b64_tr_b16 v[226:227], v246 offset:23040\n\t"
"v_mfma_f32_16x16x32_bf16 a[120:123], a[216:219], a[184:187], a[120:123]\n\t"
"ds_read_b128 a[172:175], v244 offset:34304\n\t"
"v_mfma_f32_16x16x32_bf16 v[232:235], v[232:235], v[220:223], 0\n\t"
"ds_read_b128 a[176:179], v244 offset:34816\n\t"
"v_mfma_f32_16x16x32_bf16 a[128:131], v[248:251], a[180:183], a[128:131]\n\t"
"s_waitcnt lgkmcnt(7)\n\t"
"ds_read_b128 a[180:183], v244 offset:35328\n\t"
"v_mfma_f32_16x16x32_bf16 a[128:131], a[212:215], a[184:187], a[128:131]\n\t"
"v_mfma_f32_16x16x32_bf16 a[128:131], a[216:219], a[188:191], a[128:131]\n\t"
"v_mfma_f32_16x16x32_bf16 v[236:239], v[236:239], v[220:223], 0\n\t"
"v_mfma_f32_16x16x32_bf16 a[136:139], v[248:251], a[184:187], a[136:139]\n\t"
"s_waitcnt lgkmcnt(6)\n\t"
"ds_read_b128 a[184:187], v244 offset:35840\n\t"
"v_cvt_pk_bf16_f32 v232, v232, v233\n\t"
"v_mfma_f32_16x16x32_bf16 a[136:139], a[212:215], a[188:191], a[136:139]\n\t"
"v_cvt_pk_bf16_f32 v233, v234, v235\n\t"
"v_mfma_f32_16x16x32_bf16 a[136:139], a[216:219], a[192:195], a[136:139]\n\t"
"s_mov_b32 s20, 638976\n\t"
"v_mfma_f32_16x16x32_bf16 v[228:231], v[228:231], v[220:223], 0\n\t"
"v_mfma_f32_16x16x32_bf16 a[144:147], v[248:251], a[188:191], a[144:147]\n\t"
"s_waitcnt lgkmcnt(4)\n\t"
"ds_read_b128 a[188:191], v244 offset:36352\n\t"
"v_cvt_pk_bf16_f32 v234, v236, v237\n\t"
"v_mfma_f32_16x16x32_bf16 a[144:147], a[212:215], a[192:195], a[144:147]\n\t"
"v_cvt_pk_bf16_f32 v235, v238, v239\n\t"
"v_mfma_f32_16x16x32_bf16 a[144:147], a[216:219], a[196:199], a[144:147]\n\t"
"v_mfma_f32_16x16x32_bf16 v[224:227], v[224:227], v[220:223], 0\n\t"
"v_mfma_f32_16x16x32_bf16 a[152:155], v[248:251], a[192:195], a[152:155]\n\t"
"buffer_load_dwordx4 v[236:239], v252, s[8:11], s20 offen offset:0 sc1\n\t"
"v_mfma_f32_16x16x32_bf16 a[152:155], a[212:215], a[196:199], a[152:155]\n\t"
"ds_read_b128 a[192:195], v244 offset:36864\n\t"
"v_cvt_pk_bf16_f32 v228, v228, v229\n\t"
"v_mfma_f32_16x16x32_bf16 a[152:155], a[216:219], a[200:203], a[152:155]\n\t"
"v_cvt_pk_bf16_f32 v229, v230, v231\n\t"
"v_mfma_f32_16x16x32_bf16 a[160:163], v[248:251], a[196:199], a[160:163]\n\t"
"buffer_load_dwordx4 v[248:251], v252, s[8:11], s20 offen offset:1024 sc1\n\t"
"v_mfma_f32_16x16x32_bf16 a[160:163], a[212:215], a[200:203], a[160:163]\n\t"
"s_waitcnt vmcnt(11) lgkmcnt(4)\n\t"
"ds_read_b128 a[196:199], v244 offset:37376\n\t"
"v_cvt_pk_bf16_f32 v230, v224, v225\n\t"
"v_mfma_f32_16x16x32_bf16 a[160:163], a[216:219], a[204:207], a[160:163]\n\t"
"ds_read_b128 a[200:203], v244 offset:37888\n\t"
"v_cvt_pk_bf16_f32 v231, v226, v227\n\t"
"v_mfma_f32_16x16x32_bf16 v[200:203], a[220:223], a[168:171], v[200:203]\n\t"
"ds_read_b64_tr_b16 v[224:225], v246 offset:26112\n\t"
"v_mfma_f32_16x16x32_bf16 v[200:203], a[224:227], a[172:175], v[200:203]\n\t"
"s_waitcnt lgkmcnt(6)\n\t"
"ds_read_b64_tr_b16 v[226:227], v246 offset:29184\n\t"
"v_mfma_f32_16x16x32_bf16 v[200:203], a[228:231], a[176:179], v[200:203]\n\t"
"s_waitcnt lgkmcnt(0)\n\t"
"ds_read_b128 a[204:207], v244 offset:38400\n\t"
"v_mfma_f32_16x16x32_bf16 v[224:227], v[224:227], v[220:223], 0\n\t"
"buffer_load_dwordx4 a[212:215], v252, s[8:11], s20 offen offset:2048 sc1\n\t"
"s_mov_b32 s20, 651264\n\t"
"v_mfma_f32_16x16x32_bf16 v[208:211], a[220:223], a[172:175], v[208:211]\n\t"
"buffer_load_dwordx4 a[216:219], v252, s[8:11], s20 offen offset:0 sc1\n\t"
"v_mfma_f32_16x16x32_bf16 v[208:211], a[224:227], a[176:179], v[208:211]\n\t"
"s_add_u32 m0, s24, 768\n\t"
"s_add_u32 s28, s21, 0\n\t"
"v_mfma_f32_16x16x32_bf16 v[208:211], a[228:231], a[180:183], v[208:211]\n\t"
"s_waitcnt lgkmcnt(0)\n\t"
"s_barrier\n\t"
"v_mfma_f32_16x16x32_bf16 v[216:219], a[220:223], a[176:179], v[216:219]\n\t"
"v_mfma_f32_16x16x32_bf16 v[216:219], a[224:227], a[180:183], v[216:219]\n\t"
"buffer_load_dwordx4 v247, s[12:15], s28 offen offset:256 nt lds\n\t"
"v_mfma_f32_16x16x32_bf16 v[216:219], a[228:231], a[184:187], v[216:219]\n\t"
"s_add_u32 m0, s24, 6912\n\t"
"s_add_u32 s28, s21, 196608\n\t"
"ds_write2st64_b64 v245, v[232:233], v[234:235] offset0:48 offset1:49\n\t"
"v_mfma_f32_16x16x32_bf16 a[4:7], a[220:223], a[180:183], a[4:7]\n\t"
"buffer_load_dwordx4 v247, s[12:15], s28 offen offset:256 nt lds\n\t"
"v_mfma_f32_16x16x32_bf16 a[4:7], a[224:227], a[184:187], a[4:7]\n\t"
"s_add_u32 m0, s24, 13056\n\t"
"s_add_u32 s28, s21, 393216\n\t"
"ds_read_b64_tr_b16 v[232:233], v246 offset:32256\n\t"
"v_mfma_f32_16x16x32_bf16 a[4:7], a[228:231], a[188:191], a[4:7]\n\t"
"ds_write2st64_b64 v245, v[228:229], v[230:231] offset0:50 offset1:51\n\t"
"v_cvt_pk_bf16_f32 v224, v224, v225\n\t"
"v_cvt_pk_bf16_f32 v225, v226, v227\n\t"
"v_mfma_f32_16x16x32_bf16 a[12:15], a[220:223], a[184:187], a[12:15]\n\t"
"v_mfma_f32_16x16x32_bf16 a[12:15], a[224:227], a[188:191], a[12:15]\n\t"
"ds_read_b64_tr_b16 v[234:235], v246 offset:35328\n\t"
"v_mfma_f32_16x16x32_bf16 a[12:15], a[228:231], a[192:195], a[12:15]\n\t"
"buffer_load_dwordx4 v247, s[12:15], s28 offen offset:256 nt lds\n\t"
"s_add_u32 m0, s24, 20736\n\t"
"s_add_u32 s28, s21, 589824\n\t"
"v_mfma_f32_16x16x32_bf16 a[20:23], a[220:223], a[188:191], a[20:23]\n\t"
"buffer_load_dwordx4 v247, s[12:15], s28 offen offset:256 nt lds\n\t"
"v_mfma_f32_16x16x32_bf16 a[20:23], a[224:227], a[192:195], a[20:23]\n\t"
"s_add_u32 m0, s24, 60672\n\t"
"s_add_u32 s28, s21, 1769472\n\t"
"ds_read_b64_tr_b16 v[228:229], v246 offset:39936\n\t"
"v_mfma_f32_16x16x32_bf16 a[20:23], a[228:231], a[196:199], a[20:23]\n\t"
"s_waitcnt lgkmcnt(1)\n\t"
"ds_write_b64 v245, v[224:225] offset:26624\n\t"
"ds_read_b64_tr_b16 v[230:231], v246 offset:43008\n\t"
"v_mfma_f32_16x16x32_bf16 v[232:235], v[232:235], v[220:223], 0\n\t"
"buffer_load_dwordx4 v247, s[12:15], s28 offen offset:256 nt lds\n\t"
"s_add_u32 m0, s24, 66816\n\t"
"s_add_u32 s28, s21, 1966080\n\t"
"v_mfma_f32_16x16x32_bf16 a[28:31], a[220:223], a[192:195], a[28:31]\n\t"
"buffer_load_dwordx4 v247, s[12:15], s28 offen offset:256 nt lds\n\t"
"v_mfma_f32_16x16x32_bf16 a[28:31], a[224:227], a[196:199], a[28:31]\n\t"
"ds_read_b64_tr_b16 v[224:225], v246 offset:46080\n\t"
"v_mfma_f32_16x16x32_bf16 a[28:31], a[228:231], a[200:203], a[28:31]\n\t"
"s_waitcnt lgkmcnt(1)\n\t"
"ds_read_b64_tr_b16 v[226:227], v246 offset:49152\n\t"
"v_mfma_f32_16x16x32_bf16 v[228:231], v[228:231], v[220:223], 0\n\t"
"v_mfma_f32_16x16x32_bf16 a[36:39], a[220:223], a[196:199], a[36:39]\n\t"
"v_cvt_pk_bf16_f32 v232, v232, v233\n\t"
"v_cvt_pk_bf16_f32 v233, v234, v235\n\t"
"v_mfma_f32_16x16x32_bf16 a[36:39], a[224:227], a[200:203], a[36:39]\n\t"
"s_waitcnt vmcnt(16)\n\t"
"v_mfma_f32_16x16x32_bf16 a[36:39], a[228:231], a[204:207], a[36:39]\n\t"
"s_waitcnt lgkmcnt(0)\n\t"
"v_mfma_f32_16x16x32_bf16 v[224:227], v[224:227], v[220:223], 0\n\t"
"v_mfma_f32_16x16x32_bf16 a[44:47], a[232:235], a[168:171], a[44:47]\n\t"
"v_cvt_pk_bf16_f32 v234, v228, v229\n\t"
"v_cvt_pk_bf16_f32 v235, v230, v231\n\t"
"v_mfma_f32_16x16x32_bf16 a[44:47], a[236:239], a[172:175], a[44:47]\n\t"
"ds_read_b64_tr_b16 v[228:229], v246 offset:52224\n\t"
"v_mfma_f32_16x16x32_bf16 a[44:47], a[240:243], a[176:179], a[44:47]\n\t"
"ds_write2st64_b64 v245, v[232:233], v[234:235] offset0:53 offset1:54\n\t"
"v_mfma_f32_16x16x32_bf16 a[52:55], a[232:235], a[172:175], a[52:55]\n\t"
"v_mfma_f32_16x16x32_bf16 a[52:55], a[236:239], a[176:179], a[52:55]\n\t"
"ds_read_b64_tr_b16 v[230:231], v246 offset:55296\n\t"
"v_mfma_f32_16x16x32_bf16 a[52:55], a[240:243], a[180:183], a[52:55]\n\t"
"v_cvt_pk_bf16_f32 v224, v224, v225\n\t"
"v_cvt_pk_bf16_f32 v225, v226, v227\n\t"
"v_mfma_f32_16x16x32_bf16 a[60:63], a[232:235], a[176:179], a[60:63]\n\t"
"v_mfma_f32_16x16x32_bf16 a[60:63], a[236:239], a[180:183], a[60:63]\n\t"
"v_mfma_f32_16x16x32_bf16 a[60:63], a[240:243], a[184:187], a[60:63]\n\t"
"v_mfma_f32_16x16x32_bf16 a[68:71], a[232:235], a[180:183], a[68:71]\n\t"
"v_mfma_f32_16x16x32_bf16 a[68:71], a[236:239], a[184:187], a[68:71]\n\t"
"v_mfma_f32_16x16x32_bf16 a[68:71], a[240:243], a[188:191], a[68:71]\n\t"
"v_mfma_f32_16x16x32_bf16 a[76:79], a[232:235], a[184:187], a[76:79]\n\t"
"s_waitcnt lgkmcnt(0)\n\t"
"v_mfma_f32_16x16x32_bf16 a[76:79], a[236:239], a[188:191], a[76:79]\n\t"
"v_mfma_f32_16x16x32_bf16 a[76:79], a[240:243], a[192:195], a[76:79]\n\t"
"v_mfma_f32_16x16x32_bf16 v[228:231], v[228:231], v[220:223], 0\n\t"
"v_mfma_f32_16x16x32_bf16 a[84:87], a[232:235], a[188:191], a[84:87]\n\t"
"buffer_load_dwordx4 a[220:223], v252, s[8:11], s20 offen offset:1024 sc1\n\t"
"v_mfma_f32_16x16x32_bf16 a[84:87], a[236:239], a[192:195], a[84:87]\n\t"
"v_mfma_f32_16x16x32_bf16 a[84:87], a[240:243], a[196:199], a[84:87]\n\t"
"v_mfma_f32_16x16x32_bf16 a[92:95], a[232:235], a[192:195], a[92:95]\n\t"
"v_mfma_f32_16x16x32_bf16 a[92:95], a[236:239], a[196:199], a[92:95]\n\t"
"v_cvt_pk_bf16_f32 v226, v228, v229\n\t"
"v_cvt_pk_bf16_f32 v227, v230, v231\n\t"
"v_mfma_f32_16x16x32_bf16 a[92:95], a[240:243], a[200:203], a[92:95]\n\t"
"ds_write2st64_b64 v245, v[224:225], v[226:227] offset0:55 offset1:56\n\t"
"v_mfma_f32_16x16x32_bf16 a[100:103], a[232:235], a[196:199], a[100:103]\n\t"
"s_waitcnt vmcnt(14)\n\t"
"v_mfma_f32_16x16x32_bf16 a[100:103], a[236:239], a[200:203], a[100:103]\n\t"
"v_mfma_f32_16x16x32_bf16 a[100:103], a[240:243], a[204:207], a[100:103]\n\t"
"v_mfma_f32_16x16x32_bf16 a[108:111], a[244:247], a[168:171], a[108:111]\n\t"
"v_mfma_f32_16x16x32_bf16 a[108:111], a[208:211], a[172:175], a[108:111]\n\t"
"v_mfma_f32_16x16x32_bf16 a[108:111], a[252:255], a[176:179], a[108:111]\n\t"
"s_waitcnt lgkmcnt(0)\n\t"
"s_barrier\n\t"
"v_mfma_f32_16x16x32_bf16 a[116:119], a[244:247], a[172:175], a[116:119]\n\t"
"ds_read_b128 a[168:171], v244 offset:24576\n\t"
"v_mfma_f32_16x16x32_bf16 a[116:119], a[208:211], a[176:179], a[116:119]\n\t"
"ds_read_b128 a[172:175], v244 offset:25088\n\t"
"v_mfma_f32_16x16x32_bf16 a[116:119], a[252:255], a[180:183], a[116:119]\n\t"
"v_mfma_f32_16x16x32_bf16 a[124:127], a[244:247], a[176:179], a[124:127]\n\t"
"ds_read_b128 a[176:179], v244 offset:25600\n\t"
"v_mfma_f32_16x16x32_bf16 a[124:127], a[208:211], a[180:183], a[124:127]\n\t"
"v_mfma_f32_16x16x32_bf16 a[124:127], a[252:255], a[184:187], a[124:127]\n\t"
"v_mfma_f32_16x16x32_bf16 a[132:135], a[244:247], a[180:183], a[132:135]\n\t"
"ds_read_b128 a[180:183], v244 offset:26112\n\t"
"v_mfma_f32_16x16x32_bf16 a[132:135], a[208:211], a[184:187], a[132:135]\n\t"
"v_mfma_f32_16x16x32_bf16 a[132:135], a[252:255], a[188:191], a[132:135]\n\t"
"v_mfma_f32_16x16x32_bf16 a[140:143], a[244:247], a[184:187], a[140:143]\n\t"
"ds_read_b128 a[184:187], v244 offset:26624\n\t"
"v_mfma_f32_16x16x32_bf16 a[140:143], a[208:211], a[188:191], a[140:143]\n\t"
"v_mfma_f32_16x16x32_bf16 a[140:143], a[252:255], a[192:195], a[140:143]\n\t"
"v_mfma_f32_16x16x32_bf16 a[148:151], a[244:247], a[188:191], a[148:151]\n\t"
"ds_read_b128 a[188:191], v244 offset:27136\n\t"
"v_mfma_f32_16x16x32_bf16 a[148:151], a[208:211], a[192:195], a[148:151]\n\t"
"v_mfma_f32_16x16x32_bf16 a[148:151], a[252:255], a[196:199], a[148:151]\n\t"
"v_mfma_f32_16x16x32_bf16 a[156:159], a[244:247], a[192:195], a[156:159]\n\t"
"ds_read_b128 a[192:195], v244 offset:27648\n\t"
"v_mfma_f32_16x16x32_bf16 a[156:159], a[208:211], a[196:199], a[156:159]\n\t"
"v_mfma_f32_16x16x32_bf16 a[156:159], a[252:255], a[200:203], a[156:159]\n\t"
"v_mfma_f32_16x16x32_bf16 a[164:167], a[244:247], a[196:199], a[164:167]\n\t"
"s_waitcnt vmcnt(11) lgkmcnt(4)\n\t"
"ds_read_b128 a[196:199], v244 offset:28160\n\t"
"v_mfma_f32_16x16x32_bf16 a[164:167], a[208:211], a[200:203], a[164:167]\n\t"
"ds_read_b128 a[200:203], v244 offset:28672\n\t"
"v_mfma_f32_16x16x32_bf16 a[164:167], a[252:255], a[204:207], a[164:167]\n\t"
"ds_read_b128 a[204:207], v244 offset:29184\n\t"
"Lst25_%=:\n\t"
"v_mfma_f32_16x16x32_bf16 v[0:3], v[240:243], a[168:171], v[0:3]\n\t"
"buffer_load_dwordx4 a[208:211], v252, s[8:11], s20 offen offset:2048 sc1\n\t"
"v_mfma_f32_16x16x32_bf16 v[0:3], v[96:99], a[172:175], v[0:3]\n\t"
"s_waitcnt lgkmcnt(6)\n\t"
"s_mov_b32 s20, 663552\n\t"
"v_mfma_f32_16x16x32_bf16 v[0:3], a[248:251], a[176:179], v[0:3]\n\t"
"v_mfma_f32_16x16x32_bf16 v[4:7], v[240:243], a[172:175], v[4:7]\n\t"
"s_waitcnt lgkmcnt(5)\n\t"
"v_mfma_f32_16x16x32_bf16 v[4:7], v[96:99], a[176:179], v[4:7]\n\t"
"buffer_load_dwordx4 a[224:227], v252, s[8:11], s20 offen offset:0 sc1\n\t"
"v_mfma_f32_16x16x32_bf16 v[4:7], a[248:251], a[180:183], v[4:7]\n\t"
"v_mfma_f32_16x16x32_bf16 v[8:11], v[240:243], a[176:179], v[8:11]\n\t"
"s_waitcnt lgkmcnt(4)\n\t"
"v_mfma_f32_16x16x32_bf16 v[8:11], v[96:99], a[180:183], v[8:11]\n\t"
"v_mfma_f32_16x16x32_bf16 v[8:11], a[248:251], a[184:187], v[8:11]\n\t"
"buffer_load_dwordx4 a[228:231], v252, s[8:11], s20 offen offset:1024 sc1\n\t"
"v_mfma_f32_16x16x32_bf16 v[12:15], v[240:243], a[180:183], v[12:15]\n\t"
"s_waitcnt lgkmcnt(3)\n\t"
"v_mfma_f32_16x16x32_bf16 v[12:15], v[96:99], a[184:187], v[12:15]\n\t"
"v_mfma_f32_16x16x32_bf16 v[12:15], a[248:251], a[188:191], v[12:15]\n\t"
"v_mfma_f32_16x16x32_bf16 v[16:19], v[240:243], a[184:187], v[16:19]\n\t"
"buffer_load_dwordx4 a[232:235], v252, s[8:11], s20 offen offset:2048 sc1\n\t"
"v_mfma_f32_16x16x32_bf16 v[16:19], v[96:99], a[188:191], v[16:19]\n\t"
"s_waitcnt lgkmcnt(2)\n\t"
"s_mov_b32 s20, 675840\n\t"
"v_mfma_f32_16x16x32_bf16 v[16:19], a[248:251], a[192:195], v[16:19]\n\t"
"ds_read_b64_tr_b16 v[232:233], v246 offset:512\n\t"
"v_mfma_f32_16x16x32_bf16 v[20:23], v[240:243], a[188:191], v[20:23]\n\t"
"s_waitcnt lgkmcnt(2)\n\t"
"ds_read_b64_tr_b16 v[234:235], v246 offset:3584\n\t"
"v_mfma_f32_16x16x32_bf16 v[20:23], v[96:99], a[192:195], v[20:23]\n\t"
"buffer_load_dwordx4 a[236:239], v252, s[8:11], s20 offen offset:0 sc1\n\t"
"v_mfma_f32_16x16x32_bf16 v[20:23], a[248:251], a[196:199], v[20:23]\n\t"
"v_mfma_f32_16x16x32_bf16 v[24:27], v[240:243], a[192:195], v[24:27]\n\t"
"s_waitcnt lgkmcnt(2)\n\t"
"v_mfma_f32_16x16x32_bf16 v[24:27], v[96:99], a[196:199], v[24:27]\n\t"
"v_mfma_f32_16x16x32_bf16 v[24:27], a[248:251], a[200:203], v[24:27]\n\t"
"buffer_load_dwordx4 a[240:243], v252, s[8:11], s20 offen offset:1024 sc1\n\t"
"v_mfma_f32_16x16x32_bf16 v[28:31], v[240:243], a[196:199], v[28:31]\n\t"
"s_waitcnt vmcnt(14)\n\t"
"ds_read_b64_tr_b16 v[228:229], v246 offset:6656\n\t"
"v_mfma_f32_16x16x32_bf16 v[28:31], v[96:99], a[200:203], v[28:31]\n\t"
"ds_read_b64_tr_b16 v[230:231], v246 offset:9728\n\t"
"v_mfma_f32_16x16x32_bf16 v[28:31], a[248:251], a[204:207], v[28:31]\n\t"
"v_mfma_f32_16x16x32_bf16 v[32:35], v[236:239], a[168:171], v[32:35]\n\t"
"buffer_load_dwordx4 v[240:243], v252, s[8:11], s20 offen offset:2048 sc1\n\t"
"v_mfma_f32_16x16x32_bf16 v[32:35], v[248:251], a[172:175], v[32:35]\n\t"
"s_mov_b32 s20, 688128\n\t"
"v_mfma_f32_16x16x32_bf16 v[32:35], a[212:215], a[176:179], v[32:35]\n\t"
"v_mfma_f32_16x16x32_bf16 v[36:39], v[236:239], a[172:175], v[36:39]\n\t"
"s_waitcnt lgkmcnt(2)\n\t"
"v_mfma_f32_16x16x32_bf16 v[36:39], v[248:251], a[176:179], v[36:39]\n\t"
"buffer_load_dwordx4 v[96:99], v252, s[8:11], s20 offen offset:0 sc1\n\t"
"v_mfma_f32_16x16x32_bf16 v[36:39], a[212:215], a[180:183], v[36:39]\n\t"
"v_mfma_f32_16x16x32_bf16 v[232:235], v[232:235], v[220:223], 0\n\t"
"v_mfma_f32_16x16x32_bf16 v[40:43], v[236:239], a[176:179], v[40:43]\n\t"
"ds_read_b64_tr_b16 v[224:225], v246 offset:12800\n\t"
"v_mfma_f32_16x16x32_bf16 v[40:43], v[248:251], a[180:183], v[40:43]\n\t"
"buffer_load_dwordx4 a[244:247], v252, s[8:11], s20 offen offset:1024 sc1\n\t"
"v_mfma_f32_16x16x32_bf16 v[40:43], a[212:215], a[184:187], v[40:43]\n\t"
"ds_read_b64_tr_b16 v[226:227], v246 offset:15872\n\t"
"v_mfma_f32_16x16x32_bf16 v[44:47], v[236:239], a[180:183], v[44:47]\n\t"
"s_waitcnt lgkmcnt(2)\n\t"
"v_mfma_f32_16x16x32_bf16 v[44:47], v[248:251], a[184:187], v[44:47]\n\t"
"buffer_load_dwordx4 a[248:251], v252, s[8:11], s20 offen offset:2048 sc1\n\t"
"v_mfma_f32_16x16x32_bf16 v[44:47], a[212:215], a[188:191], v[44:47]\n\t"
"s_mov_b32 s20, 921600\n\t"
"v_cvt_pk_bf16_f32 v232, v232, v233\n\t"
"v_cvt_pk_bf16_f32 v233, v234, v235\n\t"
"v_mfma_f32_16x16x32_bf16 v[228:231], v[228:231], v[220:223], 0\n\t"
"v_mfma_f32_16x16x32_bf16 v[48:51], v[236:239], a[184:187], v[48:51]\n\t"
"v_mfma_f32_16x16x32_bf16 v[48:51], v[248:251], a[188:191], v[48:51]\n\t"
"buffer_load_dwordx4 a[252:255], v252, s[8:11], s20 offen offset:0 sc1\n\t"
"v_mfma_f32_16x16x32_bf16 v[48:51], a[212:215], a[192:195], v[48:51]\n\t"
"v_mfma_f32_16x16x32_bf16 v[52:55], v[236:239], a[188:191], v[52:55]\n\t"
"v_mfma_f32_16x16x32_bf16 v[52:55], v[248:251], a[192:195], v[52:55]\n\t"
"v_cvt_pk_bf16_f32 v234, v228, v229\n\t"
"v_cvt_pk_bf16_f32 v235, v230, v231\n\t"
"v_mfma_f32_16x16x32_bf16 v[52:55], a[212:215], a[196:199], v[52:55]\n\t"
"ds_read_b64_tr_b16 v[228:229], v246 offset:20480\n\t"
"v_mfma_f32_16x16x32_bf16 v[56:59], v[236:239], a[192:195], v[56:59]\n\t"
"s_waitcnt lgkmcnt(1)\n\t"
"ds_read_b64_tr_b16 v[230:231], v246 offset:23552\n\t"
"v_mfma_f32_16x16x32_bf16 v[56:59], v[248:251], a[196:199], v[56:59]\n\t"
"v_mfma_f32_16x16x32_bf16 v[56:59], a[212:215], a[200:203], v[56:59]\n\t"
"v_mfma_f32_16x16x32_bf16 v[224:227], v[224:227], v[220:223], 0\n\t"
"v_mfma_f32_16x16x32_bf16 v[60:63], v[236:239], a[196:199], v[60:63]\n\t"
"buffer_load_dwordx4 v[236:239], v252, s[8:11], s20 offen offset:1024 sc1\n\t"
"v_mfma_f32_16x16x32_bf16 v[60:63], v[248:251], a[200:203], v[60:63]\n\t"
"s_waitcnt vmcnt(11)\n\t"
"v_mfma_f32_16x16x32_bf16 v[60:63], a[212:215], a[204:207], v[60:63]\n\t"
"v_mfma_f32_16x16x32_bf16 v[64:67], a[216:219], a[168:171], v[64:67]\n\t"
"buffer_load_dwordx4 v[248:251], v252, s[8:11], s20 offen offset:2048 sc1\n\t"
"v_mfma_f32_16x16x32_bf16 v[64:67], a[220:223], a[172:175], v[64:67]\n\t"
"s_mov_b32 s20, 933888\n\t"
"ds_read_b128 a[168:171], v244 offset:33792\n\t"
"v_cvt_pk_bf16_f32 v224, v224, v225\n\t"
"v_mfma_f32_16x16x32_bf16 v[64:67], a[208:211], a[176:179], v[64:67]\n\t"
"v_cvt_pk_bf16_f32 v225, v226, v227\n\t"
"v_mfma_f32_16x16x32_bf16 v[68:71], a[216:219], a[172:175], v[68:71]\n\t"
"buffer_load_dwordx4 a[212:215], v252, s[8:11], s20 offen offset:0 sc1\n\t"
"v_mfma_f32_16x16x32_bf16 v[68:71], a[220:223], a[176:179], v[68:71]\n\t"
"s_waitcnt lgkmcnt(1)\n\t"
"ds_read_b128 a[172:175], v244 offset:34304\n\t"
"v_mfma_f32_16x16x32_bf16 v[68:71], a[208:211], a[180:183], v[68:71]\n\t"
"v_mfma_f32_16x16x32_bf16 v[228:231], v[228:231], v[220:223], 0\n\t"
"v_mfma_f32_16x16x32_bf16 v[72:75], a[216:219], a[176:179], v[72:75]\n\t"
"ds_read_b128 a[176:179], v244 offset:34816\n\t"
"v_mfma_f32_16x16x32_bf16 v[72:75], a[220:223], a[180:183], v[72:75]\n\t"
"v_mfma_f32_16x16x32_bf16 v[72:75], a[208:211], a[184:187], v[72:75]\n\t"
"v_mfma_f32_16x16x32_bf16 v[76:79], a[216:219], a[180:183], v[76:79]\n\t"
"ds_read_b128 a[180:183], v244 offset:35328\n\t"
"v_mfma_f32_16x16x32_bf16 v[76:79], a[220:223], a[184:187], v[76:79]\n\t"
"v_cvt_pk_bf16_f32 v226, v228, v229\n\t"
"v_cvt_pk_bf16_f32 v227, v230, v231\n\t"
"v_mfma_f32_16x16x32_bf16 v[76:79], a[208:211], a[188:191], v[76:79]\n\t"
"ds_read_b64_tr_b16 v[228:229], v246 offset:26624\n\t"
"v_mfma_f32_16x16x32_bf16 v[80:83], a[216:219], a[184:187], v[80:83]\n\t"
"ds_read_b64_tr_b16 v[230:231], v246 offset:29696\n\t"
"v_mfma_f32_16x16x32_bf16 v[80:83], a[220:223], a[188:191], v[80:83]\n\t"
"ds_read_b128 a[184:187], v244 offset:35840\n\t"
"v_mfma_f32_16x16x32_bf16 v[80:83], a[208:211], a[192:195], v[80:83]\n\t"
"v_mfma_f32_16x16x32_bf16 v[84:87], a[216:219], a[188:191], v[84:87]\n\t"
"ds_read_b128 a[188:191], v244 offset:36352\n\t"
"v_mfma_f32_16x16x32_bf16 v[84:87], a[220:223], a[192:195], v[84:87]\n\t"
"v_mfma_f32_16x16x32_bf16 v[84:87], a[208:211], a[196:199], v[84:87]\n\t"
"v_mfma_f32_16x16x32_bf16 v[88:91], a[216:219], a[192:195], v[88:91]\n\t"
"ds_read_b128 a[192:195], v244 offset:36864\n\t"
"v_mfma_f32_16x16x32_bf16 v[88:91], a[220:223], a[196:199], v[88:91]\n\t"
"v_mfma_f32_16x16x32_bf16 v[88:91], a[208:211], a[200:203], v[88:91]\n\t"
"v_mfma_f32_16x16x32_bf16 v[92:95], a[216:219], a[196:199], v[92:95]\n\t"
"buffer_load_dwordx4 a[216:219], v252, s[8:11], s20 offen offset:1024 sc1\n\t"
"v_mfma_f32_16x16x32_bf16 v[92:95], a[220:223], a[200:203], v[92:95]\n\t"
"s_waitcnt vmcnt(11) lgkmcnt(3)\n\t"
"ds_read_b128 a[196:199], v244 offset:37376\n\t"
"v_mfma_f32_16x16x32_bf16 v[92:95], a[208:211], a[204:207], v[92:95]\n\t"
"ds_read_b128 a[200:203], v244 offset:37888\n\t"
"v_mfma_f32_16x16x32_bf16 v[228:231], v[228:231], v[220:223], 0\n\t"
"ds_read_b128 a[204:207], v244 offset:38400\n\t"
"v_mfma_f32_16x16x32_bf16 v[100:103], a[224:227], a[168:171], v[100:103]\n\t"
"buffer_load_dwordx4 a[208:211], v252, s[8:11], s20 offen offset:2048 sc1\n\t"
"v_mfma_f32_16x16x32_bf16 v[100:103], a[228:231], a[172:175], v[100:103]\n\t"
"s_mov_b32 s20, 946176\n\t"
"v_mfma_f32_16x16x32_bf16 v[100:103], a[232:235], a[176:179], v[100:103]\n\t"
"buffer_load_dwordx4 a[220:223], v252, s[8:11], s20 offen offset:0 sc1\n\t"
"v_mfma_f32_16x16x32_bf16 v[104:107], a[224:227], a[172:175], v[104:107]\n\t"
"s_waitcnt lgkmcnt(5)\n\t"
"v_mfma_f32_16x16x32_bf16 v[104:107], a[228:231], a[176:179], v[104:107]\n\t"
"v_cvt_pk_bf16_f32 v228, v228, v229\n\t"
"v_mfma_f32_16x16x32_bf16 v[104:107], a[232:235], a[180:183], v[104:107]\n\t"
"s_waitcnt lgkmcnt(0)\n\t"
"s_barrier\n\t"
"v_mfma_f32_16x16x32_bf16 v[108:111], a[224:227], a[176:179], v[108:111]\n\t"
"v_cvt_pk_bf16_f32 v229, v230, v231\n\t"
"v_mfma_f32_16x16x32_bf16 v[108:111], a[228:231], a[180:183], v[108:111]\n\t"
"v_mfma_f32_16x16x32_bf16 v[108:111], a[232:235], a[184:187], v[108:111]\n\t"
"ds_write2st64_b64 v245, v[232:233], v[234:235] offset0:48 offset1:49\n\t"
"v_mfma_f32_16x16x32_bf16 v[112:115], a[224:227], a[180:183], v[112:115]\n\t"
"v_mfma_f32_16x16x32_bf16 v[112:115], a[228:231], a[184:187], v[112:115]\n\t"
"ds_read_b64_tr_b16 v[232:233], v246 offset:32768\n\t"
"v_mfma_f32_16x16x32_bf16 v[112:115], a[232:235], a[188:191], v[112:115]\n\t"
"ds_write2st64_b64 v245, v[224:225], v[226:227] offset0:50 offset1:51\n\t"
"v_mfma_f32_16x16x32_bf16 v[116:119], a[224:227], a[184:187], v[116:119]\n\t"
"v_mfma_f32_16x16x32_bf16 v[116:119], a[228:231], a[188:191], v[116:119]\n\t"
"ds_read_b64_tr_b16 v[234:235], v246 offset:35840\n\t"
"v_mfma_f32_16x16x32_bf16 v[116:119], a[232:235], a[192:195], v[116:119]\n\t"
"s_add_u32 m0, s24, 72960\n\t"
"s_add_u32 s28, s21, 2162688\n\t"
"ds_write_b64 v245, v[228:229] offset:26624\n\t"
"v_mfma_f32_16x16x32_bf16 v[120:123], a[224:227], a[188:191], v[120:123]\n\t"
"ds_read_b64_tr_b16 v[224:225], v246 offset:40448\n\t"
"v_mfma_f32_16x16x32_bf16 v[120:123], a[228:231], a[192:195], v[120:123]\n\t"
"ds_read_b64_tr_b16 v[226:227], v246 offset:43520\n\t"
"v_mfma_f32_16x16x32_bf16 v[120:123], a[232:235], a[196:199], v[120:123]\n\t"
"s_waitcnt lgkmcnt(3)\n\t"
"ds_read_b64_tr_b16 v[228:229], v246 offset:46592\n\t"
"v_mfma_f32_16x16x32_bf16 v[232:235], v[232:235], v[220:223], 0\n\t"
"ds_read_b64_tr_b16 v[230:231], v246 offset:49664\n\t"
"v_mfma_f32_16x16x32_bf16 v[124:127], a[224:227], a[192:195], v[124:127]\n\t"
"buffer_load_dwordx4 v247, s[12:15], s28 offen offset:256 nt lds\n\t"
"v_mfma_f32_16x16x32_bf16 v[124:127], a[228:231], a[196:199], v[124:127]\n\t"
"s_add_u32 m0, s24, 80640\n\t"
"s_add_u32 s28, s21, 2359296\n\t"
"v_mfma_f32_16x16x32_bf16 v[124:127], a[232:235], a[200:203], v[124:127]\n\t"
"s_waitcnt lgkmcnt(2)\n\t"
"buffer_load_dwordx4 v247, s[12:15], s28 offen offset:256 nt lds\n\t"
"s_add_u32 m0, s24, 26880\n\t"
"s_add_u32 s28, s21, 786432\n\t"
"v_mfma_f32_16x16x32_bf16 v[224:227], v[224:227], v[220:223], 0\n\t"
"buffer_load_dwordx4 v247, s[12:15], s28 offen offset:256 nt lds\n\t"
"v_mfma_f32_16x16x32_bf16 v[128:131], a[224:227], a[196:199], v[128:131]\n\t"
"s_waitcnt lgkmcnt(0)\n\t"
"s_add_u32 m0, s24, 86784\n\t"
"s_add_u32 s28, s21, 2555904\n\t"
"v_mfma_f32_16x16x32_bf16 v[128:131], a[228:231], a[200:203], v[128:131]\n\t"
"buffer_load_dwordx4 v247, s[12:15], s28 offen offset:256 nt lds\n\t"
"v_mfma_f32_16x16x32_bf16 v[128:131], a[232:235], a[204:207], v[128:131]\n\t"
"s_waitcnt vmcnt(14)\n\t"
"s_add_u32 m0, s24, 33024\n\t"
"s_add_u32 s28, s21, 983040\n\t"
"v_mfma_f32_16x16x32_bf16 v[228:231], v[228:231], v[220:223], 0\n\t"
"buffer_load_dwordx4 v247, s[12:15], s28 offen offset:256 nt lds\n\t"
"v_cvt_pk_bf16_f32 v232, v232, v233\n\t"
"v_cvt_pk_bf16_f32 v233, v234, v235\n\t"
"v_mfma_f32_16x16x32_bf16 v[132:135], a[236:239], a[168:171], v[132:135]\n\t"
"s_add_u32 m0, s24, 40704\n\t"
"s_add_u32 s28, s21, 1179648\n\t"
"v_cvt_pk_bf16_f32 v234, v224, v225\n\t"
"v_mfma_f32_16x16x32_bf16 v[132:135], a[240:243], a[172:175], v[132:135]\n\t"
"v_cvt_pk_bf16_f32 v235, v226, v227\n\t"
"v_mfma_f32_16x16x32_bf16 v[132:135], v[240:243], a[176:179], v[132:135]\n\t"
"buffer_load_dwordx4 v247, s[12:15], s28 offen offset:256 nt lds\n\t"
"v_mfma_f32_16x16x32_bf16 v[136:139], a[236:239], a[172:175], v[136:139]\n\t"
"ds_read_b64_tr_b16 v[224:225], v246 offset:52736\n\t"
"v_mfma_f32_16x16x32_bf16 v[136:139], a[240:243], a[176:179], v[136:139]\n\t"
"ds_read_b64_tr_b16 v[226:227], v246 offset:55808\n\t"
"v_cvt_pk_bf16_f32 v228, v228, v229\n\t"
"v_mfma_f32_16x16x32_bf16 v[136:139], v[240:243], a[180:183], v[136:139]\n\t"
"ds_write2st64_b64 v245, v[232:233], v[234:235] offset0:53 offset1:54\n\t"
"v_mfma_f32_16x16x32_bf16 v[140:143], a[236:239], a[176:179], v[140:143]\n\t"
"v_cvt_pk_bf16_f32 v229, v230, v231\n\t"
"v_mfma_f32_16x16x32_bf16 v[140:143], a[240:243], a[180:183], v[140:143]\n\t"
"v_mfma_f32_16x16x32_bf16 v[140:143], v[240:243], a[184:187], v[140:143]\n\t"
"v_mfma_f32_16x16x32_bf16 v[144:147], a[236:239], a[180:183], v[144:147]\n\t"
"v_mfma_f32_16x16x32_bf16 v[144:147], a[240:243], a[184:187], v[144:147]\n\t"
"v_mfma_f32_16x16x32_bf16 v[144:147], v[240:243], a[188:191], v[144:147]\n\t"
"v_mfma_f32_16x16x32_bf16 v[148:151], a[236:239], a[184:187], v[148:151]\n\t"
"s_waitcnt lgkmcnt(1)\n\t"
"v_mfma_f32_16x16x32_bf16 v[148:151], a[240:243], a[188:191], v[148:151]\n\t"
"v_mfma_f32_16x16x32_bf16 v[148:151], v[240:243], a[192:195], v[148:151]\n\t"
"v_mfma_f32_16x16x32_bf16 v[224:227], v[224:227], v[220:223], 0\n\t"
"v_mfma_f32_16x16x32_bf16 v[152:155], a[236:239], a[188:191], v[152:155]\n\t"
"v_mfma_f32_16x16x32_bf16 v[152:155], a[240:243], a[192:195], v[152:155]\n\t"
"v_mfma_f32_16x16x32_bf16 v[152:155], v[240:243], a[196:199], v[152:155]\n\t"
"v_mfma_f32_16x16x32_bf16 v[156:159], a[236:239], a[192:195], v[156:159]\n\t"
"v_mfma_f32_16x16x32_bf16 v[156:159], a[240:243], a[196:199], v[156:159]\n\t"
"v_cvt_pk_bf16_f32 v230, v224, v225\n\t"
"v_cvt_pk_bf16_f32 v231, v226, v227\n\t"
"v_mfma_f32_16x16x32_bf16 v[156:159], v[240:243], a[200:203], v[156:159]\n\t"
"ds_write2st64_b64 v245, v[228:229], v[230:231] offset0:55 offset1:56\n\t"
"v_mfma_f32_16x16x32_bf16 v[160:163], a[236:239], a[196:199], v[160:163]\n\t"
"s_waitcnt vmcnt(13)\n\t"
"v_mfma_f32_16x16x32_bf16 v[160:163], a[240:243], a[200:203], v[160:163]\n\t"
"v_mfma_f32_16x16x32_bf16 v[160:163], v[240:243], a[204:207], v[160:163]\n\t"
"v_mfma_f32_16x16x32_bf16 v[164:167], v[96:99], a[168:171], v[164:167]\n\t"
"v_mfma_f32_16x16x32_bf16 v[164:167], a[244:247], a[172:175], v[164:167]\n\t"
"v_mfma_f32_16x16x32_bf16 v[164:167], a[248:251], a[176:179], v[164:167]\n\t"
"s_waitcnt lgkmcnt(0)\n\t"
"s_barrier\n\t"
"v_mfma_f32_16x16x32_bf16 v[168:171], v[96:99], a[172:175], v[168:171]\n\t"
"ds_read_b128 a[168:171], v244 offset:24576\n\t"
"v_mfma_f32_16x16x32_bf16 v[168:171], a[244:247], a[176:179], v[168:171]\n\t"
"buffer_load_dwordx4 v[240:243], v252, s[8:11], s20 offen offset:1024 sc1\n\t"
"v_mfma_f32_16x16x32_bf16 v[168:171], a[248:251], a[180:183], v[168:171]\n\t"
"ds_read_b128 a[172:175], v244 offset:25088\n\t"
"v_mfma_f32_16x16x32_bf16 v[172:175], v[96:99], a[176:179], v[172:175]\n\t"
"ds_read_b128 a[176:179], v244 offset:25600\n\t"
"v_mfma_f32_16x16x32_bf16 v[172:175], a[244:247], a[180:183], v[172:175]\n\t"
"v_mfma_f32_16x16x32_bf16 v[172:175], a[248:251], a[184:187], v[172:175]\n\t"
"v_mfma_f32_16x16x32_bf16 v[176:179], v[96:99], a[180:183], v[176:179]\n\t"
"ds_read_b128 a[180:183], v244 offset:26112\n\t"
"v_mfma_f32_16x16x32_bf16 v[176:179], a[244:247], a[184:187], v[176:179]\n\t"
"v_mfma_f32_16x16x32_bf16 v[176:179], a[248:251], a[188:191], v[176:179]\n\t"
"v_mfma_f32_16x16x32_bf16 v[180:183], v[96:99], a[184:187], v[180:183]\n\t"
"ds_read_b128 a[184:187], v244 offset:26624\n\t"
"v_mfma_f32_16x16x32_bf16 v[180:183], a[244:247], a[188:191], v[180:183]\n\t"
"v_mfma_f32_16x16x32_bf16 v[180:183], a[248:251], a[192:195], v[180:183]\n\t"
"v_mfma_f32_16x16x32_bf16 v[184:187], v[96:99], a[188:191], v[184:187]\n\t"
"ds_read_b128 a[188:191], v244 offset:27136\n\t"
"v_mfma_f32_16x16x32_bf16 v[184:187], a[244:247], a[192:195], v[184:187]\n\t"
"v_mfma_f32_16x16x32_bf16 v[184:187], a[248:251], a[196:199], v[184:187]\n\t"
"v_mfma_f32_16x16x32_bf16 v[188:191], v[96:99], a[192:195], v[188:191]\n\t"
"ds_read_b128 a[192:195], v244 offset:27648\n\t"
"v_mfma_f32_16x16x32_bf16 v[188:191], a[244:247], a[196:199], v[188:191]\n\t"
"v_mfma_f32_16x16x32_bf16 v[188:191], a[248:251], a[200:203], v[188:191]\n\t"
"v_mfma_f32_16x16x32_bf16 v[192:195], v[96:99], a[196:199], v[192:195]\n\t"
"s_waitcnt vmcnt(11) lgkmcnt(4)\n\t"
"ds_read_b128 a[196:199], v244 offset:28160\n\t"
"v_mfma_f32_16x16x32_bf16 v[192:195], a[244:247], a[200:203], v[192:195]\n\t"
"ds_read_b128 a[200:203], v244 offset:28672\n\t"
"v_mfma_f32_16x16x32_bf16 v[192:195], a[248:251], a[204:207], v[192:195]\n\t"
"ds_read_b128 a[204:207], v244 offset:29184\n\t"
"Lst26_%=:\n\t"
"v_mfma_f32_16x16x32_bf16 v[0:3], a[252:255], a[168:171], v[0:3]\n\t"
"s_waitcnt lgkmcnt(6)\n\t"
"v_xor_b32 v246, 0xc0, v246\n\t"
"v_mfma_f32_16x16x32_bf16 v[0:3], v[236:239], a[172:175], v[0:3]\n\t"
"s_add_u32 s23, s40, 6291456\n\t"
"s_add_u32 s22, s22, 6291456\n\t"
"v_mfma_f32_16x16x32_bf16 v[0:3], v[248:251], a[176:179], v[0:3]\n\t"
"v_mfma_f32_16x16x32_bf16 v[4:7], a[252:255], a[172:175], v[4:7]\n\t"
"s_waitcnt lgkmcnt(5)\n\t"
"v_mfma_f32_16x16x32_bf16 v[4:7], v[236:239], a[176:179], v[4:7]\n\t"
"v_mfma_f32_16x16x32_bf16 v[4:7], v[248:251], a[180:183], v[4:7]\n\t"
"s_waitcnt lgkmcnt(2)\n\t"
"v_mfma_f32_16x16x32_bf16 v[8:11], a[252:255], a[176:179], v[8:11]\n\t"
"v_mfma_f32_16x16x32_bf16 v[8:11], v[236:239], a[180:183], v[8:11]\n\t"
"v_mfma_f32_16x16x32_bf16 v[8:11], v[248:251], a[184:187], v[8:11]\n\t"
"s_waitcnt lgkmcnt(1)\n\t"
"v_mfma_f32_16x16x32_bf16 v[12:15], a[252:255], a[180:183], v[12:15]\n\t"
"Lpg19_%=:\n\t"
"s_cbranch_vccnz Lvr2_%=\n\t"
"v_mfma_f32_16x16x32_bf16 v[12:15], v[236:239], a[184:187], v[12:15]\n\t"
"v_mfma_f32_16x16x32_bf16 v[12:15], v[248:251], a[188:191], v[12:15]\n\t"
"buffer_load_dwordx4 v[96:99], v252, s[8:11], s20 offen offset:2048 sc1\n\t"
"v_mfma_f32_16x16x32_bf16 v[16:19], a[252:255], a[184:187], v[16:19]\n\t"
"s_mov_b32 s20, 958464\n\t"
"v_mfma_f32_16x16x32_bf16 v[16:19], v[236:239], a[188:191], v[16:19]\n\t"
"v_mfma_f32_16x16x32_bf16 v[16:19], v[248:251], a[192:195], v[16:19]\n\t"
"ds_read_b64_tr_b16 v[232:233], v246 offset:512\n\t"
"v_mfma_f32_16x16x32_bf16 v[20:23], a[252:255], a[188:191], v[20:23]\n\t"
"buffer_load_dwordx4 a[224:227], v252, s[8:11], s20 offen offset:0 sc1\n\t"
"v_mfma_f32_16x16x32_bf16 v[20:23], v[236:239], a[192:195], v[20:23]\n\t"
"ds_read_b64_tr_b16 v[234:235], v246 offset:3584\n\t"
"v_mfma_f32_16x16x32_bf16 v[20:23], v[248:251], a[196:199], v[20:23]\n\t"
"v_mfma_f32_16x16x32_bf16 v[24:27], a[252:255], a[192:195], v[24:27]\n\t"
"buffer_load_dwordx4 a[228:231], v252, s[8:11], s20 offen offset:1024 sc1\n\t"
"v_mfma_f32_16x16x32_bf16 v[24:27], v[236:239], a[196:199], v[24:27]\n\t"
"s_waitcnt lgkmcnt(2)\n\t"
"v_mfma_f32_16x16x32_bf16 v[24:27], v[248:251], a[200:203], v[24:27]\n\t"
"v_mfma_f32_16x16x32_bf16 v[28:31], a[252:255], a[196:199], v[28:31]\n\t"
"s_waitcnt vmcnt(11)\n\t"
"ds_read_b64_tr_b16 v[224:225], v246 offset:6656\n\t"
"v_mfma_f32_16x16x32_bf16 v[28:31], v[236:239], a[200:203], v[28:31]\n\t"
"buffer_load_dwordx4 v[236:239], v252, s[8:11], s20 offen offset:2048 sc1\n\t"
"v_mfma_f32_16x16x32_bf16 v[28:31], v[248:251], a[204:207], v[28:31]\n\t"
"s_mov_b32 s20, 970752\n\t"
"ds_read_b64_tr_b16 v[226:227], v246 offset:9728\n\t"
"v_mfma_f32_16x16x32_bf16 v[32:35], a[212:215], a[168:171], v[32:35]\n\t"
"v_mfma_f32_16x16x32_bf16 v[32:35], a[216:219], a[172:175], v[32:35]\n\t"
"v_mfma_f32_16x16x32_bf16 v[32:35], a[208:211], a[176:179], v[32:35]\n\t"
"buffer_load_dwordx4 v[248:251], v252, s[8:11], s20 offen offset:0 sc1\n\t"
"v_mfma_f32_16x16x32_bf16 v[36:39], a[212:215], a[172:175], v[36:39]\n\t"
"s_waitcnt lgkmcnt(2)\n\t"
"v_mfma_f32_16x16x32_bf16 v[36:39], a[216:219], a[176:179], v[36:39]\n\t"
"v_mfma_f32_16x16x32_bf16 v[36:39], a[208:211], a[180:183], v[36:39]\n\t"
"v_mfma_f32_16x16x32_bf16 v[232:235], v[232:235], v[220:223], 0\n\t"
"buffer_load_dwordx4 a[232:235], v252, s[8:11], s20 offen offset:1024 sc1\n\t"
"v_mfma_f32_16x16x32_bf16 v[40:43], a[212:215], a[176:179], v[40:43]\n\t"
"ds_read_b64_tr_b16 v[228:229], v246 offset:12800\n\t"
"v_mfma_f32_16x16x32_bf16 v[40:43], a[216:219], a[180:183], v[40:43]\n\t"
"ds_read_b64_tr_b16 v[230:231], v246 offset:15872\n\t"
"v_mfma_f32_16x16x32_bf16 v[40:43], a[208:211], a[184:187], v[40:43]\n\t"
"v_mfma_f32_16x16x32_bf16 v[44:47], a[212:215], a[180:183], v[44:47]\n\t"
"buffer_load_dwordx4 a[236:239], v252, s[8:11], s20 offen offset:2048 sc1\n\t"
"v_mfma_f32_16x16x32_bf16 v[44:47], a[216:219], a[184:187], v[44:47]\n\t"
"s_waitcnt lgkmcnt(2)\n\t"
"v_cvt_pk_bf16_f32 v232, v232, v233\n\t"
"v_cvt_pk_bf16_f32 v233, v234, v235\n\t"
"v_mfma_f32_16x16x32_bf16 v[44:47], a[208:211], a[188:191], v[44:47]\n\t"
"s_mov_b32 s20, 983040\n\t"
"v_mfma_f32_16x16x32_bf16 v[224:227], v[224:227], v[220:223], 0\n\t"
"v_mfma_f32_16x16x32_bf16 v[48:51], a[212:215], a[184:187], v[48:51]\n\t"
"buffer_load_dwordx4 a[240:243], v252, s[8:11], s20 offen offset:0 sc1\n\t"
"v_mfma_f32_16x16x32_bf16 v[48:51], a[216:219], a[188:191], v[48:51]\n\t"
"v_mfma_f32_16x16x32_bf16 v[48:51], a[208:211], a[192:195], v[48:51]\n\t"
"v_mfma_f32_16x16x32_bf16 v[52:55], a[212:215], a[188:191], v[52:55]\n\t"
"v_mfma_f32_16x16x32_bf16 v[52:55], a[216:219], a[192:195], v[52:55]\n\t"
"buffer_load_dwordx4 a[244:247], v252, s[8:11], s20 offen offset:1024 sc1\n\t"
"v_mfma_f32_16x16x32_bf16 v[52:55], a[208:211], a[196:199], v[52:55]\n\t"
"v_cvt_pk_bf16_f32 v234, v224, v225\n\t"
"v_cvt_pk_bf16_f32 v235, v226, v227\n\t"
"v_mfma_f32_16x16x32_bf16 v[56:59], a[212:215], a[192:195], v[56:59]\n\t"
"s_waitcnt lgkmcnt(0)\n\t"
"ds_read_b64_tr_b16 v[224:225], v246 offset:20480\n\t"
"v_mfma_f32_16x16x32_bf16 v[56:59], a[216:219], a[196:199], v[56:59]\n\t"
"ds_read_b64_tr_b16 v[226:227], v246 offset:23552\n\t"
"v_mfma_f32_16x16x32_bf16 v[56:59], a[208:211], a[200:203], v[56:59]\n\t"
"buffer_load_dwordx4 a[248:251], v252, s[8:11], s20 offen offset:2048 sc1\n\t"
"v_mfma_f32_16x16x32_bf16 v[228:231], v[228:231], v[220:223], 0\n\t"
"s_mov_b32 s20, 995328\n\t"
"v_mfma_f32_16x16x32_bf16 v[60:63], a[212:215], a[196:199], v[60:63]\n\t"
"s_waitcnt vmcnt(9)\n\t"
"v_mfma_f32_16x16x32_bf16 v[60:63], a[216:219], a[200:203], v[60:63]\n\t"
"v_mfma_f32_16x16x32_bf16 v[60:63], a[208:211], a[204:207], v[60:63]\n\t"
"buffer_load_dwordx4 a[212:215], v252, s[8:11], s20 offen offset:0 sc1\n\t"
"v_mfma_f32_16x16x32_bf16 v[64:67], a[220:223], a[168:171], v[64:67]\n\t"
"ds_read_b128 a[168:171], v244 offset:33792\n\t"
"v_mfma_f32_16x16x32_bf16 v[64:67], v[240:243], a[172:175], v[64:67]\n\t"
"v_cvt_pk_bf16_f32 v228, v228, v229\n\t"
"v_cvt_pk_bf16_f32 v229, v230, v231\n\t"
"v_mfma_f32_16x16x32_bf16 v[64:67], v[96:99], a[176:179], v[64:67]\n\t"
"v_mfma_f32_16x16x32_bf16 v[68:71], a[220:223], a[172:175], v[68:71]\n\t"
"buffer_load_dwordx4 a[216:219], v252, s[8:11], s20 offen offset:1024 sc1\n\t"
"v_mfma_f32_16x16x32_bf16 v[68:71], v[240:243], a[176:179], v[68:71]\n\t"
"s_waitcnt lgkmcnt(1)\n\t"
"ds_read_b128 a[172:175], v244 offset:34304\n\t"
"v_mfma_f32_16x16x32_bf16 v[68:71], v[96:99], a[180:183], v[68:71]\n\t"
"buffer_load_dwordx4 a[252:255], v252, s[8:11], s20 offen offset:2048 sc1\n\t"
"v_mfma_f32_16x16x32_bf16 v[224:227], v[224:227], v[220:223], 0\n\t"
"v_mfma_f32_16x16x32_bf16 v[72:75], a[220:223], a[176:179], v[72:75]\n\t"
"ds_read_b128 a[176:179], v244 offset:34816\n\t"
"v_mfma_f32_16x16x32_bf16 v[72:75], v[240:243], a[180:183], v[72:75]\n\t"
"v_mfma_f32_16x16x32_bf16 v[72:75], v[96:99], a[184:187], v[72:75]\n\t"
"v_mfma_f32_16x16x32_bf16 v[76:79], a[220:223], a[180:183], v[76:79]\n\t"
"ds_read_b128 a[180:183], v244 offset:35328\n\t"
"v_mfma_f32_16x16x32_bf16 v[76:79], v[240:243], a[184:187], v[76:79]\n\t"
"v_cvt_pk_bf16_f32 v230, v224, v225\n\t"
"v_cvt_pk_bf16_f32 v231, v226, v227\n\t"
"v_mfma_f32_16x16x32_bf16 v[76:79], v[96:99], a[188:191], v[76:79]\n\t"
"ds_read_b64_tr_b16 v[224:225], v246 offset:26624\n\t"
"v_mfma_f32_16x16x32_bf16 v[80:83], a[220:223], a[184:187], v[80:83]\n\t"
"ds_read_b64_tr_b16 v[226:227], v246 offset:29696\n\t"
"v_mfma_f32_16x16x32_bf16 v[80:83], v[240:243], a[188:191], v[80:83]\n\t"
"ds_read_b128 a[184:187], v244 offset:35840\n\t"
"v_mfma_f32_16x16x32_bf16 v[80:83], v[96:99], a[192:195], v[80:83]\n\t"
"v_mfma_f32_16x16x32_bf16 v[84:87], a[220:223], a[188:191], v[84:87]\n\t"
"s_mov_b32 s20, 1007616\n\t"
"ds_read_b128 a[188:191], v244 offset:36352\n\t"
"v_mfma_f32_16x16x32_bf16 v[84:87], v[240:243], a[192:195], v[84:87]\n\t"
"v_mfma_f32_16x16x32_bf16 v[84:87], v[96:99], a[196:199], v[84:87]\n\t"
"v_mfma_f32_16x16x32_bf16 v[88:91], a[220:223], a[192:195], v[88:91]\n\t"
"ds_read_b128 a[192:195], v244 offset:36864\n\t"
"v_mfma_f32_16x16x32_bf16 v[88:91], v[240:243], a[196:199], v[88:91]\n\t"
"v_mfma_f32_16x16x32_bf16 v[88:91], v[96:99], a[200:203], v[88:91]\n\t"
"v_mfma_f32_16x16x32_bf16 v[92:95], a[220:223], a[196:199], v[92:95]\n\t"
"buffer_load_dwordx4 a[220:223], v252, s[8:11], s20 offen offset:0 sc1\n\t"
"v_mfma_f32_16x16x32_bf16 v[92:95], v[240:243], a[200:203], v[92:95]\n\t"
"s_waitcnt vmcnt(10) lgkmcnt(3)\n\t"
"ds_read_b128 a[196:199], v244 offset:37376\n\t"
"v_mfma_f32_16x16x32_bf16 v[92:95], v[96:99], a[204:207], v[92:95]\n\t"
"ds_read_b128 a[200:203], v244 offset:37888\n\t"
"v_mfma_f32_16x16x32_bf16 v[224:227], v[224:227], v[220:223], 0\n\t"
"ds_read_b128 a[204:207], v244 offset:38400\n\t"
"v_mfma_f32_16x16x32_bf16 v[100:103], a[224:227], a[168:171], v[100:103]\n\t"
"buffer_load_dwordx4 v[96:99], v252, s[8:11], s20 offen offset:1024 sc1\n\t"
"v_mfma_f32_16x16x32_bf16 v[100:103], a[228:231], a[172:175], v[100:103]\n\t"
"v_mfma_f32_16x16x32_bf16 v[100:103], v[236:239], a[176:179], v[100:103]\n\t"
"v_mfma_f32_16x16x32_bf16 v[104:107], a[224:227], a[172:175], v[104:107]\n\t"
"s_waitcnt lgkmcnt(5)\n\t"
"v_mfma_f32_16x16x32_bf16 v[104:107], a[228:231], a[176:179], v[104:107]\n\t"
"v_cvt_pk_bf16_f32 v224, v224, v225\n\t"
"v_mfma_f32_16x16x32_bf16 v[104:107], v[236:239], a[180:183], v[104:107]\n\t"
"s_waitcnt lgkmcnt(0)\n\t"
"s_barrier\n\t"
"v_mfma_f32_16x16x32_bf16 v[108:111], a[224:227], a[176:179], v[108:111]\n\t"
"v_cvt_pk_bf16_f32 v225, v226, v227\n\t"
"v_mfma_f32_16x16x32_bf16 v[108:111], a[228:231], a[180:183], v[108:111]\n\t"
"v_mfma_f32_16x16x32_bf16 v[108:111], v[236:239], a[184:187], v[108:111]\n\t"
"ds_write2st64_b64 v245, v[232:233], v[234:235] offset0:48 offset1:49\n\t"
"v_mfma_f32_16x16x32_bf16 v[112:115], a[224:227], a[180:183], v[112:115]\n\t"
"v_mfma_f32_16x16x32_bf16 v[112:115], a[228:231], a[184:187], v[112:115]\n\t"
"ds_read_b64_tr_b16 v[232:233], v246 offset:32768\n\t"
"v_mfma_f32_16x16x32_bf16 v[112:115], v[236:239], a[188:191], v[112:115]\n\t"
"ds_write2st64_b64 v245, v[228:229], v[230:231] offset0:50 offset1:51\n\t"
"v_mfma_f32_16x16x32_bf16 v[116:119], a[224:227], a[184:187], v[116:119]\n\t"
"global_load_dwordx4 v[240:243], v252, s[48:49] offset:2048\n\t"
"v_mfma_f32_16x16x32_bf16 v[116:119], a[228:231], a[188:191], v[116:119]\n\t"
"ds_read_b64_tr_b16 v[234:235], v246 offset:35840\n\t"
"v_mfma_f32_16x16x32_bf16 v[116:119], v[236:239], a[192:195], v[116:119]\n\t"
"ds_write_b64 v245, v[224:225] offset:26624\n\t"
"v_mfma_f32_16x16x32_bf16 v[120:123], a[224:227], a[188:191], v[120:123]\n\t"
"ds_read_b64_tr_b16 v[228:229], v246 offset:40448\n\t"
"v_mfma_f32_16x16x32_bf16 v[120:123], a[228:231], a[192:195], v[120:123]\n\t"
"ds_read_b64_tr_b16 v[230:231], v246 offset:43520\n\t"
"v_mfma_f32_16x16x32_bf16 v[120:123], v[236:239], a[196:199], v[120:123]\n\t"
"s_waitcnt lgkmcnt(3)\n\t"
"ds_read_b64_tr_b16 v[224:225], v246 offset:46592\n\t"
"v_mfma_f32_16x16x32_bf16 v[232:235], v[232:235], v[220:223], 0\n\t"
"ds_read_b64_tr_b16 v[226:227], v246 offset:49664\n\t"
"v_mfma_f32_16x16x32_bf16 v[124:127], a[224:227], a[192:195], v[124:127]\n\t"
"global_load_dwordx4 a[208:211], v252, s[46:47] offset:1024\n\t"
"v_mfma_f32_16x16x32_bf16 v[124:127], a[228:231], a[196:199], v[124:127]\n\t"
"v_mfma_f32_16x16x32_bf16 v[124:127], v[236:239], a[200:203], v[124:127]\n\t"
"s_waitcnt lgkmcnt(2)\n\t"
"v_mfma_f32_16x16x32_bf16 v[228:231], v[228:231], v[220:223], 0\n\t"
"v_mfma_f32_16x16x32_bf16 v[128:131], a[224:227], a[196:199], v[128:131]\n\t"
"buffer_load_dwordx4 a[224:227], v252, s[8:11], s20 offen offset:2048 sc1\n\t"
"v_mfma_f32_16x16x32_bf16 v[128:131], a[228:231], a[200:203], v[128:131]\n\t"
"s_waitcnt vmcnt(11) lgkmcnt(0)\n\t"
"s_mov_b32 s20, 1019904\n\t"
"v_cvt_pk_bf16_f32 v232, v232, v233\n\t"
"v_mfma_f32_16x16x32_bf16 v[128:131], v[236:239], a[204:207], v[128:131]\n\t"
"v_cvt_pk_bf16_f32 v233, v234, v235\n\t"
"v_mfma_f32_16x16x32_bf16 v[224:227], v[224:227], v[220:223], 0\n\t"
"buffer_load_dwordx4 a[228:231], v252, s[8:11], s20 offen offset:0 sc1\n\t"
"v_mfma_f32_16x16x32_bf16 v[132:135], v[248:251], a[168:171], v[132:135]\n\t"
"s_add_u32 m0, s24, 46848\n\t"
"s_add_u32 s28, s21, 1376256\n\t"
"v_cvt_pk_bf16_f32 v234, v228, v229\n\t"
"v_mfma_f32_16x16x32_bf16 v[132:135], a[232:235], a[172:175], v[132:135]\n\t"
"v_cvt_pk_bf16_f32 v235, v230, v231\n\t"
"v_mfma_f32_16x16x32_bf16 v[132:135], a[236:239], a[176:179], v[132:135]\n\t"
"ds_write2st64_b64 v245, v[232:233], v[234:235] offset0:53 offset1:54\n\t"
"v_mfma_f32_16x16x32_bf16 v[136:139], v[248:251], a[172:175], v[136:139]\n\t"
"buffer_load_dwordx4 v247, s[12:15], s28 offen offset:256 nt lds\n\t"
"v_mfma_f32_16x16x32_bf16 v[136:139], a[232:235], a[176:179], v[136:139]\n\t"
"s_add_u32 m0, s24, 52992\n\t"
"s_add_u32 s28, s21, 1572864\n\t"
"ds_read_b64_tr_b16 v[228:229], v246 offset:52736\n\t"
"v_mfma_f32_16x16x32_bf16 v[136:139], a[236:239], a[180:183], v[136:139]\n\t"
"buffer_load_dwordx4 v247, s[12:15], s28 offen offset:256 nt lds\n\t"
"v_mfma_f32_16x16x32_bf16 v[140:143], v[248:251], a[176:179], v[140:143]\n\t"
"s_add_u32 m0, s24, 92928\n\t"
"s_add_u32 s28, s21, 2752512\n\t"
"ds_read_b64_tr_b16 v[230:231], v246 offset:55808\n\t"
"v_mfma_f32_16x16x32_bf16 v[140:143], a[232:235], a[180:183], v[140:143]\n\t"
"buffer_load_dwordx4 v247, s[12:15], s28 offen offset:256 nt lds\n\t"
"v_mfma_f32_16x16x32_bf16 v[140:143], a[236:239], a[184:187], v[140:143]\n\t"
"s_add_u32 m0, s24, 100608\n\t"
"s_add_u32 s28, s21, 2949120\n\t"
"v_cvt_pk_bf16_f32 v224, v224, v225\n\t"
"v_cvt_pk_bf16_f32 v225, v226, v227\n\t"
"v_mfma_f32_16x16x32_bf16 v[144:147], v[248:251], a[180:183], v[144:147]\n\t"
"v_mfma_f32_16x16x32_bf16 v[144:147], a[232:235], a[184:187], v[144:147]\n\t"
"buffer_load_dwordx4 v247, s[12:15], s28 offen offset:256 nt lds\n\t"
"v_mfma_f32_16x16x32_bf16 v[144:147], a[236:239], a[188:191], v[144:147]\n\t"
"s_add_u32 m0, s24, 106752\n\t"
"s_add_u32 s28, s21, 3145728\n\t"
"v_mfma_f32_16x16x32_bf16 v[148:151], v[248:251], a[184:187], v[148:151]\n\t"
"buffer_load_dwordx4 v247, s[12:15], s28 offen offset:256 nt lds\n\t"
"v_mfma_f32_16x16x32_bf16 v[148:151], a[232:235], a[188:191], v[148:151]\n\t"
"s_waitcnt lgkmcnt(0)\n\t"
"s_add_u32 m0, s24, 112896\n\t"
"s_add_u32 s28, s21, 3342336\n\t"
"v_mfma_f32_16x16x32_bf16 v[148:151], a[236:239], a[192:195], v[148:151]\n\t"
"buffer_load_dwordx4 v247, s[12:15], s28 offen offset:256 nt lds\n\t"
"s_add_u32 m0, s25, 768\n\t"
"v_mfma_f32_16x16x32_bf16 v[228:231], v[228:231], v[220:223], 0\n\t"
"buffer_load_dwordx4 v254, s[12:15], 0 offen offset:256 nt lds\n\t"
"v_mfma_f32_16x16x32_bf16 v[152:155], v[248:251], a[188:191], v[152:155]\n\t"
"s_add_u32 m0, s26, 768\n\t"
"v_mfma_f32_16x16x32_bf16 v[152:155], a[232:235], a[192:195], v[152:155]\n\t"
"v_mfma_f32_16x16x32_bf16 v[152:155], a[236:239], a[196:199], v[152:155]\n\t"
"s_mov_b64 exec, s[36:37]\n\t"
"buffer_load_dwordx4 v254, s[12:15], s27 offen offset:256 nt lds\n\t"
"s_mov_b64 exec, -1\n\t"
"v_mfma_f32_16x16x32_bf16 v[156:159], v[248:251], a[192:195], v[156:159]\n\t"
"s_waitcnt vmcnt(11)\n\t"
"ds_read_b64_tr_b16 v[236:237], v240 offset:512\n\t"
"v_mfma_f32_16x16x32_bf16 v[156:159], a[232:235], a[196:199], v[156:159]\n\t"
"ds_read_b64_tr_b16 v[238:239], v240 offset:2048\n\t"
"v_cvt_pk_bf16_f32 v226, v228, v229\n\t"
"v_mfma_f32_16x16x32_bf16 v[156:159], a[236:239], a[200:203], v[156:159]\n\t"
"ds_read_b64_tr_b16 v[232:233], v241 offset:512\n\t"
"v_cvt_pk_bf16_f32 v227, v230, v231\n\t"
"v_mfma_f32_16x16x32_bf16 v[160:163], v[248:251], a[196:199], v[160:163]\n\t"
"ds_read_b64_tr_b16 v[234:235], v241 offset:2048\n\t"
"v_mfma_f32_16x16x32_bf16 v[160:163], a[232:235], a[200:203], v[160:163]\n\t"
"v_mfma_f32_16x16x32_bf16 v[160:163], a[236:239], a[204:207], v[160:163]\n\t"
"ds_write2st64_b64 v245, v[224:225], v[226:227] offset0:55 offset1:56\n\t"
"v_mfma_f32_16x16x32_bf16 v[164:167], a[240:243], a[168:171], v[164:167]\n\t"
"v_mfma_f32_16x16x32_bf16 v[164:167], a[244:247], a[172:175], v[164:167]\n\t"
"v_mfma_f32_16x16x32_bf16 v[164:167], a[248:251], a[176:179], v[164:167]\n\t"
"s_waitcnt lgkmcnt(0)\n\t"
"s_barrier\n\t"
"v_mfma_f32_16x16x32_bf16 v[168:171], a[240:243], a[172:175], v[168:171]\n\t"
"ds_read_b64_tr_b16 v[228:229], v240 offset:20480\n\t"
"v_mfma_f32_16x16x32_bf16 v[168:171], a[244:247], a[176:179], v[168:171]\n\t"
"ds_read_b64_tr_b16 v[230:231], v240 offset:22016\n\t"
"v_mfma_f32_16x16x32_bf16 v[168:171], a[248:251], a[180:183], v[168:171]\n\t"
"ds_read_b64_tr_b16 v[224:225], v241 offset:20480\n\t"
"v_mfma_f32_16x16x32_bf16 v[172:175], a[240:243], a[176:179], v[172:175]\n\t"
"s_waitcnt vmcnt(10)\n\t"
"ds_read_b64_tr_b16 v[226:227], v241 offset:22016\n\t"
"v_mfma_f32_16x16x32_bf16 v[172:175], a[244:247], a[180:183], v[172:175]\n\t"
"ds_read_b128 a[168:171], v244 offset:24576\n\t"
"v_mfma_f32_16x16x32_bf16 v[172:175], a[248:251], a[184:187], v[172:175]\n\t"
"ds_read_b128 a[172:175], v244 offset:25088\n\t"
"v_mfma_f32_16x16x32_bf16 v[236:239], v[236:239], a[208:211], 0\n\t"
"ds_read_b128 a[176:179], v244 offset:25600\n\t"
"v_mfma_f32_16x16x32_bf16 v[232:235], v[232:235], a[208:211], 0\n\t"
"v_mfma_f32_16x16x32_bf16 v[176:179], a[240:243], a[180:183], v[176:179]\n\t"
"ds_read_b128 a[180:183], v244 offset:26112\n\t"
"v_mfma_f32_16x16x32_bf16 v[176:179], a[244:247], a[184:187], v[176:179]\n\t"
"v_mfma_f32_16x16x32_bf16 v[176:179], a[248:251], a[188:191], v[176:179]\n\t"
"v_mfma_f32_16x16x32_bf16 v[180:183], a[240:243], a[184:187], v[180:183]\n\t"
"s_waitcnt lgkmcnt(6)\n\t"
"ds_read_b128 a[184:187], v244 offset:26624\n\t"
"v_cvt_pk_bf16_f32 v236, v236, v237\n\t"
"v_mfma_f32_16x16x32_bf16 v[180:183], a[244:247], a[188:191], v[180:183]\n\t"
"s_waitcnt lgkmcnt(5)\n\t"
"v_cvt_pk_bf16_f32 v237, v238, v239\n\t"
"v_cvt_pk_bf16_f32 v238, v232, v233\n\t"
"v_mfma_f32_16x16x32_bf16 v[180:183], a[248:251], a[192:195], v[180:183]\n\t"
"v_cvt_pk_bf16_f32 v239, v234, v235\n\t"
"v_mfma_f32_16x16x32_bf16 v[228:231], v[228:231], a[208:211], 0\n\t"
"v_mfma_f32_16x16x32_bf16 v[224:227], v[224:227], a[208:211], 0\n\t"
"ds_read_b64_tr_b16 v[232:233], v240 offset:40448\n\t"
"v_mfma_f32_16x16x32_bf16 v[184:187], a[240:243], a[188:191], v[184:187]\n\t"
"ds_read_b128 a[188:191], v244 offset:27136\n\t"
"v_mfma_f32_16x16x32_bf16 v[184:187], a[244:247], a[192:195], v[184:187]\n\t"
"ds_read_b64_tr_b16 v[234:235], v240 offset:41984\n\t"
"v_mfma_f32_16x16x32_bf16 v[184:187], a[248:251], a[196:199], v[184:187]\n\t"
"v_mfma_f32_16x16x32_bf16 v[188:191], a[240:243], a[192:195], v[188:191]\n\t"
"ds_read_b128 a[192:195], v244 offset:27648\n\t"
"v_cvt_pk_bf16_f32 v228, v228, v229\n\t"
"v_mfma_f32_16x16x32_bf16 v[188:191], a[244:247], a[196:199], v[188:191]\n\t"
"v_cvt_pk_bf16_f32 v229, v230, v231\n\t"
"v_cvt_pk_bf16_f32 v230, v224, v225\n\t"
"v_mfma_f32_16x16x32_bf16 v[188:191], a[248:251], a[200:203], v[188:191]\n\t"
"v_cvt_pk_bf16_f32 v231, v226, v227\n\t"
"v_mfma_f32_16x16x32_bf16 v[192:195], a[240:243], a[196:199], v[192:195]\n\t"
"s_waitcnt lgkmcnt(6)\n\t"
"ds_read_b64_tr_b16 v[224:225], v241 offset:40448\n\t"
"v_mfma_f32_16x16x32_bf16 v[192:195], a[244:247], a[200:203], v[192:195]\n\t"
"ds_read_b64_tr_b16 v[226:227], v241 offset:41984\n\t"
"v_mfma_f32_16x16x32_bf16 v[192:195], a[248:251], a[204:207], v[192:195]\n\t"
"ds_read_b128 a[196:199], v244 offset:28160\n\t"
"Lst27_%=:\n\t"
"v_mfma_f32_16x16x32_bf16 v[196:199], a[212:215], a[168:171], v[196:199]\n\t"
"s_mov_b64 vcc, -1\n\t"
"s_bitcmp1_b32 s73, 1\n\t"
"s_cbranch_scc1 Lvh4_%=\n\t"
"s_bitcmp1_b32 s73, 0\n\t"
"s_cbranch_scc1 Lvr4_%=\n\t"
"s_branch Lpg25_%=\n\t"
"Lvh4_%=:\n\t"
"s_bitcmp1_b32 s73, 0\n\t"
"s_cbranch_scc1 Lvr4_%=\n\t"
"s_branch Lvr4_%=\n\t"
"Lvr4_%=:\n\t"
"s_mov_b64 vcc, 0\n\t"
"s_waitcnt lgkmcnt(8)\n\t"
"ds_read_b128 a[200:203], v244 offset:28672\n\t"
"v_mfma_f32_16x16x32_bf16 v[196:199], a[216:219], a[172:175], v[196:199]\n\t"
"ds_read_b128 a[204:207], v244 offset:29184\n\t"
"v_mfma_f32_16x16x32_bf16 v[196:199], a[252:255], a[176:179], v[196:199]\n\t"
"buffer_load_dwordx4 v[248:251], v252, s[8:11], s20 offen offset:1024 sc1\n\t"
"v_mfma_f32_16x16x32_bf16 v[204:207], a[212:215], a[172:175], v[204:207]\n\t"
"s_waitcnt lgkmcnt(6)\n\t"
"v_mfma_f32_16x16x32_bf16 v[204:207], a[216:219], a[176:179], v[204:207]\n\t"
"s_waitcnt lgkmcnt(3)\n\t"
"v_mfma_f32_16x16x32_bf16 v[204:207], a[252:255], a[180:183], v[204:207]\n\t"
"v_mfma_f32_16x16x32_bf16 v[232:235], v[232:235], a[208:211], 0\n\t"
"v_add_u32 v240, 59904, v240\n\t"
"v_add_u32 v241, 59904, v241\n\t"
"v_mfma_f32_16x16x32_bf16 v[224:227], v[224:227], a[208:211], 0\n\t"
"v_mfma_f32_16x16x32_bf16 v[212:215], a[212:215], a[176:179], v[212:215]\n\t"
"v_mfma_f32_16x16x32_bf16 v[212:215], a[216:219], a[180:183], v[212:215]\n\t"
"v_mfma_f32_16x16x32_bf16 v[212:215], a[252:255], a[184:187], v[212:215]\n\t"
"s_barrier\n\t"
"v_mfma_f32_16x16x32_bf16 a[0:3], a[212:215], a[180:183], a[0:3]\n\t"
"v_cvt_pk_bf16_f32 v232, v232, v233\n\t"
"v_mfma_f32_16x16x32_bf16 a[0:3], a[216:219], a[184:187], a[0:3]\n\t"
"v_cvt_pk_bf16_f32 v233, v234, v235\n\t"
"v_cvt_pk_bf16_f32 v234, v224, v225\n\t"
"v_mfma_f32_16x16x32_bf16 a[0:3], a[252:255], a[188:191], a[0:3]\n\t"
"ds_write_b128 v243, v[236:239] offset:512\n\t"
"v_mfma_f32_16x16x32_bf16 a[8:11], a[212:215], a[184:187], a[8:11]\n\t"
"s_waitcnt lgkmcnt(3)\n\t"
"v_cvt_pk_bf16_f32 v235, v226, v227\n\t"
"v_mfma_f32_16x16x32_bf16 a[8:11], a[216:219], a[188:191], a[8:11]\n\t"
"ds_read_b64_tr_b16 v[236:237], v240 offset:512\n\t"
"v_mfma_f32_16x16x32_bf16 a[8:11], a[252:255], a[192:195], a[8:11]\n\t"
"ds_write_b128 v243, v[228:231] offset:9728\n\t"
"v_mfma_f32_16x16x32_bf16 a[16:19], a[212:215], a[188:191], a[16:19]\n\t"
"buffer_load_dwordx4 a[232:235], v252, s[8:11], s20 offen offset:2048 sc1\n\t"
"v_mfma_f32_16x16x32_bf16 a[16:19], a[216:219], a[192:195], a[16:19]\n\t"
"s_waitcnt lgkmcnt(4)\n\t"
"ds_read_b64_tr_b16 v[238:239], v240 offset:2048\n\t"
"v_mfma_f32_16x16x32_bf16 a[16:19], a[252:255], a[196:199], a[16:19]\n\t"
"s_mov_b32 s20, 1032192\n\t"
"ds_write_b128 v243, v[232:235] offset:18944\n\t"
"v_mfma_f32_16x16x32_bf16 a[24:27], a[212:215], a[192:195], a[24:27]\n\t"
"buffer_load_dwordx4 a[236:239], v252, s[8:11], s20 offen offset:0 sc1\n\t"
"v_mfma_f32_16x16x32_bf16 a[24:27], a[216:219], a[196:199], a[24:27]\n\t"
"s_waitcnt lgkmcnt(5)\n\t"
"ds_read_b64_tr_b16 v[224:225], v241 offset:512\n\t"
"v_mfma_f32_16x16x32_bf16 a[24:27], a[252:255], a[200:203], a[24:27]\n\t"
"ds_read_b64_tr_b16 v[226:227], v241 offset:2048\n\t"
"v_mfma_f32_16x16x32_bf16 a[32:35], a[212:215], a[196:199], a[32:35]\n\t"
"s_waitcnt lgkmcnt(3)\n\t"
"ds_read_b64_tr_b16 v[228:229], v240 offset:20480\n\t"
"v_mfma_f32_16x16x32_bf16 a[32:35], a[216:219], a[200:203], a[32:35]\n\t"
"ds_read_b64_tr_b16 v[230:231], v240 offset:22016\n\t"
"v_mfma_f32_16x16x32_bf16 a[32:35], a[252:255], a[204:207], a[32:35]\n\t"
"s_waitcnt vmcnt(12)\n\t"
"ds_read_b64_tr_b16 v[232:233], v241 offset:20480\n\t"
"v_mfma_f32_16x16x32_bf16 v[236:239], v[236:239], a[208:211], 0\n\t"
"s_waitcnt lgkmcnt(3)\n\t"
"ds_read_b64_tr_b16 v[234:235], v241 offset:22016\n\t"
"v_mfma_f32_16x16x32_bf16 v[224:227], v[224:227], a[208:211], 0\n\t"
"buffer_load_dwordx4 a[212:215], v252, s[8:11], s20 offen offset:1024 sc1\n\t"
"v_mfma_f32_16x16x32_bf16 a[40:43], a[220:223], a[168:171], a[40:43]\n\t"
"buffer_load_dwordx4 a[216:219], v252, s[8:11], s20 offen offset:2048 sc1\n\t"
"v_mfma_f32_16x16x32_bf16 a[40:43], v[96:99], a[172:175], a[40:43]\n\t"
"s_mov_b32 s20, 1044480\n\t"
"v_mfma_f32_16x16x32_bf16 a[40:43], a[224:227], a[176:179], a[40:43]\n\t"
"v_mfma_f32_16x16x32_bf16 a[48:51], a[220:223], a[172:175], a[48:51]\n\t"
"s_waitcnt lgkmcnt(2)\n\t"
"v_cvt_pk_bf16_f32 v236, v236, v237\n\t"
"v_cvt_pk_bf16_f32 v237, v238, v239\n\t"
"v_mfma_f32_16x16x32_bf16 a[48:51], v[96:99], a[176:179], a[48:51]\n\t"
"s_waitcnt lgkmcnt(0)\n\t"
"v_cvt_pk_bf16_f32 v238, v224, v225\n\t"
"v_cvt_pk_bf16_f32 v239, v226, v227\n\t"
"v_mfma_f32_16x16x32_bf16 a[48:51], a[224:227], a[180:183], a[48:51]\n\t"
"ds_write_b128 v243, v[236:239] offset:28160\n\t"
"v_mfma_f32_16x16x32_bf16 v[228:231], v[228:231], a[208:211], 0\n\t"
"buffer_load_dwordx4 a[240:243], v252, s[8:11], s20 offen offset:0 sc1\n\t"
"v_mfma_f32_16x16x32_bf16 v[232:235], v[232:235], a[208:211], 0\n\t"
"ds_read_b64_tr_b16 v[224:225], v240 offset:40448\n\t"
"v_mfma_f32_16x16x32_bf16 a[56:59], a[220:223], a[176:179], a[56:59]\n\t"
"buffer_load_dwordx4 a[244:247], v252, s[8:11], s20 offen offset:1024 sc1\n\t"
"v_mfma_f32_16x16x32_bf16 a[56:59], v[96:99], a[180:183], a[56:59]\n\t"
"ds_read_b64_tr_b16 v[226:227], v240 offset:41984\n\t"
"v_mfma_f32_16x16x32_bf16 a[56:59], a[224:227], a[184:187], a[56:59]\n\t"
"ds_read_b64_tr_b16 v[236:237], v241 offset:40448\n\t"
"v_mfma_f32_16x16x32_bf16 a[64:67], a[220:223], a[180:183], a[64:67]\n\t"
"ds_read_b64_tr_b16 v[238:239], v241 offset:41984\n\t"
"v_cvt_pk_bf16_f32 v228, v228, v229\n\t"
"v_mfma_f32_16x16x32_bf16 a[64:67], v[96:99], a[184:187], a[64:67]\n\t"
"v_cvt_pk_bf16_f32 v229, v230, v231\n\t"
"v_cvt_pk_bf16_f32 v230, v232, v233\n\t"
"v_mfma_f32_16x16x32_bf16 a[64:67], a[224:227], a[188:191], a[64:67]\n\t"
"v_cvt_pk_bf16_f32 v231, v234, v235\n\t"
"v_mfma_f32_16x16x32_bf16 a[72:75], a[220:223], a[184:187], a[72:75]\n\t"
"buffer_load_dwordx4 a[248:251], v252, s[8:11], s20 offen offset:2048 sc1\n\t"
"v_mfma_f32_16x16x32_bf16 a[72:75], v[96:99], a[188:191], a[72:75]\n\t"
"s_mov_b32 s20, 1056768\n\t"
"v_mfma_f32_16x16x32_bf16 a[72:75], a[224:227], a[192:195], a[72:75]\n\t"
"ds_write_b128 v243, v[228:231] offset:37376\n\t"
"v_mfma_f32_16x16x32_bf16 a[80:83], a[220:223], a[188:191], a[80:83]\n\t"
"buffer_load_dwordx4 a[252:255], v252, s[8:11], s20 offen offset:0 sc1\n\t"
"v_mfma_f32_16x16x32_bf16 a[80:83], v[96:99], a[192:195], a[80:83]\n\t"
"v_mfma_f32_16x16x32_bf16 a[80:83], a[224:227], a[196:199], a[80:83]\n\t"
"v_mfma_f32_16x16x32_bf16 a[88:91], a[220:223], a[192:195], a[88:91]\n\t"
"s_waitcnt lgkmcnt(3)\n\t"
"v_mfma_f32_16x16x32_bf16 a[88:91], v[96:99], a[196:199], a[88:91]\n\t"
"s_waitcnt lgkmcnt(1)\n\t"
"v_mfma_f32_16x16x32_bf16 a[88:91], a[224:227], a[200:203], a[88:91]\n\t"
"v_mfma_f32_16x16x32_bf16 v[224:227], v[224:227], a[208:211], 0\n\t"
"v_mfma_f32_16x16x32_bf16 v[236:239], v[236:239], a[208:211], 0\n\t"
"ds_read_b64_tr_b16 v[232:233], v246 offset:1024\n\t"
"v_mfma_f32_16x16x32_bf16 a[96:99], a[220:223], a[196:199], a[96:99]\n\t"
"buffer_load_dwordx4 a[220:223], v252, s[8:11], s20 offen offset:1024 sc1\n\t"
"v_mfma_f32_16x16x32_bf16 a[96:99], v[96:99], a[200:203], a[96:99]\n\t"
"s_waitcnt vmcnt(8)\n\t"
"ds_read_b64_tr_b16 v[234:235], v246 offset:4096\n\t"
"v_mfma_f32_16x16x32_bf16 a[96:99], a[224:227], a[204:207], a[96:99]\n\t"
"v_mfma_f32_16x16x32_bf16 a[104:107], a[228:231], a[168:171], a[104:107]\n\t"
"buffer_load_dwordx4 v[96:99], v252, s[8:11], s20 offen offset:2048 sc1\n\t"
"v_mfma_f32_16x16x32_bf16 a[104:107], v[248:251], a[172:175], a[104:107]\n\t"
"s_mov_b32 s20, 1290240\n\t"
"ds_read_b64_tr_b16 v[228:229], v246 offset:7168\n\t"
"v_cvt_pk_bf16_f32 v224, v224, v225\n\t"
"v_mfma_f32_16x16x32_bf16 a[104:107], a[232:235], a[176:179], a[104:107]\n\t"
"ds_read_b64_tr_b16 v[230:231], v246 offset:10240\n\t"
"v_cvt_pk_bf16_f32 v225, v226, v227\n\t"
"v_mfma_f32_16x16x32_bf16 a[112:115], a[228:231], a[172:175], a[112:115]\n\t"
"ds_read_b128 a[168:171], v244 offset:33792\n\t"
"v_cvt_pk_bf16_f32 v226, v236, v237\n\t"
"v_mfma_f32_16x16x32_bf16 a[112:115], v[248:251], a[176:179], a[112:115]\n\t"
"ds_read_b128 a[172:175], v244 offset:34304\n\t"
"v_cvt_pk_bf16_f32 v227, v238, v239\n\t"
"v_mfma_f32_16x16x32_bf16 a[112:115], a[232:235], a[180:183], a[112:115]\n\t"
"ds_write_b128 v243, v[224:227] offset:46592\n\t"
"v_mfma_f32_16x16x32_bf16 a[120:123], a[228:231], a[176:179], a[120:123]\n\t"
"buffer_load_dwordx4 a[224:227], v252, s[8:11], s20 offen offset:0 sc1\n\t"
"v_mfma_f32_16x16x32_bf16 a[120:123], v[248:251], a[180:183], a[120:123]\n\t"
"ds_read_b64_tr_b16 v[236:237], v246 offset:13312\n\t"
"v_mfma_f32_16x16x32_bf16 a[120:123], a[232:235], a[184:187], a[120:123]\n\t"
"ds_read_b64_tr_b16 v[238:239], v246 offset:16384\n\t"
"v_mfma_f32_16x16x32_bf16 a[128:131], a[228:231], a[180:183], a[128:131]\n\t"
"s_waitcnt lgkmcnt(7)\n\t"
"ds_read_b128 a[176:179], v244 offset:34816\n\t"
"v_mfma_f32_16x16x32_bf16 a[128:131], v[248:251], a[184:187], a[128:131]\n\t"
"ds_read_b64_tr_b16 v[224:225], v246 offset:20992\n\t"
"v_mfma_f32_16x16x32_bf16 a[128:131], a[232:235], a[188:191], a[128:131]\n\t"
"ds_read_b64_tr_b16 v[226:227], v246 offset:24064\n\t"
"v_mfma_f32_16x16x32_bf16 v[232:235], v[232:235], v[220:223], 0\n\t"
"ds_read_b128 a[180:183], v244 offset:35328\n\t"
"v_mfma_f32_16x16x32_bf16 a[136:139], a[228:231], a[184:187], a[136:139]\n\t"
"s_waitcnt lgkmcnt(9)\n\t"
"ds_read_b128 a[184:187], v244 offset:35840\n\t"
"v_mfma_f32_16x16x32_bf16 a[136:139], v[248:251], a[188:191], a[136:139]\n\t"
"v_mfma_f32_16x16x32_bf16 a[136:139], a[232:235], a[192:195], a[136:139]\n\t"
"v_mfma_f32_16x16x32_bf16 v[228:231], v[228:231], v[220:223], 0\n\t"
"v_mfma_f32_16x16x32_bf16 a[144:147], a[228:231], a[188:191], a[144:147]\n\t"
"s_waitcnt lgkmcnt(5)\n\t"
"ds_read_b128 a[188:191], v244 offset:36352\n\t"
"v_cvt_pk_bf16_f32 v232, v232, v233\n\t"
"v_mfma_f32_16x16x32_bf16 a[144:147], v[248:251], a[192:195], a[144:147]\n\t"
"v_cvt_pk_bf16_f32 v233, v234, v235\n\t"
"v_mfma_f32_16x16x32_bf16 a[144:147], a[232:235], a[196:199], a[144:147]\n\t"
"v_mfma_f32_16x16x32_bf16 v[236:239], v[236:239], v[220:223], 0\n\t"
"v_mfma_f32_16x16x32_bf16 a[152:155], a[228:231], a[192:195], a[152:155]\n\t"
"s_waitcnt lgkmcnt(3)\n\t"
"ds_read_b128 a[192:195], v244 offset:36864\n\t"
"v_cvt_pk_bf16_f32 v234, v228, v229\n\t"
"v_mfma_f32_16x16x32_bf16 a[152:155], v[248:251], a[196:199], a[152:155]\n\t"
"v_cvt_pk_bf16_f32 v235, v230, v231\n\t"
"v_mfma_f32_16x16x32_bf16 a[152:155], a[232:235], a[200:203], a[152:155]\n\t"
"ds_read_b64_tr_b16 v[228:229], v246 offset:27136\n\t"
"v_mfma_f32_16x16x32_bf16 v[224:227], v[224:227], v[220:223], 0\n\t"
"ds_read_b64_tr_b16 v[230:231], v246 offset:30208\n\t"
"v_mfma_f32_16x16x32_bf16 a[160:163], a[228:231], a[196:199], a[160:163]\n\t"
"buffer_load_dwordx4 a[228:231], v252, s[8:11], s20 offen offset:1024 sc1\n\t"
"v_mfma_f32_16x16x32_bf16 a[160:163], v[248:251], a[200:203], a[160:163]\n\t"
"s_waitcnt vmcnt(8)\n\t"
"ds_read_b128 a[196:199], v244 offset:37376\n\t"
"v_cvt_pk_bf16_f32 v236, v236, v237\n\t"
"v_mfma_f32_16x16x32_bf16 a[160:163], a[232:235], a[204:207], a[160:163]\n\t"
"ds_read_b128 a[200:203], v244 offset:37888\n\t"
"v_cvt_pk_bf16_f32 v237, v238, v239\n\t"
"v_mfma_f32_16x16x32_bf16 v[200:203], a[236:239], a[168:171], v[200:203]\n\t"
"ds_read_b128 a[204:207], v244 offset:38400\n\t"
"v_mfma_f32_16x16x32_bf16 v[200:203], a[212:215], a[172:175], v[200:203]\n\t"
"buffer_load_dwordx4 v[248:251], v252, s[8:11], s20 offen offset:2048 sc1\n\t"
"v_mfma_f32_16x16x32_bf16 v[200:203], a[216:219], a[176:179], v[200:203]\n\t"
"s_waitcnt lgkmcnt(3)\n\t"
"s_mov_b32 s20, 1302528\n\t"
"v_cvt_pk_bf16_f32 v238, v224, v225\n\t"
"v_mfma_f32_16x16x32_bf16 v[228:231], v[228:231], v[220:223], 0\n\t"
"v_cvt_pk_bf16_f32 v239, v226, v227\n\t"
"v_mfma_f32_16x16x32_bf16 v[208:211], a[236:239], a[172:175], v[208:211]\n\t"
"buffer_load_dwordx4 a[232:235], v252, s[8:11], s20 offen offset:0 sc1\n\t"
"v_mfma_f32_16x16x32_bf16 v[208:211], a[212:215], a[176:179], v[208:211]\n\t"
"v_mfma_f32_16x16x32_bf16 v[208:211], a[216:219], a[180:183], v[208:211]\n\t"
"s_waitcnt lgkmcnt(0)\n\t"
"s_barrier\n\t"
"v_mfma_f32_16x16x32_bf16 v[216:219], a[236:239], a[176:179], v[216:219]\n\t"
"ds_read_b64_tr_b16 v[224:225], v246 offset:33280\n\t"
"v_mfma_f32_16x16x32_bf16 v[216:219], a[212:215], a[180:183], v[216:219]\n\t"
"ds_read_b64_tr_b16 v[226:227], v246 offset:36352\n\t"
"v_cvt_pk_bf16_f32 v228, v228, v229\n\t"
"v_mfma_f32_16x16x32_bf16 v[216:219], a[216:219], a[184:187], v[216:219]\n\t"
"ds_write2st64_b64 v245, v[232:233], v[234:235] offset0:48 offset1:49\n\t"
"v_mfma_f32_16x16x32_bf16 a[4:7], a[236:239], a[180:183], a[4:7]\n\t"
"v_cvt_pk_bf16_f32 v229, v230, v231\n\t"
"v_mfma_f32_16x16x32_bf16 a[4:7], a[212:215], a[184:187], a[4:7]\n\t"
"ds_read_b64_tr_b16 v[232:233], v246 offset:40960\n\t"
"v_mfma_f32_16x16x32_bf16 a[4:7], a[216:219], a[188:191], a[4:7]\n\t"
"ds_write2st64_b64 v245, v[236:237], v[238:239] offset0:50 offset1:51\n\t"
"v_mfma_f32_16x16x32_bf16 a[12:15], a[236:239], a[184:187], a[12:15]\n\t"
"v_mfma_f32_16x16x32_bf16 a[12:15], a[212:215], a[188:191], a[12:15]\n\t"
"ds_read_b64_tr_b16 v[234:235], v246 offset:44032\n\t"
"v_mfma_f32_16x16x32_bf16 a[12:15], a[216:219], a[192:195], a[12:15]\n\t"
"ds_write_b64 v245, v[228:229] offset:26624\n\t"
"v_mfma_f32_16x16x32_bf16 a[20:23], a[236:239], a[188:191], a[20:23]\n\t"
"s_waitcnt lgkmcnt(5)\n\t"
"ds_read_b64_tr_b16 v[228:229], v246 offset:47104\n\t"
"v_mfma_f32_16x16x32_bf16 a[20:23], a[212:215], a[192:195], a[20:23]\n\t"
"ds_read_b64_tr_b16 v[230:231], v246 offset:50176\n\t"
"v_mfma_f32_16x16x32_bf16 a[20:23], a[216:219], a[196:199], a[20:23]\n\t"
"v_mfma_f32_16x16x32_bf16 v[224:227], v[224:227], v[220:223], 0\n\t"
"v_mfma_f32_16x16x32_bf16 a[28:31], a[236:239], a[192:195], a[28:31]\n\t"
"s_waitcnt lgkmcnt(3)\n\t"
"v_mfma_f32_16x16x32_bf16 a[28:31], a[212:215], a[196:199], a[28:31]\n\t"
"v_mfma_f32_16x16x32_bf16 a[28:31], a[216:219], a[200:203], a[28:31]\n\t"
"s_waitcnt vmcnt(7) lgkmcnt(0)\n\t"
"v_mfma_f32_16x16x32_bf16 v[232:235], v[232:235], v[220:223], 0\n\t"
"v_mfma_f32_16x16x32_bf16 a[36:39], a[236:239], a[196:199], a[36:39]\n\t"
"buffer_load_dwordx4 a[236:239], v252, s[8:11], s20 offen offset:1024 sc1\n\t"
"v_mfma_f32_16x16x32_bf16 a[36:39], a[212:215], a[200:203], a[36:39]\n\t"
"Lpg20_%=:\n\t"
"s_cbranch_vccnz Lvr2_%=\n\t"
"v_cvt_pk_bf16_f32 v224, v224, v225\n\t"
"v_cvt_pk_bf16_f32 v225, v226, v227\n\t"
"v_mfma_f32_16x16x32_bf16 a[36:39], a[216:219], a[204:207], a[36:39]\n\t"
"v_mfma_f32_16x16x32_bf16 v[228:231], v[228:231], v[220:223], 0\n\t"
"buffer_load_dwordx4 a[212:215], v252, s[8:11], s20 offen offset:2048 sc1\n\t"
"v_mfma_f32_16x16x32_bf16 a[44:47], a[240:243], a[168:171], a[44:47]\n\t"
"s_mov_b32 s20, 1314816\n\t"
"v_cvt_pk_bf16_f32 v226, v232, v233\n\t"
"v_cvt_pk_bf16_f32 v227, v234, v235\n\t"
"v_mfma_f32_16x16x32_bf16 a[44:47], a[244:247], a[172:175], a[44:47]\n\t"
"ds_read_b64_tr_b16 v[232:233], v246 offset:53248\n\t"
"v_mfma_f32_16x16x32_bf16 a[44:47], a[248:251], a[176:179], a[44:47]\n\t"
"buffer_load_dwordx4 a[216:219], v252, s[8:11], s20 offen offset:0 sc1\n\t"
"v_mfma_f32_16x16x32_bf16 a[52:55], a[240:243], a[172:175], a[52:55]\n\t"
"ds_read_b64_tr_b16 v[234:235], v246 offset:56320\n\t"
"v_mfma_f32_16x16x32_bf16 a[52:55], a[244:247], a[176:179], a[52:55]\n\t"
"v_cvt_pk_bf16_f32 v228, v228, v229\n\t"
"v_cvt_pk_bf16_f32 v229, v230, v231\n\t"
"v_mfma_f32_16x16x32_bf16 a[52:55], a[248:251], a[180:183], a[52:55]\n\t"
"ds_write2st64_b64 v245, v[224:225], v[226:227] offset0:53 offset1:54\n\t"
"v_mfma_f32_16x16x32_bf16 a[60:63], a[240:243], a[176:179], a[60:63]\n\t"
"v_mfma_f32_16x16x32_bf16 a[60:63], a[244:247], a[180:183], a[60:63]\n\t"
"v_add_u32 v243, 55296, v243\n\t"
"v_mfma_f32_16x16x32_bf16 a[60:63], a[248:251], a[184:187], a[60:63]\n\t"
"v_mfma_f32_16x16x32_bf16 a[68:71], a[240:243], a[180:183], a[68:71]\n\t"
"v_mfma_f32_16x16x32_bf16 a[68:71], a[244:247], a[184:187], a[68:71]\n\t"
"v_mfma_f32_16x16x32_bf16 a[68:71], a[248:251], a[188:191], a[68:71]\n\t"
"v_mfma_f32_16x16x32_bf16 a[76:79], a[240:243], a[184:187], a[76:79]\n\t"
"s_waitcnt lgkmcnt(1)\n\t"
"v_mfma_f32_16x16x32_bf16 a[76:79], a[244:247], a[188:191], a[76:79]\n\t"
"v_mfma_f32_16x16x32_bf16 a[76:79], a[248:251], a[192:195], a[76:79]\n\t"
"v_mfma_f32_16x16x32_bf16 v[232:235], v[232:235], v[220:223], 0\n\t"
"v_mfma_f32_16x16x32_bf16 a[84:87], a[240:243], a[188:191], a[84:87]\n\t"
"v_mfma_f32_16x16x32_bf16 a[84:87], a[244:247], a[192:195], a[84:87]\n\t"
"v_mfma_f32_16x16x32_bf16 a[84:87], a[248:251], a[196:199], a[84:87]\n\t"
"v_mfma_f32_16x16x32_bf16 a[92:95], a[240:243], a[192:195], a[92:95]\n\t"
"v_mfma_f32_16x16x32_bf16 a[92:95], a[244:247], a[196:199], a[92:95]\n\t"
"v_cvt_pk_bf16_f32 v230, v232, v233\n\t"
"v_cvt_pk_bf16_f32 v231, v234, v235\n\t"
"v_mfma_f32_16x16x32_bf16 a[92:95], a[248:251], a[200:203], a[92:95]\n\t"
"ds_write2st64_b64 v245, v[228:229], v[230:231] offset0:55 offset1:56\n\t"
"v_mfma_f32_16x16x32_bf16 a[100:103], a[240:243], a[196:199], a[100:103]\n\t"
"buffer_load_dwordx4 a[240:243], v252, s[8:11], s20 offen offset:1024 sc1\n\t"
"v_mfma_f32_16x16x32_bf16 a[100:103], a[244:247], a[200:203], a[100:103]\n\t"
"s_waitcnt vmcnt(8)\n\t"
"v_mfma_f32_16x16x32_bf16 a[100:103], a[248:251], a[204:207], a[100:103]\n\t"
"v_mfma_f32_16x16x32_bf16 a[108:111], a[252:255], a[168:171], a[108:111]\n\t"
"buffer_load_dwordx4 a[244:247], v252, s[8:11], s20 offen offset:2048 sc1\n\t"
"v_mfma_f32_16x16x32_bf16 a[108:111], a[220:223], a[172:175], a[108:111]\n\t"
"s_mov_b32 s20, 1327104\n\t"
"v_mfma_f32_16x16x32_bf16 a[108:111], v[96:99], a[176:179], a[108:111]\n\t"
"s_waitcnt lgkmcnt(0)\n\t"
"s_barrier\n\t"
"v_mfma_f32_16x16x32_bf16 a[116:119], a[252:255], a[172:175], a[116:119]\n\t"
"ds_read_b64_tr_b16 v[236:237], v240 offset:1024\n\t"
"v_mfma_f32_16x16x32_bf16 a[116:119], a[220:223], a[176:179], a[116:119]\n\t"
"ds_read_b64_tr_b16 v[238:239], v240 offset:2560\n\t"
"v_mfma_f32_16x16x32_bf16 a[116:119], v[96:99], a[180:183], a[116:119]\n\t"
"ds_read_b64_tr_b16 v[224:225], v241 offset:1024\n\t"
"v_mfma_f32_16x16x32_bf16 a[124:127], a[252:255], a[176:179], a[124:127]\n\t"
"ds_read_b64_tr_b16 v[226:227], v241 offset:2560\n\t"
"v_mfma_f32_16x16x32_bf16 a[124:127], a[220:223], a[180:183], a[124:127]\n\t"
"buffer_load_dwordx4 a[248:251], v252, s[8:11], s20 offen offset:0 sc1\n\t"
"v_mfma_f32_16x16x32_bf16 a[124:127], v[96:99], a[184:187], a[124:127]\n\t"
"ds_read_b64_tr_b16 v[232:233], v240 offset:20992\n\t"
"v_mfma_f32_16x16x32_bf16 a[132:135], a[252:255], a[180:183], a[132:135]\n\t"
"s_waitcnt lgkmcnt(3)\n\t"
"ds_read_b64_tr_b16 v[234:235], v240 offset:22528\n\t"
"v_mfma_f32_16x16x32_bf16 a[132:135], a[220:223], a[184:187], a[132:135]\n\t"
"ds_read_b64_tr_b16 v[228:229], v241 offset:20992\n\t"
"v_mfma_f32_16x16x32_bf16 a[132:135], v[96:99], a[188:191], a[132:135]\n\t"
"s_waitcnt lgkmcnt(3)\n\t"
"ds_read_b64_tr_b16 v[230:231], v241 offset:22528\n\t"
"v_mfma_f32_16x16x32_bf16 v[236:239], v[236:239], a[208:211], 0\n\t"
"ds_read_b128 a[168:171], v244 offset:24576\n\t"
"v_mfma_f32_16x16x32_bf16 v[224:227], v[224:227], a[208:211], 0\n\t"
"ds_read_b128 a[172:175], v244 offset:25088\n\t"
"v_mfma_f32_16x16x32_bf16 a[140:143], a[252:255], a[184:187], a[140:143]\n\t"
"ds_read_b128 a[176:179], v244 offset:25600\n\t"
"v_mfma_f32_16x16x32_bf16 a[140:143], a[220:223], a[188:191], a[140:143]\n\t"
"ds_read_b128 a[180:183], v244 offset:26112\n\t"
"v_mfma_f32_16x16x32_bf16 a[140:143], v[96:99], a[192:195], a[140:143]\n\t"
"ds_read_b128 a[184:187], v244 offset:26624\n\t"
"v_mfma_f32_16x16x32_bf16 a[148:151], a[252:255], a[188:191], a[148:151]\n\t"
"s_waitcnt lgkmcnt(7)\n\t"
"ds_read_b128 a[188:191], v244 offset:27136\n\t"
"v_cvt_pk_bf16_f32 v236, v236, v237\n\t"
"v_mfma_f32_16x16x32_bf16 a[148:151], a[220:223], a[192:195], a[148:151]\n\t"
"s_waitcnt lgkmcnt(6)\n\t"
"v_cvt_pk_bf16_f32 v237, v238, v239\n\t"
"v_cvt_pk_bf16_f32 v238, v224, v225\n\t"
"v_mfma_f32_16x16x32_bf16 a[148:151], v[96:99], a[196:199], a[148:151]\n\t"
"v_cvt_pk_bf16_f32 v239, v226, v227\n\t"
"v_mfma_f32_16x16x32_bf16 v[232:235], v[232:235], a[208:211], 0\n\t"
"ds_write_b128 v243, v[236:239] offset:28160\n\t"
"v_mfma_f32_16x16x32_bf16 v[228:231], v[228:231], a[208:211], 0\n\t"
"v_mfma_f32_16x16x32_bf16 a[156:159], a[252:255], a[192:195], a[156:159]\n\t"
"ds_read_b128 a[192:195], v244 offset:27648\n\t"
"v_mfma_f32_16x16x32_bf16 a[156:159], a[220:223], a[196:199], a[156:159]\n\t"
"ds_read_b64_tr_b16 v[224:225], v240 offset:40960\n\t"
"v_mfma_f32_16x16x32_bf16 a[156:159], v[96:99], a[200:203], a[156:159]\n\t"
"ds_read_b64_tr_b16 v[226:227], v240 offset:42496\n\t"
"v_mfma_f32_16x16x32_bf16 a[164:167], a[252:255], a[196:199], a[164:167]\n\t"
"buffer_load_dwordx4 a[252:255], v252, s[8:11], s20 offen offset:1024 sc1\n\t"
"v_mfma_f32_16x16x32_bf16 a[164:167], a[220:223], a[200:203], a[164:167]\n\t"
"s_waitcnt vmcnt(8) lgkmcnt(7)\n\t"
"ds_read_b64_tr_b16 v[236:237], v241 offset:40960\n\t"
"v_cvt_pk_bf16_f32 v232, v232, v233\n\t"
"v_mfma_f32_16x16x32_bf16 a[164:167], v[96:99], a[204:207], a[164:167]\n\t"
"ds_read_b64_tr_b16 v[238:239], v241 offset:42496\n\t"
"v_cvt_pk_bf16_f32 v233, v234, v235\n\t"
"Lst28_%=:\n\t"
"v_mfma_f32_16x16x32_bf16 v[196:199], a[224:227], a[168:171], v[196:199]\n\t"
"s_waitcnt lgkmcnt(8)\n\t"
"ds_read_b128 a[196:199], v244 offset:28160\n\t"
"v_cvt_pk_bf16_f32 v234, v228, v229\n\t"
"v_mfma_f32_16x16x32_bf16 v[196:199], a[228:231], a[172:175], v[196:199]\n\t"
"ds_read_b128 a[200:203], v244 offset:28672\n\t"
"v_cvt_pk_bf16_f32 v235, v230, v231\n\t"
"v_mfma_f32_16x16x32_bf16 v[196:199], v[248:251], a[176:179], v[196:199]\n\t"
"ds_write_b128 v243, v[232:235] offset:37376\n\t"
"v_mfma_f32_16x16x32_bf16 v[204:207], a[224:227], a[172:175], v[204:207]\n\t"
"buffer_load_dwordx4 v[96:99], v252, s[8:11], s20 offen offset:2048 sc1\n\t"
"v_mfma_f32_16x16x32_bf16 v[204:207], a[228:231], a[176:179], v[204:207]\n\t"
"s_waitcnt lgkmcnt(10)\n\t"
"ds_read_b128 a[204:207], v244 offset:29184\n\t"
"v_mfma_f32_16x16x32_bf16 v[204:207], v[248:251], a[180:183], v[204:207]\n\t"
"s_mov_b32 s20, 1339392\n\t"
"v_subrev_u32 v240, 59904, v240\n\t"
"v_subrev_u32 v241, 59904, v241\n\t"
"v_mfma_f32_16x16x32_bf16 v[212:215], a[224:227], a[176:179], v[212:215]\n\t"
"s_waitcnt lgkmcnt(6)\n\t"
"ds_read_b64_tr_b16 v[228:229], v240 offset:1024\n\t"
"v_mfma_f32_16x16x32_bf16 v[212:215], a[228:231], a[180:183], v[212:215]\n\t"
"buffer_load_dwordx4 a[220:223], v252, s[8:11], s20 offen offset:0 sc1\n\t"
"v_mfma_f32_16x16x32_bf16 v[212:215], v[248:251], a[184:187], v[212:215]\n\t"
"s_waitcnt lgkmcnt(5)\n\t"
"ds_read_b64_tr_b16 v[230:231], v240 offset:2560\n\t"
"v_mfma_f32_16x16x32_bf16 v[224:227], v[224:227], a[208:211], 0\n\t"
"ds_read_b64_tr_b16 v[232:233], v241 offset:1024\n\t"
"v_mfma_f32_16x16x32_bf16 v[236:239], v[236:239], a[208:211], 0\n\t"
"ds_read_b64_tr_b16 v[234:235], v241 offset:2560\n\t"
"v_mfma_f32_16x16x32_bf16 a[0:3], a[224:227], a[180:183], a[0:3]\n\t"
"v_mfma_f32_16x16x32_bf16 a[0:3], a[228:231], a[184:187], a[0:3]\n\t"
"v_mfma_f32_16x16x32_bf16 a[0:3], v[248:251], a[188:191], a[0:3]\n\t"
"v_mfma_f32_16x16x32_bf16 a[8:11], a[224:227], a[184:187], a[8:11]\n\t"
"s_waitcnt lgkmcnt(2)\n\t"
"v_cvt_pk_bf16_f32 v224, v224, v225\n\t"
"v_cvt_pk_bf16_f32 v225, v226, v227\n\t"
"v_mfma_f32_16x16x32_bf16 a[8:11], a[228:231], a[188:191], a[8:11]\n\t"
"s_waitcnt lgkmcnt(0)\n\t"
"v_cvt_pk_bf16_f32 v226, v236, v237\n\t"
"v_cvt_pk_bf16_f32 v227, v238, v239\n\t"
"v_mfma_f32_16x16x32_bf16 a[8:11], v[248:251], a[192:195], a[8:11]\n\t"
"ds_write_b128 v243, v[224:227] offset:46592\n\t"
"v_mfma_f32_16x16x32_bf16 v[228:231], v[228:231], a[208:211], 0\n\t"
"v_mfma_f32_16x16x32_bf16 v[232:235], v[232:235], a[208:211], 0\n\t"
"ds_read_b64_tr_b16 v[236:237], v240 offset:20992\n\t"
"v_mfma_f32_16x16x32_bf16 a[16:19], a[224:227], a[188:191], a[16:19]\n\t"
"ds_read_b64_tr_b16 v[238:239], v240 offset:22528\n\t"
"v_mfma_f32_16x16x32_bf16 a[16:19], a[228:231], a[192:195], a[16:19]\n\t"
"ds_read_b64_tr_b16 v[224:225], v241 offset:20992\n\t"
"v_mfma_f32_16x16x32_bf16 a[16:19], v[248:251], a[196:199], a[16:19]\n\t"
"ds_read_b64_tr_b16 v[226:227], v241 offset:22528\n\t"
"v_mfma_f32_16x16x32_bf16 a[24:27], a[224:227], a[192:195], a[24:27]\n\t"
"v_cvt_pk_bf16_f32 v228, v228, v229\n\t"
"v_cvt_pk_bf16_f32 v229, v230, v231\n\t"
"v_mfma_f32_16x16x32_bf16 a[24:27], a[228:231], a[196:199], a[24:27]\n\t"
"v_cvt_pk_bf16_f32 v230, v232, v233\n\t"
"v_cvt_pk_bf16_f32 v231, v234, v235\n\t"
"v_mfma_f32_16x16x32_bf16 a[24:27], v[248:251], a[200:203], a[24:27]\n\t"
"ds_write_b128 v243, v[228:231] offset:512\n\t"
"v_mfma_f32_16x16x32_bf16 a[32:35], a[224:227], a[196:199], a[32:35]\n\t"
"buffer_load_dwordx4 a[224:227], v252, s[8:11], s20 offen offset:1024 sc1\n\t"
"v_mfma_f32_16x16x32_bf16 a[32:35], a[228:231], a[200:203], a[32:35]\n\t"
"s_waitcnt vmcnt(8)\n\t"
"ds_read_b64_tr_b16 v[232:233], v240 offset:40960\n\t"
"v_mfma_f32_16x16x32_bf16 a[32:35], v[248:251], a[204:207], a[32:35]\n\t"
"ds_read_b64_tr_b16 v[234:235], v240 offset:42496\n\t"
"v_mfma_f32_16x16x32_bf16 a[40:43], a[232:235], a[168:171], a[40:43]\n\t"
"buffer_load_dwordx4 v[248:251], v252, s[8:11], s20 offen offset:2048 sc1\n\t"
"v_mfma_f32_16x16x32_bf16 a[40:43], a[236:239], a[172:175], a[40:43]\n\t"
"s_mov_b32 s20, 1351680\n\t"
"ds_read_b64_tr_b16 v[228:229], v241 offset:40960\n\t"
"v_mfma_f32_16x16x32_bf16 a[40:43], a[212:215], a[176:179], a[40:43]\n\t"
"ds_read_b64_tr_b16 v[230:231], v241 offset:42496\n\t"
"v_mfma_f32_16x16x32_bf16 a[48:51], a[232:235], a[172:175], a[48:51]\n\t"
"buffer_load_dwordx4 a[228:231], v252, s[8:11], s20 offen offset:0 sc1\n\t"
"v_mfma_f32_16x16x32_bf16 a[48:51], a[236:239], a[176:179], a[48:51]\n\t"
"s_waitcnt lgkmcnt(5)\n\t"
"v_mfma_f32_16x16x32_bf16 a[48:51], a[212:215], a[180:183], a[48:51]\n\t"
"v_mfma_f32_16x16x32_bf16 v[236:239], v[236:239], a[208:211], 0\n\t"
"v_mfma_f32_16x16x32_bf16 v[224:227], v[224:227], a[208:211], 0\n\t"
"v_xor_b32 v246, 0xc0, v246\n\t"
"v_mfma_f32_16x16x32_bf16 a[56:59], a[232:235], a[176:179], a[56:59]\n\t"
"v_mfma_f32_16x16x32_bf16 a[56:59], a[236:239], a[180:183], a[56:59]\n\t"
"v_mfma_f32_16x16x32_bf16 a[56:59], a[212:215], a[184:187], a[56:59]\n\t"
"v_mfma_f32_16x16x32_bf16 a[64:67], a[232:235], a[180:183], a[64:67]\n\t"
"s_waitcnt lgkmcnt(2)\n\t"
"v_cvt_pk_bf16_f32 v236, v236, v237\n\t"
"v_cvt_pk_bf16_f32 v237, v238, v239\n\t"
"v_mfma_f32_16x16x32_bf16 a[64:67], a[236:239], a[184:187], a[64:67]\n\t"
"s_waitcnt lgkmcnt(0)\n\t"
"v_cvt_pk_bf16_f32 v238, v224, v225\n\t"
"v_cvt_pk_bf16_f32 v239, v226, v227\n\t"
"v_mfma_f32_16x16x32_bf16 a[64:67], a[212:215], a[188:191], a[64:67]\n\t"
"ds_write_b128 v243, v[236:239] offset:9728\n\t"
"v_mfma_f32_16x16x32_bf16 v[232:235], v[232:235], a[208:211], 0\n\t"
"v_mfma_f32_16x16x32_bf16 v[228:231], v[228:231], a[208:211], 0\n\t"
"v_mfma_f32_16x16x32_bf16 a[72:75], a[232:235], a[184:187], a[72:75]\n\t"
"v_mfma_f32_16x16x32_bf16 a[72:75], a[236:239], a[188:191], a[72:75]\n\t"
"v_mfma_f32_16x16x32_bf16 a[72:75], a[212:215], a[192:195], a[72:75]\n\t"
"v_mfma_f32_16x16x32_bf16 a[80:83], a[232:235], a[188:191], a[80:83]\n\t"
"v_cvt_pk_bf16_f32 v232, v232, v233\n\t"
"v_cvt_pk_bf16_f32 v233, v234, v235\n\t"
"v_mfma_f32_16x16x32_bf16 a[80:83], a[236:239], a[192:195], a[80:83]\n\t"
"v_cvt_pk_bf16_f32 v234, v228, v229\n\t"
"v_cvt_pk_bf16_f32 v235, v230, v231\n\t"
"v_mfma_f32_16x16x32_bf16 a[80:83], a[212:215], a[196:199], a[80:83]\n\t"
"ds_write_b128 v243, v[232:235] offset:18944\n\t"
"v_mfma_f32_16x16x32_bf16 a[88:91], a[232:235], a[192:195], a[88:91]\n\t"
"v_mfma_f32_16x16x32_bf16 a[88:91], a[236:239], a[196:199], a[88:91]\n\t"
"ds_read_b64_tr_b16 v[224:225], v246 offset:1024\n\t"
"v_mfma_f32_16x16x32_bf16 a[88:91], a[212:215], a[200:203], a[88:91]\n\t"
"ds_read_b64_tr_b16 v[226:227], v246 offset:4096\n\t"
"v_mfma_f32_16x16x32_bf16 a[96:99], a[232:235], a[196:199], a[96:99]\n\t"
"buffer_load_dwordx4 a[232:235], v252, s[8:11], s20 offen offset:1024 sc1\n\t"
"v_mfma_f32_16x16x32_bf16 a[96:99], a[236:239], a[200:203], a[96:99]\n\t"
"s_waitcnt vmcnt(8)\n\t"
"ds_read_b64_tr_b16 v[236:237], v246 offset:7168\n\t"
"v_mfma_f32_16x16x32_bf16 a[96:99], a[212:215], a[204:207], a[96:99]\n\t"
"ds_read_b64_tr_b16 v[238:239], v246 offset:10240\n\t"
"v_mfma_f32_16x16x32_bf16 a[104:107], a[216:219], a[168:171], a[104:107]\n\t"
"buffer_load_dwordx4 a[212:215], v252, s[8:11], s20 offen offset:2048 sc1\n\t"
"v_mfma_f32_16x16x32_bf16 a[104:107], a[240:243], a[172:175], a[104:107]\n\t"
"s_mov_b32 s20, 1216512\n\t"
"ds_read_b64_tr_b16 v[228:229], v246 offset:13312\n\t"
"v_mfma_f32_16x16x32_bf16 a[104:107], a[244:247], a[176:179], a[104:107]\n\t"
"ds_read_b64_tr_b16 v[230:231], v246 offset:16384\n\t"
"v_mfma_f32_16x16x32_bf16 a[112:115], a[216:219], a[172:175], a[112:115]\n\t"
"buffer_load_dwordx4 a[236:239], v252, s[8:11], s20 offen offset:0 sc1\n\t"
"v_mfma_f32_16x16x32_bf16 a[112:115], a[240:243], a[176:179], a[112:115]\n\t"
"ds_read_b128 a[168:171], v244 offset:33792\n\t"
"v_mfma_f32_16x16x32_bf16 a[112:115], a[244:247], a[180:183], a[112:115]\n\t"
"ds_read_b64_tr_b16 v[232:233], v246 offset:20992\n\t"
"v_mfma_f32_16x16x32_bf16 a[120:123], a[216:219], a[176:179], a[120:123]\n\t"
"s_waitcnt lgkmcnt(6)\n\t"
"ds_read_b64_tr_b16 v[234:235], v246 offset:24064\n\t"
"v_mfma_f32_16x16x32_bf16 a[120:123], a[240:243], a[180:183], a[120:123]\n\t"
"ds_read_b128 a[172:175], v244 offset:34304\n\t"
"v_mfma_f32_16x16x32_bf16 a[120:123], a[244:247], a[184:187], a[120:123]\n\t"
"ds_read_b128 a[176:179], v244 offset:34816\n\t"
"v_mfma_f32_16x16x32_bf16 v[224:227], v[224:227], v[220:223], 0\n\t"
"v_mfma_f32_16x16x32_bf16 a[128:131], a[216:219], a[180:183], a[128:131]\n\t"
"s_waitcnt lgkmcnt(7)\n\t"
"ds_read_b128 a[180:183], v244 offset:35328\n\t"
"v_mfma_f32_16x16x32_bf16 a[128:131], a[240:243], a[184:187], a[128:131]\n\t"
"v_mfma_f32_16x16x32_bf16 a[128:131], a[244:247], a[188:191], a[128:131]\n\t"
"v_mfma_f32_16x16x32_bf16 v[236:239], v[236:239], v[220:223], 0\n\t"
"v_mfma_f32_16x16x32_bf16 a[136:139], a[216:219], a[184:187], a[136:139]\n\t"
"s_waitcnt lgkmcnt(6)\n\t"
"ds_read_b128 a[184:187], v244 offset:35840\n\t"
"v_cvt_pk_bf16_f32 v224, v224, v225\n\t"
"v_mfma_f32_16x16x32_bf16 a[136:139], a[240:243], a[188:191], a[136:139]\n\t"
"v_cvt_pk_bf16_f32 v225, v226, v227\n\t"
"v_mfma_f32_16x16x32_bf16 a[136:139], a[244:247], a[192:195], a[136:139]\n\t"
"v_mfma_f32_16x16x32_bf16 v[228:231], v[228:231], v[220:223], 0\n\t"
"v_mfma_f32_16x16x32_bf16 a[144:147], a[216:219], a[188:191], a[144:147]\n\t"
"s_waitcnt lgkmcnt(4)\n\t"
"ds_read_b128 a[188:191], v244 offset:36352\n\t"
"v_cvt_pk_bf16_f32 v226, v236, v237\n\t"
"v_mfma_f32_16x16x32_bf16 a[144:147], a[240:243], a[192:195], a[144:147]\n\t"
"v_cvt_pk_bf16_f32 v227, v238, v239\n\t"
"v_mfma_f32_16x16x32_bf16 a[144:147], a[244:247], a[196:199], a[144:147]\n\t"
"v_mfma_f32_16x16x32_bf16 v[232:235], v[232:235], v[220:223], 0\n\t"
"v_mfma_f32_16x16x32_bf16 a[152:155], a[216:219], a[192:195], a[152:155]\n\t"
"ds_read_b128 a[192:195], v244 offset:36864\n\t"
"v_cvt_pk_bf16_f32 v228, v228, v229\n\t"
"v_mfma_f32_16x16x32_bf16 a[152:155], a[240:243], a[196:199], a[152:155]\n\t"
"v_cvt_pk_bf16_f32 v229, v230, v231\n\t"
"v_mfma_f32_16x16x32_bf16 a[152:155], a[244:247], a[200:203], a[152:155]\n\t"
"v_mfma_f32_16x16x32_bf16 a[160:163], a[216:219], a[196:199], a[160:163]\n\t"
"buffer_load_dwordx4 a[216:219], v252, s[8:11], s20 offen offset:1024 sc1\n\t"
"v_mfma_f32_16x16x32_bf16 a[160:163], a[240:243], a[200:203], a[160:163]\n\t"
"s_waitcnt vmcnt(8) lgkmcnt(4)\n\t"
"ds_read_b128 a[196:199], v244 offset:37376\n\t"
"v_cvt_pk_bf16_f32 v230, v232, v233\n\t"
"v_mfma_f32_16x16x32_bf16 a[160:163], a[244:247], a[204:207], a[160:163]\n\t"
"ds_read_b128 a[200:203], v244 offset:37888\n\t"
"v_cvt_pk_bf16_f32 v231, v234, v235\n\t"
"v_mfma_f32_16x16x32_bf16 v[200:203], a[248:251], a[168:171], v[200:203]\n\t"
"ds_read_b64_tr_b16 v[232:233], v246 offset:27136\n\t"
"v_mfma_f32_16x16x32_bf16 v[200:203], a[252:255], a[172:175], v[200:203]\n\t"
"s_waitcnt lgkmcnt(6)\n\t"
"ds_read_b64_tr_b16 v[234:235], v246 offset:30208\n\t"
"v_mfma_f32_16x16x32_bf16 v[200:203], v[96:99], a[176:179], v[200:203]\n\t"
"s_waitcnt lgkmcnt(0)\n\t"
"ds_read_b128 a[204:207], v244 offset:38400\n\t"
"v_mfma_f32_16x16x32_bf16 v[232:235], v[232:235], v[220:223], 0\n\t"
"buffer_load_dwordx4 a[240:243], v252, s[8:11], s20 offen offset:2048 sc1\n\t"
"s_mov_b32 s20, 1228800\n\t"
"v_mfma_f32_16x16x32_bf16 v[208:211], a[248:251], a[172:175], v[208:211]\n\t"
"buffer_load_dwordx4 a[244:247], v252, s[8:11], s20 offen offset:0 sc1\n\t"
"v_mfma_f32_16x16x32_bf16 v[208:211], a[252:255], a[176:179], v[208:211]\n\t"
"v_mfma_f32_16x16x32_bf16 v[208:211], v[96:99], a[180:183], v[208:211]\n\t"
"s_waitcnt lgkmcnt(0)\n\t"
"s_barrier\n\t"
"v_mfma_f32_16x16x32_bf16 v[216:219], a[248:251], a[176:179], v[216:219]\n\t"
"v_mfma_f32_16x16x32_bf16 v[216:219], a[252:255], a[180:183], v[216:219]\n\t"
"v_cvt_pk_bf16_f32 v232, v232, v233\n\t"
"v_cvt_pk_bf16_f32 v233, v234, v235\n\t"
"v_mfma_f32_16x16x32_bf16 v[216:219], v[96:99], a[184:187], v[216:219]\n\t"
"ds_write2st64_b64 v245, v[224:225], v[226:227] offset0:48 offset1:49\n\t"
"v_mfma_f32_16x16x32_bf16 a[4:7], a[248:251], a[180:183], a[4:7]\n\t"
"v_mfma_f32_16x16x32_bf16 a[4:7], a[252:255], a[184:187], a[4:7]\n\t"
"ds_read_b64_tr_b16 v[224:225], v246 offset:33280\n\t"
"v_mfma_f32_16x16x32_bf16 a[4:7], v[96:99], a[188:191], a[4:7]\n\t"
"ds_write2st64_b64 v245, v[228:229], v[230:231] offset0:50 offset1:51\n\t"
"v_mfma_f32_16x16x32_bf16 a[12:15], a[248:251], a[184:187], a[12:15]\n\t"
"v_mfma_f32_16x16x32_bf16 a[12:15], a[252:255], a[188:191], a[12:15]\n\t"
"ds_read_b64_tr_b16 v[226:227], v246 offset:36352\n\t"
"v_mfma_f32_16x16x32_bf16 a[12:15], v[96:99], a[192:195], a[12:15]\n\t"
"ds_write_b64 v245, v[232:233] offset:26624\n\t"
"v_mfma_f32_16x16x32_bf16 a[20:23], a[248:251], a[188:191], a[20:23]\n\t"
"ds_read_b64_tr_b16 v[228:229], v246 offset:40960\n\t"
"v_mfma_f32_16x16x32_bf16 a[20:23], a[252:255], a[192:195], a[20:23]\n\t"
"ds_read_b64_tr_b16 v[230:231], v246 offset:44032\n\t"
"v_mfma_f32_16x16x32_bf16 a[20:23], v[96:99], a[196:199], a[20:23]\n\t"
"s_waitcnt lgkmcnt(3)\n\t"
"ds_read_b64_tr_b16 v[232:233], v246 offset:47104\n\t"
"v_mfma_f32_16x16x32_bf16 v[224:227], v[224:227], v[220:223], 0\n\t"
"ds_read_b64_tr_b16 v[234:235], v246 offset:50176\n\t"
"v_mfma_f32_16x16x32_bf16 a[28:31], a[248:251], a[192:195], a[28:31]\n\t"
"v_mfma_f32_16x16x32_bf16 a[28:31], a[252:255], a[196:199], a[28:31]\n\t"
"v_mfma_f32_16x16x32_bf16 a[28:31], v[96:99], a[200:203], a[28:31]\n\t"
"s_waitcnt lgkmcnt(2)\n\t"
"v_mfma_f32_16x16x32_bf16 v[228:231], v[228:231], v[220:223], 0\n\t"
"v_mfma_f32_16x16x32_bf16 a[36:39], a[248:251], a[196:199], a[36:39]\n\t"
"buffer_load_dwordx4 a[248:251], v252, s[8:11], s20 offen offset:1024 sc1\n\t"
"v_mfma_f32_16x16x32_bf16 a[36:39], a[252:255], a[200:203], a[36:39]\n\t"
"s_waitcnt vmcnt(8) lgkmcnt(0)\n\t"
"v_cvt_pk_bf16_f32 v224, v224, v225\n\t"
"v_cvt_pk_bf16_f32 v225, v226, v227\n\t"
"v_mfma_f32_16x16x32_bf16 a[36:39], v[96:99], a[204:207], a[36:39]\n\t"
"v_mfma_f32_16x16x32_bf16 v[232:235], v[232:235], v[220:223], 0\n\t"
"buffer_load_dwordx4 v[96:99], v252, s[8:11], s20 offen offset:2048 sc1\n\t"
"v_mfma_f32_16x16x32_bf16 a[44:47], a[220:223], a[168:171], a[44:47]\n\t"
"s_mov_b32 s20, 1241088\n\t"
"v_cvt_pk_bf16_f32 v226, v228, v229\n\t"
"v_cvt_pk_bf16_f32 v227, v230, v231\n\t"
"v_mfma_f32_16x16x32_bf16 a[44:47], a[224:227], a[172:175], a[44:47]\n\t"
"ds_read_b64_tr_b16 v[228:229], v246 offset:53248\n\t"
"v_mfma_f32_16x16x32_bf16 a[44:47], v[248:251], a[176:179], a[44:47]\n\t"
"buffer_load_dwordx4 a[252:255], v252, s[8:11], s20 offen offset:0 sc1\n\t"
"v_mfma_f32_16x16x32_bf16 a[52:55], a[220:223], a[172:175], a[52:55]\n\t"
"ds_read_b64_tr_b16 v[230:231], v246 offset:56320\n\t"
"v_mfma_f32_16x16x32_bf16 a[52:55], a[224:227], a[176:179], a[52:55]\n\t"
"v_cvt_pk_bf16_f32 v232, v232, v233\n\t"
"v_cvt_pk_bf16_f32 v233, v234, v235\n\t"
"v_mfma_f32_16x16x32_bf16 a[52:55], v[248:251], a[180:183], a[52:55]\n\t"
"ds_write2st64_b64 v245, v[224:225], v[226:227] offset0:53 offset1:54\n\t"
"v_mfma_f32_16x16x32_bf16 a[60:63], a[220:223], a[176:179], a[60:63]\n\t"
"v_mfma_f32_16x16x32_bf16 a[60:63], a[224:227], a[180:183], a[60:63]\n\t"
"v_subrev_u32 v243, 55296, v243\n\t"
"v_mfma_f32_16x16x32_bf16 a[60:63], v[248:251], a[184:187], a[60:63]\n\t"
"v_mfma_f32_16x16x32_bf16 a[68:71], a[220:223], a[180:183], a[68:71]\n\t"
"v_mfma_f32_16x16x32_bf16 a[68:71], a[224:227], a[184:187], a[68:71]\n\t"
"v_mfma_f32_16x16x32_bf16 a[68:71], v[248:251], a[188:191], a[68:71]\n\t"
"v_mfma_f32_16x16x32_bf16 a[76:79], a[220:223], a[184:187], a[76:79]\n\t"
"s_waitcnt lgkmcnt(1)\n\t"
"v_mfma_f32_16x16x32_bf16 a[76:79], a[224:227], a[188:191], a[76:79]\n\t"
"v_mfma_f32_16x16x32_bf16 a[76:79], v[248:251], a[192:195], a[76:79]\n\t"
"v_mfma_f32_16x16x32_bf16 v[228:231], v[228:231], v[220:223], 0\n\t"
"v_mfma_f32_16x16x32_bf16 a[84:87], a[220:223], a[188:191], a[84:87]\n\t"
"v_mfma_f32_16x16x32_bf16 a[84:87], a[224:227], a[192:195], a[84:87]\n\t"
"v_mfma_f32_16x16x32_bf16 a[84:87], v[248:251], a[196:199], a[84:87]\n\t"
"v_mfma_f32_16x16x32_bf16 a[92:95], a[220:223], a[192:195], a[92:95]\n\t"
"v_mfma_f32_16x16x32_bf16 a[92:95], a[224:227], a[196:199], a[92:95]\n\t"
"v_cvt_pk_bf16_f32 v234, v228, v229\n\t"
"v_cvt_pk_bf16_f32 v235, v230, v231\n\t"
"v_mfma_f32_16x16x32_bf16 a[92:95], v[248:251], a[200:203], a[92:95]\n\t"
"ds_write2st64_b64 v245, v[232:233], v[234:235] offset0:55 offset1:56\n\t"
"v_mfma_f32_16x16x32_bf16 a[100:103], a[220:223], a[196:199], a[100:103]\n\t"
"buffer_load_dwordx4 a[220:223], v252, s[8:11], s20 offen offset:1024 sc1\n\t"
"v_mfma_f32_16x16x32_bf16 a[100:103], a[224:227], a[200:203], a[100:103]\n\t"
"s_waitcnt vmcnt(8)\n\t"
"ds_read_b64_tr_b16 v[236:237], v240 offset:1536\n\t"
"v_mfma_f32_16x16x32_bf16 a[100:103], v[248:251], a[204:207], a[100:103]\n\t"
"ds_read_b64_tr_b16 v[238:239], v240 offset:3072\n\t"
"v_mfma_f32_16x16x32_bf16 a[108:111], a[228:231], a[168:171], a[108:111]\n\t"
"buffer_load_dwordx4 v[248:251], v252, s[8:11], s20 offen offset:2048 sc1\n\t"
"v_mfma_f32_16x16x32_bf16 a[108:111], a[232:235], a[172:175], a[108:111]\n\t"
"s_mov_b32 s20, 1253376\n\t"
"ds_read_b64_tr_b16 v[224:225], v241 offset:1536\n\t"
"v_mfma_f32_16x16x32_bf16 a[108:111], a[212:215], a[176:179], a[108:111]\n\t"
"s_waitcnt lgkmcnt(3)\n\t"
"s_barrier\n\t"
"v_mfma_f32_16x16x32_bf16 a[116:119], a[228:231], a[172:175], a[116:119]\n\t"
"ds_read_b64_tr_b16 v[226:227], v241 offset:3072\n\t"
"v_mfma_f32_16x16x32_bf16 a[116:119], a[232:235], a[176:179], a[116:119]\n\t"
"buffer_load_dwordx4 a[224:227], v252, s[8:11], s20 offen offset:0 sc1\n\t"
"v_mfma_f32_16x16x32_bf16 a[116:119], a[212:215], a[180:183], a[116:119]\n\t"
"ds_read_b64_tr_b16 v[228:229], v240 offset:21504\n\t"
"v_mfma_f32_16x16x32_bf16 a[124:127], a[228:231], a[176:179], a[124:127]\n\t"
"ds_read_b64_tr_b16 v[230:231], v240 offset:23040\n\t"
"v_mfma_f32_16x16x32_bf16 a[124:127], a[232:235], a[180:183], a[124:127]\n\t"
"ds_read_b64_tr_b16 v[232:233], v241 offset:21504\n\t"
"v_mfma_f32_16x16x32_bf16 a[124:127], a[212:215], a[184:187], a[124:127]\n\t"
"ds_read_b64_tr_b16 v[234:235], v241 offset:23040\n\t"
"v_mfma_f32_16x16x32_bf16 a[132:135], a[228:231], a[180:183], a[132:135]\n\t"
"s_waitcnt lgkmcnt(6)\n\t"
"ds_read_b128 a[168:171], v244 offset:24576\n\t"
"v_mfma_f32_16x16x32_bf16 a[132:135], a[232:235], a[184:187], a[132:135]\n\t"
"s_waitcnt lgkmcnt(5)\n\t"
"ds_read_b128 a[172:175], v244 offset:25088\n\t"
"v_mfma_f32_16x16x32_bf16 a[132:135], a[212:215], a[188:191], a[132:135]\n\t"
"ds_read_b128 a[176:179], v244 offset:25600\n\t"
"v_mfma_f32_16x16x32_bf16 v[236:239], v[236:239], a[208:211], 0\n\t"
"ds_read_b128 a[180:183], v244 offset:26112\n\t"
"v_mfma_f32_16x16x32_bf16 v[224:227], v[224:227], a[208:211], 0\n\t"
"v_mfma_f32_16x16x32_bf16 a[140:143], a[228:231], a[184:187], a[140:143]\n\t"
"ds_read_b128 a[184:187], v244 offset:26624\n\t"
"v_mfma_f32_16x16x32_bf16 a[140:143], a[232:235], a[188:191], a[140:143]\n\t"
"v_mfma_f32_16x16x32_bf16 a[140:143], a[212:215], a[192:195], a[140:143]\n\t"
"v_mfma_f32_16x16x32_bf16 a[148:151], a[228:231], a[188:191], a[148:151]\n\t"
"s_waitcnt lgkmcnt(7)\n\t"
"ds_read_b128 a[188:191], v244 offset:27136\n\t"
"v_cvt_pk_bf16_f32 v236, v236, v237\n\t"
"v_mfma_f32_16x16x32_bf16 a[148:151], a[232:235], a[192:195], a[148:151]\n\t"
"s_waitcnt lgkmcnt(6)\n\t"
"v_cvt_pk_bf16_f32 v237, v238, v239\n\t"
"v_cvt_pk_bf16_f32 v238, v224, v225\n\t"
"v_mfma_f32_16x16x32_bf16 a[148:151], a[212:215], a[196:199], a[148:151]\n\t"
"v_cvt_pk_bf16_f32 v239, v226, v227\n\t"
"v_mfma_f32_16x16x32_bf16 v[228:231], v[228:231], a[208:211], 0\n\t"
"ds_write_b128 v243, v[236:239] offset:1024\n\t"
"v_mfma_f32_16x16x32_bf16 v[232:235], v[232:235], a[208:211], 0\n\t"
"v_mfma_f32_16x16x32_bf16 a[156:159], a[228:231], a[192:195], a[156:159]\n\t"
"ds_read_b128 a[192:195], v244 offset:27648\n\t"
"v_mfma_f32_16x16x32_bf16 a[156:159], a[232:235], a[196:199], a[156:159]\n\t"
"ds_read_b64_tr_b16 v[224:225], v240 offset:41472\n\t"
"v_mfma_f32_16x16x32_bf16 a[156:159], a[212:215], a[200:203], a[156:159]\n\t"
"ds_read_b64_tr_b16 v[226:227], v240 offset:43008\n\t"
"v_mfma_f32_16x16x32_bf16 a[164:167], a[228:231], a[196:199], a[164:167]\n\t"
"buffer_load_dwordx4 a[228:231], v252, s[8:11], s20 offen offset:1024 sc1\n\t"
"v_mfma_f32_16x16x32_bf16 a[164:167], a[232:235], a[200:203], a[164:167]\n\t"
"s_waitcnt vmcnt(8) lgkmcnt(7)\n\t"
"ds_read_b64_tr_b16 v[236:237], v241 offset:41472\n\t"
"v_cvt_pk_bf16_f32 v228, v228, v229\n\t"
"v_mfma_f32_16x16x32_bf16 a[164:167], a[212:215], a[204:207], a[164:167]\n\t"
"ds_read_b64_tr_b16 v[238:239], v241 offset:43008\n\t"
"v_cvt_pk_bf16_f32 v229, v230, v231\n\t"
"Lst29_%=:\n\t"
"v_mfma_f32_16x16x32_bf16 v[0:3], a[236:239], a[168:171], v[0:3]\n\t"
"s_waitcnt lgkmcnt(8)\n\t"
"ds_read_b128 a[196:199], v244 offset:28160\n\t"
"v_cvt_pk_bf16_f32 v230, v232, v233\n\t"
"v_mfma_f32_16x16x32_bf16 v[0:3], a[216:219], a[172:175], v[0:3]\n\t"
"ds_read_b128 a[200:203], v244 offset:28672\n\t"
"v_cvt_pk_bf16_f32 v231, v234, v235\n\t"
"v_mfma_f32_16x16x32_bf16 v[0:3], a[240:243], a[176:179], v[0:3]\n\t"
"ds_write_b128 v243, v[228:231] offset:10240\n\t"
"v_mfma_f32_16x16x32_bf16 v[4:7], a[236:239], a[172:175], v[4:7]\n\t"
"buffer_load_dwordx4 a[212:215], v252, s[8:11], s20 offen offset:2048 sc1\n\t"
"v_mfma_f32_16x16x32_bf16 v[4:7], a[216:219], a[176:179], v[4:7]\n\t"
"s_waitcnt lgkmcnt(10)\n\t"
"ds_read_b128 a[204:207], v244 offset:29184\n\t"
"v_mfma_f32_16x16x32_bf16 v[4:7], a[240:243], a[180:183], v[4:7]\n\t"
"s_mov_b32 s20, 1265664\n\t"
"v_add_u32 v240, 59904, v240\n\t"
"v_add_u32 v241, 59904, v241\n\t"
"v_mfma_f32_16x16x32_bf16 v[8:11], a[236:239], a[176:179], v[8:11]\n\t"
"s_waitcnt lgkmcnt(6)\n\t"
"ds_read_b64_tr_b16 v[232:233], v240 offset:1536\n\t"
"v_mfma_f32_16x16x32_bf16 v[8:11], a[216:219], a[180:183], v[8:11]\n\t"
"buffer_load_dwordx4 a[232:235], v252, s[8:11], s20 offen offset:0 sc1\n\t"
"v_mfma_f32_16x16x32_bf16 v[8:11], a[240:243], a[184:187], v[8:11]\n\t"
"s_waitcnt lgkmcnt(5)\n\t"
"ds_read_b64_tr_b16 v[234:235], v240 offset:3072\n\t"
"v_mfma_f32_16x16x32_bf16 v[224:227], v[224:227], a[208:211], 0\n\t"
"ds_read_b64_tr_b16 v[228:229], v241 offset:1536\n\t"
"v_mfma_f32_16x16x32_bf16 v[236:239], v[236:239], a[208:211], 0\n\t"
"ds_read_b64_tr_b16 v[230:231], v241 offset:3072\n\t"
"v_mfma_f32_16x16x32_bf16 v[12:15], a[236:239], a[180:183], v[12:15]\n\t"
"v_mfma_f32_16x16x32_bf16 v[12:15], a[216:219], a[184:187], v[12:15]\n\t"
"v_mfma_f32_16x16x32_bf16 v[12:15], a[240:243], a[188:191], v[12:15]\n\t"
"v_mfma_f32_16x16x32_bf16 v[16:19], a[236:239], a[184:187], v[16:19]\n\t"
"s_waitcnt lgkmcnt(2)\n\t"
"v_cvt_pk_bf16_f32 v224, v224, v225\n\t"
"v_cvt_pk_bf16_f32 v225, v226, v227\n\t"
"v_mfma_f32_16x16x32_bf16 v[16:19], a[216:219], a[188:191], v[16:19]\n\t"
"s_waitcnt lgkmcnt(0)\n\t"
"v_cvt_pk_bf16_f32 v226, v236, v237\n\t"
"v_cvt_pk_bf16_f32 v227, v238, v239\n\t"
"v_mfma_f32_16x16x32_bf16 v[16:19], a[240:243], a[192:195], v[16:19]\n\t"
"ds_write_b128 v243, v[224:227] offset:19456\n\t"
"v_mfma_f32_16x16x32_bf16 v[232:235], v[232:235], a[208:211], 0\n\t"
"v_mfma_f32_16x16x32_bf16 v[228:231], v[228:231], a[208:211], 0\n\t"
"ds_read_b64_tr_b16 v[236:237], v240 offset:21504\n\t"
"v_mfma_f32_16x16x32_bf16 v[20:23], a[236:239], a[188:191], v[20:23]\n\t"
"ds_read_b64_tr_b16 v[238:239], v240 offset:23040\n\t"
"v_mfma_f32_16x16x32_bf16 v[20:23], a[216:219], a[192:195], v[20:23]\n\t"
"ds_read_b64_tr_b16 v[224:225], v241 offset:21504\n\t"
"v_mfma_f32_16x16x32_bf16 v[20:23], a[240:243], a[196:199], v[20:23]\n\t"
"ds_read_b64_tr_b16 v[226:227], v241 offset:23040\n\t"
"v_mfma_f32_16x16x32_bf16 v[24:27], a[236:239], a[192:195], v[24:27]\n\t"
"v_cvt_pk_bf16_f32 v232, v232, v233\n\t"
"v_cvt_pk_bf16_f32 v233, v234, v235\n\t"
"v_mfma_f32_16x16x32_bf16 v[24:27], a[216:219], a[196:199], v[24:27]\n\t"
"v_cvt_pk_bf16_f32 v234, v228, v229\n\t"
"v_cvt_pk_bf16_f32 v235, v230, v231\n\t"
"v_mfma_f32_16x16x32_bf16 v[24:27], a[240:243], a[200:203], v[24:27]\n\t"
"ds_write_b128 v243, v[232:235] offset:28672\n\t"
"v_mfma_f32_16x16x32_bf16 v[28:31], a[236:239], a[196:199], v[28:31]\n\t"
"buffer_load_dwordx4 a[236:239], v252, s[8:11], s20 offen offset:1024 sc1\n\t"
"v_mfma_f32_16x16x32_bf16 v[28:31], a[216:219], a[200:203], v[28:31]\n\t"
"s_waitcnt vmcnt(8)\n\t"
"ds_read_b64_tr_b16 v[228:229], v240 offset:41472\n\t"
"v_mfma_f32_16x16x32_bf16 v[28:31], a[240:243], a[204:207], v[28:31]\n\t"
"ds_read_b64_tr_b16 v[230:231], v240 offset:43008\n\t"
"v_mfma_f32_16x16x32_bf16 v[32:35], a[244:247], a[168:171], v[32:35]\n\t"
"buffer_load_dwordx4 a[216:219], v252, s[8:11], s20 offen offset:2048 sc1\n\t"
"v_mfma_f32_16x16x32_bf16 v[32:35], a[248:251], a[172:175], v[32:35]\n\t"
"s_mov_b32 s20, 1277952\n\t"
"ds_read_b64_tr_b16 v[232:233], v241 offset:41472\n\t"
"v_mfma_f32_16x16x32_bf16 v[32:35], v[96:99], a[176:179], v[32:35]\n\t"
"ds_read_b64_tr_b16 v[234:235], v241 offset:43008\n\t"
"v_mfma_f32_16x16x32_bf16 v[36:39], a[244:247], a[172:175], v[36:39]\n\t"
"buffer_load_dwordx4 a[240:243], v252, s[8:11], s20 offen offset:0 sc1\n\t"
"v_mfma_f32_16x16x32_bf16 v[36:39], a[248:251], a[176:179], v[36:39]\n\t"
"s_waitcnt lgkmcnt(5)\n\t"
"v_mfma_f32_16x16x32_bf16 v[36:39], v[96:99], a[180:183], v[36:39]\n\t"
"v_mfma_f32_16x16x32_bf16 v[236:239], v[236:239], a[208:211], 0\n\t"
"s_waitcnt lgkmcnt(2)\n\t"
"v_mfma_f32_16x16x32_bf16 v[224:227], v[224:227], a[208:211], 0\n\t"
"s_waitcnt lgkmcnt(0)\n\t"
"v_mfma_f32_16x16x32_bf16 v[40:43], a[244:247], a[176:179], v[40:43]\n\t"
"v_mfma_f32_16x16x32_bf16 v[40:43], a[248:251], a[180:183], v[40:43]\n\t"
"v_mfma_f32_16x16x32_bf16 v[40:43], v[96:99], a[184:187], v[40:43]\n\t"
"Lpg21_%=:\n\t"
"s_cbranch_vccnz Lvr3_%=\n\t"
"v_mfma_f32_16x16x32_bf16 v[44:47], a[244:247], a[180:183], v[44:47]\n\t"
"v_cvt_pk_bf16_f32 v236, v236, v237\n\t"
"v_cvt_pk_bf16_f32 v237, v238, v239\n\t"
"v_mfma_f32_16x16x32_bf16 v[44:47], a[248:251], a[184:187], v[44:47]\n\t"
"v_cvt_pk_bf16_f32 v238, v224, v225\n\t"
"v_cvt_pk_bf16_f32 v239, v226, v227\n\t"
"v_mfma_f32_16x16x32_bf16 v[44:47], v[96:99], a[188:191], v[44:47]\n\t"
"ds_write_b128 v243, v[236:239] offset:37888\n\t"
"v_mfma_f32_16x16x32_bf16 v[228:231], v[228:231], a[208:211], 0\n\t"
"v_mfma_f32_16x16x32_bf16 v[232:235], v[232:235], a[208:211], 0\n\t"
"v_mfma_f32_16x16x32_bf16 v[48:51], a[244:247], a[184:187], v[48:51]\n\t"
"buffer_load_dwordx4 a[208:211], v252, s[8:11], s20 offen offset:1024 sc1\n\t"
"v_mfma_f32_16x16x32_bf16 v[48:51], a[248:251], a[188:191], v[48:51]\n\t"
"v_mfma_f32_16x16x32_bf16 v[48:51], v[96:99], a[192:195], v[48:51]\n\t"
"v_mfma_f32_16x16x32_bf16 v[52:55], a[244:247], a[188:191], v[52:55]\n\t"
"v_cvt_pk_bf16_f32 v228, v228, v229\n\t"
"v_cvt_pk_bf16_f32 v229, v230, v231\n\t"
"v_mfma_f32_16x16x32_bf16 v[52:55], a[248:251], a[192:195], v[52:55]\n\t"
"v_cvt_pk_bf16_f32 v230, v232, v233\n\t"
"v_cvt_pk_bf16_f32 v231, v234, v235\n\t"
"v_mfma_f32_16x16x32_bf16 v[52:55], v[96:99], a[196:199], v[52:55]\n\t"
"ds_write_b128 v243, v[228:231] offset:47104\n\t"
"v_mfma_f32_16x16x32_bf16 v[56:59], a[244:247], a[192:195], v[56:59]\n\t"
"v_mfma_f32_16x16x32_bf16 v[56:59], a[248:251], a[196:199], v[56:59]\n\t"
"ds_read_b64_tr_b16 v[224:225], v246 offset:1536\n\t"
"v_mfma_f32_16x16x32_bf16 v[56:59], v[96:99], a[200:203], v[56:59]\n\t"
"ds_read_b64_tr_b16 v[226:227], v246 offset:4608\n\t"
"v_mfma_f32_16x16x32_bf16 v[60:63], a[244:247], a[196:199], v[60:63]\n\t"
"buffer_load_dwordx4 a[244:247], v252, s[8:11], s20 offen offset:2048 sc1\n\t"
"v_mfma_f32_16x16x32_bf16 v[60:63], a[248:251], a[200:203], v[60:63]\n\t"
"s_waitcnt vmcnt(9)\n\t"
"ds_read_b64_tr_b16 v[236:237], v246 offset:7680\n\t"
"s_mov_b32 s20, 1511424\n\t"
"v_mfma_f32_16x16x32_bf16 v[60:63], v[96:99], a[204:207], v[60:63]\n\t"
"ds_read_b64_tr_b16 v[238:239], v246 offset:10752\n\t"
"v_mfma_f32_16x16x32_bf16 v[64:67], a[252:255], a[168:171], v[64:67]\n\t"
"buffer_load_dwordx4 v[96:99], v252, s[8:11], s20 offen offset:0 sc1\n\t"
"v_mfma_f32_16x16x32_bf16 v[64:67], a[220:223], a[172:175], v[64:67]\n\t"
"ds_read_b64_tr_b16 v[232:233], v246 offset:13824\n\t"
"v_mfma_f32_16x16x32_bf16 v[64:67], v[248:251], a[176:179], v[64:67]\n\t"
"ds_read_b64_tr_b16 v[234:235], v246 offset:16896\n\t"
"v_mfma_f32_16x16x32_bf16 v[68:71], a[252:255], a[172:175], v[68:71]\n\t"
"buffer_load_dwordx4 a[248:251], v252, s[8:11], s20 offen offset:1024 sc1\n\t"
"v_mfma_f32_16x16x32_bf16 v[68:71], a[220:223], a[176:179], v[68:71]\n\t"
"ds_read_b128 a[168:171], v244 offset:33792\n\t"
"v_mfma_f32_16x16x32_bf16 v[68:71], v[248:251], a[180:183], v[68:71]\n\t"
"ds_read_b64_tr_b16 v[228:229], v246 offset:21504\n\t"
"v_mfma_f32_16x16x32_bf16 v[72:75], a[252:255], a[176:179], v[72:75]\n\t"
"buffer_load_dwordx4 v[240:243], v252, s[8:11], s20 offen offset:2048 sc1\n\t"
"v_mfma_f32_16x16x32_bf16 v[72:75], a[220:223], a[180:183], v[72:75]\n\t"
"s_waitcnt lgkmcnt(6)\n\t"
"ds_read_b64_tr_b16 v[230:231], v246 offset:24576\n\t"
"v_mfma_f32_16x16x32_bf16 v[72:75], v[248:251], a[184:187], v[72:75]\n\t"
"ds_read_b128 a[172:175], v244 offset:34304\n\t"
"v_mfma_f32_16x16x32_bf16 v[224:227], v[224:227], v[220:223], 0\n\t"
"ds_read_b128 a[176:179], v244 offset:34816\n\t"
"v_mfma_f32_16x16x32_bf16 v[76:79], a[252:255], a[180:183], v[76:79]\n\t"
"s_waitcnt lgkmcnt(7)\n\t"
"ds_read_b128 a[180:183], v244 offset:35328\n\t"
"v_mfma_f32_16x16x32_bf16 v[76:79], a[220:223], a[184:187], v[76:79]\n\t"
"v_mfma_f32_16x16x32_bf16 v[76:79], v[248:251], a[188:191], v[76:79]\n\t"
"v_mfma_f32_16x16x32_bf16 v[236:239], v[236:239], v[220:223], 0\n\t"
"v_mfma_f32_16x16x32_bf16 v[80:83], a[252:255], a[184:187], v[80:83]\n\t"
"s_waitcnt lgkmcnt(6)\n\t"
"ds_read_b128 a[184:187], v244 offset:35840\n\t"
"v_cvt_pk_bf16_f32 v224, v224, v225\n\t"
"v_mfma_f32_16x16x32_bf16 v[80:83], a[220:223], a[188:191], v[80:83]\n\t"
"v_cvt_pk_bf16_f32 v225, v226, v227\n\t"
"v_mfma_f32_16x16x32_bf16 v[80:83], v[248:251], a[192:195], v[80:83]\n\t"
"s_mov_b32 s20, 1523712\n\t"
"v_mfma_f32_16x16x32_bf16 v[232:235], v[232:235], v[220:223], 0\n\t"
"v_mfma_f32_16x16x32_bf16 v[84:87], a[252:255], a[188:191], v[84:87]\n\t"
"s_waitcnt lgkmcnt(4)\n\t"
"ds_read_b128 a[188:191], v244 offset:36352\n\t"
"v_cvt_pk_bf16_f32 v226, v236, v237\n\t"
"v_mfma_f32_16x16x32_bf16 v[84:87], a[220:223], a[192:195], v[84:87]\n\t"
"v_cvt_pk_bf16_f32 v227, v238, v239\n\t"
"v_mfma_f32_16x16x32_bf16 v[84:87], v[248:251], a[196:199], v[84:87]\n\t"
"v_mfma_f32_16x16x32_bf16 v[228:231], v[228:231], v[220:223], 0\n\t"
"v_mfma_f32_16x16x32_bf16 v[88:91], a[252:255], a[192:195], v[88:91]\n\t"
"buffer_load_dwordx4 v[236:239], v252, s[8:11], s20 offen offset:0 sc1\n\t"
"v_mfma_f32_16x16x32_bf16 v[88:91], a[220:223], a[196:199], v[88:91]\n\t"
"ds_read_b128 a[192:195], v244 offset:36864\n\t"
"v_cvt_pk_bf16_f32 v232, v232, v233\n\t"
"v_mfma_f32_16x16x32_bf16 v[88:91], v[248:251], a[200:203], v[88:91]\n\t"
"v_cvt_pk_bf16_f32 v233, v234, v235\n\t"
"v_mfma_f32_16x16x32_bf16 v[92:95], a[252:255], a[196:199], v[92:95]\n\t"
"buffer_load_dwordx4 a[252:255], v252, s[8:11], s20 offen offset:1024 sc1\n\t"
"v_mfma_f32_16x16x32_bf16 v[92:95], a[220:223], a[200:203], v[92:95]\n\t"
"s_waitcnt vmcnt(11) lgkmcnt(4)\n\t"
"ds_read_b128 a[196:199], v244 offset:37376\n\t"
"v_cvt_pk_bf16_f32 v234, v228, v229\n\t"
"v_mfma_f32_16x16x32_bf16 v[92:95], v[248:251], a[204:207], v[92:95]\n\t"
"ds_read_b128 a[200:203], v244 offset:37888\n\t"
"v_cvt_pk_bf16_f32 v235, v230, v231\n\t"
"v_mfma_f32_16x16x32_bf16 v[100:103], a[224:227], a[168:171], v[100:103]\n\t"
"ds_read_b64_tr_b16 v[228:229], v246 offset:27648\n\t"
"v_mfma_f32_16x16x32_bf16 v[100:103], a[228:231], a[172:175], v[100:103]\n\t"
"s_waitcnt lgkmcnt(6)\n\t"
"ds_read_b64_tr_b16 v[230:231], v246 offset:30720\n\t"
"v_mfma_f32_16x16x32_bf16 v[100:103], a[212:215], a[176:179], v[100:103]\n\t"
"s_waitcnt lgkmcnt(0)\n\t"
"ds_read_b128 a[204:207], v244 offset:38400\n\t"
"v_mfma_f32_16x16x32_bf16 v[228:231], v[228:231], v[220:223], 0\n\t"
"buffer_load_dwordx4 v[248:251], v252, s[8:11], s20 offen offset:2048 sc1\n\t"
"s_mov_b32 s20, 1536000\n\t"
"v_mfma_f32_16x16x32_bf16 v[104:107], a[224:227], a[172:175], v[104:107]\n\t"
"buffer_load_dwordx4 a[220:223], v252, s[8:11], s20 offen offset:0 sc1\n\t"
"v_mfma_f32_16x16x32_bf16 v[104:107], a[228:231], a[176:179], v[104:107]\n\t"
"v_mfma_f32_16x16x32_bf16 v[104:107], a[212:215], a[180:183], v[104:107]\n\t"
"s_waitcnt lgkmcnt(0)\n\t"
"s_barrier\n\t"
"v_mfma_f32_16x16x32_bf16 v[108:111], a[224:227], a[176:179], v[108:111]\n\t"
"v_mfma_f32_16x16x32_bf16 v[108:111], a[228:231], a[180:183], v[108:111]\n\t"
"v_cvt_pk_bf16_f32 v228, v228, v229\n\t"
"v_cvt_pk_bf16_f32 v229, v230, v231\n\t"
"v_mfma_f32_16x16x32_bf16 v[108:111], a[212:215], a[184:187], v[108:111]\n\t"
"ds_write2st64_b64 v245, v[224:225], v[226:227] offset0:48 offset1:49\n\t"
"v_mfma_f32_16x16x32_bf16 v[112:115], a[224:227], a[180:183], v[112:115]\n\t"
"v_mfma_f32_16x16x32_bf16 v[112:115], a[228:231], a[184:187], v[112:115]\n\t"
"ds_read_b64_tr_b16 v[224:225], v246 offset:33792\n\t"
"v_mfma_f32_16x16x32_bf16 v[112:115], a[212:215], a[188:191], v[112:115]\n\t"
"ds_write2st64_b64 v245, v[232:233], v[234:235] offset0:50 offset1:51\n\t"
"v_mfma_f32_16x16x32_bf16 v[116:119], a[224:227], a[184:187], v[116:119]\n\t"
"v_mfma_f32_16x16x32_bf16 v[116:119], a[228:231], a[188:191], v[116:119]\n\t"
"ds_read_b64_tr_b16 v[226:227], v246 offset:36864\n\t"
"v_mfma_f32_16x16x32_bf16 v[116:119], a[212:215], a[192:195], v[116:119]\n\t"
"ds_write_b64 v245, v[228:229] offset:26624\n\t"
"v_mfma_f32_16x16x32_bf16 v[120:123], a[224:227], a[188:191], v[120:123]\n\t"
"ds_read_b64_tr_b16 v[232:233], v246 offset:41472\n\t"
"v_mfma_f32_16x16x32_bf16 v[120:123], a[228:231], a[192:195], v[120:123]\n\t"
"ds_read_b64_tr_b16 v[234:235], v246 offset:44544\n\t"
"v_mfma_f32_16x16x32_bf16 v[120:123], a[212:215], a[196:199], v[120:123]\n\t"
"s_waitcnt lgkmcnt(3)\n\t"
"ds_read_b64_tr_b16 v[228:229], v246 offset:47616\n\t"
"v_mfma_f32_16x16x32_bf16 v[224:227], v[224:227], v[220:223], 0\n\t"
"ds_read_b64_tr_b16 v[230:231], v246 offset:50688\n\t"
"v_mfma_f32_16x16x32_bf16 v[124:127], a[224:227], a[192:195], v[124:127]\n\t"
"v_mfma_f32_16x16x32_bf16 v[124:127], a[228:231], a[196:199], v[124:127]\n\t"
"v_mfma_f32_16x16x32_bf16 v[124:127], a[212:215], a[200:203], v[124:127]\n\t"
"s_waitcnt lgkmcnt(2)\n\t"
"v_mfma_f32_16x16x32_bf16 v[232:235], v[232:235], v[220:223], 0\n\t"
"v_mfma_f32_16x16x32_bf16 v[128:131], a[224:227], a[196:199], v[128:131]\n\t"
"buffer_load_dwordx4 a[224:227], v252, s[8:11], s20 offen offset:1024 sc1\n\t"
"v_mfma_f32_16x16x32_bf16 v[128:131], a[228:231], a[200:203], v[128:131]\n\t"
"s_waitcnt vmcnt(11) lgkmcnt(0)\n\t"
"v_cvt_pk_bf16_f32 v224, v224, v225\n\t"
"v_cvt_pk_bf16_f32 v225, v226, v227\n\t"
"v_mfma_f32_16x16x32_bf16 v[128:131], a[212:215], a[204:207], v[128:131]\n\t"
"v_mfma_f32_16x16x32_bf16 v[228:231], v[228:231], v[220:223], 0\n\t"
"buffer_load_dwordx4 a[212:215], v252, s[8:11], s20 offen offset:2048 sc1\n\t"
"v_mfma_f32_16x16x32_bf16 v[132:135], a[232:235], a[168:171], v[132:135]\n\t"
"s_mov_b32 s20, 1548288\n\t"
"v_cvt_pk_bf16_f32 v226, v232, v233\n\t"
"v_cvt_pk_bf16_f32 v227, v234, v235\n\t"
"v_mfma_f32_16x16x32_bf16 v[132:135], a[236:239], a[172:175], v[132:135]\n\t"
"ds_read_b64_tr_b16 v[232:233], v246 offset:53760\n\t"
"v_mfma_f32_16x16x32_bf16 v[132:135], a[216:219], a[176:179], v[132:135]\n\t"
"buffer_load_dwordx4 a[228:231], v252, s[8:11], s20 offen offset:0 sc1\n\t"
"v_mfma_f32_16x16x32_bf16 v[136:139], a[232:235], a[172:175], v[136:139]\n\t"
"ds_read_b64_tr_b16 v[234:235], v246 offset:56832\n\t"
"v_mfma_f32_16x16x32_bf16 v[136:139], a[236:239], a[176:179], v[136:139]\n\t"
"v_cvt_pk_bf16_f32 v228, v228, v229\n\t"
"v_cvt_pk_bf16_f32 v229, v230, v231\n\t"
"v_mfma_f32_16x16x32_bf16 v[136:139], a[216:219], a[180:183], v[136:139]\n\t"
"ds_write2st64_b64 v245, v[224:225], v[226:227] offset0:53 offset1:54\n\t"
"v_mfma_f32_16x16x32_bf16 v[140:143], a[232:235], a[176:179], v[140:143]\n\t"
"v_mfma_f32_16x16x32_bf16 v[140:143], a[236:239], a[180:183], v[140:143]\n\t"
"v_mfma_f32_16x16x32_bf16 v[140:143], a[216:219], a[184:187], v[140:143]\n\t"
"v_mfma_f32_16x16x32_bf16 v[144:147], a[232:235], a[180:183], v[144:147]\n\t"
"v_mfma_f32_16x16x32_bf16 v[144:147], a[236:239], a[184:187], v[144:147]\n\t"
"v_mfma_f32_16x16x32_bf16 v[144:147], a[216:219], a[188:191], v[144:147]\n\t"
"v_mfma_f32_16x16x32_bf16 v[148:151], a[232:235], a[184:187], v[148:151]\n\t"
"s_waitcnt lgkmcnt(1)\n\t"
"v_mfma_f32_16x16x32_bf16 v[148:151], a[236:239], a[188:191], v[148:151]\n\t"
"v_mfma_f32_16x16x32_bf16 v[148:151], a[216:219], a[192:195], v[148:151]\n\t"
"v_mfma_f32_16x16x32_bf16 v[232:235], v[232:235], v[220:223], 0\n\t"
"v_mfma_f32_16x16x32_bf16 v[152:155], a[232:235], a[188:191], v[152:155]\n\t"
"v_mfma_f32_16x16x32_bf16 v[152:155], a[236:239], a[192:195], v[152:155]\n\t"
"v_mfma_f32_16x16x32_bf16 v[152:155], a[216:219], a[196:199], v[152:155]\n\t"
"v_mfma_f32_16x16x32_bf16 v[156:159], a[232:235], a[192:195], v[156:159]\n\t"
"v_mfma_f32_16x16x32_bf16 v[156:159], a[236:239], a[196:199], v[156:159]\n\t"
"v_cvt_pk_bf16_f32 v230, v232, v233\n\t"
"v_cvt_pk_bf16_f32 v231, v234, v235\n\t"
"v_mfma_f32_16x16x32_bf16 v[156:159], a[216:219], a[200:203], v[156:159]\n\t"
"ds_write2st64_b64 v245, v[228:229], v[230:231] offset0:55 offset1:56\n\t"
"v_mfma_f32_16x16x32_bf16 v[160:163], a[232:235], a[196:199], v[160:163]\n\t"
"buffer_load_dwordx4 a[232:235], v252, s[8:11], s20 offen offset:1024 sc1\n\t"
"v_mfma_f32_16x16x32_bf16 v[160:163], a[236:239], a[200:203], v[160:163]\n\t"
"s_waitcnt vmcnt(11)\n\t"
"v_mfma_f32_16x16x32_bf16 v[160:163], a[216:219], a[204:207], v[160:163]\n\t"
"v_mfma_f32_16x16x32_bf16 v[164:167], a[240:243], a[168:171], v[164:167]\n\t"
"buffer_load_dwordx4 a[216:219], v252, s[8:11], s20 offen offset:2048 sc1\n\t"
"v_mfma_f32_16x16x32_bf16 v[164:167], a[208:211], a[172:175], v[164:167]\n\t"
"s_mov_b32 s20, 1560576\n\t"
"v_mfma_f32_16x16x32_bf16 v[164:167], a[244:247], a[176:179], v[164:167]\n\t"
"s_waitcnt lgkmcnt(0)\n\t"
"s_barrier\n\t"
"v_mfma_f32_16x16x32_bf16 v[168:171], a[240:243], a[172:175], v[168:171]\n\t"
"ds_read_b128 a[168:171], v244 offset:24576\n\t"
"v_mfma_f32_16x16x32_bf16 v[168:171], a[208:211], a[176:179], v[168:171]\n\t"
"buffer_load_dwordx4 a[236:239], v252, s[8:11], s20 offen offset:0 sc1\n\t"
"v_mfma_f32_16x16x32_bf16 v[168:171], a[244:247], a[180:183], v[168:171]\n\t"
"ds_read_b128 a[172:175], v244 offset:25088\n\t"
"v_mfma_f32_16x16x32_bf16 v[172:175], a[240:243], a[176:179], v[172:175]\n\t"
"ds_read_b128 a[176:179], v244 offset:25600\n\t"
"v_mfma_f32_16x16x32_bf16 v[172:175], a[208:211], a[180:183], v[172:175]\n\t"
"v_mfma_f32_16x16x32_bf16 v[172:175], a[244:247], a[184:187], v[172:175]\n\t"
"v_mfma_f32_16x16x32_bf16 v[176:179], a[240:243], a[180:183], v[176:179]\n\t"
"ds_read_b128 a[180:183], v244 offset:26112\n\t"
"v_mfma_f32_16x16x32_bf16 v[176:179], a[208:211], a[184:187], v[176:179]\n\t"
"v_mfma_f32_16x16x32_bf16 v[176:179], a[244:247], a[188:191], v[176:179]\n\t"
"v_mfma_f32_16x16x32_bf16 v[180:183], a[240:243], a[184:187], v[180:183]\n\t"
"ds_read_b128 a[184:187], v244 offset:26624\n\t"
"v_mfma_f32_16x16x32_bf16 v[180:183], a[208:211], a[188:191], v[180:183]\n\t"
"v_mfma_f32_16x16x32_bf16 v[180:183], a[244:247], a[192:195], v[180:183]\n\t"
"v_mfma_f32_16x16x32_bf16 v[184:187], a[240:243], a[188:191], v[184:187]\n\t"
"ds_read_b128 a[188:191], v244 offset:27136\n\t"
"v_mfma_f32_16x16x32_bf16 v[184:187], a[208:211], a[192:195], v[184:187]\n\t"
"v_mfma_f32_16x16x32_bf16 v[184:187], a[244:247], a[196:199], v[184:187]\n\t"
"v_mfma_f32_16x16x32_bf16 v[188:191], a[240:243], a[192:195], v[188:191]\n\t"
"ds_read_b128 a[192:195], v244 offset:27648\n\t"
"v_mfma_f32_16x16x32_bf16 v[188:191], a[208:211], a[196:199], v[188:191]\n\t"
"v_mfma_f32_16x16x32_bf16 v[188:191], a[244:247], a[200:203], v[188:191]\n\t"
"v_mfma_f32_16x16x32_bf16 v[192:195], a[240:243], a[196:199], v[192:195]\n\t"
"buffer_load_dwordx4 a[240:243], v252, s[8:11], s20 offen offset:1024 sc1\n\t"
"v_mfma_f32_16x16x32_bf16 v[192:195], a[208:211], a[200:203], v[192:195]\n\t"
"s_waitcnt vmcnt(11) lgkmcnt(4)\n\t"
"ds_read_b128 a[196:199], v244 offset:28160\n\t"
"v_mfma_f32_16x16x32_bf16 v[192:195], a[244:247], a[204:207], v[192:195]\n\t"
"ds_read_b128 a[200:203], v244 offset:28672\n\t"
"Lst30_%=:\n\t"
"v_mfma_f32_16x16x32_bf16 v[0:3], v[96:99], a[168:171], v[0:3]\n\t"
"buffer_load_dwordx4 a[208:211], v252, s[8:11], s20 offen offset:2048 sc1\n\t"
"v_mfma_f32_16x16x32_bf16 v[0:3], a[248:251], a[172:175], v[0:3]\n\t"
"s_waitcnt lgkmcnt(5)\n\t"
"ds_read_b128 a[204:207], v244 offset:29184\n\t"
"v_xor_b32 v246, 0xc0, v246\n\t"
"v_mfma_f32_16x16x32_bf16 v[0:3], v[240:243], a[176:179], v[0:3]\n\t"
"s_mov_b32 s20, 1572864\n\t"
"v_mfma_f32_16x16x32_bf16 v[4:7], v[96:99], a[172:175], v[4:7]\n\t"
"buffer_load_dwordx4 a[244:247], v252, s[8:11], s20 offen offset:0 sc1\n\t"
"v_mfma_f32_16x16x32_bf16 v[4:7], a[248:251], a[176:179], v[4:7]\n\t"
"s_waitcnt lgkmcnt(5)\n\t"
"v_mfma_f32_16x16x32_bf16 v[4:7], v[240:243], a[180:183], v[4:7]\n\t"
"v_mfma_f32_16x16x32_bf16 v[8:11], v[96:99], a[176:179], v[8:11]\n\t"
"s_waitcnt lgkmcnt(4)\n\t"
"v_mfma_f32_16x16x32_bf16 v[8:11], a[248:251], a[180:183], v[8:11]\n\t"
"v_mfma_f32_16x16x32_bf16 v[8:11], v[240:243], a[184:187], v[8:11]\n\t"
"v_mfma_f32_16x16x32_bf16 v[12:15], v[96:99], a[180:183], v[12:15]\n\t"
"s_waitcnt lgkmcnt(3)\n\t"
"v_mfma_f32_16x16x32_bf16 v[12:15], a[248:251], a[184:187], v[12:15]\n\t"
"v_mfma_f32_16x16x32_bf16 v[12:15], v[240:243], a[188:191], v[12:15]\n\t"
"v_mfma_f32_16x16x32_bf16 v[16:19], v[96:99], a[184:187], v[16:19]\n\t"
"s_waitcnt lgkmcnt(2)\n\t"
"v_mfma_f32_16x16x32_bf16 v[16:19], a[248:251], a[188:191], v[16:19]\n\t"
"v_mfma_f32_16x16x32_bf16 v[16:19], v[240:243], a[192:195], v[16:19]\n\t"
"ds_read_b64_tr_b16 v[224:225], v246 offset:1536\n\t"
"v_mfma_f32_16x16x32_bf16 v[20:23], v[96:99], a[188:191], v[20:23]\n\t"
"s_waitcnt lgkmcnt(2)\n\t"
"ds_read_b64_tr_b16 v[226:227], v246 offset:4608\n\t"
"v_mfma_f32_16x16x32_bf16 v[20:23], a[248:251], a[192:195], v[20:23]\n\t"
"v_mfma_f32_16x16x32_bf16 v[20:23], v[240:243], a[196:199], v[20:23]\n\t"
"v_mfma_f32_16x16x32_bf16 v[24:27], v[96:99], a[192:195], v[24:27]\n\t"
"s_waitcnt lgkmcnt(2)\n\t"
"v_mfma_f32_16x16x32_bf16 v[24:27], a[248:251], a[196:199], v[24:27]\n\t"
"v_mfma_f32_16x16x32_bf16 v[24:27], v[240:243], a[200:203], v[24:27]\n\t"
"v_mfma_f32_16x16x32_bf16 v[28:31], v[96:99], a[196:199], v[28:31]\n\t"
"buffer_load_dwordx4 v[96:99], v252, s[8:11], s20 offen offset:1024 sc1\n\t"
"v_mfma_f32_16x16x32_bf16 v[28:31], a[248:251], a[200:203], v[28:31]\n\t"
"s_waitcnt vmcnt(11)\n\t"
"ds_read_b64_tr_b16 v[232:233], v246 offset:7680\n\t"
"v_mfma_f32_16x16x32_bf16 v[28:31], v[240:243], a[204:207], v[28:31]\n\t"
"ds_read_b64_tr_b16 v[234:235], v246 offset:10752\n\t"
"v_mfma_f32_16x16x32_bf16 v[32:35], v[236:239], a[168:171], v[32:35]\n\t"
"buffer_load_dwordx4 a[248:251], v252, s[8:11], s20 offen offset:2048 sc1\n\t"
"v_mfma_f32_16x16x32_bf16 v[32:35], a[252:255], a[172:175], v[32:35]\n\t"
"s_mov_b32 s20, 1585152\n\t"
"v_mfma_f32_16x16x32_bf16 v[32:35], v[248:251], a[176:179], v[32:35]\n\t"
"v_mfma_f32_16x16x32_bf16 v[36:39], v[236:239], a[172:175], v[36:39]\n\t"
"buffer_load_dwordx4 v[240:243], v252, s[8:11], s20 offen offset:0 sc1\n\t"
"v_mfma_f32_16x16x32_bf16 v[36:39], a[252:255], a[176:179], v[36:39]\n\t"
"s_waitcnt lgkmcnt(2)\n\t"
"v_mfma_f32_16x16x32_bf16 v[36:39], v[248:251], a[180:183], v[36:39]\n\t"
"v_mfma_f32_16x16x32_bf16 v[224:227], v[224:227], v[220:223], 0\n\t"
"v_mfma_f32_16x16x32_bf16 v[40:43], v[236:239], a[176:179], v[40:43]\n\t"
"ds_read_b64_tr_b16 v[228:229], v246 offset:13824\n\t"
"v_mfma_f32_16x16x32_bf16 v[40:43], a[252:255], a[180:183], v[40:43]\n\t"
"ds_read_b64_tr_b16 v[230:231], v246 offset:16896\n\t"
"v_mfma_f32_16x16x32_bf16 v[40:43], v[248:251], a[184:187], v[40:43]\n\t"
"v_mfma_f32_16x16x32_bf16 v[44:47], v[236:239], a[180:183], v[44:47]\n\t"
"s_waitcnt lgkmcnt(2)\n\t"
"v_mfma_f32_16x16x32_bf16 v[44:47], a[252:255], a[184:187], v[44:47]\n\t"
"v_cvt_pk_bf16_f32 v224, v224, v225\n\t"
"v_cvt_pk_bf16_f32 v225, v226, v227\n\t"
"v_mfma_f32_16x16x32_bf16 v[44:47], v[248:251], a[188:191], v[44:47]\n\t"
"v_mfma_f32_16x16x32_bf16 v[232:235], v[232:235], v[220:223], 0\n\t"
"v_mfma_f32_16x16x32_bf16 v[48:51], v[236:239], a[184:187], v[48:51]\n\t"
"v_mfma_f32_16x16x32_bf16 v[48:51], a[252:255], a[188:191], v[48:51]\n\t"
"v_mfma_f32_16x16x32_bf16 v[48:51], v[248:251], a[192:195], v[48:51]\n\t"
"v_mfma_f32_16x16x32_bf16 v[52:55], v[236:239], a[188:191], v[52:55]\n\t"
"v_mfma_f32_16x16x32_bf16 v[52:55], a[252:255], a[192:195], v[52:55]\n\t"
"v_cvt_pk_bf16_f32 v226, v232, v233\n\t"
"v_cvt_pk_bf16_f32 v227, v234, v235\n\t"
"v_mfma_f32_16x16x32_bf16 v[52:55], v[248:251], a[196:199], v[52:55]\n\t"
"ds_read_b64_tr_b16 v[232:233], v246 offset:21504\n\t"
"v_mfma_f32_16x16x32_bf16 v[56:59], v[236:239], a[192:195], v[56:59]\n\t"
"s_waitcnt lgkmcnt(1)\n\t"
"ds_read_b64_tr_b16 v[234:235], v246 offset:24576\n\t"
"v_mfma_f32_16x16x32_bf16 v[56:59], a[252:255], a[196:199], v[56:59]\n\t"
"v_mfma_f32_16x16x32_bf16 v[56:59], v[248:251], a[200:203], v[56:59]\n\t"
"v_mfma_f32_16x16x32_bf16 v[228:231], v[228:231], v[220:223], 0\n\t"
"v_mfma_f32_16x16x32_bf16 v[60:63], v[236:239], a[196:199], v[60:63]\n\t"
"buffer_load_dwordx4 v[236:239], v252, s[8:11], s20 offen offset:1024 sc1\n\t"
"v_mfma_f32_16x16x32_bf16 v[60:63], a[252:255], a[200:203], v[60:63]\n\t"
"s_waitcnt vmcnt(11)\n\t"
"v_mfma_f32_16x16x32_bf16 v[60:63], v[248:251], a[204:207], v[60:63]\n\t"
"v_mfma_f32_16x16x32_bf16 v[64:67], a[220:223], a[168:171], v[64:67]\n\t"
"buffer_load_dwordx4 v[248:251], v252, s[8:11], s20 offen offset:2048 sc1\n\t"
"v_mfma_f32_16x16x32_bf16 v[64:67], a[224:227], a[172:175], v[64:67]\n\t"
"s_mov_b32 s20, 1597440\n\t"
"ds_read_b128 a[168:171], v244 offset:33792\n\t"
"v_cvt_pk_bf16_f32 v228, v228, v229\n\t"
"v_mfma_f32_16x16x32_bf16 v[64:67], a[212:215], a[176:179], v[64:67]\n\t"
"v_cvt_pk_bf16_f32 v229, v230, v231\n\t"
"v_mfma_f32_16x16x32_bf16 v[68:71], a[220:223], a[172:175], v[68:71]\n\t"
"buffer_load_dwordx4 a[252:255], v252, s[8:11], s20 offen offset:0 sc1\n\t"
"v_mfma_f32_16x16x32_bf16 v[68:71], a[224:227], a[176:179], v[68:71]\n\t"
"s_waitcnt lgkmcnt(1)\n\t"
"ds_read_b128 a[172:175], v244 offset:34304\n\t"
"v_mfma_f32_16x16x32_bf16 v[68:71], a[212:215], a[180:183], v[68:71]\n\t"
"v_mfma_f32_16x16x32_bf16 v[232:235], v[232:235], v[220:223], 0\n\t"
"v_mfma_f32_16x16x32_bf16 v[72:75], a[220:223], a[176:179], v[72:75]\n\t"
"ds_read_b128 a[176:179], v244 offset:34816\n\t"
"v_mfma_f32_16x16x32_bf16 v[72:75], a[224:227], a[180:183], v[72:75]\n\t"
"v_mfma_f32_16x16x32_bf16 v[72:75], a[212:215], a[184:187], v[72:75]\n\t"
"v_mfma_f32_16x16x32_bf16 v[76:79], a[220:223], a[180:183], v[76:79]\n\t"
"ds_read_b128 a[180:183], v244 offset:35328\n\t"
"v_mfma_f32_16x16x32_bf16 v[76:79], a[224:227], a[184:187], v[76:79]\n\t"
"v_cvt_pk_bf16_f32 v230, v232, v233\n\t"
"v_cvt_pk_bf16_f32 v231, v234, v235\n\t"
"v_mfma_f32_16x16x32_bf16 v[76:79], a[212:215], a[188:191], v[76:79]\n\t"
"ds_read_b64_tr_b16 v[232:233], v246 offset:27648\n\t"
"v_mfma_f32_16x16x32_bf16 v[80:83], a[220:223], a[184:187], v[80:83]\n\t"
"ds_read_b64_tr_b16 v[234:235], v246 offset:30720\n\t"
"v_mfma_f32_16x16x32_bf16 v[80:83], a[224:227], a[188:191], v[80:83]\n\t"
"ds_read_b128 a[184:187], v244 offset:35840\n\t"
"v_mfma_f32_16x16x32_bf16 v[80:83], a[212:215], a[192:195], v[80:83]\n\t"
"v_mfma_f32_16x16x32_bf16 v[84:87], a[220:223], a[188:191], v[84:87]\n\t"
"ds_read_b128 a[188:191], v244 offset:36352\n\t"
"v_mfma_f32_16x16x32_bf16 v[84:87], a[224:227], a[192:195], v[84:87]\n\t"
"v_mfma_f32_16x16x32_bf16 v[84:87], a[212:215], a[196:199], v[84:87]\n\t"
"v_mfma_f32_16x16x32_bf16 v[88:91], a[220:223], a[192:195], v[88:91]\n\t"
"ds_read_b128 a[192:195], v244 offset:36864\n\t"
"v_mfma_f32_16x16x32_bf16 v[88:91], a[224:227], a[196:199], v[88:91]\n\t"
"v_mfma_f32_16x16x32_bf16 v[88:91], a[212:215], a[200:203], v[88:91]\n\t"
"v_mfma_f32_16x16x32_bf16 v[92:95], a[220:223], a[196:199], v[92:95]\n\t"
"buffer_load_dwordx4 a[220:223], v252, s[8:11], s20 offen offset:1024 sc1\n\t"
"v_mfma_f32_16x16x32_bf16 v[92:95], a[224:227], a[200:203], v[92:95]\n\t"
"s_waitcnt vmcnt(11) lgkmcnt(3)\n\t"
"ds_read_b128 a[196:199], v244 offset:37376\n\t"
"v_mfma_f32_16x16x32_bf16 v[92:95], a[212:215], a[204:207], v[92:95]\n\t"
"ds_read_b128 a[200:203], v244 offset:37888\n\t"
"v_mfma_f32_16x16x32_bf16 v[232:235], v[232:235], v[220:223], 0\n\t"
"ds_read_b128 a[204:207], v244 offset:38400\n\t"
"v_mfma_f32_16x16x32_bf16 v[100:103], a[228:231], a[168:171], v[100:103]\n\t"
"buffer_load_dwordx4 a[212:215], v252, s[8:11], s20 offen offset:2048 sc1\n\t"
"v_mfma_f32_16x16x32_bf16 v[100:103], a[232:235], a[172:175], v[100:103]\n\t"
"s_mov_b32 s20, 1609728\n\t"
"v_mfma_f32_16x16x32_bf16 v[100:103], a[216:219], a[176:179], v[100:103]\n\t"
"buffer_load_dwordx4 a[224:227], v252, s[8:11], s20 offen offset:0 sc1\n\t"
"v_mfma_f32_16x16x32_bf16 v[104:107], a[228:231], a[172:175], v[104:107]\n\t"
"s_waitcnt lgkmcnt(5)\n\t"
"v_mfma_f32_16x16x32_bf16 v[104:107], a[232:235], a[176:179], v[104:107]\n\t"
"v_cvt_pk_bf16_f32 v232, v232, v233\n\t"
"v_mfma_f32_16x16x32_bf16 v[104:107], a[216:219], a[180:183], v[104:107]\n\t"
"s_waitcnt lgkmcnt(0)\n\t"
"s_barrier\n\t"
"v_mfma_f32_16x16x32_bf16 v[108:111], a[228:231], a[176:179], v[108:111]\n\t"
"v_cvt_pk_bf16_f32 v233, v234, v235\n\t"
"v_mfma_f32_16x16x32_bf16 v[108:111], a[232:235], a[180:183], v[108:111]\n\t"
"v_fma_f32 v0, v100, s52, v0\n\t"
"v_fma_f32 v1, v101, s52, v1\n\t"
"v_mfma_f32_16x16x32_bf16 v[108:111], a[216:219], a[184:187], v[108:111]\n\t"
"ds_write2st64_b64 v245, v[224:225], v[226:227] offset0:48 offset1:49\n\t"
"v_mfma_f32_16x16x32_bf16 v[112:115], a[228:231], a[180:183], v[112:115]\n\t"
"v_fma_f32 v2, v102, s52, v2\n\t"
"v_fma_f32 v3, v103, s52, v3\n\t"
"v_mfma_f32_16x16x32_bf16 v[112:115], a[232:235], a[184:187], v[112:115]\n\t"
"ds_read_b64_tr_b16 v[224:225], v246 offset:33792\n\t"
"v_mfma_f32_16x16x32_bf16 v[112:115], a[216:219], a[188:191], v[112:115]\n\t"
"ds_write2st64_b64 v245, v[228:229], v[230:231] offset0:50 offset1:51\n\t"
"v_mfma_f32_16x16x32_bf16 v[116:119], a[228:231], a[184:187], v[116:119]\n\t"
"v_fma_f32 v100, v0, s54, -v100\n\t"
"v_fma_f32 v101, v1, s54, -v101\n\t"
"v_mfma_f32_16x16x32_bf16 v[116:119], a[232:235], a[188:191], v[116:119]\n\t"
"ds_read_b64_tr_b16 v[226:227], v246 offset:36864\n\t"
"v_mfma_f32_16x16x32_bf16 v[116:119], a[216:219], a[192:195], v[116:119]\n\t"
"ds_write_b64 v245, v[232:233] offset:26624\n\t"
"v_mfma_f32_16x16x32_bf16 v[120:123], a[228:231], a[188:191], v[120:123]\n\t"
"ds_read_b64_tr_b16 v[228:229], v246 offset:41472\n\t"
"v_fma_f32 v102, v2, s54, -v102\n\t"
"v_mfma_f32_16x16x32_bf16 v[120:123], a[232:235], a[192:195], v[120:123]\n\t"
"ds_read_b64_tr_b16 v[230:231], v246 offset:44544\n\t"
"v_fma_f32 v103, v3, s54, -v103\n\t"
"v_mfma_f32_16x16x32_bf16 v[120:123], a[216:219], a[196:199], v[120:123]\n\t"
"s_waitcnt lgkmcnt(3)\n\t"
"ds_read_b64_tr_b16 v[232:233], v246 offset:47616\n\t"
"v_fma_f32 v4, v104, s52, v4\n\t"
"v_mfma_f32_16x16x32_bf16 v[224:227], v[224:227], v[220:223], 0\n\t"
"ds_read_b64_tr_b16 v[234:235], v246 offset:50688\n\t"
"v_fma_f32 v5, v105, s52, v5\n\t"
"v_mfma_f32_16x16x32_bf16 v[124:127], a[228:231], a[192:195], v[124:127]\n\t"
"v_fma_f32 v6, v106, s52, v6\n\t"
"v_fma_f32 v7, v107, s52, v7\n\t"
"v_mfma_f32_16x16x32_bf16 v[124:127], a[232:235], a[196:199], v[124:127]\n\t"
"v_fma_f32 v104, v4, s54, -v104\n\t"
"v_fma_f32 v105, v5, s54, -v105\n\t"
"v_mfma_f32_16x16x32_bf16 v[124:127], a[216:219], a[200:203], v[124:127]\n\t"
"s_waitcnt lgkmcnt(2)\n\t"
"v_fma_f32 v106, v6, s54, -v106\n\t"
"v_fma_f32 v107, v7, s54, -v107\n\t"
"v_mfma_f32_16x16x32_bf16 v[228:231], v[228:231], v[220:223], 0\n\t"
"v_fma_f32 v8, v108, s52, v8\n\t"
"v_fma_f32 v9, v109, s52, v9\n\t"
"v_mfma_f32_16x16x32_bf16 v[128:131], a[228:231], a[196:199], v[128:131]\n\t"
"s_waitcnt lgkmcnt(0)\n\t"
"v_cvt_pk_bf16_f32 v224, v224, v225\n\t"
"v_cvt_pk_bf16_f32 v225, v226, v227\n\t"
"v_mfma_f32_16x16x32_bf16 v[128:131], a[232:235], a[200:203], v[128:131]\n\t"
"s_waitcnt vmcnt(10)\n\t"
"v_fma_f32 v10, v110, s52, v10\n\t"
"v_fma_f32 v11, v111, s52, v11\n\t"
"v_mfma_f32_16x16x32_bf16 v[128:131], a[216:219], a[204:207], v[128:131]\n\t"
"v_fma_f32 v108, v8, s54, -v108\n\t"
"v_fma_f32 v109, v9, s54, -v109\n\t"
"v_mfma_f32_16x16x32_bf16 v[232:235], v[232:235], v[220:223], 0\n\t"
"buffer_load_dwordx4 a[228:231], v252, s[8:11], s20 offen offset:1024 sc1\n\t"
"v_mfma_f32_16x16x32_bf16 v[132:135], a[236:239], a[168:171], v[132:135]\n\t"
"buffer_load_dwordx4 a[216:219], v252, s[8:11], s20 offen offset:2048 sc1\n\t"
"v_mfma_f32_16x16x32_bf16 v[132:135], a[240:243], a[172:175], v[132:135]\n\t"
"s_mov_b32 s20, 1622016\n\t"
"v_cvt_pk_bf16_f32 v226, v228, v229\n\t"
"v_cvt_pk_bf16_f32 v227, v230, v231\n\t"
"v_mfma_f32_16x16x32_bf16 v[132:135], a[208:211], a[176:179], v[132:135]\n\t"
"ds_write2st64_b64 v245, v[224:225], v[226:227] offset0:53 offset1:54\n\t"
"v_mfma_f32_16x16x32_bf16 v[136:139], a[236:239], a[172:175], v[136:139]\n\t"
"buffer_load_dwordx4 a[232:235], v252, s[8:11], s20 offen offset:0 sc1\n\t"
"v_mfma_f32_16x16x32_bf16 v[136:139], a[240:243], a[176:179], v[136:139]\n\t"
"ds_read_b64_tr_b16 v[228:229], v246 offset:53760\n\t"
"v_mfma_f32_16x16x32_bf16 v[136:139], a[208:211], a[180:183], v[136:139]\n\t"
"ds_read_b64_tr_b16 v[230:231], v246 offset:56832\n\t"
"v_cvt_pk_bf16_f32 v232, v232, v233\n\t"
"v_mfma_f32_16x16x32_bf16 v[140:143], a[236:239], a[176:179], v[140:143]\n\t"
"v_cvt_pk_bf16_f32 v233, v234, v235\n\t"
"v_fma_f32 v110, v10, s54, -v110\n\t"
"v_mfma_f32_16x16x32_bf16 v[140:143], a[240:243], a[180:183], v[140:143]\n\t"
"v_fma_f32 v111, v11, s54, -v111\n\t"
"v_fma_f32 v12, v112, s52, v12\n\t"
"v_mfma_f32_16x16x32_bf16 v[140:143], a[208:211], a[184:187], v[140:143]\n\t"
"v_fma_f32 v13, v113, s52, v13\n\t"
"v_fma_f32 v14, v114, s52, v14\n\t"
"v_mfma_f32_16x16x32_bf16 v[144:147], a[236:239], a[180:183], v[144:147]\n\t"
"v_fma_f32 v15, v115, s52, v15\n\t"
"v_fma_f32 v112, v12, s54, -v112\n\t"
"v_mfma_f32_16x16x32_bf16 v[144:147], a[240:243], a[184:187], v[144:147]\n\t"
"v_fma_f32 v113, v13, s54, -v113\n\t"
"v_fma_f32 v114, v14, s54, -v114\n\t"
"v_mfma_f32_16x16x32_bf16 v[144:147], a[208:211], a[188:191], v[144:147]\n\t"
"v_fma_f32 v115, v15, s54, -v115\n\t"
"v_fma_f32 v16, v116, s52, v16\n\t"
"v_mfma_f32_16x16x32_bf16 v[148:151], a[236:239], a[184:187], v[148:151]\n\t"
"s_waitcnt lgkmcnt(0)\n\t"
"v_fma_f32 v17, v117, s52, v17\n\t"
"v_fma_f32 v18, v118, s52, v18\n\t"
"v_mfma_f32_16x16x32_bf16 v[148:151], a[240:243], a[188:191], v[148:151]\n\t"
"v_fma_f32 v19, v119, s52, v19\n\t"
"v_fma_f32 v116, v16, s54, -v116\n\t"
"v_mfma_f32_16x16x32_bf16 v[148:151], a[208:211], a[192:195], v[148:151]\n\t"
"v_fma_f32 v117, v17, s54, -v117\n\t"
"v_fma_f32 v118, v18, s54, -v118\n\t"
"v_mfma_f32_16x16x32_bf16 v[228:231], v[228:231], v[220:223], 0\n\t"
"v_fma_f32 v119, v19, s54, -v119\n\t"
"v_fma_f32 v20, v120, s52, v20\n\t"
"v_mfma_f32_16x16x32_bf16 v[152:155], a[236:239], a[188:191], v[152:155]\n\t"
"v_fma_f32 v21, v121, s52, v21\n\t"
"v_fma_f32 v22, v122, s52, v22\n\t"
"v_mfma_f32_16x16x32_bf16 v[152:155], a[240:243], a[192:195], v[152:155]\n\t"
"v_fma_f32 v23, v123, s52, v23\n\t"
"v_fma_f32 v120, v20, s54, -v120\n\t"
"v_mfma_f32_16x16x32_bf16 v[152:155], a[208:211], a[196:199], v[152:155]\n\t"
"v_fma_f32 v121, v21, s54, -v121\n\t"
"v_fma_f32 v122, v22, s54, -v122\n\t"
"v_mfma_f32_16x16x32_bf16 v[156:159], a[236:239], a[192:195], v[156:159]\n\t"
"v_fma_f32 v123, v23, s54, -v123\n\t"
"v_fma_f32 v24, v124, s52, v24\n\t"
"v_mfma_f32_16x16x32_bf16 v[156:159], a[240:243], a[196:199], v[156:159]\n\t"
"v_cvt_pk_bf16_f32 v234, v228, v229\n\t"
"v_cvt_pk_bf16_f32 v235, v230, v231\n\t"
"v_mfma_f32_16x16x32_bf16 v[156:159], a[208:211], a[200:203], v[156:159]\n\t"
"ds_write2st64_b64 v245, v[232:233], v[234:235] offset0:55 offset1:56\n\t"
"v_mfma_f32_16x16x32_bf16 v[160:163], a[236:239], a[196:199], v[160:163]\n\t"
"buffer_load_dwordx4 a[236:239], v252, s[8:11], s20 offen offset:1024 sc1\n\t"
"v_mfma_f32_16x16x32_bf16 v[160:163], a[240:243], a[200:203], v[160:163]\n\t"
"s_waitcnt vmcnt(11)\n\t"
"v_fma_f32 v25, v125, s52, v25\n\t"
"v_fma_f32 v26, v126, s52, v26\n\t"
"v_mfma_f32_16x16x32_bf16 v[160:163], a[208:211], a[204:207], v[160:163]\n\t"
"v_fma_f32 v27, v127, s52, v27\n\t"
"v_fma_f32 v124, v24, s54, -v124\n\t"
"v_mfma_f32_16x16x32_bf16 v[164:167], a[244:247], a[168:171], v[164:167]\n\t"
"v_fma_f32 v125, v25, s54, -v125\n\t"
"v_fma_f32 v126, v26, s54, -v126\n\t"
"v_mfma_f32_16x16x32_bf16 v[164:167], v[96:99], a[172:175], v[164:167]\n\t"
"v_fma_f32 v127, v27, s54, -v127\n\t"
"v_mfma_f32_16x16x32_bf16 v[164:167], a[248:251], a[176:179], v[164:167]\n\t"
"s_waitcnt lgkmcnt(0)\n\t"
"s_barrier\n\t"
"v_mfma_f32_16x16x32_bf16 v[168:171], a[244:247], a[172:175], v[168:171]\n\t"
"ds_read_b128 a[168:171], v244 offset:24576\n\t"
"v_mfma_f32_16x16x32_bf16 v[168:171], v[96:99], a[176:179], v[168:171]\n\t"
"buffer_load_dwordx4 a[208:211], v252, s[8:11], s20 offen offset:2048 sc1\n\t"
"v_mfma_f32_16x16x32_bf16 v[168:171], a[248:251], a[180:183], v[168:171]\n\t"
"s_mov_b32 s20, 1634304\n\t"
"ds_read_b128 a[172:175], v244 offset:25088\n\t"
"v_fma_f32 v28, v128, s52, v28\n\t"
"v_mfma_f32_16x16x32_bf16 v[172:175], a[244:247], a[176:179], v[172:175]\n\t"
"ds_read_b128 a[176:179], v244 offset:25600\n\t"
"v_fma_f32 v29, v129, s52, v29\n\t"
"v_mfma_f32_16x16x32_bf16 v[172:175], v[96:99], a[180:183], v[172:175]\n\t"
"v_fma_f32 v30, v130, s52, v30\n\t"
"v_fma_f32 v31, v131, s52, v31\n\t"
"v_mfma_f32_16x16x32_bf16 v[172:175], a[248:251], a[184:187], v[172:175]\n\t"
"buffer_load_dwordx4 a[240:243], v252, s[8:11], s20 offen offset:0 sc1\n\t"
"v_mfma_f32_16x16x32_bf16 v[176:179], a[244:247], a[180:183], v[176:179]\n\t"
"Lpg22_%=:\n\t"
"s_cbranch_vccnz Lvr3_%=\n\t"
"ds_read_b128 a[180:183], v244 offset:26112\n\t"
"v_fma_f32 v128, v28, s54, -v128\n\t"
"v_mfma_f32_16x16x32_bf16 v[176:179], v[96:99], a[184:187], v[176:179]\n\t"
"v_fma_f32 v129, v29, s54, -v129\n\t"
"v_fma_f32 v130, v30, s54, -v130\n\t"
"v_mfma_f32_16x16x32_bf16 v[176:179], a[248:251], a[188:191], v[176:179]\n\t"
"v_fma_f32 v131, v31, s54, -v131\n\t"
"v_fma_f32 v32, v132, s52, v32\n\t"
"v_mfma_f32_16x16x32_bf16 v[180:183], a[244:247], a[184:187], v[180:183]\n\t"
"ds_read_b128 a[184:187], v244 offset:26624\n\t"
"v_fma_f32 v33, v133, s52, v33\n\t"
"v_mfma_f32_16x16x32_bf16 v[180:183], v[96:99], a[188:191], v[180:183]\n\t"
"v_fma_f32 v34, v134, s52, v34\n\t"
"v_fma_f32 v35, v135, s52, v35\n\t"
"v_mfma_f32_16x16x32_bf16 v[180:183], a[248:251], a[192:195], v[180:183]\n\t"
"v_fma_f32 v132, v32, s54, -v132\n\t"
"v_fma_f32 v133, v33, s54, -v133\n\t"
"v_mfma_f32_16x16x32_bf16 v[184:187], a[244:247], a[188:191], v[184:187]\n\t"
"ds_read_b128 a[188:191], v244 offset:27136\n\t"
"v_fma_f32 v134, v34, s54, -v134\n\t"
"v_mfma_f32_16x16x32_bf16 v[184:187], v[96:99], a[192:195], v[184:187]\n\t"
"v_fma_f32 v135, v35, s54, -v135\n\t"
"v_fma_f32 v36, v136, s52, v36\n\t"
"v_mfma_f32_16x16x32_bf16 v[184:187], a[248:251], a[196:199], v[184:187]\n\t"
"v_fma_f32 v37, v137, s52, v37\n\t"
"v_fma_f32 v38, v138, s52, v38\n\t"
"v_mfma_f32_16x16x32_bf16 v[188:191], a[244:247], a[192:195], v[188:191]\n\t"
"ds_read_b128 a[192:195], v244 offset:27648\n\t"
"v_fma_f32 v39, v139, s52, v39\n\t"
"v_mfma_f32_16x16x32_bf16 v[188:191], v[96:99], a[196:199], v[188:191]\n\t"
"v_fma_f32 v136, v36, s54, -v136\n\t"
"v_fma_f32 v137, v37, s54, -v137\n\t"
"v_mfma_f32_16x16x32_bf16 v[188:191], a[248:251], a[200:203], v[188:191]\n\t"
"v_fma_f32 v138, v38, s54, -v138\n\t"
"v_fma_f32 v139, v39, s54, -v139\n\t"
"v_mfma_f32_16x16x32_bf16 v[192:195], a[244:247], a[196:199], v[192:195]\n\t"
"s_waitcnt vmcnt(10) lgkmcnt(4)\n\t"
"ds_read_b128 a[196:199], v244 offset:28160\n\t"
"v_fma_f32 v40, v140, s52, v40\n\t"
"v_mfma_f32_16x16x32_bf16 v[192:195], v[96:99], a[200:203], v[192:195]\n\t"
"ds_read_b128 a[200:203], v244 offset:28672\n\t"
"v_fma_f32 v41, v141, s52, v41\n\t"
"v_mfma_f32_16x16x32_bf16 v[192:195], a[248:251], a[204:207], v[192:195]\n\t"
"ds_read_b128 a[204:207], v244 offset:29184\n\t"
"v_fma_f32 v42, v142, s52, v42\n\t"
"Lst31_%=:\n\t"
"v_mfma_f32_16x16x32_bf16 v[196:199], v[240:243], a[168:171], v[196:199]\n\t"
"s_waitcnt lgkmcnt(6)\n\t"
"v_fma_f32 v43, v143, s52, v43\n\t"
"v_fma_f32 v140, v40, s54, -v140\n\t"
"v_mfma_f32_16x16x32_bf16 v[196:199], v[236:239], a[172:175], v[196:199]\n\t"
"v_fma_f32 v141, v41, s54, -v141\n\t"
"v_fma_f32 v142, v42, s54, -v142\n\t"
"v_mfma_f32_16x16x32_bf16 v[196:199], v[248:251], a[176:179], v[196:199]\n\t"
"buffer_load_dwordx4 a[244:247], v252, s[8:11], s20 offen offset:1024 sc1\n\t"
"v_mfma_f32_16x16x32_bf16 v[204:207], v[240:243], a[172:175], v[204:207]\n\t"
"buffer_load_dwordx4 a[248:251], v252, s[8:11], s20 offen offset:2048 sc1\n\t"
"v_mfma_f32_16x16x32_bf16 v[204:207], v[236:239], a[176:179], v[204:207]\n\t"
"s_waitcnt lgkmcnt(5)\n\t"
"v_fma_f32 v143, v43, s54, -v143\n\t"
"v_fma_f32 v44, v144, s52, v44\n\t"
"v_mfma_f32_16x16x32_bf16 v[204:207], v[248:251], a[180:183], v[204:207]\n\t"
"v_fma_f32 v45, v145, s52, v45\n\t"
"v_fma_f32 v46, v146, s52, v46\n\t"
"v_mfma_f32_16x16x32_bf16 v[212:215], v[240:243], a[176:179], v[212:215]\n\t"
"s_waitcnt lgkmcnt(4)\n\t"
"v_fma_f32 v47, v147, s52, v47\n\t"
"v_fma_f32 v144, v44, s54, -v144\n\t"
"v_mfma_f32_16x16x32_bf16 v[212:215], v[236:239], a[180:183], v[212:215]\n\t"
"v_fma_f32 v145, v45, s54, -v145\n\t"
"v_fma_f32 v146, v46, s54, -v146\n\t"
"v_mfma_f32_16x16x32_bf16 v[212:215], v[248:251], a[184:187], v[212:215]\n\t"
"v_fma_f32 v147, v47, s54, -v147\n\t"
"v_fma_f32 v48, v148, s52, v48\n\t"
"v_mfma_f32_16x16x32_bf16 a[0:3], v[240:243], a[180:183], a[0:3]\n\t"
"s_waitcnt lgkmcnt(3)\n\t"
"v_fma_f32 v49, v149, s52, v49\n\t"
"v_fma_f32 v50, v150, s52, v50\n\t"
"v_mfma_f32_16x16x32_bf16 a[0:3], v[236:239], a[184:187], a[0:3]\n\t"
"v_fma_f32 v51, v151, s52, v51\n\t"
"v_fma_f32 v148, v48, s54, -v148\n\t"
"v_mfma_f32_16x16x32_bf16 a[0:3], v[248:251], a[188:191], a[0:3]\n\t"
"v_fma_f32 v149, v49, s54, -v149\n\t"
"v_fma_f32 v150, v50, s54, -v150\n\t"
"v_mfma_f32_16x16x32_bf16 a[8:11], v[240:243], a[184:187], a[8:11]\n\t"
"s_waitcnt lgkmcnt(2)\n\t"
"v_fma_f32 v151, v51, s54, -v151\n\t"
"v_fma_f32 v52, v152, s52, v52\n\t"
"v_mfma_f32_16x16x32_bf16 a[8:11], v[236:239], a[188:191], a[8:11]\n\t"
"v_fma_f32 v53, v153, s52, v53\n\t"
"v_fma_f32 v54, v154, s52, v54\n\t"
"v_mfma_f32_16x16x32_bf16 a[8:11], v[248:251], a[192:195], a[8:11]\n\t"
"v_fma_f32 v55, v155, s52, v55\n\t"
"v_fma_f32 v152, v52, s54, -v152\n\t"
"v_mfma_f32_16x16x32_bf16 a[16:19], v[240:243], a[188:191], a[16:19]\n\t"
"s_waitcnt lgkmcnt(1)\n\t"
"v_fma_f32 v153, v53, s54, -v153\n\t"
"v_fma_f32 v154, v54, s54, -v154\n\t"
"v_mfma_f32_16x16x32_bf16 a[16:19], v[236:239], a[192:195], a[16:19]\n\t"
"s_mov_b32 s20, 1646592\n\t"
"v_fma_f32 v155, v55, s54, -v155\n\t"
"v_fma_f32 v56, v156, s52, v56\n\t"
"v_mfma_f32_16x16x32_bf16 a[16:19], v[248:251], a[196:199], a[16:19]\n\t"
"v_fma_f32 v57, v157, s52, v57\n\t"
"v_fma_f32 v58, v158, s52, v58\n\t"
"v_mfma_f32_16x16x32_bf16 a[24:27], v[240:243], a[192:195], a[24:27]\n\t"
"s_waitcnt lgkmcnt(0)\n\t"
"v_fma_f32 v59, v159, s52, v59\n\t"
"v_fma_f32 v156, v56, s54, -v156\n\t"
"v_mfma_f32_16x16x32_bf16 a[24:27], v[236:239], a[196:199], a[24:27]\n\t"
"v_fma_f32 v157, v57, s54, -v157\n\t"
"v_fma_f32 v158, v58, s54, -v158\n\t"
"v_mfma_f32_16x16x32_bf16 a[24:27], v[248:251], a[200:203], a[24:27]\n\t"
"v_fma_f32 v159, v59, s54, -v159\n\t"
"v_fma_f32 v60, v160, s52, v60\n\t"
"v_mfma_f32_16x16x32_bf16 a[32:35], v[240:243], a[196:199], a[32:35]\n\t"
"s_waitcnt vmcnt(9)\n\t"
"v_fma_f32 v61, v161, s52, v61\n\t"
"v_fma_f32 v62, v162, s52, v62\n\t"
"v_mfma_f32_16x16x32_bf16 a[32:35], v[236:239], a[200:203], a[32:35]\n\t"
"v_fma_f32 v63, v163, s52, v63\n\t"
"v_fma_f32 v160, v60, s54, -v160\n\t"
"v_mfma_f32_16x16x32_bf16 a[32:35], v[248:251], a[204:207], a[32:35]\n\t"
"v_fma_f32 v161, v61, s54, -v161\n\t"
"v_fma_f32 v162, v62, s54, -v162\n\t"
"v_mfma_f32_16x16x32_bf16 a[40:43], a[252:255], a[168:171], a[40:43]\n\t"
"v_fma_f32 v163, v63, s54, -v163\n\t"
"v_fma_f32 v64, v164, s52, v64\n\t"
"v_mfma_f32_16x16x32_bf16 a[40:43], a[220:223], a[172:175], a[40:43]\n\t"
"v_fma_f32 v65, v165, s52, v65\n\t"
"v_fma_f32 v66, v166, s52, v66\n\t"
"v_mfma_f32_16x16x32_bf16 a[40:43], a[212:215], a[176:179], a[40:43]\n\t"
"buffer_load_dwordx4 v[240:243], v252, s[8:11], s20 offen offset:0 sc1\n\t"
"v_mfma_f32_16x16x32_bf16 a[48:51], a[252:255], a[172:175], a[48:51]\n\t"
"v_fma_f32 v67, v167, s52, v67\n\t"
"v_fma_f32 v164, v64, s54, -v164\n\t"
"v_mfma_f32_16x16x32_bf16 a[48:51], a[220:223], a[176:179], a[48:51]\n\t"
"v_fma_f32 v165, v65, s54, -v165\n\t"
"v_fma_f32 v166, v66, s54, -v166\n\t"
"v_mfma_f32_16x16x32_bf16 a[48:51], a[212:215], a[180:183], a[48:51]\n\t"
"v_fma_f32 v167, v67, s54, -v167\n\t"
"v_fma_f32 v68, v168, s52, v68\n\t"
"v_mfma_f32_16x16x32_bf16 a[56:59], a[252:255], a[176:179], a[56:59]\n\t"
"v_fma_f32 v69, v169, s52, v69\n\t"
"v_fma_f32 v70, v170, s52, v70\n\t"
"v_mfma_f32_16x16x32_bf16 a[56:59], a[220:223], a[180:183], a[56:59]\n\t"
"v_fma_f32 v71, v171, s52, v71\n\t"
"v_fma_f32 v168, v68, s54, -v168\n\t"
"v_mfma_f32_16x16x32_bf16 a[56:59], a[212:215], a[184:187], a[56:59]\n\t"
"v_fma_f32 v169, v69, s54, -v169\n\t"
"v_fma_f32 v170, v70, s54, -v170\n\t"
"v_mfma_f32_16x16x32_bf16 a[64:67], a[252:255], a[180:183], a[64:67]\n\t"
"v_fma_f32 v171, v71, s54, -v171\n\t"
"v_fma_f32 v72, v172, s52, v72\n\t"
"v_mfma_f32_16x16x32_bf16 a[64:67], a[220:223], a[184:187], a[64:67]\n\t"
"v_fma_f32 v73, v173, s52, v73\n\t"
"v_fma_f32 v74, v174, s52, v74\n\t"
"v_mfma_f32_16x16x32_bf16 a[64:67], a[212:215], a[188:191], a[64:67]\n\t"
"v_fma_f32 v75, v175, s52, v75\n\t"
"v_fma_f32 v172, v72, s54, -v172\n\t"
"v_mfma_f32_16x16x32_bf16 a[72:75], a[252:255], a[184:187], a[72:75]\n\t"
"v_fma_f32 v173, v73, s54, -v173\n\t"
"v_fma_f32 v174, v74, s54, -v174\n\t"
"v_mfma_f32_16x16x32_bf16 a[72:75], a[220:223], a[188:191], a[72:75]\n\t"
"v_fma_f32 v175, v75, s54, -v175\n\t"
"v_fma_f32 v76, v176, s52, v76\n\t"
"v_mfma_f32_16x16x32_bf16 a[72:75], a[212:215], a[192:195], a[72:75]\n\t"
"v_fma_f32 v77, v177, s52, v77\n\t"
"v_fma_f32 v78, v178, s52, v78\n\t"
"v_mfma_f32_16x16x32_bf16 a[80:83], a[252:255], a[188:191], a[80:83]\n\t"
"v_fma_f32 v79, v179, s52, v79\n\t"
"v_fma_f32 v176, v76, s54, -v176\n\t"
"v_mfma_f32_16x16x32_bf16 a[80:83], a[220:223], a[192:195], a[80:83]\n\t"
"v_fma_f32 v177, v77, s54, -v177\n\t"
"v_fma_f32 v178, v78, s54, -v178\n\t"
"v_mfma_f32_16x16x32_bf16 a[80:83], a[212:215], a[196:199], a[80:83]\n\t"
"v_fma_f32 v179, v79, s54, -v179\n\t"
"v_fma_f32 v80, v180, s52, v80\n\t"
"v_mfma_f32_16x16x32_bf16 a[88:91], a[252:255], a[192:195], a[88:91]\n\t"
"v_fma_f32 v81, v181, s52, v81\n\t"
"v_fma_f32 v82, v182, s52, v82\n\t"
"v_mfma_f32_16x16x32_bf16 a[88:91], a[220:223], a[196:199], a[88:91]\n\t"
"v_fma_f32 v83, v183, s52, v83\n\t"
"v_fma_f32 v180, v80, s54, -v180\n\t"
"v_mfma_f32_16x16x32_bf16 a[88:91], a[212:215], a[200:203], a[88:91]\n\t"
"v_fma_f32 v181, v81, s54, -v181\n\t"
"v_fma_f32 v182, v82, s54, -v182\n\t"
"v_mfma_f32_16x16x32_bf16 a[96:99], a[252:255], a[196:199], a[96:99]\n\t"
"s_waitcnt vmcnt(7)\n\t"
"v_fma_f32 v183, v83, s54, -v183\n\t"
"v_fma_f32 v84, v184, s52, v84\n\t"
"v_mfma_f32_16x16x32_bf16 a[96:99], a[220:223], a[200:203], a[96:99]\n\t"
"v_fma_f32 v85, v185, s52, v85\n\t"
"v_fma_f32 v86, v186, s52, v86\n\t"
"v_mfma_f32_16x16x32_bf16 a[96:99], a[212:215], a[204:207], a[96:99]\n\t"
"v_fma_f32 v87, v187, s52, v87\n\t"
"v_fma_f32 v184, v84, s54, -v184\n\t"
"v_mfma_f32_16x16x32_bf16 a[104:107], a[224:227], a[168:171], a[104:107]\n\t"
"ds_read_b128 a[168:171], v244 offset:33792\n\t"
"v_fma_f32 v185, v85, s54, -v185\n\t"
"v_mfma_f32_16x16x32_bf16 a[104:107], a[228:231], a[172:175], a[104:107]\n\t"
"v_fma_f32 v186, v86, s54, -v186\n\t"
"v_fma_f32 v187, v87, s54, -v187\n\t"
"v_mfma_f32_16x16x32_bf16 a[104:107], a[216:219], a[176:179], a[104:107]\n\t"
"buffer_load_dwordx4 a[252:255], v252, s[8:11], s20 offen offset:1024 sc1\n\t"
"v_mfma_f32_16x16x32_bf16 a[112:115], a[224:227], a[172:175], a[112:115]\n\t"
"buffer_load_dwordx4 a[212:215], v252, s[8:11], s20 offen offset:2048 sc1\n\t"
"v_mfma_f32_16x16x32_bf16 a[112:115], a[228:231], a[176:179], a[112:115]\n\t"
"s_mov_b32 s20, 1179648\n\t"
"ds_read_b128 a[172:175], v244 offset:34304\n\t"
"v_fma_f32 v88, v188, s52, v88\n\t"
"v_mfma_f32_16x16x32_bf16 a[112:115], a[216:219], a[180:183], a[112:115]\n\t"
"v_fma_f32 v89, v189, s52, v89\n\t"
"v_fma_f32 v90, v190, s52, v90\n\t"
"v_mfma_f32_16x16x32_bf16 a[120:123], a[224:227], a[176:179], a[120:123]\n\t"
"ds_read_b128 a[176:179], v244 offset:34816\n\t"
"v_fma_f32 v91, v191, s52, v91\n\t"
"v_mfma_f32_16x16x32_bf16 a[120:123], a[228:231], a[180:183], a[120:123]\n\t"
"v_fma_f32 v188, v88, s54, -v188\n\t"
"v_fma_f32 v189, v89, s54, -v189\n\t"
"v_mfma_f32_16x16x32_bf16 a[120:123], a[216:219], a[184:187], a[120:123]\n\t"
"buffer_load_dwordx4 a[220:223], v252, s[8:11], s20 offen offset:0 sc1\n\t"
"v_mfma_f32_16x16x32_bf16 a[128:131], a[224:227], a[180:183], a[128:131]\n\t"
"ds_read_b128 a[180:183], v244 offset:35328\n\t"
"v_fma_f32 v190, v90, s54, -v190\n\t"
"v_mfma_f32_16x16x32_bf16 a[128:131], a[228:231], a[184:187], a[128:131]\n\t"
"v_fma_f32 v191, v91, s54, -v191\n\t"
"v_fma_f32 v92, v192, s52, v92\n\t"
"v_mfma_f32_16x16x32_bf16 a[128:131], a[216:219], a[188:191], a[128:131]\n\t"
"v_fma_f32 v93, v193, s52, v93\n\t"
"v_fma_f32 v94, v194, s52, v94\n\t"
"v_mfma_f32_16x16x32_bf16 a[136:139], a[224:227], a[184:187], a[136:139]\n\t"
"ds_read_b128 a[184:187], v244 offset:35840\n\t"
"v_fma_f32 v95, v195, s52, v95\n\t"
"v_mfma_f32_16x16x32_bf16 a[136:139], a[228:231], a[188:191], a[136:139]\n\t"
"v_fma_f32 v192, v92, s54, -v192\n\t"
"v_fma_f32 v193, v93, s54, -v193\n\t"
"v_mfma_f32_16x16x32_bf16 a[136:139], a[216:219], a[192:195], a[136:139]\n\t"
"v_fma_f32 v194, v94, s54, -v194\n\t"
"v_fma_f32 v195, v95, s54, -v195\n\t"
"v_mfma_f32_16x16x32_bf16 a[144:147], a[224:227], a[188:191], a[144:147]\n\t"
"ds_read_b128 a[188:191], v244 offset:36352\n\t"
"v_mfma_f32_16x16x32_bf16 a[144:147], a[228:231], a[192:195], a[144:147]\n\t"
"v_mfma_f32_16x16x32_bf16 a[144:147], a[216:219], a[196:199], a[144:147]\n\t"
"v_mfma_f32_16x16x32_bf16 a[152:155], a[224:227], a[192:195], a[152:155]\n\t"
"ds_read_b128 a[192:195], v244 offset:36864\n\t"
"v_mfma_f32_16x16x32_bf16 a[152:155], a[228:231], a[196:199], a[152:155]\n\t"
"v_mfma_f32_16x16x32_bf16 a[152:155], a[216:219], a[200:203], a[152:155]\n\t"
"v_mfma_f32_16x16x32_bf16 a[160:163], a[224:227], a[196:199], a[160:163]\n\t"
"buffer_load_dwordx4 a[224:227], v252, s[8:11], s20 offen offset:1024 sc1\n\t"
"v_mfma_f32_16x16x32_bf16 a[160:163], a[228:231], a[200:203], a[160:163]\n\t"
"s_waitcnt vmcnt(8) lgkmcnt(4)\n\t"
"ds_read_b128 a[196:199], v244 offset:37376\n\t"
"v_mfma_f32_16x16x32_bf16 a[160:163], a[216:219], a[204:207], a[160:163]\n\t"
"ds_read_b128 a[200:203], v244 offset:37888\n\t"
"v_mfma_f32_16x16x32_bf16 v[200:203], a[232:235], a[168:171], v[200:203]\n\t"
"s_waitcnt lgkmcnt(5)\n\t"
"ds_read_b128 a[204:207], v244 offset:38400\n\t"
"v_mfma_f32_16x16x32_bf16 v[200:203], a[236:239], a[172:175], v[200:203]\n\t"
"buffer_load_dwordx4 a[216:219], v252, s[8:11], s20 offen offset:2048 sc1\n\t"
"v_mfma_f32_16x16x32_bf16 v[200:203], a[208:211], a[176:179], v[200:203]\n\t"
"s_mov_b32 s20, 1191936\n\t"
"v_mfma_f32_16x16x32_bf16 v[208:211], a[232:235], a[172:175], v[208:211]\n\t"
"buffer_load_dwordx4 a[228:231], v252, s[8:11], s20 offen offset:0 sc1\n\t"
"v_mfma_f32_16x16x32_bf16 v[208:211], a[236:239], a[176:179], v[208:211]\n\t"
"s_waitcnt lgkmcnt(5)\n\t"
"v_mfma_f32_16x16x32_bf16 v[208:211], a[208:211], a[180:183], v[208:211]\n\t"
"s_waitcnt lgkmcnt(0)\n\t"
"s_barrier\n\t"
"v_mfma_f32_16x16x32_bf16 v[216:219], a[232:235], a[176:179], v[216:219]\n\t"
"v_mfma_f32_16x16x32_bf16 v[216:219], a[236:239], a[180:183], v[216:219]\n\t"
"v_mfma_f32_16x16x32_bf16 v[216:219], a[208:211], a[184:187], v[216:219]\n\t"
"v_pk_fma_f32 v[196:197], v[200:201], s[56:57], v[196:197]\n\t"
"v_pk_fma_f32 v[200:201], v[196:197], s[58:59], v[200:201] neg_lo:[0,0,1] neg_hi:[0,0,1]\n\t"
"v_pk_fma_f32 v[228:229], v[0:1], s[60:61], v[196:197]\n\t"
"v_pk_fma_f32 v[0:1], v[196:197], s[60:61], v[0:1]\n\t"
"v_pk_fma_f32 v[196:197], v[100:101], s[62:63], v[200:201]\n\t"
"v_pk_fma_f32 v[100:101], v[200:201], s[62:63], v[100:101]\n\t"
"v_pk_fma_f32 v[198:199], v[202:203], s[56:57], v[198:199]\n\t"
"v_pk_fma_f32 v[202:203], v[198:199], s[58:59], v[202:203] neg_lo:[0,0,1] neg_hi:[0,0,1]\n\t"
"v_pk_fma_f32 v[234:235], v[2:3], s[60:61], v[198:199]\n\t"
"v_pk_fma_f32 v[2:3], v[198:199], s[60:61], v[2:3]\n\t"
"v_pk_fma_f32 v[198:199], v[102:103], s[62:63], v[202:203]\n\t"
"v_pk_fma_f32 v[102:103], v[202:203], s[62:63], v[102:103]\n\t"
"v_mfma_f32_16x16x32_bf16 a[4:7], a[232:235], a[180:183], a[4:7]\n\t"
"v_cvt_pk_bf16_f32 v196, v196, v197\n\t"
"v_cvt_pk_bf16_f32 v200, v228, v229\n\t"
"v_mfma_f32_16x16x32_bf16 a[4:7], a[236:239], a[184:187], a[4:7]\n\t"
"v_cvt_pk_bf16_f32 v197, v198, v199\n\t"
"v_cvt_pk_bf16_f32 v201, v234, v235\n\t"
"v_mfma_f32_16x16x32_bf16 a[4:7], a[208:211], a[188:191], a[4:7]\n\t"
"v_pk_fma_f32 v[204:205], v[208:209], s[56:57], v[204:205]\n\t"
"v_pk_fma_f32 v[208:209], v[204:205], s[58:59], v[208:209] neg_lo:[0,0,1] neg_hi:[0,0,1]\n\t"
"v_pk_fma_f32 v[98:99], v[4:5], s[60:61], v[204:205]\n\t"
"v_pk_fma_f32 v[4:5], v[204:205], s[60:61], v[4:5]\n\t"
"v_pk_fma_f32 v[204:205], v[104:105], s[62:63], v[208:209]\n\t"
"v_pk_fma_f32 v[104:105], v[208:209], s[62:63], v[104:105]\n\t"
"v_mfma_f32_16x16x32_bf16 a[12:15], a[232:235], a[184:187], a[12:15]\n\t"
"v_cvt_pk_bf16_f32 v204, v204, v205\n\t"
"v_cvt_pk_bf16_f32 v208, v98, v99\n\t"
"v_mfma_f32_16x16x32_bf16 a[12:15], a[236:239], a[188:191], a[12:15]\n\t"
"v_mfma_f32_16x16x32_bf16 a[12:15], a[208:211], a[192:195], a[12:15]\n\t"
"v_pk_fma_f32 v[206:207], v[210:211], s[56:57], v[206:207]\n\t"
"v_pk_fma_f32 v[210:211], v[206:207], s[58:59], v[210:211] neg_lo:[0,0,1] neg_hi:[0,0,1]\n\t"
"v_pk_fma_f32 v[228:229], v[6:7], s[60:61], v[206:207]\n\t"
"v_pk_fma_f32 v[6:7], v[206:207], s[60:61], v[6:7]\n\t"
"v_pk_fma_f32 v[206:207], v[106:107], s[62:63], v[210:211]\n\t"
"v_pk_fma_f32 v[106:107], v[210:211], s[62:63], v[106:107]\n\t"
"v_mfma_f32_16x16x32_bf16 a[20:23], a[232:235], a[188:191], a[20:23]\n\t"
"v_cvt_pk_bf16_f32 v205, v206, v207\n\t"
"v_cvt_pk_bf16_f32 v209, v228, v229\n\t"
"v_mfma_f32_16x16x32_bf16 a[20:23], a[236:239], a[192:195], a[20:23]\n\t"
"v_accvgpr_read_b32 v224, a0\n\t"
"v_accvgpr_read_b32 v226, a4\n\t"
"v_mfma_f32_16x16x32_bf16 a[20:23], a[208:211], a[196:199], a[20:23]\n\t"
"v_accvgpr_read_b32 v225, a1\n\t"
"v_accvgpr_read_b32 v227, a5\n\t"
"v_pk_fma_f32 v[212:213], v[216:217], s[56:57], v[212:213]\n\t"
"v_pk_fma_f32 v[216:217], v[212:213], s[58:59], v[216:217] neg_lo:[0,0,1] neg_hi:[0,0,1]\n\t"
"v_pk_fma_f32 v[234:235], v[8:9], s[60:61], v[212:213]\n\t"
"v_pk_fma_f32 v[8:9], v[212:213], s[60:61], v[8:9]\n\t"
"v_pk_fma_f32 v[212:213], v[108:109], s[62:63], v[216:217]\n\t"
"v_pk_fma_f32 v[108:109], v[216:217], s[62:63], v[108:109]\n\t"
"v_mfma_f32_16x16x32_bf16 a[28:31], a[232:235], a[192:195], a[28:31]\n\t"
"v_cvt_pk_bf16_f32 v212, v212, v213\n\t"
"v_cvt_pk_bf16_f32 v216, v234, v235\n\t"
"v_mfma_f32_16x16x32_bf16 a[28:31], a[236:239], a[196:199], a[28:31]\n\t"
"v_accvgpr_read_b32 v230, a2\n\t"
"v_accvgpr_read_b32 v232, a6\n\t"
"v_mfma_f32_16x16x32_bf16 a[28:31], a[208:211], a[200:203], a[28:31]\n\t"
"v_accvgpr_read_b32 v231, a3\n\t"
"v_accvgpr_read_b32 v233, a7\n\t"
"v_pk_fma_f32 v[214:215], v[218:219], s[56:57], v[214:215]\n\t"
"v_pk_fma_f32 v[218:219], v[214:215], s[58:59], v[218:219] neg_lo:[0,0,1] neg_hi:[0,0,1]\n\t"
"v_pk_fma_f32 v[98:99], v[10:11], s[60:61], v[214:215]\n\t"
"v_pk_fma_f32 v[10:11], v[214:215], s[60:61], v[10:11]\n\t"
"v_pk_fma_f32 v[214:215], v[110:111], s[62:63], v[218:219]\n\t"
"v_pk_fma_f32 v[110:111], v[218:219], s[62:63], v[110:111]\n\t"
"v_mfma_f32_16x16x32_bf16 a[36:39], a[232:235], a[196:199], a[36:39]\n\t"
"s_waitcnt vmcnt(7)\n\t"
"v_cvt_pk_bf16_f32 v213, v214, v215\n\t"
"v_cvt_pk_bf16_f32 v217, v98, v99\n\t"
"v_mfma_f32_16x16x32_bf16 a[36:39], a[236:239], a[200:203], a[36:39]\n\t"
"v_accvgpr_read_b32 v236, a8\n\t"
"v_accvgpr_read_b32 v238, a12\n\t"
"v_mfma_f32_16x16x32_bf16 a[36:39], a[208:211], a[204:207], a[36:39]\n\t"
"buffer_load_dwordx4 a[232:235], v252, s[8:11], s20 offen offset:1024 sc1\n\t"
"v_accvgpr_read_b32 v237, a9\n\t"
"v_accvgpr_read_b32 v239, a13\n\t"
"v_pk_fma_f32 v[224:225], v[226:227], s[56:57], v[224:225]\n\t"
"v_pk_fma_f32 v[226:227], v[224:225], s[58:59], v[226:227] neg_lo:[0,0,1] neg_hi:[0,0,1]\n\t"
"v_pk_fma_f32 v[228:229], v[12:13], s[60:61], v[224:225]\n\t"
"v_pk_fma_f32 v[12:13], v[224:225], s[60:61], v[12:13]\n\t"
"v_pk_fma_f32 v[224:225], v[112:113], s[62:63], v[226:227]\n\t"
"v_pk_fma_f32 v[112:113], v[226:227], s[62:63], v[112:113]\n\t"
"v_mfma_f32_16x16x32_bf16 a[44:47], a[240:243], a[168:171], a[44:47]\n\t"
"v_cvt_pk_bf16_f32 v224, v224, v225\n\t"
"v_cvt_pk_bf16_f32 v228, v228, v229\n\t"
"v_mfma_f32_16x16x32_bf16 a[44:47], a[244:247], a[172:175], a[44:47]\n\t"
"v_accvgpr_write_b32 a0, v224\n\t"
"v_accvgpr_write_b32 a4, v228\n\t"
"v_mfma_f32_16x16x32_bf16 a[44:47], a[248:251], a[176:179], a[44:47]\n\t"
"buffer_load_dwordx4 a[208:211], v252, s[8:11], s20 offen offset:2048 sc1\n\t"
"v_accvgpr_read_b32 v224, a10\n\t"
"v_accvgpr_read_b32 v226, a14\n\t"
"s_mov_b32 s20, 1204224\n\t"
"v_pk_fma_f32 v[230:231], v[232:233], s[56:57], v[230:231]\n\t"
"v_pk_fma_f32 v[232:233], v[230:231], s[58:59], v[232:233] neg_lo:[0,0,1] neg_hi:[0,0,1]\n\t"
"v_pk_fma_f32 v[234:235], v[14:15], s[60:61], v[230:231]\n\t"
"v_pk_fma_f32 v[14:15], v[230:231], s[60:61], v[14:15]\n\t"
"v_pk_fma_f32 v[230:231], v[114:115], s[62:63], v[232:233]\n\t"
"v_pk_fma_f32 v[114:115], v[232:233], s[62:63], v[114:115]\n\t"
"v_mfma_f32_16x16x32_bf16 a[52:55], a[240:243], a[172:175], a[52:55]\n\t"
"v_cvt_pk_bf16_f32 v230, v230, v231\n\t"
"v_cvt_pk_bf16_f32 v234, v234, v235\n\t"
"v_mfma_f32_16x16x32_bf16 a[52:55], a[244:247], a[176:179], a[52:55]\n\t"
"v_accvgpr_write_b32 a1, v230\n\t"
"v_accvgpr_write_b32 a5, v234\n\t"
"v_mfma_f32_16x16x32_bf16 a[52:55], a[248:251], a[180:183], a[52:55]\n\t"
"buffer_load_dwordx4 a[236:239], v252, s[8:11], s20 offen offset:0 sc1\n\t"
"v_accvgpr_read_b32 v225, a11\n\t"
"v_accvgpr_read_b32 v227, a15\n\t"
"v_pk_fma_f32 v[236:237], v[238:239], s[56:57], v[236:237]\n\t"
"v_pk_fma_f32 v[238:239], v[236:237], s[58:59], v[238:239] neg_lo:[0,0,1] neg_hi:[0,0,1]\n\t"
"v_pk_fma_f32 v[98:99], v[16:17], s[60:61], v[236:237]\n\t"
"v_pk_fma_f32 v[16:17], v[236:237], s[60:61], v[16:17]\n\t"
"v_pk_fma_f32 v[236:237], v[116:117], s[62:63], v[238:239]\n\t"
"v_pk_fma_f32 v[116:117], v[238:239], s[62:63], v[116:117]\n\t"
"v_mfma_f32_16x16x32_bf16 a[60:63], a[240:243], a[176:179], a[60:63]\n\t"
"v_accvgpr_read_b32 v230, a16\n\t"
"v_accvgpr_read_b32 v232, a20\n\t"
"v_mfma_f32_16x16x32_bf16 a[60:63], a[244:247], a[180:183], a[60:63]\n\t"
"v_accvgpr_read_b32 v231, a17\n\t"
"v_accvgpr_read_b32 v233, a21\n\t"
"v_mfma_f32_16x16x32_bf16 a[60:63], a[248:251], a[184:187], a[60:63]\n\t"
"v_cvt_pk_bf16_f32 v236, v236, v237\n\t"
"v_cvt_pk_bf16_f32 v98, v98, v99\n\t"
"v_accvgpr_write_b32 a8, v236\n\t"
"v_accvgpr_write_b32 a12, v98\n\t"
"v_accvgpr_read_b32 v236, a18\n\t"
"v_pk_fma_f32 v[224:225], v[226:227], s[56:57], v[224:225]\n\t"
"v_pk_fma_f32 v[226:227], v[224:225], s[58:59], v[226:227] neg_lo:[0,0,1] neg_hi:[0,0,1]\n\t"
"v_pk_fma_f32 v[228:229], v[18:19], s[60:61], v[224:225]\n\t"
"v_pk_fma_f32 v[18:19], v[224:225], s[60:61], v[18:19]\n\t"
"v_pk_fma_f32 v[224:225], v[118:119], s[62:63], v[226:227]\n\t"
"v_pk_fma_f32 v[118:119], v[226:227], s[62:63], v[118:119]\n\t"
"v_pk_fma_f32 v[230:231], v[232:233], s[56:57], v[230:231]\n\t"
"v_pk_fma_f32 v[232:233], v[230:231], s[58:59], v[232:233] neg_lo:[0,0,1] neg_hi:[0,0,1]\n\t"
"v_pk_fma_f32 v[234:235], v[20:21], s[60:61], v[230:231]\n\t"
"v_pk_fma_f32 v[20:21], v[230:231], s[60:61], v[20:21]\n\t"
"v_pk_fma_f32 v[230:231], v[120:121], s[62:63], v[232:233]\n\t"
"v_pk_fma_f32 v[120:121], v[232:233], s[62:63], v[120:121]\n\t"
"v_mfma_f32_16x16x32_bf16 a[68:71], a[240:243], a[180:183], a[68:71]\n\t"
"v_accvgpr_read_b32 v238, a22\n\t"
"v_accvgpr_read_b32 v237, a19\n\t"
"v_mfma_f32_16x16x32_bf16 a[68:71], a[244:247], a[184:187], a[68:71]\n\t"
"v_accvgpr_read_b32 v239, a23\n\t"
"v_cvt_pk_bf16_f32 v224, v224, v225\n\t"
"v_mfma_f32_16x16x32_bf16 a[68:71], a[248:251], a[188:191], a[68:71]\n\t"
"v_cvt_pk_bf16_f32 v228, v228, v229\n\t"
"v_cvt_pk_bf16_f32 v230, v230, v231\n\t"
"v_cvt_pk_bf16_f32 v234, v234, v235\n\t"
"v_accvgpr_write_b32 a9, v224\n\t"
"v_accvgpr_write_b32 a13, v228\n\t"
"v_pk_fma_f32 v[236:237], v[238:239], s[56:57], v[236:237]\n\t"
"v_pk_fma_f32 v[238:239], v[236:237], s[58:59], v[238:239] neg_lo:[0,0,1] neg_hi:[0,0,1]\n\t"
"v_pk_fma_f32 v[98:99], v[22:23], s[60:61], v[236:237]\n\t"
"v_pk_fma_f32 v[22:23], v[236:237], s[60:61], v[22:23]\n\t"
"v_pk_fma_f32 v[236:237], v[122:123], s[62:63], v[238:239]\n\t"
"v_pk_fma_f32 v[122:123], v[238:239], s[62:63], v[122:123]\n\t"
"v_mfma_f32_16x16x32_bf16 a[76:79], a[240:243], a[184:187], a[76:79]\n\t"
"v_accvgpr_write_b32 a16, v230\n\t"
"v_accvgpr_write_b32 a20, v234\n\t"
"v_mfma_f32_16x16x32_bf16 a[76:79], a[244:247], a[188:191], a[76:79]\n\t"
"v_accvgpr_read_b32 v224, a24\n\t"
"v_accvgpr_read_b32 v226, a28\n\t"
"v_mfma_f32_16x16x32_bf16 a[76:79], a[248:251], a[192:195], a[76:79]\n\t"
"v_accvgpr_read_b32 v225, a25\n\t"
"v_accvgpr_read_b32 v227, a29\n\t"
"v_cvt_pk_bf16_f32 v236, v236, v237\n\t"
"v_cvt_pk_bf16_f32 v98, v98, v99\n\t"
"v_accvgpr_write_b32 a17, v236\n\t"
"v_pk_fma_f32 v[224:225], v[226:227], s[56:57], v[224:225]\n\t"
"v_pk_fma_f32 v[226:227], v[224:225], s[58:59], v[226:227] neg_lo:[0,0,1] neg_hi:[0,0,1]\n\t"
"v_pk_fma_f32 v[228:229], v[24:25], s[60:61], v[224:225]\n\t"
"v_pk_fma_f32 v[24:25], v[224:225], s[60:61], v[24:25]\n\t"
"v_pk_fma_f32 v[224:225], v[124:125], s[62:63], v[226:227]\n\t"
"v_pk_fma_f32 v[124:125], v[226:227], s[62:63], v[124:125]\n\t"
"v_mfma_f32_16x16x32_bf16 a[84:87], a[240:243], a[188:191], a[84:87]\n\t"
"v_accvgpr_write_b32 a21, v98\n\t"
"v_accvgpr_read_b32 v230, a26\n\t"
"v_mfma_f32_16x16x32_bf16 a[84:87], a[244:247], a[192:195], a[84:87]\n\t"
"v_accvgpr_read_b32 v232, a30\n\t"
"v_accvgpr_read_b32 v231, a27\n\t"
"v_mfma_f32_16x16x32_bf16 a[84:87], a[248:251], a[196:199], a[84:87]\n\t"
"v_accvgpr_read_b32 v233, a31\n\t"
"v_cvt_pk_bf16_f32 v224, v224, v225\n\t"
"v_cvt_pk_bf16_f32 v228, v228, v229\n\t"
"v_accvgpr_write_b32 a24, v224\n\t"
"v_accvgpr_write_b32 a28, v228\n\t"
"v_pk_fma_f32 v[230:231], v[232:233], s[56:57], v[230:231]\n\t"
"v_pk_fma_f32 v[232:233], v[230:231], s[58:59], v[232:233] neg_lo:[0,0,1] neg_hi:[0,0,1]\n\t"
"v_pk_fma_f32 v[234:235], v[26:27], s[60:61], v[230:231]\n\t"
"v_pk_fma_f32 v[26:27], v[230:231], s[60:61], v[26:27]\n\t"
"v_pk_fma_f32 v[230:231], v[126:127], s[62:63], v[232:233]\n\t"
"v_pk_fma_f32 v[126:127], v[232:233], s[62:63], v[126:127]\n\t"
"v_mfma_f32_16x16x32_bf16 a[92:95], a[240:243], a[192:195], a[92:95]\n\t"
"v_accvgpr_read_b32 v236, a32\n\t"
"v_accvgpr_read_b32 v238, a36\n\t"
"v_mfma_f32_16x16x32_bf16 a[92:95], a[244:247], a[196:199], a[92:95]\n\t"
"v_accvgpr_read_b32 v237, a33\n\t"
"v_accvgpr_read_b32 v239, a37\n\t"
"v_mfma_f32_16x16x32_bf16 a[92:95], a[248:251], a[200:203], a[92:95]\n\t"
"v_cvt_pk_bf16_f32 v230, v230, v231\n\t"
"v_cvt_pk_bf16_f32 v234, v234, v235\n\t"
"v_accvgpr_write_b32 a25, v230\n\t"
"v_accvgpr_write_b32 a29, v234\n\t"
"v_accvgpr_read_b32 v224, a34\n\t"
"v_pk_fma_f32 v[236:237], v[238:239], s[56:57], v[236:237]\n\t"
"v_pk_fma_f32 v[238:239], v[236:237], s[58:59], v[238:239] neg_lo:[0,0,1] neg_hi:[0,0,1]\n\t"
"v_pk_fma_f32 v[98:99], v[28:29], s[60:61], v[236:237]\n\t"
"v_pk_fma_f32 v[28:29], v[236:237], s[60:61], v[28:29]\n\t"
"v_pk_fma_f32 v[236:237], v[128:129], s[62:63], v[238:239]\n\t"
"v_pk_fma_f32 v[128:129], v[238:239], s[62:63], v[128:129]\n\t"
"v_mfma_f32_16x16x32_bf16 a[100:103], a[240:243], a[196:199], a[100:103]\n\t"
"s_waitcnt vmcnt(7)\n\t"
"v_accvgpr_read_b32 v226, a38\n\t"
"v_accvgpr_read_b32 v225, a35\n\t"
"v_mfma_f32_16x16x32_bf16 a[100:103], a[244:247], a[200:203], a[100:103]\n\t"
"v_accvgpr_read_b32 v227, a39\n\t"
"v_cvt_pk_bf16_f32 v236, v236, v237\n\t"
"v_mfma_f32_16x16x32_bf16 a[100:103], a[248:251], a[204:207], a[100:103]\n\t"
"buffer_load_dwordx4 a[240:243], v252, s[8:11], s20 offen offset:1024 sc1\n\t"
"v_cvt_pk_bf16_f32 v98, v98, v99\n\t"
"v_accvgpr_write_b32 a32, v236\n\t"
"v_pk_fma_f32 v[224:225], v[226:227], s[56:57], v[224:225]\n\t"
"v_pk_fma_f32 v[226:227], v[224:225], s[58:59], v[226:227] neg_lo:[0,0,1] neg_hi:[0,0,1]\n\t"
"v_pk_fma_f32 v[228:229], v[30:31], s[60:61], v[224:225]\n\t"
"v_pk_fma_f32 v[30:31], v[224:225], s[60:61], v[30:31]\n\t"
"v_pk_fma_f32 v[224:225], v[130:131], s[62:63], v[226:227]\n\t"
"v_pk_fma_f32 v[130:131], v[226:227], s[62:63], v[130:131]\n\t"
"v_mfma_f32_16x16x32_bf16 a[108:111], v[240:243], a[168:171], a[108:111]\n\t"
"ds_read_b128 a[168:171], v255 offset:512\n\t"
"v_accvgpr_write_b32 a36, v98\n\t"
"v_mfma_f32_16x16x32_bf16 a[108:111], a[252:255], a[172:175], a[108:111]\n\t"
"v_accvgpr_read_b32 v230, a40\n\t"
"v_accvgpr_read_b32 v232, a44\n\t"
"v_mfma_f32_16x16x32_bf16 a[108:111], a[212:215], a[176:179], a[108:111]\n\t"
"buffer_load_dwordx4 a[244:247], v252, s[8:11], s20 offen offset:2048 sc1\n\t"
"v_accvgpr_read_b32 v231, a41\n\t"
"v_accvgpr_read_b32 v233, a45\n\t"
"v_cvt_pk_bf16_f32 v224, v224, v225\n\t"
"v_cvt_pk_bf16_f32 v228, v228, v229\n\t"
"s_mov_b32 s20, 1363968\n\t"
"v_pk_fma_f32 v[230:231], v[232:233], s[56:57], v[230:231]\n\t"
"v_pk_fma_f32 v[232:233], v[230:231], s[58:59], v[232:233] neg_lo:[0,0,1] neg_hi:[0,0,1]\n\t"
"v_pk_fma_f32 v[234:235], v[32:33], s[60:61], v[230:231]\n\t"
"v_pk_fma_f32 v[32:33], v[230:231], s[60:61], v[32:33]\n\t"
"v_pk_fma_f32 v[230:231], v[132:133], s[62:63], v[232:233]\n\t"
"v_pk_fma_f32 v[132:133], v[232:233], s[62:63], v[132:133]\n\t"
"v_mfma_f32_16x16x32_bf16 a[116:119], v[240:243], a[172:175], a[116:119]\n\t"
"Lpg23_%=:\n\t"
"s_cbranch_vccnz Lvr3_%=\n\t"
"ds_read_b128 a[172:175], v255 offset:3584\n\t"
"v_accvgpr_write_b32 a33, v224\n\t"
"v_mfma_f32_16x16x32_bf16 a[116:119], a[252:255], a[176:179], a[116:119]\n\t"
"v_accvgpr_write_b32 a37, v228\n\t"
"v_accvgpr_read_b32 v236, a42\n\t"
"v_mfma_f32_16x16x32_bf16 a[116:119], a[212:215], a[180:183], a[116:119]\n\t"
"buffer_load_dwordx4 a[248:251], v252, s[8:11], s20 offen offset:0 sc1\n\t"
"v_accvgpr_read_b32 v238, a46\n\t"
"v_accvgpr_read_b32 v237, a43\n\t"
"v_accvgpr_read_b32 v239, a47\n\t"
"v_pk_fma_f32 v[236:237], v[238:239], s[56:57], v[236:237]\n\t"
"v_pk_fma_f32 v[238:239], v[236:237], s[58:59], v[238:239] neg_lo:[0,0,1] neg_hi:[0,0,1]\n\t"
"v_pk_fma_f32 v[98:99], v[34:35], s[60:61], v[236:237]\n\t"
"v_pk_fma_f32 v[34:35], v[236:237], s[60:61], v[34:35]\n\t"
"v_pk_fma_f32 v[236:237], v[134:135], s[62:63], v[238:239]\n\t"
"v_pk_fma_f32 v[134:135], v[238:239], s[62:63], v[134:135]\n\t"
"v_mfma_f32_16x16x32_bf16 a[124:127], v[240:243], a[176:179], a[124:127]\n\t"
"ds_read_b128 a[176:179], v255 offset:6656\n\t"
"v_cvt_pk_bf16_f32 v198, v230, v231\n\t"
"v_mfma_f32_16x16x32_bf16 a[124:127], a[252:255], a[180:183], a[124:127]\n\t"
"v_cvt_pk_bf16_f32 v202, v234, v235\n\t"
"v_accvgpr_read_b32 v224, a48\n\t"
"v_mfma_f32_16x16x32_bf16 a[124:127], a[212:215], a[184:187], a[124:127]\n\t"
"global_load_dwordx4 v[248:251], v252, s[48:49] offset:0\n\t"
"v_accvgpr_read_b32 v226, a52\n\t"
"v_accvgpr_read_b32 v225, a49\n\t"
"v_accvgpr_read_b32 v227, a53\n\t"
"v_accvgpr_read_b32 v230, a50\n\t"
"v_accvgpr_read_b32 v232, a54\n\t"
"v_accvgpr_read_b32 v231, a51\n\t"
"v_accvgpr_read_b32 v233, a55\n\t"
"v_pk_fma_f32 v[224:225], v[226:227], s[56:57], v[224:225]\n\t"
"v_pk_fma_f32 v[226:227], v[224:225], s[58:59], v[226:227] neg_lo:[0,0,1] neg_hi:[0,0,1]\n\t"
"v_pk_fma_f32 v[228:229], v[36:37], s[60:61], v[224:225]\n\t"
"v_pk_fma_f32 v[36:37], v[224:225], s[60:61], v[36:37]\n\t"
"v_pk_fma_f32 v[224:225], v[136:137], s[62:63], v[226:227]\n\t"
"v_pk_fma_f32 v[136:137], v[226:227], s[62:63], v[136:137]\n\t"
"v_pk_fma_f32 v[230:231], v[232:233], s[56:57], v[230:231]\n\t"
"v_pk_fma_f32 v[232:233], v[230:231], s[58:59], v[232:233] neg_lo:[0,0,1] neg_hi:[0,0,1]\n\t"
"v_pk_fma_f32 v[234:235], v[38:39], s[60:61], v[230:231]\n\t"
"v_pk_fma_f32 v[38:39], v[230:231], s[60:61], v[38:39]\n\t"
"v_pk_fma_f32 v[230:231], v[138:139], s[62:63], v[232:233]\n\t"
"v_pk_fma_f32 v[138:139], v[232:233], s[62:63], v[138:139]\n\t"
"v_mfma_f32_16x16x32_bf16 a[132:135], v[240:243], a[180:183], a[132:135]\n\t"
"ds_read_b128 a[180:183], v255 offset:9728\n\t"
"v_cvt_pk_bf16_f32 v199, v236, v237\n\t"
"v_mfma_f32_16x16x32_bf16 a[132:135], a[252:255], a[184:187], a[132:135]\n\t"
"v_cvt_pk_bf16_f32 v203, v98, v99\n\t"
"v_accvgpr_read_b32 v236, a56\n\t"
"v_mfma_f32_16x16x32_bf16 a[132:135], a[212:215], a[188:191], a[132:135]\n\t"
"buffer_load_dwordx4 a[52:55], v252, s[8:11], s20 offen offset:1024 sc1\n\t"
"v_accvgpr_read_b32 v238, a60\n\t"
"v_accvgpr_read_b32 v237, a57\n\t"
"v_accvgpr_read_b32 v239, a61\n\t"
"v_pk_fma_f32 v[236:237], v[238:239], s[56:57], v[236:237]\n\t"
"v_pk_fma_f32 v[238:239], v[236:237], s[58:59], v[238:239] neg_lo:[0,0,1] neg_hi:[0,0,1]\n\t"
"v_pk_fma_f32 v[98:99], v[40:41], s[60:61], v[236:237]\n\t"
"v_pk_fma_f32 v[40:41], v[236:237], s[60:61], v[40:41]\n\t"
"v_pk_fma_f32 v[236:237], v[140:141], s[62:63], v[238:239]\n\t"
"v_pk_fma_f32 v[140:141], v[238:239], s[62:63], v[140:141]\n\t"
"v_mfma_f32_16x16x32_bf16 a[140:143], v[240:243], a[184:187], a[140:143]\n\t"
"ds_read_b128 a[184:187], v255 offset:12800\n\t"
"v_cvt_pk_bf16_f32 v206, v224, v225\n\t"
"v_mfma_f32_16x16x32_bf16 a[140:143], a[252:255], a[188:191], a[140:143]\n\t"
"v_cvt_pk_bf16_f32 v210, v228, v229\n\t"
"v_cvt_pk_bf16_f32 v207, v230, v231\n\t"
"v_mfma_f32_16x16x32_bf16 a[140:143], a[212:215], a[192:195], a[140:143]\n\t"
"v_accvgpr_read_b32 v224, a58\n\t"
"v_accvgpr_read_b32 v226, a62\n\t"
"v_accvgpr_read_b32 v225, a59\n\t"
"v_accvgpr_read_b32 v227, a63\n\t"
"v_cvt_pk_bf16_f32 v211, v234, v235\n\t"
"v_pk_fma_f32 v[224:225], v[226:227], s[56:57], v[224:225]\n\t"
"v_pk_fma_f32 v[226:227], v[224:225], s[58:59], v[226:227] neg_lo:[0,0,1] neg_hi:[0,0,1]\n\t"
"v_pk_fma_f32 v[228:229], v[42:43], s[60:61], v[224:225]\n\t"
"v_pk_fma_f32 v[42:43], v[224:225], s[60:61], v[42:43]\n\t"
"v_pk_fma_f32 v[224:225], v[142:143], s[62:63], v[226:227]\n\t"
"v_pk_fma_f32 v[142:143], v[226:227], s[62:63], v[142:143]\n\t"
"v_mfma_f32_16x16x32_bf16 a[148:151], v[240:243], a[188:191], a[148:151]\n\t"
"ds_read_b128 a[188:191], v255 offset:15872\n\t"
"v_cvt_pk_bf16_f32 v214, v236, v237\n\t"
"v_mfma_f32_16x16x32_bf16 a[148:151], a[252:255], a[192:195], a[148:151]\n\t"
"v_cvt_pk_bf16_f32 v218, v98, v99\n\t"
"v_accvgpr_read_b32 v230, a64\n\t"
"v_mfma_f32_16x16x32_bf16 a[148:151], a[212:215], a[196:199], a[148:151]\n\t"
"buffer_load_dwordx4 a[56:59], v252, s[8:11], s20 offen offset:2048 sc1\n\t"
"v_accvgpr_read_b32 v232, a68\n\t"
"v_accvgpr_read_b32 v231, a65\n\t"
"v_accvgpr_read_b32 v233, a69\n\t"
"v_pk_fma_f32 v[230:231], v[232:233], s[56:57], v[230:231]\n\t"
"v_pk_fma_f32 v[232:233], v[230:231], s[58:59], v[232:233] neg_lo:[0,0,1] neg_hi:[0,0,1]\n\t"
"v_pk_fma_f32 v[234:235], v[44:45], s[60:61], v[230:231]\n\t"
"v_pk_fma_f32 v[44:45], v[230:231], s[60:61], v[44:45]\n\t"
"v_pk_fma_f32 v[230:231], v[144:145], s[62:63], v[232:233]\n\t"
"v_pk_fma_f32 v[144:145], v[232:233], s[62:63], v[144:145]\n\t"
"v_mfma_f32_16x16x32_bf16 a[156:159], v[240:243], a[192:195], a[156:159]\n\t"
"s_mov_b32 s20, 1376256\n\t"
"ds_read_b128 a[192:195], v255 offset:18944\n\t"
"v_cvt_pk_bf16_f32 v215, v224, v225\n\t"
"v_mfma_f32_16x16x32_bf16 a[156:159], a[252:255], a[196:199], a[156:159]\n\t"
"v_cvt_pk_bf16_f32 v219, v228, v229\n\t"
"v_accvgpr_read_b32 v236, a66\n\t"
"v_mfma_f32_16x16x32_bf16 a[156:159], a[212:215], a[200:203], a[156:159]\n\t"
"buffer_load_dwordx4 a[60:63], v252, s[8:11], s20 offen offset:0 sc1\n\t"
"v_accvgpr_read_b32 v238, a70\n\t"
"v_accvgpr_read_b32 v237, a67\n\t"
"v_accvgpr_read_b32 v239, a71\n\t"
"v_cvt_pk_bf16_f32 v230, v230, v231\n\t"
"v_cvt_pk_bf16_f32 v234, v234, v235\n\t"
"v_pk_fma_f32 v[236:237], v[238:239], s[56:57], v[236:237]\n\t"
"v_pk_fma_f32 v[238:239], v[236:237], s[58:59], v[238:239] neg_lo:[0,0,1] neg_hi:[0,0,1]\n\t"
"v_pk_fma_f32 v[98:99], v[46:47], s[60:61], v[236:237]\n\t"
"v_pk_fma_f32 v[46:47], v[236:237], s[60:61], v[46:47]\n\t"
"v_pk_fma_f32 v[236:237], v[146:147], s[62:63], v[238:239]\n\t"
"v_pk_fma_f32 v[146:147], v[238:239], s[62:63], v[146:147]\n\t"
"v_mfma_f32_16x16x32_bf16 a[164:167], v[240:243], a[196:199], a[164:167]\n\t"
"s_waitcnt vmcnt(11) lgkmcnt(4)\n\t"
"ds_read_b128 a[196:199], v255 offset:22016\n\t"
"v_accvgpr_write_b32 a2, v230\n\t"
"v_mfma_f32_16x16x32_bf16 a[164:167], a[252:255], a[200:203], a[164:167]\n\t"
"ds_read_b128 a[200:203], v255 offset:25088\n\t"
"v_accvgpr_write_b32 a6, v234\n\t"
"v_mfma_f32_16x16x32_bf16 a[164:167], a[212:215], a[204:207], a[164:167]\n\t"
"buffer_load_dwordx4 a[252:255], v252, s[8:11], s20 offen offset:1024 sc1\n\t"
"ds_read_b128 a[204:207], v255 offset:28160\n\t"
"v_accvgpr_read_b32 v224, a72\n\t"
"v_accvgpr_read_b32 v226, a76\n\t"
"v_accvgpr_read_b32 v225, a73\n\t"
"v_accvgpr_read_b32 v227, a77\n\t"
"v_cvt_pk_bf16_f32 v236, v236, v237\n\t"
"v_cvt_pk_bf16_f32 v98, v98, v99\n\t"
"v_pk_fma_f32 v[224:225], v[226:227], s[56:57], v[224:225]\n\t"
"v_pk_fma_f32 v[226:227], v[224:225], s[58:59], v[226:227] neg_lo:[0,0,1] neg_hi:[0,0,1]\n\t"
"v_pk_fma_f32 v[228:229], v[48:49], s[60:61], v[224:225]\n\t"
"v_pk_fma_f32 v[48:49], v[224:225], s[60:61], v[48:49]\n\t"
"v_pk_fma_f32 v[224:225], v[148:149], s[62:63], v[226:227]\n\t"
"v_pk_fma_f32 v[148:149], v[226:227], s[62:63], v[148:149]\n\t"
"Lst32_%=:\n\t"
"v_mfma_f32_16x16x32_bf16 v[0:3], a[220:223], a[168:171], v[0:3]\n\t"
"s_waitcnt lgkmcnt(6)\n\t"
"v_accvgpr_write_b32 a3, v236\n\t"
"v_accvgpr_write_b32 a7, v98\n\t"
"v_mfma_f32_16x16x32_bf16 v[0:3], a[224:227], a[172:175], v[0:3]\n\t"
"v_accvgpr_read_b32 v230, a74\n\t"
"v_accvgpr_read_b32 v232, a78\n\t"
"v_mfma_f32_16x16x32_bf16 v[0:3], a[216:219], a[176:179], v[0:3]\n\t"
"buffer_load_dwordx4 a[212:215], v252, s[8:11], s20 offen offset:2048 sc1\n\t"
"v_accvgpr_read_b32 v231, a75\n\t"
"v_accvgpr_read_b32 v233, a79\n\t"
"v_cvt_pk_bf16_f32 v224, v224, v225\n\t"
"v_cvt_pk_bf16_f32 v228, v228, v229\n\t"
"v_pk_fma_f32 v[230:231], v[232:233], s[56:57], v[230:231]\n\t"
"v_pk_fma_f32 v[232:233], v[230:231], s[58:59], v[232:233] neg_lo:[0,0,1] neg_hi:[0,0,1]\n\t"
"v_pk_fma_f32 v[234:235], v[50:51], s[60:61], v[230:231]\n\t"
"v_pk_fma_f32 v[50:51], v[230:231], s[60:61], v[50:51]\n\t"
"v_pk_fma_f32 v[230:231], v[150:151], s[62:63], v[232:233]\n\t"
"v_pk_fma_f32 v[150:151], v[232:233], s[62:63], v[150:151]\n\t"
"v_mfma_f32_16x16x32_bf16 v[4:7], a[220:223], a[172:175], v[4:7]\n\t"
"s_waitcnt lgkmcnt(5)\n\t"
"s_mov_b32 s20, 1388544\n\t"
"v_accvgpr_write_b32 a10, v224\n\t"
"v_mfma_f32_16x16x32_bf16 v[4:7], a[224:227], a[176:179], v[4:7]\n\t"
"v_accvgpr_write_b32 a14, v228\n\t"
"v_accvgpr_read_b32 v236, a80\n\t"
"v_mfma_f32_16x16x32_bf16 v[4:7], a[216:219], a[180:183], v[4:7]\n\t"
"buffer_load_dwordx4 a[64:67], v252, s[8:11], s20 offen offset:0 sc1\n\t"
"v_accvgpr_read_b32 v238, a84\n\t"
"v_accvgpr_read_b32 v237, a81\n\t"
"v_accvgpr_read_b32 v239, a85\n\t"
"v_cvt_pk_bf16_f32 v230, v230, v231\n\t"
"v_cvt_pk_bf16_f32 v234, v234, v235\n\t"
"v_pk_fma_f32 v[236:237], v[238:239], s[56:57], v[236:237]\n\t"
"v_pk_fma_f32 v[238:239], v[236:237], s[58:59], v[238:239] neg_lo:[0,0,1] neg_hi:[0,0,1]\n\t"
"v_pk_fma_f32 v[98:99], v[52:53], s[60:61], v[236:237]\n\t"
"v_pk_fma_f32 v[52:53], v[236:237], s[60:61], v[52:53]\n\t"
"v_pk_fma_f32 v[236:237], v[152:153], s[62:63], v[238:239]\n\t"
"v_pk_fma_f32 v[152:153], v[238:239], s[62:63], v[152:153]\n\t"
"v_mfma_f32_16x16x32_bf16 v[8:11], a[220:223], a[176:179], v[8:11]\n\t"
"s_waitcnt lgkmcnt(4)\n\t"
"v_accvgpr_write_b32 a11, v230\n\t"
"v_accvgpr_write_b32 a15, v234\n\t"
"v_mfma_f32_16x16x32_bf16 v[8:11], a[224:227], a[180:183], v[8:11]\n\t"
"v_accvgpr_read_b32 v224, a82\n\t"
"v_accvgpr_read_b32 v226, a86\n\t"
"v_mfma_f32_16x16x32_bf16 v[8:11], a[216:219], a[184:187], v[8:11]\n\t"
"buffer_load_dwordx4 a[68:71], v252, s[8:11], s20 offen offset:1024 sc1\n\t"
"v_accvgpr_read_b32 v225, a83\n\t"
"v_accvgpr_read_b32 v227, a87\n\t"
"v_cvt_pk_bf16_f32 v236, v236, v237\n\t"
"v_cvt_pk_bf16_f32 v98, v98, v99\n\t"
"v_pk_fma_f32 v[224:225], v[226:227], s[56:57], v[224:225]\n\t"
"v_pk_fma_f32 v[226:227], v[224:225], s[58:59], v[226:227] neg_lo:[0,0,1] neg_hi:[0,0,1]\n\t"
"v_pk_fma_f32 v[228:229], v[54:55], s[60:61], v[224:225]\n\t"
"v_pk_fma_f32 v[54:55], v[224:225], s[60:61], v[54:55]\n\t"
"v_pk_fma_f32 v[224:225], v[154:155], s[62:63], v[226:227]\n\t"
"v_pk_fma_f32 v[154:155], v[226:227], s[62:63], v[154:155]\n\t"
"v_mfma_f32_16x16x32_bf16 v[12:15], a[220:223], a[180:183], v[12:15]\n\t"
"s_waitcnt lgkmcnt(3)\n\t"
"v_accvgpr_write_b32 a18, v236\n\t"
"v_accvgpr_write_b32 a22, v98\n\t"
"v_mfma_f32_16x16x32_bf16 v[12:15], a[224:227], a[184:187], v[12:15]\n\t"
"v_accvgpr_read_b32 v230, a88\n\t"
"v_accvgpr_read_b32 v232, a92\n\t"
"v_mfma_f32_16x16x32_bf16 v[12:15], a[216:219], a[188:191], v[12:15]\n\t"
"buffer_load_dwordx4 a[72:75], v252, s[8:11], s20 offen offset:2048 sc1\n\t"
"v_accvgpr_read_b32 v231, a89\n\t"
"v_accvgpr_read_b32 v233, a93\n\t"
"v_accvgpr_read_b32 v236, a90\n\t"
"v_accvgpr_read_b32 v238, a94\n\t"
"v_accvgpr_read_b32 v237, a91\n\t"
"v_accvgpr_read_b32 v239, a95\n\t"
"v_cvt_pk_bf16_f32 v224, v224, v225\n\t"
"v_cvt_pk_bf16_f32 v228, v228, v229\n\t"
"v_pk_fma_f32 v[230:231], v[232:233], s[56:57], v[230:231]\n\t"
"v_pk_fma_f32 v[232:233], v[230:231], s[58:59], v[232:233] neg_lo:[0,0,1] neg_hi:[0,0,1]\n\t"
"v_pk_fma_f32 v[234:235], v[56:57], s[60:61], v[230:231]\n\t"
"v_pk_fma_f32 v[56:57], v[230:231], s[60:61], v[56:57]\n\t"
"v_pk_fma_f32 v[230:231], v[156:157], s[62:63], v[232:233]\n\t"
"v_pk_fma_f32 v[156:157], v[232:233], s[62:63], v[156:157]\n\t"
"v_pk_fma_f32 v[236:237], v[238:239], s[56:57], v[236:237]\n\t"
"v_pk_fma_f32 v[238:239], v[236:237], s[58:59], v[238:239] neg_lo:[0,0,1] neg_hi:[0,0,1]\n\t"
"v_pk_fma_f32 v[98:99], v[58:59], s[60:61], v[236:237]\n\t"
"v_pk_fma_f32 v[58:59], v[236:237], s[60:61], v[58:59]\n\t"
"v_pk_fma_f32 v[236:237], v[158:159], s[62:63], v[238:239]\n\t"
"v_pk_fma_f32 v[158:159], v[238:239], s[62:63], v[158:159]\n\t"
"v_mfma_f32_16x16x32_bf16 v[16:19], a[220:223], a[184:187], v[16:19]\n\t"
"s_waitcnt lgkmcnt(2)\n\t"
"s_mov_b32 s20, 589824\n\t"
"v_accvgpr_write_b32 a19, v224\n\t"
"v_mfma_f32_16x16x32_bf16 v[16:19], a[224:227], a[188:191], v[16:19]\n\t"
"v_accvgpr_write_b32 a23, v228\n\t"
"v_accvgpr_read_b32 v224, a96\n\t"
"v_mfma_f32_16x16x32_bf16 v[16:19], a[216:219], a[192:195], v[16:19]\n\t"
"buffer_load_dwordx4 a[76:79], v252, s[8:11], s20 offen offset:0 sc1\n\t"
"v_accvgpr_read_b32 v226, a100\n\t"
"v_accvgpr_read_b32 v225, a97\n\t"
"v_accvgpr_read_b32 v227, a101\n\t"
"v_cvt_pk_bf16_f32 v230, v230, v231\n\t"
"v_cvt_pk_bf16_f32 v234, v234, v235\n\t"
"v_pk_fma_f32 v[224:225], v[226:227], s[56:57], v[224:225]\n\t"
"v_pk_fma_f32 v[226:227], v[224:225], s[58:59], v[226:227] neg_lo:[0,0,1] neg_hi:[0,0,1]\n\t"
"v_pk_fma_f32 v[228:229], v[60:61], s[60:61], v[224:225]\n\t"
"v_pk_fma_f32 v[60:61], v[224:225], s[60:61], v[60:61]\n\t"
"v_pk_fma_f32 v[224:225], v[160:161], s[62:63], v[226:227]\n\t"
"v_pk_fma_f32 v[160:161], v[226:227], s[62:63], v[160:161]\n\t"
"v_mfma_f32_16x16x32_bf16 v[20:23], a[220:223], a[188:191], v[20:23]\n\t"
"s_waitcnt lgkmcnt(1)\n\t"
"v_accvgpr_write_b32 a26, v230\n\t"
"v_cvt_pk_bf16_f32 v236, v236, v237\n\t"
"v_mfma_f32_16x16x32_bf16 v[20:23], a[224:227], a[192:195], v[20:23]\n\t"
"v_accvgpr_write_b32 a30, v234\n\t"
"v_cvt_pk_bf16_f32 v98, v98, v99\n\t"
"v_mfma_f32_16x16x32_bf16 v[20:23], a[216:219], a[196:199], v[20:23]\n\t"
"buffer_load_dwordx4 a[80:83], v252, s[8:11], s20 offen offset:1024 sc1\n\t"
"v_accvgpr_read_b32 v230, a98\n\t"
"v_accvgpr_read_b32 v232, a102\n\t"
"v_accvgpr_read_b32 v231, a99\n\t"
"v_accvgpr_read_b32 v233, a103\n\t"
"v_accvgpr_write_b32 a27, v236\n\t"
"v_accvgpr_write_b32 a31, v98\n\t"
"v_cvt_pk_bf16_f32 v224, v224, v225\n\t"
"v_cvt_pk_bf16_f32 v228, v228, v229\n\t"
"v_pk_fma_f32 v[230:231], v[232:233], s[56:57], v[230:231]\n\t"
"v_pk_fma_f32 v[232:233], v[230:231], s[58:59], v[232:233] neg_lo:[0,0,1] neg_hi:[0,0,1]\n\t"
"v_pk_fma_f32 v[234:235], v[62:63], s[60:61], v[230:231]\n\t"
"v_pk_fma_f32 v[62:63], v[230:231], s[60:61], v[62:63]\n\t"
"v_pk_fma_f32 v[230:231], v[162:163], s[62:63], v[232:233]\n\t"
"v_pk_fma_f32 v[162:163], v[232:233], s[62:63], v[162:163]\n\t"
"v_mfma_f32_16x16x32_bf16 v[24:27], a[220:223], a[192:195], v[24:27]\n\t"
"s_waitcnt lgkmcnt(0)\n\t"
"v_accvgpr_write_b32 a34, v224\n\t"
"v_accvgpr_write_b32 a38, v228\n\t"
"v_mfma_f32_16x16x32_bf16 v[24:27], a[224:227], a[196:199], v[24:27]\n\t"
"v_accvgpr_read_b32 v236, a104\n\t"
"v_accvgpr_read_b32 v238, a108\n\t"
"v_mfma_f32_16x16x32_bf16 v[24:27], a[216:219], a[200:203], v[24:27]\n\t"
"buffer_load_dwordx4 a[84:87], v252, s[8:11], s20 offen offset:2048 sc1\n\t"
"v_accvgpr_read_b32 v237, a105\n\t"
"v_accvgpr_read_b32 v239, a109\n\t"
"v_cvt_pk_bf16_f32 v230, v230, v231\n\t"
"v_cvt_pk_bf16_f32 v234, v234, v235\n\t"
"v_pk_fma_f32 v[236:237], v[238:239], s[56:57], v[236:237]\n\t"
"v_pk_fma_f32 v[238:239], v[236:237], s[58:59], v[238:239] neg_lo:[0,0,1] neg_hi:[0,0,1]\n\t"
"v_pk_fma_f32 v[98:99], v[64:65], s[60:61], v[236:237]\n\t"
"v_pk_fma_f32 v[64:65], v[236:237], s[60:61], v[64:65]\n\t"
"v_pk_fma_f32 v[236:237], v[164:165], s[62:63], v[238:239]\n\t"
"v_pk_fma_f32 v[164:165], v[238:239], s[62:63], v[164:165]\n\t"
"v_mfma_f32_16x16x32_bf16 v[28:31], a[220:223], a[196:199], v[28:31]\n\t"
"s_waitcnt vmcnt(16)\n\t"
"s_mov_b32 s20, 602112\n\t"
"v_accvgpr_write_b32 a35, v230\n\t"
"v_mfma_f32_16x16x32_bf16 v[28:31], a[224:227], a[200:203], v[28:31]\n\t"
"v_accvgpr_write_b32 a39, v234\n\t"
"v_accvgpr_read_b32 v224, a106\n\t"
"v_mfma_f32_16x16x32_bf16 v[28:31], a[216:219], a[204:207], v[28:31]\n\t"
"buffer_load_dwordx4 a[220:223], v252, s[8:11], s20 offen offset:0 sc1\n\t"
"v_accvgpr_read_b32 v226, a110\n\t"
"v_accvgpr_read_b32 v225, a107\n\t"
"v_accvgpr_read_b32 v227, a111\n\t"
"v_cvt_pk_bf16_f32 v236, v236, v237\n\t"
"v_cvt_pk_bf16_f32 v98, v98, v99\n\t"
"v_pk_fma_f32 v[224:225], v[226:227], s[56:57], v[224:225]\n\t"
"v_pk_fma_f32 v[226:227], v[224:225], s[58:59], v[226:227] neg_lo:[0,0,1] neg_hi:[0,0,1]\n\t"
"v_pk_fma_f32 v[228:229], v[66:67], s[60:61], v[224:225]\n\t"
"v_pk_fma_f32 v[66:67], v[224:225], s[60:61], v[66:67]\n\t"
"v_pk_fma_f32 v[224:225], v[166:167], s[62:63], v[226:227]\n\t"
"v_pk_fma_f32 v[166:167], v[226:227], s[62:63], v[166:167]\n\t"
"v_mfma_f32_16x16x32_bf16 v[32:35], a[228:231], a[168:171], v[32:35]\n\t"
"v_accvgpr_write_b32 a104, v236\n\t"
"v_accvgpr_write_b32 a106, v98\n\t"
"v_mfma_f32_16x16x32_bf16 v[32:35], a[232:235], a[172:175], v[32:35]\n\t"
"v_accvgpr_read_b32 v230, a112\n\t"
"v_accvgpr_read_b32 v232, a116\n\t"
"v_mfma_f32_16x16x32_bf16 v[32:35], a[208:211], a[176:179], v[32:35]\n\t"
"buffer_load_dwordx4 a[216:219], v252, s[8:11], s20 offen offset:1024 sc1\n\t"
"v_accvgpr_read_b32 v231, a113\n\t"
"v_accvgpr_read_b32 v233, a117\n\t"
"v_cvt_pk_bf16_f32 v224, v224, v225\n\t"
"v_cvt_pk_bf16_f32 v228, v228, v229\n\t"
"v_pk_fma_f32 v[230:231], v[232:233], s[56:57], v[230:231]\n\t"
"v_pk_fma_f32 v[232:233], v[230:231], s[58:59], v[232:233] neg_lo:[0,0,1] neg_hi:[0,0,1]\n\t"
"v_pk_fma_f32 v[234:235], v[68:69], s[60:61], v[230:231]\n\t"
"v_pk_fma_f32 v[68:69], v[230:231], s[60:61], v[68:69]\n\t"
"v_pk_fma_f32 v[230:231], v[168:169], s[62:63], v[232:233]\n\t"
"v_pk_fma_f32 v[168:169], v[232:233], s[62:63], v[168:169]\n\t"
"v_mfma_f32_16x16x32_bf16 v[36:39], a[228:231], a[172:175], v[36:39]\n\t"
"v_accvgpr_write_b32 a105, v224\n\t"
"v_accvgpr_write_b32 a107, v228\n\t"
"v_mfma_f32_16x16x32_bf16 v[36:39], a[232:235], a[176:179], v[36:39]\n\t"
"v_accvgpr_read_b32 v236, a114\n\t"
"v_accvgpr_read_b32 v238, a118\n\t"
"v_mfma_f32_16x16x32_bf16 v[36:39], a[208:211], a[180:183], v[36:39]\n\t"
"buffer_load_dwordx4 a[224:227], v252, s[8:11], s20 offen offset:2048 sc1\n\t"
"v_accvgpr_read_b32 v237, a115\n\t"
"v_accvgpr_read_b32 v239, a119\n\t"
"v_cvt_pk_bf16_f32 v230, v230, v231\n\t"
"v_cvt_pk_bf16_f32 v234, v234, v235\n\t"
"v_pk_fma_f32 v[236:237], v[238:239], s[56:57], v[236:237]\n\t"
"v_pk_fma_f32 v[238:239], v[236:237], s[58:59], v[238:239] neg_lo:[0,0,1] neg_hi:[0,0,1]\n\t"
"v_pk_fma_f32 v[98:99], v[70:71], s[60:61], v[236:237]\n\t"
"v_pk_fma_f32 v[70:71], v[236:237], s[60:61], v[70:71]\n\t"
"v_pk_fma_f32 v[236:237], v[170:171], s[62:63], v[238:239]\n\t"
"v_pk_fma_f32 v[170:171], v[238:239], s[62:63], v[170:171]\n\t"
"v_mfma_f32_16x16x32_bf16 v[40:43], a[228:231], a[176:179], v[40:43]\n\t"
"s_mov_b32 s20, 614400\n\t"
"v_accvgpr_write_b32 a112, v230\n\t"
"v_accvgpr_write_b32 a114, v234\n\t"
"v_mfma_f32_16x16x32_bf16 v[40:43], a[232:235], a[180:183], v[40:43]\n\t"
"v_accvgpr_read_b32 v224, a120\n\t"
"v_accvgpr_read_b32 v226, a124\n\t"
"v_mfma_f32_16x16x32_bf16 v[40:43], a[208:211], a[184:187], v[40:43]\n\t"
"buffer_load_dwordx4 a[88:91], v252, s[8:11], s20 offen offset:0 sc1\n\t"
"v_accvgpr_read_b32 v225, a121\n\t"
"v_accvgpr_read_b32 v227, a125\n\t"
"v_cvt_pk_bf16_f32 v236, v236, v237\n\t"
"v_cvt_pk_bf16_f32 v98, v98, v99\n\t"
"v_pk_fma_f32 v[224:225], v[226:227], s[56:57], v[224:225]\n\t"
"v_pk_fma_f32 v[226:227], v[224:225], s[58:59], v[226:227] neg_lo:[0,0,1] neg_hi:[0,0,1]\n\t"
"v_pk_fma_f32 v[228:229], v[72:73], s[60:61], v[224:225]\n\t"
"v_pk_fma_f32 v[72:73], v[224:225], s[60:61], v[72:73]\n\t"
"v_pk_fma_f32 v[224:225], v[172:173], s[62:63], v[226:227]\n\t"
"v_pk_fma_f32 v[172:173], v[226:227], s[62:63], v[172:173]\n\t"
"v_mfma_f32_16x16x32_bf16 v[44:47], a[228:231], a[180:183], v[44:47]\n\t"
"v_accvgpr_write_b32 a113, v236\n\t"
"v_accvgpr_write_b32 a115, v98\n\t"
"v_mfma_f32_16x16x32_bf16 v[44:47], a[232:235], a[184:187], v[44:47]\n\t"
"v_accvgpr_read_b32 v230, a122\n\t"
"v_accvgpr_read_b32 v232, a126\n\t"
"v_mfma_f32_16x16x32_bf16 v[44:47], a[208:211], a[188:191], v[44:47]\n\t"
"buffer_load_dwordx4 a[92:95], v252, s[8:11], s20 offen offset:1024 sc1\n\t"
"v_accvgpr_read_b32 v231, a123\n\t"
"v_accvgpr_read_b32 v233, a127\n\t"
"v_accvgpr_read_b32 v236, a128\n\t"
"v_accvgpr_read_b32 v238, a132\n\t"
"v_accvgpr_read_b32 v237, a129\n\t"
"v_accvgpr_read_b32 v239, a133\n\t"
"v_cvt_pk_bf16_f32 v224, v224, v225\n\t"
"v_cvt_pk_bf16_f32 v228, v228, v229\n\t"
"v_pk_fma_f32 v[230:231], v[232:233], s[56:57], v[230:231]\n\t"
"v_pk_fma_f32 v[232:233], v[230:231], s[58:59], v[232:233] neg_lo:[0,0,1] neg_hi:[0,0,1]\n\t"
"v_pk_fma_f32 v[234:235], v[74:75], s[60:61], v[230:231]\n\t"
"v_pk_fma_f32 v[74:75], v[230:231], s[60:61], v[74:75]\n\t"
"v_pk_fma_f32 v[230:231], v[174:175], s[62:63], v[232:233]\n\t"
"v_pk_fma_f32 v[174:175], v[232:233], s[62:63], v[174:175]\n\t"
"v_pk_fma_f32 v[236:237], v[238:239], s[56:57], v[236:237]\n\t"
"v_pk_fma_f32 v[238:239], v[236:237], s[58:59], v[238:239] neg_lo:[0,0,1] neg_hi:[0,0,1]\n\t"
"v_pk_fma_f32 v[98:99], v[76:77], s[60:61], v[236:237]\n\t"
"v_pk_fma_f32 v[76:77], v[236:237], s[60:61], v[76:77]\n\t"
"v_pk_fma_f32 v[236:237], v[176:177], s[62:63], v[238:239]\n\t"
"v_pk_fma_f32 v[176:177], v[238:239], s[62:63], v[176:177]\n\t"
"v_mfma_f32_16x16x32_bf16 v[48:51], a[228:231], a[184:187], v[48:51]\n\t"
"v_accvgpr_write_b32 a120, v224\n\t"
"v_accvgpr_write_b32 a122, v228\n\t"
"v_mfma_f32_16x16x32_bf16 v[48:51], a[232:235], a[188:191], v[48:51]\n\t"
"v_accvgpr_read_b32 v224, a130\n\t"
"v_accvgpr_read_b32 v226, a134\n\t"
"v_mfma_f32_16x16x32_bf16 v[48:51], a[208:211], a[192:195], v[48:51]\n\t"
"buffer_load_dwordx4 a[96:99], v252, s[8:11], s20 offen offset:2048 sc1\n\t"
"v_accvgpr_read_b32 v225, a131\n\t"
"v_accvgpr_read_b32 v227, a135\n\t"
"v_cvt_pk_bf16_f32 v230, v230, v231\n\t"
"v_cvt_pk_bf16_f32 v234, v234, v235\n\t"
"v_cvt_pk_bf16_f32 v236, v236, v237\n\t"
"v_cvt_pk_bf16_f32 v98, v98, v99\n\t"
"v_pk_fma_f32 v[224:225], v[226:227], s[56:57], v[224:225]\n\t"
"v_pk_fma_f32 v[226:227], v[224:225], s[58:59], v[226:227] neg_lo:[0,0,1] neg_hi:[0,0,1]\n\t"
"v_pk_fma_f32 v[228:229], v[78:79], s[60:61], v[224:225]\n\t"
"v_pk_fma_f32 v[78:79], v[224:225], s[60:61], v[78:79]\n\t"
"v_pk_fma_f32 v[224:225], v[178:179], s[62:63], v[226:227]\n\t"
"v_pk_fma_f32 v[178:179], v[226:227], s[62:63], v[178:179]\n\t"
"v_mfma_f32_16x16x32_bf16 v[52:55], a[228:231], a[188:191], v[52:55]\n\t"
"s_mov_b32 s20, 774144\n\t"
"v_accvgpr_write_b32 a121, v230\n\t"
"v_accvgpr_write_b32 a123, v234\n\t"
"v_mfma_f32_16x16x32_bf16 v[52:55], a[232:235], a[192:195], v[52:55]\n\t"
"v_accvgpr_write_b32 a128, v236\n\t"
"v_accvgpr_write_b32 a130, v98\n\t"
"v_mfma_f32_16x16x32_bf16 v[52:55], a[208:211], a[196:199], v[52:55]\n\t"
"s_waitcnt vmcnt(17)\n\t"
"ds_write_b128 v249, v[196:199] offset:18432\n\t"
"v_accvgpr_read_b32 v230, a136\n\t"
"v_accvgpr_read_b32 v232, a140\n\t"
"v_accvgpr_read_b32 v231, a137\n\t"
"v_accvgpr_read_b32 v233, a141\n\t"
"v_cvt_pk_bf16_f32 v224, v224, v225\n\t"
"v_cvt_pk_bf16_f32 v228, v228, v229\n\t"
"v_pk_fma_f32 v[230:231], v[232:233], s[56:57], v[230:231]\n\t"
"v_pk_fma_f32 v[232:233], v[230:231], s[58:59], v[232:233] neg_lo:[0,0,1] neg_hi:[0,0,1]\n\t"
"v_pk_fma_f32 v[234:235], v[80:81], s[60:61], v[230:231]\n\t"
"v_pk_fma_f32 v[80:81], v[230:231], s[60:61], v[80:81]\n\t"
"v_pk_fma_f32 v[230:231], v[180:181], s[62:63], v[232:233]\n\t"
"v_pk_fma_f32 v[180:181], v[232:233], s[62:63], v[180:181]\n\t"
"v_mfma_f32_16x16x32_bf16 v[56:59], a[228:231], a[192:195], v[56:59]\n\t"
"v_accvgpr_write_b32 a129, v224\n\t"
"v_accvgpr_write_b32 a131, v228\n\t"
"v_mfma_f32_16x16x32_bf16 v[56:59], a[232:235], a[196:199], v[56:59]\n\t"
"v_accvgpr_read_b32 v236, a138\n\t"
"v_accvgpr_read_b32 v238, a142\n\t"
"v_mfma_f32_16x16x32_bf16 v[56:59], a[208:211], a[200:203], v[56:59]\n\t"
"buffer_load_dwordx4 a[100:103], v252, s[8:11], s20 offen offset:0 sc1\n\t"
"v_accvgpr_read_b32 v237, a139\n\t"
"v_accvgpr_read_b32 v239, a143\n\t"
"v_cvt_pk_bf16_f32 v230, v230, v231\n\t"
"v_cvt_pk_bf16_f32 v234, v234, v235\n\t"
"v_pk_fma_f32 v[236:237], v[238:239], s[56:57], v[236:237]\n\t"
"v_pk_fma_f32 v[238:239], v[236:237], s[58:59], v[238:239] neg_lo:[0,0,1] neg_hi:[0,0,1]\n\t"
"v_pk_fma_f32 v[98:99], v[82:83], s[60:61], v[236:237]\n\t"
"v_pk_fma_f32 v[82:83], v[236:237], s[60:61], v[82:83]\n\t"
"v_pk_fma_f32 v[236:237], v[182:183], s[62:63], v[238:239]\n\t"
"v_pk_fma_f32 v[182:183], v[238:239], s[62:63], v[182:183]\n\t"
"v_mfma_f32_16x16x32_bf16 v[60:63], a[228:231], a[196:199], v[60:63]\n\t"
"v_accvgpr_write_b32 a136, v230\n\t"
"v_accvgpr_write_b32 a138, v234\n\t"
"v_mfma_f32_16x16x32_bf16 v[60:63], a[232:235], a[200:203], v[60:63]\n\t"
"v_accvgpr_read_b32 v224, a144\n\t"
"v_accvgpr_read_b32 v226, a148\n\t"
"v_mfma_f32_16x16x32_bf16 v[60:63], a[208:211], a[204:207], v[60:63]\n\t"
"buffer_load_dwordx4 a[108:111], v252, s[8:11], s20 offen offset:1024 sc1\n\t"
"v_accvgpr_read_b32 v225, a145\n\t"
"v_accvgpr_read_b32 v227, a149\n\t"
"v_cvt_pk_bf16_f32 v236, v236, v237\n\t"
"v_cvt_pk_bf16_f32 v98, v98, v99\n\t"
"v_pk_fma_f32 v[224:225], v[226:227], s[56:57], v[224:225]\n\t"
"v_pk_fma_f32 v[226:227], v[224:225], s[58:59], v[226:227] neg_lo:[0,0,1] neg_hi:[0,0,1]\n\t"
"v_pk_fma_f32 v[228:229], v[84:85], s[60:61], v[224:225]\n\t"
"v_pk_fma_f32 v[84:85], v[224:225], s[60:61], v[84:85]\n\t"
"v_pk_fma_f32 v[224:225], v[184:185], s[62:63], v[226:227]\n\t"
"v_pk_fma_f32 v[184:185], v[226:227], s[62:63], v[184:185]\n\t"
"v_mfma_f32_16x16x32_bf16 v[64:67], a[236:239], a[168:171], v[64:67]\n\t"
"ds_read_b128 a[168:171], v255 offset:2048\n\t"
"v_accvgpr_write_b32 a137, v236\n\t"
"v_mfma_f32_16x16x32_bf16 v[64:67], a[240:243], a[172:175], v[64:67]\n\t"
"v_accvgpr_write_b32 a139, v98\n\t"
"v_accvgpr_read_b32 v230, a146\n\t"
"v_mfma_f32_16x16x32_bf16 v[64:67], a[244:247], a[176:179], v[64:67]\n\t"
"ds_write_b64 v250, a[104:105] offset:18432\n\t"
"v_accvgpr_read_b32 v232, a150\n\t"
"v_accvgpr_read_b32 v231, a147\n\t"
"v_accvgpr_read_b32 v233, a151\n\t"
"v_cvt_pk_bf16_f32 v224, v224, v225\n\t"
"v_cvt_pk_bf16_f32 v228, v228, v229\n\t"
"v_pk_fma_f32 v[230:231], v[232:233], s[56:57], v[230:231]\n\t"
"v_pk_fma_f32 v[232:233], v[230:231], s[58:59], v[232:233] neg_lo:[0,0,1] neg_hi:[0,0,1]\n\t"
"v_pk_fma_f32 v[234:235], v[86:87], s[60:61], v[230:231]\n\t"
"v_pk_fma_f32 v[86:87], v[230:231], s[60:61], v[86:87]\n\t"
"v_pk_fma_f32 v[230:231], v[186:187], s[62:63], v[232:233]\n\t"
"v_pk_fma_f32 v[186:187], v[232:233], s[62:63], v[186:187]\n\t"
"v_mfma_f32_16x16x32_bf16 v[68:71], a[236:239], a[172:175], v[68:71]\n\t"
"ds_read_b128 a[172:175], v255 offset:5120\n\t"
"v_accvgpr_write_b32 a144, v224\n\t"
"v_mfma_f32_16x16x32_bf16 v[68:71], a[240:243], a[176:179], v[68:71]\n\t"
"v_accvgpr_write_b32 a146, v228\n\t"
"v_accvgpr_read_b32 v236, a152\n\t"
"v_mfma_f32_16x16x32_bf16 v[68:71], a[244:247], a[180:183], v[68:71]\n\t"
"buffer_load_dwordx4 a[228:231], v252, s[8:11], s20 offen offset:2048 sc1\n\t"
"v_accvgpr_read_b32 v238, a156\n\t"
"v_accvgpr_read_b32 v237, a153\n\t"
"v_accvgpr_read_b32 v239, a157\n\t"
"v_cvt_pk_bf16_f32 v230, v230, v231\n\t"
"v_cvt_pk_bf16_f32 v234, v234, v235\n\t"
"s_mov_b32 s20, 786432\n\t"
"v_pk_fma_f32 v[236:237], v[238:239], s[56:57], v[236:237]\n\t"
"v_pk_fma_f32 v[238:239], v[236:237], s[58:59], v[238:239] neg_lo:[0,0,1] neg_hi:[0,0,1]\n\t"
"v_pk_fma_f32 v[98:99], v[88:89], s[60:61], v[236:237]\n\t"
"v_pk_fma_f32 v[88:89], v[236:237], s[60:61], v[88:89]\n\t"
"v_pk_fma_f32 v[236:237], v[188:189], s[62:63], v[238:239]\n\t"
"v_pk_fma_f32 v[188:189], v[238:239], s[62:63], v[188:189]\n\t"
"v_mfma_f32_16x16x32_bf16 v[72:75], a[236:239], a[176:179], v[72:75]\n\t"
"ds_read_b128 a[176:179], v255 offset:8192\n\t"
"v_accvgpr_write_b32 a145, v230\n\t"
"v_mfma_f32_16x16x32_bf16 v[72:75], a[240:243], a[180:183], v[72:75]\n\t"
"v_accvgpr_write_b32 a147, v234\n\t"
"v_accvgpr_read_b32 v224, a154\n\t"
"v_mfma_f32_16x16x32_bf16 v[72:75], a[244:247], a[184:187], v[72:75]\n\t"
"buffer_load_dwordx4 a[208:211], v252, s[8:11], s20 offen offset:0 sc1\n\t"
"v_accvgpr_read_b32 v226, a158\n\t"
"v_accvgpr_read_b32 v225, a155\n\t"
"v_accvgpr_read_b32 v227, a159\n\t"
"v_cvt_pk_bf16_f32 v236, v236, v237\n\t"
"v_cvt_pk_bf16_f32 v98, v98, v99\n\t"
"Lpg24_%=:\n\t"
"s_cbranch_vccnz Lvr3_%=\n\t"
"v_pk_fma_f32 v[224:225], v[226:227], s[56:57], v[224:225]\n\t"
"v_pk_fma_f32 v[226:227], v[224:225], s[58:59], v[226:227] neg_lo:[0,0,1] neg_hi:[0,0,1]\n\t"
"v_pk_fma_f32 v[228:229], v[90:91], s[60:61], v[224:225]\n\t"
"v_pk_fma_f32 v[90:91], v[224:225], s[60:61], v[90:91]\n\t"
"v_pk_fma_f32 v[224:225], v[190:191], s[62:63], v[226:227]\n\t"
"v_pk_fma_f32 v[190:191], v[226:227], s[62:63], v[190:191]\n\t"
"v_mfma_f32_16x16x32_bf16 v[76:79], a[236:239], a[180:183], v[76:79]\n\t"
"ds_read_b128 a[180:183], v255 offset:11264\n\t"
"v_accvgpr_write_b32 a152, v236\n\t"
"v_mfma_f32_16x16x32_bf16 v[76:79], a[240:243], a[184:187], v[76:79]\n\t"
"v_accvgpr_write_b32 a154, v98\n\t"
"v_accvgpr_read_b32 v230, a160\n\t"
"v_mfma_f32_16x16x32_bf16 v[76:79], a[244:247], a[188:191], v[76:79]\n\t"
"buffer_load_dwordx4 a[232:235], v252, s[8:11], s20 offen offset:1024 sc1\n\t"
"v_accvgpr_read_b32 v232, a164\n\t"
"v_accvgpr_read_b32 v231, a161\n\t"
"v_accvgpr_read_b32 v233, a165\n\t"
"v_cvt_pk_bf16_f32 v224, v224, v225\n\t"
"v_cvt_pk_bf16_f32 v228, v228, v229\n\t"
"v_pk_fma_f32 v[230:231], v[232:233], s[56:57], v[230:231]\n\t"
"v_pk_fma_f32 v[232:233], v[230:231], s[58:59], v[232:233] neg_lo:[0,0,1] neg_hi:[0,0,1]\n\t"
"v_pk_fma_f32 v[234:235], v[92:93], s[60:61], v[230:231]\n\t"
"v_pk_fma_f32 v[92:93], v[230:231], s[60:61], v[92:93]\n\t"
"v_pk_fma_f32 v[230:231], v[192:193], s[62:63], v[232:233]\n\t"
"v_pk_fma_f32 v[192:193], v[232:233], s[62:63], v[192:193]\n\t"
"v_mfma_f32_16x16x32_bf16 v[80:83], a[236:239], a[184:187], v[80:83]\n\t"
"ds_read_b128 a[184:187], v255 offset:14336\n\t"
"v_accvgpr_write_b32 a153, v224\n\t"
"v_mfma_f32_16x16x32_bf16 v[80:83], a[240:243], a[188:191], v[80:83]\n\t"
"v_accvgpr_write_b32 a155, v228\n\t"
"v_accvgpr_read_b32 v236, a162\n\t"
"v_mfma_f32_16x16x32_bf16 v[80:83], a[244:247], a[192:195], v[80:83]\n\t"
"buffer_load_dwordx4 a[116:119], v252, s[8:11], s20 offen offset:2048 sc1\n\t"
"v_accvgpr_read_b32 v238, a166\n\t"
"v_accvgpr_read_b32 v237, a163\n\t"
"v_accvgpr_read_b32 v239, a167\n\t"
"v_cvt_pk_bf16_f32 v230, v230, v231\n\t"
"v_cvt_pk_bf16_f32 v234, v234, v235\n\t"
"s_mov_b32 s20, 798720\n\t"
"v_pk_fma_f32 v[236:237], v[238:239], s[56:57], v[236:237]\n\t"
"v_pk_fma_f32 v[238:239], v[236:237], s[58:59], v[238:239] neg_lo:[0,0,1] neg_hi:[0,0,1]\n\t"
"v_pk_fma_f32 v[98:99], v[94:95], s[60:61], v[236:237]\n\t"
"v_pk_fma_f32 v[94:95], v[236:237], s[60:61], v[94:95]\n\t"
"v_pk_fma_f32 v[236:237], v[194:195], s[62:63], v[238:239]\n\t"
"v_pk_fma_f32 v[194:195], v[238:239], s[62:63], v[194:195]\n\t"
"v_mfma_f32_16x16x32_bf16 v[84:87], a[236:239], a[188:191], v[84:87]\n\t"
"ds_read_b128 a[188:191], v255 offset:17408\n\t"
"v_accvgpr_write_b32 a160, v230\n\t"
"v_mfma_f32_16x16x32_bf16 v[84:87], a[240:243], a[192:195], v[84:87]\n\t"
"v_accvgpr_write_b32 a162, v234\n\t"
"v_cvt_pk_bf16_f32 v236, v236, v237\n\t"
"v_mfma_f32_16x16x32_bf16 v[84:87], a[244:247], a[196:199], v[84:87]\n\t"
"buffer_load_dwordx4 a[124:127], v252, s[8:11], s20 offen offset:0 sc1\n\t"
"v_cvt_pk_bf16_f32 v98, v98, v99\n\t"
"v_mfma_f32_16x16x32_bf16 v[88:91], a[236:239], a[192:195], v[88:91]\n\t"
"ds_read_b128 a[192:195], v255 offset:20480\n\t"
"v_accvgpr_write_b32 a161, v236\n\t"
"v_mfma_f32_16x16x32_bf16 v[88:91], a[240:243], a[196:199], v[88:91]\n\t"
"v_accvgpr_write_b32 a163, v98\n\t"
"v_mfma_f32_16x16x32_bf16 v[88:91], a[244:247], a[200:203], v[88:91]\n\t"
"ds_write_b128 v249, v[200:203] offset:24576\n\t"
"buffer_load_dwordx4 a[132:135], v252, s[8:11], s20 offen offset:1024 sc1\n\t"
"v_mfma_f32_16x16x32_bf16 v[92:95], a[236:239], a[196:199], v[92:95]\n\t"
"buffer_load_dwordx4 a[140:143], v252, s[8:11], s20 offen offset:2048 sc1\n\t"
"v_mfma_f32_16x16x32_bf16 v[92:95], a[240:243], a[200:203], v[92:95]\n\t"
"s_waitcnt vmcnt(24) lgkmcnt(5)\n\t"
"ds_read_b128 a[196:199], v255 offset:23552\n\t"
"s_mov_b32 s20, 884736\n\t"
"v_mfma_f32_16x16x32_bf16 v[92:95], a[244:247], a[204:207], v[92:95]\n\t"
"ds_write_b64 v250, a[106:107] offset:24576\n\t"
"v_mfma_f32_16x16x32_bf16 v[100:103], a[248:251], a[168:171], v[100:103]\n\t"
"buffer_load_dwordx4 a[148:151], v252, s[8:11], s20 offen offset:0 sc1\n\t"
"v_mfma_f32_16x16x32_bf16 v[100:103], a[52:55], a[172:175], v[100:103]\n\t"
"s_waitcnt lgkmcnt(6)\n\t"
"ds_read_b128 a[200:203], v255 offset:26624\n\t"
"v_mfma_f32_16x16x32_bf16 v[100:103], a[56:59], a[176:179], v[100:103]\n\t"
"ds_read_b128 a[204:207], v255 offset:29696\n\t"
"v_mfma_f32_16x16x32_bf16 v[104:107], a[248:251], a[172:175], v[104:107]\n\t"
"buffer_load_dwordx4 a[236:239], v252, s[8:11], s20 offen offset:1024 sc1\n\t"
"v_mfma_f32_16x16x32_bf16 v[104:107], a[52:55], a[176:179], v[104:107]\n\t"
"s_waitcnt lgkmcnt(7)\n\t"
"v_mfma_f32_16x16x32_bf16 v[104:107], a[56:59], a[180:183], v[104:107]\n\t"
"s_waitcnt lgkmcnt(0)\n\t"
"s_barrier\n\t"
"v_mfma_f32_16x16x32_bf16 v[108:111], a[248:251], a[176:179], v[108:111]\n\t"
"ds_read_b128 a[40:43], v252 offset:18432\n\t"
"v_mfma_f32_16x16x32_bf16 v[108:111], a[52:55], a[180:183], v[108:111]\n\t"
"buffer_load_dwordx4 a[240:243], v252, s[8:11], s20 offen offset:2048 sc1\n\t"
"v_mfma_f32_16x16x32_bf16 v[108:111], a[56:59], a[184:187], v[108:111]\n\t"
"s_mov_b32 s20, 1474560\n\t"
"ds_read_b128 a[44:47], v252 offset:19456\n\t"
"v_add_u32 v224, 98304, v249\n\t"
"v_mfma_f32_16x16x32_bf16 v[112:115], a[248:251], a[180:183], v[112:115]\n\t"
"s_add_u32 s29, s22, 384\n\t"
"ds_read_b128 a[48:51], v252 offset:20480\n\t"
"v_add_u32 v225, 98304, v250\n\t"
"v_mfma_f32_16x16x32_bf16 v[112:115], a[52:55], a[184:187], v[112:115]\n\t"
"v_add_u32 v226, 98304, v252\n\t"
"v_mfma_f32_16x16x32_bf16 v[112:115], a[56:59], a[188:191], v[112:115]\n\t"
"buffer_load_dwordx4 a[244:247], v252, s[8:11], s20 offen offset:0 sc1\n\t"
"v_mfma_f32_16x16x32_bf16 v[116:119], a[248:251], a[184:187], v[116:119]\n\t"
"buffer_load_dwordx4 a[104:107], v252, s[8:11], s20 offen offset:1024 sc1\n\t"
"v_mfma_f32_16x16x32_bf16 v[116:119], a[52:55], a[188:191], v[116:119]\n\t"
"buffer_load_dwordx4 a[156:159], v252, s[8:11], s20 offen offset:2048 sc1\n\t"
"v_mfma_f32_16x16x32_bf16 v[116:119], a[56:59], a[192:195], v[116:119]\n\t"
"s_mov_b32 s20, 897024\n\t"
"ds_write_b128 v249, v[204:207] offset:30720\n\t"
"s_waitcnt lgkmcnt(3)\n\t"
"v_mfma_f32_16x16x32_bf16 v[120:123], a[248:251], a[188:191], v[120:123]\n\t"
"buffer_load_dwordx4 a[164:167], v252, s[8:11], s20 offen offset:0 sc1\n\t"
"v_mfma_f32_16x16x32_bf16 v[120:123], a[52:55], a[192:195], v[120:123]\n\t"
"buffer_load_dwordx4 v[240:243], v252, s[8:11], s20 offen offset:1024 sc1\n\t"
"v_mfma_f32_16x16x32_bf16 v[120:123], a[56:59], a[196:199], v[120:123]\n\t"
"s_waitcnt lgkmcnt(2)\n\t"
"ds_write_b128 v224, v[212:215] offset:0\n\t"
"v_mfma_f32_16x16x32_bf16 v[124:127], a[248:251], a[192:195], v[124:127]\n\t"
"s_waitcnt lgkmcnt(2)\n\t"
"v_mfma_f32_16x16x32_bf16 v[124:127], a[52:55], a[196:199], v[124:127]\n\t"
"buffer_load_dwordx4 v[96:99], v252, s[8:11], s20 offen offset:2048 sc1\n\t"
"v_mfma_f32_16x16x32_bf16 v[124:127], a[56:59], a[200:203], v[124:127]\n\t"
"s_mov_b32 s20, 1486848\n\t"
"ds_write_b128 v224, a[0:3] offset:12288\n\t"
"v_mfma_f32_16x16x32_bf16 v[128:131], a[248:251], a[196:199], v[128:131]\n\t"
"s_waitcnt vmcnt(30)\n\t"
"v_mfma_f32_16x16x32_bf16 v[128:131], a[52:55], a[200:203], v[128:131]\n\t"
"buffer_load_dwordx4 a[248:251], v252, s[8:11], s20 offen offset:0 sc1\n\t"
"v_mfma_f32_16x16x32_bf16 v[128:131], a[56:59], a[204:207], v[128:131]\n\t"
"ds_write_b128 v224, a[8:11] offset:24576\n\t"
"v_mfma_f32_16x16x32_bf16 v[132:135], a[60:63], a[168:171], v[132:135]\n\t"
"v_mfma_f32_16x16x32_bf16 v[132:135], a[252:255], a[172:175], v[132:135]\n\t"
"buffer_load_dwordx4 a[52:55], v252, s[8:11], s20 offen offset:1024 sc1\n\t"
"v_mfma_f32_16x16x32_bf16 v[132:135], a[212:215], a[176:179], v[132:135]\n\t"
"ds_write_b128 v224, a[16:19] offset:36864\n\t"
"ds_write_b128 v224, a[24:27] offset:49152\n\t"
"buffer_store_dwordx4 a[40:43], v248, s[16:19], s29 offen offset:0 nt\n\t"
"v_mfma_f32_16x16x32_bf16 v[136:139], a[60:63], a[172:175], v[136:139]\n\t"
"v_mfma_f32_16x16x32_bf16 v[136:139], a[252:255], a[176:179], v[136:139]\n\t"
"buffer_load_dwordx4 a[56:59], v252, s[8:11], s20 offen offset:2048 sc1\n\t"
"v_mfma_f32_16x16x32_bf16 v[136:139], a[212:215], a[180:183], v[136:139]\n\t"
"buffer_store_dwordx4 a[44:47], v248, s[16:19], s29 offen offset:128 nt\n\t"
"ds_write_b64 v250, a[112:113] offset:30720\n\t"
"s_mov_b32 s20, 909312\n\t"
"v_mfma_f32_16x16x32_bf16 v[140:143], a[60:63], a[176:179], v[140:143]\n\t"
"v_mfma_f32_16x16x32_bf16 v[140:143], a[252:255], a[180:183], v[140:143]\n\t"
"v_mfma_f32_16x16x32_bf16 v[140:143], a[212:215], a[184:187], v[140:143]\n\t"
"buffer_store_dwordx4 a[48:51], v248, s[16:19], s29 offen offset:256 nt\n\t"
"ds_write_b64 v225, a[120:121] offset:0\n\t"
"v_mfma_f32_16x16x32_bf16 v[144:147], a[60:63], a[180:183], v[144:147]\n\t"
"v_mfma_f32_16x16x32_bf16 v[144:147], a[252:255], a[184:187], v[144:147]\n\t"
"buffer_load_dwordx4 a[48:51], v252, s[8:11], s20 offen offset:0 sc1\n\t"
"v_mfma_f32_16x16x32_bf16 v[144:147], a[212:215], a[188:191], v[144:147]\n\t"
"ds_write_b64 v225, a[128:129] offset:12288\n\t"
"ds_write_b64 v225, a[136:137] offset:24576\n\t"
"ds_write_b64 v225, a[144:145] offset:36864\n\t"
"ds_write_b64 v225, a[152:153] offset:49152\n\t"
"v_mfma_f32_16x16x32_bf16 v[148:151], a[60:63], a[184:187], v[148:151]\n\t"
"buffer_load_dwordx4 v[236:239], v252, s[8:11], s20 offen offset:1024 sc1\n\t"
"v_mfma_f32_16x16x32_bf16 v[148:151], a[252:255], a[188:191], v[148:151]\n\t"
"v_mfma_f32_16x16x32_bf16 v[148:151], a[212:215], a[192:195], v[148:151]\n\t"
"ds_write_b128 v249, v[208:211] offset:36864\n\t"
"v_mfma_f32_16x16x32_bf16 v[152:155], a[60:63], a[188:191], v[152:155]\n\t"
"v_mfma_f32_16x16x32_bf16 v[152:155], a[252:255], a[192:195], v[152:155]\n\t"
"v_mfma_f32_16x16x32_bf16 v[152:155], a[212:215], a[196:199], v[152:155]\n\t"
"ds_write_b128 v224, v[216:219] offset:6144\n\t"
"v_mfma_f32_16x16x32_bf16 v[156:159], a[60:63], a[192:195], v[156:159]\n\t"
"v_mfma_f32_16x16x32_bf16 v[156:159], a[252:255], a[196:199], v[156:159]\n\t"
"v_mfma_f32_16x16x32_bf16 v[156:159], a[212:215], a[200:203], v[156:159]\n\t"
"ds_write_b128 v224, a[4:7] offset:18432\n\t"
"v_mfma_f32_16x16x32_bf16 v[160:163], a[60:63], a[196:199], v[160:163]\n\t"
"buffer_load_dwordx4 a[60:63], v252, s[8:11], s20 offen offset:2048 sc1\n\t"
"v_mfma_f32_16x16x32_bf16 v[160:163], a[252:255], a[200:203], v[160:163]\n\t"
"s_waitcnt vmcnt(36)\n\t"
"s_mov_b32 s20, 1499136\n\t"
"v_mfma_f32_16x16x32_bf16 v[160:163], a[212:215], a[204:207], v[160:163]\n\t"
"ds_write_b128 v224, a[12:15] offset:30720\n\t"
"ds_write_b128 v224, a[20:23] offset:43008\n\t"
"ds_write_b128 v224, a[28:31] offset:55296\n\t"
"v_mfma_f32_16x16x32_bf16 v[164:167], a[64:67], a[168:171], v[164:167]\n\t"
"buffer_load_dwordx4 a[212:215], v252, s[8:11], s20 offen offset:0 sc1\n\t"
"v_mfma_f32_16x16x32_bf16 v[164:167], a[68:71], a[172:175], v[164:167]\n\t"
"ds_read_b128 a[168:171], v244 offset:0\n\t"
"v_mfma_f32_16x16x32_bf16 v[164:167], a[72:75], a[176:179], v[164:167]\n\t"
"ds_write_b64 v250, a[114:115] offset:36864\n\t"
"v_mfma_f32_16x16x32_bf16 v[168:171], a[64:67], a[172:175], v[168:171]\n\t"
"buffer_load_dwordx4 a[252:255], v252, s[8:11], s20 offen offset:1024 sc1\n\t"
"v_mfma_f32_16x16x32_bf16 v[168:171], a[68:71], a[176:179], v[168:171]\n\t"
"ds_read_b128 a[172:175], v244 offset:512\n\t"
"v_mfma_f32_16x16x32_bf16 v[168:171], a[72:75], a[180:183], v[168:171]\n\t"
"ds_write_b64 v225, a[122:123] offset:6144\n\t"
"v_mfma_f32_16x16x32_bf16 v[172:175], a[64:67], a[176:179], v[172:175]\n\t"
"ds_read_b128 a[176:179], v244 offset:1024\n\t"
"v_mfma_f32_16x16x32_bf16 v[172:175], a[68:71], a[180:183], v[172:175]\n\t"
"v_mfma_f32_16x16x32_bf16 v[172:175], a[72:75], a[184:187], v[172:175]\n\t"
"ds_write_b64 v225, a[130:131] offset:18432\n\t"
"ds_write_b64 v225, a[138:139] offset:30720\n\t"
"ds_write_b64 v225, a[146:147] offset:43008\n\t"
"ds_write_b64 v225, a[154:155] offset:55296\n\t"
"v_mfma_f32_16x16x32_bf16 v[176:179], a[64:67], a[180:183], v[176:179]\n\t"
"ds_read_b128 a[180:183], v244 offset:1536\n\t"
"v_mfma_f32_16x16x32_bf16 v[176:179], a[68:71], a[184:187], v[176:179]\n\t"
"v_mfma_f32_16x16x32_bf16 v[176:179], a[72:75], a[188:191], v[176:179]\n\t"
"s_waitcnt lgkmcnt(1)\n\t"
"s_barrier\n\t"
"v_mfma_f32_16x16x32_bf16 v[180:183], a[64:67], a[184:187], v[180:183]\n\t"
"ds_read_b128 v[196:199], v252 offset:30720\n\t"
"v_mfma_f32_16x16x32_bf16 v[180:183], a[68:71], a[188:191], v[180:183]\n\t"
"ds_read_b128 v[200:203], v252 offset:31744\n\t"
"v_mfma_f32_16x16x32_bf16 v[180:183], a[72:75], a[192:195], v[180:183]\n\t"
"ds_read_b128 a[184:187], v244 offset:2048\n\t"
"v_mfma_f32_16x16x32_bf16 v[184:187], a[64:67], a[188:191], v[184:187]\n\t"
"buffer_load_dwordx4 a[28:31], v252, s[8:11], s20 offen offset:2048 sc1\n\t"
"v_mfma_f32_16x16x32_bf16 v[184:187], a[68:71], a[192:195], v[184:187]\n\t"
"s_mov_b32 s20, 1069056\n\t"
"ds_read_b128 a[40:43], v252 offset:32768\n\t"
"s_add_u32 s29, s22, 196992\n\t"
"v_mfma_f32_16x16x32_bf16 v[184:187], a[72:75], a[196:199], v[184:187]\n\t"
"ds_read_b128 a[188:191], v244 offset:2560\n\t"
"v_mfma_f32_16x16x32_bf16 v[188:191], a[64:67], a[192:195], v[188:191]\n\t"
"buffer_load_dwordx4 a[112:115], v252, s[8:11], s20 offen offset:0 sc1\n\t"
"v_mfma_f32_16x16x32_bf16 v[188:191], a[68:71], a[196:199], v[188:191]\n\t"
"s_waitcnt lgkmcnt(4)\n\t"
"ds_read_b128 a[44:47], v226 offset:0\n\t"
"v_mfma_f32_16x16x32_bf16 v[188:191], a[72:75], a[200:203], v[188:191]\n\t"
"ds_read_b128 a[192:195], v244 offset:3072\n\t"
"v_mfma_f32_16x16x32_bf16 v[192:195], a[64:67], a[196:199], v[192:195]\n\t"
"buffer_load_dwordx4 a[120:123], v252, s[8:11], s20 offen offset:1024 sc1\n\t"
"v_mfma_f32_16x16x32_bf16 v[192:195], a[68:71], a[200:203], v[192:195]\n\t"
"s_waitcnt vmcnt(38)\n\t"
"ds_read_b128 v[204:207], v226 offset:1024\n\t"
"s_waitcnt lgkmcnt(6)\n\t"
"v_mfma_f32_16x16x32_bf16 v[192:195], a[72:75], a[204:207], v[192:195]\n\t"
"ds_write_b128 v249, a[32:35] offset:18432\n\t"
"Lst33_%=:\n\t"
"v_mfma_f32_16x16x32_bf16 v[0:3], a[76:79], a[168:171], v[0:3]\n\t"
"buffer_load_dwordx4 a[64:67], v252, s[8:11], s20 offen offset:2048 sc1\n\t"
"v_mfma_f32_16x16x32_bf16 v[0:3], a[80:83], a[172:175], v[0:3]\n\t"
"s_mov_b32 s20, 1658880\n\t"
"ds_read_b128 a[196:199], v244 offset:3584\n\t"
"v_mfma_f32_16x16x32_bf16 v[0:3], a[84:87], a[176:179], v[0:3]\n\t"
"buffer_store_dwordx4 v[196:199], v248, s[16:19], s29 offen offset:0 nt\n\t"
"ds_read_b128 v[208:211], v226 offset:2048\n\t"
"ds_read_b128 a[200:203], v244 offset:4096\n\t"
"s_waitcnt lgkmcnt(8)\n\t"
"v_mfma_f32_16x16x32_bf16 v[4:7], a[76:79], a[172:175], v[4:7]\n\t"
"s_waitcnt lgkmcnt(6)\n\t"
"ds_read_b128 v[212:215], v226 offset:12288\n\t"
"v_mfma_f32_16x16x32_bf16 v[4:7], a[80:83], a[176:179], v[4:7]\n\t"
"buffer_load_dwordx4 a[68:71], v252, s[8:11], s20 offen offset:0 sc1\n\t"
"v_mfma_f32_16x16x32_bf16 v[4:7], a[84:87], a[180:183], v[4:7]\n\t"
"buffer_store_dwordx4 v[200:203], v248, s[16:19], s29 offen offset:128 nt\n\t"
"ds_read_b128 a[204:207], v244 offset:4608\n\t"
"buffer_load_dwordx4 a[72:75], v252, s[8:11], s20 offen offset:1024 sc1\n\t"
"v_mfma_f32_16x16x32_bf16 v[8:11], a[76:79], a[176:179], v[8:11]\n\t"
"s_waitcnt lgkmcnt(6)\n\t"
"ds_read_b128 v[216:219], v226 offset:13312\n\t"
"v_mfma_f32_16x16x32_bf16 v[8:11], a[80:83], a[180:183], v[8:11]\n\t"
"v_mfma_f32_16x16x32_bf16 v[8:11], a[84:87], a[184:187], v[8:11]\n\t"
"ds_write_b64 v250, a[160:161] offset:18432\n\t"
"v_mfma_f32_16x16x32_bf16 v[12:15], a[76:79], a[180:183], v[12:15]\n\t"
"s_waitcnt lgkmcnt(5)\n\t"
"ds_read_b128 v[196:199], v226 offset:14336\n\t"
"v_mfma_f32_16x16x32_bf16 v[12:15], a[80:83], a[184:187], v[12:15]\n\t"
"v_mfma_f32_16x16x32_bf16 v[12:15], a[84:87], a[188:191], v[12:15]\n\t"
"buffer_store_dwordx4 a[40:43], v248, s[16:19], s29 offen offset:256 nt\n\t"
"ds_write_b64 v250, a[162:163] offset:24576\n\t"
"s_add_u32 s29, s22, 393600\n\t"
"v_mfma_f32_16x16x32_bf16 v[16:19], a[76:79], a[184:187], v[16:19]\n\t"
"s_waitcnt lgkmcnt(5)\n\t"
"v_mfma_f32_16x16x32_bf16 v[16:19], a[80:83], a[188:191], v[16:19]\n\t"
"v_mfma_f32_16x16x32_bf16 v[16:19], a[84:87], a[192:195], v[16:19]\n\t"
"buffer_store_dwordx4 a[44:47], v248, s[16:19], s29 offen offset:0 nt\n\t"
"ds_read_b128 v[200:203], v226 offset:24576\n\t"
"ds_read_b128 a[4:7], v226 offset:36864\n\t"
"v_mfma_f32_16x16x32_bf16 v[20:23], a[76:79], a[188:191], v[20:23]\n\t"
"s_waitcnt lgkmcnt(5)\n\t"
"ds_read_b128 a[16:19], v226 offset:49152\n\t"
"v_mfma_f32_16x16x32_bf16 v[20:23], a[80:83], a[192:195], v[20:23]\n\t"
"ds_read_b128 a[0:3], v226 offset:25600\n\t"
"v_mfma_f32_16x16x32_bf16 v[20:23], a[84:87], a[196:199], v[20:23]\n\t"
"buffer_store_dwordx4 v[204:207], v248, s[16:19], s29 offen offset:128 nt\n\t"
"ds_read_b128 a[8:11], v226 offset:37888\n\t"
"ds_read_b128 a[20:23], v226 offset:50176\n\t"
"v_mfma_f32_16x16x32_bf16 v[24:27], a[76:79], a[192:195], v[24:27]\n\t"
"ds_read_b128 v[204:207], v226 offset:26624\n\t"
"v_mfma_f32_16x16x32_bf16 v[24:27], a[80:83], a[196:199], v[24:27]\n\t"
"ds_read_b128 a[12:15], v226 offset:38912\n\t"
"v_mfma_f32_16x16x32_bf16 v[24:27], a[84:87], a[200:203], v[24:27]\n\t"
"buffer_store_dwordx4 v[208:211], v248, s[16:19], s29 offen offset:256 nt\n\t"
"ds_read_b128 a[24:27], v226 offset:51200\n\t"
"s_add_u32 s29, s22, 590208\n\t"
"v_mfma_f32_16x16x32_bf16 v[28:31], a[76:79], a[196:199], v[28:31]\n\t"
"s_waitcnt vmcnt(44)\n\t"
"v_mfma_f32_16x16x32_bf16 v[28:31], a[80:83], a[200:203], v[28:31]\n\t"
"v_mfma_f32_16x16x32_bf16 v[28:31], a[84:87], a[204:207], v[28:31]\n\t"
"ds_write_b128 v249, a[36:39] offset:24576\n\t"
"v_mfma_f32_16x16x32_bf16 v[32:35], a[220:223], a[168:171], v[32:35]\n\t"
"s_waitcnt lgkmcnt(11)\n\t"
"v_mfma_f32_16x16x32_bf16 v[32:35], a[216:219], a[172:175], v[32:35]\n\t"
"buffer_load_dwordx4 a[40:43], v252, s[8:11], s20 offen offset:2048 sc1\n\t"
"v_mfma_f32_16x16x32_bf16 v[32:35], a[224:227], a[176:179], v[32:35]\n\t"
"buffer_store_dwordx4 v[212:215], v248, s[16:19], s29 offen offset:0 nt\n\t"
"v_mfma_f32_16x16x32_bf16 v[36:39], a[220:223], a[172:175], v[36:39]\n\t"
"s_mov_b32 s20, 1081344\n\t"
"v_mfma_f32_16x16x32_bf16 v[36:39], a[216:219], a[176:179], v[36:39]\n\t"
"v_mfma_f32_16x16x32_bf16 v[36:39], a[224:227], a[180:183], v[36:39]\n\t"
"buffer_store_dwordx4 v[216:219], v248, s[16:19], s29 offen offset:128 nt\n\t"
"v_mfma_f32_16x16x32_bf16 v[40:43], a[220:223], a[176:179], v[40:43]\n\t"
"v_mfma_f32_16x16x32_bf16 v[40:43], a[216:219], a[180:183], v[40:43]\n\t"
"v_mfma_f32_16x16x32_bf16 v[40:43], a[224:227], a[184:187], v[40:43]\n\t"
"buffer_store_dwordx4 v[196:199], v248, s[16:19], s29 offen offset:256 nt\n\t"
"s_add_u32 s29, s22, 786816\n\t"
"v_mfma_f32_16x16x32_bf16 v[44:47], a[220:223], a[180:183], v[44:47]\n\t"
"s_waitcnt lgkmcnt(9)\n\t"
"v_mfma_f32_16x16x32_bf16 v[44:47], a[216:219], a[184:187], v[44:47]\n\t"
"v_mfma_f32_16x16x32_bf16 v[44:47], a[224:227], a[188:191], v[44:47]\n\t"
"s_waitcnt lgkmcnt(0)\n\t"
"s_barrier\n\t"
"v_mfma_f32_16x16x32_bf16 v[48:51], a[220:223], a[184:187], v[48:51]\n\t"
"v_mfma_f32_16x16x32_bf16 v[48:51], a[216:219], a[188:191], v[48:51]\n\t"
"buffer_load_dwordx4 a[44:47], v252, s[8:11], s20 offen offset:0 sc1\n\t"
"v_mfma_f32_16x16x32_bf16 v[48:51], a[224:227], a[192:195], v[48:51]\n\t"
"buffer_load_dwordx4 a[32:35], v252, s[8:11], s20 offen offset:1024 sc1\n\t"
"v_mfma_f32_16x16x32_bf16 v[52:55], a[220:223], a[188:191], v[52:55]\n\t"
"v_mfma_f32_16x16x32_bf16 v[52:55], a[216:219], a[192:195], v[52:55]\n\t"
"v_mfma_f32_16x16x32_bf16 v[52:55], a[224:227], a[196:199], v[52:55]\n\t"
"buffer_store_dwordx4 v[200:203], v248, s[16:19], s29 offen offset:0 nt\n\t"
"v_mfma_f32_16x16x32_bf16 v[56:59], a[220:223], a[192:195], v[56:59]\n\t"
"v_mfma_f32_16x16x32_bf16 v[56:59], a[216:219], a[196:199], v[56:59]\n\t"
"buffer_load_dwordx4 a[36:39], v252, s[8:11], s20 offen offset:2048 sc1\n\t"
"v_mfma_f32_16x16x32_bf16 v[56:59], a[224:227], a[200:203], v[56:59]\n\t"
"s_mov_b32 s20, 1671168\n\t"
"v_mfma_f32_16x16x32_bf16 v[60:63], a[220:223], a[196:199], v[60:63]\n\t"
"s_waitcnt vmcnt(49)\n\t"
"v_mfma_f32_16x16x32_bf16 v[60:63], a[216:219], a[200:203], v[60:63]\n\t"
"v_mfma_f32_16x16x32_bf16 v[60:63], a[224:227], a[204:207], v[60:63]\n\t"
"buffer_store_dwordx4 a[0:3], v248, s[16:19], s29 offen offset:128 nt\n\t"
"v_mfma_f32_16x16x32_bf16 v[64:67], a[88:91], a[168:171], v[64:67]\n\t"
"ds_read_b128 a[168:171], v244 offset:9216\n\t"
"v_mfma_f32_16x16x32_bf16 v[64:67], a[92:95], a[172:175], v[64:67]\n\t"
"v_mfma_f32_16x16x32_bf16 v[64:67], a[96:99], a[176:179], v[64:67]\n\t"
"buffer_load_dwordx4 a[216:219], v252, s[8:11], s20 offen offset:0 sc1\n\t"
"v_mfma_f32_16x16x32_bf16 v[68:71], a[88:91], a[172:175], v[68:71]\n\t"
"ds_read_b128 a[172:175], v244 offset:9728\n\t"
"v_mfma_f32_16x16x32_bf16 v[68:71], a[92:95], a[176:179], v[68:71]\n\t"
"v_mfma_f32_16x16x32_bf16 v[68:71], a[96:99], a[180:183], v[68:71]\n\t"
"v_mfma_f32_16x16x32_bf16 v[72:75], a[88:91], a[176:179], v[72:75]\n\t"
"ds_read_b128 a[176:179], v244 offset:10240\n\t"
"v_mfma_f32_16x16x32_bf16 v[72:75], a[92:95], a[180:183], v[72:75]\n\t"
"v_mfma_f32_16x16x32_bf16 v[72:75], a[96:99], a[184:187], v[72:75]\n\t"
"buffer_store_dwordx4 v[204:207], v248, s[16:19], s29 offen offset:256 nt\n\t"
"ds_read_b128 v[196:199], v252 offset:18432\n\t"
"v_mfma_f32_16x16x32_bf16 v[76:79], a[88:91], a[180:183], v[76:79]\n\t"
"s_add_u32 s29, s22, 983424\n\t"
"ds_read_b128 a[180:183], v244 offset:10752\n\t"
"v_mfma_f32_16x16x32_bf16 v[76:79], a[92:95], a[184:187], v[76:79]\n\t"
"buffer_load_dwordx4 a[220:223], v252, s[8:11], s20 offen offset:1024 sc1\n\t"
"v_mfma_f32_16x16x32_bf16 v[76:79], a[96:99], a[188:191], v[76:79]\n\t"
"ds_read_b128 v[200:203], v252 offset:19456\n\t"
"v_mfma_f32_16x16x32_bf16 v[80:83], a[88:91], a[184:187], v[80:83]\n\t"
"ds_read_b128 a[184:187], v244 offset:11264\n\t"
"v_mfma_f32_16x16x32_bf16 v[80:83], a[92:95], a[188:191], v[80:83]\n\t"
"buffer_load_dwordx4 a[224:227], v252, s[8:11], s20 offen offset:2048 sc1\n\t"
"v_mfma_f32_16x16x32_bf16 v[80:83], a[96:99], a[192:195], v[80:83]\n\t"
"ds_read_b128 v[208:211], v252 offset:20480\n\t"
"v_mfma_f32_16x16x32_bf16 v[84:87], a[88:91], a[188:191], v[84:87]\n\t"
"s_mov_b32 s20, 1093632\n\t"
"ds_read_b128 a[188:191], v244 offset:11776\n\t"
"v_mfma_f32_16x16x32_bf16 v[84:87], a[92:95], a[192:195], v[84:87]\n\t"
"v_mfma_f32_16x16x32_bf16 v[84:87], a[96:99], a[196:199], v[84:87]\n\t"
"buffer_store_dwordx4 a[4:7], v248, s[16:19], s29 offen offset:0 nt\n\t"
"v_mfma_f32_16x16x32_bf16 v[88:91], a[88:91], a[192:195], v[88:91]\n\t"
"ds_read_b128 a[192:195], v244 offset:12288\n\t"
"v_mfma_f32_16x16x32_bf16 v[88:91], a[92:95], a[196:199], v[88:91]\n\t"
"v_mfma_f32_16x16x32_bf16 v[88:91], a[96:99], a[200:203], v[88:91]\n\t"
"v_mfma_f32_16x16x32_bf16 v[92:95], a[88:91], a[196:199], v[92:95]\n\t"
"buffer_load_dwordx4 a[76:79], v252, s[8:11], s20 offen offset:0 sc1\n\t"
"v_mfma_f32_16x16x32_bf16 v[92:95], a[92:95], a[200:203], v[92:95]\n\t"
"s_waitcnt vmcnt(53) lgkmcnt(7)\n\t"
"ds_read_b128 a[196:199], v244 offset:12800\n\t"
"v_mfma_f32_16x16x32_bf16 v[92:95], a[96:99], a[204:207], v[92:95]\n\t"
"ds_read_b128 a[200:203], v244 offset:13312\n\t"
"v_mfma_f32_16x16x32_bf16 v[100:103], a[100:103], a[168:171], v[100:103]\n\t"
"s_waitcnt lgkmcnt(7)\n\t"
"ds_read_b128 a[204:207], v244 offset:13824\n\t"
"v_mfma_f32_16x16x32_bf16 v[100:103], a[108:111], a[172:175], v[100:103]\n\t"
"v_mfma_f32_16x16x32_bf16 v[100:103], a[228:231], a[176:179], v[100:103]\n\t"
"buffer_store_dwordx4 a[8:11], v248, s[16:19], s29 offen offset:128 nt\n\t"
"v_mfma_f32_16x16x32_bf16 v[104:107], a[100:103], a[172:175], v[104:107]\n\t"
"s_waitcnt lgkmcnt(6)\n\t"
"v_mfma_f32_16x16x32_bf16 v[104:107], a[108:111], a[176:179], v[104:107]\n\t"
"v_mfma_f32_16x16x32_bf16 v[104:107], a[228:231], a[180:183], v[104:107]\n\t"
"s_waitcnt lgkmcnt(0)\n\t"
"s_barrier\n\t"
"v_mfma_f32_16x16x32_bf16 v[108:111], a[100:103], a[176:179], v[108:111]\n\t"
"v_mfma_f32_16x16x32_bf16 v[108:111], a[108:111], a[180:183], v[108:111]\n\t"
"buffer_load_dwordx4 a[4:7], v252, s[8:11], s20 offen offset:1024 sc1\n\t"
"v_mfma_f32_16x16x32_bf16 v[108:111], a[228:231], a[184:187], v[108:111]\n\t"
"buffer_load_dwordx4 a[80:83], v252, s[8:11], s20 offen offset:2048 sc1\n\t"
"v_mfma_f32_16x16x32_bf16 v[112:115], a[100:103], a[180:183], v[112:115]\n\t"
"s_mov_b32 s20, 1683456\n\t"
"v_mfma_f32_16x16x32_bf16 v[112:115], a[108:111], a[184:187], v[112:115]\n\t"
"v_mfma_f32_16x16x32_bf16 v[112:115], a[228:231], a[188:191], v[112:115]\n\t"
"buffer_store_dwordx4 a[12:15], v248, s[16:19], s29 offen offset:256 nt\n\t"
"v_mfma_f32_16x16x32_bf16 v[116:119], a[100:103], a[184:187], v[116:119]\n\t"
"s_add_u32 s29, s22, 1180032\n\t"
"v_mfma_f32_16x16x32_bf16 v[116:119], a[108:111], a[188:191], v[116:119]\n\t"
"v_mfma_f32_16x16x32_bf16 v[116:119], a[228:231], a[192:195], v[116:119]\n\t"
"buffer_load_dwordx4 a[8:11], v252, s[8:11], s20 offen offset:0 sc1\n\t"
"v_mfma_f32_16x16x32_bf16 v[120:123], a[100:103], a[188:191], v[120:123]\n\t"
"v_mfma_f32_16x16x32_bf16 v[120:123], a[108:111], a[192:195], v[120:123]\n\t"
"v_mfma_f32_16x16x32_bf16 v[120:123], a[228:231], a[196:199], v[120:123]\n\t"
"v_mfma_f32_16x16x32_bf16 v[124:127], a[100:103], a[192:195], v[124:127]\n\t"
"v_mfma_f32_16x16x32_bf16 v[124:127], a[108:111], a[196:199], v[124:127]\n\t"
"v_mfma_f32_16x16x32_bf16 v[124:127], a[228:231], a[200:203], v[124:127]\n\t"
"buffer_store_dwordx4 a[16:19], v248, s[16:19], s29 offen offset:0 nt\n\t"
"v_mfma_f32_16x16x32_bf16 v[128:131], a[100:103], a[196:199], v[128:131]\n\t"
"s_waitcnt vmcnt(56)\n\t"
"v_mfma_f32_16x16x32_bf16 v[128:131], a[108:111], a[200:203], v[128:131]\n\t"
"buffer_load_dwordx4 a[12:15], v252, s[8:11], s20 offen offset:1024 sc1\n\t"
"v_mfma_f32_16x16x32_bf16 v[128:131], a[228:231], a[204:207], v[128:131]\n\t"
"v_mfma_f32_16x16x32_bf16 v[132:135], a[208:211], a[168:171], v[132:135]\n\t"
"v_mfma_f32_16x16x32_bf16 v[132:135], a[232:235], a[172:175], v[132:135]\n\t"
"buffer_load_dwordx4 a[228:231], v252, s[8:11], s20 offen offset:2048 sc1\n\t"
"v_mfma_f32_16x16x32_bf16 v[132:135], a[116:119], a[176:179], v[132:135]\n\t"
"v_mfma_f32_16x16x32_bf16 v[136:139], a[208:211], a[172:175], v[136:139]\n\t"
"v_mfma_f32_16x16x32_bf16 v[136:139], a[232:235], a[176:179], v[136:139]\n\t"
"v_subrev_u32 v255, 55296, v255\n\t"
"v_mfma_f32_16x16x32_bf16 v[136:139], a[116:119], a[180:183], v[136:139]\n\t"
"buffer_store_dwordx4 a[20:23], v248, s[16:19], s29 offen offset:128 nt\n\t"
"v_mfma_f32_16x16x32_bf16 v[140:143], a[208:211], a[176:179], v[140:143]\n\t"
"v_mfma_f32_16x16x32_bf16 v[140:143], a[232:235], a[180:183], v[140:143]\n\t"
"v_mfma_f32_16x16x32_bf16 v[140:143], a[116:119], a[184:187], v[140:143]\n\t"
"v_mfma_f32_16x16x32_bf16 v[144:147], a[208:211], a[180:183], v[144:147]\n\t"
"v_mfma_f32_16x16x32_bf16 v[144:147], a[232:235], a[184:187], v[144:147]\n\t"
"v_mfma_f32_16x16x32_bf16 v[144:147], a[116:119], a[188:191], v[144:147]\n\t"
"v_mfma_f32_16x16x32_bf16 v[148:151], a[208:211], a[184:187], v[148:151]\n\t"
"v_mfma_f32_16x16x32_bf16 v[148:151], a[232:235], a[188:191], v[148:151]\n\t"
"v_mfma_f32_16x16x32_bf16 v[148:151], a[116:119], a[192:195], v[148:151]\n\t"
"buffer_store_dwordx4 a[24:27], v248, s[16:19], s29 offen offset:256 nt\n\t"
"v_mfma_f32_16x16x32_bf16 v[152:155], a[208:211], a[188:191], v[152:155]\n\t"
"s_add_u32 s29, s22, 1376640\n\t"
"v_mfma_f32_16x16x32_bf16 v[152:155], a[232:235], a[192:195], v[152:155]\n\t"
"v_mfma_f32_16x16x32_bf16 v[152:155], a[116:119], a[196:199], v[152:155]\n\t"
"v_mfma_f32_16x16x32_bf16 v[156:159], a[208:211], a[192:195], v[156:159]\n\t"
"v_mfma_f32_16x16x32_bf16 v[156:159], a[232:235], a[196:199], v[156:159]\n\t"
"v_mfma_f32_16x16x32_bf16 v[156:159], a[116:119], a[200:203], v[156:159]\n\t"
"v_mfma_f32_16x16x32_bf16 v[160:163], a[208:211], a[196:199], v[160:163]\n\t"
"s_waitcnt vmcnt(57)\n\t"
"v_mfma_f32_16x16x32_bf16 v[160:163], a[232:235], a[200:203], v[160:163]\n\t"
"v_mfma_f32_16x16x32_bf16 v[160:163], a[116:119], a[204:207], v[160:163]\n\t"
"buffer_store_dwordx4 v[196:199], v248, s[16:19], s29 offen offset:0 nt\n\t"
"v_mfma_f32_16x16x32_bf16 v[164:167], a[124:127], a[168:171], v[164:167]\n\t"
"ds_read_b128 a[168:171], v255 offset:512\n\t"
"v_mfma_f32_16x16x32_bf16 v[164:167], a[132:135], a[172:175], v[164:167]\n\t"
"ds_read_b128 v[204:207], v255 offset:3584\n\t"
"v_mfma_f32_16x16x32_bf16 v[164:167], a[140:143], a[176:179], v[164:167]\n\t"
"ds_read_b128 v[212:215], v255 offset:6656\n\t"
"v_mfma_f32_16x16x32_bf16 v[168:171], a[124:127], a[172:175], v[168:171]\n\t"
"ds_read_b128 a[172:175], v255 offset:1024\n\t"
"v_mfma_f32_16x16x32_bf16 v[168:171], a[132:135], a[176:179], v[168:171]\n\t"
"ds_read_b128 v[216:219], v255 offset:4096\n\t"
"v_mfma_f32_16x16x32_bf16 v[168:171], a[140:143], a[180:183], v[168:171]\n\t"
"ds_read_b128 a[0:3], v255 offset:7168\n\t"
"v_mfma_f32_16x16x32_bf16 v[172:175], a[124:127], a[176:179], v[172:175]\n\t"
"v_mfma_f32_16x16x32_bf16 v[172:175], a[132:135], a[180:183], v[172:175]\n\t"
"v_mfma_f32_16x16x32_bf16 v[172:175], a[140:143], a[184:187], v[172:175]\n\t"
"buffer_store_dwordx4 v[200:203], v248, s[16:19], s29 offen offset:128 nt\n\t"
"v_mfma_f32_16x16x32_bf16 v[176:179], a[124:127], a[180:183], v[176:179]\n\t"
"v_mfma_f32_16x16x32_bf16 v[176:179], a[132:135], a[184:187], v[176:179]\n\t"
"v_mfma_f32_16x16x32_bf16 v[176:179], a[140:143], a[188:191], v[176:179]\n\t"
"s_barrier\n\t"
"v_mfma_f32_16x16x32_bf16 v[180:183], a[124:127], a[184:187], v[180:183]\n\t"
"v_mfma_f32_16x16x32_bf16 v[180:183], a[132:135], a[188:191], v[180:183]\n\t"
"v_mfma_f32_16x16x32_bf16 v[180:183], a[140:143], a[192:195], v[180:183]\n\t"
"v_mfma_f32_16x16x32_bf16 v[184:187], a[124:127], a[188:191], v[184:187]\n\t"
"v_mfma_f32_16x16x32_bf16 v[184:187], a[132:135], a[192:195], v[184:187]\n\t"
"v_mfma_f32_16x16x32_bf16 v[184:187], a[140:143], a[196:199], v[184:187]\n\t"
"buffer_store_dwordx4 v[208:211], v248, s[16:19], s29 offen offset:256 nt\n\t"
"v_mfma_f32_16x16x32_bf16 v[188:191], a[124:127], a[192:195], v[188:191]\n\t"
"ds_read_b128 a[176:179], v255 offset:2048\n\t"
"v_mfma_f32_16x16x32_bf16 v[188:191], a[132:135], a[196:199], v[188:191]\n\t"
"ds_read_b128 a[180:183], v255 offset:5120\n\t"
"v_mfma_f32_16x16x32_bf16 v[188:191], a[140:143], a[200:203], v[188:191]\n\t"
"ds_read_b128 a[184:187], v255 offset:8192\n\t"
"v_mfma_f32_16x16x32_bf16 v[192:195], a[124:127], a[196:199], v[192:195]\n\t"
"s_waitcnt vmcnt(57) lgkmcnt(6)\n\t"
"ds_read_b128 a[188:191], v255 offset:2560\n\t"
"v_mfma_f32_16x16x32_bf16 v[192:195], a[132:135], a[200:203], v[192:195]\n\t"
"ds_read_b128 a[192:195], v255 offset:5632\n\t"
"v_mfma_f32_16x16x32_bf16 v[192:195], a[140:143], a[204:207], v[192:195]\n\t"
"ds_read_b128 a[196:199], v255 offset:8704\n\t"
"Lst34_%=:\n\t"
"v_mfma_f32_16x16x32_bf16 v[0:3], a[148:151], a[168:171], v[0:3]\n\t"
"s_waitcnt vmcnt(54) lgkmcnt(6)\n\t"
"v_mfma_f32_16x16x32_bf16 v[0:3], a[236:239], v[204:207], v[0:3]\n\t"
"v_mfma_f32_16x16x32_bf16 v[0:3], a[240:243], v[212:215], v[0:3]\n\t"
"v_mfma_f32_16x16x32_bf16 v[0:3], a[244:247], a[172:175], v[0:3]\n\t"
"s_waitcnt vmcnt(51)\n\t"
"v_mfma_f32_16x16x32_bf16 v[0:3], a[104:107], v[216:219], v[0:3]\n\t"
"v_mfma_f32_16x16x32_bf16 v[0:3], a[156:159], a[0:3], v[0:3]\n\t"
"v_mfma_f32_16x16x32_bf16 v[32:35], a[164:167], a[168:171], v[32:35]\n\t"
"s_waitcnt vmcnt(47)\n\t"
"v_mfma_f32_16x16x32_bf16 v[32:35], v[240:243], v[204:207], v[32:35]\n\t"
"v_mfma_f32_16x16x32_bf16 v[32:35], v[96:99], v[212:215], v[32:35]\n\t"
"v_mfma_f32_16x16x32_bf16 v[32:35], a[248:251], a[172:175], v[32:35]\n\t"
"s_waitcnt vmcnt(42)\n\t"
"v_cvt_pk_bf16_f32 v0, v0, v1\n\t"
"v_cvt_pk_bf16_f32 v1, v2, v3\n\t"
"v_mfma_f32_16x16x32_bf16 v[32:35], a[52:55], v[216:219], v[32:35]\n\t"
"v_mfma_f32_16x16x32_bf16 v[32:35], a[56:59], a[0:3], v[32:35]\n\t"
"v_mfma_f32_16x16x32_bf16 v[64:67], a[48:51], a[168:171], v[64:67]\n\t"
"s_waitcnt vmcnt(39)\n\t"
"ds_read_b128 a[168:171], v255 offset:9728\n\t"
"v_mfma_f32_16x16x32_bf16 v[64:67], v[236:239], v[204:207], v[64:67]\n\t"
"v_mfma_f32_16x16x32_bf16 v[64:67], a[60:63], v[212:215], v[64:67]\n\t"
"s_barrier\n\t"
"s_cmp_lt_u32 s69, 216\n\t"
"s_cbranch_scc0 Lna2_%=\n\t"
"s_add_u32 s71, s71, 1\n\t"
"v_mov_b32 v228, s71\n\t"
"s_mov_b64 exec, 1\n\t"
"buffer_store_dword v228, off, s[8:11], s70 sc1\n\t"
"s_mov_b64 exec, -1\n\t"
"Lna2_%=:\n\t"
"v_mfma_f32_16x16x32_bf16 v[64:67], a[212:215], a[172:175], v[64:67]\n\t"
"s_waitcnt vmcnt(36) lgkmcnt(4)\n\t"
"ds_read_b128 a[172:175], v255 offset:10240\n\t"
"v_mfma_f32_16x16x32_bf16 v[64:67], a[252:255], v[216:219], v[64:67]\n\t"
"v_cvt_pk_bf16_f32 v2, v32, v33\n\t"
"v_cvt_pk_bf16_f32 v3, v34, v35\n\t"
"v_mfma_f32_16x16x32_bf16 v[64:67], a[28:31], a[0:3], v[64:67]\n\t"
"s_waitcnt vmcnt(15) lgkmcnt(2)\n\t"
"ds_write_b128 v249, v[0:3] offset:18432\n\t"
"v_mfma_f32_16x16x32_bf16 v[100:103], a[112:115], a[176:179], v[100:103]\n\t"
"v_mfma_f32_16x16x32_bf16 v[100:103], a[120:123], a[180:183], v[100:103]\n\t"
"v_mfma_f32_16x16x32_bf16 v[100:103], a[64:67], a[184:187], v[100:103]\n\t"
"Lpg25_%=:\n\t"
"s_cbranch_vccnz Lvr4_%=\n\t"
"v_mfma_f32_16x16x32_bf16 v[100:103], a[68:71], a[188:191], v[100:103]\n\t"
"v_cvt_pk_bf16_f32 v64, v64, v65\n\t"
"v_cvt_pk_bf16_f32 v65, v66, v67\n\t"
"v_mfma_f32_16x16x32_bf16 v[100:103], a[72:75], a[192:195], v[100:103]\n\t"
"v_mfma_f32_16x16x32_bf16 v[100:103], a[40:43], a[196:199], v[100:103]\n\t"
"ds_write_b64 v250, v[64:65] offset:18432\n\t"
"v_mfma_f32_16x16x32_bf16 v[132:135], a[44:47], a[176:179], v[132:135]\n\t"
"v_mfma_f32_16x16x32_bf16 v[132:135], a[32:35], a[180:183], v[132:135]\n\t"
"v_mfma_f32_16x16x32_bf16 v[132:135], a[36:39], a[184:187], v[132:135]\n\t"
"v_mfma_f32_16x16x32_bf16 v[132:135], a[216:219], a[188:191], v[132:135]\n\t"
"s_waitcnt vmcnt(10)\n\t"
"v_cvt_pk_bf16_f32 v100, v100, v101\n\t"
"v_cvt_pk_bf16_f32 v101, v102, v103\n\t"
"v_mfma_f32_16x16x32_bf16 v[132:135], a[220:223], a[192:195], v[132:135]\n\t"
"v_mfma_f32_16x16x32_bf16 v[132:135], a[224:227], a[196:199], v[132:135]\n\t"
"v_mfma_f32_16x16x32_bf16 v[164:167], a[76:79], a[176:179], v[164:167]\n\t"
"s_waitcnt vmcnt(5)\n\t"
"ds_read_b128 a[176:179], v255 offset:11264\n\t"
"v_mfma_f32_16x16x32_bf16 v[164:167], a[4:7], a[180:183], v[164:167]\n\t"
"v_mfma_f32_16x16x32_bf16 v[164:167], a[80:83], a[184:187], v[164:167]\n\t"
"v_mfma_f32_16x16x32_bf16 v[164:167], a[8:11], a[188:191], v[164:167]\n\t"
"s_waitcnt lgkmcnt(4)\n\t"
"ds_read_b128 a[188:191], v255 offset:11776\n\t"
"v_cvt_pk_bf16_f32 v102, v132, v133\n\t"
"v_mfma_f32_16x16x32_bf16 v[164:167], a[12:15], a[192:195], v[164:167]\n\t"
"v_cvt_pk_bf16_f32 v103, v134, v135\n\t"
"v_mfma_f32_16x16x32_bf16 v[164:167], a[228:231], a[196:199], v[164:167]\n\t"
"ds_write_b128 v249, v[100:103] offset:24576\n\t"
"v_mfma_f32_16x16x32_bf16 v[4:7], a[148:151], v[204:207], v[4:7]\n\t"
"s_waitcnt lgkmcnt(5)\n\t"
"v_mfma_f32_16x16x32_bf16 v[4:7], a[236:239], v[212:215], v[4:7]\n\t"
"v_mfma_f32_16x16x32_bf16 v[4:7], a[240:243], a[168:171], v[4:7]\n\t"
"v_mfma_f32_16x16x32_bf16 v[4:7], a[244:247], v[216:219], v[4:7]\n\t"
"v_cvt_pk_bf16_f32 v164, v164, v165\n\t"
"v_cvt_pk_bf16_f32 v165, v166, v167\n\t"
"v_mfma_f32_16x16x32_bf16 v[4:7], a[104:107], a[0:3], v[4:7]\n\t"
"v_mfma_f32_16x16x32_bf16 v[4:7], a[156:159], a[172:175], v[4:7]\n\t"
"ds_write_b64 v250, v[164:165] offset:24576\n\t"
"v_mfma_f32_16x16x32_bf16 v[36:39], a[164:167], v[204:207], v[36:39]\n\t"
"v_mfma_f32_16x16x32_bf16 v[36:39], v[240:243], v[212:215], v[36:39]\n\t"
"v_mfma_f32_16x16x32_bf16 v[36:39], v[96:99], a[168:171], v[36:39]\n\t"
"v_mfma_f32_16x16x32_bf16 v[36:39], a[248:251], v[216:219], v[36:39]\n\t"
"v_cvt_pk_bf16_f32 v4, v4, v5\n\t"
"v_cvt_pk_bf16_f32 v5, v6, v7\n\t"
"v_mfma_f32_16x16x32_bf16 v[36:39], a[52:55], a[0:3], v[36:39]\n\t"
"v_mfma_f32_16x16x32_bf16 v[36:39], a[56:59], a[172:175], v[36:39]\n\t"
"v_mfma_f32_16x16x32_bf16 v[68:71], a[48:51], v[204:207], v[68:71]\n\t"
"ds_read_b128 a[200:203], v255 offset:12800\n\t"
"v_mfma_f32_16x16x32_bf16 v[68:71], v[236:239], v[212:215], v[68:71]\n\t"
"v_mfma_f32_16x16x32_bf16 v[68:71], a[60:63], a[168:171], v[68:71]\n\t"
"s_waitcnt lgkmcnt(1)\n\t"
"s_barrier\n\t"
"v_mfma_f32_16x16x32_bf16 v[68:71], a[212:215], v[216:219], v[68:71]\n\t"
"ds_read_b128 v[196:199], v252 offset:18432\n\t"
"v_mfma_f32_16x16x32_bf16 v[68:71], a[252:255], a[0:3], v[68:71]\n\t"
"ds_read_b128 v[200:203], v252 offset:19456\n\t"
"v_cvt_pk_bf16_f32 v6, v36, v37\n\t"
"v_mfma_f32_16x16x32_bf16 v[68:71], a[28:31], a[172:175], v[68:71]\n\t"
"ds_read_b128 v[204:207], v252 offset:20480\n\t"
"v_cvt_pk_bf16_f32 v7, v38, v39\n\t"
"v_mfma_f32_16x16x32_bf16 v[104:107], a[112:115], a[180:183], v[104:107]\n\t"
"s_add_u32 s29, s23, 0\n\t"
"ds_read_b128 a[204:207], v255 offset:13312\n\t"
"v_mfma_f32_16x16x32_bf16 v[104:107], a[120:123], a[184:187], v[104:107]\n\t"
"v_mfma_f32_16x16x32_bf16 v[104:107], a[64:67], a[176:179], v[104:107]\n\t"
"ds_write_b128 v249, v[4:7] offset:30720\n\t"
"v_mfma_f32_16x16x32_bf16 v[104:107], a[68:71], a[192:195], v[104:107]\n\t"
"s_waitcnt lgkmcnt(4)\n\t"
"v_cvt_pk_bf16_f32 v68, v68, v69\n\t"
"v_cvt_pk_bf16_f32 v69, v70, v71\n\t"
"v_mfma_f32_16x16x32_bf16 v[104:107], a[72:75], a[196:199], v[104:107]\n\t"
"v_mfma_f32_16x16x32_bf16 v[104:107], a[40:43], a[188:191], v[104:107]\n\t"
"ds_write_b64 v250, v[68:69] offset:30720\n\t"
"v_mfma_f32_16x16x32_bf16 v[136:139], a[44:47], a[180:183], v[136:139]\n\t"
"s_waitcnt lgkmcnt(4)\n\t"
"v_mfma_f32_16x16x32_bf16 v[136:139], a[32:35], a[184:187], v[136:139]\n\t"
"v_mfma_f32_16x16x32_bf16 v[136:139], a[36:39], a[176:179], v[136:139]\n\t"
"buffer_store_dwordx4 v[196:199], v248, s[16:19], s29 offen offset:0 sc1\n\t"
"v_mfma_f32_16x16x32_bf16 v[136:139], a[216:219], a[192:195], v[136:139]\n\t"
"s_waitcnt lgkmcnt(3)\n\t"
"v_cvt_pk_bf16_f32 v104, v104, v105\n\t"
"v_cvt_pk_bf16_f32 v105, v106, v107\n\t"
"v_mfma_f32_16x16x32_bf16 v[136:139], a[220:223], a[196:199], v[136:139]\n\t"
"v_mfma_f32_16x16x32_bf16 v[136:139], a[224:227], a[188:191], v[136:139]\n\t"
"buffer_store_dwordx4 v[200:203], v248, s[16:19], s29 offen offset:128 sc1\n\t"
"v_mfma_f32_16x16x32_bf16 v[168:171], a[76:79], a[180:183], v[168:171]\n\t"
"ds_read_b128 a[180:183], v255 offset:14336\n\t"
"v_mfma_f32_16x16x32_bf16 v[168:171], a[4:7], a[184:187], v[168:171]\n\t"
"v_mfma_f32_16x16x32_bf16 v[168:171], a[80:83], a[176:179], v[168:171]\n\t"
"buffer_store_dwordx4 v[204:207], v248, s[16:19], s29 offen offset:256 sc1\n\t"
"v_mfma_f32_16x16x32_bf16 v[168:171], a[8:11], a[192:195], v[168:171]\n\t"
"ds_read_b128 a[192:195], v255 offset:14848\n\t"
"v_cvt_pk_bf16_f32 v106, v136, v137\n\t"
"v_mfma_f32_16x16x32_bf16 v[168:171], a[12:15], a[196:199], v[168:171]\n\t"
"v_cvt_pk_bf16_f32 v107, v138, v139\n\t"
"v_mfma_f32_16x16x32_bf16 v[168:171], a[228:231], a[188:191], v[168:171]\n\t"
"ds_write_b128 v249, v[104:107] offset:36864\n\t"
"v_mfma_f32_16x16x32_bf16 v[8:11], a[148:151], v[212:215], v[8:11]\n\t"
"s_waitcnt lgkmcnt(5)\n\t"
"v_mfma_f32_16x16x32_bf16 v[8:11], a[236:239], a[168:171], v[8:11]\n\t"
"v_mfma_f32_16x16x32_bf16 v[8:11], a[240:243], a[200:203], v[8:11]\n\t"
"v_mfma_f32_16x16x32_bf16 v[8:11], a[244:247], a[0:3], v[8:11]\n\t"
"v_cvt_pk_bf16_f32 v168, v168, v169\n\t"
"v_cvt_pk_bf16_f32 v169, v170, v171\n\t"
"v_mfma_f32_16x16x32_bf16 v[8:11], a[104:107], a[172:175], v[8:11]\n\t"
"v_mfma_f32_16x16x32_bf16 v[8:11], a[156:159], a[204:207], v[8:11]\n\t"
"ds_write_b64 v250, v[168:169] offset:36864\n\t"
"v_mfma_f32_16x16x32_bf16 v[40:43], a[164:167], v[212:215], v[40:43]\n\t"
"v_mfma_f32_16x16x32_bf16 v[40:43], v[240:243], a[168:171], v[40:43]\n\t"
"v_mfma_f32_16x16x32_bf16 v[40:43], v[96:99], a[200:203], v[40:43]\n\t"
"v_mfma_f32_16x16x32_bf16 v[40:43], a[248:251], a[0:3], v[40:43]\n\t"
"v_cvt_pk_bf16_f32 v8, v8, v9\n\t"
"v_cvt_pk_bf16_f32 v9, v10, v11\n\t"
"v_mfma_f32_16x16x32_bf16 v[40:43], a[52:55], a[172:175], v[40:43]\n\t"
"v_mfma_f32_16x16x32_bf16 v[40:43], a[56:59], a[204:207], v[40:43]\n\t"
"v_mfma_f32_16x16x32_bf16 v[72:75], a[48:51], v[212:215], v[72:75]\n\t"
"ds_read_b128 v[208:211], v255 offset:15872\n\t"
"v_mfma_f32_16x16x32_bf16 v[72:75], v[236:239], a[168:171], v[72:75]\n\t"
"v_mfma_f32_16x16x32_bf16 v[72:75], a[60:63], a[200:203], v[72:75]\n\t"
"s_waitcnt lgkmcnt(1)\n\t"
"s_barrier\n\t"
"v_mfma_f32_16x16x32_bf16 v[72:75], a[212:215], a[0:3], v[72:75]\n\t"
"ds_read_b128 v[196:199], v252 offset:30720\n\t"
"v_mfma_f32_16x16x32_bf16 v[72:75], a[252:255], a[172:175], v[72:75]\n\t"
"ds_read_b128 v[200:203], v252 offset:31744\n\t"
"v_cvt_pk_bf16_f32 v10, v40, v41\n\t"
"v_mfma_f32_16x16x32_bf16 v[72:75], a[28:31], a[204:207], v[72:75]\n\t"
"ds_read_b128 v[204:207], v252 offset:32768\n\t"
"v_cvt_pk_bf16_f32 v11, v42, v43\n\t"
"v_mfma_f32_16x16x32_bf16 v[108:111], a[112:115], a[184:187], v[108:111]\n\t"
"s_add_u32 s29, s23, 196608\n\t"
"ds_read_b128 v[212:215], v255 offset:16384\n\t"
"v_mfma_f32_16x16x32_bf16 v[108:111], a[120:123], a[176:179], v[108:111]\n\t"
"v_mfma_f32_16x16x32_bf16 v[108:111], a[64:67], a[180:183], v[108:111]\n\t"
"ds_write_b128 v249, v[8:11] offset:18432\n\t"
"v_mfma_f32_16x16x32_bf16 v[108:111], a[68:71], a[196:199], v[108:111]\n\t"
"s_waitcnt lgkmcnt(4)\n\t"
"v_cvt_pk_bf16_f32 v72, v72, v73\n\t"
"v_cvt_pk_bf16_f32 v73, v74, v75\n\t"
"v_mfma_f32_16x16x32_bf16 v[108:111], a[72:75], a[188:191], v[108:111]\n\t"
"v_mfma_f32_16x16x32_bf16 v[108:111], a[40:43], a[192:195], v[108:111]\n\t"
"ds_write_b64 v250, v[72:73] offset:18432\n\t"
"v_mfma_f32_16x16x32_bf16 v[140:143], a[44:47], a[184:187], v[140:143]\n\t"
"s_waitcnt lgkmcnt(4)\n\t"
"v_mfma_f32_16x16x32_bf16 v[140:143], a[32:35], a[176:179], v[140:143]\n\t"
"v_mfma_f32_16x16x32_bf16 v[140:143], a[36:39], a[180:183], v[140:143]\n\t"
"buffer_store_dwordx4 v[196:199], v248, s[16:19], s29 offen offset:0 sc1\n\t"
"v_mfma_f32_16x16x32_bf16 v[140:143], a[216:219], a[196:199], v[140:143]\n\t"
"s_waitcnt lgkmcnt(3)\n\t"
"v_cvt_pk_bf16_f32 v108, v108, v109\n\t"
"v_cvt_pk_bf16_f32 v109, v110, v111\n\t"
"v_mfma_f32_16x16x32_bf16 v[140:143], a[220:223], a[188:191], v[140:143]\n\t"
"v_mfma_f32_16x16x32_bf16 v[140:143], a[224:227], a[192:195], v[140:143]\n\t"
"buffer_store_dwordx4 v[200:203], v248, s[16:19], s29 offen offset:128 sc1\n\t"
"v_mfma_f32_16x16x32_bf16 v[172:175], a[76:79], a[184:187], v[172:175]\n\t"
"ds_read_b128 a[184:187], v255 offset:17408\n\t"
"v_mfma_f32_16x16x32_bf16 v[172:175], a[4:7], a[176:179], v[172:175]\n\t"
"v_mfma_f32_16x16x32_bf16 v[172:175], a[80:83], a[180:183], v[172:175]\n\t"
"buffer_store_dwordx4 v[204:207], v248, s[16:19], s29 offen offset:256 sc1\n\t"
"v_mfma_f32_16x16x32_bf16 v[172:175], a[8:11], a[196:199], v[172:175]\n\t"
"ds_read_b128 a[196:199], v255 offset:17920\n\t"
"v_cvt_pk_bf16_f32 v110, v140, v141\n\t"
"v_mfma_f32_16x16x32_bf16 v[172:175], a[12:15], a[188:191], v[172:175]\n\t"
"v_cvt_pk_bf16_f32 v111, v142, v143\n\t"
"v_mfma_f32_16x16x32_bf16 v[172:175], a[228:231], a[192:195], v[172:175]\n\t"
"ds_write_b128 v249, v[108:111] offset:24576\n\t"
"v_mfma_f32_16x16x32_bf16 v[12:15], a[148:151], a[168:171], v[12:15]\n\t"
"s_waitcnt lgkmcnt(5)\n\t"
"v_mfma_f32_16x16x32_bf16 v[12:15], a[236:239], a[200:203], v[12:15]\n\t"
"v_mfma_f32_16x16x32_bf16 v[12:15], a[240:243], v[208:211], v[12:15]\n\t"
"v_mfma_f32_16x16x32_bf16 v[12:15], a[244:247], a[172:175], v[12:15]\n\t"
"v_cvt_pk_bf16_f32 v172, v172, v173\n\t"
"v_cvt_pk_bf16_f32 v173, v174, v175\n\t"
"v_mfma_f32_16x16x32_bf16 v[12:15], a[104:107], a[204:207], v[12:15]\n\t"
"v_mfma_f32_16x16x32_bf16 v[12:15], a[156:159], v[212:215], v[12:15]\n\t"
"ds_write_b64 v250, v[172:173] offset:24576\n\t"
"v_mfma_f32_16x16x32_bf16 v[44:47], a[164:167], a[168:171], v[44:47]\n\t"
"v_mfma_f32_16x16x32_bf16 v[44:47], v[240:243], a[200:203], v[44:47]\n\t"
"v_mfma_f32_16x16x32_bf16 v[44:47], v[96:99], v[208:211], v[44:47]\n\t"
"v_mfma_f32_16x16x32_bf16 v[44:47], a[248:251], a[172:175], v[44:47]\n\t"
"v_cvt_pk_bf16_f32 v12, v12, v13\n\t"
"v_cvt_pk_bf16_f32 v13, v14, v15\n\t"
"v_mfma_f32_16x16x32_bf16 v[44:47], a[52:55], a[204:207], v[44:47]\n\t"
"v_mfma_f32_16x16x32_bf16 v[44:47], a[56:59], v[212:215], v[44:47]\n\t"
"v_mfma_f32_16x16x32_bf16 v[76:79], a[48:51], a[168:171], v[76:79]\n\t"
"ds_read_b128 a[168:171], v255 offset:18944\n\t"
"v_mfma_f32_16x16x32_bf16 v[76:79], v[236:239], a[200:203], v[76:79]\n\t"
"v_mfma_f32_16x16x32_bf16 v[76:79], a[60:63], v[208:211], v[76:79]\n\t"
"s_waitcnt lgkmcnt(1)\n\t"
"s_barrier\n\t"
"v_mfma_f32_16x16x32_bf16 v[76:79], a[212:215], a[172:175], v[76:79]\n\t"
"ds_read_b128 v[196:199], v252 offset:18432\n\t"
"v_mfma_f32_16x16x32_bf16 v[76:79], a[252:255], a[204:207], v[76:79]\n\t"
"ds_read_b128 v[200:203], v252 offset:19456\n\t"
"v_cvt_pk_bf16_f32 v14, v44, v45\n\t"
"v_mfma_f32_16x16x32_bf16 v[76:79], a[28:31], v[212:215], v[76:79]\n\t"
"ds_read_b128 v[204:207], v252 offset:20480\n\t"
"v_cvt_pk_bf16_f32 v15, v46, v47\n\t"
"v_mfma_f32_16x16x32_bf16 v[112:115], a[112:115], a[176:179], v[112:115]\n\t"
"s_add_u32 s29, s23, 393216\n\t"
"ds_read_b128 a[172:175], v255 offset:19456\n\t"
"v_mfma_f32_16x16x32_bf16 v[112:115], a[120:123], a[180:183], v[112:115]\n\t"
"v_mfma_f32_16x16x32_bf16 v[112:115], a[64:67], a[184:187], v[112:115]\n\t"
"ds_write_b128 v249, v[12:15] offset:30720\n\t"
"v_mfma_f32_16x16x32_bf16 v[112:115], a[68:71], a[188:191], v[112:115]\n\t"
"s_waitcnt lgkmcnt(4)\n\t"
"v_cvt_pk_bf16_f32 v76, v76, v77\n\t"
"v_cvt_pk_bf16_f32 v77, v78, v79\n\t"
"v_mfma_f32_16x16x32_bf16 v[112:115], a[72:75], a[192:195], v[112:115]\n\t"
"v_mfma_f32_16x16x32_bf16 v[112:115], a[40:43], a[196:199], v[112:115]\n\t"
"ds_write_b64 v250, v[76:77] offset:30720\n\t"
"v_mfma_f32_16x16x32_bf16 v[144:147], a[44:47], a[176:179], v[144:147]\n\t"
"s_waitcnt lgkmcnt(4)\n\t"
"v_mfma_f32_16x16x32_bf16 v[144:147], a[32:35], a[180:183], v[144:147]\n\t"
"v_mfma_f32_16x16x32_bf16 v[144:147], a[36:39], a[184:187], v[144:147]\n\t"
"buffer_store_dwordx4 v[196:199], v248, s[16:19], s29 offen offset:0 sc1\n\t"
"v_mfma_f32_16x16x32_bf16 v[144:147], a[216:219], a[188:191], v[144:147]\n\t"
"s_waitcnt lgkmcnt(3)\n\t"
"v_cvt_pk_bf16_f32 v112, v112, v113\n\t"
"v_cvt_pk_bf16_f32 v113, v114, v115\n\t"
"v_mfma_f32_16x16x32_bf16 v[144:147], a[220:223], a[192:195], v[144:147]\n\t"
"v_mfma_f32_16x16x32_bf16 v[144:147], a[224:227], a[196:199], v[144:147]\n\t"
"buffer_store_dwordx4 v[200:203], v248, s[16:19], s29 offen offset:128 sc1\n\t"
"v_mfma_f32_16x16x32_bf16 v[176:179], a[76:79], a[176:179], v[176:179]\n\t"
"ds_read_b128 a[176:179], v255 offset:20480\n\t"
"v_mfma_f32_16x16x32_bf16 v[176:179], a[4:7], a[180:183], v[176:179]\n\t"
"v_mfma_f32_16x16x32_bf16 v[176:179], a[80:83], a[184:187], v[176:179]\n\t"
"buffer_store_dwordx4 v[204:207], v248, s[16:19], s29 offen offset:256 sc1\n\t"
"v_mfma_f32_16x16x32_bf16 v[176:179], a[8:11], a[188:191], v[176:179]\n\t"
"ds_read_b128 a[188:191], v255 offset:20992\n\t"
"v_cvt_pk_bf16_f32 v114, v144, v145\n\t"
"v_mfma_f32_16x16x32_bf16 v[176:179], a[12:15], a[192:195], v[176:179]\n\t"
"v_cvt_pk_bf16_f32 v115, v146, v147\n\t"
"v_mfma_f32_16x16x32_bf16 v[176:179], a[228:231], a[196:199], v[176:179]\n\t"
"ds_write_b128 v249, v[112:115] offset:36864\n\t"
"Lst35_%=:\n\t"
"v_mfma_f32_16x16x32_bf16 v[16:19], a[148:151], a[200:203], v[16:19]\n\t"
"s_waitcnt lgkmcnt(5)\n\t"
"v_mfma_f32_16x16x32_bf16 v[16:19], a[236:239], v[208:211], v[16:19]\n\t"
"v_mfma_f32_16x16x32_bf16 v[16:19], a[240:243], a[168:171], v[16:19]\n\t"
"v_mfma_f32_16x16x32_bf16 v[16:19], a[244:247], a[204:207], v[16:19]\n\t"
"v_cvt_pk_bf16_f32 v176, v176, v177\n\t"
"v_cvt_pk_bf16_f32 v177, v178, v179\n\t"
"v_mfma_f32_16x16x32_bf16 v[16:19], a[104:107], v[212:215], v[16:19]\n\t"
"v_mfma_f32_16x16x32_bf16 v[16:19], a[156:159], a[172:175], v[16:19]\n\t"
"ds_write_b64 v250, v[176:177] offset:36864\n\t"
"v_mfma_f32_16x16x32_bf16 v[48:51], a[164:167], a[200:203], v[48:51]\n\t"
"v_mfma_f32_16x16x32_bf16 v[48:51], v[240:243], v[208:211], v[48:51]\n\t"
"v_mfma_f32_16x16x32_bf16 v[48:51], v[96:99], a[168:171], v[48:51]\n\t"
"v_mfma_f32_16x16x32_bf16 v[48:51], a[248:251], a[204:207], v[48:51]\n\t"
"v_cvt_pk_bf16_f32 v16, v16, v17\n\t"
"v_cvt_pk_bf16_f32 v17, v18, v19\n\t"
"v_mfma_f32_16x16x32_bf16 v[48:51], a[52:55], v[212:215], v[48:51]\n\t"
"v_mfma_f32_16x16x32_bf16 v[48:51], a[56:59], a[172:175], v[48:51]\n\t"
"v_mfma_f32_16x16x32_bf16 v[80:83], a[48:51], a[200:203], v[80:83]\n\t"
"ds_read_b128 a[200:203], v255 offset:22016\n\t"
"v_mfma_f32_16x16x32_bf16 v[80:83], v[236:239], v[208:211], v[80:83]\n\t"
"v_mfma_f32_16x16x32_bf16 v[80:83], a[60:63], a[168:171], v[80:83]\n\t"
"s_waitcnt lgkmcnt(1)\n\t"
"s_barrier\n\t"
"v_mfma_f32_16x16x32_bf16 v[80:83], a[212:215], a[204:207], v[80:83]\n\t"
"ds_read_b128 v[196:199], v252 offset:30720\n\t"
"v_mfma_f32_16x16x32_bf16 v[80:83], a[252:255], v[212:215], v[80:83]\n\t"
"ds_read_b128 v[200:203], v252 offset:31744\n\t"
"v_cvt_pk_bf16_f32 v18, v48, v49\n\t"
"v_mfma_f32_16x16x32_bf16 v[80:83], a[28:31], a[172:175], v[80:83]\n\t"
"ds_read_b128 v[204:207], v252 offset:32768\n\t"
"v_cvt_pk_bf16_f32 v19, v50, v51\n\t"
"v_mfma_f32_16x16x32_bf16 v[116:119], a[112:115], a[180:183], v[116:119]\n\t"
"s_add_u32 s29, s23, 589824\n\t"
"ds_read_b128 a[204:207], v255 offset:22528\n\t"
"v_mfma_f32_16x16x32_bf16 v[116:119], a[120:123], a[184:187], v[116:119]\n\t"
"v_mfma_f32_16x16x32_bf16 v[116:119], a[64:67], a[176:179], v[116:119]\n\t"
"ds_write_b128 v249, v[16:19] offset:18432\n\t"
"v_mfma_f32_16x16x32_bf16 v[116:119], a[68:71], a[192:195], v[116:119]\n\t"
"s_waitcnt lgkmcnt(4)\n\t"
"v_cvt_pk_bf16_f32 v80, v80, v81\n\t"
"v_cvt_pk_bf16_f32 v81, v82, v83\n\t"
"v_mfma_f32_16x16x32_bf16 v[116:119], a[72:75], a[196:199], v[116:119]\n\t"
"v_mfma_f32_16x16x32_bf16 v[116:119], a[40:43], a[188:191], v[116:119]\n\t"
"ds_write_b64 v250, v[80:81] offset:18432\n\t"
"v_mfma_f32_16x16x32_bf16 v[148:151], a[44:47], a[180:183], v[148:151]\n\t"
"s_waitcnt lgkmcnt(4)\n\t"
"v_mfma_f32_16x16x32_bf16 v[148:151], a[32:35], a[184:187], v[148:151]\n\t"
"v_mfma_f32_16x16x32_bf16 v[148:151], a[36:39], a[176:179], v[148:151]\n\t"
"buffer_store_dwordx4 v[196:199], v248, s[16:19], s29 offen offset:0 sc1\n\t"
"v_mfma_f32_16x16x32_bf16 v[148:151], a[216:219], a[192:195], v[148:151]\n\t"
"s_waitcnt lgkmcnt(3)\n\t"
"v_cvt_pk_bf16_f32 v116, v116, v117\n\t"
"v_cvt_pk_bf16_f32 v117, v118, v119\n\t"
"v_mfma_f32_16x16x32_bf16 v[148:151], a[220:223], a[196:199], v[148:151]\n\t"
"v_mfma_f32_16x16x32_bf16 v[148:151], a[224:227], a[188:191], v[148:151]\n\t"
"buffer_store_dwordx4 v[200:203], v248, s[16:19], s29 offen offset:128 sc1\n\t"
"v_mfma_f32_16x16x32_bf16 v[180:183], a[76:79], a[180:183], v[180:183]\n\t"
"ds_read_b128 a[180:183], v255 offset:23552\n\t"
"v_mfma_f32_16x16x32_bf16 v[180:183], a[4:7], a[184:187], v[180:183]\n\t"
"v_mfma_f32_16x16x32_bf16 v[180:183], a[80:83], a[176:179], v[180:183]\n\t"
"buffer_store_dwordx4 v[204:207], v248, s[16:19], s29 offen offset:256 sc1\n\t"
"v_mfma_f32_16x16x32_bf16 v[180:183], a[8:11], a[192:195], v[180:183]\n\t"
"ds_read_b128 a[192:195], v255 offset:24064\n\t"
"v_cvt_pk_bf16_f32 v118, v148, v149\n\t"
"v_mfma_f32_16x16x32_bf16 v[180:183], a[12:15], a[196:199], v[180:183]\n\t"
"v_cvt_pk_bf16_f32 v119, v150, v151\n\t"
"v_mfma_f32_16x16x32_bf16 v[180:183], a[228:231], a[188:191], v[180:183]\n\t"
"ds_write_b128 v249, v[116:119] offset:24576\n\t"
"v_mfma_f32_16x16x32_bf16 v[20:23], a[148:151], v[208:211], v[20:23]\n\t"
"s_waitcnt lgkmcnt(5)\n\t"
"v_mfma_f32_16x16x32_bf16 v[20:23], a[236:239], a[168:171], v[20:23]\n\t"
"v_mfma_f32_16x16x32_bf16 v[20:23], a[240:243], a[200:203], v[20:23]\n\t"
"v_mfma_f32_16x16x32_bf16 v[20:23], a[244:247], v[212:215], v[20:23]\n\t"
"v_cvt_pk_bf16_f32 v180, v180, v181\n\t"
"v_cvt_pk_bf16_f32 v181, v182, v183\n\t"
"v_mfma_f32_16x16x32_bf16 v[20:23], a[104:107], a[172:175], v[20:23]\n\t"
"v_mfma_f32_16x16x32_bf16 v[20:23], a[156:159], a[204:207], v[20:23]\n\t"
"ds_write_b64 v250, v[180:181] offset:24576\n\t"
"v_mfma_f32_16x16x32_bf16 v[52:55], a[164:167], v[208:211], v[52:55]\n\t"
"v_mfma_f32_16x16x32_bf16 v[52:55], v[240:243], a[168:171], v[52:55]\n\t"
"v_mfma_f32_16x16x32_bf16 v[52:55], v[96:99], a[200:203], v[52:55]\n\t"
"v_mfma_f32_16x16x32_bf16 v[52:55], a[248:251], v[212:215], v[52:55]\n\t"
"v_cvt_pk_bf16_f32 v20, v20, v21\n\t"
"v_cvt_pk_bf16_f32 v21, v22, v23\n\t"
"v_mfma_f32_16x16x32_bf16 v[52:55], a[52:55], a[172:175], v[52:55]\n\t"
"v_mfma_f32_16x16x32_bf16 v[52:55], a[56:59], a[204:207], v[52:55]\n\t"
"v_mfma_f32_16x16x32_bf16 v[84:87], a[48:51], v[208:211], v[84:87]\n\t"
"ds_read_b128 v[208:211], v255 offset:25088\n\t"
"v_mfma_f32_16x16x32_bf16 v[84:87], v[236:239], a[168:171], v[84:87]\n\t"
"v_mfma_f32_16x16x32_bf16 v[84:87], a[60:63], a[200:203], v[84:87]\n\t"
"s_waitcnt lgkmcnt(1)\n\t"
"s_barrier\n\t"
"v_mfma_f32_16x16x32_bf16 v[84:87], a[212:215], v[212:215], v[84:87]\n\t"
"ds_read_b128 v[196:199], v252 offset:18432\n\t"
"v_mfma_f32_16x16x32_bf16 v[84:87], a[252:255], a[172:175], v[84:87]\n\t"
"ds_read_b128 v[200:203], v252 offset:19456\n\t"
"v_cvt_pk_bf16_f32 v22, v52, v53\n\t"
"v_mfma_f32_16x16x32_bf16 v[84:87], a[28:31], a[204:207], v[84:87]\n\t"
"ds_read_b128 v[204:207], v252 offset:20480\n\t"
"v_cvt_pk_bf16_f32 v23, v54, v55\n\t"
"v_mfma_f32_16x16x32_bf16 v[120:123], a[112:115], a[184:187], v[120:123]\n\t"
"s_add_u32 s29, s23, 786432\n\t"
"ds_read_b128 v[212:215], v255 offset:25600\n\t"
"v_mfma_f32_16x16x32_bf16 v[120:123], a[120:123], a[176:179], v[120:123]\n\t"
"v_mfma_f32_16x16x32_bf16 v[120:123], a[64:67], a[180:183], v[120:123]\n\t"
"ds_write_b128 v249, v[20:23] offset:30720\n\t"
"v_mfma_f32_16x16x32_bf16 v[120:123], a[68:71], a[196:199], v[120:123]\n\t"
"s_waitcnt lgkmcnt(4)\n\t"
"v_cvt_pk_bf16_f32 v84, v84, v85\n\t"
"v_cvt_pk_bf16_f32 v85, v86, v87\n\t"
"v_mfma_f32_16x16x32_bf16 v[120:123], a[72:75], a[188:191], v[120:123]\n\t"
"v_mfma_f32_16x16x32_bf16 v[120:123], a[40:43], a[192:195], v[120:123]\n\t"
"ds_write_b64 v250, v[84:85] offset:30720\n\t"
"v_mfma_f32_16x16x32_bf16 v[152:155], a[44:47], a[184:187], v[152:155]\n\t"
"s_waitcnt lgkmcnt(4)\n\t"
"v_mfma_f32_16x16x32_bf16 v[152:155], a[32:35], a[176:179], v[152:155]\n\t"
"v_mfma_f32_16x16x32_bf16 v[152:155], a[36:39], a[180:183], v[152:155]\n\t"
"buffer_store_dwordx4 v[196:199], v248, s[16:19], s29 offen offset:0 sc1\n\t"
"v_mfma_f32_16x16x32_bf16 v[152:155], a[216:219], a[196:199], v[152:155]\n\t"
"s_waitcnt lgkmcnt(3)\n\t"
"v_cvt_pk_bf16_f32 v120, v120, v121\n\t"
"v_cvt_pk_bf16_f32 v121, v122, v123\n\t"
"v_mfma_f32_16x16x32_bf16 v[152:155], a[220:223], a[188:191], v[152:155]\n\t"
"v_mfma_f32_16x16x32_bf16 v[152:155], a[224:227], a[192:195], v[152:155]\n\t"
"buffer_store_dwordx4 v[200:203], v248, s[16:19], s29 offen offset:128 sc1\n\t"
"v_mfma_f32_16x16x32_bf16 v[184:187], a[76:79], a[184:187], v[184:187]\n\t"
"ds_read_b128 a[184:187], v255 offset:26624\n\t"
"v_mfma_f32_16x16x32_bf16 v[184:187], a[4:7], a[176:179], v[184:187]\n\t"
"v_mfma_f32_16x16x32_bf16 v[184:187], a[80:83], a[180:183], v[184:187]\n\t"
"buffer_store_dwordx4 v[204:207], v248, s[16:19], s29 offen offset:256 sc1\n\t"
"v_mfma_f32_16x16x32_bf16 v[184:187], a[8:11], a[196:199], v[184:187]\n\t"
"ds_read_b128 a[196:199], v255 offset:27136\n\t"
"v_cvt_pk_bf16_f32 v122, v152, v153\n\t"
"v_mfma_f32_16x16x32_bf16 v[184:187], a[12:15], a[188:191], v[184:187]\n\t"
"v_cvt_pk_bf16_f32 v123, v154, v155\n\t"
"v_mfma_f32_16x16x32_bf16 v[184:187], a[228:231], a[192:195], v[184:187]\n\t"
"ds_write_b128 v249, v[120:123] offset:36864\n\t"
"v_mfma_f32_16x16x32_bf16 v[24:27], a[148:151], a[168:171], v[24:27]\n\t"
"s_waitcnt lgkmcnt(5)\n\t"
"v_mfma_f32_16x16x32_bf16 v[24:27], a[236:239], a[200:203], v[24:27]\n\t"
"v_mfma_f32_16x16x32_bf16 v[24:27], a[240:243], v[208:211], v[24:27]\n\t"
"v_mfma_f32_16x16x32_bf16 v[24:27], a[244:247], a[172:175], v[24:27]\n\t"
"v_cvt_pk_bf16_f32 v184, v184, v185\n\t"
"v_cvt_pk_bf16_f32 v185, v186, v187\n\t"
"v_mfma_f32_16x16x32_bf16 v[24:27], a[104:107], a[204:207], v[24:27]\n\t"
"v_mfma_f32_16x16x32_bf16 v[24:27], a[156:159], v[212:215], v[24:27]\n\t"
"ds_write_b64 v250, v[184:185] offset:36864\n\t"
"v_mfma_f32_16x16x32_bf16 v[56:59], a[164:167], a[168:171], v[56:59]\n\t"
"v_mfma_f32_16x16x32_bf16 v[56:59], v[240:243], a[200:203], v[56:59]\n\t"
"v_mfma_f32_16x16x32_bf16 v[56:59], v[96:99], v[208:211], v[56:59]\n\t"
"v_mfma_f32_16x16x32_bf16 v[56:59], a[248:251], a[172:175], v[56:59]\n\t"
"v_cvt_pk_bf16_f32 v24, v24, v25\n\t"
"v_cvt_pk_bf16_f32 v25, v26, v27\n\t"
"v_mfma_f32_16x16x32_bf16 v[56:59], a[52:55], a[204:207], v[56:59]\n\t"
"v_mfma_f32_16x16x32_bf16 v[56:59], a[56:59], v[212:215], v[56:59]\n\t"
"v_mfma_f32_16x16x32_bf16 v[88:91], a[48:51], a[168:171], v[88:91]\n\t"
"ds_read_b128 a[168:171], v255 offset:28160\n\t"
"v_mfma_f32_16x16x32_bf16 v[88:91], v[236:239], a[200:203], v[88:91]\n\t"
"v_mfma_f32_16x16x32_bf16 v[88:91], a[60:63], v[208:211], v[88:91]\n\t"
"s_waitcnt lgkmcnt(1)\n\t"
"s_barrier\n\t"
"v_mfma_f32_16x16x32_bf16 v[88:91], a[212:215], a[172:175], v[88:91]\n\t"
"ds_read_b128 v[196:199], v252 offset:30720\n\t"
"v_mfma_f32_16x16x32_bf16 v[88:91], a[252:255], a[204:207], v[88:91]\n\t"
"ds_read_b128 v[200:203], v252 offset:31744\n\t"
"v_cvt_pk_bf16_f32 v26, v56, v57\n\t"
"v_mfma_f32_16x16x32_bf16 v[88:91], a[28:31], v[212:215], v[88:91]\n\t"
"ds_read_b128 v[204:207], v252 offset:32768\n\t"
"v_cvt_pk_bf16_f32 v27, v58, v59\n\t"
"v_mfma_f32_16x16x32_bf16 v[124:127], a[112:115], a[176:179], v[124:127]\n\t"
"s_add_u32 s29, s23, 983040\n\t"
"ds_read_b128 a[172:175], v255 offset:28672\n\t"
"v_mfma_f32_16x16x32_bf16 v[124:127], a[120:123], a[180:183], v[124:127]\n\t"
"v_mfma_f32_16x16x32_bf16 v[124:127], a[64:67], a[184:187], v[124:127]\n\t"
"ds_write_b128 v249, v[24:27] offset:18432\n\t"
"v_mfma_f32_16x16x32_bf16 v[124:127], a[68:71], a[188:191], v[124:127]\n\t"
"s_waitcnt lgkmcnt(4)\n\t"
"v_cvt_pk_bf16_f32 v88, v88, v89\n\t"
"v_cvt_pk_bf16_f32 v89, v90, v91\n\t"
"v_mfma_f32_16x16x32_bf16 v[124:127], a[72:75], a[192:195], v[124:127]\n\t"
"v_mfma_f32_16x16x32_bf16 v[124:127], a[40:43], a[196:199], v[124:127]\n\t"
"ds_write_b64 v250, v[88:89] offset:18432\n\t"
"v_mfma_f32_16x16x32_bf16 v[156:159], a[44:47], a[176:179], v[156:159]\n\t"
"s_waitcnt lgkmcnt(4)\n\t"
"v_mfma_f32_16x16x32_bf16 v[156:159], a[32:35], a[180:183], v[156:159]\n\t"
"v_mfma_f32_16x16x32_bf16 v[156:159], a[36:39], a[184:187], v[156:159]\n\t"
"buffer_store_dwordx4 v[196:199], v248, s[16:19], s29 offen offset:0 sc1\n\t"
"v_mfma_f32_16x16x32_bf16 v[156:159], a[216:219], a[188:191], v[156:159]\n\t"
"s_waitcnt lgkmcnt(3)\n\t"
"v_cvt_pk_bf16_f32 v124, v124, v125\n\t"
"v_cvt_pk_bf16_f32 v125, v126, v127\n\t"
"v_mfma_f32_16x16x32_bf16 v[156:159], a[220:223], a[192:195], v[156:159]\n\t"
"v_mfma_f32_16x16x32_bf16 v[156:159], a[224:227], a[196:199], v[156:159]\n\t"
"buffer_store_dwordx4 v[200:203], v248, s[16:19], s29 offen offset:128 sc1\n\t"
"v_mfma_f32_16x16x32_bf16 v[188:191], a[76:79], a[176:179], v[188:191]\n\t"
"ds_read_b128 a[176:179], v255 offset:29696\n\t"
"v_mfma_f32_16x16x32_bf16 v[188:191], a[4:7], a[180:183], v[188:191]\n\t"
"v_mfma_f32_16x16x32_bf16 v[188:191], a[80:83], a[184:187], v[188:191]\n\t"
"buffer_store_dwordx4 v[204:207], v248, s[16:19], s29 offen offset:256 sc1\n\t"
"v_mfma_f32_16x16x32_bf16 v[188:191], a[8:11], a[188:191], v[188:191]\n\t"
"ds_read_b128 a[188:191], v255 offset:30208\n\t"
"v_cvt_pk_bf16_f32 v126, v156, v157\n\t"
"v_mfma_f32_16x16x32_bf16 v[188:191], a[12:15], a[192:195], v[188:191]\n\t"
"v_cvt_pk_bf16_f32 v127, v158, v159\n\t"
"v_mfma_f32_16x16x32_bf16 v[188:191], a[228:231], a[196:199], v[188:191]\n\t"
"ds_write_b128 v249, v[124:127] offset:24576\n\t"
"v_mfma_f32_16x16x32_bf16 v[28:31], a[148:151], a[200:203], v[28:31]\n\t"
"s_waitcnt lgkmcnt(5)\n\t"
"v_mfma_f32_16x16x32_bf16 v[28:31], a[236:239], v[208:211], v[28:31]\n\t"
"v_mfma_f32_16x16x32_bf16 v[28:31], a[240:243], a[168:171], v[28:31]\n\t"
"v_mfma_f32_16x16x32_bf16 v[28:31], a[244:247], a[204:207], v[28:31]\n\t"
"v_cvt_pk_bf16_f32 v188, v188, v189\n\t"
"v_cvt_pk_bf16_f32 v189, v190, v191\n\t"
"v_mfma_f32_16x16x32_bf16 v[28:31], a[104:107], v[212:215], v[28:31]\n\t"
"v_mfma_f32_16x16x32_bf16 v[28:31], a[156:159], a[172:175], v[28:31]\n\t"
"ds_write_b64 v250, v[188:189] offset:24576\n\t"
"v_mfma_f32_16x16x32_bf16 v[60:63], a[164:167], a[200:203], v[60:63]\n\t"
"v_mfma_f32_16x16x32_bf16 v[60:63], v[240:243], v[208:211], v[60:63]\n\t"
"v_mfma_f32_16x16x32_bf16 v[60:63], v[96:99], a[168:171], v[60:63]\n\t"
"v_mfma_f32_16x16x32_bf16 v[60:63], a[248:251], a[204:207], v[60:63]\n\t"
"v_cvt_pk_bf16_f32 v28, v28, v29\n\t"
"v_cvt_pk_bf16_f32 v29, v30, v31\n\t"
"v_mfma_f32_16x16x32_bf16 v[60:63], a[52:55], v[212:215], v[60:63]\n\t"
"v_mfma_f32_16x16x32_bf16 v[60:63], a[56:59], a[172:175], v[60:63]\n\t"
"v_mfma_f32_16x16x32_bf16 v[92:95], a[48:51], a[200:203], v[92:95]\n\t"
"v_mfma_f32_16x16x32_bf16 v[92:95], v[236:239], v[208:211], v[92:95]\n\t"
"v_mfma_f32_16x16x32_bf16 v[92:95], a[60:63], a[168:171], v[92:95]\n\t"
"s_waitcnt lgkmcnt(0)\n\t"
"s_barrier\n\t"
"v_mfma_f32_16x16x32_bf16 v[92:95], a[212:215], a[204:207], v[92:95]\n\t"
"ds_read_b128 v[196:199], v252 offset:18432\n\t"
"v_mfma_f32_16x16x32_bf16 v[92:95], a[252:255], v[212:215], v[92:95]\n\t"
"ds_read_b128 v[200:203], v252 offset:19456\n\t"
"v_cvt_pk_bf16_f32 v30, v60, v61\n\t"
"v_mfma_f32_16x16x32_bf16 v[92:95], a[28:31], a[172:175], v[92:95]\n\t"
"ds_read_b128 v[204:207], v252 offset:20480\n\t"
"v_cvt_pk_bf16_f32 v31, v62, v63\n\t"
"v_mfma_f32_16x16x32_bf16 v[128:131], a[112:115], a[180:183], v[128:131]\n\t"
"s_add_u32 s29, s23, 1179648\n\t"
"v_mfma_f32_16x16x32_bf16 v[128:131], a[120:123], a[184:187], v[128:131]\n\t"
"v_mfma_f32_16x16x32_bf16 v[128:131], a[64:67], a[176:179], v[128:131]\n\t"
"ds_write_b128 v249, v[28:31] offset:30720\n\t"
"v_mfma_f32_16x16x32_bf16 v[128:131], a[68:71], a[192:195], v[128:131]\n\t"
"s_waitcnt lgkmcnt(3)\n\t"
"v_cvt_pk_bf16_f32 v92, v92, v93\n\t"
"v_cvt_pk_bf16_f32 v93, v94, v95\n\t"
"v_mfma_f32_16x16x32_bf16 v[128:131], a[72:75], a[196:199], v[128:131]\n\t"
"v_mfma_f32_16x16x32_bf16 v[128:131], a[40:43], a[188:191], v[128:131]\n\t"
"ds_write_b64 v250, v[92:93] offset:30720\n\t"
"v_mfma_f32_16x16x32_bf16 v[160:163], a[44:47], a[180:183], v[160:163]\n\t"
"s_waitcnt lgkmcnt(3)\n\t"
"v_mfma_f32_16x16x32_bf16 v[160:163], a[32:35], a[184:187], v[160:163]\n\t"
"v_mfma_f32_16x16x32_bf16 v[160:163], a[36:39], a[176:179], v[160:163]\n\t"
"buffer_store_dwordx4 v[196:199], v248, s[16:19], s29 offen offset:0 sc1\n\t"
"v_mfma_f32_16x16x32_bf16 v[160:163], a[216:219], a[192:195], v[160:163]\n\t"
"s_waitcnt lgkmcnt(2)\n\t"
"v_cvt_pk_bf16_f32 v128, v128, v129\n\t"
"v_cvt_pk_bf16_f32 v129, v130, v131\n\t"
"v_mfma_f32_16x16x32_bf16 v[160:163], a[220:223], a[196:199], v[160:163]\n\t"
"v_mfma_f32_16x16x32_bf16 v[160:163], a[224:227], a[188:191], v[160:163]\n\t"
"buffer_store_dwordx4 v[200:203], v248, s[16:19], s29 offen offset:128 sc1\n\t"
"v_mfma_f32_16x16x32_bf16 v[192:195], a[76:79], a[180:183], v[192:195]\n\t"
"v_mfma_f32_16x16x32_bf16 v[192:195], a[4:7], a[184:187], v[192:195]\n\t"
"v_mfma_f32_16x16x32_bf16 v[192:195], a[80:83], a[176:179], v[192:195]\n\t"
"buffer_store_dwordx4 v[204:207], v248, s[16:19], s29 offen offset:256 sc1\n\t"
"v_mfma_f32_16x16x32_bf16 v[192:195], a[8:11], a[192:195], v[192:195]\n\t"
"v_cvt_pk_bf16_f32 v130, v160, v161\n\t"
"v_cvt_pk_bf16_f32 v131, v162, v163\n\t"
"v_mfma_f32_16x16x32_bf16 v[192:195], a[12:15], a[196:199], v[192:195]\n\t"
"v_mfma_f32_16x16x32_bf16 v[192:195], a[228:231], a[188:191], v[192:195]\n\t"
"ds_write_b128 v249, v[128:131] offset:36864\n\t"
"s_nop 6\n\t"
"v_cvt_pk_bf16_f32 v192, v192, v193\n\t"
"v_cvt_pk_bf16_f32 v193, v194, v195\n\t"
"ds_write_b64 v250, v[192:193] offset:36864\n\t"
"s_waitcnt lgkmcnt(0)\n\t"
"s_barrier\n\t"
"ds_read_b128 v[196:199], v252 offset:30720\n\t"
"ds_read_b128 v[200:203], v252 offset:31744\n\t"
"ds_read_b128 v[208:211], v252 offset:32768\n\t"
"s_waitcnt lgkmcnt(2)\n\t"
"s_add_u32 s29, s23, 1376256\n\t"
"buffer_store_dwordx4 v[196:199], v248, s[16:19], s29 offen offset:0 sc1\n\t"
"s_waitcnt lgkmcnt(1)\n\t"
"buffer_store_dwordx4 v[200:203], v248, s[16:19], s29 offen offset:128 sc1\n\t"
"s_waitcnt lgkmcnt(0)\n\t"
"buffer_store_dwordx4 v[208:211], v248, s[16:19], s29 offen offset:256 sc1\n\t"
"s_waitcnt vmcnt(0)\n\t"
"Lwino_end:\n\t"
  : "+{v205}"(tid)
  : "s"((unsigned long long)__builtin_amdgcn_kernarg_segment_ptr()), "s"(blockIdx.x), "s"((unsigned long long)lane_tab), "s"((unsigned)(size_t)(__attribute__((address_space(3))) char*)smem)
  : "v0", "v1", "v2", "v3", "v4", "v5", "v6", "v7", "v8", "v9", "v10", "v11", "v12", "v13", "v14", "v15", "v16", "v17", "v18", "v19", "v20", "v21", "v22", "v23", "v24", "v25", "v26", "v27", "v28", "v29", "v30", "v31", "v32", "v33", "v34", "v35", "v36", "v37", "v38", "v39", "v40", "v41", "v42", "v43", "v44", "v45", "v46", "v47", "v48", "v49", "v50", "v51", "v52", "v53", "v54", "v55", "v56", "v57", "v58", "v59", "v60", "v61", "v62", "v63", "v64", "v65", "v66", "v67", "v68", "v69", "v70", "v71", "v72", "v73", "v74", "v75", "v76", "v77", "v78", "v79", "v80", "v81", "v82", "v83", "v84", "v85", "v86", "v87", "v88", "v89", "v90", "v91", "v92", "v93", "v94", "v95", "v96", "v97", "v98", "v99", "v100", "v101", "v102", "v103", "v104", "v105", "v106", "v107", "v108", "v109", "v110", "v111", "v112", "v113", "v114", "v115", "v116", "v117", "v118", "v119", "v120", "v121", "v122", "v123", "v124", "v125", "v126", "v127", "v128", "v129", "v130", "v131", "v132", "v133", "v134", "v135", "v136", "v137", "v138", "v139", "v140", "v141", "v142", "v143", "v144", "v145", "v146", "v147", "v148", "v149", "v150", "v151", "v152", "v153", "v154", "v155", "v156", "v157", "v158", "v159", "v160", "v161", "v162", "v163", "v164", "v165", "v166", "v167", "v168", "v169", "v170", "v171", "v172", "v173", "v174", "v175", "v176", "v177", "v178", "v179", "v180", "v181", "v182", "v183", "v184", "v185", "v186", "v187", "v188", "v189", "v190", "v191", "v192", "v193", "v194", "v195", "v196", "v197", "v198", "v199", "v200", "v201", "v202", "v203", "v204", "v206", "v207", "v208", "v209", "v210", "v211", "v212", "v213", "v214", "v215", "v216", "v217", "v218", "v219", "v220", "v221", "v222", "v223", "v224", "v225", "v226", "v227", "v228", "v229", "v230", "v231", "v232", "v233", "v234", "v235", "v236", "v237", "v238", "v239", "v240", "v241", "v242", "v243", "v244", "v245", "v246", "v247", "v248", "v249", "v250", "v251", "v252", "v253", "v254", "v255", "a0", "a1", "a2", "a3", "a4", "a5", "a6", "a7", "a8", "a9", "a10", "a11", "a12", "a13", "a14", "a15", "a16", "a17", "a18", "a19", "a20", "a21", "a22", "a23", "a24", "a25", "a26", "a27", "a28", "a29", "a30", "a31", "a32", "a33", "a34", "a35", "a36", "a37", "a38", "a39", "a40", "a41", "a42", "a43", "a44", "a45", "a46", "a47", "a48", "a49", "a50", "a51", "a52", "a53", "a54", "a55", "a56", "a57", "a58", "a59", "a60", "a61", "a62", "a63", "a64", "a65", "a66", "a67", "a68", "a69", "a70", "a71", "a72", "a73", "a74", "a75", "a76", "a77", "a78", "a79", "a80", "a81", "a82", "a83", "a84", "a85", "a86", "a87", "a88", "a89", "a90", "a91", "a92", "a93", "a94", "a95", "a96", "a97", "a98", "a99", "a100", "a101", "a102", "a103", "a104", "a105", "a106", "a107", "a108", "a109", "a110", "a111", "a112", "a113", "a114", "a115", "a116", "a117", "a118", "a119", "a120", "a121", "a122", "a123", "a124", "a125", "a126", "a127", "a128", "a129", "a130", "a131", "a132", "a133", "a134", "a135", "a136", "a137", "a138", "a139", "a140", "a141", "a142", "a143", "a144", "a145", "a146", "a147", "a148", "a149", "a150", "a151", "a152", "a153", "a154", "a155", "a156", "a157", "a158", "a159", "a160", "a161", "a162", "a163", "a164", "a165", "a166", "a167", "a168", "a169", "a170", "a171", "a172", "a173", "a174", "a175", "a176", "a177", "a178", "a179", "a180", "a181", "a182", "a183", "a184", "a185", "a186", "a187", "a188", "a189", "a190", "a191", "a192", "a193", "a194", "a195", "a196", "a197", "a198", "a199", "a200", "a201", "a202", "a203", "a204", "a205", "a206", "a207", "a208", "a209", "a210", "a211", "a212", "a213", "a214", "a215", "a216", "a217", "a218", "a219", "a220", "a221", "a222", "a223", "a224", "a225", "a226", "a227", "a228", "a229", "a230", "a231", "a232", "a233", "a234", "a235", "a236", "a237", "a238", "a239", "a240", "a241", "a242", "a243", "a244", "a245", "a246", "a247", "a248", "a249", "a250", "a251", "a252", "a253", "a254", "a255", "s8", "s9", "s10", "s11", "s12", "s13", "s14", "s15", "s16", "s17", "s18", "s19", "s20", "s21", "s22", "s23", "s24", "s25", "s26", "s27", "s28", "s29", "s30", "s31", "s32", "s33", "s34", "s35", "s36", "s37", "s38", "s39", "s40", "s41", "s42", "s43", "s44", "s45", "s46", "s47", "s48", "s49", "s50", "s51", "s52", "s53", "s54", "s55", "s56", "s57", "s58", "s59", "s60", "s61", "s62", "s63", "s64", "s65", "s66", "s67", "s68", "s69", "s70", "s71", "s72", "s73", "s74", "s75", "s76", "s77", "scc", "vcc", "memory");
}

extern "C" void kernel_launch(void* const* d_in, const int* in_sizes, int n_in, void* d_out, int out_size, void* d_ws,
                              size_t ws_size, hipStream_t stream) {
  wino_main_kernel<<<256, 256, 0, stream>>>((const char*)d_in[0], (char*)d_ws, (char*)d_out, (const char*)d_in[1]);
}
```

```cpp
#include <hip/hip_runtime.h>
#include <hip/hip_bf16.h>
#include <stdint.h>

#define IMG_W 512
#define CH 192
#define PXB 384
#define ROWB (IMG_W * PXB)
#define U_BYTES 1769472

__device__ const __attribute__((aligned(1024))) unsigned lane_tab[6144] = {
  0x0,0x0,0x0,0x0,0x0,0x0,0x0,0x0,0x0,0x0,0x0,0x0,0x0,0x0,0x0,0x0,
  0x0,0x0,0x0,0x0,0x0,0x0,0x0,0x0,0x0,0x0,0x0,0x0,0x0,0x0,0x0,0x0,
  0x0,0x0,0x0,0x0,0x0,0x0,0x0,0x0,0x0,0x0,0x0,0x0,0x0,0x0,0x0,0x0,
  0x0,0x0,0x0,0x0,0x0,0x0,0x0,0x0,0x0,0x0,0x0,0x0,0x0,0x0,0x0,0x0,
  0x0,0x0,0x0,0x0,0x0,0x0,0x0,0x0,0x0,0x0,0x0,0x0,0x0,0x0,0x0,0x0,
  0x0,0x0,0x0,0x0,0x0,0x0,0x0,0x0,0x0,0x0,0x0,0x0,0x0,0x0,0x0,0x0,
  0x0,0x0,0x0,0x0,0x0,0x0,0x0,0x0,0x0,0x0,0x0,0x0,0x0,0x0,0x0,0x0,
  0x0,0x0,0x0,0x0,0x0,0x0,0x0,0x0,0x0,0x0,0x0,0x0,0x0,0x0,0x0,0x0,
  0x0,0x0,0x0,0x0,0x0,0x0,0x0,0x0,0x0,0x0,0x0,0x0,0x0,0x0,0x0,0x0,
  0x0,0x0,0x0,0x0,0x0,0x0,0x0,0x0,0x0,0x0,0x0,0x0,0x0,0x0,0x0,0x0,
  0x0,0x0,0x0,0x0,0x0,0x0,0x0,0x0,0x0,0x0,0x0,0x0,0x0,0x0,0x0,0x0,
  0x0,0x0,0x0,0x0,0x0,0x0,0x0,0x0,0x0,0x0,0x0,0x0,0x0,0x0,0x0,0x0,
  0x0,0x0,0x0,0x0,0x0,0x0,0x0,0x0,0x0,0x0,0x0,0x0,0x0,0x0,0x0,0x0,
  0x0,0x0,0x0,0x0,0x0,0x0,0x0,0x0,0x0,0x0,0x0,0x0,0x0,0x0,0x0,0x0,
  0x0,0x0,0x0,0x0,0x0,0x0,0x0,0x0,0x0,0x0,0x0,0x0,0x0,0x0,0x0,0x0,
  0x0,0x0,0x0,0x0,0x0,0x0,0x0,0x0,0x0,0x0,0x0,0x0,0x0,0x0,0x0,0x0,
  0x0,0xc042,0x0,0x0,0x0,0xc0420000,0x0,0x0,0x0,0x0,0x0,0xc042,0x0,0x0,0x0,0xc0420000,
  0x3f903f90,0x0,0x0,0x0,0x3f900000,0x0,0x3f90,0x0,0x0,0x0,0x3f903f90,0x0,0x0,0x0,0x3f900000,0x0,
  0x0,0x0,0x0,0x0,0x0,0x0,0x0,0x0,0x0,0x0,0x0,0x0,0x0,0x0,0x0,0x0,
  0x0,0x0,0x0,0x0,0x0,0x0,0x0,0x0,0x0,0x0,0x0,0x0,0x0,0x0,0x0,0x0,
  0x0,0x0,0x3f90,0x0,0x0,0x0,0x0,0x0,0x0,0x0,0x0,0x0,0x0,0x0,0x0,0x0,
  0x0,0x0,0x0,0x0,0xc0420000,0x0,0x0,0x0,0x0,0x0,0x0,0x0,0x0,0x0,0xc0420000,0x0,
  0x0,0x3f90,0x0,0x0,0x0,0x3f903f90,0x0,0x0,0x0,0x3f900000,0x0,0x3f90,0x0,0x0,0x0,0x3f903f90,
  0x0,0x3f90,0x0,0x0,0x0,0x3f903f90,0x0,0x0,0x0,0x3f900000,0x0,0x3f90,0x0,0x0,0x0,0x3f903f90,
  0x0,0x0,0x0,0x0,0x0,0x0,0x0,0x0,0x0,0x0,0x0,0x0,0x0,0x0,0x0,0x0,
  0x0,0x0,0x0,0x0,0x0,0x0,0x0,0x0,0x0,0x0,0x0,0x0,0x0,0x0,0x3f90,0x0,
  0x0,0xc042,0x3f900000,0x0,0x0,0xc0420000,0x0,0x0,0x0,0x0,0x0,0xc042,0x0,0x0,0x0,0xc0420000,
  0x0,0xc042,0x3f900000,0x0,0x0,0xc0420000,0x0,0x0,0x0,0x0,0x0,0xc042,0x0,0x0,0x0,0xc0420000,
  0x3f900000,0x0,0x0,0x0,0x3f900000,0x3f90,0x0,0x0,0x0,0x3f90,0x3f900000,0x0,0x0,0x0,0x3f900000,0x3f90,
  0xc042,0x0,0x0,0x0,0x0,0x0,0x0,0x0,0x0,0x0,0xc042,0x0,0x0,0x0,0x0,0x0,
  0x0,0x0,0x0,0x0,0x0,0x0,0x0,0x0,0x0,0x0,0x0,0x0,0x0,0x0,0x0,0x0,
  0x0,0x0,0x0,0x0,0x0,0x0,0x0,0x0,0x0,0x0,0x0,0x0,0x0,0x0,0x0,0x0,
  0xa8000000,0x0,0xa800,0x60,0xa8080040,0x400040,0xa808,0x600000,0xa8100090,0x900090,0xa810,0x6000,0xa81800d0,0xd000d0,0xa818,0x60000000,
  0xa9200120,0x24000120,0xa840,0x40004,0xa9280160,0x24400160,0xa848,0x40400,0xa93001b0,0x249001b0,0xa850,0x4000400,0xa93801f0,0x24d001f0,0xa858,0x4000000,
  0xa8401000,0x2002408,0xa880,0x0,0xa8481040,0x2402448,0xa888,0x0,0xa8501090,0x2902498,0xa890,0x0,0xa85810d0,0x2d024d8,0xa898,0x0,
  0xa9601120,0x2002528,0xa8c0,0x0,0xa9681160,0x2402568,0xa8c8,0x0,0xa97011b0,0x29025b8,0xa8d0,0x0,0xa97811f0,0x2d025f8,0xa8d8,0x0,
  0xea800010,0x100010,0xa920,0x400,0xea880050,0x500050,0xa928,0x0,0xea900080,0x800080,0xa930,0x0,0xea9800c0,0xc000c0,0xa938,0x0,
  0xa9a00130,0x24100130,0xa960,0x0,0xa9a80170,0x24500170,0xa968,0x60000,0xa9b001a0,0x248001a0,0xa970,0x0,0xa9b801e0,0x24c001e0,0xa978,0x6000000,
  0xc0c01010,0x2102418,0xa9a0,0x40,0xc0c81050,0x2502458,0xa9a8,0x400040,0xc0d01080,0x2802488,0xa9b0,0x404000,0xc0d810c0,0x2c024c8,0xa9b8,0x40004000,
  0xc1e01130,0x2102538,0xa9e0,0x40,0xc1e81170,0x2502578,0xa9e8,0x400040,0xc1f011a0,0x28025a8,0xa9f0,0x404000,0xc1f811e0,0x2c025e8,0xa9f8,0x40004000,
  0xae000020,0x200020,0xae00,0x0,0xae080060,0x600060,0xae08,0x0,0xae1000b0,0xb000b0,0xae10,0x0,0xae1800f0,0xf000f0,0xae18,0x0,
  0xeb200100,0x24200100,0xae40,0x0,0xeb280140,0x24600140,0xae48,0x0,0xeb300190,0x24b00190,0xae50,0x0,0xeb3801d0,0x24f001d0,0xae58,0x400,
  0xc0401020,0x2202428,0xae80,0x4000060,0xc0481060,0x2602468,0xae88,0x600000,0xc05010b0,0x2b024b8,0xae90,0x6000,0xc05810f0,0x2f024f8,0xae98,0x60000000,
  0xc1601100,0x2202508,0xaec0,0x4000060,0xc1681140,0x2602548,0xaec8,0x600000,0xc1701190,0x2b02598,0xaed0,0x6000,0xc17811d0,0x2f025d8,0xaed8,0x60000000,
  0xa8800030,0x300030,0xaf20,0x40000,0xa8880070,0x700070,0xaf28,0x40040,0xa89000a0,0xa000a0,0xaf30,0x4000040,0xa89800e0,0xe000e0,0xaf38,0x4004000,
  0xa8c00110,0x24300110,0xaf60,0x6,0xa8c80150,0x24700150,0xaf68,0x0,0xa8d00180,0x24a00180,0xaf70,0x600,0xa8d801c0,0x24e001c0,0xaf78,0x0,
  0xa9e01030,0x2302438,0xafa0,0x0,0xa9e81070,0x2702478,0xafa8,0x0,0xa9f010a0,0x2a024a8,0xafb0,0x0,0xa9f810e0,0x2e024e8,0xafb8,0x0,
  0xae001110,0x2302518,0xafe0,0x0,0xae081150,0x2702558,0xafe8,0x0,0xae101180,0x2a02588,0xaff0,0x0,0xae1811c0,0x2e025c8,0xaff8,0x0,
  0x0,0x0,0x0,0x0,0x0,0x0,0x0,0x0,0x0,0x0,0x0,0x0,0x0,0x0,0x0,0x0,
  0x0,0x0,0x0,0x0,0x0,0x0,0x0,0x0,0x0,0x0,0x0,0x0,0x0,0x0,0x0,0x0,
  0x0,0x0,0x0,0x0,0x0,0x0,0x0,0x0,0x0,0x0,0x0,0x0,0x0,0x0,0x0,0x0,
  0x0,0x0,0x0,0x0,0x0,0x0,0x0,0x0,0x0,0x0,0x0,0x0,0x0,0x0,0x0,0x0,
  0x0,0x0,0x0,0x0,0x0,0x0,0x0,0x0,0x0,0x0,0x0,0x0,0x0,0x0,0x0,0x0,
  0x0,0x0,0x0,0x0,0x0,0x0,0x0,0x0,0x0,0x0,0x0,0x0,0x0,0x0,0x0,0x0,
  0x0,0x0,0x0,0x0,0x0,0x0,0x0,0x0,0x0,0x0,0x0,0x0,0x0,0x0,0x0,0x0,
  0x0,0x0,0x0,0x0,0x0,0x0,0x0,0x0,0x0,0x0,0x0,0x0,0x0,0x0,0x0,0x0,
  0x0,0x0,0x0,0x0,0x0,0x0,0x0,0x0,0x0,0x0,0x0,0x0,0x0,0x0,0x0,0x0,
  0x0,0x0,0x0,0x0,0x0,0x0,0x0,0x0,0x0,0x0,0x0,0x0,0x0,0x0,0x0,0x0,
  0x0,0x0,0x0,0x0,0x0,0x0,0x0,0x0,0x0,0x0,0x0,0x0,0x0,0x0,0x0,0x0,
  0x0,0x0,0x0,0x0,0x0,0x0,0x0,0x0,0x0,0x0,0x0,0x0,0x0,0x0,0x0,0x0,
  0x0,0x0,0x0,0x0,0x0,0x0,0x0,0x0,0x0,0x0,0x0,0x0,0x0,0x0,0x0,0x0,
  0x0,0x0,0x0,0x0,0x0,0x0,0x0,0x0,0x0,0x0,0x0,0x0,0x0,0x0,0x0,0x0,
  0x0,0x0,0x0,0x0,0x0,0x0,0x0,0x0,0x0,0x0,0x0,0x0,0x0,0x0,0x0,0x0,
  0x0,0x0,0x0,0x0,0x0,0x0,0x0,0x0,0x0,0x0,0x0,0x0,0x0,0x0,0x0,0x0,
  0xc0420000,0x0,0x0,0x0,0x0,0x0,0x0,0x0,0x0,0x0,0xc0420000,0x0,0x0,0x0,0x0,0x0,
  0x3f90,0x0,0x0,0x0,0x0,0x0,0x3f90,0x0,0x0,0x0,0x3f90,0x0,0x0,0x0,0x0,0x0,
  0x0,0xc042,0x0,0x0,0x0,0xc0420000,0x0,0x0,0x0,0x0,0x0,0xc042,0x0,0x0,0x0,0xc0420000,
  0x0,0xc042,0x0,0x0,0x0,0xc0420000,0x0,0x0,0x0,0x0,0x0,0xc042,0x0,0x0,0x0,0xc0420000,
  0x0,0x0,0x0,0x3f90,0x0,0x0,0x0,0x0,0x0,0x0,0x0,0x0,0x0,0x0,0x0,0x0,
  0x0,0x0,0x0,0x0,0xc042,0x0,0x0,0x0,0x0,0x0,0x0,0x0,0x0,0x0,0xc042,0x0,
  0x3f900000,0x0,0x0,0x3f900000,0x3f900000,0x0,0x0,0x0,0x0,0x0,0x3f900000,0x0,0x0,0x0,0x3f900000,0x0,
  0x3f900000,0x0,0x0,0x3f900000,0x3f900000,0x0,0x0,0x0,0x0,0x0,0x3f900000,0x0,0x0,0x0,0x3f900000,0x0,
  0x0,0x0,0x0,0x0,0x0,0xc042,0x0,0x0,0x0,0x0,0x0,0x0,0x0,0x0,0x0,0xc042,
  0x3f90,0xc0420000,0x0,0x0,0x3f90,0x0,0x0,0x0,0x0,0x0,0x3f90,0xc0420000,0x0,0x0,0x3f903f90,0x0,
  0x0,0x0,0x0,0x0,0x0,0x0,0x0,0x0,0x0,0x0,0x0,0x0,0x0,0x0,0x0,0x0,
  0x0,0x0,0x0,0x0,0x0,0x0,0x0,0x0,0x0,0x0,0x0,0x0,0x0,0x0,0x0,0x0,
  0x3f90,0x0,0x0,0x0,0x3f903f90,0x0,0x0,0x0,0x3f900000,0x0,0x3f90,0x0,0x0,0x0,0x3f903f90,0x0,
  0x0,0x0,0x0,0x0,0x0,0x0,0x0,0x0,0x0,0x0,0x0,0x0,0x0,0x0,0x0,0x0,
  0x0,0x0,0x0,0x0,0x0,0x3f90,0x0,0x0,0x0,0x3f90,0x0,0x0,0x0,0x0,0x0,0x3f90,
  0x0,0x0,0x0,0x0,0x0,0x3f90,0x0,0x0,0x0,0x3f90,0x0,0x0,0x0,0x0,0x0,0x3f90,
  0xb4000000,0x1200008,0xa820,0x60000,0xb4080040,0x1600048,0xa828,0x0,0xb4100090,0x1b00098,0xa830,0x6000000,0xb41800d0,0x1f000d8,0xa838,0x0,
  0xb4400120,0x25200128,0xa860,0x4,0xb4480160,0x25600168,0xa868,0x400,0xb45001b0,0x25b001b8,0xa870,0x400,0xb45801f0,0x25f001f8,0xa878,0x0,
  0xcc401000,0x3202400,0xa8a0,0x60,0xcc481040,0x3602440,0xa8a8,0x600000,0xcc501090,0x3b02490,0xa8b0,0x6000,0xcc5810d0,0x3f024d0,0xa8b8,0x60000000,
  0xcd601120,0x3202520,0xa8e0,0x60,0xcd681160,0x3602560,0xa8e8,0x600000,0xcd7011b0,0x3b025b0,0xa8f0,0x6000,0xcd7811f0,0x3f025f0,0xa8f8,0x60000000,
  0xb5a00010,0x1300018,0xa900,0x4000,0xb5a80050,0x1700058,0xa908,0x0,0xb5b00080,0x1a00088,0xa910,0x0,0xb5b800c0,0x1e000c8,0xa918,0x0,
  0xccc00130,0x25300138,0xa940,0x0,0xccc80170,0x25700178,0xa948,0x6,0xccd001a0,0x25a001a8,0xa950,0x0,0xccd801e0,0x25e001e8,0xa958,0x600,
  0xa9e01010,0x3302410,0xa980,0x40040000,0xa9e81050,0x3702450,0xa988,0x40000,0xa9f01080,0x3a02480,0xa990,0x4000000,0xa9f810c0,0x3e024c0,0xa998,0x4000000,
  0xc1e01130,0x3302530,0xa9c0,0x40040000,0xc1e81170,0x3702570,0xa9c8,0x40000,0xc1f011a0,0x3a025a0,0xa9d0,0x4000000,0xc1f811e0,0x3e025e0,0xa9d8,0x4000000,
  0xb5200020,0x1000028,0xae20,0x0,0xb5280060,0x1400068,0xae28,0x60,0xb53000b0,0x19000b8,0xae30,0x0,0xb53800f0,0x1d000f8,0xae38,0x6000,
  0xea400100,0x25000108,0xae60,0x600004,0xea480140,0x25400148,0xae68,0x4,0xea500190,0x25900198,0xae70,0x60000400,0xea5801d0,0x25d001d8,0xae78,0x4000400,
  0xb5601020,0x3002420,0xaea0,0x0,0xb5681060,0x3402460,0xaea8,0x0,0xb57010b0,0x39024b0,0xaeb0,0x0,0xb57810f0,0x3d024f0,0xaeb8,0x0,
  0xb4801100,0x3002500,0xaee0,0x0,0xb4881140,0x3402540,0xaee8,0x0,0xb4901190,0x3902590,0xaef0,0x0,0xb49811d0,0x3d025d0,0xaef8,0x0,
  0xb4c00030,0x1100038,0xaf00,0x4,0xb4c80070,0x1500078,0xaf08,0x40004,0xb4d000a0,0x18000a8,0xaf10,0x40400,0xb4d800e0,0x1c000e8,0xaf18,0x4000400,
  0xb5e00110,0x25100118,0xaf40,0x0,0xb5e80150,0x25500158,0xaf48,0x0,0xb5f00180,0x25800188,0xaf50,0x0,0xb5f801c0,0x25c001c8,0xaf58,0x0,
  0xcde01030,0x3102430,0xaf80,0x0,0xcde81070,0x3502470,0xaf88,0x40,0xcdf010a0,0x38024a0,0xaf90,0x40,0xcdf810e0,0x3c024e0,0xaf98,0x4000,
  0xb5201110,0x3102510,0xafc0,0x0,0xb5281150,0x3502550,0xafc8,0x40,0xb5301180,0x3802580,0xafd0,0x40,0xb53811c0,0x3c025c0,0xafd8,0x4000,
  0x0,0x0,0x0,0x0,0x0,0x0,0x0,0x0,0x0,0x0,0x0,0x0,0x0,0x0,0x0,0x0,
  0x0,0x0,0x0,0x0,0x0,0x0,0x0,0x0,0x0,0x0,0x0,0x0,0x0,0x0,0x0,0x0,
  0x0,0x0,0x0,0x0,0x0,0x0,0x0,0x0,0x0,0x0,0x0,0x0,0x0,0x0,0x0,0x0,
  0x0,0x0,0x0,0x0,0x0,0x0,0x0,0x0,0x0,0x0,0x0,0x0,0x0,0x0,0x0,0x0,
  0x0,0x0,0x0,0x0,0x0,0x0,0x0,0x0,0x0,0x0,0x0,0x0,0x0,0x0,0x0,0x0,
  0x0,0x0,0x0,0x0,0x0,0x0,0x0,0x0,0x0,0x0,0x0,0x0,0x0,0x0,0x0,0x0,
  0x0,0x0,0x0,0x0,0x0,0x0,0x0,0x0,0x0,0x0,0x0,0x0,0x0,0x0,0x0,0x0,
  0x0,0x0,0x0,0x0,0x0,0x0,0x0,0x0,0x0,0x0,0x0,0x0,0x0,0x0,0x0,0x0,
  0x0,0x0,0x0,0x0,0x0,0x0,0x0,0x0,0x0,0x0,0x0,0x0,0x0,0x0,0x0,0x0,
  0x0,0x0,0x0,0x0,0x0,0x0,0x0,0x0,0x0,0x0,0x0,0x0,0x0,0x0,0x0,0x0,
  0x0,0x0,0x0,0x0,0x0,0x0,0x0,0x0,0x0,0x0,0x0,0x0,0x0,0x0,0x0,0x0,
  0x0,0x0,0x0,0x0,0x0,0x0,0x0,0x0,0x0,0x0,0x0,0x0,0x0,0x0,0x0,0x0,
  0x0,0x0,0x0,0x0,0x0,0x0,0x0,0x0,0x0,0x0,0x0,0x0,0x0,0x0,0x0,0x0,
  0x0,0x0,0x0,0x0,0x0,0x0,0x0,0x0,0x0,0x0,0x0,0x0,0x0,0x0,0x0,0x0,
  0x0,0x0,0x0,0x0,0x0,0x0,0x0,0x0,0x0,0x0,0x0,0x0,0x0,0x0,0x0,0x0,
  0x0,0x0,0x0,0x0,0x0,0x0,0x0,0x0,0x0,0x0,0x0,0x0,0x0,0x0,0x0,0x0,
  0x3f90,0x3f90,0x0,0x0,0x0,0x3f90,0x3f90,0x0,0x0,0x0,0x3f90,0x3f90,0x0,0x0,0x0,0x3f90,
  0x0,0xc0420000,0x0,0x0,0x0,0x0,0x0,0x0,0x0,0x0,0x0,0xc0420000,0x0,0x0,0x0,0x0,
  0x3f900000,0x0,0x0,0x0,0x0,0x0,0x3f900000,0x0,0x0,0x0,0x3f900000,0x0,0x0,0x0,0x0,0x0,
  0x3f900000,0x0,0x0,0x0,0x0,0x0,0x3f900000,0x0,0x0,0x0,0x3f900000,0x0,0x0,0x0,0x0,0x0,
  0xc042,0x0,0x0,0x0,0xc0420000,0x0,0x0,0x0,0x0,0x0,0xc042,0x0,0x0,0x0,0xc0420000,0x0,
  0x0,0x0,0x0,0x0,0x0,0x3f900000,0x0,0x0,0x0,0x3f900000,0x0,0x0,0x0,0x0,0x0,0x3f900000,
  0x0,0x0,0x0,0x0,0x0,0xc042,0x0,0x0,0x0,0x0,0x0,0x0,0x0,0x0,0x0,0xc042,
  0x0,0x0,0x0,0x0,0x0,0xc042,0x0,0x0,0x0,0x0,0x0,0x0,0x0,0x0,0x0,0xc042,
  0x0,0x0,0x0,0x0,0x0,0x0,0x0,0x0,0x0,0x0,0x0,0x0,0x0,0x0,0x3f90,0x0,
  0x0,0x0,0x0,0x0,0x0,0xc0420000,0x0,0x0,0x0,0x0,0x0,0x0,0x0,0x0,0x0,0xc0420000,
  0x0,0x3f90,0x0,0x0,0x0,0x3f90,0x0,0x0,0x0,0x0,0x0,0x3f90,0x0,0x0,0x3f900000,0x3f90,
  0x0,0x3f90,0x0,0x0,0x0,0x3f90,0x0,0x0,0x0,0x0,0x0,0x3f90,0x0,0x0,0x3f900000,0x3f90,
  0x0,0x0,0x0,0x0,0x0,0x0,0x0,0x0,0x0,0x0,0x0,0x0,0x0,0x0,0x0,0x0,
  0x0,0x3f90,0x3f900000,0x0,0x0,0x3f90,0x0,0x0,0x0,0x0,0x0,0x3f90,0x0,0x0,0x0,0x3f90,
  0xc042,0x0,0x0,0x0,0x0,0x0,0x0,0x0,0x0,0x0,0xc042,0x0,0x0,0x0,0x0,0x0,
  0xc042,0x0,0x0,0x0,0x0,0x0,0x0,0x0,0x0,0x0,0xc042,0x0,0x0,0x0,0x0,0x0,
  0xc0000000,0x26001200,0x19200,0x44,0xc0080040,0x26401240,0x19208,0x440,0xc0100090,0x26901290,0x19210,0x4400,0xc01800d0,0x26d012d0,0x19218,0x4000,
  0xd8000120,0x4001320,0x19240,0x600000,0xd8080160,0x4401360,0x19248,0x0,0xd81001b0,0x49013b0,0x19250,0x60000000,0xd81801f0,0x4d013f0,0x19258,0x0,
  0xc1201000,0x28003608,0x19280,0x40000,0xc1281040,0x28403648,0x19288,0x4000000,0xc1301090,0x28903698,0x19290,0x4000000,0xc13810d0,0x28d036d8,0x19298,0x0,
  0xd8401120,0x28003728,0x192c0,0x40000,0xd8481160,0x28403768,0x192c8,0x4000000,0xd85011b0,0x289037b8,0x192d0,0x4000000,0xd85811f0,0x28d037f8,0x192d8,0x0,
  0xc0800010,0x26101210,0x19320,0x6,0xc0880050,0x26501250,0x19328,0x60000,0xc0900080,0x26801280,0x19330,0x600,0xc09800c0,0x26c012c0,0x19338,0x6000000,
  0xc1a00130,0x4101330,0x19360,0x0,0xc1a80170,0x4501370,0x19368,0x400000,0xc1b001a0,0x48013a0,0x19370,0x400000,0xc1b801e0,0x4c013e0,0x19378,0x40000000,
  0xd9a01010,0x28103618,0x193a0,0x0,0xd9a81050,0x28503658,0x193a8,0x60,0xd9b01080,0x28803688,0x193b0,0x0,0xd9b810c0,0x28c036c8,0x193b8,0x6000,
  0xd9e01130,0x28103738,0x193e0,0x0,0xd9e81170,0x28503778,0x193e8,0x60,0xd9f011a0,0x288037a8,0x193f0,0x0,0xd9f811e0,0x28c037e8,0x193f8,0x6000,
  0xc6000020,0x26201220,0x19800,0x0,0xc6080060,0x26601260,0x19808,0x0,0xc61000b0,0x26b012b0,0x19810,0x0,0xc61800f0,0x26f012f0,0x19818,0x400,
  0xde000100,0x4201300,0x19840,0x0,0xde080140,0x4601340,0x19848,0x600000,0xde100190,0x4b01390,0x19850,0x0,0xde1801d0,0x4f013d0,0x19858,0x60000000,
  0xd9201020,0x28203628,0x19880,0x40,0xd9281060,0x28603668,0x19888,0x40,0xd93010b0,0x28b036b8,0x19890,0x4000,0xd93810f0,0x28f036f8,0x19898,0x4004000,
  0xd9601100,0x28203708,0x198c0,0x40,0xd9681140,0x28603748,0x198c8,0x40,0xd9701190,0x28b03798,0x198d0,0x4000,0xd97811d0,0x28f037d8,0x198d8,0x4004000,
  0xd8800030,0x26301230,0x19920,0x0,0xd8880070,0x26701270,0x19928,0x0,0xd89000a0,0x26a012a0,0x19930,0x0,0xd89800e0,0x26e012e0,0x19938,0x0,
  0xeba00110,0x4301310,0x19960,0x4000040,0xeba80150,0x4701350,0x19968,0x40,0xebb00180,0x4a01380,0x19970,0x4000,0xebb801c0,0x4e013c0,0x19978,0x4000,
  0xd8c01030,0x28303638,0x199a0,0x6,0xd8c81070,0x28703678,0x199a8,0x0,0xd8d010a0,0x28a036a8,0x199b0,0x600,0xd8d810e0,0x28e036e8,0x199b8,0x0,
  0xc6001110,0x28303718,0x199e0,0x6,0xc6081150,0x28703758,0x199e8,0x0,0xc6101180,0x28a03788,0x199f0,0x600,0xc61811c0,0x28e037c8,0x199f8,0x0,
  0x0,0x0,0x0,0x0,0x0,0x0,0x0,0x0,0x0,0x0,0x0,0x0,0x0,0x0,0x0,0x0,
  0x0,0x0,0x0,0x0,0x0,0x0,0x0,0x0,0x0,0x0,0x0,0x0,0x0,0x0,0x0,0x0,
  0x0,0x0,0x0,0x0,0x0,0x0,0x0,0x0,0x0,0x0,0x0,0x0,0x0,0x0,0x0,0x0,
  0x0,0x0,0x0,0x0,0x0,0x0,0x0,0x0,0x0,0x0,0x0,0x0,0x0,0x0,0x0,0x0,
  0x0,0x0,0x0,0x0,0x0,0x0,0x0,0x0,0x0,0x0,0x0,0x0,0x0,0x0,0x0,0x0,
  0x0,0x0,0x0,0x0,0x0,0x0,0x0,0x0,0x0,0x0,0x0,0x0,0x0,0x0,0x0,0x0,
  0x0,0x0,0x0,0x0,0x0,0x0,0x0,0x0,0x0,0x0,0x0,0x0,0x0,0x0,0x0,0x0,
  0x0,0x0,0x0,0x0,0x0,0x0,0x0,0x0,0x0,0x0,0x0,0x0,0x0,0x0,0x0,0x0,
  0x0,0x0,0x0,0x0,0x0,0x0,0x0,0x0,0x0,0x0,0x0,0x0,0x0,0x0,0x0,0x0,
  0x0,0x0,0x0,0x0,0x0,0x0,0x0,0x0,0x0,0x0,0x0,0x0,0x0,0x0,0x0,0x0,
  0x0,0x0,0x0,0x0,0x0,0x0,0x0,0x0,0x0,0x0,0x0,0x0,0x0,0x0,0x0,0x0,
  0x0,0x0,0x0,0x0,0x0,0x0,0x0,0x0,0x0,0x0,0x0,0x0,0x0,0x0,0x0,0x0,
  0x0,0x0,0x0,0x0,0x0,0x0,0x0,0x0,0x0,0x0,0x0,0x0,0x0,0x0,0x0,0x0,
  0x0,0x0,0x0,0x0,0x0,0x0,0x0,0x0,0x0,0x0,0x0,0x0,0x0,0x0,0x0,0x0,
  0x0,0x0,0x0,0x0,0x0,0x0,0x0,0x0,0x0,0x0,0x0,0x0,0x0,0x0,0x0,0x0,
  0x0,0x0,0x0,0x0,0x0,0x0,0x0,0x0,0x0,0x0,0x0,0x0,0x0,0x0,0x0,0x0,
  0x3f903f90,0x0,0x0,0x0,0x3f900000,0x0,0x3f90,0x0,0x0,0x0,0x3f903f90,0x0,0x0,0x0,0x3f900000,0x0,
  0x0,0xc042,0x0,0x0,0x0,0x0,0x0,0x0,0x0,0x0,0x0,0xc042,0x0,0x0,0x0,0x0,
  0x0,0x0,0x0,0x0,0x0,0x0,0x0,0x0,0x0,0x0,0x0,0x0,0x0,0x0,0x0,0x3f900000,
  0x0,0x0,0x0,0x0,0x0,0x0,0x0,0x0,0x0,0x0,0x0,0x0,0x0,0x0,0x0,0x3f900000,
  0x0,0x0,0x0,0x0,0x0,0x0,0x0,0x0,0x0,0x0,0x0,0x0,0x0,0x0,0x0,0x0,
  0x0,0x3f90,0x0,0x3f900000,0x0,0x3f90,0x0,0x0,0x0,0x0,0x0,0x3f90,0x0,0x0,0x0,0x3f90,
  0xc042,0x0,0x0,0x0,0xc0420000,0x0,0x0,0x0,0x0,0x0,0xc042,0x0,0x0,0x0,0xc0420000,0x0,
  0xc042,0x0,0x0,0x0,0xc0420000,0x0,0x0,0x0,0x0,0x0,0xc042,0x0,0x0,0x0,0xc0420000,0x0,
  0x0,0xc0420000,0x0,0x0,0x0,0x0,0x0,0x0,0x0,0x0,0x0,0xc0420000,0x0,0x0,0x0,0x0,
  0x0,0x0,0x0,0x0,0x0,0xc042,0x0,0x0,0x0,0x0,0x0,0x0,0x0,0x0,0x0,0xc042,
  0x3f903f90,0x0,0x0,0x0,0x3f900000,0x0,0x3f90,0x0,0x0,0x0,0x3f903f90,0x0,0x0,0x0,0x3f900000,0x0,
  0x3f903f90,0x0,0x0,0x0,0x3f900000,0x0,0x3f90,0x0,0x0,0x0,0x3f903f90,0x0,0x0,0x0,0x3f900000,0x0,
  0x0,0x0,0x0,0x0,0xc042,0x0,0x0,0x0,0x0,0x0,0x0,0x0,0x0,0x0,0x3f90c042,0x0,
  0x0,0x0,0x0,0x0,0x0,0x3f90,0x0,0x0,0x0,0x3f90,0x0,0x0,0x0,0x0,0x0,0x3f90,
  0x0,0x0,0x0,0x0,0x0,0x0,0x0,0x0,0x0,0x0,0x0,0x0,0x0,0x0,0x0,0x0,
  0x0,0x0,0x0,0x0,0x0,0x0,0x0,0x0,0x0,0x0,0x0,0x0,0x0,0x0,0x0,0x0,
  0xcc000000,0x27201208,0x19220,0x40004,0xcc080040,0x27601248,0x19228,0x40400,0xcc100090,0x27b01298,0x19230,0x4000400,0xcc1800d0,0x27f012d8,0x19238,0x4000000,
  0xcd200120,0x5201328,0x19260,0x60,0xcd280160,0x5601368,0x19268,0x0,0xcd3001b0,0x5b013b8,0x19270,0x6000,0xcd3801f0,0x5f013f8,0x19278,0x0,
  0xe4401000,0x29203600,0x192a0,0x0,0xe4481040,0x29603640,0x192a8,0x0,0xe4501090,0x29b03690,0x192b0,0x0,0xe45810d0,0x29f036d0,0x192b8,0x40000000,
  0xeb601120,0x29203720,0x192e0,0x0,0xeb681160,0x29603760,0x192e8,0x0,0xeb7011b0,0x29b037b0,0x192f0,0x0,0xeb7811f0,0x29f037f0,0x192f8,0x40000000,
  0xe4800010,0x27301218,0x19300,0x0,0xe4880050,0x27701258,0x19308,0x0,0xe4900080,0x27a01288,0x19310,0x0,0xe49800c0,0x27e012c8,0x19318,0x0,
  0xe5a00130,0x5301338,0x19340,0x40000040,0xe5a80170,0x5701378,0x19348,0x40,0xe5b001a0,0x5a013a8,0x19350,0x4000,0xe5b801e0,0x5e013e8,0x19358,0x4000,
  0xe4c01010,0x29303610,0x19380,0x6,0xe4c81050,0x29703650,0x19388,0x60000,0xe4d01080,0x29a03680,0x19390,0x600,0xe4d810c0,0x29e036c0,0x19398,0x6000000,
  0xd9e01130,0x29303730,0x193c0,0x6,0xd9e81170,0x29703770,0x193c8,0x60000,0xd9f011a0,0x29a037a0,0x193d0,0x600,0xd9f811e0,0x29e037e0,0x193d8,0x6000000,
  0xe4000020,0x27001228,0x19820,0x600000,0xe4080060,0x27401268,0x19828,0x0,0xe41000b0,0x279012b8,0x19830,0x60000000,0xe41800f0,0x27d012f8,0x19838,0x0,
  0xe5200100,0x5001308,0x19860,0x0,0xe5280140,0x5401348,0x19868,0x60,0xe5300190,0x5901398,0x19870,0x0,0xe53801d0,0x5d013d8,0x19878,0x6000,
  0xe5601020,0x29003620,0x198a0,0x40004,0xe5681060,0x29403660,0x198a8,0x40400,0xe57010b0,0x299036b0,0x198b0,0x4000400,0xe57810f0,0x29d036f0,0x198b8,0x4000000,
  0xcc801100,0x29003700,0x198e0,0x40004,0xcc881140,0x29403740,0x198e8,0x40400,0xcc901190,0x29903790,0x198f0,0x4000400,0xcc9811d0,0x29d037d0,0x198f8,0x4000000,
  0xcda00030,0x27101238,0x19900,0x0,0xcda80070,0x27501278,0x19908,0x6,0xcdb000a0,0x278012a8,0x19910,0x0,0xcdb800e0,0x27c012e8,0x19918,0x4000600,
  0xeac00110,0x5101318,0x19940,0x0,0xeac80150,0x5501358,0x19948,0x40,0xead00180,0x5801388,0x19950,0x40,0xead801c0,0x5c013c8,0x19958,0x4000,
  0xe5e01030,0x29103630,0x19980,0x0,0xe5e81070,0x29503670,0x19988,0x0,0xe5f010a0,0x298036a0,0x19990,0x0,0xe5f810e0,0x29c036e0,0x19998,0x0,
  0xe4001110,0x29103710,0x199c0,0x0,0xe4081150,0x29503750,0x199c8,0x0,0xe4101180,0x29803780,0x199d0,0x0,0xe41811c0,0x29c037c0,0x199d8,0x0,
  0x0,0x0,0x800,0x10000000,0x10,0x90,0x890,0x10000000,0x20,0x120,0x920,0x10000000,0x30,0x1b0,0x9b0,0x10000000,
  0x40,0x240,0xa40,0x20003180,0x50,0x2d0,0xad0,0x20003190,0x60,0x360,0xb60,0x200031a0,0x70,0x3f0,0xbf0,0x200031b0,
  0x610,0xc00,0x1400,0x40000000,0x600,0xc90,0x1490,0x40000010,0x630,0xd20,0x1520,0x40000020,0x620,0xdb0,0x15b0,0x40000030,
  0x650,0xe40,0x1640,0x20033180,0x640,0xed0,0x16d0,0x20033190,0x670,0xf60,0x1760,0x200331a0,0x660,0xff0,0x17f0,0x200331b0,
  0xc20,0x10,0x808,0x40030020,0xc30,0x80,0x898,0x40030030,0xc00,0x130,0x928,0x40030000,0xc10,0x1a0,0x9b8,0x40030010,
  0xc60,0x250,0xa48,0x200631a0,0xc70,0x2c0,0xad8,0x200631b0,0xc40,0x370,0xb68,0x20063180,0xc50,0x3e0,0xbf8,0x20063190,
  0x1230,0xc10,0x1408,0x40060020,0x1220,0xc80,0x1498,0x40060030,0x1210,0xd30,0x1528,0x40060000,0x1200,0xda0,0x15b8,0x40060010,
  0x1270,0xe50,0x1648,0x10000000,0x1260,0xec0,0x16d8,0x10000000,0x1250,0xf70,0x1768,0x10000000,0x1240,0xfe0,0x17f8,0x10000000,
  0x1840,0x20,0x810,0x10000000,0x1850,0xb0,0x880,0x10000000,0x1860,0x100,0x930,0x10000000,0x1870,0x190,0x9a0,0x10000000,
  0x1800,0x260,0xa50,0x200031c0,0x1810,0x2f0,0xac0,0x200031d0,0x1820,0x340,0xb70,0x200031e0,0x1830,0x3d0,0xbe0,0x200031f0,
  0x1e50,0xc20,0x1410,0x40000040,0x1e40,0xcb0,0x1480,0x40000050,0x1e70,0xd00,0x1530,0x40000060,0x1e60,0xd90,0x15a0,0x40000070,
  0x1e10,0xe60,0x1650,0x200331c0,0x1e00,0xef0,0x16c0,0x200331d0,0x1e30,0xf40,0x1770,0x200331e0,0x1e20,0xfd0,0x17e0,0x200331f0,
  0x2460,0x30,0x818,0x40030060,0x2470,0xa0,0x888,0x40030070,0x2440,0x110,0x938,0x40030040,0x2450,0x180,0x9a8,0x40030050,
  0x2420,0x270,0xa58,0x200631e0,0x2430,0x2e0,0xac8,0x200631f0,0x2400,0x350,0xb78,0x200631c0,0x2410,0x3c0,0xbe8,0x200631d0,
  0x2a70,0xc30,0x1418,0x40060060,0x2a60,0xca0,0x1488,0x40060070,0x2a50,0xd10,0x1538,0x40060040,0x2a40,0xd80,0x15a8,0x40060050,
  0x2a30,0xe70,0x1658,0x10000000,0x2a20,0xee0,0x16c8,0x10000000,0x2a10,0xf50,0x1778,0x10000000,0x2a00,0xfc0,0x17e8,0x10000000,
  0xbfd8bf90,0x3f403f00,0x0,0x0,0x0,0x0,0x0,0x0,0x0,0x0,0x0,0x0,0x0,0x0,0x0,0x0,
  0x0,0x0,0xbfd8bf90,0x3f403f00,0x0,0x0,0x0,0x0,0x0,0x0,0x0,0x0,0x0,0x0,0x0,0x0,
  0xbfd83f90,0x3f40bf00,0x0,0x0,0x0,0x0,0x0,0x0,0x0,0x0,0x0,0x0,0x0,0x0,0x0,0x0,
  0x0,0x0,0xbfd83f90,0x3f40bf00,0x0,0x0,0x0,0x0,0x0,0x0,0x0,0x0,0x0,0x0,0x0,0x0,
  0x0,0x0,0x0,0x0,0xbfd8bf90,0x3f403f00,0x0,0x0,0x0,0x0,0x0,0x0,0x0,0x0,0x0,0x0,
  0x0,0x0,0x0,0x0,0x0,0x0,0xbfd8bf90,0x3f403f00,0x0,0x0,0x0,0x0,0x0,0x0,0x0,0x0,
  0x0,0x0,0x0,0x0,0xbfd83f90,0x3f40bf00,0x0,0x0,0x0,0x0,0x0,0x0,0x0,0x0,0x0,0x0,
  0x0,0x0,0x0,0x0,0x0,0x0,0xbfd83f90,0x3f40bf00,0x0,0x0,0x0,0x0,0x0,0x0,0x0,0x0,
  0x0,0x0,0x0,0x0,0x0,0x0,0x0,0x0,0xbfd8bf90,0x3f403f00,0x0,0x0,0x0,0x0,0x0,0x0,
  0x0,0x0,0x0,0x0,0x0,0x0,0x0,0x0,0x0,0x0,0xbfd8bf90,0x3f403f00,0x0,0x0,0x0,0x0,
  0x0,0x0,0x0,0x0,0x0,0x0,0x0,0x0,0xbfd83f90,0x3f40bf00,0x0,0x0,0x0,0x0,0x0,0x0,
  0x0,0x0,0x0,0x0,0x0,0x0,0x0,0x0,0x0,0x0,0xbfd83f90,0x3f40bf00,0x0,0x0,0x0,0x0,
  0x0,0x0,0x0,0x0,0x0,0x0,0x0,0x0,0x0,0x0,0x0,0x0,0xbfd8bf90,0x3f403f00,0x0,0x0,
  0x0,0x0,0x0,0x0,0x0,0x0,0x0,0x0,0x0,0x0,0x0,0x0,0x0,0x0,0xbfd8bf90,0x3f403f00,
  0x0,0x0,0x0,0x0,0x0,0x0,0x0,0x0,0x0,0x0,0x0,0x0,0xbfd83f90,0x3f40bf00,0x0,0x0,
  0x0,0x0,0x0,0x0,0x0,0x0,0x0,0x0,0x0,0x0,0x0,0x0,0x0,0x0,0xbfd83f90,0x3f40bf00,
  0xa800,0xa820,0x0,0xa800,0xa808,0xa828,0x40,0xa840,0xa810,0xa830,0x90,0xa890,0xa818,0xa838,0xd0,0xa8d0,
  0xa920,0xa900,0x2400,0xae00,0xa928,0xa908,0x2440,0xae40,0xa930,0xa910,0x2490,0xae90,0xa938,0xa918,0x24d0,0xaed0,
  0xa840,0xa860,0x200,0xb400,0xa848,0xa868,0x240,0xb440,0xa850,0xa870,0x290,0xb490,0xa858,0xa878,0x2d0,0xb4d0,
  0xa960,0xa940,0x200,0xb400,0xa968,0xa948,0x240,0xb440,0xa970,0xa950,0x290,0xb490,0xa978,0xa958,0x2d0,0xb4d0,
  0xea80,0xeaa0,0x10,0xa810,0xea88,0xeaa8,0x50,0xa850,0xea90,0xeab0,0x80,0xa880,0xea98,0xeab8,0xc0,0xa8c0,
  0xa9a0,0xa980,0x2410,0xae10,0xa9a8,0xa988,0x2450,0xae50,0xa9b0,0xa990,0x2480,0xae80,0xa9b8,0xa998,0x24c0,0xaec0,
  0xc0c0,0xc0e0,0x210,0xb410,0xc0c8,0xc0e8,0x250,0xb450,0xc0d0,0xc0f0,0x280,0xb480,0xc0d8,0xc0f8,0x2c0,0xb4c0,
  0xc1e0,0xc1c0,0x210,0xb410,0xc1e8,0xc1c8,0x250,0xb450,0xc1f0,0xc1d0,0x280,0xb480,0xc1f8,0xc1d8,0x2c0,0xb4c0,
  0xae00,0xae20,0x20,0xa820,0xae08,0xae28,0x60,0xa860,0xae10,0xae30,0xb0,0xa8b0,0xae18,0xae38,0xf0,0xa8f0,
  0xeb20,0xeb00,0x2420,0xae20,0xeb28,0xeb08,0x2460,0xae60,0xeb30,0xeb10,0x24b0,0xaeb0,0xeb38,0xeb18,0x24f0,0xaef0,
  0xc040,0xc060,0x220,0xb420,0xc048,0xc068,0x260,0xb460,0xc050,0xc070,0x2b0,0xb4b0,0xc058,0xc078,0x2f0,0xb4f0,
  0xc160,0xc140,0x220,0xb420,0xc168,0xc148,0x260,0xb460,0xc170,0xc150,0x2b0,0xb4b0,0xc178,0xc158,0x2f0,0xb4f0,
  0xa880,0xa8a0,0x30,0xa830,0xa888,0xa8a8,0x70,0xa870,0xa890,0xa8b0,0xa0,0xa8a0,0xa898,0xa8b8,0xe0,0xa8e0,
  0xa8c0,0xa8e0,0x2430,0xae30,0xa8c8,0xa8e8,0x2470,0xae70,0xa8d0,0xa8f0,0x24a0,0xaea0,0xa8d8,0xa8f8,0x24e0,0xaee0,
  0xa9e0,0xa9c0,0x230,0xb430,0xa9e8,0xa9c8,0x270,0xb470,0xa9f0,0xa9d0,0x2a0,0xb4a0,0xa9f8,0xa9d8,0x2e0,0xb4e0,
  0xae00,0xae20,0x230,0xb430,0xae08,0xae28,0x270,0xb470,0xae10,0xae30,0x2a0,0xb4a0,0xae18,0xae38,0x2e0,0xb4e0,
  0x180000,0x40,0x820,0x10000000,0x180010,0xd0,0x8b0,0x10000000,0x180020,0x160,0x900,0x10000000,0x180030,0x1f0,0x990,0x10000000,
  0x180040,0x200,0xa60,0x20093180,0x180050,0x290,0xaf0,0x20093190,0x180060,0x320,0xb40,0x200931a0,0x180070,0x3b0,0xbd0,0x200931b0,
  0x180610,0xc40,0x1420,0x40090000,0x180600,0xcd0,0x14b0,0x40090010,0x180630,0xd60,0x1500,0x40090020,0x180620,0xdf0,0x1590,0x40090030,
  0x180650,0xe00,0x1660,0x200c3180,0x180640,0xe90,0x16f0,0x200c3190,0x180670,0xf20,0x1740,0x200c31a0,0x180660,0xfb0,0x17d0,0x200c31b0,
  0x180c20,0x50,0x828,0x400c0020,0x180c30,0xc0,0x8b8,0x400c0030,0x180c00,0x170,0x908,0x400c0000,0x180c10,0x1e0,0x998,0x400c0010,
  0x180c60,0x210,0xa68,0x200f31a0,0x180c70,0x280,0xaf8,0x200f31b0,0x180c40,0x330,0xb48,0x200f3180,0x180c50,0x3a0,0xbd8,0x200f3190,
  0x181230,0xc50,0x1428,0x400f0020,0x181220,0xcc0,0x14b8,0x400f0030,0x181210,0xd70,0x1508,0x400f0000,0x181200,0xde0,0x1598,0x400f0010,
  0x181270,0xe10,0x1668,0x10000000,0x181260,0xe80,0x16f8,0x10000000,0x181250,0xf30,0x1748,0x10000000,0x181240,0xfa0,0x17d8,0x10000000,
  0x181840,0x60,0x830,0x10000000,0x181850,0xf0,0x8a0,0x10000000,0x181860,0x140,0x910,0x10000000,0x181870,0x1d0,0x980,0x10000000,
  0x181800,0x220,0xa70,0x200931c0,0x181810,0x2b0,0xae0,0x200931d0,0x181820,0x300,0xb50,0x200931e0,0x181830,0x390,0xbc0,0x200931f0,
  0x181e50,0xc60,0x1430,0x40090040,0x181e40,0xcf0,0x14a0,0x40090050,0x181e70,0xd40,0x1510,0x40090060,0x181e60,0xdd0,0x1580,0x40090070,
  0x181e10,0xe20,0x1670,0x200c31c0,0x181e00,0xeb0,0x16e0,0x200c31d0,0x181e30,0xf00,0x1750,0x200c31e0,0x181e20,0xf90,0x17c0,0x200c31f0,
  0x182460,0x70,0x838,0x400c0060,0x182470,0xe0,0x8a8,0x400c0070,0x182440,0x150,0x918,0x400c0040,0x182450,0x1c0,0x988,0x400c0050,
  0x182420,0x230,0xa78,0x200f31e0,0x182430,0x2a0,0xae8,0x200f31f0,0x182400,0x310,0xb58,0x200f31c0,0x182410,0x380,0xbc8,0x200f31d0,
  0x182a70,0xc70,0x1438,0x400f0060,0x182a60,0xce0,0x14a8,0x400f0070,0x182a50,0xd50,0x1518,0x400f0040,0x182a40,0xdc0,0x1588,0x400f0050,
  0x182a30,0xe30,0x1678,0x10000000,0x182a20,0xea0,0x16e8,0x10000000,0x182a10,0xf10,0x1758,0x10000000,0x182a00,0xf80,0x17c8,0x10000000,
  0xbfd8bf90,0x3f403f00,0x0,0x0,0x0,0x0,0x0,0x0,0x0,0x0,0x0,0x0,0x0,0x0,0x0,0x0,
  0x0,0x0,0xbfd8bf90,0x3f403f00,0x0,0x0,0x0,0x0,0x0,0x0,0x0,0x0,0x0,0x0,0x0,0x0,
  0xbfd83f90,0x3f40bf00,0x0,0x0,0x0,0x0,0x0,0x0,0x0,0x0,0x0,0x0,0x0,0x0,0x0,0x0,
  0x0,0x0,0xbfd83f90,0x3f40bf00,0x0,0x0,0x0,0x0,0x0,0x0,0x0,0x0,0x0,0x0,0x0,0x0,
  0x0,0x0,0x0,0x0,0xbfd8bf90,0x3f403f00,0x0,0x0,0x0,0x0,0x0,0x0,0x0,0x0,0x0,0x0,
  0x0,0x0,0x0,0x0,0x0,0x0,0xbfd8bf90,0x3f403f00,0x0,0x0,0x0,0x0,0x0,0x0,0x0,0x0,
  0x0,0x0,0x0,0x0,0xbfd83f90,0x3f40bf00,0x0,0x0,0x0,0x0,0x0,0x0,0x0,0x0,0x0,0x0,
  0x0,0x0,0x0,0x0,0x0,0x0,0xbfd83f90,0x3f40bf00,0x0,0x0,0x0,0x0,0x0,0x0,0x0,0x0,
  0x0,0x0,0x0,0x0,0x0,0x0,0x0,0x0,0xbfd8bf90,0x3f403f00,0x0,0x0,0x0,0x0,0x0,0x0,
  0x0,0x0,0x0,0x0,0x0,0x0,0x0,0x0,0x0,0x0,0xbfd8bf90,0x3f403f00,0x0,0x0,0x0,0x0,
  0x0,0x0,0x0,0x0,0x0,0x0,0x0,0x0,0xbfd83f90,0x3f40bf00,0x0,0x0,0x0,0x0,0x0,0x0,
  0x0,0x0,0x0,0x0,0x0,0x0,0x0,0x0,0x0,0x0,0xbfd83f90,0x3f40bf00,0x0,0x0,0x0,0x0,
  0x0,0x0,0x0,0x0,0x0,0x0,0x0,0x0,0x0,0x0,0x0,0x0,0xbfd8bf90,0x3f403f00,0x0,0x0,
  0x0,0x0,0x0,0x0,0x0,0x0,0x0,0x0,0x0,0x0,0x0,0x0,0x0,0x0,0xbfd8bf90,0x3f403f00,
  0x0,0x0,0x0,0x0,0x0,0x0,0x0,0x0,0x0,0x0,0x0,0x0,0xbfd83f90,0x3f40bf00,0x0,0x0,
  0x0,0x0,0x0,0x0,0x0,0x0,0x0,0x0,0x0,0x0,0x0,0x0,0x0,0x0,0xbfd83f90,0x3f40bf00,
  0xb400,0xb420,0x120,0xa920,0xb408,0xb428,0x160,0xa960,0xb410,0xb430,0x1b0,0xa9b0,0xb418,0xb438,0x1f0,0xa9f0,
  0xb440,0xb460,0x2520,0xaf20,0xb448,0xb468,0x2560,0xaf60,0xb450,0xb470,0x25b0,0xafb0,0xb458,0xb478,0x25f0,0xaff0,
  0xcc40,0xcc60,0x320,0xb520,0xcc48,0xcc68,0x360,0xb560,0xcc50,0xcc70,0x3b0,0xb5b0,0xcc58,0xcc78,0x3f0,0xb5f0,
  0xcd60,0xcd40,0x320,0xb520,0xcd68,0xcd48,0x360,0xb560,0xcd70,0xcd50,0x3b0,0xb5b0,0xcd78,0xcd58,0x3f0,0xb5f0,
  0xb5a0,0xb580,0x130,0xa930,0xb5a8,0xb588,0x170,0xa970,0xb5b0,0xb590,0x1a0,0xa9a0,0xb5b8,0xb598,0x1e0,0xa9e0,
  0xccc0,0xcce0,0x2530,0xaf30,0xccc8,0xcce8,0x2570,0xaf70,0xccd0,0xccf0,0x25a0,0xafa0,0xccd8,0xccf8,0x25e0,0xafe0,
  0xa9e0,0xa9c0,0x330,0xb530,0xa9e8,0xa9c8,0x370,0xb570,0xa9f0,0xa9d0,0x3a0,0xb5a0,0xa9f8,0xa9d8,0x3e0,0xb5e0,
  0xc1e0,0xc1c0,0x330,0xb530,0xc1e8,0xc1c8,0x370,0xb570,0xc1f0,0xc1d0,0x3a0,0xb5a0,0xc1f8,0xc1d8,0x3e0,0xb5e0,
  0xb520,0xb500,0x100,0xa900,0xb528,0xb508,0x140,0xa940,0xb530,0xb510,0x190,0xa990,0xb538,0xb518,0x1d0,0xa9d0,
  0xea40,0xea60,0x2500,0xaf00,0xea48,0xea68,0x2540,0xaf40,0xea50,0xea70,0x2590,0xaf90,0xea58,0xea78,0x25d0,0xafd0,
  0xb560,0xb540,0x300,0xb500,0xb568,0xb548,0x340,0xb540,0xb570,0xb550,0x390,0xb590,0xb578,0xb558,0x3d0,0xb5d0,
  0xb480,0xb4a0,0x300,0xb500,0xb488,0xb4a8,0x340,0xb540,0xb490,0xb4b0,0x390,0xb590,0xb498,0xb4b8,0x3d0,0xb5d0,
  0xb4c0,0xb4e0,0x110,0xa910,0xb4c8,0xb4e8,0x150,0xa950,0xb4d0,0xb4f0,0x180,0xa980,0xb4d8,0xb4f8,0x1c0,0xa9c0,
  0xb5e0,0xb5c0,0x2510,0xaf10,0xb5e8,0xb5c8,0x2550,0xaf50,0xb5f0,0xb5d0,0x2580,0xaf80,0xb5f8,0xb5d8,0x25c0,0xafc0,
  0xcde0,0xcdc0,0x310,0xb510,0xcde8,0xcdc8,0x350,0xb550,0xcdf0,0xcdd0,0x380,0xb580,0xcdf8,0xcdd8,0x3c0,0xb5c0,
  0xb520,0xb500,0x310,0xb510,0xb528,0xb508,0x350,0xb550,0xb530,0xb510,0x380,0xb580,0xb538,0xb518,0x3c0,0xb5c0,
  0x0,0x400,0x840,0x10000000,0x10,0x490,0x8d0,0x10000000,0x20,0x520,0x960,0x10000000,0x30,0x5b0,0x9f0,0x10000000,
  0x40,0x640,0xa00,0x20123180,0x50,0x6d0,0xa90,0x20123190,0x60,0x760,0xb20,0x201231a0,0x70,0x7f0,0xbb0,0x201231b0,
  0x610,0x1000,0x1440,0x40120000,0x600,0x1090,0x14d0,0x40120010,0x630,0x1120,0x1560,0x40120020,0x620,0x11b0,0x15f0,0x40120030,
  0x650,0x1240,0x1600,0x20153180,0x640,0x12d0,0x1690,0x20153190,0x670,0x1360,0x1720,0x201531a0,0x660,0x13f0,0x17b0,0x201531b0,
  0xc20,0x410,0x848,0x40150020,0xc30,0x480,0x8d8,0x40150030,0xc00,0x530,0x968,0x40150000,0xc10,0x5a0,0x9f8,0x40150010,
  0xc60,0x650,0xa08,0x201831a0,0xc70,0x6c0,0xa98,0x201831b0,0xc40,0x770,0xb28,0x20183180,0xc50,0x7e0,0xbb8,0x20183190,
  0x1230,0x1010,0x1448,0x40180020,0x1220,0x1080,0x14d8,0x40180030,0x1210,0x1130,0x1568,0x40180000,0x1200,0x11a0,0x15f8,0x40180010,
  0x1270,0x1250,0x1608,0x10000000,0x1260,0x12c0,0x1698,0x10000000,0x1250,0x1370,0x1728,0x10000000,0x1240,0x13e0,0x17b8,0x10000000,
  0x1840,0x420,0x850,0x10000000,0x1850,0x4b0,0x8c0,0x10000000,0x1860,0x500,0x970,0x10000000,0x1870,0x590,0x9e0,0x10000000,
  0x1800,0x660,0xa10,0x201231c0,0x1810,0x6f0,0xa80,0x201231d0,0x1820,0x740,0xb30,0x201231e0,0x1830,0x7d0,0xba0,0x201231f0,
  0x1e50,0x1020,0x1450,0x40120040,0x1e40,0x10b0,0x14c0,0x40120050,0x1e70,0x1100,0x1570,0x40120060,0x1e60,0x1190,0x15e0,0x40120070,
  0x1e10,0x1260,0x1610,0x201531c0,0x1e00,0x12f0,0x1680,0x201531d0,0x1e30,0x1340,0x1730,0x201531e0,0x1e20,0x13d0,0x17a0,0x201531f0,
  0x2460,0x430,0x858,0x40150060,0x2470,0x4a0,0x8c8,0x40150070,0x2440,0x510,0x978,0x40150040,0x2450,0x580,0x9e8,0x40150050,
  0x2420,0x670,0xa18,0x201831e0,0x2430,0x6e0,0xa88,0x201831f0,0x2400,0x750,0xb38,0x201831c0,0x2410,0x7c0,0xba8,0x201831d0,
  0x2a70,0x1030,0x1458,0x40180060,0x2a60,0x10a0,0x14c8,0x40180070,0x2a50,0x1110,0x1578,0x40180040,0x2a40,0x1180,0x15e8,0x40180050,
  0x2a30,0x1270,0x1618,0x10000000,0x2a20,0x12e0,0x1688,0x10000000,0x2a10,0x1350,0x1738,0x10000000,0x2a00,0x13c0,0x17a8,0x10000000,
  0xbfd8bf90,0x3f403f00,0x0,0x0,0x0,0x0,0x0,0x0,0x0,0x0,0x0,0x0,0x0,0x0,0x0,0x0,
  0x0,0x0,0xbfd8bf90,0x3f403f00,0x0,0x0,0x0,0x0,0x0,0x0,0x0,0x0,0x0,0x0,0x0,0x0,
  0xbfd83f90,0x3f40bf00,0x0,0x0,0x0,0x0,0x0,0x0,0x0,0x0,0x0,0x0,0x0,0x0,0x0,0x0,
  0x0,0x0,0xbfd83f90,0x3f40bf00,0x0,0x0,0x0,0x0,0x0,0x0,0x0,0x0,0x0,0x0,0x0,0x0,
  0x0,0x0,0x0,0x0,0xbfd8bf90,0x3f403f00,0x0,0x0,0x0,0x0,0x0,0x0,0x0,0x0,0x0,0x0,
  0x0,0x0,0x0,0x0,0x0,0x0,0xbfd8bf90,0x3f403f00,0x0,0x0,0x0,0x0,0x0,0x0,0x0,0x0,
  0x0,0x0,0x0,0x0,0xbfd83f90,0x3f40bf00,0x0,0x0,0x0,0x0,0x0,0x0,0x0,0x0,0x0,0x0,
  0x0,0x0,0x0,0x0,0x0,0x0,0xbfd83f90,0x3f40bf00,0x0,0x0,0x0,0x0,0x0,0x0,0x0,0x0,
  0x0,0x0,0x0,0x0,0x0,0x0,0x0,0x0,0xbfd8bf90,0x3f403f00,0x0,0x0,0x0,0x0,0x0,0x0,
  0x0,0x0,0x0,0x0,0x0,0x0,0x0,0x0,0x0,0x0,0xbfd8bf90,0x3f403f00,0x0,0x0,0x0,0x0,
  0x0,0x0,0x0,0x0,0x0,0x0,0x0,0x0,0xbfd83f90,0x3f40bf00,0x0,0x0,0x0,0x0,0x0,0x0,
  0x0,0x0,0x0,0x0,0x0,0x0,0x0,0x0,0x0,0x0,0xbfd83f90,0x3f40bf00,0x0,0x0,0x0,0x0,
  0x0,0x0,0x0,0x0,0x0,0x0,0x0,0x0,0x0,0x0,0x0,0x0,0xbfd8bf90,0x3f403f00,0x0,0x0,
  0x0,0x0,0x0,0x0,0x0,0x0,0x0,0x0,0x0,0x0,0x0,0x0,0x0,0x0,0xbfd8bf90,0x3f403f00,
  0x0,0x0,0x0,0x0,0x0,0x0,0x0,0x0,0x0,0x0,0x0,0x0,0xbfd83f90,0x3f40bf00,0x0,0x0,
  0x0,0x0,0x0,0x0,0x0,0x0,0x0,0x0,0x0,0x0,0x0,0x0,0x0,0x0,0xbfd83f90,0x3f40bf00,
  0xc000,0xc020,0x2600,0xba00,0xc008,0xc028,0x2640,0xba40,0xc010,0xc030,0x2690,0xba90,0xc018,0xc038,0x26d0,0xbad0,
  0xd800,0xd820,0x400,0xc000,0xd808,0xd828,0x440,0xc040,0xd810,0xd830,0x490,0xc090,0xd818,0xd838,0x4d0,0xc0d0,
  0xc120,0xc100,0x2800,0xc600,0xc128,0xc108,0x2840,0xc640,0xc130,0xc110,0x2890,0xc690,0xc138,0xc118,0x28d0,0xc6d0,
  0xd840,0xd860,0x2800,0xc600,0xd848,0xd868,0x2840,0xc640,0xd850,0xd870,0x2890,0xc690,0xd858,0xd878,0x28d0,0xc6d0,
  0xc080,0xc0a0,0x2610,0xba10,0xc088,0xc0a8,0x2650,0xba50,0xc090,0xc0b0,0x2680,0xba80,0xc098,0xc0b8,0x26c0,0xbac0,
  0xc1a0,0xc180,0x410,0xc010,0xc1a8,0xc188,0x450,0xc050,0xc1b0,0xc190,0x480,0xc080,0xc1b8,0xc198,0x4c0,0xc0c0,
  0xd9a0,0xd980,0x2810,0xc610,0xd9a8,0xd988,0x2850,0xc650,0xd9b0,0xd990,0x2880,0xc680,0xd9b8,0xd998,0x28c0,0xc6c0,
  0xd9e0,0xd9c0,0x2810,0xc610,0xd9e8,0xd9c8,0x2850,0xc650,0xd9f0,0xd9d0,0x2880,0xc680,0xd9f8,0xd9d8,0x28c0,0xc6c0,
  0xc600,0xc620,0x2620,0xba20,0xc608,0xc628,0x2660,0xba60,0xc610,0xc630,0x26b0,0xbab0,0xc618,0xc638,0x26f0,0xbaf0,
  0xde00,0xde20,0x420,0xc020,0xde08,0xde28,0x460,0xc060,0xde10,0xde30,0x4b0,0xc0b0,0xde18,0xde38,0x4f0,0xc0f0,
  0xd920,0xd900,0x2820,0xc620,0xd928,0xd908,0x2860,0xc660,0xd930,0xd910,0x28b0,0xc6b0,0xd938,0xd918,0x28f0,0xc6f0,
  0xd960,0xd940,0x2820,0xc620,0xd968,0xd948,0x2860,0xc660,0xd970,0xd950,0x28b0,0xc6b0,0xd978,0xd958,0x28f0,0xc6f0,
  0xd880,0xd8a0,0x2630,0xba30,0xd888,0xd8a8,0x2670,0xba70,0xd890,0xd8b0,0x26a0,0xbaa0,0xd898,0xd8b8,0x26e0,0xbae0,
  0xeba0,0xeb80,0x430,0xc030,0xeba8,0xeb88,0x470,0xc070,0xebb0,0xeb90,0x4a0,0xc0a0,0xebb8,0xeb98,0x4e0,0xc0e0,
  0xd8c0,0xd8e0,0x2830,0xc630,0xd8c8,0xd8e8,0x2870,0xc670,0xd8d0,0xd8f0,0x28a0,0xc6a0,0xd8d8,0xd8f8,0x28e0,0xc6e0,
  0xc600,0xc620,0x2830,0xc630,0xc608,0xc628,0x2870,0xc670,0xc610,0xc630,0x28a0,0xc6a0,0xc618,0xc638,0x28e0,0xc6e0,
  0x180000,0x440,0x860,0x10000000,0x180010,0x4d0,0x8f0,0x10000000,0x180020,0x560,0x940,0x10000000,0x180030,0x5f0,0x9d0,0x10000000,
  0x180040,0x600,0xa20,0x201b3180,0x180050,0x690,0xab0,0x201b3190,0x180060,0x720,0xb00,0x201b31a0,0x180070,0x7b0,0xb90,0x201b31b0,
  0x180610,0x1040,0x1460,0x401b0000,0x180600,0x10d0,0x14f0,0x401b0010,0x180630,0x1160,0x1540,0x401b0020,0x180620,0x11f0,0x15d0,0x401b0030,
  0x180650,0x1200,0x1620,0x201e3180,0x180640,0x1290,0x16b0,0x201e3190,0x180670,0x1320,0x1700,0x201e31a0,0x180660,0x13b0,0x1790,0x201e31b0,
  0x180c20,0x450,0x868,0x401e0020,0x180c30,0x4c0,0x8f8,0x401e0030,0x180c00,0x570,0x948,0x401e0000,0x180c10,0x5e0,0x9d8,0x401e0010,
  0x180c60,0x610,0xa28,0x202131a0,0x180c70,0x680,0xab8,0x202131b0,0x180c40,0x730,0xb08,0x20213180,0x180c50,0x7a0,0xb98,0x20213190,
  0x181230,0x1050,0x1468,0x40210020,0x181220,0x10c0,0x14f8,0x40210030,0x181210,0x1170,0x1548,0x40210000,0x181200,0x11e0,0x15d8,0x40210010,
  0x181270,0x1210,0x1628,0x10000000,0x181260,0x1280,0x16b8,0x10000000,0x181250,0x1330,0x1708,0x10000000,0x181240,0x13a0,0x1798,0x10000000,
  0x181840,0x460,0x870,0x10000000,0x181850,0x4f0,0x8e0,0x10000000,0x181860,0x540,0x950,0x10000000,0x181870,0x5d0,0x9c0,0x10000000,
  0x181800,0x620,0xa30,0x201b31c0,0x181810,0x6b0,0xaa0,0x201b31d0,0x181820,0x700,0xb10,0x201b31e0,0x181830,0x790,0xb80,0x201b31f0,
  0x181e50,0x1060,0x1470,0x401b0040,0x181e40,0x10f0,0x14e0,0x401b0050,0x181e70,0x1140,0x1550,0x401b0060,0x181e60,0x11d0,0x15c0,0x401b0070,
  0x181e10,0x1220,0x1630,0x201e31c0,0x181e00,0x12b0,0x16a0,0x201e31d0,0x181e30,0x1300,0x1710,0x201e31e0,0x181e20,0x1390,0x1780,0x201e31f0,
  0x182460,0x470,0x878,0x401e0060,0x182470,0x4e0,0x8e8,0x401e0070,0x182440,0x550,0x958,0x401e0040,0x182450,0x5c0,0x9c8,0x401e0050,
  0x182420,0x630,0xa38,0x202131e0,0x182430,0x6a0,0xaa8,0x202131f0,0x182400,0x710,0xb18,0x202131c0,0x182410,0x780,0xb88,0x202131d0,
  0x182a70,0x1070,0x1478,0x40210060,0x182a60,0x10e0,0x14e8,0x40210070,0x182a50,0x1150,0x1558,0x40210040,0x182a40,0x11c0,0x15c8,0x40210050,
  0x182a30,0x1230,0x1638,0x10000000,0x182a20,0x12a0,0x16a8,0x10000000,0x182a10,0x1310,0x1718,0x10000000,0x182a00,0x1380,0x1788,0x10000000,
  0xbfd8bf90,0x3f403f00,0x0,0x0,0x0,0x0,0x0,0x0,0x0,0x0,0x0,0x0,0x0,0x0,0x0,0x0,
  0x0,0x0,0xbfd8bf90,0x3f403f00,0x0,0x0,0x0,0x0,0x0,0x0,0x0,0x0,0x0,0x0,0x0,0x0,
  0xbfd83f90,0x3f40bf00,0x0,0x0,0x0,0x0,0x0,0x0,0x0,0x0,0x0,0x0,0x0,0x0,0x0,0x0,
  0x0,0x0,0xbfd83f90,0x3f40bf00,0x0,0x0,0x0,0x0,0x0,0x0,0x0,0x0,0x0,0x0,0x0,0x0,
  0x0,0x0,0x0,0x0,0xbfd8bf90,0x3f403f00,0x0,0x0,0x0,0x0,0x0,0x0,0x0,0x0,0x0,0x0,
  0x0,0x0,0x0,0x0,0x0,0x0,0xbfd8bf90,0x3f403f00,0x0,0x0,0x0,0x0,0x0,0x0,0x0,0x0,
  0x0,0x0,0x0,0x0,0xbfd83f90,0x3f40bf00,0x0,0x0,0x0,0x0,0x0,0x0,0x0,0x0,0x0,0x0,
  0x0,0x0,0x0,0x0,0x0,0x0,0xbfd83f90,0x3f40bf00,0x0,0x0,0x0,0x0,0x0,0x0,0x0,0x0,
  0x0,0x0,0x0,0x0,0x0,0x0,0x0,0x0,0xbfd8bf90,0x3f403f00,0x0,0x0,0x0,0x0,0x0,0x0,
  0x0,0x0,0x0,0x0,0x0,0x0,0x0,0x0,0x0,0x0,0xbfd8bf90,0x3f403f00,0x0,0x0,0x0,0x0,
  0x0,0x0,0x0,0x0,0x0,0x0,0x0,0x0,0xbfd83f90,0x3f40bf00,0x0,0x0,0x0,0x0,0x0,0x0,
  0x0,0x0,0x0,0x0,0x0,0x0,0x0,0x0,0x0,0x0,0xbfd83f90,0x3f40bf00,0x0,0x0,0x0,0x0,
  0x0,0x0,0x0,0x0,0x0,0x0,0x0,0x0,0x0,0x0,0x0,0x0,0xbfd8bf90,0x3f403f00,0x0,0x0,
  0x0,0x0,0x0,0x0,0x0,0x0,0x0,0x0,0x0,0x0,0x0,0x0,0x0,0x0,0xbfd8bf90,0x3f403f00,
  0x0,0x0,0x0,0x0,0x0,0x0,0x0,0x0,0x0,0x0,0x0,0x0,0xbfd83f90,0x3f40bf00,0x0,0x0,
  0x0,0x0,0x0,0x0,0x0,0x0,0x0,0x0,0x0,0x0,0x0,0x0,0x0,0x0,0xbfd83f90,0x3f40bf00,
  0xcc00,0xcc20,0x2720,0xbb20,0xcc08,0xcc28,0x2760,0xbb60,0xcc10,0xcc30,0x27b0,0xbbb0,0xcc18,0xcc38,0x27f0,0xbbf0,
  0xcd20,0xcd00,0x520,0xc120,0xcd28,0xcd08,0x560,0xc160,0xcd30,0xcd10,0x5b0,0xc1b0,0xcd38,0xcd18,0x5f0,0xc1f0,
  0xe440,0xe460,0x2920,0xc720,0xe448,0xe468,0x2960,0xc760,0xe450,0xe470,0x29b0,0xc7b0,0xe458,0xe478,0x29f0,0xc7f0,
  0xeb60,0xeb40,0x2920,0xc720,0xeb68,0xeb48,0x2960,0xc760,0xeb70,0xeb50,0x29b0,0xc7b0,0xeb78,0xeb58,0x29f0,0xc7f0,
  0xe480,0xe4a0,0x2730,0xbb30,0xe488,0xe4a8,0x2770,0xbb70,0xe490,0xe4b0,0x27a0,0xbba0,0xe498,0xe4b8,0x27e0,0xbbe0,
  0xe5a0,0xe580,0x530,0xc130,0xe5a8,0xe588,0x570,0xc170,0xe5b0,0xe590,0x5a0,0xc1a0,0xe5b8,0xe598,0x5e0,0xc1e0,
  0xe4c0,0xe4e0,0x2930,0xc730,0xe4c8,0xe4e8,0x2970,0xc770,0xe4d0,0xe4f0,0x29a0,0xc7a0,0xe4d8,0xe4f8,0x29e0,0xc7e0,
  0xd9e0,0xd9c0,0x2930,0xc730,0xd9e8,0xd9c8,0x2970,0xc770,0xd9f0,0xd9d0,0x29a0,0xc7a0,0xd9f8,0xd9d8,0x29e0,0xc7e0,
  0xe400,0xe420,0x2700,0xbb00,0xe408,0xe428,0x2740,0xbb40,0xe410,0xe430,0x2790,0xbb90,0xe418,0xe438,0x27d0,0xbbd0,
  0xe520,0xe500,0x500,0xc100,0xe528,0xe508,0x540,0xc140,0xe530,0xe510,0x590,0xc190,0xe538,0xe518,0x5d0,0xc1d0,
  0xe560,0xe540,0x2900,0xc700,0xe568,0xe548,0x2940,0xc740,0xe570,0xe550,0x2990,0xc790,0xe578,0xe558,0x29d0,0xc7d0,
  0xcc80,0xcca0,0x2900,0xc700,0xcc88,0xcca8,0x2940,0xc740,0xcc90,0xccb0,0x2990,0xc790,0xcc98,0xccb8,0x29d0,0xc7d0,
  0xcda0,0xcd80,0x2710,0xbb10,0xcda8,0xcd88,0x2750,0xbb50,0xcdb0,0xcd90,0x2780,0xbb80,0xcdb8,0xcd98,0x27c0,0xbbc0,
  0xeac0,0xeae0,0x510,0xc110,0xeac8,0xeae8,0x550,0xc150,0xead0,0xeaf0,0x580,0xc180,0xead8,0xeaf8,0x5c0,0xc1c0,
  0xe5e0,0xe5c0,0x2910,0xc710,0xe5e8,0xe5c8,0x2950,0xc750,0xe5f0,0xe5d0,0x2980,0xc780,0xe5f8,0xe5d8,0x29c0,0xc7c0,
  0xe400,0xe420,0x2910,0xc710,0xe408,0xe428,0x2950,0xc750,0xe410,0xe430,0x2980,0xc780,0xe418,0xe438,0x29c0,0xc7c0,
};

__global__ __launch_bounds__(256, 1) void wino_main_kernel(const char* __restrict__ x, char* __restrict__ Uq, char* __restrict__ out, const char* __restrict__ wt,
                                                           unsigned long long region_mask) {
  __shared__ __attribute__((aligned(1024))) char smem[163840];
  unsigned tid = threadIdx.x;
  asm volatile(
"s_load_dwordx2 s[12:13], %1, 0\n\t"
"s_load_dwordx2 s[52:53], %1, 24\n\t"
"s_load_dwordx2 s[8:9], %1, 8\n\t"
"s_load_dwordx2 s[16:17], %1, 16\n\t"
"s_load_dword s76, %1, 32\n\t"
"s_getpc_b64 s[40:41]\n\t"
"Lwino_pc:\n\t"
"s_bfe_u32 s42, %2, 0x50003\n\t"
"s_lshl_b32 s42, s42, 12\n\t"
"v_lshl_add_u32 v203, v205, 4, s42\n\t"
"v_min_u32 v203, (Lwino_end-Lwino_pc-16)&-16, v203\n\t"
"global_load_dwordx4 v[100:103], v203, s[40:41]\n\t"
"v_readfirstlane_b32 s73, v205\n\t"
"s_lshl2_add_u32 s69, s73, %2\n\t"
"s_and_b32 s60, %2, 7\n\t"
"s_lshr_b32 s61, %2, 7\n\t"
"s_lshl2_add_u32 s60, s60, s61\n\t"
"s_bfe_u32 s61, %2, 0x40003\n\t"
"s_lshl_b32 s60, s60, 8\n\t"
"s_add_u32 s60, s60, s61\n\t"
"s_sub_u32 s60, s60, 16\n\t"
"s_mul_i32 s21, s60, 12288\n\t"
"s_lshr_b32 s73, s73, 6\n\t"
"s_add_u32 s24, s73, 28\n\t"
"s_mulk_i32 s24, 1536\n\t"
"v_bfe_u32 v1, v205, 2, 3\n\t"
"v_lshrrev_b32 v2, 3, v205\n\t"
"v_and_or_b32 v1, v2, 24, v1\n\t"
"v_and_b32 v3, 6, v2\n\t"
"v_and_b32 v4, 3, v205\n\t"
"v_xor_b32 v4, v3, v4\n\t"
"v_mul_u32_u24 v1, 384, v1\n\t"
"v_lshl_add_u32 v28, v4, 4, v1\n\t"
"s_mov_b32 s14, 100663296\n\t"
"s_mov_b32 s15, 0x20000\n\t"
"s_mov_b32 s54, 663552\n\t"
"s_mov_b32 s55, s15\n\t"
"s_mov_b32 s10, 0x200000\n\t"
"s_mov_b32 s11, s15\n\t"
"s_lshl_b32 s60, s73, 11\n\t"
"v_lshl_add_u32 v252, v205, 4, s60\n\t"
"s_lshr_b32 s71, %5, 6\n\t"
"s_mul_i32 s71, s71, 0x9e3779b1\n\t"
"s_lshr_b32 s71, s71, 28\n\t"
"s_add_u32 s71, s71, 2\n\t"
"s_lshl_b32 s71, s71, 21\n\t"
"s_mov_b64 s[46:47], %3\n\t"
"s_add_u32 s48, s46, 12288\n\t"
"s_addc_u32 s49, s47, 0\n\t"
"global_load_dwordx4 v[244:247], v252, s[46:47] offset:2048\n\t"
"s_and_b32 s60, s69, 3\n\t"
"s_lshr_b32 s61, s69, 2\n\t"
"s_mul_i32 s62, s61, 171\n\t"
"s_lshr_b32 s62, s62, 9\n\t"
"s_mul_i32 s63, s62, 3\n\t"
"s_sub_u32 s61, s61, s63\n\t"
"s_mul_i32 s63, s62, 43\n\t"
"s_lshr_b32 s63, s63, 7\n\t"
"s_mul_i32 s64, s63, 3\n\t"
"s_sub_u32 s62, s62, s64\n\t"
"s_mul_i32 s74, s62, 18\n\t"
"s_add_u32 s74, s74, s63\n\t"
"s_lshl3_add_u32 s74, s74, s60\n\t"
"s_mul_i32 s74, s74, 12\n\t"
"s_add_u32 s74, s74, s61\n\t"
"s_lshl_b32 s74, s74, 7\n\t"
"v_bfe_u32 v96, v205, 3, 2\n\t"
"v_and_b32 v97, 32, v205\n\t"
"v_lshl_or_b32 v96, v96, 1, v97\n\t"
"v_mul_u32_u24 v96, 192, v96\n\t"
"v_and_b32 v97, 7, v205\n\t"
"v_lshl_add_u32 v96, v97, 4, v96\n\t"
"v_and_b32 v4, 7, v205\n\t"
"v_xor_b32 v4, v3, v4\n\t"
"v_and_b32 v2, 7, v2\n\t"
"v_mul_u32_u24 v2, 384, v2\n\t"
"v_lshl_add_u32 v29, v4, 4, v2\n\t"
"s_sub_u32 s58, s73, 1\n\t"
"s_lshl_b32 s59, s58, 10\n\t"
"s_lshl_b32 s58, s58, 7\n\t"
"s_waitcnt lgkmcnt(0)\n\t"
"s_and_b32 s13, s13, 0xffff\n\t"
"s_and_b32 s53, s53, 0xffff\n\t"
"s_and_b32 s71, s71, s76\n\t"
"s_add_u32 s8, s8, s71\n\t"
"s_addc_u32 s9, s9, 0\n\t"
"s_and_b32 s9, s9, 0xffff\n\t"
"s_cmp_lg_u32 s73, 0\n\t"
"s_cbranch_scc1 Lpb_%=\n\t"
"s_cmp_lt_u32 s69, 216\n\t"
"s_cbranch_scc0 Lnf1_%=\n\t"
"s_mov_b32 m0, 95232\n\t"
"s_add_u32 s64, s74, 0\n\t"
"buffer_load_dwordx4 v96, s[52:55], s64 offen lds\n\t"
"s_mov_b32 m0, 96256\n\t"
"s_add_u32 s64, s74, 73728\n\t"
"buffer_load_dwordx4 v96, s[52:55], s64 offen lds\n\t"
"s_mov_b32 m0, 97280\n\t"
"s_add_u32 s64, s74, 147456\n\t"
"buffer_load_dwordx4 v96, s[52:55], s64 offen lds\n\t"
"Lnf1_%=:\n\t"
"s_mov_b32 s72, 1900544\n\t"
"s_lshl2_add_u32 s70, %2, s72\n\t"
"s_mov_b32 s68, 3000\n\t"
"buffer_load_dwordx4 v[220:223], v252, s[8:11], s72 offen sc1\n\t"
"s_branch Lpa_%=\n\t"
"Lpb_%=:\n\t"
"s_lshr_b32 s56, %2, 3\n\t"
"s_bitcmp1_b32 0xaaaaaaaa, s56\n\t"
"s_cbranch_scc1 Lpc_%=\n\t"
"s_add_u32 m0, s59, 24576\n\t"
"s_add_u32 s57, s58, 0\n\t"
"buffer_load_dwordx4 v29, s[52:55], s57 offen offset:0 lds\n\t"
"buffer_load_dwordx4 v29, s[52:55], s57 offen offset:3072 lds\n\t"
"s_add_u32 m0, s59, 30720\n\t"
"s_add_u32 s57, s58, 6144\n\t"
"buffer_load_dwordx4 v29, s[52:55], s57 offen offset:0 lds\n\t"
"buffer_load_dwordx4 v29, s[52:55], s57 offen offset:3072 lds\n\t"
"s_add_u32 m0, s59, 36864\n\t"
"s_add_u32 s57, s58, 221184\n\t"
"buffer_load_dwordx4 v29, s[52:55], s57 offen offset:0 lds\n\t"
"buffer_load_dwordx4 v29, s[52:55], s57 offen offset:3072 lds\n\t"
"s_add_u32 m0, s59, 82944\n\t"
"s_add_u32 s57, s58, 227328\n\t"
"buffer_load_dwordx4 v29, s[52:55], s57 offen offset:0 lds\n\t"
"buffer_load_dwordx4 v29, s[52:55], s57 offen offset:3072 lds\n\t"
"s_add_u32 m0, s59, 89088\n\t"
"s_add_u32 s57, s58, 442368\n\t"
"buffer_load_dwordx4 v29, s[52:55], s57 offen offset:0 lds\n\t"
"buffer_load_dwordx4 v29, s[52:55], s57 offen offset:3072 lds\n\t"
"s_add_u32 m0, s59, 142848\n\t"
"s_add_u32 s57, s58, 448512\n\t"
"buffer_load_dwordx4 v29, s[52:55], s57 offen offset:0 lds\n\t"
"buffer_load_dwordx4 v29, s[52:55], s57 offen offset:3072 lds\n\t"
"s_branch Lpro_%=\n\t"
"Lpa_%=:\n\t"
"s_mul_i32 s75, s63, 288\n\t"
"s_add_u32 s75, s75, s62\n\t"
"s_lshl2_add_u32 s75, s75, s60\n\t"
"s_cmp_lt_u32 s61, 2\n\t"
"s_cselect_b64 vcc, -1, 0\n\t"
"s_cselect_b32 s61, s61, 4\n\t"
"s_mulk_i32 s61, 24\n\t"
"s_add_u32 s75, s75, s61\n\t"
"s_lshl_b32 s75, s75, 8\n\t"
"v_and_b32 v99, 12, v205\n\t"
"v_and_b32 v60, 35, v205\n\t"
"v_lshl_or_b32 v60, v99, 1, v60\n\t"
"v_bfe_u32 v99, v205, 4, 1\n\t"
"v_lshl_or_b32 v60, v99, 2, v60\n\t"
"v_cndmask_b32 v60, v205, v60, vcc\n\t"
"v_lshlrev_b32 v60, 1, v60\n\t"
"v_add_u32 v60, 95232, v60\n\t"
"v_lshrrev_b32 v97, 5, v205\n\t"
"v_lshl_or_b32 v97, v99, 2, v97\n\t"
"v_lshrrev_b32 v99, 4, v205\n\t"
"v_cndmask_b32 v97, v99, v97, vcc\n\t"
"v_mul_u32_u24 v97, 3072, v97\n\t"
"v_and_b32 v99, 15, v205\n\t"
"v_lshl_add_u32 v97, v99, 4, v97\n\t"
"s_cmp_lt_u32 s63, 2\n\t"
"s_mov_b32 s56, 0x3f2aaaab\n\t"
"s_mov_b32 s57, 0x3fc00000\n\t"
"s_mov_b32 s58, 0x3ee38e39\n\t"
"s_mov_b32 s59, 0x40100000\n\t"
"s_mov_b32 s62, 0xbf54ad4b\n\t"
"s_mov_b32 s63, 0xbefc0fc1\n\t"
"s_mov_b32 s64, 0xbe7c0fc1\n\t"
"s_mov_b32 s65, 0x3ea80a81\n\t"
"s_mov_b32 s38, 0x3f638e39\n\t"
"s_mov_b32 s39, 0x3f638e39\n\t"
"s_cselect_b32 s38, 0x3ea80a81, s38\n\t"
"s_cselect_b32 s39, 0xbebd0bd1, s39\n\t"
"v_mov_b64 v[32:33], 0\n\t"
"v_mov_b64 v[34:35], 0\n\t"
"v_mov_b64 v[36:37], 0\n\t"
"v_mov_b64 v[38:39], 0\n\t"
"v_mov_b64 v[40:41], 0\n\t"
"v_mov_b64 v[42:43], 0\n\t"
"v_mov_b64 v[44:45], 0\n\t"
"v_mov_b64 v[46:47], 0\n\t"
"v_mov_b64 v[48:49], 0\n\t"
"v_mov_b64 v[50:51], 0\n\t"
"v_mov_b64 v[52:53], 0\n\t"
"v_mov_b64 v[54:55], 0\n\t"
"s_mul_i32 s25, s73, 19968\n\t"
"s_add_u32 s25, s25, 61440\n\t"
"s_bfe_u32 s41, %2, 0x40003\n\t"
"s_cmp_eq_u32 s41, 0\n\t"
"s_cselect_b32 s40, -1, 0xbfffffff\n\t"
"s_cmp_eq_u32 s41, 15\n\t"
"s_cselect_b32 s41, -1, 0xdfffffff\n\t"
"s_and_b32 s40, s40, s41\n\t"
"s_sub_u32 s42, s21, 384\n\t"
"s_mul_i32 s41, s73, 589824\n\t"
"s_add_u32 s42, s42, s41\n\t"
"s_and_b32 s41, s40, 536883584\n\t"
"s_and_b32 s40, s40, 2.0\n\t"
"v_bfe_u32 v26, v205, 2, 3\n\t"
"v_add_u32 v27, -1, v26\n\t"
"v_cmp_gt_u32 vcc, 6, v27\n\t"
"v_lshrrev_b32 v27, 1, v27\n\t"
"v_mul_u32_u24 v27, 196608, v27\n\t"
"v_and_b32 v20, 0x70, v28\n\t"
"v_add3_u32 v27, v27, v20, s42\n\t"
"v_bfe_i32 v20, v205, 2, 1\n\t"
"v_mov_b32 v21, s40\n\t"
"v_bfi_b32 v20, v20, s41, v21\n\t"
"v_add_u32 v27, v27, v20\n\t"
"v_bfrev_b32 v20, 8\n\t"
"v_cndmask_b32 v254, v20, v27, vcc\n\t"
"v_bfe_u32 v26, v205, 2, 3\n\t"
"v_bfe_u32 v27, v205, 5, 1\n\t"
"v_mul_u32_u24 v27, 3072, v27\n\t"
"v_lshl_add_u32 v27, v26, 7, v27\n\t"
"v_and_b32 v26, 6, v26\n\t"
"v_lshrrev_b32 v20, 4, v205\n\t"
"v_and_b32 v20, 4, v20\n\t"
"v_and_or_b32 v20, v205, 3, v20\n\t"
"v_xor_b32 v20, v20, v26\n\t"
"v_lshl_add_u32 v20, v20, 4, v27\n\t"
"v_lshrrev_b32 v21, 7, v205\n\t"
"v_lshl_add_u32 v20, v21, 10, v20\n\t"
"v_lshrrev_b32 v21, 5, v205\n\t"
"v_and_b32 v21, 6, v21\n\t"
"v_bfe_u32 v22, v205, 1, 1\n\t"
"v_or_b32 v21, v21, v22\n\t"
"v_xor_b32 v21, v21, v26\n\t"
"v_lshl_add_u32 v21, v21, 4, v27\n\t"
"v_and_b32 v22, 1, v205\n\t"
"v_lshl_add_u32 v21, v22, 3, v21\n\t"
"v_add_u32 v22, 142848, v20\n\t"
"v_add_u32 v23, 142848, v21\n\t"
"v_add_u32 v24, 82944, v20\n\t"
"v_add_u32 v25, 82944, v21\n\t"
"s_and_b32 s17, s17, 0xffff\n\t"
"s_mov_b32 s18, 100663296\n\t"
"s_mov_b32 s19, 0x20000\n\t"
"s_lshr_b32 s41, s73, 1\n\t"
"s_add_u32 s42, s21, 196608\n\t"
"s_mul_i32 s43, s41, 384\n\t"
"s_add_u32 s22, s42, s43\n\t"
"s_lshl1_add_u32 s40, s43, s22\n\t"
"s_mul_i32 s43, s41, 196608\n\t"
"s_add_u32 s23, s42, s43\n\t"
"s_mov_b32 s44, 0x0fff0fff\n\t"
"s_mov_b32 s45, 0x0fff0fff\n\t"
"s_mul_i32 s26, s41, 19968\n\t"
"s_add_u32 s26, s26, 141312\n\t"
"s_sub_u32 s43, s41, s73\n\t"
"s_add_u32 s43, s43, 4\n\t"
"s_mul_i32 s27, s43, 589824\n\t"
"s_bitcmp1_b32 s73, 0\n\t"
"s_mov_b32 s36, 0x0fff0000\n\t"
"s_cselect_b32 s36, 0xfff0, s36\n\t"
"s_mov_b32 s37, s36\n\t"
"s_cmp_lt_u32 s69, 216\n\t"
"s_cbranch_scc0 Lnf3_%=\n\t"
"s_waitcnt vmcnt(1)\n\t"
"ds_read_u16_d16_hi v32, v60 offset:0\n\t"
"ds_read_u16_d16_hi v33, v60 offset:128\n\t"
"ds_read_u16_d16_hi v40, v60 offset:1024\n\t"
"ds_read_u16_d16_hi v41, v60 offset:1152\n\t"
"ds_read_u16_d16_hi v48, v60 offset:2048\n\t"
"ds_read_u16_d16_hi v49, v60 offset:2176\n\t"
"ds_read_u16_d16_hi v34, v60 offset:256\n\t"
"ds_read_u16_d16_hi v35, v60 offset:384\n\t"
"ds_read_u16_d16_hi v42, v60 offset:1280\n\t"
"ds_read_u16_d16_hi v43, v60 offset:1408\n\t"
"ds_read_u16_d16_hi v50, v60 offset:2304\n\t"
"ds_read_u16_d16_hi v51, v60 offset:2432\n\t"
"ds_read_u16_d16_hi v36, v60 offset:512\n\t"
"ds_read_u16_d16_hi v37, v60 offset:640\n\t"
"ds_read_u16_d16_hi v44, v60 offset:1536\n\t"
"ds_read_u16_d16_hi v45, v60 offset:1664\n\t"
"ds_read_u16_d16_hi v52, v60 offset:2560\n\t"
"ds_read_u16_d16_hi v53, v60 offset:2688\n\t"
"ds_read_u16_d16_hi v38, v60 offset:768\n\t"
"ds_read_u16_d16_hi v39, v60 offset:896\n\t"
"ds_read_u16_d16_hi v46, v60 offset:1792\n\t"
"ds_read_u16_d16_hi v47, v60 offset:1920\n\t"
"ds_read_u16_d16_hi v54, v60 offset:2816\n\t"
"ds_read_u16_d16_hi v55, v60 offset:2944\n\t"
"s_waitcnt lgkmcnt(15)\n\t"
"v_pk_fma_f32 v[56:57], v[48:49], s[58:59], v[32:33] op_sel:[0,0,0] op_sel_hi:[1,0,1]\n\t"
"v_pk_fma_f32 v[58:59], v[48:49], s[58:59], v[32:33] op_sel:[0,1,0] op_sel_hi:[1,1,1]\n\t"
"v_pk_mul_f32 v[104:105], v[32:33], s[38:39] op_sel:[0,0] op_sel_hi:[1,0]\n\t"
"v_pk_mul_f32 v[114:115], v[48:49], s[38:39] op_sel:[0,1] op_sel_hi:[1,1]\n\t"
"v_pk_fma_f32 v[106:107], v[40:41], s[56:57], v[56:57] op_sel:[0,0,0] op_sel_hi:[1,0,1]\n\t"
"v_pk_fma_f32 v[108:109], v[40:41], s[56:57], v[56:57] op_sel:[0,0,0] op_sel_hi:[1,0,1] neg_lo:[1,0,0] neg_hi:[1,0,0]\n\t"
"v_pk_fma_f32 v[110:111], v[40:41], s[56:57], v[58:59] op_sel:[0,1,0] op_sel_hi:[1,1,1]\n\t"
"v_pk_fma_f32 v[112:113], v[40:41], s[56:57], v[58:59] op_sel:[0,1,0] op_sel_hi:[1,1,1] neg_lo:[1,0,0] neg_hi:[1,0,0]\n\t"
"v_pk_mul_f32 v[106:107], v[106:107], s[62:63] op_sel:[0,0] op_sel_hi:[1,0]\n\t"
"v_pk_mul_f32 v[108:109], v[108:109], s[62:63] op_sel:[0,1] op_sel_hi:[1,1]\n\t"
"v_pk_mul_f32 v[110:111], v[110:111], s[64:65] op_sel:[0,0] op_sel_hi:[1,0]\n\t"
"v_pk_mul_f32 v[112:113], v[112:113], s[64:65] op_sel:[0,1] op_sel_hi:[1,1]\n\t"
"v_cvt_pk_bf16_f32 v64, v104, v105\n\t"
"v_cvt_pk_bf16_f32 v84, v114, v115\n\t"
"v_cvt_pk_bf16_f32 v68, v106, v107\n\t"
"v_cvt_pk_bf16_f32 v74, v108, v109\n\t"
"v_cvt_pk_bf16_f32 v76, v110, v111\n\t"
"v_cvt_pk_bf16_f32 v82, v112, v113\n\t"
"s_waitcnt lgkmcnt(12)\n\t"
"v_pk_fma_f32 v[56:57], v[50:51], s[58:59], v[34:35] op_sel:[0,0,0] op_sel_hi:[1,0,1]\n\t"
"v_pk_fma_f32 v[58:59], v[50:51], s[58:59], v[34:35] op_sel:[0,1,0] op_sel_hi:[1,1,1]\n\t"
"v_pk_mul_f32 v[104:105], v[34:35], s[38:39] op_sel:[0,0] op_sel_hi:[1,0]\n\t"
"v_pk_mul_f32 v[114:115], v[50:51], s[38:39] op_sel:[0,1] op_sel_hi:[1,1]\n\t"
"v_pk_fma_f32 v[106:107], v[42:43], s[56:57], v[56:57] op_sel:[0,0,0] op_sel_hi:[1,0,1]\n\t"
"v_pk_fma_f32 v[108:109], v[42:43], s[56:57], v[56:57] op_sel:[0,0,0] op_sel_hi:[1,0,1] neg_lo:[1,0,0] neg_hi:[1,0,0]\n\t"
"v_pk_fma_f32 v[110:111], v[42:43], s[56:57], v[58:59] op_sel:[0,1,0] op_sel_hi:[1,1,1]\n\t"
"v_pk_fma_f32 v[112:113], v[42:43], s[56:57], v[58:59] op_sel:[0,1,0] op_sel_hi:[1,1,1] neg_lo:[1,0,0] neg_hi:[1,0,0]\n\t"
"v_pk_mul_f32 v[106:107], v[106:107], s[62:63] op_sel:[0,0] op_sel_hi:[1,0]\n\t"
"v_pk_mul_f32 v[108:109], v[108:109], s[62:63] op_sel:[0,1] op_sel_hi:[1,1]\n\t"
"v_pk_mul_f32 v[110:111], v[110:111], s[64:65] op_sel:[0,0] op_sel_hi:[1,0]\n\t"
"v_pk_mul_f32 v[112:113], v[112:113], s[64:65] op_sel:[0,1] op_sel_hi:[1,1]\n\t"
"v_cvt_pk_bf16_f32 v65, v104, v105\n\t"
"v_cvt_pk_bf16_f32 v85, v114, v115\n\t"
"v_cvt_pk_bf16_f32 v69, v106, v107\n\t"
"v_cvt_pk_bf16_f32 v75, v108, v109\n\t"
"v_cvt_pk_bf16_f32 v77, v110, v111\n\t"
"v_cvt_pk_bf16_f32 v83, v112, v113\n\t"
"s_waitcnt lgkmcnt(6)\n\t"
"v_pk_fma_f32 v[56:57], v[52:53], s[58:59], v[36:37] op_sel:[0,0,0] op_sel_hi:[1,0,1]\n\t"
"v_pk_fma_f32 v[58:59], v[52:53], s[58:59], v[36:37] op_sel:[0,1,0] op_sel_hi:[1,1,1]\n\t"
"v_pk_mul_f32 v[104:105], v[36:37], s[38:39] op_sel:[0,0] op_sel_hi:[1,0]\n\t"
"v_pk_mul_f32 v[114:115], v[52:53], s[38:39] op_sel:[0,1] op_sel_hi:[1,1]\n\t"
"v_pk_fma_f32 v[106:107], v[44:45], s[56:57], v[56:57] op_sel:[0,0,0] op_sel_hi:[1,0,1]\n\t"
"v_pk_fma_f32 v[108:109], v[44:45], s[56:57], v[56:57] op_sel:[0,0,0] op_sel_hi:[1,0,1] neg_lo:[1,0,0] neg_hi:[1,0,0]\n\t"
"v_pk_fma_f32 v[110:111], v[44:45], s[56:57], v[58:59] op_sel:[0,1,0] op_sel_hi:[1,1,1]\n\t"
"v_pk_fma_f32 v[112:113], v[44:45], s[56:57], v[58:59] op_sel:[0,1,0] op_sel_hi:[1,1,1] neg_lo:[1,0,0] neg_hi:[1,0,0]\n\t"
"v_pk_mul_f32 v[106:107], v[106:107], s[62:63] op_sel:[0,0] op_sel_hi:[1,0]\n\t"
"v_pk_mul_f32 v[108:109], v[108:109], s[62:63] op_sel:[0,1] op_sel_hi:[1,1]\n\t"
"v_pk_mul_f32 v[110:111], v[110:111], s[64:65] op_sel:[0,0] op_sel_hi:[1,0]\n\t"
"v_pk_mul_f32 v[112:113], v[112:113], s[64:65] op_sel:[0,1] op_sel_hi:[1,1]\n\t"
"v_cvt_pk_bf16_f32 v66, v104, v105\n\t"
"v_cvt_pk_bf16_f32 v86, v114, v115\n\t"
"v_cvt_pk_bf16_f32 v70, v106, v107\n\t"
"v_cvt_pk_bf16_f32 v72, v108, v109\n\t"
"v_cvt_pk_bf16_f32 v78, v110, v111\n\t"
"v_cvt_pk_bf16_f32 v80, v112, v113\n\t"
"s_waitcnt lgkmcnt(0)\n\t"
"v_pk_fma_f32 v[56:57], v[54:55], s[58:59], v[38:39] op_sel:[0,0,0] op_sel_hi:[1,0,1]\n\t"
"v_pk_fma_f32 v[58:59], v[54:55], s[58:59], v[38:39] op_sel:[0,1,0] op_sel_hi:[1,1,1]\n\t"
"v_pk_mul_f32 v[104:105], v[38:39], s[38:39] op_sel:[0,0] op_sel_hi:[1,0]\n\t"
"v_pk_mul_f32 v[114:115], v[54:55], s[38:39] op_sel:[0,1] op_sel_hi:[1,1]\n\t"
"v_pk_fma_f32 v[106:107], v[46:47], s[56:57], v[56:57] op_sel:[0,0,0] op_sel_hi:[1,0,1]\n\t"
"v_pk_fma_f32 v[108:109], v[46:47], s[56:57], v[56:57] op_sel:[0,0,0] op_sel_hi:[1,0,1] neg_lo:[1,0,0] neg_hi:[1,0,0]\n\t"
"v_pk_fma_f32 v[110:111], v[46:47], s[56:57], v[58:59] op_sel:[0,1,0] op_sel_hi:[1,1,1]\n\t"
"v_pk_fma_f32 v[112:113], v[46:47], s[56:57], v[58:59] op_sel:[0,1,0] op_sel_hi:[1,1,1] neg_lo:[1,0,0] neg_hi:[1,0,0]\n\t"
"v_pk_mul_f32 v[106:107], v[106:107], s[62:63] op_sel:[0,0] op_sel_hi:[1,0]\n\t"
"v_pk_mul_f32 v[108:109], v[108:109], s[62:63] op_sel:[0,1] op_sel_hi:[1,1]\n\t"
"v_pk_mul_f32 v[110:111], v[110:111], s[64:65] op_sel:[0,0] op_sel_hi:[1,0]\n\t"
"v_pk_mul_f32 v[112:113], v[112:113], s[64:65] op_sel:[0,1] op_sel_hi:[1,1]\n\t"
"v_cvt_pk_bf16_f32 v67, v104, v105\n\t"
"v_cvt_pk_bf16_f32 v87, v114, v115\n\t"
"v_cvt_pk_bf16_f32 v71, v106, v107\n\t"
"v_cvt_pk_bf16_f32 v73, v108, v109\n\t"
"v_cvt_pk_bf16_f32 v79, v110, v111\n\t"
"v_cvt_pk_bf16_f32 v81, v112, v113\n\t"
"s_add_u32 s60, s75, 0\n\t"
"buffer_store_dwordx4 v[64:67], v97, s[8:11], s60 offen sc1\n\t"
"s_add_u32 s60, s75, 184320\n\t"
"buffer_store_dwordx4 v[84:87], v97, s[8:11], s60 offen sc1\n\t"
"s_add_u32 s60, s75, 36864\n\t"
"buffer_store_dwordx4 v[68:71], v97, s[8:11], s60 offen sc1\n\t"
"s_add_u32 s60, s75, 73728\n\t"
"buffer_store_dwordx4 v[72:75], v97, s[8:11], s60 offen sc1\n\t"
"s_add_u32 s60, s75, 110592\n\t"
"buffer_store_dwordx4 v[76:79], v97, s[8:11], s60 offen sc1\n\t"
"s_add_u32 s60, s75, 147456\n\t"
"buffer_store_dwordx4 v[80:83], v97, s[8:11], s60 offen sc1\n\t"
"s_waitcnt vmcnt(6)\n\t"
"s_branch Lnf4_%=\n\t"
"Lnf3_%=:\n\t"
"s_waitcnt vmcnt(0)\n\t"
"Lnf4_%=:\n\t"
"s_lshr_b32 s60, %2, 2\n\t"
"v_readlane_b32 s61, v220, s60\n\t"
"v_readlane_b32 s62, v221, s60\n\t"
"v_readlane_b32 s63, v222, s60\n\t"
"v_readlane_b32 s64, v223, s60\n\t"
"s_bitcmp1_b32 %2, 0\n\t"
"s_cselect_b32 s61, s62, s61\n\t"
"s_cselect_b32 s63, s64, s63\n\t"
"s_bitcmp1_b32 %2, 1\n\t"
"s_cselect_b32 s71, s63, s61\n\t"
"s_branch Ljoin_%=\n\t"
"Lpc_%=:\n\t"
"s_add_u32 m0, s24, 59904\n\t"
"s_add_u32 s28, s21, 1769472\n\t"
"buffer_load_dwordx4 v28, s[12:15], s28 offen offset:0 nt lds\n\t"
"s_add_u32 m0, s24, 66048\n\t"
"s_add_u32 s28, s21, 1966080\n\t"
"buffer_load_dwordx4 v28, s[12:15], s28 offen offset:0 nt lds\n\t"
"s_add_u32 m0, s24, 72192\n\t"
"s_add_u32 s28, s21, 2162688\n\t"
"buffer_load_dwordx4 v28, s[12:15], s28 offen offset:0 nt lds\n\t"
"s_mul_i32 s60, s59, 6\n\t"
"s_mul_i32 s61, s59, 192\n\t"
"s_add_u32 s61, s61, s21\n\t"
"s_mul_i32 s62, s73, 3072\n\t"
"v_subrev_u32 v30, s62, v28\n\t"
"s_add_u32 m0, s60, 102912\n\t"
"s_add_u32 s28, s61, 1769472\n\t"
"buffer_load_dwordx4 v30, s[12:15], s28 offen nt lds\n\t"
"s_add_u32 m0, s24, 79872\n\t"
"s_add_u32 s28, s21, 2359296\n\t"
"buffer_load_dwordx4 v28, s[12:15], s28 offen offset:0 nt lds\n\t"
"s_add_u32 m0, s24, 86016\n\t"
"s_add_u32 s28, s21, 2555904\n\t"
"buffer_load_dwordx4 v28, s[12:15], s28 offen offset:0 nt lds\n\t"
"s_add_u32 m0, s24, 92160\n\t"
"s_add_u32 s28, s21, 2752512\n\t"
"buffer_load_dwordx4 v28, s[12:15], s28 offen offset:0 nt lds\n\t"
"s_add_u32 m0, s60, 122880\n\t"
"s_add_u32 s28, s61, 2359296\n\t"
"buffer_load_dwordx4 v30, s[12:15], s28 offen nt lds\n\t"
"s_mul_i32 s25, s73, 19968\n\t"
"s_add_u32 s25, s25, 61440\n\t"
"s_bfe_u32 s41, %2, 0x40003\n\t"
"s_cmp_eq_u32 s41, 0\n\t"
"s_cselect_b32 s40, -1, 0xbfffffff\n\t"
"s_cmp_eq_u32 s41, 15\n\t"
"s_cselect_b32 s41, -1, 0xdfffffff\n\t"
"s_and_b32 s40, s40, s41\n\t"
"s_sub_u32 s42, s21, 384\n\t"
"s_mul_i32 s41, s73, 589824\n\t"
"s_add_u32 s42, s42, s41\n\t"
"s_and_b32 s41, s40, 536883584\n\t"
"s_and_b32 s40, s40, 2.0\n\t"
"v_bfe_u32 v26, v205, 2, 3\n\t"
"v_add_u32 v27, -1, v26\n\t"
"v_cmp_gt_u32 vcc, 6, v27\n\t"
"v_lshrrev_b32 v27, 1, v27\n\t"
"v_mul_u32_u24 v27, 196608, v27\n\t"
"v_and_b32 v20, 0x70, v28\n\t"
"v_add3_u32 v27, v27, v20, s42\n\t"
"v_bfe_i32 v20, v205, 2, 1\n\t"
"v_mov_b32 v21, s40\n\t"
"v_bfi_b32 v20, v20, s41, v21\n\t"
"v_add_u32 v27, v27, v20\n\t"
"v_bfrev_b32 v20, 8\n\t"
"v_cndmask_b32 v254, v20, v27, vcc\n\t"
"s_add_u32 m0, s25, 0\n\t"
"s_nop 0\n\t"
"buffer_load_dwordx4 v254, s[12:15], 0 offen nt lds\n\t"
"s_cmp_eq_u32 s73, 3\n\t"
"s_cselect_b32 s62, -1, 1\n\t"
"s_mul_i32 s63, s62, 59904\n\t"
"s_mul_i32 s62, s62, 1769472\n\t"
"v_add_u32 v19, s62, v254\n\t"
"s_add_u32 m0, s25, s63\n\t"
"s_nop 0\n\t"
"buffer_load_dwordx4 v19, s[12:15], 0 offen nt lds\n\t"
"v_bfe_u32 v26, v205, 2, 3\n\t"
"v_bfe_u32 v27, v205, 5, 1\n\t"
"v_mul_u32_u24 v27, 3072, v27\n\t"
"v_lshl_add_u32 v27, v26, 7, v27\n\t"
"v_and_b32 v26, 6, v26\n\t"
"v_lshrrev_b32 v20, 4, v205\n\t"
"v_and_b32 v20, 4, v20\n\t"
"v_and_or_b32 v20, v205, 3, v20\n\t"
"v_xor_b32 v20, v20, v26\n\t"
"v_lshl_add_u32 v20, v20, 4, v27\n\t"
"v_lshrrev_b32 v21, 7, v205\n\t"
"v_lshl_add_u32 v20, v21, 10, v20\n\t"
"v_lshrrev_b32 v21, 5, v205\n\t"
"v_and_b32 v21, 6, v21\n\t"
"v_bfe_u32 v22, v205, 1, 1\n\t"
"v_or_b32 v21, v21, v22\n\t"
"v_xor_b32 v21, v21, v26\n\t"
"v_lshl_add_u32 v21, v21, 4, v27\n\t"
"v_and_b32 v22, 1, v205\n\t"
"v_lshl_add_u32 v21, v22, 3, v21\n\t"
"v_add_u32 v22, 142848, v20\n\t"
"v_add_u32 v23, 142848, v21\n\t"
"v_add_u32 v24, 82944, v20\n\t"
"v_add_u32 v25, 82944, v21\n\t"
"s_and_b32 s17, s17, 0xffff\n\t"
"s_mov_b32 s18, 100663296\n\t"
"s_mov_b32 s19, 0x20000\n\t"
"s_lshr_b32 s41, s73, 1\n\t"
"s_add_u32 s42, s21, 196608\n\t"
"s_mul_i32 s43, s41, 384\n\t"
"s_add_u32 s22, s42, s43\n\t"
"s_lshl1_add_u32 s40, s43, s22\n\t"
"s_mul_i32 s43, s41, 196608\n\t"
"s_add_u32 s23, s42, s43\n\t"
"s_mov_b32 s44, 0x0fff0fff\n\t"
"s_mov_b32 s45, 0x0fff0fff\n\t"
"s_mul_i32 s26, s41, 19968\n\t"
"s_add_u32 s26, s26, 141312\n\t"
"s_sub_u32 s43, s41, s73\n\t"
"s_add_u32 s43, s43, 4\n\t"
"s_mul_i32 s27, s43, 589824\n\t"
"s_bitcmp1_b32 s73, 0\n\t"
"s_mov_b32 s36, 0x0fff0000\n\t"
"s_cselect_b32 s36, 0xfff0, s36\n\t"
"s_mov_b32 s37, s36\n\t"
"s_add_u32 m0, s59, 24576\n\t"
"s_add_u32 s57, s58, 0\n\t"
"buffer_load_dwordx4 v29, s[52:55], s57 offen offset:0 lds\n\t"
"buffer_load_dwordx4 v29, s[52:55], s57 offen offset:3072 lds\n\t"
"s_add_u32 m0, s59, 30720\n\t"
"s_add_u32 s57, s58, 6144\n\t"
"buffer_load_dwordx4 v29, s[52:55], s57 offen offset:0 lds\n\t"
"buffer_load_dwordx4 v29, s[52:55], s57 offen offset:3072 lds\n\t"
"s_add_u32 m0, s59, 36864\n\t"
"s_add_u32 s57, s58, 221184\n\t"
"buffer_load_dwordx4 v29, s[52:55], s57 offen offset:0 lds\n\t"
"buffer_load_dwordx4 v29, s[52:55], s57 offen offset:3072 lds\n\t"
"s_add_u32 m0, s59, 82944\n\t"
"s_add_u32 s57, s58, 227328\n\t"
"buffer_load_dwordx4 v29, s[52:55], s57 offen offset:0 lds\n\t"
"buffer_load_dwordx4 v29, s[52:55], s57 offen offset:3072 lds\n\t"
"s_add_u32 m0, s59, 89088\n\t"
"s_add_u32 s57, s58, 442368\n\t"
"buffer_load_dwordx4 v29, s[52:55], s57 offen offset:0 lds\n\t"
"buffer_load_dwordx4 v29, s[52:55], s57 offen offset:3072 lds\n\t"
"s_add_u32 m0, s59, 142848\n\t"
"s_add_u32 s57, s58, 448512\n\t"
"buffer_load_dwordx4 v29, s[52:55], s57 offen offset:0 lds\n\t"
"buffer_load_dwordx4 v29, s[52:55], s57 offen offset:3072 lds\n\t"
"s_add_u32 m0, s59, 148992\n\t"
"s_add_u32 s57, s58, 147456\n\t"
"buffer_load_dwordx4 v29, s[52:55], s57 offen offset:0 lds\n\t"
"buffer_load_dwordx4 v29, s[52:55], s57 offen offset:3072 lds\n\t"
"s_add_u32 m0, s59, 155136\n\t"
"s_add_u32 s57, s58, 153600\n\t"
"buffer_load_dwordx4 v29, s[52:55], s57 offen offset:0 lds\n\t"
"buffer_load_dwordx4 v29, s[52:55], s57 offen offset:3072 lds\n\t"
"s_add_u32 m0, s59, 0\n\t"
"s_add_u32 s57, s58, 368640\n\t"
"buffer_load_dwordx4 v29, s[52:55], s57 offen offset:0 lds\n\t"
"buffer_load_dwordx4 v29, s[52:55], s57 offen offset:3072 lds\n\t"
"s_add_u32 m0, s59, 6144\n\t"
"s_add_u32 s57, s58, 374784\n\t"
"buffer_load_dwordx4 v29, s[52:55], s57 offen offset:0 lds\n\t"
"buffer_load_dwordx4 v29, s[52:55], s57 offen offset:3072 lds\n\t"
"s_add_u32 m0, s59, 12288\n\t"
"s_add_u32 s57, s58, 589824\n\t"
"buffer_load_dwordx4 v29, s[52:55], s57 offen offset:0 lds\n\t"
"buffer_load_dwordx4 v29, s[52:55], s57 offen offset:3072 lds\n\t"
"s_add_u32 m0, s59, 18432\n\t"
"s_add_u32 s57, s58, 595968\n\t"
"buffer_load_dwordx4 v29, s[52:55], s57 offen offset:0 lds\n\t"
"buffer_load_dwordx4 v29, s[52:55], s57 offen offset:3072 lds\n\t"
"s_add_u32 m0, s24, 19968\n\t"
"s_add_u32 s28, s21, 589824\n\t"
"buffer_load_dwordx4 v28, s[12:15], s28 offen offset:0 nt lds\n\t"
"s_add_u32 m0, s24, 26112\n\t"
"s_add_u32 s28, s21, 786432\n\t"
"buffer_load_dwordx4 v28, s[12:15], s28 offen offset:0 nt lds\n\t"
"s_add_u32 m0, s24, 32256\n\t"
"s_add_u32 s28, s21, 983040\n\t"
"buffer_load_dwordx4 v28, s[12:15], s28 offen offset:0 nt lds\n\t"
"s_add_u32 m0, s60, 62976\n\t"
"s_add_u32 s28, s61, 589824\n\t"
"buffer_load_dwordx4 v30, s[12:15], s28 offen nt lds\n\t"
"s_add_u32 m0, s24, 0\n\t"
"s_add_u32 s28, s21, 0\n\t"
"buffer_load_dwordx4 v28, s[12:15], s28 offen offset:0 nt lds\n\t"
"s_add_u32 m0, s24, 6144\n\t"
"s_add_u32 s28, s21, 196608\n\t"
"buffer_load_dwordx4 v28, s[12:15], s28 offen offset:0 nt lds\n\t"
"s_add_u32 m0, s24, 12288\n\t"
"s_add_u32 s28, s21, 393216\n\t"
"buffer_load_dwordx4 v28, s[12:15], s28 offen offset:0 nt lds\n\t"
"s_add_u32 m0, s60, 43008\n\t"
"s_add_u32 s28, s61, 0\n\t"
"buffer_load_dwordx4 v30, s[12:15], s28 offen nt lds\n\t"
"s_waitcnt vmcnt(42)\n\t"
"s_branch Ljoin_%=\n\t"
"Lfb_%=:\n\t"
"s_waitcnt vmcnt(0) lgkmcnt(0)\n\t"
"s_nop 15\n\t"
"v_lshrrev_b32 v255, 9, v252\n\t"
"v_lshl_add_u32 v255, v255, 10, v252\n\t"
"v_add_u32 v255, 44032, v255\n\t"
"ds_write_b128 v255, v[32:35] offset:0\n\t"
"ds_write_b128 v255, v[36:39] offset:3072\n\t"
"ds_write_b128 v255, v[40:43] offset:6144\n\t"
"ds_write_b128 v255, v[44:47] offset:9216\n\t"
"ds_write_b128 v255, v[48:51] offset:12288\n\t"
"ds_write_b128 v255, v[52:55] offset:15360\n\t"
"ds_write_b128 v255, v[56:59] offset:18432\n\t"
"ds_write_b128 v255, v[60:63] offset:21504\n\t"
"ds_write_b128 v255, v[64:67] offset:24576\n\t"
"ds_write_b128 v255, v[68:71] offset:27648\n\t"
"ds_write_b128 v255, v[72:75] offset:30720\n\t"
"ds_write_b128 v255, v[76:79] offset:33792\n\t"
"ds_write_b128 v255, v[80:83] offset:36864\n\t"
"ds_write_b128 v255, v[84:87] offset:39936\n\t"
"ds_write_b128 v255, v[88:91] offset:43008\n\t"
"ds_write_b128 v255, v[92:95] offset:46080\n\t"
"ds_write_b128 v255, v[96:99] offset:49152\n\t"
"ds_write_b128 v255, v[100:103] offset:52224\n\t"
"ds_write_b128 v255, v[104:107] offset:55296\n\t"
"ds_write_b128 v255, v[108:111] offset:58368\n\t"
"ds_write_b128 v255, v[112:115] offset:61440\n\t"
"ds_write_b128 v255, v[116:119] offset:64512\n\t"
"s_waitcnt lgkmcnt(0)\n\t"
"v_mov_b64 v[32:33], 0\n\t"
"v_mov_b64 v[34:35], 0\n\t"
"v_mov_b64 v[36:37], 0\n\t"
"v_mov_b64 v[38:39], 0\n\t"
"v_mov_b64 v[40:41], 0\n\t"
"v_mov_b64 v[42:43], 0\n\t"
"v_mov_b64 v[44:45], 0\n\t"
"v_mov_b64 v[46:47], 0\n\t"
"v_mov_b64 v[48:49], 0\n\t"
"v_mov_b64 v[50:51], 0\n\t"
"v_mov_b64 v[52:53], 0\n\t"
"v_mov_b64 v[54:55], 0\n\t"
"v_lshrrev_b32 v98, 4, v252\n\t"
"v_bfe_u32 v96, v98, 3, 2\n\t"
"v_and_b32 v97, 32, v98\n\t"
"v_lshl_or_b32 v96, v96, 1, v97\n\t"
"v_mul_u32_u24 v96, 192, v96\n\t"
"v_and_b32 v97, 7, v98\n\t"
"v_lshl_add_u32 v96, v97, 4, v96\n\t"
"s_mov_b32 s69, 0\n\t"
"Lfb1_%=:\n\t"
"s_and_b32 s60, s69, 3\n\t"
"s_lshr_b32 s61, s69, 2\n\t"
"s_mul_i32 s62, s61, 171\n\t"
"s_lshr_b32 s62, s62, 9\n\t"
"s_mul_i32 s63, s62, 3\n\t"
"s_sub_u32 s61, s61, s63\n\t"
"s_mul_i32 s63, s62, 43\n\t"
"s_lshr_b32 s63, s63, 7\n\t"
"s_mul_i32 s64, s63, 3\n\t"
"s_sub_u32 s62, s62, s64\n\t"
"s_mul_i32 s74, s62, 18\n\t"
"s_add_u32 s74, s74, s63\n\t"
"s_lshl3_add_u32 s74, s74, s60\n\t"
"s_mul_i32 s74, s74, 12\n\t"
"s_add_u32 s74, s74, s61\n\t"
"s_lshl_b32 s74, s74, 7\n\t"
"s_mov_b32 m0, 18432\n\t"
"s_add_u32 s64, s74, 0\n\t"
"buffer_load_dwordx4 v96, s[52:55], s64 offen lds\n\t"
"s_mov_b32 m0, 19456\n\t"
"s_add_u32 s64, s74, 73728\n\t"
"buffer_load_dwordx4 v96, s[52:55], s64 offen lds\n\t"
"s_mov_b32 m0, 20480\n\t"
"s_add_u32 s64, s74, 147456\n\t"
"buffer_load_dwordx4 v96, s[52:55], s64 offen lds\n\t"
"s_mul_i32 s75, s63, 288\n\t"
"s_add_u32 s75, s75, s62\n\t"
"s_lshl2_add_u32 s75, s75, s60\n\t"
"s_cmp_lt_u32 s61, 2\n\t"
"s_cselect_b64 vcc, -1, 0\n\t"
"s_cselect_b32 s61, s61, 4\n\t"
"s_mulk_i32 s61, 24\n\t"
"s_add_u32 s75, s75, s61\n\t"
"s_lshl_b32 s75, s75, 8\n\t"
"v_and_b32 v99, 12, v98\n\t"
"v_and_b32 v60, 35, v98\n\t"
"v_lshl_or_b32 v60, v99, 1, v60\n\t"
"v_bfe_u32 v99, v98, 4, 1\n\t"
"v_lshl_or_b32 v60, v99, 2, v60\n\t"
"v_cndmask_b32 v60, v98, v60, vcc\n\t"
"v_lshlrev_b32 v60, 1, v60\n\t"
"v_add_u32 v60, 18432, v60\n\t"
"v_lshrrev_b32 v97, 5, v98\n\t"
"v_lshl_or_b32 v97, v99, 2, v97\n\t"
"v_lshrrev_b32 v99, 4, v98\n\t"
"v_cndmask_b32 v97, v99, v97, vcc\n\t"
"v_mul_u32_u24 v97, 3072, v97\n\t"
"v_and_b32 v99, 15, v98\n\t"
"v_lshl_add_u32 v97, v99, 4, v97\n\t"
"s_cmp_lt_u32 s63, 2\n\t"
"s_mov_b32 s56, 0x3f2aaaab\n\t"
"s_mov_b32 s57, 0x3fc00000\n\t"
"s_mov_b32 s58, 0x3ee38e39\n\t"
"s_mov_b32 s59, 0x40100000\n\t"
"s_mov_b32 s62, 0xbf54ad4b\n\t"
"s_mov_b32 s63, 0xbefc0fc1\n\t"
"s_mov_b32 s64, 0xbe7c0fc1\n\t"
"s_mov_b32 s65, 0x3ea80a81\n\t"
"s_mov_b32 s38, 0x3f638e39\n\t"
"s_mov_b32 s39, 0x3f638e39\n\t"
"s_cselect_b32 s38, 0x3ea80a81, s38\n\t"
"s_cselect_b32 s39, 0xbebd0bd1, s39\n\t"
"s_waitcnt vmcnt(0)\n\t"
"ds_read_u16_d16_hi v32, v60 offset:0\n\t"
"ds_read_u16_d16_hi v33, v60 offset:128\n\t"
"ds_read_u16_d16_hi v40, v60 offset:1024\n\t"
"ds_read_u16_d16_hi v41, v60 offset:1152\n\t"
"ds_read_u16_d16_hi v48, v60 offset:2048\n\t"
"ds_read_u16_d16_hi v49, v60 offset:2176\n\t"
"ds_read_u16_d16_hi v34, v60 offset:256\n\t"
"ds_read_u16_d16_hi v35, v60 offset:384\n\t"
"ds_read_u16_d16_hi v42, v60 offset:1280\n\t"
"ds_read_u16_d16_hi v43, v60 offset:1408\n\t"
"ds_read_u16_d16_hi v50, v60 offset:2304\n\t"
"ds_read_u16_d16_hi v51, v60 offset:2432\n\t"
"ds_read_u16_d16_hi v36, v60 offset:512\n\t"
"ds_read_u16_d16_hi v37, v60 offset:640\n\t"
"ds_read_u16_d16_hi v44, v60 offset:1536\n\t"
"ds_read_u16_d16_hi v45, v60 offset:1664\n\t"
"ds_read_u16_d16_hi v52, v60 offset:2560\n\t"
"ds_read_u16_d16_hi v53, v60 offset:2688\n\t"
"ds_read_u16_d16_hi v38, v60 offset:768\n\t"
"ds_read_u16_d16_hi v39, v60 offset:896\n\t"
"ds_read_u16_d16_hi v46, v60 offset:1792\n\t"
"ds_read_u16_d16_hi v47, v60 offset:1920\n\t"
"ds_read_u16_d16_hi v54, v60 offset:2816\n\t"
"ds_read_u16_d16_hi v55, v60 offset:2944\n\t"
"s_waitcnt lgkmcnt(15)\n\t"
"v_pk_fma_f32 v[56:57], v[48:49], s[58:59], v[32:33] op_sel:[0,0,0] op_sel_hi:[1,0,1]\n\t"
"v_pk_fma_f32 v[58:59], v[48:49], s[58:59], v[32:33] op_sel:[0,1,0] op_sel_hi:[1,1,1]\n\t"
"v_pk_mul_f32 v[104:105], v[32:33], s[38:39] op_sel:[0,0] op_sel_hi:[1,0]\n\t"
"v_pk_mul_f32 v[114:115], v[48:49], s[38:39] op_sel:[0,1] op_sel_hi:[1,1]\n\t"
"v_pk_fma_f32 v[106:107], v[40:41], s[56:57], v[56:57] op_sel:[0,0,0] op_sel_hi:[1,0,1]\n\t"
"v_pk_fma_f32 v[108:109], v[40:41], s[56:57], v[56:57] op_sel:[0,0,0] op_sel_hi:[1,0,1] neg_lo:[1,0,0] neg_hi:[1,0,0]\n\t"
"v_pk_fma_f32 v[110:111], v[40:41], s[56:57], v[58:59] op_sel:[0,1,0] op_sel_hi:[1,1,1]\n\t"
"v_pk_fma_f32 v[112:113], v[40:41], s[56:57], v[58:59] op_sel:[0,1,0] op_sel_hi:[1,1,1] neg_lo:[1,0,0] neg_hi:[1,0,0]\n\t"
"v_pk_mul_f32 v[106:107], v[106:107], s[62:63] op_sel:[0,0] op_sel_hi:[1,0]\n\t"
"v_pk_mul_f32 v[108:109], v[108:109], s[62:63] op_sel:[0,1] op_sel_hi:[1,1]\n\t"
"v_pk_mul_f32 v[110:111], v[110:111], s[64:65] op_sel:[0,0] op_sel_hi:[1,0]\n\t"
"v_pk_mul_f32 v[112:113], v[112:113], s[64:65] op_sel:[0,1] op_sel_hi:[1,1]\n\t"
"v_cvt_pk_bf16_f32 v64, v104, v105\n\t"
"v_cvt_pk_bf16_f32 v84, v114, v115\n\t"
"v_cvt_pk_bf16_f32 v68, v106, v107\n\t"
"v_cvt_pk_bf16_f32 v74, v108, v109\n\t"
"v_cvt_pk_bf16_f32 v76, v110, v111\n\t"
"v_cvt_pk_bf16_f32 v82, v112, v113\n\t"
"s_waitcnt lgkmcnt(12)\n\t"
"v_pk_fma_f32 v[56:57], v[50:51], s[58:59], v[34:35] op_sel:[0,0,0] op_sel_hi:[1,0,1]\n\t"
"v_pk_fma_f32 v[58:59], v[50:51], s[58:59], v[34:35] op_sel:[0,1,0] op_sel_hi:[1,1,1]\n\t"
"v_pk_mul_f32 v[104:105], v[34:35], s[38:39] op_sel:[0,0] op_sel_hi:[1,0]\n\t"
"v_pk_mul_f32 v[114:115], v[50:51], s[38:39] op_sel:[0,1] op_sel_hi:[1,1]\n\t"
"v_pk_fma_f32 v[106:107], v[42:43], s[56:57], v[56:57] op_sel:[0,0,0] op_sel_hi:[1,0,1]\n\t"
"v_pk_fma_f32 v[108:109], v[42:43], s[56:57], v[56:57] op_sel:[0,0,0] op_sel_hi:[1,0,1] neg_lo:[1,0,0] neg_hi:[1,0,0]\n\t"
"v_pk_fma_f32 v[110:111], v[42:43], s[56:57], v[58:59] op_sel:[0,1,0] op_sel_hi:[1,1,1]\n\t"
"v_pk_fma_f32 v[112:113], v[42:43], s[56:57], v[58:59] op_sel:[0,1,0] op_sel_hi:[1,1,1] neg_lo:[1,0,0] neg_hi:[1,0,0]\n\t"
"v_pk_mul_f32 v[106:107], v[106:107], s[62:63] op_sel:[0,0] op_sel_hi:[1,0]\n\t"
"v_pk_mul_f32 v[108:109], v[108:109], s[62:63] op_sel:[0,1] op_sel_hi:[1,1]\n\t"
"v_pk_mul_f32 v[110:111], v[110:111], s[64:65] op_sel:[0,0] op_sel_hi:[1,0]\n\t"
"v_pk_mul_f32 v[112:113], v[112:113], s[64:65] op_sel:[0,1] op_sel_hi:[1,1]\n\t"
"v_cvt_pk_bf16_f32 v65, v104, v105\n\t"
"v_cvt_pk_bf16_f32 v85, v114, v115\n\t"
"v_cvt_pk_bf16_f32 v69, v106, v107\n\t"
"v_cvt_pk_bf16_f32 v75, v108, v109\n\t"
"v_cvt_pk_bf16_f32 v77, v110, v111\n\t"
"v_cvt_pk_bf16_f32 v83, v112, v113\n\t"
"s_waitcnt lgkmcnt(6)\n\t"
"v_pk_fma_f32 v[56:57], v[52:53], s[58:59], v[36:37] op_sel:[0,0,0] op_sel_hi:[1,0,1]\n\t"
"v_pk_fma_f32 v[58:59], v[52:53], s[58:59], v[36:37] op_sel:[0,1,0] op_sel_hi:[1,1,1]\n\t"
"v_pk_mul_f32 v[104:105], v[36:37], s[38:39] op_sel:[0,0] op_sel_hi:[1,0]\n\t"
"v_pk_mul_f32 v[114:115], v[52:53], s[38:39] op_sel:[0,1] op_sel_hi:[1,1]\n\t"
"v_pk_fma_f32 v[106:107], v[44:45], s[56:57], v[56:57] op_sel:[0,0,0] op_sel_hi:[1,0,1]\n\t"
"v_pk_fma_f32 v[108:109], v[44:45], s[56:57], v[56:57] op_sel:[0,0,0] op_sel_hi:[1,0,1] neg_lo:[1,0,0] neg_hi:[1,0,0]\n\t"
"v_pk_fma_f32 v[110:111], v[44:45], s[56:57], v[58:59] op_sel:[0,1,0] op_sel_hi:[1,1,1]\n\t"
"v_pk_fma_f32 v[112:113], v[44:45], s[56:57], v[58:59] op_sel:[0,1,0] op_sel_hi:[1,1,1] neg_lo:[1,0,0] neg_hi:[1,0,0]\n\t"
"v_pk_mul_f32 v[106:107], v[106:107], s[62:63] op_sel:[0,0] op_sel_hi:[1,0]\n\t"
"v_pk_mul_f32 v[108:109], v[108:109], s[62:63] op_sel:[0,1] op_sel_hi:[1,1]\n\t"
"v_pk_mul_f32 v[110:111], v[110:111], s[64:65] op_sel:[0,0] op_sel_hi:[1,0]\n\t"
"v_pk_mul_f32 v[112:113], v[112:113], s[64:65] op_sel:[0,1] op_sel_hi:[1,1]\n\t"
"v_cvt_pk_bf16_f32 v66, v104, v105\n\t"
"v_cvt_pk_bf16_f32 v86, v114, v115\n\t"
"v_cvt_pk_bf16_f32 v70, v106, v107\n\t"
"v_cvt_pk_bf16_f32 v72, v108, v109\n\t"
"v_cvt_pk_bf16_f32 v78, v110, v111\n\t"
"v_cvt_pk_bf16_f32 v80, v112, v113\n\t"
"s_waitcnt lgkmcnt(0)\n\t"
"v_pk_fma_f32 v[56:57], v[54:55], s[58:59], v[38:39] op_sel:[0,0,0] op_sel_hi:[1,0,1]\n\t"
"v_pk_fma_f32 v[58:59], v[54:55], s[58:59], v[38:39] op_sel:[0,1,0] op_sel_hi:[1,1,1]\n\t"
"v_pk_mul_f32 v[104:105], v[38:39], s[38:39] op_sel:[0,0] op_sel_hi:[1,0]\n\t"
"v_pk_mul_f32 v[114:115], v[54:55], s[38:39] op_sel:[0,1] op_sel_hi:[1,1]\n\t"
"v_pk_fma_f32 v[106:107], v[46:47], s[56:57], v[56:57] op_sel:[0,0,0] op_sel_hi:[1,0,1]\n\t"
"v_pk_fma_f32 v[108:109], v[46:47], s[56:57], v[56:57] op_sel:[0,0,0] op_sel_hi:[1,0,1] neg_lo:[1,0,0] neg_hi:[1,0,0]\n\t"
"v_pk_fma_f32 v[110:111], v[46:47], s[56:57], v[58:59] op_sel:[0,1,0] op_sel_hi:[1,1,1]\n\t"
"v_pk_fma_f32 v[112:113], v[46:47], s[56:57], v[58:59] op_sel:[0,1,0] op_sel_hi:[1,1,1] neg_lo:[1,0,0] neg_hi:[1,0,0]\n\t"
"v_pk_mul_f32 v[106:107], v[106:107], s[62:63] op_sel:[0,0] op_sel_hi:[1,0]\n\t"
"v_pk_mul_f32 v[108:109], v[108:109], s[62:63] op_sel:[0,1] op_sel_hi:[1,1]\n\t"
"v_pk_mul_f32 v[110:111], v[110:111], s[64:65] op_sel:[0,0] op_sel_hi:[1,0]\n\t"
"v_pk_mul_f32 v[112:113], v[112:113], s[64:65] op_sel:[0,1] op_sel_hi:[1,1]\n\t"
"v_cvt_pk_bf16_f32 v67, v104, v105\n\t"
"v_cvt_pk_bf16_f32 v87, v114, v115\n\t"
"v_cvt_pk_bf16_f32 v71, v106, v107\n\t"
"v_cvt_pk_bf16_f32 v73, v108, v109\n\t"
"v_cvt_pk_bf16_f32 v79, v110, v111\n\t"
"v_cvt_pk_bf16_f32 v81, v112, v113\n\t"
"s_add_u32 s60, s75, 0\n\t"
"buffer_store_dwordx4 v[64:67], v97, s[8:11], s60 offen sc1\n\t"
"s_add_u32 s60, s75, 184320\n\t"
"buffer_store_dwordx4 v[84:87], v97, s[8:11], s60 offen sc1\n\t"
"s_add_u32 s60, s75, 36864\n\t"
"buffer_store_dwordx4 v[68:71], v97, s[8:11], s60 offen sc1\n\t"
"s_add_u32 s60, s75, 73728\n\t"
"buffer_store_dwordx4 v[72:75], v97, s[8:11], s60 offen sc1\n\t"
"s_add_u32 s60, s75, 110592\n\t"
"buffer_store_dwordx4 v[76:79], v97, s[8:11], s60 offen sc1\n\t"
"s_add_u32 s60, s75, 147456\n\t"
"buffer_store_dwordx4 v[80:83], v97, s[8:11], s60 offen sc1\n\t"
"s_waitcnt vmcnt(0)\n\t"
"s_add_u32 s69, s69, 1\n\t"
"s_cmp_lt_u32 s69, 216\n\t"
"s_cbranch_scc1 Lfb1_%=\n\t"
"s_mov_b32 s69, %2\n\t"
"s_mov_b64 vcc, 0\n\t"
"ds_read_b128 v[32:35], v255 offset:0\n\t"
"ds_read_b128 v[36:39], v255 offset:3072\n\t"
"ds_read_b128 v[40:43], v255 offset:6144\n\t"
"ds_read_b128 v[44:47], v255 offset:9216\n\t"
"ds_read_b128 v[48:51], v255 offset:12288\n\t"
"ds_read_b128 v[52:55], v255 offset:15360\n\t"
"ds_read_b128 v[56:59], v255 offset:18432\n\t"
"ds_read_b128 v[60:63], v255 offset:21504\n\t"
"ds_read_b128 v[64:67], v255 offset:24576\n\t"
"ds_read_b128 v[68:71], v255 offset:27648\n\t"
"ds_read_b128 v[72:75], v255 offset:30720\n\t"
"ds_read_b128 v[76:79], v255 offset:33792\n\t"
"ds_read_b128 v[80:83], v255 offset:36864\n\t"
"ds_read_b128 v[84:87], v255 offset:39936\n\t"
"ds_read_b128 v[88:91], v255 offset:43008\n\t"
"ds_read_b128 v[92:95], v255 offset:46080\n\t"
"ds_read_b128 v[96:99], v255 offset:49152\n\t"
"ds_read_b128 v[100:103], v255 offset:52224\n\t"
"ds_read_b128 v[104:107], v255 offset:55296\n\t"
"ds_read_b128 v[108:111], v255 offset:58368\n\t"
"ds_read_b128 v[112:115], v255 offset:61440\n\t"
"ds_read_b128 v[116:119], v255 offset:64512\n\t"
"s_waitcnt lgkmcnt(0)\n\t"
"s_branch Lgo_%=\n\t"
".p2align 12\n\t"
"Lpro_%=:\n\t"
"s_mul_i32 s25, s73, 19968\n\t"
"s_add_u32 s25, s25, 61440\n\t"
"s_bfe_u32 s41, %2, 0x40003\n\t"
"s_cmp_eq_u32 s41, 0\n\t"
"s_cselect_b32 s40, -1, 0xbfffffff\n\t"
"s_cmp_eq_u32 s41, 15\n\t"
"s_cselect_b32 s41, -1, 0xdfffffff\n\t"
"s_and_b32 s40, s40, s41\n\t"
"s_sub_u32 s42, s21, 384\n\t"
"s_mul_i32 s41, s73, 589824\n\t"
"s_add_u32 s42, s42, s41\n\t"
"s_and_b32 s41, s40, 536883584\n\t"
"s_and_b32 s40, s40, 2.0\n\t"
"v_bfe_u32 v26, v205, 2, 3\n\t"
"v_add_u32 v27, -1, v26\n\t"
"v_cmp_gt_u32 vcc, 6, v27\n\t"
"v_lshrrev_b32 v27, 1, v27\n\t"
"v_mul_u32_u24 v27, 196608, v27\n\t"
"v_and_b32 v20, 0x70, v28\n\t"
"v_add3_u32 v27, v27, v20, s42\n\t"
"v_bfe_i32 v20, v205, 2, 1\n\t"
"v_mov_b32 v21, s40\n\t"
"v_bfi_b32 v20, v20, s41, v21\n\t"
"v_add_u32 v27, v27, v20\n\t"
"v_bfrev_b32 v20, 8\n\t"
"v_cndmask_b32 v254, v20, v27, vcc\n\t"
"v_bfe_u32 v26, v205, 2, 3\n\t"
"v_bfe_u32 v27, v205, 5, 1\n\t"
"v_mul_u32_u24 v27, 3072, v27\n\t"
"v_lshl_add_u32 v27, v26, 7, v27\n\t"
"v_and_b32 v26, 6, v26\n\t"
"v_lshrrev_b32 v20, 4, v205\n\t"
"v_and_b32 v20, 4, v20\n\t"
"v_and_or_b32 v20, v205, 3, v20\n\t"
"v_xor_b32 v20, v20, v26\n\t"
"v_lshl_add_u32 v20, v20, 4, v27\n\t"
"v_lshrrev_b32 v21, 7, v205\n\t"
"v_lshl_add_u32 v20, v21, 10, v20\n\t"
"v_lshrrev_b32 v21, 5, v205\n\t"
"v_and_b32 v21, 6, v21\n\t"
"v_bfe_u32 v22, v205, 1, 1\n\t"
"v_or_b32 v21, v21, v22\n\t"
"v_xor_b32 v21, v21, v26\n\t"
"v_lshl_add_u32 v21, v21, 4, v27\n\t"
"v_and_b32 v22, 1, v205\n\t"
"v_lshl_add_u32 v21, v22, 3, v21\n\t"
"v_add_u32 v22, 142848, v20\n\t"
"v_add_u32 v23, 142848, v21\n\t"
"v_add_u32 v24, 82944, v20\n\t"
"v_add_u32 v25, 82944, v21\n\t"
"s_and_b32 s17, s17, 0xffff\n\t"
"s_mov_b32 s18, 100663296\n\t"
"s_mov_b32 s19, 0x20000\n\t"
"s_lshr_b32 s41, s73, 1\n\t"
"s_add_u32 s42, s21, 196608\n\t"
"s_mul_i32 s43, s41, 384\n\t"
"s_add_u32 s22, s42, s43\n\t"
"s_lshl1_add_u32 s40, s43, s22\n\t"
"s_mul_i32 s43, s41, 196608\n\t"
"s_add_u32 s23, s42, s43\n\t"
"s_mov_b32 s44, 0x0fff0fff\n\t"
"s_mov_b32 s45, 0x0fff0fff\n\t"
"s_mul_i32 s26, s41, 19968\n\t"
"s_add_u32 s26, s26, 141312\n\t"
"s_sub_u32 s43, s41, s73\n\t"
"s_add_u32 s43, s43, 4\n\t"
"s_mul_i32 s27, s43, 589824\n\t"
"s_bitcmp1_b32 s73, 0\n\t"
"s_mov_b32 s36, 0x0fff0000\n\t"
"s_cselect_b32 s36, 0xfff0, s36\n\t"
"s_mov_b32 s37, s36\n\t"
"s_add_u32 m0, s59, 148992\n\t"
"s_add_u32 s57, s58, 147456\n\t"
"buffer_load_dwordx4 v29, s[52:55], s57 offen offset:0 lds\n\t"
"buffer_load_dwordx4 v29, s[52:55], s57 offen offset:3072 lds\n\t"
"s_add_u32 m0, s59, 155136\n\t"
"s_add_u32 s57, s58, 153600\n\t"
"buffer_load_dwordx4 v29, s[52:55], s57 offen offset:0 lds\n\t"
"buffer_load_dwordx4 v29, s[52:55], s57 offen offset:3072 lds\n\t"
"s_add_u32 m0, s59, 0\n\t"
"s_add_u32 s57, s58, 368640\n\t"
"buffer_load_dwordx4 v29, s[52:55], s57 offen offset:0 lds\n\t"
"buffer_load_dwordx4 v29, s[52:55], s57 offen offset:3072 lds\n\t"
"s_add_u32 m0, s59, 6144\n\t"
"s_add_u32 s57, s58, 374784\n\t"
"buffer_load_dwordx4 v29, s[52:55], s57 offen offset:0 lds\n\t"
"buffer_load_dwordx4 v29, s[52:55], s57 offen offset:3072 lds\n\t"
"s_add_u32 m0, s59, 12288\n\t"
"s_add_u32 s57, s58, 589824\n\t"
"buffer_load_dwordx4 v29, s[52:55], s57 offen offset:0 lds\n\t"
"buffer_load_dwordx4 v29, s[52:55], s57 offen offset:3072 lds\n\t"
"s_add_u32 m0, s59, 18432\n\t"
"s_add_u32 s57, s58, 595968\n\t"
"buffer_load_dwordx4 v29, s[52:55], s57 offen offset:0 lds\n\t"
"buffer_load_dwordx4 v29, s[52:55], s57 offen offset:3072 lds\n\t"
"s_add_u32 m0, s25, 0\n\t"
"s_nop 0\n\t"
"buffer_load_dwordx4 v254, s[12:15], 0 offen nt lds\n\t"
"s_cmp_eq_u32 s73, 3\n\t"
"s_cselect_b32 s62, -1, 1\n\t"
"s_mul_i32 s63, s62, 59904\n\t"
"s_mul_i32 s62, s62, 1769472\n\t"
"v_add_u32 v19, s62, v254\n\t"
"s_add_u32 m0, s25, s63\n\t"
"s_nop 0\n\t"
"buffer_load_dwordx4 v19, s[12:15], 0 offen nt lds\n\t"
"s_add_u32 m0, s24, 59904\n\t"
"s_add_u32 s28, s21, 1769472\n\t"
"buffer_load_dwordx4 v28, s[12:15], s28 offen offset:0 nt lds\n\t"
"s_add_u32 m0, s24, 66048\n\t"
"s_add_u32 s28, s21, 1966080\n\t"
"buffer_load_dwordx4 v28, s[12:15], s28 offen offset:0 nt lds\n\t"
"s_add_u32 m0, s24, 72192\n\t"
"s_add_u32 s28, s21, 2162688\n\t"
"buffer_load_dwordx4 v28, s[12:15], s28 offen offset:0 nt lds\n\t"
"s_mul_i32 s60, s59, 6\n\t"
"s_mul_i32 s61, s59, 192\n\t"
"s_add_u32 s61, s61, s21\n\t"
"s_mul_i32 s62, s73, 3072\n\t"
"v_subrev_u32 v30, s62, v28\n\t"
"s_add_u32 m0, s60, 102912\n\t"
"s_add_u32 s28, s61, 1769472\n\t"
"buffer_load_dwordx4 v30, s[12:15], s28 offen nt lds\n\t"
"s_add_u32 m0, s24, 79872\n\t"
"s_add_u32 s28, s21, 2359296\n\t"
"buffer_load_dwordx4 v28, s[12:15], s28 offen offset:0 nt lds\n\t"
"s_add_u32 m0, s24, 86016\n\t"
"s_add_u32 s28, s21, 2555904\n\t"
"buffer_load_dwordx4 v28, s[12:15], s28 offen offset:0 nt lds\n\t"
"s_add_u32 m0, s24, 92160\n\t"
"s_add_u32 s28, s21, 2752512\n\t"
"buffer_load_dwordx4 v28, s[12:15], s28 offen offset:0 nt lds\n\t"
"s_add_u32 m0, s60, 122880\n\t"
"s_add_u32 s28, s61, 2359296\n\t"
"buffer_load_dwordx4 v30, s[12:15], s28 offen nt lds\n\t"
"s_add_u32 m0, s24, 19968\n\t"
"s_add_u32 s28, s21, 589824\n\t"
"buffer_load_dwordx4 v28, s[12:15], s28 offen offset:0 nt lds\n\t"
"s_add_u32 m0, s24, 26112\n\t"
"s_add_u32 s28, s21, 786432\n\t"
"buffer_load_dwordx4 v28, s[12:15], s28 offen offset:0 nt lds\n\t"
"s_add_u32 m0, s24, 32256\n\t"
"s_add_u32 s28, s21, 983040\n\t"
"buffer_load_dwordx4 v28, s[12:15], s28 offen offset:0 nt lds\n\t"
"s_add_u32 m0, s60, 62976\n\t"
"s_add_u32 s28, s61, 589824\n\t"
"buffer_load_dwordx4 v30, s[12:15], s28 offen nt lds\n\t"
"s_add_u32 m0, s24, 0\n\t"
"s_add_u32 s28, s21, 0\n\t"
"buffer_load_dwordx4 v28, s[12:15], s28 offen offset:0 nt lds\n\t"
"s_add_u32 m0, s24, 6144\n\t"
"s_add_u32 s28, s21, 196608\n\t"
"buffer_load_dwordx4 v28, s[12:15], s28 offen offset:0 nt lds\n\t"
"s_add_u32 m0, s24, 12288\n\t"
"s_add_u32 s28, s21, 393216\n\t"
"buffer_load_dwordx4 v28, s[12:15], s28 offen offset:0 nt lds\n\t"
"s_add_u32 m0, s60, 43008\n\t"
"s_add_u32 s28, s61, 0\n\t"
"buffer_load_dwordx4 v30, s[12:15], s28 offen nt lds\n\t"
"s_waitcnt vmcnt(42)\n\t"
"Ljoin_%=:\n\t"
"s_mov_b32 s41, 0xc0423f90\n\t"
"s_movk_i32 s43, 0x101\n\t"
"v_mov_b32 v6, 0x01000100\n\t"
"v_mov_b32 v7, 0xbebd0bd1\n\t"
"v_lshrrev_b32 v240, 16, v244\n\t"
"v_lshrrev_b32 v242, 16, v245\n\t"
"v_xor_b32 v241, 32, v240\n\t"
"v_and_b32 v244, 0xffff, v244\n\t"
"v_and_b32 v245, 0xffff, v245\n\t"
"v_cmp_le_u32 vcc, 9216, v242\n\t"
"v_mov_b32 v243, 0x3ea80a81\n\t"
"v_cndmask_b32 v243, v243, v7, vcc\n\t"
"v_and_b32 v8, 0xf000f, v247\n\t"
"v_mad_u32_u24 v8, v8, s43, v6\n\t"
"v_perm_b32 v196, s41, 0, v8\n\t"
"v_lshrrev_b32 v9, 4, v247\n\t"
"v_and_b32 v9, 0xf000f, v9\n\t"
"v_mad_u32_u24 v9, v9, s43, v6\n\t"
"v_perm_b32 v197, s41, 0, v9\n\t"
"v_lshrrev_b32 v10, 8, v247\n\t"
"v_and_b32 v10, 0xf000f, v10\n\t"
"v_mad_u32_u24 v10, v10, s43, v6\n\t"
"v_perm_b32 v198, s41, 0, v10\n\t"
"v_lshrrev_b32 v11, 12, v247\n\t"
"v_and_b32 v11, 0xf000f, v11\n\t"
"v_mad_u32_u24 v11, v11, s43, v6\n\t"
"v_perm_b32 v199, s41, 0, v11\n\t"
"v_mov_b32 v247, v28\n\t"
"s_mov_b64 vcc, -1\n\t"
"s_bitcmp1_b32 s73, 1\n\t"
"s_cbranch_scc1 Lvh0_%=\n\t"
"s_bitcmp1_b32 s73, 0\n\t"
"s_cbranch_scc1 Lpg2_%=\n\t"
"s_branch Lpg1_%=\n\t"
"Lvh0_%=:\n\t"
"s_bitcmp1_b32 s73, 0\n\t"
"s_cbranch_scc1 Lpg4_%=\n\t"
"s_branch Lpg3_%=\n\t"
"Lvr0_%=:\n\t"
"s_mov_b64 vcc, 0\n\t"
"v_add_u32 v240, 59904, v240\n\t"
"v_add_u32 v241, 59904, v241\n\t"
"s_cmp_lt_u32 s69, 216\n\t"
"s_cbranch_scc0 Lna1_%=\n\t"
"s_waitcnt vmcnt(0)\n\t"
"s_add_u32 s71, s71, 1\n\t"
"v_mov_b32 v31, s71\n\t"
"s_mov_b64 exec, 1\n\t"
"buffer_store_dword v31, off, s[8:11], s70 sc1\n\t"
"s_mov_b64 exec, -1\n\t"
"Lna1_%=:\n\t"
"s_cmp_eq_u32 s73, 0\n\t"
"s_cbranch_scc1 Lsk0_%=\n\t"
"s_lshr_b32 s56, %2, 3\n\t"
"s_bitcmp1_b32 0xaaaaaaaa, s56\n\t"
"s_cbranch_scc1 Lkc2_%=\n\t"
"s_waitcnt vmcnt(8)\n\t"
"s_branch Lke2_%=\n\t"
"Lkc2_%=:\n\t"
"s_waitcnt vmcnt(32)\n\t"
"Lke2_%=:\n\t"
"Lsk0_%=:\n\t"
"s_barrier\n\t"
"ds_read_b64_tr_b16 v[120:121], v240 offset:0\n\t"
"ds_read_b64_tr_b16 v[122:123], v240 offset:1536\n\t"
"ds_read_b64_tr_b16 v[124:125], v241 offset:0\n\t"
"ds_read_b64_tr_b16 v[126:127], v241 offset:1536\n\t"
"ds_read_b64_tr_b16 v[128:129], v240 offset:19968\n\t"
"ds_read_b64_tr_b16 v[130:131], v240 offset:21504\n\t"
"ds_read_b64_tr_b16 v[132:133], v241 offset:19968\n\t"
"ds_read_b64_tr_b16 v[134:135], v241 offset:21504\n\t"
"s_waitcnt lgkmcnt(6)\n\t"
"v_mfma_f32_16x16x32_bf16 v[120:123], v[120:123], v[196:199], 0\n\t"
"s_waitcnt lgkmcnt(4)\n\t"
"v_mfma_f32_16x16x32_bf16 v[124:127], v[124:127], v[196:199], 0\n\t"
"s_waitcnt lgkmcnt(2)\n\t"
"v_mfma_f32_16x16x32_bf16 v[128:131], v[128:131], v[196:199], 0\n\t"
"s_waitcnt lgkmcnt(0)\n\t"
"v_mfma_f32_16x16x32_bf16 v[132:135], v[132:135], v[196:199], 0\n\t"
"v_pk_mul_f32 v[120:121], v[120:121], v[242:243] op_sel:[0,1] op_sel_hi:[1,1]\n\t"
"v_pk_mul_f32 v[122:123], v[122:123], v[242:243] op_sel:[0,1] op_sel_hi:[1,1]\n\t"
"v_cvt_pk_bf16_f32 v120, v120, v121\n\t"
"v_cvt_pk_bf16_f32 v121, v122, v123\n\t"
"v_pk_mul_f32 v[124:125], v[124:125], v[242:243] op_sel:[0,1] op_sel_hi:[1,1]\n\t"
"v_pk_mul_f32 v[126:127], v[126:127], v[242:243] op_sel:[0,1] op_sel_hi:[1,1]\n\t"
"v_cvt_pk_bf16_f32 v122, v124, v125\n\t"
"v_cvt_pk_bf16_f32 v123, v126, v127\n\t"
"v_pk_mul_f32 v[128:129], v[128:129], v[242:243] op_sel:[0,1] op_sel_hi:[1,1]\n\t"
"v_pk_mul_f32 v[130:131], v[130:131], v[242:243] op_sel:[0,1] op_sel_hi:[1,1]\n\t"
"v_cvt_pk_bf16_f32 v128, v128, v129\n\t"
"v_cvt_pk_bf16_f32 v129, v130, v131\n\t"
"v_pk_mul_f32 v[132:133], v[132:133], v[242:243] op_sel:[0,1] op_sel_hi:[1,1]\n\t"
"v_pk_mul_f32 v[134:135], v[134:135], v[242:243] op_sel:[0,1] op_sel_hi:[1,1]\n\t"
"v_cvt_pk_bf16_f32 v130, v132, v133\n\t"
"v_cvt_pk_bf16_f32 v131, v134, v135\n\t"
"s_cmp_eq_u32 s73, 0\n\t"
"s_cbranch_scc1 Lsk1_%=\n\t"
"s_lshr_b32 s56, %2, 3\n\t"
"s_bitcmp1_b32 0xaaaaaaaa, s56\n\t"
"s_cbranch_scc1 Lkc3_%=\n\t"
"s_branch Lke3_%=\n\t"
"Lkc3_%=:\n\t"
"s_waitcnt vmcnt(8)\n\t"
"Lke3_%=:\n\t"
"Lsk1_%=:\n\t"
"s_barrier\n\t"
"ds_read_b64_tr_b16 a[0:1], v20 offset:24576\n\t"
"ds_read_b64_tr_b16 a[2:3], v20 offset:30720\n\t"
"ds_read_b64_tr_b16 a[24:25], v20 offset:24584\n\t"
"ds_read_b64_tr_b16 a[26:27], v20 offset:30728\n\t"
"ds_read_b64_tr_b16 a[48:49], v21 offset:26624\n\t"
"ds_read_b64_tr_b16 a[50:51], v21 offset:32768\n\t"
"ds_read_b64_tr_b16 a[8:9], v20 offset:36864\n\t"
"ds_read_b64_tr_b16 a[10:11], v24 offset:0\n\t"
"ds_read_b64_tr_b16 a[32:33], v20 offset:36872\n\t"
"ds_read_b64_tr_b16 a[34:35], v24 offset:8\n\t"
"ds_read_b64_tr_b16 a[56:57], v21 offset:38912\n\t"
"ds_read_b64_tr_b16 a[58:59], v25 offset:2048\n\t"
"ds_read_b64_tr_b16 a[16:17], v24 offset:6144\n\t"
"ds_read_b64_tr_b16 a[18:19], v22 offset:0\n\t"
"ds_read_b64_tr_b16 a[40:41], v24 offset:6152\n\t"
"ds_read_b64_tr_b16 a[42:43], v22 offset:8\n\t"
"ds_read_b64_tr_b16 a[64:65], v25 offset:8192\n\t"
"ds_read_b64_tr_b16 a[66:67], v23 offset:2048\n\t"
"ds_read_b64_tr_b16 a[72:73], v22 offset:6144\n\t"
"ds_read_b64_tr_b16 a[74:75], v22 offset:12288\n\t"
"ds_read_b64_tr_b16 a[96:97], v22 offset:6152\n\t"
"ds_read_b64_tr_b16 a[98:99], v22 offset:12296\n\t"
"ds_read_b64_tr_b16 a[120:121], v23 offset:8192\n\t"
"ds_read_b64_tr_b16 a[122:123], v23 offset:14336\n\t"
"s_waitcnt lgkmcnt(0)\n\t"
"s_barrier\n\t"
"s_cmp_eq_u32 s73, 0\n\t"
"s_cbranch_scc1 Lsk2_%=\n\t"
"s_add_u32 m0, s24, 39936\n\t"
"s_add_u32 s28, s21, 1179648\n\t"
"buffer_load_dwordx4 v28, s[12:15], s28 offen offset:0 nt lds\n\t"
"s_add_u32 m0, s24, 46080\n\t"
"s_add_u32 s28, s21, 1376256\n\t"
"buffer_load_dwordx4 v28, s[12:15], s28 offen offset:0 nt lds\n\t"
"s_add_u32 m0, s24, 52224\n\t"
"s_add_u32 s28, s21, 1572864\n\t"
"buffer_load_dwordx4 v28, s[12:15], s28 offen offset:0 nt lds\n\t"
"s_add_u32 m0, s60, 82944\n\t"
"s_add_u32 s28, s61, 1179648\n\t"
"buffer_load_dwordx4 v30, s[12:15], s28 offen nt lds\n\t"
"Lsk2_%=:\n\t"
"ds_read_b64_tr_b16 a[80:81], v20 offset:0\n\t"
"ds_read_b64_tr_b16 a[82:83], v20 offset:6144\n\t"
"ds_read_b64_tr_b16 a[104:105], v20 offset:8\n\t"
"ds_read_b64_tr_b16 a[106:107], v20 offset:6152\n\t"
"ds_read_b64_tr_b16 a[128:129], v21 offset:2048\n\t"
"ds_read_b64_tr_b16 a[130:131], v21 offset:8192\n\t"
"ds_read_b64_tr_b16 a[88:89], v20 offset:12288\n\t"
"ds_read_b64_tr_b16 a[90:91], v20 offset:18432\n\t"
"ds_read_b64_tr_b16 a[112:113], v20 offset:12296\n\t"
"ds_read_b64_tr_b16 a[114:115], v20 offset:18440\n\t"
"ds_read_b64_tr_b16 a[136:137], v21 offset:14336\n\t"
"ds_read_b64_tr_b16 a[138:139], v21 offset:20480\n\t"
"ds_write_b128 v242, v[120:123] offset:29184\n\t"
"ds_write_b128 v242, v[128:131] offset:30720\n\t"
"s_waitcnt lgkmcnt(0)\n\t"
"v_subrev_u32 v240, 59904, v240\n\t"
"v_subrev_u32 v241, 59904, v241\n\t"
"s_cmp_eq_u32 s73, 0\n\t"
"s_cbranch_scc1 Lsk3_%=\n\t"
"s_waitcnt vmcnt(4)\n\t"
"Lsk3_%=:\n\t"
"s_barrier\n\t"
"ds_read_b64_tr_b16 v[136:137], v240 offset:19968\n\t"
"ds_read_b64_tr_b16 v[138:139], v240 offset:21504\n\t"
"ds_read_b64_tr_b16 v[140:141], v241 offset:19968\n\t"
"ds_read_b64_tr_b16 v[142:143], v241 offset:21504\n\t"
"ds_read_b64_tr_b16 v[144:145], v240 offset:0\n\t"
"ds_read_b64_tr_b16 v[146:147], v240 offset:1536\n\t"
"ds_read_b64_tr_b16 v[148:149], v241 offset:0\n\t"
"ds_read_b64_tr_b16 v[150:151], v241 offset:1536\n\t"
"s_cmp_eq_u32 s73, 0\n\t"
"s_cbranch_scc1 Lsk4_%=\n\t"
"s_add_u32 m0, s24, 99840\n\t"
"s_add_u32 s28, s21, 2949120\n\t"
"buffer_load_dwordx4 v28, s[12:15], s28 offen offset:0 nt lds\n\t"
"s_add_u32 m0, s24, 105984\n\t"
"s_add_u32 s28, s21, 3145728\n\t"
"buffer_load_dwordx4 v28, s[12:15], s28 offen offset:0 nt lds\n\t"
"s_add_u32 m0, s24, 112128\n\t"
"s_add_u32 s28, s21, 3342336\n\t"
"buffer_load_dwordx4 v28, s[12:15], s28 offen offset:0 nt lds\n\t"
"s_add_u32 m0, s60, 142848\n\t"
"s_add_u32 s28, s61, 2949120\n\t"
"buffer_load_dwordx4 v30, s[12:15], s28 offen nt lds\n\t"
"Lsk4_%=:\n\t"
"s_waitcnt lgkmcnt(6)\n\t"
"v_mfma_f32_16x16x32_bf16 v[136:139], v[136:139], v[196:199], 0\n\t"
"s_waitcnt lgkmcnt(4)\n\t"
"v_mfma_f32_16x16x32_bf16 v[140:143], v[140:143], v[196:199], 0\n\t"
"s_waitcnt lgkmcnt(2)\n\t"
"v_mfma_f32_16x16x32_bf16 v[144:147], v[144:147], v[196:199], 0\n\t"
"s_waitcnt lgkmcnt(0)\n\t"
"v_mfma_f32_16x16x32_bf16 v[148:151], v[148:151], v[196:199], 0\n\t"
"v_pk_mul_f32 v[136:137], v[136:137], v[242:243] op_sel:[0,1] op_sel_hi:[1,1]\n\t"
"v_pk_mul_f32 v[138:139], v[138:139], v[242:243] op_sel:[0,1] op_sel_hi:[1,1]\n\t"
"v_cvt_pk_bf16_f32 v136, v136, v137\n\t"
"v_cvt_pk_bf16_f32 v137, v138, v139\n\t"
"v_pk_mul_f32 v[140:141], v[140:141], v[242:243] op_sel:[0,1] op_sel_hi:[1,1]\n\t"
"v_pk_mul_f32 v[142:143], v[142:143], v[242:243] op_sel:[0,1] op_sel_hi:[1,1]\n\t"
"v_cvt_pk_bf16_f32 v138, v140, v141\n\t"
"v_cvt_pk_bf16_f32 v139, v142, v143\n\t"
"ds_write_b128 v242, v[136:139] offset:26112\n\t"
"v_pk_mul_f32 v[144:145], v[144:145], v[242:243] op_sel:[0,1] op_sel_hi:[1,1]\n\t"
"v_pk_mul_f32 v[146:147], v[146:147], v[242:243] op_sel:[0,1] op_sel_hi:[1,1]\n\t"
"v_cvt_pk_bf16_f32 v144, v144, v145\n\t"
"v_cvt_pk_bf16_f32 v145, v146, v147\n\t"
"v_pk_mul_f32 v[148:149], v[148:149], v[242:243] op_sel:[0,1] op_sel_hi:[1,1]\n\t"
"v_pk_mul_f32 v[150:151], v[150:151], v[242:243] op_sel:[0,1] op_sel_hi:[1,1]\n\t"
"v_cvt_pk_bf16_f32 v146, v148, v149\n\t"
"v_cvt_pk_bf16_f32 v147, v150, v151\n\t"
"ds_write_b128 v242, v[144:147] offset:24576\n\t"
"s_mov_b64 vcc, 0\n\t"
"s_waitcnt lgkmcnt(0)\n\t"
"s_barrier\n\t"
"ds_read_b128 a[168:171], v244 offset:26112\n\t"
"ds_read_b128 a[172:175], v244 offset:26624\n\t"
"ds_read_b128 a[176:179], v244 offset:27136\n\t"
"ds_read_b128 a[180:183], v244 offset:35328\n\t"
"ds_read_b128 a[184:187], v244 offset:35840\n\t"
"ds_read_b128 a[188:191], v244 offset:36352\n\t"
"ds_read_b128 a[192:195], v244 offset:25600\n\t"
"s_waitcnt lgkmcnt(4)\n\t"
"v_mfma_f32_16x16x32_bf16 v[14:17], a[0:3], a[168:171], 0\n\t"
"ds_read_b64_tr_b16 v[224:225], v240 offset:512\n\t"
"v_mfma_f32_16x16x32_bf16 v[14:17], a[8:11], a[172:175], v[14:17]\n\t"
"ds_read_b64_tr_b16 v[226:227], v240 offset:2048\n\t"
"v_mfma_f32_16x16x32_bf16 v[14:17], a[16:19], a[176:179], v[14:17]\n\t"
"ds_read_b64_tr_b16 v[228:229], v241 offset:512\n\t"
"v_mfma_f32_16x16x32_bf16 v[46:49], a[24:27], a[168:171], 0\n\t"
"ds_read_b64_tr_b16 v[230:231], v241 offset:2048\n\t"
"v_mfma_f32_16x16x32_bf16 v[46:49], a[32:35], a[172:175], v[46:49]\n\t"
"v_mfma_f32_16x16x32_bf16 v[46:49], a[40:43], a[176:179], v[46:49]\n\t"
"v_mfma_f32_16x16x32_bf16 v[78:81], a[48:51], a[168:171], 0\n\t"
"s_waitcnt lgkmcnt(5)\n\t"
"v_mfma_f32_16x16x32_bf16 v[78:81], a[56:59], a[172:175], v[78:81]\n\t"
"ds_read_b64_tr_b16 v[232:233], v240 offset:20480\n\t"
"v_mfma_f32_16x16x32_bf16 v[78:81], a[64:67], a[176:179], v[78:81]\n\t"
"ds_read_b64_tr_b16 v[234:235], v240 offset:22016\n\t"
"v_mfma_f32_16x16x32_bf16 v[112:115], a[72:75], a[180:183], 0\n\t"
"s_waitcnt lgkmcnt(4)\n\t"
"ds_read_b64_tr_b16 v[236:237], v241 offset:20480\n\t"
"v_mfma_f32_16x16x32_bf16 v[112:115], a[80:83], a[184:187], v[112:115]\n\t"
"s_waitcnt lgkmcnt(3)\n\t"
"ds_read_b64_tr_b16 v[238:239], v241 offset:22016\n\t"
"v_mfma_f32_16x16x32_bf16 v[112:115], a[88:91], a[188:191], v[112:115]\n\t"
"ds_read_b128 a[196:199], v244 offset:34816\n\t"
"v_mfma_f32_16x16x32_bf16 v[224:227], v[224:227], v[196:199], 0\n\t"
"v_mfma_f32_16x16x32_bf16 v[228:231], v[228:231], v[196:199], 0\n\t"
"v_mfma_f32_16x16x32_bf16 v[144:147], a[96:99], a[180:183], 0\n\t"
"v_mfma_f32_16x16x32_bf16 v[144:147], a[104:107], a[184:187], v[144:147]\n\t"
"v_mfma_f32_16x16x32_bf16 v[144:147], a[112:115], a[188:191], v[144:147]\n\t"
"v_mfma_f32_16x16x32_bf16 v[176:179], a[120:123], a[180:183], 0\n\t"
"v_cvt_pk_bf16_f32 v224, v224, v225\n\t"
"v_cvt_pk_bf16_f32 v225, v226, v227\n\t"
"v_mfma_f32_16x16x32_bf16 v[176:179], a[128:131], a[184:187], v[176:179]\n\t"
"ds_read_b128 a[176:179], v244 offset:25088\n\t"
"v_cvt_pk_bf16_f32 v226, v228, v229\n\t"
"v_mfma_f32_16x16x32_bf16 v[176:179], a[136:139], a[188:191], v[176:179]\n\t"
"v_cvt_pk_bf16_f32 v227, v230, v231\n\t"
"v_mfma_f32_16x16x32_bf16 v[10:13], a[0:3], a[192:195], 0\n\t"
"s_waitcnt lgkmcnt(4)\n\t"
"v_mfma_f32_16x16x32_bf16 v[10:13], a[8:11], a[168:171], v[10:13]\n\t"
"s_waitcnt lgkmcnt(2)\n\t"
"v_mfma_f32_16x16x32_bf16 v[10:13], a[16:19], a[172:175], v[10:13]\n\t"
"v_mfma_f32_16x16x32_bf16 v[232:235], v[232:235], v[196:199], 0\n\t"
"v_mfma_f32_16x16x32_bf16 v[236:239], v[236:239], v[196:199], 0\n\t"
"s_waitcnt vmcnt(4)\n\t"
"s_barrier\n\t"
"v_mfma_f32_16x16x32_bf16 v[42:45], a[24:27], a[192:195], 0\n\t"
"ds_read_b64_tr_b16 v[228:229], v240 offset:39936\n\t"
"v_mfma_f32_16x16x32_bf16 v[42:45], a[32:35], a[168:171], v[42:45]\n\t"
"ds_read_b64_tr_b16 v[230:231], v240 offset:41472\n\t"
"v_mfma_f32_16x16x32_bf16 v[42:45], a[40:43], a[172:175], v[42:45]\n\t"
"ds_write_b128 v242, v[224:227] offset:0\n\t"
"v_mfma_f32_16x16x32_bf16 v[74:77], a[48:51], a[192:195], 0\n\t"
"s_waitcnt lgkmcnt(4)\n\t"
"v_cvt_pk_bf16_f32 v232, v232, v233\n\t"
"v_cvt_pk_bf16_f32 v233, v234, v235\n\t"
"v_mfma_f32_16x16x32_bf16 v[74:77], a[56:59], a[168:171], v[74:77]\n\t"
"ds_read_b64_tr_b16 v[224:225], v241 offset:39936\n\t"
"v_mfma_f32_16x16x32_bf16 v[74:77], a[64:67], a[172:175], v[74:77]\n\t"
"ds_read_b64_tr_b16 v[226:227], v241 offset:41472\n\t"
"v_cvt_pk_bf16_f32 v234, v236, v237\n\t"
"v_mfma_f32_16x16x32_bf16 v[108:111], a[72:75], a[196:199], 0\n\t"
"s_waitcnt lgkmcnt(3)\n\t"
"ds_read_b128 a[188:191], v244 offset:34304\n\t"
"v_cvt_pk_bf16_f32 v235, v238, v239\n\t"
"v_mfma_f32_16x16x32_bf16 v[108:111], a[80:83], a[180:183], v[108:111]\n\t"
"ds_read_b64_tr_b16 v[236:237], v240 offset:40448\n\t"
"v_mfma_f32_16x16x32_bf16 v[108:111], a[88:91], a[184:187], v[108:111]\n\t"
"ds_write_b128 v242, v[232:235] offset:1536\n\t"
"v_mfma_f32_16x16x32_bf16 v[228:231], v[228:231], v[196:199], 0\n\t"
"s_waitcnt lgkmcnt(3)\n\t"
"v_mfma_f32_16x16x32_bf16 v[224:227], v[224:227], v[196:199], 0\n\t"
"ds_read_b64_tr_b16 v[238:239], v240 offset:41984\n\t"
"v_mfma_f32_16x16x32_bf16 v[140:143], a[96:99], a[196:199], 0\n\t"
"ds_read_b64_tr_b16 v[232:233], v241 offset:40448\n\t"
"v_mfma_f32_16x16x32_bf16 v[140:143], a[104:107], a[180:183], v[140:143]\n\t"
"ds_read_b64_tr_b16 v[234:235], v241 offset:41984\n\t"
"v_mfma_f32_16x16x32_bf16 v[140:143], a[112:115], a[184:187], v[140:143]\n\t"
"v_pk_mul_f32 v[228:229], v[228:229], v[242:243] op_sel:[0,1] op_sel_hi:[1,1]\n\t"
"v_pk_mul_f32 v[230:231], v[230:231], v[242:243] op_sel:[0,1] op_sel_hi:[1,1]\n\t"
"v_mfma_f32_16x16x32_bf16 v[172:175], a[120:123], a[196:199], 0\n\t"
"v_cvt_pk_bf16_f32 v228, v228, v229\n\t"
"v_cvt_pk_bf16_f32 v229, v230, v231\n\t"
"v_mfma_f32_16x16x32_bf16 v[172:175], a[128:131], a[180:183], v[172:175]\n\t"
"v_mfma_f32_16x16x32_bf16 v[172:175], a[136:139], a[184:187], v[172:175]\n\t"
"v_pk_mul_f32 v[224:225], v[224:225], v[242:243] op_sel:[0,1] op_sel_hi:[1,1]\n\t"
"v_pk_mul_f32 v[226:227], v[226:227], v[242:243] op_sel:[0,1] op_sel_hi:[1,1]\n\t"
"v_mfma_f32_16x16x32_bf16 v[6:9], a[0:3], a[176:179], 0\n\t"
"v_cvt_pk_bf16_f32 v230, v224, v225\n\t"
"v_cvt_pk_bf16_f32 v231, v226, v227\n\t"
"v_mfma_f32_16x16x32_bf16 v[6:9], a[8:11], a[192:195], v[6:9]\n\t"
"v_add_u32 v240, 59904, v240\n\t"
"v_add_u32 v241, 59904, v241\n\t"
"v_mfma_f32_16x16x32_bf16 v[6:9], a[16:19], a[168:171], v[6:9]\n\t"
"ds_write_b128 v242, v[228:231] offset:27648\n\t"
"v_mfma_f32_16x16x32_bf16 v[38:41], a[24:27], a[176:179], 0\n\t"
"s_waitcnt lgkmcnt(3)\n\t"
"v_mfma_f32_16x16x32_bf16 v[38:41], a[32:35], a[192:195], v[38:41]\n\t"
"s_waitcnt lgkmcnt(1)\n\t"
"ds_read_b64_tr_b16 v[224:225], v240 offset:512\n\t"
"v_mfma_f32_16x16x32_bf16 v[38:41], a[40:43], a[168:171], v[38:41]\n\t"
"ds_read_b64_tr_b16 v[226:227], v240 offset:2048\n\t"
"v_mfma_f32_16x16x32_bf16 v[236:239], v[236:239], v[196:199], 0\n\t"
"ds_read_b64_tr_b16 v[228:229], v241 offset:512\n\t"
"v_mfma_f32_16x16x32_bf16 v[232:235], v[232:235], v[196:199], 0\n\t"
"ds_read_b64_tr_b16 v[230:231], v241 offset:2048\n\t"
"v_mfma_f32_16x16x32_bf16 v[70:73], a[48:51], a[176:179], 0\n\t"
"v_mfma_f32_16x16x32_bf16 v[70:73], a[56:59], a[192:195], v[70:73]\n\t"
"v_mfma_f32_16x16x32_bf16 v[70:73], a[64:67], a[168:171], v[70:73]\n\t"
"v_mfma_f32_16x16x32_bf16 v[104:107], a[72:75], a[188:191], 0\n\t"
"v_cvt_pk_bf16_f32 v236, v236, v237\n\t"
"v_cvt_pk_bf16_f32 v237, v238, v239\n\t"
"v_mfma_f32_16x16x32_bf16 v[104:107], a[80:83], a[196:199], v[104:107]\n\t"
"v_cvt_pk_bf16_f32 v238, v232, v233\n\t"
"v_mfma_f32_16x16x32_bf16 v[104:107], a[88:91], a[180:183], v[104:107]\n\t"
"s_waitcnt lgkmcnt(4)\n\t"
"s_barrier\n\t"
"v_mfma_f32_16x16x32_bf16 v[136:139], a[96:99], a[188:191], 0\n\t"
"ds_read_b128 a[168:171], v244 offset:24576\n\t"
"v_mfma_f32_16x16x32_bf16 v[136:139], a[104:107], a[196:199], v[136:139]\n\t"
"ds_read_b128 a[172:175], v244 offset:33792\n\t"
"v_cvt_pk_bf16_f32 v239, v234, v235\n\t"
"v_mfma_f32_16x16x32_bf16 v[136:139], a[112:115], a[180:183], v[136:139]\n\t"
"ds_write_b128 v242, v[236:239] offset:3072\n\t"
"v_mfma_f32_16x16x32_bf16 v[168:171], a[120:123], a[188:191], 0\n\t"
"s_waitcnt lgkmcnt(5)\n\t"
"v_mfma_f32_16x16x32_bf16 v[168:171], a[128:131], a[196:199], v[168:171]\n\t"
"s_waitcnt lgkmcnt(3)\n\t"
"ds_read_b128 a[184:187], v244 offset:26624\n\t"
"v_mfma_f32_16x16x32_bf16 v[168:171], a[136:139], a[180:183], v[168:171]\n\t"
"s_waitcnt lgkmcnt(3)\n\t"
"ds_read_b128 a[200:203], v244 offset:27136\n\t"
"v_mfma_f32_16x16x32_bf16 v[224:227], v[224:227], v[196:199], 0\n\t"
"ds_read_b128 a[204:207], v244 offset:27648\n\t"
"v_mfma_f32_16x16x32_bf16 v[228:231], v[228:231], v[196:199], 0\n\t"
"v_mfma_f32_16x16x32_bf16 v[2:5], a[0:3], a[168:171], 0\n\t"
"v_mfma_f32_16x16x32_bf16 v[2:5], a[8:11], a[176:179], v[2:5]\n\t"
"v_mfma_f32_16x16x32_bf16 v[2:5], a[16:19], a[192:195], v[2:5]\n\t"
"s_waitcnt vmcnt(0)\n\t"
"s_barrier\n\t"
"v_mfma_f32_16x16x32_bf16 v[34:37], a[24:27], a[168:171], 0\n\t"
"ds_read_b64_tr_b16 v[232:233], v240 offset:39936\n\t"
"v_mfma_f32_16x16x32_bf16 v[34:37], a[32:35], a[176:179], v[34:37]\n\t"
"ds_read_b64_tr_b16 v[234:235], v240 offset:41472\n\t"
"v_cvt_pk_bf16_f32 v224, v224, v225\n\t"
"v_mfma_f32_16x16x32_bf16 v[34:37], a[40:43], a[192:195], v[34:37]\n\t"
"ds_read_b64_tr_b16 v[236:237], v241 offset:39936\n\t"
"v_cvt_pk_bf16_f32 v225, v226, v227\n\t"
"v_mfma_f32_16x16x32_bf16 v[66:69], a[48:51], a[168:171], 0\n\t"
"s_waitcnt lgkmcnt(7)\n\t"
"ds_read_b64_tr_b16 v[238:239], v241 offset:41472\n\t"
"v_cvt_pk_bf16_f32 v226, v228, v229\n\t"
"v_mfma_f32_16x16x32_bf16 v[66:69], a[56:59], a[176:179], v[66:69]\n\t"
"ds_read_b128 a[168:171], v244 offset:35840\n\t"
"v_cvt_pk_bf16_f32 v227, v230, v231\n\t"
"v_mfma_f32_16x16x32_bf16 v[66:69], a[64:67], a[192:195], v[66:69]\n\t"
"s_cmp_lg_u32 s73, 0\n\t"
"s_cbranch_scc1 Lnp_%=\n\t"
"buffer_load_dwordx4 v[248:251], v252, s[8:11], s72 offen sc1\n\t"
"Lnp_%=:\n\t"
"ds_write_b128 v242, v[224:227] offset:4608\n\t"
"v_mfma_f32_16x16x32_bf16 v[100:103], a[72:75], a[172:175], 0\n\t"
"s_waitcnt lgkmcnt(4)\n\t"
"v_mfma_f32_16x16x32_bf16 v[100:103], a[80:83], a[188:191], v[100:103]\n\t"
"ds_read_b128 a[176:179], v244 offset:36352\n\t"
"v_mfma_f32_16x16x32_bf16 v[100:103], a[88:91], a[196:199], v[100:103]\n\t"
"s_waitcnt lgkmcnt(3)\n\t"
"ds_read_b128 a[180:183], v244 offset:36864\n\t"
"v_mfma_f32_16x16x32_bf16 v[232:235], v[232:235], v[196:199], 0\n\t"
"ds_read_b64_tr_b16 v[228:229], v240 offset:20480\n\t"
"v_mfma_f32_16x16x32_bf16 v[236:239], v[236:239], v[196:199], 0\n\t"
"ds_read_b64_tr_b16 v[230:231], v240 offset:22016\n\t"
"v_mfma_f32_16x16x32_bf16 v[132:135], a[96:99], a[172:175], 0\n\t"
"ds_read_b64_tr_b16 v[224:225], v241 offset:20480\n\t"
"v_mfma_f32_16x16x32_bf16 v[132:135], a[104:107], a[188:191], v[132:135]\n\t"
"ds_read_b64_tr_b16 v[226:227], v241 offset:22016\n\t"
"v_mfma_f32_16x16x32_bf16 v[132:135], a[112:115], a[196:199], v[132:135]\n\t"
"v_pk_mul_f32 v[232:233], v[232:233], v[242:243] op_sel:[0,1] op_sel_hi:[1,1]\n\t"
"v_pk_mul_f32 v[234:235], v[234:235], v[242:243] op_sel:[0,1] op_sel_hi:[1,1]\n\t"
"v_mfma_f32_16x16x32_bf16 v[164:167], a[120:123], a[172:175], 0\n\t"
"v_cvt_pk_bf16_f32 v232, v232, v233\n\t"
"v_cvt_pk_bf16_f32 v233, v234, v235\n\t"
"v_mfma_f32_16x16x32_bf16 v[164:167], a[128:131], a[188:191], v[164:167]\n\t"
"v_mfma_f32_16x16x32_bf16 v[164:167], a[136:139], a[196:199], v[164:167]\n\t"
"v_pk_mul_f32 v[236:237], v[236:237], v[242:243] op_sel:[0,1] op_sel_hi:[1,1]\n\t"
"v_pk_mul_f32 v[238:239], v[238:239], v[242:243] op_sel:[0,1] op_sel_hi:[1,1]\n\t"
"v_mfma_f32_16x16x32_bf16 v[18:21], a[0:3], a[184:187], 0\n\t"
"v_cvt_pk_bf16_f32 v234, v236, v237\n\t"
"v_cvt_pk_bf16_f32 v235, v238, v239\n\t"
"v_mfma_f32_16x16x32_bf16 v[18:21], a[8:11], a[200:203], v[18:21]\n\t"
"ds_read_b64_tr_b16 v[236:237], v240 offset:40448\n\t"
"v_mfma_f32_16x16x32_bf16 v[18:21], a[16:19], a[204:207], v[18:21]\n\t"
"ds_write_b128 v242, v[232:235] offset:32256\n\t"
"v_mfma_f32_16x16x32_bf16 v[50:53], a[24:27], a[184:187], 0\n\t"
"s_waitcnt lgkmcnt(4)\n\t"
"v_mfma_f32_16x16x32_bf16 v[50:53], a[32:35], a[200:203], v[50:53]\n\t"
"s_waitcnt lgkmcnt(2)\n\t"
"ds_read_b64_tr_b16 v[238:239], v240 offset:41984\n\t"
"v_mfma_f32_16x16x32_bf16 v[50:53], a[40:43], a[204:207], v[50:53]\n\t"
"ds_read_b64_tr_b16 v[232:233], v241 offset:40448\n\t"
"v_mfma_f32_16x16x32_bf16 v[228:231], v[228:231], v[196:199], 0\n\t"
"ds_read_b64_tr_b16 v[234:235], v241 offset:41984\n\t"
"v_mfma_f32_16x16x32_bf16 v[224:227], v[224:227], v[196:199], 0\n\t"
"v_mfma_f32_16x16x32_bf16 v[82:85], a[48:51], a[184:187], 0\n\t"
"Lpg1_%=:\n\t"
"s_cbranch_vccnz Lpg5_%=\n\t"
"v_mfma_f32_16x16x32_bf16 v[82:85], a[56:59], a[200:203], v[82:85]\n\t"
"v_mfma_f32_16x16x32_bf16 v[82:85], a[64:67], a[204:207], v[82:85]\n\t"
"v_mfma_f32_16x16x32_bf16 v[116:119], a[72:75], a[168:171], 0\n\t"
"v_cvt_pk_bf16_f32 v228, v228, v229\n\t"
"v_cvt_pk_bf16_f32 v229, v230, v231\n\t"
"v_mfma_f32_16x16x32_bf16 v[116:119], a[80:83], a[176:179], v[116:119]\n\t"
"v_cvt_pk_bf16_f32 v230, v224, v225\n\t"
"v_mfma_f32_16x16x32_bf16 v[116:119], a[88:91], a[180:183], v[116:119]\n\t"
"s_waitcnt lgkmcnt(3)\n\t"
"s_barrier\n\t"
"v_mfma_f32_16x16x32_bf16 v[148:151], a[96:99], a[168:171], 0\n\t"
"s_waitcnt lgkmcnt(2)\n\t"
"ds_read_b128 a[188:191], v244 offset:28160\n\t"
"v_mfma_f32_16x16x32_bf16 v[148:151], a[104:107], a[176:179], v[148:151]\n\t"
"s_waitcnt lgkmcnt(1)\n\t"
"ds_read_b128 v[204:207], v244 offset:37376\n\t"
"v_cvt_pk_bf16_f32 v231, v226, v227\n\t"
"v_mfma_f32_16x16x32_bf16 v[148:151], a[112:115], a[180:183], v[148:151]\n\t"
"ds_write_b128 v242, v[228:231] offset:6144\n\t"
"v_mfma_f32_16x16x32_bf16 v[236:239], v[236:239], v[196:199], 0\n\t"
"v_mfma_f32_16x16x32_bf16 v[232:235], v[232:235], v[196:199], 0\n\t"
"ds_read_b128 a[192:195], v244 offset:28672\n\t"
"v_mfma_f32_16x16x32_bf16 v[180:183], a[120:123], a[168:171], 0\n\t"
"s_waitcnt lgkmcnt(3)\n\t"
"v_mfma_f32_16x16x32_bf16 v[180:183], a[128:131], a[176:179], v[180:183]\n\t"
"v_mfma_f32_16x16x32_bf16 v[180:183], a[136:139], a[180:183], v[180:183]\n\t"
"v_mfma_f32_16x16x32_bf16 v[22:25], a[0:3], a[200:203], 0\n\t"
"v_cvt_pk_bf16_f32 v236, v236, v237\n\t"
"v_cvt_pk_bf16_f32 v237, v238, v239\n\t"
"v_mfma_f32_16x16x32_bf16 v[22:25], a[8:11], a[204:207], v[22:25]\n\t"
"v_cvt_pk_bf16_f32 v238, v232, v233\n\t"
"v_cvt_pk_bf16_f32 v239, v234, v235\n\t"
"v_mfma_f32_16x16x32_bf16 v[22:25], a[16:19], a[188:191], v[22:25]\n\t"
"ds_write_b128 v242, v[236:239] offset:7680\n\t"
"v_mfma_f32_16x16x32_bf16 v[54:57], a[24:27], a[200:203], 0\n\t"
"v_mfma_f32_16x16x32_bf16 v[54:57], a[32:35], a[204:207], v[54:57]\n\t"
"ds_read_b128 v[196:199], v244 offset:37888\n\t"
"v_mfma_f32_16x16x32_bf16 v[54:57], a[40:43], a[188:191], v[54:57]\n\t"
"v_mfma_f32_16x16x32_bf16 v[86:89], a[48:51], a[200:203], 0\n\t"
"s_waitcnt lgkmcnt(4)\n\t"
"v_mfma_f32_16x16x32_bf16 v[86:89], a[56:59], a[204:207], v[86:89]\n\t"
"v_mfma_f32_16x16x32_bf16 v[86:89], a[64:67], a[188:191], v[86:89]\n\t"
"v_mfma_f32_16x16x32_bf16 v[120:123], a[72:75], a[176:179], 0\n\t"
"s_mov_b32 s20, 294912\n\t"
"v_mfma_f32_16x16x32_bf16 v[120:123], a[80:83], a[180:183], v[120:123]\n\t"
"v_mfma_f32_16x16x32_bf16 v[120:123], a[88:91], v[204:207], v[120:123]\n\t"
"v_mfma_f32_16x16x32_bf16 v[152:155], a[96:99], a[176:179], 0\n\t"
"ds_read_b128 a[196:199], v244 offset:29184\n\t"
"v_mfma_f32_16x16x32_bf16 v[152:155], a[104:107], a[180:183], v[152:155]\n\t"
"v_mfma_f32_16x16x32_bf16 v[152:155], a[112:115], v[204:207], v[152:155]\n\t"
"s_waitcnt lgkmcnt(2)\n\t"
"s_cmp_lg_u32 s73, 0\n\t"
"s_cbranch_scc1 Lgo_%=\n\t"
"Lchk_%=:\n\t"
"s_waitcnt vmcnt(0)\n\t"
"v_cmpx_ne_u32_e64 s[60:61], v220, v248\n\t"
"v_cmpx_ne_u32_e64 s[60:61], v221, v249\n\t"
"v_cmpx_ne_u32_e64 s[60:61], v222, v250\n\t"
"v_cmpx_ne_u32_e64 s[60:61], v223, v251\n\t"
"s_not_b64 s[60:61], s[60:61]\n\t"
"s_mov_b64 exec, -1\n\t"
"s_lshl_b64 s[60:61], s[60:61], 10\n\t"
"s_cbranch_scc0 Lgo_%=\n\t"
"s_sub_u32 s68, s68, 1\n\t"
"s_cbranch_scc1 Lfb_%=\n\t"
"buffer_load_dwordx4 v[248:251], v252, s[8:11], s72 offen sc1\n\t"
"s_branch Lchk_%=\n\t"
"Lgo_%=:\n\t"
"s_barrier\n\t"
"v_mfma_f32_16x16x32_bf16 v[184:187], a[120:123], a[176:179], 0\n\t"
"s_mov_b32 s52, 0x3fd80000\n\t"
"s_mov_b32 s53, s52\n\t"
"v_mfma_f32_16x16x32_bf16 v[184:187], a[128:131], a[180:183], v[184:187]\n\t"
"buffer_load_dwordx4 v[236:239], v252, s[8:11], s20 offen offset:0 sc1\n\t"
"v_mfma_f32_16x16x32_bf16 v[184:187], a[136:139], v[204:207], v[184:187]\n\t"
"s_mov_b32 s54, 0x3e97b426\n\t"
"s_mov_b32 s55, s54\n\t"
"s_mov_b32 s56, 0x3f400000\n\t"
"v_mfma_f32_16x16x32_bf16 v[26:29], a[0:3], a[204:207], 0\n\t"
"buffer_load_dwordx4 a[144:147], v252, s[8:11], s20 offen offset:1024 sc1\n\t"
"v_mfma_f32_16x16x32_bf16 v[26:29], a[8:11], a[188:191], v[26:29]\n\t"
"s_mov_b32 s57, s56\n\t"
"s_mov_b32 s58, 0x3f2aaaab\n\t"
"s_mov_b32 s59, s58\n\t"
"v_mfma_f32_16x16x32_bf16 v[26:29], a[16:19], a[192:195], v[26:29]\n\t"
"s_mov_b32 s60, 0x3ee38e39\n\t"
"s_mov_b32 s61, s60\n\t"
"s_mov_b32 s62, 0x40100000\n\t"
"v_mfma_f32_16x16x32_bf16 v[58:61], a[24:27], a[204:207], 0\n\t"
"buffer_load_dwordx4 a[152:155], v252, s[8:11], s20 offen offset:2048 sc1\n\t"
"v_mfma_f32_16x16x32_bf16 v[58:61], a[32:35], a[188:191], v[58:61]\n\t"
"s_mov_b32 s20, 307200\n\t"
"ds_read_b128 v[212:215], v244 offset:38400\n\t"
"s_mov_b32 s63, s62\n\t"
"v_mfma_f32_16x16x32_bf16 v[58:61], a[40:43], a[192:195], v[58:61]\n\t"
"s_mov_b32 s64, 0x3faaaaab\n\t"
"s_mov_b32 s65, s64\n\t"
"s_mov_b32 s66, 0x3f17b426\n\t"
"v_mfma_f32_16x16x32_bf16 v[90:93], a[48:51], a[204:207], 0\n\t"
"buffer_load_dwordx4 a[160:163], v252, s[8:11], s20 offen offset:0 sc1\n\t"
"v_mfma_f32_16x16x32_bf16 v[90:93], a[56:59], a[188:191], v[90:93]\n\t"
"s_waitcnt lgkmcnt(2)\n\t"
"s_mov_b32 s67, s66\n\t"
"v_mfma_f32_16x16x32_bf16 v[90:93], a[64:67], a[192:195], v[90:93]\n\t"
"v_mfma_f32_16x16x32_bf16 v[124:127], a[72:75], a[180:183], 0\n\t"
"buffer_load_dwordx4 v[200:203], v252, s[8:11], s20 offen offset:1024 sc1\n\t"
"v_mfma_f32_16x16x32_bf16 v[124:127], a[80:83], v[204:207], v[124:127]\n\t"
"v_mfma_f32_16x16x32_bf16 v[124:127], a[88:91], v[196:199], v[124:127]\n\t"
"v_mfma_f32_16x16x32_bf16 v[156:159], a[96:99], a[180:183], 0\n\t"
"buffer_load_dwordx4 v[208:211], v252, s[8:11], s20 offen offset:2048 sc1\n\t"
"v_mfma_f32_16x16x32_bf16 v[156:159], a[104:107], v[204:207], v[156:159]\n\t"
"s_mov_b32 s20, 319488\n\t"
"v_mfma_f32_16x16x32_bf16 v[156:159], a[112:115], v[196:199], v[156:159]\n\t"
"v_mfma_f32_16x16x32_bf16 v[188:191], a[120:123], a[180:183], 0\n\t"
"buffer_load_dwordx4 v[216:219], v252, s[8:11], s20 offen offset:0 sc1\n\t"
"v_mfma_f32_16x16x32_bf16 v[188:191], a[128:131], v[204:207], v[188:191]\n\t"
"s_waitcnt lgkmcnt(1)\n\t"
"ds_read_b128 a[168:171], v244 offset:0\n\t"
"v_mfma_f32_16x16x32_bf16 v[188:191], a[136:139], v[196:199], v[188:191]\n\t"
"ds_read_b128 a[172:175], v244 offset:512\n\t"
"v_mfma_f32_16x16x32_bf16 v[30:33], a[0:3], a[188:191], 0\n\t"
"buffer_load_dwordx4 a[0:3], v252, s[8:11], s20 offen offset:1024 sc1\n\t"
"v_mfma_f32_16x16x32_bf16 v[30:33], a[8:11], a[192:195], v[30:33]\n\t"
"ds_read_b128 a[176:179], v244 offset:1024\n\t"
"v_mfma_f32_16x16x32_bf16 v[30:33], a[16:19], a[196:199], v[30:33]\n\t"
"ds_read_b128 a[180:183], v244 offset:1536\n\t"
"v_mfma_f32_16x16x32_bf16 v[62:65], a[24:27], a[188:191], 0\n\t"
"buffer_load_dwordx4 a[8:11], v252, s[8:11], s20 offen offset:2048 sc1\n\t"
"v_mfma_f32_16x16x32_bf16 v[62:65], a[32:35], a[192:195], v[62:65]\n\t"
"s_mov_b32 s20, 479232\n\t"
"ds_read_b128 a[184:187], v244 offset:2048\n\t"
"v_mfma_f32_16x16x32_bf16 v[62:65], a[40:43], a[196:199], v[62:65]\n\t"
"global_load_dwordx4 v[220:223], v252, s[48:49] offset:1024\n\t"
"v_mfma_f32_16x16x32_bf16 v[94:97], a[48:51], a[188:191], 0\n\t"
"buffer_load_dwordx4 a[16:19], v252, s[8:11], s20 offen offset:0 sc1\n\t"
"v_mfma_f32_16x16x32_bf16 v[94:97], a[56:59], a[192:195], v[94:97]\n\t"
"s_waitcnt lgkmcnt(5)\n\t"
"ds_read_b128 a[188:191], v244 offset:2560\n\t"
"v_mfma_f32_16x16x32_bf16 v[94:97], a[64:67], a[196:199], v[94:97]\n\t"
"v_mfma_f32_16x16x32_bf16 v[128:131], a[72:75], v[204:207], 0\n\t"
"buffer_load_dwordx4 a[24:27], v252, s[8:11], s20 offen offset:1024 sc1\n\t"
"v_mfma_f32_16x16x32_bf16 v[128:131], a[80:83], v[196:199], v[128:131]\n\t"
"ds_read_b128 a[192:195], v244 offset:3072\n\t"
"v_mfma_f32_16x16x32_bf16 v[128:131], a[88:91], v[212:215], v[128:131]\n\t"
"v_mfma_f32_16x16x32_bf16 v[160:163], a[96:99], v[204:207], 0\n\t"
"buffer_load_dwordx4 a[32:35], v252, s[8:11], s20 offen offset:2048 sc1\n\t"
"v_mfma_f32_16x16x32_bf16 v[160:163], a[104:107], v[196:199], v[160:163]\n\t"
"s_mov_b32 s20, 491520\n\t"
"ds_read_b128 a[196:199], v244 offset:3584\n\t"
"v_mfma_f32_16x16x32_bf16 v[160:163], a[112:115], v[212:215], v[160:163]\n\t"
"v_mfma_f32_16x16x32_bf16 v[192:195], a[120:123], v[204:207], 0\n\t"
"buffer_load_dwordx4 a[40:43], v252, s[8:11], s20 offen offset:0 sc1\n\t"
"v_mfma_f32_16x16x32_bf16 v[192:195], a[128:131], v[196:199], v[192:195]\n\t"
"s_waitcnt vmcnt(11) lgkmcnt(5)\n\t"
"ds_read_b128 a[200:203], v244 offset:4096\n\t"
"v_mfma_f32_16x16x32_bf16 v[192:195], a[136:139], v[212:215], v[192:195]\n\t"
"Lst1_%=:\n\t"
"v_mfma_f32_16x16x32_bf16 v[2:5], v[236:239], a[168:171], v[2:5]\n\t"
"buffer_load_dwordx4 a[48:51], v252, s[8:11], s20 offen offset:1024 sc1\n\t"
"v_mfma_f32_16x16x32_bf16 v[2:5], a[144:147], a[172:175], v[2:5]\n\t"
"s_waitcnt lgkmcnt(5)\n\t"
"ds_read_b128 a[204:207], v244 offset:4608\n\t"
"v_mfma_f32_16x16x32_bf16 v[2:5], a[152:155], a[176:179], v[2:5]\n\t"
"v_mfma_f32_16x16x32_bf16 v[6:9], v[236:239], a[172:175], v[6:9]\n\t"
"buffer_load_dwordx4 a[56:59], v252, s[8:11], s20 offen offset:2048 sc1\n\t"
"v_mfma_f32_16x16x32_bf16 v[6:9], a[144:147], a[176:179], v[6:9]\n\t"
"s_waitcnt lgkmcnt(5)\n\t"
"s_mov_b32 s20, 503808\n\t"
"v_mfma_f32_16x16x32_bf16 v[6:9], a[152:155], a[180:183], v[6:9]\n\t"
"v_mfma_f32_16x16x32_bf16 v[10:13], v[236:239], a[176:179], v[10:13]\n\t"
"buffer_load_dwordx4 a[80:83], v252, s[8:11], s20 offen offset:0 sc1\n\t"
"v_mfma_f32_16x16x32_bf16 v[10:13], a[144:147], a[180:183], v[10:13]\n\t"
"s_waitcnt lgkmcnt(4)\n\t"
"v_mfma_f32_16x16x32_bf16 v[10:13], a[152:155], a[184:187], v[10:13]\n\t"
"v_mfma_f32_16x16x32_bf16 v[14:17], v[236:239], a[180:183], v[14:17]\n\t"
"buffer_load_dwordx4 a[88:91], v252, s[8:11], s20 offen offset:1024 sc1\n\t"
"v_mfma_f32_16x16x32_bf16 v[14:17], a[144:147], a[184:187], v[14:17]\n\t"
"s_waitcnt lgkmcnt(3)\n\t"
"v_mfma_f32_16x16x32_bf16 v[14:17], a[152:155], a[188:191], v[14:17]\n\t"
"v_mfma_f32_16x16x32_bf16 v[18:21], v[236:239], a[184:187], v[18:21]\n\t"
"buffer_load_dwordx4 a[96:99], v252, s[8:11], s20 offen offset:2048 sc1\n\t"
"v_mfma_f32_16x16x32_bf16 v[18:21], a[144:147], a[188:191], v[18:21]\n\t"
"s_waitcnt lgkmcnt(2)\n\t"
"s_mov_b32 s20, 110592\n\t"
"v_xor_b32 v246, 0xc0, v246\n\t"
"v_mfma_f32_16x16x32_bf16 v[18:21], a[152:155], a[192:195], v[18:21]\n\t"
"v_mfma_f32_16x16x32_bf16 v[22:25], v[236:239], a[188:191], v[22:25]\n\t"
"buffer_load_dwordx4 a[136:139], v252, s[8:11], s20 offen offset:0 sc1\n\t"
"v_mfma_f32_16x16x32_bf16 v[22:25], a[144:147], a[192:195], v[22:25]\n\t"
"s_waitcnt lgkmcnt(1)\n\t"
"v_mfma_f32_16x16x32_bf16 v[22:25], a[152:155], a[196:199], v[22:25]\n\t"
"v_mfma_f32_16x16x32_bf16 v[26:29], v[236:239], a[192:195], v[26:29]\n\t"
"buffer_load_dwordx4 a[4:7], v252, s[8:11], s20 offen offset:1024 sc1\n\t"
"v_mfma_f32_16x16x32_bf16 v[26:29], a[144:147], a[196:199], v[26:29]\n\t"
"s_waitcnt lgkmcnt(0)\n\t"
"v_mfma_f32_16x16x32_bf16 v[26:29], a[152:155], a[200:203], v[26:29]\n\t"
"v_mfma_f32_16x16x32_bf16 v[30:33], v[236:239], a[196:199], v[30:33]\n\t"
"buffer_load_dwordx4 a[12:15], v252, s[8:11], s20 offen offset:2048 sc1\n\t"
"v_mfma_f32_16x16x32_bf16 v[30:33], a[144:147], a[200:203], v[30:33]\n\t"
"s_waitcnt vmcnt(16)\n\t"
"s_mov_b32 s20, 122880\n\t"
"v_mfma_f32_16x16x32_bf16 v[30:33], a[152:155], a[204:207], v[30:33]\n\t"
"v_mfma_f32_16x16x32_bf16 v[34:37], a[160:163], a[168:171], v[34:37]\n\t"
"buffer_load_dwordx4 a[20:23], v252, s[8:11], s20 offen offset:0 sc1\n\t"
"v_mfma_f32_16x16x32_bf16 v[34:37], v[200:203], a[172:175], v[34:37]\n\t"
"v_mfma_f32_16x16x32_bf16 v[34:37], v[208:211], a[176:179], v[34:37]\n\t"
"ds_read_b64_tr_b16 v[224:225], v246 offset:0\n\t"
"v_mfma_f32_16x16x32_bf16 v[38:41], a[160:163], a[172:175], v[38:41]\n\t"
"buffer_load_dwordx4 a[28:31], v252, s[8:11], s20 offen offset:1024 sc1\n\t"
"v_mfma_f32_16x16x32_bf16 v[38:41], v[200:203], a[176:179], v[38:41]\n\t"
"ds_read_b64_tr_b16 v[226:227], v246 offset:3072\n\t"
"v_mfma_f32_16x16x32_bf16 v[38:41], v[208:211], a[180:183], v[38:41]\n\t"
"v_mfma_f32_16x16x32_bf16 v[42:45], a[160:163], a[176:179], v[42:45]\n\t"
"buffer_load_dwordx4 a[36:39], v252, s[8:11], s20 offen offset:2048 sc1\n\t"
"v_mfma_f32_16x16x32_bf16 v[42:45], v[200:203], a[180:183], v[42:45]\n\t"
"s_mov_b32 s20, 135168\n\t"
"v_mfma_f32_16x16x32_bf16 v[42:45], v[208:211], a[184:187], v[42:45]\n\t"
"v_mfma_f32_16x16x32_bf16 v[46:49], a[160:163], a[180:183], v[46:49]\n\t"
"buffer_load_dwordx4 a[76:79], v252, s[8:11], s20 offen offset:0 sc1\n\t"
"v_mfma_f32_16x16x32_bf16 v[46:49], v[200:203], a[184:187], v[46:49]\n\t"
"ds_read_b64_tr_b16 v[228:229], v246 offset:6144\n\t"
"v_mfma_f32_16x16x32_bf16 v[46:49], v[208:211], a[188:191], v[46:49]\n\t"
"ds_read_b64_tr_b16 v[230:231], v246 offset:9216\n\t"
"v_mfma_f32_16x16x32_bf16 v[50:53], a[160:163], a[184:187], v[50:53]\n\t"
"buffer_load_dwordx4 a[84:87], v252, s[8:11], s20 offen offset:1024 sc1\n\t"
"v_mfma_f32_16x16x32_bf16 v[50:53], v[200:203], a[188:191], v[50:53]\n\t"
"v_mfma_f32_16x16x32_bf16 v[50:53], v[208:211], a[192:195], v[50:53]\n\t"
"v_mfma_f32_16x16x32_bf16 v[54:57], a[160:163], a[188:191], v[54:57]\n\t"
"buffer_load_dwordx4 a[92:95], v252, s[8:11], s20 offen offset:2048 sc1\n\t"
"v_mfma_f32_16x16x32_bf16 v[54:57], v[200:203], a[192:195], v[54:57]\n\t"
"s_waitcnt vmcnt(18) lgkmcnt(2)\n\t"
"s_mov_b32 s20, 147456\n\t"
"v_mfma_f32_16x16x32_bf16 v[54:57], v[208:211], a[196:199], v[54:57]\n\t"
"v_mfma_f32_16x16x32_bf16 v[224:227], v[224:227], v[220:223], 0\n\t"
"buffer_load_dwordx4 a[148:151], v252, s[8:11], s20 offen offset:0 sc1\n\t"
"v_mfma_f32_16x16x32_bf16 v[58:61], a[160:163], a[192:195], v[58:61]\n\t"
"ds_read_b64_tr_b16 v[232:233], v246 offset:12288\n\t"
"v_mfma_f32_16x16x32_bf16 v[58:61], v[200:203], a[196:199], v[58:61]\n\t"
"ds_read_b64_tr_b16 v[234:235], v246 offset:15360\n\t"
"v_mfma_f32_16x16x32_bf16 v[58:61], v[208:211], a[200:203], v[58:61]\n\t"
"v_mfma_f32_16x16x32_bf16 v[62:65], a[160:163], a[196:199], v[62:65]\n\t"
"buffer_load_dwordx4 a[156:159], v252, s[8:11], s20 offen offset:1024 sc1\n\t"
"v_mfma_f32_16x16x32_bf16 v[62:65], v[200:203], a[200:203], v[62:65]\n\t"
"s_waitcnt lgkmcnt(2)\n\t"
"v_cvt_pk_bf16_f32 v224, v224, v225\n\t"
"v_cvt_pk_bf16_f32 v225, v226, v227\n\t"
"v_mfma_f32_16x16x32_bf16 v[62:65], v[208:211], a[204:207], v[62:65]\n\t"
"v_mfma_f32_16x16x32_bf16 v[228:231], v[228:231], v[220:223], 0\n\t"
"v_mfma_f32_16x16x32_bf16 v[66:69], v[216:219], a[168:171], v[66:69]\n\t"
"buffer_load_dwordx4 a[164:167], v252, s[8:11], s20 offen offset:2048 sc1\n\t"
"v_mfma_f32_16x16x32_bf16 v[66:69], a[0:3], a[172:175], v[66:69]\n\t"
"s_mov_b32 s20, 159744\n\t"
"ds_read_b64_tr_b16 v[236:237], v246 offset:19968\n\t"
"v_mfma_f32_16x16x32_bf16 v[66:69], a[8:11], a[176:179], v[66:69]\n\t"
"ds_read_b64_tr_b16 v[238:239], v246 offset:23040\n\t"
"v_mfma_f32_16x16x32_bf16 v[70:73], v[216:219], a[172:175], v[70:73]\n\t"
"ds_read_b128 a[168:171], v244 offset:9216\n\t"
"v_mfma_f32_16x16x32_bf16 v[70:73], a[0:3], a[176:179], v[70:73]\n\t"
"buffer_load_dwordx4 v[240:243], v252, s[8:11], s20 offen offset:0 sc1\n\t"
"v_mfma_f32_16x16x32_bf16 v[70:73], a[8:11], a[180:183], v[70:73]\n\t"
"ds_read_b128 a[172:175], v244 offset:9728\n\t"
"v_cvt_pk_bf16_f32 v228, v228, v229\n\t"
"v_mfma_f32_16x16x32_bf16 v[74:77], v[216:219], a[176:179], v[74:77]\n\t"
"s_waitcnt lgkmcnt(4)\n\t"
"ds_read_b128 a[176:179], v244 offset:10240\n\t"
"v_cvt_pk_bf16_f32 v229, v230, v231\n\t"
"v_mfma_f32_16x16x32_bf16 v[74:77], a[0:3], a[180:183], v[74:77]\n\t"
"v_mfma_f32_16x16x32_bf16 v[74:77], a[8:11], a[184:187], v[74:77]\n\t"
"buffer_load_dwordx4 a[208:211], v252, s[8:11], s20 offen offset:1024 sc1\n\t"
"v_mfma_f32_16x16x32_bf16 v[232:235], v[232:235], v[220:223], 0\n\t"
"v_mfma_f32_16x16x32_bf16 v[78:81], v[216:219], a[180:183], v[78:81]\n\t"
"ds_read_b128 a[180:183], v244 offset:10752\n\t"
"v_mfma_f32_16x16x32_bf16 v[78:81], a[0:3], a[184:187], v[78:81]\n\t"
"v_mfma_f32_16x16x32_bf16 v[78:81], a[8:11], a[188:191], v[78:81]\n\t"
"buffer_load_dwordx4 v[248:251], v252, s[8:11], s20 offen offset:2048 sc1\n\t"
"v_mfma_f32_16x16x32_bf16 v[82:85], v[216:219], a[184:187], v[82:85]\n\t"
"s_waitcnt lgkmcnt(4)\n\t"
"ds_read_b128 a[184:187], v244 offset:11264\n\t"
"s_mov_b32 s20, 172032\n\t"
"v_mfma_f32_16x16x32_bf16 v[82:85], a[0:3], a[188:191], v[82:85]\n\t"
"v_cvt_pk_bf16_f32 v232, v232, v233\n\t"
"v_cvt_pk_bf16_f32 v233, v234, v235\n\t"
"v_mfma_f32_16x16x32_bf16 v[82:85], a[8:11], a[192:195], v[82:85]\n\t"
"v_mfma_f32_16x16x32_bf16 v[236:239], v[236:239], v[220:223], 0\n\t"
"buffer_load_dwordx4 a[212:215], v252, s[8:11], s20 offen offset:0 sc1\n\t"
"v_mfma_f32_16x16x32_bf16 v[86:89], v[216:219], a[188:191], v[86:89]\n\t"
"ds_read_b128 a[188:191], v244 offset:11776\n\t"
"v_mfma_f32_16x16x32_bf16 v[86:89], a[0:3], a[192:195], v[86:89]\n\t"
"v_mfma_f32_16x16x32_bf16 v[86:89], a[8:11], a[196:199], v[86:89]\n\t"
"v_mfma_f32_16x16x32_bf16 v[90:93], v[216:219], a[192:195], v[90:93]\n\t"
"buffer_load_dwordx4 a[216:219], v252, s[8:11], s20 offen offset:1024 sc1\n\t"
"v_mfma_f32_16x16x32_bf16 v[90:93], a[0:3], a[196:199], v[90:93]\n\t"
"ds_read_b128 a[192:195], v244 offset:12288\n\t"
"v_cvt_pk_bf16_f32 v236, v236, v237\n\t"
"v_mfma_f32_16x16x32_bf16 v[90:93], a[8:11], a[200:203], v[90:93]\n\t"
"v_cvt_pk_bf16_f32 v237, v238, v239\n\t"
"v_mfma_f32_16x16x32_bf16 v[94:97], v[216:219], a[196:199], v[94:97]\n\t"
"s_waitcnt vmcnt(23) lgkmcnt(4)\n\t"
"ds_read_b128 a[196:199], v244 offset:12800\n\t"
"v_mfma_f32_16x16x32_bf16 v[94:97], a[0:3], a[200:203], v[94:97]\n\t"
"ds_read_b128 a[200:203], v244 offset:13312\n\t"
"v_mfma_f32_16x16x32_bf16 v[94:97], a[8:11], a[204:207], v[94:97]\n\t"
"ds_read_b128 a[204:207], v244 offset:13824\n\t"
"v_mfma_f32_16x16x32_bf16 v[100:103], a[16:19], a[168:171], v[100:103]\n\t"
"buffer_load_dwordx4 a[220:223], v252, s[8:11], s20 offen offset:2048 sc1\n\t"
"v_mfma_f32_16x16x32_bf16 v[100:103], a[24:27], a[172:175], v[100:103]\n\t"
"s_waitcnt lgkmcnt(6)\n\t"
"s_mov_b32 s20, 36864\n\t"
"v_mfma_f32_16x16x32_bf16 v[100:103], a[32:35], a[176:179], v[100:103]\n\t"
"buffer_load_dwordx4 a[224:227], v252, s[8:11], s20 offen offset:0 sc1\n\t"
"v_mfma_f32_16x16x32_bf16 v[104:107], a[16:19], a[172:175], v[104:107]\n\t"
"s_waitcnt lgkmcnt(5)\n\t"
"v_mfma_f32_16x16x32_bf16 v[104:107], a[24:27], a[176:179], v[104:107]\n\t"
"v_mfma_f32_16x16x32_bf16 v[104:107], a[32:35], a[180:183], v[104:107]\n\t"
"s_waitcnt lgkmcnt(0)\n\t"
"s_barrier\n\t"
"v_mfma_f32_16x16x32_bf16 v[108:111], a[16:19], a[176:179], v[108:111]\n\t"
"v_fma_f32 v196, -v2, s56, -v100\n\t"
"v_mfma_f32_16x16x32_bf16 v[108:111], a[24:27], a[180:183], v[108:111]\n\t"
"buffer_load_dwordx4 a[228:231], v252, s[8:11], s20 offen offset:1024 sc1\n\t"
"v_mfma_f32_16x16x32_bf16 v[108:111], a[32:35], a[184:187], v[108:111]\n\t"
"ds_write_b64 v245, v[224:225] offset:0\n\t"
"v_mfma_f32_16x16x32_bf16 v[112:115], a[16:19], a[180:183], v[112:115]\n\t"
"ds_read_b64_tr_b16 v[224:225], v246 offset:26112\n\t"
"v_fma_f32 v197, -v3, s56, -v101\n\t"
"v_mfma_f32_16x16x32_bf16 v[112:115], a[24:27], a[184:187], v[112:115]\n\t"
"ds_read_b64_tr_b16 v[226:227], v246 offset:29184\n\t"
"v_fma_f32 v198, -v4, s56, -v102\n\t"
"v_mfma_f32_16x16x32_bf16 v[112:115], a[32:35], a[188:191], v[112:115]\n\t"
"ds_write_b64 v245, v[228:229] offset:512\n\t"
"v_mfma_f32_16x16x32_bf16 v[116:119], a[16:19], a[184:187], v[116:119]\n\t"
"buffer_load_dwordx4 a[232:235], v252, s[8:11], s20 offen offset:2048 sc1\n\t"
"v_mfma_f32_16x16x32_bf16 v[116:119], a[24:27], a[188:191], v[116:119]\n\t"
"s_mov_b32 s20, 49152\n\t"
"ds_read_b64_tr_b16 v[228:229], v246 offset:32256\n\t"
"v_fma_f32 v199, -v5, s56, -v103\n\t"
"v_mfma_f32_16x16x32_bf16 v[116:119], a[32:35], a[192:195], v[116:119]\n\t"
"ds_write_b64 v245, v[232:233] offset:1024\n\t"
"v_mfma_f32_16x16x32_bf16 v[120:123], a[16:19], a[188:191], v[120:123]\n\t"
"buffer_load_dwordx4 a[236:239], v252, s[8:11], s20 offen offset:0 sc1\n\t"
"v_mfma_f32_16x16x32_bf16 v[120:123], a[24:27], a[192:195], v[120:123]\n\t"
"s_waitcnt lgkmcnt(3)\n\t"
"ds_read_b64_tr_b16 v[230:231], v246 offset:35328\n\t"
"v_fma_f32 v204, -v6, s56, -v104\n\t"
"v_mfma_f32_16x16x32_bf16 v[120:123], a[32:35], a[196:199], v[120:123]\n\t"
"ds_write_b64 v245, v[236:237] offset:1536\n\t"
"v_mfma_f32_16x16x32_bf16 v[224:227], v[224:227], v[220:223], 0\n\t"
"buffer_load_dwordx4 a[240:243], v252, s[8:11], s20 offen offset:1024 sc1\n\t"
"v_mfma_f32_16x16x32_bf16 v[124:127], a[16:19], a[192:195], v[124:127]\n\t"
"ds_read_b64_tr_b16 v[232:233], v246 offset:39936\n\t"
"v_fma_f32 v205, -v7, s56, -v105\n\t"
"v_mfma_f32_16x16x32_bf16 v[124:127], a[24:27], a[196:199], v[124:127]\n\t"
"ds_read_b64_tr_b16 v[234:235], v246 offset:43008\n\t"
"v_fma_f32 v206, -v8, s56, -v106\n\t"
"v_mfma_f32_16x16x32_bf16 v[124:127], a[32:35], a[200:203], v[124:127]\n\t"
"s_waitcnt lgkmcnt(3)\n\t"
"buffer_load_dwordx4 a[244:247], v252, s[8:11], s20 offen offset:2048 sc1\n\t"
"s_mov_b32 s20, 61440\n\t"
"v_mfma_f32_16x16x32_bf16 v[228:231], v[228:231], v[220:223], 0\n\t"
"ds_read_b64_tr_b16 v[236:237], v246 offset:46080\n\t"
"v_fma_f32 v207, -v9, s56, -v107\n\t"
"v_mfma_f32_16x16x32_bf16 v[128:131], a[16:19], a[196:199], v[128:131]\n\t"
"ds_read_b64_tr_b16 v[238:239], v246 offset:49152\n\t"
"v_cvt_pk_bf16_f32 v224, v224, v225\n\t"
"v_mfma_f32_16x16x32_bf16 v[128:131], a[24:27], a[200:203], v[128:131]\n\t"
"s_waitcnt vmcnt(27)\n\t"
"v_cvt_pk_bf16_f32 v225, v226, v227\n\t"
"v_fma_f32 v212, -v10, s56, -v108\n\t"
"v_mfma_f32_16x16x32_bf16 v[128:131], a[32:35], a[204:207], v[128:131]\n\t"
"s_waitcnt lgkmcnt(2)\n\t"
"buffer_load_dwordx4 a[248:251], v252, s[8:11], s20 offen offset:0 sc1\n\t"
"v_mfma_f32_16x16x32_bf16 v[232:235], v[232:235], v[220:223], 0\n\t"
"buffer_load_dwordx4 a[252:255], v252, s[8:11], s20 offen offset:1024 sc1\n\t"
"v_mfma_f32_16x16x32_bf16 v[132:135], a[40:43], a[168:171], v[132:135]\n\t"
"v_cvt_pk_bf16_f32 v228, v228, v229\n\t"
"v_cvt_pk_bf16_f32 v229, v230, v231\n\t"
"v_mfma_f32_16x16x32_bf16 v[132:135], a[48:51], a[172:175], v[132:135]\n\t"
"s_waitcnt lgkmcnt(0)\n\t"
"v_fma_f32 v213, -v11, s56, -v109\n\t"
"v_fma_f32 v214, -v12, s56, -v110\n\t"
"v_mfma_f32_16x16x32_bf16 v[132:135], a[56:59], a[176:179], v[132:135]\n\t"
"ds_write_b64 v245, v[224:225] offset:2048\n\t"
"v_mfma_f32_16x16x32_bf16 v[236:239], v[236:239], v[220:223], 0\n\t"
"ds_read_b64_tr_b16 v[224:225], v246 offset:52224\n\t"
"v_fma_f32 v215, -v13, s56, -v111\n\t"
"v_mfma_f32_16x16x32_bf16 v[136:139], a[40:43], a[172:175], v[136:139]\n\t"
"ds_read_b64_tr_b16 v[226:227], v246 offset:55296\n\t"
"v_cvt_pk_bf16_f32 v232, v232, v233\n\t"
"v_mfma_f32_16x16x32_bf16 v[136:139], a[48:51], a[176:179], v[136:139]\n\t"
"v_cvt_pk_bf16_f32 v233, v234, v235\n\t"
"v_fma_f32 v98, -v14, s56, -v112\n\t"
"v_mfma_f32_16x16x32_bf16 v[136:139], a[56:59], a[180:183], v[136:139]\n\t"
"ds_write_b64 v245, v[228:229] offset:2560\n\t"
"v_mfma_f32_16x16x32_bf16 v[140:143], a[40:43], a[176:179], v[140:143]\n\t"
"v_fma_f32 v99, -v15, s56, -v113\n\t"
"v_accvgpr_write_b32 a0, v98\n\t"
"v_mfma_f32_16x16x32_bf16 v[140:143], a[48:51], a[180:183], v[140:143]\n\t"
"v_cvt_pk_bf16_f32 v236, v236, v237\n\t"
"v_cvt_pk_bf16_f32 v237, v238, v239\n\t"
"v_mfma_f32_16x16x32_bf16 v[140:143], a[56:59], a[184:187], v[140:143]\n\t"
"ds_write_b64 v245, v[232:233] offset:3072\n\t"
"v_mfma_f32_16x16x32_bf16 v[144:147], a[40:43], a[180:183], v[144:147]\n\t"
"s_waitcnt lgkmcnt(2)\n\t"
"v_fma_f32 v98, -v16, s56, -v114\n\t"
"v_accvgpr_write_b32 a1, v99\n\t"
"v_mfma_f32_16x16x32_bf16 v[144:147], a[48:51], a[184:187], v[144:147]\n\t"
"v_fma_f32 v99, -v17, s56, -v115\n\t"
"v_accvgpr_write_b32 a2, v98\n\t"
"v_mfma_f32_16x16x32_bf16 v[144:147], a[56:59], a[188:191], v[144:147]\n\t"
"ds_write_b64 v245, v[236:237] offset:3584\n\t"
"v_mfma_f32_16x16x32_bf16 v[224:227], v[224:227], v[220:223], 0\n\t"
"v_accvgpr_write_b32 a3, v99\n\t"
"v_fma_f32 v98, -v18, s56, -v116\n\t"
"v_mfma_f32_16x16x32_bf16 v[148:151], a[40:43], a[184:187], v[148:151]\n\t"
"v_fma_f32 v99, -v19, s56, -v117\n\t"
"v_accvgpr_write_b32 a8, v98\n\t"
"v_mfma_f32_16x16x32_bf16 v[148:151], a[48:51], a[188:191], v[148:151]\n\t"
"v_fma_f32 v98, -v20, s56, -v118\n\t"
"v_accvgpr_write_b32 a9, v99\n\t"
"v_mfma_f32_16x16x32_bf16 v[148:151], a[56:59], a[192:195], v[148:151]\n\t"
"v_fma_f32 v99, -v21, s56, -v119\n\t"
"v_accvgpr_write_b32 a10, v98\n\t"
"v_mfma_f32_16x16x32_bf16 v[152:155], a[40:43], a[188:191], v[152:155]\n\t"
"v_accvgpr_write_b32 a11, v99\n\t"
"v_fma_f32 v98, -v22, s56, -v120\n\t"
"v_mfma_f32_16x16x32_bf16 v[152:155], a[48:51], a[192:195], v[152:155]\n\t"
"v_cvt_pk_bf16_f32 v224, v224, v225\n\t"
"v_cvt_pk_bf16_f32 v225, v226, v227\n\t"
"v_mfma_f32_16x16x32_bf16 v[152:155], a[56:59], a[196:199], v[152:155]\n\t"
"buffer_load_dwordx4 v[236:239], v252, s[8:11], s20 offen offset:2048 sc1\n\t"
"v_mfma_f32_16x16x32_bf16 v[156:159], a[40:43], a[192:195], v[156:159]\n\t"
"v_fma_f32 v99, -v23, s56, -v121\n\t"
"v_accvgpr_write_b32 a16, v98\n\t"
"v_mfma_f32_16x16x32_bf16 v[156:159], a[48:51], a[196:199], v[156:159]\n\t"
"v_fma_f32 v98, -v24, s56, -v122\n\t"
"v_accvgpr_write_b32 a17, v99\n\t"
"v_mfma_f32_16x16x32_bf16 v[156:159], a[56:59], a[200:203], v[156:159]\n\t"
"ds_write_b64 v245, v[224:225] offset:4096\n\t"
"v_mfma_f32_16x16x32_bf16 v[160:163], a[40:43], a[196:199], v[160:163]\n\t"
"s_waitcnt vmcnt(27)\n\t"
"v_fma_f32 v99, -v25, s56, -v123\n\t"
"v_accvgpr_write_b32 a18, v98\n\t"
"v_mfma_f32_16x16x32_bf16 v[160:163], a[48:51], a[200:203], v[160:163]\n\t"
"v_accvgpr_write_b32 a19, v99\n\t"
"v_fma_f32 v98, -v26, s56, -v124\n\t"
"v_mfma_f32_16x16x32_bf16 v[160:163], a[56:59], a[204:207], v[160:163]\n\t"
"v_fma_f32 v99, -v27, s56, -v125\n\t"
"v_accvgpr_write_b32 a24, v98\n\t"
"v_mfma_f32_16x16x32_bf16 v[164:167], a[80:83], a[168:171], v[164:167]\n\t"
"v_fma_f32 v98, -v28, s56, -v126\n\t"
"v_accvgpr_write_b32 a25, v99\n\t"
"v_mfma_f32_16x16x32_bf16 v[164:167], a[88:91], a[172:175], v[164:167]\n\t"
"v_fma_f32 v99, -v29, s56, -v127\n\t"
"v_mfma_f32_16x16x32_bf16 v[164:167], a[96:99], a[176:179], v[164:167]\n\t"
"s_waitcnt lgkmcnt(0)\n\t"
"s_barrier\n\t"
"v_mfma_f32_16x16x32_bf16 v[168:171], a[80:83], a[172:175], v[168:171]\n\t"
"ds_read_b128 a[168:171], v244 offset:0\n\t"
"v_mfma_f32_16x16x32_bf16 v[168:171], a[88:91], a[176:179], v[168:171]\n\t"
"ds_read_b128 a[172:175], v244 offset:512\n\t"
"v_accvgpr_write_b32 a26, v98\n\t"
"v_mfma_f32_16x16x32_bf16 v[168:171], a[96:99], a[180:183], v[168:171]\n\t"
"v_accvgpr_write_b32 a27, v99\n\t"
"v_fma_f32 v98, -v30, s56, -v128\n\t"
"v_mfma_f32_16x16x32_bf16 v[172:175], a[80:83], a[176:179], v[172:175]\n\t"
"ds_read_b128 a[176:179], v244 offset:1024\n\t"
"v_fma_f32 v99, -v31, s56, -v129\n\t"
"v_mfma_f32_16x16x32_bf16 v[172:175], a[88:91], a[180:183], v[172:175]\n\t"
"v_accvgpr_write_b32 a32, v98\n\t"
"v_fma_f32 v98, -v32, s56, -v130\n\t"
"v_mfma_f32_16x16x32_bf16 v[172:175], a[96:99], a[184:187], v[172:175]\n\t"
"v_accvgpr_write_b32 a33, v99\n\t"
"v_fma_f32 v99, -v33, s56, -v131\n\t"
"v_mfma_f32_16x16x32_bf16 v[176:179], a[80:83], a[180:183], v[176:179]\n\t"
"ds_read_b128 a[180:183], v244 offset:1536\n\t"
"v_accvgpr_write_b32 a34, v98\n\t"
"v_mfma_f32_16x16x32_bf16 v[176:179], a[88:91], a[184:187], v[176:179]\n\t"
"v_accvgpr_write_b32 a35, v99\n\t"
"v_fma_f32 v98, -v34, s56, -v132\n\t"
"v_mfma_f32_16x16x32_bf16 v[176:179], a[96:99], a[188:191], v[176:179]\n\t"
"v_fma_f32 v99, -v35, s56, -v133\n\t"
"v_accvgpr_write_b32 a40, v98\n\t"
"v_mfma_f32_16x16x32_bf16 v[180:183], a[80:83], a[184:187], v[180:183]\n\t"
"ds_read_b128 a[184:187], v244 offset:2048\n\t"
"v_fma_f32 v98, -v36, s56, -v134\n\t"
"v_mfma_f32_16x16x32_bf16 v[180:183], a[88:91], a[188:191], v[180:183]\n\t"
"v_accvgpr_write_b32 a41, v99\n\t"
"v_fma_f32 v99, -v37, s56, -v135\n\t"
"v_mfma_f32_16x16x32_bf16 v[180:183], a[96:99], a[192:195], v[180:183]\n\t"
"v_accvgpr_write_b32 a42, v98\n\t"
"v_accvgpr_write_b32 a43, v99\n\t"
"v_mfma_f32_16x16x32_bf16 v[184:187], a[80:83], a[188:191], v[184:187]\n\t"
"ds_read_b128 a[188:191], v244 offset:2560\n\t"
"v_fma_f32 v98, -v38, s56, -v136\n\t"
"v_mfma_f32_16x16x32_bf16 v[184:187], a[88:91], a[192:195], v[184:187]\n\t"
"v_fma_f32 v99, -v39, s56, -v137\n\t"
"v_accvgpr_write_b32 a48, v98\n\t"
"v_mfma_f32_16x16x32_bf16 v[184:187], a[96:99], a[196:199], v[184:187]\n\t"
"v_fma_f32 v98, -v40, s56, -v138\n\t"
"v_accvgpr_write_b32 a49, v99\n\t"
"v_mfma_f32_16x16x32_bf16 v[188:191], a[80:83], a[192:195], v[188:191]\n\t"
"ds_read_b128 a[192:195], v244 offset:3072\n\t"
"v_fma_f32 v99, -v41, s56, -v139\n\t"
"v_mfma_f32_16x16x32_bf16 v[188:191], a[88:91], a[196:199], v[188:191]\n\t"
"v_accvgpr_write_b32 a50, v98\n\t"
"v_accvgpr_write_b32 a51, v99\n\t"
"v_mfma_f32_16x16x32_bf16 v[188:191], a[96:99], a[200:203], v[188:191]\n\t"
"v_fma_f32 v98, -v42, s56, -v140\n\t"
"v_fma_f32 v99, -v43, s56, -v141\n\t"
"v_mfma_f32_16x16x32_bf16 v[192:195], a[80:83], a[196:199], v[192:195]\n\t"
"s_waitcnt vmcnt(24) lgkmcnt(4)\n\t"
"ds_read_b128 a[196:199], v244 offset:3584\n\t"
"v_accvgpr_write_b32 a56, v98\n\t"
"v_mfma_f32_16x16x32_bf16 v[192:195], a[88:91], a[200:203], v[192:195]\n\t"
"ds_read_b128 a[200:203], v244 offset:4096\n\t"
"v_fma_f32 v98, -v44, s56, -v142\n\t"
"v_mfma_f32_16x16x32_bf16 v[192:195], a[96:99], a[204:207], v[192:195]\n\t"
"ds_read_b128 a[204:207], v244 offset:4608\n\t"
"v_accvgpr_write_b32 a57, v99\n\t"
"Lst2_%=:\n\t"
"v_mfma_f32_16x16x32_bf16 v[196:199], a[136:139], a[168:171], v[196:199]\n\t"
"s_waitcnt lgkmcnt(6)\n\t"
"v_xor_b32 v246, 0xc0, v246\n\t"
"v_fma_f32 v99, -v45, s56, -v143\n\t"
"v_mfma_f32_16x16x32_bf16 v[196:199], a[4:7], a[172:175], v[196:199]\n\t"
"v_accvgpr_write_b32 a58, v98\n\t"
"v_accvgpr_write_b32 a59, v99\n\t"
"v_mfma_f32_16x16x32_bf16 v[196:199], a[12:15], a[176:179], v[196:199]\n\t"
"v_fma_f32 v98, -v46, s56, -v144\n\t"
"v_fma_f32 v99, -v47, s56, -v145\n\t"
"v_mfma_f32_16x16x32_bf16 v[204:207], a[136:139], a[172:175], v[204:207]\n\t"
"s_waitcnt lgkmcnt(5)\n\t"
"v_accvgpr_write_b32 a64, v98\n\t"
"v_fma_f32 v98, -v48, s56, -v146\n\t"
"v_mfma_f32_16x16x32_bf16 v[204:207], a[4:7], a[176:179], v[204:207]\n\t"
"v_accvgpr_write_b32 a65, v99\n\t"
"v_fma_f32 v99, -v49, s56, -v147\n\t"
"v_mfma_f32_16x16x32_bf16 v[204:207], a[12:15], a[180:183], v[204:207]\n\t"
"v_accvgpr_write_b32 a66, v98\n\t"
"v_accvgpr_write_b32 a67, v99\n\t"
"v_mfma_f32_16x16x32_bf16 v[212:215], a[136:139], a[176:179], v[212:215]\n\t"
"s_waitcnt lgkmcnt(2)\n\t"
"v_fma_f32 v98, -v50, s56, -v148\n\t"
"v_fma_f32 v99, -v51, s56, -v149\n\t"
"v_mfma_f32_16x16x32_bf16 v[212:215], a[4:7], a[180:183], v[212:215]\n\t"
"v_accvgpr_write_b32 a72, v98\n\t"
"v_fma_f32 v98, -v52, s56, -v150\n\t"
"v_mfma_f32_16x16x32_bf16 v[212:215], a[12:15], a[184:187], v[212:215]\n\t"
"Lpg2_%=:\n\t"
"s_cbranch_vccnz Lpg6_%=\n\t"
"v_accvgpr_write_b32 a73, v99\n\t"
"v_fma_f32 v99, -v53, s56, -v151\n\t"
"v_mfma_f32_16x16x32_bf16 a[0:3], a[136:139], a[180:183], a[0:3]\n\t"
"v_accvgpr_write_b32 a74, v98\n\t"
"v_accvgpr_write_b32 a75, v99\n\t"
"v_mfma_f32_16x16x32_bf16 a[0:3], a[4:7], a[184:187], a[0:3]\n\t"
"v_fma_f32 v98, -v54, s56, -v152\n\t"
"v_fma_f32 v99, -v55, s56, -v153\n\t"
"v_mfma_f32_16x16x32_bf16 a[0:3], a[12:15], a[188:191], a[0:3]\n\t"
"v_accvgpr_write_b32 a80, v98\n\t"
"v_fma_f32 v98, -v56, s56, -v154\n\t"
"v_mfma_f32_16x16x32_bf16 a[8:11], a[136:139], a[184:187], a[8:11]\n\t"
"v_accvgpr_write_b32 a81, v99\n\t"
"v_fma_f32 v99, -v57, s56, -v155\n\t"
"v_mfma_f32_16x16x32_bf16 a[8:11], a[4:7], a[188:191], a[8:11]\n\t"
"v_accvgpr_write_b32 a82, v98\n\t"
"v_accvgpr_write_b32 a83, v99\n\t"
"v_mfma_f32_16x16x32_bf16 a[8:11], a[12:15], a[192:195], a[8:11]\n\t"
"ds_read_b64_tr_b16 v[228:229], v246 offset:0\n\t"
"v_fma_f32 v98, -v58, s56, -v156\n\t"
"v_mfma_f32_16x16x32_bf16 a[16:19], a[136:139], a[188:191], a[16:19]\n\t"
"s_waitcnt lgkmcnt(2)\n\t"
"ds_read_b64_tr_b16 v[230:231], v246 offset:3072\n\t"
"v_fma_f32 v99, -v59, s56, -v157\n\t"
"v_mfma_f32_16x16x32_bf16 a[16:19], a[4:7], a[192:195], a[16:19]\n\t"
"v_accvgpr_write_b32 a88, v98\n\t"
"v_fma_f32 v98, -v60, s56, -v158\n\t"
"v_mfma_f32_16x16x32_bf16 a[16:19], a[12:15], a[196:199], a[16:19]\n\t"
"v_accvgpr_write_b32 a89, v99\n\t"
"v_fma_f32 v99, -v61, s56, -v159\n\t"
"v_mfma_f32_16x16x32_bf16 a[24:27], a[136:139], a[192:195], a[24:27]\n\t"
"s_waitcnt lgkmcnt(2)\n\t"
"v_accvgpr_write_b32 a90, v98\n\t"
"v_accvgpr_write_b32 a91, v99\n\t"
"v_mfma_f32_16x16x32_bf16 a[24:27], a[4:7], a[196:199], a[24:27]\n\t"
"v_fma_f32 v98, -v62, s56, -v160\n\t"
"v_fma_f32 v99, -v63, s56, -v161\n\t"
"v_mfma_f32_16x16x32_bf16 a[24:27], a[12:15], a[200:203], a[24:27]\n\t"
"v_accvgpr_write_b32 a96, v98\n\t"
"v_fma_f32 v98, -v64, s56, -v162\n\t"
"v_mfma_f32_16x16x32_bf16 a[32:35], a[136:139], a[196:199], a[32:35]\n\t"
"s_waitcnt vmcnt(21)\n\t"
"ds_read_b64_tr_b16 v[232:233], v246 offset:6144\n\t"
"v_accvgpr_write_b32 a97, v99\n\t"
"v_mfma_f32_16x16x32_bf16 a[32:35], a[4:7], a[200:203], a[32:35]\n\t"
"ds_read_b64_tr_b16 v[234:235], v246 offset:9216\n\t"
"v_fma_f32 v99, -v65, s56, -v163\n\t"
"v_mfma_f32_16x16x32_bf16 a[32:35], a[12:15], a[204:207], a[32:35]\n\t"
"v_accvgpr_write_b32 a98, v98\n\t"
"v_accvgpr_write_b32 a99, v99\n\t"
"v_mfma_f32_16x16x32_bf16 a[40:43], a[20:23], a[168:171], a[40:43]\n\t"
"v_fma_f32 v98, -v66, s56, -v164\n\t"
"v_fma_f32 v99, -v67, s56, -v165\n\t"
"v_mfma_f32_16x16x32_bf16 a[40:43], a[28:31], a[172:175], a[40:43]\n\t"
"v_accvgpr_write_b32 a104, v98\n\t"
"v_fma_f32 v98, -v68, s56, -v166\n\t"
"v_mfma_f32_16x16x32_bf16 a[40:43], a[36:39], a[176:179], a[40:43]\n\t"
"v_accvgpr_write_b32 a105, v99\n\t"
"v_fma_f32 v99, -v69, s56, -v167\n\t"
"v_mfma_f32_16x16x32_bf16 a[48:51], a[20:23], a[172:175], a[48:51]\n\t"
"s_waitcnt lgkmcnt(2)\n\t"
"v_accvgpr_write_b32 a106, v98\n\t"
"v_accvgpr_write_b32 a107, v99\n\t"
"v_mfma_f32_16x16x32_bf16 a[48:51], a[28:31], a[176:179], a[48:51]\n\t"
"v_fma_f32 v98, -v70, s56, -v168\n\t"
"v_fma_f32 v99, -v71, s56, -v169\n\t"
"v_mfma_f32_16x16x32_bf16 a[48:51], a[36:39], a[180:183], a[48:51]\n\t"
"v_accvgpr_write_b32 a112, v98\n\t"
"v_fma_f32 v98, -v72, s56, -v170\n\t"
"v_mfma_f32_16x16x32_bf16 v[228:231], v[228:231], v[220:223], 0\n\t"
"v_accvgpr_write_b32 a113, v99\n\t"
"v_fma_f32 v99, -v73, s56, -v171\n\t"
"v_mfma_f32_16x16x32_bf16 a[56:59], a[20:23], a[176:179], a[56:59]\n\t"
"ds_read_b64_tr_b16 v[224:225], v246 offset:12288\n\t"
"v_accvgpr_write_b32 a114, v98\n\t"
"v_mfma_f32_16x16x32_bf16 a[56:59], a[28:31], a[180:183], a[56:59]\n\t"
"ds_read_b64_tr_b16 v[226:227], v246 offset:15360\n\t"
"v_accvgpr_write_b32 a115, v99\n\t"
"v_mfma_f32_16x16x32_bf16 a[56:59], a[36:39], a[184:187], a[56:59]\n\t"
"v_fma_f32 v98, -v74, s56, -v172\n\t"
"v_fma_f32 v99, -v75, s56, -v173\n\t"
"v_mfma_f32_16x16x32_bf16 a[64:67], a[20:23], a[180:183], a[64:67]\n\t"
"s_waitcnt lgkmcnt(2)\n\t"
"v_accvgpr_write_b32 a120, v98\n\t"
"v_fma_f32 v98, -v76, s56, -v174\n\t"
"v_mfma_f32_16x16x32_bf16 a[64:67], a[28:31], a[184:187], a[64:67]\n\t"
"v_cvt_pk_bf16_f32 v228, v228, v229\n\t"
"v_cvt_pk_bf16_f32 v229, v230, v231\n\t"
"v_mfma_f32_16x16x32_bf16 a[64:67], a[36:39], a[188:191], a[64:67]\n\t"
"v_accvgpr_write_b32 a121, v99\n\t"
"v_fma_f32 v99, -v77, s56, -v175\n\t"
"v_mfma_f32_16x16x32_bf16 v[232:235], v[232:235], v[220:223], 0\n\t"
"v_accvgpr_write_b32 a122, v98\n\t"
"v_accvgpr_write_b32 a123, v99\n\t"
"v_mfma_f32_16x16x32_bf16 a[72:75], a[20:23], a[184:187], a[72:75]\n\t"
"v_fma_f32 v98, -v78, s56, -v176\n\t"
"v_fma_f32 v99, -v79, s56, -v177\n\t"
"v_mfma_f32_16x16x32_bf16 a[72:75], a[28:31], a[188:191], a[72:75]\n\t"
"v_accvgpr_write_b32 a128, v98\n\t"
"v_fma_f32 v98, -v80, s56, -v178\n\t"
"v_mfma_f32_16x16x32_bf16 a[72:75], a[36:39], a[192:195], a[72:75]\n\t"
"v_accvgpr_write_b32 a129, v99\n\t"
"v_fma_f32 v99, -v81, s56, -v179\n\t"
"v_mfma_f32_16x16x32_bf16 a[80:83], a[20:23], a[188:191], a[80:83]\n\t"
"v_accvgpr_write_b32 a130, v98\n\t"
"v_accvgpr_write_b32 a131, v99\n\t"
"v_mfma_f32_16x16x32_bf16 a[80:83], a[28:31], a[192:195], a[80:83]\n\t"
"v_cvt_pk_bf16_f32 v230, v232, v233\n\t"
"v_cvt_pk_bf16_f32 v231, v234, v235\n\t"
"v_mfma_f32_16x16x32_bf16 a[80:83], a[36:39], a[196:199], a[80:83]\n\t"
"ds_read_b64_tr_b16 v[232:233], v246 offset:19968\n\t"
"v_fma_f32 v98, -v82, s56, -v180\n\t"
"v_mfma_f32_16x16x32_bf16 a[88:91], a[20:23], a[192:195], a[88:91]\n\t"
"s_waitcnt lgkmcnt(1)\n\t"
"ds_read_b64_tr_b16 v[234:235], v246 offset:23040\n\t"
"v_fma_f32 v99, -v83, s56, -v181\n\t"
"v_mfma_f32_16x16x32_bf16 a[88:91], a[28:31], a[196:199], a[88:91]\n\t"
"v_accvgpr_write_b32 a136, v98\n\t"
"v_fma_f32 v98, -v84, s56, -v182\n\t"
"v_mfma_f32_16x16x32_bf16 a[88:91], a[36:39], a[200:203], a[88:91]\n\t"
"v_accvgpr_write_b32 a137, v99\n\t"
"v_fma_f32 v99, -v85, s56, -v183\n\t"
"v_mfma_f32_16x16x32_bf16 v[224:227], v[224:227], v[220:223], 0\n\t"
"v_accvgpr_write_b32 a138, v98\n\t"
"v_accvgpr_write_b32 a139, v99\n\t"
"v_mfma_f32_16x16x32_bf16 a[96:99], a[20:23], a[196:199], a[96:99]\n\t"
"s_waitcnt vmcnt(18)\n\t"
"v_fma_f32 v98, -v86, s56, -v184\n\t"
"v_fma_f32 v99, -v87, s56, -v185\n\t"
"v_mfma_f32_16x16x32_bf16 a[96:99], a[28:31], a[200:203], a[96:99]\n\t"
"v_accvgpr_write_b32 a144, v98\n\t"
"v_fma_f32 v98, -v88, s56, -v186\n\t"
"v_mfma_f32_16x16x32_bf16 a[96:99], a[36:39], a[204:207], a[96:99]\n\t"
"v_accvgpr_write_b32 a145, v99\n\t"
"v_fma_f32 v99, -v89, s56, -v187\n\t"
"v_mfma_f32_16x16x32_bf16 a[104:107], a[76:79], a[168:171], a[104:107]\n\t"
"ds_read_b128 a[168:171], v244 offset:9216\n\t"
"v_accvgpr_write_b32 a146, v98\n\t"
"v_mfma_f32_16x16x32_bf16 a[104:107], a[84:87], a[172:175], a[104:107]\n\t"
"v_cvt_pk_bf16_f32 v224, v224, v225\n\t"
"v_cvt_pk_bf16_f32 v225, v226, v227\n\t"
"v_mfma_f32_16x16x32_bf16 a[104:107], a[92:95], a[176:179], a[104:107]\n\t"
"v_accvgpr_write_b32 a147, v99\n\t"
"v_fma_f32 v98, -v90, s56, -v188\n\t"
"v_mfma_f32_16x16x32_bf16 a[112:115], a[76:79], a[172:175], a[112:115]\n\t"
"s_waitcnt lgkmcnt(1)\n\t"
"ds_read_b128 a[172:175], v244 offset:9728\n\t"
"v_fma_f32 v99, -v91, s56, -v189\n\t"
"v_mfma_f32_16x16x32_bf16 a[112:115], a[84:87], a[176:179], a[112:115]\n\t"
"v_accvgpr_write_b32 a152, v98\n\t"
"v_fma_f32 v98, -v92, s56, -v190\n\t"
"v_mfma_f32_16x16x32_bf16 a[112:115], a[92:95], a[180:183], a[112:115]\n\t"
"v_accvgpr_write_b32 a153, v99\n\t"
"v_fma_f32 v99, -v93, s56, -v191\n\t"
"v_mfma_f32_16x16x32_bf16 v[232:235], v[232:235], v[220:223], 0\n\t"
"v_accvgpr_write_b32 a154, v98\n\t"
"v_accvgpr_write_b32 a155, v99\n\t"
"v_mfma_f32_16x16x32_bf16 a[120:123], a[76:79], a[176:179], a[120:123]\n\t"
"ds_read_b128 a[176:179], v244 offset:10240\n\t"
"v_fma_f32 v98, -v94, s56, -v192\n\t"
"v_mfma_f32_16x16x32_bf16 a[120:123], a[84:87], a[180:183], a[120:123]\n\t"
"v_fma_f32 v99, -v95, s56, -v193\n\t"
"v_accvgpr_write_b32 a160, v98\n\t"
"v_mfma_f32_16x16x32_bf16 a[120:123], a[92:95], a[184:187], a[120:123]\n\t"
"v_fma_f32 v98, -v96, s56, -v194\n\t"
"v_accvgpr_write_b32 a161, v99\n\t"
"v_mfma_f32_16x16x32_bf16 a[128:131], a[76:79], a[180:183], a[128:131]\n\t"
"ds_read_b128 a[180:183], v244 offset:10752\n\t"
"v_fma_f32 v99, -v97, s56, -v195\n\t"
"v_mfma_f32_16x16x32_bf16 a[128:131], a[84:87], a[184:187], a[128:131]\n\t"
"v_cvt_pk_bf16_f32 v226, v232, v233\n\t"
"v_cvt_pk_bf16_f32 v227, v234, v235\n\t"
"v_mfma_f32_16x16x32_bf16 a[128:131], a[92:95], a[188:191], a[128:131]\n\t"
"ds_read_b64_tr_b16 v[232:233], v246 offset:26112\n\t"
"v_accvgpr_write_b32 a162, v98\n\t"
"v_mfma_f32_16x16x32_bf16 a[136:139], a[76:79], a[184:187], a[136:139]\n\t"
"ds_read_b64_tr_b16 v[234:235], v246 offset:29184\n\t"
"v_accvgpr_write_b32 a163, v99\n\t"
"v_mfma_f32_16x16x32_bf16 a[136:139], a[84:87], a[188:191], a[136:139]\n\t"
"ds_read_b128 a[184:187], v244 offset:11264\n\t"
"v_fma_f32 v200, v100, s64, -v2\n\t"
"v_mfma_f32_16x16x32_bf16 a[136:139], a[92:95], a[192:195], a[136:139]\n\t"
"v_fma_f32 v201, v101, s64, -v3\n\t"
"v_fma_f32 v202, v102, s64, -v4\n\t"
"v_mfma_f32_16x16x32_bf16 a[144:147], a[76:79], a[188:191], a[144:147]\n\t"
"ds_read_b128 a[188:191], v244 offset:11776\n\t"
"v_fma_f32 v203, v103, s64, -v5\n\t"
"v_mfma_f32_16x16x32_bf16 a[144:147], a[84:87], a[192:195], a[144:147]\n\t"
"v_fma_f32 v208, v104, s64, -v6\n\t"
"v_fma_f32 v209, v105, s64, -v7\n\t"
"v_mfma_f32_16x16x32_bf16 a[144:147], a[92:95], a[196:199], a[144:147]\n\t"
"v_fma_f32 v210, v106, s64, -v8\n\t"
"v_fma_f32 v211, v107, s64, -v9\n\t"
"v_mfma_f32_16x16x32_bf16 a[152:155], a[76:79], a[192:195], a[152:155]\n\t"
"ds_read_b128 a[192:195], v244 offset:12288\n\t"
"v_fma_f32 v216, v108, s64, -v10\n\t"
"v_mfma_f32_16x16x32_bf16 a[152:155], a[84:87], a[196:199], a[152:155]\n\t"
"v_fma_f32 v217, v109, s64, -v11\n\t"
"v_fma_f32 v218, v110, s64, -v12\n\t"
"v_mfma_f32_16x16x32_bf16 a[152:155], a[92:95], a[200:203], a[152:155]\n\t"
"v_fma_f32 v219, v111, s64, -v13\n\t"
"v_fma_f32 v98, v112, s64, -v14\n\t"
"v_mfma_f32_16x16x32_bf16 a[160:163], a[76:79], a[196:199], a[160:163]\n\t"
"s_waitcnt lgkmcnt(3)\n\t"
"ds_read_b128 a[196:199], v244 offset:12800\n\t"
"v_fma_f32 v99, v113, s64, -v15\n\t"
"v_mfma_f32_16x16x32_bf16 a[160:163], a[84:87], a[200:203], a[160:163]\n\t"
"s_waitcnt vmcnt(15)\n\t"
"ds_read_b128 a[200:203], v244 offset:13312\n\t"
"v_accvgpr_write_b32 a4, v98\n\t"
"v_mfma_f32_16x16x32_bf16 a[160:163], a[92:95], a[204:207], a[160:163]\n\t"
"ds_read_b128 a[204:207], v244 offset:13824\n\t"
"v_fma_f32 v98, v114, s64, -v16\n\t"
"v_mfma_f32_16x16x32_bf16 v[232:235], v[232:235], v[220:223], 0\n\t"
"v_accvgpr_write_b32 a5, v99\n\t"
"v_fma_f32 v99, v115, s64, -v17\n\t"
"v_mfma_f32_16x16x32_bf16 v[200:203], a[148:151], a[168:171], v[200:203]\n\t"
"v_accvgpr_write_b32 a6, v98\n\t"
"v_accvgpr_write_b32 a7, v99\n\t"
"v_mfma_f32_16x16x32_bf16 v[200:203], a[156:159], a[172:175], v[200:203]\n\t"
"v_fma_f32 v98, v116, s64, -v18\n\t"
"v_fma_f32 v99, v117, s64, -v19\n\t"
"v_mfma_f32_16x16x32_bf16 v[200:203], a[164:167], a[176:179], v[200:203]\n\t"
"s_add_u32 m0, s24, 896\n\t"
"s_add_u32 s28, s21, 0\n\t"
"v_accvgpr_write_b32 a12, v98\n\t"
"v_mfma_f32_16x16x32_bf16 v[208:211], a[148:151], a[172:175], v[208:211]\n\t"
"s_waitcnt lgkmcnt(5)\n\t"
"v_fma_f32 v98, v118, s64, -v20\n\t"
"v_accvgpr_write_b32 a13, v99\n\t"
"v_mfma_f32_16x16x32_bf16 v[208:211], a[156:159], a[176:179], v[208:211]\n\t"
"v_fma_f32 v99, v119, s64, -v21\n\t"
"v_mfma_f32_16x16x32_bf16 v[208:211], a[164:167], a[180:183], v[208:211]\n\t"
"s_waitcnt lgkmcnt(0)\n\t"
"s_barrier\n\t"
"v_mfma_f32_16x16x32_bf16 v[216:219], a[148:151], a[176:179], v[216:219]\n\t"
"v_cvt_pk_bf16_f32 v232, v232, v233\n\t"
"v_mfma_f32_16x16x32_bf16 v[216:219], a[156:159], a[180:183], v[216:219]\n\t"
"buffer_load_dwordx4 v247, s[12:15], s28 offen offset:128 nt lds\n\t"
"v_mfma_f32_16x16x32_bf16 v[216:219], a[164:167], a[184:187], v[216:219]\n\t"
"s_add_u32 m0, s24, 7040\n\t"
"s_add_u32 s28, s21, 196608\n\t"
"ds_write2st64_b64 v245, v[228:229], v[230:231] offset0:0 offset1:1\n\t"
"v_cvt_pk_bf16_f32 v233, v234, v235\n\t"
"v_mfma_f32_16x16x32_bf16 a[4:7], a[148:151], a[180:183], a[4:7]\n\t"
"v_accvgpr_write_b32 a14, v98\n\t"
"v_accvgpr_write_b32 a15, v99\n\t"
"v_mfma_f32_16x16x32_bf16 a[4:7], a[156:159], a[184:187], a[4:7]\n\t"
"ds_read_b64_tr_b16 v[228:229], v246 offset:32256\n\t"
"v_fma_f32 v98, v120, s64, -v22\n\t"
"v_mfma_f32_16x16x32_bf16 a[4:7], a[164:167], a[188:191], a[4:7]\n\t"
"buffer_load_dwordx4 v247, s[12:15], s28 offen offset:128 nt lds\n\t"
"v_fma_f32 v99, v121, s64, -v23\n\t"
"s_add_u32 m0, s24, 13184\n\t"
"s_add_u32 s28, s21, 393216\n\t"
"v_mfma_f32_16x16x32_bf16 a[12:15], a[148:151], a[184:187], a[12:15]\n\t"
"ds_read_b64_tr_b16 v[230:231], v246 offset:35328\n\t"
"v_accvgpr_write_b32 a20, v98\n\t"
"v_mfma_f32_16x16x32_bf16 a[12:15], a[156:159], a[188:191], a[12:15]\n\t"
"v_fma_f32 v98, v122, s64, -v24\n\t"
"v_accvgpr_write_b32 a21, v99\n\t"
"v_mfma_f32_16x16x32_bf16 a[12:15], a[164:167], a[192:195], a[12:15]\n\t"
"ds_write2st64_b64 v245, v[224:225], v[226:227] offset0:2 offset1:3\n\t"
"buffer_load_dwordx4 v247, s[12:15], s28 offen offset:128 nt lds\n\t"
"v_fma_f32 v99, v123, s64, -v25\n\t"
"v_accvgpr_write_b32 a22, v98\n\t"
"v_accvgpr_write_b32 a23, v99\n\t"
"v_fma_f32 v98, v124, s64, -v26\n\t"
"v_fma_f32 v99, v125, s64, -v27\n\t"
"s_add_u32 m0, s24, 20864\n\t"
"s_add_u32 s28, s21, 589824\n\t"
"v_mfma_f32_16x16x32_bf16 a[20:23], a[148:151], a[188:191], a[20:23]\n\t"
"buffer_load_dwordx4 v247, s[12:15], s28 offen offset:128 nt lds\n\t"
"v_mfma_f32_16x16x32_bf16 a[20:23], a[156:159], a[192:195], a[20:23]\n\t"
"s_add_u32 m0, s24, 60800\n\t"
"s_add_u32 s28, s21, 1769472\n\t"
"ds_read_b64_tr_b16 v[224:225], v246 offset:39936\n\t"
"v_mfma_f32_16x16x32_bf16 a[20:23], a[164:167], a[196:199], a[20:23]\n\t"
"s_waitcnt lgkmcnt(2)\n\t"
"ds_write_b64 v245, v[232:233] offset:2048\n\t"
"ds_read_b64_tr_b16 v[226:227], v246 offset:43008\n\t"
"v_accvgpr_write_b32 a28, v98\n\t"
"v_fma_f32 v98, v126, s64, -v28\n\t"
"v_accvgpr_write_b32 a29, v99\n\t"
"v_fma_f32 v99, v127, s64, -v29\n\t"
"v_accvgpr_write_b32 a30, v98\n\t"
"v_accvgpr_write_b32 a31, v99\n\t"
"v_mfma_f32_16x16x32_bf16 v[228:231], v[228:231], v[220:223], 0\n\t"
"buffer_load_dwordx4 v247, s[12:15], s28 offen offset:128 nt lds\n\t"
"v_fma_f32 v98, v128, s64, -v30\n\t"
"v_fma_f32 v99, v129, s64, -v31\n\t"
"s_add_u32 m0, s24, 66944\n\t"
"s_add_u32 s28, s21, 1966080\n\t"
"v_mfma_f32_16x16x32_bf16 a[28:31], a[148:151], a[192:195], a[28:31]\n\t"
"buffer_load_dwordx4 v247, s[12:15], s28 offen offset:128 nt lds\n\t"
"v_mfma_f32_16x16x32_bf16 a[28:31], a[156:159], a[196:199], a[28:31]\n\t"
"ds_read_b64_tr_b16 v[232:233], v246 offset:46080\n\t"
"v_accvgpr_write_b32 a36, v98\n\t"
"v_mfma_f32_16x16x32_bf16 a[28:31], a[164:167], a[200:203], a[28:31]\n\t"
"s_waitcnt lgkmcnt(1)\n\t"
"ds_read_b64_tr_b16 v[234:235], v246 offset:49152\n\t"
"v_fma_f32 v98, v130, s64, -v32\n\t"
"v_accvgpr_write_b32 a37, v99\n\t"
"v_fma_f32 v99, v131, s64, -v33\n\t"
"v_accvgpr_write_b32 a38, v98\n\t"
"v_accvgpr_write_b32 a39, v99\n\t"
"v_mfma_f32_16x16x32_bf16 v[224:227], v[224:227], v[220:223], 0\n\t"
"v_fma_f32 v98, v132, s64, -v34\n\t"
"v_fma_f32 v99, v133, s64, -v35\n\t"
"v_mfma_f32_16x16x32_bf16 a[36:39], a[148:151], a[196:199], a[36:39]\n\t"
"v_accvgpr_write_b32 a44, v98\n\t"
"v_fma_f32 v98, v134, s64, -v36\n\t"
"v_mfma_f32_16x16x32_bf16 a[36:39], a[156:159], a[200:203], a[36:39]\n\t"
"v_accvgpr_write_b32 a45, v99\n\t"
"v_fma_f32 v99, v135, s64, -v37\n\t"
"v_mfma_f32_16x16x32_bf16 a[36:39], a[164:167], a[204:207], a[36:39]\n\t"
"s_waitcnt lgkmcnt(0)\n\t"
"v_accvgpr_write_b32 a46, v98\n\t"
"v_accvgpr_write_b32 a47, v99\n\t"
"v_fma_f32 v98, v136, s64, -v38\n\t"
"v_fma_f32 v99, v137, s64, -v39\n\t"
"v_mfma_f32_16x16x32_bf16 v[232:235], v[232:235], v[220:223], 0\n\t"
"s_waitcnt vmcnt(18)\n\t"
"v_cvt_pk_bf16_f32 v228, v228, v229\n\t"
"v_cvt_pk_bf16_f32 v229, v230, v231\n\t"
"v_mfma_f32_16x16x32_bf16 a[44:47], v[240:243], a[168:171], a[44:47]\n\t"
"v_accvgpr_write_b32 a52, v98\n\t"
"v_fma_f32 v98, v138, s64, -v40\n\t"
"v_mfma_f32_16x16x32_bf16 a[44:47], a[208:211], a[172:175], a[44:47]\n\t"
"v_accvgpr_write_b32 a53, v99\n\t"
"v_fma_f32 v99, v139, s64, -v41\n\t"
"v_mfma_f32_16x16x32_bf16 a[44:47], v[248:251], a[176:179], a[44:47]\n\t"
"v_accvgpr_write_b32 a54, v98\n\t"
"v_accvgpr_write_b32 a55, v99\n\t"
"v_fma_f32 v98, v140, s64, -v42\n\t"
"v_fma_f32 v99, v141, s64, -v43\n\t"
"v_mfma_f32_16x16x32_bf16 a[52:55], v[240:243], a[172:175], a[52:55]\n\t"
"v_accvgpr_write_b32 a60, v98\n\t"
"v_fma_f32 v98, v142, s64, -v44\n\t"
"v_mfma_f32_16x16x32_bf16 a[52:55], a[208:211], a[176:179], a[52:55]\n\t"
"v_accvgpr_write_b32 a61, v99\n\t"
"v_fma_f32 v99, v143, s64, -v45\n\t"
"v_mfma_f32_16x16x32_bf16 a[52:55], v[248:251], a[180:183], a[52:55]\n\t"
"v_accvgpr_write_b32 a62, v98\n\t"
"v_accvgpr_write_b32 a63, v99\n\t"
"v_cvt_pk_bf16_f32 v230, v224, v225\n\t"
"v_cvt_pk_bf16_f32 v231, v226, v227\n\t"
"v_fma_f32 v98, v144, s64, -v46\n\t"
"v_fma_f32 v99, v145, s64, -v47\n\t"
"v_mfma_f32_16x16x32_bf16 a[60:63], v[240:243], a[176:179], a[60:63]\n\t"
"ds_read_b64_tr_b16 v[224:225], v246 offset:52224\n\t"
"v_accvgpr_write_b32 a68, v98\n\t"
"v_mfma_f32_16x16x32_bf16 a[60:63], a[208:211], a[180:183], a[60:63]\n\t"
"ds_read_b64_tr_b16 v[226:227], v246 offset:55296\n\t"
"v_fma_f32 v98, v146, s64, -v48\n\t"
"v_mfma_f32_16x16x32_bf16 a[60:63], v[248:251], a[184:187], a[60:63]\n\t"
"ds_write2st64_b64 v245, v[228:229], v[230:231] offset0:5 offset1:6\n\t"
"v_accvgpr_write_b32 a69, v99\n\t"
"v_fma_f32 v99, v147, s64, -v49\n\t"
"v_accvgpr_write_b32 a70, v98\n\t"
"v_accvgpr_write_b32 a71, v99\n\t"
"v_fma_f32 v98, v148, s64, -v50\n\t"
"v_fma_f32 v99, v149, s64, -v51\n\t"
"v_cvt_pk_bf16_f32 v232, v232, v233\n\t"
"v_cvt_pk_bf16_f32 v233, v234, v235\n\t"
"v_mfma_f32_16x16x32_bf16 a[68:71], v[240:243], a[180:183], a[68:71]\n\t"
"v_accvgpr_write_b32 a76, v98\n\t"
"v_fma_f32 v98, v150, s64, -v52\n\t"
"v_mfma_f32_16x16x32_bf16 a[68:71], a[208:211], a[184:187], a[68:71]\n\t"
"v_accvgpr_write_b32 a77, v99\n\t"
"v_fma_f32 v99, v151, s64, -v53\n\t"
"v_mfma_f32_16x16x32_bf16 a[68:71], v[248:251], a[188:191], a[68:71]\n\t"
"v_accvgpr_write_b32 a78, v98\n\t"
"v_accvgpr_write_b32 a79, v99\n\t"
"v_fma_f32 v98, v152, s64, -v54\n\t"
"v_fma_f32 v99, v153, s64, -v55\n\t"
"v_mfma_f32_16x16x32_bf16 a[76:79], v[240:243], a[184:187], a[76:79]\n\t"
"s_waitcnt lgkmcnt(1)\n\t"
"v_accvgpr_write_b32 a84, v98\n\t"
"v_fma_f32 v98, v154, s64, -v56\n\t"
"v_mfma_f32_16x16x32_bf16 a[76:79], a[208:211], a[188:191], a[76:79]\n\t"
"v_accvgpr_write_b32 a85, v99\n\t"
"v_fma_f32 v99, v155, s64, -v57\n\t"
"v_mfma_f32_16x16x32_bf16 a[76:79], v[248:251], a[192:195], a[76:79]\n\t"
"v_accvgpr_write_b32 a86, v98\n\t"
"v_accvgpr_write_b32 a87, v99\n\t"
"v_fma_f32 v98, v156, s64, -v58\n\t"
"v_fma_f32 v99, v157, s64, -v59\n\t"
"v_mfma_f32_16x16x32_bf16 v[224:227], v[224:227], v[220:223], 0\n\t"
"v_accvgpr_write_b32 a92, v98\n\t"
"v_fma_f32 v98, v158, s64, -v60\n\t"
"v_mfma_f32_16x16x32_bf16 a[84:87], v[240:243], a[188:191], a[84:87]\n\t"
"s_mov_b32 s20, 73728\n\t"
"v_accvgpr_write_b32 a93, v99\n\t"
"v_fma_f32 v99, v159, s64, -v61\n\t"
"v_mfma_f32_16x16x32_bf16 a[84:87], a[208:211], a[192:195], a[84:87]\n\t"
"v_accvgpr_write_b32 a94, v98\n\t"
"v_accvgpr_write_b32 a95, v99\n\t"
"v_mfma_f32_16x16x32_bf16 a[84:87], v[248:251], a[196:199], a[84:87]\n\t"
"v_fma_f32 v98, v160, s64, -v62\n\t"
"v_fma_f32 v99, v161, s64, -v63\n\t"
"v_mfma_f32_16x16x32_bf16 a[92:95], v[240:243], a[192:195], a[92:95]\n\t"
"v_accvgpr_write_b32 a100, v98\n\t"
"v_fma_f32 v98, v162, s64, -v64\n\t"
"v_mfma_f32_16x16x32_bf16 a[92:95], a[208:211], a[196:199], a[92:95]\n\t"
"v_accvgpr_write_b32 a101, v99\n\t"
"v_fma_f32 v99, v163, s64, -v65\n\t"
"v_mfma_f32_16x16x32_bf16 a[92:95], v[248:251], a[200:203], a[92:95]\n\t"
"v_accvgpr_write_b32 a102, v98\n\t"
"v_accvgpr_write_b32 a103, v99\n\t"
"v_fma_f32 v98, v164, s64, -v66\n\t"
"v_fma_f32 v99, v165, s64, -v67\n\t"
"v_cvt_pk_bf16_f32 v234, v224, v225\n\t"
"v_cvt_pk_bf16_f32 v235, v226, v227\n\t"
"v_mfma_f32_16x16x32_bf16 a[100:103], v[240:243], a[196:199], a[100:103]\n\t"
"v_accvgpr_write_b32 a108, v98\n\t"
"v_fma_f32 v98, v166, s64, -v68\n\t"
"v_mfma_f32_16x16x32_bf16 a[100:103], a[208:211], a[200:203], a[100:103]\n\t"
"v_accvgpr_write_b32 a109, v99\n\t"
"v_fma_f32 v99, v167, s64, -v69\n\t"
"v_mfma_f32_16x16x32_bf16 a[100:103], v[248:251], a[204:207], a[100:103]\n\t"
"ds_write2st64_b64 v245, v[232:233], v[234:235] offset0:7 offset1:8\n\t"
"v_accvgpr_write_b32 a110, v98\n\t"
"v_accvgpr_write_b32 a111, v99\n\t"
"v_fma_f32 v98, v168, s64, -v70\n\t"
"v_fma_f32 v99, v169, s64, -v71\n\t"
"s_waitcnt vmcnt(15)\n\t"
"v_mfma_f32_16x16x32_bf16 a[108:111], a[212:215], a[168:171], a[108:111]\n\t"
"buffer_load_dwordx4 v[240:243], v252, s[8:11], s20 offen offset:0 sc1\n\t"
"v_mfma_f32_16x16x32_bf16 a[108:111], a[216:219], a[172:175], a[108:111]\n\t"
"v_accvgpr_write_b32 a116, v98\n\t"
"v_mfma_f32_16x16x32_bf16 a[108:111], a[220:223], a[176:179], a[108:111]\n\t"
"v_accvgpr_write_b32 a117, v99\n\t"
"v_fma_f32 v98, v170, s64, -v72\n\t"
"v_accvgpr_write_b32 a118, v98\n\t"
"v_fma_f32 v99, v171, s64, -v73\n\t"
"v_accvgpr_write_b32 a119, v99\n\t"
"s_waitcnt lgkmcnt(0)\n\t"
"s_barrier\n\t"
"v_mfma_f32_16x16x32_bf16 a[116:119], a[212:215], a[172:175], a[116:119]\n\t"
"ds_read_b128 a[168:171], v244 offset:0\n\t"
"v_mfma_f32_16x16x32_bf16 a[116:119], a[216:219], a[176:179], a[116:119]\n\t"
"buffer_load_dwordx4 a[208:211], v252, s[8:11], s20 offen offset:1024 sc1\n\t"
"v_mfma_f32_16x16x32_bf16 a[116:119], a[220:223], a[180:183], a[116:119]\n\t"
"ds_read_b128 a[172:175], v244 offset:512\n\t"
"v_fma_f32 v98, v172, s64, -v74\n\t"
"v_fma_f32 v99, v173, s64, -v75\n\t"
"v_accvgpr_write_b32 a124, v98\n\t"
"v_fma_f32 v98, v174, s64, -v76\n\t"
"v_accvgpr_write_b32 a125, v99\n\t"
"v_fma_f32 v99, v175, s64, -v77\n\t"
"v_accvgpr_write_b32 a126, v98\n\t"
"v_accvgpr_write_b32 a127, v99\n\t"
"v_fma_f32 v98, v176, s64, -v78\n\t"
"v_fma_f32 v99, v177, s64, -v79\n\t"
"v_mfma_f32_16x16x32_bf16 a[124:127], a[212:215], a[176:179], a[124:127]\n\t"
"ds_read_b128 a[176:179], v244 offset:1024\n\t"
"v_accvgpr_write_b32 a132, v98\n\t"
"v_mfma_f32_16x16x32_bf16 a[124:127], a[216:219], a[180:183], a[124:127]\n\t"
"v_fma_f32 v98, v178, s64, -v80\n\t"
"v_accvgpr_write_b32 a133, v99\n\t"
"v_mfma_f32_16x16x32_bf16 a[124:127], a[220:223], a[184:187], a[124:127]\n\t"
"v_fma_f32 v99, v179, s64, -v81\n\t"
"v_accvgpr_write_b32 a134, v98\n\t"
"v_accvgpr_write_b32 a135, v99\n\t"
"v_fma_f32 v98, v180, s64, -v82\n\t"
"v_fma_f32 v99, v181, s64, -v83\n\t"
"v_mfma_f32_16x16x32_bf16 a[132:135], a[212:215], a[180:183], a[132:135]\n\t"
"ds_read_b128 a[180:183], v244 offset:1536\n\t"
"v_accvgpr_write_b32 a140, v98\n\t"
"v_mfma_f32_16x16x32_bf16 a[132:135], a[216:219], a[184:187], a[132:135]\n\t"
"v_fma_f32 v98, v182, s64, -v84\n\t"
"v_accvgpr_write_b32 a141, v99\n\t"
"v_mfma_f32_16x16x32_bf16 a[132:135], a[220:223], a[188:191], a[132:135]\n\t"
"buffer_load_dwordx4 v[248:251], v252, s[8:11], s20 offen offset:2048 sc1\n\t"
"v_fma_f32 v99, v183, s64, -v85\n\t"
"v_accvgpr_write_b32 a142, v98\n\t"
"v_accvgpr_write_b32 a143, v99\n\t"
"v_fma_f32 v98, v184, s64, -v86\n\t"
"v_fma_f32 v99, v185, s64, -v87\n\t"
"v_mfma_f32_16x16x32_bf16 a[140:143], a[212:215], a[184:187], a[140:143]\n\t"
"ds_read_b128 a[184:187], v244 offset:2048\n\t"
"v_accvgpr_write_b32 a148, v98\n\t"
"v_mfma_f32_16x16x32_bf16 a[140:143], a[216:219], a[188:191], a[140:143]\n\t"
"v_fma_f32 v98, v186, s64, -v88\n\t"
"v_accvgpr_write_b32 a149, v99\n\t"
"v_mfma_f32_16x16x32_bf16 a[140:143], a[220:223], a[192:195], a[140:143]\n\t"
"v_fma_f32 v99, v187, s64, -v89\n\t"
"v_accvgpr_write_b32 a150, v98\n\t"
"v_accvgpr_write_b32 a151, v99\n\t"
"v_fma_f32 v98, v188, s64, -v90\n\t"
"v_fma_f32 v99, v189, s64, -v91\n\t"
"v_mfma_f32_16x16x32_bf16 a[148:151], a[212:215], a[188:191], a[148:151]\n\t"
"s_mov_b32 s20, 86016\n\t"
"ds_read_b128 a[188:191], v244 offset:2560\n\t"
"v_accvgpr_write_b32 a156, v98\n\t"
"v_mfma_f32_16x16x32_bf16 a[148:151], a[216:219], a[192:195], a[148:151]\n\t"
"v_fma_f32 v98, v190, s64, -v92\n\t"
"v_accvgpr_write_b32 a157, v99\n\t"
"v_mfma_f32_16x16x32_bf16 a[148:151], a[220:223], a[196:199], a[148:151]\n\t"
"v_fma_f32 v99, v191, s64, -v93\n\t"
"v_accvgpr_write_b32 a158, v98\n\t"
"v_accvgpr_write_b32 a159, v99\n\t"
"v_fma_f32 v98, v192, s64, -v94\n\t"
"v_fma_f32 v99, v193, s64, -v95\n\t"
"v_mfma_f32_16x16x32_bf16 a[156:159], a[212:215], a[192:195], a[156:159]\n\t"
"ds_read_b128 a[192:195], v244 offset:3072\n\t"
"v_accvgpr_write_b32 a164, v98\n\t"
"v_mfma_f32_16x16x32_bf16 a[156:159], a[216:219], a[196:199], a[156:159]\n\t"
"v_fma_f32 v98, v194, s64, -v96\n\t"
"v_accvgpr_write_b32 a165, v99\n\t"
"v_mfma_f32_16x16x32_bf16 a[156:159], a[220:223], a[200:203], a[156:159]\n\t"
"v_fma_f32 v99, v195, s64, -v97\n\t"
"v_accvgpr_write_b32 a166, v98\n\t"
"v_accvgpr_write_b32 a167, v99\n\t"
"v_fma_f32 v0, v2, s52, v100\n\t"
"v_fma_f32 v1, v3, s52, v101\n\t"
"v_mfma_f32_16x16x32_bf16 a[164:167], a[212:215], a[196:199], a[164:167]\n\t"
"ds_read_b128 a[196:199], v244 offset:3584\n\t"
"v_fma_f32 v100, -v100, s66, v2\n\t"
"v_mfma_f32_16x16x32_bf16 a[164:167], a[216:219], a[200:203], a[164:167]\n\t"
"Lpg3_%=:\n\t"
"s_cbranch_vccnz Lpg7_%=\n\t"
"ds_read_b128 a[200:203], v244 offset:4096\n\t"
"v_fma_f32 v2, v4, s52, v102\n\t"
"v_mfma_f32_16x16x32_bf16 a[164:167], a[220:223], a[204:207], a[164:167]\n\t"
"ds_read_b128 a[204:207], v244 offset:4608\n\t"
"v_fma_f32 v101, -v101, s66, v3\n\t"
"v_fma_f32 v3, v5, s52, v103\n\t"
"v_fma_f32 v102, -v102, s66, v4\n\t"
"v_fma_f32 v103, -v103, s66, v5\n\t"
"v_fma_f32 v4, v6, s52, v104\n\t"
"v_fma_f32 v5, v7, s52, v105\n\t"
"Lst3_%=:\n\t"
"s_waitcnt vmcnt(15) lgkmcnt(7)\n\t"
"v_mfma_f32_16x16x32_bf16 v[0:3], a[224:227], a[168:171], v[0:3]\n\t"
"buffer_load_dwordx4 a[212:215], v252, s[8:11], s20 offen offset:0 sc1\n\t"
"v_mfma_f32_16x16x32_bf16 v[0:3], a[228:231], a[172:175], v[0:3]\n\t"
"buffer_load_dwordx4 a[216:219], v252, s[8:11], s20 offen offset:1024 sc1\n\t"
"v_mfma_f32_16x16x32_bf16 v[0:3], a[232:235], a[176:179], v[0:3]\n\t"
"v_fma_f32 v104, -v104, s66, v6\n\t"
"v_fma_f32 v6, v8, s52, v106\n\t"
"v_fma_f32 v105, -v105, s66, v7\n\t"
"v_fma_f32 v7, v9, s52, v107\n\t"
"v_fma_f32 v106, -v106, s66, v8\n\t"
"v_fma_f32 v107, -v107, s66, v9\n\t"
"v_fma_f32 v8, v10, s52, v108\n\t"
"v_fma_f32 v9, v11, s52, v109\n\t"
"s_waitcnt lgkmcnt(6)\n\t"
"v_mfma_f32_16x16x32_bf16 v[4:7], a[224:227], a[172:175], v[4:7]\n\t"
"buffer_load_dwordx4 a[220:223], v252, s[8:11], s20 offen offset:2048 sc1\n\t"
"v_mfma_f32_16x16x32_bf16 v[4:7], a[228:231], a[176:179], v[4:7]\n\t"
"v_fma_f32 v108, -v108, s66, v10\n\t"
"v_fma_f32 v10, v12, s52, v110\n\t"
"v_mfma_f32_16x16x32_bf16 v[4:7], a[232:235], a[180:183], v[4:7]\n\t"
"v_fma_f32 v109, -v109, s66, v11\n\t"
"v_fma_f32 v11, v13, s52, v111\n\t"
"v_fma_f32 v110, -v110, s66, v12\n\t"
"v_fma_f32 v111, -v111, s66, v13\n\t"
"v_fma_f32 v12, v14, s52, v112\n\t"
"v_fma_f32 v13, v15, s52, v113\n\t"
"s_waitcnt lgkmcnt(5)\n\t"
"v_mfma_f32_16x16x32_bf16 v[8:11], a[224:227], a[176:179], v[8:11]\n\t"
"v_fma_f32 v112, -v112, s66, v14\n\t"
"v_fma_f32 v14, v16, s52, v114\n\t"
"v_mfma_f32_16x16x32_bf16 v[8:11], a[228:231], a[180:183], v[8:11]\n\t"
"v_fma_f32 v113, -v113, s66, v15\n\t"
"v_fma_f32 v15, v17, s52, v115\n\t"
"v_mfma_f32_16x16x32_bf16 v[8:11], a[232:235], a[184:187], v[8:11]\n\t"
"v_fma_f32 v114, -v114, s66, v16\n\t"
"v_fma_f32 v115, -v115, s66, v17\n\t"
"v_fma_f32 v16, v18, s52, v116\n\t"
"v_fma_f32 v17, v19, s52, v117\n\t"
"s_waitcnt lgkmcnt(4)\n\t"
"v_mfma_f32_16x16x32_bf16 v[12:15], a[224:227], a[180:183], v[12:15]\n\t"
"v_fma_f32 v116, -v116, s66, v18\n\t"
"v_fma_f32 v18, v20, s52, v118\n\t"
"v_mfma_f32_16x16x32_bf16 v[12:15], a[228:231], a[184:187], v[12:15]\n\t"
"v_fma_f32 v117, -v117, s66, v19\n\t"
"v_fma_f32 v19, v21, s52, v119\n\t"
"v_mfma_f32_16x16x32_bf16 v[12:15], a[232:235], a[188:191], v[12:15]\n\t"
"v_fma_f32 v118, -v118, s66, v20\n\t"
"v_fma_f32 v119, -v119, s66, v21\n\t"
"v_fma_f32 v20, v22, s52, v120\n\t"
"v_fma_f32 v21, v23, s52, v121\n\t"
"s_waitcnt lgkmcnt(3)\n\t"
"v_mfma_f32_16x16x32_bf16 v[16:19], a[224:227], a[184:187], v[16:19]\n\t"
"v_fma_f32 v120, -v120, s66, v22\n\t"
"v_fma_f32 v22, v24, s52, v122\n\t"
"v_mfma_f32_16x16x32_bf16 v[16:19], a[228:231], a[188:191], v[16:19]\n\t"
"v_fma_f32 v121, -v121, s66, v23\n\t"
"v_fma_f32 v23, v25, s52, v123\n\t"
"v_mfma_f32_16x16x32_bf16 v[16:19], a[232:235], a[192:195], v[16:19]\n\t"
"ds_read_b64_tr_b16 v[228:229], v246 offset:512\n\t"
"v_fma_f32 v122, -v122, s66, v24\n\t"
"v_fma_f32 v123, -v123, s66, v25\n\t"
"v_fma_f32 v24, v26, s52, v124\n\t"
"v_fma_f32 v25, v27, s52, v125\n\t"
"s_waitcnt lgkmcnt(3)\n\t"
"v_mfma_f32_16x16x32_bf16 v[20:23], a[224:227], a[188:191], v[20:23]\n\t"
"s_mov_b32 s20, 98304\n\t"
"ds_read_b64_tr_b16 v[230:231], v246 offset:3584\n\t"
"v_fma_f32 v124, -v124, s66, v26\n\t"
"v_mfma_f32_16x16x32_bf16 v[20:23], a[228:231], a[192:195], v[20:23]\n\t"
"v_fma_f32 v26, v28, s52, v126\n\t"
"v_fma_f32 v125, -v125, s66, v27\n\t"
"v_mfma_f32_16x16x32_bf16 v[20:23], a[232:235], a[196:199], v[20:23]\n\t"
"v_fma_f32 v27, v29, s52, v127\n\t"
"v_fma_f32 v126, -v126, s66, v28\n\t"
"v_fma_f32 v127, -v127, s66, v29\n\t"
"v_fma_f32 v28, v30, s52, v128\n\t"
"v_fma_f32 v29, v31, s52, v129\n\t"
"s_waitcnt lgkmcnt(3)\n\t"
"v_mfma_f32_16x16x32_bf16 v[24:27], a[224:227], a[192:195], v[24:27]\n\t"
"v_fma_f32 v128, -v128, s66, v30\n\t"
"v_fma_f32 v30, v32, s52, v130\n\t"
"v_mfma_f32_16x16x32_bf16 v[24:27], a[228:231], a[196:199], v[24:27]\n\t"
"v_fma_f32 v129, -v129, s66, v31\n\t"
"v_fma_f32 v31, v33, s52, v131\n\t"
"v_mfma_f32_16x16x32_bf16 v[24:27], a[232:235], a[200:203], v[24:27]\n\t"
"v_fma_f32 v130, -v130, s66, v32\n\t"
"v_fma_f32 v131, -v131, s66, v33\n\t"
"v_fma_f32 v32, v34, s52, v132\n\t"
"v_fma_f32 v33, v35, s52, v133\n\t"
"s_waitcnt lgkmcnt(2)\n\t"
"v_mfma_f32_16x16x32_bf16 v[28:31], a[224:227], a[196:199], v[28:31]\n\t"
"buffer_load_dwordx4 a[224:227], v252, s[8:11], s20 offen offset:0 sc1\n\t"
"v_mfma_f32_16x16x32_bf16 v[28:31], a[228:231], a[200:203], v[28:31]\n\t"
"ds_read_b64_tr_b16 v[224:225], v246 offset:6656\n\t"
"v_fma_f32 v132, -v132, s66, v34\n\t"
"v_mfma_f32_16x16x32_bf16 v[28:31], a[232:235], a[204:207], v[28:31]\n\t"
"ds_read_b64_tr_b16 v[226:227], v246 offset:9728\n\t"
"v_fma_f32 v34, v36, s52, v134\n\t"
"v_fma_f32 v133, -v133, s66, v35\n\t"
"v_fma_f32 v35, v37, s52, v135\n\t"
"v_fma_f32 v134, -v134, s66, v36\n\t"
"v_fma_f32 v135, -v135, s66, v37\n\t"
"v_fma_f32 v36, v38, s52, v136\n\t"
"v_fma_f32 v37, v39, s52, v137\n\t"
"s_waitcnt vmcnt(16)\n\t"
"v_mfma_f32_16x16x32_bf16 v[32:35], a[236:239], a[168:171], v[32:35]\n\t"
"buffer_load_dwordx4 a[228:231], v252, s[8:11], s20 offen offset:1024 sc1\n\t"
"v_mfma_f32_16x16x32_bf16 v[32:35], a[240:243], a[172:175], v[32:35]\n\t"
"v_fma_f32 v136, -v136, s66, v38\n\t"
"v_fma_f32 v38, v40, s52, v138\n\t"
"v_mfma_f32_16x16x32_bf16 v[32:35], a[244:247], a[176:179], v[32:35]\n\t"
"v_fma_f32 v137, -v137, s66, v39\n\t"
"v_fma_f32 v39, v41, s52, v139\n\t"
"v_fma_f32 v138, -v138, s66, v40\n\t"
"v_fma_f32 v139, -v139, s66, v41\n\t"
"v_mfma_f32_16x16x32_bf16 v[36:39], a[236:239], a[172:175], v[36:39]\n\t"
"s_waitcnt lgkmcnt(2)\n\t"
"v_fma_f32 v40, v42, s52, v140\n\t"
"v_fma_f32 v41, v43, s52, v141\n\t"
"v_mfma_f32_16x16x32_bf16 v[36:39], a[240:243], a[176:179], v[36:39]\n\t"
"v_fma_f32 v140, -v140, s66, v42\n\t"
"v_fma_f32 v42, v44, s52, v142\n\t"
"v_mfma_f32_16x16x32_bf16 v[36:39], a[244:247], a[180:183], v[36:39]\n\t"
"v_fma_f32 v141, -v141, s66, v43\n\t"
"v_fma_f32 v43, v45, s52, v143\n\t"
"v_fma_f32 v142, -v142, s66, v44\n\t"
"v_fma_f32 v143, -v143, s66, v45\n\t"
"v_mfma_f32_16x16x32_bf16 v[228:231], v[228:231], v[220:223], 0\n\t"
"buffer_load_dwordx4 a[232:235], v252, s[8:11], s20 offen offset:2048 sc1\n\t"
"v_fma_f32 v44, v46, s52, v144\n\t"
"v_fma_f32 v45, v47, s52, v145\n\t"
"v_mfma_f32_16x16x32_bf16 v[40:43], a[236:239], a[176:179], v[40:43]\n\t"
"ds_read_b64_tr_b16 v[232:233], v246 offset:12800\n\t"
"v_fma_f32 v144, -v144, s66, v46\n\t"
"v_mfma_f32_16x16x32_bf16 v[40:43], a[240:243], a[180:183], v[40:43]\n\t"
"ds_read_b64_tr_b16 v[234:235], v246 offset:15872\n\t"
"v_fma_f32 v46, v48, s52, v146\n\t"
"v_mfma_f32_16x16x32_bf16 v[40:43], a[244:247], a[184:187], v[40:43]\n\t"
"v_fma_f32 v145, -v145, s66, v47\n\t"
"v_fma_f32 v47, v49, s52, v147\n\t"
"v_fma_f32 v146, -v146, s66, v48\n\t"
"v_fma_f32 v147, -v147, s66, v49\n\t"
"v_mfma_f32_16x16x32_bf16 v[44:47], a[236:239], a[180:183], v[44:47]\n\t"
"s_waitcnt lgkmcnt(2)\n\t"
"v_fma_f32 v48, v50, s52, v148\n\t"
"v_fma_f32 v49, v51, s52, v149\n\t"
"v_mfma_f32_16x16x32_bf16 v[44:47], a[240:243], a[184:187], v[44:47]\n\t"
"v_fma_f32 v148, -v148, s66, v50\n\t"
"v_fma_f32 v50, v52, s52, v150\n\t"
"v_mfma_f32_16x16x32_bf16 v[44:47], a[244:247], a[188:191], v[44:47]\n\t"
"v_fma_f32 v149, -v149, s66, v51\n\t"
"v_fma_f32 v51, v53, s52, v151\n\t"
"v_fma_f32 v150, -v150, s66, v52\n\t"
"v_fma_f32 v151, -v151, s66, v53\n\t"
"v_mfma_f32_16x16x32_bf16 v[224:227], v[224:227], v[220:223], 0\n\t"
"v_fma_f32 v52, v54, s52, v152\n\t"
"v_fma_f32 v53, v55, s52, v153\n\t"
"v_mfma_f32_16x16x32_bf16 v[48:51], a[236:239], a[184:187], v[48:51]\n\t"
"v_fma_f32 v152, -v152, s66, v54\n\t"
"v_fma_f32 v54, v56, s52, v154\n\t"
"v_mfma_f32_16x16x32_bf16 v[48:51], a[240:243], a[188:191], v[48:51]\n\t"
"v_fma_f32 v153, -v153, s66, v55\n\t"
"v_fma_f32 v55, v57, s52, v155\n\t"
"v_mfma_f32_16x16x32_bf16 v[48:51], a[244:247], a[192:195], v[48:51]\n\t"
"v_fma_f32 v154, -v154, s66, v56\n\t"
"v_fma_f32 v155, -v155, s66, v57\n\t"
"v_cvt_pk_bf16_f32 v228, v228, v229\n\t"
"v_cvt_pk_bf16_f32 v229, v230, v231\n\t"
"v_fma_f32 v56, v58, s52, v156\n\t"
"v_fma_f32 v57, v59, s52, v157\n\t"
"v_mfma_f32_16x16x32_bf16 v[52:55], a[236:239], a[188:191], v[52:55]\n\t"
"v_fma_f32 v156, -v156, s66, v58\n\t"
"v_fma_f32 v58, v60, s52, v158\n\t"
"v_mfma_f32_16x16x32_bf16 v[52:55], a[240:243], a[192:195], v[52:55]\n\t"
"s_mov_b32 s20, 331776\n\t"
"v_fma_f32 v157, -v157, s66, v59\n\t"
"v_fma_f32 v59, v61, s52, v159\n\t"
"v_mfma_f32_16x16x32_bf16 v[52:55], a[244:247], a[196:199], v[52:55]\n\t"
"v_fma_f32 v158, -v158, s66, v60\n\t"
"v_fma_f32 v159, -v159, s66, v61\n\t"
"v_mfma_f32_16x16x32_bf16 v[56:59], a[236:239], a[192:195], v[56:59]\n\t"
"s_waitcnt lgkmcnt(0)\n\t"
"v_fma_f32 v60, v62, s52, v160\n\t"
"v_fma_f32 v61, v63, s52, v161\n\t"
"v_mfma_f32_16x16x32_bf16 v[56:59], a[240:243], a[196:199], v[56:59]\n\t"
"v_fma_f32 v160, -v160, s66, v62\n\t"
"v_fma_f32 v62, v64, s52, v162\n\t"
"v_mfma_f32_16x16x32_bf16 v[56:59], a[244:247], a[200:203], v[56:59]\n\t"
"v_fma_f32 v161, -v161, s66, v63\n\t"
"v_fma_f32 v63, v65, s52, v163\n\t"
"v_fma_f32 v162, -v162, s66, v64\n\t"
"v_fma_f32 v163, -v163, s66, v65\n\t"
"v_cvt_pk_bf16_f32 v230, v224, v225\n\t"
"v_cvt_pk_bf16_f32 v231, v226, v227\n\t"
"v_mfma_f32_16x16x32_bf16 v[232:235], v[232:235], v[220:223], 0\n\t"
"ds_read_b64_tr_b16 v[224:225], v246 offset:20480\n\t"
"v_fma_f32 v64, v66, s52, v164\n\t"
"v_fma_f32 v65, v67, s52, v165\n\t"
"v_mfma_f32_16x16x32_bf16 v[60:63], a[236:239], a[196:199], v[60:63]\n\t"
"ds_read_b64_tr_b16 v[226:227], v246 offset:23552\n\t"
"v_fma_f32 v164, -v164, s66, v66\n\t"
"v_mfma_f32_16x16x32_bf16 v[60:63], a[240:243], a[200:203], v[60:63]\n\t"
"v_fma_f32 v66, v68, s52, v166\n\t"
"v_fma_f32 v165, -v165, s66, v67\n\t"
"v_mfma_f32_16x16x32_bf16 v[60:63], a[244:247], a[204:207], v[60:63]\n\t"
"v_fma_f32 v67, v69, s52, v167\n\t"
"v_fma_f32 v166, -v166, s66, v68\n\t"
"v_fma_f32 v167, -v167, s66, v69\n\t"
"v_fma_f32 v68, v70, s52, v168\n\t"
"v_fma_f32 v69, v71, s52, v169\n\t"
"s_waitcnt vmcnt(15)\n\t"
"v_mfma_f32_16x16x32_bf16 v[64:67], a[248:251], a[168:171], v[64:67]\n\t"
"buffer_load_dwordx4 a[236:239], v252, s[8:11], s20 offen offset:0 sc1\n\t"
"v_mfma_f32_16x16x32_bf16 v[64:67], a[252:255], a[172:175], v[64:67]\n\t"
"buffer_load_dwordx4 a[240:243], v252, s[8:11], s20 offen offset:1024 sc1\n\t"
"v_mfma_f32_16x16x32_bf16 v[64:67], v[236:239], a[176:179], v[64:67]\n\t"
"ds_read_b128 a[168:171], v244 offset:9216\n\t"
"v_fma_f32 v168, -v168, s66, v70\n\t"
"v_fma_f32 v70, v72, s52, v170\n\t"
"v_fma_f32 v169, -v169, s66, v71\n\t"
"v_fma_f32 v71, v73, s52, v171\n\t"
"v_fma_f32 v170, -v170, s66, v72\n\t"
"v_fma_f32 v171, -v171, s66, v73\n\t"
"v_mfma_f32_16x16x32_bf16 v[68:71], a[248:251], a[172:175], v[68:71]\n\t"
"s_waitcnt lgkmcnt(1)\n\t"
"ds_read_b128 a[172:175], v244 offset:9728\n\t"
"v_fma_f32 v72, v74, s52, v172\n\t"
"v_mfma_f32_16x16x32_bf16 v[68:71], a[252:255], a[176:179], v[68:71]\n\t"
"v_fma_f32 v73, v75, s52, v173\n\t"
"v_fma_f32 v172, -v172, s66, v74\n\t"
"v_mfma_f32_16x16x32_bf16 v[68:71], v[236:239], a[180:183], v[68:71]\n\t"
"v_fma_f32 v74, v76, s52, v174\n\t"
"v_fma_f32 v173, -v173, s66, v75\n\t"
"v_fma_f32 v75, v77, s52, v175\n\t"
"v_fma_f32 v174, -v174, s66, v76\n\t"
"v_fma_f32 v175, -v175, s66, v77\n\t"
"v_cvt_pk_bf16_f32 v232, v232, v233\n\t"
"v_cvt_pk_bf16_f32 v233, v234, v235\n\t"
"v_mfma_f32_16x16x32_bf16 v[224:227], v[224:227], v[220:223], 0\n\t"
"buffer_load_dwordx4 a[244:247], v252, s[8:11], s20 offen offset:2048 sc1\n\t"
"v_fma_f32 v76, v78, s52, v176\n\t"
"v_fma_f32 v77, v79, s52, v177\n\t"
"v_mfma_f32_16x16x32_bf16 v[72:75], a[248:251], a[176:179], v[72:75]\n\t"
"ds_read_b128 a[176:179], v244 offset:10240\n\t"
"v_fma_f32 v176, -v176, s66, v78\n\t"
"v_mfma_f32_16x16x32_bf16 v[72:75], a[252:255], a[180:183], v[72:75]\n\t"
"v_fma_f32 v78, v80, s52, v178\n\t"
"v_fma_f32 v177, -v177, s66, v79\n\t"
"v_mfma_f32_16x16x32_bf16 v[72:75], v[236:239], a[184:187], v[72:75]\n\t"
"v_fma_f32 v79, v81, s52, v179\n\t"
"v_fma_f32 v178, -v178, s66, v80\n\t"
"v_fma_f32 v179, -v179, s66, v81\n\t"
"v_fma_f32 v80, v82, s52, v180\n\t"
"v_fma_f32 v81, v83, s52, v181\n\t"
"v_mfma_f32_16x16x32_bf16 v[76:79], a[248:251], a[180:183], v[76:79]\n\t"
"ds_read_b128 a[180:183], v244 offset:10752\n\t"
"v_fma_f32 v180, -v180, s66, v82\n\t"
"v_mfma_f32_16x16x32_bf16 v[76:79], a[252:255], a[184:187], v[76:79]\n\t"
"v_fma_f32 v82, v84, s52, v182\n\t"
"v_fma_f32 v181, -v181, s66, v83\n\t"
"v_mfma_f32_16x16x32_bf16 v[76:79], v[236:239], a[188:191], v[76:79]\n\t"
"v_fma_f32 v83, v85, s52, v183\n\t"
"v_fma_f32 v182, -v182, s66, v84\n\t"
"v_fma_f32 v183, -v183, s66, v85\n\t"
"v_fma_f32 v84, v86, s52, v184\n\t"
"v_fma_f32 v85, v87, s52, v185\n\t"
"v_mfma_f32_16x16x32_bf16 v[80:83], a[248:251], a[184:187], v[80:83]\n\t"
"ds_read_b128 a[184:187], v244 offset:11264\n\t"
"v_fma_f32 v184, -v184, s66, v86\n\t"
"v_mfma_f32_16x16x32_bf16 v[80:83], a[252:255], a[188:191], v[80:83]\n\t"
"s_mov_b32 s20, 344064\n\t"
"v_fma_f32 v86, v88, s52, v186\n\t"
"v_fma_f32 v185, -v185, s66, v87\n\t"
"v_mfma_f32_16x16x32_bf16 v[80:83], v[236:239], a[192:195], v[80:83]\n\t"
"v_fma_f32 v87, v89, s52, v187\n\t"
"v_fma_f32 v186, -v186, s66, v88\n\t"
"v_fma_f32 v187, -v187, s66, v89\n\t"
"v_cvt_pk_bf16_f32 v234, v224, v225\n\t"
"v_cvt_pk_bf16_f32 v235, v226, v227\n\t"
"v_fma_f32 v88, v90, s52, v188\n\t"
"v_fma_f32 v89, v91, s52, v189\n\t"
"v_mfma_f32_16x16x32_bf16 v[84:87], a[248:251], a[188:191], v[84:87]\n\t"
"ds_read_b64_tr_b16 v[224:225], v246 offset:26624\n\t"
"v_fma_f32 v188, -v188, s66, v90\n\t"
"v_mfma_f32_16x16x32_bf16 v[84:87], a[252:255], a[192:195], v[84:87]\n\t"
"ds_read_b64_tr_b16 v[226:227], v246 offset:29696\n\t"
"v_fma_f32 v90, v92, s52, v190\n\t"
"v_mfma_f32_16x16x32_bf16 v[84:87], v[236:239], a[196:199], v[84:87]\n\t"
"ds_read_b128 a[188:191], v244 offset:11776\n\t"
"v_fma_f32 v189, -v189, s66, v91\n\t"
"v_fma_f32 v91, v93, s52, v191\n\t"
"v_fma_f32 v190, -v190, s66, v92\n\t"
"v_fma_f32 v191, -v191, s66, v93\n\t"
"v_fma_f32 v92, v94, s52, v192\n\t"
"v_fma_f32 v93, v95, s52, v193\n\t"
"v_mfma_f32_16x16x32_bf16 v[88:91], a[248:251], a[192:195], v[88:91]\n\t"
"ds_read_b128 a[192:195], v244 offset:12288\n\t"
"v_fma_f32 v192, -v192, s66, v94\n\t"
"v_mfma_f32_16x16x32_bf16 v[88:91], a[252:255], a[196:199], v[88:91]\n\t"
"v_fma_f32 v94, v96, s52, v194\n\t"
"v_fma_f32 v193, -v193, s66, v95\n\t"
"v_mfma_f32_16x16x32_bf16 v[88:91], v[236:239], a[200:203], v[88:91]\n\t"
"v_fma_f32 v95, v97, s52, v195\n\t"
"v_fma_f32 v194, -v194, s66, v96\n\t"
"v_fma_f32 v195, -v195, s66, v97\n\t"
"v_mfma_f32_16x16x32_bf16 v[92:95], a[248:251], a[196:199], v[92:95]\n\t"
"s_waitcnt lgkmcnt(2)\n\t"
"ds_read_b128 a[196:199], v244 offset:12800\n\t"
"v_mfma_f32_16x16x32_bf16 v[92:95], a[252:255], a[200:203], v[92:95]\n\t"
"buffer_load_dwordx4 v[96:99], v252, s[8:11], s20 offen offset:0 sc1\n\t"
"v_mfma_f32_16x16x32_bf16 v[92:95], v[236:239], a[204:207], v[92:95]\n\t"
"buffer_load_dwordx4 a[248:251], v252, s[8:11], s20 offen offset:1024 sc1\n\t"
"v_mfma_f32_16x16x32_bf16 v[224:227], v[224:227], v[220:223], 0\n\t"
"s_waitcnt vmcnt(11)\n\t"
"ds_read_b128 a[200:203], v244 offset:13312\n\t"
"v_mfma_f32_16x16x32_bf16 v[100:103], v[240:243], a[168:171], v[100:103]\n\t"
"ds_read_b128 a[204:207], v244 offset:13824\n\t"
"v_mfma_f32_16x16x32_bf16 v[100:103], a[208:211], a[172:175], v[100:103]\n\t"
"buffer_load_dwordx4 v[236:239], v252, s[8:11], s20 offen offset:2048 sc1\n\t"
"v_mfma_f32_16x16x32_bf16 v[100:103], v[248:251], a[176:179], v[100:103]\n\t"
"s_mov_b32 s20, 356352\n\t"
"v_mfma_f32_16x16x32_bf16 v[104:107], v[240:243], a[172:175], v[104:107]\n\t"
"buffer_load_dwordx4 a[252:255], v252, s[8:11], s20 offen offset:0 sc1\n\t"
"v_mfma_f32_16x16x32_bf16 v[104:107], a[208:211], a[176:179], v[104:107]\n\t"
"v_cvt_pk_bf16_f32 v224, v224, v225\n\t"
"v_mfma_f32_16x16x32_bf16 v[104:107], v[248:251], a[180:183], v[104:107]\n\t"
"s_waitcnt lgkmcnt(0)\n\t"
"s_barrier\n\t"
"v_mfma_f32_16x16x32_bf16 v[108:111], v[240:243], a[176:179], v[108:111]\n\t"
"v_cvt_pk_bf16_f32 v225, v226, v227\n\t"
"v_mfma_f32_16x16x32_bf16 v[108:111], a[208:211], a[180:183], v[108:111]\n\t"
"v_mfma_f32_16x16x32_bf16 v[108:111], v[248:251], a[184:187], v[108:111]\n\t"
"ds_write2st64_b64 v245, v[228:229], v[230:231] offset0:0 offset1:1\n\t"
"v_mfma_f32_16x16x32_bf16 v[112:115], v[240:243], a[180:183], v[112:115]\n\t"
"v_mfma_f32_16x16x32_bf16 v[112:115], a[208:211], a[184:187], v[112:115]\n\t"
"ds_read_b64_tr_b16 v[228:229], v246 offset:32768\n\t"
"v_mfma_f32_16x16x32_bf16 v[112:115], v[248:251], a[188:191], v[112:115]\n\t"
"ds_write2st64_b64 v245, v[232:233], v[234:235] offset0:2 offset1:3\n\t"
"v_mfma_f32_16x16x32_bf16 v[116:119], v[240:243], a[184:187], v[116:119]\n\t"
"v_mfma_f32_16x16x32_bf16 v[116:119], a[208:211], a[188:191], v[116:119]\n\t"
"ds_read_b64_tr_b16 v[230:231], v246 offset:35840\n\t"
"v_mfma_f32_16x16x32_bf16 v[116:119], v[248:251], a[192:195], v[116:119]\n\t"
"s_add_u32 m0, s24, 73088\n\t"
"s_add_u32 s28, s21, 2162688\n\t"
"ds_write_b64 v245, v[224:225] offset:2048\n\t"
"v_mfma_f32_16x16x32_bf16 v[120:123], v[240:243], a[188:191], v[120:123]\n\t"
"ds_read_b64_tr_b16 v[232:233], v246 offset:40448\n\t"
"v_mfma_f32_16x16x32_bf16 v[120:123], a[208:211], a[192:195], v[120:123]\n\t"
"ds_read_b64_tr_b16 v[234:235], v246 offset:43520\n\t"
"v_mfma_f32_16x16x32_bf16 v[120:123], v[248:251], a[196:199], v[120:123]\n\t"
"s_waitcnt lgkmcnt(3)\n\t"
"ds_read_b64_tr_b16 v[224:225], v246 offset:46592\n\t"
"v_mfma_f32_16x16x32_bf16 v[228:231], v[228:231], v[220:223], 0\n\t"
"ds_read_b64_tr_b16 v[226:227], v246 offset:49664\n\t"
"v_mfma_f32_16x16x32_bf16 v[124:127], v[240:243], a[192:195], v[124:127]\n\t"
"buffer_load_dwordx4 v247, s[12:15], s28 offen offset:128 nt lds\n\t"
"v_mfma_f32_16x16x32_bf16 v[124:127], a[208:211], a[196:199], v[124:127]\n\t"
"s_add_u32 m0, s24, 80768\n\t"
"s_add_u32 s28, s21, 2359296\n\t"
"v_mfma_f32_16x16x32_bf16 v[124:127], v[248:251], a[200:203], v[124:127]\n\t"
"s_waitcnt lgkmcnt(2)\n\t"
"buffer_load_dwordx4 v247, s[12:15], s28 offen offset:128 nt lds\n\t"
"s_add_u32 m0, s24, 27008\n\t"
"s_add_u32 s28, s21, 786432\n\t"
"v_mfma_f32_16x16x32_bf16 v[232:235], v[232:235], v[220:223], 0\n\t"
"buffer_load_dwordx4 v247, s[12:15], s28 offen offset:128 nt lds\n\t"
"v_mfma_f32_16x16x32_bf16 v[128:131], v[240:243], a[196:199], v[128:131]\n\t"
"s_waitcnt lgkmcnt(0)\n\t"
"s_add_u32 m0, s24, 86912\n\t"
"s_add_u32 s28, s21, 2555904\n\t"
"v_mfma_f32_16x16x32_bf16 v[128:131], a[208:211], a[200:203], v[128:131]\n\t"
"buffer_load_dwordx4 v247, s[12:15], s28 offen offset:128 nt lds\n\t"
"v_mfma_f32_16x16x32_bf16 v[128:131], v[248:251], a[204:207], v[128:131]\n\t"
"s_waitcnt vmcnt(14)\n\t"
"s_add_u32 m0, s24, 33152\n\t"
"s_add_u32 s28, s21, 983040\n\t"
"v_mfma_f32_16x16x32_bf16 v[224:227], v[224:227], v[220:223], 0\n\t"
"buffer_load_dwordx4 v247, s[12:15], s28 offen offset:128 nt lds\n\t"
"v_cvt_pk_bf16_f32 v228, v228, v229\n\t"
"v_cvt_pk_bf16_f32 v229, v230, v231\n\t"
"v_mfma_f32_16x16x32_bf16 v[132:135], a[212:215], a[168:171], v[132:135]\n\t"
"s_add_u32 m0, s24, 40832\n\t"
"s_add_u32 s28, s21, 1179648\n\t"
"v_cvt_pk_bf16_f32 v230, v232, v233\n\t"
"v_mfma_f32_16x16x32_bf16 v[132:135], a[216:219], a[172:175], v[132:135]\n\t"
"v_cvt_pk_bf16_f32 v231, v234, v235\n\t"
"v_mfma_f32_16x16x32_bf16 v[132:135], a[220:223], a[176:179], v[132:135]\n\t"
"buffer_load_dwordx4 v247, s[12:15], s28 offen offset:128 nt lds\n\t"
"v_mfma_f32_16x16x32_bf16 v[136:139], a[212:215], a[172:175], v[136:139]\n\t"
"ds_read_b64_tr_b16 v[232:233], v246 offset:52736\n\t"
"v_mfma_f32_16x16x32_bf16 v[136:139], a[216:219], a[176:179], v[136:139]\n\t"
"ds_read_b64_tr_b16 v[234:235], v246 offset:55808\n\t"
"v_cvt_pk_bf16_f32 v224, v224, v225\n\t"
"v_mfma_f32_16x16x32_bf16 v[136:139], a[220:223], a[180:183], v[136:139]\n\t"
"ds_write2st64_b64 v245, v[228:229], v[230:231] offset0:5 offset1:6\n\t"
"v_mfma_f32_16x16x32_bf16 v[140:143], a[212:215], a[176:179], v[140:143]\n\t"
"v_cvt_pk_bf16_f32 v225, v226, v227\n\t"
"v_mfma_f32_16x16x32_bf16 v[140:143], a[216:219], a[180:183], v[140:143]\n\t"
"v_mfma_f32_16x16x32_bf16 v[140:143], a[220:223], a[184:187], v[140:143]\n\t"
"v_mfma_f32_16x16x32_bf16 v[144:147], a[212:215], a[180:183], v[144:147]\n\t"
"v_mfma_f32_16x16x32_bf16 v[144:147], a[216:219], a[184:187], v[144:147]\n\t"
"v_mfma_f32_16x16x32_bf16 v[144:147], a[220:223], a[188:191], v[144:147]\n\t"
"v_mfma_f32_16x16x32_bf16 v[148:151], a[212:215], a[184:187], v[148:151]\n\t"
"s_waitcnt lgkmcnt(1)\n\t"
"v_mfma_f32_16x16x32_bf16 v[148:151], a[216:219], a[188:191], v[148:151]\n\t"
"v_mfma_f32_16x16x32_bf16 v[148:151], a[220:223], a[192:195], v[148:151]\n\t"
"v_mfma_f32_16x16x32_bf16 v[232:235], v[232:235], v[220:223], 0\n\t"
"v_mfma_f32_16x16x32_bf16 v[152:155], a[212:215], a[188:191], v[152:155]\n\t"
"v_mfma_f32_16x16x32_bf16 v[152:155], a[216:219], a[192:195], v[152:155]\n\t"
"v_mfma_f32_16x16x32_bf16 v[152:155], a[220:223], a[196:199], v[152:155]\n\t"
"v_mfma_f32_16x16x32_bf16 v[156:159], a[212:215], a[192:195], v[156:159]\n\t"
"v_mfma_f32_16x16x32_bf16 v[156:159], a[216:219], a[196:199], v[156:159]\n\t"
"v_cvt_pk_bf16_f32 v226, v232, v233\n\t"
"v_cvt_pk_bf16_f32 v227, v234, v235\n\t"
"v_mfma_f32_16x16x32_bf16 v[156:159], a[220:223], a[200:203], v[156:159]\n\t"
"ds_write2st64_b64 v245, v[224:225], v[226:227] offset0:7 offset1:8\n\t"
"v_mfma_f32_16x16x32_bf16 v[160:163], a[212:215], a[196:199], v[160:163]\n\t"
"s_waitcnt vmcnt(13)\n\t"
"v_mfma_f32_16x16x32_bf16 v[160:163], a[216:219], a[200:203], v[160:163]\n\t"
"v_mfma_f32_16x16x32_bf16 v[160:163], a[220:223], a[204:207], v[160:163]\n\t"
"v_mfma_f32_16x16x32_bf16 v[164:167], a[224:227], a[168:171], v[164:167]\n\t"
"v_mfma_f32_16x16x32_bf16 v[164:167], a[228:231], a[172:175], v[164:167]\n\t"
"v_mfma_f32_16x16x32_bf16 v[164:167], a[232:235], a[176:179], v[164:167]\n\t"
"s_waitcnt lgkmcnt(0)\n\t"
"s_barrier\n\t"
"v_mfma_f32_16x16x32_bf16 v[168:171], a[224:227], a[172:175], v[168:171]\n\t"
"ds_read_b128 a[168:171], v244 offset:0\n\t"
"v_mfma_f32_16x16x32_bf16 v[168:171], a[228:231], a[176:179], v[168:171]\n\t"
"buffer_load_dwordx4 v[240:243], v252, s[8:11], s20 offen offset:1024 sc1\n\t"
"v_mfma_f32_16x16x32_bf16 v[168:171], a[232:235], a[180:183], v[168:171]\n\t"
"ds_read_b128 a[172:175], v244 offset:512\n\t"
"v_mfma_f32_16x16x32_bf16 v[172:175], a[224:227], a[176:179], v[172:175]\n\t"
"ds_read_b128 a[176:179], v244 offset:1024\n\t"
"v_mfma_f32_16x16x32_bf16 v[172:175], a[228:231], a[180:183], v[172:175]\n\t"
"v_mfma_f32_16x16x32_bf16 v[172:175], a[232:235], a[184:187], v[172:175]\n\t"
"v_mfma_f32_16x16x32_bf16 v[176:179], a[224:227], a[180:183], v[176:179]\n\t"
"ds_read_b128 a[180:183], v244 offset:1536\n\t"
"v_mfma_f32_16x16x32_bf16 v[176:179], a[228:231], a[184:187], v[176:179]\n\t"
"v_mfma_f32_16x16x32_bf16 v[176:179], a[232:235], a[188:191], v[176:179]\n\t"
"v_mfma_f32_16x16x32_bf16 v[180:183], a[224:227], a[184:187], v[180:183]\n\t"
"ds_read_b128 a[184:187], v244 offset:2048\n\t"
"v_mfma_f32_16x16x32_bf16 v[180:183], a[228:231], a[188:191], v[180:183]\n\t"
"v_mfma_f32_16x16x32_bf16 v[180:183], a[232:235], a[192:195], v[180:183]\n\t"
"v_mfma_f32_16x16x32_bf16 v[184:187], a[224:227], a[188:191], v[184:187]\n\t"
"ds_read_b128 a[188:191], v244 offset:2560\n\t"
"v_mfma_f32_16x16x32_bf16 v[184:187], a[228:231], a[192:195], v[184:187]\n\t"
"v_mfma_f32_16x16x32_bf16 v[184:187], a[232:235], a[196:199], v[184:187]\n\t"
"v_mfma_f32_16x16x32_bf16 v[188:191], a[224:227], a[192:195], v[188:191]\n\t"
"ds_read_b128 a[192:195], v244 offset:3072\n\t"
"v_mfma_f32_16x16x32_bf16 v[188:191], a[228:231], a[196:199], v[188:191]\n\t"
"v_mfma_f32_16x16x32_bf16 v[188:191], a[232:235], a[200:203], v[188:191]\n\t"
"v_mfma_f32_16x16x32_bf16 v[192:195], a[224:227], a[196:199], v[192:195]\n\t"
"s_waitcnt vmcnt(11) lgkmcnt(4)\n\t"
"ds_read_b128 a[196:199], v244 offset:3584\n\t"
"v_mfma_f32_16x16x32_bf16 v[192:195], a[228:231], a[200:203], v[192:195]\n\t"
"ds_read_b128 a[200:203], v244 offset:4096\n\t"
"v_mfma_f32_16x16x32_bf16 v[192:195], a[232:235], a[204:207], v[192:195]\n\t"
"ds_read_b128 a[204:207], v244 offset:4608\n\t"
"Lst4_%=:\n\t"
"v_mfma_f32_16x16x32_bf16 v[0:3], a[236:239], a[168:171], v[0:3]\n\t"
"s_waitcnt lgkmcnt(6)\n\t"
"v_xor_b32 v246, 0xc0, v246\n\t"
"v_mfma_f32_16x16x32_bf16 v[0:3], a[240:243], a[172:175], v[0:3]\n\t"
"v_mfma_f32_16x16x32_bf16 v[0:3], a[244:247], a[176:179], v[0:3]\n\t"
"v_mfma_f32_16x16x32_bf16 v[4:7], a[236:239], a[172:175], v[4:7]\n\t"
"s_waitcnt lgkmcnt(5)\n\t"
"v_mfma_f32_16x16x32_bf16 v[4:7], a[240:243], a[176:179], v[4:7]\n\t"
"v_mfma_f32_16x16x32_bf16 v[4:7], a[244:247], a[180:183], v[4:7]\n\t"
"v_mfma_f32_16x16x32_bf16 v[8:11], a[236:239], a[176:179], v[8:11]\n\t"
"s_waitcnt lgkmcnt(4)\n\t"
"v_mfma_f32_16x16x32_bf16 v[8:11], a[240:243], a[180:183], v[8:11]\n\t"
"v_mfma_f32_16x16x32_bf16 v[8:11], a[244:247], a[184:187], v[8:11]\n\t"
"v_mfma_f32_16x16x32_bf16 v[12:15], a[236:239], a[180:183], v[12:15]\n\t"
"s_waitcnt lgkmcnt(3)\n\t"
"v_mfma_f32_16x16x32_bf16 v[12:15], a[240:243], a[184:187], v[12:15]\n\t"
"v_mfma_f32_16x16x32_bf16 v[12:15], a[244:247], a[188:191], v[12:15]\n\t"
"buffer_load_dwordx4 a[208:211], v252, s[8:11], s20 offen offset:2048 sc1\n\t"
"v_mfma_f32_16x16x32_bf16 v[16:19], a[236:239], a[184:187], v[16:19]\n\t"
"s_waitcnt lgkmcnt(2)\n\t"
"s_mov_b32 s20, 368640\n\t"
"v_mfma_f32_16x16x32_bf16 v[16:19], a[240:243], a[188:191], v[16:19]\n\t"
"v_mfma_f32_16x16x32_bf16 v[16:19], a[244:247], a[192:195], v[16:19]\n\t"
"ds_read_b64_tr_b16 v[228:229], v246 offset:512\n\t"
"v_mfma_f32_16x16x32_bf16 v[20:23], a[236:239], a[188:191], v[20:23]\n\t"
"buffer_load_dwordx4 v[248:251], v252, s[8:11], s20 offen offset:0 sc1\n\t"
"v_mfma_f32_16x16x32_bf16 v[20:23], a[240:243], a[192:195], v[20:23]\n\t"
"s_waitcnt lgkmcnt(2)\n\t"
"ds_read_b64_tr_b16 v[230:231], v246 offset:3584\n\t"
"v_mfma_f32_16x16x32_bf16 v[20:23], a[244:247], a[196:199], v[20:23]\n\t"
"v_mfma_f32_16x16x32_bf16 v[24:27], a[236:239], a[192:195], v[24:27]\n\t"
"buffer_load_dwordx4 a[212:215], v252, s[8:11], s20 offen offset:1024 sc1\n\t"
"v_mfma_f32_16x16x32_bf16 v[24:27], a[240:243], a[196:199], v[24:27]\n\t"
"s_waitcnt lgkmcnt(2)\n\t"
"v_mfma_f32_16x16x32_bf16 v[24:27], a[244:247], a[200:203], v[24:27]\n\t"
"v_mfma_f32_16x16x32_bf16 v[28:31], a[236:239], a[196:199], v[28:31]\n\t"
"s_waitcnt vmcnt(11)\n\t"
"ds_read_b64_tr_b16 v[232:233], v246 offset:6656\n\t"
"v_mfma_f32_16x16x32_bf16 v[28:31], a[240:243], a[200:203], v[28:31]\n\t"
"buffer_load_dwordx4 a[216:219], v252, s[8:11], s20 offen offset:2048 sc1\n\t"
"v_mfma_f32_16x16x32_bf16 v[28:31], a[244:247], a[204:207], v[28:31]\n\t"
"s_mov_b32 s20, 380928\n\t"
"ds_read_b64_tr_b16 v[234:235], v246 offset:9728\n\t"
"v_mfma_f32_16x16x32_bf16 v[32:35], v[96:99], a[168:171], v[32:35]\n\t"
"v_mfma_f32_16x16x32_bf16 v[32:35], a[248:251], a[172:175], v[32:35]\n\t"
"v_mfma_f32_16x16x32_bf16 v[32:35], v[236:239], a[176:179], v[32:35]\n\t"
"buffer_load_dwordx4 a[220:223], v252, s[8:11], s20 offen offset:0 sc1\n\t"
"v_mfma_f32_16x16x32_bf16 v[36:39], v[96:99], a[172:175], v[36:39]\n\t"
"s_waitcnt lgkmcnt(2)\n\t"
"v_mfma_f32_16x16x32_bf16 v[36:39], a[248:251], a[176:179], v[36:39]\n\t"
"v_mfma_f32_16x16x32_bf16 v[36:39], v[236:239], a[180:183], v[36:39]\n\t"
"v_mfma_f32_16x16x32_bf16 v[228:231], v[228:231], v[220:223], 0\n\t"
"buffer_load_dwordx4 a[224:227], v252, s[8:11], s20 offen offset:1024 sc1\n\t"
"v_mfma_f32_16x16x32_bf16 v[40:43], v[96:99], a[176:179], v[40:43]\n\t"
"ds_read_b64_tr_b16 v[224:225], v246 offset:12800\n\t"
"v_mfma_f32_16x16x32_bf16 v[40:43], a[248:251], a[180:183], v[40:43]\n\t"
"s_waitcnt lgkmcnt(1)\n\t"
"ds_read_b64_tr_b16 v[226:227], v246 offset:15872\n\t"
"v_mfma_f32_16x16x32_bf16 v[40:43], v[236:239], a[184:187], v[40:43]\n\t"
"v_mfma_f32_16x16x32_bf16 v[44:47], v[96:99], a[180:183], v[44:47]\n\t"
"Lpg4_%=:\n\t"
"s_cbranch_vccnz Lpg8_%=\n\t"
"v_mfma_f32_16x16x32_bf16 v[44:47], a[248:251], a[184:187], v[44:47]\n\t"
"buffer_load_dwordx4 a[228:231], v252, s[8:11], s20 offen offset:2048 sc1\n\t"
"v_mfma_f32_16x16x32_bf16 v[44:47], v[236:239], a[188:191], v[44:47]\n\t"
"s_mov_b32 s20, 393216\n\t"
"v_cvt_pk_bf16_f32 v228, v228, v229\n\t"
"v_cvt_pk_bf16_f32 v229, v230, v231\n\t"
"v_mfma_f32_16x16x32_bf16 v[232:235], v[232:235], v[220:223], 0\n\t"
"v_mfma_f32_16x16x32_bf16 v[48:51], v[96:99], a[184:187], v[48:51]\n\t"
"buffer_load_dwordx4 a[232:235], v252, s[8:11], s20 offen offset:0 sc1\n\t"
"v_mfma_f32_16x16x32_bf16 v[48:51], a[248:251], a[188:191], v[48:51]\n\t"
"v_mfma_f32_16x16x32_bf16 v[48:51], v[236:239], a[192:195], v[48:51]\n\t"
"v_mfma_f32_16x16x32_bf16 v[52:55], v[96:99], a[188:191], v[52:55]\n\t"
"v_mfma_f32_16x16x32_bf16 v[52:55], a[248:251], a[192:195], v[52:55]\n\t"
"buffer_load_dwordx4 a[236:239], v252, s[8:11], s20 offen offset:1024 sc1\n\t"
"v_mfma_f32_16x16x32_bf16 v[52:55], v[236:239], a[196:199], v[52:55]\n\t"
"v_cvt_pk_bf16_f32 v230, v232, v233\n\t"
"v_cvt_pk_bf16_f32 v231, v234, v235\n\t"
"v_mfma_f32_16x16x32_bf16 v[56:59], v[96:99], a[192:195], v[56:59]\n\t"
"s_waitcnt lgkmcnt(0)\n\t"
"ds_read_b64_tr_b16 v[232:233], v246 offset:20480\n\t"
"v_mfma_f32_16x16x32_bf16 v[56:59], a[248:251], a[196:199], v[56:59]\n\t"
"ds_read_b64_tr_b16 v[234:235], v246 offset:23552\n\t"
"v_mfma_f32_16x16x32_bf16 v[56:59], v[236:239], a[200:203], v[56:59]\n\t"
"buffer_load_dwordx4 a[240:243], v252, s[8:11], s20 offen offset:2048 sc1\n\t"
"v_mfma_f32_16x16x32_bf16 v[224:227], v[224:227], v[220:223], 0\n\t"
"s_mov_b32 s20, 405504\n\t"
"v_mfma_f32_16x16x32_bf16 v[60:63], v[96:99], a[196:199], v[60:63]\n\t"
"s_waitcnt vmcnt(9)\n\t"
"v_mfma_f32_16x16x32_bf16 v[60:63], a[248:251], a[200:203], v[60:63]\n\t"
"v_mfma_f32_16x16x32_bf16 v[60:63], v[236:239], a[204:207], v[60:63]\n\t"
"buffer_load_dwordx4 v[236:239], v252, s[8:11], s20 offen offset:0 sc1\n\t"
"v_mfma_f32_16x16x32_bf16 v[64:67], a[252:255], a[168:171], v[64:67]\n\t"
"ds_read_b128 a[168:171], v244 offset:9216\n\t"
"v_mfma_f32_16x16x32_bf16 v[64:67], v[240:243], a[172:175], v[64:67]\n\t"
"v_cvt_pk_bf16_f32 v224, v224, v225\n\t"
"v_cvt_pk_bf16_f32 v225, v226, v227\n\t"
"v_mfma_f32_16x16x32_bf16 v[64:67], a[208:211], a[176:179], v[64:67]\n\t"
"v_mfma_f32_16x16x32_bf16 v[68:71], a[252:255], a[172:175], v[68:71]\n\t"
"buffer_load_dwordx4 v[96:99], v252, s[8:11], s20 offen offset:1024 sc1\n\t"
"v_mfma_f32_16x16x32_bf16 v[68:71], v[240:243], a[176:179], v[68:71]\n\t"
"s_waitcnt lgkmcnt(1)\n\t"
"ds_read_b128 a[172:175], v244 offset:9728\n\t"
"v_mfma_f32_16x16x32_bf16 v[68:71], a[208:211], a[180:183], v[68:71]\n\t"
"buffer_load_dwordx4 a[244:247], v252, s[8:11], s20 offen offset:2048 sc1\n\t"
"v_mfma_f32_16x16x32_bf16 v[232:235], v[232:235], v[220:223], 0\n\t"
"s_mov_b32 s20, 417792\n\t"
"v_mfma_f32_16x16x32_bf16 v[72:75], a[252:255], a[176:179], v[72:75]\n\t"
"ds_read_b128 a[176:179], v244 offset:10240\n\t"
"v_mfma_f32_16x16x32_bf16 v[72:75], v[240:243], a[180:183], v[72:75]\n\t"
"buffer_load_dwordx4 a[248:251], v252, s[8:11], s20 offen offset:0 sc1\n\t"
"v_mfma_f32_16x16x32_bf16 v[72:75], a[208:211], a[184:187], v[72:75]\n\t"
"v_mfma_f32_16x16x32_bf16 v[76:79], a[252:255], a[180:183], v[76:79]\n\t"
"ds_read_b128 a[180:183], v244 offset:10752\n\t"
"v_mfma_f32_16x16x32_bf16 v[76:79], v[240:243], a[184:187], v[76:79]\n\t"
"v_cvt_pk_bf16_f32 v226, v232, v233\n\t"
"v_cvt_pk_bf16_f32 v227, v234, v235\n\t"
"v_mfma_f32_16x16x32_bf16 v[76:79], a[208:211], a[188:191], v[76:79]\n\t"
"ds_read_b64_tr_b16 v[232:233], v246 offset:26624\n\t"
"v_mfma_f32_16x16x32_bf16 v[80:83], a[252:255], a[184:187], v[80:83]\n\t"
"ds_read_b64_tr_b16 v[234:235], v246 offset:29696\n\t"
"v_mfma_f32_16x16x32_bf16 v[80:83], v[240:243], a[188:191], v[80:83]\n\t"
"ds_read_b128 a[184:187], v244 offset:11264\n\t"
"v_mfma_f32_16x16x32_bf16 v[80:83], a[208:211], a[192:195], v[80:83]\n\t"
"v_mfma_f32_16x16x32_bf16 v[84:87], a[252:255], a[188:191], v[84:87]\n\t"
"ds_read_b128 a[188:191], v244 offset:11776\n\t"
"v_mfma_f32_16x16x32_bf16 v[84:87], v[240:243], a[192:195], v[84:87]\n\t"
"v_mfma_f32_16x16x32_bf16 v[84:87], a[208:211], a[196:199], v[84:87]\n\t"
"v_mfma_f32_16x16x32_bf16 v[88:91], a[252:255], a[192:195], v[88:91]\n\t"
"ds_read_b128 a[192:195], v244 offset:12288\n\t"
"v_mfma_f32_16x16x32_bf16 v[88:91], v[240:243], a[196:199], v[88:91]\n\t"
"v_mfma_f32_16x16x32_bf16 v[88:91], a[208:211], a[200:203], v[88:91]\n\t"
"v_mfma_f32_16x16x32_bf16 v[92:95], a[252:255], a[196:199], v[92:95]\n\t"
"buffer_load_dwordx4 a[252:255], v252, s[8:11], s20 offen offset:1024 sc1\n\t"
"v_mfma_f32_16x16x32_bf16 v[92:95], v[240:243], a[200:203], v[92:95]\n\t"
"s_waitcnt vmcnt(11) lgkmcnt(3)\n\t"
"ds_read_b128 a[196:199], v244 offset:12800\n\t"
"v_mfma_f32_16x16x32_bf16 v[92:95], a[208:211], a[204:207], v[92:95]\n\t"
"ds_read_b128 a[200:203], v244 offset:13312\n\t"
"v_mfma_f32_16x16x32_bf16 v[232:235], v[232:235], v[220:223], 0\n\t"
"ds_read_b128 a[204:207], v244 offset:13824\n\t"
"v_mfma_f32_16x16x32_bf16 v[100:103], v[248:251], a[168:171], v[100:103]\n\t"
"buffer_load_dwordx4 v[240:243], v252, s[8:11], s20 offen offset:2048 sc1\n\t"
"v_mfma_f32_16x16x32_bf16 v[100:103], a[212:215], a[172:175], v[100:103]\n\t"
"s_mov_b32 s20, 430080\n\t"
"v_mfma_f32_16x16x32_bf16 v[100:103], a[216:219], a[176:179], v[100:103]\n\t"
"buffer_load_dwordx4 a[208:211], v252, s[8:11], s20 offen offset:0 sc1\n\t"
"v_mfma_f32_16x16x32_bf16 v[104:107], v[248:251], a[172:175], v[104:107]\n\t"
"s_waitcnt lgkmcnt(5)\n\t"
"v_mfma_f32_16x16x32_bf16 v[104:107], a[212:215], a[176:179], v[104:107]\n\t"
"v_cvt_pk_bf16_f32 v232, v232, v233\n\t"
"v_mfma_f32_16x16x32_bf16 v[104:107], a[216:219], a[180:183], v[104:107]\n\t"
"s_waitcnt lgkmcnt(0)\n\t"
"s_barrier\n\t"
"v_mfma_f32_16x16x32_bf16 v[108:111], v[248:251], a[176:179], v[108:111]\n\t"
"v_cvt_pk_bf16_f32 v233, v234, v235\n\t"
"v_mfma_f32_16x16x32_bf16 v[108:111], a[212:215], a[180:183], v[108:111]\n\t"
"v_mfma_f32_16x16x32_bf16 v[108:111], a[216:219], a[184:187], v[108:111]\n\t"
"ds_write2st64_b64 v245, v[228:229], v[230:231] offset0:0 offset1:1\n\t"
"v_mfma_f32_16x16x32_bf16 v[112:115], v[248:251], a[180:183], v[112:115]\n\t"
"v_mfma_f32_16x16x32_bf16 v[112:115], a[212:215], a[184:187], v[112:115]\n\t"
"ds_read_b64_tr_b16 v[228:229], v246 offset:32768\n\t"
"v_mfma_f32_16x16x32_bf16 v[112:115], a[216:219], a[188:191], v[112:115]\n\t"
"ds_write2st64_b64 v245, v[224:225], v[226:227] offset0:2 offset1:3\n\t"
"v_mfma_f32_16x16x32_bf16 v[116:119], v[248:251], a[184:187], v[116:119]\n\t"
"v_mfma_f32_16x16x32_bf16 v[116:119], a[212:215], a[188:191], v[116:119]\n\t"
"ds_read_b64_tr_b16 v[230:231], v246 offset:35840\n\t"
"v_mfma_f32_16x16x32_bf16 v[116:119], a[216:219], a[192:195], v[116:119]\n\t"
"ds_write_b64 v245, v[232:233] offset:2048\n\t"
"v_mfma_f32_16x16x32_bf16 v[120:123], v[248:251], a[188:191], v[120:123]\n\t"
"ds_read_b64_tr_b16 v[224:225], v246 offset:40448\n\t"
"v_mfma_f32_16x16x32_bf16 v[120:123], a[212:215], a[192:195], v[120:123]\n\t"
"ds_read_b64_tr_b16 v[226:227], v246 offset:43520\n\t"
"v_mfma_f32_16x16x32_bf16 v[120:123], a[216:219], a[196:199], v[120:123]\n\t"
"s_waitcnt lgkmcnt(3)\n\t"
"ds_read_b64_tr_b16 v[232:233], v246 offset:46592\n\t"
"v_mfma_f32_16x16x32_bf16 v[228:231], v[228:231], v[220:223], 0\n\t"
"ds_read_b64_tr_b16 v[234:235], v246 offset:49664\n\t"
"v_mfma_f32_16x16x32_bf16 v[124:127], v[248:251], a[192:195], v[124:127]\n\t"
"v_mfma_f32_16x16x32_bf16 v[124:127], a[212:215], a[196:199], v[124:127]\n\t"
"v_mfma_f32_16x16x32_bf16 v[124:127], a[216:219], a[200:203], v[124:127]\n\t"
"s_waitcnt lgkmcnt(2)\n\t"
"v_mfma_f32_16x16x32_bf16 v[224:227], v[224:227], v[220:223], 0\n\t"
"v_mfma_f32_16x16x32_bf16 v[128:131], v[248:251], a[196:199], v[128:131]\n\t"
"buffer_load_dwordx4 v[248:251], v252, s[8:11], s20 offen offset:1024 sc1\n\t"
"v_mfma_f32_16x16x32_bf16 v[128:131], a[212:215], a[200:203], v[128:131]\n\t"
"s_waitcnt vmcnt(11) lgkmcnt(0)\n\t"
"v_cvt_pk_bf16_f32 v228, v228, v229\n\t"
"v_cvt_pk_bf16_f32 v229, v230, v231\n\t"
"v_mfma_f32_16x16x32_bf16 v[128:131], a[216:219], a[204:207], v[128:131]\n\t"
"v_mfma_f32_16x16x32_bf16 v[232:235], v[232:235], v[220:223], 0\n\t"
"buffer_load_dwordx4 a[212:215], v252, s[8:11], s20 offen offset:2048 sc1\n\t"
"v_mfma_f32_16x16x32_bf16 v[132:135], a[220:223], a[168:171], v[132:135]\n\t"
"s_mov_b32 s20, 442368\n\t"
"v_cvt_pk_bf16_f32 v230, v224, v225\n\t"
"v_cvt_pk_bf16_f32 v231, v226, v227\n\t"
"v_mfma_f32_16x16x32_bf16 v[132:135], a[224:227], a[172:175], v[132:135]\n\t"
"s_add_u32 m0, s24, 46976\n\t"
"s_add_u32 s28, s21, 1376256\n\t"
"ds_read_b64_tr_b16 v[224:225], v246 offset:52736\n\t"
"v_mfma_f32_16x16x32_bf16 v[132:135], a[228:231], a[176:179], v[132:135]\n\t"
"buffer_load_dwordx4 a[216:219], v252, s[8:11], s20 offen offset:0 sc1\n\t"
"v_mfma_f32_16x16x32_bf16 v[136:139], a[220:223], a[172:175], v[136:139]\n\t"
"buffer_load_dwordx4 v247, s[12:15], s28 offen offset:128 nt lds\n\t"
"v_mfma_f32_16x16x32_bf16 v[136:139], a[224:227], a[176:179], v[136:139]\n\t"
"s_add_u32 m0, s24, 53120\n\t"
"s_add_u32 s28, s21, 1572864\n\t"
"ds_read_b64_tr_b16 v[226:227], v246 offset:55808\n\t"
"v_mfma_f32_16x16x32_bf16 v[136:139], a[228:231], a[180:183], v[136:139]\n\t"
"buffer_load_dwordx4 v247, s[12:15], s28 offen offset:128 nt lds\n\t"
"v_mfma_f32_16x16x32_bf16 v[140:143], a[220:223], a[176:179], v[140:143]\n\t"
"s_add_u32 m0, s24, 93056\n\t"
"s_add_u32 s28, s21, 2752512\n\t"
"v_cvt_pk_bf16_f32 v232, v232, v233\n\t"
"v_mfma_f32_16x16x32_bf16 v[140:143], a[224:227], a[180:183], v[140:143]\n\t"
"v_cvt_pk_bf16_f32 v233, v234, v235\n\t"
"v_mfma_f32_16x16x32_bf16 v[140:143], a[228:231], a[184:187], v[140:143]\n\t"
"ds_write2st64_b64 v245, v[228:229], v[230:231] offset0:5 offset1:6\n\t"
"v_mfma_f32_16x16x32_bf16 v[144:147], a[220:223], a[180:183], v[144:147]\n\t"
"buffer_load_dwordx4 v247, s[12:15], s28 offen offset:128 nt lds\n\t"
"v_mfma_f32_16x16x32_bf16 v[144:147], a[224:227], a[184:187], v[144:147]\n\t"
"s_add_u32 m0, s24, 100736\n\t"
"s_add_u32 s28, s21, 2949120\n\t"
"v_mfma_f32_16x16x32_bf16 v[144:147], a[228:231], a[188:191], v[144:147]\n\t"
"buffer_load_dwordx4 v247, s[12:15], s28 offen offset:128 nt lds\n\t"
"s_add_u32 m0, s24, 106880\n\t"
"s_add_u32 s28, s21, 3145728\n\t"
"v_mfma_f32_16x16x32_bf16 v[148:151], a[220:223], a[184:187], v[148:151]\n\t"
"buffer_load_dwordx4 v247, s[12:15], s28 offen offset:128 nt lds\n\t"
"v_mfma_f32_16x16x32_bf16 v[148:151], a[224:227], a[188:191], v[148:151]\n\t"
"s_waitcnt lgkmcnt(1)\n\t"
"s_add_u32 m0, s24, 113024\n\t"
"s_add_u32 s28, s21, 3342336\n\t"
"v_mfma_f32_16x16x32_bf16 v[148:151], a[228:231], a[192:195], v[148:151]\n\t"
"buffer_load_dwordx4 v247, s[12:15], s28 offen offset:128 nt lds\n\t"
"s_add_u32 m0, s25, 896\n\t"
"v_mfma_f32_16x16x32_bf16 v[224:227], v[224:227], v[220:223], 0\n\t"
"buffer_load_dwordx4 v254, s[12:15], 0 offen offset:128 nt lds\n\t"
"v_mfma_f32_16x16x32_bf16 v[152:155], a[220:223], a[188:191], v[152:155]\n\t"
"s_add_u32 m0, s26, 896\n\t"
"v_mfma_f32_16x16x32_bf16 v[152:155], a[224:227], a[192:195], v[152:155]\n\t"
"v_mfma_f32_16x16x32_bf16 v[152:155], a[228:231], a[196:199], v[152:155]\n\t"
"s_mov_b64 exec, s[36:37]\n\t"
"buffer_load_dwordx4 v254, s[12:15], s27 offen offset:128 nt lds\n\t"
"s_mov_b64 exec, -1\n\t"
"v_mfma_f32_16x16x32_bf16 v[156:159], a[220:223], a[192:195], v[156:159]\n\t"
"v_mfma_f32_16x16x32_bf16 v[156:159], a[224:227], a[196:199], v[156:159]\n\t"
"v_cvt_pk_bf16_f32 v234, v224, v225\n\t"
"v_cvt_pk_bf16_f32 v235, v226, v227\n\t"
"v_mfma_f32_16x16x32_bf16 v[156:159], a[228:231], a[200:203], v[156:159]\n\t"
"ds_write2st64_b64 v245, v[232:233], v[234:235] offset0:7 offset1:8\n\t"
"v_mfma_f32_16x16x32_bf16 v[160:163], a[220:223], a[196:199], v[160:163]\n\t"
"s_waitcnt vmcnt(18)\n\t"
"v_mfma_f32_16x16x32_bf16 v[160:163], a[224:227], a[200:203], v[160:163]\n\t"
"v_mfma_f32_16x16x32_bf16 v[160:163], a[228:231], a[204:207], v[160:163]\n\t"
"v_mfma_f32_16x16x32_bf16 v[164:167], a[232:235], a[168:171], v[164:167]\n\t"
"v_mfma_f32_16x16x32_bf16 v[164:167], a[236:239], a[172:175], v[164:167]\n\t"
"v_mfma_f32_16x16x32_bf16 v[164:167], a[240:243], a[176:179], v[164:167]\n\t"
"s_waitcnt lgkmcnt(0)\n\t"
"s_barrier\n\t"
"v_mfma_f32_16x16x32_bf16 v[168:171], a[232:235], a[172:175], v[168:171]\n\t"
"ds_read_b128 a[168:171], v244 offset:0\n\t"
"v_mfma_f32_16x16x32_bf16 v[168:171], a[236:239], a[176:179], v[168:171]\n\t"
"ds_read_b128 a[172:175], v244 offset:512\n\t"
"v_mfma_f32_16x16x32_bf16 v[168:171], a[240:243], a[180:183], v[168:171]\n\t"
"v_mfma_f32_16x16x32_bf16 v[172:175], a[232:235], a[176:179], v[172:175]\n\t"
"ds_read_b128 a[176:179], v244 offset:1024\n\t"
"v_mfma_f32_16x16x32_bf16 v[172:175], a[236:239], a[180:183], v[172:175]\n\t"
"v_mfma_f32_16x16x32_bf16 v[172:175], a[240:243], a[184:187], v[172:175]\n\t"
"v_mfma_f32_16x16x32_bf16 v[176:179], a[232:235], a[180:183], v[176:179]\n\t"
"ds_read_b128 a[180:183], v244 offset:1536\n\t"
"v_mfma_f32_16x16x32_bf16 v[176:179], a[236:239], a[184:187], v[176:179]\n\t"
"v_mfma_f32_16x16x32_bf16 v[176:179], a[240:243], a[188:191], v[176:179]\n\t"
"v_mfma_f32_16x16x32_bf16 v[180:183], a[232:235], a[184:187], v[180:183]\n\t"
"ds_read_b128 a[184:187], v244 offset:2048\n\t"
"v_mfma_f32_16x16x32_bf16 v[180:183], a[236:239], a[188:191], v[180:183]\n\t"
"v_mfma_f32_16x16x32_bf16 v[180:183], a[240:243], a[192:195], v[180:183]\n\t"
"v_mfma_f32_16x16x32_bf16 v[184:187], a[232:235], a[188:191], v[184:187]\n\t"
"ds_read_b128 a[188:191], v244 offset:2560\n\t"
"v_mfma_f32_16x16x32_bf16 v[184:187], a[236:239], a[192:195], v[184:187]\n\t"
"v_mfma_f32_16x16x32_bf16 v[184:187], a[240:243], a[196:199], v[184:187]\n\t"
"v_mfma_f32_16x16x32_bf16 v[188:191], a[232:235], a[192:195], v[188:191]\n\t"
"ds_read_b128 a[192:195], v244 offset:3072\n\t"
"v_mfma_f32_16x16x32_bf16 v[188:191], a[236:239], a[196:199], v[188:191]\n\t"
"v_mfma_f32_16x16x32_bf16 v[188:191], a[240:243], a[200:203], v[188:191]\n\t"
"v_mfma_f32_16x16x32_bf16 v[192:195], a[232:235], a[196:199], v[192:195]\n\t"
"s_waitcnt vmcnt(15) lgkmcnt(4)\n\t"
"ds_read_b128 a[196:199], v244 offset:3584\n\t"
"v_mfma_f32_16x16x32_bf16 v[192:195], a[236:239], a[200:203], v[192:195]\n\t"
"ds_read_b128 a[200:203], v244 offset:4096\n\t"
"v_mfma_f32_16x16x32_bf16 v[192:195], a[240:243], a[204:207], v[192:195]\n\t"
"ds_read_b128 a[204:207], v244 offset:4608\n\t"
"Lst5_%=:\n\t"
"v_mfma_f32_16x16x32_bf16 v[196:199], v[236:239], a[168:171], v[196:199]\n\t"
"s_waitcnt lgkmcnt(6)\n\t"
"v_mfma_f32_16x16x32_bf16 v[196:199], v[96:99], a[172:175], v[196:199]\n\t"
"v_mfma_f32_16x16x32_bf16 v[196:199], a[244:247], a[176:179], v[196:199]\n\t"
"v_mfma_f32_16x16x32_bf16 v[204:207], v[236:239], a[172:175], v[204:207]\n\t"
"s_waitcnt lgkmcnt(5)\n\t"
"v_mfma_f32_16x16x32_bf16 v[204:207], v[96:99], a[176:179], v[204:207]\n\t"
"v_mfma_f32_16x16x32_bf16 v[204:207], a[244:247], a[180:183], v[204:207]\n\t"
"v_mfma_f32_16x16x32_bf16 v[212:215], v[236:239], a[176:179], v[212:215]\n\t"
"s_waitcnt lgkmcnt(4)\n\t"
"v_mfma_f32_16x16x32_bf16 v[212:215], v[96:99], a[180:183], v[212:215]\n\t"
"v_mfma_f32_16x16x32_bf16 v[212:215], a[244:247], a[184:187], v[212:215]\n\t"
"v_mfma_f32_16x16x32_bf16 a[0:3], v[236:239], a[180:183], a[0:3]\n\t"
"s_waitcnt lgkmcnt(3)\n\t"
"v_mfma_f32_16x16x32_bf16 a[0:3], v[96:99], a[184:187], a[0:3]\n\t"
"buffer_load_dwordx4 a[220:223], v252, s[8:11], s20 offen offset:1024 sc1\n\t"
"v_mfma_f32_16x16x32_bf16 a[0:3], a[244:247], a[188:191], a[0:3]\n\t"
"v_mfma_f32_16x16x32_bf16 a[8:11], v[236:239], a[184:187], a[8:11]\n\t"
"s_waitcnt lgkmcnt(2)\n\t"
"v_mfma_f32_16x16x32_bf16 a[8:11], v[96:99], a[188:191], a[8:11]\n\t"
"v_mfma_f32_16x16x32_bf16 a[8:11], a[244:247], a[192:195], a[8:11]\n\t"
"ds_read_b64_tr_b16 v[228:229], v246 offset:1024\n\t"
"v_mfma_f32_16x16x32_bf16 a[16:19], v[236:239], a[188:191], a[16:19]\n\t"
"s_waitcnt lgkmcnt(2)\n\t"
"ds_read_b64_tr_b16 v[230:231], v246 offset:4096\n\t"
"v_mfma_f32_16x16x32_bf16 a[16:19], v[96:99], a[192:195], a[16:19]\n\t"
"v_mfma_f32_16x16x32_bf16 a[16:19], a[244:247], a[196:199], a[16:19]\n\t"
"v_mfma_f32_16x16x32_bf16 a[24:27], v[236:239], a[192:195], a[24:27]\n\t"
"s_waitcnt lgkmcnt(2)\n\t"
"v_mfma_f32_16x16x32_bf16 a[24:27], v[96:99], a[196:199], a[24:27]\n\t"
"v_mfma_f32_16x16x32_bf16 a[24:27], a[244:247], a[200:203], a[24:27]\n\t"
"v_mfma_f32_16x16x32_bf16 a[32:35], v[236:239], a[196:199], a[32:35]\n\t"
"s_waitcnt vmcnt(13)\n\t"
"ds_read_b64_tr_b16 v[224:225], v246 offset:7168\n\t"
"v_mfma_f32_16x16x32_bf16 a[32:35], v[96:99], a[200:203], a[32:35]\n\t"
"ds_read_b64_tr_b16 v[226:227], v246 offset:10240\n\t"
"v_mfma_f32_16x16x32_bf16 a[32:35], a[244:247], a[204:207], a[32:35]\n\t"
"v_mfma_f32_16x16x32_bf16 a[40:43], a[248:251], a[168:171], a[40:43]\n\t"
"v_mfma_f32_16x16x32_bf16 a[40:43], a[252:255], a[172:175], a[40:43]\n\t"
"v_mfma_f32_16x16x32_bf16 a[40:43], v[240:243], a[176:179], a[40:43]\n\t"
"v_mfma_f32_16x16x32_bf16 a[48:51], a[248:251], a[172:175], a[48:51]\n\t"
"s_waitcnt lgkmcnt(2)\n\t"
"v_mfma_f32_16x16x32_bf16 a[48:51], a[252:255], a[176:179], a[48:51]\n\t"
"v_mfma_f32_16x16x32_bf16 a[48:51], v[240:243], a[180:183], a[48:51]\n\t"
"v_mfma_f32_16x16x32_bf16 v[228:231], v[228:231], v[220:223], 0\n\t"
"v_mfma_f32_16x16x32_bf16 a[56:59], a[248:251], a[176:179], a[56:59]\n\t"
"ds_read_b64_tr_b16 v[232:233], v246 offset:13312\n\t"
"v_mfma_f32_16x16x32_bf16 a[56:59], a[252:255], a[180:183], a[56:59]\n\t"
"ds_read_b64_tr_b16 v[234:235], v246 offset:16384\n\t"
"v_mfma_f32_16x16x32_bf16 a[56:59], v[240:243], a[184:187], a[56:59]\n\t"
"v_mfma_f32_16x16x32_bf16 a[64:67], a[248:251], a[180:183], a[64:67]\n\t"
"s_waitcnt lgkmcnt(2)\n\t"
"v_mfma_f32_16x16x32_bf16 a[64:67], a[252:255], a[184:187], a[64:67]\n\t"
"v_cvt_pk_bf16_f32 v228, v228, v229\n\t"
"v_cvt_pk_bf16_f32 v229, v230, v231\n\t"
"v_mfma_f32_16x16x32_bf16 a[64:67], v[240:243], a[188:191], a[64:67]\n\t"
"v_mfma_f32_16x16x32_bf16 v[224:227], v[224:227], v[220:223], 0\n\t"
"buffer_load_dwordx4 v[236:239], v252, s[8:11], s20 offen offset:2048 sc1\n\t"
"v_mfma_f32_16x16x32_bf16 a[72:75], a[248:251], a[184:187], a[72:75]\n\t"
"s_mov_b32 s20, 454656\n\t"
"v_mfma_f32_16x16x32_bf16 a[72:75], a[252:255], a[188:191], a[72:75]\n\t"
"v_mfma_f32_16x16x32_bf16 a[72:75], v[240:243], a[192:195], a[72:75]\n\t"
"v_mfma_f32_16x16x32_bf16 a[80:83], a[248:251], a[188:191], a[80:83]\n\t"
"buffer_load_dwordx4 v[96:99], v252, s[8:11], s20 offen offset:0 sc1\n\t"
"v_mfma_f32_16x16x32_bf16 a[80:83], a[252:255], a[192:195], a[80:83]\n\t"
"v_cvt_pk_bf16_f32 v230, v224, v225\n\t"
"v_cvt_pk_bf16_f32 v231, v226, v227\n\t"
"v_mfma_f32_16x16x32_bf16 a[80:83], v[240:243], a[196:199], a[80:83]\n\t"
"ds_read_b64_tr_b16 v[224:225], v246 offset:20992\n\t"
"v_mfma_f32_16x16x32_bf16 a[88:91], a[248:251], a[192:195], a[88:91]\n\t"
"s_waitcnt lgkmcnt(1)\n\t"
"ds_read_b64_tr_b16 v[226:227], v246 offset:24064\n\t"
"v_mfma_f32_16x16x32_bf16 a[88:91], a[252:255], a[196:199], a[88:91]\n\t"
"buffer_load_dwordx4 a[224:227], v252, s[8:11], s20 offen offset:1024 sc1\n\t"
"v_mfma_f32_16x16x32_bf16 a[88:91], v[240:243], a[200:203], a[88:91]\n\t"
"v_mfma_f32_16x16x32_bf16 v[232:235], v[232:235], v[220:223], 0\n\t"
"v_mfma_f32_16x16x32_bf16 a[96:99], a[248:251], a[196:199], a[96:99]\n\t"
"s_waitcnt vmcnt(13)\n\t"
"v_mfma_f32_16x16x32_bf16 a[96:99], a[252:255], a[200:203], a[96:99]\n\t"
"buffer_load_dwordx4 a[228:231], v252, s[8:11], s20 offen offset:2048 sc1\n\t"
"v_mfma_f32_16x16x32_bf16 a[96:99], v[240:243], a[204:207], a[96:99]\n\t"
"s_mov_b32 s20, 466944\n\t"
"v_mfma_f32_16x16x32_bf16 a[104:107], a[208:211], a[168:171], a[104:107]\n\t"
"ds_read_b128 a[168:171], v244 offset:9216\n\t"
"v_mfma_f32_16x16x32_bf16 a[104:107], v[248:251], a[172:175], a[104:107]\n\t"
"v_cvt_pk_bf16_f32 v232, v232, v233\n\t"
"v_cvt_pk_bf16_f32 v233, v234, v235\n\t"
"v_mfma_f32_16x16x32_bf16 a[104:107], a[212:215], a[176:179], a[104:107]\n\t"
"v_mfma_f32_16x16x32_bf16 a[112:115], a[208:211], a[172:175], a[112:115]\n\t"
"buffer_load_dwordx4 a[232:235], v252, s[8:11], s20 offen offset:0 sc1\n\t"
"v_mfma_f32_16x16x32_bf16 a[112:115], v[248:251], a[176:179], a[112:115]\n\t"
"s_waitcnt lgkmcnt(1)\n\t"
"ds_read_b128 a[172:175], v244 offset:9728\n\t"
"v_mfma_f32_16x16x32_bf16 a[112:115], a[212:215], a[180:183], a[112:115]\n\t"
"v_mfma_f32_16x16x32_bf16 v[224:227], v[224:227], v[220:223], 0\n\t"
"buffer_load_dwordx4 a[236:239], v252, s[8:11], s20 offen offset:1024 sc1\n\t"
"v_mfma_f32_16x16x32_bf16 a[120:123], a[208:211], a[176:179], a[120:123]\n\t"
"ds_read_b128 a[176:179], v244 offset:10240\n\t"
"v_mfma_f32_16x16x32_bf16 a[120:123], v[248:251], a[180:183], a[120:123]\n\t"
"v_mfma_f32_16x16x32_bf16 a[120:123], a[212:215], a[184:187], a[120:123]\n\t"
"v_mfma_f32_16x16x32_bf16 a[128:131], a[208:211], a[180:183], a[128:131]\n\t"
"buffer_load_dwordx4 a[240:243], v252, s[8:11], s20 offen offset:2048 sc1\n\t"
"v_mfma_f32_16x16x32_bf16 a[128:131], v[248:251], a[184:187], a[128:131]\n\t"
"s_mov_b32 s20, 700416\n\t"
"ds_read_b128 a[180:183], v244 offset:10752\n\t"
"v_cvt_pk_bf16_f32 v234, v224, v225\n\t"
"v_mfma_f32_16x16x32_bf16 a[128:131], a[212:215], a[188:191], a[128:131]\n\t"
"v_cvt_pk_bf16_f32 v235, v226, v227\n\t"
"v_mfma_f32_16x16x32_bf16 a[136:139], a[208:211], a[184:187], a[136:139]\n\t"
"ds_read_b64_tr_b16 v[224:225], v246 offset:27136\n\t"
"v_mfma_f32_16x16x32_bf16 a[136:139], v[248:251], a[188:191], a[136:139]\n\t"
"ds_read_b64_tr_b16 v[226:227], v246 offset:30208\n\t"
"v_mfma_f32_16x16x32_bf16 a[136:139], a[212:215], a[192:195], a[136:139]\n\t"
"buffer_load_dwordx4 a[244:247], v252, s[8:11], s20 offen offset:0 sc1\n\t"
"v_mfma_f32_16x16x32_bf16 a[144:147], a[208:211], a[188:191], a[144:147]\n\t"
"ds_read_b128 a[184:187], v244 offset:11264\n\t"
"v_mfma_f32_16x16x32_bf16 a[144:147], v[248:251], a[192:195], a[144:147]\n\t"
"ds_read_b128 a[188:191], v244 offset:11776\n\t"
"v_mfma_f32_16x16x32_bf16 a[144:147], a[212:215], a[196:199], a[144:147]\n\t"
"buffer_load_dwordx4 a[248:251], v252, s[8:11], s20 offen offset:1024 sc1\n\t"
"v_mfma_f32_16x16x32_bf16 a[152:155], a[208:211], a[192:195], a[152:155]\n\t"
"ds_read_b128 a[192:195], v244 offset:12288\n\t"
"v_mfma_f32_16x16x32_bf16 a[152:155], v[248:251], a[196:199], a[152:155]\n\t"
"v_mfma_f32_16x16x32_bf16 a[152:155], a[212:215], a[200:203], a[152:155]\n\t"
"v_mfma_f32_16x16x32_bf16 a[160:163], a[208:211], a[196:199], a[160:163]\n\t"
"buffer_load_dwordx4 a[252:255], v252, s[8:11], s20 offen offset:2048 sc1\n\t"
"v_mfma_f32_16x16x32_bf16 a[160:163], v[248:251], a[200:203], a[160:163]\n\t"
"s_waitcnt vmcnt(9) lgkmcnt(3)\n\t"
"ds_read_b128 a[196:199], v244 offset:12800\n\t"
"s_mov_b32 s20, 712704\n\t"
"v_mfma_f32_16x16x32_bf16 a[160:163], a[212:215], a[204:207], a[160:163]\n\t"
"ds_read_b128 a[200:203], v244 offset:13312\n\t"
"v_mfma_f32_16x16x32_bf16 v[224:227], v[224:227], v[220:223], 0\n\t"
"ds_read_b128 a[204:207], v244 offset:13824\n\t"
"v_mfma_f32_16x16x32_bf16 v[200:203], a[216:219], a[168:171], v[200:203]\n\t"
"buffer_load_dwordx4 v[248:251], v252, s[8:11], s20 offen offset:0 sc1\n\t"
"v_mfma_f32_16x16x32_bf16 v[200:203], a[220:223], a[172:175], v[200:203]\n\t"
"v_mfma_f32_16x16x32_bf16 v[200:203], v[236:239], a[176:179], v[200:203]\n\t"
"v_mfma_f32_16x16x32_bf16 v[208:211], a[216:219], a[172:175], v[208:211]\n\t"
"buffer_load_dwordx4 a[212:215], v252, s[8:11], s20 offen offset:1024 sc1\n\t"
"v_mfma_f32_16x16x32_bf16 v[208:211], a[220:223], a[176:179], v[208:211]\n\t"
"s_waitcnt lgkmcnt(5)\n\t"
"v_cvt_pk_bf16_f32 v224, v224, v225\n\t"
"v_mfma_f32_16x16x32_bf16 v[208:211], v[236:239], a[180:183], v[208:211]\n\t"
"s_waitcnt lgkmcnt(0)\n\t"
"s_barrier\n\t"
"v_mfma_f32_16x16x32_bf16 v[216:219], a[216:219], a[176:179], v[216:219]\n\t"
"v_cvt_pk_bf16_f32 v225, v226, v227\n\t"
"v_mfma_f32_16x16x32_bf16 v[216:219], a[220:223], a[180:183], v[216:219]\n\t"
"v_mfma_f32_16x16x32_bf16 v[216:219], v[236:239], a[184:187], v[216:219]\n\t"
"ds_write2st64_b64 v245, v[228:229], v[230:231] offset0:0 offset1:1\n\t"
"v_mfma_f32_16x16x32_bf16 a[4:7], a[216:219], a[180:183], a[4:7]\n\t"
"v_mfma_f32_16x16x32_bf16 a[4:7], a[220:223], a[184:187], a[4:7]\n\t"
"ds_read_b64_tr_b16 v[228:229], v246 offset:33280\n\t"
"v_mfma_f32_16x16x32_bf16 a[4:7], v[236:239], a[188:191], a[4:7]\n\t"
"ds_write2st64_b64 v245, v[232:233], v[234:235] offset0:2 offset1:3\n\t"
"v_mfma_f32_16x16x32_bf16 a[12:15], a[216:219], a[184:187], a[12:15]\n\t"
"v_mfma_f32_16x16x32_bf16 a[12:15], a[220:223], a[188:191], a[12:15]\n\t"
"ds_read_b64_tr_b16 v[230:231], v246 offset:36352\n\t"
"v_mfma_f32_16x16x32_bf16 a[12:15], v[236:239], a[192:195], a[12:15]\n\t"
"ds_write_b64 v245, v[224:225] offset:2048\n\t"
"v_mfma_f32_16x16x32_bf16 a[20:23], a[216:219], a[188:191], a[20:23]\n\t"
"ds_read_b64_tr_b16 v[232:233], v246 offset:40960\n\t"
"v_mfma_f32_16x16x32_bf16 a[20:23], a[220:223], a[192:195], a[20:23]\n\t"
"ds_read_b64_tr_b16 v[234:235], v246 offset:44032\n\t"
"v_mfma_f32_16x16x32_bf16 a[20:23], v[236:239], a[196:199], a[20:23]\n\t"
"s_waitcnt lgkmcnt(3)\n\t"
"ds_read_b64_tr_b16 v[224:225], v246 offset:47104\n\t"
"v_mfma_f32_16x16x32_bf16 v[228:231], v[228:231], v[220:223], 0\n\t"
"ds_read_b64_tr_b16 v[226:227], v246 offset:50176\n\t"
"v_mfma_f32_16x16x32_bf16 a[28:31], a[216:219], a[192:195], a[28:31]\n\t"
"global_load_dwordx4 v[240:243], v252, s[48:49] offset:2048\n\t"
"v_mfma_f32_16x16x32_bf16 a[28:31], a[220:223], a[196:199], a[28:31]\n\t"
"global_load_dwordx4 a[208:211], v252, s[46:47] offset:1024\n\t"
"v_mfma_f32_16x16x32_bf16 a[28:31], v[236:239], a[200:203], a[28:31]\n\t"
"s_waitcnt lgkmcnt(2)\n\t"
"v_mfma_f32_16x16x32_bf16 v[232:235], v[232:235], v[220:223], 0\n\t"
"v_mfma_f32_16x16x32_bf16 a[36:39], a[216:219], a[196:199], a[36:39]\n\t"
"buffer_load_dwordx4 a[216:219], v252, s[8:11], s20 offen offset:2048 sc1\n\t"
"v_mfma_f32_16x16x32_bf16 a[36:39], a[220:223], a[200:203], a[36:39]\n\t"
"s_waitcnt vmcnt(11) lgkmcnt(0)\n\t"
"s_mov_b32 s20, 724992\n\t"
"v_cvt_pk_bf16_f32 v228, v228, v229\n\t"
"v_mfma_f32_16x16x32_bf16 a[36:39], v[236:239], a[204:207], a[36:39]\n\t"
"v_cvt_pk_bf16_f32 v229, v230, v231\n\t"
"v_mfma_f32_16x16x32_bf16 v[224:227], v[224:227], v[220:223], 0\n\t"
"buffer_load_dwordx4 a[220:223], v252, s[8:11], s20 offen offset:0 sc1\n\t"
"v_mfma_f32_16x16x32_bf16 a[44:47], v[96:99], a[168:171], a[44:47]\n\t"
"v_cvt_pk_bf16_f32 v230, v232, v233\n\t"
"v_cvt_pk_bf16_f32 v231, v234, v235\n\t"
"v_mfma_f32_16x16x32_bf16 a[44:47], a[224:227], a[172:175], a[44:47]\n\t"
"ds_read_b64_tr_b16 v[232:233], v246 offset:53248\n\t"
"v_mfma_f32_16x16x32_bf16 a[44:47], a[228:231], a[176:179], a[44:47]\n\t"
"ds_write2st64_b64 v245, v[228:229], v[230:231] offset0:5 offset1:6\n\t"
"v_mfma_f32_16x16x32_bf16 a[52:55], v[96:99], a[172:175], a[52:55]\n\t"
"v_mfma_f32_16x16x32_bf16 a[52:55], a[224:227], a[176:179], a[52:55]\n\t"
"ds_read_b64_tr_b16 v[234:235], v246 offset:56320\n\t"
"v_mfma_f32_16x16x32_bf16 a[52:55], a[228:231], a[180:183], a[52:55]\n\t"
"v_cvt_pk_bf16_f32 v224, v224, v225\n\t"
"v_cvt_pk_bf16_f32 v225, v226, v227\n\t"
"v_mfma_f32_16x16x32_bf16 a[60:63], v[96:99], a[176:179], a[60:63]\n\t"
"v_mfma_f32_16x16x32_bf16 a[60:63], a[224:227], a[180:183], a[60:63]\n\t"
"v_mfma_f32_16x16x32_bf16 a[60:63], a[228:231], a[184:187], a[60:63]\n\t"
"v_mfma_f32_16x16x32_bf16 a[68:71], v[96:99], a[180:183], a[68:71]\n\t"
"v_mfma_f32_16x16x32_bf16 a[68:71], a[224:227], a[184:187], a[68:71]\n\t"
"v_mfma_f32_16x16x32_bf16 a[68:71], a[228:231], a[188:191], a[68:71]\n\t"
"v_mfma_f32_16x16x32_bf16 a[76:79], v[96:99], a[184:187], a[76:79]\n\t"
"s_waitcnt lgkmcnt(0)\n\t"
"v_mfma_f32_16x16x32_bf16 a[76:79], a[224:227], a[188:191], a[76:79]\n\t"
"v_mfma_f32_16x16x32_bf16 a[76:79], a[228:231], a[192:195], a[76:79]\n\t"
"v_mfma_f32_16x16x32_bf16 v[232:235], v[232:235], v[220:223], 0\n\t"
"v_mfma_f32_16x16x32_bf16 a[84:87], v[96:99], a[188:191], a[84:87]\n\t"
"v_mfma_f32_16x16x32_bf16 a[84:87], a[224:227], a[192:195], a[84:87]\n\t"
"v_mfma_f32_16x16x32_bf16 a[84:87], a[228:231], a[196:199], a[84:87]\n\t"
"v_mfma_f32_16x16x32_bf16 a[92:95], v[96:99], a[192:195], a[92:95]\n\t"
"v_mfma_f32_16x16x32_bf16 a[92:95], a[224:227], a[196:199], a[92:95]\n\t"
"v_cvt_pk_bf16_f32 v226, v232, v233\n\t"
"v_cvt_pk_bf16_f32 v227, v234, v235\n\t"
"v_mfma_f32_16x16x32_bf16 a[92:95], a[228:231], a[200:203], a[92:95]\n\t"
"ds_write2st64_b64 v245, v[224:225], v[226:227] offset0:7 offset1:8\n\t"
"v_mfma_f32_16x16x32_bf16 a[100:103], v[96:99], a[196:199], a[100:103]\n\t"
"buffer_load_dwordx4 v[96:99], v252, s[8:11], s20 offen offset:1024 sc1\n\t"
"v_mfma_f32_16x16x32_bf16 a[100:103], a[224:227], a[200:203], a[100:103]\n\t"
"s_waitcnt vmcnt(10)\n\t"
"v_mfma_f32_16x16x32_bf16 a[100:103], a[228:231], a[204:207], a[100:103]\n\t"
"v_mfma_f32_16x16x32_bf16 a[108:111], a[232:235], a[168:171], a[108:111]\n\t"
"buffer_load_dwordx4 a[224:227], v252, s[8:11], s20 offen offset:2048 sc1\n\t"
"v_mfma_f32_16x16x32_bf16 a[108:111], a[236:239], a[172:175], a[108:111]\n\t"
"s_mov_b32 s20, 737280\n\t"
"v_mfma_f32_16x16x32_bf16 a[108:111], a[240:243], a[176:179], a[108:111]\n\t"
"s_waitcnt lgkmcnt(0)\n\t"
"s_barrier\n\t"
"v_mfma_f32_16x16x32_bf16 a[116:119], a[232:235], a[172:175], a[116:119]\n\t"
"s_waitcnt vmcnt(5)\n\t"
"ds_read_b64_tr_b16 v[236:237], v240 offset:1024\n\t"
"v_mfma_f32_16x16x32_bf16 a[116:119], a[236:239], a[176:179], a[116:119]\n\t"
"ds_read_b64_tr_b16 v[238:239], v240 offset:2560\n\t"
"v_mfma_f32_16x16x32_bf16 a[116:119], a[240:243], a[180:183], a[116:119]\n\t"
"ds_read_b64_tr_b16 v[228:229], v241 offset:1024\n\t"
"v_mfma_f32_16x16x32_bf16 a[124:127], a[232:235], a[176:179], a[124:127]\n\t"
"ds_read_b64_tr_b16 v[230:231], v241 offset:2560\n\t"
"v_mfma_f32_16x16x32_bf16 a[124:127], a[236:239], a[180:183], a[124:127]\n\t"
"buffer_load_dwordx4 a[228:231], v252, s[8:11], s20 offen offset:0 sc1\n\t"
"v_mfma_f32_16x16x32_bf16 a[124:127], a[240:243], a[184:187], a[124:127]\n\t"
"ds_read_b64_tr_b16 v[232:233], v240 offset:20992\n\t"
"v_mfma_f32_16x16x32_bf16 a[132:135], a[232:235], a[180:183], a[132:135]\n\t"
"s_waitcnt vmcnt(5) lgkmcnt(3)\n\t"
"ds_read_b64_tr_b16 v[234:235], v240 offset:22528\n\t"
"v_mfma_f32_16x16x32_bf16 a[132:135], a[236:239], a[184:187], a[132:135]\n\t"
"ds_read_b64_tr_b16 v[224:225], v241 offset:20992\n\t"
"v_mfma_f32_16x16x32_bf16 a[132:135], a[240:243], a[188:191], a[132:135]\n\t"
"s_waitcnt lgkmcnt(3)\n\t"
"ds_read_b64_tr_b16 v[226:227], v241 offset:22528\n\t"
"v_mfma_f32_16x16x32_bf16 v[236:239], v[236:239], a[208:211], 0\n\t"
"ds_read_b128 a[168:171], v244 offset:0\n\t"
"v_mfma_f32_16x16x32_bf16 v[228:231], v[228:231], a[208:211], 0\n\t"
"ds_read_b128 a[172:175], v244 offset:512\n\t"
"v_mfma_f32_16x16x32_bf16 a[140:143], a[232:235], a[184:187], a[140:143]\n\t"
"ds_read_b128 a[176:179], v244 offset:1024\n\t"
"v_mfma_f32_16x16x32_bf16 a[140:143], a[236:239], a[188:191], a[140:143]\n\t"
"ds_read_b128 a[180:183], v244 offset:1536\n\t"
"v_mfma_f32_16x16x32_bf16 a[140:143], a[240:243], a[192:195], a[140:143]\n\t"
"ds_read_b128 a[184:187], v244 offset:2048\n\t"
"v_mfma_f32_16x16x32_bf16 a[148:151], a[232:235], a[188:191], a[148:151]\n\t"
"s_waitcnt lgkmcnt(7)\n\t"
"ds_read_b128 a[188:191], v244 offset:2560\n\t"
"v_cvt_pk_bf16_f32 v236, v236, v237\n\t"
"v_mfma_f32_16x16x32_bf16 a[148:151], a[236:239], a[192:195], a[148:151]\n\t"
"s_waitcnt lgkmcnt(6)\n\t"
"v_cvt_pk_bf16_f32 v237, v238, v239\n\t"
"v_cvt_pk_bf16_f32 v238, v228, v229\n\t"
"v_mfma_f32_16x16x32_bf16 a[148:151], a[240:243], a[196:199], a[148:151]\n\t"
"v_cvt_pk_bf16_f32 v239, v230, v231\n\t"
"v_mfma_f32_16x16x32_bf16 v[232:235], v[232:235], a[208:211], 0\n\t"
"ds_write_b128 v242, v[236:239] offset:24576\n\t"
"v_mfma_f32_16x16x32_bf16 v[224:227], v[224:227], a[208:211], 0\n\t"
"v_mfma_f32_16x16x32_bf16 a[156:159], a[232:235], a[192:195], a[156:159]\n\t"
"ds_read_b128 a[192:195], v244 offset:3072\n\t"
"v_mfma_f32_16x16x32_bf16 a[156:159], a[236:239], a[196:199], a[156:159]\n\t"
"ds_read_b64_tr_b16 v[228:229], v240 offset:40960\n\t"
"v_mfma_f32_16x16x32_bf16 a[156:159], a[240:243], a[200:203], a[156:159]\n\t"
"ds_read_b64_tr_b16 v[230:231], v240 offset:42496\n\t"
"v_mfma_f32_16x16x32_bf16 a[164:167], a[232:235], a[196:199], a[164:167]\n\t"
"buffer_load_dwordx4 a[232:235], v252, s[8:11], s20 offen offset:1024 sc1\n\t"
"v_mfma_f32_16x16x32_bf16 a[164:167], a[236:239], a[200:203], a[164:167]\n\t"
"s_waitcnt lgkmcnt(7)\n\t"
"ds_read_b64_tr_b16 v[236:237], v241 offset:40960\n\t"
"v_cvt_pk_bf16_f32 v232, v232, v233\n\t"
"v_mfma_f32_16x16x32_bf16 a[164:167], a[240:243], a[204:207], a[164:167]\n\t"
"ds_read_b64_tr_b16 v[238:239], v241 offset:42496\n\t"
"v_cvt_pk_bf16_f32 v233, v234, v235\n\t"
"Lst6_%=:\n\t"
"v_mfma_f32_16x16x32_bf16 v[196:199], a[244:247], a[168:171], v[196:199]\n\t"
"s_waitcnt lgkmcnt(8)\n\t"
"ds_read_b128 a[196:199], v244 offset:3584\n\t"
"v_cvt_pk_bf16_f32 v234, v224, v225\n\t"
"v_mfma_f32_16x16x32_bf16 v[196:199], a[248:251], a[172:175], v[196:199]\n\t"
"ds_read_b128 a[200:203], v244 offset:4096\n\t"
"v_cvt_pk_bf16_f32 v235, v226, v227\n\t"
"v_mfma_f32_16x16x32_bf16 v[196:199], a[252:255], a[176:179], v[196:199]\n\t"
"ds_write_b128 v242, v[232:235] offset:26112\n\t"
"v_mfma_f32_16x16x32_bf16 v[204:207], a[244:247], a[172:175], v[204:207]\n\t"
"buffer_load_dwordx4 a[236:239], v252, s[8:11], s20 offen offset:2048 sc1\n\t"
"v_mfma_f32_16x16x32_bf16 v[204:207], a[248:251], a[176:179], v[204:207]\n\t"
"s_waitcnt lgkmcnt(3)\n\t"
"ds_read_b128 a[204:207], v244 offset:4608\n\t"
"v_mfma_f32_16x16x32_bf16 v[204:207], a[252:255], a[180:183], v[204:207]\n\t"
"Lpg5_%=:\n\t"
"s_cbranch_vccnz Lpg9_%=\n\t"
"s_mov_b32 s20, 749568\n\t"
"v_add_u32 v240, 59904, v240\n\t"
"v_mfma_f32_16x16x32_bf16 v[212:215], a[244:247], a[176:179], v[212:215]\n\t"
"v_add_u32 v241, 59904, v241\n\t"
"v_mfma_f32_16x16x32_bf16 v[212:215], a[248:251], a[180:183], v[212:215]\n\t"
"buffer_load_dwordx4 a[240:243], v252, s[8:11], s20 offen offset:0 sc1\n\t"
"v_mfma_f32_16x16x32_bf16 v[212:215], a[252:255], a[184:187], v[212:215]\n\t"
"ds_read_b64_tr_b16 v[224:225], v240 offset:1024\n\t"
"v_mfma_f32_16x16x32_bf16 v[228:231], v[228:231], a[208:211], 0\n\t"
"ds_read_b64_tr_b16 v[226:227], v240 offset:2560\n\t"
"v_mfma_f32_16x16x32_bf16 v[236:239], v[236:239], a[208:211], 0\n\t"
"ds_read_b64_tr_b16 v[232:233], v241 offset:1024\n\t"
"v_mfma_f32_16x16x32_bf16 a[0:3], a[244:247], a[180:183], a[0:3]\n\t"
"ds_read_b64_tr_b16 v[234:235], v241 offset:2560\n\t"
"v_mfma_f32_16x16x32_bf16 a[0:3], a[248:251], a[184:187], a[0:3]\n\t"
"v_mfma_f32_16x16x32_bf16 a[0:3], a[252:255], a[188:191], a[0:3]\n\t"
"v_mfma_f32_16x16x32_bf16 a[8:11], a[244:247], a[184:187], a[8:11]\n\t"
"s_waitcnt lgkmcnt(2)\n\t"
"v_cvt_pk_bf16_f32 v228, v228, v229\n\t"
"v_cvt_pk_bf16_f32 v229, v230, v231\n\t"
"v_mfma_f32_16x16x32_bf16 a[8:11], a[248:251], a[188:191], a[8:11]\n\t"
"v_cvt_pk_bf16_f32 v230, v236, v237\n\t"
"v_cvt_pk_bf16_f32 v231, v238, v239\n\t"
"v_mfma_f32_16x16x32_bf16 a[8:11], a[252:255], a[192:195], a[8:11]\n\t"
"s_waitcnt lgkmcnt(0)\n\t"
"ds_write_b128 v242, v[228:231] offset:27648\n\t"
"v_mfma_f32_16x16x32_bf16 v[224:227], v[224:227], a[208:211], 0\n\t"
"v_mfma_f32_16x16x32_bf16 v[232:235], v[232:235], a[208:211], 0\n\t"
"ds_read_b64_tr_b16 v[236:237], v240 offset:20992\n\t"
"v_mfma_f32_16x16x32_bf16 a[16:19], a[244:247], a[188:191], a[16:19]\n\t"
"ds_read_b64_tr_b16 v[238:239], v240 offset:22528\n\t"
"v_mfma_f32_16x16x32_bf16 a[16:19], a[248:251], a[192:195], a[16:19]\n\t"
"ds_read_b64_tr_b16 v[228:229], v241 offset:20992\n\t"
"v_mfma_f32_16x16x32_bf16 a[16:19], a[252:255], a[196:199], a[16:19]\n\t"
"ds_read_b64_tr_b16 v[230:231], v241 offset:22528\n\t"
"v_mfma_f32_16x16x32_bf16 a[24:27], a[244:247], a[192:195], a[24:27]\n\t"
"v_cvt_pk_bf16_f32 v224, v224, v225\n\t"
"v_cvt_pk_bf16_f32 v225, v226, v227\n\t"
"v_mfma_f32_16x16x32_bf16 a[24:27], a[248:251], a[196:199], a[24:27]\n\t"
"v_cvt_pk_bf16_f32 v226, v232, v233\n\t"
"v_cvt_pk_bf16_f32 v227, v234, v235\n\t"
"v_mfma_f32_16x16x32_bf16 a[24:27], a[252:255], a[200:203], a[24:27]\n\t"
"ds_write_b128 v242, v[224:227] offset:29184\n\t"
"v_mfma_f32_16x16x32_bf16 a[32:35], a[244:247], a[196:199], a[32:35]\n\t"
"buffer_load_dwordx4 a[244:247], v252, s[8:11], s20 offen offset:1024 sc1\n\t"
"v_mfma_f32_16x16x32_bf16 a[32:35], a[248:251], a[200:203], a[32:35]\n\t"
"s_waitcnt vmcnt(8)\n\t"
"ds_read_b64_tr_b16 v[232:233], v240 offset:40960\n\t"
"v_mfma_f32_16x16x32_bf16 a[32:35], a[252:255], a[204:207], a[32:35]\n\t"
"ds_read_b64_tr_b16 v[234:235], v240 offset:42496\n\t"
"v_mfma_f32_16x16x32_bf16 a[40:43], v[248:251], a[168:171], a[40:43]\n\t"
"buffer_load_dwordx4 a[248:251], v252, s[8:11], s20 offen offset:2048 sc1\n\t"
"v_mfma_f32_16x16x32_bf16 a[40:43], a[212:215], a[172:175], a[40:43]\n\t"
"s_mov_b32 s20, 761856\n\t"
"ds_read_b64_tr_b16 v[224:225], v241 offset:40960\n\t"
"v_mfma_f32_16x16x32_bf16 a[40:43], a[216:219], a[176:179], a[40:43]\n\t"
"ds_read_b64_tr_b16 v[226:227], v241 offset:42496\n\t"
"v_mfma_f32_16x16x32_bf16 a[48:51], v[248:251], a[172:175], a[48:51]\n\t"
"buffer_load_dwordx4 a[252:255], v252, s[8:11], s20 offen offset:0 sc1\n\t"
"v_mfma_f32_16x16x32_bf16 a[48:51], a[212:215], a[176:179], a[48:51]\n\t"
"s_waitcnt lgkmcnt(5)\n\t"
"v_mfma_f32_16x16x32_bf16 a[48:51], a[216:219], a[180:183], a[48:51]\n\t"
"v_mfma_f32_16x16x32_bf16 v[236:239], v[236:239], a[208:211], 0\n\t"
"v_mfma_f32_16x16x32_bf16 v[228:231], v[228:231], a[208:211], 0\n\t"
"v_xor_b32 v246, 0xc0, v246\n\t"
"v_mfma_f32_16x16x32_bf16 a[56:59], v[248:251], a[176:179], a[56:59]\n\t"
"v_mfma_f32_16x16x32_bf16 a[56:59], a[212:215], a[180:183], a[56:59]\n\t"
"v_mfma_f32_16x16x32_bf16 a[56:59], a[216:219], a[184:187], a[56:59]\n\t"
"v_mfma_f32_16x16x32_bf16 a[64:67], v[248:251], a[180:183], a[64:67]\n\t"
"s_waitcnt lgkmcnt(2)\n\t"
"v_cvt_pk_bf16_f32 v236, v236, v237\n\t"
"v_cvt_pk_bf16_f32 v237, v238, v239\n\t"
"v_mfma_f32_16x16x32_bf16 a[64:67], a[212:215], a[184:187], a[64:67]\n\t"
"s_waitcnt lgkmcnt(0)\n\t"
"v_cvt_pk_bf16_f32 v238, v228, v229\n\t"
"v_cvt_pk_bf16_f32 v239, v230, v231\n\t"
"v_mfma_f32_16x16x32_bf16 a[64:67], a[216:219], a[188:191], a[64:67]\n\t"
"ds_write_b128 v242, v[236:239] offset:30720\n\t"
"v_mfma_f32_16x16x32_bf16 v[232:235], v[232:235], a[208:211], 0\n\t"
"v_mfma_f32_16x16x32_bf16 v[224:227], v[224:227], a[208:211], 0\n\t"
"v_mfma_f32_16x16x32_bf16 a[72:75], v[248:251], a[184:187], a[72:75]\n\t"
"buffer_load_dwordx4 a[208:211], v252, s[8:11], s20 offen offset:1024 sc1\n\t"
"v_mfma_f32_16x16x32_bf16 a[72:75], a[212:215], a[188:191], a[72:75]\n\t"
"v_mfma_f32_16x16x32_bf16 a[72:75], a[216:219], a[192:195], a[72:75]\n\t"
"v_mfma_f32_16x16x32_bf16 a[80:83], v[248:251], a[188:191], a[80:83]\n\t"
"v_cvt_pk_bf16_f32 v232, v232, v233\n\t"
"v_cvt_pk_bf16_f32 v233, v234, v235\n\t"
"v_mfma_f32_16x16x32_bf16 a[80:83], a[212:215], a[192:195], a[80:83]\n\t"
"v_cvt_pk_bf16_f32 v234, v224, v225\n\t"
"v_cvt_pk_bf16_f32 v235, v226, v227\n\t"
"v_mfma_f32_16x16x32_bf16 a[80:83], a[216:219], a[196:199], a[80:83]\n\t"
"ds_write_b128 v242, v[232:235] offset:32256\n\t"
"v_mfma_f32_16x16x32_bf16 a[88:91], v[248:251], a[192:195], a[88:91]\n\t"
"v_mfma_f32_16x16x32_bf16 a[88:91], a[212:215], a[196:199], a[88:91]\n\t"
"ds_read_b64_tr_b16 v[228:229], v246 offset:1024\n\t"
"v_mfma_f32_16x16x32_bf16 a[88:91], a[216:219], a[200:203], a[88:91]\n\t"
"ds_read_b64_tr_b16 v[230:231], v246 offset:4096\n\t"
"v_mfma_f32_16x16x32_bf16 a[96:99], v[248:251], a[196:199], a[96:99]\n\t"
"buffer_load_dwordx4 v[248:251], v252, s[8:11], s20 offen offset:2048 sc1\n\t"
"v_mfma_f32_16x16x32_bf16 a[96:99], a[212:215], a[200:203], a[96:99]\n\t"
"s_waitcnt vmcnt(9)\n\t"
"ds_read_b64_tr_b16 v[236:237], v246 offset:7168\n\t"
"s_mov_b32 s20, 626688\n\t"
"v_mfma_f32_16x16x32_bf16 a[96:99], a[216:219], a[204:207], a[96:99]\n\t"
"ds_read_b64_tr_b16 v[238:239], v246 offset:10240\n\t"
"v_mfma_f32_16x16x32_bf16 a[104:107], a[220:223], a[168:171], a[104:107]\n\t"
"buffer_load_dwordx4 v[240:243], v252, s[8:11], s20 offen offset:0 sc1\n\t"
"v_mfma_f32_16x16x32_bf16 a[104:107], v[96:99], a[172:175], a[104:107]\n\t"
"ds_read_b64_tr_b16 v[224:225], v246 offset:13312\n\t"
"v_mfma_f32_16x16x32_bf16 a[104:107], a[224:227], a[176:179], a[104:107]\n\t"
"ds_read_b64_tr_b16 v[226:227], v246 offset:16384\n\t"
"v_mfma_f32_16x16x32_bf16 a[112:115], a[220:223], a[172:175], a[112:115]\n\t"
"buffer_load_dwordx4 a[212:215], v252, s[8:11], s20 offen offset:1024 sc1\n\t"
"v_mfma_f32_16x16x32_bf16 a[112:115], v[96:99], a[176:179], a[112:115]\n\t"
"ds_read_b128 a[168:171], v244 offset:9216\n\t"
"v_mfma_f32_16x16x32_bf16 a[112:115], a[224:227], a[180:183], a[112:115]\n\t"
"ds_read_b64_tr_b16 v[232:233], v246 offset:20992\n\t"
"v_mfma_f32_16x16x32_bf16 a[120:123], a[220:223], a[176:179], a[120:123]\n\t"
"buffer_load_dwordx4 a[216:219], v252, s[8:11], s20 offen offset:2048 sc1\n\t"
"v_mfma_f32_16x16x32_bf16 a[120:123], v[96:99], a[180:183], a[120:123]\n\t"
"s_waitcnt lgkmcnt(6)\n\t"
"ds_read_b64_tr_b16 v[234:235], v246 offset:24064\n\t"
"v_mfma_f32_16x16x32_bf16 a[120:123], a[224:227], a[184:187], a[120:123]\n\t"
"ds_read_b128 a[172:175], v244 offset:9728\n\t"
"v_mfma_f32_16x16x32_bf16 v[228:231], v[228:231], v[220:223], 0\n\t"
"ds_read_b128 a[176:179], v244 offset:10240\n\t"
"v_mfma_f32_16x16x32_bf16 a[128:131], a[220:223], a[180:183], a[128:131]\n\t"
"s_waitcnt lgkmcnt(7)\n\t"
"ds_read_b128 a[180:183], v244 offset:10752\n\t"
"v_mfma_f32_16x16x32_bf16 a[128:131], v[96:99], a[184:187], a[128:131]\n\t"
"v_mfma_f32_16x16x32_bf16 a[128:131], a[224:227], a[188:191], a[128:131]\n\t"
"v_mfma_f32_16x16x32_bf16 v[236:239], v[236:239], v[220:223], 0\n\t"
"v_mfma_f32_16x16x32_bf16 a[136:139], a[220:223], a[184:187], a[136:139]\n\t"
"s_waitcnt lgkmcnt(6)\n\t"
"ds_read_b128 a[184:187], v244 offset:11264\n\t"
"v_cvt_pk_bf16_f32 v228, v228, v229\n\t"
"v_mfma_f32_16x16x32_bf16 a[136:139], v[96:99], a[188:191], a[136:139]\n\t"
"v_cvt_pk_bf16_f32 v229, v230, v231\n\t"
"v_mfma_f32_16x16x32_bf16 a[136:139], a[224:227], a[192:195], a[136:139]\n\t"
"s_mov_b32 s20, 638976\n\t"
"v_mfma_f32_16x16x32_bf16 v[224:227], v[224:227], v[220:223], 0\n\t"
"v_mfma_f32_16x16x32_bf16 a[144:147], a[220:223], a[188:191], a[144:147]\n\t"
"s_waitcnt lgkmcnt(4)\n\t"
"ds_read_b128 a[188:191], v244 offset:11776\n\t"
"v_cvt_pk_bf16_f32 v230, v236, v237\n\t"
"v_mfma_f32_16x16x32_bf16 a[144:147], v[96:99], a[192:195], a[144:147]\n\t"
"v_cvt_pk_bf16_f32 v231, v238, v239\n\t"
"v_mfma_f32_16x16x32_bf16 a[144:147], a[224:227], a[196:199], a[144:147]\n\t"
"v_mfma_f32_16x16x32_bf16 v[232:235], v[232:235], v[220:223], 0\n\t"
"v_mfma_f32_16x16x32_bf16 a[152:155], a[220:223], a[192:195], a[152:155]\n\t"
"buffer_load_dwordx4 v[236:239], v252, s[8:11], s20 offen offset:0 sc1\n\t"
"v_mfma_f32_16x16x32_bf16 a[152:155], v[96:99], a[196:199], a[152:155]\n\t"
"ds_read_b128 a[192:195], v244 offset:12288\n\t"
"v_cvt_pk_bf16_f32 v224, v224, v225\n\t"
"v_mfma_f32_16x16x32_bf16 a[152:155], a[224:227], a[200:203], a[152:155]\n\t"
"v_cvt_pk_bf16_f32 v225, v226, v227\n\t"
"v_mfma_f32_16x16x32_bf16 a[160:163], a[220:223], a[196:199], a[160:163]\n\t"
"buffer_load_dwordx4 a[220:223], v252, s[8:11], s20 offen offset:1024 sc1\n\t"
"v_mfma_f32_16x16x32_bf16 a[160:163], v[96:99], a[200:203], a[160:163]\n\t"
"s_waitcnt vmcnt(11) lgkmcnt(4)\n\t"
"ds_read_b128 a[196:199], v244 offset:12800\n\t"
"v_cvt_pk_bf16_f32 v226, v232, v233\n\t"
"v_mfma_f32_16x16x32_bf16 a[160:163], a[224:227], a[204:207], a[160:163]\n\t"
"ds_read_b128 a[200:203], v244 offset:13312\n\t"
"v_cvt_pk_bf16_f32 v227, v234, v235\n\t"
"v_mfma_f32_16x16x32_bf16 v[200:203], a[228:231], a[168:171], v[200:203]\n\t"
"ds_read_b64_tr_b16 v[232:233], v246 offset:27136\n\t"
"v_mfma_f32_16x16x32_bf16 v[200:203], a[232:235], a[172:175], v[200:203]\n\t"
"s_waitcnt lgkmcnt(6)\n\t"
"ds_read_b64_tr_b16 v[234:235], v246 offset:30208\n\t"
"v_mfma_f32_16x16x32_bf16 v[200:203], a[236:239], a[176:179], v[200:203]\n\t"
"s_waitcnt lgkmcnt(0)\n\t"
"ds_read_b128 a[204:207], v244 offset:13824\n\t"
"v_mfma_f32_16x16x32_bf16 v[232:235], v[232:235], v[220:223], 0\n\t"
"buffer_load_dwordx4 v[96:99], v252, s[8:11], s20 offen offset:2048 sc1\n\t"
"s_mov_b32 s20, 651264\n\t"
"v_mfma_f32_16x16x32_bf16 v[208:211], a[228:231], a[172:175], v[208:211]\n\t"
"buffer_load_dwordx4 a[224:227], v252, s[8:11], s20 offen offset:0 sc1\n\t"
"v_mfma_f32_16x16x32_bf16 v[208:211], a[232:235], a[176:179], v[208:211]\n\t"
"s_add_u32 m0, s24, 256\n\t"
"s_add_u32 s28, s21, 0\n\t"
"v_mfma_f32_16x16x32_bf16 v[208:211], a[236:239], a[180:183], v[208:211]\n\t"
"s_waitcnt lgkmcnt(0)\n\t"
"s_barrier\n\t"
"v_mfma_f32_16x16x32_bf16 v[216:219], a[228:231], a[176:179], v[216:219]\n\t"
"v_mfma_f32_16x16x32_bf16 v[216:219], a[232:235], a[180:183], v[216:219]\n\t"
"buffer_load_dwordx4 v247, s[12:15], s28 offen offset:256 nt lds\n\t"
"v_mfma_f32_16x16x32_bf16 v[216:219], a[236:239], a[184:187], v[216:219]\n\t"
"s_add_u32 m0, s24, 6400\n\t"
"s_add_u32 s28, s21, 196608\n\t"
"ds_write2st64_b64 v245, v[228:229], v[230:231] offset0:0 offset1:1\n\t"
"v_mfma_f32_16x16x32_bf16 a[4:7], a[228:231], a[180:183], a[4:7]\n\t"
"buffer_load_dwordx4 v247, s[12:15], s28 offen offset:256 nt lds\n\t"
"v_mfma_f32_16x16x32_bf16 a[4:7], a[232:235], a[184:187], a[4:7]\n\t"
"s_add_u32 m0, s24, 12544\n\t"
"s_add_u32 s28, s21, 393216\n\t"
"ds_read_b64_tr_b16 v[228:229], v246 offset:33280\n\t"
"v_mfma_f32_16x16x32_bf16 a[4:7], a[236:239], a[188:191], a[4:7]\n\t"
"ds_write2st64_b64 v245, v[224:225], v[226:227] offset0:2 offset1:3\n\t"
"v_cvt_pk_bf16_f32 v232, v232, v233\n\t"
"v_cvt_pk_bf16_f32 v233, v234, v235\n\t"
"v_mfma_f32_16x16x32_bf16 a[12:15], a[228:231], a[184:187], a[12:15]\n\t"
"v_mfma_f32_16x16x32_bf16 a[12:15], a[232:235], a[188:191], a[12:15]\n\t"
"ds_read_b64_tr_b16 v[230:231], v246 offset:36352\n\t"
"v_mfma_f32_16x16x32_bf16 a[12:15], a[236:239], a[192:195], a[12:15]\n\t"
"buffer_load_dwordx4 v247, s[12:15], s28 offen offset:256 nt lds\n\t"
"s_add_u32 m0, s24, 20224\n\t"
"s_add_u32 s28, s21, 589824\n\t"
"v_mfma_f32_16x16x32_bf16 a[20:23], a[228:231], a[188:191], a[20:23]\n\t"
"buffer_load_dwordx4 v247, s[12:15], s28 offen offset:256 nt lds\n\t"
"v_mfma_f32_16x16x32_bf16 a[20:23], a[232:235], a[192:195], a[20:23]\n\t"
"s_add_u32 m0, s24, 26368\n\t"
"s_add_u32 s28, s21, 786432\n\t"
"ds_read_b64_tr_b16 v[224:225], v246 offset:40960\n\t"
"v_mfma_f32_16x16x32_bf16 a[20:23], a[236:239], a[196:199], a[20:23]\n\t"
"s_waitcnt lgkmcnt(1)\n\t"
"ds_write_b64 v245, v[232:233] offset:2048\n\t"
"ds_read_b64_tr_b16 v[226:227], v246 offset:44032\n\t"
"v_mfma_f32_16x16x32_bf16 v[228:231], v[228:231], v[220:223], 0\n\t"
"buffer_load_dwordx4 v247, s[12:15], s28 offen offset:256 nt lds\n\t"
"s_add_u32 m0, s24, 60160\n\t"
"s_add_u32 s28, s21, 1769472\n\t"
"v_mfma_f32_16x16x32_bf16 a[28:31], a[228:231], a[192:195], a[28:31]\n\t"
"buffer_load_dwordx4 v247, s[12:15], s28 offen offset:256 nt lds\n\t"
"v_mfma_f32_16x16x32_bf16 a[28:31], a[232:235], a[196:199], a[28:31]\n\t"
"ds_read_b64_tr_b16 v[232:233], v246 offset:47104\n\t"
"v_mfma_f32_16x16x32_bf16 a[28:31], a[236:239], a[200:203], a[28:31]\n\t"
"s_waitcnt lgkmcnt(1)\n\t"
"ds_read_b64_tr_b16 v[234:235], v246 offset:50176\n\t"
"v_mfma_f32_16x16x32_bf16 v[224:227], v[224:227], v[220:223], 0\n\t"
"v_mfma_f32_16x16x32_bf16 a[36:39], a[228:231], a[196:199], a[36:39]\n\t"
"v_cvt_pk_bf16_f32 v228, v228, v229\n\t"
"v_cvt_pk_bf16_f32 v229, v230, v231\n\t"
"v_mfma_f32_16x16x32_bf16 a[36:39], a[232:235], a[200:203], a[36:39]\n\t"
"s_waitcnt vmcnt(16)\n\t"
"v_mfma_f32_16x16x32_bf16 a[36:39], a[236:239], a[204:207], a[36:39]\n\t"
"s_waitcnt lgkmcnt(0)\n\t"
"v_mfma_f32_16x16x32_bf16 v[232:235], v[232:235], v[220:223], 0\n\t"
"v_mfma_f32_16x16x32_bf16 a[44:47], a[240:243], a[168:171], a[44:47]\n\t"
"v_cvt_pk_bf16_f32 v230, v224, v225\n\t"
"v_cvt_pk_bf16_f32 v231, v226, v227\n\t"
"v_mfma_f32_16x16x32_bf16 a[44:47], a[244:247], a[172:175], a[44:47]\n\t"
"ds_read_b64_tr_b16 v[224:225], v246 offset:53248\n\t"
"v_mfma_f32_16x16x32_bf16 a[44:47], a[248:251], a[176:179], a[44:47]\n\t"
"ds_write2st64_b64 v245, v[228:229], v[230:231] offset0:5 offset1:6\n\t"
"v_mfma_f32_16x16x32_bf16 a[52:55], a[240:243], a[172:175], a[52:55]\n\t"
"v_mfma_f32_16x16x32_bf16 a[52:55], a[244:247], a[176:179], a[52:55]\n\t"
"ds_read_b64_tr_b16 v[226:227], v246 offset:56320\n\t"
"v_mfma_f32_16x16x32_bf16 a[52:55], a[248:251], a[180:183], a[52:55]\n\t"
"v_cvt_pk_bf16_f32 v232, v232, v233\n\t"
"v_cvt_pk_bf16_f32 v233, v234, v235\n\t"
"v_mfma_f32_16x16x32_bf16 a[60:63], a[240:243], a[176:179], a[60:63]\n\t"
"v_mfma_f32_16x16x32_bf16 a[60:63], a[244:247], a[180:183], a[60:63]\n\t"
"v_mfma_f32_16x16x32_bf16 a[60:63], a[248:251], a[184:187], a[60:63]\n\t"
"v_mfma_f32_16x16x32_bf16 a[68:71], a[240:243], a[180:183], a[68:71]\n\t"
"v_mfma_f32_16x16x32_bf16 a[68:71], a[244:247], a[184:187], a[68:71]\n\t"
"v_mfma_f32_16x16x32_bf16 a[68:71], a[248:251], a[188:191], a[68:71]\n\t"
"v_mfma_f32_16x16x32_bf16 a[76:79], a[240:243], a[184:187], a[76:79]\n\t"
"s_waitcnt lgkmcnt(0)\n\t"
"v_mfma_f32_16x16x32_bf16 a[76:79], a[244:247], a[188:191], a[76:79]\n\t"
"v_mfma_f32_16x16x32_bf16 a[76:79], a[248:251], a[192:195], a[76:79]\n\t"
"v_mfma_f32_16x16x32_bf16 v[224:227], v[224:227], v[220:223], 0\n\t"
"v_mfma_f32_16x16x32_bf16 a[84:87], a[240:243], a[188:191], a[84:87]\n\t"
"v_mfma_f32_16x16x32_bf16 a[84:87], a[244:247], a[192:195], a[84:87]\n\t"
"buffer_load_dwordx4 a[228:231], v252, s[8:11], s20 offen offset:1024 sc1\n\t"
"v_mfma_f32_16x16x32_bf16 a[84:87], a[248:251], a[196:199], a[84:87]\n\t"
"v_mfma_f32_16x16x32_bf16 a[92:95], a[240:243], a[192:195], a[92:95]\n\t"
"v_mfma_f32_16x16x32_bf16 a[92:95], a[244:247], a[196:199], a[92:95]\n\t"
"v_cvt_pk_bf16_f32 v234, v224, v225\n\t"
"v_cvt_pk_bf16_f32 v235, v226, v227\n\t"
"v_mfma_f32_16x16x32_bf16 a[92:95], a[248:251], a[200:203], a[92:95]\n\t"
"ds_write2st64_b64 v245, v[232:233], v[234:235] offset0:7 offset1:8\n\t"
"v_mfma_f32_16x16x32_bf16 a[100:103], a[240:243], a[196:199], a[100:103]\n\t"
"s_waitcnt vmcnt(14)\n\t"
"v_mfma_f32_16x16x32_bf16 a[100:103], a[244:247], a[200:203], a[100:103]\n\t"
"v_mfma_f32_16x16x32_bf16 a[100:103], a[248:251], a[204:207], a[100:103]\n\t"
"v_mfma_f32_16x16x32_bf16 a[108:111], a[252:255], a[168:171], a[108:111]\n\t"
"v_mfma_f32_16x16x32_bf16 a[108:111], a[208:211], a[172:175], a[108:111]\n\t"
"v_mfma_f32_16x16x32_bf16 a[108:111], v[248:251], a[176:179], a[108:111]\n\t"
"s_waitcnt lgkmcnt(0)\n\t"
"s_barrier\n\t"
"v_mfma_f32_16x16x32_bf16 a[116:119], a[252:255], a[172:175], a[116:119]\n\t"
"ds_read_b128 a[168:171], v244 offset:0\n\t"
"v_mfma_f32_16x16x32_bf16 a[116:119], a[208:211], a[176:179], a[116:119]\n\t"
"ds_read_b128 a[172:175], v244 offset:512\n\t"
"v_mfma_f32_16x16x32_bf16 a[116:119], v[248:251], a[180:183], a[116:119]\n\t"
"v_mfma_f32_16x16x32_bf16 a[124:127], a[252:255], a[176:179], a[124:127]\n\t"
"ds_read_b128 a[176:179], v244 offset:1024\n\t"
"v_mfma_f32_16x16x32_bf16 a[124:127], a[208:211], a[180:183], a[124:127]\n\t"
"v_mfma_f32_16x16x32_bf16 a[124:127], v[248:251], a[184:187], a[124:127]\n\t"
"v_mfma_f32_16x16x32_bf16 a[132:135], a[252:255], a[180:183], a[132:135]\n\t"
"ds_read_b128 a[180:183], v244 offset:1536\n\t"
"v_mfma_f32_16x16x32_bf16 a[132:135], a[208:211], a[184:187], a[132:135]\n\t"
"v_mfma_f32_16x16x32_bf16 a[132:135], v[248:251], a[188:191], a[132:135]\n\t"
"v_mfma_f32_16x16x32_bf16 a[140:143], a[252:255], a[184:187], a[140:143]\n\t"
"ds_read_b128 a[184:187], v244 offset:2048\n\t"
"v_mfma_f32_16x16x32_bf16 a[140:143], a[208:211], a[188:191], a[140:143]\n\t"
"v_mfma_f32_16x16x32_bf16 a[140:143], v[248:251], a[192:195], a[140:143]\n\t"
"v_mfma_f32_16x16x32_bf16 a[148:151], a[252:255], a[188:191], a[148:151]\n\t"
"ds_read_b128 a[188:191], v244 offset:2560\n\t"
"v_mfma_f32_16x16x32_bf16 a[148:151], a[208:211], a[192:195], a[148:151]\n\t"
"v_mfma_f32_16x16x32_bf16 a[148:151], v[248:251], a[196:199], a[148:151]\n\t"
"v_mfma_f32_16x16x32_bf16 a[156:159], a[252:255], a[192:195], a[156:159]\n\t"
"ds_read_b128 a[192:195], v244 offset:3072\n\t"
"v_mfma_f32_16x16x32_bf16 a[156:159], a[208:211], a[196:199], a[156:159]\n\t"
"v_mfma_f32_16x16x32_bf16 a[156:159], v[248:251], a[200:203], a[156:159]\n\t"
"v_mfma_f32_16x16x32_bf16 a[164:167], a[252:255], a[196:199], a[164:167]\n\t"
"s_waitcnt vmcnt(11) lgkmcnt(4)\n\t"
"ds_read_b128 a[196:199], v244 offset:3584\n\t"
"v_mfma_f32_16x16x32_bf16 a[164:167], a[208:211], a[200:203], a[164:167]\n\t"
"ds_read_b128 a[200:203], v244 offset:4096\n\t"
"v_mfma_f32_16x16x32_bf16 a[164:167], v[248:251], a[204:207], a[164:167]\n\t"
"ds_read_b128 a[204:207], v244 offset:4608\n\t"
"Lst7_%=:\n\t"
"v_mfma_f32_16x16x32_bf16 v[0:3], v[240:243], a[168:171], v[0:3]\n\t"
"buffer_load_dwordx4 a[208:211], v252, s[8:11], s20 offen offset:2048 sc1\n\t"
"v_mfma_f32_16x16x32_bf16 v[0:3], a[212:215], a[172:175], v[0:3]\n\t"
"s_waitcnt lgkmcnt(6)\n\t"
"s_mov_b32 s20, 663552\n\t"
"v_mfma_f32_16x16x32_bf16 v[0:3], a[216:219], a[176:179], v[0:3]\n\t"
"v_mfma_f32_16x16x32_bf16 v[4:7], v[240:243], a[172:175], v[4:7]\n\t"
"s_waitcnt lgkmcnt(5)\n\t"
"v_mfma_f32_16x16x32_bf16 v[4:7], a[212:215], a[176:179], v[4:7]\n\t"
"buffer_load_dwordx4 v[248:251], v252, s[8:11], s20 offen offset:0 sc1\n\t"
"v_mfma_f32_16x16x32_bf16 v[4:7], a[216:219], a[180:183], v[4:7]\n\t"
"v_mfma_f32_16x16x32_bf16 v[8:11], v[240:243], a[176:179], v[8:11]\n\t"
"s_waitcnt lgkmcnt(4)\n\t"
"v_mfma_f32_16x16x32_bf16 v[8:11], a[212:215], a[180:183], v[8:11]\n\t"
"v_mfma_f32_16x16x32_bf16 v[8:11], a[216:219], a[184:187], v[8:11]\n\t"
"buffer_load_dwordx4 a[232:235], v252, s[8:11], s20 offen offset:1024 sc1\n\t"
"v_mfma_f32_16x16x32_bf16 v[12:15], v[240:243], a[180:183], v[12:15]\n\t"
"s_waitcnt lgkmcnt(3)\n\t"
"v_mfma_f32_16x16x32_bf16 v[12:15], a[212:215], a[184:187], v[12:15]\n\t"
"v_mfma_f32_16x16x32_bf16 v[12:15], a[216:219], a[188:191], v[12:15]\n\t"
"v_mfma_f32_16x16x32_bf16 v[16:19], v[240:243], a[184:187], v[16:19]\n\t"
"buffer_load_dwordx4 a[236:239], v252, s[8:11], s20 offen offset:2048 sc1\n\t"
"v_mfma_f32_16x16x32_bf16 v[16:19], a[212:215], a[188:191], v[16:19]\n\t"
"s_waitcnt lgkmcnt(2)\n\t"
"s_mov_b32 s20, 675840\n\t"
"v_mfma_f32_16x16x32_bf16 v[16:19], a[216:219], a[192:195], v[16:19]\n\t"
"ds_read_b64_tr_b16 v[228:229], v246 offset:1536\n\t"
"v_mfma_f32_16x16x32_bf16 v[20:23], v[240:243], a[188:191], v[20:23]\n\t"
"s_waitcnt lgkmcnt(2)\n\t"
"ds_read_b64_tr_b16 v[230:231], v246 offset:4608\n\t"
"v_mfma_f32_16x16x32_bf16 v[20:23], a[212:215], a[192:195], v[20:23]\n\t"
"buffer_load_dwordx4 a[240:243], v252, s[8:11], s20 offen offset:0 sc1\n\t"
"v_mfma_f32_16x16x32_bf16 v[20:23], a[216:219], a[196:199], v[20:23]\n\t"
"v_mfma_f32_16x16x32_bf16 v[24:27], v[240:243], a[192:195], v[24:27]\n\t"
"s_waitcnt lgkmcnt(2)\n\t"
"v_mfma_f32_16x16x32_bf16 v[24:27], a[212:215], a[196:199], v[24:27]\n\t"
"v_mfma_f32_16x16x32_bf16 v[24:27], a[216:219], a[200:203], v[24:27]\n\t"
"buffer_load_dwordx4 a[244:247], v252, s[8:11], s20 offen offset:1024 sc1\n\t"
"v_mfma_f32_16x16x32_bf16 v[28:31], v[240:243], a[196:199], v[28:31]\n\t"
"s_waitcnt vmcnt(14)\n\t"
"ds_read_b64_tr_b16 v[224:225], v246 offset:7680\n\t"
"v_mfma_f32_16x16x32_bf16 v[28:31], a[212:215], a[200:203], v[28:31]\n\t"
"ds_read_b64_tr_b16 v[226:227], v246 offset:10752\n\t"
"v_mfma_f32_16x16x32_bf16 v[28:31], a[216:219], a[204:207], v[28:31]\n\t"
"v_mfma_f32_16x16x32_bf16 v[32:35], v[236:239], a[168:171], v[32:35]\n\t"
"buffer_load_dwordx4 v[240:243], v252, s[8:11], s20 offen offset:2048 sc1\n\t"
"v_mfma_f32_16x16x32_bf16 v[32:35], a[220:223], a[172:175], v[32:35]\n\t"
"s_mov_b32 s20, 688128\n\t"
"v_mfma_f32_16x16x32_bf16 v[32:35], v[96:99], a[176:179], v[32:35]\n\t"
"v_mfma_f32_16x16x32_bf16 v[36:39], v[236:239], a[172:175], v[36:39]\n\t"
"s_waitcnt lgkmcnt(2)\n\t"
"v_mfma_f32_16x16x32_bf16 v[36:39], a[220:223], a[176:179], v[36:39]\n\t"
"buffer_load_dwordx4 a[212:215], v252, s[8:11], s20 offen offset:0 sc1\n\t"
"v_mfma_f32_16x16x32_bf16 v[36:39], v[96:99], a[180:183], v[36:39]\n\t"
"v_mfma_f32_16x16x32_bf16 v[228:231], v[228:231], v[220:223], 0\n\t"
"v_mfma_f32_16x16x32_bf16 v[40:43], v[236:239], a[176:179], v[40:43]\n\t"
"ds_read_b64_tr_b16 v[232:233], v246 offset:13824\n\t"
"v_mfma_f32_16x16x32_bf16 v[40:43], a[220:223], a[180:183], v[40:43]\n\t"
"buffer_load_dwordx4 a[216:219], v252, s[8:11], s20 offen offset:1024 sc1\n\t"
"v_mfma_f32_16x16x32_bf16 v[40:43], v[96:99], a[184:187], v[40:43]\n\t"
"ds_read_b64_tr_b16 v[234:235], v246 offset:16896\n\t"
"v_mfma_f32_16x16x32_bf16 v[44:47], v[236:239], a[180:183], v[44:47]\n\t"
"s_waitcnt lgkmcnt(2)\n\t"
"v_mfma_f32_16x16x32_bf16 v[44:47], a[220:223], a[184:187], v[44:47]\n\t"
"buffer_load_dwordx4 a[248:251], v252, s[8:11], s20 offen offset:2048 sc1\n\t"
"v_mfma_f32_16x16x32_bf16 v[44:47], v[96:99], a[188:191], v[44:47]\n\t"
"s_mov_b32 s20, 921600\n\t"
"v_cvt_pk_bf16_f32 v228, v228, v229\n\t"
"v_cvt_pk_bf16_f32 v229, v230, v231\n\t"
"v_mfma_f32_16x16x32_bf16 v[224:227], v[224:227], v[220:223], 0\n\t"
"v_mfma_f32_16x16x32_bf16 v[48:51], v[236:239], a[184:187], v[48:51]\n\t"
"v_mfma_f32_16x16x32_bf16 v[48:51], a[220:223], a[188:191], v[48:51]\n\t"
"buffer_load_dwordx4 a[252:255], v252, s[8:11], s20 offen offset:0 sc1\n\t"
"v_mfma_f32_16x16x32_bf16 v[48:51], v[96:99], a[192:195], v[48:51]\n\t"
"v_mfma_f32_16x16x32_bf16 v[52:55], v[236:239], a[188:191], v[52:55]\n\t"
"v_mfma_f32_16x16x32_bf16 v[52:55], a[220:223], a[192:195], v[52:55]\n\t"
"v_cvt_pk_bf16_f32 v230, v224, v225\n\t"
"v_cvt_pk_bf16_f32 v231, v226, v227\n\t"
"v_mfma_f32_16x16x32_bf16 v[52:55], v[96:99], a[196:199], v[52:55]\n\t"
"ds_read_b64_tr_b16 v[224:225], v246 offset:21504\n\t"
"v_mfma_f32_16x16x32_bf16 v[56:59], v[236:239], a[192:195], v[56:59]\n\t"
"s_waitcnt lgkmcnt(1)\n\t"
"ds_read_b64_tr_b16 v[226:227], v246 offset:24576\n\t"
"v_mfma_f32_16x16x32_bf16 v[56:59], a[220:223], a[196:199], v[56:59]\n\t"
"v_mfma_f32_16x16x32_bf16 v[56:59], v[96:99], a[200:203], v[56:59]\n\t"
"v_mfma_f32_16x16x32_bf16 v[232:235], v[232:235], v[220:223], 0\n\t"
"v_mfma_f32_16x16x32_bf16 v[60:63], v[236:239], a[196:199], v[60:63]\n\t"
"buffer_load_dwordx4 v[236:239], v252, s[8:11], s20 offen offset:1024 sc1\n\t"
"v_mfma_f32_16x16x32_bf16 v[60:63], a[220:223], a[200:203], v[60:63]\n\t"
"s_waitcnt vmcnt(11)\n\t"
"v_mfma_f32_16x16x32_bf16 v[60:63], v[96:99], a[204:207], v[60:63]\n\t"
"v_mfma_f32_16x16x32_bf16 v[64:67], a[224:227], a[168:171], v[64:67]\n\t"
"buffer_load_dwordx4 v[96:99], v252, s[8:11], s20 offen offset:2048 sc1\n\t"
"v_mfma_f32_16x16x32_bf16 v[64:67], a[228:231], a[172:175], v[64:67]\n\t"
"s_mov_b32 s20, 933888\n\t"
"ds_read_b128 a[168:171], v244 offset:9216\n\t"
"v_cvt_pk_bf16_f32 v232, v232, v233\n\t"
"v_mfma_f32_16x16x32_bf16 v[64:67], a[208:211], a[176:179], v[64:67]\n\t"
"v_cvt_pk_bf16_f32 v233, v234, v235\n\t"
"v_mfma_f32_16x16x32_bf16 v[68:71], a[224:227], a[172:175], v[68:71]\n\t"
"buffer_load_dwordx4 a[220:223], v252, s[8:11], s20 offen offset:0 sc1\n\t"
"v_mfma_f32_16x16x32_bf16 v[68:71], a[228:231], a[176:179], v[68:71]\n\t"
"s_waitcnt lgkmcnt(1)\n\t"
"ds_read_b128 a[172:175], v244 offset:9728\n\t"
"v_mfma_f32_16x16x32_bf16 v[68:71], a[208:211], a[180:183], v[68:71]\n\t"
"v_mfma_f32_16x16x32_bf16 v[224:227], v[224:227], v[220:223], 0\n\t"
"v_mfma_f32_16x16x32_bf16 v[72:75], a[224:227], a[176:179], v[72:75]\n\t"
"ds_read_b128 a[176:179], v244 offset:10240\n\t"
"v_mfma_f32_16x16x32_bf16 v[72:75], a[228:231], a[180:183], v[72:75]\n\t"
"v_mfma_f32_16x16x32_bf16 v[72:75], a[208:211], a[184:187], v[72:75]\n\t"
"v_mfma_f32_16x16x32_bf16 v[76:79], a[224:227], a[180:183], v[76:79]\n\t"
"ds_read_b128 a[180:183], v244 offset:10752\n\t"
"v_mfma_f32_16x16x32_bf16 v[76:79], a[228:231], a[184:187], v[76:79]\n\t"
"v_cvt_pk_bf16_f32 v234, v224, v225\n\t"
"v_cvt_pk_bf16_f32 v235, v226, v227\n\t"
"v_mfma_f32_16x16x32_bf16 v[76:79], a[208:211], a[188:191], v[76:79]\n\t"
"ds_read_b64_tr_b16 v[224:225], v246 offset:27648\n\t"
"v_mfma_f32_16x16x32_bf16 v[80:83], a[224:227], a[184:187], v[80:83]\n\t"
"ds_read_b64_tr_b16 v[226:227], v246 offset:30720\n\t"
"v_mfma_f32_16x16x32_bf16 v[80:83], a[228:231], a[188:191], v[80:83]\n\t"
"ds_read_b128 a[184:187], v244 offset:11264\n\t"
"v_mfma_f32_16x16x32_bf16 v[80:83], a[208:211], a[192:195], v[80:83]\n\t"
"v_mfma_f32_16x16x32_bf16 v[84:87], a[224:227], a[188:191], v[84:87]\n\t"
"ds_read_b128 a[188:191], v244 offset:11776\n\t"
"v_mfma_f32_16x16x32_bf16 v[84:87], a[228:231], a[192:195], v[84:87]\n\t"
"v_mfma_f32_16x16x32_bf16 v[84:87], a[208:211], a[196:199], v[84:87]\n\t"
"v_mfma_f32_16x16x32_bf16 v[88:91], a[224:227], a[192:195], v[88:91]\n\t"
"ds_read_b128 a[192:195], v244 offset:12288\n\t"
"v_mfma_f32_16x16x32_bf16 v[88:91], a[228:231], a[196:199], v[88:91]\n\t"
"v_mfma_f32_16x16x32_bf16 v[88:91], a[208:211], a[200:203], v[88:91]\n\t"
"v_mfma_f32_16x16x32_bf16 v[92:95], a[224:227], a[196:199], v[92:95]\n\t"
"buffer_load_dwordx4 a[224:227], v252, s[8:11], s20 offen offset:1024 sc1\n\t"
"v_mfma_f32_16x16x32_bf16 v[92:95], a[228:231], a[200:203], v[92:95]\n\t"
"s_waitcnt vmcnt(11) lgkmcnt(3)\n\t"
"ds_read_b128 a[196:199], v244 offset:12800\n\t"
"v_mfma_f32_16x16x32_bf16 v[92:95], a[208:211], a[204:207], v[92:95]\n\t"
"ds_read_b128 a[200:203], v244 offset:13312\n\t"
"v_mfma_f32_16x16x32_bf16 v[224:227], v[224:227], v[220:223], 0\n\t"
"ds_read_b128 a[204:207], v244 offset:13824\n\t"
"v_mfma_f32_16x16x32_bf16 v[100:103], v[248:251], a[168:171], v[100:103]\n\t"
"buffer_load_dwordx4 a[208:211], v252, s[8:11], s20 offen offset:2048 sc1\n\t"
"v_mfma_f32_16x16x32_bf16 v[100:103], a[232:235], a[172:175], v[100:103]\n\t"
"s_mov_b32 s20, 946176\n\t"
"v_mfma_f32_16x16x32_bf16 v[100:103], a[236:239], a[176:179], v[100:103]\n\t"
"buffer_load_dwordx4 a[228:231], v252, s[8:11], s20 offen offset:0 sc1\n\t"
"v_mfma_f32_16x16x32_bf16 v[104:107], v[248:251], a[172:175], v[104:107]\n\t"
"s_waitcnt lgkmcnt(5)\n\t"
"v_mfma_f32_16x16x32_bf16 v[104:107], a[232:235], a[176:179], v[104:107]\n\t"
"v_cvt_pk_bf16_f32 v224, v224, v225\n\t"
"v_mfma_f32_16x16x32_bf16 v[104:107], a[236:239], a[180:183], v[104:107]\n\t"
"s_waitcnt lgkmcnt(0)\n\t"
"s_barrier\n\t"
"v_mfma_f32_16x16x32_bf16 v[108:111], v[248:251], a[176:179], v[108:111]\n\t"
"v_cvt_pk_bf16_f32 v225, v226, v227\n\t"
"v_mfma_f32_16x16x32_bf16 v[108:111], a[232:235], a[180:183], v[108:111]\n\t"
"v_mfma_f32_16x16x32_bf16 v[108:111], a[236:239], a[184:187], v[108:111]\n\t"
"ds_write2st64_b64 v245, v[228:229], v[230:231] offset0:0 offset1:1\n\t"
"v_mfma_f32_16x16x32_bf16 v[112:115], v[248:251], a[180:183], v[112:115]\n\t"
"v_mfma_f32_16x16x32_bf16 v[112:115], a[232:235], a[184:187], v[112:115]\n\t"
"ds_read_b64_tr_b16 v[228:229], v246 offset:33792\n\t"
"v_mfma_f32_16x16x32_bf16 v[112:115], a[236:239], a[188:191], v[112:115]\n\t"
"ds_write2st64_b64 v245, v[232:233], v[234:235] offset0:2 offset1:3\n\t"
"v_mfma_f32_16x16x32_bf16 v[116:119], v[248:251], a[184:187], v[116:119]\n\t"
"v_mfma_f32_16x16x32_bf16 v[116:119], a[232:235], a[188:191], v[116:119]\n\t"
"ds_read_b64_tr_b16 v[230:231], v246 offset:36864\n\t"
"v_mfma_f32_16x16x32_bf16 v[116:119], a[236:239], a[192:195], v[116:119]\n\t"
"s_add_u32 m0, s24, 66304\n\t"
"s_add_u32 s28, s21, 1966080\n\t"
"ds_write_b64 v245, v[224:225] offset:2048\n\t"
"v_mfma_f32_16x16x32_bf16 v[120:123], v[248:251], a[188:191], v[120:123]\n\t"
"ds_read_b64_tr_b16 v[232:233], v246 offset:41472\n\t"
"v_mfma_f32_16x16x32_bf16 v[120:123], a[232:235], a[192:195], v[120:123]\n\t"
"ds_read_b64_tr_b16 v[234:235], v246 offset:44544\n\t"
"v_mfma_f32_16x16x32_bf16 v[120:123], a[236:239], a[196:199], v[120:123]\n\t"
"s_waitcnt lgkmcnt(3)\n\t"
"ds_read_b64_tr_b16 v[224:225], v246 offset:47616\n\t"
"v_mfma_f32_16x16x32_bf16 v[228:231], v[228:231], v[220:223], 0\n\t"
"ds_read_b64_tr_b16 v[226:227], v246 offset:50688\n\t"
"v_mfma_f32_16x16x32_bf16 v[124:127], v[248:251], a[192:195], v[124:127]\n\t"
"buffer_load_dwordx4 v247, s[12:15], s28 offen offset:256 nt lds\n\t"
"v_mfma_f32_16x16x32_bf16 v[124:127], a[232:235], a[196:199], v[124:127]\n\t"
"s_add_u32 m0, s24, 72448\n\t"
"s_add_u32 s28, s21, 2162688\n\t"
"v_mfma_f32_16x16x32_bf16 v[124:127], a[236:239], a[200:203], v[124:127]\n\t"
"s_waitcnt lgkmcnt(2)\n\t"
"buffer_load_dwordx4 v247, s[12:15], s28 offen offset:256 nt lds\n\t"
"s_add_u32 m0, s24, 80128\n\t"
"s_add_u32 s28, s21, 2359296\n\t"
"v_mfma_f32_16x16x32_bf16 v[232:235], v[232:235], v[220:223], 0\n\t"
"buffer_load_dwordx4 v247, s[12:15], s28 offen offset:256 nt lds\n\t"
"v_mfma_f32_16x16x32_bf16 v[128:131], v[248:251], a[196:199], v[128:131]\n\t"
"s_waitcnt lgkmcnt(0)\n\t"
"s_add_u32 m0, s24, 86272\n\t"
"s_add_u32 s28, s21, 2555904\n\t"
"v_mfma_f32_16x16x32_bf16 v[128:131], a[232:235], a[200:203], v[128:131]\n\t"
"buffer_load_dwordx4 v247, s[12:15], s28 offen offset:256 nt lds\n\t"
"v_mfma_f32_16x16x32_bf16 v[128:131], a[236:239], a[204:207], v[128:131]\n\t"
"s_waitcnt vmcnt(14)\n\t"
"s_add_u32 m0, s24, 32512\n\t"
"s_add_u32 s28, s21, 983040\n\t"
"v_mfma_f32_16x16x32_bf16 v[224:227], v[224:227], v[220:223], 0\n\t"
"buffer_load_dwordx4 v247, s[12:15], s28 offen offset:256 nt lds\n\t"
"v_cvt_pk_bf16_f32 v228, v228, v229\n\t"
"v_cvt_pk_bf16_f32 v229, v230, v231\n\t"
"v_mfma_f32_16x16x32_bf16 v[132:135], a[240:243], a[168:171], v[132:135]\n\t"
"s_add_u32 m0, s24, 40192\n\t"
"s_add_u32 s28, s21, 1179648\n\t"
"v_cvt_pk_bf16_f32 v230, v232, v233\n\t"
"v_mfma_f32_16x16x32_bf16 v[132:135], a[244:247], a[172:175], v[132:135]\n\t"
"v_cvt_pk_bf16_f32 v231, v234, v235\n\t"
"v_mfma_f32_16x16x32_bf16 v[132:135], v[240:243], a[176:179], v[132:135]\n\t"
"buffer_load_dwordx4 v247, s[12:15], s28 offen offset:256 nt lds\n\t"
"v_mfma_f32_16x16x32_bf16 v[136:139], a[240:243], a[172:175], v[136:139]\n\t"
"ds_read_b64_tr_b16 v[232:233], v246 offset:53760\n\t"
"v_mfma_f32_16x16x32_bf16 v[136:139], a[244:247], a[176:179], v[136:139]\n\t"
"ds_read_b64_tr_b16 v[234:235], v246 offset:56832\n\t"
"v_cvt_pk_bf16_f32 v224, v224, v225\n\t"
"v_mfma_f32_16x16x32_bf16 v[136:139], v[240:243], a[180:183], v[136:139]\n\t"
"ds_write2st64_b64 v245, v[228:229], v[230:231] offset0:5 offset1:6\n\t"
"v_mfma_f32_16x16x32_bf16 v[140:143], a[240:243], a[176:179], v[140:143]\n\t"
"v_cvt_pk_bf16_f32 v225, v226, v227\n\t"
"v_mfma_f32_16x16x32_bf16 v[140:143], a[244:247], a[180:183], v[140:143]\n\t"
"Lpg6_%=:\n\t"
"s_cbranch_vccnz Lpg10_%=\n\t"
"v_mfma_f32_16x16x32_bf16 v[140:143], v[240:243], a[184:187], v[140:143]\n\t"
"v_mfma_f32_16x16x32_bf16 v[144:147], a[240:243], a[180:183], v[144:147]\n\t"
"v_mfma_f32_16x16x32_bf16 v[144:147], a[244:247], a[184:187], v[144:147]\n\t"
"v_mfma_f32_16x16x32_bf16 v[144:147], v[240:243], a[188:191], v[144:147]\n\t"
"v_mfma_f32_16x16x32_bf16 v[148:151], a[240:243], a[184:187], v[148:151]\n\t"
"s_waitcnt lgkmcnt(1)\n\t"
"v_mfma_f32_16x16x32_bf16 v[148:151], a[244:247], a[188:191], v[148:151]\n\t"
"v_mfma_f32_16x16x32_bf16 v[148:151], v[240:243], a[192:195], v[148:151]\n\t"
"v_mfma_f32_16x16x32_bf16 v[232:235], v[232:235], v[220:223], 0\n\t"
"v_mfma_f32_16x16x32_bf16 v[152:155], a[240:243], a[188:191], v[152:155]\n\t"
"v_mfma_f32_16x16x32_bf16 v[152:155], a[244:247], a[192:195], v[152:155]\n\t"
"v_mfma_f32_16x16x32_bf16 v[152:155], v[240:243], a[196:199], v[152:155]\n\t"
"v_mfma_f32_16x16x32_bf16 v[156:159], a[240:243], a[192:195], v[156:159]\n\t"
"v_mfma_f32_16x16x32_bf16 v[156:159], a[244:247], a[196:199], v[156:159]\n\t"
"v_cvt_pk_bf16_f32 v226, v232, v233\n\t"
"v_cvt_pk_bf16_f32 v227, v234, v235\n\t"
"v_mfma_f32_16x16x32_bf16 v[156:159], v[240:243], a[200:203], v[156:159]\n\t"
"ds_write2st64_b64 v245, v[224:225], v[226:227] offset0:7 offset1:8\n\t"
"v_mfma_f32_16x16x32_bf16 v[160:163], a[240:243], a[196:199], v[160:163]\n\t"
"s_waitcnt vmcnt(13)\n\t"
"v_mfma_f32_16x16x32_bf16 v[160:163], a[244:247], a[200:203], v[160:163]\n\t"
"v_mfma_f32_16x16x32_bf16 v[160:163], v[240:243], a[204:207], v[160:163]\n\t"
"v_mfma_f32_16x16x32_bf16 v[164:167], a[212:215], a[168:171], v[164:167]\n\t"
"v_mfma_f32_16x16x32_bf16 v[164:167], a[216:219], a[172:175], v[164:167]\n\t"
"v_mfma_f32_16x16x32_bf16 v[164:167], a[248:251], a[176:179], v[164:167]\n\t"
"s_waitcnt lgkmcnt(0)\n\t"
"s_barrier\n\t"
"v_mfma_f32_16x16x32_bf16 v[168:171], a[212:215], a[172:175], v[168:171]\n\t"
"ds_read_b128 a[168:171], v244 offset:0\n\t"
"v_mfma_f32_16x16x32_bf16 v[168:171], a[216:219], a[176:179], v[168:171]\n\t"
"buffer_load_dwordx4 v[240:243], v252, s[8:11], s20 offen offset:1024 sc1\n\t"
"v_mfma_f32_16x16x32_bf16 v[168:171], a[248:251], a[180:183], v[168:171]\n\t"
"ds_read_b128 a[172:175], v244 offset:512\n\t"
"v_mfma_f32_16x16x32_bf16 v[172:175], a[212:215], a[176:179], v[172:175]\n\t"
"ds_read_b128 a[176:179], v244 offset:1024\n\t"
"v_mfma_f32_16x16x32_bf16 v[172:175], a[216:219], a[180:183], v[172:175]\n\t"
"v_mfma_f32_16x16x32_bf16 v[172:175], a[248:251], a[184:187], v[172:175]\n\t"
"v_mfma_f32_16x16x32_bf16 v[176:179], a[212:215], a[180:183], v[176:179]\n\t"
"ds_read_b128 a[180:183], v244 offset:1536\n\t"
"v_mfma_f32_16x16x32_bf16 v[176:179], a[216:219], a[184:187], v[176:179]\n\t"
"v_mfma_f32_16x16x32_bf16 v[176:179], a[248:251], a[188:191], v[176:179]\n\t"
"v_mfma_f32_16x16x32_bf16 v[180:183], a[212:215], a[184:187], v[180:183]\n\t"
"ds_read_b128 a[184:187], v244 offset:2048\n\t"
"v_mfma_f32_16x16x32_bf16 v[180:183], a[216:219], a[188:191], v[180:183]\n\t"
"v_mfma_f32_16x16x32_bf16 v[180:183], a[248:251], a[192:195], v[180:183]\n\t"
"v_mfma_f32_16x16x32_bf16 v[184:187], a[212:215], a[188:191], v[184:187]\n\t"
"ds_read_b128 a[188:191], v244 offset:2560\n\t"
"v_mfma_f32_16x16x32_bf16 v[184:187], a[216:219], a[192:195], v[184:187]\n\t"
"v_mfma_f32_16x16x32_bf16 v[184:187], a[248:251], a[196:199], v[184:187]\n\t"
"v_mfma_f32_16x16x32_bf16 v[188:191], a[212:215], a[192:195], v[188:191]\n\t"
"ds_read_b128 a[192:195], v244 offset:3072\n\t"
"v_mfma_f32_16x16x32_bf16 v[188:191], a[216:219], a[196:199], v[188:191]\n\t"
"v_mfma_f32_16x16x32_bf16 v[188:191], a[248:251], a[200:203], v[188:191]\n\t"
"v_mfma_f32_16x16x32_bf16 v[192:195], a[212:215], a[196:199], v[192:195]\n\t"
"s_waitcnt vmcnt(11) lgkmcnt(4)\n\t"
"ds_read_b128 a[196:199], v244 offset:3584\n\t"
"v_mfma_f32_16x16x32_bf16 v[192:195], a[216:219], a[200:203], v[192:195]\n\t"
"ds_read_b128 a[200:203], v244 offset:4096\n\t"
"v_mfma_f32_16x16x32_bf16 v[192:195], a[248:251], a[204:207], v[192:195]\n\t"
"ds_read_b128 a[204:207], v244 offset:4608\n\t"
"Lst8_%=:\n\t"
"v_mfma_f32_16x16x32_bf16 v[0:3], a[252:255], a[168:171], v[0:3]\n\t"
"s_mov_b64 vcc, -1\n\t"
"s_bitcmp1_b32 s73, 1\n\t"
"s_cbranch_scc1 Lvh1_%=\n\t"
"s_bitcmp1_b32 s73, 0\n\t"
"s_cbranch_scc1 Lpg14_%=\n\t"
"s_branch Lpg13_%=\n\t"
"Lvh1_%=:\n\t"
"s_bitcmp1_b32 s73, 0\n\t"
"s_cbranch_scc1 Lpg16_%=\n\t"
"s_branch Lpg15_%=\n\t"
"Lvr1_%=:\n\t"
"s_mov_b64 vcc, 0\n\t"
"s_waitcnt lgkmcnt(6)\n\t"
"v_xor_b32 v246, 0xc0, v246\n\t"
"v_mfma_f32_16x16x32_bf16 v[0:3], v[236:239], a[172:175], v[0:3]\n\t"
"v_mfma_f32_16x16x32_bf16 v[0:3], v[96:99], a[176:179], v[0:3]\n\t"
"v_mfma_f32_16x16x32_bf16 v[4:7], a[252:255], a[172:175], v[4:7]\n\t"
"s_waitcnt lgkmcnt(5)\n\t"
"v_mfma_f32_16x16x32_bf16 v[4:7], v[236:239], a[176:179], v[4:7]\n\t"
"v_mfma_f32_16x16x32_bf16 v[4:7], v[96:99], a[180:183], v[4:7]\n\t"
"v_mfma_f32_16x16x32_bf16 v[8:11], a[252:255], a[176:179], v[8:11]\n\t"
"s_waitcnt lgkmcnt(4)\n\t"
"v_mfma_f32_16x16x32_bf16 v[8:11], v[236:239], a[180:183], v[8:11]\n\t"
"v_mfma_f32_16x16x32_bf16 v[8:11], v[96:99], a[184:187], v[8:11]\n\t"
"v_mfma_f32_16x16x32_bf16 v[12:15], a[252:255], a[180:183], v[12:15]\n\t"
"s_waitcnt lgkmcnt(3)\n\t"
"v_mfma_f32_16x16x32_bf16 v[12:15], v[236:239], a[184:187], v[12:15]\n\t"
"v_mfma_f32_16x16x32_bf16 v[12:15], v[96:99], a[188:191], v[12:15]\n\t"
"buffer_load_dwordx4 v[248:251], v252, s[8:11], s20 offen offset:2048 sc1\n\t"
"v_mfma_f32_16x16x32_bf16 v[16:19], a[252:255], a[184:187], v[16:19]\n\t"
"s_waitcnt lgkmcnt(2)\n\t"
"s_mov_b32 s20, 958464\n\t"
"v_mfma_f32_16x16x32_bf16 v[16:19], v[236:239], a[188:191], v[16:19]\n\t"
"v_mfma_f32_16x16x32_bf16 v[16:19], v[96:99], a[192:195], v[16:19]\n\t"
"ds_read_b64_tr_b16 v[228:229], v246 offset:1536\n\t"
"v_mfma_f32_16x16x32_bf16 v[20:23], a[252:255], a[188:191], v[20:23]\n\t"
"buffer_load_dwordx4 a[212:215], v252, s[8:11], s20 offen offset:0 sc1\n\t"
"v_mfma_f32_16x16x32_bf16 v[20:23], v[236:239], a[192:195], v[20:23]\n\t"
"s_waitcnt lgkmcnt(2)\n\t"
"ds_read_b64_tr_b16 v[230:231], v246 offset:4608\n\t"
"v_mfma_f32_16x16x32_bf16 v[20:23], v[96:99], a[196:199], v[20:23]\n\t"
"v_mfma_f32_16x16x32_bf16 v[24:27], a[252:255], a[192:195], v[24:27]\n\t"
"buffer_load_dwordx4 a[216:219], v252, s[8:11], s20 offen offset:1024 sc1\n\t"
"v_mfma_f32_16x16x32_bf16 v[24:27], v[236:239], a[196:199], v[24:27]\n\t"
"s_waitcnt lgkmcnt(2)\n\t"
"v_mfma_f32_16x16x32_bf16 v[24:27], v[96:99], a[200:203], v[24:27]\n\t"
"v_mfma_f32_16x16x32_bf16 v[28:31], a[252:255], a[196:199], v[28:31]\n\t"
"s_waitcnt vmcnt(11)\n\t"
"ds_read_b64_tr_b16 v[232:233], v246 offset:7680\n\t"
"v_mfma_f32_16x16x32_bf16 v[28:31], v[236:239], a[200:203], v[28:31]\n\t"
"buffer_load_dwordx4 v[236:239], v252, s[8:11], s20 offen offset:2048 sc1\n\t"
"v_mfma_f32_16x16x32_bf16 v[28:31], v[96:99], a[204:207], v[28:31]\n\t"
"s_mov_b32 s20, 970752\n\t"
"ds_read_b64_tr_b16 v[234:235], v246 offset:10752\n\t"
"v_mfma_f32_16x16x32_bf16 v[32:35], a[220:223], a[168:171], v[32:35]\n\t"
"v_mfma_f32_16x16x32_bf16 v[32:35], a[224:227], a[172:175], v[32:35]\n\t"
"v_mfma_f32_16x16x32_bf16 v[32:35], a[208:211], a[176:179], v[32:35]\n\t"
"buffer_load_dwordx4 v[96:99], v252, s[8:11], s20 offen offset:0 sc1\n\t"
"v_mfma_f32_16x16x32_bf16 v[36:39], a[220:223], a[172:175], v[36:39]\n\t"
"s_waitcnt lgkmcnt(2)\n\t"
"v_mfma_f32_16x16x32_bf16 v[36:39], a[224:227], a[176:179], v[36:39]\n\t"
"v_mfma_f32_16x16x32_bf16 v[36:39], a[208:211], a[180:183], v[36:39]\n\t"
"v_mfma_f32_16x16x32_bf16 v[228:231], v[228:231], v[220:223], 0\n\t"
"buffer_load_dwordx4 a[232:235], v252, s[8:11], s20 offen offset:1024 sc1\n\t"
"v_mfma_f32_16x16x32_bf16 v[40:43], a[220:223], a[176:179], v[40:43]\n\t"
"ds_read_b64_tr_b16 v[224:225], v246 offset:13824\n\t"
"v_mfma_f32_16x16x32_bf16 v[40:43], a[224:227], a[180:183], v[40:43]\n\t"
"ds_read_b64_tr_b16 v[226:227], v246 offset:16896\n\t"
"v_mfma_f32_16x16x32_bf16 v[40:43], a[208:211], a[184:187], v[40:43]\n\t"
"v_mfma_f32_16x16x32_bf16 v[44:47], a[220:223], a[180:183], v[44:47]\n\t"
"buffer_load_dwordx4 a[236:239], v252, s[8:11], s20 offen offset:2048 sc1\n\t"
"v_mfma_f32_16x16x32_bf16 v[44:47], a[224:227], a[184:187], v[44:47]\n\t"
"s_waitcnt lgkmcnt(2)\n\t"
"v_cvt_pk_bf16_f32 v228, v228, v229\n\t"
"v_cvt_pk_bf16_f32 v229, v230, v231\n\t"
"v_mfma_f32_16x16x32_bf16 v[44:47], a[208:211], a[188:191], v[44:47]\n\t"
"s_mov_b32 s20, 983040\n\t"
"v_mfma_f32_16x16x32_bf16 v[232:235], v[232:235], v[220:223], 0\n\t"
"v_mfma_f32_16x16x32_bf16 v[48:51], a[220:223], a[184:187], v[48:51]\n\t"
"buffer_load_dwordx4 a[240:243], v252, s[8:11], s20 offen offset:0 sc1\n\t"
"v_mfma_f32_16x16x32_bf16 v[48:51], a[224:227], a[188:191], v[48:51]\n\t"
"v_mfma_f32_16x16x32_bf16 v[48:51], a[208:211], a[192:195], v[48:51]\n\t"
"v_mfma_f32_16x16x32_bf16 v[52:55], a[220:223], a[188:191], v[52:55]\n\t"
"v_mfma_f32_16x16x32_bf16 v[52:55], a[224:227], a[192:195], v[52:55]\n\t"
"buffer_load_dwordx4 a[244:247], v252, s[8:11], s20 offen offset:1024 sc1\n\t"
"v_mfma_f32_16x16x32_bf16 v[52:55], a[208:211], a[196:199], v[52:55]\n\t"
"v_cvt_pk_bf16_f32 v230, v232, v233\n\t"
"v_cvt_pk_bf16_f32 v231, v234, v235\n\t"
"v_mfma_f32_16x16x32_bf16 v[56:59], a[220:223], a[192:195], v[56:59]\n\t"
"s_waitcnt lgkmcnt(0)\n\t"
"ds_read_b64_tr_b16 v[232:233], v246 offset:21504\n\t"
"v_mfma_f32_16x16x32_bf16 v[56:59], a[224:227], a[196:199], v[56:59]\n\t"
"ds_read_b64_tr_b16 v[234:235], v246 offset:24576\n\t"
"v_mfma_f32_16x16x32_bf16 v[56:59], a[208:211], a[200:203], v[56:59]\n\t"
"buffer_load_dwordx4 a[248:251], v252, s[8:11], s20 offen offset:2048 sc1\n\t"
"v_mfma_f32_16x16x32_bf16 v[224:227], v[224:227], v[220:223], 0\n\t"
"s_mov_b32 s20, 995328\n\t"
"v_mfma_f32_16x16x32_bf16 v[60:63], a[220:223], a[196:199], v[60:63]\n\t"
"s_waitcnt vmcnt(9)\n\t"
"v_mfma_f32_16x16x32_bf16 v[60:63], a[224:227], a[200:203], v[60:63]\n\t"
"v_mfma_f32_16x16x32_bf16 v[60:63], a[208:211], a[204:207], v[60:63]\n\t"
"buffer_load_dwordx4 a[220:223], v252, s[8:11], s20 offen offset:0 sc1\n\t"
"v_mfma_f32_16x16x32_bf16 v[64:67], a[228:231], a[168:171], v[64:67]\n\t"
"ds_read_b128 a[168:171], v244 offset:9216\n\t"
"v_mfma_f32_16x16x32_bf16 v[64:67], v[240:243], a[172:175], v[64:67]\n\t"
"v_cvt_pk_bf16_f32 v224, v224, v225\n\t"
"v_cvt_pk_bf16_f32 v225, v226, v227\n\t"
"v_mfma_f32_16x16x32_bf16 v[64:67], v[248:251], a[176:179], v[64:67]\n\t"
"v_mfma_f32_16x16x32_bf16 v[68:71], a[228:231], a[172:175], v[68:71]\n\t"
"buffer_load_dwordx4 a[224:227], v252, s[8:11], s20 offen offset:1024 sc1\n\t"
"v_mfma_f32_16x16x32_bf16 v[68:71], v[240:243], a[176:179], v[68:71]\n\t"
"s_waitcnt lgkmcnt(1)\n\t"
"ds_read_b128 a[172:175], v244 offset:9728\n\t"
"v_mfma_f32_16x16x32_bf16 v[68:71], v[248:251], a[180:183], v[68:71]\n\t"
"buffer_load_dwordx4 a[252:255], v252, s[8:11], s20 offen offset:2048 sc1\n\t"
"v_mfma_f32_16x16x32_bf16 v[232:235], v[232:235], v[220:223], 0\n\t"
"v_mfma_f32_16x16x32_bf16 v[72:75], a[228:231], a[176:179], v[72:75]\n\t"
"ds_read_b128 a[176:179], v244 offset:10240\n\t"
"v_mfma_f32_16x16x32_bf16 v[72:75], v[240:243], a[180:183], v[72:75]\n\t"
"v_mfma_f32_16x16x32_bf16 v[72:75], v[248:251], a[184:187], v[72:75]\n\t"
"v_mfma_f32_16x16x32_bf16 v[76:79], a[228:231], a[180:183], v[76:79]\n\t"
"ds_read_b128 a[180:183], v244 offset:10752\n\t"
"v_mfma_f32_16x16x32_bf16 v[76:79], v[240:243], a[184:187], v[76:79]\n\t"
"v_cvt_pk_bf16_f32 v226, v232, v233\n\t"
"v_cvt_pk_bf16_f32 v227, v234, v235\n\t"
"v_mfma_f32_16x16x32_bf16 v[76:79], v[248:251], a[188:191], v[76:79]\n\t"
"ds_read_b64_tr_b16 v[232:233], v246 offset:27648\n\t"
"v_mfma_f32_16x16x32_bf16 v[80:83], a[228:231], a[184:187], v[80:83]\n\t"
"ds_read_b64_tr_b16 v[234:235], v246 offset:30720\n\t"
"v_mfma_f32_16x16x32_bf16 v[80:83], v[240:243], a[188:191], v[80:83]\n\t"
"ds_read_b128 a[184:187], v244 offset:11264\n\t"
"v_mfma_f32_16x16x32_bf16 v[80:83], v[248:251], a[192:195], v[80:83]\n\t"
"v_mfma_f32_16x16x32_bf16 v[84:87], a[228:231], a[188:191], v[84:87]\n\t"
"s_mov_b32 s20, 1007616\n\t"
"ds_read_b128 a[188:191], v244 offset:11776\n\t"
"v_mfma_f32_16x16x32_bf16 v[84:87], v[240:243], a[192:195], v[84:87]\n\t"
"v_mfma_f32_16x16x32_bf16 v[84:87], v[248:251], a[196:199], v[84:87]\n\t"
"v_mfma_f32_16x16x32_bf16 v[88:91], a[228:231], a[192:195], v[88:91]\n\t"
"ds_read_b128 a[192:195], v244 offset:12288\n\t"
"v_mfma_f32_16x16x32_bf16 v[88:91], v[240:243], a[196:199], v[88:91]\n\t"
"v_mfma_f32_16x16x32_bf16 v[88:91], v[248:251], a[200:203], v[88:91]\n\t"
"v_mfma_f32_16x16x32_bf16 v[92:95], a[228:231], a[196:199], v[92:95]\n\t"
"buffer_load_dwordx4 a[228:231], v252, s[8:11], s20 offen offset:0 sc1\n\t"
"v_mfma_f32_16x16x32_bf16 v[92:95], v[240:243], a[200:203], v[92:95]\n\t"
"s_waitcnt vmcnt(10) lgkmcnt(3)\n\t"
"ds_read_b128 a[196:199], v244 offset:12800\n\t"
"v_mfma_f32_16x16x32_bf16 v[92:95], v[248:251], a[204:207], v[92:95]\n\t"
"ds_read_b128 a[200:203], v244 offset:13312\n\t"
"v_mfma_f32_16x16x32_bf16 v[232:235], v[232:235], v[220:223], 0\n\t"
"ds_read_b128 a[204:207], v244 offset:13824\n\t"
"v_mfma_f32_16x16x32_bf16 v[100:103], a[212:215], a[168:171], v[100:103]\n\t"
"buffer_load_dwordx4 v[248:251], v252, s[8:11], s20 offen offset:1024 sc1\n\t"
"v_mfma_f32_16x16x32_bf16 v[100:103], a[216:219], a[172:175], v[100:103]\n\t"
"v_mfma_f32_16x16x32_bf16 v[100:103], v[236:239], a[176:179], v[100:103]\n\t"
"v_mfma_f32_16x16x32_bf16 v[104:107], a[212:215], a[172:175], v[104:107]\n\t"
"s_waitcnt lgkmcnt(5)\n\t"
"v_mfma_f32_16x16x32_bf16 v[104:107], a[216:219], a[176:179], v[104:107]\n\t"
"v_cvt_pk_bf16_f32 v232, v232, v233\n\t"
"v_mfma_f32_16x16x32_bf16 v[104:107], v[236:239], a[180:183], v[104:107]\n\t"
"s_waitcnt lgkmcnt(0)\n\t"
"s_barrier\n\t"
"v_mfma_f32_16x16x32_bf16 v[108:111], a[212:215], a[176:179], v[108:111]\n\t"
"v_cvt_pk_bf16_f32 v233, v234, v235\n\t"
"v_mfma_f32_16x16x32_bf16 v[108:111], a[216:219], a[180:183], v[108:111]\n\t"
"v_mfma_f32_16x16x32_bf16 v[108:111], v[236:239], a[184:187], v[108:111]\n\t"
"ds_write2st64_b64 v245, v[228:229], v[230:231] offset0:0 offset1:1\n\t"
"v_mfma_f32_16x16x32_bf16 v[112:115], a[212:215], a[180:183], v[112:115]\n\t"
"v_mfma_f32_16x16x32_bf16 v[112:115], a[216:219], a[184:187], v[112:115]\n\t"
"ds_read_b64_tr_b16 v[228:229], v246 offset:33792\n\t"
"v_mfma_f32_16x16x32_bf16 v[112:115], v[236:239], a[188:191], v[112:115]\n\t"
"ds_write2st64_b64 v245, v[224:225], v[226:227] offset0:2 offset1:3\n\t"
"v_mfma_f32_16x16x32_bf16 v[116:119], a[212:215], a[184:187], v[116:119]\n\t"
"global_load_dwordx4 v[240:243], v252, s[48:49] offset:2048\n\t"
"v_mfma_f32_16x16x32_bf16 v[116:119], a[216:219], a[188:191], v[116:119]\n\t"
"ds_read_b64_tr_b16 v[230:231], v246 offset:36864\n\t"
"v_mfma_f32_16x16x32_bf16 v[116:119], v[236:239], a[192:195], v[116:119]\n\t"
"ds_write_b64 v245, v[232:233] offset:2048\n\t"
"v_mfma_f32_16x16x32_bf16 v[120:123], a[212:215], a[188:191], v[120:123]\n\t"
"ds_read_b64_tr_b16 v[224:225], v246 offset:41472\n\t"
"v_mfma_f32_16x16x32_bf16 v[120:123], a[216:219], a[192:195], v[120:123]\n\t"
"ds_read_b64_tr_b16 v[226:227], v246 offset:44544\n\t"
"v_mfma_f32_16x16x32_bf16 v[120:123], v[236:239], a[196:199], v[120:123]\n\t"
"s_waitcnt lgkmcnt(3)\n\t"
"ds_read_b64_tr_b16 v[232:233], v246 offset:47616\n\t"
"v_mfma_f32_16x16x32_bf16 v[228:231], v[228:231], v[220:223], 0\n\t"
"ds_read_b64_tr_b16 v[234:235], v246 offset:50688\n\t"
"v_mfma_f32_16x16x32_bf16 v[124:127], a[212:215], a[192:195], v[124:127]\n\t"
"global_load_dwordx4 a[208:211], v252, s[46:47] offset:1024\n\t"
"v_mfma_f32_16x16x32_bf16 v[124:127], a[216:219], a[196:199], v[124:127]\n\t"
"v_mfma_f32_16x16x32_bf16 v[124:127], v[236:239], a[200:203], v[124:127]\n\t"
"s_waitcnt lgkmcnt(2)\n\t"
"v_mfma_f32_16x16x32_bf16 v[224:227], v[224:227], v[220:223], 0\n\t"
"v_mfma_f32_16x16x32_bf16 v[128:131], a[212:215], a[196:199], v[128:131]\n\t"
"buffer_load_dwordx4 a[212:215], v252, s[8:11], s20 offen offset:2048 sc1\n\t"
"v_mfma_f32_16x16x32_bf16 v[128:131], a[216:219], a[200:203], v[128:131]\n\t"
"s_waitcnt vmcnt(11) lgkmcnt(0)\n\t"
"s_mov_b32 s20, 1019904\n\t"
"v_cvt_pk_bf16_f32 v228, v228, v229\n\t"
"v_mfma_f32_16x16x32_bf16 v[128:131], v[236:239], a[204:207], v[128:131]\n\t"
"v_cvt_pk_bf16_f32 v229, v230, v231\n\t"
"v_mfma_f32_16x16x32_bf16 v[232:235], v[232:235], v[220:223], 0\n\t"
"buffer_load_dwordx4 a[216:219], v252, s[8:11], s20 offen offset:0 sc1\n\t"
"v_mfma_f32_16x16x32_bf16 v[132:135], v[96:99], a[168:171], v[132:135]\n\t"
"s_add_u32 m0, s24, 46336\n\t"
"s_add_u32 s28, s21, 1376256\n\t"
"v_cvt_pk_bf16_f32 v230, v224, v225\n\t"
"v_mfma_f32_16x16x32_bf16 v[132:135], a[232:235], a[172:175], v[132:135]\n\t"
"v_cvt_pk_bf16_f32 v231, v226, v227\n\t"
"v_mfma_f32_16x16x32_bf16 v[132:135], a[236:239], a[176:179], v[132:135]\n\t"
"ds_write2st64_b64 v245, v[228:229], v[230:231] offset0:5 offset1:6\n\t"
"v_mfma_f32_16x16x32_bf16 v[136:139], v[96:99], a[172:175], v[136:139]\n\t"
"buffer_load_dwordx4 v247, s[12:15], s28 offen offset:256 nt lds\n\t"
"v_mfma_f32_16x16x32_bf16 v[136:139], a[232:235], a[176:179], v[136:139]\n\t"
"s_add_u32 m0, s24, 52480\n\t"
"s_add_u32 s28, s21, 1572864\n\t"
"ds_read_b64_tr_b16 v[224:225], v246 offset:53760\n\t"
"v_mfma_f32_16x16x32_bf16 v[136:139], a[236:239], a[180:183], v[136:139]\n\t"
"buffer_load_dwordx4 v247, s[12:15], s28 offen offset:256 nt lds\n\t"
"v_mfma_f32_16x16x32_bf16 v[140:143], v[96:99], a[176:179], v[140:143]\n\t"
"s_add_u32 m0, s24, 92416\n\t"
"s_add_u32 s28, s21, 2752512\n\t"
"ds_read_b64_tr_b16 v[226:227], v246 offset:56832\n\t"
"v_mfma_f32_16x16x32_bf16 v[140:143], a[232:235], a[180:183], v[140:143]\n\t"
"buffer_load_dwordx4 v247, s[12:15], s28 offen offset:256 nt lds\n\t"
"v_mfma_f32_16x16x32_bf16 v[140:143], a[236:239], a[184:187], v[140:143]\n\t"
"s_add_u32 m0, s24, 100096\n\t"
"s_add_u32 s28, s21, 2949120\n\t"
"v_cvt_pk_bf16_f32 v232, v232, v233\n\t"
"v_cvt_pk_bf16_f32 v233, v234, v235\n\t"
"v_mfma_f32_16x16x32_bf16 v[144:147], v[96:99], a[180:183], v[144:147]\n\t"
"v_mfma_f32_16x16x32_bf16 v[144:147], a[232:235], a[184:187], v[144:147]\n\t"
"buffer_load_dwordx4 v247, s[12:15], s28 offen offset:256 nt lds\n\t"
"v_mfma_f32_16x16x32_bf16 v[144:147], a[236:239], a[188:191], v[144:147]\n\t"
"s_add_u32 m0, s24, 106240\n\t"
"s_add_u32 s28, s21, 3145728\n\t"
"v_mfma_f32_16x16x32_bf16 v[148:151], v[96:99], a[184:187], v[148:151]\n\t"
"buffer_load_dwordx4 v247, s[12:15], s28 offen offset:256 nt lds\n\t"
"v_mfma_f32_16x16x32_bf16 v[148:151], a[232:235], a[188:191], v[148:151]\n\t"
"s_waitcnt lgkmcnt(0)\n\t"
"s_add_u32 m0, s24, 112384\n\t"
"s_add_u32 s28, s21, 3342336\n\t"
"v_mfma_f32_16x16x32_bf16 v[148:151], a[236:239], a[192:195], v[148:151]\n\t"
"buffer_load_dwordx4 v247, s[12:15], s28 offen offset:256 nt lds\n\t"
"s_add_u32 m0, s25, 256\n\t"
"v_mfma_f32_16x16x32_bf16 v[224:227], v[224:227], v[220:223], 0\n\t"
"buffer_load_dwordx4 v254, s[12:15], 0 offen offset:256 nt lds\n\t"
"v_mfma_f32_16x16x32_bf16 v[152:155], v[96:99], a[188:191], v[152:155]\n\t"
"s_add_u32 m0, s26, 256\n\t"
"v_mfma_f32_16x16x32_bf16 v[152:155], a[232:235], a[192:195], v[152:155]\n\t"
"v_mfma_f32_16x16x32_bf16 v[152:155], a[236:239], a[196:199], v[152:155]\n\t"
"s_mov_b64 exec, s[36:37]\n\t"
"buffer_load_dwordx4 v254, s[12:15], s27 offen offset:256 nt lds\n\t"
"s_mov_b64 exec, -1\n\t"
"v_mfma_f32_16x16x32_bf16 v[156:159], v[96:99], a[192:195], v[156:159]\n\t"
"s_waitcnt vmcnt(11)\n\t"
"ds_read_b64_tr_b16 v[236:237], v240 offset:1536\n\t"
"v_mfma_f32_16x16x32_bf16 v[156:159], a[232:235], a[196:199], v[156:159]\n\t"
"ds_read_b64_tr_b16 v[238:239], v240 offset:3072\n\t"
"v_cvt_pk_bf16_f32 v234, v224, v225\n\t"
"v_mfma_f32_16x16x32_bf16 v[156:159], a[236:239], a[200:203], v[156:159]\n\t"
"ds_read_b64_tr_b16 v[228:229], v241 offset:1536\n\t"
"v_cvt_pk_bf16_f32 v235, v226, v227\n\t"
"v_mfma_f32_16x16x32_bf16 v[160:163], v[96:99], a[196:199], v[160:163]\n\t"
"ds_read_b64_tr_b16 v[230:231], v241 offset:3072\n\t"
"v_mfma_f32_16x16x32_bf16 v[160:163], a[232:235], a[200:203], v[160:163]\n\t"
"v_mfma_f32_16x16x32_bf16 v[160:163], a[236:239], a[204:207], v[160:163]\n\t"
"ds_write2st64_b64 v245, v[232:233], v[234:235] offset0:7 offset1:8\n\t"
"v_mfma_f32_16x16x32_bf16 v[164:167], a[240:243], a[168:171], v[164:167]\n\t"
"v_mfma_f32_16x16x32_bf16 v[164:167], a[244:247], a[172:175], v[164:167]\n\t"
"v_mfma_f32_16x16x32_bf16 v[164:167], a[248:251], a[176:179], v[164:167]\n\t"
"s_waitcnt lgkmcnt(0)\n\t"
"s_barrier\n\t"
"v_mfma_f32_16x16x32_bf16 v[168:171], a[240:243], a[172:175], v[168:171]\n\t"
"ds_read_b64_tr_b16 v[224:225], v240 offset:21504\n\t"
"v_mfma_f32_16x16x32_bf16 v[168:171], a[244:247], a[176:179], v[168:171]\n\t"
"ds_read_b64_tr_b16 v[226:227], v240 offset:23040\n\t"
"v_mfma_f32_16x16x32_bf16 v[168:171], a[248:251], a[180:183], v[168:171]\n\t"
"ds_read_b64_tr_b16 v[232:233], v241 offset:21504\n\t"
"v_mfma_f32_16x16x32_bf16 v[172:175], a[240:243], a[176:179], v[172:175]\n\t"
"s_waitcnt vmcnt(10)\n\t"
"ds_read_b64_tr_b16 v[234:235], v241 offset:23040\n\t"
"v_mfma_f32_16x16x32_bf16 v[172:175], a[244:247], a[180:183], v[172:175]\n\t"
"ds_read_b128 a[168:171], v244 offset:0\n\t"
"v_mfma_f32_16x16x32_bf16 v[172:175], a[248:251], a[184:187], v[172:175]\n\t"
"ds_read_b128 a[172:175], v244 offset:512\n\t"
"v_mfma_f32_16x16x32_bf16 v[236:239], v[236:239], a[208:211], 0\n\t"
"ds_read_b128 a[176:179], v244 offset:1024\n\t"
"v_mfma_f32_16x16x32_bf16 v[228:231], v[228:231], a[208:211], 0\n\t"
"v_mfma_f32_16x16x32_bf16 v[176:179], a[240:243], a[180:183], v[176:179]\n\t"
"ds_read_b128 a[180:183], v244 offset:1536\n\t"
"v_mfma_f32_16x16x32_bf16 v[176:179], a[244:247], a[184:187], v[176:179]\n\t"
"v_mfma_f32_16x16x32_bf16 v[176:179], a[248:251], a[188:191], v[176:179]\n\t"
"v_mfma_f32_16x16x32_bf16 v[180:183], a[240:243], a[184:187], v[180:183]\n\t"
"s_waitcnt lgkmcnt(6)\n\t"
"ds_read_b128 a[184:187], v244 offset:2048\n\t"
"v_cvt_pk_bf16_f32 v236, v236, v237\n\t"
"v_mfma_f32_16x16x32_bf16 v[180:183], a[244:247], a[188:191], v[180:183]\n\t"
"s_waitcnt lgkmcnt(5)\n\t"
"v_cvt_pk_bf16_f32 v237, v238, v239\n\t"
"v_cvt_pk_bf16_f32 v238, v228, v229\n\t"
"v_mfma_f32_16x16x32_bf16 v[180:183], a[248:251], a[192:195], v[180:183]\n\t"
"v_cvt_pk_bf16_f32 v239, v230, v231\n\t"
"v_mfma_f32_16x16x32_bf16 v[224:227], v[224:227], a[208:211], 0\n\t"
"v_mfma_f32_16x16x32_bf16 v[232:235], v[232:235], a[208:211], 0\n\t"
"ds_read_b64_tr_b16 v[228:229], v240 offset:41472\n\t"
"v_mfma_f32_16x16x32_bf16 v[184:187], a[240:243], a[188:191], v[184:187]\n\t"
"ds_read_b128 a[188:191], v244 offset:2560\n\t"
"v_mfma_f32_16x16x32_bf16 v[184:187], a[244:247], a[192:195], v[184:187]\n\t"
"ds_read_b64_tr_b16 v[230:231], v240 offset:43008\n\t"
"v_mfma_f32_16x16x32_bf16 v[184:187], a[248:251], a[196:199], v[184:187]\n\t"
"v_mfma_f32_16x16x32_bf16 v[188:191], a[240:243], a[192:195], v[188:191]\n\t"
"ds_read_b128 a[192:195], v244 offset:3072\n\t"
"v_cvt_pk_bf16_f32 v224, v224, v225\n\t"
"v_mfma_f32_16x16x32_bf16 v[188:191], a[244:247], a[196:199], v[188:191]\n\t"
"v_cvt_pk_bf16_f32 v225, v226, v227\n\t"
"v_cvt_pk_bf16_f32 v226, v232, v233\n\t"
"v_mfma_f32_16x16x32_bf16 v[188:191], a[248:251], a[200:203], v[188:191]\n\t"
"v_cvt_pk_bf16_f32 v227, v234, v235\n\t"
"v_mfma_f32_16x16x32_bf16 v[192:195], a[240:243], a[196:199], v[192:195]\n\t"
"s_waitcnt lgkmcnt(6)\n\t"
"ds_read_b64_tr_b16 v[232:233], v241 offset:41472\n\t"
"v_mfma_f32_16x16x32_bf16 v[192:195], a[244:247], a[200:203], v[192:195]\n\t"
"ds_read_b64_tr_b16 v[234:235], v241 offset:43008\n\t"
"v_mfma_f32_16x16x32_bf16 v[192:195], a[248:251], a[204:207], v[192:195]\n\t"
"ds_read_b128 a[196:199], v244 offset:3584\n\t"
"Lst9_%=:\n\t"
"v_mfma_f32_16x16x32_bf16 v[196:199], a[220:223], a[168:171], v[196:199]\n\t"
"s_mov_b64 vcc, -1\n\t"
"s_bitcmp1_b32 s73, 1\n\t"
"s_cbranch_scc1 Lvh2_%=\n\t"
"s_bitcmp1_b32 s73, 0\n\t"
"s_cbranch_scc1 Lpg18_%=\n\t"
"s_branch Lpg17_%=\n\t"
"Lvh2_%=:\n\t"
"s_bitcmp1_b32 s73, 0\n\t"
"s_cbranch_scc1 Lpg20_%=\n\t"
"s_branch Lpg19_%=\n\t"
"Lvr2_%=:\n\t"
"s_mov_b64 vcc, 0\n\t"
"s_waitcnt lgkmcnt(8)\n\t"
"ds_read_b128 a[200:203], v244 offset:4096\n\t"
"v_mfma_f32_16x16x32_bf16 v[196:199], a[224:227], a[172:175], v[196:199]\n\t"
"ds_read_b128 a[204:207], v244 offset:4608\n\t"
"v_mfma_f32_16x16x32_bf16 v[196:199], a[252:255], a[176:179], v[196:199]\n\t"
"buffer_load_dwordx4 v[96:99], v252, s[8:11], s20 offen offset:1024 sc1\n\t"
"v_mfma_f32_16x16x32_bf16 v[204:207], a[220:223], a[172:175], v[204:207]\n\t"
"s_waitcnt lgkmcnt(6)\n\t"
"v_mfma_f32_16x16x32_bf16 v[204:207], a[224:227], a[176:179], v[204:207]\n\t"
"s_waitcnt lgkmcnt(3)\n\t"
"v_mfma_f32_16x16x32_bf16 v[204:207], a[252:255], a[180:183], v[204:207]\n\t"
"v_mfma_f32_16x16x32_bf16 v[228:231], v[228:231], a[208:211], 0\n\t"
"v_add_u32 v240, 59904, v240\n\t"
"v_add_u32 v241, 59904, v241\n\t"
"v_mfma_f32_16x16x32_bf16 v[232:235], v[232:235], a[208:211], 0\n\t"
"v_mfma_f32_16x16x32_bf16 v[212:215], a[220:223], a[176:179], v[212:215]\n\t"
"v_mfma_f32_16x16x32_bf16 v[212:215], a[224:227], a[180:183], v[212:215]\n\t"
"v_mfma_f32_16x16x32_bf16 v[212:215], a[252:255], a[184:187], v[212:215]\n\t"
"s_barrier\n\t"
"v_mfma_f32_16x16x32_bf16 a[0:3], a[220:223], a[180:183], a[0:3]\n\t"
"v_cvt_pk_bf16_f32 v228, v228, v229\n\t"
"v_mfma_f32_16x16x32_bf16 a[0:3], a[224:227], a[184:187], a[0:3]\n\t"
"v_cvt_pk_bf16_f32 v229, v230, v231\n\t"
"v_cvt_pk_bf16_f32 v230, v232, v233\n\t"
"v_mfma_f32_16x16x32_bf16 a[0:3], a[252:255], a[188:191], a[0:3]\n\t"
"ds_write_b128 v243, v[236:239] offset:0\n\t"
"v_mfma_f32_16x16x32_bf16 a[8:11], a[220:223], a[184:187], a[8:11]\n\t"
"s_waitcnt lgkmcnt(3)\n\t"
"v_cvt_pk_bf16_f32 v231, v234, v235\n\t"
"v_mfma_f32_16x16x32_bf16 a[8:11], a[224:227], a[188:191], a[8:11]\n\t"
"ds_read_b64_tr_b16 v[236:237], v240 offset:1536\n\t"
"v_mfma_f32_16x16x32_bf16 a[8:11], a[252:255], a[192:195], a[8:11]\n\t"
"ds_write_b128 v243, v[224:227] offset:9216\n\t"
"v_mfma_f32_16x16x32_bf16 a[16:19], a[220:223], a[188:191], a[16:19]\n\t"
"buffer_load_dwordx4 a[232:235], v252, s[8:11], s20 offen offset:2048 sc1\n\t"
"v_mfma_f32_16x16x32_bf16 a[16:19], a[224:227], a[192:195], a[16:19]\n\t"
"s_waitcnt lgkmcnt(4)\n\t"
"ds_read_b64_tr_b16 v[238:239], v240 offset:3072\n\t"
"v_mfma_f32_16x16x32_bf16 a[16:19], a[252:255], a[196:199], a[16:19]\n\t"
"s_mov_b32 s20, 1032192\n\t"
"ds_write_b128 v243, v[228:231] offset:18432\n\t"
"v_mfma_f32_16x16x32_bf16 a[24:27], a[220:223], a[192:195], a[24:27]\n\t"
"buffer_load_dwordx4 a[236:239], v252, s[8:11], s20 offen offset:0 sc1\n\t"
"v_mfma_f32_16x16x32_bf16 a[24:27], a[224:227], a[196:199], a[24:27]\n\t"
"s_waitcnt lgkmcnt(5)\n\t"
"ds_read_b64_tr_b16 v[224:225], v241 offset:1536\n\t"
"v_mfma_f32_16x16x32_bf16 a[24:27], a[252:255], a[200:203], a[24:27]\n\t"
"ds_read_b64_tr_b16 v[226:227], v241 offset:3072\n\t"
"v_mfma_f32_16x16x32_bf16 a[32:35], a[220:223], a[196:199], a[32:35]\n\t"
"s_waitcnt lgkmcnt(3)\n\t"
"ds_read_b64_tr_b16 v[232:233], v240 offset:21504\n\t"
"v_mfma_f32_16x16x32_bf16 a[32:35], a[224:227], a[200:203], a[32:35]\n\t"
"ds_read_b64_tr_b16 v[234:235], v240 offset:23040\n\t"
"v_mfma_f32_16x16x32_bf16 a[32:35], a[252:255], a[204:207], a[32:35]\n\t"
"s_waitcnt vmcnt(12)\n\t"
"ds_read_b64_tr_b16 v[228:229], v241 offset:21504\n\t"
"v_mfma_f32_16x16x32_bf16 v[236:239], v[236:239], a[208:211], 0\n\t"
"s_waitcnt lgkmcnt(3)\n\t"
"ds_read_b64_tr_b16 v[230:231], v241 offset:23040\n\t"
"v_mfma_f32_16x16x32_bf16 v[224:227], v[224:227], a[208:211], 0\n\t"
"buffer_load_dwordx4 a[220:223], v252, s[8:11], s20 offen offset:1024 sc1\n\t"
"v_mfma_f32_16x16x32_bf16 a[40:43], a[228:231], a[168:171], a[40:43]\n\t"
"buffer_load_dwordx4 a[224:227], v252, s[8:11], s20 offen offset:2048 sc1\n\t"
"v_mfma_f32_16x16x32_bf16 a[40:43], v[248:251], a[172:175], a[40:43]\n\t"
"s_mov_b32 s20, 1044480\n\t"
"v_mfma_f32_16x16x32_bf16 a[40:43], a[212:215], a[176:179], a[40:43]\n\t"
"v_mfma_f32_16x16x32_bf16 a[48:51], a[228:231], a[172:175], a[48:51]\n\t"
"s_waitcnt lgkmcnt(2)\n\t"
"v_cvt_pk_bf16_f32 v236, v236, v237\n\t"
"v_cvt_pk_bf16_f32 v237, v238, v239\n\t"
"v_mfma_f32_16x16x32_bf16 a[48:51], v[248:251], a[176:179], a[48:51]\n\t"
"s_waitcnt lgkmcnt(0)\n\t"
"v_cvt_pk_bf16_f32 v238, v224, v225\n\t"
"v_cvt_pk_bf16_f32 v239, v226, v227\n\t"
"v_mfma_f32_16x16x32_bf16 a[48:51], a[212:215], a[180:183], a[48:51]\n\t"
"ds_write_b128 v243, v[236:239] offset:27648\n\t"
"v_mfma_f32_16x16x32_bf16 v[232:235], v[232:235], a[208:211], 0\n\t"
"buffer_load_dwordx4 a[240:243], v252, s[8:11], s20 offen offset:0 sc1\n\t"
"v_mfma_f32_16x16x32_bf16 v[228:231], v[228:231], a[208:211], 0\n\t"
"ds_read_b64_tr_b16 v[224:225], v240 offset:41472\n\t"
"v_mfma_f32_16x16x32_bf16 a[56:59], a[228:231], a[176:179], a[56:59]\n\t"
"buffer_load_dwordx4 a[244:247], v252, s[8:11], s20 offen offset:1024 sc1\n\t"
"v_mfma_f32_16x16x32_bf16 a[56:59], v[248:251], a[180:183], a[56:59]\n\t"
"ds_read_b64_tr_b16 v[226:227], v240 offset:43008\n\t"
"v_mfma_f32_16x16x32_bf16 a[56:59], a[212:215], a[184:187], a[56:59]\n\t"
"ds_read_b64_tr_b16 v[236:237], v241 offset:41472\n\t"
"v_mfma_f32_16x16x32_bf16 a[64:67], a[228:231], a[180:183], a[64:67]\n\t"
"ds_read_b64_tr_b16 v[238:239], v241 offset:43008\n\t"
"v_cvt_pk_bf16_f32 v232, v232, v233\n\t"
"v_mfma_f32_16x16x32_bf16 a[64:67], v[248:251], a[184:187], a[64:67]\n\t"
"v_cvt_pk_bf16_f32 v233, v234, v235\n\t"
"v_cvt_pk_bf16_f32 v234, v228, v229\n\t"
"v_mfma_f32_16x16x32_bf16 a[64:67], a[212:215], a[188:191], a[64:67]\n\t"
"v_cvt_pk_bf16_f32 v235, v230, v231\n\t"
"v_mfma_f32_16x16x32_bf16 a[72:75], a[228:231], a[184:187], a[72:75]\n\t"
"buffer_load_dwordx4 a[248:251], v252, s[8:11], s20 offen offset:2048 sc1\n\t"
"v_mfma_f32_16x16x32_bf16 a[72:75], v[248:251], a[188:191], a[72:75]\n\t"
"s_mov_b32 s20, 1056768\n\t"
"v_mfma_f32_16x16x32_bf16 a[72:75], a[212:215], a[192:195], a[72:75]\n\t"
"ds_write_b128 v243, v[232:235] offset:36864\n\t"
"v_mfma_f32_16x16x32_bf16 a[80:83], a[228:231], a[188:191], a[80:83]\n\t"
"buffer_load_dwordx4 a[252:255], v252, s[8:11], s20 offen offset:0 sc1\n\t"
"v_mfma_f32_16x16x32_bf16 a[80:83], v[248:251], a[192:195], a[80:83]\n\t"
"v_mfma_f32_16x16x32_bf16 a[80:83], a[212:215], a[196:199], a[80:83]\n\t"
"v_mfma_f32_16x16x32_bf16 a[88:91], a[228:231], a[192:195], a[88:91]\n\t"
"s_waitcnt lgkmcnt(3)\n\t"
"v_mfma_f32_16x16x32_bf16 a[88:91], v[248:251], a[196:199], a[88:91]\n\t"
"s_waitcnt lgkmcnt(1)\n\t"
"v_mfma_f32_16x16x32_bf16 a[88:91], a[212:215], a[200:203], a[88:91]\n\t"
"v_mfma_f32_16x16x32_bf16 v[224:227], v[224:227], a[208:211], 0\n\t"
"v_mfma_f32_16x16x32_bf16 v[236:239], v[236:239], a[208:211], 0\n\t"
"ds_read_b64_tr_b16 v[228:229], v246 offset:512\n\t"
"v_mfma_f32_16x16x32_bf16 a[96:99], a[228:231], a[196:199], a[96:99]\n\t"
"buffer_load_dwordx4 a[228:231], v252, s[8:11], s20 offen offset:1024 sc1\n\t"
"v_mfma_f32_16x16x32_bf16 a[96:99], v[248:251], a[200:203], a[96:99]\n\t"
"s_waitcnt vmcnt(8)\n\t"
"ds_read_b64_tr_b16 v[230:231], v246 offset:3584\n\t"
"v_mfma_f32_16x16x32_bf16 a[96:99], a[212:215], a[204:207], a[96:99]\n\t"
"v_mfma_f32_16x16x32_bf16 a[104:107], a[216:219], a[168:171], a[104:107]\n\t"
"buffer_load_dwordx4 v[248:251], v252, s[8:11], s20 offen offset:2048 sc1\n\t"
"v_mfma_f32_16x16x32_bf16 a[104:107], v[96:99], a[172:175], a[104:107]\n\t"
"s_mov_b32 s20, 1290240\n\t"
"ds_read_b64_tr_b16 v[232:233], v246 offset:6656\n\t"
"v_cvt_pk_bf16_f32 v224, v224, v225\n\t"
"v_mfma_f32_16x16x32_bf16 a[104:107], a[232:235], a[176:179], a[104:107]\n\t"
"ds_read_b64_tr_b16 v[234:235], v246 offset:9728\n\t"
"v_cvt_pk_bf16_f32 v225, v226, v227\n\t"
"v_mfma_f32_16x16x32_bf16 a[112:115], a[216:219], a[172:175], a[112:115]\n\t"
"ds_read_b128 a[168:171], v244 offset:9216\n\t"
"v_cvt_pk_bf16_f32 v226, v236, v237\n\t"
"v_mfma_f32_16x16x32_bf16 a[112:115], v[96:99], a[176:179], a[112:115]\n\t"
"ds_read_b128 a[172:175], v244 offset:9728\n\t"
"v_cvt_pk_bf16_f32 v227, v238, v239\n\t"
"v_mfma_f32_16x16x32_bf16 a[112:115], a[232:235], a[180:183], a[112:115]\n\t"
"ds_write_b128 v243, v[224:227] offset:46080\n\t"
"v_mfma_f32_16x16x32_bf16 a[120:123], a[216:219], a[176:179], a[120:123]\n\t"
"buffer_load_dwordx4 a[212:215], v252, s[8:11], s20 offen offset:0 sc1\n\t"
"v_mfma_f32_16x16x32_bf16 a[120:123], v[96:99], a[180:183], a[120:123]\n\t"
"ds_read_b64_tr_b16 v[236:237], v246 offset:12800\n\t"
"v_mfma_f32_16x16x32_bf16 a[120:123], a[232:235], a[184:187], a[120:123]\n\t"
"ds_read_b64_tr_b16 v[238:239], v246 offset:15872\n\t"
"v_mfma_f32_16x16x32_bf16 a[128:131], a[216:219], a[180:183], a[128:131]\n\t"
"s_waitcnt lgkmcnt(7)\n\t"
"ds_read_b128 a[176:179], v244 offset:10240\n\t"
"v_mfma_f32_16x16x32_bf16 a[128:131], v[96:99], a[184:187], a[128:131]\n\t"
"ds_read_b64_tr_b16 v[224:225], v246 offset:20480\n\t"
"v_mfma_f32_16x16x32_bf16 a[128:131], a[232:235], a[188:191], a[128:131]\n\t"
"s_waitcnt lgkmcnt(7)\n\t"
"ds_read_b64_tr_b16 v[226:227], v246 offset:23552\n\t"
"v_mfma_f32_16x16x32_bf16 v[228:231], v[228:231], v[220:223], 0\n\t"
"ds_read_b128 a[180:183], v244 offset:10752\n\t"
"v_mfma_f32_16x16x32_bf16 a[136:139], a[216:219], a[184:187], a[136:139]\n\t"
"Lpg7_%=:\n\t"
"s_cbranch_vccnz Lpg11_%=\n\t"
"ds_read_b128 a[184:187], v244 offset:11264\n\t"
"v_mfma_f32_16x16x32_bf16 a[136:139], v[96:99], a[188:191], a[136:139]\n\t"
"v_mfma_f32_16x16x32_bf16 a[136:139], a[232:235], a[192:195], a[136:139]\n\t"
"v_mfma_f32_16x16x32_bf16 v[232:235], v[232:235], v[220:223], 0\n\t"
"v_mfma_f32_16x16x32_bf16 a[144:147], a[216:219], a[188:191], a[144:147]\n\t"
"s_waitcnt lgkmcnt(5)\n\t"
"ds_read_b128 a[188:191], v244 offset:11776\n\t"
"v_cvt_pk_bf16_f32 v228, v228, v229\n\t"
"v_mfma_f32_16x16x32_bf16 a[144:147], v[96:99], a[192:195], a[144:147]\n\t"
"v_cvt_pk_bf16_f32 v229, v230, v231\n\t"
"v_mfma_f32_16x16x32_bf16 a[144:147], a[232:235], a[196:199], a[144:147]\n\t"
"v_mfma_f32_16x16x32_bf16 v[236:239], v[236:239], v[220:223], 0\n\t"
"v_mfma_f32_16x16x32_bf16 a[152:155], a[216:219], a[192:195], a[152:155]\n\t"
"s_waitcnt lgkmcnt(3)\n\t"
"ds_read_b128 a[192:195], v244 offset:12288\n\t"
"v_cvt_pk_bf16_f32 v230, v232, v233\n\t"
"v_mfma_f32_16x16x32_bf16 a[152:155], v[96:99], a[196:199], a[152:155]\n\t"
"v_cvt_pk_bf16_f32 v231, v234, v235\n\t"
"v_mfma_f32_16x16x32_bf16 a[152:155], a[232:235], a[200:203], a[152:155]\n\t"
"ds_read_b64_tr_b16 v[232:233], v246 offset:26624\n\t"
"v_mfma_f32_16x16x32_bf16 v[224:227], v[224:227], v[220:223], 0\n\t"
"ds_read_b64_tr_b16 v[234:235], v246 offset:29696\n\t"
"v_mfma_f32_16x16x32_bf16 a[160:163], a[216:219], a[196:199], a[160:163]\n\t"
"buffer_load_dwordx4 a[216:219], v252, s[8:11], s20 offen offset:1024 sc1\n\t"
"v_mfma_f32_16x16x32_bf16 a[160:163], v[96:99], a[200:203], a[160:163]\n\t"
"s_waitcnt vmcnt(8)\n\t"
"ds_read_b128 a[196:199], v244 offset:12800\n\t"
"v_cvt_pk_bf16_f32 v236, v236, v237\n\t"
"v_mfma_f32_16x16x32_bf16 a[160:163], a[232:235], a[204:207], a[160:163]\n\t"
"ds_read_b128 a[200:203], v244 offset:13312\n\t"
"v_cvt_pk_bf16_f32 v237, v238, v239\n\t"
"v_mfma_f32_16x16x32_bf16 v[200:203], a[236:239], a[168:171], v[200:203]\n\t"
"ds_read_b128 a[204:207], v244 offset:13824\n\t"
"v_mfma_f32_16x16x32_bf16 v[200:203], a[220:223], a[172:175], v[200:203]\n\t"
"buffer_load_dwordx4 v[96:99], v252, s[8:11], s20 offen offset:2048 sc1\n\t"
"v_mfma_f32_16x16x32_bf16 v[200:203], a[224:227], a[176:179], v[200:203]\n\t"
"s_waitcnt lgkmcnt(3)\n\t"
"s_mov_b32 s20, 1302528\n\t"
"v_cvt_pk_bf16_f32 v238, v224, v225\n\t"
"v_mfma_f32_16x16x32_bf16 v[232:235], v[232:235], v[220:223], 0\n\t"
"v_cvt_pk_bf16_f32 v239, v226, v227\n\t"
"v_mfma_f32_16x16x32_bf16 v[208:211], a[236:239], a[172:175], v[208:211]\n\t"
"buffer_load_dwordx4 a[232:235], v252, s[8:11], s20 offen offset:0 sc1\n\t"
"v_mfma_f32_16x16x32_bf16 v[208:211], a[220:223], a[176:179], v[208:211]\n\t"
"v_mfma_f32_16x16x32_bf16 v[208:211], a[224:227], a[180:183], v[208:211]\n\t"
"s_waitcnt lgkmcnt(0)\n\t"
"s_barrier\n\t"
"v_mfma_f32_16x16x32_bf16 v[216:219], a[236:239], a[176:179], v[216:219]\n\t"
"ds_read_b64_tr_b16 v[224:225], v246 offset:32768\n\t"
"v_mfma_f32_16x16x32_bf16 v[216:219], a[220:223], a[180:183], v[216:219]\n\t"
"ds_read_b64_tr_b16 v[226:227], v246 offset:35840\n\t"
"v_cvt_pk_bf16_f32 v232, v232, v233\n\t"
"v_mfma_f32_16x16x32_bf16 v[216:219], a[224:227], a[184:187], v[216:219]\n\t"
"ds_write2st64_b64 v245, v[228:229], v[230:231] offset0:0 offset1:1\n\t"
"v_mfma_f32_16x16x32_bf16 a[4:7], a[236:239], a[180:183], a[4:7]\n\t"
"v_cvt_pk_bf16_f32 v233, v234, v235\n\t"
"v_mfma_f32_16x16x32_bf16 a[4:7], a[220:223], a[184:187], a[4:7]\n\t"
"ds_read_b64_tr_b16 v[228:229], v246 offset:40448\n\t"
"v_mfma_f32_16x16x32_bf16 a[4:7], a[224:227], a[188:191], a[4:7]\n\t"
"ds_write2st64_b64 v245, v[236:237], v[238:239] offset0:2 offset1:3\n\t"
"v_mfma_f32_16x16x32_bf16 a[12:15], a[236:239], a[184:187], a[12:15]\n\t"
"v_mfma_f32_16x16x32_bf16 a[12:15], a[220:223], a[188:191], a[12:15]\n\t"
"ds_read_b64_tr_b16 v[230:231], v246 offset:43520\n\t"
"v_mfma_f32_16x16x32_bf16 a[12:15], a[224:227], a[192:195], a[12:15]\n\t"
"ds_write_b64 v245, v[232:233] offset:2048\n\t"
"v_mfma_f32_16x16x32_bf16 a[20:23], a[236:239], a[188:191], a[20:23]\n\t"
"s_waitcnt lgkmcnt(5)\n\t"
"ds_read_b64_tr_b16 v[232:233], v246 offset:46592\n\t"
"v_mfma_f32_16x16x32_bf16 a[20:23], a[220:223], a[192:195], a[20:23]\n\t"
"ds_read_b64_tr_b16 v[234:235], v246 offset:49664\n\t"
"v_mfma_f32_16x16x32_bf16 a[20:23], a[224:227], a[196:199], a[20:23]\n\t"
"v_mfma_f32_16x16x32_bf16 v[224:227], v[224:227], v[220:223], 0\n\t"
"v_mfma_f32_16x16x32_bf16 a[28:31], a[236:239], a[192:195], a[28:31]\n\t"
"s_waitcnt lgkmcnt(3)\n\t"
"v_mfma_f32_16x16x32_bf16 a[28:31], a[220:223], a[196:199], a[28:31]\n\t"
"v_mfma_f32_16x16x32_bf16 a[28:31], a[224:227], a[200:203], a[28:31]\n\t"
"v_mfma_f32_16x16x32_bf16 v[228:231], v[228:231], v[220:223], 0\n\t"
"v_mfma_f32_16x16x32_bf16 a[36:39], a[236:239], a[196:199], a[36:39]\n\t"
"buffer_load_dwordx4 a[236:239], v252, s[8:11], s20 offen offset:1024 sc1\n\t"
"v_mfma_f32_16x16x32_bf16 a[36:39], a[220:223], a[200:203], a[36:39]\n\t"
"s_waitcnt vmcnt(8) lgkmcnt(0)\n\t"
"v_cvt_pk_bf16_f32 v224, v224, v225\n\t"
"v_cvt_pk_bf16_f32 v225, v226, v227\n\t"
"v_mfma_f32_16x16x32_bf16 a[36:39], a[224:227], a[204:207], a[36:39]\n\t"
"v_mfma_f32_16x16x32_bf16 v[232:235], v[232:235], v[220:223], 0\n\t"
"buffer_load_dwordx4 a[220:223], v252, s[8:11], s20 offen offset:2048 sc1\n\t"
"v_mfma_f32_16x16x32_bf16 a[44:47], a[240:243], a[168:171], a[44:47]\n\t"
"s_mov_b32 s20, 1314816\n\t"
"v_cvt_pk_bf16_f32 v226, v228, v229\n\t"
"v_cvt_pk_bf16_f32 v227, v230, v231\n\t"
"v_mfma_f32_16x16x32_bf16 a[44:47], a[244:247], a[172:175], a[44:47]\n\t"
"ds_read_b64_tr_b16 v[228:229], v246 offset:52736\n\t"
"v_mfma_f32_16x16x32_bf16 a[44:47], a[248:251], a[176:179], a[44:47]\n\t"
"buffer_load_dwordx4 a[224:227], v252, s[8:11], s20 offen offset:0 sc1\n\t"
"v_mfma_f32_16x16x32_bf16 a[52:55], a[240:243], a[172:175], a[52:55]\n\t"
"ds_read_b64_tr_b16 v[230:231], v246 offset:55808\n\t"
"v_mfma_f32_16x16x32_bf16 a[52:55], a[244:247], a[176:179], a[52:55]\n\t"
"v_cvt_pk_bf16_f32 v232, v232, v233\n\t"
"v_cvt_pk_bf16_f32 v233, v234, v235\n\t"
"v_mfma_f32_16x16x32_bf16 a[52:55], a[248:251], a[180:183], a[52:55]\n\t"
"ds_write2st64_b64 v245, v[224:225], v[226:227] offset0:5 offset1:6\n\t"
"v_mfma_f32_16x16x32_bf16 a[60:63], a[240:243], a[176:179], a[60:63]\n\t"
"v_mfma_f32_16x16x32_bf16 a[60:63], a[244:247], a[180:183], a[60:63]\n\t"
"v_add_u32 v243, 55296, v243\n\t"
"v_mfma_f32_16x16x32_bf16 a[60:63], a[248:251], a[184:187], a[60:63]\n\t"
"v_mfma_f32_16x16x32_bf16 a[68:71], a[240:243], a[180:183], a[68:71]\n\t"
"v_mfma_f32_16x16x32_bf16 a[68:71], a[244:247], a[184:187], a[68:71]\n\t"
"v_mfma_f32_16x16x32_bf16 a[68:71], a[248:251], a[188:191], a[68:71]\n\t"
"v_mfma_f32_16x16x32_bf16 a[76:79], a[240:243], a[184:187], a[76:79]\n\t"
"s_waitcnt lgkmcnt(1)\n\t"
"v_mfma_f32_16x16x32_bf16 a[76:79], a[244:247], a[188:191], a[76:79]\n\t"
"v_mfma_f32_16x16x32_bf16 a[76:79], a[248:251], a[192:195], a[76:79]\n\t"
"v_mfma_f32_16x16x32_bf16 v[228:231], v[228:231], v[220:223], 0\n\t"
"v_mfma_f32_16x16x32_bf16 a[84:87], a[240:243], a[188:191], a[84:87]\n\t"
"v_mfma_f32_16x16x32_bf16 a[84:87], a[244:247], a[192:195], a[84:87]\n\t"
"v_mfma_f32_16x16x32_bf16 a[84:87], a[248:251], a[196:199], a[84:87]\n\t"
"v_mfma_f32_16x16x32_bf16 a[92:95], a[240:243], a[192:195], a[92:95]\n\t"
"v_mfma_f32_16x16x32_bf16 a[92:95], a[244:247], a[196:199], a[92:95]\n\t"
"v_cvt_pk_bf16_f32 v234, v228, v229\n\t"
"v_cvt_pk_bf16_f32 v235, v230, v231\n\t"
"v_mfma_f32_16x16x32_bf16 a[92:95], a[248:251], a[200:203], a[92:95]\n\t"
"ds_write2st64_b64 v245, v[232:233], v[234:235] offset0:7 offset1:8\n\t"
"v_mfma_f32_16x16x32_bf16 a[100:103], a[240:243], a[196:199], a[100:103]\n\t"
"buffer_load_dwordx4 a[240:243], v252, s[8:11], s20 offen offset:1024 sc1\n\t"
"v_mfma_f32_16x16x32_bf16 a[100:103], a[244:247], a[200:203], a[100:103]\n\t"
"s_waitcnt vmcnt(8)\n\t"
"v_mfma_f32_16x16x32_bf16 a[100:103], a[248:251], a[204:207], a[100:103]\n\t"
"v_mfma_f32_16x16x32_bf16 a[108:111], a[252:255], a[168:171], a[108:111]\n\t"
"buffer_load_dwordx4 a[244:247], v252, s[8:11], s20 offen offset:2048 sc1\n\t"
"v_mfma_f32_16x16x32_bf16 a[108:111], a[228:231], a[172:175], a[108:111]\n\t"
"s_mov_b32 s20, 1327104\n\t"
"v_mfma_f32_16x16x32_bf16 a[108:111], v[248:251], a[176:179], a[108:111]\n\t"
"s_waitcnt lgkmcnt(0)\n\t"
"s_barrier\n\t"
"v_mfma_f32_16x16x32_bf16 a[116:119], a[252:255], a[172:175], a[116:119]\n\t"
"ds_read_b64_tr_b16 v[236:237], v240 offset:512\n\t"
"v_mfma_f32_16x16x32_bf16 a[116:119], a[228:231], a[176:179], a[116:119]\n\t"
"ds_read_b64_tr_b16 v[238:239], v240 offset:2048\n\t"
"v_mfma_f32_16x16x32_bf16 a[116:119], v[248:251], a[180:183], a[116:119]\n\t"
"ds_read_b64_tr_b16 v[224:225], v241 offset:512\n\t"
"v_mfma_f32_16x16x32_bf16 a[124:127], a[252:255], a[176:179], a[124:127]\n\t"
"ds_read_b64_tr_b16 v[226:227], v241 offset:2048\n\t"
"v_mfma_f32_16x16x32_bf16 a[124:127], a[228:231], a[180:183], a[124:127]\n\t"
"buffer_load_dwordx4 a[248:251], v252, s[8:11], s20 offen offset:0 sc1\n\t"
"v_mfma_f32_16x16x32_bf16 a[124:127], v[248:251], a[184:187], a[124:127]\n\t"
"ds_read_b64_tr_b16 v[228:229], v240 offset:20480\n\t"
"v_mfma_f32_16x16x32_bf16 a[132:135], a[252:255], a[180:183], a[132:135]\n\t"
"s_waitcnt lgkmcnt(3)\n\t"
"ds_read_b64_tr_b16 v[230:231], v240 offset:22016\n\t"
"v_mfma_f32_16x16x32_bf16 a[132:135], a[228:231], a[184:187], a[132:135]\n\t"
"ds_read_b64_tr_b16 v[232:233], v241 offset:20480\n\t"
"v_mfma_f32_16x16x32_bf16 a[132:135], v[248:251], a[188:191], a[132:135]\n\t"
"s_waitcnt lgkmcnt(3)\n\t"
"ds_read_b64_tr_b16 v[234:235], v241 offset:22016\n\t"
"v_mfma_f32_16x16x32_bf16 v[236:239], v[236:239], a[208:211], 0\n\t"
"ds_read_b128 a[168:171], v244 offset:0\n\t"
"v_mfma_f32_16x16x32_bf16 v[224:227], v[224:227], a[208:211], 0\n\t"
"ds_read_b128 a[172:175], v244 offset:512\n\t"
"v_mfma_f32_16x16x32_bf16 a[140:143], a[252:255], a[184:187], a[140:143]\n\t"
"ds_read_b128 a[176:179], v244 offset:1024\n\t"
"v_mfma_f32_16x16x32_bf16 a[140:143], a[228:231], a[188:191], a[140:143]\n\t"
"ds_read_b128 a[180:183], v244 offset:1536\n\t"
"v_mfma_f32_16x16x32_bf16 a[140:143], v[248:251], a[192:195], a[140:143]\n\t"
"ds_read_b128 a[184:187], v244 offset:2048\n\t"
"v_mfma_f32_16x16x32_bf16 a[148:151], a[252:255], a[188:191], a[148:151]\n\t"
"s_waitcnt lgkmcnt(7)\n\t"
"ds_read_b128 a[188:191], v244 offset:2560\n\t"
"v_cvt_pk_bf16_f32 v236, v236, v237\n\t"
"v_mfma_f32_16x16x32_bf16 a[148:151], a[228:231], a[192:195], a[148:151]\n\t"
"s_waitcnt lgkmcnt(6)\n\t"
"v_cvt_pk_bf16_f32 v237, v238, v239\n\t"
"v_cvt_pk_bf16_f32 v238, v224, v225\n\t"
"v_mfma_f32_16x16x32_bf16 a[148:151], v[248:251], a[196:199], a[148:151]\n\t"
"v_cvt_pk_bf16_f32 v239, v226, v227\n\t"
"v_mfma_f32_16x16x32_bf16 v[228:231], v[228:231], a[208:211], 0\n\t"
"ds_write_b128 v243, v[236:239] offset:27648\n\t"
"v_mfma_f32_16x16x32_bf16 v[232:235], v[232:235], a[208:211], 0\n\t"
"v_mfma_f32_16x16x32_bf16 a[156:159], a[252:255], a[192:195], a[156:159]\n\t"
"ds_read_b128 a[192:195], v244 offset:3072\n\t"
"v_mfma_f32_16x16x32_bf16 a[156:159], a[228:231], a[196:199], a[156:159]\n\t"
"ds_read_b64_tr_b16 v[224:225], v240 offset:40448\n\t"
"v_mfma_f32_16x16x32_bf16 a[156:159], v[248:251], a[200:203], a[156:159]\n\t"
"ds_read_b64_tr_b16 v[226:227], v240 offset:41984\n\t"
"v_mfma_f32_16x16x32_bf16 a[164:167], a[252:255], a[196:199], a[164:167]\n\t"
"buffer_load_dwordx4 a[252:255], v252, s[8:11], s20 offen offset:1024 sc1\n\t"
"v_mfma_f32_16x16x32_bf16 a[164:167], a[228:231], a[200:203], a[164:167]\n\t"
"s_waitcnt vmcnt(8) lgkmcnt(7)\n\t"
"ds_read_b64_tr_b16 v[236:237], v241 offset:40448\n\t"
"v_cvt_pk_bf16_f32 v228, v228, v229\n\t"
"v_mfma_f32_16x16x32_bf16 a[164:167], v[248:251], a[204:207], a[164:167]\n\t"
"ds_read_b64_tr_b16 v[238:239], v241 offset:41984\n\t"
"v_cvt_pk_bf16_f32 v229, v230, v231\n\t"
"Lst10_%=:\n\t"
"v_mfma_f32_16x16x32_bf16 v[196:199], a[212:215], a[168:171], v[196:199]\n\t"
"s_waitcnt lgkmcnt(8)\n\t"
"ds_read_b128 a[196:199], v244 offset:3584\n\t"
"v_cvt_pk_bf16_f32 v230, v232, v233\n\t"
"v_mfma_f32_16x16x32_bf16 v[196:199], a[216:219], a[172:175], v[196:199]\n\t"
"ds_read_b128 a[200:203], v244 offset:4096\n\t"
"v_cvt_pk_bf16_f32 v231, v234, v235\n\t"
"v_mfma_f32_16x16x32_bf16 v[196:199], v[96:99], a[176:179], v[196:199]\n\t"
"ds_write_b128 v243, v[228:231] offset:36864\n\t"
"v_mfma_f32_16x16x32_bf16 v[204:207], a[212:215], a[172:175], v[204:207]\n\t"
"buffer_load_dwordx4 v[248:251], v252, s[8:11], s20 offen offset:2048 sc1\n\t"
"v_mfma_f32_16x16x32_bf16 v[204:207], a[216:219], a[176:179], v[204:207]\n\t"
"s_waitcnt lgkmcnt(10)\n\t"
"ds_read_b128 a[204:207], v244 offset:4608\n\t"
"v_mfma_f32_16x16x32_bf16 v[204:207], v[96:99], a[180:183], v[204:207]\n\t"
"s_mov_b32 s20, 1339392\n\t"
"v_subrev_u32 v240, 59904, v240\n\t"
"v_subrev_u32 v241, 59904, v241\n\t"
"v_mfma_f32_16x16x32_bf16 v[212:215], a[212:215], a[176:179], v[212:215]\n\t"
"s_waitcnt lgkmcnt(6)\n\t"
"ds_read_b64_tr_b16 v[232:233], v240 offset:512\n\t"
"v_mfma_f32_16x16x32_bf16 v[212:215], a[216:219], a[180:183], v[212:215]\n\t"
"buffer_load_dwordx4 a[228:231], v252, s[8:11], s20 offen offset:0 sc1\n\t"
"v_mfma_f32_16x16x32_bf16 v[212:215], v[96:99], a[184:187], v[212:215]\n\t"
"s_waitcnt lgkmcnt(5)\n\t"
"ds_read_b64_tr_b16 v[234:235], v240 offset:2048\n\t"
"v_mfma_f32_16x16x32_bf16 v[224:227], v[224:227], a[208:211], 0\n\t"
"ds_read_b64_tr_b16 v[228:229], v241 offset:512\n\t"
"v_mfma_f32_16x16x32_bf16 v[236:239], v[236:239], a[208:211], 0\n\t"
"ds_read_b64_tr_b16 v[230:231], v241 offset:2048\n\t"
"v_mfma_f32_16x16x32_bf16 a[0:3], a[212:215], a[180:183], a[0:3]\n\t"
"v_mfma_f32_16x16x32_bf16 a[0:3], a[216:219], a[184:187], a[0:3]\n\t"
"v_mfma_f32_16x16x32_bf16 a[0:3], v[96:99], a[188:191], a[0:3]\n\t"
"v_mfma_f32_16x16x32_bf16 a[8:11], a[212:215], a[184:187], a[8:11]\n\t"
"s_waitcnt lgkmcnt(2)\n\t"
"v_cvt_pk_bf16_f32 v224, v224, v225\n\t"
"v_cvt_pk_bf16_f32 v225, v226, v227\n\t"
"v_mfma_f32_16x16x32_bf16 a[8:11], a[216:219], a[188:191], a[8:11]\n\t"
"s_waitcnt lgkmcnt(0)\n\t"
"v_cvt_pk_bf16_f32 v226, v236, v237\n\t"
"v_cvt_pk_bf16_f32 v227, v238, v239\n\t"
"v_mfma_f32_16x16x32_bf16 a[8:11], v[96:99], a[192:195], a[8:11]\n\t"
"ds_write_b128 v243, v[224:227] offset:46080\n\t"
"v_mfma_f32_16x16x32_bf16 v[232:235], v[232:235], a[208:211], 0\n\t"
"v_mfma_f32_16x16x32_bf16 v[228:231], v[228:231], a[208:211], 0\n\t"
"ds_read_b64_tr_b16 v[236:237], v240 offset:20480\n\t"
"v_mfma_f32_16x16x32_bf16 a[16:19], a[212:215], a[188:191], a[16:19]\n\t"
"ds_read_b64_tr_b16 v[238:239], v240 offset:22016\n\t"
"v_mfma_f32_16x16x32_bf16 a[16:19], a[216:219], a[192:195], a[16:19]\n\t"
"ds_read_b64_tr_b16 v[224:225], v241 offset:20480\n\t"
"v_mfma_f32_16x16x32_bf16 a[16:19], v[96:99], a[196:199], a[16:19]\n\t"
"ds_read_b64_tr_b16 v[226:227], v241 offset:22016\n\t"
"v_mfma_f32_16x16x32_bf16 a[24:27], a[212:215], a[192:195], a[24:27]\n\t"
"v_cvt_pk_bf16_f32 v232, v232, v233\n\t"
"v_cvt_pk_bf16_f32 v233, v234, v235\n\t"
"v_mfma_f32_16x16x32_bf16 a[24:27], a[216:219], a[196:199], a[24:27]\n\t"
"v_cvt_pk_bf16_f32 v234, v228, v229\n\t"
"v_cvt_pk_bf16_f32 v235, v230, v231\n\t"
"v_mfma_f32_16x16x32_bf16 a[24:27], v[96:99], a[200:203], a[24:27]\n\t"
"ds_write_b128 v243, v[232:235] offset:0\n\t"
"v_mfma_f32_16x16x32_bf16 a[32:35], a[212:215], a[196:199], a[32:35]\n\t"
"buffer_load_dwordx4 a[212:215], v252, s[8:11], s20 offen offset:1024 sc1\n\t"
"v_mfma_f32_16x16x32_bf16 a[32:35], a[216:219], a[200:203], a[32:35]\n\t"
"s_waitcnt vmcnt(8)\n\t"
"ds_read_b64_tr_b16 v[228:229], v240 offset:40448\n\t"
"v_mfma_f32_16x16x32_bf16 a[32:35], v[96:99], a[204:207], a[32:35]\n\t"
"ds_read_b64_tr_b16 v[230:231], v240 offset:41984\n\t"
"v_mfma_f32_16x16x32_bf16 a[40:43], a[232:235], a[168:171], a[40:43]\n\t"
"buffer_load_dwordx4 v[96:99], v252, s[8:11], s20 offen offset:2048 sc1\n\t"
"v_mfma_f32_16x16x32_bf16 a[40:43], a[236:239], a[172:175], a[40:43]\n\t"
"s_mov_b32 s20, 1351680\n\t"
"ds_read_b64_tr_b16 v[232:233], v241 offset:40448\n\t"
"v_mfma_f32_16x16x32_bf16 a[40:43], a[220:223], a[176:179], a[40:43]\n\t"
"ds_read_b64_tr_b16 v[234:235], v241 offset:41984\n\t"
"v_mfma_f32_16x16x32_bf16 a[48:51], a[232:235], a[172:175], a[48:51]\n\t"
"buffer_load_dwordx4 a[216:219], v252, s[8:11], s20 offen offset:0 sc1\n\t"
"v_mfma_f32_16x16x32_bf16 a[48:51], a[236:239], a[176:179], a[48:51]\n\t"
"s_waitcnt lgkmcnt(5)\n\t"
"v_mfma_f32_16x16x32_bf16 a[48:51], a[220:223], a[180:183], a[48:51]\n\t"
"v_mfma_f32_16x16x32_bf16 v[236:239], v[236:239], a[208:211], 0\n\t"
"v_mfma_f32_16x16x32_bf16 v[224:227], v[224:227], a[208:211], 0\n\t"
"v_xor_b32 v246, 0xc0, v246\n\t"
"v_mfma_f32_16x16x32_bf16 a[56:59], a[232:235], a[176:179], a[56:59]\n\t"
"v_mfma_f32_16x16x32_bf16 a[56:59], a[236:239], a[180:183], a[56:59]\n\t"
"v_mfma_f32_16x16x32_bf16 a[56:59], a[220:223], a[184:187], a[56:59]\n\t"
"v_mfma_f32_16x16x32_bf16 a[64:67], a[232:235], a[180:183], a[64:67]\n\t"
"s_waitcnt lgkmcnt(2)\n\t"
"v_cvt_pk_bf16_f32 v236, v236, v237\n\t"
"v_cvt_pk_bf16_f32 v237, v238, v239\n\t"
"v_mfma_f32_16x16x32_bf16 a[64:67], a[236:239], a[184:187], a[64:67]\n\t"
"s_waitcnt lgkmcnt(0)\n\t"
"v_cvt_pk_bf16_f32 v238, v224, v225\n\t"
"v_cvt_pk_bf16_f32 v239, v226, v227\n\t"
"v_mfma_f32_16x16x32_bf16 a[64:67], a[220:223], a[188:191], a[64:67]\n\t"
"ds_write_b128 v243, v[236:239] offset:9216\n\t"
"v_mfma_f32_16x16x32_bf16 v[228:231], v[228:231], a[208:211], 0\n\t"
"v_mfma_f32_16x16x32_bf16 v[232:235], v[232:235], a[208:211], 0\n\t"
"v_mfma_f32_16x16x32_bf16 a[72:75], a[232:235], a[184:187], a[72:75]\n\t"
"v_mfma_f32_16x16x32_bf16 a[72:75], a[236:239], a[188:191], a[72:75]\n\t"
"v_mfma_f32_16x16x32_bf16 a[72:75], a[220:223], a[192:195], a[72:75]\n\t"
"v_mfma_f32_16x16x32_bf16 a[80:83], a[232:235], a[188:191], a[80:83]\n\t"
"v_cvt_pk_bf16_f32 v228, v228, v229\n\t"
"v_cvt_pk_bf16_f32 v229, v230, v231\n\t"
"v_mfma_f32_16x16x32_bf16 a[80:83], a[236:239], a[192:195], a[80:83]\n\t"
"v_cvt_pk_bf16_f32 v230, v232, v233\n\t"
"v_cvt_pk_bf16_f32 v231, v234, v235\n\t"
"v_mfma_f32_16x16x32_bf16 a[80:83], a[220:223], a[196:199], a[80:83]\n\t"
"ds_write_b128 v243, v[228:231] offset:18432\n\t"
"v_mfma_f32_16x16x32_bf16 a[88:91], a[232:235], a[192:195], a[88:91]\n\t"
"v_mfma_f32_16x16x32_bf16 a[88:91], a[236:239], a[196:199], a[88:91]\n\t"
"ds_read_b64_tr_b16 v[224:225], v246 offset:512\n\t"
"v_mfma_f32_16x16x32_bf16 a[88:91], a[220:223], a[200:203], a[88:91]\n\t"
"ds_read_b64_tr_b16 v[226:227], v246 offset:3584\n\t"
"v_mfma_f32_16x16x32_bf16 a[96:99], a[232:235], a[196:199], a[96:99]\n\t"
"buffer_load_dwordx4 a[232:235], v252, s[8:11], s20 offen offset:1024 sc1\n\t"
"v_mfma_f32_16x16x32_bf16 a[96:99], a[236:239], a[200:203], a[96:99]\n\t"
"s_waitcnt vmcnt(8)\n\t"
"ds_read_b64_tr_b16 v[236:237], v246 offset:6656\n\t"
"v_mfma_f32_16x16x32_bf16 a[96:99], a[220:223], a[204:207], a[96:99]\n\t"
"ds_read_b64_tr_b16 v[238:239], v246 offset:9728\n\t"
"v_mfma_f32_16x16x32_bf16 a[104:107], a[224:227], a[168:171], a[104:107]\n\t"
"buffer_load_dwordx4 a[220:223], v252, s[8:11], s20 offen offset:2048 sc1\n\t"
"v_mfma_f32_16x16x32_bf16 a[104:107], a[240:243], a[172:175], a[104:107]\n\t"
"s_mov_b32 s20, 1216512\n\t"
"ds_read_b64_tr_b16 v[232:233], v246 offset:12800\n\t"
"v_mfma_f32_16x16x32_bf16 a[104:107], a[244:247], a[176:179], a[104:107]\n\t"
"ds_read_b64_tr_b16 v[234:235], v246 offset:15872\n\t"
"v_mfma_f32_16x16x32_bf16 a[112:115], a[224:227], a[172:175], a[112:115]\n\t"
"buffer_load_dwordx4 a[236:239], v252, s[8:11], s20 offen offset:0 sc1\n\t"
"v_mfma_f32_16x16x32_bf16 a[112:115], a[240:243], a[176:179], a[112:115]\n\t"
"ds_read_b128 a[168:171], v244 offset:9216\n\t"
"v_mfma_f32_16x16x32_bf16 a[112:115], a[244:247], a[180:183], a[112:115]\n\t"
"ds_read_b64_tr_b16 v[228:229], v246 offset:20480\n\t"
"v_mfma_f32_16x16x32_bf16 a[120:123], a[224:227], a[176:179], a[120:123]\n\t"
"s_waitcnt lgkmcnt(6)\n\t"
"ds_read_b64_tr_b16 v[230:231], v246 offset:23552\n\t"
"v_mfma_f32_16x16x32_bf16 a[120:123], a[240:243], a[180:183], a[120:123]\n\t"
"ds_read_b128 a[172:175], v244 offset:9728\n\t"
"v_mfma_f32_16x16x32_bf16 a[120:123], a[244:247], a[184:187], a[120:123]\n\t"
"ds_read_b128 a[176:179], v244 offset:10240\n\t"
"v_mfma_f32_16x16x32_bf16 v[224:227], v[224:227], v[220:223], 0\n\t"
"v_mfma_f32_16x16x32_bf16 a[128:131], a[224:227], a[180:183], a[128:131]\n\t"
"s_waitcnt lgkmcnt(7)\n\t"
"ds_read_b128 a[180:183], v244 offset:10752\n\t"
"v_mfma_f32_16x16x32_bf16 a[128:131], a[240:243], a[184:187], a[128:131]\n\t"
"v_mfma_f32_16x16x32_bf16 a[128:131], a[244:247], a[188:191], a[128:131]\n\t"
"v_mfma_f32_16x16x32_bf16 v[236:239], v[236:239], v[220:223], 0\n\t"
"v_mfma_f32_16x16x32_bf16 a[136:139], a[224:227], a[184:187], a[136:139]\n\t"
"s_waitcnt lgkmcnt(6)\n\t"
"ds_read_b128 a[184:187], v244 offset:11264\n\t"
"v_cvt_pk_bf16_f32 v224, v224, v225\n\t"
"v_mfma_f32_16x16x32_bf16 a[136:139], a[240:243], a[188:191], a[136:139]\n\t"
"v_cvt_pk_bf16_f32 v225, v226, v227\n\t"
"v_mfma_f32_16x16x32_bf16 a[136:139], a[244:247], a[192:195], a[136:139]\n\t"
"v_mfma_f32_16x16x32_bf16 v[232:235], v[232:235], v[220:223], 0\n\t"
"v_mfma_f32_16x16x32_bf16 a[144:147], a[224:227], a[188:191], a[144:147]\n\t"
"s_waitcnt lgkmcnt(4)\n\t"
"ds_read_b128 a[188:191], v244 offset:11776\n\t"
"v_cvt_pk_bf16_f32 v226, v236, v237\n\t"
"v_mfma_f32_16x16x32_bf16 a[144:147], a[240:243], a[192:195], a[144:147]\n\t"
"v_cvt_pk_bf16_f32 v227, v238, v239\n\t"
"v_mfma_f32_16x16x32_bf16 a[144:147], a[244:247], a[196:199], a[144:147]\n\t"
"v_mfma_f32_16x16x32_bf16 v[228:231], v[228:231], v[220:223], 0\n\t"
"v_mfma_f32_16x16x32_bf16 a[152:155], a[224:227], a[192:195], a[152:155]\n\t"
"ds_read_b128 a[192:195], v244 offset:12288\n\t"
"v_cvt_pk_bf16_f32 v232, v232, v233\n\t"
"v_mfma_f32_16x16x32_bf16 a[152:155], a[240:243], a[196:199], a[152:155]\n\t"
"v_cvt_pk_bf16_f32 v233, v234, v235\n\t"
"v_mfma_f32_16x16x32_bf16 a[152:155], a[244:247], a[200:203], a[152:155]\n\t"
"v_mfma_f32_16x16x32_bf16 a[160:163], a[224:227], a[196:199], a[160:163]\n\t"
"buffer_load_dwordx4 a[224:227], v252, s[8:11], s20 offen offset:1024 sc1\n\t"
"v_mfma_f32_16x16x32_bf16 a[160:163], a[240:243], a[200:203], a[160:163]\n\t"
"s_waitcnt vmcnt(8) lgkmcnt(4)\n\t"
"ds_read_b128 a[196:199], v244 offset:12800\n\t"
"v_cvt_pk_bf16_f32 v234, v228, v229\n\t"
"v_mfma_f32_16x16x32_bf16 a[160:163], a[244:247], a[204:207], a[160:163]\n\t"
"ds_read_b128 a[200:203], v244 offset:13312\n\t"
"v_cvt_pk_bf16_f32 v235, v230, v231\n\t"
"v_mfma_f32_16x16x32_bf16 v[200:203], a[248:251], a[168:171], v[200:203]\n\t"
"ds_read_b64_tr_b16 v[228:229], v246 offset:26624\n\t"
"v_mfma_f32_16x16x32_bf16 v[200:203], a[252:255], a[172:175], v[200:203]\n\t"
"s_waitcnt lgkmcnt(6)\n\t"
"ds_read_b64_tr_b16 v[230:231], v246 offset:29696\n\t"
"v_mfma_f32_16x16x32_bf16 v[200:203], v[248:251], a[176:179], v[200:203]\n\t"
"s_waitcnt lgkmcnt(0)\n\t"
"ds_read_b128 a[204:207], v244 offset:13824\n\t"
"v_mfma_f32_16x16x32_bf16 v[228:231], v[228:231], v[220:223], 0\n\t"
"buffer_load_dwordx4 a[240:243], v252, s[8:11], s20 offen offset:2048 sc1\n\t"
"s_mov_b32 s20, 1228800\n\t"
"v_mfma_f32_16x16x32_bf16 v[208:211], a[248:251], a[172:175], v[208:211]\n\t"
"buffer_load_dwordx4 a[244:247], v252, s[8:11], s20 offen offset:0 sc1\n\t"
"v_mfma_f32_16x16x32_bf16 v[208:211], a[252:255], a[176:179], v[208:211]\n\t"
"v_mfma_f32_16x16x32_bf16 v[208:211], v[248:251], a[180:183], v[208:211]\n\t"
"s_waitcnt lgkmcnt(0)\n\t"
"s_barrier\n\t"
"v_mfma_f32_16x16x32_bf16 v[216:219], a[248:251], a[176:179], v[216:219]\n\t"
"v_mfma_f32_16x16x32_bf16 v[216:219], a[252:255], a[180:183], v[216:219]\n\t"
"v_cvt_pk_bf16_f32 v228, v228, v229\n\t"
"v_cvt_pk_bf16_f32 v229, v230, v231\n\t"
"v_mfma_f32_16x16x32_bf16 v[216:219], v[248:251], a[184:187], v[216:219]\n\t"
"ds_write2st64_b64 v245, v[224:225], v[226:227] offset0:0 offset1:1\n\t"
"v_mfma_f32_16x16x32_bf16 a[4:7], a[248:251], a[180:183], a[4:7]\n\t"
"v_mfma_f32_16x16x32_bf16 a[4:7], a[252:255], a[184:187], a[4:7]\n\t"
"ds_read_b64_tr_b16 v[224:225], v246 offset:32768\n\t"
"v_mfma_f32_16x16x32_bf16 a[4:7], v[248:251], a[188:191], a[4:7]\n\t"
"ds_write2st64_b64 v245, v[232:233], v[234:235] offset0:2 offset1:3\n\t"
"v_mfma_f32_16x16x32_bf16 a[12:15], a[248:251], a[184:187], a[12:15]\n\t"
"v_mfma_f32_16x16x32_bf16 a[12:15], a[252:255], a[188:191], a[12:15]\n\t"
"ds_read_b64_tr_b16 v[226:227], v246 offset:35840\n\t"
"v_mfma_f32_16x16x32_bf16 a[12:15], v[248:251], a[192:195], a[12:15]\n\t"
"ds_write_b64 v245, v[228:229] offset:2048\n\t"
"v_mfma_f32_16x16x32_bf16 a[20:23], a[248:251], a[188:191], a[20:23]\n\t"
"ds_read_b64_tr_b16 v[232:233], v246 offset:40448\n\t"
"v_mfma_f32_16x16x32_bf16 a[20:23], a[252:255], a[192:195], a[20:23]\n\t"
"ds_read_b64_tr_b16 v[234:235], v246 offset:43520\n\t"
"v_mfma_f32_16x16x32_bf16 a[20:23], v[248:251], a[196:199], a[20:23]\n\t"
"s_waitcnt lgkmcnt(3)\n\t"
"ds_read_b64_tr_b16 v[228:229], v246 offset:46592\n\t"
"v_mfma_f32_16x16x32_bf16 v[224:227], v[224:227], v[220:223], 0\n\t"
"ds_read_b64_tr_b16 v[230:231], v246 offset:49664\n\t"
"v_mfma_f32_16x16x32_bf16 a[28:31], a[248:251], a[192:195], a[28:31]\n\t"
"v_mfma_f32_16x16x32_bf16 a[28:31], a[252:255], a[196:199], a[28:31]\n\t"
"v_mfma_f32_16x16x32_bf16 a[28:31], v[248:251], a[200:203], a[28:31]\n\t"
"s_waitcnt lgkmcnt(2)\n\t"
"v_mfma_f32_16x16x32_bf16 v[232:235], v[232:235], v[220:223], 0\n\t"
"v_mfma_f32_16x16x32_bf16 a[36:39], a[248:251], a[196:199], a[36:39]\n\t"
"buffer_load_dwordx4 a[248:251], v252, s[8:11], s20 offen offset:1024 sc1\n\t"
"v_mfma_f32_16x16x32_bf16 a[36:39], a[252:255], a[200:203], a[36:39]\n\t"
"s_waitcnt vmcnt(8) lgkmcnt(0)\n\t"
"v_cvt_pk_bf16_f32 v224, v224, v225\n\t"
"v_cvt_pk_bf16_f32 v225, v226, v227\n\t"
"v_mfma_f32_16x16x32_bf16 a[36:39], v[248:251], a[204:207], a[36:39]\n\t"
"v_mfma_f32_16x16x32_bf16 v[228:231], v[228:231], v[220:223], 0\n\t"
"buffer_load_dwordx4 v[248:251], v252, s[8:11], s20 offen offset:2048 sc1\n\t"
"v_mfma_f32_16x16x32_bf16 a[44:47], a[228:231], a[168:171], a[44:47]\n\t"
"s_mov_b32 s20, 1241088\n\t"
"v_cvt_pk_bf16_f32 v226, v232, v233\n\t"
"v_cvt_pk_bf16_f32 v227, v234, v235\n\t"
"v_mfma_f32_16x16x32_bf16 a[44:47], a[212:215], a[172:175], a[44:47]\n\t"
"ds_read_b64_tr_b16 v[232:233], v246 offset:52736\n\t"
"v_mfma_f32_16x16x32_bf16 a[44:47], v[96:99], a[176:179], a[44:47]\n\t"
"buffer_load_dwordx4 a[252:255], v252, s[8:11], s20 offen offset:0 sc1\n\t"
"v_mfma_f32_16x16x32_bf16 a[52:55], a[228:231], a[172:175], a[52:55]\n\t"
"ds_read_b64_tr_b16 v[234:235], v246 offset:55808\n\t"
"v_mfma_f32_16x16x32_bf16 a[52:55], a[212:215], a[176:179], a[52:55]\n\t"
"v_cvt_pk_bf16_f32 v228, v228, v229\n\t"
"v_cvt_pk_bf16_f32 v229, v230, v231\n\t"
"v_mfma_f32_16x16x32_bf16 a[52:55], v[96:99], a[180:183], a[52:55]\n\t"
"ds_write2st64_b64 v245, v[224:225], v[226:227] offset0:5 offset1:6\n\t"
"v_mfma_f32_16x16x32_bf16 a[60:63], a[228:231], a[176:179], a[60:63]\n\t"
"v_mfma_f32_16x16x32_bf16 a[60:63], a[212:215], a[180:183], a[60:63]\n\t"
"v_subrev_u32 v243, 55296, v243\n\t"
"v_mfma_f32_16x16x32_bf16 a[60:63], v[96:99], a[184:187], a[60:63]\n\t"
"v_mfma_f32_16x16x32_bf16 a[68:71], a[228:231], a[180:183], a[68:71]\n\t"
"v_mfma_f32_16x16x32_bf16 a[68:71], a[212:215], a[184:187], a[68:71]\n\t"
"v_mfma_f32_16x16x32_bf16 a[68:71], v[96:99], a[188:191], a[68:71]\n\t"
"v_mfma_f32_16x16x32_bf16 a[76:79], a[228:231], a[184:187], a[76:79]\n\t"
"s_waitcnt lgkmcnt(1)\n\t"
"v_mfma_f32_16x16x32_bf16 a[76:79], a[212:215], a[188:191], a[76:79]\n\t"
"v_mfma_f32_16x16x32_bf16 a[76:79], v[96:99], a[192:195], a[76:79]\n\t"
"v_mfma_f32_16x16x32_bf16 v[232:235], v[232:235], v[220:223], 0\n\t"
"v_mfma_f32_16x16x32_bf16 a[84:87], a[228:231], a[188:191], a[84:87]\n\t"
"v_mfma_f32_16x16x32_bf16 a[84:87], a[212:215], a[192:195], a[84:87]\n\t"
"v_mfma_f32_16x16x32_bf16 a[84:87], v[96:99], a[196:199], a[84:87]\n\t"
"v_mfma_f32_16x16x32_bf16 a[92:95], a[228:231], a[192:195], a[92:95]\n\t"
"v_mfma_f32_16x16x32_bf16 a[92:95], a[212:215], a[196:199], a[92:95]\n\t"
"v_cvt_pk_bf16_f32 v230, v232, v233\n\t"
"v_cvt_pk_bf16_f32 v231, v234, v235\n\t"
"v_mfma_f32_16x16x32_bf16 a[92:95], v[96:99], a[200:203], a[92:95]\n\t"
"ds_write2st64_b64 v245, v[228:229], v[230:231] offset0:7 offset1:8\n\t"
"v_mfma_f32_16x16x32_bf16 a[100:103], a[228:231], a[196:199], a[100:103]\n\t"
"buffer_load_dwordx4 a[228:231], v252, s[8:11], s20 offen offset:1024 sc1\n\t"
"v_mfma_f32_16x16x32_bf16 a[100:103], a[212:215], a[200:203], a[100:103]\n\t"
"s_waitcnt vmcnt(8)\n\t"
"ds_read_b64_tr_b16 v[236:237], v240 offset:1024\n\t"
"v_mfma_f32_16x16x32_bf16 a[100:103], v[96:99], a[204:207], a[100:103]\n\t"
"ds_read_b64_tr_b16 v[238:239], v240 offset:2560\n\t"
"v_mfma_f32_16x16x32_bf16 a[108:111], a[216:219], a[168:171], a[108:111]\n\t"
"buffer_load_dwordx4 v[96:99], v252, s[8:11], s20 offen offset:2048 sc1\n\t"
"v_mfma_f32_16x16x32_bf16 a[108:111], a[232:235], a[172:175], a[108:111]\n\t"
"s_mov_b32 s20, 1253376\n\t"
"ds_read_b64_tr_b16 v[224:225], v241 offset:1024\n\t"
"v_mfma_f32_16x16x32_bf16 a[108:111], a[220:223], a[176:179], a[108:111]\n\t"
"s_waitcnt lgkmcnt(3)\n\t"
"s_barrier\n\t"
"v_mfma_f32_16x16x32_bf16 a[116:119], a[216:219], a[172:175], a[116:119]\n\t"
"ds_read_b64_tr_b16 v[226:227], v241 offset:2560\n\t"
"v_mfma_f32_16x16x32_bf16 a[116:119], a[232:235], a[176:179], a[116:119]\n\t"
"buffer_load_dwordx4 a[212:215], v252, s[8:11], s20 offen offset:0 sc1\n\t"
"v_mfma_f32_16x16x32_bf16 a[116:119], a[220:223], a[180:183], a[116:119]\n\t"
"ds_read_b64_tr_b16 v[232:233], v240 offset:20992\n\t"
"v_mfma_f32_16x16x32_bf16 a[124:127], a[216:219], a[176:179], a[124:127]\n\t"
"ds_read_b64_tr_b16 v[234:235], v240 offset:22528\n\t"
"v_mfma_f32_16x16x32_bf16 a[124:127], a[232:235], a[180:183], a[124:127]\n\t"
"ds_read_b64_tr_b16 v[228:229], v241 offset:20992\n\t"
"v_mfma_f32_16x16x32_bf16 a[124:127], a[220:223], a[184:187], a[124:127]\n\t"
"ds_read_b64_tr_b16 v[230:231], v241 offset:22528\n\t"
"v_mfma_f32_16x16x32_bf16 a[132:135], a[216:219], a[180:183], a[132:135]\n\t"
"s_waitcnt lgkmcnt(6)\n\t"
"ds_read_b128 a[168:171], v244 offset:0\n\t"
"v_mfma_f32_16x16x32_bf16 a[132:135], a[232:235], a[184:187], a[132:135]\n\t"
"s_waitcnt lgkmcnt(5)\n\t"
"ds_read_b128 a[172:175], v244 offset:512\n\t"
"v_mfma_f32_16x16x32_bf16 a[132:135], a[220:223], a[188:191], a[132:135]\n\t"
"ds_read_b128 a[176:179], v244 offset:1024\n\t"
"v_mfma_f32_16x16x32_bf16 v[236:239], v[236:239], a[208:211], 0\n\t"
"ds_read_b128 a[180:183], v244 offset:1536\n\t"
"v_mfma_f32_16x16x32_bf16 v[224:227], v[224:227], a[208:211], 0\n\t"
"v_mfma_f32_16x16x32_bf16 a[140:143], a[216:219], a[184:187], a[140:143]\n\t"
"ds_read_b128 a[184:187], v244 offset:2048\n\t"
"v_mfma_f32_16x16x32_bf16 a[140:143], a[232:235], a[188:191], a[140:143]\n\t"
"v_mfma_f32_16x16x32_bf16 a[140:143], a[220:223], a[192:195], a[140:143]\n\t"
"v_mfma_f32_16x16x32_bf16 a[148:151], a[216:219], a[188:191], a[148:151]\n\t"
"s_waitcnt lgkmcnt(7)\n\t"
"ds_read_b128 a[188:191], v244 offset:2560\n\t"
"v_cvt_pk_bf16_f32 v236, v236, v237\n\t"
"v_mfma_f32_16x16x32_bf16 a[148:151], a[232:235], a[192:195], a[148:151]\n\t"
"s_waitcnt lgkmcnt(6)\n\t"
"v_cvt_pk_bf16_f32 v237, v238, v239\n\t"
"v_cvt_pk_bf16_f32 v238, v224, v225\n\t"
"v_mfma_f32_16x16x32_bf16 a[148:151], a[220:223], a[196:199], a[148:151]\n\t"
"v_cvt_pk_bf16_f32 v239, v226, v227\n\t"
"v_mfma_f32_16x16x32_bf16 v[232:235], v[232:235], a[208:211], 0\n\t"
"ds_write_b128 v243, v[236:239] offset:512\n\t"
"v_mfma_f32_16x16x32_bf16 v[228:231], v[228:231], a[208:211], 0\n\t"
"v_mfma_f32_16x16x32_bf16 a[156:159], a[216:219], a[192:195], a[156:159]\n\t"
"ds_read_b128 a[192:195], v244 offset:3072\n\t"
"v_mfma_f32_16x16x32_bf16 a[156:159], a[232:235], a[196:199], a[156:159]\n\t"
"ds_read_b64_tr_b16 v[224:225], v240 offset:40960\n\t"
"v_mfma_f32_16x16x32_bf16 a[156:159], a[220:223], a[200:203], a[156:159]\n\t"
"ds_read_b64_tr_b16 v[226:227], v240 offset:42496\n\t"
"v_mfma_f32_16x16x32_bf16 a[164:167], a[216:219], a[196:199], a[164:167]\n\t"
"buffer_load_dwordx4 a[216:219], v252, s[8:11], s20 offen offset:1024 sc1\n\t"
"v_mfma_f32_16x16x32_bf16 a[164:167], a[232:235], a[200:203], a[164:167]\n\t"
"s_waitcnt vmcnt(8) lgkmcnt(7)\n\t"
"ds_read_b64_tr_b16 v[236:237], v241 offset:40960\n\t"
"v_cvt_pk_bf16_f32 v232, v232, v233\n\t"
"v_mfma_f32_16x16x32_bf16 a[164:167], a[220:223], a[204:207], a[164:167]\n\t"
"ds_read_b64_tr_b16 v[238:239], v241 offset:42496\n\t"
"v_cvt_pk_bf16_f32 v233, v234, v235\n\t"
"Lst11_%=:\n\t"
"v_mfma_f32_16x16x32_bf16 v[0:3], a[236:239], a[168:171], v[0:3]\n\t"
"s_waitcnt lgkmcnt(7)\n\t"
"ds_read_b128 a[196:199], v244 offset:3584\n\t"
"v_cvt_pk_bf16_f32 v234, v228, v229\n\t"
"v_mfma_f32_16x16x32_bf16 v[0:3], a[224:227], a[172:175], v[0:3]\n\t"
"Lpg8_%=:\n\t"
"s_cbranch_vccnz Lpg12_%=\n\t"
"ds_read_b128 a[200:203], v244 offset:4096\n\t"
"v_cvt_pk_bf16_f32 v235, v230, v231\n\t"
"v_mfma_f32_16x16x32_bf16 v[0:3], a[240:243], a[176:179], v[0:3]\n\t"
"ds_write_b128 v243, v[232:235] offset:9728\n\t"
"v_mfma_f32_16x16x32_bf16 v[4:7], a[236:239], a[172:175], v[4:7]\n\t"
"buffer_load_dwordx4 a[220:223], v252, s[8:11], s20 offen offset:2048 sc1\n\t"
"v_mfma_f32_16x16x32_bf16 v[4:7], a[224:227], a[176:179], v[4:7]\n\t"
"s_mov_b32 s20, 1265664\n\t"
"ds_read_b128 a[204:207], v244 offset:4608\n\t"
"v_mfma_f32_16x16x32_bf16 v[4:7], a[240:243], a[180:183], v[4:7]\n\t"
"buffer_load_dwordx4 a[232:235], v252, s[8:11], s20 offen offset:0 sc1\n\t"
"v_mfma_f32_16x16x32_bf16 v[8:11], a[236:239], a[176:179], v[8:11]\n\t"
"s_waitcnt lgkmcnt(6)\n\t"
"v_add_u32 v240, 59904, v240\n\t"
"v_add_u32 v241, 59904, v241\n\t"
"v_mfma_f32_16x16x32_bf16 v[8:11], a[224:227], a[180:183], v[8:11]\n\t"
"s_waitcnt lgkmcnt(4)\n\t"
"ds_read_b64_tr_b16 v[228:229], v240 offset:1024\n\t"
"v_mfma_f32_16x16x32_bf16 v[8:11], a[240:243], a[184:187], v[8:11]\n\t"
"ds_read_b64_tr_b16 v[230:231], v240 offset:2560\n\t"
"v_mfma_f32_16x16x32_bf16 v[224:227], v[224:227], a[208:211], 0\n\t"
"ds_read_b64_tr_b16 v[232:233], v241 offset:1024\n\t"
"v_mfma_f32_16x16x32_bf16 v[236:239], v[236:239], a[208:211], 0\n\t"
"ds_read_b64_tr_b16 v[234:235], v241 offset:2560\n\t"
"v_mfma_f32_16x16x32_bf16 v[12:15], a[236:239], a[180:183], v[12:15]\n\t"
"v_mfma_f32_16x16x32_bf16 v[12:15], a[224:227], a[184:187], v[12:15]\n\t"
"v_mfma_f32_16x16x32_bf16 v[12:15], a[240:243], a[188:191], v[12:15]\n\t"
"v_mfma_f32_16x16x32_bf16 v[16:19], a[236:239], a[184:187], v[16:19]\n\t"
"s_waitcnt lgkmcnt(2)\n\t"
"v_cvt_pk_bf16_f32 v224, v224, v225\n\t"
"v_cvt_pk_bf16_f32 v225, v226, v227\n\t"
"v_mfma_f32_16x16x32_bf16 v[16:19], a[224:227], a[188:191], v[16:19]\n\t"
"s_waitcnt lgkmcnt(0)\n\t"
"v_cvt_pk_bf16_f32 v226, v236, v237\n\t"
"v_cvt_pk_bf16_f32 v227, v238, v239\n\t"
"v_mfma_f32_16x16x32_bf16 v[16:19], a[240:243], a[192:195], v[16:19]\n\t"
"ds_write_b128 v243, v[224:227] offset:18944\n\t"
"v_mfma_f32_16x16x32_bf16 v[228:231], v[228:231], a[208:211], 0\n\t"
"v_mfma_f32_16x16x32_bf16 v[232:235], v[232:235], a[208:211], 0\n\t"
"ds_read_b64_tr_b16 v[236:237], v240 offset:20992\n\t"
"v_mfma_f32_16x16x32_bf16 v[20:23], a[236:239], a[188:191], v[20:23]\n\t"
"ds_read_b64_tr_b16 v[238:239], v240 offset:22528\n\t"
"v_mfma_f32_16x16x32_bf16 v[20:23], a[224:227], a[192:195], v[20:23]\n\t"
"ds_read_b64_tr_b16 v[224:225], v241 offset:20992\n\t"
"v_mfma_f32_16x16x32_bf16 v[20:23], a[240:243], a[196:199], v[20:23]\n\t"
"ds_read_b64_tr_b16 v[226:227], v241 offset:22528\n\t"
"v_mfma_f32_16x16x32_bf16 v[24:27], a[236:239], a[192:195], v[24:27]\n\t"
"v_cvt_pk_bf16_f32 v228, v228, v229\n\t"
"v_cvt_pk_bf16_f32 v229, v230, v231\n\t"
"v_mfma_f32_16x16x32_bf16 v[24:27], a[224:227], a[196:199], v[24:27]\n\t"
"v_cvt_pk_bf16_f32 v230, v232, v233\n\t"
"v_cvt_pk_bf16_f32 v231, v234, v235\n\t"
"v_mfma_f32_16x16x32_bf16 v[24:27], a[240:243], a[200:203], v[24:27]\n\t"
"ds_write_b128 v243, v[228:231] offset:28160\n\t"
"v_mfma_f32_16x16x32_bf16 v[28:31], a[236:239], a[196:199], v[28:31]\n\t"
"buffer_load_dwordx4 a[236:239], v252, s[8:11], s20 offen offset:1024 sc1\n\t"
"v_mfma_f32_16x16x32_bf16 v[28:31], a[224:227], a[200:203], v[28:31]\n\t"
"s_waitcnt vmcnt(8)\n\t"
"ds_read_b64_tr_b16 v[232:233], v240 offset:40960\n\t"
"v_mfma_f32_16x16x32_bf16 v[28:31], a[240:243], a[204:207], v[28:31]\n\t"
"ds_read_b64_tr_b16 v[234:235], v240 offset:42496\n\t"
"v_mfma_f32_16x16x32_bf16 v[32:35], a[244:247], a[168:171], v[32:35]\n\t"
"buffer_load_dwordx4 a[224:227], v252, s[8:11], s20 offen offset:2048 sc1\n\t"
"v_mfma_f32_16x16x32_bf16 v[32:35], a[248:251], a[172:175], v[32:35]\n\t"
"s_mov_b32 s20, 1277952\n\t"
"ds_read_b64_tr_b16 v[228:229], v241 offset:40960\n\t"
"v_mfma_f32_16x16x32_bf16 v[32:35], v[248:251], a[176:179], v[32:35]\n\t"
"ds_read_b64_tr_b16 v[230:231], v241 offset:42496\n\t"
"v_mfma_f32_16x16x32_bf16 v[36:39], a[244:247], a[172:175], v[36:39]\n\t"
"buffer_load_dwordx4 a[240:243], v252, s[8:11], s20 offen offset:0 sc1\n\t"
"v_mfma_f32_16x16x32_bf16 v[36:39], a[248:251], a[176:179], v[36:39]\n\t"
"s_waitcnt lgkmcnt(5)\n\t"
"v_mfma_f32_16x16x32_bf16 v[36:39], v[248:251], a[180:183], v[36:39]\n\t"
"v_mfma_f32_16x16x32_bf16 v[236:239], v[236:239], a[208:211], 0\n\t"
"v_mfma_f32_16x16x32_bf16 v[224:227], v[224:227], a[208:211], 0\n\t"
"v_mfma_f32_16x16x32_bf16 v[40:43], a[244:247], a[176:179], v[40:43]\n\t"
"v_mfma_f32_16x16x32_bf16 v[40:43], a[248:251], a[180:183], v[40:43]\n\t"
"v_mfma_f32_16x16x32_bf16 v[40:43], v[248:251], a[184:187], v[40:43]\n\t"
"v_mfma_f32_16x16x32_bf16 v[44:47], a[244:247], a[180:183], v[44:47]\n\t"
"s_waitcnt lgkmcnt(2)\n\t"
"v_cvt_pk_bf16_f32 v236, v236, v237\n\t"
"v_cvt_pk_bf16_f32 v237, v238, v239\n\t"
"v_mfma_f32_16x16x32_bf16 v[44:47], a[248:251], a[184:187], v[44:47]\n\t"
"s_waitcnt lgkmcnt(0)\n\t"
"v_cvt_pk_bf16_f32 v238, v224, v225\n\t"
"v_cvt_pk_bf16_f32 v239, v226, v227\n\t"
"v_mfma_f32_16x16x32_bf16 v[44:47], v[248:251], a[188:191], v[44:47]\n\t"
"ds_write_b128 v243, v[236:239] offset:37376\n\t"
"v_mfma_f32_16x16x32_bf16 v[232:235], v[232:235], a[208:211], 0\n\t"
"v_mfma_f32_16x16x32_bf16 v[228:231], v[228:231], a[208:211], 0\n\t"
"v_mfma_f32_16x16x32_bf16 v[48:51], a[244:247], a[184:187], v[48:51]\n\t"
"buffer_load_dwordx4 a[208:211], v252, s[8:11], s20 offen offset:1024 sc1\n\t"
"v_mfma_f32_16x16x32_bf16 v[48:51], a[248:251], a[188:191], v[48:51]\n\t"
"v_mfma_f32_16x16x32_bf16 v[48:51], v[248:251], a[192:195], v[48:51]\n\t"
"v_mfma_f32_16x16x32_bf16 v[52:55], a[244:247], a[188:191], v[52:55]\n\t"
"v_cvt_pk_bf16_f32 v232, v232, v233\n\t"
"v_cvt_pk_bf16_f32 v233, v234, v235\n\t"
"v_mfma_f32_16x16x32_bf16 v[52:55], a[248:251], a[192:195], v[52:55]\n\t"
"v_cvt_pk_bf16_f32 v234, v228, v229\n\t"
"v_cvt_pk_bf16_f32 v235, v230, v231\n\t"
"v_mfma_f32_16x16x32_bf16 v[52:55], v[248:251], a[196:199], v[52:55]\n\t"
"ds_write_b128 v243, v[232:235] offset:46592\n\t"
"v_mfma_f32_16x16x32_bf16 v[56:59], a[244:247], a[192:195], v[56:59]\n\t"
"v_mfma_f32_16x16x32_bf16 v[56:59], a[248:251], a[196:199], v[56:59]\n\t"
"ds_read_b64_tr_b16 v[224:225], v246 offset:1024\n\t"
"v_mfma_f32_16x16x32_bf16 v[56:59], v[248:251], a[200:203], v[56:59]\n\t"
"ds_read_b64_tr_b16 v[226:227], v246 offset:4096\n\t"
"v_mfma_f32_16x16x32_bf16 v[60:63], a[244:247], a[196:199], v[60:63]\n\t"
"buffer_load_dwordx4 a[244:247], v252, s[8:11], s20 offen offset:2048 sc1\n\t"
"v_mfma_f32_16x16x32_bf16 v[60:63], a[248:251], a[200:203], v[60:63]\n\t"
"s_waitcnt vmcnt(9)\n\t"
"ds_read_b64_tr_b16 v[236:237], v246 offset:7168\n\t"
"s_mov_b32 s20, 1511424\n\t"
"v_mfma_f32_16x16x32_bf16 v[60:63], v[248:251], a[204:207], v[60:63]\n\t"
"ds_read_b64_tr_b16 v[238:239], v246 offset:10240\n\t"
"v_mfma_f32_16x16x32_bf16 v[64:67], a[252:255], a[168:171], v[64:67]\n\t"
"buffer_load_dwordx4 v[248:251], v252, s[8:11], s20 offen offset:0 sc1\n\t"
"v_mfma_f32_16x16x32_bf16 v[64:67], a[228:231], a[172:175], v[64:67]\n\t"
"ds_read_b64_tr_b16 v[228:229], v246 offset:13312\n\t"
"v_mfma_f32_16x16x32_bf16 v[64:67], v[96:99], a[176:179], v[64:67]\n\t"
"ds_read_b64_tr_b16 v[230:231], v246 offset:16384\n\t"
"v_mfma_f32_16x16x32_bf16 v[68:71], a[252:255], a[172:175], v[68:71]\n\t"
"buffer_load_dwordx4 v[240:243], v252, s[8:11], s20 offen offset:1024 sc1\n\t"
"v_mfma_f32_16x16x32_bf16 v[68:71], a[228:231], a[176:179], v[68:71]\n\t"
"ds_read_b128 a[168:171], v244 offset:9216\n\t"
"v_mfma_f32_16x16x32_bf16 v[68:71], v[96:99], a[180:183], v[68:71]\n\t"
"ds_read_b64_tr_b16 v[232:233], v246 offset:20992\n\t"
"v_mfma_f32_16x16x32_bf16 v[72:75], a[252:255], a[176:179], v[72:75]\n\t"
"buffer_load_dwordx4 a[248:251], v252, s[8:11], s20 offen offset:2048 sc1\n\t"
"v_mfma_f32_16x16x32_bf16 v[72:75], a[228:231], a[180:183], v[72:75]\n\t"
"s_waitcnt lgkmcnt(6)\n\t"
"ds_read_b64_tr_b16 v[234:235], v246 offset:24064\n\t"
"v_mfma_f32_16x16x32_bf16 v[72:75], v[96:99], a[184:187], v[72:75]\n\t"
"ds_read_b128 a[172:175], v244 offset:9728\n\t"
"v_mfma_f32_16x16x32_bf16 v[224:227], v[224:227], v[220:223], 0\n\t"
"ds_read_b128 a[176:179], v244 offset:10240\n\t"
"v_mfma_f32_16x16x32_bf16 v[76:79], a[252:255], a[180:183], v[76:79]\n\t"
"s_waitcnt lgkmcnt(7)\n\t"
"ds_read_b128 a[180:183], v244 offset:10752\n\t"
"v_mfma_f32_16x16x32_bf16 v[76:79], a[228:231], a[184:187], v[76:79]\n\t"
"v_mfma_f32_16x16x32_bf16 v[76:79], v[96:99], a[188:191], v[76:79]\n\t"
"v_mfma_f32_16x16x32_bf16 v[236:239], v[236:239], v[220:223], 0\n\t"
"v_mfma_f32_16x16x32_bf16 v[80:83], a[252:255], a[184:187], v[80:83]\n\t"
"s_waitcnt lgkmcnt(6)\n\t"
"ds_read_b128 a[184:187], v244 offset:11264\n\t"
"v_cvt_pk_bf16_f32 v224, v224, v225\n\t"
"v_mfma_f32_16x16x32_bf16 v[80:83], a[228:231], a[188:191], v[80:83]\n\t"
"v_cvt_pk_bf16_f32 v225, v226, v227\n\t"
"v_mfma_f32_16x16x32_bf16 v[80:83], v[96:99], a[192:195], v[80:83]\n\t"
"s_mov_b32 s20, 1523712\n\t"
"v_mfma_f32_16x16x32_bf16 v[228:231], v[228:231], v[220:223], 0\n\t"
"v_mfma_f32_16x16x32_bf16 v[84:87], a[252:255], a[188:191], v[84:87]\n\t"
"s_waitcnt lgkmcnt(4)\n\t"
"ds_read_b128 a[188:191], v244 offset:11776\n\t"
"v_cvt_pk_bf16_f32 v226, v236, v237\n\t"
"v_mfma_f32_16x16x32_bf16 v[84:87], a[228:231], a[192:195], v[84:87]\n\t"
"v_cvt_pk_bf16_f32 v227, v238, v239\n\t"
"v_mfma_f32_16x16x32_bf16 v[84:87], v[96:99], a[196:199], v[84:87]\n\t"
"v_mfma_f32_16x16x32_bf16 v[232:235], v[232:235], v[220:223], 0\n\t"
"v_mfma_f32_16x16x32_bf16 v[88:91], a[252:255], a[192:195], v[88:91]\n\t"
"buffer_load_dwordx4 v[236:239], v252, s[8:11], s20 offen offset:0 sc1\n\t"
"v_mfma_f32_16x16x32_bf16 v[88:91], a[228:231], a[196:199], v[88:91]\n\t"
"ds_read_b128 a[192:195], v244 offset:12288\n\t"
"v_cvt_pk_bf16_f32 v228, v228, v229\n\t"
"v_mfma_f32_16x16x32_bf16 v[88:91], v[96:99], a[200:203], v[88:91]\n\t"
"v_cvt_pk_bf16_f32 v229, v230, v231\n\t"
"v_mfma_f32_16x16x32_bf16 v[92:95], a[252:255], a[196:199], v[92:95]\n\t"
"buffer_load_dwordx4 a[252:255], v252, s[8:11], s20 offen offset:1024 sc1\n\t"
"v_mfma_f32_16x16x32_bf16 v[92:95], a[228:231], a[200:203], v[92:95]\n\t"
"s_waitcnt vmcnt(11) lgkmcnt(4)\n\t"
"ds_read_b128 a[196:199], v244 offset:12800\n\t"
"v_cvt_pk_bf16_f32 v230, v232, v233\n\t"
"v_mfma_f32_16x16x32_bf16 v[92:95], v[96:99], a[204:207], v[92:95]\n\t"
"ds_read_b128 a[200:203], v244 offset:13312\n\t"
"v_cvt_pk_bf16_f32 v231, v234, v235\n\t"
"v_mfma_f32_16x16x32_bf16 v[100:103], a[212:215], a[168:171], v[100:103]\n\t"
"ds_read_b64_tr_b16 v[232:233], v246 offset:27136\n\t"
"v_mfma_f32_16x16x32_bf16 v[100:103], a[216:219], a[172:175], v[100:103]\n\t"
"s_waitcnt lgkmcnt(6)\n\t"
"ds_read_b64_tr_b16 v[234:235], v246 offset:30208\n\t"
"v_mfma_f32_16x16x32_bf16 v[100:103], a[220:223], a[176:179], v[100:103]\n\t"
"s_waitcnt lgkmcnt(0)\n\t"
"ds_read_b128 a[204:207], v244 offset:13824\n\t"
"v_mfma_f32_16x16x32_bf16 v[232:235], v[232:235], v[220:223], 0\n\t"
"buffer_load_dwordx4 v[96:99], v252, s[8:11], s20 offen offset:2048 sc1\n\t"
"s_mov_b32 s20, 1536000\n\t"
"v_mfma_f32_16x16x32_bf16 v[104:107], a[212:215], a[172:175], v[104:107]\n\t"
"buffer_load_dwordx4 a[228:231], v252, s[8:11], s20 offen offset:0 sc1\n\t"
"v_mfma_f32_16x16x32_bf16 v[104:107], a[216:219], a[176:179], v[104:107]\n\t"
"v_mfma_f32_16x16x32_bf16 v[104:107], a[220:223], a[180:183], v[104:107]\n\t"
"s_waitcnt lgkmcnt(0)\n\t"
"s_barrier\n\t"
"v_mfma_f32_16x16x32_bf16 v[108:111], a[212:215], a[176:179], v[108:111]\n\t"
"v_mfma_f32_16x16x32_bf16 v[108:111], a[216:219], a[180:183], v[108:111]\n\t"
"v_cvt_pk_bf16_f32 v232, v232, v233\n\t"
"v_cvt_pk_bf16_f32 v233, v234, v235\n\t"
"v_mfma_f32_16x16x32_bf16 v[108:111], a[220:223], a[184:187], v[108:111]\n\t"
"ds_write2st64_b64 v245, v[224:225], v[226:227] offset0:0 offset1:1\n\t"
"v_mfma_f32_16x16x32_bf16 v[112:115], a[212:215], a[180:183], v[112:115]\n\t"
"v_mfma_f32_16x16x32_bf16 v[112:115], a[216:219], a[184:187], v[112:115]\n\t"
"ds_read_b64_tr_b16 v[224:225], v246 offset:33280\n\t"
"v_mfma_f32_16x16x32_bf16 v[112:115], a[220:223], a[188:191], v[112:115]\n\t"
"ds_write2st64_b64 v245, v[228:229], v[230:231] offset0:2 offset1:3\n\t"
"v_mfma_f32_16x16x32_bf16 v[116:119], a[212:215], a[184:187], v[116:119]\n\t"
"v_mfma_f32_16x16x32_bf16 v[116:119], a[216:219], a[188:191], v[116:119]\n\t"
"ds_read_b64_tr_b16 v[226:227], v246 offset:36352\n\t"
"v_mfma_f32_16x16x32_bf16 v[116:119], a[220:223], a[192:195], v[116:119]\n\t"
"ds_write_b64 v245, v[232:233] offset:2048\n\t"
"v_mfma_f32_16x16x32_bf16 v[120:123], a[212:215], a[188:191], v[120:123]\n\t"
"ds_read_b64_tr_b16 v[228:229], v246 offset:40960\n\t"
"v_mfma_f32_16x16x32_bf16 v[120:123], a[216:219], a[192:195], v[120:123]\n\t"
"ds_read_b64_tr_b16 v[230:231], v246 offset:44032\n\t"
"v_mfma_f32_16x16x32_bf16 v[120:123], a[220:223], a[196:199], v[120:123]\n\t"
"s_waitcnt lgkmcnt(3)\n\t"
"ds_read_b64_tr_b16 v[232:233], v246 offset:47104\n\t"
"v_mfma_f32_16x16x32_bf16 v[224:227], v[224:227], v[220:223], 0\n\t"
"ds_read_b64_tr_b16 v[234:235], v246 offset:50176\n\t"
"v_mfma_f32_16x16x32_bf16 v[124:127], a[212:215], a[192:195], v[124:127]\n\t"
"v_mfma_f32_16x16x32_bf16 v[124:127], a[216:219], a[196:199], v[124:127]\n\t"
"v_mfma_f32_16x16x32_bf16 v[124:127], a[220:223], a[200:203], v[124:127]\n\t"
"s_waitcnt lgkmcnt(2)\n\t"
"v_mfma_f32_16x16x32_bf16 v[228:231], v[228:231], v[220:223], 0\n\t"
"v_mfma_f32_16x16x32_bf16 v[128:131], a[212:215], a[196:199], v[128:131]\n\t"
"buffer_load_dwordx4 a[212:215], v252, s[8:11], s20 offen offset:1024 sc1\n\t"
"v_mfma_f32_16x16x32_bf16 v[128:131], a[216:219], a[200:203], v[128:131]\n\t"
"s_waitcnt vmcnt(11) lgkmcnt(0)\n\t"
"v_cvt_pk_bf16_f32 v224, v224, v225\n\t"
"v_cvt_pk_bf16_f32 v225, v226, v227\n\t"
"v_mfma_f32_16x16x32_bf16 v[128:131], a[220:223], a[204:207], v[128:131]\n\t"
"v_mfma_f32_16x16x32_bf16 v[232:235], v[232:235], v[220:223], 0\n\t"
"buffer_load_dwordx4 a[216:219], v252, s[8:11], s20 offen offset:2048 sc1\n\t"
"v_mfma_f32_16x16x32_bf16 v[132:135], a[232:235], a[168:171], v[132:135]\n\t"
"s_mov_b32 s20, 1548288\n\t"
"v_cvt_pk_bf16_f32 v226, v228, v229\n\t"
"v_cvt_pk_bf16_f32 v227, v230, v231\n\t"
"v_mfma_f32_16x16x32_bf16 v[132:135], a[236:239], a[172:175], v[132:135]\n\t"
"ds_read_b64_tr_b16 v[228:229], v246 offset:53248\n\t"
"v_mfma_f32_16x16x32_bf16 v[132:135], a[224:227], a[176:179], v[132:135]\n\t"
"buffer_load_dwordx4 a[220:223], v252, s[8:11], s20 offen offset:0 sc1\n\t"
"v_mfma_f32_16x16x32_bf16 v[136:139], a[232:235], a[172:175], v[136:139]\n\t"
"ds_read_b64_tr_b16 v[230:231], v246 offset:56320\n\t"
"v_mfma_f32_16x16x32_bf16 v[136:139], a[236:239], a[176:179], v[136:139]\n\t"
"v_cvt_pk_bf16_f32 v232, v232, v233\n\t"
"v_cvt_pk_bf16_f32 v233, v234, v235\n\t"
"v_mfma_f32_16x16x32_bf16 v[136:139], a[224:227], a[180:183], v[136:139]\n\t"
"ds_write2st64_b64 v245, v[224:225], v[226:227] offset0:5 offset1:6\n\t"
"v_mfma_f32_16x16x32_bf16 v[140:143], a[232:235], a[176:179], v[140:143]\n\t"
"v_mfma_f32_16x16x32_bf16 v[140:143], a[236:239], a[180:183], v[140:143]\n\t"
"v_mfma_f32_16x16x32_bf16 v[140:143], a[224:227], a[184:187], v[140:143]\n\t"
"v_mfma_f32_16x16x32_bf16 v[144:147], a[232:235], a[180:183], v[144:147]\n\t"
"v_mfma_f32_16x16x32_bf16 v[144:147], a[236:239], a[184:187], v[144:147]\n\t"
"v_mfma_f32_16x16x32_bf16 v[144:147], a[224:227], a[188:191], v[144:147]\n\t"
"v_mfma_f32_16x16x32_bf16 v[148:151], a[232:235], a[184:187], v[148:151]\n\t"
"s_waitcnt lgkmcnt(1)\n\t"
"v_mfma_f32_16x16x32_bf16 v[148:151], a[236:239], a[188:191], v[148:151]\n\t"
"v_mfma_f32_16x16x32_bf16 v[148:151], a[224:227], a[192:195], v[148:151]\n\t"
"v_mfma_f32_16x16x32_bf16 v[228:231], v[228:231], v[220:223], 0\n\t"
"v_mfma_f32_16x16x32_bf16 v[152:155], a[232:235], a[188:191], v[152:155]\n\t"
"v_mfma_f32_16x16x32_bf16 v[152:155], a[236:239], a[192:195], v[152:155]\n\t"
"v_mfma_f32_16x16x32_bf16 v[152:155], a[224:227], a[196:199], v[152:155]\n\t"
"v_mfma_f32_16x16x32_bf16 v[156:159], a[232:235], a[192:195], v[156:159]\n\t"
"v_mfma_f32_16x16x32_bf16 v[156:159], a[236:239], a[196:199], v[156:159]\n\t"
"v_cvt_pk_bf16_f32 v234, v228, v229\n\t"
"v_cvt_pk_bf16_f32 v235, v230, v231\n\t"
"v_mfma_f32_16x16x32_bf16 v[156:159], a[224:227], a[200:203], v[156:159]\n\t"
"ds_write2st64_b64 v245, v[232:233], v[234:235] offset0:7 offset1:8\n\t"
"v_mfma_f32_16x16x32_bf16 v[160:163], a[232:235], a[196:199], v[160:163]\n\t"
"buffer_load_dwordx4 a[232:235], v252, s[8:11], s20 offen offset:1024 sc1\n\t"
"v_mfma_f32_16x16x32_bf16 v[160:163], a[236:239], a[200:203], v[160:163]\n\t"
"s_waitcnt vmcnt(11)\n\t"
"v_mfma_f32_16x16x32_bf16 v[160:163], a[224:227], a[204:207], v[160:163]\n\t"
"v_mfma_f32_16x16x32_bf16 v[164:167], a[240:243], a[168:171], v[164:167]\n\t"
"buffer_load_dwordx4 a[224:227], v252, s[8:11], s20 offen offset:2048 sc1\n\t"
"v_mfma_f32_16x16x32_bf16 v[164:167], a[208:211], a[172:175], v[164:167]\n\t"
"s_mov_b32 s20, 1560576\n\t"
"v_mfma_f32_16x16x32_bf16 v[164:167], a[244:247], a[176:179], v[164:167]\n\t"
"s_waitcnt lgkmcnt(0)\n\t"
"s_barrier\n\t"
"v_mfma_f32_16x16x32_bf16 v[168:171], a[240:243], a[172:175], v[168:171]\n\t"
"ds_read_b128 a[168:171], v244 offset:0\n\t"
"v_mfma_f32_16x16x32_bf16 v[168:171], a[208:211], a[176:179], v[168:171]\n\t"
"buffer_load_dwordx4 a[236:239], v252, s[8:11], s20 offen offset:0 sc1\n\t"
"v_mfma_f32_16x16x32_bf16 v[168:171], a[244:247], a[180:183], v[168:171]\n\t"
"ds_read_b128 a[172:175], v244 offset:512\n\t"
"v_mfma_f32_16x16x32_bf16 v[172:175], a[240:243], a[176:179], v[172:175]\n\t"
"ds_read_b128 a[176:179], v244 offset:1024\n\t"
"v_mfma_f32_16x16x32_bf16 v[172:175], a[208:211], a[180:183], v[172:175]\n\t"
"v_mfma_f32_16x16x32_bf16 v[172:175], a[244:247], a[184:187], v[172:175]\n\t"
"v_mfma_f32_16x16x32_bf16 v[176:179], a[240:243], a[180:183], v[176:179]\n\t"
"ds_read_b128 a[180:183], v244 offset:1536\n\t"
"v_mfma_f32_16x16x32_bf16 v[176:179], a[208:211], a[184:187], v[176:179]\n\t"
"v_mfma_f32_16x16x32_bf16 v[176:179], a[244:247], a[188:191], v[176:179]\n\t"
"v_mfma_f32_16x16x32_bf16 v[180:183], a[240:243], a[184:187], v[180:183]\n\t"
"ds_read_b128 a[184:187], v244 offset:2048\n\t"
"v_mfma_f32_16x16x32_bf16 v[180:183], a[208:211], a[188:191], v[180:183]\n\t"
"v_mfma_f32_16x16x32_bf16 v[180:183], a[244:247], a[192:195], v[180:183]\n\t"
"v_mfma_f32_16x16x32_bf16 v[184:187], a[240:243], a[188:191], v[184:187]\n\t"
"ds_read_b128 a[188:191], v244 offset:2560\n\t"
"v_mfma_f32_16x16x32_bf16 v[184:187], a[208:211], a[192:195], v[184:187]\n\t"
"v_mfma_f32_16x16x32_bf16 v[184:187], a[244:247], a[196:199], v[184:187]\n\t"
"v_mfma_f32_16x16x32_bf16 v[188:191], a[240:243], a[192:195], v[188:191]\n\t"
"ds_read_b128 a[192:195], v244 offset:3072\n\t"
"v_mfma_f32_16x16x32_bf16 v[188:191], a[208:211], a[196:199], v[188:191]\n\t"
"v_mfma_f32_16x16x32_bf16 v[188:191], a[244:247], a[200:203], v[188:191]\n\t"
"v_mfma_f32_16x16x32_bf16 v[192:195], a[240:243], a[196:199], v[192:195]\n\t"
"buffer_load_dwordx4 a[240:243], v252, s[8:11], s20 offen offset:1024 sc1\n\t"
"v_mfma_f32_16x16x32_bf16 v[192:195], a[208:211], a[200:203], v[192:195]\n\t"
"s_waitcnt vmcnt(11) lgkmcnt(4)\n\t"
"ds_read_b128 a[196:199], v244 offset:3584\n\t"
"v_mfma_f32_16x16x32_bf16 v[192:195], a[244:247], a[204:207], v[192:195]\n\t"
"ds_read_b128 a[200:203], v244 offset:4096\n\t"
"Lst12_%=:\n\t"
"v_mfma_f32_16x16x32_bf16 v[0:3], v[248:251], a[168:171], v[0:3]\n\t"
"buffer_load_dwordx4 a[208:211], v252, s[8:11], s20 offen offset:2048 sc1\n\t"
"v_mfma_f32_16x16x32_bf16 v[0:3], v[240:243], a[172:175], v[0:3]\n\t"
"s_waitcnt lgkmcnt(5)\n\t"
"ds_read_b128 a[204:207], v244 offset:4608\n\t"
"v_xor_b32 v246, 0xc0, v246\n\t"
"v_mfma_f32_16x16x32_bf16 v[0:3], a[248:251], a[176:179], v[0:3]\n\t"
"s_mov_b32 s20, 1572864\n\t"
"v_mfma_f32_16x16x32_bf16 v[4:7], v[248:251], a[172:175], v[4:7]\n\t"
"buffer_load_dwordx4 a[244:247], v252, s[8:11], s20 offen offset:0 sc1\n\t"
"v_mfma_f32_16x16x32_bf16 v[4:7], v[240:243], a[176:179], v[4:7]\n\t"
"s_waitcnt lgkmcnt(5)\n\t"
"v_mfma_f32_16x16x32_bf16 v[4:7], a[248:251], a[180:183], v[4:7]\n\t"
"v_mfma_f32_16x16x32_bf16 v[8:11], v[248:251], a[176:179], v[8:11]\n\t"
"s_waitcnt lgkmcnt(4)\n\t"
"v_mfma_f32_16x16x32_bf16 v[8:11], v[240:243], a[180:183], v[8:11]\n\t"
"v_mfma_f32_16x16x32_bf16 v[8:11], a[248:251], a[184:187], v[8:11]\n\t"
"v_mfma_f32_16x16x32_bf16 v[12:15], v[248:251], a[180:183], v[12:15]\n\t"
"s_waitcnt lgkmcnt(3)\n\t"
"v_mfma_f32_16x16x32_bf16 v[12:15], v[240:243], a[184:187], v[12:15]\n\t"
"v_mfma_f32_16x16x32_bf16 v[12:15], a[248:251], a[188:191], v[12:15]\n\t"
"v_mfma_f32_16x16x32_bf16 v[16:19], v[248:251], a[184:187], v[16:19]\n\t"
"s_waitcnt lgkmcnt(2)\n\t"
"v_mfma_f32_16x16x32_bf16 v[16:19], v[240:243], a[188:191], v[16:19]\n\t"
"v_mfma_f32_16x16x32_bf16 v[16:19], a[248:251], a[192:195], v[16:19]\n\t"
"ds_read_b64_tr_b16 v[224:225], v246 offset:1024\n\t"
"v_mfma_f32_16x16x32_bf16 v[20:23], v[248:251], a[188:191], v[20:23]\n\t"
"s_waitcnt lgkmcnt(2)\n\t"
"ds_read_b64_tr_b16 v[226:227], v246 offset:4096\n\t"
"v_mfma_f32_16x16x32_bf16 v[20:23], v[240:243], a[192:195], v[20:23]\n\t"
"v_mfma_f32_16x16x32_bf16 v[20:23], a[248:251], a[196:199], v[20:23]\n\t"
"v_mfma_f32_16x16x32_bf16 v[24:27], v[248:251], a[192:195], v[24:27]\n\t"
"s_waitcnt lgkmcnt(2)\n\t"
"v_mfma_f32_16x16x32_bf16 v[24:27], v[240:243], a[196:199], v[24:27]\n\t"
"v_mfma_f32_16x16x32_bf16 v[24:27], a[248:251], a[200:203], v[24:27]\n\t"
"v_mfma_f32_16x16x32_bf16 v[28:31], v[248:251], a[196:199], v[28:31]\n\t"
"buffer_load_dwordx4 v[248:251], v252, s[8:11], s20 offen offset:1024 sc1\n\t"
"v_mfma_f32_16x16x32_bf16 v[28:31], v[240:243], a[200:203], v[28:31]\n\t"
"s_waitcnt vmcnt(11)\n\t"
"ds_read_b64_tr_b16 v[228:229], v246 offset:7168\n\t"
"v_mfma_f32_16x16x32_bf16 v[28:31], a[248:251], a[204:207], v[28:31]\n\t"
"s_add_u32 s21, s21, 6291456\n\t"
"v_add_u32 v254, 6291456, v254\n\t"
"ds_read_b64_tr_b16 v[230:231], v246 offset:10240\n\t"
"v_mfma_f32_16x16x32_bf16 v[32:35], v[236:239], a[168:171], v[32:35]\n\t"
"buffer_load_dwordx4 v[240:243], v252, s[8:11], s20 offen offset:2048 sc1\n\t"
"v_mfma_f32_16x16x32_bf16 v[32:35], a[252:255], a[172:175], v[32:35]\n\t"
"s_mov_b32 s20, 1585152\n\t"
"v_mfma_f32_16x16x32_bf16 v[32:35], v[96:99], a[176:179], v[32:35]\n\t"
"v_mfma_f32_16x16x32_bf16 v[36:39], v[236:239], a[172:175], v[36:39]\n\t"
"buffer_load_dwordx4 a[248:251], v252, s[8:11], s20 offen offset:0 sc1\n\t"
"v_mfma_f32_16x16x32_bf16 v[36:39], a[252:255], a[176:179], v[36:39]\n\t"
"s_waitcnt lgkmcnt(2)\n\t"
"v_mfma_f32_16x16x32_bf16 v[36:39], v[96:99], a[180:183], v[36:39]\n\t"
"v_mfma_f32_16x16x32_bf16 v[224:227], v[224:227], v[220:223], 0\n\t"
"v_mfma_f32_16x16x32_bf16 v[40:43], v[236:239], a[176:179], v[40:43]\n\t"
"ds_read_b64_tr_b16 v[232:233], v246 offset:13312\n\t"
"v_mfma_f32_16x16x32_bf16 v[40:43], a[252:255], a[180:183], v[40:43]\n\t"
"ds_read_b64_tr_b16 v[234:235], v246 offset:16384\n\t"
"v_mfma_f32_16x16x32_bf16 v[40:43], v[96:99], a[184:187], v[40:43]\n\t"
"v_mfma_f32_16x16x32_bf16 v[44:47], v[236:239], a[180:183], v[44:47]\n\t"
"s_waitcnt lgkmcnt(2)\n\t"
"v_mfma_f32_16x16x32_bf16 v[44:47], a[252:255], a[184:187], v[44:47]\n\t"
"v_cvt_pk_bf16_f32 v224, v224, v225\n\t"
"v_cvt_pk_bf16_f32 v225, v226, v227\n\t"
"v_mfma_f32_16x16x32_bf16 v[44:47], v[96:99], a[188:191], v[44:47]\n\t"
"v_mfma_f32_16x16x32_bf16 v[228:231], v[228:231], v[220:223], 0\n\t"
"v_mfma_f32_16x16x32_bf16 v[48:51], v[236:239], a[184:187], v[48:51]\n\t"
"v_mfma_f32_16x16x32_bf16 v[48:51], a[252:255], a[188:191], v[48:51]\n\t"
"v_mfma_f32_16x16x32_bf16 v[48:51], v[96:99], a[192:195], v[48:51]\n\t"
"v_mfma_f32_16x16x32_bf16 v[52:55], v[236:239], a[188:191], v[52:55]\n\t"
"v_mfma_f32_16x16x32_bf16 v[52:55], a[252:255], a[192:195], v[52:55]\n\t"
"v_cvt_pk_bf16_f32 v226, v228, v229\n\t"
"v_cvt_pk_bf16_f32 v227, v230, v231\n\t"
"v_mfma_f32_16x16x32_bf16 v[52:55], v[96:99], a[196:199], v[52:55]\n\t"
"ds_read_b64_tr_b16 v[228:229], v246 offset:20992\n\t"
"v_mfma_f32_16x16x32_bf16 v[56:59], v[236:239], a[192:195], v[56:59]\n\t"
"s_waitcnt lgkmcnt(1)\n\t"
"ds_read_b64_tr_b16 v[230:231], v246 offset:24064\n\t"
"v_mfma_f32_16x16x32_bf16 v[56:59], a[252:255], a[196:199], v[56:59]\n\t"
"v_mfma_f32_16x16x32_bf16 v[56:59], v[96:99], a[200:203], v[56:59]\n\t"
"v_mfma_f32_16x16x32_bf16 v[232:235], v[232:235], v[220:223], 0\n\t"
"v_mfma_f32_16x16x32_bf16 v[60:63], v[236:239], a[196:199], v[60:63]\n\t"
"buffer_load_dwordx4 v[236:239], v252, s[8:11], s20 offen offset:1024 sc1\n\t"
"v_mfma_f32_16x16x32_bf16 v[60:63], a[252:255], a[200:203], v[60:63]\n\t"
"s_waitcnt vmcnt(11)\n\t"
"v_mfma_f32_16x16x32_bf16 v[60:63], v[96:99], a[204:207], v[60:63]\n\t"
"v_mfma_f32_16x16x32_bf16 v[64:67], a[228:231], a[168:171], v[64:67]\n\t"
"buffer_load_dwordx4 v[96:99], v252, s[8:11], s20 offen offset:2048 sc1\n\t"
"v_mfma_f32_16x16x32_bf16 v[64:67], a[212:215], a[172:175], v[64:67]\n\t"
"s_mov_b32 s20, 1597440\n\t"
"ds_read_b128 a[168:171], v244 offset:9216\n\t"
"v_cvt_pk_bf16_f32 v232, v232, v233\n\t"
"v_mfma_f32_16x16x32_bf16 v[64:67], a[216:219], a[176:179], v[64:67]\n\t"
"v_cvt_pk_bf16_f32 v233, v234, v235\n\t"
"v_mfma_f32_16x16x32_bf16 v[68:71], a[228:231], a[172:175], v[68:71]\n\t"
"buffer_load_dwordx4 a[252:255], v252, s[8:11], s20 offen offset:0 sc1\n\t"
"v_mfma_f32_16x16x32_bf16 v[68:71], a[212:215], a[176:179], v[68:71]\n\t"
"s_waitcnt lgkmcnt(1)\n\t"
"ds_read_b128 a[172:175], v244 offset:9728\n\t"
"v_mfma_f32_16x16x32_bf16 v[68:71], a[216:219], a[180:183], v[68:71]\n\t"
"v_mfma_f32_16x16x32_bf16 v[228:231], v[228:231], v[220:223], 0\n\t"
"v_mfma_f32_16x16x32_bf16 v[72:75], a[228:231], a[176:179], v[72:75]\n\t"
"ds_read_b128 a[176:179], v244 offset:10240\n\t"
"v_mfma_f32_16x16x32_bf16 v[72:75], a[212:215], a[180:183], v[72:75]\n\t"
"v_mfma_f32_16x16x32_bf16 v[72:75], a[216:219], a[184:187], v[72:75]\n\t"
"v_mfma_f32_16x16x32_bf16 v[76:79], a[228:231], a[180:183], v[76:79]\n\t"
"ds_read_b128 a[180:183], v244 offset:10752\n\t"
"v_mfma_f32_16x16x32_bf16 v[76:79], a[212:215], a[184:187], v[76:79]\n\t"
"v_cvt_pk_bf16_f32 v234, v228, v229\n\t"
"v_cvt_pk_bf16_f32 v235, v230, v231\n\t"
"v_mfma_f32_16x16x32_bf16 v[76:79], a[216:219], a[188:191], v[76:79]\n\t"
"ds_read_b64_tr_b16 v[228:229], v246 offset:27136\n\t"
"v_mfma_f32_16x16x32_bf16 v[80:83], a[228:231], a[184:187], v[80:83]\n\t"
"ds_read_b64_tr_b16 v[230:231], v246 offset:30208\n\t"
"v_mfma_f32_16x16x32_bf16 v[80:83], a[212:215], a[188:191], v[80:83]\n\t"
"ds_read_b128 a[184:187], v244 offset:11264\n\t"
"v_mfma_f32_16x16x32_bf16 v[80:83], a[216:219], a[192:195], v[80:83]\n\t"
"v_mfma_f32_16x16x32_bf16 v[84:87], a[228:231], a[188:191], v[84:87]\n\t"
"ds_read_b128 a[188:191], v244 offset:11776\n\t"
"v_mfma_f32_16x16x32_bf16 v[84:87], a[212:215], a[192:195], v[84:87]\n\t"
"v_mfma_f32_16x16x32_bf16 v[84:87], a[216:219], a[196:199], v[84:87]\n\t"
"v_mfma_f32_16x16x32_bf16 v[88:91], a[228:231], a[192:195], v[88:91]\n\t"
"ds_read_b128 a[192:195], v244 offset:12288\n\t"
"v_mfma_f32_16x16x32_bf16 v[88:91], a[212:215], a[196:199], v[88:91]\n\t"
"v_mfma_f32_16x16x32_bf16 v[88:91], a[216:219], a[200:203], v[88:91]\n\t"
"v_mfma_f32_16x16x32_bf16 v[92:95], a[228:231], a[196:199], v[92:95]\n\t"
"buffer_load_dwordx4 a[228:231], v252, s[8:11], s20 offen offset:1024 sc1\n\t"
"v_mfma_f32_16x16x32_bf16 v[92:95], a[212:215], a[200:203], v[92:95]\n\t"
"s_waitcnt vmcnt(11) lgkmcnt(3)\n\t"
"ds_read_b128 a[196:199], v244 offset:12800\n\t"
"v_mfma_f32_16x16x32_bf16 v[92:95], a[216:219], a[204:207], v[92:95]\n\t"
"ds_read_b128 a[200:203], v244 offset:13312\n\t"
"v_mfma_f32_16x16x32_bf16 v[228:231], v[228:231], v[220:223], 0\n\t"
"ds_read_b128 a[204:207], v244 offset:13824\n\t"
"v_mfma_f32_16x16x32_bf16 v[100:103], a[220:223], a[168:171], v[100:103]\n\t"
"buffer_load_dwordx4 a[212:215], v252, s[8:11], s20 offen offset:2048 sc1\n\t"
"v_mfma_f32_16x16x32_bf16 v[100:103], a[232:235], a[172:175], v[100:103]\n\t"
"s_mov_b32 s20, 1609728\n\t"
"v_mfma_f32_16x16x32_bf16 v[100:103], a[224:227], a[176:179], v[100:103]\n\t"
"buffer_load_dwordx4 a[216:219], v252, s[8:11], s20 offen offset:0 sc1\n\t"
"v_mfma_f32_16x16x32_bf16 v[104:107], a[220:223], a[172:175], v[104:107]\n\t"
"s_waitcnt lgkmcnt(5)\n\t"
"s_add_u32 m0, s26, 512\n\t"
"v_mfma_f32_16x16x32_bf16 v[104:107], a[232:235], a[176:179], v[104:107]\n\t"
"v_cvt_pk_bf16_f32 v228, v228, v229\n\t"
"v_mfma_f32_16x16x32_bf16 v[104:107], a[224:227], a[180:183], v[104:107]\n\t"
"s_waitcnt lgkmcnt(0)\n\t"
"s_barrier\n\t"
"v_mfma_f32_16x16x32_bf16 v[108:111], a[220:223], a[176:179], v[108:111]\n\t"
"v_cvt_pk_bf16_f32 v229, v230, v231\n\t"
"v_mfma_f32_16x16x32_bf16 v[108:111], a[232:235], a[180:183], v[108:111]\n\t"
"v_fma_f32 v0, v100, s52, v0\n\t"
"v_fma_f32 v1, v101, s52, v1\n\t"
"v_mfma_f32_16x16x32_bf16 v[108:111], a[224:227], a[184:187], v[108:111]\n\t"
"s_mov_b64 exec, s[36:37]\n\t"
"buffer_load_dwordx4 v254, s[12:15], s27 offen offset:0 nt lds\n\t"
"s_mov_b64 exec, -1\n\t"
"s_add_u32 m0, s24, 80384\n\t"
"s_add_u32 s28, s21, 2359296\n\t"
"v_mfma_f32_16x16x32_bf16 v[112:115], a[220:223], a[180:183], v[112:115]\n\t"
"buffer_load_dwordx4 v247, s[12:15], s28 offen offset:0 nt lds\n\t"
"v_mfma_f32_16x16x32_bf16 v[112:115], a[232:235], a[184:187], v[112:115]\n\t"
"s_add_u32 m0, s24, 86528\n\t"
"s_add_u32 s28, s21, 2555904\n\t"
"v_fma_f32 v2, v102, s52, v2\n\t"
"v_mfma_f32_16x16x32_bf16 v[112:115], a[224:227], a[188:191], v[112:115]\n\t"
"ds_write2st64_b64 v245, v[224:225], v[226:227] offset0:0 offset1:1\n\t"
"v_mfma_f32_16x16x32_bf16 v[116:119], a[220:223], a[184:187], v[116:119]\n\t"
"buffer_load_dwordx4 v247, s[12:15], s28 offen offset:0 nt lds\n\t"
"v_mfma_f32_16x16x32_bf16 v[116:119], a[232:235], a[188:191], v[116:119]\n\t"
"s_add_u32 m0, s24, 60416\n\t"
"s_add_u32 s28, s21, 1769472\n\t"
"ds_read_b64_tr_b16 v[224:225], v246 offset:33280\n\t"
"v_mfma_f32_16x16x32_bf16 v[116:119], a[224:227], a[192:195], v[116:119]\n\t"
"ds_write2st64_b64 v245, v[232:233], v[234:235] offset0:2 offset1:3\n\t"
"ds_read_b64_tr_b16 v[226:227], v246 offset:36352\n\t"
"v_mfma_f32_16x16x32_bf16 v[120:123], a[220:223], a[188:191], v[120:123]\n\t"
"buffer_load_dwordx4 v247, s[12:15], s28 offen offset:0 nt lds\n\t"
"v_mfma_f32_16x16x32_bf16 v[120:123], a[232:235], a[192:195], v[120:123]\n\t"
"s_add_u32 m0, s24, 66560\n\t"
"s_add_u32 s28, s21, 1966080\n\t"
"ds_read_b64_tr_b16 v[232:233], v246 offset:40960\n\t"
"v_mfma_f32_16x16x32_bf16 v[120:123], a[224:227], a[196:199], v[120:123]\n\t"
"s_waitcnt lgkmcnt(1)\n\t"
"ds_write_b64 v245, v[228:229] offset:2048\n\t"
"ds_read_b64_tr_b16 v[234:235], v246 offset:44032\n\t"
"v_mfma_f32_16x16x32_bf16 v[224:227], v[224:227], v[220:223], 0\n\t"
"buffer_load_dwordx4 v247, s[12:15], s28 offen offset:0 nt lds\n\t"
"v_mfma_f32_16x16x32_bf16 v[124:127], a[220:223], a[192:195], v[124:127]\n\t"
"ds_read_b64_tr_b16 v[228:229], v246 offset:47104\n\t"
"v_fma_f32 v3, v103, s52, v3\n\t"
"v_mfma_f32_16x16x32_bf16 v[124:127], a[232:235], a[196:199], v[124:127]\n\t"
"ds_read_b64_tr_b16 v[230:231], v246 offset:50176\n\t"
"v_fma_f32 v100, v0, s54, -v100\n\t"
"v_mfma_f32_16x16x32_bf16 v[124:127], a[224:227], a[200:203], v[124:127]\n\t"
"s_waitcnt lgkmcnt(2)\n\t"
"v_fma_f32 v101, v1, s54, -v101\n\t"
"v_fma_f32 v102, v2, s54, -v102\n\t"
"v_mfma_f32_16x16x32_bf16 v[232:235], v[232:235], v[220:223], 0\n\t"
"v_fma_f32 v103, v3, s54, -v103\n\t"
"v_fma_f32 v4, v104, s52, v4\n\t"
"v_mfma_f32_16x16x32_bf16 v[128:131], a[220:223], a[196:199], v[128:131]\n\t"
"v_cvt_pk_bf16_f32 v224, v224, v225\n\t"
"v_cvt_pk_bf16_f32 v225, v226, v227\n\t"
"v_mfma_f32_16x16x32_bf16 v[128:131], a[232:235], a[200:203], v[128:131]\n\t"
"s_waitcnt vmcnt(15)\n\t"
"v_fma_f32 v5, v105, s52, v5\n\t"
"v_fma_f32 v6, v106, s52, v6\n\t"
"v_mfma_f32_16x16x32_bf16 v[128:131], a[224:227], a[204:207], v[128:131]\n\t"
"s_waitcnt lgkmcnt(0)\n\t"
"v_fma_f32 v7, v107, s52, v7\n\t"
"v_fma_f32 v104, v4, s54, -v104\n\t"
"v_mfma_f32_16x16x32_bf16 v[228:231], v[228:231], v[220:223], 0\n\t"
"v_fma_f32 v105, v5, s54, -v105\n\t"
"v_fma_f32 v106, v6, s54, -v106\n\t"
"v_mfma_f32_16x16x32_bf16 v[132:135], a[236:239], a[168:171], v[132:135]\n\t"
"v_cvt_pk_bf16_f32 v226, v232, v233\n\t"
"v_cvt_pk_bf16_f32 v227, v234, v235\n\t"
"v_mfma_f32_16x16x32_bf16 v[132:135], a[240:243], a[172:175], v[132:135]\n\t"
"ds_read_b64_tr_b16 v[232:233], v246 offset:53248\n\t"
"v_fma_f32 v107, v7, s54, -v107\n\t"
"v_mfma_f32_16x16x32_bf16 v[132:135], a[208:211], a[176:179], v[132:135]\n\t"
"ds_write2st64_b64 v245, v[224:225], v[226:227] offset0:5 offset1:6\n\t"
"v_mfma_f32_16x16x32_bf16 v[136:139], a[236:239], a[172:175], v[136:139]\n\t"
"Lpg9_%=:\n\t"
"s_cbranch_vccnz Lvr0_%=\n\t"
"v_fma_f32 v8, v108, s52, v8\n\t"
"v_fma_f32 v9, v109, s52, v9\n\t"
"v_mfma_f32_16x16x32_bf16 v[136:139], a[240:243], a[176:179], v[136:139]\n\t"
"ds_read_b64_tr_b16 v[234:235], v246 offset:56320\n\t"
"v_mfma_f32_16x16x32_bf16 v[136:139], a[208:211], a[180:183], v[136:139]\n\t"
"v_cvt_pk_bf16_f32 v228, v228, v229\n\t"
"v_cvt_pk_bf16_f32 v229, v230, v231\n\t"
"v_mfma_f32_16x16x32_bf16 v[140:143], a[236:239], a[176:179], v[140:143]\n\t"
"v_fma_f32 v10, v110, s52, v10\n\t"
"v_fma_f32 v11, v111, s52, v11\n\t"
"v_mfma_f32_16x16x32_bf16 v[140:143], a[240:243], a[180:183], v[140:143]\n\t"
"v_fma_f32 v108, v8, s54, -v108\n\t"
"v_fma_f32 v109, v9, s54, -v109\n\t"
"v_mfma_f32_16x16x32_bf16 v[140:143], a[208:211], a[184:187], v[140:143]\n\t"
"v_fma_f32 v110, v10, s54, -v110\n\t"
"v_fma_f32 v111, v11, s54, -v111\n\t"
"v_mfma_f32_16x16x32_bf16 v[144:147], a[236:239], a[180:183], v[144:147]\n\t"
"v_fma_f32 v12, v112, s52, v12\n\t"
"v_fma_f32 v13, v113, s52, v13\n\t"
"v_mfma_f32_16x16x32_bf16 v[144:147], a[240:243], a[184:187], v[144:147]\n\t"
"v_fma_f32 v14, v114, s52, v14\n\t"
"v_fma_f32 v15, v115, s52, v15\n\t"
"v_mfma_f32_16x16x32_bf16 v[144:147], a[208:211], a[188:191], v[144:147]\n\t"
"buffer_load_dwordx4 a[220:223], v252, s[8:11], s20 offen offset:1024 sc1\n\t"
"v_mfma_f32_16x16x32_bf16 v[148:151], a[236:239], a[184:187], v[148:151]\n\t"
"s_waitcnt lgkmcnt(0)\n\t"
"v_fma_f32 v112, v12, s54, -v112\n\t"
"v_fma_f32 v113, v13, s54, -v113\n\t"
"v_mfma_f32_16x16x32_bf16 v[148:151], a[240:243], a[188:191], v[148:151]\n\t"
"v_fma_f32 v114, v14, s54, -v114\n\t"
"v_fma_f32 v115, v15, s54, -v115\n\t"
"v_mfma_f32_16x16x32_bf16 v[148:151], a[208:211], a[192:195], v[148:151]\n\t"
"v_fma_f32 v16, v116, s52, v16\n\t"
"v_fma_f32 v17, v117, s52, v17\n\t"
"v_mfma_f32_16x16x32_bf16 v[232:235], v[232:235], v[220:223], 0\n\t"
"v_fma_f32 v18, v118, s52, v18\n\t"
"v_fma_f32 v19, v119, s52, v19\n\t"
"v_mfma_f32_16x16x32_bf16 v[152:155], a[236:239], a[188:191], v[152:155]\n\t"
"v_fma_f32 v116, v16, s54, -v116\n\t"
"v_fma_f32 v117, v17, s54, -v117\n\t"
"v_mfma_f32_16x16x32_bf16 v[152:155], a[240:243], a[192:195], v[152:155]\n\t"
"v_fma_f32 v118, v18, s54, -v118\n\t"
"v_fma_f32 v119, v19, s54, -v119\n\t"
"v_mfma_f32_16x16x32_bf16 v[152:155], a[208:211], a[196:199], v[152:155]\n\t"
"v_fma_f32 v20, v120, s52, v20\n\t"
"v_fma_f32 v21, v121, s52, v21\n\t"
"v_mfma_f32_16x16x32_bf16 v[156:159], a[236:239], a[192:195], v[156:159]\n\t"
"v_fma_f32 v22, v122, s52, v22\n\t"
"v_fma_f32 v23, v123, s52, v23\n\t"
"v_mfma_f32_16x16x32_bf16 v[156:159], a[240:243], a[196:199], v[156:159]\n\t"
"v_cvt_pk_bf16_f32 v230, v232, v233\n\t"
"v_cvt_pk_bf16_f32 v231, v234, v235\n\t"
"v_mfma_f32_16x16x32_bf16 v[156:159], a[208:211], a[200:203], v[156:159]\n\t"
"ds_write2st64_b64 v245, v[228:229], v[230:231] offset0:7 offset1:8\n\t"
"v_mfma_f32_16x16x32_bf16 v[160:163], a[236:239], a[196:199], v[160:163]\n\t"
"s_waitcnt vmcnt(13)\n\t"
"v_fma_f32 v120, v20, s54, -v120\n\t"
"v_fma_f32 v121, v21, s54, -v121\n\t"
"v_mfma_f32_16x16x32_bf16 v[160:163], a[240:243], a[200:203], v[160:163]\n\t"
"v_fma_f32 v122, v22, s54, -v122\n\t"
"v_fma_f32 v123, v23, s54, -v123\n\t"
"v_mfma_f32_16x16x32_bf16 v[160:163], a[208:211], a[204:207], v[160:163]\n\t"
"v_fma_f32 v24, v124, s52, v24\n\t"
"v_fma_f32 v25, v125, s52, v25\n\t"
"v_mfma_f32_16x16x32_bf16 v[164:167], a[244:247], a[168:171], v[164:167]\n\t"
"v_fma_f32 v26, v126, s52, v26\n\t"
"v_fma_f32 v27, v127, s52, v27\n\t"
"v_mfma_f32_16x16x32_bf16 v[164:167], v[248:251], a[172:175], v[164:167]\n\t"
"v_fma_f32 v124, v24, s54, -v124\n\t"
"v_mfma_f32_16x16x32_bf16 v[164:167], v[240:243], a[176:179], v[164:167]\n\t"
"s_waitcnt lgkmcnt(0)\n\t"
"s_barrier\n\t"
"v_mfma_f32_16x16x32_bf16 v[168:171], a[244:247], a[172:175], v[168:171]\n\t"
"ds_read_b128 a[168:171], v244 offset:0\n\t"
"v_mfma_f32_16x16x32_bf16 v[168:171], v[248:251], a[176:179], v[168:171]\n\t"
"ds_read_b128 a[172:175], v244 offset:512\n\t"
"v_fma_f32 v125, v25, s54, -v125\n\t"
"v_mfma_f32_16x16x32_bf16 v[168:171], v[240:243], a[180:183], v[168:171]\n\t"
"v_fma_f32 v126, v26, s54, -v126\n\t"
"v_fma_f32 v127, v27, s54, -v127\n\t"
"v_mfma_f32_16x16x32_bf16 v[172:175], a[244:247], a[176:179], v[172:175]\n\t"
"ds_read_b128 a[176:179], v244 offset:1024\n\t"
"v_fma_f32 v28, v128, s52, v28\n\t"
"v_mfma_f32_16x16x32_bf16 v[172:175], v[248:251], a[180:183], v[172:175]\n\t"
"v_fma_f32 v29, v129, s52, v29\n\t"
"v_fma_f32 v30, v130, s52, v30\n\t"
"v_mfma_f32_16x16x32_bf16 v[172:175], v[240:243], a[184:187], v[172:175]\n\t"
"v_fma_f32 v31, v131, s52, v31\n\t"
"v_fma_f32 v128, v28, s54, -v128\n\t"
"v_mfma_f32_16x16x32_bf16 v[176:179], a[244:247], a[180:183], v[176:179]\n\t"
"ds_read_b128 a[180:183], v244 offset:1536\n\t"
"v_fma_f32 v129, v29, s54, -v129\n\t"
"v_mfma_f32_16x16x32_bf16 v[176:179], v[248:251], a[184:187], v[176:179]\n\t"
"v_fma_f32 v130, v30, s54, -v130\n\t"
"v_fma_f32 v131, v31, s54, -v131\n\t"
"v_mfma_f32_16x16x32_bf16 v[176:179], v[240:243], a[188:191], v[176:179]\n\t"
"v_fma_f32 v32, v132, s52, v32\n\t"
"v_fma_f32 v33, v133, s52, v33\n\t"
"v_mfma_f32_16x16x32_bf16 v[180:183], a[244:247], a[184:187], v[180:183]\n\t"
"ds_read_b128 a[184:187], v244 offset:2048\n\t"
"v_fma_f32 v34, v134, s52, v34\n\t"
"v_mfma_f32_16x16x32_bf16 v[180:183], v[248:251], a[188:191], v[180:183]\n\t"
"v_fma_f32 v35, v135, s52, v35\n\t"
"v_fma_f32 v132, v32, s54, -v132\n\t"
"v_mfma_f32_16x16x32_bf16 v[180:183], v[240:243], a[192:195], v[180:183]\n\t"
"v_fma_f32 v133, v33, s54, -v133\n\t"
"v_fma_f32 v134, v34, s54, -v134\n\t"
"v_mfma_f32_16x16x32_bf16 v[184:187], a[244:247], a[188:191], v[184:187]\n\t"
"ds_read_b128 a[188:191], v244 offset:2560\n\t"
"v_fma_f32 v135, v35, s54, -v135\n\t"
"v_mfma_f32_16x16x32_bf16 v[184:187], v[248:251], a[192:195], v[184:187]\n\t"
"v_fma_f32 v36, v136, s52, v36\n\t"
"v_fma_f32 v37, v137, s52, v37\n\t"
"v_mfma_f32_16x16x32_bf16 v[184:187], v[240:243], a[196:199], v[184:187]\n\t"
"v_fma_f32 v38, v138, s52, v38\n\t"
"v_fma_f32 v39, v139, s52, v39\n\t"
"v_mfma_f32_16x16x32_bf16 v[188:191], a[244:247], a[192:195], v[188:191]\n\t"
"ds_read_b128 a[192:195], v244 offset:3072\n\t"
"v_fma_f32 v136, v36, s54, -v136\n\t"
"v_mfma_f32_16x16x32_bf16 v[188:191], v[248:251], a[196:199], v[188:191]\n\t"
"v_fma_f32 v137, v37, s54, -v137\n\t"
"v_fma_f32 v138, v38, s54, -v138\n\t"
"v_mfma_f32_16x16x32_bf16 v[188:191], v[240:243], a[200:203], v[188:191]\n\t"
"buffer_load_dwordx4 a[208:211], v252, s[8:11], s20 offen offset:2048 sc1\n\t"
"s_mov_b32 s20, 1622016\n\t"
"v_mfma_f32_16x16x32_bf16 v[192:195], a[244:247], a[196:199], v[192:195]\n\t"
"buffer_load_dwordx4 a[224:227], v252, s[8:11], s20 offen offset:0 sc1\n\t"
"v_mfma_f32_16x16x32_bf16 v[192:195], v[248:251], a[200:203], v[192:195]\n\t"
"s_waitcnt vmcnt(12) lgkmcnt(4)\n\t"
"ds_read_b128 a[196:199], v244 offset:3584\n\t"
"v_fma_f32 v139, v39, s54, -v139\n\t"
"v_mfma_f32_16x16x32_bf16 v[192:195], v[240:243], a[204:207], v[192:195]\n\t"
"ds_read_b128 a[200:203], v244 offset:4096\n\t"
"v_fma_f32 v40, v140, s52, v40\n\t"
"Lst13_%=:\n\t"
"v_mfma_f32_16x16x32_bf16 v[196:199], a[248:251], a[168:171], v[196:199]\n\t"
"s_waitcnt lgkmcnt(5)\n\t"
"ds_read_b128 a[204:207], v244 offset:4608\n\t"
"v_fma_f32 v41, v141, s52, v41\n\t"
"v_mfma_f32_16x16x32_bf16 v[196:199], v[236:239], a[172:175], v[196:199]\n\t"
"v_fma_f32 v42, v142, s52, v42\n\t"
"v_fma_f32 v43, v143, s52, v43\n\t"
"v_mfma_f32_16x16x32_bf16 v[196:199], v[96:99], a[176:179], v[196:199]\n\t"
"buffer_load_dwordx4 v[248:251], v252, s[8:11], s20 offen offset:1024 sc1\n\t"
"v_mfma_f32_16x16x32_bf16 v[204:207], a[248:251], a[172:175], v[204:207]\n\t"
"buffer_load_dwordx4 v[240:243], v252, s[8:11], s20 offen offset:2048 sc1\n\t"
"v_mfma_f32_16x16x32_bf16 v[204:207], v[236:239], a[176:179], v[204:207]\n\t"
"s_waitcnt lgkmcnt(5)\n\t"
"s_mov_b32 s20, 1634304\n\t"
"v_fma_f32 v140, v40, s54, -v140\n\t"
"v_mfma_f32_16x16x32_bf16 v[204:207], v[96:99], a[180:183], v[204:207]\n\t"
"v_fma_f32 v141, v41, s54, -v141\n\t"
"v_fma_f32 v142, v42, s54, -v142\n\t"
"v_mfma_f32_16x16x32_bf16 v[212:215], a[248:251], a[176:179], v[212:215]\n\t"
"s_waitcnt lgkmcnt(4)\n\t"
"v_fma_f32 v143, v43, s54, -v143\n\t"
"v_fma_f32 v44, v144, s52, v44\n\t"
"v_mfma_f32_16x16x32_bf16 v[212:215], v[236:239], a[180:183], v[212:215]\n\t"
"v_fma_f32 v45, v145, s52, v45\n\t"
"v_fma_f32 v46, v146, s52, v46\n\t"
"v_mfma_f32_16x16x32_bf16 v[212:215], v[96:99], a[184:187], v[212:215]\n\t"
"v_fma_f32 v47, v147, s52, v47\n\t"
"v_fma_f32 v144, v44, s54, -v144\n\t"
"v_mfma_f32_16x16x32_bf16 a[0:3], a[248:251], a[180:183], a[0:3]\n\t"
"s_waitcnt lgkmcnt(3)\n\t"
"v_fma_f32 v145, v45, s54, -v145\n\t"
"v_fma_f32 v146, v46, s54, -v146\n\t"
"v_mfma_f32_16x16x32_bf16 a[0:3], v[236:239], a[184:187], a[0:3]\n\t"
"v_fma_f32 v147, v47, s54, -v147\n\t"
"v_fma_f32 v48, v148, s52, v48\n\t"
"v_mfma_f32_16x16x32_bf16 a[0:3], v[96:99], a[188:191], a[0:3]\n\t"
"buffer_load_dwordx4 a[232:235], v252, s[8:11], s20 offen offset:0 sc1\n\t"
"v_mfma_f32_16x16x32_bf16 a[8:11], a[248:251], a[184:187], a[8:11]\n\t"
"buffer_load_dwordx4 a[236:239], v252, s[8:11], s20 offen offset:1024 sc1\n\t"
"v_mfma_f32_16x16x32_bf16 a[8:11], v[236:239], a[188:191], a[8:11]\n\t"
"s_waitcnt lgkmcnt(2)\n\t"
"s_add_u32 m0, s24, 72704\n\t"
"s_add_u32 s28, s21, 2162688\n\t"
"v_mfma_f32_16x16x32_bf16 a[8:11], v[96:99], a[192:195], a[8:11]\n\t"
"buffer_load_dwordx4 a[240:243], v252, s[8:11], s20 offen offset:2048 sc1\n\t"
"v_mfma_f32_16x16x32_bf16 a[16:19], a[248:251], a[188:191], a[16:19]\n\t"
"buffer_load_dwordx4 v247, s[12:15], s28 offen offset:0 nt lds\n\t"
"v_mfma_f32_16x16x32_bf16 a[16:19], v[236:239], a[192:195], a[16:19]\n\t"
"s_waitcnt lgkmcnt(1)\n\t"
"s_add_u32 m0, s24, 92672\n\t"
"s_add_u32 s28, s21, 2752512\n\t"
"v_mfma_f32_16x16x32_bf16 a[16:19], v[96:99], a[196:199], a[16:19]\n\t"
"buffer_load_dwordx4 v247, s[12:15], s28 offen offset:0 nt lds\n\t"
"v_mfma_f32_16x16x32_bf16 a[24:27], a[248:251], a[192:195], a[24:27]\n\t"
"s_waitcnt lgkmcnt(0)\n\t"
"s_add_u32 m0, s24, 100352\n\t"
"s_add_u32 s28, s21, 2949120\n\t"
"v_mfma_f32_16x16x32_bf16 a[24:27], v[236:239], a[196:199], a[24:27]\n\t"
"buffer_load_dwordx4 v247, s[12:15], s28 offen offset:0 nt lds\n\t"
"v_mfma_f32_16x16x32_bf16 a[24:27], v[96:99], a[200:203], a[24:27]\n\t"
"s_add_u32 m0, s24, 106496\n\t"
"s_add_u32 s28, s21, 3145728\n\t"
"v_fma_f32 v49, v149, s52, v49\n\t"
"v_mfma_f32_16x16x32_bf16 a[32:35], a[248:251], a[196:199], a[32:35]\n\t"
"s_waitcnt vmcnt(17)\n\t"
"v_fma_f32 v50, v150, s52, v50\n\t"
"v_fma_f32 v51, v151, s52, v51\n\t"
"v_mfma_f32_16x16x32_bf16 a[32:35], v[236:239], a[200:203], a[32:35]\n\t"
"v_fma_f32 v148, v48, s54, -v148\n\t"
"v_fma_f32 v149, v49, s54, -v149\n\t"
"v_mfma_f32_16x16x32_bf16 a[32:35], v[96:99], a[204:207], a[32:35]\n\t"
"buffer_load_dwordx4 v247, s[12:15], s28 offen offset:0 nt lds\n\t"
"s_add_u32 m0, s24, 112640\n\t"
"s_add_u32 s28, s21, 3342336\n\t"
"v_mfma_f32_16x16x32_bf16 a[40:43], a[252:255], a[168:171], a[40:43]\n\t"
"buffer_load_dwordx4 v247, s[12:15], s28 offen offset:0 nt lds\n\t"
"v_mfma_f32_16x16x32_bf16 a[40:43], a[228:231], a[172:175], a[40:43]\n\t"
"v_fma_f32 v150, v50, s54, -v150\n\t"
"v_fma_f32 v151, v51, s54, -v151\n\t"
"v_mfma_f32_16x16x32_bf16 a[40:43], a[212:215], a[176:179], a[40:43]\n\t"
"v_fma_f32 v52, v152, s52, v52\n\t"
"v_fma_f32 v53, v153, s52, v53\n\t"
"v_mfma_f32_16x16x32_bf16 a[48:51], a[252:255], a[172:175], a[48:51]\n\t"
"v_fma_f32 v54, v154, s52, v54\n\t"
"v_fma_f32 v55, v155, s52, v55\n\t"
"v_mfma_f32_16x16x32_bf16 a[48:51], a[228:231], a[176:179], a[48:51]\n\t"
"v_fma_f32 v152, v52, s54, -v152\n\t"
"v_fma_f32 v153, v53, s54, -v153\n\t"
"v_mfma_f32_16x16x32_bf16 a[48:51], a[212:215], a[180:183], a[48:51]\n\t"
"v_fma_f32 v154, v54, s54, -v154\n\t"
"v_fma_f32 v155, v55, s54, -v155\n\t"
"v_mfma_f32_16x16x32_bf16 a[56:59], a[252:255], a[176:179], a[56:59]\n\t"
"v_fma_f32 v56, v156, s52, v56\n\t"
"v_fma_f32 v57, v157, s52, v57\n\t"
"v_mfma_f32_16x16x32_bf16 a[56:59], a[228:231], a[180:183], a[56:59]\n\t"
"v_fma_f32 v58, v158, s52, v58\n\t"
"v_fma_f32 v59, v159, s52, v59\n\t"
"v_mfma_f32_16x16x32_bf16 a[56:59], a[212:215], a[184:187], a[56:59]\n\t"
"v_fma_f32 v156, v56, s54, -v156\n\t"
"v_fma_f32 v157, v57, s54, -v157\n\t"
"v_mfma_f32_16x16x32_bf16 a[64:67], a[252:255], a[180:183], a[64:67]\n\t"
"v_fma_f32 v158, v58, s54, -v158\n\t"
"v_fma_f32 v159, v59, s54, -v159\n\t"
"v_mfma_f32_16x16x32_bf16 a[64:67], a[228:231], a[184:187], a[64:67]\n\t"
"v_fma_f32 v60, v160, s52, v60\n\t"
"v_fma_f32 v61, v161, s52, v61\n\t"
"v_mfma_f32_16x16x32_bf16 a[64:67], a[212:215], a[188:191], a[64:67]\n\t"
"v_fma_f32 v62, v162, s52, v62\n\t"
"v_fma_f32 v63, v163, s52, v63\n\t"
"v_mfma_f32_16x16x32_bf16 a[72:75], a[252:255], a[184:187], a[72:75]\n\t"
"v_fma_f32 v160, v60, s54, -v160\n\t"
"v_fma_f32 v161, v61, s54, -v161\n\t"
"v_mfma_f32_16x16x32_bf16 a[72:75], a[228:231], a[188:191], a[72:75]\n\t"
"v_fma_f32 v162, v62, s54, -v162\n\t"
"v_fma_f32 v163, v63, s54, -v163\n\t"
"v_mfma_f32_16x16x32_bf16 a[72:75], a[212:215], a[192:195], a[72:75]\n\t"
"v_fma_f32 v64, v164, s52, v64\n\t"
"v_fma_f32 v65, v165, s52, v65\n\t"
"v_mfma_f32_16x16x32_bf16 a[80:83], a[252:255], a[188:191], a[80:83]\n\t"
"v_fma_f32 v66, v166, s52, v66\n\t"
"v_fma_f32 v67, v167, s52, v67\n\t"
"v_mfma_f32_16x16x32_bf16 a[80:83], a[228:231], a[192:195], a[80:83]\n\t"
"v_fma_f32 v164, v64, s54, -v164\n\t"
"v_fma_f32 v165, v65, s54, -v165\n\t"
"v_mfma_f32_16x16x32_bf16 a[80:83], a[212:215], a[196:199], a[80:83]\n\t"
"v_fma_f32 v166, v66, s54, -v166\n\t"
"v_fma_f32 v167, v67, s54, -v167\n\t"
"v_mfma_f32_16x16x32_bf16 a[88:91], a[252:255], a[192:195], a[88:91]\n\t"
"v_fma_f32 v68, v168, s52, v68\n\t"
"v_fma_f32 v69, v169, s52, v69\n\t"
"v_mfma_f32_16x16x32_bf16 a[88:91], a[228:231], a[196:199], a[88:91]\n\t"
"v_fma_f32 v70, v170, s52, v70\n\t"
"v_fma_f32 v71, v171, s52, v71\n\t"
"v_mfma_f32_16x16x32_bf16 a[88:91], a[212:215], a[200:203], a[88:91]\n\t"
"v_fma_f32 v168, v68, s54, -v168\n\t"
"v_fma_f32 v169, v69, s54, -v169\n\t"
"v_mfma_f32_16x16x32_bf16 a[96:99], a[252:255], a[196:199], a[96:99]\n\t"
"s_waitcnt vmcnt(11)\n\t"
"v_fma_f32 v170, v70, s54, -v170\n\t"
"v_fma_f32 v171, v71, s54, -v171\n\t"
"v_mfma_f32_16x16x32_bf16 a[96:99], a[228:231], a[200:203], a[96:99]\n\t"
"v_fma_f32 v72, v172, s52, v72\n\t"
"v_fma_f32 v73, v173, s52, v73\n\t"
"v_mfma_f32_16x16x32_bf16 a[96:99], a[212:215], a[204:207], a[96:99]\n\t"
"v_fma_f32 v74, v174, s52, v74\n\t"
"v_fma_f32 v75, v175, s52, v75\n\t"
"v_mfma_f32_16x16x32_bf16 a[104:107], a[216:219], a[168:171], a[104:107]\n\t"
"ds_read_b128 a[168:171], v244 offset:9216\n\t"
"v_fma_f32 v172, v72, s54, -v172\n\t"
"v_mfma_f32_16x16x32_bf16 a[104:107], a[220:223], a[172:175], a[104:107]\n\t"
"v_fma_f32 v173, v73, s54, -v173\n\t"
"v_fma_f32 v174, v74, s54, -v174\n\t"
"v_mfma_f32_16x16x32_bf16 a[104:107], a[208:211], a[176:179], a[104:107]\n\t"
"v_fma_f32 v175, v75, s54, -v175\n\t"
"v_fma_f32 v76, v176, s52, v76\n\t"
"v_mfma_f32_16x16x32_bf16 a[112:115], a[216:219], a[172:175], a[112:115]\n\t"
"ds_read_b128 a[172:175], v244 offset:9728\n\t"
"v_fma_f32 v77, v177, s52, v77\n\t"
"v_mfma_f32_16x16x32_bf16 a[112:115], a[220:223], a[176:179], a[112:115]\n\t"
"v_fma_f32 v78, v178, s52, v78\n\t"
"v_fma_f32 v79, v179, s52, v79\n\t"
"v_mfma_f32_16x16x32_bf16 a[112:115], a[208:211], a[180:183], a[112:115]\n\t"
"v_and_b32 v255, 4096, v244\n\t"
"v_mad_u32_u24 v255, v255, 5, v244\n\t"
"v_mfma_f32_16x16x32_bf16 a[120:123], a[216:219], a[176:179], a[120:123]\n\t"
"ds_read_b128 a[176:179], v244 offset:10240\n\t"
"v_add_u32 v255, 43008, v255\n\t"
"v_mfma_f32_16x16x32_bf16 a[120:123], a[220:223], a[180:183], a[120:123]\n\t"
"v_fma_f32 v176, v76, s54, -v176\n\t"
"v_fma_f32 v177, v77, s54, -v177\n\t"
"v_mfma_f32_16x16x32_bf16 a[120:123], a[208:211], a[184:187], a[120:123]\n\t"
"v_fma_f32 v178, v78, s54, -v178\n\t"
"v_fma_f32 v179, v79, s54, -v179\n\t"
"v_mfma_f32_16x16x32_bf16 a[128:131], a[216:219], a[180:183], a[128:131]\n\t"
"ds_read_b128 a[180:183], v244 offset:10752\n\t"
"v_fma_f32 v80, v180, s52, v80\n\t"
"v_mfma_f32_16x16x32_bf16 a[128:131], a[220:223], a[184:187], a[128:131]\n\t"
"v_fma_f32 v81, v181, s52, v81\n\t"
"v_fma_f32 v82, v182, s52, v82\n\t"
"v_mfma_f32_16x16x32_bf16 a[128:131], a[208:211], a[188:191], a[128:131]\n\t"
"v_fma_f32 v83, v183, s52, v83\n\t"
"v_fma_f32 v180, v80, s54, -v180\n\t"
"v_mfma_f32_16x16x32_bf16 a[136:139], a[216:219], a[184:187], a[136:139]\n\t"
"ds_read_b128 a[184:187], v244 offset:11264\n\t"
"v_fma_f32 v181, v81, s54, -v181\n\t"
"v_mfma_f32_16x16x32_bf16 a[136:139], a[220:223], a[188:191], a[136:139]\n\t"
"v_fma_f32 v182, v82, s54, -v182\n\t"
"v_fma_f32 v183, v83, s54, -v183\n\t"
"v_mfma_f32_16x16x32_bf16 a[136:139], a[208:211], a[192:195], a[136:139]\n\t"
"v_fma_f32 v84, v184, s52, v84\n\t"
"v_fma_f32 v85, v185, s52, v85\n\t"
"v_mfma_f32_16x16x32_bf16 a[144:147], a[216:219], a[188:191], a[144:147]\n\t"
"ds_read_b128 a[188:191], v244 offset:11776\n\t"
"v_fma_f32 v86, v186, s52, v86\n\t"
"v_mfma_f32_16x16x32_bf16 a[144:147], a[220:223], a[192:195], a[144:147]\n\t"
"v_fma_f32 v87, v187, s52, v87\n\t"
"v_fma_f32 v184, v84, s54, -v184\n\t"
"v_mfma_f32_16x16x32_bf16 a[144:147], a[208:211], a[196:199], a[144:147]\n\t"
"v_fma_f32 v185, v85, s54, -v185\n\t"
"v_fma_f32 v186, v86, s54, -v186\n\t"
"v_mfma_f32_16x16x32_bf16 a[152:155], a[216:219], a[192:195], a[152:155]\n\t"
"s_mov_b32 s20, 1646592\n\t"
"ds_read_b128 a[192:195], v244 offset:12288\n\t"
"v_fma_f32 v187, v87, s54, -v187\n\t"
"v_mfma_f32_16x16x32_bf16 a[152:155], a[220:223], a[196:199], a[152:155]\n\t"
"v_fma_f32 v88, v188, s52, v88\n\t"
"v_fma_f32 v89, v189, s52, v89\n\t"
"v_mfma_f32_16x16x32_bf16 a[152:155], a[208:211], a[200:203], a[152:155]\n\t"
"v_fma_f32 v90, v190, s52, v90\n\t"
"v_fma_f32 v91, v191, s52, v91\n\t"
"v_mfma_f32_16x16x32_bf16 a[160:163], a[216:219], a[196:199], a[160:163]\n\t"
"s_waitcnt vmcnt(8) lgkmcnt(4)\n\t"
"ds_read_b128 a[196:199], v244 offset:12800\n\t"
"v_fma_f32 v188, v88, s54, -v188\n\t"
"v_mfma_f32_16x16x32_bf16 a[160:163], a[220:223], a[200:203], a[160:163]\n\t"
"ds_read_b128 a[200:203], v244 offset:13312\n\t"
"v_fma_f32 v189, v89, s54, -v189\n\t"
"v_mfma_f32_16x16x32_bf16 a[160:163], a[208:211], a[204:207], a[160:163]\n\t"
"ds_read_b128 a[204:207], v244 offset:13824\n\t"
"v_fma_f32 v190, v90, s54, -v190\n\t"
"v_fma_f32 v191, v91, s54, -v191\n\t"
"v_mfma_f32_16x16x32_bf16 v[200:203], a[224:227], a[168:171], v[200:203]\n\t"
"s_waitcnt lgkmcnt(6)\n\t"
"v_fma_f32 v92, v192, s52, v92\n\t"
"v_fma_f32 v93, v193, s52, v93\n\t"
"v_mfma_f32_16x16x32_bf16 v[200:203], v[248:251], a[172:175], v[200:203]\n\t"
"v_fma_f32 v94, v194, s52, v94\n\t"
"v_fma_f32 v95, v195, s52, v95\n\t"
"v_mfma_f32_16x16x32_bf16 v[200:203], v[240:243], a[176:179], v[200:203]\n\t"
"v_fma_f32 v192, v92, s54, -v192\n\t"
"v_fma_f32 v193, v93, s54, -v193\n\t"
"v_fma_f32 v194, v94, s54, -v194\n\t"
"v_fma_f32 v195, v95, s54, -v195\n\t"
"v_mfma_f32_16x16x32_bf16 v[208:211], a[224:227], a[172:175], v[208:211]\n\t"
"s_waitcnt lgkmcnt(5)\n\t"
"v_mfma_f32_16x16x32_bf16 v[208:211], v[248:251], a[176:179], v[208:211]\n\t"
"v_mfma_f32_16x16x32_bf16 v[208:211], v[240:243], a[180:183], v[208:211]\n\t"
"s_waitcnt lgkmcnt(0)\n\t"
"s_barrier\n\t"
"v_mfma_f32_16x16x32_bf16 v[216:219], a[224:227], a[176:179], v[216:219]\n\t"
"v_mfma_f32_16x16x32_bf16 v[216:219], v[248:251], a[180:183], v[216:219]\n\t"
"buffer_load_dwordx4 a[208:211], v252, s[8:11], s20 offen offset:0 sc1\n\t"
"v_mfma_f32_16x16x32_bf16 v[216:219], v[240:243], a[184:187], v[216:219]\n\t"
"buffer_load_dwordx4 a[212:215], v252, s[8:11], s20 offen offset:1024 sc1\n\t"
"v_pk_fma_f32 v[196:197], v[200:201], s[56:57], v[196:197]\n\t"
"v_pk_fma_f32 v[200:201], v[196:197], s[58:59], v[200:201] neg_lo:[0,0,1] neg_hi:[0,0,1]\n\t"
"v_pk_fma_f32 v[228:229], v[0:1], s[60:61], v[196:197]\n\t"
"v_pk_fma_f32 v[0:1], v[196:197], s[60:61], v[0:1]\n\t"
"v_pk_fma_f32 v[196:197], v[100:101], s[62:63], v[200:201]\n\t"
"v_pk_fma_f32 v[100:101], v[200:201], s[62:63], v[100:101]\n\t"
"v_pk_fma_f32 v[198:199], v[202:203], s[56:57], v[198:199]\n\t"
"v_pk_fma_f32 v[202:203], v[198:199], s[58:59], v[202:203] neg_lo:[0,0,1] neg_hi:[0,0,1]\n\t"
"v_pk_fma_f32 v[234:235], v[2:3], s[60:61], v[198:199]\n\t"
"v_pk_fma_f32 v[2:3], v[198:199], s[60:61], v[2:3]\n\t"
"v_pk_fma_f32 v[198:199], v[102:103], s[62:63], v[202:203]\n\t"
"v_pk_fma_f32 v[102:103], v[202:203], s[62:63], v[102:103]\n\t"
"v_mfma_f32_16x16x32_bf16 a[4:7], a[224:227], a[180:183], a[4:7]\n\t"
"v_cvt_pk_bf16_f32 v196, v196, v197\n\t"
"v_cvt_pk_bf16_f32 v200, v228, v229\n\t"
"v_mfma_f32_16x16x32_bf16 a[4:7], v[248:251], a[184:187], a[4:7]\n\t"
"v_cvt_pk_bf16_f32 v197, v198, v199\n\t"
"v_cvt_pk_bf16_f32 v201, v234, v235\n\t"
"v_mfma_f32_16x16x32_bf16 a[4:7], v[240:243], a[188:191], a[4:7]\n\t"
"buffer_load_dwordx4 a[216:219], v252, s[8:11], s20 offen offset:2048 sc1\n\t"
"s_mov_b32 s20, 1474560\n\t"
"v_pk_fma_f32 v[204:205], v[208:209], s[56:57], v[204:205]\n\t"
"v_pk_fma_f32 v[208:209], v[204:205], s[58:59], v[208:209] neg_lo:[0,0,1] neg_hi:[0,0,1]\n\t"
"v_pk_fma_f32 v[98:99], v[4:5], s[60:61], v[204:205]\n\t"
"v_pk_fma_f32 v[4:5], v[204:205], s[60:61], v[4:5]\n\t"
"v_pk_fma_f32 v[204:205], v[104:105], s[62:63], v[208:209]\n\t"
"v_pk_fma_f32 v[104:105], v[208:209], s[62:63], v[104:105]\n\t"
"v_mfma_f32_16x16x32_bf16 a[12:15], a[224:227], a[184:187], a[12:15]\n\t"
"v_cvt_pk_bf16_f32 v204, v204, v205\n\t"
"v_cvt_pk_bf16_f32 v208, v98, v99\n\t"
"v_mfma_f32_16x16x32_bf16 a[12:15], v[248:251], a[188:191], a[12:15]\n\t"
"v_mfma_f32_16x16x32_bf16 a[12:15], v[240:243], a[192:195], a[12:15]\n\t"
"buffer_load_dwordx4 a[220:223], v252, s[8:11], s20 offen offset:0 sc1\n\t"
"v_pk_fma_f32 v[206:207], v[210:211], s[56:57], v[206:207]\n\t"
"v_pk_fma_f32 v[210:211], v[206:207], s[58:59], v[210:211] neg_lo:[0,0,1] neg_hi:[0,0,1]\n\t"
"v_pk_fma_f32 v[228:229], v[6:7], s[60:61], v[206:207]\n\t"
"v_pk_fma_f32 v[6:7], v[206:207], s[60:61], v[6:7]\n\t"
"v_pk_fma_f32 v[206:207], v[106:107], s[62:63], v[210:211]\n\t"
"v_pk_fma_f32 v[106:107], v[210:211], s[62:63], v[106:107]\n\t"
"v_mfma_f32_16x16x32_bf16 a[20:23], a[224:227], a[188:191], a[20:23]\n\t"
"v_cvt_pk_bf16_f32 v205, v206, v207\n\t"
"v_cvt_pk_bf16_f32 v209, v228, v229\n\t"
"v_mfma_f32_16x16x32_bf16 a[20:23], v[248:251], a[192:195], a[20:23]\n\t"
"v_accvgpr_read_b32 v224, a0\n\t"
"v_accvgpr_read_b32 v226, a4\n\t"
"v_mfma_f32_16x16x32_bf16 a[20:23], v[240:243], a[196:199], a[20:23]\n\t"
"buffer_load_dwordx4 a[228:231], v252, s[8:11], s20 offen offset:1024 sc1\n\t"
"v_accvgpr_read_b32 v225, a1\n\t"
"v_accvgpr_read_b32 v227, a5\n\t"
"v_pk_fma_f32 v[212:213], v[216:217], s[56:57], v[212:213]\n\t"
"v_pk_fma_f32 v[216:217], v[212:213], s[58:59], v[216:217] neg_lo:[0,0,1] neg_hi:[0,0,1]\n\t"
"v_pk_fma_f32 v[234:235], v[8:9], s[60:61], v[212:213]\n\t"
"v_pk_fma_f32 v[8:9], v[212:213], s[60:61], v[8:9]\n\t"
"v_pk_fma_f32 v[212:213], v[108:109], s[62:63], v[216:217]\n\t"
"v_pk_fma_f32 v[108:109], v[216:217], s[62:63], v[108:109]\n\t"
"v_mfma_f32_16x16x32_bf16 a[28:31], a[224:227], a[192:195], a[28:31]\n\t"
"v_cvt_pk_bf16_f32 v212, v212, v213\n\t"
"v_cvt_pk_bf16_f32 v216, v234, v235\n\t"
"v_mfma_f32_16x16x32_bf16 a[28:31], v[248:251], a[196:199], a[28:31]\n\t"
"v_accvgpr_read_b32 v230, a2\n\t"
"v_accvgpr_read_b32 v232, a6\n\t"
"v_mfma_f32_16x16x32_bf16 a[28:31], v[240:243], a[200:203], a[28:31]\n\t"
"buffer_load_dwordx4 a[244:247], v252, s[8:11], s20 offen offset:2048 sc1\n\t"
"v_accvgpr_read_b32 v231, a3\n\t"
"v_accvgpr_read_b32 v233, a7\n\t"
"v_pk_fma_f32 v[214:215], v[218:219], s[56:57], v[214:215]\n\t"
"v_pk_fma_f32 v[218:219], v[214:215], s[58:59], v[218:219] neg_lo:[0,0,1] neg_hi:[0,0,1]\n\t"
"v_pk_fma_f32 v[98:99], v[10:11], s[60:61], v[214:215]\n\t"
"v_pk_fma_f32 v[10:11], v[214:215], s[60:61], v[10:11]\n\t"
"v_pk_fma_f32 v[214:215], v[110:111], s[62:63], v[218:219]\n\t"
"v_pk_fma_f32 v[110:111], v[218:219], s[62:63], v[110:111]\n\t"
"v_mfma_f32_16x16x32_bf16 a[36:39], a[224:227], a[196:199], a[36:39]\n\t"
"s_waitcnt vmcnt(11)\n\t"
"s_mov_b32 s20, 1486848\n\t"
"v_cvt_pk_bf16_f32 v213, v214, v215\n\t"
"v_mfma_f32_16x16x32_bf16 a[36:39], v[248:251], a[200:203], a[36:39]\n\t"
"v_cvt_pk_bf16_f32 v217, v98, v99\n\t"
"v_accvgpr_read_b32 v236, a8\n\t"
"v_mfma_f32_16x16x32_bf16 a[36:39], v[240:243], a[204:207], a[36:39]\n\t"
"buffer_load_dwordx4 v[240:243], v252, s[8:11], s20 offen offset:0 sc1\n\t"
"v_accvgpr_read_b32 v238, a12\n\t"
"v_accvgpr_read_b32 v237, a9\n\t"
"v_pk_fma_f32 v[224:225], v[226:227], s[56:57], v[224:225]\n\t"
"v_pk_fma_f32 v[226:227], v[224:225], s[58:59], v[226:227] neg_lo:[0,0,1] neg_hi:[0,0,1]\n\t"
"v_pk_fma_f32 v[228:229], v[12:13], s[60:61], v[224:225]\n\t"
"v_pk_fma_f32 v[12:13], v[224:225], s[60:61], v[12:13]\n\t"
"v_pk_fma_f32 v[224:225], v[112:113], s[62:63], v[226:227]\n\t"
"v_pk_fma_f32 v[112:113], v[226:227], s[62:63], v[112:113]\n\t"
"v_mfma_f32_16x16x32_bf16 a[44:47], a[232:235], a[168:171], a[44:47]\n\t"
"v_cvt_pk_bf16_f32 v224, v224, v225\n\t"
"v_cvt_pk_bf16_f32 v228, v228, v229\n\t"
"v_mfma_f32_16x16x32_bf16 a[44:47], a[236:239], a[172:175], a[44:47]\n\t"
"v_accvgpr_write_b32 a0, v224\n\t"
"v_accvgpr_write_b32 a4, v228\n\t"
"v_mfma_f32_16x16x32_bf16 a[44:47], a[240:243], a[176:179], a[44:47]\n\t"
"buffer_load_dwordx4 a[224:227], v252, s[8:11], s20 offen offset:1024 sc1\n\t"
"v_accvgpr_read_b32 v239, a13\n\t"
"v_accvgpr_read_b32 v224, a10\n\t"
"v_pk_fma_f32 v[230:231], v[232:233], s[56:57], v[230:231]\n\t"
"v_pk_fma_f32 v[232:233], v[230:231], s[58:59], v[232:233] neg_lo:[0,0,1] neg_hi:[0,0,1]\n\t"
"v_pk_fma_f32 v[234:235], v[14:15], s[60:61], v[230:231]\n\t"
"v_pk_fma_f32 v[14:15], v[230:231], s[60:61], v[14:15]\n\t"
"v_pk_fma_f32 v[230:231], v[114:115], s[62:63], v[232:233]\n\t"
"v_pk_fma_f32 v[114:115], v[232:233], s[62:63], v[114:115]\n\t"
"v_mfma_f32_16x16x32_bf16 a[52:55], a[232:235], a[172:175], a[52:55]\n\t"
"v_cvt_pk_bf16_f32 v230, v230, v231\n\t"
"v_cvt_pk_bf16_f32 v234, v234, v235\n\t"
"v_mfma_f32_16x16x32_bf16 a[52:55], a[236:239], a[176:179], a[52:55]\n\t"
"v_accvgpr_write_b32 a1, v230\n\t"
"v_accvgpr_write_b32 a5, v234\n\t"
"v_mfma_f32_16x16x32_bf16 a[52:55], a[240:243], a[180:183], a[52:55]\n\t"
"buffer_load_dwordx4 a[248:251], v252, s[8:11], s20 offen offset:2048 sc1\n\t"
"v_accvgpr_read_b32 v226, a14\n\t"
"v_accvgpr_read_b32 v225, a11\n\t"
"v_pk_fma_f32 v[236:237], v[238:239], s[56:57], v[236:237]\n\t"
"v_pk_fma_f32 v[238:239], v[236:237], s[58:59], v[238:239] neg_lo:[0,0,1] neg_hi:[0,0,1]\n\t"
"v_pk_fma_f32 v[98:99], v[16:17], s[60:61], v[236:237]\n\t"
"v_pk_fma_f32 v[16:17], v[236:237], s[60:61], v[16:17]\n\t"
"v_pk_fma_f32 v[236:237], v[116:117], s[62:63], v[238:239]\n\t"
"v_pk_fma_f32 v[116:117], v[238:239], s[62:63], v[116:117]\n\t"
"v_mfma_f32_16x16x32_bf16 a[60:63], a[232:235], a[176:179], a[60:63]\n\t"
"s_mov_b32 s20, 1499136\n\t"
"v_accvgpr_read_b32 v227, a15\n\t"
"v_accvgpr_read_b32 v230, a16\n\t"
"v_mfma_f32_16x16x32_bf16 a[60:63], a[236:239], a[180:183], a[60:63]\n\t"
"v_accvgpr_read_b32 v232, a20\n\t"
"v_accvgpr_read_b32 v231, a17\n\t"
"v_mfma_f32_16x16x32_bf16 a[60:63], a[240:243], a[184:187], a[60:63]\n\t"
"buffer_load_dwordx4 a[252:255], v252, s[8:11], s20 offen offset:0 sc1\n\t"
"v_accvgpr_read_b32 v233, a21\n\t"
"v_cvt_pk_bf16_f32 v236, v236, v237\n\t"
"v_cvt_pk_bf16_f32 v98, v98, v99\n\t"
"v_pk_fma_f32 v[224:225], v[226:227], s[56:57], v[224:225]\n\t"
"v_pk_fma_f32 v[226:227], v[224:225], s[58:59], v[226:227] neg_lo:[0,0,1] neg_hi:[0,0,1]\n\t"
"v_pk_fma_f32 v[228:229], v[18:19], s[60:61], v[224:225]\n\t"
"v_pk_fma_f32 v[18:19], v[224:225], s[60:61], v[18:19]\n\t"
"v_pk_fma_f32 v[224:225], v[118:119], s[62:63], v[226:227]\n\t"
"v_pk_fma_f32 v[118:119], v[226:227], s[62:63], v[118:119]\n\t"
"v_pk_fma_f32 v[230:231], v[232:233], s[56:57], v[230:231]\n\t"
"v_pk_fma_f32 v[232:233], v[230:231], s[58:59], v[232:233] neg_lo:[0,0,1] neg_hi:[0,0,1]\n\t"
"v_pk_fma_f32 v[234:235], v[20:21], s[60:61], v[230:231]\n\t"
"v_pk_fma_f32 v[20:21], v[230:231], s[60:61], v[20:21]\n\t"
"v_pk_fma_f32 v[230:231], v[120:121], s[62:63], v[232:233]\n\t"
"v_pk_fma_f32 v[120:121], v[232:233], s[62:63], v[120:121]\n\t"
"v_mfma_f32_16x16x32_bf16 a[68:71], a[232:235], a[180:183], a[68:71]\n\t"
"Lpg10_%=:\n\t"
"s_cbranch_vccnz Lvr0_%=\n\t"
"v_accvgpr_write_b32 a8, v236\n\t"
"v_accvgpr_write_b32 a12, v98\n\t"
"v_mfma_f32_16x16x32_bf16 a[68:71], a[236:239], a[184:187], a[68:71]\n\t"
"v_accvgpr_read_b32 v236, a18\n\t"
"v_accvgpr_read_b32 v238, a22\n\t"
"v_mfma_f32_16x16x32_bf16 a[68:71], a[240:243], a[188:191], a[68:71]\n\t"
"v_accvgpr_read_b32 v237, a19\n\t"
"v_accvgpr_read_b32 v239, a23\n\t"
"v_cvt_pk_bf16_f32 v224, v224, v225\n\t"
"v_cvt_pk_bf16_f32 v228, v228, v229\n\t"
"v_cvt_pk_bf16_f32 v230, v230, v231\n\t"
"v_cvt_pk_bf16_f32 v234, v234, v235\n\t"
"v_pk_fma_f32 v[236:237], v[238:239], s[56:57], v[236:237]\n\t"
"v_pk_fma_f32 v[238:239], v[236:237], s[58:59], v[238:239] neg_lo:[0,0,1] neg_hi:[0,0,1]\n\t"
"v_pk_fma_f32 v[98:99], v[22:23], s[60:61], v[236:237]\n\t"
"v_pk_fma_f32 v[22:23], v[236:237], s[60:61], v[22:23]\n\t"
"v_pk_fma_f32 v[236:237], v[122:123], s[62:63], v[238:239]\n\t"
"v_pk_fma_f32 v[122:123], v[238:239], s[62:63], v[122:123]\n\t"
"v_mfma_f32_16x16x32_bf16 a[76:79], a[232:235], a[184:187], a[76:79]\n\t"
"v_accvgpr_write_b32 a9, v224\n\t"
"v_accvgpr_write_b32 a13, v228\n\t"
"v_mfma_f32_16x16x32_bf16 a[76:79], a[236:239], a[188:191], a[76:79]\n\t"
"v_accvgpr_write_b32 a16, v230\n\t"
"v_accvgpr_write_b32 a20, v234\n\t"
"v_mfma_f32_16x16x32_bf16 a[76:79], a[240:243], a[192:195], a[76:79]\n\t"
"v_accvgpr_read_b32 v224, a24\n\t"
"v_accvgpr_read_b32 v226, a28\n\t"
"v_accvgpr_read_b32 v225, a25\n\t"
"v_accvgpr_read_b32 v227, a29\n\t"
"v_cvt_pk_bf16_f32 v236, v236, v237\n\t"
"v_cvt_pk_bf16_f32 v98, v98, v99\n\t"
"v_pk_fma_f32 v[224:225], v[226:227], s[56:57], v[224:225]\n\t"
"v_pk_fma_f32 v[226:227], v[224:225], s[58:59], v[226:227] neg_lo:[0,0,1] neg_hi:[0,0,1]\n\t"
"v_pk_fma_f32 v[228:229], v[24:25], s[60:61], v[224:225]\n\t"
"v_pk_fma_f32 v[24:25], v[224:225], s[60:61], v[24:25]\n\t"
"v_pk_fma_f32 v[224:225], v[124:125], s[62:63], v[226:227]\n\t"
"v_pk_fma_f32 v[124:125], v[226:227], s[62:63], v[124:125]\n\t"
"v_mfma_f32_16x16x32_bf16 a[84:87], a[232:235], a[188:191], a[84:87]\n\t"
"v_accvgpr_write_b32 a17, v236\n\t"
"v_accvgpr_write_b32 a21, v98\n\t"
"v_mfma_f32_16x16x32_bf16 a[84:87], a[236:239], a[192:195], a[84:87]\n\t"
"v_accvgpr_read_b32 v230, a26\n\t"
"v_accvgpr_read_b32 v232, a30\n\t"
"v_mfma_f32_16x16x32_bf16 a[84:87], a[240:243], a[196:199], a[84:87]\n\t"
"v_accvgpr_read_b32 v231, a27\n\t"
"v_accvgpr_read_b32 v233, a31\n\t"
"v_cvt_pk_bf16_f32 v224, v224, v225\n\t"
"v_cvt_pk_bf16_f32 v228, v228, v229\n\t"
"v_accvgpr_write_b32 a24, v224\n\t"
"v_pk_fma_f32 v[230:231], v[232:233], s[56:57], v[230:231]\n\t"
"v_pk_fma_f32 v[232:233], v[230:231], s[58:59], v[232:233] neg_lo:[0,0,1] neg_hi:[0,0,1]\n\t"
"v_pk_fma_f32 v[234:235], v[26:27], s[60:61], v[230:231]\n\t"
"v_pk_fma_f32 v[26:27], v[230:231], s[60:61], v[26:27]\n\t"
"v_pk_fma_f32 v[230:231], v[126:127], s[62:63], v[232:233]\n\t"
"v_pk_fma_f32 v[126:127], v[232:233], s[62:63], v[126:127]\n\t"
"v_mfma_f32_16x16x32_bf16 a[92:95], a[232:235], a[192:195], a[92:95]\n\t"
"v_accvgpr_write_b32 a28, v228\n\t"
"v_accvgpr_read_b32 v236, a32\n\t"
"v_mfma_f32_16x16x32_bf16 a[92:95], a[236:239], a[196:199], a[92:95]\n\t"
"v_accvgpr_read_b32 v238, a36\n\t"
"v_accvgpr_read_b32 v237, a33\n\t"
"v_mfma_f32_16x16x32_bf16 a[92:95], a[240:243], a[200:203], a[92:95]\n\t"
"v_accvgpr_read_b32 v239, a37\n\t"
"v_cvt_pk_bf16_f32 v230, v230, v231\n\t"
"v_cvt_pk_bf16_f32 v234, v234, v235\n\t"
"v_accvgpr_write_b32 a25, v230\n\t"
"v_accvgpr_write_b32 a29, v234\n\t"
"v_pk_fma_f32 v[236:237], v[238:239], s[56:57], v[236:237]\n\t"
"v_pk_fma_f32 v[238:239], v[236:237], s[58:59], v[238:239] neg_lo:[0,0,1] neg_hi:[0,0,1]\n\t"
"v_pk_fma_f32 v[98:99], v[28:29], s[60:61], v[236:237]\n\t"
"v_pk_fma_f32 v[28:29], v[236:237], s[60:61], v[28:29]\n\t"
"v_pk_fma_f32 v[236:237], v[128:129], s[62:63], v[238:239]\n\t"
"v_pk_fma_f32 v[128:129], v[238:239], s[62:63], v[128:129]\n\t"
"v_mfma_f32_16x16x32_bf16 a[100:103], a[232:235], a[196:199], a[100:103]\n\t"
"s_waitcnt vmcnt(7)\n\t"
"v_accvgpr_read_b32 v224, a34\n\t"
"v_accvgpr_read_b32 v226, a38\n\t"
"v_mfma_f32_16x16x32_bf16 a[100:103], a[236:239], a[200:203], a[100:103]\n\t"
"v_accvgpr_read_b32 v225, a35\n\t"
"v_accvgpr_read_b32 v227, a39\n\t"
"v_mfma_f32_16x16x32_bf16 a[100:103], a[240:243], a[204:207], a[100:103]\n\t"
"buffer_load_dwordx4 a[232:235], v252, s[8:11], s20 offen offset:1024 sc1\n\t"
"v_cvt_pk_bf16_f32 v236, v236, v237\n\t"
"v_cvt_pk_bf16_f32 v98, v98, v99\n\t"
"v_pk_fma_f32 v[224:225], v[226:227], s[56:57], v[224:225]\n\t"
"v_pk_fma_f32 v[226:227], v[224:225], s[58:59], v[226:227] neg_lo:[0,0,1] neg_hi:[0,0,1]\n\t"
"v_pk_fma_f32 v[228:229], v[30:31], s[60:61], v[224:225]\n\t"
"v_pk_fma_f32 v[30:31], v[224:225], s[60:61], v[30:31]\n\t"
"v_pk_fma_f32 v[224:225], v[130:131], s[62:63], v[226:227]\n\t"
"v_pk_fma_f32 v[130:131], v[226:227], s[62:63], v[130:131]\n\t"
"v_mfma_f32_16x16x32_bf16 a[108:111], a[208:211], a[168:171], a[108:111]\n\t"
"ds_read_b128 a[168:171], v255 offset:512\n\t"
"v_accvgpr_write_b32 a32, v236\n\t"
"v_mfma_f32_16x16x32_bf16 a[108:111], a[212:215], a[172:175], a[108:111]\n\t"
"v_accvgpr_write_b32 a36, v98\n\t"
"v_accvgpr_read_b32 v230, a40\n\t"
"v_mfma_f32_16x16x32_bf16 a[108:111], a[216:219], a[176:179], a[108:111]\n\t"
"buffer_load_dwordx4 a[236:239], v252, s[8:11], s20 offen offset:2048 sc1\n\t"
"v_accvgpr_read_b32 v232, a44\n\t"
"v_accvgpr_read_b32 v231, a41\n\t"
"v_accvgpr_read_b32 v233, a45\n\t"
"v_cvt_pk_bf16_f32 v224, v224, v225\n\t"
"v_cvt_pk_bf16_f32 v228, v228, v229\n\t"
"s_mov_b32 s20, 1658880\n\t"
"v_pk_fma_f32 v[230:231], v[232:233], s[56:57], v[230:231]\n\t"
"v_pk_fma_f32 v[232:233], v[230:231], s[58:59], v[232:233] neg_lo:[0,0,1] neg_hi:[0,0,1]\n\t"
"v_pk_fma_f32 v[234:235], v[32:33], s[60:61], v[230:231]\n\t"
"v_pk_fma_f32 v[32:33], v[230:231], s[60:61], v[32:33]\n\t"
"v_pk_fma_f32 v[230:231], v[132:133], s[62:63], v[232:233]\n\t"
"v_pk_fma_f32 v[132:133], v[232:233], s[62:63], v[132:133]\n\t"
"v_mfma_f32_16x16x32_bf16 a[116:119], a[208:211], a[172:175], a[116:119]\n\t"
"ds_read_b128 a[172:175], v255 offset:3584\n\t"
"v_accvgpr_write_b32 a33, v224\n\t"
"v_mfma_f32_16x16x32_bf16 a[116:119], a[212:215], a[176:179], a[116:119]\n\t"
"v_accvgpr_write_b32 a37, v228\n\t"
"v_accvgpr_read_b32 v236, a42\n\t"
"v_mfma_f32_16x16x32_bf16 a[116:119], a[216:219], a[180:183], a[116:119]\n\t"
"buffer_load_dwordx4 a[240:243], v252, s[8:11], s20 offen offset:0 sc1\n\t"
"v_accvgpr_read_b32 v238, a46\n\t"
"v_accvgpr_read_b32 v237, a43\n\t"
"v_accvgpr_read_b32 v239, a47\n\t"
"v_pk_fma_f32 v[236:237], v[238:239], s[56:57], v[236:237]\n\t"
"v_pk_fma_f32 v[238:239], v[236:237], s[58:59], v[238:239] neg_lo:[0,0,1] neg_hi:[0,0,1]\n\t"
"v_pk_fma_f32 v[98:99], v[34:35], s[60:61], v[236:237]\n\t"
"v_pk_fma_f32 v[34:35], v[236:237], s[60:61], v[34:35]\n\t"
"v_pk_fma_f32 v[236:237], v[134:135], s[62:63], v[238:239]\n\t"
"v_pk_fma_f32 v[134:135], v[238:239], s[62:63], v[134:135]\n\t"
"v_mfma_f32_16x16x32_bf16 a[124:127], a[208:211], a[176:179], a[124:127]\n\t"
"ds_read_b128 a[176:179], v255 offset:6656\n\t"
"v_cvt_pk_bf16_f32 v198, v230, v231\n\t"
"v_mfma_f32_16x16x32_bf16 a[124:127], a[212:215], a[180:183], a[124:127]\n\t"
"v_cvt_pk_bf16_f32 v202, v234, v235\n\t"
"v_accvgpr_read_b32 v224, a48\n\t"
"v_mfma_f32_16x16x32_bf16 a[124:127], a[216:219], a[184:187], a[124:127]\n\t"
"global_load_dwordx4 v[248:251], v252, s[48:49] offset:0\n\t"
"v_accvgpr_read_b32 v226, a52\n\t"
"v_accvgpr_read_b32 v225, a49\n\t"
"v_accvgpr_read_b32 v227, a53\n\t"
"v_accvgpr_read_b32 v230, a50\n\t"
"v_accvgpr_read_b32 v232, a54\n\t"
"v_accvgpr_read_b32 v231, a51\n\t"
"v_accvgpr_read_b32 v233, a55\n\t"
"v_pk_fma_f32 v[224:225], v[226:227], s[56:57], v[224:225]\n\t"
"v_pk_fma_f32 v[226:227], v[224:225], s[58:59], v[226:227] neg_lo:[0,0,1] neg_hi:[0,0,1]\n\t"
"v_pk_fma_f32 v[228:229], v[36:37], s[60:61], v[224:225]\n\t"
"v_pk_fma_f32 v[36:37], v[224:225], s[60:61], v[36:37]\n\t"
"v_pk_fma_f32 v[224:225], v[136:137], s[62:63], v[226:227]\n\t"
"v_pk_fma_f32 v[136:137], v[226:227], s[62:63], v[136:137]\n\t"
"v_pk_fma_f32 v[230:231], v[232:233], s[56:57], v[230:231]\n\t"
"v_pk_fma_f32 v[232:233], v[230:231], s[58:59], v[232:233] neg_lo:[0,0,1] neg_hi:[0,0,1]\n\t"
"v_pk_fma_f32 v[234:235], v[38:39], s[60:61], v[230:231]\n\t"
"v_pk_fma_f32 v[38:39], v[230:231], s[60:61], v[38:39]\n\t"
"v_pk_fma_f32 v[230:231], v[138:139], s[62:63], v[232:233]\n\t"
"v_pk_fma_f32 v[138:139], v[232:233], s[62:63], v[138:139]\n\t"
"v_mfma_f32_16x16x32_bf16 a[132:135], a[208:211], a[180:183], a[132:135]\n\t"
"ds_read_b128 a[180:183], v255 offset:9728\n\t"
"v_cvt_pk_bf16_f32 v199, v236, v237\n\t"
"v_mfma_f32_16x16x32_bf16 a[132:135], a[212:215], a[184:187], a[132:135]\n\t"
"v_cvt_pk_bf16_f32 v203, v98, v99\n\t"
"v_accvgpr_read_b32 v236, a56\n\t"
"v_mfma_f32_16x16x32_bf16 a[132:135], a[216:219], a[188:191], a[132:135]\n\t"
"buffer_load_dwordx4 a[40:43], v252, s[8:11], s20 offen offset:1024 sc1\n\t"
"v_accvgpr_read_b32 v238, a60\n\t"
"v_accvgpr_read_b32 v237, a57\n\t"
"v_accvgpr_read_b32 v239, a61\n\t"
"v_pk_fma_f32 v[236:237], v[238:239], s[56:57], v[236:237]\n\t"
"v_pk_fma_f32 v[238:239], v[236:237], s[58:59], v[238:239] neg_lo:[0,0,1] neg_hi:[0,0,1]\n\t"
"v_pk_fma_f32 v[98:99], v[40:41], s[60:61], v[236:237]\n\t"
"v_pk_fma_f32 v[40:41], v[236:237], s[60:61], v[40:41]\n\t"
"v_pk_fma_f32 v[236:237], v[140:141], s[62:63], v[238:239]\n\t"
"v_pk_fma_f32 v[140:141], v[238:239], s[62:63], v[140:141]\n\t"
"v_mfma_f32_16x16x32_bf16 a[140:143], a[208:211], a[184:187], a[140:143]\n\t"
"ds_read_b128 a[184:187], v255 offset:12800\n\t"
"v_cvt_pk_bf16_f32 v206, v224, v225\n\t"
"v_mfma_f32_16x16x32_bf16 a[140:143], a[212:215], a[188:191], a[140:143]\n\t"
"v_cvt_pk_bf16_f32 v210, v228, v229\n\t"
"v_cvt_pk_bf16_f32 v207, v230, v231\n\t"
"v_mfma_f32_16x16x32_bf16 a[140:143], a[216:219], a[192:195], a[140:143]\n\t"
"buffer_load_dwordx4 a[48:51], v252, s[8:11], s20 offen offset:2048 sc1\n\t"
"v_accvgpr_read_b32 v224, a58\n\t"
"v_accvgpr_read_b32 v226, a62\n\t"
"v_accvgpr_read_b32 v225, a59\n\t"
"v_accvgpr_read_b32 v227, a63\n\t"
"v_cvt_pk_bf16_f32 v211, v234, v235\n\t"
"s_mov_b32 s20, 1671168\n\t"
"v_pk_fma_f32 v[224:225], v[226:227], s[56:57], v[224:225]\n\t"
"v_pk_fma_f32 v[226:227], v[224:225], s[58:59], v[226:227] neg_lo:[0,0,1] neg_hi:[0,0,1]\n\t"
"v_pk_fma_f32 v[228:229], v[42:43], s[60:61], v[224:225]\n\t"
"v_pk_fma_f32 v[42:43], v[224:225], s[60:61], v[42:43]\n\t"
"v_pk_fma_f32 v[224:225], v[142:143], s[62:63], v[226:227]\n\t"
"v_pk_fma_f32 v[142:143], v[226:227], s[62:63], v[142:143]\n\t"
"v_mfma_f32_16x16x32_bf16 a[148:151], a[208:211], a[188:191], a[148:151]\n\t"
"ds_read_b128 a[188:191], v255 offset:15872\n\t"
"v_cvt_pk_bf16_f32 v214, v236, v237\n\t"
"v_mfma_f32_16x16x32_bf16 a[148:151], a[212:215], a[192:195], a[148:151]\n\t"
"v_cvt_pk_bf16_f32 v218, v98, v99\n\t"
"v_accvgpr_read_b32 v230, a64\n\t"
"v_mfma_f32_16x16x32_bf16 a[148:151], a[216:219], a[196:199], a[148:151]\n\t"
"buffer_load_dwordx4 a[52:55], v252, s[8:11], s20 offen offset:0 sc1\n\t"
"v_accvgpr_read_b32 v232, a68\n\t"
"v_accvgpr_read_b32 v231, a65\n\t"
"v_accvgpr_read_b32 v233, a69\n\t"
"v_pk_fma_f32 v[230:231], v[232:233], s[56:57], v[230:231]\n\t"
"v_pk_fma_f32 v[232:233], v[230:231], s[58:59], v[232:233] neg_lo:[0,0,1] neg_hi:[0,0,1]\n\t"
"v_pk_fma_f32 v[234:235], v[44:45], s[60:61], v[230:231]\n\t"
"v_pk_fma_f32 v[44:45], v[230:231], s[60:61], v[44:45]\n\t"
"v_pk_fma_f32 v[230:231], v[144:145], s[62:63], v[232:233]\n\t"
"v_pk_fma_f32 v[144:145], v[232:233], s[62:63], v[144:145]\n\t"
"v_mfma_f32_16x16x32_bf16 a[156:159], a[208:211], a[192:195], a[156:159]\n\t"
"ds_read_b128 a[192:195], v255 offset:18944\n\t"
"v_cvt_pk_bf16_f32 v215, v224, v225\n\t"
"v_mfma_f32_16x16x32_bf16 a[156:159], a[212:215], a[196:199], a[156:159]\n\t"
"v_cvt_pk_bf16_f32 v219, v228, v229\n\t"
"v_accvgpr_read_b32 v236, a66\n\t"
"v_mfma_f32_16x16x32_bf16 a[156:159], a[216:219], a[200:203], a[156:159]\n\t"
"buffer_load_dwordx4 a[44:47], v252, s[8:11], s20 offen offset:1024 sc1\n\t"
"v_accvgpr_read_b32 v238, a70\n\t"
"v_accvgpr_read_b32 v237, a67\n\t"
"v_accvgpr_read_b32 v239, a71\n\t"
"v_cvt_pk_bf16_f32 v230, v230, v231\n\t"
"v_cvt_pk_bf16_f32 v234, v234, v235\n\t"
"v_pk_fma_f32 v[236:237], v[238:239], s[56:57], v[236:237]\n\t"
"v_pk_fma_f32 v[238:239], v[236:237], s[58:59], v[238:239] neg_lo:[0,0,1] neg_hi:[0,0,1]\n\t"
"v_pk_fma_f32 v[98:99], v[46:47], s[60:61], v[236:237]\n\t"
"v_pk_fma_f32 v[46:47], v[236:237], s[60:61], v[46:47]\n\t"
"v_pk_fma_f32 v[236:237], v[146:147], s[62:63], v[238:239]\n\t"
"v_pk_fma_f32 v[146:147], v[238:239], s[62:63], v[146:147]\n\t"
"v_mfma_f32_16x16x32_bf16 a[164:167], a[208:211], a[196:199], a[164:167]\n\t"
"s_waitcnt vmcnt(12) lgkmcnt(4)\n\t"
"ds_read_b128 a[196:199], v255 offset:22016\n\t"
"v_accvgpr_write_b32 a2, v230\n\t"
"v_mfma_f32_16x16x32_bf16 a[164:167], a[212:215], a[200:203], a[164:167]\n\t"
"ds_read_b128 a[200:203], v255 offset:25088\n\t"
"v_accvgpr_write_b32 a6, v234\n\t"
"v_mfma_f32_16x16x32_bf16 a[164:167], a[216:219], a[204:207], a[164:167]\n\t"
"buffer_load_dwordx4 a[60:63], v252, s[8:11], s20 offen offset:2048 sc1\n\t"
"ds_read_b128 a[204:207], v255 offset:28160\n\t"
"v_accvgpr_read_b32 v224, a72\n\t"
"v_accvgpr_read_b32 v226, a76\n\t"
"v_accvgpr_read_b32 v225, a73\n\t"
"v_accvgpr_read_b32 v227, a77\n\t"
"v_cvt_pk_bf16_f32 v236, v236, v237\n\t"
"v_cvt_pk_bf16_f32 v98, v98, v99\n\t"
"s_mov_b32 s20, 1683456\n\t"
"v_pk_fma_f32 v[224:225], v[226:227], s[56:57], v[224:225]\n\t"
"v_pk_fma_f32 v[226:227], v[224:225], s[58:59], v[226:227] neg_lo:[0,0,1] neg_hi:[0,0,1]\n\t"
"v_pk_fma_f32 v[228:229], v[48:49], s[60:61], v[224:225]\n\t"
"v_pk_fma_f32 v[48:49], v[224:225], s[60:61], v[48:49]\n\t"
"v_pk_fma_f32 v[224:225], v[148:149], s[62:63], v[226:227]\n\t"
"v_pk_fma_f32 v[148:149], v[226:227], s[62:63], v[148:149]\n\t"
"Lst14_%=:\n\t"
"v_mfma_f32_16x16x32_bf16 v[0:3], a[220:223], a[168:171], v[0:3]\n\t"
"s_waitcnt lgkmcnt(6)\n\t"
"v_accvgpr_write_b32 a3, v236\n\t"
"v_accvgpr_write_b32 a7, v98\n\t"
"v_mfma_f32_16x16x32_bf16 v[0:3], a[228:231], a[172:175], v[0:3]\n\t"
"v_accvgpr_read_b32 v230, a74\n\t"
"v_accvgpr_read_b32 v232, a78\n\t"
"v_mfma_f32_16x16x32_bf16 v[0:3], a[244:247], a[176:179], v[0:3]\n\t"
"buffer_load_dwordx4 a[64:67], v252, s[8:11], s20 offen offset:0 sc1\n\t"
"v_accvgpr_read_b32 v231, a75\n\t"
"v_accvgpr_read_b32 v233, a79\n\t"
"v_cvt_pk_bf16_f32 v224, v224, v225\n\t"
"v_cvt_pk_bf16_f32 v228, v228, v229\n\t"
"v_pk_fma_f32 v[230:231], v[232:233], s[56:57], v[230:231]\n\t"
"v_pk_fma_f32 v[232:233], v[230:231], s[58:59], v[232:233] neg_lo:[0,0,1] neg_hi:[0,0,1]\n\t"
"v_pk_fma_f32 v[234:235], v[50:51], s[60:61], v[230:231]\n\t"
"v_pk_fma_f32 v[50:51], v[230:231], s[60:61], v[50:51]\n\t"
"v_pk_fma_f32 v[230:231], v[150:151], s[62:63], v[232:233]\n\t"
"v_pk_fma_f32 v[150:151], v[232:233], s[62:63], v[150:151]\n\t"
"v_mfma_f32_16x16x32_bf16 v[4:7], a[220:223], a[172:175], v[4:7]\n\t"
"s_waitcnt lgkmcnt(5)\n\t"
"v_accvgpr_write_b32 a10, v224\n\t"
"v_accvgpr_write_b32 a14, v228\n\t"
"v_mfma_f32_16x16x32_bf16 v[4:7], a[228:231], a[176:179], v[4:7]\n\t"
"v_accvgpr_read_b32 v236, a80\n\t"
"v_accvgpr_read_b32 v238, a84\n\t"
"v_mfma_f32_16x16x32_bf16 v[4:7], a[244:247], a[180:183], v[4:7]\n\t"
"buffer_load_dwordx4 a[68:71], v252, s[8:11], s20 offen offset:1024 sc1\n\t"
"v_accvgpr_read_b32 v237, a81\n\t"
"v_accvgpr_read_b32 v239, a85\n\t"
"v_cvt_pk_bf16_f32 v230, v230, v231\n\t"
"v_cvt_pk_bf16_f32 v234, v234, v235\n\t"
"v_pk_fma_f32 v[236:237], v[238:239], s[56:57], v[236:237]\n\t"
"v_pk_fma_f32 v[238:239], v[236:237], s[58:59], v[238:239] neg_lo:[0,0,1] neg_hi:[0,0,1]\n\t"
"v_pk_fma_f32 v[98:99], v[52:53], s[60:61], v[236:237]\n\t"
"v_pk_fma_f32 v[52:53], v[236:237], s[60:61], v[52:53]\n\t"
"v_pk_fma_f32 v[236:237], v[152:153], s[62:63], v[238:239]\n\t"
"v_pk_fma_f32 v[152:153], v[238:239], s[62:63], v[152:153]\n\t"
"v_mfma_f32_16x16x32_bf16 v[8:11], a[220:223], a[176:179], v[8:11]\n\t"
"s_waitcnt lgkmcnt(4)\n\t"
"v_accvgpr_write_b32 a11, v230\n\t"
"v_accvgpr_write_b32 a15, v234\n\t"
"v_mfma_f32_16x16x32_bf16 v[8:11], a[228:231], a[180:183], v[8:11]\n\t"
"v_accvgpr_read_b32 v224, a82\n\t"
"v_accvgpr_read_b32 v226, a86\n\t"
"v_mfma_f32_16x16x32_bf16 v[8:11], a[244:247], a[184:187], v[8:11]\n\t"
"buffer_load_dwordx4 a[72:75], v252, s[8:11], s20 offen offset:2048 sc1\n\t"
"v_accvgpr_read_b32 v225, a83\n\t"
"v_accvgpr_read_b32 v227, a87\n\t"
"v_cvt_pk_bf16_f32 v236, v236, v237\n\t"
"v_cvt_pk_bf16_f32 v98, v98, v99\n\t"
"s_mov_b32 s20, 589824\n\t"
"v_pk_fma_f32 v[224:225], v[226:227], s[56:57], v[224:225]\n\t"
"v_pk_fma_f32 v[226:227], v[224:225], s[58:59], v[226:227] neg_lo:[0,0,1] neg_hi:[0,0,1]\n\t"
"v_pk_fma_f32 v[228:229], v[54:55], s[60:61], v[224:225]\n\t"
"v_pk_fma_f32 v[54:55], v[224:225], s[60:61], v[54:55]\n\t"
"v_pk_fma_f32 v[224:225], v[154:155], s[62:63], v[226:227]\n\t"
"v_pk_fma_f32 v[154:155], v[226:227], s[62:63], v[154:155]\n\t"
"v_mfma_f32_16x16x32_bf16 v[12:15], a[220:223], a[180:183], v[12:15]\n\t"
"s_waitcnt lgkmcnt(3)\n\t"
"v_accvgpr_write_b32 a18, v236\n\t"
"v_accvgpr_write_b32 a22, v98\n\t"
"v_mfma_f32_16x16x32_bf16 v[12:15], a[228:231], a[184:187], v[12:15]\n\t"
"v_accvgpr_read_b32 v230, a88\n\t"
"v_accvgpr_read_b32 v232, a92\n\t"
"v_mfma_f32_16x16x32_bf16 v[12:15], a[244:247], a[188:191], v[12:15]\n\t"
"buffer_load_dwordx4 a[76:79], v252, s[8:11], s20 offen offset:0 sc1\n\t"
"v_accvgpr_read_b32 v231, a89\n\t"
"v_accvgpr_read_b32 v233, a93\n\t"
"v_accvgpr_read_b32 v236, a90\n\t"
"v_accvgpr_read_b32 v238, a94\n\t"
"v_accvgpr_read_b32 v237, a91\n\t"
"v_accvgpr_read_b32 v239, a95\n\t"
"v_cvt_pk_bf16_f32 v224, v224, v225\n\t"
"v_cvt_pk_bf16_f32 v228, v228, v229\n\t"
"v_pk_fma_f32 v[230:231], v[232:233], s[56:57], v[230:231]\n\t"
"v_pk_fma_f32 v[232:233], v[230:231], s[58:59], v[232:233] neg_lo:[0,0,1] neg_hi:[0,0,1]\n\t"
"v_pk_fma_f32 v[234:235], v[56:57], s[60:61], v[230:231]\n\t"
"v_pk_fma_f32 v[56:57], v[230:231], s[60:61], v[56:57]\n\t"
"v_pk_fma_f32 v[230:231], v[156:157], s[62:63], v[232:233]\n\t"
"v_pk_fma_f32 v[156:157], v[232:233], s[62:63], v[156:157]\n\t"
"v_pk_fma_f32 v[236:237], v[238:239], s[56:57], v[236:237]\n\t"
"v_pk_fma_f32 v[238:239], v[236:237], s[58:59], v[238:239] neg_lo:[0,0,1] neg_hi:[0,0,1]\n\t"
"v_pk_fma_f32 v[98:99], v[58:59], s[60:61], v[236:237]\n\t"
"v_pk_fma_f32 v[58:59], v[236:237], s[60:61], v[58:59]\n\t"
"v_pk_fma_f32 v[236:237], v[158:159], s[62:63], v[238:239]\n\t"
"v_pk_fma_f32 v[158:159], v[238:239], s[62:63], v[158:159]\n\t"
"v_mfma_f32_16x16x32_bf16 v[16:19], a[220:223], a[184:187], v[16:19]\n\t"
"s_waitcnt lgkmcnt(2)\n\t"
"v_accvgpr_write_b32 a19, v224\n\t"
"v_accvgpr_write_b32 a23, v228\n\t"
"v_mfma_f32_16x16x32_bf16 v[16:19], a[228:231], a[188:191], v[16:19]\n\t"
"v_accvgpr_read_b32 v224, a96\n\t"
"v_accvgpr_read_b32 v226, a100\n\t"
"v_mfma_f32_16x16x32_bf16 v[16:19], a[244:247], a[192:195], v[16:19]\n\t"
"buffer_load_dwordx4 a[84:87], v252, s[8:11], s20 offen offset:1024 sc1\n\t"
"v_accvgpr_read_b32 v225, a97\n\t"
"v_accvgpr_read_b32 v227, a101\n\t"
"v_cvt_pk_bf16_f32 v230, v230, v231\n\t"
"v_cvt_pk_bf16_f32 v234, v234, v235\n\t"
"v_pk_fma_f32 v[224:225], v[226:227], s[56:57], v[224:225]\n\t"
"v_pk_fma_f32 v[226:227], v[224:225], s[58:59], v[226:227] neg_lo:[0,0,1] neg_hi:[0,0,1]\n\t"
"v_pk_fma_f32 v[228:229], v[60:61], s[60:61], v[224:225]\n\t"
"v_pk_fma_f32 v[60:61], v[224:225], s[60:61], v[60:61]\n\t"
"v_pk_fma_f32 v[224:225], v[160:161], s[62:63], v[226:227]\n\t"
"v_pk_fma_f32 v[160:161], v[226:227], s[62:63], v[160:161]\n\t"
"v_mfma_f32_16x16x32_bf16 v[20:23], a[220:223], a[188:191], v[20:23]\n\t"
"s_waitcnt lgkmcnt(1)\n\t"
"v_accvgpr_write_b32 a26, v230\n\t"
"v_cvt_pk_bf16_f32 v236, v236, v237\n\t"
"v_mfma_f32_16x16x32_bf16 v[20:23], a[228:231], a[192:195], v[20:23]\n\t"
"v_accvgpr_write_b32 a30, v234\n\t"
"v_cvt_pk_bf16_f32 v98, v98, v99\n\t"
"v_mfma_f32_16x16x32_bf16 v[20:23], a[244:247], a[196:199], v[20:23]\n\t"
"buffer_load_dwordx4 a[88:91], v252, s[8:11], s20 offen offset:2048 sc1\n\t"
"v_accvgpr_read_b32 v230, a98\n\t"
"v_accvgpr_read_b32 v232, a102\n\t"
"v_accvgpr_read_b32 v231, a99\n\t"
"v_accvgpr_read_b32 v233, a103\n\t"
"v_accvgpr_write_b32 a27, v236\n\t"
"v_accvgpr_write_b32 a31, v98\n\t"
"v_cvt_pk_bf16_f32 v224, v224, v225\n\t"
"v_cvt_pk_bf16_f32 v228, v228, v229\n\t"
"s_mov_b32 s20, 602112\n\t"
"v_pk_fma_f32 v[230:231], v[232:233], s[56:57], v[230:231]\n\t"
"v_pk_fma_f32 v[232:233], v[230:231], s[58:59], v[232:233] neg_lo:[0,0,1] neg_hi:[0,0,1]\n\t"
"v_pk_fma_f32 v[234:235], v[62:63], s[60:61], v[230:231]\n\t"
"v_pk_fma_f32 v[62:63], v[230:231], s[60:61], v[62:63]\n\t"
"v_pk_fma_f32 v[230:231], v[162:163], s[62:63], v[232:233]\n\t"
"v_pk_fma_f32 v[162:163], v[232:233], s[62:63], v[162:163]\n\t"
"v_mfma_f32_16x16x32_bf16 v[24:27], a[220:223], a[192:195], v[24:27]\n\t"
"s_waitcnt lgkmcnt(0)\n\t"
"v_accvgpr_write_b32 a34, v224\n\t"
"v_accvgpr_write_b32 a38, v228\n\t"
"v_mfma_f32_16x16x32_bf16 v[24:27], a[228:231], a[196:199], v[24:27]\n\t"
"v_accvgpr_read_b32 v236, a104\n\t"
"v_accvgpr_read_b32 v238, a108\n\t"
"v_mfma_f32_16x16x32_bf16 v[24:27], a[244:247], a[200:203], v[24:27]\n\t"
"buffer_load_dwordx4 a[92:95], v252, s[8:11], s20 offen offset:0 sc1\n\t"
"v_accvgpr_read_b32 v237, a105\n\t"
"v_accvgpr_read_b32 v239, a109\n\t"
"v_cvt_pk_bf16_f32 v230, v230, v231\n\t"
"v_cvt_pk_bf16_f32 v234, v234, v235\n\t"
"v_pk_fma_f32 v[236:237], v[238:239], s[56:57], v[236:237]\n\t"
"v_pk_fma_f32 v[238:239], v[236:237], s[58:59], v[238:239] neg_lo:[0,0,1] neg_hi:[0,0,1]\n\t"
"v_pk_fma_f32 v[98:99], v[64:65], s[60:61], v[236:237]\n\t"
"v_pk_fma_f32 v[64:65], v[236:237], s[60:61], v[64:65]\n\t"
"v_pk_fma_f32 v[236:237], v[164:165], s[62:63], v[238:239]\n\t"
"v_pk_fma_f32 v[164:165], v[238:239], s[62:63], v[164:165]\n\t"
"v_mfma_f32_16x16x32_bf16 v[28:31], a[220:223], a[196:199], v[28:31]\n\t"
"s_waitcnt vmcnt(17)\n\t"
"v_accvgpr_write_b32 a35, v230\n\t"
"v_accvgpr_write_b32 a39, v234\n\t"
"v_mfma_f32_16x16x32_bf16 v[28:31], a[228:231], a[200:203], v[28:31]\n\t"
"v_accvgpr_read_b32 v224, a106\n\t"
"v_accvgpr_read_b32 v226, a110\n\t"
"v_mfma_f32_16x16x32_bf16 v[28:31], a[244:247], a[204:207], v[28:31]\n\t"
"buffer_load_dwordx4 a[96:99], v252, s[8:11], s20 offen offset:1024 sc1\n\t"
"v_accvgpr_read_b32 v225, a107\n\t"
"v_accvgpr_read_b32 v227, a111\n\t"
"v_cvt_pk_bf16_f32 v236, v236, v237\n\t"
"v_cvt_pk_bf16_f32 v98, v98, v99\n\t"
"v_pk_fma_f32 v[224:225], v[226:227], s[56:57], v[224:225]\n\t"
"v_pk_fma_f32 v[226:227], v[224:225], s[58:59], v[226:227] neg_lo:[0,0,1] neg_hi:[0,0,1]\n\t"
"v_pk_fma_f32 v[228:229], v[66:67], s[60:61], v[224:225]\n\t"
"v_pk_fma_f32 v[66:67], v[224:225], s[60:61], v[66:67]\n\t"
"v_pk_fma_f32 v[224:225], v[166:167], s[62:63], v[226:227]\n\t"
"v_pk_fma_f32 v[166:167], v[226:227], s[62:63], v[166:167]\n\t"
"v_mfma_f32_16x16x32_bf16 v[32:35], v[240:243], a[168:171], v[32:35]\n\t"
"v_accvgpr_write_b32 a104, v236\n\t"
"v_accvgpr_write_b32 a106, v98\n\t"
"v_mfma_f32_16x16x32_bf16 v[32:35], a[224:227], a[172:175], v[32:35]\n\t"
"v_accvgpr_read_b32 v230, a112\n\t"
"v_accvgpr_read_b32 v232, a116\n\t"
"v_mfma_f32_16x16x32_bf16 v[32:35], a[248:251], a[176:179], v[32:35]\n\t"
"buffer_load_dwordx4 a[100:103], v252, s[8:11], s20 offen offset:2048 sc1\n\t"
"v_accvgpr_read_b32 v231, a113\n\t"
"v_accvgpr_read_b32 v233, a117\n\t"
"v_cvt_pk_bf16_f32 v224, v224, v225\n\t"
"v_cvt_pk_bf16_f32 v228, v228, v229\n\t"
"s_mov_b32 s20, 614400\n\t"
"v_pk_fma_f32 v[230:231], v[232:233], s[56:57], v[230:231]\n\t"
"v_pk_fma_f32 v[232:233], v[230:231], s[58:59], v[232:233] neg_lo:[0,0,1] neg_hi:[0,0,1]\n\t"
"v_pk_fma_f32 v[234:235], v[68:69], s[60:61], v[230:231]\n\t"
"v_pk_fma_f32 v[68:69], v[230:231], s[60:61], v[68:69]\n\t"
"v_pk_fma_f32 v[230:231], v[168:169], s[62:63], v[232:233]\n\t"
"v_pk_fma_f32 v[168:169], v[232:233], s[62:63], v[168:169]\n\t"
"v_mfma_f32_16x16x32_bf16 v[36:39], v[240:243], a[172:175], v[36:39]\n\t"
"v_accvgpr_write_b32 a105, v224\n\t"
"v_accvgpr_write_b32 a107, v228\n\t"
"v_mfma_f32_16x16x32_bf16 v[36:39], a[224:227], a[176:179], v[36:39]\n\t"
"v_accvgpr_read_b32 v236, a114\n\t"
"v_accvgpr_read_b32 v238, a118\n\t"
"v_mfma_f32_16x16x32_bf16 v[36:39], a[248:251], a[180:183], v[36:39]\n\t"
"buffer_load_dwordx4 a[208:211], v252, s[8:11], s20 offen offset:0 sc1\n\t"
"v_accvgpr_read_b32 v237, a115\n\t"
"v_accvgpr_read_b32 v239, a119\n\t"
"v_cvt_pk_bf16_f32 v230, v230, v231\n\t"
"v_cvt_pk_bf16_f32 v234, v234, v235\n\t"
"v_pk_fma_f32 v[236:237], v[238:239], s[56:57], v[236:237]\n\t"
"v_pk_fma_f32 v[238:239], v[236:237], s[58:59], v[238:239] neg_lo:[0,0,1] neg_hi:[0,0,1]\n\t"
"v_pk_fma_f32 v[98:99], v[70:71], s[60:61], v[236:237]\n\t"
"v_pk_fma_f32 v[70:71], v[236:237], s[60:61], v[70:71]\n\t"
"v_pk_fma_f32 v[236:237], v[170:171], s[62:63], v[238:239]\n\t"
"v_pk_fma_f32 v[170:171], v[238:239], s[62:63], v[170:171]\n\t"
"v_mfma_f32_16x16x32_bf16 v[40:43], v[240:243], a[176:179], v[40:43]\n\t"
"v_accvgpr_write_b32 a112, v230\n\t"
"v_accvgpr_write_b32 a114, v234\n\t"
"v_mfma_f32_16x16x32_bf16 v[40:43], a[224:227], a[180:183], v[40:43]\n\t"
"v_accvgpr_read_b32 v224, a120\n\t"
"v_accvgpr_read_b32 v226, a124\n\t"
"v_mfma_f32_16x16x32_bf16 v[40:43], a[248:251], a[184:187], v[40:43]\n\t"
"buffer_load_dwordx4 a[212:215], v252, s[8:11], s20 offen offset:1024 sc1\n\t"
"v_accvgpr_read_b32 v225, a121\n\t"
"v_accvgpr_read_b32 v227, a125\n\t"
"v_cvt_pk_bf16_f32 v236, v236, v237\n\t"
"v_cvt_pk_bf16_f32 v98, v98, v99\n\t"
"v_pk_fma_f32 v[224:225], v[226:227], s[56:57], v[224:225]\n\t"
"v_pk_fma_f32 v[226:227], v[224:225], s[58:59], v[226:227] neg_lo:[0,0,1] neg_hi:[0,0,1]\n\t"
"v_pk_fma_f32 v[228:229], v[72:73], s[60:61], v[224:225]\n\t"
"v_pk_fma_f32 v[72:73], v[224:225], s[60:61], v[72:73]\n\t"
"v_pk_fma_f32 v[224:225], v[172:173], s[62:63], v[226:227]\n\t"
"v_pk_fma_f32 v[172:173], v[226:227], s[62:63], v[172:173]\n\t"
"v_mfma_f32_16x16x32_bf16 v[44:47], v[240:243], a[180:183], v[44:47]\n\t"
"v_accvgpr_write_b32 a113, v236\n\t"
"v_accvgpr_write_b32 a115, v98\n\t"
"v_mfma_f32_16x16x32_bf16 v[44:47], a[224:227], a[184:187], v[44:47]\n\t"
"v_accvgpr_read_b32 v230, a122\n\t"
"v_accvgpr_read_b32 v232, a126\n\t"
"v_mfma_f32_16x16x32_bf16 v[44:47], a[248:251], a[188:191], v[44:47]\n\t"
"buffer_load_dwordx4 a[116:119], v252, s[8:11], s20 offen offset:2048 sc1\n\t"
"v_accvgpr_read_b32 v231, a123\n\t"
"v_accvgpr_read_b32 v233, a127\n\t"
"v_accvgpr_read_b32 v236, a128\n\t"
"v_accvgpr_read_b32 v238, a132\n\t"
"v_accvgpr_read_b32 v237, a129\n\t"
"v_accvgpr_read_b32 v239, a133\n\t"
"v_cvt_pk_bf16_f32 v224, v224, v225\n\t"
"v_cvt_pk_bf16_f32 v228, v228, v229\n\t"
"s_mov_b32 s20, 774144\n\t"
"v_pk_fma_f32 v[230:231], v[232:233], s[56:57], v[230:231]\n\t"
"v_pk_fma_f32 v[232:233], v[230:231], s[58:59], v[232:233] neg_lo:[0,0,1] neg_hi:[0,0,1]\n\t"
"v_pk_fma_f32 v[234:235], v[74:75], s[60:61], v[230:231]\n\t"
"v_pk_fma_f32 v[74:75], v[230:231], s[60:61], v[74:75]\n\t"
"v_pk_fma_f32 v[230:231], v[174:175], s[62:63], v[232:233]\n\t"
"v_pk_fma_f32 v[174:175], v[232:233], s[62:63], v[174:175]\n\t"
"v_pk_fma_f32 v[236:237], v[238:239], s[56:57], v[236:237]\n\t"
"v_pk_fma_f32 v[238:239], v[236:237], s[58:59], v[238:239] neg_lo:[0,0,1] neg_hi:[0,0,1]\n\t"
"v_pk_fma_f32 v[98:99], v[76:77], s[60:61], v[236:237]\n\t"
"v_pk_fma_f32 v[76:77], v[236:237], s[60:61], v[76:77]\n\t"
"v_pk_fma_f32 v[236:237], v[176:177], s[62:63], v[238:239]\n\t"
"v_pk_fma_f32 v[176:177], v[238:239], s[62:63], v[176:177]\n\t"
"v_mfma_f32_16x16x32_bf16 v[48:51], v[240:243], a[184:187], v[48:51]\n\t"
"v_accvgpr_write_b32 a120, v224\n\t"
"v_accvgpr_write_b32 a122, v228\n\t"
"v_mfma_f32_16x16x32_bf16 v[48:51], a[224:227], a[188:191], v[48:51]\n\t"
"v_accvgpr_read_b32 v224, a130\n\t"
"v_accvgpr_read_b32 v226, a134\n\t"
"v_mfma_f32_16x16x32_bf16 v[48:51], a[248:251], a[192:195], v[48:51]\n\t"
"buffer_load_dwordx4 a[216:219], v252, s[8:11], s20 offen offset:0 sc1\n\t"
"v_accvgpr_read_b32 v225, a131\n\t"
"v_accvgpr_read_b32 v227, a135\n\t"
"v_cvt_pk_bf16_f32 v230, v230, v231\n\t"
"v_cvt_pk_bf16_f32 v234, v234, v235\n\t"
"v_cvt_pk_bf16_f32 v236, v236, v237\n\t"
"v_cvt_pk_bf16_f32 v98, v98, v99\n\t"
"Lpg11_%=:\n\t"
"s_cbranch_vccnz Lvr0_%=\n\t"
"v_pk_fma_f32 v[224:225], v[226:227], s[56:57], v[224:225]\n\t"
"v_pk_fma_f32 v[226:227], v[224:225], s[58:59], v[226:227] neg_lo:[0,0,1] neg_hi:[0,0,1]\n\t"
"v_pk_fma_f32 v[228:229], v[78:79], s[60:61], v[224:225]\n\t"
"v_pk_fma_f32 v[78:79], v[224:225], s[60:61], v[78:79]\n\t"
"v_pk_fma_f32 v[224:225], v[178:179], s[62:63], v[226:227]\n\t"
"v_pk_fma_f32 v[178:179], v[226:227], s[62:63], v[178:179]\n\t"
"v_mfma_f32_16x16x32_bf16 v[52:55], v[240:243], a[188:191], v[52:55]\n\t"
"v_accvgpr_write_b32 a121, v230\n\t"
"v_accvgpr_write_b32 a123, v234\n\t"
"v_mfma_f32_16x16x32_bf16 v[52:55], a[224:227], a[192:195], v[52:55]\n\t"
"v_accvgpr_write_b32 a128, v236\n\t"
"v_accvgpr_write_b32 a130, v98\n\t"
"v_mfma_f32_16x16x32_bf16 v[52:55], a[248:251], a[196:199], v[52:55]\n\t"
"buffer_load_dwordx4 a[124:127], v252, s[8:11], s20 offen offset:1024 sc1\n\t"
"v_accvgpr_read_b32 v230, a136\n\t"
"v_accvgpr_read_b32 v232, a140\n\t"
"v_accvgpr_read_b32 v231, a137\n\t"
"v_accvgpr_read_b32 v233, a141\n\t"
"v_cvt_pk_bf16_f32 v224, v224, v225\n\t"
"v_cvt_pk_bf16_f32 v228, v228, v229\n\t"
"v_pk_fma_f32 v[230:231], v[232:233], s[56:57], v[230:231]\n\t"
"v_pk_fma_f32 v[232:233], v[230:231], s[58:59], v[232:233] neg_lo:[0,0,1] neg_hi:[0,0,1]\n\t"
"v_pk_fma_f32 v[234:235], v[80:81], s[60:61], v[230:231]\n\t"
"v_pk_fma_f32 v[80:81], v[230:231], s[60:61], v[80:81]\n\t"
"v_pk_fma_f32 v[230:231], v[180:181], s[62:63], v[232:233]\n\t"
"v_pk_fma_f32 v[180:181], v[232:233], s[62:63], v[180:181]\n\t"
"v_mfma_f32_16x16x32_bf16 v[56:59], v[240:243], a[192:195], v[56:59]\n\t"
"v_accvgpr_write_b32 a129, v224\n\t"
"v_accvgpr_write_b32 a131, v228\n\t"
"v_mfma_f32_16x16x32_bf16 v[56:59], a[224:227], a[196:199], v[56:59]\n\t"
"v_accvgpr_read_b32 v236, a138\n\t"
"v_accvgpr_read_b32 v238, a142\n\t"
"v_mfma_f32_16x16x32_bf16 v[56:59], a[248:251], a[200:203], v[56:59]\n\t"
"s_waitcnt vmcnt(19)\n\t"
"ds_write_b128 v249, v[196:199] offset:0\n\t"
"v_accvgpr_read_b32 v237, a139\n\t"
"v_accvgpr_read_b32 v239, a143\n\t"
"v_cvt_pk_bf16_f32 v230, v230, v231\n\t"
"v_cvt_pk_bf16_f32 v234, v234, v235\n\t"
"v_pk_fma_f32 v[236:237], v[238:239], s[56:57], v[236:237]\n\t"
"v_pk_fma_f32 v[238:239], v[236:237], s[58:59], v[238:239] neg_lo:[0,0,1] neg_hi:[0,0,1]\n\t"
"v_pk_fma_f32 v[98:99], v[82:83], s[60:61], v[236:237]\n\t"
"v_pk_fma_f32 v[82:83], v[236:237], s[60:61], v[82:83]\n\t"
"v_pk_fma_f32 v[236:237], v[182:183], s[62:63], v[238:239]\n\t"
"v_pk_fma_f32 v[182:183], v[238:239], s[62:63], v[182:183]\n\t"
"v_mfma_f32_16x16x32_bf16 v[60:63], v[240:243], a[196:199], v[60:63]\n\t"
"v_accvgpr_write_b32 a136, v230\n\t"
"v_accvgpr_write_b32 a138, v234\n\t"
"v_mfma_f32_16x16x32_bf16 v[60:63], a[224:227], a[200:203], v[60:63]\n\t"
"v_accvgpr_read_b32 v224, a144\n\t"
"v_accvgpr_read_b32 v226, a148\n\t"
"v_mfma_f32_16x16x32_bf16 v[60:63], a[248:251], a[204:207], v[60:63]\n\t"
"buffer_load_dwordx4 a[132:135], v252, s[8:11], s20 offen offset:2048 sc1\n\t"
"v_accvgpr_read_b32 v225, a145\n\t"
"v_accvgpr_read_b32 v227, a149\n\t"
"v_cvt_pk_bf16_f32 v236, v236, v237\n\t"
"v_cvt_pk_bf16_f32 v98, v98, v99\n\t"
"s_mov_b32 s20, 786432\n\t"
"v_pk_fma_f32 v[224:225], v[226:227], s[56:57], v[224:225]\n\t"
"v_pk_fma_f32 v[226:227], v[224:225], s[58:59], v[226:227] neg_lo:[0,0,1] neg_hi:[0,0,1]\n\t"
"v_pk_fma_f32 v[228:229], v[84:85], s[60:61], v[224:225]\n\t"
"v_pk_fma_f32 v[84:85], v[224:225], s[60:61], v[84:85]\n\t"
"v_pk_fma_f32 v[224:225], v[184:185], s[62:63], v[226:227]\n\t"
"v_pk_fma_f32 v[184:185], v[226:227], s[62:63], v[184:185]\n\t"
"v_mfma_f32_16x16x32_bf16 v[64:67], a[252:255], a[168:171], v[64:67]\n\t"
"ds_read_b128 a[168:171], v255 offset:2048\n\t"
"v_accvgpr_write_b32 a137, v236\n\t"
"v_mfma_f32_16x16x32_bf16 v[64:67], a[232:235], a[172:175], v[64:67]\n\t"
"v_accvgpr_write_b32 a139, v98\n\t"
"v_accvgpr_read_b32 v230, a146\n\t"
"v_mfma_f32_16x16x32_bf16 v[64:67], a[236:239], a[176:179], v[64:67]\n\t"
"buffer_load_dwordx4 a[220:223], v252, s[8:11], s20 offen offset:0 sc1\n\t"
"v_accvgpr_read_b32 v232, a150\n\t"
"v_accvgpr_read_b32 v231, a147\n\t"
"v_accvgpr_read_b32 v233, a151\n\t"
"v_cvt_pk_bf16_f32 v224, v224, v225\n\t"
"v_cvt_pk_bf16_f32 v228, v228, v229\n\t"
"v_pk_fma_f32 v[230:231], v[232:233], s[56:57], v[230:231]\n\t"
"v_pk_fma_f32 v[232:233], v[230:231], s[58:59], v[232:233] neg_lo:[0,0,1] neg_hi:[0,0,1]\n\t"
"v_pk_fma_f32 v[234:235], v[86:87], s[60:61], v[230:231]\n\t"
"v_pk_fma_f32 v[86:87], v[230:231], s[60:61], v[86:87]\n\t"
"v_pk_fma_f32 v[230:231], v[186:187], s[62:63], v[232:233]\n\t"
"v_pk_fma_f32 v[186:187], v[232:233], s[62:63], v[186:187]\n\t"
"v_mfma_f32_16x16x32_bf16 v[68:71], a[252:255], a[172:175], v[68:71]\n\t"
"ds_read_b128 a[172:175], v255 offset:5120\n\t"
"v_accvgpr_write_b32 a144, v224\n\t"
"v_mfma_f32_16x16x32_bf16 v[68:71], a[232:235], a[176:179], v[68:71]\n\t"
"v_accvgpr_write_b32 a146, v228\n\t"
"v_accvgpr_read_b32 v236, a152\n\t"
"v_mfma_f32_16x16x32_bf16 v[68:71], a[236:239], a[180:183], v[68:71]\n\t"
"ds_write_b64 v250, a[104:105] offset:0\n\t"
"v_accvgpr_read_b32 v238, a156\n\t"
"v_accvgpr_read_b32 v237, a153\n\t"
"v_accvgpr_read_b32 v239, a157\n\t"
"v_cvt_pk_bf16_f32 v230, v230, v231\n\t"
"v_cvt_pk_bf16_f32 v234, v234, v235\n\t"
"v_pk_fma_f32 v[236:237], v[238:239], s[56:57], v[236:237]\n\t"
"v_pk_fma_f32 v[238:239], v[236:237], s[58:59], v[238:239] neg_lo:[0,0,1] neg_hi:[0,0,1]\n\t"
"v_pk_fma_f32 v[98:99], v[88:89], s[60:61], v[236:237]\n\t"
"v_pk_fma_f32 v[88:89], v[236:237], s[60:61], v[88:89]\n\t"
"v_pk_fma_f32 v[236:237], v[188:189], s[62:63], v[238:239]\n\t"
"v_pk_fma_f32 v[188:189], v[238:239], s[62:63], v[188:189]\n\t"
"v_mfma_f32_16x16x32_bf16 v[72:75], a[252:255], a[176:179], v[72:75]\n\t"
"ds_read_b128 a[176:179], v255 offset:8192\n\t"
"v_accvgpr_write_b32 a145, v230\n\t"
"v_mfma_f32_16x16x32_bf16 v[72:75], a[232:235], a[180:183], v[72:75]\n\t"
"v_accvgpr_write_b32 a147, v234\n\t"
"v_accvgpr_read_b32 v224, a154\n\t"
"v_mfma_f32_16x16x32_bf16 v[72:75], a[236:239], a[184:187], v[72:75]\n\t"
"buffer_load_dwordx4 v[240:243], v252, s[8:11], s20 offen offset:1024 sc1\n\t"
"v_accvgpr_read_b32 v226, a158\n\t"
"v_accvgpr_read_b32 v225, a155\n\t"
"v_accvgpr_read_b32 v227, a159\n\t"
"v_cvt_pk_bf16_f32 v236, v236, v237\n\t"
"v_cvt_pk_bf16_f32 v98, v98, v99\n\t"
"v_pk_fma_f32 v[224:225], v[226:227], s[56:57], v[224:225]\n\t"
"v_pk_fma_f32 v[226:227], v[224:225], s[58:59], v[226:227] neg_lo:[0,0,1] neg_hi:[0,0,1]\n\t"
"v_pk_fma_f32 v[228:229], v[90:91], s[60:61], v[224:225]\n\t"
"v_pk_fma_f32 v[90:91], v[224:225], s[60:61], v[90:91]\n\t"
"v_pk_fma_f32 v[224:225], v[190:191], s[62:63], v[226:227]\n\t"
"v_pk_fma_f32 v[190:191], v[226:227], s[62:63], v[190:191]\n\t"
"v_mfma_f32_16x16x32_bf16 v[76:79], a[252:255], a[180:183], v[76:79]\n\t"
"ds_read_b128 a[180:183], v255 offset:11264\n\t"
"v_accvgpr_write_b32 a152, v236\n\t"
"v_mfma_f32_16x16x32_bf16 v[76:79], a[232:235], a[184:187], v[76:79]\n\t"
"v_accvgpr_write_b32 a154, v98\n\t"
"v_accvgpr_read_b32 v230, a160\n\t"
"v_mfma_f32_16x16x32_bf16 v[76:79], a[236:239], a[188:191], v[76:79]\n\t"
"buffer_load_dwordx4 a[140:143], v252, s[8:11], s20 offen offset:2048 sc1\n\t"
"v_accvgpr_read_b32 v232, a164\n\t"
"v_accvgpr_read_b32 v231, a161\n\t"
"v_accvgpr_read_b32 v233, a165\n\t"
"v_cvt_pk_bf16_f32 v224, v224, v225\n\t"
"v_cvt_pk_bf16_f32 v228, v228, v229\n\t"
"s_mov_b32 s20, 798720\n\t"
"v_pk_fma_f32 v[230:231], v[232:233], s[56:57], v[230:231]\n\t"
"v_pk_fma_f32 v[232:233], v[230:231], s[58:59], v[232:233] neg_lo:[0,0,1] neg_hi:[0,0,1]\n\t"
"v_pk_fma_f32 v[234:235], v[92:93], s[60:61], v[230:231]\n\t"
"v_pk_fma_f32 v[92:93], v[230:231], s[60:61], v[92:93]\n\t"
"v_pk_fma_f32 v[230:231], v[192:193], s[62:63], v[232:233]\n\t"
"v_pk_fma_f32 v[192:193], v[232:233], s[62:63], v[192:193]\n\t"
"v_mfma_f32_16x16x32_bf16 v[80:83], a[252:255], a[184:187], v[80:83]\n\t"
"ds_read_b128 a[184:187], v255 offset:14336\n\t"
"v_accvgpr_write_b32 a153, v224\n\t"
"v_mfma_f32_16x16x32_bf16 v[80:83], a[232:235], a[188:191], v[80:83]\n\t"
"v_accvgpr_write_b32 a155, v228\n\t"
"v_accvgpr_read_b32 v236, a162\n\t"
"v_mfma_f32_16x16x32_bf16 v[80:83], a[236:239], a[192:195], v[80:83]\n\t"
"buffer_load_dwordx4 a[148:151], v252, s[8:11], s20 offen offset:0 sc1\n\t"
"v_accvgpr_read_b32 v238, a166\n\t"
"v_accvgpr_read_b32 v237, a163\n\t"
"v_accvgpr_read_b32 v239, a167\n\t"
"v_cvt_pk_bf16_f32 v230, v230, v231\n\t"
"v_cvt_pk_bf16_f32 v234, v234, v235\n\t"
"v_pk_fma_f32 v[236:237], v[238:239], s[56:57], v[236:237]\n\t"
"v_pk_fma_f32 v[238:239], v[236:237], s[58:59], v[238:239] neg_lo:[0,0,1] neg_hi:[0,0,1]\n\t"
"v_pk_fma_f32 v[98:99], v[94:95], s[60:61], v[236:237]\n\t"
"v_pk_fma_f32 v[94:95], v[236:237], s[60:61], v[94:95]\n\t"
"v_pk_fma_f32 v[236:237], v[194:195], s[62:63], v[238:239]\n\t"
"v_pk_fma_f32 v[194:195], v[238:239], s[62:63], v[194:195]\n\t"
"v_mfma_f32_16x16x32_bf16 v[84:87], a[252:255], a[188:191], v[84:87]\n\t"
"ds_read_b128 a[188:191], v255 offset:17408\n\t"
"v_accvgpr_write_b32 a160, v230\n\t"
"v_mfma_f32_16x16x32_bf16 v[84:87], a[232:235], a[192:195], v[84:87]\n\t"
"v_accvgpr_write_b32 a162, v234\n\t"
"v_cvt_pk_bf16_f32 v236, v236, v237\n\t"
"v_mfma_f32_16x16x32_bf16 v[84:87], a[236:239], a[196:199], v[84:87]\n\t"
"buffer_load_dwordx4 a[224:227], v252, s[8:11], s20 offen offset:1024 sc1\n\t"
"v_cvt_pk_bf16_f32 v98, v98, v99\n\t"
"v_mfma_f32_16x16x32_bf16 v[88:91], a[252:255], a[192:195], v[88:91]\n\t"
"ds_read_b128 a[192:195], v255 offset:20480\n\t"
"v_accvgpr_write_b32 a161, v236\n\t"
"v_mfma_f32_16x16x32_bf16 v[88:91], a[232:235], a[196:199], v[88:91]\n\t"
"v_accvgpr_write_b32 a163, v98\n\t"
"v_mfma_f32_16x16x32_bf16 v[88:91], a[236:239], a[200:203], v[88:91]\n\t"
"buffer_load_dwordx4 a[228:231], v252, s[8:11], s20 offen offset:2048 sc1\n\t"
"ds_write_b128 v249, v[200:203] offset:6144\n\t"
"s_mov_b32 s20, 884736\n\t"
"v_mfma_f32_16x16x32_bf16 v[92:95], a[252:255], a[196:199], v[92:95]\n\t"
"buffer_load_dwordx4 a[244:247], v252, s[8:11], s20 offen offset:0 sc1\n\t"
"v_mfma_f32_16x16x32_bf16 v[92:95], a[232:235], a[200:203], v[92:95]\n\t"
"buffer_load_dwordx4 a[164:167], v252, s[8:11], s20 offen offset:1024 sc1\n\t"
"v_mfma_f32_16x16x32_bf16 v[92:95], a[236:239], a[204:207], v[92:95]\n\t"
"s_waitcnt vmcnt(26) lgkmcnt(5)\n\t"
"ds_write_b64 v250, a[106:107] offset:6144\n\t"
"v_mfma_f32_16x16x32_bf16 v[100:103], a[240:243], a[168:171], v[100:103]\n\t"
"buffer_load_dwordx4 a[248:251], v252, s[8:11], s20 offen offset:2048 sc1\n\t"
"v_mfma_f32_16x16x32_bf16 v[100:103], a[40:43], a[172:175], v[100:103]\n\t"
"s_waitcnt lgkmcnt(5)\n\t"
"ds_read_b128 a[196:199], v255 offset:23552\n\t"
"s_mov_b32 s20, 897024\n\t"
"v_mfma_f32_16x16x32_bf16 v[100:103], a[48:51], a[176:179], v[100:103]\n\t"
"s_add_u32 m0, s25, 512\n\t"
"ds_read_b128 a[200:203], v255 offset:26624\n\t"
"v_mfma_f32_16x16x32_bf16 v[104:107], a[240:243], a[172:175], v[104:107]\n\t"
"s_waitcnt lgkmcnt(6)\n\t"
"ds_read_b128 a[204:207], v255 offset:29696\n\t"
"v_mfma_f32_16x16x32_bf16 v[104:107], a[40:43], a[176:179], v[104:107]\n\t"
"v_mfma_f32_16x16x32_bf16 v[104:107], a[48:51], a[180:183], v[104:107]\n\t"
"s_waitcnt lgkmcnt(0)\n\t"
"s_barrier\n\t"
"v_mfma_f32_16x16x32_bf16 v[108:111], a[240:243], a[176:179], v[108:111]\n\t"
"v_mfma_f32_16x16x32_bf16 v[108:111], a[40:43], a[180:183], v[108:111]\n\t"
"buffer_load_dwordx4 v[236:239], v252, s[8:11], s20 offen offset:0 sc1\n\t"
"v_mfma_f32_16x16x32_bf16 v[108:111], a[48:51], a[184:187], v[108:111]\n\t"
"buffer_load_dwordx4 v[96:99], v252, s[8:11], s20 offen offset:1024 sc1\n\t"
"v_mfma_f32_16x16x32_bf16 v[112:115], a[240:243], a[180:183], v[112:115]\n\t"
"buffer_load_dwordx4 v254, s[12:15], 0 offen offset:0 nt lds\n\t"
"v_mfma_f32_16x16x32_bf16 v[112:115], a[40:43], a[184:187], v[112:115]\n\t"
"buffer_load_dwordx4 a[232:235], v252, s[8:11], s20 offen offset:2048 sc1\n\t"
"v_mfma_f32_16x16x32_bf16 v[112:115], a[48:51], a[188:191], v[112:115]\n\t"
"s_add_u32 m0, s24, 512\n\t"
"s_add_u32 s28, s21, 0\n\t"
"ds_write_b128 v249, v[204:207] offset:12288\n\t"
"v_mfma_f32_16x16x32_bf16 v[116:119], a[240:243], a[184:187], v[116:119]\n\t"
"buffer_load_dwordx4 v247, s[12:15], s28 offen offset:0 nt lds\n\t"
"v_mfma_f32_16x16x32_bf16 v[116:119], a[40:43], a[188:191], v[116:119]\n\t"
"s_add_u32 m0, s24, 6656\n\t"
"s_add_u32 s28, s21, 196608\n\t"
"v_mfma_f32_16x16x32_bf16 v[116:119], a[48:51], a[192:195], v[116:119]\n\t"
"buffer_load_dwordx4 v247, s[12:15], s28 offen offset:0 nt lds\n\t"
"s_add_u32 m0, s24, 12800\n\t"
"s_add_u32 s28, s21, 393216\n\t"
"v_mfma_f32_16x16x32_bf16 v[120:123], a[240:243], a[188:191], v[120:123]\n\t"
"buffer_load_dwordx4 v247, s[12:15], s28 offen offset:0 nt lds\n\t"
"v_mfma_f32_16x16x32_bf16 v[120:123], a[40:43], a[192:195], v[120:123]\n\t"
"s_add_u32 m0, s24, 20480\n\t"
"s_add_u32 s28, s21, 589824\n\t"
"v_mfma_f32_16x16x32_bf16 v[120:123], a[48:51], a[196:199], v[120:123]\n\t"
"buffer_load_dwordx4 v247, s[12:15], s28 offen offset:0 nt lds\n\t"
"v_mfma_f32_16x16x32_bf16 v[124:127], a[240:243], a[192:195], v[124:127]\n\t"
"v_mfma_f32_16x16x32_bf16 v[124:127], a[40:43], a[196:199], v[124:127]\n\t"
"v_mfma_f32_16x16x32_bf16 v[124:127], a[48:51], a[200:203], v[124:127]\n\t"
"ds_write_b64 v250, a[112:113] offset:12288\n\t"
"v_mfma_f32_16x16x32_bf16 v[128:131], a[240:243], a[196:199], v[128:131]\n\t"
"s_waitcnt vmcnt(32)\n\t"
"v_mfma_f32_16x16x32_bf16 v[128:131], a[40:43], a[200:203], v[128:131]\n\t"
"v_mfma_f32_16x16x32_bf16 v[128:131], a[48:51], a[204:207], v[128:131]\n\t"
"ds_write_b128 v249, v[208:211] offset:18432\n\t"
"v_mfma_f32_16x16x32_bf16 v[132:135], a[52:55], a[168:171], v[132:135]\n\t"
"v_mfma_f32_16x16x32_bf16 v[132:135], a[44:47], a[172:175], v[132:135]\n\t"
"v_mfma_f32_16x16x32_bf16 v[132:135], a[60:63], a[176:179], v[132:135]\n\t"
"ds_write_b64 v250, a[114:115] offset:18432\n\t"
"v_mfma_f32_16x16x32_bf16 v[136:139], a[52:55], a[172:175], v[136:139]\n\t"
"v_mfma_f32_16x16x32_bf16 v[136:139], a[44:47], a[176:179], v[136:139]\n\t"
"v_mfma_f32_16x16x32_bf16 v[136:139], a[60:63], a[180:183], v[136:139]\n\t"
"v_mfma_f32_16x16x32_bf16 v[140:143], a[52:55], a[176:179], v[140:143]\n\t"
"v_mfma_f32_16x16x32_bf16 v[140:143], a[44:47], a[180:183], v[140:143]\n\t"
"v_mfma_f32_16x16x32_bf16 v[140:143], a[60:63], a[184:187], v[140:143]\n\t"
"s_mov_b32 s20, 909312\n\t"
"v_mfma_f32_16x16x32_bf16 v[144:147], a[52:55], a[180:183], v[144:147]\n\t"
"v_mfma_f32_16x16x32_bf16 v[144:147], a[44:47], a[184:187], v[144:147]\n\t"
"v_mfma_f32_16x16x32_bf16 v[144:147], a[60:63], a[188:191], v[144:147]\n\t"
"v_mfma_f32_16x16x32_bf16 v[148:151], a[52:55], a[184:187], v[148:151]\n\t"
"v_mfma_f32_16x16x32_bf16 v[148:151], a[44:47], a[188:191], v[148:151]\n\t"
"v_mfma_f32_16x16x32_bf16 v[148:151], a[60:63], a[192:195], v[148:151]\n\t"
"buffer_load_dwordx4 a[104:107], v252, s[8:11], s20 offen offset:0 sc1\n\t"
"v_mfma_f32_16x16x32_bf16 v[152:155], a[52:55], a[188:191], v[152:155]\n\t"
"ds_read_b128 a[40:43], v252 offset:0\n\t"
"v_mfma_f32_16x16x32_bf16 v[152:155], a[44:47], a[192:195], v[152:155]\n\t"
"ds_read_b128 a[48:51], v252 offset:1024\n\t"
"v_mfma_f32_16x16x32_bf16 v[152:155], a[60:63], a[196:199], v[152:155]\n\t"
"v_mfma_f32_16x16x32_bf16 v[156:159], a[52:55], a[192:195], v[156:159]\n\t"
"s_add_u32 m0, s24, 26624\n\t"
"s_add_u32 s28, s21, 786432\n\t"
"ds_read_b128 a[56:59], v252 offset:2048\n\t"
"v_mfma_f32_16x16x32_bf16 v[156:159], a[44:47], a[196:199], v[156:159]\n\t"
"v_mfma_f32_16x16x32_bf16 v[156:159], a[60:63], a[200:203], v[156:159]\n\t"
"v_mfma_f32_16x16x32_bf16 v[160:163], a[52:55], a[196:199], v[160:163]\n\t"
"s_waitcnt vmcnt(30)\n\t"
"v_mfma_f32_16x16x32_bf16 v[160:163], a[44:47], a[200:203], v[160:163]\n\t"
"buffer_load_dwordx4 v247, s[12:15], s28 offen offset:0 nt lds\n\t"
"v_mfma_f32_16x16x32_bf16 v[160:163], a[60:63], a[204:207], v[160:163]\n\t"
"s_add_u32 m0, s24, 32768\n\t"
"s_add_u32 s28, s21, 983040\n\t"
"v_mfma_f32_16x16x32_bf16 v[164:167], a[64:67], a[168:171], v[164:167]\n\t"
"buffer_load_dwordx4 v247, s[12:15], s28 offen offset:0 nt lds\n\t"
"v_mfma_f32_16x16x32_bf16 v[164:167], a[68:71], a[172:175], v[164:167]\n\t"
"s_add_u32 m0, s24, 40448\n\t"
"s_add_u32 s28, s21, 1179648\n\t"
"ds_read_b128 a[168:171], v244 offset:24576\n\t"
"v_mfma_f32_16x16x32_bf16 v[164:167], a[72:75], a[176:179], v[164:167]\n\t"
"buffer_load_dwordx4 v247, s[12:15], s28 offen offset:0 nt lds\n\t"
"v_mfma_f32_16x16x32_bf16 v[168:171], a[64:67], a[172:175], v[168:171]\n\t"
"s_add_u32 m0, s24, 46592\n\t"
"s_add_u32 s28, s21, 1376256\n\t"
"ds_read_b128 a[172:175], v244 offset:25088\n\t"
"v_mfma_f32_16x16x32_bf16 v[168:171], a[68:71], a[176:179], v[168:171]\n\t"
"buffer_load_dwordx4 v247, s[12:15], s28 offen offset:0 nt lds\n\t"
"v_mfma_f32_16x16x32_bf16 v[168:171], a[72:75], a[180:183], v[168:171]\n\t"
"s_add_u32 m0, s24, 52736\n\t"
"s_add_u32 s28, s21, 1572864\n\t"
"v_mfma_f32_16x16x32_bf16 v[172:175], a[64:67], a[176:179], v[172:175]\n\t"
"buffer_load_dwordx4 v247, s[12:15], s28 offen offset:0 nt lds\n\t"
"v_mfma_f32_16x16x32_bf16 v[172:175], a[68:71], a[180:183], v[172:175]\n\t"
"ds_read_b128 a[176:179], v244 offset:25600\n\t"
"v_mfma_f32_16x16x32_bf16 v[172:175], a[72:75], a[184:187], v[172:175]\n\t"
"v_mfma_f32_16x16x32_bf16 v[176:179], a[64:67], a[180:183], v[176:179]\n\t"
"ds_read_b128 a[180:183], v244 offset:26112\n\t"
"v_mfma_f32_16x16x32_bf16 v[176:179], a[68:71], a[184:187], v[176:179]\n\t"
"v_mfma_f32_16x16x32_bf16 v[176:179], a[72:75], a[188:191], v[176:179]\n\t"
"s_waitcnt lgkmcnt(4)\n\t"
"s_barrier\n\t"
"v_mfma_f32_16x16x32_bf16 v[180:183], a[64:67], a[184:187], v[180:183]\n\t"
"ds_read_b128 a[184:187], v244 offset:26624\n\t"
"v_mfma_f32_16x16x32_bf16 v[180:183], a[68:71], a[188:191], v[180:183]\n\t"
"v_mfma_f32_16x16x32_bf16 v[180:183], a[72:75], a[192:195], v[180:183]\n\t"
"ds_write_b128 v249, v[212:215] offset:0\n\t"
"v_mfma_f32_16x16x32_bf16 v[184:187], a[64:67], a[188:191], v[184:187]\n\t"
"v_mfma_f32_16x16x32_bf16 v[184:187], a[68:71], a[192:195], v[184:187]\n\t"
"ds_read_b128 a[188:191], v244 offset:27136\n\t"
"v_mfma_f32_16x16x32_bf16 v[184:187], a[72:75], a[196:199], v[184:187]\n\t"
"ds_write_b64 v250, a[120:121] offset:0\n\t"
"v_mfma_f32_16x16x32_bf16 v[188:191], a[64:67], a[192:195], v[188:191]\n\t"
"ds_read_b128 a[192:195], v244 offset:27648\n\t"
"v_mfma_f32_16x16x32_bf16 v[188:191], a[68:71], a[196:199], v[188:191]\n\t"
"v_mfma_f32_16x16x32_bf16 v[188:191], a[72:75], a[200:203], v[188:191]\n\t"
"ds_write_b128 v249, v[216:219] offset:6144\n\t"
"v_mfma_f32_16x16x32_bf16 v[192:195], a[64:67], a[196:199], v[192:195]\n\t"
"s_waitcnt vmcnt(32) lgkmcnt(7)\n\t"
"v_mfma_f32_16x16x32_bf16 v[192:195], a[68:71], a[200:203], v[192:195]\n\t"
"ds_read_b128 a[196:199], v244 offset:28160\n\t"
"v_mfma_f32_16x16x32_bf16 v[192:195], a[72:75], a[204:207], v[192:195]\n\t"
"ds_read_b128 a[200:203], v244 offset:28672\n\t"
"Lst15_%=:\n\t"
"v_mfma_f32_16x16x32_bf16 v[0:3], a[76:79], a[168:171], v[0:3]\n\t"
"s_waitcnt lgkmcnt(8)\n\t"
"ds_read_b128 a[204:207], v244 offset:29184\n\t"
"v_mfma_f32_16x16x32_bf16 v[0:3], a[84:87], a[172:175], v[0:3]\n\t"
"v_mfma_f32_16x16x32_bf16 v[0:3], a[88:91], a[176:179], v[0:3]\n\t"
"ds_write_b64 v250, a[122:123] offset:6144\n\t"
"v_mfma_f32_16x16x32_bf16 v[4:7], a[76:79], a[172:175], v[4:7]\n\t"
"s_waitcnt lgkmcnt(9)\n\t"
"v_mfma_f32_16x16x32_bf16 v[4:7], a[84:87], a[176:179], v[4:7]\n\t"
"v_mfma_f32_16x16x32_bf16 v[4:7], a[88:91], a[180:183], v[4:7]\n\t"
"v_mfma_f32_16x16x32_bf16 v[8:11], a[76:79], a[176:179], v[8:11]\n\t"
"s_waitcnt lgkmcnt(7)\n\t"
"v_mfma_f32_16x16x32_bf16 v[8:11], a[84:87], a[180:183], v[8:11]\n\t"
"v_mfma_f32_16x16x32_bf16 v[8:11], a[88:91], a[184:187], v[8:11]\n\t"
"v_mfma_f32_16x16x32_bf16 v[12:15], a[76:79], a[180:183], v[12:15]\n\t"
"s_waitcnt lgkmcnt(5)\n\t"
"v_mfma_f32_16x16x32_bf16 v[12:15], a[84:87], a[184:187], v[12:15]\n\t"
"v_mfma_f32_16x16x32_bf16 v[12:15], a[88:91], a[188:191], v[12:15]\n\t"
"v_mfma_f32_16x16x32_bf16 v[16:19], a[76:79], a[184:187], v[16:19]\n\t"
"s_waitcnt lgkmcnt(3)\n\t"
"v_mfma_f32_16x16x32_bf16 v[16:19], a[84:87], a[188:191], v[16:19]\n\t"
"v_mfma_f32_16x16x32_bf16 v[16:19], a[88:91], a[192:195], v[16:19]\n\t"
"ds_read_b128 a[64:67], v252 offset:12288\n\t"
"v_mfma_f32_16x16x32_bf16 v[20:23], a[76:79], a[188:191], v[20:23]\n\t"
"s_waitcnt lgkmcnt(3)\n\t"
"v_mfma_f32_16x16x32_bf16 v[20:23], a[84:87], a[192:195], v[20:23]\n\t"
"ds_read_b128 a[72:75], v252 offset:13312\n\t"
"v_mfma_f32_16x16x32_bf16 v[20:23], a[88:91], a[196:199], v[20:23]\n\t"
"v_mfma_f32_16x16x32_bf16 v[24:27], a[76:79], a[192:195], v[24:27]\n\t"
"s_waitcnt lgkmcnt(3)\n\t"
"ds_read_b128 a[80:83], v252 offset:14336\n\t"
"v_mfma_f32_16x16x32_bf16 v[24:27], a[84:87], a[196:199], v[24:27]\n\t"
"v_mfma_f32_16x16x32_bf16 v[24:27], a[88:91], a[200:203], v[24:27]\n\t"
"v_mfma_f32_16x16x32_bf16 v[28:31], a[76:79], a[196:199], v[28:31]\n\t"
"s_waitcnt vmcnt(29)\n\t"
"v_mfma_f32_16x16x32_bf16 v[28:31], a[84:87], a[200:203], v[28:31]\n\t"
"v_mfma_f32_16x16x32_bf16 v[28:31], a[88:91], a[204:207], v[28:31]\n\t"
"v_mfma_f32_16x16x32_bf16 v[32:35], a[92:95], a[168:171], v[32:35]\n\t"
"v_mfma_f32_16x16x32_bf16 v[32:35], a[96:99], a[172:175], v[32:35]\n\t"
"v_mfma_f32_16x16x32_bf16 v[32:35], a[100:103], a[176:179], v[32:35]\n\t"
"v_mfma_f32_16x16x32_bf16 v[36:39], a[92:95], a[172:175], v[36:39]\n\t"
"v_mfma_f32_16x16x32_bf16 v[36:39], a[96:99], a[176:179], v[36:39]\n\t"
"v_mfma_f32_16x16x32_bf16 v[36:39], a[100:103], a[180:183], v[36:39]\n\t"
"v_mfma_f32_16x16x32_bf16 v[40:43], a[92:95], a[176:179], v[40:43]\n\t"
"v_mfma_f32_16x16x32_bf16 v[40:43], a[96:99], a[180:183], v[40:43]\n\t"
"v_mfma_f32_16x16x32_bf16 v[40:43], a[100:103], a[184:187], v[40:43]\n\t"
"v_mfma_f32_16x16x32_bf16 v[44:47], a[92:95], a[180:183], v[44:47]\n\t"
"v_mfma_f32_16x16x32_bf16 v[44:47], a[96:99], a[184:187], v[44:47]\n\t"
"v_mfma_f32_16x16x32_bf16 v[44:47], a[100:103], a[188:191], v[44:47]\n\t"
"s_waitcnt lgkmcnt(0)\n\t"
"s_barrier\n\t"
"v_mfma_f32_16x16x32_bf16 v[48:51], a[92:95], a[184:187], v[48:51]\n\t"
"v_mfma_f32_16x16x32_bf16 v[48:51], a[96:99], a[188:191], v[48:51]\n\t"
"v_mfma_f32_16x16x32_bf16 v[48:51], a[100:103], a[192:195], v[48:51]\n\t"
"ds_write_b128 v249, a[0:3] offset:12288\n\t"
"v_mfma_f32_16x16x32_bf16 v[52:55], a[92:95], a[188:191], v[52:55]\n\t"
"v_mfma_f32_16x16x32_bf16 v[52:55], a[96:99], a[192:195], v[52:55]\n\t"
"v_mfma_f32_16x16x32_bf16 v[52:55], a[100:103], a[196:199], v[52:55]\n\t"
"ds_write_b64 v250, a[128:129] offset:12288\n\t"
"v_mfma_f32_16x16x32_bf16 v[56:59], a[92:95], a[192:195], v[56:59]\n\t"
"v_mfma_f32_16x16x32_bf16 v[56:59], a[96:99], a[196:199], v[56:59]\n\t"
"v_mfma_f32_16x16x32_bf16 v[56:59], a[100:103], a[200:203], v[56:59]\n\t"
"ds_write_b128 v249, a[4:7] offset:18432\n\t"
"v_mfma_f32_16x16x32_bf16 v[60:63], a[92:95], a[196:199], v[60:63]\n\t"
"s_waitcnt vmcnt(26)\n\t"
"v_mfma_f32_16x16x32_bf16 v[60:63], a[96:99], a[200:203], v[60:63]\n\t"
"v_mfma_f32_16x16x32_bf16 v[60:63], a[100:103], a[204:207], v[60:63]\n\t"
"ds_write_b64 v250, a[130:131] offset:18432\n\t"
"v_mfma_f32_16x16x32_bf16 v[64:67], a[208:211], a[168:171], v[64:67]\n\t"
"ds_read_b128 a[168:171], v244 offset:33792\n\t"
"v_mfma_f32_16x16x32_bf16 v[64:67], a[212:215], a[172:175], v[64:67]\n\t"
"v_mfma_f32_16x16x32_bf16 v[64:67], a[116:119], a[176:179], v[64:67]\n\t"
"buffer_load_dwordx4 a[112:115], v252, s[8:11], s20 offen offset:1024 sc1\n\t"
"v_mfma_f32_16x16x32_bf16 v[68:71], a[208:211], a[172:175], v[68:71]\n\t"
"ds_read_b128 a[172:175], v244 offset:34304\n\t"
"v_mfma_f32_16x16x32_bf16 v[68:71], a[212:215], a[176:179], v[68:71]\n\t"
"v_mfma_f32_16x16x32_bf16 v[68:71], a[116:119], a[180:183], v[68:71]\n\t"
"v_mfma_f32_16x16x32_bf16 v[72:75], a[208:211], a[176:179], v[72:75]\n\t"
"ds_read_b128 a[176:179], v244 offset:34816\n\t"
"v_mfma_f32_16x16x32_bf16 v[72:75], a[212:215], a[180:183], v[72:75]\n\t"
"v_mfma_f32_16x16x32_bf16 v[72:75], a[116:119], a[184:187], v[72:75]\n\t"
"ds_read_b128 a[88:91], v252 offset:0\n\t"
"v_mfma_f32_16x16x32_bf16 v[76:79], a[208:211], a[180:183], v[76:79]\n\t"
"ds_read_b128 a[180:183], v244 offset:35328\n\t"
"v_mfma_f32_16x16x32_bf16 v[76:79], a[212:215], a[184:187], v[76:79]\n\t"
"ds_read_b128 a[96:99], v252 offset:1024\n\t"
"v_mfma_f32_16x16x32_bf16 v[76:79], a[116:119], a[188:191], v[76:79]\n\t"
"v_mfma_f32_16x16x32_bf16 v[80:83], a[208:211], a[184:187], v[80:83]\n\t"
"ds_read_b128 a[184:187], v244 offset:35840\n\t"
"v_mfma_f32_16x16x32_bf16 v[80:83], a[212:215], a[188:191], v[80:83]\n\t"
"ds_read_b128 a[108:111], v252 offset:2048\n\t"
"v_mfma_f32_16x16x32_bf16 v[80:83], a[116:119], a[192:195], v[80:83]\n\t"
"v_mfma_f32_16x16x32_bf16 v[84:87], a[208:211], a[188:191], v[84:87]\n\t"
"ds_read_b128 a[188:191], v244 offset:36352\n\t"
"v_mfma_f32_16x16x32_bf16 v[84:87], a[212:215], a[192:195], v[84:87]\n\t"
"v_mfma_f32_16x16x32_bf16 v[84:87], a[116:119], a[196:199], v[84:87]\n\t"
"v_mfma_f32_16x16x32_bf16 v[88:91], a[208:211], a[192:195], v[88:91]\n\t"
"ds_read_b128 a[192:195], v244 offset:36864\n\t"
"v_mfma_f32_16x16x32_bf16 v[88:91], a[212:215], a[196:199], v[88:91]\n\t"
"v_mfma_f32_16x16x32_bf16 v[88:91], a[116:119], a[200:203], v[88:91]\n\t"
"v_mfma_f32_16x16x32_bf16 v[92:95], a[208:211], a[196:199], v[92:95]\n\t"
"s_waitcnt vmcnt(24) lgkmcnt(7)\n\t"
"ds_read_b128 a[196:199], v244 offset:37376\n\t"
"v_mfma_f32_16x16x32_bf16 v[92:95], a[212:215], a[200:203], v[92:95]\n\t"
"ds_read_b128 a[200:203], v244 offset:37888\n\t"
"v_mfma_f32_16x16x32_bf16 v[92:95], a[116:119], a[204:207], v[92:95]\n\t"
"ds_read_b128 a[204:207], v244 offset:38400\n\t"
"v_mfma_f32_16x16x32_bf16 v[100:103], a[216:219], a[168:171], v[100:103]\n\t"
"s_waitcnt lgkmcnt(8)\n\t"
"v_mfma_f32_16x16x32_bf16 v[100:103], a[124:127], a[172:175], v[100:103]\n\t"
"v_mfma_f32_16x16x32_bf16 v[100:103], a[132:135], a[176:179], v[100:103]\n\t"
"v_mfma_f32_16x16x32_bf16 v[104:107], a[216:219], a[172:175], v[104:107]\n\t"
"s_waitcnt lgkmcnt(6)\n\t"
"v_mfma_f32_16x16x32_bf16 v[104:107], a[124:127], a[176:179], v[104:107]\n\t"
"v_mfma_f32_16x16x32_bf16 v[104:107], a[132:135], a[180:183], v[104:107]\n\t"
"s_waitcnt lgkmcnt(0)\n\t"
"s_barrier\n\t"
"v_mfma_f32_16x16x32_bf16 v[108:111], a[216:219], a[176:179], v[108:111]\n\t"
"v_mfma_f32_16x16x32_bf16 v[108:111], a[124:127], a[180:183], v[108:111]\n\t"
"v_mfma_f32_16x16x32_bf16 v[108:111], a[132:135], a[184:187], v[108:111]\n\t"
"ds_write_b128 v249, a[8:11] offset:0\n\t"
"v_mfma_f32_16x16x32_bf16 v[112:115], a[216:219], a[180:183], v[112:115]\n\t"
"v_mfma_f32_16x16x32_bf16 v[112:115], a[124:127], a[184:187], v[112:115]\n\t"
"buffer_load_dwordx4 a[44:47], v252, s[8:11], s20 offen offset:2048 sc1\n\t"
"v_mfma_f32_16x16x32_bf16 v[112:115], a[132:135], a[188:191], v[112:115]\n\t"
"s_mov_b32 s20, 1069056\n\t"
"ds_write_b64 v250, a[136:137] offset:0\n\t"
"v_mfma_f32_16x16x32_bf16 v[116:119], a[216:219], a[184:187], v[116:119]\n\t"
"v_mfma_f32_16x16x32_bf16 v[116:119], a[124:127], a[188:191], v[116:119]\n\t"
"v_mfma_f32_16x16x32_bf16 v[116:119], a[132:135], a[192:195], v[116:119]\n\t"
"buffer_load_dwordx4 a[60:63], v252, s[8:11], s20 offen offset:0 sc1\n\t"
"v_mfma_f32_16x16x32_bf16 v[120:123], a[216:219], a[188:191], v[120:123]\n\t"
"v_mfma_f32_16x16x32_bf16 v[120:123], a[124:127], a[192:195], v[120:123]\n\t"
"v_mfma_f32_16x16x32_bf16 v[120:123], a[132:135], a[196:199], v[120:123]\n\t"
"ds_write_b128 v249, a[12:15] offset:6144\n\t"
"v_mfma_f32_16x16x32_bf16 v[124:127], a[216:219], a[192:195], v[124:127]\n\t"
"buffer_load_dwordx4 a[68:71], v252, s[8:11], s20 offen offset:1024 sc1\n\t"
"v_mfma_f32_16x16x32_bf16 v[124:127], a[124:127], a[196:199], v[124:127]\n\t"
"v_mfma_f32_16x16x32_bf16 v[124:127], a[132:135], a[200:203], v[124:127]\n\t"
"ds_write_b64 v250, a[138:139] offset:6144\n\t"
"v_mfma_f32_16x16x32_bf16 v[128:131], a[216:219], a[196:199], v[128:131]\n\t"
"s_waitcnt vmcnt(24)\n\t"
"v_mfma_f32_16x16x32_bf16 v[128:131], a[124:127], a[200:203], v[128:131]\n\t"
"buffer_load_dwordx4 a[76:79], v252, s[8:11], s20 offen offset:2048 sc1\n\t"
"v_mfma_f32_16x16x32_bf16 v[128:131], a[132:135], a[204:207], v[128:131]\n\t"
"s_mov_b32 s20, 1081344\n\t"
"v_mfma_f32_16x16x32_bf16 v[132:135], a[220:223], a[168:171], v[132:135]\n\t"
"v_mfma_f32_16x16x32_bf16 v[132:135], v[240:243], a[172:175], v[132:135]\n\t"
"v_mfma_f32_16x16x32_bf16 v[132:135], a[140:143], a[176:179], v[132:135]\n\t"
"buffer_load_dwordx4 a[132:135], v252, s[8:11], s20 offen offset:0 sc1\n\t"
"v_mfma_f32_16x16x32_bf16 v[136:139], a[220:223], a[172:175], v[136:139]\n\t"
"v_mfma_f32_16x16x32_bf16 v[136:139], v[240:243], a[176:179], v[136:139]\n\t"
"v_mfma_f32_16x16x32_bf16 v[136:139], a[140:143], a[180:183], v[136:139]\n\t"
"v_mfma_f32_16x16x32_bf16 v[140:143], a[220:223], a[176:179], v[140:143]\n\t"
"buffer_load_dwordx4 a[92:95], v252, s[8:11], s20 offen offset:1024 sc1\n\t"
"v_mfma_f32_16x16x32_bf16 v[140:143], v[240:243], a[180:183], v[140:143]\n\t"
"v_mfma_f32_16x16x32_bf16 v[140:143], a[140:143], a[184:187], v[140:143]\n\t"
"v_mfma_f32_16x16x32_bf16 v[144:147], a[220:223], a[180:183], v[144:147]\n\t"
"v_mfma_f32_16x16x32_bf16 v[144:147], v[240:243], a[184:187], v[144:147]\n\t"
"buffer_load_dwordx4 a[100:103], v252, s[8:11], s20 offen offset:2048 sc1\n\t"
"v_mfma_f32_16x16x32_bf16 v[144:147], a[140:143], a[188:191], v[144:147]\n\t"
"s_mov_b32 s20, 1093632\n\t"
"v_mfma_f32_16x16x32_bf16 v[148:151], a[220:223], a[184:187], v[148:151]\n\t"
"v_mfma_f32_16x16x32_bf16 v[148:151], v[240:243], a[188:191], v[148:151]\n\t"
"v_mfma_f32_16x16x32_bf16 v[148:151], a[140:143], a[192:195], v[148:151]\n\t"
"buffer_load_dwordx4 v[200:203], v252, s[8:11], s20 offen offset:0 sc1\n\t"
"v_mfma_f32_16x16x32_bf16 v[152:155], a[220:223], a[188:191], v[152:155]\n\t"
"ds_read_b128 a[116:119], v252 offset:12288\n\t"
"v_mfma_f32_16x16x32_bf16 v[152:155], v[240:243], a[192:195], v[152:155]\n\t"
"ds_read_b128 a[124:127], v252 offset:13312\n\t"
"v_mfma_f32_16x16x32_bf16 v[152:155], a[140:143], a[196:199], v[152:155]\n\t"
"v_mfma_f32_16x16x32_bf16 v[156:159], a[220:223], a[192:195], v[156:159]\n\t"
"buffer_load_dwordx4 a[120:123], v252, s[8:11], s20 offen offset:1024 sc1\n\t"
"v_mfma_f32_16x16x32_bf16 v[156:159], v[240:243], a[196:199], v[156:159]\n\t"
"ds_read_b128 a[156:159], v252 offset:14336\n\t"
"v_mfma_f32_16x16x32_bf16 v[156:159], a[140:143], a[200:203], v[156:159]\n\t"
"v_mfma_f32_16x16x32_bf16 v[160:163], a[220:223], a[196:199], v[160:163]\n\t"
"s_waitcnt vmcnt(27)\n\t"
"v_mfma_f32_16x16x32_bf16 v[160:163], v[240:243], a[200:203], v[160:163]\n\t"
"buffer_load_dwordx4 a[128:131], v252, s[8:11], s20 offen offset:2048 sc1\n\t"
"v_mfma_f32_16x16x32_bf16 v[160:163], a[140:143], a[204:207], v[160:163]\n\t"
"s_mov_b32 s20, 1179648\n\t"
"v_mfma_f32_16x16x32_bf16 v[164:167], a[148:151], a[168:171], v[164:167]\n\t"
"ds_read_b128 a[168:171], v255 offset:0\n\t"
"v_mfma_f32_16x16x32_bf16 v[164:167], a[224:227], a[172:175], v[164:167]\n\t"
"v_mfma_f32_16x16x32_bf16 v[164:167], a[228:231], a[176:179], v[164:167]\n\t"
"buffer_load_dwordx4 a[140:143], v252, s[8:11], s20 offen offset:0 sc1\n\t"
"v_mfma_f32_16x16x32_bf16 v[168:171], a[148:151], a[172:175], v[168:171]\n\t"
"ds_read_b128 a[172:175], v255 offset:3072\n\t"
"v_mfma_f32_16x16x32_bf16 v[168:171], a[224:227], a[176:179], v[168:171]\n\t"
"v_mfma_f32_16x16x32_bf16 v[168:171], a[228:231], a[180:183], v[168:171]\n\t"
"v_mfma_f32_16x16x32_bf16 v[172:175], a[148:151], a[176:179], v[172:175]\n\t"
"buffer_load_dwordx4 v[208:211], v252, s[8:11], s20 offen offset:1024 sc1\n\t"
"v_mfma_f32_16x16x32_bf16 v[172:175], a[224:227], a[180:183], v[172:175]\n\t"
"ds_read_b128 a[176:179], v255 offset:6144\n\t"
"v_mfma_f32_16x16x32_bf16 v[172:175], a[228:231], a[184:187], v[172:175]\n\t"
"v_mfma_f32_16x16x32_bf16 v[176:179], a[148:151], a[180:183], v[176:179]\n\t"
"buffer_load_dwordx4 v[216:219], v252, s[8:11], s20 offen offset:2048 sc1\n\t"
"v_mfma_f32_16x16x32_bf16 v[176:179], a[224:227], a[184:187], v[176:179]\n\t"
"s_mov_b32 s20, 1191936\n\t"
"ds_read_b128 a[180:183], v255 offset:9216\n\t"
"v_mfma_f32_16x16x32_bf16 v[176:179], a[228:231], a[188:191], v[176:179]\n\t"
"Lpg12_%=:\n\t"
"s_cbranch_vccnz Lvr0_%=\n\t"
"s_waitcnt lgkmcnt(4)\n\t"
"s_barrier\n\t"
"v_mfma_f32_16x16x32_bf16 v[180:183], a[148:151], a[184:187], v[180:183]\n\t"
"ds_read_b128 a[184:187], v255 offset:12288\n\t"
"v_mfma_f32_16x16x32_bf16 v[180:183], a[224:227], a[188:191], v[180:183]\n\t"
"buffer_load_dwordx4 a[4:7], v252, s[8:11], s20 offen offset:0 sc1\n\t"
"v_mfma_f32_16x16x32_bf16 v[180:183], a[228:231], a[192:195], v[180:183]\n\t"
"ds_write_b128 v249, a[16:19] offset:12288\n\t"
"v_mfma_f32_16x16x32_bf16 v[184:187], a[148:151], a[188:191], v[184:187]\n\t"
"buffer_load_dwordx4 a[12:15], v252, s[8:11], s20 offen offset:1024 sc1\n\t"
"v_mfma_f32_16x16x32_bf16 v[184:187], a[224:227], a[192:195], v[184:187]\n\t"
"ds_read_b128 a[188:191], v255 offset:15360\n\t"
"v_mfma_f32_16x16x32_bf16 v[184:187], a[228:231], a[196:199], v[184:187]\n\t"
"ds_write_b64 v250, a[144:145] offset:12288\n\t"
"v_mfma_f32_16x16x32_bf16 v[188:191], a[148:151], a[192:195], v[188:191]\n\t"
"ds_read_b128 a[192:195], v255 offset:18432\n\t"
"v_mfma_f32_16x16x32_bf16 v[188:191], a[224:227], a[196:199], v[188:191]\n\t"
"buffer_load_dwordx4 a[208:211], v252, s[8:11], s20 offen offset:2048 sc1\n\t"
"v_mfma_f32_16x16x32_bf16 v[188:191], a[228:231], a[200:203], v[188:191]\n\t"
"s_mov_b32 s20, 1204224\n\t"
"ds_write_b128 v249, a[20:23] offset:18432\n\t"
"v_mfma_f32_16x16x32_bf16 v[192:195], a[148:151], a[196:199], v[192:195]\n\t"
"s_waitcnt vmcnt(31) lgkmcnt(7)\n\t"
"v_mfma_f32_16x16x32_bf16 v[192:195], a[224:227], a[200:203], v[192:195]\n\t"
"ds_read_b128 a[196:199], v255 offset:21504\n\t"
"v_mfma_f32_16x16x32_bf16 v[192:195], a[228:231], a[204:207], v[192:195]\n\t"
"buffer_load_dwordx4 a[212:215], v252, s[8:11], s20 offen offset:0 sc1\n\t"
"Lst16_%=:\n\t"
"v_mfma_f32_16x16x32_bf16 v[0:3], a[244:247], a[168:171], v[0:3]\n\t"
"s_waitcnt lgkmcnt(7)\n\t"
"ds_read_b128 a[200:203], v255 offset:24576\n\t"
"v_mfma_f32_16x16x32_bf16 v[0:3], a[164:167], a[172:175], v[0:3]\n\t"
"ds_read_b128 a[204:207], v255 offset:27648\n\t"
"v_mfma_f32_16x16x32_bf16 v[0:3], a[248:251], a[176:179], v[0:3]\n\t"
"ds_write_b64 v250, a[146:147] offset:18432\n\t"
"v_mfma_f32_16x16x32_bf16 v[4:7], a[244:247], a[172:175], v[4:7]\n\t"
"buffer_load_dwordx4 a[216:219], v252, s[8:11], s20 offen offset:1024 sc1\n\t"
"v_mfma_f32_16x16x32_bf16 v[4:7], a[164:167], a[176:179], v[4:7]\n\t"
"s_waitcnt lgkmcnt(9)\n\t"
"v_mfma_f32_16x16x32_bf16 v[4:7], a[248:251], a[180:183], v[4:7]\n\t"
"v_mfma_f32_16x16x32_bf16 v[8:11], a[244:247], a[176:179], v[8:11]\n\t"
"s_waitcnt lgkmcnt(7)\n\t"
"v_mfma_f32_16x16x32_bf16 v[8:11], a[164:167], a[180:183], v[8:11]\n\t"
"buffer_load_dwordx4 a[220:223], v252, s[8:11], s20 offen offset:2048 sc1\n\t"
"v_mfma_f32_16x16x32_bf16 v[8:11], a[248:251], a[184:187], v[8:11]\n\t"
"s_mov_b32 s20, 1363968\n\t"
"v_mfma_f32_16x16x32_bf16 v[12:15], a[244:247], a[180:183], v[12:15]\n\t"
"s_waitcnt lgkmcnt(5)\n\t"
"v_mfma_f32_16x16x32_bf16 v[12:15], a[164:167], a[184:187], v[12:15]\n\t"
"v_mfma_f32_16x16x32_bf16 v[12:15], a[248:251], a[188:191], v[12:15]\n\t"
"buffer_load_dwordx4 a[224:227], v252, s[8:11], s20 offen offset:0 sc1\n\t"
"v_mfma_f32_16x16x32_bf16 v[16:19], a[244:247], a[184:187], v[16:19]\n\t"
"s_waitcnt lgkmcnt(3)\n\t"
"v_mfma_f32_16x16x32_bf16 v[16:19], a[164:167], a[188:191], v[16:19]\n\t"
"v_mfma_f32_16x16x32_bf16 v[16:19], a[248:251], a[192:195], v[16:19]\n\t"
"ds_read_b128 a[52:55], v252 offset:0\n\t"
"v_mfma_f32_16x16x32_bf16 v[20:23], a[244:247], a[188:191], v[20:23]\n\t"
"buffer_load_dwordx4 a[228:231], v252, s[8:11], s20 offen offset:1024 sc1\n\t"
"v_mfma_f32_16x16x32_bf16 v[20:23], a[164:167], a[192:195], v[20:23]\n\t"
"s_waitcnt lgkmcnt(3)\n\t"
"ds_read_b128 a[84:87], v252 offset:1024\n\t"
"v_mfma_f32_16x16x32_bf16 v[20:23], a[248:251], a[196:199], v[20:23]\n\t"
"v_mfma_f32_16x16x32_bf16 v[24:27], a[244:247], a[192:195], v[24:27]\n\t"
"s_waitcnt lgkmcnt(3)\n\t"
"ds_read_b128 v[196:199], v252 offset:2048\n\t"
"v_mfma_f32_16x16x32_bf16 v[24:27], a[164:167], a[196:199], v[24:27]\n\t"
"buffer_load_dwordx4 a[236:239], v252, s[8:11], s20 offen offset:2048 sc1\n\t"
"v_mfma_f32_16x16x32_bf16 v[24:27], a[248:251], a[200:203], v[24:27]\n\t"
"s_mov_b32 s20, 1376256\n\t"
"v_mfma_f32_16x16x32_bf16 v[28:31], a[244:247], a[196:199], v[28:31]\n\t"
"s_waitcnt vmcnt(33)\n\t"
"v_mfma_f32_16x16x32_bf16 v[28:31], a[164:167], a[200:203], v[28:31]\n\t"
"v_mfma_f32_16x16x32_bf16 v[28:31], a[248:251], a[204:207], v[28:31]\n\t"
"buffer_load_dwordx4 a[240:243], v252, s[8:11], s20 offen offset:0 sc1\n\t"
"v_mfma_f32_16x16x32_bf16 v[32:35], v[236:239], a[168:171], v[32:35]\n\t"
"v_mfma_f32_16x16x32_bf16 v[32:35], v[96:99], a[172:175], v[32:35]\n\t"
"v_mfma_f32_16x16x32_bf16 v[32:35], a[232:235], a[176:179], v[32:35]\n\t"
"buffer_load_dwordx4 a[244:247], v252, s[8:11], s20 offen offset:1024 sc1\n\t"
"v_mfma_f32_16x16x32_bf16 v[36:39], v[236:239], a[172:175], v[36:39]\n\t"
"v_mfma_f32_16x16x32_bf16 v[36:39], v[96:99], a[176:179], v[36:39]\n\t"
"v_mfma_f32_16x16x32_bf16 v[36:39], a[232:235], a[180:183], v[36:39]\n\t"
"v_mfma_f32_16x16x32_bf16 v[40:43], v[236:239], a[176:179], v[40:43]\n\t"
"buffer_load_dwordx4 a[248:251], v252, s[8:11], s20 offen offset:2048 sc1\n\t"
"v_mfma_f32_16x16x32_bf16 v[40:43], v[96:99], a[180:183], v[40:43]\n\t"
"s_mov_b32 s20, 1388544\n\t"
"v_mfma_f32_16x16x32_bf16 v[40:43], a[232:235], a[184:187], v[40:43]\n\t"
"v_mfma_f32_16x16x32_bf16 v[44:47], v[236:239], a[180:183], v[44:47]\n\t"
"v_mfma_f32_16x16x32_bf16 v[44:47], v[96:99], a[184:187], v[44:47]\n\t"
"v_mfma_f32_16x16x32_bf16 v[44:47], a[232:235], a[188:191], v[44:47]\n\t"
"s_waitcnt lgkmcnt(0)\n\t"
"s_barrier\n\t"
"v_mfma_f32_16x16x32_bf16 v[48:51], v[236:239], a[184:187], v[48:51]\n\t"
"v_mfma_f32_16x16x32_bf16 v[48:51], v[96:99], a[188:191], v[48:51]\n\t"
"buffer_load_dwordx4 a[252:255], v252, s[8:11], s20 offen offset:0 sc1\n\t"
"v_mfma_f32_16x16x32_bf16 v[48:51], a[232:235], a[192:195], v[48:51]\n\t"
"buffer_load_dwordx4 a[144:147], v252, s[8:11], s20 offen offset:1024 sc1\n\t"
"v_mfma_f32_16x16x32_bf16 v[52:55], v[236:239], a[188:191], v[52:55]\n\t"
"v_mfma_f32_16x16x32_bf16 v[52:55], v[96:99], a[192:195], v[52:55]\n\t"
"buffer_load_dwordx4 a[20:23], v252, s[8:11], s20 offen offset:2048 sc1\n\t"
"v_mfma_f32_16x16x32_bf16 v[52:55], a[232:235], a[196:199], v[52:55]\n\t"
"s_mov_b32 s20, 0\n\t"
"ds_write_b128 v249, a[24:27] offset:0\n\t"
"v_mfma_f32_16x16x32_bf16 v[56:59], v[236:239], a[192:195], v[56:59]\n\t"
"v_mfma_f32_16x16x32_bf16 v[56:59], v[96:99], a[196:199], v[56:59]\n\t"
"v_mfma_f32_16x16x32_bf16 v[56:59], a[232:235], a[200:203], v[56:59]\n\t"
"ds_write_b64 v250, a[152:153] offset:0\n\t"
"v_mfma_f32_16x16x32_bf16 v[60:63], v[236:239], a[196:199], v[60:63]\n\t"
"s_waitcnt vmcnt(27)\n\t"
"v_mfma_f32_16x16x32_bf16 v[60:63], v[96:99], a[200:203], v[60:63]\n\t"
"buffer_load_dwordx4 v[96:99], v252, s[8:11], s20 offen offset:0 sc1\n\t"
"v_mfma_f32_16x16x32_bf16 v[60:63], a[232:235], a[204:207], v[60:63]\n\t"
"ds_write_b128 v249, a[28:31] offset:6144\n\t"
"v_mfma_f32_16x16x32_bf16 v[64:67], a[104:107], a[168:171], v[64:67]\n\t"
"v_mfma_f32_16x16x32_bf16 v[64:67], a[112:115], a[172:175], v[64:67]\n\t"
"buffer_load_dwordx4 a[232:235], v252, s[8:11], s20 offen offset:1024 sc1\n\t"
"v_mfma_f32_16x16x32_bf16 v[64:67], a[44:47], a[176:179], v[64:67]\n\t"
"ds_read_b128 a[168:171], v255 offset:1536\n\t"
"v_mfma_f32_16x16x32_bf16 v[68:71], a[104:107], a[172:175], v[68:71]\n\t"
"ds_read_b128 a[172:175], v255 offset:4608\n\t"
"v_mfma_f32_16x16x32_bf16 v[68:71], a[112:115], a[176:179], v[68:71]\n\t"
"v_mfma_f32_16x16x32_bf16 v[68:71], a[44:47], a[180:183], v[68:71]\n\t"
"ds_write_b64 v250, a[154:155] offset:6144\n\t"
"v_mfma_f32_16x16x32_bf16 v[72:75], a[104:107], a[176:179], v[72:75]\n\t"
"ds_read_b128 a[176:179], v255 offset:7680\n\t"
"v_mfma_f32_16x16x32_bf16 v[72:75], a[112:115], a[180:183], v[72:75]\n\t"
"v_mfma_f32_16x16x32_bf16 v[72:75], a[44:47], a[184:187], v[72:75]\n\t"
"ds_read_b128 v[204:207], v252 offset:12288\n\t"
"v_mfma_f32_16x16x32_bf16 v[76:79], a[104:107], a[180:183], v[76:79]\n\t"
"ds_read_b128 a[180:183], v255 offset:10752\n\t"
"v_mfma_f32_16x16x32_bf16 v[76:79], a[112:115], a[184:187], v[76:79]\n\t"
"ds_read_b128 v[212:215], v252 offset:13312\n\t"
"v_mfma_f32_16x16x32_bf16 v[76:79], a[44:47], a[188:191], v[76:79]\n\t"
"v_mfma_f32_16x16x32_bf16 v[80:83], a[104:107], a[184:187], v[80:83]\n\t"
"ds_read_b128 a[184:187], v255 offset:13824\n\t"
"v_mfma_f32_16x16x32_bf16 v[80:83], a[112:115], a[188:191], v[80:83]\n\t"
"ds_read_b128 a[0:3], v252 offset:14336\n\t"
"v_mfma_f32_16x16x32_bf16 v[80:83], a[44:47], a[192:195], v[80:83]\n\t"
"v_mfma_f32_16x16x32_bf16 v[84:87], a[104:107], a[188:191], v[84:87]\n\t"
"ds_read_b128 a[188:191], v255 offset:16896\n\t"
"v_mfma_f32_16x16x32_bf16 v[84:87], a[112:115], a[192:195], v[84:87]\n\t"
"v_mfma_f32_16x16x32_bf16 v[84:87], a[44:47], a[196:199], v[84:87]\n\t"
"v_mfma_f32_16x16x32_bf16 v[88:91], a[104:107], a[192:195], v[88:91]\n\t"
"s_add_u32 s29, s22, 384\n\t"
"ds_read_b128 a[192:195], v255 offset:19968\n\t"
"v_mfma_f32_16x16x32_bf16 v[88:91], a[112:115], a[196:199], v[88:91]\n\t"
"v_mfma_f32_16x16x32_bf16 v[88:91], a[44:47], a[200:203], v[88:91]\n\t"
"v_mfma_f32_16x16x32_bf16 v[92:95], a[104:107], a[196:199], v[92:95]\n\t"
"s_waitcnt vmcnt(26) lgkmcnt(7)\n\t"
"ds_read_b128 a[196:199], v255 offset:23040\n\t"
"v_mfma_f32_16x16x32_bf16 v[92:95], a[112:115], a[200:203], v[92:95]\n\t"
"ds_read_b128 a[200:203], v255 offset:26112\n\t"
"v_mfma_f32_16x16x32_bf16 v[92:95], a[44:47], a[204:207], v[92:95]\n\t"
"ds_read_b128 a[204:207], v255 offset:29184\n\t"
"v_mfma_f32_16x16x32_bf16 v[100:103], a[60:63], a[168:171], v[100:103]\n\t"
"s_waitcnt lgkmcnt(8)\n\t"
"v_mfma_f32_16x16x32_bf16 v[100:103], a[68:71], a[172:175], v[100:103]\n\t"
"v_mfma_f32_16x16x32_bf16 v[100:103], a[76:79], a[176:179], v[100:103]\n\t"
"buffer_store_dwordx4 a[40:43], v248, s[16:19], s29 offen offset:0 nt\n\t"
"v_mfma_f32_16x16x32_bf16 v[104:107], a[60:63], a[172:175], v[104:107]\n\t"
"s_waitcnt lgkmcnt(6)\n\t"
"v_mfma_f32_16x16x32_bf16 v[104:107], a[68:71], a[176:179], v[104:107]\n\t"
"v_mfma_f32_16x16x32_bf16 v[104:107], a[76:79], a[180:183], v[104:107]\n\t"
"s_waitcnt lgkmcnt(0)\n\t"
"s_barrier\n\t"
"v_mfma_f32_16x16x32_bf16 v[108:111], a[60:63], a[176:179], v[108:111]\n\t"
"v_mfma_f32_16x16x32_bf16 v[108:111], a[68:71], a[180:183], v[108:111]\n\t"
"buffer_load_dwordx4 a[152:155], v252, s[8:11], s20 offen offset:2048 sc1\n\t"
"v_mfma_f32_16x16x32_bf16 v[108:111], a[76:79], a[184:187], v[108:111]\n\t"
"s_mov_b32 s20, 12288\n\t"
"ds_write_b128 v249, a[32:35] offset:12288\n\t"
"v_mfma_f32_16x16x32_bf16 v[112:115], a[60:63], a[180:183], v[112:115]\n\t"
"v_mfma_f32_16x16x32_bf16 v[112:115], a[68:71], a[184:187], v[112:115]\n\t"
"buffer_load_dwordx4 a[28:31], v252, s[8:11], s20 offen offset:0 sc1\n\t"
"v_mfma_f32_16x16x32_bf16 v[112:115], a[76:79], a[188:191], v[112:115]\n\t"
"ds_write_b64 v250, a[160:161] offset:12288\n\t"
"v_mfma_f32_16x16x32_bf16 v[116:119], a[60:63], a[184:187], v[116:119]\n\t"
"v_mfma_f32_16x16x32_bf16 v[116:119], a[68:71], a[188:191], v[116:119]\n\t"
"v_mfma_f32_16x16x32_bf16 v[116:119], a[76:79], a[192:195], v[116:119]\n\t"
"buffer_store_dwordx4 a[48:51], v248, s[16:19], s29 offen offset:128 nt\n\t"
"ds_write_b64 v250, a[162:163] offset:18432\n\t"
"v_mfma_f32_16x16x32_bf16 v[120:123], a[60:63], a[188:191], v[120:123]\n\t"
"v_mfma_f32_16x16x32_bf16 v[120:123], a[68:71], a[192:195], v[120:123]\n\t"
"v_mfma_f32_16x16x32_bf16 v[120:123], a[76:79], a[196:199], v[120:123]\n\t"
"ds_write_b128 v249, a[36:39] offset:18432\n\t"
"v_mfma_f32_16x16x32_bf16 v[124:127], a[60:63], a[192:195], v[124:127]\n\t"
"v_mfma_f32_16x16x32_bf16 v[124:127], a[68:71], a[196:199], v[124:127]\n\t"
"v_mfma_f32_16x16x32_bf16 v[124:127], a[76:79], a[200:203], v[124:127]\n\t"
"v_mfma_f32_16x16x32_bf16 v[128:131], a[60:63], a[196:199], v[128:131]\n\t"
"s_waitcnt vmcnt(27)\n\t"
"v_mfma_f32_16x16x32_bf16 v[128:131], a[68:71], a[200:203], v[128:131]\n\t"
"v_mfma_f32_16x16x32_bf16 v[128:131], a[76:79], a[204:207], v[128:131]\n\t"
"v_mfma_f32_16x16x32_bf16 v[132:135], a[132:135], a[168:171], v[132:135]\n\t"
"v_mfma_f32_16x16x32_bf16 v[132:135], a[92:95], a[172:175], v[132:135]\n\t"
"v_mfma_f32_16x16x32_bf16 v[132:135], a[100:103], a[176:179], v[132:135]\n\t"
"buffer_store_dwordx4 a[56:59], v248, s[16:19], s29 offen offset:256 nt\n\t"
"v_mfma_f32_16x16x32_bf16 v[136:139], a[132:135], a[172:175], v[136:139]\n\t"
"v_mfma_f32_16x16x32_bf16 v[136:139], a[92:95], a[176:179], v[136:139]\n\t"
"v_add_u32 v255, 55296, v255\n\t"
"v_mfma_f32_16x16x32_bf16 v[136:139], a[100:103], a[180:183], v[136:139]\n\t"
"v_mfma_f32_16x16x32_bf16 v[140:143], a[132:135], a[176:179], v[140:143]\n\t"
"v_mfma_f32_16x16x32_bf16 v[140:143], a[92:95], a[180:183], v[140:143]\n\t"
"v_mfma_f32_16x16x32_bf16 v[140:143], a[100:103], a[184:187], v[140:143]\n\t"
"s_add_u32 s29, s22, 196992\n\t"
"v_mfma_f32_16x16x32_bf16 v[144:147], a[132:135], a[180:183], v[144:147]\n\t"
"v_mfma_f32_16x16x32_bf16 v[144:147], a[92:95], a[184:187], v[144:147]\n\t"
"global_load_dwordx4 v[240:243], v252, s[48:49] offset:2048\n\t"
"v_mfma_f32_16x16x32_bf16 v[144:147], a[100:103], a[188:191], v[144:147]\n\t"
"v_mfma_f32_16x16x32_bf16 v[148:151], a[132:135], a[184:187], v[148:151]\n\t"
"v_mfma_f32_16x16x32_bf16 v[148:151], a[92:95], a[188:191], v[148:151]\n\t"
"v_mfma_f32_16x16x32_bf16 v[148:151], a[100:103], a[192:195], v[148:151]\n\t"
"buffer_store_dwordx4 a[64:67], v248, s[16:19], s29 offen offset:0 nt\n\t"
"ds_read_b128 a[8:11], v252 offset:0\n\t"
"v_mfma_f32_16x16x32_bf16 v[152:155], a[132:135], a[188:191], v[152:155]\n\t"
"v_mfma_f32_16x16x32_bf16 v[152:155], a[92:95], a[192:195], v[152:155]\n\t"
"global_load_dwordx4 a[136:139], v252, s[46:47] offset:1024\n\t"
"v_mfma_f32_16x16x32_bf16 v[152:155], a[100:103], a[196:199], v[152:155]\n\t"
"ds_read_b128 a[16:19], v252 offset:1024\n\t"
"v_mfma_f32_16x16x32_bf16 v[156:159], a[132:135], a[192:195], v[156:159]\n\t"
"ds_read_b128 a[24:27], v252 offset:2048\n\t"
"v_mfma_f32_16x16x32_bf16 v[156:159], a[92:95], a[196:199], v[156:159]\n\t"
"v_mfma_f32_16x16x32_bf16 v[156:159], a[100:103], a[200:203], v[156:159]\n\t"
"v_mfma_f32_16x16x32_bf16 v[160:163], a[132:135], a[196:199], v[160:163]\n\t"
"s_waitcnt vmcnt(28)\n\t"
"v_mfma_f32_16x16x32_bf16 v[160:163], a[92:95], a[200:203], v[160:163]\n\t"
"v_mfma_f32_16x16x32_bf16 v[160:163], a[100:103], a[204:207], v[160:163]\n\t"
"v_mfma_f32_16x16x32_bf16 v[164:167], v[200:203], a[168:171], v[164:167]\n\t"
"ds_read_b128 a[168:171], v255 offset:0\n\t"
"v_mfma_f32_16x16x32_bf16 v[164:167], a[120:123], a[172:175], v[164:167]\n\t"
"v_mfma_f32_16x16x32_bf16 v[164:167], a[128:131], a[176:179], v[164:167]\n\t"
"buffer_store_dwordx4 a[72:75], v248, s[16:19], s29 offen offset:128 nt\n\t"
"v_mfma_f32_16x16x32_bf16 v[168:171], v[200:203], a[172:175], v[168:171]\n\t"
"ds_read_b128 a[172:175], v255 offset:3072\n\t"
"v_mfma_f32_16x16x32_bf16 v[168:171], a[120:123], a[176:179], v[168:171]\n\t"
"v_mfma_f32_16x16x32_bf16 v[168:171], a[128:131], a[180:183], v[168:171]\n\t"
"v_mfma_f32_16x16x32_bf16 v[172:175], v[200:203], a[176:179], v[172:175]\n\t"
"ds_read_b128 a[176:179], v255 offset:6144\n\t"
"v_mfma_f32_16x16x32_bf16 v[172:175], a[120:123], a[180:183], v[172:175]\n\t"
"v_mfma_f32_16x16x32_bf16 v[172:175], a[128:131], a[184:187], v[172:175]\n\t"
"v_mfma_f32_16x16x32_bf16 v[176:179], v[200:203], a[180:183], v[176:179]\n\t"
"ds_read_b128 a[180:183], v255 offset:9216\n\t"
"v_mfma_f32_16x16x32_bf16 v[176:179], a[120:123], a[184:187], v[176:179]\n\t"
"v_mfma_f32_16x16x32_bf16 v[176:179], a[128:131], a[188:191], v[176:179]\n\t"
"s_waitcnt lgkmcnt(4)\n\t"
"s_barrier\n\t"
"v_mfma_f32_16x16x32_bf16 v[180:183], v[200:203], a[184:187], v[180:183]\n\t"
"ds_read_b128 a[184:187], v255 offset:12288\n\t"
"v_mfma_f32_16x16x32_bf16 v[180:183], a[120:123], a[188:191], v[180:183]\n\t"
"buffer_load_dwordx4 a[160:163], v252, s[8:11], s20 offen offset:1024 sc1\n\t"
"v_mfma_f32_16x16x32_bf16 v[180:183], a[128:131], a[192:195], v[180:183]\n\t"
"v_mfma_f32_16x16x32_bf16 v[184:187], v[200:203], a[188:191], v[184:187]\n\t"
"ds_read_b128 a[188:191], v255 offset:15360\n\t"
"v_mfma_f32_16x16x32_bf16 v[184:187], a[120:123], a[192:195], v[184:187]\n\t"
"buffer_load_dwordx4 a[36:39], v252, s[8:11], s20 offen offset:2048 sc1\n\t"
"v_mfma_f32_16x16x32_bf16 v[184:187], a[128:131], a[196:199], v[184:187]\n\t"
"buffer_store_dwordx4 a[80:83], v248, s[16:19], s29 offen offset:256 nt\n\t"
"s_waitcnt vmcnt(6)\n\t"
"v_add_u32 v240, 59904, v240\n\t"
"v_add_u32 v241, 59904, v241\n\t"
"s_mov_b32 s20, 24576\n\t"
"v_mfma_f32_16x16x32_bf16 v[188:191], v[200:203], a[192:195], v[188:191]\n\t"
"ds_read_b128 a[192:195], v255 offset:18432\n\t"
"v_mfma_f32_16x16x32_bf16 v[188:191], a[120:123], a[196:199], v[188:191]\n\t"
"v_mfma_f32_16x16x32_bf16 v[188:191], a[128:131], a[200:203], v[188:191]\n\t"
"ds_read_b64_tr_b16 v[236:237], v240 offset:20480\n\t"
"v_mfma_f32_16x16x32_bf16 v[192:195], v[200:203], a[196:199], v[192:195]\n\t"
"buffer_load_dwordx4 v[200:203], v252, s[8:11], s20 offen offset:0 sc1\n\t"
"v_mfma_f32_16x16x32_bf16 v[192:195], a[120:123], a[200:203], v[192:195]\n\t"
"s_waitcnt lgkmcnt(5)\n\t"
"ds_read_b128 a[196:199], v255 offset:21504\n\t"
"s_add_u32 s29, s22, 393600\n\t"
"v_mfma_f32_16x16x32_bf16 v[192:195], a[128:131], a[204:207], v[192:195]\n\t"
"ds_read_b64_tr_b16 v[238:239], v240 offset:22016\n\t"
"Lst17_%=:\n\t"
"v_mfma_f32_16x16x32_bf16 v[0:3], a[140:143], a[168:171], v[0:3]\n\t"
"buffer_load_dwordx4 a[120:123], v252, s[8:11], s20 offen offset:1024 sc1\n\t"
"v_mfma_f32_16x16x32_bf16 v[0:3], v[208:211], a[172:175], v[0:3]\n\t"
"s_waitcnt lgkmcnt(6)\n\t"
"ds_read_b64_tr_b16 v[224:225], v241 offset:20480\n\t"
"v_mfma_f32_16x16x32_bf16 v[0:3], v[216:219], a[176:179], v[0:3]\n\t"
"ds_read_b64_tr_b16 v[226:227], v241 offset:22016\n\t"
"v_mfma_f32_16x16x32_bf16 v[4:7], a[140:143], a[172:175], v[4:7]\n\t"
"s_waitcnt lgkmcnt(7)\n\t"
"ds_read_b128 a[200:203], v255 offset:24576\n\t"
"v_mfma_f32_16x16x32_bf16 v[4:7], v[208:211], a[176:179], v[4:7]\n\t"
"buffer_load_dwordx4 a[128:131], v252, s[8:11], s20 offen offset:2048 sc1\n\t"
"v_mfma_f32_16x16x32_bf16 v[4:7], v[216:219], a[180:183], v[4:7]\n\t"
"ds_read_b128 a[204:207], v255 offset:27648\n\t"
"v_mfma_f32_16x16x32_bf16 v[8:11], a[140:143], a[176:179], v[8:11]\n\t"
"s_waitcnt lgkmcnt(8)\n\t"
"v_cvt_pk_bf16_f32 v0, v0, v1\n\t"
"v_cvt_pk_bf16_f32 v2, v2, v3\n\t"
"v_mfma_f32_16x16x32_bf16 v[8:11], v[208:211], a[180:183], v[8:11]\n\t"
"v_accvgpr_write_b32 a40, v0\n\t"
"v_accvgpr_write_b32 a41, v2\n\t"
"v_mfma_f32_16x16x32_bf16 v[8:11], v[216:219], a[184:187], v[8:11]\n\t"
"v_mfma_f32_16x16x32_bf16 v[12:15], a[140:143], a[180:183], v[12:15]\n\t"
"s_waitcnt vmcnt(7) lgkmcnt(4)\n\t"
"v_cvt_pk_bf16_f32 v4, v4, v5\n\t"
"v_cvt_pk_bf16_f32 v6, v6, v7\n\t"
"v_mfma_f32_16x16x32_bf16 v[12:15], v[208:211], a[184:187], v[12:15]\n\t"
"s_waitcnt lgkmcnt(2)\n\t"
"v_accvgpr_write_b32 a48, v4\n\t"
"v_accvgpr_write_b32 a49, v6\n\t"
"v_mfma_f32_16x16x32_bf16 v[12:15], v[216:219], a[188:191], v[12:15]\n\t"
"buffer_store_dwordx4 a[88:91], v248, s[16:19], s29 offen offset:0 nt\n\t"
"v_mfma_f32_16x16x32_bf16 v[236:239], v[236:239], a[136:139], 0\n\t"
"v_cvt_pk_bf16_f32 v8, v8, v9\n\t"
"v_cvt_pk_bf16_f32 v10, v10, v11\n\t"
"v_mfma_f32_16x16x32_bf16 v[224:227], v[224:227], a[136:139], 0\n\t"
"v_accvgpr_write_b32 a56, v8\n\t"
"v_accvgpr_write_b32 a57, v10\n\t"
"v_mfma_f32_16x16x32_bf16 v[16:19], a[140:143], a[184:187], v[16:19]\n\t"
"v_mfma_f32_16x16x32_bf16 v[16:19], v[208:211], a[188:191], v[16:19]\n\t"
"v_cvt_pk_bf16_f32 v12, v12, v13\n\t"
"v_cvt_pk_bf16_f32 v14, v14, v15\n\t"
"v_mfma_f32_16x16x32_bf16 v[16:19], v[216:219], a[192:195], v[16:19]\n\t"
"ds_read_b64_tr_b16 v[232:233], v240 offset:40448\n\t"
"v_accvgpr_write_b32 a64, v12\n\t"
"v_mfma_f32_16x16x32_bf16 v[20:23], a[140:143], a[188:191], v[20:23]\n\t"
"s_waitcnt lgkmcnt(2)\n\t"
"ds_read_b64_tr_b16 v[234:235], v240 offset:41984\n\t"
"v_cvt_pk_bf16_f32 v236, v236, v237\n\t"
"v_mfma_f32_16x16x32_bf16 v[20:23], v[208:211], a[192:195], v[20:23]\n\t"
"s_mov_b32 s20, 184320\n\t"
"ds_read_b64_tr_b16 v[228:229], v241 offset:40448\n\t"
"v_cvt_pk_bf16_f32 v237, v238, v239\n\t"
"v_mfma_f32_16x16x32_bf16 v[20:23], v[216:219], a[196:199], v[20:23]\n\t"
"ds_read_b64_tr_b16 v[230:231], v241 offset:41984\n\t"
"v_cvt_pk_bf16_f32 v238, v224, v225\n\t"
"v_mfma_f32_16x16x32_bf16 v[24:27], a[140:143], a[192:195], v[24:27]\n\t"
"s_waitcnt lgkmcnt(4)\n\t"
"ds_read_b128 a[32:35], v252 offset:12288\n\t"
"v_cvt_pk_bf16_f32 v239, v226, v227\n\t"
"v_mfma_f32_16x16x32_bf16 v[24:27], v[208:211], a[196:199], v[24:27]\n\t"
"ds_read_b128 a[104:107], v252 offset:13312\n\t"
"v_accvgpr_write_b32 a65, v14\n\t"
"v_mfma_f32_16x16x32_bf16 v[24:27], v[216:219], a[200:203], v[24:27]\n\t"
"buffer_store_dwordx4 a[96:99], v248, s[16:19], s29 offen offset:128 nt\n\t"
"ds_read_b128 a[112:115], v252 offset:14336\n\t"
"v_cvt_pk_bf16_f32 v16, v16, v17\n\t"
"v_cvt_pk_bf16_f32 v18, v18, v19\n\t"
"v_accvgpr_write_b32 a72, v16\n\t"
"v_mfma_f32_16x16x32_bf16 v[28:31], a[140:143], a[196:199], v[28:31]\n\t"
"v_cvt_pk_bf16_f32 v20, v20, v21\n\t"
"v_cvt_pk_bf16_f32 v22, v22, v23\n\t"
"v_mfma_f32_16x16x32_bf16 v[28:31], v[208:211], a[200:203], v[28:31]\n\t"
"v_accvgpr_write_b32 a73, v18\n\t"
"v_accvgpr_write_b32 a80, v20\n\t"
"v_mfma_f32_16x16x32_bf16 v[28:31], v[216:219], a[204:207], v[28:31]\n\t"
"ds_write_b128 v242, v[236:239] offset:30720\n\t"
"v_mfma_f32_16x16x32_bf16 v[32:35], a[4:7], a[168:171], v[32:35]\n\t"
"buffer_load_dwordx4 v[208:211], v252, s[8:11], s20 offen offset:0 sc1\n\t"
"v_mfma_f32_16x16x32_bf16 v[32:35], a[12:15], a[172:175], v[32:35]\n\t"
"buffer_load_dwordx4 v[216:219], v252, s[8:11], s20 offen offset:1024 sc1\n\t"
"v_mfma_f32_16x16x32_bf16 v[32:35], a[208:211], a[176:179], v[32:35]\n\t"
"v_cvt_pk_bf16_f32 v24, v24, v25\n\t"
"v_cvt_pk_bf16_f32 v26, v26, v27\n\t"
"v_mfma_f32_16x16x32_bf16 v[36:39], a[4:7], a[172:175], v[36:39]\n\t"
"s_waitcnt lgkmcnt(6)\n\t"
"v_accvgpr_write_b32 a81, v22\n\t"
"v_cvt_pk_bf16_f32 v28, v28, v29\n\t"
"v_mfma_f32_16x16x32_bf16 v[36:39], a[12:15], a[176:179], v[36:39]\n\t"
"s_waitcnt lgkmcnt(4)\n\t"
"v_cvt_pk_bf16_f32 v30, v30, v31\n\t"
"v_accvgpr_write_b32 a88, v24\n\t"
"v_mfma_f32_16x16x32_bf16 v[36:39], a[208:211], a[180:183], v[36:39]\n\t"
"v_accvgpr_write_b32 a89, v26\n\t"
"v_accvgpr_write_b32 a96, v28\n\t"
"v_mfma_f32_16x16x32_bf16 v[232:235], v[232:235], a[136:139], 0\n\t"
"v_cvt_pk_bf16_f32 v32, v32, v33\n\t"
"v_cvt_pk_bf16_f32 v34, v34, v35\n\t"
"v_mfma_f32_16x16x32_bf16 v[228:231], v[228:231], a[136:139], 0\n\t"
"v_accvgpr_write_b32 a97, v30\n\t"
"v_accvgpr_write_b32 a42, v32\n\t"
"v_mfma_f32_16x16x32_bf16 v[40:43], a[4:7], a[176:179], v[40:43]\n\t"
"v_accvgpr_write_b32 a43, v34\n\t"
"v_mfma_f32_16x16x32_bf16 v[40:43], a[12:15], a[180:183], v[40:43]\n\t"
"v_cvt_pk_bf16_f32 v36, v36, v37\n\t"
"v_cvt_pk_bf16_f32 v38, v38, v39\n\t"
"v_mfma_f32_16x16x32_bf16 v[40:43], a[208:211], a[184:187], v[40:43]\n\t"
"buffer_store_dwordx4 a[108:111], v248, s[16:19], s29 offen offset:256 nt\n\t"
"v_accvgpr_write_b32 a50, v36\n\t"
"v_accvgpr_write_b32 a51, v38\n\t"
"s_add_u32 s29, s22, 590208\n\t"
"v_mfma_f32_16x16x32_bf16 v[44:47], a[4:7], a[180:183], v[44:47]\n\t"
"v_cvt_pk_bf16_f32 v232, v232, v233\n\t"
"v_cvt_pk_bf16_f32 v233, v234, v235\n\t"
"v_mfma_f32_16x16x32_bf16 v[44:47], a[12:15], a[184:187], v[44:47]\n\t"
"v_cvt_pk_bf16_f32 v234, v228, v229\n\t"
"v_mfma_f32_16x16x32_bf16 v[44:47], a[208:211], a[188:191], v[44:47]\n\t"
"s_waitcnt lgkmcnt(1)\n\t"
"s_barrier\n\t"
"v_mfma_f32_16x16x32_bf16 v[48:51], a[4:7], a[184:187], v[48:51]\n\t"
"ds_read_b64_tr_b16 v[224:225], v240 offset:512\n\t"
"v_mfma_f32_16x16x32_bf16 v[48:51], a[12:15], a[188:191], v[48:51]\n\t"
"ds_read_b64_tr_b16 v[226:227], v240 offset:2048\n\t"
"v_cvt_pk_bf16_f32 v235, v230, v231\n\t"
"v_mfma_f32_16x16x32_bf16 v[48:51], a[208:211], a[192:195], v[48:51]\n\t"
"ds_write_b128 v242, v[232:235] offset:32256\n\t"
"v_mfma_f32_16x16x32_bf16 v[52:55], a[4:7], a[188:191], v[52:55]\n\t"
"v_cvt_pk_bf16_f32 v40, v40, v41\n\t"
"v_cvt_pk_bf16_f32 v42, v42, v43\n\t"
"v_mfma_f32_16x16x32_bf16 v[52:55], a[12:15], a[192:195], v[52:55]\n\t"
"ds_read_b64_tr_b16 v[236:237], v241 offset:512\n\t"
"v_mfma_f32_16x16x32_bf16 v[52:55], a[208:211], a[196:199], v[52:55]\n\t"
"ds_read_b64_tr_b16 v[238:239], v241 offset:2048\n\t"
"v_cvt_pk_bf16_f32 v44, v44, v45\n\t"
"v_mfma_f32_16x16x32_bf16 v[56:59], a[4:7], a[192:195], v[56:59]\n\t"
"v_cvt_pk_bf16_f32 v46, v46, v47\n\t"
"v_accvgpr_write_b32 a58, v40\n\t"
"v_mfma_f32_16x16x32_bf16 v[56:59], a[12:15], a[196:199], v[56:59]\n\t"
"v_accvgpr_write_b32 a59, v42\n\t"
"v_cvt_pk_bf16_f32 v48, v48, v49\n\t"
"v_mfma_f32_16x16x32_bf16 v[56:59], a[208:211], a[200:203], v[56:59]\n\t"
"buffer_store_dwordx4 a[116:119], v248, s[16:19], s29 offen offset:0 nt\n\t"
"v_cvt_pk_bf16_f32 v50, v50, v51\n\t"
"v_accvgpr_write_b32 a66, v44\n\t"
"v_accvgpr_write_b32 a67, v46\n\t"
"v_accvgpr_write_b32 a74, v48\n\t"
"v_mfma_f32_16x16x32_bf16 v[60:63], a[4:7], a[196:199], v[60:63]\n\t"
"s_waitcnt lgkmcnt(3)\n\t"
"v_cvt_pk_bf16_f32 v52, v52, v53\n\t"
"v_cvt_pk_bf16_f32 v54, v54, v55\n\t"
"v_mfma_f32_16x16x32_bf16 v[60:63], a[12:15], a[200:203], v[60:63]\n\t"
"s_waitcnt lgkmcnt(0)\n\t"
"v_accvgpr_write_b32 a75, v50\n\t"
"v_accvgpr_write_b32 a82, v52\n\t"
"v_mfma_f32_16x16x32_bf16 v[60:63], a[208:211], a[204:207], v[60:63]\n\t"
"v_accvgpr_write_b32 a83, v54\n\t"
"v_mfma_f32_16x16x32_bf16 v[224:227], v[224:227], a[136:139], 0\n\t"
"buffer_load_dwordx4 a[4:7], v252, s[8:11], s20 offen offset:2048 sc1\n\t"
"s_mov_b32 s20, 196608\n\t"
"v_mfma_f32_16x16x32_bf16 v[236:239], v[236:239], a[136:139], 0\n\t"
"buffer_load_dwordx4 a[12:15], v252, s[8:11], s20 offen offset:0 sc1\n\t"
"v_mfma_f32_16x16x32_bf16 v[64:67], a[212:215], a[168:171], v[64:67]\n\t"
"ds_read_b128 a[168:171], v255 offset:1536\n\t"
"v_subrev_u32 v240, 59904, v240\n\t"
"v_mfma_f32_16x16x32_bf16 v[64:67], a[216:219], a[172:175], v[64:67]\n\t"
"v_subrev_u32 v241, 59904, v241\n\t"
"v_cvt_pk_bf16_f32 v56, v56, v57\n\t"
"v_mfma_f32_16x16x32_bf16 v[64:67], a[220:223], a[176:179], v[64:67]\n\t"
"ds_read_b64_tr_b16 v[228:229], v240 offset:512\n\t"
"v_cvt_pk_bf16_f32 v58, v58, v59\n\t"
"v_mfma_f32_16x16x32_bf16 v[68:71], a[212:215], a[172:175], v[68:71]\n\t"
"ds_read_b64_tr_b16 v[230:231], v240 offset:2048\n\t"
"v_cvt_pk_bf16_f32 v224, v224, v225\n\t"
"v_mfma_f32_16x16x32_bf16 v[68:71], a[216:219], a[176:179], v[68:71]\n\t"
"ds_read_b64_tr_b16 v[232:233], v241 offset:512\n\t"
"v_cvt_pk_bf16_f32 v225, v226, v227\n\t"
"v_mfma_f32_16x16x32_bf16 v[68:71], a[220:223], a[180:183], v[68:71]\n\t"
"buffer_load_dwordx4 a[208:211], v252, s[8:11], s20 offen offset:1024 sc1\n\t"
"v_mfma_f32_16x16x32_bf16 v[72:75], a[212:215], a[176:179], v[72:75]\n\t"
"ds_read_b64_tr_b16 v[234:235], v241 offset:2048\n\t"
"v_cvt_pk_bf16_f32 v226, v236, v237\n\t"
"v_mfma_f32_16x16x32_bf16 v[72:75], a[216:219], a[180:183], v[72:75]\n\t"
"ds_read_b128 a[172:175], v255 offset:4608\n\t"
"v_cvt_pk_bf16_f32 v227, v238, v239\n\t"
"v_mfma_f32_16x16x32_bf16 v[72:75], a[220:223], a[184:187], v[72:75]\n\t"
"buffer_store_dwordx4 a[124:127], v248, s[16:19], s29 offen offset:128 nt\n\t"
"ds_read_b128 a[176:179], v255 offset:7680\n\t"
"v_cvt_pk_bf16_f32 v60, v60, v61\n\t"
"v_cvt_pk_bf16_f32 v62, v62, v63\n\t"
"v_accvgpr_write_b32 a90, v56\n\t"
"v_mfma_f32_16x16x32_bf16 v[76:79], a[212:215], a[180:183], v[76:79]\n\t"
"ds_read_b128 a[180:183], v255 offset:10752\n\t"
"v_accvgpr_write_b32 a91, v58\n\t"
"v_mfma_f32_16x16x32_bf16 v[76:79], a[216:219], a[184:187], v[76:79]\n\t"
"v_accvgpr_write_b32 a98, v60\n\t"
"v_accvgpr_write_b32 a99, v62\n\t"
"v_mfma_f32_16x16x32_bf16 v[76:79], a[220:223], a[188:191], v[76:79]\n\t"
"ds_write_b128 v242, v[224:227] offset:29184\n\t"
"v_mfma_f32_16x16x32_bf16 v[80:83], a[212:215], a[184:187], v[80:83]\n\t"
"v_cvt_pk_bf16_f32 v64, v64, v65\n\t"
"v_cvt_pk_bf16_f32 v66, v66, v67\n\t"
"v_mfma_f32_16x16x32_bf16 v[80:83], a[216:219], a[188:191], v[80:83]\n\t"
"ds_read_b128 a[184:187], v255 offset:13824\n\t"
"v_mfma_f32_16x16x32_bf16 v[80:83], a[220:223], a[192:195], v[80:83]\n\t"
"buffer_store_dwordx4 a[156:159], v248, s[16:19], s29 offen offset:256 nt\n\t"
"v_cvt_pk_bf16_f32 v68, v68, v69\n\t"
"v_cvt_pk_bf16_f32 v70, v70, v71\n\t"
"v_accvgpr_write_b32 a108, v64\n\t"
"v_accvgpr_write_b32 a109, v66\n\t"
"v_mfma_f32_16x16x32_bf16 v[84:87], a[212:215], a[188:191], v[84:87]\n\t"
"s_waitcnt lgkmcnt(7)\n\t"
"ds_read_b128 a[188:191], v255 offset:16896\n\t"
"v_cvt_pk_bf16_f32 v72, v72, v73\n\t"
"v_mfma_f32_16x16x32_bf16 v[84:87], a[216:219], a[192:195], v[84:87]\n\t"
"s_waitcnt lgkmcnt(6)\n\t"
"v_cvt_pk_bf16_f32 v74, v74, v75\n\t"
"v_accvgpr_write_b32 a116, v68\n\t"
"v_mfma_f32_16x16x32_bf16 v[84:87], a[220:223], a[196:199], v[84:87]\n\t"
"v_accvgpr_write_b32 a117, v70\n\t"
"v_cvt_pk_bf16_f32 v76, v76, v77\n\t"
"v_mfma_f32_16x16x32_bf16 v[228:231], v[228:231], a[136:139], 0\n\t"
"v_cvt_pk_bf16_f32 v78, v78, v79\n\t"
"v_accvgpr_write_b32 a124, v72\n\t"
"v_mfma_f32_16x16x32_bf16 v[232:235], v[232:235], a[136:139], 0\n\t"
"v_accvgpr_write_b32 a125, v74\n\t"
"v_cvt_pk_bf16_f32 v80, v80, v81\n\t"
"v_mfma_f32_16x16x32_bf16 v[88:91], a[212:215], a[192:195], v[88:91]\n\t"
"s_add_u32 s29, s22, 786816\n\t"
"ds_read_b128 a[192:195], v255 offset:19968\n\t"
"v_cvt_pk_bf16_f32 v82, v82, v83\n\t"
"v_mfma_f32_16x16x32_bf16 v[88:91], a[216:219], a[196:199], v[88:91]\n\t"
"v_accvgpr_write_b32 a132, v76\n\t"
"v_accvgpr_write_b32 a133, v78\n\t"
"v_mfma_f32_16x16x32_bf16 v[88:91], a[220:223], a[200:203], v[88:91]\n\t"
"ds_read_b64_tr_b16 v[236:237], v240 offset:20480\n\t"
"v_cvt_pk_bf16_f32 v84, v84, v85\n\t"
"v_mfma_f32_16x16x32_bf16 v[92:95], a[212:215], a[196:199], v[92:95]\n\t"
"s_waitcnt lgkmcnt(6)\n\t"
"ds_read_b128 a[196:199], v255 offset:23040\n\t"
"v_cvt_pk_bf16_f32 v228, v228, v229\n\t"
"v_mfma_f32_16x16x32_bf16 v[92:95], a[216:219], a[200:203], v[92:95]\n\t"
"ds_read_b128 a[200:203], v255 offset:26112\n\t"
"v_cvt_pk_bf16_f32 v229, v230, v231\n\t"
"v_mfma_f32_16x16x32_bf16 v[92:95], a[220:223], a[204:207], v[92:95]\n\t"
"ds_read_b128 a[204:207], v255 offset:29184\n\t"
"v_cvt_pk_bf16_f32 v230, v232, v233\n\t"
"v_mfma_f32_16x16x32_bf16 v[100:103], a[224:227], a[168:171], v[100:103]\n\t"
"s_waitcnt lgkmcnt(8)\n\t"
"ds_read_b64_tr_b16 v[238:239], v240 offset:22016\n\t"
"v_cvt_pk_bf16_f32 v231, v234, v235\n\t"
"v_mfma_f32_16x16x32_bf16 v[100:103], a[228:231], a[172:175], v[100:103]\n\t"
"ds_read_b64_tr_b16 v[224:225], v241 offset:20480\n\t"
"v_cvt_pk_bf16_f32 v86, v86, v87\n\t"
"v_mfma_f32_16x16x32_bf16 v[100:103], a[236:239], a[176:179], v[100:103]\n\t"
"buffer_load_dwordx4 a[212:215], v252, s[8:11], s20 offen offset:2048 sc1\n\t"
"s_mov_b32 s20, 208896\n\t"
"v_mfma_f32_16x16x32_bf16 v[104:107], a[224:227], a[172:175], v[104:107]\n\t"
"buffer_load_dwordx4 a[216:219], v252, s[8:11], s20 offen offset:0 sc1\n\t"
"v_mfma_f32_16x16x32_bf16 v[104:107], a[228:231], a[176:179], v[104:107]\n\t"
"s_waitcnt lgkmcnt(6)\n\t"
"ds_read_b64_tr_b16 v[226:227], v241 offset:22016\n\t"
"v_mfma_f32_16x16x32_bf16 v[104:107], a[236:239], a[180:183], v[104:107]\n\t"
"Lpg13_%=:\n\t"
"s_cbranch_vccnz Lvr1_%=\n\t"
"s_waitcnt lgkmcnt(3)\n\t"
"s_barrier\n\t"
"v_mfma_f32_16x16x32_bf16 v[108:111], a[224:227], a[176:179], v[108:111]\n\t"
"v_accvgpr_write_b32 a140, v80\n\t"
"v_mfma_f32_16x16x32_bf16 v[108:111], a[228:231], a[180:183], v[108:111]\n\t"
"buffer_load_dwordx4 a[220:223], v252, s[8:11], s20 offen offset:1024 sc1\n\t"
"v_mfma_f32_16x16x32_bf16 v[108:111], a[236:239], a[184:187], v[108:111]\n\t"
"ds_write_b128 v242, v[228:231] offset:24576\n\t"
"v_mfma_f32_16x16x32_bf16 v[112:115], a[224:227], a[180:183], v[112:115]\n\t"
"s_waitcnt lgkmcnt(3)\n\t"
"v_accvgpr_write_b32 a141, v82\n\t"
"v_accvgpr_write_b32 a148, v84\n\t"
"v_mfma_f32_16x16x32_bf16 v[112:115], a[228:231], a[184:187], v[112:115]\n\t"
"s_waitcnt lgkmcnt(1)\n\t"
"v_accvgpr_write_b32 a149, v86\n\t"
"v_cvt_pk_bf16_f32 v88, v88, v89\n\t"
"v_mfma_f32_16x16x32_bf16 v[112:115], a[236:239], a[188:191], v[112:115]\n\t"
"buffer_store_dwordx4 a[52:55], v248, s[16:19], s29 offen offset:0 nt\n\t"
"ds_write_b64 v250, a[108:109] offset:0\n\t"
"v_cvt_pk_bf16_f32 v90, v90, v91\n\t"
"v_mfma_f32_16x16x32_bf16 v[236:239], v[236:239], a[136:139], 0\n\t"
"ds_write_b128 v249, a[40:43] offset:0\n\t"
"v_mfma_f32_16x16x32_bf16 v[224:227], v[224:227], a[136:139], 0\n\t"
"buffer_store_dwordx4 a[84:87], v248, s[16:19], s29 offen offset:128 nt\n\t"
"v_cvt_pk_bf16_f32 v92, v92, v93\n\t"
"v_cvt_pk_bf16_f32 v94, v94, v95\n\t"
"v_accvgpr_write_b32 a156, v88\n\t"
"v_accvgpr_write_b32 a157, v90\n\t"
"v_mfma_f32_16x16x32_bf16 v[116:119], a[224:227], a[184:187], v[116:119]\n\t"
"v_cvt_pk_bf16_f32 v100, v100, v101\n\t"
"v_cvt_pk_bf16_f32 v102, v102, v103\n\t"
"v_mfma_f32_16x16x32_bf16 v[116:119], a[228:231], a[188:191], v[116:119]\n\t"
"v_accvgpr_write_b32 a164, v92\n\t"
"v_accvgpr_write_b32 a165, v94\n\t"
"v_mfma_f32_16x16x32_bf16 v[116:119], a[236:239], a[192:195], v[116:119]\n\t"
"ds_read_b64_tr_b16 v[232:233], v240 offset:40448\n\t"
"v_cvt_pk_bf16_f32 v104, v104, v105\n\t"
"v_mfma_f32_16x16x32_bf16 v[120:123], a[224:227], a[188:191], v[120:123]\n\t"
"ds_read_b64_tr_b16 v[234:235], v240 offset:41984\n\t"
"v_cvt_pk_bf16_f32 v236, v236, v237\n\t"
"v_mfma_f32_16x16x32_bf16 v[120:123], a[228:231], a[192:195], v[120:123]\n\t"
"ds_read_b64_tr_b16 v[228:229], v241 offset:40448\n\t"
"v_cvt_pk_bf16_f32 v237, v238, v239\n\t"
"v_mfma_f32_16x16x32_bf16 v[120:123], a[236:239], a[196:199], v[120:123]\n\t"
"ds_read_b64_tr_b16 v[230:231], v241 offset:41984\n\t"
"v_cvt_pk_bf16_f32 v238, v224, v225\n\t"
"v_mfma_f32_16x16x32_bf16 v[124:127], a[224:227], a[192:195], v[124:127]\n\t"
"v_cvt_pk_bf16_f32 v239, v226, v227\n\t"
"v_cvt_pk_bf16_f32 v106, v106, v107\n\t"
"v_mfma_f32_16x16x32_bf16 v[124:127], a[228:231], a[196:199], v[124:127]\n\t"
"v_accvgpr_write_b32 a44, v100\n\t"
"v_accvgpr_write_b32 a45, v102\n\t"
"v_mfma_f32_16x16x32_bf16 v[124:127], a[236:239], a[200:203], v[124:127]\n\t"
"ds_write_b128 v242, v[236:239] offset:26112\n\t"
"v_mfma_f32_16x16x32_bf16 v[128:131], a[224:227], a[196:199], v[128:131]\n\t"
"buffer_load_dwordx4 a[224:227], v252, s[8:11], s20 offen offset:2048 sc1\n\t"
"v_mfma_f32_16x16x32_bf16 v[128:131], a[228:231], a[200:203], v[128:131]\n\t"
"s_mov_b32 s20, 294912\n\t"
"v_cvt_pk_bf16_f32 v108, v108, v109\n\t"
"v_cvt_pk_bf16_f32 v110, v110, v111\n\t"
"v_mfma_f32_16x16x32_bf16 v[128:131], a[236:239], a[204:207], v[128:131]\n\t"
"ds_write_b128 v249, a[56:59] offset:12288\n\t"
"v_mfma_f32_16x16x32_bf16 v[132:135], a[240:243], a[168:171], v[132:135]\n\t"
"buffer_load_dwordx4 a[228:231], v252, s[8:11], s20 offen offset:0 sc1\n\t"
"v_mfma_f32_16x16x32_bf16 v[132:135], a[244:247], a[172:175], v[132:135]\n\t"
"v_accvgpr_write_b32 a52, v104\n\t"
"v_accvgpr_write_b32 a53, v106\n\t"
"v_mfma_f32_16x16x32_bf16 v[132:135], a[248:251], a[176:179], v[132:135]\n\t"
"ds_write_b64 v250, a[124:125] offset:12288\n\t"
"v_mfma_f32_16x16x32_bf16 v[136:139], a[240:243], a[172:175], v[136:139]\n\t"
"buffer_load_dwordx4 a[236:239], v252, s[8:11], s20 offen offset:1024 sc1\n\t"
"v_mfma_f32_16x16x32_bf16 v[136:139], a[244:247], a[176:179], v[136:139]\n\t"
"s_waitcnt lgkmcnt(3)\n\t"
"v_cvt_pk_bf16_f32 v112, v112, v113\n\t"
"v_cvt_pk_bf16_f32 v114, v114, v115\n\t"
"v_mfma_f32_16x16x32_bf16 v[136:139], a[248:251], a[180:183], v[136:139]\n\t"
"buffer_store_dwordx4 v[196:199], v248, s[16:19], s29 offen offset:256 nt\n\t"
"ds_write_b64 v250, a[116:117] offset:6144\n\t"
"v_accvgpr_write_b32 a60, v108\n\t"
"v_mfma_f32_16x16x32_bf16 v[232:235], v[232:235], a[136:139], 0\n\t"
"s_add_u32 s29, s22, 983424\n\t"
"ds_write_b128 v249, a[48:51] offset:6144\n\t"
"v_accvgpr_write_b32 a61, v110\n\t"
"v_mfma_f32_16x16x32_bf16 v[228:231], v[228:231], a[136:139], 0\n\t"
"v_accvgpr_write_b32 a68, v112\n\t"
"v_accvgpr_write_b32 a69, v114\n\t"
"v_mfma_f32_16x16x32_bf16 v[140:143], a[240:243], a[176:179], v[140:143]\n\t"
"v_cvt_pk_bf16_f32 v116, v116, v117\n\t"
"v_cvt_pk_bf16_f32 v118, v118, v119\n\t"
"v_mfma_f32_16x16x32_bf16 v[140:143], a[244:247], a[180:183], v[140:143]\n\t"
"v_cvt_pk_bf16_f32 v120, v120, v121\n\t"
"v_cvt_pk_bf16_f32 v122, v122, v123\n\t"
"v_mfma_f32_16x16x32_bf16 v[140:143], a[248:251], a[184:187], v[140:143]\n\t"
"ds_write_b128 v249, a[64:67] offset:18432\n\t"
"v_mfma_f32_16x16x32_bf16 v[144:147], a[240:243], a[180:183], v[144:147]\n\t"
"v_cvt_pk_bf16_f32 v232, v232, v233\n\t"
"v_cvt_pk_bf16_f32 v233, v234, v235\n\t"
"v_mfma_f32_16x16x32_bf16 v[144:147], a[244:247], a[184:187], v[144:147]\n\t"
"v_accvgpr_write_b32 a76, v116\n\t"
"v_accvgpr_write_b32 a77, v118\n\t"
"v_mfma_f32_16x16x32_bf16 v[144:147], a[248:251], a[188:191], v[144:147]\n\t"
"ds_write_b64 v250, a[132:133] offset:18432\n\t"
"v_mfma_f32_16x16x32_bf16 v[148:151], a[240:243], a[184:187], v[148:151]\n\t"
"v_cvt_pk_bf16_f32 v234, v228, v229\n\t"
"v_cvt_pk_bf16_f32 v235, v230, v231\n\t"
"v_mfma_f32_16x16x32_bf16 v[148:151], a[244:247], a[188:191], v[148:151]\n\t"
"v_cvt_pk_bf16_f32 v124, v124, v125\n\t"
"v_cvt_pk_bf16_f32 v126, v126, v127\n\t"
"v_mfma_f32_16x16x32_bf16 v[148:151], a[248:251], a[192:195], v[148:151]\n\t"
"ds_write_b128 v242, v[232:235] offset:27648\n\t"
"v_mfma_f32_16x16x32_bf16 v[152:155], a[240:243], a[188:191], v[152:155]\n\t"
"v_accvgpr_write_b32 a84, v120\n\t"
"v_accvgpr_write_b32 a85, v122\n\t"
"v_mfma_f32_16x16x32_bf16 v[152:155], a[244:247], a[192:195], v[152:155]\n\t"
"v_cvt_pk_bf16_f32 v128, v128, v129\n\t"
"v_cvt_pk_bf16_f32 v130, v130, v131\n\t"
"v_mfma_f32_16x16x32_bf16 v[152:155], a[248:251], a[196:199], v[152:155]\n\t"
"buffer_store_dwordx4 v[204:207], v248, s[16:19], s29 offen offset:0 nt\n\t"
"v_accvgpr_write_b32 a92, v124\n\t"
"v_accvgpr_write_b32 a93, v126\n\t"
"v_cvt_pk_bf16_f32 v132, v132, v133\n\t"
"v_cvt_pk_bf16_f32 v134, v134, v135\n\t"
"v_mfma_f32_16x16x32_bf16 v[156:159], a[240:243], a[192:195], v[156:159]\n\t"
"v_accvgpr_write_b32 a100, v128\n\t"
"v_accvgpr_write_b32 a101, v130\n\t"
"v_mfma_f32_16x16x32_bf16 v[156:159], a[244:247], a[196:199], v[156:159]\n\t"
"v_cvt_pk_bf16_f32 v136, v136, v137\n\t"
"v_cvt_pk_bf16_f32 v138, v138, v139\n\t"
"v_mfma_f32_16x16x32_bf16 v[156:159], a[248:251], a[200:203], v[156:159]\n\t"
"v_accvgpr_write_b32 a46, v132\n\t"
"v_accvgpr_write_b32 a47, v134\n\t"
"v_mfma_f32_16x16x32_bf16 v[160:163], a[240:243], a[196:199], v[160:163]\n\t"
"v_accvgpr_write_b32 a54, v136\n\t"
"v_accvgpr_write_b32 a55, v138\n\t"
"v_mfma_f32_16x16x32_bf16 v[160:163], a[244:247], a[200:203], v[160:163]\n\t"
"v_cvt_pk_bf16_f32 v140, v140, v141\n\t"
"v_cvt_pk_bf16_f32 v142, v142, v143\n\t"
"v_mfma_f32_16x16x32_bf16 v[160:163], a[248:251], a[204:207], v[160:163]\n\t"
"v_cvt_pk_bf16_f32 v144, v144, v145\n\t"
"v_cvt_pk_bf16_f32 v146, v146, v147\n\t"
"v_mfma_f32_16x16x32_bf16 v[164:167], a[252:255], a[168:171], v[164:167]\n\t"
"v_accvgpr_write_b32 a62, v140\n\t"
"v_accvgpr_write_b32 a63, v142\n\t"
"v_mfma_f32_16x16x32_bf16 v[164:167], a[144:147], a[172:175], v[164:167]\n\t"
"v_cvt_pk_bf16_f32 v148, v148, v149\n\t"
"v_cvt_pk_bf16_f32 v150, v150, v151\n\t"
"v_mfma_f32_16x16x32_bf16 v[164:167], a[20:23], a[176:179], v[164:167]\n\t"
"buffer_load_dwordx4 a[240:243], v252, s[8:11], s20 offen offset:2048 sc1\n\t"
"s_mov_b32 s20, 307200\n\t"
"v_mfma_f32_16x16x32_bf16 v[168:171], a[252:255], a[172:175], v[168:171]\n\t"
"buffer_load_dwordx4 a[244:247], v252, s[8:11], s20 offen offset:0 sc1\n\t"
"v_mfma_f32_16x16x32_bf16 v[168:171], a[144:147], a[176:179], v[168:171]\n\t"
"buffer_load_dwordx4 a[248:251], v252, s[8:11], s20 offen offset:1024 sc1\n\t"
"v_mfma_f32_16x16x32_bf16 v[168:171], a[20:23], a[180:183], v[168:171]\n\t"
"buffer_store_dwordx4 v[212:215], v248, s[16:19], s29 offen offset:128 nt\n\t"
"v_accvgpr_write_b32 a70, v144\n\t"
"v_accvgpr_write_b32 a71, v146\n\t"
"v_cvt_pk_bf16_f32 v152, v152, v153\n\t"
"v_cvt_pk_bf16_f32 v154, v154, v155\n\t"
"v_mfma_f32_16x16x32_bf16 v[172:175], a[252:255], a[176:179], v[172:175]\n\t"
"v_accvgpr_write_b32 a78, v148\n\t"
"v_accvgpr_write_b32 a79, v150\n\t"
"v_mfma_f32_16x16x32_bf16 v[172:175], a[144:147], a[180:183], v[172:175]\n\t"
"v_cvt_pk_bf16_f32 v156, v156, v157\n\t"
"v_cvt_pk_bf16_f32 v158, v158, v159\n\t"
"v_mfma_f32_16x16x32_bf16 v[172:175], a[20:23], a[184:187], v[172:175]\n\t"
"v_accvgpr_write_b32 a86, v152\n\t"
"v_accvgpr_write_b32 a87, v154\n\t"
"v_mfma_f32_16x16x32_bf16 v[176:179], a[252:255], a[180:183], v[176:179]\n\t"
"v_cvt_pk_bf16_f32 v160, v160, v161\n\t"
"v_cvt_pk_bf16_f32 v162, v162, v163\n\t"
"v_mfma_f32_16x16x32_bf16 v[176:179], a[144:147], a[184:187], v[176:179]\n\t"
"v_accvgpr_write_b32 a94, v156\n\t"
"v_mfma_f32_16x16x32_bf16 v[176:179], a[20:23], a[188:191], v[176:179]\n\t"
"s_waitcnt lgkmcnt(0)\n\t"
"s_barrier\n\t"
"v_mfma_f32_16x16x32_bf16 v[180:183], a[252:255], a[184:187], v[180:183]\n\t"
"ds_read_b128 a[168:171], v244 offset:24576\n\t"
"v_mfma_f32_16x16x32_bf16 v[180:183], a[144:147], a[188:191], v[180:183]\n\t"
"ds_read_b128 a[172:175], v244 offset:25088\n\t"
"v_accvgpr_write_b32 a95, v158\n\t"
"v_mfma_f32_16x16x32_bf16 v[180:183], a[20:23], a[192:195], v[180:183]\n\t"
"ds_read_b128 a[176:179], v244 offset:25600\n\t"
"v_cvt_pk_bf16_f32 v164, v164, v165\n\t"
"v_mfma_f32_16x16x32_bf16 v[184:187], a[252:255], a[188:191], v[184:187]\n\t"
"ds_read_b128 a[180:183], v244 offset:26112\n\t"
"v_cvt_pk_bf16_f32 v166, v166, v167\n\t"
"v_mfma_f32_16x16x32_bf16 v[184:187], a[144:147], a[192:195], v[184:187]\n\t"
"ds_read_b128 a[184:187], v244 offset:26624\n\t"
"v_accvgpr_write_b32 a102, v160\n\t"
"v_mfma_f32_16x16x32_bf16 v[184:187], a[20:23], a[196:199], v[184:187]\n\t"
"buffer_load_dwordx4 a[48:51], v252, s[8:11], s20 offen offset:2048 sc1\n\t"
"s_mov_b32 s20, 319488\n\t"
"v_mfma_f32_16x16x32_bf16 v[188:191], a[252:255], a[192:195], v[188:191]\n\t"
"ds_read_b128 a[188:191], v244 offset:27136\n\t"
"v_accvgpr_write_b32 a103, v162\n\t"
"v_mfma_f32_16x16x32_bf16 v[188:191], a[144:147], a[196:199], v[188:191]\n\t"
"ds_read_b128 a[192:195], v244 offset:27648\n\t"
"v_cvt_pk_bf16_f32 v168, v168, v169\n\t"
"v_mfma_f32_16x16x32_bf16 v[188:191], a[20:23], a[200:203], v[188:191]\n\t"
"buffer_store_dwordx4 a[0:3], v248, s[16:19], s29 offen offset:256 nt\n\t"
"buffer_load_dwordx4 a[56:59], v252, s[8:11], s20 offen offset:0 sc1\n\t"
"v_cvt_pk_bf16_f32 v170, v170, v171\n\t"
"v_mfma_f32_16x16x32_bf16 v[192:195], a[252:255], a[196:199], v[192:195]\n\t"
"s_waitcnt lgkmcnt(4)\n\t"
"ds_read_b128 a[196:199], v244 offset:28160\n\t"
"v_accvgpr_write_b32 a110, v164\n\t"
"v_mfma_f32_16x16x32_bf16 v[192:195], a[144:147], a[200:203], v[192:195]\n\t"
"ds_read_b128 a[200:203], v244 offset:28672\n\t"
"v_accvgpr_write_b32 a111, v166\n\t"
"v_mfma_f32_16x16x32_bf16 v[192:195], a[20:23], a[204:207], v[192:195]\n\t"
"buffer_load_dwordx4 a[64:67], v252, s[8:11], s20 offen offset:1024 sc1\n\t"
"s_add_u32 s29, s22, 1180032\n\t"
"Lst18_%=:\n\t"
"v_mfma_f32_16x16x32_bf16 v[2:5], v[96:99], a[168:171], 0\n\t"
"buffer_load_dwordx4 a[144:147], v252, s[8:11], s20 offen offset:2048 sc1\n\t"
"v_mfma_f32_16x16x32_bf16 v[2:5], a[232:235], a[172:175], v[2:5]\n\t"
"s_waitcnt lgkmcnt(5)\n\t"
"ds_read_b128 a[204:207], v244 offset:29184\n\t"
"v_cvt_pk_bf16_f32 v172, v172, v173\n\t"
"v_mfma_f32_16x16x32_bf16 v[2:5], a[152:155], a[176:179], v[2:5]\n\t"
"s_mov_b32 s20, 479232\n\t"
"v_cvt_pk_bf16_f32 v174, v174, v175\n\t"
"v_accvgpr_write_b32 a118, v168\n\t"
"v_mfma_f32_16x16x32_bf16 v[6:9], v[96:99], a[172:175], 0\n\t"
"s_waitcnt lgkmcnt(5)\n\t"
"v_accvgpr_write_b32 a119, v170\n\t"
"v_cvt_pk_bf16_f32 v176, v176, v177\n\t"
"v_mfma_f32_16x16x32_bf16 v[6:9], a[232:235], a[176:179], v[6:9]\n\t"
"v_cvt_pk_bf16_f32 v178, v178, v179\n\t"
"v_accvgpr_write_b32 a126, v172\n\t"
"v_mfma_f32_16x16x32_bf16 v[6:9], a[152:155], a[180:183], v[6:9]\n\t"
"buffer_load_dwordx4 a[20:23], v252, s[8:11], s20 offen offset:0 sc1\n\t"
"v_mfma_f32_16x16x32_bf16 v[10:13], v[96:99], a[176:179], 0\n\t"
"buffer_load_dwordx4 a[252:255], v252, s[8:11], s20 offen offset:1024 sc1\n\t"
"v_mfma_f32_16x16x32_bf16 v[10:13], a[232:235], a[180:183], v[10:13]\n\t"
"s_waitcnt lgkmcnt(4)\n\t"
"v_accvgpr_write_b32 a127, v174\n\t"
"v_cvt_pk_bf16_f32 v180, v180, v181\n\t"
"v_mfma_f32_16x16x32_bf16 v[10:13], a[152:155], a[184:187], v[10:13]\n\t"
"buffer_store_dwordx4 a[8:11], v248, s[16:19], s29 offen offset:0 nt\n\t"
"v_cvt_pk_bf16_f32 v182, v182, v183\n\t"
"v_accvgpr_write_b32 a134, v176\n\t"
"v_accvgpr_write_b32 a135, v178\n\t"
"v_cvt_pk_bf16_f32 v184, v184, v185\n\t"
"v_mfma_f32_16x16x32_bf16 v[14:17], v[96:99], a[180:183], 0\n\t"
"s_waitcnt lgkmcnt(3)\n\t"
"v_cvt_pk_bf16_f32 v186, v186, v187\n\t"
"v_accvgpr_write_b32 a142, v180\n\t"
"v_mfma_f32_16x16x32_bf16 v[14:17], a[232:235], a[184:187], v[14:17]\n\t"
"v_accvgpr_write_b32 a143, v182\n\t"
"v_cvt_pk_bf16_f32 v188, v188, v189\n\t"
"v_mfma_f32_16x16x32_bf16 v[14:17], a[152:155], a[188:191], v[14:17]\n\t"
"v_cvt_pk_bf16_f32 v190, v190, v191\n\t"
"v_accvgpr_write_b32 a150, v184\n\t"
"v_mfma_f32_16x16x32_bf16 v[18:21], v[96:99], a[184:187], 0\n\t"
"s_waitcnt lgkmcnt(2)\n\t"
"v_accvgpr_write_b32 a151, v186\n\t"
"v_cvt_pk_bf16_f32 v192, v192, v193\n\t"
"v_mfma_f32_16x16x32_bf16 v[18:21], a[232:235], a[188:191], v[18:21]\n\t"
"v_cvt_pk_bf16_f32 v194, v194, v195\n\t"
"v_accvgpr_write_b32 a158, v188\n\t"
"v_mfma_f32_16x16x32_bf16 v[18:21], a[152:155], a[192:195], v[18:21]\n\t"
"ds_read_b128 v[196:199], v252 offset:0\n\t"
"v_accvgpr_write_b32 a159, v190\n\t"
"v_mfma_f32_16x16x32_bf16 v[22:25], v[96:99], a[188:191], 0\n\t"
"s_waitcnt lgkmcnt(2)\n\t"
"ds_read_b128 a[0:3], v252 offset:12288\n\t"
"v_accvgpr_write_b32 a166, v192\n\t"
"v_mfma_f32_16x16x32_bf16 v[22:25], a[232:235], a[192:195], v[22:25]\n\t"
"ds_read_b128 v[204:207], v252 offset:1024\n\t"
"v_accvgpr_write_b32 a167, v194\n\t"
"v_mfma_f32_16x16x32_bf16 v[22:25], a[152:155], a[196:199], v[22:25]\n\t"
"ds_read_b128 a[8:11], v252 offset:13312\n\t"
"v_mfma_f32_16x16x32_bf16 v[26:29], v[96:99], a[192:195], 0\n\t"
"s_waitcnt lgkmcnt(4)\n\t"
"ds_read_b128 v[212:215], v252 offset:2048\n\t"
"v_mfma_f32_16x16x32_bf16 v[26:29], a[232:235], a[196:199], v[26:29]\n\t"
"ds_read_b128 a[40:43], v252 offset:14336\n\t"
"v_mfma_f32_16x16x32_bf16 v[26:29], a[152:155], a[200:203], v[26:29]\n\t"
"buffer_store_dwordx4 a[16:19], v248, s[16:19], s29 offen offset:128 nt\n\t"
"v_mfma_f32_16x16x32_bf16 v[30:33], v[96:99], a[196:199], 0\n\t"
"s_waitcnt vmcnt(38)\n\t"
"v_mfma_f32_16x16x32_bf16 v[30:33], a[232:235], a[200:203], v[30:33]\n\t"
"buffer_load_dwordx4 a[232:235], v252, s[8:11], s20 offen offset:2048 sc1\n\t"
"v_mfma_f32_16x16x32_bf16 v[30:33], a[152:155], a[204:207], v[30:33]\n\t"
"s_mov_b32 s20, 491520\n\t"
"v_mfma_f32_16x16x32_bf16 v[34:37], a[28:31], a[168:171], 0\n\t"
"v_mfma_f32_16x16x32_bf16 v[34:37], a[160:163], a[172:175], v[34:37]\n\t"
"buffer_load_dwordx4 a[152:155], v252, s[8:11], s20 offen offset:0 sc1\n\t"
"v_mfma_f32_16x16x32_bf16 v[34:37], a[36:39], a[176:179], v[34:37]\n\t"
"buffer_store_dwordx4 a[24:27], v248, s[16:19], s29 offen offset:256 nt\n\t"
"ds_read_b64_tr_b16 v[224:225], v240 offset:1024\n\t"
"ds_read_b64_tr_b16 v[226:227], v240 offset:2560\n\t"
"v_mfma_f32_16x16x32_bf16 v[38:41], a[28:31], a[172:175], 0\n\t"
"v_mfma_f32_16x16x32_bf16 v[38:41], a[160:163], a[176:179], v[38:41]\n\t"
"v_mfma_f32_16x16x32_bf16 v[38:41], a[36:39], a[180:183], v[38:41]\n\t"
"v_mfma_f32_16x16x32_bf16 v[42:45], a[28:31], a[176:179], 0\n\t"
"ds_read_b64_tr_b16 v[236:237], v241 offset:1024\n\t"
"v_mfma_f32_16x16x32_bf16 v[42:45], a[160:163], a[180:183], v[42:45]\n\t"
"ds_read_b64_tr_b16 v[238:239], v241 offset:2560\n\t"
"v_mfma_f32_16x16x32_bf16 v[42:45], a[36:39], a[184:187], v[42:45]\n\t"
"s_add_u32 s29, s22, 1376640\n\t"
"v_mfma_f32_16x16x32_bf16 v[46:49], a[28:31], a[180:183], 0\n\t"
"v_mfma_f32_16x16x32_bf16 v[46:49], a[160:163], a[184:187], v[46:49]\n\t"
"v_mfma_f32_16x16x32_bf16 v[46:49], a[36:39], a[188:191], v[46:49]\n\t"
"s_waitcnt lgkmcnt(4)\n\t"
"s_barrier\n\t"
"v_mfma_f32_16x16x32_bf16 v[50:53], a[28:31], a[184:187], 0\n\t"
"v_mfma_f32_16x16x32_bf16 v[50:53], a[160:163], a[188:191], v[50:53]\n\t"
"v_mfma_f32_16x16x32_bf16 v[50:53], a[36:39], a[192:195], v[50:53]\n\t"
"buffer_store_dwordx4 a[32:35], v248, s[16:19], s29 offen offset:0 nt\n\t"
"ds_write_b64 v250, a[140:141] offset:0\n\t"
"v_mfma_f32_16x16x32_bf16 v[54:57], a[28:31], a[188:191], 0\n\t"
"s_waitcnt lgkmcnt(3)\n\t"
"v_mfma_f32_16x16x32_bf16 v[54:57], a[160:163], a[192:195], v[54:57]\n\t"
"ds_read_b64_tr_b16 v[228:229], v240 offset:20992\n\t"
"v_mfma_f32_16x16x32_bf16 v[54:57], a[36:39], a[196:199], v[54:57]\n\t"
"ds_read_b64_tr_b16 v[230:231], v240 offset:22528\n\t"
"v_mfma_f32_16x16x32_bf16 v[224:227], v[224:227], a[136:139], 0\n\t"
"ds_write_b128 v249, a[72:75] offset:0\n\t"
"v_mfma_f32_16x16x32_bf16 v[58:61], a[28:31], a[192:195], 0\n\t"
"s_waitcnt lgkmcnt(4)\n\t"
"v_mfma_f32_16x16x32_bf16 v[58:61], a[160:163], a[196:199], v[58:61]\n\t"
"ds_read_b64_tr_b16 v[232:233], v241 offset:20992\n\t"
"v_mfma_f32_16x16x32_bf16 v[58:61], a[36:39], a[200:203], v[58:61]\n\t"
"ds_write_b128 v249, a[88:91] offset:12288\n\t"
"v_mfma_f32_16x16x32_bf16 v[236:239], v[236:239], a[136:139], 0\n\t"
"v_mfma_f32_16x16x32_bf16 v[62:65], a[28:31], a[196:199], 0\n\t"
"buffer_load_dwordx4 a[28:31], v252, s[8:11], s20 offen offset:1024 sc1\n\t"
"v_mfma_f32_16x16x32_bf16 v[62:65], a[160:163], a[200:203], v[62:65]\n\t"
"s_waitcnt vmcnt(39)\n\t"
"ds_read_b64_tr_b16 v[234:235], v241 offset:22528\n\t"
"v_cvt_pk_bf16_f32 v224, v224, v225\n\t"
"v_mfma_f32_16x16x32_bf16 v[62:65], a[36:39], a[204:207], v[62:65]\n\t"
"ds_write_b64 v250, a[156:157] offset:12288\n\t"
"v_mfma_f32_16x16x32_bf16 v[66:69], v[200:203], a[168:171], 0\n\t"
"buffer_load_dwordx4 a[160:163], v252, s[8:11], s20 offen offset:2048 sc1\n\t"
"v_mfma_f32_16x16x32_bf16 v[66:69], a[120:123], a[172:175], v[66:69]\n\t"
"s_mov_b32 s20, 503808\n\t"
"ds_read_b128 a[168:171], v244 offset:33792\n\t"
"v_cvt_pk_bf16_f32 v225, v226, v227\n\t"
"v_mfma_f32_16x16x32_bf16 v[66:69], a[128:131], a[176:179], v[66:69]\n\t"
"ds_write_b128 v249, a[80:83] offset:6144\n\t"
"v_mfma_f32_16x16x32_bf16 v[70:73], v[200:203], a[172:175], 0\n\t"
"buffer_load_dwordx4 a[36:39], v252, s[8:11], s20 offen offset:0 sc1\n\t"
"v_mfma_f32_16x16x32_bf16 v[70:73], a[120:123], a[176:179], v[70:73]\n\t"
"s_waitcnt lgkmcnt(7)\n\t"
"ds_read_b128 a[172:175], v244 offset:34304\n\t"
"v_mfma_f32_16x16x32_bf16 v[70:73], a[128:131], a[180:183], v[70:73]\n\t"
"buffer_store_dwordx4 a[104:107], v248, s[16:19], s29 offen offset:128 nt\n\t"
"ds_write_b64 v250, a[148:149] offset:6144\n\t"
"v_cvt_pk_bf16_f32 v226, v236, v237\n\t"
"v_cvt_pk_bf16_f32 v227, v238, v239\n\t"
"v_mfma_f32_16x16x32_bf16 v[228:231], v[228:231], a[136:139], 0\n\t"
"ds_write_b128 v249, a[96:99] offset:18432\n\t"
"v_mfma_f32_16x16x32_bf16 v[74:77], v[200:203], a[176:179], 0\n\t"
"s_waitcnt lgkmcnt(6)\n\t"
"v_mfma_f32_16x16x32_bf16 v[74:77], a[120:123], a[180:183], v[74:77]\n\t"
"ds_read_b128 a[176:179], v244 offset:34816\n\t"
"v_mfma_f32_16x16x32_bf16 v[74:77], a[128:131], a[184:187], v[74:77]\n\t"
"ds_write_b64 v250, a[164:165] offset:18432\n\t"
"v_mfma_f32_16x16x32_bf16 v[232:235], v[232:235], a[136:139], 0\n\t"
"v_mfma_f32_16x16x32_bf16 v[78:81], v[200:203], a[180:183], 0\n\t"
"ds_read_b128 a[180:183], v244 offset:35328\n\t"
"v_cvt_pk_bf16_f32 v228, v228, v229\n\t"
"v_mfma_f32_16x16x32_bf16 v[78:81], a[120:123], a[184:187], v[78:81]\n\t"
"v_cvt_pk_bf16_f32 v229, v230, v231\n\t"
"v_mfma_f32_16x16x32_bf16 v[78:81], a[128:131], a[188:191], v[78:81]\n\t"
"buffer_store_dwordx4 a[112:115], v248, s[16:19], s29 offen offset:256 nt\n\t"
"v_mfma_f32_16x16x32_bf16 v[82:85], v[200:203], a[184:187], 0\n\t"
"ds_read_b128 a[184:187], v244 offset:35840\n\t"
"v_mfma_f32_16x16x32_bf16 v[82:85], a[120:123], a[188:191], v[82:85]\n\t"
"v_cvt_pk_bf16_f32 v230, v232, v233\n\t"
"v_cvt_pk_bf16_f32 v231, v234, v235\n\t"
"v_mfma_f32_16x16x32_bf16 v[82:85], a[128:131], a[192:195], v[82:85]\n\t"
"v_mfma_f32_16x16x32_bf16 v[86:89], v[200:203], a[188:191], 0\n\t"
"ds_read_b128 a[188:191], v244 offset:36352\n\t"
"v_mfma_f32_16x16x32_bf16 v[86:89], a[120:123], a[192:195], v[86:89]\n\t"
"v_mfma_f32_16x16x32_bf16 v[86:89], a[128:131], a[196:199], v[86:89]\n\t"
"v_mfma_f32_16x16x32_bf16 v[90:93], v[200:203], a[192:195], 0\n\t"
"ds_read_b128 a[192:195], v244 offset:36864\n\t"
"v_mfma_f32_16x16x32_bf16 v[90:93], a[120:123], a[196:199], v[90:93]\n\t"
"v_mfma_f32_16x16x32_bf16 v[90:93], a[128:131], a[200:203], v[90:93]\n\t"
"buffer_load_dwordx4 a[112:115], v252, s[8:11], s20 offen offset:1024 sc1\n\t"
"v_mfma_f32_16x16x32_bf16 v[94:97], v[200:203], a[196:199], 0\n\t"
"s_waitcnt vmcnt(37) lgkmcnt(5)\n\t"
"ds_read_b128 a[196:199], v244 offset:37376\n\t"
"v_mfma_f32_16x16x32_bf16 v[94:97], a[120:123], a[200:203], v[94:97]\n\t"
"ds_read_b128 a[200:203], v244 offset:37888\n\t"
"v_mfma_f32_16x16x32_bf16 v[94:97], a[128:131], a[204:207], v[94:97]\n\t"
"ds_read_b128 a[204:207], v244 offset:38400\n\t"
"v_mfma_f32_16x16x32_bf16 v[100:103], v[208:211], a[168:171], 0\n\t"
"buffer_load_dwordx4 v[200:203], v252, s[8:11], s20 offen offset:2048 sc1\n\t"
"v_mfma_f32_16x16x32_bf16 v[100:103], v[216:219], a[172:175], v[100:103]\n\t"
"s_waitcnt lgkmcnt(6)\n\t"
"v_mfma_f32_16x16x32_bf16 v[100:103], a[4:7], a[176:179], v[100:103]\n\t"
"ds_read_b64_tr_b16 v[236:237], v240 offset:40960\n\t"
"v_mfma_f32_16x16x32_bf16 v[104:107], v[208:211], a[172:175], 0\n\t"
"s_waitcnt lgkmcnt(6)\n\t"
"ds_read_b64_tr_b16 v[238:239], v240 offset:42496\n\t"
"v_mfma_f32_16x16x32_bf16 v[104:107], v[216:219], a[176:179], v[104:107]\n\t"
"ds_read_b64_tr_b16 v[232:233], v241 offset:40960\n\t"
"v_mfma_f32_16x16x32_bf16 v[104:107], a[4:7], a[180:183], v[104:107]\n\t"
"s_waitcnt lgkmcnt(3)\n\t"
"s_barrier\n\t"
"v_mfma_f32_16x16x32_bf16 v[108:111], v[208:211], a[176:179], 0\n\t"
"s_add_u32 s29, s23, 0\n\t"
"ds_read_b64_tr_b16 v[234:235], v241 offset:42496\n\t"
"v_mfma_f32_16x16x32_bf16 v[108:111], v[216:219], a[180:183], v[108:111]\n\t"
"v_add_u32 v240, 59904, v240\n\t"
"v_add_u32 v241, 59904, v241\n\t"
"v_mfma_f32_16x16x32_bf16 v[108:111], a[4:7], a[184:187], v[108:111]\n\t"
"ds_write_b128 v242, v[224:227] offset:24576\n\t"
"v_mfma_f32_16x16x32_bf16 v[112:115], v[208:211], a[180:183], 0\n\t"
"v_mfma_f32_16x16x32_bf16 v[112:115], v[216:219], a[184:187], v[112:115]\n\t"
"ds_read_b64_tr_b16 v[224:225], v240 offset:1024\n\t"
"v_mfma_f32_16x16x32_bf16 v[112:115], a[4:7], a[188:191], v[112:115]\n\t"
"ds_write_b128 v242, v[228:231] offset:26112\n\t"
"v_mfma_f32_16x16x32_bf16 v[116:119], v[208:211], a[184:187], 0\n\t"
"v_mfma_f32_16x16x32_bf16 v[116:119], v[216:219], a[188:191], v[116:119]\n\t"
"ds_read_b64_tr_b16 v[226:227], v240 offset:2560\n\t"
"v_mfma_f32_16x16x32_bf16 v[116:119], a[4:7], a[192:195], v[116:119]\n\t"
"buffer_store_dwordx4 v[196:199], v248, s[16:19], s29 offen offset:0 nt\n\t"
"ds_read_b64_tr_b16 v[228:229], v241 offset:1024\n\t"
"ds_read_b64_tr_b16 v[230:231], v241 offset:2560\n\t"
"v_mfma_f32_16x16x32_bf16 v[120:123], v[208:211], a[188:191], 0\n\t"
"s_waitcnt lgkmcnt(8)\n\t"
"v_mfma_f32_16x16x32_bf16 v[120:123], v[216:219], a[192:195], v[120:123]\n\t"
"s_waitcnt lgkmcnt(6)\n\t"
"v_mfma_f32_16x16x32_bf16 v[120:123], a[4:7], a[196:199], v[120:123]\n\t"
"s_mov_b32 s20, 110592\n\t"
"v_mfma_f32_16x16x32_bf16 v[236:239], v[236:239], a[136:139], 0\n\t"
"v_mfma_f32_16x16x32_bf16 v[232:235], v[232:235], a[136:139], 0\n\t"
"v_mfma_f32_16x16x32_bf16 v[124:127], v[208:211], a[192:195], 0\n\t"
"v_mfma_f32_16x16x32_bf16 v[124:127], v[216:219], a[196:199], v[124:127]\n\t"
"v_mfma_f32_16x16x32_bf16 v[124:127], a[4:7], a[200:203], v[124:127]\n\t"
"v_mfma_f32_16x16x32_bf16 v[128:131], v[208:211], a[196:199], 0\n\t"
"buffer_load_dwordx4 v[208:211], v252, s[8:11], s20 offen offset:0 sc1\n\t"
"v_mfma_f32_16x16x32_bf16 v[128:131], v[216:219], a[200:203], v[128:131]\n\t"
"s_waitcnt lgkmcnt(0)\n\t"
"v_cvt_pk_bf16_f32 v236, v236, v237\n\t"
"v_cvt_pk_bf16_f32 v237, v238, v239\n\t"
"v_mfma_f32_16x16x32_bf16 v[128:131], a[4:7], a[204:207], v[128:131]\n\t"
"s_waitcnt vmcnt(35)\n\t"
"v_cvt_pk_bf16_f32 v238, v232, v233\n\t"
"v_cvt_pk_bf16_f32 v239, v234, v235\n\t"
"v_mfma_f32_16x16x32_bf16 v[224:227], v[224:227], a[136:139], 0\n\t"
"ds_write_b128 v242, v[236:239] offset:27648\n\t"
"v_mfma_f32_16x16x32_bf16 v[228:231], v[228:231], a[136:139], 0\n\t"
"buffer_load_dwordx4 v[216:219], v252, s[8:11], s20 offen offset:1024 sc1\n\t"
"v_mfma_f32_16x16x32_bf16 v[132:135], a[12:15], a[168:171], 0\n\t"
"ds_read_b64_tr_b16 v[232:233], v240 offset:20992\n\t"
"v_mfma_f32_16x16x32_bf16 v[132:135], a[208:211], a[172:175], v[132:135]\n\t"
"ds_read_b64_tr_b16 v[234:235], v240 offset:22528\n\t"
"v_mfma_f32_16x16x32_bf16 v[132:135], a[212:215], a[176:179], v[132:135]\n\t"
"buffer_store_dwordx4 v[204:207], v248, s[16:19], s29 offen offset:128 nt\n\t"
"buffer_load_dwordx4 a[4:7], v252, s[8:11], s20 offen offset:2048 sc1\n\t"
"ds_read_b64_tr_b16 v[236:237], v241 offset:20992\n\t"
"v_mfma_f32_16x16x32_bf16 v[136:139], a[12:15], a[172:175], 0\n\t"
"ds_read_b64_tr_b16 v[238:239], v241 offset:22528\n\t"
"v_cvt_pk_bf16_f32 v224, v224, v225\n\t"
"v_mfma_f32_16x16x32_bf16 v[136:139], a[208:211], a[176:179], v[136:139]\n\t"
"v_cvt_pk_bf16_f32 v225, v226, v227\n\t"
"v_cvt_pk_bf16_f32 v226, v228, v229\n\t"
"v_mfma_f32_16x16x32_bf16 v[136:139], a[212:215], a[180:183], v[136:139]\n\t"
"v_cvt_pk_bf16_f32 v227, v230, v231\n\t"
"v_mfma_f32_16x16x32_bf16 v[140:143], a[12:15], a[176:179], 0\n\t"
"ds_read_b64_tr_b16 v[228:229], v240 offset:40960\n\t"
"v_mfma_f32_16x16x32_bf16 v[140:143], a[208:211], a[180:183], v[140:143]\n\t"
"ds_read_b64_tr_b16 v[230:231], v240 offset:42496\n\t"
"v_mfma_f32_16x16x32_bf16 v[140:143], a[212:215], a[184:187], v[140:143]\n\t"
"ds_write_b128 v242, v[224:227] offset:29184\n\t"
"v_mfma_f32_16x16x32_bf16 v[144:147], a[12:15], a[180:183], 0\n\t"
"s_waitcnt lgkmcnt(5)\n\t"
"v_mfma_f32_16x16x32_bf16 v[144:147], a[208:211], a[184:187], v[144:147]\n\t"
"s_waitcnt lgkmcnt(3)\n\t"
"ds_read_b64_tr_b16 v[224:225], v241 offset:40960\n\t"
"v_mfma_f32_16x16x32_bf16 v[144:147], a[212:215], a[188:191], v[144:147]\n\t"
"ds_read_b64_tr_b16 v[226:227], v241 offset:42496\n\t"
"v_mfma_f32_16x16x32_bf16 v[232:235], v[232:235], a[136:139], 0\n\t"
"buffer_store_dwordx4 v[212:215], v248, s[16:19], s29 offen offset:256 nt\n\t"
"v_mfma_f32_16x16x32_bf16 v[236:239], v[236:239], a[136:139], 0\n\t"
"v_mfma_f32_16x16x32_bf16 v[148:151], a[12:15], a[184:187], 0\n\t"
"v_mfma_f32_16x16x32_bf16 v[148:151], a[208:211], a[188:191], v[148:151]\n\t"
"v_mfma_f32_16x16x32_bf16 v[148:151], a[212:215], a[192:195], v[148:151]\n\t"
"s_add_u32 s29, s23, 393216\n\t"
"v_mfma_f32_16x16x32_bf16 v[152:155], a[12:15], a[188:191], 0\n\t"
"s_waitcnt lgkmcnt(3)\n\t"
"v_cvt_pk_bf16_f32 v232, v232, v233\n\t"
"v_cvt_pk_bf16_f32 v233, v234, v235\n\t"
"v_mfma_f32_16x16x32_bf16 v[152:155], a[208:211], a[192:195], v[152:155]\n\t"
"s_waitcnt lgkmcnt(0)\n\t"
"ds_read_b128 v[196:199], v252 offset:0\n\t"
"v_cvt_pk_bf16_f32 v234, v236, v237\n\t"
"v_mfma_f32_16x16x32_bf16 v[152:155], a[212:215], a[196:199], v[152:155]\n\t"
"ds_read_b128 a[16:19], v252 offset:12288\n\t"
"v_cvt_pk_bf16_f32 v235, v238, v239\n\t"
"v_mfma_f32_16x16x32_bf16 v[228:231], v[228:231], a[136:139], 0\n\t"
"s_mov_b32 s20, 122880\n\t"
"ds_write_b128 v242, v[232:235] offset:30720\n\t"
"v_mfma_f32_16x16x32_bf16 v[224:227], v[224:227], a[136:139], 0\n\t"
"v_mfma_f32_16x16x32_bf16 v[156:159], a[12:15], a[192:195], 0\n\t"
"ds_read_b128 v[204:207], v252 offset:1024\n\t"
"v_mfma_f32_16x16x32_bf16 v[156:159], a[208:211], a[196:199], v[156:159]\n\t"
"ds_read_b128 a[24:27], v252 offset:13312\n\t"
"v_mfma_f32_16x16x32_bf16 v[156:159], a[212:215], a[200:203], v[156:159]\n\t"
"buffer_store_dwordx4 a[0:3], v248, s[16:19], s29 offen offset:0 nt\n\t"
"ds_read_b128 v[212:215], v252 offset:2048\n\t"
"ds_read_b128 a[32:35], v252 offset:14336\n\t"
"v_mfma_f32_16x16x32_bf16 v[160:163], a[12:15], a[196:199], 0\n\t"
"s_waitcnt vmcnt(35)\n\t"
"v_cvt_pk_bf16_f32 v228, v228, v229\n\t"
"v_cvt_pk_bf16_f32 v229, v230, v231\n\t"
"v_mfma_f32_16x16x32_bf16 v[160:163], a[208:211], a[200:203], v[160:163]\n\t"
"v_cvt_pk_bf16_f32 v230, v224, v225\n\t"
"v_cvt_pk_bf16_f32 v231, v226, v227\n\t"
"v_mfma_f32_16x16x32_bf16 v[160:163], a[212:215], a[204:207], v[160:163]\n\t"
"ds_write_b128 v242, v[228:231] offset:32256\n\t"
"v_mfma_f32_16x16x32_bf16 v[164:167], a[216:219], a[168:171], 0\n\t"
"buffer_load_dwordx4 a[208:211], v252, s[8:11], s20 offen offset:0 sc1\n\t"
"v_mfma_f32_16x16x32_bf16 v[164:167], a[220:223], a[172:175], v[164:167]\n\t"
"buffer_load_dwordx4 a[212:215], v252, s[8:11], s20 offen offset:1024 sc1\n\t"
"v_mfma_f32_16x16x32_bf16 v[164:167], a[224:227], a[176:179], v[164:167]\n\t"
"v_mfma_f32_16x16x32_bf16 v[168:171], a[216:219], a[172:175], 0\n\t"
"v_mfma_f32_16x16x32_bf16 v[168:171], a[220:223], a[176:179], v[168:171]\n\t"
"v_mfma_f32_16x16x32_bf16 v[168:171], a[224:227], a[180:183], v[168:171]\n\t"
"v_mfma_f32_16x16x32_bf16 v[172:175], a[216:219], a[176:179], 0\n\t"
"buffer_load_dwordx4 v[240:243], v252, s[8:11], s20 offen offset:2048 sc1\n\t"
"v_mfma_f32_16x16x32_bf16 v[172:175], a[220:223], a[180:183], v[172:175]\n\t"
"v_mfma_f32_16x16x32_bf16 v[172:175], a[224:227], a[184:187], v[172:175]\n\t"
"Lpg14_%=:\n\t"
"s_cbranch_vccnz Lvr1_%=\n\t"
"v_mfma_f32_16x16x32_bf16 v[176:179], a[216:219], a[180:183], 0\n\t"
"v_mfma_f32_16x16x32_bf16 v[176:179], a[220:223], a[184:187], v[176:179]\n\t"
"v_mfma_f32_16x16x32_bf16 v[176:179], a[224:227], a[188:191], v[176:179]\n\t"
"s_waitcnt lgkmcnt(0)\n\t"
"s_barrier\n\t"
"v_mfma_f32_16x16x32_bf16 v[180:183], a[216:219], a[184:187], 0\n\t"
"ds_read_b128 a[168:171], v244 offset:24576\n\t"
"v_mfma_f32_16x16x32_bf16 v[180:183], a[220:223], a[188:191], v[180:183]\n\t"
"ds_read_b128 a[172:175], v244 offset:25088\n\t"
"v_mfma_f32_16x16x32_bf16 v[180:183], a[224:227], a[192:195], v[180:183]\n\t"
"ds_read_b128 a[176:179], v244 offset:25600\n\t"
"v_mfma_f32_16x16x32_bf16 v[184:187], a[216:219], a[188:191], 0\n\t"
"s_mov_b32 s20, 135168\n\t"
"ds_read_b128 a[180:183], v244 offset:26112\n\t"
"v_mfma_f32_16x16x32_bf16 v[184:187], a[220:223], a[192:195], v[184:187]\n\t"
"ds_read_b128 a[184:187], v244 offset:26624\n\t"
"v_mfma_f32_16x16x32_bf16 v[184:187], a[224:227], a[196:199], v[184:187]\n\t"
"buffer_store_dwordx4 a[8:11], v248, s[16:19], s29 offen offset:128 nt\n\t"
"ds_read_b128 a[188:191], v244 offset:27136\n\t"
"v_mfma_f32_16x16x32_bf16 v[188:191], a[216:219], a[192:195], 0\n\t"
"ds_read_b128 a[192:195], v244 offset:27648\n\t"
"v_mfma_f32_16x16x32_bf16 v[188:191], a[220:223], a[196:199], v[188:191]\n\t"
"v_mfma_f32_16x16x32_bf16 v[188:191], a[224:227], a[200:203], v[188:191]\n\t"
"ds_write_b128 v249, a[44:47] offset:0\n\t"
"v_mfma_f32_16x16x32_bf16 v[192:195], a[216:219], a[196:199], 0\n\t"
"buffer_load_dwordx4 a[216:219], v252, s[8:11], s20 offen offset:0 sc1\n\t"
"v_mfma_f32_16x16x32_bf16 v[192:195], a[220:223], a[200:203], v[192:195]\n\t"
"s_waitcnt vmcnt(35) lgkmcnt(5)\n\t"
"ds_read_b128 a[196:199], v244 offset:28160\n\t"
"v_mfma_f32_16x16x32_bf16 v[192:195], a[224:227], a[204:207], v[192:195]\n\t"
"ds_write_b128 v249, a[60:63] offset:12288\n\t"
"Lst19_%=:\n\t"
"v_mfma_f32_16x16x32_bf16 v[2:5], a[228:231], a[168:171], v[2:5]\n\t"
"buffer_load_dwordx4 a[220:223], v252, s[8:11], s20 offen offset:1024 sc1\n\t"
"v_mfma_f32_16x16x32_bf16 v[2:5], a[236:239], a[172:175], v[2:5]\n\t"
"s_waitcnt lgkmcnt(6)\n\t"
"ds_read_b128 a[200:203], v244 offset:28672\n\t"
"v_mfma_f32_16x16x32_bf16 v[2:5], a[240:243], a[176:179], v[2:5]\n\t"
"buffer_store_dwordx4 a[40:43], v248, s[16:19], s29 offen offset:256 nt\n\t"
"ds_read_b128 a[204:207], v244 offset:29184\n\t"
"v_mfma_f32_16x16x32_bf16 v[6:9], a[228:231], a[172:175], v[6:9]\n\t"
"s_waitcnt lgkmcnt(7)\n\t"
"s_add_u32 s29, s23, 786432\n\t"
"v_mfma_f32_16x16x32_bf16 v[6:9], a[236:239], a[176:179], v[6:9]\n\t"
"buffer_load_dwordx4 a[224:227], v252, s[8:11], s20 offen offset:2048 sc1\n\t"
"v_mfma_f32_16x16x32_bf16 v[6:9], a[240:243], a[180:183], v[6:9]\n\t"
"ds_write_b64 v250, a[110:111] offset:0\n\t"
"v_mfma_f32_16x16x32_bf16 v[10:13], a[228:231], a[176:179], v[10:13]\n\t"
"s_waitcnt lgkmcnt(7)\n\t"
"v_mfma_f32_16x16x32_bf16 v[10:13], a[236:239], a[180:183], v[10:13]\n\t"
"v_mfma_f32_16x16x32_bf16 v[10:13], a[240:243], a[184:187], v[10:13]\n\t"
"ds_write_b64 v250, a[126:127] offset:12288\n\t"
"v_mfma_f32_16x16x32_bf16 v[14:17], a[228:231], a[180:183], v[14:17]\n\t"
"s_waitcnt lgkmcnt(7)\n\t"
"v_mfma_f32_16x16x32_bf16 v[14:17], a[236:239], a[184:187], v[14:17]\n\t"
"v_mfma_f32_16x16x32_bf16 v[14:17], a[240:243], a[188:191], v[14:17]\n\t"
"buffer_store_dwordx4 v[196:199], v248, s[16:19], s29 offen offset:0 nt\n\t"
"ds_write_b64 v250, a[118:119] offset:6144\n\t"
"v_mfma_f32_16x16x32_bf16 v[18:21], a[228:231], a[184:187], v[18:21]\n\t"
"s_waitcnt lgkmcnt(6)\n\t"
"v_mfma_f32_16x16x32_bf16 v[18:21], a[236:239], a[188:191], v[18:21]\n\t"
"v_mfma_f32_16x16x32_bf16 v[18:21], a[240:243], a[192:195], v[18:21]\n\t"
"ds_write_b128 v249, a[52:55] offset:6144\n\t"
"v_mfma_f32_16x16x32_bf16 v[22:25], a[228:231], a[188:191], v[22:25]\n\t"
"s_waitcnt lgkmcnt(5)\n\t"
"s_mov_b32 s20, 147456\n\t"
"v_mfma_f32_16x16x32_bf16 v[22:25], a[236:239], a[192:195], v[22:25]\n\t"
"v_mfma_f32_16x16x32_bf16 v[22:25], a[240:243], a[196:199], v[22:25]\n\t"
"ds_write_b128 v249, a[68:71] offset:18432\n\t"
"v_mfma_f32_16x16x32_bf16 v[26:29], a[228:231], a[192:195], v[26:29]\n\t"
"s_waitcnt lgkmcnt(5)\n\t"
"v_mfma_f32_16x16x32_bf16 v[26:29], a[236:239], a[196:199], v[26:29]\n\t"
"v_mfma_f32_16x16x32_bf16 v[26:29], a[240:243], a[200:203], v[26:29]\n\t"
"ds_write_b64 v250, a[134:135] offset:18432\n\t"
"v_mfma_f32_16x16x32_bf16 v[30:33], a[228:231], a[196:199], v[30:33]\n\t"
"buffer_load_dwordx4 a[228:231], v252, s[8:11], s20 offen offset:0 sc1\n\t"
"v_mfma_f32_16x16x32_bf16 v[30:33], a[236:239], a[200:203], v[30:33]\n\t"
"s_waitcnt vmcnt(36)\n\t"
"v_mfma_f32_16x16x32_bf16 v[30:33], a[240:243], a[204:207], v[30:33]\n\t"
"buffer_store_dwordx4 v[204:207], v248, s[16:19], s29 offen offset:128 nt\n\t"
"v_mfma_f32_16x16x32_bf16 v[34:37], a[244:247], a[168:171], v[34:37]\n\t"
"v_mfma_f32_16x16x32_bf16 v[34:37], a[248:251], a[172:175], v[34:37]\n\t"
"buffer_load_dwordx4 a[236:239], v252, s[8:11], s20 offen offset:1024 sc1\n\t"
"v_mfma_f32_16x16x32_bf16 v[34:37], a[48:51], a[176:179], v[34:37]\n\t"
"ds_read_b64_tr_b16 v[236:237], v246 offset:512\n\t"
"v_mfma_f32_16x16x32_bf16 v[38:41], a[244:247], a[172:175], v[38:41]\n\t"
"buffer_load_dwordx4 a[240:243], v252, s[8:11], s20 offen offset:2048 sc1\n\t"
"v_mfma_f32_16x16x32_bf16 v[38:41], a[248:251], a[176:179], v[38:41]\n\t"
"ds_read_b64_tr_b16 v[238:239], v246 offset:3584\n\t"
"v_mfma_f32_16x16x32_bf16 v[38:41], a[48:51], a[180:183], v[38:41]\n\t"
"v_mfma_f32_16x16x32_bf16 v[42:45], a[244:247], a[176:179], v[42:45]\n\t"
"v_mfma_f32_16x16x32_bf16 v[42:45], a[248:251], a[180:183], v[42:45]\n\t"
"v_mfma_f32_16x16x32_bf16 v[42:45], a[48:51], a[184:187], v[42:45]\n\t"
"v_mfma_f32_16x16x32_bf16 v[46:49], a[244:247], a[180:183], v[46:49]\n\t"
"ds_read_b64_tr_b16 v[232:233], v246 offset:6656\n\t"
"v_mfma_f32_16x16x32_bf16 v[46:49], a[248:251], a[184:187], v[46:49]\n\t"
"ds_read_b64_tr_b16 v[234:235], v246 offset:9728\n\t"
"v_mfma_f32_16x16x32_bf16 v[46:49], a[48:51], a[188:191], v[46:49]\n\t"
"s_waitcnt lgkmcnt(4)\n\t"
"s_barrier\n\t"
"v_mfma_f32_16x16x32_bf16 v[50:53], a[244:247], a[184:187], v[50:53]\n\t"
"v_mfma_f32_16x16x32_bf16 v[50:53], a[248:251], a[188:191], v[50:53]\n\t"
"v_mfma_f32_16x16x32_bf16 v[50:53], a[48:51], a[192:195], v[50:53]\n\t"
"buffer_store_dwordx4 v[212:215], v248, s[16:19], s29 offen offset:256 nt\n\t"
"v_mfma_f32_16x16x32_bf16 v[54:57], a[244:247], a[188:191], v[54:57]\n\t"
"s_waitcnt lgkmcnt(2)\n\t"
"v_mfma_f32_16x16x32_bf16 v[54:57], a[248:251], a[192:195], v[54:57]\n\t"
"s_mov_b32 s20, 159744\n\t"
"v_mfma_f32_16x16x32_bf16 v[54:57], a[48:51], a[196:199], v[54:57]\n\t"
"v_mfma_f32_16x16x32_bf16 v[236:239], v[236:239], v[220:223], 0\n\t"
"s_add_u32 s29, s23, 1179648\n\t"
"v_mfma_f32_16x16x32_bf16 v[58:61], a[244:247], a[192:195], v[58:61]\n\t"
"ds_read_b64_tr_b16 v[224:225], v246 offset:12800\n\t"
"v_mfma_f32_16x16x32_bf16 v[58:61], a[248:251], a[196:199], v[58:61]\n\t"
"ds_read_b64_tr_b16 v[226:227], v246 offset:15872\n\t"
"v_mfma_f32_16x16x32_bf16 v[58:61], a[48:51], a[200:203], v[58:61]\n\t"
"v_mfma_f32_16x16x32_bf16 v[62:65], a[244:247], a[196:199], v[62:65]\n\t"
"buffer_load_dwordx4 a[244:247], v252, s[8:11], s20 offen offset:0 sc1\n\t"
"v_mfma_f32_16x16x32_bf16 v[62:65], a[248:251], a[200:203], v[62:65]\n\t"
"s_waitcnt vmcnt(37) lgkmcnt(2)\n\t"
"v_cvt_pk_bf16_f32 v236, v236, v237\n\t"
"v_cvt_pk_bf16_f32 v237, v238, v239\n\t"
"v_mfma_f32_16x16x32_bf16 v[62:65], a[48:51], a[204:207], v[62:65]\n\t"
"v_mfma_f32_16x16x32_bf16 v[232:235], v[232:235], v[220:223], 0\n\t"
"buffer_load_dwordx4 a[248:251], v252, s[8:11], s20 offen offset:1024 sc1\n\t"
"v_mfma_f32_16x16x32_bf16 v[66:69], a[56:59], a[168:171], v[66:69]\n\t"
"ds_read_b64_tr_b16 v[228:229], v246 offset:20480\n\t"
"v_mfma_f32_16x16x32_bf16 v[66:69], a[64:67], a[172:175], v[66:69]\n\t"
"ds_read_b64_tr_b16 v[230:231], v246 offset:23552\n\t"
"v_mfma_f32_16x16x32_bf16 v[66:69], a[144:147], a[176:179], v[66:69]\n\t"
"buffer_store_dwordx4 a[16:19], v248, s[16:19], s29 offen offset:0 nt\n\t"
"ds_read_b128 a[168:171], v244 offset:33792\n\t"
"v_mfma_f32_16x16x32_bf16 v[70:73], a[56:59], a[172:175], v[70:73]\n\t"
"ds_read_b128 a[172:175], v244 offset:34304\n\t"
"v_mfma_f32_16x16x32_bf16 v[70:73], a[64:67], a[176:179], v[70:73]\n\t"
"v_cvt_pk_bf16_f32 v232, v232, v233\n\t"
"v_cvt_pk_bf16_f32 v233, v234, v235\n\t"
"v_mfma_f32_16x16x32_bf16 v[70:73], a[144:147], a[180:183], v[70:73]\n\t"
"v_mfma_f32_16x16x32_bf16 v[74:77], a[56:59], a[176:179], v[74:77]\n\t"
"s_waitcnt lgkmcnt(4)\n\t"
"ds_read_b128 a[176:179], v244 offset:34816\n\t"
"v_mfma_f32_16x16x32_bf16 v[74:77], a[64:67], a[180:183], v[74:77]\n\t"
"v_mfma_f32_16x16x32_bf16 v[74:77], a[144:147], a[184:187], v[74:77]\n\t"
"v_mfma_f32_16x16x32_bf16 v[224:227], v[224:227], v[220:223], 0\n\t"
"v_mfma_f32_16x16x32_bf16 v[78:81], a[56:59], a[180:183], v[78:81]\n\t"
"ds_read_b128 a[180:183], v244 offset:35328\n\t"
"v_mfma_f32_16x16x32_bf16 v[78:81], a[64:67], a[184:187], v[78:81]\n\t"
"ds_read_b128 v[196:199], v252 offset:0\n\t"
"v_mfma_f32_16x16x32_bf16 v[78:81], a[144:147], a[188:191], v[78:81]\n\t"
"buffer_store_dwordx4 a[24:27], v248, s[16:19], s29 offen offset:128 nt\n\t"
"ds_read_b128 a[40:43], v252 offset:12288\n\t"
"ds_read_b128 v[204:207], v252 offset:1024\n\t"
"v_mfma_f32_16x16x32_bf16 v[82:85], a[56:59], a[184:187], v[82:85]\n\t"
"s_waitcnt lgkmcnt(7)\n\t"
"ds_read_b128 a[184:187], v244 offset:35840\n\t"
"v_mfma_f32_16x16x32_bf16 v[82:85], a[64:67], a[188:191], v[82:85]\n\t"
"ds_read_b128 a[72:75], v252 offset:13312\n\t"
"v_cvt_pk_bf16_f32 v224, v224, v225\n\t"
"v_mfma_f32_16x16x32_bf16 v[82:85], a[144:147], a[192:195], v[82:85]\n\t"
"ds_read_b128 a[8:11], v252 offset:2048\n\t"
"v_cvt_pk_bf16_f32 v225, v226, v227\n\t"
"v_mfma_f32_16x16x32_bf16 v[228:231], v[228:231], v[220:223], 0\n\t"
"ds_read_b128 a[104:107], v252 offset:14336\n\t"
"v_mfma_f32_16x16x32_bf16 v[86:89], a[56:59], a[188:191], v[86:89]\n\t"
"ds_read_b128 a[188:191], v244 offset:36352\n\t"
"v_mfma_f32_16x16x32_bf16 v[86:89], a[64:67], a[192:195], v[86:89]\n\t"
"v_mfma_f32_16x16x32_bf16 v[86:89], a[144:147], a[196:199], v[86:89]\n\t"
"v_mfma_f32_16x16x32_bf16 v[90:93], a[56:59], a[192:195], v[90:93]\n\t"
"ds_read_b128 a[192:195], v244 offset:36864\n\t"
"v_mfma_f32_16x16x32_bf16 v[90:93], a[64:67], a[196:199], v[90:93]\n\t"
"v_cvt_pk_bf16_f32 v228, v228, v229\n\t"
"v_cvt_pk_bf16_f32 v229, v230, v231\n\t"
"v_mfma_f32_16x16x32_bf16 v[90:93], a[144:147], a[200:203], v[90:93]\n\t"
"v_mfma_f32_16x16x32_bf16 v[94:97], a[56:59], a[196:199], v[94:97]\n\t"
"s_waitcnt vmcnt(35) lgkmcnt(10)\n\t"
"ds_read_b128 a[196:199], v244 offset:37376\n\t"
"v_mfma_f32_16x16x32_bf16 v[94:97], a[64:67], a[200:203], v[94:97]\n\t"
"ds_read_b128 a[200:203], v244 offset:37888\n\t"
"v_mfma_f32_16x16x32_bf16 v[94:97], a[144:147], a[204:207], v[94:97]\n\t"
"ds_read_b128 a[204:207], v244 offset:38400\n\t"
"v_mfma_f32_16x16x32_bf16 v[100:103], a[20:23], a[168:171], v[100:103]\n\t"
"s_waitcnt lgkmcnt(12)\n\t"
"v_mfma_f32_16x16x32_bf16 v[100:103], a[252:255], a[172:175], v[100:103]\n\t"
"v_mfma_f32_16x16x32_bf16 v[100:103], a[232:235], a[176:179], v[100:103]\n\t"
"buffer_store_dwordx4 a[32:35], v248, s[16:19], s29 offen offset:256 nt\n\t"
"v_mfma_f32_16x16x32_bf16 v[104:107], a[20:23], a[172:175], v[104:107]\n\t"
"s_waitcnt lgkmcnt(8)\n\t"
"v_mfma_f32_16x16x32_bf16 v[104:107], a[252:255], a[176:179], v[104:107]\n\t"
"v_mfma_f32_16x16x32_bf16 v[104:107], a[232:235], a[180:183], v[104:107]\n\t"
"s_waitcnt lgkmcnt(0)\n\t"
"s_barrier\n\t"
"v_mfma_f32_16x16x32_bf16 v[108:111], a[20:23], a[176:179], v[108:111]\n\t"
"s_add_u32 s29, s23, 1152\n\t"
"v_fma_f32 v0, v2, s52, v100\n\t"
"v_mfma_f32_16x16x32_bf16 v[108:111], a[252:255], a[180:183], v[108:111]\n\t"
"v_fma_f32 v1, v3, s52, v101\n\t"
"v_mfma_f32_16x16x32_bf16 v[108:111], a[232:235], a[184:187], v[108:111]\n\t"
"ds_write_b64 v245, v[236:237] offset:24576\n\t"
"v_mfma_f32_16x16x32_bf16 v[112:115], a[20:23], a[180:183], v[112:115]\n\t"
"ds_read_b64_tr_b16 v[236:237], v246 offset:26624\n\t"
"v_mfma_f32_16x16x32_bf16 v[112:115], a[252:255], a[184:187], v[112:115]\n\t"
"ds_read_b64_tr_b16 v[238:239], v246 offset:29696\n\t"
"v_mfma_f32_16x16x32_bf16 v[112:115], a[232:235], a[188:191], v[112:115]\n\t"
"ds_write_b64 v245, v[232:233] offset:25088\n\t"
"v_mfma_f32_16x16x32_bf16 v[116:119], a[20:23], a[184:187], v[116:119]\n\t"
"ds_read_b64_tr_b16 v[232:233], v246 offset:32768\n\t"
"v_fma_f32 v212, -v10, s56, -v108\n\t"
"v_mfma_f32_16x16x32_bf16 v[116:119], a[252:255], a[188:191], v[116:119]\n\t"
"ds_read_b64_tr_b16 v[234:235], v246 offset:35840\n\t"
"v_fma_f32 v213, -v11, s56, -v109\n\t"
"v_mfma_f32_16x16x32_bf16 v[116:119], a[232:235], a[192:195], v[116:119]\n\t"
"ds_write_b64 v245, v[224:225] offset:25600\n\t"
"v_mfma_f32_16x16x32_bf16 v[120:123], a[20:23], a[188:191], v[120:123]\n\t"
"s_waitcnt lgkmcnt(4)\n\t"
"ds_read_b64_tr_b16 v[224:225], v246 offset:40448\n\t"
"v_fma_f32 v214, -v12, s56, -v110\n\t"
"v_mfma_f32_16x16x32_bf16 v[120:123], a[252:255], a[192:195], v[120:123]\n\t"
"ds_read_b64_tr_b16 v[226:227], v246 offset:43520\n\t"
"v_fma_f32 v215, -v13, s56, -v111\n\t"
"v_mfma_f32_16x16x32_bf16 v[120:123], a[232:235], a[196:199], v[120:123]\n\t"
"ds_write_b64 v245, v[228:229] offset:26112\n\t"
"v_mfma_f32_16x16x32_bf16 v[236:239], v[236:239], v[220:223], 0\n\t"
"buffer_store_dwordx4 v[196:199], v248, s[16:19], s29 offen offset:0 nt\n\t"
"ds_read_b64_tr_b16 v[228:229], v246 offset:46592\n\t"
"ds_read_b64_tr_b16 v[230:231], v246 offset:49664\n\t"
"v_fma_f32 v98, -v14, s56, -v112\n\t"
"v_mfma_f32_16x16x32_bf16 v[124:127], a[20:23], a[192:195], v[124:127]\n\t"
"s_waitcnt lgkmcnt(6)\n\t"
"v_fma_f32 v99, -v15, s56, -v113\n\t"
"v_accvgpr_write_b32 a0, v98\n\t"
"v_mfma_f32_16x16x32_bf16 v[124:127], a[252:255], a[196:199], v[124:127]\n\t"
"v_fma_f32 v196, -v2, s56, -v100\n\t"
"v_fma_f32 v197, -v3, s56, -v101\n\t"
"v_mfma_f32_16x16x32_bf16 v[124:127], a[232:235], a[200:203], v[124:127]\n\t"
"ds_write_b128 v249, a[76:79] offset:0\n\t"
"v_mfma_f32_16x16x32_bf16 v[232:235], v[232:235], v[220:223], 0\n\t"
"ds_write_b128 v249, a[92:95] offset:12288\n\t"
"v_mfma_f32_16x16x32_bf16 v[128:131], a[20:23], a[196:199], v[128:131]\n\t"
"s_waitcnt lgkmcnt(5)\n\t"
"v_cvt_pk_bf16_f32 v236, v236, v237\n\t"
"v_cvt_pk_bf16_f32 v237, v238, v239\n\t"
"v_mfma_f32_16x16x32_bf16 v[128:131], a[252:255], a[200:203], v[128:131]\n\t"
"s_waitcnt vmcnt(32)\n\t"
"v_fma_f32 v198, -v4, s56, -v102\n\t"
"v_fma_f32 v199, -v5, s56, -v103\n\t"
"v_mfma_f32_16x16x32_bf16 v[128:131], a[232:235], a[204:207], v[128:131]\n\t"
"ds_write_b64 v250, a[142:143] offset:0\n\t"
"v_mfma_f32_16x16x32_bf16 v[224:227], v[224:227], v[220:223], 0\n\t"
"buffer_load_dwordx4 a[252:255], v252, s[8:11], s20 offen offset:2048 sc1\n\t"
"s_mov_b32 s20, 172032\n\t"
"v_mfma_f32_16x16x32_bf16 v[132:135], a[152:155], a[168:171], v[132:135]\n\t"
"buffer_load_dwordx4 a[232:235], v252, s[8:11], s20 offen offset:0 sc1\n\t"
"v_mfma_f32_16x16x32_bf16 v[132:135], a[28:31], a[172:175], v[132:135]\n\t"
"s_waitcnt lgkmcnt(3)\n\t"
"v_cvt_pk_bf16_f32 v232, v232, v233\n\t"
"v_cvt_pk_bf16_f32 v233, v234, v235\n\t"
"v_mfma_f32_16x16x32_bf16 v[132:135], a[160:163], a[176:179], v[132:135]\n\t"
"ds_write_b64 v250, a[158:159] offset:12288\n\t"
"v_mfma_f32_16x16x32_bf16 v[228:231], v[228:231], v[220:223], 0\n\t"
"ds_write_b64 v245, v[236:237] offset:26624\n\t"
"v_mfma_f32_16x16x32_bf16 v[136:139], a[152:155], a[172:175], v[136:139]\n\t"
"ds_read_b64_tr_b16 v[236:237], v246 offset:52736\n\t"
"v_cvt_pk_bf16_f32 v224, v224, v225\n\t"
"v_mfma_f32_16x16x32_bf16 v[136:139], a[28:31], a[176:179], v[136:139]\n\t"
"ds_read_b64_tr_b16 v[238:239], v246 offset:55808\n\t"
"v_cvt_pk_bf16_f32 v225, v226, v227\n\t"
"v_mfma_f32_16x16x32_bf16 v[136:139], a[160:163], a[180:183], v[136:139]\n\t"
"buffer_store_dwordx4 v[204:207], v248, s[16:19], s29 offen offset:128 nt\n\t"
"ds_write_b64 v245, v[232:233] offset:27136\n\t"
"v_fma_f32 v98, -v16, s56, -v114\n\t"
"v_mfma_f32_16x16x32_bf16 v[140:143], a[152:155], a[176:179], v[140:143]\n\t"
"v_fma_f32 v204, -v6, s56, -v104\n\t"
"v_fma_f32 v205, -v7, s56, -v105\n\t"
"v_mfma_f32_16x16x32_bf16 v[140:143], a[28:31], a[180:183], v[140:143]\n\t"
"v_cvt_pk_bf16_f32 v228, v228, v229\n\t"
"v_cvt_pk_bf16_f32 v229, v230, v231\n\t"
"v_mfma_f32_16x16x32_bf16 v[140:143], a[160:163], a[184:187], v[140:143]\n\t"
"ds_write_b64 v245, v[224:225] offset:27648\n\t"
"v_mfma_f32_16x16x32_bf16 v[144:147], a[152:155], a[180:183], v[144:147]\n\t"
"s_waitcnt lgkmcnt(2)\n\t"
"v_fma_f32 v206, -v8, s56, -v106\n\t"
"v_fma_f32 v207, -v9, s56, -v107\n\t"
"v_mfma_f32_16x16x32_bf16 v[144:147], a[28:31], a[184:187], v[144:147]\n\t"
"v_accvgpr_write_b32 a1, v99\n\t"
"v_fma_f32 v99, -v17, s56, -v115\n\t"
"v_mfma_f32_16x16x32_bf16 v[144:147], a[160:163], a[188:191], v[144:147]\n\t"
"ds_write_b128 v249, a[84:87] offset:6144\n\t"
"v_mfma_f32_16x16x32_bf16 v[236:239], v[236:239], v[220:223], 0\n\t"
"buffer_store_dwordx4 a[8:11], v248, s[16:19], s29 offen offset:256 nt\n\t"
"v_accvgpr_write_b32 a2, v98\n\t"
"v_accvgpr_write_b32 a3, v99\n\t"
"v_mfma_f32_16x16x32_bf16 v[148:151], a[152:155], a[184:187], v[148:151]\n\t"
"v_mfma_f32_16x16x32_bf16 v[148:151], a[28:31], a[188:191], v[148:151]\n\t"
"v_mfma_f32_16x16x32_bf16 v[148:151], a[160:163], a[192:195], v[148:151]\n\t"
"ds_write_b128 v249, a[100:103] offset:18432\n\t"
"v_mfma_f32_16x16x32_bf16 v[152:155], a[152:155], a[188:191], v[152:155]\n\t"
"s_add_u32 s29, s23, 394368\n\t"
"v_mfma_f32_16x16x32_bf16 v[152:155], a[28:31], a[192:195], v[152:155]\n\t"
"v_cvt_pk_bf16_f32 v236, v236, v237\n\t"
"v_cvt_pk_bf16_f32 v237, v238, v239\n\t"
"v_mfma_f32_16x16x32_bf16 v[152:155], a[160:163], a[196:199], v[152:155]\n\t"
"ds_write_b64 v245, v[228:229] offset:28160\n\t"
"v_mfma_f32_16x16x32_bf16 v[156:159], a[152:155], a[192:195], v[156:159]\n\t"
"v_fma_f32 v98, -v18, s56, -v116\n\t"
"v_fma_f32 v99, -v19, s56, -v117\n\t"
"v_mfma_f32_16x16x32_bf16 v[156:159], a[28:31], a[196:199], v[156:159]\n\t"
"v_accvgpr_write_b32 a8, v98\n\t"
"v_fma_f32 v98, -v20, s56, -v118\n\t"
"v_mfma_f32_16x16x32_bf16 v[156:159], a[160:163], a[200:203], v[156:159]\n\t"
"ds_write_b64 v250, a[150:151] offset:6144\n\t"
"v_mfma_f32_16x16x32_bf16 v[160:163], a[152:155], a[196:199], v[160:163]\n\t"
"s_waitcnt vmcnt(31)\n\t"
"v_accvgpr_write_b32 a9, v99\n\t"
"v_fma_f32 v99, -v21, s56, -v119\n\t"
"v_mfma_f32_16x16x32_bf16 v[160:163], a[28:31], a[200:203], v[160:163]\n\t"
"v_accvgpr_write_b32 a10, v98\n\t"
"v_accvgpr_write_b32 a11, v99\n\t"
"v_mfma_f32_16x16x32_bf16 v[160:163], a[160:163], a[204:207], v[160:163]\n\t"
"ds_write_b64 v250, a[166:167] offset:18432\n\t"
"v_mfma_f32_16x16x32_bf16 v[164:167], a[36:39], a[168:171], v[164:167]\n\t"
"v_fma_f32 v98, -v22, s56, -v120\n\t"
"v_fma_f32 v99, -v23, s56, -v121\n\t"
"v_mfma_f32_16x16x32_bf16 v[164:167], a[112:115], a[172:175], v[164:167]\n\t"
"v_accvgpr_write_b32 a16, v98\n\t"
"v_mfma_f32_16x16x32_bf16 v[164:167], v[200:203], a[176:179], v[164:167]\n\t"
"ds_write_b64 v245, v[236:237] offset:28672\n\t"
"s_waitcnt lgkmcnt(0)\n\t"
"s_barrier\n\t"
"v_mfma_f32_16x16x32_bf16 v[168:171], a[36:39], a[172:175], v[168:171]\n\t"
"ds_read_b128 a[168:171], v244 offset:24576\n\t"
"v_mfma_f32_16x16x32_bf16 v[168:171], a[112:115], a[176:179], v[168:171]\n\t"
"buffer_load_dwordx4 v[236:239], v252, s[8:11], s20 offen offset:1024 sc1\n\t"
"v_mfma_f32_16x16x32_bf16 v[168:171], v[200:203], a[180:183], v[168:171]\n\t"
"buffer_store_dwordx4 a[40:43], v248, s[16:19], s29 offen offset:0 nt\n\t"
"ds_read_b128 a[172:175], v244 offset:25088\n\t"
"v_fma_f32 v98, -v24, s56, -v122\n\t"
"v_accvgpr_write_b32 a17, v99\n\t"
"v_fma_f32 v99, -v25, s56, -v123\n\t"
"v_mfma_f32_16x16x32_bf16 v[172:175], a[36:39], a[176:179], v[172:175]\n\t"
"ds_read_b128 a[176:179], v244 offset:25600\n\t"
"v_accvgpr_write_b32 a18, v98\n\t"
"v_mfma_f32_16x16x32_bf16 v[172:175], a[112:115], a[180:183], v[172:175]\n\t"
"v_accvgpr_write_b32 a19, v99\n\t"
"v_fma_f32 v98, -v26, s56, -v124\n\t"
"v_mfma_f32_16x16x32_bf16 v[172:175], v[200:203], a[184:187], v[172:175]\n\t"
"v_fma_f32 v99, -v27, s56, -v125\n\t"
"v_accvgpr_write_b32 a24, v98\n\t"
"v_mfma_f32_16x16x32_bf16 v[176:179], a[36:39], a[180:183], v[176:179]\n\t"
"ds_read_b128 a[180:183], v244 offset:26112\n\t"
"v_fma_f32 v98, -v28, s56, -v126\n\t"
"v_mfma_f32_16x16x32_bf16 v[176:179], a[112:115], a[184:187], v[176:179]\n\t"
"v_accvgpr_write_b32 a25, v99\n\t"
"v_fma_f32 v99, -v29, s56, -v127\n\t"
"v_mfma_f32_16x16x32_bf16 v[176:179], v[200:203], a[188:191], v[176:179]\n\t"
"v_accvgpr_write_b32 a26, v98\n\t"
"v_accvgpr_write_b32 a27, v99\n\t"
"v_mfma_f32_16x16x32_bf16 v[180:183], a[36:39], a[184:187], v[180:183]\n\t"
"ds_read_b128 a[184:187], v244 offset:26624\n\t"
"v_fma_f32 v98, -v30, s56, -v128\n\t"
"v_mfma_f32_16x16x32_bf16 v[180:183], a[112:115], a[188:191], v[180:183]\n\t"
"v_fma_f32 v99, -v31, s56, -v129\n\t"
"v_accvgpr_write_b32 a32, v98\n\t"
"v_mfma_f32_16x16x32_bf16 v[180:183], v[200:203], a[192:195], v[180:183]\n\t"
"v_fma_f32 v98, -v32, s56, -v130\n\t"
"v_accvgpr_write_b32 a33, v99\n\t"
"v_mfma_f32_16x16x32_bf16 v[184:187], a[36:39], a[188:191], v[184:187]\n\t"
"ds_read_b128 a[188:191], v244 offset:27136\n\t"
"v_fma_f32 v99, -v33, s56, -v131\n\t"
"v_mfma_f32_16x16x32_bf16 v[184:187], a[112:115], a[192:195], v[184:187]\n\t"
"v_accvgpr_write_b32 a34, v98\n\t"
"v_accvgpr_write_b32 a35, v99\n\t"
"v_mfma_f32_16x16x32_bf16 v[184:187], v[200:203], a[196:199], v[184:187]\n\t"
"buffer_store_dwordx4 a[72:75], v248, s[16:19], s29 offen offset:128 nt\n\t"
"v_fma_f32 v98, -v34, s56, -v132\n\t"
"v_fma_f32 v99, -v35, s56, -v133\n\t"
"v_accvgpr_write_b32 a40, v98\n\t"
"v_fma_f32 v98, -v36, s56, -v134\n\t"
"v_mfma_f32_16x16x32_bf16 v[188:191], a[36:39], a[192:195], v[188:191]\n\t"
"ds_read_b128 a[192:195], v244 offset:27648\n\t"
"v_accvgpr_write_b32 a41, v99\n\t"
"v_mfma_f32_16x16x32_bf16 v[188:191], a[112:115], a[196:199], v[188:191]\n\t"
"ds_read_b128 a[136:139], v252 offset:0\n\t"
"v_fma_f32 v99, -v37, s56, -v135\n\t"
"v_mfma_f32_16x16x32_bf16 v[188:191], v[200:203], a[200:203], v[188:191]\n\t"
"v_accvgpr_write_b32 a42, v98\n\t"
"v_accvgpr_write_b32 a43, v99\n\t"
"v_mfma_f32_16x16x32_bf16 v[192:195], a[36:39], a[196:199], v[192:195]\n\t"
"s_waitcnt vmcnt(29) lgkmcnt(5)\n\t"
"ds_read_b128 a[196:199], v244 offset:28160\n\t"
"v_fma_f32 v98, -v38, s56, -v136\n\t"
"v_mfma_f32_16x16x32_bf16 v[192:195], a[112:115], a[200:203], v[192:195]\n\t"
"ds_read_b128 a[200:203], v244 offset:28672\n\t"
"v_fma_f32 v99, -v39, s56, -v137\n\t"
"v_mfma_f32_16x16x32_bf16 v[192:195], v[200:203], a[204:207], v[192:195]\n\t"
"ds_read_b128 a[204:207], v244 offset:29184\n\t"
"v_accvgpr_write_b32 a48, v98\n\t"
"Lst20_%=:\n\t"
"v_mfma_f32_16x16x32_bf16 v[196:199], v[208:211], a[168:171], v[196:199]\n\t"
"s_waitcnt lgkmcnt(7)\n\t"
"v_xor_b32 v246, 0xc0, v246\n\t"
"v_fma_f32 v98, -v40, s56, -v138\n\t"
"v_mfma_f32_16x16x32_bf16 v[196:199], v[216:219], a[172:175], v[196:199]\n\t"
"v_accvgpr_write_b32 a49, v99\n\t"
"v_fma_f32 v99, -v41, s56, -v139\n\t"
"v_mfma_f32_16x16x32_bf16 v[196:199], a[4:7], a[176:179], v[196:199]\n\t"
"v_accvgpr_write_b32 a50, v98\n\t"
"v_accvgpr_write_b32 a51, v99\n\t"
"v_mfma_f32_16x16x32_bf16 v[204:207], v[208:211], a[172:175], v[204:207]\n\t"
"s_waitcnt lgkmcnt(6)\n\t"
"v_fma_f32 v98, -v42, s56, -v140\n\t"
"v_fma_f32 v99, -v43, s56, -v141\n\t"
"v_mfma_f32_16x16x32_bf16 v[204:207], v[216:219], a[176:179], v[204:207]\n\t"
"v_accvgpr_write_b32 a56, v98\n\t"
"v_fma_f32 v98, -v44, s56, -v142\n\t"
"v_mfma_f32_16x16x32_bf16 v[204:207], a[4:7], a[180:183], v[204:207]\n\t"
"buffer_store_dwordx4 a[104:107], v248, s[16:19], s29 offen offset:256 nt\n\t"
"v_accvgpr_write_b32 a57, v99\n\t"
"v_fma_f32 v99, -v45, s56, -v143\n\t"
"v_accvgpr_write_b32 a58, v98\n\t"
"v_accvgpr_write_b32 a59, v99\n\t"
"v_mfma_f32_16x16x32_bf16 v[212:215], v[208:211], a[176:179], v[212:215]\n\t"
"s_waitcnt lgkmcnt(5)\n\t"
"v_fma_f32 v98, -v46, s56, -v144\n\t"
"v_fma_f32 v99, -v47, s56, -v145\n\t"
"v_mfma_f32_16x16x32_bf16 v[212:215], v[216:219], a[180:183], v[212:215]\n\t"
"s_add_u32 s29, s23, 787584\n\t"
"ds_read_b128 v[200:203], v252 offset:1024\n\t"
"v_accvgpr_write_b32 a64, v98\n\t"
"v_mfma_f32_16x16x32_bf16 v[212:215], a[4:7], a[184:187], v[212:215]\n\t"
"v_fma_f32 v98, -v48, s56, -v146\n\t"
"v_accvgpr_write_b32 a65, v99\n\t"
"v_mfma_f32_16x16x32_bf16 a[0:3], v[208:211], a[180:183], a[0:3]\n\t"
"s_waitcnt lgkmcnt(5)\n\t"
"v_fma_f32 v99, -v49, s56, -v147\n\t"
"v_accvgpr_write_b32 a66, v98\n\t"
"v_mfma_f32_16x16x32_bf16 a[0:3], v[216:219], a[184:187], a[0:3]\n\t"
"s_waitcnt lgkmcnt(4)\n\t"
"v_accvgpr_write_b32 a67, v99\n\t"
"v_fma_f32 v98, -v50, s56, -v148\n\t"
"v_mfma_f32_16x16x32_bf16 a[0:3], a[4:7], a[188:191], a[0:3]\n\t"
"v_fma_f32 v99, -v51, s56, -v149\n\t"
"v_accvgpr_write_b32 a72, v98\n\t"
"v_mfma_f32_16x16x32_bf16 a[8:11], v[208:211], a[184:187], a[8:11]\n\t"
"s_waitcnt lgkmcnt(3)\n\t"
"v_fma_f32 v98, -v52, s56, -v150\n\t"
"v_accvgpr_write_b32 a73, v99\n\t"
"v_mfma_f32_16x16x32_bf16 a[8:11], v[216:219], a[188:191], a[8:11]\n\t"
"v_fma_f32 v99, -v53, s56, -v151\n\t"
"v_accvgpr_write_b32 a74, v98\n\t"
"v_mfma_f32_16x16x32_bf16 a[8:11], a[4:7], a[192:195], a[8:11]\n\t"
"ds_read_b64_tr_b16 v[232:233], v246 offset:512\n\t"
"v_accvgpr_write_b32 a75, v99\n\t"
"v_mfma_f32_16x16x32_bf16 a[16:19], v[208:211], a[188:191], a[16:19]\n\t"
"s_waitcnt lgkmcnt(3)\n\t"
"ds_read_b64_tr_b16 v[234:235], v246 offset:3584\n\t"
"v_fma_f32 v98, -v54, s56, -v152\n\t"
"v_mfma_f32_16x16x32_bf16 a[16:19], v[216:219], a[192:195], a[16:19]\n\t"
"ds_read_b128 a[44:47], v252 offset:12288\n\t"
"v_fma_f32 v99, -v55, s56, -v153\n\t"
"v_mfma_f32_16x16x32_bf16 a[16:19], a[4:7], a[196:199], a[16:19]\n\t"
"v_accvgpr_write_b32 a80, v98\n\t"
"v_fma_f32 v98, -v56, s56, -v154\n\t"
"v_mfma_f32_16x16x32_bf16 a[24:27], v[208:211], a[192:195], a[24:27]\n\t"
"s_waitcnt lgkmcnt(4)\n\t"
"ds_read_b128 a[76:79], v252 offset:13312\n\t"
"v_accvgpr_write_b32 a81, v99\n\t"
"v_mfma_f32_16x16x32_bf16 a[24:27], v[216:219], a[196:199], a[24:27]\n\t"
"v_fma_f32 v99, -v57, s56, -v155\n\t"
"v_accvgpr_write_b32 a82, v98\n\t"
"v_mfma_f32_16x16x32_bf16 a[24:27], a[4:7], a[200:203], a[24:27]\n\t"
"buffer_store_dwordx4 a[136:139], v248, s[16:19], s29 offen offset:0 nt\n\t"
"ds_read_b128 a[12:15], v252 offset:2048\n\t"
"ds_read_b128 a[108:111], v252 offset:14336\n\t"
"v_accvgpr_write_b32 a83, v99\n\t"
"v_mfma_f32_16x16x32_bf16 a[32:35], v[208:211], a[196:199], a[32:35]\n\t"
"s_waitcnt vmcnt(26)\n\t"
"ds_read_b64_tr_b16 v[224:225], v246 offset:6656\n\t"
"v_fma_f32 v98, -v58, s56, -v156\n\t"
"v_mfma_f32_16x16x32_bf16 a[32:35], v[216:219], a[200:203], a[32:35]\n\t"
"s_waitcnt lgkmcnt(7)\n\t"
"ds_read_b64_tr_b16 v[226:227], v246 offset:9728\n\t"
"v_fma_f32 v99, -v59, s56, -v157\n\t"
"v_mfma_f32_16x16x32_bf16 a[32:35], a[4:7], a[204:207], a[32:35]\n\t"
"v_accvgpr_write_b32 a88, v98\n\t"
"v_fma_f32 v98, -v60, s56, -v158\n\t"
"v_mfma_f32_16x16x32_bf16 a[40:43], a[208:211], a[168:171], a[40:43]\n\t"
"v_accvgpr_write_b32 a89, v99\n\t"
"v_fma_f32 v99, -v61, s56, -v159\n\t"
"v_mfma_f32_16x16x32_bf16 a[40:43], a[212:215], a[172:175], a[40:43]\n\t"
"v_accvgpr_write_b32 a90, v98\n\t"
"v_accvgpr_write_b32 a91, v99\n\t"
"v_mfma_f32_16x16x32_bf16 a[40:43], v[240:243], a[176:179], a[40:43]\n\t"
"v_fma_f32 v98, -v62, s56, -v160\n\t"
"v_fma_f32 v99, -v63, s56, -v161\n\t"
"v_mfma_f32_16x16x32_bf16 a[48:51], a[208:211], a[172:175], a[48:51]\n\t"
"s_waitcnt lgkmcnt(6)\n\t"
"v_accvgpr_write_b32 a96, v98\n\t"
"v_fma_f32 v98, -v64, s56, -v162\n\t"
"v_mfma_f32_16x16x32_bf16 a[48:51], a[212:215], a[176:179], a[48:51]\n\t"
"v_accvgpr_write_b32 a97, v99\n\t"
"v_fma_f32 v99, -v65, s56, -v163\n\t"
"v_mfma_f32_16x16x32_bf16 a[48:51], v[240:243], a[180:183], a[48:51]\n\t"
"v_accvgpr_write_b32 a98, v98\n\t"
"v_accvgpr_write_b32 a99, v99\n\t"
"v_mfma_f32_16x16x32_bf16 v[232:235], v[232:235], v[220:223], 0\n\t"
"v_fma_f32 v98, -v66, s56, -v164\n\t"
"v_fma_f32 v99, -v67, s56, -v165\n\t"
"v_mfma_f32_16x16x32_bf16 a[56:59], a[208:211], a[176:179], a[56:59]\n\t"
"ds_read_b64_tr_b16 v[228:229], v246 offset:12800\n\t"
"v_accvgpr_write_b32 a104, v98\n\t"
"v_mfma_f32_16x16x32_bf16 a[56:59], a[212:215], a[180:183], a[56:59]\n\t"
"ds_read_b64_tr_b16 v[230:231], v246 offset:15872\n\t"
"v_fma_f32 v98, -v68, s56, -v166\n\t"
"v_mfma_f32_16x16x32_bf16 a[56:59], v[240:243], a[184:187], a[56:59]\n\t"
"buffer_store_dwordx4 v[200:203], v248, s[16:19], s29 offen offset:128 nt\n\t"
"v_accvgpr_write_b32 a105, v99\n\t"
"v_fma_f32 v99, -v69, s56, -v167\n\t"
"v_accvgpr_write_b32 a106, v98\n\t"
"v_accvgpr_write_b32 a107, v99\n\t"
"v_mfma_f32_16x16x32_bf16 a[64:67], a[208:211], a[180:183], a[64:67]\n\t"
"s_waitcnt lgkmcnt(2)\n\t"
"v_fma_f32 v98, -v70, s56, -v168\n\t"
"v_fma_f32 v99, -v71, s56, -v169\n\t"
"v_mfma_f32_16x16x32_bf16 a[64:67], a[212:215], a[184:187], a[64:67]\n\t"
"v_cvt_pk_bf16_f32 v232, v232, v233\n\t"
"v_cvt_pk_bf16_f32 v233, v234, v235\n\t"
"v_mfma_f32_16x16x32_bf16 a[64:67], v[240:243], a[188:191], a[64:67]\n\t"
"v_accvgpr_write_b32 a112, v98\n\t"
"v_mfma_f32_16x16x32_bf16 v[224:227], v[224:227], v[220:223], 0\n\t"
"s_barrier\n\t"
"v_mfma_f32_16x16x32_bf16 a[72:75], a[208:211], a[184:187], a[72:75]\n\t"
"s_waitcnt lgkmcnt(0)\n\t"
"v_fma_f32 v98, -v72, s56, -v170\n\t"
"v_mfma_f32_16x16x32_bf16 a[72:75], a[212:215], a[188:191], a[72:75]\n\t"
"v_accvgpr_write_b32 a113, v99\n\t"
"v_fma_f32 v99, -v73, s56, -v171\n\t"
"v_mfma_f32_16x16x32_bf16 a[72:75], v[240:243], a[192:195], a[72:75]\n\t"
"Lpg15_%=:\n\t"
"s_cbranch_vccnz Lvr1_%=\n\t"
"v_accvgpr_write_b32 a114, v98\n\t"
"v_accvgpr_write_b32 a115, v99\n\t"
"v_mfma_f32_16x16x32_bf16 a[80:83], a[208:211], a[188:191], a[80:83]\n\t"
"v_fma_f32 v98, -v74, s56, -v172\n\t"
"v_fma_f32 v99, -v75, s56, -v173\n\t"
"v_mfma_f32_16x16x32_bf16 a[80:83], a[212:215], a[192:195], a[80:83]\n\t"
"v_cvt_pk_bf16_f32 v234, v224, v225\n\t"
"v_cvt_pk_bf16_f32 v235, v226, v227\n\t"
"v_mfma_f32_16x16x32_bf16 a[80:83], v[240:243], a[196:199], a[80:83]\n\t"
"ds_read_b64_tr_b16 v[224:225], v246 offset:20480\n\t"
"v_accvgpr_write_b32 a120, v98\n\t"
"v_mfma_f32_16x16x32_bf16 a[88:91], a[208:211], a[192:195], a[88:91]\n\t"
"ds_read_b64_tr_b16 v[226:227], v246 offset:23552\n\t"
"v_fma_f32 v98, -v76, s56, -v174\n\t"
"v_mfma_f32_16x16x32_bf16 a[88:91], a[212:215], a[196:199], a[88:91]\n\t"
"v_accvgpr_write_b32 a121, v99\n\t"
"v_fma_f32 v99, -v77, s56, -v175\n\t"
"v_mfma_f32_16x16x32_bf16 a[88:91], v[240:243], a[200:203], a[88:91]\n\t"
"buffer_store_dwordx4 a[12:15], v248, s[16:19], s29 offen offset:256 nt\n\t"
"v_accvgpr_write_b32 a122, v98\n\t"
"v_accvgpr_write_b32 a123, v99\n\t"
"v_fma_f32 v98, -v78, s56, -v176\n\t"
"v_fma_f32 v99, -v79, s56, -v177\n\t"
"v_mfma_f32_16x16x32_bf16 v[228:231], v[228:231], v[220:223], 0\n\t"
"s_add_u32 s29, s23, 1180800\n\t"
"v_accvgpr_write_b32 a128, v98\n\t"
"v_fma_f32 v98, -v80, s56, -v178\n\t"
"v_mfma_f32_16x16x32_bf16 a[96:99], a[208:211], a[196:199], a[96:99]\n\t"
"s_waitcnt vmcnt(23)\n\t"
"v_accvgpr_write_b32 a129, v99\n\t"
"v_fma_f32 v99, -v81, s56, -v179\n\t"
"v_mfma_f32_16x16x32_bf16 a[96:99], a[212:215], a[200:203], a[96:99]\n\t"
"v_accvgpr_write_b32 a130, v98\n\t"
"v_accvgpr_write_b32 a131, v99\n\t"
"v_mfma_f32_16x16x32_bf16 a[96:99], v[240:243], a[204:207], a[96:99]\n\t"
"v_fma_f32 v98, -v82, s56, -v180\n\t"
"v_fma_f32 v99, -v83, s56, -v181\n\t"
"v_mfma_f32_16x16x32_bf16 a[104:107], a[216:219], a[168:171], a[104:107]\n\t"
"ds_read_b128 a[168:171], v244 offset:33792\n\t"
"v_accvgpr_write_b32 a136, v98\n\t"
"v_mfma_f32_16x16x32_bf16 a[104:107], a[220:223], a[172:175], a[104:107]\n\t"
"v_cvt_pk_bf16_f32 v228, v228, v229\n\t"
"v_cvt_pk_bf16_f32 v229, v230, v231\n\t"
"v_mfma_f32_16x16x32_bf16 a[104:107], a[224:227], a[176:179], a[104:107]\n\t"
"buffer_load_dwordx4 a[208:211], v252, s[8:11], s20 offen offset:2048 sc1\n\t"
"s_mov_b32 s20, 36864\n\t"
"v_mfma_f32_16x16x32_bf16 a[112:115], a[216:219], a[172:175], a[112:115]\n\t"
"buffer_load_dwordx4 v[240:243], v252, s[8:11], s20 offen offset:0 sc1\n\t"
"v_mfma_f32_16x16x32_bf16 a[112:115], a[220:223], a[176:179], a[112:115]\n\t"
"buffer_load_dwordx4 a[212:215], v252, s[8:11], s20 offen offset:1024 sc1\n\t"
"v_mfma_f32_16x16x32_bf16 a[112:115], a[224:227], a[180:183], a[112:115]\n\t"
"s_waitcnt lgkmcnt(1)\n\t"
"ds_read_b128 a[172:175], v244 offset:34304\n\t"
"v_fma_f32 v98, -v84, s56, -v182\n\t"
"v_mfma_f32_16x16x32_bf16 v[224:227], v[224:227], v[220:223], 0\n\t"
"buffer_store_dwordx4 a[44:47], v248, s[16:19], s29 offen offset:0 nt\n\t"
"v_accvgpr_write_b32 a137, v99\n\t"
"v_fma_f32 v99, -v85, s56, -v183\n\t"
"v_accvgpr_write_b32 a138, v98\n\t"
"v_accvgpr_write_b32 a139, v99\n\t"
"v_mfma_f32_16x16x32_bf16 a[120:123], a[216:219], a[176:179], a[120:123]\n\t"
"ds_read_b128 a[176:179], v244 offset:34816\n\t"
"v_fma_f32 v98, -v86, s56, -v184\n\t"
"v_mfma_f32_16x16x32_bf16 a[120:123], a[220:223], a[180:183], a[120:123]\n\t"
"v_fma_f32 v99, -v87, s56, -v185\n\t"
"v_accvgpr_write_b32 a144, v98\n\t"
"v_mfma_f32_16x16x32_bf16 a[120:123], a[224:227], a[184:187], a[120:123]\n\t"
"v_fma_f32 v98, -v88, s56, -v186\n\t"
"v_accvgpr_write_b32 a145, v99\n\t"
"v_mfma_f32_16x16x32_bf16 a[128:131], a[216:219], a[180:183], a[128:131]\n\t"
"ds_read_b128 a[180:183], v244 offset:35328\n\t"
"v_fma_f32 v99, -v89, s56, -v187\n\t"
"v_mfma_f32_16x16x32_bf16 a[128:131], a[220:223], a[184:187], a[128:131]\n\t"
"v_accvgpr_write_b32 a146, v98\n\t"
"v_accvgpr_write_b32 a147, v99\n\t"
"v_mfma_f32_16x16x32_bf16 a[128:131], a[224:227], a[188:191], a[128:131]\n\t"
"v_fma_f32 v98, -v90, s56, -v188\n\t"
"v_fma_f32 v99, -v91, s56, -v189\n\t"
"v_mfma_f32_16x16x32_bf16 a[136:139], a[216:219], a[184:187], a[136:139]\n\t"
"ds_read_b128 a[184:187], v244 offset:35840\n\t"
"v_cvt_pk_bf16_f32 v230, v224, v225\n\t"
"v_mfma_f32_16x16x32_bf16 a[136:139], a[220:223], a[188:191], a[136:139]\n\t"
"v_cvt_pk_bf16_f32 v231, v226, v227\n\t"
"v_accvgpr_write_b32 a152, v98\n\t"
"v_mfma_f32_16x16x32_bf16 a[136:139], a[224:227], a[192:195], a[136:139]\n\t"
"ds_read_b64_tr_b16 v[224:225], v246 offset:26624\n\t"
"v_fma_f32 v98, -v92, s56, -v190\n\t"
"v_mfma_f32_16x16x32_bf16 a[144:147], a[216:219], a[188:191], a[144:147]\n\t"
"ds_read_b64_tr_b16 v[226:227], v246 offset:29696\n\t"
"v_accvgpr_write_b32 a153, v99\n\t"
"v_mfma_f32_16x16x32_bf16 a[144:147], a[220:223], a[192:195], a[144:147]\n\t"
"ds_read_b128 a[188:191], v244 offset:36352\n\t"
"v_fma_f32 v99, -v93, s56, -v191\n\t"
"v_mfma_f32_16x16x32_bf16 a[144:147], a[224:227], a[196:199], a[144:147]\n\t"
"v_accvgpr_write_b32 a154, v98\n\t"
"v_accvgpr_write_b32 a155, v99\n\t"
"v_fma_f32 v98, -v94, s56, -v192\n\t"
"v_fma_f32 v99, -v95, s56, -v193\n\t"
"v_mfma_f32_16x16x32_bf16 a[152:155], a[216:219], a[192:195], a[152:155]\n\t"
"ds_read_b128 a[192:195], v244 offset:36864\n\t"
"v_accvgpr_write_b32 a160, v98\n\t"
"v_mfma_f32_16x16x32_bf16 a[152:155], a[220:223], a[196:199], a[152:155]\n\t"
"v_fma_f32 v98, -v96, s56, -v194\n\t"
"v_accvgpr_write_b32 a161, v99\n\t"
"v_mfma_f32_16x16x32_bf16 a[152:155], a[224:227], a[200:203], a[152:155]\n\t"
"buffer_store_dwordx4 a[76:79], v248, s[16:19], s29 offen offset:128 nt\n\t"
"v_fma_f32 v99, -v97, s56, -v195\n\t"
"v_accvgpr_write_b32 a162, v98\n\t"
"v_accvgpr_write_b32 a163, v99\n\t"
"v_fma_f32 v200, v100, s64, -v2\n\t"
"v_fma_f32 v201, v101, s64, -v3\n\t"
"v_fma_f32 v202, v102, s64, -v4\n\t"
"v_fma_f32 v203, v103, s64, -v5\n\t"
"v_mfma_f32_16x16x32_bf16 a[160:163], a[216:219], a[196:199], a[160:163]\n\t"
"s_waitcnt lgkmcnt(2)\n\t"
"ds_read_b128 a[196:199], v244 offset:37376\n\t"
"v_fma_f32 v208, v104, s64, -v6\n\t"
"v_mfma_f32_16x16x32_bf16 a[160:163], a[220:223], a[200:203], a[160:163]\n\t"
"s_waitcnt vmcnt(23)\n\t"
"ds_read_b128 a[200:203], v244 offset:37888\n\t"
"v_fma_f32 v209, v105, s64, -v7\n\t"
"v_mfma_f32_16x16x32_bf16 a[160:163], a[224:227], a[204:207], a[160:163]\n\t"
"ds_read_b128 a[204:207], v244 offset:38400\n\t"
"v_fma_f32 v210, v106, s64, -v8\n\t"
"v_fma_f32 v211, v107, s64, -v9\n\t"
"v_mfma_f32_16x16x32_bf16 v[224:227], v[224:227], v[220:223], 0\n\t"
"buffer_load_dwordx4 a[216:219], v252, s[8:11], s20 offen offset:2048 sc1\n\t"
"s_mov_b32 s20, 49152\n\t"
"v_mfma_f32_16x16x32_bf16 v[200:203], a[228:231], a[168:171], v[200:203]\n\t"
"buffer_load_dwordx4 a[220:223], v252, s[8:11], s20 offen offset:0 sc1\n\t"
"v_mfma_f32_16x16x32_bf16 v[200:203], a[236:239], a[172:175], v[200:203]\n\t"
"v_fma_f32 v216, v108, s64, -v10\n\t"
"v_fma_f32 v217, v109, s64, -v11\n\t"
"v_mfma_f32_16x16x32_bf16 v[200:203], a[240:243], a[176:179], v[200:203]\n\t"
"v_fma_f32 v218, v110, s64, -v12\n\t"
"v_fma_f32 v219, v111, s64, -v13\n\t"
"v_mfma_f32_16x16x32_bf16 v[208:211], a[228:231], a[172:175], v[208:211]\n\t"
"v_fma_f32 v98, v112, s64, -v14\n\t"
"v_fma_f32 v99, v113, s64, -v15\n\t"
"v_mfma_f32_16x16x32_bf16 v[208:211], a[236:239], a[176:179], v[208:211]\n\t"
"v_accvgpr_write_b32 a4, v98\n\t"
"v_mfma_f32_16x16x32_bf16 v[208:211], a[240:243], a[180:183], v[208:211]\n\t"
"s_waitcnt lgkmcnt(0)\n\t"
"s_barrier\n\t"
"v_mfma_f32_16x16x32_bf16 v[216:219], a[228:231], a[176:179], v[216:219]\n\t"
"v_fma_f32 v98, v114, s64, -v16\n\t"
"v_mfma_f32_16x16x32_bf16 v[216:219], a[236:239], a[180:183], v[216:219]\n\t"
"buffer_load_dwordx4 a[224:227], v252, s[8:11], s20 offen offset:1024 sc1\n\t"
"v_mfma_f32_16x16x32_bf16 v[216:219], a[240:243], a[184:187], v[216:219]\n\t"
"buffer_store_dwordx4 a[108:111], v248, s[16:19], s29 offen offset:256 nt\n\t"
"v_accvgpr_write_b32 a5, v99\n\t"
"v_fma_f32 v99, v115, s64, -v17\n\t"
"v_accvgpr_write_b32 a6, v98\n\t"
"v_accvgpr_write_b32 a7, v99\n\t"
"v_fma_f32 v98, v116, s64, -v18\n\t"
"v_fma_f32 v99, v117, s64, -v19\n\t"
"v_cvt_pk_bf16_f32 v224, v224, v225\n\t"
"v_cvt_pk_bf16_f32 v225, v226, v227\n\t"
"v_mfma_f32_16x16x32_bf16 a[4:7], a[228:231], a[180:183], a[4:7]\n\t"
"v_accvgpr_write_b32 a12, v98\n\t"
"v_fma_f32 v98, v118, s64, -v20\n\t"
"v_mfma_f32_16x16x32_bf16 a[4:7], a[236:239], a[184:187], a[4:7]\n\t"
"v_accvgpr_write_b32 a13, v99\n\t"
"v_fma_f32 v99, v119, s64, -v21\n\t"
"v_mfma_f32_16x16x32_bf16 a[4:7], a[240:243], a[188:191], a[4:7]\n\t"
"ds_write2st64_b64 v245, v[232:233], v[234:235] offset0:48 offset1:49\n\t"
"v_accvgpr_write_b32 a14, v98\n\t"
"v_accvgpr_write_b32 a15, v99\n\t"
"v_fma_f32 v98, v120, s64, -v22\n\t"
"v_fma_f32 v99, v121, s64, -v23\n\t"
"v_mfma_f32_16x16x32_bf16 a[12:15], a[228:231], a[184:187], a[12:15]\n\t"
"ds_read_b64_tr_b16 v[232:233], v246 offset:32768\n\t"
"v_mfma_f32_16x16x32_bf16 a[12:15], a[236:239], a[188:191], a[12:15]\n\t"
"ds_read_b64_tr_b16 v[234:235], v246 offset:35840\n\t"
"v_accvgpr_write_b32 a20, v98\n\t"
"v_mfma_f32_16x16x32_bf16 a[12:15], a[240:243], a[192:195], a[12:15]\n\t"
"ds_write2st64_b64 v245, v[228:229], v[230:231] offset0:50 offset1:51\n\t"
"buffer_load_dwordx4 v[248:251], v252, s[8:11], s20 offen offset:2048 sc1\n\t"
"v_fma_f32 v98, v122, s64, -v24\n\t"
"v_accvgpr_write_b32 a21, v99\n\t"
"v_fma_f32 v99, v123, s64, -v25\n\t"
"v_accvgpr_write_b32 a22, v98\n\t"
"v_accvgpr_write_b32 a23, v99\n\t"
"v_fma_f32 v98, v124, s64, -v26\n\t"
"v_fma_f32 v99, v125, s64, -v27\n\t"
"v_mfma_f32_16x16x32_bf16 a[20:23], a[228:231], a[188:191], a[20:23]\n\t"
"ds_read_b64_tr_b16 v[228:229], v246 offset:40448\n\t"
"v_accvgpr_write_b32 a28, v98\n\t"
"v_mfma_f32_16x16x32_bf16 a[20:23], a[236:239], a[192:195], a[20:23]\n\t"
"ds_read_b64_tr_b16 v[230:231], v246 offset:43520\n\t"
"v_fma_f32 v98, v126, s64, -v28\n\t"
"v_mfma_f32_16x16x32_bf16 a[20:23], a[240:243], a[196:199], a[20:23]\n\t"
"s_waitcnt lgkmcnt(3)\n\t"
"ds_write_b64 v245, v[224:225] offset:26624\n\t"
"v_accvgpr_write_b32 a29, v99\n\t"
"v_fma_f32 v99, v127, s64, -v29\n\t"
"v_accvgpr_write_b32 a30, v98\n\t"
"v_accvgpr_write_b32 a31, v99\n\t"
"v_mfma_f32_16x16x32_bf16 v[232:235], v[232:235], v[220:223], 0\n\t"
"s_mov_b32 s20, 61440\n\t"
"ds_read_b64_tr_b16 v[224:225], v246 offset:46592\n\t"
"v_fma_f32 v98, v128, s64, -v30\n\t"
"v_fma_f32 v99, v129, s64, -v31\n\t"
"v_mfma_f32_16x16x32_bf16 a[28:31], a[228:231], a[192:195], a[28:31]\n\t"
"ds_read_b64_tr_b16 v[226:227], v246 offset:49664\n\t"
"v_accvgpr_write_b32 a36, v98\n\t"
"v_mfma_f32_16x16x32_bf16 a[28:31], a[236:239], a[196:199], a[28:31]\n\t"
"v_fma_f32 v98, v130, s64, -v32\n\t"
"v_accvgpr_write_b32 a37, v99\n\t"
"v_mfma_f32_16x16x32_bf16 a[28:31], a[240:243], a[200:203], a[28:31]\n\t"
"s_waitcnt lgkmcnt(3)\n\t"
"v_fma_f32 v99, v131, s64, -v33\n\t"
"v_accvgpr_write_b32 a38, v98\n\t"
"v_accvgpr_write_b32 a39, v99\n\t"
"v_mfma_f32_16x16x32_bf16 v[228:231], v[228:231], v[220:223], 0\n\t"
"v_fma_f32 v98, v132, s64, -v34\n\t"
"v_fma_f32 v99, v133, s64, -v35\n\t"
"v_mfma_f32_16x16x32_bf16 a[36:39], a[228:231], a[196:199], a[36:39]\n\t"
"v_accvgpr_write_b32 a44, v98\n\t"
"v_fma_f32 v98, v134, s64, -v36\n\t"
"v_mfma_f32_16x16x32_bf16 a[36:39], a[236:239], a[200:203], a[36:39]\n\t"
"s_waitcnt lgkmcnt(0)\n\t"
"v_accvgpr_write_b32 a45, v99\n\t"
"v_fma_f32 v99, v135, s64, -v37\n\t"
"v_mfma_f32_16x16x32_bf16 a[36:39], a[240:243], a[204:207], a[36:39]\n\t"
"v_accvgpr_write_b32 a46, v98\n\t"
"v_accvgpr_write_b32 a47, v99\n\t"
"v_fma_f32 v98, v136, s64, -v38\n\t"
"v_fma_f32 v99, v137, s64, -v39\n\t"
"v_mfma_f32_16x16x32_bf16 v[224:227], v[224:227], v[220:223], 0\n\t"
"s_waitcnt vmcnt(20)\n\t"
"buffer_load_dwordx4 a[228:231], v252, s[8:11], s20 offen offset:0 sc1\n\t"
"v_cvt_pk_bf16_f32 v232, v232, v233\n\t"
"v_cvt_pk_bf16_f32 v233, v234, v235\n\t"
"v_mfma_f32_16x16x32_bf16 a[44:47], a[244:247], a[168:171], a[44:47]\n\t"
"v_accvgpr_write_b32 a52, v98\n\t"
"v_fma_f32 v98, v138, s64, -v40\n\t"
"v_mfma_f32_16x16x32_bf16 a[44:47], a[248:251], a[172:175], a[44:47]\n\t"
"v_accvgpr_write_b32 a53, v99\n\t"
"v_fma_f32 v99, v139, s64, -v41\n\t"
"v_mfma_f32_16x16x32_bf16 a[44:47], a[252:255], a[176:179], a[44:47]\n\t"
"buffer_load_dwordx4 a[236:239], v252, s[8:11], s20 offen offset:1024 sc1\n\t"
"v_accvgpr_write_b32 a54, v98\n\t"
"v_accvgpr_write_b32 a55, v99\n\t"
"v_fma_f32 v98, v140, s64, -v42\n\t"
"v_fma_f32 v99, v141, s64, -v43\n\t"
"v_mfma_f32_16x16x32_bf16 a[52:55], a[244:247], a[172:175], a[52:55]\n\t"
"v_accvgpr_write_b32 a60, v98\n\t"
"v_fma_f32 v98, v142, s64, -v44\n\t"
"v_mfma_f32_16x16x32_bf16 a[52:55], a[248:251], a[176:179], a[52:55]\n\t"
"v_accvgpr_write_b32 a61, v99\n\t"
"v_fma_f32 v99, v143, s64, -v45\n\t"
"v_mfma_f32_16x16x32_bf16 a[52:55], a[252:255], a[180:183], a[52:55]\n\t"
"buffer_load_dwordx4 a[240:243], v252, s[8:11], s20 offen offset:2048 sc1\n\t"
"v_accvgpr_write_b32 a62, v98\n\t"
"v_accvgpr_write_b32 a63, v99\n\t"
"v_cvt_pk_bf16_f32 v234, v228, v229\n\t"
"v_cvt_pk_bf16_f32 v235, v230, v231\n\t"
"v_fma_f32 v98, v144, s64, -v46\n\t"
"v_fma_f32 v99, v145, s64, -v47\n\t"
"v_mfma_f32_16x16x32_bf16 a[60:63], a[244:247], a[176:179], a[60:63]\n\t"
"ds_read_b64_tr_b16 v[228:229], v246 offset:52736\n\t"
"v_accvgpr_write_b32 a68, v98\n\t"
"v_mfma_f32_16x16x32_bf16 a[60:63], a[248:251], a[180:183], a[60:63]\n\t"
"ds_read_b64_tr_b16 v[230:231], v246 offset:55808\n\t"
"v_fma_f32 v98, v146, s64, -v48\n\t"
"v_mfma_f32_16x16x32_bf16 a[60:63], a[252:255], a[184:187], a[60:63]\n\t"
"ds_write2st64_b64 v245, v[232:233], v[234:235] offset0:53 offset1:54\n\t"
"v_accvgpr_write_b32 a69, v99\n\t"
"v_fma_f32 v99, v147, s64, -v49\n\t"
"v_accvgpr_write_b32 a70, v98\n\t"
"v_accvgpr_write_b32 a71, v99\n\t"
"v_fma_f32 v98, v148, s64, -v50\n\t"
"v_fma_f32 v99, v149, s64, -v51\n\t"
"v_cvt_pk_bf16_f32 v224, v224, v225\n\t"
"v_cvt_pk_bf16_f32 v225, v226, v227\n\t"
"v_mfma_f32_16x16x32_bf16 a[68:71], a[244:247], a[180:183], a[68:71]\n\t"
"v_accvgpr_write_b32 a76, v98\n\t"
"v_fma_f32 v98, v150, s64, -v52\n\t"
"v_mfma_f32_16x16x32_bf16 a[68:71], a[248:251], a[184:187], a[68:71]\n\t"
"v_accvgpr_write_b32 a77, v99\n\t"
"v_fma_f32 v99, v151, s64, -v53\n\t"
"v_mfma_f32_16x16x32_bf16 a[68:71], a[252:255], a[188:191], a[68:71]\n\t"
"v_accvgpr_write_b32 a78, v98\n\t"
"v_accvgpr_write_b32 a79, v99\n\t"
"v_fma_f32 v98, v152, s64, -v54\n\t"
"v_fma_f32 v99, v153, s64, -v55\n\t"
"v_mfma_f32_16x16x32_bf16 a[76:79], a[244:247], a[184:187], a[76:79]\n\t"
"s_waitcnt lgkmcnt(1)\n\t"
"v_accvgpr_write_b32 a84, v98\n\t"
"v_fma_f32 v98, v154, s64, -v56\n\t"
"v_mfma_f32_16x16x32_bf16 a[76:79], a[248:251], a[188:191], a[76:79]\n\t"
"v_accvgpr_write_b32 a85, v99\n\t"
"v_fma_f32 v99, v155, s64, -v57\n\t"
"v_mfma_f32_16x16x32_bf16 a[76:79], a[252:255], a[192:195], a[76:79]\n\t"
"v_accvgpr_write_b32 a86, v98\n\t"
"v_accvgpr_write_b32 a87, v99\n\t"
"v_fma_f32 v98, v156, s64, -v58\n\t"
"v_fma_f32 v99, v157, s64, -v59\n\t"
"v_mfma_f32_16x16x32_bf16 v[228:231], v[228:231], v[220:223], 0\n\t"
"v_accvgpr_write_b32 a92, v98\n\t"
"v_fma_f32 v98, v158, s64, -v60\n\t"
"v_mfma_f32_16x16x32_bf16 a[84:87], a[244:247], a[188:191], a[84:87]\n\t"
"s_mov_b32 s20, 73728\n\t"
"v_accvgpr_write_b32 a93, v99\n\t"
"v_fma_f32 v99, v159, s64, -v61\n\t"
"v_mfma_f32_16x16x32_bf16 a[84:87], a[248:251], a[192:195], a[84:87]\n\t"
"v_accvgpr_write_b32 a94, v98\n\t"
"v_accvgpr_write_b32 a95, v99\n\t"
"v_mfma_f32_16x16x32_bf16 a[84:87], a[252:255], a[196:199], a[84:87]\n\t"
"v_fma_f32 v98, v160, s64, -v62\n\t"
"v_fma_f32 v99, v161, s64, -v63\n\t"
"v_mfma_f32_16x16x32_bf16 a[92:95], a[244:247], a[192:195], a[92:95]\n\t"
"v_accvgpr_write_b32 a100, v98\n\t"
"v_fma_f32 v98, v162, s64, -v64\n\t"
"v_mfma_f32_16x16x32_bf16 a[92:95], a[248:251], a[196:199], a[92:95]\n\t"
"v_accvgpr_write_b32 a101, v99\n\t"
"v_fma_f32 v99, v163, s64, -v65\n\t"
"v_mfma_f32_16x16x32_bf16 a[92:95], a[252:255], a[200:203], a[92:95]\n\t"
"v_accvgpr_write_b32 a102, v98\n\t"
"v_accvgpr_write_b32 a103, v99\n\t"
"v_fma_f32 v98, v164, s64, -v66\n\t"
"v_fma_f32 v99, v165, s64, -v67\n\t"
"v_cvt_pk_bf16_f32 v226, v228, v229\n\t"
"v_cvt_pk_bf16_f32 v227, v230, v231\n\t"
"v_mfma_f32_16x16x32_bf16 a[100:103], a[244:247], a[196:199], a[100:103]\n\t"
"v_accvgpr_write_b32 a108, v98\n\t"
"v_fma_f32 v98, v166, s64, -v68\n\t"
"v_mfma_f32_16x16x32_bf16 a[100:103], a[248:251], a[200:203], a[100:103]\n\t"
"v_accvgpr_write_b32 a109, v99\n\t"
"v_fma_f32 v99, v167, s64, -v69\n\t"
"v_mfma_f32_16x16x32_bf16 a[100:103], a[252:255], a[204:207], a[100:103]\n\t"
"ds_write2st64_b64 v245, v[224:225], v[226:227] offset0:55 offset1:56\n\t"
"v_accvgpr_write_b32 a110, v98\n\t"
"v_accvgpr_write_b32 a111, v99\n\t"
"v_fma_f32 v98, v168, s64, -v70\n\t"
"v_fma_f32 v99, v169, s64, -v71\n\t"
"s_waitcnt vmcnt(12)\n\t"
"v_mfma_f32_16x16x32_bf16 a[108:111], a[232:235], a[168:171], a[108:111]\n\t"
"buffer_load_dwordx4 a[244:247], v252, s[8:11], s20 offen offset:0 sc1\n\t"
"v_mfma_f32_16x16x32_bf16 a[108:111], v[236:239], a[172:175], a[108:111]\n\t"
"v_accvgpr_write_b32 a116, v98\n\t"
"v_mfma_f32_16x16x32_bf16 a[108:111], a[208:211], a[176:179], a[108:111]\n\t"
"v_accvgpr_write_b32 a117, v99\n\t"
"v_fma_f32 v98, v170, s64, -v72\n\t"
"v_accvgpr_write_b32 a118, v98\n\t"
"v_fma_f32 v99, v171, s64, -v73\n\t"
"v_accvgpr_write_b32 a119, v99\n\t"
"s_waitcnt lgkmcnt(0)\n\t"
"s_barrier\n\t"
"v_mfma_f32_16x16x32_bf16 a[116:119], a[232:235], a[172:175], a[116:119]\n\t"
"ds_read_b128 a[168:171], v244 offset:24576\n\t"
"v_mfma_f32_16x16x32_bf16 a[116:119], v[236:239], a[176:179], a[116:119]\n\t"
"buffer_load_dwordx4 a[248:251], v252, s[8:11], s20 offen offset:1024 sc1\n\t"
"v_mfma_f32_16x16x32_bf16 a[116:119], a[208:211], a[180:183], a[116:119]\n\t"
"buffer_load_dwordx4 a[252:255], v252, s[8:11], s20 offen offset:2048 sc1\n\t"
"v_fma_f32 v98, v172, s64, -v74\n\t"
"v_fma_f32 v99, v173, s64, -v75\n\t"
"v_accvgpr_write_b32 a124, v98\n\t"
"v_fma_f32 v98, v174, s64, -v76\n\t"
"v_accvgpr_write_b32 a125, v99\n\t"
"v_fma_f32 v99, v175, s64, -v77\n\t"
"v_accvgpr_write_b32 a126, v98\n\t"
"v_accvgpr_write_b32 a127, v99\n\t"
"v_fma_f32 v98, v176, s64, -v78\n\t"
"v_fma_f32 v99, v177, s64, -v79\n\t"
"v_mfma_f32_16x16x32_bf16 a[124:127], a[232:235], a[176:179], a[124:127]\n\t"
"ds_read_b128 a[172:175], v244 offset:25088\n\t"
"v_accvgpr_write_b32 a132, v98\n\t"
"v_mfma_f32_16x16x32_bf16 a[124:127], v[236:239], a[180:183], a[124:127]\n\t"
"ds_read_b128 a[176:179], v244 offset:25600\n\t"
"v_fma_f32 v98, v178, s64, -v80\n\t"
"v_mfma_f32_16x16x32_bf16 a[124:127], a[208:211], a[184:187], a[124:127]\n\t"
"v_accvgpr_write_b32 a133, v99\n\t"
"v_fma_f32 v99, v179, s64, -v81\n\t"
"v_accvgpr_write_b32 a134, v98\n\t"
"v_accvgpr_write_b32 a135, v99\n\t"
"v_fma_f32 v98, v180, s64, -v82\n\t"
"v_fma_f32 v99, v181, s64, -v83\n\t"
"v_mfma_f32_16x16x32_bf16 a[132:135], a[232:235], a[180:183], a[132:135]\n\t"
"ds_read_b128 a[180:183], v244 offset:26112\n\t"
"v_accvgpr_write_b32 a140, v98\n\t"
"v_mfma_f32_16x16x32_bf16 a[132:135], v[236:239], a[184:187], a[132:135]\n\t"
"v_fma_f32 v98, v182, s64, -v84\n\t"
"v_accvgpr_write_b32 a141, v99\n\t"
"v_mfma_f32_16x16x32_bf16 a[132:135], a[208:211], a[188:191], a[132:135]\n\t"
"v_fma_f32 v99, v183, s64, -v85\n\t"
"v_accvgpr_write_b32 a142, v98\n\t"
"v_accvgpr_write_b32 a143, v99\n\t"
"v_fma_f32 v98, v184, s64, -v86\n\t"
"v_fma_f32 v99, v185, s64, -v87\n\t"
"v_mfma_f32_16x16x32_bf16 a[140:143], a[232:235], a[184:187], a[140:143]\n\t"
"ds_read_b128 a[184:187], v244 offset:26624\n\t"
"v_accvgpr_write_b32 a148, v98\n\t"
"v_mfma_f32_16x16x32_bf16 a[140:143], v[236:239], a[188:191], a[140:143]\n\t"
"v_fma_f32 v98, v186, s64, -v88\n\t"
"v_accvgpr_write_b32 a149, v99\n\t"
"v_mfma_f32_16x16x32_bf16 a[140:143], a[208:211], a[192:195], a[140:143]\n\t"
"v_fma_f32 v99, v187, s64, -v89\n\t"
"v_accvgpr_write_b32 a150, v98\n\t"
"v_accvgpr_write_b32 a151, v99\n\t"
"v_fma_f32 v98, v188, s64, -v90\n\t"
"v_fma_f32 v99, v189, s64, -v91\n\t"
"v_mfma_f32_16x16x32_bf16 a[148:151], a[232:235], a[188:191], a[148:151]\n\t"
"s_mov_b32 s20, 86016\n\t"
"ds_read_b128 a[188:191], v244 offset:27136\n\t"
"v_accvgpr_write_b32 a156, v98\n\t"
"v_mfma_f32_16x16x32_bf16 a[148:151], v[236:239], a[192:195], a[148:151]\n\t"
"v_fma_f32 v98, v190, s64, -v92\n\t"
"v_accvgpr_write_b32 a157, v99\n\t"
"v_mfma_f32_16x16x32_bf16 a[148:151], a[208:211], a[196:199], a[148:151]\n\t"
"v_fma_f32 v99, v191, s64, -v93\n\t"
"v_accvgpr_write_b32 a158, v98\n\t"
"v_accvgpr_write_b32 a159, v99\n\t"
"v_fma_f32 v98, v192, s64, -v94\n\t"
"v_fma_f32 v99, v193, s64, -v95\n\t"
"v_mfma_f32_16x16x32_bf16 a[156:159], a[232:235], a[192:195], a[156:159]\n\t"
"ds_read_b128 a[192:195], v244 offset:27648\n\t"
"v_accvgpr_write_b32 a164, v98\n\t"
"v_mfma_f32_16x16x32_bf16 a[156:159], v[236:239], a[196:199], a[156:159]\n\t"
"v_fma_f32 v98, v194, s64, -v96\n\t"
"v_accvgpr_write_b32 a165, v99\n\t"
"v_mfma_f32_16x16x32_bf16 a[156:159], a[208:211], a[200:203], a[156:159]\n\t"
"s_add_u32 m0, s24, -128\n\t"
"s_add_u32 s28, s21, 0\n\t"
"v_fma_f32 v99, v195, s64, -v97\n\t"
"v_accvgpr_write_b32 a166, v98\n\t"
"v_accvgpr_write_b32 a167, v99\n\t"
"s_nop 1\n\t"
"v_mfma_f32_16x16x32_bf16 a[164:167], a[232:235], a[196:199], a[164:167]\n\t"
"buffer_load_dwordx4 a[232:235], v252, s[8:11], s20 offen offset:0 sc1\n\t"
"v_mfma_f32_16x16x32_bf16 a[164:167], v[236:239], a[200:203], a[164:167]\n\t"
"ds_read_b128 a[196:199], v244 offset:28160\n\t"
"v_fma_f32 v100, -v100, s66, v2\n\t"
"v_mfma_f32_16x16x32_bf16 a[164:167], a[208:211], a[204:207], a[164:167]\n\t"
"ds_read_b128 a[200:203], v244 offset:28672\n\t"
"v_fma_f32 v2, v4, s52, v102\n\t"
"v_fma_f32 v101, -v101, s66, v3\n\t"
"v_fma_f32 v3, v5, s52, v103\n\t"
"v_fma_f32 v102, -v102, s66, v4\n\t"
"v_fma_f32 v103, -v103, s66, v5\n\t"
"v_fma_f32 v4, v6, s52, v104\n\t"
"v_fma_f32 v5, v7, s52, v105\n\t"
"Lst21_%=:\n\t"
"s_waitcnt vmcnt(11) lgkmcnt(6)\n\t"
"v_mfma_f32_16x16x32_bf16 v[0:3], v[240:243], a[168:171], v[0:3]\n\t"
"buffer_load_dwordx4 v247, s[12:15], s28 offen offset:128 nt lds\n\t"
"v_mfma_f32_16x16x32_bf16 v[0:3], a[212:215], a[172:175], v[0:3]\n\t"
"s_add_u32 m0, s24, 6016\n\t"
"s_add_u32 s28, s21, 196608\n\t"
"ds_read_b128 a[204:207], v244 offset:29184\n\t"
"v_mfma_f32_16x16x32_bf16 v[0:3], a[216:219], a[176:179], v[0:3]\n\t"
"buffer_load_dwordx4 v247, s[12:15], s28 offen offset:128 nt lds\n\t"
"v_fma_f32 v104, -v104, s66, v6\n\t"
"v_fma_f32 v6, v8, s52, v106\n\t"
"v_fma_f32 v105, -v105, s66, v7\n\t"
"v_fma_f32 v7, v9, s52, v107\n\t"
"v_fma_f32 v106, -v106, s66, v8\n\t"
"v_fma_f32 v107, -v107, s66, v9\n\t"
"v_fma_f32 v8, v10, s52, v108\n\t"
"v_fma_f32 v9, v11, s52, v109\n\t"
"s_waitcnt lgkmcnt(6)\n\t"
"v_mfma_f32_16x16x32_bf16 v[4:7], v[240:243], a[172:175], v[4:7]\n\t"
"s_add_u32 m0, s24, 59776\n\t"
"s_add_u32 s28, s21, 1769472\n\t"
"v_fma_f32 v108, -v108, s66, v10\n\t"
"v_mfma_f32_16x16x32_bf16 v[4:7], a[212:215], a[176:179], v[4:7]\n\t"
"v_fma_f32 v10, v12, s52, v110\n\t"
"v_fma_f32 v109, -v109, s66, v11\n\t"
"v_mfma_f32_16x16x32_bf16 v[4:7], a[216:219], a[180:183], v[4:7]\n\t"
"buffer_load_dwordx4 v247, s[12:15], s28 offen offset:128 nt lds\n\t"
"v_fma_f32 v11, v13, s52, v111\n\t"
"v_fma_f32 v110, -v110, s66, v12\n\t"
"v_fma_f32 v111, -v111, s66, v13\n\t"
"v_fma_f32 v12, v14, s52, v112\n\t"
"v_fma_f32 v13, v15, s52, v113\n\t"
"s_add_u32 m0, s24, 65920\n\t"
"s_add_u32 s28, s21, 1966080\n\t"
"s_waitcnt lgkmcnt(5)\n\t"
"v_mfma_f32_16x16x32_bf16 v[8:11], v[240:243], a[176:179], v[8:11]\n\t"
"buffer_load_dwordx4 v247, s[12:15], s28 offen offset:128 nt lds\n\t"
"v_mfma_f32_16x16x32_bf16 v[8:11], a[212:215], a[180:183], v[8:11]\n\t"
"s_add_u32 m0, s24, 12160\n\t"
"s_add_u32 s28, s21, 393216\n\t"
"v_fma_f32 v112, -v112, s66, v14\n\t"
"v_mfma_f32_16x16x32_bf16 v[8:11], a[216:219], a[184:187], v[8:11]\n\t"
"v_fma_f32 v14, v16, s52, v114\n\t"
"v_fma_f32 v113, -v113, s66, v15\n\t"
"v_fma_f32 v15, v17, s52, v115\n\t"
"v_fma_f32 v114, -v114, s66, v16\n\t"
"v_fma_f32 v115, -v115, s66, v17\n\t"
"v_fma_f32 v16, v18, s52, v116\n\t"
"v_fma_f32 v17, v19, s52, v117\n\t"
"s_waitcnt lgkmcnt(4)\n\t"
"v_mfma_f32_16x16x32_bf16 v[12:15], v[240:243], a[180:183], v[12:15]\n\t"
"buffer_load_dwordx4 v247, s[12:15], s28 offen offset:128 nt lds\n\t"
"v_mfma_f32_16x16x32_bf16 v[12:15], a[212:215], a[184:187], v[12:15]\n\t"
"s_add_u32 m0, s24, 19840\n\t"
"s_add_u32 s28, s21, 589824\n\t"
"v_fma_f32 v116, -v116, s66, v18\n\t"
"v_mfma_f32_16x16x32_bf16 v[12:15], a[216:219], a[188:191], v[12:15]\n\t"
"buffer_load_dwordx4 v247, s[12:15], s28 offen offset:128 nt lds\n\t"
"v_fma_f32 v18, v20, s52, v118\n\t"
"v_fma_f32 v117, -v117, s66, v19\n\t"
"v_fma_f32 v19, v21, s52, v119\n\t"
"v_fma_f32 v118, -v118, s66, v20\n\t"
"v_fma_f32 v119, -v119, s66, v21\n\t"
"v_fma_f32 v20, v22, s52, v120\n\t"
"v_fma_f32 v21, v23, s52, v121\n\t"
"s_waitcnt lgkmcnt(3)\n\t"
"v_mfma_f32_16x16x32_bf16 v[16:19], v[240:243], a[184:187], v[16:19]\n\t"
"v_fma_f32 v120, -v120, s66, v22\n\t"
"v_fma_f32 v22, v24, s52, v122\n\t"
"v_mfma_f32_16x16x32_bf16 v[16:19], a[212:215], a[188:191], v[16:19]\n\t"
"v_fma_f32 v121, -v121, s66, v23\n\t"
"v_fma_f32 v23, v25, s52, v123\n\t"
"v_mfma_f32_16x16x32_bf16 v[16:19], a[216:219], a[192:195], v[16:19]\n\t"
"ds_read_b64_tr_b16 v[232:233], v246 offset:1024\n\t"
"v_fma_f32 v122, -v122, s66, v24\n\t"
"v_fma_f32 v123, -v123, s66, v25\n\t"
"v_fma_f32 v24, v26, s52, v124\n\t"
"v_fma_f32 v25, v27, s52, v125\n\t"
"s_waitcnt lgkmcnt(3)\n\t"
"v_mfma_f32_16x16x32_bf16 v[20:23], v[240:243], a[188:191], v[20:23]\n\t"
"Lpg16_%=:\n\t"
"s_cbranch_vccnz Lvr1_%=\n\t"
"ds_read_b64_tr_b16 v[234:235], v246 offset:4096\n\t"
"v_fma_f32 v124, -v124, s66, v26\n\t"
"v_mfma_f32_16x16x32_bf16 v[20:23], a[212:215], a[192:195], v[20:23]\n\t"
"v_fma_f32 v26, v28, s52, v126\n\t"
"v_fma_f32 v125, -v125, s66, v27\n\t"
"v_mfma_f32_16x16x32_bf16 v[20:23], a[216:219], a[196:199], v[20:23]\n\t"
"v_fma_f32 v27, v29, s52, v127\n\t"
"v_fma_f32 v126, -v126, s66, v28\n\t"
"v_fma_f32 v127, -v127, s66, v29\n\t"
"v_fma_f32 v28, v30, s52, v128\n\t"
"v_fma_f32 v29, v31, s52, v129\n\t"
"s_waitcnt lgkmcnt(3)\n\t"
"v_mfma_f32_16x16x32_bf16 v[24:27], v[240:243], a[192:195], v[24:27]\n\t"
"v_fma_f32 v128, -v128, s66, v30\n\t"
"v_fma_f32 v30, v32, s52, v130\n\t"
"v_mfma_f32_16x16x32_bf16 v[24:27], a[212:215], a[196:199], v[24:27]\n\t"
"v_fma_f32 v129, -v129, s66, v31\n\t"
"v_fma_f32 v31, v33, s52, v131\n\t"
"v_mfma_f32_16x16x32_bf16 v[24:27], a[216:219], a[200:203], v[24:27]\n\t"
"v_fma_f32 v130, -v130, s66, v32\n\t"
"v_fma_f32 v131, -v131, s66, v33\n\t"
"v_fma_f32 v32, v34, s52, v132\n\t"
"v_fma_f32 v33, v35, s52, v133\n\t"
"s_waitcnt lgkmcnt(2)\n\t"
"v_mfma_f32_16x16x32_bf16 v[28:31], v[240:243], a[196:199], v[28:31]\n\t"
"ds_read_b64_tr_b16 v[228:229], v246 offset:7168\n\t"
"v_fma_f32 v132, -v132, s66, v34\n\t"
"v_mfma_f32_16x16x32_bf16 v[28:31], a[212:215], a[200:203], v[28:31]\n\t"
"ds_read_b64_tr_b16 v[230:231], v246 offset:10240\n\t"
"v_fma_f32 v34, v36, s52, v134\n\t"
"v_mfma_f32_16x16x32_bf16 v[28:31], a[216:219], a[204:207], v[28:31]\n\t"
"v_fma_f32 v133, -v133, s66, v35\n\t"
"v_fma_f32 v35, v37, s52, v135\n\t"
"v_fma_f32 v134, -v134, s66, v36\n\t"
"v_fma_f32 v135, -v135, s66, v37\n\t"
"v_fma_f32 v36, v38, s52, v136\n\t"
"v_fma_f32 v37, v39, s52, v137\n\t"
"s_waitcnt vmcnt(13)\n\t"
"v_mfma_f32_16x16x32_bf16 v[32:35], a[220:223], a[168:171], v[32:35]\n\t"
"v_fma_f32 v136, -v136, s66, v38\n\t"
"v_fma_f32 v38, v40, s52, v138\n\t"
"v_mfma_f32_16x16x32_bf16 v[32:35], a[224:227], a[172:175], v[32:35]\n\t"
"v_fma_f32 v137, -v137, s66, v39\n\t"
"v_fma_f32 v39, v41, s52, v139\n\t"
"v_mfma_f32_16x16x32_bf16 v[32:35], v[248:251], a[176:179], v[32:35]\n\t"
"v_fma_f32 v138, -v138, s66, v40\n\t"
"v_fma_f32 v139, -v139, s66, v41\n\t"
"v_mfma_f32_16x16x32_bf16 v[36:39], a[220:223], a[172:175], v[36:39]\n\t"
"s_waitcnt lgkmcnt(2)\n\t"
"v_fma_f32 v40, v42, s52, v140\n\t"
"v_fma_f32 v41, v43, s52, v141\n\t"
"v_mfma_f32_16x16x32_bf16 v[36:39], a[224:227], a[176:179], v[36:39]\n\t"
"v_fma_f32 v140, -v140, s66, v42\n\t"
"v_fma_f32 v42, v44, s52, v142\n\t"
"v_mfma_f32_16x16x32_bf16 v[36:39], v[248:251], a[180:183], v[36:39]\n\t"
"v_fma_f32 v141, -v141, s66, v43\n\t"
"v_fma_f32 v43, v45, s52, v143\n\t"
"v_fma_f32 v142, -v142, s66, v44\n\t"
"v_fma_f32 v143, -v143, s66, v45\n\t"
"v_mfma_f32_16x16x32_bf16 v[232:235], v[232:235], v[220:223], 0\n\t"
"v_fma_f32 v44, v46, s52, v144\n\t"
"v_fma_f32 v45, v47, s52, v145\n\t"
"v_mfma_f32_16x16x32_bf16 v[40:43], a[220:223], a[176:179], v[40:43]\n\t"
"ds_read_b64_tr_b16 v[224:225], v246 offset:13312\n\t"
"v_fma_f32 v144, -v144, s66, v46\n\t"
"v_mfma_f32_16x16x32_bf16 v[40:43], a[224:227], a[180:183], v[40:43]\n\t"
"ds_read_b64_tr_b16 v[226:227], v246 offset:16384\n\t"
"v_fma_f32 v46, v48, s52, v146\n\t"
"v_mfma_f32_16x16x32_bf16 v[40:43], v[248:251], a[184:187], v[40:43]\n\t"
"buffer_load_dwordx4 v[240:243], v252, s[8:11], s20 offen offset:1024 sc1\n\t"
"v_fma_f32 v145, -v145, s66, v47\n\t"
"v_fma_f32 v47, v49, s52, v147\n\t"
"v_fma_f32 v146, -v146, s66, v48\n\t"
"v_fma_f32 v147, -v147, s66, v49\n\t"
"v_mfma_f32_16x16x32_bf16 v[44:47], a[220:223], a[180:183], v[44:47]\n\t"
"s_waitcnt lgkmcnt(2)\n\t"
"v_fma_f32 v48, v50, s52, v148\n\t"
"v_fma_f32 v49, v51, s52, v149\n\t"
"v_mfma_f32_16x16x32_bf16 v[44:47], a[224:227], a[184:187], v[44:47]\n\t"
"v_fma_f32 v148, -v148, s66, v50\n\t"
"v_fma_f32 v50, v52, s52, v150\n\t"
"v_mfma_f32_16x16x32_bf16 v[44:47], v[248:251], a[188:191], v[44:47]\n\t"
"v_fma_f32 v149, -v149, s66, v51\n\t"
"v_fma_f32 v51, v53, s52, v151\n\t"
"v_fma_f32 v150, -v150, s66, v52\n\t"
"v_fma_f32 v151, -v151, s66, v53\n\t"
"v_mfma_f32_16x16x32_bf16 v[228:231], v[228:231], v[220:223], 0\n\t"
"v_fma_f32 v52, v54, s52, v152\n\t"
"v_fma_f32 v53, v55, s52, v153\n\t"
"v_mfma_f32_16x16x32_bf16 v[48:51], a[220:223], a[184:187], v[48:51]\n\t"
"v_fma_f32 v152, -v152, s66, v54\n\t"
"v_fma_f32 v54, v56, s52, v154\n\t"
"v_mfma_f32_16x16x32_bf16 v[48:51], a[224:227], a[188:191], v[48:51]\n\t"
"v_fma_f32 v153, -v153, s66, v55\n\t"
"v_fma_f32 v55, v57, s52, v155\n\t"
"v_mfma_f32_16x16x32_bf16 v[48:51], v[248:251], a[192:195], v[48:51]\n\t"
"v_fma_f32 v154, -v154, s66, v56\n\t"
"v_fma_f32 v155, -v155, s66, v57\n\t"
"v_cvt_pk_bf16_f32 v232, v232, v233\n\t"
"v_cvt_pk_bf16_f32 v233, v234, v235\n\t"
"v_fma_f32 v56, v58, s52, v156\n\t"
"v_fma_f32 v57, v59, s52, v157\n\t"
"v_mfma_f32_16x16x32_bf16 v[52:55], a[220:223], a[188:191], v[52:55]\n\t"
"v_fma_f32 v156, -v156, s66, v58\n\t"
"v_fma_f32 v58, v60, s52, v158\n\t"
"v_mfma_f32_16x16x32_bf16 v[52:55], a[224:227], a[192:195], v[52:55]\n\t"
"v_fma_f32 v157, -v157, s66, v59\n\t"
"v_fma_f32 v59, v61, s52, v159\n\t"
"v_mfma_f32_16x16x32_bf16 v[52:55], v[248:251], a[196:199], v[52:55]\n\t"
"v_fma_f32 v158, -v158, s66, v60\n\t"
"v_fma_f32 v159, -v159, s66, v61\n\t"
"v_mfma_f32_16x16x32_bf16 v[56:59], a[220:223], a[192:195], v[56:59]\n\t"
"s_waitcnt lgkmcnt(0)\n\t"
"v_fma_f32 v60, v62, s52, v160\n\t"
"v_fma_f32 v61, v63, s52, v161\n\t"
"v_mfma_f32_16x16x32_bf16 v[56:59], a[224:227], a[196:199], v[56:59]\n\t"
"v_fma_f32 v160, -v160, s66, v62\n\t"
"v_fma_f32 v62, v64, s52, v162\n\t"
"v_mfma_f32_16x16x32_bf16 v[56:59], v[248:251], a[200:203], v[56:59]\n\t"
"v_fma_f32 v161, -v161, s66, v63\n\t"
"v_fma_f32 v63, v65, s52, v163\n\t"
"v_fma_f32 v162, -v162, s66, v64\n\t"
"v_fma_f32 v163, -v163, s66, v65\n\t"
"v_cvt_pk_bf16_f32 v234, v228, v229\n\t"
"v_cvt_pk_bf16_f32 v235, v230, v231\n\t"
"v_mfma_f32_16x16x32_bf16 v[224:227], v[224:227], v[220:223], 0\n\t"
"ds_read_b64_tr_b16 v[228:229], v246 offset:20992\n\t"
"v_fma_f32 v64, v66, s52, v164\n\t"
"v_fma_f32 v65, v67, s52, v165\n\t"
"v_mfma_f32_16x16x32_bf16 v[60:63], a[220:223], a[196:199], v[60:63]\n\t"
"ds_read_b64_tr_b16 v[230:231], v246 offset:24064\n\t"
"v_fma_f32 v164, -v164, s66, v66\n\t"
"v_mfma_f32_16x16x32_bf16 v[60:63], a[224:227], a[200:203], v[60:63]\n\t"
"v_fma_f32 v66, v68, s52, v166\n\t"
"v_fma_f32 v165, -v165, s66, v67\n\t"
"v_mfma_f32_16x16x32_bf16 v[60:63], v[248:251], a[204:207], v[60:63]\n\t"
"v_fma_f32 v67, v69, s52, v167\n\t"
"v_fma_f32 v166, -v166, s66, v68\n\t"
"v_fma_f32 v167, -v167, s66, v69\n\t"
"v_fma_f32 v68, v70, s52, v168\n\t"
"v_fma_f32 v69, v71, s52, v169\n\t"
"s_waitcnt vmcnt(11)\n\t"
"v_mfma_f32_16x16x32_bf16 v[64:67], a[228:231], a[168:171], v[64:67]\n\t"
"ds_read_b128 a[168:171], v244 offset:33792\n\t"
"v_fma_f32 v168, -v168, s66, v70\n\t"
"v_mfma_f32_16x16x32_bf16 v[64:67], a[236:239], a[172:175], v[64:67]\n\t"
"v_fma_f32 v70, v72, s52, v170\n\t"
"v_fma_f32 v169, -v169, s66, v71\n\t"
"v_mfma_f32_16x16x32_bf16 v[64:67], a[240:243], a[176:179], v[64:67]\n\t"
"v_fma_f32 v71, v73, s52, v171\n\t"
"v_fma_f32 v170, -v170, s66, v72\n\t"
"v_fma_f32 v171, -v171, s66, v73\n\t"
"v_mfma_f32_16x16x32_bf16 v[68:71], a[228:231], a[172:175], v[68:71]\n\t"
"s_waitcnt lgkmcnt(1)\n\t"
"ds_read_b128 a[172:175], v244 offset:34304\n\t"
"v_fma_f32 v72, v74, s52, v172\n\t"
"v_mfma_f32_16x16x32_bf16 v[68:71], a[236:239], a[176:179], v[68:71]\n\t"
"v_fma_f32 v73, v75, s52, v173\n\t"
"v_fma_f32 v172, -v172, s66, v74\n\t"
"v_mfma_f32_16x16x32_bf16 v[68:71], a[240:243], a[180:183], v[68:71]\n\t"
"v_fma_f32 v74, v76, s52, v174\n\t"
"v_fma_f32 v173, -v173, s66, v75\n\t"
"v_fma_f32 v75, v77, s52, v175\n\t"
"v_fma_f32 v174, -v174, s66, v76\n\t"
"v_fma_f32 v175, -v175, s66, v77\n\t"
"v_cvt_pk_bf16_f32 v224, v224, v225\n\t"
"v_cvt_pk_bf16_f32 v225, v226, v227\n\t"
"v_mfma_f32_16x16x32_bf16 v[228:231], v[228:231], v[220:223], 0\n\t"
"buffer_load_dwordx4 a[208:211], v252, s[8:11], s20 offen offset:2048 sc1\n\t"
"v_fma_f32 v76, v78, s52, v176\n\t"
"v_fma_f32 v77, v79, s52, v177\n\t"
"s_mov_b32 s20, 98304\n\t"
"v_mfma_f32_16x16x32_bf16 v[72:75], a[228:231], a[176:179], v[72:75]\n\t"
"buffer_load_dwordx4 v[236:239], v252, s[8:11], s20 offen offset:0 sc1\n\t"
"v_mfma_f32_16x16x32_bf16 v[72:75], a[236:239], a[180:183], v[72:75]\n\t"
"ds_read_b128 a[176:179], v244 offset:34816\n\t"
"v_fma_f32 v176, -v176, s66, v78\n\t"
"v_mfma_f32_16x16x32_bf16 v[72:75], a[240:243], a[184:187], v[72:75]\n\t"
"v_fma_f32 v78, v80, s52, v178\n\t"
"v_fma_f32 v177, -v177, s66, v79\n\t"
"v_fma_f32 v79, v81, s52, v179\n\t"
"v_fma_f32 v178, -v178, s66, v80\n\t"
"v_fma_f32 v179, -v179, s66, v81\n\t"
"v_fma_f32 v80, v82, s52, v180\n\t"
"v_fma_f32 v81, v83, s52, v181\n\t"
"v_mfma_f32_16x16x32_bf16 v[76:79], a[228:231], a[180:183], v[76:79]\n\t"
"ds_read_b128 a[180:183], v244 offset:35328\n\t"
"v_fma_f32 v180, -v180, s66, v82\n\t"
"v_mfma_f32_16x16x32_bf16 v[76:79], a[236:239], a[184:187], v[76:79]\n\t"
"v_fma_f32 v82, v84, s52, v182\n\t"
"v_fma_f32 v181, -v181, s66, v83\n\t"
"v_mfma_f32_16x16x32_bf16 v[76:79], a[240:243], a[188:191], v[76:79]\n\t"
"buffer_load_dwordx4 v[248:251], v252, s[8:11], s20 offen offset:1024 sc1\n\t"
"v_fma_f32 v83, v85, s52, v183\n\t"
"v_fma_f32 v182, -v182, s66, v84\n\t"
"v_fma_f32 v183, -v183, s66, v85\n\t"
"v_fma_f32 v84, v86, s52, v184\n\t"
"v_fma_f32 v85, v87, s52, v185\n\t"
"v_mfma_f32_16x16x32_bf16 v[80:83], a[228:231], a[184:187], v[80:83]\n\t"
"ds_read_b128 a[184:187], v244 offset:35840\n\t"
"v_fma_f32 v184, -v184, s66, v86\n\t"
"v_mfma_f32_16x16x32_bf16 v[80:83], a[236:239], a[188:191], v[80:83]\n\t"
"v_fma_f32 v86, v88, s52, v186\n\t"
"v_fma_f32 v185, -v185, s66, v87\n\t"
"v_mfma_f32_16x16x32_bf16 v[80:83], a[240:243], a[192:195], v[80:83]\n\t"
"buffer_load_dwordx4 a[212:215], v252, s[8:11], s20 offen offset:2048 sc1\n\t"
"v_fma_f32 v87, v89, s52, v187\n\t"
"v_fma_f32 v186, -v186, s66, v88\n\t"
"v_fma_f32 v187, -v187, s66, v89\n\t"
"v_cvt_pk_bf16_f32 v226, v228, v229\n\t"
"v_cvt_pk_bf16_f32 v227, v230, v231\n\t"
"v_fma_f32 v88, v90, s52, v188\n\t"
"v_fma_f32 v89, v91, s52, v189\n\t"
"s_mov_b32 s20, 331776\n\t"
"v_mfma_f32_16x16x32_bf16 v[84:87], a[228:231], a[188:191], v[84:87]\n\t"
"ds_read_b64_tr_b16 v[228:229], v246 offset:27136\n\t"
"v_fma_f32 v188, -v188, s66, v90\n\t"
"v_mfma_f32_16x16x32_bf16 v[84:87], a[236:239], a[192:195], v[84:87]\n\t"
"ds_read_b64_tr_b16 v[230:231], v246 offset:30208\n\t"
"v_fma_f32 v90, v92, s52, v190\n\t"
"v_mfma_f32_16x16x32_bf16 v[84:87], a[240:243], a[196:199], v[84:87]\n\t"
"buffer_load_dwordx4 a[216:219], v252, s[8:11], s20 offen offset:0 sc1\n\t"
"v_fma_f32 v189, -v189, s66, v91\n\t"
"v_fma_f32 v91, v93, s52, v191\n\t"
"v_fma_f32 v190, -v190, s66, v92\n\t"
"v_fma_f32 v191, -v191, s66, v93\n\t"
"v_fma_f32 v92, v94, s52, v192\n\t"
"v_fma_f32 v93, v95, s52, v193\n\t"
"v_mfma_f32_16x16x32_bf16 v[88:91], a[228:231], a[192:195], v[88:91]\n\t"
"ds_read_b128 a[188:191], v244 offset:36352\n\t"
"v_fma_f32 v192, -v192, s66, v94\n\t"
"v_mfma_f32_16x16x32_bf16 v[88:91], a[236:239], a[196:199], v[88:91]\n\t"
"ds_read_b128 a[192:195], v244 offset:36864\n\t"
"v_fma_f32 v94, v96, s52, v194\n\t"
"v_mfma_f32_16x16x32_bf16 v[88:91], a[240:243], a[200:203], v[88:91]\n\t"
"buffer_load_dwordx4 a[220:223], v252, s[8:11], s20 offen offset:1024 sc1\n\t"
"v_fma_f32 v193, -v193, s66, v95\n\t"
"v_fma_f32 v95, v97, s52, v195\n\t"
"v_fma_f32 v194, -v194, s66, v96\n\t"
"v_fma_f32 v195, -v195, s66, v97\n\t"
"v_mfma_f32_16x16x32_bf16 v[92:95], a[228:231], a[196:199], v[92:95]\n\t"
"s_waitcnt lgkmcnt(2)\n\t"
"ds_read_b128 a[196:199], v244 offset:37376\n\t"
"v_mfma_f32_16x16x32_bf16 v[92:95], a[236:239], a[200:203], v[92:95]\n\t"
"buffer_load_dwordx4 v[96:99], v252, s[8:11], s20 offen offset:2048 sc1\n\t"
"v_mfma_f32_16x16x32_bf16 v[92:95], a[240:243], a[204:207], v[92:95]\n\t"
"s_waitcnt vmcnt(15)\n\t"
"ds_read_b128 a[200:203], v244 offset:37888\n\t"
"s_mov_b32 s20, 344064\n\t"
"v_mfma_f32_16x16x32_bf16 v[228:231], v[228:231], v[220:223], 0\n\t"
"ds_read_b128 a[204:207], v244 offset:38400\n\t"
"v_mfma_f32_16x16x32_bf16 v[100:103], a[244:247], a[168:171], v[100:103]\n\t"
"buffer_load_dwordx4 a[224:227], v252, s[8:11], s20 offen offset:0 sc1\n\t"
"v_mfma_f32_16x16x32_bf16 v[100:103], a[248:251], a[172:175], v[100:103]\n\t"
"buffer_load_dwordx4 a[228:231], v252, s[8:11], s20 offen offset:1024 sc1\n\t"
"v_mfma_f32_16x16x32_bf16 v[100:103], a[252:255], a[176:179], v[100:103]\n\t"
"v_mfma_f32_16x16x32_bf16 v[104:107], a[244:247], a[172:175], v[104:107]\n\t"
"v_mfma_f32_16x16x32_bf16 v[104:107], a[248:251], a[176:179], v[104:107]\n\t"
"v_cvt_pk_bf16_f32 v228, v228, v229\n\t"
"v_mfma_f32_16x16x32_bf16 v[104:107], a[252:255], a[180:183], v[104:107]\n\t"
"s_waitcnt lgkmcnt(0)\n\t"
"s_barrier\n\t"
"v_mfma_f32_16x16x32_bf16 v[108:111], a[244:247], a[176:179], v[108:111]\n\t"
"v_cvt_pk_bf16_f32 v229, v230, v231\n\t"
"v_mfma_f32_16x16x32_bf16 v[108:111], a[248:251], a[180:183], v[108:111]\n\t"
"buffer_load_dwordx4 a[236:239], v252, s[8:11], s20 offen offset:2048 sc1\n\t"
"v_mfma_f32_16x16x32_bf16 v[108:111], a[252:255], a[184:187], v[108:111]\n\t"
"s_mov_b32 s20, 356352\n\t"
"ds_write2st64_b64 v245, v[232:233], v[234:235] offset0:48 offset1:49\n\t"
"v_mfma_f32_16x16x32_bf16 v[112:115], a[244:247], a[180:183], v[112:115]\n\t"
"buffer_load_dwordx4 a[240:243], v252, s[8:11], s20 offen offset:0 sc1\n\t"
"v_mfma_f32_16x16x32_bf16 v[112:115], a[248:251], a[184:187], v[112:115]\n\t"
"ds_read_b64_tr_b16 v[232:233], v246 offset:33280\n\t"
"v_mfma_f32_16x16x32_bf16 v[112:115], a[252:255], a[188:191], v[112:115]\n\t"
"ds_write2st64_b64 v245, v[224:225], v[226:227] offset0:50 offset1:51\n\t"
"v_mfma_f32_16x16x32_bf16 v[116:119], a[244:247], a[184:187], v[116:119]\n\t"
"v_mfma_f32_16x16x32_bf16 v[116:119], a[248:251], a[188:191], v[116:119]\n\t"
"ds_read_b64_tr_b16 v[234:235], v246 offset:36352\n\t"
"v_mfma_f32_16x16x32_bf16 v[116:119], a[252:255], a[192:195], v[116:119]\n\t"
"ds_write_b64 v245, v[228:229] offset:26624\n\t"
"v_mfma_f32_16x16x32_bf16 v[120:123], a[244:247], a[188:191], v[120:123]\n\t"
"ds_read_b64_tr_b16 v[224:225], v246 offset:40960\n\t"
"v_mfma_f32_16x16x32_bf16 v[120:123], a[248:251], a[192:195], v[120:123]\n\t"
"ds_read_b64_tr_b16 v[226:227], v246 offset:44032\n\t"
"v_mfma_f32_16x16x32_bf16 v[120:123], a[252:255], a[196:199], v[120:123]\n\t"
"s_waitcnt lgkmcnt(3)\n\t"
"ds_read_b64_tr_b16 v[228:229], v246 offset:47104\n\t"
"v_mfma_f32_16x16x32_bf16 v[232:235], v[232:235], v[220:223], 0\n\t"
"ds_read_b64_tr_b16 v[230:231], v246 offset:50176\n\t"
"v_mfma_f32_16x16x32_bf16 v[124:127], a[244:247], a[192:195], v[124:127]\n\t"
"v_mfma_f32_16x16x32_bf16 v[124:127], a[248:251], a[196:199], v[124:127]\n\t"
"v_mfma_f32_16x16x32_bf16 v[124:127], a[252:255], a[200:203], v[124:127]\n\t"
"s_waitcnt lgkmcnt(2)\n\t"
"v_mfma_f32_16x16x32_bf16 v[224:227], v[224:227], v[220:223], 0\n\t"
"v_mfma_f32_16x16x32_bf16 v[128:131], a[244:247], a[196:199], v[128:131]\n\t"
"buffer_load_dwordx4 a[244:247], v252, s[8:11], s20 offen offset:1024 sc1\n\t"
"v_mfma_f32_16x16x32_bf16 v[128:131], a[248:251], a[200:203], v[128:131]\n\t"
"s_waitcnt vmcnt(11) lgkmcnt(0)\n\t"
"v_cvt_pk_bf16_f32 v232, v232, v233\n\t"
"v_cvt_pk_bf16_f32 v233, v234, v235\n\t"
"v_mfma_f32_16x16x32_bf16 v[128:131], a[252:255], a[204:207], v[128:131]\n\t"
"v_mfma_f32_16x16x32_bf16 v[228:231], v[228:231], v[220:223], 0\n\t"
"buffer_load_dwordx4 a[248:251], v252, s[8:11], s20 offen offset:2048 sc1\n\t"
"v_mfma_f32_16x16x32_bf16 v[132:135], a[232:235], a[168:171], v[132:135]\n\t"
"s_mov_b32 s20, 368640\n\t"
"v_cvt_pk_bf16_f32 v234, v224, v225\n\t"
"v_cvt_pk_bf16_f32 v235, v226, v227\n\t"
"v_mfma_f32_16x16x32_bf16 v[132:135], v[240:243], a[172:175], v[132:135]\n\t"
"ds_read_b64_tr_b16 v[224:225], v246 offset:53248\n\t"
"v_mfma_f32_16x16x32_bf16 v[132:135], a[208:211], a[176:179], v[132:135]\n\t"
"buffer_load_dwordx4 a[252:255], v252, s[8:11], s20 offen offset:0 sc1\n\t"
"v_mfma_f32_16x16x32_bf16 v[136:139], a[232:235], a[172:175], v[136:139]\n\t"
"ds_read_b64_tr_b16 v[226:227], v246 offset:56320\n\t"
"v_mfma_f32_16x16x32_bf16 v[136:139], v[240:243], a[176:179], v[136:139]\n\t"
"v_cvt_pk_bf16_f32 v228, v228, v229\n\t"
"v_cvt_pk_bf16_f32 v229, v230, v231\n\t"
"v_mfma_f32_16x16x32_bf16 v[136:139], a[208:211], a[180:183], v[136:139]\n\t"
"ds_write2st64_b64 v245, v[232:233], v[234:235] offset0:53 offset1:54\n\t"
"v_mfma_f32_16x16x32_bf16 v[140:143], a[232:235], a[176:179], v[140:143]\n\t"
"v_mfma_f32_16x16x32_bf16 v[140:143], v[240:243], a[180:183], v[140:143]\n\t"
"v_mfma_f32_16x16x32_bf16 v[140:143], a[208:211], a[184:187], v[140:143]\n\t"
"v_mfma_f32_16x16x32_bf16 v[144:147], a[232:235], a[180:183], v[144:147]\n\t"
"v_mfma_f32_16x16x32_bf16 v[144:147], v[240:243], a[184:187], v[144:147]\n\t"
"v_mfma_f32_16x16x32_bf16 v[144:147], a[208:211], a[188:191], v[144:147]\n\t"
"v_mfma_f32_16x16x32_bf16 v[148:151], a[232:235], a[184:187], v[148:151]\n\t"
"s_waitcnt lgkmcnt(1)\n\t"
"s_add_u32 m0, s24, 25984\n\t"
"s_add_u32 s28, s21, 786432\n\t"
"v_mfma_f32_16x16x32_bf16 v[148:151], v[240:243], a[188:191], v[148:151]\n\t"
"v_mfma_f32_16x16x32_bf16 v[148:151], a[208:211], a[192:195], v[148:151]\n\t"
"v_mfma_f32_16x16x32_bf16 v[224:227], v[224:227], v[220:223], 0\n\t"
"v_mfma_f32_16x16x32_bf16 v[152:155], a[232:235], a[188:191], v[152:155]\n\t"
"buffer_load_dwordx4 v247, s[12:15], s28 offen offset:128 nt lds\n\t"
"v_mfma_f32_16x16x32_bf16 v[152:155], v[240:243], a[192:195], v[152:155]\n\t"
"s_add_u32 m0, s24, 72064\n\t"
"s_add_u32 s28, s21, 2162688\n\t"
"v_mfma_f32_16x16x32_bf16 v[152:155], a[208:211], a[196:199], v[152:155]\n\t"
"buffer_load_dwordx4 v247, s[12:15], s28 offen offset:128 nt lds\n\t"
"v_mfma_f32_16x16x32_bf16 v[156:159], a[232:235], a[192:195], v[156:159]\n\t"
"s_add_u32 m0, s24, 79744\n\t"
"s_add_u32 s28, s21, 2359296\n\t"
"v_mfma_f32_16x16x32_bf16 v[156:159], v[240:243], a[196:199], v[156:159]\n\t"
"buffer_load_dwordx4 v247, s[12:15], s28 offen offset:128 nt lds\n\t"
"v_mfma_f32_16x16x32_bf16 v[156:159], a[208:211], a[200:203], v[156:159]\n\t"
"s_add_u32 m0, s24, 85888\n\t"
"s_add_u32 s28, s21, 2555904\n\t"
"v_cvt_pk_bf16_f32 v230, v224, v225\n\t"
"v_cvt_pk_bf16_f32 v231, v226, v227\n\t"
"v_mfma_f32_16x16x32_bf16 v[160:163], a[232:235], a[196:199], v[160:163]\n\t"
"s_waitcnt vmcnt(13)\n\t"
"v_mfma_f32_16x16x32_bf16 v[160:163], v[240:243], a[200:203], v[160:163]\n\t"
"buffer_load_dwordx4 v247, s[12:15], s28 offen offset:128 nt lds\n\t"
"v_mfma_f32_16x16x32_bf16 v[160:163], a[208:211], a[204:207], v[160:163]\n\t"
"s_add_u32 m0, s25, -128\n\t"
"ds_write2st64_b64 v245, v[228:229], v[230:231] offset0:55 offset1:56\n\t"
"v_mfma_f32_16x16x32_bf16 v[164:167], v[236:239], a[168:171], v[164:167]\n\t"
"buffer_load_dwordx4 v254, s[12:15], 0 offen offset:128 nt lds\n\t"
"v_mfma_f32_16x16x32_bf16 v[164:167], v[248:251], a[172:175], v[164:167]\n\t"
"s_add_u32 m0, s26, -128\n\t"
"v_mfma_f32_16x16x32_bf16 v[164:167], a[212:215], a[176:179], v[164:167]\n\t"
"s_waitcnt lgkmcnt(0)\n\t"
"s_barrier\n\t"
"v_mfma_f32_16x16x32_bf16 v[168:171], v[236:239], a[172:175], v[168:171]\n\t"
"ds_read_b128 a[168:171], v244 offset:24576\n\t"
"v_mfma_f32_16x16x32_bf16 v[168:171], v[248:251], a[176:179], v[168:171]\n\t"
"ds_read_b128 a[172:175], v244 offset:25088\n\t"
"v_mfma_f32_16x16x32_bf16 v[168:171], a[212:215], a[180:183], v[168:171]\n\t"
"s_mov_b64 exec, s[36:37]\n\t"
"buffer_load_dwordx4 v254, s[12:15], s27 offen offset:128 nt lds\n\t"
"s_mov_b64 exec, -1\n\t"
"v_mfma_f32_16x16x32_bf16 v[172:175], v[236:239], a[176:179], v[172:175]\n\t"
"ds_read_b128 a[176:179], v244 offset:25600\n\t"
"v_mfma_f32_16x16x32_bf16 v[172:175], v[248:251], a[180:183], v[172:175]\n\t"
"v_mfma_f32_16x16x32_bf16 v[172:175], a[212:215], a[184:187], v[172:175]\n\t"
"v_mfma_f32_16x16x32_bf16 v[176:179], v[236:239], a[180:183], v[176:179]\n\t"
"ds_read_b128 a[180:183], v244 offset:26112\n\t"
"v_mfma_f32_16x16x32_bf16 v[176:179], v[248:251], a[184:187], v[176:179]\n\t"
"v_mfma_f32_16x16x32_bf16 v[176:179], a[212:215], a[188:191], v[176:179]\n\t"
"v_mfma_f32_16x16x32_bf16 v[180:183], v[236:239], a[184:187], v[180:183]\n\t"
"ds_read_b128 a[184:187], v244 offset:26624\n\t"
"v_mfma_f32_16x16x32_bf16 v[180:183], v[248:251], a[188:191], v[180:183]\n\t"
"v_mfma_f32_16x16x32_bf16 v[180:183], a[212:215], a[192:195], v[180:183]\n\t"
"v_mfma_f32_16x16x32_bf16 v[184:187], v[236:239], a[188:191], v[184:187]\n\t"
"ds_read_b128 a[188:191], v244 offset:27136\n\t"
"v_mfma_f32_16x16x32_bf16 v[184:187], v[248:251], a[192:195], v[184:187]\n\t"
"v_mfma_f32_16x16x32_bf16 v[184:187], a[212:215], a[196:199], v[184:187]\n\t"
"v_mfma_f32_16x16x32_bf16 v[188:191], v[236:239], a[192:195], v[188:191]\n\t"
"ds_read_b128 a[192:195], v244 offset:27648\n\t"
"v_mfma_f32_16x16x32_bf16 v[188:191], v[248:251], a[196:199], v[188:191]\n\t"
"v_mfma_f32_16x16x32_bf16 v[188:191], a[212:215], a[200:203], v[188:191]\n\t"
"v_mfma_f32_16x16x32_bf16 v[192:195], v[236:239], a[196:199], v[192:195]\n\t"
"s_waitcnt vmcnt(13) lgkmcnt(4)\n\t"
"ds_read_b128 a[196:199], v244 offset:28160\n\t"
"v_mfma_f32_16x16x32_bf16 v[192:195], v[248:251], a[200:203], v[192:195]\n\t"
"ds_read_b128 a[200:203], v244 offset:28672\n\t"
"v_mfma_f32_16x16x32_bf16 v[192:195], a[212:215], a[204:207], v[192:195]\n\t"
"ds_read_b128 a[204:207], v244 offset:29184\n\t"
"Lst22_%=:\n\t"
"v_mfma_f32_16x16x32_bf16 v[0:3], a[216:219], a[168:171], v[0:3]\n\t"
"s_waitcnt lgkmcnt(6)\n\t"
"v_xor_b32 v246, 0xc0, v246\n\t"
"v_mfma_f32_16x16x32_bf16 v[0:3], a[220:223], a[172:175], v[0:3]\n\t"
"v_mfma_f32_16x16x32_bf16 v[0:3], v[96:99], a[176:179], v[0:3]\n\t"
"v_mfma_f32_16x16x32_bf16 v[4:7], a[216:219], a[172:175], v[4:7]\n\t"
"s_waitcnt lgkmcnt(5)\n\t"
"v_mfma_f32_16x16x32_bf16 v[4:7], a[220:223], a[176:179], v[4:7]\n\t"
"v_mfma_f32_16x16x32_bf16 v[4:7], v[96:99], a[180:183], v[4:7]\n\t"
"v_mfma_f32_16x16x32_bf16 v[8:11], a[216:219], a[176:179], v[8:11]\n\t"
"buffer_load_dwordx4 v[240:243], v252, s[8:11], s20 offen offset:1024 sc1\n\t"
"v_mfma_f32_16x16x32_bf16 v[8:11], a[220:223], a[180:183], v[8:11]\n\t"
"s_waitcnt lgkmcnt(4)\n\t"
"v_mfma_f32_16x16x32_bf16 v[8:11], v[96:99], a[184:187], v[8:11]\n\t"
"v_mfma_f32_16x16x32_bf16 v[12:15], a[216:219], a[180:183], v[12:15]\n\t"
"s_waitcnt lgkmcnt(3)\n\t"
"v_mfma_f32_16x16x32_bf16 v[12:15], a[220:223], a[184:187], v[12:15]\n\t"
"v_mfma_f32_16x16x32_bf16 v[12:15], v[96:99], a[188:191], v[12:15]\n\t"
"v_mfma_f32_16x16x32_bf16 v[16:19], a[216:219], a[184:187], v[16:19]\n\t"
"s_waitcnt lgkmcnt(2)\n\t"
"v_mfma_f32_16x16x32_bf16 v[16:19], a[220:223], a[188:191], v[16:19]\n\t"
"v_mfma_f32_16x16x32_bf16 v[16:19], v[96:99], a[192:195], v[16:19]\n\t"
"ds_read_b64_tr_b16 v[232:233], v246 offset:1024\n\t"
"v_mfma_f32_16x16x32_bf16 v[20:23], a[216:219], a[188:191], v[20:23]\n\t"
"s_waitcnt lgkmcnt(2)\n\t"
"ds_read_b64_tr_b16 v[234:235], v246 offset:4096\n\t"
"v_mfma_f32_16x16x32_bf16 v[20:23], a[220:223], a[192:195], v[20:23]\n\t"
"v_mfma_f32_16x16x32_bf16 v[20:23], v[96:99], a[196:199], v[20:23]\n\t"
"v_mfma_f32_16x16x32_bf16 v[24:27], a[216:219], a[192:195], v[24:27]\n\t"
"s_waitcnt lgkmcnt(2)\n\t"
"v_mfma_f32_16x16x32_bf16 v[24:27], a[220:223], a[196:199], v[24:27]\n\t"
"v_mfma_f32_16x16x32_bf16 v[24:27], v[96:99], a[200:203], v[24:27]\n\t"
"v_mfma_f32_16x16x32_bf16 v[28:31], a[216:219], a[196:199], v[28:31]\n\t"
"s_waitcnt vmcnt(11)\n\t"
"ds_read_b64_tr_b16 v[224:225], v246 offset:7168\n\t"
"v_mfma_f32_16x16x32_bf16 v[28:31], a[220:223], a[200:203], v[28:31]\n\t"
"ds_read_b64_tr_b16 v[226:227], v246 offset:10240\n\t"
"v_mfma_f32_16x16x32_bf16 v[28:31], v[96:99], a[204:207], v[28:31]\n\t"
"v_mfma_f32_16x16x32_bf16 v[32:35], a[224:227], a[168:171], v[32:35]\n\t"
"v_mfma_f32_16x16x32_bf16 v[32:35], a[228:231], a[172:175], v[32:35]\n\t"
"v_mfma_f32_16x16x32_bf16 v[32:35], a[236:239], a[176:179], v[32:35]\n\t"
"v_mfma_f32_16x16x32_bf16 v[36:39], a[224:227], a[172:175], v[36:39]\n\t"
"s_waitcnt lgkmcnt(2)\n\t"
"v_mfma_f32_16x16x32_bf16 v[36:39], a[228:231], a[176:179], v[36:39]\n\t"
"v_mfma_f32_16x16x32_bf16 v[36:39], a[236:239], a[180:183], v[36:39]\n\t"
"v_mfma_f32_16x16x32_bf16 v[232:235], v[232:235], v[220:223], 0\n\t"
"v_mfma_f32_16x16x32_bf16 v[40:43], a[224:227], a[176:179], v[40:43]\n\t"
"ds_read_b64_tr_b16 v[228:229], v246 offset:13312\n\t"
"v_mfma_f32_16x16x32_bf16 v[40:43], a[228:231], a[180:183], v[40:43]\n\t"
"ds_read_b64_tr_b16 v[230:231], v246 offset:16384\n\t"
"v_mfma_f32_16x16x32_bf16 v[40:43], a[236:239], a[184:187], v[40:43]\n\t"
"v_mfma_f32_16x16x32_bf16 v[44:47], a[224:227], a[180:183], v[44:47]\n\t"
"s_waitcnt lgkmcnt(2)\n\t"
"v_mfma_f32_16x16x32_bf16 v[44:47], a[228:231], a[184:187], v[44:47]\n\t"
"v_cvt_pk_bf16_f32 v232, v232, v233\n\t"
"v_cvt_pk_bf16_f32 v233, v234, v235\n\t"
"v_mfma_f32_16x16x32_bf16 v[44:47], a[236:239], a[188:191], v[44:47]\n\t"
"v_mfma_f32_16x16x32_bf16 v[224:227], v[224:227], v[220:223], 0\n\t"
"buffer_load_dwordx4 a[208:211], v252, s[8:11], s20 offen offset:2048 sc1\n\t"
"v_mfma_f32_16x16x32_bf16 v[48:51], a[224:227], a[184:187], v[48:51]\n\t"
"s_mov_b32 s20, 380928\n\t"
"v_mfma_f32_16x16x32_bf16 v[48:51], a[228:231], a[188:191], v[48:51]\n\t"
"v_mfma_f32_16x16x32_bf16 v[48:51], a[236:239], a[192:195], v[48:51]\n\t"
"v_mfma_f32_16x16x32_bf16 v[52:55], a[224:227], a[188:191], v[52:55]\n\t"
"buffer_load_dwordx4 v[236:239], v252, s[8:11], s20 offen offset:0 sc1\n\t"
"v_mfma_f32_16x16x32_bf16 v[52:55], a[228:231], a[192:195], v[52:55]\n\t"
"v_cvt_pk_bf16_f32 v234, v224, v225\n\t"
"v_cvt_pk_bf16_f32 v235, v226, v227\n\t"
"v_mfma_f32_16x16x32_bf16 v[52:55], a[236:239], a[196:199], v[52:55]\n\t"
"ds_read_b64_tr_b16 v[224:225], v246 offset:20992\n\t"
"v_mfma_f32_16x16x32_bf16 v[56:59], a[224:227], a[192:195], v[56:59]\n\t"
"s_waitcnt lgkmcnt(1)\n\t"
"ds_read_b64_tr_b16 v[226:227], v246 offset:24064\n\t"
"v_mfma_f32_16x16x32_bf16 v[56:59], a[228:231], a[196:199], v[56:59]\n\t"
"buffer_load_dwordx4 v[96:99], v252, s[8:11], s20 offen offset:1024 sc1\n\t"
"v_mfma_f32_16x16x32_bf16 v[56:59], a[236:239], a[200:203], v[56:59]\n\t"
"v_mfma_f32_16x16x32_bf16 v[228:231], v[228:231], v[220:223], 0\n\t"
"v_mfma_f32_16x16x32_bf16 v[60:63], a[224:227], a[196:199], v[60:63]\n\t"
"s_waitcnt vmcnt(11)\n\t"
"v_mfma_f32_16x16x32_bf16 v[60:63], a[228:231], a[200:203], v[60:63]\n\t"
"buffer_load_dwordx4 v[248:251], v252, s[8:11], s20 offen offset:2048 sc1\n\t"
"v_mfma_f32_16x16x32_bf16 v[60:63], a[236:239], a[204:207], v[60:63]\n\t"
"s_mov_b32 s20, 393216\n\t"
"v_mfma_f32_16x16x32_bf16 v[64:67], a[240:243], a[168:171], v[64:67]\n\t"
"ds_read_b128 a[168:171], v244 offset:33792\n\t"
"v_mfma_f32_16x16x32_bf16 v[64:67], a[244:247], a[172:175], v[64:67]\n\t"
"v_cvt_pk_bf16_f32 v228, v228, v229\n\t"
"v_cvt_pk_bf16_f32 v229, v230, v231\n\t"
"v_mfma_f32_16x16x32_bf16 v[64:67], a[248:251], a[176:179], v[64:67]\n\t"
"v_mfma_f32_16x16x32_bf16 v[68:71], a[240:243], a[172:175], v[68:71]\n\t"
"buffer_load_dwordx4 a[212:215], v252, s[8:11], s20 offen offset:0 sc1\n\t"
"v_mfma_f32_16x16x32_bf16 v[68:71], a[244:247], a[176:179], v[68:71]\n\t"
"s_waitcnt lgkmcnt(1)\n\t"
"ds_read_b128 a[172:175], v244 offset:34304\n\t"
"v_mfma_f32_16x16x32_bf16 v[68:71], a[248:251], a[180:183], v[68:71]\n\t"
"v_mfma_f32_16x16x32_bf16 v[224:227], v[224:227], v[220:223], 0\n\t"
"buffer_load_dwordx4 a[216:219], v252, s[8:11], s20 offen offset:1024 sc1\n\t"
"v_mfma_f32_16x16x32_bf16 v[72:75], a[240:243], a[176:179], v[72:75]\n\t"
"ds_read_b128 a[176:179], v244 offset:34816\n\t"
"v_mfma_f32_16x16x32_bf16 v[72:75], a[244:247], a[180:183], v[72:75]\n\t"
"v_mfma_f32_16x16x32_bf16 v[72:75], a[248:251], a[184:187], v[72:75]\n\t"
"v_mfma_f32_16x16x32_bf16 v[76:79], a[240:243], a[180:183], v[76:79]\n\t"
"buffer_load_dwordx4 a[220:223], v252, s[8:11], s20 offen offset:2048 sc1\n\t"
"v_mfma_f32_16x16x32_bf16 v[76:79], a[244:247], a[184:187], v[76:79]\n\t"
"s_mov_b32 s20, 405504\n\t"
"ds_read_b128 a[180:183], v244 offset:35328\n\t"
"v_cvt_pk_bf16_f32 v230, v224, v225\n\t"
"v_mfma_f32_16x16x32_bf16 v[76:79], a[248:251], a[188:191], v[76:79]\n\t"
"v_cvt_pk_bf16_f32 v231, v226, v227\n\t"
"v_mfma_f32_16x16x32_bf16 v[80:83], a[240:243], a[184:187], v[80:83]\n\t"
"ds_read_b64_tr_b16 v[224:225], v246 offset:27136\n\t"
"v_mfma_f32_16x16x32_bf16 v[80:83], a[244:247], a[188:191], v[80:83]\n\t"
"ds_read_b64_tr_b16 v[226:227], v246 offset:30208\n\t"
"v_mfma_f32_16x16x32_bf16 v[80:83], a[248:251], a[192:195], v[80:83]\n\t"
"buffer_load_dwordx4 a[224:227], v252, s[8:11], s20 offen offset:0 sc1\n\t"
"v_mfma_f32_16x16x32_bf16 v[84:87], a[240:243], a[188:191], v[84:87]\n\t"
"ds_read_b128 a[184:187], v244 offset:35840\n\t"
"v_mfma_f32_16x16x32_bf16 v[84:87], a[244:247], a[192:195], v[84:87]\n\t"
"ds_read_b128 a[188:191], v244 offset:36352\n\t"
"v_mfma_f32_16x16x32_bf16 v[84:87], a[248:251], a[196:199], v[84:87]\n\t"
"buffer_load_dwordx4 a[228:231], v252, s[8:11], s20 offen offset:1024 sc1\n\t"
"v_mfma_f32_16x16x32_bf16 v[88:91], a[240:243], a[192:195], v[88:91]\n\t"
"ds_read_b128 a[192:195], v244 offset:36864\n\t"
"v_mfma_f32_16x16x32_bf16 v[88:91], a[244:247], a[196:199], v[88:91]\n\t"
"v_mfma_f32_16x16x32_bf16 v[88:91], a[248:251], a[200:203], v[88:91]\n\t"
"v_mfma_f32_16x16x32_bf16 v[92:95], a[240:243], a[196:199], v[92:95]\n\t"
"buffer_load_dwordx4 a[232:235], v252, s[8:11], s20 offen offset:2048 sc1\n\t"
"v_mfma_f32_16x16x32_bf16 v[92:95], a[244:247], a[200:203], v[92:95]\n\t"
"s_waitcnt vmcnt(9) lgkmcnt(2)\n\t"
"ds_read_b128 a[196:199], v244 offset:37376\n\t"
"s_mov_b32 s20, 417792\n\t"
"v_mfma_f32_16x16x32_bf16 v[92:95], a[248:251], a[204:207], v[92:95]\n\t"
"Lpg17_%=:\n\t"
"s_cbranch_vccnz Lvr2_%=\n\t"
"ds_read_b128 a[200:203], v244 offset:37888\n\t"
"v_mfma_f32_16x16x32_bf16 v[224:227], v[224:227], v[220:223], 0\n\t"
"ds_read_b128 a[204:207], v244 offset:38400\n\t"
"v_mfma_f32_16x16x32_bf16 v[100:103], a[252:255], a[168:171], v[100:103]\n\t"
"buffer_load_dwordx4 a[236:239], v252, s[8:11], s20 offen offset:0 sc1\n\t"
"v_mfma_f32_16x16x32_bf16 v[100:103], v[240:243], a[172:175], v[100:103]\n\t"
"v_mfma_f32_16x16x32_bf16 v[100:103], a[208:211], a[176:179], v[100:103]\n\t"
"v_mfma_f32_16x16x32_bf16 v[104:107], a[252:255], a[172:175], v[104:107]\n\t"
"buffer_load_dwordx4 a[240:243], v252, s[8:11], s20 offen offset:1024 sc1\n\t"
"v_mfma_f32_16x16x32_bf16 v[104:107], v[240:243], a[176:179], v[104:107]\n\t"
"v_cvt_pk_bf16_f32 v224, v224, v225\n\t"
"v_mfma_f32_16x16x32_bf16 v[104:107], a[208:211], a[180:183], v[104:107]\n\t"
"s_waitcnt lgkmcnt(0)\n\t"
"s_barrier\n\t"
"v_mfma_f32_16x16x32_bf16 v[108:111], a[252:255], a[176:179], v[108:111]\n\t"
"s_add_u32 m0, s24, 32128\n\t"
"s_add_u32 s28, s21, 983040\n\t"
"v_mfma_f32_16x16x32_bf16 v[108:111], v[240:243], a[180:183], v[108:111]\n\t"
"buffer_load_dwordx4 a[244:247], v252, s[8:11], s20 offen offset:2048 sc1\n\t"
"v_mfma_f32_16x16x32_bf16 v[108:111], a[208:211], a[184:187], v[108:111]\n\t"
"s_mov_b32 s20, 430080\n\t"
"ds_write2st64_b64 v245, v[232:233], v[234:235] offset0:48 offset1:49\n\t"
"v_cvt_pk_bf16_f32 v225, v226, v227\n\t"
"v_mfma_f32_16x16x32_bf16 v[112:115], a[252:255], a[180:183], v[112:115]\n\t"
"v_mfma_f32_16x16x32_bf16 v[112:115], v[240:243], a[184:187], v[112:115]\n\t"
"ds_read_b64_tr_b16 v[232:233], v246 offset:33280\n\t"
"v_mfma_f32_16x16x32_bf16 v[112:115], a[208:211], a[188:191], v[112:115]\n\t"
"ds_write2st64_b64 v245, v[228:229], v[230:231] offset0:50 offset1:51\n\t"
"v_mfma_f32_16x16x32_bf16 v[116:119], a[252:255], a[184:187], v[116:119]\n\t"
"buffer_load_dwordx4 v247, s[12:15], s28 offen offset:128 nt lds\n\t"
"v_mfma_f32_16x16x32_bf16 v[116:119], v[240:243], a[188:191], v[116:119]\n\t"
"s_add_u32 m0, s24, 39808\n\t"
"s_add_u32 s28, s21, 1179648\n\t"
"ds_read_b64_tr_b16 v[234:235], v246 offset:36352\n\t"
"v_mfma_f32_16x16x32_bf16 v[116:119], a[208:211], a[192:195], v[116:119]\n\t"
"ds_write_b64 v245, v[224:225] offset:26624\n\t"
"v_mfma_f32_16x16x32_bf16 v[120:123], a[252:255], a[188:191], v[120:123]\n\t"
"buffer_load_dwordx4 a[248:251], v252, s[8:11], s20 offen offset:0 sc1\n\t"
"v_mfma_f32_16x16x32_bf16 v[120:123], v[240:243], a[192:195], v[120:123]\n\t"
"buffer_load_dwordx4 v247, s[12:15], s28 offen offset:128 nt lds\n\t"
"v_mfma_f32_16x16x32_bf16 v[120:123], a[208:211], a[196:199], v[120:123]\n\t"
"s_waitcnt lgkmcnt(1)\n\t"
"ds_read_b64_tr_b16 v[228:229], v246 offset:40960\n\t"
"ds_read_b64_tr_b16 v[230:231], v246 offset:44032\n\t"
"s_add_u32 m0, s24, 45952\n\t"
"s_add_u32 s28, s21, 1376256\n\t"
"v_mfma_f32_16x16x32_bf16 v[232:235], v[232:235], v[220:223], 0\n\t"
"buffer_load_dwordx4 v247, s[12:15], s28 offen offset:128 nt lds\n\t"
"s_add_u32 m0, s24, 52096\n\t"
"s_add_u32 s28, s21, 1572864\n\t"
"v_mfma_f32_16x16x32_bf16 v[124:127], a[252:255], a[192:195], v[124:127]\n\t"
"buffer_load_dwordx4 v247, s[12:15], s28 offen offset:128 nt lds\n\t"
"v_mfma_f32_16x16x32_bf16 v[124:127], v[240:243], a[196:199], v[124:127]\n\t"
"s_add_u32 m0, s24, 92032\n\t"
"s_add_u32 s28, s21, 2752512\n\t"
"ds_read_b64_tr_b16 v[224:225], v246 offset:47104\n\t"
"v_mfma_f32_16x16x32_bf16 v[124:127], a[208:211], a[200:203], v[124:127]\n\t"
"s_waitcnt lgkmcnt(1)\n\t"
"ds_read_b64_tr_b16 v[226:227], v246 offset:50176\n\t"
"v_mfma_f32_16x16x32_bf16 v[228:231], v[228:231], v[220:223], 0\n\t"
"buffer_load_dwordx4 v247, s[12:15], s28 offen offset:128 nt lds\n\t"
"s_add_u32 m0, s24, 99712\n\t"
"s_add_u32 s28, s21, 2949120\n\t"
"v_mfma_f32_16x16x32_bf16 v[128:131], a[252:255], a[196:199], v[128:131]\n\t"
"buffer_load_dwordx4 v247, s[12:15], s28 offen offset:128 nt lds\n\t"
"v_mfma_f32_16x16x32_bf16 v[128:131], v[240:243], a[200:203], v[128:131]\n\t"
"s_waitcnt vmcnt(16)\n\t"
"s_add_u32 m0, s24, 105856\n\t"
"s_add_u32 s28, s21, 3145728\n\t"
"v_mfma_f32_16x16x32_bf16 v[128:131], a[208:211], a[204:207], v[128:131]\n\t"
"s_waitcnt lgkmcnt(0)\n\t"
"buffer_load_dwordx4 v247, s[12:15], s28 offen offset:128 nt lds\n\t"
"s_add_u32 m0, s24, 112000\n\t"
"s_add_u32 s28, s21, 3342336\n\t"
"v_mfma_f32_16x16x32_bf16 v[224:227], v[224:227], v[220:223], 0\n\t"
"buffer_load_dwordx4 v247, s[12:15], s28 offen offset:128 nt lds\n\t"
"v_cvt_pk_bf16_f32 v232, v232, v233\n\t"
"v_cvt_pk_bf16_f32 v233, v234, v235\n\t"
"v_mfma_f32_16x16x32_bf16 v[132:135], v[236:239], a[168:171], v[132:135]\n\t"
"v_cvt_pk_bf16_f32 v234, v228, v229\n\t"
"v_cvt_pk_bf16_f32 v235, v230, v231\n\t"
"v_mfma_f32_16x16x32_bf16 v[132:135], v[96:99], a[172:175], v[132:135]\n\t"
"ds_read_b64_tr_b16 v[228:229], v246 offset:53248\n\t"
"v_mfma_f32_16x16x32_bf16 v[132:135], v[248:251], a[176:179], v[132:135]\n\t"
"ds_write2st64_b64 v245, v[232:233], v[234:235] offset0:53 offset1:54\n\t"
"v_mfma_f32_16x16x32_bf16 v[136:139], v[236:239], a[172:175], v[136:139]\n\t"
"v_mfma_f32_16x16x32_bf16 v[136:139], v[96:99], a[176:179], v[136:139]\n\t"
"ds_read_b64_tr_b16 v[230:231], v246 offset:56320\n\t"
"v_mfma_f32_16x16x32_bf16 v[136:139], v[248:251], a[180:183], v[136:139]\n\t"
"v_cvt_pk_bf16_f32 v224, v224, v225\n\t"
"v_cvt_pk_bf16_f32 v225, v226, v227\n\t"
"v_mfma_f32_16x16x32_bf16 v[140:143], v[236:239], a[176:179], v[140:143]\n\t"
"v_mfma_f32_16x16x32_bf16 v[140:143], v[96:99], a[180:183], v[140:143]\n\t"
"v_mfma_f32_16x16x32_bf16 v[140:143], v[248:251], a[184:187], v[140:143]\n\t"
"v_mfma_f32_16x16x32_bf16 v[144:147], v[236:239], a[180:183], v[144:147]\n\t"
"v_mfma_f32_16x16x32_bf16 v[144:147], v[96:99], a[184:187], v[144:147]\n\t"
"v_mfma_f32_16x16x32_bf16 v[144:147], v[248:251], a[188:191], v[144:147]\n\t"
"v_mfma_f32_16x16x32_bf16 v[148:151], v[236:239], a[184:187], v[148:151]\n\t"
"s_waitcnt lgkmcnt(0)\n\t"
"v_mfma_f32_16x16x32_bf16 v[148:151], v[96:99], a[188:191], v[148:151]\n\t"
"v_mfma_f32_16x16x32_bf16 v[148:151], v[248:251], a[192:195], v[148:151]\n\t"
"v_mfma_f32_16x16x32_bf16 v[228:231], v[228:231], v[220:223], 0\n\t"
"v_mfma_f32_16x16x32_bf16 v[152:155], v[236:239], a[188:191], v[152:155]\n\t"
"v_mfma_f32_16x16x32_bf16 v[152:155], v[96:99], a[192:195], v[152:155]\n\t"
"v_mfma_f32_16x16x32_bf16 v[152:155], v[248:251], a[196:199], v[152:155]\n\t"
"v_mfma_f32_16x16x32_bf16 v[156:159], v[236:239], a[192:195], v[156:159]\n\t"
"v_mfma_f32_16x16x32_bf16 v[156:159], v[96:99], a[196:199], v[156:159]\n\t"
"v_cvt_pk_bf16_f32 v226, v228, v229\n\t"
"v_cvt_pk_bf16_f32 v227, v230, v231\n\t"
"v_mfma_f32_16x16x32_bf16 v[156:159], v[248:251], a[200:203], v[156:159]\n\t"
"ds_write2st64_b64 v245, v[224:225], v[226:227] offset0:55 offset1:56\n\t"
"v_mfma_f32_16x16x32_bf16 v[160:163], v[236:239], a[196:199], v[160:163]\n\t"
"s_waitcnt vmcnt(15)\n\t"
"v_mfma_f32_16x16x32_bf16 v[160:163], v[96:99], a[200:203], v[160:163]\n\t"
"v_mfma_f32_16x16x32_bf16 v[160:163], v[248:251], a[204:207], v[160:163]\n\t"
"v_mfma_f32_16x16x32_bf16 v[164:167], a[212:215], a[168:171], v[164:167]\n\t"
"v_mfma_f32_16x16x32_bf16 v[164:167], a[216:219], a[172:175], v[164:167]\n\t"
"v_mfma_f32_16x16x32_bf16 v[164:167], a[220:223], a[176:179], v[164:167]\n\t"
"s_waitcnt lgkmcnt(0)\n\t"
"s_barrier\n\t"
"v_mfma_f32_16x16x32_bf16 v[168:171], a[212:215], a[172:175], v[168:171]\n\t"
"ds_read_b128 a[168:171], v244 offset:24576\n\t"
"v_mfma_f32_16x16x32_bf16 v[168:171], a[216:219], a[176:179], v[168:171]\n\t"
"ds_read_b128 a[172:175], v244 offset:25088\n\t"
"v_mfma_f32_16x16x32_bf16 v[168:171], a[220:223], a[180:183], v[168:171]\n\t"
"v_mfma_f32_16x16x32_bf16 v[172:175], a[212:215], a[176:179], v[172:175]\n\t"
"ds_read_b128 a[176:179], v244 offset:25600\n\t"
"v_mfma_f32_16x16x32_bf16 v[172:175], a[216:219], a[180:183], v[172:175]\n\t"
"v_mfma_f32_16x16x32_bf16 v[172:175], a[220:223], a[184:187], v[172:175]\n\t"
"v_mfma_f32_16x16x32_bf16 v[176:179], a[212:215], a[180:183], v[176:179]\n\t"
"ds_read_b128 a[180:183], v244 offset:26112\n\t"
"v_mfma_f32_16x16x32_bf16 v[176:179], a[216:219], a[184:187], v[176:179]\n\t"
"v_mfma_f32_16x16x32_bf16 v[176:179], a[220:223], a[188:191], v[176:179]\n\t"
"v_mfma_f32_16x16x32_bf16 v[180:183], a[212:215], a[184:187], v[180:183]\n\t"
"ds_read_b128 a[184:187], v244 offset:26624\n\t"
"v_mfma_f32_16x16x32_bf16 v[180:183], a[216:219], a[188:191], v[180:183]\n\t"
"v_mfma_f32_16x16x32_bf16 v[180:183], a[220:223], a[192:195], v[180:183]\n\t"
"v_mfma_f32_16x16x32_bf16 v[184:187], a[212:215], a[188:191], v[184:187]\n\t"
"ds_read_b128 a[188:191], v244 offset:27136\n\t"
"v_mfma_f32_16x16x32_bf16 v[184:187], a[216:219], a[192:195], v[184:187]\n\t"
"v_mfma_f32_16x16x32_bf16 v[184:187], a[220:223], a[196:199], v[184:187]\n\t"
"v_mfma_f32_16x16x32_bf16 v[188:191], a[212:215], a[192:195], v[188:191]\n\t"
"ds_read_b128 a[192:195], v244 offset:27648\n\t"
"v_mfma_f32_16x16x32_bf16 v[188:191], a[216:219], a[196:199], v[188:191]\n\t"
"v_mfma_f32_16x16x32_bf16 v[188:191], a[220:223], a[200:203], v[188:191]\n\t"
"v_mfma_f32_16x16x32_bf16 v[192:195], a[212:215], a[196:199], v[192:195]\n\t"
"s_waitcnt vmcnt(12) lgkmcnt(4)\n\t"
"ds_read_b128 a[196:199], v244 offset:28160\n\t"
"v_mfma_f32_16x16x32_bf16 v[192:195], a[216:219], a[200:203], v[192:195]\n\t"
"ds_read_b128 a[200:203], v244 offset:28672\n\t"
"v_mfma_f32_16x16x32_bf16 v[192:195], a[220:223], a[204:207], v[192:195]\n\t"
"ds_read_b128 a[204:207], v244 offset:29184\n\t"
"Lst23_%=:\n\t"
"v_mfma_f32_16x16x32_bf16 v[196:199], a[224:227], a[168:171], v[196:199]\n\t"
"s_mov_b64 vcc, -1\n\t"
"s_bitcmp1_b32 s73, 1\n\t"
"s_cbranch_scc1 Lvh3_%=\n\t"
"s_bitcmp1_b32 s73, 0\n\t"
"s_cbranch_scc1 Lpg22_%=\n\t"
"s_branch Lpg21_%=\n\t"
"Lvh3_%=:\n\t"
"s_bitcmp1_b32 s73, 0\n\t"
"s_cbranch_scc1 Lpg24_%=\n\t"
"s_branch Lpg23_%=\n\t"
"Lvr3_%=:\n\t"
"s_mov_b64 vcc, 0\n\t"
"s_waitcnt lgkmcnt(6)\n\t"
"v_mfma_f32_16x16x32_bf16 v[196:199], a[228:231], a[172:175], v[196:199]\n\t"
"buffer_load_dwordx4 v[240:243], v252, s[8:11], s20 offen offset:1024 sc1\n\t"
"v_mfma_f32_16x16x32_bf16 v[196:199], a[232:235], a[176:179], v[196:199]\n\t"
"v_mfma_f32_16x16x32_bf16 v[204:207], a[224:227], a[172:175], v[204:207]\n\t"
"s_waitcnt lgkmcnt(5)\n\t"
"v_mfma_f32_16x16x32_bf16 v[204:207], a[228:231], a[176:179], v[204:207]\n\t"
"v_mfma_f32_16x16x32_bf16 v[204:207], a[232:235], a[180:183], v[204:207]\n\t"
"v_mfma_f32_16x16x32_bf16 v[212:215], a[224:227], a[176:179], v[212:215]\n\t"
"s_waitcnt lgkmcnt(4)\n\t"
"v_mfma_f32_16x16x32_bf16 v[212:215], a[228:231], a[180:183], v[212:215]\n\t"
"v_mfma_f32_16x16x32_bf16 v[212:215], a[232:235], a[184:187], v[212:215]\n\t"
"v_mfma_f32_16x16x32_bf16 a[0:3], a[224:227], a[180:183], a[0:3]\n\t"
"s_waitcnt lgkmcnt(3)\n\t"
"v_mfma_f32_16x16x32_bf16 a[0:3], a[228:231], a[184:187], a[0:3]\n\t"
"v_mfma_f32_16x16x32_bf16 a[0:3], a[232:235], a[188:191], a[0:3]\n\t"
"buffer_load_dwordx4 a[208:211], v252, s[8:11], s20 offen offset:2048 sc1\n\t"
"v_mfma_f32_16x16x32_bf16 a[8:11], a[224:227], a[184:187], a[8:11]\n\t"
"s_waitcnt lgkmcnt(2)\n\t"
"s_mov_b32 s20, 442368\n\t"
"v_mfma_f32_16x16x32_bf16 a[8:11], a[228:231], a[188:191], a[8:11]\n\t"
"v_mfma_f32_16x16x32_bf16 a[8:11], a[232:235], a[192:195], a[8:11]\n\t"
"ds_read_b64_tr_b16 v[232:233], v246 offset:0\n\t"
"v_mfma_f32_16x16x32_bf16 a[16:19], a[224:227], a[188:191], a[16:19]\n\t"
"buffer_load_dwordx4 v[236:239], v252, s[8:11], s20 offen offset:0 sc1\n\t"
"v_mfma_f32_16x16x32_bf16 a[16:19], a[228:231], a[192:195], a[16:19]\n\t"
"s_waitcnt lgkmcnt(2)\n\t"
"ds_read_b64_tr_b16 v[234:235], v246 offset:3072\n\t"
"v_mfma_f32_16x16x32_bf16 a[16:19], a[232:235], a[196:199], a[16:19]\n\t"
"v_mfma_f32_16x16x32_bf16 a[24:27], a[224:227], a[192:195], a[24:27]\n\t"
"s_waitcnt lgkmcnt(2)\n\t"
"v_mfma_f32_16x16x32_bf16 a[24:27], a[228:231], a[196:199], a[24:27]\n\t"
"buffer_load_dwordx4 v[96:99], v252, s[8:11], s20 offen offset:1024 sc1\n\t"
"v_mfma_f32_16x16x32_bf16 a[24:27], a[232:235], a[200:203], a[24:27]\n\t"
"v_mfma_f32_16x16x32_bf16 a[32:35], a[224:227], a[196:199], a[32:35]\n\t"
"s_waitcnt vmcnt(13)\n\t"
"ds_read_b64_tr_b16 v[228:229], v246 offset:6144\n\t"
"v_mfma_f32_16x16x32_bf16 a[32:35], a[228:231], a[200:203], a[32:35]\n\t"
"ds_read_b64_tr_b16 v[230:231], v246 offset:9216\n\t"
"v_mfma_f32_16x16x32_bf16 a[32:35], a[232:235], a[204:207], a[32:35]\n\t"
"buffer_load_dwordx4 v[248:251], v252, s[8:11], s20 offen offset:2048 sc1\n\t"
"v_mfma_f32_16x16x32_bf16 a[40:43], a[236:239], a[168:171], a[40:43]\n\t"
"s_mov_b32 s20, 454656\n\t"
"v_mfma_f32_16x16x32_bf16 a[40:43], a[240:243], a[172:175], a[40:43]\n\t"
"v_mfma_f32_16x16x32_bf16 a[40:43], a[244:247], a[176:179], a[40:43]\n\t"
"v_mfma_f32_16x16x32_bf16 a[48:51], a[236:239], a[172:175], a[48:51]\n\t"
"buffer_load_dwordx4 a[212:215], v252, s[8:11], s20 offen offset:0 sc1\n\t"
"v_mfma_f32_16x16x32_bf16 a[48:51], a[240:243], a[176:179], a[48:51]\n\t"
"s_waitcnt lgkmcnt(2)\n\t"
"v_mfma_f32_16x16x32_bf16 a[48:51], a[244:247], a[180:183], a[48:51]\n\t"
"v_mfma_f32_16x16x32_bf16 v[232:235], v[232:235], v[220:223], 0\n\t"
"v_mfma_f32_16x16x32_bf16 a[56:59], a[236:239], a[176:179], a[56:59]\n\t"
"buffer_load_dwordx4 a[216:219], v252, s[8:11], s20 offen offset:1024 sc1\n\t"
"v_mfma_f32_16x16x32_bf16 a[56:59], a[240:243], a[180:183], a[56:59]\n\t"
"ds_read_b64_tr_b16 v[224:225], v246 offset:12288\n\t"
"v_mfma_f32_16x16x32_bf16 a[56:59], a[244:247], a[184:187], a[56:59]\n\t"
"ds_read_b64_tr_b16 v[226:227], v246 offset:15360\n\t"
"v_mfma_f32_16x16x32_bf16 a[64:67], a[236:239], a[180:183], a[64:67]\n\t"
"s_waitcnt lgkmcnt(2)\n\t"
"v_mfma_f32_16x16x32_bf16 a[64:67], a[240:243], a[184:187], a[64:67]\n\t"
"buffer_load_dwordx4 a[220:223], v252, s[8:11], s20 offen offset:2048 sc1\n\t"
"v_mfma_f32_16x16x32_bf16 a[64:67], a[244:247], a[188:191], a[64:67]\n\t"
"s_mov_b32 s20, 466944\n\t"
"v_cvt_pk_bf16_f32 v232, v232, v233\n\t"
"v_cvt_pk_bf16_f32 v233, v234, v235\n\t"
"v_mfma_f32_16x16x32_bf16 v[228:231], v[228:231], v[220:223], 0\n\t"
"v_mfma_f32_16x16x32_bf16 a[72:75], a[236:239], a[184:187], a[72:75]\n\t"
"v_mfma_f32_16x16x32_bf16 a[72:75], a[240:243], a[188:191], a[72:75]\n\t"
"buffer_load_dwordx4 a[224:227], v252, s[8:11], s20 offen offset:0 sc1\n\t"
"v_mfma_f32_16x16x32_bf16 a[72:75], a[244:247], a[192:195], a[72:75]\n\t"
"v_mfma_f32_16x16x32_bf16 a[80:83], a[236:239], a[188:191], a[80:83]\n\t"
"v_mfma_f32_16x16x32_bf16 a[80:83], a[240:243], a[192:195], a[80:83]\n\t"
"v_cvt_pk_bf16_f32 v234, v228, v229\n\t"
"v_cvt_pk_bf16_f32 v235, v230, v231\n\t"
"v_mfma_f32_16x16x32_bf16 a[80:83], a[244:247], a[196:199], a[80:83]\n\t"
"ds_read_b64_tr_b16 v[228:229], v246 offset:19968\n\t"
"v_mfma_f32_16x16x32_bf16 a[88:91], a[236:239], a[192:195], a[88:91]\n\t"
"buffer_load_dwordx4 a[228:231], v252, s[8:11], s20 offen offset:1024 sc1\n\t"
"v_mfma_f32_16x16x32_bf16 a[88:91], a[240:243], a[196:199], a[88:91]\n\t"
"s_waitcnt lgkmcnt(1)\n\t"
"ds_read_b64_tr_b16 v[230:231], v246 offset:23040\n\t"
"v_mfma_f32_16x16x32_bf16 a[88:91], a[244:247], a[200:203], a[88:91]\n\t"
"v_mfma_f32_16x16x32_bf16 v[224:227], v[224:227], v[220:223], 0\n\t"
"buffer_load_dwordx4 a[232:235], v252, s[8:11], s20 offen offset:2048 sc1\n\t"
"v_mfma_f32_16x16x32_bf16 a[96:99], a[236:239], a[196:199], a[96:99]\n\t"
"s_waitcnt vmcnt(9)\n\t"
"s_mov_b32 s20, 700416\n\t"
"v_mfma_f32_16x16x32_bf16 a[96:99], a[240:243], a[200:203], a[96:99]\n\t"
"v_mfma_f32_16x16x32_bf16 a[96:99], a[244:247], a[204:207], a[96:99]\n\t"
"v_mfma_f32_16x16x32_bf16 a[104:107], a[248:251], a[168:171], a[104:107]\n\t"
"buffer_load_dwordx4 a[236:239], v252, s[8:11], s20 offen offset:0 sc1\n\t"
"v_mfma_f32_16x16x32_bf16 a[104:107], v[240:243], a[172:175], a[104:107]\n\t"
"ds_read_b128 a[168:171], v244 offset:33792\n\t"
"v_cvt_pk_bf16_f32 v224, v224, v225\n\t"
"v_mfma_f32_16x16x32_bf16 a[104:107], a[208:211], a[176:179], a[104:107]\n\t"
"v_cvt_pk_bf16_f32 v225, v226, v227\n\t"
"v_mfma_f32_16x16x32_bf16 a[112:115], a[248:251], a[172:175], a[112:115]\n\t"
"buffer_load_dwordx4 a[240:243], v252, s[8:11], s20 offen offset:1024 sc1\n\t"
"v_mfma_f32_16x16x32_bf16 a[112:115], v[240:243], a[176:179], a[112:115]\n\t"
"s_waitcnt lgkmcnt(1)\n\t"
"ds_read_b128 a[172:175], v244 offset:34304\n\t"
"v_mfma_f32_16x16x32_bf16 a[112:115], a[208:211], a[180:183], a[112:115]\n\t"
"v_mfma_f32_16x16x32_bf16 v[228:231], v[228:231], v[220:223], 0\n\t"
"buffer_load_dwordx4 a[244:247], v252, s[8:11], s20 offen offset:2048 sc1\n\t"
"v_mfma_f32_16x16x32_bf16 a[120:123], a[248:251], a[176:179], a[120:123]\n\t"
"s_mov_b32 s20, 712704\n\t"
"ds_read_b128 a[176:179], v244 offset:34816\n\t"
"v_mfma_f32_16x16x32_bf16 a[120:123], v[240:243], a[180:183], a[120:123]\n\t"
"v_mfma_f32_16x16x32_bf16 a[120:123], a[208:211], a[184:187], a[120:123]\n\t"
"buffer_load_dwordx4 a[252:255], v252, s[8:11], s20 offen offset:0 sc1\n\t"
"v_mfma_f32_16x16x32_bf16 a[128:131], a[248:251], a[180:183], a[128:131]\n\t"
"ds_read_b128 a[180:183], v244 offset:35328\n\t"
"v_mfma_f32_16x16x32_bf16 a[128:131], v[240:243], a[184:187], a[128:131]\n\t"
"v_cvt_pk_bf16_f32 v226, v228, v229\n\t"
"v_cvt_pk_bf16_f32 v227, v230, v231\n\t"
"v_mfma_f32_16x16x32_bf16 a[128:131], a[208:211], a[188:191], a[128:131]\n\t"
"ds_read_b64_tr_b16 v[228:229], v246 offset:26112\n\t"
"v_mfma_f32_16x16x32_bf16 a[136:139], a[248:251], a[184:187], a[136:139]\n\t"
"ds_read_b64_tr_b16 v[230:231], v246 offset:29184\n\t"
"v_mfma_f32_16x16x32_bf16 a[136:139], v[240:243], a[188:191], a[136:139]\n\t"
"ds_read_b128 a[184:187], v244 offset:35840\n\t"
"v_mfma_f32_16x16x32_bf16 a[136:139], a[208:211], a[192:195], a[136:139]\n\t"
"v_mfma_f32_16x16x32_bf16 a[144:147], a[248:251], a[188:191], a[144:147]\n\t"
"ds_read_b128 a[188:191], v244 offset:36352\n\t"
"v_mfma_f32_16x16x32_bf16 a[144:147], v[240:243], a[192:195], a[144:147]\n\t"
"v_mfma_f32_16x16x32_bf16 a[144:147], a[208:211], a[196:199], a[144:147]\n\t"
"v_mfma_f32_16x16x32_bf16 a[152:155], a[248:251], a[192:195], a[152:155]\n\t"
"ds_read_b128 a[192:195], v244 offset:36864\n\t"
"v_mfma_f32_16x16x32_bf16 a[152:155], v[240:243], a[196:199], a[152:155]\n\t"
"v_mfma_f32_16x16x32_bf16 a[152:155], a[208:211], a[200:203], a[152:155]\n\t"
"v_mfma_f32_16x16x32_bf16 a[160:163], a[248:251], a[196:199], a[160:163]\n\t"
"buffer_load_dwordx4 a[248:251], v252, s[8:11], s20 offen offset:1024 sc1\n\t"
"v_mfma_f32_16x16x32_bf16 a[160:163], v[240:243], a[200:203], a[160:163]\n\t"
"s_waitcnt vmcnt(11) lgkmcnt(3)\n\t"
"ds_read_b128 a[196:199], v244 offset:37376\n\t"
"v_mfma_f32_16x16x32_bf16 a[160:163], a[208:211], a[204:207], a[160:163]\n\t"
"ds_read_b128 a[200:203], v244 offset:37888\n\t"
"v_mfma_f32_16x16x32_bf16 v[228:231], v[228:231], v[220:223], 0\n\t"
"ds_read_b128 a[204:207], v244 offset:38400\n\t"
"v_mfma_f32_16x16x32_bf16 v[200:203], v[236:239], a[168:171], v[200:203]\n\t"
"v_mfma_f32_16x16x32_bf16 v[200:203], v[96:99], a[172:175], v[200:203]\n\t"
"v_mfma_f32_16x16x32_bf16 v[200:203], v[248:251], a[176:179], v[200:203]\n\t"
"v_mfma_f32_16x16x32_bf16 v[208:211], v[236:239], a[172:175], v[208:211]\n\t"
"s_waitcnt lgkmcnt(5)\n\t"
"v_mfma_f32_16x16x32_bf16 v[208:211], v[96:99], a[176:179], v[208:211]\n\t"
"v_cvt_pk_bf16_f32 v228, v228, v229\n\t"
"v_mfma_f32_16x16x32_bf16 v[208:211], v[248:251], a[180:183], v[208:211]\n\t"
"s_waitcnt lgkmcnt(0)\n\t"
"s_barrier\n\t"
"v_mfma_f32_16x16x32_bf16 v[216:219], v[236:239], a[176:179], v[216:219]\n\t"
"v_cvt_pk_bf16_f32 v229, v230, v231\n\t"
"v_mfma_f32_16x16x32_bf16 v[216:219], v[96:99], a[180:183], v[216:219]\n\t"
"v_mfma_f32_16x16x32_bf16 v[216:219], v[248:251], a[184:187], v[216:219]\n\t"
"ds_write2st64_b64 v245, v[232:233], v[234:235] offset0:48 offset1:49\n\t"
"v_mfma_f32_16x16x32_bf16 a[4:7], v[236:239], a[180:183], a[4:7]\n\t"
"v_mfma_f32_16x16x32_bf16 a[4:7], v[96:99], a[184:187], a[4:7]\n\t"
"ds_read_b64_tr_b16 v[232:233], v246 offset:32256\n\t"
"v_mfma_f32_16x16x32_bf16 a[4:7], v[248:251], a[188:191], a[4:7]\n\t"
"ds_write2st64_b64 v245, v[224:225], v[226:227] offset0:50 offset1:51\n\t"
"v_mfma_f32_16x16x32_bf16 a[12:15], v[236:239], a[184:187], a[12:15]\n\t"
"v_mfma_f32_16x16x32_bf16 a[12:15], v[96:99], a[188:191], a[12:15]\n\t"
"ds_read_b64_tr_b16 v[234:235], v246 offset:35328\n\t"
"v_mfma_f32_16x16x32_bf16 a[12:15], v[248:251], a[192:195], a[12:15]\n\t"
"ds_write_b64 v245, v[228:229] offset:26624\n\t"
"v_mfma_f32_16x16x32_bf16 a[20:23], v[236:239], a[188:191], a[20:23]\n\t"
"ds_read_b64_tr_b16 v[224:225], v246 offset:39936\n\t"
"v_mfma_f32_16x16x32_bf16 a[20:23], v[96:99], a[192:195], a[20:23]\n\t"
"ds_read_b64_tr_b16 v[226:227], v246 offset:43008\n\t"
"v_mfma_f32_16x16x32_bf16 a[20:23], v[248:251], a[196:199], a[20:23]\n\t"
"s_waitcnt lgkmcnt(3)\n\t"
"ds_read_b64_tr_b16 v[228:229], v246 offset:46080\n\t"
"v_mfma_f32_16x16x32_bf16 v[232:235], v[232:235], v[220:223], 0\n\t"
"ds_read_b64_tr_b16 v[230:231], v246 offset:49152\n\t"
"v_mfma_f32_16x16x32_bf16 a[28:31], v[236:239], a[192:195], a[28:31]\n\t"
"global_load_dwordx4 v[240:243], v252, s[48:49] offset:2048\n\t"
"v_mfma_f32_16x16x32_bf16 a[28:31], v[96:99], a[196:199], a[28:31]\n\t"
"global_load_dwordx4 a[208:211], v252, s[46:47] offset:1024\n\t"
"v_mfma_f32_16x16x32_bf16 a[28:31], v[248:251], a[200:203], a[28:31]\n\t"
"s_waitcnt lgkmcnt(2)\n\t"
"v_mfma_f32_16x16x32_bf16 v[224:227], v[224:227], v[220:223], 0\n\t"
"v_mfma_f32_16x16x32_bf16 a[36:39], v[236:239], a[196:199], a[36:39]\n\t"
"s_waitcnt lgkmcnt(0)\n\t"
"v_cvt_pk_bf16_f32 v232, v232, v233\n\t"
"v_cvt_pk_bf16_f32 v233, v234, v235\n\t"
"v_mfma_f32_16x16x32_bf16 a[36:39], v[96:99], a[200:203], a[36:39]\n\t"
"s_waitcnt vmcnt(10)\n\t"
"v_mfma_f32_16x16x32_bf16 a[36:39], v[248:251], a[204:207], a[36:39]\n\t"
"buffer_load_dwordx4 v[96:99], v252, s[8:11], s20 offen offset:2048 sc1\n\t"
"v_mfma_f32_16x16x32_bf16 v[228:231], v[228:231], v[220:223], 0\n\t"
"s_mov_b32 s20, 724992\n\t"
"v_mfma_f32_16x16x32_bf16 a[44:47], a[212:215], a[168:171], a[44:47]\n\t"
"buffer_load_dwordx4 v[248:251], v252, s[8:11], s20 offen offset:0 sc1\n\t"
"v_mfma_f32_16x16x32_bf16 a[44:47], a[216:219], a[172:175], a[44:47]\n\t"
"v_cvt_pk_bf16_f32 v234, v224, v225\n\t"
"v_cvt_pk_bf16_f32 v235, v226, v227\n\t"
"v_mfma_f32_16x16x32_bf16 a[44:47], a[220:223], a[176:179], a[44:47]\n\t"
"ds_write2st64_b64 v245, v[232:233], v[234:235] offset0:53 offset1:54\n\t"
"v_mfma_f32_16x16x32_bf16 a[52:55], a[212:215], a[172:175], a[52:55]\n\t"
"v_mfma_f32_16x16x32_bf16 a[52:55], a[216:219], a[176:179], a[52:55]\n\t"
"ds_read_b64_tr_b16 v[224:225], v246 offset:52224\n\t"
"v_mfma_f32_16x16x32_bf16 a[52:55], a[220:223], a[180:183], a[52:55]\n\t"
"ds_read_b64_tr_b16 v[226:227], v246 offset:55296\n\t"
"v_cvt_pk_bf16_f32 v228, v228, v229\n\t"
"v_mfma_f32_16x16x32_bf16 a[60:63], a[212:215], a[176:179], a[60:63]\n\t"
"v_cvt_pk_bf16_f32 v229, v230, v231\n\t"
"v_mfma_f32_16x16x32_bf16 a[60:63], a[216:219], a[180:183], a[60:63]\n\t"
"v_mfma_f32_16x16x32_bf16 a[60:63], a[220:223], a[184:187], a[60:63]\n\t"
"v_mfma_f32_16x16x32_bf16 a[68:71], a[212:215], a[180:183], a[68:71]\n\t"
"v_mfma_f32_16x16x32_bf16 a[68:71], a[216:219], a[184:187], a[68:71]\n\t"
"v_mfma_f32_16x16x32_bf16 a[68:71], a[220:223], a[188:191], a[68:71]\n\t"
"v_mfma_f32_16x16x32_bf16 a[76:79], a[212:215], a[184:187], a[76:79]\n\t"
"s_waitcnt lgkmcnt(0)\n\t"
"v_mfma_f32_16x16x32_bf16 a[76:79], a[216:219], a[188:191], a[76:79]\n\t"
"v_mfma_f32_16x16x32_bf16 a[76:79], a[220:223], a[192:195], a[76:79]\n\t"
"v_mfma_f32_16x16x32_bf16 v[224:227], v[224:227], v[220:223], 0\n\t"
"v_mfma_f32_16x16x32_bf16 a[84:87], a[212:215], a[188:191], a[84:87]\n\t"
"v_mfma_f32_16x16x32_bf16 a[84:87], a[216:219], a[192:195], a[84:87]\n\t"
"v_mfma_f32_16x16x32_bf16 a[84:87], a[220:223], a[196:199], a[84:87]\n\t"
"v_mfma_f32_16x16x32_bf16 a[92:95], a[212:215], a[192:195], a[92:95]\n\t"
"v_mfma_f32_16x16x32_bf16 a[92:95], a[216:219], a[196:199], a[92:95]\n\t"
"v_cvt_pk_bf16_f32 v230, v224, v225\n\t"
"v_cvt_pk_bf16_f32 v231, v226, v227\n\t"
"v_mfma_f32_16x16x32_bf16 a[92:95], a[220:223], a[200:203], a[92:95]\n\t"
"ds_write2st64_b64 v245, v[228:229], v[230:231] offset0:55 offset1:56\n\t"
"v_mfma_f32_16x16x32_bf16 a[100:103], a[212:215], a[196:199], a[100:103]\n\t"
"buffer_load_dwordx4 a[212:215], v252, s[8:11], s20 offen offset:1024 sc1\n\t"
"v_mfma_f32_16x16x32_bf16 a[100:103], a[216:219], a[200:203], a[100:103]\n\t"
"s_waitcnt vmcnt(10)\n\t"
"v_mfma_f32_16x16x32_bf16 a[100:103], a[220:223], a[204:207], a[100:103]\n\t"
"v_mfma_f32_16x16x32_bf16 a[108:111], a[224:227], a[168:171], a[108:111]\n\t"
"buffer_load_dwordx4 a[216:219], v252, s[8:11], s20 offen offset:2048 sc1\n\t"
"v_mfma_f32_16x16x32_bf16 a[108:111], a[228:231], a[172:175], a[108:111]\n\t"
"s_mov_b32 s20, 737280\n\t"
"v_mfma_f32_16x16x32_bf16 a[108:111], a[232:235], a[176:179], a[108:111]\n\t"
"s_waitcnt lgkmcnt(0)\n\t"
"s_barrier\n\t"
"v_mfma_f32_16x16x32_bf16 a[116:119], a[224:227], a[172:175], a[116:119]\n\t"
"s_waitcnt vmcnt(5)\n\t"
"ds_read_b64_tr_b16 v[236:237], v240 offset:0\n\t"
"v_mfma_f32_16x16x32_bf16 a[116:119], a[228:231], a[176:179], a[116:119]\n\t"
"ds_read_b64_tr_b16 v[238:239], v240 offset:1536\n\t"
"v_mfma_f32_16x16x32_bf16 a[116:119], a[232:235], a[180:183], a[116:119]\n\t"
"ds_read_b64_tr_b16 v[232:233], v241 offset:0\n\t"
"v_mfma_f32_16x16x32_bf16 a[124:127], a[224:227], a[176:179], a[124:127]\n\t"
"ds_read_b64_tr_b16 v[234:235], v241 offset:1536\n\t"
"v_mfma_f32_16x16x32_bf16 a[124:127], a[228:231], a[180:183], a[124:127]\n\t"
"buffer_load_dwordx4 a[220:223], v252, s[8:11], s20 offen offset:0 sc1\n\t"
"v_mfma_f32_16x16x32_bf16 a[124:127], a[232:235], a[184:187], a[124:127]\n\t"
"ds_read_b64_tr_b16 v[224:225], v240 offset:19968\n\t"
"v_mfma_f32_16x16x32_bf16 a[132:135], a[224:227], a[180:183], a[132:135]\n\t"
"s_waitcnt vmcnt(5) lgkmcnt(3)\n\t"
"ds_read_b64_tr_b16 v[226:227], v240 offset:21504\n\t"
"v_mfma_f32_16x16x32_bf16 a[132:135], a[228:231], a[184:187], a[132:135]\n\t"
"ds_read_b64_tr_b16 v[228:229], v241 offset:19968\n\t"
"v_mfma_f32_16x16x32_bf16 a[132:135], a[232:235], a[188:191], a[132:135]\n\t"
"s_waitcnt lgkmcnt(3)\n\t"
"ds_read_b64_tr_b16 v[230:231], v241 offset:21504\n\t"
"v_mfma_f32_16x16x32_bf16 v[236:239], v[236:239], a[208:211], 0\n\t"
"ds_read_b128 a[168:171], v244 offset:24576\n\t"
"v_mfma_f32_16x16x32_bf16 v[232:235], v[232:235], a[208:211], 0\n\t"
"ds_read_b128 a[172:175], v244 offset:25088\n\t"
"v_mfma_f32_16x16x32_bf16 a[140:143], a[224:227], a[184:187], a[140:143]\n\t"
"ds_read_b128 a[176:179], v244 offset:25600\n\t"
"v_mfma_f32_16x16x32_bf16 a[140:143], a[228:231], a[188:191], a[140:143]\n\t"
"ds_read_b128 a[180:183], v244 offset:26112\n\t"
"v_mfma_f32_16x16x32_bf16 a[140:143], a[232:235], a[192:195], a[140:143]\n\t"
"ds_read_b128 a[184:187], v244 offset:26624\n\t"
"v_mfma_f32_16x16x32_bf16 a[148:151], a[224:227], a[188:191], a[148:151]\n\t"
"s_waitcnt lgkmcnt(7)\n\t"
"ds_read_b128 a[188:191], v244 offset:27136\n\t"
"v_cvt_pk_bf16_f32 v236, v236, v237\n\t"
"v_mfma_f32_16x16x32_bf16 a[148:151], a[228:231], a[192:195], a[148:151]\n\t"
"s_waitcnt lgkmcnt(6)\n\t"
"v_cvt_pk_bf16_f32 v237, v238, v239\n\t"
"v_cvt_pk_bf16_f32 v238, v232, v233\n\t"
"v_mfma_f32_16x16x32_bf16 a[148:151], a[232:235], a[196:199], a[148:151]\n\t"
"v_cvt_pk_bf16_f32 v239, v234, v235\n\t"
"v_mfma_f32_16x16x32_bf16 v[224:227], v[224:227], a[208:211], 0\n\t"
"ds_write_b128 v242, v[236:239] offset:0\n\t"
"v_mfma_f32_16x16x32_bf16 v[228:231], v[228:231], a[208:211], 0\n\t"
"v_mfma_f32_16x16x32_bf16 a[156:159], a[224:227], a[192:195], a[156:159]\n\t"
"ds_read_b128 a[192:195], v244 offset:27648\n\t"
"v_mfma_f32_16x16x32_bf16 a[156:159], a[228:231], a[196:199], a[156:159]\n\t"
"ds_read_b64_tr_b16 v[232:233], v240 offset:39936\n\t"
"v_mfma_f32_16x16x32_bf16 a[156:159], a[232:235], a[200:203], a[156:159]\n\t"
"ds_read_b64_tr_b16 v[234:235], v240 offset:41472\n\t"
"v_mfma_f32_16x16x32_bf16 a[164:167], a[224:227], a[196:199], a[164:167]\n\t"
"buffer_load_dwordx4 a[224:227], v252, s[8:11], s20 offen offset:1024 sc1\n\t"
"v_mfma_f32_16x16x32_bf16 a[164:167], a[228:231], a[200:203], a[164:167]\n\t"
"s_waitcnt lgkmcnt(7)\n\t"
"ds_read_b64_tr_b16 v[236:237], v241 offset:39936\n\t"
"v_cvt_pk_bf16_f32 v224, v224, v225\n\t"
"v_mfma_f32_16x16x32_bf16 a[164:167], a[232:235], a[204:207], a[164:167]\n\t"
"ds_read_b64_tr_b16 v[238:239], v241 offset:41472\n\t"
"v_cvt_pk_bf16_f32 v225, v226, v227\n\t"
"Lst24_%=:\n\t"
"v_mfma_f32_16x16x32_bf16 v[196:199], a[236:239], a[168:171], v[196:199]\n\t"
"s_waitcnt lgkmcnt(8)\n\t"
"ds_read_b128 a[196:199], v244 offset:28160\n\t"
"v_cvt_pk_bf16_f32 v226, v228, v229\n\t"
"v_mfma_f32_16x16x32_bf16 v[196:199], a[240:243], a[172:175], v[196:199]\n\t"
"ds_read_b128 a[200:203], v244 offset:28672\n\t"
"v_cvt_pk_bf16_f32 v227, v230, v231\n\t"
"v_mfma_f32_16x16x32_bf16 v[196:199], a[244:247], a[176:179], v[196:199]\n\t"
"ds_write_b128 v242, v[224:227] offset:1536\n\t"
"v_mfma_f32_16x16x32_bf16 v[204:207], a[236:239], a[172:175], v[204:207]\n\t"
"buffer_load_dwordx4 a[228:231], v252, s[8:11], s20 offen offset:2048 sc1\n\t"
"v_mfma_f32_16x16x32_bf16 v[204:207], a[240:243], a[176:179], v[204:207]\n\t"
"s_waitcnt lgkmcnt(10)\n\t"
"ds_read_b128 a[204:207], v244 offset:29184\n\t"
"v_mfma_f32_16x16x32_bf16 v[204:207], a[244:247], a[180:183], v[204:207]\n\t"
"s_mov_b32 s20, 749568\n\t"
"v_add_u32 v240, 59904, v240\n\t"
"v_add_u32 v241, 59904, v241\n\t"
"v_mfma_f32_16x16x32_bf16 v[212:215], a[236:239], a[176:179], v[212:215]\n\t"
"s_waitcnt lgkmcnt(6)\n\t"
"ds_read_b64_tr_b16 v[228:229], v240 offset:0\n\t"
"v_mfma_f32_16x16x32_bf16 v[212:215], a[240:243], a[180:183], v[212:215]\n\t"
"buffer_load_dwordx4 a[232:235], v252, s[8:11], s20 offen offset:0 sc1\n\t"
"v_mfma_f32_16x16x32_bf16 v[212:215], a[244:247], a[184:187], v[212:215]\n\t"
"s_waitcnt lgkmcnt(5)\n\t"
"ds_read_b64_tr_b16 v[230:231], v240 offset:1536\n\t"
"v_mfma_f32_16x16x32_bf16 v[232:235], v[232:235], a[208:211], 0\n\t"
"ds_read_b64_tr_b16 v[224:225], v241 offset:0\n\t"
"v_mfma_f32_16x16x32_bf16 v[236:239], v[236:239], a[208:211], 0\n\t"
"ds_read_b64_tr_b16 v[226:227], v241 offset:1536\n\t"
"v_mfma_f32_16x16x32_bf16 a[0:3], a[236:239], a[180:183], a[0:3]\n\t"
"v_mfma_f32_16x16x32_bf16 a[0:3], a[240:243], a[184:187], a[0:3]\n\t"
"v_mfma_f32_16x16x32_bf16 a[0:3], a[244:247], a[188:191], a[0:3]\n\t"
"v_mfma_f32_16x16x32_bf16 a[8:11], a[236:239], a[184:187], a[8:11]\n\t"
"s_waitcnt lgkmcnt(2)\n\t"
"v_cvt_pk_bf16_f32 v232, v232, v233\n\t"
"v_cvt_pk_bf16_f32 v233, v234, v235\n\t"
"v_mfma_f32_16x16x32_bf16 a[8:11], a[240:243], a[188:191], a[8:11]\n\t"
"s_waitcnt lgkmcnt(0)\n\t"
"v_cvt_pk_bf16_f32 v234, v236, v237\n\t"
"v_cvt_pk_bf16_f32 v235, v238, v239\n\t"
"v_mfma_f32_16x16x32_bf16 a[8:11], a[244:247], a[192:195], a[8:11]\n\t"
"ds_write_b128 v242, v[232:235] offset:3072\n\t"
"v_mfma_f32_16x16x32_bf16 v[228:231], v[228:231], a[208:211], 0\n\t"
"v_mfma_f32_16x16x32_bf16 v[224:227], v[224:227], a[208:211], 0\n\t"
"ds_read_b64_tr_b16 v[236:237], v240 offset:19968\n\t"
"v_mfma_f32_16x16x32_bf16 a[16:19], a[236:239], a[188:191], a[16:19]\n\t"
"ds_read_b64_tr_b16 v[238:239], v240 offset:21504\n\t"
"v_mfma_f32_16x16x32_bf16 a[16:19], a[240:243], a[192:195], a[16:19]\n\t"
"ds_read_b64_tr_b16 v[232:233], v241 offset:19968\n\t"
"v_mfma_f32_16x16x32_bf16 a[16:19], a[244:247], a[196:199], a[16:19]\n\t"
"ds_read_b64_tr_b16 v[234:235], v241 offset:21504\n\t"
"v_mfma_f32_16x16x32_bf16 a[24:27], a[236:239], a[192:195], a[24:27]\n\t"
"v_cvt_pk_bf16_f32 v228, v228, v229\n\t"
"v_cvt_pk_bf16_f32 v229, v230, v231\n\t"
"v_mfma_f32_16x16x32_bf16 a[24:27], a[240:243], a[196:199], a[24:27]\n\t"
"v_cvt_pk_bf16_f32 v230, v224, v225\n\t"
"v_cvt_pk_bf16_f32 v231, v226, v227\n\t"
"v_mfma_f32_16x16x32_bf16 a[24:27], a[244:247], a[200:203], a[24:27]\n\t"
"ds_write_b128 v242, v[228:231] offset:4608\n\t"
"v_mfma_f32_16x16x32_bf16 a[32:35], a[236:239], a[196:199], a[32:35]\n\t"
"buffer_load_dwordx4 a[236:239], v252, s[8:11], s20 offen offset:1024 sc1\n\t"
"v_mfma_f32_16x16x32_bf16 a[32:35], a[240:243], a[200:203], a[32:35]\n\t"
"s_waitcnt vmcnt(8)\n\t"
"ds_read_b64_tr_b16 v[224:225], v240 offset:39936\n\t"
"v_mfma_f32_16x16x32_bf16 a[32:35], a[244:247], a[204:207], a[32:35]\n\t"
"ds_read_b64_tr_b16 v[226:227], v240 offset:41472\n\t"
"v_mfma_f32_16x16x32_bf16 a[40:43], a[252:255], a[168:171], a[40:43]\n\t"
"buffer_load_dwordx4 a[240:243], v252, s[8:11], s20 offen offset:2048 sc1\n\t"
"v_mfma_f32_16x16x32_bf16 a[40:43], a[248:251], a[172:175], a[40:43]\n\t"
"s_mov_b32 s20, 761856\n\t"
"ds_read_b64_tr_b16 v[228:229], v241 offset:39936\n\t"
"v_mfma_f32_16x16x32_bf16 a[40:43], v[96:99], a[176:179], a[40:43]\n\t"
"ds_read_b64_tr_b16 v[230:231], v241 offset:41472\n\t"
"v_mfma_f32_16x16x32_bf16 a[48:51], a[252:255], a[172:175], a[48:51]\n\t"
"buffer_load_dwordx4 a[244:247], v252, s[8:11], s20 offen offset:0 sc1\n\t"
"v_mfma_f32_16x16x32_bf16 a[48:51], a[248:251], a[176:179], a[48:51]\n\t"
"s_waitcnt lgkmcnt(5)\n\t"
"v_mfma_f32_16x16x32_bf16 a[48:51], v[96:99], a[180:183], a[48:51]\n\t"
"s_waitcnt lgkmcnt(2)\n\t"
"v_mfma_f32_16x16x32_bf16 v[236:239], v[236:239], a[208:211], 0\n\t"
"v_mfma_f32_16x16x32_bf16 v[232:235], v[232:235], a[208:211], 0\n\t"
"s_waitcnt lgkmcnt(0)\n\t"
"v_xor_b32 v246, 0xc0, v246\n\t"
"v_mfma_f32_16x16x32_bf16 a[56:59], a[252:255], a[176:179], a[56:59]\n\t"
"Lpg18_%=:\n\t"
"s_cbranch_vccnz Lvr2_%=\n\t"
"v_mfma_f32_16x16x32_bf16 a[56:59], a[248:251], a[180:183], a[56:59]\n\t"
"v_mfma_f32_16x16x32_bf16 a[56:59], v[96:99], a[184:187], a[56:59]\n\t"
"v_mfma_f32_16x16x32_bf16 a[64:67], a[252:255], a[180:183], a[64:67]\n\t"
"v_cvt_pk_bf16_f32 v236, v236, v237\n\t"
"v_cvt_pk_bf16_f32 v237, v238, v239\n\t"
"v_mfma_f32_16x16x32_bf16 a[64:67], a[248:251], a[184:187], a[64:67]\n\t"
"v_cvt_pk_bf16_f32 v238, v232, v233\n\t"
"v_cvt_pk_bf16_f32 v239, v234, v235\n\t"
"v_mfma_f32_16x16x32_bf16 a[64:67], v[96:99], a[188:191], a[64:67]\n\t"
"ds_write_b128 v242, v[236:239] offset:6144\n\t"
"v_mfma_f32_16x16x32_bf16 v[224:227], v[224:227], a[208:211], 0\n\t"
"v_mfma_f32_16x16x32_bf16 v[228:231], v[228:231], a[208:211], 0\n\t"
"v_mfma_f32_16x16x32_bf16 a[72:75], a[252:255], a[184:187], a[72:75]\n\t"
"buffer_load_dwordx4 a[208:211], v252, s[8:11], s20 offen offset:1024 sc1\n\t"
"v_mfma_f32_16x16x32_bf16 a[72:75], a[248:251], a[188:191], a[72:75]\n\t"
"v_mfma_f32_16x16x32_bf16 a[72:75], v[96:99], a[192:195], a[72:75]\n\t"
"v_mfma_f32_16x16x32_bf16 a[80:83], a[252:255], a[188:191], a[80:83]\n\t"
"v_cvt_pk_bf16_f32 v224, v224, v225\n\t"
"v_cvt_pk_bf16_f32 v225, v226, v227\n\t"
"v_mfma_f32_16x16x32_bf16 a[80:83], a[248:251], a[192:195], a[80:83]\n\t"
"v_cvt_pk_bf16_f32 v226, v228, v229\n\t"
"v_cvt_pk_bf16_f32 v227, v230, v231\n\t"
"v_mfma_f32_16x16x32_bf16 a[80:83], v[96:99], a[196:199], a[80:83]\n\t"
"ds_write_b128 v242, v[224:227] offset:7680\n\t"
"v_mfma_f32_16x16x32_bf16 a[88:91], a[252:255], a[192:195], a[88:91]\n\t"
"v_mfma_f32_16x16x32_bf16 a[88:91], a[248:251], a[196:199], a[88:91]\n\t"
"ds_read_b64_tr_b16 v[232:233], v246 offset:0\n\t"
"v_mfma_f32_16x16x32_bf16 a[88:91], v[96:99], a[200:203], a[88:91]\n\t"
"ds_read_b64_tr_b16 v[234:235], v246 offset:3072\n\t"
"v_mfma_f32_16x16x32_bf16 a[96:99], a[252:255], a[196:199], a[96:99]\n\t"
"buffer_load_dwordx4 a[252:255], v252, s[8:11], s20 offen offset:2048 sc1\n\t"
"v_mfma_f32_16x16x32_bf16 a[96:99], a[248:251], a[200:203], a[96:99]\n\t"
"s_waitcnt vmcnt(9)\n\t"
"ds_read_b64_tr_b16 v[236:237], v246 offset:6144\n\t"
"s_mov_b32 s20, 626688\n\t"
"v_mfma_f32_16x16x32_bf16 a[96:99], v[96:99], a[204:207], a[96:99]\n\t"
"ds_read_b64_tr_b16 v[238:239], v246 offset:9216\n\t"
"v_mfma_f32_16x16x32_bf16 a[104:107], v[248:251], a[168:171], a[104:107]\n\t"
"buffer_load_dwordx4 v[240:243], v252, s[8:11], s20 offen offset:0 sc1\n\t"
"v_mfma_f32_16x16x32_bf16 a[104:107], a[212:215], a[172:175], a[104:107]\n\t"
"ds_read_b64_tr_b16 v[228:229], v246 offset:12288\n\t"
"v_mfma_f32_16x16x32_bf16 a[104:107], a[216:219], a[176:179], a[104:107]\n\t"
"ds_read_b64_tr_b16 v[230:231], v246 offset:15360\n\t"
"v_mfma_f32_16x16x32_bf16 a[112:115], v[248:251], a[172:175], a[112:115]\n\t"
"buffer_load_dwordx4 v[96:99], v252, s[8:11], s20 offen offset:1024 sc1\n\t"
"v_mfma_f32_16x16x32_bf16 a[112:115], a[212:215], a[176:179], a[112:115]\n\t"
"ds_read_b128 a[168:171], v244 offset:33792\n\t"
"v_mfma_f32_16x16x32_bf16 a[112:115], a[216:219], a[180:183], a[112:115]\n\t"
"ds_read_b64_tr_b16 v[224:225], v246 offset:19968\n\t"
"v_mfma_f32_16x16x32_bf16 a[120:123], v[248:251], a[176:179], a[120:123]\n\t"
"buffer_load_dwordx4 a[248:251], v252, s[8:11], s20 offen offset:2048 sc1\n\t"
"v_mfma_f32_16x16x32_bf16 a[120:123], a[212:215], a[180:183], a[120:123]\n\t"
"s_waitcnt lgkmcnt(6)\n\t"
"ds_read_b64_tr_b16 v[226:227], v246 offset:23040\n\t"
"v_mfma_f32_16x16x32_bf16 a[120:123], a[216:219], a[184:187], a[120:123]\n\t"
"ds_read_b128 a[172:175], v244 offset:34304\n\t"
"v_mfma_f32_16x16x32_bf16 v[232:235], v[232:235], v[220:223], 0\n\t"
"ds_read_b128 a[176:179], v244 offset:34816\n\t"
"v_mfma_f32_16x16x32_bf16 a[128:131], v[248:251], a[180:183], a[128:131]\n\t"
"s_waitcnt lgkmcnt(7)\n\t"
"ds_read_b128 a[180:183], v244 offset:35328\n\t"
"v_mfma_f32_16x16x32_bf16 a[128:131], a[212:215], a[184:187], a[128:131]\n\t"
"v_mfma_f32_16x16x32_bf16 a[128:131], a[216:219], a[188:191], a[128:131]\n\t"
"v_mfma_f32_16x16x32_bf16 v[236:239], v[236:239], v[220:223], 0\n\t"
"v_mfma_f32_16x16x32_bf16 a[136:139], v[248:251], a[184:187], a[136:139]\n\t"
"s_waitcnt lgkmcnt(6)\n\t"
"ds_read_b128 a[184:187], v244 offset:35840\n\t"
"v_cvt_pk_bf16_f32 v232, v232, v233\n\t"
"v_mfma_f32_16x16x32_bf16 a[136:139], a[212:215], a[188:191], a[136:139]\n\t"
"v_cvt_pk_bf16_f32 v233, v234, v235\n\t"
"v_mfma_f32_16x16x32_bf16 a[136:139], a[216:219], a[192:195], a[136:139]\n\t"
"s_mov_b32 s20, 638976\n\t"
"v_mfma_f32_16x16x32_bf16 v[228:231], v[228:231], v[220:223], 0\n\t"
"v_mfma_f32_16x16x32_bf16 a[144:147], v[248:251], a[188:191], a[144:147]\n\t"
"s_waitcnt lgkmcnt(4)\n\t"
"ds_read_b128 a[188:191], v244 offset:36352\n\t"
"v_cvt_pk_bf16_f32 v234, v236, v237\n\t"
"v_mfma_f32_16x16x32_bf16 a[144:147], a[212:215], a[192:195], a[144:147]\n\t"
"v_cvt_pk_bf16_f32 v235, v238, v239\n\t"
"v_mfma_f32_16x16x32_bf16 a[144:147], a[216:219], a[196:199], a[144:147]\n\t"
"v_mfma_f32_16x16x32_bf16 v[224:227], v[224:227], v[220:223], 0\n\t"
"v_mfma_f32_16x16x32_bf16 a[152:155], v[248:251], a[192:195], a[152:155]\n\t"
"buffer_load_dwordx4 v[236:239], v252, s[8:11], s20 offen offset:0 sc1\n\t"
"v_mfma_f32_16x16x32_bf16 a[152:155], a[212:215], a[196:199], a[152:155]\n\t"
"ds_read_b128 a[192:195], v244 offset:36864\n\t"
"v_cvt_pk_bf16_f32 v228, v228, v229\n\t"
"v_mfma_f32_16x16x32_bf16 a[152:155], a[216:219], a[200:203], a[152:155]\n\t"
"v_cvt_pk_bf16_f32 v229, v230, v231\n\t"
"v_mfma_f32_16x16x32_bf16 a[160:163], v[248:251], a[196:199], a[160:163]\n\t"
"buffer_load_dwordx4 v[248:251], v252, s[8:11], s20 offen offset:1024 sc1\n\t"
"v_mfma_f32_16x16x32_bf16 a[160:163], a[212:215], a[200:203], a[160:163]\n\t"
"s_waitcnt vmcnt(11) lgkmcnt(4)\n\t"
"ds_read_b128 a[196:199], v244 offset:37376\n\t"
"v_cvt_pk_bf16_f32 v230, v224, v225\n\t"
"v_mfma_f32_16x16x32_bf16 a[160:163], a[216:219], a[204:207], a[160:163]\n\t"
"ds_read_b128 a[200:203], v244 offset:37888\n\t"
"v_cvt_pk_bf16_f32 v231, v226, v227\n\t"
"v_mfma_f32_16x16x32_bf16 v[200:203], a[220:223], a[168:171], v[200:203]\n\t"
"ds_read_b64_tr_b16 v[224:225], v246 offset:26112\n\t"
"v_mfma_f32_16x16x32_bf16 v[200:203], a[224:227], a[172:175], v[200:203]\n\t"
"s_waitcnt lgkmcnt(6)\n\t"
"ds_read_b64_tr_b16 v[226:227], v246 offset:29184\n\t"
"v_mfma_f32_16x16x32_bf16 v[200:203], a[228:231], a[176:179], v[200:203]\n\t"
"s_waitcnt lgkmcnt(0)\n\t"
"ds_read_b128 a[204:207], v244 offset:38400\n\t"
"v_mfma_f32_16x16x32_bf16 v[224:227], v[224:227], v[220:223], 0\n\t"
"buffer_load_dwordx4 a[212:215], v252, s[8:11], s20 offen offset:2048 sc1\n\t"
"s_mov_b32 s20, 651264\n\t"
"v_mfma_f32_16x16x32_bf16 v[208:211], a[220:223], a[172:175], v[208:211]\n\t"
"buffer_load_dwordx4 a[216:219], v252, s[8:11], s20 offen offset:0 sc1\n\t"
"v_mfma_f32_16x16x32_bf16 v[208:211], a[224:227], a[176:179], v[208:211]\n\t"
"s_add_u32 m0, s24, 768\n\t"
"s_add_u32 s28, s21, 0\n\t"
"v_mfma_f32_16x16x32_bf16 v[208:211], a[228:231], a[180:183], v[208:211]\n\t"
"s_waitcnt lgkmcnt(0)\n\t"
"s_barrier\n\t"
"v_mfma_f32_16x16x32_bf16 v[216:219], a[220:223], a[176:179], v[216:219]\n\t"
"v_mfma_f32_16x16x32_bf16 v[216:219], a[224:227], a[180:183], v[216:219]\n\t"
"buffer_load_dwordx4 v247, s[12:15], s28 offen offset:256 nt lds\n\t"
"v_mfma_f32_16x16x32_bf16 v[216:219], a[228:231], a[184:187], v[216:219]\n\t"
"s_add_u32 m0, s24, 6912\n\t"
"s_add_u32 s28, s21, 196608\n\t"
"ds_write2st64_b64 v245, v[232:233], v[234:235] offset0:48 offset1:49\n\t"
"v_mfma_f32_16x16x32_bf16 a[4:7], a[220:223], a[180:183], a[4:7]\n\t"
"buffer_load_dwordx4 v247, s[12:15], s28 offen offset:256 nt lds\n\t"
"v_mfma_f32_16x16x32_bf16 a[4:7], a[224:227], a[184:187], a[4:7]\n\t"
"s_add_u32 m0, s24, 13056\n\t"
"s_add_u32 s28, s21, 393216\n\t"
"ds_read_b64_tr_b16 v[232:233], v246 offset:32256\n\t"
"v_mfma_f32_16x16x32_bf16 a[4:7], a[228:231], a[188:191], a[4:7]\n\t"
"ds_write2st64_b64 v245, v[228:229], v[230:231] offset0:50 offset1:51\n\t"
"v_cvt_pk_bf16_f32 v224, v224, v225\n\t"
"v_cvt_pk_bf16_f32 v225, v226, v227\n\t"
"v_mfma_f32_16x16x32_bf16 a[12:15], a[220:223], a[184:187], a[12:15]\n\t"
"v_mfma_f32_16x16x32_bf16 a[12:15], a[224:227], a[188:191], a[12:15]\n\t"
"ds_read_b64_tr_b16 v[234:235], v246 offset:35328\n\t"
"v_mfma_f32_16x16x32_bf16 a[12:15], a[228:231], a[192:195], a[12:15]\n\t"
"buffer_load_dwordx4 v247, s[12:15], s28 offen offset:256 nt lds\n\t"
"s_add_u32 m0, s24, 20736\n\t"
"s_add_u32 s28, s21, 589824\n\t"
"v_mfma_f32_16x16x32_bf16 a[20:23], a[220:223], a[188:191], a[20:23]\n\t"
"buffer_load_dwordx4 v247, s[12:15], s28 offen offset:256 nt lds\n\t"
"v_mfma_f32_16x16x32_bf16 a[20:23], a[224:227], a[192:195], a[20:23]\n\t"
"s_add_u32 m0, s24, 60672\n\t"
"s_add_u32 s28, s21, 1769472\n\t"
"ds_read_b64_tr_b16 v[228:229], v246 offset:39936\n\t"
"v_mfma_f32_16x16x32_bf16 a[20:23], a[228:231], a[196:199], a[20:23]\n\t"
"s_waitcnt lgkmcnt(1)\n\t"
"ds_write_b64 v245, v[224:225] offset:26624\n\t"
"ds_read_b64_tr_b16 v[230:231], v246 offset:43008\n\t"
"v_mfma_f32_16x16x32_bf16 v[232:235], v[232:235], v[220:223], 0\n\t"
"buffer_load_dwordx4 v247, s[12:15], s28 offen offset:256 nt lds\n\t"
"s_add_u32 m0, s24, 66816\n\t"
"s_add_u32 s28, s21, 1966080\n\t"
"v_mfma_f32_16x16x32_bf16 a[28:31], a[220:223], a[192:195], a[28:31]\n\t"
"buffer_load_dwordx4 v247, s[12:15], s28 offen offset:256 nt lds\n\t"
"v_mfma_f32_16x16x32_bf16 a[28:31], a[224:227], a[196:199], a[28:31]\n\t"
"ds_read_b64_tr_b16 v[224:225], v246 offset:46080\n\t"
"v_mfma_f32_16x16x32_bf16 a[28:31], a[228:231], a[200:203], a[28:31]\n\t"
"s_waitcnt lgkmcnt(1)\n\t"
"ds_read_b64_tr_b16 v[226:227], v246 offset:49152\n\t"
"v_mfma_f32_16x16x32_bf16 v[228:231], v[228:231], v[220:223], 0\n\t"
"v_mfma_f32_16x16x32_bf16 a[36:39], a[220:223], a[196:199], a[36:39]\n\t"
"v_cvt_pk_bf16_f32 v232, v232, v233\n\t"
"v_cvt_pk_bf16_f32 v233, v234, v235\n\t"
"v_mfma_f32_16x16x32_bf16 a[36:39], a[224:227], a[200:203], a[36:39]\n\t"
"s_waitcnt vmcnt(16)\n\t"
"v_mfma_f32_16x16x32_bf16 a[36:39], a[228:231], a[204:207], a[36:39]\n\t"
"s_waitcnt lgkmcnt(0)\n\t"
"v_mfma_f32_16x16x32_bf16 v[224:227], v[224:227], v[220:223], 0\n\t"
"v_mfma_f32_16x16x32_bf16 a[44:47], a[232:235], a[168:171], a[44:47]\n\t"
"v_cvt_pk_bf16_f32 v234, v228, v229\n\t"
"v_cvt_pk_bf16_f32 v235, v230, v231\n\t"
"v_mfma_f32_16x16x32_bf16 a[44:47], a[236:239], a[172:175], a[44:47]\n\t"
"ds_read_b64_tr_b16 v[228:229], v246 offset:52224\n\t"
"v_mfma_f32_16x16x32_bf16 a[44:47], a[240:243], a[176:179], a[44:47]\n\t"
"ds_write2st64_b64 v245, v[232:233], v[234:235] offset0:53 offset1:54\n\t"
"v_mfma_f32_16x16x32_bf16 a[52:55], a[232:235], a[172:175], a[52:55]\n\t"
"v_mfma_f32_16x16x32_bf16 a[52:55], a[236:239], a[176:179], a[52:55]\n\t"
"ds_read_b64_tr_b16 v[230:231], v246 offset:55296\n\t"
"v_mfma_f32_16x16x32_bf16 a[52:55], a[240:243], a[180:183], a[52:55]\n\t"
"v_cvt_pk_bf16_f32 v224, v224, v225\n\t"
"v_cvt_pk_bf16_f32 v225, v226, v227\n\t"
"v_mfma_f32_16x16x32_bf16 a[60:63], a[232:235], a[176:179], a[60:63]\n\t"
"v_mfma_f32_16x16x32_bf16 a[60:63], a[236:239], a[180:183], a[60:63]\n\t"
"v_mfma_f32_16x16x32_bf16 a[60:63], a[240:243], a[184:187], a[60:63]\n\t"
"v_mfma_f32_16x16x32_bf16 a[68:71], a[232:235], a[180:183], a[68:71]\n\t"
"v_mfma_f32_16x16x32_bf16 a[68:71], a[236:239], a[184:187], a[68:71]\n\t"
"v_mfma_f32_16x16x32_bf16 a[68:71], a[240:243], a[188:191], a[68:71]\n\t"
"v_mfma_f32_16x16x32_bf16 a[76:79], a[232:235], a[184:187], a[76:79]\n\t"
"s_waitcnt lgkmcnt(0)\n\t"
"v_mfma_f32_16x16x32_bf16 a[76:79], a[236:239], a[188:191], a[76:79]\n\t"
"v_mfma_f32_16x16x32_bf16 a[76:79], a[240:243], a[192:195], a[76:79]\n\t"
"v_mfma_f32_16x16x32_bf16 v[228:231], v[228:231], v[220:223], 0\n\t"
"v_mfma_f32_16x16x32_bf16 a[84:87], a[232:235], a[188:191], a[84:87]\n\t"
"buffer_load_dwordx4 a[220:223], v252, s[8:11], s20 offen offset:1024 sc1\n\t"
"v_mfma_f32_16x16x32_bf16 a[84:87], a[236:239], a[192:195], a[84:87]\n\t"
"v_mfma_f32_16x16x32_bf16 a[84:87], a[240:243], a[196:199], a[84:87]\n\t"
"v_mfma_f32_16x16x32_bf16 a[92:95], a[232:235], a[192:195], a[92:95]\n\t"
"v_mfma_f32_16x16x32_bf16 a[92:95], a[236:239], a[196:199], a[92:95]\n\t"
"v_cvt_pk_bf16_f32 v226, v228, v229\n\t"
"v_cvt_pk_bf16_f32 v227, v230, v231\n\t"
"v_mfma_f32_16x16x32_bf16 a[92:95], a[240:243], a[200:203], a[92:95]\n\t"
"ds_write2st64_b64 v245, v[224:225], v[226:227] offset0:55 offset1:56\n\t"
"v_mfma_f32_16x16x32_bf16 a[100:103], a[232:235], a[196:199], a[100:103]\n\t"
"s_waitcnt vmcnt(14)\n\t"
"v_mfma_f32_16x16x32_bf16 a[100:103], a[236:239], a[200:203], a[100:103]\n\t"
"v_mfma_f32_16x16x32_bf16 a[100:103], a[240:243], a[204:207], a[100:103]\n\t"
"v_mfma_f32_16x16x32_bf16 a[108:111], a[244:247], a[168:171], a[108:111]\n\t"
"v_mfma_f32_16x16x32_bf16 a[108:111], a[208:211], a[172:175], a[108:111]\n\t"
"v_mfma_f32_16x16x32_bf16 a[108:111], a[252:255], a[176:179], a[108:111]\n\t"
"s_waitcnt lgkmcnt(0)\n\t"
"s_barrier\n\t"
"v_mfma_f32_16x16x32_bf16 a[116:119], a[244:247], a[172:175], a[116:119]\n\t"
"ds_read_b128 a[168:171], v244 offset:24576\n\t"
"v_mfma_f32_16x16x32_bf16 a[116:119], a[208:211], a[176:179], a[116:119]\n\t"
"ds_read_b128 a[172:175], v244 offset:25088\n\t"
"v_mfma_f32_16x16x32_bf16 a[116:119], a[252:255], a[180:183], a[116:119]\n\t"
"v_mfma_f32_16x16x32_bf16 a[124:127], a[244:247], a[176:179], a[124:127]\n\t"
"ds_read_b128 a[176:179], v244 offset:25600\n\t"
"v_mfma_f32_16x16x32_bf16 a[124:127], a[208:211], a[180:183], a[124:127]\n\t"
"v_mfma_f32_16x16x32_bf16 a[124:127], a[252:255], a[184:187], a[124:127]\n\t"
"v_mfma_f32_16x16x32_bf16 a[132:135], a[244:247], a[180:183], a[132:135]\n\t"
"ds_read_b128 a[180:183], v244 offset:26112\n\t"
"v_mfma_f32_16x16x32_bf16 a[132:135], a[208:211], a[184:187], a[132:135]\n\t"
"v_mfma_f32_16x16x32_bf16 a[132:135], a[252:255], a[188:191], a[132:135]\n\t"
"v_mfma_f32_16x16x32_bf16 a[140:143], a[244:247], a[184:187], a[140:143]\n\t"
"ds_read_b128 a[184:187], v244 offset:26624\n\t"
"v_mfma_f32_16x16x32_bf16 a[140:143], a[208:211], a[188:191], a[140:143]\n\t"
"v_mfma_f32_16x16x32_bf16 a[140:143], a[252:255], a[192:195], a[140:143]\n\t"
"v_mfma_f32_16x16x32_bf16 a[148:151], a[244:247], a[188:191], a[148:151]\n\t"
"ds_read_b128 a[188:191], v244 offset:27136\n\t"
"v_mfma_f32_16x16x32_bf16 a[148:151], a[208:211], a[192:195], a[148:151]\n\t"
"v_mfma_f32_16x16x32_bf16 a[148:151], a[252:255], a[196:199], a[148:151]\n\t"
"v_mfma_f32_16x16x32_bf16 a[156:159], a[244:247], a[192:195], a[156:159]\n\t"
"ds_read_b128 a[192:195], v244 offset:27648\n\t"
"v_mfma_f32_16x16x32_bf16 a[156:159], a[208:211], a[196:199], a[156:159]\n\t"
"v_mfma_f32_16x16x32_bf16 a[156:159], a[252:255], a[200:203], a[156:159]\n\t"
"v_mfma_f32_16x16x32_bf16 a[164:167], a[244:247], a[196:199], a[164:167]\n\t"
"s_waitcnt vmcnt(11) lgkmcnt(4)\n\t"
"ds_read_b128 a[196:199], v244 offset:28160\n\t"
"v_mfma_f32_16x16x32_bf16 a[164:167], a[208:211], a[200:203], a[164:167]\n\t"
"ds_read_b128 a[200:203], v244 offset:28672\n\t"
"v_mfma_f32_16x16x32_bf16 a[164:167], a[252:255], a[204:207], a[164:167]\n\t"
"ds_read_b128 a[204:207], v244 offset:29184\n\t"
"Lst25_%=:\n\t"
"v_mfma_f32_16x16x32_bf16 v[0:3], v[240:243], a[168:171], v[0:3]\n\t"
"buffer_load_dwordx4 a[208:211], v252, s[8:11], s20 offen offset:2048 sc1\n\t"
"v_mfma_f32_16x16x32_bf16 v[0:3], v[96:99], a[172:175], v[0:3]\n\t"
"s_waitcnt lgkmcnt(6)\n\t"
"s_mov_b32 s20, 663552\n\t"
"v_mfma_f32_16x16x32_bf16 v[0:3], a[248:251], a[176:179], v[0:3]\n\t"
"v_mfma_f32_16x16x32_bf16 v[4:7], v[240:243], a[172:175], v[4:7]\n\t"
"s_waitcnt lgkmcnt(5)\n\t"
"v_mfma_f32_16x16x32_bf16 v[4:7], v[96:99], a[176:179], v[4:7]\n\t"
"buffer_load_dwordx4 a[224:227], v252, s[8:11], s20 offen offset:0 sc1\n\t"
"v_mfma_f32_16x16x32_bf16 v[4:7], a[248:251], a[180:183], v[4:7]\n\t"
"v_mfma_f32_16x16x32_bf16 v[8:11], v[240:243], a[176:179], v[8:11]\n\t"
"s_waitcnt lgkmcnt(4)\n\t"
"v_mfma_f32_16x16x32_bf16 v[8:11], v[96:99], a[180:183], v[8:11]\n\t"
"v_mfma_f32_16x16x32_bf16 v[8:11], a[248:251], a[184:187], v[8:11]\n\t"
"buffer_load_dwordx4 a[228:231], v252, s[8:11], s20 offen offset:1024 sc1\n\t"
"v_mfma_f32_16x16x32_bf16 v[12:15], v[240:243], a[180:183], v[12:15]\n\t"
"s_waitcnt lgkmcnt(3)\n\t"
"v_mfma_f32_16x16x32_bf16 v[12:15], v[96:99], a[184:187], v[12:15]\n\t"
"v_mfma_f32_16x16x32_bf16 v[12:15], a[248:251], a[188:191], v[12:15]\n\t"
"v_mfma_f32_16x16x32_bf16 v[16:19], v[240:243], a[184:187], v[16:19]\n\t"
"buffer_load_dwordx4 a[232:235], v252, s[8:11], s20 offen offset:2048 sc1\n\t"
"v_mfma_f32_16x16x32_bf16 v[16:19], v[96:99], a[188:191], v[16:19]\n\t"
"s_waitcnt lgkmcnt(2)\n\t"
"s_mov_b32 s20, 675840\n\t"
"v_mfma_f32_16x16x32_bf16 v[16:19], a[248:251], a[192:195], v[16:19]\n\t"
"ds_read_b64_tr_b16 v[232:233], v246 offset:512\n\t"
"v_mfma_f32_16x16x32_bf16 v[20:23], v[240:243], a[188:191], v[20:23]\n\t"
"s_waitcnt lgkmcnt(2)\n\t"
"ds_read_b64_tr_b16 v[234:235], v246 offset:3584\n\t"
"v_mfma_f32_16x16x32_bf16 v[20:23], v[96:99], a[192:195], v[20:23]\n\t"
"buffer_load_dwordx4 a[236:239], v252, s[8:11], s20 offen offset:0 sc1\n\t"
"v_mfma_f32_16x16x32_bf16 v[20:23], a[248:251], a[196:199], v[20:23]\n\t"
"v_mfma_f32_16x16x32_bf16 v[24:27], v[240:243], a[192:195], v[24:27]\n\t"
"s_waitcnt lgkmcnt(2)\n\t"
"v_mfma_f32_16x16x32_bf16 v[24:27], v[96:99], a[196:199], v[24:27]\n\t"
"v_mfma_f32_16x16x32_bf16 v[24:27], a[248:251], a[200:203], v[24:27]\n\t"
"buffer_load_dwordx4 a[240:243], v252, s[8:11], s20 offen offset:1024 sc1\n\t"
"v_mfma_f32_16x16x32_bf16 v[28:31], v[240:243], a[196:199], v[28:31]\n\t"
"s_waitcnt vmcnt(14)\n\t"
"ds_read_b64_tr_b16 v[228:229], v246 offset:6656\n\t"
"v_mfma_f32_16x16x32_bf16 v[28:31], v[96:99], a[200:203], v[28:31]\n\t"
"ds_read_b64_tr_b16 v[230:231], v246 offset:9728\n\t"
"v_mfma_f32_16x16x32_bf16 v[28:31], a[248:251], a[204:207], v[28:31]\n\t"
"v_mfma_f32_16x16x32_bf16 v[32:35], v[236:239], a[168:171], v[32:35]\n\t"
"buffer_load_dwordx4 v[240:243], v252, s[8:11], s20 offen offset:2048 sc1\n\t"
"v_mfma_f32_16x16x32_bf16 v[32:35], v[248:251], a[172:175], v[32:35]\n\t"
"s_mov_b32 s20, 688128\n\t"
"v_mfma_f32_16x16x32_bf16 v[32:35], a[212:215], a[176:179], v[32:35]\n\t"
"v_mfma_f32_16x16x32_bf16 v[36:39], v[236:239], a[172:175], v[36:39]\n\t"
"s_waitcnt lgkmcnt(2)\n\t"
"v_mfma_f32_16x16x32_bf16 v[36:39], v[248:251], a[176:179], v[36:39]\n\t"
"buffer_load_dwordx4 v[96:99], v252, s[8:11], s20 offen offset:0 sc1\n\t"
"v_mfma_f32_16x16x32_bf16 v[36:39], a[212:215], a[180:183], v[36:39]\n\t"
"v_mfma_f32_16x16x32_bf16 v[232:235], v[232:235], v[220:223], 0\n\t"
"v_mfma_f32_16x16x32_bf16 v[40:43], v[236:239], a[176:179], v[40:43]\n\t"
"ds_read_b64_tr_b16 v[224:225], v246 offset:12800\n\t"
"v_mfma_f32_16x16x32_bf16 v[40:43], v[248:251], a[180:183], v[40:43]\n\t"
"buffer_load_dwordx4 a[244:247], v252, s[8:11], s20 offen offset:1024 sc1\n\t"
"v_mfma_f32_16x16x32_bf16 v[40:43], a[212:215], a[184:187], v[40:43]\n\t"
"ds_read_b64_tr_b16 v[226:227], v246 offset:15872\n\t"
"v_mfma_f32_16x16x32_bf16 v[44:47], v[236:239], a[180:183], v[44:47]\n\t"
"s_waitcnt lgkmcnt(2)\n\t"
"v_mfma_f32_16x16x32_bf16 v[44:47], v[248:251], a[184:187], v[44:47]\n\t"
"buffer_load_dwordx4 a[248:251], v252, s[8:11], s20 offen offset:2048 sc1\n\t"
"v_mfma_f32_16x16x32_bf16 v[44:47], a[212:215], a[188:191], v[44:47]\n\t"
"s_mov_b32 s20, 921600\n\t"
"v_cvt_pk_bf16_f32 v232, v232, v233\n\t"
"v_cvt_pk_bf16_f32 v233, v234, v235\n\t"
"v_mfma_f32_16x16x32_bf16 v[228:231], v[228:231], v[220:223], 0\n\t"
"v_mfma_f32_16x16x32_bf16 v[48:51], v[236:239], a[184:187], v[48:51]\n\t"
"v_mfma_f32_16x16x32_bf16 v[48:51], v[248:251], a[188:191], v[48:51]\n\t"
"buffer_load_dwordx4 a[252:255], v252, s[8:11], s20 offen offset:0 sc1\n\t"
"v_mfma_f32_16x16x32_bf16 v[48:51], a[212:215], a[192:195], v[48:51]\n\t"
"v_mfma_f32_16x16x32_bf16 v[52:55], v[236:239], a[188:191], v[52:55]\n\t"
"v_mfma_f32_16x16x32_bf16 v[52:55], v[248:251], a[192:195], v[52:55]\n\t"
"v_cvt_pk_bf16_f32 v234, v228, v229\n\t"
"v_cvt_pk_bf16_f32 v235, v230, v231\n\t"
"v_mfma_f32_16x16x32_bf16 v[52:55], a[212:215], a[196:199], v[52:55]\n\t"
"ds_read_b64_tr_b16 v[228:229], v246 offset:20480\n\t"
"v_mfma_f32_16x16x32_bf16 v[56:59], v[236:239], a[192:195], v[56:59]\n\t"
"s_waitcnt lgkmcnt(1)\n\t"
"ds_read_b64_tr_b16 v[230:231], v246 offset:23552\n\t"
"v_mfma_f32_16x16x32_bf16 v[56:59], v[248:251], a[196:199], v[56:59]\n\t"
"v_mfma_f32_16x16x32_bf16 v[56:59], a[212:215], a[200:203], v[56:59]\n\t"
"v_mfma_f32_16x16x32_bf16 v[224:227], v[224:227], v[220:223], 0\n\t"
"v_mfma_f32_16x16x32_bf16 v[60:63], v[236:239], a[196:199], v[60:63]\n\t"
"buffer_load_dwordx4 v[236:239], v252, s[8:11], s20 offen offset:1024 sc1\n\t"
"v_mfma_f32_16x16x32_bf16 v[60:63], v[248:251], a[200:203], v[60:63]\n\t"
"s_waitcnt vmcnt(11)\n\t"
"v_mfma_f32_16x16x32_bf16 v[60:63], a[212:215], a[204:207], v[60:63]\n\t"
"v_mfma_f32_16x16x32_bf16 v[64:67], a[216:219], a[168:171], v[64:67]\n\t"
"buffer_load_dwordx4 v[248:251], v252, s[8:11], s20 offen offset:2048 sc1\n\t"
"v_mfma_f32_16x16x32_bf16 v[64:67], a[220:223], a[172:175], v[64:67]\n\t"
"s_mov_b32 s20, 933888\n\t"
"ds_read_b128 a[168:171], v244 offset:33792\n\t"
"v_cvt_pk_bf16_f32 v224, v224, v225\n\t"
"v_mfma_f32_16x16x32_bf16 v[64:67], a[208:211], a[176:179], v[64:67]\n\t"
"v_cvt_pk_bf16_f32 v225, v226, v227\n\t"
"v_mfma_f32_16x16x32_bf16 v[68:71], a[216:219], a[172:175], v[68:71]\n\t"
"buffer_load_dwordx4 a[212:215], v252, s[8:11], s20 offen offset:0 sc1\n\t"
"v_mfma_f32_16x16x32_bf16 v[68:71], a[220:223], a[176:179], v[68:71]\n\t"
"s_waitcnt lgkmcnt(1)\n\t"
"ds_read_b128 a[172:175], v244 offset:34304\n\t"
"v_mfma_f32_16x16x32_bf16 v[68:71], a[208:211], a[180:183], v[68:71]\n\t"
"v_mfma_f32_16x16x32_bf16 v[228:231], v[228:231], v[220:223], 0\n\t"
"v_mfma_f32_16x16x32_bf16 v[72:75], a[216:219], a[176:179], v[72:75]\n\t"
"ds_read_b128 a[176:179], v244 offset:34816\n\t"
"v_mfma_f32_16x16x32_bf16 v[72:75], a[220:223], a[180:183], v[72:75]\n\t"
"v_mfma_f32_16x16x32_bf16 v[72:75], a[208:211], a[184:187], v[72:75]\n\t"
"v_mfma_f32_16x16x32_bf16 v[76:79], a[216:219], a[180:183], v[76:79]\n\t"
"ds_read_b128 a[180:183], v244 offset:35328\n\t"
"v_mfma_f32_16x16x32_bf16 v[76:79], a[220:223], a[184:187], v[76:79]\n\t"
"v_cvt_pk_bf16_f32 v226, v228, v229\n\t"
"v_cvt_pk_bf16_f32 v227, v230, v231\n\t"
"v_mfma_f32_16x16x32_bf16 v[76:79], a[208:211], a[188:191], v[76:79]\n\t"
"ds_read_b64_tr_b16 v[228:229], v246 offset:26624\n\t"
"v_mfma_f32_16x16x32_bf16 v[80:83], a[216:219], a[184:187], v[80:83]\n\t"
"ds_read_b64_tr_b16 v[230:231], v246 offset:29696\n\t"
"v_mfma_f32_16x16x32_bf16 v[80:83], a[220:223], a[188:191], v[80:83]\n\t"
"ds_read_b128 a[184:187], v244 offset:35840\n\t"
"v_mfma_f32_16x16x32_bf16 v[80:83], a[208:211], a[192:195], v[80:83]\n\t"
"v_mfma_f32_16x16x32_bf16 v[84:87], a[216:219], a[188:191], v[84:87]\n\t"
"ds_read_b128 a[188:191], v244 offset:36352\n\t"
"v_mfma_f32_16x16x32_bf16 v[84:87], a[220:223], a[192:195], v[84:87]\n\t"
"v_mfma_f32_16x16x32_bf16 v[84:87], a[208:211], a[196:199], v[84:87]\n\t"
"v_mfma_f32_16x16x32_bf16 v[88:91], a[216:219], a[192:195], v[88:91]\n\t"
"ds_read_b128 a[192:195], v244 offset:36864\n\t"
"v_mfma_f32_16x16x32_bf16 v[88:91], a[220:223], a[196:199], v[88:91]\n\t"
"v_mfma_f32_16x16x32_bf16 v[88:91], a[208:211], a[200:203], v[88:91]\n\t"
"v_mfma_f32_16x16x32_bf16 v[92:95], a[216:219], a[196:199], v[92:95]\n\t"
"buffer_load_dwordx4 a[216:219], v252, s[8:11], s20 offen offset:1024 sc1\n\t"
"v_mfma_f32_16x16x32_bf16 v[92:95], a[220:223], a[200:203], v[92:95]\n\t"
"s_waitcnt vmcnt(11) lgkmcnt(3)\n\t"
"ds_read_b128 a[196:199], v244 offset:37376\n\t"
"v_mfma_f32_16x16x32_bf16 v[92:95], a[208:211], a[204:207], v[92:95]\n\t"
"ds_read_b128 a[200:203], v244 offset:37888\n\t"
"v_mfma_f32_16x16x32_bf16 v[228:231], v[228:231], v[220:223], 0\n\t"
"ds_read_b128 a[204:207], v244 offset:38400\n\t"
"v_mfma_f32_16x16x32_bf16 v[100:103], a[224:227], a[168:171], v[100:103]\n\t"
"buffer_load_dwordx4 a[208:211], v252, s[8:11], s20 offen offset:2048 sc1\n\t"
"v_mfma_f32_16x16x32_bf16 v[100:103], a[228:231], a[172:175], v[100:103]\n\t"
"s_mov_b32 s20, 946176\n\t"
"v_mfma_f32_16x16x32_bf16 v[100:103], a[232:235], a[176:179], v[100:103]\n\t"
"buffer_load_dwordx4 a[220:223], v252, s[8:11], s20 offen offset:0 sc1\n\t"
"v_mfma_f32_16x16x32_bf16 v[104:107], a[224:227], a[172:175], v[104:107]\n\t"
"s_waitcnt lgkmcnt(5)\n\t"
"v_mfma_f32_16x16x32_bf16 v[104:107], a[228:231], a[176:179], v[104:107]\n\t"
"v_cvt_pk_bf16_f32 v228, v228, v229\n\t"
"v_mfma_f32_16x16x32_bf16 v[104:107], a[232:235], a[180:183], v[104:107]\n\t"
"s_waitcnt lgkmcnt(0)\n\t"
"s_barrier\n\t"
"v_mfma_f32_16x16x32_bf16 v[108:111], a[224:227], a[176:179], v[108:111]\n\t"
"v_cvt_pk_bf16_f32 v229, v230, v231\n\t"
"v_mfma_f32_16x16x32_bf16 v[108:111], a[228:231], a[180:183], v[108:111]\n\t"
"v_mfma_f32_16x16x32_bf16 v[108:111], a[232:235], a[184:187], v[108:111]\n\t"
"ds_write2st64_b64 v245, v[232:233], v[234:235] offset0:48 offset1:49\n\t"
"v_mfma_f32_16x16x32_bf16 v[112:115], a[224:227], a[180:183], v[112:115]\n\t"
"v_mfma_f32_16x16x32_bf16 v[112:115], a[228:231], a[184:187], v[112:115]\n\t"
"ds_read_b64_tr_b16 v[232:233], v246 offset:32768\n\t"
"v_mfma_f32_16x16x32_bf16 v[112:115], a[232:235], a[188:191], v[112:115]\n\t"
"ds_write2st64_b64 v245, v[224:225], v[226:227] offset0:50 offset1:51\n\t"
"v_mfma_f32_16x16x32_bf16 v[116:119], a[224:227], a[184:187], v[116:119]\n\t"
"v_mfma_f32_16x16x32_bf16 v[116:119], a[228:231], a[188:191], v[116:119]\n\t"
"ds_read_b64_tr_b16 v[234:235], v246 offset:35840\n\t"
"v_mfma_f32_16x16x32_bf16 v[116:119], a[232:235], a[192:195], v[116:119]\n\t"
"s_add_u32 m0, s24, 72960\n\t"
"s_add_u32 s28, s21, 2162688\n\t"
"ds_write_b64 v245, v[228:229] offset:26624\n\t"
"v_mfma_f32_16x16x32_bf16 v[120:123], a[224:227], a[188:191], v[120:123]\n\t"
"ds_read_b64_tr_b16 v[224:225], v246 offset:40448\n\t"
"v_mfma_f32_16x16x32_bf16 v[120:123], a[228:231], a[192:195], v[120:123]\n\t"
"ds_read_b64_tr_b16 v[226:227], v246 offset:43520\n\t"
"v_mfma_f32_16x16x32_bf16 v[120:123], a[232:235], a[196:199], v[120:123]\n\t"
"s_waitcnt lgkmcnt(3)\n\t"
"ds_read_b64_tr_b16 v[228:229], v246 offset:46592\n\t"
"v_mfma_f32_16x16x32_bf16 v[232:235], v[232:235], v[220:223], 0\n\t"
"ds_read_b64_tr_b16 v[230:231], v246 offset:49664\n\t"
"v_mfma_f32_16x16x32_bf16 v[124:127], a[224:227], a[192:195], v[124:127]\n\t"
"buffer_load_dwordx4 v247, s[12:15], s28 offen offset:256 nt lds\n\t"
"v_mfma_f32_16x16x32_bf16 v[124:127], a[228:231], a[196:199], v[124:127]\n\t"
"s_add_u32 m0, s24, 80640\n\t"
"s_add_u32 s28, s21, 2359296\n\t"
"v_mfma_f32_16x16x32_bf16 v[124:127], a[232:235], a[200:203], v[124:127]\n\t"
"s_waitcnt lgkmcnt(2)\n\t"
"buffer_load_dwordx4 v247, s[12:15], s28 offen offset:256 nt lds\n\t"
"s_add_u32 m0, s24, 26880\n\t"
"s_add_u32 s28, s21, 786432\n\t"
"v_mfma_f32_16x16x32_bf16 v[224:227], v[224:227], v[220:223], 0\n\t"
"buffer_load_dwordx4 v247, s[12:15], s28 offen offset:256 nt lds\n\t"
"v_mfma_f32_16x16x32_bf16 v[128:131], a[224:227], a[196:199], v[128:131]\n\t"
"s_waitcnt lgkmcnt(0)\n\t"
"s_add_u32 m0, s24, 86784\n\t"
"s_add_u32 s28, s21, 2555904\n\t"
"v_mfma_f32_16x16x32_bf16 v[128:131], a[228:231], a[200:203], v[128:131]\n\t"
"buffer_load_dwordx4 v247, s[12:15], s28 offen offset:256 nt lds\n\t"
"v_mfma_f32_16x16x32_bf16 v[128:131], a[232:235], a[204:207], v[128:131]\n\t"
"s_waitcnt vmcnt(14)\n\t"
"s_add_u32 m0, s24, 33024\n\t"
"s_add_u32 s28, s21, 983040\n\t"
"v_mfma_f32_16x16x32_bf16 v[228:231], v[228:231], v[220:223], 0\n\t"
"buffer_load_dwordx4 v247, s[12:15], s28 offen offset:256 nt lds\n\t"
"v_cvt_pk_bf16_f32 v232, v232, v233\n\t"
"v_cvt_pk_bf16_f32 v233, v234, v235\n\t"
"v_mfma_f32_16x16x32_bf16 v[132:135], a[236:239], a[168:171], v[132:135]\n\t"
"s_add_u32 m0, s24, 40704\n\t"
"s_add_u32 s28, s21, 1179648\n\t"
"v_cvt_pk_bf16_f32 v234, v224, v225\n\t"
"v_mfma_f32_16x16x32_bf16 v[132:135], a[240:243], a[172:175], v[132:135]\n\t"
"v_cvt_pk_bf16_f32 v235, v226, v227\n\t"
"v_mfma_f32_16x16x32_bf16 v[132:135], v[240:243], a[176:179], v[132:135]\n\t"
"buffer_load_dwordx4 v247, s[12:15], s28 offen offset:256 nt lds\n\t"
"v_mfma_f32_16x16x32_bf16 v[136:139], a[236:239], a[172:175], v[136:139]\n\t"
"ds_read_b64_tr_b16 v[224:225], v246 offset:52736\n\t"
"v_mfma_f32_16x16x32_bf16 v[136:139], a[240:243], a[176:179], v[136:139]\n\t"
"ds_read_b64_tr_b16 v[226:227], v246 offset:55808\n\t"
"v_cvt_pk_bf16_f32 v228, v228, v229\n\t"
"v_mfma_f32_16x16x32_bf16 v[136:139], v[240:243], a[180:183], v[136:139]\n\t"
"ds_write2st64_b64 v245, v[232:233], v[234:235] offset0:53 offset1:54\n\t"
"v_mfma_f32_16x16x32_bf16 v[140:143], a[236:239], a[176:179], v[140:143]\n\t"
"v_cvt_pk_bf16_f32 v229, v230, v231\n\t"
"v_mfma_f32_16x16x32_bf16 v[140:143], a[240:243], a[180:183], v[140:143]\n\t"
"v_mfma_f32_16x16x32_bf16 v[140:143], v[240:243], a[184:187], v[140:143]\n\t"
"v_mfma_f32_16x16x32_bf16 v[144:147], a[236:239], a[180:183], v[144:147]\n\t"
"v_mfma_f32_16x16x32_bf16 v[144:147], a[240:243], a[184:187], v[144:147]\n\t"
"v_mfma_f32_16x16x32_bf16 v[144:147], v[240:243], a[188:191], v[144:147]\n\t"
"v_mfma_f32_16x16x32_bf16 v[148:151], a[236:239], a[184:187], v[148:151]\n\t"
"s_waitcnt lgkmcnt(1)\n\t"
"v_mfma_f32_16x16x32_bf16 v[148:151], a[240:243], a[188:191], v[148:151]\n\t"
"v_mfma_f32_16x16x32_bf16 v[148:151], v[240:243], a[192:195], v[148:151]\n\t"
"v_mfma_f32_16x16x32_bf16 v[224:227], v[224:227], v[220:223], 0\n\t"
"v_mfma_f32_16x16x32_bf16 v[152:155], a[236:239], a[188:191], v[152:155]\n\t"
"v_mfma_f32_16x16x32_bf16 v[152:155], a[240:243], a[192:195], v[152:155]\n\t"
"v_mfma_f32_16x16x32_bf16 v[152:155], v[240:243], a[196:199], v[152:155]\n\t"
"v_mfma_f32_16x16x32_bf16 v[156:159], a[236:239], a[192:195], v[156:159]\n\t"
"v_mfma_f32_16x16x32_bf16 v[156:159], a[240:243], a[196:199], v[156:159]\n\t"
"v_cvt_pk_bf16_f32 v230, v224, v225\n\t"
"v_cvt_pk_bf16_f32 v231, v226, v227\n\t"
"v_mfma_f32_16x16x32_bf16 v[156:159], v[240:243], a[200:203], v[156:159]\n\t"
"ds_write2st64_b64 v245, v[228:229], v[230:231] offset0:55 offset1:56\n\t"
"v_mfma_f32_16x16x32_bf16 v[160:163], a[236:239], a[196:199], v[160:163]\n\t"
"s_waitcnt vmcnt(13)\n\t"
"v_mfma_f32_16x16x32_bf16 v[160:163], a[240:243], a[200:203], v[160:163]\n\t"
"v_mfma_f32_16x16x32_bf16 v[160:163], v[240:243], a[204:207], v[160:163]\n\t"
"v_mfma_f32_16x16x32_bf16 v[164:167], v[96:99], a[168:171], v[164:167]\n\t"
"v_mfma_f32_16x16x32_bf16 v[164:167], a[244:247], a[172:175], v[164:167]\n\t"
"v_mfma_f32_16x16x32_bf16 v[164:167], a[248:251], a[176:179], v[164:167]\n\t"
"s_waitcnt lgkmcnt(0)\n\t"
"s_barrier\n\t"
"v_mfma_f32_16x16x32_bf16 v[168:171], v[96:99], a[172:175], v[168:171]\n\t"
"ds_read_b128 a[168:171], v244 offset:24576\n\t"
"v_mfma_f32_16x16x32_bf16 v[168:171], a[244:247], a[176:179], v[168:171]\n\t"
"buffer_load_dwordx4 v[240:243], v252, s[8:11], s20 offen offset:1024 sc1\n\t"
"v_mfma_f32_16x16x32_bf16 v[168:171], a[248:251], a[180:183], v[168:171]\n\t"
"ds_read_b128 a[172:175], v244 offset:25088\n\t"
"v_mfma_f32_16x16x32_bf16 v[172:175], v[96:99], a[176:179], v[172:175]\n\t"
"ds_read_b128 a[176:179], v244 offset:25600\n\t"
"v_mfma_f32_16x16x32_bf16 v[172:175], a[244:247], a[180:183], v[172:175]\n\t"
"v_mfma_f32_16x16x32_bf16 v[172:175], a[248:251], a[184:187], v[172:175]\n\t"
"v_mfma_f32_16x16x32_bf16 v[176:179], v[96:99], a[180:183], v[176:179]\n\t"
"ds_read_b128 a[180:183], v244 offset:26112\n\t"
"v_mfma_f32_16x16x32_bf16 v[176:179], a[244:247], a[184:187], v[176:179]\n\t"
"v_mfma_f32_16x16x32_bf16 v[176:179], a[248:251], a[188:191], v[176:179]\n\t"
"v_mfma_f32_16x16x32_bf16 v[180:183], v[96:99], a[184:187], v[180:183]\n\t"
"ds_read_b128 a[184:187], v244 offset:26624\n\t"
"v_mfma_f32_16x16x32_bf16 v[180:183], a[244:247], a[188:191], v[180:183]\n\t"
"v_mfma_f32_16x16x32_bf16 v[180:183], a[248:251], a[192:195], v[180:183]\n\t"
"v_mfma_f32_16x16x32_bf16 v[184:187], v[96:99], a[188:191], v[184:187]\n\t"
"ds_read_b128 a[188:191], v244 offset:27136\n\t"
"v_mfma_f32_16x16x32_bf16 v[184:187], a[244:247], a[192:195], v[184:187]\n\t"
"v_mfma_f32_16x16x32_bf16 v[184:187], a[248:251], a[196:199], v[184:187]\n\t"
"v_mfma_f32_16x16x32_bf16 v[188:191], v[96:99], a[192:195], v[188:191]\n\t"
"ds_read_b128 a[192:195], v244 offset:27648\n\t"
"v_mfma_f32_16x16x32_bf16 v[188:191], a[244:247], a[196:199], v[188:191]\n\t"
"v_mfma_f32_16x16x32_bf16 v[188:191], a[248:251], a[200:203], v[188:191]\n\t"
"v_mfma_f32_16x16x32_bf16 v[192:195], v[96:99], a[196:199], v[192:195]\n\t"
"s_waitcnt vmcnt(11) lgkmcnt(4)\n\t"
"ds_read_b128 a[196:199], v244 offset:28160\n\t"
"v_mfma_f32_16x16x32_bf16 v[192:195], a[244:247], a[200:203], v[192:195]\n\t"
"ds_read_b128 a[200:203], v244 offset:28672\n\t"
"v_mfma_f32_16x16x32_bf16 v[192:195], a[248:251], a[204:207], v[192:195]\n\t"
"ds_read_b128 a[204:207], v244 offset:29184\n\t"
"Lst26_%=:\n\t"
"v_mfma_f32_16x16x32_bf16 v[0:3], a[252:255], a[168:171], v[0:3]\n\t"
"s_waitcnt lgkmcnt(6)\n\t"
"v_xor_b32 v246, 0xc0, v246\n\t"
"v_mfma_f32_16x16x32_bf16 v[0:3], v[236:239], a[172:175], v[0:3]\n\t"
"s_waitcnt lgkmcnt(4)\n\t"
"s_add_u32 s23, s40, 6291456\n\t"
"s_add_u32 s22, s22, 6291456\n\t"
"v_mfma_f32_16x16x32_bf16 v[0:3], v[248:251], a[176:179], v[0:3]\n\t"
"Lpg19_%=:\n\t"
"s_cbranch_vccnz Lvr2_%=\n\t"
"v_mfma_f32_16x16x32_bf16 v[4:7], a[252:255], a[172:175], v[4:7]\n\t"
"v_mfma_f32_16x16x32_bf16 v[4:7], v[236:239], a[176:179], v[4:7]\n\t"
"v_mfma_f32_16x16x32_bf16 v[4:7], v[248:251], a[180:183], v[4:7]\n\t"
"v_mfma_f32_16x16x32_bf16 v[8:11], a[252:255], a[176:179], v[8:11]\n\t"
"v_mfma_f32_16x16x32_bf16 v[8:11], v[236:239], a[180:183], v[8:11]\n\t"
"v_mfma_f32_16x16x32_bf16 v[8:11], v[248:251], a[184:187], v[8:11]\n\t"
"v_mfma_f32_16x16x32_bf16 v[12:15], a[252:255], a[180:183], v[12:15]\n\t"
"s_waitcnt lgkmcnt(3)\n\t"
"v_mfma_f32_16x16x32_bf16 v[12:15], v[236:239], a[184:187], v[12:15]\n\t"
"v_mfma_f32_16x16x32_bf16 v[12:15], v[248:251], a[188:191], v[12:15]\n\t"
"buffer_load_dwordx4 v[96:99], v252, s[8:11], s20 offen offset:2048 sc1\n\t"
"v_mfma_f32_16x16x32_bf16 v[16:19], a[252:255], a[184:187], v[16:19]\n\t"
"s_waitcnt lgkmcnt(2)\n\t"
"s_mov_b32 s20, 958464\n\t"
"v_mfma_f32_16x16x32_bf16 v[16:19], v[236:239], a[188:191], v[16:19]\n\t"
"v_mfma_f32_16x16x32_bf16 v[16:19], v[248:251], a[192:195], v[16:19]\n\t"
"ds_read_b64_tr_b16 v[232:233], v246 offset:512\n\t"
"v_mfma_f32_16x16x32_bf16 v[20:23], a[252:255], a[188:191], v[20:23]\n\t"
"buffer_load_dwordx4 a[224:227], v252, s[8:11], s20 offen offset:0 sc1\n\t"
"v_mfma_f32_16x16x32_bf16 v[20:23], v[236:239], a[192:195], v[20:23]\n\t"
"s_waitcnt lgkmcnt(2)\n\t"
"ds_read_b64_tr_b16 v[234:235], v246 offset:3584\n\t"
"v_mfma_f32_16x16x32_bf16 v[20:23], v[248:251], a[196:199], v[20:23]\n\t"
"v_mfma_f32_16x16x32_bf16 v[24:27], a[252:255], a[192:195], v[24:27]\n\t"
"buffer_load_dwordx4 a[228:231], v252, s[8:11], s20 offen offset:1024 sc1\n\t"
"v_mfma_f32_16x16x32_bf16 v[24:27], v[236:239], a[196:199], v[24:27]\n\t"
"s_waitcnt lgkmcnt(2)\n\t"
"v_mfma_f32_16x16x32_bf16 v[24:27], v[248:251], a[200:203], v[24:27]\n\t"
"v_mfma_f32_16x16x32_bf16 v[28:31], a[252:255], a[196:199], v[28:31]\n\t"
"s_waitcnt vmcnt(11)\n\t"
"ds_read_b64_tr_b16 v[224:225], v246 offset:6656\n\t"
"v_mfma_f32_16x16x32_bf16 v[28:31], v[236:239], a[200:203], v[28:31]\n\t"
"buffer_load_dwordx4 v[236:239], v252, s[8:11], s20 offen offset:2048 sc1\n\t"
"v_mfma_f32_16x16x32_bf16 v[28:31], v[248:251], a[204:207], v[28:31]\n\t"
"s_mov_b32 s20, 970752\n\t"
"ds_read_b64_tr_b16 v[226:227], v246 offset:9728\n\t"
"v_mfma_f32_16x16x32_bf16 v[32:35], a[212:215], a[168:171], v[32:35]\n\t"
"v_mfma_f32_16x16x32_bf16 v[32:35], a[216:219], a[172:175], v[32:35]\n\t"
"v_mfma_f32_16x16x32_bf16 v[32:35], a[208:211], a[176:179], v[32:35]\n\t"
"buffer_load_dwordx4 v[248:251], v252, s[8:11], s20 offen offset:0 sc1\n\t"
"v_mfma_f32_16x16x32_bf16 v[36:39], a[212:215], a[172:175], v[36:39]\n\t"
"s_waitcnt lgkmcnt(2)\n\t"
"v_mfma_f32_16x16x32_bf16 v[36:39], a[216:219], a[176:179], v[36:39]\n\t"
"v_mfma_f32_16x16x32_bf16 v[36:39], a[208:211], a[180:183], v[36:39]\n\t"
"v_mfma_f32_16x16x32_bf16 v[232:235], v[232:235], v[220:223], 0\n\t"
"buffer_load_dwordx4 a[232:235], v252, s[8:11], s20 offen offset:1024 sc1\n\t"
"v_mfma_f32_16x16x32_bf16 v[40:43], a[212:215], a[176:179], v[40:43]\n\t"
"ds_read_b64_tr_b16 v[228:229], v246 offset:12800\n\t"
"v_mfma_f32_16x16x32_bf16 v[40:43], a[216:219], a[180:183], v[40:43]\n\t"
"ds_read_b64_tr_b16 v[230:231], v246 offset:15872\n\t"
"v_mfma_f32_16x16x32_bf16 v[40:43], a[208:211], a[184:187], v[40:43]\n\t"
"v_mfma_f32_16x16x32_bf16 v[44:47], a[212:215], a[180:183], v[44:47]\n\t"
"buffer_load_dwordx4 a[236:239], v252, s[8:11], s20 offen offset:2048 sc1\n\t"
"v_mfma_f32_16x16x32_bf16 v[44:47], a[216:219], a[184:187], v[44:47]\n\t"
"s_waitcnt lgkmcnt(2)\n\t"
"v_cvt_pk_bf16_f32 v232, v232, v233\n\t"
"v_cvt_pk_bf16_f32 v233, v234, v235\n\t"
"v_mfma_f32_16x16x32_bf16 v[44:47], a[208:211], a[188:191], v[44:47]\n\t"
"s_mov_b32 s20, 983040\n\t"
"v_mfma_f32_16x16x32_bf16 v[224:227], v[224:227], v[220:223], 0\n\t"
"v_mfma_f32_16x16x32_bf16 v[48:51], a[212:215], a[184:187], v[48:51]\n\t"
"buffer_load_dwordx4 a[240:243], v252, s[8:11], s20 offen offset:0 sc1\n\t"
"v_mfma_f32_16x16x32_bf16 v[48:51], a[216:219], a[188:191], v[48:51]\n\t"
"v_mfma_f32_16x16x32_bf16 v[48:51], a[208:211], a[192:195], v[48:51]\n\t"
"v_mfma_f32_16x16x32_bf16 v[52:55], a[212:215], a[188:191], v[52:55]\n\t"
"v_mfma_f32_16x16x32_bf16 v[52:55], a[216:219], a[192:195], v[52:55]\n\t"
"buffer_load_dwordx4 a[244:247], v252, s[8:11], s20 offen offset:1024 sc1\n\t"
"v_mfma_f32_16x16x32_bf16 v[52:55], a[208:211], a[196:199], v[52:55]\n\t"
"v_cvt_pk_bf16_f32 v234, v224, v225\n\t"
"v_cvt_pk_bf16_f32 v235, v226, v227\n\t"
"v_mfma_f32_16x16x32_bf16 v[56:59], a[212:215], a[192:195], v[56:59]\n\t"
"s_waitcnt lgkmcnt(0)\n\t"
"ds_read_b64_tr_b16 v[224:225], v246 offset:20480\n\t"
"v_mfma_f32_16x16x32_bf16 v[56:59], a[216:219], a[196:199], v[56:59]\n\t"
"ds_read_b64_tr_b16 v[226:227], v246 offset:23552\n\t"
"v_mfma_f32_16x16x32_bf16 v[56:59], a[208:211], a[200:203], v[56:59]\n\t"
"buffer_load_dwordx4 a[248:251], v252, s[8:11], s20 offen offset:2048 sc1\n\t"
"v_mfma_f32_16x16x32_bf16 v[228:231], v[228:231], v[220:223], 0\n\t"
"s_mov_b32 s20, 995328\n\t"
"v_mfma_f32_16x16x32_bf16 v[60:63], a[212:215], a[196:199], v[60:63]\n\t"
"s_waitcnt vmcnt(9)\n\t"
"v_mfma_f32_16x16x32_bf16 v[60:63], a[216:219], a[200:203], v[60:63]\n\t"
"v_mfma_f32_16x16x32_bf16 v[60:63], a[208:211], a[204:207], v[60:63]\n\t"
"buffer_load_dwordx4 a[212:215], v252, s[8:11], s20 offen offset:0 sc1\n\t"
"v_mfma_f32_16x16x32_bf16 v[64:67], a[220:223], a[168:171], v[64:67]\n\t"
"ds_read_b128 a[168:171], v244 offset:33792\n\t"
"v_mfma_f32_16x16x32_bf16 v[64:67], v[240:243], a[172:175], v[64:67]\n\t"
"v_cvt_pk_bf16_f32 v228, v228, v229\n\t"
"v_cvt_pk_bf16_f32 v229, v230, v231\n\t"
"v_mfma_f32_16x16x32_bf16 v[64:67], v[96:99], a[176:179], v[64:67]\n\t"
"v_mfma_f32_16x16x32_bf16 v[68:71], a[220:223], a[172:175], v[68:71]\n\t"
"buffer_load_dwordx4 a[216:219], v252, s[8:11], s20 offen offset:1024 sc1\n\t"
"v_mfma_f32_16x16x32_bf16 v[68:71], v[240:243], a[176:179], v[68:71]\n\t"
"s_waitcnt lgkmcnt(1)\n\t"
"ds_read_b128 a[172:175], v244 offset:34304\n\t"
"v_mfma_f32_16x16x32_bf16 v[68:71], v[96:99], a[180:183], v[68:71]\n\t"
"buffer_load_dwordx4 a[252:255], v252, s[8:11], s20 offen offset:2048 sc1\n\t"
"v_mfma_f32_16x16x32_bf16 v[224:227], v[224:227], v[220:223], 0\n\t"
"v_mfma_f32_16x16x32_bf16 v[72:75], a[220:223], a[176:179], v[72:75]\n\t"
"ds_read_b128 a[176:179], v244 offset:34816\n\t"
"v_mfma_f32_16x16x32_bf16 v[72:75], v[240:243], a[180:183], v[72:75]\n\t"
"v_mfma_f32_16x16x32_bf16 v[72:75], v[96:99], a[184:187], v[72:75]\n\t"
"v_mfma_f32_16x16x32_bf16 v[76:79], a[220:223], a[180:183], v[76:79]\n\t"
"ds_read_b128 a[180:183], v244 offset:35328\n\t"
"v_mfma_f32_16x16x32_bf16 v[76:79], v[240:243], a[184:187], v[76:79]\n\t"
"v_cvt_pk_bf16_f32 v230, v224, v225\n\t"
"v_cvt_pk_bf16_f32 v231, v226, v227\n\t"
"v_mfma_f32_16x16x32_bf16 v[76:79], v[96:99], a[188:191], v[76:79]\n\t"
"ds_read_b64_tr_b16 v[224:225], v246 offset:26624\n\t"
"v_mfma_f32_16x16x32_bf16 v[80:83], a[220:223], a[184:187], v[80:83]\n\t"
"ds_read_b64_tr_b16 v[226:227], v246 offset:29696\n\t"
"v_mfma_f32_16x16x32_bf16 v[80:83], v[240:243], a[188:191], v[80:83]\n\t"
"ds_read_b128 a[184:187], v244 offset:35840\n\t"
"v_mfma_f32_16x16x32_bf16 v[80:83], v[96:99], a[192:195], v[80:83]\n\t"
"v_mfma_f32_16x16x32_bf16 v[84:87], a[220:223], a[188:191], v[84:87]\n\t"
"s_mov_b32 s20, 1007616\n\t"
"ds_read_b128 a[188:191], v244 offset:36352\n\t"
"v_mfma_f32_16x16x32_bf16 v[84:87], v[240:243], a[192:195], v[84:87]\n\t"
"v_mfma_f32_16x16x32_bf16 v[84:87], v[96:99], a[196:199], v[84:87]\n\t"
"v_mfma_f32_16x16x32_bf16 v[88:91], a[220:223], a[192:195], v[88:91]\n\t"
"ds_read_b128 a[192:195], v244 offset:36864\n\t"
"v_mfma_f32_16x16x32_bf16 v[88:91], v[240:243], a[196:199], v[88:91]\n\t"
"v_mfma_f32_16x16x32_bf16 v[88:91], v[96:99], a[200:203], v[88:91]\n\t"
"v_mfma_f32_16x16x32_bf16 v[92:95], a[220:223], a[196:199], v[92:95]\n\t"
"buffer_load_dwordx4 a[220:223], v252, s[8:11], s20 offen offset:0 sc1\n\t"
"v_mfma_f32_16x16x32_bf16 v[92:95], v[240:243], a[200:203], v[92:95]\n\t"
"s_waitcnt vmcnt(10) lgkmcnt(3)\n\t"
"ds_read_b128 a[196:199], v244 offset:37376\n\t"
"v_mfma_f32_16x16x32_bf16 v[92:95], v[96:99], a[204:207], v[92:95]\n\t"
"ds_read_b128 a[200:203], v244 offset:37888\n\t"
"v_mfma_f32_16x16x32_bf16 v[224:227], v[224:227], v[220:223], 0\n\t"
"ds_read_b128 a[204:207], v244 offset:38400\n\t"
"v_mfma_f32_16x16x32_bf16 v[100:103], a[224:227], a[168:171], v[100:103]\n\t"
"buffer_load_dwordx4 v[96:99], v252, s[8:11], s20 offen offset:1024 sc1\n\t"
"v_mfma_f32_16x16x32_bf16 v[100:103], a[228:231], a[172:175], v[100:103]\n\t"
"v_mfma_f32_16x16x32_bf16 v[100:103], v[236:239], a[176:179], v[100:103]\n\t"
"v_mfma_f32_16x16x32_bf16 v[104:107], a[224:227], a[172:175], v[104:107]\n\t"
"s_waitcnt lgkmcnt(5)\n\t"
"v_mfma_f32_16x16x32_bf16 v[104:107], a[228:231], a[176:179], v[104:107]\n\t"
"v_cvt_pk_bf16_f32 v224, v224, v225\n\t"
"v_mfma_f32_16x16x32_bf16 v[104:107], v[236:239], a[180:183], v[104:107]\n\t"
"s_waitcnt lgkmcnt(0)\n\t"
"s_barrier\n\t"
"v_mfma_f32_16x16x32_bf16 v[108:111], a[224:227], a[176:179], v[108:111]\n\t"
"v_cvt_pk_bf16_f32 v225, v226, v227\n\t"
"v_mfma_f32_16x16x32_bf16 v[108:111], a[228:231], a[180:183], v[108:111]\n\t"
"v_mfma_f32_16x16x32_bf16 v[108:111], v[236:239], a[184:187], v[108:111]\n\t"
"ds_write2st64_b64 v245, v[232:233], v[234:235] offset0:48 offset1:49\n\t"
"v_mfma_f32_16x16x32_bf16 v[112:115], a[224:227], a[180:183], v[112:115]\n\t"
"v_mfma_f32_16x16x32_bf16 v[112:115], a[228:231], a[184:187], v[112:115]\n\t"
"ds_read_b64_tr_b16 v[232:233], v246 offset:32768\n\t"
"v_mfma_f32_16x16x32_bf16 v[112:115], v[236:239], a[188:191], v[112:115]\n\t"
"ds_write2st64_b64 v245, v[228:229], v[230:231] offset0:50 offset1:51\n\t"
"v_mfma_f32_16x16x32_bf16 v[116:119], a[224:227], a[184:187], v[116:119]\n\t"
"global_load_dwordx4 v[240:243], v252, s[48:49] offset:2048\n\t"
"v_mfma_f32_16x16x32_bf16 v[116:119], a[228:231], a[188:191], v[116:119]\n\t"
"ds_read_b64_tr_b16 v[234:235], v246 offset:35840\n\t"
"v_mfma_f32_16x16x32_bf16 v[116:119], v[236:239], a[192:195], v[116:119]\n\t"
"ds_write_b64 v245, v[224:225] offset:26624\n\t"
"v_mfma_f32_16x16x32_bf16 v[120:123], a[224:227], a[188:191], v[120:123]\n\t"
"ds_read_b64_tr_b16 v[228:229], v246 offset:40448\n\t"
"v_mfma_f32_16x16x32_bf16 v[120:123], a[228:231], a[192:195], v[120:123]\n\t"
"ds_read_b64_tr_b16 v[230:231], v246 offset:43520\n\t"
"v_mfma_f32_16x16x32_bf16 v[120:123], v[236:239], a[196:199], v[120:123]\n\t"
"s_waitcnt lgkmcnt(3)\n\t"
"ds_read_b64_tr_b16 v[224:225], v246 offset:46592\n\t"
"v_mfma_f32_16x16x32_bf16 v[232:235], v[232:235], v[220:223], 0\n\t"
"ds_read_b64_tr_b16 v[226:227], v246 offset:49664\n\t"
"v_mfma_f32_16x16x32_bf16 v[124:127], a[224:227], a[192:195], v[124:127]\n\t"
"global_load_dwordx4 a[208:211], v252, s[46:47] offset:1024\n\t"
"v_mfma_f32_16x16x32_bf16 v[124:127], a[228:231], a[196:199], v[124:127]\n\t"
"v_mfma_f32_16x16x32_bf16 v[124:127], v[236:239], a[200:203], v[124:127]\n\t"
"s_waitcnt lgkmcnt(2)\n\t"
"v_mfma_f32_16x16x32_bf16 v[228:231], v[228:231], v[220:223], 0\n\t"
"v_mfma_f32_16x16x32_bf16 v[128:131], a[224:227], a[196:199], v[128:131]\n\t"
"buffer_load_dwordx4 a[224:227], v252, s[8:11], s20 offen offset:2048 sc1\n\t"
"v_mfma_f32_16x16x32_bf16 v[128:131], a[228:231], a[200:203], v[128:131]\n\t"
"s_waitcnt vmcnt(11) lgkmcnt(0)\n\t"
"s_mov_b32 s20, 1019904\n\t"
"v_cvt_pk_bf16_f32 v232, v232, v233\n\t"
"v_mfma_f32_16x16x32_bf16 v[128:131], v[236:239], a[204:207], v[128:131]\n\t"
"v_cvt_pk_bf16_f32 v233, v234, v235\n\t"
"v_mfma_f32_16x16x32_bf16 v[224:227], v[224:227], v[220:223], 0\n\t"
"buffer_load_dwordx4 a[228:231], v252, s[8:11], s20 offen offset:0 sc1\n\t"
"v_mfma_f32_16x16x32_bf16 v[132:135], v[248:251], a[168:171], v[132:135]\n\t"
"s_add_u32 m0, s24, 46848\n\t"
"s_add_u32 s28, s21, 1376256\n\t"
"v_cvt_pk_bf16_f32 v234, v228, v229\n\t"
"v_mfma_f32_16x16x32_bf16 v[132:135], a[232:235], a[172:175], v[132:135]\n\t"
"v_cvt_pk_bf16_f32 v235, v230, v231\n\t"
"v_mfma_f32_16x16x32_bf16 v[132:135], a[236:239], a[176:179], v[132:135]\n\t"
"ds_write2st64_b64 v245, v[232:233], v[234:235] offset0:53 offset1:54\n\t"
"v_mfma_f32_16x16x32_bf16 v[136:139], v[248:251], a[172:175], v[136:139]\n\t"
"buffer_load_dwordx4 v247, s[12:15], s28 offen offset:256 nt lds\n\t"
"v_mfma_f32_16x16x32_bf16 v[136:139], a[232:235], a[176:179], v[136:139]\n\t"
"s_add_u32 m0, s24, 52992\n\t"
"s_add_u32 s28, s21, 1572864\n\t"
"ds_read_b64_tr_b16 v[228:229], v246 offset:52736\n\t"
"v_mfma_f32_16x16x32_bf16 v[136:139], a[236:239], a[180:183], v[136:139]\n\t"
"buffer_load_dwordx4 v247, s[12:15], s28 offen offset:256 nt lds\n\t"
"v_mfma_f32_16x16x32_bf16 v[140:143], v[248:251], a[176:179], v[140:143]\n\t"
"s_add_u32 m0, s24, 92928\n\t"
"s_add_u32 s28, s21, 2752512\n\t"
"ds_read_b64_tr_b16 v[230:231], v246 offset:55808\n\t"
"v_mfma_f32_16x16x32_bf16 v[140:143], a[232:235], a[180:183], v[140:143]\n\t"
"buffer_load_dwordx4 v247, s[12:15], s28 offen offset:256 nt lds\n\t"
"v_mfma_f32_16x16x32_bf16 v[140:143], a[236:239], a[184:187], v[140:143]\n\t"
"s_add_u32 m0, s24, 100608\n\t"
"s_add_u32 s28, s21, 2949120\n\t"
"v_cvt_pk_bf16_f32 v224, v224, v225\n\t"
"v_cvt_pk_bf16_f32 v225, v226, v227\n\t"
"v_mfma_f32_16x16x32_bf16 v[144:147], v[248:251], a[180:183], v[144:147]\n\t"
"v_mfma_f32_16x16x32_bf16 v[144:147], a[232:235], a[184:187], v[144:147]\n\t"
"buffer_load_dwordx4 v247, s[12:15], s28 offen offset:256 nt lds\n\t"
"v_mfma_f32_16x16x32_bf16 v[144:147], a[236:239], a[188:191], v[144:147]\n\t"
"s_add_u32 m0, s24, 106752\n\t"
"s_add_u32 s28, s21, 3145728\n\t"
"v_mfma_f32_16x16x32_bf16 v[148:151], v[248:251], a[184:187], v[148:151]\n\t"
"buffer_load_dwordx4 v247, s[12:15], s28 offen offset:256 nt lds\n\t"
"v_mfma_f32_16x16x32_bf16 v[148:151], a[232:235], a[188:191], v[148:151]\n\t"
"s_waitcnt lgkmcnt(0)\n\t"
"s_add_u32 m0, s24, 112896\n\t"
"s_add_u32 s28, s21, 3342336\n\t"
"v_mfma_f32_16x16x32_bf16 v[148:151], a[236:239], a[192:195], v[148:151]\n\t"
"buffer_load_dwordx4 v247, s[12:15], s28 offen offset:256 nt lds\n\t"
"s_add_u32 m0, s25, 768\n\t"
"v_mfma_f32_16x16x32_bf16 v[228:231], v[228:231], v[220:223], 0\n\t"
"buffer_load_dwordx4 v254, s[12:15], 0 offen offset:256 nt lds\n\t"
"v_mfma_f32_16x16x32_bf16 v[152:155], v[248:251], a[188:191], v[152:155]\n\t"
"s_add_u32 m0, s26, 768\n\t"
"v_mfma_f32_16x16x32_bf16 v[152:155], a[232:235], a[192:195], v[152:155]\n\t"
"v_mfma_f32_16x16x32_bf16 v[152:155], a[236:239], a[196:199], v[152:155]\n\t"
"s_mov_b64 exec, s[36:37]\n\t"
"buffer_load_dwordx4 v254, s[12:15], s27 offen offset:256 nt lds\n\t"
"s_mov_b64 exec, -1\n\t"
"v_mfma_f32_16x16x32_bf16 v[156:159], v[248:251], a[192:195], v[156:159]\n\t"
"s_waitcnt vmcnt(11)\n\t"
"ds_read_b64_tr_b16 v[236:237], v240 offset:512\n\t"
"v_mfma_f32_16x16x32_bf16 v[156:159], a[232:235], a[196:199], v[156:159]\n\t"
"ds_read_b64_tr_b16 v[238:239], v240 offset:2048\n\t"
"v_cvt_pk_bf16_f32 v226, v228, v229\n\t"
"v_mfma_f32_16x16x32_bf16 v[156:159], a[236:239], a[200:203], v[156:159]\n\t"
"ds_read_b64_tr_b16 v[232:233], v241 offset:512\n\t"
"v_cvt_pk_bf16_f32 v227, v230, v231\n\t"
"v_mfma_f32_16x16x32_bf16 v[160:163], v[248:251], a[196:199], v[160:163]\n\t"
"ds_read_b64_tr_b16 v[234:235], v241 offset:2048\n\t"
"v_mfma_f32_16x16x32_bf16 v[160:163], a[232:235], a[200:203], v[160:163]\n\t"
"v_mfma_f32_16x16x32_bf16 v[160:163], a[236:239], a[204:207], v[160:163]\n\t"
"ds_write2st64_b64 v245, v[224:225], v[226:227] offset0:55 offset1:56\n\t"
"v_mfma_f32_16x16x32_bf16 v[164:167], a[240:243], a[168:171], v[164:167]\n\t"
"v_mfma_f32_16x16x32_bf16 v[164:167], a[244:247], a[172:175], v[164:167]\n\t"
"v_mfma_f32_16x16x32_bf16 v[164:167], a[248:251], a[176:179], v[164:167]\n\t"
"s_waitcnt lgkmcnt(0)\n\t"
"s_barrier\n\t"
"v_mfma_f32_16x16x32_bf16 v[168:171], a[240:243], a[172:175], v[168:171]\n\t"
"ds_read_b64_tr_b16 v[228:229], v240 offset:20480\n\t"
"v_mfma_f32_16x16x32_bf16 v[168:171], a[244:247], a[176:179], v[168:171]\n\t"
"ds_read_b64_tr_b16 v[230:231], v240 offset:22016\n\t"
"v_mfma_f32_16x16x32_bf16 v[168:171], a[248:251], a[180:183], v[168:171]\n\t"
"ds_read_b64_tr_b16 v[224:225], v241 offset:20480\n\t"
"v_mfma_f32_16x16x32_bf16 v[172:175], a[240:243], a[176:179], v[172:175]\n\t"
"s_waitcnt vmcnt(10)\n\t"
"ds_read_b64_tr_b16 v[226:227], v241 offset:22016\n\t"
"v_mfma_f32_16x16x32_bf16 v[172:175], a[244:247], a[180:183], v[172:175]\n\t"
"ds_read_b128 a[168:171], v244 offset:24576\n\t"
"v_mfma_f32_16x16x32_bf16 v[172:175], a[248:251], a[184:187], v[172:175]\n\t"
"ds_read_b128 a[172:175], v244 offset:25088\n\t"
"v_mfma_f32_16x16x32_bf16 v[236:239], v[236:239], a[208:211], 0\n\t"
"ds_read_b128 a[176:179], v244 offset:25600\n\t"
"v_mfma_f32_16x16x32_bf16 v[232:235], v[232:235], a[208:211], 0\n\t"
"v_mfma_f32_16x16x32_bf16 v[176:179], a[240:243], a[180:183], v[176:179]\n\t"
"ds_read_b128 a[180:183], v244 offset:26112\n\t"
"v_mfma_f32_16x16x32_bf16 v[176:179], a[244:247], a[184:187], v[176:179]\n\t"
"v_mfma_f32_16x16x32_bf16 v[176:179], a[248:251], a[188:191], v[176:179]\n\t"
"v_mfma_f32_16x16x32_bf16 v[180:183], a[240:243], a[184:187], v[180:183]\n\t"
"s_waitcnt lgkmcnt(6)\n\t"
"ds_read_b128 a[184:187], v244 offset:26624\n\t"
"v_cvt_pk_bf16_f32 v236, v236, v237\n\t"
"v_mfma_f32_16x16x32_bf16 v[180:183], a[244:247], a[188:191], v[180:183]\n\t"
"s_waitcnt lgkmcnt(5)\n\t"
"v_cvt_pk_bf16_f32 v237, v238, v239\n\t"
"v_cvt_pk_bf16_f32 v238, v232, v233\n\t"
"v_mfma_f32_16x16x32_bf16 v[180:183], a[248:251], a[192:195], v[180:183]\n\t"
"v_cvt_pk_bf16_f32 v239, v234, v235\n\t"
"v_mfma_f32_16x16x32_bf16 v[228:231], v[228:231], a[208:211], 0\n\t"
"v_mfma_f32_16x16x32_bf16 v[224:227], v[224:227], a[208:211], 0\n\t"
"ds_read_b64_tr_b16 v[232:233], v240 offset:40448\n\t"
"v_mfma_f32_16x16x32_bf16 v[184:187], a[240:243], a[188:191], v[184:187]\n\t"
"ds_read_b128 a[188:191], v244 offset:27136\n\t"
"v_mfma_f32_16x16x32_bf16 v[184:187], a[244:247], a[192:195], v[184:187]\n\t"
"ds_read_b64_tr_b16 v[234:235], v240 offset:41984\n\t"
"v_mfma_f32_16x16x32_bf16 v[184:187], a[248:251], a[196:199], v[184:187]\n\t"
"v_mfma_f32_16x16x32_bf16 v[188:191], a[240:243], a[192:195], v[188:191]\n\t"
"ds_read_b128 a[192:195], v244 offset:27648\n\t"
"v_cvt_pk_bf16_f32 v228, v228, v229\n\t"
"v_mfma_f32_16x16x32_bf16 v[188:191], a[244:247], a[196:199], v[188:191]\n\t"
"v_cvt_pk_bf16_f32 v229, v230, v231\n\t"
"v_cvt_pk_bf16_f32 v230, v224, v225\n\t"
"v_mfma_f32_16x16x32_bf16 v[188:191], a[248:251], a[200:203], v[188:191]\n\t"
"v_cvt_pk_bf16_f32 v231, v226, v227\n\t"
"v_mfma_f32_16x16x32_bf16 v[192:195], a[240:243], a[196:199], v[192:195]\n\t"
"s_waitcnt lgkmcnt(6)\n\t"
"ds_read_b64_tr_b16 v[224:225], v241 offset:40448\n\t"
"v_mfma_f32_16x16x32_bf16 v[192:195], a[244:247], a[200:203], v[192:195]\n\t"
"ds_read_b64_tr_b16 v[226:227], v241 offset:41984\n\t"
"v_mfma_f32_16x16x32_bf16 v[192:195], a[248:251], a[204:207], v[192:195]\n\t"
"ds_read_b128 a[196:199], v244 offset:28160\n\t"
"Lst27_%=:\n\t"
"v_mfma_f32_16x16x32_bf16 v[196:199], a[212:215], a[168:171], v[196:199]\n\t"
"s_mov_b64 vcc, -1\n\t"
"s_bitcmp1_b32 s73, 1\n\t"
"s_cbranch_scc1 Lvh4_%=\n\t"
"s_bitcmp1_b32 s73, 0\n\t"
"s_cbranch_scc1 Lvr4_%=\n\t"
"s_branch Lpg25_%=\n\t"
"Lvh4_%=:\n\t"
"s_bitcmp1_b32 s73, 0\n\t"
"s_cbranch_scc1 Lvr4_%=\n\t"
"s_branch Lvr4_%=\n\t"
"Lvr4_%=:\n\t"
"s_mov_b64 vcc, 0\n\t"
"s_waitcnt lgkmcnt(8)\n\t"
"ds_read_b128 a[200:203], v244 offset:28672\n\t"
"v_mfma_f32_16x16x32_bf16 v[196:199], a[216:219], a[172:175], v[196:199]\n\t"
"ds_read_b128 a[204:207], v244 offset:29184\n\t"
"v_mfma_f32_16x16x32_bf16 v[196:199], a[252:255], a[176:179], v[196:199]\n\t"
"buffer_load_dwordx4 v[248:251], v252, s[8:11], s20 offen offset:1024 sc1\n\t"
"v_mfma_f32_16x16x32_bf16 v[204:207], a[212:215], a[172:175], v[204:207]\n\t"
"s_waitcnt lgkmcnt(6)\n\t"
"v_mfma_f32_16x16x32_bf16 v[204:207], a[216:219], a[176:179], v[204:207]\n\t"
"s_waitcnt lgkmcnt(3)\n\t"
"v_mfma_f32_16x16x32_bf16 v[204:207], a[252:255], a[180:183], v[204:207]\n\t"
"v_mfma_f32_16x16x32_bf16 v[232:235], v[232:235], a[208:211], 0\n\t"
"v_add_u32 v240, 59904, v240\n\t"
"v_add_u32 v241, 59904, v241\n\t"
"v_mfma_f32_16x16x32_bf16 v[224:227], v[224:227], a[208:211], 0\n\t"
"v_mfma_f32_16x16x32_bf16 v[212:215], a[212:215], a[176:179], v[212:215]\n\t"
"v_mfma_f32_16x16x32_bf16 v[212:215], a[216:219], a[180:183], v[212:215]\n\t"
"v_mfma_f32_16x16x32_bf16 v[212:215], a[252:255], a[184:187], v[212:215]\n\t"
"s_barrier\n\t"
"v_mfma_f32_16x16x32_bf16 a[0:3], a[212:215], a[180:183], a[0:3]\n\t"
"v_cvt_pk_bf16_f32 v232, v232, v233\n\t"
"v_mfma_f32_16x16x32_bf16 a[0:3], a[216:219], a[184:187], a[0:3]\n\t"
"v_cvt_pk_bf16_f32 v233, v234, v235\n\t"
"v_cvt_pk_bf16_f32 v234, v224, v225\n\t"
"v_mfma_f32_16x16x32_bf16 a[0:3], a[252:255], a[188:191], a[0:3]\n\t"
"ds_write_b128 v243, v[236:239] offset:512\n\t"
"v_mfma_f32_16x16x32_bf16 a[8:11], a[212:215], a[184:187], a[8:11]\n\t"
"s_waitcnt lgkmcnt(3)\n\t"
"v_cvt_pk_bf16_f32 v235, v226, v227\n\t"
"v_mfma_f32_16x16x32_bf16 a[8:11], a[216:219], a[188:191], a[8:11]\n\t"
"ds_read_b64_tr_b16 v[236:237], v240 offset:512\n\t"
"v_mfma_f32_16x16x32_bf16 a[8:11], a[252:255], a[192:195], a[8:11]\n\t"
"ds_write_b128 v243, v[228:231] offset:9728\n\t"
"v_mfma_f32_16x16x32_bf16 a[16:19], a[212:215], a[188:191], a[16:19]\n\t"
"buffer_load_dwordx4 a[232:235], v252, s[8:11], s20 offen offset:2048 sc1\n\t"
"v_mfma_f32_16x16x32_bf16 a[16:19], a[216:219], a[192:195], a[16:19]\n\t"
"s_waitcnt lgkmcnt(4)\n\t"
"ds_read_b64_tr_b16 v[238:239], v240 offset:2048\n\t"
"v_mfma_f32_16x16x32_bf16 a[16:19], a[252:255], a[196:199], a[16:19]\n\t"
"s_mov_b32 s20, 1032192\n\t"
"ds_write_b128 v243, v[232:235] offset:18944\n\t"
"v_mfma_f32_16x16x32_bf16 a[24:27], a[212:215], a[192:195], a[24:27]\n\t"
"buffer_load_dwordx4 a[236:239], v252, s[8:11], s20 offen offset:0 sc1\n\t"
"v_mfma_f32_16x16x32_bf16 a[24:27], a[216:219], a[196:199], a[24:27]\n\t"
"s_waitcnt lgkmcnt(5)\n\t"
"ds_read_b64_tr_b16 v[224:225], v241 offset:512\n\t"
"v_mfma_f32_16x16x32_bf16 a[24:27], a[252:255], a[200:203], a[24:27]\n\t"
"ds_read_b64_tr_b16 v[226:227], v241 offset:2048\n\t"
"v_mfma_f32_16x16x32_bf16 a[32:35], a[212:215], a[196:199], a[32:35]\n\t"
"s_waitcnt lgkmcnt(3)\n\t"
"ds_read_b64_tr_b16 v[228:229], v240 offset:20480\n\t"
"v_mfma_f32_16x16x32_bf16 a[32:35], a[216:219], a[200:203], a[32:35]\n\t"
"ds_read_b64_tr_b16 v[230:231], v240 offset:22016\n\t"
"v_mfma_f32_16x16x32_bf16 a[32:35], a[252:255], a[204:207], a[32:35]\n\t"
"s_waitcnt vmcnt(12)\n\t"
"ds_read_b64_tr_b16 v[232:233], v241 offset:20480\n\t"
"v_mfma_f32_16x16x32_bf16 v[236:239], v[236:239], a[208:211], 0\n\t"
"s_waitcnt lgkmcnt(3)\n\t"
"ds_read_b64_tr_b16 v[234:235], v241 offset:22016\n\t"
"v_mfma_f32_16x16x32_bf16 v[224:227], v[224:227], a[208:211], 0\n\t"
"buffer_load_dwordx4 a[212:215], v252, s[8:11], s20 offen offset:1024 sc1\n\t"
"v_mfma_f32_16x16x32_bf16 a[40:43], a[220:223], a[168:171], a[40:43]\n\t"
"buffer_load_dwordx4 a[216:219], v252, s[8:11], s20 offen offset:2048 sc1\n\t"
"v_mfma_f32_16x16x32_bf16 a[40:43], v[96:99], a[172:175], a[40:43]\n\t"
"s_mov_b32 s20, 1044480\n\t"
"v_mfma_f32_16x16x32_bf16 a[40:43], a[224:227], a[176:179], a[40:43]\n\t"
"v_mfma_f32_16x16x32_bf16 a[48:51], a[220:223], a[172:175], a[48:51]\n\t"
"s_waitcnt lgkmcnt(2)\n\t"
"v_cvt_pk_bf16_f32 v236, v236, v237\n\t"
"v_cvt_pk_bf16_f32 v237, v238, v239\n\t"
"v_mfma_f32_16x16x32_bf16 a[48:51], v[96:99], a[176:179], a[48:51]\n\t"
"s_waitcnt lgkmcnt(0)\n\t"
"v_cvt_pk_bf16_f32 v238, v224, v225\n\t"
"v_cvt_pk_bf16_f32 v239, v226, v227\n\t"
"v_mfma_f32_16x16x32_bf16 a[48:51], a[224:227], a[180:183], a[48:51]\n\t"
"ds_write_b128 v243, v[236:239] offset:28160\n\t"
"v_mfma_f32_16x16x32_bf16 v[228:231], v[228:231], a[208:211], 0\n\t"
"buffer_load_dwordx4 a[240:243], v252, s[8:11], s20 offen offset:0 sc1\n\t"
"v_mfma_f32_16x16x32_bf16 v[232:235], v[232:235], a[208:211], 0\n\t"
"ds_read_b64_tr_b16 v[224:225], v240 offset:40448\n\t"
"v_mfma_f32_16x16x32_bf16 a[56:59], a[220:223], a[176:179], a[56:59]\n\t"
"buffer_load_dwordx4 a[244:247], v252, s[8:11], s20 offen offset:1024 sc1\n\t"
"v_mfma_f32_16x16x32_bf16 a[56:59], v[96:99], a[180:183], a[56:59]\n\t"
"ds_read_b64_tr_b16 v[226:227], v240 offset:41984\n\t"
"v_mfma_f32_16x16x32_bf16 a[56:59], a[224:227], a[184:187], a[56:59]\n\t"
"ds_read_b64_tr_b16 v[236:237], v241 offset:40448\n\t"
"v_mfma_f32_16x16x32_bf16 a[64:67], a[220:223], a[180:183], a[64:67]\n\t"
"ds_read_b64_tr_b16 v[238:239], v241 offset:41984\n\t"
"v_cvt_pk_bf16_f32 v228, v228, v229\n\t"
"v_mfma_f32_16x16x32_bf16 a[64:67], v[96:99], a[184:187], a[64:67]\n\t"
"v_cvt_pk_bf16_f32 v229, v230, v231\n\t"
"v_cvt_pk_bf16_f32 v230, v232, v233\n\t"
"v_mfma_f32_16x16x32_bf16 a[64:67], a[224:227], a[188:191], a[64:67]\n\t"
"v_cvt_pk_bf16_f32 v231, v234, v235\n\t"
"v_mfma_f32_16x16x32_bf16 a[72:75], a[220:223], a[184:187], a[72:75]\n\t"
"buffer_load_dwordx4 a[248:251], v252, s[8:11], s20 offen offset:2048 sc1\n\t"
"v_mfma_f32_16x16x32_bf16 a[72:75], v[96:99], a[188:191], a[72:75]\n\t"
"s_mov_b32 s20, 1056768\n\t"
"v_mfma_f32_16x16x32_bf16 a[72:75], a[224:227], a[192:195], a[72:75]\n\t"
"ds_write_b128 v243, v[228:231] offset:37376\n\t"
"v_mfma_f32_16x16x32_bf16 a[80:83], a[220:223], a[188:191], a[80:83]\n\t"
"buffer_load_dwordx4 a[252:255], v252, s[8:11], s20 offen offset:0 sc1\n\t"
"v_mfma_f32_16x16x32_bf16 a[80:83], v[96:99], a[192:195], a[80:83]\n\t"
"v_mfma_f32_16x16x32_bf16 a[80:83], a[224:227], a[196:199], a[80:83]\n\t"
"v_mfma_f32_16x16x32_bf16 a[88:91], a[220:223], a[192:195], a[88:91]\n\t"
"s_waitcnt lgkmcnt(3)\n\t"
"v_mfma_f32_16x16x32_bf16 a[88:91], v[96:99], a[196:199], a[88:91]\n\t"
"s_waitcnt lgkmcnt(1)\n\t"
"v_mfma_f32_16x16x32_bf16 a[88:91], a[224:227], a[200:203], a[88:91]\n\t"
"v_mfma_f32_16x16x32_bf16 v[224:227], v[224:227], a[208:211], 0\n\t"
"v_mfma_f32_16x16x32_bf16 v[236:239], v[236:239], a[208:211], 0\n\t"
"ds_read_b64_tr_b16 v[232:233], v246 offset:1024\n\t"
"v_mfma_f32_16x16x32_bf16 a[96:99], a[220:223], a[196:199], a[96:99]\n\t"
"buffer_load_dwordx4 a[220:223], v252, s[8:11], s20 offen offset:1024 sc1\n\t"
"v_mfma_f32_16x16x32_bf16 a[96:99], v[96:99], a[200:203], a[96:99]\n\t"
"s_waitcnt vmcnt(8)\n\t"
"ds_read_b64_tr_b16 v[234:235], v246 offset:4096\n\t"
"v_mfma_f32_16x16x32_bf16 a[96:99], a[224:227], a[204:207], a[96:99]\n\t"
"v_mfma_f32_16x16x32_bf16 a[104:107], a[228:231], a[168:171], a[104:107]\n\t"
"buffer_load_dwordx4 v[96:99], v252, s[8:11], s20 offen offset:2048 sc1\n\t"
"v_mfma_f32_16x16x32_bf16 a[104:107], v[248:251], a[172:175], a[104:107]\n\t"
"s_mov_b32 s20, 1290240\n\t"
"ds_read_b64_tr_b16 v[228:229], v246 offset:7168\n\t"
"v_cvt_pk_bf16_f32 v224, v224, v225\n\t"
"v_mfma_f32_16x16x32_bf16 a[104:107], a[232:235], a[176:179], a[104:107]\n\t"
"ds_read_b64_tr_b16 v[230:231], v246 offset:10240\n\t"
"v_cvt_pk_bf16_f32 v225, v226, v227\n\t"
"v_mfma_f32_16x16x32_bf16 a[112:115], a[228:231], a[172:175], a[112:115]\n\t"
"ds_read_b128 a[168:171], v244 offset:33792\n\t"
"v_cvt_pk_bf16_f32 v226, v236, v237\n\t"
"v_mfma_f32_16x16x32_bf16 a[112:115], v[248:251], a[176:179], a[112:115]\n\t"
"ds_read_b128 a[172:175], v244 offset:34304\n\t"
"v_cvt_pk_bf16_f32 v227, v238, v239\n\t"
"v_mfma_f32_16x16x32_bf16 a[112:115], a[232:235], a[180:183], a[112:115]\n\t"
"ds_write_b128 v243, v[224:227] offset:46592\n\t"
"v_mfma_f32_16x16x32_bf16 a[120:123], a[228:231], a[176:179], a[120:123]\n\t"
"buffer_load_dwordx4 a[224:227], v252, s[8:11], s20 offen offset:0 sc1\n\t"
"v_mfma_f32_16x16x32_bf16 a[120:123], v[248:251], a[180:183], a[120:123]\n\t"
"ds_read_b64_tr_b16 v[236:237], v246 offset:13312\n\t"
"v_mfma_f32_16x16x32_bf16 a[120:123], a[232:235], a[184:187], a[120:123]\n\t"
"ds_read_b64_tr_b16 v[238:239], v246 offset:16384\n\t"
"v_mfma_f32_16x16x32_bf16 a[128:131], a[228:231], a[180:183], a[128:131]\n\t"
"s_waitcnt lgkmcnt(7)\n\t"
"ds_read_b128 a[176:179], v244 offset:34816\n\t"
"v_mfma_f32_16x16x32_bf16 a[128:131], v[248:251], a[184:187], a[128:131]\n\t"
"ds_read_b64_tr_b16 v[224:225], v246 offset:20992\n\t"
"v_mfma_f32_16x16x32_bf16 a[128:131], a[232:235], a[188:191], a[128:131]\n\t"
"ds_read_b64_tr_b16 v[226:227], v246 offset:24064\n\t"
"v_mfma_f32_16x16x32_bf16 v[232:235], v[232:235], v[220:223], 0\n\t"
"ds_read_b128 a[180:183], v244 offset:35328\n\t"
"v_mfma_f32_16x16x32_bf16 a[136:139], a[228:231], a[184:187], a[136:139]\n\t"
"s_waitcnt lgkmcnt(9)\n\t"
"ds_read_b128 a[184:187], v244 offset:35840\n\t"
"v_mfma_f32_16x16x32_bf16 a[136:139], v[248:251], a[188:191], a[136:139]\n\t"
"v_mfma_f32_16x16x32_bf16 a[136:139], a[232:235], a[192:195], a[136:139]\n\t"
"v_mfma_f32_16x16x32_bf16 v[228:231], v[228:231], v[220:223], 0\n\t"
"v_mfma_f32_16x16x32_bf16 a[144:147], a[228:231], a[188:191], a[144:147]\n\t"
"s_waitcnt lgkmcnt(5)\n\t"
"ds_read_b128 a[188:191], v244 offset:36352\n\t"
"v_cvt_pk_bf16_f32 v232, v232, v233\n\t"
"v_mfma_f32_16x16x32_bf16 a[144:147], v[248:251], a[192:195], a[144:147]\n\t"
"v_cvt_pk_bf16_f32 v233, v234, v235\n\t"
"v_mfma_f32_16x16x32_bf16 a[144:147], a[232:235], a[196:199], a[144:147]\n\t"
"v_mfma_f32_16x16x32_bf16 v[236:239], v[236:239], v[220:223], 0\n\t"
"v_mfma_f32_16x16x32_bf16 a[152:155], a[228:231], a[192:195], a[152:155]\n\t"
"s_waitcnt lgkmcnt(3)\n\t"
"ds_read_b128 a[192:195], v244 offset:36864\n\t"
"v_cvt_pk_bf16_f32 v234, v228, v229\n\t"
"v_mfma_f32_16x16x32_bf16 a[152:155], v[248:251], a[196:199], a[152:155]\n\t"
"v_cvt_pk_bf16_f32 v235, v230, v231\n\t"
"v_mfma_f32_16x16x32_bf16 a[152:155], a[232:235], a[200:203], a[152:155]\n\t"
"ds_read_b64_tr_b16 v[228:229], v246 offset:27136\n\t"
"v_mfma_f32_16x16x32_bf16 v[224:227], v[224:227], v[220:223], 0\n\t"
"ds_read_b64_tr_b16 v[230:231], v246 offset:30208\n\t"
"v_mfma_f32_16x16x32_bf16 a[160:163], a[228:231], a[196:199], a[160:163]\n\t"
"buffer_load_dwordx4 a[228:231], v252, s[8:11], s20 offen offset:1024 sc1\n\t"
"v_mfma_f32_16x16x32_bf16 a[160:163], v[248:251], a[200:203], a[160:163]\n\t"
"s_waitcnt vmcnt(8)\n\t"
"ds_read_b128 a[196:199], v244 offset:37376\n\t"
"v_cvt_pk_bf16_f32 v236, v236, v237\n\t"
"v_mfma_f32_16x16x32_bf16 a[160:163], a[232:235], a[204:207], a[160:163]\n\t"
"ds_read_b128 a[200:203], v244 offset:37888\n\t"
"v_cvt_pk_bf16_f32 v237, v238, v239\n\t"
"v_mfma_f32_16x16x32_bf16 v[200:203], a[236:239], a[168:171], v[200:203]\n\t"
"ds_read_b128 a[204:207], v244 offset:38400\n\t"
"v_mfma_f32_16x16x32_bf16 v[200:203], a[212:215], a[172:175], v[200:203]\n\t"
"buffer_load_dwordx4 v[248:251], v252, s[8:11], s20 offen offset:2048 sc1\n\t"
"v_mfma_f32_16x16x32_bf16 v[200:203], a[216:219], a[176:179], v[200:203]\n\t"
"s_waitcnt lgkmcnt(3)\n\t"
"s_mov_b32 s20, 1302528\n\t"
"v_cvt_pk_bf16_f32 v238, v224, v225\n\t"
"v_mfma_f32_16x16x32_bf16 v[228:231], v[228:231], v[220:223], 0\n\t"
"v_cvt_pk_bf16_f32 v239, v226, v227\n\t"
"v_mfma_f32_16x16x32_bf16 v[208:211], a[236:239], a[172:175], v[208:211]\n\t"
"buffer_load_dwordx4 a[232:235], v252, s[8:11], s20 offen offset:0 sc1\n\t"
"v_mfma_f32_16x16x32_bf16 v[208:211], a[212:215], a[176:179], v[208:211]\n\t"
"v_mfma_f32_16x16x32_bf16 v[208:211], a[216:219], a[180:183], v[208:211]\n\t"
"s_waitcnt lgkmcnt(0)\n\t"
"s_barrier\n\t"
"v_mfma_f32_16x16x32_bf16 v[216:219], a[236:239], a[176:179], v[216:219]\n\t"
"ds_read_b64_tr_b16 v[224:225], v246 offset:33280\n\t"
"v_mfma_f32_16x16x32_bf16 v[216:219], a[212:215], a[180:183], v[216:219]\n\t"
"ds_read_b64_tr_b16 v[226:227], v246 offset:36352\n\t"
"v_cvt_pk_bf16_f32 v228, v228, v229\n\t"
"v_mfma_f32_16x16x32_bf16 v[216:219], a[216:219], a[184:187], v[216:219]\n\t"
"ds_write2st64_b64 v245, v[232:233], v[234:235] offset0:48 offset1:49\n\t"
"v_mfma_f32_16x16x32_bf16 a[4:7], a[236:239], a[180:183], a[4:7]\n\t"
"v_cvt_pk_bf16_f32 v229, v230, v231\n\t"
"v_mfma_f32_16x16x32_bf16 a[4:7], a[212:215], a[184:187], a[4:7]\n\t"
"ds_read_b64_tr_b16 v[232:233], v246 offset:40960\n\t"
"v_mfma_f32_16x16x32_bf16 a[4:7], a[216:219], a[188:191], a[4:7]\n\t"
"ds_write2st64_b64 v245, v[236:237], v[238:239] offset0:50 offset1:51\n\t"
"v_mfma_f32_16x16x32_bf16 a[12:15], a[236:239], a[184:187], a[12:15]\n\t"
"v_mfma_f32_16x16x32_bf16 a[12:15], a[212:215], a[188:191], a[12:15]\n\t"
"ds_read_b64_tr_b16 v[234:235], v246 offset:44032\n\t"
"v_mfma_f32_16x16x32_bf16 a[12:15], a[216:219], a[192:195], a[12:15]\n\t"
"ds_write_b64 v245, v[228:229] offset:26624\n\t"
"v_mfma_f32_16x16x32_bf16 a[20:23], a[236:239], a[188:191], a[20:23]\n\t"
"s_waitcnt lgkmcnt(5)\n\t"
"ds_read_b64_tr_b16 v[228:229], v246 offset:47104\n\t"
"v_mfma_f32_16x16x32_bf16 a[20:23], a[212:215], a[192:195], a[20:23]\n\t"
"ds_read_b64_tr_b16 v[230:231], v246 offset:50176\n\t"
"v_mfma_f32_16x16x32_bf16 a[20:23], a[216:219], a[196:199], a[20:23]\n\t"
"s_waitcnt vmcnt(7)\n\t"
"v_mfma_f32_16x16x32_bf16 v[224:227], v[224:227], v[220:223], 0\n\t"
"Lpg20_%=:\n\t"
"s_cbranch_vccnz Lvr2_%=\n\t"
"v_mfma_f32_16x16x32_bf16 a[28:31], a[236:239], a[192:195], a[28:31]\n\t"
"s_waitcnt lgkmcnt(3)\n\t"
"v_mfma_f32_16x16x32_bf16 a[28:31], a[212:215], a[196:199], a[28:31]\n\t"
"v_mfma_f32_16x16x32_bf16 a[28:31], a[216:219], a[200:203], a[28:31]\n\t"
"v_mfma_f32_16x16x32_bf16 v[232:235], v[232:235], v[220:223], 0\n\t"
"v_mfma_f32_16x16x32_bf16 a[36:39], a[236:239], a[196:199], a[36:39]\n\t"
"buffer_load_dwordx4 a[236:239], v252, s[8:11], s20 offen offset:1024 sc1\n\t"
"v_mfma_f32_16x16x32_bf16 a[36:39], a[212:215], a[200:203], a[36:39]\n\t"
"s_waitcnt lgkmcnt(0)\n\t"
"v_cvt_pk_bf16_f32 v224, v224, v225\n\t"
"v_cvt_pk_bf16_f32 v225, v226, v227\n\t"
"v_mfma_f32_16x16x32_bf16 a[36:39], a[216:219], a[204:207], a[36:39]\n\t"
"v_mfma_f32_16x16x32_bf16 v[228:231], v[228:231], v[220:223], 0\n\t"
"buffer_load_dwordx4 a[212:215], v252, s[8:11], s20 offen offset:2048 sc1\n\t"
"v_mfma_f32_16x16x32_bf16 a[44:47], a[240:243], a[168:171], a[44:47]\n\t"
"s_mov_b32 s20, 1314816\n\t"
"v_cvt_pk_bf16_f32 v226, v232, v233\n\t"
"v_cvt_pk_bf16_f32 v227, v234, v235\n\t"
"v_mfma_f32_16x16x32_bf16 a[44:47], a[244:247], a[172:175], a[44:47]\n\t"
"ds_read_b64_tr_b16 v[232:233], v246 offset:53248\n\t"
"v_mfma_f32_16x16x32_bf16 a[44:47], a[248:251], a[176:179], a[44:47]\n\t"
"buffer_load_dwordx4 a[216:219], v252, s[8:11], s20 offen offset:0 sc1\n\t"
"v_mfma_f32_16x16x32_bf16 a[52:55], a[240:243], a[172:175], a[52:55]\n\t"
"ds_read_b64_tr_b16 v[234:235], v246 offset:56320\n\t"
"v_mfma_f32_16x16x32_bf16 a[52:55], a[244:247], a[176:179], a[52:55]\n\t"
"v_cvt_pk_bf16_f32 v228, v228, v229\n\t"
"v_cvt_pk_bf16_f32 v229, v230, v231\n\t"
"v_mfma_f32_16x16x32_bf16 a[52:55], a[248:251], a[180:183], a[52:55]\n\t"
"ds_write2st64_b64 v245, v[224:225], v[226:227] offset0:53 offset1:54\n\t"
"v_mfma_f32_16x16x32_bf16 a[60:63], a[240:243], a[176:179], a[60:63]\n\t"
"v_mfma_f32_16x16x32_bf16 a[60:63], a[244:247], a[180:183], a[60:63]\n\t"
"v_add_u32 v243, 55296, v243\n\t"
"v_mfma_f32_16x16x32_bf16 a[60:63], a[248:251], a[184:187], a[60:63]\n\t"
"v_mfma_f32_16x16x32_bf16 a[68:71], a[240:243], a[180:183], a[68:71]\n\t"
"v_mfma_f32_16x16x32_bf16 a[68:71], a[244:247], a[184:187], a[68:71]\n\t"
"v_mfma_f32_16x16x32_bf16 a[68:71], a[248:251], a[188:191], a[68:71]\n\t"
"v_mfma_f32_16x16x32_bf16 a[76:79], a[240:243], a[184:187], a[76:79]\n\t"
"s_waitcnt lgkmcnt(1)\n\t"
"v_mfma_f32_16x16x32_bf16 a[76:79], a[244:247], a[188:191], a[76:79]\n\t"
"v_mfma_f32_16x16x32_bf16 a[76:79], a[248:251], a[192:195], a[76:79]\n\t"
"v_mfma_f32_16x16x32_bf16 v[232:235], v[232:235], v[220:223], 0\n\t"
"v_mfma_f32_16x16x32_bf16 a[84:87], a[240:243], a[188:191], a[84:87]\n\t"
"v_mfma_f32_16x16x32_bf16 a[84:87], a[244:247], a[192:195], a[84:87]\n\t"
"v_mfma_f32_16x16x32_bf16 a[84:87], a[248:251], a[196:199], a[84:87]\n\t"
"v_mfma_f32_16x16x32_bf16 a[92:95], a[240:243], a[192:195], a[92:95]\n\t"
"v_mfma_f32_16x16x32_bf16 a[92:95], a[244:247], a[196:199], a[92:95]\n\t"
"v_cvt_pk_bf16_f32 v230, v232, v233\n\t"
"v_cvt_pk_bf16_f32 v231, v234, v235\n\t"
"v_mfma_f32_16x16x32_bf16 a[92:95], a[248:251], a[200:203], a[92:95]\n\t"
"ds_write2st64_b64 v245, v[228:229], v[230:231] offset0:55 offset1:56\n\t"
"v_mfma_f32_16x16x32_bf16 a[100:103], a[240:243], a[196:199], a[100:103]\n\t"
"buffer_load_dwordx4 a[240:243], v252, s[8:11], s20 offen offset:1024 sc1\n\t"
"v_mfma_f32_16x16x32_bf16 a[100:103], a[244:247], a[200:203], a[100:103]\n\t"
"s_waitcnt vmcnt(8)\n\t"
"v_mfma_f32_16x16x32_bf16 a[100:103], a[248:251], a[204:207], a[100:103]\n\t"
"v_mfma_f32_16x16x32_bf16 a[108:111], a[252:255], a[168:171], a[108:111]\n\t"
"buffer_load_dwordx4 a[244:247], v252, s[8:11], s20 offen offset:2048 sc1\n\t"
"v_mfma_f32_16x16x32_bf16 a[108:111], a[220:223], a[172:175], a[108:111]\n\t"
"s_mov_b32 s20, 1327104\n\t"
"v_mfma_f32_16x16x32_bf16 a[108:111], v[96:99], a[176:179], a[108:111]\n\t"
"s_waitcnt lgkmcnt(0)\n\t"
"s_barrier\n\t"
"v_mfma_f32_16x16x32_bf16 a[116:119], a[252:255], a[172:175], a[116:119]\n\t"
"ds_read_b64_tr_b16 v[236:237], v240 offset:1024\n\t"
"v_mfma_f32_16x16x32_bf16 a[116:119], a[220:223], a[176:179], a[116:119]\n\t"
"ds_read_b64_tr_b16 v[238:239], v240 offset:2560\n\t"
"v_mfma_f32_16x16x32_bf16 a[116:119], v[96:99], a[180:183], a[116:119]\n\t"
"ds_read_b64_tr_b16 v[224:225], v241 offset:1024\n\t"
"v_mfma_f32_16x16x32_bf16 a[124:127], a[252:255], a[176:179], a[124:127]\n\t"
"ds_read_b64_tr_b16 v[226:227], v241 offset:2560\n\t"
"v_mfma_f32_16x16x32_bf16 a[124:127], a[220:223], a[180:183], a[124:127]\n\t"
"buffer_load_dwordx4 a[248:251], v252, s[8:11], s20 offen offset:0 sc1\n\t"
"v_mfma_f32_16x16x32_bf16 a[124:127], v[96:99], a[184:187], a[124:127]\n\t"
"ds_read_b64_tr_b16 v[232:233], v240 offset:20992\n\t"
"v_mfma_f32_16x16x32_bf16 a[132:135], a[252:255], a[180:183], a[132:135]\n\t"
"s_waitcnt lgkmcnt(3)\n\t"
"ds_read_b64_tr_b16 v[234:235], v240 offset:22528\n\t"
"v_mfma_f32_16x16x32_bf16 a[132:135], a[220:223], a[184:187], a[132:135]\n\t"
"ds_read_b64_tr_b16 v[228:229], v241 offset:20992\n\t"
"v_mfma_f32_16x16x32_bf16 a[132:135], v[96:99], a[188:191], a[132:135]\n\t"
"s_waitcnt lgkmcnt(3)\n\t"
"ds_read_b64_tr_b16 v[230:231], v241 offset:22528\n\t"
"v_mfma_f32_16x16x32_bf16 v[236:239], v[236:239], a[208:211], 0\n\t"
"ds_read_b128 a[168:171], v244 offset:24576\n\t"
"v_mfma_f32_16x16x32_bf16 v[224:227], v[224:227], a[208:211], 0\n\t"
"ds_read_b128 a[172:175], v244 offset:25088\n\t"
"v_mfma_f32_16x16x32_bf16 a[140:143], a[252:255], a[184:187], a[140:143]\n\t"
"ds_read_b128 a[176:179], v244 offset:25600\n\t"
"v_mfma_f32_16x16x32_bf16 a[140:143], a[220:223], a[188:191], a[140:143]\n\t"
"ds_read_b128 a[180:183], v244 offset:26112\n\t"
"v_mfma_f32_16x16x32_bf16 a[140:143], v[96:99], a[192:195], a[140:143]\n\t"
"ds_read_b128 a[184:187], v244 offset:26624\n\t"
"v_mfma_f32_16x16x32_bf16 a[148:151], a[252:255], a[188:191], a[148:151]\n\t"
"s_waitcnt lgkmcnt(7)\n\t"
"ds_read_b128 a[188:191], v244 offset:27136\n\t"
"v_cvt_pk_bf16_f32 v236, v236, v237\n\t"
"v_mfma_f32_16x16x32_bf16 a[148:151], a[220:223], a[192:195], a[148:151]\n\t"
"s_waitcnt lgkmcnt(6)\n\t"
"v_cvt_pk_bf16_f32 v237, v238, v239\n\t"
"v_cvt_pk_bf16_f32 v238, v224, v225\n\t"
"v_mfma_f32_16x16x32_bf16 a[148:151], v[96:99], a[196:199], a[148:151]\n\t"
"v_cvt_pk_bf16_f32 v239, v226, v227\n\t"
"v_mfma_f32_16x16x32_bf16 v[232:235], v[232:235], a[208:211], 0\n\t"
"ds_write_b128 v243, v[236:239] offset:28160\n\t"
"v_mfma_f32_16x16x32_bf16 v[228:231], v[228:231], a[208:211], 0\n\t"
"v_mfma_f32_16x16x32_bf16 a[156:159], a[252:255], a[192:195], a[156:159]\n\t"
"ds_read_b128 a[192:195], v244 offset:27648\n\t"
"v_mfma_f32_16x16x32_bf16 a[156:159], a[220:223], a[196:199], a[156:159]\n\t"
"ds_read_b64_tr_b16 v[224:225], v240 offset:40960\n\t"
"v_mfma_f32_16x16x32_bf16 a[156:159], v[96:99], a[200:203], a[156:159]\n\t"
"ds_read_b64_tr_b16 v[226:227], v240 offset:42496\n\t"
"v_mfma_f32_16x16x32_bf16 a[164:167], a[252:255], a[196:199], a[164:167]\n\t"
"buffer_load_dwordx4 a[252:255], v252, s[8:11], s20 offen offset:1024 sc1\n\t"
"v_mfma_f32_16x16x32_bf16 a[164:167], a[220:223], a[200:203], a[164:167]\n\t"
"s_waitcnt vmcnt(8) lgkmcnt(7)\n\t"
"ds_read_b64_tr_b16 v[236:237], v241 offset:40960\n\t"
"v_cvt_pk_bf16_f32 v232, v232, v233\n\t"
"v_mfma_f32_16x16x32_bf16 a[164:167], v[96:99], a[204:207], a[164:167]\n\t"
"ds_read_b64_tr_b16 v[238:239], v241 offset:42496\n\t"
"v_cvt_pk_bf16_f32 v233, v234, v235\n\t"
"Lst28_%=:\n\t"
"v_mfma_f32_16x16x32_bf16 v[196:199], a[224:227], a[168:171], v[196:199]\n\t"
"s_waitcnt lgkmcnt(8)\n\t"
"ds_read_b128 a[196:199], v244 offset:28160\n\t"
"v_cvt_pk_bf16_f32 v234, v228, v229\n\t"
"v_mfma_f32_16x16x32_bf16 v[196:199], a[228:231], a[172:175], v[196:199]\n\t"
"ds_read_b128 a[200:203], v244 offset:28672\n\t"
"v_cvt_pk_bf16_f32 v235, v230, v231\n\t"
"v_mfma_f32_16x16x32_bf16 v[196:199], v[248:251], a[176:179], v[196:199]\n\t"
"ds_write_b128 v243, v[232:235] offset:37376\n\t"
"v_mfma_f32_16x16x32_bf16 v[204:207], a[224:227], a[172:175], v[204:207]\n\t"
"buffer_load_dwordx4 v[96:99], v252, s[8:11], s20 offen offset:2048 sc1\n\t"
"v_mfma_f32_16x16x32_bf16 v[204:207], a[228:231], a[176:179], v[204:207]\n\t"
"s_waitcnt lgkmcnt(10)\n\t"
"ds_read_b128 a[204:207], v244 offset:29184\n\t"
"v_mfma_f32_16x16x32_bf16 v[204:207], v[248:251], a[180:183], v[204:207]\n\t"
"s_mov_b32 s20, 1339392\n\t"
"v_subrev_u32 v240, 59904, v240\n\t"
"v_subrev_u32 v241, 59904, v241\n\t"
"v_mfma_f32_16x16x32_bf16 v[212:215], a[224:227], a[176:179], v[212:215]\n\t"
"s_waitcnt lgkmcnt(6)\n\t"
"ds_read_b64_tr_b16 v[228:229], v240 offset:1024\n\t"
"v_mfma_f32_16x16x32_bf16 v[212:215], a[228:231], a[180:183], v[212:215]\n\t"
"buffer_load_dwordx4 a[220:223], v252, s[8:11], s20 offen offset:0 sc1\n\t"
"v_mfma_f32_16x16x32_bf16 v[212:215], v[248:251], a[184:187], v[212:215]\n\t"
"s_waitcnt lgkmcnt(5)\n\t"
"ds_read_b64_tr_b16 v[230:231], v240 offset:2560\n\t"
"v_mfma_f32_16x16x32_bf16 v[224:227], v[224:227], a[208:211], 0\n\t"
"ds_read_b64_tr_b16 v[232:233], v241 offset:1024\n\t"
"v_mfma_f32_16x16x32_bf16 v[236:239], v[236:239], a[208:211], 0\n\t"
"ds_read_b64_tr_b16 v[234:235], v241 offset:2560\n\t"
"v_mfma_f32_16x16x32_bf16 a[0:3], a[224:227], a[180:183], a[0:3]\n\t"
"v_mfma_f32_16x16x32_bf16 a[0:3], a[228:231], a[184:187], a[0:3]\n\t"
"v_mfma_f32_16x16x32_bf16 a[0:3], v[248:251], a[188:191], a[0:3]\n\t"
"v_mfma_f32_16x16x32_bf16 a[8:11], a[224:227], a[184:187], a[8:11]\n\t"
"s_waitcnt lgkmcnt(2)\n\t"
"v_cvt_pk_bf16_f32 v224, v224, v225\n\t"
"v_cvt_pk_bf16_f32 v225, v226, v227\n\t"
"v_mfma_f32_16x16x32_bf16 a[8:11], a[228:231], a[188:191], a[8:11]\n\t"
"s_waitcnt lgkmcnt(0)\n\t"
"v_cvt_pk_bf16_f32 v226, v236, v237\n\t"
"v_cvt_pk_bf16_f32 v227, v238, v239\n\t"
"v_mfma_f32_16x16x32_bf16 a[8:11], v[248:251], a[192:195], a[8:11]\n\t"
"ds_write_b128 v243, v[224:227] offset:46592\n\t"
"v_mfma_f32_16x16x32_bf16 v[228:231], v[228:231], a[208:211], 0\n\t"
"v_mfma_f32_16x16x32_bf16 v[232:235], v[232:235], a[208:211], 0\n\t"
"ds_read_b64_tr_b16 v[236:237], v240 offset:20992\n\t"
"v_mfma_f32_16x16x32_bf16 a[16:19], a[224:227], a[188:191], a[16:19]\n\t"
"ds_read_b64_tr_b16 v[238:239], v240 offset:22528\n\t"
"v_mfma_f32_16x16x32_bf16 a[16:19], a[228:231], a[192:195], a[16:19]\n\t"
"ds_read_b64_tr_b16 v[224:225], v241 offset:20992\n\t"
"v_mfma_f32_16x16x32_bf16 a[16:19], v[248:251], a[196:199], a[16:19]\n\t"
"ds_read_b64_tr_b16 v[226:227], v241 offset:22528\n\t"
"v_mfma_f32_16x16x32_bf16 a[24:27], a[224:227], a[192:195], a[24:27]\n\t"
"v_cvt_pk_bf16_f32 v228, v228, v229\n\t"
"v_cvt_pk_bf16_f32 v229, v230, v231\n\t"
"v_mfma_f32_16x16x32_bf16 a[24:27], a[228:231], a[196:199], a[24:27]\n\t"
"v_cvt_pk_bf16_f32 v230, v232, v233\n\t"
"v_cvt_pk_bf16_f32 v231, v234, v235\n\t"
"v_mfma_f32_16x16x32_bf16 a[24:27], v[248:251], a[200:203], a[24:27]\n\t"
"ds_write_b128 v243, v[228:231] offset:512\n\t"
"v_mfma_f32_16x16x32_bf16 a[32:35], a[224:227], a[196:199], a[32:35]\n\t"
"buffer_load_dwordx4 a[224:227], v252, s[8:11], s20 offen offset:1024 sc1\n\t"
"v_mfma_f32_16x16x32_bf16 a[32:35], a[228:231], a[200:203], a[32:35]\n\t"
"s_waitcnt vmcnt(8)\n\t"
"ds_read_b64_tr_b16 v[232:233], v240 offset:40960\n\t"
"v_mfma_f32_16x16x32_bf16 a[32:35], v[248:251], a[204:207], a[32:35]\n\t"
"ds_read_b64_tr_b16 v[234:235], v240 offset:42496\n\t"
"v_mfma_f32_16x16x32_bf16 a[40:43], a[232:235], a[168:171], a[40:43]\n\t"
"buffer_load_dwordx4 v[248:251], v252, s[8:11], s20 offen offset:2048 sc1\n\t"
"v_mfma_f32_16x16x32_bf16 a[40:43], a[236:239], a[172:175], a[40:43]\n\t"
"s_mov_b32 s20, 1351680\n\t"
"ds_read_b64_tr_b16 v[228:229], v241 offset:40960\n\t"
"v_mfma_f32_16x16x32_bf16 a[40:43], a[212:215], a[176:179], a[40:43]\n\t"
"ds_read_b64_tr_b16 v[230:231], v241 offset:42496\n\t"
"v_mfma_f32_16x16x32_bf16 a[48:51], a[232:235], a[172:175], a[48:51]\n\t"
"buffer_load_dwordx4 a[228:231], v252, s[8:11], s20 offen offset:0 sc1\n\t"
"v_mfma_f32_16x16x32_bf16 a[48:51], a[236:239], a[176:179], a[48:51]\n\t"
"s_waitcnt lgkmcnt(5)\n\t"
"v_mfma_f32_16x16x32_bf16 a[48:51], a[212:215], a[180:183], a[48:51]\n\t"
"v_mfma_f32_16x16x32_bf16 v[236:239], v[236:239], a[208:211], 0\n\t"
"v_mfma_f32_16x16x32_bf16 v[224:227], v[224:227], a[208:211], 0\n\t"
"v_xor_b32 v246, 0xc0, v246\n\t"
"v_mfma_f32_16x16x32_bf16 a[56:59], a[232:235], a[176:179], a[56:59]\n\t"
"v_mfma_f32_16x16x32_bf16 a[56:59], a[236:239], a[180:183], a[56:59]\n\t"
"v_mfma_f32_16x16x32_bf16 a[56:59], a[212:215], a[184:187], a[56:59]\n\t"
"v_mfma_f32_16x16x32_bf16 a[64:67], a[232:235], a[180:183], a[64:67]\n\t"
"s_waitcnt lgkmcnt(2)\n\t"
"v_cvt_pk_bf16_f32 v236, v236, v237\n\t"
"v_cvt_pk_bf16_f32 v237, v238, v239\n\t"
"v_mfma_f32_16x16x32_bf16 a[64:67], a[236:239], a[184:187], a[64:67]\n\t"
"s_waitcnt lgkmcnt(0)\n\t"
"v_cvt_pk_bf16_f32 v238, v224, v225\n\t"
"v_cvt_pk_bf16_f32 v239, v226, v227\n\t"
"v_mfma_f32_16x16x32_bf16 a[64:67], a[212:215], a[188:191], a[64:67]\n\t"
"ds_write_b128 v243, v[236:239] offset:9728\n\t"
"v_mfma_f32_16x16x32_bf16 v[232:235], v[232:235], a[208:211], 0\n\t"
"v_mfma_f32_16x16x32_bf16 v[228:231], v[228:231], a[208:211], 0\n\t"
"v_mfma_f32_16x16x32_bf16 a[72:75], a[232:235], a[184:187], a[72:75]\n\t"
"v_mfma_f32_16x16x32_bf16 a[72:75], a[236:239], a[188:191], a[72:75]\n\t"
"v_mfma_f32_16x16x32_bf16 a[72:75], a[212:215], a[192:195], a[72:75]\n\t"
"v_mfma_f32_16x16x32_bf16 a[80:83], a[232:235], a[188:191], a[80:83]\n\t"
"v_cvt_pk_bf16_f32 v232, v232, v233\n\t"
"v_cvt_pk_bf16_f32 v233, v234, v235\n\t"
"v_mfma_f32_16x16x32_bf16 a[80:83], a[236:239], a[192:195], a[80:83]\n\t"
"v_cvt_pk_bf16_f32 v234, v228, v229\n\t"
"v_cvt_pk_bf16_f32 v235, v230, v231\n\t"
"v_mfma_f32_16x16x32_bf16 a[80:83], a[212:215], a[196:199], a[80:83]\n\t"
"ds_write_b128 v243, v[232:235] offset:18944\n\t"
"v_mfma_f32_16x16x32_bf16 a[88:91], a[232:235], a[192:195], a[88:91]\n\t"
"v_mfma_f32_16x16x32_bf16 a[88:91], a[236:239], a[196:199], a[88:91]\n\t"
"ds_read_b64_tr_b16 v[224:225], v246 offset:1024\n\t"
"v_mfma_f32_16x16x32_bf16 a[88:91], a[212:215], a[200:203], a[88:91]\n\t"
"ds_read_b64_tr_b16 v[226:227], v246 offset:4096\n\t"
"v_mfma_f32_16x16x32_bf16 a[96:99], a[232:235], a[196:199], a[96:99]\n\t"
"buffer_load_dwordx4 a[232:235], v252, s[8:11], s20 offen offset:1024 sc1\n\t"
"v_mfma_f32_16x16x32_bf16 a[96:99], a[236:239], a[200:203], a[96:99]\n\t"
"s_waitcnt vmcnt(8)\n\t"
"ds_read_b64_tr_b16 v[236:237], v246 offset:7168\n\t"
"v_mfma_f32_16x16x32_bf16 a[96:99], a[212:215], a[204:207], a[96:99]\n\t"
"ds_read_b64_tr_b16 v[238:239], v246 offset:10240\n\t"
"v_mfma_f32_16x16x32_bf16 a[104:107], a[216:219], a[168:171], a[104:107]\n\t"
"buffer_load_dwordx4 a[212:215], v252, s[8:11], s20 offen offset:2048 sc1\n\t"
"v_mfma_f32_16x16x32_bf16 a[104:107], a[240:243], a[172:175], a[104:107]\n\t"
"s_mov_b32 s20, 1216512\n\t"
"ds_read_b64_tr_b16 v[228:229], v246 offset:13312\n\t"
"v_mfma_f32_16x16x32_bf16 a[104:107], a[244:247], a[176:179], a[104:107]\n\t"
"ds_read_b64_tr_b16 v[230:231], v246 offset:16384\n\t"
"v_mfma_f32_16x16x32_bf16 a[112:115], a[216:219], a[172:175], a[112:115]\n\t"
"buffer_load_dwordx4 a[236:239], v252, s[8:11], s20 offen offset:0 sc1\n\t"
"v_mfma_f32_16x16x32_bf16 a[112:115], a[240:243], a[176:179], a[112:115]\n\t"
"ds_read_b128 a[168:171], v244 offset:33792\n\t"
"v_mfma_f32_16x16x32_bf16 a[112:115], a[244:247], a[180:183], a[112:115]\n\t"
"ds_read_b64_tr_b16 v[232:233], v246 offset:20992\n\t"
"v_mfma_f32_16x16x32_bf16 a[120:123], a[216:219], a[176:179], a[120:123]\n\t"
"s_waitcnt lgkmcnt(6)\n\t"
"ds_read_b64_tr_b16 v[234:235], v246 offset:24064\n\t"
"v_mfma_f32_16x16x32_bf16 a[120:123], a[240:243], a[180:183], a[120:123]\n\t"
"ds_read_b128 a[172:175], v244 offset:34304\n\t"
"v_mfma_f32_16x16x32_bf16 a[120:123], a[244:247], a[184:187], a[120:123]\n\t"
"ds_read_b128 a[176:179], v244 offset:34816\n\t"
"v_mfma_f32_16x16x32_bf16 v[224:227], v[224:227], v[220:223], 0\n\t"
"v_mfma_f32_16x16x32_bf16 a[128:131], a[216:219], a[180:183], a[128:131]\n\t"
"s_waitcnt lgkmcnt(7)\n\t"
"ds_read_b128 a[180:183], v244 offset:35328\n\t"
"v_mfma_f32_16x16x32_bf16 a[128:131], a[240:243], a[184:187], a[128:131]\n\t"
"v_mfma_f32_16x16x32_bf16 a[128:131], a[244:247], a[188:191], a[128:131]\n\t"
"v_mfma_f32_16x16x32_bf16 v[236:239], v[236:239], v[220:223], 0\n\t"
"v_mfma_f32_16x16x32_bf16 a[136:139], a[216:219], a[184:187], a[136:139]\n\t"
"s_waitcnt lgkmcnt(6)\n\t"
"ds_read_b128 a[184:187], v244 offset:35840\n\t"
"v_cvt_pk_bf16_f32 v224, v224, v225\n\t"
"v_mfma_f32_16x16x32_bf16 a[136:139], a[240:243], a[188:191], a[136:139]\n\t"
"v_cvt_pk_bf16_f32 v225, v226, v227\n\t"
"v_mfma_f32_16x16x32_bf16 a[136:139], a[244:247], a[192:195], a[136:139]\n\t"
"v_mfma_f32_16x16x32_bf16 v[228:231], v[228:231], v[220:223], 0\n\t"
"v_mfma_f32_16x16x32_bf16 a[144:147], a[216:219], a[188:191], a[144:147]\n\t"
"s_waitcnt lgkmcnt(4)\n\t"
"ds_read_b128 a[188:191], v244 offset:36352\n\t"
"v_cvt_pk_bf16_f32 v226, v236, v237\n\t"
"v_mfma_f32_16x16x32_bf16 a[144:147], a[240:243], a[192:195], a[144:147]\n\t"
"v_cvt_pk_bf16_f32 v227, v238, v239\n\t"
"v_mfma_f32_16x16x32_bf16 a[144:147], a[244:247], a[196:199], a[144:147]\n\t"
"v_mfma_f32_16x16x32_bf16 v[232:235], v[232:235], v[220:223], 0\n\t"
"v_mfma_f32_16x16x32_bf16 a[152:155], a[216:219], a[192:195], a[152:155]\n\t"
"ds_read_b128 a[192:195], v244 offset:36864\n\t"
"v_cvt_pk_bf16_f32 v228, v228, v229\n\t"
"v_mfma_f32_16x16x32_bf16 a[152:155], a[240:243], a[196:199], a[152:155]\n\t"
"v_cvt_pk_bf16_f32 v229, v230, v231\n\t"
"v_mfma_f32_16x16x32_bf16 a[152:155], a[244:247], a[200:203], a[152:155]\n\t"
"v_mfma_f32_16x16x32_bf16 a[160:163], a[216:219], a[196:199], a[160:163]\n\t"
"buffer_load_dwordx4 a[216:219], v252, s[8:11], s20 offen offset:1024 sc1\n\t"
"v_mfma_f32_16x16x32_bf16 a[160:163], a[240:243], a[200:203], a[160:163]\n\t"
"s_waitcnt vmcnt(8) lgkmcnt(4)\n\t"
"ds_read_b128 a[196:199], v244 offset:37376\n\t"
"v_cvt_pk_bf16_f32 v230, v232, v233\n\t"
"v_mfma_f32_16x16x32_bf16 a[160:163], a[244:247], a[204:207], a[160:163]\n\t"
"ds_read_b128 a[200:203], v244 offset:37888\n\t"
"v_cvt_pk_bf16_f32 v231, v234, v235\n\t"
"v_mfma_f32_16x16x32_bf16 v[200:203], a[248:251], a[168:171], v[200:203]\n\t"
"ds_read_b64_tr_b16 v[232:233], v246 offset:27136\n\t"
"v_mfma_f32_16x16x32_bf16 v[200:203], a[252:255], a[172:175], v[200:203]\n\t"
"s_waitcnt lgkmcnt(6)\n\t"
"ds_read_b64_tr_b16 v[234:235], v246 offset:30208\n\t"
"v_mfma_f32_16x16x32_bf16 v[200:203], v[96:99], a[176:179], v[200:203]\n\t"
"s_waitcnt lgkmcnt(0)\n\t"
"ds_read_b128 a[204:207], v244 offset:38400\n\t"
"v_mfma_f32_16x16x32_bf16 v[232:235], v[232:235], v[220:223], 0\n\t"
"buffer_load_dwordx4 a[240:243], v252, s[8:11], s20 offen offset:2048 sc1\n\t"
"s_mov_b32 s20, 1228800\n\t"
"v_mfma_f32_16x16x32_bf16 v[208:211], a[248:251], a[172:175], v[208:211]\n\t"
"buffer_load_dwordx4 a[244:247], v252, s[8:11], s20 offen offset:0 sc1\n\t"
"v_mfma_f32_16x16x32_bf16 v[208:211], a[252:255], a[176:179], v[208:211]\n\t"
"v_mfma_f32_16x16x32_bf16 v[208:211], v[96:99], a[180:183], v[208:211]\n\t"
"s_waitcnt lgkmcnt(0)\n\t"
"s_barrier\n\t"
"v_mfma_f32_16x16x32_bf16 v[216:219], a[248:251], a[176:179], v[216:219]\n\t"
"v_mfma_f32_16x16x32_bf16 v[216:219], a[252:255], a[180:183], v[216:219]\n\t"
"v_cvt_pk_bf16_f32 v232, v232, v233\n\t"
"v_cvt_pk_bf16_f32 v233, v234, v235\n\t"
"v_mfma_f32_16x16x32_bf16 v[216:219], v[96:99], a[184:187], v[216:219]\n\t"
"ds_write2st64_b64 v245, v[224:225], v[226:227] offset0:48 offset1:49\n\t"
"v_mfma_f32_16x16x32_bf16 a[4:7], a[248:251], a[180:183], a[4:7]\n\t"
"v_mfma_f32_16x16x32_bf16 a[4:7], a[252:255], a[184:187], a[4:7]\n\t"
"ds_read_b64_tr_b16 v[224:225], v246 offset:33280\n\t"
"v_mfma_f32_16x16x32_bf16 a[4:7], v[96:99], a[188:191], a[4:7]\n\t"
"ds_write2st64_b64 v245, v[228:229], v[230:231] offset0:50 offset1:51\n\t"
"v_mfma_f32_16x16x32_bf16 a[12:15], a[248:251], a[184:187], a[12:15]\n\t"
"v_mfma_f32_16x16x32_bf16 a[12:15], a[252:255], a[188:191], a[12:15]\n\t"
"ds_read_b64_tr_b16 v[226:227], v246 offset:36352\n\t"
"v_mfma_f32_16x16x32_bf16 a[12:15], v[96:99], a[192:195], a[12:15]\n\t"
"ds_write_b64 v245, v[232:233] offset:26624\n\t"
"v_mfma_f32_16x16x32_bf16 a[20:23], a[248:251], a[188:191], a[20:23]\n\t"
"ds_read_b64_tr_b16 v[228:229], v246 offset:40960\n\t"
"v_mfma_f32_16x16x32_bf16 a[20:23], a[252:255], a[192:195], a[20:23]\n\t"
"ds_read_b64_tr_b16 v[230:231], v246 offset:44032\n\t"
"v_mfma_f32_16x16x32_bf16 a[20:23], v[96:99], a[196:199], a[20:23]\n\t"
"s_waitcnt lgkmcnt(3)\n\t"
"ds_read_b64_tr_b16 v[232:233], v246 offset:47104\n\t"
"v_mfma_f32_16x16x32_bf16 v[224:227], v[224:227], v[220:223], 0\n\t"
"ds_read_b64_tr_b16 v[234:235], v246 offset:50176\n\t"
"v_mfma_f32_16x16x32_bf16 a[28:31], a[248:251], a[192:195], a[28:31]\n\t"
"v_mfma_f32_16x16x32_bf16 a[28:31], a[252:255], a[196:199], a[28:31]\n\t"
"v_mfma_f32_16x16x32_bf16 a[28:31], v[96:99], a[200:203], a[28:31]\n\t"
"s_waitcnt lgkmcnt(2)\n\t"
"v_mfma_f32_16x16x32_bf16 v[228:231], v[228:231], v[220:223], 0\n\t"
"v_mfma_f32_16x16x32_bf16 a[36:39], a[248:251], a[196:199], a[36:39]\n\t"
"buffer_load_dwordx4 a[248:251], v252, s[8:11], s20 offen offset:1024 sc1\n\t"
"v_mfma_f32_16x16x32_bf16 a[36:39], a[252:255], a[200:203], a[36:39]\n\t"
"s_waitcnt vmcnt(8) lgkmcnt(0)\n\t"
"v_cvt_pk_bf16_f32 v224, v224, v225\n\t"
"v_cvt_pk_bf16_f32 v225, v226, v227\n\t"
"v_mfma_f32_16x16x32_bf16 a[36:39], v[96:99], a[204:207], a[36:39]\n\t"
"v_mfma_f32_16x16x32_bf16 v[232:235], v[232:235], v[220:223], 0\n\t"
"buffer_load_dwordx4 v[96:99], v252, s[8:11], s20 offen offset:2048 sc1\n\t"
"v_mfma_f32_16x16x32_bf16 a[44:47], a[220:223], a[168:171], a[44:47]\n\t"
"s_mov_b32 s20, 1241088\n\t"
"v_cvt_pk_bf16_f32 v226, v228, v229\n\t"
"v_cvt_pk_bf16_f32 v227, v230, v231\n\t"
"v_mfma_f32_16x16x32_bf16 a[44:47], a[224:227], a[172:175], a[44:47]\n\t"
"ds_read_b64_tr_b16 v[228:229], v246 offset:53248\n\t"
"v_mfma_f32_16x16x32_bf16 a[44:47], v[248:251], a[176:179], a[44:47]\n\t"
"buffer_load_dwordx4 a[252:255], v252, s[8:11], s20 offen offset:0 sc1\n\t"
"v_mfma_f32_16x16x32_bf16 a[52:55], a[220:223], a[172:175], a[52:55]\n\t"
"ds_read_b64_tr_b16 v[230:231], v246 offset:56320\n\t"
"v_mfma_f32_16x16x32_bf16 a[52:55], a[224:227], a[176:179], a[52:55]\n\t"
"v_cvt_pk_bf16_f32 v232, v232, v233\n\t"
"v_cvt_pk_bf16_f32 v233, v234, v235\n\t"
"v_mfma_f32_16x16x32_bf16 a[52:55], v[248:251], a[180:183], a[52:55]\n\t"
"ds_write2st64_b64 v245, v[224:225], v[226:227] offset0:53 offset1:54\n\t"
"v_mfma_f32_16x16x32_bf16 a[60:63], a[220:223], a[176:179], a[60:63]\n\t"
"v_mfma_f32_16x16x32_bf16 a[60:63], a[224:227], a[180:183], a[60:63]\n\t"
"v_subrev_u32 v243, 55296, v243\n\t"
"v_mfma_f32_16x16x32_bf16 a[60:63], v[248:251], a[184:187], a[60:63]\n\t"
"v_mfma_f32_16x16x32_bf16 a[68:71], a[220:223], a[180:183], a[68:71]\n\t"
"v_mfma_f32_16x16x32_bf16 a[68:71], a[224:227], a[184:187], a[68:71]\n\t"
"v_mfma_f32_16x16x32_bf16 a[68:71], v[248:251], a[188:191], a[68:71]\n\t"
"v_mfma_f32_16x16x32_bf16 a[76:79], a[220:223], a[184:187], a[76:79]\n\t"
"s_waitcnt lgkmcnt(1)\n\t"
"v_mfma_f32_16x16x32_bf16 a[76:79], a[224:227], a[188:191], a[76:79]\n\t"
"v_mfma_f32_16x16x32_bf16 a[76:79], v[248:251], a[192:195], a[76:79]\n\t"
"v_mfma_f32_16x16x32_bf16 v[228:231], v[228:231], v[220:223], 0\n\t"
"v_mfma_f32_16x16x32_bf16 a[84:87], a[220:223], a[188:191], a[84:87]\n\t"
"v_mfma_f32_16x16x32_bf16 a[84:87], a[224:227], a[192:195], a[84:87]\n\t"
"v_mfma_f32_16x16x32_bf16 a[84:87], v[248:251], a[196:199], a[84:87]\n\t"
"v_mfma_f32_16x16x32_bf16 a[92:95], a[220:223], a[192:195], a[92:95]\n\t"
"v_mfma_f32_16x16x32_bf16 a[92:95], a[224:227], a[196:199], a[92:95]\n\t"
"v_cvt_pk_bf16_f32 v234, v228, v229\n\t"
"v_cvt_pk_bf16_f32 v235, v230, v231\n\t"
"v_mfma_f32_16x16x32_bf16 a[92:95], v[248:251], a[200:203], a[92:95]\n\t"
"ds_write2st64_b64 v245, v[232:233], v[234:235] offset0:55 offset1:56\n\t"
"v_mfma_f32_16x16x32_bf16 a[100:103], a[220:223], a[196:199], a[100:103]\n\t"
"buffer_load_dwordx4 a[220:223], v252, s[8:11], s20 offen offset:1024 sc1\n\t"
"v_mfma_f32_16x16x32_bf16 a[100:103], a[224:227], a[200:203], a[100:103]\n\t"
"s_waitcnt vmcnt(8)\n\t"
"ds_read_b64_tr_b16 v[236:237], v240 offset:1536\n\t"
"v_mfma_f32_16x16x32_bf16 a[100:103], v[248:251], a[204:207], a[100:103]\n\t"
"ds_read_b64_tr_b16 v[238:239], v240 offset:3072\n\t"
"v_mfma_f32_16x16x32_bf16 a[108:111], a[228:231], a[168:171], a[108:111]\n\t"
"buffer_load_dwordx4 v[248:251], v252, s[8:11], s20 offen offset:2048 sc1\n\t"
"v_mfma_f32_16x16x32_bf16 a[108:111], a[232:235], a[172:175], a[108:111]\n\t"
"s_mov_b32 s20, 1253376\n\t"
"ds_read_b64_tr_b16 v[224:225], v241 offset:1536\n\t"
"v_mfma_f32_16x16x32_bf16 a[108:111], a[212:215], a[176:179], a[108:111]\n\t"
"s_waitcnt lgkmcnt(3)\n\t"
"s_barrier\n\t"
"v_mfma_f32_16x16x32_bf16 a[116:119], a[228:231], a[172:175], a[116:119]\n\t"
"ds_read_b64_tr_b16 v[226:227], v241 offset:3072\n\t"
"v_mfma_f32_16x16x32_bf16 a[116:119], a[232:235], a[176:179], a[116:119]\n\t"
"buffer_load_dwordx4 a[224:227], v252, s[8:11], s20 offen offset:0 sc1\n\t"
"v_mfma_f32_16x16x32_bf16 a[116:119], a[212:215], a[180:183], a[116:119]\n\t"
"ds_read_b64_tr_b16 v[228:229], v240 offset:21504\n\t"
"v_mfma_f32_16x16x32_bf16 a[124:127], a[228:231], a[176:179], a[124:127]\n\t"
"ds_read_b64_tr_b16 v[230:231], v240 offset:23040\n\t"
"v_mfma_f32_16x16x32_bf16 a[124:127], a[232:235], a[180:183], a[124:127]\n\t"
"ds_read_b64_tr_b16 v[232:233], v241 offset:21504\n\t"
"v_mfma_f32_16x16x32_bf16 a[124:127], a[212:215], a[184:187], a[124:127]\n\t"
"ds_read_b64_tr_b16 v[234:235], v241 offset:23040\n\t"
"v_mfma_f32_16x16x32_bf16 a[132:135], a[228:231], a[180:183], a[132:135]\n\t"
"s_waitcnt lgkmcnt(6)\n\t"
"ds_read_b128 a[168:171], v244 offset:24576\n\t"
"v_mfma_f32_16x16x32_bf16 a[132:135], a[232:235], a[184:187], a[132:135]\n\t"
"s_waitcnt lgkmcnt(5)\n\t"
"ds_read_b128 a[172:175], v244 offset:25088\n\t"
"v_mfma_f32_16x16x32_bf16 a[132:135], a[212:215], a[188:191], a[132:135]\n\t"
"ds_read_b128 a[176:179], v244 offset:25600\n\t"
"v_mfma_f32_16x16x32_bf16 v[236:239], v[236:239], a[208:211], 0\n\t"
"ds_read_b128 a[180:183], v244 offset:26112\n\t"
"v_mfma_f32_16x16x32_bf16 v[224:227], v[224:227], a[208:211], 0\n\t"
"v_mfma_f32_16x16x32_bf16 a[140:143], a[228:231], a[184:187], a[140:143]\n\t"
"ds_read_b128 a[184:187], v244 offset:26624\n\t"
"v_mfma_f32_16x16x32_bf16 a[140:143], a[232:235], a[188:191], a[140:143]\n\t"
"v_mfma_f32_16x16x32_bf16 a[140:143], a[212:215], a[192:195], a[140:143]\n\t"
"v_mfma_f32_16x16x32_bf16 a[148:151], a[228:231], a[188:191], a[148:151]\n\t"
"s_waitcnt lgkmcnt(7)\n\t"
"ds_read_b128 a[188:191], v244 offset:27136\n\t"
"v_cvt_pk_bf16_f32 v236, v236, v237\n\t"
"v_mfma_f32_16x16x32_bf16 a[148:151], a[232:235], a[192:195], a[148:151]\n\t"
"s_waitcnt lgkmcnt(6)\n\t"
"v_cvt_pk_bf16_f32 v237, v238, v239\n\t"
"v_cvt_pk_bf16_f32 v238, v224, v225\n\t"
"v_mfma_f32_16x16x32_bf16 a[148:151], a[212:215], a[196:199], a[148:151]\n\t"
"v_cvt_pk_bf16_f32 v239, v226, v227\n\t"
"v_mfma_f32_16x16x32_bf16 v[228:231], v[228:231], a[208:211], 0\n\t"
"ds_write_b128 v243, v[236:239] offset:1024\n\t"
"v_mfma_f32_16x16x32_bf16 v[232:235], v[232:235], a[208:211], 0\n\t"
"v_mfma_f32_16x16x32_bf16 a[156:159], a[228:231], a[192:195], a[156:159]\n\t"
"ds_read_b128 a[192:195], v244 offset:27648\n\t"
"v_mfma_f32_16x16x32_bf16 a[156:159], a[232:235], a[196:199], a[156:159]\n\t"
"ds_read_b64_tr_b16 v[224:225], v240 offset:41472\n\t"
"v_mfma_f32_16x16x32_bf16 a[156:159], a[212:215], a[200:203], a[156:159]\n\t"
"ds_read_b64_tr_b16 v[226:227], v240 offset:43008\n\t"
"v_mfma_f32_16x16x32_bf16 a[164:167], a[228:231], a[196:199], a[164:167]\n\t"
"buffer_load_dwordx4 a[228:231], v252, s[8:11], s20 offen offset:1024 sc1\n\t"
"v_mfma_f32_16x16x32_bf16 a[164:167], a[232:235], a[200:203], a[164:167]\n\t"
"s_waitcnt vmcnt(8) lgkmcnt(7)\n\t"
"ds_read_b64_tr_b16 v[236:237], v241 offset:41472\n\t"
"v_cvt_pk_bf16_f32 v228, v228, v229\n\t"
"v_mfma_f32_16x16x32_bf16 a[164:167], a[212:215], a[204:207], a[164:167]\n\t"
"ds_read_b64_tr_b16 v[238:239], v241 offset:43008\n\t"
"v_cvt_pk_bf16_f32 v229, v230, v231\n\t"
"Lst29_%=:\n\t"
"v_mfma_f32_16x16x32_bf16 v[0:3], a[236:239], a[168:171], v[0:3]\n\t"
"s_waitcnt lgkmcnt(8)\n\t"
"ds_read_b128 a[196:199], v244 offset:28160\n\t"
"v_cvt_pk_bf16_f32 v230, v232, v233\n\t"
"v_mfma_f32_16x16x32_bf16 v[0:3], a[216:219], a[172:175], v[0:3]\n\t"
"ds_read_b128 a[200:203], v244 offset:28672\n\t"
"v_cvt_pk_bf16_f32 v231, v234, v235\n\t"
"v_mfma_f32_16x16x32_bf16 v[0:3], a[240:243], a[176:179], v[0:3]\n\t"
"ds_write_b128 v243, v[228:231] offset:10240\n\t"
"v_mfma_f32_16x16x32_bf16 v[4:7], a[236:239], a[172:175], v[4:7]\n\t"
"buffer_load_dwordx4 a[212:215], v252, s[8:11], s20 offen offset:2048 sc1\n\t"
"v_mfma_f32_16x16x32_bf16 v[4:7], a[216:219], a[176:179], v[4:7]\n\t"
"s_waitcnt lgkmcnt(10)\n\t"
"ds_read_b128 a[204:207], v244 offset:29184\n\t"
"v_mfma_f32_16x16x32_bf16 v[4:7], a[240:243], a[180:183], v[4:7]\n\t"
"s_mov_b32 s20, 1265664\n\t"
"v_add_u32 v240, 59904, v240\n\t"
"v_add_u32 v241, 59904, v241\n\t"
"v_mfma_f32_16x16x32_bf16 v[8:11], a[236:239], a[176:179], v[8:11]\n\t"
"s_waitcnt lgkmcnt(6)\n\t"
"ds_read_b64_tr_b16 v[232:233], v240 offset:1536\n\t"
"v_mfma_f32_16x16x32_bf16 v[8:11], a[216:219], a[180:183], v[8:11]\n\t"
"buffer_load_dwordx4 a[232:235], v252, s[8:11], s20 offen offset:0 sc1\n\t"
"v_mfma_f32_16x16x32_bf16 v[8:11], a[240:243], a[184:187], v[8:11]\n\t"
"s_waitcnt lgkmcnt(5)\n\t"
"ds_read_b64_tr_b16 v[234:235], v240 offset:3072\n\t"
"v_mfma_f32_16x16x32_bf16 v[224:227], v[224:227], a[208:211], 0\n\t"
"ds_read_b64_tr_b16 v[228:229], v241 offset:1536\n\t"
"v_mfma_f32_16x16x32_bf16 v[236:239], v[236:239], a[208:211], 0\n\t"
"ds_read_b64_tr_b16 v[230:231], v241 offset:3072\n\t"
"v_mfma_f32_16x16x32_bf16 v[12:15], a[236:239], a[180:183], v[12:15]\n\t"
"v_mfma_f32_16x16x32_bf16 v[12:15], a[216:219], a[184:187], v[12:15]\n\t"
"v_mfma_f32_16x16x32_bf16 v[12:15], a[240:243], a[188:191], v[12:15]\n\t"
"v_mfma_f32_16x16x32_bf16 v[16:19], a[236:239], a[184:187], v[16:19]\n\t"
"s_waitcnt lgkmcnt(2)\n\t"
"v_cvt_pk_bf16_f32 v224, v224, v225\n\t"
"v_cvt_pk_bf16_f32 v225, v226, v227\n\t"
"v_mfma_f32_16x16x32_bf16 v[16:19], a[216:219], a[188:191], v[16:19]\n\t"
"s_waitcnt lgkmcnt(0)\n\t"
"v_cvt_pk_bf16_f32 v226, v236, v237\n\t"
"v_cvt_pk_bf16_f32 v227, v238, v239\n\t"
"v_mfma_f32_16x16x32_bf16 v[16:19], a[240:243], a[192:195], v[16:19]\n\t"
"ds_write_b128 v243, v[224:227] offset:19456\n\t"
"v_mfma_f32_16x16x32_bf16 v[232:235], v[232:235], a[208:211], 0\n\t"
"v_mfma_f32_16x16x32_bf16 v[228:231], v[228:231], a[208:211], 0\n\t"
"ds_read_b64_tr_b16 v[236:237], v240 offset:21504\n\t"
"v_mfma_f32_16x16x32_bf16 v[20:23], a[236:239], a[188:191], v[20:23]\n\t"
"ds_read_b64_tr_b16 v[238:239], v240 offset:23040\n\t"
"v_mfma_f32_16x16x32_bf16 v[20:23], a[216:219], a[192:195], v[20:23]\n\t"
"ds_read_b64_tr_b16 v[224:225], v241 offset:21504\n\t"
"v_mfma_f32_16x16x32_bf16 v[20:23], a[240:243], a[196:199], v[20:23]\n\t"
"ds_read_b64_tr_b16 v[226:227], v241 offset:23040\n\t"
"v_mfma_f32_16x16x32_bf16 v[24:27], a[236:239], a[192:195], v[24:27]\n\t"
"v_cvt_pk_bf16_f32 v232, v232, v233\n\t"
"v_cvt_pk_bf16_f32 v233, v234, v235\n\t"
"v_mfma_f32_16x16x32_bf16 v[24:27], a[216:219], a[196:199], v[24:27]\n\t"
"v_cvt_pk_bf16_f32 v234, v228, v229\n\t"
"v_cvt_pk_bf16_f32 v235, v230, v231\n\t"
"v_mfma_f32_16x16x32_bf16 v[24:27], a[240:243], a[200:203], v[24:27]\n\t"
"ds_write_b128 v243, v[232:235] offset:28672\n\t"
"v_mfma_f32_16x16x32_bf16 v[28:31], a[236:239], a[196:199], v[28:31]\n\t"
"buffer_load_dwordx4 a[236:239], v252, s[8:11], s20 offen offset:1024 sc1\n\t"
"v_mfma_f32_16x16x32_bf16 v[28:31], a[216:219], a[200:203], v[28:31]\n\t"
"s_waitcnt vmcnt(8)\n\t"
"ds_read_b64_tr_b16 v[228:229], v240 offset:41472\n\t"
"v_mfma_f32_16x16x32_bf16 v[28:31], a[240:243], a[204:207], v[28:31]\n\t"
"ds_read_b64_tr_b16 v[230:231], v240 offset:43008\n\t"
"v_mfma_f32_16x16x32_bf16 v[32:35], a[244:247], a[168:171], v[32:35]\n\t"
"buffer_load_dwordx4 a[216:219], v252, s[8:11], s20 offen offset:2048 sc1\n\t"
"v_mfma_f32_16x16x32_bf16 v[32:35], a[248:251], a[172:175], v[32:35]\n\t"
"s_mov_b32 s20, 1277952\n\t"
"ds_read_b64_tr_b16 v[232:233], v241 offset:41472\n\t"
"v_mfma_f32_16x16x32_bf16 v[32:35], v[96:99], a[176:179], v[32:35]\n\t"
"s_waitcnt lgkmcnt(4)\n\t"
"ds_read_b64_tr_b16 v[234:235], v241 offset:43008\n\t"
"v_mfma_f32_16x16x32_bf16 v[36:39], a[244:247], a[172:175], v[36:39]\n\t"
"Lpg21_%=:\n\t"
"s_cbranch_vccnz Lvr3_%=\n\t"
"v_mfma_f32_16x16x32_bf16 v[36:39], a[248:251], a[176:179], v[36:39]\n\t"
"buffer_load_dwordx4 a[240:243], v252, s[8:11], s20 offen offset:0 sc1\n\t"
"v_mfma_f32_16x16x32_bf16 v[36:39], v[96:99], a[180:183], v[36:39]\n\t"
"v_mfma_f32_16x16x32_bf16 v[236:239], v[236:239], a[208:211], 0\n\t"
"v_mfma_f32_16x16x32_bf16 v[224:227], v[224:227], a[208:211], 0\n\t"
"v_mfma_f32_16x16x32_bf16 v[40:43], a[244:247], a[176:179], v[40:43]\n\t"
"v_mfma_f32_16x16x32_bf16 v[40:43], a[248:251], a[180:183], v[40:43]\n\t"
"v_mfma_f32_16x16x32_bf16 v[40:43], v[96:99], a[184:187], v[40:43]\n\t"
"v_mfma_f32_16x16x32_bf16 v[44:47], a[244:247], a[180:183], v[44:47]\n\t"
"s_waitcnt lgkmcnt(2)\n\t"
"v_cvt_pk_bf16_f32 v236, v236, v237\n\t"
"v_cvt_pk_bf16_f32 v237, v238, v239\n\t"
"v_mfma_f32_16x16x32_bf16 v[44:47], a[248:251], a[184:187], v[44:47]\n\t"
"s_waitcnt lgkmcnt(0)\n\t"
"v_cvt_pk_bf16_f32 v238, v224, v225\n\t"
"v_cvt_pk_bf16_f32 v239, v226, v227\n\t"
"v_mfma_f32_16x16x32_bf16 v[44:47], v[96:99], a[188:191], v[44:47]\n\t"
"ds_write_b128 v243, v[236:239] offset:37888\n\t"
"v_mfma_f32_16x16x32_bf16 v[228:231], v[228:231], a[208:211], 0\n\t"
"v_mfma_f32_16x16x32_bf16 v[232:235], v[232:235], a[208:211], 0\n\t"
"v_mfma_f32_16x16x32_bf16 v[48:51], a[244:247], a[184:187], v[48:51]\n\t"
"buffer_load_dwordx4 a[208:211], v252, s[8:11], s20 offen offset:1024 sc1\n\t"
"v_mfma_f32_16x16x32_bf16 v[48:51], a[248:251], a[188:191], v[48:51]\n\t"
"v_mfma_f32_16x16x32_bf16 v[48:51], v[96:99], a[192:195], v[48:51]\n\t"
"v_mfma_f32_16x16x32_bf16 v[52:55], a[244:247], a[188:191], v[52:55]\n\t"
"v_cvt_pk_bf16_f32 v228, v228, v229\n\t"
"v_cvt_pk_bf16_f32 v229, v230, v231\n\t"
"v_mfma_f32_16x16x32_bf16 v[52:55], a[248:251], a[192:195], v[52:55]\n\t"
"v_cvt_pk_bf16_f32 v230, v232, v233\n\t"
"v_cvt_pk_bf16_f32 v231, v234, v235\n\t"
"v_mfma_f32_16x16x32_bf16 v[52:55], v[96:99], a[196:199], v[52:55]\n\t"
"ds_write_b128 v243, v[228:231] offset:47104\n\t"
"v_mfma_f32_16x16x32_bf16 v[56:59], a[244:247], a[192:195], v[56:59]\n\t"
"v_mfma_f32_16x16x32_bf16 v[56:59], a[248:251], a[196:199], v[56:59]\n\t"
"ds_read_b64_tr_b16 v[224:225], v246 offset:1536\n\t"
"v_mfma_f32_16x16x32_bf16 v[56:59], v[96:99], a[200:203], v[56:59]\n\t"
"ds_read_b64_tr_b16 v[226:227], v246 offset:4608\n\t"
"v_mfma_f32_16x16x32_bf16 v[60:63], a[244:247], a[196:199], v[60:63]\n\t"
"buffer_load_dwordx4 a[244:247], v252, s[8:11], s20 offen offset:2048 sc1\n\t"
"v_mfma_f32_16x16x32_bf16 v[60:63], a[248:251], a[200:203], v[60:63]\n\t"
"s_waitcnt vmcnt(9)\n\t"
"ds_read_b64_tr_b16 v[236:237], v246 offset:7680\n\t"
"s_mov_b32 s20, 1511424\n\t"
"v_mfma_f32_16x16x32_bf16 v[60:63], v[96:99], a[204:207], v[60:63]\n\t"
"ds_read_b64_tr_b16 v[238:239], v246 offset:10752\n\t"
"v_mfma_f32_16x16x32_bf16 v[64:67], a[252:255], a[168:171], v[64:67]\n\t"
"buffer_load_dwordx4 v[96:99], v252, s[8:11], s20 offen offset:0 sc1\n\t"
"v_mfma_f32_16x16x32_bf16 v[64:67], a[220:223], a[172:175], v[64:67]\n\t"
"ds_read_b64_tr_b16 v[232:233], v246 offset:13824\n\t"
"v_mfma_f32_16x16x32_bf16 v[64:67], v[248:251], a[176:179], v[64:67]\n\t"
"ds_read_b64_tr_b16 v[234:235], v246 offset:16896\n\t"
"v_mfma_f32_16x16x32_bf16 v[68:71], a[252:255], a[172:175], v[68:71]\n\t"
"buffer_load_dwordx4 a[248:251], v252, s[8:11], s20 offen offset:1024 sc1\n\t"
"v_mfma_f32_16x16x32_bf16 v[68:71], a[220:223], a[176:179], v[68:71]\n\t"
"ds_read_b128 a[168:171], v244 offset:33792\n\t"
"v_mfma_f32_16x16x32_bf16 v[68:71], v[248:251], a[180:183], v[68:71]\n\t"
"ds_read_b64_tr_b16 v[228:229], v246 offset:21504\n\t"
"v_mfma_f32_16x16x32_bf16 v[72:75], a[252:255], a[176:179], v[72:75]\n\t"
"buffer_load_dwordx4 v[240:243], v252, s[8:11], s20 offen offset:2048 sc1\n\t"
"v_mfma_f32_16x16x32_bf16 v[72:75], a[220:223], a[180:183], v[72:75]\n\t"
"s_waitcnt lgkmcnt(6)\n\t"
"ds_read_b64_tr_b16 v[230:231], v246 offset:24576\n\t"
"v_mfma_f32_16x16x32_bf16 v[72:75], v[248:251], a[184:187], v[72:75]\n\t"
"ds_read_b128 a[172:175], v244 offset:34304\n\t"
"v_mfma_f32_16x16x32_bf16 v[224:227], v[224:227], v[220:223], 0\n\t"
"ds_read_b128 a[176:179], v244 offset:34816\n\t"
"v_mfma_f32_16x16x32_bf16 v[76:79], a[252:255], a[180:183], v[76:79]\n\t"
"s_waitcnt lgkmcnt(7)\n\t"
"ds_read_b128 a[180:183], v244 offset:35328\n\t"
"v_mfma_f32_16x16x32_bf16 v[76:79], a[220:223], a[184:187], v[76:79]\n\t"
"v_mfma_f32_16x16x32_bf16 v[76:79], v[248:251], a[188:191], v[76:79]\n\t"
"v_mfma_f32_16x16x32_bf16 v[236:239], v[236:239], v[220:223], 0\n\t"
"v_mfma_f32_16x16x32_bf16 v[80:83], a[252:255], a[184:187], v[80:83]\n\t"
"s_waitcnt lgkmcnt(6)\n\t"
"ds_read_b128 a[184:187], v244 offset:35840\n\t"
"v_cvt_pk_bf16_f32 v224, v224, v225\n\t"
"v_mfma_f32_16x16x32_bf16 v[80:83], a[220:223], a[188:191], v[80:83]\n\t"
"v_cvt_pk_bf16_f32 v225, v226, v227\n\t"
"v_mfma_f32_16x16x32_bf16 v[80:83], v[248:251], a[192:195], v[80:83]\n\t"
"s_mov_b32 s20, 1523712\n\t"
"v_mfma_f32_16x16x32_bf16 v[232:235], v[232:235], v[220:223], 0\n\t"
"v_mfma_f32_16x16x32_bf16 v[84:87], a[252:255], a[188:191], v[84:87]\n\t"
"s_waitcnt lgkmcnt(4)\n\t"
"ds_read_b128 a[188:191], v244 offset:36352\n\t"
"v_cvt_pk_bf16_f32 v226, v236, v237\n\t"
"v_mfma_f32_16x16x32_bf16 v[84:87], a[220:223], a[192:195], v[84:87]\n\t"
"v_cvt_pk_bf16_f32 v227, v238, v239\n\t"
"v_mfma_f32_16x16x32_bf16 v[84:87], v[248:251], a[196:199], v[84:87]\n\t"
"v_mfma_f32_16x16x32_bf16 v[228:231], v[228:231], v[220:223], 0\n\t"
"v_mfma_f32_16x16x32_bf16 v[88:91], a[252:255], a[192:195], v[88:91]\n\t"
"buffer_load_dwordx4 v[236:239], v252, s[8:11], s20 offen offset:0 sc1\n\t"
"v_mfma_f32_16x16x32_bf16 v[88:91], a[220:223], a[196:199], v[88:91]\n\t"
"ds_read_b128 a[192:195], v244 offset:36864\n\t"
"v_cvt_pk_bf16_f32 v232, v232, v233\n\t"
"v_mfma_f32_16x16x32_bf16 v[88:91], v[248:251], a[200:203], v[88:91]\n\t"
"v_cvt_pk_bf16_f32 v233, v234, v235\n\t"
"v_mfma_f32_16x16x32_bf16 v[92:95], a[252:255], a[196:199], v[92:95]\n\t"
"buffer_load_dwordx4 a[252:255], v252, s[8:11], s20 offen offset:1024 sc1\n\t"
"v_mfma_f32_16x16x32_bf16 v[92:95], a[220:223], a[200:203], v[92:95]\n\t"
"s_waitcnt vmcnt(11) lgkmcnt(4)\n\t"
"ds_read_b128 a[196:199], v244 offset:37376\n\t"
"v_cvt_pk_bf16_f32 v234, v228, v229\n\t"
"v_mfma_f32_16x16x32_bf16 v[92:95], v[248:251], a[204:207], v[92:95]\n\t"
"ds_read_b128 a[200:203], v244 offset:37888\n\t"
"v_cvt_pk_bf16_f32 v235, v230, v231\n\t"
"v_mfma_f32_16x16x32_bf16 v[100:103], a[224:227], a[168:171], v[100:103]\n\t"
"ds_read_b64_tr_b16 v[228:229], v246 offset:27648\n\t"
"v_mfma_f32_16x16x32_bf16 v[100:103], a[228:231], a[172:175], v[100:103]\n\t"
"s_waitcnt lgkmcnt(6)\n\t"
"ds_read_b64_tr_b16 v[230:231], v246 offset:30720\n\t"
"v_mfma_f32_16x16x32_bf16 v[100:103], a[212:215], a[176:179], v[100:103]\n\t"
"s_waitcnt lgkmcnt(0)\n\t"
"ds_read_b128 a[204:207], v244 offset:38400\n\t"
"v_mfma_f32_16x16x32_bf16 v[228:231], v[228:231], v[220:223], 0\n\t"
"buffer_load_dwordx4 v[248:251], v252, s[8:11], s20 offen offset:2048 sc1\n\t"
"s_mov_b32 s20, 1536000\n\t"
"v_mfma_f32_16x16x32_bf16 v[104:107], a[224:227], a[172:175], v[104:107]\n\t"
"buffer_load_dwordx4 a[220:223], v252, s[8:11], s20 offen offset:0 sc1\n\t"
"v_mfma_f32_16x16x32_bf16 v[104:107], a[228:231], a[176:179], v[104:107]\n\t"
"v_mfma_f32_16x16x32_bf16 v[104:107], a[212:215], a[180:183], v[104:107]\n\t"
"s_waitcnt lgkmcnt(0)\n\t"
"s_barrier\n\t"
"v_mfma_f32_16x16x32_bf16 v[108:111], a[224:227], a[176:179], v[108:111]\n\t"
"v_mfma_f32_16x16x32_bf16 v[108:111], a[228:231], a[180:183], v[108:111]\n\t"
"v_cvt_pk_bf16_f32 v228, v228, v229\n\t"
"v_cvt_pk_bf16_f32 v229, v230, v231\n\t"
"v_mfma_f32_16x16x32_bf16 v[108:111], a[212:215], a[184:187], v[108:111]\n\t"
"ds_write2st64_b64 v245, v[224:225], v[226:227] offset0:48 offset1:49\n\t"
"v_mfma_f32_16x16x32_bf16 v[112:115], a[224:227], a[180:183], v[112:115]\n\t"
"v_mfma_f32_16x16x32_bf16 v[112:115], a[228:231], a[184:187], v[112:115]\n\t"
"ds_read_b64_tr_b16 v[224:225], v246 offset:33792\n\t"
"v_mfma_f32_16x16x32_bf16 v[112:115], a[212:215], a[188:191], v[112:115]\n\t"
"ds_write2st64_b64 v245, v[232:233], v[234:235] offset0:50 offset1:51\n\t"
"v_mfma_f32_16x16x32_bf16 v[116:119], a[224:227], a[184:187], v[116:119]\n\t"
"v_mfma_f32_16x16x32_bf16 v[116:119], a[228:231], a[188:191], v[116:119]\n\t"
"ds_read_b64_tr_b16 v[226:227], v246 offset:36864\n\t"
"v_mfma_f32_16x16x32_bf16 v[116:119], a[212:215], a[192:195], v[116:119]\n\t"
"ds_write_b64 v245, v[228:229] offset:26624\n\t"
"v_mfma_f32_16x16x32_bf16 v[120:123], a[224:227], a[188:191], v[120:123]\n\t"
"ds_read_b64_tr_b16 v[232:233], v246 offset:41472\n\t"
"v_mfma_f32_16x16x32_bf16 v[120:123], a[228:231], a[192:195], v[120:123]\n\t"
"ds_read_b64_tr_b16 v[234:235], v246 offset:44544\n\t"
"v_mfma_f32_16x16x32_bf16 v[120:123], a[212:215], a[196:199], v[120:123]\n\t"
"s_waitcnt lgkmcnt(3)\n\t"
"ds_read_b64_tr_b16 v[228:229], v246 offset:47616\n\t"
"v_mfma_f32_16x16x32_bf16 v[224:227], v[224:227], v[220:223], 0\n\t"
"ds_read_b64_tr_b16 v[230:231], v246 offset:50688\n\t"
"v_mfma_f32_16x16x32_bf16 v[124:127], a[224:227], a[192:195], v[124:127]\n\t"
"v_mfma_f32_16x16x32_bf16 v[124:127], a[228:231], a[196:199], v[124:127]\n\t"
"v_mfma_f32_16x16x32_bf16 v[124:127], a[212:215], a[200:203], v[124:127]\n\t"
"s_waitcnt lgkmcnt(2)\n\t"
"v_mfma_f32_16x16x32_bf16 v[232:235], v[232:235], v[220:223], 0\n\t"
"v_mfma_f32_16x16x32_bf16 v[128:131], a[224:227], a[196:199], v[128:131]\n\t"
"buffer_load_dwordx4 a[224:227], v252, s[8:11], s20 offen offset:1024 sc1\n\t"
"v_mfma_f32_16x16x32_bf16 v[128:131], a[228:231], a[200:203], v[128:131]\n\t"
"s_waitcnt vmcnt(11) lgkmcnt(0)\n\t"
"v_cvt_pk_bf16_f32 v224, v224, v225\n\t"
"v_cvt_pk_bf16_f32 v225, v226, v227\n\t"
"v_mfma_f32_16x16x32_bf16 v[128:131], a[212:215], a[204:207], v[128:131]\n\t"
"v_mfma_f32_16x16x32_bf16 v[228:231], v[228:231], v[220:223], 0\n\t"
"buffer_load_dwordx4 a[212:215], v252, s[8:11], s20 offen offset:2048 sc1\n\t"
"v_mfma_f32_16x16x32_bf16 v[132:135], a[232:235], a[168:171], v[132:135]\n\t"
"s_mov_b32 s20, 1548288\n\t"
"v_cvt_pk_bf16_f32 v226, v232, v233\n\t"
"v_cvt_pk_bf16_f32 v227, v234, v235\n\t"
"v_mfma_f32_16x16x32_bf16 v[132:135], a[236:239], a[172:175], v[132:135]\n\t"
"ds_read_b64_tr_b16 v[232:233], v246 offset:53760\n\t"
"v_mfma_f32_16x16x32_bf16 v[132:135], a[216:219], a[176:179], v[132:135]\n\t"
"buffer_load_dwordx4 a[228:231], v252, s[8:11], s20 offen offset:0 sc1\n\t"
"v_mfma_f32_16x16x32_bf16 v[136:139], a[232:235], a[172:175], v[136:139]\n\t"
"ds_read_b64_tr_b16 v[234:235], v246 offset:56832\n\t"
"v_mfma_f32_16x16x32_bf16 v[136:139], a[236:239], a[176:179], v[136:139]\n\t"
"v_cvt_pk_bf16_f32 v228, v228, v229\n\t"
"v_cvt_pk_bf16_f32 v229, v230, v231\n\t"
"v_mfma_f32_16x16x32_bf16 v[136:139], a[216:219], a[180:183], v[136:139]\n\t"
"ds_write2st64_b64 v245, v[224:225], v[226:227] offset0:53 offset1:54\n\t"
"v_mfma_f32_16x16x32_bf16 v[140:143], a[232:235], a[176:179], v[140:143]\n\t"
"v_mfma_f32_16x16x32_bf16 v[140:143], a[236:239], a[180:183], v[140:143]\n\t"
"v_mfma_f32_16x16x32_bf16 v[140:143], a[216:219], a[184:187], v[140:143]\n\t"
"v_mfma_f32_16x16x32_bf16 v[144:147], a[232:235], a[180:183], v[144:147]\n\t"
"v_mfma_f32_16x16x32_bf16 v[144:147], a[236:239], a[184:187], v[144:147]\n\t"
"v_mfma_f32_16x16x32_bf16 v[144:147], a[216:219], a[188:191], v[144:147]\n\t"
"v_mfma_f32_16x16x32_bf16 v[148:151], a[232:235], a[184:187], v[148:151]\n\t"
"s_waitcnt lgkmcnt(1)\n\t"
"v_mfma_f32_16x16x32_bf16 v[148:151], a[236:239], a[188:191], v[148:151]\n\t"
"v_mfma_f32_16x16x32_bf16 v[148:151], a[216:219], a[192:195], v[148:151]\n\t"
"v_mfma_f32_16x16x32_bf16 v[232:235], v[232:235], v[220:223], 0\n\t"
"v_mfma_f32_16x16x32_bf16 v[152:155], a[232:235], a[188:191], v[152:155]\n\t"
"v_mfma_f32_16x16x32_bf16 v[152:155], a[236:239], a[192:195], v[152:155]\n\t"
"v_mfma_f32_16x16x32_bf16 v[152:155], a[216:219], a[196:199], v[152:155]\n\t"
"v_mfma_f32_16x16x32_bf16 v[156:159], a[232:235], a[192:195], v[156:159]\n\t"
"v_mfma_f32_16x16x32_bf16 v[156:159], a[236:239], a[196:199], v[156:159]\n\t"
"v_cvt_pk_bf16_f32 v230, v232, v233\n\t"
"v_cvt_pk_bf16_f32 v231, v234, v235\n\t"
"v_mfma_f32_16x16x32_bf16 v[156:159], a[216:219], a[200:203], v[156:159]\n\t"
"ds_write2st64_b64 v245, v[228:229], v[230:231] offset0:55 offset1:56\n\t"
"v_mfma_f32_16x16x32_bf16 v[160:163], a[232:235], a[196:199], v[160:163]\n\t"
"buffer_load_dwordx4 a[232:235], v252, s[8:11], s20 offen offset:1024 sc1\n\t"
"v_mfma_f32_16x16x32_bf16 v[160:163], a[236:239], a[200:203], v[160:163]\n\t"
"s_waitcnt vmcnt(11)\n\t"
"v_mfma_f32_16x16x32_bf16 v[160:163], a[216:219], a[204:207], v[160:163]\n\t"
"v_mfma_f32_16x16x32_bf16 v[164:167], a[240:243], a[168:171], v[164:167]\n\t"
"buffer_load_dwordx4 a[216:219], v252, s[8:11], s20 offen offset:2048 sc1\n\t"
"v_mfma_f32_16x16x32_bf16 v[164:167], a[208:211], a[172:175], v[164:167]\n\t"
"s_mov_b32 s20, 1560576\n\t"
"v_mfma_f32_16x16x32_bf16 v[164:167], a[244:247], a[176:179], v[164:167]\n\t"
"s_waitcnt lgkmcnt(0)\n\t"
"s_barrier\n\t"
"v_mfma_f32_16x16x32_bf16 v[168:171], a[240:243], a[172:175], v[168:171]\n\t"
"ds_read_b128 a[168:171], v244 offset:24576\n\t"
"v_mfma_f32_16x16x32_bf16 v[168:171], a[208:211], a[176:179], v[168:171]\n\t"
"buffer_load_dwordx4 a[236:239], v252, s[8:11], s20 offen offset:0 sc1\n\t"
"v_mfma_f32_16x16x32_bf16 v[168:171], a[244:247], a[180:183], v[168:171]\n\t"
"ds_read_b128 a[172:175], v244 offset:25088\n\t"
"v_mfma_f32_16x16x32_bf16 v[172:175], a[240:243], a[176:179], v[172:175]\n\t"
"ds_read_b128 a[176:179], v244 offset:25600\n\t"
"v_mfma_f32_16x16x32_bf16 v[172:175], a[208:211], a[180:183], v[172:175]\n\t"
"v_mfma_f32_16x16x32_bf16 v[172:175], a[244:247], a[184:187], v[172:175]\n\t"
"v_mfma_f32_16x16x32_bf16 v[176:179], a[240:243], a[180:183], v[176:179]\n\t"
"ds_read_b128 a[180:183], v244 offset:26112\n\t"
"v_mfma_f32_16x16x32_bf16 v[176:179], a[208:211], a[184:187], v[176:179]\n\t"
"v_mfma_f32_16x16x32_bf16 v[176:179], a[244:247], a[188:191], v[176:179]\n\t"
"v_mfma_f32_16x16x32_bf16 v[180:183], a[240:243], a[184:187], v[180:183]\n\t"
"ds_read_b128 a[184:187], v244 offset:26624\n\t"
"v_mfma_f32_16x16x32_bf16 v[180:183], a[208:211], a[188:191], v[180:183]\n\t"
"v_mfma_f32_16x16x32_bf16 v[180:183], a[244:247], a[192:195], v[180:183]\n\t"
"v_mfma_f32_16x16x32_bf16 v[184:187], a[240:243], a[188:191], v[184:187]\n\t"
"ds_read_b128 a[188:191], v244 offset:27136\n\t"
"v_mfma_f32_16x16x32_bf16 v[184:187], a[208:211], a[192:195], v[184:187]\n\t"
"v_mfma_f32_16x16x32_bf16 v[184:187], a[244:247], a[196:199], v[184:187]\n\t"
"v_mfma_f32_16x16x32_bf16 v[188:191], a[240:243], a[192:195], v[188:191]\n\t"
"ds_read_b128 a[192:195], v244 offset:27648\n\t"
"v_mfma_f32_16x16x32_bf16 v[188:191], a[208:211], a[196:199], v[188:191]\n\t"
"v_mfma_f32_16x16x32_bf16 v[188:191], a[244:247], a[200:203], v[188:191]\n\t"
"v_mfma_f32_16x16x32_bf16 v[192:195], a[240:243], a[196:199], v[192:195]\n\t"
"buffer_load_dwordx4 a[240:243], v252, s[8:11], s20 offen offset:1024 sc1\n\t"
"v_mfma_f32_16x16x32_bf16 v[192:195], a[208:211], a[200:203], v[192:195]\n\t"
"s_waitcnt vmcnt(11) lgkmcnt(4)\n\t"
"ds_read_b128 a[196:199], v244 offset:28160\n\t"
"v_mfma_f32_16x16x32_bf16 v[192:195], a[244:247], a[204:207], v[192:195]\n\t"
"ds_read_b128 a[200:203], v244 offset:28672\n\t"
"Lst30_%=:\n\t"
"v_mfma_f32_16x16x32_bf16 v[0:3], v[96:99], a[168:171], v[0:3]\n\t"
"buffer_load_dwordx4 a[208:211], v252, s[8:11], s20 offen offset:2048 sc1\n\t"
"v_mfma_f32_16x16x32_bf16 v[0:3], a[248:251], a[172:175], v[0:3]\n\t"
"s_waitcnt lgkmcnt(5)\n\t"
"ds_read_b128 a[204:207], v244 offset:29184\n\t"
"v_xor_b32 v246, 0xc0, v246\n\t"
"v_mfma_f32_16x16x32_bf16 v[0:3], v[240:243], a[176:179], v[0:3]\n\t"
"s_mov_b32 s20, 1572864\n\t"
"v_mfma_f32_16x16x32_bf16 v[4:7], v[96:99], a[172:175], v[4:7]\n\t"
"buffer_load_dwordx4 a[244:247], v252, s[8:11], s20 offen offset:0 sc1\n\t"
"v_mfma_f32_16x16x32_bf16 v[4:7], a[248:251], a[176:179], v[4:7]\n\t"
"s_waitcnt lgkmcnt(5)\n\t"
"v_mfma_f32_16x16x32_bf16 v[4:7], v[240:243], a[180:183], v[4:7]\n\t"
"v_mfma_f32_16x16x32_bf16 v[8:11], v[96:99], a[176:179], v[8:11]\n\t"
"s_waitcnt lgkmcnt(4)\n\t"
"v_mfma_f32_16x16x32_bf16 v[8:11], a[248:251], a[180:183], v[8:11]\n\t"
"v_mfma_f32_16x16x32_bf16 v[8:11], v[240:243], a[184:187], v[8:11]\n\t"
"v_mfma_f32_16x16x32_bf16 v[12:15], v[96:99], a[180:183], v[12:15]\n\t"
"s_waitcnt lgkmcnt(3)\n\t"
"v_mfma_f32_16x16x32_bf16 v[12:15], a[248:251], a[184:187], v[12:15]\n\t"
"v_mfma_f32_16x16x32_bf16 v[12:15], v[240:243], a[188:191], v[12:15]\n\t"
"v_mfma_f32_16x16x32_bf16 v[16:19], v[96:99], a[184:187], v[16:19]\n\t"
"s_waitcnt lgkmcnt(2)\n\t"
"v_mfma_f32_16x16x32_bf16 v[16:19], a[248:251], a[188:191], v[16:19]\n\t"
"v_mfma_f32_16x16x32_bf16 v[16:19], v[240:243], a[192:195], v[16:19]\n\t"
"ds_read_b64_tr_b16 v[224:225], v246 offset:1536\n\t"
"v_mfma_f32_16x16x32_bf16 v[20:23], v[96:99], a[188:191], v[20:23]\n\t"
"s_waitcnt lgkmcnt(2)\n\t"
"ds_read_b64_tr_b16 v[226:227], v246 offset:4608\n\t"
"v_mfma_f32_16x16x32_bf16 v[20:23], a[248:251], a[192:195], v[20:23]\n\t"
"v_mfma_f32_16x16x32_bf16 v[20:23], v[240:243], a[196:199], v[20:23]\n\t"
"v_mfma_f32_16x16x32_bf16 v[24:27], v[96:99], a[192:195], v[24:27]\n\t"
"s_waitcnt lgkmcnt(2)\n\t"
"v_mfma_f32_16x16x32_bf16 v[24:27], a[248:251], a[196:199], v[24:27]\n\t"
"v_mfma_f32_16x16x32_bf16 v[24:27], v[240:243], a[200:203], v[24:27]\n\t"
"v_mfma_f32_16x16x32_bf16 v[28:31], v[96:99], a[196:199], v[28:31]\n\t"
"buffer_load_dwordx4 v[96:99], v252, s[8:11], s20 offen offset:1024 sc1\n\t"
"v_mfma_f32_16x16x32_bf16 v[28:31], a[248:251], a[200:203], v[28:31]\n\t"
"s_waitcnt vmcnt(11)\n\t"
"ds_read_b64_tr_b16 v[232:233], v246 offset:7680\n\t"
"v_mfma_f32_16x16x32_bf16 v[28:31], v[240:243], a[204:207], v[28:31]\n\t"
"ds_read_b64_tr_b16 v[234:235], v246 offset:10752\n\t"
"v_mfma_f32_16x16x32_bf16 v[32:35], v[236:239], a[168:171], v[32:35]\n\t"
"buffer_load_dwordx4 a[248:251], v252, s[8:11], s20 offen offset:2048 sc1\n\t"
"v_mfma_f32_16x16x32_bf16 v[32:35], a[252:255], a[172:175], v[32:35]\n\t"
"s_mov_b32 s20, 1585152\n\t"
"v_mfma_f32_16x16x32_bf16 v[32:35], v[248:251], a[176:179], v[32:35]\n\t"
"v_mfma_f32_16x16x32_bf16 v[36:39], v[236:239], a[172:175], v[36:39]\n\t"
"buffer_load_dwordx4 v[240:243], v252, s[8:11], s20 offen offset:0 sc1\n\t"
"v_mfma_f32_16x16x32_bf16 v[36:39], a[252:255], a[176:179], v[36:39]\n\t"
"s_waitcnt lgkmcnt(2)\n\t"
"v_mfma_f32_16x16x32_bf16 v[36:39], v[248:251], a[180:183], v[36:39]\n\t"
"v_mfma_f32_16x16x32_bf16 v[224:227], v[224:227], v[220:223], 0\n\t"
"v_mfma_f32_16x16x32_bf16 v[40:43], v[236:239], a[176:179], v[40:43]\n\t"
"ds_read_b64_tr_b16 v[228:229], v246 offset:13824\n\t"
"v_mfma_f32_16x16x32_bf16 v[40:43], a[252:255], a[180:183], v[40:43]\n\t"
"ds_read_b64_tr_b16 v[230:231], v246 offset:16896\n\t"
"v_mfma_f32_16x16x32_bf16 v[40:43], v[248:251], a[184:187], v[40:43]\n\t"
"v_mfma_f32_16x16x32_bf16 v[44:47], v[236:239], a[180:183], v[44:47]\n\t"
"s_waitcnt lgkmcnt(2)\n\t"
"v_mfma_f32_16x16x32_bf16 v[44:47], a[252:255], a[184:187], v[44:47]\n\t"
"v_cvt_pk_bf16_f32 v224, v224, v225\n\t"
"v_cvt_pk_bf16_f32 v225, v226, v227\n\t"
"v_mfma_f32_16x16x32_bf16 v[44:47], v[248:251], a[188:191], v[44:47]\n\t"
"v_mfma_f32_16x16x32_bf16 v[232:235], v[232:235], v[220:223], 0\n\t"
"v_mfma_f32_16x16x32_bf16 v[48:51], v[236:239], a[184:187], v[48:51]\n\t"
"v_mfma_f32_16x16x32_bf16 v[48:51], a[252:255], a[188:191], v[48:51]\n\t"
"v_mfma_f32_16x16x32_bf16 v[48:51], v[248:251], a[192:195], v[48:51]\n\t"
"v_mfma_f32_16x16x32_bf16 v[52:55], v[236:239], a[188:191], v[52:55]\n\t"
"v_mfma_f32_16x16x32_bf16 v[52:55], a[252:255], a[192:195], v[52:55]\n\t"
"v_cvt_pk_bf16_f32 v226, v232, v233\n\t"
"v_cvt_pk_bf16_f32 v227, v234, v235\n\t"
"v_mfma_f32_16x16x32_bf16 v[52:55], v[248:251], a[196:199], v[52:55]\n\t"
"ds_read_b64_tr_b16 v[232:233], v246 offset:21504\n\t"
"v_mfma_f32_16x16x32_bf16 v[56:59], v[236:239], a[192:195], v[56:59]\n\t"
"s_waitcnt lgkmcnt(1)\n\t"
"ds_read_b64_tr_b16 v[234:235], v246 offset:24576\n\t"
"v_mfma_f32_16x16x32_bf16 v[56:59], a[252:255], a[196:199], v[56:59]\n\t"
"v_mfma_f32_16x16x32_bf16 v[56:59], v[248:251], a[200:203], v[56:59]\n\t"
"v_mfma_f32_16x16x32_bf16 v[228:231], v[228:231], v[220:223], 0\n\t"
"v_mfma_f32_16x16x32_bf16 v[60:63], v[236:239], a[196:199], v[60:63]\n\t"
"buffer_load_dwordx4 v[236:239], v252, s[8:11], s20 offen offset:1024 sc1\n\t"
"v_mfma_f32_16x16x32_bf16 v[60:63], a[252:255], a[200:203], v[60:63]\n\t"
"s_waitcnt vmcnt(11)\n\t"
"v_mfma_f32_16x16x32_bf16 v[60:63], v[248:251], a[204:207], v[60:63]\n\t"
"v_mfma_f32_16x16x32_bf16 v[64:67], a[220:223], a[168:171], v[64:67]\n\t"
"buffer_load_dwordx4 v[248:251], v252, s[8:11], s20 offen offset:2048 sc1\n\t"
"v_mfma_f32_16x16x32_bf16 v[64:67], a[224:227], a[172:175], v[64:67]\n\t"
"s_mov_b32 s20, 1597440\n\t"
"ds_read_b128 a[168:171], v244 offset:33792\n\t"
"v_cvt_pk_bf16_f32 v228, v228, v229\n\t"
"v_mfma_f32_16x16x32_bf16 v[64:67], a[212:215], a[176:179], v[64:67]\n\t"
"v_cvt_pk_bf16_f32 v229, v230, v231\n\t"
"v_mfma_f32_16x16x32_bf16 v[68:71], a[220:223], a[172:175], v[68:71]\n\t"
"buffer_load_dwordx4 a[252:255], v252, s[8:11], s20 offen offset:0 sc1\n\t"
"v_mfma_f32_16x16x32_bf16 v[68:71], a[224:227], a[176:179], v[68:71]\n\t"
"s_waitcnt lgkmcnt(1)\n\t"
"ds_read_b128 a[172:175], v244 offset:34304\n\t"
"v_mfma_f32_16x16x32_bf16 v[68:71], a[212:215], a[180:183], v[68:71]\n\t"
"v_mfma_f32_16x16x32_bf16 v[232:235], v[232:235], v[220:223], 0\n\t"
"v_mfma_f32_16x16x32_bf16 v[72:75], a[220:223], a[176:179], v[72:75]\n\t"
"ds_read_b128 a[176:179], v244 offset:34816\n\t"
"v_mfma_f32_16x16x32_bf16 v[72:75], a[224:227], a[180:183], v[72:75]\n\t"
"v_mfma_f32_16x16x32_bf16 v[72:75], a[212:215], a[184:187], v[72:75]\n\t"
"v_mfma_f32_16x16x32_bf16 v[76:79], a[220:223], a[180:183], v[76:79]\n\t"
"ds_read_b128 a[180:183], v244 offset:35328\n\t"
"v_mfma_f32_16x16x32_bf16 v[76:79], a[224:227], a[184:187], v[76:79]\n\t"
"v_cvt_pk_bf16_f32 v230, v232, v233\n\t"
"v_cvt_pk_bf16_f32 v231, v234, v235\n\t"
"v_mfma_f32_16x16x32_bf16 v[76:79], a[212:215], a[188:191], v[76:79]\n\t"
"ds_read_b64_tr_b16 v[232:233], v246 offset:27648\n\t"
"v_mfma_f32_16x16x32_bf16 v[80:83], a[220:223], a[184:187], v[80:83]\n\t"
"ds_read_b64_tr_b16 v[234:235], v246 offset:30720\n\t"
"v_mfma_f32_16x16x32_bf16 v[80:83], a[224:227], a[188:191], v[80:83]\n\t"
"ds_read_b128 a[184:187], v244 offset:35840\n\t"
"v_mfma_f32_16x16x32_bf16 v[80:83], a[212:215], a[192:195], v[80:83]\n\t"
"v_mfma_f32_16x16x32_bf16 v[84:87], a[220:223], a[188:191], v[84:87]\n\t"
"ds_read_b128 a[188:191], v244 offset:36352\n\t"
"v_mfma_f32_16x16x32_bf16 v[84:87], a[224:227], a[192:195], v[84:87]\n\t"
"v_mfma_f32_16x16x32_bf16 v[84:87], a[212:215], a[196:199], v[84:87]\n\t"
"v_mfma_f32_16x16x32_bf16 v[88:91], a[220:223], a[192:195], v[88:91]\n\t"
"ds_read_b128 a[192:195], v244 offset:36864\n\t"
"v_mfma_f32_16x16x32_bf16 v[88:91], a[224:227], a[196:199], v[88:91]\n\t"
"v_mfma_f32_16x16x32_bf16 v[88:91], a[212:215], a[200:203], v[88:91]\n\t"
"v_mfma_f32_16x16x32_bf16 v[92:95], a[220:223], a[196:199], v[92:95]\n\t"
"buffer_load_dwordx4 a[220:223], v252, s[8:11], s20 offen offset:1024 sc1\n\t"
"v_mfma_f32_16x16x32_bf16 v[92:95], a[224:227], a[200:203], v[92:95]\n\t"
"s_waitcnt vmcnt(11) lgkmcnt(3)\n\t"
"ds_read_b128 a[196:199], v244 offset:37376\n\t"
"v_mfma_f32_16x16x32_bf16 v[92:95], a[212:215], a[204:207], v[92:95]\n\t"
"ds_read_b128 a[200:203], v244 offset:37888\n\t"
"v_mfma_f32_16x16x32_bf16 v[232:235], v[232:235], v[220:223], 0\n\t"
"ds_read_b128 a[204:207], v244 offset:38400\n\t"
"v_mfma_f32_16x16x32_bf16 v[100:103], a[228:231], a[168:171], v[100:103]\n\t"
"buffer_load_dwordx4 a[212:215], v252, s[8:11], s20 offen offset:2048 sc1\n\t"
"v_mfma_f32_16x16x32_bf16 v[100:103], a[232:235], a[172:175], v[100:103]\n\t"
"s_mov_b32 s20, 1609728\n\t"
"v_mfma_f32_16x16x32_bf16 v[100:103], a[216:219], a[176:179], v[100:103]\n\t"
"buffer_load_dwordx4 a[224:227], v252, s[8:11], s20 offen offset:0 sc1\n\t"
"v_mfma_f32_16x16x32_bf16 v[104:107], a[228:231], a[172:175], v[104:107]\n\t"
"s_waitcnt lgkmcnt(5)\n\t"
"v_mfma_f32_16x16x32_bf16 v[104:107], a[232:235], a[176:179], v[104:107]\n\t"
"v_cvt_pk_bf16_f32 v232, v232, v233\n\t"
"v_mfma_f32_16x16x32_bf16 v[104:107], a[216:219], a[180:183], v[104:107]\n\t"
"s_waitcnt lgkmcnt(0)\n\t"
"s_barrier\n\t"
"v_mfma_f32_16x16x32_bf16 v[108:111], a[228:231], a[176:179], v[108:111]\n\t"
"v_cvt_pk_bf16_f32 v233, v234, v235\n\t"
"v_mfma_f32_16x16x32_bf16 v[108:111], a[232:235], a[180:183], v[108:111]\n\t"
"v_fma_f32 v0, v100, s52, v0\n\t"
"v_fma_f32 v1, v101, s52, v1\n\t"
"v_mfma_f32_16x16x32_bf16 v[108:111], a[216:219], a[184:187], v[108:111]\n\t"
"ds_write2st64_b64 v245, v[224:225], v[226:227] offset0:48 offset1:49\n\t"
"v_mfma_f32_16x16x32_bf16 v[112:115], a[228:231], a[180:183], v[112:115]\n\t"
"v_fma_f32 v2, v102, s52, v2\n\t"
"v_fma_f32 v3, v103, s52, v3\n\t"
"v_mfma_f32_16x16x32_bf16 v[112:115], a[232:235], a[184:187], v[112:115]\n\t"
"ds_read_b64_tr_b16 v[224:225], v246 offset:33792\n\t"
"v_mfma_f32_16x16x32_bf16 v[112:115], a[216:219], a[188:191], v[112:115]\n\t"
"ds_write2st64_b64 v245, v[228:229], v[230:231] offset0:50 offset1:51\n\t"
"v_mfma_f32_16x16x32_bf16 v[116:119], a[228:231], a[184:187], v[116:119]\n\t"
"v_fma_f32 v100, v0, s54, -v100\n\t"
"v_fma_f32 v101, v1, s54, -v101\n\t"
"v_mfma_f32_16x16x32_bf16 v[116:119], a[232:235], a[188:191], v[116:119]\n\t"
"ds_read_b64_tr_b16 v[226:227], v246 offset:36864\n\t"
"v_mfma_f32_16x16x32_bf16 v[116:119], a[216:219], a[192:195], v[116:119]\n\t"
"ds_write_b64 v245, v[232:233] offset:26624\n\t"
"v_mfma_f32_16x16x32_bf16 v[120:123], a[228:231], a[188:191], v[120:123]\n\t"
"ds_read_b64_tr_b16 v[228:229], v246 offset:41472\n\t"
"v_fma_f32 v102, v2, s54, -v102\n\t"
"v_mfma_f32_16x16x32_bf16 v[120:123], a[232:235], a[192:195], v[120:123]\n\t"
"ds_read_b64_tr_b16 v[230:231], v246 offset:44544\n\t"
"v_fma_f32 v103, v3, s54, -v103\n\t"
"v_mfma_f32_16x16x32_bf16 v[120:123], a[216:219], a[196:199], v[120:123]\n\t"
"s_waitcnt lgkmcnt(3)\n\t"
"ds_read_b64_tr_b16 v[232:233], v246 offset:47616\n\t"
"v_fma_f32 v4, v104, s52, v4\n\t"
"v_mfma_f32_16x16x32_bf16 v[224:227], v[224:227], v[220:223], 0\n\t"
"ds_read_b64_tr_b16 v[234:235], v246 offset:50688\n\t"
"v_fma_f32 v5, v105, s52, v5\n\t"
"v_mfma_f32_16x16x32_bf16 v[124:127], a[228:231], a[192:195], v[124:127]\n\t"
"v_fma_f32 v6, v106, s52, v6\n\t"
"v_fma_f32 v7, v107, s52, v7\n\t"
"v_mfma_f32_16x16x32_bf16 v[124:127], a[232:235], a[196:199], v[124:127]\n\t"
"v_fma_f32 v104, v4, s54, -v104\n\t"
"v_fma_f32 v105, v5, s54, -v105\n\t"
"v_mfma_f32_16x16x32_bf16 v[124:127], a[216:219], a[200:203], v[124:127]\n\t"
"s_waitcnt lgkmcnt(2)\n\t"
"v_fma_f32 v106, v6, s54, -v106\n\t"
"v_fma_f32 v107, v7, s54, -v107\n\t"
"v_mfma_f32_16x16x32_bf16 v[228:231], v[228:231], v[220:223], 0\n\t"
"v_fma_f32 v8, v108, s52, v8\n\t"
"v_fma_f32 v9, v109, s52, v9\n\t"
"v_mfma_f32_16x16x32_bf16 v[128:131], a[228:231], a[196:199], v[128:131]\n\t"
"s_waitcnt lgkmcnt(0)\n\t"
"v_cvt_pk_bf16_f32 v224, v224, v225\n\t"
"v_cvt_pk_bf16_f32 v225, v226, v227\n\t"
"v_mfma_f32_16x16x32_bf16 v[128:131], a[232:235], a[200:203], v[128:131]\n\t"
"s_waitcnt vmcnt(10)\n\t"
"v_fma_f32 v10, v110, s52, v10\n\t"
"v_fma_f32 v11, v111, s52, v11\n\t"
"v_mfma_f32_16x16x32_bf16 v[128:131], a[216:219], a[204:207], v[128:131]\n\t"
"v_fma_f32 v108, v8, s54, -v108\n\t"
"v_fma_f32 v109, v9, s54, -v109\n\t"
"v_mfma_f32_16x16x32_bf16 v[232:235], v[232:235], v[220:223], 0\n\t"
"buffer_load_dwordx4 a[228:231], v252, s[8:11], s20 offen offset:1024 sc1\n\t"
"v_mfma_f32_16x16x32_bf16 v[132:135], a[236:239], a[168:171], v[132:135]\n\t"
"buffer_load_dwordx4 a[216:219], v252, s[8:11], s20 offen offset:2048 sc1\n\t"
"v_mfma_f32_16x16x32_bf16 v[132:135], a[240:243], a[172:175], v[132:135]\n\t"
"s_mov_b32 s20, 1622016\n\t"
"v_cvt_pk_bf16_f32 v226, v228, v229\n\t"
"v_cvt_pk_bf16_f32 v227, v230, v231\n\t"
"v_mfma_f32_16x16x32_bf16 v[132:135], a[208:211], a[176:179], v[132:135]\n\t"
"ds_write2st64_b64 v245, v[224:225], v[226:227] offset0:53 offset1:54\n\t"
"v_mfma_f32_16x16x32_bf16 v[136:139], a[236:239], a[172:175], v[136:139]\n\t"
"buffer_load_dwordx4 a[232:235], v252, s[8:11], s20 offen offset:0 sc1\n\t"
"v_mfma_f32_16x16x32_bf16 v[136:139], a[240:243], a[176:179], v[136:139]\n\t"
"ds_read_b64_tr_b16 v[228:229], v246 offset:53760\n\t"
"v_mfma_f32_16x16x32_bf16 v[136:139], a[208:211], a[180:183], v[136:139]\n\t"
"ds_read_b64_tr_b16 v[230:231], v246 offset:56832\n\t"
"v_cvt_pk_bf16_f32 v232, v232, v233\n\t"
"v_mfma_f32_16x16x32_bf16 v[140:143], a[236:239], a[176:179], v[140:143]\n\t"
"v_cvt_pk_bf16_f32 v233, v234, v235\n\t"
"v_fma_f32 v110, v10, s54, -v110\n\t"
"v_mfma_f32_16x16x32_bf16 v[140:143], a[240:243], a[180:183], v[140:143]\n\t"
"v_fma_f32 v111, v11, s54, -v111\n\t"
"v_fma_f32 v12, v112, s52, v12\n\t"
"v_mfma_f32_16x16x32_bf16 v[140:143], a[208:211], a[184:187], v[140:143]\n\t"
"v_fma_f32 v13, v113, s52, v13\n\t"
"v_fma_f32 v14, v114, s52, v14\n\t"
"v_mfma_f32_16x16x32_bf16 v[144:147], a[236:239], a[180:183], v[144:147]\n\t"
"v_fma_f32 v15, v115, s52, v15\n\t"
"v_fma_f32 v112, v12, s54, -v112\n\t"
"v_mfma_f32_16x16x32_bf16 v[144:147], a[240:243], a[184:187], v[144:147]\n\t"
"v_fma_f32 v113, v13, s54, -v113\n\t"
"v_fma_f32 v114, v14, s54, -v114\n\t"
"v_mfma_f32_16x16x32_bf16 v[144:147], a[208:211], a[188:191], v[144:147]\n\t"
"v_fma_f32 v115, v15, s54, -v115\n\t"
"v_fma_f32 v16, v116, s52, v16\n\t"
"v_mfma_f32_16x16x32_bf16 v[148:151], a[236:239], a[184:187], v[148:151]\n\t"
"s_waitcnt lgkmcnt(0)\n\t"
"v_fma_f32 v17, v117, s52, v17\n\t"
"v_fma_f32 v18, v118, s52, v18\n\t"
"v_mfma_f32_16x16x32_bf16 v[148:151], a[240:243], a[188:191], v[148:151]\n\t"
"v_fma_f32 v19, v119, s52, v19\n\t"
"v_fma_f32 v116, v16, s54, -v116\n\t"
"v_mfma_f32_16x16x32_bf16 v[148:151], a[208:211], a[192:195], v[148:151]\n\t"
"v_fma_f32 v117, v17, s54, -v117\n\t"
"v_fma_f32 v118, v18, s54, -v118\n\t"
"v_mfma_f32_16x16x32_bf16 v[228:231], v[228:231], v[220:223], 0\n\t"
"v_fma_f32 v119, v19, s54, -v119\n\t"
"v_fma_f32 v20, v120, s52, v20\n\t"
"v_mfma_f32_16x16x32_bf16 v[152:155], a[236:239], a[188:191], v[152:155]\n\t"
"v_fma_f32 v21, v121, s52, v21\n\t"
"v_fma_f32 v22, v122, s52, v22\n\t"
"v_mfma_f32_16x16x32_bf16 v[152:155], a[240:243], a[192:195], v[152:155]\n\t"
"v_fma_f32 v23, v123, s52, v23\n\t"
"v_fma_f32 v120, v20, s54, -v120\n\t"
"v_mfma_f32_16x16x32_bf16 v[152:155], a[208:211], a[196:199], v[152:155]\n\t"
"v_fma_f32 v121, v21, s54, -v121\n\t"
"v_fma_f32 v122, v22, s54, -v122\n\t"
"v_mfma_f32_16x16x32_bf16 v[156:159], a[236:239], a[192:195], v[156:159]\n\t"
"v_fma_f32 v123, v23, s54, -v123\n\t"
"v_fma_f32 v24, v124, s52, v24\n\t"
"v_mfma_f32_16x16x32_bf16 v[156:159], a[240:243], a[196:199], v[156:159]\n\t"
"v_cvt_pk_bf16_f32 v234, v228, v229\n\t"
"v_cvt_pk_bf16_f32 v235, v230, v231\n\t"
"v_mfma_f32_16x16x32_bf16 v[156:159], a[208:211], a[200:203], v[156:159]\n\t"
"ds_write2st64_b64 v245, v[232:233], v[234:235] offset0:55 offset1:56\n\t"
"v_mfma_f32_16x16x32_bf16 v[160:163], a[236:239], a[196:199], v[160:163]\n\t"
"buffer_load_dwordx4 a[236:239], v252, s[8:11], s20 offen offset:1024 sc1\n\t"
"v_mfma_f32_16x16x32_bf16 v[160:163], a[240:243], a[200:203], v[160:163]\n\t"
"s_waitcnt vmcnt(11)\n\t"
"v_fma_f32 v25, v125, s52, v25\n\t"
"v_fma_f32 v26, v126, s52, v26\n\t"
"v_mfma_f32_16x16x32_bf16 v[160:163], a[208:211], a[204:207], v[160:163]\n\t"
"v_fma_f32 v27, v127, s52, v27\n\t"
"v_fma_f32 v124, v24, s54, -v124\n\t"
"v_mfma_f32_16x16x32_bf16 v[164:167], a[244:247], a[168:171], v[164:167]\n\t"
"v_fma_f32 v125, v25, s54, -v125\n\t"
"v_fma_f32 v126, v26, s54, -v126\n\t"
"v_mfma_f32_16x16x32_bf16 v[164:167], v[96:99], a[172:175], v[164:167]\n\t"
"v_fma_f32 v127, v27, s54, -v127\n\t"
"v_mfma_f32_16x16x32_bf16 v[164:167], a[248:251], a[176:179], v[164:167]\n\t"
"s_waitcnt lgkmcnt(0)\n\t"
"s_barrier\n\t"
"v_mfma_f32_16x16x32_bf16 v[168:171], a[244:247], a[172:175], v[168:171]\n\t"
"ds_read_b128 a[168:171], v244 offset:24576\n\t"
"v_mfma_f32_16x16x32_bf16 v[168:171], v[96:99], a[176:179], v[168:171]\n\t"
"buffer_load_dwordx4 a[208:211], v252, s[8:11], s20 offen offset:2048 sc1\n\t"
"v_mfma_f32_16x16x32_bf16 v[168:171], a[248:251], a[180:183], v[168:171]\n\t"
"s_mov_b32 s20, 1634304\n\t"
"ds_read_b128 a[172:175], v244 offset:25088\n\t"
"v_fma_f32 v28, v128, s52, v28\n\t"
"v_mfma_f32_16x16x32_bf16 v[172:175], a[244:247], a[176:179], v[172:175]\n\t"
"Lpg22_%=:\n\t"
"s_cbranch_vccnz Lvr3_%=\n\t"
"ds_read_b128 a[176:179], v244 offset:25600\n\t"
"v_fma_f32 v29, v129, s52, v29\n\t"
"v_mfma_f32_16x16x32_bf16 v[172:175], v[96:99], a[180:183], v[172:175]\n\t"
"v_fma_f32 v30, v130, s52, v30\n\t"
"v_fma_f32 v31, v131, s52, v31\n\t"
"v_mfma_f32_16x16x32_bf16 v[172:175], a[248:251], a[184:187], v[172:175]\n\t"
"buffer_load_dwordx4 a[240:243], v252, s[8:11], s20 offen offset:0 sc1\n\t"
"v_mfma_f32_16x16x32_bf16 v[176:179], a[244:247], a[180:183], v[176:179]\n\t"
"ds_read_b128 a[180:183], v244 offset:26112\n\t"
"v_fma_f32 v128, v28, s54, -v128\n\t"
"v_mfma_f32_16x16x32_bf16 v[176:179], v[96:99], a[184:187], v[176:179]\n\t"
"v_fma_f32 v129, v29, s54, -v129\n\t"
"v_fma_f32 v130, v30, s54, -v130\n\t"
"v_mfma_f32_16x16x32_bf16 v[176:179], a[248:251], a[188:191], v[176:179]\n\t"
"v_fma_f32 v131, v31, s54, -v131\n\t"
"v_fma_f32 v32, v132, s52, v32\n\t"
"v_mfma_f32_16x16x32_bf16 v[180:183], a[244:247], a[184:187], v[180:183]\n\t"
"ds_read_b128 a[184:187], v244 offset:26624\n\t"
"v_fma_f32 v33, v133, s52, v33\n\t"
"v_mfma_f32_16x16x32_bf16 v[180:183], v[96:99], a[188:191], v[180:183]\n\t"
"v_fma_f32 v34, v134, s52, v34\n\t"
"v_fma_f32 v35, v135, s52, v35\n\t"
"v_mfma_f32_16x16x32_bf16 v[180:183], a[248:251], a[192:195], v[180:183]\n\t"
"v_fma_f32 v132, v32, s54, -v132\n\t"
"v_fma_f32 v133, v33, s54, -v133\n\t"
"v_mfma_f32_16x16x32_bf16 v[184:187], a[244:247], a[188:191], v[184:187]\n\t"
"ds_read_b128 a[188:191], v244 offset:27136\n\t"
"v_fma_f32 v134, v34, s54, -v134\n\t"
"v_mfma_f32_16x16x32_bf16 v[184:187], v[96:99], a[192:195], v[184:187]\n\t"
"v_fma_f32 v135, v35, s54, -v135\n\t"
"v_fma_f32 v36, v136, s52, v36\n\t"
"v_mfma_f32_16x16x32_bf16 v[184:187], a[248:251], a[196:199], v[184:187]\n\t"
"v_fma_f32 v37, v137, s52, v37\n\t"
"v_fma_f32 v38, v138, s52, v38\n\t"
"v_mfma_f32_16x16x32_bf16 v[188:191], a[244:247], a[192:195], v[188:191]\n\t"
"ds_read_b128 a[192:195], v244 offset:27648\n\t"
"v_fma_f32 v39, v139, s52, v39\n\t"
"v_mfma_f32_16x16x32_bf16 v[188:191], v[96:99], a[196:199], v[188:191]\n\t"
"v_fma_f32 v136, v36, s54, -v136\n\t"
"v_fma_f32 v137, v37, s54, -v137\n\t"
"v_mfma_f32_16x16x32_bf16 v[188:191], a[248:251], a[200:203], v[188:191]\n\t"
"v_fma_f32 v138, v38, s54, -v138\n\t"
"v_fma_f32 v139, v39, s54, -v139\n\t"
"v_mfma_f32_16x16x32_bf16 v[192:195], a[244:247], a[196:199], v[192:195]\n\t"
"s_waitcnt vmcnt(10) lgkmcnt(4)\n\t"
"ds_read_b128 a[196:199], v244 offset:28160\n\t"
"v_fma_f32 v40, v140, s52, v40\n\t"
"v_mfma_f32_16x16x32_bf16 v[192:195], v[96:99], a[200:203], v[192:195]\n\t"
"ds_read_b128 a[200:203], v244 offset:28672\n\t"
"v_fma_f32 v41, v141, s52, v41\n\t"
"v_mfma_f32_16x16x32_bf16 v[192:195], a[248:251], a[204:207], v[192:195]\n\t"
"ds_read_b128 a[204:207], v244 offset:29184\n\t"
"v_fma_f32 v42, v142, s52, v42\n\t"
"Lst31_%=:\n\t"
"v_mfma_f32_16x16x32_bf16 v[196:199], v[240:243], a[168:171], v[196:199]\n\t"
"s_waitcnt lgkmcnt(6)\n\t"
"v_fma_f32 v43, v143, s52, v43\n\t"
"v_fma_f32 v140, v40, s54, -v140\n\t"
"v_mfma_f32_16x16x32_bf16 v[196:199], v[236:239], a[172:175], v[196:199]\n\t"
"v_fma_f32 v141, v41, s54, -v141\n\t"
"v_fma_f32 v142, v42, s54, -v142\n\t"
"v_mfma_f32_16x16x32_bf16 v[196:199], v[248:251], a[176:179], v[196:199]\n\t"
"buffer_load_dwordx4 a[244:247], v252, s[8:11], s20 offen offset:1024 sc1\n\t"
"v_mfma_f32_16x16x32_bf16 v[204:207], v[240:243], a[172:175], v[204:207]\n\t"
"buffer_load_dwordx4 a[248:251], v252, s[8:11], s20 offen offset:2048 sc1\n\t"
"v_mfma_f32_16x16x32_bf16 v[204:207], v[236:239], a[176:179], v[204:207]\n\t"
"s_waitcnt lgkmcnt(5)\n\t"
"v_fma_f32 v143, v43, s54, -v143\n\t"
"v_fma_f32 v44, v144, s52, v44\n\t"
"v_mfma_f32_16x16x32_bf16 v[204:207], v[248:251], a[180:183], v[204:207]\n\t"
"v_fma_f32 v45, v145, s52, v45\n\t"
"v_fma_f32 v46, v146, s52, v46\n\t"
"v_mfma_f32_16x16x32_bf16 v[212:215], v[240:243], a[176:179], v[212:215]\n\t"
"s_waitcnt lgkmcnt(4)\n\t"
"v_fma_f32 v47, v147, s52, v47\n\t"
"v_fma_f32 v144, v44, s54, -v144\n\t"
"v_mfma_f32_16x16x32_bf16 v[212:215], v[236:239], a[180:183], v[212:215]\n\t"
"v_fma_f32 v145, v45, s54, -v145\n\t"
"v_fma_f32 v146, v46, s54, -v146\n\t"
"v_mfma_f32_16x16x32_bf16 v[212:215], v[248:251], a[184:187], v[212:215]\n\t"
"v_fma_f32 v147, v47, s54, -v147\n\t"
"v_fma_f32 v48, v148, s52, v48\n\t"
"v_mfma_f32_16x16x32_bf16 a[0:3], v[240:243], a[180:183], a[0:3]\n\t"
"s_waitcnt lgkmcnt(3)\n\t"
"v_fma_f32 v49, v149, s52, v49\n\t"
"v_fma_f32 v50, v150, s52, v50\n\t"
"v_mfma_f32_16x16x32_bf16 a[0:3], v[236:239], a[184:187], a[0:3]\n\t"
"v_fma_f32 v51, v151, s52, v51\n\t"
"v_fma_f32 v148, v48, s54, -v148\n\t"
"v_mfma_f32_16x16x32_bf16 a[0:3], v[248:251], a[188:191], a[0:3]\n\t"
"v_fma_f32 v149, v49, s54, -v149\n\t"
"v_fma_f32 v150, v50, s54, -v150\n\t"
"v_mfma_f32_16x16x32_bf16 a[8:11], v[240:243], a[184:187], a[8:11]\n\t"
"s_waitcnt lgkmcnt(2)\n\t"
"v_fma_f32 v151, v51, s54, -v151\n\t"
"v_fma_f32 v52, v152, s52, v52\n\t"
"v_mfma_f32_16x16x32_bf16 a[8:11], v[236:239], a[188:191], a[8:11]\n\t"
"v_fma_f32 v53, v153, s52, v53\n\t"
"v_fma_f32 v54, v154, s52, v54\n\t"
"v_mfma_f32_16x16x32_bf16 a[8:11], v[248:251], a[192:195], a[8:11]\n\t"
"v_fma_f32 v55, v155, s52, v55\n\t"
"v_fma_f32 v152, v52, s54, -v152\n\t"
"v_mfma_f32_16x16x32_bf16 a[16:19], v[240:243], a[188:191], a[16:19]\n\t"
"s_waitcnt lgkmcnt(1)\n\t"
"v_fma_f32 v153, v53, s54, -v153\n\t"
"v_fma_f32 v154, v54, s54, -v154\n\t"
"v_mfma_f32_16x16x32_bf16 a[16:19], v[236:239], a[192:195], a[16:19]\n\t"
"s_mov_b32 s20, 1646592\n\t"
"v_fma_f32 v155, v55, s54, -v155\n\t"
"v_fma_f32 v56, v156, s52, v56\n\t"
"v_mfma_f32_16x16x32_bf16 a[16:19], v[248:251], a[196:199], a[16:19]\n\t"
"v_fma_f32 v57, v157, s52, v57\n\t"
"v_fma_f32 v58, v158, s52, v58\n\t"
"v_mfma_f32_16x16x32_bf16 a[24:27], v[240:243], a[192:195], a[24:27]\n\t"
"s_waitcnt lgkmcnt(0)\n\t"
"v_fma_f32 v59, v159, s52, v59\n\t"
"v_fma_f32 v156, v56, s54, -v156\n\t"
"v_mfma_f32_16x16x32_bf16 a[24:27], v[236:239], a[196:199], a[24:27]\n\t"
"v_fma_f32 v157, v57, s54, -v157\n\t"
"v_fma_f32 v158, v58, s54, -v158\n\t"
"v_mfma_f32_16x16x32_bf16 a[24:27], v[248:251], a[200:203], a[24:27]\n\t"
"v_fma_f32 v159, v59, s54, -v159\n\t"
"v_fma_f32 v60, v160, s52, v60\n\t"
"v_mfma_f32_16x16x32_bf16 a[32:35], v[240:243], a[196:199], a[32:35]\n\t"
"s_waitcnt vmcnt(9)\n\t"
"v_fma_f32 v61, v161, s52, v61\n\t"
"v_fma_f32 v62, v162, s52, v62\n\t"
"v_mfma_f32_16x16x32_bf16 a[32:35], v[236:239], a[200:203], a[32:35]\n\t"
"v_fma_f32 v63, v163, s52, v63\n\t"
"v_fma_f32 v160, v60, s54, -v160\n\t"
"v_mfma_f32_16x16x32_bf16 a[32:35], v[248:251], a[204:207], a[32:35]\n\t"
"v_fma_f32 v161, v61, s54, -v161\n\t"
"v_fma_f32 v162, v62, s54, -v162\n\t"
"v_mfma_f32_16x16x32_bf16 a[40:43], a[252:255], a[168:171], a[40:43]\n\t"
"v_fma_f32 v163, v63, s54, -v163\n\t"
"v_fma_f32 v64, v164, s52, v64\n\t"
"v_mfma_f32_16x16x32_bf16 a[40:43], a[220:223], a[172:175], a[40:43]\n\t"
"v_fma_f32 v65, v165, s52, v65\n\t"
"v_fma_f32 v66, v166, s52, v66\n\t"
"v_mfma_f32_16x16x32_bf16 a[40:43], a[212:215], a[176:179], a[40:43]\n\t"
"buffer_load_dwordx4 v[240:243], v252, s[8:11], s20 offen offset:0 sc1\n\t"
"v_mfma_f32_16x16x32_bf16 a[48:51], a[252:255], a[172:175], a[48:51]\n\t"
"v_fma_f32 v67, v167, s52, v67\n\t"
"v_fma_f32 v164, v64, s54, -v164\n\t"
"v_mfma_f32_16x16x32_bf16 a[48:51], a[220:223], a[176:179], a[48:51]\n\t"
"v_fma_f32 v165, v65, s54, -v165\n\t"
"v_fma_f32 v166, v66, s54, -v166\n\t"
"v_mfma_f32_16x16x32_bf16 a[48:51], a[212:215], a[180:183], a[48:51]\n\t"
"v_fma_f32 v167, v67, s54, -v167\n\t"
"v_fma_f32 v68, v168, s52, v68\n\t"
"v_mfma_f32_16x16x32_bf16 a[56:59], a[252:255], a[176:179], a[56:59]\n\t"
"v_fma_f32 v69, v169, s52, v69\n\t"
"v_fma_f32 v70, v170, s52, v70\n\t"
"v_mfma_f32_16x16x32_bf16 a[56:59], a[220:223], a[180:183], a[56:59]\n\t"
"v_fma_f32 v71, v171, s52, v71\n\t"
"v_fma_f32 v168, v68, s54, -v168\n\t"
"v_mfma_f32_16x16x32_bf16 a[56:59], a[212:215], a[184:187], a[56:59]\n\t"
"v_fma_f32 v169, v69, s54, -v169\n\t"
"v_fma_f32 v170, v70, s54, -v170\n\t"
"v_mfma_f32_16x16x32_bf16 a[64:67], a[252:255], a[180:183], a[64:67]\n\t"
"v_fma_f32 v171, v71, s54, -v171\n\t"
"v_fma_f32 v72, v172, s52, v72\n\t"
"v_mfma_f32_16x16x32_bf16 a[64:67], a[220:223], a[184:187], a[64:67]\n\t"
"v_fma_f32 v73, v173, s52, v73\n\t"
"v_fma_f32 v74, v174, s52, v74\n\t"
"v_mfma_f32_16x16x32_bf16 a[64:67], a[212:215], a[188:191], a[64:67]\n\t"
"v_fma_f32 v75, v175, s52, v75\n\t"
"v_fma_f32 v172, v72, s54, -v172\n\t"
"v_mfma_f32_16x16x32_bf16 a[72:75], a[252:255], a[184:187], a[72:75]\n\t"
"v_fma_f32 v173, v73, s54, -v173\n\t"
"v_fma_f32 v174, v74, s54, -v174\n\t"
"v_mfma_f32_16x16x32_bf16 a[72:75], a[220:223], a[188:191], a[72:75]\n\t"
"v_fma_f32 v175, v75, s54, -v175\n\t"
"v_fma_f32 v76, v176, s52, v76\n\t"
"v_mfma_f32_16x16x32_bf16 a[72:75], a[212:215], a[192:195], a[72:75]\n\t"
"v_fma_f32 v77, v177, s52, v77\n\t"
"v_fma_f32 v78, v178, s52, v78\n\t"
"v_mfma_f32_16x16x32_bf16 a[80:83], a[252:255], a[188:191], a[80:83]\n\t"
"v_fma_f32 v79, v179, s52, v79\n\t"
"v_fma_f32 v176, v76, s54, -v176\n\t"
"v_mfma_f32_16x16x32_bf16 a[80:83], a[220:223], a[192:195], a[80:83]\n\t"
"v_fma_f32 v177, v77, s54, -v177\n\t"
"v_fma_f32 v178, v78, s54, -v178\n\t"
"v_mfma_f32_16x16x32_bf16 a[80:83], a[212:215], a[196:199], a[80:83]\n\t"
"v_fma_f32 v179, v79, s54, -v179\n\t"
"v_fma_f32 v80, v180, s52, v80\n\t"
"v_mfma_f32_16x16x32_bf16 a[88:91], a[252:255], a[192:195], a[88:91]\n\t"
"v_fma_f32 v81, v181, s52, v81\n\t"
"v_fma_f32 v82, v182, s52, v82\n\t"
"v_mfma_f32_16x16x32_bf16 a[88:91], a[220:223], a[196:199], a[88:91]\n\t"
"v_fma_f32 v83, v183, s52, v83\n\t"
"v_fma_f32 v180, v80, s54, -v180\n\t"
"v_mfma_f32_16x16x32_bf16 a[88:91], a[212:215], a[200:203], a[88:91]\n\t"
"v_fma_f32 v181, v81, s54, -v181\n\t"
"v_fma_f32 v182, v82, s54, -v182\n\t"
"v_mfma_f32_16x16x32_bf16 a[96:99], a[252:255], a[196:199], a[96:99]\n\t"
"s_waitcnt vmcnt(7)\n\t"
"v_fma_f32 v183, v83, s54, -v183\n\t"
"v_fma_f32 v84, v184, s52, v84\n\t"
"v_mfma_f32_16x16x32_bf16 a[96:99], a[220:223], a[200:203], a[96:99]\n\t"
"v_fma_f32 v85, v185, s52, v85\n\t"
"v_fma_f32 v86, v186, s52, v86\n\t"
"v_mfma_f32_16x16x32_bf16 a[96:99], a[212:215], a[204:207], a[96:99]\n\t"
"v_fma_f32 v87, v187, s52, v87\n\t"
"v_fma_f32 v184, v84, s54, -v184\n\t"
"v_mfma_f32_16x16x32_bf16 a[104:107], a[224:227], a[168:171], a[104:107]\n\t"
"ds_read_b128 a[168:171], v244 offset:33792\n\t"
"v_fma_f32 v185, v85, s54, -v185\n\t"
"v_mfma_f32_16x16x32_bf16 a[104:107], a[228:231], a[172:175], a[104:107]\n\t"
"v_fma_f32 v186, v86, s54, -v186\n\t"
"v_fma_f32 v187, v87, s54, -v187\n\t"
"v_mfma_f32_16x16x32_bf16 a[104:107], a[216:219], a[176:179], a[104:107]\n\t"
"buffer_load_dwordx4 a[252:255], v252, s[8:11], s20 offen offset:1024 sc1\n\t"
"v_mfma_f32_16x16x32_bf16 a[112:115], a[224:227], a[172:175], a[112:115]\n\t"
"buffer_load_dwordx4 a[212:215], v252, s[8:11], s20 offen offset:2048 sc1\n\t"
"v_mfma_f32_16x16x32_bf16 a[112:115], a[228:231], a[176:179], a[112:115]\n\t"
"s_mov_b32 s20, 1179648\n\t"
"ds_read_b128 a[172:175], v244 offset:34304\n\t"
"v_fma_f32 v88, v188, s52, v88\n\t"
"v_mfma_f32_16x16x32_bf16 a[112:115], a[216:219], a[180:183], a[112:115]\n\t"
"v_fma_f32 v89, v189, s52, v89\n\t"
"v_fma_f32 v90, v190, s52, v90\n\t"
"v_mfma_f32_16x16x32_bf16 a[120:123], a[224:227], a[176:179], a[120:123]\n\t"
"ds_read_b128 a[176:179], v244 offset:34816\n\t"
"v_fma_f32 v91, v191, s52, v91\n\t"
"v_mfma_f32_16x16x32_bf16 a[120:123], a[228:231], a[180:183], a[120:123]\n\t"
"v_fma_f32 v188, v88, s54, -v188\n\t"
"v_fma_f32 v189, v89, s54, -v189\n\t"
"v_mfma_f32_16x16x32_bf16 a[120:123], a[216:219], a[184:187], a[120:123]\n\t"
"buffer_load_dwordx4 a[220:223], v252, s[8:11], s20 offen offset:0 sc1\n\t"
"v_mfma_f32_16x16x32_bf16 a[128:131], a[224:227], a[180:183], a[128:131]\n\t"
"ds_read_b128 a[180:183], v244 offset:35328\n\t"
"v_fma_f32 v190, v90, s54, -v190\n\t"
"v_mfma_f32_16x16x32_bf16 a[128:131], a[228:231], a[184:187], a[128:131]\n\t"
"v_fma_f32 v191, v91, s54, -v191\n\t"
"v_fma_f32 v92, v192, s52, v92\n\t"
"v_mfma_f32_16x16x32_bf16 a[128:131], a[216:219], a[188:191], a[128:131]\n\t"
"v_fma_f32 v93, v193, s52, v93\n\t"
"v_fma_f32 v94, v194, s52, v94\n\t"
"v_mfma_f32_16x16x32_bf16 a[136:139], a[224:227], a[184:187], a[136:139]\n\t"
"ds_read_b128 a[184:187], v244 offset:35840\n\t"
"v_fma_f32 v95, v195, s52, v95\n\t"
"v_mfma_f32_16x16x32_bf16 a[136:139], a[228:231], a[188:191], a[136:139]\n\t"
"v_fma_f32 v192, v92, s54, -v192\n\t"
"v_fma_f32 v193, v93, s54, -v193\n\t"
"v_mfma_f32_16x16x32_bf16 a[136:139], a[216:219], a[192:195], a[136:139]\n\t"
"v_fma_f32 v194, v94, s54, -v194\n\t"
"v_fma_f32 v195, v95, s54, -v195\n\t"
"v_mfma_f32_16x16x32_bf16 a[144:147], a[224:227], a[188:191], a[144:147]\n\t"
"ds_read_b128 a[188:191], v244 offset:36352\n\t"
"v_mfma_f32_16x16x32_bf16 a[144:147], a[228:231], a[192:195], a[144:147]\n\t"
"v_mfma_f32_16x16x32_bf16 a[144:147], a[216:219], a[196:199], a[144:147]\n\t"
"v_mfma_f32_16x16x32_bf16 a[152:155], a[224:227], a[192:195], a[152:155]\n\t"
"ds_read_b128 a[192:195], v244 offset:36864\n\t"
"v_mfma_f32_16x16x32_bf16 a[152:155], a[228:231], a[196:199], a[152:155]\n\t"
"v_mfma_f32_16x16x32_bf16 a[152:155], a[216:219], a[200:203], a[152:155]\n\t"
"v_mfma_f32_16x16x32_bf16 a[160:163], a[224:227], a[196:199], a[160:163]\n\t"
"buffer_load_dwordx4 a[224:227], v252, s[8:11], s20 offen offset:1024 sc1\n\t"
"v_mfma_f32_16x16x32_bf16 a[160:163], a[228:231], a[200:203], a[160:163]\n\t"
"s_waitcnt vmcnt(8) lgkmcnt(4)\n\t"
"ds_read_b128 a[196:199], v244 offset:37376\n\t"
"v_mfma_f32_16x16x32_bf16 a[160:163], a[216:219], a[204:207], a[160:163]\n\t"
"ds_read_b128 a[200:203], v244 offset:37888\n\t"
"v_mfma_f32_16x16x32_bf16 v[200:203], a[232:235], a[168:171], v[200:203]\n\t"
"s_waitcnt lgkmcnt(5)\n\t"
"ds_read_b128 a[204:207], v244 offset:38400\n\t"
"v_mfma_f32_16x16x32_bf16 v[200:203], a[236:239], a[172:175], v[200:203]\n\t"
"buffer_load_dwordx4 a[216:219], v252, s[8:11], s20 offen offset:2048 sc1\n\t"
"v_mfma_f32_16x16x32_bf16 v[200:203], a[208:211], a[176:179], v[200:203]\n\t"
"s_mov_b32 s20, 1191936\n\t"
"v_mfma_f32_16x16x32_bf16 v[208:211], a[232:235], a[172:175], v[208:211]\n\t"
"buffer_load_dwordx4 a[228:231], v252, s[8:11], s20 offen offset:0 sc1\n\t"
"v_mfma_f32_16x16x32_bf16 v[208:211], a[236:239], a[176:179], v[208:211]\n\t"
"s_waitcnt lgkmcnt(5)\n\t"
"v_mfma_f32_16x16x32_bf16 v[208:211], a[208:211], a[180:183], v[208:211]\n\t"
"s_waitcnt lgkmcnt(0)\n\t"
"s_barrier\n\t"
"v_mfma_f32_16x16x32_bf16 v[216:219], a[232:235], a[176:179], v[216:219]\n\t"
"v_mfma_f32_16x16x32_bf16 v[216:219], a[236:239], a[180:183], v[216:219]\n\t"
"v_mfma_f32_16x16x32_bf16 v[216:219], a[208:211], a[184:187], v[216:219]\n\t"
"v_pk_fma_f32 v[196:197], v[200:201], s[56:57], v[196:197]\n\t"
"v_pk_fma_f32 v[200:201], v[196:197], s[58:59], v[200:201] neg_lo:[0,0,1] neg_hi:[0,0,1]\n\t"
"v_pk_fma_f32 v[228:229], v[0:1], s[60:61], v[196:197]\n\t"
"v_pk_fma_f32 v[0:1], v[196:197], s[60:61], v[0:1]\n\t"
"v_pk_fma_f32 v[196:197], v[100:101], s[62:63], v[200:201]\n\t"
"v_pk_fma_f32 v[100:101], v[200:201], s[62:63], v[100:101]\n\t"
"v_pk_fma_f32 v[198:199], v[202:203], s[56:57], v[198:199]\n\t"
"v_pk_fma_f32 v[202:203], v[198:199], s[58:59], v[202:203] neg_lo:[0,0,1] neg_hi:[0,0,1]\n\t"
"v_pk_fma_f32 v[234:235], v[2:3], s[60:61], v[198:199]\n\t"
"v_pk_fma_f32 v[2:3], v[198:199], s[60:61], v[2:3]\n\t"
"v_pk_fma_f32 v[198:199], v[102:103], s[62:63], v[202:203]\n\t"
"v_pk_fma_f32 v[102:103], v[202:203], s[62:63], v[102:103]\n\t"
"v_mfma_f32_16x16x32_bf16 a[4:7], a[232:235], a[180:183], a[4:7]\n\t"
"v_cvt_pk_bf16_f32 v196, v196, v197\n\t"
"v_cvt_pk_bf16_f32 v200, v228, v229\n\t"
"v_mfma_f32_16x16x32_bf16 a[4:7], a[236:239], a[184:187], a[4:7]\n\t"
"v_cvt_pk_bf16_f32 v197, v198, v199\n\t"
"v_cvt_pk_bf16_f32 v201, v234, v235\n\t"
"v_mfma_f32_16x16x32_bf16 a[4:7], a[208:211], a[188:191], a[4:7]\n\t"
"v_pk_fma_f32 v[204:205], v[208:209], s[56:57], v[204:205]\n\t"
"v_pk_fma_f32 v[208:209], v[204:205], s[58:59], v[208:209] neg_lo:[0,0,1] neg_hi:[0,0,1]\n\t"
"v_pk_fma_f32 v[98:99], v[4:5], s[60:61], v[204:205]\n\t"
"v_pk_fma_f32 v[4:5], v[204:205], s[60:61], v[4:5]\n\t"
"v_pk_fma_f32 v[204:205], v[104:105], s[62:63], v[208:209]\n\t"
"v_pk_fma_f32 v[104:105], v[208:209], s[62:63], v[104:105]\n\t"
"v_mfma_f32_16x16x32_bf16 a[12:15], a[232:235], a[184:187], a[12:15]\n\t"
"v_cvt_pk_bf16_f32 v204, v204, v205\n\t"
"v_cvt_pk_bf16_f32 v208, v98, v99\n\t"
"v_mfma_f32_16x16x32_bf16 a[12:15], a[236:239], a[188:191], a[12:15]\n\t"
"v_mfma_f32_16x16x32_bf16 a[12:15], a[208:211], a[192:195], a[12:15]\n\t"
"v_pk_fma_f32 v[206:207], v[210:211], s[56:57], v[206:207]\n\t"
"v_pk_fma_f32 v[210:211], v[206:207], s[58:59], v[210:211] neg_lo:[0,0,1] neg_hi:[0,0,1]\n\t"
"v_pk_fma_f32 v[228:229], v[6:7], s[60:61], v[206:207]\n\t"
"v_pk_fma_f32 v[6:7], v[206:207], s[60:61], v[6:7]\n\t"
"v_pk_fma_f32 v[206:207], v[106:107], s[62:63], v[210:211]\n\t"
"v_pk_fma_f32 v[106:107], v[210:211], s[62:63], v[106:107]\n\t"
"v_mfma_f32_16x16x32_bf16 a[20:23], a[232:235], a[188:191], a[20:23]\n\t"
"v_cvt_pk_bf16_f32 v205, v206, v207\n\t"
"v_cvt_pk_bf16_f32 v209, v228, v229\n\t"
"v_mfma_f32_16x16x32_bf16 a[20:23], a[236:239], a[192:195], a[20:23]\n\t"
"v_accvgpr_read_b32 v224, a0\n\t"
"v_accvgpr_read_b32 v226, a4\n\t"
"v_mfma_f32_16x16x32_bf16 a[20:23], a[208:211], a[196:199], a[20:23]\n\t"
"v_accvgpr_read_b32 v225, a1\n\t"
"v_accvgpr_read_b32 v227, a5\n\t"
"v_pk_fma_f32 v[212:213], v[216:217], s[56:57], v[212:213]\n\t"
"v_pk_fma_f32 v[216:217], v[212:213], s[58:59], v[216:217] neg_lo:[0,0,1] neg_hi:[0,0,1]\n\t"
"v_pk_fma_f32 v[234:235], v[8:9], s[60:61], v[212:213]\n\t"
"v_pk_fma_f32 v[8:9], v[212:213], s[60:61], v[8:9]\n\t"
"v_pk_fma_f32 v[212:213], v[108:109], s[62:63], v[216:217]\n\t"
"v_pk_fma_f32 v[108:109], v[216:217], s[62:63], v[108:109]\n\t"
"v_mfma_f32_16x16x32_bf16 a[28:31], a[232:235], a[192:195], a[28:31]\n\t"
"v_cvt_pk_bf16_f32 v212, v212, v213\n\t"
"v_cvt_pk_bf16_f32 v216, v234, v235\n\t"
"v_mfma_f32_16x16x32_bf16 a[28:31], a[236:239], a[196:199], a[28:31]\n\t"
"v_accvgpr_read_b32 v230, a2\n\t"
"v_accvgpr_read_b32 v232, a6\n\t"
"v_mfma_f32_16x16x32_bf16 a[28:31], a[208:211], a[200:203], a[28:31]\n\t"
"v_accvgpr_read_b32 v231, a3\n\t"
"v_accvgpr_read_b32 v233, a7\n\t"
"v_pk_fma_f32 v[214:215], v[218:219], s[56:57], v[214:215]\n\t"
"v_pk_fma_f32 v[218:219], v[214:215], s[58:59], v[218:219] neg_lo:[0,0,1] neg_hi:[0,0,1]\n\t"
"v_pk_fma_f32 v[98:99], v[10:11], s[60:61], v[214:215]\n\t"
"v_pk_fma_f32 v[10:11], v[214:215], s[60:61], v[10:11]\n\t"
"v_pk_fma_f32 v[214:215], v[110:111], s[62:63], v[218:219]\n\t"
"v_pk_fma_f32 v[110:111], v[218:219], s[62:63], v[110:111]\n\t"
"v_mfma_f32_16x16x32_bf16 a[36:39], a[232:235], a[196:199], a[36:39]\n\t"
"s_waitcnt vmcnt(7)\n\t"
"v_cvt_pk_bf16_f32 v213, v214, v215\n\t"
"v_cvt_pk_bf16_f32 v217, v98, v99\n\t"
"v_mfma_f32_16x16x32_bf16 a[36:39], a[236:239], a[200:203], a[36:39]\n\t"
"v_accvgpr_read_b32 v236, a8\n\t"
"v_accvgpr_read_b32 v238, a12\n\t"
"v_mfma_f32_16x16x32_bf16 a[36:39], a[208:211], a[204:207], a[36:39]\n\t"
"buffer_load_dwordx4 a[232:235], v252, s[8:11], s20 offen offset:1024 sc1\n\t"
"v_accvgpr_read_b32 v237, a9\n\t"
"v_accvgpr_read_b32 v239, a13\n\t"
"v_pk_fma_f32 v[224:225], v[226:227], s[56:57], v[224:225]\n\t"
"v_pk_fma_f32 v[226:227], v[224:225], s[58:59], v[226:227] neg_lo:[0,0,1] neg_hi:[0,0,1]\n\t"
"v_pk_fma_f32 v[228:229], v[12:13], s[60:61], v[224:225]\n\t"
"v_pk_fma_f32 v[12:13], v[224:225], s[60:61], v[12:13]\n\t"
"v_pk_fma_f32 v[224:225], v[112:113], s[62:63], v[226:227]\n\t"
"v_pk_fma_f32 v[112:113], v[226:227], s[62:63], v[112:113]\n\t"
"v_mfma_f32_16x16x32_bf16 a[44:47], a[240:243], a[168:171], a[44:47]\n\t"
"v_cvt_pk_bf16_f32 v224, v224, v225\n\t"
"v_cvt_pk_bf16_f32 v228, v228, v229\n\t"
"v_mfma_f32_16x16x32_bf16 a[44:47], a[244:247], a[172:175], a[44:47]\n\t"
"v_accvgpr_write_b32 a0, v224\n\t"
"v_accvgpr_write_b32 a4, v228\n\t"
"v_mfma_f32_16x16x32_bf16 a[44:47], a[248:251], a[176:179], a[44:47]\n\t"
"buffer_load_dwordx4 a[208:211], v252, s[8:11], s20 offen offset:2048 sc1\n\t"
"v_accvgpr_read_b32 v224, a10\n\t"
"v_accvgpr_read_b32 v226, a14\n\t"
"s_mov_b32 s20, 1204224\n\t"
"v_pk_fma_f32 v[230:231], v[232:233], s[56:57], v[230:231]\n\t"
"v_pk_fma_f32 v[232:233], v[230:231], s[58:59], v[232:233] neg_lo:[0,0,1] neg_hi:[0,0,1]\n\t"
"v_pk_fma_f32 v[234:235], v[14:15], s[60:61], v[230:231]\n\t"
"v_pk_fma_f32 v[14:15], v[230:231], s[60:61], v[14:15]\n\t"
"v_pk_fma_f32 v[230:231], v[114:115], s[62:63], v[232:233]\n\t"
"v_pk_fma_f32 v[114:115], v[232:233], s[62:63], v[114:115]\n\t"
"v_mfma_f32_16x16x32_bf16 a[52:55], a[240:243], a[172:175], a[52:55]\n\t"
"v_cvt_pk_bf16_f32 v230, v230, v231\n\t"
"v_cvt_pk_bf16_f32 v234, v234, v235\n\t"
"v_mfma_f32_16x16x32_bf16 a[52:55], a[244:247], a[176:179], a[52:55]\n\t"
"v_accvgpr_write_b32 a1, v230\n\t"
"v_accvgpr_write_b32 a5, v234\n\t"
"v_mfma_f32_16x16x32_bf16 a[52:55], a[248:251], a[180:183], a[52:55]\n\t"
"buffer_load_dwordx4 a[236:239], v252, s[8:11], s20 offen offset:0 sc1\n\t"
"v_accvgpr_read_b32 v225, a11\n\t"
"v_accvgpr_read_b32 v227, a15\n\t"
"v_pk_fma_f32 v[236:237], v[238:239], s[56:57], v[236:237]\n\t"
"v_pk_fma_f32 v[238:239], v[236:237], s[58:59], v[238:239] neg_lo:[0,0,1] neg_hi:[0,0,1]\n\t"
"v_pk_fma_f32 v[98:99], v[16:17], s[60:61], v[236:237]\n\t"
"v_pk_fma_f32 v[16:17], v[236:237], s[60:61], v[16:17]\n\t"
"v_pk_fma_f32 v[236:237], v[116:117], s[62:63], v[238:239]\n\t"
"v_pk_fma_f32 v[116:117], v[238:239], s[62:63], v[116:117]\n\t"
"v_mfma_f32_16x16x32_bf16 a[60:63], a[240:243], a[176:179], a[60:63]\n\t"
"v_accvgpr_read_b32 v230, a16\n\t"
"v_accvgpr_read_b32 v232, a20\n\t"
"v_mfma_f32_16x16x32_bf16 a[60:63], a[244:247], a[180:183], a[60:63]\n\t"
"v_accvgpr_read_b32 v231, a17\n\t"
"v_accvgpr_read_b32 v233, a21\n\t"
"v_mfma_f32_16x16x32_bf16 a[60:63], a[248:251], a[184:187], a[60:63]\n\t"
"v_cvt_pk_bf16_f32 v236, v236, v237\n\t"
"v_cvt_pk_bf16_f32 v98, v98, v99\n\t"
"v_accvgpr_write_b32 a8, v236\n\t"
"v_accvgpr_write_b32 a12, v98\n\t"
"v_accvgpr_read_b32 v236, a18\n\t"
"v_pk_fma_f32 v[224:225], v[226:227], s[56:57], v[224:225]\n\t"
"v_pk_fma_f32 v[226:227], v[224:225], s[58:59], v[226:227] neg_lo:[0,0,1] neg_hi:[0,0,1]\n\t"
"v_pk_fma_f32 v[228:229], v[18:19], s[60:61], v[224:225]\n\t"
"v_pk_fma_f32 v[18:19], v[224:225], s[60:61], v[18:19]\n\t"
"v_pk_fma_f32 v[224:225], v[118:119], s[62:63], v[226:227]\n\t"
"v_pk_fma_f32 v[118:119], v[226:227], s[62:63], v[118:119]\n\t"
"v_pk_fma_f32 v[230:231], v[232:233], s[56:57], v[230:231]\n\t"
"v_pk_fma_f32 v[232:233], v[230:231], s[58:59], v[232:233] neg_lo:[0,0,1] neg_hi:[0,0,1]\n\t"
"v_pk_fma_f32 v[234:235], v[20:21], s[60:61], v[230:231]\n\t"
"v_pk_fma_f32 v[20:21], v[230:231], s[60:61], v[20:21]\n\t"
"v_pk_fma_f32 v[230:231], v[120:121], s[62:63], v[232:233]\n\t"
"v_pk_fma_f32 v[120:121], v[232:233], s[62:63], v[120:121]\n\t"
"v_mfma_f32_16x16x32_bf16 a[68:71], a[240:243], a[180:183], a[68:71]\n\t"
"v_accvgpr_read_b32 v238, a22\n\t"
"v_accvgpr_read_b32 v237, a19\n\t"
"v_mfma_f32_16x16x32_bf16 a[68:71], a[244:247], a[184:187], a[68:71]\n\t"
"v_accvgpr_read_b32 v239, a23\n\t"
"v_cvt_pk_bf16_f32 v224, v224, v225\n\t"
"v_mfma_f32_16x16x32_bf16 a[68:71], a[248:251], a[188:191], a[68:71]\n\t"
"v_cvt_pk_bf16_f32 v228, v228, v229\n\t"
"v_cvt_pk_bf16_f32 v230, v230, v231\n\t"
"v_cvt_pk_bf16_f32 v234, v234, v235\n\t"
"v_accvgpr_write_b32 a9, v224\n\t"
"v_accvgpr_write_b32 a13, v228\n\t"
"v_pk_fma_f32 v[236:237], v[238:239], s[56:57], v[236:237]\n\t"
"v_pk_fma_f32 v[238:239], v[236:237], s[58:59], v[238:239] neg_lo:[0,0,1] neg_hi:[0,0,1]\n\t"
"v_pk_fma_f32 v[98:99], v[22:23], s[60:61], v[236:237]\n\t"
"v_pk_fma_f32 v[22:23], v[236:237], s[60:61], v[22:23]\n\t"
"v_pk_fma_f32 v[236:237], v[122:123], s[62:63], v[238:239]\n\t"
"v_pk_fma_f32 v[122:123], v[238:239], s[62:63], v[122:123]\n\t"
"v_mfma_f32_16x16x32_bf16 a[76:79], a[240:243], a[184:187], a[76:79]\n\t"
"v_accvgpr_write_b32 a16, v230\n\t"
"v_accvgpr_write_b32 a20, v234\n\t"
"v_mfma_f32_16x16x32_bf16 a[76:79], a[244:247], a[188:191], a[76:79]\n\t"
"v_accvgpr_read_b32 v224, a24\n\t"
"v_accvgpr_read_b32 v226, a28\n\t"
"v_mfma_f32_16x16x32_bf16 a[76:79], a[248:251], a[192:195], a[76:79]\n\t"
"v_accvgpr_read_b32 v225, a25\n\t"
"v_accvgpr_read_b32 v227, a29\n\t"
"v_cvt_pk_bf16_f32 v236, v236, v237\n\t"
"v_cvt_pk_bf16_f32 v98, v98, v99\n\t"
"v_accvgpr_write_b32 a17, v236\n\t"
"v_pk_fma_f32 v[224:225], v[226:227], s[56:57], v[224:225]\n\t"
"v_pk_fma_f32 v[226:227], v[224:225], s[58:59], v[226:227] neg_lo:[0,0,1] neg_hi:[0,0,1]\n\t"
"v_pk_fma_f32 v[228:229], v[24:25], s[60:61], v[224:225]\n\t"
"v_pk_fma_f32 v[24:25], v[224:225], s[60:61], v[24:25]\n\t"
"v_pk_fma_f32 v[224:225], v[124:125], s[62:63], v[226:227]\n\t"
"v_pk_fma_f32 v[124:125], v[226:227], s[62:63], v[124:125]\n\t"
"v_mfma_f32_16x16x32_bf16 a[84:87], a[240:243], a[188:191], a[84:87]\n\t"
"v_accvgpr_write_b32 a21, v98\n\t"
"v_accvgpr_read_b32 v230, a26\n\t"
"v_mfma_f32_16x16x32_bf16 a[84:87], a[244:247], a[192:195], a[84:87]\n\t"
"v_accvgpr_read_b32 v232, a30\n\t"
"v_accvgpr_read_b32 v231, a27\n\t"
"v_mfma_f32_16x16x32_bf16 a[84:87], a[248:251], a[196:199], a[84:87]\n\t"
"v_accvgpr_read_b32 v233, a31\n\t"
"v_cvt_pk_bf16_f32 v224, v224, v225\n\t"
"v_cvt_pk_bf16_f32 v228, v228, v229\n\t"
"v_accvgpr_write_b32 a24, v224\n\t"
"v_accvgpr_write_b32 a28, v228\n\t"
"v_pk_fma_f32 v[230:231], v[232:233], s[56:57], v[230:231]\n\t"
"v_pk_fma_f32 v[232:233], v[230:231], s[58:59], v[232:233] neg_lo:[0,0,1] neg_hi:[0,0,1]\n\t"
"v_pk_fma_f32 v[234:235], v[26:27], s[60:61], v[230:231]\n\t"
"v_pk_fma_f32 v[26:27], v[230:231], s[60:61], v[26:27]\n\t"
"v_pk_fma_f32 v[230:231], v[126:127], s[62:63], v[232:233]\n\t"
"v_pk_fma_f32 v[126:127], v[232:233], s[62:63], v[126:127]\n\t"
"v_mfma_f32_16x16x32_bf16 a[92:95], a[240:243], a[192:195], a[92:95]\n\t"
"v_accvgpr_read_b32 v236, a32\n\t"
"v_accvgpr_read_b32 v238, a36\n\t"
"v_mfma_f32_16x16x32_bf16 a[92:95], a[244:247], a[196:199], a[92:95]\n\t"
"v_accvgpr_read_b32 v237, a33\n\t"
"v_accvgpr_read_b32 v239, a37\n\t"
"v_mfma_f32_16x16x32_bf16 a[92:95], a[248:251], a[200:203], a[92:95]\n\t"
"v_cvt_pk_bf16_f32 v230, v230, v231\n\t"
"v_cvt_pk_bf16_f32 v234, v234, v235\n\t"
"v_accvgpr_write_b32 a25, v230\n\t"
"v_accvgpr_write_b32 a29, v234\n\t"
"v_accvgpr_read_b32 v224, a34\n\t"
"v_pk_fma_f32 v[236:237], v[238:239], s[56:57], v[236:237]\n\t"
"v_pk_fma_f32 v[238:239], v[236:237], s[58:59], v[238:239] neg_lo:[0,0,1] neg_hi:[0,0,1]\n\t"
"v_pk_fma_f32 v[98:99], v[28:29], s[60:61], v[236:237]\n\t"
"v_pk_fma_f32 v[28:29], v[236:237], s[60:61], v[28:29]\n\t"
"v_pk_fma_f32 v[236:237], v[128:129], s[62:63], v[238:239]\n\t"
"v_pk_fma_f32 v[128:129], v[238:239], s[62:63], v[128:129]\n\t"
"v_mfma_f32_16x16x32_bf16 a[100:103], a[240:243], a[196:199], a[100:103]\n\t"
"s_waitcnt vmcnt(7)\n\t"
"v_accvgpr_read_b32 v226, a38\n\t"
"v_accvgpr_read_b32 v225, a35\n\t"
"v_mfma_f32_16x16x32_bf16 a[100:103], a[244:247], a[200:203], a[100:103]\n\t"
"v_accvgpr_read_b32 v227, a39\n\t"
"v_cvt_pk_bf16_f32 v236, v236, v237\n\t"
"v_mfma_f32_16x16x32_bf16 a[100:103], a[248:251], a[204:207], a[100:103]\n\t"
"buffer_load_dwordx4 a[240:243], v252, s[8:11], s20 offen offset:1024 sc1\n\t"
"v_cvt_pk_bf16_f32 v98, v98, v99\n\t"
"v_accvgpr_write_b32 a32, v236\n\t"
"v_pk_fma_f32 v[224:225], v[226:227], s[56:57], v[224:225]\n\t"
"v_pk_fma_f32 v[226:227], v[224:225], s[58:59], v[226:227] neg_lo:[0,0,1] neg_hi:[0,0,1]\n\t"
"v_pk_fma_f32 v[228:229], v[30:31], s[60:61], v[224:225]\n\t"
"v_pk_fma_f32 v[30:31], v[224:225], s[60:61], v[30:31]\n\t"
"v_pk_fma_f32 v[224:225], v[130:131], s[62:63], v[226:227]\n\t"
"v_pk_fma_f32 v[130:131], v[226:227], s[62:63], v[130:131]\n\t"
"v_mfma_f32_16x16x32_bf16 a[108:111], v[240:243], a[168:171], a[108:111]\n\t"
"ds_read_b128 a[168:171], v255 offset:512\n\t"
"v_accvgpr_write_b32 a36, v98\n\t"
"v_mfma_f32_16x16x32_bf16 a[108:111], a[252:255], a[172:175], a[108:111]\n\t"
"v_accvgpr_read_b32 v230, a40\n\t"
"v_accvgpr_read_b32 v232, a44\n\t"
"v_mfma_f32_16x16x32_bf16 a[108:111], a[212:215], a[176:179], a[108:111]\n\t"
"buffer_load_dwordx4 a[244:247], v252, s[8:11], s20 offen offset:2048 sc1\n\t"
"v_accvgpr_read_b32 v231, a41\n\t"
"v_accvgpr_read_b32 v233, a45\n\t"
"v_cvt_pk_bf16_f32 v224, v224, v225\n\t"
"v_cvt_pk_bf16_f32 v228, v228, v229\n\t"
"s_mov_b32 s20, 1363968\n\t"
"Lpg23_%=:\n\t"
"s_cbranch_vccnz Lvr3_%=\n\t"
"v_pk_fma_f32 v[230:231], v[232:233], s[56:57], v[230:231]\n\t"
"v_pk_fma_f32 v[232:233], v[230:231], s[58:59], v[232:233] neg_lo:[0,0,1] neg_hi:[0,0,1]\n\t"
"v_pk_fma_f32 v[234:235], v[32:33], s[60:61], v[230:231]\n\t"
"v_pk_fma_f32 v[32:33], v[230:231], s[60:61], v[32:33]\n\t"
"v_pk_fma_f32 v[230:231], v[132:133], s[62:63], v[232:233]\n\t"
"v_pk_fma_f32 v[132:133], v[232:233], s[62:63], v[132:133]\n\t"
"v_mfma_f32_16x16x32_bf16 a[116:119], v[240:243], a[172:175], a[116:119]\n\t"
"ds_read_b128 a[172:175], v255 offset:3584\n\t"
"v_accvgpr_write_b32 a33, v224\n\t"
"v_mfma_f32_16x16x32_bf16 a[116:119], a[252:255], a[176:179], a[116:119]\n\t"
"v_accvgpr_write_b32 a37, v228\n\t"
"v_accvgpr_read_b32 v236, a42\n\t"
"v_mfma_f32_16x16x32_bf16 a[116:119], a[212:215], a[180:183], a[116:119]\n\t"
"buffer_load_dwordx4 a[248:251], v252, s[8:11], s20 offen offset:0 sc1\n\t"
"v_accvgpr_read_b32 v238, a46\n\t"
"v_accvgpr_read_b32 v237, a43\n\t"
"v_accvgpr_read_b32 v239, a47\n\t"
"v_pk_fma_f32 v[236:237], v[238:239], s[56:57], v[236:237]\n\t"
"v_pk_fma_f32 v[238:239], v[236:237], s[58:59], v[238:239] neg_lo:[0,0,1] neg_hi:[0,0,1]\n\t"
"v_pk_fma_f32 v[98:99], v[34:35], s[60:61], v[236:237]\n\t"
"v_pk_fma_f32 v[34:35], v[236:237], s[60:61], v[34:35]\n\t"
"v_pk_fma_f32 v[236:237], v[134:135], s[62:63], v[238:239]\n\t"
"v_pk_fma_f32 v[134:135], v[238:239], s[62:63], v[134:135]\n\t"
"v_mfma_f32_16x16x32_bf16 a[124:127], v[240:243], a[176:179], a[124:127]\n\t"
"ds_read_b128 a[176:179], v255 offset:6656\n\t"
"v_cvt_pk_bf16_f32 v198, v230, v231\n\t"
"v_mfma_f32_16x16x32_bf16 a[124:127], a[252:255], a[180:183], a[124:127]\n\t"
"v_cvt_pk_bf16_f32 v202, v234, v235\n\t"
"v_accvgpr_read_b32 v224, a48\n\t"
"v_mfma_f32_16x16x32_bf16 a[124:127], a[212:215], a[184:187], a[124:127]\n\t"
"global_load_dwordx4 v[248:251], v252, s[48:49] offset:0\n\t"
"v_accvgpr_read_b32 v226, a52\n\t"
"v_accvgpr_read_b32 v225, a49\n\t"
"v_accvgpr_read_b32 v227, a53\n\t"
"v_accvgpr_read_b32 v230, a50\n\t"
"v_accvgpr_read_b32 v232, a54\n\t"
"v_accvgpr_read_b32 v231, a51\n\t"
"v_accvgpr_read_b32 v233, a55\n\t"
"v_pk_fma_f32 v[224:225], v[226:227], s[56:57], v[224:225]\n\t"
"v_pk_fma_f32 v[226:227], v[224:225], s[58:59], v[226:227] neg_lo:[0,0,1] neg_hi:[0,0,1]\n\t"
"v_pk_fma_f32 v[228:229], v[36:37], s[60:61], v[224:225]\n\t"
"v_pk_fma_f32 v[36:37], v[224:225], s[60:61], v[36:37]\n\t"
"v_pk_fma_f32 v[224:225], v[136:137], s[62:63], v[226:227]\n\t"
"v_pk_fma_f32 v[136:137], v[226:227], s[62:63], v[136:137]\n\t"
"v_pk_fma_f32 v[230:231], v[232:233], s[56:57], v[230:231]\n\t"
"v_pk_fma_f32 v[232:233], v[230:231], s[58:59], v[232:233] neg_lo:[0,0,1] neg_hi:[0,0,1]\n\t"
"v_pk_fma_f32 v[234:235], v[38:39], s[60:61], v[230:231]\n\t"
"v_pk_fma_f32 v[38:39], v[230:231], s[60:61], v[38:39]\n\t"
"v_pk_fma_f32 v[230:231], v[138:139], s[62:63], v[232:233]\n\t"
"v_pk_fma_f32 v[138:139], v[232:233], s[62:63], v[138:139]\n\t"
"v_mfma_f32_16x16x32_bf16 a[132:135], v[240:243], a[180:183], a[132:135]\n\t"
"ds_read_b128 a[180:183], v255 offset:9728\n\t"
"v_cvt_pk_bf16_f32 v199, v236, v237\n\t"
"v_mfma_f32_16x16x32_bf16 a[132:135], a[252:255], a[184:187], a[132:135]\n\t"
"v_cvt_pk_bf16_f32 v203, v98, v99\n\t"
"v_accvgpr_read_b32 v236, a56\n\t"
"v_mfma_f32_16x16x32_bf16 a[132:135], a[212:215], a[188:191], a[132:135]\n\t"
"buffer_load_dwordx4 a[52:55], v252, s[8:11], s20 offen offset:1024 sc1\n\t"
"v_accvgpr_read_b32 v238, a60\n\t"
"v_accvgpr_read_b32 v237, a57\n\t"
"v_accvgpr_read_b32 v239, a61\n\t"
"v_pk_fma_f32 v[236:237], v[238:239], s[56:57], v[236:237]\n\t"
"v_pk_fma_f32 v[238:239], v[236:237], s[58:59], v[238:239] neg_lo:[0,0,1] neg_hi:[0,0,1]\n\t"
"v_pk_fma_f32 v[98:99], v[40:41], s[60:61], v[236:237]\n\t"
"v_pk_fma_f32 v[40:41], v[236:237], s[60:61], v[40:41]\n\t"
"v_pk_fma_f32 v[236:237], v[140:141], s[62:63], v[238:239]\n\t"
"v_pk_fma_f32 v[140:141], v[238:239], s[62:63], v[140:141]\n\t"
"v_mfma_f32_16x16x32_bf16 a[140:143], v[240:243], a[184:187], a[140:143]\n\t"
"ds_read_b128 a[184:187], v255 offset:12800\n\t"
"v_cvt_pk_bf16_f32 v206, v224, v225\n\t"
"v_mfma_f32_16x16x32_bf16 a[140:143], a[252:255], a[188:191], a[140:143]\n\t"
"v_cvt_pk_bf16_f32 v210, v228, v229\n\t"
"v_cvt_pk_bf16_f32 v207, v230, v231\n\t"
"v_mfma_f32_16x16x32_bf16 a[140:143], a[212:215], a[192:195], a[140:143]\n\t"
"v_accvgpr_read_b32 v224, a58\n\t"
"v_accvgpr_read_b32 v226, a62\n\t"
"v_accvgpr_read_b32 v225, a59\n\t"
"v_accvgpr_read_b32 v227, a63\n\t"
"v_cvt_pk_bf16_f32 v211, v234, v235\n\t"
"v_pk_fma_f32 v[224:225], v[226:227], s[56:57], v[224:225]\n\t"
"v_pk_fma_f32 v[226:227], v[224:225], s[58:59], v[226:227] neg_lo:[0,0,1] neg_hi:[0,0,1]\n\t"
"v_pk_fma_f32 v[228:229], v[42:43], s[60:61], v[224:225]\n\t"
"v_pk_fma_f32 v[42:43], v[224:225], s[60:61], v[42:43]\n\t"
"v_pk_fma_f32 v[224:225], v[142:143], s[62:63], v[226:227]\n\t"
"v_pk_fma_f32 v[142:143], v[226:227], s[62:63], v[142:143]\n\t"
"v_mfma_f32_16x16x32_bf16 a[148:151], v[240:243], a[188:191], a[148:151]\n\t"
"ds_read_b128 a[188:191], v255 offset:15872\n\t"
"v_cvt_pk_bf16_f32 v214, v236, v237\n\t"
"v_mfma_f32_16x16x32_bf16 a[148:151], a[252:255], a[192:195], a[148:151]\n\t"
"v_cvt_pk_bf16_f32 v218, v98, v99\n\t"
"v_accvgpr_read_b32 v230, a64\n\t"
"v_mfma_f32_16x16x32_bf16 a[148:151], a[212:215], a[196:199], a[148:151]\n\t"
"buffer_load_dwordx4 a[56:59], v252, s[8:11], s20 offen offset:2048 sc1\n\t"
"v_accvgpr_read_b32 v232, a68\n\t"
"v_accvgpr_read_b32 v231, a65\n\t"
"v_accvgpr_read_b32 v233, a69\n\t"
"v_pk_fma_f32 v[230:231], v[232:233], s[56:57], v[230:231]\n\t"
"v_pk_fma_f32 v[232:233], v[230:231], s[58:59], v[232:233] neg_lo:[0,0,1] neg_hi:[0,0,1]\n\t"
"v_pk_fma_f32 v[234:235], v[44:45], s[60:61], v[230:231]\n\t"
"v_pk_fma_f32 v[44:45], v[230:231], s[60:61], v[44:45]\n\t"
"v_pk_fma_f32 v[230:231], v[144:145], s[62:63], v[232:233]\n\t"
"v_pk_fma_f32 v[144:145], v[232:233], s[62:63], v[144:145]\n\t"
"v_mfma_f32_16x16x32_bf16 a[156:159], v[240:243], a[192:195], a[156:159]\n\t"
"s_mov_b32 s20, 1376256\n\t"
"ds_read_b128 a[192:195], v255 offset:18944\n\t"
"v_cvt_pk_bf16_f32 v215, v224, v225\n\t"
"v_mfma_f32_16x16x32_bf16 a[156:159], a[252:255], a[196:199], a[156:159]\n\t"
"v_cvt_pk_bf16_f32 v219, v228, v229\n\t"
"v_accvgpr_read_b32 v236, a66\n\t"
"v_mfma_f32_16x16x32_bf16 a[156:159], a[212:215], a[200:203], a[156:159]\n\t"
"buffer_load_dwordx4 a[60:63], v252, s[8:11], s20 offen offset:0 sc1\n\t"
"v_accvgpr_read_b32 v238, a70\n\t"
"v_accvgpr_read_b32 v237, a67\n\t"
"v_accvgpr_read_b32 v239, a71\n\t"
"v_cvt_pk_bf16_f32 v230, v230, v231\n\t"
"v_cvt_pk_bf16_f32 v234, v234, v235\n\t"
"v_pk_fma_f32 v[236:237], v[238:239], s[56:57], v[236:237]\n\t"
"v_pk_fma_f32 v[238:239], v[236:237], s[58:59], v[238:239] neg_lo:[0,0,1] neg_hi:[0,0,1]\n\t"
"v_pk_fma_f32 v[98:99], v[46:47], s[60:61], v[236:237]\n\t"
"v_pk_fma_f32 v[46:47], v[236:237], s[60:61], v[46:47]\n\t"
"v_pk_fma_f32 v[236:237], v[146:147], s[62:63], v[238:239]\n\t"
"v_pk_fma_f32 v[146:147], v[238:239], s[62:63], v[146:147]\n\t"
"v_mfma_f32_16x16x32_bf16 a[164:167], v[240:243], a[196:199], a[164:167]\n\t"
"s_waitcnt vmcnt(11) lgkmcnt(4)\n\t"
"ds_read_b128 a[196:199], v255 offset:22016\n\t"
"v_accvgpr_write_b32 a2, v230\n\t"
"v_mfma_f32_16x16x32_bf16 a[164:167], a[252:255], a[200:203], a[164:167]\n\t"
"ds_read_b128 a[200:203], v255 offset:25088\n\t"
"v_accvgpr_write_b32 a6, v234\n\t"
"v_mfma_f32_16x16x32_bf16 a[164:167], a[212:215], a[204:207], a[164:167]\n\t"
"buffer_load_dwordx4 a[252:255], v252, s[8:11], s20 offen offset:1024 sc1\n\t"
"ds_read_b128 a[204:207], v255 offset:28160\n\t"
"v_accvgpr_read_b32 v224, a72\n\t"
"v_accvgpr_read_b32 v226, a76\n\t"
"v_accvgpr_read_b32 v225, a73\n\t"
"v_accvgpr_read_b32 v227, a77\n\t"
"v_cvt_pk_bf16_f32 v236, v236, v237\n\t"
"v_cvt_pk_bf16_f32 v98, v98, v99\n\t"
"v_pk_fma_f32 v[224:225], v[226:227], s[56:57], v[224:225]\n\t"
"v_pk_fma_f32 v[226:227], v[224:225], s[58:59], v[226:227] neg_lo:[0,0,1] neg_hi:[0,0,1]\n\t"
"v_pk_fma_f32 v[228:229], v[48:49], s[60:61], v[224:225]\n\t"
"v_pk_fma_f32 v[48:49], v[224:225], s[60:61], v[48:49]\n\t"
"v_pk_fma_f32 v[224:225], v[148:149], s[62:63], v[226:227]\n\t"
"v_pk_fma_f32 v[148:149], v[226:227], s[62:63], v[148:149]\n\t"
"Lst32_%=:\n\t"
"v_mfma_f32_16x16x32_bf16 v[0:3], a[220:223], a[168:171], v[0:3]\n\t"
"s_waitcnt lgkmcnt(6)\n\t"
"v_accvgpr_write_b32 a3, v236\n\t"
"v_accvgpr_write_b32 a7, v98\n\t"
"v_mfma_f32_16x16x32_bf16 v[0:3], a[224:227], a[172:175], v[0:3]\n\t"
"v_accvgpr_read_b32 v230, a74\n\t"
"v_accvgpr_read_b32 v232, a78\n\t"
"v_mfma_f32_16x16x32_bf16 v[0:3], a[216:219], a[176:179], v[0:3]\n\t"
"buffer_load_dwordx4 a[212:215], v252, s[8:11], s20 offen offset:2048 sc1\n\t"
"v_accvgpr_read_b32 v231, a75\n\t"
"v_accvgpr_read_b32 v233, a79\n\t"
"v_cvt_pk_bf16_f32 v224, v224, v225\n\t"
"v_cvt_pk_bf16_f32 v228, v228, v229\n\t"
"v_pk_fma_f32 v[230:231], v[232:233], s[56:57], v[230:231]\n\t"
"v_pk_fma_f32 v[232:233], v[230:231], s[58:59], v[232:233] neg_lo:[0,0,1] neg_hi:[0,0,1]\n\t"
"v_pk_fma_f32 v[234:235], v[50:51], s[60:61], v[230:231]\n\t"
"v_pk_fma_f32 v[50:51], v[230:231], s[60:61], v[50:51]\n\t"
"v_pk_fma_f32 v[230:231], v[150:151], s[62:63], v[232:233]\n\t"
"v_pk_fma_f32 v[150:151], v[232:233], s[62:63], v[150:151]\n\t"
"v_mfma_f32_16x16x32_bf16 v[4:7], a[220:223], a[172:175], v[4:7]\n\t"
"s_waitcnt lgkmcnt(5)\n\t"
"s_mov_b32 s20, 1388544\n\t"
"v_accvgpr_write_b32 a10, v224\n\t"
"v_mfma_f32_16x16x32_bf16 v[4:7], a[224:227], a[176:179], v[4:7]\n\t"
"v_accvgpr_write_b32 a14, v228\n\t"
"v_accvgpr_read_b32 v236, a80\n\t"
"v_mfma_f32_16x16x32_bf16 v[4:7], a[216:219], a[180:183], v[4:7]\n\t"
"buffer_load_dwordx4 a[64:67], v252, s[8:11], s20 offen offset:0 sc1\n\t"
"v_accvgpr_read_b32 v238, a84\n\t"
"v_accvgpr_read_b32 v237, a81\n\t"
"v_accvgpr_read_b32 v239, a85\n\t"
"v_cvt_pk_bf16_f32 v230, v230, v231\n\t"
"v_cvt_pk_bf16_f32 v234, v234, v235\n\t"
"v_pk_fma_f32 v[236:237], v[238:239], s[56:57], v[236:237]\n\t"
"v_pk_fma_f32 v[238:239], v[236:237], s[58:59], v[238:239] neg_lo:[0,0,1] neg_hi:[0,0,1]\n\t"
"v_pk_fma_f32 v[98:99], v[52:53], s[60:61], v[236:237]\n\t"
"v_pk_fma_f32 v[52:53], v[236:237], s[60:61], v[52:53]\n\t"
"v_pk_fma_f32 v[236:237], v[152:153], s[62:63], v[238:239]\n\t"
"v_pk_fma_f32 v[152:153], v[238:239], s[62:63], v[152:153]\n\t"
"v_mfma_f32_16x16x32_bf16 v[8:11], a[220:223], a[176:179], v[8:11]\n\t"
"s_waitcnt lgkmcnt(4)\n\t"
"v_accvgpr_write_b32 a11, v230\n\t"
"v_accvgpr_write_b32 a15, v234\n\t"
"v_mfma_f32_16x16x32_bf16 v[8:11], a[224:227], a[180:183], v[8:11]\n\t"
"v_accvgpr_read_b32 v224, a82\n\t"
"v_accvgpr_read_b32 v226, a86\n\t"
"v_mfma_f32_16x16x32_bf16 v[8:11], a[216:219], a[184:187], v[8:11]\n\t"
"buffer_load_dwordx4 a[68:71], v252, s[8:11], s20 offen offset:1024 sc1\n\t"
"v_accvgpr_read_b32 v225, a83\n\t"
"v_accvgpr_read_b32 v227, a87\n\t"
"v_cvt_pk_bf16_f32 v236, v236, v237\n\t"
"v_cvt_pk_bf16_f32 v98, v98, v99\n\t"
"v_pk_fma_f32 v[224:225], v[226:227], s[56:57], v[224:225]\n\t"
"v_pk_fma_f32 v[226:227], v[224:225], s[58:59], v[226:227] neg_lo:[0,0,1] neg_hi:[0,0,1]\n\t"
"v_pk_fma_f32 v[228:229], v[54:55], s[60:61], v[224:225]\n\t"
"v_pk_fma_f32 v[54:55], v[224:225], s[60:61], v[54:55]\n\t"
"v_pk_fma_f32 v[224:225], v[154:155], s[62:63], v[226:227]\n\t"
"v_pk_fma_f32 v[154:155], v[226:227], s[62:63], v[154:155]\n\t"
"v_mfma_f32_16x16x32_bf16 v[12:15], a[220:223], a[180:183], v[12:15]\n\t"
"s_waitcnt lgkmcnt(3)\n\t"
"v_accvgpr_write_b32 a18, v236\n\t"
"v_accvgpr_write_b32 a22, v98\n\t"
"v_mfma_f32_16x16x32_bf16 v[12:15], a[224:227], a[184:187], v[12:15]\n\t"
"v_accvgpr_read_b32 v230, a88\n\t"
"v_accvgpr_read_b32 v232, a92\n\t"
"v_mfma_f32_16x16x32_bf16 v[12:15], a[216:219], a[188:191], v[12:15]\n\t"
"buffer_load_dwordx4 a[72:75], v252, s[8:11], s20 offen offset:2048 sc1\n\t"
"v_accvgpr_read_b32 v231, a89\n\t"
"v_accvgpr_read_b32 v233, a93\n\t"
"v_accvgpr_read_b32 v236, a90\n\t"
"v_accvgpr_read_b32 v238, a94\n\t"
"v_accvgpr_read_b32 v237, a91\n\t"
"v_accvgpr_read_b32 v239, a95\n\t"
"v_cvt_pk_bf16_f32 v224, v224, v225\n\t"
"v_cvt_pk_bf16_f32 v228, v228, v229\n\t"
"v_pk_fma_f32 v[230:231], v[232:233], s[56:57], v[230:231]\n\t"
"v_pk_fma_f32 v[232:233], v[230:231], s[58:59], v[232:233] neg_lo:[0,0,1] neg_hi:[0,0,1]\n\t"
"v_pk_fma_f32 v[234:235], v[56:57], s[60:61], v[230:231]\n\t"
"v_pk_fma_f32 v[56:57], v[230:231], s[60:61], v[56:57]\n\t"
"v_pk_fma_f32 v[230:231], v[156:157], s[62:63], v[232:233]\n\t"
"v_pk_fma_f32 v[156:157], v[232:233], s[62:63], v[156:157]\n\t"
"v_pk_fma_f32 v[236:237], v[238:239], s[56:57], v[236:237]\n\t"
"v_pk_fma_f32 v[238:239], v[236:237], s[58:59], v[238:239] neg_lo:[0,0,1] neg_hi:[0,0,1]\n\t"
"v_pk_fma_f32 v[98:99], v[58:59], s[60:61], v[236:237]\n\t"
"v_pk_fma_f32 v[58:59], v[236:237], s[60:61], v[58:59]\n\t"
"v_pk_fma_f32 v[236:237], v[158:159], s[62:63], v[238:239]\n\t"
"v_pk_fma_f32 v[158:159], v[238:239], s[62:63], v[158:159]\n\t"
"v_mfma_f32_16x16x32_bf16 v[16:19], a[220:223], a[184:187], v[16:19]\n\t"
"s_waitcnt lgkmcnt(2)\n\t"
"s_mov_b32 s20, 589824\n\t"
"v_accvgpr_write_b32 a19, v224\n\t"
"v_mfma_f32_16x16x32_bf16 v[16:19], a[224:227], a[188:191], v[16:19]\n\t"
"v_accvgpr_write_b32 a23, v228\n\t"
"v_accvgpr_read_b32 v224, a96\n\t"
"v_mfma_f32_16x16x32_bf16 v[16:19], a[216:219], a[192:195], v[16:19]\n\t"
"buffer_load_dwordx4 a[76:79], v252, s[8:11], s20 offen offset:0 sc1\n\t"
"v_accvgpr_read_b32 v226, a100\n\t"
"v_accvgpr_read_b32 v225, a97\n\t"
"v_accvgpr_read_b32 v227, a101\n\t"
"v_cvt_pk_bf16_f32 v230, v230, v231\n\t"
"v_cvt_pk_bf16_f32 v234, v234, v235\n\t"
"v_pk_fma_f32 v[224:225], v[226:227], s[56:57], v[224:225]\n\t"
"v_pk_fma_f32 v[226:227], v[224:225], s[58:59], v[226:227] neg_lo:[0,0,1] neg_hi:[0,0,1]\n\t"
"v_pk_fma_f32 v[228:229], v[60:61], s[60:61], v[224:225]\n\t"
"v_pk_fma_f32 v[60:61], v[224:225], s[60:61], v[60:61]\n\t"
"v_pk_fma_f32 v[224:225], v[160:161], s[62:63], v[226:227]\n\t"
"v_pk_fma_f32 v[160:161], v[226:227], s[62:63], v[160:161]\n\t"
"v_mfma_f32_16x16x32_bf16 v[20:23], a[220:223], a[188:191], v[20:23]\n\t"
"s_waitcnt lgkmcnt(1)\n\t"
"v_accvgpr_write_b32 a26, v230\n\t"
"v_cvt_pk_bf16_f32 v236, v236, v237\n\t"
"v_mfma_f32_16x16x32_bf16 v[20:23], a[224:227], a[192:195], v[20:23]\n\t"
"v_accvgpr_write_b32 a30, v234\n\t"
"v_cvt_pk_bf16_f32 v98, v98, v99\n\t"
"v_mfma_f32_16x16x32_bf16 v[20:23], a[216:219], a[196:199], v[20:23]\n\t"
"buffer_load_dwordx4 a[80:83], v252, s[8:11], s20 offen offset:1024 sc1\n\t"
"v_accvgpr_read_b32 v230, a98\n\t"
"v_accvgpr_read_b32 v232, a102\n\t"
"v_accvgpr_read_b32 v231, a99\n\t"
"v_accvgpr_read_b32 v233, a103\n\t"
"v_accvgpr_write_b32 a27, v236\n\t"
"v_accvgpr_write_b32 a31, v98\n\t"
"v_cvt_pk_bf16_f32 v224, v224, v225\n\t"
"v_cvt_pk_bf16_f32 v228, v228, v229\n\t"
"v_pk_fma_f32 v[230:231], v[232:233], s[56:57], v[230:231]\n\t"
"v_pk_fma_f32 v[232:233], v[230:231], s[58:59], v[232:233] neg_lo:[0,0,1] neg_hi:[0,0,1]\n\t"
"v_pk_fma_f32 v[234:235], v[62:63], s[60:61], v[230:231]\n\t"
"v_pk_fma_f32 v[62:63], v[230:231], s[60:61], v[62:63]\n\t"
"v_pk_fma_f32 v[230:231], v[162:163], s[62:63], v[232:233]\n\t"
"v_pk_fma_f32 v[162:163], v[232:233], s[62:63], v[162:163]\n\t"
"v_mfma_f32_16x16x32_bf16 v[24:27], a[220:223], a[192:195], v[24:27]\n\t"
"s_waitcnt lgkmcnt(0)\n\t"
"v_accvgpr_write_b32 a34, v224\n\t"
"v_accvgpr_write_b32 a38, v228\n\t"
"v_mfma_f32_16x16x32_bf16 v[24:27], a[224:227], a[196:199], v[24:27]\n\t"
"v_accvgpr_read_b32 v236, a104\n\t"
"v_accvgpr_read_b32 v238, a108\n\t"
"v_mfma_f32_16x16x32_bf16 v[24:27], a[216:219], a[200:203], v[24:27]\n\t"
"buffer_load_dwordx4 a[84:87], v252, s[8:11], s20 offen offset:2048 sc1\n\t"
"v_accvgpr_read_b32 v237, a105\n\t"
"v_accvgpr_read_b32 v239, a109\n\t"
"v_cvt_pk_bf16_f32 v230, v230, v231\n\t"
"v_cvt_pk_bf16_f32 v234, v234, v235\n\t"
"v_pk_fma_f32 v[236:237], v[238:239], s[56:57], v[236:237]\n\t"
"v_pk_fma_f32 v[238:239], v[236:237], s[58:59], v[238:239] neg_lo:[0,0,1] neg_hi:[0,0,1]\n\t"
"v_pk_fma_f32 v[98:99], v[64:65], s[60:61], v[236:237]\n\t"
"v_pk_fma_f32 v[64:65], v[236:237], s[60:61], v[64:65]\n\t"
"v_pk_fma_f32 v[236:237], v[164:165], s[62:63], v[238:239]\n\t"
"v_pk_fma_f32 v[164:165], v[238:239], s[62:63], v[164:165]\n\t"
"v_mfma_f32_16x16x32_bf16 v[28:31], a[220:223], a[196:199], v[28:31]\n\t"
"s_waitcnt vmcnt(16)\n\t"
"s_mov_b32 s20, 602112\n\t"
"v_accvgpr_write_b32 a35, v230\n\t"
"v_mfma_f32_16x16x32_bf16 v[28:31], a[224:227], a[200:203], v[28:31]\n\t"
"v_accvgpr_write_b32 a39, v234\n\t"
"v_accvgpr_read_b32 v224, a106\n\t"
"v_mfma_f32_16x16x32_bf16 v[28:31], a[216:219], a[204:207], v[28:31]\n\t"
"buffer_load_dwordx4 a[220:223], v252, s[8:11], s20 offen offset:0 sc1\n\t"
"v_accvgpr_read_b32 v226, a110\n\t"
"v_accvgpr_read_b32 v225, a107\n\t"
"v_accvgpr_read_b32 v227, a111\n\t"
"v_cvt_pk_bf16_f32 v236, v236, v237\n\t"
"v_cvt_pk_bf16_f32 v98, v98, v99\n\t"
"v_pk_fma_f32 v[224:225], v[226:227], s[56:57], v[224:225]\n\t"
"v_pk_fma_f32 v[226:227], v[224:225], s[58:59], v[226:227] neg_lo:[0,0,1] neg_hi:[0,0,1]\n\t"
"v_pk_fma_f32 v[228:229], v[66:67], s[60:61], v[224:225]\n\t"
"v_pk_fma_f32 v[66:67], v[224:225], s[60:61], v[66:67]\n\t"
"v_pk_fma_f32 v[224:225], v[166:167], s[62:63], v[226:227]\n\t"
"v_pk_fma_f32 v[166:167], v[226:227], s[62:63], v[166:167]\n\t"
"v_mfma_f32_16x16x32_bf16 v[32:35], a[228:231], a[168:171], v[32:35]\n\t"
"v_accvgpr_write_b32 a104, v236\n\t"
"v_accvgpr_write_b32 a106, v98\n\t"
"v_mfma_f32_16x16x32_bf16 v[32:35], a[232:235], a[172:175], v[32:35]\n\t"
"v_accvgpr_read_b32 v230, a112\n\t"
"v_accvgpr_read_b32 v232, a116\n\t"
"v_mfma_f32_16x16x32_bf16 v[32:35], a[208:211], a[176:179], v[32:35]\n\t"
"buffer_load_dwordx4 a[216:219], v252, s[8:11], s20 offen offset:1024 sc1\n\t"
"v_accvgpr_read_b32 v231, a113\n\t"
"v_accvgpr_read_b32 v233, a117\n\t"
"v_cvt_pk_bf16_f32 v224, v224, v225\n\t"
"v_cvt_pk_bf16_f32 v228, v228, v229\n\t"
"v_pk_fma_f32 v[230:231], v[232:233], s[56:57], v[230:231]\n\t"
"v_pk_fma_f32 v[232:233], v[230:231], s[58:59], v[232:233] neg_lo:[0,0,1] neg_hi:[0,0,1]\n\t"
"v_pk_fma_f32 v[234:235], v[68:69], s[60:61], v[230:231]\n\t"
"v_pk_fma_f32 v[68:69], v[230:231], s[60:61], v[68:69]\n\t"
"v_pk_fma_f32 v[230:231], v[168:169], s[62:63], v[232:233]\n\t"
"v_pk_fma_f32 v[168:169], v[232:233], s[62:63], v[168:169]\n\t"
"v_mfma_f32_16x16x32_bf16 v[36:39], a[228:231], a[172:175], v[36:39]\n\t"
"v_accvgpr_write_b32 a105, v224\n\t"
"v_accvgpr_write_b32 a107, v228\n\t"
"v_mfma_f32_16x16x32_bf16 v[36:39], a[232:235], a[176:179], v[36:39]\n\t"
"v_accvgpr_read_b32 v236, a114\n\t"
"v_accvgpr_read_b32 v238, a118\n\t"
"v_mfma_f32_16x16x32_bf16 v[36:39], a[208:211], a[180:183], v[36:39]\n\t"
"buffer_load_dwordx4 a[224:227], v252, s[8:11], s20 offen offset:2048 sc1\n\t"
"v_accvgpr_read_b32 v237, a115\n\t"
"v_accvgpr_read_b32 v239, a119\n\t"
"v_cvt_pk_bf16_f32 v230, v230, v231\n\t"
"v_cvt_pk_bf16_f32 v234, v234, v235\n\t"
"v_pk_fma_f32 v[236:237], v[238:239], s[56:57], v[236:237]\n\t"
"v_pk_fma_f32 v[238:239], v[236:237], s[58:59], v[238:239] neg_lo:[0,0,1] neg_hi:[0,0,1]\n\t"
"v_pk_fma_f32 v[98:99], v[70:71], s[60:61], v[236:237]\n\t"
"v_pk_fma_f32 v[70:71], v[236:237], s[60:61], v[70:71]\n\t"
"v_pk_fma_f32 v[236:237], v[170:171], s[62:63], v[238:239]\n\t"
"v_pk_fma_f32 v[170:171], v[238:239], s[62:63], v[170:171]\n\t"
"v_mfma_f32_16x16x32_bf16 v[40:43], a[228:231], a[176:179], v[40:43]\n\t"
"s_mov_b32 s20, 614400\n\t"
"v_accvgpr_write_b32 a112, v230\n\t"
"v_accvgpr_write_b32 a114, v234\n\t"
"v_mfma_f32_16x16x32_bf16 v[40:43], a[232:235], a[180:183], v[40:43]\n\t"
"v_accvgpr_read_b32 v224, a120\n\t"
"v_accvgpr_read_b32 v226, a124\n\t"
"v_mfma_f32_16x16x32_bf16 v[40:43], a[208:211], a[184:187], v[40:43]\n\t"
"buffer_load_dwordx4 a[88:91], v252, s[8:11], s20 offen offset:0 sc1\n\t"
"v_accvgpr_read_b32 v225, a121\n\t"
"v_accvgpr_read_b32 v227, a125\n\t"
"v_cvt_pk_bf16_f32 v236, v236, v237\n\t"
"v_cvt_pk_bf16_f32 v98, v98, v99\n\t"
"v_pk_fma_f32 v[224:225], v[226:227], s[56:57], v[224:225]\n\t"
"v_pk_fma_f32 v[226:227], v[224:225], s[58:59], v[226:227] neg_lo:[0,0,1] neg_hi:[0,0,1]\n\t"
"v_pk_fma_f32 v[228:229], v[72:73], s[60:61], v[224:225]\n\t"
"v_pk_fma_f32 v[72:73], v[224:225], s[60:61], v[72:73]\n\t"
"v_pk_fma_f32 v[224:225], v[172:173], s[62:63], v[226:227]\n\t"
"v_pk_fma_f32 v[172:173], v[226:227], s[62:63], v[172:173]\n\t"
"v_mfma_f32_16x16x32_bf16 v[44:47], a[228:231], a[180:183], v[44:47]\n\t"
"v_accvgpr_write_b32 a113, v236\n\t"
"v_accvgpr_write_b32 a115, v98\n\t"
"v_mfma_f32_16x16x32_bf16 v[44:47], a[232:235], a[184:187], v[44:47]\n\t"
"v_accvgpr_read_b32 v230, a122\n\t"
"v_accvgpr_read_b32 v232, a126\n\t"
"v_mfma_f32_16x16x32_bf16 v[44:47], a[208:211], a[188:191], v[44:47]\n\t"
"buffer_load_dwordx4 a[92:95], v252, s[8:11], s20 offen offset:1024 sc1\n\t"
"v_accvgpr_read_b32 v231, a123\n\t"
"v_accvgpr_read_b32 v233, a127\n\t"
"v_accvgpr_read_b32 v236, a128\n\t"
"v_accvgpr_read_b32 v238, a132\n\t"
"v_accvgpr_read_b32 v237, a129\n\t"
"v_accvgpr_read_b32 v239, a133\n\t"
"v_cvt_pk_bf16_f32 v224, v224, v225\n\t"
"v_cvt_pk_bf16_f32 v228, v228, v229\n\t"
"v_pk_fma_f32 v[230:231], v[232:233], s[56:57], v[230:231]\n\t"
"v_pk_fma_f32 v[232:233], v[230:231], s[58:59], v[232:233] neg_lo:[0,0,1] neg_hi:[0,0,1]\n\t"
"v_pk_fma_f32 v[234:235], v[74:75], s[60:61], v[230:231]\n\t"
"v_pk_fma_f32 v[74:75], v[230:231], s[60:61], v[74:75]\n\t"
"v_pk_fma_f32 v[230:231], v[174:175], s[62:63], v[232:233]\n\t"
"v_pk_fma_f32 v[174:175], v[232:233], s[62:63], v[174:175]\n\t"
"v_pk_fma_f32 v[236:237], v[238:239], s[56:57], v[236:237]\n\t"
"v_pk_fma_f32 v[238:239], v[236:237], s[58:59], v[238:239] neg_lo:[0,0,1] neg_hi:[0,0,1]\n\t"
"v_pk_fma_f32 v[98:99], v[76:77], s[60:61], v[236:237]\n\t"
"v_pk_fma_f32 v[76:77], v[236:237], s[60:61], v[76:77]\n\t"
"v_pk_fma_f32 v[236:237], v[176:177], s[62:63], v[238:239]\n\t"
"v_pk_fma_f32 v[176:177], v[238:239], s[62:63], v[176:177]\n\t"
"v_mfma_f32_16x16x32_bf16 v[48:51], a[228:231], a[184:187], v[48:51]\n\t"
"v_accvgpr_write_b32 a120, v224\n\t"
"v_accvgpr_write_b32 a122, v228\n\t"
"v_mfma_f32_16x16x32_bf16 v[48:51], a[232:235], a[188:191], v[48:51]\n\t"
"v_accvgpr_read_b32 v224, a130\n\t"
"v_accvgpr_read_b32 v226, a134\n\t"
"v_mfma_f32_16x16x32_bf16 v[48:51], a[208:211], a[192:195], v[48:51]\n\t"
"buffer_load_dwordx4 a[96:99], v252, s[8:11], s20 offen offset:2048 sc1\n\t"
"v_accvgpr_read_b32 v225, a131\n\t"
"v_accvgpr_read_b32 v227, a135\n\t"
"v_cvt_pk_bf16_f32 v230, v230, v231\n\t"
"v_cvt_pk_bf16_f32 v234, v234, v235\n\t"
"v_cvt_pk_bf16_f32 v236, v236, v237\n\t"
"v_cvt_pk_bf16_f32 v98, v98, v99\n\t"
"v_pk_fma_f32 v[224:225], v[226:227], s[56:57], v[224:225]\n\t"
"v_pk_fma_f32 v[226:227], v[224:225], s[58:59], v[226:227] neg_lo:[0,0,1] neg_hi:[0,0,1]\n\t"
"v_pk_fma_f32 v[228:229], v[78:79], s[60:61], v[224:225]\n\t"
"v_pk_fma_f32 v[78:79], v[224:225], s[60:61], v[78:79]\n\t"
"v_pk_fma_f32 v[224:225], v[178:179], s[62:63], v[226:227]\n\t"
"v_pk_fma_f32 v[178:179], v[226:227], s[62:63], v[178:179]\n\t"
"v_mfma_f32_16x16x32_bf16 v[52:55], a[228:231], a[188:191], v[52:55]\n\t"
"s_mov_b32 s20, 774144\n\t"
"v_accvgpr_write_b32 a121, v230\n\t"
"v_accvgpr_write_b32 a123, v234\n\t"
"v_mfma_f32_16x16x32_bf16 v[52:55], a[232:235], a[192:195], v[52:55]\n\t"
"v_accvgpr_write_b32 a128, v236\n\t"
"v_accvgpr_write_b32 a130, v98\n\t"
"v_mfma_f32_16x16x32_bf16 v[52:55], a[208:211], a[196:199], v[52:55]\n\t"
"s_waitcnt vmcnt(17)\n\t"
"ds_write_b128 v249, v[196:199] offset:18432\n\t"
"v_accvgpr_read_b32 v230, a136\n\t"
"v_accvgpr_read_b32 v232, a140\n\t"
"v_accvgpr_read_b32 v231, a137\n\t"
"v_accvgpr_read_b32 v233, a141\n\t"
"v_cvt_pk_bf16_f32 v224, v224, v225\n\t"
"v_cvt_pk_bf16_f32 v228, v228, v229\n\t"
"v_pk_fma_f32 v[230:231], v[232:233], s[56:57], v[230:231]\n\t"
"v_pk_fma_f32 v[232:233], v[230:231], s[58:59], v[232:233] neg_lo:[0,0,1] neg_hi:[0,0,1]\n\t"
"v_pk_fma_f32 v[234:235], v[80:81], s[60:61], v[230:231]\n\t"
"v_pk_fma_f32 v[80:81], v[230:231], s[60:61], v[80:81]\n\t"
"v_pk_fma_f32 v[230:231], v[180:181], s[62:63], v[232:233]\n\t"
"v_pk_fma_f32 v[180:181], v[232:233], s[62:63], v[180:181]\n\t"
"v_mfma_f32_16x16x32_bf16 v[56:59], a[228:231], a[192:195], v[56:59]\n\t"
"v_accvgpr_write_b32 a129, v224\n\t"
"v_accvgpr_write_b32 a131, v228\n\t"
"v_mfma_f32_16x16x32_bf16 v[56:59], a[232:235], a[196:199], v[56:59]\n\t"
"v_accvgpr_read_b32 v236, a138\n\t"
"v_accvgpr_read_b32 v238, a142\n\t"
"v_mfma_f32_16x16x32_bf16 v[56:59], a[208:211], a[200:203], v[56:59]\n\t"
"buffer_load_dwordx4 a[100:103], v252, s[8:11], s20 offen offset:0 sc1\n\t"
"v_accvgpr_read_b32 v237, a139\n\t"
"v_accvgpr_read_b32 v239, a143\n\t"
"v_cvt_pk_bf16_f32 v230, v230, v231\n\t"
"v_cvt_pk_bf16_f32 v234, v234, v235\n\t"
"v_pk_fma_f32 v[236:237], v[238:239], s[56:57], v[236:237]\n\t"
"v_pk_fma_f32 v[238:239], v[236:237], s[58:59], v[238:239] neg_lo:[0,0,1] neg_hi:[0,0,1]\n\t"
"v_pk_fma_f32 v[98:99], v[82:83], s[60:61], v[236:237]\n\t"
"v_pk_fma_f32 v[82:83], v[236:237], s[60:61], v[82:83]\n\t"
"v_pk_fma_f32 v[236:237], v[182:183], s[62:63], v[238:239]\n\t"
"v_pk_fma_f32 v[182:183], v[238:239], s[62:63], v[182:183]\n\t"
"v_mfma_f32_16x16x32_bf16 v[60:63], a[228:231], a[196:199], v[60:63]\n\t"
"v_accvgpr_write_b32 a136, v230\n\t"
"v_accvgpr_write_b32 a138, v234\n\t"
"v_mfma_f32_16x16x32_bf16 v[60:63], a[232:235], a[200:203], v[60:63]\n\t"
"v_accvgpr_read_b32 v224, a144\n\t"
"v_accvgpr_read_b32 v226, a148\n\t"
"v_mfma_f32_16x16x32_bf16 v[60:63], a[208:211], a[204:207], v[60:63]\n\t"
"buffer_load_dwordx4 a[108:111], v252, s[8:11], s20 offen offset:1024 sc1\n\t"
"v_accvgpr_read_b32 v225, a145\n\t"
"v_accvgpr_read_b32 v227, a149\n\t"
"v_cvt_pk_bf16_f32 v236, v236, v237\n\t"
"v_cvt_pk_bf16_f32 v98, v98, v99\n\t"
"v_pk_fma_f32 v[224:225], v[226:227], s[56:57], v[224:225]\n\t"
"v_pk_fma_f32 v[226:227], v[224:225], s[58:59], v[226:227] neg_lo:[0,0,1] neg_hi:[0,0,1]\n\t"
"v_pk_fma_f32 v[228:229], v[84:85], s[60:61], v[224:225]\n\t"
"v_pk_fma_f32 v[84:85], v[224:225], s[60:61], v[84:85]\n\t"
"v_pk_fma_f32 v[224:225], v[184:185], s[62:63], v[226:227]\n\t"
"v_pk_fma_f32 v[184:185], v[226:227], s[62:63], v[184:185]\n\t"
"v_mfma_f32_16x16x32_bf16 v[64:67], a[236:239], a[168:171], v[64:67]\n\t"
"ds_read_b128 a[168:171], v255 offset:2048\n\t"
"v_accvgpr_write_b32 a137, v236\n\t"
"v_mfma_f32_16x16x32_bf16 v[64:67], a[240:243], a[172:175], v[64:67]\n\t"
"v_accvgpr_write_b32 a139, v98\n\t"
"v_accvgpr_read_b32 v230, a146\n\t"
"v_mfma_f32_16x16x32_bf16 v[64:67], a[244:247], a[176:179], v[64:67]\n\t"
"ds_write_b64 v250, a[104:105] offset:18432\n\t"
"v_accvgpr_read_b32 v232, a150\n\t"
"v_accvgpr_read_b32 v231, a147\n\t"
"v_accvgpr_read_b32 v233, a151\n\t"
"v_cvt_pk_bf16_f32 v224, v224, v225\n\t"
"v_cvt_pk_bf16_f32 v228, v228, v229\n\t"
"v_pk_fma_f32 v[230:231], v[232:233], s[56:57], v[230:231]\n\t"
"v_pk_fma_f32 v[232:233], v[230:231], s[58:59], v[232:233] neg_lo:[0,0,1] neg_hi:[0,0,1]\n\t"
"v_pk_fma_f32 v[234:235], v[86:87], s[60:61], v[230:231]\n\t"
"v_pk_fma_f32 v[86:87], v[230:231], s[60:61], v[86:87]\n\t"
"v_pk_fma_f32 v[230:231], v[186:187], s[62:63], v[232:233]\n\t"
"v_pk_fma_f32 v[186:187], v[232:233], s[62:63], v[186:187]\n\t"
"v_mfma_f32_16x16x32_bf16 v[68:71], a[236:239], a[172:175], v[68:71]\n\t"
"ds_read_b128 a[172:175], v255 offset:5120\n\t"
"v_accvgpr_write_b32 a144, v224\n\t"
"v_mfma_f32_16x16x32_bf16 v[68:71], a[240:243], a[176:179], v[68:71]\n\t"
"v_accvgpr_write_b32 a146, v228\n\t"
"v_accvgpr_read_b32 v236, a152\n\t"
"v_mfma_f32_16x16x32_bf16 v[68:71], a[244:247], a[180:183], v[68:71]\n\t"
"buffer_load_dwordx4 a[228:231], v252, s[8:11], s20 offen offset:2048 sc1\n\t"
"v_accvgpr_read_b32 v238, a156\n\t"
"v_accvgpr_read_b32 v237, a153\n\t"
"v_accvgpr_read_b32 v239, a157\n\t"
"v_cvt_pk_bf16_f32 v230, v230, v231\n\t"
"v_cvt_pk_bf16_f32 v234, v234, v235\n\t"
"s_mov_b32 s20, 786432\n\t"
"v_pk_fma_f32 v[236:237], v[238:239], s[56:57], v[236:237]\n\t"
"v_pk_fma_f32 v[238:239], v[236:237], s[58:59], v[238:239] neg_lo:[0,0,1] neg_hi:[0,0,1]\n\t"
"v_pk_fma_f32 v[98:99], v[88:89], s[60:61], v[236:237]\n\t"
"v_pk_fma_f32 v[88:89], v[236:237], s[60:61], v[88:89]\n\t"
"v_pk_fma_f32 v[236:237], v[188:189], s[62:63], v[238:239]\n\t"
"v_pk_fma_f32 v[188:189], v[238:239], s[62:63], v[188:189]\n\t"
"v_mfma_f32_16x16x32_bf16 v[72:75], a[236:239], a[176:179], v[72:75]\n\t"
"Lpg24_%=:\n\t"
"s_cbranch_vccnz Lvr3_%=\n\t"
"ds_read_b128 a[176:179], v255 offset:8192\n\t"
"v_accvgpr_write_b32 a145, v230\n\t"
"v_mfma_f32_16x16x32_bf16 v[72:75], a[240:243], a[180:183], v[72:75]\n\t"
"v_accvgpr_write_b32 a147, v234\n\t"
"v_accvgpr_read_b32 v224, a154\n\t"
"v_mfma_f32_16x16x32_bf16 v[72:75], a[244:247], a[184:187], v[72:75]\n\t"
"buffer_load_dwordx4 a[208:211], v252, s[8:11], s20 offen offset:0 sc1\n\t"
"v_accvgpr_read_b32 v226, a158\n\t"
"v_accvgpr_read_b32 v225, a155\n\t"
"v_accvgpr_read_b32 v227, a159\n\t"
"v_cvt_pk_bf16_f32 v236, v236, v237\n\t"
"v_cvt_pk_bf16_f32 v98, v98, v99\n\t"
"v_pk_fma_f32 v[224:225], v[226:227], s[56:57], v[224:225]\n\t"
"v_pk_fma_f32 v[226:227], v[224:225], s[58:59], v[226:227] neg_lo:[0,0,1] neg_hi:[0,0,1]\n\t"
"v_pk_fma_f32 v[228:229], v[90:91], s[60:61], v[224:225]\n\t"
"v_pk_fma_f32 v[90:91], v[224:225], s[60:61], v[90:91]\n\t"
"v_pk_fma_f32 v[224:225], v[190:191], s[62:63], v[226:227]\n\t"
"v_pk_fma_f32 v[190:191], v[226:227], s[62:63], v[190:191]\n\t"
"v_mfma_f32_16x16x32_bf16 v[76:79], a[236:239], a[180:183], v[76:79]\n\t"
"ds_read_b128 a[180:183], v255 offset:11264\n\t"
"v_accvgpr_write_b32 a152, v236\n\t"
"v_mfma_f32_16x16x32_bf16 v[76:79], a[240:243], a[184:187], v[76:79]\n\t"
"v_accvgpr_write_b32 a154, v98\n\t"
"v_accvgpr_read_b32 v230, a160\n\t"
"v_mfma_f32_16x16x32_bf16 v[76:79], a[244:247], a[188:191], v[76:79]\n\t"
"buffer_load_dwordx4 a[232:235], v252, s[8:11], s20 offen offset:1024 sc1\n\t"
"v_accvgpr_read_b32 v232, a164\n\t"
"v_accvgpr_read_b32 v231, a161\n\t"
"v_accvgpr_read_b32 v233, a165\n\t"
"v_cvt_pk_bf16_f32 v224, v224, v225\n\t"
"v_cvt_pk_bf16_f32 v228, v228, v229\n\t"
"v_pk_fma_f32 v[230:231], v[232:233], s[56:57], v[230:231]\n\t"
"v_pk_fma_f32 v[232:233], v[230:231], s[58:59], v[232:233] neg_lo:[0,0,1] neg_hi:[0,0,1]\n\t"
"v_pk_fma_f32 v[234:235], v[92:93], s[60:61], v[230:231]\n\t"
"v_pk_fma_f32 v[92:93], v[230:231], s[60:61], v[92:93]\n\t"
"v_pk_fma_f32 v[230:231], v[192:193], s[62:63], v[232:233]\n\t"
"v_pk_fma_f32 v[192:193], v[232:233], s[62:63], v[192:193]\n\t"
"v_mfma_f32_16x16x32_bf16 v[80:83], a[236:239], a[184:187], v[80:83]\n\t"
"ds_read_b128 a[184:187], v255 offset:14336\n\t"
"v_accvgpr_write_b32 a153, v224\n\t"
"v_mfma_f32_16x16x32_bf16 v[80:83], a[240:243], a[188:191], v[80:83]\n\t"
"v_accvgpr_write_b32 a155, v228\n\t"
"v_accvgpr_read_b32 v236, a162\n\t"
"v_mfma_f32_16x16x32_bf16 v[80:83], a[244:247], a[192:195], v[80:83]\n\t"
"buffer_load_dwordx4 a[116:119], v252, s[8:11], s20 offen offset:2048 sc1\n\t"
"v_accvgpr_read_b32 v238, a166\n\t"
"v_accvgpr_read_b32 v237, a163\n\t"
"v_accvgpr_read_b32 v239, a167\n\t"
"v_cvt_pk_bf16_f32 v230, v230, v231\n\t"
"v_cvt_pk_bf16_f32 v234, v234, v235\n\t"
"s_mov_b32 s20, 798720\n\t"
"v_pk_fma_f32 v[236:237], v[238:239], s[56:57], v[236:237]\n\t"
"v_pk_fma_f32 v[238:239], v[236:237], s[58:59], v[238:239] neg_lo:[0,0,1] neg_hi:[0,0,1]\n\t"
"v_pk_fma_f32 v[98:99], v[94:95], s[60:61], v[236:237]\n\t"
"v_pk_fma_f32 v[94:95], v[236:237], s[60:61], v[94:95]\n\t"
"v_pk_fma_f32 v[236:237], v[194:195], s[62:63], v[238:239]\n\t"
"v_pk_fma_f32 v[194:195], v[238:239], s[62:63], v[194:195]\n\t"
"v_mfma_f32_16x16x32_bf16 v[84:87], a[236:239], a[188:191], v[84:87]\n\t"
"ds_read_b128 a[188:191], v255 offset:17408\n\t"
"v_accvgpr_write_b32 a160, v230\n\t"
"v_mfma_f32_16x16x32_bf16 v[84:87], a[240:243], a[192:195], v[84:87]\n\t"
"v_accvgpr_write_b32 a162, v234\n\t"
"v_cvt_pk_bf16_f32 v236, v236, v237\n\t"
"v_mfma_f32_16x16x32_bf16 v[84:87], a[244:247], a[196:199], v[84:87]\n\t"
"buffer_load_dwordx4 a[124:127], v252, s[8:11], s20 offen offset:0 sc1\n\t"
"v_cvt_pk_bf16_f32 v98, v98, v99\n\t"
"v_mfma_f32_16x16x32_bf16 v[88:91], a[236:239], a[192:195], v[88:91]\n\t"
"ds_read_b128 a[192:195], v255 offset:20480\n\t"
"v_accvgpr_write_b32 a161, v236\n\t"
"v_mfma_f32_16x16x32_bf16 v[88:91], a[240:243], a[196:199], v[88:91]\n\t"
"v_accvgpr_write_b32 a163, v98\n\t"
"v_mfma_f32_16x16x32_bf16 v[88:91], a[244:247], a[200:203], v[88:91]\n\t"
"ds_write_b128 v249, v[200:203] offset:24576\n\t"
"buffer_load_dwordx4 a[132:135], v252, s[8:11], s20 offen offset:1024 sc1\n\t"
"v_mfma_f32_16x16x32_bf16 v[92:95], a[236:239], a[196:199], v[92:95]\n\t"
"buffer_load_dwordx4 a[140:143], v252, s[8:11], s20 offen offset:2048 sc1\n\t"
"v_mfma_f32_16x16x32_bf16 v[92:95], a[240:243], a[200:203], v[92:95]\n\t"
"s_waitcnt vmcnt(24) lgkmcnt(5)\n\t"
"ds_read_b128 a[196:199], v255 offset:23552\n\t"
"s_mov_b32 s20, 884736\n\t"
"v_mfma_f32_16x16x32_bf16 v[92:95], a[244:247], a[204:207], v[92:95]\n\t"
"ds_write_b64 v250, a[106:107] offset:24576\n\t"
"v_mfma_f32_16x16x32_bf16 v[100:103], a[248:251], a[168:171], v[100:103]\n\t"
"buffer_load_dwordx4 a[148:151], v252, s[8:11], s20 offen offset:0 sc1\n\t"
"v_mfma_f32_16x16x32_bf16 v[100:103], a[52:55], a[172:175], v[100:103]\n\t"
"s_waitcnt lgkmcnt(6)\n\t"
"ds_read_b128 a[200:203], v255 offset:26624\n\t"
"v_mfma_f32_16x16x32_bf16 v[100:103], a[56:59], a[176:179], v[100:103]\n\t"
"ds_read_b128 a[204:207], v255 offset:29696\n\t"
"v_mfma_f32_16x16x32_bf16 v[104:107], a[248:251], a[172:175], v[104:107]\n\t"
"buffer_load_dwordx4 a[236:239], v252, s[8:11], s20 offen offset:1024 sc1\n\t"
"v_mfma_f32_16x16x32_bf16 v[104:107], a[52:55], a[176:179], v[104:107]\n\t"
"s_waitcnt lgkmcnt(7)\n\t"
"v_mfma_f32_16x16x32_bf16 v[104:107], a[56:59], a[180:183], v[104:107]\n\t"
"s_waitcnt lgkmcnt(0)\n\t"
"s_barrier\n\t"
"v_mfma_f32_16x16x32_bf16 v[108:111], a[248:251], a[176:179], v[108:111]\n\t"
"ds_read_b128 a[40:43], v252 offset:18432\n\t"
"v_mfma_f32_16x16x32_bf16 v[108:111], a[52:55], a[180:183], v[108:111]\n\t"
"buffer_load_dwordx4 a[240:243], v252, s[8:11], s20 offen offset:2048 sc1\n\t"
"v_mfma_f32_16x16x32_bf16 v[108:111], a[56:59], a[184:187], v[108:111]\n\t"
"s_mov_b32 s20, 1474560\n\t"
"ds_read_b128 a[44:47], v252 offset:19456\n\t"
"v_add_u32 v224, 98304, v249\n\t"
"v_mfma_f32_16x16x32_bf16 v[112:115], a[248:251], a[180:183], v[112:115]\n\t"
"s_add_u32 s29, s22, 384\n\t"
"ds_read_b128 a[48:51], v252 offset:20480\n\t"
"v_add_u32 v225, 98304, v250\n\t"
"v_mfma_f32_16x16x32_bf16 v[112:115], a[52:55], a[184:187], v[112:115]\n\t"
"v_add_u32 v226, 98304, v252\n\t"
"v_mfma_f32_16x16x32_bf16 v[112:115], a[56:59], a[188:191], v[112:115]\n\t"
"buffer_load_dwordx4 a[244:247], v252, s[8:11], s20 offen offset:0 sc1\n\t"
"v_mfma_f32_16x16x32_bf16 v[116:119], a[248:251], a[184:187], v[116:119]\n\t"
"buffer_load_dwordx4 a[104:107], v252, s[8:11], s20 offen offset:1024 sc1\n\t"
"v_mfma_f32_16x16x32_bf16 v[116:119], a[52:55], a[188:191], v[116:119]\n\t"
"buffer_load_dwordx4 a[156:159], v252, s[8:11], s20 offen offset:2048 sc1\n\t"
"v_mfma_f32_16x16x32_bf16 v[116:119], a[56:59], a[192:195], v[116:119]\n\t"
"s_mov_b32 s20, 897024\n\t"
"ds_write_b128 v249, v[204:207] offset:30720\n\t"
"s_waitcnt lgkmcnt(3)\n\t"
"v_mfma_f32_16x16x32_bf16 v[120:123], a[248:251], a[188:191], v[120:123]\n\t"
"buffer_load_dwordx4 a[164:167], v252, s[8:11], s20 offen offset:0 sc1\n\t"
"v_mfma_f32_16x16x32_bf16 v[120:123], a[52:55], a[192:195], v[120:123]\n\t"
"buffer_load_dwordx4 v[240:243], v252, s[8:11], s20 offen offset:1024 sc1\n\t"
"v_mfma_f32_16x16x32_bf16 v[120:123], a[56:59], a[196:199], v[120:123]\n\t"
"s_waitcnt lgkmcnt(2)\n\t"
"ds_write_b128 v224, v[212:215] offset:0\n\t"
"v_mfma_f32_16x16x32_bf16 v[124:127], a[248:251], a[192:195], v[124:127]\n\t"
"s_waitcnt lgkmcnt(2)\n\t"
"v_mfma_f32_16x16x32_bf16 v[124:127], a[52:55], a[196:199], v[124:127]\n\t"
"buffer_load_dwordx4 v[96:99], v252, s[8:11], s20 offen offset:2048 sc1\n\t"
"v_mfma_f32_16x16x32_bf16 v[124:127], a[56:59], a[200:203], v[124:127]\n\t"
"s_mov_b32 s20, 1486848\n\t"
"ds_write_b128 v224, a[0:3] offset:12288\n\t"
"v_mfma_f32_16x16x32_bf16 v[128:131], a[248:251], a[196:199], v[128:131]\n\t"
"s_waitcnt vmcnt(30)\n\t"
"v_mfma_f32_16x16x32_bf16 v[128:131], a[52:55], a[200:203], v[128:131]\n\t"
"buffer_load_dwordx4 a[248:251], v252, s[8:11], s20 offen offset:0 sc1\n\t"
"v_mfma_f32_16x16x32_bf16 v[128:131], a[56:59], a[204:207], v[128:131]\n\t"
"ds_write_b128 v224, a[8:11] offset:24576\n\t"
"v_mfma_f32_16x16x32_bf16 v[132:135], a[60:63], a[168:171], v[132:135]\n\t"
"v_mfma_f32_16x16x32_bf16 v[132:135], a[252:255], a[172:175], v[132:135]\n\t"
"buffer_load_dwordx4 a[52:55], v252, s[8:11], s20 offen offset:1024 sc1\n\t"
"v_mfma_f32_16x16x32_bf16 v[132:135], a[212:215], a[176:179], v[132:135]\n\t"
"ds_write_b128 v224, a[16:19] offset:36864\n\t"
"ds_write_b128 v224, a[24:27] offset:49152\n\t"
"buffer_store_dwordx4 a[40:43], v248, s[16:19], s29 offen offset:0 nt\n\t"
"v_mfma_f32_16x16x32_bf16 v[136:139], a[60:63], a[172:175], v[136:139]\n\t"
"v_mfma_f32_16x16x32_bf16 v[136:139], a[252:255], a[176:179], v[136:139]\n\t"
"buffer_load_dwordx4 a[56:59], v252, s[8:11], s20 offen offset:2048 sc1\n\t"
"v_mfma_f32_16x16x32_bf16 v[136:139], a[212:215], a[180:183], v[136:139]\n\t"
"buffer_store_dwordx4 a[44:47], v248, s[16:19], s29 offen offset:128 nt\n\t"
"ds_write_b64 v250, a[112:113] offset:30720\n\t"
"s_mov_b32 s20, 909312\n\t"
"v_mfma_f32_16x16x32_bf16 v[140:143], a[60:63], a[176:179], v[140:143]\n\t"
"v_mfma_f32_16x16x32_bf16 v[140:143], a[252:255], a[180:183], v[140:143]\n\t"
"v_mfma_f32_16x16x32_bf16 v[140:143], a[212:215], a[184:187], v[140:143]\n\t"
"buffer_store_dwordx4 a[48:51], v248, s[16:19], s29 offen offset:256 nt\n\t"
"ds_write_b64 v225, a[120:121] offset:0\n\t"
"v_mfma_f32_16x16x32_bf16 v[144:147], a[60:63], a[180:183], v[144:147]\n\t"
"v_mfma_f32_16x16x32_bf16 v[144:147], a[252:255], a[184:187], v[144:147]\n\t"
"buffer_load_dwordx4 a[48:51], v252, s[8:11], s20 offen offset:0 sc1\n\t"
"v_mfma_f32_16x16x32_bf16 v[144:147], a[212:215], a[188:191], v[144:147]\n\t"
"ds_write_b64 v225, a[128:129] offset:12288\n\t"
"ds_write_b64 v225, a[136:137] offset:24576\n\t"
"ds_write_b64 v225, a[144:145] offset:36864\n\t"
"ds_write_b64 v225, a[152:153] offset:49152\n\t"
"v_mfma_f32_16x16x32_bf16 v[148:151], a[60:63], a[184:187], v[148:151]\n\t"
"buffer_load_dwordx4 v[236:239], v252, s[8:11], s20 offen offset:1024 sc1\n\t"
"v_mfma_f32_16x16x32_bf16 v[148:151], a[252:255], a[188:191], v[148:151]\n\t"
"v_mfma_f32_16x16x32_bf16 v[148:151], a[212:215], a[192:195], v[148:151]\n\t"
"ds_write_b128 v249, v[208:211] offset:36864\n\t"
"v_mfma_f32_16x16x32_bf16 v[152:155], a[60:63], a[188:191], v[152:155]\n\t"
"v_mfma_f32_16x16x32_bf16 v[152:155], a[252:255], a[192:195], v[152:155]\n\t"
"v_mfma_f32_16x16x32_bf16 v[152:155], a[212:215], a[196:199], v[152:155]\n\t"
"ds_write_b128 v224, v[216:219] offset:6144\n\t"
"v_mfma_f32_16x16x32_bf16 v[156:159], a[60:63], a[192:195], v[156:159]\n\t"
"v_mfma_f32_16x16x32_bf16 v[156:159], a[252:255], a[196:199], v[156:159]\n\t"
"v_mfma_f32_16x16x32_bf16 v[156:159], a[212:215], a[200:203], v[156:159]\n\t"
"ds_write_b128 v224, a[4:7] offset:18432\n\t"
"v_mfma_f32_16x16x32_bf16 v[160:163], a[60:63], a[196:199], v[160:163]\n\t"
"buffer_load_dwordx4 a[60:63], v252, s[8:11], s20 offen offset:2048 sc1\n\t"
"v_mfma_f32_16x16x32_bf16 v[160:163], a[252:255], a[200:203], v[160:163]\n\t"
"s_waitcnt vmcnt(36)\n\t"
"s_mov_b32 s20, 1499136\n\t"
"v_mfma_f32_16x16x32_bf16 v[160:163], a[212:215], a[204:207], v[160:163]\n\t"
"ds_write_b128 v224, a[12:15] offset:30720\n\t"
"ds_write_b128 v224, a[20:23] offset:43008\n\t"
"ds_write_b128 v224, a[28:31] offset:55296\n\t"
"v_mfma_f32_16x16x32_bf16 v[164:167], a[64:67], a[168:171], v[164:167]\n\t"
"buffer_load_dwordx4 a[212:215], v252, s[8:11], s20 offen offset:0 sc1\n\t"
"v_mfma_f32_16x16x32_bf16 v[164:167], a[68:71], a[172:175], v[164:167]\n\t"
"ds_read_b128 a[168:171], v244 offset:0\n\t"
"v_mfma_f32_16x16x32_bf16 v[164:167], a[72:75], a[176:179], v[164:167]\n\t"
"ds_write_b64 v250, a[114:115] offset:36864\n\t"
"v_mfma_f32_16x16x32_bf16 v[168:171], a[64:67], a[172:175], v[168:171]\n\t"
"buffer_load_dwordx4 a[252:255], v252, s[8:11], s20 offen offset:1024 sc1\n\t"
"v_mfma_f32_16x16x32_bf16 v[168:171], a[68:71], a[176:179], v[168:171]\n\t"
"ds_read_b128 a[172:175], v244 offset:512\n\t"
"v_mfma_f32_16x16x32_bf16 v[168:171], a[72:75], a[180:183], v[168:171]\n\t"
"ds_write_b64 v225, a[122:123] offset:6144\n\t"
"v_mfma_f32_16x16x32_bf16 v[172:175], a[64:67], a[176:179], v[172:175]\n\t"
"ds_read_b128 a[176:179], v244 offset:1024\n\t"
"v_mfma_f32_16x16x32_bf16 v[172:175], a[68:71], a[180:183], v[172:175]\n\t"
"v_mfma_f32_16x16x32_bf16 v[172:175], a[72:75], a[184:187], v[172:175]\n\t"
"ds_write_b64 v225, a[130:131] offset:18432\n\t"
"ds_write_b64 v225, a[138:139] offset:30720\n\t"
"ds_write_b64 v225, a[146:147] offset:43008\n\t"
"ds_write_b64 v225, a[154:155] offset:55296\n\t"
"v_mfma_f32_16x16x32_bf16 v[176:179], a[64:67], a[180:183], v[176:179]\n\t"
"ds_read_b128 a[180:183], v244 offset:1536\n\t"
"v_mfma_f32_16x16x32_bf16 v[176:179], a[68:71], a[184:187], v[176:179]\n\t"
"v_mfma_f32_16x16x32_bf16 v[176:179], a[72:75], a[188:191], v[176:179]\n\t"
"s_waitcnt lgkmcnt(1)\n\t"
"s_barrier\n\t"
"v_mfma_f32_16x16x32_bf16 v[180:183], a[64:67], a[184:187], v[180:183]\n\t"
"ds_read_b128 v[196:199], v252 offset:30720\n\t"
"v_mfma_f32_16x16x32_bf16 v[180:183], a[68:71], a[188:191], v[180:183]\n\t"
"ds_read_b128 v[200:203], v252 offset:31744\n\t"
"v_mfma_f32_16x16x32_bf16 v[180:183], a[72:75], a[192:195], v[180:183]\n\t"
"ds_read_b128 a[184:187], v244 offset:2048\n\t"
"v_mfma_f32_16x16x32_bf16 v[184:187], a[64:67], a[188:191], v[184:187]\n\t"
"buffer_load_dwordx4 a[28:31], v252, s[8:11], s20 offen offset:2048 sc1\n\t"
"v_mfma_f32_16x16x32_bf16 v[184:187], a[68:71], a[192:195], v[184:187]\n\t"
"s_mov_b32 s20, 1069056\n\t"
"ds_read_b128 a[40:43], v252 offset:32768\n\t"
"s_add_u32 s29, s22, 196992\n\t"
"v_mfma_f32_16x16x32_bf16 v[184:187], a[72:75], a[196:199], v[184:187]\n\t"
"ds_read_b128 a[188:191], v244 offset:2560\n\t"
"v_mfma_f32_16x16x32_bf16 v[188:191], a[64:67], a[192:195], v[188:191]\n\t"
"buffer_load_dwordx4 a[112:115], v252, s[8:11], s20 offen offset:0 sc1\n\t"
"v_mfma_f32_16x16x32_bf16 v[188:191], a[68:71], a[196:199], v[188:191]\n\t"
"s_waitcnt lgkmcnt(4)\n\t"
"ds_read_b128 a[44:47], v226 offset:0\n\t"
"v_mfma_f32_16x16x32_bf16 v[188:191], a[72:75], a[200:203], v[188:191]\n\t"
"ds_read_b128 a[192:195], v244 offset:3072\n\t"
"v_mfma_f32_16x16x32_bf16 v[192:195], a[64:67], a[196:199], v[192:195]\n\t"
"buffer_load_dwordx4 a[120:123], v252, s[8:11], s20 offen offset:1024 sc1\n\t"
"v_mfma_f32_16x16x32_bf16 v[192:195], a[68:71], a[200:203], v[192:195]\n\t"
"s_waitcnt vmcnt(38)\n\t"
"ds_read_b128 v[204:207], v226 offset:1024\n\t"
"s_waitcnt lgkmcnt(6)\n\t"
"v_mfma_f32_16x16x32_bf16 v[192:195], a[72:75], a[204:207], v[192:195]\n\t"
"ds_write_b128 v249, a[32:35] offset:18432\n\t"
"Lst33_%=:\n\t"
"v_mfma_f32_16x16x32_bf16 v[0:3], a[76:79], a[168:171], v[0:3]\n\t"
"buffer_load_dwordx4 a[64:67], v252, s[8:11], s20 offen offset:2048 sc1\n\t"
"v_mfma_f32_16x16x32_bf16 v[0:3], a[80:83], a[172:175], v[0:3]\n\t"
"s_mov_b32 s20, 1658880\n\t"
"ds_read_b128 a[196:199], v244 offset:3584\n\t"
"v_mfma_f32_16x16x32_bf16 v[0:3], a[84:87], a[176:179], v[0:3]\n\t"
"buffer_store_dwordx4 v[196:199], v248, s[16:19], s29 offen offset:0 nt\n\t"
"ds_read_b128 v[208:211], v226 offset:2048\n\t"
"ds_read_b128 a[200:203], v244 offset:4096\n\t"
"s_waitcnt lgkmcnt(8)\n\t"
"v_mfma_f32_16x16x32_bf16 v[4:7], a[76:79], a[172:175], v[4:7]\n\t"
"s_waitcnt lgkmcnt(6)\n\t"
"ds_read_b128 v[212:215], v226 offset:12288\n\t"
"v_mfma_f32_16x16x32_bf16 v[4:7], a[80:83], a[176:179], v[4:7]\n\t"
"buffer_load_dwordx4 a[68:71], v252, s[8:11], s20 offen offset:0 sc1\n\t"
"v_mfma_f32_16x16x32_bf16 v[4:7], a[84:87], a[180:183], v[4:7]\n\t"
"buffer_store_dwordx4 v[200:203], v248, s[16:19], s29 offen offset:128 nt\n\t"
"ds_read_b128 a[204:207], v244 offset:4608\n\t"
"buffer_load_dwordx4 a[72:75], v252, s[8:11], s20 offen offset:1024 sc1\n\t"
"v_mfma_f32_16x16x32_bf16 v[8:11], a[76:79], a[176:179], v[8:11]\n\t"
"s_waitcnt lgkmcnt(6)\n\t"
"ds_read_b128 v[216:219], v226 offset:13312\n\t"
"v_mfma_f32_16x16x32_bf16 v[8:11], a[80:83], a[180:183], v[8:11]\n\t"
"v_mfma_f32_16x16x32_bf16 v[8:11], a[84:87], a[184:187], v[8:11]\n\t"
"ds_write_b64 v250, a[160:161] offset:18432\n\t"
"v_mfma_f32_16x16x32_bf16 v[12:15], a[76:79], a[180:183], v[12:15]\n\t"
"s_waitcnt lgkmcnt(5)\n\t"
"ds_read_b128 v[196:199], v226 offset:14336\n\t"
"v_mfma_f32_16x16x32_bf16 v[12:15], a[80:83], a[184:187], v[12:15]\n\t"
"v_mfma_f32_16x16x32_bf16 v[12:15], a[84:87], a[188:191], v[12:15]\n\t"
"buffer_store_dwordx4 a[40:43], v248, s[16:19], s29 offen offset:256 nt\n\t"
"ds_write_b64 v250, a[162:163] offset:24576\n\t"
"s_add_u32 s29, s22, 393600\n\t"
"v_mfma_f32_16x16x32_bf16 v[16:19], a[76:79], a[184:187], v[16:19]\n\t"
"s_waitcnt lgkmcnt(5)\n\t"
"v_mfma_f32_16x16x32_bf16 v[16:19], a[80:83], a[188:191], v[16:19]\n\t"
"v_mfma_f32_16x16x32_bf16 v[16:19], a[84:87], a[192:195], v[16:19]\n\t"
"buffer_store_dwordx4 a[44:47], v248, s[16:19], s29 offen offset:0 nt\n\t"
"ds_read_b128 v[200:203], v226 offset:24576\n\t"
"ds_read_b128 a[4:7], v226 offset:36864\n\t"
"v_mfma_f32_16x16x32_bf16 v[20:23], a[76:79], a[188:191], v[20:23]\n\t"
"s_waitcnt lgkmcnt(5)\n\t"
"ds_read_b128 a[16:19], v226 offset:49152\n\t"
"v_mfma_f32_16x16x32_bf16 v[20:23], a[80:83], a[192:195], v[20:23]\n\t"
"ds_read_b128 a[0:3], v226 offset:25600\n\t"
"v_mfma_f32_16x16x32_bf16 v[20:23], a[84:87], a[196:199], v[20:23]\n\t"
"buffer_store_dwordx4 v[204:207], v248, s[16:19], s29 offen offset:128 nt\n\t"
"ds_read_b128 a[8:11], v226 offset:37888\n\t"
"ds_read_b128 a[20:23], v226 offset:50176\n\t"
"v_mfma_f32_16x16x32_bf16 v[24:27], a[76:79], a[192:195], v[24:27]\n\t"
"ds_read_b128 v[204:207], v226 offset:26624\n\t"
"v_mfma_f32_16x16x32_bf16 v[24:27], a[80:83], a[196:199], v[24:27]\n\t"
"ds_read_b128 a[12:15], v226 offset:38912\n\t"
"v_mfma_f32_16x16x32_bf16 v[24:27], a[84:87], a[200:203], v[24:27]\n\t"
"buffer_store_dwordx4 v[208:211], v248, s[16:19], s29 offen offset:256 nt\n\t"
"ds_read_b128 a[24:27], v226 offset:51200\n\t"
"s_add_u32 s29, s22, 590208\n\t"
"v_mfma_f32_16x16x32_bf16 v[28:31], a[76:79], a[196:199], v[28:31]\n\t"
"s_waitcnt vmcnt(44)\n\t"
"v_mfma_f32_16x16x32_bf16 v[28:31], a[80:83], a[200:203], v[28:31]\n\t"
"v_mfma_f32_16x16x32_bf16 v[28:31], a[84:87], a[204:207], v[28:31]\n\t"
"ds_write_b128 v249, a[36:39] offset:24576\n\t"
"v_mfma_f32_16x16x32_bf16 v[32:35], a[220:223], a[168:171], v[32:35]\n\t"
"s_waitcnt lgkmcnt(11)\n\t"
"v_mfma_f32_16x16x32_bf16 v[32:35], a[216:219], a[172:175], v[32:35]\n\t"
"buffer_load_dwordx4 a[40:43], v252, s[8:11], s20 offen offset:2048 sc1\n\t"
"v_mfma_f32_16x16x32_bf16 v[32:35], a[224:227], a[176:179], v[32:35]\n\t"
"buffer_store_dwordx4 v[212:215], v248, s[16:19], s29 offen offset:0 nt\n\t"
"v_mfma_f32_16x16x32_bf16 v[36:39], a[220:223], a[172:175], v[36:39]\n\t"
"s_mov_b32 s20, 1081344\n\t"
"v_mfma_f32_16x16x32_bf16 v[36:39], a[216:219], a[176:179], v[36:39]\n\t"
"v_mfma_f32_16x16x32_bf16 v[36:39], a[224:227], a[180:183], v[36:39]\n\t"
"buffer_store_dwordx4 v[216:219], v248, s[16:19], s29 offen offset:128 nt\n\t"
"v_mfma_f32_16x16x32_bf16 v[40:43], a[220:223], a[176:179], v[40:43]\n\t"
"v_mfma_f32_16x16x32_bf16 v[40:43], a[216:219], a[180:183], v[40:43]\n\t"
"v_mfma_f32_16x16x32_bf16 v[40:43], a[224:227], a[184:187], v[40:43]\n\t"
"buffer_store_dwordx4 v[196:199], v248, s[16:19], s29 offen offset:256 nt\n\t"
"s_add_u32 s29, s22, 786816\n\t"
"v_mfma_f32_16x16x32_bf16 v[44:47], a[220:223], a[180:183], v[44:47]\n\t"
"s_waitcnt lgkmcnt(9)\n\t"
"v_mfma_f32_16x16x32_bf16 v[44:47], a[216:219], a[184:187], v[44:47]\n\t"
"v_mfma_f32_16x16x32_bf16 v[44:47], a[224:227], a[188:191], v[44:47]\n\t"
"s_waitcnt lgkmcnt(0)\n\t"
"s_barrier\n\t"
"v_mfma_f32_16x16x32_bf16 v[48:51], a[220:223], a[184:187], v[48:51]\n\t"
"v_mfma_f32_16x16x32_bf16 v[48:51], a[216:219], a[188:191], v[48:51]\n\t"
"buffer_load_dwordx4 a[44:47], v252, s[8:11], s20 offen offset:0 sc1\n\t"
"v_mfma_f32_16x16x32_bf16 v[48:51], a[224:227], a[192:195], v[48:51]\n\t"
"buffer_load_dwordx4 a[32:35], v252, s[8:11], s20 offen offset:1024 sc1\n\t"
"v_mfma_f32_16x16x32_bf16 v[52:55], a[220:223], a[188:191], v[52:55]\n\t"
"v_mfma_f32_16x16x32_bf16 v[52:55], a[216:219], a[192:195], v[52:55]\n\t"
"v_mfma_f32_16x16x32_bf16 v[52:55], a[224:227], a[196:199], v[52:55]\n\t"
"buffer_store_dwordx4 v[200:203], v248, s[16:19], s29 offen offset:0 nt\n\t"
"v_mfma_f32_16x16x32_bf16 v[56:59], a[220:223], a[192:195], v[56:59]\n\t"
"v_mfma_f32_16x16x32_bf16 v[56:59], a[216:219], a[196:199], v[56:59]\n\t"
"buffer_load_dwordx4 a[36:39], v252, s[8:11], s20 offen offset:2048 sc1\n\t"
"v_mfma_f32_16x16x32_bf16 v[56:59], a[224:227], a[200:203], v[56:59]\n\t"
"s_mov_b32 s20, 1671168\n\t"
"v_mfma_f32_16x16x32_bf16 v[60:63], a[220:223], a[196:199], v[60:63]\n\t"
"s_waitcnt vmcnt(49)\n\t"
"v_mfma_f32_16x16x32_bf16 v[60:63], a[216:219], a[200:203], v[60:63]\n\t"
"v_mfma_f32_16x16x32_bf16 v[60:63], a[224:227], a[204:207], v[60:63]\n\t"
"buffer_store_dwordx4 a[0:3], v248, s[16:19], s29 offen offset:128 nt\n\t"
"v_mfma_f32_16x16x32_bf16 v[64:67], a[88:91], a[168:171], v[64:67]\n\t"
"ds_read_b128 a[168:171], v244 offset:9216\n\t"
"v_mfma_f32_16x16x32_bf16 v[64:67], a[92:95], a[172:175], v[64:67]\n\t"
"v_mfma_f32_16x16x32_bf16 v[64:67], a[96:99], a[176:179], v[64:67]\n\t"
"buffer_load_dwordx4 a[216:219], v252, s[8:11], s20 offen offset:0 sc1\n\t"
"v_mfma_f32_16x16x32_bf16 v[68:71], a[88:91], a[172:175], v[68:71]\n\t"
"ds_read_b128 a[172:175], v244 offset:9728\n\t"
"v_mfma_f32_16x16x32_bf16 v[68:71], a[92:95], a[176:179], v[68:71]\n\t"
"v_mfma_f32_16x16x32_bf16 v[68:71], a[96:99], a[180:183], v[68:71]\n\t"
"v_mfma_f32_16x16x32_bf16 v[72:75], a[88:91], a[176:179], v[72:75]\n\t"
"ds_read_b128 a[176:179], v244 offset:10240\n\t"
"v_mfma_f32_16x16x32_bf16 v[72:75], a[92:95], a[180:183], v[72:75]\n\t"
"v_mfma_f32_16x16x32_bf16 v[72:75], a[96:99], a[184:187], v[72:75]\n\t"
"buffer_store_dwordx4 v[204:207], v248, s[16:19], s29 offen offset:256 nt\n\t"
"ds_read_b128 v[196:199], v252 offset:18432\n\t"
"v_mfma_f32_16x16x32_bf16 v[76:79], a[88:91], a[180:183], v[76:79]\n\t"
"s_add_u32 s29, s22, 983424\n\t"
"ds_read_b128 a[180:183], v244 offset:10752\n\t"
"v_mfma_f32_16x16x32_bf16 v[76:79], a[92:95], a[184:187], v[76:79]\n\t"
"buffer_load_dwordx4 a[220:223], v252, s[8:11], s20 offen offset:1024 sc1\n\t"
"v_mfma_f32_16x16x32_bf16 v[76:79], a[96:99], a[188:191], v[76:79]\n\t"
"ds_read_b128 v[200:203], v252 offset:19456\n\t"
"v_mfma_f32_16x16x32_bf16 v[80:83], a[88:91], a[184:187], v[80:83]\n\t"
"ds_read_b128 a[184:187], v244 offset:11264\n\t"
"v_mfma_f32_16x16x32_bf16 v[80:83], a[92:95], a[188:191], v[80:83]\n\t"
"buffer_load_dwordx4 a[224:227], v252, s[8:11], s20 offen offset:2048 sc1\n\t"
"v_mfma_f32_16x16x32_bf16 v[80:83], a[96:99], a[192:195], v[80:83]\n\t"
"ds_read_b128 v[208:211], v252 offset:20480\n\t"
"v_mfma_f32_16x16x32_bf16 v[84:87], a[88:91], a[188:191], v[84:87]\n\t"
"s_mov_b32 s20, 1093632\n\t"
"ds_read_b128 a[188:191], v244 offset:11776\n\t"
"v_mfma_f32_16x16x32_bf16 v[84:87], a[92:95], a[192:195], v[84:87]\n\t"
"v_mfma_f32_16x16x32_bf16 v[84:87], a[96:99], a[196:199], v[84:87]\n\t"
"buffer_store_dwordx4 a[4:7], v248, s[16:19], s29 offen offset:0 nt\n\t"
"v_mfma_f32_16x16x32_bf16 v[88:91], a[88:91], a[192:195], v[88:91]\n\t"
"ds_read_b128 a[192:195], v244 offset:12288\n\t"
"v_mfma_f32_16x16x32_bf16 v[88:91], a[92:95], a[196:199], v[88:91]\n\t"
"v_mfma_f32_16x16x32_bf16 v[88:91], a[96:99], a[200:203], v[88:91]\n\t"
"v_mfma_f32_16x16x32_bf16 v[92:95], a[88:91], a[196:199], v[92:95]\n\t"
"buffer_load_dwordx4 a[76:79], v252, s[8:11], s20 offen offset:0 sc1\n\t"
"v_mfma_f32_16x16x32_bf16 v[92:95], a[92:95], a[200:203], v[92:95]\n\t"
"s_waitcnt vmcnt(53) lgkmcnt(7)\n\t"
"ds_read_b128 a[196:199], v244 offset:12800\n\t"
"v_mfma_f32_16x16x32_bf16 v[92:95], a[96:99], a[204:207], v[92:95]\n\t"
"ds_read_b128 a[200:203], v244 offset:13312\n\t"
"v_mfma_f32_16x16x32_bf16 v[100:103], a[100:103], a[168:171], v[100:103]\n\t"
"s_waitcnt lgkmcnt(7)\n\t"
"ds_read_b128 a[204:207], v244 offset:13824\n\t"
"v_mfma_f32_16x16x32_bf16 v[100:103], a[108:111], a[172:175], v[100:103]\n\t"
"v_mfma_f32_16x16x32_bf16 v[100:103], a[228:231], a[176:179], v[100:103]\n\t"
"buffer_store_dwordx4 a[8:11], v248, s[16:19], s29 offen offset:128 nt\n\t"
"v_mfma_f32_16x16x32_bf16 v[104:107], a[100:103], a[172:175], v[104:107]\n\t"
"s_waitcnt lgkmcnt(6)\n\t"
"v_mfma_f32_16x16x32_bf16 v[104:107], a[108:111], a[176:179], v[104:107]\n\t"
"v_mfma_f32_16x16x32_bf16 v[104:107], a[228:231], a[180:183], v[104:107]\n\t"
"s_waitcnt lgkmcnt(0)\n\t"
"s_barrier\n\t"
"v_mfma_f32_16x16x32_bf16 v[108:111], a[100:103], a[176:179], v[108:111]\n\t"
"v_mfma_f32_16x16x32_bf16 v[108:111], a[108:111], a[180:183], v[108:111]\n\t"
"buffer_load_dwordx4 a[4:7], v252, s[8:11], s20 offen offset:1024 sc1\n\t"
"v_mfma_f32_16x16x32_bf16 v[108:111], a[228:231], a[184:187], v[108:111]\n\t"
"buffer_load_dwordx4 a[80:83], v252, s[8:11], s20 offen offset:2048 sc1\n\t"
"v_mfma_f32_16x16x32_bf16 v[112:115], a[100:103], a[180:183], v[112:115]\n\t"
"s_mov_b32 s20, 1683456\n\t"
"v_mfma_f32_16x16x32_bf16 v[112:115], a[108:111], a[184:187], v[112:115]\n\t"
"v_mfma_f32_16x16x32_bf16 v[112:115], a[228:231], a[188:191], v[112:115]\n\t"
"buffer_store_dwordx4 a[12:15], v248, s[16:19], s29 offen offset:256 nt\n\t"
"v_mfma_f32_16x16x32_bf16 v[116:119], a[100:103], a[184:187], v[116:119]\n\t"
"s_add_u32 s29, s22, 1180032\n\t"
"v_mfma_f32_16x16x32_bf16 v[116:119], a[108:111], a[188:191], v[116:119]\n\t"
"v_mfma_f32_16x16x32_bf16 v[116:119], a[228:231], a[192:195], v[116:119]\n\t"
"buffer_load_dwordx4 a[8:11], v252, s[8:11], s20 offen offset:0 sc1\n\t"
"v_mfma_f32_16x16x32_bf16 v[120:123], a[100:103], a[188:191], v[120:123]\n\t"
"v_mfma_f32_16x16x32_bf16 v[120:123], a[108:111], a[192:195], v[120:123]\n\t"
"v_mfma_f32_16x16x32_bf16 v[120:123], a[228:231], a[196:199], v[120:123]\n\t"
"v_mfma_f32_16x16x32_bf16 v[124:127], a[100:103], a[192:195], v[124:127]\n\t"
"v_mfma_f32_16x16x32_bf16 v[124:127], a[108:111], a[196:199], v[124:127]\n\t"
"v_mfma_f32_16x16x32_bf16 v[124:127], a[228:231], a[200:203], v[124:127]\n\t"
"buffer_store_dwordx4 a[16:19], v248, s[16:19], s29 offen offset:0 nt\n\t"
"v_mfma_f32_16x16x32_bf16 v[128:131], a[100:103], a[196:199], v[128:131]\n\t"
"s_waitcnt vmcnt(56)\n\t"
"v_mfma_f32_16x16x32_bf16 v[128:131], a[108:111], a[200:203], v[128:131]\n\t"
"buffer_load_dwordx4 a[12:15], v252, s[8:11], s20 offen offset:1024 sc1\n\t"
"v_mfma_f32_16x16x32_bf16 v[128:131], a[228:231], a[204:207], v[128:131]\n\t"
"v_mfma_f32_16x16x32_bf16 v[132:135], a[208:211], a[168:171], v[132:135]\n\t"
"v_mfma_f32_16x16x32_bf16 v[132:135], a[232:235], a[172:175], v[132:135]\n\t"
"buffer_load_dwordx4 a[228:231], v252, s[8:11], s20 offen offset:2048 sc1\n\t"
"v_mfma_f32_16x16x32_bf16 v[132:135], a[116:119], a[176:179], v[132:135]\n\t"
"v_mfma_f32_16x16x32_bf16 v[136:139], a[208:211], a[172:175], v[136:139]\n\t"
"v_mfma_f32_16x16x32_bf16 v[136:139], a[232:235], a[176:179], v[136:139]\n\t"
"v_subrev_u32 v255, 55296, v255\n\t"
"v_mfma_f32_16x16x32_bf16 v[136:139], a[116:119], a[180:183], v[136:139]\n\t"
"buffer_store_dwordx4 a[20:23], v248, s[16:19], s29 offen offset:128 nt\n\t"
"v_mfma_f32_16x16x32_bf16 v[140:143], a[208:211], a[176:179], v[140:143]\n\t"
"v_mfma_f32_16x16x32_bf16 v[140:143], a[232:235], a[180:183], v[140:143]\n\t"
"v_mfma_f32_16x16x32_bf16 v[140:143], a[116:119], a[184:187], v[140:143]\n\t"
"v_mfma_f32_16x16x32_bf16 v[144:147], a[208:211], a[180:183], v[144:147]\n\t"
"v_mfma_f32_16x16x32_bf16 v[144:147], a[232:235], a[184:187], v[144:147]\n\t"
"v_mfma_f32_16x16x32_bf16 v[144:147], a[116:119], a[188:191], v[144:147]\n\t"
"v_mfma_f32_16x16x32_bf16 v[148:151], a[208:211], a[184:187], v[148:151]\n\t"
"v_mfma_f32_16x16x32_bf16 v[148:151], a[232:235], a[188:191], v[148:151]\n\t"
"v_mfma_f32_16x16x32_bf16 v[148:151], a[116:119], a[192:195], v[148:151]\n\t"
"buffer_store_dwordx4 a[24:27], v248, s[16:19], s29 offen offset:256 nt\n\t"
"v_mfma_f32_16x16x32_bf16 v[152:155], a[208:211], a[188:191], v[152:155]\n\t"
"s_add_u32 s29, s22, 1376640\n\t"
"v_mfma_f32_16x16x32_bf16 v[152:155], a[232:235], a[192:195], v[152:155]\n\t"
"v_mfma_f32_16x16x32_bf16 v[152:155], a[116:119], a[196:199], v[152:155]\n\t"
"v_mfma_f32_16x16x32_bf16 v[156:159], a[208:211], a[192:195], v[156:159]\n\t"
"v_mfma_f32_16x16x32_bf16 v[156:159], a[232:235], a[196:199], v[156:159]\n\t"
"v_mfma_f32_16x16x32_bf16 v[156:159], a[116:119], a[200:203], v[156:159]\n\t"
"v_mfma_f32_16x16x32_bf16 v[160:163], a[208:211], a[196:199], v[160:163]\n\t"
"s_waitcnt vmcnt(57)\n\t"
"v_mfma_f32_16x16x32_bf16 v[160:163], a[232:235], a[200:203], v[160:163]\n\t"
"v_mfma_f32_16x16x32_bf16 v[160:163], a[116:119], a[204:207], v[160:163]\n\t"
"buffer_store_dwordx4 v[196:199], v248, s[16:19], s29 offen offset:0 nt\n\t"
"v_mfma_f32_16x16x32_bf16 v[164:167], a[124:127], a[168:171], v[164:167]\n\t"
"ds_read_b128 a[168:171], v255 offset:512\n\t"
"v_mfma_f32_16x16x32_bf16 v[164:167], a[132:135], a[172:175], v[164:167]\n\t"
"ds_read_b128 v[204:207], v255 offset:3584\n\t"
"v_mfma_f32_16x16x32_bf16 v[164:167], a[140:143], a[176:179], v[164:167]\n\t"
"ds_read_b128 v[212:215], v255 offset:6656\n\t"
"v_mfma_f32_16x16x32_bf16 v[168:171], a[124:127], a[172:175], v[168:171]\n\t"
"ds_read_b128 a[172:175], v255 offset:1024\n\t"
"v_mfma_f32_16x16x32_bf16 v[168:171], a[132:135], a[176:179], v[168:171]\n\t"
"ds_read_b128 v[216:219], v255 offset:4096\n\t"
"v_mfma_f32_16x16x32_bf16 v[168:171], a[140:143], a[180:183], v[168:171]\n\t"
"ds_read_b128 a[0:3], v255 offset:7168\n\t"
"v_mfma_f32_16x16x32_bf16 v[172:175], a[124:127], a[176:179], v[172:175]\n\t"
"v_mfma_f32_16x16x32_bf16 v[172:175], a[132:135], a[180:183], v[172:175]\n\t"
"v_mfma_f32_16x16x32_bf16 v[172:175], a[140:143], a[184:187], v[172:175]\n\t"
"buffer_store_dwordx4 v[200:203], v248, s[16:19], s29 offen offset:128 nt\n\t"
"v_mfma_f32_16x16x32_bf16 v[176:179], a[124:127], a[180:183], v[176:179]\n\t"
"v_mfma_f32_16x16x32_bf16 v[176:179], a[132:135], a[184:187], v[176:179]\n\t"
"v_mfma_f32_16x16x32_bf16 v[176:179], a[140:143], a[188:191], v[176:179]\n\t"
"s_barrier\n\t"
"v_mfma_f32_16x16x32_bf16 v[180:183], a[124:127], a[184:187], v[180:183]\n\t"
"v_mfma_f32_16x16x32_bf16 v[180:183], a[132:135], a[188:191], v[180:183]\n\t"
"v_mfma_f32_16x16x32_bf16 v[180:183], a[140:143], a[192:195], v[180:183]\n\t"
"v_mfma_f32_16x16x32_bf16 v[184:187], a[124:127], a[188:191], v[184:187]\n\t"
"v_mfma_f32_16x16x32_bf16 v[184:187], a[132:135], a[192:195], v[184:187]\n\t"
"v_mfma_f32_16x16x32_bf16 v[184:187], a[140:143], a[196:199], v[184:187]\n\t"
"buffer_store_dwordx4 v[208:211], v248, s[16:19], s29 offen offset:256 nt\n\t"
"v_mfma_f32_16x16x32_bf16 v[188:191], a[124:127], a[192:195], v[188:191]\n\t"
"ds_read_b128 a[176:179], v255 offset:2048\n\t"
"v_mfma_f32_16x16x32_bf16 v[188:191], a[132:135], a[196:199], v[188:191]\n\t"
"ds_read_b128 a[180:183], v255 offset:5120\n\t"
"v_mfma_f32_16x16x32_bf16 v[188:191], a[140:143], a[200:203], v[188:191]\n\t"
"ds_read_b128 a[184:187], v255 offset:8192\n\t"
"v_mfma_f32_16x16x32_bf16 v[192:195], a[124:127], a[196:199], v[192:195]\n\t"
"s_waitcnt vmcnt(57) lgkmcnt(6)\n\t"
"ds_read_b128 a[188:191], v255 offset:2560\n\t"
"v_mfma_f32_16x16x32_bf16 v[192:195], a[132:135], a[200:203], v[192:195]\n\t"
"ds_read_b128 a[192:195], v255 offset:5632\n\t"
"v_mfma_f32_16x16x32_bf16 v[192:195], a[140:143], a[204:207], v[192:195]\n\t"
"ds_read_b128 a[196:199], v255 offset:8704\n\t"
"Lst34_%=:\n\t"
"v_mfma_f32_16x16x32_bf16 v[0:3], a[148:151], a[168:171], v[0:3]\n\t"
"s_waitcnt vmcnt(54) lgkmcnt(6)\n\t"
"v_mfma_f32_16x16x32_bf16 v[0:3], a[236:239], v[204:207], v[0:3]\n\t"
"v_mfma_f32_16x16x32_bf16 v[0:3], a[240:243], v[212:215], v[0:3]\n\t"
"v_mfma_f32_16x16x32_bf16 v[0:3], a[244:247], a[172:175], v[0:3]\n\t"
"s_waitcnt vmcnt(51)\n\t"
"v_mfma_f32_16x16x32_bf16 v[0:3], a[104:107], v[216:219], v[0:3]\n\t"
"v_mfma_f32_16x16x32_bf16 v[0:3], a[156:159], a[0:3], v[0:3]\n\t"
"v_mfma_f32_16x16x32_bf16 v[32:35], a[164:167], a[168:171], v[32:35]\n\t"
"s_waitcnt vmcnt(47)\n\t"
"v_mfma_f32_16x16x32_bf16 v[32:35], v[240:243], v[204:207], v[32:35]\n\t"
"v_mfma_f32_16x16x32_bf16 v[32:35], v[96:99], v[212:215], v[32:35]\n\t"
"v_mfma_f32_16x16x32_bf16 v[32:35], a[248:251], a[172:175], v[32:35]\n\t"
"s_waitcnt vmcnt(42)\n\t"
"v_cvt_pk_bf16_f32 v0, v0, v1\n\t"
"v_cvt_pk_bf16_f32 v1, v2, v3\n\t"
"v_mfma_f32_16x16x32_bf16 v[32:35], a[52:55], v[216:219], v[32:35]\n\t"
"v_mfma_f32_16x16x32_bf16 v[32:35], a[56:59], a[0:3], v[32:35]\n\t"
"v_mfma_f32_16x16x32_bf16 v[64:67], a[48:51], a[168:171], v[64:67]\n\t"
"s_waitcnt vmcnt(39)\n\t"
"ds_read_b128 a[168:171], v255 offset:9728\n\t"
"v_mfma_f32_16x16x32_bf16 v[64:67], v[236:239], v[204:207], v[64:67]\n\t"
"v_mfma_f32_16x16x32_bf16 v[64:67], a[60:63], v[212:215], v[64:67]\n\t"
"s_barrier\n\t"
"s_cmp_lt_u32 s69, 216\n\t"
"s_cbranch_scc0 Lna2_%=\n\t"
"s_add_u32 s71, s71, 1\n\t"
"v_mov_b32 v228, s71\n\t"
"s_mov_b64 exec, 1\n\t"
"buffer_store_dword v228, off, s[8:11], s70 sc1\n\t"
"s_mov_b64 exec, -1\n\t"
"Lna2_%=:\n\t"
"v_mfma_f32_16x16x32_bf16 v[64:67], a[212:215], a[172:175], v[64:67]\n\t"
"s_waitcnt vmcnt(20) lgkmcnt(1)\n\t"
"ds_read_b128 a[172:175], v255 offset:10240\n\t"
"v_mfma_f32_16x16x32_bf16 v[64:67], a[252:255], v[216:219], v[64:67]\n\t"
"Lpg25_%=:\n\t"
"s_cbranch_vccnz Lvr4_%=\n\t"
"v_cvt_pk_bf16_f32 v2, v32, v33\n\t"
"v_cvt_pk_bf16_f32 v3, v34, v35\n\t"
"v_mfma_f32_16x16x32_bf16 v[64:67], a[28:31], a[0:3], v[64:67]\n\t"
"ds_write_b128 v249, v[0:3] offset:18432\n\t"
"v_mfma_f32_16x16x32_bf16 v[100:103], a[112:115], a[176:179], v[100:103]\n\t"
"v_mfma_f32_16x16x32_bf16 v[100:103], a[120:123], a[180:183], v[100:103]\n\t"
"v_mfma_f32_16x16x32_bf16 v[100:103], a[64:67], a[184:187], v[100:103]\n\t"
"v_mfma_f32_16x16x32_bf16 v[100:103], a[68:71], a[188:191], v[100:103]\n\t"
"v_cvt_pk_bf16_f32 v64, v64, v65\n\t"
"v_cvt_pk_bf16_f32 v65, v66, v67\n\t"
"v_mfma_f32_16x16x32_bf16 v[100:103], a[72:75], a[192:195], v[100:103]\n\t"
"v_mfma_f32_16x16x32_bf16 v[100:103], a[40:43], a[196:199], v[100:103]\n\t"
"ds_write_b64 v250, v[64:65] offset:18432\n\t"
"v_mfma_f32_16x16x32_bf16 v[132:135], a[44:47], a[176:179], v[132:135]\n\t"
"s_waitcnt vmcnt(15)\n\t"
"v_mfma_f32_16x16x32_bf16 v[132:135], a[32:35], a[180:183], v[132:135]\n\t"
"v_mfma_f32_16x16x32_bf16 v[132:135], a[36:39], a[184:187], v[132:135]\n\t"
"v_mfma_f32_16x16x32_bf16 v[132:135], a[216:219], a[188:191], v[132:135]\n\t"
"s_waitcnt vmcnt(10)\n\t"
"v_cvt_pk_bf16_f32 v100, v100, v101\n\t"
"v_cvt_pk_bf16_f32 v101, v102, v103\n\t"
"v_mfma_f32_16x16x32_bf16 v[132:135], a[220:223], a[192:195], v[132:135]\n\t"
"v_mfma_f32_16x16x32_bf16 v[132:135], a[224:227], a[196:199], v[132:135]\n\t"
"v_mfma_f32_16x16x32_bf16 v[164:167], a[76:79], a[176:179], v[164:167]\n\t"
"s_waitcnt vmcnt(5)\n\t"
"ds_read_b128 a[176:179], v255 offset:11264\n\t"
"v_mfma_f32_16x16x32_bf16 v[164:167], a[4:7], a[180:183], v[164:167]\n\t"
"v_mfma_f32_16x16x32_bf16 v[164:167], a[80:83], a[184:187], v[164:167]\n\t"
"v_mfma_f32_16x16x32_bf16 v[164:167], a[8:11], a[188:191], v[164:167]\n\t"
"s_waitcnt lgkmcnt(4)\n\t"
"ds_read_b128 a[188:191], v255 offset:11776\n\t"
"v_cvt_pk_bf16_f32 v102, v132, v133\n\t"
"v_mfma_f32_16x16x32_bf16 v[164:167], a[12:15], a[192:195], v[164:167]\n\t"
"v_cvt_pk_bf16_f32 v103, v134, v135\n\t"
"v_mfma_f32_16x16x32_bf16 v[164:167], a[228:231], a[196:199], v[164:167]\n\t"
"ds_write_b128 v249, v[100:103] offset:24576\n\t"
"v_mfma_f32_16x16x32_bf16 v[4:7], a[148:151], v[204:207], v[4:7]\n\t"
"s_waitcnt lgkmcnt(5)\n\t"
"v_mfma_f32_16x16x32_bf16 v[4:7], a[236:239], v[212:215], v[4:7]\n\t"
"v_mfma_f32_16x16x32_bf16 v[4:7], a[240:243], a[168:171], v[4:7]\n\t"
"v_mfma_f32_16x16x32_bf16 v[4:7], a[244:247], v[216:219], v[4:7]\n\t"
"v_cvt_pk_bf16_f32 v164, v164, v165\n\t"
"v_cvt_pk_bf16_f32 v165, v166, v167\n\t"
"v_mfma_f32_16x16x32_bf16 v[4:7], a[104:107], a[0:3], v[4:7]\n\t"
"v_mfma_f32_16x16x32_bf16 v[4:7], a[156:159], a[172:175], v[4:7]\n\t"
"ds_write_b64 v250, v[164:165] offset:24576\n\t"
"v_mfma_f32_16x16x32_bf16 v[36:39], a[164:167], v[204:207], v[36:39]\n\t"
"v_mfma_f32_16x16x32_bf16 v[36:39], v[240:243], v[212:215], v[36:39]\n\t"
"v_mfma_f32_16x16x32_bf16 v[36:39], v[96:99], a[168:171], v[36:39]\n\t"
"v_mfma_f32_16x16x32_bf16 v[36:39], a[248:251], v[216:219], v[36:39]\n\t"
"v_cvt_pk_bf16_f32 v4, v4, v5\n\t"
"v_cvt_pk_bf16_f32 v5, v6, v7\n\t"
"v_mfma_f32_16x16x32_bf16 v[36:39], a[52:55], a[0:3], v[36:39]\n\t"
"v_mfma_f32_16x16x32_bf16 v[36:39], a[56:59], a[172:175], v[36:39]\n\t"
"v_mfma_f32_16x16x32_bf16 v[68:71], a[48:51], v[204:207], v[68:71]\n\t"
"ds_read_b128 a[200:203], v255 offset:12800\n\t"
"v_mfma_f32_16x16x32_bf16 v[68:71], v[236:239], v[212:215], v[68:71]\n\t"
"v_mfma_f32_16x16x32_bf16 v[68:71], a[60:63], a[168:171], v[68:71]\n\t"
"s_waitcnt lgkmcnt(1)\n\t"
"s_barrier\n\t"
"v_mfma_f32_16x16x32_bf16 v[68:71], a[212:215], v[216:219], v[68:71]\n\t"
"ds_read_b128 v[196:199], v252 offset:18432\n\t"
"v_mfma_f32_16x16x32_bf16 v[68:71], a[252:255], a[0:3], v[68:71]\n\t"
"ds_read_b128 v[200:203], v252 offset:19456\n\t"
"v_cvt_pk_bf16_f32 v6, v36, v37\n\t"
"v_mfma_f32_16x16x32_bf16 v[68:71], a[28:31], a[172:175], v[68:71]\n\t"
"ds_read_b128 v[204:207], v252 offset:20480\n\t"
"v_cvt_pk_bf16_f32 v7, v38, v39\n\t"
"v_mfma_f32_16x16x32_bf16 v[104:107], a[112:115], a[180:183], v[104:107]\n\t"
"s_add_u32 s29, s23, 0\n\t"
"ds_read_b128 a[204:207], v255 offset:13312\n\t"
"v_mfma_f32_16x16x32_bf16 v[104:107], a[120:123], a[184:187], v[104:107]\n\t"
"v_mfma_f32_16x16x32_bf16 v[104:107], a[64:67], a[176:179], v[104:107]\n\t"
"ds_write_b128 v249, v[4:7] offset:30720\n\t"
"v_mfma_f32_16x16x32_bf16 v[104:107], a[68:71], a[192:195], v[104:107]\n\t"
"s_waitcnt lgkmcnt(4)\n\t"
"v_cvt_pk_bf16_f32 v68, v68, v69\n\t"
"v_cvt_pk_bf16_f32 v69, v70, v71\n\t"
"v_mfma_f32_16x16x32_bf16 v[104:107], a[72:75], a[196:199], v[104:107]\n\t"
"v_mfma_f32_16x16x32_bf16 v[104:107], a[40:43], a[188:191], v[104:107]\n\t"
"ds_write_b64 v250, v[68:69] offset:30720\n\t"
"v_mfma_f32_16x16x32_bf16 v[136:139], a[44:47], a[180:183], v[136:139]\n\t"
"s_waitcnt lgkmcnt(4)\n\t"
"v_mfma_f32_16x16x32_bf16 v[136:139], a[32:35], a[184:187], v[136:139]\n\t"
"v_mfma_f32_16x16x32_bf16 v[136:139], a[36:39], a[176:179], v[136:139]\n\t"
"buffer_store_dwordx4 v[196:199], v248, s[16:19], s29 offen offset:0 sc1\n\t"
"v_mfma_f32_16x16x32_bf16 v[136:139], a[216:219], a[192:195], v[136:139]\n\t"
"s_waitcnt lgkmcnt(3)\n\t"
"v_cvt_pk_bf16_f32 v104, v104, v105\n\t"
"v_cvt_pk_bf16_f32 v105, v106, v107\n\t"
"v_mfma_f32_16x16x32_bf16 v[136:139], a[220:223], a[196:199], v[136:139]\n\t"
"v_mfma_f32_16x16x32_bf16 v[136:139], a[224:227], a[188:191], v[136:139]\n\t"
"buffer_store_dwordx4 v[200:203], v248, s[16:19], s29 offen offset:128 sc1\n\t"
"v_mfma_f32_16x16x32_bf16 v[168:171], a[76:79], a[180:183], v[168:171]\n\t"
"ds_read_b128 a[180:183], v255 offset:14336\n\t"
"v_mfma_f32_16x16x32_bf16 v[168:171], a[4:7], a[184:187], v[168:171]\n\t"
"v_mfma_f32_16x16x32_bf16 v[168:171], a[80:83], a[176:179], v[168:171]\n\t"
"buffer_store_dwordx4 v[204:207], v248, s[16:19], s29 offen offset:256 sc1\n\t"
"v_mfma_f32_16x16x32_bf16 v[168:171], a[8:11], a[192:195], v[168:171]\n\t"
"ds_read_b128 a[192:195], v255 offset:14848\n\t"
"v_cvt_pk_bf16_f32 v106, v136, v137\n\t"
"v_mfma_f32_16x16x32_bf16 v[168:171], a[12:15], a[196:199], v[168:171]\n\t"
"v_cvt_pk_bf16_f32 v107, v138, v139\n\t"
"v_mfma_f32_16x16x32_bf16 v[168:171], a[228:231], a[188:191], v[168:171]\n\t"
"ds_write_b128 v249, v[104:107] offset:36864\n\t"
"v_mfma_f32_16x16x32_bf16 v[8:11], a[148:151], v[212:215], v[8:11]\n\t"
"s_waitcnt lgkmcnt(5)\n\t"
"v_mfma_f32_16x16x32_bf16 v[8:11], a[236:239], a[168:171], v[8:11]\n\t"
"v_mfma_f32_16x16x32_bf16 v[8:11], a[240:243], a[200:203], v[8:11]\n\t"
"v_mfma_f32_16x16x32_bf16 v[8:11], a[244:247], a[0:3], v[8:11]\n\t"
"v_cvt_pk_bf16_f32 v168, v168, v169\n\t"
"v_cvt_pk_bf16_f32 v169, v170, v171\n\t"
"v_mfma_f32_16x16x32_bf16 v[8:11], a[104:107], a[172:175], v[8:11]\n\t"
"v_mfma_f32_16x16x32_bf16 v[8:11], a[156:159], a[204:207], v[8:11]\n\t"
"ds_write_b64 v250, v[168:169] offset:36864\n\t"
"v_mfma_f32_16x16x32_bf16 v[40:43], a[164:167], v[212:215], v[40:43]\n\t"
"v_mfma_f32_16x16x32_bf16 v[40:43], v[240:243], a[168:171], v[40:43]\n\t"
"v_mfma_f32_16x16x32_bf16 v[40:43], v[96:99], a[200:203], v[40:43]\n\t"
"v_mfma_f32_16x16x32_bf16 v[40:43], a[248:251], a[0:3], v[40:43]\n\t"
"v_cvt_pk_bf16_f32 v8, v8, v9\n\t"
"v_cvt_pk_bf16_f32 v9, v10, v11\n\t"
"v_mfma_f32_16x16x32_bf16 v[40:43], a[52:55], a[172:175], v[40:43]\n\t"
"v_mfma_f32_16x16x32_bf16 v[40:43], a[56:59], a[204:207], v[40:43]\n\t"
"v_mfma_f32_16x16x32_bf16 v[72:75], a[48:51], v[212:215], v[72:75]\n\t"
"ds_read_b128 v[208:211], v255 offset:15872\n\t"
"v_mfma_f32_16x16x32_bf16 v[72:75], v[236:239], a[168:171], v[72:75]\n\t"
"v_mfma_f32_16x16x32_bf16 v[72:75], a[60:63], a[200:203], v[72:75]\n\t"
"s_waitcnt lgkmcnt(1)\n\t"
"s_barrier\n\t"
"v_mfma_f32_16x16x32_bf16 v[72:75], a[212:215], a[0:3], v[72:75]\n\t"
"ds_read_b128 v[196:199], v252 offset:30720\n\t"
"v_mfma_f32_16x16x32_bf16 v[72:75], a[252:255], a[172:175], v[72:75]\n\t"
"ds_read_b128 v[200:203], v252 offset:31744\n\t"
"v_cvt_pk_bf16_f32 v10, v40, v41\n\t"
"v_mfma_f32_16x16x32_bf16 v[72:75], a[28:31], a[204:207], v[72:75]\n\t"
"ds_read_b128 v[204:207], v252 offset:32768\n\t"
"v_cvt_pk_bf16_f32 v11, v42, v43\n\t"
"v_mfma_f32_16x16x32_bf16 v[108:111], a[112:115], a[184:187], v[108:111]\n\t"
"s_add_u32 s29, s23, 196608\n\t"
"ds_read_b128 v[212:215], v255 offset:16384\n\t"
"v_mfma_f32_16x16x32_bf16 v[108:111], a[120:123], a[176:179], v[108:111]\n\t"
"v_mfma_f32_16x16x32_bf16 v[108:111], a[64:67], a[180:183], v[108:111]\n\t"
"ds_write_b128 v249, v[8:11] offset:18432\n\t"
"v_mfma_f32_16x16x32_bf16 v[108:111], a[68:71], a[196:199], v[108:111]\n\t"
"s_waitcnt lgkmcnt(4)\n\t"
"v_cvt_pk_bf16_f32 v72, v72, v73\n\t"
"v_cvt_pk_bf16_f32 v73, v74, v75\n\t"
"v_mfma_f32_16x16x32_bf16 v[108:111], a[72:75], a[188:191], v[108:111]\n\t"
"v_mfma_f32_16x16x32_bf16 v[108:111], a[40:43], a[192:195], v[108:111]\n\t"
"ds_write_b64 v250, v[72:73] offset:18432\n\t"
"v_mfma_f32_16x16x32_bf16 v[140:143], a[44:47], a[184:187], v[140:143]\n\t"
"s_waitcnt lgkmcnt(4)\n\t"
"v_mfma_f32_16x16x32_bf16 v[140:143], a[32:35], a[176:179], v[140:143]\n\t"
"v_mfma_f32_16x16x32_bf16 v[140:143], a[36:39], a[180:183], v[140:143]\n\t"
"buffer_store_dwordx4 v[196:199], v248, s[16:19], s29 offen offset:0 sc1\n\t"
"v_mfma_f32_16x16x32_bf16 v[140:143], a[216:219], a[196:199], v[140:143]\n\t"
"s_waitcnt lgkmcnt(3)\n\t"
"v_cvt_pk_bf16_f32 v108, v108, v109\n\t"
"v_cvt_pk_bf16_f32 v109, v110, v111\n\t"
"v_mfma_f32_16x16x32_bf16 v[140:143], a[220:223], a[188:191], v[140:143]\n\t"
"v_mfma_f32_16x16x32_bf16 v[140:143], a[224:227], a[192:195], v[140:143]\n\t"
"buffer_store_dwordx4 v[200:203], v248, s[16:19], s29 offen offset:128 sc1\n\t"
"v_mfma_f32_16x16x32_bf16 v[172:175], a[76:79], a[184:187], v[172:175]\n\t"
"ds_read_b128 a[184:187], v255 offset:17408\n\t"
"v_mfma_f32_16x16x32_bf16 v[172:175], a[4:7], a[176:179], v[172:175]\n\t"
"v_mfma_f32_16x16x32_bf16 v[172:175], a[80:83], a[180:183], v[172:175]\n\t"
"buffer_store_dwordx4 v[204:207], v248, s[16:19], s29 offen offset:256 sc1\n\t"
"v_mfma_f32_16x16x32_bf16 v[172:175], a[8:11], a[196:199], v[172:175]\n\t"
"ds_read_b128 a[196:199], v255 offset:17920\n\t"
"v_cvt_pk_bf16_f32 v110, v140, v141\n\t"
"v_mfma_f32_16x16x32_bf16 v[172:175], a[12:15], a[188:191], v[172:175]\n\t"
"v_cvt_pk_bf16_f32 v111, v142, v143\n\t"
"v_mfma_f32_16x16x32_bf16 v[172:175], a[228:231], a[192:195], v[172:175]\n\t"
"ds_write_b128 v249, v[108:111] offset:24576\n\t"
"v_mfma_f32_16x16x32_bf16 v[12:15], a[148:151], a[168:171], v[12:15]\n\t"
"s_waitcnt lgkmcnt(5)\n\t"
"v_mfma_f32_16x16x32_bf16 v[12:15], a[236:239], a[200:203], v[12:15]\n\t"
"v_mfma_f32_16x16x32_bf16 v[12:15], a[240:243], v[208:211], v[12:15]\n\t"
"v_mfma_f32_16x16x32_bf16 v[12:15], a[244:247], a[172:175], v[12:15]\n\t"
"v_cvt_pk_bf16_f32 v172, v172, v173\n\t"
"v_cvt_pk_bf16_f32 v173, v174, v175\n\t"
"v_mfma_f32_16x16x32_bf16 v[12:15], a[104:107], a[204:207], v[12:15]\n\t"
"v_mfma_f32_16x16x32_bf16 v[12:15], a[156:159], v[212:215], v[12:15]\n\t"
"ds_write_b64 v250, v[172:173] offset:24576\n\t"
"v_mfma_f32_16x16x32_bf16 v[44:47], a[164:167], a[168:171], v[44:47]\n\t"
"v_mfma_f32_16x16x32_bf16 v[44:47], v[240:243], a[200:203], v[44:47]\n\t"
"v_mfma_f32_16x16x32_bf16 v[44:47], v[96:99], v[208:211], v[44:47]\n\t"
"v_mfma_f32_16x16x32_bf16 v[44:47], a[248:251], a[172:175], v[44:47]\n\t"
"v_cvt_pk_bf16_f32 v12, v12, v13\n\t"
"v_cvt_pk_bf16_f32 v13, v14, v15\n\t"
"v_mfma_f32_16x16x32_bf16 v[44:47], a[52:55], a[204:207], v[44:47]\n\t"
"v_mfma_f32_16x16x32_bf16 v[44:47], a[56:59], v[212:215], v[44:47]\n\t"
"v_mfma_f32_16x16x32_bf16 v[76:79], a[48:51], a[168:171], v[76:79]\n\t"
"ds_read_b128 a[168:171], v255 offset:18944\n\t"
"v_mfma_f32_16x16x32_bf16 v[76:79], v[236:239], a[200:203], v[76:79]\n\t"
"v_mfma_f32_16x16x32_bf16 v[76:79], a[60:63], v[208:211], v[76:79]\n\t"
"s_waitcnt lgkmcnt(1)\n\t"
"s_barrier\n\t"
"v_mfma_f32_16x16x32_bf16 v[76:79], a[212:215], a[172:175], v[76:79]\n\t"
"ds_read_b128 v[196:199], v252 offset:18432\n\t"
"v_mfma_f32_16x16x32_bf16 v[76:79], a[252:255], a[204:207], v[76:79]\n\t"
"ds_read_b128 v[200:203], v252 offset:19456\n\t"
"v_cvt_pk_bf16_f32 v14, v44, v45\n\t"
"v_mfma_f32_16x16x32_bf16 v[76:79], a[28:31], v[212:215], v[76:79]\n\t"
"ds_read_b128 v[204:207], v252 offset:20480\n\t"
"v_cvt_pk_bf16_f32 v15, v46, v47\n\t"
"v_mfma_f32_16x16x32_bf16 v[112:115], a[112:115], a[176:179], v[112:115]\n\t"
"s_add_u32 s29, s23, 393216\n\t"
"ds_read_b128 a[172:175], v255 offset:19456\n\t"
"v_mfma_f32_16x16x32_bf16 v[112:115], a[120:123], a[180:183], v[112:115]\n\t"
"v_mfma_f32_16x16x32_bf16 v[112:115], a[64:67], a[184:187], v[112:115]\n\t"
"ds_write_b128 v249, v[12:15] offset:30720\n\t"
"v_mfma_f32_16x16x32_bf16 v[112:115], a[68:71], a[188:191], v[112:115]\n\t"
"s_waitcnt lgkmcnt(4)\n\t"
"v_cvt_pk_bf16_f32 v76, v76, v77\n\t"
"v_cvt_pk_bf16_f32 v77, v78, v79\n\t"
"v_mfma_f32_16x16x32_bf16 v[112:115], a[72:75], a[192:195], v[112:115]\n\t"
"v_mfma_f32_16x16x32_bf16 v[112:115], a[40:43], a[196:199], v[112:115]\n\t"
"ds_write_b64 v250, v[76:77] offset:30720\n\t"
"v_mfma_f32_16x16x32_bf16 v[144:147], a[44:47], a[176:179], v[144:147]\n\t"
"s_waitcnt lgkmcnt(4)\n\t"
"v_mfma_f32_16x16x32_bf16 v[144:147], a[32:35], a[180:183], v[144:147]\n\t"
"v_mfma_f32_16x16x32_bf16 v[144:147], a[36:39], a[184:187], v[144:147]\n\t"
"buffer_store_dwordx4 v[196:199], v248, s[16:19], s29 offen offset:0 sc1\n\t"
"v_mfma_f32_16x16x32_bf16 v[144:147], a[216:219], a[188:191], v[144:147]\n\t"
"s_waitcnt lgkmcnt(3)\n\t"
"v_cvt_pk_bf16_f32 v112, v112, v113\n\t"
"v_cvt_pk_bf16_f32 v113, v114, v115\n\t"
"v_mfma_f32_16x16x32_bf16 v[144:147], a[220:223], a[192:195], v[144:147]\n\t"
"v_mfma_f32_16x16x32_bf16 v[144:147], a[224:227], a[196:199], v[144:147]\n\t"
"buffer_store_dwordx4 v[200:203], v248, s[16:19], s29 offen offset:128 sc1\n\t"
"v_mfma_f32_16x16x32_bf16 v[176:179], a[76:79], a[176:179], v[176:179]\n\t"
"ds_read_b128 a[176:179], v255 offset:20480\n\t"
"v_mfma_f32_16x16x32_bf16 v[176:179], a[4:7], a[180:183], v[176:179]\n\t"
"v_mfma_f32_16x16x32_bf16 v[176:179], a[80:83], a[184:187], v[176:179]\n\t"
"buffer_store_dwordx4 v[204:207], v248, s[16:19], s29 offen offset:256 sc1\n\t"
"v_mfma_f32_16x16x32_bf16 v[176:179], a[8:11], a[188:191], v[176:179]\n\t"
"ds_read_b128 a[188:191], v255 offset:20992\n\t"
"v_cvt_pk_bf16_f32 v114, v144, v145\n\t"
"v_mfma_f32_16x16x32_bf16 v[176:179], a[12:15], a[192:195], v[176:179]\n\t"
"v_cvt_pk_bf16_f32 v115, v146, v147\n\t"
"v_mfma_f32_16x16x32_bf16 v[176:179], a[228:231], a[196:199], v[176:179]\n\t"
"ds_write_b128 v249, v[112:115] offset:36864\n\t"
"Lst35_%=:\n\t"
"v_mfma_f32_16x16x32_bf16 v[16:19], a[148:151], a[200:203], v[16:19]\n\t"
"s_waitcnt lgkmcnt(5)\n\t"
"v_mfma_f32_16x16x32_bf16 v[16:19], a[236:239], v[208:211], v[16:19]\n\t"
"v_mfma_f32_16x16x32_bf16 v[16:19], a[240:243], a[168:171], v[16:19]\n\t"
"v_mfma_f32_16x16x32_bf16 v[16:19], a[244:247], a[204:207], v[16:19]\n\t"
"v_cvt_pk_bf16_f32 v176, v176, v177\n\t"
"v_cvt_pk_bf16_f32 v177, v178, v179\n\t"
"v_mfma_f32_16x16x32_bf16 v[16:19], a[104:107], v[212:215], v[16:19]\n\t"
"v_mfma_f32_16x16x32_bf16 v[16:19], a[156:159], a[172:175], v[16:19]\n\t"
"ds_write_b64 v250, v[176:177] offset:36864\n\t"
"v_mfma_f32_16x16x32_bf16 v[48:51], a[164:167], a[200:203], v[48:51]\n\t"
"v_mfma_f32_16x16x32_bf16 v[48:51], v[240:243], v[208:211], v[48:51]\n\t"
"v_mfma_f32_16x16x32_bf16 v[48:51], v[96:99], a[168:171], v[48:51]\n\t"
"v_mfma_f32_16x16x32_bf16 v[48:51], a[248:251], a[204:207], v[48:51]\n\t"
"v_cvt_pk_bf16_f32 v16, v16, v17\n\t"
"v_cvt_pk_bf16_f32 v17, v18, v19\n\t"
"v_mfma_f32_16x16x32_bf16 v[48:51], a[52:55], v[212:215], v[48:51]\n\t"
"v_mfma_f32_16x16x32_bf16 v[48:51], a[56:59], a[172:175], v[48:51]\n\t"
"v_mfma_f32_16x16x32_bf16 v[80:83], a[48:51], a[200:203], v[80:83]\n\t"
"ds_read_b128 a[200:203], v255 offset:22016\n\t"
"v_mfma_f32_16x16x32_bf16 v[80:83], v[236:239], v[208:211], v[80:83]\n\t"
"v_mfma_f32_16x16x32_bf16 v[80:83], a[60:63], a[168:171], v[80:83]\n\t"
"s_waitcnt lgkmcnt(1)\n\t"
"s_barrier\n\t"
"v_mfma_f32_16x16x32_bf16 v[80:83], a[212:215], a[204:207], v[80:83]\n\t"
"ds_read_b128 v[196:199], v252 offset:30720\n\t"
"v_mfma_f32_16x16x32_bf16 v[80:83], a[252:255], v[212:215], v[80:83]\n\t"
"ds_read_b128 v[200:203], v252 offset:31744\n\t"
"v_cvt_pk_bf16_f32 v18, v48, v49\n\t"
"v_mfma_f32_16x16x32_bf16 v[80:83], a[28:31], a[172:175], v[80:83]\n\t"
"ds_read_b128 v[204:207], v252 offset:32768\n\t"
"v_cvt_pk_bf16_f32 v19, v50, v51\n\t"
"v_mfma_f32_16x16x32_bf16 v[116:119], a[112:115], a[180:183], v[116:119]\n\t"
"s_add_u32 s29, s23, 589824\n\t"
"ds_read_b128 a[204:207], v255 offset:22528\n\t"
"v_mfma_f32_16x16x32_bf16 v[116:119], a[120:123], a[184:187], v[116:119]\n\t"
"v_mfma_f32_16x16x32_bf16 v[116:119], a[64:67], a[176:179], v[116:119]\n\t"
"ds_write_b128 v249, v[16:19] offset:18432\n\t"
"v_mfma_f32_16x16x32_bf16 v[116:119], a[68:71], a[192:195], v[116:119]\n\t"
"s_waitcnt lgkmcnt(4)\n\t"
"v_cvt_pk_bf16_f32 v80, v80, v81\n\t"
"v_cvt_pk_bf16_f32 v81, v82, v83\n\t"
"v_mfma_f32_16x16x32_bf16 v[116:119], a[72:75], a[196:199], v[116:119]\n\t"
"v_mfma_f32_16x16x32_bf16 v[116:119], a[40:43], a[188:191], v[116:119]\n\t"
"ds_write_b64 v250, v[80:81] offset:18432\n\t"
"v_mfma_f32_16x16x32_bf16 v[148:151], a[44:47], a[180:183], v[148:151]\n\t"
"s_waitcnt lgkmcnt(4)\n\t"
"v_mfma_f32_16x16x32_bf16 v[148:151], a[32:35], a[184:187], v[148:151]\n\t"
"v_mfma_f32_16x16x32_bf16 v[148:151], a[36:39], a[176:179], v[148:151]\n\t"
"buffer_store_dwordx4 v[196:199], v248, s[16:19], s29 offen offset:0 sc1\n\t"
"v_mfma_f32_16x16x32_bf16 v[148:151], a[216:219], a[192:195], v[148:151]\n\t"
"s_waitcnt lgkmcnt(3)\n\t"
"v_cvt_pk_bf16_f32 v116, v116, v117\n\t"
"v_cvt_pk_bf16_f32 v117, v118, v119\n\t"
"v_mfma_f32_16x16x32_bf16 v[148:151], a[220:223], a[196:199], v[148:151]\n\t"
"v_mfma_f32_16x16x32_bf16 v[148:151], a[224:227], a[188:191], v[148:151]\n\t"
"buffer_store_dwordx4 v[200:203], v248, s[16:19], s29 offen offset:128 sc1\n\t"
"v_mfma_f32_16x16x32_bf16 v[180:183], a[76:79], a[180:183], v[180:183]\n\t"
"ds_read_b128 a[180:183], v255 offset:23552\n\t"
"v_mfma_f32_16x16x32_bf16 v[180:183], a[4:7], a[184:187], v[180:183]\n\t"
"v_mfma_f32_16x16x32_bf16 v[180:183], a[80:83], a[176:179], v[180:183]\n\t"
"buffer_store_dwordx4 v[204:207], v248, s[16:19], s29 offen offset:256 sc1\n\t"
"v_mfma_f32_16x16x32_bf16 v[180:183], a[8:11], a[192:195], v[180:183]\n\t"
"ds_read_b128 a[192:195], v255 offset:24064\n\t"
"v_cvt_pk_bf16_f32 v118, v148, v149\n\t"
"v_mfma_f32_16x16x32_bf16 v[180:183], a[12:15], a[196:199], v[180:183]\n\t"
"v_cvt_pk_bf16_f32 v119, v150, v151\n\t"
"v_mfma_f32_16x16x32_bf16 v[180:183], a[228:231], a[188:191], v[180:183]\n\t"
"ds_write_b128 v249, v[116:119] offset:24576\n\t"
"v_mfma_f32_16x16x32_bf16 v[20:23], a[148:151], v[208:211], v[20:23]\n\t"
"s_waitcnt lgkmcnt(5)\n\t"
"v_mfma_f32_16x16x32_bf16 v[20:23], a[236:239], a[168:171], v[20:23]\n\t"
"v_mfma_f32_16x16x32_bf16 v[20:23], a[240:243], a[200:203], v[20:23]\n\t"
"v_mfma_f32_16x16x32_bf16 v[20:23], a[244:247], v[212:215], v[20:23]\n\t"
"v_cvt_pk_bf16_f32 v180, v180, v181\n\t"
"v_cvt_pk_bf16_f32 v181, v182, v183\n\t"
"v_mfma_f32_16x16x32_bf16 v[20:23], a[104:107], a[172:175], v[20:23]\n\t"
"v_mfma_f32_16x16x32_bf16 v[20:23], a[156:159], a[204:207], v[20:23]\n\t"
"ds_write_b64 v250, v[180:181] offset:24576\n\t"
"v_mfma_f32_16x16x32_bf16 v[52:55], a[164:167], v[208:211], v[52:55]\n\t"
"v_mfma_f32_16x16x32_bf16 v[52:55], v[240:243], a[168:171], v[52:55]\n\t"
"v_mfma_f32_16x16x32_bf16 v[52:55], v[96:99], a[200:203], v[52:55]\n\t"
"v_mfma_f32_16x16x32_bf16 v[52:55], a[248:251], v[212:215], v[52:55]\n\t"
"v_cvt_pk_bf16_f32 v20, v20, v21\n\t"
"v_cvt_pk_bf16_f32 v21, v22, v23\n\t"
"v_mfma_f32_16x16x32_bf16 v[52:55], a[52:55], a[172:175], v[52:55]\n\t"
"v_mfma_f32_16x16x32_bf16 v[52:55], a[56:59], a[204:207], v[52:55]\n\t"
"v_mfma_f32_16x16x32_bf16 v[84:87], a[48:51], v[208:211], v[84:87]\n\t"
"ds_read_b128 v[208:211], v255 offset:25088\n\t"
"v_mfma_f32_16x16x32_bf16 v[84:87], v[236:239], a[168:171], v[84:87]\n\t"
"v_mfma_f32_16x16x32_bf16 v[84:87], a[60:63], a[200:203], v[84:87]\n\t"
"s_waitcnt lgkmcnt(1)\n\t"
"s_barrier\n\t"
"v_mfma_f32_16x16x32_bf16 v[84:87], a[212:215], v[212:215], v[84:87]\n\t"
"ds_read_b128 v[196:199], v252 offset:18432\n\t"
"v_mfma_f32_16x16x32_bf16 v[84:87], a[252:255], a[172:175], v[84:87]\n\t"
"ds_read_b128 v[200:203], v252 offset:19456\n\t"
"v_cvt_pk_bf16_f32 v22, v52, v53\n\t"
"v_mfma_f32_16x16x32_bf16 v[84:87], a[28:31], a[204:207], v[84:87]\n\t"
"ds_read_b128 v[204:207], v252 offset:20480\n\t"
"v_cvt_pk_bf16_f32 v23, v54, v55\n\t"
"v_mfma_f32_16x16x32_bf16 v[120:123], a[112:115], a[184:187], v[120:123]\n\t"
"s_add_u32 s29, s23, 786432\n\t"
"ds_read_b128 v[212:215], v255 offset:25600\n\t"
"v_mfma_f32_16x16x32_bf16 v[120:123], a[120:123], a[176:179], v[120:123]\n\t"
"v_mfma_f32_16x16x32_bf16 v[120:123], a[64:67], a[180:183], v[120:123]\n\t"
"ds_write_b128 v249, v[20:23] offset:30720\n\t"
"v_mfma_f32_16x16x32_bf16 v[120:123], a[68:71], a[196:199], v[120:123]\n\t"
"s_waitcnt lgkmcnt(4)\n\t"
"v_cvt_pk_bf16_f32 v84, v84, v85\n\t"
"v_cvt_pk_bf16_f32 v85, v86, v87\n\t"
"v_mfma_f32_16x16x32_bf16 v[120:123], a[72:75], a[188:191], v[120:123]\n\t"
"v_mfma_f32_16x16x32_bf16 v[120:123], a[40:43], a[192:195], v[120:123]\n\t"
"ds_write_b64 v250, v[84:85] offset:30720\n\t"
"v_mfma_f32_16x16x32_bf16 v[152:155], a[44:47], a[184:187], v[152:155]\n\t"
"s_waitcnt lgkmcnt(4)\n\t"
"v_mfma_f32_16x16x32_bf16 v[152:155], a[32:35], a[176:179], v[152:155]\n\t"
"v_mfma_f32_16x16x32_bf16 v[152:155], a[36:39], a[180:183], v[152:155]\n\t"
"buffer_store_dwordx4 v[196:199], v248, s[16:19], s29 offen offset:0 sc1\n\t"
"v_mfma_f32_16x16x32_bf16 v[152:155], a[216:219], a[196:199], v[152:155]\n\t"
"s_waitcnt lgkmcnt(3)\n\t"
"v_cvt_pk_bf16_f32 v120, v120, v121\n\t"
"v_cvt_pk_bf16_f32 v121, v122, v123\n\t"
"v_mfma_f32_16x16x32_bf16 v[152:155], a[220:223], a[188:191], v[152:155]\n\t"
"v_mfma_f32_16x16x32_bf16 v[152:155], a[224:227], a[192:195], v[152:155]\n\t"
"buffer_store_dwordx4 v[200:203], v248, s[16:19], s29 offen offset:128 sc1\n\t"
"v_mfma_f32_16x16x32_bf16 v[184:187], a[76:79], a[184:187], v[184:187]\n\t"
"ds_read_b128 a[184:187], v255 offset:26624\n\t"
"v_mfma_f32_16x16x32_bf16 v[184:187], a[4:7], a[176:179], v[184:187]\n\t"
"v_mfma_f32_16x16x32_bf16 v[184:187], a[80:83], a[180:183], v[184:187]\n\t"
"buffer_store_dwordx4 v[204:207], v248, s[16:19], s29 offen offset:256 sc1\n\t"
"v_mfma_f32_16x16x32_bf16 v[184:187], a[8:11], a[196:199], v[184:187]\n\t"
"ds_read_b128 a[196:199], v255 offset:27136\n\t"
"v_cvt_pk_bf16_f32 v122, v152, v153\n\t"
"v_mfma_f32_16x16x32_bf16 v[184:187], a[12:15], a[188:191], v[184:187]\n\t"
"v_cvt_pk_bf16_f32 v123, v154, v155\n\t"
"v_mfma_f32_16x16x32_bf16 v[184:187], a[228:231], a[192:195], v[184:187]\n\t"
"ds_write_b128 v249, v[120:123] offset:36864\n\t"
"v_mfma_f32_16x16x32_bf16 v[24:27], a[148:151], a[168:171], v[24:27]\n\t"
"s_waitcnt lgkmcnt(5)\n\t"
"v_mfma_f32_16x16x32_bf16 v[24:27], a[236:239], a[200:203], v[24:27]\n\t"
"v_mfma_f32_16x16x32_bf16 v[24:27], a[240:243], v[208:211], v[24:27]\n\t"
"v_mfma_f32_16x16x32_bf16 v[24:27], a[244:247], a[172:175], v[24:27]\n\t"
"v_cvt_pk_bf16_f32 v184, v184, v185\n\t"
"v_cvt_pk_bf16_f32 v185, v186, v187\n\t"
"v_mfma_f32_16x16x32_bf16 v[24:27], a[104:107], a[204:207], v[24:27]\n\t"
"v_mfma_f32_16x16x32_bf16 v[24:27], a[156:159], v[212:215], v[24:27]\n\t"
"ds_write_b64 v250, v[184:185] offset:36864\n\t"
"v_mfma_f32_16x16x32_bf16 v[56:59], a[164:167], a[168:171], v[56:59]\n\t"
"v_mfma_f32_16x16x32_bf16 v[56:59], v[240:243], a[200:203], v[56:59]\n\t"
"v_mfma_f32_16x16x32_bf16 v[56:59], v[96:99], v[208:211], v[56:59]\n\t"
"v_mfma_f32_16x16x32_bf16 v[56:59], a[248:251], a[172:175], v[56:59]\n\t"
"v_cvt_pk_bf16_f32 v24, v24, v25\n\t"
"v_cvt_pk_bf16_f32 v25, v26, v27\n\t"
"v_mfma_f32_16x16x32_bf16 v[56:59], a[52:55], a[204:207], v[56:59]\n\t"
"v_mfma_f32_16x16x32_bf16 v[56:59], a[56:59], v[212:215], v[56:59]\n\t"
"v_mfma_f32_16x16x32_bf16 v[88:91], a[48:51], a[168:171], v[88:91]\n\t"
"ds_read_b128 a[168:171], v255 offset:28160\n\t"
"v_mfma_f32_16x16x32_bf16 v[88:91], v[236:239], a[200:203], v[88:91]\n\t"
"v_mfma_f32_16x16x32_bf16 v[88:91], a[60:63], v[208:211], v[88:91]\n\t"
"s_waitcnt lgkmcnt(1)\n\t"
"s_barrier\n\t"
"v_mfma_f32_16x16x32_bf16 v[88:91], a[212:215], a[172:175], v[88:91]\n\t"
"ds_read_b128 v[196:199], v252 offset:30720\n\t"
"v_mfma_f32_16x16x32_bf16 v[88:91], a[252:255], a[204:207], v[88:91]\n\t"
"ds_read_b128 v[200:203], v252 offset:31744\n\t"
"v_cvt_pk_bf16_f32 v26, v56, v57\n\t"
"v_mfma_f32_16x16x32_bf16 v[88:91], a[28:31], v[212:215], v[88:91]\n\t"
"ds_read_b128 v[204:207], v252 offset:32768\n\t"
"v_cvt_pk_bf16_f32 v27, v58, v59\n\t"
"v_mfma_f32_16x16x32_bf16 v[124:127], a[112:115], a[176:179], v[124:127]\n\t"
"s_add_u32 s29, s23, 983040\n\t"
"ds_read_b128 a[172:175], v255 offset:28672\n\t"
"v_mfma_f32_16x16x32_bf16 v[124:127], a[120:123], a[180:183], v[124:127]\n\t"
"v_mfma_f32_16x16x32_bf16 v[124:127], a[64:67], a[184:187], v[124:127]\n\t"
"ds_write_b128 v249, v[24:27] offset:18432\n\t"
"v_mfma_f32_16x16x32_bf16 v[124:127], a[68:71], a[188:191], v[124:127]\n\t"
"s_waitcnt lgkmcnt(4)\n\t"
"v_cvt_pk_bf16_f32 v88, v88, v89\n\t"
"v_cvt_pk_bf16_f32 v89, v90, v91\n\t"
"v_mfma_f32_16x16x32_bf16 v[124:127], a[72:75], a[192:195], v[124:127]\n\t"
"v_mfma_f32_16x16x32_bf16 v[124:127], a[40:43], a[196:199], v[124:127]\n\t"
"ds_write_b64 v250, v[88:89] offset:18432\n\t"
"v_mfma_f32_16x16x32_bf16 v[156:159], a[44:47], a[176:179], v[156:159]\n\t"
"s_waitcnt lgkmcnt(4)\n\t"
"v_mfma_f32_16x16x32_bf16 v[156:159], a[32:35], a[180:183], v[156:159]\n\t"
"v_mfma_f32_16x16x32_bf16 v[156:159], a[36:39], a[184:187], v[156:159]\n\t"
"buffer_store_dwordx4 v[196:199], v248, s[16:19], s29 offen offset:0 sc1\n\t"
"v_mfma_f32_16x16x32_bf16 v[156:159], a[216:219], a[188:191], v[156:159]\n\t"
"s_waitcnt lgkmcnt(3)\n\t"
"v_cvt_pk_bf16_f32 v124, v124, v125\n\t"
"v_cvt_pk_bf16_f32 v125, v126, v127\n\t"
"v_mfma_f32_16x16x32_bf16 v[156:159], a[220:223], a[192:195], v[156:159]\n\t"
"v_mfma_f32_16x16x32_bf16 v[156:159], a[224:227], a[196:199], v[156:159]\n\t"
"buffer_store_dwordx4 v[200:203], v248, s[16:19], s29 offen offset:128 sc1\n\t"
"v_mfma_f32_16x16x32_bf16 v[188:191], a[76:79], a[176:179], v[188:191]\n\t"
"ds_read_b128 a[176:179], v255 offset:29696\n\t"
"v_mfma_f32_16x16x32_bf16 v[188:191], a[4:7], a[180:183], v[188:191]\n\t"
"v_mfma_f32_16x16x32_bf16 v[188:191], a[80:83], a[184:187], v[188:191]\n\t"
"buffer_store_dwordx4 v[204:207], v248, s[16:19], s29 offen offset:256 sc1\n\t"
"v_mfma_f32_16x16x32_bf16 v[188:191], a[8:11], a[188:191], v[188:191]\n\t"
"ds_read_b128 a[188:191], v255 offset:30208\n\t"
"v_cvt_pk_bf16_f32 v126, v156, v157\n\t"
"v_mfma_f32_16x16x32_bf16 v[188:191], a[12:15], a[192:195], v[188:191]\n\t"
"v_cvt_pk_bf16_f32 v127, v158, v159\n\t"
"v_mfma_f32_16x16x32_bf16 v[188:191], a[228:231], a[196:199], v[188:191]\n\t"
"ds_write_b128 v249, v[124:127] offset:24576\n\t"
"v_mfma_f32_16x16x32_bf16 v[28:31], a[148:151], a[200:203], v[28:31]\n\t"
"s_waitcnt lgkmcnt(5)\n\t"
"v_mfma_f32_16x16x32_bf16 v[28:31], a[236:239], v[208:211], v[28:31]\n\t"
"v_mfma_f32_16x16x32_bf16 v[28:31], a[240:243], a[168:171], v[28:31]\n\t"
"v_mfma_f32_16x16x32_bf16 v[28:31], a[244:247], a[204:207], v[28:31]\n\t"
"v_cvt_pk_bf16_f32 v188, v188, v189\n\t"
"v_cvt_pk_bf16_f32 v189, v190, v191\n\t"
"v_mfma_f32_16x16x32_bf16 v[28:31], a[104:107], v[212:215], v[28:31]\n\t"
"v_mfma_f32_16x16x32_bf16 v[28:31], a[156:159], a[172:175], v[28:31]\n\t"
"ds_write_b64 v250, v[188:189] offset:24576\n\t"
"v_mfma_f32_16x16x32_bf16 v[60:63], a[164:167], a[200:203], v[60:63]\n\t"
"v_mfma_f32_16x16x32_bf16 v[60:63], v[240:243], v[208:211], v[60:63]\n\t"
"v_mfma_f32_16x16x32_bf16 v[60:63], v[96:99], a[168:171], v[60:63]\n\t"
"v_mfma_f32_16x16x32_bf16 v[60:63], a[248:251], a[204:207], v[60:63]\n\t"
"v_cvt_pk_bf16_f32 v28, v28, v29\n\t"
"v_cvt_pk_bf16_f32 v29, v30, v31\n\t"
"v_mfma_f32_16x16x32_bf16 v[60:63], a[52:55], v[212:215], v[60:63]\n\t"
"v_mfma_f32_16x16x32_bf16 v[60:63], a[56:59], a[172:175], v[60:63]\n\t"
"v_mfma_f32_16x16x32_bf16 v[92:95], a[48:51], a[200:203], v[92:95]\n\t"
"v_mfma_f32_16x16x32_bf16 v[92:95], v[236:239], v[208:211], v[92:95]\n\t"
"v_mfma_f32_16x16x32_bf16 v[92:95], a[60:63], a[168:171], v[92:95]\n\t"
"s_waitcnt lgkmcnt(0)\n\t"
"s_barrier\n\t"
"v_mfma_f32_16x16x32_bf16 v[92:95], a[212:215], a[204:207], v[92:95]\n\t"
"ds_read_b128 v[196:199], v252 offset:18432\n\t"
"v_mfma_f32_16x16x32_bf16 v[92:95], a[252:255], v[212:215], v[92:95]\n\t"
"ds_read_b128 v[200:203], v252 offset:19456\n\t"
"v_cvt_pk_bf16_f32 v30, v60, v61\n\t"
"v_mfma_f32_16x16x32_bf16 v[92:95], a[28:31], a[172:175], v[92:95]\n\t"
"ds_read_b128 v[204:207], v252 offset:20480\n\t"
"v_cvt_pk_bf16_f32 v31, v62, v63\n\t"
"v_mfma_f32_16x16x32_bf16 v[128:131], a[112:115], a[180:183], v[128:131]\n\t"
"s_add_u32 s29, s23, 1179648\n\t"
"v_mfma_f32_16x16x32_bf16 v[128:131], a[120:123], a[184:187], v[128:131]\n\t"
"v_mfma_f32_16x16x32_bf16 v[128:131], a[64:67], a[176:179], v[128:131]\n\t"
"ds_write_b128 v249, v[28:31] offset:30720\n\t"
"v_mfma_f32_16x16x32_bf16 v[128:131], a[68:71], a[192:195], v[128:131]\n\t"
"s_waitcnt lgkmcnt(3)\n\t"
"v_cvt_pk_bf16_f32 v92, v92, v93\n\t"
"v_cvt_pk_bf16_f32 v93, v94, v95\n\t"
"v_mfma_f32_16x16x32_bf16 v[128:131], a[72:75], a[196:199], v[128:131]\n\t"
"v_mfma_f32_16x16x32_bf16 v[128:131], a[40:43], a[188:191], v[128:131]\n\t"
"ds_write_b64 v250, v[92:93] offset:30720\n\t"
"v_mfma_f32_16x16x32_bf16 v[160:163], a[44:47], a[180:183], v[160:163]\n\t"
"s_waitcnt lgkmcnt(3)\n\t"
"v_mfma_f32_16x16x32_bf16 v[160:163], a[32:35], a[184:187], v[160:163]\n\t"
"v_mfma_f32_16x16x32_bf16 v[160:163], a[36:39], a[176:179], v[160:163]\n\t"
"buffer_store_dwordx4 v[196:199], v248, s[16:19], s29 offen offset:0 sc1\n\t"
"v_mfma_f32_16x16x32_bf16 v[160:163], a[216:219], a[192:195], v[160:163]\n\t"
"s_waitcnt lgkmcnt(2)\n\t"
"v_cvt_pk_bf16_f32 v128, v128, v129\n\t"
"v_cvt_pk_bf16_f32 v129, v130, v131\n\t"
"v_mfma_f32_16x16x32_bf16 v[160:163], a[220:223], a[196:199], v[160:163]\n\t"
"v_mfma_f32_16x16x32_bf16 v[160:163], a[224:227], a[188:191], v[160:163]\n\t"
"buffer_store_dwordx4 v[200:203], v248, s[16:19], s29 offen offset:128 sc1\n\t"
"v_mfma_f32_16x16x32_bf16 v[192:195], a[76:79], a[180:183], v[192:195]\n\t"
"v_mfma_f32_16x16x32_bf16 v[192:195], a[4:7], a[184:187], v[192:195]\n\t"
"v_mfma_f32_16x16x32_bf16 v[192:195], a[80:83], a[176:179], v[192:195]\n\t"
"buffer_store_dwordx4 v[204:207], v248, s[16:19], s29 offen offset:256 sc1\n\t"
"v_mfma_f32_16x16x32_bf16 v[192:195], a[8:11], a[192:195], v[192:195]\n\t"
"v_cvt_pk_bf16_f32 v130, v160, v161\n\t"
"v_cvt_pk_bf16_f32 v131, v162, v163\n\t"
"v_mfma_f32_16x16x32_bf16 v[192:195], a[12:15], a[196:199], v[192:195]\n\t"
"v_mfma_f32_16x16x32_bf16 v[192:195], a[228:231], a[188:191], v[192:195]\n\t"
"ds_write_b128 v249, v[128:131] offset:36864\n\t"
"s_nop 6\n\t"
"v_cvt_pk_bf16_f32 v192, v192, v193\n\t"
"v_cvt_pk_bf16_f32 v193, v194, v195\n\t"
"ds_write_b64 v250, v[192:193] offset:36864\n\t"
"s_waitcnt lgkmcnt(0)\n\t"
"s_barrier\n\t"
"ds_read_b128 v[196:199], v252 offset:30720\n\t"
"ds_read_b128 v[200:203], v252 offset:31744\n\t"
"ds_read_b128 v[208:211], v252 offset:32768\n\t"
"s_waitcnt lgkmcnt(2)\n\t"
"s_add_u32 s29, s23, 1376256\n\t"
"buffer_store_dwordx4 v[196:199], v248, s[16:19], s29 offen offset:0 sc1\n\t"
"s_waitcnt lgkmcnt(1)\n\t"
"buffer_store_dwordx4 v[200:203], v248, s[16:19], s29 offen offset:128 sc1\n\t"
"s_waitcnt lgkmcnt(0)\n\t"
"buffer_store_dwordx4 v[208:211], v248, s[16:19], s29 offen offset:256 sc1\n\t"
"s_waitcnt vmcnt(0)\n\t"
"Lwino_end:\n\t"
  : "+{v205}"(tid)
  : "s"((unsigned long long)__builtin_amdgcn_kernarg_segment_ptr()), "s"(blockIdx.x), "s"((unsigned long long)lane_tab), "s"((unsigned)(size_t)(__attribute__((address_space(3))) char*)smem), "s"((unsigned)(size_t)__builtin_amdgcn_dispatch_ptr())
  : "v0", "v1", "v2", "v3", "v4", "v5", "v6", "v7", "v8", "v9", "v10", "v11", "v12", "v13", "v14", "v15", "v16", "v17", "v18", "v19", "v20", "v21", "v22", "v23", "v24", "v25", "v26", "v27", "v28", "v29", "v30", "v31", "v32", "v33", "v34", "v35", "v36", "v37", "v38", "v39", "v40", "v41", "v42", "v43", "v44", "v45", "v46", "v47", "v48", "v49", "v50", "v51", "v52", "v53", "v54", "v55", "v56", "v57", "v58", "v59", "v60", "v61", "v62", "v63", "v64", "v65", "v66", "v67", "v68", "v69", "v70", "v71", "v72", "v73", "v74", "v75", "v76", "v77", "v78", "v79", "v80", "v81", "v82", "v83", "v84", "v85", "v86", "v87", "v88", "v89", "v90", "v91", "v92", "v93", "v94", "v95", "v96", "v97", "v98", "v99", "v100", "v101", "v102", "v103", "v104", "v105", "v106", "v107", "v108", "v109", "v110", "v111", "v112", "v113", "v114", "v115", "v116", "v117", "v118", "v119", "v120", "v121", "v122", "v123", "v124", "v125", "v126", "v127", "v128", "v129", "v130", "v131", "v132", "v133", "v134", "v135", "v136", "v137", "v138", "v139", "v140", "v141", "v142", "v143", "v144", "v145", "v146", "v147", "v148", "v149", "v150", "v151", "v152", "v153", "v154", "v155", "v156", "v157", "v158", "v159", "v160", "v161", "v162", "v163", "v164", "v165", "v166", "v167", "v168", "v169", "v170", "v171", "v172", "v173", "v174", "v175", "v176", "v177", "v178", "v179", "v180", "v181", "v182", "v183", "v184", "v185", "v186", "v187", "v188", "v189", "v190", "v191", "v192", "v193", "v194", "v195", "v196", "v197", "v198", "v199", "v200", "v201", "v202", "v203", "v204", "v206", "v207", "v208", "v209", "v210", "v211", "v212", "v213", "v214", "v215", "v216", "v217", "v218", "v219", "v220", "v221", "v222", "v223", "v224", "v225", "v226", "v227", "v228", "v229", "v230", "v231", "v232", "v233", "v234", "v235", "v236", "v237", "v238", "v239", "v240", "v241", "v242", "v243", "v244", "v245", "v246", "v247", "v248", "v249", "v250", "v251", "v252", "v253", "v254", "v255", "a0", "a1", "a2", "a3", "a4", "a5", "a6", "a7", "a8", "a9", "a10", "a11", "a12", "a13", "a14", "a15", "a16", "a17", "a18", "a19", "a20", "a21", "a22", "a23", "a24", "a25", "a26", "a27", "a28", "a29", "a30", "a31", "a32", "a33", "a34", "a35", "a36", "a37", "a38", "a39", "a40", "a41", "a42", "a43", "a44", "a45", "a46", "a47", "a48", "a49", "a50", "a51", "a52", "a53", "a54", "a55", "a56", "a57", "a58", "a59", "a60", "a61", "a62", "a63", "a64", "a65", "a66", "a67", "a68", "a69", "a70", "a71", "a72", "a73", "a74", "a75", "a76", "a77", "a78", "a79", "a80", "a81", "a82", "a83", "a84", "a85", "a86", "a87", "a88", "a89", "a90", "a91", "a92", "a93", "a94", "a95", "a96", "a97", "a98", "a99", "a100", "a101", "a102", "a103", "a104", "a105", "a106", "a107", "a108", "a109", "a110", "a111", "a112", "a113", "a114", "a115", "a116", "a117", "a118", "a119", "a120", "a121", "a122", "a123", "a124", "a125", "a126", "a127", "a128", "a129", "a130", "a131", "a132", "a133", "a134", "a135", "a136", "a137", "a138", "a139", "a140", "a141", "a142", "a143", "a144", "a145", "a146", "a147", "a148", "a149", "a150", "a151", "a152", "a153", "a154", "a155", "a156", "a157", "a158", "a159", "a160", "a161", "a162", "a163", "a164", "a165", "a166", "a167", "a168", "a169", "a170", "a171", "a172", "a173", "a174", "a175", "a176", "a177", "a178", "a179", "a180", "a181", "a182", "a183", "a184", "a185", "a186", "a187", "a188", "a189", "a190", "a191", "a192", "a193", "a194", "a195", "a196", "a197", "a198", "a199", "a200", "a201", "a202", "a203", "a204", "a205", "a206", "a207", "a208", "a209", "a210", "a211", "a212", "a213", "a214", "a215", "a216", "a217", "a218", "a219", "a220", "a221", "a222", "a223", "a224", "a225", "a226", "a227", "a228", "a229", "a230", "a231", "a232", "a233", "a234", "a235", "a236", "a237", "a238", "a239", "a240", "a241", "a242", "a243", "a244", "a245", "a246", "a247", "a248", "a249", "a250", "a251", "a252", "a253", "a254", "a255", "s8", "s9", "s10", "s11", "s12", "s13", "s14", "s15", "s16", "s17", "s18", "s19", "s20", "s21", "s22", "s23", "s24", "s25", "s26", "s27", "s28", "s29", "s30", "s31", "s32", "s33", "s34", "s35", "s36", "s37", "s38", "s39", "s40", "s41", "s42", "s43", "s44", "s45", "s46", "s47", "s48", "s49", "s50", "s51", "s52", "s53", "s54", "s55", "s56", "s57", "s58", "s59", "s60", "s61", "s62", "s63", "s64", "s65", "s66", "s67", "s68", "s69", "s70", "s71", "s72", "s73", "s74", "s75", "s76", "s77", "scc", "vcc", "memory");
}

extern "C" void kernel_launch(void* const* d_in, const int* in_sizes, int n_in, void* d_out, int out_size, void* d_ws,
                              size_t ws_size, hipStream_t stream) {
  wino_main_kernel<<<256, 256, 0, stream>>>((const char*)d_in[0], (char*)d_ws, (char*)d_out, (const char*)d_in[1],
                                            ws_size >= (36ull << 20) ? ~0ull : 0ull);
}
```

```cpp
#include <hip/hip_runtime.h>
#include <hip/hip_bf16.h>
#include <stdint.h>

#define IMG_W 512
#define CH 192
#define PXB 384
#define ROWB (IMG_W * PXB)
#define U_BYTES 1769472

__device__ const __attribute__((aligned(1024))) unsigned lane_tab[6144] = {
  0x0,0x0,0x0,0x0,0x0,0x0,0x0,0x0,0x0,0x0,0x0,0x0,0x0,0x0,0x0,0x0,
  0x0,0x0,0x0,0x0,0x0,0x0,0x0,0x0,0x0,0x0,0x0,0x0,0x0,0x0,0x0,0x0,
  0x0,0x0,0x0,0x0,0x0,0x0,0x0,0x0,0x0,0x0,0x0,0x0,0x0,0x0,0x0,0x0,
  0x0,0x0,0x0,0x0,0x0,0x0,0x0,0x0,0x0,0x0,0x0,0x0,0x0,0x0,0x0,0x0,
  0x0,0x0,0x0,0x0,0x0,0x0,0x0,0x0,0x0,0x0,0x0,0x0,0x0,0x0,0x0,0x0,
  0x0,0x0,0x0,0x0,0x0,0x0,0x0,0x0,0x0,0x0,0x0,0x0,0x0,0x0,0x0,0x0,
  0x0,0x0,0x0,0x0,0x0,0x0,0x0,0x0,0x0,0x0,0x0,0x0,0x0,0x0,0x0,0x0,
  0x0,0x0,0x0,0x0,0x0,0x0,0x0,0x0,0x0,0x0,0x0,0x0,0x0,0x0,0x0,0x0,
  0x0,0x0,0x0,0x0,0x0,0x0,0x0,0x0,0x0,0x0,0x0,0x0,0x0,0x0,0x0,0x0,
  0x0,0x0,0x0,0x0,0x0,0x0,0x0,0x0,0x0,0x0,0x0,0x0,0x0,0x0,0x0,0x0,
  0x0,0x0,0x0,0x0,0x0,0x0,0x0,0x0,0x0,0x0,0x0,0x0,0x0,0x0,0x0,0x0,
  0x0,0x0,0x0,0x0,0x0,0x0,0x0,0x0,0x0,0x0,0x0,0x0,0x0,0x0,0x0,0x0,
  0x0,0x0,0x0,0x0,0x0,0x0,0x0,0x0,0x0,0x0,0x0,0x0,0x0,0x0,0x0,0x0,
  0x0,0x0,0x0,0x0,0x0,0x0,0x0,0x0,0x0,0x0,0x0,0x0,0x0,0x0,0x0,0x0,
  0x0,0x0,0x0,0x0,0x0,0x0,0x0,0x0,0x0,0x0,0x0,0x0,0x0,0x0,0x0,0x0,
  0x0,0x0,0x0,0x0,0x0,0x0,0x0,0x0,0x0,0x0,0x0,0x0,0x0,0x0,0x0,0x0,
  0x0,0xc042,0x0,0x0,0x0,0xc0420000,0x0,0x0,0x0,0x0,0x0,0xc042,0x0,0x0,0x0,0xc0420000,
  0x3f903f90,0x0,0x0,0x0,0x3f900000,0x0,0x3f90,0x0,0x0,0x0,0x3f903f90,0x0,0x0,0x0,0x3f900000,0x0,
  0x0,0x0,0x0,0x0,0x0,0x0,0x0,0x0,0x0,0x0,0x0,0x0,0x0,0x0,0x0,0x0,
  0x0,0x0,0x0,0x0,0x0,0x0,0x0,0x0,0x0,0x0,0x0,0x0,0x0,0x0,0x0,0x0,
  0x0,0x0,0x3f90,0x0,0x0,0x0,0x0,0x0,0x0,0x0,0x0,0x0,0x0,0x0,0x0,0x0,
  0x0,0x0,0x0,0x0,0xc0420000,0x0,0x0,0x0,0x0,0x0,0x0,0x0,0x0,0x0,0xc0420000,0x0,
  0x0,0x3f90,0x0,0x0,0x0,0x3f903f90,0x0,0x0,0x0,0x3f900000,0x0,0x3f90,0x0,0x0,0x0,0x3f903f90,
  0x0,0x3f90,0x0,0x0,0x0,0x3f903f90,0x0,0x0,0x0,0x3f900000,0x0,0x3f90,0x0,0x0,0x0,0x3f903f90,
  0x0,0x0,0x0,0x0,0x0,0x0,0x0,0x0,0x0,0x0,0x0,0x0,0x0,0x0,0x0,0x0,
  0x0,0x0,0x0,0x0,0x0,0x0,0x0,0x0,0x0,0x0,0x0,0x0,0x0,0x0,0x3f90,0x0,
  0x0,0xc042,0x3f900000,0x0,0x0,0xc0420000,0x0,0x0,0x0,0x0,0x0,0xc042,0x0,0x0,0x0,0xc0420000,
  0x0,0xc042,0x3f900000,0x0,0x0,0xc0420000,0x0,0x0,0x0,0x0,0x0,0xc042,0x0,0x0,0x0,0xc0420000,
  0x3f900000,0x0,0x0,0x0,0x3f900000,0x3f90,0x0,0x0,0x0,0x3f90,0x3f900000,0x0,0x0,0x0,0x3f900000,0x3f90,
  0xc042,0x0,0x0,0x0,0x0,0x0,0x0,0x0,0x0,0x0,0xc042,0x0,0x0,0x0,0x0,0x0,
  0x0,0x0,0x0,0x0,0x0,0x0,0x0,0x0,0x0,0x0,0x0,0x0,0x0,0x0,0x0,0x0,
  0x0,0x0,0x0,0x0,0x0,0x0,0x0,0x0,0x0,0x0,0x0,0x0,0x0,0x0,0x0,0x0,
  0xa8000000,0x0,0xa800,0x60,0xa8080040,0x400040,0xa808,0x600000,0xa8100090,0x900090,0xa810,0x6000,0xa81800d0,0xd000d0,0xa818,0x60000000,
  0xa9200120,0x24000120,0xa840,0x40004,0xa9280160,0x24400160,0xa848,0x40400,0xa93001b0,0x249001b0,0xa850,0x4000400,0xa93801f0,0x24d001f0,0xa858,0x4000000,
  0xa8401000,0x2002408,0xa880,0x0,0xa8481040,0x2402448,0xa888,0x0,0xa8501090,0x2902498,0xa890,0x0,0xa85810d0,0x2d024d8,0xa898,0x0,
  0xa9601120,0x2002528,0xa8c0,0x0,0xa9681160,0x2402568,0xa8c8,0x0,0xa97011b0,0x29025b8,0xa8d0,0x0,0xa97811f0,0x2d025f8,0xa8d8,0x0,
  0xea800010,0x100010,0xa920,0x400,0xea880050,0x500050,0xa928,0x0,0xea900080,0x800080,0xa930,0x0,0xea9800c0,0xc000c0,0xa938,0x0,
  0xa9a00130,0x24100130,0xa960,0x0,0xa9a80170,0x24500170,0xa968,0x60000,0xa9b001a0,0x248001a0,0xa970,0x0,0xa9b801e0,0x24c001e0,0xa978,0x6000000,
  0xc0c01010,0x2102418,0xa9a0,0x40,0xc0c81050,0x2502458,0xa9a8,0x400040,0xc0d01080,0x2802488,0xa9b0,0x404000,0xc0d810c0,0x2c024c8,0xa9b8,0x40004000,
  0xc1e01130,0x2102538,0xa9e0,0x40,0xc1e81170,0x2502578,0xa9e8,0x400040,0xc1f011a0,0x28025a8,0xa9f0,0x404000,0xc1f811e0,0x2c025e8,0xa9f8,0x40004000,
  0xae000020,0x200020,0xae00,0x0,0xae080060,0x600060,0xae08,0x0,0xae1000b0,0xb000b0,0xae10,0x0,0xae1800f0,0xf000f0,0xae18,0x0,
  0xeb200100,0x24200100,0xae40,0x0,0xeb280140,0x24600140,0xae48,0x0,0xeb300190,0x24b00190,0xae50,0x0,0xeb3801d0,0x24f001d0,0xae58,0x400,
  0xc0401020,0x2202428,0xae80,0x4000060,0xc0481060,0x2602468,0xae88,0x600000,0xc05010b0,0x2b024b8,0xae90,0x6000,0xc05810f0,0x2f024f8,0xae98,0x60000000,
  0xc1601100,0x2202508,0xaec0,0x4000060,0xc1681140,0x2602548,0xaec8,0x600000,0xc1701190,0x2b02598,0xaed0,0x6000,0xc17811d0,0x2f025d8,0xaed8,0x60000000,
  0xa8800030,0x300030,0xaf20,0x40000,0xa8880070,0x700070,0xaf28,0x40040,0xa89000a0,0xa000a0,0xaf30,0x4000040,0xa89800e0,0xe000e0,0xaf38,0x4004000,
  0xa8c00110,0x24300110,0xaf60,0x6,0xa8c80150,0x24700150,0xaf68,0x0,0xa8d00180,0x24a00180,0xaf70,0x600,0xa8d801c0,0x24e001c0,0xaf78,0x0,
  0xa9e01030,0x2302438,0xafa0,0x0,0xa9e81070,0x2702478,0xafa8,0x0,0xa9f010a0,0x2a024a8,0xafb0,0x0,0xa9f810e0,0x2e024e8,0xafb8,0x0,
  0xae001110,0x2302518,0xafe0,0x0,0xae081150,0x2702558,0xafe8,0x0,0xae101180,0x2a02588,0xaff0,0x0,0xae1811c0,0x2e025c8,0xaff8,0x0,
  0x0,0x0,0x0,0x0,0x0,0x0,0x0,0x0,0x0,0x0,0x0,0x0,0x0,0x0,0x0,0x0,
  0x0,0x0,0x0,0x0,0x0,0x0,0x0,0x0,0x0,0x0,0x0,0x0,0x0,0x0,0x0,0x0,
  0x0,0x0,0x0,0x0,0x0,0x0,0x0,0x0,0x0,0x0,0x0,0x0,0x0,0x0,0x0,0x0,
  0x0,0x0,0x0,0x0,0x0,0x0,0x0,0x0,0x0,0x0,0x0,0x0,0x0,0x0,0x0,0x0,
  0x0,0x0,0x0,0x0,0x0,0x0,0x0,0x0,0x0,0x0,0x0,0x0,0x0,0x0,0x0,0x0,
  0x0,0x0,0x0,0x0,0x0,0x0,0x0,0x0,0x0,0x0,0x0,0x0,0x0,0x0,0x0,0x0,
  0x0,0x0,0x0,0x0,0x0,0x0,0x0,0x0,0x0,0x0,0x0,0x0,0x0,0x0,0x0,0x0,
  0x0,0x0,0x0,0x0,0x0,0x0,0x0,0x0,0x0,0x0,0x0,0x0,0x0,0x0,0x0,0x0,
  0x0,0x0,0x0,0x0,0x0,0x0,0x0,0x0,0x0,0x0,0x0,0x0,0x0,0x0,0x0,0x0,
  0x0,0x0,0x0,0x0,0x0,0x0,0x0,0x0,0x0,0x0,0x0,0x0,0x0,0x0,0x0,0x0,
  0x0,0x0,0x0,0x0,0x0,0x0,0x0,0x0,0x0,0x0,0x0,0x0,0x0,0x0,0x0,0x0,
  0x0,0x0,0x0,0x0,0x0,0x0,0x0,0x0,0x0,0x0,0x0,0x0,0x0,0x0,0x0,0x0,
  0x0,0x0,0x0,0x0,0x0,0x0,0x0,0x0,0x0,0x0,0x0,0x0,0x0,0x0,0x0,0x0,
  0x0,0x0,0x0,0x0,0x0,0x0,0x0,0x0,0x0,0x0,0x0,0x0,0x0,0x0,0x0,0x0,
  0x0,0x0,0x0,0x0,0x0,0x0,0x0,0x0,0x0,0x0,0x0,0x0,0x0,0x0,0x0,0x0,
  0x0,0x0,0x0,0x0,0x0,0x0,0x0,0x0,0x0,0x0,0x0,0x0,0x0,0x0,0x0,0x0,
  0xc0420000,0x0,0x0,0x0,0x0,0x0,0x0,0x0,0x0,0x0,0xc0420000,0x0,0x0,0x0,0x0,0x0,
  0x3f90,0x0,0x0,0x0,0x0,0x0,0x3f90,0x0,0x0,0x0,0x3f90,0x0,0x0,0x0,0x0,0x0,
  0x0,0xc042,0x0,0x0,0x0,0xc0420000,0x0,0x0,0x0,0x0,0x0,0xc042,0x0,0x0,0x0,0xc0420000,
  0x0,0xc042,0x0,0x0,0x0,0xc0420000,0x0,0x0,0x0,0x0,0x0,0xc042,0x0,0x0,0x0,0xc0420000,
  0x0,0x0,0x0,0x3f90,0x0,0x0,0x0,0x0,0x0,0x0,0x0,0x0,0x0,0x0,0x0,0x0,
  0x0,0x0,0x0,0x0,0xc042,0x0,0x0,0x0,0x0,0x0,0x0,0x0,0x0,0x0,0xc042,0x0,
  0x3f900000,0x0,0x0,0x3f900000,0x3f900000,0x0,0x0,0x0,0x0,0x0,0x3f900000,0x0,0x0,0x0,0x3f900000,0x0,
  0x3f900000,0x0,0x0,0x3f900000,0x3f900000,0x0,0x0,0x0,0x0,0x0,0x3f900000,0x0,0x0,0x0,0x3f900000,0x0,
  0x0,0x0,0x0,0x0,0x0,0xc042,0x0,0x0,0x0,0x0,0x0,0x0,0x0,0x0,0x0,0xc042,
  0x3f90,0xc0420000,0x0,0x0,0x3f90,0x0,0x0,0x0,0x0,0x0,0x3f90,0xc0420000,0x0,0x0,0x3f903f90,0x0,
  0x0,0x0,0x0,0x0,0x0,0x0,0x0,0x0,0x0,0x0,0x0,0x0,0x0,0x0,0x0,0x0,
  0x0,0x0,0x0,0x0,0x0,0x0,0x0,0x0,0x0,0x0,0x0,0x0,0x0,0x0,0x0,0x0,
  0x3f90,0x0,0x0,0x0,0x3f903f90,0x0,0x0,0x0,0x3f900000,0x0,0x3f90,0x0,0x0,0x0,0x3f903f90,0x0,
  0x0,0x0,0x0,0x0,0x0,0x0,0x0,0x0,0x0,0x0,0x0,0x0,0x0,0x0,0x0,0x0,
  0x0,0x0,0x0,0x0,0x0,0x3f90,0x0,0x0,0x0,0x3f90,0x0,0x0,0x0,0x0,0x0,0x3f90,
  0x0,0x0,0x0,0x0,0x0,0x3f90,0x0,0x0,0x0,0x3f90,0x0,0x0,0x0,0x0,0x0,0x3f90,
  0xb4000000,0x1200008,0xa820,0x60000,0xb4080040,0x1600048,0xa828,0x0,0xb4100090,0x1b00098,0xa830,0x6000000,0xb41800d0,0x1f000d8,0xa838,0x0,
  0xb4400120,0x25200128,0xa860,0x4,0xb4480160,0x25600168,0xa868,0x400,0xb45001b0,0x25b001b8,0xa870,0x400,0xb45801f0,0x25f001f8,0xa878,0x0,
  0xcc401000,0x3202400,0xa8a0,0x60,0xcc481040,0x3602440,0xa8a8,0x600000,0xcc501090,0x3b02490,0xa8b0,0x6000,0xcc5810d0,0x3f024d0,0xa8b8,0x60000000,
  0xcd601120,0x3202520,0xa8e0,0x60,0xcd681160,0x3602560,0xa8e8,0x600000,0xcd7011b0,0x3b025b0,0xa8f0,0x6000,0xcd7811f0,0x3f025f0,0xa8f8,0x60000000,
  0xb5a00010,0x1300018,0xa900,0x4000,0xb5a80050,0x1700058,0xa908,0x0,0xb5b00080,0x1a00088,0xa910,0x0,0xb5b800c0,0x1e000c8,0xa918,0x0,
  0xccc00130,0x25300138,0xa940,0x0,0xccc80170,0x25700178,0xa948,0x6,0xccd001a0,0x25a001a8,0xa950,0x0,0xccd801e0,0x25e001e8,0xa958,0x600,
  0xa9e01010,0x3302410,0xa980,0x40040000,0xa9e81050,0x3702450,0xa988,0x40000,0xa9f01080,0x3a02480,0xa990,0x4000000,0xa9f810c0,0x3e024c0,0xa998,0x4000000,
  0xc1e01130,0x3302530,0xa9c0,0x40040000,0xc1e81170,0x3702570,0xa9c8,0x40000,0xc1f011a0,0x3a025a0,0xa9d0,0x4000000,0xc1f811e0,0x3e025e0,0xa9d8,0x4000000,
  0xb5200020,0x1000028,0xae20,0x0,0xb5280060,0x1400068,0xae28,0x60,0xb53000b0,0x19000b8,0xae30,0x0,0xb53800f0,0x1d000f8,0xae38,0x6000,
  0xea400100,0x25000108,0xae60,0x600004,0xea480140,0x25400148,0xae68,0x4,0xea500190,0x25900198,0xae70,0x60000400,0xea5801d0,0x25d001d8,0xae78,0x4000400,
  0xb5601020,0x3002420,0xaea0,0x0,0xb5681060,0x3402460,0xaea8,0x0,0xb57010b0,0x39024b0,0xaeb0,0x0,0xb57810f0,0x3d024f0,0xaeb8,0x0,
  0xb4801100,0x3002500,0xaee0,0x0,0xb4881140,0x3402540,0xaee8,0x0,0xb4901190,0x3902590,0xaef0,0x0,0xb49811d0,0x3d025d0,0xaef8,0x0,
  0xb4c00030,0x1100038,0xaf00,0x4,0xb4c80070,0x1500078,0xaf08,0x40004,0xb4d000a0,0x18000a8,0xaf10,0x40400,0xb4d800e0,0x1c000e8,0xaf18,0x4000400,
  0xb5e00110,0x25100118,0xaf40,0x0,0xb5e80150,0x25500158,0xaf48,0x0,0xb5f00180,0x25800188,0xaf50,0x0,0xb5f801c0,0x25c001c8,0xaf58,0x0,
  0xcde01030,0x3102430,0xaf80,0x0,0xcde81070,0x3502470,0xaf88,0x40,0xcdf010a0,0x38024a0,0xaf90,0x40,0xcdf810e0,0x3c024e0,0xaf98,0x4000,
  0xb5201110,0x3102510,0xafc0,0x0,0xb5281150,0x3502550,0xafc8,0x40,0xb5301180,0x3802580,0xafd0,0x40,0xb53811c0,0x3c025c0,0xafd8,0x4000,
  0x0,0x0,0x0,0x0,0x0,0x0,0x0,0x0,0x0,0x0,0x0,0x0,0x0,0x0,0x0,0x0,
  0x0,0x0,0x0,0x0,0x0,0x0,0x0,0x0,0x0,0x0,0x0,0x0,0x0,0x0,0x0,0x0,
  0x0,0x0,0x0,0x0,0x0,0x0,0x0,0x0,0x0,0x0,0x0,0x0,0x0,0x0,0x0,0x0,
  0x0,0x0,0x0,0x0,0x0,0x0,0x0,0x0,0x0,0x0,0x0,0x0,0x0,0x0,0x0,0x0,
  0x0,0x0,0x0,0x0,0x0,0x0,0x0,0x0,0x0,0x0,0x0,0x0,0x0,0x0,0x0,0x0,
  0x0,0x0,0x0,0x0,0x0,0x0,0x0,0x0,0x0,0x0,0x0,0x0,0x0,0x0,0x0,0x0,
  0x0,0x0,0x0,0x0,0x0,0x0,0x0,0x0,0x0,0x0,0x0,0x0,0x0,0x0,0x0,0x0,
  0x0,0x0,0x0,0x0,0x0,0x0,0x0,0x0,0x0,0x0,0x0,0x0,0x0,0x0,0x0,0x0,
  0x0,0x0,0x0,0x0,0x0,0x0,0x0,0x0,0x0,0x0,0x0,0x0,0x0,0x0,0x0,0x0,
  0x0,0x0,0x0,0x0,0x0,0x0,0x0,0x0,0x0,0x0,0x0,0x0,0x0,0x0,0x0,0x0,
  0x0,0x0,0x0,0x0,0x0,0x0,0x0,0x0,0x0,0x0,0x0,0x0,0x0,0x0,0x0,0x0,
  0x0,0x0,0x0,0x0,0x0,0x0,0x0,0x0,0x0,0x0,0x0,0x0,0x0,0x0,0x0,0x0,
  0x0,0x0,0x0,0x0,0x0,0x0,0x0,0x0,0x0,0x0,0x0,0x0,0x0,0x0,0x0,0x0,
  0x0,0x0,0x0,0x0,0x0,0x0,0x0,0x0,0x0,0x0,0x0,0x0,0x0,0x0,0x0,0x0,
  0x0,0x0,0x0,0x0,0x0,0x0,0x0,0x0,0x0,0x0,0x0,0x0,0x0,0x0,0x0,0x0,
  0x0,0x0,0x0,0x0,0x0,0x0,0x0,0x0,0x0,0x0,0x0,0x0,0x0,0x0,0x0,0x0,
  0x3f90,0x3f90,0x0,0x0,0x0,0x3f90,0x3f90,0x0,0x0,0x0,0x3f90,0x3f90,0x0,0x0,0x0,0x3f90,
  0x0,0xc0420000,0x0,0x0,0x0,0x0,0x0,0x0,0x0,0x0,0x0,0xc0420000,0x0,0x0,0x0,0x0,
  0x3f900000,0x0,0x0,0x0,0x0,0x0,0x3f900000,0x0,0x0,0x0,0x3f900000,0x0,0x0,0x0,0x0,0x0,
  0x3f900000,0x0,0x0,0x0,0x0,0x0,0x3f900000,0x0,0x0,0x0,0x3f900000,0x0,0x0,0x0,0x0,0x0,
  0xc042,0x0,0x0,0x0,0xc0420000,0x0,0x0,0x0,0x0,0x0,0xc042,0x0,0x0,0x0,0xc0420000,0x0,
  0x0,0x0,0x0,0x0,0x0,0x3f900000,0x0,0x0,0x0,0x3f900000,0x0,0x0,0x0,0x0,0x0,0x3f900000,
  0x0,0x0,0x0,0x0,0x0,0xc042,0x0,0x0,0x0,0x0,0x0,0x0,0x0,0x0,0x0,0xc042,
  0x0,0x0,0x0,0x0,0x0,0xc042,0x0,0x0,0x0,0x0,0x0,0x0,0x0,0x0,0x0,0xc042,
  0x0,0x0,0x0,0x0,0x0,0x0,0x0,0x0,0x0,0x0,0x0,0x0,0x0,0x0,0x3f90,0x0,
  0x0,0x0,0x0,0x0,0x0,0xc0420000,0x0,0x0,0x0,0x0,0x0,0x0,0x0,0x0,0x0,0xc0420000,
  0x0,0x3f90,0x0,0x0,0x0,0x3f90,0x0,0x0,0x0,0x0,0x0,0x3f90,0x0,0x0,0x3f900000,0x3f90,
  0x0,0x3f90,0x0,0x0,0x0,0x3f90,0x0,0x0,0x0,0x0,0x0,0x3f90,0x0,0x0,0x3f900000,0x3f90,
  0x0,0x0,0x0,0x0,0x0,0x0,0x0,0x0,0x0,0x0,0x0,0x0,0x0,0x0,0x0,0x0,
  0x0,0x3f90,0x3f900000,0x0,0x0,0x3f90,0x0,0x0,0x0,0x0,0x0,0x3f90,0x0,0x0,0x0,0x3f90,
  0xc042,0x0,0x0,0x0,0x0,0x0,0x0,0x0,0x0,0x0,0xc042,0x0,0x0,0x0,0x0,0x0,
  0xc042,0x0,0x0,0x0,0x0,0x0,0x0,0x0,0x0,0x0,0xc042,0x0,0x0,0x0,0x0,0x0,
  0xc0000000,0x26001200,0x19200,0x44,0xc0080040,0x26401240,0x19208,0x440,0xc0100090,0x26901290,0x19210,0x4400,0xc01800d0,0x26d012d0,0x19218,0x4000,
  0xd8000120,0x4001320,0x19240,0x600000,0xd8080160,0x4401360,0x19248,0x0,0xd81001b0,0x49013b0,0x19250,0x60000000,0xd81801f0,0x4d013f0,0x19258,0x0,
  0xc1201000,0x28003608,0x19280,0x40000,0xc1281040,0x28403648,0x19288,0x4000000,0xc1301090,0x28903698,0x19290,0x4000000,0xc13810d0,0x28d036d8,0x19298,0x0,
  0xd8401120,0x28003728,0x192c0,0x40000,0xd8481160,0x28403768,0x192c8,0x4000000,0xd85011b0,0x289037b8,0x192d0,0x4000000,0xd85811f0,0x28d037f8,0x192d8,0x0,
  0xc0800010,0x26101210,0x19320,0x6,0xc0880050,0x26501250,0x19328,0x60000,0xc0900080,0x26801280,0x19330,0x600,0xc09800c0,0x26c012c0,0x19338,0x6000000,
  0xc1a00130,0x4101330,0x19360,0x0,0xc1a80170,0x4501370,0x19368,0x400000,0xc1b001a0,0x48013a0,0x19370,0x400000,0xc1b801e0,0x4c013e0,0x19378,0x40000000,
  0xd9a01010,0x28103618,0x193a0,0x0,0xd9a81050,0x28503658,0x193a8,0x60,0xd9b01080,0x28803688,0x193b0,0x0,0xd9b810c0,0x28c036c8,0x193b8,0x6000,
  0xd9e01130,0x28103738,0x193e0,0x0,0xd9e81170,0x28503778,0x193e8,0x60,0xd9f011a0,0x288037a8,0x193f0,0x0,0xd9f811e0,0x28c037e8,0x193f8,0x6000,
  0xc6000020,0x26201220,0x19800,0x0,0xc6080060,0x26601260,0x19808,0x0,0xc61000b0,0x26b012b0,0x19810,0x0,0xc61800f0,0x26f012f0,0x19818,0x400,
  0xde000100,0x4201300,0x19840,0x0,0xde080140,0x4601340,0x19848,0x600000,0xde100190,0x4b01390,0x19850,0x0,0xde1801d0,0x4f013d0,0x19858,0x60000000,
  0xd9201020,0x28203628,0x19880,0x40,0xd9281060,0x28603668,0x19888,0x40,0xd93010b0,0x28b036b8,0x19890,0x4000,0xd93810f0,0x28f036f8,0x19898,0x4004000,
  0xd9601100,0x28203708,0x198c0,0x40,0xd9681140,0x28603748,0x198c8,0x40,0xd9701190,0x28b03798,0x198d0,0x4000,0xd97811d0,0x28f037d8,0x198d8,0x4004000,
  0xd8800030,0x26301230,0x19920,0x0,0xd8880070,0x26701270,0x19928,0x0,0xd89000a0,0x26a012a0,0x19930,0x0,0xd89800e0,0x26e012e0,0x19938,0x0,
  0xeba00110,0x4301310,0x19960,0x4000040,0xeba80150,0x4701350,0x19968,0x40,0xebb00180,0x4a01380,0x19970,0x4000,0xebb801c0,0x4e013c0,0x19978,0x4000,
  0xd8c01030,0x28303638,0x199a0,0x6,0xd8c81070,0x28703678,0x199a8,0x0,0xd8d010a0,0x28a036a8,0x199b0,0x600,0xd8d810e0,0x28e036e8,0x199b8,0x0,
  0xc6001110,0x28303718,0x199e0,0x6,0xc6081150,0x28703758,0x199e8,0x0,0xc6101180,0x28a03788,0x199f0,0x600,0xc61811c0,0x28e037c8,0x199f8,0x0,
  0x0,0x0,0x0,0x0,0x0,0x0,0x0,0x0,0x0,0x0,0x0,0x0,0x0,0x0,0x0,0x0,
  0x0,0x0,0x0,0x0,0x0,0x0,0x0,0x0,0x0,0x0,0x0,0x0,0x0,0x0,0x0,0x0,
  0x0,0x0,0x0,0x0,0x0,0x0,0x0,0x0,0x0,0x0,0x0,0x0,0x0,0x0,0x0,0x0,
  0x0,0x0,0x0,0x0,0x0,0x0,0x0,0x0,0x0,0x0,0x0,0x0,0x0,0x0,0x0,0x0,
  0x0,0x0,0x0,0x0,0x0,0x0,0x0,0x0,0x0,0x0,0x0,0x0,0x0,0x0,0x0,0x0,
  0x0,0x0,0x0,0x0,0x0,0x0,0x0,0x0,0x0,0x0,0x0,0x0,0x0,0x0,0x0,0x0,
  0x0,0x0,0x0,0x0,0x0,0x0,0x0,0x0,0x0,0x0,0x0,0x0,0x0,0x0,0x0,0x0,
  0x0,0x0,0x0,0x0,0x0,0x0,0x0,0x0,0x0,0x0,0x0,0x0,0x0,0x0,0x0,0x0,
  0x0,0x0,0x0,0x0,0x0,0x0,0x0,0x0,0x0,0x0,0x0,0x0,0x0,0x0,0x0,0x0,
  0x0,0x0,0x0,0x0,0x0,0x0,0x0,0x0,0x0,0x0,0x0,0x0,0x0,0x0,0x0,0x0,
  0x0,0x0,0x0,0x0,0x0,0x0,0x0,0x0,0x0,0x0,0x0,0x0,0x0,0x0,0x0,0x0,
  0x0,0x0,0x0,0x0,0x0,0x0,0x0,0x0,0x0,0x0,0x0,0x0,0x0,0x0,0x0,0x0,
  0x0,0x0,0x0,0x0,0x0,0x0,0x0,0x0,0x0,0x0,0x0,0x0,0x0,0x0,0x0,0x0,
  0x0,0x0,0x0,0x0,0x0,0x0,0x0,0x0,0x0,0x0,0x0,0x0,0x0,0x0,0x0,0x0,
  0x0,0x0,0x0,0x0,0x0,0x0,0x0,0x0,0x0,0x0,0x0,0x0,0x0,0x0,0x0,0x0,
  0x0,0x0,0x0,0x0,0x0,0x0,0x0,0x0,0x0,0x0,0x0,0x0,0x0,0x0,0x0,0x0,
  0x3f903f90,0x0,0x0,0x0,0x3f900000,0x0,0x3f90,0x0,0x0,0x0,0x3f903f90,0x0,0x0,0x0,0x3f900000,0x0,
  0x0,0xc042,0x0,0x0,0x0,0x0,0x0,0x0,0x0,0x0,0x0,0xc042,0x0,0x0,0x0,0x0,
  0x0,0x0,0x0,0x0,0x0,0x0,0x0,0x0,0x0,0x0,0x0,0x0,0x0,0x0,0x0,0x3f900000,
  0x0,0x0,0x0,0x0,0x0,0x0,0x0,0x0,0x0,0x0,0x0,0x0,0x0,0x0,0x0,0x3f900000,
  0x0,0x0,0x0,0x0,0x0,0x0,0x0,0x0,0x0,0x0,0x0,0x0,0x0,0x0,0x0,0x0,
  0x0,0x3f90,0x0,0x3f900000,0x0,0x3f90,0x0,0x0,0x0,0x0,0x0,0x3f90,0x0,0x0,0x0,0x3f90,
  0xc042,0x0,0x0,0x0,0xc0420000,0x0,0x0,0x0,0x0,0x0,0xc042,0x0,0x0,0x0,0xc0420000,0x0,
  0xc042,0x0,0x0,0x0,0xc0420000,0x0,0x0,0x0,0x0,0x0,0xc042,0x0,0x0,0x0,0xc0420000,0x0,
  0x0,0xc0420000,0x0,0x0,0x0,0x0,0x0,0x0,0x0,0x0,0x0,0xc0420000,0x0,0x0,0x0,0x0,
  0x0,0x0,0x0,0x0,0x0,0xc042,0x0,0x0,0x0,0x0,0x0,0x0,0x0,0x0,0x0,0xc042,
  0x3f903f90,0x0,0x0,0x0,0x3f900000,0x0,0x3f90,0x0,0x0,0x0,0x3f903f90,0x0,0x0,0x0,0x3f900000,0x0,
  0x3f903f90,0x0,0x0,0x0,0x3f900000,0x0,0x3f90,0x0,0x0,0x0,0x3f903f90,0x0,0x0,0x0,0x3f900000,0x0,
  0x0,0x0,0x0,0x0,0xc042,0x0,0x0,0x0,0x0,0x0,0x0,0x0,0x0,0x0,0x3f90c042,0x0,
  0x0,0x0,0x0,0x0,0x0,0x3f90,0x0,0x0,0x0,0x3f90,0x0,0x0,0x0,0x0,0x0,0x3f90,
  0x0,0x0,0x0,0x0,0x0,0x0,0x0,0x0,0x0,0x0,0x0,0x0,0x0,0x0,0x0,0x0,
  0x0,0x0,0x0,0x0,0x0,0x0,0x0,0x0,0x0,0x0,0x0,0x0,0x0,0x0,0x0,0x0,
  0xcc000000,0x27201208,0x19220,0x40004,0xcc080040,0x27601248,0x19228,0x40400,0xcc100090,0x27b01298,0x19230,0x4000400,0xcc1800d0,0x27f012d8,0x19238,0x4000000,
  0xcd200120,0x5201328,0x19260,0x60,0xcd280160,0x5601368,0x19268,0x0,0xcd3001b0,0x5b013b8,0x19270,0x6000,0xcd3801f0,0x5f013f8,0x19278,0x0,
  0xe4401000,0x29203600,0x192a0,0x0,0xe4481040,0x29603640,0x192a8,0x0,0xe4501090,0x29b03690,0x192b0,0x0,0xe45810d0,0x29f036d0,0x192b8,0x40000000,
  0xeb601120,0x29203720,0x192e0,0x0,0xeb681160,0x29603760,0x192e8,0x0,0xeb7011b0,0x29b037b0,0x192f0,0x0,0xeb7811f0,0x29f037f0,0x192f8,0x40000000,
  0xe4800010,0x27301218,0x19300,0x0,0xe4880050,0x27701258,0x19308,0x0,0xe4900080,0x27a01288,0x19310,0x0,0xe49800c0,0x27e012c8,0x19318,0x0,
  0xe5a00130,0x5301338,0x19340,0x40000040,0xe5a80170,0x5701378,0x19348,0x40,0xe5b001a0,0x5a013a8,0x19350,0x4000,0xe5b801e0,0x5e013e8,0x19358,0x4000,
  0xe4c01010,0x29303610,0x19380,0x6,0xe4c81050,0x29703650,0x19388,0x60000,0xe4d01080,0x29a03680,0x19390,0x600,0xe4d810c0,0x29e036c0,0x19398,0x6000000,
  0xd9e01130,0x29303730,0x193c0,0x6,0xd9e81170,0x29703770,0x193c8,0x60000,0xd9f011a0,0x29a037a0,0x193d0,0x600,0xd9f811e0,0x29e037e0,0x193d8,0x6000000,
  0xe4000020,0x27001228,0x19820,0x600000,0xe4080060,0x27401268,0x19828,0x0,0xe41000b0,0x279012b8,0x19830,0x60000000,0xe41800f0,0x27d012f8,0x19838,0x0,
  0xe5200100,0x5001308,0x19860,0x0,0xe5280140,0x5401348,0x19868,0x60,0xe5300190,0x5901398,0x19870,0x0,0xe53801d0,0x5d013d8,0x19878,0x6000,
  0xe5601020,0x29003620,0x198a0,0x40004,0xe5681060,0x29403660,0x198a8,0x40400,0xe57010b0,0x299036b0,0x198b0,0x4000400,0xe57810f0,0x29d036f0,0x198b8,0x4000000,
  0xcc801100,0x29003700,0x198e0,0x40004,0xcc881140,0x29403740,0x198e8,0x40400,0xcc901190,0x29903790,0x198f0,0x4000400,0xcc9811d0,0x29d037d0,0x198f8,0x4000000,
  0xcda00030,0x27101238,0x19900,0x0,0xcda80070,0x27501278,0x19908,0x6,0xcdb000a0,0x278012a8,0x19910,0x0,0xcdb800e0,0x27c012e8,0x19918,0x4000600,
  0xeac00110,0x5101318,0x19940,0x0,0xeac80150,0x5501358,0x19948,0x40,0xead00180,0x5801388,0x19950,0x40,0xead801c0,0x5c013c8,0x19958,0x4000,
  0xe5e01030,0x29103630,0x19980,0x0,0xe5e81070,0x29503670,0x19988,0x0,0xe5f010a0,0x298036a0,0x19990,0x0,0xe5f810e0,0x29c036e0,0x19998,0x0,
  0xe4001110,0x29103710,0x199c0,0x0,0xe4081150,0x29503750,0x199c8,0x0,0xe4101180,0x29803780,0x199d0,0x0,0xe41811c0,0x29c037c0,0x199d8,0x0,
  0x0,0x0,0x800,0x10000000,0x10,0x90,0x890,0x10000000,0x20,0x120,0x920,0x10000000,0x30,0x1b0,0x9b0,0x10000000,
  0x40,0x240,0xa40,0x20003180,0x50,0x2d0,0xad0,0x20003190,0x60,0x360,0xb60,0x200031a0,0x70,0x3f0,0xbf0,0x200031b0,
  0x610,0xc00,0x1400,0x40000000,0x600,0xc90,0x1490,0x40000010,0x630,0xd20,0x1520,0x40000020,0x620,0xdb0,0x15b0,0x40000030,
  0x650,0xe40,0x1640,0x20033180,0x640,0xed0,0x16d0,0x20033190,0x670,0xf60,0x1760,0x200331a0,0x660,0xff0,0x17f0,0x200331b0,
  0xc20,0x10,0x808,0x40030020,0xc30,0x80,0x898,0x40030030,0xc00,0x130,0x928,0x40030000,0xc10,0x1a0,0x9b8,0x40030010,
  0xc60,0x250,0xa48,0x200631a0,0xc70,0x2c0,0xad8,0x200631b0,0xc40,0x370,0xb68,0x20063180,0xc50,0x3e0,0xbf8,0x20063190,
  0x1230,0xc10,0x1408,0x40060020,0x1220,0xc80,0x1498,0x40060030,0x1210,0xd30,0x1528,0x40060000,0x1200,0xda0,0x15b8,0x40060010,
  0x1270,0xe50,0x1648,0x10000000,0x1260,0xec0,0x16d8,0x10000000,0x1250,0xf70,0x1768,0x10000000,0x1240,0xfe0,0x17f8,0x10000000,
  0x1840,0x20,0x810,0x10000000,0x1850,0xb0,0x880,0x10000000,0x1860,0x100,0x930,0x10000000,0x1870,0x190,0x9a0,0x10000000,
  0x1800,0x260,0xa50,0x200031c0,0x1810,0x2f0,0xac0,0x200031d0,0x1820,0x340,0xb70,0x200031e0,0x1830,0x3d0,0xbe0,0x200031f0,
  0x1e50,0xc20,0x1410,0x40000040,0x1e40,0xcb0,0x1480,0x40000050,0x1e70,0xd00,0x1530,0x40000060,0x1e60,0xd90,0x15a0,0x40000070,
  0x1e10,0xe60,0x1650,0x200331c0,0x1e00,0xef0,0x16c0,0x200331d0,0x1e30,0xf40,0x1770,0x200331e0,0x1e20,0xfd0,0x17e0,0x200331f0,
  0x2460,0x30,0x818,0x40030060,0x2470,0xa0,0x888,0x40030070,0x2440,0x110,0x938,0x40030040,0x2450,0x180,0x9a8,0x40030050,
  0x2420,0x270,0xa58,0x200631e0,0x2430,0x2e0,0xac8,0x200631f0,0x2400,0x350,0xb78,0x200631c0,0x2410,0x3c0,0xbe8,0x200631d0,
  0x2a70,0xc30,0x1418,0x40060060,0x2a60,0xca0,0x1488,0x40060070,0x2a50,0xd10,0x1538,0x40060040,0x2a40,0xd80,0x15a8,0x40060050,
  0x2a30,0xe70,0x1658,0x10000000,0x2a20,0xee0,0x16c8,0x10000000,0x2a10,0xf50,0x1778,0x10000000,0x2a00,0xfc0,0x17e8,0x10000000,
  0xbfd8bf90,0x3f403f00,0x0,0x0,0x0,0x0,0x0,0x0,0x0,0x0,0x0,0x0,0x0,0x0,0x0,0x0,
  0x0,0x0,0xbfd8bf90,0x3f403f00,0x0,0x0,0x0,0x0,0x0,0x0,0x0,0x0,0x0,0x0,0x0,0x0,
  0xbfd83f90,0x3f40bf00,0x0,0x0,0x0,0x0,0x0,0x0,0x0,0x0,0x0,0x0,0x0,0x0,0x0,0x0,
  0x0,0x0,0xbfd83f90,0x3f40bf00,0x0,0x0,0x0,0x0,0x0,0x0,0x0,0x0,0x0,0x0,0x0,0x0,
  0x0,0x0,0x0,0x0,0xbfd8bf90,0x3f403f00,0x0,0x0,0x0,0x0,0x0,0x0,0x0,0x0,0x0,0x0,
  0x0,0x0,0x0,0x0,0x0,0x0,0xbfd8bf90,0x3f403f00,0x0,0x0,0x0,0x0,0x0,0x0,0x0,0x0,
  0x0,0x0,0x0,0x0,0xbfd83f90,0x3f40bf00,0x0,0x0,0x0,0x0,0x0,0x0,0x0,0x0,0x0,0x0,
  0x0,0x0,0x0,0x0,0x0,0x0,0xbfd83f90,0x3f40bf00,0x0,0x0,0x0,0x0,0x0,0x0,0x0,0x0,
  0x0,0x0,0x0,0x0,0x0,0x0,0x0,0x0,0xbfd8bf90,0x3f403f00,0x0,0x0,0x0,0x0,0x0,0x0,
  0x0,0x0,0x0,0x0,0x0,0x0,0x0,0x0,0x0,0x0,0xbfd8bf90,0x3f403f00,0x0,0x0,0x0,0x0,
  0x0,0x0,0x0,0x0,0x0,0x0,0x0,0x0,0xbfd83f90,0x3f40bf00,0x0,0x0,0x0,0x0,0x0,0x0,
  0x0,0x0,0x0,0x0,0x0,0x0,0x0,0x0,0x0,0x0,0xbfd83f90,0x3f40bf00,0x0,0x0,0x0,0x0,
  0x0,0x0,0x0,0x0,0x0,0x0,0x0,0x0,0x0,0x0,0x0,0x0,0xbfd8bf90,0x3f403f00,0x0,0x0,
  0x0,0x0,0x0,0x0,0x0,0x0,0x0,0x0,0x0,0x0,0x0,0x0,0x0,0x0,0xbfd8bf90,0x3f403f00,
  0x0,0x0,0x0,0x0,0x0,0x0,0x0,0x0,0x0,0x0,0x0,0x0,0xbfd83f90,0x3f40bf00,0x0,0x0,
  0x0,0x0,0x0,0x0,0x0,0x0,0x0,0x0,0x0,0x0,0x0,0x0,0x0,0x0,0xbfd83f90,0x3f40bf00,
  0xa800,0xa820,0x0,0xa800,0xa808,0xa828,0x40,0xa840,0xa810,0xa830,0x90,0xa890,0xa818,0xa838,0xd0,0xa8d0,
  0xa920,0xa900,0x2400,0xae00,0xa928,0xa908,0x2440,0xae40,0xa930,0xa910,0x2490,0xae90,0xa938,0xa918,0x24d0,0xaed0,
  0xa840,0xa860,0x200,0xb400,0xa848,0xa868,0x240,0xb440,0xa850,0xa870,0x290,0xb490,0xa858,0xa878,0x2d0,0xb4d0,
  0xa960,0xa940,0x200,0xb400,0xa968,0xa948,0x240,0xb440,0xa970,0xa950,0x290,0xb490,0xa978,0xa958,0x2d0,0xb4d0,
  0xea80,0xeaa0,0x10,0xa810,0xea88,0xeaa8,0x50,0xa850,0xea90,0xeab0,0x80,0xa880,0xea98,0xeab8,0xc0,0xa8c0,
  0xa9a0,0xa980,0x2410,0xae10,0xa9a8,0xa988,0x2450,0xae50,0xa9b0,0xa990,0x2480,0xae80,0xa9b8,0xa998,0x24c0,0xaec0,
  0xc0c0,0xc0e0,0x210,0xb410,0xc0c8,0xc0e8,0x250,0xb450,0xc0d0,0xc0f0,0x280,0xb480,0xc0d8,0xc0f8,0x2c0,0xb4c0,
  0xc1e0,0xc1c0,0x210,0xb410,0xc1e8,0xc1c8,0x250,0xb450,0xc1f0,0xc1d0,0x280,0xb480,0xc1f8,0xc1d8,0x2c0,0xb4c0,
  0xae00,0xae20,0x20,0xa820,0xae08,0xae28,0x60,0xa860,0xae10,0xae30,0xb0,0xa8b0,0xae18,0xae38,0xf0,0xa8f0,
  0xeb20,0xeb00,0x2420,0xae20,0xeb28,0xeb08,0x2460,0xae60,0xeb30,0xeb10,0x24b0,0xaeb0,0xeb38,0xeb18,0x24f0,0xaef0,
  0xc040,0xc060,0x220,0xb420,0xc048,0xc068,0x260,0xb460,0xc050,0xc070,0x2b0,0xb4b0,0xc058,0xc078,0x2f0,0xb4f0,
  0xc160,0xc140,0x220,0xb420,0xc168,0xc148,0x260,0xb460,0xc170,0xc150,0x2b0,0xb4b0,0xc178,0xc158,0x2f0,0xb4f0,
  0xa880,0xa8a0,0x30,0xa830,0xa888,0xa8a8,0x70,0xa870,0xa890,0xa8b0,0xa0,0xa8a0,0xa898,0xa8b8,0xe0,0xa8e0,
  0xa8c0,0xa8e0,0x2430,0xae30,0xa8c8,0xa8e8,0x2470,0xae70,0xa8d0,0xa8f0,0x24a0,0xaea0,0xa8d8,0xa8f8,0x24e0,0xaee0,
  0xa9e0,0xa9c0,0x230,0xb430,0xa9e8,0xa9c8,0x270,0xb470,0xa9f0,0xa9d0,0x2a0,0xb4a0,0xa9f8,0xa9d8,0x2e0,0xb4e0,
  0xae00,0xae20,0x230,0xb430,0xae08,0xae28,0x270,0xb470,0xae10,0xae30,0x2a0,0xb4a0,0xae18,0xae38,0x2e0,0xb4e0,
  0x180000,0x40,0x820,0x10000000,0x180010,0xd0,0x8b0,0x10000000,0x180020,0x160,0x900,0x10000000,0x180030,0x1f0,0x990,0x10000000,
  0x180040,0x200,0xa60,0x20093180,0x180050,0x290,0xaf0,0x20093190,0x180060,0x320,0xb40,0x200931a0,0x180070,0x3b0,0xbd0,0x200931b0,
  0x180610,0xc40,0x1420,0x40090000,0x180600,0xcd0,0x14b0,0x40090010,0x180630,0xd60,0x1500,0x40090020,0x180620,0xdf0,0x1590,0x40090030,
  0x180650,0xe00,0x1660,0x200c3180,0x180640,0xe90,0x16f0,0x200c3190,0x180670,0xf20,0x1740,0x200c31a0,0x180660,0xfb0,0x17d0,0x200c31b0,
  0x180c20,0x50,0x828,0x400c0020,0x180c30,0xc0,0x8b8,0x400c0030,0x180c00,0x170,0x908,0x400c0000,0x180c10,0x1e0,0x998,0x400c0010,
  0x180c60,0x210,0xa68,0x200f31a0,0x180c70,0x280,0xaf8,0x200f31b0,0x180c40,0x330,0xb48,0x200f3180,0x180c50,0x3a0,0xbd8,0x200f3190,
  0x181230,0xc50,0x1428,0x400f0020,0x181220,0xcc0,0x14b8,0x400f0030,0x181210,0xd70,0x1508,0x400f0000,0x181200,0xde0,0x1598,0x400f0010,
  0x181270,0xe10,0x1668,0x10000000,0x181260,0xe80,0x16f8,0x10000000,0x181250,0xf30,0x1748,0x10000000,0x181240,0xfa0,0x17d8,0x10000000,
  0x181840,0x60,0x830,0x10000000,0x181850,0xf0,0x8a0,0x10000000,0x181860,0x140,0x910,0x10000000,0x181870,0x1d0,0x980,0x10000000,
  0x181800,0x220,0xa70,0x200931c0,0x181810,0x2b0,0xae0,0x200931d0,0x181820,0x300,0xb50,0x200931e0,0x181830,0x390,0xbc0,0x200931f0,
  0x181e50,0xc60,0x1430,0x40090040,0x181e40,0xcf0,0x14a0,0x40090050,0x181e70,0xd40,0x1510,0x40090060,0x181e60,0xdd0,0x1580,0x40090070,
  0x181e10,0xe20,0x1670,0x200c31c0,0x181e00,0xeb0,0x16e0,0x200c31d0,0x181e30,0xf00,0x1750,0x200c31e0,0x181e20,0xf90,0x17c0,0x200c31f0,
  0x182460,0x70,0x838,0x400c0060,0x182470,0xe0,0x8a8,0x400c0070,0x182440,0x150,0x918,0x400c0040,0x182450,0x1c0,0x988,0x400c0050,
  0x182420,0x230,0xa78,0x200f31e0,0x182430,0x2a0,0xae8,0x200f31f0,0x182400,0x310,0xb58,0x200f31c0,0x182410,0x380,0xbc8,0x200f31d0,
  0x182a70,0xc70,0x1438,0x400f0060,0x182a60,0xce0,0x14a8,0x400f0070,0x182a50,0xd50,0x1518,0x400f0040,0x182a40,0xdc0,0x1588,0x400f0050,
  0x182a30,0xe30,0x1678,0x10000000,0x182a20,0xea0,0x16e8,0x10000000,0x182a10,0xf10,0x1758,0x10000000,0x182a00,0xf80,0x17c8,0x10000000,
  0xbfd8bf90,0x3f403f00,0x0,0x0,0x0,0x0,0x0,0x0,0x0,0x0,0x0,0x0,0x0,0x0,0x0,0x0,
  0x0,0x0,0xbfd8bf90,0x3f403f00,0x0,0x0,0x0,0x0,0x0,0x0,0x0,0x0,0x0,0x0,0x0,0x0,
  0xbfd83f90,0x3f40bf00,0x0,0x0,0x0,0x0,0x0,0x0,0x0,0x0,0x0,0x0,0x0,0x0,0x0,0x0,
  0x0,0x0,0xbfd83f90,0x3f40bf00,0x0,0x0,0x0,0x0,0x0,0x0,0x0,0x0,0x0,0x0,0x0,0x0,
  0x0,0x0,0x0,0x0,0xbfd8bf90,0x3f403f00,0x0,0x0,0x0,0x0,0x0,0x0,0x0,0x0,0x0,0x0,
  0x0,0x0,0x0,0x0,0x0,0x0,0xbfd8bf90,0x3f403f00,0x0,0x0,0x0,0x0,0x0,0x0,0x0,0x0,
  0x0,0x0,0x0,0x0,0xbfd83f90,0x3f40bf00,0x0,0x0,0x0,0x0,0x0,0x0,0x0,0x0,0x0,0x0,
  0x0,0x0,0x0,0x0,0x0,0x0,0xbfd83f90,0x3f40bf00,0x0,0x0,0x0,0x0,0x0,0x0,0x0,0x0,
  0x0,0x0,0x0,0x0,0x0,0x0,0x0,0x0,0xbfd8bf90,0x3f403f00,0x0,0x0,0x0,0x0,0x0,0x0,
  0x0,0x0,0x0,0x0,0x0,0x0,0x0,0x0,0x0,0x0,0xbfd8bf90,0x3f403f00,0x0,0x0,0x0,0x0,
  0x0,0x0,0x0,0x0,0x0,0x0,0x0,0x0,0xbfd83f90,0x3f40bf00,0x0,0x0,0x0,0x0,0x0,0x0,
  0x0,0x0,0x0,0x0,0x0,0x0,0x0,0x0,0x0,0x0,0xbfd83f90,0x3f40bf00,0x0,0x0,0x0,0x0,
  0x0,0x0,0x0,0x0,0x0,0x0,0x0,0x0,0x0,0x0,0x0,0x0,0xbfd8bf90,0x3f403f00,0x0,0x0,
  0x0,0x0,0x0,0x0,0x0,0x0,0x0,0x0,0x0,0x0,0x0,0x0,0x0,0x0,0xbfd8bf90,0x3f403f00,
  0x0,0x0,0x0,0x0,0x0,0x0,0x0,0x0,0x0,0x0,0x0,0x0,0xbfd83f90,0x3f40bf00,0x0,0x0,
  0x0,0x0,0x0,0x0,0x0,0x0,0x0,0x0,0x0,0x0,0x0,0x0,0x0,0x0,0xbfd83f90,0x3f40bf00,
  0xb400,0xb420,0x120,0xa920,0xb408,0xb428,0x160,0xa960,0xb410,0xb430,0x1b0,0xa9b0,0xb418,0xb438,0x1f0,0xa9f0,
  0xb440,0xb460,0x2520,0xaf20,0xb448,0xb468,0x2560,0xaf60,0xb450,0xb470,0x25b0,0xafb0,0xb458,0xb478,0x25f0,0xaff0,
  0xcc40,0xcc60,0x320,0xb520,0xcc48,0xcc68,0x360,0xb560,0xcc50,0xcc70,0x3b0,0xb5b0,0xcc58,0xcc78,0x3f0,0xb5f0,
  0xcd60,0xcd40,0x320,0xb520,0xcd68,0xcd48,0x360,0xb560,0xcd70,0xcd50,0x3b0,0xb5b0,0xcd78,0xcd58,0x3f0,0xb5f0,
  0xb5a0,0xb580,0x130,0xa930,0xb5a8,0xb588,0x170,0xa970,0xb5b0,0xb590,0x1a0,0xa9a0,0xb5b8,0xb598,0x1e0,0xa9e0,
  0xccc0,0xcce0,0x2530,0xaf30,0xccc8,0xcce8,0x2570,0xaf70,0xccd0,0xccf0,0x25a0,0xafa0,0xccd8,0xccf8,0x25e0,0xafe0,
  0xa9e0,0xa9c0,0x330,0xb530,0xa9e8,0xa9c8,0x370,0xb570,0xa9f0,0xa9d0,0x3a0,0xb5a0,0xa9f8,0xa9d8,0x3e0,0xb5e0,
  0xc1e0,0xc1c0,0x330,0xb530,0xc1e8,0xc1c8,0x370,0xb570,0xc1f0,0xc1d0,0x3a0,0xb5a0,0xc1f8,0xc1d8,0x3e0,0xb5e0,
  0xb520,0xb500,0x100,0xa900,0xb528,0xb508,0x140,0xa940,0xb530,0xb510,0x190,0xa990,0xb538,0xb518,0x1d0,0xa9d0,
  0xea40,0xea60,0x2500,0xaf00,0xea48,0xea68,0x2540,0xaf40,0xea50,0xea70,0x2590,0xaf90,0xea58,0xea78,0x25d0,0xafd0,
  0xb560,0xb540,0x300,0xb500,0xb568,0xb548,0x340,0xb540,0xb570,0xb550,0x390,0xb590,0xb578,0xb558,0x3d0,0xb5d0,
  0xb480,0xb4a0,0x300,0xb500,0xb488,0xb4a8,0x340,0xb540,0xb490,0xb4b0,0x390,0xb590,0xb498,0xb4b8,0x3d0,0xb5d0,
  0xb4c0,0xb4e0,0x110,0xa910,0xb4c8,0xb4e8,0x150,0xa950,0xb4d0,0xb4f0,0x180,0xa980,0xb4d8,0xb4f8,0x1c0,0xa9c0,
  0xb5e0,0xb5c0,0x2510,0xaf10,0xb5e8,0xb5c8,0x2550,0xaf50,0xb5f0,0xb5d0,0x2580,0xaf80,0xb5f8,0xb5d8,0x25c0,0xafc0,
  0xcde0,0xcdc0,0x310,0xb510,0xcde8,0xcdc8,0x350,0xb550,0xcdf0,0xcdd0,0x380,0xb580,0xcdf8,0xcdd8,0x3c0,0xb5c0,
  0xb520,0xb500,0x310,0xb510,0xb528,0xb508,0x350,0xb550,0xb530,0xb510,0x380,0xb580,0xb538,0xb518,0x3c0,0xb5c0,
  0x0,0x400,0x840,0x10000000,0x10,0x490,0x8d0,0x10000000,0x20,0x520,0x960,0x10000000,0x30,0x5b0,0x9f0,0x10000000,
  0x40,0x640,0xa00,0x20123180,0x50,0x6d0,0xa90,0x20123190,0x60,0x760,0xb20,0x201231a0,0x70,0x7f0,0xbb0,0x201231b0,
  0x610,0x1000,0x1440,0x40120000,0x600,0x1090,0x14d0,0x40120010,0x630,0x1120,0x1560,0x40120020,0x620,0x11b0,0x15f0,0x40120030,
  0x650,0x1240,0x1600,0x20153180,0x640,0x12d0,0x1690,0x20153190,0x670,0x1360,0x1720,0x201531a0,0x660,0x13f0,0x17b0,0x201531b0,
  0xc20,0x410,0x848,0x40150020,0xc30,0x480,0x8d8,0x40150030,0xc00,0x530,0x968,0x40150000,0xc10,0x5a0,0x9f8,0x40150010,
  0xc60,0x650,0xa08,0x201831a0,0xc70,0x6c0,0xa98,0x201831b0,0xc40,0x770,0xb28,0x20183180,0xc50,0x7e0,0xbb8,0x20183190,
  0x1230,0x1010,0x1448,0x40180020,0x1220,0x1080,0x14d8,0x40180030,0x1210,0x1130,0x1568,0x40180000,0x1200,0x11a0,0x15f8,0x40180010,
  0x1270,0x1250,0x1608,0x10000000,0x1260,0x12c0,0x1698,0x10000000,0x1250,0x1370,0x1728,0x10000000,0x1240,0x13e0,0x17b8,0x10000000,
  0x1840,0x420,0x850,0x10000000,0x1850,0x4b0,0x8c0,0x10000000,0x1860,0x500,0x970,0x10000000,0x1870,0x590,0x9e0,0x10000000,
  0x1800,0x660,0xa10,0x201231c0,0x1810,0x6f0,0xa80,0x201231d0,0x1820,0x740,0xb30,0x201231e0,0x1830,0x7d0,0xba0,0x201231f0,
  0x1e50,0x1020,0x1450,0x40120040,0x1e40,0x10b0,0x14c0,0x40120050,0x1e70,0x1100,0x1570,0x40120060,0x1e60,0x1190,0x15e0,0x40120070,
  0x1e10,0x1260,0x1610,0x201531c0,0x1e00,0x12f0,0x1680,0x201531d0,0x1e30,0x1340,0x1730,0x201531e0,0x1e20,0x13d0,0x17a0,0x201531f0,
  0x2460,0x430,0x858,0x40150060,0x2470,0x4a0,0x8c8,0x40150070,0x2440,0x510,0x978,0x40150040,0x2450,0x580,0x9e8,0x40150050,
  0x2420,0x670,0xa18,0x201831e0,0x2430,0x6e0,0xa88,0x201831f0,0x2400,0x750,0xb38,0x201831c0,0x2410,0x7c0,0xba8,0x201831d0,
  0x2a70,0x1030,0x1458,0x40180060,0x2a60,0x10a0,0x14c8,0x40180070,0x2a50,0x1110,0x1578,0x40180040,0x2a40,0x1180,0x15e8,0x40180050,
  0x2a30,0x1270,0x1618,0x10000000,0x2a20,0x12e0,0x1688,0x10000000,0x2a10,0x1350,0x1738,0x10000000,0x2a00,0x13c0,0x17a8,0x10000000,
  0xbfd8bf90,0x3f403f00,0x0,0x0,0x0,0x0,0x0,0x0,0x0,0x0,0x0,0x0,0x0,0x0,0x0,0x0,
  0x0,0x0,0xbfd8bf90,0x3f403f00,0x0,0x0,0x0,0x0,0x0,0x0,0x0,0x0,0x0,0x0,0x0,0x0,
  0xbfd83f90,0x3f40bf00,0x0,0x0,0x0,0x0,0x0,0x0,0x0,0x0,0x0,0x0,0x0,0x0,0x0,0x0,
  0x0,0x0,0xbfd83f90,0x3f40bf00,0x0,0x0,0x0,0x0,0x0,0x0,0x0,0x0,0x0,0x0,0x0,0x0,
  0x0,0x0,0x0,0x0,0xbfd8bf90,0x3f403f00,0x0,0x0,0x0,0x0,0x0,0x0,0x0,0x0,0x0,0x0,
  0x0,0x0,0x0,0x0,0x0,0x0,0xbfd8bf90,0x3f403f00,0x0,0x0,0x0,0x0,0x0,0x0,0x0,0x0,
  0x0,0x0,0x0,0x0,0xbfd83f90,0x3f40bf00,0x0,0x0,0x0,0x0,0x0,0x0,0x0,0x0,0x0,0x0,
  0x0,0x0,0x0,0x0,0x0,0x0,0xbfd83f90,0x3f40bf00,0x0,0x0,0x0,0x0,0x0,0x0,0x0,0x0,
  0x0,0x0,0x0,0x0,0x0,0x0,0x0,0x0,0xbfd8bf90,0x3f403f00,0x0,0x0,0x0,0x0,0x0,0x0,
  0x0,0x0,0x0,0x0,0x0,0x0,0x0,0x0,0x0,0x0,0xbfd8bf90,0x3f403f00,0x0,0x0,0x0,0x0,
  0x0,0x0,0x0,0x0,0x0,0x0,0x0,0x0,0xbfd83f90,0x3f40bf00,0x0,0x0,0x0,0x0,0x0,0x0,
  0x0,0x0,0x0,0x0,0x0,0x0,0x0,0x0,0x0,0x0,0xbfd83f90,0x3f40bf00,0x0,0x0,0x0,0x0,
  0x0,0x0,0x0,0x0,0x0,0x0,0x0,0x0,0x0,0x0,0x0,0x0,0xbfd8bf90,0x3f403f00,0x0,0x0,
  0x0,0x0,0x0,0x0,0x0,0x0,0x0,0x0,0x0,0x0,0x0,0x0,0x0,0x0,0xbfd8bf90,0x3f403f00,
  0x0,0x0,0x0,0x0,0x0,0x0,0x0,0x0,0x0,0x0,0x0,0x0,0xbfd83f90,0x3f40bf00,0x0,0x0,
  0x0,0x0,0x0,0x0,0x0,0x0,0x0,0x0,0x0,0x0,0x0,0x0,0x0,0x0,0xbfd83f90,0x3f40bf00,
  0xc000,0xc020,0x2600,0xba00,0xc008,0xc028,0x2640,0xba40,0xc010,0xc030,0x2690,0xba90,0xc018,0xc038,0x26d0,0xbad0,
  0xd800,0xd820,0x400,0xc000,0xd808,0xd828,0x440,0xc040,0xd810,0xd830,0x490,0xc090,0xd818,0xd838,0x4d0,0xc0d0,
  0xc120,0xc100,0x2800,0xc600,0xc128,0xc108,0x2840,0xc640,0xc130,0xc110,0x2890,0xc690,0xc138,0xc118,0x28d0,0xc6d0,
  0xd840,0xd860,0x2800,0xc600,0xd848,0xd868,0x2840,0xc640,0xd850,0xd870,0x2890,0xc690,0xd858,0xd878,0x28d0,0xc6d0,
  0xc080,0xc0a0,0x2610,0xba10,0xc088,0xc0a8,0x2650,0xba50,0xc090,0xc0b0,0x2680,0xba80,0xc098,0xc0b8,0x26c0,0xbac0,
  0xc1a0,0xc180,0x410,0xc010,0xc1a8,0xc188,0x450,0xc050,0xc1b0,0xc190,0x480,0xc080,0xc1b8,0xc198,0x4c0,0xc0c0,
  0xd9a0,0xd980,0x2810,0xc610,0xd9a8,0xd988,0x2850,0xc650,0xd9b0,0xd990,0x2880,0xc680,0xd9b8,0xd998,0x28c0,0xc6c0,
  0xd9e0,0xd9c0,0x2810,0xc610,0xd9e8,0xd9c8,0x2850,0xc650,0xd9f0,0xd9d0,0x2880,0xc680,0xd9f8,0xd9d8,0x28c0,0xc6c0,
  0xc600,0xc620,0x2620,0xba20,0xc608,0xc628,0x2660,0xba60,0xc610,0xc630,0x26b0,0xbab0,0xc618,0xc638,0x26f0,0xbaf0,
  0xde00,0xde20,0x420,0xc020,0xde08,0xde28,0x460,0xc060,0xde10,0xde30,0x4b0,0xc0b0,0xde18,0xde38,0x4f0,0xc0f0,
  0xd920,0xd900,0x2820,0xc620,0xd928,0xd908,0x2860,0xc660,0xd930,0xd910,0x28b0,0xc6b0,0xd938,0xd918,0x28f0,0xc6f0,
  0xd960,0xd940,0x2820,0xc620,0xd968,0xd948,0x2860,0xc660,0xd970,0xd950,0x28b0,0xc6b0,0xd978,0xd958,0x28f0,0xc6f0,
  0xd880,0xd8a0,0x2630,0xba30,0xd888,0xd8a8,0x2670,0xba70,0xd890,0xd8b0,0x26a0,0xbaa0,0xd898,0xd8b8,0x26e0,0xbae0,
  0xeba0,0xeb80,0x430,0xc030,0xeba8,0xeb88,0x470,0xc070,0xebb0,0xeb90,0x4a0,0xc0a0,0xebb8,0xeb98,0x4e0,0xc0e0,
  0xd8c0,0xd8e0,0x2830,0xc630,0xd8c8,0xd8e8,0x2870,0xc670,0xd8d0,0xd8f0,0x28a0,0xc6a0,0xd8d8,0xd8f8,0x28e0,0xc6e0,
  0xc600,0xc620,0x2830,0xc630,0xc608,0xc628,0x2870,0xc670,0xc610,0xc630,0x28a0,0xc6a0,0xc618,0xc638,0x28e0,0xc6e0,
  0x180000,0x440,0x860,0x10000000,0x180010,0x4d0,0x8f0,0x10000000,0x180020,0x560,0x940,0x10000000,0x180030,0x5f0,0x9d0,0x10000000,
  0x180040,0x600,0xa20,0x201b3180,0x180050,0x690,0xab0,0x201b3190,0x180060,0x720,0xb00,0x201b31a0,0x180070,0x7b0,0xb90,0x201b31b0,
  0x180610,0x1040,0x1460,0x401b0000,0x180600,0x10d0,0x14f0,0x401b0010,0x180630,0x1160,0x1540,0x401b0020,0x180620,0x11f0,0x15d0,0x401b0030,
  0x180650,0x1200,0x1620,0x201e3180,0x180640,0x1290,0x16b0,0x201e3190,0x180670,0x1320,0x1700,0x201e31a0,0x180660,0x13b0,0x1790,0x201e31b0,
  0x180c20,0x450,0x868,0x401e0020,0x180c30,0x4c0,0x8f8,0x401e0030,0x180c00,0x570,0x948,0x401e0000,0x180c10,0x5e0,0x9d8,0x401e0010,
  0x180c60,0x610,0xa28,0x202131a0,0x180c70,0x680,0xab8,0x202131b0,0x180c40,0x730,0xb08,0x20213180,0x180c50,0x7a0,0xb98,0x20213190,
  0x181230,0x1050,0x1468,0x40210020,0x181220,0x10c0,0x14f8,0x40210030,0x181210,0x1170,0x1548,0x40210000,0x181200,0x11e0,0x15d8,0x40210010,
  0x181270,0x1210,0x1628,0x10000000,0x181260,0x1280,0x16b8,0x10000000,0x181250,0x1330,0x1708,0x10000000,0x181240,0x13a0,0x1798,0x10000000,
  0x181840,0x460,0x870,0x10000000,0x181850,0x4f0,0x8e0,0x10000000,0x181860,0x540,0x950,0x10000000,0x181870,0x5d0,0x9c0,0x10000000,
  0x181800,0x620,0xa30,0x201b31c0,0x181810,0x6b0,0xaa0,0x201b31d0,0x181820,0x700,0xb10,0x201b31e0,0x181830,0x790,0xb80,0x201b31f0,
  0x181e50,0x1060,0x1470,0x401b0040,0x181e40,0x10f0,0x14e0,0x401b0050,0x181e70,0x1140,0x1550,0x401b0060,0x181e60,0x11d0,0x15c0,0x401b0070,
  0x181e10,0x1220,0x1630,0x201e31c0,0x181e00,0x12b0,0x16a0,0x201e31d0,0x181e30,0x1300,0x1710,0x201e31e0,0x181e20,0x1390,0x1780,0x201e31f0,
  0x182460,0x470,0x878,0x401e0060,0x182470,0x4e0,0x8e8,0x401e0070,0x182440,0x550,0x958,0x401e0040,0x182450,0x5c0,0x9c8,0x401e0050,
  0x182420,0x630,0xa38,0x202131e0,0x182430,0x6a0,0xaa8,0x202131f0,0x182400,0x710,0xb18,0x202131c0,0x182410,0x780,0xb88,0x202131d0,
  0x182a70,0x1070,0x1478,0x40210060,0x182a60,0x10e0,0x14e8,0x40210070,0x182a50,0x1150,0x1558,0x40210040,0x182a40,0x11c0,0x15c8,0x40210050,
  0x182a30,0x1230,0x1638,0x10000000,0x182a20,0x12a0,0x16a8,0x10000000,0x182a10,0x1310,0x1718,0x10000000,0x182a00,0x1380,0x1788,0x10000000,
  0xbfd8bf90,0x3f403f00,0x0,0x0,0x0,0x0,0x0,0x0,0x0,0x0,0x0,0x0,0x0,0x0,0x0,0x0,
  0x0,0x0,0xbfd8bf90,0x3f403f00,0x0,0x0,0x0,0x0,0x0,0x0,0x0,0x0,0x0,0x0,0x0,0x0,
  0xbfd83f90,0x3f40bf00,0x0,0x0,0x0,0x0,0x0,0x0,0x0,0x0,0x0,0x0,0x0,0x0,0x0,0x0,
  0x0,0x0,0xbfd83f90,0x3f40bf00,0x0,0x0,0x0,0x0,0x0,0x0,0x0,0x0,0x0,0x0,0x0,0x0,
  0x0,0x0,0x0,0x0,0xbfd8bf90,0x3f403f00,0x0,0x0,0x0,0x0,0x0,0x0,0x0,0x0,0x0,0x0,
  0x0,0x0,0x0,0x0,0x0,0x0,0xbfd8bf90,0x3f403f00,0x0,0x0,0x0,0x0,0x0,0x0,0x0,0x0,
  0x0,0x0,0x0,0x0,0xbfd83f90,0x3f40bf00,0x0,0x0,0x0,0x0,0x0,0x0,0x0,0x0,0x0,0x0,
  0x0,0x0,0x0,0x0,0x0,0x0,0xbfd83f90,0x3f40bf00,0x0,0x0,0x0,0x0,0x0,0x0,0x0,0x0,
  0x0,0x0,0x0,0x0,0x0,0x0,0x0,0x0,0xbfd8bf90,0x3f403f00,0x0,0x0,0x0,0x0,0x0,0x0,
  0x0,0x0,0x0,0x0,0x0,0x0,0x0,0x0,0x0,0x0,0xbfd8bf90,0x3f403f00,0x0,0x0,0x0,0x0,
  0x0,0x0,0x0,0x0,0x0,0x0,0x0,0x0,0xbfd83f90,0x3f40bf00,0x0,0x0,0x0,0x0,0x0,0x0,
  0x0,0x0,0x0,0x0,0x0,0x0,0x0,0x0,0x0,0x0,0xbfd83f90,0x3f40bf00,0x0,0x0,0x0,0x0,
  0x0,0x0,0x0,0x0,0x0,0x0,0x0,0x0,0x0,0x0,0x0,0x0,0xbfd8bf90,0x3f403f00,0x0,0x0,
  0x0,0x0,0x0,0x0,0x0,0x0,0x0,0x0,0x0,0x0,0x0,0x0,0x0,0x0,0xbfd8bf90,0x3f403f00,
  0x0,0x0,0x0,0x0,0x0,0x0,0x0,0x0,0x0,0x0,0x0,0x0,0xbfd83f90,0x3f40bf00,0x0,0x0,
  0x0,0x0,0x0,0x0,0x0,0x0,0x0,0x0,0x0,0x0,0x0,0x0,0x0,0x0,0xbfd83f90,0x3f40bf00,
  0xcc00,0xcc20,0x2720,0xbb20,0xcc08,0xcc28,0x2760,0xbb60,0xcc10,0xcc30,0x27b0,0xbbb0,0xcc18,0xcc38,0x27f0,0xbbf0,
  0xcd20,0xcd00,0x520,0xc120,0xcd28,0xcd08,0x560,0xc160,0xcd30,0xcd10,0x5b0,0xc1b0,0xcd38,0xcd18,0x5f0,0xc1f0,
  0xe440,0xe460,0x2920,0xc720,0xe448,0xe468,0x2960,0xc760,0xe450,0xe470,0x29b0,0xc7b0,0xe458,0xe478,0x29f0,0xc7f0,
  0xeb60,0xeb40,0x2920,0xc720,0xeb68,0xeb48,0x2960,0xc760,0xeb70,0xeb50,0x29b0,0xc7b0,0xeb78,0xeb58,0x29f0,0xc7f0,
  0xe480,0xe4a0,0x2730,0xbb30,0xe488,0xe4a8,0x2770,0xbb70,0xe490,0xe4b0,0x27a0,0xbba0,0xe498,0xe4b8,0x27e0,0xbbe0,
  0xe5a0,0xe580,0x530,0xc130,0xe5a8,0xe588,0x570,0xc170,0xe5b0,0xe590,0x5a0,0xc1a0,0xe5b8,0xe598,0x5e0,0xc1e0,
  0xe4c0,0xe4e0,0x2930,0xc730,0xe4c8,0xe4e8,0x2970,0xc770,0xe4d0,0xe4f0,0x29a0,0xc7a0,0xe4d8,0xe4f8,0x29e0,0xc7e0,
  0xd9e0,0xd9c0,0x2930,0xc730,0xd9e8,0xd9c8,0x2970,0xc770,0xd9f0,0xd9d0,0x29a0,0xc7a0,0xd9f8,0xd9d8,0x29e0,0xc7e0,
  0xe400,0xe420,0x2700,0xbb00,0xe408,0xe428,0x2740,0xbb40,0xe410,0xe430,0x2790,0xbb90,0xe418,0xe438,0x27d0,0xbbd0,
  0xe520,0xe500,0x500,0xc100,0xe528,0xe508,0x540,0xc140,0xe530,0xe510,0x590,0xc190,0xe538,0xe518,0x5d0,0xc1d0,
  0xe560,0xe540,0x2900,0xc700,0xe568,0xe548,0x2940,0xc740,0xe570,0xe550,0x2990,0xc790,0xe578,0xe558,0x29d0,0xc7d0,
  0xcc80,0xcca0,0x2900,0xc700,0xcc88,0xcca8,0x2940,0xc740,0xcc90,0xccb0,0x2990,0xc790,0xcc98,0xccb8,0x29d0,0xc7d0,
  0xcda0,0xcd80,0x2710,0xbb10,0xcda8,0xcd88,0x2750,0xbb50,0xcdb0,0xcd90,0x2780,0xbb80,0xcdb8,0xcd98,0x27c0,0xbbc0,
  0xeac0,0xeae0,0x510,0xc110,0xeac8,0xeae8,0x550,0xc150,0xead0,0xeaf0,0x580,0xc180,0xead8,0xeaf8,0x5c0,0xc1c0,
  0xe5e0,0xe5c0,0x2910,0xc710,0xe5e8,0xe5c8,0x2950,0xc750,0xe5f0,0xe5d0,0x2980,0xc780,0xe5f8,0xe5d8,0x29c0,0xc7c0,
  0xe400,0xe420,0x2910,0xc710,0xe408,0xe428,0x2950,0xc750,0xe410,0xe430,0x2980,0xc780,0xe418,0xe438,0x29c0,0xc7c0,
};

__global__ __launch_bounds__(256, 1) void wino_main_kernel(const char* __restrict__ x, char* __restrict__ Uq, char* __restrict__ out, const char* __restrict__ wt,
                                                           unsigned long long region_mask) {
  __shared__ __attribute__((aligned(1024))) char smem[163840];
  unsigned tid = threadIdx.x;
  asm volatile(
"s_load_dwordx2 s[12:13], %1, 0\n\t"
"s_load_dwordx2 s[52:53], %1, 24\n\t"
"s_load_dwordx2 s[8:9], %1, 8\n\t"
"s_load_dwordx2 s[16:17], %1, 16\n\t"
"s_load_dword s76, %1, 32\n\t"
"s_getpc_b64 s[40:41]\n\t"
"Lwino_pc:\n\t"
"s_bfe_u32 s42, %2, 0x50003\n\t"
"s_lshl_b32 s42, s42, 12\n\t"
"v_lshl_add_u32 v203, v205, 4, s42\n\t"
"v_min_u32 v203, (Lwino_end-Lwino_pc-16)&-16, v203\n\t"
"global_load_dwordx4 v[100:103], v203, s[40:41]\n\t"
"v_readfirstlane_b32 s73, v205\n\t"
"s_lshl2_add_u32 s69, s73, %2\n\t"
"s_and_b32 s60, %2, 7\n\t"
"s_lshr_b32 s61, %2, 7\n\t"
"s_lshl2_add_u32 s60, s60, s61\n\t"
"s_bfe_u32 s61, %2, 0x40003\n\t"
"s_lshl_b32 s60, s60, 8\n\t"
"s_add_u32 s60, s60, s61\n\t"
"s_sub_u32 s60, s60, 16\n\t"
"s_mul_i32 s21, s60, 12288\n\t"
"s_lshr_b32 s73, s73, 6\n\t"
"s_add_u32 s24, s73, 28\n\t"
"s_mulk_i32 s24, 1536\n\t"
"v_bfe_u32 v1, v205, 2, 3\n\t"
"v_lshrrev_b32 v2, 3, v205\n\t"
"v_and_or_b32 v1, v2, 24, v1\n\t"
"v_and_b32 v3, 6, v2\n\t"
"v_and_b32 v4, 3, v205\n\t"
"v_xor_b32 v4, v3, v4\n\t"
"v_mul_u32_u24 v1, 384, v1\n\t"
"v_lshl_add_u32 v28, v4, 4, v1\n\t"
"s_mov_b32 s14, 100663296\n\t"
"s_mov_b32 s15, 0x20000\n\t"
"s_mov_b32 s54, 663552\n\t"
"s_mov_b32 s55, s15\n\t"
"s_mov_b32 s10, 0x200000\n\t"
"s_mov_b32 s11, s15\n\t"
"s_lshl_b32 s60, s73, 11\n\t"
"v_lshl_add_u32 v252, v205, 4, s60\n\t"
"s_lshr_b32 s71, %5, 6\n\t"
"s_mul_i32 s71, s71, 0x9e3779b1\n\t"
"s_lshr_b32 s71, s71, 28\n\t"
"s_add_u32 s71, s71, 2\n\t"
"s_lshl_b32 s71, s71, 21\n\t"
"s_mov_b64 s[46:47], %3\n\t"
"s_add_u32 s48, s46, 12288\n\t"
"s_addc_u32 s49, s47, 0\n\t"
"global_load_dwordx4 v[244:247], v252, s[46:47] offset:2048\n\t"
"s_and_b32 s60, s69, 3\n\t"
"s_lshr_b32 s61, s69, 2\n\t"
"s_mul_i32 s62, s61, 171\n\t"
"s_lshr_b32 s62, s62, 9\n\t"
"s_mul_i32 s63, s62, 3\n\t"
"s_sub_u32 s61, s61, s63\n\t"
"s_mul_i32 s63, s62, 43\n\t"
"s_lshr_b32 s63, s63, 7\n\t"
"s_mul_i32 s64, s63, 3\n\t"
"s_sub_u32 s62, s62, s64\n\t"
"s_mul_i32 s74, s62, 18\n\t"
"s_add_u32 s74, s74, s63\n\t"
"s_lshl3_add_u32 s74, s74, s60\n\t"
"s_mul_i32 s74, s74, 12\n\t"
"s_add_u32 s74, s74, s61\n\t"
"s_lshl_b32 s74, s74, 7\n\t"
"v_bfe_u32 v96, v205, 3, 2\n\t"
"v_and_b32 v97, 32, v205\n\t"
"v_lshl_or_b32 v96, v96, 1, v97\n\t"
"v_mul_u32_u24 v96, 192, v96\n\t"
"v_and_b32 v97, 7, v205\n\t"
"v_lshl_add_u32 v96, v97, 4, v96\n\t"
"v_and_b32 v4, 7, v205\n\t"
"v_xor_b32 v4, v3, v4\n\t"
"v_and_b32 v2, 7, v2\n\t"
"v_mul_u32_u24 v2, 384, v2\n\t"
"v_lshl_add_u32 v29, v4, 4, v2\n\t"
"s_sub_u32 s58, s73, 1\n\t"
"s_lshl_b32 s59, s58, 10\n\t"
"s_lshl_b32 s58, s58, 7\n\t"
"s_waitcnt lgkmcnt(0)\n\t"
"s_and_b32 s13, s13, 0xffff\n\t"
"s_and_b32 s53, s53, 0xffff\n\t"
"s_and_b32 s71, s71, s76\n\t"
"s_add_u32 s8, s8, s71\n\t"
"s_addc_u32 s9, s9, 0\n\t"
"s_and_b32 s9, s9, 0xffff\n\t"
"s_cmp_lg_u32 s73, 0\n\t"
"s_cbranch_scc1 Lpb_%=\n\t"
"s_cmp_lt_u32 s69, 216\n\t"
"s_cbranch_scc0 Lnf1_%=\n\t"
"s_mov_b32 m0, 95232\n\t"
"s_add_u32 s64, s74, 0\n\t"
"buffer_load_dwordx4 v96, s[52:55], s64 offen lds\n\t"
"s_mov_b32 m0, 96256\n\t"
"s_add_u32 s64, s74, 73728\n\t"
"buffer_load_dwordx4 v96, s[52:55], s64 offen lds\n\t"
"s_mov_b32 m0, 97280\n\t"
"s_add_u32 s64, s74, 147456\n\t"
"buffer_load_dwordx4 v96, s[52:55], s64 offen lds\n\t"
"Lnf1_%=:\n\t"
"s_mov_b32 s72, 1900544\n\t"
"s_lshl2_add_u32 s70, %2, s72\n\t"
"s_mov_b32 s68, 3000\n\t"
"buffer_load_dwordx4 v[220:223], v252, s[8:11], s72 offen sc1\n\t"
"s_branch Lpa_%=\n\t"
"Lpb_%=:\n\t"
"s_lshr_b32 s56, %2, 3\n\t"
"s_bitcmp1_b32 0xaaaaaaaa, s56\n\t"
"s_cbranch_scc1 Lpc_%=\n\t"
"s_add_u32 m0, s59, 24576\n\t"
"s_add_u32 s57, s58, 0\n\t"
"buffer_load_dwordx4 v29, s[52:55], s57 offen offset:0 lds\n\t"
"buffer_load_dwordx4 v29, s[52:55], s57 offen offset:3072 lds\n\t"
"s_add_u32 m0, s59, 30720\n\t"
"s_add_u32 s57, s58, 6144\n\t"
"buffer_load_dwordx4 v29, s[52:55], s57 offen offset:0 lds\n\t"
"buffer_load_dwordx4 v29, s[52:55], s57 offen offset:3072 lds\n\t"
"s_add_u32 m0, s59, 36864\n\t"
"s_add_u32 s57, s58, 221184\n\t"
"buffer_load_dwordx4 v29, s[52:55], s57 offen offset:0 lds\n\t"
"buffer_load_dwordx4 v29, s[52:55], s57 offen offset:3072 lds\n\t"
"s_add_u32 m0, s59, 82944\n\t"
"s_add_u32 s57, s58, 227328\n\t"
"buffer_load_dwordx4 v29, s[52:55], s57 offen offset:0 lds\n\t"
"buffer_load_dwordx4 v29, s[52:55], s57 offen offset:3072 lds\n\t"
"s_add_u32 m0, s59, 89088\n\t"
"s_add_u32 s57, s58, 442368\n\t"
"buffer_load_dwordx4 v29, s[52:55], s57 offen offset:0 lds\n\t"
"buffer_load_dwordx4 v29, s[52:55], s57 offen offset:3072 lds\n\t"
"s_add_u32 m0, s59, 142848\n\t"
"s_add_u32 s57, s58, 448512\n\t"
"buffer_load_dwordx4 v29, s[52:55], s57 offen offset:0 lds\n\t"
"buffer_load_dwordx4 v29, s[52:55], s57 offen offset:3072 lds\n\t"
"s_branch Lpro_%=\n\t"
"Lpa_%=:\n\t"
"s_mul_i32 s75, s63, 288\n\t"
"s_add_u32 s75, s75, s62\n\t"
"s_lshl2_add_u32 s75, s75, s60\n\t"
"s_cmp_lt_u32 s61, 2\n\t"
"s_cselect_b64 vcc, -1, 0\n\t"
"s_cselect_b32 s61, s61, 4\n\t"
"s_mulk_i32 s61, 24\n\t"
"s_add_u32 s75, s75, s61\n\t"
"s_lshl_b32 s75, s75, 8\n\t"
"v_and_b32 v99, 12, v205\n\t"
"v_and_b32 v60, 35, v205\n\t"
"v_lshl_or_b32 v60, v99, 1, v60\n\t"
"v_bfe_u32 v99, v205, 4, 1\n\t"
"v_lshl_or_b32 v60, v99, 2, v60\n\t"
"v_cndmask_b32 v60, v205, v60, vcc\n\t"
"v_lshlrev_b32 v60, 1, v60\n\t"
"v_add_u32 v60, 95232, v60\n\t"
"v_lshrrev_b32 v97, 5, v205\n\t"
"v_lshl_or_b32 v97, v99, 2, v97\n\t"
"v_lshrrev_b32 v99, 4, v205\n\t"
"v_cndmask_b32 v97, v99, v97, vcc\n\t"
"v_mul_u32_u24 v97, 3072, v97\n\t"
"v_and_b32 v99, 15, v205\n\t"
"v_lshl_add_u32 v97, v99, 4, v97\n\t"
"s_cmp_lt_u32 s63, 2\n\t"
"s_mov_b32 s56, 0x3f2aaaab\n\t"
"s_mov_b32 s57, 0x3fc00000\n\t"
"s_mov_b32 s58, 0x3ee38e39\n\t"
"s_mov_b32 s59, 0x40100000\n\t"
"s_mov_b32 s62, 0xbf54ad4b\n\t"
"s_mov_b32 s63, 0xbefc0fc1\n\t"
"s_mov_b32 s64, 0xbe7c0fc1\n\t"
"s_mov_b32 s65, 0x3ea80a81\n\t"
"s_mov_b32 s38, 0x3f638e39\n\t"
"s_mov_b32 s39, 0x3f638e39\n\t"
"s_cselect_b32 s38, 0x3ea80a81, s38\n\t"
"s_cselect_b32 s39, 0xbebd0bd1, s39\n\t"
"v_mov_b64 v[32:33], 0\n\t"
"v_mov_b64 v[34:35], 0\n\t"
"v_mov_b64 v[36:37], 0\n\t"
"v_mov_b64 v[38:39], 0\n\t"
"v_mov_b64 v[40:41], 0\n\t"
"v_mov_b64 v[42:43], 0\n\t"
"v_mov_b64 v[44:45], 0\n\t"
"v_mov_b64 v[46:47], 0\n\t"
"v_mov_b64 v[48:49], 0\n\t"
"v_mov_b64 v[50:51], 0\n\t"
"v_mov_b64 v[52:53], 0\n\t"
"v_mov_b64 v[54:55], 0\n\t"
"s_mul_i32 s25, s73, 19968\n\t"
"s_add_u32 s25, s25, 61440\n\t"
"s_bfe_u32 s41, %2, 0x40003\n\t"
"s_cmp_eq_u32 s41, 0\n\t"
"s_cselect_b32 s40, -1, 0xbfffffff\n\t"
"s_cmp_eq_u32 s41, 15\n\t"
"s_cselect_b32 s41, -1, 0xdfffffff\n\t"
"s_and_b32 s40, s40, s41\n\t"
"s_sub_u32 s42, s21, 384\n\t"
"s_mul_i32 s41, s73, 589824\n\t"
"s_add_u32 s42, s42, s41\n\t"
"s_and_b32 s41, s40, 536883584\n\t"
"s_and_b32 s40, s40, 2.0\n\t"
"v_bfe_u32 v26, v205, 2, 3\n\t"
"v_add_u32 v27, -1, v26\n\t"
"v_cmp_gt_u32 vcc, 6, v27\n\t"
"v_lshrrev_b32 v27, 1, v27\n\t"
"v_mul_u32_u24 v27, 196608, v27\n\t"
"v_and_b32 v20, 0x70, v28\n\t"
"v_add3_u32 v27, v27, v20, s42\n\t"
"v_bfe_i32 v20, v205, 2, 1\n\t"
"v_mov_b32 v21, s40\n\t"
"v_bfi_b32 v20, v20, s41, v21\n\t"
"v_add_u32 v27, v27, v20\n\t"
"v_bfrev_b32 v20, 8\n\t"
"v_cndmask_b32 v254, v20, v27, vcc\n\t"
"v_bfe_u32 v26, v205, 2, 3\n\t"
"v_bfe_u32 v27, v205, 5, 1\n\t"
"v_mul_u32_u24 v27, 3072, v27\n\t"
"v_lshl_add_u32 v27, v26, 7, v27\n\t"
"v_and_b32 v26, 6, v26\n\t"
"v_lshrrev_b32 v20, 4, v205\n\t"
"v_and_b32 v20, 4, v20\n\t"
"v_and_or_b32 v20, v205, 3, v20\n\t"
"v_xor_b32 v20, v20, v26\n\t"
"v_lshl_add_u32 v20, v20, 4, v27\n\t"
"v_lshrrev_b32 v21, 7, v205\n\t"
"v_lshl_add_u32 v20, v21, 10, v20\n\t"
"v_lshrrev_b32 v21, 5, v205\n\t"
"v_and_b32 v21, 6, v21\n\t"
"v_bfe_u32 v22, v205, 1, 1\n\t"
"v_or_b32 v21, v21, v22\n\t"
"v_xor_b32 v21, v21, v26\n\t"
"v_lshl_add_u32 v21, v21, 4, v27\n\t"
"v_and_b32 v22, 1, v205\n\t"
"v_lshl_add_u32 v21, v22, 3, v21\n\t"
"v_add_u32 v22, 142848, v20\n\t"
"v_add_u32 v23, 142848, v21\n\t"
"v_add_u32 v24, 82944, v20\n\t"
"v_add_u32 v25, 82944, v21\n\t"
"s_and_b32 s17, s17, 0xffff\n\t"
"s_mov_b32 s18, 100663296\n\t"
"s_mov_b32 s19, 0x20000\n\t"
"s_lshr_b32 s41, s73, 1\n\t"
"s_add_u32 s42, s21, 196608\n\t"
"s_mul_i32 s43, s41, 384\n\t"
"s_add_u32 s22, s42, s43\n\t"
"s_lshl1_add_u32 s40, s43, s22\n\t"
"s_mul_i32 s43, s41, 196608\n\t"
"s_add_u32 s23, s42, s43\n\t"
"s_mov_b32 s44, 0x0fff0fff\n\t"
"s_mov_b32 s45, 0x0fff0fff\n\t"
"s_mul_i32 s26, s41, 19968\n\t"
"s_add_u32 s26, s26, 141312\n\t"
"s_sub_u32 s43, s41, s73\n\t"
"s_add_u32 s43, s43, 4\n\t"
"s_mul_i32 s27, s43, 589824\n\t"
"s_bitcmp1_b32 s73, 0\n\t"
"s_mov_b32 s36, 0x0fff0000\n\t"
"s_cselect_b32 s36, 0xfff0, s36\n\t"
"s_mov_b32 s37, s36\n\t"
"s_mul_i32 s80, s73, 3072\n\t"
"s_add_u32 s80, s80, s8\n\t"
"s_addc_u32 s81, s9, 0x100000\n\t"
"s_mov_b32 s82, 0x1c0000\n\t"
"s_mov_b32 s83, 0x800000\n\t"
"s_cmp_lt_u32 s69, 216\n\t"
"s_cbranch_scc0 Lnf3_%=\n\t"
"s_waitcnt vmcnt(1)\n\t"
"ds_read_u16_d16_hi v32, v60 offset:0\n\t"
"ds_read_u16_d16_hi v33, v60 offset:128\n\t"
"ds_read_u16_d16_hi v40, v60 offset:1024\n\t"
"ds_read_u16_d16_hi v41, v60 offset:1152\n\t"
"ds_read_u16_d16_hi v48, v60 offset:2048\n\t"
"ds_read_u16_d16_hi v49, v60 offset:2176\n\t"
"ds_read_u16_d16_hi v34, v60 offset:256\n\t"
"ds_read_u16_d16_hi v35, v60 offset:384\n\t"
"ds_read_u16_d16_hi v42, v60 offset:1280\n\t"
"ds_read_u16_d16_hi v43, v60 offset:1408\n\t"
"ds_read_u16_d16_hi v50, v60 offset:2304\n\t"
"ds_read_u16_d16_hi v51, v60 offset:2432\n\t"
"ds_read_u16_d16_hi v36, v60 offset:512\n\t"
"ds_read_u16_d16_hi v37, v60 offset:640\n\t"
"ds_read_u16_d16_hi v44, v60 offset:1536\n\t"
"ds_read_u16_d16_hi v45, v60 offset:1664\n\t"
"ds_read_u16_d16_hi v52, v60 offset:2560\n\t"
"ds_read_u16_d16_hi v53, v60 offset:2688\n\t"
"ds_read_u16_d16_hi v38, v60 offset:768\n\t"
"ds_read_u16_d16_hi v39, v60 offset:896\n\t"
"ds_read_u16_d16_hi v46, v60 offset:1792\n\t"
"ds_read_u16_d16_hi v47, v60 offset:1920\n\t"
"ds_read_u16_d16_hi v54, v60 offset:2816\n\t"
"ds_read_u16_d16_hi v55, v60 offset:2944\n\t"
"s_waitcnt lgkmcnt(15)\n\t"
"v_pk_fma_f32 v[56:57], v[48:49], s[58:59], v[32:33] op_sel:[0,0,0] op_sel_hi:[1,0,1]\n\t"
"v_pk_fma_f32 v[58:59], v[48:49], s[58:59], v[32:33] op_sel:[0,1,0] op_sel_hi:[1,1,1]\n\t"
"v_pk_mul_f32 v[104:105], v[32:33], s[38:39] op_sel:[0,0] op_sel_hi:[1,0]\n\t"
"v_pk_mul_f32 v[114:115], v[48:49], s[38:39] op_sel:[0,1] op_sel_hi:[1,1]\n\t"
"v_pk_fma_f32 v[106:107], v[40:41], s[56:57], v[56:57] op_sel:[0,0,0] op_sel_hi:[1,0,1]\n\t"
"v_pk_fma_f32 v[108:109], v[40:41], s[56:57], v[56:57] op_sel:[0,0,0] op_sel_hi:[1,0,1] neg_lo:[1,0,0] neg_hi:[1,0,0]\n\t"
"v_pk_fma_f32 v[110:111], v[40:41], s[56:57], v[58:59] op_sel:[0,1,0] op_sel_hi:[1,1,1]\n\t"
"v_pk_fma_f32 v[112:113], v[40:41], s[56:57], v[58:59] op_sel:[0,1,0] op_sel_hi:[1,1,1] neg_lo:[1,0,0] neg_hi:[1,0,0]\n\t"
"v_pk_mul_f32 v[106:107], v[106:107], s[62:63] op_sel:[0,0] op_sel_hi:[1,0]\n\t"
"v_pk_mul_f32 v[108:109], v[108:109], s[62:63] op_sel:[0,1] op_sel_hi:[1,1]\n\t"
"v_pk_mul_f32 v[110:111], v[110:111], s[64:65] op_sel:[0,0] op_sel_hi:[1,0]\n\t"
"v_pk_mul_f32 v[112:113], v[112:113], s[64:65] op_sel:[0,1] op_sel_hi:[1,1]\n\t"
"v_cvt_pk_bf16_f32 v64, v104, v105\n\t"
"v_cvt_pk_bf16_f32 v84, v114, v115\n\t"
"v_cvt_pk_bf16_f32 v68, v106, v107\n\t"
"v_cvt_pk_bf16_f32 v74, v108, v109\n\t"
"v_cvt_pk_bf16_f32 v76, v110, v111\n\t"
"v_cvt_pk_bf16_f32 v82, v112, v113\n\t"
"s_waitcnt lgkmcnt(12)\n\t"
"v_pk_fma_f32 v[56:57], v[50:51], s[58:59], v[34:35] op_sel:[0,0,0] op_sel_hi:[1,0,1]\n\t"
"v_pk_fma_f32 v[58:59], v[50:51], s[58:59], v[34:35] op_sel:[0,1,0] op_sel_hi:[1,1,1]\n\t"
"v_pk_mul_f32 v[104:105], v[34:35], s[38:39] op_sel:[0,0] op_sel_hi:[1,0]\n\t"
"v_pk_mul_f32 v[114:115], v[50:51], s[38:39] op_sel:[0,1] op_sel_hi:[1,1]\n\t"
"v_pk_fma_f32 v[106:107], v[42:43], s[56:57], v[56:57] op_sel:[0,0,0] op_sel_hi:[1,0,1]\n\t"
"v_pk_fma_f32 v[108:109], v[42:43], s[56:57], v[56:57] op_sel:[0,0,0] op_sel_hi:[1,0,1] neg_lo:[1,0,0] neg_hi:[1,0,0]\n\t"
"v_pk_fma_f32 v[110:111], v[42:43], s[56:57], v[58:59] op_sel:[0,1,0] op_sel_hi:[1,1,1]\n\t"
"v_pk_fma_f32 v[112:113], v[42:43], s[56:57], v[58:59] op_sel:[0,1,0] op_sel_hi:[1,1,1] neg_lo:[1,0,0] neg_hi:[1,0,0]\n\t"
"v_pk_mul_f32 v[106:107], v[106:107], s[62:63] op_sel:[0,0] op_sel_hi:[1,0]\n\t"
"v_pk_mul_f32 v[108:109], v[108:109], s[62:63] op_sel:[0,1] op_sel_hi:[1,1]\n\t"
"v_pk_mul_f32 v[110:111], v[110:111], s[64:65] op_sel:[0,0] op_sel_hi:[1,0]\n\t"
"v_pk_mul_f32 v[112:113], v[112:113], s[64:65] op_sel:[0,1] op_sel_hi:[1,1]\n\t"
"v_cvt_pk_bf16_f32 v65, v104, v105\n\t"
"v_cvt_pk_bf16_f32 v85, v114, v115\n\t"
"v_cvt_pk_bf16_f32 v69, v106, v107\n\t"
"v_cvt_pk_bf16_f32 v75, v108, v109\n\t"
"v_cvt_pk_bf16_f32 v77, v110, v111\n\t"
"v_cvt_pk_bf16_f32 v83, v112, v113\n\t"
"s_waitcnt lgkmcnt(6)\n\t"
"v_pk_fma_f32 v[56:57], v[52:53], s[58:59], v[36:37] op_sel:[0,0,0] op_sel_hi:[1,0,1]\n\t"
"v_pk_fma_f32 v[58:59], v[52:53], s[58:59], v[36:37] op_sel:[0,1,0] op_sel_hi:[1,1,1]\n\t"
"v_pk_mul_f32 v[104:105], v[36:37], s[38:39] op_sel:[0,0] op_sel_hi:[1,0]\n\t"
"v_pk_mul_f32 v[114:115], v[52:53], s[38:39] op_sel:[0,1] op_sel_hi:[1,1]\n\t"
"v_pk_fma_f32 v[106:107], v[44:45], s[56:57], v[56:57] op_sel:[0,0,0] op_sel_hi:[1,0,1]\n\t"
"v_pk_fma_f32 v[108:109], v[44:45], s[56:57], v[56:57] op_sel:[0,0,0] op_sel_hi:[1,0,1] neg_lo:[1,0,0] neg_hi:[1,0,0]\n\t"
"v_pk_fma_f32 v[110:111], v[44:45], s[56:57], v[58:59] op_sel:[0,1,0] op_sel_hi:[1,1,1]\n\t"
"v_pk_fma_f32 v[112:113], v[44:45], s[56:57], v[58:59] op_sel:[0,1,0] op_sel_hi:[1,1,1] neg_lo:[1,0,0] neg_hi:[1,0,0]\n\t"
"v_pk_mul_f32 v[106:107], v[106:107], s[62:63] op_sel:[0,0] op_sel_hi:[1,0]\n\t"
"v_pk_mul_f32 v[108:109], v[108:109], s[62:63] op_sel:[0,1] op_sel_hi:[1,1]\n\t"
"v_pk_mul_f32 v[110:111], v[110:111], s[64:65] op_sel:[0,0] op_sel_hi:[1,0]\n\t"
"v_pk_mul_f32 v[112:113], v[112:113], s[64:65] op_sel:[0,1] op_sel_hi:[1,1]\n\t"
"v_cvt_pk_bf16_f32 v66, v104, v105\n\t"
"v_cvt_pk_bf16_f32 v86, v114, v115\n\t"
"v_cvt_pk_bf16_f32 v70, v106, v107\n\t"
"v_cvt_pk_bf16_f32 v72, v108, v109\n\t"
"v_cvt_pk_bf16_f32 v78, v110, v111\n\t"
"v_cvt_pk_bf16_f32 v80, v112, v113\n\t"
"s_waitcnt lgkmcnt(0)\n\t"
"v_pk_fma_f32 v[56:57], v[54:55], s[58:59], v[38:39] op_sel:[0,0,0] op_sel_hi:[1,0,1]\n\t"
"v_pk_fma_f32 v[58:59], v[54:55], s[58:59], v[38:39] op_sel:[0,1,0] op_sel_hi:[1,1,1]\n\t"
"v_pk_mul_f32 v[104:105], v[38:39], s[38:39] op_sel:[0,0] op_sel_hi:[1,0]\n\t"
"v_pk_mul_f32 v[114:115], v[54:55], s[38:39] op_sel:[0,1] op_sel_hi:[1,1]\n\t"
"v_pk_fma_f32 v[106:107], v[46:47], s[56:57], v[56:57] op_sel:[0,0,0] op_sel_hi:[1,0,1]\n\t"
"v_pk_fma_f32 v[108:109], v[46:47], s[56:57], v[56:57] op_sel:[0,0,0] op_sel_hi:[1,0,1] neg_lo:[1,0,0] neg_hi:[1,0,0]\n\t"
"v_pk_fma_f32 v[110:111], v[46:47], s[56:57], v[58:59] op_sel:[0,1,0] op_sel_hi:[1,1,1]\n\t"
"v_pk_fma_f32 v[112:113], v[46:47], s[56:57], v[58:59] op_sel:[0,1,0] op_sel_hi:[1,1,1] neg_lo:[1,0,0] neg_hi:[1,0,0]\n\t"
"v_pk_mul_f32 v[106:107], v[106:107], s[62:63] op_sel:[0,0] op_sel_hi:[1,0]\n\t"
"v_pk_mul_f32 v[108:109], v[108:109], s[62:63] op_sel:[0,1] op_sel_hi:[1,1]\n\t"
"v_pk_mul_f32 v[110:111], v[110:111], s[64:65] op_sel:[0,0] op_sel_hi:[1,0]\n\t"
"v_pk_mul_f32 v[112:113], v[112:113], s[64:65] op_sel:[0,1] op_sel_hi:[1,1]\n\t"
"v_cvt_pk_bf16_f32 v67, v104, v105\n\t"
"v_cvt_pk_bf16_f32 v87, v114, v115\n\t"
"v_cvt_pk_bf16_f32 v71, v106, v107\n\t"
"v_cvt_pk_bf16_f32 v73, v108, v109\n\t"
"v_cvt_pk_bf16_f32 v79, v110, v111\n\t"
"v_cvt_pk_bf16_f32 v81, v112, v113\n\t"
"s_add_u32 s60, s75, 0\n\t"
"buffer_store_dwordx4 v[64:67], v97, s[8:11], s60 offen sc1\n\t"
"s_add_u32 s60, s75, 184320\n\t"
"buffer_store_dwordx4 v[84:87], v97, s[8:11], s60 offen sc1\n\t"
"s_add_u32 s60, s75, 36864\n\t"
"buffer_store_dwordx4 v[68:71], v97, s[8:11], s60 offen sc1\n\t"
"s_add_u32 s60, s75, 73728\n\t"
"buffer_store_dwordx4 v[72:75], v97, s[8:11], s60 offen sc1\n\t"
"s_add_u32 s60, s75, 110592\n\t"
"buffer_store_dwordx4 v[76:79], v97, s[8:11], s60 offen sc1\n\t"
"s_add_u32 s60, s75, 147456\n\t"
"buffer_store_dwordx4 v[80:83], v97, s[8:11], s60 offen sc1\n\t"
"s_waitcnt vmcnt(6)\n\t"
"s_branch Lnf4_%=\n\t"
"Lnf3_%=:\n\t"
"s_waitcnt vmcnt(0)\n\t"
"Lnf4_%=:\n\t"
"s_lshr_b32 s60, %2, 2\n\t"
"v_readlane_b32 s61, v220, s60\n\t"
"v_readlane_b32 s62, v221, s60\n\t"
"v_readlane_b32 s63, v222, s60\n\t"
"v_readlane_b32 s64, v223, s60\n\t"
"s_bitcmp1_b32 %2, 0\n\t"
"s_cselect_b32 s61, s62, s61\n\t"
"s_cselect_b32 s63, s64, s63\n\t"
"s_bitcmp1_b32 %2, 1\n\t"
"s_cselect_b32 s71, s63, s61\n\t"
"s_branch Ljoin_%=\n\t"
"Lpc_%=:\n\t"
"s_add_u32 m0, s24, 59904\n\t"
"s_add_u32 s28, s21, 1769472\n\t"
"buffer_load_dwordx4 v28, s[12:15], s28 offen offset:0 nt lds\n\t"
"s_add_u32 m0, s24, 66048\n\t"
"s_add_u32 s28, s21, 1966080\n\t"
"buffer_load_dwordx4 v28, s[12:15], s28 offen offset:0 nt lds\n\t"
"s_add_u32 m0, s24, 72192\n\t"
"s_add_u32 s28, s21, 2162688\n\t"
"buffer_load_dwordx4 v28, s[12:15], s28 offen offset:0 nt lds\n\t"
"s_mul_i32 s60, s59, 6\n\t"
"s_mul_i32 s61, s59, 192\n\t"
"s_add_u32 s61, s61, s21\n\t"
"s_mul_i32 s62, s73, 3072\n\t"
"v_subrev_u32 v30, s62, v28\n\t"
"s_add_u32 m0, s60, 102912\n\t"
"s_add_u32 s28, s61, 1769472\n\t"
"buffer_load_dwordx4 v30, s[12:15], s28 offen nt lds\n\t"
"s_add_u32 m0, s24, 79872\n\t"
"s_add_u32 s28, s21, 2359296\n\t"
"buffer_load_dwordx4 v28, s[12:15], s28 offen offset:0 nt lds\n\t"
"s_add_u32 m0, s24, 86016\n\t"
"s_add_u32 s28, s21, 2555904\n\t"
"buffer_load_dwordx4 v28, s[12:15], s28 offen offset:0 nt lds\n\t"
"s_add_u32 m0, s24, 92160\n\t"
"s_add_u32 s28, s21, 2752512\n\t"
"buffer_load_dwordx4 v28, s[12:15], s28 offen offset:0 nt lds\n\t"
"s_add_u32 m0, s60, 122880\n\t"
"s_add_u32 s28, s61, 2359296\n\t"
"buffer_load_dwordx4 v30, s[12:15], s28 offen nt lds\n\t"
"s_mul_i32 s25, s73, 19968\n\t"
"s_add_u32 s25, s25, 61440\n\t"
"s_bfe_u32 s41, %2, 0x40003\n\t"
"s_cmp_eq_u32 s41, 0\n\t"
"s_cselect_b32 s40, -1, 0xbfffffff\n\t"
"s_cmp_eq_u32 s41, 15\n\t"
"s_cselect_b32 s41, -1, 0xdfffffff\n\t"
"s_and_b32 s40, s40, s41\n\t"
"s_sub_u32 s42, s21, 384\n\t"
"s_mul_i32 s41, s73, 589824\n\t"
"s_add_u32 s42, s42, s41\n\t"
"s_and_b32 s41, s40, 536883584\n\t"
"s_and_b32 s40, s40, 2.0\n\t"
"v_bfe_u32 v26, v205, 2, 3\n\t"
"v_add_u32 v27, -1, v26\n\t"
"v_cmp_gt_u32 vcc, 6, v27\n\t"
"v_lshrrev_b32 v27, 1, v27\n\t"
"v_mul_u32_u24 v27, 196608, v27\n\t"
"v_and_b32 v20, 0x70, v28\n\t"
"v_add3_u32 v27, v27, v20, s42\n\t"
"v_bfe_i32 v20, v205, 2, 1\n\t"
"v_mov_b32 v21, s40\n\t"
"v_bfi_b32 v20, v20, s41, v21\n\t"
"v_add_u32 v27, v27, v20\n\t"
"v_bfrev_b32 v20, 8\n\t"
"v_cndmask_b32 v254, v20, v27, vcc\n\t"
"s_add_u32 m0, s25, 0\n\t"
"s_nop 0\n\t"
"buffer_load_dwordx4 v254, s[12:15], 0 offen nt lds\n\t"
"s_cmp_eq_u32 s73, 3\n\t"
"s_cselect_b32 s62, -1, 1\n\t"
"s_mul_i32 s63, s62, 59904\n\t"
"s_mul_i32 s62, s62, 1769472\n\t"
"v_add_u32 v19, s62, v254\n\t"
"s_add_u32 m0, s25, s63\n\t"
"s_nop 0\n\t"
"buffer_load_dwordx4 v19, s[12:15], 0 offen nt lds\n\t"
"v_bfe_u32 v26, v205, 2, 3\n\t"
"v_bfe_u32 v27, v205, 5, 1\n\t"
"v_mul_u32_u24 v27, 3072, v27\n\t"
"v_lshl_add_u32 v27, v26, 7, v27\n\t"
"v_and_b32 v26, 6, v26\n\t"
"v_lshrrev_b32 v20, 4, v205\n\t"
"v_and_b32 v20, 4, v20\n\t"
"v_and_or_b32 v20, v205, 3, v20\n\t"
"v_xor_b32 v20, v20, v26\n\t"
"v_lshl_add_u32 v20, v20, 4, v27\n\t"
"v_lshrrev_b32 v21, 7, v205\n\t"
"v_lshl_add_u32 v20, v21, 10, v20\n\t"
"v_lshrrev_b32 v21, 5, v205\n\t"
"v_and_b32 v21, 6, v21\n\t"
"v_bfe_u32 v22, v205, 1, 1\n\t"
"v_or_b32 v21, v21, v22\n\t"
"v_xor_b32 v21, v21, v26\n\t"
"v_lshl_add_u32 v21, v21, 4, v27\n\t"
"v_and_b32 v22, 1, v205\n\t"
"v_lshl_add_u32 v21, v22, 3, v21\n\t"
"v_add_u32 v22, 142848, v20\n\t"
"v_add_u32 v23, 142848, v21\n\t"
"v_add_u32 v24, 82944, v20\n\t"
"v_add_u32 v25, 82944, v21\n\t"
"s_and_b32 s17, s17, 0xffff\n\t"
"s_mov_b32 s18, 100663296\n\t"
"s_mov_b32 s19, 0x20000\n\t"
"s_lshr_b32 s41, s73, 1\n\t"
"s_add_u32 s42, s21, 196608\n\t"
"s_mul_i32 s43, s41, 384\n\t"
"s_add_u32 s22, s42, s43\n\t"
"s_lshl1_add_u32 s40, s43, s22\n\t"
"s_mul_i32 s43, s41, 196608\n\t"
"s_add_u32 s23, s42, s43\n\t"
"s_mov_b32 s44, 0x0fff0fff\n\t"
"s_mov_b32 s45, 0x0fff0fff\n\t"
"s_mul_i32 s26, s41, 19968\n\t"
"s_add_u32 s26, s26, 141312\n\t"
"s_sub_u32 s43, s41, s73\n\t"
"s_add_u32 s43, s43, 4\n\t"
"s_mul_i32 s27, s43, 589824\n\t"
"s_bitcmp1_b32 s73, 0\n\t"
"s_mov_b32 s36, 0x0fff0000\n\t"
"s_cselect_b32 s36, 0xfff0, s36\n\t"
"s_mov_b32 s37, s36\n\t"
"s_mul_i32 s80, s73, 3072\n\t"
"s_add_u32 s80, s80, s8\n\t"
"s_addc_u32 s81, s9, 0x100000\n\t"
"s_mov_b32 s82, 0x1c0000\n\t"
"s_mov_b32 s83, 0x800000\n\t"
"s_add_u32 m0, s59, 24576\n\t"
"s_add_u32 s57, s58, 0\n\t"
"buffer_load_dwordx4 v29, s[52:55], s57 offen offset:0 lds\n\t"
"buffer_load_dwordx4 v29, s[52:55], s57 offen offset:3072 lds\n\t"
"s_add_u32 m0, s59, 30720\n\t"
"s_add_u32 s57, s58, 6144\n\t"
"buffer_load_dwordx4 v29, s[52:55], s57 offen offset:0 lds\n\t"
"buffer_load_dwordx4 v29, s[52:55], s57 offen offset:3072 lds\n\t"
"s_add_u32 m0, s59, 36864\n\t"
"s_add_u32 s57, s58, 221184\n\t"
"buffer_load_dwordx4 v29, s[52:55], s57 offen offset:0 lds\n\t"
"buffer_load_dwordx4 v29, s[52:55], s57 offen offset:3072 lds\n\t"
"s_add_u32 m0, s59, 82944\n\t"
"s_add_u32 s57, s58, 227328\n\t"
"buffer_load_dwordx4 v29, s[52:55], s57 offen offset:0 lds\n\t"
"buffer_load_dwordx4 v29, s[52:55], s57 offen offset:3072 lds\n\t"
"s_add_u32 m0, s59, 89088\n\t"
"s_add_u32 s57, s58, 442368\n\t"
"buffer_load_dwordx4 v29, s[52:55], s57 offen offset:0 lds\n\t"
"buffer_load_dwordx4 v29, s[52:55], s57 offen offset:3072 lds\n\t"
"s_add_u32 m0, s59, 142848\n\t"
"s_add_u32 s57, s58, 448512\n\t"
"buffer_load_dwordx4 v29, s[52:55], s57 offen offset:0 lds\n\t"
"buffer_load_dwordx4 v29, s[52:55], s57 offen offset:3072 lds\n\t"
"s_add_u32 m0, s59, 148992\n\t"
"s_add_u32 s57, s58, 147456\n\t"
"buffer_load_dwordx4 v29, s[52:55], s57 offen offset:0 lds\n\t"
"buffer_load_dwordx4 v29, s[52:55], s57 offen offset:3072 lds\n\t"
"s_add_u32 m0, s59, 155136\n\t"
"s_add_u32 s57, s58, 153600\n\t"
"buffer_load_dwordx4 v29, s[52:55], s57 offen offset:0 lds\n\t"
"buffer_load_dwordx4 v29, s[52:55], s57 offen offset:3072 lds\n\t"
"s_add_u32 m0, s59, 0\n\t"
"s_add_u32 s57, s58, 368640\n\t"
"buffer_load_dwordx4 v29, s[52:55], s57 offen offset:0 lds\n\t"
"buffer_load_dwordx4 v29, s[52:55], s57 offen offset:3072 lds\n\t"
"s_add_u32 m0, s59, 6144\n\t"
"s_add_u32 s57, s58, 374784\n\t"
"buffer_load_dwordx4 v29, s[52:55], s57 offen offset:0 lds\n\t"
"buffer_load_dwordx4 v29, s[52:55], s57 offen offset:3072 lds\n\t"
"s_add_u32 m0, s59, 12288\n\t"
"s_add_u32 s57, s58, 589824\n\t"
"buffer_load_dwordx4 v29, s[52:55], s57 offen offset:0 lds\n\t"
"buffer_load_dwordx4 v29, s[52:55], s57 offen offset:3072 lds\n\t"
"s_add_u32 m0, s59, 18432\n\t"
"s_add_u32 s57, s58, 595968\n\t"
"buffer_load_dwordx4 v29, s[52:55], s57 offen offset:0 lds\n\t"
"buffer_load_dwordx4 v29, s[52:55], s57 offen offset:3072 lds\n\t"
"s_add_u32 m0, s24, 19968\n\t"
"s_add_u32 s28, s21, 589824\n\t"
"buffer_load_dwordx4 v28, s[12:15], s28 offen offset:0 nt lds\n\t"
"s_add_u32 m0, s24, 26112\n\t"
"s_add_u32 s28, s21, 786432\n\t"
"buffer_load_dwordx4 v28, s[12:15], s28 offen offset:0 nt lds\n\t"
"s_add_u32 m0, s24, 32256\n\t"
"s_add_u32 s28, s21, 983040\n\t"
"buffer_load_dwordx4 v28, s[12:15], s28 offen offset:0 nt lds\n\t"
"s_add_u32 m0, s60, 62976\n\t"
"s_add_u32 s28, s61, 589824\n\t"
"buffer_load_dwordx4 v30, s[12:15], s28 offen nt lds\n\t"
"s_add_u32 m0, s24, 0\n\t"
"s_add_u32 s28, s21, 0\n\t"
"buffer_load_dwordx4 v28, s[12:15], s28 offen offset:0 nt lds\n\t"
"s_add_u32 m0, s24, 6144\n\t"
"s_add_u32 s28, s21, 196608\n\t"
"buffer_load_dwordx4 v28, s[12:15], s28 offen offset:0 nt lds\n\t"
"s_add_u32 m0, s24, 12288\n\t"
"s_add_u32 s28, s21, 393216\n\t"
"buffer_load_dwordx4 v28, s[12:15], s28 offen offset:0 nt lds\n\t"
"s_add_u32 m0, s60, 43008\n\t"
"s_add_u32 s28, s61, 0\n\t"
"buffer_load_dwordx4 v30, s[12:15], s28 offen nt lds\n\t"
"s_waitcnt vmcnt(42)\n\t"
"s_branch Ljoin_%=\n\t"
"Lfb_%=:\n\t"
"s_waitcnt vmcnt(0) lgkmcnt(0)\n\t"
"s_nop 15\n\t"
"v_lshrrev_b32 v255, 9, v252\n\t"
"v_lshl_add_u32 v255, v255, 10, v252\n\t"
"v_add_u32 v255, 44032, v255\n\t"
"ds_write_b128 v255, v[32:35] offset:0\n\t"
"ds_write_b128 v255, v[36:39] offset:3072\n\t"
"ds_write_b128 v255, v[40:43] offset:6144\n\t"
"ds_write_b128 v255, v[44:47] offset:9216\n\t"
"ds_write_b128 v255, v[48:51] offset:12288\n\t"
"ds_write_b128 v255, v[52:55] offset:15360\n\t"
"ds_write_b128 v255, v[56:59] offset:18432\n\t"
"ds_write_b128 v255, v[60:63] offset:21504\n\t"
"ds_write_b128 v255, v[64:67] offset:24576\n\t"
"ds_write_b128 v255, v[68:71] offset:27648\n\t"
"ds_write_b128 v255, v[72:75] offset:30720\n\t"
"ds_write_b128 v255, v[76:79] offset:33792\n\t"
"ds_write_b128 v255, v[80:83] offset:36864\n\t"
"ds_write_b128 v255, v[84:87] offset:39936\n\t"
"ds_write_b128 v255, v[88:91] offset:43008\n\t"
"ds_write_b128 v255, v[92:95] offset:46080\n\t"
"ds_write_b128 v255, v[96:99] offset:49152\n\t"
"ds_write_b128 v255, v[100:103] offset:52224\n\t"
"ds_write_b128 v255, v[104:107] offset:55296\n\t"
"ds_write_b128 v255, v[108:111] offset:58368\n\t"
"ds_write_b128 v255, v[112:115] offset:61440\n\t"
"ds_write_b128 v255, v[116:119] offset:64512\n\t"
"s_waitcnt lgkmcnt(0)\n\t"
"v_mov_b64 v[32:33], 0\n\t"
"v_mov_b64 v[34:35], 0\n\t"
"v_mov_b64 v[36:37], 0\n\t"
"v_mov_b64 v[38:39], 0\n\t"
"v_mov_b64 v[40:41], 0\n\t"
"v_mov_b64 v[42:43], 0\n\t"
"v_mov_b64 v[44:45], 0\n\t"
"v_mov_b64 v[46:47], 0\n\t"
"v_mov_b64 v[48:49], 0\n\t"
"v_mov_b64 v[50:51], 0\n\t"
"v_mov_b64 v[52:53], 0\n\t"
"v_mov_b64 v[54:55], 0\n\t"
"v_lshrrev_b32 v98, 4, v252\n\t"
"v_bfe_u32 v96, v98, 3, 2\n\t"
"v_and_b32 v97, 32, v98\n\t"
"v_lshl_or_b32 v96, v96, 1, v97\n\t"
"v_mul_u32_u24 v96, 192, v96\n\t"
"v_and_b32 v97, 7, v98\n\t"
"v_lshl_add_u32 v96, v97, 4, v96\n\t"
"s_mov_b32 s69, 0\n\t"
"Lfb1_%=:\n\t"
"s_and_b32 s60, s69, 3\n\t"
"s_lshr_b32 s61, s69, 2\n\t"
"s_mul_i32 s62, s61, 171\n\t"
"s_lshr_b32 s62, s62, 9\n\t"
"s_mul_i32 s63, s62, 3\n\t"
"s_sub_u32 s61, s61, s63\n\t"
"s_mul_i32 s63, s62, 43\n\t"
"s_lshr_b32 s63, s63, 7\n\t"
"s_mul_i32 s64, s63, 3\n\t"
"s_sub_u32 s62, s62, s64\n\t"
"s_mul_i32 s74, s62, 18\n\t"
"s_add_u32 s74, s74, s63\n\t"
"s_lshl3_add_u32 s74, s74, s60\n\t"
"s_mul_i32 s74, s74, 12\n\t"
"s_add_u32 s74, s74, s61\n\t"
"s_lshl_b32 s74, s74, 7\n\t"
"s_mov_b32 m0, 18432\n\t"
"s_add_u32 s64, s74, 0\n\t"
"buffer_load_dwordx4 v96, s[52:55], s64 offen lds\n\t"
"s_mov_b32 m0, 19456\n\t"
"s_add_u32 s64, s74, 73728\n\t"
"buffer_load_dwordx4 v96, s[52:55], s64 offen lds\n\t"
"s_mov_b32 m0, 20480\n\t"
"s_add_u32 s64, s74, 147456\n\t"
"buffer_load_dwordx4 v96, s[52:55], s64 offen lds\n\t"
"s_mul_i32 s75, s63, 288\n\t"
"s_add_u32 s75, s75, s62\n\t"
"s_lshl2_add_u32 s75, s75, s60\n\t"
"s_cmp_lt_u32 s61, 2\n\t"
"s_cselect_b64 vcc, -1, 0\n\t"
"s_cselect_b32 s61, s61, 4\n\t"
"s_mulk_i32 s61, 24\n\t"
"s_add_u32 s75, s75, s61\n\t"
"s_lshl_b32 s75, s75, 8\n\t"
"v_and_b32 v99, 12, v98\n\t"
"v_and_b32 v60, 35, v98\n\t"
"v_lshl_or_b32 v60, v99, 1, v60\n\t"
"v_bfe_u32 v99, v98, 4, 1\n\t"
"v_lshl_or_b32 v60, v99, 2, v60\n\t"
"v_cndmask_b32 v60, v98, v60, vcc\n\t"
"v_lshlrev_b32 v60, 1, v60\n\t"
"v_add_u32 v60, 18432, v60\n\t"
"v_lshrrev_b32 v97, 5, v98\n\t"
"v_lshl_or_b32 v97, v99, 2, v97\n\t"
"v_lshrrev_b32 v99, 4, v98\n\t"
"v_cndmask_b32 v97, v99, v97, vcc\n\t"
"v_mul_u32_u24 v97, 3072, v97\n\t"
"v_and_b32 v99, 15, v98\n\t"
"v_lshl_add_u32 v97, v99, 4, v97\n\t"
"s_cmp_lt_u32 s63, 2\n\t"
"s_mov_b32 s56, 0x3f2aaaab\n\t"
"s_mov_b32 s57, 0x3fc00000\n\t"
"s_mov_b32 s58, 0x3ee38e39\n\t"
"s_mov_b32 s59, 0x40100000\n\t"
"s_mov_b32 s62, 0xbf54ad4b\n\t"
"s_mov_b32 s63, 0xbefc0fc1\n\t"
"s_mov_b32 s64, 0xbe7c0fc1\n\t"
"s_mov_b32 s65, 0x3ea80a81\n\t"
"s_mov_b32 s38, 0x3f638e39\n\t"
"s_mov_b32 s39, 0x3f638e39\n\t"
"s_cselect_b32 s38, 0x3ea80a81, s38\n\t"
"s_cselect_b32 s39, 0xbebd0bd1, s39\n\t"
"s_waitcnt vmcnt(0)\n\t"
"ds_read_u16_d16_hi v32, v60 offset:0\n\t"
"ds_read_u16_d16_hi v33, v60 offset:128\n\t"
"ds_read_u16_d16_hi v40, v60 offset:1024\n\t"
"ds_read_u16_d16_hi v41, v60 offset:1152\n\t"
"ds_read_u16_d16_hi v48, v60 offset:2048\n\t"
"ds_read_u16_d16_hi v49, v60 offset:2176\n\t"
"ds_read_u16_d16_hi v34, v60 offset:256\n\t"
"ds_read_u16_d16_hi v35, v60 offset:384\n\t"
"ds_read_u16_d16_hi v42, v60 offset:1280\n\t"
"ds_read_u16_d16_hi v43, v60 offset:1408\n\t"
"ds_read_u16_d16_hi v50, v60 offset:2304\n\t"
"ds_read_u16_d16_hi v51, v60 offset:2432\n\t"
"ds_read_u16_d16_hi v36, v60 offset:512\n\t"
"ds_read_u16_d16_hi v37, v60 offset:640\n\t"
"ds_read_u16_d16_hi v44, v60 offset:1536\n\t"
"ds_read_u16_d16_hi v45, v60 offset:1664\n\t"
"ds_read_u16_d16_hi v52, v60 offset:2560\n\t"
"ds_read_u16_d16_hi v53, v60 offset:2688\n\t"
"ds_read_u16_d16_hi v38, v60 offset:768\n\t"
"ds_read_u16_d16_hi v39, v60 offset:896\n\t"
"ds_read_u16_d16_hi v46, v60 offset:1792\n\t"
"ds_read_u16_d16_hi v47, v60 offset:1920\n\t"
"ds_read_u16_d16_hi v54, v60 offset:2816\n\t"
"ds_read_u16_d16_hi v55, v60 offset:2944\n\t"
"s_waitcnt lgkmcnt(15)\n\t"
"v_pk_fma_f32 v[56:57], v[48:49], s[58:59], v[32:33] op_sel:[0,0,0] op_sel_hi:[1,0,1]\n\t"
"v_pk_fma_f32 v[58:59], v[48:49], s[58:59], v[32:33] op_sel:[0,1,0] op_sel_hi:[1,1,1]\n\t"
"v_pk_mul_f32 v[104:105], v[32:33], s[38:39] op_sel:[0,0] op_sel_hi:[1,0]\n\t"
"v_pk_mul_f32 v[114:115], v[48:49], s[38:39] op_sel:[0,1] op_sel_hi:[1,1]\n\t"
"v_pk_fma_f32 v[106:107], v[40:41], s[56:57], v[56:57] op_sel:[0,0,0] op_sel_hi:[1,0,1]\n\t"
"v_pk_fma_f32 v[108:109], v[40:41], s[56:57], v[56:57] op_sel:[0,0,0] op_sel_hi:[1,0,1] neg_lo:[1,0,0] neg_hi:[1,0,0]\n\t"
"v_pk_fma_f32 v[110:111], v[40:41], s[56:57], v[58:59] op_sel:[0,1,0] op_sel_hi:[1,1,1]\n\t"
"v_pk_fma_f32 v[112:113], v[40:41], s[56:57], v[58:59] op_sel:[0,1,0] op_sel_hi:[1,1,1] neg_lo:[1,0,0] neg_hi:[1,0,0]\n\t"
"v_pk_mul_f32 v[106:107], v[106:107], s[62:63] op_sel:[0,0] op_sel_hi:[1,0]\n\t"
"v_pk_mul_f32 v[108:109], v[108:109], s[62:63] op_sel:[0,1] op_sel_hi:[1,1]\n\t"
"v_pk_mul_f32 v[110:111], v[110:111], s[64:65] op_sel:[0,0] op_sel_hi:[1,0]\n\t"
"v_pk_mul_f32 v[112:113], v[112:113], s[64:65] op_sel:[0,1] op_sel_hi:[1,1]\n\t"
"v_cvt_pk_bf16_f32 v64, v104, v105\n\t"
"v_cvt_pk_bf16_f32 v84, v114, v115\n\t"
"v_cvt_pk_bf16_f32 v68, v106, v107\n\t"
"v_cvt_pk_bf16_f32 v74, v108, v109\n\t"
"v_cvt_pk_bf16_f32 v76, v110, v111\n\t"
"v_cvt_pk_bf16_f32 v82, v112, v113\n\t"
"s_waitcnt lgkmcnt(12)\n\t"
"v_pk_fma_f32 v[56:57], v[50:51], s[58:59], v[34:35] op_sel:[0,0,0] op_sel_hi:[1,0,1]\n\t"
"v_pk_fma_f32 v[58:59], v[50:51], s[58:59], v[34:35] op_sel:[0,1,0] op_sel_hi:[1,1,1]\n\t"
"v_pk_mul_f32 v[104:105], v[34:35], s[38:39] op_sel:[0,0] op_sel_hi:[1,0]\n\t"
"v_pk_mul_f32 v[114:115], v[50:51], s[38:39] op_sel:[0,1] op_sel_hi:[1,1]\n\t"
"v_pk_fma_f32 v[106:107], v[42:43], s[56:57], v[56:57] op_sel:[0,0,0] op_sel_hi:[1,0,1]\n\t"
"v_pk_fma_f32 v[108:109], v[42:43], s[56:57], v[56:57] op_sel:[0,0,0] op_sel_hi:[1,0,1] neg_lo:[1,0,0] neg_hi:[1,0,0]\n\t"
"v_pk_fma_f32 v[110:111], v[42:43], s[56:57], v[58:59] op_sel:[0,1,0] op_sel_hi:[1,1,1]\n\t"
"v_pk_fma_f32 v[112:113], v[42:43], s[56:57], v[58:59] op_sel:[0,1,0] op_sel_hi:[1,1,1] neg_lo:[1,0,0] neg_hi:[1,0,0]\n\t"
"v_pk_mul_f32 v[106:107], v[106:107], s[62:63] op_sel:[0,0] op_sel_hi:[1,0]\n\t"
"v_pk_mul_f32 v[108:109], v[108:109], s[62:63] op_sel:[0,1] op_sel_hi:[1,1]\n\t"
"v_pk_mul_f32 v[110:111], v[110:111], s[64:65] op_sel:[0,0] op_sel_hi:[1,0]\n\t"
"v_pk_mul_f32 v[112:113], v[112:113], s[64:65] op_sel:[0,1] op_sel_hi:[1,1]\n\t"
"v_cvt_pk_bf16_f32 v65, v104, v105\n\t"
"v_cvt_pk_bf16_f32 v85, v114, v115\n\t"
"v_cvt_pk_bf16_f32 v69, v106, v107\n\t"
"v_cvt_pk_bf16_f32 v75, v108, v109\n\t"
"v_cvt_pk_bf16_f32 v77, v110, v111\n\t"
"v_cvt_pk_bf16_f32 v83, v112, v113\n\t"
"s_waitcnt lgkmcnt(6)\n\t"
"v_pk_fma_f32 v[56:57], v[52:53], s[58:59], v[36:37] op_sel:[0,0,0] op_sel_hi:[1,0,1]\n\t"
"v_pk_fma_f32 v[58:59], v[52:53], s[58:59], v[36:37] op_sel:[0,1,0] op_sel_hi:[1,1,1]\n\t"
"v_pk_mul_f32 v[104:105], v[36:37], s[38:39] op_sel:[0,0] op_sel_hi:[1,0]\n\t"
"v_pk_mul_f32 v[114:115], v[52:53], s[38:39] op_sel:[0,1] op_sel_hi:[1,1]\n\t"
"v_pk_fma_f32 v[106:107], v[44:45], s[56:57], v[56:57] op_sel:[0,0,0] op_sel_hi:[1,0,1]\n\t"
"v_pk_fma_f32 v[108:109], v[44:45], s[56:57], v[56:57] op_sel:[0,0,0] op_sel_hi:[1,0,1] neg_lo:[1,0,0] neg_hi:[1,0,0]\n\t"
"v_pk_fma_f32 v[110:111], v[44:45], s[56:57], v[58:59] op_sel:[0,1,0] op_sel_hi:[1,1,1]\n\t"
"v_pk_fma_f32 v[112:113], v[44:45], s[56:57], v[58:59] op_sel:[0,1,0] op_sel_hi:[1,1,1] neg_lo:[1,0,0] neg_hi:[1,0,0]\n\t"
"v_pk_mul_f32 v[106:107], v[106:107], s[62:63] op_sel:[0,0] op_sel_hi:[1,0]\n\t"
"v_pk_mul_f32 v[108:109], v[108:109], s[62:63] op_sel:[0,1] op_sel_hi:[1,1]\n\t"
"v_pk_mul_f32 v[110:111], v[110:111], s[64:65] op_sel:[0,0] op_sel_hi:[1,0]\n\t"
"v_pk_mul_f32 v[112:113], v[112:113], s[64:65] op_sel:[0,1] op_sel_hi:[1,1]\n\t"
"v_cvt_pk_bf16_f32 v66, v104, v105\n\t"
"v_cvt_pk_bf16_f32 v86, v114, v115\n\t"
"v_cvt_pk_bf16_f32 v70, v106, v107\n\t"
"v_cvt_pk_bf16_f32 v72, v108, v109\n\t"
"v_cvt_pk_bf16_f32 v78, v110, v111\n\t"
"v_cvt_pk_bf16_f32 v80, v112, v113\n\t"
"s_waitcnt lgkmcnt(0)\n\t"
"v_pk_fma_f32 v[56:57], v[54:55], s[58:59], v[38:39] op_sel:[0,0,0] op_sel_hi:[1,0,1]\n\t"
"v_pk_fma_f32 v[58:59], v[54:55], s[58:59], v[38:39] op_sel:[0,1,0] op_sel_hi:[1,1,1]\n\t"
"v_pk_mul_f32 v[104:105], v[38:39], s[38:39] op_sel:[0,0] op_sel_hi:[1,0]\n\t"
"v_pk_mul_f32 v[114:115], v[54:55], s[38:39] op_sel:[0,1] op_sel_hi:[1,1]\n\t"
"v_pk_fma_f32 v[106:107], v[46:47], s[56:57], v[56:57] op_sel:[0,0,0] op_sel_hi:[1,0,1]\n\t"
"v_pk_fma_f32 v[108:109], v[46:47], s[56:57], v[56:57] op_sel:[0,0,0] op_sel_hi:[1,0,1] neg_lo:[1,0,0] neg_hi:[1,0,0]\n\t"
"v_pk_fma_f32 v[110:111], v[46:47], s[56:57], v[58:59] op_sel:[0,1,0] op_sel_hi:[1,1,1]\n\t"
"v_pk_fma_f32 v[112:113], v[46:47], s[56:57], v[58:59] op_sel:[0,1,0] op_sel_hi:[1,1,1] neg_lo:[1,0,0] neg_hi:[1,0,0]\n\t"
"v_pk_mul_f32 v[106:107], v[106:107], s[62:63] op_sel:[0,0] op_sel_hi:[1,0]\n\t"
"v_pk_mul_f32 v[108:109], v[108:109], s[62:63] op_sel:[0,1] op_sel_hi:[1,1]\n\t"
"v_pk_mul_f32 v[110:111], v[110:111], s[64:65] op_sel:[0,0] op_sel_hi:[1,0]\n\t"
"v_pk_mul_f32 v[112:113], v[112:113], s[64:65] op_sel:[0,1] op_sel_hi:[1,1]\n\t"
"v_cvt_pk_bf16_f32 v67, v104, v105\n\t"
"v_cvt_pk_bf16_f32 v87, v114, v115\n\t"
"v_cvt_pk_bf16_f32 v71, v106, v107\n\t"
"v_cvt_pk_bf16_f32 v73, v108, v109\n\t"
"v_cvt_pk_bf16_f32 v79, v110, v111\n\t"
"v_cvt_pk_bf16_f32 v81, v112, v113\n\t"
"s_add_u32 s60, s75, 0\n\t"
"buffer_store_dwordx4 v[64:67], v97, s[8:11], s60 offen sc1\n\t"
"s_add_u32 s60, s75, 184320\n\t"
"buffer_store_dwordx4 v[84:87], v97, s[8:11], s60 offen sc1\n\t"
"s_add_u32 s60, s75, 36864\n\t"
"buffer_store_dwordx4 v[68:71], v97, s[8:11], s60 offen sc1\n\t"
"s_add_u32 s60, s75, 73728\n\t"
"buffer_store_dwordx4 v[72:75], v97, s[8:11], s60 offen sc1\n\t"
"s_add_u32 s60, s75, 110592\n\t"
"buffer_store_dwordx4 v[76:79], v97, s[8:11], s60 offen sc1\n\t"
"s_add_u32 s60, s75, 147456\n\t"
"buffer_store_dwordx4 v[80:83], v97, s[8:11], s60 offen sc1\n\t"
"s_waitcnt vmcnt(0)\n\t"
"s_add_u32 s69, s69, 1\n\t"
"s_cmp_lt_u32 s69, 216\n\t"
"s_cbranch_scc1 Lfb1_%=\n\t"
"s_mov_b32 s69, %2\n\t"
"s_mov_b64 vcc, 0\n\t"
"ds_read_b128 v[32:35], v255 offset:0\n\t"
"ds_read_b128 v[36:39], v255 offset:3072\n\t"
"ds_read_b128 v[40:43], v255 offset:6144\n\t"
"ds_read_b128 v[44:47], v255 offset:9216\n\t"
"ds_read_b128 v[48:51], v255 offset:12288\n\t"
"ds_read_b128 v[52:55], v255 offset:15360\n\t"
"ds_read_b128 v[56:59], v255 offset:18432\n\t"
"ds_read_b128 v[60:63], v255 offset:21504\n\t"
"ds_read_b128 v[64:67], v255 offset:24576\n\t"
"ds_read_b128 v[68:71], v255 offset:27648\n\t"
"ds_read_b128 v[72:75], v255 offset:30720\n\t"
"ds_read_b128 v[76:79], v255 offset:33792\n\t"
"ds_read_b128 v[80:83], v255 offset:36864\n\t"
"ds_read_b128 v[84:87], v255 offset:39936\n\t"
"ds_read_b128 v[88:91], v255 offset:43008\n\t"
"ds_read_b128 v[92:95], v255 offset:46080\n\t"
"ds_read_b128 v[96:99], v255 offset:49152\n\t"
"ds_read_b128 v[100:103], v255 offset:52224\n\t"
"ds_read_b128 v[104:107], v255 offset:55296\n\t"
"ds_read_b128 v[108:111], v255 offset:58368\n\t"
"ds_read_b128 v[112:115], v255 offset:61440\n\t"
"ds_read_b128 v[116:119], v255 offset:64512\n\t"
"s_waitcnt lgkmcnt(0)\n\t"
"s_branch Lgo_%=\n\t"
".p2align 12\n\t"
"Lpro_%=:\n\t"
"s_mul_i32 s25, s73, 19968\n\t"
"s_add_u32 s25, s25, 61440\n\t"
"s_bfe_u32 s41, %2, 0x40003\n\t"
"s_cmp_eq_u32 s41, 0\n\t"
"s_cselect_b32 s40, -1, 0xbfffffff\n\t"
"s_cmp_eq_u32 s41, 15\n\t"
"s_cselect_b32 s41, -1, 0xdfffffff\n\t"
"s_and_b32 s40, s40, s41\n\t"
"s_sub_u32 s42, s21, 384\n\t"
"s_mul_i32 s41, s73, 589824\n\t"
"s_add_u32 s42, s42, s41\n\t"
"s_and_b32 s41, s40, 536883584\n\t"
"s_and_b32 s40, s40, 2.0\n\t"
"v_bfe_u32 v26, v205, 2, 3\n\t"
"v_add_u32 v27, -1, v26\n\t"
"v_cmp_gt_u32 vcc, 6, v27\n\t"
"v_lshrrev_b32 v27, 1, v27\n\t"
"v_mul_u32_u24 v27, 196608, v27\n\t"
"v_and_b32 v20, 0x70, v28\n\t"
"v_add3_u32 v27, v27, v20, s42\n\t"
"v_bfe_i32 v20, v205, 2, 1\n\t"
"v_mov_b32 v21, s40\n\t"
"v_bfi_b32 v20, v20, s41, v21\n\t"
"v_add_u32 v27, v27, v20\n\t"
"v_bfrev_b32 v20, 8\n\t"
"v_cndmask_b32 v254, v20, v27, vcc\n\t"
"v_bfe_u32 v26, v205, 2, 3\n\t"
"v_bfe_u32 v27, v205, 5, 1\n\t"
"v_mul_u32_u24 v27, 3072, v27\n\t"
"v_lshl_add_u32 v27, v26, 7, v27\n\t"
"v_and_b32 v26, 6, v26\n\t"
"v_lshrrev_b32 v20, 4, v205\n\t"
"v_and_b32 v20, 4, v20\n\t"
"v_and_or_b32 v20, v205, 3, v20\n\t"
"v_xor_b32 v20, v20, v26\n\t"
"v_lshl_add_u32 v20, v20, 4, v27\n\t"
"v_lshrrev_b32 v21, 7, v205\n\t"
"v_lshl_add_u32 v20, v21, 10, v20\n\t"
"v_lshrrev_b32 v21, 5, v205\n\t"
"v_and_b32 v21, 6, v21\n\t"
"v_bfe_u32 v22, v205, 1, 1\n\t"
"v_or_b32 v21, v21, v22\n\t"
"v_xor_b32 v21, v21, v26\n\t"
"v_lshl_add_u32 v21, v21, 4, v27\n\t"
"v_and_b32 v22, 1, v205\n\t"
"v_lshl_add_u32 v21, v22, 3, v21\n\t"
"v_add_u32 v22, 142848, v20\n\t"
"v_add_u32 v23, 142848, v21\n\t"
"v_add_u32 v24, 82944, v20\n\t"
"v_add_u32 v25, 82944, v21\n\t"
"s_and_b32 s17, s17, 0xffff\n\t"
"s_mov_b32 s18, 100663296\n\t"
"s_mov_b32 s19, 0x20000\n\t"
"s_lshr_b32 s41, s73, 1\n\t"
"s_add_u32 s42, s21, 196608\n\t"
"s_mul_i32 s43, s41, 384\n\t"
"s_add_u32 s22, s42, s43\n\t"
"s_lshl1_add_u32 s40, s43, s22\n\t"
"s_mul_i32 s43, s41, 196608\n\t"
"s_add_u32 s23, s42, s43\n\t"
"s_mov_b32 s44, 0x0fff0fff\n\t"
"s_mov_b32 s45, 0x0fff0fff\n\t"
"s_mul_i32 s26, s41, 19968\n\t"
"s_add_u32 s26, s26, 141312\n\t"
"s_sub_u32 s43, s41, s73\n\t"
"s_add_u32 s43, s43, 4\n\t"
"s_mul_i32 s27, s43, 589824\n\t"
"s_bitcmp1_b32 s73, 0\n\t"
"s_mov_b32 s36, 0x0fff0000\n\t"
"s_cselect_b32 s36, 0xfff0, s36\n\t"
"s_mov_b32 s37, s36\n\t"
"s_mul_i32 s80, s73, 3072\n\t"
"s_add_u32 s80, s80, s8\n\t"
"s_addc_u32 s81, s9, 0x100000\n\t"
"s_mov_b32 s82, 0x1c0000\n\t"
"s_mov_b32 s83, 0x800000\n\t"
"s_add_u32 m0, s59, 148992\n\t"
"s_add_u32 s57, s58, 147456\n\t"
"buffer_load_dwordx4 v29, s[52:55], s57 offen offset:0 lds\n\t"
"buffer_load_dwordx4 v29, s[52:55], s57 offen offset:3072 lds\n\t"
"s_add_u32 m0, s59, 155136\n\t"
"s_add_u32 s57, s58, 153600\n\t"
"buffer_load_dwordx4 v29, s[52:55], s57 offen offset:0 lds\n\t"
"buffer_load_dwordx4 v29, s[52:55], s57 offen offset:3072 lds\n\t"
"s_add_u32 m0, s59, 0\n\t"
"s_add_u32 s57, s58, 368640\n\t"
"buffer_load_dwordx4 v29, s[52:55], s57 offen offset:0 lds\n\t"
"buffer_load_dwordx4 v29, s[52:55], s57 offen offset:3072 lds\n\t"
"s_add_u32 m0, s59, 6144\n\t"
"s_add_u32 s57, s58, 374784\n\t"
"buffer_load_dwordx4 v29, s[52:55], s57 offen offset:0 lds\n\t"
"buffer_load_dwordx4 v29, s[52:55], s57 offen offset:3072 lds\n\t"
"s_add_u32 m0, s59, 12288\n\t"
"s_add_u32 s57, s58, 589824\n\t"
"buffer_load_dwordx4 v29, s[52:55], s57 offen offset:0 lds\n\t"
"buffer_load_dwordx4 v29, s[52:55], s57 offen offset:3072 lds\n\t"
"s_add_u32 m0, s59, 18432\n\t"
"s_add_u32 s57, s58, 595968\n\t"
"buffer_load_dwordx4 v29, s[52:55], s57 offen offset:0 lds\n\t"
"buffer_load_dwordx4 v29, s[52:55], s57 offen offset:3072 lds\n\t"
"s_add_u32 m0, s25, 0\n\t"
"s_nop 0\n\t"
"buffer_load_dwordx4 v254, s[12:15], 0 offen nt lds\n\t"
"s_cmp_eq_u32 s73, 3\n\t"
"s_cselect_b32 s62, -1, 1\n\t"
"s_mul_i32 s63, s62, 59904\n\t"
"s_mul_i32 s62, s62, 1769472\n\t"
"v_add_u32 v19, s62, v254\n\t"
"s_add_u32 m0, s25, s63\n\t"
"s_nop 0\n\t"
"buffer_load_dwordx4 v19, s[12:15], 0 offen nt lds\n\t"
"s_add_u32 m0, s24, 59904\n\t"
"s_add_u32 s28, s21, 1769472\n\t"
"buffer_load_dwordx4 v28, s[12:15], s28 offen offset:0 nt lds\n\t"
"s_add_u32 m0, s24, 66048\n\t"
"s_add_u32 s28, s21, 1966080\n\t"
"buffer_load_dwordx4 v28, s[12:15], s28 offen offset:0 nt lds\n\t"
"s_add_u32 m0, s24, 72192\n\t"
"s_add_u32 s28, s21, 2162688\n\t"
"buffer_load_dwordx4 v28, s[12:15], s28 offen offset:0 nt lds\n\t"
"s_mul_i32 s60, s59, 6\n\t"
"s_mul_i32 s61, s59, 192\n\t"
"s_add_u32 s61, s61, s21\n\t"
"s_mul_i32 s62, s73, 3072\n\t"
"v_subrev_u32 v30, s62, v28\n\t"
"s_add_u32 m0, s60, 102912\n\t"
"s_add_u32 s28, s61, 1769472\n\t"
"buffer_load_dwordx4 v30, s[12:15], s28 offen nt lds\n\t"
"s_add_u32 m0, s24, 79872\n\t"
"s_add_u32 s28, s21, 2359296\n\t"
"buffer_load_dwordx4 v28, s[12:15], s28 offen offset:0 nt lds\n\t"
"s_add_u32 m0, s24, 86016\n\t"
"s_add_u32 s28, s21, 2555904\n\t"
"buffer_load_dwordx4 v28, s[12:15], s28 offen offset:0 nt lds\n\t"
"s_add_u32 m0, s24, 92160\n\t"
"s_add_u32 s28, s21, 2752512\n\t"
"buffer_load_dwordx4 v28, s[12:15], s28 offen offset:0 nt lds\n\t"
"s_add_u32 m0, s60, 122880\n\t"
"s_add_u32 s28, s61, 2359296\n\t"
"buffer_load_dwordx4 v30, s[12:15], s28 offen nt lds\n\t"
"s_add_u32 m0, s24, 19968\n\t"
"s_add_u32 s28, s21, 589824\n\t"
"buffer_load_dwordx4 v28, s[12:15], s28 offen offset:0 nt lds\n\t"
"s_add_u32 m0, s24, 26112\n\t"
"s_add_u32 s28, s21, 786432\n\t"
"buffer_load_dwordx4 v28, s[12:15], s28 offen offset:0 nt lds\n\t"
"s_add_u32 m0, s24, 32256\n\t"
"s_add_u32 s28, s21, 983040\n\t"
"buffer_load_dwordx4 v28, s[12:15], s28 offen offset:0 nt lds\n\t"
"s_add_u32 m0, s60, 62976\n\t"
"s_add_u32 s28, s61, 589824\n\t"
"buffer_load_dwordx4 v30, s[12:15], s28 offen nt lds\n\t"
"s_add_u32 m0, s24, 0\n\t"
"s_add_u32 s28, s21, 0\n\t"
"buffer_load_dwordx4 v28, s[12:15], s28 offen offset:0 nt lds\n\t"
"s_add_u32 m0, s24, 6144\n\t"
"s_add_u32 s28, s21, 196608\n\t"
"buffer_load_dwordx4 v28, s[12:15], s28 offen offset:0 nt lds\n\t"
"s_add_u32 m0, s24, 12288\n\t"
"s_add_u32 s28, s21, 393216\n\t"
"buffer_load_dwordx4 v28, s[12:15], s28 offen offset:0 nt lds\n\t"
"s_add_u32 m0, s60, 43008\n\t"
"s_add_u32 s28, s61, 0\n\t"
"buffer_load_dwordx4 v30, s[12:15], s28 offen nt lds\n\t"
"s_waitcnt vmcnt(42)\n\t"
"Ljoin_%=:\n\t"
"s_mov_b32 s41, 0xc0423f90\n\t"
"s_movk_i32 s43, 0x101\n\t"
"v_mov_b32 v6, 0x01000100\n\t"
"v_mov_b32 v7, 0xbebd0bd1\n\t"
"v_lshrrev_b32 v240, 16, v244\n\t"
"v_lshrrev_b32 v242, 16, v245\n\t"
"v_xor_b32 v241, 32, v240\n\t"
"v_and_b32 v244, 0xffff, v244\n\t"
"v_and_b32 v245, 0xffff, v245\n\t"
"v_cmp_le_u32 vcc, 9216, v242\n\t"
"v_mov_b32 v243, 0x3ea80a81\n\t"
"v_cndmask_b32 v243, v243, v7, vcc\n\t"
"v_and_b32 v8, 0xf000f, v247\n\t"
"v_mad_u32_u24 v8, v8, s43, v6\n\t"
"v_perm_b32 v196, s41, 0, v8\n\t"
"v_lshrrev_b32 v9, 4, v247\n\t"
"v_and_b32 v9, 0xf000f, v9\n\t"
"v_mad_u32_u24 v9, v9, s43, v6\n\t"
"v_perm_b32 v197, s41, 0, v9\n\t"
"v_lshrrev_b32 v10, 8, v247\n\t"
"v_and_b32 v10, 0xf000f, v10\n\t"
"v_mad_u32_u24 v10, v10, s43, v6\n\t"
"v_perm_b32 v198, s41, 0, v10\n\t"
"v_lshrrev_b32 v11, 12, v247\n\t"
"v_and_b32 v11, 0xf000f, v11\n\t"
"v_mad_u32_u24 v11, v11, s43, v6\n\t"
"v_perm_b32 v199, s41, 0, v11\n\t"
"v_mov_b32 v247, v28\n\t"
"s_mov_b64 vcc, -1\n\t"
"s_bitcmp1_b32 s73, 1\n\t"
"s_cbranch_scc1 Lvh0_%=\n\t"
"s_bitcmp1_b32 s73, 0\n\t"
"s_cbranch_scc1 Lpg2_%=\n\t"
"s_branch Lpg1_%=\n\t"
"Lvh0_%=:\n\t"
"s_bitcmp1_b32 s73, 0\n\t"
"s_cbranch_scc1 Lpg4_%=\n\t"
"s_branch Lpg3_%=\n\t"
"Lvr0_%=:\n\t"
"s_mov_b64 vcc, 0\n\t"
"v_add_u32 v240, 59904, v240\n\t"
"v_add_u32 v241, 59904, v241\n\t"
"s_cmp_lt_u32 s69, 216\n\t"
"s_cbranch_scc0 Lna1_%=\n\t"
"s_waitcnt vmcnt(0)\n\t"
"s_add_u32 s71, s71, 1\n\t"
"v_mov_b32 v31, s71\n\t"
"s_mov_b64 exec, 1\n\t"
"buffer_store_dword v31, off, s[8:11], s70 sc1\n\t"
"s_mov_b64 exec, -1\n\t"
"Lna1_%=:\n\t"
"s_cmp_eq_u32 s73, 0\n\t"
"s_cbranch_scc1 Lsk0_%=\n\t"
"s_lshr_b32 s56, %2, 3\n\t"
"s_bitcmp1_b32 0xaaaaaaaa, s56\n\t"
"s_cbranch_scc1 Lkc2_%=\n\t"
"s_waitcnt vmcnt(8)\n\t"
"s_branch Lke2_%=\n\t"
"Lkc2_%=:\n\t"
"s_waitcnt vmcnt(32)\n\t"
"Lke2_%=:\n\t"
"Lsk0_%=:\n\t"
"s_barrier\n\t"
"ds_read_b64_tr_b16 v[120:121], v240 offset:0\n\t"
"ds_read_b64_tr_b16 v[122:123], v240 offset:1536\n\t"
"ds_read_b64_tr_b16 v[124:125], v241 offset:0\n\t"
"ds_read_b64_tr_b16 v[126:127], v241 offset:1536\n\t"
"ds_read_b64_tr_b16 v[128:129], v240 offset:19968\n\t"
"ds_read_b64_tr_b16 v[130:131], v240 offset:21504\n\t"
"ds_read_b64_tr_b16 v[132:133], v241 offset:19968\n\t"
"ds_read_b64_tr_b16 v[134:135], v241 offset:21504\n\t"
"s_waitcnt lgkmcnt(6)\n\t"
"v_mfma_f32_16x16x32_bf16 v[120:123], v[120:123], v[196:199], 0\n\t"
"s_waitcnt lgkmcnt(4)\n\t"
"v_mfma_f32_16x16x32_bf16 v[124:127], v[124:127], v[196:199], 0\n\t"
"s_waitcnt lgkmcnt(2)\n\t"
"v_mfma_f32_16x16x32_bf16 v[128:131], v[128:131], v[196:199], 0\n\t"
"s_waitcnt lgkmcnt(0)\n\t"
"v_mfma_f32_16x16x32_bf16 v[132:135], v[132:135], v[196:199], 0\n\t"
"v_pk_mul_f32 v[120:121], v[120:121], v[242:243] op_sel:[0,1] op_sel_hi:[1,1]\n\t"
"v_pk_mul_f32 v[122:123], v[122:123], v[242:243] op_sel:[0,1] op_sel_hi:[1,1]\n\t"
"v_cvt_pk_bf16_f32 v120, v120, v121\n\t"
"v_cvt_pk_bf16_f32 v121, v122, v123\n\t"
"v_pk_mul_f32 v[124:125], v[124:125], v[242:243] op_sel:[0,1] op_sel_hi:[1,1]\n\t"
"v_pk_mul_f32 v[126:127], v[126:127], v[242:243] op_sel:[0,1] op_sel_hi:[1,1]\n\t"
"v_cvt_pk_bf16_f32 v122, v124, v125\n\t"
"v_cvt_pk_bf16_f32 v123, v126, v127\n\t"
"v_pk_mul_f32 v[128:129], v[128:129], v[242:243] op_sel:[0,1] op_sel_hi:[1,1]\n\t"
"v_pk_mul_f32 v[130:131], v[130:131], v[242:243] op_sel:[0,1] op_sel_hi:[1,1]\n\t"
"v_cvt_pk_bf16_f32 v128, v128, v129\n\t"
"v_cvt_pk_bf16_f32 v129, v130, v131\n\t"
"v_pk_mul_f32 v[132:133], v[132:133], v[242:243] op_sel:[0,1] op_sel_hi:[1,1]\n\t"
"v_pk_mul_f32 v[134:135], v[134:135], v[242:243] op_sel:[0,1] op_sel_hi:[1,1]\n\t"
"v_cvt_pk_bf16_f32 v130, v132, v133\n\t"
"v_cvt_pk_bf16_f32 v131, v134, v135\n\t"
"s_cmp_eq_u32 s73, 0\n\t"
"s_cbranch_scc1 Lsk1_%=\n\t"
"s_lshr_b32 s56, %2, 3\n\t"
"s_bitcmp1_b32 0xaaaaaaaa, s56\n\t"
"s_cbranch_scc1 Lkc3_%=\n\t"
"s_branch Lke3_%=\n\t"
"Lkc3_%=:\n\t"
"s_waitcnt vmcnt(8)\n\t"
"Lke3_%=:\n\t"
"Lsk1_%=:\n\t"
"s_barrier\n\t"
"ds_read_b64_tr_b16 a[8:9], v20 offset:24576\n\t"
"ds_read_b64_tr_b16 a[10:11], v20 offset:30720\n\t"
"ds_read_b64_tr_b16 a[32:33], v20 offset:24584\n\t"
"ds_read_b64_tr_b16 a[34:35], v20 offset:30728\n\t"
"ds_read_b64_tr_b16 a[56:57], v21 offset:26624\n\t"
"ds_read_b64_tr_b16 a[58:59], v21 offset:32768\n\t"
"ds_read_b64_tr_b16 a[16:17], v20 offset:36864\n\t"
"ds_read_b64_tr_b16 a[18:19], v24 offset:0\n\t"
"ds_read_b64_tr_b16 a[40:41], v20 offset:36872\n\t"
"ds_read_b64_tr_b16 a[42:43], v24 offset:8\n\t"
"ds_read_b64_tr_b16 a[64:65], v21 offset:38912\n\t"
"ds_read_b64_tr_b16 a[66:67], v25 offset:2048\n\t"
"ds_read_b64_tr_b16 a[24:25], v24 offset:6144\n\t"
"ds_read_b64_tr_b16 a[26:27], v22 offset:0\n\t"
"ds_read_b64_tr_b16 a[48:49], v24 offset:6152\n\t"
"ds_read_b64_tr_b16 a[50:51], v22 offset:8\n\t"
"ds_read_b64_tr_b16 a[72:73], v25 offset:8192\n\t"
"ds_read_b64_tr_b16 a[74:75], v23 offset:2048\n\t"
"ds_read_b64_tr_b16 a[80:81], v22 offset:6144\n\t"
"ds_read_b64_tr_b16 a[82:83], v22 offset:12288\n\t"
"ds_read_b64_tr_b16 a[104:105], v22 offset:6152\n\t"
"ds_read_b64_tr_b16 a[106:107], v22 offset:12296\n\t"
"ds_read_b64_tr_b16 a[128:129], v23 offset:8192\n\t"
"ds_read_b64_tr_b16 a[130:131], v23 offset:14336\n\t"
"s_waitcnt lgkmcnt(0)\n\t"
"s_barrier\n\t"
"s_cmp_eq_u32 s73, 0\n\t"
"s_cbranch_scc1 Lsk2_%=\n\t"
"s_add_u32 m0, s24, 39936\n\t"
"s_add_u32 s28, s21, 1179648\n\t"
"buffer_load_dwordx4 v28, s[12:15], s28 offen offset:0 nt lds\n\t"
"s_add_u32 m0, s24, 46080\n\t"
"s_add_u32 s28, s21, 1376256\n\t"
"buffer_load_dwordx4 v28, s[12:15], s28 offen offset:0 nt lds\n\t"
"s_add_u32 m0, s24, 52224\n\t"
"s_add_u32 s28, s21, 1572864\n\t"
"buffer_load_dwordx4 v28, s[12:15], s28 offen offset:0 nt lds\n\t"
"s_add_u32 m0, s60, 82944\n\t"
"s_add_u32 s28, s61, 1179648\n\t"
"buffer_load_dwordx4 v30, s[12:15], s28 offen nt lds\n\t"
"Lsk2_%=:\n\t"
"ds_read_b64_tr_b16 a[88:89], v20 offset:0\n\t"
"ds_read_b64_tr_b16 a[90:91], v20 offset:6144\n\t"
"ds_read_b64_tr_b16 a[112:113], v20 offset:8\n\t"
"ds_read_b64_tr_b16 a[114:115], v20 offset:6152\n\t"
"ds_read_b64_tr_b16 a[136:137], v21 offset:2048\n\t"
"ds_read_b64_tr_b16 a[138:139], v21 offset:8192\n\t"
"ds_read_b64_tr_b16 a[96:97], v20 offset:12288\n\t"
"ds_read_b64_tr_b16 a[98:99], v20 offset:18432\n\t"
"ds_read_b64_tr_b16 a[120:121], v20 offset:12296\n\t"
"ds_read_b64_tr_b16 a[122:123], v20 offset:18440\n\t"
"ds_read_b64_tr_b16 a[144:145], v21 offset:14336\n\t"
"ds_read_b64_tr_b16 a[146:147], v21 offset:20480\n\t"
"ds_write_b128 v242, v[120:123] offset:29184\n\t"
"ds_write_b128 v242, v[128:131] offset:30720\n\t"
"s_waitcnt lgkmcnt(0)\n\t"
"v_subrev_u32 v240, 59904, v240\n\t"
"v_subrev_u32 v241, 59904, v241\n\t"
"s_cmp_eq_u32 s73, 0\n\t"
"s_cbranch_scc1 Lsk3_%=\n\t"
"s_waitcnt vmcnt(4)\n\t"
"Lsk3_%=:\n\t"
"s_barrier\n\t"
"ds_read_b64_tr_b16 v[136:137], v240 offset:19968\n\t"
"ds_read_b64_tr_b16 v[138:139], v240 offset:21504\n\t"
"ds_read_b64_tr_b16 v[140:141], v241 offset:19968\n\t"
"ds_read_b64_tr_b16 v[142:143], v241 offset:21504\n\t"
"ds_read_b64_tr_b16 v[144:145], v240 offset:0\n\t"
"ds_read_b64_tr_b16 v[146:147], v240 offset:1536\n\t"
"ds_read_b64_tr_b16 v[148:149], v241 offset:0\n\t"
"ds_read_b64_tr_b16 v[150:151], v241 offset:1536\n\t"
"s_cmp_eq_u32 s73, 0\n\t"
"s_cbranch_scc1 Lsk4_%=\n\t"
"s_add_u32 m0, s24, 99840\n\t"
"s_add_u32 s28, s21, 2949120\n\t"
"buffer_load_dwordx4 v28, s[12:15], s28 offen offset:0 nt lds\n\t"
"s_add_u32 m0, s24, 105984\n\t"
"s_add_u32 s28, s21, 3145728\n\t"
"buffer_load_dwordx4 v28, s[12:15], s28 offen offset:0 nt lds\n\t"
"s_add_u32 m0, s24, 112128\n\t"
"s_add_u32 s28, s21, 3342336\n\t"
"buffer_load_dwordx4 v28, s[12:15], s28 offen offset:0 nt lds\n\t"
"s_add_u32 m0, s60, 142848\n\t"
"s_add_u32 s28, s61, 2949120\n\t"
"buffer_load_dwordx4 v30, s[12:15], s28 offen nt lds\n\t"
"Lsk4_%=:\n\t"
"s_waitcnt lgkmcnt(6)\n\t"
"v_mfma_f32_16x16x32_bf16 v[136:139], v[136:139], v[196:199], 0\n\t"
"s_waitcnt lgkmcnt(4)\n\t"
"v_mfma_f32_16x16x32_bf16 v[140:143], v[140:143], v[196:199], 0\n\t"
"s_waitcnt lgkmcnt(2)\n\t"
"v_mfma_f32_16x16x32_bf16 v[144:147], v[144:147], v[196:199], 0\n\t"
"s_waitcnt lgkmcnt(0)\n\t"
"v_mfma_f32_16x16x32_bf16 v[148:151], v[148:151], v[196:199], 0\n\t"
"v_pk_mul_f32 v[136:137], v[136:137], v[242:243] op_sel:[0,1] op_sel_hi:[1,1]\n\t"
"v_pk_mul_f32 v[138:139], v[138:139], v[242:243] op_sel:[0,1] op_sel_hi:[1,1]\n\t"
"v_cvt_pk_bf16_f32 v136, v136, v137\n\t"
"v_cvt_pk_bf16_f32 v137, v138, v139\n\t"
"v_pk_mul_f32 v[140:141], v[140:141], v[242:243] op_sel:[0,1] op_sel_hi:[1,1]\n\t"
"v_pk_mul_f32 v[142:143], v[142:143], v[242:243] op_sel:[0,1] op_sel_hi:[1,1]\n\t"
"v_cvt_pk_bf16_f32 v138, v140, v141\n\t"
"v_cvt_pk_bf16_f32 v139, v142, v143\n\t"
"ds_write_b128 v242, v[136:139] offset:26112\n\t"
"v_pk_mul_f32 v[144:145], v[144:145], v[242:243] op_sel:[0,1] op_sel_hi:[1,1]\n\t"
"v_pk_mul_f32 v[146:147], v[146:147], v[242:243] op_sel:[0,1] op_sel_hi:[1,1]\n\t"
"v_cvt_pk_bf16_f32 v144, v144, v145\n\t"
"v_cvt_pk_bf16_f32 v145, v146, v147\n\t"
"v_pk_mul_f32 v[148:149], v[148:149], v[242:243] op_sel:[0,1] op_sel_hi:[1,1]\n\t"
"v_pk_mul_f32 v[150:151], v[150:151], v[242:243] op_sel:[0,1] op_sel_hi:[1,1]\n\t"
"v_cvt_pk_bf16_f32 v146, v148, v149\n\t"
"v_cvt_pk_bf16_f32 v147, v150, v151\n\t"
"ds_write_b128 v242, v[144:147] offset:24576\n\t"
"s_mov_b64 vcc, 0\n\t"
"s_waitcnt lgkmcnt(0)\n\t"
"s_barrier\n\t"
"ds_read_b128 a[168:171], v244 offset:26112\n\t"
"ds_read_b128 a[172:175], v244 offset:26624\n\t"
"ds_read_b128 a[176:179], v244 offset:27136\n\t"
"ds_read_b128 a[180:183], v244 offset:35328\n\t"
"ds_read_b128 a[184:187], v244 offset:35840\n\t"
"ds_read_b128 a[188:191], v244 offset:36352\n\t"
"ds_read_b128 a[192:195], v244 offset:25600\n\t"
"s_waitcnt lgkmcnt(4)\n\t"
"v_mfma_f32_16x16x32_bf16 v[14:17], a[8:11], a[168:171], 0\n\t"
"ds_read_b64_tr_b16 v[224:225], v240 offset:512\n\t"
"v_mfma_f32_16x16x32_bf16 v[14:17], a[16:19], a[172:175], v[14:17]\n\t"
"ds_read_b64_tr_b16 v[226:227], v240 offset:2048\n\t"
"v_mfma_f32_16x16x32_bf16 v[14:17], a[24:27], a[176:179], v[14:17]\n\t"
"ds_read_b64_tr_b16 v[228:229], v241 offset:512\n\t"
"v_mfma_f32_16x16x32_bf16 v[46:49], a[40:43], a[172:175], 0\n\t"
"ds_read_b64_tr_b16 v[230:231], v241 offset:2048\n\t"
"v_mfma_f32_16x16x32_bf16 v[46:49], a[48:51], a[176:179], v[46:49]\n\t"
"v_mfma_f32_16x16x32_bf16 v[46:49], a[32:35], a[168:171], v[46:49]\n\t"
"v_mfma_f32_16x16x32_bf16 v[78:81], a[56:59], a[168:171], 0\n\t"
"s_waitcnt lgkmcnt(5)\n\t"
"v_mfma_f32_16x16x32_bf16 v[78:81], a[72:75], a[176:179], v[78:81]\n\t"
"ds_read_b64_tr_b16 v[232:233], v240 offset:20480\n\t"
"v_mfma_f32_16x16x32_bf16 v[78:81], a[64:67], a[172:175], v[78:81]\n\t"
"ds_read_b64_tr_b16 v[234:235], v240 offset:22016\n\t"
"v_mfma_f32_16x16x32_bf16 v[112:115], a[80:83], a[180:183], 0\n\t"
"s_waitcnt lgkmcnt(4)\n\t"
"ds_read_b64_tr_b16 v[236:237], v241 offset:20480\n\t"
"v_mfma_f32_16x16x32_bf16 v[112:115], a[88:91], a[184:187], v[112:115]\n\t"
"s_waitcnt lgkmcnt(3)\n\t"
"ds_read_b64_tr_b16 v[238:239], v241 offset:22016\n\t"
"v_mfma_f32_16x16x32_bf16 v[112:115], a[96:99], a[188:191], v[112:115]\n\t"
"ds_read_b128 a[176:179], v244 offset:34816\n\t"
"v_mfma_f32_16x16x32_bf16 v[224:227], v[224:227], v[196:199], 0\n\t"
"v_mfma_f32_16x16x32_bf16 v[228:231], v[228:231], v[196:199], 0\n\t"
"v_mfma_f32_16x16x32_bf16 v[144:147], a[120:123], a[188:191], 0\n\t"
"v_mfma_f32_16x16x32_bf16 v[144:147], a[104:107], a[180:183], v[144:147]\n\t"
"v_mfma_f32_16x16x32_bf16 v[144:147], a[112:115], a[184:187], v[144:147]\n\t"
"v_mfma_f32_16x16x32_bf16 v[176:179], a[128:131], a[180:183], 0\n\t"
"v_cvt_pk_bf16_f32 v224, v224, v225\n\t"
"v_cvt_pk_bf16_f32 v225, v226, v227\n\t"
"v_mfma_f32_16x16x32_bf16 v[176:179], a[136:139], a[184:187], v[176:179]\n\t"
"ds_read_b128 a[196:199], v244 offset:25088\n\t"
"v_cvt_pk_bf16_f32 v226, v228, v229\n\t"
"v_mfma_f32_16x16x32_bf16 v[176:179], a[144:147], a[188:191], v[176:179]\n\t"
"v_cvt_pk_bf16_f32 v227, v230, v231\n\t"
"v_mfma_f32_16x16x32_bf16 v[10:13], a[8:11], a[192:195], 0\n\t"
"s_waitcnt lgkmcnt(4)\n\t"
"v_mfma_f32_16x16x32_bf16 v[10:13], a[24:27], a[172:175], v[10:13]\n\t"
"s_waitcnt lgkmcnt(2)\n\t"
"v_mfma_f32_16x16x32_bf16 v[10:13], a[16:19], a[168:171], v[10:13]\n\t"
"v_mfma_f32_16x16x32_bf16 v[232:235], v[232:235], v[196:199], 0\n\t"
"v_mfma_f32_16x16x32_bf16 v[236:239], v[236:239], v[196:199], 0\n\t"
"s_waitcnt vmcnt(4)\n\t"
"s_barrier\n\t"
"v_mfma_f32_16x16x32_bf16 v[42:45], a[40:43], a[168:171], 0\n\t"
"ds_read_b64_tr_b16 v[228:229], v240 offset:39936\n\t"
"v_mfma_f32_16x16x32_bf16 v[42:45], a[32:35], a[192:195], v[42:45]\n\t"
"ds_read_b64_tr_b16 v[230:231], v240 offset:41472\n\t"
"v_mfma_f32_16x16x32_bf16 v[42:45], a[48:51], a[172:175], v[42:45]\n\t"
"ds_write_b128 v242, v[224:227] offset:0\n\t"
"v_mfma_f32_16x16x32_bf16 v[74:77], a[56:59], a[192:195], 0\n\t"
"s_waitcnt lgkmcnt(4)\n\t"
"v_cvt_pk_bf16_f32 v232, v232, v233\n\t"
"v_cvt_pk_bf16_f32 v233, v234, v235\n\t"
"v_mfma_f32_16x16x32_bf16 v[74:77], a[72:75], a[172:175], v[74:77]\n\t"
"ds_read_b64_tr_b16 v[224:225], v241 offset:39936\n\t"
"v_mfma_f32_16x16x32_bf16 v[74:77], a[64:67], a[168:171], v[74:77]\n\t"
"ds_read_b64_tr_b16 v[226:227], v241 offset:41472\n\t"
"v_cvt_pk_bf16_f32 v234, v236, v237\n\t"
"v_mfma_f32_16x16x32_bf16 v[108:111], a[80:83], a[176:179], 0\n\t"
"s_waitcnt lgkmcnt(3)\n\t"
"ds_read_b128 a[172:175], v244 offset:34304\n\t"
"v_cvt_pk_bf16_f32 v235, v238, v239\n\t"
"v_mfma_f32_16x16x32_bf16 v[108:111], a[96:99], a[184:187], v[108:111]\n\t"
"ds_read_b64_tr_b16 v[236:237], v240 offset:40448\n\t"
"v_mfma_f32_16x16x32_bf16 v[108:111], a[88:91], a[180:183], v[108:111]\n\t"
"ds_write_b128 v242, v[232:235] offset:1536\n\t"
"v_mfma_f32_16x16x32_bf16 v[228:231], v[228:231], v[196:199], 0\n\t"
"s_waitcnt lgkmcnt(3)\n\t"
"v_mfma_f32_16x16x32_bf16 v[224:227], v[224:227], v[196:199], 0\n\t"
"ds_read_b64_tr_b16 v[238:239], v240 offset:41984\n\t"
"v_mfma_f32_16x16x32_bf16 v[140:143], a[112:115], a[180:183], 0\n\t"
"ds_read_b64_tr_b16 v[232:233], v241 offset:40448\n\t"
"v_mfma_f32_16x16x32_bf16 v[140:143], a[104:107], a[176:179], v[140:143]\n\t"
"ds_read_b64_tr_b16 v[234:235], v241 offset:41984\n\t"
"v_mfma_f32_16x16x32_bf16 v[140:143], a[120:123], a[184:187], v[140:143]\n\t"
"v_pk_mul_f32 v[228:229], v[228:229], v[242:243] op_sel:[0,1] op_sel_hi:[1,1]\n\t"
"v_pk_mul_f32 v[230:231], v[230:231], v[242:243] op_sel:[0,1] op_sel_hi:[1,1]\n\t"
"v_mfma_f32_16x16x32_bf16 v[172:175], a[128:131], a[176:179], 0\n\t"
"v_cvt_pk_bf16_f32 v228, v228, v229\n\t"
"v_cvt_pk_bf16_f32 v229, v230, v231\n\t"
"v_mfma_f32_16x16x32_bf16 v[172:175], a[144:147], a[184:187], v[172:175]\n\t"
"v_mfma_f32_16x16x32_bf16 v[172:175], a[136:139], a[180:183], v[172:175]\n\t"
"v_pk_mul_f32 v[224:225], v[224:225], v[242:243] op_sel:[0,1] op_sel_hi:[1,1]\n\t"
"v_pk_mul_f32 v[226:227], v[226:227], v[242:243] op_sel:[0,1] op_sel_hi:[1,1]\n\t"
"v_mfma_f32_16x16x32_bf16 v[6:9], a[8:11], a[196:199], 0\n\t"
"v_cvt_pk_bf16_f32 v230, v224, v225\n\t"
"v_cvt_pk_bf16_f32 v231, v226, v227\n\t"
"v_mfma_f32_16x16x32_bf16 v[6:9], a[16:19], a[192:195], v[6:9]\n\t"
"v_add_u32 v240, 59904, v240\n\t"
"v_add_u32 v241, 59904, v241\n\t"
"v_mfma_f32_16x16x32_bf16 v[6:9], a[24:27], a[168:171], v[6:9]\n\t"
"ds_write_b128 v242, v[228:231] offset:27648\n\t"
"v_mfma_f32_16x16x32_bf16 v[38:41], a[40:43], a[192:195], 0\n\t"
"s_waitcnt lgkmcnt(3)\n\t"
"v_mfma_f32_16x16x32_bf16 v[38:41], a[48:51], a[168:171], v[38:41]\n\t"
"s_waitcnt lgkmcnt(1)\n\t"
"ds_read_b64_tr_b16 v[224:225], v240 offset:512\n\t"
"v_mfma_f32_16x16x32_bf16 v[38:41], a[32:35], a[196:199], v[38:41]\n\t"
"ds_read_b64_tr_b16 v[226:227], v240 offset:2048\n\t"
"v_mfma_f32_16x16x32_bf16 v[236:239], v[236:239], v[196:199], 0\n\t"
"ds_read_b64_tr_b16 v[228:229], v241 offset:512\n\t"
"v_mfma_f32_16x16x32_bf16 v[232:235], v[232:235], v[196:199], 0\n\t"
"ds_read_b64_tr_b16 v[230:231], v241 offset:2048\n\t"
"v_mfma_f32_16x16x32_bf16 v[70:73], a[56:59], a[196:199], 0\n\t"
"v_mfma_f32_16x16x32_bf16 v[70:73], a[64:67], a[192:195], v[70:73]\n\t"
"v_mfma_f32_16x16x32_bf16 v[70:73], a[72:75], a[168:171], v[70:73]\n\t"
"v_mfma_f32_16x16x32_bf16 v[104:107], a[80:83], a[172:175], 0\n\t"
"v_cvt_pk_bf16_f32 v236, v236, v237\n\t"
"v_cvt_pk_bf16_f32 v237, v238, v239\n\t"
"v_mfma_f32_16x16x32_bf16 v[104:107], a[88:91], a[176:179], v[104:107]\n\t"
"v_cvt_pk_bf16_f32 v238, v232, v233\n\t"
"v_mfma_f32_16x16x32_bf16 v[104:107], a[96:99], a[180:183], v[104:107]\n\t"
"s_waitcnt lgkmcnt(4)\n\t"
"s_barrier\n\t"
"v_mfma_f32_16x16x32_bf16 v[136:139], a[112:115], a[176:179], 0\n\t"
"ds_read_b128 a[168:171], v244 offset:24576\n\t"
"v_mfma_f32_16x16x32_bf16 v[136:139], a[120:123], a[180:183], v[136:139]\n\t"
"ds_read_b128 a[184:187], v244 offset:33792\n\t"
"v_cvt_pk_bf16_f32 v239, v234, v235\n\t"
"v_mfma_f32_16x16x32_bf16 v[136:139], a[104:107], a[172:175], v[136:139]\n\t"
"ds_write_b128 v242, v[236:239] offset:3072\n\t"
"v_mfma_f32_16x16x32_bf16 v[168:171], a[128:131], a[172:175], 0\n\t"
"s_waitcnt lgkmcnt(5)\n\t"
"v_mfma_f32_16x16x32_bf16 v[168:171], a[144:147], a[180:183], v[168:171]\n\t"
"s_waitcnt lgkmcnt(3)\n\t"
"ds_read_b128 a[180:183], v244 offset:26624\n\t"
"v_mfma_f32_16x16x32_bf16 v[168:171], a[136:139], a[176:179], v[168:171]\n\t"
"s_waitcnt lgkmcnt(3)\n\t"
"ds_read_b128 a[188:191], v244 offset:27136\n\t"
"v_mfma_f32_16x16x32_bf16 v[224:227], v[224:227], v[196:199], 0\n\t"
"ds_read_b128 a[200:203], v244 offset:27648\n\t"
"v_mfma_f32_16x16x32_bf16 v[228:231], v[228:231], v[196:199], 0\n\t"
"v_mfma_f32_16x16x32_bf16 v[2:5], a[8:11], a[168:171], 0\n\t"
"v_mfma_f32_16x16x32_bf16 v[2:5], a[16:19], a[196:199], v[2:5]\n\t"
"v_mfma_f32_16x16x32_bf16 v[2:5], a[24:27], a[192:195], v[2:5]\n\t"
"s_waitcnt vmcnt(0)\n\t"
"s_barrier\n\t"
"v_mfma_f32_16x16x32_bf16 v[34:37], a[40:43], a[196:199], 0\n\t"
"ds_read_b64_tr_b16 v[232:233], v240 offset:39936\n\t"
"v_mfma_f32_16x16x32_bf16 v[34:37], a[48:51], a[192:195], v[34:37]\n\t"
"ds_read_b64_tr_b16 v[234:235], v240 offset:41472\n\t"
"v_cvt_pk_bf16_f32 v224, v224, v225\n\t"
"v_mfma_f32_16x16x32_bf16 v[34:37], a[32:35], a[168:171], v[34:37]\n\t"
"ds_read_b64_tr_b16 v[236:237], v241 offset:39936\n\t"
"v_cvt_pk_bf16_f32 v225, v226, v227\n\t"
"v_mfma_f32_16x16x32_bf16 v[66:69], a[56:59], a[168:171], 0\n\t"
"s_waitcnt lgkmcnt(7)\n\t"
"ds_read_b64_tr_b16 v[238:239], v241 offset:41472\n\t"
"v_cvt_pk_bf16_f32 v226, v228, v229\n\t"
"v_mfma_f32_16x16x32_bf16 v[66:69], a[72:75], a[192:195], v[66:69]\n\t"
"ds_read_b128 a[168:171], v244 offset:35840\n\t"
"v_cvt_pk_bf16_f32 v227, v230, v231\n\t"
"v_mfma_f32_16x16x32_bf16 v[66:69], a[64:67], a[196:199], v[66:69]\n\t"
"s_cmp_lg_u32 s73, 0\n\t"
"s_cbranch_scc1 Lnp_%=\n\t"
"buffer_load_dwordx4 v[248:251], v252, s[8:11], s72 offen sc1\n\t"
"Lnp_%=:\n\t"
"ds_write_b128 v242, v[224:227] offset:4608\n\t"
"v_mfma_f32_16x16x32_bf16 v[100:103], a[80:83], a[184:187], 0\n\t"
"s_waitcnt lgkmcnt(4)\n\t"
"v_mfma_f32_16x16x32_bf16 v[100:103], a[88:91], a[172:175], v[100:103]\n\t"
"ds_read_b128 a[192:195], v244 offset:36352\n\t"
"v_mfma_f32_16x16x32_bf16 v[100:103], a[96:99], a[176:179], v[100:103]\n\t"
"s_waitcnt lgkmcnt(3)\n\t"
"ds_read_b128 a[204:207], v244 offset:36864\n\t"
"v_mfma_f32_16x16x32_bf16 v[232:235], v[232:235], v[196:199], 0\n\t"
"ds_read_b64_tr_b16 v[228:229], v240 offset:20480\n\t"
"v_mfma_f32_16x16x32_bf16 v[236:239], v[236:239], v[196:199], 0\n\t"
"ds_read_b64_tr_b16 v[230:231], v240 offset:22016\n\t"
"v_mfma_f32_16x16x32_bf16 v[132:135], a[120:123], a[176:179], 0\n\t"
"ds_read_b64_tr_b16 v[224:225], v241 offset:20480\n\t"
"v_mfma_f32_16x16x32_bf16 v[132:135], a[104:107], a[184:187], v[132:135]\n\t"
"ds_read_b64_tr_b16 v[226:227], v241 offset:22016\n\t"
"v_mfma_f32_16x16x32_bf16 v[132:135], a[112:115], a[172:175], v[132:135]\n\t"
"v_pk_mul_f32 v[232:233], v[232:233], v[242:243] op_sel:[0,1] op_sel_hi:[1,1]\n\t"
"v_pk_mul_f32 v[234:235], v[234:235], v[242:243] op_sel:[0,1] op_sel_hi:[1,1]\n\t"
"v_mfma_f32_16x16x32_bf16 v[164:167], a[128:131], a[184:187], 0\n\t"
"v_cvt_pk_bf16_f32 v232, v232, v233\n\t"
"v_cvt_pk_bf16_f32 v233, v234, v235\n\t"
"v_mfma_f32_16x16x32_bf16 v[164:167], a[136:139], a[172:175], v[164:167]\n\t"
"v_mfma_f32_16x16x32_bf16 v[164:167], a[144:147], a[176:179], v[164:167]\n\t"
"v_pk_mul_f32 v[236:237], v[236:237], v[242:243] op_sel:[0,1] op_sel_hi:[1,1]\n\t"
"v_pk_mul_f32 v[238:239], v[238:239], v[242:243] op_sel:[0,1] op_sel_hi:[1,1]\n\t"
"v_mfma_f32_16x16x32_bf16 v[18:21], a[8:11], a[180:183], 0\n\t"
"v_cvt_pk_bf16_f32 v234, v236, v237\n\t"
"v_cvt_pk_bf16_f32 v235, v238, v239\n\t"
"v_mfma_f32_16x16x32_bf16 v[18:21], a[16:19], a[188:191], v[18:21]\n\t"
"ds_read_b64_tr_b16 v[236:237], v240 offset:40448\n\t"
"v_mfma_f32_16x16x32_bf16 v[18:21], a[24:27], a[200:203], v[18:21]\n\t"
"ds_write_b128 v242, v[232:235] offset:32256\n\t"
"v_mfma_f32_16x16x32_bf16 v[50:53], a[40:43], a[188:191], 0\n\t"
"s_waitcnt lgkmcnt(2)\n\t"
"v_mfma_f32_16x16x32_bf16 v[50:53], a[48:51], a[200:203], v[50:53]\n\t"
"ds_read_b64_tr_b16 v[238:239], v240 offset:41984\n\t"
"v_mfma_f32_16x16x32_bf16 v[50:53], a[32:35], a[180:183], v[50:53]\n\t"
"ds_read_b64_tr_b16 v[232:233], v241 offset:40448\n\t"
"v_mfma_f32_16x16x32_bf16 v[228:231], v[228:231], v[196:199], 0\n\t"
"Lpg1_%=:\n\t"
"s_cbranch_vccnz Lpg5_%=\n\t"
"ds_read_b64_tr_b16 v[234:235], v241 offset:41984\n\t"
"v_mfma_f32_16x16x32_bf16 v[224:227], v[224:227], v[196:199], 0\n\t"
"v_mfma_f32_16x16x32_bf16 v[82:85], a[56:59], a[180:183], 0\n\t"
"v_mfma_f32_16x16x32_bf16 v[82:85], a[64:67], a[188:191], v[82:85]\n\t"
"v_mfma_f32_16x16x32_bf16 v[82:85], a[72:75], a[200:203], v[82:85]\n\t"
"v_mfma_f32_16x16x32_bf16 v[116:119], a[80:83], a[168:171], 0\n\t"
"v_cvt_pk_bf16_f32 v228, v228, v229\n\t"
"v_cvt_pk_bf16_f32 v229, v230, v231\n\t"
"v_mfma_f32_16x16x32_bf16 v[116:119], a[88:91], a[192:195], v[116:119]\n\t"
"v_cvt_pk_bf16_f32 v230, v224, v225\n\t"
"v_mfma_f32_16x16x32_bf16 v[116:119], a[96:99], a[204:207], v[116:119]\n\t"
"s_waitcnt lgkmcnt(3)\n\t"
"s_barrier\n\t"
"v_mfma_f32_16x16x32_bf16 v[148:151], a[112:115], a[192:195], 0\n\t"
"s_waitcnt lgkmcnt(2)\n\t"
"ds_read_b128 a[196:199], v244 offset:28160\n\t"
"v_mfma_f32_16x16x32_bf16 v[148:151], a[120:123], a[204:207], v[148:151]\n\t"
"s_waitcnt lgkmcnt(1)\n\t"
"ds_read_b128 v[204:207], v244 offset:37376\n\t"
"v_cvt_pk_bf16_f32 v231, v226, v227\n\t"
"v_mfma_f32_16x16x32_bf16 v[148:151], a[104:107], a[168:171], v[148:151]\n\t"
"ds_write_b128 v242, v[228:231] offset:6144\n\t"
"v_mfma_f32_16x16x32_bf16 v[236:239], v[236:239], v[196:199], 0\n\t"
"v_mfma_f32_16x16x32_bf16 v[232:235], v[232:235], v[196:199], 0\n\t"
"ds_read_b128 v[212:215], v244 offset:28672\n\t"
"v_mfma_f32_16x16x32_bf16 v[180:183], a[128:131], a[168:171], 0\n\t"
"s_waitcnt lgkmcnt(3)\n\t"
"v_mfma_f32_16x16x32_bf16 v[180:183], a[136:139], a[192:195], v[180:183]\n\t"
"v_mfma_f32_16x16x32_bf16 v[180:183], a[144:147], a[204:207], v[180:183]\n\t"
"v_mfma_f32_16x16x32_bf16 v[22:25], a[8:11], a[188:191], 0\n\t"
"v_cvt_pk_bf16_f32 v236, v236, v237\n\t"
"v_cvt_pk_bf16_f32 v237, v238, v239\n\t"
"v_mfma_f32_16x16x32_bf16 v[22:25], a[16:19], a[200:203], v[22:25]\n\t"
"v_cvt_pk_bf16_f32 v238, v232, v233\n\t"
"v_cvt_pk_bf16_f32 v239, v234, v235\n\t"
"v_mfma_f32_16x16x32_bf16 v[22:25], a[24:27], a[196:199], v[22:25]\n\t"
"ds_write_b128 v242, v[236:239] offset:7680\n\t"
"v_mfma_f32_16x16x32_bf16 v[54:57], a[40:43], a[200:203], 0\n\t"
"v_mfma_f32_16x16x32_bf16 v[54:57], a[48:51], a[196:199], v[54:57]\n\t"
"ds_read_b128 v[196:199], v244 offset:37888\n\t"
"v_mfma_f32_16x16x32_bf16 v[54:57], a[32:35], a[188:191], v[54:57]\n\t"
"v_mfma_f32_16x16x32_bf16 v[86:89], a[56:59], a[188:191], 0\n\t"
"s_waitcnt lgkmcnt(4)\n\t"
"v_mfma_f32_16x16x32_bf16 v[86:89], a[72:75], a[196:199], v[86:89]\n\t"
"v_mfma_f32_16x16x32_bf16 v[86:89], a[64:67], a[200:203], v[86:89]\n\t"
"v_mfma_f32_16x16x32_bf16 v[120:123], a[80:83], a[192:195], 0\n\t"
"s_mov_b32 s20, 294912\n\t"
"v_mfma_f32_16x16x32_bf16 v[120:123], a[88:91], a[204:207], v[120:123]\n\t"
"v_mfma_f32_16x16x32_bf16 v[120:123], a[96:99], v[204:207], v[120:123]\n\t"
"v_mfma_f32_16x16x32_bf16 v[152:155], a[112:115], a[204:207], 0\n\t"
"ds_read_b128 a[188:191], v244 offset:29184\n\t"
"v_mfma_f32_16x16x32_bf16 v[152:155], a[120:123], v[204:207], v[152:155]\n\t"
"v_mfma_f32_16x16x32_bf16 v[152:155], a[104:107], a[192:195], v[152:155]\n\t"
"s_waitcnt lgkmcnt(2)\n\t"
"s_cmp_lg_u32 s73, 0\n\t"
"s_cbranch_scc1 Lgo_%=\n\t"
"Lchk_%=:\n\t"
"s_waitcnt vmcnt(0)\n\t"
"v_cmpx_ne_u32_e64 s[60:61], v220, v248\n\t"
"v_cmpx_ne_u32_e64 s[60:61], v221, v249\n\t"
"v_cmpx_ne_u32_e64 s[60:61], v222, v250\n\t"
"v_cmpx_ne_u32_e64 s[60:61], v223, v251\n\t"
"s_not_b64 s[60:61], s[60:61]\n\t"
"s_mov_b64 exec, -1\n\t"
"s_lshl_b64 s[60:61], s[60:61], 10\n\t"
"s_cbranch_scc0 Lgo_%=\n\t"
"s_sub_u32 s68, s68, 1\n\t"
"s_cbranch_scc1 Lfb_%=\n\t"
"buffer_load_dwordx4 v[248:251], v252, s[8:11], s72 offen sc1\n\t"
"s_branch Lchk_%=\n\t"
"Lgo_%=:\n\t"
"s_barrier\n\t"
"v_mfma_f32_16x16x32_bf16 v[184:187], a[128:131], a[192:195], 0\n\t"
"s_mov_b32 s52, 0x3fd80000\n\t"
"s_mov_b32 s53, s52\n\t"
"v_mfma_f32_16x16x32_bf16 v[184:187], a[144:147], v[204:207], v[184:187]\n\t"
"buffer_load_dwordx4 v[236:239], off, s[80:83], s20 offset:0 sc1\n\t"
"v_mfma_f32_16x16x32_bf16 v[184:187], a[136:139], a[204:207], v[184:187]\n\t"
"s_mov_b32 s54, 0x3e97b426\n\t"
"s_mov_b32 s55, s54\n\t"
"s_mov_b32 s56, 0x3f400000\n\t"
"v_mfma_f32_16x16x32_bf16 v[26:29], a[8:11], a[200:203], 0\n\t"
"buffer_load_dwordx4 a[152:155], off, s[80:83], s20 offset:1024 sc1\n\t"
"v_mfma_f32_16x16x32_bf16 v[26:29], a[16:19], a[196:199], v[26:29]\n\t"
"s_mov_b32 s57, s56\n\t"
"s_mov_b32 s58, 0x3f2aaaab\n\t"
"s_mov_b32 s59, s58\n\t"
"v_mfma_f32_16x16x32_bf16 v[26:29], a[24:27], v[212:215], v[26:29]\n\t"
"s_mov_b32 s60, 0x3ee38e39\n\t"
"s_mov_b32 s61, s60\n\t"
"s_mov_b32 s62, 0x40100000\n\t"
"v_mfma_f32_16x16x32_bf16 v[58:61], a[40:43], a[196:199], 0\n\t"
"buffer_load_dwordx4 a[160:163], off, s[80:83], s20 offset:2048 sc1\n\t"
"v_mfma_f32_16x16x32_bf16 v[58:61], a[48:51], v[212:215], v[58:61]\n\t"
"s_mov_b32 s20, 307200\n\t"
"ds_read_b128 a[0:3], v244 offset:38400\n\t"
"s_mov_b32 s63, s62\n\t"
"v_mfma_f32_16x16x32_bf16 v[58:61], a[32:35], a[200:203], v[58:61]\n\t"
"s_mov_b32 s64, 0x3faaaaab\n\t"
"s_mov_b32 s65, s64\n\t"
"s_mov_b32 s66, 0x3f17b426\n\t"
"v_mfma_f32_16x16x32_bf16 v[90:93], a[56:59], a[200:203], 0\n\t"
"buffer_load_dwordx4 v[200:203], off, s[80:83], s20 offset:0 sc1\n\t"
"v_mfma_f32_16x16x32_bf16 v[90:93], a[72:75], v[212:215], v[90:93]\n\t"
"s_waitcnt lgkmcnt(2)\n\t"
"s_mov_b32 s67, s66\n\t"
"v_mfma_f32_16x16x32_bf16 v[90:93], a[64:67], a[196:199], v[90:93]\n\t"
"v_mfma_f32_16x16x32_bf16 v[124:127], a[80:83], a[204:207], 0\n\t"
"buffer_load_dwordx4 v[208:211], off, s[80:83], s20 offset:1024 sc1\n\t"
"v_mfma_f32_16x16x32_bf16 v[124:127], a[88:91], v[204:207], v[124:127]\n\t"
"v_mfma_f32_16x16x32_bf16 v[124:127], a[96:99], v[196:199], v[124:127]\n\t"
"v_mfma_f32_16x16x32_bf16 v[156:159], a[112:115], v[204:207], 0\n\t"
"buffer_load_dwordx4 v[216:219], off, s[80:83], s20 offset:2048 sc1\n\t"
"v_mfma_f32_16x16x32_bf16 v[156:159], a[120:123], v[196:199], v[156:159]\n\t"
"s_mov_b32 s20, 319488\n\t"
"v_mfma_f32_16x16x32_bf16 v[156:159], a[104:107], a[204:207], v[156:159]\n\t"
"v_mfma_f32_16x16x32_bf16 v[188:191], a[128:131], a[204:207], 0\n\t"
"buffer_load_dwordx4 a[4:7], off, s[80:83], s20 offset:0 sc1\n\t"
"v_mfma_f32_16x16x32_bf16 v[188:191], a[144:147], v[196:199], v[188:191]\n\t"
"s_waitcnt lgkmcnt(1)\n\t"
"ds_read_b128 a[168:171], v244 offset:0\n\t"
"v_mfma_f32_16x16x32_bf16 v[188:191], a[136:139], v[204:207], v[188:191]\n\t"
"ds_read_b128 a[172:175], v244 offset:512\n\t"
"v_mfma_f32_16x16x32_bf16 v[30:33], a[8:11], a[196:199], 0\n\t"
"buffer_load_dwordx4 a[8:11], off, s[80:83], s20 offset:1024 sc1\n\t"
"v_mfma_f32_16x16x32_bf16 v[30:33], a[16:19], v[212:215], v[30:33]\n\t"
"ds_read_b128 a[176:179], v244 offset:1024\n\t"
"v_mfma_f32_16x16x32_bf16 v[30:33], a[24:27], a[188:191], v[30:33]\n\t"
"ds_read_b128 a[180:183], v244 offset:1536\n\t"
"v_mfma_f32_16x16x32_bf16 v[62:65], a[40:43], v[212:215], 0\n\t"
"buffer_load_dwordx4 a[16:19], off, s[80:83], s20 offset:2048 sc1\n\t"
"v_mfma_f32_16x16x32_bf16 v[62:65], a[48:51], a[188:191], v[62:65]\n\t"
"s_mov_b32 s20, 479232\n\t"
"ds_read_b128 a[184:187], v244 offset:2048\n\t"
"v_mfma_f32_16x16x32_bf16 v[62:65], a[32:35], a[196:199], v[62:65]\n\t"
"v_mfma_f32_16x16x32_bf16 v[94:97], a[56:59], a[196:199], 0\n\t"
"buffer_load_dwordx4 a[24:27], off, s[80:83], s20 offset:0 sc1\n\t"
"v_mfma_f32_16x16x32_bf16 v[94:97], a[72:75], a[188:191], v[94:97]\n\t"
"s_waitcnt lgkmcnt(5)\n\t"
"ds_read_b128 a[188:191], v244 offset:2560\n\t"
"v_mfma_f32_16x16x32_bf16 v[94:97], a[64:67], v[212:215], v[94:97]\n\t"
"global_load_dwordx4 v[220:223], v252, s[48:49] offset:1024\n\t"
"v_mfma_f32_16x16x32_bf16 v[128:131], a[80:83], v[204:207], 0\n\t"
"buffer_load_dwordx4 v[212:215], off, s[80:83], s20 offset:1024 sc1\n\t"
"v_mfma_f32_16x16x32_bf16 v[128:131], a[88:91], v[196:199], v[128:131]\n\t"
"ds_read_b128 a[192:195], v244 offset:3072\n\t"
"v_mfma_f32_16x16x32_bf16 v[128:131], a[96:99], a[0:3], v[128:131]\n\t"
"v_mfma_f32_16x16x32_bf16 v[160:163], a[112:115], v[196:199], 0\n\t"
"buffer_load_dwordx4 a[32:35], off, s[80:83], s20 offset:2048 sc1\n\t"
"v_mfma_f32_16x16x32_bf16 v[160:163], a[120:123], a[0:3], v[160:163]\n\t"
"s_mov_b32 s20, 491520\n\t"
"ds_read_b128 a[196:199], v244 offset:3584\n\t"
"v_mfma_f32_16x16x32_bf16 v[160:163], a[104:107], v[204:207], v[160:163]\n\t"
"v_mfma_f32_16x16x32_bf16 v[192:195], a[128:131], v[204:207], 0\n\t"
"buffer_load_dwordx4 a[40:43], off, s[80:83], s20 offset:0 sc1\n\t"
"v_mfma_f32_16x16x32_bf16 v[192:195], a[144:147], a[0:3], v[192:195]\n\t"
"s_waitcnt vmcnt(11) lgkmcnt(5)\n\t"
"ds_read_b128 a[200:203], v244 offset:4096\n\t"
"v_mfma_f32_16x16x32_bf16 v[192:195], a[136:139], v[196:199], v[192:195]\n\t"
"Lst1_%=:\n\t"
"v_mfma_f32_16x16x32_bf16 v[2:5], v[236:239], a[168:171], v[2:5]\n\t"
"buffer_load_dwordx4 a[48:51], off, s[80:83], s20 offset:1024 sc1\n\t"
"v_mfma_f32_16x16x32_bf16 v[2:5], a[160:163], a[176:179], v[2:5]\n\t"
"s_waitcnt lgkmcnt(5)\n\t"
"ds_read_b128 a[204:207], v244 offset:4608\n\t"
"v_mfma_f32_16x16x32_bf16 v[2:5], a[152:155], a[172:175], v[2:5]\n\t"
"v_mfma_f32_16x16x32_bf16 v[6:9], v[236:239], a[172:175], v[6:9]\n\t"
"buffer_load_dwordx4 a[56:59], off, s[80:83], s20 offset:2048 sc1\n\t"
"v_mfma_f32_16x16x32_bf16 v[6:9], a[152:155], a[176:179], v[6:9]\n\t"
"s_waitcnt lgkmcnt(5)\n\t"
"s_mov_b32 s20, 503808\n\t"
"v_mfma_f32_16x16x32_bf16 v[6:9], a[160:163], a[180:183], v[6:9]\n\t"
"v_mfma_f32_16x16x32_bf16 v[10:13], a[160:163], a[184:187], v[10:13]\n\t"
"buffer_load_dwordx4 a[80:83], off, s[80:83], s20 offset:0 sc1\n\t"
"v_mfma_f32_16x16x32_bf16 v[10:13], a[152:155], a[180:183], v[10:13]\n\t"
"s_waitcnt lgkmcnt(4)\n\t"
"v_mfma_f32_16x16x32_bf16 v[10:13], v[236:239], a[176:179], v[10:13]\n\t"
"v_mfma_f32_16x16x32_bf16 v[14:17], v[236:239], a[180:183], v[14:17]\n\t"
"buffer_load_dwordx4 a[88:91], off, s[80:83], s20 offset:1024 sc1\n\t"
"v_mfma_f32_16x16x32_bf16 v[14:17], a[152:155], a[184:187], v[14:17]\n\t"
"s_waitcnt lgkmcnt(3)\n\t"
"v_mfma_f32_16x16x32_bf16 v[14:17], a[160:163], a[188:191], v[14:17]\n\t"
"v_mfma_f32_16x16x32_bf16 v[18:21], a[160:163], a[192:195], v[18:21]\n\t"
"buffer_load_dwordx4 a[96:99], off, s[80:83], s20 offset:2048 sc1\n\t"
"v_mfma_f32_16x16x32_bf16 v[18:21], a[152:155], a[188:191], v[18:21]\n\t"
"s_waitcnt lgkmcnt(2)\n\t"
"s_mov_b32 s20, 110592\n\t"
"v_mfma_f32_16x16x32_bf16 v[18:21], v[236:239], a[184:187], v[18:21]\n\t"
"v_mfma_f32_16x16x32_bf16 v[22:25], v[236:239], a[188:191], v[22:25]\n\t"
"buffer_load_dwordx4 a[136:139], off, s[80:83], s20 offset:0 sc1\n\t"
"v_mfma_f32_16x16x32_bf16 v[22:25], a[152:155], a[192:195], v[22:25]\n\t"
"s_waitcnt lgkmcnt(1)\n\t"
"v_mfma_f32_16x16x32_bf16 v[22:25], a[160:163], a[196:199], v[22:25]\n\t"
"v_mfma_f32_16x16x32_bf16 v[26:29], a[160:163], a[200:203], v[26:29]\n\t"
"buffer_load_dwordx4 a[144:147], off, s[80:83], s20 offset:1024 sc1\n\t"
"v_mfma_f32_16x16x32_bf16 v[26:29], a[152:155], a[196:199], v[26:29]\n\t"
"s_waitcnt lgkmcnt(0)\n\t"
"v_xor_b32 v246, 0xc0, v246\n\t"
"v_mfma_f32_16x16x32_bf16 v[26:29], v[236:239], a[192:195], v[26:29]\n\t"
"v_mfma_f32_16x16x32_bf16 v[30:33], v[236:239], a[196:199], v[30:33]\n\t"
"buffer_load_dwordx4 a[12:15], off, s[80:83], s20 offset:2048 sc1\n\t"
"v_mfma_f32_16x16x32_bf16 v[30:33], a[152:155], a[200:203], v[30:33]\n\t"
"s_waitcnt vmcnt(16)\n\t"
"s_mov_b32 s20, 122880\n\t"
"v_mfma_f32_16x16x32_bf16 v[30:33], a[160:163], a[204:207], v[30:33]\n\t"
"v_mfma_f32_16x16x32_bf16 v[34:37], v[200:203], a[168:171], v[34:37]\n\t"
"buffer_load_dwordx4 a[20:23], off, s[80:83], s20 offset:0 sc1\n\t"
"v_mfma_f32_16x16x32_bf16 v[34:37], v[216:219], a[176:179], v[34:37]\n\t"
"v_mfma_f32_16x16x32_bf16 v[34:37], v[208:211], a[172:175], v[34:37]\n\t"
"v_mfma_f32_16x16x32_bf16 v[38:41], v[200:203], a[172:175], v[38:41]\n\t"
"buffer_load_dwordx4 a[28:31], off, s[80:83], s20 offset:1024 sc1\n\t"
"v_mfma_f32_16x16x32_bf16 v[38:41], v[208:211], a[176:179], v[38:41]\n\t"
"v_mfma_f32_16x16x32_bf16 v[38:41], v[216:219], a[180:183], v[38:41]\n\t"
"v_mfma_f32_16x16x32_bf16 v[42:45], v[216:219], a[184:187], v[42:45]\n\t"
"buffer_load_dwordx4 a[36:39], off, s[80:83], s20 offset:2048 sc1\n\t"
"v_mfma_f32_16x16x32_bf16 v[42:45], v[208:211], a[180:183], v[42:45]\n\t"
"s_mov_b32 s20, 135168\n\t"
"v_mfma_f32_16x16x32_bf16 v[42:45], v[200:203], a[176:179], v[42:45]\n\t"
"ds_read_b64_tr_b16 v[224:225], v246 offset:0\n\t"
"v_mfma_f32_16x16x32_bf16 v[46:49], v[200:203], a[180:183], v[46:49]\n\t"
"buffer_load_dwordx4 a[76:79], off, s[80:83], s20 offset:0 sc1\n\t"
"v_mfma_f32_16x16x32_bf16 v[46:49], v[208:211], a[184:187], v[46:49]\n\t"
"ds_read_b64_tr_b16 v[226:227], v246 offset:3072\n\t"
"v_mfma_f32_16x16x32_bf16 v[46:49], v[216:219], a[188:191], v[46:49]\n\t"
"ds_read_b64_tr_b16 v[228:229], v246 offset:6144\n\t"
"v_mfma_f32_16x16x32_bf16 v[50:53], v[216:219], a[192:195], v[50:53]\n\t"
"buffer_load_dwordx4 a[84:87], off, s[80:83], s20 offset:1024 sc1\n\t"
"v_mfma_f32_16x16x32_bf16 v[50:53], v[208:211], a[188:191], v[50:53]\n\t"
"ds_read_b64_tr_b16 v[230:231], v246 offset:9216\n\t"
"v_mfma_f32_16x16x32_bf16 v[50:53], v[200:203], a[184:187], v[50:53]\n\t"
"ds_read_b64_tr_b16 v[232:233], v246 offset:12288\n\t"
"v_mfma_f32_16x16x32_bf16 v[54:57], v[200:203], a[188:191], v[54:57]\n\t"
"ds_read_b64_tr_b16 v[234:235], v246 offset:15360\n\t"
"v_mfma_f32_16x16x32_bf16 v[54:57], v[208:211], a[192:195], v[54:57]\n\t"
"buffer_load_dwordx4 a[92:95], off, s[80:83], s20 offset:2048 sc1\n\t"
"v_mfma_f32_16x16x32_bf16 v[54:57], v[216:219], a[196:199], v[54:57]\n\t"
"s_mov_b32 s20, 147456\n\t"
"ds_read_b64_tr_b16 v[236:237], v246 offset:19968\n\t"
"v_mfma_f32_16x16x32_bf16 v[58:61], v[216:219], a[200:203], v[58:61]\n\t"
"ds_read_b64_tr_b16 v[238:239], v246 offset:23040\n\t"
"v_mfma_f32_16x16x32_bf16 v[58:61], v[208:211], a[196:199], v[58:61]\n\t"
"buffer_load_dwordx4 a[148:151], off, s[80:83], s20 offset:0 sc1\n\t"
"v_mfma_f32_16x16x32_bf16 v[58:61], v[200:203], a[192:195], v[58:61]\n\t"
"v_mfma_f32_16x16x32_bf16 v[62:65], v[200:203], a[196:199], v[62:65]\n\t"
"s_waitcnt vmcnt(18) lgkmcnt(6)\n\t"
"v_mfma_f32_16x16x32_bf16 v[62:65], v[208:211], a[200:203], v[62:65]\n\t"
"buffer_load_dwordx4 a[156:159], off, s[80:83], s20 offset:1024 sc1\n\t"
"v_mfma_f32_16x16x32_bf16 v[62:65], v[216:219], a[204:207], v[62:65]\n\t"
"s_waitcnt lgkmcnt(2)\n\t"
"v_mfma_f32_16x16x32_bf16 v[224:227], v[224:227], v[220:223], 0\n\t"
"s_waitcnt lgkmcnt(0)\n\t"
"v_mfma_f32_16x16x32_bf16 v[228:231], v[228:231], v[220:223], 0\n\t"
"v_mfma_f32_16x16x32_bf16 v[232:235], v[232:235], v[220:223], 0\n\t"
"buffer_load_dwordx4 a[164:167], off, s[80:83], s20 offset:2048 sc1\n\t"
"v_mfma_f32_16x16x32_bf16 v[236:239], v[236:239], v[220:223], 0\n\t"
"s_mov_b32 s20, 159744\n\t"
"v_mfma_f32_16x16x32_bf16 v[66:69], a[4:7], a[168:171], v[66:69]\n\t"
"ds_read_b128 a[168:171], v244 offset:9216\n\t"
"v_mfma_f32_16x16x32_bf16 v[66:69], a[16:19], a[176:179], v[66:69]\n\t"
"v_cvt_pk_bf16_f32 v224, v224, v225\n\t"
"v_cvt_pk_bf16_f32 v225, v226, v227\n\t"
"v_mfma_f32_16x16x32_bf16 v[66:69], a[8:11], a[172:175], v[66:69]\n\t"
"v_cvt_pk_bf16_f32 v228, v228, v229\n\t"
"v_cvt_pk_bf16_f32 v229, v230, v231\n\t"
"v_mfma_f32_16x16x32_bf16 v[70:73], a[4:7], a[172:175], v[70:73]\n\t"
"ds_read_b128 a[172:175], v244 offset:9728\n\t"
"v_cvt_pk_bf16_f32 v232, v232, v233\n\t"
"v_mfma_f32_16x16x32_bf16 v[70:73], a[8:11], a[176:179], v[70:73]\n\t"
"v_cvt_pk_bf16_f32 v233, v234, v235\n\t"
"v_cvt_pk_bf16_f32 v236, v236, v237\n\t"
"v_mfma_f32_16x16x32_bf16 v[70:73], a[16:19], a[180:183], v[70:73]\n\t"
"buffer_load_dwordx4 v[240:243], off, s[80:83], s20 offset:0 sc1\n\t"
"v_mfma_f32_16x16x32_bf16 v[74:77], a[16:19], a[184:187], v[74:77]\n\t"
"buffer_load_dwordx4 a[208:211], off, s[80:83], s20 offset:1024 sc1\n\t"
"v_mfma_f32_16x16x32_bf16 v[74:77], a[8:11], a[180:183], v[74:77]\n\t"
"v_cvt_pk_bf16_f32 v237, v238, v239\n\t"
"v_mfma_f32_16x16x32_bf16 v[74:77], a[4:7], a[176:179], v[74:77]\n\t"
"ds_read_b128 a[176:179], v244 offset:10240\n\t"
"v_mfma_f32_16x16x32_bf16 v[78:81], a[4:7], a[180:183], v[78:81]\n\t"
"ds_read_b128 a[180:183], v244 offset:10752\n\t"
"v_mfma_f32_16x16x32_bf16 v[78:81], a[8:11], a[184:187], v[78:81]\n\t"
"buffer_load_dwordx4 v[248:251], off, s[80:83], s20 offset:2048 sc1\n\t"
"v_mfma_f32_16x16x32_bf16 v[78:81], a[16:19], a[188:191], v[78:81]\n\t"
"s_mov_b32 s20, 172032\n\t"
"v_mfma_f32_16x16x32_bf16 v[82:85], a[16:19], a[192:195], v[82:85]\n\t"
"v_mfma_f32_16x16x32_bf16 v[82:85], a[8:11], a[188:191], v[82:85]\n\t"
"v_mfma_f32_16x16x32_bf16 v[82:85], a[4:7], a[184:187], v[82:85]\n\t"
"ds_read_b128 a[184:187], v244 offset:11264\n\t"
"v_mfma_f32_16x16x32_bf16 v[86:89], a[4:7], a[188:191], v[86:89]\n\t"
"buffer_load_dwordx4 a[212:215], off, s[80:83], s20 offset:0 sc1\n\t"
"v_mfma_f32_16x16x32_bf16 v[86:89], a[8:11], a[192:195], v[86:89]\n\t"
"ds_read_b128 a[188:191], v244 offset:11776\n\t"
"v_mfma_f32_16x16x32_bf16 v[86:89], a[16:19], a[196:199], v[86:89]\n\t"
"v_mfma_f32_16x16x32_bf16 v[90:93], a[16:19], a[200:203], v[90:93]\n\t"
"buffer_load_dwordx4 a[216:219], off, s[80:83], s20 offset:1024 sc1\n\t"
"v_mfma_f32_16x16x32_bf16 v[90:93], a[8:11], a[196:199], v[90:93]\n\t"
"v_mfma_f32_16x16x32_bf16 v[90:93], a[4:7], a[192:195], v[90:93]\n\t"
"ds_read_b128 a[192:195], v244 offset:12288\n\t"
"v_mfma_f32_16x16x32_bf16 v[94:97], a[4:7], a[196:199], v[94:97]\n\t"
"s_waitcnt vmcnt(23) lgkmcnt(4)\n\t"
"ds_read_b128 a[196:199], v244 offset:12800\n\t"
"v_mfma_f32_16x16x32_bf16 v[94:97], a[8:11], a[200:203], v[94:97]\n\t"
"ds_read_b128 a[200:203], v244 offset:13312\n\t"
"v_mfma_f32_16x16x32_bf16 v[94:97], a[16:19], a[204:207], v[94:97]\n\t"
"ds_read_b128 a[204:207], v244 offset:13824\n\t"
"v_mfma_f32_16x16x32_bf16 v[100:103], a[24:27], a[168:171], v[100:103]\n\t"
"buffer_load_dwordx4 a[220:223], off, s[80:83], s20 offset:2048 sc1\n\t"
"v_mfma_f32_16x16x32_bf16 v[100:103], v[212:215], a[172:175], v[100:103]\n\t"
"s_waitcnt lgkmcnt(6)\n\t"
"s_mov_b32 s20, 36864\n\t"
"v_mfma_f32_16x16x32_bf16 v[100:103], a[32:35], a[176:179], v[100:103]\n\t"
"buffer_load_dwordx4 a[224:227], off, s[80:83], s20 offset:0 sc1\n\t"
"v_mfma_f32_16x16x32_bf16 v[104:107], a[32:35], a[180:183], v[104:107]\n\t"
"s_waitcnt lgkmcnt(5)\n\t"
"v_mfma_f32_16x16x32_bf16 v[104:107], v[212:215], a[176:179], v[104:107]\n\t"
"v_mfma_f32_16x16x32_bf16 v[104:107], a[24:27], a[172:175], v[104:107]\n\t"
"s_waitcnt lgkmcnt(0)\n\t"
"s_barrier\n\t"
"v_mfma_f32_16x16x32_bf16 v[108:111], a[24:27], a[176:179], v[108:111]\n\t"
"v_fma_f32 v196, -v2, s56, -v100\n\t"
"v_mfma_f32_16x16x32_bf16 v[108:111], v[212:215], a[180:183], v[108:111]\n\t"
"buffer_load_dwordx4 a[228:231], off, s[80:83], s20 offset:1024 sc1\n\t"
"v_mfma_f32_16x16x32_bf16 v[108:111], a[32:35], a[184:187], v[108:111]\n\t"
"ds_write_b64 v245, v[224:225] offset:0\n\t"
"v_mfma_f32_16x16x32_bf16 v[112:115], a[32:35], a[188:191], v[112:115]\n\t"
"ds_read_b64_tr_b16 v[224:225], v246 offset:26112\n\t"
"v_fma_f32 v197, -v3, s56, -v101\n\t"
"v_mfma_f32_16x16x32_bf16 v[112:115], v[212:215], a[184:187], v[112:115]\n\t"
"ds_read_b64_tr_b16 v[226:227], v246 offset:29184\n\t"
"v_fma_f32 v198, -v4, s56, -v102\n\t"
"v_mfma_f32_16x16x32_bf16 v[112:115], a[24:27], a[180:183], v[112:115]\n\t"
"ds_write_b64 v245, v[228:229] offset:512\n\t"
"v_mfma_f32_16x16x32_bf16 v[116:119], a[24:27], a[184:187], v[116:119]\n\t"
"buffer_load_dwordx4 a[232:235], off, s[80:83], s20 offset:2048 sc1\n\t"
"v_mfma_f32_16x16x32_bf16 v[116:119], v[212:215], a[188:191], v[116:119]\n\t"
"s_mov_b32 s20, 49152\n\t"
"ds_read_b64_tr_b16 v[228:229], v246 offset:32256\n\t"
"v_fma_f32 v199, -v5, s56, -v103\n\t"
"v_mfma_f32_16x16x32_bf16 v[116:119], a[32:35], a[192:195], v[116:119]\n\t"
"ds_write_b64 v245, v[232:233] offset:1024\n\t"
"v_mfma_f32_16x16x32_bf16 v[120:123], v[212:215], a[192:195], v[120:123]\n\t"
"buffer_load_dwordx4 a[236:239], off, s[80:83], s20 offset:0 sc1\n\t"
"v_mfma_f32_16x16x32_bf16 v[120:123], a[32:35], a[196:199], v[120:123]\n\t"
"buffer_load_dwordx4 a[240:243], off, s[80:83], s20 offset:1024 sc1\n\t"
"v_mfma_f32_16x16x32_bf16 v[120:123], a[24:27], a[188:191], v[120:123]\n\t"
"s_waitcnt lgkmcnt(3)\n\t"
"ds_write_b64 v245, v[236:237] offset:1536\n\t"
"v_mfma_f32_16x16x32_bf16 v[224:227], v[224:227], v[220:223], 0\n\t"
"ds_read_b64_tr_b16 v[230:231], v246 offset:35328\n\t"
"v_fma_f32 v204, -v6, s56, -v104\n\t"
"v_mfma_f32_16x16x32_bf16 v[124:127], a[24:27], a[192:195], v[124:127]\n\t"
"ds_read_b64_tr_b16 v[232:233], v246 offset:39936\n\t"
"v_fma_f32 v205, -v7, s56, -v105\n\t"
"v_mfma_f32_16x16x32_bf16 v[124:127], a[32:35], a[200:203], v[124:127]\n\t"
"ds_read_b64_tr_b16 v[234:235], v246 offset:43008\n\t"
"v_fma_f32 v206, -v8, s56, -v106\n\t"
"v_mfma_f32_16x16x32_bf16 v[124:127], v[212:215], a[196:199], v[124:127]\n\t"
"ds_read_b64_tr_b16 v[236:237], v246 offset:46080\n\t"
"v_fma_f32 v207, -v9, s56, -v107\n\t"
"v_mfma_f32_16x16x32_bf16 v[128:131], a[24:27], a[196:199], v[128:131]\n\t"
"ds_read_b64_tr_b16 v[238:239], v246 offset:49152\n\t"
"v_fma_f32 v98, -v14, s56, -v112\n\t"
"v_mfma_f32_16x16x32_bf16 v[128:131], v[212:215], a[200:203], v[128:131]\n\t"
"s_waitcnt lgkmcnt(4)\n\t"
"v_cvt_pk_bf16_f32 v224, v224, v225\n\t"
"v_cvt_pk_bf16_f32 v225, v226, v227\n\t"
"v_mfma_f32_16x16x32_bf16 v[128:131], a[32:35], a[204:207], v[128:131]\n\t"
"buffer_load_dwordx4 a[244:247], off, s[80:83], s20 offset:2048 sc1\n\t"
"s_mov_b32 s20, 61440\n\t"
"v_mfma_f32_16x16x32_bf16 v[228:231], v[228:231], v[220:223], 0\n\t"
"s_waitcnt vmcnt(27) lgkmcnt(2)\n\t"
"buffer_load_dwordx4 a[248:251], off, s[80:83], s20 offset:0 sc1\n\t"
"v_mfma_f32_16x16x32_bf16 v[232:235], v[232:235], v[220:223], 0\n\t"
"s_waitcnt lgkmcnt(0)\n\t"
"buffer_load_dwordx4 a[252:255], off, s[80:83], s20 offset:1024 sc1\n\t"
"v_mfma_f32_16x16x32_bf16 v[236:239], v[236:239], v[220:223], 0\n\t"
"ds_write_b64 v245, v[224:225] offset:2048\n\t"
"v_mfma_f32_16x16x32_bf16 v[132:135], a[40:43], a[168:171], v[132:135]\n\t"
"ds_read_b64_tr_b16 v[224:225], v246 offset:52224\n\t"
"v_fma_f32 v212, -v10, s56, -v108\n\t"
"v_mfma_f32_16x16x32_bf16 v[132:135], a[48:51], a[172:175], v[132:135]\n\t"
"ds_read_b64_tr_b16 v[226:227], v246 offset:55296\n\t"
"v_fma_f32 v213, -v11, s56, -v109\n\t"
"v_mfma_f32_16x16x32_bf16 v[132:135], a[56:59], a[176:179], v[132:135]\n\t"
"v_cvt_pk_bf16_f32 v228, v228, v229\n\t"
"v_cvt_pk_bf16_f32 v229, v230, v231\n\t"
"v_mfma_f32_16x16x32_bf16 v[136:139], a[56:59], a[180:183], v[136:139]\n\t"
"v_cvt_pk_bf16_f32 v232, v232, v233\n\t"
"v_cvt_pk_bf16_f32 v233, v234, v235\n\t"
"v_mfma_f32_16x16x32_bf16 v[136:139], a[48:51], a[176:179], v[136:139]\n\t"
"v_cvt_pk_bf16_f32 v236, v236, v237\n\t"
"v_cvt_pk_bf16_f32 v237, v238, v239\n\t"
"v_mfma_f32_16x16x32_bf16 v[136:139], a[40:43], a[172:175], v[136:139]\n\t"
"ds_write_b64 v245, v[228:229] offset:2560\n\t"
"v_mfma_f32_16x16x32_bf16 v[140:143], a[40:43], a[176:179], v[140:143]\n\t"
"v_fma_f32 v214, -v12, s56, -v110\n\t"
"v_fma_f32 v215, -v13, s56, -v111\n\t"
"v_mfma_f32_16x16x32_bf16 v[140:143], a[48:51], a[180:183], v[140:143]\n\t"
"v_fma_f32 v99, -v15, s56, -v113\n\t"
"v_accvgpr_write_b32 a0, v98\n\t"
"v_mfma_f32_16x16x32_bf16 v[140:143], a[56:59], a[184:187], v[140:143]\n\t"
"ds_write_b64 v245, v[232:233] offset:3072\n\t"
"v_mfma_f32_16x16x32_bf16 v[144:147], a[48:51], a[184:187], v[144:147]\n\t"
"s_waitcnt lgkmcnt(2)\n\t"
"v_fma_f32 v98, -v16, s56, -v114\n\t"
"v_accvgpr_write_b32 a1, v99\n\t"
"v_mfma_f32_16x16x32_bf16 v[144:147], a[56:59], a[188:191], v[144:147]\n\t"
"v_fma_f32 v99, -v17, s56, -v115\n\t"
"v_accvgpr_write_b32 a2, v98\n\t"
"v_mfma_f32_16x16x32_bf16 v[144:147], a[40:43], a[180:183], v[144:147]\n\t"
"ds_write_b64 v245, v[236:237] offset:3584\n\t"
"v_mfma_f32_16x16x32_bf16 v[224:227], v[224:227], v[220:223], 0\n\t"
"buffer_load_dwordx4 v[236:239], off, s[80:83], s20 offset:2048 sc1\n\t"
"v_mfma_f32_16x16x32_bf16 v[148:151], a[40:43], a[184:187], v[148:151]\n\t"
"v_accvgpr_write_b32 a3, v99\n\t"
"v_fma_f32 v98, -v18, s56, -v116\n\t"
"v_mfma_f32_16x16x32_bf16 v[148:151], a[56:59], a[192:195], v[148:151]\n\t"
"v_fma_f32 v99, -v19, s56, -v117\n\t"
"v_accvgpr_write_b32 a8, v98\n\t"
"v_mfma_f32_16x16x32_bf16 v[148:151], a[48:51], a[188:191], v[148:151]\n\t"
"v_fma_f32 v98, -v20, s56, -v118\n\t"
"v_accvgpr_write_b32 a9, v99\n\t"
"v_mfma_f32_16x16x32_bf16 v[152:155], a[40:43], a[188:191], v[152:155]\n\t"
"v_fma_f32 v99, -v21, s56, -v119\n\t"
"v_accvgpr_write_b32 a10, v98\n\t"
"v_mfma_f32_16x16x32_bf16 v[152:155], a[48:51], a[192:195], v[152:155]\n\t"
"v_cvt_pk_bf16_f32 v224, v224, v225\n\t"
"v_cvt_pk_bf16_f32 v225, v226, v227\n\t"
"v_mfma_f32_16x16x32_bf16 v[152:155], a[56:59], a[196:199], v[152:155]\n\t"
"ds_write_b64 v245, v[224:225] offset:4096\n\t"
"v_mfma_f32_16x16x32_bf16 v[156:159], a[56:59], a[200:203], v[156:159]\n\t"
"v_accvgpr_write_b32 a11, v99\n\t"
"v_fma_f32 v98, -v22, s56, -v120\n\t"
"v_mfma_f32_16x16x32_bf16 v[156:159], a[48:51], a[196:199], v[156:159]\n\t"
"v_fma_f32 v99, -v23, s56, -v121\n\t"
"v_accvgpr_write_b32 a16, v98\n\t"
"v_mfma_f32_16x16x32_bf16 v[156:159], a[40:43], a[192:195], v[156:159]\n\t"
"v_fma_f32 v98, -v24, s56, -v122\n\t"
"v_accvgpr_write_b32 a17, v99\n\t"
"v_mfma_f32_16x16x32_bf16 v[160:163], a[40:43], a[196:199], v[160:163]\n\t"
"s_waitcnt vmcnt(27)\n\t"
"v_fma_f32 v99, -v25, s56, -v123\n\t"
"v_accvgpr_write_b32 a18, v98\n\t"
"v_mfma_f32_16x16x32_bf16 v[160:163], a[48:51], a[200:203], v[160:163]\n\t"
"v_accvgpr_write_b32 a19, v99\n\t"
"v_fma_f32 v98, -v26, s56, -v124\n\t"
"v_mfma_f32_16x16x32_bf16 v[160:163], a[56:59], a[204:207], v[160:163]\n\t"
"v_fma_f32 v99, -v27, s56, -v125\n\t"
"v_accvgpr_write_b32 a24, v98\n\t"
"v_mfma_f32_16x16x32_bf16 v[164:167], a[80:83], a[168:171], v[164:167]\n\t"
"v_fma_f32 v98, -v28, s56, -v126\n\t"
"v_accvgpr_write_b32 a25, v99\n\t"
"v_mfma_f32_16x16x32_bf16 v[164:167], a[96:99], a[176:179], v[164:167]\n\t"
"v_fma_f32 v99, -v29, s56, -v127\n\t"
"v_mfma_f32_16x16x32_bf16 v[164:167], a[88:91], a[172:175], v[164:167]\n\t"
"s_waitcnt lgkmcnt(0)\n\t"
"s_barrier\n\t"
"v_mfma_f32_16x16x32_bf16 v[168:171], a[80:83], a[172:175], v[168:171]\n\t"
"ds_read_b128 a[168:171], v244 offset:0\n\t"
"v_mfma_f32_16x16x32_bf16 v[168:171], a[88:91], a[176:179], v[168:171]\n\t"
"ds_read_b128 a[172:175], v244 offset:512\n\t"
"v_accvgpr_write_b32 a26, v98\n\t"
"v_mfma_f32_16x16x32_bf16 v[168:171], a[96:99], a[180:183], v[168:171]\n\t"
"v_accvgpr_write_b32 a27, v99\n\t"
"v_fma_f32 v98, -v30, s56, -v128\n\t"
"v_mfma_f32_16x16x32_bf16 v[172:175], a[96:99], a[184:187], v[172:175]\n\t"
"v_fma_f32 v99, -v31, s56, -v129\n\t"
"v_accvgpr_write_b32 a32, v98\n\t"
"v_mfma_f32_16x16x32_bf16 v[172:175], a[88:91], a[180:183], v[172:175]\n\t"
"v_fma_f32 v98, -v32, s56, -v130\n\t"
"v_accvgpr_write_b32 a33, v99\n\t"
"v_mfma_f32_16x16x32_bf16 v[172:175], a[80:83], a[176:179], v[172:175]\n\t"
"ds_read_b128 a[176:179], v244 offset:1024\n\t"
"v_fma_f32 v99, -v33, s56, -v131\n\t"
"v_mfma_f32_16x16x32_bf16 v[176:179], a[80:83], a[180:183], v[176:179]\n\t"
"ds_read_b128 a[180:183], v244 offset:1536\n\t"
"v_accvgpr_write_b32 a34, v98\n\t"
"v_mfma_f32_16x16x32_bf16 v[176:179], a[88:91], a[184:187], v[176:179]\n\t"
"v_accvgpr_write_b32 a35, v99\n\t"
"v_fma_f32 v98, -v34, s56, -v132\n\t"
"v_mfma_f32_16x16x32_bf16 v[176:179], a[96:99], a[188:191], v[176:179]\n\t"
"v_fma_f32 v99, -v35, s56, -v133\n\t"
"v_accvgpr_write_b32 a40, v98\n\t"
"v_mfma_f32_16x16x32_bf16 v[180:183], a[96:99], a[192:195], v[180:183]\n\t"
"v_fma_f32 v98, -v36, s56, -v134\n\t"
"v_accvgpr_write_b32 a41, v99\n\t"
"v_mfma_f32_16x16x32_bf16 v[180:183], a[88:91], a[188:191], v[180:183]\n\t"
"v_fma_f32 v99, -v37, s56, -v135\n\t"
"v_accvgpr_write_b32 a42, v98\n\t"
"v_mfma_f32_16x16x32_bf16 v[180:183], a[80:83], a[184:187], v[180:183]\n\t"
"ds_read_b128 a[184:187], v244 offset:2048\n\t"
"v_accvgpr_write_b32 a43, v99\n\t"
"v_mfma_f32_16x16x32_bf16 v[184:187], a[80:83], a[188:191], v[184:187]\n\t"
"ds_read_b128 a[188:191], v244 offset:2560\n\t"
"v_fma_f32 v98, -v38, s56, -v136\n\t"
"v_mfma_f32_16x16x32_bf16 v[184:187], a[88:91], a[192:195], v[184:187]\n\t"
"v_fma_f32 v99, -v39, s56, -v137\n\t"
"v_accvgpr_write_b32 a48, v98\n\t"
"v_mfma_f32_16x16x32_bf16 v[184:187], a[96:99], a[196:199], v[184:187]\n\t"
"v_fma_f32 v98, -v40, s56, -v138\n\t"
"v_accvgpr_write_b32 a49, v99\n\t"
"v_mfma_f32_16x16x32_bf16 v[188:191], a[96:99], a[200:203], v[188:191]\n\t"
"v_xor_b32 v246, 0xc0, v246\n\t"
"v_fma_f32 v99, -v41, s56, -v139\n\t"
"v_mfma_f32_16x16x32_bf16 v[188:191], a[88:91], a[196:199], v[188:191]\n\t"
"v_accvgpr_write_b32 a50, v98\n\t"
"v_accvgpr_write_b32 a51, v99\n\t"
"v_mfma_f32_16x16x32_bf16 v[188:191], a[80:83], a[192:195], v[188:191]\n\t"
"ds_read_b128 a[192:195], v244 offset:3072\n\t"
"v_fma_f32 v98, -v42, s56, -v140\n\t"
"v_mfma_f32_16x16x32_bf16 v[192:195], a[80:83], a[196:199], v[192:195]\n\t"
"s_waitcnt vmcnt(24) lgkmcnt(4)\n\t"
"ds_read_b128 a[196:199], v244 offset:3584\n\t"
"v_fma_f32 v99, -v43, s56, -v141\n\t"
"v_mfma_f32_16x16x32_bf16 v[192:195], a[88:91], a[200:203], v[192:195]\n\t"
"ds_read_b128 a[200:203], v244 offset:4096\n\t"
"v_accvgpr_write_b32 a56, v98\n\t"
"v_mfma_f32_16x16x32_bf16 v[192:195], a[96:99], a[204:207], v[192:195]\n\t"
"ds_read_b128 a[204:207], v244 offset:4608\n\t"
"v_fma_f32 v98, -v44, s56, -v142\n\t"
"Lst2_%=:\n\t"
"v_mfma_f32_16x16x32_bf16 v[196:199], a[136:139], a[168:171], v[196:199]\n\t"
"s_waitcnt lgkmcnt(6)\n\t"
"v_accvgpr_write_b32 a57, v99\n\t"
"v_fma_f32 v99, -v45, s56, -v143\n\t"
"v_mfma_f32_16x16x32_bf16 v[196:199], a[12:15], a[176:179], v[196:199]\n\t"
"v_accvgpr_write_b32 a58, v98\n\t"
"v_accvgpr_write_b32 a59, v99\n\t"
"v_mfma_f32_16x16x32_bf16 v[196:199], a[144:147], a[172:175], v[196:199]\n\t"
"v_fma_f32 v98, -v46, s56, -v144\n\t"
"v_fma_f32 v99, -v47, s56, -v145\n\t"
"v_mfma_f32_16x16x32_bf16 v[204:207], a[136:139], a[172:175], v[204:207]\n\t"
"s_waitcnt lgkmcnt(5)\n\t"
"v_accvgpr_write_b32 a64, v98\n\t"
"v_fma_f32 v98, -v48, s56, -v146\n\t"
"v_mfma_f32_16x16x32_bf16 v[204:207], a[144:147], a[176:179], v[204:207]\n\t"
"v_accvgpr_write_b32 a65, v99\n\t"
"v_fma_f32 v99, -v49, s56, -v147\n\t"
"v_mfma_f32_16x16x32_bf16 v[204:207], a[12:15], a[180:183], v[204:207]\n\t"
"s_waitcnt lgkmcnt(2)\n\t"
"v_accvgpr_write_b32 a66, v98\n\t"
"v_accvgpr_write_b32 a67, v99\n\t"
"v_mfma_f32_16x16x32_bf16 v[212:215], a[12:15], a[184:187], v[212:215]\n\t"
"v_fma_f32 v98, -v50, s56, -v148\n\t"
"v_fma_f32 v99, -v51, s56, -v149\n\t"
"v_mfma_f32_16x16x32_bf16 v[212:215], a[144:147], a[180:183], v[212:215]\n\t"
"Lpg2_%=:\n\t"
"s_cbranch_vccnz Lpg6_%=\n\t"
"v_accvgpr_write_b32 a72, v98\n\t"
"v_fma_f32 v98, -v52, s56, -v150\n\t"
"v_mfma_f32_16x16x32_bf16 v[212:215], a[136:139], a[176:179], v[212:215]\n\t"
"ds_read_b64_tr_b16 v[228:229], v246 offset:0\n\t"
"v_accvgpr_write_b32 a73, v99\n\t"
"v_mfma_f32_16x16x32_bf16 a[0:3], a[136:139], a[180:183], a[0:3]\n\t"
"ds_read_b64_tr_b16 v[230:231], v246 offset:3072\n\t"
"v_fma_f32 v99, -v53, s56, -v151\n\t"
"v_mfma_f32_16x16x32_bf16 a[0:3], a[144:147], a[184:187], a[0:3]\n\t"
"ds_read_b64_tr_b16 v[232:233], v246 offset:6144\n\t"
"v_accvgpr_write_b32 a74, v98\n\t"
"v_mfma_f32_16x16x32_bf16 a[0:3], a[12:15], a[188:191], a[0:3]\n\t"
"ds_read_b64_tr_b16 v[234:235], v246 offset:9216\n\t"
"v_accvgpr_write_b32 a75, v99\n\t"
"v_mfma_f32_16x16x32_bf16 a[8:11], a[12:15], a[192:195], a[8:11]\n\t"
"v_fma_f32 v98, -v54, s56, -v152\n\t"
"v_fma_f32 v99, -v55, s56, -v153\n\t"
"v_mfma_f32_16x16x32_bf16 a[8:11], a[144:147], a[188:191], a[8:11]\n\t"
"v_accvgpr_write_b32 a80, v98\n\t"
"v_fma_f32 v98, -v56, s56, -v154\n\t"
"v_mfma_f32_16x16x32_bf16 a[8:11], a[136:139], a[184:187], a[8:11]\n\t"
"v_accvgpr_write_b32 a81, v99\n\t"
"v_fma_f32 v99, -v57, s56, -v155\n\t"
"v_mfma_f32_16x16x32_bf16 a[16:19], a[136:139], a[188:191], a[16:19]\n\t"
"s_waitcnt lgkmcnt(5)\n\t"
"v_accvgpr_write_b32 a82, v98\n\t"
"v_accvgpr_write_b32 a83, v99\n\t"
"v_mfma_f32_16x16x32_bf16 a[16:19], a[144:147], a[192:195], a[16:19]\n\t"
"v_fma_f32 v98, -v58, s56, -v156\n\t"
"v_fma_f32 v99, -v59, s56, -v157\n\t"
"v_mfma_f32_16x16x32_bf16 a[16:19], a[12:15], a[196:199], a[16:19]\n\t"
"v_accvgpr_write_b32 a88, v98\n\t"
"v_fma_f32 v98, -v60, s56, -v158\n\t"
"v_mfma_f32_16x16x32_bf16 a[24:27], a[12:15], a[200:203], a[24:27]\n\t"
"s_waitcnt lgkmcnt(4)\n\t"
"v_accvgpr_write_b32 a89, v99\n\t"
"v_fma_f32 v99, -v61, s56, -v159\n\t"
"v_mfma_f32_16x16x32_bf16 a[24:27], a[144:147], a[196:199], a[24:27]\n\t"
"v_accvgpr_write_b32 a90, v98\n\t"
"v_accvgpr_write_b32 a91, v99\n\t"
"v_mfma_f32_16x16x32_bf16 a[24:27], a[136:139], a[192:195], a[24:27]\n\t"
"v_fma_f32 v98, -v62, s56, -v160\n\t"
"v_fma_f32 v99, -v63, s56, -v161\n\t"
"v_mfma_f32_16x16x32_bf16 a[32:35], a[136:139], a[196:199], a[32:35]\n\t"
"s_waitcnt lgkmcnt(2)\n\t"
"v_accvgpr_write_b32 a96, v98\n\t"
"v_fma_f32 v98, -v64, s56, -v162\n\t"
"v_mfma_f32_16x16x32_bf16 a[32:35], a[144:147], a[200:203], a[32:35]\n\t"
"s_waitcnt lgkmcnt(0)\n\t"
"v_accvgpr_write_b32 a97, v99\n\t"
"v_fma_f32 v99, -v65, s56, -v163\n\t"
"v_mfma_f32_16x16x32_bf16 a[32:35], a[12:15], a[204:207], a[32:35]\n\t"
"s_waitcnt vmcnt(21)\n\t"
"v_accvgpr_write_b32 a98, v98\n\t"
"v_accvgpr_write_b32 a99, v99\n\t"
"v_mfma_f32_16x16x32_bf16 v[228:231], v[228:231], v[220:223], 0\n\t"
"v_fma_f32 v98, -v66, s56, -v164\n\t"
"v_fma_f32 v99, -v67, s56, -v165\n\t"
"v_mfma_f32_16x16x32_bf16 v[232:235], v[232:235], v[220:223], 0\n\t"
"v_accvgpr_write_b32 a104, v98\n\t"
"v_fma_f32 v98, -v68, s56, -v166\n\t"
"v_mfma_f32_16x16x32_bf16 a[40:43], a[20:23], a[168:171], a[40:43]\n\t"
"v_accvgpr_write_b32 a105, v99\n\t"
"v_fma_f32 v99, -v69, s56, -v167\n\t"
"v_mfma_f32_16x16x32_bf16 a[40:43], a[36:39], a[176:179], a[40:43]\n\t"
"v_accvgpr_write_b32 a106, v98\n\t"
"v_accvgpr_write_b32 a107, v99\n\t"
"v_mfma_f32_16x16x32_bf16 a[40:43], a[28:31], a[172:175], a[40:43]\n\t"
"v_fma_f32 v98, -v70, s56, -v168\n\t"
"v_fma_f32 v99, -v71, s56, -v169\n\t"
"v_mfma_f32_16x16x32_bf16 a[48:51], a[20:23], a[172:175], a[48:51]\n\t"
"v_cvt_pk_bf16_f32 v228, v228, v229\n\t"
"v_cvt_pk_bf16_f32 v229, v230, v231\n\t"
"v_mfma_f32_16x16x32_bf16 a[48:51], a[28:31], a[176:179], a[48:51]\n\t"
"v_cvt_pk_bf16_f32 v230, v232, v233\n\t"
"v_cvt_pk_bf16_f32 v231, v234, v235\n\t"
"v_mfma_f32_16x16x32_bf16 a[48:51], a[36:39], a[180:183], a[48:51]\n\t"
"v_accvgpr_write_b32 a112, v98\n\t"
"v_fma_f32 v98, -v72, s56, -v170\n\t"
"v_mfma_f32_16x16x32_bf16 a[56:59], a[36:39], a[184:187], a[56:59]\n\t"
"v_accvgpr_write_b32 a113, v99\n\t"
"v_fma_f32 v99, -v73, s56, -v171\n\t"
"v_mfma_f32_16x16x32_bf16 a[56:59], a[28:31], a[180:183], a[56:59]\n\t"
"v_accvgpr_write_b32 a114, v98\n\t"
"v_accvgpr_write_b32 a115, v99\n\t"
"v_mfma_f32_16x16x32_bf16 a[56:59], a[20:23], a[176:179], a[56:59]\n\t"
"ds_read_b64_tr_b16 v[224:225], v246 offset:12288\n\t"
"v_fma_f32 v98, -v74, s56, -v172\n\t"
"v_mfma_f32_16x16x32_bf16 a[64:67], a[20:23], a[180:183], a[64:67]\n\t"
"ds_read_b64_tr_b16 v[226:227], v246 offset:15360\n\t"
"v_fma_f32 v99, -v75, s56, -v173\n\t"
"v_mfma_f32_16x16x32_bf16 a[64:67], a[28:31], a[184:187], a[64:67]\n\t"
"ds_read_b64_tr_b16 v[232:233], v246 offset:19968\n\t"
"v_accvgpr_write_b32 a120, v98\n\t"
"v_mfma_f32_16x16x32_bf16 a[64:67], a[36:39], a[188:191], a[64:67]\n\t"
"ds_read_b64_tr_b16 v[234:235], v246 offset:23040\n\t"
"v_fma_f32 v98, -v76, s56, -v174\n\t"
"v_mfma_f32_16x16x32_bf16 a[72:75], a[36:39], a[192:195], a[72:75]\n\t"
"v_accvgpr_write_b32 a121, v99\n\t"
"v_fma_f32 v99, -v77, s56, -v175\n\t"
"v_mfma_f32_16x16x32_bf16 a[72:75], a[28:31], a[188:191], a[72:75]\n\t"
"v_accvgpr_write_b32 a122, v98\n\t"
"v_accvgpr_write_b32 a123, v99\n\t"
"v_mfma_f32_16x16x32_bf16 a[72:75], a[20:23], a[184:187], a[72:75]\n\t"
"v_fma_f32 v98, -v78, s56, -v176\n\t"
"v_fma_f32 v99, -v79, s56, -v177\n\t"
"v_mfma_f32_16x16x32_bf16 a[80:83], a[20:23], a[188:191], a[80:83]\n\t"
"v_accvgpr_write_b32 a128, v98\n\t"
"v_fma_f32 v98, -v80, s56, -v178\n\t"
"v_mfma_f32_16x16x32_bf16 a[80:83], a[28:31], a[192:195], a[80:83]\n\t"
"v_accvgpr_write_b32 a129, v99\n\t"
"v_fma_f32 v99, -v81, s56, -v179\n\t"
"v_mfma_f32_16x16x32_bf16 a[80:83], a[36:39], a[196:199], a[80:83]\n\t"
"v_accvgpr_write_b32 a130, v98\n\t"
"v_accvgpr_write_b32 a131, v99\n\t"
"v_mfma_f32_16x16x32_bf16 a[88:91], a[36:39], a[200:203], a[88:91]\n\t"
"v_fma_f32 v98, -v82, s56, -v180\n\t"
"v_fma_f32 v99, -v83, s56, -v181\n\t"
"v_mfma_f32_16x16x32_bf16 a[88:91], a[28:31], a[196:199], a[88:91]\n\t"
"v_accvgpr_write_b32 a136, v98\n\t"
"v_fma_f32 v98, -v84, s56, -v182\n\t"
"v_mfma_f32_16x16x32_bf16 a[88:91], a[20:23], a[192:195], a[88:91]\n\t"
"v_accvgpr_write_b32 a137, v99\n\t"
"v_fma_f32 v99, -v85, s56, -v183\n\t"
"v_mfma_f32_16x16x32_bf16 a[96:99], a[20:23], a[196:199], a[96:99]\n\t"
"s_waitcnt lgkmcnt(2)\n\t"
"v_accvgpr_write_b32 a138, v98\n\t"
"v_accvgpr_write_b32 a139, v99\n\t"
"v_mfma_f32_16x16x32_bf16 a[96:99], a[28:31], a[200:203], a[96:99]\n\t"
"s_waitcnt lgkmcnt(0)\n\t"
"v_fma_f32 v98, -v86, s56, -v184\n\t"
"v_fma_f32 v99, -v87, s56, -v185\n\t"
"v_mfma_f32_16x16x32_bf16 a[96:99], a[36:39], a[204:207], a[96:99]\n\t"
"s_waitcnt vmcnt(18)\n\t"
"v_accvgpr_write_b32 a144, v98\n\t"
"v_fma_f32 v98, -v88, s56, -v186\n\t"
"v_mfma_f32_16x16x32_bf16 v[224:227], v[224:227], v[220:223], 0\n\t"
"v_accvgpr_write_b32 a145, v99\n\t"
"v_fma_f32 v99, -v89, s56, -v187\n\t"
"v_mfma_f32_16x16x32_bf16 v[232:235], v[232:235], v[220:223], 0\n\t"
"v_accvgpr_write_b32 a146, v98\n\t"
"v_accvgpr_write_b32 a147, v99\n\t"
"v_mfma_f32_16x16x32_bf16 a[104:107], a[76:79], a[168:171], a[104:107]\n\t"
"ds_read_b128 a[168:171], v244 offset:9216\n\t"
"v_fma_f32 v98, -v90, s56, -v188\n\t"
"v_mfma_f32_16x16x32_bf16 a[104:107], a[92:95], a[176:179], a[104:107]\n\t"
"v_fma_f32 v99, -v91, s56, -v189\n\t"
"v_accvgpr_write_b32 a152, v98\n\t"
"v_mfma_f32_16x16x32_bf16 a[104:107], a[84:87], a[172:175], a[104:107]\n\t"
"v_fma_f32 v98, -v92, s56, -v190\n\t"
"v_accvgpr_write_b32 a153, v99\n\t"
"v_mfma_f32_16x16x32_bf16 a[112:115], a[76:79], a[172:175], a[112:115]\n\t"
"ds_read_b128 a[172:175], v244 offset:9728\n\t"
"v_cvt_pk_bf16_f32 v224, v224, v225\n\t"
"v_mfma_f32_16x16x32_bf16 a[112:115], a[84:87], a[176:179], a[112:115]\n\t"
"v_cvt_pk_bf16_f32 v225, v226, v227\n\t"
"v_cvt_pk_bf16_f32 v226, v232, v233\n\t"
"v_mfma_f32_16x16x32_bf16 a[112:115], a[92:95], a[180:183], a[112:115]\n\t"
"v_cvt_pk_bf16_f32 v227, v234, v235\n\t"
"v_fma_f32 v99, -v93, s56, -v191\n\t"
"v_mfma_f32_16x16x32_bf16 a[120:123], a[92:95], a[184:187], a[120:123]\n\t"
"v_accvgpr_write_b32 a154, v98\n\t"
"v_accvgpr_write_b32 a155, v99\n\t"
"v_mfma_f32_16x16x32_bf16 a[120:123], a[84:87], a[180:183], a[120:123]\n\t"
"v_fma_f32 v98, -v94, s56, -v192\n\t"
"v_fma_f32 v99, -v95, s56, -v193\n\t"
"v_mfma_f32_16x16x32_bf16 a[120:123], a[76:79], a[176:179], a[120:123]\n\t"
"ds_read_b128 a[176:179], v244 offset:10240\n\t"
"v_accvgpr_write_b32 a160, v98\n\t"
"v_mfma_f32_16x16x32_bf16 a[128:131], a[76:79], a[180:183], a[128:131]\n\t"
"ds_read_b128 a[180:183], v244 offset:10752\n\t"
"v_fma_f32 v98, -v96, s56, -v194\n\t"
"v_mfma_f32_16x16x32_bf16 a[128:131], a[84:87], a[184:187], a[128:131]\n\t"
"ds_read_b64_tr_b16 v[232:233], v246 offset:26112\n\t"
"v_accvgpr_write_b32 a161, v99\n\t"
"v_mfma_f32_16x16x32_bf16 a[128:131], a[92:95], a[188:191], a[128:131]\n\t"
"ds_read_b64_tr_b16 v[234:235], v246 offset:29184\n\t"
"v_fma_f32 v99, -v97, s56, -v195\n\t"
"v_mfma_f32_16x16x32_bf16 a[136:139], a[92:95], a[192:195], a[136:139]\n\t"
"v_accvgpr_write_b32 a162, v98\n\t"
"v_accvgpr_write_b32 a163, v99\n\t"
"v_mfma_f32_16x16x32_bf16 a[136:139], a[84:87], a[188:191], a[136:139]\n\t"
"v_fma_f32 v200, v100, s64, -v2\n\t"
"v_fma_f32 v201, v101, s64, -v3\n\t"
"v_mfma_f32_16x16x32_bf16 a[136:139], a[76:79], a[184:187], a[136:139]\n\t"
"ds_read_b128 a[184:187], v244 offset:11264\n\t"
"v_fma_f32 v202, v102, s64, -v4\n\t"
"v_mfma_f32_16x16x32_bf16 a[144:147], a[76:79], a[188:191], a[144:147]\n\t"
"ds_read_b128 a[188:191], v244 offset:11776\n\t"
"v_fma_f32 v203, v103, s64, -v5\n\t"
"v_mfma_f32_16x16x32_bf16 a[144:147], a[84:87], a[192:195], a[144:147]\n\t"
"v_fma_f32 v208, v104, s64, -v6\n\t"
"v_fma_f32 v209, v105, s64, -v7\n\t"
"v_mfma_f32_16x16x32_bf16 a[144:147], a[92:95], a[196:199], a[144:147]\n\t"
"v_fma_f32 v210, v106, s64, -v8\n\t"
"v_fma_f32 v211, v107, s64, -v9\n\t"
"v_mfma_f32_16x16x32_bf16 a[152:155], a[92:95], a[200:203], a[152:155]\n\t"
"v_fma_f32 v216, v108, s64, -v10\n\t"
"v_fma_f32 v217, v109, s64, -v11\n\t"
"v_mfma_f32_16x16x32_bf16 a[152:155], a[84:87], a[196:199], a[152:155]\n\t"
"v_fma_f32 v218, v110, s64, -v12\n\t"
"v_fma_f32 v219, v111, s64, -v13\n\t"
"v_mfma_f32_16x16x32_bf16 a[152:155], a[76:79], a[192:195], a[152:155]\n\t"
"ds_read_b128 a[192:195], v244 offset:12288\n\t"
"v_fma_f32 v98, v112, s64, -v14\n\t"
"v_mfma_f32_16x16x32_bf16 a[160:163], a[76:79], a[196:199], a[160:163]\n\t"
"s_waitcnt lgkmcnt(3)\n\t"
"ds_read_b128 a[196:199], v244 offset:12800\n\t"
"v_fma_f32 v99, v113, s64, -v15\n\t"
"v_mfma_f32_16x16x32_bf16 a[160:163], a[84:87], a[200:203], a[160:163]\n\t"
"s_waitcnt vmcnt(15)\n\t"
"ds_read_b128 a[200:203], v244 offset:13312\n\t"
"v_accvgpr_write_b32 a4, v98\n\t"
"v_mfma_f32_16x16x32_bf16 a[160:163], a[92:95], a[204:207], a[160:163]\n\t"
"ds_read_b128 a[204:207], v244 offset:13824\n\t"
"v_fma_f32 v98, v114, s64, -v16\n\t"
"v_mfma_f32_16x16x32_bf16 v[232:235], v[232:235], v[220:223], 0\n\t"
"v_accvgpr_write_b32 a5, v99\n\t"
"v_fma_f32 v99, v115, s64, -v17\n\t"
"v_mfma_f32_16x16x32_bf16 v[200:203], a[148:151], a[168:171], v[200:203]\n\t"
"v_accvgpr_write_b32 a6, v98\n\t"
"v_accvgpr_write_b32 a7, v99\n\t"
"v_mfma_f32_16x16x32_bf16 v[200:203], a[164:167], a[176:179], v[200:203]\n\t"
"v_fma_f32 v98, v116, s64, -v18\n\t"
"v_fma_f32 v99, v117, s64, -v19\n\t"
"v_mfma_f32_16x16x32_bf16 v[200:203], a[156:159], a[172:175], v[200:203]\n\t"
"s_add_u32 m0, s24, 896\n\t"
"s_add_u32 s28, s21, 0\n\t"
"v_accvgpr_write_b32 a12, v98\n\t"
"v_mfma_f32_16x16x32_bf16 v[208:211], a[148:151], a[172:175], v[208:211]\n\t"
"s_waitcnt lgkmcnt(5)\n\t"
"v_fma_f32 v98, v118, s64, -v20\n\t"
"v_accvgpr_write_b32 a13, v99\n\t"
"v_mfma_f32_16x16x32_bf16 v[208:211], a[156:159], a[176:179], v[208:211]\n\t"
"v_fma_f32 v99, v119, s64, -v21\n\t"
"v_mfma_f32_16x16x32_bf16 v[208:211], a[164:167], a[180:183], v[208:211]\n\t"
"s_waitcnt lgkmcnt(0)\n\t"
"s_barrier\n\t"
"v_mfma_f32_16x16x32_bf16 v[216:219], a[164:167], a[184:187], v[216:219]\n\t"
"v_cvt_pk_bf16_f32 v232, v232, v233\n\t"
"v_mfma_f32_16x16x32_bf16 v[216:219], a[156:159], a[180:183], v[216:219]\n\t"
"buffer_load_dwordx4 v247, s[12:15], s28 offen offset:128 nt lds\n\t"
"v_mfma_f32_16x16x32_bf16 v[216:219], a[148:151], a[176:179], v[216:219]\n\t"
"s_add_u32 m0, s24, 7040\n\t"
"s_add_u32 s28, s21, 196608\n\t"
"ds_write2st64_b64 v245, v[228:229], v[230:231] offset0:0 offset1:1\n\t"
"v_cvt_pk_bf16_f32 v233, v234, v235\n\t"
"v_mfma_f32_16x16x32_bf16 a[4:7], a[148:151], a[180:183], a[4:7]\n\t"
"v_accvgpr_write_b32 a14, v98\n\t"
"v_accvgpr_write_b32 a15, v99\n\t"
"v_mfma_f32_16x16x32_bf16 a[4:7], a[156:159], a[184:187], a[4:7]\n\t"
"ds_read_b64_tr_b16 v[228:229], v246 offset:32256\n\t"
"v_fma_f32 v98, v120, s64, -v22\n\t"
"v_mfma_f32_16x16x32_bf16 a[4:7], a[164:167], a[188:191], a[4:7]\n\t"
"buffer_load_dwordx4 v247, s[12:15], s28 offen offset:128 nt lds\n\t"
"v_fma_f32 v99, v121, s64, -v23\n\t"
"s_add_u32 m0, s24, 13184\n\t"
"s_add_u32 s28, s21, 393216\n\t"
"v_mfma_f32_16x16x32_bf16 a[12:15], a[164:167], a[192:195], a[12:15]\n\t"
"buffer_load_dwordx4 v247, s[12:15], s28 offen offset:128 nt lds\n\t"
"v_mfma_f32_16x16x32_bf16 a[12:15], a[156:159], a[188:191], a[12:15]\n\t"
"s_add_u32 m0, s24, 20864\n\t"
"s_add_u32 s28, s21, 589824\n\t"
"ds_read_b64_tr_b16 v[230:231], v246 offset:35328\n\t"
"v_mfma_f32_16x16x32_bf16 a[12:15], a[148:151], a[184:187], a[12:15]\n\t"
"ds_write2st64_b64 v245, v[224:225], v[226:227] offset0:2 offset1:3\n\t"
"v_accvgpr_write_b32 a20, v98\n\t"
"v_fma_f32 v98, v122, s64, -v24\n\t"
"v_accvgpr_write_b32 a21, v99\n\t"
"v_fma_f32 v99, v123, s64, -v25\n\t"
"v_accvgpr_write_b32 a22, v98\n\t"
"v_accvgpr_write_b32 a23, v99\n\t"
"v_fma_f32 v98, v124, s64, -v26\n\t"
"v_fma_f32 v99, v125, s64, -v27\n\t"
"v_mfma_f32_16x16x32_bf16 a[20:23], a[148:151], a[188:191], a[20:23]\n\t"
"ds_read_b64_tr_b16 v[224:225], v246 offset:39936\n\t"
"v_accvgpr_write_b32 a28, v98\n\t"
"v_mfma_f32_16x16x32_bf16 a[20:23], a[156:159], a[192:195], a[20:23]\n\t"
"ds_read_b64_tr_b16 v[226:227], v246 offset:43008\n\t"
"v_fma_f32 v98, v126, s64, -v28\n\t"
"v_mfma_f32_16x16x32_bf16 a[20:23], a[164:167], a[196:199], a[20:23]\n\t"
"ds_write_b64 v245, v[232:233] offset:2048\n\t"
"buffer_load_dwordx4 v247, s[12:15], s28 offen offset:128 nt lds\n\t"
"v_accvgpr_write_b32 a29, v99\n\t"
"v_fma_f32 v99, v127, s64, -v29\n\t"
"v_accvgpr_write_b32 a30, v98\n\t"
"v_accvgpr_write_b32 a31, v99\n\t"
"s_add_u32 m0, s24, 60800\n\t"
"s_add_u32 s28, s21, 1769472\n\t"
"v_mfma_f32_16x16x32_bf16 a[28:31], a[156:159], a[196:199], a[28:31]\n\t"
"buffer_load_dwordx4 v247, s[12:15], s28 offen offset:128 nt lds\n\t"
"v_mfma_f32_16x16x32_bf16 a[28:31], a[148:151], a[192:195], a[28:31]\n\t"
"s_waitcnt lgkmcnt(4)\n\t"
"ds_read_b64_tr_b16 v[232:233], v246 offset:46080\n\t"
"v_fma_f32 v98, v128, s64, -v30\n\t"
"v_mfma_f32_16x16x32_bf16 a[28:31], a[164:167], a[200:203], a[28:31]\n\t"
"s_add_u32 m0, s24, 66944\n\t"
"s_add_u32 s28, s21, 1966080\n\t"
"ds_read_b64_tr_b16 v[234:235], v246 offset:49152\n\t"
"v_fma_f32 v99, v129, s64, -v31\n\t"
"v_accvgpr_write_b32 a36, v98\n\t"
"v_fma_f32 v98, v130, s64, -v32\n\t"
"v_accvgpr_write_b32 a37, v99\n\t"
"v_fma_f32 v99, v131, s64, -v33\n\t"
"v_accvgpr_write_b32 a38, v98\n\t"
"v_accvgpr_write_b32 a39, v99\n\t"
"v_mfma_f32_16x16x32_bf16 v[228:231], v[228:231], v[220:223], 0\n\t"
"s_waitcnt lgkmcnt(3)\n\t"
"buffer_load_dwordx4 v247, s[12:15], s28 offen offset:128 nt lds\n\t"
"v_mfma_f32_16x16x32_bf16 v[224:227], v[224:227], v[220:223], 0\n\t"
"v_fma_f32 v98, v132, s64, -v34\n\t"
"v_fma_f32 v99, v133, s64, -v35\n\t"
"v_mfma_f32_16x16x32_bf16 a[36:39], a[156:159], a[200:203], a[36:39]\n\t"
"v_accvgpr_write_b32 a44, v98\n\t"
"v_fma_f32 v98, v134, s64, -v36\n\t"
"v_mfma_f32_16x16x32_bf16 a[36:39], a[148:151], a[196:199], a[36:39]\n\t"
"v_accvgpr_write_b32 a45, v99\n\t"
"v_fma_f32 v99, v135, s64, -v37\n\t"
"v_mfma_f32_16x16x32_bf16 a[36:39], a[164:167], a[204:207], a[36:39]\n\t"
"s_waitcnt lgkmcnt(0)\n\t"
"v_accvgpr_write_b32 a46, v98\n\t"
"v_accvgpr_write_b32 a47, v99\n\t"
"v_fma_f32 v98, v136, s64, -v38\n\t"
"v_fma_f32 v99, v137, s64, -v39\n\t"
"v_mfma_f32_16x16x32_bf16 v[232:235], v[232:235], v[220:223], 0\n\t"
"s_waitcnt vmcnt(18)\n\t"
"v_accvgpr_write_b32 a52, v98\n\t"
"v_fma_f32 v98, v138, s64, -v40\n\t"
"v_mfma_f32_16x16x32_bf16 a[44:47], v[240:243], a[168:171], a[44:47]\n\t"
"v_accvgpr_write_b32 a53, v99\n\t"
"v_fma_f32 v99, v139, s64, -v41\n\t"
"v_mfma_f32_16x16x32_bf16 a[44:47], v[248:251], a[176:179], a[44:47]\n\t"
"v_accvgpr_write_b32 a54, v98\n\t"
"v_accvgpr_write_b32 a55, v99\n\t"
"v_mfma_f32_16x16x32_bf16 a[44:47], a[208:211], a[172:175], a[44:47]\n\t"
"v_fma_f32 v98, v140, s64, -v42\n\t"
"v_fma_f32 v99, v141, s64, -v43\n\t"
"v_cvt_pk_bf16_f32 v228, v228, v229\n\t"
"v_cvt_pk_bf16_f32 v229, v230, v231\n\t"
"v_mfma_f32_16x16x32_bf16 a[52:55], v[240:243], a[172:175], a[52:55]\n\t"
"v_accvgpr_write_b32 a60, v98\n\t"
"v_fma_f32 v98, v142, s64, -v44\n\t"
"v_mfma_f32_16x16x32_bf16 a[52:55], a[208:211], a[176:179], a[52:55]\n\t"
"v_accvgpr_write_b32 a61, v99\n\t"
"v_fma_f32 v99, v143, s64, -v45\n\t"
"v_mfma_f32_16x16x32_bf16 a[52:55], v[248:251], a[180:183], a[52:55]\n\t"
"v_accvgpr_write_b32 a62, v98\n\t"
"v_accvgpr_write_b32 a63, v99\n\t"
"v_cvt_pk_bf16_f32 v230, v224, v225\n\t"
"v_cvt_pk_bf16_f32 v231, v226, v227\n\t"
"v_fma_f32 v98, v144, s64, -v46\n\t"
"v_fma_f32 v99, v145, s64, -v47\n\t"
"v_mfma_f32_16x16x32_bf16 a[60:63], v[248:251], a[184:187], a[60:63]\n\t"
"ds_read_b64_tr_b16 v[224:225], v246 offset:52224\n\t"
"v_accvgpr_write_b32 a68, v98\n\t"
"v_mfma_f32_16x16x32_bf16 a[60:63], a[208:211], a[180:183], a[60:63]\n\t"
"ds_read_b64_tr_b16 v[226:227], v246 offset:55296\n\t"
"v_fma_f32 v98, v146, s64, -v48\n\t"
"v_mfma_f32_16x16x32_bf16 a[60:63], v[240:243], a[176:179], a[60:63]\n\t"
"ds_write2st64_b64 v245, v[228:229], v[230:231] offset0:5 offset1:6\n\t"
"v_accvgpr_write_b32 a69, v99\n\t"
"v_fma_f32 v99, v147, s64, -v49\n\t"
"v_accvgpr_write_b32 a70, v98\n\t"
"v_accvgpr_write_b32 a71, v99\n\t"
"v_fma_f32 v98, v148, s64, -v50\n\t"
"v_fma_f32 v99, v149, s64, -v51\n\t"
"v_cvt_pk_bf16_f32 v232, v232, v233\n\t"
"v_cvt_pk_bf16_f32 v233, v234, v235\n\t"
"v_mfma_f32_16x16x32_bf16 a[68:71], v[240:243], a[180:183], a[68:71]\n\t"
"v_accvgpr_write_b32 a76, v98\n\t"
"v_fma_f32 v98, v150, s64, -v52\n\t"
"v_mfma_f32_16x16x32_bf16 a[68:71], a[208:211], a[184:187], a[68:71]\n\t"
"v_accvgpr_write_b32 a77, v99\n\t"
"v_fma_f32 v99, v151, s64, -v53\n\t"
"v_mfma_f32_16x16x32_bf16 a[68:71], v[248:251], a[188:191], a[68:71]\n\t"
"v_accvgpr_write_b32 a78, v98\n\t"
"v_accvgpr_write_b32 a79, v99\n\t"
"v_fma_f32 v98, v152, s64, -v54\n\t"
"v_fma_f32 v99, v153, s64, -v55\n\t"
"v_mfma_f32_16x16x32_bf16 a[76:79], a[208:211], a[188:191], a[76:79]\n\t"
"s_waitcnt lgkmcnt(1)\n\t"
"v_accvgpr_write_b32 a84, v98\n\t"
"v_fma_f32 v98, v154, s64, -v56\n\t"
"v_mfma_f32_16x16x32_bf16 a[76:79], v[248:251], a[192:195], a[76:79]\n\t"
"v_accvgpr_write_b32 a85, v99\n\t"
"v_fma_f32 v99, v155, s64, -v57\n\t"
"v_mfma_f32_16x16x32_bf16 a[76:79], v[240:243], a[184:187], a[76:79]\n\t"
"v_accvgpr_write_b32 a86, v98\n\t"
"v_accvgpr_write_b32 a87, v99\n\t"
"v_fma_f32 v98, v156, s64, -v58\n\t"
"v_fma_f32 v99, v157, s64, -v59\n\t"
"v_mfma_f32_16x16x32_bf16 v[224:227], v[224:227], v[220:223], 0\n\t"
"v_accvgpr_write_b32 a92, v98\n\t"
"v_fma_f32 v98, v158, s64, -v60\n\t"
"v_mfma_f32_16x16x32_bf16 a[84:87], v[240:243], a[188:191], a[84:87]\n\t"
"s_mov_b32 s20, 73728\n\t"
"v_accvgpr_write_b32 a93, v99\n\t"
"v_fma_f32 v99, v159, s64, -v61\n\t"
"v_mfma_f32_16x16x32_bf16 a[84:87], v[248:251], a[196:199], a[84:87]\n\t"
"v_accvgpr_write_b32 a94, v98\n\t"
"v_accvgpr_write_b32 a95, v99\n\t"
"v_mfma_f32_16x16x32_bf16 a[84:87], a[208:211], a[192:195], a[84:87]\n\t"
"v_fma_f32 v98, v160, s64, -v62\n\t"
"v_fma_f32 v99, v161, s64, -v63\n\t"
"v_mfma_f32_16x16x32_bf16 a[92:95], v[240:243], a[192:195], a[92:95]\n\t"
"v_accvgpr_write_b32 a100, v98\n\t"
"v_fma_f32 v98, v162, s64, -v64\n\t"
"v_mfma_f32_16x16x32_bf16 a[92:95], a[208:211], a[196:199], a[92:95]\n\t"
"v_accvgpr_write_b32 a101, v99\n\t"
"v_fma_f32 v99, v163, s64, -v65\n\t"
"v_mfma_f32_16x16x32_bf16 a[92:95], v[248:251], a[200:203], a[92:95]\n\t"
"v_accvgpr_write_b32 a102, v98\n\t"
"v_accvgpr_write_b32 a103, v99\n\t"
"v_fma_f32 v98, v164, s64, -v66\n\t"
"v_fma_f32 v99, v165, s64, -v67\n\t"
"v_cvt_pk_bf16_f32 v234, v224, v225\n\t"
"v_cvt_pk_bf16_f32 v235, v226, v227\n\t"
"v_mfma_f32_16x16x32_bf16 a[100:103], a[208:211], a[200:203], a[100:103]\n\t"
"v_accvgpr_write_b32 a108, v98\n\t"
"v_fma_f32 v98, v166, s64, -v68\n\t"
"v_mfma_f32_16x16x32_bf16 a[100:103], v[240:243], a[196:199], a[100:103]\n\t"
"v_accvgpr_write_b32 a109, v99\n\t"
"v_fma_f32 v99, v167, s64, -v69\n\t"
"v_mfma_f32_16x16x32_bf16 a[100:103], v[248:251], a[204:207], a[100:103]\n\t"
"ds_write2st64_b64 v245, v[232:233], v[234:235] offset0:7 offset1:8\n\t"
"v_accvgpr_write_b32 a110, v98\n\t"
"v_accvgpr_write_b32 a111, v99\n\t"
"v_fma_f32 v98, v168, s64, -v70\n\t"
"v_fma_f32 v99, v169, s64, -v71\n\t"
"s_waitcnt vmcnt(15)\n\t"
"v_mfma_f32_16x16x32_bf16 a[108:111], a[212:215], a[168:171], a[108:111]\n\t"
"buffer_load_dwordx4 a[208:211], off, s[80:83], s20 offset:0 sc1\n\t"
"v_mfma_f32_16x16x32_bf16 a[108:111], a[220:223], a[176:179], a[108:111]\n\t"
"v_accvgpr_write_b32 a116, v98\n\t"
"v_mfma_f32_16x16x32_bf16 a[108:111], a[216:219], a[172:175], a[108:111]\n\t"
"v_accvgpr_write_b32 a117, v99\n\t"
"v_fma_f32 v98, v170, s64, -v72\n\t"
"v_accvgpr_write_b32 a118, v98\n\t"
"v_fma_f32 v99, v171, s64, -v73\n\t"
"v_accvgpr_write_b32 a119, v99\n\t"
"s_waitcnt lgkmcnt(0)\n\t"
"s_barrier\n\t"
"v_mfma_f32_16x16x32_bf16 a[116:119], a[212:215], a[172:175], a[116:119]\n\t"
"ds_read_b128 a[168:171], v244 offset:0\n\t"
"v_mfma_f32_16x16x32_bf16 a[116:119], a[216:219], a[176:179], a[116:119]\n\t"
"buffer_load_dwordx4 v[240:243], off, s[80:83], s20 offset:1024 sc1\n\t"
"v_mfma_f32_16x16x32_bf16 a[116:119], a[220:223], a[180:183], a[116:119]\n\t"
"ds_read_b128 a[172:175], v244 offset:512\n\t"
"v_fma_f32 v98, v172, s64, -v74\n\t"
"v_fma_f32 v99, v173, s64, -v75\n\t"
"v_accvgpr_write_b32 a124, v98\n\t"
"v_fma_f32 v98, v174, s64, -v76\n\t"
"v_accvgpr_write_b32 a125, v99\n\t"
"v_fma_f32 v99, v175, s64, -v77\n\t"
"v_accvgpr_write_b32 a126, v98\n\t"
"v_accvgpr_write_b32 a127, v99\n\t"
"v_fma_f32 v98, v176, s64, -v78\n\t"
"v_fma_f32 v99, v177, s64, -v79\n\t"
"v_mfma_f32_16x16x32_bf16 a[124:127], a[220:223], a[184:187], a[124:127]\n\t"
"v_accvgpr_write_b32 a132, v98\n\t"
"v_fma_f32 v98, v178, s64, -v80\n\t"
"v_mfma_f32_16x16x32_bf16 a[124:127], a[216:219], a[180:183], a[124:127]\n\t"
"v_accvgpr_write_b32 a133, v99\n\t"
"v_fma_f32 v99, v179, s64, -v81\n\t"
"v_mfma_f32_16x16x32_bf16 a[124:127], a[212:215], a[176:179], a[124:127]\n\t"
"ds_read_b128 a[176:179], v244 offset:1024\n\t"
"v_accvgpr_write_b32 a134, v98\n\t"
"v_accvgpr_write_b32 a135, v99\n\t"
"v_fma_f32 v98, v180, s64, -v82\n\t"
"v_fma_f32 v99, v181, s64, -v83\n\t"
"v_mfma_f32_16x16x32_bf16 a[132:135], a[212:215], a[180:183], a[132:135]\n\t"
"ds_read_b128 a[180:183], v244 offset:1536\n\t"
"v_accvgpr_write_b32 a140, v98\n\t"
"v_mfma_f32_16x16x32_bf16 a[132:135], a[216:219], a[184:187], a[132:135]\n\t"
"v_fma_f32 v98, v182, s64, -v84\n\t"
"v_accvgpr_write_b32 a141, v99\n\t"
"v_mfma_f32_16x16x32_bf16 a[132:135], a[220:223], a[188:191], a[132:135]\n\t"
"buffer_load_dwordx4 v[248:251], off, s[80:83], s20 offset:2048 sc1\n\t"
"v_fma_f32 v99, v183, s64, -v85\n\t"
"v_accvgpr_write_b32 a142, v98\n\t"
"v_accvgpr_write_b32 a143, v99\n\t"
"v_fma_f32 v98, v184, s64, -v86\n\t"
"v_fma_f32 v99, v185, s64, -v87\n\t"
"v_mfma_f32_16x16x32_bf16 a[140:143], a[220:223], a[192:195], a[140:143]\n\t"
"v_accvgpr_write_b32 a148, v98\n\t"
"v_fma_f32 v98, v186, s64, -v88\n\t"
"v_mfma_f32_16x16x32_bf16 a[140:143], a[216:219], a[188:191], a[140:143]\n\t"
"v_accvgpr_write_b32 a149, v99\n\t"
"v_fma_f32 v99, v187, s64, -v89\n\t"
"v_mfma_f32_16x16x32_bf16 a[140:143], a[212:215], a[184:187], a[140:143]\n\t"
"ds_read_b128 a[184:187], v244 offset:2048\n\t"
"v_accvgpr_write_b32 a150, v98\n\t"
"v_accvgpr_write_b32 a151, v99\n\t"
"v_fma_f32 v98, v188, s64, -v90\n\t"
"v_fma_f32 v99, v189, s64, -v91\n\t"
"v_mfma_f32_16x16x32_bf16 a[148:151], a[212:215], a[188:191], a[148:151]\n\t"
"s_mov_b32 s20, 86016\n\t"
"ds_read_b128 a[188:191], v244 offset:2560\n\t"
"v_accvgpr_write_b32 a156, v98\n\t"
"v_mfma_f32_16x16x32_bf16 a[148:151], a[216:219], a[192:195], a[148:151]\n\t"
"v_fma_f32 v98, v190, s64, -v92\n\t"
"v_accvgpr_write_b32 a157, v99\n\t"
"v_mfma_f32_16x16x32_bf16 a[148:151], a[220:223], a[196:199], a[148:151]\n\t"
"v_fma_f32 v99, v191, s64, -v93\n\t"
"v_accvgpr_write_b32 a158, v98\n\t"
"v_accvgpr_write_b32 a159, v99\n\t"
"v_fma_f32 v98, v192, s64, -v94\n\t"
"v_fma_f32 v99, v193, s64, -v95\n\t"
"v_mfma_f32_16x16x32_bf16 a[156:159], a[220:223], a[200:203], a[156:159]\n\t"
"v_accvgpr_write_b32 a164, v98\n\t"
"v_fma_f32 v98, v194, s64, -v96\n\t"
"v_mfma_f32_16x16x32_bf16 a[156:159], a[216:219], a[196:199], a[156:159]\n\t"
"v_accvgpr_write_b32 a165, v99\n\t"
"v_fma_f32 v99, v195, s64, -v97\n\t"
"v_mfma_f32_16x16x32_bf16 a[156:159], a[212:215], a[192:195], a[156:159]\n\t"
"ds_read_b128 a[192:195], v244 offset:3072\n\t"
"v_accvgpr_write_b32 a166, v98\n\t"
"v_accvgpr_write_b32 a167, v99\n\t"
"v_fma_f32 v0, v2, s52, v100\n\t"
"v_fma_f32 v1, v3, s52, v101\n\t"
"v_mfma_f32_16x16x32_bf16 a[164:167], a[212:215], a[196:199], a[164:167]\n\t"
"Lpg3_%=:\n\t"
"s_cbranch_vccnz Lpg7_%=\n\t"
"ds_read_b128 a[196:199], v244 offset:3584\n\t"
"v_fma_f32 v100, -v100, s66, v2\n\t"
"v_mfma_f32_16x16x32_bf16 a[164:167], a[216:219], a[200:203], a[164:167]\n\t"
"ds_read_b128 a[200:203], v244 offset:4096\n\t"
"v_fma_f32 v2, v4, s52, v102\n\t"
"v_mfma_f32_16x16x32_bf16 a[164:167], a[220:223], a[204:207], a[164:167]\n\t"
"ds_read_b128 a[204:207], v244 offset:4608\n\t"
"v_fma_f32 v101, -v101, s66, v3\n\t"
"v_fma_f32 v3, v5, s52, v103\n\t"
"v_fma_f32 v102, -v102, s66, v4\n\t"
"v_fma_f32 v103, -v103, s66, v5\n\t"
"v_fma_f32 v4, v6, s52, v104\n\t"
"v_fma_f32 v5, v7, s52, v105\n\t"
"Lst3_%=:\n\t"
"s_waitcnt vmcnt(15) lgkmcnt(7)\n\t"
"v_mfma_f32_16x16x32_bf16 v[0:3], a[224:227], a[168:171], v[0:3]\n\t"
"buffer_load_dwordx4 a[212:215], off, s[80:83], s20 offset:0 sc1\n\t"
"v_mfma_f32_16x16x32_bf16 v[0:3], a[232:235], a[176:179], v[0:3]\n\t"
"buffer_load_dwordx4 a[216:219], off, s[80:83], s20 offset:1024 sc1\n\t"
"v_mfma_f32_16x16x32_bf16 v[0:3], a[228:231], a[172:175], v[0:3]\n\t"
"v_fma_f32 v104, -v104, s66, v6\n\t"
"v_fma_f32 v6, v8, s52, v106\n\t"
"v_fma_f32 v105, -v105, s66, v7\n\t"
"v_fma_f32 v7, v9, s52, v107\n\t"
"v_fma_f32 v106, -v106, s66, v8\n\t"
"v_fma_f32 v107, -v107, s66, v9\n\t"
"v_fma_f32 v8, v10, s52, v108\n\t"
"v_fma_f32 v9, v11, s52, v109\n\t"
"s_waitcnt lgkmcnt(6)\n\t"
"v_mfma_f32_16x16x32_bf16 v[4:7], a[224:227], a[172:175], v[4:7]\n\t"
"buffer_load_dwordx4 a[220:223], off, s[80:83], s20 offset:2048 sc1\n\t"
"v_mfma_f32_16x16x32_bf16 v[4:7], a[228:231], a[176:179], v[4:7]\n\t"
"v_fma_f32 v108, -v108, s66, v10\n\t"
"v_fma_f32 v10, v12, s52, v110\n\t"
"v_mfma_f32_16x16x32_bf16 v[4:7], a[232:235], a[180:183], v[4:7]\n\t"
"v_fma_f32 v109, -v109, s66, v11\n\t"
"v_fma_f32 v11, v13, s52, v111\n\t"
"v_fma_f32 v110, -v110, s66, v12\n\t"
"v_fma_f32 v111, -v111, s66, v13\n\t"
"v_fma_f32 v12, v14, s52, v112\n\t"
"v_fma_f32 v13, v15, s52, v113\n\t"
"s_waitcnt lgkmcnt(5)\n\t"
"v_mfma_f32_16x16x32_bf16 v[8:11], a[232:235], a[184:187], v[8:11]\n\t"
"v_fma_f32 v112, -v112, s66, v14\n\t"
"v_fma_f32 v14, v16, s52, v114\n\t"
"v_mfma_f32_16x16x32_bf16 v[8:11], a[228:231], a[180:183], v[8:11]\n\t"
"v_fma_f32 v113, -v113, s66, v15\n\t"
"v_fma_f32 v15, v17, s52, v115\n\t"
"v_mfma_f32_16x16x32_bf16 v[8:11], a[224:227], a[176:179], v[8:11]\n\t"
"ds_read_b64_tr_b16 v[228:229], v246 offset:512\n\t"
"v_fma_f32 v114, -v114, s66, v16\n\t"
"v_fma_f32 v115, -v115, s66, v17\n\t"
"v_fma_f32 v16, v18, s52, v116\n\t"
"v_fma_f32 v17, v19, s52, v117\n\t"
"s_waitcnt lgkmcnt(5)\n\t"
"v_mfma_f32_16x16x32_bf16 v[12:15], a[224:227], a[180:183], v[12:15]\n\t"
"ds_read_b64_tr_b16 v[230:231], v246 offset:3584\n\t"
"v_fma_f32 v116, -v116, s66, v18\n\t"
"v_mfma_f32_16x16x32_bf16 v[12:15], a[228:231], a[184:187], v[12:15]\n\t"
"ds_read_b64_tr_b16 v[224:225], v246 offset:6656\n\t"
"v_fma_f32 v18, v20, s52, v118\n\t"
"v_mfma_f32_16x16x32_bf16 v[12:15], a[232:235], a[188:191], v[12:15]\n\t"
"ds_read_b64_tr_b16 v[226:227], v246 offset:9728\n\t"
"v_fma_f32 v117, -v117, s66, v19\n\t"
"v_fma_f32 v19, v21, s52, v119\n\t"
"v_fma_f32 v118, -v118, s66, v20\n\t"
"v_fma_f32 v119, -v119, s66, v21\n\t"
"v_fma_f32 v20, v22, s52, v120\n\t"
"v_fma_f32 v21, v23, s52, v121\n\t"
"s_waitcnt lgkmcnt(7)\n\t"
"v_mfma_f32_16x16x32_bf16 v[16:19], a[232:235], a[192:195], v[16:19]\n\t"
"v_fma_f32 v120, -v120, s66, v22\n\t"
"v_fma_f32 v22, v24, s52, v122\n\t"
"v_mfma_f32_16x16x32_bf16 v[16:19], a[228:231], a[188:191], v[16:19]\n\t"
"v_fma_f32 v121, -v121, s66, v23\n\t"
"v_fma_f32 v23, v25, s52, v123\n\t"
"v_mfma_f32_16x16x32_bf16 v[16:19], a[224:227], a[184:187], v[16:19]\n\t"
"v_fma_f32 v122, -v122, s66, v24\n\t"
"v_fma_f32 v123, -v123, s66, v25\n\t"
"v_fma_f32 v24, v26, s52, v124\n\t"
"v_fma_f32 v25, v27, s52, v125\n\t"
"s_waitcnt lgkmcnt(6)\n\t"
"v_mfma_f32_16x16x32_bf16 v[20:23], a[224:227], a[188:191], v[20:23]\n\t"
"s_mov_b32 s20, 98304\n\t"
"v_fma_f32 v124, -v124, s66, v26\n\t"
"v_fma_f32 v26, v28, s52, v126\n\t"
"v_mfma_f32_16x16x32_bf16 v[20:23], a[228:231], a[192:195], v[20:23]\n\t"
"v_fma_f32 v125, -v125, s66, v27\n\t"
"v_fma_f32 v27, v29, s52, v127\n\t"
"v_mfma_f32_16x16x32_bf16 v[20:23], a[232:235], a[196:199], v[20:23]\n\t"
"v_fma_f32 v126, -v126, s66, v28\n\t"
"v_fma_f32 v127, -v127, s66, v29\n\t"
"v_fma_f32 v28, v30, s52, v128\n\t"
"v_fma_f32 v29, v31, s52, v129\n\t"
"s_waitcnt lgkmcnt(5)\n\t"
"v_mfma_f32_16x16x32_bf16 v[24:27], a[232:235], a[200:203], v[24:27]\n\t"
"v_fma_f32 v128, -v128, s66, v30\n\t"
"v_fma_f32 v30, v32, s52, v130\n\t"
"v_mfma_f32_16x16x32_bf16 v[24:27], a[228:231], a[196:199], v[24:27]\n\t"
"v_fma_f32 v129, -v129, s66, v31\n\t"
"v_fma_f32 v31, v33, s52, v131\n\t"
"v_mfma_f32_16x16x32_bf16 v[24:27], a[224:227], a[192:195], v[24:27]\n\t"
"v_fma_f32 v130, -v130, s66, v32\n\t"
"v_fma_f32 v131, -v131, s66, v33\n\t"
"s_waitcnt lgkmcnt(4)\n\t"
"v_mfma_f32_16x16x32_bf16 v[28:31], a[224:227], a[196:199], v[28:31]\n\t"
"buffer_load_dwordx4 a[224:227], off, s[80:83], s20 offset:0 sc1\n\t"
"v_mfma_f32_16x16x32_bf16 v[28:31], a[228:231], a[200:203], v[28:31]\n\t"
"s_waitcnt lgkmcnt(0)\n\t"
"v_fma_f32 v32, v34, s52, v132\n\t"
"v_fma_f32 v33, v35, s52, v133\n\t"
"v_mfma_f32_16x16x32_bf16 v[28:31], a[232:235], a[204:207], v[28:31]\n\t"
"v_fma_f32 v132, -v132, s66, v34\n\t"
"v_fma_f32 v34, v36, s52, v134\n\t"
"v_fma_f32 v133, -v133, s66, v35\n\t"
"v_fma_f32 v35, v37, s52, v135\n\t"
"v_fma_f32 v134, -v134, s66, v36\n\t"
"v_fma_f32 v135, -v135, s66, v37\n\t"
"v_mfma_f32_16x16x32_bf16 v[228:231], v[228:231], v[220:223], 0\n\t"
"s_waitcnt vmcnt(16)\n\t"
"v_fma_f32 v36, v38, s52, v136\n\t"
"v_fma_f32 v37, v39, s52, v137\n\t"
"v_mfma_f32_16x16x32_bf16 v[224:227], v[224:227], v[220:223], 0\n\t"
"v_fma_f32 v136, -v136, s66, v38\n\t"
"v_fma_f32 v38, v40, s52, v138\n\t"
"v_mfma_f32_16x16x32_bf16 v[32:35], a[236:239], a[168:171], v[32:35]\n\t"
"v_fma_f32 v137, -v137, s66, v39\n\t"
"v_fma_f32 v39, v41, s52, v139\n\t"
"v_mfma_f32_16x16x32_bf16 v[32:35], a[244:247], a[176:179], v[32:35]\n\t"
"v_fma_f32 v138, -v138, s66, v40\n\t"
"v_fma_f32 v139, -v139, s66, v41\n\t"
"v_mfma_f32_16x16x32_bf16 v[32:35], a[240:243], a[172:175], v[32:35]\n\t"
"buffer_load_dwordx4 a[228:231], off, s[80:83], s20 offset:1024 sc1\n\t"
"v_fma_f32 v40, v42, s52, v140\n\t"
"v_fma_f32 v41, v43, s52, v141\n\t"
"v_mfma_f32_16x16x32_bf16 v[36:39], a[236:239], a[172:175], v[36:39]\n\t"
"v_fma_f32 v140, -v140, s66, v42\n\t"
"v_fma_f32 v42, v44, s52, v142\n\t"
"v_mfma_f32_16x16x32_bf16 v[36:39], a[240:243], a[176:179], v[36:39]\n\t"
"v_fma_f32 v141, -v141, s66, v43\n\t"
"v_fma_f32 v43, v45, s52, v143\n\t"
"v_mfma_f32_16x16x32_bf16 v[36:39], a[244:247], a[180:183], v[36:39]\n\t"
"buffer_load_dwordx4 a[232:235], off, s[80:83], s20 offset:2048 sc1\n\t"
"v_fma_f32 v142, -v142, s66, v44\n\t"
"v_fma_f32 v143, -v143, s66, v45\n\t"
"v_fma_f32 v44, v46, s52, v144\n\t"
"v_fma_f32 v45, v47, s52, v145\n\t"
"v_mfma_f32_16x16x32_bf16 v[40:43], a[244:247], a[184:187], v[40:43]\n\t"
"v_fma_f32 v144, -v144, s66, v46\n\t"
"v_fma_f32 v46, v48, s52, v146\n\t"
"v_mfma_f32_16x16x32_bf16 v[40:43], a[240:243], a[180:183], v[40:43]\n\t"
"v_fma_f32 v145, -v145, s66, v47\n\t"
"v_fma_f32 v47, v49, s52, v147\n\t"
"v_mfma_f32_16x16x32_bf16 v[40:43], a[236:239], a[176:179], v[40:43]\n\t"
"ds_read_b64_tr_b16 v[232:233], v246 offset:12800\n\t"
"v_fma_f32 v146, -v146, s66, v48\n\t"
"v_fma_f32 v147, -v147, s66, v49\n\t"
"v_cvt_pk_bf16_f32 v228, v228, v229\n\t"
"v_cvt_pk_bf16_f32 v229, v230, v231\n\t"
"v_cvt_pk_bf16_f32 v230, v224, v225\n\t"
"v_cvt_pk_bf16_f32 v231, v226, v227\n\t"
"v_fma_f32 v48, v50, s52, v148\n\t"
"v_fma_f32 v49, v51, s52, v149\n\t"
"v_mfma_f32_16x16x32_bf16 v[44:47], a[236:239], a[180:183], v[44:47]\n\t"
"ds_read_b64_tr_b16 v[234:235], v246 offset:15872\n\t"
"v_fma_f32 v148, -v148, s66, v50\n\t"
"v_mfma_f32_16x16x32_bf16 v[44:47], a[240:243], a[184:187], v[44:47]\n\t"
"ds_read_b64_tr_b16 v[224:225], v246 offset:20480\n\t"
"v_fma_f32 v50, v52, s52, v150\n\t"
"v_mfma_f32_16x16x32_bf16 v[44:47], a[244:247], a[188:191], v[44:47]\n\t"
"ds_read_b64_tr_b16 v[226:227], v246 offset:23552\n\t"
"v_fma_f32 v149, -v149, s66, v51\n\t"
"v_fma_f32 v51, v53, s52, v151\n\t"
"v_fma_f32 v150, -v150, s66, v52\n\t"
"v_fma_f32 v151, -v151, s66, v53\n\t"
"v_fma_f32 v52, v54, s52, v152\n\t"
"v_fma_f32 v53, v55, s52, v153\n\t"
"v_mfma_f32_16x16x32_bf16 v[48:51], a[244:247], a[192:195], v[48:51]\n\t"
"v_fma_f32 v152, -v152, s66, v54\n\t"
"v_fma_f32 v54, v56, s52, v154\n\t"
"v_mfma_f32_16x16x32_bf16 v[48:51], a[240:243], a[188:191], v[48:51]\n\t"
"v_fma_f32 v153, -v153, s66, v55\n\t"
"v_fma_f32 v55, v57, s52, v155\n\t"
"v_mfma_f32_16x16x32_bf16 v[48:51], a[236:239], a[184:187], v[48:51]\n\t"
"v_fma_f32 v154, -v154, s66, v56\n\t"
"v_fma_f32 v155, -v155, s66, v57\n\t"
"v_fma_f32 v56, v58, s52, v156\n\t"
"v_fma_f32 v57, v59, s52, v157\n\t"
"v_mfma_f32_16x16x32_bf16 v[52:55], a[236:239], a[188:191], v[52:55]\n\t"
"s_mov_b32 s20, 331776\n\t"
"v_fma_f32 v156, -v156, s66, v58\n\t"
"v_fma_f32 v58, v60, s52, v158\n\t"
"v_mfma_f32_16x16x32_bf16 v[52:55], a[240:243], a[192:195], v[52:55]\n\t"
"v_fma_f32 v157, -v157, s66, v59\n\t"
"v_fma_f32 v59, v61, s52, v159\n\t"
"v_mfma_f32_16x16x32_bf16 v[52:55], a[244:247], a[196:199], v[52:55]\n\t"
"v_fma_f32 v158, -v158, s66, v60\n\t"
"v_fma_f32 v159, -v159, s66, v61\n\t"
"v_fma_f32 v60, v62, s52, v160\n\t"
"v_fma_f32 v61, v63, s52, v161\n\t"
"v_mfma_f32_16x16x32_bf16 v[56:59], a[244:247], a[200:203], v[56:59]\n\t"
"v_fma_f32 v160, -v160, s66, v62\n\t"
"v_fma_f32 v62, v64, s52, v162\n\t"
"v_mfma_f32_16x16x32_bf16 v[56:59], a[240:243], a[196:199], v[56:59]\n\t"
"v_fma_f32 v161, -v161, s66, v63\n\t"
"v_fma_f32 v63, v65, s52, v163\n\t"
"v_mfma_f32_16x16x32_bf16 v[56:59], a[236:239], a[192:195], v[56:59]\n\t"
"v_fma_f32 v162, -v162, s66, v64\n\t"
"v_fma_f32 v163, -v163, s66, v65\n\t"
"v_mfma_f32_16x16x32_bf16 v[60:63], a[236:239], a[196:199], v[60:63]\n\t"
"s_waitcnt lgkmcnt(2)\n\t"
"v_fma_f32 v64, v66, s52, v164\n\t"
"v_fma_f32 v65, v67, s52, v165\n\t"
"v_mfma_f32_16x16x32_bf16 v[60:63], a[240:243], a[200:203], v[60:63]\n\t"
"s_waitcnt lgkmcnt(0)\n\t"
"v_fma_f32 v164, -v164, s66, v66\n\t"
"v_fma_f32 v66, v68, s52, v166\n\t"
"v_mfma_f32_16x16x32_bf16 v[60:63], a[244:247], a[204:207], v[60:63]\n\t"
"v_fma_f32 v165, -v165, s66, v67\n\t"
"v_fma_f32 v67, v69, s52, v167\n\t"
"v_fma_f32 v166, -v166, s66, v68\n\t"
"v_fma_f32 v167, -v167, s66, v69\n\t"
"v_mfma_f32_16x16x32_bf16 v[232:235], v[232:235], v[220:223], 0\n\t"
"buffer_load_dwordx4 a[236:239], off, s[80:83], s20 offset:0 sc1\n\t"
"v_mfma_f32_16x16x32_bf16 v[224:227], v[224:227], v[220:223], 0\n\t"
"s_waitcnt vmcnt(16)\n\t"
"v_fma_f32 v68, v70, s52, v168\n\t"
"v_fma_f32 v69, v71, s52, v169\n\t"
"v_mfma_f32_16x16x32_bf16 v[64:67], a[248:251], a[168:171], v[64:67]\n\t"
"ds_read_b128 a[168:171], v244 offset:9216\n\t"
"v_fma_f32 v168, -v168, s66, v70\n\t"
"v_mfma_f32_16x16x32_bf16 v[64:67], v[236:239], a[176:179], v[64:67]\n\t"
"v_fma_f32 v70, v72, s52, v170\n\t"
"v_fma_f32 v169, -v169, s66, v71\n\t"
"v_mfma_f32_16x16x32_bf16 v[64:67], a[252:255], a[172:175], v[64:67]\n\t"
"buffer_load_dwordx4 a[240:243], off, s[80:83], s20 offset:1024 sc1\n\t"
"v_fma_f32 v71, v73, s52, v171\n\t"
"v_fma_f32 v170, -v170, s66, v72\n\t"
"v_fma_f32 v171, -v171, s66, v73\n\t"
"v_fma_f32 v72, v74, s52, v172\n\t"
"v_fma_f32 v73, v75, s52, v173\n\t"
"v_mfma_f32_16x16x32_bf16 v[68:71], a[248:251], a[172:175], v[68:71]\n\t"
"ds_read_b128 a[172:175], v244 offset:9728\n\t"
"v_fma_f32 v172, -v172, s66, v74\n\t"
"v_mfma_f32_16x16x32_bf16 v[68:71], a[252:255], a[176:179], v[68:71]\n\t"
"v_fma_f32 v74, v76, s52, v174\n\t"
"v_fma_f32 v173, -v173, s66, v75\n\t"
"v_mfma_f32_16x16x32_bf16 v[68:71], v[236:239], a[180:183], v[68:71]\n\t"
"buffer_load_dwordx4 a[244:247], off, s[80:83], s20 offset:2048 sc1\n\t"
"v_fma_f32 v75, v77, s52, v175\n\t"
"v_fma_f32 v174, -v174, s66, v76\n\t"
"v_fma_f32 v175, -v175, s66, v77\n\t"
"v_fma_f32 v76, v78, s52, v176\n\t"
"v_fma_f32 v77, v79, s52, v177\n\t"
"v_mfma_f32_16x16x32_bf16 v[72:75], v[236:239], a[184:187], v[72:75]\n\t"
"v_fma_f32 v176, -v176, s66, v78\n\t"
"v_fma_f32 v78, v80, s52, v178\n\t"
"v_mfma_f32_16x16x32_bf16 v[72:75], a[252:255], a[180:183], v[72:75]\n\t"
"v_fma_f32 v177, -v177, s66, v79\n\t"
"v_fma_f32 v79, v81, s52, v179\n\t"
"v_mfma_f32_16x16x32_bf16 v[72:75], a[248:251], a[176:179], v[72:75]\n\t"
"ds_read_b128 a[176:179], v244 offset:10240\n\t"
"v_fma_f32 v178, -v178, s66, v80\n\t"
"v_fma_f32 v179, -v179, s66, v81\n\t"
"v_cvt_pk_bf16_f32 v232, v232, v233\n\t"
"v_cvt_pk_bf16_f32 v233, v234, v235\n\t"
"v_cvt_pk_bf16_f32 v234, v224, v225\n\t"
"v_cvt_pk_bf16_f32 v235, v226, v227\n\t"
"v_fma_f32 v80, v82, s52, v180\n\t"
"v_fma_f32 v81, v83, s52, v181\n\t"
"v_mfma_f32_16x16x32_bf16 v[76:79], a[248:251], a[180:183], v[76:79]\n\t"
"ds_read_b128 a[180:183], v244 offset:10752\n\t"
"v_fma_f32 v180, -v180, s66, v82\n\t"
"v_mfma_f32_16x16x32_bf16 v[76:79], a[252:255], a[184:187], v[76:79]\n\t"
"ds_read_b64_tr_b16 v[224:225], v246 offset:26624\n\t"
"v_fma_f32 v82, v84, s52, v182\n\t"
"v_mfma_f32_16x16x32_bf16 v[76:79], v[236:239], a[188:191], v[76:79]\n\t"
"ds_read_b64_tr_b16 v[226:227], v246 offset:29696\n\t"
"v_fma_f32 v181, -v181, s66, v83\n\t"
"v_fma_f32 v83, v85, s52, v183\n\t"
"v_fma_f32 v182, -v182, s66, v84\n\t"
"v_fma_f32 v183, -v183, s66, v85\n\t"
"v_fma_f32 v84, v86, s52, v184\n\t"
"v_fma_f32 v85, v87, s52, v185\n\t"
"v_mfma_f32_16x16x32_bf16 v[80:83], v[236:239], a[192:195], v[80:83]\n\t"
"v_fma_f32 v184, -v184, s66, v86\n\t"
"v_fma_f32 v86, v88, s52, v186\n\t"
"v_mfma_f32_16x16x32_bf16 v[80:83], a[252:255], a[188:191], v[80:83]\n\t"
"s_mov_b32 s20, 344064\n\t"
"v_fma_f32 v185, -v185, s66, v87\n\t"
"v_fma_f32 v87, v89, s52, v187\n\t"
"v_mfma_f32_16x16x32_bf16 v[80:83], a[248:251], a[184:187], v[80:83]\n\t"
"ds_read_b128 a[184:187], v244 offset:11264\n\t"
"v_fma_f32 v186, -v186, s66, v88\n\t"
"v_fma_f32 v187, -v187, s66, v89\n\t"
"v_fma_f32 v88, v90, s52, v188\n\t"
"v_fma_f32 v89, v91, s52, v189\n\t"
"v_mfma_f32_16x16x32_bf16 v[84:87], a[248:251], a[188:191], v[84:87]\n\t"
"ds_read_b128 a[188:191], v244 offset:11776\n\t"
"v_fma_f32 v188, -v188, s66, v90\n\t"
"v_mfma_f32_16x16x32_bf16 v[84:87], a[252:255], a[192:195], v[84:87]\n\t"
"v_fma_f32 v90, v92, s52, v190\n\t"
"v_fma_f32 v189, -v189, s66, v91\n\t"
"v_mfma_f32_16x16x32_bf16 v[84:87], v[236:239], a[196:199], v[84:87]\n\t"
"v_fma_f32 v91, v93, s52, v191\n\t"
"v_fma_f32 v190, -v190, s66, v92\n\t"
"v_fma_f32 v191, -v191, s66, v93\n\t"
"v_fma_f32 v92, v94, s52, v192\n\t"
"v_fma_f32 v93, v95, s52, v193\n\t"
"v_mfma_f32_16x16x32_bf16 v[88:91], v[236:239], a[200:203], v[88:91]\n\t"
"v_fma_f32 v192, -v192, s66, v94\n\t"
"v_fma_f32 v94, v96, s52, v194\n\t"
"v_mfma_f32_16x16x32_bf16 v[88:91], a[252:255], a[196:199], v[88:91]\n\t"
"v_fma_f32 v193, -v193, s66, v95\n\t"
"v_fma_f32 v95, v97, s52, v195\n\t"
"v_mfma_f32_16x16x32_bf16 v[88:91], a[248:251], a[192:195], v[88:91]\n\t"
"ds_read_b128 a[192:195], v244 offset:12288\n\t"
"v_fma_f32 v194, -v194, s66, v96\n\t"
"v_fma_f32 v195, -v195, s66, v97\n\t"
"v_mfma_f32_16x16x32_bf16 v[92:95], a[248:251], a[196:199], v[92:95]\n\t"
"s_waitcnt lgkmcnt(3)\n\t"
"ds_read_b128 a[196:199], v244 offset:12800\n\t"
"v_mfma_f32_16x16x32_bf16 v[92:95], a[252:255], a[200:203], v[92:95]\n\t"
"buffer_load_dwordx4 v[96:99], off, s[80:83], s20 offset:0 sc1\n\t"
"v_mfma_f32_16x16x32_bf16 v[92:95], v[236:239], a[204:207], v[92:95]\n\t"
"buffer_load_dwordx4 a[248:251], off, s[80:83], s20 offset:1024 sc1\n\t"
"v_mfma_f32_16x16x32_bf16 v[224:227], v[224:227], v[220:223], 0\n\t"
"s_waitcnt vmcnt(11)\n\t"
"ds_read_b128 a[200:203], v244 offset:13312\n\t"
"v_mfma_f32_16x16x32_bf16 v[100:103], a[208:211], a[168:171], v[100:103]\n\t"
"ds_read_b128 a[204:207], v244 offset:13824\n\t"
"v_mfma_f32_16x16x32_bf16 v[100:103], v[248:251], a[176:179], v[100:103]\n\t"
"buffer_load_dwordx4 v[236:239], off, s[80:83], s20 offset:2048 sc1\n\t"
"v_mfma_f32_16x16x32_bf16 v[100:103], v[240:243], a[172:175], v[100:103]\n\t"
"s_mov_b32 s20, 356352\n\t"
"v_mfma_f32_16x16x32_bf16 v[104:107], a[208:211], a[172:175], v[104:107]\n\t"
"buffer_load_dwordx4 a[252:255], off, s[80:83], s20 offset:0 sc1\n\t"
"v_mfma_f32_16x16x32_bf16 v[104:107], v[240:243], a[176:179], v[104:107]\n\t"
"s_waitcnt lgkmcnt(5)\n\t"
"v_cvt_pk_bf16_f32 v224, v224, v225\n\t"
"v_mfma_f32_16x16x32_bf16 v[104:107], v[248:251], a[180:183], v[104:107]\n\t"
"s_waitcnt lgkmcnt(0)\n\t"
"s_barrier\n\t"
"v_mfma_f32_16x16x32_bf16 v[108:111], v[248:251], a[184:187], v[108:111]\n\t"
"v_cvt_pk_bf16_f32 v225, v226, v227\n\t"
"v_mfma_f32_16x16x32_bf16 v[108:111], v[240:243], a[180:183], v[108:111]\n\t"
"v_mfma_f32_16x16x32_bf16 v[108:111], a[208:211], a[176:179], v[108:111]\n\t"
"ds_write2st64_b64 v245, v[228:229], v[230:231] offset0:0 offset1:1\n\t"
"v_mfma_f32_16x16x32_bf16 v[112:115], a[208:211], a[180:183], v[112:115]\n\t"
"v_mfma_f32_16x16x32_bf16 v[112:115], v[240:243], a[184:187], v[112:115]\n\t"
"ds_read_b64_tr_b16 v[228:229], v246 offset:32768\n\t"
"v_mfma_f32_16x16x32_bf16 v[112:115], v[248:251], a[188:191], v[112:115]\n\t"
"ds_write2st64_b64 v245, v[232:233], v[234:235] offset0:2 offset1:3\n\t"
"v_mfma_f32_16x16x32_bf16 v[116:119], v[248:251], a[192:195], v[116:119]\n\t"
"v_mfma_f32_16x16x32_bf16 v[116:119], v[240:243], a[188:191], v[116:119]\n\t"
"ds_read_b64_tr_b16 v[230:231], v246 offset:35840\n\t"
"v_mfma_f32_16x16x32_bf16 v[116:119], a[208:211], a[184:187], v[116:119]\n\t"
"s_add_u32 m0, s24, 73088\n\t"
"s_add_u32 s28, s21, 2162688\n\t"
"ds_write_b64 v245, v[224:225] offset:2048\n\t"
"v_mfma_f32_16x16x32_bf16 v[120:123], a[208:211], a[188:191], v[120:123]\n\t"
"ds_read_b64_tr_b16 v[232:233], v246 offset:40448\n\t"
"v_mfma_f32_16x16x32_bf16 v[120:123], v[240:243], a[192:195], v[120:123]\n\t"
"ds_read_b64_tr_b16 v[234:235], v246 offset:43520\n\t"
"v_mfma_f32_16x16x32_bf16 v[120:123], v[248:251], a[196:199], v[120:123]\n\t"
"ds_read_b64_tr_b16 v[224:225], v246 offset:46592\n\t"
"v_mfma_f32_16x16x32_bf16 v[124:127], v[240:243], a[196:199], v[124:127]\n\t"
"s_waitcnt lgkmcnt(4)\n\t"
"ds_read_b64_tr_b16 v[226:227], v246 offset:49664\n\t"
"v_mfma_f32_16x16x32_bf16 v[124:127], a[208:211], a[192:195], v[124:127]\n\t"
"buffer_load_dwordx4 v247, s[12:15], s28 offen offset:128 nt lds\n\t"
"v_mfma_f32_16x16x32_bf16 v[124:127], v[248:251], a[200:203], v[124:127]\n\t"
"s_add_u32 m0, s24, 80768\n\t"
"s_add_u32 s28, s21, 2359296\n\t"
"v_mfma_f32_16x16x32_bf16 v[228:231], v[228:231], v[220:223], 0\n\t"
"s_waitcnt lgkmcnt(2)\n\t"
"buffer_load_dwordx4 v247, s[12:15], s28 offen offset:128 nt lds\n\t"
"s_add_u32 m0, s24, 27008\n\t"
"s_add_u32 s28, s21, 786432\n\t"
"v_mfma_f32_16x16x32_bf16 v[232:235], v[232:235], v[220:223], 0\n\t"
"buffer_load_dwordx4 v247, s[12:15], s28 offen offset:128 nt lds\n\t"
"v_mfma_f32_16x16x32_bf16 v[128:131], v[240:243], a[200:203], v[128:131]\n\t"
"s_waitcnt lgkmcnt(0)\n\t"
"s_add_u32 m0, s24, 86912\n\t"
"s_add_u32 s28, s21, 2555904\n\t"
"v_mfma_f32_16x16x32_bf16 v[128:131], a[208:211], a[196:199], v[128:131]\n\t"
"buffer_load_dwordx4 v247, s[12:15], s28 offen offset:128 nt lds\n\t"
"v_mfma_f32_16x16x32_bf16 v[128:131], v[248:251], a[204:207], v[128:131]\n\t"
"s_waitcnt vmcnt(14)\n\t"
"s_add_u32 m0, s24, 33152\n\t"
"s_add_u32 s28, s21, 983040\n\t"
"v_mfma_f32_16x16x32_bf16 v[224:227], v[224:227], v[220:223], 0\n\t"
"buffer_load_dwordx4 v247, s[12:15], s28 offen offset:128 nt lds\n\t"
"v_mfma_f32_16x16x32_bf16 v[132:135], a[212:215], a[168:171], v[132:135]\n\t"
"s_add_u32 m0, s24, 40832\n\t"
"s_add_u32 s28, s21, 1179648\n\t"
"v_cvt_pk_bf16_f32 v228, v228, v229\n\t"
"v_mfma_f32_16x16x32_bf16 v[132:135], a[220:223], a[176:179], v[132:135]\n\t"
"v_cvt_pk_bf16_f32 v229, v230, v231\n\t"
"v_cvt_pk_bf16_f32 v230, v232, v233\n\t"
"v_mfma_f32_16x16x32_bf16 v[132:135], a[216:219], a[172:175], v[132:135]\n\t"
"buffer_load_dwordx4 v247, s[12:15], s28 offen offset:128 nt lds\n\t"
"v_mfma_f32_16x16x32_bf16 v[136:139], a[212:215], a[172:175], v[136:139]\n\t"
"v_cvt_pk_bf16_f32 v231, v234, v235\n\t"
"v_mfma_f32_16x16x32_bf16 v[136:139], a[216:219], a[176:179], v[136:139]\n\t"
"ds_read_b64_tr_b16 v[232:233], v246 offset:52736\n\t"
"v_cvt_pk_bf16_f32 v224, v224, v225\n\t"
"v_mfma_f32_16x16x32_bf16 v[136:139], a[220:223], a[180:183], v[136:139]\n\t"
"ds_write2st64_b64 v245, v[228:229], v[230:231] offset0:5 offset1:6\n\t"
"v_mfma_f32_16x16x32_bf16 v[140:143], a[220:223], a[184:187], v[140:143]\n\t"
"v_cvt_pk_bf16_f32 v225, v226, v227\n\t"
"v_mfma_f32_16x16x32_bf16 v[140:143], a[216:219], a[180:183], v[140:143]\n\t"
"ds_read_b64_tr_b16 v[234:235], v246 offset:55808\n\t"
"v_mfma_f32_16x16x32_bf16 v[140:143], a[212:215], a[176:179], v[140:143]\n\t"
"v_mfma_f32_16x16x32_bf16 v[144:147], a[212:215], a[180:183], v[144:147]\n\t"
"v_mfma_f32_16x16x32_bf16 v[144:147], a[216:219], a[184:187], v[144:147]\n\t"
"v_mfma_f32_16x16x32_bf16 v[144:147], a[220:223], a[188:191], v[144:147]\n\t"
"v_mfma_f32_16x16x32_bf16 v[148:151], a[216:219], a[188:191], v[148:151]\n\t"
"s_waitcnt lgkmcnt(0)\n\t"
"v_mfma_f32_16x16x32_bf16 v[148:151], a[220:223], a[192:195], v[148:151]\n\t"
"v_mfma_f32_16x16x32_bf16 v[148:151], a[212:215], a[184:187], v[148:151]\n\t"
"v_mfma_f32_16x16x32_bf16 v[232:235], v[232:235], v[220:223], 0\n\t"
"v_mfma_f32_16x16x32_bf16 v[152:155], a[212:215], a[188:191], v[152:155]\n\t"
"v_mfma_f32_16x16x32_bf16 v[152:155], a[220:223], a[196:199], v[152:155]\n\t"
"v_mfma_f32_16x16x32_bf16 v[152:155], a[216:219], a[192:195], v[152:155]\n\t"
"v_mfma_f32_16x16x32_bf16 v[156:159], a[212:215], a[192:195], v[156:159]\n\t"
"v_mfma_f32_16x16x32_bf16 v[156:159], a[216:219], a[196:199], v[156:159]\n\t"
"v_cvt_pk_bf16_f32 v226, v232, v233\n\t"
"v_cvt_pk_bf16_f32 v227, v234, v235\n\t"
"v_mfma_f32_16x16x32_bf16 v[156:159], a[220:223], a[200:203], v[156:159]\n\t"
"ds_write2st64_b64 v245, v[224:225], v[226:227] offset0:7 offset1:8\n\t"
"v_mfma_f32_16x16x32_bf16 v[160:163], a[216:219], a[200:203], v[160:163]\n\t"
"s_waitcnt vmcnt(13)\n\t"
"v_mfma_f32_16x16x32_bf16 v[160:163], a[212:215], a[196:199], v[160:163]\n\t"
"v_mfma_f32_16x16x32_bf16 v[160:163], a[220:223], a[204:207], v[160:163]\n\t"
"v_mfma_f32_16x16x32_bf16 v[164:167], a[224:227], a[168:171], v[164:167]\n\t"
"v_mfma_f32_16x16x32_bf16 v[164:167], a[232:235], a[176:179], v[164:167]\n\t"
"v_mfma_f32_16x16x32_bf16 v[164:167], a[228:231], a[172:175], v[164:167]\n\t"
"s_waitcnt lgkmcnt(0)\n\t"
"s_barrier\n\t"
"v_mfma_f32_16x16x32_bf16 v[168:171], a[224:227], a[172:175], v[168:171]\n\t"
"ds_read_b128 a[168:171], v244 offset:0\n\t"
"v_mfma_f32_16x16x32_bf16 v[168:171], a[228:231], a[176:179], v[168:171]\n\t"
"buffer_load_dwordx4 v[240:243], off, s[80:83], s20 offset:1024 sc1\n\t"
"v_mfma_f32_16x16x32_bf16 v[168:171], a[232:235], a[180:183], v[168:171]\n\t"
"ds_read_b128 a[172:175], v244 offset:512\n\t"
"v_mfma_f32_16x16x32_bf16 v[172:175], a[232:235], a[184:187], v[172:175]\n\t"
"v_mfma_f32_16x16x32_bf16 v[172:175], a[228:231], a[180:183], v[172:175]\n\t"
"v_mfma_f32_16x16x32_bf16 v[172:175], a[224:227], a[176:179], v[172:175]\n\t"
"ds_read_b128 a[176:179], v244 offset:1024\n\t"
"v_mfma_f32_16x16x32_bf16 v[176:179], a[224:227], a[180:183], v[176:179]\n\t"
"ds_read_b128 a[180:183], v244 offset:1536\n\t"
"v_mfma_f32_16x16x32_bf16 v[176:179], a[228:231], a[184:187], v[176:179]\n\t"
"v_mfma_f32_16x16x32_bf16 v[176:179], a[232:235], a[188:191], v[176:179]\n\t"
"v_mfma_f32_16x16x32_bf16 v[180:183], a[232:235], a[192:195], v[180:183]\n\t"
"v_mfma_f32_16x16x32_bf16 v[180:183], a[228:231], a[188:191], v[180:183]\n\t"
"v_mfma_f32_16x16x32_bf16 v[180:183], a[224:227], a[184:187], v[180:183]\n\t"
"ds_read_b128 a[184:187], v244 offset:2048\n\t"
"v_mfma_f32_16x16x32_bf16 v[184:187], a[224:227], a[188:191], v[184:187]\n\t"
"ds_read_b128 a[188:191], v244 offset:2560\n\t"
"v_mfma_f32_16x16x32_bf16 v[184:187], a[228:231], a[192:195], v[184:187]\n\t"
"v_mfma_f32_16x16x32_bf16 v[184:187], a[232:235], a[196:199], v[184:187]\n\t"
"v_mfma_f32_16x16x32_bf16 v[188:191], a[232:235], a[200:203], v[188:191]\n\t"
"v_xor_b32 v246, 0xc0, v246\n\t"
"v_mfma_f32_16x16x32_bf16 v[188:191], a[228:231], a[196:199], v[188:191]\n\t"
"v_mfma_f32_16x16x32_bf16 v[188:191], a[224:227], a[192:195], v[188:191]\n\t"
"ds_read_b128 a[192:195], v244 offset:3072\n\t"
"v_mfma_f32_16x16x32_bf16 v[192:195], a[224:227], a[196:199], v[192:195]\n\t"
"s_waitcnt vmcnt(11) lgkmcnt(4)\n\t"
"ds_read_b128 a[196:199], v244 offset:3584\n\t"
"v_mfma_f32_16x16x32_bf16 v[192:195], a[228:231], a[200:203], v[192:195]\n\t"
"ds_read_b128 a[200:203], v244 offset:4096\n\t"
"v_mfma_f32_16x16x32_bf16 v[192:195], a[232:235], a[204:207], v[192:195]\n\t"
"ds_read_b128 a[204:207], v244 offset:4608\n\t"
"Lst4_%=:\n\t"
"v_mfma_f32_16x16x32_bf16 v[0:3], a[236:239], a[168:171], v[0:3]\n\t"
"s_waitcnt lgkmcnt(6)\n\t"
"v_mfma_f32_16x16x32_bf16 v[0:3], a[244:247], a[176:179], v[0:3]\n\t"
"v_mfma_f32_16x16x32_bf16 v[0:3], a[240:243], a[172:175], v[0:3]\n\t"
"v_mfma_f32_16x16x32_bf16 v[4:7], a[236:239], a[172:175], v[4:7]\n\t"
"s_waitcnt lgkmcnt(5)\n\t"
"v_mfma_f32_16x16x32_bf16 v[4:7], a[240:243], a[176:179], v[4:7]\n\t"
"v_mfma_f32_16x16x32_bf16 v[4:7], a[244:247], a[180:183], v[4:7]\n\t"
"v_mfma_f32_16x16x32_bf16 v[8:11], a[244:247], a[184:187], v[8:11]\n\t"
"s_waitcnt lgkmcnt(4)\n\t"
"v_mfma_f32_16x16x32_bf16 v[8:11], a[240:243], a[180:183], v[8:11]\n\t"
"v_mfma_f32_16x16x32_bf16 v[8:11], a[236:239], a[176:179], v[8:11]\n\t"
"ds_read_b64_tr_b16 v[228:229], v246 offset:512\n\t"
"v_mfma_f32_16x16x32_bf16 v[12:15], a[236:239], a[180:183], v[12:15]\n\t"
"s_waitcnt lgkmcnt(4)\n\t"
"ds_read_b64_tr_b16 v[230:231], v246 offset:3584\n\t"
"v_mfma_f32_16x16x32_bf16 v[12:15], a[240:243], a[184:187], v[12:15]\n\t"
"ds_read_b64_tr_b16 v[232:233], v246 offset:6656\n\t"
"v_mfma_f32_16x16x32_bf16 v[12:15], a[244:247], a[188:191], v[12:15]\n\t"
"buffer_load_dwordx4 a[208:211], off, s[80:83], s20 offset:2048 sc1\n\t"
"v_mfma_f32_16x16x32_bf16 v[16:19], a[244:247], a[192:195], v[16:19]\n\t"
"s_waitcnt lgkmcnt(5)\n\t"
"ds_read_b64_tr_b16 v[234:235], v246 offset:9728\n\t"
"s_mov_b32 s20, 368640\n\t"
"v_mfma_f32_16x16x32_bf16 v[16:19], a[240:243], a[188:191], v[16:19]\n\t"
"v_mfma_f32_16x16x32_bf16 v[16:19], a[236:239], a[184:187], v[16:19]\n\t"
"v_mfma_f32_16x16x32_bf16 v[20:23], a[236:239], a[188:191], v[20:23]\n\t"
"buffer_load_dwordx4 v[248:251], off, s[80:83], s20 offset:0 sc1\n\t"
"v_mfma_f32_16x16x32_bf16 v[20:23], a[240:243], a[192:195], v[20:23]\n\t"
"s_waitcnt lgkmcnt(5)\n\t"
"v_mfma_f32_16x16x32_bf16 v[20:23], a[244:247], a[196:199], v[20:23]\n\t"
"v_mfma_f32_16x16x32_bf16 v[24:27], a[244:247], a[200:203], v[24:27]\n\t"
"buffer_load_dwordx4 a[212:215], off, s[80:83], s20 offset:1024 sc1\n\t"
"v_mfma_f32_16x16x32_bf16 v[24:27], a[240:243], a[196:199], v[24:27]\n\t"
"s_waitcnt lgkmcnt(4)\n\t"
"v_mfma_f32_16x16x32_bf16 v[24:27], a[236:239], a[192:195], v[24:27]\n\t"
"v_mfma_f32_16x16x32_bf16 v[28:31], a[236:239], a[196:199], v[28:31]\n\t"
"s_waitcnt lgkmcnt(2)\n\t"
"v_mfma_f32_16x16x32_bf16 v[28:31], a[240:243], a[200:203], v[28:31]\n\t"
"buffer_load_dwordx4 a[216:219], off, s[80:83], s20 offset:2048 sc1\n\t"
"v_mfma_f32_16x16x32_bf16 v[28:31], a[244:247], a[204:207], v[28:31]\n\t"
"s_waitcnt vmcnt(12) lgkmcnt(0)\n\t"
"s_mov_b32 s20, 380928\n\t"
"v_mfma_f32_16x16x32_bf16 v[228:231], v[228:231], v[220:223], 0\n\t"
"v_mfma_f32_16x16x32_bf16 v[232:235], v[232:235], v[220:223], 0\n\t"
"v_mfma_f32_16x16x32_bf16 v[32:35], v[96:99], a[168:171], v[32:35]\n\t"
"buffer_load_dwordx4 a[220:223], off, s[80:83], s20 offset:0 sc1\n\t"
"v_mfma_f32_16x16x32_bf16 v[32:35], v[236:239], a[176:179], v[32:35]\n\t"
"v_mfma_f32_16x16x32_bf16 v[32:35], a[248:251], a[172:175], v[32:35]\n\t"
"v_mfma_f32_16x16x32_bf16 v[36:39], v[96:99], a[172:175], v[36:39]\n\t"
"v_cvt_pk_bf16_f32 v228, v228, v229\n\t"
"v_cvt_pk_bf16_f32 v229, v230, v231\n\t"
"v_mfma_f32_16x16x32_bf16 v[36:39], a[248:251], a[176:179], v[36:39]\n\t"
"v_cvt_pk_bf16_f32 v230, v232, v233\n\t"
"v_cvt_pk_bf16_f32 v231, v234, v235\n\t"
"v_mfma_f32_16x16x32_bf16 v[36:39], v[236:239], a[180:183], v[36:39]\n\t"
"Lpg4_%=:\n\t"
"s_cbranch_vccnz Lpg8_%=\n\t"
"v_mfma_f32_16x16x32_bf16 v[40:43], v[236:239], a[184:187], v[40:43]\n\t"
"buffer_load_dwordx4 a[224:227], off, s[80:83], s20 offset:1024 sc1\n\t"
"v_mfma_f32_16x16x32_bf16 v[40:43], a[248:251], a[180:183], v[40:43]\n\t"
"v_mfma_f32_16x16x32_bf16 v[40:43], v[96:99], a[176:179], v[40:43]\n\t"
"buffer_load_dwordx4 a[228:231], off, s[80:83], s20 offset:2048 sc1\n\t"
"v_mfma_f32_16x16x32_bf16 v[44:47], v[96:99], a[180:183], v[44:47]\n\t"
"s_mov_b32 s20, 393216\n\t"
"ds_read_b64_tr_b16 v[224:225], v246 offset:12800\n\t"
"v_mfma_f32_16x16x32_bf16 v[44:47], a[248:251], a[184:187], v[44:47]\n\t"
"ds_read_b64_tr_b16 v[226:227], v246 offset:15872\n\t"
"v_mfma_f32_16x16x32_bf16 v[44:47], v[236:239], a[188:191], v[44:47]\n\t"
"ds_read_b64_tr_b16 v[232:233], v246 offset:20480\n\t"
"v_mfma_f32_16x16x32_bf16 v[48:51], v[236:239], a[192:195], v[48:51]\n\t"
"buffer_load_dwordx4 a[232:235], off, s[80:83], s20 offset:0 sc1\n\t"
"v_mfma_f32_16x16x32_bf16 v[48:51], a[248:251], a[188:191], v[48:51]\n\t"
"ds_read_b64_tr_b16 v[234:235], v246 offset:23552\n\t"
"v_mfma_f32_16x16x32_bf16 v[48:51], v[96:99], a[184:187], v[48:51]\n\t"
"v_mfma_f32_16x16x32_bf16 v[52:55], v[96:99], a[188:191], v[52:55]\n\t"
"v_mfma_f32_16x16x32_bf16 v[52:55], a[248:251], a[192:195], v[52:55]\n\t"
"buffer_load_dwordx4 a[236:239], off, s[80:83], s20 offset:1024 sc1\n\t"
"v_mfma_f32_16x16x32_bf16 v[52:55], v[236:239], a[196:199], v[52:55]\n\t"
"v_mfma_f32_16x16x32_bf16 v[56:59], v[236:239], a[200:203], v[56:59]\n\t"
"v_mfma_f32_16x16x32_bf16 v[56:59], a[248:251], a[196:199], v[56:59]\n\t"
"v_mfma_f32_16x16x32_bf16 v[56:59], v[96:99], a[192:195], v[56:59]\n\t"
"buffer_load_dwordx4 a[240:243], off, s[80:83], s20 offset:2048 sc1\n\t"
"v_mfma_f32_16x16x32_bf16 v[60:63], v[96:99], a[196:199], v[60:63]\n\t"
"s_waitcnt lgkmcnt(2)\n\t"
"s_mov_b32 s20, 405504\n\t"
"v_mfma_f32_16x16x32_bf16 v[60:63], a[248:251], a[200:203], v[60:63]\n\t"
"s_waitcnt lgkmcnt(0)\n\t"
"v_mfma_f32_16x16x32_bf16 v[60:63], v[236:239], a[204:207], v[60:63]\n\t"
"s_waitcnt vmcnt(9)\n\t"
"v_mfma_f32_16x16x32_bf16 v[224:227], v[224:227], v[220:223], 0\n\t"
"buffer_load_dwordx4 v[236:239], off, s[80:83], s20 offset:0 sc1\n\t"
"v_mfma_f32_16x16x32_bf16 v[232:235], v[232:235], v[220:223], 0\n\t"
"v_mfma_f32_16x16x32_bf16 v[64:67], a[252:255], a[168:171], v[64:67]\n\t"
"ds_read_b128 a[168:171], v244 offset:9216\n\t"
"v_mfma_f32_16x16x32_bf16 v[64:67], a[208:211], a[176:179], v[64:67]\n\t"
"buffer_load_dwordx4 v[96:99], off, s[80:83], s20 offset:1024 sc1\n\t"
"v_mfma_f32_16x16x32_bf16 v[64:67], v[240:243], a[172:175], v[64:67]\n\t"
"v_mfma_f32_16x16x32_bf16 v[68:71], a[252:255], a[172:175], v[68:71]\n\t"
"ds_read_b128 a[172:175], v244 offset:9728\n\t"
"v_cvt_pk_bf16_f32 v224, v224, v225\n\t"
"v_mfma_f32_16x16x32_bf16 v[68:71], v[240:243], a[176:179], v[68:71]\n\t"
"v_cvt_pk_bf16_f32 v225, v226, v227\n\t"
"v_cvt_pk_bf16_f32 v226, v232, v233\n\t"
"v_mfma_f32_16x16x32_bf16 v[68:71], a[208:211], a[180:183], v[68:71]\n\t"
"v_cvt_pk_bf16_f32 v227, v234, v235\n\t"
"v_mfma_f32_16x16x32_bf16 v[72:75], a[208:211], a[184:187], v[72:75]\n\t"
"buffer_load_dwordx4 a[244:247], off, s[80:83], s20 offset:2048 sc1\n\t"
"v_mfma_f32_16x16x32_bf16 v[72:75], v[240:243], a[180:183], v[72:75]\n\t"
"s_mov_b32 s20, 417792\n\t"
"v_mfma_f32_16x16x32_bf16 v[72:75], a[252:255], a[176:179], v[72:75]\n\t"
"buffer_load_dwordx4 a[248:251], off, s[80:83], s20 offset:0 sc1\n\t"
"v_mfma_f32_16x16x32_bf16 v[76:79], a[252:255], a[180:183], v[76:79]\n\t"
"ds_read_b128 a[176:179], v244 offset:10240\n\t"
"v_mfma_f32_16x16x32_bf16 v[76:79], v[240:243], a[184:187], v[76:79]\n\t"
"ds_read_b128 a[180:183], v244 offset:10752\n\t"
"v_mfma_f32_16x16x32_bf16 v[76:79], a[208:211], a[188:191], v[76:79]\n\t"
"ds_read_b64_tr_b16 v[232:233], v246 offset:26624\n\t"
"v_mfma_f32_16x16x32_bf16 v[80:83], a[208:211], a[192:195], v[80:83]\n\t"
"ds_read_b64_tr_b16 v[234:235], v246 offset:29696\n\t"
"v_mfma_f32_16x16x32_bf16 v[80:83], v[240:243], a[188:191], v[80:83]\n\t"
"v_mfma_f32_16x16x32_bf16 v[80:83], a[252:255], a[184:187], v[80:83]\n\t"
"ds_read_b128 a[184:187], v244 offset:11264\n\t"
"v_mfma_f32_16x16x32_bf16 v[84:87], a[252:255], a[188:191], v[84:87]\n\t"
"ds_read_b128 a[188:191], v244 offset:11776\n\t"
"v_mfma_f32_16x16x32_bf16 v[84:87], v[240:243], a[192:195], v[84:87]\n\t"
"v_mfma_f32_16x16x32_bf16 v[84:87], a[208:211], a[196:199], v[84:87]\n\t"
"v_mfma_f32_16x16x32_bf16 v[88:91], a[208:211], a[200:203], v[88:91]\n\t"
"v_mfma_f32_16x16x32_bf16 v[88:91], v[240:243], a[196:199], v[88:91]\n\t"
"v_mfma_f32_16x16x32_bf16 v[88:91], a[252:255], a[192:195], v[88:91]\n\t"
"ds_read_b128 a[192:195], v244 offset:12288\n\t"
"v_mfma_f32_16x16x32_bf16 v[92:95], a[252:255], a[196:199], v[92:95]\n\t"
"buffer_load_dwordx4 a[252:255], off, s[80:83], s20 offset:1024 sc1\n\t"
"v_mfma_f32_16x16x32_bf16 v[92:95], v[240:243], a[200:203], v[92:95]\n\t"
"s_waitcnt vmcnt(11) lgkmcnt(3)\n\t"
"ds_read_b128 a[196:199], v244 offset:12800\n\t"
"v_mfma_f32_16x16x32_bf16 v[92:95], a[208:211], a[204:207], v[92:95]\n\t"
"ds_read_b128 a[200:203], v244 offset:13312\n\t"
"v_mfma_f32_16x16x32_bf16 v[232:235], v[232:235], v[220:223], 0\n\t"
"ds_read_b128 a[204:207], v244 offset:13824\n\t"
"v_mfma_f32_16x16x32_bf16 v[100:103], v[248:251], a[168:171], v[100:103]\n\t"
"buffer_load_dwordx4 v[240:243], off, s[80:83], s20 offset:2048 sc1\n\t"
"v_mfma_f32_16x16x32_bf16 v[100:103], a[216:219], a[176:179], v[100:103]\n\t"
"s_mov_b32 s20, 430080\n\t"
"v_mfma_f32_16x16x32_bf16 v[100:103], a[212:215], a[172:175], v[100:103]\n\t"
"buffer_load_dwordx4 a[208:211], off, s[80:83], s20 offset:0 sc1\n\t"
"v_mfma_f32_16x16x32_bf16 v[104:107], v[248:251], a[172:175], v[104:107]\n\t"
"s_waitcnt lgkmcnt(5)\n\t"
"v_mfma_f32_16x16x32_bf16 v[104:107], a[212:215], a[176:179], v[104:107]\n\t"
"v_cvt_pk_bf16_f32 v232, v232, v233\n\t"
"v_mfma_f32_16x16x32_bf16 v[104:107], a[216:219], a[180:183], v[104:107]\n\t"
"s_waitcnt lgkmcnt(0)\n\t"
"s_barrier\n\t"
"v_mfma_f32_16x16x32_bf16 v[108:111], a[216:219], a[184:187], v[108:111]\n\t"
"v_cvt_pk_bf16_f32 v233, v234, v235\n\t"
"v_mfma_f32_16x16x32_bf16 v[108:111], a[212:215], a[180:183], v[108:111]\n\t"
"v_mfma_f32_16x16x32_bf16 v[108:111], v[248:251], a[176:179], v[108:111]\n\t"
"ds_write2st64_b64 v245, v[228:229], v[230:231] offset0:0 offset1:1\n\t"
"v_mfma_f32_16x16x32_bf16 v[112:115], v[248:251], a[180:183], v[112:115]\n\t"
"v_mfma_f32_16x16x32_bf16 v[112:115], a[212:215], a[184:187], v[112:115]\n\t"
"ds_read_b64_tr_b16 v[228:229], v246 offset:32768\n\t"
"v_mfma_f32_16x16x32_bf16 v[112:115], a[216:219], a[188:191], v[112:115]\n\t"
"ds_write2st64_b64 v245, v[224:225], v[226:227] offset0:2 offset1:3\n\t"
"v_mfma_f32_16x16x32_bf16 v[116:119], a[216:219], a[192:195], v[116:119]\n\t"
"v_mfma_f32_16x16x32_bf16 v[116:119], a[212:215], a[188:191], v[116:119]\n\t"
"ds_read_b64_tr_b16 v[230:231], v246 offset:35840\n\t"
"v_mfma_f32_16x16x32_bf16 v[116:119], v[248:251], a[184:187], v[116:119]\n\t"
"ds_write_b64 v245, v[232:233] offset:2048\n\t"
"v_mfma_f32_16x16x32_bf16 v[120:123], v[248:251], a[188:191], v[120:123]\n\t"
"ds_read_b64_tr_b16 v[224:225], v246 offset:40448\n\t"
"v_mfma_f32_16x16x32_bf16 v[120:123], a[212:215], a[192:195], v[120:123]\n\t"
"ds_read_b64_tr_b16 v[226:227], v246 offset:43520\n\t"
"v_mfma_f32_16x16x32_bf16 v[120:123], a[216:219], a[196:199], v[120:123]\n\t"
"ds_read_b64_tr_b16 v[232:233], v246 offset:46592\n\t"
"v_mfma_f32_16x16x32_bf16 v[124:127], a[212:215], a[196:199], v[124:127]\n\t"
"s_waitcnt lgkmcnt(4)\n\t"
"ds_read_b64_tr_b16 v[234:235], v246 offset:49664\n\t"
"v_mfma_f32_16x16x32_bf16 v[124:127], v[248:251], a[192:195], v[124:127]\n\t"
"v_mfma_f32_16x16x32_bf16 v[124:127], a[216:219], a[200:203], v[124:127]\n\t"
"v_mfma_f32_16x16x32_bf16 v[228:231], v[228:231], v[220:223], 0\n\t"
"s_waitcnt lgkmcnt(2)\n\t"
"v_mfma_f32_16x16x32_bf16 v[224:227], v[224:227], v[220:223], 0\n\t"
"v_mfma_f32_16x16x32_bf16 v[128:131], a[212:215], a[200:203], v[128:131]\n\t"
"buffer_load_dwordx4 a[212:215], off, s[80:83], s20 offset:1024 sc1\n\t"
"v_mfma_f32_16x16x32_bf16 v[128:131], v[248:251], a[196:199], v[128:131]\n\t"
"s_waitcnt vmcnt(11) lgkmcnt(0)\n\t"
"v_mfma_f32_16x16x32_bf16 v[128:131], a[216:219], a[204:207], v[128:131]\n\t"
"v_mfma_f32_16x16x32_bf16 v[232:235], v[232:235], v[220:223], 0\n\t"
"buffer_load_dwordx4 v[248:251], off, s[80:83], s20 offset:2048 sc1\n\t"
"v_mfma_f32_16x16x32_bf16 v[132:135], a[220:223], a[168:171], v[132:135]\n\t"
"s_mov_b32 s20, 442368\n\t"
"v_cvt_pk_bf16_f32 v228, v228, v229\n\t"
"v_cvt_pk_bf16_f32 v229, v230, v231\n\t"
"v_mfma_f32_16x16x32_bf16 v[132:135], a[228:231], a[176:179], v[132:135]\n\t"
"s_add_u32 m0, s24, 46976\n\t"
"s_add_u32 s28, s21, 1376256\n\t"
"v_cvt_pk_bf16_f32 v230, v224, v225\n\t"
"v_mfma_f32_16x16x32_bf16 v[132:135], a[224:227], a[172:175], v[132:135]\n\t"
"v_cvt_pk_bf16_f32 v231, v226, v227\n\t"
"v_mfma_f32_16x16x32_bf16 v[136:139], a[220:223], a[172:175], v[136:139]\n\t"
"buffer_load_dwordx4 v247, s[12:15], s28 offen offset:128 nt lds\n\t"
"v_mfma_f32_16x16x32_bf16 v[136:139], a[224:227], a[176:179], v[136:139]\n\t"
"buffer_load_dwordx4 a[216:219], off, s[80:83], s20 offset:0 sc1\n\t"
"v_mfma_f32_16x16x32_bf16 v[136:139], a[228:231], a[180:183], v[136:139]\n\t"
"s_add_u32 m0, s24, 53120\n\t"
"s_add_u32 s28, s21, 1572864\n\t"
"ds_write2st64_b64 v245, v[228:229], v[230:231] offset0:5 offset1:6\n\t"
"v_mfma_f32_16x16x32_bf16 v[140:143], a[228:231], a[184:187], v[140:143]\n\t"
"buffer_load_dwordx4 v247, s[12:15], s28 offen offset:128 nt lds\n\t"
"v_mfma_f32_16x16x32_bf16 v[140:143], a[224:227], a[180:183], v[140:143]\n\t"
"s_add_u32 m0, s24, 93056\n\t"
"s_add_u32 s28, s21, 2752512\n\t"
"ds_read_b64_tr_b16 v[224:225], v246 offset:52736\n\t"
"v_mfma_f32_16x16x32_bf16 v[140:143], a[220:223], a[176:179], v[140:143]\n\t"
"ds_read_b64_tr_b16 v[226:227], v246 offset:55808\n\t"
"v_cvt_pk_bf16_f32 v232, v232, v233\n\t"
"v_cvt_pk_bf16_f32 v233, v234, v235\n\t"
"v_mfma_f32_16x16x32_bf16 v[144:147], a[220:223], a[180:183], v[144:147]\n\t"
"v_mfma_f32_16x16x32_bf16 v[144:147], a[224:227], a[184:187], v[144:147]\n\t"
"buffer_load_dwordx4 v247, s[12:15], s28 offen offset:128 nt lds\n\t"
"v_mfma_f32_16x16x32_bf16 v[144:147], a[228:231], a[188:191], v[144:147]\n\t"
"s_add_u32 m0, s24, 100736\n\t"
"s_add_u32 s28, s21, 2949120\n\t"
"v_mfma_f32_16x16x32_bf16 v[148:151], a[224:227], a[188:191], v[148:151]\n\t"
"buffer_load_dwordx4 v247, s[12:15], s28 offen offset:128 nt lds\n\t"
"v_mfma_f32_16x16x32_bf16 v[148:151], a[228:231], a[192:195], v[148:151]\n\t"
"s_waitcnt lgkmcnt(0)\n\t"
"s_add_u32 m0, s24, 106880\n\t"
"s_add_u32 s28, s21, 3145728\n\t"
"v_mfma_f32_16x16x32_bf16 v[148:151], a[220:223], a[184:187], v[148:151]\n\t"
"buffer_load_dwordx4 v247, s[12:15], s28 offen offset:128 nt lds\n\t"
"s_add_u32 m0, s24, 113024\n\t"
"s_add_u32 s28, s21, 3342336\n\t"
"v_mfma_f32_16x16x32_bf16 v[224:227], v[224:227], v[220:223], 0\n\t"
"buffer_load_dwordx4 v247, s[12:15], s28 offen offset:128 nt lds\n\t"
"s_add_u32 m0, s25, 896\n\t"
"v_mfma_f32_16x16x32_bf16 v[152:155], a[220:223], a[188:191], v[152:155]\n\t"
"buffer_load_dwordx4 v254, s[12:15], 0 offen offset:128 nt lds\n\t"
"v_mfma_f32_16x16x32_bf16 v[152:155], a[228:231], a[196:199], v[152:155]\n\t"
"s_add_u32 m0, s26, 896\n\t"
"v_mfma_f32_16x16x32_bf16 v[152:155], a[224:227], a[192:195], v[152:155]\n\t"
"s_mov_b64 exec, s[36:37]\n\t"
"buffer_load_dwordx4 v254, s[12:15], s27 offen offset:128 nt lds\n\t"
"s_mov_b64 exec, -1\n\t"
"v_mfma_f32_16x16x32_bf16 v[156:159], a[220:223], a[192:195], v[156:159]\n\t"
"v_mfma_f32_16x16x32_bf16 v[156:159], a[224:227], a[196:199], v[156:159]\n\t"
"v_cvt_pk_bf16_f32 v234, v224, v225\n\t"
"v_cvt_pk_bf16_f32 v235, v226, v227\n\t"
"v_mfma_f32_16x16x32_bf16 v[156:159], a[228:231], a[200:203], v[156:159]\n\t"
"ds_write2st64_b64 v245, v[232:233], v[234:235] offset0:7 offset1:8\n\t"
"v_mfma_f32_16x16x32_bf16 v[160:163], a[224:227], a[200:203], v[160:163]\n\t"
"s_waitcnt vmcnt(18)\n\t"
"v_mfma_f32_16x16x32_bf16 v[160:163], a[220:223], a[196:199], v[160:163]\n\t"
"v_mfma_f32_16x16x32_bf16 v[160:163], a[228:231], a[204:207], v[160:163]\n\t"
"v_mfma_f32_16x16x32_bf16 v[164:167], a[232:235], a[168:171], v[164:167]\n\t"
"v_mfma_f32_16x16x32_bf16 v[164:167], a[240:243], a[176:179], v[164:167]\n\t"
"v_mfma_f32_16x16x32_bf16 v[164:167], a[236:239], a[172:175], v[164:167]\n\t"
"s_waitcnt lgkmcnt(0)\n\t"
"s_barrier\n\t"
"v_mfma_f32_16x16x32_bf16 v[168:171], a[232:235], a[172:175], v[168:171]\n\t"
"ds_read_b128 a[168:171], v244 offset:0\n\t"
"v_mfma_f32_16x16x32_bf16 v[168:171], a[236:239], a[176:179], v[168:171]\n\t"
"ds_read_b128 a[172:175], v244 offset:512\n\t"
"v_mfma_f32_16x16x32_bf16 v[168:171], a[240:243], a[180:183], v[168:171]\n\t"
"v_mfma_f32_16x16x32_bf16 v[172:175], a[240:243], a[184:187], v[172:175]\n\t"
"v_mfma_f32_16x16x32_bf16 v[172:175], a[236:239], a[180:183], v[172:175]\n\t"
"v_mfma_f32_16x16x32_bf16 v[172:175], a[232:235], a[176:179], v[172:175]\n\t"
"ds_read_b128 a[176:179], v244 offset:1024\n\t"
"v_mfma_f32_16x16x32_bf16 v[176:179], a[232:235], a[180:183], v[176:179]\n\t"
"ds_read_b128 a[180:183], v244 offset:1536\n\t"
"v_mfma_f32_16x16x32_bf16 v[176:179], a[236:239], a[184:187], v[176:179]\n\t"
"v_mfma_f32_16x16x32_bf16 v[176:179], a[240:243], a[188:191], v[176:179]\n\t"
"v_mfma_f32_16x16x32_bf16 v[180:183], a[240:243], a[192:195], v[180:183]\n\t"
"v_mfma_f32_16x16x32_bf16 v[180:183], a[236:239], a[188:191], v[180:183]\n\t"
"v_mfma_f32_16x16x32_bf16 v[180:183], a[232:235], a[184:187], v[180:183]\n\t"
"ds_read_b128 a[184:187], v244 offset:2048\n\t"
"v_mfma_f32_16x16x32_bf16 v[184:187], a[232:235], a[188:191], v[184:187]\n\t"
"ds_read_b128 a[188:191], v244 offset:2560\n\t"
"v_mfma_f32_16x16x32_bf16 v[184:187], a[236:239], a[192:195], v[184:187]\n\t"
"v_mfma_f32_16x16x32_bf16 v[184:187], a[240:243], a[196:199], v[184:187]\n\t"
"v_mfma_f32_16x16x32_bf16 v[188:191], a[240:243], a[200:203], v[188:191]\n\t"
"v_mfma_f32_16x16x32_bf16 v[188:191], a[236:239], a[196:199], v[188:191]\n\t"
"v_mfma_f32_16x16x32_bf16 v[188:191], a[232:235], a[192:195], v[188:191]\n\t"
"ds_read_b128 a[192:195], v244 offset:3072\n\t"
"v_mfma_f32_16x16x32_bf16 v[192:195], a[232:235], a[196:199], v[192:195]\n\t"
"s_waitcnt vmcnt(15) lgkmcnt(4)\n\t"
"ds_read_b128 a[196:199], v244 offset:3584\n\t"
"v_mfma_f32_16x16x32_bf16 v[192:195], a[236:239], a[200:203], v[192:195]\n\t"
"ds_read_b128 a[200:203], v244 offset:4096\n\t"
"v_mfma_f32_16x16x32_bf16 v[192:195], a[240:243], a[204:207], v[192:195]\n\t"
"ds_read_b128 a[204:207], v244 offset:4608\n\t"
"Lst5_%=:\n\t"
"v_mfma_f32_16x16x32_bf16 v[196:199], v[236:239], a[168:171], v[196:199]\n\t"
"s_waitcnt lgkmcnt(6)\n\t"
"v_mfma_f32_16x16x32_bf16 v[196:199], a[244:247], a[176:179], v[196:199]\n\t"
"v_mfma_f32_16x16x32_bf16 v[196:199], v[96:99], a[172:175], v[196:199]\n\t"
"v_mfma_f32_16x16x32_bf16 v[204:207], v[236:239], a[172:175], v[204:207]\n\t"
"s_waitcnt lgkmcnt(5)\n\t"
"v_mfma_f32_16x16x32_bf16 v[204:207], v[96:99], a[176:179], v[204:207]\n\t"
"v_mfma_f32_16x16x32_bf16 v[204:207], a[244:247], a[180:183], v[204:207]\n\t"
"v_mfma_f32_16x16x32_bf16 v[212:215], a[244:247], a[184:187], v[212:215]\n\t"
"s_waitcnt lgkmcnt(4)\n\t"
"v_mfma_f32_16x16x32_bf16 v[212:215], v[96:99], a[180:183], v[212:215]\n\t"
"v_mfma_f32_16x16x32_bf16 v[212:215], v[236:239], a[176:179], v[212:215]\n\t"
"ds_read_b64_tr_b16 v[228:229], v246 offset:1024\n\t"
"v_mfma_f32_16x16x32_bf16 a[0:3], v[236:239], a[180:183], a[0:3]\n\t"
"s_waitcnt lgkmcnt(4)\n\t"
"ds_read_b64_tr_b16 v[230:231], v246 offset:4096\n\t"
"v_mfma_f32_16x16x32_bf16 a[0:3], v[96:99], a[184:187], a[0:3]\n\t"
"buffer_load_dwordx4 a[220:223], off, s[80:83], s20 offset:1024 sc1\n\t"
"v_mfma_f32_16x16x32_bf16 a[0:3], a[244:247], a[188:191], a[0:3]\n\t"
"ds_read_b64_tr_b16 v[224:225], v246 offset:7168\n\t"
"v_mfma_f32_16x16x32_bf16 a[8:11], a[244:247], a[192:195], a[8:11]\n\t"
"s_waitcnt lgkmcnt(5)\n\t"
"ds_read_b64_tr_b16 v[226:227], v246 offset:10240\n\t"
"v_mfma_f32_16x16x32_bf16 a[8:11], v[96:99], a[188:191], a[8:11]\n\t"
"v_mfma_f32_16x16x32_bf16 a[8:11], v[236:239], a[184:187], a[8:11]\n\t"
"v_mfma_f32_16x16x32_bf16 a[16:19], v[236:239], a[188:191], a[16:19]\n\t"
"s_waitcnt lgkmcnt(5)\n\t"
"v_mfma_f32_16x16x32_bf16 a[16:19], v[96:99], a[192:195], a[16:19]\n\t"
"v_mfma_f32_16x16x32_bf16 a[16:19], a[244:247], a[196:199], a[16:19]\n\t"
"v_mfma_f32_16x16x32_bf16 a[24:27], a[244:247], a[200:203], a[24:27]\n\t"
"s_waitcnt lgkmcnt(4)\n\t"
"v_mfma_f32_16x16x32_bf16 a[24:27], v[96:99], a[196:199], a[24:27]\n\t"
"v_mfma_f32_16x16x32_bf16 a[24:27], v[236:239], a[192:195], a[24:27]\n\t"
"v_mfma_f32_16x16x32_bf16 a[32:35], v[236:239], a[196:199], a[32:35]\n\t"
"s_waitcnt lgkmcnt(2)\n\t"
"v_mfma_f32_16x16x32_bf16 a[32:35], v[96:99], a[200:203], a[32:35]\n\t"
"s_waitcnt lgkmcnt(0)\n\t"
"v_mfma_f32_16x16x32_bf16 a[32:35], a[244:247], a[204:207], a[32:35]\n\t"
"s_waitcnt vmcnt(13)\n\t"
"v_mfma_f32_16x16x32_bf16 v[228:231], v[228:231], v[220:223], 0\n\t"
"v_mfma_f32_16x16x32_bf16 v[224:227], v[224:227], v[220:223], 0\n\t"
"v_mfma_f32_16x16x32_bf16 a[40:43], a[248:251], a[168:171], a[40:43]\n\t"
"v_mfma_f32_16x16x32_bf16 a[40:43], v[240:243], a[176:179], a[40:43]\n\t"
"v_mfma_f32_16x16x32_bf16 a[40:43], a[252:255], a[172:175], a[40:43]\n\t"
"v_mfma_f32_16x16x32_bf16 a[48:51], a[248:251], a[172:175], a[48:51]\n\t"
"v_cvt_pk_bf16_f32 v228, v228, v229\n\t"
"v_cvt_pk_bf16_f32 v229, v230, v231\n\t"
"v_mfma_f32_16x16x32_bf16 a[48:51], a[252:255], a[176:179], a[48:51]\n\t"
"v_cvt_pk_bf16_f32 v230, v224, v225\n\t"
"v_cvt_pk_bf16_f32 v231, v226, v227\n\t"
"v_mfma_f32_16x16x32_bf16 a[48:51], v[240:243], a[180:183], a[48:51]\n\t"
"v_mfma_f32_16x16x32_bf16 a[56:59], v[240:243], a[184:187], a[56:59]\n\t"
"v_mfma_f32_16x16x32_bf16 a[56:59], a[252:255], a[180:183], a[56:59]\n\t"
"v_mfma_f32_16x16x32_bf16 a[56:59], a[248:251], a[176:179], a[56:59]\n\t"
"ds_read_b64_tr_b16 v[232:233], v246 offset:13312\n\t"
"v_mfma_f32_16x16x32_bf16 a[64:67], a[248:251], a[180:183], a[64:67]\n\t"
"ds_read_b64_tr_b16 v[234:235], v246 offset:16384\n\t"
"v_mfma_f32_16x16x32_bf16 a[64:67], a[252:255], a[184:187], a[64:67]\n\t"
"ds_read_b64_tr_b16 v[224:225], v246 offset:20992\n\t"
"v_mfma_f32_16x16x32_bf16 a[64:67], v[240:243], a[188:191], a[64:67]\n\t"
"buffer_load_dwordx4 v[236:239], off, s[80:83], s20 offset:2048 sc1\n\t"
"v_mfma_f32_16x16x32_bf16 a[72:75], v[240:243], a[192:195], a[72:75]\n\t"
"s_mov_b32 s20, 454656\n\t"
"ds_read_b64_tr_b16 v[226:227], v246 offset:24064\n\t"
"v_mfma_f32_16x16x32_bf16 a[72:75], a[252:255], a[188:191], a[72:75]\n\t"
"v_mfma_f32_16x16x32_bf16 a[72:75], a[248:251], a[184:187], a[72:75]\n\t"
"v_mfma_f32_16x16x32_bf16 a[80:83], a[248:251], a[188:191], a[80:83]\n\t"
"buffer_load_dwordx4 v[96:99], off, s[80:83], s20 offset:0 sc1\n\t"
"v_mfma_f32_16x16x32_bf16 a[80:83], a[252:255], a[192:195], a[80:83]\n\t"
"v_mfma_f32_16x16x32_bf16 a[80:83], v[240:243], a[196:199], a[80:83]\n\t"
"v_mfma_f32_16x16x32_bf16 a[88:91], v[240:243], a[200:203], a[88:91]\n\t"
"v_mfma_f32_16x16x32_bf16 a[88:91], a[252:255], a[196:199], a[88:91]\n\t"
"buffer_load_dwordx4 a[224:227], off, s[80:83], s20 offset:1024 sc1\n\t"
"v_mfma_f32_16x16x32_bf16 a[88:91], a[248:251], a[192:195], a[88:91]\n\t"
"v_mfma_f32_16x16x32_bf16 a[96:99], a[248:251], a[196:199], a[96:99]\n\t"
"s_waitcnt lgkmcnt(2)\n\t"
"v_mfma_f32_16x16x32_bf16 a[96:99], a[252:255], a[200:203], a[96:99]\n\t"
"s_waitcnt lgkmcnt(0)\n\t"
"v_mfma_f32_16x16x32_bf16 a[96:99], v[240:243], a[204:207], a[96:99]\n\t"
"buffer_load_dwordx4 a[228:231], off, s[80:83], s20 offset:2048 sc1\n\t"
"v_mfma_f32_16x16x32_bf16 v[232:235], v[232:235], v[220:223], 0\n\t"
"s_waitcnt vmcnt(14)\n\t"
"s_mov_b32 s20, 466944\n\t"
"v_mfma_f32_16x16x32_bf16 v[224:227], v[224:227], v[220:223], 0\n\t"
"v_mfma_f32_16x16x32_bf16 a[104:107], a[208:211], a[168:171], a[104:107]\n\t"
"ds_read_b128 a[168:171], v244 offset:9216\n\t"
"v_mfma_f32_16x16x32_bf16 a[104:107], v[248:251], a[176:179], a[104:107]\n\t"
"buffer_load_dwordx4 a[232:235], off, s[80:83], s20 offset:0 sc1\n\t"
"v_mfma_f32_16x16x32_bf16 a[104:107], a[212:215], a[172:175], a[104:107]\n\t"
"v_mfma_f32_16x16x32_bf16 a[112:115], a[208:211], a[172:175], a[112:115]\n\t"
"ds_read_b128 a[172:175], v244 offset:9728\n\t"
"v_cvt_pk_bf16_f32 v232, v232, v233\n\t"
"v_mfma_f32_16x16x32_bf16 a[112:115], a[212:215], a[176:179], a[112:115]\n\t"
"v_cvt_pk_bf16_f32 v233, v234, v235\n\t"
"v_cvt_pk_bf16_f32 v234, v224, v225\n\t"
"v_mfma_f32_16x16x32_bf16 a[112:115], v[248:251], a[180:183], a[112:115]\n\t"
"v_cvt_pk_bf16_f32 v235, v226, v227\n\t"
"v_mfma_f32_16x16x32_bf16 a[120:123], v[248:251], a[184:187], a[120:123]\n\t"
"buffer_load_dwordx4 a[236:239], off, s[80:83], s20 offset:1024 sc1\n\t"
"v_mfma_f32_16x16x32_bf16 a[120:123], a[212:215], a[180:183], a[120:123]\n\t"
"v_mfma_f32_16x16x32_bf16 a[120:123], a[208:211], a[176:179], a[120:123]\n\t"
"ds_read_b128 a[176:179], v244 offset:10240\n\t"
"v_mfma_f32_16x16x32_bf16 a[128:131], a[208:211], a[180:183], a[128:131]\n\t"
"buffer_load_dwordx4 a[240:243], off, s[80:83], s20 offset:2048 sc1\n\t"
"v_mfma_f32_16x16x32_bf16 a[128:131], a[212:215], a[184:187], a[128:131]\n\t"
"s_mov_b32 s20, 700416\n\t"
"ds_read_b128 a[180:183], v244 offset:10752\n\t"
"v_mfma_f32_16x16x32_bf16 a[128:131], v[248:251], a[188:191], a[128:131]\n\t"
"ds_read_b64_tr_b16 v[224:225], v246 offset:27136\n\t"
"v_mfma_f32_16x16x32_bf16 a[136:139], v[248:251], a[192:195], a[136:139]\n\t"
"ds_read_b64_tr_b16 v[226:227], v246 offset:30208\n\t"
"v_mfma_f32_16x16x32_bf16 a[136:139], a[212:215], a[188:191], a[136:139]\n\t"
"buffer_load_dwordx4 a[244:247], off, s[80:83], s20 offset:0 sc1\n\t"
"v_mfma_f32_16x16x32_bf16 a[136:139], a[208:211], a[184:187], a[136:139]\n\t"
"ds_read_b128 a[184:187], v244 offset:11264\n\t"
"v_mfma_f32_16x16x32_bf16 a[144:147], a[208:211], a[188:191], a[144:147]\n\t"
"ds_read_b128 a[188:191], v244 offset:11776\n\t"
"v_mfma_f32_16x16x32_bf16 a[144:147], a[212:215], a[192:195], a[144:147]\n\t"
"v_mfma_f32_16x16x32_bf16 a[144:147], v[248:251], a[196:199], a[144:147]\n\t"
"buffer_load_dwordx4 a[248:251], off, s[80:83], s20 offset:1024 sc1\n\t"
"v_mfma_f32_16x16x32_bf16 a[152:155], v[248:251], a[200:203], a[152:155]\n\t"
"v_mfma_f32_16x16x32_bf16 a[152:155], a[212:215], a[196:199], a[152:155]\n\t"
"v_mfma_f32_16x16x32_bf16 a[152:155], a[208:211], a[192:195], a[152:155]\n\t"
"ds_read_b128 a[192:195], v244 offset:12288\n\t"
"v_mfma_f32_16x16x32_bf16 a[160:163], a[208:211], a[196:199], a[160:163]\n\t"
"buffer_load_dwordx4 a[252:255], off, s[80:83], s20 offset:2048 sc1\n\t"
"v_mfma_f32_16x16x32_bf16 a[160:163], a[212:215], a[200:203], a[160:163]\n\t"
"s_waitcnt vmcnt(9) lgkmcnt(3)\n\t"
"ds_read_b128 a[196:199], v244 offset:12800\n\t"
"s_mov_b32 s20, 712704\n\t"
"v_mfma_f32_16x16x32_bf16 a[160:163], v[248:251], a[204:207], a[160:163]\n\t"
"ds_read_b128 a[200:203], v244 offset:13312\n\t"
"v_mfma_f32_16x16x32_bf16 v[224:227], v[224:227], v[220:223], 0\n\t"
"ds_read_b128 a[204:207], v244 offset:13824\n\t"
"v_mfma_f32_16x16x32_bf16 v[200:203], a[216:219], a[168:171], v[200:203]\n\t"
"buffer_load_dwordx4 v[248:251], off, s[80:83], s20 offset:0 sc1\n\t"
"v_mfma_f32_16x16x32_bf16 v[200:203], v[236:239], a[176:179], v[200:203]\n\t"
"v_mfma_f32_16x16x32_bf16 v[200:203], a[220:223], a[172:175], v[200:203]\n\t"
"v_mfma_f32_16x16x32_bf16 v[208:211], a[216:219], a[172:175], v[208:211]\n\t"
"buffer_load_dwordx4 a[212:215], off, s[80:83], s20 offset:1024 sc1\n\t"
"v_mfma_f32_16x16x32_bf16 v[208:211], a[220:223], a[176:179], v[208:211]\n\t"
"s_waitcnt lgkmcnt(5)\n\t"
"v_cvt_pk_bf16_f32 v224, v224, v225\n\t"
"v_mfma_f32_16x16x32_bf16 v[208:211], v[236:239], a[180:183], v[208:211]\n\t"
"s_waitcnt lgkmcnt(0)\n\t"
"s_barrier\n\t"
"v_mfma_f32_16x16x32_bf16 v[216:219], v[236:239], a[184:187], v[216:219]\n\t"
"v_cvt_pk_bf16_f32 v225, v226, v227\n\t"
"v_mfma_f32_16x16x32_bf16 v[216:219], a[220:223], a[180:183], v[216:219]\n\t"
"v_mfma_f32_16x16x32_bf16 v[216:219], a[216:219], a[176:179], v[216:219]\n\t"
"ds_write2st64_b64 v245, v[228:229], v[230:231] offset0:0 offset1:1\n\t"
"v_mfma_f32_16x16x32_bf16 a[4:7], a[216:219], a[180:183], a[4:7]\n\t"
"v_mfma_f32_16x16x32_bf16 a[4:7], a[220:223], a[184:187], a[4:7]\n\t"
"ds_read_b64_tr_b16 v[228:229], v246 offset:33280\n\t"
"v_mfma_f32_16x16x32_bf16 a[4:7], v[236:239], a[188:191], a[4:7]\n\t"
"ds_write2st64_b64 v245, v[232:233], v[234:235] offset0:2 offset1:3\n\t"
"v_mfma_f32_16x16x32_bf16 a[12:15], v[236:239], a[192:195], a[12:15]\n\t"
"v_mfma_f32_16x16x32_bf16 a[12:15], a[220:223], a[188:191], a[12:15]\n\t"
"ds_read_b64_tr_b16 v[230:231], v246 offset:36352\n\t"
"v_mfma_f32_16x16x32_bf16 a[12:15], a[216:219], a[184:187], a[12:15]\n\t"
"ds_write_b64 v245, v[224:225] offset:2048\n\t"
"v_mfma_f32_16x16x32_bf16 a[20:23], a[216:219], a[188:191], a[20:23]\n\t"
"ds_read_b64_tr_b16 v[232:233], v246 offset:40960\n\t"
"v_mfma_f32_16x16x32_bf16 a[20:23], a[220:223], a[192:195], a[20:23]\n\t"
"ds_read_b64_tr_b16 v[234:235], v246 offset:44032\n\t"
"v_mfma_f32_16x16x32_bf16 a[20:23], v[236:239], a[196:199], a[20:23]\n\t"
"ds_read_b64_tr_b16 v[224:225], v246 offset:47104\n\t"
"v_mfma_f32_16x16x32_bf16 a[28:31], a[220:223], a[196:199], a[28:31]\n\t"
"s_waitcnt lgkmcnt(4)\n\t"
"ds_read_b64_tr_b16 v[226:227], v246 offset:50176\n\t"
"v_mfma_f32_16x16x32_bf16 a[28:31], a[216:219], a[192:195], a[28:31]\n\t"
"global_load_dwordx4 v[240:243], v252, s[48:49] offset:2048\n\t"
"v_mfma_f32_16x16x32_bf16 a[28:31], v[236:239], a[200:203], a[28:31]\n\t"
"v_mfma_f32_16x16x32_bf16 v[228:231], v[228:231], v[220:223], 0\n\t"
"s_waitcnt lgkmcnt(2)\n\t"
"v_mfma_f32_16x16x32_bf16 v[232:235], v[232:235], v[220:223], 0\n\t"
"v_mfma_f32_16x16x32_bf16 a[36:39], a[220:223], a[200:203], a[36:39]\n\t"
"buffer_load_dwordx4 a[220:223], off, s[80:83], s20 offset:2048 sc1\n\t"
"v_mfma_f32_16x16x32_bf16 a[36:39], a[216:219], a[196:199], a[36:39]\n\t"
"global_load_dwordx4 a[208:211], v252, s[46:47] offset:1024\n\t"
"v_mfma_f32_16x16x32_bf16 a[36:39], v[236:239], a[204:207], a[36:39]\n\t"
"s_waitcnt vmcnt(11) lgkmcnt(0)\n\t"
"s_mov_b32 s20, 724992\n\t"
"v_mfma_f32_16x16x32_bf16 v[224:227], v[224:227], v[220:223], 0\n\t"
"buffer_load_dwordx4 a[216:219], off, s[80:83], s20 offset:0 sc1\n\t"
"v_mfma_f32_16x16x32_bf16 a[44:47], v[96:99], a[168:171], a[44:47]\n\t"
"v_cvt_pk_bf16_f32 v228, v228, v229\n\t"
"v_cvt_pk_bf16_f32 v229, v230, v231\n\t"
"v_mfma_f32_16x16x32_bf16 a[44:47], a[228:231], a[176:179], a[44:47]\n\t"
"v_cvt_pk_bf16_f32 v230, v232, v233\n\t"
"v_cvt_pk_bf16_f32 v231, v234, v235\n\t"
"v_mfma_f32_16x16x32_bf16 a[44:47], a[224:227], a[172:175], a[44:47]\n\t"
"ds_write2st64_b64 v245, v[228:229], v[230:231] offset0:5 offset1:6\n\t"
"v_mfma_f32_16x16x32_bf16 a[52:55], v[96:99], a[172:175], a[52:55]\n\t"
"v_mfma_f32_16x16x32_bf16 a[52:55], a[224:227], a[176:179], a[52:55]\n\t"
"ds_read_b64_tr_b16 v[232:233], v246 offset:53248\n\t"
"v_mfma_f32_16x16x32_bf16 a[52:55], a[228:231], a[180:183], a[52:55]\n\t"
"ds_read_b64_tr_b16 v[234:235], v246 offset:56320\n\t"
"v_cvt_pk_bf16_f32 v224, v224, v225\n\t"
"v_mfma_f32_16x16x32_bf16 a[60:63], a[228:231], a[184:187], a[60:63]\n\t"
"v_cvt_pk_bf16_f32 v225, v226, v227\n\t"
"v_mfma_f32_16x16x32_bf16 a[60:63], a[224:227], a[180:183], a[60:63]\n\t"
"v_mfma_f32_16x16x32_bf16 a[60:63], v[96:99], a[176:179], a[60:63]\n\t"
"v_mfma_f32_16x16x32_bf16 a[68:71], v[96:99], a[180:183], a[68:71]\n\t"
"v_mfma_f32_16x16x32_bf16 a[68:71], a[224:227], a[184:187], a[68:71]\n\t"
"v_mfma_f32_16x16x32_bf16 a[68:71], a[228:231], a[188:191], a[68:71]\n\t"
"v_mfma_f32_16x16x32_bf16 a[76:79], a[224:227], a[188:191], a[76:79]\n\t"
"s_waitcnt lgkmcnt(0)\n\t"
"v_mfma_f32_16x16x32_bf16 a[76:79], a[228:231], a[192:195], a[76:79]\n\t"
"v_mfma_f32_16x16x32_bf16 a[76:79], v[96:99], a[184:187], a[76:79]\n\t"
"v_mfma_f32_16x16x32_bf16 v[232:235], v[232:235], v[220:223], 0\n\t"
"v_mfma_f32_16x16x32_bf16 a[84:87], v[96:99], a[188:191], a[84:87]\n\t"
"v_mfma_f32_16x16x32_bf16 a[84:87], a[228:231], a[196:199], a[84:87]\n\t"
"v_mfma_f32_16x16x32_bf16 a[84:87], a[224:227], a[192:195], a[84:87]\n\t"
"v_mfma_f32_16x16x32_bf16 a[92:95], v[96:99], a[192:195], a[92:95]\n\t"
"v_mfma_f32_16x16x32_bf16 a[92:95], a[224:227], a[196:199], a[92:95]\n\t"
"v_cvt_pk_bf16_f32 v226, v232, v233\n\t"
"v_cvt_pk_bf16_f32 v227, v234, v235\n\t"
"v_mfma_f32_16x16x32_bf16 a[92:95], a[228:231], a[200:203], a[92:95]\n\t"
"ds_write2st64_b64 v245, v[224:225], v[226:227] offset0:7 offset1:8\n\t"
"v_mfma_f32_16x16x32_bf16 a[100:103], a[224:227], a[200:203], a[100:103]\n\t"
"buffer_load_dwordx4 a[224:227], off, s[80:83], s20 offset:1024 sc1\n\t"
"v_mfma_f32_16x16x32_bf16 a[100:103], v[96:99], a[196:199], a[100:103]\n\t"
"s_waitcnt vmcnt(10)\n\t"
"v_mfma_f32_16x16x32_bf16 a[100:103], a[228:231], a[204:207], a[100:103]\n\t"
"v_mfma_f32_16x16x32_bf16 a[108:111], a[232:235], a[168:171], a[108:111]\n\t"
"buffer_load_dwordx4 v[96:99], off, s[80:83], s20 offset:2048 sc1\n\t"
"v_mfma_f32_16x16x32_bf16 a[108:111], a[240:243], a[176:179], a[108:111]\n\t"
"s_mov_b32 s20, 737280\n\t"
"v_mfma_f32_16x16x32_bf16 a[108:111], a[236:239], a[172:175], a[108:111]\n\t"
"s_waitcnt lgkmcnt(0)\n\t"
"s_barrier\n\t"
"v_mfma_f32_16x16x32_bf16 a[116:119], a[232:235], a[172:175], a[116:119]\n\t"
"s_waitcnt vmcnt(5)\n\t"
"ds_read_b64_tr_b16 v[236:237], v240 offset:1024\n\t"
"v_mfma_f32_16x16x32_bf16 a[116:119], a[236:239], a[176:179], a[116:119]\n\t"
"buffer_load_dwordx4 a[228:231], off, s[80:83], s20 offset:0 sc1\n\t"
"v_mfma_f32_16x16x32_bf16 a[116:119], a[240:243], a[180:183], a[116:119]\n\t"
"ds_read_b64_tr_b16 v[238:239], v240 offset:2560\n\t"
"v_mfma_f32_16x16x32_bf16 a[124:127], a[240:243], a[184:187], a[124:127]\n\t"
"ds_read_b64_tr_b16 v[228:229], v241 offset:1024\n\t"
"v_mfma_f32_16x16x32_bf16 a[124:127], a[236:239], a[180:183], a[124:127]\n\t"
"ds_read_b64_tr_b16 v[230:231], v241 offset:2560\n\t"
"v_mfma_f32_16x16x32_bf16 a[124:127], a[232:235], a[176:179], a[124:127]\n\t"
"ds_read_b64_tr_b16 v[232:233], v240 offset:20992\n\t"
"v_mfma_f32_16x16x32_bf16 a[132:135], a[232:235], a[180:183], a[132:135]\n\t"
"ds_read_b64_tr_b16 v[234:235], v240 offset:22528\n\t"
"v_mfma_f32_16x16x32_bf16 a[132:135], a[236:239], a[184:187], a[132:135]\n\t"
"ds_read_b64_tr_b16 v[224:225], v241 offset:20992\n\t"
"v_mfma_f32_16x16x32_bf16 a[132:135], a[240:243], a[188:191], a[132:135]\n\t"
"ds_read_b64_tr_b16 v[226:227], v241 offset:22528\n\t"
"v_mfma_f32_16x16x32_bf16 a[140:143], a[232:235], a[184:187], a[140:143]\n\t"
"s_waitcnt vmcnt(4) lgkmcnt(6)\n\t"
"ds_read_b128 a[168:171], v244 offset:0\n\t"
"v_mfma_f32_16x16x32_bf16 a[140:143], a[240:243], a[192:195], a[140:143]\n\t"
"s_waitcnt lgkmcnt(5)\n\t"
"ds_read_b128 a[172:175], v244 offset:512\n\t"
"v_mfma_f32_16x16x32_bf16 a[140:143], a[236:239], a[188:191], a[140:143]\n\t"
"s_waitcnt lgkmcnt(4)\n\t"
"ds_read_b128 a[176:179], v244 offset:1024\n\t"
"v_mfma_f32_16x16x32_bf16 v[236:239], v[236:239], a[208:211], 0\n\t"
"ds_read_b128 a[180:183], v244 offset:1536\n\t"
"v_mfma_f32_16x16x32_bf16 v[228:231], v[228:231], a[208:211], 0\n\t"
"s_waitcnt lgkmcnt(4)\n\t"
"ds_read_b128 a[184:187], v244 offset:2048\n\t"
"v_mfma_f32_16x16x32_bf16 v[232:235], v[232:235], a[208:211], 0\n\t"
"v_mfma_f32_16x16x32_bf16 v[224:227], v[224:227], a[208:211], 0\n\t"
"v_mfma_f32_16x16x32_bf16 a[148:151], a[232:235], a[188:191], a[148:151]\n\t"
"ds_read_b128 a[188:191], v244 offset:2560\n\t"
"v_mfma_f32_16x16x32_bf16 a[148:151], a[240:243], a[196:199], a[148:151]\n\t"
"v_cvt_pk_bf16_f32 v236, v236, v237\n\t"
"v_cvt_pk_bf16_f32 v237, v238, v239\n\t"
"v_mfma_f32_16x16x32_bf16 a[148:151], a[236:239], a[192:195], a[148:151]\n\t"
"v_cvt_pk_bf16_f32 v238, v228, v229\n\t"
"v_cvt_pk_bf16_f32 v239, v230, v231\n\t"
"v_mfma_f32_16x16x32_bf16 a[156:159], a[232:235], a[192:195], a[156:159]\n\t"
"ds_read_b128 a[192:195], v244 offset:3072\n\t"
"v_cvt_pk_bf16_f32 v232, v232, v233\n\t"
"v_mfma_f32_16x16x32_bf16 a[156:159], a[236:239], a[196:199], a[156:159]\n\t"
"v_cvt_pk_bf16_f32 v233, v234, v235\n\t"
"v_cvt_pk_bf16_f32 v234, v224, v225\n\t"
"v_mfma_f32_16x16x32_bf16 a[156:159], a[240:243], a[200:203], a[156:159]\n\t"
"ds_write_b128 v242, v[236:239] offset:24576\n\t"
"v_mfma_f32_16x16x32_bf16 a[164:167], a[236:239], a[200:203], a[164:167]\n\t"
"buffer_load_dwordx4 a[236:239], off, s[80:83], s20 offset:1024 sc1\n\t"
"v_mfma_f32_16x16x32_bf16 a[164:167], a[232:235], a[196:199], a[164:167]\n\t"
"s_waitcnt lgkmcnt(5)\n\t"
"ds_read_b64_tr_b16 v[228:229], v240 offset:40960\n\t"
"v_mfma_f32_16x16x32_bf16 a[164:167], a[240:243], a[204:207], a[164:167]\n\t"
"ds_read_b64_tr_b16 v[230:231], v240 offset:42496\n\t"
"v_cvt_pk_bf16_f32 v235, v226, v227\n\t"
"Lst6_%=:\n\t"
"v_mfma_f32_16x16x32_bf16 v[196:199], a[244:247], a[168:171], v[196:199]\n\t"
"s_waitcnt lgkmcnt(6)\n\t"
"ds_read_b64_tr_b16 v[224:225], v241 offset:40960\n\t"
"v_mfma_f32_16x16x32_bf16 v[196:199], a[252:255], a[176:179], v[196:199]\n\t"
"buffer_load_dwordx4 a[232:235], off, s[80:83], s20 offset:2048 sc1\n\t"
"v_mfma_f32_16x16x32_bf16 v[196:199], a[248:251], a[172:175], v[196:199]\n\t"
"s_waitcnt lgkmcnt(5)\n\t"
"ds_write_b128 v242, v[232:235] offset:26112\n\t"
"s_mov_b32 s20, 749568\n\t"
"v_mfma_f32_16x16x32_bf16 v[204:207], a[244:247], a[172:175], v[204:207]\n\t"
"buffer_load_dwordx4 a[240:243], off, s[80:83], s20 offset:0 sc1\n\t"
"v_mfma_f32_16x16x32_bf16 v[204:207], a[248:251], a[176:179], v[204:207]\n\t"
"Lpg5_%=:\n\t"
"s_cbranch_vccnz Lpg9_%=\n\t"
"ds_read_b64_tr_b16 v[226:227], v241 offset:42496\n\t"
"v_add_u32 v240, 59904, v240\n\t"
"v_mfma_f32_16x16x32_bf16 v[204:207], a[252:255], a[180:183], v[204:207]\n\t"
"ds_read_b128 a[196:199], v244 offset:3584\n\t"
"v_add_u32 v241, 59904, v241\n\t"
"v_mfma_f32_16x16x32_bf16 v[212:215], a[248:251], a[180:183], v[212:215]\n\t"
"ds_read_b64_tr_b16 v[236:237], v240 offset:1024\n\t"
"v_mfma_f32_16x16x32_bf16 v[212:215], a[244:247], a[176:179], v[212:215]\n\t"
"ds_read_b64_tr_b16 v[238:239], v240 offset:2560\n\t"
"v_mfma_f32_16x16x32_bf16 v[212:215], a[252:255], a[184:187], v[212:215]\n\t"
"ds_read_b64_tr_b16 v[232:233], v241 offset:1024\n\t"
"v_mfma_f32_16x16x32_bf16 a[0:3], a[244:247], a[180:183], a[0:3]\n\t"
"s_waitcnt lgkmcnt(7)\n\t"
"ds_read_b64_tr_b16 v[234:235], v241 offset:2560\n\t"
"v_mfma_f32_16x16x32_bf16 a[0:3], a[252:255], a[188:191], a[0:3]\n\t"
"s_waitcnt lgkmcnt(5)\n\t"
"ds_read_b128 a[200:203], v244 offset:4096\n\t"
"v_mfma_f32_16x16x32_bf16 a[0:3], a[248:251], a[184:187], a[0:3]\n\t"
"ds_read_b128 a[204:207], v244 offset:4608\n\t"
"v_mfma_f32_16x16x32_bf16 v[228:231], v[228:231], a[208:211], 0\n\t"
"s_waitcnt lgkmcnt(4)\n\t"
"v_mfma_f32_16x16x32_bf16 v[224:227], v[224:227], a[208:211], 0\n\t"
"v_mfma_f32_16x16x32_bf16 v[236:239], v[236:239], a[208:211], 0\n\t"
"s_waitcnt lgkmcnt(2)\n\t"
"v_mfma_f32_16x16x32_bf16 v[232:235], v[232:235], a[208:211], 0\n\t"
"v_mfma_f32_16x16x32_bf16 a[8:11], a[244:247], a[184:187], a[8:11]\n\t"
"v_mfma_f32_16x16x32_bf16 a[8:11], a[252:255], a[192:195], a[8:11]\n\t"
"v_cvt_pk_bf16_f32 v228, v228, v229\n\t"
"v_cvt_pk_bf16_f32 v229, v230, v231\n\t"
"v_mfma_f32_16x16x32_bf16 a[8:11], a[248:251], a[188:191], a[8:11]\n\t"
"v_cvt_pk_bf16_f32 v230, v224, v225\n\t"
"v_cvt_pk_bf16_f32 v231, v226, v227\n\t"
"v_mfma_f32_16x16x32_bf16 a[16:19], a[244:247], a[188:191], a[16:19]\n\t"
"s_waitcnt lgkmcnt(1)\n\t"
"v_cvt_pk_bf16_f32 v236, v236, v237\n\t"
"v_cvt_pk_bf16_f32 v237, v238, v239\n\t"
"v_mfma_f32_16x16x32_bf16 a[16:19], a[248:251], a[192:195], a[16:19]\n\t"
"v_cvt_pk_bf16_f32 v238, v232, v233\n\t"
"v_cvt_pk_bf16_f32 v239, v234, v235\n\t"
"v_mfma_f32_16x16x32_bf16 a[16:19], a[252:255], a[196:199], a[16:19]\n\t"
"ds_write_b128 v242, v[228:231] offset:27648\n\t"
"v_mfma_f32_16x16x32_bf16 a[24:27], a[252:255], a[200:203], a[24:27]\n\t"
"s_waitcnt lgkmcnt(1)\n\t"
"v_mfma_f32_16x16x32_bf16 a[24:27], a[248:251], a[196:199], a[24:27]\n\t"
"ds_read_b64_tr_b16 v[224:225], v240 offset:20992\n\t"
"v_mfma_f32_16x16x32_bf16 a[24:27], a[244:247], a[192:195], a[24:27]\n\t"
"ds_write_b128 v242, v[236:239] offset:29184\n\t"
"v_mfma_f32_16x16x32_bf16 a[32:35], a[244:247], a[196:199], a[32:35]\n\t"
"buffer_load_dwordx4 a[244:247], off, s[80:83], s20 offset:1024 sc1\n\t"
"v_mfma_f32_16x16x32_bf16 a[32:35], a[248:251], a[200:203], a[32:35]\n\t"
"ds_read_b64_tr_b16 v[226:227], v240 offset:22528\n\t"
"v_mfma_f32_16x16x32_bf16 a[32:35], a[252:255], a[204:207], a[32:35]\n\t"
"ds_read_b64_tr_b16 v[232:233], v241 offset:20992\n\t"
"v_mfma_f32_16x16x32_bf16 a[40:43], v[248:251], a[168:171], a[40:43]\n\t"
"ds_read_b64_tr_b16 v[234:235], v241 offset:22528\n\t"
"v_mfma_f32_16x16x32_bf16 a[40:43], a[220:223], a[176:179], a[40:43]\n\t"
"ds_read_b64_tr_b16 v[228:229], v240 offset:40960\n\t"
"v_mfma_f32_16x16x32_bf16 a[40:43], a[212:215], a[172:175], a[40:43]\n\t"
"ds_read_b64_tr_b16 v[230:231], v240 offset:42496\n\t"
"v_mfma_f32_16x16x32_bf16 a[48:51], v[248:251], a[172:175], a[48:51]\n\t"
"buffer_load_dwordx4 a[248:251], off, s[80:83], s20 offset:2048 sc1\n\t"
"v_mfma_f32_16x16x32_bf16 a[48:51], a[212:215], a[176:179], a[48:51]\n\t"
"s_mov_b32 s20, 761856\n\t"
"ds_read_b64_tr_b16 v[236:237], v241 offset:40960\n\t"
"v_mfma_f32_16x16x32_bf16 a[48:51], a[220:223], a[180:183], a[48:51]\n\t"
"ds_read_b64_tr_b16 v[238:239], v241 offset:42496\n\t"
"v_mfma_f32_16x16x32_bf16 a[56:59], v[248:251], a[176:179], a[56:59]\n\t"
"buffer_load_dwordx4 a[252:255], off, s[80:83], s20 offset:0 sc1\n\t"
"v_mfma_f32_16x16x32_bf16 a[56:59], a[220:223], a[184:187], a[56:59]\n\t"
"s_waitcnt lgkmcnt(4)\n\t"
"v_mfma_f32_16x16x32_bf16 a[56:59], a[212:215], a[180:183], a[56:59]\n\t"
"s_waitcnt lgkmcnt(2)\n\t"
"v_mfma_f32_16x16x32_bf16 v[224:227], v[224:227], a[208:211], 0\n\t"
"v_mfma_f32_16x16x32_bf16 v[232:235], v[232:235], a[208:211], 0\n\t"
"s_waitcnt lgkmcnt(0)\n\t"
"v_xor_b32 v246, 0xc0, v246\n\t"
"v_mfma_f32_16x16x32_bf16 v[228:231], v[228:231], a[208:211], 0\n\t"
"v_mfma_f32_16x16x32_bf16 v[236:239], v[236:239], a[208:211], 0\n\t"
"v_mfma_f32_16x16x32_bf16 a[64:67], v[248:251], a[180:183], a[64:67]\n\t"
"buffer_load_dwordx4 a[208:211], off, s[80:83], s20 offset:1024 sc1\n\t"
"v_mfma_f32_16x16x32_bf16 a[64:67], a[220:223], a[188:191], a[64:67]\n\t"
"v_cvt_pk_bf16_f32 v224, v224, v225\n\t"
"v_cvt_pk_bf16_f32 v225, v226, v227\n\t"
"v_mfma_f32_16x16x32_bf16 a[64:67], a[212:215], a[184:187], a[64:67]\n\t"
"v_cvt_pk_bf16_f32 v226, v232, v233\n\t"
"v_cvt_pk_bf16_f32 v227, v234, v235\n\t"
"v_mfma_f32_16x16x32_bf16 a[72:75], v[248:251], a[184:187], a[72:75]\n\t"
"v_cvt_pk_bf16_f32 v228, v228, v229\n\t"
"v_cvt_pk_bf16_f32 v229, v230, v231\n\t"
"v_mfma_f32_16x16x32_bf16 a[72:75], a[212:215], a[188:191], a[72:75]\n\t"
"v_cvt_pk_bf16_f32 v230, v236, v237\n\t"
"v_cvt_pk_bf16_f32 v231, v238, v239\n\t"
"v_mfma_f32_16x16x32_bf16 a[72:75], a[220:223], a[192:195], a[72:75]\n\t"
"ds_write_b128 v242, v[224:227] offset:30720\n\t"
"v_mfma_f32_16x16x32_bf16 a[80:83], a[220:223], a[196:199], a[80:83]\n\t"
"v_mfma_f32_16x16x32_bf16 a[80:83], a[212:215], a[192:195], a[80:83]\n\t"
"v_mfma_f32_16x16x32_bf16 a[80:83], v[248:251], a[188:191], a[80:83]\n\t"
"ds_write_b128 v242, v[228:231] offset:32256\n\t"
"v_mfma_f32_16x16x32_bf16 a[88:91], v[248:251], a[192:195], a[88:91]\n\t"
"v_mfma_f32_16x16x32_bf16 a[88:91], a[212:215], a[196:199], a[88:91]\n\t"
"v_mfma_f32_16x16x32_bf16 a[88:91], a[220:223], a[200:203], a[88:91]\n\t"
"ds_read_b64_tr_b16 v[232:233], v246 offset:1024\n\t"
"v_mfma_f32_16x16x32_bf16 a[96:99], a[212:215], a[200:203], a[96:99]\n\t"
"buffer_load_dwordx4 v[240:243], off, s[80:83], s20 offset:2048 sc1\n\t"
"v_mfma_f32_16x16x32_bf16 a[96:99], v[248:251], a[196:199], a[96:99]\n\t"
"s_waitcnt vmcnt(9)\n\t"
"ds_read_b64_tr_b16 v[234:235], v246 offset:4096\n\t"
"s_mov_b32 s20, 626688\n\t"
"v_mfma_f32_16x16x32_bf16 a[96:99], a[220:223], a[204:207], a[96:99]\n\t"
"ds_read_b64_tr_b16 v[236:237], v246 offset:7168\n\t"
"v_mfma_f32_16x16x32_bf16 a[104:107], a[216:219], a[168:171], a[104:107]\n\t"
"buffer_load_dwordx4 v[248:251], off, s[80:83], s20 offset:0 sc1\n\t"
"v_mfma_f32_16x16x32_bf16 a[104:107], v[96:99], a[176:179], a[104:107]\n\t"
"ds_read_b64_tr_b16 v[238:239], v246 offset:10240\n\t"
"v_mfma_f32_16x16x32_bf16 a[104:107], a[224:227], a[172:175], a[104:107]\n\t"
"ds_read_b64_tr_b16 v[224:225], v246 offset:13312\n\t"
"v_mfma_f32_16x16x32_bf16 a[112:115], a[216:219], a[172:175], a[112:115]\n\t"
"ds_read_b64_tr_b16 v[226:227], v246 offset:16384\n\t"
"v_mfma_f32_16x16x32_bf16 a[112:115], a[224:227], a[176:179], a[112:115]\n\t"
"buffer_load_dwordx4 a[212:215], off, s[80:83], s20 offset:1024 sc1\n\t"
"v_mfma_f32_16x16x32_bf16 a[112:115], v[96:99], a[180:183], a[112:115]\n\t"
"ds_read_b64_tr_b16 v[228:229], v246 offset:20992\n\t"
"v_mfma_f32_16x16x32_bf16 a[120:123], a[224:227], a[180:183], a[120:123]\n\t"
"ds_read_b64_tr_b16 v[230:231], v246 offset:24064\n\t"
"v_mfma_f32_16x16x32_bf16 a[120:123], a[216:219], a[176:179], a[120:123]\n\t"
"buffer_load_dwordx4 a[220:223], off, s[80:83], s20 offset:2048 sc1\n\t"
"v_mfma_f32_16x16x32_bf16 a[120:123], v[96:99], a[184:187], a[120:123]\n\t"
"ds_read_b128 a[168:171], v244 offset:9216\n\t"
"v_mfma_f32_16x16x32_bf16 a[128:131], a[216:219], a[180:183], a[128:131]\n\t"
"s_waitcnt lgkmcnt(7)\n\t"
"ds_read_b128 a[172:175], v244 offset:9728\n\t"
"v_mfma_f32_16x16x32_bf16 a[128:131], v[96:99], a[188:191], a[128:131]\n\t"
"s_waitcnt lgkmcnt(6)\n\t"
"ds_read_b128 a[176:179], v244 offset:10240\n\t"
"v_mfma_f32_16x16x32_bf16 a[128:131], a[224:227], a[184:187], a[128:131]\n\t"
"s_waitcnt lgkmcnt(5)\n\t"
"ds_read_b128 a[180:183], v244 offset:10752\n\t"
"v_mfma_f32_16x16x32_bf16 v[232:235], v[232:235], v[220:223], 0\n\t"
"s_waitcnt lgkmcnt(4)\n\t"
"v_mfma_f32_16x16x32_bf16 v[236:239], v[236:239], v[220:223], 0\n\t"
"v_mfma_f32_16x16x32_bf16 v[224:227], v[224:227], v[220:223], 0\n\t"
"v_mfma_f32_16x16x32_bf16 v[228:231], v[228:231], v[220:223], 0\n\t"
"v_mfma_f32_16x16x32_bf16 a[136:139], a[216:219], a[184:187], a[136:139]\n\t"
"s_mov_b32 s20, 638976\n\t"
"ds_read_b128 a[184:187], v244 offset:11264\n\t"
"v_mfma_f32_16x16x32_bf16 a[136:139], v[96:99], a[192:195], a[136:139]\n\t"
"v_cvt_pk_bf16_f32 v232, v232, v233\n\t"
"v_cvt_pk_bf16_f32 v233, v234, v235\n\t"
"v_mfma_f32_16x16x32_bf16 a[136:139], a[224:227], a[188:191], a[136:139]\n\t"
"v_cvt_pk_bf16_f32 v234, v236, v237\n\t"
"v_cvt_pk_bf16_f32 v235, v238, v239\n\t"
"v_mfma_f32_16x16x32_bf16 a[144:147], a[216:219], a[188:191], a[144:147]\n\t"
"ds_read_b128 a[188:191], v244 offset:11776\n\t"
"v_cvt_pk_bf16_f32 v224, v224, v225\n\t"
"v_mfma_f32_16x16x32_bf16 a[144:147], a[224:227], a[192:195], a[144:147]\n\t"
"v_cvt_pk_bf16_f32 v225, v226, v227\n\t"
"v_cvt_pk_bf16_f32 v226, v228, v229\n\t"
"v_mfma_f32_16x16x32_bf16 a[144:147], v[96:99], a[196:199], a[144:147]\n\t"
"v_cvt_pk_bf16_f32 v227, v230, v231\n\t"
"v_mfma_f32_16x16x32_bf16 a[152:155], v[96:99], a[200:203], a[152:155]\n\t"
"ds_read_b64_tr_b16 v[228:229], v246 offset:27136\n\t"
"v_mfma_f32_16x16x32_bf16 a[152:155], a[224:227], a[196:199], a[152:155]\n\t"
"ds_read_b64_tr_b16 v[230:231], v246 offset:30208\n\t"
"v_mfma_f32_16x16x32_bf16 a[152:155], a[216:219], a[192:195], a[152:155]\n\t"
"buffer_load_dwordx4 v[236:239], off, s[80:83], s20 offset:0 sc1\n\t"
"v_mfma_f32_16x16x32_bf16 a[160:163], a[216:219], a[196:199], a[160:163]\n\t"
"ds_read_b128 a[192:195], v244 offset:12288\n\t"
"v_mfma_f32_16x16x32_bf16 a[160:163], a[224:227], a[200:203], a[160:163]\n\t"
"buffer_load_dwordx4 a[216:219], off, s[80:83], s20 offset:1024 sc1\n\t"
"v_mfma_f32_16x16x32_bf16 a[160:163], v[96:99], a[204:207], a[160:163]\n\t"
"s_waitcnt vmcnt(11) lgkmcnt(1)\n\t"
"ds_read_b128 a[196:199], v244 offset:12800\n\t"
"v_mfma_f32_16x16x32_bf16 v[228:231], v[228:231], v[220:223], 0\n\t"
"ds_read_b128 a[200:203], v244 offset:13312\n\t"
"v_mfma_f32_16x16x32_bf16 v[200:203], a[228:231], a[168:171], v[200:203]\n\t"
"ds_read_b128 a[204:207], v244 offset:13824\n\t"
"v_mfma_f32_16x16x32_bf16 v[200:203], a[232:235], a[176:179], v[200:203]\n\t"
"buffer_load_dwordx4 v[96:99], off, s[80:83], s20 offset:2048 sc1\n\t"
"v_mfma_f32_16x16x32_bf16 v[200:203], a[236:239], a[172:175], v[200:203]\n\t"
"s_mov_b32 s20, 651264\n\t"
"s_add_u32 m0, s24, 256\n\t"
"s_add_u32 s28, s21, 0\n\t"
"v_mfma_f32_16x16x32_bf16 v[208:211], a[228:231], a[172:175], v[208:211]\n\t"
"buffer_load_dwordx4 a[224:227], off, s[80:83], s20 offset:0 sc1\n\t"
"v_mfma_f32_16x16x32_bf16 v[208:211], a[236:239], a[176:179], v[208:211]\n\t"
"v_cvt_pk_bf16_f32 v228, v228, v229\n\t"
"v_mfma_f32_16x16x32_bf16 v[208:211], a[232:235], a[180:183], v[208:211]\n\t"
"s_waitcnt lgkmcnt(0)\n\t"
"s_barrier\n\t"
"v_mfma_f32_16x16x32_bf16 v[216:219], a[232:235], a[184:187], v[216:219]\n\t"
"v_cvt_pk_bf16_f32 v229, v230, v231\n\t"
"v_mfma_f32_16x16x32_bf16 v[216:219], a[236:239], a[180:183], v[216:219]\n\t"
"buffer_load_dwordx4 v247, s[12:15], s28 offen offset:256 nt lds\n\t"
"v_mfma_f32_16x16x32_bf16 v[216:219], a[228:231], a[176:179], v[216:219]\n\t"
"s_add_u32 m0, s24, 6400\n\t"
"s_add_u32 s28, s21, 196608\n\t"
"ds_write2st64_b64 v245, v[232:233], v[234:235] offset0:0 offset1:1\n\t"
"v_mfma_f32_16x16x32_bf16 a[4:7], a[228:231], a[180:183], a[4:7]\n\t"
"buffer_load_dwordx4 v247, s[12:15], s28 offen offset:256 nt lds\n\t"
"v_mfma_f32_16x16x32_bf16 a[4:7], a[236:239], a[184:187], a[4:7]\n\t"
"s_add_u32 m0, s24, 12544\n\t"
"s_add_u32 s28, s21, 393216\n\t"
"ds_read_b64_tr_b16 v[232:233], v246 offset:33280\n\t"
"v_mfma_f32_16x16x32_bf16 a[4:7], a[232:235], a[188:191], a[4:7]\n\t"
"ds_write2st64_b64 v245, v[224:225], v[226:227] offset0:2 offset1:3\n\t"
"v_mfma_f32_16x16x32_bf16 a[12:15], a[232:235], a[192:195], a[12:15]\n\t"
"buffer_load_dwordx4 v247, s[12:15], s28 offen offset:256 nt lds\n\t"
"v_mfma_f32_16x16x32_bf16 a[12:15], a[236:239], a[188:191], a[12:15]\n\t"
"s_add_u32 m0, s24, 20224\n\t"
"s_add_u32 s28, s21, 589824\n\t"
"ds_read_b64_tr_b16 v[234:235], v246 offset:36352\n\t"
"v_mfma_f32_16x16x32_bf16 a[12:15], a[228:231], a[184:187], a[12:15]\n\t"
"ds_write_b64 v245, v[228:229] offset:2048\n\t"
"v_mfma_f32_16x16x32_bf16 a[20:23], a[228:231], a[188:191], a[20:23]\n\t"
"buffer_load_dwordx4 v247, s[12:15], s28 offen offset:256 nt lds\n\t"
"v_mfma_f32_16x16x32_bf16 a[20:23], a[236:239], a[192:195], a[20:23]\n\t"
"s_add_u32 m0, s24, 26368\n\t"
"s_add_u32 s28, s21, 786432\n\t"
"ds_read_b64_tr_b16 v[224:225], v246 offset:40960\n\t"
"v_mfma_f32_16x16x32_bf16 a[20:23], a[232:235], a[196:199], a[20:23]\n\t"
"ds_read_b64_tr_b16 v[226:227], v246 offset:44032\n\t"
"v_mfma_f32_16x16x32_bf16 a[28:31], a[236:239], a[196:199], a[28:31]\n\t"
"buffer_load_dwordx4 v247, s[12:15], s28 offen offset:256 nt lds\n\t"
"v_mfma_f32_16x16x32_bf16 a[28:31], a[228:231], a[192:195], a[28:31]\n\t"
"s_waitcnt lgkmcnt(3)\n\t"
"ds_read_b64_tr_b16 v[228:229], v246 offset:47104\n\t"
"v_mfma_f32_16x16x32_bf16 a[28:31], a[232:235], a[200:203], a[28:31]\n\t"
"s_add_u32 m0, s24, 60160\n\t"
"s_add_u32 s28, s21, 1769472\n\t"
"ds_read_b64_tr_b16 v[230:231], v246 offset:50176\n\t"
"v_mfma_f32_16x16x32_bf16 v[232:235], v[232:235], v[220:223], 0\n\t"
"s_waitcnt lgkmcnt(2)\n\t"
"buffer_load_dwordx4 v247, s[12:15], s28 offen offset:256 nt lds\n\t"
"v_mfma_f32_16x16x32_bf16 v[224:227], v[224:227], v[220:223], 0\n\t"
"v_mfma_f32_16x16x32_bf16 a[36:39], a[236:239], a[200:203], a[36:39]\n\t"
"v_mfma_f32_16x16x32_bf16 a[36:39], a[228:231], a[196:199], a[36:39]\n\t"
"s_waitcnt vmcnt(16)\n\t"
"v_mfma_f32_16x16x32_bf16 a[36:39], a[232:235], a[204:207], a[36:39]\n\t"
"s_waitcnt lgkmcnt(0)\n\t"
"v_mfma_f32_16x16x32_bf16 v[228:231], v[228:231], v[220:223], 0\n\t"
"v_cvt_pk_bf16_f32 v232, v232, v233\n\t"
"v_cvt_pk_bf16_f32 v233, v234, v235\n\t"
"v_mfma_f32_16x16x32_bf16 a[44:47], a[240:243], a[168:171], a[44:47]\n\t"
"v_cvt_pk_bf16_f32 v234, v224, v225\n\t"
"v_cvt_pk_bf16_f32 v235, v226, v227\n\t"
"v_mfma_f32_16x16x32_bf16 a[44:47], a[248:251], a[176:179], a[44:47]\n\t"
"ds_read_b64_tr_b16 v[224:225], v246 offset:53248\n\t"
"v_mfma_f32_16x16x32_bf16 a[44:47], a[244:247], a[172:175], a[44:47]\n\t"
"ds_write2st64_b64 v245, v[232:233], v[234:235] offset0:5 offset1:6\n\t"
"v_mfma_f32_16x16x32_bf16 a[52:55], a[240:243], a[172:175], a[52:55]\n\t"
"v_mfma_f32_16x16x32_bf16 a[52:55], a[244:247], a[176:179], a[52:55]\n\t"
"ds_read_b64_tr_b16 v[226:227], v246 offset:56320\n\t"
"v_mfma_f32_16x16x32_bf16 a[52:55], a[248:251], a[180:183], a[52:55]\n\t"
"v_cvt_pk_bf16_f32 v228, v228, v229\n\t"
"v_cvt_pk_bf16_f32 v229, v230, v231\n\t"
"v_mfma_f32_16x16x32_bf16 a[60:63], a[248:251], a[184:187], a[60:63]\n\t"
"v_mfma_f32_16x16x32_bf16 a[60:63], a[244:247], a[180:183], a[60:63]\n\t"
"v_mfma_f32_16x16x32_bf16 a[60:63], a[240:243], a[176:179], a[60:63]\n\t"
"v_mfma_f32_16x16x32_bf16 a[68:71], a[240:243], a[180:183], a[68:71]\n\t"
"v_mfma_f32_16x16x32_bf16 a[68:71], a[244:247], a[184:187], a[68:71]\n\t"
"v_mfma_f32_16x16x32_bf16 a[68:71], a[248:251], a[188:191], a[68:71]\n\t"
"v_mfma_f32_16x16x32_bf16 a[76:79], a[244:247], a[188:191], a[76:79]\n\t"
"s_waitcnt lgkmcnt(0)\n\t"
"v_mfma_f32_16x16x32_bf16 a[76:79], a[248:251], a[192:195], a[76:79]\n\t"
"v_mfma_f32_16x16x32_bf16 a[76:79], a[240:243], a[184:187], a[76:79]\n\t"
"v_mfma_f32_16x16x32_bf16 v[224:227], v[224:227], v[220:223], 0\n\t"
"v_mfma_f32_16x16x32_bf16 a[84:87], a[240:243], a[188:191], a[84:87]\n\t"
"v_mfma_f32_16x16x32_bf16 a[84:87], a[248:251], a[196:199], a[84:87]\n\t"
"buffer_load_dwordx4 a[228:231], off, s[80:83], s20 offset:1024 sc1\n\t"
"v_mfma_f32_16x16x32_bf16 a[84:87], a[244:247], a[192:195], a[84:87]\n\t"
"v_mfma_f32_16x16x32_bf16 a[92:95], a[240:243], a[192:195], a[92:95]\n\t"
"v_mfma_f32_16x16x32_bf16 a[92:95], a[244:247], a[196:199], a[92:95]\n\t"
"v_cvt_pk_bf16_f32 v230, v224, v225\n\t"
"v_cvt_pk_bf16_f32 v231, v226, v227\n\t"
"v_mfma_f32_16x16x32_bf16 a[92:95], a[248:251], a[200:203], a[92:95]\n\t"
"ds_write2st64_b64 v245, v[228:229], v[230:231] offset0:7 offset1:8\n\t"
"v_mfma_f32_16x16x32_bf16 a[100:103], a[244:247], a[200:203], a[100:103]\n\t"
"s_waitcnt vmcnt(14)\n\t"
"v_mfma_f32_16x16x32_bf16 a[100:103], a[240:243], a[196:199], a[100:103]\n\t"
"v_mfma_f32_16x16x32_bf16 a[100:103], a[248:251], a[204:207], a[100:103]\n\t"
"v_mfma_f32_16x16x32_bf16 a[108:111], a[252:255], a[168:171], a[108:111]\n\t"
"v_mfma_f32_16x16x32_bf16 a[108:111], v[240:243], a[176:179], a[108:111]\n\t"
"v_mfma_f32_16x16x32_bf16 a[108:111], a[208:211], a[172:175], a[108:111]\n\t"
"s_waitcnt lgkmcnt(0)\n\t"
"s_barrier\n\t"
"v_mfma_f32_16x16x32_bf16 a[116:119], a[252:255], a[172:175], a[116:119]\n\t"
"ds_read_b128 a[168:171], v244 offset:0\n\t"
"v_mfma_f32_16x16x32_bf16 a[116:119], a[208:211], a[176:179], a[116:119]\n\t"
"ds_read_b128 a[172:175], v244 offset:512\n\t"
"v_mfma_f32_16x16x32_bf16 a[116:119], v[240:243], a[180:183], a[116:119]\n\t"
"v_mfma_f32_16x16x32_bf16 a[124:127], v[240:243], a[184:187], a[124:127]\n\t"
"v_mfma_f32_16x16x32_bf16 a[124:127], a[208:211], a[180:183], a[124:127]\n\t"
"v_mfma_f32_16x16x32_bf16 a[124:127], a[252:255], a[176:179], a[124:127]\n\t"
"ds_read_b128 a[176:179], v244 offset:1024\n\t"
"v_mfma_f32_16x16x32_bf16 a[132:135], a[252:255], a[180:183], a[132:135]\n\t"
"ds_read_b128 a[180:183], v244 offset:1536\n\t"
"v_mfma_f32_16x16x32_bf16 a[132:135], a[208:211], a[184:187], a[132:135]\n\t"
"v_mfma_f32_16x16x32_bf16 a[132:135], v[240:243], a[188:191], a[132:135]\n\t"
"v_mfma_f32_16x16x32_bf16 a[140:143], v[240:243], a[192:195], a[140:143]\n\t"
"v_mfma_f32_16x16x32_bf16 a[140:143], a[208:211], a[188:191], a[140:143]\n\t"
"v_mfma_f32_16x16x32_bf16 a[140:143], a[252:255], a[184:187], a[140:143]\n\t"
"ds_read_b128 a[184:187], v244 offset:2048\n\t"
"v_mfma_f32_16x16x32_bf16 a[148:151], a[252:255], a[188:191], a[148:151]\n\t"
"ds_read_b128 a[188:191], v244 offset:2560\n\t"
"v_mfma_f32_16x16x32_bf16 a[148:151], a[208:211], a[192:195], a[148:151]\n\t"
"v_mfma_f32_16x16x32_bf16 a[148:151], v[240:243], a[196:199], a[148:151]\n\t"
"v_mfma_f32_16x16x32_bf16 a[156:159], v[240:243], a[200:203], a[156:159]\n\t"
"v_mfma_f32_16x16x32_bf16 a[156:159], a[208:211], a[196:199], a[156:159]\n\t"
"v_mfma_f32_16x16x32_bf16 a[156:159], a[252:255], a[192:195], a[156:159]\n\t"
"ds_read_b128 a[192:195], v244 offset:3072\n\t"
"v_mfma_f32_16x16x32_bf16 a[164:167], a[252:255], a[196:199], a[164:167]\n\t"
"s_waitcnt vmcnt(11) lgkmcnt(4)\n\t"
"ds_read_b128 a[196:199], v244 offset:3584\n\t"
"v_mfma_f32_16x16x32_bf16 a[164:167], a[208:211], a[200:203], a[164:167]\n\t"
"ds_read_b128 a[200:203], v244 offset:4096\n\t"
"v_mfma_f32_16x16x32_bf16 a[164:167], v[240:243], a[204:207], a[164:167]\n\t"
"ds_read_b128 a[204:207], v244 offset:4608\n\t"
"Lst7_%=:\n\t"
"v_mfma_f32_16x16x32_bf16 v[0:3], v[248:251], a[168:171], v[0:3]\n\t"
"buffer_load_dwordx4 v[240:243], off, s[80:83], s20 offset:2048 sc1\n\t"
"v_mfma_f32_16x16x32_bf16 v[0:3], a[220:223], a[176:179], v[0:3]\n\t"
"s_waitcnt lgkmcnt(6)\n\t"
"s_mov_b32 s20, 663552\n\t"
"v_mfma_f32_16x16x32_bf16 v[0:3], a[212:215], a[172:175], v[0:3]\n\t"
"v_mfma_f32_16x16x32_bf16 v[4:7], v[248:251], a[172:175], v[4:7]\n\t"
"s_waitcnt lgkmcnt(5)\n\t"
"v_mfma_f32_16x16x32_bf16 v[4:7], a[212:215], a[176:179], v[4:7]\n\t"
"buffer_load_dwordx4 a[208:211], off, s[80:83], s20 offset:0 sc1\n\t"
"v_mfma_f32_16x16x32_bf16 v[4:7], a[220:223], a[180:183], v[4:7]\n\t"
"v_mfma_f32_16x16x32_bf16 v[8:11], a[220:223], a[184:187], v[8:11]\n\t"
"s_waitcnt lgkmcnt(4)\n\t"
"v_mfma_f32_16x16x32_bf16 v[8:11], a[212:215], a[180:183], v[8:11]\n\t"
"v_mfma_f32_16x16x32_bf16 v[8:11], v[248:251], a[176:179], v[8:11]\n\t"
"buffer_load_dwordx4 a[232:235], off, s[80:83], s20 offset:1024 sc1\n\t"
"v_mfma_f32_16x16x32_bf16 v[12:15], v[248:251], a[180:183], v[12:15]\n\t"
"s_waitcnt lgkmcnt(3)\n\t"
"ds_read_b64_tr_b16 v[232:233], v246 offset:1536\n\t"
"v_mfma_f32_16x16x32_bf16 v[12:15], a[212:215], a[184:187], v[12:15]\n\t"
"ds_read_b64_tr_b16 v[234:235], v246 offset:4608\n\t"
"v_mfma_f32_16x16x32_bf16 v[12:15], a[220:223], a[188:191], v[12:15]\n\t"
"ds_read_b64_tr_b16 v[224:225], v246 offset:7680\n\t"
"v_mfma_f32_16x16x32_bf16 v[16:19], a[220:223], a[192:195], v[16:19]\n\t"
"buffer_load_dwordx4 a[236:239], off, s[80:83], s20 offset:2048 sc1\n\t"
"v_mfma_f32_16x16x32_bf16 v[16:19], a[212:215], a[188:191], v[16:19]\n\t"
"s_waitcnt lgkmcnt(5)\n\t"
"ds_read_b64_tr_b16 v[226:227], v246 offset:10752\n\t"
"s_mov_b32 s20, 675840\n\t"
"v_mfma_f32_16x16x32_bf16 v[16:19], v[248:251], a[184:187], v[16:19]\n\t"
"v_mfma_f32_16x16x32_bf16 v[20:23], v[248:251], a[188:191], v[20:23]\n\t"
"s_waitcnt lgkmcnt(5)\n\t"
"v_mfma_f32_16x16x32_bf16 v[20:23], a[212:215], a[192:195], v[20:23]\n\t"
"buffer_load_dwordx4 a[240:243], off, s[80:83], s20 offset:0 sc1\n\t"
"v_mfma_f32_16x16x32_bf16 v[20:23], a[220:223], a[196:199], v[20:23]\n\t"
"v_mfma_f32_16x16x32_bf16 v[24:27], a[220:223], a[200:203], v[24:27]\n\t"
"s_waitcnt lgkmcnt(4)\n\t"
"v_mfma_f32_16x16x32_bf16 v[24:27], a[212:215], a[196:199], v[24:27]\n\t"
"v_mfma_f32_16x16x32_bf16 v[24:27], v[248:251], a[192:195], v[24:27]\n\t"
"buffer_load_dwordx4 a[244:247], off, s[80:83], s20 offset:1024 sc1\n\t"
"v_mfma_f32_16x16x32_bf16 v[28:31], v[248:251], a[196:199], v[28:31]\n\t"
"s_waitcnt lgkmcnt(2)\n\t"
"v_mfma_f32_16x16x32_bf16 v[28:31], a[212:215], a[200:203], v[28:31]\n\t"
"s_waitcnt lgkmcnt(0)\n\t"
"v_mfma_f32_16x16x32_bf16 v[28:31], a[220:223], a[204:207], v[28:31]\n\t"
"s_waitcnt vmcnt(14)\n\t"
"v_mfma_f32_16x16x32_bf16 v[232:235], v[232:235], v[220:223], 0\n\t"
"buffer_load_dwordx4 v[248:251], off, s[80:83], s20 offset:2048 sc1\n\t"
"v_mfma_f32_16x16x32_bf16 v[224:227], v[224:227], v[220:223], 0\n\t"
"s_mov_b32 s20, 688128\n\t"
"v_mfma_f32_16x16x32_bf16 v[32:35], v[236:239], a[168:171], v[32:35]\n\t"
"v_mfma_f32_16x16x32_bf16 v[32:35], v[96:99], a[176:179], v[32:35]\n\t"
"v_mfma_f32_16x16x32_bf16 v[32:35], a[216:219], a[172:175], v[32:35]\n\t"
"buffer_load_dwordx4 a[212:215], off, s[80:83], s20 offset:0 sc1\n\t"
"v_mfma_f32_16x16x32_bf16 v[36:39], v[236:239], a[172:175], v[36:39]\n\t"
"v_cvt_pk_bf16_f32 v232, v232, v233\n\t"
"v_cvt_pk_bf16_f32 v233, v234, v235\n\t"
"v_mfma_f32_16x16x32_bf16 v[36:39], a[216:219], a[176:179], v[36:39]\n\t"
"v_cvt_pk_bf16_f32 v234, v224, v225\n\t"
"v_cvt_pk_bf16_f32 v235, v226, v227\n\t"
"v_mfma_f32_16x16x32_bf16 v[36:39], v[96:99], a[180:183], v[36:39]\n\t"
"v_mfma_f32_16x16x32_bf16 v[40:43], v[96:99], a[184:187], v[40:43]\n\t"
"buffer_load_dwordx4 a[220:223], off, s[80:83], s20 offset:1024 sc1\n\t"
"v_mfma_f32_16x16x32_bf16 v[40:43], a[216:219], a[180:183], v[40:43]\n\t"
"v_mfma_f32_16x16x32_bf16 v[40:43], v[236:239], a[176:179], v[40:43]\n\t"
"ds_read_b64_tr_b16 v[228:229], v246 offset:13824\n\t"
"v_mfma_f32_16x16x32_bf16 v[44:47], v[236:239], a[180:183], v[44:47]\n\t"
"buffer_load_dwordx4 a[248:251], off, s[80:83], s20 offset:2048 sc1\n\t"
"v_mfma_f32_16x16x32_bf16 v[44:47], a[216:219], a[184:187], v[44:47]\n\t"
"s_mov_b32 s20, 921600\n\t"
"ds_read_b64_tr_b16 v[230:231], v246 offset:16896\n\t"
"v_mfma_f32_16x16x32_bf16 v[44:47], v[96:99], a[188:191], v[44:47]\n\t"
"ds_read_b64_tr_b16 v[224:225], v246 offset:21504\n\t"
"v_mfma_f32_16x16x32_bf16 v[48:51], v[96:99], a[192:195], v[48:51]\n\t"
"ds_read_b64_tr_b16 v[226:227], v246 offset:24576\n\t"
"v_mfma_f32_16x16x32_bf16 v[48:51], a[216:219], a[188:191], v[48:51]\n\t"
"buffer_load_dwordx4 a[252:255], off, s[80:83], s20 offset:0 sc1\n\t"
"v_mfma_f32_16x16x32_bf16 v[48:51], v[236:239], a[184:187], v[48:51]\n\t"
"v_mfma_f32_16x16x32_bf16 v[52:55], v[236:239], a[188:191], v[52:55]\n\t"
"v_mfma_f32_16x16x32_bf16 v[52:55], a[216:219], a[192:195], v[52:55]\n\t"
"v_mfma_f32_16x16x32_bf16 v[52:55], v[96:99], a[196:199], v[52:55]\n\t"
"v_mfma_f32_16x16x32_bf16 v[56:59], v[96:99], a[200:203], v[56:59]\n\t"
"v_mfma_f32_16x16x32_bf16 v[56:59], a[216:219], a[196:199], v[56:59]\n\t"
"v_mfma_f32_16x16x32_bf16 v[56:59], v[236:239], a[192:195], v[56:59]\n\t"
"v_mfma_f32_16x16x32_bf16 v[60:63], v[236:239], a[196:199], v[60:63]\n\t"
"buffer_load_dwordx4 v[236:239], off, s[80:83], s20 offset:1024 sc1\n\t"
"v_mfma_f32_16x16x32_bf16 v[60:63], a[216:219], a[200:203], v[60:63]\n\t"
"s_waitcnt lgkmcnt(0)\n\t"
"v_mfma_f32_16x16x32_bf16 v[60:63], v[96:99], a[204:207], v[60:63]\n\t"
"s_waitcnt vmcnt(11)\n\t"
"v_mfma_f32_16x16x32_bf16 v[228:231], v[228:231], v[220:223], 0\n\t"
"buffer_load_dwordx4 v[96:99], off, s[80:83], s20 offset:2048 sc1\n\t"
"v_mfma_f32_16x16x32_bf16 v[224:227], v[224:227], v[220:223], 0\n\t"
"s_mov_b32 s20, 933888\n\t"
"v_mfma_f32_16x16x32_bf16 v[64:67], a[224:227], a[168:171], v[64:67]\n\t"
"ds_read_b128 a[168:171], v244 offset:9216\n\t"
"v_mfma_f32_16x16x32_bf16 v[64:67], v[240:243], a[176:179], v[64:67]\n\t"
"buffer_load_dwordx4 a[216:219], off, s[80:83], s20 offset:0 sc1\n\t"
"v_mfma_f32_16x16x32_bf16 v[64:67], a[228:231], a[172:175], v[64:67]\n\t"
"v_mfma_f32_16x16x32_bf16 v[68:71], a[224:227], a[172:175], v[68:71]\n\t"
"ds_read_b128 a[172:175], v244 offset:9728\n\t"
"v_cvt_pk_bf16_f32 v228, v228, v229\n\t"
"v_mfma_f32_16x16x32_bf16 v[68:71], a[228:231], a[176:179], v[68:71]\n\t"
"v_cvt_pk_bf16_f32 v229, v230, v231\n\t"
"v_cvt_pk_bf16_f32 v230, v224, v225\n\t"
"v_mfma_f32_16x16x32_bf16 v[68:71], v[240:243], a[180:183], v[68:71]\n\t"
"v_cvt_pk_bf16_f32 v231, v226, v227\n\t"
"v_mfma_f32_16x16x32_bf16 v[72:75], v[240:243], a[184:187], v[72:75]\n\t"
"v_mfma_f32_16x16x32_bf16 v[72:75], a[228:231], a[180:183], v[72:75]\n\t"
"v_mfma_f32_16x16x32_bf16 v[72:75], a[224:227], a[176:179], v[72:75]\n\t"
"ds_read_b128 a[176:179], v244 offset:10240\n\t"
"v_mfma_f32_16x16x32_bf16 v[76:79], a[224:227], a[180:183], v[76:79]\n\t"
"ds_read_b128 a[180:183], v244 offset:10752\n\t"
"v_mfma_f32_16x16x32_bf16 v[76:79], a[228:231], a[184:187], v[76:79]\n\t"
"ds_read_b64_tr_b16 v[224:225], v246 offset:27648\n\t"
"v_mfma_f32_16x16x32_bf16 v[76:79], v[240:243], a[188:191], v[76:79]\n\t"
"ds_read_b64_tr_b16 v[226:227], v246 offset:30720\n\t"
"v_mfma_f32_16x16x32_bf16 v[80:83], v[240:243], a[192:195], v[80:83]\n\t"
"v_mfma_f32_16x16x32_bf16 v[80:83], a[228:231], a[188:191], v[80:83]\n\t"
"v_mfma_f32_16x16x32_bf16 v[80:83], a[224:227], a[184:187], v[80:83]\n\t"
"ds_read_b128 a[184:187], v244 offset:11264\n\t"
"v_mfma_f32_16x16x32_bf16 v[84:87], a[224:227], a[188:191], v[84:87]\n\t"
"ds_read_b128 a[188:191], v244 offset:11776\n\t"
"v_mfma_f32_16x16x32_bf16 v[84:87], a[228:231], a[192:195], v[84:87]\n\t"
"v_mfma_f32_16x16x32_bf16 v[84:87], v[240:243], a[196:199], v[84:87]\n\t"
"v_mfma_f32_16x16x32_bf16 v[88:91], v[240:243], a[200:203], v[88:91]\n\t"
"v_mfma_f32_16x16x32_bf16 v[88:91], a[228:231], a[196:199], v[88:91]\n\t"
"v_mfma_f32_16x16x32_bf16 v[88:91], a[224:227], a[192:195], v[88:91]\n\t"
"ds_read_b128 a[192:195], v244 offset:12288\n\t"
"v_mfma_f32_16x16x32_bf16 v[92:95], a[224:227], a[196:199], v[92:95]\n\t"
"buffer_load_dwordx4 a[224:227], off, s[80:83], s20 offset:1024 sc1\n\t"
"v_mfma_f32_16x16x32_bf16 v[92:95], a[228:231], a[200:203], v[92:95]\n\t"
"s_waitcnt vmcnt(11) lgkmcnt(3)\n\t"
"ds_read_b128 a[196:199], v244 offset:12800\n\t"
"v_mfma_f32_16x16x32_bf16 v[92:95], v[240:243], a[204:207], v[92:95]\n\t"
"ds_read_b128 a[200:203], v244 offset:13312\n\t"
"v_mfma_f32_16x16x32_bf16 v[224:227], v[224:227], v[220:223], 0\n\t"
"ds_read_b128 a[204:207], v244 offset:13824\n\t"
"v_mfma_f32_16x16x32_bf16 v[100:103], a[208:211], a[168:171], v[100:103]\n\t"
"buffer_load_dwordx4 v[240:243], off, s[80:83], s20 offset:2048 sc1\n\t"
"v_mfma_f32_16x16x32_bf16 v[100:103], a[236:239], a[176:179], v[100:103]\n\t"
"s_mov_b32 s20, 946176\n\t"
"v_mfma_f32_16x16x32_bf16 v[100:103], a[232:235], a[172:175], v[100:103]\n\t"
"buffer_load_dwordx4 a[228:231], off, s[80:83], s20 offset:0 sc1\n\t"
"v_mfma_f32_16x16x32_bf16 v[104:107], a[208:211], a[172:175], v[104:107]\n\t"
"s_waitcnt lgkmcnt(5)\n\t"
"v_mfma_f32_16x16x32_bf16 v[104:107], a[232:235], a[176:179], v[104:107]\n\t"
"v_cvt_pk_bf16_f32 v224, v224, v225\n\t"
"v_mfma_f32_16x16x32_bf16 v[104:107], a[236:239], a[180:183], v[104:107]\n\t"
"s_waitcnt lgkmcnt(0)\n\t"
"s_barrier\n\t"
"v_mfma_f32_16x16x32_bf16 v[108:111], a[236:239], a[184:187], v[108:111]\n\t"
"v_cvt_pk_bf16_f32 v225, v226, v227\n\t"
"v_mfma_f32_16x16x32_bf16 v[108:111], a[232:235], a[180:183], v[108:111]\n\t"
"v_mfma_f32_16x16x32_bf16 v[108:111], a[208:211], a[176:179], v[108:111]\n\t"
"ds_write2st64_b64 v245, v[232:233], v[234:235] offset0:0 offset1:1\n\t"
"v_mfma_f32_16x16x32_bf16 v[112:115], a[208:211], a[180:183], v[112:115]\n\t"
"v_mfma_f32_16x16x32_bf16 v[112:115], a[232:235], a[184:187], v[112:115]\n\t"
"ds_read_b64_tr_b16 v[232:233], v246 offset:33792\n\t"
"v_mfma_f32_16x16x32_bf16 v[112:115], a[236:239], a[188:191], v[112:115]\n\t"
"ds_write2st64_b64 v245, v[228:229], v[230:231] offset0:2 offset1:3\n\t"
"v_mfma_f32_16x16x32_bf16 v[116:119], a[236:239], a[192:195], v[116:119]\n\t"
"v_mfma_f32_16x16x32_bf16 v[116:119], a[232:235], a[188:191], v[116:119]\n\t"
"ds_read_b64_tr_b16 v[234:235], v246 offset:36864\n\t"
"v_mfma_f32_16x16x32_bf16 v[116:119], a[208:211], a[184:187], v[116:119]\n\t"
"s_add_u32 m0, s24, 66304\n\t"
"s_add_u32 s28, s21, 1966080\n\t"
"ds_write_b64 v245, v[224:225] offset:2048\n\t"
"v_mfma_f32_16x16x32_bf16 v[120:123], a[208:211], a[188:191], v[120:123]\n\t"
"ds_read_b64_tr_b16 v[228:229], v246 offset:41472\n\t"
"v_mfma_f32_16x16x32_bf16 v[120:123], a[232:235], a[192:195], v[120:123]\n\t"
"ds_read_b64_tr_b16 v[230:231], v246 offset:44544\n\t"
"v_mfma_f32_16x16x32_bf16 v[120:123], a[236:239], a[196:199], v[120:123]\n\t"
"ds_read_b64_tr_b16 v[224:225], v246 offset:47616\n\t"
"v_mfma_f32_16x16x32_bf16 v[124:127], a[232:235], a[196:199], v[124:127]\n\t"
"s_waitcnt lgkmcnt(4)\n\t"
"ds_read_b64_tr_b16 v[226:227], v246 offset:50688\n\t"
"v_mfma_f32_16x16x32_bf16 v[124:127], a[208:211], a[192:195], v[124:127]\n\t"
"buffer_load_dwordx4 v247, s[12:15], s28 offen offset:256 nt lds\n\t"
"v_mfma_f32_16x16x32_bf16 v[124:127], a[236:239], a[200:203], v[124:127]\n\t"
"s_add_u32 m0, s24, 72448\n\t"
"s_add_u32 s28, s21, 2162688\n\t"
"v_mfma_f32_16x16x32_bf16 v[232:235], v[232:235], v[220:223], 0\n\t"
"s_waitcnt lgkmcnt(2)\n\t"
"buffer_load_dwordx4 v247, s[12:15], s28 offen offset:256 nt lds\n\t"
"s_add_u32 m0, s24, 80128\n\t"
"s_add_u32 s28, s21, 2359296\n\t"
"v_mfma_f32_16x16x32_bf16 v[228:231], v[228:231], v[220:223], 0\n\t"
"buffer_load_dwordx4 v247, s[12:15], s28 offen offset:256 nt lds\n\t"
"v_mfma_f32_16x16x32_bf16 v[128:131], a[232:235], a[200:203], v[128:131]\n\t"
"s_waitcnt lgkmcnt(0)\n\t"
"s_add_u32 m0, s24, 86272\n\t"
"s_add_u32 s28, s21, 2555904\n\t"
"v_mfma_f32_16x16x32_bf16 v[128:131], a[208:211], a[196:199], v[128:131]\n\t"
"buffer_load_dwordx4 v247, s[12:15], s28 offen offset:256 nt lds\n\t"
"v_mfma_f32_16x16x32_bf16 v[128:131], a[236:239], a[204:207], v[128:131]\n\t"
"s_waitcnt vmcnt(14)\n\t"
"s_add_u32 m0, s24, 32512\n\t"
"s_add_u32 s28, s21, 983040\n\t"
"v_mfma_f32_16x16x32_bf16 v[224:227], v[224:227], v[220:223], 0\n\t"
"buffer_load_dwordx4 v247, s[12:15], s28 offen offset:256 nt lds\n\t"
"v_mfma_f32_16x16x32_bf16 v[132:135], a[240:243], a[168:171], v[132:135]\n\t"
"s_add_u32 m0, s24, 40192\n\t"
"s_add_u32 s28, s21, 1179648\n\t"
"v_cvt_pk_bf16_f32 v232, v232, v233\n\t"
"v_mfma_f32_16x16x32_bf16 v[132:135], v[248:251], a[176:179], v[132:135]\n\t"
"v_cvt_pk_bf16_f32 v233, v234, v235\n\t"
"v_cvt_pk_bf16_f32 v234, v228, v229\n\t"
"v_mfma_f32_16x16x32_bf16 v[132:135], a[244:247], a[172:175], v[132:135]\n\t"
"buffer_load_dwordx4 v247, s[12:15], s28 offen offset:256 nt lds\n\t"
"v_mfma_f32_16x16x32_bf16 v[136:139], a[240:243], a[172:175], v[136:139]\n\t"
"v_cvt_pk_bf16_f32 v235, v230, v231\n\t"
"v_mfma_f32_16x16x32_bf16 v[136:139], a[244:247], a[176:179], v[136:139]\n\t"
"ds_read_b64_tr_b16 v[228:229], v246 offset:53760\n\t"
"v_cvt_pk_bf16_f32 v224, v224, v225\n\t"
"v_mfma_f32_16x16x32_bf16 v[136:139], v[248:251], a[180:183], v[136:139]\n\t"
"ds_write2st64_b64 v245, v[232:233], v[234:235] offset0:5 offset1:6\n\t"
"v_mfma_f32_16x16x32_bf16 v[140:143], v[248:251], a[184:187], v[140:143]\n\t"
"Lpg6_%=:\n\t"
"s_cbranch_vccnz Lpg10_%=\n\t"
"v_cvt_pk_bf16_f32 v225, v226, v227\n\t"
"v_mfma_f32_16x16x32_bf16 v[140:143], a[244:247], a[180:183], v[140:143]\n\t"
"ds_read_b64_tr_b16 v[230:231], v246 offset:56832\n\t"
"v_mfma_f32_16x16x32_bf16 v[140:143], a[240:243], a[176:179], v[140:143]\n\t"
"v_mfma_f32_16x16x32_bf16 v[144:147], a[240:243], a[180:183], v[144:147]\n\t"
"v_mfma_f32_16x16x32_bf16 v[144:147], a[244:247], a[184:187], v[144:147]\n\t"
"v_mfma_f32_16x16x32_bf16 v[144:147], v[248:251], a[188:191], v[144:147]\n\t"
"v_mfma_f32_16x16x32_bf16 v[148:151], a[244:247], a[188:191], v[148:151]\n\t"
"s_waitcnt lgkmcnt(0)\n\t"
"v_mfma_f32_16x16x32_bf16 v[148:151], v[248:251], a[192:195], v[148:151]\n\t"
"v_mfma_f32_16x16x32_bf16 v[148:151], a[240:243], a[184:187], v[148:151]\n\t"
"v_mfma_f32_16x16x32_bf16 v[228:231], v[228:231], v[220:223], 0\n\t"
"v_mfma_f32_16x16x32_bf16 v[152:155], a[240:243], a[188:191], v[152:155]\n\t"
"v_mfma_f32_16x16x32_bf16 v[152:155], v[248:251], a[196:199], v[152:155]\n\t"
"v_mfma_f32_16x16x32_bf16 v[152:155], a[244:247], a[192:195], v[152:155]\n\t"
"v_mfma_f32_16x16x32_bf16 v[156:159], a[240:243], a[192:195], v[156:159]\n\t"
"v_mfma_f32_16x16x32_bf16 v[156:159], a[244:247], a[196:199], v[156:159]\n\t"
"v_cvt_pk_bf16_f32 v226, v228, v229\n\t"
"v_cvt_pk_bf16_f32 v227, v230, v231\n\t"
"v_mfma_f32_16x16x32_bf16 v[156:159], v[248:251], a[200:203], v[156:159]\n\t"
"ds_write2st64_b64 v245, v[224:225], v[226:227] offset0:7 offset1:8\n\t"
"v_mfma_f32_16x16x32_bf16 v[160:163], a[244:247], a[200:203], v[160:163]\n\t"
"s_waitcnt vmcnt(13)\n\t"
"v_mfma_f32_16x16x32_bf16 v[160:163], a[240:243], a[196:199], v[160:163]\n\t"
"v_mfma_f32_16x16x32_bf16 v[160:163], v[248:251], a[204:207], v[160:163]\n\t"
"v_mfma_f32_16x16x32_bf16 v[164:167], a[212:215], a[168:171], v[164:167]\n\t"
"v_mfma_f32_16x16x32_bf16 v[164:167], a[248:251], a[176:179], v[164:167]\n\t"
"v_mfma_f32_16x16x32_bf16 v[164:167], a[220:223], a[172:175], v[164:167]\n\t"
"s_waitcnt lgkmcnt(0)\n\t"
"s_barrier\n\t"
"v_mfma_f32_16x16x32_bf16 v[168:171], a[212:215], a[172:175], v[168:171]\n\t"
"ds_read_b128 a[168:171], v244 offset:0\n\t"
"v_mfma_f32_16x16x32_bf16 v[168:171], a[220:223], a[176:179], v[168:171]\n\t"
"buffer_load_dwordx4 a[208:211], off, s[80:83], s20 offset:1024 sc1\n\t"
"v_mfma_f32_16x16x32_bf16 v[168:171], a[248:251], a[180:183], v[168:171]\n\t"
"ds_read_b128 a[172:175], v244 offset:512\n\t"
"v_mfma_f32_16x16x32_bf16 v[172:175], a[248:251], a[184:187], v[172:175]\n\t"
"v_mfma_f32_16x16x32_bf16 v[172:175], a[220:223], a[180:183], v[172:175]\n\t"
"v_mfma_f32_16x16x32_bf16 v[172:175], a[212:215], a[176:179], v[172:175]\n\t"
"ds_read_b128 a[176:179], v244 offset:1024\n\t"
"v_mfma_f32_16x16x32_bf16 v[176:179], a[212:215], a[180:183], v[176:179]\n\t"
"ds_read_b128 a[180:183], v244 offset:1536\n\t"
"v_mfma_f32_16x16x32_bf16 v[176:179], a[220:223], a[184:187], v[176:179]\n\t"
"v_mfma_f32_16x16x32_bf16 v[176:179], a[248:251], a[188:191], v[176:179]\n\t"
"v_mfma_f32_16x16x32_bf16 v[180:183], a[248:251], a[192:195], v[180:183]\n\t"
"v_mfma_f32_16x16x32_bf16 v[180:183], a[220:223], a[188:191], v[180:183]\n\t"
"v_mfma_f32_16x16x32_bf16 v[180:183], a[212:215], a[184:187], v[180:183]\n\t"
"ds_read_b128 a[184:187], v244 offset:2048\n\t"
"v_mfma_f32_16x16x32_bf16 v[184:187], a[212:215], a[188:191], v[184:187]\n\t"
"ds_read_b128 a[188:191], v244 offset:2560\n\t"
"v_mfma_f32_16x16x32_bf16 v[184:187], a[220:223], a[192:195], v[184:187]\n\t"
"v_mfma_f32_16x16x32_bf16 v[184:187], a[248:251], a[196:199], v[184:187]\n\t"
"v_mfma_f32_16x16x32_bf16 v[188:191], a[248:251], a[200:203], v[188:191]\n\t"
"v_xor_b32 v246, 0xc0, v246\n\t"
"v_mfma_f32_16x16x32_bf16 v[188:191], a[220:223], a[196:199], v[188:191]\n\t"
"v_mfma_f32_16x16x32_bf16 v[188:191], a[212:215], a[192:195], v[188:191]\n\t"
"ds_read_b128 a[192:195], v244 offset:3072\n\t"
"v_mfma_f32_16x16x32_bf16 v[192:195], a[212:215], a[196:199], v[192:195]\n\t"
"s_waitcnt vmcnt(11) lgkmcnt(4)\n\t"
"ds_read_b128 a[196:199], v244 offset:3584\n\t"
"v_mfma_f32_16x16x32_bf16 v[192:195], a[220:223], a[200:203], v[192:195]\n\t"
"ds_read_b128 a[200:203], v244 offset:4096\n\t"
"v_mfma_f32_16x16x32_bf16 v[192:195], a[248:251], a[204:207], v[192:195]\n\t"
"ds_read_b128 a[204:207], v244 offset:4608\n\t"
"Lst8_%=:\n\t"
"v_mfma_f32_16x16x32_bf16 v[0:3], a[252:255], a[168:171], v[0:3]\n\t"
"s_mov_b64 vcc, -1\n\t"
"s_bitcmp1_b32 s73, 1\n\t"
"s_cbranch_scc1 Lvh1_%=\n\t"
"s_bitcmp1_b32 s73, 0\n\t"
"s_cbranch_scc1 Lpg14_%=\n\t"
"s_branch Lpg13_%=\n\t"
"Lvh1_%=:\n\t"
"s_bitcmp1_b32 s73, 0\n\t"
"s_cbranch_scc1 Lpg16_%=\n\t"
"s_branch Lpg15_%=\n\t"
"Lvr1_%=:\n\t"
"s_mov_b64 vcc, 0\n\t"
"s_waitcnt lgkmcnt(6)\n\t"
"v_mfma_f32_16x16x32_bf16 v[0:3], v[96:99], a[176:179], v[0:3]\n\t"
"v_mfma_f32_16x16x32_bf16 v[0:3], v[236:239], a[172:175], v[0:3]\n\t"
"v_mfma_f32_16x16x32_bf16 v[4:7], a[252:255], a[172:175], v[4:7]\n\t"
"s_waitcnt lgkmcnt(5)\n\t"
"v_mfma_f32_16x16x32_bf16 v[4:7], v[236:239], a[176:179], v[4:7]\n\t"
"v_mfma_f32_16x16x32_bf16 v[4:7], v[96:99], a[180:183], v[4:7]\n\t"
"v_mfma_f32_16x16x32_bf16 v[8:11], v[96:99], a[184:187], v[8:11]\n\t"
"s_waitcnt lgkmcnt(4)\n\t"
"v_mfma_f32_16x16x32_bf16 v[8:11], v[236:239], a[180:183], v[8:11]\n\t"
"v_mfma_f32_16x16x32_bf16 v[8:11], a[252:255], a[176:179], v[8:11]\n\t"
"ds_read_b64_tr_b16 v[232:233], v246 offset:1536\n\t"
"v_mfma_f32_16x16x32_bf16 v[12:15], a[252:255], a[180:183], v[12:15]\n\t"
"s_waitcnt lgkmcnt(4)\n\t"
"ds_read_b64_tr_b16 v[234:235], v246 offset:4608\n\t"
"v_mfma_f32_16x16x32_bf16 v[12:15], v[236:239], a[184:187], v[12:15]\n\t"
"ds_read_b64_tr_b16 v[228:229], v246 offset:7680\n\t"
"v_mfma_f32_16x16x32_bf16 v[12:15], v[96:99], a[188:191], v[12:15]\n\t"
"buffer_load_dwordx4 v[248:251], off, s[80:83], s20 offset:2048 sc1\n\t"
"v_mfma_f32_16x16x32_bf16 v[16:19], v[96:99], a[192:195], v[16:19]\n\t"
"s_waitcnt lgkmcnt(5)\n\t"
"ds_read_b64_tr_b16 v[230:231], v246 offset:10752\n\t"
"s_mov_b32 s20, 958464\n\t"
"v_mfma_f32_16x16x32_bf16 v[16:19], v[236:239], a[188:191], v[16:19]\n\t"
"v_mfma_f32_16x16x32_bf16 v[16:19], a[252:255], a[184:187], v[16:19]\n\t"
"v_mfma_f32_16x16x32_bf16 v[20:23], a[252:255], a[188:191], v[20:23]\n\t"
"buffer_load_dwordx4 a[212:215], off, s[80:83], s20 offset:0 sc1\n\t"
"v_mfma_f32_16x16x32_bf16 v[20:23], v[236:239], a[192:195], v[20:23]\n\t"
"s_waitcnt lgkmcnt(5)\n\t"
"v_mfma_f32_16x16x32_bf16 v[20:23], v[96:99], a[196:199], v[20:23]\n\t"
"v_mfma_f32_16x16x32_bf16 v[24:27], v[96:99], a[200:203], v[24:27]\n\t"
"buffer_load_dwordx4 a[220:223], off, s[80:83], s20 offset:1024 sc1\n\t"
"v_mfma_f32_16x16x32_bf16 v[24:27], v[236:239], a[196:199], v[24:27]\n\t"
"s_waitcnt lgkmcnt(4)\n\t"
"v_mfma_f32_16x16x32_bf16 v[24:27], a[252:255], a[192:195], v[24:27]\n\t"
"v_mfma_f32_16x16x32_bf16 v[28:31], a[252:255], a[196:199], v[28:31]\n\t"
"s_waitcnt lgkmcnt(2)\n\t"
"v_mfma_f32_16x16x32_bf16 v[28:31], v[236:239], a[200:203], v[28:31]\n\t"
"buffer_load_dwordx4 v[236:239], off, s[80:83], s20 offset:2048 sc1\n\t"
"v_mfma_f32_16x16x32_bf16 v[28:31], v[96:99], a[204:207], v[28:31]\n\t"
"s_waitcnt vmcnt(12) lgkmcnt(0)\n\t"
"s_mov_b32 s20, 970752\n\t"
"v_mfma_f32_16x16x32_bf16 v[232:235], v[232:235], v[220:223], 0\n\t"
"v_mfma_f32_16x16x32_bf16 v[228:231], v[228:231], v[220:223], 0\n\t"
"v_mfma_f32_16x16x32_bf16 v[32:35], a[216:219], a[168:171], v[32:35]\n\t"
"buffer_load_dwordx4 v[96:99], off, s[80:83], s20 offset:0 sc1\n\t"
"v_mfma_f32_16x16x32_bf16 v[32:35], v[240:243], a[176:179], v[32:35]\n\t"
"v_mfma_f32_16x16x32_bf16 v[32:35], a[224:227], a[172:175], v[32:35]\n\t"
"v_mfma_f32_16x16x32_bf16 v[36:39], a[216:219], a[172:175], v[36:39]\n\t"
"v_cvt_pk_bf16_f32 v232, v232, v233\n\t"
"v_cvt_pk_bf16_f32 v233, v234, v235\n\t"
"v_mfma_f32_16x16x32_bf16 v[36:39], a[224:227], a[176:179], v[36:39]\n\t"
"v_cvt_pk_bf16_f32 v234, v228, v229\n\t"
"v_cvt_pk_bf16_f32 v235, v230, v231\n\t"
"v_mfma_f32_16x16x32_bf16 v[36:39], v[240:243], a[180:183], v[36:39]\n\t"
"v_mfma_f32_16x16x32_bf16 v[40:43], v[240:243], a[184:187], v[40:43]\n\t"
"buffer_load_dwordx4 a[232:235], off, s[80:83], s20 offset:1024 sc1\n\t"
"v_mfma_f32_16x16x32_bf16 v[40:43], a[224:227], a[180:183], v[40:43]\n\t"
"v_mfma_f32_16x16x32_bf16 v[40:43], a[216:219], a[176:179], v[40:43]\n\t"
"buffer_load_dwordx4 a[236:239], off, s[80:83], s20 offset:2048 sc1\n\t"
"v_mfma_f32_16x16x32_bf16 v[44:47], a[216:219], a[180:183], v[44:47]\n\t"
"s_mov_b32 s20, 983040\n\t"
"ds_read_b64_tr_b16 v[224:225], v246 offset:13824\n\t"
"v_mfma_f32_16x16x32_bf16 v[44:47], a[224:227], a[184:187], v[44:47]\n\t"
"ds_read_b64_tr_b16 v[226:227], v246 offset:16896\n\t"
"v_mfma_f32_16x16x32_bf16 v[44:47], v[240:243], a[188:191], v[44:47]\n\t"
"ds_read_b64_tr_b16 v[228:229], v246 offset:21504\n\t"
"v_mfma_f32_16x16x32_bf16 v[48:51], v[240:243], a[192:195], v[48:51]\n\t"
"buffer_load_dwordx4 a[240:243], off, s[80:83], s20 offset:0 sc1\n\t"
"v_mfma_f32_16x16x32_bf16 v[48:51], a[224:227], a[188:191], v[48:51]\n\t"
"ds_read_b64_tr_b16 v[230:231], v246 offset:24576\n\t"
"v_mfma_f32_16x16x32_bf16 v[48:51], a[216:219], a[184:187], v[48:51]\n\t"
"v_mfma_f32_16x16x32_bf16 v[52:55], a[216:219], a[188:191], v[52:55]\n\t"
"v_mfma_f32_16x16x32_bf16 v[52:55], a[224:227], a[192:195], v[52:55]\n\t"
"buffer_load_dwordx4 a[244:247], off, s[80:83], s20 offset:1024 sc1\n\t"
"v_mfma_f32_16x16x32_bf16 v[52:55], v[240:243], a[196:199], v[52:55]\n\t"
"v_mfma_f32_16x16x32_bf16 v[56:59], v[240:243], a[200:203], v[56:59]\n\t"
"v_mfma_f32_16x16x32_bf16 v[56:59], a[224:227], a[196:199], v[56:59]\n\t"
"v_mfma_f32_16x16x32_bf16 v[56:59], a[216:219], a[192:195], v[56:59]\n\t"
"buffer_load_dwordx4 a[248:251], off, s[80:83], s20 offset:2048 sc1\n\t"
"v_mfma_f32_16x16x32_bf16 v[60:63], a[216:219], a[196:199], v[60:63]\n\t"
"s_waitcnt lgkmcnt(2)\n\t"
"s_mov_b32 s20, 995328\n\t"
"v_mfma_f32_16x16x32_bf16 v[60:63], a[224:227], a[200:203], v[60:63]\n\t"
"s_waitcnt lgkmcnt(0)\n\t"
"v_mfma_f32_16x16x32_bf16 v[60:63], v[240:243], a[204:207], v[60:63]\n\t"
"s_waitcnt vmcnt(9)\n\t"
"v_mfma_f32_16x16x32_bf16 v[224:227], v[224:227], v[220:223], 0\n\t"
"buffer_load_dwordx4 a[216:219], off, s[80:83], s20 offset:0 sc1\n\t"
"v_mfma_f32_16x16x32_bf16 v[228:231], v[228:231], v[220:223], 0\n\t"
"v_mfma_f32_16x16x32_bf16 v[64:67], a[228:231], a[168:171], v[64:67]\n\t"
"ds_read_b128 a[168:171], v244 offset:9216\n\t"
"v_mfma_f32_16x16x32_bf16 v[64:67], v[248:251], a[176:179], v[64:67]\n\t"
"buffer_load_dwordx4 a[224:227], off, s[80:83], s20 offset:1024 sc1\n\t"
"v_mfma_f32_16x16x32_bf16 v[64:67], a[208:211], a[172:175], v[64:67]\n\t"
"v_mfma_f32_16x16x32_bf16 v[68:71], a[228:231], a[172:175], v[68:71]\n\t"
"ds_read_b128 a[172:175], v244 offset:9728\n\t"
"v_cvt_pk_bf16_f32 v224, v224, v225\n\t"
"v_mfma_f32_16x16x32_bf16 v[68:71], a[208:211], a[176:179], v[68:71]\n\t"
"v_cvt_pk_bf16_f32 v225, v226, v227\n\t"
"v_cvt_pk_bf16_f32 v226, v228, v229\n\t"
"v_mfma_f32_16x16x32_bf16 v[68:71], v[248:251], a[180:183], v[68:71]\n\t"
"v_cvt_pk_bf16_f32 v227, v230, v231\n\t"
"v_mfma_f32_16x16x32_bf16 v[72:75], v[248:251], a[184:187], v[72:75]\n\t"
"buffer_load_dwordx4 a[252:255], off, s[80:83], s20 offset:2048 sc1\n\t"
"v_mfma_f32_16x16x32_bf16 v[72:75], a[208:211], a[180:183], v[72:75]\n\t"
"v_mfma_f32_16x16x32_bf16 v[72:75], a[228:231], a[176:179], v[72:75]\n\t"
"ds_read_b128 a[176:179], v244 offset:10240\n\t"
"v_mfma_f32_16x16x32_bf16 v[76:79], a[228:231], a[180:183], v[76:79]\n\t"
"ds_read_b128 a[180:183], v244 offset:10752\n\t"
"v_mfma_f32_16x16x32_bf16 v[76:79], a[208:211], a[184:187], v[76:79]\n\t"
"ds_read_b64_tr_b16 v[228:229], v246 offset:27648\n\t"
"v_mfma_f32_16x16x32_bf16 v[76:79], v[248:251], a[188:191], v[76:79]\n\t"
"ds_read_b64_tr_b16 v[230:231], v246 offset:30720\n\t"
"v_mfma_f32_16x16x32_bf16 v[80:83], v[248:251], a[192:195], v[80:83]\n\t"
"v_mfma_f32_16x16x32_bf16 v[80:83], a[208:211], a[188:191], v[80:83]\n\t"
"v_mfma_f32_16x16x32_bf16 v[80:83], a[228:231], a[184:187], v[80:83]\n\t"
"ds_read_b128 a[184:187], v244 offset:11264\n\t"
"v_mfma_f32_16x16x32_bf16 v[84:87], a[228:231], a[188:191], v[84:87]\n\t"
"s_mov_b32 s20, 1007616\n\t"
"ds_read_b128 a[188:191], v244 offset:11776\n\t"
"v_mfma_f32_16x16x32_bf16 v[84:87], a[208:211], a[192:195], v[84:87]\n\t"
"v_mfma_f32_16x16x32_bf16 v[84:87], v[248:251], a[196:199], v[84:87]\n\t"
"v_mfma_f32_16x16x32_bf16 v[88:91], v[248:251], a[200:203], v[88:91]\n\t"
"v_mfma_f32_16x16x32_bf16 v[88:91], a[208:211], a[196:199], v[88:91]\n\t"
"v_mfma_f32_16x16x32_bf16 v[88:91], a[228:231], a[192:195], v[88:91]\n\t"
"ds_read_b128 a[192:195], v244 offset:12288\n\t"
"v_mfma_f32_16x16x32_bf16 v[92:95], a[228:231], a[196:199], v[92:95]\n\t"
"buffer_load_dwordx4 a[228:231], off, s[80:83], s20 offset:0 sc1\n\t"
"v_mfma_f32_16x16x32_bf16 v[92:95], a[208:211], a[200:203], v[92:95]\n\t"
"s_waitcnt vmcnt(10) lgkmcnt(3)\n\t"
"ds_read_b128 a[196:199], v244 offset:12800\n\t"
"v_mfma_f32_16x16x32_bf16 v[92:95], v[248:251], a[204:207], v[92:95]\n\t"
"ds_read_b128 a[200:203], v244 offset:13312\n\t"
"v_mfma_f32_16x16x32_bf16 v[228:231], v[228:231], v[220:223], 0\n\t"
"ds_read_b128 a[204:207], v244 offset:13824\n\t"
"v_mfma_f32_16x16x32_bf16 v[100:103], a[212:215], a[168:171], v[100:103]\n\t"
"buffer_load_dwordx4 v[248:251], off, s[80:83], s20 offset:1024 sc1\n\t"
"v_mfma_f32_16x16x32_bf16 v[100:103], v[236:239], a[176:179], v[100:103]\n\t"
"v_mfma_f32_16x16x32_bf16 v[100:103], a[220:223], a[172:175], v[100:103]\n\t"
"v_mfma_f32_16x16x32_bf16 v[104:107], a[212:215], a[172:175], v[104:107]\n\t"
"s_waitcnt lgkmcnt(5)\n\t"
"v_mfma_f32_16x16x32_bf16 v[104:107], a[220:223], a[176:179], v[104:107]\n\t"
"v_cvt_pk_bf16_f32 v228, v228, v229\n\t"
"v_mfma_f32_16x16x32_bf16 v[104:107], v[236:239], a[180:183], v[104:107]\n\t"
"s_waitcnt lgkmcnt(0)\n\t"
"s_barrier\n\t"
"v_mfma_f32_16x16x32_bf16 v[108:111], v[236:239], a[184:187], v[108:111]\n\t"
"v_cvt_pk_bf16_f32 v229, v230, v231\n\t"
"v_mfma_f32_16x16x32_bf16 v[108:111], a[220:223], a[180:183], v[108:111]\n\t"
"v_mfma_f32_16x16x32_bf16 v[108:111], a[212:215], a[176:179], v[108:111]\n\t"
"ds_write2st64_b64 v245, v[232:233], v[234:235] offset0:0 offset1:1\n\t"
"v_mfma_f32_16x16x32_bf16 v[112:115], a[212:215], a[180:183], v[112:115]\n\t"
"v_mfma_f32_16x16x32_bf16 v[112:115], a[220:223], a[184:187], v[112:115]\n\t"
"ds_read_b64_tr_b16 v[232:233], v246 offset:33792\n\t"
"v_mfma_f32_16x16x32_bf16 v[112:115], v[236:239], a[188:191], v[112:115]\n\t"
"ds_write2st64_b64 v245, v[224:225], v[226:227] offset0:2 offset1:3\n\t"
"v_mfma_f32_16x16x32_bf16 v[116:119], v[236:239], a[192:195], v[116:119]\n\t"
"v_mfma_f32_16x16x32_bf16 v[116:119], a[220:223], a[188:191], v[116:119]\n\t"
"ds_read_b64_tr_b16 v[234:235], v246 offset:36864\n\t"
"v_mfma_f32_16x16x32_bf16 v[116:119], a[212:215], a[184:187], v[116:119]\n\t"
"ds_write_b64 v245, v[228:229] offset:2048\n\t"
"v_mfma_f32_16x16x32_bf16 v[120:123], a[212:215], a[188:191], v[120:123]\n\t"
"ds_read_b64_tr_b16 v[224:225], v246 offset:41472\n\t"
"v_mfma_f32_16x16x32_bf16 v[120:123], a[220:223], a[192:195], v[120:123]\n\t"
"ds_read_b64_tr_b16 v[226:227], v246 offset:44544\n\t"
"v_mfma_f32_16x16x32_bf16 v[120:123], v[236:239], a[196:199], v[120:123]\n\t"
"ds_read_b64_tr_b16 v[228:229], v246 offset:47616\n\t"
"v_mfma_f32_16x16x32_bf16 v[124:127], a[220:223], a[196:199], v[124:127]\n\t"
"s_waitcnt lgkmcnt(4)\n\t"
"ds_read_b64_tr_b16 v[230:231], v246 offset:50688\n\t"
"v_mfma_f32_16x16x32_bf16 v[124:127], a[212:215], a[192:195], v[124:127]\n\t"
"global_load_dwordx4 v[240:243], v252, s[48:49] offset:2048\n\t"
"v_mfma_f32_16x16x32_bf16 v[124:127], v[236:239], a[200:203], v[124:127]\n\t"
"global_load_dwordx4 a[208:211], v252, s[46:47] offset:1024\n\t"
"v_mfma_f32_16x16x32_bf16 v[232:235], v[232:235], v[220:223], 0\n\t"
"s_waitcnt lgkmcnt(2)\n\t"
"v_mfma_f32_16x16x32_bf16 v[224:227], v[224:227], v[220:223], 0\n\t"
"v_mfma_f32_16x16x32_bf16 v[128:131], a[220:223], a[200:203], v[128:131]\n\t"
"buffer_load_dwordx4 a[220:223], off, s[80:83], s20 offset:2048 sc1\n\t"
"v_mfma_f32_16x16x32_bf16 v[128:131], a[212:215], a[196:199], v[128:131]\n\t"
"s_waitcnt vmcnt(11) lgkmcnt(0)\n\t"
"s_mov_b32 s20, 1019904\n\t"
"v_mfma_f32_16x16x32_bf16 v[128:131], v[236:239], a[204:207], v[128:131]\n\t"
"v_mfma_f32_16x16x32_bf16 v[228:231], v[228:231], v[220:223], 0\n\t"
"buffer_load_dwordx4 a[212:215], off, s[80:83], s20 offset:0 sc1\n\t"
"v_mfma_f32_16x16x32_bf16 v[132:135], v[96:99], a[168:171], v[132:135]\n\t"
"s_add_u32 m0, s24, 46336\n\t"
"s_add_u32 s28, s21, 1376256\n\t"
"v_cvt_pk_bf16_f32 v232, v232, v233\n\t"
"v_mfma_f32_16x16x32_bf16 v[132:135], a[236:239], a[176:179], v[132:135]\n\t"
"v_cvt_pk_bf16_f32 v233, v234, v235\n\t"
"v_cvt_pk_bf16_f32 v234, v224, v225\n\t"
"v_mfma_f32_16x16x32_bf16 v[132:135], a[232:235], a[172:175], v[132:135]\n\t"
"v_cvt_pk_bf16_f32 v235, v226, v227\n\t"
"v_mfma_f32_16x16x32_bf16 v[136:139], v[96:99], a[172:175], v[136:139]\n\t"
"buffer_load_dwordx4 v247, s[12:15], s28 offen offset:256 nt lds\n\t"
"v_mfma_f32_16x16x32_bf16 v[136:139], a[232:235], a[176:179], v[136:139]\n\t"
"s_add_u32 m0, s24, 52480\n\t"
"s_add_u32 s28, s21, 1572864\n\t"
"ds_read_b64_tr_b16 v[224:225], v246 offset:53760\n\t"
"v_mfma_f32_16x16x32_bf16 v[136:139], a[236:239], a[180:183], v[136:139]\n\t"
"buffer_load_dwordx4 v247, s[12:15], s28 offen offset:256 nt lds\n\t"
"v_mfma_f32_16x16x32_bf16 v[140:143], a[236:239], a[184:187], v[140:143]\n\t"
"s_add_u32 m0, s24, 92416\n\t"
"s_add_u32 s28, s21, 2752512\n\t"
"ds_read_b64_tr_b16 v[226:227], v246 offset:56832\n\t"
"v_mfma_f32_16x16x32_bf16 v[140:143], a[232:235], a[180:183], v[140:143]\n\t"
"buffer_load_dwordx4 v247, s[12:15], s28 offen offset:256 nt lds\n\t"
"v_mfma_f32_16x16x32_bf16 v[140:143], v[96:99], a[176:179], v[140:143]\n\t"
"s_add_u32 m0, s24, 100096\n\t"
"s_add_u32 s28, s21, 2949120\n\t"
"ds_write2st64_b64 v245, v[232:233], v[234:235] offset0:5 offset1:6\n\t"
"v_cvt_pk_bf16_f32 v228, v228, v229\n\t"
"v_cvt_pk_bf16_f32 v229, v230, v231\n\t"
"v_mfma_f32_16x16x32_bf16 v[144:147], v[96:99], a[180:183], v[144:147]\n\t"
"v_mfma_f32_16x16x32_bf16 v[144:147], a[232:235], a[184:187], v[144:147]\n\t"
"buffer_load_dwordx4 v247, s[12:15], s28 offen offset:256 nt lds\n\t"
"v_mfma_f32_16x16x32_bf16 v[144:147], a[236:239], a[188:191], v[144:147]\n\t"
"s_add_u32 m0, s24, 106240\n\t"
"s_add_u32 s28, s21, 3145728\n\t"
"v_mfma_f32_16x16x32_bf16 v[148:151], a[232:235], a[188:191], v[148:151]\n\t"
"buffer_load_dwordx4 v247, s[12:15], s28 offen offset:256 nt lds\n\t"
"v_mfma_f32_16x16x32_bf16 v[148:151], a[236:239], a[192:195], v[148:151]\n\t"
"s_waitcnt lgkmcnt(1)\n\t"
"s_add_u32 m0, s24, 112384\n\t"
"s_add_u32 s28, s21, 3342336\n\t"
"v_mfma_f32_16x16x32_bf16 v[148:151], v[96:99], a[184:187], v[148:151]\n\t"
"buffer_load_dwordx4 v247, s[12:15], s28 offen offset:256 nt lds\n\t"
"s_add_u32 m0, s25, 256\n\t"
"v_mfma_f32_16x16x32_bf16 v[224:227], v[224:227], v[220:223], 0\n\t"
"buffer_load_dwordx4 v254, s[12:15], 0 offen offset:256 nt lds\n\t"
"v_mfma_f32_16x16x32_bf16 v[152:155], v[96:99], a[188:191], v[152:155]\n\t"
"s_add_u32 m0, s26, 256\n\t"
"v_mfma_f32_16x16x32_bf16 v[152:155], a[236:239], a[196:199], v[152:155]\n\t"
"v_mfma_f32_16x16x32_bf16 v[152:155], a[232:235], a[192:195], v[152:155]\n\t"
"s_mov_b64 exec, s[36:37]\n\t"
"buffer_load_dwordx4 v254, s[12:15], s27 offen offset:256 nt lds\n\t"
"s_mov_b64 exec, -1\n\t"
"v_mfma_f32_16x16x32_bf16 v[156:159], v[96:99], a[192:195], v[156:159]\n\t"
"v_mfma_f32_16x16x32_bf16 v[156:159], a[232:235], a[196:199], v[156:159]\n\t"
"v_cvt_pk_bf16_f32 v230, v224, v225\n\t"
"v_cvt_pk_bf16_f32 v231, v226, v227\n\t"
"v_mfma_f32_16x16x32_bf16 v[156:159], a[236:239], a[200:203], v[156:159]\n\t"
"ds_write2st64_b64 v245, v[228:229], v[230:231] offset0:7 offset1:8\n\t"
"v_mfma_f32_16x16x32_bf16 v[160:163], a[232:235], a[200:203], v[160:163]\n\t"
"s_waitcnt vmcnt(17)\n\t"
"v_mfma_f32_16x16x32_bf16 v[160:163], v[96:99], a[196:199], v[160:163]\n\t"
"s_waitcnt vmcnt(11)\n\t"
"ds_read_b64_tr_b16 v[236:237], v240 offset:1536\n\t"
"v_mfma_f32_16x16x32_bf16 v[160:163], a[236:239], a[204:207], v[160:163]\n\t"
"ds_read_b64_tr_b16 v[238:239], v240 offset:3072\n\t"
"v_mfma_f32_16x16x32_bf16 v[164:167], a[240:243], a[168:171], v[164:167]\n\t"
"ds_read_b64_tr_b16 v[232:233], v241 offset:1536\n\t"
"v_mfma_f32_16x16x32_bf16 v[164:167], a[248:251], a[176:179], v[164:167]\n\t"
"ds_read_b64_tr_b16 v[234:235], v241 offset:3072\n\t"
"v_mfma_f32_16x16x32_bf16 v[164:167], a[244:247], a[172:175], v[164:167]\n\t"
"s_waitcnt lgkmcnt(4)\n\t"
"s_barrier\n\t"
"v_mfma_f32_16x16x32_bf16 v[168:171], a[240:243], a[172:175], v[168:171]\n\t"
"ds_read_b64_tr_b16 v[224:225], v240 offset:21504\n\t"
"v_mfma_f32_16x16x32_bf16 v[168:171], a[244:247], a[176:179], v[168:171]\n\t"
"ds_read_b64_tr_b16 v[226:227], v240 offset:23040\n\t"
"v_mfma_f32_16x16x32_bf16 v[168:171], a[248:251], a[180:183], v[168:171]\n\t"
"ds_read_b64_tr_b16 v[228:229], v241 offset:21504\n\t"
"v_mfma_f32_16x16x32_bf16 v[172:175], a[244:247], a[180:183], v[172:175]\n\t"
"ds_read_b64_tr_b16 v[230:231], v241 offset:23040\n\t"
"v_mfma_f32_16x16x32_bf16 v[172:175], a[240:243], a[176:179], v[172:175]\n\t"
"ds_read_b128 a[168:171], v244 offset:0\n\t"
"v_mfma_f32_16x16x32_bf16 v[172:175], a[248:251], a[184:187], v[172:175]\n\t"
"ds_read_b128 a[172:175], v244 offset:512\n\t"
"v_mfma_f32_16x16x32_bf16 v[176:179], a[240:243], a[180:183], v[176:179]\n\t"
"s_waitcnt vmcnt(10) lgkmcnt(8)\n\t"
"ds_read_b128 a[176:179], v244 offset:1024\n\t"
"v_mfma_f32_16x16x32_bf16 v[176:179], a[248:251], a[188:191], v[176:179]\n\t"
"s_waitcnt lgkmcnt(7)\n\t"
"ds_read_b128 a[180:183], v244 offset:1536\n\t"
"v_mfma_f32_16x16x32_bf16 v[176:179], a[244:247], a[184:187], v[176:179]\n\t"
"s_waitcnt lgkmcnt(6)\n\t"
"v_mfma_f32_16x16x32_bf16 v[236:239], v[236:239], a[208:211], 0\n\t"
"s_waitcnt lgkmcnt(4)\n\t"
"v_mfma_f32_16x16x32_bf16 v[232:235], v[232:235], a[208:211], 0\n\t"
"v_mfma_f32_16x16x32_bf16 v[224:227], v[224:227], a[208:211], 0\n\t"
"v_mfma_f32_16x16x32_bf16 v[228:231], v[228:231], a[208:211], 0\n\t"
"v_mfma_f32_16x16x32_bf16 v[180:183], a[240:243], a[184:187], v[180:183]\n\t"
"ds_read_b128 a[184:187], v244 offset:2048\n\t"
"v_mfma_f32_16x16x32_bf16 v[180:183], a[248:251], a[192:195], v[180:183]\n\t"
"v_cvt_pk_bf16_f32 v236, v236, v237\n\t"
"v_cvt_pk_bf16_f32 v237, v238, v239\n\t"
"v_mfma_f32_16x16x32_bf16 v[180:183], a[244:247], a[188:191], v[180:183]\n\t"
"v_cvt_pk_bf16_f32 v238, v232, v233\n\t"
"v_cvt_pk_bf16_f32 v239, v234, v235\n\t"
"v_mfma_f32_16x16x32_bf16 v[184:187], a[240:243], a[188:191], v[184:187]\n\t"
"ds_read_b128 a[188:191], v244 offset:2560\n\t"
"v_cvt_pk_bf16_f32 v224, v224, v225\n\t"
"v_mfma_f32_16x16x32_bf16 v[184:187], a[244:247], a[192:195], v[184:187]\n\t"
"ds_read_b64_tr_b16 v[232:233], v240 offset:41472\n\t"
"v_cvt_pk_bf16_f32 v225, v226, v227\n\t"
"v_mfma_f32_16x16x32_bf16 v[184:187], a[248:251], a[196:199], v[184:187]\n\t"
"ds_read_b64_tr_b16 v[234:235], v240 offset:43008\n\t"
"v_cvt_pk_bf16_f32 v226, v228, v229\n\t"
"v_mfma_f32_16x16x32_bf16 v[188:191], a[248:251], a[200:203], v[188:191]\n\t"
"v_cvt_pk_bf16_f32 v227, v230, v231\n\t"
"v_mfma_f32_16x16x32_bf16 v[188:191], a[244:247], a[196:199], v[188:191]\n\t"
"ds_read_b64_tr_b16 v[228:229], v241 offset:41472\n\t"
"v_mfma_f32_16x16x32_bf16 v[188:191], a[240:243], a[192:195], v[188:191]\n\t"
"ds_read_b64_tr_b16 v[230:231], v241 offset:43008\n\t"
"v_mfma_f32_16x16x32_bf16 v[192:195], a[240:243], a[196:199], v[192:195]\n\t"
"s_waitcnt lgkmcnt(7)\n\t"
"ds_read_b128 a[192:195], v244 offset:3072\n\t"
"v_mfma_f32_16x16x32_bf16 v[192:195], a[244:247], a[200:203], v[192:195]\n\t"
"ds_read_b128 a[196:199], v244 offset:3584\n\t"
"v_mfma_f32_16x16x32_bf16 v[192:195], a[248:251], a[204:207], v[192:195]\n\t"
"ds_read_b128 a[200:203], v244 offset:4096\n\t"
"Lst9_%=:\n\t"
"v_mfma_f32_16x16x32_bf16 v[196:199], a[216:219], a[168:171], v[196:199]\n\t"
"s_mov_b64 vcc, -1\n\t"
"s_bitcmp1_b32 s73, 1\n\t"
"s_cbranch_scc1 Lvh2_%=\n\t"
"s_bitcmp1_b32 s73, 0\n\t"
"s_cbranch_scc1 Lpg18_%=\n\t"
"s_branch Lpg17_%=\n\t"
"Lvh2_%=:\n\t"
"s_bitcmp1_b32 s73, 0\n\t"
"s_cbranch_scc1 Lpg20_%=\n\t"
"s_branch Lpg19_%=\n\t"
"Lvr2_%=:\n\t"
"s_mov_b64 vcc, 0\n\t"
"s_waitcnt lgkmcnt(9)\n\t"
"ds_read_b128 a[204:207], v244 offset:4608\n\t"
"v_mfma_f32_16x16x32_bf16 v[196:199], a[252:255], a[176:179], v[196:199]\n\t"
"v_mfma_f32_16x16x32_bf16 v[196:199], a[224:227], a[172:175], v[196:199]\n\t"
"buffer_load_dwordx4 v[96:99], off, s[80:83], s20 offset:1024 sc1\n\t"
"v_mfma_f32_16x16x32_bf16 v[204:207], a[216:219], a[172:175], v[204:207]\n\t"
"s_waitcnt lgkmcnt(6)\n\t"
"v_mfma_f32_16x16x32_bf16 v[204:207], a[224:227], a[176:179], v[204:207]\n\t"
"s_waitcnt lgkmcnt(4)\n\t"
"v_mfma_f32_16x16x32_bf16 v[204:207], a[252:255], a[180:183], v[204:207]\n\t"
"v_mfma_f32_16x16x32_bf16 v[232:235], v[232:235], a[208:211], 0\n\t"
"v_add_u32 v240, 59904, v240\n\t"
"v_add_u32 v241, 59904, v241\n\t"
"v_mfma_f32_16x16x32_bf16 v[228:231], v[228:231], a[208:211], 0\n\t"
"v_mfma_f32_16x16x32_bf16 v[212:215], a[224:227], a[180:183], v[212:215]\n\t"
"v_mfma_f32_16x16x32_bf16 v[212:215], a[216:219], a[176:179], v[212:215]\n\t"
"v_mfma_f32_16x16x32_bf16 v[212:215], a[252:255], a[184:187], v[212:215]\n\t"
"s_barrier\n\t"
"v_mfma_f32_16x16x32_bf16 a[0:3], a[216:219], a[180:183], a[0:3]\n\t"
"s_waitcnt lgkmcnt(3)\n\t"
"v_cvt_pk_bf16_f32 v232, v232, v233\n\t"
"v_mfma_f32_16x16x32_bf16 a[0:3], a[252:255], a[188:191], a[0:3]\n\t"
"v_cvt_pk_bf16_f32 v233, v234, v235\n\t"
"v_cvt_pk_bf16_f32 v234, v228, v229\n\t"
"v_mfma_f32_16x16x32_bf16 a[0:3], a[224:227], a[184:187], a[0:3]\n\t"
"ds_write_b128 v243, v[236:239] offset:0\n\t"
"v_mfma_f32_16x16x32_bf16 a[8:11], a[216:219], a[184:187], a[8:11]\n\t"
"s_waitcnt lgkmcnt(3)\n\t"
"v_cvt_pk_bf16_f32 v235, v230, v231\n\t"
"v_mfma_f32_16x16x32_bf16 a[8:11], a[224:227], a[188:191], a[8:11]\n\t"
"ds_read_b64_tr_b16 v[236:237], v240 offset:1536\n\t"
"v_mfma_f32_16x16x32_bf16 a[8:11], a[252:255], a[192:195], a[8:11]\n\t"
"ds_write_b128 v243, v[224:227] offset:9216\n\t"
"v_mfma_f32_16x16x32_bf16 a[16:19], a[252:255], a[196:199], a[16:19]\n\t"
"buffer_load_dwordx4 a[232:235], off, s[80:83], s20 offset:2048 sc1\n\t"
"v_mfma_f32_16x16x32_bf16 a[16:19], a[224:227], a[192:195], a[16:19]\n\t"
"s_waitcnt lgkmcnt(4)\n\t"
"ds_read_b64_tr_b16 v[238:239], v240 offset:3072\n\t"
"v_mfma_f32_16x16x32_bf16 a[16:19], a[216:219], a[188:191], a[16:19]\n\t"
"s_mov_b32 s20, 1032192\n\t"
"ds_write_b128 v243, v[232:235] offset:18432\n\t"
"v_mfma_f32_16x16x32_bf16 a[24:27], a[216:219], a[192:195], a[24:27]\n\t"
"buffer_load_dwordx4 a[236:239], off, s[80:83], s20 offset:0 sc1\n\t"
"v_mfma_f32_16x16x32_bf16 a[24:27], a[224:227], a[196:199], a[24:27]\n\t"
"s_waitcnt lgkmcnt(5)\n\t"
"ds_read_b64_tr_b16 v[224:225], v241 offset:1536\n\t"
"v_mfma_f32_16x16x32_bf16 a[24:27], a[252:255], a[200:203], a[24:27]\n\t"
"ds_read_b64_tr_b16 v[226:227], v241 offset:3072\n\t"
"v_mfma_f32_16x16x32_bf16 a[32:35], a[224:227], a[200:203], a[32:35]\n\t"
"s_waitcnt lgkmcnt(3)\n\t"
"ds_read_b64_tr_b16 v[228:229], v240 offset:21504\n\t"
"v_mfma_f32_16x16x32_bf16 a[32:35], a[216:219], a[196:199], a[32:35]\n\t"
"ds_read_b64_tr_b16 v[230:231], v240 offset:23040\n\t"
"v_mfma_f32_16x16x32_bf16 a[32:35], a[252:255], a[204:207], a[32:35]\n\t"
"ds_read_b64_tr_b16 v[232:233], v241 offset:21504\n\t"
"ds_read_b64_tr_b16 v[234:235], v241 offset:23040\n\t"
"v_mfma_f32_16x16x32_bf16 v[236:239], v[236:239], a[208:211], 0\n\t"
"s_waitcnt lgkmcnt(4)\n\t"
"v_mfma_f32_16x16x32_bf16 v[224:227], v[224:227], a[208:211], 0\n\t"
"s_waitcnt vmcnt(12) lgkmcnt(2)\n\t"
"buffer_load_dwordx4 a[216:219], off, s[80:83], s20 offset:1024 sc1\n\t"
"v_mfma_f32_16x16x32_bf16 v[228:231], v[228:231], a[208:211], 0\n\t"
"s_waitcnt lgkmcnt(0)\n\t"
"v_mfma_f32_16x16x32_bf16 v[232:235], v[232:235], a[208:211], 0\n\t"
"buffer_load_dwordx4 a[224:227], off, s[80:83], s20 offset:2048 sc1\n\t"
"v_mfma_f32_16x16x32_bf16 a[40:43], a[228:231], a[168:171], a[40:43]\n\t"
"s_mov_b32 s20, 1044480\n\t"
"v_mfma_f32_16x16x32_bf16 a[40:43], a[220:223], a[176:179], a[40:43]\n\t"
"v_cvt_pk_bf16_f32 v236, v236, v237\n\t"
"v_cvt_pk_bf16_f32 v237, v238, v239\n\t"
"v_mfma_f32_16x16x32_bf16 a[40:43], v[248:251], a[172:175], a[40:43]\n\t"
"v_cvt_pk_bf16_f32 v238, v224, v225\n\t"
"v_cvt_pk_bf16_f32 v239, v226, v227\n\t"
"v_mfma_f32_16x16x32_bf16 a[48:51], a[228:231], a[172:175], a[48:51]\n\t"
"v_cvt_pk_bf16_f32 v228, v228, v229\n\t"
"v_cvt_pk_bf16_f32 v229, v230, v231\n\t"
"v_mfma_f32_16x16x32_bf16 a[48:51], v[248:251], a[176:179], a[48:51]\n\t"
"v_cvt_pk_bf16_f32 v230, v232, v233\n\t"
"v_cvt_pk_bf16_f32 v231, v234, v235\n\t"
"v_mfma_f32_16x16x32_bf16 a[48:51], a[220:223], a[180:183], a[48:51]\n\t"
"buffer_load_dwordx4 a[240:243], off, s[80:83], s20 offset:0 sc1\n\t"
"v_mfma_f32_16x16x32_bf16 a[56:59], a[220:223], a[184:187], a[56:59]\n\t"
"buffer_load_dwordx4 a[244:247], off, s[80:83], s20 offset:1024 sc1\n\t"
"v_mfma_f32_16x16x32_bf16 a[56:59], v[248:251], a[180:183], a[56:59]\n\t"
"ds_read_b64_tr_b16 v[224:225], v240 offset:41472\n\t"
"v_mfma_f32_16x16x32_bf16 a[56:59], a[228:231], a[176:179], a[56:59]\n\t"
"ds_write_b128 v243, v[236:239] offset:27648\n\t"
"v_mfma_f32_16x16x32_bf16 a[64:67], a[228:231], a[180:183], a[64:67]\n\t"
"v_mfma_f32_16x16x32_bf16 a[64:67], v[248:251], a[184:187], a[64:67]\n\t"
"ds_read_b64_tr_b16 v[226:227], v240 offset:43008\n\t"
"v_mfma_f32_16x16x32_bf16 a[64:67], a[220:223], a[188:191], a[64:67]\n\t"
"ds_write_b128 v243, v[228:231] offset:36864\n\t"
"v_mfma_f32_16x16x32_bf16 a[72:75], a[220:223], a[192:195], a[72:75]\n\t"
"buffer_load_dwordx4 a[248:251], off, s[80:83], s20 offset:2048 sc1\n\t"
"v_mfma_f32_16x16x32_bf16 a[72:75], v[248:251], a[188:191], a[72:75]\n\t"
"s_mov_b32 s20, 1056768\n\t"
"ds_read_b64_tr_b16 v[232:233], v241 offset:41472\n\t"
"v_mfma_f32_16x16x32_bf16 a[72:75], a[228:231], a[184:187], a[72:75]\n\t"
"ds_read_b64_tr_b16 v[234:235], v241 offset:43008\n\t"
"v_mfma_f32_16x16x32_bf16 a[80:83], a[228:231], a[188:191], a[80:83]\n\t"
"buffer_load_dwordx4 a[252:255], off, s[80:83], s20 offset:0 sc1\n\t"
"v_mfma_f32_16x16x32_bf16 a[80:83], v[248:251], a[192:195], a[80:83]\n\t"
"v_mfma_f32_16x16x32_bf16 a[80:83], a[220:223], a[196:199], a[80:83]\n\t"
"v_mfma_f32_16x16x32_bf16 a[88:91], v[248:251], a[196:199], a[88:91]\n\t"
"s_waitcnt lgkmcnt(3)\n\t"
"v_mfma_f32_16x16x32_bf16 a[88:91], a[228:231], a[192:195], a[88:91]\n\t"
"s_waitcnt lgkmcnt(0)\n\t"
"v_mfma_f32_16x16x32_bf16 a[88:91], a[220:223], a[200:203], a[88:91]\n\t"
"v_mfma_f32_16x16x32_bf16 v[224:227], v[224:227], a[208:211], 0\n\t"
"v_mfma_f32_16x16x32_bf16 v[232:235], v[232:235], a[208:211], 0\n\t"
"v_mfma_f32_16x16x32_bf16 a[96:99], v[248:251], a[200:203], a[96:99]\n\t"
"buffer_load_dwordx4 v[248:251], off, s[80:83], s20 offset:1024 sc1\n\t"
"v_mfma_f32_16x16x32_bf16 a[96:99], a[228:231], a[196:199], a[96:99]\n\t"
"s_waitcnt vmcnt(8)\n\t"
"v_mfma_f32_16x16x32_bf16 a[96:99], a[220:223], a[204:207], a[96:99]\n\t"
"ds_read_b64_tr_b16 v[236:237], v246 offset:512\n\t"
"v_mfma_f32_16x16x32_bf16 a[104:107], a[212:215], a[168:171], a[104:107]\n\t"
"buffer_load_dwordx4 a[220:223], off, s[80:83], s20 offset:2048 sc1\n\t"
"v_mfma_f32_16x16x32_bf16 a[104:107], a[232:235], a[176:179], a[104:107]\n\t"
"s_mov_b32 s20, 1290240\n\t"
"ds_read_b64_tr_b16 v[238:239], v246 offset:3584\n\t"
"v_cvt_pk_bf16_f32 v224, v224, v225\n\t"
"v_mfma_f32_16x16x32_bf16 a[104:107], v[96:99], a[172:175], a[104:107]\n\t"
"ds_read_b64_tr_b16 v[228:229], v246 offset:6656\n\t"
"v_cvt_pk_bf16_f32 v225, v226, v227\n\t"
"v_mfma_f32_16x16x32_bf16 a[112:115], a[212:215], a[172:175], a[112:115]\n\t"
"ds_read_b64_tr_b16 v[230:231], v246 offset:9728\n\t"
"v_cvt_pk_bf16_f32 v226, v232, v233\n\t"
"v_mfma_f32_16x16x32_bf16 a[112:115], v[96:99], a[176:179], a[112:115]\n\t"
"ds_read_b128 a[168:171], v244 offset:9216\n\t"
"v_cvt_pk_bf16_f32 v227, v234, v235\n\t"
"v_mfma_f32_16x16x32_bf16 a[112:115], a[232:235], a[180:183], a[112:115]\n\t"
"ds_write_b128 v243, v[224:227] offset:46080\n\t"
"v_mfma_f32_16x16x32_bf16 a[120:123], a[232:235], a[184:187], a[120:123]\n\t"
"buffer_load_dwordx4 a[228:231], off, s[80:83], s20 offset:0 sc1\n\t"
"v_mfma_f32_16x16x32_bf16 a[120:123], v[96:99], a[180:183], a[120:123]\n\t"
"ds_read_b64_tr_b16 v[232:233], v246 offset:12800\n\t"
"v_mfma_f32_16x16x32_bf16 a[120:123], a[212:215], a[176:179], a[120:123]\n\t"
"ds_read_b64_tr_b16 v[234:235], v246 offset:15872\n\t"
"v_mfma_f32_16x16x32_bf16 a[128:131], a[212:215], a[180:183], a[128:131]\n\t"
"ds_read_b64_tr_b16 v[224:225], v246 offset:20480\n\t"
"v_mfma_f32_16x16x32_bf16 a[128:131], v[96:99], a[184:187], a[128:131]\n\t"
"s_waitcnt lgkmcnt(5)\n\t"
"ds_read_b64_tr_b16 v[226:227], v246 offset:23552\n\t"
"v_mfma_f32_16x16x32_bf16 a[128:131], a[232:235], a[188:191], a[128:131]\n\t"
"ds_read_b128 a[172:175], v244 offset:9728\n\t"
"v_mfma_f32_16x16x32_bf16 a[136:139], a[212:215], a[184:187], a[136:139]\n\t"
"Lpg7_%=:\n\t"
"s_cbranch_vccnz Lpg11_%=\n\t"
"ds_read_b128 a[176:179], v244 offset:10240\n\t"
"v_mfma_f32_16x16x32_bf16 a[136:139], a[232:235], a[192:195], a[136:139]\n\t"
"ds_read_b128 a[180:183], v244 offset:10752\n\t"
"v_mfma_f32_16x16x32_bf16 a[136:139], v[96:99], a[188:191], a[136:139]\n\t"
"s_waitcnt lgkmcnt(5)\n\t"
"ds_read_b128 a[184:187], v244 offset:11264\n\t"
"v_mfma_f32_16x16x32_bf16 v[236:239], v[236:239], v[220:223], 0\n\t"
"s_waitcnt lgkmcnt(4)\n\t"
"v_mfma_f32_16x16x32_bf16 v[228:231], v[228:231], v[220:223], 0\n\t"
"v_mfma_f32_16x16x32_bf16 v[232:235], v[232:235], v[220:223], 0\n\t"
"v_mfma_f32_16x16x32_bf16 v[224:227], v[224:227], v[220:223], 0\n\t"
"v_mfma_f32_16x16x32_bf16 a[144:147], a[212:215], a[188:191], a[144:147]\n\t"
"ds_read_b128 a[188:191], v244 offset:11776\n\t"
"v_mfma_f32_16x16x32_bf16 a[144:147], a[232:235], a[196:199], a[144:147]\n\t"
"v_cvt_pk_bf16_f32 v236, v236, v237\n\t"
"v_cvt_pk_bf16_f32 v237, v238, v239\n\t"
"v_mfma_f32_16x16x32_bf16 a[144:147], v[96:99], a[192:195], a[144:147]\n\t"
"v_cvt_pk_bf16_f32 v238, v228, v229\n\t"
"v_cvt_pk_bf16_f32 v239, v230, v231\n\t"
"v_mfma_f32_16x16x32_bf16 a[152:155], a[212:215], a[192:195], a[152:155]\n\t"
"ds_read_b64_tr_b16 v[228:229], v246 offset:26624\n\t"
"v_cvt_pk_bf16_f32 v232, v232, v233\n\t"
"v_mfma_f32_16x16x32_bf16 a[152:155], v[96:99], a[196:199], a[152:155]\n\t"
"ds_read_b64_tr_b16 v[230:231], v246 offset:29696\n\t"
"v_cvt_pk_bf16_f32 v233, v234, v235\n\t"
"v_mfma_f32_16x16x32_bf16 a[152:155], a[232:235], a[200:203], a[152:155]\n\t"
"ds_read_b128 a[192:195], v244 offset:12288\n\t"
"v_cvt_pk_bf16_f32 v234, v224, v225\n\t"
"v_mfma_f32_16x16x32_bf16 a[160:163], v[96:99], a[200:203], a[160:163]\n\t"
"s_waitcnt vmcnt(7) lgkmcnt(6)\n\t"
"ds_read_b128 a[200:203], v244 offset:13312\n\t"
"v_cvt_pk_bf16_f32 v235, v226, v227\n\t"
"v_mfma_f32_16x16x32_bf16 a[160:163], a[212:215], a[196:199], a[160:163]\n\t"
"ds_read_b128 a[196:199], v244 offset:12800\n\t"
"v_mfma_f32_16x16x32_bf16 a[160:163], a[232:235], a[204:207], a[160:163]\n\t"
"buffer_load_dwordx4 v[96:99], off, s[80:83], s20 offset:1024 sc1\n\t"
"v_mfma_f32_16x16x32_bf16 v[200:203], a[236:239], a[168:171], v[200:203]\n\t"
"s_waitcnt lgkmcnt(3)\n\t"
"ds_read_b128 a[204:207], v244 offset:13824\n\t"
"v_mfma_f32_16x16x32_bf16 v[200:203], a[224:227], a[176:179], v[200:203]\n\t"
"buffer_load_dwordx4 a[212:215], off, s[80:83], s20 offset:2048 sc1\n\t"
"v_mfma_f32_16x16x32_bf16 v[200:203], a[216:219], a[172:175], v[200:203]\n\t"
"s_mov_b32 s20, 1302528\n\t"
"v_mfma_f32_16x16x32_bf16 v[228:231], v[228:231], v[220:223], 0\n\t"
"buffer_load_dwordx4 a[232:235], off, s[80:83], s20 offset:0 sc1\n\t"
"v_mfma_f32_16x16x32_bf16 v[208:211], a[236:239], a[172:175], v[208:211]\n\t"
"v_mfma_f32_16x16x32_bf16 v[208:211], a[224:227], a[180:183], v[208:211]\n\t"
"v_mfma_f32_16x16x32_bf16 v[208:211], a[216:219], a[176:179], v[208:211]\n\t"
"s_waitcnt lgkmcnt(0)\n\t"
"s_barrier\n\t"
"v_mfma_f32_16x16x32_bf16 v[216:219], a[236:239], a[176:179], v[216:219]\n\t"
"ds_read_b64_tr_b16 v[224:225], v246 offset:32768\n\t"
"v_mfma_f32_16x16x32_bf16 v[216:219], a[216:219], a[180:183], v[216:219]\n\t"
"ds_read_b64_tr_b16 v[226:227], v246 offset:35840\n\t"
"v_cvt_pk_bf16_f32 v228, v228, v229\n\t"
"v_mfma_f32_16x16x32_bf16 v[216:219], a[224:227], a[184:187], v[216:219]\n\t"
"ds_write2st64_b64 v245, v[236:237], v[238:239] offset0:0 offset1:1\n\t"
"v_mfma_f32_16x16x32_bf16 a[4:7], a[224:227], a[188:191], a[4:7]\n\t"
"v_cvt_pk_bf16_f32 v229, v230, v231\n\t"
"v_mfma_f32_16x16x32_bf16 a[4:7], a[216:219], a[184:187], a[4:7]\n\t"
"v_mfma_f32_16x16x32_bf16 a[4:7], a[236:239], a[180:183], a[4:7]\n\t"
"ds_write2st64_b64 v245, v[232:233], v[234:235] offset0:2 offset1:3\n\t"
"v_mfma_f32_16x16x32_bf16 a[12:15], a[236:239], a[184:187], a[12:15]\n\t"
"v_mfma_f32_16x16x32_bf16 a[12:15], a[216:219], a[188:191], a[12:15]\n\t"
"ds_read_b64_tr_b16 v[232:233], v246 offset:40448\n\t"
"v_mfma_f32_16x16x32_bf16 a[12:15], a[224:227], a[192:195], a[12:15]\n\t"
"ds_write_b64 v245, v[228:229] offset:2048\n\t"
"v_mfma_f32_16x16x32_bf16 a[20:23], a[224:227], a[196:199], a[20:23]\n\t"
"ds_read_b64_tr_b16 v[234:235], v246 offset:43520\n\t"
"v_mfma_f32_16x16x32_bf16 a[20:23], a[216:219], a[192:195], a[20:23]\n\t"
"ds_read_b64_tr_b16 v[228:229], v246 offset:46592\n\t"
"v_mfma_f32_16x16x32_bf16 a[20:23], a[236:239], a[188:191], a[20:23]\n\t"
"ds_read_b64_tr_b16 v[230:231], v246 offset:49664\n\t"
"v_mfma_f32_16x16x32_bf16 a[28:31], a[236:239], a[192:195], a[28:31]\n\t"
"s_waitcnt lgkmcnt(7)\n\t"
"v_mfma_f32_16x16x32_bf16 a[28:31], a[216:219], a[196:199], a[28:31]\n\t"
"v_mfma_f32_16x16x32_bf16 a[28:31], a[224:227], a[200:203], a[28:31]\n\t"
"s_waitcnt lgkmcnt(2)\n\t"
"v_mfma_f32_16x16x32_bf16 v[224:227], v[224:227], v[220:223], 0\n\t"
"v_mfma_f32_16x16x32_bf16 v[232:235], v[232:235], v[220:223], 0\n\t"
"v_mfma_f32_16x16x32_bf16 a[36:39], a[216:219], a[200:203], a[36:39]\n\t"
"buffer_load_dwordx4 a[216:219], off, s[80:83], s20 offset:1024 sc1\n\t"
"v_mfma_f32_16x16x32_bf16 a[36:39], a[236:239], a[196:199], a[36:39]\n\t"
"s_waitcnt vmcnt(8) lgkmcnt(0)\n\t"
"v_mfma_f32_16x16x32_bf16 a[36:39], a[224:227], a[204:207], a[36:39]\n\t"
"v_mfma_f32_16x16x32_bf16 v[228:231], v[228:231], v[220:223], 0\n\t"
"buffer_load_dwordx4 a[224:227], off, s[80:83], s20 offset:2048 sc1\n\t"
"v_mfma_f32_16x16x32_bf16 a[44:47], a[240:243], a[168:171], a[44:47]\n\t"
"s_mov_b32 s20, 1314816\n\t"
"v_cvt_pk_bf16_f32 v224, v224, v225\n\t"
"v_cvt_pk_bf16_f32 v225, v226, v227\n\t"
"v_mfma_f32_16x16x32_bf16 a[44:47], a[248:251], a[176:179], a[44:47]\n\t"
"v_cvt_pk_bf16_f32 v226, v232, v233\n\t"
"v_cvt_pk_bf16_f32 v227, v234, v235\n\t"
"v_mfma_f32_16x16x32_bf16 a[44:47], a[244:247], a[172:175], a[44:47]\n\t"
"ds_write2st64_b64 v245, v[224:225], v[226:227] offset0:5 offset1:6\n\t"
"v_mfma_f32_16x16x32_bf16 a[52:55], a[240:243], a[172:175], a[52:55]\n\t"
"buffer_load_dwordx4 a[236:239], off, s[80:83], s20 offset:0 sc1\n\t"
"v_mfma_f32_16x16x32_bf16 a[52:55], a[244:247], a[176:179], a[52:55]\n\t"
"ds_read_b64_tr_b16 v[232:233], v246 offset:52736\n\t"
"v_mfma_f32_16x16x32_bf16 a[52:55], a[248:251], a[180:183], a[52:55]\n\t"
"ds_read_b64_tr_b16 v[234:235], v246 offset:55808\n\t"
"v_cvt_pk_bf16_f32 v228, v228, v229\n\t"
"v_mfma_f32_16x16x32_bf16 a[60:63], a[248:251], a[184:187], a[60:63]\n\t"
"v_cvt_pk_bf16_f32 v229, v230, v231\n\t"
"v_mfma_f32_16x16x32_bf16 a[60:63], a[244:247], a[180:183], a[60:63]\n\t"
"v_mfma_f32_16x16x32_bf16 a[60:63], a[240:243], a[176:179], a[60:63]\n\t"
"v_mfma_f32_16x16x32_bf16 a[68:71], a[240:243], a[180:183], a[68:71]\n\t"
"v_mfma_f32_16x16x32_bf16 a[68:71], a[244:247], a[184:187], a[68:71]\n\t"
"v_add_u32 v243, 55296, v243\n\t"
"v_mfma_f32_16x16x32_bf16 a[68:71], a[248:251], a[188:191], a[68:71]\n\t"
"v_mfma_f32_16x16x32_bf16 a[76:79], a[244:247], a[188:191], a[76:79]\n\t"
"s_waitcnt lgkmcnt(0)\n\t"
"v_mfma_f32_16x16x32_bf16 a[76:79], a[248:251], a[192:195], a[76:79]\n\t"
"v_mfma_f32_16x16x32_bf16 a[76:79], a[240:243], a[184:187], a[76:79]\n\t"
"v_mfma_f32_16x16x32_bf16 v[232:235], v[232:235], v[220:223], 0\n\t"
"v_mfma_f32_16x16x32_bf16 a[84:87], a[240:243], a[188:191], a[84:87]\n\t"
"v_mfma_f32_16x16x32_bf16 a[84:87], a[248:251], a[196:199], a[84:87]\n\t"
"v_mfma_f32_16x16x32_bf16 a[84:87], a[244:247], a[192:195], a[84:87]\n\t"
"v_mfma_f32_16x16x32_bf16 a[92:95], a[240:243], a[192:195], a[92:95]\n\t"
"v_mfma_f32_16x16x32_bf16 a[92:95], a[244:247], a[196:199], a[92:95]\n\t"
"v_cvt_pk_bf16_f32 v230, v232, v233\n\t"
"v_cvt_pk_bf16_f32 v231, v234, v235\n\t"
"v_mfma_f32_16x16x32_bf16 a[92:95], a[248:251], a[200:203], a[92:95]\n\t"
"ds_write2st64_b64 v245, v[228:229], v[230:231] offset0:7 offset1:8\n\t"
"v_mfma_f32_16x16x32_bf16 a[100:103], a[244:247], a[200:203], a[100:103]\n\t"
"buffer_load_dwordx4 a[244:247], off, s[80:83], s20 offset:1024 sc1\n\t"
"v_mfma_f32_16x16x32_bf16 a[100:103], a[240:243], a[196:199], a[100:103]\n\t"
"s_waitcnt vmcnt(8)\n\t"
"v_mfma_f32_16x16x32_bf16 a[100:103], a[248:251], a[204:207], a[100:103]\n\t"
"v_mfma_f32_16x16x32_bf16 a[108:111], a[252:255], a[168:171], a[108:111]\n\t"
"buffer_load_dwordx4 a[240:243], off, s[80:83], s20 offset:2048 sc1\n\t"
"v_mfma_f32_16x16x32_bf16 a[108:111], a[220:223], a[176:179], a[108:111]\n\t"
"s_mov_b32 s20, 1327104\n\t"
"v_mfma_f32_16x16x32_bf16 a[108:111], v[248:251], a[172:175], a[108:111]\n\t"
"s_waitcnt lgkmcnt(0)\n\t"
"s_barrier\n\t"
"v_mfma_f32_16x16x32_bf16 a[116:119], a[252:255], a[172:175], a[116:119]\n\t"
"ds_read_b64_tr_b16 v[236:237], v240 offset:512\n\t"
"v_mfma_f32_16x16x32_bf16 a[116:119], v[248:251], a[176:179], a[116:119]\n\t"
"buffer_load_dwordx4 a[248:251], off, s[80:83], s20 offset:0 sc1\n\t"
"v_mfma_f32_16x16x32_bf16 a[116:119], a[220:223], a[180:183], a[116:119]\n\t"
"ds_read_b64_tr_b16 v[238:239], v240 offset:2048\n\t"
"v_mfma_f32_16x16x32_bf16 a[124:127], a[220:223], a[184:187], a[124:127]\n\t"
"ds_read_b64_tr_b16 v[224:225], v241 offset:512\n\t"
"v_mfma_f32_16x16x32_bf16 a[124:127], v[248:251], a[180:183], a[124:127]\n\t"
"ds_read_b64_tr_b16 v[226:227], v241 offset:2048\n\t"
"v_mfma_f32_16x16x32_bf16 a[124:127], a[252:255], a[176:179], a[124:127]\n\t"
"ds_read_b64_tr_b16 v[232:233], v240 offset:20480\n\t"
"v_mfma_f32_16x16x32_bf16 a[132:135], a[252:255], a[180:183], a[132:135]\n\t"
"ds_read_b64_tr_b16 v[234:235], v240 offset:22016\n\t"
"v_mfma_f32_16x16x32_bf16 a[132:135], v[248:251], a[184:187], a[132:135]\n\t"
"ds_read_b64_tr_b16 v[228:229], v241 offset:20480\n\t"
"v_mfma_f32_16x16x32_bf16 a[132:135], a[220:223], a[188:191], a[132:135]\n\t"
"ds_read_b64_tr_b16 v[230:231], v241 offset:22016\n\t"
"v_mfma_f32_16x16x32_bf16 a[140:143], a[252:255], a[184:187], a[140:143]\n\t"
"s_waitcnt lgkmcnt(6)\n\t"
"ds_read_b128 a[168:171], v244 offset:0\n\t"
"v_mfma_f32_16x16x32_bf16 a[140:143], a[220:223], a[192:195], a[140:143]\n\t"
"s_waitcnt lgkmcnt(5)\n\t"
"ds_read_b128 a[172:175], v244 offset:512\n\t"
"v_mfma_f32_16x16x32_bf16 a[140:143], v[248:251], a[188:191], a[140:143]\n\t"
"s_waitcnt lgkmcnt(4)\n\t"
"ds_read_b128 a[176:179], v244 offset:1024\n\t"
"v_mfma_f32_16x16x32_bf16 v[236:239], v[236:239], a[208:211], 0\n\t"
"ds_read_b128 a[180:183], v244 offset:1536\n\t"
"v_mfma_f32_16x16x32_bf16 v[224:227], v[224:227], a[208:211], 0\n\t"
"s_waitcnt lgkmcnt(4)\n\t"
"ds_read_b128 a[184:187], v244 offset:2048\n\t"
"v_mfma_f32_16x16x32_bf16 v[232:235], v[232:235], a[208:211], 0\n\t"
"v_mfma_f32_16x16x32_bf16 v[228:231], v[228:231], a[208:211], 0\n\t"
"v_mfma_f32_16x16x32_bf16 a[148:151], a[252:255], a[188:191], a[148:151]\n\t"
"ds_read_b128 a[188:191], v244 offset:2560\n\t"
"v_mfma_f32_16x16x32_bf16 a[148:151], a[220:223], a[196:199], a[148:151]\n\t"
"v_cvt_pk_bf16_f32 v236, v236, v237\n\t"
"v_cvt_pk_bf16_f32 v237, v238, v239\n\t"
"v_mfma_f32_16x16x32_bf16 a[148:151], v[248:251], a[192:195], a[148:151]\n\t"
"v_cvt_pk_bf16_f32 v238, v224, v225\n\t"
"v_cvt_pk_bf16_f32 v239, v226, v227\n\t"
"v_mfma_f32_16x16x32_bf16 a[156:159], a[252:255], a[192:195], a[156:159]\n\t"
"ds_read_b128 a[192:195], v244 offset:3072\n\t"
"v_cvt_pk_bf16_f32 v232, v232, v233\n\t"
"v_mfma_f32_16x16x32_bf16 a[156:159], v[248:251], a[196:199], a[156:159]\n\t"
"v_cvt_pk_bf16_f32 v233, v234, v235\n\t"
"v_cvt_pk_bf16_f32 v234, v228, v229\n\t"
"v_mfma_f32_16x16x32_bf16 a[156:159], a[220:223], a[200:203], a[156:159]\n\t"
"ds_write_b128 v243, v[236:239] offset:27648\n\t"
"v_mfma_f32_16x16x32_bf16 a[164:167], v[248:251], a[200:203], a[164:167]\n\t"
"buffer_load_dwordx4 v[248:251], off, s[80:83], s20 offset:1024 sc1\n\t"
"v_mfma_f32_16x16x32_bf16 a[164:167], a[252:255], a[196:199], a[164:167]\n\t"
"s_waitcnt vmcnt(8) lgkmcnt(5)\n\t"
"ds_read_b64_tr_b16 v[224:225], v240 offset:40448\n\t"
"v_mfma_f32_16x16x32_bf16 a[164:167], a[220:223], a[204:207], a[164:167]\n\t"
"ds_read_b64_tr_b16 v[226:227], v240 offset:41984\n\t"
"v_cvt_pk_bf16_f32 v235, v230, v231\n\t"
"Lst10_%=:\n\t"
"v_mfma_f32_16x16x32_bf16 v[196:199], a[228:231], a[168:171], v[196:199]\n\t"
"s_waitcnt lgkmcnt(6)\n\t"
"ds_read_b64_tr_b16 v[228:229], v241 offset:40448\n\t"
"v_mfma_f32_16x16x32_bf16 v[196:199], a[212:215], a[176:179], v[196:199]\n\t"
"buffer_load_dwordx4 a[220:223], off, s[80:83], s20 offset:2048 sc1\n\t"
"v_mfma_f32_16x16x32_bf16 v[196:199], v[96:99], a[172:175], v[196:199]\n\t"
"s_mov_b32 s20, 1339392\n\t"
"ds_write_b128 v243, v[232:235] offset:36864\n\t"
"v_mfma_f32_16x16x32_bf16 v[204:207], a[228:231], a[172:175], v[204:207]\n\t"
"buffer_load_dwordx4 a[252:255], off, s[80:83], s20 offset:0 sc1\n\t"
"v_mfma_f32_16x16x32_bf16 v[204:207], v[96:99], a[176:179], v[204:207]\n\t"
"s_waitcnt lgkmcnt(7)\n\t"
"ds_read_b64_tr_b16 v[230:231], v241 offset:41984\n\t"
"v_mfma_f32_16x16x32_bf16 v[204:207], a[212:215], a[180:183], v[204:207]\n\t"
"ds_read_b128 a[196:199], v244 offset:3584\n\t"
"v_subrev_u32 v240, 59904, v240\n\t"
"v_subrev_u32 v241, 59904, v241\n\t"
"v_mfma_f32_16x16x32_bf16 v[212:215], v[96:99], a[180:183], v[212:215]\n\t"
"s_waitcnt lgkmcnt(8)\n\t"
"ds_read_b64_tr_b16 v[236:237], v240 offset:512\n\t"
"v_mfma_f32_16x16x32_bf16 v[212:215], a[228:231], a[176:179], v[212:215]\n\t"
"ds_read_b64_tr_b16 v[238:239], v240 offset:2048\n\t"
"v_mfma_f32_16x16x32_bf16 v[212:215], a[212:215], a[184:187], v[212:215]\n\t"
"ds_read_b64_tr_b16 v[232:233], v241 offset:512\n\t"
"v_mfma_f32_16x16x32_bf16 a[0:3], a[228:231], a[180:183], a[0:3]\n\t"
"s_waitcnt lgkmcnt(7)\n\t"
"ds_read_b64_tr_b16 v[234:235], v241 offset:2048\n\t"
"v_mfma_f32_16x16x32_bf16 a[0:3], a[212:215], a[188:191], a[0:3]\n\t"
"s_waitcnt lgkmcnt(5)\n\t"
"ds_read_b128 a[200:203], v244 offset:4096\n\t"
"v_mfma_f32_16x16x32_bf16 a[0:3], v[96:99], a[184:187], a[0:3]\n\t"
"ds_read_b128 a[204:207], v244 offset:4608\n\t"
"v_mfma_f32_16x16x32_bf16 v[224:227], v[224:227], a[208:211], 0\n\t"
"s_waitcnt lgkmcnt(4)\n\t"
"v_mfma_f32_16x16x32_bf16 v[228:231], v[228:231], a[208:211], 0\n\t"
"v_mfma_f32_16x16x32_bf16 v[236:239], v[236:239], a[208:211], 0\n\t"
"s_waitcnt lgkmcnt(2)\n\t"
"v_mfma_f32_16x16x32_bf16 v[232:235], v[232:235], a[208:211], 0\n\t"
"v_mfma_f32_16x16x32_bf16 a[8:11], a[228:231], a[184:187], a[8:11]\n\t"
"v_mfma_f32_16x16x32_bf16 a[8:11], a[212:215], a[192:195], a[8:11]\n\t"
"v_cvt_pk_bf16_f32 v224, v224, v225\n\t"
"v_cvt_pk_bf16_f32 v225, v226, v227\n\t"
"v_mfma_f32_16x16x32_bf16 a[8:11], v[96:99], a[188:191], a[8:11]\n\t"
"v_cvt_pk_bf16_f32 v226, v228, v229\n\t"
"v_cvt_pk_bf16_f32 v227, v230, v231\n\t"
"v_mfma_f32_16x16x32_bf16 a[16:19], a[228:231], a[188:191], a[16:19]\n\t"
"s_waitcnt lgkmcnt(1)\n\t"
"v_cvt_pk_bf16_f32 v236, v236, v237\n\t"
"v_cvt_pk_bf16_f32 v237, v238, v239\n\t"
"v_mfma_f32_16x16x32_bf16 a[16:19], v[96:99], a[192:195], a[16:19]\n\t"
"v_cvt_pk_bf16_f32 v238, v232, v233\n\t"
"v_cvt_pk_bf16_f32 v239, v234, v235\n\t"
"v_mfma_f32_16x16x32_bf16 a[16:19], a[212:215], a[196:199], a[16:19]\n\t"
"ds_write_b128 v243, v[224:227] offset:46080\n\t"
"v_mfma_f32_16x16x32_bf16 a[24:27], a[212:215], a[200:203], a[24:27]\n\t"
"s_waitcnt lgkmcnt(1)\n\t"
"v_mfma_f32_16x16x32_bf16 a[24:27], v[96:99], a[196:199], a[24:27]\n\t"
"ds_read_b64_tr_b16 v[228:229], v240 offset:20480\n\t"
"v_mfma_f32_16x16x32_bf16 a[24:27], a[228:231], a[192:195], a[24:27]\n\t"
"ds_write_b128 v243, v[236:239] offset:0\n\t"
"v_mfma_f32_16x16x32_bf16 a[32:35], a[228:231], a[196:199], a[32:35]\n\t"
"buffer_load_dwordx4 a[228:231], off, s[80:83], s20 offset:1024 sc1\n\t"
"v_mfma_f32_16x16x32_bf16 a[32:35], v[96:99], a[200:203], a[32:35]\n\t"
"s_waitcnt vmcnt(8)\n\t"
"ds_read_b64_tr_b16 v[230:231], v240 offset:22016\n\t"
"v_mfma_f32_16x16x32_bf16 a[32:35], a[212:215], a[204:207], a[32:35]\n\t"
"ds_read_b64_tr_b16 v[232:233], v241 offset:20480\n\t"
"v_mfma_f32_16x16x32_bf16 a[40:43], a[232:235], a[168:171], a[40:43]\n\t"
"ds_read_b64_tr_b16 v[234:235], v241 offset:22016\n\t"
"v_mfma_f32_16x16x32_bf16 a[40:43], a[224:227], a[176:179], a[40:43]\n\t"
"ds_read_b64_tr_b16 v[224:225], v240 offset:40448\n\t"
"v_mfma_f32_16x16x32_bf16 a[40:43], a[216:219], a[172:175], a[40:43]\n\t"
"ds_read_b64_tr_b16 v[226:227], v240 offset:41984\n\t"
"v_mfma_f32_16x16x32_bf16 a[48:51], a[232:235], a[172:175], a[48:51]\n\t"
"buffer_load_dwordx4 v[96:99], off, s[80:83], s20 offset:2048 sc1\n\t"
"v_mfma_f32_16x16x32_bf16 a[48:51], a[216:219], a[176:179], a[48:51]\n\t"
"s_mov_b32 s20, 1351680\n\t"
"ds_read_b64_tr_b16 v[236:237], v241 offset:40448\n\t"
"v_mfma_f32_16x16x32_bf16 a[48:51], a[224:227], a[180:183], a[48:51]\n\t"
"ds_read_b64_tr_b16 v[238:239], v241 offset:41984\n\t"
"v_mfma_f32_16x16x32_bf16 a[56:59], a[232:235], a[176:179], a[56:59]\n\t"
"buffer_load_dwordx4 a[212:215], off, s[80:83], s20 offset:0 sc1\n\t"
"v_mfma_f32_16x16x32_bf16 a[56:59], a[224:227], a[184:187], a[56:59]\n\t"
"s_waitcnt lgkmcnt(4)\n\t"
"v_mfma_f32_16x16x32_bf16 a[56:59], a[216:219], a[180:183], a[56:59]\n\t"
"s_waitcnt lgkmcnt(2)\n\t"
"v_mfma_f32_16x16x32_bf16 v[228:231], v[228:231], a[208:211], 0\n\t"
"v_mfma_f32_16x16x32_bf16 v[232:235], v[232:235], a[208:211], 0\n\t"
"s_waitcnt lgkmcnt(0)\n\t"
"v_xor_b32 v246, 0xc0, v246\n\t"
"v_mfma_f32_16x16x32_bf16 v[224:227], v[224:227], a[208:211], 0\n\t"
"v_mfma_f32_16x16x32_bf16 v[236:239], v[236:239], a[208:211], 0\n\t"
"v_mfma_f32_16x16x32_bf16 a[64:67], a[232:235], a[180:183], a[64:67]\n\t"
"v_mfma_f32_16x16x32_bf16 a[64:67], a[224:227], a[188:191], a[64:67]\n\t"
"v_cvt_pk_bf16_f32 v228, v228, v229\n\t"
"v_cvt_pk_bf16_f32 v229, v230, v231\n\t"
"v_mfma_f32_16x16x32_bf16 a[64:67], a[216:219], a[184:187], a[64:67]\n\t"
"v_cvt_pk_bf16_f32 v230, v232, v233\n\t"
"v_cvt_pk_bf16_f32 v231, v234, v235\n\t"
"v_mfma_f32_16x16x32_bf16 a[72:75], a[232:235], a[184:187], a[72:75]\n\t"
"v_cvt_pk_bf16_f32 v224, v224, v225\n\t"
"v_cvt_pk_bf16_f32 v225, v226, v227\n\t"
"v_mfma_f32_16x16x32_bf16 a[72:75], a[216:219], a[188:191], a[72:75]\n\t"
"v_cvt_pk_bf16_f32 v226, v236, v237\n\t"
"v_cvt_pk_bf16_f32 v227, v238, v239\n\t"
"v_mfma_f32_16x16x32_bf16 a[72:75], a[224:227], a[192:195], a[72:75]\n\t"
"ds_write_b128 v243, v[228:231] offset:9216\n\t"
"v_mfma_f32_16x16x32_bf16 a[80:83], a[224:227], a[196:199], a[80:83]\n\t"
"v_mfma_f32_16x16x32_bf16 a[80:83], a[216:219], a[192:195], a[80:83]\n\t"
"v_mfma_f32_16x16x32_bf16 a[80:83], a[232:235], a[188:191], a[80:83]\n\t"
"ds_write_b128 v243, v[224:227] offset:18432\n\t"
"v_mfma_f32_16x16x32_bf16 a[88:91], a[232:235], a[192:195], a[88:91]\n\t"
"v_mfma_f32_16x16x32_bf16 a[88:91], a[216:219], a[196:199], a[88:91]\n\t"
"v_mfma_f32_16x16x32_bf16 a[88:91], a[224:227], a[200:203], a[88:91]\n\t"
"ds_read_b64_tr_b16 v[232:233], v246 offset:512\n\t"
"v_mfma_f32_16x16x32_bf16 a[96:99], a[216:219], a[200:203], a[96:99]\n\t"
"buffer_load_dwordx4 a[216:219], off, s[80:83], s20 offset:1024 sc1\n\t"
"v_mfma_f32_16x16x32_bf16 a[96:99], a[232:235], a[196:199], a[96:99]\n\t"
"s_waitcnt vmcnt(8)\n\t"
"ds_read_b64_tr_b16 v[234:235], v246 offset:3584\n\t"
"v_mfma_f32_16x16x32_bf16 a[96:99], a[224:227], a[204:207], a[96:99]\n\t"
"ds_read_b64_tr_b16 v[236:237], v246 offset:6656\n\t"
"v_mfma_f32_16x16x32_bf16 a[104:107], a[236:239], a[168:171], a[104:107]\n\t"
"buffer_load_dwordx4 a[224:227], off, s[80:83], s20 offset:2048 sc1\n\t"
"v_mfma_f32_16x16x32_bf16 a[104:107], a[240:243], a[176:179], a[104:107]\n\t"
"s_mov_b32 s20, 1216512\n\t"
"ds_read_b64_tr_b16 v[238:239], v246 offset:9728\n\t"
"v_mfma_f32_16x16x32_bf16 a[104:107], a[244:247], a[172:175], a[104:107]\n\t"
"ds_read_b64_tr_b16 v[228:229], v246 offset:12800\n\t"
"v_mfma_f32_16x16x32_bf16 a[112:115], a[236:239], a[172:175], a[112:115]\n\t"
"ds_read_b64_tr_b16 v[230:231], v246 offset:15872\n\t"
"v_mfma_f32_16x16x32_bf16 a[112:115], a[244:247], a[176:179], a[112:115]\n\t"
"buffer_load_dwordx4 a[232:235], off, s[80:83], s20 offset:0 sc1\n\t"
"v_mfma_f32_16x16x32_bf16 a[112:115], a[240:243], a[180:183], a[112:115]\n\t"
"ds_read_b64_tr_b16 v[224:225], v246 offset:20480\n\t"
"v_mfma_f32_16x16x32_bf16 a[120:123], a[244:247], a[180:183], a[120:123]\n\t"
"ds_read_b64_tr_b16 v[226:227], v246 offset:23552\n\t"
"v_mfma_f32_16x16x32_bf16 a[120:123], a[236:239], a[176:179], a[120:123]\n\t"
"ds_read_b128 a[168:171], v244 offset:9216\n\t"
"v_mfma_f32_16x16x32_bf16 a[120:123], a[240:243], a[184:187], a[120:123]\n\t"
"ds_read_b128 a[172:175], v244 offset:9728\n\t"
"v_mfma_f32_16x16x32_bf16 a[128:131], a[236:239], a[180:183], a[128:131]\n\t"
"s_waitcnt lgkmcnt(8)\n\t"
"ds_read_b128 a[176:179], v244 offset:10240\n\t"
"v_mfma_f32_16x16x32_bf16 a[128:131], a[240:243], a[188:191], a[128:131]\n\t"
"s_waitcnt lgkmcnt(7)\n\t"
"ds_read_b128 a[180:183], v244 offset:10752\n\t"
"v_mfma_f32_16x16x32_bf16 a[128:131], a[244:247], a[184:187], a[128:131]\n\t"
"s_waitcnt lgkmcnt(6)\n\t"
"v_mfma_f32_16x16x32_bf16 v[232:235], v[232:235], v[220:223], 0\n\t"
"s_waitcnt lgkmcnt(4)\n\t"
"v_mfma_f32_16x16x32_bf16 v[236:239], v[236:239], v[220:223], 0\n\t"
"v_mfma_f32_16x16x32_bf16 v[228:231], v[228:231], v[220:223], 0\n\t"
"v_mfma_f32_16x16x32_bf16 v[224:227], v[224:227], v[220:223], 0\n\t"
"v_mfma_f32_16x16x32_bf16 a[136:139], a[236:239], a[184:187], a[136:139]\n\t"
"ds_read_b128 a[184:187], v244 offset:11264\n\t"
"v_mfma_f32_16x16x32_bf16 a[136:139], a[240:243], a[192:195], a[136:139]\n\t"
"v_cvt_pk_bf16_f32 v232, v232, v233\n\t"
"v_cvt_pk_bf16_f32 v233, v234, v235\n\t"
"v_mfma_f32_16x16x32_bf16 a[136:139], a[244:247], a[188:191], a[136:139]\n\t"
"v_cvt_pk_bf16_f32 v234, v236, v237\n\t"
"v_cvt_pk_bf16_f32 v235, v238, v239\n\t"
"v_mfma_f32_16x16x32_bf16 a[144:147], a[236:239], a[188:191], a[144:147]\n\t"
"ds_read_b128 a[188:191], v244 offset:11776\n\t"
"v_cvt_pk_bf16_f32 v228, v228, v229\n\t"
"v_mfma_f32_16x16x32_bf16 a[144:147], a[244:247], a[192:195], a[144:147]\n\t"
"v_cvt_pk_bf16_f32 v229, v230, v231\n\t"
"v_cvt_pk_bf16_f32 v230, v224, v225\n\t"
"v_mfma_f32_16x16x32_bf16 a[144:147], a[240:243], a[196:199], a[144:147]\n\t"
"v_cvt_pk_bf16_f32 v231, v226, v227\n\t"
"v_mfma_f32_16x16x32_bf16 a[152:155], a[240:243], a[200:203], a[152:155]\n\t"
"ds_read_b64_tr_b16 v[224:225], v246 offset:26624\n\t"
"v_mfma_f32_16x16x32_bf16 a[152:155], a[244:247], a[196:199], a[152:155]\n\t"
"ds_read_b64_tr_b16 v[226:227], v246 offset:29696\n\t"
"v_mfma_f32_16x16x32_bf16 a[152:155], a[236:239], a[192:195], a[152:155]\n\t"
"ds_read_b128 a[192:195], v244 offset:12288\n\t"
"v_mfma_f32_16x16x32_bf16 a[160:163], a[236:239], a[196:199], a[160:163]\n\t"
"buffer_load_dwordx4 a[236:239], off, s[80:83], s20 offset:1024 sc1\n\t"
"v_mfma_f32_16x16x32_bf16 a[160:163], a[244:247], a[200:203], a[160:163]\n\t"
"s_waitcnt vmcnt(8) lgkmcnt(6)\n\t"
"ds_read_b128 a[196:199], v244 offset:12800\n\t"
"v_mfma_f32_16x16x32_bf16 a[160:163], a[240:243], a[204:207], a[160:163]\n\t"
"s_waitcnt lgkmcnt(2)\n\t"
"ds_read_b128 a[200:203], v244 offset:13312\n\t"
"v_mfma_f32_16x16x32_bf16 v[224:227], v[224:227], v[220:223], 0\n\t"
"ds_read_b128 a[204:207], v244 offset:13824\n\t"
"v_mfma_f32_16x16x32_bf16 v[200:203], a[248:251], a[168:171], v[200:203]\n\t"
"buffer_load_dwordx4 a[240:243], off, s[80:83], s20 offset:2048 sc1\n\t"
"v_mfma_f32_16x16x32_bf16 v[200:203], a[220:223], a[176:179], v[200:203]\n\t"
"s_mov_b32 s20, 1228800\n\t"
"v_mfma_f32_16x16x32_bf16 v[200:203], v[248:251], a[172:175], v[200:203]\n\t"
"buffer_load_dwordx4 a[244:247], off, s[80:83], s20 offset:0 sc1\n\t"
"v_mfma_f32_16x16x32_bf16 v[208:211], a[248:251], a[172:175], v[208:211]\n\t"
"v_mfma_f32_16x16x32_bf16 v[208:211], v[248:251], a[176:179], v[208:211]\n\t"
"v_cvt_pk_bf16_f32 v224, v224, v225\n\t"
"v_mfma_f32_16x16x32_bf16 v[208:211], a[220:223], a[180:183], v[208:211]\n\t"
"s_waitcnt lgkmcnt(0)\n\t"
"s_barrier\n\t"
"v_mfma_f32_16x16x32_bf16 v[216:219], a[220:223], a[184:187], v[216:219]\n\t"
"v_cvt_pk_bf16_f32 v225, v226, v227\n\t"
"v_mfma_f32_16x16x32_bf16 v[216:219], v[248:251], a[180:183], v[216:219]\n\t"
"v_mfma_f32_16x16x32_bf16 v[216:219], a[248:251], a[176:179], v[216:219]\n\t"
"ds_write2st64_b64 v245, v[232:233], v[234:235] offset0:0 offset1:1\n\t"
"v_mfma_f32_16x16x32_bf16 a[4:7], a[248:251], a[180:183], a[4:7]\n\t"
"v_mfma_f32_16x16x32_bf16 a[4:7], v[248:251], a[184:187], a[4:7]\n\t"
"ds_read_b64_tr_b16 v[232:233], v246 offset:32768\n\t"
"v_mfma_f32_16x16x32_bf16 a[4:7], a[220:223], a[188:191], a[4:7]\n\t"
"ds_write2st64_b64 v245, v[228:229], v[230:231] offset0:2 offset1:3\n\t"
"v_mfma_f32_16x16x32_bf16 a[12:15], a[220:223], a[192:195], a[12:15]\n\t"
"v_mfma_f32_16x16x32_bf16 a[12:15], v[248:251], a[188:191], a[12:15]\n\t"
"ds_read_b64_tr_b16 v[234:235], v246 offset:35840\n\t"
"v_mfma_f32_16x16x32_bf16 a[12:15], a[248:251], a[184:187], a[12:15]\n\t"
"ds_write_b64 v245, v[224:225] offset:2048\n\t"
"v_mfma_f32_16x16x32_bf16 a[20:23], a[248:251], a[188:191], a[20:23]\n\t"
"ds_read_b64_tr_b16 v[228:229], v246 offset:40448\n\t"
"v_mfma_f32_16x16x32_bf16 a[20:23], v[248:251], a[192:195], a[20:23]\n\t"
"ds_read_b64_tr_b16 v[230:231], v246 offset:43520\n\t"
"v_mfma_f32_16x16x32_bf16 a[20:23], a[220:223], a[196:199], a[20:23]\n\t"
"ds_read_b64_tr_b16 v[224:225], v246 offset:46592\n\t"
"v_mfma_f32_16x16x32_bf16 a[28:31], v[248:251], a[196:199], a[28:31]\n\t"
"s_waitcnt lgkmcnt(4)\n\t"
"ds_read_b64_tr_b16 v[226:227], v246 offset:49664\n\t"
"v_mfma_f32_16x16x32_bf16 a[28:31], a[248:251], a[192:195], a[28:31]\n\t"
"v_mfma_f32_16x16x32_bf16 a[28:31], a[220:223], a[200:203], a[28:31]\n\t"
"v_mfma_f32_16x16x32_bf16 v[232:235], v[232:235], v[220:223], 0\n\t"
"s_waitcnt lgkmcnt(2)\n\t"
"v_mfma_f32_16x16x32_bf16 v[228:231], v[228:231], v[220:223], 0\n\t"
"v_mfma_f32_16x16x32_bf16 a[36:39], v[248:251], a[200:203], a[36:39]\n\t"
"buffer_load_dwordx4 v[248:251], off, s[80:83], s20 offset:1024 sc1\n\t"
"v_mfma_f32_16x16x32_bf16 a[36:39], a[248:251], a[196:199], a[36:39]\n\t"
"s_waitcnt vmcnt(8) lgkmcnt(0)\n\t"
"v_mfma_f32_16x16x32_bf16 a[36:39], a[220:223], a[204:207], a[36:39]\n\t"
"v_mfma_f32_16x16x32_bf16 v[224:227], v[224:227], v[220:223], 0\n\t"
"buffer_load_dwordx4 a[220:223], off, s[80:83], s20 offset:2048 sc1\n\t"
"v_mfma_f32_16x16x32_bf16 a[44:47], a[252:255], a[168:171], a[44:47]\n\t"
"s_mov_b32 s20, 1241088\n\t"
"v_cvt_pk_bf16_f32 v232, v232, v233\n\t"
"v_cvt_pk_bf16_f32 v233, v234, v235\n\t"
"v_mfma_f32_16x16x32_bf16 a[44:47], v[96:99], a[176:179], a[44:47]\n\t"
"v_cvt_pk_bf16_f32 v234, v228, v229\n\t"
"v_cvt_pk_bf16_f32 v235, v230, v231\n\t"
"v_mfma_f32_16x16x32_bf16 a[44:47], a[228:231], a[172:175], a[44:47]\n\t"
"ds_write2st64_b64 v245, v[232:233], v[234:235] offset0:5 offset1:6\n\t"
"v_mfma_f32_16x16x32_bf16 a[52:55], a[252:255], a[172:175], a[52:55]\n\t"
"buffer_load_dwordx4 a[248:251], off, s[80:83], s20 offset:0 sc1\n\t"
"v_mfma_f32_16x16x32_bf16 a[52:55], a[228:231], a[176:179], a[52:55]\n\t"
"ds_read_b64_tr_b16 v[228:229], v246 offset:52736\n\t"
"v_mfma_f32_16x16x32_bf16 a[52:55], v[96:99], a[180:183], a[52:55]\n\t"
"ds_read_b64_tr_b16 v[230:231], v246 offset:55808\n\t"
"v_cvt_pk_bf16_f32 v224, v224, v225\n\t"
"v_mfma_f32_16x16x32_bf16 a[60:63], v[96:99], a[184:187], a[60:63]\n\t"
"v_cvt_pk_bf16_f32 v225, v226, v227\n\t"
"v_mfma_f32_16x16x32_bf16 a[60:63], a[228:231], a[180:183], a[60:63]\n\t"
"v_mfma_f32_16x16x32_bf16 a[60:63], a[252:255], a[176:179], a[60:63]\n\t"
"v_mfma_f32_16x16x32_bf16 a[68:71], a[252:255], a[180:183], a[68:71]\n\t"
"v_mfma_f32_16x16x32_bf16 a[68:71], a[228:231], a[184:187], a[68:71]\n\t"
"v_subrev_u32 v243, 55296, v243\n\t"
"v_mfma_f32_16x16x32_bf16 a[68:71], v[96:99], a[188:191], a[68:71]\n\t"
"v_mfma_f32_16x16x32_bf16 a[76:79], a[228:231], a[188:191], a[76:79]\n\t"
"s_waitcnt lgkmcnt(0)\n\t"
"v_mfma_f32_16x16x32_bf16 a[76:79], v[96:99], a[192:195], a[76:79]\n\t"
"v_mfma_f32_16x16x32_bf16 a[76:79], a[252:255], a[184:187], a[76:79]\n\t"
"v_mfma_f32_16x16x32_bf16 v[228:231], v[228:231], v[220:223], 0\n\t"
"v_mfma_f32_16x16x32_bf16 a[84:87], a[252:255], a[188:191], a[84:87]\n\t"
"v_mfma_f32_16x16x32_bf16 a[84:87], v[96:99], a[196:199], a[84:87]\n\t"
"v_mfma_f32_16x16x32_bf16 a[84:87], a[228:231], a[192:195], a[84:87]\n\t"
"v_mfma_f32_16x16x32_bf16 a[92:95], a[252:255], a[192:195], a[92:95]\n\t"
"v_mfma_f32_16x16x32_bf16 a[92:95], a[228:231], a[196:199], a[92:95]\n\t"
"v_cvt_pk_bf16_f32 v226, v228, v229\n\t"
"v_cvt_pk_bf16_f32 v227, v230, v231\n\t"
"v_mfma_f32_16x16x32_bf16 a[92:95], v[96:99], a[200:203], a[92:95]\n\t"
"ds_write2st64_b64 v245, v[224:225], v[226:227] offset0:7 offset1:8\n\t"
"v_mfma_f32_16x16x32_bf16 a[100:103], a[228:231], a[200:203], a[100:103]\n\t"
"buffer_load_dwordx4 a[228:231], off, s[80:83], s20 offset:1024 sc1\n\t"
"v_mfma_f32_16x16x32_bf16 a[100:103], a[252:255], a[196:199], a[100:103]\n\t"
"s_waitcnt vmcnt(8)\n\t"
"v_mfma_f32_16x16x32_bf16 a[100:103], v[96:99], a[204:207], a[100:103]\n\t"
"ds_read_b64_tr_b16 v[236:237], v240 offset:1024\n\t"
"v_mfma_f32_16x16x32_bf16 a[108:111], a[212:215], a[168:171], a[108:111]\n\t"
"buffer_load_dwordx4 v[96:99], off, s[80:83], s20 offset:2048 sc1\n\t"
"v_mfma_f32_16x16x32_bf16 a[108:111], a[224:227], a[176:179], a[108:111]\n\t"
"s_mov_b32 s20, 1253376\n\t"
"ds_read_b64_tr_b16 v[238:239], v240 offset:2560\n\t"
"v_mfma_f32_16x16x32_bf16 a[108:111], a[216:219], a[172:175], a[108:111]\n\t"
"s_waitcnt lgkmcnt(2)\n\t"
"s_barrier\n\t"
"v_mfma_f32_16x16x32_bf16 a[116:119], a[212:215], a[172:175], a[116:119]\n\t"
"ds_read_b64_tr_b16 v[232:233], v241 offset:1024\n\t"
"v_mfma_f32_16x16x32_bf16 a[116:119], a[216:219], a[176:179], a[116:119]\n\t"
"buffer_load_dwordx4 a[252:255], off, s[80:83], s20 offset:0 sc1\n\t"
"v_mfma_f32_16x16x32_bf16 a[116:119], a[224:227], a[180:183], a[116:119]\n\t"
"ds_read_b64_tr_b16 v[234:235], v241 offset:2560\n\t"
"v_mfma_f32_16x16x32_bf16 a[124:127], a[224:227], a[184:187], a[124:127]\n\t"
"ds_read_b64_tr_b16 v[228:229], v240 offset:20992\n\t"
"v_mfma_f32_16x16x32_bf16 a[124:127], a[216:219], a[180:183], a[124:127]\n\t"
"ds_read_b64_tr_b16 v[230:231], v240 offset:22528\n\t"
"v_mfma_f32_16x16x32_bf16 a[124:127], a[212:215], a[176:179], a[124:127]\n\t"
"ds_read_b64_tr_b16 v[224:225], v241 offset:20992\n\t"
"v_mfma_f32_16x16x32_bf16 a[132:135], a[212:215], a[180:183], a[132:135]\n\t"
"ds_read_b64_tr_b16 v[226:227], v241 offset:22528\n\t"
"v_mfma_f32_16x16x32_bf16 a[132:135], a[216:219], a[184:187], a[132:135]\n\t"
"ds_read_b128 a[168:171], v244 offset:0\n\t"
"v_mfma_f32_16x16x32_bf16 a[132:135], a[224:227], a[188:191], a[132:135]\n\t"
"ds_read_b128 a[172:175], v244 offset:512\n\t"
"v_mfma_f32_16x16x32_bf16 a[140:143], a[212:215], a[184:187], a[140:143]\n\t"
"s_waitcnt lgkmcnt(8)\n\t"
"ds_read_b128 a[176:179], v244 offset:1024\n\t"
"v_mfma_f32_16x16x32_bf16 a[140:143], a[224:227], a[192:195], a[140:143]\n\t"
"s_waitcnt lgkmcnt(7)\n\t"
"ds_read_b128 a[180:183], v244 offset:1536\n\t"
"v_mfma_f32_16x16x32_bf16 a[140:143], a[216:219], a[188:191], a[140:143]\n\t"
"s_waitcnt lgkmcnt(6)\n\t"
"ds_read_b128 a[184:187], v244 offset:2048\n\t"
"v_mfma_f32_16x16x32_bf16 v[236:239], v[236:239], a[208:211], 0\n\t"
"s_waitcnt lgkmcnt(5)\n\t"
"v_mfma_f32_16x16x32_bf16 v[232:235], v[232:235], a[208:211], 0\n\t"
"v_mfma_f32_16x16x32_bf16 v[228:231], v[228:231], a[208:211], 0\n\t"
"v_mfma_f32_16x16x32_bf16 v[224:227], v[224:227], a[208:211], 0\n\t"
"v_mfma_f32_16x16x32_bf16 a[148:151], a[212:215], a[188:191], a[148:151]\n\t"
"ds_read_b128 a[188:191], v244 offset:2560\n\t"
"v_mfma_f32_16x16x32_bf16 a[148:151], a[224:227], a[196:199], a[148:151]\n\t"
"v_cvt_pk_bf16_f32 v236, v236, v237\n\t"
"v_cvt_pk_bf16_f32 v237, v238, v239\n\t"
"v_mfma_f32_16x16x32_bf16 a[148:151], a[216:219], a[192:195], a[148:151]\n\t"
"v_cvt_pk_bf16_f32 v238, v232, v233\n\t"
"v_cvt_pk_bf16_f32 v239, v234, v235\n\t"
"v_mfma_f32_16x16x32_bf16 a[156:159], a[212:215], a[192:195], a[156:159]\n\t"
"ds_read_b128 a[192:195], v244 offset:3072\n\t"
"v_cvt_pk_bf16_f32 v228, v228, v229\n\t"
"v_mfma_f32_16x16x32_bf16 a[156:159], a[216:219], a[196:199], a[156:159]\n\t"
"v_cvt_pk_bf16_f32 v229, v230, v231\n\t"
"v_cvt_pk_bf16_f32 v230, v224, v225\n\t"
"v_mfma_f32_16x16x32_bf16 a[156:159], a[224:227], a[200:203], a[156:159]\n\t"
"ds_write_b128 v243, v[236:239] offset:512\n\t"
"v_mfma_f32_16x16x32_bf16 a[164:167], a[216:219], a[200:203], a[164:167]\n\t"
"buffer_load_dwordx4 a[216:219], off, s[80:83], s20 offset:1024 sc1\n\t"
"v_mfma_f32_16x16x32_bf16 a[164:167], a[212:215], a[196:199], a[164:167]\n\t"
"s_waitcnt vmcnt(8) lgkmcnt(3)\n\t"
"ds_read_b64_tr_b16 v[232:233], v240 offset:40960\n\t"
"v_mfma_f32_16x16x32_bf16 a[164:167], a[224:227], a[204:207], a[164:167]\n\t"
"s_waitcnt lgkmcnt(3)\n\t"
"ds_read_b64_tr_b16 v[234:235], v240 offset:42496\n\t"
"v_cvt_pk_bf16_f32 v231, v226, v227\n\t"
"Lst11_%=:\n\t"
"v_mfma_f32_16x16x32_bf16 v[0:3], a[232:235], a[168:171], v[0:3]\n\t"
"Lpg8_%=:\n\t"
"s_cbranch_vccnz Lpg12_%=\n\t"
"ds_read_b64_tr_b16 v[224:225], v241 offset:40960\n\t"
"v_mfma_f32_16x16x32_bf16 v[0:3], a[240:243], a[176:179], v[0:3]\n\t"
"buffer_load_dwordx4 a[212:215], off, s[80:83], s20 offset:2048 sc1\n\t"
"v_mfma_f32_16x16x32_bf16 v[0:3], a[236:239], a[172:175], v[0:3]\n\t"
"s_mov_b32 s20, 1265664\n\t"
"ds_write_b128 v243, v[228:231] offset:9728\n\t"
"v_mfma_f32_16x16x32_bf16 v[4:7], a[232:235], a[172:175], v[4:7]\n\t"
"buffer_load_dwordx4 a[224:227], off, s[80:83], s20 offset:0 sc1\n\t"
"v_mfma_f32_16x16x32_bf16 v[4:7], a[236:239], a[176:179], v[4:7]\n\t"
"ds_read_b64_tr_b16 v[226:227], v241 offset:42496\n\t"
"v_mfma_f32_16x16x32_bf16 v[4:7], a[240:243], a[180:183], v[4:7]\n\t"
"ds_read_b128 a[196:199], v244 offset:3584\n\t"
"v_add_u32 v240, 59904, v240\n\t"
"v_add_u32 v241, 59904, v241\n\t"
"v_mfma_f32_16x16x32_bf16 v[8:11], a[236:239], a[180:183], v[8:11]\n\t"
"ds_read_b64_tr_b16 v[236:237], v240 offset:1024\n\t"
"v_mfma_f32_16x16x32_bf16 v[8:11], a[232:235], a[176:179], v[8:11]\n\t"
"ds_read_b64_tr_b16 v[238:239], v240 offset:2560\n\t"
"v_mfma_f32_16x16x32_bf16 v[8:11], a[240:243], a[184:187], v[8:11]\n\t"
"ds_read_b64_tr_b16 v[228:229], v241 offset:1024\n\t"
"v_mfma_f32_16x16x32_bf16 v[12:15], a[232:235], a[180:183], v[12:15]\n\t"
"s_waitcnt lgkmcnt(7)\n\t"
"ds_read_b64_tr_b16 v[230:231], v241 offset:2560\n\t"
"v_mfma_f32_16x16x32_bf16 v[12:15], a[240:243], a[188:191], v[12:15]\n\t"
"s_waitcnt lgkmcnt(5)\n\t"
"ds_read_b128 a[200:203], v244 offset:4096\n\t"
"v_mfma_f32_16x16x32_bf16 v[12:15], a[236:239], a[184:187], v[12:15]\n\t"
"ds_read_b128 a[204:207], v244 offset:4608\n\t"
"v_mfma_f32_16x16x32_bf16 v[232:235], v[232:235], a[208:211], 0\n\t"
"s_waitcnt lgkmcnt(4)\n\t"
"v_mfma_f32_16x16x32_bf16 v[224:227], v[224:227], a[208:211], 0\n\t"
"v_mfma_f32_16x16x32_bf16 v[236:239], v[236:239], a[208:211], 0\n\t"
"s_waitcnt lgkmcnt(2)\n\t"
"v_mfma_f32_16x16x32_bf16 v[228:231], v[228:231], a[208:211], 0\n\t"
"v_mfma_f32_16x16x32_bf16 v[16:19], a[232:235], a[184:187], v[16:19]\n\t"
"v_mfma_f32_16x16x32_bf16 v[16:19], a[240:243], a[192:195], v[16:19]\n\t"
"v_cvt_pk_bf16_f32 v232, v232, v233\n\t"
"v_cvt_pk_bf16_f32 v233, v234, v235\n\t"
"v_mfma_f32_16x16x32_bf16 v[16:19], a[236:239], a[188:191], v[16:19]\n\t"
"v_cvt_pk_bf16_f32 v234, v224, v225\n\t"
"v_cvt_pk_bf16_f32 v235, v226, v227\n\t"
"v_mfma_f32_16x16x32_bf16 v[20:23], a[232:235], a[188:191], v[20:23]\n\t"
"s_waitcnt lgkmcnt(1)\n\t"
"v_cvt_pk_bf16_f32 v236, v236, v237\n\t"
"v_cvt_pk_bf16_f32 v237, v238, v239\n\t"
"v_mfma_f32_16x16x32_bf16 v[20:23], a[236:239], a[192:195], v[20:23]\n\t"
"v_cvt_pk_bf16_f32 v238, v228, v229\n\t"
"v_cvt_pk_bf16_f32 v239, v230, v231\n\t"
"v_mfma_f32_16x16x32_bf16 v[20:23], a[240:243], a[196:199], v[20:23]\n\t"
"ds_write_b128 v243, v[232:235] offset:18944\n\t"
"v_mfma_f32_16x16x32_bf16 v[24:27], a[240:243], a[200:203], v[24:27]\n\t"
"s_waitcnt lgkmcnt(1)\n\t"
"v_mfma_f32_16x16x32_bf16 v[24:27], a[236:239], a[196:199], v[24:27]\n\t"
"ds_read_b64_tr_b16 v[224:225], v240 offset:20992\n\t"
"v_mfma_f32_16x16x32_bf16 v[24:27], a[232:235], a[192:195], v[24:27]\n\t"
"ds_write_b128 v243, v[236:239] offset:28160\n\t"
"v_mfma_f32_16x16x32_bf16 v[28:31], a[232:235], a[196:199], v[28:31]\n\t"
"buffer_load_dwordx4 a[232:235], off, s[80:83], s20 offset:1024 sc1\n\t"
"v_mfma_f32_16x16x32_bf16 v[28:31], a[236:239], a[200:203], v[28:31]\n\t"
"s_waitcnt vmcnt(8)\n\t"
"ds_read_b64_tr_b16 v[226:227], v240 offset:22528\n\t"
"v_mfma_f32_16x16x32_bf16 v[28:31], a[240:243], a[204:207], v[28:31]\n\t"
"ds_read_b64_tr_b16 v[228:229], v241 offset:20992\n\t"
"v_mfma_f32_16x16x32_bf16 v[32:35], a[244:247], a[168:171], v[32:35]\n\t"
"ds_read_b64_tr_b16 v[230:231], v241 offset:22528\n\t"
"v_mfma_f32_16x16x32_bf16 v[32:35], a[220:223], a[176:179], v[32:35]\n\t"
"ds_read_b64_tr_b16 v[232:233], v240 offset:40960\n\t"
"v_mfma_f32_16x16x32_bf16 v[32:35], v[248:251], a[172:175], v[32:35]\n\t"
"ds_read_b64_tr_b16 v[234:235], v240 offset:42496\n\t"
"v_mfma_f32_16x16x32_bf16 v[36:39], a[244:247], a[172:175], v[36:39]\n\t"
"buffer_load_dwordx4 a[236:239], off, s[80:83], s20 offset:2048 sc1\n\t"
"v_mfma_f32_16x16x32_bf16 v[36:39], v[248:251], a[176:179], v[36:39]\n\t"
"s_mov_b32 s20, 1277952\n\t"
"ds_read_b64_tr_b16 v[236:237], v241 offset:40960\n\t"
"v_mfma_f32_16x16x32_bf16 v[36:39], a[220:223], a[180:183], v[36:39]\n\t"
"ds_read_b64_tr_b16 v[238:239], v241 offset:42496\n\t"
"v_mfma_f32_16x16x32_bf16 v[40:43], a[244:247], a[176:179], v[40:43]\n\t"
"buffer_load_dwordx4 a[240:243], off, s[80:83], s20 offset:0 sc1\n\t"
"v_mfma_f32_16x16x32_bf16 v[40:43], a[220:223], a[184:187], v[40:43]\n\t"
"s_waitcnt lgkmcnt(4)\n\t"
"v_mfma_f32_16x16x32_bf16 v[40:43], v[248:251], a[180:183], v[40:43]\n\t"
"s_waitcnt lgkmcnt(2)\n\t"
"v_mfma_f32_16x16x32_bf16 v[224:227], v[224:227], a[208:211], 0\n\t"
"v_mfma_f32_16x16x32_bf16 v[228:231], v[228:231], a[208:211], 0\n\t"
"s_waitcnt lgkmcnt(0)\n\t"
"v_mfma_f32_16x16x32_bf16 v[232:235], v[232:235], a[208:211], 0\n\t"
"v_mfma_f32_16x16x32_bf16 v[236:239], v[236:239], a[208:211], 0\n\t"
"v_mfma_f32_16x16x32_bf16 v[44:47], a[244:247], a[180:183], v[44:47]\n\t"
"buffer_load_dwordx4 a[208:211], off, s[80:83], s20 offset:1024 sc1\n\t"
"v_mfma_f32_16x16x32_bf16 v[44:47], a[220:223], a[188:191], v[44:47]\n\t"
"v_cvt_pk_bf16_f32 v224, v224, v225\n\t"
"v_cvt_pk_bf16_f32 v225, v226, v227\n\t"
"v_mfma_f32_16x16x32_bf16 v[44:47], v[248:251], a[184:187], v[44:47]\n\t"
"v_cvt_pk_bf16_f32 v226, v228, v229\n\t"
"v_cvt_pk_bf16_f32 v227, v230, v231\n\t"
"v_mfma_f32_16x16x32_bf16 v[48:51], a[244:247], a[184:187], v[48:51]\n\t"
"v_cvt_pk_bf16_f32 v232, v232, v233\n\t"
"v_cvt_pk_bf16_f32 v233, v234, v235\n\t"
"v_mfma_f32_16x16x32_bf16 v[48:51], v[248:251], a[188:191], v[48:51]\n\t"
"v_cvt_pk_bf16_f32 v234, v236, v237\n\t"
"v_cvt_pk_bf16_f32 v235, v238, v239\n\t"
"v_mfma_f32_16x16x32_bf16 v[48:51], a[220:223], a[192:195], v[48:51]\n\t"
"ds_write_b128 v243, v[224:227] offset:37376\n\t"
"v_mfma_f32_16x16x32_bf16 v[52:55], a[220:223], a[196:199], v[52:55]\n\t"
"v_mfma_f32_16x16x32_bf16 v[52:55], v[248:251], a[192:195], v[52:55]\n\t"
"v_mfma_f32_16x16x32_bf16 v[52:55], a[244:247], a[188:191], v[52:55]\n\t"
"ds_write_b128 v243, v[232:235] offset:46592\n\t"
"v_mfma_f32_16x16x32_bf16 v[56:59], a[244:247], a[192:195], v[56:59]\n\t"
"v_mfma_f32_16x16x32_bf16 v[56:59], v[248:251], a[196:199], v[56:59]\n\t"
"v_mfma_f32_16x16x32_bf16 v[56:59], a[220:223], a[200:203], v[56:59]\n\t"
"ds_read_b64_tr_b16 v[228:229], v246 offset:1024\n\t"
"v_mfma_f32_16x16x32_bf16 v[60:63], v[248:251], a[200:203], v[60:63]\n\t"
"buffer_load_dwordx4 v[248:251], off, s[80:83], s20 offset:2048 sc1\n\t"
"v_mfma_f32_16x16x32_bf16 v[60:63], a[244:247], a[196:199], v[60:63]\n\t"
"s_waitcnt vmcnt(9)\n\t"
"ds_read_b64_tr_b16 v[230:231], v246 offset:4096\n\t"
"s_mov_b32 s20, 1511424\n\t"
"v_mfma_f32_16x16x32_bf16 v[60:63], a[220:223], a[204:207], v[60:63]\n\t"
"ds_read_b64_tr_b16 v[236:237], v246 offset:7168\n\t"
"v_mfma_f32_16x16x32_bf16 v[64:67], a[248:251], a[168:171], v[64:67]\n\t"
"buffer_load_dwordx4 v[240:243], off, s[80:83], s20 offset:0 sc1\n\t"
"v_mfma_f32_16x16x32_bf16 v[64:67], v[96:99], a[176:179], v[64:67]\n\t"
"ds_read_b64_tr_b16 v[238:239], v246 offset:10240\n\t"
"v_mfma_f32_16x16x32_bf16 v[64:67], a[228:231], a[172:175], v[64:67]\n\t"
"ds_read_b64_tr_b16 v[224:225], v246 offset:13312\n\t"
"v_mfma_f32_16x16x32_bf16 v[68:71], a[248:251], a[172:175], v[68:71]\n\t"
"ds_read_b64_tr_b16 v[226:227], v246 offset:16384\n\t"
"v_mfma_f32_16x16x32_bf16 v[68:71], a[228:231], a[176:179], v[68:71]\n\t"
"buffer_load_dwordx4 a[220:223], off, s[80:83], s20 offset:1024 sc1\n\t"
"v_mfma_f32_16x16x32_bf16 v[68:71], v[96:99], a[180:183], v[68:71]\n\t"
"ds_read_b64_tr_b16 v[232:233], v246 offset:20992\n\t"
"v_mfma_f32_16x16x32_bf16 v[72:75], a[228:231], a[180:183], v[72:75]\n\t"
"ds_read_b64_tr_b16 v[234:235], v246 offset:24064\n\t"
"v_mfma_f32_16x16x32_bf16 v[72:75], a[248:251], a[176:179], v[72:75]\n\t"
"buffer_load_dwordx4 a[244:247], off, s[80:83], s20 offset:2048 sc1\n\t"
"v_mfma_f32_16x16x32_bf16 v[72:75], v[96:99], a[184:187], v[72:75]\n\t"
"ds_read_b128 a[168:171], v244 offset:9216\n\t"
"v_mfma_f32_16x16x32_bf16 v[76:79], a[248:251], a[180:183], v[76:79]\n\t"
"s_waitcnt lgkmcnt(7)\n\t"
"ds_read_b128 a[172:175], v244 offset:9728\n\t"
"v_mfma_f32_16x16x32_bf16 v[76:79], v[96:99], a[188:191], v[76:79]\n\t"
"s_waitcnt lgkmcnt(6)\n\t"
"ds_read_b128 a[176:179], v244 offset:10240\n\t"
"v_mfma_f32_16x16x32_bf16 v[76:79], a[228:231], a[184:187], v[76:79]\n\t"
"s_waitcnt lgkmcnt(5)\n\t"
"ds_read_b128 a[180:183], v244 offset:10752\n\t"
"v_mfma_f32_16x16x32_bf16 v[228:231], v[228:231], v[220:223], 0\n\t"
"s_waitcnt lgkmcnt(4)\n\t"
"v_mfma_f32_16x16x32_bf16 v[236:239], v[236:239], v[220:223], 0\n\t"
"v_mfma_f32_16x16x32_bf16 v[224:227], v[224:227], v[220:223], 0\n\t"
"v_mfma_f32_16x16x32_bf16 v[232:235], v[232:235], v[220:223], 0\n\t"
"v_mfma_f32_16x16x32_bf16 v[80:83], a[248:251], a[184:187], v[80:83]\n\t"
"s_mov_b32 s20, 1523712\n\t"
"ds_read_b128 a[184:187], v244 offset:11264\n\t"
"v_mfma_f32_16x16x32_bf16 v[80:83], v[96:99], a[192:195], v[80:83]\n\t"
"v_cvt_pk_bf16_f32 v228, v228, v229\n\t"
"v_cvt_pk_bf16_f32 v229, v230, v231\n\t"
"v_mfma_f32_16x16x32_bf16 v[80:83], a[228:231], a[188:191], v[80:83]\n\t"
"v_cvt_pk_bf16_f32 v230, v236, v237\n\t"
"v_cvt_pk_bf16_f32 v231, v238, v239\n\t"
"v_mfma_f32_16x16x32_bf16 v[84:87], a[248:251], a[188:191], v[84:87]\n\t"
"ds_read_b128 a[188:191], v244 offset:11776\n\t"
"v_cvt_pk_bf16_f32 v224, v224, v225\n\t"
"v_mfma_f32_16x16x32_bf16 v[84:87], a[228:231], a[192:195], v[84:87]\n\t"
"v_cvt_pk_bf16_f32 v225, v226, v227\n\t"
"v_cvt_pk_bf16_f32 v226, v232, v233\n\t"
"v_mfma_f32_16x16x32_bf16 v[84:87], v[96:99], a[196:199], v[84:87]\n\t"
"v_cvt_pk_bf16_f32 v227, v234, v235\n\t"
"v_mfma_f32_16x16x32_bf16 v[88:91], v[96:99], a[200:203], v[88:91]\n\t"
"ds_read_b64_tr_b16 v[232:233], v246 offset:27136\n\t"
"v_mfma_f32_16x16x32_bf16 v[88:91], a[228:231], a[196:199], v[88:91]\n\t"
"ds_read_b64_tr_b16 v[234:235], v246 offset:30208\n\t"
"v_mfma_f32_16x16x32_bf16 v[88:91], a[248:251], a[192:195], v[88:91]\n\t"
"buffer_load_dwordx4 v[236:239], off, s[80:83], s20 offset:0 sc1\n\t"
"v_mfma_f32_16x16x32_bf16 v[92:95], a[248:251], a[196:199], v[92:95]\n\t"
"ds_read_b128 a[192:195], v244 offset:12288\n\t"
"v_mfma_f32_16x16x32_bf16 v[92:95], a[228:231], a[200:203], v[92:95]\n\t"
"buffer_load_dwordx4 a[248:251], off, s[80:83], s20 offset:1024 sc1\n\t"
"v_mfma_f32_16x16x32_bf16 v[92:95], v[96:99], a[204:207], v[92:95]\n\t"
"s_waitcnt vmcnt(11) lgkmcnt(1)\n\t"
"ds_read_b128 a[196:199], v244 offset:12800\n\t"
"v_mfma_f32_16x16x32_bf16 v[232:235], v[232:235], v[220:223], 0\n\t"
"ds_read_b128 a[200:203], v244 offset:13312\n\t"
"v_mfma_f32_16x16x32_bf16 v[100:103], a[252:255], a[168:171], v[100:103]\n\t"
"ds_read_b128 a[204:207], v244 offset:13824\n\t"
"v_mfma_f32_16x16x32_bf16 v[100:103], a[212:215], a[176:179], v[100:103]\n\t"
"buffer_load_dwordx4 v[96:99], off, s[80:83], s20 offset:2048 sc1\n\t"
"v_mfma_f32_16x16x32_bf16 v[100:103], a[216:219], a[172:175], v[100:103]\n\t"
"s_mov_b32 s20, 1536000\n\t"
"v_mfma_f32_16x16x32_bf16 v[104:107], a[252:255], a[172:175], v[104:107]\n\t"
"buffer_load_dwordx4 a[228:231], off, s[80:83], s20 offset:0 sc1\n\t"
"v_mfma_f32_16x16x32_bf16 v[104:107], a[216:219], a[176:179], v[104:107]\n\t"
"v_cvt_pk_bf16_f32 v232, v232, v233\n\t"
"v_mfma_f32_16x16x32_bf16 v[104:107], a[212:215], a[180:183], v[104:107]\n\t"
"s_waitcnt lgkmcnt(0)\n\t"
"s_barrier\n\t"
"v_mfma_f32_16x16x32_bf16 v[108:111], a[212:215], a[184:187], v[108:111]\n\t"
"v_cvt_pk_bf16_f32 v233, v234, v235\n\t"
"v_mfma_f32_16x16x32_bf16 v[108:111], a[216:219], a[180:183], v[108:111]\n\t"
"v_mfma_f32_16x16x32_bf16 v[108:111], a[252:255], a[176:179], v[108:111]\n\t"
"ds_write2st64_b64 v245, v[228:229], v[230:231] offset0:0 offset1:1\n\t"
"v_mfma_f32_16x16x32_bf16 v[112:115], a[252:255], a[180:183], v[112:115]\n\t"
"v_mfma_f32_16x16x32_bf16 v[112:115], a[216:219], a[184:187], v[112:115]\n\t"
"ds_read_b64_tr_b16 v[228:229], v246 offset:33280\n\t"
"v_mfma_f32_16x16x32_bf16 v[112:115], a[212:215], a[188:191], v[112:115]\n\t"
"ds_write2st64_b64 v245, v[224:225], v[226:227] offset0:2 offset1:3\n\t"
"v_mfma_f32_16x16x32_bf16 v[116:119], a[212:215], a[192:195], v[116:119]\n\t"
"v_mfma_f32_16x16x32_bf16 v[116:119], a[216:219], a[188:191], v[116:119]\n\t"
"ds_read_b64_tr_b16 v[230:231], v246 offset:36352\n\t"
"v_mfma_f32_16x16x32_bf16 v[116:119], a[252:255], a[184:187], v[116:119]\n\t"
"ds_write_b64 v245, v[232:233] offset:2048\n\t"
"v_mfma_f32_16x16x32_bf16 v[120:123], a[252:255], a[188:191], v[120:123]\n\t"
"ds_read_b64_tr_b16 v[224:225], v246 offset:40960\n\t"
"v_mfma_f32_16x16x32_bf16 v[120:123], a[216:219], a[192:195], v[120:123]\n\t"
"ds_read_b64_tr_b16 v[226:227], v246 offset:44032\n\t"
"v_mfma_f32_16x16x32_bf16 v[120:123], a[212:215], a[196:199], v[120:123]\n\t"
"ds_read_b64_tr_b16 v[232:233], v246 offset:47104\n\t"
"v_mfma_f32_16x16x32_bf16 v[124:127], a[216:219], a[196:199], v[124:127]\n\t"
"s_waitcnt lgkmcnt(4)\n\t"
"ds_read_b64_tr_b16 v[234:235], v246 offset:50176\n\t"
"v_mfma_f32_16x16x32_bf16 v[124:127], a[252:255], a[192:195], v[124:127]\n\t"
"v_mfma_f32_16x16x32_bf16 v[124:127], a[212:215], a[200:203], v[124:127]\n\t"
"v_mfma_f32_16x16x32_bf16 v[228:231], v[228:231], v[220:223], 0\n\t"
"s_waitcnt lgkmcnt(2)\n\t"
"v_mfma_f32_16x16x32_bf16 v[224:227], v[224:227], v[220:223], 0\n\t"
"v_mfma_f32_16x16x32_bf16 v[128:131], a[216:219], a[200:203], v[128:131]\n\t"
"buffer_load_dwordx4 a[216:219], off, s[80:83], s20 offset:1024 sc1\n\t"
"v_mfma_f32_16x16x32_bf16 v[128:131], a[252:255], a[196:199], v[128:131]\n\t"
"s_waitcnt vmcnt(11) lgkmcnt(0)\n\t"
"v_mfma_f32_16x16x32_bf16 v[128:131], a[212:215], a[204:207], v[128:131]\n\t"
"v_mfma_f32_16x16x32_bf16 v[232:235], v[232:235], v[220:223], 0\n\t"
"buffer_load_dwordx4 a[212:215], off, s[80:83], s20 offset:2048 sc1\n\t"
"v_mfma_f32_16x16x32_bf16 v[132:135], a[224:227], a[168:171], v[132:135]\n\t"
"s_mov_b32 s20, 1548288\n\t"
"v_cvt_pk_bf16_f32 v228, v228, v229\n\t"
"v_cvt_pk_bf16_f32 v229, v230, v231\n\t"
"v_mfma_f32_16x16x32_bf16 v[132:135], a[236:239], a[176:179], v[132:135]\n\t"
"v_cvt_pk_bf16_f32 v230, v224, v225\n\t"
"v_cvt_pk_bf16_f32 v231, v226, v227\n\t"
"v_mfma_f32_16x16x32_bf16 v[132:135], a[232:235], a[172:175], v[132:135]\n\t"
"ds_write2st64_b64 v245, v[228:229], v[230:231] offset0:5 offset1:6\n\t"
"v_mfma_f32_16x16x32_bf16 v[136:139], a[224:227], a[172:175], v[136:139]\n\t"
"buffer_load_dwordx4 a[252:255], off, s[80:83], s20 offset:0 sc1\n\t"
"v_mfma_f32_16x16x32_bf16 v[136:139], a[232:235], a[176:179], v[136:139]\n\t"
"ds_read_b64_tr_b16 v[224:225], v246 offset:53248\n\t"
"v_mfma_f32_16x16x32_bf16 v[136:139], a[236:239], a[180:183], v[136:139]\n\t"
"ds_read_b64_tr_b16 v[226:227], v246 offset:56320\n\t"
"v_cvt_pk_bf16_f32 v232, v232, v233\n\t"
"v_mfma_f32_16x16x32_bf16 v[140:143], a[236:239], a[184:187], v[140:143]\n\t"
"v_cvt_pk_bf16_f32 v233, v234, v235\n\t"
"v_mfma_f32_16x16x32_bf16 v[140:143], a[232:235], a[180:183], v[140:143]\n\t"
"v_mfma_f32_16x16x32_bf16 v[140:143], a[224:227], a[176:179], v[140:143]\n\t"
"v_mfma_f32_16x16x32_bf16 v[144:147], a[224:227], a[180:183], v[144:147]\n\t"
"v_mfma_f32_16x16x32_bf16 v[144:147], a[232:235], a[184:187], v[144:147]\n\t"
"v_mfma_f32_16x16x32_bf16 v[144:147], a[236:239], a[188:191], v[144:147]\n\t"
"v_mfma_f32_16x16x32_bf16 v[148:151], a[232:235], a[188:191], v[148:151]\n\t"
"s_waitcnt lgkmcnt(0)\n\t"
"v_mfma_f32_16x16x32_bf16 v[148:151], a[236:239], a[192:195], v[148:151]\n\t"
"v_mfma_f32_16x16x32_bf16 v[148:151], a[224:227], a[184:187], v[148:151]\n\t"
"v_mfma_f32_16x16x32_bf16 v[224:227], v[224:227], v[220:223], 0\n\t"
"v_mfma_f32_16x16x32_bf16 v[152:155], a[224:227], a[188:191], v[152:155]\n\t"
"v_mfma_f32_16x16x32_bf16 v[152:155], a[236:239], a[196:199], v[152:155]\n\t"
"v_mfma_f32_16x16x32_bf16 v[152:155], a[232:235], a[192:195], v[152:155]\n\t"
"v_mfma_f32_16x16x32_bf16 v[156:159], a[224:227], a[192:195], v[156:159]\n\t"
"v_mfma_f32_16x16x32_bf16 v[156:159], a[232:235], a[196:199], v[156:159]\n\t"
"v_cvt_pk_bf16_f32 v234, v224, v225\n\t"
"v_cvt_pk_bf16_f32 v235, v226, v227\n\t"
"v_mfma_f32_16x16x32_bf16 v[156:159], a[236:239], a[200:203], v[156:159]\n\t"
"ds_write2st64_b64 v245, v[232:233], v[234:235] offset0:7 offset1:8\n\t"
"v_mfma_f32_16x16x32_bf16 v[160:163], a[232:235], a[200:203], v[160:163]\n\t"
"buffer_load_dwordx4 a[232:235], off, s[80:83], s20 offset:1024 sc1\n\t"
"v_mfma_f32_16x16x32_bf16 v[160:163], a[224:227], a[196:199], v[160:163]\n\t"
"s_waitcnt vmcnt(11)\n\t"
"v_mfma_f32_16x16x32_bf16 v[160:163], a[236:239], a[204:207], v[160:163]\n\t"
"v_mfma_f32_16x16x32_bf16 v[164:167], a[240:243], a[168:171], v[164:167]\n\t"
"buffer_load_dwordx4 a[224:227], off, s[80:83], s20 offset:2048 sc1\n\t"
"v_mfma_f32_16x16x32_bf16 v[164:167], v[248:251], a[176:179], v[164:167]\n\t"
"s_mov_b32 s20, 1560576\n\t"
"v_mfma_f32_16x16x32_bf16 v[164:167], a[208:211], a[172:175], v[164:167]\n\t"
"s_waitcnt lgkmcnt(0)\n\t"
"s_barrier\n\t"
"v_mfma_f32_16x16x32_bf16 v[168:171], a[240:243], a[172:175], v[168:171]\n\t"
"ds_read_b128 a[168:171], v244 offset:0\n\t"
"v_mfma_f32_16x16x32_bf16 v[168:171], a[208:211], a[176:179], v[168:171]\n\t"
"buffer_load_dwordx4 a[236:239], off, s[80:83], s20 offset:0 sc1\n\t"
"v_mfma_f32_16x16x32_bf16 v[168:171], v[248:251], a[180:183], v[168:171]\n\t"
"ds_read_b128 a[172:175], v244 offset:512\n\t"
"v_mfma_f32_16x16x32_bf16 v[172:175], v[248:251], a[184:187], v[172:175]\n\t"
"v_mfma_f32_16x16x32_bf16 v[172:175], a[208:211], a[180:183], v[172:175]\n\t"
"v_mfma_f32_16x16x32_bf16 v[172:175], a[240:243], a[176:179], v[172:175]\n\t"
"ds_read_b128 a[176:179], v244 offset:1024\n\t"
"v_mfma_f32_16x16x32_bf16 v[176:179], a[240:243], a[180:183], v[176:179]\n\t"
"ds_read_b128 a[180:183], v244 offset:1536\n\t"
"v_mfma_f32_16x16x32_bf16 v[176:179], a[208:211], a[184:187], v[176:179]\n\t"
"v_mfma_f32_16x16x32_bf16 v[176:179], v[248:251], a[188:191], v[176:179]\n\t"
"v_mfma_f32_16x16x32_bf16 v[180:183], v[248:251], a[192:195], v[180:183]\n\t"
"v_mfma_f32_16x16x32_bf16 v[180:183], a[208:211], a[188:191], v[180:183]\n\t"
"v_mfma_f32_16x16x32_bf16 v[180:183], a[240:243], a[184:187], v[180:183]\n\t"
"ds_read_b128 a[184:187], v244 offset:2048\n\t"
"v_mfma_f32_16x16x32_bf16 v[184:187], a[240:243], a[188:191], v[184:187]\n\t"
"ds_read_b128 a[188:191], v244 offset:2560\n\t"
"v_mfma_f32_16x16x32_bf16 v[184:187], a[208:211], a[192:195], v[184:187]\n\t"
"v_mfma_f32_16x16x32_bf16 v[184:187], v[248:251], a[196:199], v[184:187]\n\t"
"v_mfma_f32_16x16x32_bf16 v[188:191], v[248:251], a[200:203], v[188:191]\n\t"
"v_xor_b32 v246, 0xc0, v246\n\t"
"v_mfma_f32_16x16x32_bf16 v[188:191], a[208:211], a[196:199], v[188:191]\n\t"
"v_mfma_f32_16x16x32_bf16 v[188:191], a[240:243], a[192:195], v[188:191]\n\t"
"ds_read_b128 a[192:195], v244 offset:3072\n\t"
"v_mfma_f32_16x16x32_bf16 v[192:195], a[240:243], a[196:199], v[192:195]\n\t"
"buffer_load_dwordx4 a[240:243], off, s[80:83], s20 offset:1024 sc1\n\t"
"v_mfma_f32_16x16x32_bf16 v[192:195], a[208:211], a[200:203], v[192:195]\n\t"
"s_waitcnt vmcnt(11) lgkmcnt(4)\n\t"
"ds_read_b128 a[196:199], v244 offset:3584\n\t"
"v_mfma_f32_16x16x32_bf16 v[192:195], v[248:251], a[204:207], v[192:195]\n\t"
"ds_read_b128 a[200:203], v244 offset:4096\n\t"
"Lst12_%=:\n\t"
"v_mfma_f32_16x16x32_bf16 v[0:3], v[240:243], a[168:171], v[0:3]\n\t"
"buffer_load_dwordx4 v[248:251], off, s[80:83], s20 offset:2048 sc1\n\t"
"v_mfma_f32_16x16x32_bf16 v[0:3], a[244:247], a[176:179], v[0:3]\n\t"
"s_waitcnt lgkmcnt(5)\n\t"
"ds_read_b128 a[204:207], v244 offset:4608\n\t"
"s_mov_b32 s20, 1572864\n\t"
"v_mfma_f32_16x16x32_bf16 v[0:3], a[220:223], a[172:175], v[0:3]\n\t"
"v_mfma_f32_16x16x32_bf16 v[4:7], v[240:243], a[172:175], v[4:7]\n\t"
"buffer_load_dwordx4 a[208:211], off, s[80:83], s20 offset:0 sc1\n\t"
"v_mfma_f32_16x16x32_bf16 v[4:7], a[220:223], a[176:179], v[4:7]\n\t"
"s_waitcnt lgkmcnt(5)\n\t"
"v_mfma_f32_16x16x32_bf16 v[4:7], a[244:247], a[180:183], v[4:7]\n\t"
"v_mfma_f32_16x16x32_bf16 v[8:11], a[244:247], a[184:187], v[8:11]\n\t"
"s_waitcnt lgkmcnt(4)\n\t"
"v_mfma_f32_16x16x32_bf16 v[8:11], a[220:223], a[180:183], v[8:11]\n\t"
"v_mfma_f32_16x16x32_bf16 v[8:11], v[240:243], a[176:179], v[8:11]\n\t"
"ds_read_b64_tr_b16 v[228:229], v246 offset:1024\n\t"
"v_mfma_f32_16x16x32_bf16 v[12:15], v[240:243], a[180:183], v[12:15]\n\t"
"s_waitcnt lgkmcnt(4)\n\t"
"ds_read_b64_tr_b16 v[230:231], v246 offset:4096\n\t"
"v_mfma_f32_16x16x32_bf16 v[12:15], a[220:223], a[184:187], v[12:15]\n\t"
"ds_read_b64_tr_b16 v[224:225], v246 offset:7168\n\t"
"v_mfma_f32_16x16x32_bf16 v[12:15], a[244:247], a[188:191], v[12:15]\n\t"
"ds_read_b64_tr_b16 v[226:227], v246 offset:10240\n\t"
"v_mfma_f32_16x16x32_bf16 v[16:19], a[244:247], a[192:195], v[16:19]\n\t"
"s_waitcnt lgkmcnt(6)\n\t"
"v_mfma_f32_16x16x32_bf16 v[16:19], a[220:223], a[188:191], v[16:19]\n\t"
"v_mfma_f32_16x16x32_bf16 v[16:19], v[240:243], a[184:187], v[16:19]\n\t"
"v_mfma_f32_16x16x32_bf16 v[20:23], v[240:243], a[188:191], v[20:23]\n\t"
"s_waitcnt lgkmcnt(5)\n\t"
"v_mfma_f32_16x16x32_bf16 v[20:23], a[220:223], a[192:195], v[20:23]\n\t"
"v_mfma_f32_16x16x32_bf16 v[20:23], a[244:247], a[196:199], v[20:23]\n\t"
"v_mfma_f32_16x16x32_bf16 v[24:27], a[244:247], a[200:203], v[24:27]\n\t"
"s_waitcnt lgkmcnt(4)\n\t"
"v_mfma_f32_16x16x32_bf16 v[24:27], a[220:223], a[196:199], v[24:27]\n\t"
"v_mfma_f32_16x16x32_bf16 v[24:27], v[240:243], a[192:195], v[24:27]\n\t"
"v_mfma_f32_16x16x32_bf16 v[28:31], v[240:243], a[196:199], v[28:31]\n\t"
"buffer_load_dwordx4 v[240:243], off, s[80:83], s20 offset:1024 sc1\n\t"
"v_mfma_f32_16x16x32_bf16 v[28:31], a[220:223], a[200:203], v[28:31]\n\t"
"s_waitcnt lgkmcnt(0)\n\t"
"v_mfma_f32_16x16x32_bf16 v[28:31], a[244:247], a[204:207], v[28:31]\n\t"
"s_waitcnt vmcnt(11)\n\t"
"s_add_u32 s21, s21, 6291456\n\t"
"v_add_u32 v254, 6291456, v254\n\t"
"v_mfma_f32_16x16x32_bf16 v[228:231], v[228:231], v[220:223], 0\n\t"
"v_mfma_f32_16x16x32_bf16 v[224:227], v[224:227], v[220:223], 0\n\t"
"buffer_load_dwordx4 a[220:223], off, s[80:83], s20 offset:2048 sc1\n\t"
"v_mfma_f32_16x16x32_bf16 v[32:35], v[236:239], a[168:171], v[32:35]\n\t"
"s_mov_b32 s20, 1585152\n\t"
"v_mfma_f32_16x16x32_bf16 v[32:35], v[96:99], a[176:179], v[32:35]\n\t"
"buffer_load_dwordx4 a[244:247], off, s[80:83], s20 offset:0 sc1\n\t"
"v_mfma_f32_16x16x32_bf16 v[32:35], a[248:251], a[172:175], v[32:35]\n\t"
"v_mfma_f32_16x16x32_bf16 v[36:39], v[236:239], a[172:175], v[36:39]\n\t"
"v_cvt_pk_bf16_f32 v228, v228, v229\n\t"
"v_cvt_pk_bf16_f32 v229, v230, v231\n\t"
"v_mfma_f32_16x16x32_bf16 v[36:39], a[248:251], a[176:179], v[36:39]\n\t"
"v_cvt_pk_bf16_f32 v230, v224, v225\n\t"
"v_cvt_pk_bf16_f32 v231, v226, v227\n\t"
"v_mfma_f32_16x16x32_bf16 v[36:39], v[96:99], a[180:183], v[36:39]\n\t"
"v_mfma_f32_16x16x32_bf16 v[40:43], v[96:99], a[184:187], v[40:43]\n\t"
"v_mfma_f32_16x16x32_bf16 v[40:43], a[248:251], a[180:183], v[40:43]\n\t"
"v_mfma_f32_16x16x32_bf16 v[40:43], v[236:239], a[176:179], v[40:43]\n\t"
"ds_read_b64_tr_b16 v[232:233], v246 offset:13312\n\t"
"v_mfma_f32_16x16x32_bf16 v[44:47], v[236:239], a[180:183], v[44:47]\n\t"
"ds_read_b64_tr_b16 v[234:235], v246 offset:16384\n\t"
"v_mfma_f32_16x16x32_bf16 v[44:47], a[248:251], a[184:187], v[44:47]\n\t"
"ds_read_b64_tr_b16 v[224:225], v246 offset:20992\n\t"
"v_mfma_f32_16x16x32_bf16 v[44:47], v[96:99], a[188:191], v[44:47]\n\t"
"ds_read_b64_tr_b16 v[226:227], v246 offset:24064\n\t"
"v_mfma_f32_16x16x32_bf16 v[48:51], v[96:99], a[192:195], v[48:51]\n\t"
"v_mfma_f32_16x16x32_bf16 v[48:51], a[248:251], a[188:191], v[48:51]\n\t"
"v_mfma_f32_16x16x32_bf16 v[48:51], v[236:239], a[184:187], v[48:51]\n\t"
"v_mfma_f32_16x16x32_bf16 v[52:55], v[236:239], a[188:191], v[52:55]\n\t"
"v_mfma_f32_16x16x32_bf16 v[52:55], a[248:251], a[192:195], v[52:55]\n\t"
"v_mfma_f32_16x16x32_bf16 v[52:55], v[96:99], a[196:199], v[52:55]\n\t"
"v_mfma_f32_16x16x32_bf16 v[56:59], v[96:99], a[200:203], v[56:59]\n\t"
"v_mfma_f32_16x16x32_bf16 v[56:59], a[248:251], a[196:199], v[56:59]\n\t"
"v_mfma_f32_16x16x32_bf16 v[56:59], v[236:239], a[192:195], v[56:59]\n\t"
"v_mfma_f32_16x16x32_bf16 v[60:63], v[236:239], a[196:199], v[60:63]\n\t"
"buffer_load_dwordx4 v[236:239], off, s[80:83], s20 offset:1024 sc1\n\t"
"v_mfma_f32_16x16x32_bf16 v[60:63], a[248:251], a[200:203], v[60:63]\n\t"
"s_waitcnt lgkmcnt(0)\n\t"
"v_mfma_f32_16x16x32_bf16 v[60:63], v[96:99], a[204:207], v[60:63]\n\t"
"s_waitcnt vmcnt(11)\n\t"
"v_mfma_f32_16x16x32_bf16 v[232:235], v[232:235], v[220:223], 0\n\t"
"buffer_load_dwordx4 v[96:99], off, s[80:83], s20 offset:2048 sc1\n\t"
"v_mfma_f32_16x16x32_bf16 v[224:227], v[224:227], v[220:223], 0\n\t"
"s_mov_b32 s20, 1597440\n\t"
"v_mfma_f32_16x16x32_bf16 v[64:67], a[228:231], a[168:171], v[64:67]\n\t"
"ds_read_b128 a[168:171], v244 offset:9216\n\t"
"v_mfma_f32_16x16x32_bf16 v[64:67], a[212:215], a[176:179], v[64:67]\n\t"
"buffer_load_dwordx4 a[248:251], off, s[80:83], s20 offset:0 sc1\n\t"
"v_mfma_f32_16x16x32_bf16 v[64:67], a[216:219], a[172:175], v[64:67]\n\t"
"v_mfma_f32_16x16x32_bf16 v[68:71], a[228:231], a[172:175], v[68:71]\n\t"
"ds_read_b128 a[172:175], v244 offset:9728\n\t"
"v_cvt_pk_bf16_f32 v232, v232, v233\n\t"
"v_mfma_f32_16x16x32_bf16 v[68:71], a[216:219], a[176:179], v[68:71]\n\t"
"v_cvt_pk_bf16_f32 v233, v234, v235\n\t"
"v_cvt_pk_bf16_f32 v234, v224, v225\n\t"
"v_mfma_f32_16x16x32_bf16 v[68:71], a[212:215], a[180:183], v[68:71]\n\t"
"v_cvt_pk_bf16_f32 v235, v226, v227\n\t"
"v_mfma_f32_16x16x32_bf16 v[72:75], a[212:215], a[184:187], v[72:75]\n\t"
"v_mfma_f32_16x16x32_bf16 v[72:75], a[216:219], a[180:183], v[72:75]\n\t"
"v_mfma_f32_16x16x32_bf16 v[72:75], a[228:231], a[176:179], v[72:75]\n\t"
"ds_read_b128 a[176:179], v244 offset:10240\n\t"
"v_mfma_f32_16x16x32_bf16 v[76:79], a[228:231], a[180:183], v[76:79]\n\t"
"ds_read_b128 a[180:183], v244 offset:10752\n\t"
"v_mfma_f32_16x16x32_bf16 v[76:79], a[216:219], a[184:187], v[76:79]\n\t"
"ds_read_b64_tr_b16 v[224:225], v246 offset:27136\n\t"
"v_mfma_f32_16x16x32_bf16 v[76:79], a[212:215], a[188:191], v[76:79]\n\t"
"ds_read_b64_tr_b16 v[226:227], v246 offset:30208\n\t"
"v_mfma_f32_16x16x32_bf16 v[80:83], a[212:215], a[192:195], v[80:83]\n\t"
"v_mfma_f32_16x16x32_bf16 v[80:83], a[216:219], a[188:191], v[80:83]\n\t"
"v_mfma_f32_16x16x32_bf16 v[80:83], a[228:231], a[184:187], v[80:83]\n\t"
"ds_read_b128 a[184:187], v244 offset:11264\n\t"
"v_mfma_f32_16x16x32_bf16 v[84:87], a[228:231], a[188:191], v[84:87]\n\t"
"ds_read_b128 a[188:191], v244 offset:11776\n\t"
"v_mfma_f32_16x16x32_bf16 v[84:87], a[216:219], a[192:195], v[84:87]\n\t"
"v_mfma_f32_16x16x32_bf16 v[84:87], a[212:215], a[196:199], v[84:87]\n\t"
"v_mfma_f32_16x16x32_bf16 v[88:91], a[212:215], a[200:203], v[88:91]\n\t"
"v_mfma_f32_16x16x32_bf16 v[88:91], a[216:219], a[196:199], v[88:91]\n\t"
"v_mfma_f32_16x16x32_bf16 v[88:91], a[228:231], a[192:195], v[88:91]\n\t"
"ds_read_b128 a[192:195], v244 offset:12288\n\t"
"v_mfma_f32_16x16x32_bf16 v[92:95], a[228:231], a[196:199], v[92:95]\n\t"
"buffer_load_dwordx4 a[228:231], off, s[80:83], s20 offset:1024 sc1\n\t"
"v_mfma_f32_16x16x32_bf16 v[92:95], a[216:219], a[200:203], v[92:95]\n\t"
"s_waitcnt vmcnt(11) lgkmcnt(3)\n\t"
"ds_read_b128 a[196:199], v244 offset:12800\n\t"
"v_mfma_f32_16x16x32_bf16 v[92:95], a[212:215], a[204:207], v[92:95]\n\t"
"ds_read_b128 a[200:203], v244 offset:13312\n\t"
"v_mfma_f32_16x16x32_bf16 v[224:227], v[224:227], v[220:223], 0\n\t"
"ds_read_b128 a[204:207], v244 offset:13824\n\t"
"v_mfma_f32_16x16x32_bf16 v[100:103], a[252:255], a[168:171], v[100:103]\n\t"
"buffer_load_dwordx4 a[212:215], off, s[80:83], s20 offset:2048 sc1\n\t"
"v_mfma_f32_16x16x32_bf16 v[100:103], a[224:227], a[176:179], v[100:103]\n\t"
"s_mov_b32 s20, 1609728\n\t"
"v_mfma_f32_16x16x32_bf16 v[100:103], a[232:235], a[172:175], v[100:103]\n\t"
"buffer_load_dwordx4 a[216:219], off, s[80:83], s20 offset:0 sc1\n\t"
"v_mfma_f32_16x16x32_bf16 v[104:107], a[252:255], a[172:175], v[104:107]\n\t"
"s_waitcnt lgkmcnt(5)\n\t"
"s_add_u32 m0, s26, 512\n\t"
"v_mfma_f32_16x16x32_bf16 v[104:107], a[232:235], a[176:179], v[104:107]\n\t"
"v_cvt_pk_bf16_f32 v224, v224, v225\n\t"
"v_mfma_f32_16x16x32_bf16 v[104:107], a[224:227], a[180:183], v[104:107]\n\t"
"s_waitcnt lgkmcnt(0)\n\t"
"s_barrier\n\t"
"v_mfma_f32_16x16x32_bf16 v[108:111], a[224:227], a[184:187], v[108:111]\n\t"
"v_cvt_pk_bf16_f32 v225, v226, v227\n\t"
"v_mfma_f32_16x16x32_bf16 v[108:111], a[232:235], a[180:183], v[108:111]\n\t"
"v_fma_f32 v0, v100, s52, v0\n\t"
"v_fma_f32 v1, v101, s52, v1\n\t"
"v_mfma_f32_16x16x32_bf16 v[108:111], a[252:255], a[176:179], v[108:111]\n\t"
"s_mov_b64 exec, s[36:37]\n\t"
"buffer_load_dwordx4 v254, s[12:15], s27 offen offset:0 nt lds\n\t"
"s_mov_b64 exec, -1\n\t"
"s_add_u32 m0, s24, 80384\n\t"
"s_add_u32 s28, s21, 2359296\n\t"
"v_mfma_f32_16x16x32_bf16 v[112:115], a[252:255], a[180:183], v[112:115]\n\t"
"buffer_load_dwordx4 v247, s[12:15], s28 offen offset:0 nt lds\n\t"
"v_mfma_f32_16x16x32_bf16 v[112:115], a[232:235], a[184:187], v[112:115]\n\t"
"s_add_u32 m0, s24, 86528\n\t"
"s_add_u32 s28, s21, 2555904\n\t"
"v_fma_f32 v2, v102, s52, v2\n\t"
"v_mfma_f32_16x16x32_bf16 v[112:115], a[224:227], a[188:191], v[112:115]\n\t"
"ds_write2st64_b64 v245, v[228:229], v[230:231] offset0:0 offset1:1\n\t"
"v_mfma_f32_16x16x32_bf16 v[116:119], a[224:227], a[192:195], v[116:119]\n\t"
"buffer_load_dwordx4 v247, s[12:15], s28 offen offset:0 nt lds\n\t"
"v_mfma_f32_16x16x32_bf16 v[116:119], a[232:235], a[188:191], v[116:119]\n\t"
"s_add_u32 m0, s24, 60416\n\t"
"s_add_u32 s28, s21, 1769472\n\t"
"ds_read_b64_tr_b16 v[228:229], v246 offset:33280\n\t"
"v_mfma_f32_16x16x32_bf16 v[116:119], a[252:255], a[184:187], v[116:119]\n\t"
"ds_write2st64_b64 v245, v[232:233], v[234:235] offset0:2 offset1:3\n\t"
"v_mfma_f32_16x16x32_bf16 v[120:123], a[252:255], a[188:191], v[120:123]\n\t"
"buffer_load_dwordx4 v247, s[12:15], s28 offen offset:0 nt lds\n\t"
"v_mfma_f32_16x16x32_bf16 v[120:123], a[232:235], a[192:195], v[120:123]\n\t"
"s_add_u32 m0, s24, 66560\n\t"
"s_add_u32 s28, s21, 1966080\n\t"
"ds_read_b64_tr_b16 v[230:231], v246 offset:36352\n\t"
"v_mfma_f32_16x16x32_bf16 v[120:123], a[224:227], a[196:199], v[120:123]\n\t"
"ds_write_b64 v245, v[224:225] offset:2048\n\t"
"ds_read_b64_tr_b16 v[232:233], v246 offset:40960\n\t"
"ds_read_b64_tr_b16 v[234:235], v246 offset:44032\n\t"
"v_mfma_f32_16x16x32_bf16 v[124:127], a[232:235], a[196:199], v[124:127]\n\t"
"buffer_load_dwordx4 v247, s[12:15], s28 offen offset:0 nt lds\n\t"
"v_mfma_f32_16x16x32_bf16 v[124:127], a[252:255], a[192:195], v[124:127]\n\t"
"ds_read_b64_tr_b16 v[224:225], v246 offset:47104\n\t"
"v_fma_f32 v3, v103, s52, v3\n\t"
"v_mfma_f32_16x16x32_bf16 v[124:127], a[224:227], a[200:203], v[124:127]\n\t"
"s_waitcnt lgkmcnt(4)\n\t"
"ds_read_b64_tr_b16 v[226:227], v246 offset:50176\n\t"
"v_fma_f32 v100, v0, s54, -v100\n\t"
"v_mfma_f32_16x16x32_bf16 v[228:231], v[228:231], v[220:223], 0\n\t"
"s_waitcnt lgkmcnt(2)\n\t"
"v_fma_f32 v101, v1, s54, -v101\n\t"
"v_fma_f32 v102, v2, s54, -v102\n\t"
"v_mfma_f32_16x16x32_bf16 v[232:235], v[232:235], v[220:223], 0\n\t"
"v_fma_f32 v103, v3, s54, -v103\n\t"
"v_fma_f32 v4, v104, s52, v4\n\t"
"v_mfma_f32_16x16x32_bf16 v[128:131], a[232:235], a[200:203], v[128:131]\n\t"
"v_fma_f32 v5, v105, s52, v5\n\t"
"v_fma_f32 v6, v106, s52, v6\n\t"
"v_mfma_f32_16x16x32_bf16 v[128:131], a[252:255], a[196:199], v[128:131]\n\t"
"s_waitcnt vmcnt(15)\n\t"
"v_fma_f32 v7, v107, s52, v7\n\t"
"v_fma_f32 v104, v4, s54, -v104\n\t"
"v_mfma_f32_16x16x32_bf16 v[128:131], a[224:227], a[204:207], v[128:131]\n\t"
"s_waitcnt lgkmcnt(0)\n\t"
"v_fma_f32 v105, v5, s54, -v105\n\t"
"v_fma_f32 v106, v6, s54, -v106\n\t"
"v_mfma_f32_16x16x32_bf16 v[224:227], v[224:227], v[220:223], 0\n\t"
"v_cvt_pk_bf16_f32 v228, v228, v229\n\t"
"v_cvt_pk_bf16_f32 v229, v230, v231\n\t"
"v_mfma_f32_16x16x32_bf16 v[132:135], a[236:239], a[168:171], v[132:135]\n\t"
"v_cvt_pk_bf16_f32 v230, v232, v233\n\t"
"v_cvt_pk_bf16_f32 v231, v234, v235\n\t"
"v_mfma_f32_16x16x32_bf16 v[132:135], v[248:251], a[176:179], v[132:135]\n\t"
"ds_read_b64_tr_b16 v[232:233], v246 offset:53248\n\t"
"v_fma_f32 v107, v7, s54, -v107\n\t"
"v_mfma_f32_16x16x32_bf16 v[132:135], a[240:243], a[172:175], v[132:135]\n\t"
"Lpg9_%=:\n\t"
"s_cbranch_vccnz Lvr0_%=\n\t"
"ds_write2st64_b64 v245, v[228:229], v[230:231] offset0:5 offset1:6\n\t"
"v_mfma_f32_16x16x32_bf16 v[136:139], a[236:239], a[172:175], v[136:139]\n\t"
"v_fma_f32 v8, v108, s52, v8\n\t"
"v_fma_f32 v9, v109, s52, v9\n\t"
"v_mfma_f32_16x16x32_bf16 v[136:139], a[240:243], a[176:179], v[136:139]\n\t"
"ds_read_b64_tr_b16 v[234:235], v246 offset:56320\n\t"
"v_mfma_f32_16x16x32_bf16 v[136:139], v[248:251], a[180:183], v[136:139]\n\t"
"v_cvt_pk_bf16_f32 v224, v224, v225\n\t"
"v_cvt_pk_bf16_f32 v225, v226, v227\n\t"
"v_mfma_f32_16x16x32_bf16 v[140:143], v[248:251], a[184:187], v[140:143]\n\t"
"v_fma_f32 v10, v110, s52, v10\n\t"
"v_fma_f32 v11, v111, s52, v11\n\t"
"v_mfma_f32_16x16x32_bf16 v[140:143], a[240:243], a[180:183], v[140:143]\n\t"
"v_fma_f32 v108, v8, s54, -v108\n\t"
"v_fma_f32 v109, v9, s54, -v109\n\t"
"v_mfma_f32_16x16x32_bf16 v[140:143], a[236:239], a[176:179], v[140:143]\n\t"
"v_fma_f32 v110, v10, s54, -v110\n\t"
"v_fma_f32 v111, v11, s54, -v111\n\t"
"v_mfma_f32_16x16x32_bf16 v[144:147], a[236:239], a[180:183], v[144:147]\n\t"
"v_fma_f32 v12, v112, s52, v12\n\t"
"v_fma_f32 v13, v113, s52, v13\n\t"
"v_mfma_f32_16x16x32_bf16 v[144:147], a[240:243], a[184:187], v[144:147]\n\t"
"v_fma_f32 v14, v114, s52, v14\n\t"
"v_fma_f32 v15, v115, s52, v15\n\t"
"v_mfma_f32_16x16x32_bf16 v[144:147], v[248:251], a[188:191], v[144:147]\n\t"
"buffer_load_dwordx4 a[224:227], off, s[80:83], s20 offset:1024 sc1\n\t"
"v_mfma_f32_16x16x32_bf16 v[148:151], a[240:243], a[188:191], v[148:151]\n\t"
"s_waitcnt lgkmcnt(0)\n\t"
"v_fma_f32 v112, v12, s54, -v112\n\t"
"v_fma_f32 v113, v13, s54, -v113\n\t"
"v_mfma_f32_16x16x32_bf16 v[148:151], v[248:251], a[192:195], v[148:151]\n\t"
"v_fma_f32 v114, v14, s54, -v114\n\t"
"v_fma_f32 v115, v15, s54, -v115\n\t"
"v_mfma_f32_16x16x32_bf16 v[148:151], a[236:239], a[184:187], v[148:151]\n\t"
"v_fma_f32 v16, v116, s52, v16\n\t"
"v_fma_f32 v17, v117, s52, v17\n\t"
"v_mfma_f32_16x16x32_bf16 v[232:235], v[232:235], v[220:223], 0\n\t"
"v_fma_f32 v18, v118, s52, v18\n\t"
"v_fma_f32 v19, v119, s52, v19\n\t"
"v_mfma_f32_16x16x32_bf16 v[152:155], a[236:239], a[188:191], v[152:155]\n\t"
"v_fma_f32 v116, v16, s54, -v116\n\t"
"v_fma_f32 v117, v17, s54, -v117\n\t"
"v_mfma_f32_16x16x32_bf16 v[152:155], v[248:251], a[196:199], v[152:155]\n\t"
"v_fma_f32 v118, v18, s54, -v118\n\t"
"v_fma_f32 v119, v19, s54, -v119\n\t"
"v_mfma_f32_16x16x32_bf16 v[152:155], a[240:243], a[192:195], v[152:155]\n\t"
"v_fma_f32 v20, v120, s52, v20\n\t"
"v_fma_f32 v21, v121, s52, v21\n\t"
"v_mfma_f32_16x16x32_bf16 v[156:159], a[236:239], a[192:195], v[156:159]\n\t"
"v_fma_f32 v22, v122, s52, v22\n\t"
"v_fma_f32 v23, v123, s52, v23\n\t"
"v_mfma_f32_16x16x32_bf16 v[156:159], a[240:243], a[196:199], v[156:159]\n\t"
"v_cvt_pk_bf16_f32 v226, v232, v233\n\t"
"v_cvt_pk_bf16_f32 v227, v234, v235\n\t"
"v_mfma_f32_16x16x32_bf16 v[156:159], v[248:251], a[200:203], v[156:159]\n\t"
"ds_write2st64_b64 v245, v[224:225], v[226:227] offset0:7 offset1:8\n\t"
"v_mfma_f32_16x16x32_bf16 v[160:163], a[240:243], a[200:203], v[160:163]\n\t"
"s_waitcnt vmcnt(13)\n\t"
"v_fma_f32 v120, v20, s54, -v120\n\t"
"v_fma_f32 v121, v21, s54, -v121\n\t"
"v_mfma_f32_16x16x32_bf16 v[160:163], a[236:239], a[196:199], v[160:163]\n\t"
"v_fma_f32 v122, v22, s54, -v122\n\t"
"v_fma_f32 v123, v23, s54, -v123\n\t"
"v_mfma_f32_16x16x32_bf16 v[160:163], v[248:251], a[204:207], v[160:163]\n\t"
"v_fma_f32 v24, v124, s52, v24\n\t"
"v_fma_f32 v25, v125, s52, v25\n\t"
"v_mfma_f32_16x16x32_bf16 v[164:167], a[208:211], a[168:171], v[164:167]\n\t"
"v_fma_f32 v26, v126, s52, v26\n\t"
"v_fma_f32 v27, v127, s52, v27\n\t"
"v_mfma_f32_16x16x32_bf16 v[164:167], a[220:223], a[176:179], v[164:167]\n\t"
"v_fma_f32 v124, v24, s54, -v124\n\t"
"v_mfma_f32_16x16x32_bf16 v[164:167], v[240:243], a[172:175], v[164:167]\n\t"
"s_waitcnt lgkmcnt(0)\n\t"
"s_barrier\n\t"
"v_mfma_f32_16x16x32_bf16 v[168:171], a[208:211], a[172:175], v[168:171]\n\t"
"ds_read_b128 a[168:171], v244 offset:0\n\t"
"v_mfma_f32_16x16x32_bf16 v[168:171], v[240:243], a[176:179], v[168:171]\n\t"
"ds_read_b128 a[172:175], v244 offset:512\n\t"
"v_fma_f32 v125, v25, s54, -v125\n\t"
"v_mfma_f32_16x16x32_bf16 v[168:171], a[220:223], a[180:183], v[168:171]\n\t"
"v_fma_f32 v126, v26, s54, -v126\n\t"
"v_fma_f32 v127, v27, s54, -v127\n\t"
"v_mfma_f32_16x16x32_bf16 v[172:175], a[220:223], a[184:187], v[172:175]\n\t"
"v_fma_f32 v28, v128, s52, v28\n\t"
"v_fma_f32 v29, v129, s52, v29\n\t"
"v_mfma_f32_16x16x32_bf16 v[172:175], v[240:243], a[180:183], v[172:175]\n\t"
"v_fma_f32 v30, v130, s52, v30\n\t"
"v_fma_f32 v31, v131, s52, v31\n\t"
"v_mfma_f32_16x16x32_bf16 v[172:175], a[208:211], a[176:179], v[172:175]\n\t"
"ds_read_b128 a[176:179], v244 offset:1024\n\t"
"v_fma_f32 v128, v28, s54, -v128\n\t"
"v_mfma_f32_16x16x32_bf16 v[176:179], a[208:211], a[180:183], v[176:179]\n\t"
"ds_read_b128 a[180:183], v244 offset:1536\n\t"
"v_fma_f32 v129, v29, s54, -v129\n\t"
"v_mfma_f32_16x16x32_bf16 v[176:179], v[240:243], a[184:187], v[176:179]\n\t"
"v_fma_f32 v130, v30, s54, -v130\n\t"
"v_fma_f32 v131, v31, s54, -v131\n\t"
"v_mfma_f32_16x16x32_bf16 v[176:179], a[220:223], a[188:191], v[176:179]\n\t"
"v_fma_f32 v32, v132, s52, v32\n\t"
"v_fma_f32 v33, v133, s52, v33\n\t"
"v_mfma_f32_16x16x32_bf16 v[180:183], a[220:223], a[192:195], v[180:183]\n\t"
"v_fma_f32 v34, v134, s52, v34\n\t"
"v_fma_f32 v35, v135, s52, v35\n\t"
"v_mfma_f32_16x16x32_bf16 v[180:183], v[240:243], a[188:191], v[180:183]\n\t"
"v_fma_f32 v132, v32, s54, -v132\n\t"
"v_fma_f32 v133, v33, s54, -v133\n\t"
"v_mfma_f32_16x16x32_bf16 v[180:183], a[208:211], a[184:187], v[180:183]\n\t"
"ds_read_b128 a[184:187], v244 offset:2048\n\t"
"v_fma_f32 v134, v34, s54, -v134\n\t"
"v_mfma_f32_16x16x32_bf16 v[184:187], a[208:211], a[188:191], v[184:187]\n\t"
"ds_read_b128 a[188:191], v244 offset:2560\n\t"
"v_fma_f32 v135, v35, s54, -v135\n\t"
"v_mfma_f32_16x16x32_bf16 v[184:187], v[240:243], a[192:195], v[184:187]\n\t"
"v_fma_f32 v36, v136, s52, v36\n\t"
"v_fma_f32 v37, v137, s52, v37\n\t"
"v_mfma_f32_16x16x32_bf16 v[184:187], a[220:223], a[196:199], v[184:187]\n\t"
"v_fma_f32 v38, v138, s52, v38\n\t"
"v_fma_f32 v39, v139, s52, v39\n\t"
"v_mfma_f32_16x16x32_bf16 v[188:191], a[220:223], a[200:203], v[188:191]\n\t"
"v_fma_f32 v136, v36, s54, -v136\n\t"
"v_fma_f32 v137, v37, s54, -v137\n\t"
"v_mfma_f32_16x16x32_bf16 v[188:191], v[240:243], a[196:199], v[188:191]\n\t"
"v_fma_f32 v138, v38, s54, -v138\n\t"
"v_fma_f32 v139, v39, s54, -v139\n\t"
"v_mfma_f32_16x16x32_bf16 v[188:191], a[208:211], a[192:195], v[188:191]\n\t"
"buffer_load_dwordx4 v[248:251], off, s[80:83], s20 offset:2048 sc1\n\t"
"s_mov_b32 s20, 1622016\n\t"
"v_mfma_f32_16x16x32_bf16 v[192:195], a[208:211], a[196:199], v[192:195]\n\t"
"s_waitcnt vmcnt(11) lgkmcnt(3)\n\t"
"ds_read_b128 a[192:195], v244 offset:3072\n\t"
"v_fma_f32 v40, v140, s52, v40\n\t"
"v_mfma_f32_16x16x32_bf16 v[192:195], v[240:243], a[200:203], v[192:195]\n\t"
"ds_read_b128 a[196:199], v244 offset:3584\n\t"
"v_fma_f32 v41, v141, s52, v41\n\t"
"v_mfma_f32_16x16x32_bf16 v[192:195], a[220:223], a[204:207], v[192:195]\n\t"
"buffer_load_dwordx4 a[232:235], off, s[80:83], s20 offset:0 sc1\n\t"
"Lst13_%=:\n\t"
"v_mfma_f32_16x16x32_bf16 v[196:199], a[244:247], a[168:171], v[196:199]\n\t"
"buffer_load_dwordx4 v[240:243], off, s[80:83], s20 offset:1024 sc1\n\t"
"v_mfma_f32_16x16x32_bf16 v[196:199], v[96:99], a[176:179], v[196:199]\n\t"
"s_waitcnt lgkmcnt(4)\n\t"
"ds_read_b128 a[200:203], v244 offset:4096\n\t"
"v_fma_f32 v42, v142, s52, v42\n\t"
"v_mfma_f32_16x16x32_bf16 v[196:199], v[236:239], a[172:175], v[196:199]\n\t"
"ds_read_b128 a[204:207], v244 offset:4608\n\t"
"v_fma_f32 v43, v143, s52, v43\n\t"
"v_mfma_f32_16x16x32_bf16 v[204:207], a[244:247], a[172:175], v[204:207]\n\t"
"s_waitcnt lgkmcnt(5)\n\t"
"v_fma_f32 v140, v40, s54, -v140\n\t"
"v_fma_f32 v141, v41, s54, -v141\n\t"
"v_mfma_f32_16x16x32_bf16 v[204:207], v[236:239], a[176:179], v[204:207]\n\t"
"v_fma_f32 v142, v42, s54, -v142\n\t"
"v_fma_f32 v143, v43, s54, -v143\n\t"
"v_mfma_f32_16x16x32_bf16 v[204:207], v[96:99], a[180:183], v[204:207]\n\t"
"v_fma_f32 v44, v144, s52, v44\n\t"
"v_fma_f32 v45, v145, s52, v45\n\t"
"v_mfma_f32_16x16x32_bf16 v[212:215], v[96:99], a[184:187], v[212:215]\n\t"
"s_waitcnt lgkmcnt(4)\n\t"
"v_fma_f32 v46, v146, s52, v46\n\t"
"v_fma_f32 v47, v147, s52, v47\n\t"
"v_mfma_f32_16x16x32_bf16 v[212:215], v[236:239], a[180:183], v[212:215]\n\t"
"v_fma_f32 v144, v44, s54, -v144\n\t"
"v_fma_f32 v145, v45, s54, -v145\n\t"
"v_mfma_f32_16x16x32_bf16 v[212:215], a[244:247], a[176:179], v[212:215]\n\t"
"buffer_load_dwordx4 a[208:211], off, s[80:83], s20 offset:2048 sc1\n\t"
"s_mov_b32 s20, 1634304\n\t"
"v_mfma_f32_16x16x32_bf16 a[0:3], a[244:247], a[180:183], a[0:3]\n\t"
"buffer_load_dwordx4 a[220:223], off, s[80:83], s20 offset:0 sc1\n\t"
"v_mfma_f32_16x16x32_bf16 a[0:3], v[236:239], a[184:187], a[0:3]\n\t"
"buffer_load_dwordx4 a[236:239], off, s[80:83], s20 offset:1024 sc1\n\t"
"v_mfma_f32_16x16x32_bf16 a[0:3], v[96:99], a[188:191], a[0:3]\n\t"
"s_waitcnt lgkmcnt(3)\n\t"
"v_fma_f32 v146, v46, s54, -v146\n\t"
"v_fma_f32 v147, v47, s54, -v147\n\t"
"v_mfma_f32_16x16x32_bf16 a[8:11], v[96:99], a[192:195], a[8:11]\n\t"
"s_waitcnt lgkmcnt(2)\n\t"
"v_fma_f32 v48, v148, s52, v48\n\t"
"v_fma_f32 v49, v149, s52, v49\n\t"
"v_mfma_f32_16x16x32_bf16 a[8:11], v[236:239], a[188:191], a[8:11]\n\t"
"s_add_u32 m0, s24, 72704\n\t"
"s_add_u32 s28, s21, 2162688\n\t"
"v_fma_f32 v50, v150, s52, v50\n\t"
"v_mfma_f32_16x16x32_bf16 a[8:11], a[244:247], a[184:187], a[8:11]\n\t"
"v_fma_f32 v51, v151, s52, v51\n\t"
"v_fma_f32 v148, v48, s54, -v148\n\t"
"v_mfma_f32_16x16x32_bf16 a[16:19], a[244:247], a[188:191], a[16:19]\n\t"
"s_waitcnt lgkmcnt(1)\n\t"
"v_fma_f32 v149, v49, s54, -v149\n\t"
"v_fma_f32 v150, v50, s54, -v150\n\t"
"v_mfma_f32_16x16x32_bf16 a[16:19], v[236:239], a[192:195], a[16:19]\n\t"
"v_fma_f32 v151, v51, s54, -v151\n\t"
"v_fma_f32 v52, v152, s52, v52\n\t"
"v_mfma_f32_16x16x32_bf16 a[16:19], v[96:99], a[196:199], a[16:19]\n\t"
"buffer_load_dwordx4 v247, s[12:15], s28 offen offset:0 nt lds\n\t"
"s_add_u32 m0, s24, 92672\n\t"
"s_add_u32 s28, s21, 2752512\n\t"
"v_mfma_f32_16x16x32_bf16 a[24:27], v[96:99], a[200:203], a[24:27]\n\t"
"buffer_load_dwordx4 a[240:243], off, s[80:83], s20 offset:2048 sc1\n\t"
"v_mfma_f32_16x16x32_bf16 a[24:27], v[236:239], a[196:199], a[24:27]\n\t"
"buffer_load_dwordx4 v247, s[12:15], s28 offen offset:0 nt lds\n\t"
"v_mfma_f32_16x16x32_bf16 a[24:27], a[244:247], a[192:195], a[24:27]\n\t"
"s_waitcnt lgkmcnt(0)\n\t"
"s_add_u32 m0, s24, 100352\n\t"
"s_add_u32 s28, s21, 2949120\n\t"
"v_mfma_f32_16x16x32_bf16 a[32:35], a[244:247], a[196:199], a[32:35]\n\t"
"buffer_load_dwordx4 v247, s[12:15], s28 offen offset:0 nt lds\n\t"
"v_mfma_f32_16x16x32_bf16 a[32:35], v[236:239], a[200:203], a[32:35]\n\t"
"s_waitcnt vmcnt(17)\n\t"
"s_add_u32 m0, s24, 106496\n\t"
"s_add_u32 s28, s21, 3145728\n\t"
"v_mfma_f32_16x16x32_bf16 a[32:35], v[96:99], a[204:207], a[32:35]\n\t"
"buffer_load_dwordx4 v247, s[12:15], s28 offen offset:0 nt lds\n\t"
"s_add_u32 m0, s24, 112640\n\t"
"s_add_u32 s28, s21, 3342336\n\t"
"v_mfma_f32_16x16x32_bf16 a[40:43], a[248:251], a[168:171], a[40:43]\n\t"
"buffer_load_dwordx4 v247, s[12:15], s28 offen offset:0 nt lds\n\t"
"v_mfma_f32_16x16x32_bf16 a[40:43], a[212:215], a[176:179], a[40:43]\n\t"
"v_fma_f32 v53, v153, s52, v53\n\t"
"v_fma_f32 v54, v154, s52, v54\n\t"
"v_mfma_f32_16x16x32_bf16 a[40:43], a[228:231], a[172:175], a[40:43]\n\t"
"v_fma_f32 v55, v155, s52, v55\n\t"
"v_fma_f32 v152, v52, s54, -v152\n\t"
"v_mfma_f32_16x16x32_bf16 a[48:51], a[248:251], a[172:175], a[48:51]\n\t"
"v_fma_f32 v153, v53, s54, -v153\n\t"
"v_fma_f32 v154, v54, s54, -v154\n\t"
"v_mfma_f32_16x16x32_bf16 a[48:51], a[228:231], a[176:179], a[48:51]\n\t"
"v_fma_f32 v155, v55, s54, -v155\n\t"
"v_fma_f32 v56, v156, s52, v56\n\t"
"v_mfma_f32_16x16x32_bf16 a[48:51], a[212:215], a[180:183], a[48:51]\n\t"
"v_fma_f32 v57, v157, s52, v57\n\t"
"v_fma_f32 v58, v158, s52, v58\n\t"
"v_mfma_f32_16x16x32_bf16 a[56:59], a[212:215], a[184:187], a[56:59]\n\t"
"v_fma_f32 v59, v159, s52, v59\n\t"
"v_fma_f32 v156, v56, s54, -v156\n\t"
"v_mfma_f32_16x16x32_bf16 a[56:59], a[228:231], a[180:183], a[56:59]\n\t"
"v_fma_f32 v157, v57, s54, -v157\n\t"
"v_fma_f32 v158, v58, s54, -v158\n\t"
"v_mfma_f32_16x16x32_bf16 a[56:59], a[248:251], a[176:179], a[56:59]\n\t"
"v_fma_f32 v159, v59, s54, -v159\n\t"
"v_fma_f32 v60, v160, s52, v60\n\t"
"v_mfma_f32_16x16x32_bf16 a[64:67], a[248:251], a[180:183], a[64:67]\n\t"
"v_fma_f32 v61, v161, s52, v61\n\t"
"v_fma_f32 v62, v162, s52, v62\n\t"
"v_mfma_f32_16x16x32_bf16 a[64:67], a[228:231], a[184:187], a[64:67]\n\t"
"v_fma_f32 v63, v163, s52, v63\n\t"
"v_fma_f32 v160, v60, s54, -v160\n\t"
"v_mfma_f32_16x16x32_bf16 a[64:67], a[212:215], a[188:191], a[64:67]\n\t"
"v_fma_f32 v161, v61, s54, -v161\n\t"
"v_fma_f32 v162, v62, s54, -v162\n\t"
"v_mfma_f32_16x16x32_bf16 a[72:75], a[212:215], a[192:195], a[72:75]\n\t"
"v_fma_f32 v163, v63, s54, -v163\n\t"
"v_fma_f32 v64, v164, s52, v64\n\t"
"v_mfma_f32_16x16x32_bf16 a[72:75], a[228:231], a[188:191], a[72:75]\n\t"
"v_fma_f32 v65, v165, s52, v65\n\t"
"v_fma_f32 v66, v166, s52, v66\n\t"
"v_mfma_f32_16x16x32_bf16 a[72:75], a[248:251], a[184:187], a[72:75]\n\t"
"v_fma_f32 v67, v167, s52, v67\n\t"
"v_fma_f32 v164, v64, s54, -v164\n\t"
"v_mfma_f32_16x16x32_bf16 a[80:83], a[248:251], a[188:191], a[80:83]\n\t"
"v_fma_f32 v165, v65, s54, -v165\n\t"
"v_fma_f32 v166, v66, s54, -v166\n\t"
"v_mfma_f32_16x16x32_bf16 a[80:83], a[228:231], a[192:195], a[80:83]\n\t"
"v_fma_f32 v167, v67, s54, -v167\n\t"
"v_fma_f32 v68, v168, s52, v68\n\t"
"v_mfma_f32_16x16x32_bf16 a[80:83], a[212:215], a[196:199], a[80:83]\n\t"
"v_fma_f32 v69, v169, s52, v69\n\t"
"v_fma_f32 v70, v170, s52, v70\n\t"
"v_mfma_f32_16x16x32_bf16 a[88:91], a[212:215], a[200:203], a[88:91]\n\t"
"v_fma_f32 v71, v171, s52, v71\n\t"
"v_fma_f32 v168, v68, s54, -v168\n\t"
"v_mfma_f32_16x16x32_bf16 a[88:91], a[228:231], a[196:199], a[88:91]\n\t"
"v_fma_f32 v169, v69, s54, -v169\n\t"
"v_fma_f32 v170, v70, s54, -v170\n\t"
"v_mfma_f32_16x16x32_bf16 a[88:91], a[248:251], a[192:195], a[88:91]\n\t"
"v_fma_f32 v171, v71, s54, -v171\n\t"
"v_fma_f32 v72, v172, s52, v72\n\t"
"v_mfma_f32_16x16x32_bf16 a[96:99], a[248:251], a[196:199], a[96:99]\n\t"
"s_waitcnt vmcnt(11)\n\t"
"v_fma_f32 v73, v173, s52, v73\n\t"
"v_fma_f32 v74, v174, s52, v74\n\t"
"v_mfma_f32_16x16x32_bf16 a[96:99], a[228:231], a[200:203], a[96:99]\n\t"
"v_fma_f32 v75, v175, s52, v75\n\t"
"v_fma_f32 v172, v72, s54, -v172\n\t"
"v_mfma_f32_16x16x32_bf16 a[96:99], a[212:215], a[204:207], a[96:99]\n\t"
"v_fma_f32 v173, v73, s54, -v173\n\t"
"v_fma_f32 v174, v74, s54, -v174\n\t"
"v_mfma_f32_16x16x32_bf16 a[104:107], a[216:219], a[168:171], a[104:107]\n\t"
"ds_read_b128 a[168:171], v244 offset:9216\n\t"
"v_fma_f32 v175, v75, s54, -v175\n\t"
"v_mfma_f32_16x16x32_bf16 a[104:107], v[248:251], a[176:179], a[104:107]\n\t"
"v_fma_f32 v76, v176, s52, v76\n\t"
"v_fma_f32 v77, v177, s52, v77\n\t"
"v_mfma_f32_16x16x32_bf16 a[104:107], a[224:227], a[172:175], a[104:107]\n\t"
"v_fma_f32 v78, v178, s52, v78\n\t"
"v_fma_f32 v79, v179, s52, v79\n\t"
"v_mfma_f32_16x16x32_bf16 a[112:115], a[216:219], a[172:175], a[112:115]\n\t"
"ds_read_b128 a[172:175], v244 offset:9728\n\t"
"v_and_b32 v255, 4096, v244\n\t"
"v_mfma_f32_16x16x32_bf16 a[112:115], a[224:227], a[176:179], a[112:115]\n\t"
"v_mad_u32_u24 v255, v255, 5, v244\n\t"
"v_add_u32 v255, 43008, v255\n\t"
"v_mfma_f32_16x16x32_bf16 a[112:115], v[248:251], a[180:183], a[112:115]\n\t"
"v_fma_f32 v176, v76, s54, -v176\n\t"
"v_fma_f32 v177, v77, s54, -v177\n\t"
"v_mfma_f32_16x16x32_bf16 a[120:123], v[248:251], a[184:187], a[120:123]\n\t"
"v_fma_f32 v178, v78, s54, -v178\n\t"
"v_fma_f32 v179, v79, s54, -v179\n\t"
"v_mfma_f32_16x16x32_bf16 a[120:123], a[224:227], a[180:183], a[120:123]\n\t"
"v_fma_f32 v80, v180, s52, v80\n\t"
"v_fma_f32 v81, v181, s52, v81\n\t"
"v_mfma_f32_16x16x32_bf16 a[120:123], a[216:219], a[176:179], a[120:123]\n\t"
"ds_read_b128 a[176:179], v244 offset:10240\n\t"
"v_fma_f32 v82, v182, s52, v82\n\t"
"v_mfma_f32_16x16x32_bf16 a[128:131], a[216:219], a[180:183], a[128:131]\n\t"
"ds_read_b128 a[180:183], v244 offset:10752\n\t"
"v_fma_f32 v83, v183, s52, v83\n\t"
"v_mfma_f32_16x16x32_bf16 a[128:131], a[224:227], a[184:187], a[128:131]\n\t"
"v_fma_f32 v180, v80, s54, -v180\n\t"
"v_fma_f32 v181, v81, s54, -v181\n\t"
"v_mfma_f32_16x16x32_bf16 a[128:131], v[248:251], a[188:191], a[128:131]\n\t"
"v_fma_f32 v182, v82, s54, -v182\n\t"
"v_fma_f32 v183, v83, s54, -v183\n\t"
"v_mfma_f32_16x16x32_bf16 a[136:139], v[248:251], a[192:195], a[136:139]\n\t"
"v_fma_f32 v84, v184, s52, v84\n\t"
"v_fma_f32 v85, v185, s52, v85\n\t"
"v_mfma_f32_16x16x32_bf16 a[136:139], a[224:227], a[188:191], a[136:139]\n\t"
"v_fma_f32 v86, v186, s52, v86\n\t"
"v_fma_f32 v87, v187, s52, v87\n\t"
"v_mfma_f32_16x16x32_bf16 a[136:139], a[216:219], a[184:187], a[136:139]\n\t"
"ds_read_b128 a[184:187], v244 offset:11264\n\t"
"v_fma_f32 v184, v84, s54, -v184\n\t"
"v_mfma_f32_16x16x32_bf16 a[144:147], a[216:219], a[188:191], a[144:147]\n\t"
"ds_read_b128 a[188:191], v244 offset:11776\n\t"
"v_fma_f32 v185, v85, s54, -v185\n\t"
"v_mfma_f32_16x16x32_bf16 a[144:147], a[224:227], a[192:195], a[144:147]\n\t"
"v_fma_f32 v186, v86, s54, -v186\n\t"
"v_fma_f32 v187, v87, s54, -v187\n\t"
"v_mfma_f32_16x16x32_bf16 a[144:147], v[248:251], a[196:199], a[144:147]\n\t"
"v_fma_f32 v88, v188, s52, v88\n\t"
"v_fma_f32 v89, v189, s52, v89\n\t"
"v_mfma_f32_16x16x32_bf16 a[152:155], v[248:251], a[200:203], a[152:155]\n\t"
"s_mov_b32 s20, 1646592\n\t"
"v_fma_f32 v90, v190, s52, v90\n\t"
"v_fma_f32 v91, v191, s52, v91\n\t"
"v_mfma_f32_16x16x32_bf16 a[152:155], a[224:227], a[196:199], a[152:155]\n\t"
"v_fma_f32 v188, v88, s54, -v188\n\t"
"v_fma_f32 v189, v89, s54, -v189\n\t"
"v_mfma_f32_16x16x32_bf16 a[152:155], a[216:219], a[192:195], a[152:155]\n\t"
"ds_read_b128 a[192:195], v244 offset:12288\n\t"
"v_fma_f32 v190, v90, s54, -v190\n\t"
"v_mfma_f32_16x16x32_bf16 a[160:163], a[216:219], a[196:199], a[160:163]\n\t"
"s_waitcnt vmcnt(8) lgkmcnt(4)\n\t"
"ds_read_b128 a[196:199], v244 offset:12800\n\t"
"v_fma_f32 v191, v91, s54, -v191\n\t"
"v_mfma_f32_16x16x32_bf16 a[160:163], a[224:227], a[200:203], a[160:163]\n\t"
"ds_read_b128 a[200:203], v244 offset:13312\n\t"
"v_fma_f32 v92, v192, s52, v92\n\t"
"v_mfma_f32_16x16x32_bf16 a[160:163], v[248:251], a[204:207], a[160:163]\n\t"
"ds_read_b128 a[204:207], v244 offset:13824\n\t"
"v_fma_f32 v93, v193, s52, v93\n\t"
"v_mfma_f32_16x16x32_bf16 v[200:203], a[232:235], a[168:171], v[200:203]\n\t"
"s_waitcnt lgkmcnt(6)\n\t"
"v_fma_f32 v94, v194, s52, v94\n\t"
"v_fma_f32 v95, v195, s52, v95\n\t"
"v_mfma_f32_16x16x32_bf16 v[200:203], a[208:211], a[176:179], v[200:203]\n\t"
"v_fma_f32 v192, v92, s54, -v192\n\t"
"v_fma_f32 v193, v93, s54, -v193\n\t"
"v_mfma_f32_16x16x32_bf16 v[200:203], v[240:243], a[172:175], v[200:203]\n\t"
"v_fma_f32 v194, v94, s54, -v194\n\t"
"v_fma_f32 v195, v95, s54, -v195\n\t"
"v_mfma_f32_16x16x32_bf16 v[208:211], a[232:235], a[172:175], v[208:211]\n\t"
"s_waitcnt lgkmcnt(5)\n\t"
"v_mfma_f32_16x16x32_bf16 v[208:211], v[240:243], a[176:179], v[208:211]\n\t"
"v_mfma_f32_16x16x32_bf16 v[208:211], a[208:211], a[180:183], v[208:211]\n\t"
"s_waitcnt lgkmcnt(0)\n\t"
"s_barrier\n\t"
"v_mfma_f32_16x16x32_bf16 v[216:219], a[208:211], a[184:187], v[216:219]\n\t"
"v_mfma_f32_16x16x32_bf16 v[216:219], v[240:243], a[180:183], v[216:219]\n\t"
"buffer_load_dwordx4 a[212:215], off, s[80:83], s20 offset:0 sc1\n\t"
"v_mfma_f32_16x16x32_bf16 v[216:219], a[232:235], a[176:179], v[216:219]\n\t"
"buffer_load_dwordx4 a[216:219], off, s[80:83], s20 offset:1024 sc1\n\t"
"v_pk_fma_f32 v[196:197], v[200:201], s[56:57], v[196:197]\n\t"
"v_pk_fma_f32 v[200:201], v[196:197], s[58:59], v[200:201] neg_lo:[0,0,1] neg_hi:[0,0,1]\n\t"
"v_pk_fma_f32 v[228:229], v[0:1], s[60:61], v[196:197]\n\t"
"v_pk_fma_f32 v[0:1], v[196:197], s[60:61], v[0:1]\n\t"
"v_pk_fma_f32 v[196:197], v[100:101], s[62:63], v[200:201]\n\t"
"v_pk_fma_f32 v[100:101], v[200:201], s[62:63], v[100:101]\n\t"
"v_pk_fma_f32 v[198:199], v[202:203], s[56:57], v[198:199]\n\t"
"v_pk_fma_f32 v[202:203], v[198:199], s[58:59], v[202:203] neg_lo:[0,0,1] neg_hi:[0,0,1]\n\t"
"v_pk_fma_f32 v[234:235], v[2:3], s[60:61], v[198:199]\n\t"
"v_pk_fma_f32 v[2:3], v[198:199], s[60:61], v[2:3]\n\t"
"v_pk_fma_f32 v[198:199], v[102:103], s[62:63], v[202:203]\n\t"
"v_pk_fma_f32 v[102:103], v[202:203], s[62:63], v[102:103]\n\t"
"v_mfma_f32_16x16x32_bf16 a[4:7], a[232:235], a[180:183], a[4:7]\n\t"
"v_cvt_pk_bf16_f32 v196, v196, v197\n\t"
"v_cvt_pk_bf16_f32 v200, v228, v229\n\t"
"v_mfma_f32_16x16x32_bf16 a[4:7], v[240:243], a[184:187], a[4:7]\n\t"
"v_cvt_pk_bf16_f32 v197, v198, v199\n\t"
"v_cvt_pk_bf16_f32 v201, v234, v235\n\t"
"v_mfma_f32_16x16x32_bf16 a[4:7], a[208:211], a[188:191], a[4:7]\n\t"
"buffer_load_dwordx4 a[224:227], off, s[80:83], s20 offset:2048 sc1\n\t"
"s_mov_b32 s20, 1474560\n\t"
"v_pk_fma_f32 v[204:205], v[208:209], s[56:57], v[204:205]\n\t"
"v_pk_fma_f32 v[208:209], v[204:205], s[58:59], v[208:209] neg_lo:[0,0,1] neg_hi:[0,0,1]\n\t"
"v_pk_fma_f32 v[98:99], v[4:5], s[60:61], v[204:205]\n\t"
"v_pk_fma_f32 v[4:5], v[204:205], s[60:61], v[4:5]\n\t"
"v_pk_fma_f32 v[204:205], v[104:105], s[62:63], v[208:209]\n\t"
"v_pk_fma_f32 v[104:105], v[208:209], s[62:63], v[104:105]\n\t"
"v_mfma_f32_16x16x32_bf16 a[12:15], a[208:211], a[192:195], a[12:15]\n\t"
"v_cvt_pk_bf16_f32 v204, v204, v205\n\t"
"v_cvt_pk_bf16_f32 v208, v98, v99\n\t"
"v_mfma_f32_16x16x32_bf16 a[12:15], v[240:243], a[188:191], a[12:15]\n\t"
"v_mfma_f32_16x16x32_bf16 a[12:15], a[232:235], a[184:187], a[12:15]\n\t"
"buffer_load_dwordx4 a[228:231], off, s[80:83], s20 offset:0 sc1\n\t"
"v_pk_fma_f32 v[206:207], v[210:211], s[56:57], v[206:207]\n\t"
"v_pk_fma_f32 v[210:211], v[206:207], s[58:59], v[210:211] neg_lo:[0,0,1] neg_hi:[0,0,1]\n\t"
"v_pk_fma_f32 v[228:229], v[6:7], s[60:61], v[206:207]\n\t"
"v_pk_fma_f32 v[6:7], v[206:207], s[60:61], v[6:7]\n\t"
"v_pk_fma_f32 v[206:207], v[106:107], s[62:63], v[210:211]\n\t"
"v_pk_fma_f32 v[106:107], v[210:211], s[62:63], v[106:107]\n\t"
"v_mfma_f32_16x16x32_bf16 a[20:23], a[232:235], a[188:191], a[20:23]\n\t"
"v_cvt_pk_bf16_f32 v205, v206, v207\n\t"
"v_cvt_pk_bf16_f32 v209, v228, v229\n\t"
"v_mfma_f32_16x16x32_bf16 a[20:23], v[240:243], a[192:195], a[20:23]\n\t"
"v_accvgpr_read_b32 v224, a0\n\t"
"v_accvgpr_read_b32 v226, a4\n\t"
"v_mfma_f32_16x16x32_bf16 a[20:23], a[208:211], a[196:199], a[20:23]\n\t"
"buffer_load_dwordx4 a[244:247], off, s[80:83], s20 offset:1024 sc1\n\t"
"v_accvgpr_read_b32 v225, a1\n\t"
"v_accvgpr_read_b32 v227, a5\n\t"
"v_pk_fma_f32 v[212:213], v[216:217], s[56:57], v[212:213]\n\t"
"v_pk_fma_f32 v[216:217], v[212:213], s[58:59], v[216:217] neg_lo:[0,0,1] neg_hi:[0,0,1]\n\t"
"v_pk_fma_f32 v[234:235], v[8:9], s[60:61], v[212:213]\n\t"
"v_pk_fma_f32 v[8:9], v[212:213], s[60:61], v[8:9]\n\t"
"v_pk_fma_f32 v[212:213], v[108:109], s[62:63], v[216:217]\n\t"
"v_pk_fma_f32 v[108:109], v[216:217], s[62:63], v[108:109]\n\t"
"v_mfma_f32_16x16x32_bf16 a[28:31], a[208:211], a[200:203], a[28:31]\n\t"
"v_cvt_pk_bf16_f32 v212, v212, v213\n\t"
"v_cvt_pk_bf16_f32 v216, v234, v235\n\t"
"v_mfma_f32_16x16x32_bf16 a[28:31], v[240:243], a[196:199], a[28:31]\n\t"
"v_accvgpr_read_b32 v230, a2\n\t"
"v_accvgpr_read_b32 v232, a6\n\t"
"v_mfma_f32_16x16x32_bf16 a[28:31], a[232:235], a[192:195], a[28:31]\n\t"
"buffer_load_dwordx4 a[248:251], off, s[80:83], s20 offset:2048 sc1\n\t"
"v_accvgpr_read_b32 v231, a3\n\t"
"v_accvgpr_read_b32 v233, a7\n\t"
"v_pk_fma_f32 v[214:215], v[218:219], s[56:57], v[214:215]\n\t"
"v_pk_fma_f32 v[218:219], v[214:215], s[58:59], v[218:219] neg_lo:[0,0,1] neg_hi:[0,0,1]\n\t"
"v_pk_fma_f32 v[98:99], v[10:11], s[60:61], v[214:215]\n\t"
"v_pk_fma_f32 v[10:11], v[214:215], s[60:61], v[10:11]\n\t"
"v_pk_fma_f32 v[214:215], v[110:111], s[62:63], v[218:219]\n\t"
"v_pk_fma_f32 v[110:111], v[218:219], s[62:63], v[110:111]\n\t"
"v_mfma_f32_16x16x32_bf16 a[36:39], a[232:235], a[196:199], a[36:39]\n\t"
"s_waitcnt vmcnt(10)\n\t"
"s_mov_b32 s20, 1486848\n\t"
"v_cvt_pk_bf16_f32 v213, v214, v215\n\t"
"v_mfma_f32_16x16x32_bf16 a[36:39], v[240:243], a[200:203], a[36:39]\n\t"
"v_cvt_pk_bf16_f32 v217, v98, v99\n\t"
"v_accvgpr_read_b32 v236, a8\n\t"
"v_mfma_f32_16x16x32_bf16 a[36:39], a[208:211], a[204:207], a[36:39]\n\t"
"buffer_load_dwordx4 v[240:243], off, s[80:83], s20 offset:0 sc1\n\t"
"v_accvgpr_read_b32 v238, a12\n\t"
"v_accvgpr_read_b32 v237, a9\n\t"
"v_pk_fma_f32 v[224:225], v[226:227], s[56:57], v[224:225]\n\t"
"v_pk_fma_f32 v[226:227], v[224:225], s[58:59], v[226:227] neg_lo:[0,0,1] neg_hi:[0,0,1]\n\t"
"v_pk_fma_f32 v[228:229], v[12:13], s[60:61], v[224:225]\n\t"
"v_pk_fma_f32 v[12:13], v[224:225], s[60:61], v[12:13]\n\t"
"v_pk_fma_f32 v[224:225], v[112:113], s[62:63], v[226:227]\n\t"
"v_pk_fma_f32 v[112:113], v[226:227], s[62:63], v[112:113]\n\t"
"v_mfma_f32_16x16x32_bf16 a[44:47], a[220:223], a[168:171], a[44:47]\n\t"
"v_cvt_pk_bf16_f32 v224, v224, v225\n\t"
"v_cvt_pk_bf16_f32 v228, v228, v229\n\t"
"v_mfma_f32_16x16x32_bf16 a[44:47], a[240:243], a[176:179], a[44:47]\n\t"
"v_accvgpr_write_b32 a0, v224\n\t"
"v_accvgpr_write_b32 a4, v228\n\t"
"v_mfma_f32_16x16x32_bf16 a[44:47], a[236:239], a[172:175], a[44:47]\n\t"
"buffer_load_dwordx4 a[208:211], off, s[80:83], s20 offset:1024 sc1\n\t"
"v_accvgpr_read_b32 v239, a13\n\t"
"v_accvgpr_read_b32 v224, a10\n\t"
"v_pk_fma_f32 v[230:231], v[232:233], s[56:57], v[230:231]\n\t"
"v_pk_fma_f32 v[232:233], v[230:231], s[58:59], v[232:233] neg_lo:[0,0,1] neg_hi:[0,0,1]\n\t"
"v_pk_fma_f32 v[234:235], v[14:15], s[60:61], v[230:231]\n\t"
"v_pk_fma_f32 v[14:15], v[230:231], s[60:61], v[14:15]\n\t"
"v_pk_fma_f32 v[230:231], v[114:115], s[62:63], v[232:233]\n\t"
"v_pk_fma_f32 v[114:115], v[232:233], s[62:63], v[114:115]\n\t"
"v_mfma_f32_16x16x32_bf16 a[52:55], a[220:223], a[172:175], a[52:55]\n\t"
"v_cvt_pk_bf16_f32 v230, v230, v231\n\t"
"v_cvt_pk_bf16_f32 v234, v234, v235\n\t"
"v_mfma_f32_16x16x32_bf16 a[52:55], a[236:239], a[176:179], a[52:55]\n\t"
"v_accvgpr_write_b32 a1, v230\n\t"
"v_accvgpr_write_b32 a5, v234\n\t"
"v_mfma_f32_16x16x32_bf16 a[52:55], a[240:243], a[180:183], a[52:55]\n\t"
"buffer_load_dwordx4 a[232:235], off, s[80:83], s20 offset:2048 sc1\n\t"
"v_accvgpr_read_b32 v226, a14\n\t"
"v_accvgpr_read_b32 v225, a11\n\t"
"v_pk_fma_f32 v[236:237], v[238:239], s[56:57], v[236:237]\n\t"
"v_pk_fma_f32 v[238:239], v[236:237], s[58:59], v[238:239] neg_lo:[0,0,1] neg_hi:[0,0,1]\n\t"
"v_pk_fma_f32 v[98:99], v[16:17], s[60:61], v[236:237]\n\t"
"v_pk_fma_f32 v[16:17], v[236:237], s[60:61], v[16:17]\n\t"
"v_pk_fma_f32 v[236:237], v[116:117], s[62:63], v[238:239]\n\t"
"v_pk_fma_f32 v[116:117], v[238:239], s[62:63], v[116:117]\n\t"
"v_mfma_f32_16x16x32_bf16 a[60:63], a[240:243], a[184:187], a[60:63]\n\t"
"s_mov_b32 s20, 1499136\n\t"
"v_accvgpr_read_b32 v227, a15\n\t"
"v_accvgpr_read_b32 v230, a16\n\t"
"v_mfma_f32_16x16x32_bf16 a[60:63], a[236:239], a[180:183], a[60:63]\n\t"
"v_accvgpr_read_b32 v232, a20\n\t"
"v_accvgpr_read_b32 v231, a17\n\t"
"v_mfma_f32_16x16x32_bf16 a[60:63], a[220:223], a[176:179], a[60:63]\n\t"
"buffer_load_dwordx4 a[252:255], off, s[80:83], s20 offset:0 sc1\n\t"
"v_accvgpr_read_b32 v233, a21\n\t"
"v_cvt_pk_bf16_f32 v236, v236, v237\n\t"
"v_cvt_pk_bf16_f32 v98, v98, v99\n\t"
"v_pk_fma_f32 v[224:225], v[226:227], s[56:57], v[224:225]\n\t"
"v_pk_fma_f32 v[226:227], v[224:225], s[58:59], v[226:227] neg_lo:[0,0,1] neg_hi:[0,0,1]\n\t"
"v_pk_fma_f32 v[228:229], v[18:19], s[60:61], v[224:225]\n\t"
"v_pk_fma_f32 v[18:19], v[224:225], s[60:61], v[18:19]\n\t"
"v_pk_fma_f32 v[224:225], v[118:119], s[62:63], v[226:227]\n\t"
"v_pk_fma_f32 v[118:119], v[226:227], s[62:63], v[118:119]\n\t"
"v_pk_fma_f32 v[230:231], v[232:233], s[56:57], v[230:231]\n\t"
"v_pk_fma_f32 v[232:233], v[230:231], s[58:59], v[232:233] neg_lo:[0,0,1] neg_hi:[0,0,1]\n\t"
"v_pk_fma_f32 v[234:235], v[20:21], s[60:61], v[230:231]\n\t"
"v_pk_fma_f32 v[20:21], v[230:231], s[60:61], v[20:21]\n\t"
"v_pk_fma_f32 v[230:231], v[120:121], s[62:63], v[232:233]\n\t"
"v_pk_fma_f32 v[120:121], v[232:233], s[62:63], v[120:121]\n\t"
"v_mfma_f32_16x16x32_bf16 a[68:71], a[220:223], a[180:183], a[68:71]\n\t"
"Lpg10_%=:\n\t"
"s_cbranch_vccnz Lvr0_%=\n\t"
"v_accvgpr_write_b32 a8, v236\n\t"
"v_accvgpr_write_b32 a12, v98\n\t"
"v_mfma_f32_16x16x32_bf16 a[68:71], a[236:239], a[184:187], a[68:71]\n\t"
"v_accvgpr_read_b32 v236, a18\n\t"
"v_accvgpr_read_b32 v238, a22\n\t"
"v_mfma_f32_16x16x32_bf16 a[68:71], a[240:243], a[188:191], a[68:71]\n\t"
"v_accvgpr_read_b32 v237, a19\n\t"
"v_accvgpr_read_b32 v239, a23\n\t"
"v_cvt_pk_bf16_f32 v224, v224, v225\n\t"
"v_cvt_pk_bf16_f32 v228, v228, v229\n\t"
"v_cvt_pk_bf16_f32 v230, v230, v231\n\t"
"v_cvt_pk_bf16_f32 v234, v234, v235\n\t"
"v_pk_fma_f32 v[236:237], v[238:239], s[56:57], v[236:237]\n\t"
"v_pk_fma_f32 v[238:239], v[236:237], s[58:59], v[238:239] neg_lo:[0,0,1] neg_hi:[0,0,1]\n\t"
"v_pk_fma_f32 v[98:99], v[22:23], s[60:61], v[236:237]\n\t"
"v_pk_fma_f32 v[22:23], v[236:237], s[60:61], v[22:23]\n\t"
"v_pk_fma_f32 v[236:237], v[122:123], s[62:63], v[238:239]\n\t"
"v_pk_fma_f32 v[122:123], v[238:239], s[62:63], v[122:123]\n\t"
"v_mfma_f32_16x16x32_bf16 a[76:79], a[240:243], a[192:195], a[76:79]\n\t"
"v_accvgpr_write_b32 a9, v224\n\t"
"v_accvgpr_write_b32 a13, v228\n\t"
"v_mfma_f32_16x16x32_bf16 a[76:79], a[236:239], a[188:191], a[76:79]\n\t"
"v_accvgpr_write_b32 a16, v230\n\t"
"v_accvgpr_write_b32 a20, v234\n\t"
"v_mfma_f32_16x16x32_bf16 a[76:79], a[220:223], a[184:187], a[76:79]\n\t"
"v_accvgpr_read_b32 v224, a24\n\t"
"v_accvgpr_read_b32 v226, a28\n\t"
"v_accvgpr_read_b32 v225, a25\n\t"
"v_accvgpr_read_b32 v227, a29\n\t"
"v_cvt_pk_bf16_f32 v236, v236, v237\n\t"
"v_cvt_pk_bf16_f32 v98, v98, v99\n\t"
"v_pk_fma_f32 v[224:225], v[226:227], s[56:57], v[224:225]\n\t"
"v_pk_fma_f32 v[226:227], v[224:225], s[58:59], v[226:227] neg_lo:[0,0,1] neg_hi:[0,0,1]\n\t"
"v_pk_fma_f32 v[228:229], v[24:25], s[60:61], v[224:225]\n\t"
"v_pk_fma_f32 v[24:25], v[224:225], s[60:61], v[24:25]\n\t"
"v_pk_fma_f32 v[224:225], v[124:125], s[62:63], v[226:227]\n\t"
"v_pk_fma_f32 v[124:125], v[226:227], s[62:63], v[124:125]\n\t"
"v_mfma_f32_16x16x32_bf16 a[84:87], a[220:223], a[188:191], a[84:87]\n\t"
"v_accvgpr_write_b32 a17, v236\n\t"
"v_accvgpr_write_b32 a21, v98\n\t"
"v_mfma_f32_16x16x32_bf16 a[84:87], a[236:239], a[192:195], a[84:87]\n\t"
"v_accvgpr_read_b32 v230, a26\n\t"
"v_accvgpr_read_b32 v232, a30\n\t"
"v_mfma_f32_16x16x32_bf16 a[84:87], a[240:243], a[196:199], a[84:87]\n\t"
"v_accvgpr_read_b32 v231, a27\n\t"
"v_accvgpr_read_b32 v233, a31\n\t"
"v_cvt_pk_bf16_f32 v224, v224, v225\n\t"
"v_cvt_pk_bf16_f32 v228, v228, v229\n\t"
"v_accvgpr_write_b32 a24, v224\n\t"
"v_pk_fma_f32 v[230:231], v[232:233], s[56:57], v[230:231]\n\t"
"v_pk_fma_f32 v[232:233], v[230:231], s[58:59], v[232:233] neg_lo:[0,0,1] neg_hi:[0,0,1]\n\t"
"v_pk_fma_f32 v[234:235], v[26:27], s[60:61], v[230:231]\n\t"
"v_pk_fma_f32 v[26:27], v[230:231], s[60:61], v[26:27]\n\t"
"v_pk_fma_f32 v[230:231], v[126:127], s[62:63], v[232:233]\n\t"
"v_pk_fma_f32 v[126:127], v[232:233], s[62:63], v[126:127]\n\t"
"v_mfma_f32_16x16x32_bf16 a[92:95], a[240:243], a[200:203], a[92:95]\n\t"
"v_accvgpr_write_b32 a28, v228\n\t"
"v_accvgpr_read_b32 v236, a32\n\t"
"v_mfma_f32_16x16x32_bf16 a[92:95], a[236:239], a[196:199], a[92:95]\n\t"
"v_accvgpr_read_b32 v238, a36\n\t"
"v_accvgpr_read_b32 v237, a33\n\t"
"v_mfma_f32_16x16x32_bf16 a[92:95], a[220:223], a[192:195], a[92:95]\n\t"
"v_accvgpr_read_b32 v239, a37\n\t"
"v_cvt_pk_bf16_f32 v230, v230, v231\n\t"
"v_cvt_pk_bf16_f32 v234, v234, v235\n\t"
"v_accvgpr_write_b32 a25, v230\n\t"
"v_accvgpr_write_b32 a29, v234\n\t"
"v_pk_fma_f32 v[236:237], v[238:239], s[56:57], v[236:237]\n\t"
"v_pk_fma_f32 v[238:239], v[236:237], s[58:59], v[238:239] neg_lo:[0,0,1] neg_hi:[0,0,1]\n\t"
"v_pk_fma_f32 v[98:99], v[28:29], s[60:61], v[236:237]\n\t"
"v_pk_fma_f32 v[28:29], v[236:237], s[60:61], v[28:29]\n\t"
"v_pk_fma_f32 v[236:237], v[128:129], s[62:63], v[238:239]\n\t"
"v_pk_fma_f32 v[128:129], v[238:239], s[62:63], v[128:129]\n\t"
"v_mfma_f32_16x16x32_bf16 a[100:103], a[220:223], a[196:199], a[100:103]\n\t"
"s_waitcnt vmcnt(7)\n\t"
"v_accvgpr_read_b32 v224, a34\n\t"
"v_accvgpr_read_b32 v226, a38\n\t"
"v_mfma_f32_16x16x32_bf16 a[100:103], a[236:239], a[200:203], a[100:103]\n\t"
"v_accvgpr_read_b32 v225, a35\n\t"
"v_accvgpr_read_b32 v227, a39\n\t"
"v_mfma_f32_16x16x32_bf16 a[100:103], a[240:243], a[204:207], a[100:103]\n\t"
"buffer_load_dwordx4 a[220:223], off, s[80:83], s20 offset:1024 sc1\n\t"
"v_cvt_pk_bf16_f32 v236, v236, v237\n\t"
"v_cvt_pk_bf16_f32 v98, v98, v99\n\t"
"v_pk_fma_f32 v[224:225], v[226:227], s[56:57], v[224:225]\n\t"
"v_pk_fma_f32 v[226:227], v[224:225], s[58:59], v[226:227] neg_lo:[0,0,1] neg_hi:[0,0,1]\n\t"
"v_pk_fma_f32 v[228:229], v[30:31], s[60:61], v[224:225]\n\t"
"v_pk_fma_f32 v[30:31], v[224:225], s[60:61], v[30:31]\n\t"
"v_pk_fma_f32 v[224:225], v[130:131], s[62:63], v[226:227]\n\t"
"v_pk_fma_f32 v[130:131], v[226:227], s[62:63], v[130:131]\n\t"
"v_mfma_f32_16x16x32_bf16 a[108:111], a[212:215], a[168:171], a[108:111]\n\t"
"ds_read_b128 a[168:171], v255 offset:512\n\t"
"v_accvgpr_write_b32 a32, v236\n\t"
"v_mfma_f32_16x16x32_bf16 a[108:111], a[224:227], a[176:179], a[108:111]\n\t"
"v_accvgpr_write_b32 a36, v98\n\t"
"v_accvgpr_read_b32 v230, a40\n\t"
"v_mfma_f32_16x16x32_bf16 a[108:111], a[216:219], a[172:175], a[108:111]\n\t"
"buffer_load_dwordx4 a[236:239], off, s[80:83], s20 offset:2048 sc1\n\t"
"v_accvgpr_read_b32 v232, a44\n\t"
"v_accvgpr_read_b32 v231, a41\n\t"
"v_accvgpr_read_b32 v233, a45\n\t"
"v_cvt_pk_bf16_f32 v224, v224, v225\n\t"
"v_cvt_pk_bf16_f32 v228, v228, v229\n\t"
"s_mov_b32 s20, 1658880\n\t"
"v_pk_fma_f32 v[230:231], v[232:233], s[56:57], v[230:231]\n\t"
"v_pk_fma_f32 v[232:233], v[230:231], s[58:59], v[232:233] neg_lo:[0,0,1] neg_hi:[0,0,1]\n\t"
"v_pk_fma_f32 v[234:235], v[32:33], s[60:61], v[230:231]\n\t"
"v_pk_fma_f32 v[32:33], v[230:231], s[60:61], v[32:33]\n\t"
"v_pk_fma_f32 v[230:231], v[132:133], s[62:63], v[232:233]\n\t"
"v_pk_fma_f32 v[132:133], v[232:233], s[62:63], v[132:133]\n\t"
"v_mfma_f32_16x16x32_bf16 a[116:119], a[212:215], a[172:175], a[116:119]\n\t"
"ds_read_b128 a[172:175], v255 offset:3584\n\t"
"v_accvgpr_write_b32 a33, v224\n\t"
"v_mfma_f32_16x16x32_bf16 a[116:119], a[216:219], a[176:179], a[116:119]\n\t"
"v_accvgpr_write_b32 a37, v228\n\t"
"v_accvgpr_read_b32 v236, a42\n\t"
"v_mfma_f32_16x16x32_bf16 a[116:119], a[224:227], a[180:183], a[116:119]\n\t"
"buffer_load_dwordx4 a[240:243], off, s[80:83], s20 offset:0 sc1\n\t"
"v_accvgpr_read_b32 v238, a46\n\t"
"v_accvgpr_read_b32 v237, a43\n\t"
"v_accvgpr_read_b32 v239, a47\n\t"
"v_pk_fma_f32 v[236:237], v[238:239], s[56:57], v[236:237]\n\t"
"v_pk_fma_f32 v[238:239], v[236:237], s[58:59], v[238:239] neg_lo:[0,0,1] neg_hi:[0,0,1]\n\t"
"v_pk_fma_f32 v[98:99], v[34:35], s[60:61], v[236:237]\n\t"
"v_pk_fma_f32 v[34:35], v[236:237], s[60:61], v[34:35]\n\t"
"v_pk_fma_f32 v[236:237], v[134:135], s[62:63], v[238:239]\n\t"
"v_pk_fma_f32 v[134:135], v[238:239], s[62:63], v[134:135]\n\t"
"v_mfma_f32_16x16x32_bf16 a[124:127], a[224:227], a[184:187], a[124:127]\n\t"
"v_cvt_pk_bf16_f32 v198, v230, v231\n\t"
"v_cvt_pk_bf16_f32 v202, v234, v235\n\t"
"v_mfma_f32_16x16x32_bf16 a[124:127], a[216:219], a[180:183], a[124:127]\n\t"
"v_accvgpr_read_b32 v224, a48\n\t"
"v_accvgpr_read_b32 v226, a52\n\t"
"v_mfma_f32_16x16x32_bf16 a[124:127], a[212:215], a[176:179], a[124:127]\n\t"
"global_load_dwordx4 v[248:251], v252, s[48:49] offset:0\n\t"
"ds_read_b128 a[176:179], v255 offset:6656\n\t"
"v_accvgpr_read_b32 v225, a49\n\t"
"v_accvgpr_read_b32 v227, a53\n\t"
"v_accvgpr_read_b32 v230, a50\n\t"
"v_accvgpr_read_b32 v232, a54\n\t"
"v_accvgpr_read_b32 v231, a51\n\t"
"v_accvgpr_read_b32 v233, a55\n\t"
"v_pk_fma_f32 v[224:225], v[226:227], s[56:57], v[224:225]\n\t"
"v_pk_fma_f32 v[226:227], v[224:225], s[58:59], v[226:227] neg_lo:[0,0,1] neg_hi:[0,0,1]\n\t"
"v_pk_fma_f32 v[228:229], v[36:37], s[60:61], v[224:225]\n\t"
"v_pk_fma_f32 v[36:37], v[224:225], s[60:61], v[36:37]\n\t"
"v_pk_fma_f32 v[224:225], v[136:137], s[62:63], v[226:227]\n\t"
"v_pk_fma_f32 v[136:137], v[226:227], s[62:63], v[136:137]\n\t"
"v_pk_fma_f32 v[230:231], v[232:233], s[56:57], v[230:231]\n\t"
"v_pk_fma_f32 v[232:233], v[230:231], s[58:59], v[232:233] neg_lo:[0,0,1] neg_hi:[0,0,1]\n\t"
"v_pk_fma_f32 v[234:235], v[38:39], s[60:61], v[230:231]\n\t"
"v_pk_fma_f32 v[38:39], v[230:231], s[60:61], v[38:39]\n\t"
"v_pk_fma_f32 v[230:231], v[138:139], s[62:63], v[232:233]\n\t"
"v_pk_fma_f32 v[138:139], v[232:233], s[62:63], v[138:139]\n\t"
"v_mfma_f32_16x16x32_bf16 a[132:135], a[212:215], a[180:183], a[132:135]\n\t"
"ds_read_b128 a[180:183], v255 offset:9728\n\t"
"v_cvt_pk_bf16_f32 v199, v236, v237\n\t"
"v_mfma_f32_16x16x32_bf16 a[132:135], a[216:219], a[184:187], a[132:135]\n\t"
"v_cvt_pk_bf16_f32 v203, v98, v99\n\t"
"v_accvgpr_read_b32 v236, a56\n\t"
"v_mfma_f32_16x16x32_bf16 a[132:135], a[224:227], a[188:191], a[132:135]\n\t"
"buffer_load_dwordx4 a[40:43], off, s[80:83], s20 offset:1024 sc1\n\t"
"v_accvgpr_read_b32 v238, a60\n\t"
"v_accvgpr_read_b32 v237, a57\n\t"
"v_accvgpr_read_b32 v239, a61\n\t"
"v_pk_fma_f32 v[236:237], v[238:239], s[56:57], v[236:237]\n\t"
"v_pk_fma_f32 v[238:239], v[236:237], s[58:59], v[238:239] neg_lo:[0,0,1] neg_hi:[0,0,1]\n\t"
"v_pk_fma_f32 v[98:99], v[40:41], s[60:61], v[236:237]\n\t"
"v_pk_fma_f32 v[40:41], v[236:237], s[60:61], v[40:41]\n\t"
"v_pk_fma_f32 v[236:237], v[140:141], s[62:63], v[238:239]\n\t"
"v_pk_fma_f32 v[140:141], v[238:239], s[62:63], v[140:141]\n\t"
"v_mfma_f32_16x16x32_bf16 a[140:143], a[224:227], a[192:195], a[140:143]\n\t"
"v_cvt_pk_bf16_f32 v206, v224, v225\n\t"
"v_cvt_pk_bf16_f32 v210, v228, v229\n\t"
"v_mfma_f32_16x16x32_bf16 a[140:143], a[216:219], a[188:191], a[140:143]\n\t"
"v_cvt_pk_bf16_f32 v207, v230, v231\n\t"
"v_cvt_pk_bf16_f32 v211, v234, v235\n\t"
"v_mfma_f32_16x16x32_bf16 a[140:143], a[212:215], a[184:187], a[140:143]\n\t"
"buffer_load_dwordx4 a[48:51], off, s[80:83], s20 offset:2048 sc1\n\t"
"ds_read_b128 a[184:187], v255 offset:12800\n\t"
"v_accvgpr_read_b32 v224, a58\n\t"
"v_accvgpr_read_b32 v226, a62\n\t"
"v_accvgpr_read_b32 v225, a59\n\t"
"v_accvgpr_read_b32 v227, a63\n\t"
"s_mov_b32 s20, 1671168\n\t"
"v_pk_fma_f32 v[224:225], v[226:227], s[56:57], v[224:225]\n\t"
"v_pk_fma_f32 v[226:227], v[224:225], s[58:59], v[226:227] neg_lo:[0,0,1] neg_hi:[0,0,1]\n\t"
"v_pk_fma_f32 v[228:229], v[42:43], s[60:61], v[224:225]\n\t"
"v_pk_fma_f32 v[42:43], v[224:225], s[60:61], v[42:43]\n\t"
"v_pk_fma_f32 v[224:225], v[142:143], s[62:63], v[226:227]\n\t"
"v_pk_fma_f32 v[142:143], v[226:227], s[62:63], v[142:143]\n\t"
"v_mfma_f32_16x16x32_bf16 a[148:151], a[212:215], a[188:191], a[148:151]\n\t"
"ds_read_b128 a[188:191], v255 offset:15872\n\t"
"v_cvt_pk_bf16_f32 v214, v236, v237\n\t"
"v_mfma_f32_16x16x32_bf16 a[148:151], a[216:219], a[192:195], a[148:151]\n\t"
"v_cvt_pk_bf16_f32 v218, v98, v99\n\t"
"v_accvgpr_read_b32 v230, a64\n\t"
"v_mfma_f32_16x16x32_bf16 a[148:151], a[224:227], a[196:199], a[148:151]\n\t"
"buffer_load_dwordx4 a[52:55], off, s[80:83], s20 offset:0 sc1\n\t"
"v_accvgpr_read_b32 v232, a68\n\t"
"v_accvgpr_read_b32 v231, a65\n\t"
"v_accvgpr_read_b32 v233, a69\n\t"
"v_pk_fma_f32 v[230:231], v[232:233], s[56:57], v[230:231]\n\t"
"v_pk_fma_f32 v[232:233], v[230:231], s[58:59], v[232:233] neg_lo:[0,0,1] neg_hi:[0,0,1]\n\t"
"v_pk_fma_f32 v[234:235], v[44:45], s[60:61], v[230:231]\n\t"
"v_pk_fma_f32 v[44:45], v[230:231], s[60:61], v[44:45]\n\t"
"v_pk_fma_f32 v[230:231], v[144:145], s[62:63], v[232:233]\n\t"
"v_pk_fma_f32 v[144:145], v[232:233], s[62:63], v[144:145]\n\t"
"v_mfma_f32_16x16x32_bf16 a[156:159], a[224:227], a[200:203], a[156:159]\n\t"
"v_cvt_pk_bf16_f32 v215, v224, v225\n\t"
"v_cvt_pk_bf16_f32 v219, v228, v229\n\t"
"v_mfma_f32_16x16x32_bf16 a[156:159], a[216:219], a[196:199], a[156:159]\n\t"
"v_accvgpr_read_b32 v236, a66\n\t"
"v_accvgpr_read_b32 v238, a70\n\t"
"v_mfma_f32_16x16x32_bf16 a[156:159], a[212:215], a[192:195], a[156:159]\n\t"
"buffer_load_dwordx4 a[44:47], off, s[80:83], s20 offset:1024 sc1\n\t"
"ds_read_b128 a[192:195], v255 offset:18944\n\t"
"v_accvgpr_read_b32 v237, a67\n\t"
"v_accvgpr_read_b32 v239, a71\n\t"
"v_cvt_pk_bf16_f32 v230, v230, v231\n\t"
"v_cvt_pk_bf16_f32 v234, v234, v235\n\t"
"v_pk_fma_f32 v[236:237], v[238:239], s[56:57], v[236:237]\n\t"
"v_pk_fma_f32 v[238:239], v[236:237], s[58:59], v[238:239] neg_lo:[0,0,1] neg_hi:[0,0,1]\n\t"
"v_pk_fma_f32 v[98:99], v[46:47], s[60:61], v[236:237]\n\t"
"v_pk_fma_f32 v[46:47], v[236:237], s[60:61], v[46:47]\n\t"
"v_pk_fma_f32 v[236:237], v[146:147], s[62:63], v[238:239]\n\t"
"v_pk_fma_f32 v[146:147], v[238:239], s[62:63], v[146:147]\n\t"
"v_mfma_f32_16x16x32_bf16 a[164:167], a[212:215], a[196:199], a[164:167]\n\t"
"s_waitcnt vmcnt(12) lgkmcnt(4)\n\t"
"ds_read_b128 a[196:199], v255 offset:22016\n\t"
"v_accvgpr_write_b32 a2, v230\n\t"
"v_mfma_f32_16x16x32_bf16 a[164:167], a[216:219], a[200:203], a[164:167]\n\t"
"ds_read_b128 a[200:203], v255 offset:25088\n\t"
"v_accvgpr_write_b32 a6, v234\n\t"
"v_mfma_f32_16x16x32_bf16 a[164:167], a[224:227], a[204:207], a[164:167]\n\t"
"buffer_load_dwordx4 a[60:63], off, s[80:83], s20 offset:2048 sc1\n\t"
"ds_read_b128 a[204:207], v255 offset:28160\n\t"
"v_accvgpr_read_b32 v224, a72\n\t"
"v_accvgpr_read_b32 v226, a76\n\t"
"v_accvgpr_read_b32 v225, a73\n\t"
"v_accvgpr_read_b32 v227, a77\n\t"
"v_cvt_pk_bf16_f32 v236, v236, v237\n\t"
"v_cvt_pk_bf16_f32 v98, v98, v99\n\t"
"s_mov_b32 s20, 1683456\n\t"
"v_pk_fma_f32 v[224:225], v[226:227], s[56:57], v[224:225]\n\t"
"v_pk_fma_f32 v[226:227], v[224:225], s[58:59], v[226:227] neg_lo:[0,0,1] neg_hi:[0,0,1]\n\t"
"v_pk_fma_f32 v[228:229], v[48:49], s[60:61], v[224:225]\n\t"
"v_pk_fma_f32 v[48:49], v[224:225], s[60:61], v[48:49]\n\t"
"v_pk_fma_f32 v[224:225], v[148:149], s[62:63], v[226:227]\n\t"
"v_pk_fma_f32 v[148:149], v[226:227], s[62:63], v[148:149]\n\t"
"Lst14_%=:\n\t"
"v_mfma_f32_16x16x32_bf16 v[0:3], a[228:231], a[168:171], v[0:3]\n\t"
"s_waitcnt lgkmcnt(6)\n\t"
"v_accvgpr_write_b32 a3, v236\n\t"
"v_accvgpr_write_b32 a7, v98\n\t"
"v_mfma_f32_16x16x32_bf16 v[0:3], a[248:251], a[176:179], v[0:3]\n\t"
"v_accvgpr_read_b32 v230, a74\n\t"
"v_accvgpr_read_b32 v232, a78\n\t"
"v_mfma_f32_16x16x32_bf16 v[0:3], a[244:247], a[172:175], v[0:3]\n\t"
"buffer_load_dwordx4 a[64:67], off, s[80:83], s20 offset:0 sc1\n\t"
"v_accvgpr_read_b32 v231, a75\n\t"
"v_accvgpr_read_b32 v233, a79\n\t"
"v_cvt_pk_bf16_f32 v224, v224, v225\n\t"
"v_cvt_pk_bf16_f32 v228, v228, v229\n\t"
"v_pk_fma_f32 v[230:231], v[232:233], s[56:57], v[230:231]\n\t"
"v_pk_fma_f32 v[232:233], v[230:231], s[58:59], v[232:233] neg_lo:[0,0,1] neg_hi:[0,0,1]\n\t"
"v_pk_fma_f32 v[234:235], v[50:51], s[60:61], v[230:231]\n\t"
"v_pk_fma_f32 v[50:51], v[230:231], s[60:61], v[50:51]\n\t"
"v_pk_fma_f32 v[230:231], v[150:151], s[62:63], v[232:233]\n\t"
"v_pk_fma_f32 v[150:151], v[232:233], s[62:63], v[150:151]\n\t"
"v_mfma_f32_16x16x32_bf16 v[4:7], a[228:231], a[172:175], v[4:7]\n\t"
"s_waitcnt lgkmcnt(5)\n\t"
"v_accvgpr_write_b32 a10, v224\n\t"
"v_accvgpr_write_b32 a14, v228\n\t"
"v_mfma_f32_16x16x32_bf16 v[4:7], a[244:247], a[176:179], v[4:7]\n\t"
"v_accvgpr_read_b32 v236, a80\n\t"
"v_accvgpr_read_b32 v238, a84\n\t"
"v_mfma_f32_16x16x32_bf16 v[4:7], a[248:251], a[180:183], v[4:7]\n\t"
"buffer_load_dwordx4 a[68:71], off, s[80:83], s20 offset:1024 sc1\n\t"
"v_accvgpr_read_b32 v237, a81\n\t"
"v_accvgpr_read_b32 v239, a85\n\t"
"v_cvt_pk_bf16_f32 v230, v230, v231\n\t"
"v_cvt_pk_bf16_f32 v234, v234, v235\n\t"
"v_pk_fma_f32 v[236:237], v[238:239], s[56:57], v[236:237]\n\t"
"v_pk_fma_f32 v[238:239], v[236:237], s[58:59], v[238:239] neg_lo:[0,0,1] neg_hi:[0,0,1]\n\t"
"v_pk_fma_f32 v[98:99], v[52:53], s[60:61], v[236:237]\n\t"
"v_pk_fma_f32 v[52:53], v[236:237], s[60:61], v[52:53]\n\t"
"v_pk_fma_f32 v[236:237], v[152:153], s[62:63], v[238:239]\n\t"
"v_pk_fma_f32 v[152:153], v[238:239], s[62:63], v[152:153]\n\t"
"v_mfma_f32_16x16x32_bf16 v[8:11], a[248:251], a[184:187], v[8:11]\n\t"
"s_waitcnt lgkmcnt(4)\n\t"
"v_accvgpr_write_b32 a11, v230\n\t"
"v_accvgpr_write_b32 a15, v234\n\t"
"v_mfma_f32_16x16x32_bf16 v[8:11], a[244:247], a[180:183], v[8:11]\n\t"
"v_accvgpr_read_b32 v224, a82\n\t"
"v_accvgpr_read_b32 v226, a86\n\t"
"v_mfma_f32_16x16x32_bf16 v[8:11], a[228:231], a[176:179], v[8:11]\n\t"
"buffer_load_dwordx4 a[72:75], off, s[80:83], s20 offset:2048 sc1\n\t"
"v_accvgpr_read_b32 v225, a83\n\t"
"v_accvgpr_read_b32 v227, a87\n\t"
"v_cvt_pk_bf16_f32 v236, v236, v237\n\t"
"v_cvt_pk_bf16_f32 v98, v98, v99\n\t"
"s_mov_b32 s20, 589824\n\t"
"v_pk_fma_f32 v[224:225], v[226:227], s[56:57], v[224:225]\n\t"
"v_pk_fma_f32 v[226:227], v[224:225], s[58:59], v[226:227] neg_lo:[0,0,1] neg_hi:[0,0,1]\n\t"
"v_pk_fma_f32 v[228:229], v[54:55], s[60:61], v[224:225]\n\t"
"v_pk_fma_f32 v[54:55], v[224:225], s[60:61], v[54:55]\n\t"
"v_pk_fma_f32 v[224:225], v[154:155], s[62:63], v[226:227]\n\t"
"v_pk_fma_f32 v[154:155], v[226:227], s[62:63], v[154:155]\n\t"
"v_mfma_f32_16x16x32_bf16 v[12:15], a[228:231], a[180:183], v[12:15]\n\t"
"s_waitcnt lgkmcnt(3)\n\t"
"v_accvgpr_write_b32 a18, v236\n\t"
"v_accvgpr_write_b32 a22, v98\n\t"
"v_mfma_f32_16x16x32_bf16 v[12:15], a[244:247], a[184:187], v[12:15]\n\t"
"v_accvgpr_read_b32 v230, a88\n\t"
"v_accvgpr_read_b32 v232, a92\n\t"
"v_mfma_f32_16x16x32_bf16 v[12:15], a[248:251], a[188:191], v[12:15]\n\t"
"buffer_load_dwordx4 a[76:79], off, s[80:83], s20 offset:0 sc1\n\t"
"v_accvgpr_read_b32 v231, a89\n\t"
"v_accvgpr_read_b32 v233, a93\n\t"
"v_accvgpr_read_b32 v236, a90\n\t"
"v_accvgpr_read_b32 v238, a94\n\t"
"v_accvgpr_read_b32 v237, a91\n\t"
"v_accvgpr_read_b32 v239, a95\n\t"
"v_cvt_pk_bf16_f32 v224, v224, v225\n\t"
"v_cvt_pk_bf16_f32 v228, v228, v229\n\t"
"v_pk_fma_f32 v[230:231], v[232:233], s[56:57], v[230:231]\n\t"
"v_pk_fma_f32 v[232:233], v[230:231], s[58:59], v[232:233] neg_lo:[0,0,1] neg_hi:[0,0,1]\n\t"
"v_pk_fma_f32 v[234:235], v[56:57], s[60:61], v[230:231]\n\t"
"v_pk_fma_f32 v[56:57], v[230:231], s[60:61], v[56:57]\n\t"
"v_pk_fma_f32 v[230:231], v[156:157], s[62:63], v[232:233]\n\t"
"v_pk_fma_f32 v[156:157], v[232:233], s[62:63], v[156:157]\n\t"
"v_pk_fma_f32 v[236:237], v[238:239], s[56:57], v[236:237]\n\t"
"v_pk_fma_f32 v[238:239], v[236:237], s[58:59], v[238:239] neg_lo:[0,0,1] neg_hi:[0,0,1]\n\t"
"v_pk_fma_f32 v[98:99], v[58:59], s[60:61], v[236:237]\n\t"
"v_pk_fma_f32 v[58:59], v[236:237], s[60:61], v[58:59]\n\t"
"v_pk_fma_f32 v[236:237], v[158:159], s[62:63], v[238:239]\n\t"
"v_pk_fma_f32 v[158:159], v[238:239], s[62:63], v[158:159]\n\t"
"v_mfma_f32_16x16x32_bf16 v[16:19], a[248:251], a[192:195], v[16:19]\n\t"
"s_waitcnt lgkmcnt(2)\n\t"
"v_accvgpr_write_b32 a19, v224\n\t"
"v_accvgpr_write_b32 a23, v228\n\t"
"v_mfma_f32_16x16x32_bf16 v[16:19], a[244:247], a[188:191], v[16:19]\n\t"
"v_accvgpr_read_b32 v224, a96\n\t"
"v_accvgpr_read_b32 v226, a100\n\t"
"v_mfma_f32_16x16x32_bf16 v[16:19], a[228:231], a[184:187], v[16:19]\n\t"
"buffer_load_dwordx4 a[84:87], off, s[80:83], s20 offset:1024 sc1\n\t"
"v_accvgpr_read_b32 v225, a97\n\t"
"v_accvgpr_read_b32 v227, a101\n\t"
"v_cvt_pk_bf16_f32 v230, v230, v231\n\t"
"v_cvt_pk_bf16_f32 v234, v234, v235\n\t"
"v_pk_fma_f32 v[224:225], v[226:227], s[56:57], v[224:225]\n\t"
"v_pk_fma_f32 v[226:227], v[224:225], s[58:59], v[226:227] neg_lo:[0,0,1] neg_hi:[0,0,1]\n\t"
"v_pk_fma_f32 v[228:229], v[60:61], s[60:61], v[224:225]\n\t"
"v_pk_fma_f32 v[60:61], v[224:225], s[60:61], v[60:61]\n\t"
"v_pk_fma_f32 v[224:225], v[160:161], s[62:63], v[226:227]\n\t"
"v_pk_fma_f32 v[160:161], v[226:227], s[62:63], v[160:161]\n\t"
"v_mfma_f32_16x16x32_bf16 v[20:23], a[228:231], a[188:191], v[20:23]\n\t"
"s_waitcnt lgkmcnt(1)\n\t"
"v_accvgpr_write_b32 a26, v230\n\t"
"v_cvt_pk_bf16_f32 v236, v236, v237\n\t"
"v_mfma_f32_16x16x32_bf16 v[20:23], a[244:247], a[192:195], v[20:23]\n\t"
"v_accvgpr_write_b32 a30, v234\n\t"
"v_cvt_pk_bf16_f32 v98, v98, v99\n\t"
"v_mfma_f32_16x16x32_bf16 v[20:23], a[248:251], a[196:199], v[20:23]\n\t"
"buffer_load_dwordx4 a[88:91], off, s[80:83], s20 offset:2048 sc1\n\t"
"v_accvgpr_read_b32 v230, a98\n\t"
"v_accvgpr_read_b32 v232, a102\n\t"
"v_accvgpr_read_b32 v231, a99\n\t"
"v_accvgpr_read_b32 v233, a103\n\t"
"v_accvgpr_write_b32 a27, v236\n\t"
"v_accvgpr_write_b32 a31, v98\n\t"
"v_cvt_pk_bf16_f32 v224, v224, v225\n\t"
"v_cvt_pk_bf16_f32 v228, v228, v229\n\t"
"s_mov_b32 s20, 602112\n\t"
"v_pk_fma_f32 v[230:231], v[232:233], s[56:57], v[230:231]\n\t"
"v_pk_fma_f32 v[232:233], v[230:231], s[58:59], v[232:233] neg_lo:[0,0,1] neg_hi:[0,0,1]\n\t"
"v_pk_fma_f32 v[234:235], v[62:63], s[60:61], v[230:231]\n\t"
"v_pk_fma_f32 v[62:63], v[230:231], s[60:61], v[62:63]\n\t"
"v_pk_fma_f32 v[230:231], v[162:163], s[62:63], v[232:233]\n\t"
"v_pk_fma_f32 v[162:163], v[232:233], s[62:63], v[162:163]\n\t"
"v_mfma_f32_16x16x32_bf16 v[24:27], a[248:251], a[200:203], v[24:27]\n\t"
"s_waitcnt lgkmcnt(0)\n\t"
"v_accvgpr_write_b32 a34, v224\n\t"
"v_accvgpr_write_b32 a38, v228\n\t"
"v_mfma_f32_16x16x32_bf16 v[24:27], a[244:247], a[196:199], v[24:27]\n\t"
"v_accvgpr_read_b32 v236, a104\n\t"
"v_accvgpr_read_b32 v238, a108\n\t"
"v_mfma_f32_16x16x32_bf16 v[24:27], a[228:231], a[192:195], v[24:27]\n\t"
"buffer_load_dwordx4 a[92:95], off, s[80:83], s20 offset:0 sc1\n\t"
"v_accvgpr_read_b32 v237, a105\n\t"
"v_accvgpr_read_b32 v239, a109\n\t"
"v_cvt_pk_bf16_f32 v230, v230, v231\n\t"
"v_cvt_pk_bf16_f32 v234, v234, v235\n\t"
"v_pk_fma_f32 v[236:237], v[238:239], s[56:57], v[236:237]\n\t"
"v_pk_fma_f32 v[238:239], v[236:237], s[58:59], v[238:239] neg_lo:[0,0,1] neg_hi:[0,0,1]\n\t"
"v_pk_fma_f32 v[98:99], v[64:65], s[60:61], v[236:237]\n\t"
"v_pk_fma_f32 v[64:65], v[236:237], s[60:61], v[64:65]\n\t"
"v_pk_fma_f32 v[236:237], v[164:165], s[62:63], v[238:239]\n\t"
"v_pk_fma_f32 v[164:165], v[238:239], s[62:63], v[164:165]\n\t"
"v_mfma_f32_16x16x32_bf16 v[28:31], a[228:231], a[196:199], v[28:31]\n\t"
"s_waitcnt vmcnt(17)\n\t"
"v_accvgpr_write_b32 a35, v230\n\t"
"v_accvgpr_write_b32 a39, v234\n\t"
"v_mfma_f32_16x16x32_bf16 v[28:31], a[244:247], a[200:203], v[28:31]\n\t"
"v_accvgpr_read_b32 v224, a106\n\t"
"v_accvgpr_read_b32 v226, a110\n\t"
"v_mfma_f32_16x16x32_bf16 v[28:31], a[248:251], a[204:207], v[28:31]\n\t"
"buffer_load_dwordx4 a[96:99], off, s[80:83], s20 offset:1024 sc1\n\t"
"v_accvgpr_read_b32 v225, a107\n\t"
"v_accvgpr_read_b32 v227, a111\n\t"
"v_cvt_pk_bf16_f32 v236, v236, v237\n\t"
"v_cvt_pk_bf16_f32 v98, v98, v99\n\t"
"v_pk_fma_f32 v[224:225], v[226:227], s[56:57], v[224:225]\n\t"
"v_pk_fma_f32 v[226:227], v[224:225], s[58:59], v[226:227] neg_lo:[0,0,1] neg_hi:[0,0,1]\n\t"
"v_pk_fma_f32 v[228:229], v[66:67], s[60:61], v[224:225]\n\t"
"v_pk_fma_f32 v[66:67], v[224:225], s[60:61], v[66:67]\n\t"
"v_pk_fma_f32 v[224:225], v[166:167], s[62:63], v[226:227]\n\t"
"v_pk_fma_f32 v[166:167], v[226:227], s[62:63], v[166:167]\n\t"
"v_mfma_f32_16x16x32_bf16 v[32:35], v[240:243], a[168:171], v[32:35]\n\t"
"v_accvgpr_write_b32 a104, v236\n\t"
"v_accvgpr_write_b32 a106, v98\n\t"
"v_mfma_f32_16x16x32_bf16 v[32:35], a[232:235], a[176:179], v[32:35]\n\t"
"v_accvgpr_read_b32 v230, a112\n\t"
"v_accvgpr_read_b32 v232, a116\n\t"
"v_mfma_f32_16x16x32_bf16 v[32:35], a[208:211], a[172:175], v[32:35]\n\t"
"buffer_load_dwordx4 a[100:103], off, s[80:83], s20 offset:2048 sc1\n\t"
"v_accvgpr_read_b32 v231, a113\n\t"
"v_accvgpr_read_b32 v233, a117\n\t"
"v_cvt_pk_bf16_f32 v224, v224, v225\n\t"
"v_cvt_pk_bf16_f32 v228, v228, v229\n\t"
"s_mov_b32 s20, 614400\n\t"
"v_pk_fma_f32 v[230:231], v[232:233], s[56:57], v[230:231]\n\t"
"v_pk_fma_f32 v[232:233], v[230:231], s[58:59], v[232:233] neg_lo:[0,0,1] neg_hi:[0,0,1]\n\t"
"v_pk_fma_f32 v[234:235], v[68:69], s[60:61], v[230:231]\n\t"
"v_pk_fma_f32 v[68:69], v[230:231], s[60:61], v[68:69]\n\t"
"v_pk_fma_f32 v[230:231], v[168:169], s[62:63], v[232:233]\n\t"
"v_pk_fma_f32 v[168:169], v[232:233], s[62:63], v[168:169]\n\t"
"v_mfma_f32_16x16x32_bf16 v[36:39], v[240:243], a[172:175], v[36:39]\n\t"
"v_accvgpr_write_b32 a105, v224\n\t"
"v_accvgpr_write_b32 a107, v228\n\t"
"v_mfma_f32_16x16x32_bf16 v[36:39], a[208:211], a[176:179], v[36:39]\n\t"
"v_accvgpr_read_b32 v236, a114\n\t"
"v_accvgpr_read_b32 v238, a118\n\t"
"v_mfma_f32_16x16x32_bf16 v[36:39], a[232:235], a[180:183], v[36:39]\n\t"
"buffer_load_dwordx4 a[212:215], off, s[80:83], s20 offset:0 sc1\n\t"
"v_accvgpr_read_b32 v237, a115\n\t"
"v_accvgpr_read_b32 v239, a119\n\t"
"v_cvt_pk_bf16_f32 v230, v230, v231\n\t"
"v_cvt_pk_bf16_f32 v234, v234, v235\n\t"
"v_pk_fma_f32 v[236:237], v[238:239], s[56:57], v[236:237]\n\t"
"v_pk_fma_f32 v[238:239], v[236:237], s[58:59], v[238:239] neg_lo:[0,0,1] neg_hi:[0,0,1]\n\t"
"v_pk_fma_f32 v[98:99], v[70:71], s[60:61], v[236:237]\n\t"
"v_pk_fma_f32 v[70:71], v[236:237], s[60:61], v[70:71]\n\t"
"v_pk_fma_f32 v[236:237], v[170:171], s[62:63], v[238:239]\n\t"
"v_pk_fma_f32 v[170:171], v[238:239], s[62:63], v[170:171]\n\t"
"v_mfma_f32_16x16x32_bf16 v[40:43], a[232:235], a[184:187], v[40:43]\n\t"
"v_accvgpr_write_b32 a112, v230\n\t"
"v_accvgpr_write_b32 a114, v234\n\t"
"v_mfma_f32_16x16x32_bf16 v[40:43], a[208:211], a[180:183], v[40:43]\n\t"
"v_accvgpr_read_b32 v224, a120\n\t"
"v_accvgpr_read_b32 v226, a124\n\t"
"v_mfma_f32_16x16x32_bf16 v[40:43], v[240:243], a[176:179], v[40:43]\n\t"
"buffer_load_dwordx4 a[216:219], off, s[80:83], s20 offset:1024 sc1\n\t"
"v_accvgpr_read_b32 v225, a121\n\t"
"v_accvgpr_read_b32 v227, a125\n\t"
"v_cvt_pk_bf16_f32 v236, v236, v237\n\t"
"v_cvt_pk_bf16_f32 v98, v98, v99\n\t"
"v_pk_fma_f32 v[224:225], v[226:227], s[56:57], v[224:225]\n\t"
"v_pk_fma_f32 v[226:227], v[224:225], s[58:59], v[226:227] neg_lo:[0,0,1] neg_hi:[0,0,1]\n\t"
"v_pk_fma_f32 v[228:229], v[72:73], s[60:61], v[224:225]\n\t"
"v_pk_fma_f32 v[72:73], v[224:225], s[60:61], v[72:73]\n\t"
"v_pk_fma_f32 v[224:225], v[172:173], s[62:63], v[226:227]\n\t"
"v_pk_fma_f32 v[172:173], v[226:227], s[62:63], v[172:173]\n\t"
"v_mfma_f32_16x16x32_bf16 v[44:47], v[240:243], a[180:183], v[44:47]\n\t"
"v_accvgpr_write_b32 a113, v236\n\t"
"v_accvgpr_write_b32 a115, v98\n\t"
"v_mfma_f32_16x16x32_bf16 v[44:47], a[208:211], a[184:187], v[44:47]\n\t"
"v_accvgpr_read_b32 v230, a122\n\t"
"v_accvgpr_read_b32 v232, a126\n\t"
"v_mfma_f32_16x16x32_bf16 v[44:47], a[232:235], a[188:191], v[44:47]\n\t"
"buffer_load_dwordx4 a[116:119], off, s[80:83], s20 offset:2048 sc1\n\t"
"v_accvgpr_read_b32 v231, a123\n\t"
"v_accvgpr_read_b32 v233, a127\n\t"
"v_accvgpr_read_b32 v236, a128\n\t"
"v_accvgpr_read_b32 v238, a132\n\t"
"v_accvgpr_read_b32 v237, a129\n\t"
"v_accvgpr_read_b32 v239, a133\n\t"
"v_cvt_pk_bf16_f32 v224, v224, v225\n\t"
"v_cvt_pk_bf16_f32 v228, v228, v229\n\t"
"s_mov_b32 s20, 774144\n\t"
"v_pk_fma_f32 v[230:231], v[232:233], s[56:57], v[230:231]\n\t"
"v_pk_fma_f32 v[232:233], v[230:231], s[58:59], v[232:233] neg_lo:[0,0,1] neg_hi:[0,0,1]\n\t"
"v_pk_fma_f32 v[234:235], v[74:75], s[60:61], v[230:231]\n\t"
"v_pk_fma_f32 v[74:75], v[230:231], s[60:61], v[74:75]\n\t"
"v_pk_fma_f32 v[230:231], v[174:175], s[62:63], v[232:233]\n\t"
"v_pk_fma_f32 v[174:175], v[232:233], s[62:63], v[174:175]\n\t"
"v_pk_fma_f32 v[236:237], v[238:239], s[56:57], v[236:237]\n\t"
"v_pk_fma_f32 v[238:239], v[236:237], s[58:59], v[238:239] neg_lo:[0,0,1] neg_hi:[0,0,1]\n\t"
"v_pk_fma_f32 v[98:99], v[76:77], s[60:61], v[236:237]\n\t"
"v_pk_fma_f32 v[76:77], v[236:237], s[60:61], v[76:77]\n\t"
"v_pk_fma_f32 v[236:237], v[176:177], s[62:63], v[238:239]\n\t"
"v_pk_fma_f32 v[176:177], v[238:239], s[62:63], v[176:177]\n\t"
"v_mfma_f32_16x16x32_bf16 v[48:51], a[232:235], a[192:195], v[48:51]\n\t"
"v_accvgpr_write_b32 a120, v224\n\t"
"v_accvgpr_write_b32 a122, v228\n\t"
"v_mfma_f32_16x16x32_bf16 v[48:51], a[208:211], a[188:191], v[48:51]\n\t"
"v_accvgpr_read_b32 v224, a130\n\t"
"v_accvgpr_read_b32 v226, a134\n\t"
"v_mfma_f32_16x16x32_bf16 v[48:51], v[240:243], a[184:187], v[48:51]\n\t"
"buffer_load_dwordx4 a[224:227], off, s[80:83], s20 offset:0 sc1\n\t"
"v_accvgpr_read_b32 v225, a131\n\t"
"v_accvgpr_read_b32 v227, a135\n\t"
"v_cvt_pk_bf16_f32 v230, v230, v231\n\t"
"v_cvt_pk_bf16_f32 v234, v234, v235\n\t"
"v_cvt_pk_bf16_f32 v236, v236, v237\n\t"
"v_cvt_pk_bf16_f32 v98, v98, v99\n\t"
"Lpg11_%=:\n\t"
"s_cbranch_vccnz Lvr0_%=\n\t"
"v_pk_fma_f32 v[224:225], v[226:227], s[56:57], v[224:225]\n\t"
"v_pk_fma_f32 v[226:227], v[224:225], s[58:59], v[226:227] neg_lo:[0,0,1] neg_hi:[0,0,1]\n\t"
"v_pk_fma_f32 v[228:229], v[78:79], s[60:61], v[224:225]\n\t"
"v_pk_fma_f32 v[78:79], v[224:225], s[60:61], v[78:79]\n\t"
"v_pk_fma_f32 v[224:225], v[178:179], s[62:63], v[226:227]\n\t"
"v_pk_fma_f32 v[178:179], v[226:227], s[62:63], v[178:179]\n\t"
"v_mfma_f32_16x16x32_bf16 v[52:55], v[240:243], a[188:191], v[52:55]\n\t"
"v_accvgpr_write_b32 a121, v230\n\t"
"v_accvgpr_write_b32 a123, v234\n\t"
"v_mfma_f32_16x16x32_bf16 v[52:55], a[208:211], a[192:195], v[52:55]\n\t"
"v_accvgpr_write_b32 a128, v236\n\t"
"v_accvgpr_write_b32 a130, v98\n\t"
"v_mfma_f32_16x16x32_bf16 v[52:55], a[232:235], a[196:199], v[52:55]\n\t"
"buffer_load_dwordx4 a[124:127], off, s[80:83], s20 offset:1024 sc1\n\t"
"v_accvgpr_read_b32 v230, a136\n\t"
"v_accvgpr_read_b32 v232, a140\n\t"
"v_accvgpr_read_b32 v231, a137\n\t"
"v_accvgpr_read_b32 v233, a141\n\t"
"v_cvt_pk_bf16_f32 v224, v224, v225\n\t"
"v_cvt_pk_bf16_f32 v228, v228, v229\n\t"
"v_pk_fma_f32 v[230:231], v[232:233], s[56:57], v[230:231]\n\t"
"v_pk_fma_f32 v[232:233], v[230:231], s[58:59], v[232:233] neg_lo:[0,0,1] neg_hi:[0,0,1]\n\t"
"v_pk_fma_f32 v[234:235], v[80:81], s[60:61], v[230:231]\n\t"
"v_pk_fma_f32 v[80:81], v[230:231], s[60:61], v[80:81]\n\t"
"v_pk_fma_f32 v[230:231], v[180:181], s[62:63], v[232:233]\n\t"
"v_pk_fma_f32 v[180:181], v[232:233], s[62:63], v[180:181]\n\t"
"v_mfma_f32_16x16x32_bf16 v[56:59], a[232:235], a[200:203], v[56:59]\n\t"
"v_accvgpr_write_b32 a129, v224\n\t"
"v_accvgpr_write_b32 a131, v228\n\t"
"v_mfma_f32_16x16x32_bf16 v[56:59], a[208:211], a[196:199], v[56:59]\n\t"
"v_accvgpr_read_b32 v236, a138\n\t"
"v_accvgpr_read_b32 v238, a142\n\t"
"v_mfma_f32_16x16x32_bf16 v[56:59], v[240:243], a[192:195], v[56:59]\n\t"
"s_waitcnt vmcnt(19)\n\t"
"ds_write_b128 v249, v[196:199] offset:0\n\t"
"v_accvgpr_read_b32 v237, a139\n\t"
"v_accvgpr_read_b32 v239, a143\n\t"
"v_cvt_pk_bf16_f32 v230, v230, v231\n\t"
"v_cvt_pk_bf16_f32 v234, v234, v235\n\t"
"v_pk_fma_f32 v[236:237], v[238:239], s[56:57], v[236:237]\n\t"
"v_pk_fma_f32 v[238:239], v[236:237], s[58:59], v[238:239] neg_lo:[0,0,1] neg_hi:[0,0,1]\n\t"
"v_pk_fma_f32 v[98:99], v[82:83], s[60:61], v[236:237]\n\t"
"v_pk_fma_f32 v[82:83], v[236:237], s[60:61], v[82:83]\n\t"
"v_pk_fma_f32 v[236:237], v[182:183], s[62:63], v[238:239]\n\t"
"v_pk_fma_f32 v[182:183], v[238:239], s[62:63], v[182:183]\n\t"
"v_mfma_f32_16x16x32_bf16 v[60:63], v[240:243], a[196:199], v[60:63]\n\t"
"v_accvgpr_write_b32 a136, v230\n\t"
"v_accvgpr_write_b32 a138, v234\n\t"
"v_mfma_f32_16x16x32_bf16 v[60:63], a[208:211], a[200:203], v[60:63]\n\t"
"v_accvgpr_read_b32 v224, a144\n\t"
"v_accvgpr_read_b32 v226, a148\n\t"
"v_mfma_f32_16x16x32_bf16 v[60:63], a[232:235], a[204:207], v[60:63]\n\t"
"buffer_load_dwordx4 a[132:135], off, s[80:83], s20 offset:2048 sc1\n\t"
"v_accvgpr_read_b32 v225, a145\n\t"
"v_accvgpr_read_b32 v227, a149\n\t"
"v_cvt_pk_bf16_f32 v236, v236, v237\n\t"
"v_cvt_pk_bf16_f32 v98, v98, v99\n\t"
"s_mov_b32 s20, 786432\n\t"
"v_pk_fma_f32 v[224:225], v[226:227], s[56:57], v[224:225]\n\t"
"v_pk_fma_f32 v[226:227], v[224:225], s[58:59], v[226:227] neg_lo:[0,0,1] neg_hi:[0,0,1]\n\t"
"v_pk_fma_f32 v[228:229], v[84:85], s[60:61], v[224:225]\n\t"
"v_pk_fma_f32 v[84:85], v[224:225], s[60:61], v[84:85]\n\t"
"v_pk_fma_f32 v[224:225], v[184:185], s[62:63], v[226:227]\n\t"
"v_pk_fma_f32 v[184:185], v[226:227], s[62:63], v[184:185]\n\t"
"v_mfma_f32_16x16x32_bf16 v[64:67], a[252:255], a[168:171], v[64:67]\n\t"
"ds_read_b128 a[168:171], v255 offset:2048\n\t"
"v_accvgpr_write_b32 a137, v236\n\t"
"v_mfma_f32_16x16x32_bf16 v[64:67], a[236:239], a[176:179], v[64:67]\n\t"
"v_accvgpr_write_b32 a139, v98\n\t"
"v_accvgpr_read_b32 v230, a146\n\t"
"v_mfma_f32_16x16x32_bf16 v[64:67], a[220:223], a[172:175], v[64:67]\n\t"
"buffer_load_dwordx4 a[228:231], off, s[80:83], s20 offset:0 sc1\n\t"
"v_accvgpr_read_b32 v232, a150\n\t"
"v_accvgpr_read_b32 v231, a147\n\t"
"v_accvgpr_read_b32 v233, a151\n\t"
"v_cvt_pk_bf16_f32 v224, v224, v225\n\t"
"v_cvt_pk_bf16_f32 v228, v228, v229\n\t"
"v_pk_fma_f32 v[230:231], v[232:233], s[56:57], v[230:231]\n\t"
"v_pk_fma_f32 v[232:233], v[230:231], s[58:59], v[232:233] neg_lo:[0,0,1] neg_hi:[0,0,1]\n\t"
"v_pk_fma_f32 v[234:235], v[86:87], s[60:61], v[230:231]\n\t"
"v_pk_fma_f32 v[86:87], v[230:231], s[60:61], v[86:87]\n\t"
"v_pk_fma_f32 v[230:231], v[186:187], s[62:63], v[232:233]\n\t"
"v_pk_fma_f32 v[186:187], v[232:233], s[62:63], v[186:187]\n\t"
"v_mfma_f32_16x16x32_bf16 v[68:71], a[252:255], a[172:175], v[68:71]\n\t"
"ds_read_b128 a[172:175], v255 offset:5120\n\t"
"v_accvgpr_write_b32 a144, v224\n\t"
"v_mfma_f32_16x16x32_bf16 v[68:71], a[220:223], a[176:179], v[68:71]\n\t"
"v_accvgpr_write_b32 a146, v228\n\t"
"v_accvgpr_read_b32 v236, a152\n\t"
"v_mfma_f32_16x16x32_bf16 v[68:71], a[236:239], a[180:183], v[68:71]\n\t"
"ds_write_b64 v250, a[104:105] offset:0\n\t"
"v_accvgpr_read_b32 v238, a156\n\t"
"v_accvgpr_read_b32 v237, a153\n\t"
"v_accvgpr_read_b32 v239, a157\n\t"
"v_cvt_pk_bf16_f32 v230, v230, v231\n\t"
"v_cvt_pk_bf16_f32 v234, v234, v235\n\t"
"v_pk_fma_f32 v[236:237], v[238:239], s[56:57], v[236:237]\n\t"
"v_pk_fma_f32 v[238:239], v[236:237], s[58:59], v[238:239] neg_lo:[0,0,1] neg_hi:[0,0,1]\n\t"
"v_pk_fma_f32 v[98:99], v[88:89], s[60:61], v[236:237]\n\t"
"v_pk_fma_f32 v[88:89], v[236:237], s[60:61], v[88:89]\n\t"
"v_pk_fma_f32 v[236:237], v[188:189], s[62:63], v[238:239]\n\t"
"v_pk_fma_f32 v[188:189], v[238:239], s[62:63], v[188:189]\n\t"
"v_mfma_f32_16x16x32_bf16 v[72:75], a[236:239], a[184:187], v[72:75]\n\t"
"v_accvgpr_write_b32 a145, v230\n\t"
"v_accvgpr_write_b32 a147, v234\n\t"
"v_mfma_f32_16x16x32_bf16 v[72:75], a[220:223], a[180:183], v[72:75]\n\t"
"v_accvgpr_read_b32 v224, a154\n\t"
"v_accvgpr_read_b32 v226, a158\n\t"
"v_mfma_f32_16x16x32_bf16 v[72:75], a[252:255], a[176:179], v[72:75]\n\t"
"buffer_load_dwordx4 v[240:243], off, s[80:83], s20 offset:1024 sc1\n\t"
"ds_read_b128 a[176:179], v255 offset:8192\n\t"
"v_accvgpr_read_b32 v225, a155\n\t"
"v_accvgpr_read_b32 v227, a159\n\t"
"v_cvt_pk_bf16_f32 v236, v236, v237\n\t"
"v_cvt_pk_bf16_f32 v98, v98, v99\n\t"
"v_pk_fma_f32 v[224:225], v[226:227], s[56:57], v[224:225]\n\t"
"v_pk_fma_f32 v[226:227], v[224:225], s[58:59], v[226:227] neg_lo:[0,0,1] neg_hi:[0,0,1]\n\t"
"v_pk_fma_f32 v[228:229], v[90:91], s[60:61], v[224:225]\n\t"
"v_pk_fma_f32 v[90:91], v[224:225], s[60:61], v[90:91]\n\t"
"v_pk_fma_f32 v[224:225], v[190:191], s[62:63], v[226:227]\n\t"
"v_pk_fma_f32 v[190:191], v[226:227], s[62:63], v[190:191]\n\t"
"v_mfma_f32_16x16x32_bf16 v[76:79], a[252:255], a[180:183], v[76:79]\n\t"
"ds_read_b128 a[180:183], v255 offset:11264\n\t"
"v_accvgpr_write_b32 a152, v236\n\t"
"v_mfma_f32_16x16x32_bf16 v[76:79], a[220:223], a[184:187], v[76:79]\n\t"
"v_accvgpr_write_b32 a154, v98\n\t"
"v_accvgpr_read_b32 v230, a160\n\t"
"v_mfma_f32_16x16x32_bf16 v[76:79], a[236:239], a[188:191], v[76:79]\n\t"
"buffer_load_dwordx4 a[140:143], off, s[80:83], s20 offset:2048 sc1\n\t"
"v_accvgpr_read_b32 v232, a164\n\t"
"v_accvgpr_read_b32 v231, a161\n\t"
"v_accvgpr_read_b32 v233, a165\n\t"
"v_cvt_pk_bf16_f32 v224, v224, v225\n\t"
"v_cvt_pk_bf16_f32 v228, v228, v229\n\t"
"s_mov_b32 s20, 798720\n\t"
"v_pk_fma_f32 v[230:231], v[232:233], s[56:57], v[230:231]\n\t"
"v_pk_fma_f32 v[232:233], v[230:231], s[58:59], v[232:233] neg_lo:[0,0,1] neg_hi:[0,0,1]\n\t"
"v_pk_fma_f32 v[234:235], v[92:93], s[60:61], v[230:231]\n\t"
"v_pk_fma_f32 v[92:93], v[230:231], s[60:61], v[92:93]\n\t"
"v_pk_fma_f32 v[230:231], v[192:193], s[62:63], v[232:233]\n\t"
"v_pk_fma_f32 v[192:193], v[232:233], s[62:63], v[192:193]\n\t"
"v_mfma_f32_16x16x32_bf16 v[80:83], a[236:239], a[192:195], v[80:83]\n\t"
"v_accvgpr_write_b32 a153, v224\n\t"
"v_accvgpr_write_b32 a155, v228\n\t"
"v_mfma_f32_16x16x32_bf16 v[80:83], a[220:223], a[188:191], v[80:83]\n\t"
"v_accvgpr_read_b32 v236, a162\n\t"
"v_accvgpr_read_b32 v238, a166\n\t"
"v_mfma_f32_16x16x32_bf16 v[80:83], a[252:255], a[184:187], v[80:83]\n\t"
"buffer_load_dwordx4 a[148:151], off, s[80:83], s20 offset:0 sc1\n\t"
"ds_read_b128 a[184:187], v255 offset:14336\n\t"
"v_accvgpr_read_b32 v237, a163\n\t"
"v_accvgpr_read_b32 v239, a167\n\t"
"v_cvt_pk_bf16_f32 v230, v230, v231\n\t"
"v_cvt_pk_bf16_f32 v234, v234, v235\n\t"
"v_pk_fma_f32 v[236:237], v[238:239], s[56:57], v[236:237]\n\t"
"v_pk_fma_f32 v[238:239], v[236:237], s[58:59], v[238:239] neg_lo:[0,0,1] neg_hi:[0,0,1]\n\t"
"v_pk_fma_f32 v[98:99], v[94:95], s[60:61], v[236:237]\n\t"
"v_pk_fma_f32 v[94:95], v[236:237], s[60:61], v[94:95]\n\t"
"v_pk_fma_f32 v[236:237], v[194:195], s[62:63], v[238:239]\n\t"
"v_pk_fma_f32 v[194:195], v[238:239], s[62:63], v[194:195]\n\t"
"v_mfma_f32_16x16x32_bf16 v[84:87], a[252:255], a[188:191], v[84:87]\n\t"
"ds_read_b128 a[188:191], v255 offset:17408\n\t"
"v_accvgpr_write_b32 a160, v230\n\t"
"v_mfma_f32_16x16x32_bf16 v[84:87], a[220:223], a[192:195], v[84:87]\n\t"
"v_accvgpr_write_b32 a162, v234\n\t"
"v_cvt_pk_bf16_f32 v236, v236, v237\n\t"
"v_mfma_f32_16x16x32_bf16 v[84:87], a[236:239], a[196:199], v[84:87]\n\t"
"buffer_load_dwordx4 a[208:211], off, s[80:83], s20 offset:1024 sc1\n\t"
"v_cvt_pk_bf16_f32 v98, v98, v99\n\t"
"v_mfma_f32_16x16x32_bf16 v[88:91], a[236:239], a[200:203], v[88:91]\n\t"
"v_accvgpr_write_b32 a161, v236\n\t"
"v_accvgpr_write_b32 a163, v98\n\t"
"v_mfma_f32_16x16x32_bf16 v[88:91], a[220:223], a[196:199], v[88:91]\n\t"
"v_mfma_f32_16x16x32_bf16 v[88:91], a[252:255], a[192:195], v[88:91]\n\t"
"buffer_load_dwordx4 a[232:235], off, s[80:83], s20 offset:2048 sc1\n\t"
"ds_write_b128 v249, v[200:203] offset:6144\n\t"
"s_mov_b32 s20, 884736\n\t"
"v_mfma_f32_16x16x32_bf16 v[92:95], a[252:255], a[196:199], v[92:95]\n\t"
"buffer_load_dwordx4 a[244:247], off, s[80:83], s20 offset:0 sc1\n\t"
"v_mfma_f32_16x16x32_bf16 v[92:95], a[220:223], a[200:203], v[92:95]\n\t"
"buffer_load_dwordx4 a[164:167], off, s[80:83], s20 offset:1024 sc1\n\t"
"v_mfma_f32_16x16x32_bf16 v[92:95], a[236:239], a[204:207], v[92:95]\n\t"
"s_waitcnt vmcnt(26) lgkmcnt(4)\n\t"
"ds_write_b64 v250, a[106:107] offset:6144\n\t"
"v_mfma_f32_16x16x32_bf16 v[100:103], a[240:243], a[168:171], v[100:103]\n\t"
"s_waitcnt lgkmcnt(4)\n\t"
"ds_read_b128 a[192:195], v255 offset:20480\n\t"
"v_mfma_f32_16x16x32_bf16 v[100:103], a[48:51], a[176:179], v[100:103]\n\t"
"buffer_load_dwordx4 a[248:251], off, s[80:83], s20 offset:2048 sc1\n\t"
"v_mfma_f32_16x16x32_bf16 v[100:103], a[40:43], a[172:175], v[100:103]\n\t"
"s_mov_b32 s20, 897024\n\t"
"ds_read_b128 a[196:199], v255 offset:23552\n\t"
"s_add_u32 m0, s25, 512\n\t"
"v_mfma_f32_16x16x32_bf16 v[104:107], a[240:243], a[172:175], v[104:107]\n\t"
"s_waitcnt lgkmcnt(5)\n\t"
"ds_read_b128 a[200:203], v255 offset:26624\n\t"
"v_mfma_f32_16x16x32_bf16 v[104:107], a[40:43], a[176:179], v[104:107]\n\t"
"ds_read_b128 a[204:207], v255 offset:29696\n\t"
"v_mfma_f32_16x16x32_bf16 v[104:107], a[48:51], a[180:183], v[104:107]\n\t"
"s_waitcnt lgkmcnt(0)\n\t"
"s_barrier\n\t"
"v_mfma_f32_16x16x32_bf16 v[108:111], a[48:51], a[184:187], v[108:111]\n\t"
"v_mfma_f32_16x16x32_bf16 v[108:111], a[40:43], a[180:183], v[108:111]\n\t"
"buffer_load_dwordx4 v[236:239], off, s[80:83], s20 offset:0 sc1\n\t"
"v_mfma_f32_16x16x32_bf16 v[108:111], a[240:243], a[176:179], v[108:111]\n\t"
"buffer_load_dwordx4 v[96:99], off, s[80:83], s20 offset:1024 sc1\n\t"
"v_mfma_f32_16x16x32_bf16 v[112:115], a[240:243], a[180:183], v[112:115]\n\t"
"buffer_load_dwordx4 v254, s[12:15], 0 offen offset:0 nt lds\n\t"
"v_mfma_f32_16x16x32_bf16 v[112:115], a[40:43], a[184:187], v[112:115]\n\t"
"buffer_load_dwordx4 a[220:223], off, s[80:83], s20 offset:2048 sc1\n\t"
"v_mfma_f32_16x16x32_bf16 v[112:115], a[48:51], a[188:191], v[112:115]\n\t"
"s_add_u32 m0, s24, 512\n\t"
"s_add_u32 s28, s21, 0\n\t"
"ds_write_b128 v249, v[204:207] offset:12288\n\t"
"v_mfma_f32_16x16x32_bf16 v[116:119], a[48:51], a[192:195], v[116:119]\n\t"
"buffer_load_dwordx4 v247, s[12:15], s28 offen offset:0 nt lds\n\t"
"v_mfma_f32_16x16x32_bf16 v[116:119], a[40:43], a[188:191], v[116:119]\n\t"
"s_add_u32 m0, s24, 6656\n\t"
"s_add_u32 s28, s21, 196608\n\t"
"v_mfma_f32_16x16x32_bf16 v[116:119], a[240:243], a[184:187], v[116:119]\n\t"
"buffer_load_dwordx4 v247, s[12:15], s28 offen offset:0 nt lds\n\t"
"s_add_u32 m0, s24, 12800\n\t"
"s_add_u32 s28, s21, 393216\n\t"
"v_mfma_f32_16x16x32_bf16 v[120:123], a[240:243], a[188:191], v[120:123]\n\t"
"buffer_load_dwordx4 v247, s[12:15], s28 offen offset:0 nt lds\n\t"
"v_mfma_f32_16x16x32_bf16 v[120:123], a[40:43], a[192:195], v[120:123]\n\t"
"s_add_u32 m0, s24, 20480\n\t"
"s_add_u32 s28, s21, 589824\n\t"
"v_mfma_f32_16x16x32_bf16 v[120:123], a[48:51], a[196:199], v[120:123]\n\t"
"buffer_load_dwordx4 v247, s[12:15], s28 offen offset:0 nt lds\n\t"
"v_mfma_f32_16x16x32_bf16 v[124:127], a[48:51], a[200:203], v[124:127]\n\t"
"v_mfma_f32_16x16x32_bf16 v[124:127], a[40:43], a[196:199], v[124:127]\n\t"
"v_mfma_f32_16x16x32_bf16 v[124:127], a[240:243], a[192:195], v[124:127]\n\t"
"ds_write_b64 v250, a[112:113] offset:12288\n\t"
"v_mfma_f32_16x16x32_bf16 v[128:131], a[240:243], a[196:199], v[128:131]\n\t"
"s_waitcnt vmcnt(32)\n\t"
"v_mfma_f32_16x16x32_bf16 v[128:131], a[40:43], a[200:203], v[128:131]\n\t"
"v_mfma_f32_16x16x32_bf16 v[128:131], a[48:51], a[204:207], v[128:131]\n\t"
"ds_write_b128 v249, v[208:211] offset:18432\n\t"
"v_mfma_f32_16x16x32_bf16 v[132:135], a[52:55], a[168:171], v[132:135]\n\t"
"v_mfma_f32_16x16x32_bf16 v[132:135], a[60:63], a[176:179], v[132:135]\n\t"
"v_mfma_f32_16x16x32_bf16 v[132:135], a[44:47], a[172:175], v[132:135]\n\t"
"ds_write_b64 v250, a[114:115] offset:18432\n\t"
"v_mfma_f32_16x16x32_bf16 v[136:139], a[52:55], a[172:175], v[136:139]\n\t"
"v_mfma_f32_16x16x32_bf16 v[136:139], a[44:47], a[176:179], v[136:139]\n\t"
"v_mfma_f32_16x16x32_bf16 v[136:139], a[60:63], a[180:183], v[136:139]\n\t"
"v_mfma_f32_16x16x32_bf16 v[140:143], a[60:63], a[184:187], v[140:143]\n\t"
"v_mfma_f32_16x16x32_bf16 v[140:143], a[44:47], a[180:183], v[140:143]\n\t"
"v_mfma_f32_16x16x32_bf16 v[140:143], a[52:55], a[176:179], v[140:143]\n\t"
"s_mov_b32 s20, 909312\n\t"
"v_mfma_f32_16x16x32_bf16 v[144:147], a[52:55], a[180:183], v[144:147]\n\t"
"v_mfma_f32_16x16x32_bf16 v[144:147], a[44:47], a[184:187], v[144:147]\n\t"
"v_mfma_f32_16x16x32_bf16 v[144:147], a[60:63], a[188:191], v[144:147]\n\t"
"v_mfma_f32_16x16x32_bf16 v[148:151], a[60:63], a[192:195], v[148:151]\n\t"
"v_mfma_f32_16x16x32_bf16 v[148:151], a[44:47], a[188:191], v[148:151]\n\t"
"v_mfma_f32_16x16x32_bf16 v[148:151], a[52:55], a[184:187], v[148:151]\n\t"
"buffer_load_dwordx4 a[104:107], off, s[80:83], s20 offset:0 sc1\n\t"
"v_mfma_f32_16x16x32_bf16 v[152:155], a[52:55], a[188:191], v[152:155]\n\t"
"ds_read_b128 a[40:43], v252 offset:0\n\t"
"v_mfma_f32_16x16x32_bf16 v[152:155], a[44:47], a[192:195], v[152:155]\n\t"
"ds_read_b128 a[48:51], v252 offset:1024\n\t"
"v_mfma_f32_16x16x32_bf16 v[152:155], a[60:63], a[196:199], v[152:155]\n\t"
"v_mfma_f32_16x16x32_bf16 v[156:159], a[60:63], a[200:203], v[156:159]\n\t"
"s_add_u32 m0, s24, 26624\n\t"
"s_add_u32 s28, s21, 786432\n\t"
"ds_read_b128 a[56:59], v252 offset:2048\n\t"
"v_mfma_f32_16x16x32_bf16 v[156:159], a[44:47], a[196:199], v[156:159]\n\t"
"v_mfma_f32_16x16x32_bf16 v[156:159], a[52:55], a[192:195], v[156:159]\n\t"
"v_mfma_f32_16x16x32_bf16 v[160:163], a[52:55], a[196:199], v[160:163]\n\t"
"s_waitcnt vmcnt(30)\n\t"
"v_mfma_f32_16x16x32_bf16 v[160:163], a[44:47], a[200:203], v[160:163]\n\t"
"buffer_load_dwordx4 v247, s[12:15], s28 offen offset:0 nt lds\n\t"
"v_mfma_f32_16x16x32_bf16 v[160:163], a[60:63], a[204:207], v[160:163]\n\t"
"s_add_u32 m0, s24, 32768\n\t"
"s_add_u32 s28, s21, 983040\n\t"
"v_mfma_f32_16x16x32_bf16 v[164:167], a[64:67], a[168:171], v[164:167]\n\t"
"buffer_load_dwordx4 v247, s[12:15], s28 offen offset:0 nt lds\n\t"
"v_mfma_f32_16x16x32_bf16 v[164:167], a[72:75], a[176:179], v[164:167]\n\t"
"s_add_u32 m0, s24, 40448\n\t"
"s_add_u32 s28, s21, 1179648\n\t"
"ds_read_b128 a[168:171], v244 offset:24576\n\t"
"v_mfma_f32_16x16x32_bf16 v[164:167], a[68:71], a[172:175], v[164:167]\n\t"
"buffer_load_dwordx4 v247, s[12:15], s28 offen offset:0 nt lds\n\t"
"v_mfma_f32_16x16x32_bf16 v[168:171], a[64:67], a[172:175], v[168:171]\n\t"
"s_add_u32 m0, s24, 46592\n\t"
"s_add_u32 s28, s21, 1376256\n\t"
"ds_read_b128 a[172:175], v244 offset:25088\n\t"
"v_mfma_f32_16x16x32_bf16 v[168:171], a[68:71], a[176:179], v[168:171]\n\t"
"buffer_load_dwordx4 v247, s[12:15], s28 offen offset:0 nt lds\n\t"
"v_mfma_f32_16x16x32_bf16 v[168:171], a[72:75], a[180:183], v[168:171]\n\t"
"s_add_u32 m0, s24, 52736\n\t"
"s_add_u32 s28, s21, 1572864\n\t"
"v_mfma_f32_16x16x32_bf16 v[172:175], a[72:75], a[184:187], v[172:175]\n\t"
"buffer_load_dwordx4 v247, s[12:15], s28 offen offset:0 nt lds\n\t"
"v_mfma_f32_16x16x32_bf16 v[172:175], a[68:71], a[180:183], v[172:175]\n\t"
"v_mfma_f32_16x16x32_bf16 v[172:175], a[64:67], a[176:179], v[172:175]\n\t"
"ds_read_b128 a[176:179], v244 offset:25600\n\t"
"v_mfma_f32_16x16x32_bf16 v[176:179], a[64:67], a[180:183], v[176:179]\n\t"
"ds_read_b128 a[180:183], v244 offset:26112\n\t"
"v_mfma_f32_16x16x32_bf16 v[176:179], a[68:71], a[184:187], v[176:179]\n\t"
"v_mfma_f32_16x16x32_bf16 v[176:179], a[72:75], a[188:191], v[176:179]\n\t"
"s_waitcnt lgkmcnt(4)\n\t"
"s_barrier\n\t"
"v_mfma_f32_16x16x32_bf16 v[180:183], a[72:75], a[192:195], v[180:183]\n\t"
"v_mfma_f32_16x16x32_bf16 v[180:183], a[68:71], a[188:191], v[180:183]\n\t"
"v_mfma_f32_16x16x32_bf16 v[180:183], a[64:67], a[184:187], v[180:183]\n\t"
"ds_read_b128 a[184:187], v244 offset:26624\n\t"
"v_mfma_f32_16x16x32_bf16 v[184:187], a[64:67], a[188:191], v[184:187]\n\t"
"ds_read_b128 a[188:191], v244 offset:27136\n\t"
"v_mfma_f32_16x16x32_bf16 v[184:187], a[68:71], a[192:195], v[184:187]\n\t"
"v_mfma_f32_16x16x32_bf16 v[184:187], a[72:75], a[196:199], v[184:187]\n\t"
"ds_write_b128 v249, v[212:215] offset:0\n\t"
"v_mfma_f32_16x16x32_bf16 v[188:191], a[72:75], a[200:203], v[188:191]\n\t"
"v_mfma_f32_16x16x32_bf16 v[188:191], a[68:71], a[196:199], v[188:191]\n\t"
"v_mfma_f32_16x16x32_bf16 v[188:191], a[64:67], a[192:195], v[188:191]\n\t"
"ds_read_b128 a[192:195], v244 offset:27648\n\t"
"v_mfma_f32_16x16x32_bf16 v[192:195], a[64:67], a[196:199], v[192:195]\n\t"
"s_waitcnt vmcnt(32) lgkmcnt(5)\n\t"
"ds_read_b128 a[196:199], v244 offset:28160\n\t"
"v_mfma_f32_16x16x32_bf16 v[192:195], a[68:71], a[200:203], v[192:195]\n\t"
"ds_read_b128 a[200:203], v244 offset:28672\n\t"
"v_mfma_f32_16x16x32_bf16 v[192:195], a[72:75], a[204:207], v[192:195]\n\t"
"ds_read_b128 a[204:207], v244 offset:29184\n\t"
"Lst15_%=:\n\t"
"v_mfma_f32_16x16x32_bf16 v[0:3], a[76:79], a[168:171], v[0:3]\n\t"
"s_waitcnt lgkmcnt(7)\n\t"
"v_mfma_f32_16x16x32_bf16 v[0:3], a[88:91], a[176:179], v[0:3]\n\t"
"v_mfma_f32_16x16x32_bf16 v[0:3], a[84:87], a[172:175], v[0:3]\n\t"
"ds_write_b64 v250, a[120:121] offset:0\n\t"
"v_mfma_f32_16x16x32_bf16 v[4:7], a[76:79], a[172:175], v[4:7]\n\t"
"s_waitcnt lgkmcnt(7)\n\t"
"v_mfma_f32_16x16x32_bf16 v[4:7], a[84:87], a[176:179], v[4:7]\n\t"
"v_mfma_f32_16x16x32_bf16 v[4:7], a[88:91], a[180:183], v[4:7]\n\t"
"ds_write_b128 v249, v[216:219] offset:6144\n\t"
"v_mfma_f32_16x16x32_bf16 v[8:11], a[88:91], a[184:187], v[8:11]\n\t"
"s_waitcnt lgkmcnt(7)\n\t"
"v_mfma_f32_16x16x32_bf16 v[8:11], a[84:87], a[180:183], v[8:11]\n\t"
"v_mfma_f32_16x16x32_bf16 v[8:11], a[76:79], a[176:179], v[8:11]\n\t"
"ds_write_b64 v250, a[122:123] offset:6144\n\t"
"v_mfma_f32_16x16x32_bf16 v[12:15], a[76:79], a[180:183], v[12:15]\n\t"
"s_waitcnt lgkmcnt(6)\n\t"
"v_mfma_f32_16x16x32_bf16 v[12:15], a[84:87], a[184:187], v[12:15]\n\t"
"v_mfma_f32_16x16x32_bf16 v[12:15], a[88:91], a[188:191], v[12:15]\n\t"
"v_mfma_f32_16x16x32_bf16 v[16:19], a[88:91], a[192:195], v[16:19]\n\t"
"s_waitcnt lgkmcnt(5)\n\t"
"v_mfma_f32_16x16x32_bf16 v[16:19], a[84:87], a[188:191], v[16:19]\n\t"
"v_mfma_f32_16x16x32_bf16 v[16:19], a[76:79], a[184:187], v[16:19]\n\t"
"ds_read_b128 a[64:67], v252 offset:12288\n\t"
"v_mfma_f32_16x16x32_bf16 v[20:23], a[76:79], a[188:191], v[20:23]\n\t"
"s_waitcnt lgkmcnt(5)\n\t"
"v_mfma_f32_16x16x32_bf16 v[20:23], a[84:87], a[192:195], v[20:23]\n\t"
"ds_read_b128 a[72:75], v252 offset:13312\n\t"
"v_mfma_f32_16x16x32_bf16 v[20:23], a[88:91], a[196:199], v[20:23]\n\t"
"v_mfma_f32_16x16x32_bf16 v[24:27], a[88:91], a[200:203], v[24:27]\n\t"
"s_waitcnt lgkmcnt(5)\n\t"
"ds_read_b128 a[80:83], v252 offset:14336\n\t"
"v_mfma_f32_16x16x32_bf16 v[24:27], a[84:87], a[196:199], v[24:27]\n\t"
"v_mfma_f32_16x16x32_bf16 v[24:27], a[76:79], a[192:195], v[24:27]\n\t"
"v_mfma_f32_16x16x32_bf16 v[28:31], a[76:79], a[196:199], v[28:31]\n\t"
"s_waitcnt vmcnt(29)\n\t"
"v_mfma_f32_16x16x32_bf16 v[28:31], a[84:87], a[200:203], v[28:31]\n\t"
"v_mfma_f32_16x16x32_bf16 v[28:31], a[88:91], a[204:207], v[28:31]\n\t"
"v_mfma_f32_16x16x32_bf16 v[32:35], a[92:95], a[168:171], v[32:35]\n\t"
"v_mfma_f32_16x16x32_bf16 v[32:35], a[100:103], a[176:179], v[32:35]\n\t"
"v_mfma_f32_16x16x32_bf16 v[32:35], a[96:99], a[172:175], v[32:35]\n\t"
"v_mfma_f32_16x16x32_bf16 v[36:39], a[92:95], a[172:175], v[36:39]\n\t"
"v_mfma_f32_16x16x32_bf16 v[36:39], a[96:99], a[176:179], v[36:39]\n\t"
"v_mfma_f32_16x16x32_bf16 v[36:39], a[100:103], a[180:183], v[36:39]\n\t"
"v_mfma_f32_16x16x32_bf16 v[40:43], a[100:103], a[184:187], v[40:43]\n\t"
"v_mfma_f32_16x16x32_bf16 v[40:43], a[96:99], a[180:183], v[40:43]\n\t"
"v_mfma_f32_16x16x32_bf16 v[40:43], a[92:95], a[176:179], v[40:43]\n\t"
"v_mfma_f32_16x16x32_bf16 v[44:47], a[92:95], a[180:183], v[44:47]\n\t"
"v_mfma_f32_16x16x32_bf16 v[44:47], a[96:99], a[184:187], v[44:47]\n\t"
"v_mfma_f32_16x16x32_bf16 v[44:47], a[100:103], a[188:191], v[44:47]\n\t"
"s_waitcnt lgkmcnt(0)\n\t"
"s_barrier\n\t"
"v_mfma_f32_16x16x32_bf16 v[48:51], a[100:103], a[192:195], v[48:51]\n\t"
"v_mfma_f32_16x16x32_bf16 v[48:51], a[96:99], a[188:191], v[48:51]\n\t"
"v_mfma_f32_16x16x32_bf16 v[48:51], a[92:95], a[184:187], v[48:51]\n\t"
"ds_write_b128 v249, a[0:3] offset:12288\n\t"
"v_mfma_f32_16x16x32_bf16 v[52:55], a[92:95], a[188:191], v[52:55]\n\t"
"v_mfma_f32_16x16x32_bf16 v[52:55], a[96:99], a[192:195], v[52:55]\n\t"
"v_mfma_f32_16x16x32_bf16 v[52:55], a[100:103], a[196:199], v[52:55]\n\t"
"ds_write_b64 v250, a[128:129] offset:12288\n\t"
"v_mfma_f32_16x16x32_bf16 v[56:59], a[100:103], a[200:203], v[56:59]\n\t"
"v_mfma_f32_16x16x32_bf16 v[56:59], a[96:99], a[196:199], v[56:59]\n\t"
"v_mfma_f32_16x16x32_bf16 v[56:59], a[92:95], a[192:195], v[56:59]\n\t"
"ds_write_b128 v249, a[4:7] offset:18432\n\t"
"v_mfma_f32_16x16x32_bf16 v[60:63], a[92:95], a[196:199], v[60:63]\n\t"
"s_waitcnt vmcnt(26)\n\t"
"v_mfma_f32_16x16x32_bf16 v[60:63], a[96:99], a[200:203], v[60:63]\n\t"
"v_mfma_f32_16x16x32_bf16 v[60:63], a[100:103], a[204:207], v[60:63]\n\t"
"ds_write_b64 v250, a[130:131] offset:18432\n\t"
"v_mfma_f32_16x16x32_bf16 v[64:67], a[212:215], a[168:171], v[64:67]\n\t"
"ds_read_b128 a[168:171], v244 offset:33792\n\t"
"v_mfma_f32_16x16x32_bf16 v[64:67], a[116:119], a[176:179], v[64:67]\n\t"
"v_mfma_f32_16x16x32_bf16 v[64:67], a[216:219], a[172:175], v[64:67]\n\t"
"buffer_load_dwordx4 a[112:115], off, s[80:83], s20 offset:1024 sc1\n\t"
"v_mfma_f32_16x16x32_bf16 v[68:71], a[212:215], a[172:175], v[68:71]\n\t"
"ds_read_b128 a[172:175], v244 offset:34304\n\t"
"v_mfma_f32_16x16x32_bf16 v[68:71], a[216:219], a[176:179], v[68:71]\n\t"
"v_mfma_f32_16x16x32_bf16 v[68:71], a[116:119], a[180:183], v[68:71]\n\t"
"v_mfma_f32_16x16x32_bf16 v[72:75], a[116:119], a[184:187], v[72:75]\n\t"
"v_mfma_f32_16x16x32_bf16 v[72:75], a[216:219], a[180:183], v[72:75]\n\t"
"v_mfma_f32_16x16x32_bf16 v[72:75], a[212:215], a[176:179], v[72:75]\n\t"
"ds_read_b128 a[176:179], v244 offset:34816\n\t"
"v_mfma_f32_16x16x32_bf16 v[76:79], a[212:215], a[180:183], v[76:79]\n\t"
"ds_read_b128 a[180:183], v244 offset:35328\n\t"
"v_mfma_f32_16x16x32_bf16 v[76:79], a[216:219], a[184:187], v[76:79]\n\t"
"ds_read_b128 a[88:91], v252 offset:0\n\t"
"v_mfma_f32_16x16x32_bf16 v[76:79], a[116:119], a[188:191], v[76:79]\n\t"
"ds_read_b128 a[96:99], v252 offset:1024\n\t"
"v_mfma_f32_16x16x32_bf16 v[80:83], a[116:119], a[192:195], v[80:83]\n\t"
"ds_read_b128 a[108:111], v252 offset:2048\n\t"
"v_mfma_f32_16x16x32_bf16 v[80:83], a[216:219], a[188:191], v[80:83]\n\t"
"v_mfma_f32_16x16x32_bf16 v[80:83], a[212:215], a[184:187], v[80:83]\n\t"
"ds_read_b128 a[184:187], v244 offset:35840\n\t"
"v_mfma_f32_16x16x32_bf16 v[84:87], a[212:215], a[188:191], v[84:87]\n\t"
"ds_read_b128 a[188:191], v244 offset:36352\n\t"
"v_mfma_f32_16x16x32_bf16 v[84:87], a[216:219], a[192:195], v[84:87]\n\t"
"v_mfma_f32_16x16x32_bf16 v[84:87], a[116:119], a[196:199], v[84:87]\n\t"
"v_mfma_f32_16x16x32_bf16 v[88:91], a[116:119], a[200:203], v[88:91]\n\t"
"v_mfma_f32_16x16x32_bf16 v[88:91], a[216:219], a[196:199], v[88:91]\n\t"
"v_mfma_f32_16x16x32_bf16 v[88:91], a[212:215], a[192:195], v[88:91]\n\t"
"ds_read_b128 a[192:195], v244 offset:36864\n\t"
"v_mfma_f32_16x16x32_bf16 v[92:95], a[212:215], a[196:199], v[92:95]\n\t"
"s_waitcnt vmcnt(24) lgkmcnt(7)\n\t"
"ds_read_b128 a[196:199], v244 offset:37376\n\t"
"v_mfma_f32_16x16x32_bf16 v[92:95], a[216:219], a[200:203], v[92:95]\n\t"
"ds_read_b128 a[200:203], v244 offset:37888\n\t"
"v_mfma_f32_16x16x32_bf16 v[92:95], a[116:119], a[204:207], v[92:95]\n\t"
"ds_read_b128 a[204:207], v244 offset:38400\n\t"
"v_mfma_f32_16x16x32_bf16 v[100:103], a[224:227], a[168:171], v[100:103]\n\t"
"s_waitcnt lgkmcnt(9)\n\t"
"v_mfma_f32_16x16x32_bf16 v[100:103], a[132:135], a[176:179], v[100:103]\n\t"
"v_mfma_f32_16x16x32_bf16 v[100:103], a[124:127], a[172:175], v[100:103]\n\t"
"v_mfma_f32_16x16x32_bf16 v[104:107], a[224:227], a[172:175], v[104:107]\n\t"
"s_waitcnt lgkmcnt(5)\n\t"
"v_mfma_f32_16x16x32_bf16 v[104:107], a[124:127], a[176:179], v[104:107]\n\t"
"v_mfma_f32_16x16x32_bf16 v[104:107], a[132:135], a[180:183], v[104:107]\n\t"
"s_waitcnt lgkmcnt(0)\n\t"
"s_barrier\n\t"
"v_mfma_f32_16x16x32_bf16 v[108:111], a[132:135], a[184:187], v[108:111]\n\t"
"v_mfma_f32_16x16x32_bf16 v[108:111], a[124:127], a[180:183], v[108:111]\n\t"
"v_mfma_f32_16x16x32_bf16 v[108:111], a[224:227], a[176:179], v[108:111]\n\t"
"ds_write_b128 v249, a[8:11] offset:0\n\t"
"v_mfma_f32_16x16x32_bf16 v[112:115], a[224:227], a[180:183], v[112:115]\n\t"
"v_mfma_f32_16x16x32_bf16 v[112:115], a[124:127], a[184:187], v[112:115]\n\t"
"buffer_load_dwordx4 a[44:47], off, s[80:83], s20 offset:2048 sc1\n\t"
"v_mfma_f32_16x16x32_bf16 v[112:115], a[132:135], a[188:191], v[112:115]\n\t"
"s_mov_b32 s20, 1069056\n\t"
"ds_write_b64 v250, a[136:137] offset:0\n\t"
"v_mfma_f32_16x16x32_bf16 v[116:119], a[132:135], a[192:195], v[116:119]\n\t"
"v_mfma_f32_16x16x32_bf16 v[116:119], a[124:127], a[188:191], v[116:119]\n\t"
"v_mfma_f32_16x16x32_bf16 v[116:119], a[224:227], a[184:187], v[116:119]\n\t"
"buffer_load_dwordx4 a[60:63], off, s[80:83], s20 offset:0 sc1\n\t"
"v_mfma_f32_16x16x32_bf16 v[120:123], a[224:227], a[188:191], v[120:123]\n\t"
"v_mfma_f32_16x16x32_bf16 v[120:123], a[124:127], a[192:195], v[120:123]\n\t"
"v_mfma_f32_16x16x32_bf16 v[120:123], a[132:135], a[196:199], v[120:123]\n\t"
"ds_write_b128 v249, a[12:15] offset:6144\n\t"
"v_mfma_f32_16x16x32_bf16 v[124:127], a[132:135], a[200:203], v[124:127]\n\t"
"buffer_load_dwordx4 a[68:71], off, s[80:83], s20 offset:1024 sc1\n\t"
"v_mfma_f32_16x16x32_bf16 v[124:127], a[124:127], a[196:199], v[124:127]\n\t"
"v_mfma_f32_16x16x32_bf16 v[124:127], a[224:227], a[192:195], v[124:127]\n\t"
"ds_write_b64 v250, a[138:139] offset:6144\n\t"
"v_mfma_f32_16x16x32_bf16 v[128:131], a[224:227], a[196:199], v[128:131]\n\t"
"s_waitcnt vmcnt(24)\n\t"
"v_mfma_f32_16x16x32_bf16 v[128:131], a[124:127], a[200:203], v[128:131]\n\t"
"buffer_load_dwordx4 a[76:79], off, s[80:83], s20 offset:2048 sc1\n\t"
"v_mfma_f32_16x16x32_bf16 v[128:131], a[132:135], a[204:207], v[128:131]\n\t"
"s_mov_b32 s20, 1081344\n\t"
"v_mfma_f32_16x16x32_bf16 v[132:135], a[228:231], a[168:171], v[132:135]\n\t"
"v_mfma_f32_16x16x32_bf16 v[132:135], a[140:143], a[176:179], v[132:135]\n\t"
"v_mfma_f32_16x16x32_bf16 v[132:135], v[240:243], a[172:175], v[132:135]\n\t"
"buffer_load_dwordx4 a[132:135], off, s[80:83], s20 offset:0 sc1\n\t"
"v_mfma_f32_16x16x32_bf16 v[136:139], a[228:231], a[172:175], v[136:139]\n\t"
"v_mfma_f32_16x16x32_bf16 v[136:139], v[240:243], a[176:179], v[136:139]\n\t"
"v_mfma_f32_16x16x32_bf16 v[136:139], a[140:143], a[180:183], v[136:139]\n\t"
"v_mfma_f32_16x16x32_bf16 v[140:143], a[140:143], a[184:187], v[140:143]\n\t"
"buffer_load_dwordx4 a[92:95], off, s[80:83], s20 offset:1024 sc1\n\t"
"v_mfma_f32_16x16x32_bf16 v[140:143], v[240:243], a[180:183], v[140:143]\n\t"
"v_mfma_f32_16x16x32_bf16 v[140:143], a[228:231], a[176:179], v[140:143]\n\t"
"v_mfma_f32_16x16x32_bf16 v[144:147], a[228:231], a[180:183], v[144:147]\n\t"
"v_mfma_f32_16x16x32_bf16 v[144:147], v[240:243], a[184:187], v[144:147]\n\t"
"buffer_load_dwordx4 a[100:103], off, s[80:83], s20 offset:2048 sc1\n\t"
"v_mfma_f32_16x16x32_bf16 v[144:147], a[140:143], a[188:191], v[144:147]\n\t"
"s_mov_b32 s20, 1093632\n\t"
"v_mfma_f32_16x16x32_bf16 v[148:151], a[140:143], a[192:195], v[148:151]\n\t"
"v_mfma_f32_16x16x32_bf16 v[148:151], v[240:243], a[188:191], v[148:151]\n\t"
"v_mfma_f32_16x16x32_bf16 v[148:151], a[228:231], a[184:187], v[148:151]\n\t"
"buffer_load_dwordx4 v[200:203], off, s[80:83], s20 offset:0 sc1\n\t"
"v_mfma_f32_16x16x32_bf16 v[152:155], a[228:231], a[188:191], v[152:155]\n\t"
"ds_read_b128 a[116:119], v252 offset:12288\n\t"
"v_mfma_f32_16x16x32_bf16 v[152:155], v[240:243], a[192:195], v[152:155]\n\t"
"ds_read_b128 a[124:127], v252 offset:13312\n\t"
"v_mfma_f32_16x16x32_bf16 v[152:155], a[140:143], a[196:199], v[152:155]\n\t"
"v_mfma_f32_16x16x32_bf16 v[156:159], a[140:143], a[200:203], v[156:159]\n\t"
"buffer_load_dwordx4 a[120:123], off, s[80:83], s20 offset:1024 sc1\n\t"
"v_mfma_f32_16x16x32_bf16 v[156:159], v[240:243], a[196:199], v[156:159]\n\t"
"ds_read_b128 a[156:159], v252 offset:14336\n\t"
"v_mfma_f32_16x16x32_bf16 v[156:159], a[228:231], a[192:195], v[156:159]\n\t"
"v_mfma_f32_16x16x32_bf16 v[160:163], a[228:231], a[196:199], v[160:163]\n\t"
"s_waitcnt vmcnt(27)\n\t"
"v_mfma_f32_16x16x32_bf16 v[160:163], v[240:243], a[200:203], v[160:163]\n\t"
"buffer_load_dwordx4 a[128:131], off, s[80:83], s20 offset:2048 sc1\n\t"
"v_mfma_f32_16x16x32_bf16 v[160:163], a[140:143], a[204:207], v[160:163]\n\t"
"s_mov_b32 s20, 1179648\n\t"
"v_mfma_f32_16x16x32_bf16 v[164:167], a[148:151], a[168:171], v[164:167]\n\t"
"ds_read_b128 a[168:171], v255 offset:0\n\t"
"v_mfma_f32_16x16x32_bf16 v[164:167], a[232:235], a[176:179], v[164:167]\n\t"
"v_mfma_f32_16x16x32_bf16 v[164:167], a[208:211], a[172:175], v[164:167]\n\t"
"buffer_load_dwordx4 a[140:143], off, s[80:83], s20 offset:0 sc1\n\t"
"v_mfma_f32_16x16x32_bf16 v[168:171], a[148:151], a[172:175], v[168:171]\n\t"
"ds_read_b128 a[172:175], v255 offset:3072\n\t"
"v_mfma_f32_16x16x32_bf16 v[168:171], a[208:211], a[176:179], v[168:171]\n\t"
"v_mfma_f32_16x16x32_bf16 v[168:171], a[232:235], a[180:183], v[168:171]\n\t"
"v_mfma_f32_16x16x32_bf16 v[172:175], a[232:235], a[184:187], v[172:175]\n\t"
"buffer_load_dwordx4 v[208:211], off, s[80:83], s20 offset:1024 sc1\n\t"
"v_mfma_f32_16x16x32_bf16 v[172:175], a[208:211], a[180:183], v[172:175]\n\t"
"v_mfma_f32_16x16x32_bf16 v[172:175], a[148:151], a[176:179], v[172:175]\n\t"
"ds_read_b128 a[176:179], v255 offset:6144\n\t"
"v_mfma_f32_16x16x32_bf16 v[176:179], a[148:151], a[180:183], v[176:179]\n\t"
"buffer_load_dwordx4 v[216:219], off, s[80:83], s20 offset:2048 sc1\n\t"
"v_mfma_f32_16x16x32_bf16 v[176:179], a[208:211], a[184:187], v[176:179]\n\t"
"Lpg12_%=:\n\t"
"s_cbranch_vccnz Lvr0_%=\n\t"
"ds_read_b128 a[180:183], v255 offset:9216\n\t"
"v_mfma_f32_16x16x32_bf16 v[176:179], a[232:235], a[188:191], v[176:179]\n\t"
"s_waitcnt lgkmcnt(4)\n\t"
"s_barrier\n\t"
"v_mfma_f32_16x16x32_bf16 v[180:183], a[232:235], a[192:195], v[180:183]\n\t"
"s_mov_b32 s20, 1191936\n\t"
"v_mfma_f32_16x16x32_bf16 v[180:183], a[208:211], a[188:191], v[180:183]\n\t"
"buffer_load_dwordx4 a[4:7], off, s[80:83], s20 offset:0 sc1\n\t"
"v_mfma_f32_16x16x32_bf16 v[180:183], a[148:151], a[184:187], v[180:183]\n\t"
"ds_read_b128 a[184:187], v255 offset:12288\n\t"
"v_mfma_f32_16x16x32_bf16 v[184:187], a[148:151], a[188:191], v[184:187]\n\t"
"buffer_load_dwordx4 a[12:15], off, s[80:83], s20 offset:1024 sc1\n\t"
"v_mfma_f32_16x16x32_bf16 v[184:187], a[208:211], a[192:195], v[184:187]\n\t"
"ds_read_b128 a[188:191], v255 offset:15360\n\t"
"v_mfma_f32_16x16x32_bf16 v[184:187], a[232:235], a[196:199], v[184:187]\n\t"
"ds_write_b128 v249, a[16:19] offset:12288\n\t"
"v_mfma_f32_16x16x32_bf16 v[188:191], a[232:235], a[200:203], v[188:191]\n\t"
"v_mfma_f32_16x16x32_bf16 v[188:191], a[208:211], a[196:199], v[188:191]\n\t"
"buffer_load_dwordx4 a[212:215], off, s[80:83], s20 offset:2048 sc1\n\t"
"v_mfma_f32_16x16x32_bf16 v[188:191], a[148:151], a[192:195], v[188:191]\n\t"
"s_mov_b32 s20, 1204224\n\t"
"ds_read_b128 a[192:195], v255 offset:18432\n\t"
"v_mfma_f32_16x16x32_bf16 v[192:195], a[148:151], a[196:199], v[192:195]\n\t"
"s_waitcnt vmcnt(31) lgkmcnt(5)\n\t"
"ds_read_b128 a[196:199], v255 offset:21504\n\t"
"v_mfma_f32_16x16x32_bf16 v[192:195], a[208:211], a[200:203], v[192:195]\n\t"
"ds_read_b128 a[200:203], v255 offset:24576\n\t"
"v_mfma_f32_16x16x32_bf16 v[192:195], a[232:235], a[204:207], v[192:195]\n\t"
"buffer_load_dwordx4 a[148:151], off, s[80:83], s20 offset:0 sc1\n\t"
"Lst16_%=:\n\t"
"v_mfma_f32_16x16x32_bf16 v[0:3], a[244:247], a[168:171], v[0:3]\n\t"
"s_waitcnt lgkmcnt(6)\n\t"
"ds_read_b128 a[204:207], v255 offset:27648\n\t"
"v_mfma_f32_16x16x32_bf16 v[0:3], a[248:251], a[176:179], v[0:3]\n\t"
"v_mfma_f32_16x16x32_bf16 v[0:3], a[164:167], a[172:175], v[0:3]\n\t"
"ds_write_b64 v250, a[144:145] offset:12288\n\t"
"v_mfma_f32_16x16x32_bf16 v[4:7], a[244:247], a[172:175], v[4:7]\n\t"
"buffer_load_dwordx4 a[208:211], off, s[80:83], s20 offset:1024 sc1\n\t"
"v_mfma_f32_16x16x32_bf16 v[4:7], a[164:167], a[176:179], v[4:7]\n\t"
"s_waitcnt lgkmcnt(7)\n\t"
"v_mfma_f32_16x16x32_bf16 v[4:7], a[248:251], a[180:183], v[4:7]\n\t"
"ds_write_b128 v249, a[20:23] offset:18432\n\t"
"v_mfma_f32_16x16x32_bf16 v[8:11], a[248:251], a[184:187], v[8:11]\n\t"
"s_waitcnt lgkmcnt(7)\n\t"
"v_mfma_f32_16x16x32_bf16 v[8:11], a[164:167], a[180:183], v[8:11]\n\t"
"buffer_load_dwordx4 a[216:219], off, s[80:83], s20 offset:2048 sc1\n\t"
"v_mfma_f32_16x16x32_bf16 v[8:11], a[244:247], a[176:179], v[8:11]\n\t"
"s_mov_b32 s20, 1363968\n\t"
"ds_write_b64 v250, a[146:147] offset:18432\n\t"
"v_mfma_f32_16x16x32_bf16 v[12:15], a[244:247], a[180:183], v[12:15]\n\t"
"s_waitcnt lgkmcnt(6)\n\t"
"v_mfma_f32_16x16x32_bf16 v[12:15], a[164:167], a[184:187], v[12:15]\n\t"
"v_mfma_f32_16x16x32_bf16 v[12:15], a[248:251], a[188:191], v[12:15]\n\t"
"buffer_load_dwordx4 a[224:227], off, s[80:83], s20 offset:0 sc1\n\t"
"v_mfma_f32_16x16x32_bf16 v[16:19], a[248:251], a[192:195], v[16:19]\n\t"
"s_waitcnt lgkmcnt(5)\n\t"
"v_mfma_f32_16x16x32_bf16 v[16:19], a[164:167], a[188:191], v[16:19]\n\t"
"v_mfma_f32_16x16x32_bf16 v[16:19], a[244:247], a[184:187], v[16:19]\n\t"
"ds_read_b128 a[52:55], v252 offset:0\n\t"
"v_mfma_f32_16x16x32_bf16 v[20:23], a[244:247], a[188:191], v[20:23]\n\t"
"buffer_load_dwordx4 a[228:231], off, s[80:83], s20 offset:1024 sc1\n\t"
"v_mfma_f32_16x16x32_bf16 v[20:23], a[164:167], a[192:195], v[20:23]\n\t"
"s_waitcnt lgkmcnt(5)\n\t"
"ds_read_b128 a[84:87], v252 offset:1024\n\t"
"v_mfma_f32_16x16x32_bf16 v[20:23], a[248:251], a[196:199], v[20:23]\n\t"
"v_mfma_f32_16x16x32_bf16 v[24:27], a[248:251], a[200:203], v[24:27]\n\t"
"s_waitcnt lgkmcnt(5)\n\t"
"ds_read_b128 v[196:199], v252 offset:2048\n\t"
"v_mfma_f32_16x16x32_bf16 v[24:27], a[164:167], a[196:199], v[24:27]\n\t"
"buffer_load_dwordx4 a[232:235], off, s[80:83], s20 offset:2048 sc1\n\t"
"v_mfma_f32_16x16x32_bf16 v[24:27], a[244:247], a[192:195], v[24:27]\n\t"
"s_mov_b32 s20, 1376256\n\t"
"v_mfma_f32_16x16x32_bf16 v[28:31], a[244:247], a[196:199], v[28:31]\n\t"
"s_waitcnt vmcnt(33)\n\t"
"v_mfma_f32_16x16x32_bf16 v[28:31], a[164:167], a[200:203], v[28:31]\n\t"
"v_mfma_f32_16x16x32_bf16 v[28:31], a[248:251], a[204:207], v[28:31]\n\t"
"buffer_load_dwordx4 a[236:239], off, s[80:83], s20 offset:0 sc1\n\t"
"v_mfma_f32_16x16x32_bf16 v[32:35], v[236:239], a[168:171], v[32:35]\n\t"
"v_mfma_f32_16x16x32_bf16 v[32:35], a[220:223], a[176:179], v[32:35]\n\t"
"v_mfma_f32_16x16x32_bf16 v[32:35], v[96:99], a[172:175], v[32:35]\n\t"
"buffer_load_dwordx4 a[240:243], off, s[80:83], s20 offset:1024 sc1\n\t"
"v_mfma_f32_16x16x32_bf16 v[36:39], v[236:239], a[172:175], v[36:39]\n\t"
"v_mfma_f32_16x16x32_bf16 v[36:39], v[96:99], a[176:179], v[36:39]\n\t"
"v_mfma_f32_16x16x32_bf16 v[36:39], a[220:223], a[180:183], v[36:39]\n\t"
"v_mfma_f32_16x16x32_bf16 v[40:43], a[220:223], a[184:187], v[40:43]\n\t"
"buffer_load_dwordx4 a[244:247], off, s[80:83], s20 offset:2048 sc1\n\t"
"v_mfma_f32_16x16x32_bf16 v[40:43], v[96:99], a[180:183], v[40:43]\n\t"
"s_mov_b32 s20, 1388544\n\t"
"v_mfma_f32_16x16x32_bf16 v[40:43], v[236:239], a[176:179], v[40:43]\n\t"
"v_mfma_f32_16x16x32_bf16 v[44:47], v[236:239], a[180:183], v[44:47]\n\t"
"v_mfma_f32_16x16x32_bf16 v[44:47], v[96:99], a[184:187], v[44:47]\n\t"
"v_mfma_f32_16x16x32_bf16 v[44:47], a[220:223], a[188:191], v[44:47]\n\t"
"s_waitcnt lgkmcnt(0)\n\t"
"s_barrier\n\t"
"v_mfma_f32_16x16x32_bf16 v[48:51], a[220:223], a[192:195], v[48:51]\n\t"
"v_mfma_f32_16x16x32_bf16 v[48:51], v[96:99], a[188:191], v[48:51]\n\t"
"buffer_load_dwordx4 a[248:251], off, s[80:83], s20 offset:0 sc1\n\t"
"v_mfma_f32_16x16x32_bf16 v[48:51], v[236:239], a[184:187], v[48:51]\n\t"
"buffer_load_dwordx4 a[144:147], off, s[80:83], s20 offset:1024 sc1\n\t"
"v_mfma_f32_16x16x32_bf16 v[52:55], v[236:239], a[188:191], v[52:55]\n\t"
"v_mfma_f32_16x16x32_bf16 v[52:55], v[96:99], a[192:195], v[52:55]\n\t"
"buffer_load_dwordx4 a[20:23], off, s[80:83], s20 offset:2048 sc1\n\t"
"v_mfma_f32_16x16x32_bf16 v[52:55], a[220:223], a[196:199], v[52:55]\n\t"
"s_mov_b32 s20, 0\n\t"
"ds_write_b128 v249, a[24:27] offset:0\n\t"
"v_mfma_f32_16x16x32_bf16 v[56:59], a[220:223], a[200:203], v[56:59]\n\t"
"v_mfma_f32_16x16x32_bf16 v[56:59], v[96:99], a[196:199], v[56:59]\n\t"
"v_mfma_f32_16x16x32_bf16 v[56:59], v[236:239], a[192:195], v[56:59]\n\t"
"buffer_load_dwordx4 a[252:255], off, s[80:83], s20 offset:0 sc1\n\t"
"v_mfma_f32_16x16x32_bf16 v[60:63], v[236:239], a[196:199], v[60:63]\n\t"
"s_waitcnt vmcnt(28)\n\t"
"v_mfma_f32_16x16x32_bf16 v[60:63], v[96:99], a[200:203], v[60:63]\n\t"
"v_mfma_f32_16x16x32_bf16 v[60:63], a[220:223], a[204:207], v[60:63]\n\t"
"ds_write_b64 v250, a[152:153] offset:0\n\t"
"v_mfma_f32_16x16x32_bf16 v[64:67], a[104:107], a[168:171], v[64:67]\n\t"
"buffer_load_dwordx4 v[96:99], off, s[80:83], s20 offset:1024 sc1\n\t"
"v_mfma_f32_16x16x32_bf16 v[64:67], a[44:47], a[176:179], v[64:67]\n\t"
"ds_read_b128 a[168:171], v255 offset:1536\n\t"
"v_mfma_f32_16x16x32_bf16 v[64:67], a[112:115], a[172:175], v[64:67]\n\t"
"ds_write_b128 v249, a[28:31] offset:6144\n\t"
"v_mfma_f32_16x16x32_bf16 v[68:71], a[104:107], a[172:175], v[68:71]\n\t"
"v_mfma_f32_16x16x32_bf16 v[68:71], a[112:115], a[176:179], v[68:71]\n\t"
"buffer_load_dwordx4 a[220:223], off, s[80:83], s20 offset:2048 sc1\n\t"
"v_mfma_f32_16x16x32_bf16 v[68:71], a[44:47], a[180:183], v[68:71]\n\t"
"ds_read_b128 a[172:175], v255 offset:4608\n\t"
"v_mfma_f32_16x16x32_bf16 v[72:75], a[44:47], a[184:187], v[72:75]\n\t"
"v_mfma_f32_16x16x32_bf16 v[72:75], a[112:115], a[180:183], v[72:75]\n\t"
"v_mfma_f32_16x16x32_bf16 v[72:75], a[104:107], a[176:179], v[72:75]\n\t"
"ds_read_b128 a[176:179], v255 offset:7680\n\t"
"v_mfma_f32_16x16x32_bf16 v[76:79], a[104:107], a[180:183], v[76:79]\n\t"
"ds_read_b128 a[180:183], v255 offset:10752\n\t"
"v_mfma_f32_16x16x32_bf16 v[76:79], a[112:115], a[184:187], v[76:79]\n\t"
"ds_read_b128 v[204:207], v252 offset:12288\n\t"
"v_mfma_f32_16x16x32_bf16 v[76:79], a[44:47], a[188:191], v[76:79]\n\t"
"ds_read_b128 v[212:215], v252 offset:13312\n\t"
"v_mfma_f32_16x16x32_bf16 v[80:83], a[44:47], a[192:195], v[80:83]\n\t"
"ds_read_b128 a[0:3], v252 offset:14336\n\t"
"v_mfma_f32_16x16x32_bf16 v[80:83], a[112:115], a[188:191], v[80:83]\n\t"
"v_mfma_f32_16x16x32_bf16 v[80:83], a[104:107], a[184:187], v[80:83]\n\t"
"ds_read_b128 a[184:187], v255 offset:13824\n\t"
"v_mfma_f32_16x16x32_bf16 v[84:87], a[104:107], a[188:191], v[84:87]\n\t"
"ds_read_b128 a[188:191], v255 offset:16896\n\t"
"v_mfma_f32_16x16x32_bf16 v[84:87], a[112:115], a[192:195], v[84:87]\n\t"
"v_mfma_f32_16x16x32_bf16 v[84:87], a[44:47], a[196:199], v[84:87]\n\t"
"ds_write_b64 v250, a[154:155] offset:6144\n\t"
"v_mfma_f32_16x16x32_bf16 v[88:91], a[44:47], a[200:203], v[88:91]\n\t"
"s_add_u32 s29, s22, 384\n\t"
"v_mfma_f32_16x16x32_bf16 v[88:91], a[112:115], a[196:199], v[88:91]\n\t"
"v_mfma_f32_16x16x32_bf16 v[88:91], a[104:107], a[192:195], v[88:91]\n\t"
"ds_read_b128 a[192:195], v255 offset:19968\n\t"
"v_mfma_f32_16x16x32_bf16 v[92:95], a[104:107], a[196:199], v[92:95]\n\t"
"s_waitcnt vmcnt(27) lgkmcnt(8)\n\t"
"ds_read_b128 a[196:199], v255 offset:23040\n\t"
"v_mfma_f32_16x16x32_bf16 v[92:95], a[112:115], a[200:203], v[92:95]\n\t"
"ds_read_b128 a[200:203], v255 offset:26112\n\t"
"v_mfma_f32_16x16x32_bf16 v[92:95], a[44:47], a[204:207], v[92:95]\n\t"
"ds_read_b128 a[204:207], v255 offset:29184\n\t"
"v_mfma_f32_16x16x32_bf16 v[100:103], a[60:63], a[168:171], v[100:103]\n\t"
"s_waitcnt lgkmcnt(10)\n\t"
"v_mfma_f32_16x16x32_bf16 v[100:103], a[76:79], a[176:179], v[100:103]\n\t"
"s_mov_b32 s20, 12288\n\t"
"v_mfma_f32_16x16x32_bf16 v[100:103], a[68:71], a[172:175], v[100:103]\n\t"
"buffer_store_dwordx4 a[40:43], v248, s[16:19], s29 offen offset:0 nt\n\t"
"v_mfma_f32_16x16x32_bf16 v[104:107], a[60:63], a[172:175], v[104:107]\n\t"
"s_waitcnt lgkmcnt(6)\n\t"
"v_mfma_f32_16x16x32_bf16 v[104:107], a[68:71], a[176:179], v[104:107]\n\t"
"v_mfma_f32_16x16x32_bf16 v[104:107], a[76:79], a[180:183], v[104:107]\n\t"
"s_waitcnt lgkmcnt(0)\n\t"
"s_barrier\n\t"
"v_mfma_f32_16x16x32_bf16 v[108:111], a[76:79], a[184:187], v[108:111]\n\t"
"v_mfma_f32_16x16x32_bf16 v[108:111], a[68:71], a[180:183], v[108:111]\n\t"
"buffer_load_dwordx4 a[152:155], off, s[80:83], s20 offset:0 sc1\n\t"
"v_mfma_f32_16x16x32_bf16 v[108:111], a[60:63], a[176:179], v[108:111]\n\t"
"ds_write_b128 v249, a[32:35] offset:12288\n\t"
"v_mfma_f32_16x16x32_bf16 v[112:115], a[60:63], a[180:183], v[112:115]\n\t"
"v_mfma_f32_16x16x32_bf16 v[112:115], a[68:71], a[184:187], v[112:115]\n\t"
"buffer_load_dwordx4 a[28:31], off, s[80:83], s20 offset:1024 sc1\n\t"
"v_mfma_f32_16x16x32_bf16 v[112:115], a[76:79], a[188:191], v[112:115]\n\t"
"ds_write_b64 v250, a[160:161] offset:12288\n\t"
"v_mfma_f32_16x16x32_bf16 v[116:119], a[76:79], a[192:195], v[116:119]\n\t"
"v_mfma_f32_16x16x32_bf16 v[116:119], a[68:71], a[188:191], v[116:119]\n\t"
"v_mfma_f32_16x16x32_bf16 v[116:119], a[60:63], a[184:187], v[116:119]\n\t"
"buffer_store_dwordx4 a[48:51], v248, s[16:19], s29 offen offset:128 nt\n\t"
"ds_write_b64 v250, a[162:163] offset:18432\n\t"
"v_mfma_f32_16x16x32_bf16 v[120:123], a[60:63], a[188:191], v[120:123]\n\t"
"v_mfma_f32_16x16x32_bf16 v[120:123], a[68:71], a[192:195], v[120:123]\n\t"
"v_mfma_f32_16x16x32_bf16 v[120:123], a[76:79], a[196:199], v[120:123]\n\t"
"ds_write_b128 v249, a[36:39] offset:18432\n\t"
"v_mfma_f32_16x16x32_bf16 v[124:127], a[76:79], a[200:203], v[124:127]\n\t"
"v_mfma_f32_16x16x32_bf16 v[124:127], a[68:71], a[196:199], v[124:127]\n\t"
"v_mfma_f32_16x16x32_bf16 v[124:127], a[60:63], a[192:195], v[124:127]\n\t"
"v_mfma_f32_16x16x32_bf16 v[128:131], a[60:63], a[196:199], v[128:131]\n\t"
"s_waitcnt vmcnt(28)\n\t"
"v_mfma_f32_16x16x32_bf16 v[128:131], a[68:71], a[200:203], v[128:131]\n\t"
"v_mfma_f32_16x16x32_bf16 v[128:131], a[76:79], a[204:207], v[128:131]\n\t"
"v_mfma_f32_16x16x32_bf16 v[132:135], a[132:135], a[168:171], v[132:135]\n\t"
"v_mfma_f32_16x16x32_bf16 v[132:135], a[100:103], a[176:179], v[132:135]\n\t"
"v_mfma_f32_16x16x32_bf16 v[132:135], a[92:95], a[172:175], v[132:135]\n\t"
"buffer_store_dwordx4 a[56:59], v248, s[16:19], s29 offen offset:256 nt\n\t"
"v_mfma_f32_16x16x32_bf16 v[136:139], a[132:135], a[172:175], v[136:139]\n\t"
"v_mfma_f32_16x16x32_bf16 v[136:139], a[92:95], a[176:179], v[136:139]\n\t"
"v_add_u32 v255, 55296, v255\n\t"
"v_mfma_f32_16x16x32_bf16 v[136:139], a[100:103], a[180:183], v[136:139]\n\t"
"v_mfma_f32_16x16x32_bf16 v[140:143], a[100:103], a[184:187], v[140:143]\n\t"
"v_mfma_f32_16x16x32_bf16 v[140:143], a[92:95], a[180:183], v[140:143]\n\t"
"v_mfma_f32_16x16x32_bf16 v[140:143], a[132:135], a[176:179], v[140:143]\n\t"
"s_add_u32 s29, s22, 196992\n\t"
"v_mfma_f32_16x16x32_bf16 v[144:147], a[132:135], a[180:183], v[144:147]\n\t"
"v_mfma_f32_16x16x32_bf16 v[144:147], a[92:95], a[184:187], v[144:147]\n\t"
"v_mfma_f32_16x16x32_bf16 v[144:147], a[100:103], a[188:191], v[144:147]\n\t"
"v_mfma_f32_16x16x32_bf16 v[148:151], a[100:103], a[192:195], v[148:151]\n\t"
"v_mfma_f32_16x16x32_bf16 v[148:151], a[92:95], a[188:191], v[148:151]\n\t"
"v_mfma_f32_16x16x32_bf16 v[148:151], a[132:135], a[184:187], v[148:151]\n\t"
"buffer_store_dwordx4 a[64:67], v248, s[16:19], s29 offen offset:0 nt\n\t"
"ds_read_b128 a[8:11], v252 offset:0\n\t"
"v_mfma_f32_16x16x32_bf16 v[152:155], a[132:135], a[188:191], v[152:155]\n\t"
"v_mfma_f32_16x16x32_bf16 v[152:155], a[92:95], a[192:195], v[152:155]\n\t"
"ds_read_b128 a[16:19], v252 offset:1024\n\t"
"v_mfma_f32_16x16x32_bf16 v[152:155], a[100:103], a[196:199], v[152:155]\n\t"
"v_mfma_f32_16x16x32_bf16 v[156:159], a[100:103], a[200:203], v[156:159]\n\t"
"ds_read_b128 a[24:27], v252 offset:2048\n\t"
"v_mfma_f32_16x16x32_bf16 v[156:159], a[92:95], a[196:199], v[156:159]\n\t"
"v_mfma_f32_16x16x32_bf16 v[156:159], a[132:135], a[192:195], v[156:159]\n\t"
"v_mfma_f32_16x16x32_bf16 v[160:163], a[132:135], a[196:199], v[160:163]\n\t"
"s_waitcnt vmcnt(27)\n\t"
"v_mfma_f32_16x16x32_bf16 v[160:163], a[92:95], a[200:203], v[160:163]\n\t"
"global_load_dwordx4 v[240:243], v252, s[48:49] offset:2048\n\t"
"v_mfma_f32_16x16x32_bf16 v[160:163], a[100:103], a[204:207], v[160:163]\n\t"
"v_mfma_f32_16x16x32_bf16 v[164:167], v[200:203], a[168:171], v[164:167]\n\t"
"ds_read_b128 a[168:171], v255 offset:0\n\t"
"v_mfma_f32_16x16x32_bf16 v[164:167], a[128:131], a[176:179], v[164:167]\n\t"
"v_mfma_f32_16x16x32_bf16 v[164:167], a[120:123], a[172:175], v[164:167]\n\t"
"buffer_store_dwordx4 a[72:75], v248, s[16:19], s29 offen offset:128 nt\n\t"
"v_mfma_f32_16x16x32_bf16 v[168:171], v[200:203], a[172:175], v[168:171]\n\t"
"ds_read_b128 a[172:175], v255 offset:3072\n\t"
"v_mfma_f32_16x16x32_bf16 v[168:171], a[120:123], a[176:179], v[168:171]\n\t"
"global_load_dwordx4 a[136:139], v252, s[46:47] offset:1024\n\t"
"v_mfma_f32_16x16x32_bf16 v[168:171], a[128:131], a[180:183], v[168:171]\n\t"
"v_mfma_f32_16x16x32_bf16 v[172:175], a[128:131], a[184:187], v[172:175]\n\t"
"v_mfma_f32_16x16x32_bf16 v[172:175], a[120:123], a[180:183], v[172:175]\n\t"
"v_mfma_f32_16x16x32_bf16 v[172:175], v[200:203], a[176:179], v[172:175]\n\t"
"ds_read_b128 a[176:179], v255 offset:6144\n\t"
"v_mfma_f32_16x16x32_bf16 v[176:179], v[200:203], a[180:183], v[176:179]\n\t"
"ds_read_b128 a[180:183], v255 offset:9216\n\t"
"v_mfma_f32_16x16x32_bf16 v[176:179], a[120:123], a[184:187], v[176:179]\n\t"
"v_mfma_f32_16x16x32_bf16 v[176:179], a[128:131], a[188:191], v[176:179]\n\t"
"s_waitcnt lgkmcnt(4)\n\t"
"s_barrier\n\t"
"v_mfma_f32_16x16x32_bf16 v[180:183], a[128:131], a[192:195], v[180:183]\n\t"
"v_mfma_f32_16x16x32_bf16 v[180:183], a[120:123], a[188:191], v[180:183]\n\t"
"buffer_load_dwordx4 a[160:163], off, s[80:83], s20 offset:2048 sc1\n\t"
"v_mfma_f32_16x16x32_bf16 v[180:183], v[200:203], a[184:187], v[180:183]\n\t"
"s_mov_b32 s20, 24576\n\t"
"ds_read_b128 a[184:187], v255 offset:12288\n\t"
"v_mfma_f32_16x16x32_bf16 v[184:187], v[200:203], a[188:191], v[184:187]\n\t"
"ds_read_b128 a[188:191], v255 offset:15360\n\t"
"v_mfma_f32_16x16x32_bf16 v[184:187], a[120:123], a[192:195], v[184:187]\n\t"
"buffer_load_dwordx4 a[36:39], off, s[80:83], s20 offset:0 sc1\n\t"
"v_mfma_f32_16x16x32_bf16 v[184:187], a[128:131], a[196:199], v[184:187]\n\t"
"buffer_store_dwordx4 a[80:83], v248, s[16:19], s29 offen offset:256 nt\n\t"
"v_mfma_f32_16x16x32_bf16 v[188:191], a[128:131], a[200:203], v[188:191]\n\t"
"v_mfma_f32_16x16x32_bf16 v[188:191], a[120:123], a[196:199], v[188:191]\n\t"
"v_mfma_f32_16x16x32_bf16 v[188:191], v[200:203], a[192:195], v[188:191]\n\t"
"ds_read_b128 a[192:195], v255 offset:18432\n\t"
"v_mfma_f32_16x16x32_bf16 v[192:195], v[200:203], a[196:199], v[192:195]\n\t"
"buffer_load_dwordx4 v[200:203], off, s[80:83], s20 offset:1024 sc1\n\t"
"v_mfma_f32_16x16x32_bf16 v[192:195], a[120:123], a[200:203], v[192:195]\n\t"
"s_waitcnt vmcnt(31) lgkmcnt(4)\n\t"
"ds_read_b128 a[196:199], v255 offset:21504\n\t"
"s_add_u32 s29, s22, 393600\n\t"
"v_mfma_f32_16x16x32_bf16 v[192:195], a[128:131], a[204:207], v[192:195]\n\t"
"ds_read_b128 a[200:203], v255 offset:24576\n\t"
"Lst17_%=:\n\t"
"v_mfma_f32_16x16x32_bf16 v[0:3], a[140:143], a[168:171], v[0:3]\n\t"
"buffer_load_dwordx4 a[120:123], off, s[80:83], s20 offset:2048 sc1\n\t"
"v_mfma_f32_16x16x32_bf16 v[0:3], v[216:219], a[176:179], v[0:3]\n\t"
"s_waitcnt lgkmcnt(5)\n\t"
"ds_read_b128 a[204:207], v255 offset:27648\n\t"
"s_mov_b32 s20, 184320\n\t"
"v_mfma_f32_16x16x32_bf16 v[0:3], v[208:211], a[172:175], v[0:3]\n\t"
"v_mfma_f32_16x16x32_bf16 v[4:7], a[140:143], a[172:175], v[4:7]\n\t"
"buffer_load_dwordx4 a[128:131], off, s[80:83], s20 offset:0 sc1\n\t"
"v_mfma_f32_16x16x32_bf16 v[4:7], v[208:211], a[176:179], v[4:7]\n\t"
"s_waitcnt lgkmcnt(5)\n\t"
"s_waitcnt vmcnt(8)\n\t"
"v_add_u32 v240, 59904, v240\n\t"
"v_mfma_f32_16x16x32_bf16 v[4:7], v[216:219], a[180:183], v[4:7]\n\t"
"v_add_u32 v241, 59904, v241\n\t"
"v_mfma_f32_16x16x32_bf16 v[8:11], v[216:219], a[184:187], v[8:11]\n\t"
"s_waitcnt lgkmcnt(4)\n\t"
"v_cvt_pk_bf16_f32 v0, v0, v1\n\t"
"v_cvt_pk_bf16_f32 v2, v2, v3\n\t"
"v_mfma_f32_16x16x32_bf16 v[8:11], v[208:211], a[180:183], v[8:11]\n\t"
"v_accvgpr_write_b32 a40, v0\n\t"
"v_accvgpr_write_b32 a41, v2\n\t"
"v_mfma_f32_16x16x32_bf16 v[8:11], a[140:143], a[176:179], v[8:11]\n\t"
"ds_read_b64_tr_b16 v[236:237], v240 offset:20480\n\t"
"v_mfma_f32_16x16x32_bf16 v[12:15], a[140:143], a[180:183], v[12:15]\n\t"
"s_waitcnt lgkmcnt(4)\n\t"
"ds_read_b64_tr_b16 v[238:239], v240 offset:22016\n\t"
"v_cvt_pk_bf16_f32 v4, v4, v5\n\t"
"v_mfma_f32_16x16x32_bf16 v[12:15], v[208:211], a[184:187], v[12:15]\n\t"
"ds_read_b64_tr_b16 v[228:229], v241 offset:20480\n\t"
"v_cvt_pk_bf16_f32 v6, v6, v7\n\t"
"v_mfma_f32_16x16x32_bf16 v[12:15], v[216:219], a[188:191], v[12:15]\n\t"
"buffer_store_dwordx4 a[88:91], v248, s[16:19], s29 offen offset:0 nt\n\t"
"ds_read_b64_tr_b16 v[230:231], v241 offset:22016\n\t"
"ds_read_b64_tr_b16 v[232:233], v240 offset:40448\n\t"
"v_accvgpr_write_b32 a48, v4\n\t"
"v_mfma_f32_16x16x32_bf16 v[16:19], v[216:219], a[192:195], v[16:19]\n\t"
"s_waitcnt lgkmcnt(7)\n\t"
"ds_read_b64_tr_b16 v[234:235], v240 offset:41984\n\t"
"v_cvt_pk_bf16_f32 v8, v8, v9\n\t"
"v_mfma_f32_16x16x32_bf16 v[16:19], v[208:211], a[188:191], v[16:19]\n\t"
"ds_read_b64_tr_b16 v[224:225], v241 offset:40448\n\t"
"v_cvt_pk_bf16_f32 v10, v10, v11\n\t"
"v_mfma_f32_16x16x32_bf16 v[16:19], a[140:143], a[184:187], v[16:19]\n\t"
"ds_read_b64_tr_b16 v[226:227], v241 offset:41984\n\t"
"v_accvgpr_write_b32 a49, v6\n\t"
"v_mfma_f32_16x16x32_bf16 v[20:23], a[140:143], a[188:191], v[20:23]\n\t"
"s_waitcnt lgkmcnt(9)\n\t"
"v_cvt_pk_bf16_f32 v12, v12, v13\n\t"
"v_cvt_pk_bf16_f32 v14, v14, v15\n\t"
"v_mfma_f32_16x16x32_bf16 v[20:23], v[208:211], a[192:195], v[20:23]\n\t"
"v_accvgpr_write_b32 a56, v8\n\t"
"v_accvgpr_write_b32 a57, v10\n\t"
"v_mfma_f32_16x16x32_bf16 v[20:23], v[216:219], a[196:199], v[20:23]\n\t"
"v_accvgpr_write_b32 a64, v12\n\t"
"v_accvgpr_write_b32 a65, v14\n\t"
"v_mfma_f32_16x16x32_bf16 v[24:27], v[216:219], a[200:203], v[24:27]\n\t"
"s_waitcnt lgkmcnt(8)\n\t"
"ds_read_b128 a[32:35], v252 offset:12288\n\t"
"v_cvt_pk_bf16_f32 v16, v16, v17\n\t"
"v_mfma_f32_16x16x32_bf16 v[24:27], v[208:211], a[196:199], v[24:27]\n\t"
"v_cvt_pk_bf16_f32 v18, v18, v19\n\t"
"v_accvgpr_write_b32 a72, v16\n\t"
"v_mfma_f32_16x16x32_bf16 v[24:27], a[140:143], a[192:195], v[24:27]\n\t"
"ds_read_b128 a[104:107], v252 offset:13312\n\t"
"v_accvgpr_write_b32 a73, v18\n\t"
"v_mfma_f32_16x16x32_bf16 v[28:31], a[140:143], a[196:199], v[28:31]\n\t"
"s_waitcnt vmcnt(7) lgkmcnt(8)\n\t"
"v_cvt_pk_bf16_f32 v20, v20, v21\n\t"
"v_cvt_pk_bf16_f32 v22, v22, v23\n\t"
"v_mfma_f32_16x16x32_bf16 v[28:31], v[208:211], a[200:203], v[28:31]\n\t"
"s_waitcnt lgkmcnt(6)\n\t"
"ds_read_b128 a[112:115], v252 offset:14336\n\t"
"v_accvgpr_write_b32 a80, v20\n\t"
"v_mfma_f32_16x16x32_bf16 v[28:31], v[216:219], a[204:207], v[28:31]\n\t"
"buffer_store_dwordx4 a[96:99], v248, s[16:19], s29 offen offset:128 nt\n\t"
"buffer_load_dwordx4 v[208:211], off, s[80:83], s20 offset:1024 sc1\n\t"
"v_accvgpr_write_b32 a81, v22\n\t"
"v_mfma_f32_16x16x32_bf16 v[236:239], v[236:239], a[136:139], 0\n\t"
"s_waitcnt lgkmcnt(3)\n\t"
"v_cvt_pk_bf16_f32 v24, v24, v25\n\t"
"v_cvt_pk_bf16_f32 v26, v26, v27\n\t"
"v_mfma_f32_16x16x32_bf16 v[228:231], v[228:231], a[136:139], 0\n\t"
"v_accvgpr_write_b32 a88, v24\n\t"
"v_accvgpr_write_b32 a89, v26\n\t"
"v_mfma_f32_16x16x32_bf16 v[232:235], v[232:235], a[136:139], 0\n\t"
"v_mfma_f32_16x16x32_bf16 v[224:227], v[224:227], a[136:139], 0\n\t"
"buffer_load_dwordx4 v[216:219], off, s[80:83], s20 offset:2048 sc1\n\t"
"v_mfma_f32_16x16x32_bf16 v[32:35], a[4:7], a[168:171], v[32:35]\n\t"
"v_cvt_pk_bf16_f32 v28, v28, v29\n\t"
"v_cvt_pk_bf16_f32 v30, v30, v31\n\t"
"v_mfma_f32_16x16x32_bf16 v[32:35], a[212:215], a[176:179], v[32:35]\n\t"
"v_cvt_pk_bf16_f32 v236, v236, v237\n\t"
"v_cvt_pk_bf16_f32 v237, v238, v239\n\t"
"v_mfma_f32_16x16x32_bf16 v[32:35], a[12:15], a[172:175], v[32:35]\n\t"
"v_cvt_pk_bf16_f32 v238, v228, v229\n\t"
"v_cvt_pk_bf16_f32 v239, v230, v231\n\t"
"v_mfma_f32_16x16x32_bf16 v[36:39], a[4:7], a[172:175], v[36:39]\n\t"
"v_cvt_pk_bf16_f32 v232, v232, v233\n\t"
"v_cvt_pk_bf16_f32 v233, v234, v235\n\t"
"v_mfma_f32_16x16x32_bf16 v[36:39], a[12:15], a[176:179], v[36:39]\n\t"
"v_cvt_pk_bf16_f32 v234, v224, v225\n\t"
"v_cvt_pk_bf16_f32 v235, v226, v227\n\t"
"v_mfma_f32_16x16x32_bf16 v[36:39], a[212:215], a[180:183], v[36:39]\n\t"
"ds_write_b128 v242, v[236:239] offset:30720\n\t"
"v_mfma_f32_16x16x32_bf16 v[40:43], a[212:215], a[184:187], v[40:43]\n\t"
"v_accvgpr_write_b32 a96, v28\n\t"
"v_accvgpr_write_b32 a97, v30\n\t"
"v_mfma_f32_16x16x32_bf16 v[40:43], a[12:15], a[180:183], v[40:43]\n\t"
"v_cvt_pk_bf16_f32 v32, v32, v33\n\t"
"v_cvt_pk_bf16_f32 v34, v34, v35\n\t"
"v_mfma_f32_16x16x32_bf16 v[40:43], a[4:7], a[176:179], v[40:43]\n\t"
"buffer_store_dwordx4 a[108:111], v248, s[16:19], s29 offen offset:256 nt\n\t"
"v_accvgpr_write_b32 a42, v32\n\t"
"v_accvgpr_write_b32 a43, v34\n\t"
"s_add_u32 s29, s22, 590208\n\t"
"v_mfma_f32_16x16x32_bf16 v[44:47], a[4:7], a[180:183], v[44:47]\n\t"
"v_cvt_pk_bf16_f32 v36, v36, v37\n\t"
"v_cvt_pk_bf16_f32 v38, v38, v39\n\t"
"v_mfma_f32_16x16x32_bf16 v[44:47], a[12:15], a[184:187], v[44:47]\n\t"
"v_accvgpr_write_b32 a50, v36\n\t"
"v_mfma_f32_16x16x32_bf16 v[44:47], a[212:215], a[188:191], v[44:47]\n\t"
"s_waitcnt lgkmcnt(1)\n\t"
"s_barrier\n\t"
"v_mfma_f32_16x16x32_bf16 v[48:51], a[212:215], a[192:195], v[48:51]\n\t"
"ds_read_b64_tr_b16 v[228:229], v240 offset:512\n\t"
"v_mfma_f32_16x16x32_bf16 v[48:51], a[12:15], a[188:191], v[48:51]\n\t"
"ds_read_b64_tr_b16 v[230:231], v240 offset:2048\n\t"
"v_cvt_pk_bf16_f32 v40, v40, v41\n\t"
"v_mfma_f32_16x16x32_bf16 v[48:51], a[4:7], a[184:187], v[48:51]\n\t"
"ds_write_b128 v242, v[232:235] offset:32256\n\t"
"v_mfma_f32_16x16x32_bf16 v[52:55], a[4:7], a[188:191], v[52:55]\n\t"
"s_mov_b32 s20, 196608\n\t"
"v_cvt_pk_bf16_f32 v42, v42, v43\n\t"
"v_accvgpr_write_b32 a51, v38\n\t"
"v_mfma_f32_16x16x32_bf16 v[52:55], a[12:15], a[192:195], v[52:55]\n\t"
"ds_read_b64_tr_b16 v[224:225], v241 offset:512\n\t"
"v_mfma_f32_16x16x32_bf16 v[52:55], a[212:215], a[196:199], v[52:55]\n\t"
"ds_read_b64_tr_b16 v[226:227], v241 offset:2048\n\t"
"v_cvt_pk_bf16_f32 v44, v44, v45\n\t"
"v_mfma_f32_16x16x32_bf16 v[56:59], a[212:215], a[200:203], v[56:59]\n\t"
"v_cvt_pk_bf16_f32 v46, v46, v47\n\t"
"v_accvgpr_write_b32 a58, v40\n\t"
"v_mfma_f32_16x16x32_bf16 v[56:59], a[12:15], a[196:199], v[56:59]\n\t"
"v_accvgpr_write_b32 a59, v42\n\t"
"v_cvt_pk_bf16_f32 v48, v48, v49\n\t"
"v_mfma_f32_16x16x32_bf16 v[56:59], a[4:7], a[192:195], v[56:59]\n\t"
"buffer_store_dwordx4 a[116:119], v248, s[16:19], s29 offen offset:0 nt\n\t"
"v_cvt_pk_bf16_f32 v50, v50, v51\n\t"
"v_accvgpr_write_b32 a66, v44\n\t"
"v_accvgpr_write_b32 a67, v46\n\t"
"v_accvgpr_write_b32 a74, v48\n\t"
"v_mfma_f32_16x16x32_bf16 v[60:63], a[4:7], a[196:199], v[60:63]\n\t"
"s_waitcnt lgkmcnt(3)\n\t"
"v_cvt_pk_bf16_f32 v52, v52, v53\n\t"
"v_cvt_pk_bf16_f32 v54, v54, v55\n\t"
"v_mfma_f32_16x16x32_bf16 v[60:63], a[12:15], a[200:203], v[60:63]\n\t"
"s_waitcnt lgkmcnt(0)\n\t"
"v_accvgpr_write_b32 a75, v50\n\t"
"v_accvgpr_write_b32 a82, v52\n\t"
"v_mfma_f32_16x16x32_bf16 v[60:63], a[212:215], a[204:207], v[60:63]\n\t"
"v_accvgpr_write_b32 a83, v54\n\t"
"v_mfma_f32_16x16x32_bf16 v[228:231], v[228:231], a[136:139], 0\n\t"
"buffer_load_dwordx4 a[4:7], off, s[80:83], s20 offset:0 sc1\n\t"
"v_mfma_f32_16x16x32_bf16 v[224:227], v[224:227], a[136:139], 0\n\t"
"buffer_load_dwordx4 a[12:15], off, s[80:83], s20 offset:1024 sc1\n\t"
"v_mfma_f32_16x16x32_bf16 v[64:67], a[148:151], a[168:171], v[64:67]\n\t"
"ds_read_b128 a[168:171], v255 offset:1536\n\t"
"v_cvt_pk_bf16_f32 v56, v56, v57\n\t"
"v_mfma_f32_16x16x32_bf16 v[64:67], a[216:219], a[176:179], v[64:67]\n\t"
"v_cvt_pk_bf16_f32 v58, v58, v59\n\t"
"v_cvt_pk_bf16_f32 v60, v60, v61\n\t"
"v_mfma_f32_16x16x32_bf16 v[64:67], a[208:211], a[172:175], v[64:67]\n\t"
"v_cvt_pk_bf16_f32 v62, v62, v63\n\t"
"v_accvgpr_write_b32 a90, v56\n\t"
"v_mfma_f32_16x16x32_bf16 v[68:71], a[148:151], a[172:175], v[68:71]\n\t"
"ds_read_b128 a[172:175], v255 offset:4608\n\t"
"v_cvt_pk_bf16_f32 v228, v228, v229\n\t"
"v_mfma_f32_16x16x32_bf16 v[68:71], a[208:211], a[176:179], v[68:71]\n\t"
"v_cvt_pk_bf16_f32 v229, v230, v231\n\t"
"v_cvt_pk_bf16_f32 v230, v224, v225\n\t"
"v_mfma_f32_16x16x32_bf16 v[68:71], a[216:219], a[180:183], v[68:71]\n\t"
"buffer_load_dwordx4 a[212:215], off, s[80:83], s20 offset:2048 sc1\n\t"
"v_mfma_f32_16x16x32_bf16 v[72:75], a[216:219], a[184:187], v[72:75]\n\t"
"v_cvt_pk_bf16_f32 v231, v226, v227\n\t"
"v_subrev_u32 v240, 59904, v240\n\t"
"v_mfma_f32_16x16x32_bf16 v[72:75], a[208:211], a[180:183], v[72:75]\n\t"
"v_subrev_u32 v241, 59904, v241\n\t"
"v_accvgpr_write_b32 a91, v58\n\t"
"v_mfma_f32_16x16x32_bf16 v[72:75], a[148:151], a[176:179], v[72:75]\n\t"
"buffer_store_dwordx4 a[124:127], v248, s[16:19], s29 offen offset:128 nt\n\t"
"ds_read_b128 a[176:179], v255 offset:7680\n\t"
"ds_read_b64_tr_b16 v[236:237], v240 offset:512\n\t"
"v_accvgpr_write_b32 a98, v60\n\t"
"v_mfma_f32_16x16x32_bf16 v[76:79], a[148:151], a[180:183], v[76:79]\n\t"
"ds_read_b128 a[180:183], v255 offset:10752\n\t"
"v_accvgpr_write_b32 a99, v62\n\t"
"v_mfma_f32_16x16x32_bf16 v[76:79], a[208:211], a[184:187], v[76:79]\n\t"
"ds_read_b64_tr_b16 v[238:239], v240 offset:2048\n\t"
"v_cvt_pk_bf16_f32 v64, v64, v65\n\t"
"v_mfma_f32_16x16x32_bf16 v[76:79], a[216:219], a[188:191], v[76:79]\n\t"
"ds_write_b128 v242, v[228:231] offset:29184\n\t"
"v_mfma_f32_16x16x32_bf16 v[80:83], a[216:219], a[192:195], v[80:83]\n\t"
"v_cvt_pk_bf16_f32 v66, v66, v67\n\t"
"v_cvt_pk_bf16_f32 v68, v68, v69\n\t"
"v_mfma_f32_16x16x32_bf16 v[80:83], a[208:211], a[188:191], v[80:83]\n\t"
"ds_read_b64_tr_b16 v[232:233], v241 offset:512\n\t"
"v_mfma_f32_16x16x32_bf16 v[80:83], a[148:151], a[184:187], v[80:83]\n\t"
"ds_read_b64_tr_b16 v[234:235], v241 offset:2048\n\t"
"v_cvt_pk_bf16_f32 v70, v70, v71\n\t"
"v_mfma_f32_16x16x32_bf16 v[84:87], a[148:151], a[188:191], v[84:87]\n\t"
"ds_read_b128 a[184:187], v255 offset:13824\n\t"
"v_accvgpr_write_b32 a108, v64\n\t"
"v_mfma_f32_16x16x32_bf16 v[84:87], a[208:211], a[192:195], v[84:87]\n\t"
"s_mov_b32 s20, 208896\n\t"
"ds_read_b128 a[188:191], v255 offset:16896\n\t"
"v_accvgpr_write_b32 a109, v66\n\t"
"v_mfma_f32_16x16x32_bf16 v[84:87], a[216:219], a[196:199], v[84:87]\n\t"
"v_cvt_pk_bf16_f32 v72, v72, v73\n\t"
"v_cvt_pk_bf16_f32 v74, v74, v75\n\t"
"v_mfma_f32_16x16x32_bf16 v[88:91], a[216:219], a[200:203], v[88:91]\n\t"
"v_accvgpr_write_b32 a116, v68\n\t"
"v_accvgpr_write_b32 a117, v70\n\t"
"v_mfma_f32_16x16x32_bf16 v[88:91], a[208:211], a[196:199], v[88:91]\n\t"
"v_cvt_pk_bf16_f32 v76, v76, v77\n\t"
"v_cvt_pk_bf16_f32 v78, v78, v79\n\t"
"v_mfma_f32_16x16x32_bf16 v[88:91], a[148:151], a[192:195], v[88:91]\n\t"
"buffer_store_dwordx4 a[156:159], v248, s[16:19], s29 offen offset:256 nt\n\t"
"ds_read_b128 a[192:195], v255 offset:19968\n\t"
"v_accvgpr_write_b32 a124, v72\n\t"
"v_accvgpr_write_b32 a125, v74\n\t"
"v_cvt_pk_bf16_f32 v80, v80, v81\n\t"
"v_mfma_f32_16x16x32_bf16 v[92:95], a[148:151], a[196:199], v[92:95]\n\t"
"s_waitcnt lgkmcnt(6)\n\t"
"ds_read_b128 a[196:199], v255 offset:23040\n\t"
"v_cvt_pk_bf16_f32 v82, v82, v83\n\t"
"v_mfma_f32_16x16x32_bf16 v[92:95], a[208:211], a[200:203], v[92:95]\n\t"
"s_waitcnt lgkmcnt(4)\n\t"
"ds_read_b128 a[200:203], v255 offset:26112\n\t"
"v_accvgpr_write_b32 a132, v76\n\t"
"v_mfma_f32_16x16x32_bf16 v[92:95], a[216:219], a[204:207], v[92:95]\n\t"
"ds_read_b128 a[204:207], v255 offset:29184\n\t"
"v_accvgpr_write_b32 a133, v78\n\t"
"v_mfma_f32_16x16x32_bf16 v[236:239], v[236:239], a[136:139], 0\n\t"
"s_add_u32 s29, s22, 786816\n\t"
"v_cvt_pk_bf16_f32 v84, v84, v85\n\t"
"v_cvt_pk_bf16_f32 v86, v86, v87\n\t"
"v_mfma_f32_16x16x32_bf16 v[232:235], v[232:235], a[136:139], 0\n\t"
"buffer_load_dwordx4 a[208:211], off, s[80:83], s20 offset:0 sc1\n\t"
"v_mfma_f32_16x16x32_bf16 v[100:103], a[224:227], a[168:171], v[100:103]\n\t"
"buffer_load_dwordx4 a[216:219], off, s[80:83], s20 offset:1024 sc1\n\t"
"v_mfma_f32_16x16x32_bf16 v[100:103], a[232:235], a[176:179], v[100:103]\n\t"
"v_accvgpr_write_b32 a140, v80\n\t"
"v_accvgpr_write_b32 a141, v82\n\t"
"v_mfma_f32_16x16x32_bf16 v[100:103], a[228:231], a[172:175], v[100:103]\n\t"
"v_cvt_pk_bf16_f32 v88, v88, v89\n\t"
"v_cvt_pk_bf16_f32 v90, v90, v91\n\t"
"v_mfma_f32_16x16x32_bf16 v[104:107], a[224:227], a[172:175], v[104:107]\n\t"
"s_waitcnt lgkmcnt(3)\n\t"
"v_cvt_pk_bf16_f32 v236, v236, v237\n\t"
"v_cvt_pk_bf16_f32 v237, v238, v239\n\t"
"v_mfma_f32_16x16x32_bf16 v[104:107], a[228:231], a[176:179], v[104:107]\n\t"
"Lpg13_%=:\n\t"
"s_cbranch_vccnz Lvr1_%=\n\t"
"v_cvt_pk_bf16_f32 v238, v232, v233\n\t"
"v_mfma_f32_16x16x32_bf16 v[104:107], a[232:235], a[180:183], v[104:107]\n\t"
"s_waitcnt lgkmcnt(0)\n\t"
"s_barrier\n\t"
"v_mfma_f32_16x16x32_bf16 v[108:111], a[232:235], a[184:187], v[108:111]\n\t"
"v_cvt_pk_bf16_f32 v239, v234, v235\n\t"
"v_mfma_f32_16x16x32_bf16 v[108:111], a[228:231], a[180:183], v[108:111]\n\t"
"v_accvgpr_write_b32 a148, v84\n\t"
"v_accvgpr_write_b32 a149, v86\n\t"
"v_mfma_f32_16x16x32_bf16 v[108:111], a[224:227], a[176:179], v[108:111]\n\t"
"buffer_store_dwordx4 a[52:55], v248, s[16:19], s29 offen offset:0 nt\n\t"
"ds_read_b64_tr_b16 v[224:225], v240 offset:20480\n\t"
"ds_read_b64_tr_b16 v[226:227], v240 offset:22016\n\t"
"v_cvt_pk_bf16_f32 v92, v92, v93\n\t"
"v_mfma_f32_16x16x32_bf16 v[112:115], a[224:227], a[180:183], v[112:115]\n\t"
"ds_read_b64_tr_b16 v[228:229], v241 offset:20480\n\t"
"v_cvt_pk_bf16_f32 v94, v94, v95\n\t"
"v_mfma_f32_16x16x32_bf16 v[112:115], a[228:231], a[184:187], v[112:115]\n\t"
"ds_read_b64_tr_b16 v[230:231], v241 offset:22016\n\t"
"v_accvgpr_write_b32 a156, v88\n\t"
"v_mfma_f32_16x16x32_bf16 v[112:115], a[232:235], a[188:191], v[112:115]\n\t"
"ds_write_b128 v242, v[236:239] offset:24576\n\t"
"v_mfma_f32_16x16x32_bf16 v[116:119], a[232:235], a[192:195], v[116:119]\n\t"
"v_accvgpr_write_b32 a157, v90\n\t"
"v_accvgpr_write_b32 a164, v92\n\t"
"v_mfma_f32_16x16x32_bf16 v[116:119], a[228:231], a[188:191], v[116:119]\n\t"
"ds_read_b64_tr_b16 v[232:233], v240 offset:40448\n\t"
"v_mfma_f32_16x16x32_bf16 v[116:119], a[224:227], a[184:187], v[116:119]\n\t"
"ds_read_b64_tr_b16 v[234:235], v240 offset:41984\n\t"
"v_accvgpr_write_b32 a165, v94\n\t"
"v_mfma_f32_16x16x32_bf16 v[120:123], a[224:227], a[188:191], v[120:123]\n\t"
"ds_read_b64_tr_b16 v[236:237], v241 offset:40448\n\t"
"v_cvt_pk_bf16_f32 v100, v100, v101\n\t"
"v_mfma_f32_16x16x32_bf16 v[120:123], a[228:231], a[192:195], v[120:123]\n\t"
"ds_read_b64_tr_b16 v[238:239], v241 offset:41984\n\t"
"v_cvt_pk_bf16_f32 v102, v102, v103\n\t"
"v_mfma_f32_16x16x32_bf16 v[120:123], a[232:235], a[196:199], v[120:123]\n\t"
"ds_write_b128 v249, a[40:43] offset:0\n\t"
"v_mfma_f32_16x16x32_bf16 v[124:127], a[232:235], a[200:203], v[124:127]\n\t"
"v_cvt_pk_bf16_f32 v104, v104, v105\n\t"
"v_cvt_pk_bf16_f32 v106, v106, v107\n\t"
"v_mfma_f32_16x16x32_bf16 v[124:127], a[228:231], a[196:199], v[124:127]\n\t"
"v_accvgpr_write_b32 a44, v100\n\t"
"v_accvgpr_write_b32 a45, v102\n\t"
"v_mfma_f32_16x16x32_bf16 v[124:127], a[224:227], a[192:195], v[124:127]\n\t"
"buffer_store_dwordx4 a[84:87], v248, s[16:19], s29 offen offset:128 nt\n\t"
"ds_write_b64 v250, a[108:109] offset:0\n\t"
"v_cvt_pk_bf16_f32 v108, v108, v109\n\t"
"v_mfma_f32_16x16x32_bf16 v[128:131], a[224:227], a[196:199], v[128:131]\n\t"
"s_waitcnt lgkmcnt(9)\n\t"
"v_cvt_pk_bf16_f32 v110, v110, v111\n\t"
"v_accvgpr_write_b32 a52, v104\n\t"
"v_mfma_f32_16x16x32_bf16 v[128:131], a[228:231], a[200:203], v[128:131]\n\t"
"s_waitcnt lgkmcnt(7)\n\t"
"v_accvgpr_write_b32 a53, v106\n\t"
"v_cvt_pk_bf16_f32 v112, v112, v113\n\t"
"v_mfma_f32_16x16x32_bf16 v[128:131], a[232:235], a[204:207], v[128:131]\n\t"
"s_waitcnt lgkmcnt(4)\n\t"
"ds_write_b128 v249, a[56:59] offset:12288\n\t"
"v_mfma_f32_16x16x32_bf16 v[224:227], v[224:227], a[136:139], 0\n\t"
"buffer_load_dwordx4 a[224:227], off, s[80:83], s20 offset:2048 sc1\n\t"
"s_mov_b32 s20, 294912\n\t"
"v_mfma_f32_16x16x32_bf16 v[228:231], v[228:231], a[136:139], 0\n\t"
"buffer_load_dwordx4 a[228:231], off, s[80:83], s20 offset:0 sc1\n\t"
"v_mfma_f32_16x16x32_bf16 v[232:235], v[232:235], a[136:139], 0\n\t"
"s_waitcnt lgkmcnt(3)\n\t"
"ds_write_b64 v250, a[124:125] offset:12288\n\t"
"v_mfma_f32_16x16x32_bf16 v[236:239], v[236:239], a[136:139], 0\n\t"
"buffer_load_dwordx4 a[232:235], off, s[80:83], s20 offset:1024 sc1\n\t"
"v_mfma_f32_16x16x32_bf16 v[132:135], a[236:239], a[168:171], v[132:135]\n\t"
"v_cvt_pk_bf16_f32 v114, v114, v115\n\t"
"v_accvgpr_write_b32 a60, v108\n\t"
"v_mfma_f32_16x16x32_bf16 v[132:135], a[244:247], a[176:179], v[132:135]\n\t"
"v_cvt_pk_bf16_f32 v224, v224, v225\n\t"
"v_cvt_pk_bf16_f32 v225, v226, v227\n\t"
"v_mfma_f32_16x16x32_bf16 v[132:135], a[240:243], a[172:175], v[132:135]\n\t"
"ds_write_b128 v249, a[48:51] offset:6144\n\t"
"v_mfma_f32_16x16x32_bf16 v[136:139], a[236:239], a[172:175], v[136:139]\n\t"
"v_accvgpr_write_b32 a61, v110\n\t"
"v_cvt_pk_bf16_f32 v226, v228, v229\n\t"
"v_mfma_f32_16x16x32_bf16 v[136:139], a[240:243], a[176:179], v[136:139]\n\t"
"v_cvt_pk_bf16_f32 v227, v230, v231\n\t"
"v_cvt_pk_bf16_f32 v232, v232, v233\n\t"
"v_mfma_f32_16x16x32_bf16 v[136:139], a[244:247], a[180:183], v[136:139]\n\t"
"buffer_store_dwordx4 v[196:199], v248, s[16:19], s29 offen offset:256 nt\n\t"
"ds_write_b64 v250, a[116:117] offset:6144\n\t"
"v_cvt_pk_bf16_f32 v233, v234, v235\n\t"
"v_mfma_f32_16x16x32_bf16 v[140:143], a[244:247], a[184:187], v[140:143]\n\t"
"s_add_u32 s29, s22, 983424\n\t"
"v_cvt_pk_bf16_f32 v116, v116, v117\n\t"
"v_cvt_pk_bf16_f32 v118, v118, v119\n\t"
"v_mfma_f32_16x16x32_bf16 v[140:143], a[240:243], a[180:183], v[140:143]\n\t"
"v_cvt_pk_bf16_f32 v234, v236, v237\n\t"
"v_cvt_pk_bf16_f32 v235, v238, v239\n\t"
"v_mfma_f32_16x16x32_bf16 v[140:143], a[236:239], a[176:179], v[140:143]\n\t"
"ds_write_b128 v242, v[224:227] offset:26112\n\t"
"v_accvgpr_write_b32 a68, v112\n\t"
"v_accvgpr_write_b32 a69, v114\n\t"
"v_mfma_f32_16x16x32_bf16 v[144:147], a[236:239], a[180:183], v[144:147]\n\t"
"v_cvt_pk_bf16_f32 v120, v120, v121\n\t"
"v_cvt_pk_bf16_f32 v122, v122, v123\n\t"
"v_mfma_f32_16x16x32_bf16 v[144:147], a[240:243], a[184:187], v[144:147]\n\t"
"v_accvgpr_write_b32 a76, v116\n\t"
"v_accvgpr_write_b32 a77, v118\n\t"
"v_mfma_f32_16x16x32_bf16 v[144:147], a[244:247], a[188:191], v[144:147]\n\t"
"ds_write_b128 v242, v[232:235] offset:27648\n\t"
"v_mfma_f32_16x16x32_bf16 v[148:151], a[244:247], a[192:195], v[148:151]\n\t"
"v_cvt_pk_bf16_f32 v124, v124, v125\n\t"
"v_cvt_pk_bf16_f32 v126, v126, v127\n\t"
"v_mfma_f32_16x16x32_bf16 v[148:151], a[240:243], a[188:191], v[148:151]\n\t"
"v_accvgpr_write_b32 a84, v120\n\t"
"v_accvgpr_write_b32 a85, v122\n\t"
"v_mfma_f32_16x16x32_bf16 v[148:151], a[236:239], a[184:187], v[148:151]\n\t"
"ds_write_b128 v249, a[64:67] offset:18432\n\t"
"v_mfma_f32_16x16x32_bf16 v[152:155], a[236:239], a[188:191], v[152:155]\n\t"
"v_cvt_pk_bf16_f32 v128, v128, v129\n\t"
"v_cvt_pk_bf16_f32 v130, v130, v131\n\t"
"v_mfma_f32_16x16x32_bf16 v[152:155], a[240:243], a[192:195], v[152:155]\n\t"
"v_accvgpr_write_b32 a92, v124\n\t"
"v_accvgpr_write_b32 a93, v126\n\t"
"v_mfma_f32_16x16x32_bf16 v[152:155], a[244:247], a[196:199], v[152:155]\n\t"
"buffer_store_dwordx4 v[204:207], v248, s[16:19], s29 offen offset:0 nt\n\t"
"ds_write_b64 v250, a[132:133] offset:18432\n\t"
"v_accvgpr_write_b32 a100, v128\n\t"
"v_mfma_f32_16x16x32_bf16 v[156:159], a[244:247], a[200:203], v[156:159]\n\t"
"v_accvgpr_write_b32 a101, v130\n\t"
"v_cvt_pk_bf16_f32 v132, v132, v133\n\t"
"v_mfma_f32_16x16x32_bf16 v[156:159], a[240:243], a[196:199], v[156:159]\n\t"
"v_cvt_pk_bf16_f32 v134, v134, v135\n\t"
"v_cvt_pk_bf16_f32 v136, v136, v137\n\t"
"v_mfma_f32_16x16x32_bf16 v[156:159], a[236:239], a[192:195], v[156:159]\n\t"
"v_cvt_pk_bf16_f32 v138, v138, v139\n\t"
"v_accvgpr_write_b32 a46, v132\n\t"
"v_mfma_f32_16x16x32_bf16 v[160:163], a[236:239], a[196:199], v[160:163]\n\t"
"v_accvgpr_write_b32 a47, v134\n\t"
"v_cvt_pk_bf16_f32 v140, v140, v141\n\t"
"v_mfma_f32_16x16x32_bf16 v[160:163], a[240:243], a[200:203], v[160:163]\n\t"
"v_cvt_pk_bf16_f32 v142, v142, v143\n\t"
"v_accvgpr_write_b32 a54, v136\n\t"
"v_mfma_f32_16x16x32_bf16 v[160:163], a[244:247], a[204:207], v[160:163]\n\t"
"v_accvgpr_write_b32 a55, v138\n\t"
"v_cvt_pk_bf16_f32 v144, v144, v145\n\t"
"v_mfma_f32_16x16x32_bf16 v[164:167], a[248:251], a[168:171], v[164:167]\n\t"
"v_cvt_pk_bf16_f32 v146, v146, v147\n\t"
"v_accvgpr_write_b32 a62, v140\n\t"
"v_mfma_f32_16x16x32_bf16 v[164:167], a[20:23], a[176:179], v[164:167]\n\t"
"v_accvgpr_write_b32 a63, v142\n\t"
"v_cvt_pk_bf16_f32 v148, v148, v149\n\t"
"v_mfma_f32_16x16x32_bf16 v[164:167], a[144:147], a[172:175], v[164:167]\n\t"
"buffer_load_dwordx4 a[236:239], off, s[80:83], s20 offset:2048 sc1\n\t"
"s_mov_b32 s20, 307200\n\t"
"v_mfma_f32_16x16x32_bf16 v[168:171], a[248:251], a[172:175], v[168:171]\n\t"
"buffer_load_dwordx4 a[240:243], off, s[80:83], s20 offset:0 sc1\n\t"
"v_mfma_f32_16x16x32_bf16 v[168:171], a[144:147], a[176:179], v[168:171]\n\t"
"buffer_load_dwordx4 a[244:247], off, s[80:83], s20 offset:1024 sc1\n\t"
"v_mfma_f32_16x16x32_bf16 v[168:171], a[20:23], a[180:183], v[168:171]\n\t"
"buffer_store_dwordx4 v[212:215], v248, s[16:19], s29 offen offset:128 nt\n\t"
"v_cvt_pk_bf16_f32 v150, v150, v151\n\t"
"v_accvgpr_write_b32 a70, v144\n\t"
"v_accvgpr_write_b32 a71, v146\n\t"
"v_cvt_pk_bf16_f32 v152, v152, v153\n\t"
"v_mfma_f32_16x16x32_bf16 v[172:175], a[20:23], a[184:187], v[172:175]\n\t"
"v_cvt_pk_bf16_f32 v154, v154, v155\n\t"
"v_accvgpr_write_b32 a78, v148\n\t"
"v_mfma_f32_16x16x32_bf16 v[172:175], a[144:147], a[180:183], v[172:175]\n\t"
"v_accvgpr_write_b32 a79, v150\n\t"
"v_cvt_pk_bf16_f32 v156, v156, v157\n\t"
"v_mfma_f32_16x16x32_bf16 v[172:175], a[248:251], a[176:179], v[172:175]\n\t"
"v_cvt_pk_bf16_f32 v158, v158, v159\n\t"
"v_accvgpr_write_b32 a86, v152\n\t"
"v_mfma_f32_16x16x32_bf16 v[176:179], a[248:251], a[180:183], v[176:179]\n\t"
"v_accvgpr_write_b32 a87, v154\n\t"
"v_cvt_pk_bf16_f32 v160, v160, v161\n\t"
"v_mfma_f32_16x16x32_bf16 v[176:179], a[144:147], a[184:187], v[176:179]\n\t"
"v_cvt_pk_bf16_f32 v162, v162, v163\n\t"
"v_mfma_f32_16x16x32_bf16 v[176:179], a[20:23], a[188:191], v[176:179]\n\t"
"s_waitcnt lgkmcnt(0)\n\t"
"s_barrier\n\t"
"v_mfma_f32_16x16x32_bf16 v[180:183], a[20:23], a[192:195], v[180:183]\n\t"
"ds_read_b128 a[168:171], v244 offset:24576\n\t"
"v_mfma_f32_16x16x32_bf16 v[180:183], a[144:147], a[188:191], v[180:183]\n\t"
"ds_read_b128 a[172:175], v244 offset:25088\n\t"
"v_accvgpr_write_b32 a94, v156\n\t"
"v_mfma_f32_16x16x32_bf16 v[180:183], a[248:251], a[184:187], v[180:183]\n\t"
"ds_read_b128 a[176:179], v244 offset:25600\n\t"
"v_accvgpr_write_b32 a95, v158\n\t"
"v_mfma_f32_16x16x32_bf16 v[184:187], a[248:251], a[188:191], v[184:187]\n\t"
"ds_read_b128 a[180:183], v244 offset:26112\n\t"
"v_cvt_pk_bf16_f32 v164, v164, v165\n\t"
"v_mfma_f32_16x16x32_bf16 v[184:187], a[144:147], a[192:195], v[184:187]\n\t"
"ds_read_b128 a[184:187], v244 offset:26624\n\t"
"v_cvt_pk_bf16_f32 v166, v166, v167\n\t"
"v_mfma_f32_16x16x32_bf16 v[184:187], a[20:23], a[196:199], v[184:187]\n\t"
"buffer_load_dwordx4 a[48:51], off, s[80:83], s20 offset:2048 sc1\n\t"
"s_mov_b32 s20, 319488\n\t"
"v_mfma_f32_16x16x32_bf16 v[188:191], a[20:23], a[200:203], v[188:191]\n\t"
"ds_read_b128 a[188:191], v244 offset:27136\n\t"
"v_accvgpr_write_b32 a102, v160\n\t"
"v_mfma_f32_16x16x32_bf16 v[188:191], a[144:147], a[196:199], v[188:191]\n\t"
"v_accvgpr_write_b32 a103, v162\n\t"
"v_cvt_pk_bf16_f32 v168, v168, v169\n\t"
"v_mfma_f32_16x16x32_bf16 v[188:191], a[248:251], a[192:195], v[188:191]\n\t"
"buffer_store_dwordx4 a[0:3], v248, s[16:19], s29 offen offset:256 nt\n\t"
"buffer_load_dwordx4 a[56:59], off, s[80:83], s20 offset:0 sc1\n\t"
"ds_read_b128 a[192:195], v244 offset:27648\n\t"
"v_mfma_f32_16x16x32_bf16 v[192:195], a[248:251], a[196:199], v[192:195]\n\t"
"s_waitcnt lgkmcnt(4)\n\t"
"ds_read_b128 a[196:199], v244 offset:28160\n\t"
"v_cvt_pk_bf16_f32 v170, v170, v171\n\t"
"v_mfma_f32_16x16x32_bf16 v[192:195], a[144:147], a[200:203], v[192:195]\n\t"
"ds_read_b128 a[200:203], v244 offset:28672\n\t"
"v_accvgpr_write_b32 a110, v164\n\t"
"v_mfma_f32_16x16x32_bf16 v[192:195], a[20:23], a[204:207], v[192:195]\n\t"
"buffer_load_dwordx4 a[64:67], off, s[80:83], s20 offset:1024 sc1\n\t"
"s_add_u32 s29, s22, 1180032\n\t"
"Lst18_%=:\n\t"
"v_mfma_f32_16x16x32_bf16 v[2:5], a[252:255], a[168:171], 0\n\t"
"buffer_load_dwordx4 a[144:147], off, s[80:83], s20 offset:2048 sc1\n\t"
"v_mfma_f32_16x16x32_bf16 v[2:5], a[220:223], a[176:179], v[2:5]\n\t"
"s_waitcnt lgkmcnt(5)\n\t"
"ds_read_b128 a[204:207], v244 offset:29184\n\t"
"v_accvgpr_write_b32 a111, v166\n\t"
"v_mfma_f32_16x16x32_bf16 v[2:5], v[96:99], a[172:175], v[2:5]\n\t"
"s_mov_b32 s20, 479232\n\t"
"v_cvt_pk_bf16_f32 v172, v172, v173\n\t"
"v_cvt_pk_bf16_f32 v174, v174, v175\n\t"
"v_mfma_f32_16x16x32_bf16 v[6:9], a[252:255], a[172:175], 0\n\t"
"s_waitcnt lgkmcnt(5)\n\t"
"v_accvgpr_write_b32 a118, v168\n\t"
"v_accvgpr_write_b32 a119, v170\n\t"
"v_mfma_f32_16x16x32_bf16 v[6:9], v[96:99], a[176:179], v[6:9]\n\t"
"v_cvt_pk_bf16_f32 v176, v176, v177\n\t"
"v_cvt_pk_bf16_f32 v178, v178, v179\n\t"
"v_mfma_f32_16x16x32_bf16 v[6:9], a[220:223], a[180:183], v[6:9]\n\t"
"buffer_load_dwordx4 a[20:23], off, s[80:83], s20 offset:0 sc1\n\t"
"v_mfma_f32_16x16x32_bf16 v[10:13], a[220:223], a[184:187], 0\n\t"
"buffer_load_dwordx4 a[248:251], off, s[80:83], s20 offset:1024 sc1\n\t"
"v_mfma_f32_16x16x32_bf16 v[10:13], v[96:99], a[180:183], v[10:13]\n\t"
"s_waitcnt lgkmcnt(4)\n\t"
"v_accvgpr_write_b32 a126, v172\n\t"
"v_accvgpr_write_b32 a127, v174\n\t"
"v_mfma_f32_16x16x32_bf16 v[10:13], a[252:255], a[176:179], v[10:13]\n\t"
"buffer_store_dwordx4 a[8:11], v248, s[16:19], s29 offen offset:0 nt\n\t"
"v_cvt_pk_bf16_f32 v180, v180, v181\n\t"
"v_cvt_pk_bf16_f32 v182, v182, v183\n\t"
"v_accvgpr_write_b32 a134, v176\n\t"
"v_accvgpr_write_b32 a135, v178\n\t"
"v_mfma_f32_16x16x32_bf16 v[14:17], a[252:255], a[180:183], 0\n\t"
"s_waitcnt lgkmcnt(3)\n\t"
"v_cvt_pk_bf16_f32 v184, v184, v185\n\t"
"v_cvt_pk_bf16_f32 v186, v186, v187\n\t"
"v_mfma_f32_16x16x32_bf16 v[14:17], v[96:99], a[184:187], v[14:17]\n\t"
"v_accvgpr_write_b32 a142, v180\n\t"
"v_accvgpr_write_b32 a143, v182\n\t"
"v_mfma_f32_16x16x32_bf16 v[14:17], a[220:223], a[188:191], v[14:17]\n\t"
"v_cvt_pk_bf16_f32 v188, v188, v189\n\t"
"v_cvt_pk_bf16_f32 v190, v190, v191\n\t"
"v_mfma_f32_16x16x32_bf16 v[18:21], a[220:223], a[192:195], 0\n\t"
"s_waitcnt lgkmcnt(2)\n\t"
"v_accvgpr_write_b32 a150, v184\n\t"
"v_accvgpr_write_b32 a151, v186\n\t"
"v_mfma_f32_16x16x32_bf16 v[18:21], v[96:99], a[188:191], v[18:21]\n\t"
"v_cvt_pk_bf16_f32 v192, v192, v193\n\t"
"v_cvt_pk_bf16_f32 v194, v194, v195\n\t"
"v_mfma_f32_16x16x32_bf16 v[18:21], a[252:255], a[184:187], v[18:21]\n\t"
"ds_read_b128 v[196:199], v252 offset:0\n\t"
"v_accvgpr_write_b32 a158, v188\n\t"
"v_mfma_f32_16x16x32_bf16 v[22:25], a[252:255], a[188:191], 0\n\t"
"s_waitcnt lgkmcnt(2)\n\t"
"ds_read_b128 a[0:3], v252 offset:12288\n\t"
"v_accvgpr_write_b32 a159, v190\n\t"
"v_mfma_f32_16x16x32_bf16 v[22:25], v[96:99], a[192:195], v[22:25]\n\t"
"ds_read_b128 v[204:207], v252 offset:1024\n\t"
"v_accvgpr_write_b32 a166, v192\n\t"
"v_mfma_f32_16x16x32_bf16 v[22:25], a[220:223], a[196:199], v[22:25]\n\t"
"ds_read_b128 a[8:11], v252 offset:13312\n\t"
"v_accvgpr_write_b32 a167, v194\n\t"
"v_mfma_f32_16x16x32_bf16 v[26:29], a[220:223], a[200:203], 0\n\t"
"s_waitcnt lgkmcnt(4)\n\t"
"ds_read_b128 v[212:215], v252 offset:2048\n\t"
"v_mfma_f32_16x16x32_bf16 v[26:29], v[96:99], a[196:199], v[26:29]\n\t"
"ds_read_b128 a[40:43], v252 offset:14336\n\t"
"v_mfma_f32_16x16x32_bf16 v[26:29], a[252:255], a[192:195], v[26:29]\n\t"
"buffer_store_dwordx4 a[16:19], v248, s[16:19], s29 offen offset:128 nt\n\t"
"v_mfma_f32_16x16x32_bf16 v[30:33], a[252:255], a[196:199], 0\n\t"
"s_waitcnt vmcnt(38)\n\t"
"v_mfma_f32_16x16x32_bf16 v[30:33], v[96:99], a[200:203], v[30:33]\n\t"
"buffer_load_dwordx4 a[252:255], off, s[80:83], s20 offset:2048 sc1\n\t"
"v_mfma_f32_16x16x32_bf16 v[30:33], a[220:223], a[204:207], v[30:33]\n\t"
"s_mov_b32 s20, 491520\n\t"
"v_mfma_f32_16x16x32_bf16 v[34:37], a[152:155], a[168:171], 0\n\t"
"buffer_load_dwordx4 a[220:223], off, s[80:83], s20 offset:0 sc1\n\t"
"v_mfma_f32_16x16x32_bf16 v[34:37], a[160:163], a[176:179], v[34:37]\n\t"
"v_mfma_f32_16x16x32_bf16 v[34:37], a[28:31], a[172:175], v[34:37]\n\t"
"buffer_store_dwordx4 a[24:27], v248, s[16:19], s29 offen offset:256 nt\n\t"
"v_mfma_f32_16x16x32_bf16 v[38:41], a[152:155], a[172:175], 0\n\t"
"v_mfma_f32_16x16x32_bf16 v[38:41], a[28:31], a[176:179], v[38:41]\n\t"
"v_mfma_f32_16x16x32_bf16 v[38:41], a[160:163], a[180:183], v[38:41]\n\t"
"v_mfma_f32_16x16x32_bf16 v[42:45], a[160:163], a[184:187], 0\n\t"
"v_mfma_f32_16x16x32_bf16 v[42:45], a[28:31], a[180:183], v[42:45]\n\t"
"v_mfma_f32_16x16x32_bf16 v[42:45], a[152:155], a[176:179], v[42:45]\n\t"
"s_add_u32 s29, s22, 1376640\n\t"
"ds_read_b64_tr_b16 v[228:229], v240 offset:1024\n\t"
"v_mfma_f32_16x16x32_bf16 v[46:49], a[152:155], a[180:183], 0\n\t"
"ds_read_b64_tr_b16 v[230:231], v240 offset:2560\n\t"
"v_mfma_f32_16x16x32_bf16 v[46:49], a[28:31], a[184:187], v[46:49]\n\t"
"ds_read_b64_tr_b16 v[236:237], v241 offset:1024\n\t"
"v_mfma_f32_16x16x32_bf16 v[46:49], a[160:163], a[188:191], v[46:49]\n\t"
"s_waitcnt lgkmcnt(3)\n\t"
"s_barrier\n\t"
"v_mfma_f32_16x16x32_bf16 v[50:53], a[160:163], a[192:195], 0\n\t"
"ds_read_b64_tr_b16 v[238:239], v241 offset:2560\n\t"
"v_mfma_f32_16x16x32_bf16 v[50:53], a[28:31], a[188:191], v[50:53]\n\t"
"ds_read_b64_tr_b16 v[224:225], v240 offset:20992\n\t"
"v_mfma_f32_16x16x32_bf16 v[50:53], a[152:155], a[184:187], v[50:53]\n\t"
"ds_read_b64_tr_b16 v[226:227], v240 offset:22528\n\t"
"v_mfma_f32_16x16x32_bf16 v[54:57], a[152:155], a[188:191], 0\n\t"
"ds_read_b64_tr_b16 v[232:233], v241 offset:20992\n\t"
"v_mfma_f32_16x16x32_bf16 v[54:57], a[28:31], a[192:195], v[54:57]\n\t"
"ds_read_b64_tr_b16 v[234:235], v241 offset:22528\n\t"
"v_mfma_f32_16x16x32_bf16 v[54:57], a[160:163], a[196:199], v[54:57]\n\t"
"buffer_store_dwordx4 a[32:35], v248, s[16:19], s29 offen offset:0 nt\n\t"
"ds_write_b64 v250, a[140:141] offset:0\n\t"
"v_mfma_f32_16x16x32_bf16 v[58:61], a[160:163], a[200:203], 0\n\t"
"v_mfma_f32_16x16x32_bf16 v[58:61], a[28:31], a[196:199], v[58:61]\n\t"
"v_mfma_f32_16x16x32_bf16 v[58:61], a[152:155], a[192:195], v[58:61]\n\t"
"ds_write_b128 v249, a[72:75] offset:0\n\t"
"v_mfma_f32_16x16x32_bf16 v[62:65], a[152:155], a[196:199], 0\n\t"
"buffer_load_dwordx4 a[152:155], off, s[80:83], s20 offset:1024 sc1\n\t"
"v_mfma_f32_16x16x32_bf16 v[62:65], a[28:31], a[200:203], v[62:65]\n\t"
"s_waitcnt lgkmcnt(6)\n\t"
"v_mfma_f32_16x16x32_bf16 v[62:65], a[160:163], a[204:207], v[62:65]\n\t"
"s_waitcnt lgkmcnt(4)\n\t"
"ds_write_b128 v249, a[88:91] offset:12288\n\t"
"v_mfma_f32_16x16x32_bf16 v[228:231], v[228:231], a[136:139], 0\n\t"
"buffer_load_dwordx4 a[160:163], off, s[80:83], s20 offset:2048 sc1\n\t"
"v_mfma_f32_16x16x32_bf16 v[236:239], v[236:239], a[136:139], 0\n\t"
"s_waitcnt vmcnt(40) lgkmcnt(3)\n\t"
"s_mov_b32 s20, 503808\n\t"
"v_mfma_f32_16x16x32_bf16 v[224:227], v[224:227], a[136:139], 0\n\t"
"ds_write_b64 v250, a[156:157] offset:12288\n\t"
"v_mfma_f32_16x16x32_bf16 v[232:235], v[232:235], a[136:139], 0\n\t"
"buffer_load_dwordx4 a[28:31], off, s[80:83], s20 offset:0 sc1\n\t"
"v_mfma_f32_16x16x32_bf16 v[66:69], a[36:39], a[168:171], 0\n\t"
"ds_read_b128 a[168:171], v244 offset:33792\n\t"
"v_mfma_f32_16x16x32_bf16 v[66:69], a[120:123], a[176:179], v[66:69]\n\t"
"v_cvt_pk_bf16_f32 v228, v228, v229\n\t"
"v_cvt_pk_bf16_f32 v229, v230, v231\n\t"
"v_mfma_f32_16x16x32_bf16 v[66:69], v[200:203], a[172:175], v[66:69]\n\t"
"ds_write_b128 v249, a[80:83] offset:6144\n\t"
"v_mfma_f32_16x16x32_bf16 v[70:73], a[36:39], a[172:175], 0\n\t"
"v_cvt_pk_bf16_f32 v230, v236, v237\n\t"
"v_cvt_pk_bf16_f32 v231, v238, v239\n\t"
"v_mfma_f32_16x16x32_bf16 v[70:73], v[200:203], a[176:179], v[70:73]\n\t"
"ds_read_b128 a[172:175], v244 offset:34304\n\t"
"v_mfma_f32_16x16x32_bf16 v[70:73], a[120:123], a[180:183], v[70:73]\n\t"
"buffer_store_dwordx4 a[104:107], v248, s[16:19], s29 offen offset:128 nt\n\t"
"ds_write_b64 v250, a[148:149] offset:6144\n\t"
"v_cvt_pk_bf16_f32 v224, v224, v225\n\t"
"v_mfma_f32_16x16x32_bf16 v[74:77], a[120:123], a[184:187], 0\n\t"
"v_cvt_pk_bf16_f32 v225, v226, v227\n\t"
"v_cvt_pk_bf16_f32 v226, v232, v233\n\t"
"v_mfma_f32_16x16x32_bf16 v[74:77], v[200:203], a[180:183], v[74:77]\n\t"
"v_cvt_pk_bf16_f32 v227, v234, v235\n\t"
"v_mfma_f32_16x16x32_bf16 v[74:77], a[36:39], a[176:179], v[74:77]\n\t"
"ds_read_b128 a[176:179], v244 offset:34816\n\t"
"v_mfma_f32_16x16x32_bf16 v[78:81], a[36:39], a[180:183], 0\n\t"
"ds_read_b128 a[180:183], v244 offset:35328\n\t"
"v_mfma_f32_16x16x32_bf16 v[78:81], v[200:203], a[184:187], v[78:81]\n\t"
"v_mfma_f32_16x16x32_bf16 v[78:81], a[120:123], a[188:191], v[78:81]\n\t"
"ds_write_b128 v249, a[96:99] offset:18432\n\t"
"v_mfma_f32_16x16x32_bf16 v[82:85], a[120:123], a[192:195], 0\n\t"
"v_mfma_f32_16x16x32_bf16 v[82:85], v[200:203], a[188:191], v[82:85]\n\t"
"v_mfma_f32_16x16x32_bf16 v[82:85], a[36:39], a[184:187], v[82:85]\n\t"
"ds_read_b128 a[184:187], v244 offset:35840\n\t"
"v_mfma_f32_16x16x32_bf16 v[86:89], a[36:39], a[188:191], 0\n\t"
"ds_read_b128 a[188:191], v244 offset:36352\n\t"
"v_mfma_f32_16x16x32_bf16 v[86:89], v[200:203], a[192:195], v[86:89]\n\t"
"v_mfma_f32_16x16x32_bf16 v[86:89], a[120:123], a[196:199], v[86:89]\n\t"
"buffer_store_dwordx4 a[112:115], v248, s[16:19], s29 offen offset:256 nt\n\t"
"ds_write_b64 v250, a[164:165] offset:18432\n\t"
"v_mfma_f32_16x16x32_bf16 v[90:93], a[120:123], a[200:203], 0\n\t"
"v_mfma_f32_16x16x32_bf16 v[90:93], v[200:203], a[196:199], v[90:93]\n\t"
"v_mfma_f32_16x16x32_bf16 v[90:93], a[36:39], a[192:195], v[90:93]\n\t"
"ds_read_b128 a[192:195], v244 offset:36864\n\t"
"v_mfma_f32_16x16x32_bf16 v[94:97], a[36:39], a[196:199], 0\n\t"
"buffer_load_dwordx4 a[112:115], off, s[80:83], s20 offset:1024 sc1\n\t"
"v_mfma_f32_16x16x32_bf16 v[94:97], v[200:203], a[200:203], v[94:97]\n\t"
"s_waitcnt vmcnt(39) lgkmcnt(6)\n\t"
"ds_read_b128 a[196:199], v244 offset:37376\n\t"
"v_mfma_f32_16x16x32_bf16 v[94:97], a[120:123], a[204:207], v[94:97]\n\t"
"ds_read_b128 a[200:203], v244 offset:37888\n\t"
"v_mfma_f32_16x16x32_bf16 v[100:103], a[128:131], a[168:171], 0\n\t"
"s_waitcnt lgkmcnt(7)\n\t"
"ds_read_b128 a[204:207], v244 offset:38400\n\t"
"v_mfma_f32_16x16x32_bf16 v[100:103], v[216:219], a[176:179], v[100:103]\n\t"
"buffer_load_dwordx4 v[200:203], off, s[80:83], s20 offset:2048 sc1\n\t"
"v_mfma_f32_16x16x32_bf16 v[100:103], v[208:211], a[172:175], v[100:103]\n\t"
"s_mov_b32 s20, 110592\n\t"
"v_mfma_f32_16x16x32_bf16 v[104:107], a[128:131], a[172:175], 0\n\t"
"buffer_load_dwordx4 a[36:39], off, s[80:83], s20 offset:0 sc1\n\t"
"v_mfma_f32_16x16x32_bf16 v[104:107], v[208:211], a[176:179], v[104:107]\n\t"
"s_waitcnt lgkmcnt(6)\n\t"
"v_mfma_f32_16x16x32_bf16 v[104:107], v[216:219], a[180:183], v[104:107]\n\t"
"s_waitcnt lgkmcnt(0)\n\t"
"s_barrier\n\t"
"v_mfma_f32_16x16x32_bf16 v[108:111], v[216:219], a[184:187], 0\n\t"
"s_add_u32 s29, s23, 0\n\t"
"ds_read_b64_tr_b16 v[236:237], v240 offset:40960\n\t"
"v_mfma_f32_16x16x32_bf16 v[108:111], v[208:211], a[180:183], v[108:111]\n\t"
"ds_read_b64_tr_b16 v[238:239], v240 offset:42496\n\t"
"v_mfma_f32_16x16x32_bf16 v[108:111], a[128:131], a[176:179], v[108:111]\n\t"
"ds_write_b128 v242, v[228:231] offset:24576\n\t"
"v_mfma_f32_16x16x32_bf16 v[112:115], a[128:131], a[180:183], 0\n\t"
"v_mfma_f32_16x16x32_bf16 v[112:115], v[208:211], a[184:187], v[112:115]\n\t"
"ds_read_b64_tr_b16 v[232:233], v241 offset:40960\n\t"
"v_mfma_f32_16x16x32_bf16 v[112:115], v[216:219], a[188:191], v[112:115]\n\t"
"ds_write_b128 v242, v[224:227] offset:26112\n\t"
"v_mfma_f32_16x16x32_bf16 v[116:119], v[216:219], a[192:195], 0\n\t"
"v_mfma_f32_16x16x32_bf16 v[116:119], v[208:211], a[188:191], v[116:119]\n\t"
"ds_read_b64_tr_b16 v[234:235], v241 offset:42496\n\t"
"v_mfma_f32_16x16x32_bf16 v[116:119], a[128:131], a[184:187], v[116:119]\n\t"
"buffer_store_dwordx4 v[196:199], v248, s[16:19], s29 offen offset:0 nt\n\t"
"v_add_u32 v240, 59904, v240\n\t"
"v_add_u32 v241, 59904, v241\n\t"
"v_mfma_f32_16x16x32_bf16 v[120:123], a[128:131], a[188:191], 0\n\t"
"ds_read_b64_tr_b16 v[228:229], v240 offset:1024\n\t"
"v_mfma_f32_16x16x32_bf16 v[120:123], v[208:211], a[192:195], v[120:123]\n\t"
"ds_read_b64_tr_b16 v[230:231], v240 offset:2560\n\t"
"v_mfma_f32_16x16x32_bf16 v[120:123], v[216:219], a[196:199], v[120:123]\n\t"
"ds_read_b64_tr_b16 v[224:225], v241 offset:1024\n\t"
"v_mfma_f32_16x16x32_bf16 v[124:127], v[208:211], a[196:199], 0\n\t"
"s_waitcnt lgkmcnt(7)\n\t"
"ds_read_b64_tr_b16 v[226:227], v241 offset:2560\n\t"
"v_mfma_f32_16x16x32_bf16 v[124:127], a[128:131], a[192:195], v[124:127]\n\t"
"s_waitcnt lgkmcnt(4)\n\t"
"v_mfma_f32_16x16x32_bf16 v[124:127], v[216:219], a[200:203], v[124:127]\n\t"
"v_mfma_f32_16x16x32_bf16 v[236:239], v[236:239], a[136:139], 0\n\t"
"s_waitcnt lgkmcnt(2)\n\t"
"v_mfma_f32_16x16x32_bf16 v[232:235], v[232:235], a[136:139], 0\n\t"
"v_mfma_f32_16x16x32_bf16 v[228:231], v[228:231], a[136:139], 0\n\t"
"s_waitcnt lgkmcnt(0)\n\t"
"v_mfma_f32_16x16x32_bf16 v[224:227], v[224:227], a[136:139], 0\n\t"
"buffer_store_dwordx4 v[204:207], v248, s[16:19], s29 offen offset:128 nt\n\t"
"v_mfma_f32_16x16x32_bf16 v[128:131], v[208:211], a[200:203], 0\n\t"
"s_waitcnt vmcnt(38)\n\t"
"v_mfma_f32_16x16x32_bf16 v[128:131], a[128:131], a[196:199], v[128:131]\n\t"
"buffer_load_dwordx4 v[208:211], off, s[80:83], s20 offset:1024 sc1\n\t"
"v_mfma_f32_16x16x32_bf16 v[128:131], v[216:219], a[204:207], v[128:131]\n\t"
"v_cvt_pk_bf16_f32 v236, v236, v237\n\t"
"v_cvt_pk_bf16_f32 v237, v238, v239\n\t"
"v_mfma_f32_16x16x32_bf16 v[132:135], a[4:7], a[168:171], 0\n\t"
"v_cvt_pk_bf16_f32 v238, v232, v233\n\t"
"v_cvt_pk_bf16_f32 v239, v234, v235\n\t"
"v_mfma_f32_16x16x32_bf16 v[132:135], a[212:215], a[176:179], v[132:135]\n\t"
"ds_read_b64_tr_b16 v[232:233], v240 offset:20992\n\t"
"v_cvt_pk_bf16_f32 v228, v228, v229\n\t"
"v_mfma_f32_16x16x32_bf16 v[132:135], a[12:15], a[172:175], v[132:135]\n\t"
"ds_write_b128 v242, v[236:239] offset:27648\n\t"
"v_mfma_f32_16x16x32_bf16 v[136:139], a[4:7], a[172:175], 0\n\t"
"buffer_load_dwordx4 v[216:219], off, s[80:83], s20 offset:2048 sc1\n\t"
"v_mfma_f32_16x16x32_bf16 v[136:139], a[12:15], a[176:179], v[136:139]\n\t"
"ds_read_b64_tr_b16 v[234:235], v240 offset:22528\n\t"
"v_mfma_f32_16x16x32_bf16 v[136:139], a[212:215], a[180:183], v[136:139]\n\t"
"v_cvt_pk_bf16_f32 v229, v230, v231\n\t"
"v_cvt_pk_bf16_f32 v230, v224, v225\n\t"
"v_cvt_pk_bf16_f32 v231, v226, v227\n\t"
"v_mfma_f32_16x16x32_bf16 v[140:143], a[212:215], a[184:187], 0\n\t"
"ds_read_b64_tr_b16 v[224:225], v241 offset:20992\n\t"
"v_mfma_f32_16x16x32_bf16 v[140:143], a[12:15], a[180:183], v[140:143]\n\t"
"ds_read_b64_tr_b16 v[226:227], v241 offset:22528\n\t"
"v_mfma_f32_16x16x32_bf16 v[140:143], a[4:7], a[176:179], v[140:143]\n\t"
"ds_write_b128 v242, v[228:231] offset:29184\n\t"
"v_mfma_f32_16x16x32_bf16 v[144:147], a[4:7], a[180:183], 0\n\t"
"v_mfma_f32_16x16x32_bf16 v[144:147], a[12:15], a[184:187], v[144:147]\n\t"
"ds_read_b64_tr_b16 v[236:237], v240 offset:40960\n\t"
"v_mfma_f32_16x16x32_bf16 v[144:147], a[212:215], a[188:191], v[144:147]\n\t"
"ds_read_b64_tr_b16 v[238:239], v240 offset:42496\n\t"
"v_mfma_f32_16x16x32_bf16 v[148:151], a[4:7], a[184:187], 0\n\t"
"s_waitcnt lgkmcnt(5)\n\t"
"ds_read_b64_tr_b16 v[228:229], v241 offset:40960\n\t"
"v_mfma_f32_16x16x32_bf16 v[148:151], a[212:215], a[192:195], v[148:151]\n\t"
"s_waitcnt lgkmcnt(4)\n\t"
"ds_read_b64_tr_b16 v[230:231], v241 offset:42496\n\t"
"v_mfma_f32_16x16x32_bf16 v[148:151], a[12:15], a[188:191], v[148:151]\n\t"
"buffer_store_dwordx4 v[212:215], v248, s[16:19], s29 offen offset:256 nt\n\t"
"v_mfma_f32_16x16x32_bf16 v[232:235], v[232:235], a[136:139], 0\n\t"
"v_mfma_f32_16x16x32_bf16 v[224:227], v[224:227], a[136:139], 0\n\t"
"s_waitcnt lgkmcnt(2)\n\t"
"s_add_u32 s29, s23, 393216\n\t"
"v_mfma_f32_16x16x32_bf16 v[236:239], v[236:239], a[136:139], 0\n\t"
"s_waitcnt lgkmcnt(0)\n\t"
"v_mfma_f32_16x16x32_bf16 v[228:231], v[228:231], a[136:139], 0\n\t"
"ds_read_b128 v[196:199], v252 offset:0\n\t"
"v_mfma_f32_16x16x32_bf16 v[152:155], a[4:7], a[188:191], 0\n\t"
"ds_read_b128 a[16:19], v252 offset:12288\n\t"
"v_mfma_f32_16x16x32_bf16 v[152:155], a[212:215], a[196:199], v[152:155]\n\t"
"ds_read_b128 v[204:207], v252 offset:1024\n\t"
"v_cvt_pk_bf16_f32 v232, v232, v233\n\t"
"v_mfma_f32_16x16x32_bf16 v[152:155], a[12:15], a[192:195], v[152:155]\n\t"
"s_mov_b32 s20, 122880\n\t"
"ds_read_b128 a[24:27], v252 offset:13312\n\t"
"v_cvt_pk_bf16_f32 v233, v234, v235\n\t"
"v_mfma_f32_16x16x32_bf16 v[156:159], a[4:7], a[192:195], 0\n\t"
"ds_read_b128 v[212:215], v252 offset:2048\n\t"
"v_cvt_pk_bf16_f32 v234, v224, v225\n\t"
"v_mfma_f32_16x16x32_bf16 v[156:159], a[12:15], a[196:199], v[156:159]\n\t"
"ds_read_b128 a[32:35], v252 offset:14336\n\t"
"v_cvt_pk_bf16_f32 v235, v226, v227\n\t"
"v_mfma_f32_16x16x32_bf16 v[156:159], a[212:215], a[200:203], v[156:159]\n\t"
"ds_write_b128 v242, v[232:235] offset:30720\n\t"
"v_mfma_f32_16x16x32_bf16 v[160:163], a[12:15], a[200:203], 0\n\t"
"s_waitcnt vmcnt(34)\n\t"
"v_cvt_pk_bf16_f32 v236, v236, v237\n\t"
"v_cvt_pk_bf16_f32 v237, v238, v239\n\t"
"v_mfma_f32_16x16x32_bf16 v[160:163], a[4:7], a[196:199], v[160:163]\n\t"
"v_cvt_pk_bf16_f32 v238, v228, v229\n\t"
"v_cvt_pk_bf16_f32 v239, v230, v231\n\t"
"v_mfma_f32_16x16x32_bf16 v[160:163], a[212:215], a[204:207], v[160:163]\n\t"
"ds_write_b128 v242, v[236:239] offset:32256\n\t"
"v_mfma_f32_16x16x32_bf16 v[164:167], a[208:211], a[168:171], 0\n\t"
"buffer_load_dwordx4 a[212:215], off, s[80:83], s20 offset:0 sc1\n\t"
"v_mfma_f32_16x16x32_bf16 v[164:167], a[224:227], a[176:179], v[164:167]\n\t"
"v_mfma_f32_16x16x32_bf16 v[164:167], a[216:219], a[172:175], v[164:167]\n\t"
"buffer_store_dwordx4 a[0:3], v248, s[16:19], s29 offen offset:0 nt\n\t"
"v_mfma_f32_16x16x32_bf16 v[168:171], a[208:211], a[172:175], 0\n\t"
"v_mfma_f32_16x16x32_bf16 v[168:171], a[216:219], a[176:179], v[168:171]\n\t"
"buffer_load_dwordx4 v[240:243], off, s[80:83], s20 offset:1024 sc1\n\t"
"v_mfma_f32_16x16x32_bf16 v[168:171], a[224:227], a[180:183], v[168:171]\n\t"
"v_mfma_f32_16x16x32_bf16 v[172:175], a[224:227], a[184:187], 0\n\t"
"v_mfma_f32_16x16x32_bf16 v[172:175], a[216:219], a[180:183], v[172:175]\n\t"
"Lpg14_%=:\n\t"
"s_cbranch_vccnz Lvr1_%=\n\t"
"v_mfma_f32_16x16x32_bf16 v[172:175], a[208:211], a[176:179], v[172:175]\n\t"
"buffer_store_dwordx4 a[8:11], v248, s[16:19], s29 offen offset:128 nt\n\t"
"v_mfma_f32_16x16x32_bf16 v[176:179], a[208:211], a[180:183], 0\n\t"
"v_mfma_f32_16x16x32_bf16 v[176:179], a[216:219], a[184:187], v[176:179]\n\t"
"v_mfma_f32_16x16x32_bf16 v[176:179], a[224:227], a[188:191], v[176:179]\n\t"
"s_waitcnt lgkmcnt(0)\n\t"
"s_barrier\n\t"
"v_mfma_f32_16x16x32_bf16 v[180:183], a[224:227], a[192:195], 0\n\t"
"ds_read_b128 a[168:171], v244 offset:24576\n\t"
"v_mfma_f32_16x16x32_bf16 v[180:183], a[216:219], a[188:191], v[180:183]\n\t"
"ds_read_b128 a[172:175], v244 offset:25088\n\t"
"v_mfma_f32_16x16x32_bf16 v[180:183], a[208:211], a[184:187], v[180:183]\n\t"
"ds_read_b128 a[176:179], v244 offset:25600\n\t"
"v_mfma_f32_16x16x32_bf16 v[184:187], a[208:211], a[188:191], 0\n\t"
"ds_read_b128 a[180:183], v244 offset:26112\n\t"
"v_mfma_f32_16x16x32_bf16 v[184:187], a[216:219], a[192:195], v[184:187]\n\t"
"ds_read_b128 a[184:187], v244 offset:26624\n\t"
"v_mfma_f32_16x16x32_bf16 v[184:187], a[224:227], a[196:199], v[184:187]\n\t"
"ds_read_b128 a[188:191], v244 offset:27136\n\t"
"v_mfma_f32_16x16x32_bf16 v[188:191], a[224:227], a[200:203], 0\n\t"
"v_mfma_f32_16x16x32_bf16 v[188:191], a[216:219], a[196:199], v[188:191]\n\t"
"v_mfma_f32_16x16x32_bf16 v[188:191], a[208:211], a[192:195], v[188:191]\n\t"
"ds_read_b128 a[192:195], v244 offset:27648\n\t"
"v_mfma_f32_16x16x32_bf16 v[192:195], a[208:211], a[196:199], 0\n\t"
"buffer_load_dwordx4 a[208:211], off, s[80:83], s20 offset:2048 sc1\n\t"
"v_mfma_f32_16x16x32_bf16 v[192:195], a[216:219], a[200:203], v[192:195]\n\t"
"s_waitcnt vmcnt(34) lgkmcnt(4)\n\t"
"ds_read_b128 a[196:199], v244 offset:28160\n\t"
"s_mov_b32 s20, 135168\n\t"
"v_mfma_f32_16x16x32_bf16 v[192:195], a[224:227], a[204:207], v[192:195]\n\t"
"ds_write_b128 v249, a[44:47] offset:0\n\t"
"Lst19_%=:\n\t"
"v_mfma_f32_16x16x32_bf16 v[2:5], a[228:231], a[168:171], v[2:5]\n\t"
"buffer_load_dwordx4 a[216:219], off, s[80:83], s20 offset:0 sc1\n\t"
"v_mfma_f32_16x16x32_bf16 v[2:5], a[236:239], a[176:179], v[2:5]\n\t"
"s_waitcnt lgkmcnt(5)\n\t"
"ds_read_b128 a[200:203], v244 offset:28672\n\t"
"v_mfma_f32_16x16x32_bf16 v[2:5], a[232:235], a[172:175], v[2:5]\n\t"
"ds_write_b128 v249, a[60:63] offset:12288\n\t"
"v_mfma_f32_16x16x32_bf16 v[6:9], a[228:231], a[172:175], v[6:9]\n\t"
"buffer_load_dwordx4 a[224:227], off, s[80:83], s20 offset:1024 sc1\n\t"
"v_mfma_f32_16x16x32_bf16 v[6:9], a[232:235], a[176:179], v[6:9]\n\t"
"s_waitcnt lgkmcnt(6)\n\t"
"ds_read_b128 a[204:207], v244 offset:29184\n\t"
"v_mfma_f32_16x16x32_bf16 v[6:9], a[236:239], a[180:183], v[6:9]\n\t"
"buffer_store_dwordx4 a[40:43], v248, s[16:19], s29 offen offset:256 nt\n\t"
"ds_write_b64 v250, a[110:111] offset:0\n\t"
"s_add_u32 s29, s23, 786432\n\t"
"v_mfma_f32_16x16x32_bf16 v[10:13], a[236:239], a[184:187], v[10:13]\n\t"
"s_waitcnt lgkmcnt(7)\n\t"
"v_mfma_f32_16x16x32_bf16 v[10:13], a[232:235], a[180:183], v[10:13]\n\t"
"v_mfma_f32_16x16x32_bf16 v[10:13], a[228:231], a[176:179], v[10:13]\n\t"
"ds_write_b64 v250, a[126:127] offset:12288\n\t"
"v_mfma_f32_16x16x32_bf16 v[14:17], a[228:231], a[180:183], v[14:17]\n\t"
"s_waitcnt lgkmcnt(7)\n\t"
"v_mfma_f32_16x16x32_bf16 v[14:17], a[232:235], a[184:187], v[14:17]\n\t"
"v_mfma_f32_16x16x32_bf16 v[14:17], a[236:239], a[188:191], v[14:17]\n\t"
"buffer_store_dwordx4 v[196:199], v248, s[16:19], s29 offen offset:0 nt\n\t"
"ds_write_b64 v250, a[118:119] offset:6144\n\t"
"v_mfma_f32_16x16x32_bf16 v[18:21], a[236:239], a[192:195], v[18:21]\n\t"
"s_waitcnt lgkmcnt(7)\n\t"
"v_mfma_f32_16x16x32_bf16 v[18:21], a[232:235], a[188:191], v[18:21]\n\t"
"v_mfma_f32_16x16x32_bf16 v[18:21], a[228:231], a[184:187], v[18:21]\n\t"
"ds_write_b128 v249, a[52:55] offset:6144\n\t"
"v_mfma_f32_16x16x32_bf16 v[22:25], a[228:231], a[188:191], v[22:25]\n\t"
"s_waitcnt lgkmcnt(6)\n\t"
"v_mfma_f32_16x16x32_bf16 v[22:25], a[232:235], a[192:195], v[22:25]\n\t"
"v_mfma_f32_16x16x32_bf16 v[22:25], a[236:239], a[196:199], v[22:25]\n\t"
"ds_write_b128 v249, a[68:71] offset:18432\n\t"
"v_mfma_f32_16x16x32_bf16 v[26:29], a[236:239], a[200:203], v[26:29]\n\t"
"s_waitcnt lgkmcnt(5)\n\t"
"v_mfma_f32_16x16x32_bf16 v[26:29], a[232:235], a[196:199], v[26:29]\n\t"
"v_mfma_f32_16x16x32_bf16 v[26:29], a[228:231], a[192:195], v[26:29]\n\t"
"ds_write_b64 v250, a[134:135] offset:18432\n\t"
"v_mfma_f32_16x16x32_bf16 v[30:33], a[228:231], a[196:199], v[30:33]\n\t"
"buffer_load_dwordx4 a[228:231], off, s[80:83], s20 offset:2048 sc1\n\t"
"v_mfma_f32_16x16x32_bf16 v[30:33], a[232:235], a[200:203], v[30:33]\n\t"
"s_waitcnt vmcnt(35)\n\t"
"s_mov_b32 s20, 147456\n\t"
"v_mfma_f32_16x16x32_bf16 v[30:33], a[236:239], a[204:207], v[30:33]\n\t"
"buffer_store_dwordx4 v[204:207], v248, s[16:19], s29 offen offset:128 nt\n\t"
"v_mfma_f32_16x16x32_bf16 v[34:37], a[240:243], a[168:171], v[34:37]\n\t"
"v_mfma_f32_16x16x32_bf16 v[34:37], a[48:51], a[176:179], v[34:37]\n\t"
"buffer_load_dwordx4 a[232:235], off, s[80:83], s20 offset:0 sc1\n\t"
"v_mfma_f32_16x16x32_bf16 v[34:37], a[244:247], a[172:175], v[34:37]\n\t"
"v_mfma_f32_16x16x32_bf16 v[38:41], a[240:243], a[172:175], v[38:41]\n\t"
"buffer_load_dwordx4 a[236:239], off, s[80:83], s20 offset:1024 sc1\n\t"
"v_mfma_f32_16x16x32_bf16 v[38:41], a[244:247], a[176:179], v[38:41]\n\t"
"v_mfma_f32_16x16x32_bf16 v[38:41], a[48:51], a[180:183], v[38:41]\n\t"
"v_mfma_f32_16x16x32_bf16 v[42:45], a[48:51], a[184:187], v[42:45]\n\t"
"v_mfma_f32_16x16x32_bf16 v[42:45], a[244:247], a[180:183], v[42:45]\n\t"
"v_mfma_f32_16x16x32_bf16 v[42:45], a[240:243], a[176:179], v[42:45]\n\t"
"ds_read_b64_tr_b16 v[224:225], v246 offset:512\n\t"
"v_mfma_f32_16x16x32_bf16 v[46:49], a[240:243], a[180:183], v[46:49]\n\t"
"ds_read_b64_tr_b16 v[226:227], v246 offset:3584\n\t"
"v_mfma_f32_16x16x32_bf16 v[46:49], a[244:247], a[184:187], v[46:49]\n\t"
"ds_read_b64_tr_b16 v[228:229], v246 offset:6656\n\t"
"v_mfma_f32_16x16x32_bf16 v[46:49], a[48:51], a[188:191], v[46:49]\n\t"
"s_waitcnt lgkmcnt(3)\n\t"
"s_barrier\n\t"
"v_mfma_f32_16x16x32_bf16 v[50:53], a[48:51], a[192:195], v[50:53]\n\t"
"ds_read_b64_tr_b16 v[230:231], v246 offset:9728\n\t"
"v_mfma_f32_16x16x32_bf16 v[50:53], a[244:247], a[188:191], v[50:53]\n\t"
"ds_read_b64_tr_b16 v[232:233], v246 offset:12800\n\t"
"v_mfma_f32_16x16x32_bf16 v[50:53], a[240:243], a[184:187], v[50:53]\n\t"
"buffer_store_dwordx4 v[212:215], v248, s[16:19], s29 offen offset:256 nt\n\t"
"ds_read_b64_tr_b16 v[234:235], v246 offset:15872\n\t"
"ds_read_b64_tr_b16 v[236:237], v246 offset:20480\n\t"
"v_mfma_f32_16x16x32_bf16 v[54:57], a[240:243], a[188:191], v[54:57]\n\t"
"ds_read_b64_tr_b16 v[238:239], v246 offset:23552\n\t"
"v_mfma_f32_16x16x32_bf16 v[54:57], a[244:247], a[192:195], v[54:57]\n\t"
"v_mfma_f32_16x16x32_bf16 v[54:57], a[48:51], a[196:199], v[54:57]\n\t"
"v_mfma_f32_16x16x32_bf16 v[58:61], a[48:51], a[200:203], v[58:61]\n\t"
"s_add_u32 s29, s23, 1179648\n\t"
"v_mfma_f32_16x16x32_bf16 v[58:61], a[244:247], a[196:199], v[58:61]\n\t"
"v_mfma_f32_16x16x32_bf16 v[58:61], a[240:243], a[192:195], v[58:61]\n\t"
"v_mfma_f32_16x16x32_bf16 v[62:65], a[240:243], a[196:199], v[62:65]\n\t"
"buffer_load_dwordx4 a[240:243], off, s[80:83], s20 offset:2048 sc1\n\t"
"v_mfma_f32_16x16x32_bf16 v[62:65], a[244:247], a[200:203], v[62:65]\n\t"
"s_waitcnt lgkmcnt(4)\n\t"
"s_mov_b32 s20, 159744\n\t"
"v_mfma_f32_16x16x32_bf16 v[62:65], a[48:51], a[204:207], v[62:65]\n\t"
"s_waitcnt lgkmcnt(2)\n\t"
"v_mfma_f32_16x16x32_bf16 v[224:227], v[224:227], v[220:223], 0\n\t"
"buffer_load_dwordx4 a[244:247], off, s[80:83], s20 offset:0 sc1\n\t"
"v_mfma_f32_16x16x32_bf16 v[228:231], v[228:231], v[220:223], 0\n\t"
"buffer_store_dwordx4 a[16:19], v248, s[16:19], s29 offen offset:0 nt\n\t"
"v_mfma_f32_16x16x32_bf16 v[232:235], v[232:235], v[220:223], 0\n\t"
"s_waitcnt vmcnt(38) lgkmcnt(0)\n\t"
"v_mfma_f32_16x16x32_bf16 v[236:239], v[236:239], v[220:223], 0\n\t"
"v_mfma_f32_16x16x32_bf16 v[66:69], a[56:59], a[168:171], v[66:69]\n\t"
"ds_read_b128 a[168:171], v244 offset:33792\n\t"
"v_mfma_f32_16x16x32_bf16 v[66:69], a[144:147], a[176:179], v[66:69]\n\t"
"v_cvt_pk_bf16_f32 v224, v224, v225\n\t"
"v_cvt_pk_bf16_f32 v225, v226, v227\n\t"
"v_mfma_f32_16x16x32_bf16 v[66:69], a[64:67], a[172:175], v[66:69]\n\t"
"v_cvt_pk_bf16_f32 v228, v228, v229\n\t"
"v_cvt_pk_bf16_f32 v229, v230, v231\n\t"
"v_mfma_f32_16x16x32_bf16 v[70:73], a[56:59], a[172:175], v[70:73]\n\t"
"ds_read_b128 a[172:175], v244 offset:34304\n\t"
"v_cvt_pk_bf16_f32 v232, v232, v233\n\t"
"v_mfma_f32_16x16x32_bf16 v[70:73], a[64:67], a[176:179], v[70:73]\n\t"
"v_cvt_pk_bf16_f32 v233, v234, v235\n\t"
"v_cvt_pk_bf16_f32 v236, v236, v237\n\t"
"v_mfma_f32_16x16x32_bf16 v[70:73], a[144:147], a[180:183], v[70:73]\n\t"
"v_cvt_pk_bf16_f32 v237, v238, v239\n\t"
"v_mfma_f32_16x16x32_bf16 v[74:77], a[144:147], a[184:187], v[74:77]\n\t"
"v_mfma_f32_16x16x32_bf16 v[74:77], a[64:67], a[180:183], v[74:77]\n\t"
"v_mfma_f32_16x16x32_bf16 v[74:77], a[56:59], a[176:179], v[74:77]\n\t"
"ds_read_b128 a[176:179], v244 offset:34816\n\t"
"v_mfma_f32_16x16x32_bf16 v[78:81], a[56:59], a[180:183], v[78:81]\n\t"
"ds_read_b128 a[180:183], v244 offset:35328\n\t"
"v_mfma_f32_16x16x32_bf16 v[78:81], a[64:67], a[184:187], v[78:81]\n\t"
"ds_read_b128 v[196:199], v252 offset:0\n\t"
"v_mfma_f32_16x16x32_bf16 v[78:81], a[144:147], a[188:191], v[78:81]\n\t"
"buffer_store_dwordx4 a[24:27], v248, s[16:19], s29 offen offset:128 nt\n\t"
"ds_read_b128 a[40:43], v252 offset:12288\n\t"
"ds_read_b128 v[204:207], v252 offset:1024\n\t"
"v_mfma_f32_16x16x32_bf16 v[82:85], a[144:147], a[192:195], v[82:85]\n\t"
"ds_read_b128 a[72:75], v252 offset:13312\n\t"
"v_mfma_f32_16x16x32_bf16 v[82:85], a[64:67], a[188:191], v[82:85]\n\t"
"ds_read_b128 a[8:11], v252 offset:2048\n\t"
"v_mfma_f32_16x16x32_bf16 v[82:85], a[56:59], a[184:187], v[82:85]\n\t"
"ds_read_b128 a[184:187], v244 offset:35840\n\t"
"v_mfma_f32_16x16x32_bf16 v[86:89], a[56:59], a[188:191], v[86:89]\n\t"
"ds_read_b128 a[188:191], v244 offset:36352\n\t"
"v_mfma_f32_16x16x32_bf16 v[86:89], a[64:67], a[192:195], v[86:89]\n\t"
"ds_read_b128 a[104:107], v252 offset:14336\n\t"
"v_mfma_f32_16x16x32_bf16 v[86:89], a[144:147], a[196:199], v[86:89]\n\t"
"v_mfma_f32_16x16x32_bf16 v[90:93], a[144:147], a[200:203], v[90:93]\n\t"
"v_mfma_f32_16x16x32_bf16 v[90:93], a[64:67], a[196:199], v[90:93]\n\t"
"v_mfma_f32_16x16x32_bf16 v[90:93], a[56:59], a[192:195], v[90:93]\n\t"
"ds_read_b128 a[192:195], v244 offset:36864\n\t"
"v_mfma_f32_16x16x32_bf16 v[94:97], a[56:59], a[196:199], v[94:97]\n\t"
"s_waitcnt vmcnt(34) lgkmcnt(10)\n\t"
"ds_read_b128 a[196:199], v244 offset:37376\n\t"
"v_mfma_f32_16x16x32_bf16 v[94:97], a[64:67], a[200:203], v[94:97]\n\t"
"ds_read_b128 a[200:203], v244 offset:37888\n\t"
"v_mfma_f32_16x16x32_bf16 v[94:97], a[144:147], a[204:207], v[94:97]\n\t"
"ds_read_b128 a[204:207], v244 offset:38400\n\t"
"v_mfma_f32_16x16x32_bf16 v[100:103], a[20:23], a[168:171], v[100:103]\n\t"
"s_waitcnt lgkmcnt(12)\n\t"
"v_mfma_f32_16x16x32_bf16 v[100:103], a[248:251], a[172:175], v[100:103]\n\t"
"v_mfma_f32_16x16x32_bf16 v[100:103], a[252:255], a[176:179], v[100:103]\n\t"
"buffer_store_dwordx4 a[32:35], v248, s[16:19], s29 offen offset:256 nt\n\t"
"v_mfma_f32_16x16x32_bf16 v[104:107], a[252:255], a[180:183], v[104:107]\n\t"
"s_waitcnt lgkmcnt(6)\n\t"
"v_mfma_f32_16x16x32_bf16 v[104:107], a[248:251], a[176:179], v[104:107]\n\t"
"v_mfma_f32_16x16x32_bf16 v[104:107], a[20:23], a[172:175], v[104:107]\n\t"
"s_waitcnt lgkmcnt(0)\n\t"
"s_barrier\n\t"
"v_mfma_f32_16x16x32_bf16 v[108:111], a[20:23], a[176:179], v[108:111]\n\t"
"s_add_u32 s29, s23, 1152\n\t"
"v_fma_f32 v0, v2, s52, v100\n\t"
"v_mfma_f32_16x16x32_bf16 v[108:111], a[248:251], a[180:183], v[108:111]\n\t"
"v_fma_f32 v1, v3, s52, v101\n\t"
"v_mfma_f32_16x16x32_bf16 v[108:111], a[252:255], a[184:187], v[108:111]\n\t"
"ds_write_b64 v245, v[224:225] offset:24576\n\t"
"v_mfma_f32_16x16x32_bf16 v[112:115], a[252:255], a[188:191], v[112:115]\n\t"
"ds_read_b64_tr_b16 v[224:225], v246 offset:26624\n\t"
"v_mfma_f32_16x16x32_bf16 v[112:115], a[248:251], a[184:187], v[112:115]\n\t"
"ds_read_b64_tr_b16 v[226:227], v246 offset:29696\n\t"
"v_mfma_f32_16x16x32_bf16 v[112:115], a[20:23], a[180:183], v[112:115]\n\t"
"ds_write_b64 v245, v[228:229] offset:25088\n\t"
"v_mfma_f32_16x16x32_bf16 v[116:119], a[20:23], a[184:187], v[116:119]\n\t"
"ds_read_b64_tr_b16 v[228:229], v246 offset:32768\n\t"
"v_fma_f32 v212, -v10, s56, -v108\n\t"
"v_mfma_f32_16x16x32_bf16 v[116:119], a[248:251], a[188:191], v[116:119]\n\t"
"ds_read_b64_tr_b16 v[230:231], v246 offset:35840\n\t"
"v_fma_f32 v213, -v11, s56, -v109\n\t"
"v_mfma_f32_16x16x32_bf16 v[116:119], a[252:255], a[192:195], v[116:119]\n\t"
"ds_write_b64 v245, v[232:233] offset:25600\n\t"
"v_mfma_f32_16x16x32_bf16 v[120:123], a[248:251], a[192:195], v[120:123]\n\t"
"s_waitcnt lgkmcnt(4)\n\t"
"ds_read_b64_tr_b16 v[232:233], v246 offset:40448\n\t"
"v_fma_f32 v214, -v12, s56, -v110\n\t"
"v_mfma_f32_16x16x32_bf16 v[120:123], a[252:255], a[196:199], v[120:123]\n\t"
"ds_read_b64_tr_b16 v[234:235], v246 offset:43520\n\t"
"v_fma_f32 v215, -v13, s56, -v111\n\t"
"v_mfma_f32_16x16x32_bf16 v[120:123], a[20:23], a[188:191], v[120:123]\n\t"
"ds_write_b64 v245, v[236:237] offset:26112\n\t"
"v_mfma_f32_16x16x32_bf16 v[224:227], v[224:227], v[220:223], 0\n\t"
"buffer_store_dwordx4 v[196:199], v248, s[16:19], s29 offen offset:0 nt\n\t"
"ds_read_b64_tr_b16 v[236:237], v246 offset:46592\n\t"
"ds_read_b64_tr_b16 v[238:239], v246 offset:49664\n\t"
"v_fma_f32 v98, -v14, s56, -v112\n\t"
"v_mfma_f32_16x16x32_bf16 v[124:127], a[20:23], a[192:195], v[124:127]\n\t"
"v_fma_f32 v99, -v15, s56, -v113\n\t"
"v_accvgpr_write_b32 a0, v98\n\t"
"v_mfma_f32_16x16x32_bf16 v[124:127], a[252:255], a[200:203], v[124:127]\n\t"
"v_fma_f32 v196, -v2, s56, -v100\n\t"
"v_fma_f32 v197, -v3, s56, -v101\n\t"
"v_mfma_f32_16x16x32_bf16 v[124:127], a[248:251], a[196:199], v[124:127]\n\t"
"ds_write_b128 v249, a[76:79] offset:0\n\t"
"v_mfma_f32_16x16x32_bf16 v[128:131], a[20:23], a[196:199], v[128:131]\n\t"
"s_waitcnt lgkmcnt(7)\n\t"
"v_fma_f32 v198, -v4, s56, -v102\n\t"
"v_fma_f32 v199, -v5, s56, -v103\n\t"
"v_mfma_f32_16x16x32_bf16 v[128:131], a[248:251], a[200:203], v[128:131]\n\t"
"s_waitcnt lgkmcnt(4)\n\t"
"v_cvt_pk_bf16_f32 v224, v224, v225\n\t"
"v_cvt_pk_bf16_f32 v225, v226, v227\n\t"
"v_mfma_f32_16x16x32_bf16 v[128:131], a[252:255], a[204:207], v[128:131]\n\t"
"s_waitcnt lgkmcnt(1)\n\t"
"ds_write_b128 v249, a[92:95] offset:12288\n\t"
"v_mfma_f32_16x16x32_bf16 v[228:231], v[228:231], v[220:223], 0\n\t"
"buffer_load_dwordx4 a[248:251], off, s[80:83], s20 offset:1024 sc1\n\t"
"v_mfma_f32_16x16x32_bf16 v[232:235], v[232:235], v[220:223], 0\n\t"
"buffer_load_dwordx4 a[252:255], off, s[80:83], s20 offset:2048 sc1\n\t"
"v_mfma_f32_16x16x32_bf16 v[236:239], v[236:239], v[220:223], 0\n\t"
"s_waitcnt vmcnt(33)\n\t"
"ds_write_b64 v250, a[142:143] offset:0\n\t"
"v_mfma_f32_16x16x32_bf16 v[132:135], a[220:223], a[168:171], v[132:135]\n\t"
"v_fma_f32 v98, -v16, s56, -v114\n\t"
"v_accvgpr_write_b32 a1, v99\n\t"
"v_mfma_f32_16x16x32_bf16 v[132:135], a[152:155], a[172:175], v[132:135]\n\t"
"v_fma_f32 v99, -v17, s56, -v115\n\t"
"v_accvgpr_write_b32 a2, v98\n\t"
"v_mfma_f32_16x16x32_bf16 v[132:135], a[160:163], a[176:179], v[132:135]\n\t"
"ds_write_b64 v250, a[158:159] offset:12288\n\t"
"v_mfma_f32_16x16x32_bf16 v[136:139], a[160:163], a[180:183], v[136:139]\n\t"
"v_cvt_pk_bf16_f32 v228, v228, v229\n\t"
"v_cvt_pk_bf16_f32 v229, v230, v231\n\t"
"v_mfma_f32_16x16x32_bf16 v[136:139], a[152:155], a[176:179], v[136:139]\n\t"
"v_cvt_pk_bf16_f32 v232, v232, v233\n\t"
"v_cvt_pk_bf16_f32 v233, v234, v235\n\t"
"v_mfma_f32_16x16x32_bf16 v[136:139], a[220:223], a[172:175], v[136:139]\n\t"
"ds_write_b64 v245, v[224:225] offset:26624\n\t"
"buffer_store_dwordx4 v[204:207], v248, s[16:19], s29 offen offset:128 nt\n\t"
"ds_read_b64_tr_b16 v[224:225], v246 offset:52736\n\t"
"ds_read_b64_tr_b16 v[226:227], v246 offset:55808\n\t"
"v_cvt_pk_bf16_f32 v236, v236, v237\n\t"
"v_mfma_f32_16x16x32_bf16 v[140:143], a[220:223], a[176:179], v[140:143]\n\t"
"v_cvt_pk_bf16_f32 v237, v238, v239\n\t"
"v_fma_f32 v204, -v6, s56, -v104\n\t"
"v_mfma_f32_16x16x32_bf16 v[140:143], a[152:155], a[180:183], v[140:143]\n\t"
"s_mov_b32 s20, 172032\n\t"
"v_fma_f32 v205, -v7, s56, -v105\n\t"
"v_fma_f32 v206, -v8, s56, -v106\n\t"
"v_mfma_f32_16x16x32_bf16 v[140:143], a[160:163], a[184:187], v[140:143]\n\t"
"ds_write_b64 v245, v[228:229] offset:27136\n\t"
"v_mfma_f32_16x16x32_bf16 v[144:147], a[152:155], a[184:187], v[144:147]\n\t"
"v_fma_f32 v207, -v9, s56, -v107\n\t"
"v_accvgpr_write_b32 a3, v99\n\t"
"v_mfma_f32_16x16x32_bf16 v[144:147], a[160:163], a[188:191], v[144:147]\n\t"
"s_waitcnt lgkmcnt(1)\n\t"
"v_mfma_f32_16x16x32_bf16 v[144:147], a[220:223], a[180:183], v[144:147]\n\t"
"ds_write_b64 v245, v[232:233] offset:27648\n\t"
"v_mfma_f32_16x16x32_bf16 v[224:227], v[224:227], v[220:223], 0\n\t"
"ds_write_b64 v245, v[236:237] offset:28160\n\t"
"v_mfma_f32_16x16x32_bf16 v[148:151], a[220:223], a[184:187], v[148:151]\n\t"
"buffer_load_dwordx4 v[236:239], off, s[80:83], s20 offset:0 sc1\n\t"
"v_mfma_f32_16x16x32_bf16 v[148:151], a[160:163], a[192:195], v[148:151]\n\t"
"v_mfma_f32_16x16x32_bf16 v[148:151], a[152:155], a[188:191], v[148:151]\n\t"
"ds_write_b128 v249, a[84:87] offset:6144\n\t"
"v_mfma_f32_16x16x32_bf16 v[152:155], a[220:223], a[188:191], v[152:155]\n\t"
"v_mfma_f32_16x16x32_bf16 v[152:155], a[152:155], a[192:195], v[152:155]\n\t"
"v_cvt_pk_bf16_f32 v224, v224, v225\n\t"
"v_cvt_pk_bf16_f32 v225, v226, v227\n\t"
"v_mfma_f32_16x16x32_bf16 v[152:155], a[160:163], a[196:199], v[152:155]\n\t"
"ds_write_b128 v249, a[100:103] offset:18432\n\t"
"buffer_store_dwordx4 a[8:11], v248, s[16:19], s29 offen offset:256 nt\n\t"
"s_add_u32 s29, s23, 394368\n\t"
"v_mfma_f32_16x16x32_bf16 v[156:159], a[160:163], a[200:203], v[156:159]\n\t"
"v_fma_f32 v98, -v18, s56, -v116\n\t"
"v_fma_f32 v99, -v19, s56, -v117\n\t"
"v_mfma_f32_16x16x32_bf16 v[156:159], a[152:155], a[196:199], v[156:159]\n\t"
"v_accvgpr_write_b32 a8, v98\n\t"
"v_fma_f32 v98, -v20, s56, -v118\n\t"
"v_mfma_f32_16x16x32_bf16 v[156:159], a[220:223], a[192:195], v[156:159]\n\t"
"ds_write_b64 v250, a[150:151] offset:6144\n\t"
"v_mfma_f32_16x16x32_bf16 v[160:163], a[220:223], a[196:199], v[160:163]\n\t"
"buffer_load_dwordx4 a[220:223], off, s[80:83], s20 offset:1024 sc1\n\t"
"v_mfma_f32_16x16x32_bf16 v[160:163], a[152:155], a[200:203], v[160:163]\n\t"
"s_waitcnt vmcnt(32)\n\t"
"v_accvgpr_write_b32 a9, v99\n\t"
"v_fma_f32 v99, -v21, s56, -v119\n\t"
"v_mfma_f32_16x16x32_bf16 v[160:163], a[160:163], a[204:207], v[160:163]\n\t"
"ds_write_b64 v250, a[166:167] offset:18432\n\t"
"v_mfma_f32_16x16x32_bf16 v[164:167], a[28:31], a[168:171], v[164:167]\n\t"
"v_accvgpr_write_b32 a10, v98\n\t"
"v_accvgpr_write_b32 a11, v99\n\t"
"v_mfma_f32_16x16x32_bf16 v[164:167], v[200:203], a[176:179], v[164:167]\n\t"
"v_fma_f32 v98, -v22, s56, -v120\n\t"
"v_mfma_f32_16x16x32_bf16 v[164:167], a[112:115], a[172:175], v[164:167]\n\t"
"ds_write_b64 v245, v[224:225] offset:28672\n\t"
"s_waitcnt lgkmcnt(0)\n\t"
"s_barrier\n\t"
"v_mfma_f32_16x16x32_bf16 v[168:171], a[28:31], a[172:175], v[168:171]\n\t"
"ds_read_b128 a[168:171], v244 offset:24576\n\t"
"v_mfma_f32_16x16x32_bf16 v[168:171], a[112:115], a[176:179], v[168:171]\n\t"
"ds_read_b128 a[172:175], v244 offset:25088\n\t"
"v_fma_f32 v99, -v23, s56, -v121\n\t"
"v_mfma_f32_16x16x32_bf16 v[168:171], v[200:203], a[180:183], v[168:171]\n\t"
"buffer_store_dwordx4 a[40:43], v248, s[16:19], s29 offen offset:0 nt\n\t"
"v_accvgpr_write_b32 a16, v98\n\t"
"v_fma_f32 v98, -v24, s56, -v122\n\t"
"v_accvgpr_write_b32 a17, v99\n\t"
"v_fma_f32 v99, -v25, s56, -v123\n\t"
"v_mfma_f32_16x16x32_bf16 v[172:175], v[200:203], a[184:187], v[172:175]\n\t"
"v_accvgpr_write_b32 a18, v98\n\t"
"v_accvgpr_write_b32 a19, v99\n\t"
"v_mfma_f32_16x16x32_bf16 v[172:175], a[112:115], a[180:183], v[172:175]\n\t"
"v_fma_f32 v98, -v26, s56, -v124\n\t"
"v_fma_f32 v99, -v27, s56, -v125\n\t"
"v_mfma_f32_16x16x32_bf16 v[172:175], a[28:31], a[176:179], v[172:175]\n\t"
"ds_read_b128 a[176:179], v244 offset:25600\n\t"
"v_accvgpr_write_b32 a24, v98\n\t"
"v_mfma_f32_16x16x32_bf16 v[176:179], a[28:31], a[180:183], v[176:179]\n\t"
"ds_read_b128 a[180:183], v244 offset:26112\n\t"
"v_fma_f32 v98, -v28, s56, -v126\n\t"
"v_mfma_f32_16x16x32_bf16 v[176:179], a[112:115], a[184:187], v[176:179]\n\t"
"v_accvgpr_write_b32 a25, v99\n\t"
"v_fma_f32 v99, -v29, s56, -v127\n\t"
"v_mfma_f32_16x16x32_bf16 v[176:179], v[200:203], a[188:191], v[176:179]\n\t"
"v_accvgpr_write_b32 a26, v98\n\t"
"v_accvgpr_write_b32 a27, v99\n\t"
"v_mfma_f32_16x16x32_bf16 v[180:183], v[200:203], a[192:195], v[180:183]\n\t"
"v_fma_f32 v98, -v30, s56, -v128\n\t"
"v_fma_f32 v99, -v31, s56, -v129\n\t"
"v_mfma_f32_16x16x32_bf16 v[180:183], a[112:115], a[188:191], v[180:183]\n\t"
"v_accvgpr_write_b32 a32, v98\n\t"
"v_fma_f32 v98, -v32, s56, -v130\n\t"
"v_mfma_f32_16x16x32_bf16 v[180:183], a[28:31], a[184:187], v[180:183]\n\t"
"ds_read_b128 a[184:187], v244 offset:26624\n\t"
"v_accvgpr_write_b32 a33, v99\n\t"
"v_mfma_f32_16x16x32_bf16 v[184:187], a[28:31], a[188:191], v[184:187]\n\t"
"ds_read_b128 a[188:191], v244 offset:27136\n\t"
"v_fma_f32 v99, -v33, s56, -v131\n\t"
"v_mfma_f32_16x16x32_bf16 v[184:187], a[112:115], a[192:195], v[184:187]\n\t"
"v_accvgpr_write_b32 a34, v98\n\t"
"v_accvgpr_write_b32 a35, v99\n\t"
"v_mfma_f32_16x16x32_bf16 v[184:187], v[200:203], a[196:199], v[184:187]\n\t"
"buffer_store_dwordx4 a[72:75], v248, s[16:19], s29 offen offset:128 nt\n\t"
"v_fma_f32 v98, -v34, s56, -v132\n\t"
"v_fma_f32 v99, -v35, s56, -v133\n\t"
"v_accvgpr_write_b32 a40, v98\n\t"
"v_fma_f32 v98, -v36, s56, -v134\n\t"
"v_mfma_f32_16x16x32_bf16 v[188:191], v[200:203], a[200:203], v[188:191]\n\t"
"ds_read_b128 a[136:139], v252 offset:0\n\t"
"v_xor_b32 v246, 0xc0, v246\n\t"
"v_mfma_f32_16x16x32_bf16 v[188:191], a[112:115], a[196:199], v[188:191]\n\t"
"v_accvgpr_write_b32 a41, v99\n\t"
"v_fma_f32 v99, -v37, s56, -v135\n\t"
"v_mfma_f32_16x16x32_bf16 v[188:191], a[28:31], a[192:195], v[188:191]\n\t"
"ds_read_b128 a[192:195], v244 offset:27648\n\t"
"v_accvgpr_write_b32 a42, v98\n\t"
"v_mfma_f32_16x16x32_bf16 v[192:195], a[28:31], a[196:199], v[192:195]\n\t"
"s_waitcnt vmcnt(29) lgkmcnt(5)\n\t"
"ds_read_b128 a[196:199], v244 offset:28160\n\t"
"v_accvgpr_write_b32 a43, v99\n\t"
"v_mfma_f32_16x16x32_bf16 v[192:195], a[112:115], a[200:203], v[192:195]\n\t"
"ds_read_b128 a[200:203], v244 offset:28672\n\t"
"v_fma_f32 v98, -v38, s56, -v136\n\t"
"v_mfma_f32_16x16x32_bf16 v[192:195], v[200:203], a[204:207], v[192:195]\n\t"
"ds_read_b128 a[204:207], v244 offset:29184\n\t"
"v_fma_f32 v99, -v39, s56, -v137\n\t"
"Lst20_%=:\n\t"
"v_mfma_f32_16x16x32_bf16 v[196:199], a[36:39], a[168:171], v[196:199]\n\t"
"s_waitcnt lgkmcnt(7)\n\t"
"v_accvgpr_write_b32 a48, v98\n\t"
"v_fma_f32 v98, -v40, s56, -v138\n\t"
"v_mfma_f32_16x16x32_bf16 v[196:199], v[216:219], a[176:179], v[196:199]\n\t"
"v_accvgpr_write_b32 a49, v99\n\t"
"v_fma_f32 v99, -v41, s56, -v139\n\t"
"v_mfma_f32_16x16x32_bf16 v[196:199], v[208:211], a[172:175], v[196:199]\n\t"
"v_accvgpr_write_b32 a50, v98\n\t"
"v_accvgpr_write_b32 a51, v99\n\t"
"v_mfma_f32_16x16x32_bf16 v[204:207], a[36:39], a[172:175], v[204:207]\n\t"
"s_waitcnt lgkmcnt(6)\n\t"
"v_fma_f32 v98, -v42, s56, -v140\n\t"
"v_fma_f32 v99, -v43, s56, -v141\n\t"
"v_mfma_f32_16x16x32_bf16 v[204:207], v[208:211], a[176:179], v[204:207]\n\t"
"v_accvgpr_write_b32 a56, v98\n\t"
"v_fma_f32 v98, -v44, s56, -v142\n\t"
"v_mfma_f32_16x16x32_bf16 v[204:207], v[216:219], a[180:183], v[204:207]\n\t"
"buffer_store_dwordx4 a[104:107], v248, s[16:19], s29 offen offset:256 nt\n\t"
"v_accvgpr_write_b32 a57, v99\n\t"
"v_fma_f32 v99, -v45, s56, -v143\n\t"
"v_accvgpr_write_b32 a58, v98\n\t"
"v_accvgpr_write_b32 a59, v99\n\t"
"v_mfma_f32_16x16x32_bf16 v[212:215], v[216:219], a[184:187], v[212:215]\n\t"
"s_waitcnt lgkmcnt(5)\n\t"
"v_fma_f32 v98, -v46, s56, -v144\n\t"
"v_fma_f32 v99, -v47, s56, -v145\n\t"
"v_mfma_f32_16x16x32_bf16 v[212:215], v[208:211], a[180:183], v[212:215]\n\t"
"s_add_u32 s29, s23, 787584\n\t"
"ds_read_b128 v[200:203], v252 offset:1024\n\t"
"v_accvgpr_write_b32 a64, v98\n\t"
"v_mfma_f32_16x16x32_bf16 v[212:215], a[36:39], a[176:179], v[212:215]\n\t"
"ds_read_b64_tr_b16 v[228:229], v246 offset:512\n\t"
"v_fma_f32 v98, -v48, s56, -v146\n\t"
"v_mfma_f32_16x16x32_bf16 a[0:3], a[36:39], a[180:183], a[0:3]\n\t"
"s_waitcnt lgkmcnt(5)\n\t"
"ds_read_b64_tr_b16 v[230:231], v246 offset:3584\n\t"
"v_accvgpr_write_b32 a65, v99\n\t"
"v_mfma_f32_16x16x32_bf16 a[0:3], v[208:211], a[184:187], a[0:3]\n\t"
"ds_read_b64_tr_b16 v[232:233], v246 offset:6656\n\t"
"v_fma_f32 v99, -v49, s56, -v147\n\t"
"v_mfma_f32_16x16x32_bf16 a[0:3], v[216:219], a[188:191], a[0:3]\n\t"
"ds_read_b64_tr_b16 v[234:235], v246 offset:9728\n\t"
"v_accvgpr_write_b32 a66, v98\n\t"
"v_mfma_f32_16x16x32_bf16 a[8:11], v[216:219], a[192:195], a[8:11]\n\t"
"s_waitcnt lgkmcnt(7)\n\t"
"v_accvgpr_write_b32 a67, v99\n\t"
"v_fma_f32 v98, -v50, s56, -v148\n\t"
"v_mfma_f32_16x16x32_bf16 a[8:11], v[208:211], a[188:191], a[8:11]\n\t"
"v_fma_f32 v99, -v51, s56, -v149\n\t"
"v_accvgpr_write_b32 a72, v98\n\t"
"v_mfma_f32_16x16x32_bf16 a[8:11], a[36:39], a[184:187], a[8:11]\n\t"
"v_fma_f32 v98, -v52, s56, -v150\n\t"
"v_accvgpr_write_b32 a73, v99\n\t"
"v_mfma_f32_16x16x32_bf16 a[16:19], a[36:39], a[188:191], a[16:19]\n\t"
"s_waitcnt lgkmcnt(6)\n\t"
"v_fma_f32 v99, -v53, s56, -v151\n\t"
"v_accvgpr_write_b32 a74, v98\n\t"
"v_mfma_f32_16x16x32_bf16 a[16:19], v[208:211], a[192:195], a[16:19]\n\t"
"ds_read_b128 a[44:47], v252 offset:12288\n\t"
"v_accvgpr_write_b32 a75, v99\n\t"
"v_mfma_f32_16x16x32_bf16 a[16:19], v[216:219], a[196:199], a[16:19]\n\t"
"v_fma_f32 v98, -v54, s56, -v152\n\t"
"v_fma_f32 v99, -v55, s56, -v153\n\t"
"v_mfma_f32_16x16x32_bf16 a[24:27], v[216:219], a[200:203], a[24:27]\n\t"
"s_waitcnt lgkmcnt(6)\n\t"
"ds_read_b128 a[76:79], v252 offset:13312\n\t"
"v_accvgpr_write_b32 a80, v98\n\t"
"v_mfma_f32_16x16x32_bf16 a[24:27], v[208:211], a[196:199], a[24:27]\n\t"
"v_fma_f32 v98, -v56, s56, -v154\n\t"
"v_accvgpr_write_b32 a81, v99\n\t"
"v_mfma_f32_16x16x32_bf16 a[24:27], a[36:39], a[192:195], a[24:27]\n\t"
"buffer_store_dwordx4 a[136:139], v248, s[16:19], s29 offen offset:0 nt\n\t"
"ds_read_b128 a[12:15], v252 offset:2048\n\t"
"ds_read_b128 a[108:111], v252 offset:14336\n\t"
"v_fma_f32 v99, -v57, s56, -v155\n\t"
"v_mfma_f32_16x16x32_bf16 a[32:35], a[36:39], a[196:199], a[32:35]\n\t"
"s_waitcnt lgkmcnt(6)\n\t"
"v_accvgpr_write_b32 a82, v98\n\t"
"v_accvgpr_write_b32 a83, v99\n\t"
"v_mfma_f32_16x16x32_bf16 a[32:35], v[208:211], a[200:203], a[32:35]\n\t"
"s_waitcnt lgkmcnt(4)\n\t"
"v_fma_f32 v98, -v58, s56, -v156\n\t"
"v_fma_f32 v99, -v59, s56, -v157\n\t"
"v_mfma_f32_16x16x32_bf16 a[32:35], v[216:219], a[204:207], a[32:35]\n\t"
"s_waitcnt vmcnt(25)\n\t"
"v_accvgpr_write_b32 a88, v98\n\t"
"v_fma_f32 v98, -v60, s56, -v158\n\t"
"v_mfma_f32_16x16x32_bf16 v[228:231], v[228:231], v[220:223], 0\n\t"
"v_accvgpr_write_b32 a89, v99\n\t"
"v_fma_f32 v99, -v61, s56, -v159\n\t"
"v_mfma_f32_16x16x32_bf16 v[232:235], v[232:235], v[220:223], 0\n\t"
"v_accvgpr_write_b32 a90, v98\n\t"
"v_accvgpr_write_b32 a91, v99\n\t"
"v_mfma_f32_16x16x32_bf16 a[40:43], a[212:215], a[168:171], a[40:43]\n\t"
"v_fma_f32 v98, -v62, s56, -v160\n\t"
"v_fma_f32 v99, -v63, s56, -v161\n\t"
"v_mfma_f32_16x16x32_bf16 a[40:43], a[208:211], a[176:179], a[40:43]\n\t"
"v_accvgpr_write_b32 a96, v98\n\t"
"v_fma_f32 v98, -v64, s56, -v162\n\t"
"v_mfma_f32_16x16x32_bf16 a[40:43], v[240:243], a[172:175], a[40:43]\n\t"
"v_accvgpr_write_b32 a97, v99\n\t"
"v_fma_f32 v99, -v65, s56, -v163\n\t"
"v_mfma_f32_16x16x32_bf16 a[48:51], a[212:215], a[172:175], a[48:51]\n\t"
"v_cvt_pk_bf16_f32 v228, v228, v229\n\t"
"v_cvt_pk_bf16_f32 v229, v230, v231\n\t"
"v_mfma_f32_16x16x32_bf16 a[48:51], v[240:243], a[176:179], a[48:51]\n\t"
"v_cvt_pk_bf16_f32 v230, v232, v233\n\t"
"v_cvt_pk_bf16_f32 v231, v234, v235\n\t"
"v_mfma_f32_16x16x32_bf16 a[48:51], a[208:211], a[180:183], a[48:51]\n\t"
"buffer_store_dwordx4 v[200:203], v248, s[16:19], s29 offen offset:128 nt\n\t"
"v_accvgpr_write_b32 a98, v98\n\t"
"v_accvgpr_write_b32 a99, v99\n\t"
"v_fma_f32 v98, -v66, s56, -v164\n\t"
"v_fma_f32 v99, -v67, s56, -v165\n\t"
"v_mfma_f32_16x16x32_bf16 a[56:59], a[208:211], a[184:187], a[56:59]\n\t"
"v_accvgpr_write_b32 a104, v98\n\t"
"v_fma_f32 v98, -v68, s56, -v166\n\t"
"v_mfma_f32_16x16x32_bf16 a[56:59], v[240:243], a[180:183], a[56:59]\n\t"
"v_accvgpr_write_b32 a105, v99\n\t"
"v_fma_f32 v99, -v69, s56, -v167\n\t"
"v_mfma_f32_16x16x32_bf16 a[56:59], a[212:215], a[176:179], a[56:59]\n\t"
"ds_read_b64_tr_b16 v[224:225], v246 offset:12800\n\t"
"v_accvgpr_write_b32 a106, v98\n\t"
"v_mfma_f32_16x16x32_bf16 a[64:67], a[212:215], a[180:183], a[64:67]\n\t"
"s_waitcnt lgkmcnt(2)\n\t"
"ds_read_b64_tr_b16 v[226:227], v246 offset:15872\n\t"
"v_accvgpr_write_b32 a107, v99\n\t"
"v_mfma_f32_16x16x32_bf16 a[64:67], v[240:243], a[184:187], a[64:67]\n\t"
"ds_read_b64_tr_b16 v[232:233], v246 offset:20480\n\t"
"v_mfma_f32_16x16x32_bf16 a[64:67], a[208:211], a[188:191], a[64:67]\n\t"
"s_waitcnt lgkmcnt(3)\n\t"
"s_barrier\n\t"
"v_mfma_f32_16x16x32_bf16 a[72:75], a[208:211], a[192:195], a[72:75]\n\t"
"ds_read_b64_tr_b16 v[234:235], v246 offset:23552\n\t"
"v_mfma_f32_16x16x32_bf16 a[72:75], v[240:243], a[188:191], a[72:75]\n\t"
"v_fma_f32 v98, -v70, s56, -v168\n\t"
"v_fma_f32 v99, -v71, s56, -v169\n\t"
"v_mfma_f32_16x16x32_bf16 a[72:75], a[212:215], a[184:187], a[72:75]\n\t"
"Lpg15_%=:\n\t"
"s_cbranch_vccnz Lvr1_%=\n\t"
"v_accvgpr_write_b32 a112, v98\n\t"
"v_fma_f32 v98, -v72, s56, -v170\n\t"
"v_mfma_f32_16x16x32_bf16 a[80:83], a[212:215], a[188:191], a[80:83]\n\t"
"v_accvgpr_write_b32 a113, v99\n\t"
"v_fma_f32 v99, -v73, s56, -v171\n\t"
"v_mfma_f32_16x16x32_bf16 a[80:83], v[240:243], a[192:195], a[80:83]\n\t"
"v_accvgpr_write_b32 a114, v98\n\t"
"v_accvgpr_write_b32 a115, v99\n\t"
"v_mfma_f32_16x16x32_bf16 a[80:83], a[208:211], a[196:199], a[80:83]\n\t"
"v_fma_f32 v98, -v74, s56, -v172\n\t"
"v_fma_f32 v99, -v75, s56, -v173\n\t"
"v_mfma_f32_16x16x32_bf16 a[88:91], a[208:211], a[200:203], a[88:91]\n\t"
"v_accvgpr_write_b32 a120, v98\n\t"
"v_fma_f32 v98, -v76, s56, -v174\n\t"
"v_mfma_f32_16x16x32_bf16 a[88:91], v[240:243], a[196:199], a[88:91]\n\t"
"v_accvgpr_write_b32 a121, v99\n\t"
"v_fma_f32 v99, -v77, s56, -v175\n\t"
"v_mfma_f32_16x16x32_bf16 a[88:91], a[212:215], a[192:195], a[88:91]\n\t"
"buffer_store_dwordx4 a[12:15], v248, s[16:19], s29 offen offset:256 nt\n\t"
"v_accvgpr_write_b32 a122, v98\n\t"
"v_accvgpr_write_b32 a123, v99\n\t"
"v_fma_f32 v98, -v78, s56, -v176\n\t"
"v_fma_f32 v99, -v79, s56, -v177\n\t"
"v_mfma_f32_16x16x32_bf16 a[96:99], a[212:215], a[196:199], a[96:99]\n\t"
"s_waitcnt lgkmcnt(2)\n\t"
"v_accvgpr_write_b32 a128, v98\n\t"
"v_fma_f32 v98, -v80, s56, -v178\n\t"
"v_mfma_f32_16x16x32_bf16 a[96:99], v[240:243], a[200:203], a[96:99]\n\t"
"s_waitcnt lgkmcnt(0)\n\t"
"v_accvgpr_write_b32 a129, v99\n\t"
"v_fma_f32 v99, -v81, s56, -v179\n\t"
"v_mfma_f32_16x16x32_bf16 a[96:99], a[208:211], a[204:207], a[96:99]\n\t"
"s_waitcnt vmcnt(22)\n\t"
"s_add_u32 s29, s23, 1180800\n\t"
"v_accvgpr_write_b32 a130, v98\n\t"
"v_mfma_f32_16x16x32_bf16 v[224:227], v[224:227], v[220:223], 0\n\t"
"buffer_load_dwordx4 a[212:215], off, s[80:83], s20 offset:2048 sc1\n\t"
"s_mov_b32 s20, 36864\n\t"
"v_mfma_f32_16x16x32_bf16 v[232:235], v[232:235], v[220:223], 0\n\t"
"buffer_load_dwordx4 v[240:243], off, s[80:83], s20 offset:0 sc1\n\t"
"v_mfma_f32_16x16x32_bf16 a[104:107], a[216:219], a[168:171], a[104:107]\n\t"
"ds_read_b128 a[168:171], v244 offset:33792\n\t"
"v_accvgpr_write_b32 a131, v99\n\t"
"v_mfma_f32_16x16x32_bf16 a[104:107], a[228:231], a[176:179], a[104:107]\n\t"
"v_fma_f32 v98, -v82, s56, -v180\n\t"
"v_fma_f32 v99, -v83, s56, -v181\n\t"
"v_mfma_f32_16x16x32_bf16 a[104:107], a[224:227], a[172:175], a[104:107]\n\t"
"v_accvgpr_write_b32 a136, v98\n\t"
"v_fma_f32 v98, -v84, s56, -v182\n\t"
"v_mfma_f32_16x16x32_bf16 a[112:115], a[216:219], a[172:175], a[112:115]\n\t"
"ds_read_b128 a[172:175], v244 offset:34304\n\t"
"v_cvt_pk_bf16_f32 v224, v224, v225\n\t"
"v_mfma_f32_16x16x32_bf16 a[112:115], a[224:227], a[176:179], a[112:115]\n\t"
"v_cvt_pk_bf16_f32 v225, v226, v227\n\t"
"v_cvt_pk_bf16_f32 v226, v232, v233\n\t"
"v_mfma_f32_16x16x32_bf16 a[112:115], a[228:231], a[180:183], a[112:115]\n\t"
"buffer_load_dwordx4 a[208:211], off, s[80:83], s20 offset:1024 sc1\n\t"
"v_mfma_f32_16x16x32_bf16 a[120:123], a[228:231], a[184:187], a[120:123]\n\t"
"v_cvt_pk_bf16_f32 v227, v234, v235\n\t"
"v_accvgpr_write_b32 a137, v99\n\t"
"v_mfma_f32_16x16x32_bf16 a[120:123], a[224:227], a[180:183], a[120:123]\n\t"
"v_fma_f32 v99, -v85, s56, -v183\n\t"
"v_accvgpr_write_b32 a138, v98\n\t"
"v_mfma_f32_16x16x32_bf16 a[120:123], a[216:219], a[176:179], a[120:123]\n\t"
"buffer_store_dwordx4 a[44:47], v248, s[16:19], s29 offen offset:0 nt\n\t"
"ds_read_b128 a[176:179], v244 offset:34816\n\t"
"ds_read_b64_tr_b16 v[232:233], v246 offset:26624\n\t"
"v_accvgpr_write_b32 a139, v99\n\t"
"v_mfma_f32_16x16x32_bf16 a[128:131], a[216:219], a[180:183], a[128:131]\n\t"
"ds_read_b128 a[180:183], v244 offset:35328\n\t"
"v_fma_f32 v98, -v86, s56, -v184\n\t"
"v_mfma_f32_16x16x32_bf16 a[128:131], a[224:227], a[184:187], a[128:131]\n\t"
"ds_read_b64_tr_b16 v[234:235], v246 offset:29696\n\t"
"v_fma_f32 v99, -v87, s56, -v185\n\t"
"v_mfma_f32_16x16x32_bf16 a[128:131], a[228:231], a[188:191], a[128:131]\n\t"
"v_accvgpr_write_b32 a144, v98\n\t"
"v_fma_f32 v98, -v88, s56, -v186\n\t"
"v_mfma_f32_16x16x32_bf16 a[136:139], a[228:231], a[192:195], a[136:139]\n\t"
"v_accvgpr_write_b32 a145, v99\n\t"
"v_fma_f32 v99, -v89, s56, -v187\n\t"
"v_mfma_f32_16x16x32_bf16 a[136:139], a[224:227], a[188:191], a[136:139]\n\t"
"v_accvgpr_write_b32 a146, v98\n\t"
"v_accvgpr_write_b32 a147, v99\n\t"
"v_mfma_f32_16x16x32_bf16 a[136:139], a[216:219], a[184:187], a[136:139]\n\t"
"ds_read_b128 a[184:187], v244 offset:35840\n\t"
"v_fma_f32 v98, -v90, s56, -v188\n\t"
"v_fma_f32 v99, -v91, s56, -v189\n\t"
"v_mfma_f32_16x16x32_bf16 a[144:147], a[216:219], a[188:191], a[144:147]\n\t"
"ds_read_b128 a[188:191], v244 offset:36352\n\t"
"v_accvgpr_write_b32 a152, v98\n\t"
"v_mfma_f32_16x16x32_bf16 a[144:147], a[224:227], a[192:195], a[144:147]\n\t"
"v_fma_f32 v98, -v92, s56, -v190\n\t"
"v_accvgpr_write_b32 a153, v99\n\t"
"v_mfma_f32_16x16x32_bf16 a[144:147], a[228:231], a[196:199], a[144:147]\n\t"
"v_fma_f32 v99, -v93, s56, -v191\n\t"
"v_accvgpr_write_b32 a154, v98\n\t"
"v_accvgpr_write_b32 a155, v99\n\t"
"v_fma_f32 v98, -v94, s56, -v192\n\t"
"v_fma_f32 v99, -v95, s56, -v193\n\t"
"v_mfma_f32_16x16x32_bf16 a[152:155], a[228:231], a[200:203], a[152:155]\n\t"
"v_accvgpr_write_b32 a160, v98\n\t"
"v_fma_f32 v98, -v96, s56, -v194\n\t"
"v_mfma_f32_16x16x32_bf16 a[152:155], a[224:227], a[196:199], a[152:155]\n\t"
"v_accvgpr_write_b32 a161, v99\n\t"
"v_fma_f32 v99, -v97, s56, -v195\n\t"
"v_mfma_f32_16x16x32_bf16 a[152:155], a[216:219], a[192:195], a[152:155]\n\t"
"buffer_store_dwordx4 a[76:79], v248, s[16:19], s29 offen offset:128 nt\n\t"
"ds_read_b128 a[192:195], v244 offset:36864\n\t"
"v_accvgpr_write_b32 a162, v98\n\t"
"v_accvgpr_write_b32 a163, v99\n\t"
"v_fma_f32 v200, v100, s64, -v2\n\t"
"v_fma_f32 v201, v101, s64, -v3\n\t"
"v_fma_f32 v202, v102, s64, -v4\n\t"
"v_fma_f32 v203, v103, s64, -v5\n\t"
"v_mfma_f32_16x16x32_bf16 a[160:163], a[216:219], a[196:199], a[160:163]\n\t"
"s_waitcnt lgkmcnt(3)\n\t"
"ds_read_b128 a[196:199], v244 offset:37376\n\t"
"v_fma_f32 v208, v104, s64, -v6\n\t"
"v_mfma_f32_16x16x32_bf16 a[160:163], a[224:227], a[200:203], a[160:163]\n\t"
"s_waitcnt vmcnt(22)\n\t"
"ds_read_b128 a[200:203], v244 offset:37888\n\t"
"v_fma_f32 v209, v105, s64, -v7\n\t"
"v_mfma_f32_16x16x32_bf16 a[160:163], a[228:231], a[204:207], a[160:163]\n\t"
"ds_read_b128 a[204:207], v244 offset:38400\n\t"
"v_fma_f32 v210, v106, s64, -v8\n\t"
"v_fma_f32 v211, v107, s64, -v9\n\t"
"v_mfma_f32_16x16x32_bf16 v[232:235], v[232:235], v[220:223], 0\n\t"
"buffer_load_dwordx4 a[216:219], off, s[80:83], s20 offset:2048 sc1\n\t"
"s_mov_b32 s20, 49152\n\t"
"v_mfma_f32_16x16x32_bf16 v[200:203], a[232:235], a[168:171], v[200:203]\n\t"
"buffer_load_dwordx4 a[224:227], off, s[80:83], s20 offset:0 sc1\n\t"
"v_mfma_f32_16x16x32_bf16 v[200:203], a[240:243], a[176:179], v[200:203]\n\t"
"v_fma_f32 v216, v108, s64, -v10\n\t"
"v_fma_f32 v217, v109, s64, -v11\n\t"
"v_mfma_f32_16x16x32_bf16 v[200:203], a[236:239], a[172:175], v[200:203]\n\t"
"v_fma_f32 v218, v110, s64, -v12\n\t"
"v_fma_f32 v219, v111, s64, -v13\n\t"
"v_mfma_f32_16x16x32_bf16 v[208:211], a[232:235], a[172:175], v[208:211]\n\t"
"s_waitcnt lgkmcnt(5)\n\t"
"v_fma_f32 v98, v112, s64, -v14\n\t"
"v_fma_f32 v99, v113, s64, -v15\n\t"
"v_mfma_f32_16x16x32_bf16 v[208:211], a[236:239], a[176:179], v[208:211]\n\t"
"v_accvgpr_write_b32 a4, v98\n\t"
"v_mfma_f32_16x16x32_bf16 v[208:211], a[240:243], a[180:183], v[208:211]\n\t"
"s_waitcnt lgkmcnt(0)\n\t"
"s_barrier\n\t"
"v_mfma_f32_16x16x32_bf16 v[216:219], a[240:243], a[184:187], v[216:219]\n\t"
"v_fma_f32 v98, v114, s64, -v16\n\t"
"v_mfma_f32_16x16x32_bf16 v[216:219], a[236:239], a[180:183], v[216:219]\n\t"
"buffer_load_dwordx4 a[228:231], off, s[80:83], s20 offset:1024 sc1\n\t"
"v_mfma_f32_16x16x32_bf16 v[216:219], a[232:235], a[176:179], v[216:219]\n\t"
"buffer_store_dwordx4 a[108:111], v248, s[16:19], s29 offen offset:256 nt\n\t"
"v_accvgpr_write_b32 a5, v99\n\t"
"v_fma_f32 v99, v115, s64, -v17\n\t"
"v_accvgpr_write_b32 a6, v98\n\t"
"v_accvgpr_write_b32 a7, v99\n\t"
"v_fma_f32 v98, v116, s64, -v18\n\t"
"v_fma_f32 v99, v117, s64, -v19\n\t"
"v_cvt_pk_bf16_f32 v232, v232, v233\n\t"
"v_cvt_pk_bf16_f32 v233, v234, v235\n\t"
"v_mfma_f32_16x16x32_bf16 a[4:7], a[232:235], a[180:183], a[4:7]\n\t"
"v_accvgpr_write_b32 a12, v98\n\t"
"v_fma_f32 v98, v118, s64, -v20\n\t"
"v_mfma_f32_16x16x32_bf16 a[4:7], a[236:239], a[184:187], a[4:7]\n\t"
"v_accvgpr_write_b32 a13, v99\n\t"
"v_fma_f32 v99, v119, s64, -v21\n\t"
"v_mfma_f32_16x16x32_bf16 a[4:7], a[240:243], a[188:191], a[4:7]\n\t"
"ds_write2st64_b64 v245, v[228:229], v[230:231] offset0:48 offset1:49\n\t"
"v_accvgpr_write_b32 a14, v98\n\t"
"v_accvgpr_write_b32 a15, v99\n\t"
"v_fma_f32 v98, v120, s64, -v22\n\t"
"v_fma_f32 v99, v121, s64, -v23\n\t"
"v_mfma_f32_16x16x32_bf16 a[12:15], a[240:243], a[192:195], a[12:15]\n\t"
"ds_read_b64_tr_b16 v[228:229], v246 offset:32768\n\t"
"v_mfma_f32_16x16x32_bf16 a[12:15], a[236:239], a[188:191], a[12:15]\n\t"
"buffer_load_dwordx4 v[248:251], off, s[80:83], s20 offset:2048 sc1\n\t"
"v_mfma_f32_16x16x32_bf16 a[12:15], a[232:235], a[184:187], a[12:15]\n\t"
"ds_write2st64_b64 v245, v[224:225], v[226:227] offset0:50 offset1:51\n\t"
"v_accvgpr_write_b32 a20, v98\n\t"
"v_fma_f32 v98, v122, s64, -v24\n\t"
"v_accvgpr_write_b32 a21, v99\n\t"
"v_fma_f32 v99, v123, s64, -v25\n\t"
"v_accvgpr_write_b32 a22, v98\n\t"
"v_accvgpr_write_b32 a23, v99\n\t"
"v_fma_f32 v98, v124, s64, -v26\n\t"
"v_fma_f32 v99, v125, s64, -v27\n\t"
"v_mfma_f32_16x16x32_bf16 a[20:23], a[232:235], a[188:191], a[20:23]\n\t"
"ds_read_b64_tr_b16 v[230:231], v246 offset:35840\n\t"
"v_accvgpr_write_b32 a28, v98\n\t"
"v_mfma_f32_16x16x32_bf16 a[20:23], a[236:239], a[192:195], a[20:23]\n\t"
"ds_read_b64_tr_b16 v[224:225], v246 offset:40448\n\t"
"v_fma_f32 v98, v126, s64, -v28\n\t"
"v_mfma_f32_16x16x32_bf16 a[20:23], a[240:243], a[196:199], a[20:23]\n\t"
"s_mov_b32 s20, 61440\n\t"
"ds_write_b64 v245, v[232:233] offset:26624\n\t"
"ds_read_b64_tr_b16 v[226:227], v246 offset:43520\n\t"
"v_accvgpr_write_b32 a29, v99\n\t"
"v_fma_f32 v99, v127, s64, -v29\n\t"
"v_accvgpr_write_b32 a30, v98\n\t"
"v_accvgpr_write_b32 a31, v99\n\t"
"s_nop 1\n\t"
"v_mfma_f32_16x16x32_bf16 a[28:31], a[236:239], a[196:199], a[28:31]\n\t"
"ds_read_b64_tr_b16 v[232:233], v246 offset:46592\n\t"
"v_fma_f32 v98, v128, s64, -v30\n\t"
"v_mfma_f32_16x16x32_bf16 a[28:31], a[232:235], a[192:195], a[28:31]\n\t"
"ds_read_b64_tr_b16 v[234:235], v246 offset:49664\n\t"
"v_fma_f32 v99, v129, s64, -v31\n\t"
"v_mfma_f32_16x16x32_bf16 a[28:31], a[240:243], a[200:203], a[28:31]\n\t"
"s_waitcnt lgkmcnt(5)\n\t"
"v_accvgpr_write_b32 a36, v98\n\t"
"v_fma_f32 v98, v130, s64, -v32\n\t"
"v_accvgpr_write_b32 a37, v99\n\t"
"v_fma_f32 v99, v131, s64, -v33\n\t"
"v_accvgpr_write_b32 a38, v98\n\t"
"v_accvgpr_write_b32 a39, v99\n\t"
"v_mfma_f32_16x16x32_bf16 v[228:231], v[228:231], v[220:223], 0\n\t"
"s_waitcnt lgkmcnt(2)\n\t"
"v_fma_f32 v98, v132, s64, -v34\n\t"
"v_fma_f32 v99, v133, s64, -v35\n\t"
"v_mfma_f32_16x16x32_bf16 v[224:227], v[224:227], v[220:223], 0\n\t"
"v_accvgpr_write_b32 a44, v98\n\t"
"v_fma_f32 v98, v134, s64, -v36\n\t"
"v_mfma_f32_16x16x32_bf16 a[36:39], a[236:239], a[200:203], a[36:39]\n\t"
"v_accvgpr_write_b32 a45, v99\n\t"
"v_fma_f32 v99, v135, s64, -v37\n\t"
"v_mfma_f32_16x16x32_bf16 a[36:39], a[232:235], a[196:199], a[36:39]\n\t"
"s_waitcnt lgkmcnt(0)\n\t"
"v_accvgpr_write_b32 a46, v98\n\t"
"v_accvgpr_write_b32 a47, v99\n\t"
"v_mfma_f32_16x16x32_bf16 a[36:39], a[240:243], a[204:207], a[36:39]\n\t"
"s_waitcnt vmcnt(20)\n\t"
"v_fma_f32 v98, v136, s64, -v38\n\t"
"v_fma_f32 v99, v137, s64, -v39\n\t"
"v_mfma_f32_16x16x32_bf16 v[232:235], v[232:235], v[220:223], 0\n\t"
"buffer_load_dwordx4 a[236:239], off, s[80:83], s20 offset:0 sc1\n\t"
"v_mfma_f32_16x16x32_bf16 a[44:47], a[244:247], a[168:171], a[44:47]\n\t"
"buffer_load_dwordx4 a[232:235], off, s[80:83], s20 offset:1024 sc1\n\t"
"v_mfma_f32_16x16x32_bf16 a[44:47], a[252:255], a[176:179], a[44:47]\n\t"
"v_accvgpr_write_b32 a52, v98\n\t"
"v_fma_f32 v98, v138, s64, -v40\n\t"
"v_mfma_f32_16x16x32_bf16 a[44:47], a[248:251], a[172:175], a[44:47]\n\t"
"v_accvgpr_write_b32 a53, v99\n\t"
"v_fma_f32 v99, v139, s64, -v41\n\t"
"v_accvgpr_write_b32 a54, v98\n\t"
"v_accvgpr_write_b32 a55, v99\n\t"
"v_fma_f32 v98, v140, s64, -v42\n\t"
"v_fma_f32 v99, v141, s64, -v43\n\t"
"v_cvt_pk_bf16_f32 v228, v228, v229\n\t"
"v_cvt_pk_bf16_f32 v229, v230, v231\n\t"
"v_mfma_f32_16x16x32_bf16 a[52:55], a[244:247], a[172:175], a[52:55]\n\t"
"v_accvgpr_write_b32 a60, v98\n\t"
"v_fma_f32 v98, v142, s64, -v44\n\t"
"v_mfma_f32_16x16x32_bf16 a[52:55], a[248:251], a[176:179], a[52:55]\n\t"
"v_accvgpr_write_b32 a61, v99\n\t"
"v_fma_f32 v99, v143, s64, -v45\n\t"
"v_mfma_f32_16x16x32_bf16 a[52:55], a[252:255], a[180:183], a[52:55]\n\t"
"buffer_load_dwordx4 a[240:243], off, s[80:83], s20 offset:2048 sc1\n\t"
"v_accvgpr_write_b32 a62, v98\n\t"
"v_accvgpr_write_b32 a63, v99\n\t"
"v_cvt_pk_bf16_f32 v230, v224, v225\n\t"
"v_cvt_pk_bf16_f32 v231, v226, v227\n\t"
"v_fma_f32 v98, v144, s64, -v46\n\t"
"v_fma_f32 v99, v145, s64, -v47\n\t"
"v_mfma_f32_16x16x32_bf16 a[60:63], a[252:255], a[184:187], a[60:63]\n\t"
"ds_read_b64_tr_b16 v[224:225], v246 offset:52736\n\t"
"v_accvgpr_write_b32 a68, v98\n\t"
"v_mfma_f32_16x16x32_bf16 a[60:63], a[248:251], a[180:183], a[60:63]\n\t"
"ds_read_b64_tr_b16 v[226:227], v246 offset:55808\n\t"
"v_fma_f32 v98, v146, s64, -v48\n\t"
"v_mfma_f32_16x16x32_bf16 a[60:63], a[244:247], a[176:179], a[60:63]\n\t"
"ds_write2st64_b64 v245, v[228:229], v[230:231] offset0:53 offset1:54\n\t"
"v_accvgpr_write_b32 a69, v99\n\t"
"v_fma_f32 v99, v147, s64, -v49\n\t"
"v_accvgpr_write_b32 a70, v98\n\t"
"v_accvgpr_write_b32 a71, v99\n\t"
"v_fma_f32 v98, v148, s64, -v50\n\t"
"v_fma_f32 v99, v149, s64, -v51\n\t"
"v_cvt_pk_bf16_f32 v232, v232, v233\n\t"
"v_cvt_pk_bf16_f32 v233, v234, v235\n\t"
"v_mfma_f32_16x16x32_bf16 a[68:71], a[244:247], a[180:183], a[68:71]\n\t"
"v_accvgpr_write_b32 a76, v98\n\t"
"v_fma_f32 v98, v150, s64, -v52\n\t"
"v_mfma_f32_16x16x32_bf16 a[68:71], a[248:251], a[184:187], a[68:71]\n\t"
"v_accvgpr_write_b32 a77, v99\n\t"
"v_fma_f32 v99, v151, s64, -v53\n\t"
"v_mfma_f32_16x16x32_bf16 a[68:71], a[252:255], a[188:191], a[68:71]\n\t"
"v_accvgpr_write_b32 a78, v98\n\t"
"v_accvgpr_write_b32 a79, v99\n\t"
"v_fma_f32 v98, v152, s64, -v54\n\t"
"v_fma_f32 v99, v153, s64, -v55\n\t"
"v_mfma_f32_16x16x32_bf16 a[76:79], a[248:251], a[188:191], a[76:79]\n\t"
"s_waitcnt lgkmcnt(1)\n\t"
"v_accvgpr_write_b32 a84, v98\n\t"
"v_fma_f32 v98, v154, s64, -v56\n\t"
"v_mfma_f32_16x16x32_bf16 a[76:79], a[252:255], a[192:195], a[76:79]\n\t"
"v_accvgpr_write_b32 a85, v99\n\t"
"v_fma_f32 v99, v155, s64, -v57\n\t"
"v_mfma_f32_16x16x32_bf16 a[76:79], a[244:247], a[184:187], a[76:79]\n\t"
"v_accvgpr_write_b32 a86, v98\n\t"
"v_accvgpr_write_b32 a87, v99\n\t"
"v_fma_f32 v98, v156, s64, -v58\n\t"
"v_fma_f32 v99, v157, s64, -v59\n\t"
"v_mfma_f32_16x16x32_bf16 v[224:227], v[224:227], v[220:223], 0\n\t"
"v_accvgpr_write_b32 a92, v98\n\t"
"v_fma_f32 v98, v158, s64, -v60\n\t"
"v_mfma_f32_16x16x32_bf16 a[84:87], a[244:247], a[188:191], a[84:87]\n\t"
"s_mov_b32 s20, 73728\n\t"
"v_accvgpr_write_b32 a93, v99\n\t"
"v_fma_f32 v99, v159, s64, -v61\n\t"
"v_mfma_f32_16x16x32_bf16 a[84:87], a[252:255], a[196:199], a[84:87]\n\t"
"v_accvgpr_write_b32 a94, v98\n\t"
"v_accvgpr_write_b32 a95, v99\n\t"
"v_mfma_f32_16x16x32_bf16 a[84:87], a[248:251], a[192:195], a[84:87]\n\t"
"v_fma_f32 v98, v160, s64, -v62\n\t"
"v_fma_f32 v99, v161, s64, -v63\n\t"
"v_mfma_f32_16x16x32_bf16 a[92:95], a[244:247], a[192:195], a[92:95]\n\t"
"v_accvgpr_write_b32 a100, v98\n\t"
"v_fma_f32 v98, v162, s64, -v64\n\t"
"v_mfma_f32_16x16x32_bf16 a[92:95], a[248:251], a[196:199], a[92:95]\n\t"
"v_accvgpr_write_b32 a101, v99\n\t"
"v_fma_f32 v99, v163, s64, -v65\n\t"
"v_mfma_f32_16x16x32_bf16 a[92:95], a[252:255], a[200:203], a[92:95]\n\t"
"v_accvgpr_write_b32 a102, v98\n\t"
"v_accvgpr_write_b32 a103, v99\n\t"
"v_fma_f32 v98, v164, s64, -v66\n\t"
"v_fma_f32 v99, v165, s64, -v67\n\t"
"v_cvt_pk_bf16_f32 v234, v224, v225\n\t"
"v_cvt_pk_bf16_f32 v235, v226, v227\n\t"
"v_mfma_f32_16x16x32_bf16 a[100:103], a[248:251], a[200:203], a[100:103]\n\t"
"v_accvgpr_write_b32 a108, v98\n\t"
"v_fma_f32 v98, v166, s64, -v68\n\t"
"v_mfma_f32_16x16x32_bf16 a[100:103], a[244:247], a[196:199], a[100:103]\n\t"
"v_accvgpr_write_b32 a109, v99\n\t"
"v_fma_f32 v99, v167, s64, -v69\n\t"
"v_mfma_f32_16x16x32_bf16 a[100:103], a[252:255], a[204:207], a[100:103]\n\t"
"ds_write2st64_b64 v245, v[232:233], v[234:235] offset0:55 offset1:56\n\t"
"v_accvgpr_write_b32 a110, v98\n\t"
"v_accvgpr_write_b32 a111, v99\n\t"
"v_fma_f32 v98, v168, s64, -v70\n\t"
"v_fma_f32 v99, v169, s64, -v71\n\t"
"s_waitcnt vmcnt(12)\n\t"
"v_mfma_f32_16x16x32_bf16 a[108:111], v[236:239], a[168:171], a[108:111]\n\t"
"buffer_load_dwordx4 a[248:251], off, s[80:83], s20 offset:0 sc1\n\t"
"v_mfma_f32_16x16x32_bf16 a[108:111], a[212:215], a[176:179], a[108:111]\n\t"
"v_accvgpr_write_b32 a116, v98\n\t"
"v_mfma_f32_16x16x32_bf16 a[108:111], a[220:223], a[172:175], a[108:111]\n\t"
"v_accvgpr_write_b32 a117, v99\n\t"
"v_fma_f32 v98, v170, s64, -v72\n\t"
"v_accvgpr_write_b32 a118, v98\n\t"
"v_fma_f32 v99, v171, s64, -v73\n\t"
"v_accvgpr_write_b32 a119, v99\n\t"
"s_waitcnt lgkmcnt(0)\n\t"
"s_barrier\n\t"
"v_mfma_f32_16x16x32_bf16 a[116:119], v[236:239], a[172:175], a[116:119]\n\t"
"ds_read_b128 a[168:171], v244 offset:24576\n\t"
"v_mfma_f32_16x16x32_bf16 a[116:119], a[220:223], a[176:179], a[116:119]\n\t"
"buffer_load_dwordx4 a[244:247], off, s[80:83], s20 offset:1024 sc1\n\t"
"v_mfma_f32_16x16x32_bf16 a[116:119], a[212:215], a[180:183], a[116:119]\n\t"
"buffer_load_dwordx4 a[252:255], off, s[80:83], s20 offset:2048 sc1\n\t"
"v_fma_f32 v98, v172, s64, -v74\n\t"
"v_fma_f32 v99, v173, s64, -v75\n\t"
"v_accvgpr_write_b32 a124, v98\n\t"
"v_fma_f32 v98, v174, s64, -v76\n\t"
"v_accvgpr_write_b32 a125, v99\n\t"
"v_fma_f32 v99, v175, s64, -v77\n\t"
"v_accvgpr_write_b32 a126, v98\n\t"
"v_accvgpr_write_b32 a127, v99\n\t"
"v_fma_f32 v98, v176, s64, -v78\n\t"
"v_fma_f32 v99, v177, s64, -v79\n\t"
"v_mfma_f32_16x16x32_bf16 a[124:127], a[212:215], a[184:187], a[124:127]\n\t"
"ds_read_b128 a[172:175], v244 offset:25088\n\t"
"v_accvgpr_write_b32 a132, v98\n\t"
"v_mfma_f32_16x16x32_bf16 a[124:127], a[220:223], a[180:183], a[124:127]\n\t"
"v_fma_f32 v98, v178, s64, -v80\n\t"
"v_accvgpr_write_b32 a133, v99\n\t"
"v_mfma_f32_16x16x32_bf16 a[124:127], v[236:239], a[176:179], a[124:127]\n\t"
"ds_read_b128 a[176:179], v244 offset:25600\n\t"
"v_fma_f32 v99, v179, s64, -v81\n\t"
"v_accvgpr_write_b32 a134, v98\n\t"
"v_accvgpr_write_b32 a135, v99\n\t"
"v_fma_f32 v98, v180, s64, -v82\n\t"
"v_fma_f32 v99, v181, s64, -v83\n\t"
"v_mfma_f32_16x16x32_bf16 a[132:135], v[236:239], a[180:183], a[132:135]\n\t"
"ds_read_b128 a[180:183], v244 offset:26112\n\t"
"v_accvgpr_write_b32 a140, v98\n\t"
"v_mfma_f32_16x16x32_bf16 a[132:135], a[220:223], a[184:187], a[132:135]\n\t"
"v_fma_f32 v98, v182, s64, -v84\n\t"
"v_accvgpr_write_b32 a141, v99\n\t"
"v_mfma_f32_16x16x32_bf16 a[132:135], a[212:215], a[188:191], a[132:135]\n\t"
"v_fma_f32 v99, v183, s64, -v85\n\t"
"v_accvgpr_write_b32 a142, v98\n\t"
"v_accvgpr_write_b32 a143, v99\n\t"
"v_fma_f32 v98, v184, s64, -v86\n\t"
"v_fma_f32 v99, v185, s64, -v87\n\t"
"v_mfma_f32_16x16x32_bf16 a[140:143], a[212:215], a[192:195], a[140:143]\n\t"
"v_accvgpr_write_b32 a148, v98\n\t"
"v_fma_f32 v98, v186, s64, -v88\n\t"
"v_mfma_f32_16x16x32_bf16 a[140:143], a[220:223], a[188:191], a[140:143]\n\t"
"v_accvgpr_write_b32 a149, v99\n\t"
"v_fma_f32 v99, v187, s64, -v89\n\t"
"v_mfma_f32_16x16x32_bf16 a[140:143], v[236:239], a[184:187], a[140:143]\n\t"
"ds_read_b128 a[184:187], v244 offset:26624\n\t"
"v_accvgpr_write_b32 a150, v98\n\t"
"v_accvgpr_write_b32 a151, v99\n\t"
"v_fma_f32 v98, v188, s64, -v90\n\t"
"v_fma_f32 v99, v189, s64, -v91\n\t"
"v_mfma_f32_16x16x32_bf16 a[148:151], v[236:239], a[188:191], a[148:151]\n\t"
"s_mov_b32 s20, 86016\n\t"
"ds_read_b128 a[188:191], v244 offset:27136\n\t"
"v_accvgpr_write_b32 a156, v98\n\t"
"v_mfma_f32_16x16x32_bf16 a[148:151], a[220:223], a[192:195], a[148:151]\n\t"
"v_fma_f32 v98, v190, s64, -v92\n\t"
"v_accvgpr_write_b32 a157, v99\n\t"
"v_mfma_f32_16x16x32_bf16 a[148:151], a[212:215], a[196:199], a[148:151]\n\t"
"v_fma_f32 v99, v191, s64, -v93\n\t"
"v_accvgpr_write_b32 a158, v98\n\t"
"v_accvgpr_write_b32 a159, v99\n\t"
"v_fma_f32 v98, v192, s64, -v94\n\t"
"v_fma_f32 v99, v193, s64, -v95\n\t"
"v_mfma_f32_16x16x32_bf16 a[156:159], a[212:215], a[200:203], a[156:159]\n\t"
"v_accvgpr_write_b32 a164, v98\n\t"
"v_fma_f32 v98, v194, s64, -v96\n\t"
"v_mfma_f32_16x16x32_bf16 a[156:159], a[220:223], a[196:199], a[156:159]\n\t"
"v_accvgpr_write_b32 a165, v99\n\t"
"v_fma_f32 v99, v195, s64, -v97\n\t"
"v_mfma_f32_16x16x32_bf16 a[156:159], v[236:239], a[192:195], a[156:159]\n\t"
"s_add_u32 m0, s24, -128\n\t"
"s_add_u32 s28, s21, 0\n\t"
"ds_read_b128 a[192:195], v244 offset:27648\n\t"
"v_accvgpr_write_b32 a166, v98\n\t"
"v_accvgpr_write_b32 a167, v99\n\t"
"s_nop 1\n\t"
"v_mfma_f32_16x16x32_bf16 a[164:167], v[236:239], a[196:199], a[164:167]\n\t"
"buffer_load_dwordx4 v[236:239], off, s[80:83], s20 offset:0 sc1\n\t"
"v_mfma_f32_16x16x32_bf16 a[164:167], a[220:223], a[200:203], a[164:167]\n\t"
"ds_read_b128 a[196:199], v244 offset:28160\n\t"
"v_fma_f32 v100, -v100, s66, v2\n\t"
"v_mfma_f32_16x16x32_bf16 a[164:167], a[212:215], a[204:207], a[164:167]\n\t"
"ds_read_b128 a[200:203], v244 offset:28672\n\t"
"v_fma_f32 v2, v4, s52, v102\n\t"
"v_fma_f32 v101, -v101, s66, v3\n\t"
"v_fma_f32 v3, v5, s52, v103\n\t"
"v_fma_f32 v102, -v102, s66, v4\n\t"
"v_fma_f32 v103, -v103, s66, v5\n\t"
"v_fma_f32 v4, v6, s52, v104\n\t"
"v_fma_f32 v5, v7, s52, v105\n\t"
"Lst21_%=:\n\t"
"s_waitcnt vmcnt(11) lgkmcnt(6)\n\t"
"v_mfma_f32_16x16x32_bf16 v[0:3], v[240:243], a[168:171], v[0:3]\n\t"
"buffer_load_dwordx4 v247, s[12:15], s28 offen offset:128 nt lds\n\t"
"v_mfma_f32_16x16x32_bf16 v[0:3], a[216:219], a[176:179], v[0:3]\n\t"
"s_add_u32 m0, s24, 6016\n\t"
"s_add_u32 s28, s21, 196608\n\t"
"ds_read_b128 a[204:207], v244 offset:29184\n\t"
"v_mfma_f32_16x16x32_bf16 v[0:3], a[208:211], a[172:175], v[0:3]\n\t"
"buffer_load_dwordx4 v247, s[12:15], s28 offen offset:128 nt lds\n\t"
"v_fma_f32 v104, -v104, s66, v6\n\t"
"v_fma_f32 v6, v8, s52, v106\n\t"
"v_fma_f32 v105, -v105, s66, v7\n\t"
"v_fma_f32 v7, v9, s52, v107\n\t"
"v_fma_f32 v106, -v106, s66, v8\n\t"
"v_fma_f32 v107, -v107, s66, v9\n\t"
"v_fma_f32 v8, v10, s52, v108\n\t"
"v_fma_f32 v9, v11, s52, v109\n\t"
"s_waitcnt lgkmcnt(6)\n\t"
"v_mfma_f32_16x16x32_bf16 v[4:7], v[240:243], a[172:175], v[4:7]\n\t"
"s_add_u32 m0, s24, 59776\n\t"
"s_add_u32 s28, s21, 1769472\n\t"
"v_fma_f32 v108, -v108, s66, v10\n\t"
"v_mfma_f32_16x16x32_bf16 v[4:7], a[208:211], a[176:179], v[4:7]\n\t"
"v_fma_f32 v10, v12, s52, v110\n\t"
"v_fma_f32 v109, -v109, s66, v11\n\t"
"v_mfma_f32_16x16x32_bf16 v[4:7], a[216:219], a[180:183], v[4:7]\n\t"
"buffer_load_dwordx4 v247, s[12:15], s28 offen offset:128 nt lds\n\t"
"v_fma_f32 v11, v13, s52, v111\n\t"
"v_fma_f32 v110, -v110, s66, v12\n\t"
"v_fma_f32 v111, -v111, s66, v13\n\t"
"v_fma_f32 v12, v14, s52, v112\n\t"
"v_fma_f32 v13, v15, s52, v113\n\t"
"s_add_u32 m0, s24, 65920\n\t"
"s_add_u32 s28, s21, 1966080\n\t"
"s_waitcnt lgkmcnt(5)\n\t"
"v_mfma_f32_16x16x32_bf16 v[8:11], a[216:219], a[184:187], v[8:11]\n\t"
"buffer_load_dwordx4 v247, s[12:15], s28 offen offset:128 nt lds\n\t"
"v_mfma_f32_16x16x32_bf16 v[8:11], a[208:211], a[180:183], v[8:11]\n\t"
"s_add_u32 m0, s24, 12160\n\t"
"s_add_u32 s28, s21, 393216\n\t"
"v_fma_f32 v112, -v112, s66, v14\n\t"
"v_mfma_f32_16x16x32_bf16 v[8:11], v[240:243], a[176:179], v[8:11]\n\t"
"ds_read_b64_tr_b16 v[228:229], v246 offset:1024\n\t"
"v_fma_f32 v14, v16, s52, v114\n\t"
"v_fma_f32 v113, -v113, s66, v15\n\t"
"v_fma_f32 v15, v17, s52, v115\n\t"
"v_fma_f32 v114, -v114, s66, v16\n\t"
"v_fma_f32 v115, -v115, s66, v17\n\t"
"v_fma_f32 v16, v18, s52, v116\n\t"
"v_fma_f32 v17, v19, s52, v117\n\t"
"s_waitcnt lgkmcnt(5)\n\t"
"v_mfma_f32_16x16x32_bf16 v[12:15], v[240:243], a[180:183], v[12:15]\n\t"
"buffer_load_dwordx4 v247, s[12:15], s28 offen offset:128 nt lds\n\t"
"v_mfma_f32_16x16x32_bf16 v[12:15], a[208:211], a[184:187], v[12:15]\n\t"
"s_add_u32 m0, s24, 19840\n\t"
"s_add_u32 s28, s21, 589824\n\t"
"ds_read_b64_tr_b16 v[230:231], v246 offset:4096\n\t"
"v_mfma_f32_16x16x32_bf16 v[12:15], a[216:219], a[188:191], v[12:15]\n\t"
"buffer_load_dwordx4 v247, s[12:15], s28 offen offset:128 nt lds\n\t"
"v_fma_f32 v116, -v116, s66, v18\n\t"
"v_fma_f32 v18, v20, s52, v118\n\t"
"v_fma_f32 v117, -v117, s66, v19\n\t"
"v_fma_f32 v19, v21, s52, v119\n\t"
"v_fma_f32 v118, -v118, s66, v20\n\t"
"v_fma_f32 v119, -v119, s66, v21\n\t"
"v_fma_f32 v20, v22, s52, v120\n\t"
"v_fma_f32 v21, v23, s52, v121\n\t"
"s_waitcnt lgkmcnt(5)\n\t"
"v_mfma_f32_16x16x32_bf16 v[16:19], a[216:219], a[192:195], v[16:19]\n\t"
"ds_read_b64_tr_b16 v[224:225], v246 offset:7168\n\t"
"v_fma_f32 v120, -v120, s66, v22\n\t"
"v_mfma_f32_16x16x32_bf16 v[16:19], a[208:211], a[188:191], v[16:19]\n\t"
"ds_read_b64_tr_b16 v[226:227], v246 offset:10240\n\t"
"v_fma_f32 v22, v24, s52, v122\n\t"
"v_mfma_f32_16x16x32_bf16 v[16:19], v[240:243], a[184:187], v[16:19]\n\t"
"v_fma_f32 v121, -v121, s66, v23\n\t"
"v_fma_f32 v23, v25, s52, v123\n\t"
"v_fma_f32 v122, -v122, s66, v24\n\t"
"v_fma_f32 v123, -v123, s66, v25\n\t"
"v_fma_f32 v24, v26, s52, v124\n\t"
"v_fma_f32 v25, v27, s52, v125\n\t"
"Lpg16_%=:\n\t"
"s_cbranch_vccnz Lvr1_%=\n\t"
"s_waitcnt lgkmcnt(6)\n\t"
"v_mfma_f32_16x16x32_bf16 v[20:23], v[240:243], a[188:191], v[20:23]\n\t"
"v_fma_f32 v124, -v124, s66, v26\n\t"
"v_fma_f32 v26, v28, s52, v126\n\t"
"v_mfma_f32_16x16x32_bf16 v[20:23], a[208:211], a[192:195], v[20:23]\n\t"
"v_fma_f32 v125, -v125, s66, v27\n\t"
"v_fma_f32 v27, v29, s52, v127\n\t"
"v_mfma_f32_16x16x32_bf16 v[20:23], a[216:219], a[196:199], v[20:23]\n\t"
"v_fma_f32 v126, -v126, s66, v28\n\t"
"v_fma_f32 v127, -v127, s66, v29\n\t"
"v_fma_f32 v28, v30, s52, v128\n\t"
"v_fma_f32 v29, v31, s52, v129\n\t"
"s_waitcnt lgkmcnt(5)\n\t"
"v_mfma_f32_16x16x32_bf16 v[24:27], a[216:219], a[200:203], v[24:27]\n\t"
"v_fma_f32 v128, -v128, s66, v30\n\t"
"v_fma_f32 v30, v32, s52, v130\n\t"
"v_mfma_f32_16x16x32_bf16 v[24:27], a[208:211], a[196:199], v[24:27]\n\t"
"v_fma_f32 v129, -v129, s66, v31\n\t"
"v_fma_f32 v31, v33, s52, v131\n\t"
"v_mfma_f32_16x16x32_bf16 v[24:27], v[240:243], a[192:195], v[24:27]\n\t"
"v_fma_f32 v130, -v130, s66, v32\n\t"
"v_fma_f32 v131, -v131, s66, v33\n\t"
"s_waitcnt lgkmcnt(4)\n\t"
"v_mfma_f32_16x16x32_bf16 v[28:31], v[240:243], a[196:199], v[28:31]\n\t"
"s_waitcnt lgkmcnt(2)\n\t"
"v_fma_f32 v32, v34, s52, v132\n\t"
"v_fma_f32 v33, v35, s52, v133\n\t"
"v_mfma_f32_16x16x32_bf16 v[28:31], a[208:211], a[200:203], v[28:31]\n\t"
"s_waitcnt lgkmcnt(0)\n\t"
"v_fma_f32 v132, -v132, s66, v34\n\t"
"v_fma_f32 v34, v36, s52, v134\n\t"
"v_mfma_f32_16x16x32_bf16 v[28:31], a[216:219], a[204:207], v[28:31]\n\t"
"v_fma_f32 v133, -v133, s66, v35\n\t"
"v_fma_f32 v35, v37, s52, v135\n\t"
"v_fma_f32 v134, -v134, s66, v36\n\t"
"v_fma_f32 v135, -v135, s66, v37\n\t"
"v_mfma_f32_16x16x32_bf16 v[228:231], v[228:231], v[220:223], 0\n\t"
"s_waitcnt vmcnt(13)\n\t"
"v_fma_f32 v36, v38, s52, v136\n\t"
"v_fma_f32 v37, v39, s52, v137\n\t"
"v_mfma_f32_16x16x32_bf16 v[224:227], v[224:227], v[220:223], 0\n\t"
"v_fma_f32 v136, -v136, s66, v38\n\t"
"v_fma_f32 v38, v40, s52, v138\n\t"
"v_mfma_f32_16x16x32_bf16 v[32:35], a[224:227], a[168:171], v[32:35]\n\t"
"v_fma_f32 v137, -v137, s66, v39\n\t"
"v_fma_f32 v39, v41, s52, v139\n\t"
"v_mfma_f32_16x16x32_bf16 v[32:35], v[248:251], a[176:179], v[32:35]\n\t"
"v_fma_f32 v138, -v138, s66, v40\n\t"
"v_fma_f32 v139, -v139, s66, v41\n\t"
"v_mfma_f32_16x16x32_bf16 v[32:35], a[228:231], a[172:175], v[32:35]\n\t"
"v_fma_f32 v40, v42, s52, v140\n\t"
"v_fma_f32 v41, v43, s52, v141\n\t"
"v_mfma_f32_16x16x32_bf16 v[36:39], a[224:227], a[172:175], v[36:39]\n\t"
"v_fma_f32 v140, -v140, s66, v42\n\t"
"v_fma_f32 v42, v44, s52, v142\n\t"
"v_mfma_f32_16x16x32_bf16 v[36:39], a[228:231], a[176:179], v[36:39]\n\t"
"v_fma_f32 v141, -v141, s66, v43\n\t"
"v_fma_f32 v43, v45, s52, v143\n\t"
"v_mfma_f32_16x16x32_bf16 v[36:39], v[248:251], a[180:183], v[36:39]\n\t"
"buffer_load_dwordx4 v[240:243], off, s[80:83], s20 offset:1024 sc1\n\t"
"v_fma_f32 v142, -v142, s66, v44\n\t"
"v_fma_f32 v143, -v143, s66, v45\n\t"
"v_fma_f32 v44, v46, s52, v144\n\t"
"v_fma_f32 v45, v47, s52, v145\n\t"
"v_mfma_f32_16x16x32_bf16 v[40:43], v[248:251], a[184:187], v[40:43]\n\t"
"v_fma_f32 v144, -v144, s66, v46\n\t"
"v_fma_f32 v46, v48, s52, v146\n\t"
"v_mfma_f32_16x16x32_bf16 v[40:43], a[228:231], a[180:183], v[40:43]\n\t"
"v_fma_f32 v145, -v145, s66, v47\n\t"
"v_fma_f32 v47, v49, s52, v147\n\t"
"v_mfma_f32_16x16x32_bf16 v[40:43], a[224:227], a[176:179], v[40:43]\n\t"
"ds_read_b64_tr_b16 v[232:233], v246 offset:13312\n\t"
"v_fma_f32 v146, -v146, s66, v48\n\t"
"v_fma_f32 v147, -v147, s66, v49\n\t"
"v_cvt_pk_bf16_f32 v228, v228, v229\n\t"
"v_cvt_pk_bf16_f32 v229, v230, v231\n\t"
"v_cvt_pk_bf16_f32 v230, v224, v225\n\t"
"v_cvt_pk_bf16_f32 v231, v226, v227\n\t"
"v_fma_f32 v48, v50, s52, v148\n\t"
"v_fma_f32 v49, v51, s52, v149\n\t"
"v_mfma_f32_16x16x32_bf16 v[44:47], a[224:227], a[180:183], v[44:47]\n\t"
"ds_read_b64_tr_b16 v[234:235], v246 offset:16384\n\t"
"v_fma_f32 v148, -v148, s66, v50\n\t"
"v_mfma_f32_16x16x32_bf16 v[44:47], a[228:231], a[184:187], v[44:47]\n\t"
"ds_read_b64_tr_b16 v[224:225], v246 offset:20992\n\t"
"v_fma_f32 v50, v52, s52, v150\n\t"
"v_mfma_f32_16x16x32_bf16 v[44:47], v[248:251], a[188:191], v[44:47]\n\t"
"ds_read_b64_tr_b16 v[226:227], v246 offset:24064\n\t"
"v_fma_f32 v149, -v149, s66, v51\n\t"
"v_fma_f32 v51, v53, s52, v151\n\t"
"v_fma_f32 v150, -v150, s66, v52\n\t"
"v_fma_f32 v151, -v151, s66, v53\n\t"
"v_fma_f32 v52, v54, s52, v152\n\t"
"v_fma_f32 v53, v55, s52, v153\n\t"
"v_mfma_f32_16x16x32_bf16 v[48:51], v[248:251], a[192:195], v[48:51]\n\t"
"v_fma_f32 v152, -v152, s66, v54\n\t"
"v_fma_f32 v54, v56, s52, v154\n\t"
"v_mfma_f32_16x16x32_bf16 v[48:51], a[228:231], a[188:191], v[48:51]\n\t"
"v_fma_f32 v153, -v153, s66, v55\n\t"
"v_fma_f32 v55, v57, s52, v155\n\t"
"v_mfma_f32_16x16x32_bf16 v[48:51], a[224:227], a[184:187], v[48:51]\n\t"
"v_fma_f32 v154, -v154, s66, v56\n\t"
"v_fma_f32 v155, -v155, s66, v57\n\t"
"v_fma_f32 v56, v58, s52, v156\n\t"
"v_fma_f32 v57, v59, s52, v157\n\t"
"v_mfma_f32_16x16x32_bf16 v[52:55], a[224:227], a[188:191], v[52:55]\n\t"
"v_fma_f32 v156, -v156, s66, v58\n\t"
"v_fma_f32 v58, v60, s52, v158\n\t"
"v_mfma_f32_16x16x32_bf16 v[52:55], a[228:231], a[192:195], v[52:55]\n\t"
"v_fma_f32 v157, -v157, s66, v59\n\t"
"v_fma_f32 v59, v61, s52, v159\n\t"
"v_mfma_f32_16x16x32_bf16 v[52:55], v[248:251], a[196:199], v[52:55]\n\t"
"v_fma_f32 v158, -v158, s66, v60\n\t"
"v_fma_f32 v159, -v159, s66, v61\n\t"
"v_fma_f32 v60, v62, s52, v160\n\t"
"v_fma_f32 v61, v63, s52, v161\n\t"
"v_mfma_f32_16x16x32_bf16 v[56:59], v[248:251], a[200:203], v[56:59]\n\t"
"v_fma_f32 v160, -v160, s66, v62\n\t"
"v_fma_f32 v62, v64, s52, v162\n\t"
"v_mfma_f32_16x16x32_bf16 v[56:59], a[228:231], a[196:199], v[56:59]\n\t"
"v_fma_f32 v161, -v161, s66, v63\n\t"
"v_fma_f32 v63, v65, s52, v163\n\t"
"v_mfma_f32_16x16x32_bf16 v[56:59], a[224:227], a[192:195], v[56:59]\n\t"
"v_fma_f32 v162, -v162, s66, v64\n\t"
"v_fma_f32 v163, -v163, s66, v65\n\t"
"v_mfma_f32_16x16x32_bf16 v[60:63], a[224:227], a[196:199], v[60:63]\n\t"
"s_waitcnt lgkmcnt(2)\n\t"
"v_fma_f32 v64, v66, s52, v164\n\t"
"v_fma_f32 v65, v67, s52, v165\n\t"
"v_mfma_f32_16x16x32_bf16 v[60:63], a[228:231], a[200:203], v[60:63]\n\t"
"s_waitcnt lgkmcnt(0)\n\t"
"v_fma_f32 v164, -v164, s66, v66\n\t"
"v_fma_f32 v66, v68, s52, v166\n\t"
"v_mfma_f32_16x16x32_bf16 v[60:63], v[248:251], a[204:207], v[60:63]\n\t"
"v_fma_f32 v165, -v165, s66, v67\n\t"
"v_fma_f32 v67, v69, s52, v167\n\t"
"v_fma_f32 v166, -v166, s66, v68\n\t"
"v_fma_f32 v167, -v167, s66, v69\n\t"
"v_mfma_f32_16x16x32_bf16 v[232:235], v[232:235], v[220:223], 0\n\t"
"s_waitcnt vmcnt(11)\n\t"
"v_fma_f32 v68, v70, s52, v168\n\t"
"v_fma_f32 v69, v71, s52, v169\n\t"
"v_mfma_f32_16x16x32_bf16 v[224:227], v[224:227], v[220:223], 0\n\t"
"v_fma_f32 v168, -v168, s66, v70\n\t"
"v_fma_f32 v70, v72, s52, v170\n\t"
"v_mfma_f32_16x16x32_bf16 v[64:67], a[236:239], a[168:171], v[64:67]\n\t"
"ds_read_b128 a[168:171], v244 offset:33792\n\t"
"v_fma_f32 v169, -v169, s66, v71\n\t"
"v_mfma_f32_16x16x32_bf16 v[64:67], a[240:243], a[176:179], v[64:67]\n\t"
"v_fma_f32 v71, v73, s52, v171\n\t"
"v_fma_f32 v170, -v170, s66, v72\n\t"
"v_mfma_f32_16x16x32_bf16 v[64:67], a[232:235], a[172:175], v[64:67]\n\t"
"v_fma_f32 v171, -v171, s66, v73\n\t"
"v_fma_f32 v72, v74, s52, v172\n\t"
"v_fma_f32 v73, v75, s52, v173\n\t"
"v_mfma_f32_16x16x32_bf16 v[68:71], a[236:239], a[172:175], v[68:71]\n\t"
"ds_read_b128 a[172:175], v244 offset:34304\n\t"
"v_fma_f32 v172, -v172, s66, v74\n\t"
"v_mfma_f32_16x16x32_bf16 v[68:71], a[232:235], a[176:179], v[68:71]\n\t"
"v_fma_f32 v74, v76, s52, v174\n\t"
"v_fma_f32 v173, -v173, s66, v75\n\t"
"v_mfma_f32_16x16x32_bf16 v[68:71], a[240:243], a[180:183], v[68:71]\n\t"
"buffer_load_dwordx4 a[208:211], off, s[80:83], s20 offset:2048 sc1\n\t"
"v_fma_f32 v75, v77, s52, v175\n\t"
"v_fma_f32 v174, -v174, s66, v76\n\t"
"v_fma_f32 v175, -v175, s66, v77\n\t"
"v_fma_f32 v76, v78, s52, v176\n\t"
"v_fma_f32 v77, v79, s52, v177\n\t"
"s_mov_b32 s20, 98304\n\t"
"v_mfma_f32_16x16x32_bf16 v[72:75], a[240:243], a[184:187], v[72:75]\n\t"
"buffer_load_dwordx4 v[248:251], off, s[80:83], s20 offset:0 sc1\n\t"
"v_mfma_f32_16x16x32_bf16 v[72:75], a[232:235], a[180:183], v[72:75]\n\t"
"v_fma_f32 v176, -v176, s66, v78\n\t"
"v_fma_f32 v78, v80, s52, v178\n\t"
"v_mfma_f32_16x16x32_bf16 v[72:75], a[236:239], a[176:179], v[72:75]\n\t"
"ds_read_b128 a[176:179], v244 offset:34816\n\t"
"v_fma_f32 v177, -v177, s66, v79\n\t"
"v_fma_f32 v79, v81, s52, v179\n\t"
"v_fma_f32 v178, -v178, s66, v80\n\t"
"v_fma_f32 v179, -v179, s66, v81\n\t"
"v_cvt_pk_bf16_f32 v232, v232, v233\n\t"
"v_cvt_pk_bf16_f32 v233, v234, v235\n\t"
"v_cvt_pk_bf16_f32 v234, v224, v225\n\t"
"v_cvt_pk_bf16_f32 v235, v226, v227\n\t"
"v_fma_f32 v80, v82, s52, v180\n\t"
"v_fma_f32 v81, v83, s52, v181\n\t"
"v_mfma_f32_16x16x32_bf16 v[76:79], a[236:239], a[180:183], v[76:79]\n\t"
"ds_read_b128 a[180:183], v244 offset:35328\n\t"
"v_fma_f32 v180, -v180, s66, v82\n\t"
"v_mfma_f32_16x16x32_bf16 v[76:79], a[232:235], a[184:187], v[76:79]\n\t"
"ds_read_b64_tr_b16 v[224:225], v246 offset:27136\n\t"
"v_fma_f32 v82, v84, s52, v182\n\t"
"v_mfma_f32_16x16x32_bf16 v[76:79], a[240:243], a[188:191], v[76:79]\n\t"
"buffer_load_dwordx4 a[212:215], off, s[80:83], s20 offset:1024 sc1\n\t"
"v_fma_f32 v181, -v181, s66, v83\n\t"
"v_fma_f32 v83, v85, s52, v183\n\t"
"v_fma_f32 v182, -v182, s66, v84\n\t"
"v_fma_f32 v183, -v183, s66, v85\n\t"
"v_fma_f32 v84, v86, s52, v184\n\t"
"v_fma_f32 v85, v87, s52, v185\n\t"
"v_mfma_f32_16x16x32_bf16 v[80:83], a[240:243], a[192:195], v[80:83]\n\t"
"ds_read_b64_tr_b16 v[226:227], v246 offset:30208\n\t"
"v_fma_f32 v184, -v184, s66, v86\n\t"
"v_mfma_f32_16x16x32_bf16 v[80:83], a[232:235], a[188:191], v[80:83]\n\t"
"v_fma_f32 v86, v88, s52, v186\n\t"
"v_fma_f32 v185, -v185, s66, v87\n\t"
"v_mfma_f32_16x16x32_bf16 v[80:83], a[236:239], a[184:187], v[80:83]\n\t"
"buffer_load_dwordx4 a[216:219], off, s[80:83], s20 offset:2048 sc1\n\t"
"v_fma_f32 v87, v89, s52, v187\n\t"
"v_fma_f32 v186, -v186, s66, v88\n\t"
"v_fma_f32 v187, -v187, s66, v89\n\t"
"v_fma_f32 v88, v90, s52, v188\n\t"
"v_fma_f32 v89, v91, s52, v189\n\t"
"s_mov_b32 s20, 331776\n\t"
"v_mfma_f32_16x16x32_bf16 v[84:87], a[236:239], a[188:191], v[84:87]\n\t"
"ds_read_b128 a[184:187], v244 offset:35840\n\t"
"v_fma_f32 v188, -v188, s66, v90\n\t"
"v_mfma_f32_16x16x32_bf16 v[84:87], a[232:235], a[192:195], v[84:87]\n\t"
"ds_read_b128 a[188:191], v244 offset:36352\n\t"
"v_fma_f32 v90, v92, s52, v190\n\t"
"v_mfma_f32_16x16x32_bf16 v[84:87], a[240:243], a[196:199], v[84:87]\n\t"
"buffer_load_dwordx4 a[220:223], off, s[80:83], s20 offset:0 sc1\n\t"
"v_fma_f32 v189, -v189, s66, v91\n\t"
"v_fma_f32 v91, v93, s52, v191\n\t"
"v_fma_f32 v190, -v190, s66, v92\n\t"
"v_fma_f32 v191, -v191, s66, v93\n\t"
"v_fma_f32 v92, v94, s52, v192\n\t"
"v_fma_f32 v93, v95, s52, v193\n\t"
"v_mfma_f32_16x16x32_bf16 v[88:91], a[240:243], a[200:203], v[88:91]\n\t"
"v_fma_f32 v192, -v192, s66, v94\n\t"
"v_fma_f32 v94, v96, s52, v194\n\t"
"v_mfma_f32_16x16x32_bf16 v[88:91], a[232:235], a[196:199], v[88:91]\n\t"
"v_fma_f32 v193, -v193, s66, v95\n\t"
"v_fma_f32 v95, v97, s52, v195\n\t"
"v_mfma_f32_16x16x32_bf16 v[88:91], a[236:239], a[192:195], v[88:91]\n\t"
"buffer_load_dwordx4 a[224:227], off, s[80:83], s20 offset:1024 sc1\n\t"
"v_fma_f32 v194, -v194, s66, v96\n\t"
"v_fma_f32 v195, -v195, s66, v97\n\t"
"v_mfma_f32_16x16x32_bf16 v[92:95], a[236:239], a[196:199], v[92:95]\n\t"
"s_waitcnt lgkmcnt(2)\n\t"
"ds_read_b128 a[192:195], v244 offset:36864\n\t"
"v_mfma_f32_16x16x32_bf16 v[92:95], a[232:235], a[200:203], v[92:95]\n\t"
"buffer_load_dwordx4 v[96:99], off, s[80:83], s20 offset:2048 sc1\n\t"
"v_mfma_f32_16x16x32_bf16 v[92:95], a[240:243], a[204:207], v[92:95]\n\t"
"s_waitcnt vmcnt(15)\n\t"
"ds_read_b128 a[196:199], v244 offset:37376\n\t"
"s_mov_b32 s20, 344064\n\t"
"v_mfma_f32_16x16x32_bf16 v[224:227], v[224:227], v[220:223], 0\n\t"
"ds_read_b128 a[200:203], v244 offset:37888\n\t"
"v_mfma_f32_16x16x32_bf16 v[100:103], a[248:251], a[168:171], v[100:103]\n\t"
"ds_read_b128 a[204:207], v244 offset:38400\n\t"
"v_mfma_f32_16x16x32_bf16 v[100:103], a[252:255], a[176:179], v[100:103]\n\t"
"buffer_load_dwordx4 a[228:231], off, s[80:83], s20 offset:0 sc1\n\t"
"v_mfma_f32_16x16x32_bf16 v[100:103], a[244:247], a[172:175], v[100:103]\n\t"
"buffer_load_dwordx4 a[232:235], off, s[80:83], s20 offset:1024 sc1\n\t"
"v_mfma_f32_16x16x32_bf16 v[104:107], a[248:251], a[172:175], v[104:107]\n\t"
"s_waitcnt lgkmcnt(5)\n\t"
"v_mfma_f32_16x16x32_bf16 v[104:107], a[244:247], a[176:179], v[104:107]\n\t"
"v_cvt_pk_bf16_f32 v224, v224, v225\n\t"
"v_mfma_f32_16x16x32_bf16 v[104:107], a[252:255], a[180:183], v[104:107]\n\t"
"s_waitcnt lgkmcnt(0)\n\t"
"s_barrier\n\t"
"v_mfma_f32_16x16x32_bf16 v[108:111], a[252:255], a[184:187], v[108:111]\n\t"
"v_cvt_pk_bf16_f32 v225, v226, v227\n\t"
"v_mfma_f32_16x16x32_bf16 v[108:111], a[244:247], a[180:183], v[108:111]\n\t"
"buffer_load_dwordx4 a[236:239], off, s[80:83], s20 offset:2048 sc1\n\t"
"v_mfma_f32_16x16x32_bf16 v[108:111], a[248:251], a[176:179], v[108:111]\n\t"
"s_mov_b32 s20, 356352\n\t"
"ds_write2st64_b64 v245, v[228:229], v[230:231] offset0:48 offset1:49\n\t"
"v_mfma_f32_16x16x32_bf16 v[112:115], a[248:251], a[180:183], v[112:115]\n\t"
"buffer_load_dwordx4 a[240:243], off, s[80:83], s20 offset:0 sc1\n\t"
"v_mfma_f32_16x16x32_bf16 v[112:115], a[244:247], a[184:187], v[112:115]\n\t"
"ds_read_b64_tr_b16 v[228:229], v246 offset:33280\n\t"
"v_mfma_f32_16x16x32_bf16 v[112:115], a[252:255], a[188:191], v[112:115]\n\t"
"ds_write2st64_b64 v245, v[232:233], v[234:235] offset0:50 offset1:51\n\t"
"v_mfma_f32_16x16x32_bf16 v[116:119], a[252:255], a[192:195], v[116:119]\n\t"
"v_mfma_f32_16x16x32_bf16 v[116:119], a[244:247], a[188:191], v[116:119]\n\t"
"ds_read_b64_tr_b16 v[230:231], v246 offset:36352\n\t"
"v_mfma_f32_16x16x32_bf16 v[116:119], a[248:251], a[184:187], v[116:119]\n\t"
"ds_write_b64 v245, v[224:225] offset:26624\n\t"
"v_mfma_f32_16x16x32_bf16 v[120:123], a[248:251], a[188:191], v[120:123]\n\t"
"ds_read_b64_tr_b16 v[232:233], v246 offset:40960\n\t"
"v_mfma_f32_16x16x32_bf16 v[120:123], a[244:247], a[192:195], v[120:123]\n\t"
"ds_read_b64_tr_b16 v[234:235], v246 offset:44032\n\t"
"v_mfma_f32_16x16x32_bf16 v[120:123], a[252:255], a[196:199], v[120:123]\n\t"
"ds_read_b64_tr_b16 v[224:225], v246 offset:47104\n\t"
"v_mfma_f32_16x16x32_bf16 v[124:127], a[244:247], a[196:199], v[124:127]\n\t"
"s_waitcnt lgkmcnt(4)\n\t"
"ds_read_b64_tr_b16 v[226:227], v246 offset:50176\n\t"
"v_mfma_f32_16x16x32_bf16 v[124:127], a[248:251], a[192:195], v[124:127]\n\t"
"v_mfma_f32_16x16x32_bf16 v[124:127], a[252:255], a[200:203], v[124:127]\n\t"
"v_mfma_f32_16x16x32_bf16 v[228:231], v[228:231], v[220:223], 0\n\t"
"s_waitcnt lgkmcnt(2)\n\t"
"v_mfma_f32_16x16x32_bf16 v[232:235], v[232:235], v[220:223], 0\n\t"
"v_mfma_f32_16x16x32_bf16 v[128:131], a[244:247], a[200:203], v[128:131]\n\t"
"buffer_load_dwordx4 a[244:247], off, s[80:83], s20 offset:1024 sc1\n\t"
"v_mfma_f32_16x16x32_bf16 v[128:131], a[248:251], a[196:199], v[128:131]\n\t"
"s_waitcnt vmcnt(11) lgkmcnt(0)\n\t"
"v_mfma_f32_16x16x32_bf16 v[128:131], a[252:255], a[204:207], v[128:131]\n\t"
"v_mfma_f32_16x16x32_bf16 v[224:227], v[224:227], v[220:223], 0\n\t"
"buffer_load_dwordx4 a[248:251], off, s[80:83], s20 offset:2048 sc1\n\t"
"v_mfma_f32_16x16x32_bf16 v[132:135], v[236:239], a[168:171], v[132:135]\n\t"
"s_mov_b32 s20, 368640\n\t"
"v_cvt_pk_bf16_f32 v228, v228, v229\n\t"
"v_cvt_pk_bf16_f32 v229, v230, v231\n\t"
"v_mfma_f32_16x16x32_bf16 v[132:135], a[208:211], a[176:179], v[132:135]\n\t"
"v_cvt_pk_bf16_f32 v230, v232, v233\n\t"
"v_cvt_pk_bf16_f32 v231, v234, v235\n\t"
"v_mfma_f32_16x16x32_bf16 v[132:135], v[240:243], a[172:175], v[132:135]\n\t"
"ds_write2st64_b64 v245, v[228:229], v[230:231] offset0:53 offset1:54\n\t"
"v_mfma_f32_16x16x32_bf16 v[136:139], v[236:239], a[172:175], v[136:139]\n\t"
"buffer_load_dwordx4 a[252:255], off, s[80:83], s20 offset:0 sc1\n\t"
"v_mfma_f32_16x16x32_bf16 v[136:139], v[240:243], a[176:179], v[136:139]\n\t"
"ds_read_b64_tr_b16 v[232:233], v246 offset:53248\n\t"
"v_mfma_f32_16x16x32_bf16 v[136:139], a[208:211], a[180:183], v[136:139]\n\t"
"ds_read_b64_tr_b16 v[234:235], v246 offset:56320\n\t"
"v_cvt_pk_bf16_f32 v224, v224, v225\n\t"
"v_mfma_f32_16x16x32_bf16 v[140:143], a[208:211], a[184:187], v[140:143]\n\t"
"v_cvt_pk_bf16_f32 v225, v226, v227\n\t"
"v_mfma_f32_16x16x32_bf16 v[140:143], v[240:243], a[180:183], v[140:143]\n\t"
"v_mfma_f32_16x16x32_bf16 v[140:143], v[236:239], a[176:179], v[140:143]\n\t"
"v_mfma_f32_16x16x32_bf16 v[144:147], v[236:239], a[180:183], v[144:147]\n\t"
"v_mfma_f32_16x16x32_bf16 v[144:147], v[240:243], a[184:187], v[144:147]\n\t"
"v_mfma_f32_16x16x32_bf16 v[144:147], a[208:211], a[188:191], v[144:147]\n\t"
"v_mfma_f32_16x16x32_bf16 v[148:151], v[240:243], a[188:191], v[148:151]\n\t"
"s_waitcnt lgkmcnt(0)\n\t"
"s_add_u32 m0, s24, 25984\n\t"
"s_add_u32 s28, s21, 786432\n\t"
"v_mfma_f32_16x16x32_bf16 v[148:151], a[208:211], a[192:195], v[148:151]\n\t"
"v_mfma_f32_16x16x32_bf16 v[148:151], v[236:239], a[184:187], v[148:151]\n\t"
"v_mfma_f32_16x16x32_bf16 v[232:235], v[232:235], v[220:223], 0\n\t"
"v_mfma_f32_16x16x32_bf16 v[152:155], v[236:239], a[188:191], v[152:155]\n\t"
"buffer_load_dwordx4 v247, s[12:15], s28 offen offset:128 nt lds\n\t"
"v_mfma_f32_16x16x32_bf16 v[152:155], a[208:211], a[196:199], v[152:155]\n\t"
"s_add_u32 m0, s24, 72064\n\t"
"s_add_u32 s28, s21, 2162688\n\t"
"v_mfma_f32_16x16x32_bf16 v[152:155], v[240:243], a[192:195], v[152:155]\n\t"
"buffer_load_dwordx4 v247, s[12:15], s28 offen offset:128 nt lds\n\t"
"v_mfma_f32_16x16x32_bf16 v[156:159], v[236:239], a[192:195], v[156:159]\n\t"
"s_add_u32 m0, s24, 79744\n\t"
"s_add_u32 s28, s21, 2359296\n\t"
"v_mfma_f32_16x16x32_bf16 v[156:159], v[240:243], a[196:199], v[156:159]\n\t"
"buffer_load_dwordx4 v247, s[12:15], s28 offen offset:128 nt lds\n\t"
"v_mfma_f32_16x16x32_bf16 v[156:159], a[208:211], a[200:203], v[156:159]\n\t"
"s_add_u32 m0, s24, 85888\n\t"
"s_add_u32 s28, s21, 2555904\n\t"
"v_cvt_pk_bf16_f32 v226, v232, v233\n\t"
"v_cvt_pk_bf16_f32 v227, v234, v235\n\t"
"v_mfma_f32_16x16x32_bf16 v[160:163], v[240:243], a[200:203], v[160:163]\n\t"
"s_waitcnt vmcnt(13)\n\t"
"v_mfma_f32_16x16x32_bf16 v[160:163], v[236:239], a[196:199], v[160:163]\n\t"
"buffer_load_dwordx4 v247, s[12:15], s28 offen offset:128 nt lds\n\t"
"v_mfma_f32_16x16x32_bf16 v[160:163], a[208:211], a[204:207], v[160:163]\n\t"
"s_add_u32 m0, s25, -128\n\t"
"ds_write2st64_b64 v245, v[224:225], v[226:227] offset0:55 offset1:56\n\t"
"v_mfma_f32_16x16x32_bf16 v[164:167], v[248:251], a[168:171], v[164:167]\n\t"
"buffer_load_dwordx4 v254, s[12:15], 0 offen offset:128 nt lds\n\t"
"v_mfma_f32_16x16x32_bf16 v[164:167], a[216:219], a[176:179], v[164:167]\n\t"
"s_add_u32 m0, s26, -128\n\t"
"v_mfma_f32_16x16x32_bf16 v[164:167], a[212:215], a[172:175], v[164:167]\n\t"
"s_waitcnt lgkmcnt(0)\n\t"
"s_barrier\n\t"
"v_mfma_f32_16x16x32_bf16 v[168:171], v[248:251], a[172:175], v[168:171]\n\t"
"ds_read_b128 a[168:171], v244 offset:24576\n\t"
"v_mfma_f32_16x16x32_bf16 v[168:171], a[212:215], a[176:179], v[168:171]\n\t"
"ds_read_b128 a[172:175], v244 offset:25088\n\t"
"v_mfma_f32_16x16x32_bf16 v[168:171], a[216:219], a[180:183], v[168:171]\n\t"
"s_mov_b64 exec, s[36:37]\n\t"
"buffer_load_dwordx4 v254, s[12:15], s27 offen offset:128 nt lds\n\t"
"s_mov_b64 exec, -1\n\t"
"v_mfma_f32_16x16x32_bf16 v[172:175], a[216:219], a[184:187], v[172:175]\n\t"
"v_mfma_f32_16x16x32_bf16 v[172:175], a[212:215], a[180:183], v[172:175]\n\t"
"v_mfma_f32_16x16x32_bf16 v[172:175], v[248:251], a[176:179], v[172:175]\n\t"
"ds_read_b128 a[176:179], v244 offset:25600\n\t"
"v_mfma_f32_16x16x32_bf16 v[176:179], v[248:251], a[180:183], v[176:179]\n\t"
"ds_read_b128 a[180:183], v244 offset:26112\n\t"
"v_mfma_f32_16x16x32_bf16 v[176:179], a[212:215], a[184:187], v[176:179]\n\t"
"v_mfma_f32_16x16x32_bf16 v[176:179], a[216:219], a[188:191], v[176:179]\n\t"
"v_mfma_f32_16x16x32_bf16 v[180:183], a[216:219], a[192:195], v[180:183]\n\t"
"v_mfma_f32_16x16x32_bf16 v[180:183], a[212:215], a[188:191], v[180:183]\n\t"
"v_mfma_f32_16x16x32_bf16 v[180:183], v[248:251], a[184:187], v[180:183]\n\t"
"ds_read_b128 a[184:187], v244 offset:26624\n\t"
"v_mfma_f32_16x16x32_bf16 v[184:187], v[248:251], a[188:191], v[184:187]\n\t"
"ds_read_b128 a[188:191], v244 offset:27136\n\t"
"v_mfma_f32_16x16x32_bf16 v[184:187], a[212:215], a[192:195], v[184:187]\n\t"
"v_mfma_f32_16x16x32_bf16 v[184:187], a[216:219], a[196:199], v[184:187]\n\t"
"v_mfma_f32_16x16x32_bf16 v[188:191], a[216:219], a[200:203], v[188:191]\n\t"
"v_xor_b32 v246, 0xc0, v246\n\t"
"v_mfma_f32_16x16x32_bf16 v[188:191], a[212:215], a[196:199], v[188:191]\n\t"
"v_mfma_f32_16x16x32_bf16 v[188:191], v[248:251], a[192:195], v[188:191]\n\t"
"ds_read_b128 a[192:195], v244 offset:27648\n\t"
"v_mfma_f32_16x16x32_bf16 v[192:195], v[248:251], a[196:199], v[192:195]\n\t"
"s_waitcnt vmcnt(13) lgkmcnt(4)\n\t"
"ds_read_b128 a[196:199], v244 offset:28160\n\t"
"v_mfma_f32_16x16x32_bf16 v[192:195], a[212:215], a[200:203], v[192:195]\n\t"
"ds_read_b128 a[200:203], v244 offset:28672\n\t"
"v_mfma_f32_16x16x32_bf16 v[192:195], a[216:219], a[204:207], v[192:195]\n\t"
"ds_read_b128 a[204:207], v244 offset:29184\n\t"
"Lst22_%=:\n\t"
"v_mfma_f32_16x16x32_bf16 v[0:3], a[220:223], a[168:171], v[0:3]\n\t"
"s_waitcnt lgkmcnt(6)\n\t"
"v_mfma_f32_16x16x32_bf16 v[0:3], v[96:99], a[176:179], v[0:3]\n\t"
"v_mfma_f32_16x16x32_bf16 v[0:3], a[224:227], a[172:175], v[0:3]\n\t"
"v_mfma_f32_16x16x32_bf16 v[4:7], a[220:223], a[172:175], v[4:7]\n\t"
"s_waitcnt lgkmcnt(5)\n\t"
"v_mfma_f32_16x16x32_bf16 v[4:7], a[224:227], a[176:179], v[4:7]\n\t"
"v_mfma_f32_16x16x32_bf16 v[4:7], v[96:99], a[180:183], v[4:7]\n\t"
"v_mfma_f32_16x16x32_bf16 v[8:11], v[96:99], a[184:187], v[8:11]\n\t"
"buffer_load_dwordx4 v[240:243], off, s[80:83], s20 offset:1024 sc1\n\t"
"v_mfma_f32_16x16x32_bf16 v[8:11], a[224:227], a[180:183], v[8:11]\n\t"
"s_waitcnt lgkmcnt(4)\n\t"
"v_mfma_f32_16x16x32_bf16 v[8:11], a[220:223], a[176:179], v[8:11]\n\t"
"ds_read_b64_tr_b16 v[228:229], v246 offset:1024\n\t"
"v_mfma_f32_16x16x32_bf16 v[12:15], a[220:223], a[180:183], v[12:15]\n\t"
"s_waitcnt lgkmcnt(4)\n\t"
"ds_read_b64_tr_b16 v[230:231], v246 offset:4096\n\t"
"v_mfma_f32_16x16x32_bf16 v[12:15], a[224:227], a[184:187], v[12:15]\n\t"
"ds_read_b64_tr_b16 v[232:233], v246 offset:7168\n\t"
"v_mfma_f32_16x16x32_bf16 v[12:15], v[96:99], a[188:191], v[12:15]\n\t"
"ds_read_b64_tr_b16 v[234:235], v246 offset:10240\n\t"
"v_mfma_f32_16x16x32_bf16 v[16:19], v[96:99], a[192:195], v[16:19]\n\t"
"s_waitcnt lgkmcnt(6)\n\t"
"v_mfma_f32_16x16x32_bf16 v[16:19], a[224:227], a[188:191], v[16:19]\n\t"
"v_mfma_f32_16x16x32_bf16 v[16:19], a[220:223], a[184:187], v[16:19]\n\t"
"v_mfma_f32_16x16x32_bf16 v[20:23], a[220:223], a[188:191], v[20:23]\n\t"
"s_waitcnt lgkmcnt(5)\n\t"
"v_mfma_f32_16x16x32_bf16 v[20:23], a[224:227], a[192:195], v[20:23]\n\t"
"v_mfma_f32_16x16x32_bf16 v[20:23], v[96:99], a[196:199], v[20:23]\n\t"
"v_mfma_f32_16x16x32_bf16 v[24:27], v[96:99], a[200:203], v[24:27]\n\t"
"s_waitcnt lgkmcnt(4)\n\t"
"v_mfma_f32_16x16x32_bf16 v[24:27], a[224:227], a[196:199], v[24:27]\n\t"
"v_mfma_f32_16x16x32_bf16 v[24:27], a[220:223], a[192:195], v[24:27]\n\t"
"v_mfma_f32_16x16x32_bf16 v[28:31], a[220:223], a[196:199], v[28:31]\n\t"
"s_waitcnt lgkmcnt(2)\n\t"
"v_mfma_f32_16x16x32_bf16 v[28:31], a[224:227], a[200:203], v[28:31]\n\t"
"s_waitcnt lgkmcnt(0)\n\t"
"v_mfma_f32_16x16x32_bf16 v[28:31], v[96:99], a[204:207], v[28:31]\n\t"
"s_waitcnt vmcnt(11)\n\t"
"v_mfma_f32_16x16x32_bf16 v[228:231], v[228:231], v[220:223], 0\n\t"
"v_mfma_f32_16x16x32_bf16 v[232:235], v[232:235], v[220:223], 0\n\t"
"v_mfma_f32_16x16x32_bf16 v[32:35], a[228:231], a[168:171], v[32:35]\n\t"
"v_mfma_f32_16x16x32_bf16 v[32:35], a[236:239], a[176:179], v[32:35]\n\t"
"v_mfma_f32_16x16x32_bf16 v[32:35], a[232:235], a[172:175], v[32:35]\n\t"
"v_mfma_f32_16x16x32_bf16 v[36:39], a[228:231], a[172:175], v[36:39]\n\t"
"v_cvt_pk_bf16_f32 v228, v228, v229\n\t"
"v_cvt_pk_bf16_f32 v229, v230, v231\n\t"
"v_mfma_f32_16x16x32_bf16 v[36:39], a[232:235], a[176:179], v[36:39]\n\t"
"v_cvt_pk_bf16_f32 v230, v232, v233\n\t"
"v_cvt_pk_bf16_f32 v231, v234, v235\n\t"
"v_mfma_f32_16x16x32_bf16 v[36:39], a[236:239], a[180:183], v[36:39]\n\t"
"v_mfma_f32_16x16x32_bf16 v[40:43], a[236:239], a[184:187], v[40:43]\n\t"
"v_mfma_f32_16x16x32_bf16 v[40:43], a[232:235], a[180:183], v[40:43]\n\t"
"v_mfma_f32_16x16x32_bf16 v[40:43], a[228:231], a[176:179], v[40:43]\n\t"
"ds_read_b64_tr_b16 v[224:225], v246 offset:13312\n\t"
"v_mfma_f32_16x16x32_bf16 v[44:47], a[228:231], a[180:183], v[44:47]\n\t"
"ds_read_b64_tr_b16 v[226:227], v246 offset:16384\n\t"
"v_mfma_f32_16x16x32_bf16 v[44:47], a[232:235], a[184:187], v[44:47]\n\t"
"ds_read_b64_tr_b16 v[232:233], v246 offset:20992\n\t"
"v_mfma_f32_16x16x32_bf16 v[44:47], a[236:239], a[188:191], v[44:47]\n\t"
"buffer_load_dwordx4 a[208:211], off, s[80:83], s20 offset:2048 sc1\n\t"
"v_mfma_f32_16x16x32_bf16 v[48:51], a[236:239], a[192:195], v[48:51]\n\t"
"s_mov_b32 s20, 380928\n\t"
"ds_read_b64_tr_b16 v[234:235], v246 offset:24064\n\t"
"v_mfma_f32_16x16x32_bf16 v[48:51], a[232:235], a[188:191], v[48:51]\n\t"
"v_mfma_f32_16x16x32_bf16 v[48:51], a[228:231], a[184:187], v[48:51]\n\t"
"v_mfma_f32_16x16x32_bf16 v[52:55], a[228:231], a[188:191], v[52:55]\n\t"
"buffer_load_dwordx4 v[236:239], off, s[80:83], s20 offset:0 sc1\n\t"
"v_mfma_f32_16x16x32_bf16 v[52:55], a[232:235], a[192:195], v[52:55]\n\t"
"v_mfma_f32_16x16x32_bf16 v[52:55], a[236:239], a[196:199], v[52:55]\n\t"
"v_mfma_f32_16x16x32_bf16 v[56:59], a[236:239], a[200:203], v[56:59]\n\t"
"v_mfma_f32_16x16x32_bf16 v[56:59], a[232:235], a[196:199], v[56:59]\n\t"
"buffer_load_dwordx4 v[96:99], off, s[80:83], s20 offset:1024 sc1\n\t"
"v_mfma_f32_16x16x32_bf16 v[56:59], a[228:231], a[192:195], v[56:59]\n\t"
"v_mfma_f32_16x16x32_bf16 v[60:63], a[228:231], a[196:199], v[60:63]\n\t"
"s_waitcnt lgkmcnt(2)\n\t"
"v_mfma_f32_16x16x32_bf16 v[60:63], a[232:235], a[200:203], v[60:63]\n\t"
"s_waitcnt lgkmcnt(0)\n\t"
"v_mfma_f32_16x16x32_bf16 v[60:63], a[236:239], a[204:207], v[60:63]\n\t"
"buffer_load_dwordx4 v[248:251], off, s[80:83], s20 offset:2048 sc1\n\t"
"v_mfma_f32_16x16x32_bf16 v[224:227], v[224:227], v[220:223], 0\n\t"
"s_waitcnt vmcnt(12)\n\t"
"s_mov_b32 s20, 393216\n\t"
"v_mfma_f32_16x16x32_bf16 v[232:235], v[232:235], v[220:223], 0\n\t"
"v_mfma_f32_16x16x32_bf16 v[64:67], a[240:243], a[168:171], v[64:67]\n\t"
"ds_read_b128 a[168:171], v244 offset:33792\n\t"
"v_mfma_f32_16x16x32_bf16 v[64:67], a[248:251], a[176:179], v[64:67]\n\t"
"buffer_load_dwordx4 a[212:215], off, s[80:83], s20 offset:0 sc1\n\t"
"v_mfma_f32_16x16x32_bf16 v[64:67], a[244:247], a[172:175], v[64:67]\n\t"
"v_mfma_f32_16x16x32_bf16 v[68:71], a[240:243], a[172:175], v[68:71]\n\t"
"ds_read_b128 a[172:175], v244 offset:34304\n\t"
"v_cvt_pk_bf16_f32 v224, v224, v225\n\t"
"v_mfma_f32_16x16x32_bf16 v[68:71], a[244:247], a[176:179], v[68:71]\n\t"
"v_cvt_pk_bf16_f32 v225, v226, v227\n\t"
"v_cvt_pk_bf16_f32 v226, v232, v233\n\t"
"v_mfma_f32_16x16x32_bf16 v[68:71], a[248:251], a[180:183], v[68:71]\n\t"
"v_cvt_pk_bf16_f32 v227, v234, v235\n\t"
"v_mfma_f32_16x16x32_bf16 v[72:75], a[248:251], a[184:187], v[72:75]\n\t"
"buffer_load_dwordx4 a[216:219], off, s[80:83], s20 offset:1024 sc1\n\t"
"v_mfma_f32_16x16x32_bf16 v[72:75], a[244:247], a[180:183], v[72:75]\n\t"
"v_mfma_f32_16x16x32_bf16 v[72:75], a[240:243], a[176:179], v[72:75]\n\t"
"ds_read_b128 a[176:179], v244 offset:34816\n\t"
"v_mfma_f32_16x16x32_bf16 v[76:79], a[240:243], a[180:183], v[76:79]\n\t"
"buffer_load_dwordx4 a[220:223], off, s[80:83], s20 offset:2048 sc1\n\t"
"v_mfma_f32_16x16x32_bf16 v[76:79], a[244:247], a[184:187], v[76:79]\n\t"
"s_mov_b32 s20, 405504\n\t"
"ds_read_b128 a[180:183], v244 offset:35328\n\t"
"v_mfma_f32_16x16x32_bf16 v[76:79], a[248:251], a[188:191], v[76:79]\n\t"
"ds_read_b64_tr_b16 v[232:233], v246 offset:27136\n\t"
"v_mfma_f32_16x16x32_bf16 v[80:83], a[248:251], a[192:195], v[80:83]\n\t"
"ds_read_b64_tr_b16 v[234:235], v246 offset:30208\n\t"
"v_mfma_f32_16x16x32_bf16 v[80:83], a[244:247], a[188:191], v[80:83]\n\t"
"buffer_load_dwordx4 a[224:227], off, s[80:83], s20 offset:0 sc1\n\t"
"v_mfma_f32_16x16x32_bf16 v[80:83], a[240:243], a[184:187], v[80:83]\n\t"
"ds_read_b128 a[184:187], v244 offset:35840\n\t"
"v_mfma_f32_16x16x32_bf16 v[84:87], a[240:243], a[188:191], v[84:87]\n\t"
"ds_read_b128 a[188:191], v244 offset:36352\n\t"
"v_mfma_f32_16x16x32_bf16 v[84:87], a[244:247], a[192:195], v[84:87]\n\t"
"v_mfma_f32_16x16x32_bf16 v[84:87], a[248:251], a[196:199], v[84:87]\n\t"
"buffer_load_dwordx4 a[228:231], off, s[80:83], s20 offset:1024 sc1\n\t"
"v_mfma_f32_16x16x32_bf16 v[88:91], a[248:251], a[200:203], v[88:91]\n\t"
"v_mfma_f32_16x16x32_bf16 v[88:91], a[244:247], a[196:199], v[88:91]\n\t"
"s_waitcnt vmcnt(8) lgkmcnt(2)\n\t"
"v_mfma_f32_16x16x32_bf16 v[88:91], a[240:243], a[192:195], v[88:91]\n\t"
"ds_read_b128 a[192:195], v244 offset:36864\n\t"
"v_mfma_f32_16x16x32_bf16 v[92:95], a[240:243], a[196:199], v[92:95]\n\t"
"buffer_load_dwordx4 a[232:235], off, s[80:83], s20 offset:2048 sc1\n\t"
"v_mfma_f32_16x16x32_bf16 v[92:95], a[244:247], a[200:203], v[92:95]\n\t"
"Lpg17_%=:\n\t"
"s_cbranch_vccnz Lvr2_%=\n\t"
"ds_read_b128 a[196:199], v244 offset:37376\n\t"
"s_mov_b32 s20, 417792\n\t"
"v_mfma_f32_16x16x32_bf16 v[92:95], a[248:251], a[204:207], v[92:95]\n\t"
"ds_read_b128 a[200:203], v244 offset:37888\n\t"
"v_mfma_f32_16x16x32_bf16 v[232:235], v[232:235], v[220:223], 0\n\t"
"ds_read_b128 a[204:207], v244 offset:38400\n\t"
"v_mfma_f32_16x16x32_bf16 v[100:103], a[252:255], a[168:171], v[100:103]\n\t"
"buffer_load_dwordx4 a[236:239], off, s[80:83], s20 offset:0 sc1\n\t"
"v_mfma_f32_16x16x32_bf16 v[100:103], a[208:211], a[176:179], v[100:103]\n\t"
"v_mfma_f32_16x16x32_bf16 v[100:103], v[240:243], a[172:175], v[100:103]\n\t"
"v_mfma_f32_16x16x32_bf16 v[104:107], a[252:255], a[172:175], v[104:107]\n\t"
"buffer_load_dwordx4 a[240:243], off, s[80:83], s20 offset:1024 sc1\n\t"
"v_mfma_f32_16x16x32_bf16 v[104:107], v[240:243], a[176:179], v[104:107]\n\t"
"s_waitcnt lgkmcnt(5)\n\t"
"v_cvt_pk_bf16_f32 v232, v232, v233\n\t"
"v_mfma_f32_16x16x32_bf16 v[104:107], a[208:211], a[180:183], v[104:107]\n\t"
"s_waitcnt lgkmcnt(0)\n\t"
"s_barrier\n\t"
"v_mfma_f32_16x16x32_bf16 v[108:111], a[208:211], a[184:187], v[108:111]\n\t"
"s_add_u32 m0, s24, 32128\n\t"
"s_add_u32 s28, s21, 983040\n\t"
"v_mfma_f32_16x16x32_bf16 v[108:111], v[240:243], a[180:183], v[108:111]\n\t"
"buffer_load_dwordx4 a[244:247], off, s[80:83], s20 offset:2048 sc1\n\t"
"v_mfma_f32_16x16x32_bf16 v[108:111], a[252:255], a[176:179], v[108:111]\n\t"
"s_mov_b32 s20, 430080\n\t"
"ds_write2st64_b64 v245, v[228:229], v[230:231] offset0:48 offset1:49\n\t"
"v_cvt_pk_bf16_f32 v233, v234, v235\n\t"
"v_mfma_f32_16x16x32_bf16 v[112:115], a[252:255], a[180:183], v[112:115]\n\t"
"v_mfma_f32_16x16x32_bf16 v[112:115], v[240:243], a[184:187], v[112:115]\n\t"
"buffer_load_dwordx4 v247, s[12:15], s28 offen offset:128 nt lds\n\t"
"v_mfma_f32_16x16x32_bf16 v[112:115], a[208:211], a[188:191], v[112:115]\n\t"
"s_add_u32 m0, s24, 39808\n\t"
"s_add_u32 s28, s21, 1179648\n\t"
"ds_write2st64_b64 v245, v[224:225], v[226:227] offset0:50 offset1:51\n\t"
"v_mfma_f32_16x16x32_bf16 v[116:119], a[208:211], a[192:195], v[116:119]\n\t"
"buffer_load_dwordx4 a[248:251], off, s[80:83], s20 offset:0 sc1\n\t"
"v_mfma_f32_16x16x32_bf16 v[116:119], v[240:243], a[188:191], v[116:119]\n\t"
"ds_read_b64_tr_b16 v[228:229], v246 offset:33280\n\t"
"v_mfma_f32_16x16x32_bf16 v[116:119], a[252:255], a[184:187], v[116:119]\n\t"
"ds_write_b64 v245, v[232:233] offset:26624\n\t"
"v_mfma_f32_16x16x32_bf16 v[120:123], a[252:255], a[188:191], v[120:123]\n\t"
"ds_read_b64_tr_b16 v[230:231], v246 offset:36352\n\t"
"v_mfma_f32_16x16x32_bf16 v[120:123], v[240:243], a[192:195], v[120:123]\n\t"
"buffer_load_dwordx4 v247, s[12:15], s28 offen offset:128 nt lds\n\t"
"v_mfma_f32_16x16x32_bf16 v[120:123], a[208:211], a[196:199], v[120:123]\n\t"
"s_add_u32 m0, s24, 45952\n\t"
"s_add_u32 s28, s21, 1376256\n\t"
"ds_read_b64_tr_b16 v[224:225], v246 offset:40960\n\t"
"ds_read_b64_tr_b16 v[226:227], v246 offset:44032\n\t"
"v_mfma_f32_16x16x32_bf16 v[124:127], v[240:243], a[196:199], v[124:127]\n\t"
"buffer_load_dwordx4 v247, s[12:15], s28 offen offset:128 nt lds\n\t"
"v_mfma_f32_16x16x32_bf16 v[124:127], a[252:255], a[192:195], v[124:127]\n\t"
"s_add_u32 m0, s24, 52096\n\t"
"s_add_u32 s28, s21, 1572864\n\t"
"ds_read_b64_tr_b16 v[232:233], v246 offset:47104\n\t"
"v_mfma_f32_16x16x32_bf16 v[124:127], a[208:211], a[200:203], v[124:127]\n\t"
"s_waitcnt lgkmcnt(3)\n\t"
"buffer_load_dwordx4 v247, s[12:15], s28 offen offset:128 nt lds\n\t"
"s_add_u32 m0, s24, 92032\n\t"
"s_add_u32 s28, s21, 2752512\n\t"
"v_mfma_f32_16x16x32_bf16 v[228:231], v[228:231], v[220:223], 0\n\t"
"s_waitcnt lgkmcnt(1)\n\t"
"ds_read_b64_tr_b16 v[234:235], v246 offset:50176\n\t"
"v_mfma_f32_16x16x32_bf16 v[224:227], v[224:227], v[220:223], 0\n\t"
"buffer_load_dwordx4 v247, s[12:15], s28 offen offset:128 nt lds\n\t"
"s_add_u32 m0, s24, 99712\n\t"
"s_add_u32 s28, s21, 2949120\n\t"
"v_mfma_f32_16x16x32_bf16 v[128:131], v[240:243], a[200:203], v[128:131]\n\t"
"buffer_load_dwordx4 v247, s[12:15], s28 offen offset:128 nt lds\n\t"
"v_mfma_f32_16x16x32_bf16 v[128:131], a[252:255], a[196:199], v[128:131]\n\t"
"s_waitcnt vmcnt(16)\n\t"
"s_add_u32 m0, s24, 105856\n\t"
"s_add_u32 s28, s21, 3145728\n\t"
"v_mfma_f32_16x16x32_bf16 v[128:131], a[208:211], a[204:207], v[128:131]\n\t"
"s_waitcnt lgkmcnt(0)\n\t"
"buffer_load_dwordx4 v247, s[12:15], s28 offen offset:128 nt lds\n\t"
"s_add_u32 m0, s24, 112000\n\t"
"s_add_u32 s28, s21, 3342336\n\t"
"v_mfma_f32_16x16x32_bf16 v[232:235], v[232:235], v[220:223], 0\n\t"
"buffer_load_dwordx4 v247, s[12:15], s28 offen offset:128 nt lds\n\t"
"v_mfma_f32_16x16x32_bf16 v[132:135], v[236:239], a[168:171], v[132:135]\n\t"
"v_cvt_pk_bf16_f32 v228, v228, v229\n\t"
"v_cvt_pk_bf16_f32 v229, v230, v231\n\t"
"v_mfma_f32_16x16x32_bf16 v[132:135], v[248:251], a[176:179], v[132:135]\n\t"
"v_cvt_pk_bf16_f32 v230, v224, v225\n\t"
"v_cvt_pk_bf16_f32 v231, v226, v227\n\t"
"v_mfma_f32_16x16x32_bf16 v[132:135], v[96:99], a[172:175], v[132:135]\n\t"
"ds_write2st64_b64 v245, v[228:229], v[230:231] offset0:53 offset1:54\n\t"
"v_mfma_f32_16x16x32_bf16 v[136:139], v[236:239], a[172:175], v[136:139]\n\t"
"v_mfma_f32_16x16x32_bf16 v[136:139], v[96:99], a[176:179], v[136:139]\n\t"
"ds_read_b64_tr_b16 v[224:225], v246 offset:53248\n\t"
"v_mfma_f32_16x16x32_bf16 v[136:139], v[248:251], a[180:183], v[136:139]\n\t"
"ds_read_b64_tr_b16 v[226:227], v246 offset:56320\n\t"
"v_cvt_pk_bf16_f32 v232, v232, v233\n\t"
"v_mfma_f32_16x16x32_bf16 v[140:143], v[248:251], a[184:187], v[140:143]\n\t"
"v_cvt_pk_bf16_f32 v233, v234, v235\n\t"
"v_mfma_f32_16x16x32_bf16 v[140:143], v[96:99], a[180:183], v[140:143]\n\t"
"v_mfma_f32_16x16x32_bf16 v[140:143], v[236:239], a[176:179], v[140:143]\n\t"
"v_mfma_f32_16x16x32_bf16 v[144:147], v[236:239], a[180:183], v[144:147]\n\t"
"v_mfma_f32_16x16x32_bf16 v[144:147], v[96:99], a[184:187], v[144:147]\n\t"
"v_mfma_f32_16x16x32_bf16 v[144:147], v[248:251], a[188:191], v[144:147]\n\t"
"v_mfma_f32_16x16x32_bf16 v[148:151], v[96:99], a[188:191], v[148:151]\n\t"
"s_waitcnt lgkmcnt(0)\n\t"
"v_mfma_f32_16x16x32_bf16 v[148:151], v[248:251], a[192:195], v[148:151]\n\t"
"v_mfma_f32_16x16x32_bf16 v[148:151], v[236:239], a[184:187], v[148:151]\n\t"
"v_mfma_f32_16x16x32_bf16 v[224:227], v[224:227], v[220:223], 0\n\t"
"v_mfma_f32_16x16x32_bf16 v[152:155], v[236:239], a[188:191], v[152:155]\n\t"
"v_mfma_f32_16x16x32_bf16 v[152:155], v[248:251], a[196:199], v[152:155]\n\t"
"v_mfma_f32_16x16x32_bf16 v[152:155], v[96:99], a[192:195], v[152:155]\n\t"
"v_mfma_f32_16x16x32_bf16 v[156:159], v[236:239], a[192:195], v[156:159]\n\t"
"v_mfma_f32_16x16x32_bf16 v[156:159], v[96:99], a[196:199], v[156:159]\n\t"
"v_cvt_pk_bf16_f32 v234, v224, v225\n\t"
"v_cvt_pk_bf16_f32 v235, v226, v227\n\t"
"v_mfma_f32_16x16x32_bf16 v[156:159], v[248:251], a[200:203], v[156:159]\n\t"
"ds_write2st64_b64 v245, v[232:233], v[234:235] offset0:55 offset1:56\n\t"
"v_mfma_f32_16x16x32_bf16 v[160:163], v[96:99], a[200:203], v[160:163]\n\t"
"s_waitcnt vmcnt(15)\n\t"
"v_mfma_f32_16x16x32_bf16 v[160:163], v[236:239], a[196:199], v[160:163]\n\t"
"v_mfma_f32_16x16x32_bf16 v[160:163], v[248:251], a[204:207], v[160:163]\n\t"
"v_mfma_f32_16x16x32_bf16 v[164:167], a[212:215], a[168:171], v[164:167]\n\t"
"v_mfma_f32_16x16x32_bf16 v[164:167], a[220:223], a[176:179], v[164:167]\n\t"
"v_mfma_f32_16x16x32_bf16 v[164:167], a[216:219], a[172:175], v[164:167]\n\t"
"s_waitcnt lgkmcnt(0)\n\t"
"s_barrier\n\t"
"v_mfma_f32_16x16x32_bf16 v[168:171], a[212:215], a[172:175], v[168:171]\n\t"
"ds_read_b128 a[168:171], v244 offset:24576\n\t"
"v_mfma_f32_16x16x32_bf16 v[168:171], a[216:219], a[176:179], v[168:171]\n\t"
"ds_read_b128 a[172:175], v244 offset:25088\n\t"
"v_mfma_f32_16x16x32_bf16 v[168:171], a[220:223], a[180:183], v[168:171]\n\t"
"v_mfma_f32_16x16x32_bf16 v[172:175], a[220:223], a[184:187], v[172:175]\n\t"
"v_mfma_f32_16x16x32_bf16 v[172:175], a[216:219], a[180:183], v[172:175]\n\t"
"v_mfma_f32_16x16x32_bf16 v[172:175], a[212:215], a[176:179], v[172:175]\n\t"
"ds_read_b128 a[176:179], v244 offset:25600\n\t"
"v_mfma_f32_16x16x32_bf16 v[176:179], a[212:215], a[180:183], v[176:179]\n\t"
"ds_read_b128 a[180:183], v244 offset:26112\n\t"
"v_mfma_f32_16x16x32_bf16 v[176:179], a[216:219], a[184:187], v[176:179]\n\t"
"v_mfma_f32_16x16x32_bf16 v[176:179], a[220:223], a[188:191], v[176:179]\n\t"
"v_mfma_f32_16x16x32_bf16 v[180:183], a[220:223], a[192:195], v[180:183]\n\t"
"v_mfma_f32_16x16x32_bf16 v[180:183], a[216:219], a[188:191], v[180:183]\n\t"
"v_mfma_f32_16x16x32_bf16 v[180:183], a[212:215], a[184:187], v[180:183]\n\t"
"ds_read_b128 a[184:187], v244 offset:26624\n\t"
"v_mfma_f32_16x16x32_bf16 v[184:187], a[212:215], a[188:191], v[184:187]\n\t"
"ds_read_b128 a[188:191], v244 offset:27136\n\t"
"v_mfma_f32_16x16x32_bf16 v[184:187], a[216:219], a[192:195], v[184:187]\n\t"
"v_mfma_f32_16x16x32_bf16 v[184:187], a[220:223], a[196:199], v[184:187]\n\t"
"v_mfma_f32_16x16x32_bf16 v[188:191], a[220:223], a[200:203], v[188:191]\n\t"
"v_mfma_f32_16x16x32_bf16 v[188:191], a[216:219], a[196:199], v[188:191]\n\t"
"v_mfma_f32_16x16x32_bf16 v[188:191], a[212:215], a[192:195], v[188:191]\n\t"
"ds_read_b128 a[192:195], v244 offset:27648\n\t"
"v_mfma_f32_16x16x32_bf16 v[192:195], a[212:215], a[196:199], v[192:195]\n\t"
"s_waitcnt vmcnt(12) lgkmcnt(4)\n\t"
"ds_read_b128 a[196:199], v244 offset:28160\n\t"
"v_mfma_f32_16x16x32_bf16 v[192:195], a[216:219], a[200:203], v[192:195]\n\t"
"ds_read_b128 a[200:203], v244 offset:28672\n\t"
"v_mfma_f32_16x16x32_bf16 v[192:195], a[220:223], a[204:207], v[192:195]\n\t"
"ds_read_b128 a[204:207], v244 offset:29184\n\t"
"Lst23_%=:\n\t"
"v_mfma_f32_16x16x32_bf16 v[196:199], a[224:227], a[168:171], v[196:199]\n\t"
"s_mov_b64 vcc, -1\n\t"
"s_bitcmp1_b32 s73, 1\n\t"
"s_cbranch_scc1 Lvh3_%=\n\t"
"s_bitcmp1_b32 s73, 0\n\t"
"s_cbranch_scc1 Lpg22_%=\n\t"
"s_branch Lpg21_%=\n\t"
"Lvh3_%=:\n\t"
"s_bitcmp1_b32 s73, 0\n\t"
"s_cbranch_scc1 Lpg24_%=\n\t"
"s_branch Lpg23_%=\n\t"
"Lvr3_%=:\n\t"
"s_mov_b64 vcc, 0\n\t"
"s_waitcnt lgkmcnt(6)\n\t"
"v_mfma_f32_16x16x32_bf16 v[196:199], a[232:235], a[176:179], v[196:199]\n\t"
"buffer_load_dwordx4 v[240:243], off, s[80:83], s20 offset:1024 sc1\n\t"
"v_mfma_f32_16x16x32_bf16 v[196:199], a[228:231], a[172:175], v[196:199]\n\t"
"v_mfma_f32_16x16x32_bf16 v[204:207], a[224:227], a[172:175], v[204:207]\n\t"
"s_waitcnt lgkmcnt(5)\n\t"
"v_mfma_f32_16x16x32_bf16 v[204:207], a[228:231], a[176:179], v[204:207]\n\t"
"v_mfma_f32_16x16x32_bf16 v[204:207], a[232:235], a[180:183], v[204:207]\n\t"
"v_mfma_f32_16x16x32_bf16 v[212:215], a[232:235], a[184:187], v[212:215]\n\t"
"s_waitcnt lgkmcnt(4)\n\t"
"v_mfma_f32_16x16x32_bf16 v[212:215], a[228:231], a[180:183], v[212:215]\n\t"
"v_mfma_f32_16x16x32_bf16 v[212:215], a[224:227], a[176:179], v[212:215]\n\t"
"ds_read_b64_tr_b16 v[228:229], v246 offset:0\n\t"
"v_mfma_f32_16x16x32_bf16 a[0:3], a[224:227], a[180:183], a[0:3]\n\t"
"s_waitcnt lgkmcnt(4)\n\t"
"ds_read_b64_tr_b16 v[230:231], v246 offset:3072\n\t"
"v_mfma_f32_16x16x32_bf16 a[0:3], a[228:231], a[184:187], a[0:3]\n\t"
"ds_read_b64_tr_b16 v[224:225], v246 offset:6144\n\t"
"v_mfma_f32_16x16x32_bf16 a[0:3], a[232:235], a[188:191], a[0:3]\n\t"
"ds_read_b64_tr_b16 v[226:227], v246 offset:9216\n\t"
"v_mfma_f32_16x16x32_bf16 a[8:11], a[232:235], a[192:195], a[8:11]\n\t"
"buffer_load_dwordx4 a[208:211], off, s[80:83], s20 offset:2048 sc1\n\t"
"v_mfma_f32_16x16x32_bf16 a[8:11], a[228:231], a[188:191], a[8:11]\n\t"
"s_waitcnt lgkmcnt(6)\n\t"
"s_mov_b32 s20, 442368\n\t"
"v_mfma_f32_16x16x32_bf16 a[8:11], a[224:227], a[184:187], a[8:11]\n\t"
"v_mfma_f32_16x16x32_bf16 a[16:19], a[224:227], a[188:191], a[16:19]\n\t"
"s_waitcnt lgkmcnt(5)\n\t"
"v_mfma_f32_16x16x32_bf16 a[16:19], a[228:231], a[192:195], a[16:19]\n\t"
"buffer_load_dwordx4 v[236:239], off, s[80:83], s20 offset:0 sc1\n\t"
"v_mfma_f32_16x16x32_bf16 a[16:19], a[232:235], a[196:199], a[16:19]\n\t"
"v_mfma_f32_16x16x32_bf16 a[24:27], a[232:235], a[200:203], a[24:27]\n\t"
"s_waitcnt lgkmcnt(4)\n\t"
"v_mfma_f32_16x16x32_bf16 a[24:27], a[228:231], a[196:199], a[24:27]\n\t"
"v_mfma_f32_16x16x32_bf16 a[24:27], a[224:227], a[192:195], a[24:27]\n\t"
"buffer_load_dwordx4 v[96:99], off, s[80:83], s20 offset:1024 sc1\n\t"
"v_mfma_f32_16x16x32_bf16 a[32:35], a[224:227], a[196:199], a[32:35]\n\t"
"s_waitcnt lgkmcnt(2)\n\t"
"v_mfma_f32_16x16x32_bf16 a[32:35], a[228:231], a[200:203], a[32:35]\n\t"
"s_waitcnt lgkmcnt(0)\n\t"
"v_mfma_f32_16x16x32_bf16 a[32:35], a[232:235], a[204:207], a[32:35]\n\t"
"s_waitcnt vmcnt(13)\n\t"
"v_mfma_f32_16x16x32_bf16 v[228:231], v[228:231], v[220:223], 0\n\t"
"buffer_load_dwordx4 v[248:251], off, s[80:83], s20 offset:2048 sc1\n\t"
"v_mfma_f32_16x16x32_bf16 v[224:227], v[224:227], v[220:223], 0\n\t"
"s_mov_b32 s20, 454656\n\t"
"v_mfma_f32_16x16x32_bf16 a[40:43], a[236:239], a[168:171], a[40:43]\n\t"
"v_mfma_f32_16x16x32_bf16 a[40:43], a[244:247], a[176:179], a[40:43]\n\t"
"v_mfma_f32_16x16x32_bf16 a[40:43], a[240:243], a[172:175], a[40:43]\n\t"
"buffer_load_dwordx4 a[212:215], off, s[80:83], s20 offset:0 sc1\n\t"
"v_mfma_f32_16x16x32_bf16 a[48:51], a[236:239], a[172:175], a[48:51]\n\t"
"v_cvt_pk_bf16_f32 v228, v228, v229\n\t"
"v_cvt_pk_bf16_f32 v229, v230, v231\n\t"
"v_mfma_f32_16x16x32_bf16 a[48:51], a[240:243], a[176:179], a[48:51]\n\t"
"v_cvt_pk_bf16_f32 v230, v224, v225\n\t"
"v_cvt_pk_bf16_f32 v231, v226, v227\n\t"
"v_mfma_f32_16x16x32_bf16 a[48:51], a[244:247], a[180:183], a[48:51]\n\t"
"v_mfma_f32_16x16x32_bf16 a[56:59], a[244:247], a[184:187], a[56:59]\n\t"
"buffer_load_dwordx4 a[216:219], off, s[80:83], s20 offset:1024 sc1\n\t"
"v_mfma_f32_16x16x32_bf16 a[56:59], a[240:243], a[180:183], a[56:59]\n\t"
"v_mfma_f32_16x16x32_bf16 a[56:59], a[236:239], a[176:179], a[56:59]\n\t"
"ds_read_b64_tr_b16 v[232:233], v246 offset:12288\n\t"
"v_mfma_f32_16x16x32_bf16 a[64:67], a[236:239], a[180:183], a[64:67]\n\t"
"ds_read_b64_tr_b16 v[234:235], v246 offset:15360\n\t"
"v_mfma_f32_16x16x32_bf16 a[64:67], a[240:243], a[184:187], a[64:67]\n\t"
"buffer_load_dwordx4 a[220:223], off, s[80:83], s20 offset:2048 sc1\n\t"
"v_mfma_f32_16x16x32_bf16 a[64:67], a[244:247], a[188:191], a[64:67]\n\t"
"s_mov_b32 s20, 466944\n\t"
"ds_read_b64_tr_b16 v[224:225], v246 offset:19968\n\t"
"v_mfma_f32_16x16x32_bf16 a[72:75], a[244:247], a[192:195], a[72:75]\n\t"
"ds_read_b64_tr_b16 v[226:227], v246 offset:23040\n\t"
"v_mfma_f32_16x16x32_bf16 a[72:75], a[240:243], a[188:191], a[72:75]\n\t"
"v_mfma_f32_16x16x32_bf16 a[72:75], a[236:239], a[184:187], a[72:75]\n\t"
"buffer_load_dwordx4 a[224:227], off, s[80:83], s20 offset:0 sc1\n\t"
"v_mfma_f32_16x16x32_bf16 a[80:83], a[236:239], a[188:191], a[80:83]\n\t"
"v_mfma_f32_16x16x32_bf16 a[80:83], a[240:243], a[192:195], a[80:83]\n\t"
"v_mfma_f32_16x16x32_bf16 a[80:83], a[244:247], a[196:199], a[80:83]\n\t"
"v_mfma_f32_16x16x32_bf16 a[88:91], a[244:247], a[200:203], a[88:91]\n\t"
"buffer_load_dwordx4 a[228:231], off, s[80:83], s20 offset:1024 sc1\n\t"
"v_mfma_f32_16x16x32_bf16 a[88:91], a[240:243], a[196:199], a[88:91]\n\t"
"v_mfma_f32_16x16x32_bf16 a[88:91], a[236:239], a[192:195], a[88:91]\n\t"
"v_mfma_f32_16x16x32_bf16 a[96:99], a[236:239], a[196:199], a[96:99]\n\t"
"s_waitcnt lgkmcnt(2)\n\t"
"v_mfma_f32_16x16x32_bf16 a[96:99], a[240:243], a[200:203], a[96:99]\n\t"
"buffer_load_dwordx4 a[232:235], off, s[80:83], s20 offset:2048 sc1\n\t"
"v_mfma_f32_16x16x32_bf16 a[96:99], a[244:247], a[204:207], a[96:99]\n\t"
"s_waitcnt vmcnt(9) lgkmcnt(0)\n\t"
"s_mov_b32 s20, 700416\n\t"
"v_mfma_f32_16x16x32_bf16 v[232:235], v[232:235], v[220:223], 0\n\t"
"v_mfma_f32_16x16x32_bf16 v[224:227], v[224:227], v[220:223], 0\n\t"
"v_mfma_f32_16x16x32_bf16 a[104:107], a[248:251], a[168:171], a[104:107]\n\t"
"buffer_load_dwordx4 a[236:239], off, s[80:83], s20 offset:0 sc1\n\t"
"v_mfma_f32_16x16x32_bf16 a[104:107], a[208:211], a[176:179], a[104:107]\n\t"
"ds_read_b128 a[168:171], v244 offset:33792\n\t"
"v_mfma_f32_16x16x32_bf16 a[104:107], v[240:243], a[172:175], a[104:107]\n\t"
"v_mfma_f32_16x16x32_bf16 a[112:115], a[248:251], a[172:175], a[112:115]\n\t"
"buffer_load_dwordx4 a[240:243], off, s[80:83], s20 offset:1024 sc1\n\t"
"v_mfma_f32_16x16x32_bf16 a[112:115], v[240:243], a[176:179], a[112:115]\n\t"
"ds_read_b128 a[172:175], v244 offset:34304\n\t"
"v_cvt_pk_bf16_f32 v232, v232, v233\n\t"
"v_mfma_f32_16x16x32_bf16 a[112:115], a[208:211], a[180:183], a[112:115]\n\t"
"v_cvt_pk_bf16_f32 v233, v234, v235\n\t"
"v_cvt_pk_bf16_f32 v234, v224, v225\n\t"
"v_mfma_f32_16x16x32_bf16 a[120:123], a[208:211], a[184:187], a[120:123]\n\t"
"v_cvt_pk_bf16_f32 v235, v226, v227\n\t"
"v_mfma_f32_16x16x32_bf16 a[120:123], v[240:243], a[180:183], a[120:123]\n\t"
"buffer_load_dwordx4 a[244:247], off, s[80:83], s20 offset:2048 sc1\n\t"
"v_mfma_f32_16x16x32_bf16 a[120:123], a[248:251], a[176:179], a[120:123]\n\t"
"s_mov_b32 s20, 712704\n\t"
"ds_read_b128 a[176:179], v244 offset:34816\n\t"
"v_mfma_f32_16x16x32_bf16 a[128:131], a[248:251], a[180:183], a[128:131]\n\t"
"buffer_load_dwordx4 a[252:255], off, s[80:83], s20 offset:0 sc1\n\t"
"v_mfma_f32_16x16x32_bf16 a[128:131], v[240:243], a[184:187], a[128:131]\n\t"
"ds_read_b128 a[180:183], v244 offset:35328\n\t"
"v_mfma_f32_16x16x32_bf16 a[128:131], a[208:211], a[188:191], a[128:131]\n\t"
"ds_read_b64_tr_b16 v[224:225], v246 offset:26112\n\t"
"v_mfma_f32_16x16x32_bf16 a[136:139], a[208:211], a[192:195], a[136:139]\n\t"
"ds_read_b64_tr_b16 v[226:227], v246 offset:29184\n\t"
"v_mfma_f32_16x16x32_bf16 a[136:139], v[240:243], a[188:191], a[136:139]\n\t"
"v_mfma_f32_16x16x32_bf16 a[136:139], a[248:251], a[184:187], a[136:139]\n\t"
"ds_read_b128 a[184:187], v244 offset:35840\n\t"
"v_mfma_f32_16x16x32_bf16 a[144:147], a[248:251], a[188:191], a[144:147]\n\t"
"ds_read_b128 a[188:191], v244 offset:36352\n\t"
"v_mfma_f32_16x16x32_bf16 a[144:147], v[240:243], a[192:195], a[144:147]\n\t"
"v_mfma_f32_16x16x32_bf16 a[144:147], a[208:211], a[196:199], a[144:147]\n\t"
"v_mfma_f32_16x16x32_bf16 a[152:155], a[208:211], a[200:203], a[152:155]\n\t"
"v_mfma_f32_16x16x32_bf16 a[152:155], v[240:243], a[196:199], a[152:155]\n\t"
"v_mfma_f32_16x16x32_bf16 a[152:155], a[248:251], a[192:195], a[152:155]\n\t"
"ds_read_b128 a[192:195], v244 offset:36864\n\t"
"v_mfma_f32_16x16x32_bf16 a[160:163], a[248:251], a[196:199], a[160:163]\n\t"
"buffer_load_dwordx4 a[248:251], off, s[80:83], s20 offset:1024 sc1\n\t"
"v_mfma_f32_16x16x32_bf16 a[160:163], v[240:243], a[200:203], a[160:163]\n\t"
"s_waitcnt vmcnt(11) lgkmcnt(3)\n\t"
"ds_read_b128 a[196:199], v244 offset:37376\n\t"
"v_mfma_f32_16x16x32_bf16 a[160:163], a[208:211], a[204:207], a[160:163]\n\t"
"ds_read_b128 a[200:203], v244 offset:37888\n\t"
"v_mfma_f32_16x16x32_bf16 v[224:227], v[224:227], v[220:223], 0\n\t"
"ds_read_b128 a[204:207], v244 offset:38400\n\t"
"v_mfma_f32_16x16x32_bf16 v[200:203], v[236:239], a[168:171], v[200:203]\n\t"
"v_mfma_f32_16x16x32_bf16 v[200:203], v[248:251], a[176:179], v[200:203]\n\t"
"v_mfma_f32_16x16x32_bf16 v[200:203], v[96:99], a[172:175], v[200:203]\n\t"
"v_mfma_f32_16x16x32_bf16 v[208:211], v[236:239], a[172:175], v[208:211]\n\t"
"s_waitcnt lgkmcnt(5)\n\t"
"v_mfma_f32_16x16x32_bf16 v[208:211], v[96:99], a[176:179], v[208:211]\n\t"
"v_cvt_pk_bf16_f32 v224, v224, v225\n\t"
"v_mfma_f32_16x16x32_bf16 v[208:211], v[248:251], a[180:183], v[208:211]\n\t"
"s_waitcnt lgkmcnt(0)\n\t"
"s_barrier\n\t"
"v_mfma_f32_16x16x32_bf16 v[216:219], v[248:251], a[184:187], v[216:219]\n\t"
"v_cvt_pk_bf16_f32 v225, v226, v227\n\t"
"v_mfma_f32_16x16x32_bf16 v[216:219], v[96:99], a[180:183], v[216:219]\n\t"
"v_mfma_f32_16x16x32_bf16 v[216:219], v[236:239], a[176:179], v[216:219]\n\t"
"ds_write2st64_b64 v245, v[228:229], v[230:231] offset0:48 offset1:49\n\t"
"v_mfma_f32_16x16x32_bf16 a[4:7], v[236:239], a[180:183], a[4:7]\n\t"
"v_mfma_f32_16x16x32_bf16 a[4:7], v[96:99], a[184:187], a[4:7]\n\t"
"ds_read_b64_tr_b16 v[228:229], v246 offset:32256\n\t"
"v_mfma_f32_16x16x32_bf16 a[4:7], v[248:251], a[188:191], a[4:7]\n\t"
"ds_write2st64_b64 v245, v[232:233], v[234:235] offset0:50 offset1:51\n\t"
"v_mfma_f32_16x16x32_bf16 a[12:15], v[248:251], a[192:195], a[12:15]\n\t"
"v_mfma_f32_16x16x32_bf16 a[12:15], v[96:99], a[188:191], a[12:15]\n\t"
"ds_read_b64_tr_b16 v[230:231], v246 offset:35328\n\t"
"v_mfma_f32_16x16x32_bf16 a[12:15], v[236:239], a[184:187], a[12:15]\n\t"
"ds_write_b64 v245, v[224:225] offset:26624\n\t"
"v_mfma_f32_16x16x32_bf16 a[20:23], v[236:239], a[188:191], a[20:23]\n\t"
"ds_read_b64_tr_b16 v[232:233], v246 offset:39936\n\t"
"v_mfma_f32_16x16x32_bf16 a[20:23], v[96:99], a[192:195], a[20:23]\n\t"
"ds_read_b64_tr_b16 v[234:235], v246 offset:43008\n\t"
"v_mfma_f32_16x16x32_bf16 a[20:23], v[248:251], a[196:199], a[20:23]\n\t"
"ds_read_b64_tr_b16 v[224:225], v246 offset:46080\n\t"
"v_mfma_f32_16x16x32_bf16 a[28:31], v[96:99], a[196:199], a[28:31]\n\t"
"s_waitcnt lgkmcnt(4)\n\t"
"ds_read_b64_tr_b16 v[226:227], v246 offset:49152\n\t"
"v_mfma_f32_16x16x32_bf16 a[28:31], v[236:239], a[192:195], a[28:31]\n\t"
"global_load_dwordx4 v[240:243], v252, s[48:49] offset:2048\n\t"
"v_mfma_f32_16x16x32_bf16 a[28:31], v[248:251], a[200:203], a[28:31]\n\t"
"v_mfma_f32_16x16x32_bf16 v[228:231], v[228:231], v[220:223], 0\n\t"
"s_waitcnt lgkmcnt(2)\n\t"
"v_mfma_f32_16x16x32_bf16 v[232:235], v[232:235], v[220:223], 0\n\t"
"v_mfma_f32_16x16x32_bf16 a[36:39], v[96:99], a[200:203], a[36:39]\n\t"
"buffer_load_dwordx4 v[96:99], off, s[80:83], s20 offset:2048 sc1\n\t"
"v_mfma_f32_16x16x32_bf16 a[36:39], v[236:239], a[196:199], a[36:39]\n\t"
"global_load_dwordx4 a[208:211], v252, s[46:47] offset:1024\n\t"
"v_mfma_f32_16x16x32_bf16 a[36:39], v[248:251], a[204:207], a[36:39]\n\t"
"s_waitcnt vmcnt(11) lgkmcnt(0)\n\t"
"s_mov_b32 s20, 724992\n\t"
"v_mfma_f32_16x16x32_bf16 v[224:227], v[224:227], v[220:223], 0\n\t"
"buffer_load_dwordx4 v[248:251], off, s[80:83], s20 offset:0 sc1\n\t"
"v_mfma_f32_16x16x32_bf16 a[44:47], a[212:215], a[168:171], a[44:47]\n\t"
"v_cvt_pk_bf16_f32 v228, v228, v229\n\t"
"v_cvt_pk_bf16_f32 v229, v230, v231\n\t"
"v_mfma_f32_16x16x32_bf16 a[44:47], a[220:223], a[176:179], a[44:47]\n\t"
"v_cvt_pk_bf16_f32 v230, v232, v233\n\t"
"v_cvt_pk_bf16_f32 v231, v234, v235\n\t"
"v_mfma_f32_16x16x32_bf16 a[44:47], a[216:219], a[172:175], a[44:47]\n\t"
"ds_write2st64_b64 v245, v[228:229], v[230:231] offset0:53 offset1:54\n\t"
"v_mfma_f32_16x16x32_bf16 a[52:55], a[212:215], a[172:175], a[52:55]\n\t"
"v_mfma_f32_16x16x32_bf16 a[52:55], a[216:219], a[176:179], a[52:55]\n\t"
"ds_read_b64_tr_b16 v[232:233], v246 offset:52224\n\t"
"v_mfma_f32_16x16x32_bf16 a[52:55], a[220:223], a[180:183], a[52:55]\n\t"
"ds_read_b64_tr_b16 v[234:235], v246 offset:55296\n\t"
"v_cvt_pk_bf16_f32 v224, v224, v225\n\t"
"v_mfma_f32_16x16x32_bf16 a[60:63], a[220:223], a[184:187], a[60:63]\n\t"
"v_cvt_pk_bf16_f32 v225, v226, v227\n\t"
"v_mfma_f32_16x16x32_bf16 a[60:63], a[216:219], a[180:183], a[60:63]\n\t"
"v_mfma_f32_16x16x32_bf16 a[60:63], a[212:215], a[176:179], a[60:63]\n\t"
"v_mfma_f32_16x16x32_bf16 a[68:71], a[212:215], a[180:183], a[68:71]\n\t"
"v_mfma_f32_16x16x32_bf16 a[68:71], a[216:219], a[184:187], a[68:71]\n\t"
"v_mfma_f32_16x16x32_bf16 a[68:71], a[220:223], a[188:191], a[68:71]\n\t"
"v_mfma_f32_16x16x32_bf16 a[76:79], a[216:219], a[188:191], a[76:79]\n\t"
"s_waitcnt lgkmcnt(0)\n\t"
"v_mfma_f32_16x16x32_bf16 a[76:79], a[220:223], a[192:195], a[76:79]\n\t"
"v_mfma_f32_16x16x32_bf16 a[76:79], a[212:215], a[184:187], a[76:79]\n\t"
"v_mfma_f32_16x16x32_bf16 v[232:235], v[232:235], v[220:223], 0\n\t"
"v_mfma_f32_16x16x32_bf16 a[84:87], a[212:215], a[188:191], a[84:87]\n\t"
"v_mfma_f32_16x16x32_bf16 a[84:87], a[220:223], a[196:199], a[84:87]\n\t"
"v_mfma_f32_16x16x32_bf16 a[84:87], a[216:219], a[192:195], a[84:87]\n\t"
"v_mfma_f32_16x16x32_bf16 a[92:95], a[212:215], a[192:195], a[92:95]\n\t"
"v_mfma_f32_16x16x32_bf16 a[92:95], a[216:219], a[196:199], a[92:95]\n\t"
"v_cvt_pk_bf16_f32 v226, v232, v233\n\t"
"v_cvt_pk_bf16_f32 v227, v234, v235\n\t"
"v_mfma_f32_16x16x32_bf16 a[92:95], a[220:223], a[200:203], a[92:95]\n\t"
"ds_write2st64_b64 v245, v[224:225], v[226:227] offset0:55 offset1:56\n\t"
"v_mfma_f32_16x16x32_bf16 a[100:103], a[216:219], a[200:203], a[100:103]\n\t"
"buffer_load_dwordx4 a[216:219], off, s[80:83], s20 offset:1024 sc1\n\t"
"v_mfma_f32_16x16x32_bf16 a[100:103], a[212:215], a[196:199], a[100:103]\n\t"
"s_waitcnt vmcnt(10)\n\t"
"v_mfma_f32_16x16x32_bf16 a[100:103], a[220:223], a[204:207], a[100:103]\n\t"
"v_mfma_f32_16x16x32_bf16 a[108:111], a[224:227], a[168:171], a[108:111]\n\t"
"buffer_load_dwordx4 a[212:215], off, s[80:83], s20 offset:2048 sc1\n\t"
"v_mfma_f32_16x16x32_bf16 a[108:111], a[232:235], a[176:179], a[108:111]\n\t"
"s_mov_b32 s20, 737280\n\t"
"v_mfma_f32_16x16x32_bf16 a[108:111], a[228:231], a[172:175], a[108:111]\n\t"
"s_waitcnt lgkmcnt(0)\n\t"
"s_barrier\n\t"
"v_mfma_f32_16x16x32_bf16 a[116:119], a[224:227], a[172:175], a[116:119]\n\t"
"s_waitcnt vmcnt(5)\n\t"
"ds_read_b64_tr_b16 v[236:237], v240 offset:0\n\t"
"v_mfma_f32_16x16x32_bf16 a[116:119], a[228:231], a[176:179], a[116:119]\n\t"
"buffer_load_dwordx4 a[220:223], off, s[80:83], s20 offset:0 sc1\n\t"
"v_mfma_f32_16x16x32_bf16 a[116:119], a[232:235], a[180:183], a[116:119]\n\t"
"ds_read_b64_tr_b16 v[238:239], v240 offset:1536\n\t"
"v_mfma_f32_16x16x32_bf16 a[124:127], a[232:235], a[184:187], a[124:127]\n\t"
"ds_read_b64_tr_b16 v[228:229], v241 offset:0\n\t"
"v_mfma_f32_16x16x32_bf16 a[124:127], a[228:231], a[180:183], a[124:127]\n\t"
"ds_read_b64_tr_b16 v[230:231], v241 offset:1536\n\t"
"v_mfma_f32_16x16x32_bf16 a[124:127], a[224:227], a[176:179], a[124:127]\n\t"
"ds_read_b64_tr_b16 v[232:233], v240 offset:19968\n\t"
"v_mfma_f32_16x16x32_bf16 a[132:135], a[224:227], a[180:183], a[132:135]\n\t"
"ds_read_b64_tr_b16 v[234:235], v240 offset:21504\n\t"
"v_mfma_f32_16x16x32_bf16 a[132:135], a[228:231], a[184:187], a[132:135]\n\t"
"ds_read_b64_tr_b16 v[224:225], v241 offset:19968\n\t"
"v_mfma_f32_16x16x32_bf16 a[132:135], a[232:235], a[188:191], a[132:135]\n\t"
"ds_read_b64_tr_b16 v[226:227], v241 offset:21504\n\t"
"v_mfma_f32_16x16x32_bf16 a[140:143], a[224:227], a[184:187], a[140:143]\n\t"
"s_waitcnt vmcnt(4) lgkmcnt(6)\n\t"
"ds_read_b128 a[168:171], v244 offset:24576\n\t"
"v_mfma_f32_16x16x32_bf16 a[140:143], a[232:235], a[192:195], a[140:143]\n\t"
"s_waitcnt lgkmcnt(5)\n\t"
"ds_read_b128 a[172:175], v244 offset:25088\n\t"
"v_mfma_f32_16x16x32_bf16 a[140:143], a[228:231], a[188:191], a[140:143]\n\t"
"s_waitcnt lgkmcnt(4)\n\t"
"ds_read_b128 a[176:179], v244 offset:25600\n\t"
"v_mfma_f32_16x16x32_bf16 v[236:239], v[236:239], a[208:211], 0\n\t"
"ds_read_b128 a[180:183], v244 offset:26112\n\t"
"v_mfma_f32_16x16x32_bf16 v[228:231], v[228:231], a[208:211], 0\n\t"
"s_waitcnt lgkmcnt(4)\n\t"
"ds_read_b128 a[184:187], v244 offset:26624\n\t"
"v_mfma_f32_16x16x32_bf16 v[232:235], v[232:235], a[208:211], 0\n\t"
"v_mfma_f32_16x16x32_bf16 v[224:227], v[224:227], a[208:211], 0\n\t"
"v_mfma_f32_16x16x32_bf16 a[148:151], a[224:227], a[188:191], a[148:151]\n\t"
"ds_read_b128 a[188:191], v244 offset:27136\n\t"
"v_mfma_f32_16x16x32_bf16 a[148:151], a[232:235], a[196:199], a[148:151]\n\t"
"v_cvt_pk_bf16_f32 v236, v236, v237\n\t"
"v_cvt_pk_bf16_f32 v237, v238, v239\n\t"
"v_mfma_f32_16x16x32_bf16 a[148:151], a[228:231], a[192:195], a[148:151]\n\t"
"v_cvt_pk_bf16_f32 v238, v228, v229\n\t"
"v_cvt_pk_bf16_f32 v239, v230, v231\n\t"
"v_mfma_f32_16x16x32_bf16 a[156:159], a[224:227], a[192:195], a[156:159]\n\t"
"ds_read_b128 a[192:195], v244 offset:27648\n\t"
"v_cvt_pk_bf16_f32 v232, v232, v233\n\t"
"v_mfma_f32_16x16x32_bf16 a[156:159], a[228:231], a[196:199], a[156:159]\n\t"
"v_cvt_pk_bf16_f32 v233, v234, v235\n\t"
"v_cvt_pk_bf16_f32 v234, v224, v225\n\t"
"v_mfma_f32_16x16x32_bf16 a[156:159], a[232:235], a[200:203], a[156:159]\n\t"
"ds_write_b128 v242, v[236:239] offset:0\n\t"
"v_mfma_f32_16x16x32_bf16 a[164:167], a[228:231], a[200:203], a[164:167]\n\t"
"buffer_load_dwordx4 a[228:231], off, s[80:83], s20 offset:1024 sc1\n\t"
"v_mfma_f32_16x16x32_bf16 a[164:167], a[224:227], a[196:199], a[164:167]\n\t"
"s_waitcnt lgkmcnt(5)\n\t"
"ds_read_b64_tr_b16 v[228:229], v240 offset:39936\n\t"
"v_mfma_f32_16x16x32_bf16 a[164:167], a[232:235], a[204:207], a[164:167]\n\t"
"ds_read_b64_tr_b16 v[230:231], v240 offset:41472\n\t"
"v_cvt_pk_bf16_f32 v235, v226, v227\n\t"
"Lst24_%=:\n\t"
"v_mfma_f32_16x16x32_bf16 v[196:199], a[236:239], a[168:171], v[196:199]\n\t"
"s_waitcnt lgkmcnt(6)\n\t"
"ds_read_b64_tr_b16 v[224:225], v241 offset:39936\n\t"
"v_mfma_f32_16x16x32_bf16 v[196:199], a[244:247], a[176:179], v[196:199]\n\t"
"buffer_load_dwordx4 a[224:227], off, s[80:83], s20 offset:2048 sc1\n\t"
"v_mfma_f32_16x16x32_bf16 v[196:199], a[240:243], a[172:175], v[196:199]\n\t"
"s_mov_b32 s20, 749568\n\t"
"ds_write_b128 v242, v[232:235] offset:1536\n\t"
"v_mfma_f32_16x16x32_bf16 v[204:207], a[236:239], a[172:175], v[204:207]\n\t"
"buffer_load_dwordx4 a[232:235], off, s[80:83], s20 offset:0 sc1\n\t"
"v_mfma_f32_16x16x32_bf16 v[204:207], a[240:243], a[176:179], v[204:207]\n\t"
"s_waitcnt lgkmcnt(7)\n\t"
"ds_read_b64_tr_b16 v[226:227], v241 offset:41472\n\t"
"v_mfma_f32_16x16x32_bf16 v[204:207], a[244:247], a[180:183], v[204:207]\n\t"
"ds_read_b128 a[196:199], v244 offset:28160\n\t"
"v_add_u32 v240, 59904, v240\n\t"
"v_add_u32 v241, 59904, v241\n\t"
"v_mfma_f32_16x16x32_bf16 v[212:215], a[240:243], a[180:183], v[212:215]\n\t"
"s_waitcnt lgkmcnt(8)\n\t"
"ds_read_b64_tr_b16 v[236:237], v240 offset:0\n\t"
"v_mfma_f32_16x16x32_bf16 v[212:215], a[236:239], a[176:179], v[212:215]\n\t"
"ds_read_b64_tr_b16 v[238:239], v240 offset:1536\n\t"
"v_mfma_f32_16x16x32_bf16 v[212:215], a[244:247], a[184:187], v[212:215]\n\t"
"ds_read_b64_tr_b16 v[232:233], v241 offset:0\n\t"
"v_mfma_f32_16x16x32_bf16 a[0:3], a[236:239], a[180:183], a[0:3]\n\t"
"s_waitcnt lgkmcnt(7)\n\t"
"ds_read_b64_tr_b16 v[234:235], v241 offset:1536\n\t"
"v_mfma_f32_16x16x32_bf16 a[0:3], a[244:247], a[188:191], a[0:3]\n\t"
"s_waitcnt lgkmcnt(5)\n\t"
"ds_read_b128 a[200:203], v244 offset:28672\n\t"
"v_mfma_f32_16x16x32_bf16 a[0:3], a[240:243], a[184:187], a[0:3]\n\t"
"ds_read_b128 a[204:207], v244 offset:29184\n\t"
"v_mfma_f32_16x16x32_bf16 v[228:231], v[228:231], a[208:211], 0\n\t"
"s_waitcnt lgkmcnt(4)\n\t"
"v_mfma_f32_16x16x32_bf16 v[224:227], v[224:227], a[208:211], 0\n\t"
"v_mfma_f32_16x16x32_bf16 v[236:239], v[236:239], a[208:211], 0\n\t"
"s_waitcnt lgkmcnt(2)\n\t"
"v_mfma_f32_16x16x32_bf16 v[232:235], v[232:235], a[208:211], 0\n\t"
"v_mfma_f32_16x16x32_bf16 a[8:11], a[236:239], a[184:187], a[8:11]\n\t"
"v_mfma_f32_16x16x32_bf16 a[8:11], a[244:247], a[192:195], a[8:11]\n\t"
"v_cvt_pk_bf16_f32 v228, v228, v229\n\t"
"v_cvt_pk_bf16_f32 v229, v230, v231\n\t"
"v_mfma_f32_16x16x32_bf16 a[8:11], a[240:243], a[188:191], a[8:11]\n\t"
"v_cvt_pk_bf16_f32 v230, v224, v225\n\t"
"v_cvt_pk_bf16_f32 v231, v226, v227\n\t"
"v_mfma_f32_16x16x32_bf16 a[16:19], a[236:239], a[188:191], a[16:19]\n\t"
"s_waitcnt lgkmcnt(1)\n\t"
"v_cvt_pk_bf16_f32 v236, v236, v237\n\t"
"v_cvt_pk_bf16_f32 v237, v238, v239\n\t"
"v_mfma_f32_16x16x32_bf16 a[16:19], a[240:243], a[192:195], a[16:19]\n\t"
"v_cvt_pk_bf16_f32 v238, v232, v233\n\t"
"v_cvt_pk_bf16_f32 v239, v234, v235\n\t"
"v_mfma_f32_16x16x32_bf16 a[16:19], a[244:247], a[196:199], a[16:19]\n\t"
"ds_write_b128 v242, v[228:231] offset:3072\n\t"
"v_mfma_f32_16x16x32_bf16 a[24:27], a[244:247], a[200:203], a[24:27]\n\t"
"s_waitcnt lgkmcnt(1)\n\t"
"v_mfma_f32_16x16x32_bf16 a[24:27], a[240:243], a[196:199], a[24:27]\n\t"
"ds_read_b64_tr_b16 v[224:225], v240 offset:19968\n\t"
"v_mfma_f32_16x16x32_bf16 a[24:27], a[236:239], a[192:195], a[24:27]\n\t"
"ds_write_b128 v242, v[236:239] offset:4608\n\t"
"v_mfma_f32_16x16x32_bf16 a[32:35], a[236:239], a[196:199], a[32:35]\n\t"
"buffer_load_dwordx4 a[236:239], off, s[80:83], s20 offset:1024 sc1\n\t"
"v_mfma_f32_16x16x32_bf16 a[32:35], a[240:243], a[200:203], a[32:35]\n\t"
"ds_read_b64_tr_b16 v[226:227], v240 offset:21504\n\t"
"v_mfma_f32_16x16x32_bf16 a[32:35], a[244:247], a[204:207], a[32:35]\n\t"
"ds_read_b64_tr_b16 v[232:233], v241 offset:19968\n\t"
"v_mfma_f32_16x16x32_bf16 a[40:43], a[252:255], a[168:171], a[40:43]\n\t"
"ds_read_b64_tr_b16 v[234:235], v241 offset:21504\n\t"
"v_mfma_f32_16x16x32_bf16 a[40:43], v[96:99], a[176:179], a[40:43]\n\t"
"ds_read_b64_tr_b16 v[228:229], v240 offset:39936\n\t"
"v_mfma_f32_16x16x32_bf16 a[40:43], a[248:251], a[172:175], a[40:43]\n\t"
"ds_read_b64_tr_b16 v[230:231], v240 offset:41472\n\t"
"v_mfma_f32_16x16x32_bf16 a[48:51], a[252:255], a[172:175], a[48:51]\n\t"
"buffer_load_dwordx4 a[240:243], off, s[80:83], s20 offset:2048 sc1\n\t"
"v_mfma_f32_16x16x32_bf16 a[48:51], a[248:251], a[176:179], a[48:51]\n\t"
"s_mov_b32 s20, 761856\n\t"
"ds_read_b64_tr_b16 v[236:237], v241 offset:39936\n\t"
"v_mfma_f32_16x16x32_bf16 a[48:51], v[96:99], a[180:183], a[48:51]\n\t"
"s_waitcnt lgkmcnt(3)\n\t"
"ds_read_b64_tr_b16 v[238:239], v241 offset:41472\n\t"
"v_mfma_f32_16x16x32_bf16 a[56:59], a[252:255], a[176:179], a[56:59]\n\t"
"buffer_load_dwordx4 a[244:247], off, s[80:83], s20 offset:0 sc1\n\t"
"v_mfma_f32_16x16x32_bf16 a[56:59], v[96:99], a[184:187], a[56:59]\n\t"
"Lpg18_%=:\n\t"
"s_cbranch_vccnz Lvr2_%=\n\t"
"v_mfma_f32_16x16x32_bf16 a[56:59], a[248:251], a[180:183], a[56:59]\n\t"
"s_waitcnt lgkmcnt(2)\n\t"
"v_mfma_f32_16x16x32_bf16 v[224:227], v[224:227], a[208:211], 0\n\t"
"v_mfma_f32_16x16x32_bf16 v[232:235], v[232:235], a[208:211], 0\n\t"
"s_waitcnt lgkmcnt(0)\n\t"
"v_xor_b32 v246, 0xc0, v246\n\t"
"v_mfma_f32_16x16x32_bf16 v[228:231], v[228:231], a[208:211], 0\n\t"
"v_mfma_f32_16x16x32_bf16 v[236:239], v[236:239], a[208:211], 0\n\t"
"v_mfma_f32_16x16x32_bf16 a[64:67], a[252:255], a[180:183], a[64:67]\n\t"
"buffer_load_dwordx4 a[208:211], off, s[80:83], s20 offset:1024 sc1\n\t"
"v_mfma_f32_16x16x32_bf16 a[64:67], v[96:99], a[188:191], a[64:67]\n\t"
"v_cvt_pk_bf16_f32 v224, v224, v225\n\t"
"v_cvt_pk_bf16_f32 v225, v226, v227\n\t"
"v_mfma_f32_16x16x32_bf16 a[64:67], a[248:251], a[184:187], a[64:67]\n\t"
"v_cvt_pk_bf16_f32 v226, v232, v233\n\t"
"v_cvt_pk_bf16_f32 v227, v234, v235\n\t"
"v_mfma_f32_16x16x32_bf16 a[72:75], a[252:255], a[184:187], a[72:75]\n\t"
"v_cvt_pk_bf16_f32 v228, v228, v229\n\t"
"v_cvt_pk_bf16_f32 v229, v230, v231\n\t"
"v_mfma_f32_16x16x32_bf16 a[72:75], a[248:251], a[188:191], a[72:75]\n\t"
"v_cvt_pk_bf16_f32 v230, v236, v237\n\t"
"v_cvt_pk_bf16_f32 v231, v238, v239\n\t"
"v_mfma_f32_16x16x32_bf16 a[72:75], v[96:99], a[192:195], a[72:75]\n\t"
"ds_write_b128 v242, v[224:227] offset:6144\n\t"
"v_mfma_f32_16x16x32_bf16 a[80:83], v[96:99], a[196:199], a[80:83]\n\t"
"v_mfma_f32_16x16x32_bf16 a[80:83], a[248:251], a[192:195], a[80:83]\n\t"
"v_mfma_f32_16x16x32_bf16 a[80:83], a[252:255], a[188:191], a[80:83]\n\t"
"ds_write_b128 v242, v[228:231] offset:7680\n\t"
"v_mfma_f32_16x16x32_bf16 a[88:91], a[252:255], a[192:195], a[88:91]\n\t"
"v_mfma_f32_16x16x32_bf16 a[88:91], a[248:251], a[196:199], a[88:91]\n\t"
"v_mfma_f32_16x16x32_bf16 a[88:91], v[96:99], a[200:203], a[88:91]\n\t"
"ds_read_b64_tr_b16 v[232:233], v246 offset:0\n\t"
"v_mfma_f32_16x16x32_bf16 a[96:99], a[248:251], a[200:203], a[96:99]\n\t"
"buffer_load_dwordx4 v[240:243], off, s[80:83], s20 offset:2048 sc1\n\t"
"v_mfma_f32_16x16x32_bf16 a[96:99], a[252:255], a[196:199], a[96:99]\n\t"
"s_waitcnt vmcnt(9)\n\t"
"ds_read_b64_tr_b16 v[234:235], v246 offset:3072\n\t"
"s_mov_b32 s20, 626688\n\t"
"v_mfma_f32_16x16x32_bf16 a[96:99], v[96:99], a[204:207], a[96:99]\n\t"
"ds_read_b64_tr_b16 v[236:237], v246 offset:6144\n\t"
"v_mfma_f32_16x16x32_bf16 a[104:107], v[248:251], a[168:171], a[104:107]\n\t"
"buffer_load_dwordx4 v[96:99], off, s[80:83], s20 offset:0 sc1\n\t"
"v_mfma_f32_16x16x32_bf16 a[104:107], a[212:215], a[176:179], a[104:107]\n\t"
"ds_read_b64_tr_b16 v[238:239], v246 offset:9216\n\t"
"v_mfma_f32_16x16x32_bf16 a[104:107], a[216:219], a[172:175], a[104:107]\n\t"
"ds_read_b64_tr_b16 v[224:225], v246 offset:12288\n\t"
"v_mfma_f32_16x16x32_bf16 a[112:115], v[248:251], a[172:175], a[112:115]\n\t"
"ds_read_b64_tr_b16 v[226:227], v246 offset:15360\n\t"
"v_mfma_f32_16x16x32_bf16 a[112:115], a[216:219], a[176:179], a[112:115]\n\t"
"buffer_load_dwordx4 a[248:251], off, s[80:83], s20 offset:1024 sc1\n\t"
"v_mfma_f32_16x16x32_bf16 a[112:115], a[212:215], a[180:183], a[112:115]\n\t"
"ds_read_b64_tr_b16 v[228:229], v246 offset:19968\n\t"
"v_mfma_f32_16x16x32_bf16 a[120:123], a[216:219], a[180:183], a[120:123]\n\t"
"ds_read_b64_tr_b16 v[230:231], v246 offset:23040\n\t"
"v_mfma_f32_16x16x32_bf16 a[120:123], v[248:251], a[176:179], a[120:123]\n\t"
"buffer_load_dwordx4 a[252:255], off, s[80:83], s20 offset:2048 sc1\n\t"
"v_mfma_f32_16x16x32_bf16 a[120:123], a[212:215], a[184:187], a[120:123]\n\t"
"ds_read_b128 a[168:171], v244 offset:33792\n\t"
"v_mfma_f32_16x16x32_bf16 a[128:131], v[248:251], a[180:183], a[128:131]\n\t"
"s_waitcnt lgkmcnt(7)\n\t"
"ds_read_b128 a[172:175], v244 offset:34304\n\t"
"v_mfma_f32_16x16x32_bf16 a[128:131], a[212:215], a[188:191], a[128:131]\n\t"
"s_waitcnt lgkmcnt(6)\n\t"
"ds_read_b128 a[176:179], v244 offset:34816\n\t"
"v_mfma_f32_16x16x32_bf16 a[128:131], a[216:219], a[184:187], a[128:131]\n\t"
"s_waitcnt lgkmcnt(5)\n\t"
"ds_read_b128 a[180:183], v244 offset:35328\n\t"
"v_mfma_f32_16x16x32_bf16 v[232:235], v[232:235], v[220:223], 0\n\t"
"s_waitcnt lgkmcnt(4)\n\t"
"v_mfma_f32_16x16x32_bf16 v[236:239], v[236:239], v[220:223], 0\n\t"
"v_mfma_f32_16x16x32_bf16 v[224:227], v[224:227], v[220:223], 0\n\t"
"v_mfma_f32_16x16x32_bf16 v[228:231], v[228:231], v[220:223], 0\n\t"
"v_mfma_f32_16x16x32_bf16 a[136:139], v[248:251], a[184:187], a[136:139]\n\t"
"s_mov_b32 s20, 638976\n\t"
"ds_read_b128 a[184:187], v244 offset:35840\n\t"
"v_mfma_f32_16x16x32_bf16 a[136:139], a[212:215], a[192:195], a[136:139]\n\t"
"v_cvt_pk_bf16_f32 v232, v232, v233\n\t"
"v_cvt_pk_bf16_f32 v233, v234, v235\n\t"
"v_mfma_f32_16x16x32_bf16 a[136:139], a[216:219], a[188:191], a[136:139]\n\t"
"v_cvt_pk_bf16_f32 v234, v236, v237\n\t"
"v_cvt_pk_bf16_f32 v235, v238, v239\n\t"
"v_mfma_f32_16x16x32_bf16 a[144:147], v[248:251], a[188:191], a[144:147]\n\t"
"ds_read_b128 a[188:191], v244 offset:36352\n\t"
"v_cvt_pk_bf16_f32 v224, v224, v225\n\t"
"v_mfma_f32_16x16x32_bf16 a[144:147], a[216:219], a[192:195], a[144:147]\n\t"
"v_cvt_pk_bf16_f32 v225, v226, v227\n\t"
"v_cvt_pk_bf16_f32 v226, v228, v229\n\t"
"v_mfma_f32_16x16x32_bf16 a[144:147], a[212:215], a[196:199], a[144:147]\n\t"
"v_cvt_pk_bf16_f32 v227, v230, v231\n\t"
"v_mfma_f32_16x16x32_bf16 a[152:155], a[212:215], a[200:203], a[152:155]\n\t"
"ds_read_b64_tr_b16 v[228:229], v246 offset:26112\n\t"
"v_mfma_f32_16x16x32_bf16 a[152:155], a[216:219], a[196:199], a[152:155]\n\t"
"ds_read_b64_tr_b16 v[230:231], v246 offset:29184\n\t"
"v_mfma_f32_16x16x32_bf16 a[152:155], v[248:251], a[192:195], a[152:155]\n\t"
"buffer_load_dwordx4 v[236:239], off, s[80:83], s20 offset:0 sc1\n\t"
"v_mfma_f32_16x16x32_bf16 a[160:163], v[248:251], a[196:199], a[160:163]\n\t"
"ds_read_b128 a[192:195], v244 offset:36864\n\t"
"v_mfma_f32_16x16x32_bf16 a[160:163], a[216:219], a[200:203], a[160:163]\n\t"
"buffer_load_dwordx4 v[248:251], off, s[80:83], s20 offset:1024 sc1\n\t"
"v_mfma_f32_16x16x32_bf16 a[160:163], a[212:215], a[204:207], a[160:163]\n\t"
"s_waitcnt vmcnt(11) lgkmcnt(1)\n\t"
"ds_read_b128 a[196:199], v244 offset:37376\n\t"
"v_mfma_f32_16x16x32_bf16 v[228:231], v[228:231], v[220:223], 0\n\t"
"ds_read_b128 a[200:203], v244 offset:37888\n\t"
"v_mfma_f32_16x16x32_bf16 v[200:203], a[220:223], a[168:171], v[200:203]\n\t"
"ds_read_b128 a[204:207], v244 offset:38400\n\t"
"v_mfma_f32_16x16x32_bf16 v[200:203], a[224:227], a[176:179], v[200:203]\n\t"
"buffer_load_dwordx4 a[212:215], off, s[80:83], s20 offset:2048 sc1\n\t"
"v_mfma_f32_16x16x32_bf16 v[200:203], a[228:231], a[172:175], v[200:203]\n\t"
"s_mov_b32 s20, 651264\n\t"
"s_add_u32 m0, s24, 768\n\t"
"s_add_u32 s28, s21, 0\n\t"
"v_mfma_f32_16x16x32_bf16 v[208:211], a[220:223], a[172:175], v[208:211]\n\t"
"buffer_load_dwordx4 a[216:219], off, s[80:83], s20 offset:0 sc1\n\t"
"v_mfma_f32_16x16x32_bf16 v[208:211], a[228:231], a[176:179], v[208:211]\n\t"
"v_cvt_pk_bf16_f32 v228, v228, v229\n\t"
"v_mfma_f32_16x16x32_bf16 v[208:211], a[224:227], a[180:183], v[208:211]\n\t"
"s_waitcnt lgkmcnt(0)\n\t"
"s_barrier\n\t"
"v_mfma_f32_16x16x32_bf16 v[216:219], a[224:227], a[184:187], v[216:219]\n\t"
"v_cvt_pk_bf16_f32 v229, v230, v231\n\t"
"v_mfma_f32_16x16x32_bf16 v[216:219], a[228:231], a[180:183], v[216:219]\n\t"
"buffer_load_dwordx4 v247, s[12:15], s28 offen offset:256 nt lds\n\t"
"v_mfma_f32_16x16x32_bf16 v[216:219], a[220:223], a[176:179], v[216:219]\n\t"
"s_add_u32 m0, s24, 6912\n\t"
"s_add_u32 s28, s21, 196608\n\t"
"ds_write2st64_b64 v245, v[232:233], v[234:235] offset0:48 offset1:49\n\t"
"v_mfma_f32_16x16x32_bf16 a[4:7], a[220:223], a[180:183], a[4:7]\n\t"
"buffer_load_dwordx4 v247, s[12:15], s28 offen offset:256 nt lds\n\t"
"v_mfma_f32_16x16x32_bf16 a[4:7], a[228:231], a[184:187], a[4:7]\n\t"
"s_add_u32 m0, s24, 13056\n\t"
"s_add_u32 s28, s21, 393216\n\t"
"ds_read_b64_tr_b16 v[232:233], v246 offset:32256\n\t"
"v_mfma_f32_16x16x32_bf16 a[4:7], a[224:227], a[188:191], a[4:7]\n\t"
"ds_write2st64_b64 v245, v[224:225], v[226:227] offset0:50 offset1:51\n\t"
"v_mfma_f32_16x16x32_bf16 a[12:15], a[224:227], a[192:195], a[12:15]\n\t"
"buffer_load_dwordx4 v247, s[12:15], s28 offen offset:256 nt lds\n\t"
"v_mfma_f32_16x16x32_bf16 a[12:15], a[228:231], a[188:191], a[12:15]\n\t"
"s_add_u32 m0, s24, 20736\n\t"
"s_add_u32 s28, s21, 589824\n\t"
"ds_read_b64_tr_b16 v[234:235], v246 offset:35328\n\t"
"v_mfma_f32_16x16x32_bf16 a[12:15], a[220:223], a[184:187], a[12:15]\n\t"
"ds_write_b64 v245, v[228:229] offset:26624\n\t"
"v_mfma_f32_16x16x32_bf16 a[20:23], a[220:223], a[188:191], a[20:23]\n\t"
"buffer_load_dwordx4 v247, s[12:15], s28 offen offset:256 nt lds\n\t"
"v_mfma_f32_16x16x32_bf16 a[20:23], a[228:231], a[192:195], a[20:23]\n\t"
"s_add_u32 m0, s24, 60672\n\t"
"s_add_u32 s28, s21, 1769472\n\t"
"ds_read_b64_tr_b16 v[224:225], v246 offset:39936\n\t"
"v_mfma_f32_16x16x32_bf16 a[20:23], a[224:227], a[196:199], a[20:23]\n\t"
"ds_read_b64_tr_b16 v[226:227], v246 offset:43008\n\t"
"v_mfma_f32_16x16x32_bf16 a[28:31], a[228:231], a[196:199], a[28:31]\n\t"
"buffer_load_dwordx4 v247, s[12:15], s28 offen offset:256 nt lds\n\t"
"v_mfma_f32_16x16x32_bf16 a[28:31], a[220:223], a[192:195], a[28:31]\n\t"
"s_waitcnt lgkmcnt(3)\n\t"
"ds_read_b64_tr_b16 v[228:229], v246 offset:46080\n\t"
"v_mfma_f32_16x16x32_bf16 a[28:31], a[224:227], a[200:203], a[28:31]\n\t"
"s_add_u32 m0, s24, 66816\n\t"
"s_add_u32 s28, s21, 1966080\n\t"
"ds_read_b64_tr_b16 v[230:231], v246 offset:49152\n\t"
"v_mfma_f32_16x16x32_bf16 v[232:235], v[232:235], v[220:223], 0\n\t"
"s_waitcnt lgkmcnt(2)\n\t"
"buffer_load_dwordx4 v247, s[12:15], s28 offen offset:256 nt lds\n\t"
"v_mfma_f32_16x16x32_bf16 v[224:227], v[224:227], v[220:223], 0\n\t"
"v_mfma_f32_16x16x32_bf16 a[36:39], a[228:231], a[200:203], a[36:39]\n\t"
"v_mfma_f32_16x16x32_bf16 a[36:39], a[220:223], a[196:199], a[36:39]\n\t"
"s_waitcnt vmcnt(16)\n\t"
"v_mfma_f32_16x16x32_bf16 a[36:39], a[224:227], a[204:207], a[36:39]\n\t"
"s_waitcnt lgkmcnt(0)\n\t"
"v_mfma_f32_16x16x32_bf16 v[228:231], v[228:231], v[220:223], 0\n\t"
"v_cvt_pk_bf16_f32 v232, v232, v233\n\t"
"v_cvt_pk_bf16_f32 v233, v234, v235\n\t"
"v_mfma_f32_16x16x32_bf16 a[44:47], a[232:235], a[168:171], a[44:47]\n\t"
"v_cvt_pk_bf16_f32 v234, v224, v225\n\t"
"v_cvt_pk_bf16_f32 v235, v226, v227\n\t"
"v_mfma_f32_16x16x32_bf16 a[44:47], a[240:243], a[176:179], a[44:47]\n\t"
"ds_read_b64_tr_b16 v[224:225], v246 offset:52224\n\t"
"v_mfma_f32_16x16x32_bf16 a[44:47], a[236:239], a[172:175], a[44:47]\n\t"
"ds_write2st64_b64 v245, v[232:233], v[234:235] offset0:53 offset1:54\n\t"
"v_mfma_f32_16x16x32_bf16 a[52:55], a[232:235], a[172:175], a[52:55]\n\t"
"v_mfma_f32_16x16x32_bf16 a[52:55], a[236:239], a[176:179], a[52:55]\n\t"
"ds_read_b64_tr_b16 v[226:227], v246 offset:55296\n\t"
"v_mfma_f32_16x16x32_bf16 a[52:55], a[240:243], a[180:183], a[52:55]\n\t"
"v_cvt_pk_bf16_f32 v228, v228, v229\n\t"
"v_cvt_pk_bf16_f32 v229, v230, v231\n\t"
"v_mfma_f32_16x16x32_bf16 a[60:63], a[240:243], a[184:187], a[60:63]\n\t"
"v_mfma_f32_16x16x32_bf16 a[60:63], a[236:239], a[180:183], a[60:63]\n\t"
"v_mfma_f32_16x16x32_bf16 a[60:63], a[232:235], a[176:179], a[60:63]\n\t"
"v_mfma_f32_16x16x32_bf16 a[68:71], a[232:235], a[180:183], a[68:71]\n\t"
"v_mfma_f32_16x16x32_bf16 a[68:71], a[236:239], a[184:187], a[68:71]\n\t"
"v_mfma_f32_16x16x32_bf16 a[68:71], a[240:243], a[188:191], a[68:71]\n\t"
"v_mfma_f32_16x16x32_bf16 a[76:79], a[236:239], a[188:191], a[76:79]\n\t"
"s_waitcnt lgkmcnt(0)\n\t"
"v_mfma_f32_16x16x32_bf16 a[76:79], a[240:243], a[192:195], a[76:79]\n\t"
"v_mfma_f32_16x16x32_bf16 a[76:79], a[232:235], a[184:187], a[76:79]\n\t"
"v_mfma_f32_16x16x32_bf16 v[224:227], v[224:227], v[220:223], 0\n\t"
"v_mfma_f32_16x16x32_bf16 a[84:87], a[232:235], a[188:191], a[84:87]\n\t"
"buffer_load_dwordx4 a[220:223], off, s[80:83], s20 offset:1024 sc1\n\t"
"v_mfma_f32_16x16x32_bf16 a[84:87], a[240:243], a[196:199], a[84:87]\n\t"
"v_mfma_f32_16x16x32_bf16 a[84:87], a[236:239], a[192:195], a[84:87]\n\t"
"v_mfma_f32_16x16x32_bf16 a[92:95], a[232:235], a[192:195], a[92:95]\n\t"
"v_mfma_f32_16x16x32_bf16 a[92:95], a[236:239], a[196:199], a[92:95]\n\t"
"v_cvt_pk_bf16_f32 v230, v224, v225\n\t"
"v_cvt_pk_bf16_f32 v231, v226, v227\n\t"
"v_mfma_f32_16x16x32_bf16 a[92:95], a[240:243], a[200:203], a[92:95]\n\t"
"ds_write2st64_b64 v245, v[228:229], v[230:231] offset0:55 offset1:56\n\t"
"v_mfma_f32_16x16x32_bf16 a[100:103], a[236:239], a[200:203], a[100:103]\n\t"
"s_waitcnt vmcnt(14)\n\t"
"v_mfma_f32_16x16x32_bf16 a[100:103], a[232:235], a[196:199], a[100:103]\n\t"
"v_mfma_f32_16x16x32_bf16 a[100:103], a[240:243], a[204:207], a[100:103]\n\t"
"v_mfma_f32_16x16x32_bf16 a[108:111], a[244:247], a[168:171], a[108:111]\n\t"
"v_mfma_f32_16x16x32_bf16 a[108:111], v[240:243], a[176:179], a[108:111]\n\t"
"v_mfma_f32_16x16x32_bf16 a[108:111], a[208:211], a[172:175], a[108:111]\n\t"
"s_waitcnt lgkmcnt(0)\n\t"
"s_barrier\n\t"
"v_mfma_f32_16x16x32_bf16 a[116:119], a[244:247], a[172:175], a[116:119]\n\t"
"ds_read_b128 a[168:171], v244 offset:24576\n\t"
"v_mfma_f32_16x16x32_bf16 a[116:119], a[208:211], a[176:179], a[116:119]\n\t"
"ds_read_b128 a[172:175], v244 offset:25088\n\t"
"v_mfma_f32_16x16x32_bf16 a[116:119], v[240:243], a[180:183], a[116:119]\n\t"
"v_mfma_f32_16x16x32_bf16 a[124:127], v[240:243], a[184:187], a[124:127]\n\t"
"v_mfma_f32_16x16x32_bf16 a[124:127], a[208:211], a[180:183], a[124:127]\n\t"
"v_mfma_f32_16x16x32_bf16 a[124:127], a[244:247], a[176:179], a[124:127]\n\t"
"ds_read_b128 a[176:179], v244 offset:25600\n\t"
"v_mfma_f32_16x16x32_bf16 a[132:135], a[244:247], a[180:183], a[132:135]\n\t"
"ds_read_b128 a[180:183], v244 offset:26112\n\t"
"v_mfma_f32_16x16x32_bf16 a[132:135], a[208:211], a[184:187], a[132:135]\n\t"
"v_mfma_f32_16x16x32_bf16 a[132:135], v[240:243], a[188:191], a[132:135]\n\t"
"v_mfma_f32_16x16x32_bf16 a[140:143], v[240:243], a[192:195], a[140:143]\n\t"
"v_mfma_f32_16x16x32_bf16 a[140:143], a[208:211], a[188:191], a[140:143]\n\t"
"v_mfma_f32_16x16x32_bf16 a[140:143], a[244:247], a[184:187], a[140:143]\n\t"
"ds_read_b128 a[184:187], v244 offset:26624\n\t"
"v_mfma_f32_16x16x32_bf16 a[148:151], a[244:247], a[188:191], a[148:151]\n\t"
"ds_read_b128 a[188:191], v244 offset:27136\n\t"
"v_mfma_f32_16x16x32_bf16 a[148:151], a[208:211], a[192:195], a[148:151]\n\t"
"v_mfma_f32_16x16x32_bf16 a[148:151], v[240:243], a[196:199], a[148:151]\n\t"
"v_mfma_f32_16x16x32_bf16 a[156:159], v[240:243], a[200:203], a[156:159]\n\t"
"v_mfma_f32_16x16x32_bf16 a[156:159], a[208:211], a[196:199], a[156:159]\n\t"
"v_mfma_f32_16x16x32_bf16 a[156:159], a[244:247], a[192:195], a[156:159]\n\t"
"ds_read_b128 a[192:195], v244 offset:27648\n\t"
"v_mfma_f32_16x16x32_bf16 a[164:167], a[244:247], a[196:199], a[164:167]\n\t"
"s_waitcnt vmcnt(11) lgkmcnt(4)\n\t"
"ds_read_b128 a[196:199], v244 offset:28160\n\t"
"v_mfma_f32_16x16x32_bf16 a[164:167], a[208:211], a[200:203], a[164:167]\n\t"
"ds_read_b128 a[200:203], v244 offset:28672\n\t"
"v_mfma_f32_16x16x32_bf16 a[164:167], v[240:243], a[204:207], a[164:167]\n\t"
"ds_read_b128 a[204:207], v244 offset:29184\n\t"
"Lst25_%=:\n\t"
"v_mfma_f32_16x16x32_bf16 v[0:3], v[96:99], a[168:171], v[0:3]\n\t"
"buffer_load_dwordx4 v[240:243], off, s[80:83], s20 offset:2048 sc1\n\t"
"v_mfma_f32_16x16x32_bf16 v[0:3], a[252:255], a[176:179], v[0:3]\n\t"
"s_waitcnt lgkmcnt(6)\n\t"
"s_mov_b32 s20, 663552\n\t"
"v_mfma_f32_16x16x32_bf16 v[0:3], a[248:251], a[172:175], v[0:3]\n\t"
"v_mfma_f32_16x16x32_bf16 v[4:7], v[96:99], a[172:175], v[4:7]\n\t"
"s_waitcnt lgkmcnt(5)\n\t"
"v_mfma_f32_16x16x32_bf16 v[4:7], a[248:251], a[176:179], v[4:7]\n\t"
"buffer_load_dwordx4 a[208:211], off, s[80:83], s20 offset:0 sc1\n\t"
"v_mfma_f32_16x16x32_bf16 v[4:7], a[252:255], a[180:183], v[4:7]\n\t"
"v_mfma_f32_16x16x32_bf16 v[8:11], a[252:255], a[184:187], v[8:11]\n\t"
"s_waitcnt lgkmcnt(4)\n\t"
"v_mfma_f32_16x16x32_bf16 v[8:11], a[248:251], a[180:183], v[8:11]\n\t"
"v_mfma_f32_16x16x32_bf16 v[8:11], v[96:99], a[176:179], v[8:11]\n\t"
"buffer_load_dwordx4 a[224:227], off, s[80:83], s20 offset:1024 sc1\n\t"
"v_mfma_f32_16x16x32_bf16 v[12:15], v[96:99], a[180:183], v[12:15]\n\t"
"s_waitcnt lgkmcnt(3)\n\t"
"ds_read_b64_tr_b16 v[232:233], v246 offset:512\n\t"
"v_mfma_f32_16x16x32_bf16 v[12:15], a[248:251], a[184:187], v[12:15]\n\t"
"ds_read_b64_tr_b16 v[234:235], v246 offset:3584\n\t"
"v_mfma_f32_16x16x32_bf16 v[12:15], a[252:255], a[188:191], v[12:15]\n\t"
"ds_read_b64_tr_b16 v[224:225], v246 offset:6656\n\t"
"v_mfma_f32_16x16x32_bf16 v[16:19], a[252:255], a[192:195], v[16:19]\n\t"
"buffer_load_dwordx4 a[228:231], off, s[80:83], s20 offset:2048 sc1\n\t"
"v_mfma_f32_16x16x32_bf16 v[16:19], a[248:251], a[188:191], v[16:19]\n\t"
"s_waitcnt lgkmcnt(5)\n\t"
"ds_read_b64_tr_b16 v[226:227], v246 offset:9728\n\t"
"s_mov_b32 s20, 675840\n\t"
"v_mfma_f32_16x16x32_bf16 v[16:19], v[96:99], a[184:187], v[16:19]\n\t"
"v_mfma_f32_16x16x32_bf16 v[20:23], v[96:99], a[188:191], v[20:23]\n\t"
"s_waitcnt lgkmcnt(5)\n\t"
"v_mfma_f32_16x16x32_bf16 v[20:23], a[248:251], a[192:195], v[20:23]\n\t"
"buffer_load_dwordx4 a[232:235], off, s[80:83], s20 offset:0 sc1\n\t"
"v_mfma_f32_16x16x32_bf16 v[20:23], a[252:255], a[196:199], v[20:23]\n\t"
"v_mfma_f32_16x16x32_bf16 v[24:27], a[252:255], a[200:203], v[24:27]\n\t"
"s_waitcnt lgkmcnt(4)\n\t"
"v_mfma_f32_16x16x32_bf16 v[24:27], a[248:251], a[196:199], v[24:27]\n\t"
"v_mfma_f32_16x16x32_bf16 v[24:27], v[96:99], a[192:195], v[24:27]\n\t"
"buffer_load_dwordx4 a[236:239], off, s[80:83], s20 offset:1024 sc1\n\t"
"v_mfma_f32_16x16x32_bf16 v[28:31], v[96:99], a[196:199], v[28:31]\n\t"
"s_waitcnt lgkmcnt(2)\n\t"
"v_mfma_f32_16x16x32_bf16 v[28:31], a[248:251], a[200:203], v[28:31]\n\t"
"s_waitcnt lgkmcnt(0)\n\t"
"v_mfma_f32_16x16x32_bf16 v[28:31], a[252:255], a[204:207], v[28:31]\n\t"
"s_waitcnt vmcnt(14)\n\t"
"v_mfma_f32_16x16x32_bf16 v[232:235], v[232:235], v[220:223], 0\n\t"
"buffer_load_dwordx4 v[96:99], off, s[80:83], s20 offset:2048 sc1\n\t"
"v_mfma_f32_16x16x32_bf16 v[224:227], v[224:227], v[220:223], 0\n\t"
"s_mov_b32 s20, 688128\n\t"
"v_mfma_f32_16x16x32_bf16 v[32:35], v[236:239], a[168:171], v[32:35]\n\t"
"v_mfma_f32_16x16x32_bf16 v[32:35], a[212:215], a[176:179], v[32:35]\n\t"
"v_mfma_f32_16x16x32_bf16 v[32:35], v[248:251], a[172:175], v[32:35]\n\t"
"buffer_load_dwordx4 a[240:243], off, s[80:83], s20 offset:0 sc1\n\t"
"v_mfma_f32_16x16x32_bf16 v[36:39], v[236:239], a[172:175], v[36:39]\n\t"
"v_cvt_pk_bf16_f32 v232, v232, v233\n\t"
"v_cvt_pk_bf16_f32 v233, v234, v235\n\t"
"v_mfma_f32_16x16x32_bf16 v[36:39], v[248:251], a[176:179], v[36:39]\n\t"
"v_cvt_pk_bf16_f32 v234, v224, v225\n\t"
"v_cvt_pk_bf16_f32 v235, v226, v227\n\t"
"v_mfma_f32_16x16x32_bf16 v[36:39], a[212:215], a[180:183], v[36:39]\n\t"
"v_mfma_f32_16x16x32_bf16 v[40:43], a[212:215], a[184:187], v[40:43]\n\t"
"buffer_load_dwordx4 a[244:247], off, s[80:83], s20 offset:1024 sc1\n\t"
"v_mfma_f32_16x16x32_bf16 v[40:43], v[248:251], a[180:183], v[40:43]\n\t"
"v_mfma_f32_16x16x32_bf16 v[40:43], v[236:239], a[176:179], v[40:43]\n\t"
"ds_read_b64_tr_b16 v[228:229], v246 offset:12800\n\t"
"v_mfma_f32_16x16x32_bf16 v[44:47], v[236:239], a[180:183], v[44:47]\n\t"
"buffer_load_dwordx4 a[248:251], off, s[80:83], s20 offset:2048 sc1\n\t"
"v_mfma_f32_16x16x32_bf16 v[44:47], v[248:251], a[184:187], v[44:47]\n\t"
"s_mov_b32 s20, 921600\n\t"
"ds_read_b64_tr_b16 v[230:231], v246 offset:15872\n\t"
"v_mfma_f32_16x16x32_bf16 v[44:47], a[212:215], a[188:191], v[44:47]\n\t"
"ds_read_b64_tr_b16 v[224:225], v246 offset:20480\n\t"
"v_mfma_f32_16x16x32_bf16 v[48:51], a[212:215], a[192:195], v[48:51]\n\t"
"ds_read_b64_tr_b16 v[226:227], v246 offset:23552\n\t"
"v_mfma_f32_16x16x32_bf16 v[48:51], v[248:251], a[188:191], v[48:51]\n\t"
"buffer_load_dwordx4 a[252:255], off, s[80:83], s20 offset:0 sc1\n\t"
"v_mfma_f32_16x16x32_bf16 v[48:51], v[236:239], a[184:187], v[48:51]\n\t"
"v_mfma_f32_16x16x32_bf16 v[52:55], v[236:239], a[188:191], v[52:55]\n\t"
"v_mfma_f32_16x16x32_bf16 v[52:55], v[248:251], a[192:195], v[52:55]\n\t"
"v_mfma_f32_16x16x32_bf16 v[52:55], a[212:215], a[196:199], v[52:55]\n\t"
"v_mfma_f32_16x16x32_bf16 v[56:59], a[212:215], a[200:203], v[56:59]\n\t"
"v_mfma_f32_16x16x32_bf16 v[56:59], v[248:251], a[196:199], v[56:59]\n\t"
"v_mfma_f32_16x16x32_bf16 v[56:59], v[236:239], a[192:195], v[56:59]\n\t"
"v_mfma_f32_16x16x32_bf16 v[60:63], v[236:239], a[196:199], v[60:63]\n\t"
"buffer_load_dwordx4 v[236:239], off, s[80:83], s20 offset:1024 sc1\n\t"
"v_mfma_f32_16x16x32_bf16 v[60:63], v[248:251], a[200:203], v[60:63]\n\t"
"s_waitcnt lgkmcnt(0)\n\t"
"v_mfma_f32_16x16x32_bf16 v[60:63], a[212:215], a[204:207], v[60:63]\n\t"
"s_waitcnt vmcnt(11)\n\t"
"v_mfma_f32_16x16x32_bf16 v[228:231], v[228:231], v[220:223], 0\n\t"
"buffer_load_dwordx4 v[248:251], off, s[80:83], s20 offset:2048 sc1\n\t"
"v_mfma_f32_16x16x32_bf16 v[224:227], v[224:227], v[220:223], 0\n\t"
"s_mov_b32 s20, 933888\n\t"
"v_mfma_f32_16x16x32_bf16 v[64:67], a[216:219], a[168:171], v[64:67]\n\t"
"ds_read_b128 a[168:171], v244 offset:33792\n\t"
"v_mfma_f32_16x16x32_bf16 v[64:67], v[240:243], a[176:179], v[64:67]\n\t"
"buffer_load_dwordx4 a[212:215], off, s[80:83], s20 offset:0 sc1\n\t"
"v_mfma_f32_16x16x32_bf16 v[64:67], a[220:223], a[172:175], v[64:67]\n\t"
"v_mfma_f32_16x16x32_bf16 v[68:71], a[216:219], a[172:175], v[68:71]\n\t"
"ds_read_b128 a[172:175], v244 offset:34304\n\t"
"v_cvt_pk_bf16_f32 v228, v228, v229\n\t"
"v_mfma_f32_16x16x32_bf16 v[68:71], a[220:223], a[176:179], v[68:71]\n\t"
"v_cvt_pk_bf16_f32 v229, v230, v231\n\t"
"v_cvt_pk_bf16_f32 v230, v224, v225\n\t"
"v_mfma_f32_16x16x32_bf16 v[68:71], v[240:243], a[180:183], v[68:71]\n\t"
"v_cvt_pk_bf16_f32 v231, v226, v227\n\t"
"v_mfma_f32_16x16x32_bf16 v[72:75], v[240:243], a[184:187], v[72:75]\n\t"
"v_mfma_f32_16x16x32_bf16 v[72:75], a[220:223], a[180:183], v[72:75]\n\t"
"v_mfma_f32_16x16x32_bf16 v[72:75], a[216:219], a[176:179], v[72:75]\n\t"
"ds_read_b128 a[176:179], v244 offset:34816\n\t"
"v_mfma_f32_16x16x32_bf16 v[76:79], a[216:219], a[180:183], v[76:79]\n\t"
"ds_read_b128 a[180:183], v244 offset:35328\n\t"
"v_mfma_f32_16x16x32_bf16 v[76:79], a[220:223], a[184:187], v[76:79]\n\t"
"ds_read_b64_tr_b16 v[224:225], v246 offset:26624\n\t"
"v_mfma_f32_16x16x32_bf16 v[76:79], v[240:243], a[188:191], v[76:79]\n\t"
"ds_read_b64_tr_b16 v[226:227], v246 offset:29696\n\t"
"v_mfma_f32_16x16x32_bf16 v[80:83], v[240:243], a[192:195], v[80:83]\n\t"
"v_mfma_f32_16x16x32_bf16 v[80:83], a[220:223], a[188:191], v[80:83]\n\t"
"v_mfma_f32_16x16x32_bf16 v[80:83], a[216:219], a[184:187], v[80:83]\n\t"
"ds_read_b128 a[184:187], v244 offset:35840\n\t"
"v_mfma_f32_16x16x32_bf16 v[84:87], a[216:219], a[188:191], v[84:87]\n\t"
"ds_read_b128 a[188:191], v244 offset:36352\n\t"
"v_mfma_f32_16x16x32_bf16 v[84:87], a[220:223], a[192:195], v[84:87]\n\t"
"v_mfma_f32_16x16x32_bf16 v[84:87], v[240:243], a[196:199], v[84:87]\n\t"
"v_mfma_f32_16x16x32_bf16 v[88:91], v[240:243], a[200:203], v[88:91]\n\t"
"v_mfma_f32_16x16x32_bf16 v[88:91], a[220:223], a[196:199], v[88:91]\n\t"
"v_mfma_f32_16x16x32_bf16 v[88:91], a[216:219], a[192:195], v[88:91]\n\t"
"ds_read_b128 a[192:195], v244 offset:36864\n\t"
"v_mfma_f32_16x16x32_bf16 v[92:95], a[216:219], a[196:199], v[92:95]\n\t"
"buffer_load_dwordx4 a[216:219], off, s[80:83], s20 offset:1024 sc1\n\t"
"v_mfma_f32_16x16x32_bf16 v[92:95], a[220:223], a[200:203], v[92:95]\n\t"
"s_waitcnt vmcnt(11) lgkmcnt(3)\n\t"
"ds_read_b128 a[196:199], v244 offset:37376\n\t"
"v_mfma_f32_16x16x32_bf16 v[92:95], v[240:243], a[204:207], v[92:95]\n\t"
"ds_read_b128 a[200:203], v244 offset:37888\n\t"
"v_mfma_f32_16x16x32_bf16 v[224:227], v[224:227], v[220:223], 0\n\t"
"ds_read_b128 a[204:207], v244 offset:38400\n\t"
"v_mfma_f32_16x16x32_bf16 v[100:103], a[208:211], a[168:171], v[100:103]\n\t"
"buffer_load_dwordx4 v[240:243], off, s[80:83], s20 offset:2048 sc1\n\t"
"v_mfma_f32_16x16x32_bf16 v[100:103], a[228:231], a[176:179], v[100:103]\n\t"
"s_mov_b32 s20, 946176\n\t"
"v_mfma_f32_16x16x32_bf16 v[100:103], a[224:227], a[172:175], v[100:103]\n\t"
"buffer_load_dwordx4 a[220:223], off, s[80:83], s20 offset:0 sc1\n\t"
"v_mfma_f32_16x16x32_bf16 v[104:107], a[208:211], a[172:175], v[104:107]\n\t"
"s_waitcnt lgkmcnt(5)\n\t"
"v_mfma_f32_16x16x32_bf16 v[104:107], a[224:227], a[176:179], v[104:107]\n\t"
"v_cvt_pk_bf16_f32 v224, v224, v225\n\t"
"v_mfma_f32_16x16x32_bf16 v[104:107], a[228:231], a[180:183], v[104:107]\n\t"
"s_waitcnt lgkmcnt(0)\n\t"
"s_barrier\n\t"
"v_mfma_f32_16x16x32_bf16 v[108:111], a[228:231], a[184:187], v[108:111]\n\t"
"v_cvt_pk_bf16_f32 v225, v226, v227\n\t"
"v_mfma_f32_16x16x32_bf16 v[108:111], a[224:227], a[180:183], v[108:111]\n\t"
"v_mfma_f32_16x16x32_bf16 v[108:111], a[208:211], a[176:179], v[108:111]\n\t"
"ds_write2st64_b64 v245, v[232:233], v[234:235] offset0:48 offset1:49\n\t"
"v_mfma_f32_16x16x32_bf16 v[112:115], a[208:211], a[180:183], v[112:115]\n\t"
"v_mfma_f32_16x16x32_bf16 v[112:115], a[224:227], a[184:187], v[112:115]\n\t"
"ds_read_b64_tr_b16 v[232:233], v246 offset:32768\n\t"
"v_mfma_f32_16x16x32_bf16 v[112:115], a[228:231], a[188:191], v[112:115]\n\t"
"ds_write2st64_b64 v245, v[228:229], v[230:231] offset0:50 offset1:51\n\t"
"v_mfma_f32_16x16x32_bf16 v[116:119], a[228:231], a[192:195], v[116:119]\n\t"
"v_mfma_f32_16x16x32_bf16 v[116:119], a[224:227], a[188:191], v[116:119]\n\t"
"ds_read_b64_tr_b16 v[234:235], v246 offset:35840\n\t"
"v_mfma_f32_16x16x32_bf16 v[116:119], a[208:211], a[184:187], v[116:119]\n\t"
"s_add_u32 m0, s24, 72960\n\t"
"s_add_u32 s28, s21, 2162688\n\t"
"ds_write_b64 v245, v[224:225] offset:26624\n\t"
"v_mfma_f32_16x16x32_bf16 v[120:123], a[208:211], a[188:191], v[120:123]\n\t"
"ds_read_b64_tr_b16 v[228:229], v246 offset:40448\n\t"
"v_mfma_f32_16x16x32_bf16 v[120:123], a[224:227], a[192:195], v[120:123]\n\t"
"ds_read_b64_tr_b16 v[230:231], v246 offset:43520\n\t"
"v_mfma_f32_16x16x32_bf16 v[120:123], a[228:231], a[196:199], v[120:123]\n\t"
"ds_read_b64_tr_b16 v[224:225], v246 offset:46592\n\t"
"v_mfma_f32_16x16x32_bf16 v[124:127], a[224:227], a[196:199], v[124:127]\n\t"
"s_waitcnt lgkmcnt(4)\n\t"
"ds_read_b64_tr_b16 v[226:227], v246 offset:49664\n\t"
"v_mfma_f32_16x16x32_bf16 v[124:127], a[208:211], a[192:195], v[124:127]\n\t"
"buffer_load_dwordx4 v247, s[12:15], s28 offen offset:256 nt lds\n\t"
"v_mfma_f32_16x16x32_bf16 v[124:127], a[228:231], a[200:203], v[124:127]\n\t"
"s_add_u32 m0, s24, 80640\n\t"
"s_add_u32 s28, s21, 2359296\n\t"
"v_mfma_f32_16x16x32_bf16 v[232:235], v[232:235], v[220:223], 0\n\t"
"s_waitcnt lgkmcnt(2)\n\t"
"buffer_load_dwordx4 v247, s[12:15], s28 offen offset:256 nt lds\n\t"
"s_add_u32 m0, s24, 26880\n\t"
"s_add_u32 s28, s21, 786432\n\t"
"v_mfma_f32_16x16x32_bf16 v[228:231], v[228:231], v[220:223], 0\n\t"
"buffer_load_dwordx4 v247, s[12:15], s28 offen offset:256 nt lds\n\t"
"v_mfma_f32_16x16x32_bf16 v[128:131], a[224:227], a[200:203], v[128:131]\n\t"
"s_waitcnt lgkmcnt(0)\n\t"
"s_add_u32 m0, s24, 86784\n\t"
"s_add_u32 s28, s21, 2555904\n\t"
"v_mfma_f32_16x16x32_bf16 v[128:131], a[208:211], a[196:199], v[128:131]\n\t"
"buffer_load_dwordx4 v247, s[12:15], s28 offen offset:256 nt lds\n\t"
"v_mfma_f32_16x16x32_bf16 v[128:131], a[228:231], a[204:207], v[128:131]\n\t"
"s_waitcnt vmcnt(14)\n\t"
"s_add_u32 m0, s24, 33024\n\t"
"s_add_u32 s28, s21, 983040\n\t"
"v_mfma_f32_16x16x32_bf16 v[224:227], v[224:227], v[220:223], 0\n\t"
"buffer_load_dwordx4 v247, s[12:15], s28 offen offset:256 nt lds\n\t"
"v_mfma_f32_16x16x32_bf16 v[132:135], a[232:235], a[168:171], v[132:135]\n\t"
"s_add_u32 m0, s24, 40704\n\t"
"s_add_u32 s28, s21, 1179648\n\t"
"v_cvt_pk_bf16_f32 v232, v232, v233\n\t"
"v_mfma_f32_16x16x32_bf16 v[132:135], v[96:99], a[176:179], v[132:135]\n\t"
"v_cvt_pk_bf16_f32 v233, v234, v235\n\t"
"v_cvt_pk_bf16_f32 v234, v228, v229\n\t"
"v_mfma_f32_16x16x32_bf16 v[132:135], a[236:239], a[172:175], v[132:135]\n\t"
"buffer_load_dwordx4 v247, s[12:15], s28 offen offset:256 nt lds\n\t"
"v_mfma_f32_16x16x32_bf16 v[136:139], a[232:235], a[172:175], v[136:139]\n\t"
"v_cvt_pk_bf16_f32 v235, v230, v231\n\t"
"v_mfma_f32_16x16x32_bf16 v[136:139], a[236:239], a[176:179], v[136:139]\n\t"
"ds_read_b64_tr_b16 v[228:229], v246 offset:52736\n\t"
"v_cvt_pk_bf16_f32 v224, v224, v225\n\t"
"v_mfma_f32_16x16x32_bf16 v[136:139], v[96:99], a[180:183], v[136:139]\n\t"
"ds_write2st64_b64 v245, v[232:233], v[234:235] offset0:53 offset1:54\n\t"
"v_mfma_f32_16x16x32_bf16 v[140:143], v[96:99], a[184:187], v[140:143]\n\t"
"v_cvt_pk_bf16_f32 v225, v226, v227\n\t"
"v_mfma_f32_16x16x32_bf16 v[140:143], a[236:239], a[180:183], v[140:143]\n\t"
"ds_read_b64_tr_b16 v[230:231], v246 offset:55808\n\t"
"v_mfma_f32_16x16x32_bf16 v[140:143], a[232:235], a[176:179], v[140:143]\n\t"
"v_mfma_f32_16x16x32_bf16 v[144:147], a[232:235], a[180:183], v[144:147]\n\t"
"v_mfma_f32_16x16x32_bf16 v[144:147], a[236:239], a[184:187], v[144:147]\n\t"
"v_mfma_f32_16x16x32_bf16 v[144:147], v[96:99], a[188:191], v[144:147]\n\t"
"v_mfma_f32_16x16x32_bf16 v[148:151], a[236:239], a[188:191], v[148:151]\n\t"
"s_waitcnt lgkmcnt(0)\n\t"
"v_mfma_f32_16x16x32_bf16 v[148:151], v[96:99], a[192:195], v[148:151]\n\t"
"v_mfma_f32_16x16x32_bf16 v[148:151], a[232:235], a[184:187], v[148:151]\n\t"
"v_mfma_f32_16x16x32_bf16 v[228:231], v[228:231], v[220:223], 0\n\t"
"v_mfma_f32_16x16x32_bf16 v[152:155], a[232:235], a[188:191], v[152:155]\n\t"
"v_mfma_f32_16x16x32_bf16 v[152:155], v[96:99], a[196:199], v[152:155]\n\t"
"v_mfma_f32_16x16x32_bf16 v[152:155], a[236:239], a[192:195], v[152:155]\n\t"
"v_mfma_f32_16x16x32_bf16 v[156:159], a[232:235], a[192:195], v[156:159]\n\t"
"v_mfma_f32_16x16x32_bf16 v[156:159], a[236:239], a[196:199], v[156:159]\n\t"
"v_cvt_pk_bf16_f32 v226, v228, v229\n\t"
"v_cvt_pk_bf16_f32 v227, v230, v231\n\t"
"v_mfma_f32_16x16x32_bf16 v[156:159], v[96:99], a[200:203], v[156:159]\n\t"
"ds_write2st64_b64 v245, v[224:225], v[226:227] offset0:55 offset1:56\n\t"
"v_mfma_f32_16x16x32_bf16 v[160:163], a[236:239], a[200:203], v[160:163]\n\t"
"s_waitcnt vmcnt(13)\n\t"
"v_mfma_f32_16x16x32_bf16 v[160:163], a[232:235], a[196:199], v[160:163]\n\t"
"v_mfma_f32_16x16x32_bf16 v[160:163], v[96:99], a[204:207], v[160:163]\n\t"
"v_mfma_f32_16x16x32_bf16 v[164:167], a[240:243], a[168:171], v[164:167]\n\t"
"v_mfma_f32_16x16x32_bf16 v[164:167], a[248:251], a[176:179], v[164:167]\n\t"
"v_mfma_f32_16x16x32_bf16 v[164:167], a[244:247], a[172:175], v[164:167]\n\t"
"s_waitcnt lgkmcnt(0)\n\t"
"s_barrier\n\t"
"v_mfma_f32_16x16x32_bf16 v[168:171], a[240:243], a[172:175], v[168:171]\n\t"
"ds_read_b128 a[168:171], v244 offset:24576\n\t"
"v_mfma_f32_16x16x32_bf16 v[168:171], a[244:247], a[176:179], v[168:171]\n\t"
"buffer_load_dwordx4 a[208:211], off, s[80:83], s20 offset:1024 sc1\n\t"
"v_mfma_f32_16x16x32_bf16 v[168:171], a[248:251], a[180:183], v[168:171]\n\t"
"ds_read_b128 a[172:175], v244 offset:25088\n\t"
"v_mfma_f32_16x16x32_bf16 v[172:175], a[248:251], a[184:187], v[172:175]\n\t"
"v_mfma_f32_16x16x32_bf16 v[172:175], a[244:247], a[180:183], v[172:175]\n\t"
"v_mfma_f32_16x16x32_bf16 v[172:175], a[240:243], a[176:179], v[172:175]\n\t"
"ds_read_b128 a[176:179], v244 offset:25600\n\t"
"v_mfma_f32_16x16x32_bf16 v[176:179], a[240:243], a[180:183], v[176:179]\n\t"
"ds_read_b128 a[180:183], v244 offset:26112\n\t"
"v_mfma_f32_16x16x32_bf16 v[176:179], a[244:247], a[184:187], v[176:179]\n\t"
"v_mfma_f32_16x16x32_bf16 v[176:179], a[248:251], a[188:191], v[176:179]\n\t"
"v_mfma_f32_16x16x32_bf16 v[180:183], a[248:251], a[192:195], v[180:183]\n\t"
"v_mfma_f32_16x16x32_bf16 v[180:183], a[244:247], a[188:191], v[180:183]\n\t"
"v_mfma_f32_16x16x32_bf16 v[180:183], a[240:243], a[184:187], v[180:183]\n\t"
"ds_read_b128 a[184:187], v244 offset:26624\n\t"
"v_mfma_f32_16x16x32_bf16 v[184:187], a[240:243], a[188:191], v[184:187]\n\t"
"ds_read_b128 a[188:191], v244 offset:27136\n\t"
"v_mfma_f32_16x16x32_bf16 v[184:187], a[244:247], a[192:195], v[184:187]\n\t"
"v_mfma_f32_16x16x32_bf16 v[184:187], a[248:251], a[196:199], v[184:187]\n\t"
"v_mfma_f32_16x16x32_bf16 v[188:191], a[248:251], a[200:203], v[188:191]\n\t"
"v_xor_b32 v246, 0xc0, v246\n\t"
"v_mfma_f32_16x16x32_bf16 v[188:191], a[244:247], a[196:199], v[188:191]\n\t"
"v_mfma_f32_16x16x32_bf16 v[188:191], a[240:243], a[192:195], v[188:191]\n\t"
"ds_read_b128 a[192:195], v244 offset:27648\n\t"
"v_mfma_f32_16x16x32_bf16 v[192:195], a[240:243], a[196:199], v[192:195]\n\t"
"s_waitcnt vmcnt(11) lgkmcnt(4)\n\t"
"ds_read_b128 a[196:199], v244 offset:28160\n\t"
"v_mfma_f32_16x16x32_bf16 v[192:195], a[244:247], a[200:203], v[192:195]\n\t"
"ds_read_b128 a[200:203], v244 offset:28672\n\t"
"v_mfma_f32_16x16x32_bf16 v[192:195], a[248:251], a[204:207], v[192:195]\n\t"
"s_waitcnt lgkmcnt(3)\n\t"
"ds_read_b128 a[204:207], v244 offset:29184\n\t"
"Lst26_%=:\n\t"
"v_mfma_f32_16x16x32_bf16 v[0:3], a[252:255], a[168:171], v[0:3]\n\t"
"s_add_u32 s23, s40, 6291456\n\t"
"s_add_u32 s22, s22, 6291456\n\t"
"v_mfma_f32_16x16x32_bf16 v[0:3], v[248:251], a[176:179], v[0:3]\n\t"
"Lpg19_%=:\n\t"
"s_cbranch_vccnz Lvr2_%=\n\t"
"v_mfma_f32_16x16x32_bf16 v[0:3], v[236:239], a[172:175], v[0:3]\n\t"
"v_mfma_f32_16x16x32_bf16 v[4:7], a[252:255], a[172:175], v[4:7]\n\t"
"v_mfma_f32_16x16x32_bf16 v[4:7], v[236:239], a[176:179], v[4:7]\n\t"
"v_mfma_f32_16x16x32_bf16 v[4:7], v[248:251], a[180:183], v[4:7]\n\t"
"v_mfma_f32_16x16x32_bf16 v[8:11], v[248:251], a[184:187], v[8:11]\n\t"
"v_mfma_f32_16x16x32_bf16 v[8:11], v[236:239], a[180:183], v[8:11]\n\t"
"v_mfma_f32_16x16x32_bf16 v[8:11], a[252:255], a[176:179], v[8:11]\n\t"
"ds_read_b64_tr_b16 v[232:233], v246 offset:512\n\t"
"v_mfma_f32_16x16x32_bf16 v[12:15], a[252:255], a[180:183], v[12:15]\n\t"
"s_waitcnt lgkmcnt(4)\n\t"
"ds_read_b64_tr_b16 v[234:235], v246 offset:3584\n\t"
"v_mfma_f32_16x16x32_bf16 v[12:15], v[236:239], a[184:187], v[12:15]\n\t"
"ds_read_b64_tr_b16 v[228:229], v246 offset:6656\n\t"
"v_mfma_f32_16x16x32_bf16 v[12:15], v[248:251], a[188:191], v[12:15]\n\t"
"buffer_load_dwordx4 v[96:99], off, s[80:83], s20 offset:2048 sc1\n\t"
"v_mfma_f32_16x16x32_bf16 v[16:19], v[248:251], a[192:195], v[16:19]\n\t"
"s_waitcnt lgkmcnt(5)\n\t"
"ds_read_b64_tr_b16 v[230:231], v246 offset:9728\n\t"
"s_mov_b32 s20, 958464\n\t"
"v_mfma_f32_16x16x32_bf16 v[16:19], v[236:239], a[188:191], v[16:19]\n\t"
"v_mfma_f32_16x16x32_bf16 v[16:19], a[252:255], a[184:187], v[16:19]\n\t"
"v_mfma_f32_16x16x32_bf16 v[20:23], a[252:255], a[188:191], v[20:23]\n\t"
"buffer_load_dwordx4 a[224:227], off, s[80:83], s20 offset:0 sc1\n\t"
"v_mfma_f32_16x16x32_bf16 v[20:23], v[236:239], a[192:195], v[20:23]\n\t"
"s_waitcnt lgkmcnt(5)\n\t"
"v_mfma_f32_16x16x32_bf16 v[20:23], v[248:251], a[196:199], v[20:23]\n\t"
"v_mfma_f32_16x16x32_bf16 v[24:27], v[248:251], a[200:203], v[24:27]\n\t"
"buffer_load_dwordx4 a[228:231], off, s[80:83], s20 offset:1024 sc1\n\t"
"v_mfma_f32_16x16x32_bf16 v[24:27], v[236:239], a[196:199], v[24:27]\n\t"
"s_waitcnt lgkmcnt(4)\n\t"
"v_mfma_f32_16x16x32_bf16 v[24:27], a[252:255], a[192:195], v[24:27]\n\t"
"v_mfma_f32_16x16x32_bf16 v[28:31], a[252:255], a[196:199], v[28:31]\n\t"
"s_waitcnt lgkmcnt(2)\n\t"
"v_mfma_f32_16x16x32_bf16 v[28:31], v[236:239], a[200:203], v[28:31]\n\t"
"buffer_load_dwordx4 v[236:239], off, s[80:83], s20 offset:2048 sc1\n\t"
"v_mfma_f32_16x16x32_bf16 v[28:31], v[248:251], a[204:207], v[28:31]\n\t"
"s_waitcnt vmcnt(12) lgkmcnt(0)\n\t"
"s_mov_b32 s20, 970752\n\t"
"v_mfma_f32_16x16x32_bf16 v[232:235], v[232:235], v[220:223], 0\n\t"
"v_mfma_f32_16x16x32_bf16 v[228:231], v[228:231], v[220:223], 0\n\t"
"v_mfma_f32_16x16x32_bf16 v[32:35], a[212:215], a[168:171], v[32:35]\n\t"
"buffer_load_dwordx4 v[248:251], off, s[80:83], s20 offset:0 sc1\n\t"
"v_mfma_f32_16x16x32_bf16 v[32:35], v[240:243], a[176:179], v[32:35]\n\t"
"v_mfma_f32_16x16x32_bf16 v[32:35], a[216:219], a[172:175], v[32:35]\n\t"
"v_mfma_f32_16x16x32_bf16 v[36:39], a[212:215], a[172:175], v[36:39]\n\t"
"v_cvt_pk_bf16_f32 v232, v232, v233\n\t"
"v_cvt_pk_bf16_f32 v233, v234, v235\n\t"
"v_mfma_f32_16x16x32_bf16 v[36:39], a[216:219], a[176:179], v[36:39]\n\t"
"v_cvt_pk_bf16_f32 v234, v228, v229\n\t"
"v_cvt_pk_bf16_f32 v235, v230, v231\n\t"
"v_mfma_f32_16x16x32_bf16 v[36:39], v[240:243], a[180:183], v[36:39]\n\t"
"v_mfma_f32_16x16x32_bf16 v[40:43], v[240:243], a[184:187], v[40:43]\n\t"
"buffer_load_dwordx4 a[232:235], off, s[80:83], s20 offset:1024 sc1\n\t"
"v_mfma_f32_16x16x32_bf16 v[40:43], a[216:219], a[180:183], v[40:43]\n\t"
"v_mfma_f32_16x16x32_bf16 v[40:43], a[212:215], a[176:179], v[40:43]\n\t"
"buffer_load_dwordx4 a[236:239], off, s[80:83], s20 offset:2048 sc1\n\t"
"v_mfma_f32_16x16x32_bf16 v[44:47], a[212:215], a[180:183], v[44:47]\n\t"
"s_mov_b32 s20, 983040\n\t"
"ds_read_b64_tr_b16 v[224:225], v246 offset:12800\n\t"
"v_mfma_f32_16x16x32_bf16 v[44:47], a[216:219], a[184:187], v[44:47]\n\t"
"ds_read_b64_tr_b16 v[226:227], v246 offset:15872\n\t"
"v_mfma_f32_16x16x32_bf16 v[44:47], v[240:243], a[188:191], v[44:47]\n\t"
"ds_read_b64_tr_b16 v[228:229], v246 offset:20480\n\t"
"v_mfma_f32_16x16x32_bf16 v[48:51], v[240:243], a[192:195], v[48:51]\n\t"
"buffer_load_dwordx4 a[240:243], off, s[80:83], s20 offset:0 sc1\n\t"
"v_mfma_f32_16x16x32_bf16 v[48:51], a[216:219], a[188:191], v[48:51]\n\t"
"ds_read_b64_tr_b16 v[230:231], v246 offset:23552\n\t"
"v_mfma_f32_16x16x32_bf16 v[48:51], a[212:215], a[184:187], v[48:51]\n\t"
"v_mfma_f32_16x16x32_bf16 v[52:55], a[212:215], a[188:191], v[52:55]\n\t"
"v_mfma_f32_16x16x32_bf16 v[52:55], a[216:219], a[192:195], v[52:55]\n\t"
"buffer_load_dwordx4 a[244:247], off, s[80:83], s20 offset:1024 sc1\n\t"
"v_mfma_f32_16x16x32_bf16 v[52:55], v[240:243], a[196:199], v[52:55]\n\t"
"v_mfma_f32_16x16x32_bf16 v[56:59], v[240:243], a[200:203], v[56:59]\n\t"
"v_mfma_f32_16x16x32_bf16 v[56:59], a[216:219], a[196:199], v[56:59]\n\t"
"v_mfma_f32_16x16x32_bf16 v[56:59], a[212:215], a[192:195], v[56:59]\n\t"
"buffer_load_dwordx4 a[248:251], off, s[80:83], s20 offset:2048 sc1\n\t"
"v_mfma_f32_16x16x32_bf16 v[60:63], a[212:215], a[196:199], v[60:63]\n\t"
"s_waitcnt lgkmcnt(2)\n\t"
"s_mov_b32 s20, 995328\n\t"
"v_mfma_f32_16x16x32_bf16 v[60:63], a[216:219], a[200:203], v[60:63]\n\t"
"s_waitcnt lgkmcnt(0)\n\t"
"v_mfma_f32_16x16x32_bf16 v[60:63], v[240:243], a[204:207], v[60:63]\n\t"
"s_waitcnt vmcnt(9)\n\t"
"v_mfma_f32_16x16x32_bf16 v[224:227], v[224:227], v[220:223], 0\n\t"
"buffer_load_dwordx4 a[212:215], off, s[80:83], s20 offset:0 sc1\n\t"
"v_mfma_f32_16x16x32_bf16 v[228:231], v[228:231], v[220:223], 0\n\t"
"v_mfma_f32_16x16x32_bf16 v[64:67], a[220:223], a[168:171], v[64:67]\n\t"
"ds_read_b128 a[168:171], v244 offset:33792\n\t"
"v_mfma_f32_16x16x32_bf16 v[64:67], v[96:99], a[176:179], v[64:67]\n\t"
"buffer_load_dwordx4 a[216:219], off, s[80:83], s20 offset:1024 sc1\n\t"
"v_mfma_f32_16x16x32_bf16 v[64:67], a[208:211], a[172:175], v[64:67]\n\t"
"v_mfma_f32_16x16x32_bf16 v[68:71], a[220:223], a[172:175], v[68:71]\n\t"
"ds_read_b128 a[172:175], v244 offset:34304\n\t"
"v_cvt_pk_bf16_f32 v224, v224, v225\n\t"
"v_mfma_f32_16x16x32_bf16 v[68:71], a[208:211], a[176:179], v[68:71]\n\t"
"v_cvt_pk_bf16_f32 v225, v226, v227\n\t"
"v_cvt_pk_bf16_f32 v226, v228, v229\n\t"
"v_mfma_f32_16x16x32_bf16 v[68:71], v[96:99], a[180:183], v[68:71]\n\t"
"v_cvt_pk_bf16_f32 v227, v230, v231\n\t"
"v_mfma_f32_16x16x32_bf16 v[72:75], v[96:99], a[184:187], v[72:75]\n\t"
"buffer_load_dwordx4 a[252:255], off, s[80:83], s20 offset:2048 sc1\n\t"
"v_mfma_f32_16x16x32_bf16 v[72:75], a[208:211], a[180:183], v[72:75]\n\t"
"v_mfma_f32_16x16x32_bf16 v[72:75], a[220:223], a[176:179], v[72:75]\n\t"
"ds_read_b128 a[176:179], v244 offset:34816\n\t"
"v_mfma_f32_16x16x32_bf16 v[76:79], a[220:223], a[180:183], v[76:79]\n\t"
"ds_read_b128 a[180:183], v244 offset:35328\n\t"
"v_mfma_f32_16x16x32_bf16 v[76:79], a[208:211], a[184:187], v[76:79]\n\t"
"ds_read_b64_tr_b16 v[228:229], v246 offset:26624\n\t"
"v_mfma_f32_16x16x32_bf16 v[76:79], v[96:99], a[188:191], v[76:79]\n\t"
"ds_read_b64_tr_b16 v[230:231], v246 offset:29696\n\t"
"v_mfma_f32_16x16x32_bf16 v[80:83], v[96:99], a[192:195], v[80:83]\n\t"
"v_mfma_f32_16x16x32_bf16 v[80:83], a[208:211], a[188:191], v[80:83]\n\t"
"v_mfma_f32_16x16x32_bf16 v[80:83], a[220:223], a[184:187], v[80:83]\n\t"
"ds_read_b128 a[184:187], v244 offset:35840\n\t"
"v_mfma_f32_16x16x32_bf16 v[84:87], a[220:223], a[188:191], v[84:87]\n\t"
"s_mov_b32 s20, 1007616\n\t"
"ds_read_b128 a[188:191], v244 offset:36352\n\t"
"v_mfma_f32_16x16x32_bf16 v[84:87], a[208:211], a[192:195], v[84:87]\n\t"
"v_mfma_f32_16x16x32_bf16 v[84:87], v[96:99], a[196:199], v[84:87]\n\t"
"v_mfma_f32_16x16x32_bf16 v[88:91], v[96:99], a[200:203], v[88:91]\n\t"
"v_mfma_f32_16x16x32_bf16 v[88:91], a[208:211], a[196:199], v[88:91]\n\t"
"v_mfma_f32_16x16x32_bf16 v[88:91], a[220:223], a[192:195], v[88:91]\n\t"
"ds_read_b128 a[192:195], v244 offset:36864\n\t"
"v_mfma_f32_16x16x32_bf16 v[92:95], a[220:223], a[196:199], v[92:95]\n\t"
"buffer_load_dwordx4 a[220:223], off, s[80:83], s20 offset:0 sc1\n\t"
"v_mfma_f32_16x16x32_bf16 v[92:95], a[208:211], a[200:203], v[92:95]\n\t"
"s_waitcnt vmcnt(10) lgkmcnt(3)\n\t"
"ds_read_b128 a[196:199], v244 offset:37376\n\t"
"v_mfma_f32_16x16x32_bf16 v[92:95], v[96:99], a[204:207], v[92:95]\n\t"
"ds_read_b128 a[200:203], v244 offset:37888\n\t"
"v_mfma_f32_16x16x32_bf16 v[228:231], v[228:231], v[220:223], 0\n\t"
"ds_read_b128 a[204:207], v244 offset:38400\n\t"
"v_mfma_f32_16x16x32_bf16 v[100:103], a[224:227], a[168:171], v[100:103]\n\t"
"buffer_load_dwordx4 v[96:99], off, s[80:83], s20 offset:1024 sc1\n\t"
"v_mfma_f32_16x16x32_bf16 v[100:103], v[236:239], a[176:179], v[100:103]\n\t"
"v_mfma_f32_16x16x32_bf16 v[100:103], a[228:231], a[172:175], v[100:103]\n\t"
"v_mfma_f32_16x16x32_bf16 v[104:107], a[224:227], a[172:175], v[104:107]\n\t"
"s_waitcnt lgkmcnt(5)\n\t"
"v_mfma_f32_16x16x32_bf16 v[104:107], a[228:231], a[176:179], v[104:107]\n\t"
"v_cvt_pk_bf16_f32 v228, v228, v229\n\t"
"v_mfma_f32_16x16x32_bf16 v[104:107], v[236:239], a[180:183], v[104:107]\n\t"
"s_waitcnt lgkmcnt(0)\n\t"
"s_barrier\n\t"
"v_mfma_f32_16x16x32_bf16 v[108:111], v[236:239], a[184:187], v[108:111]\n\t"
"v_cvt_pk_bf16_f32 v229, v230, v231\n\t"
"v_mfma_f32_16x16x32_bf16 v[108:111], a[228:231], a[180:183], v[108:111]\n\t"
"v_mfma_f32_16x16x32_bf16 v[108:111], a[224:227], a[176:179], v[108:111]\n\t"
"ds_write2st64_b64 v245, v[232:233], v[234:235] offset0:48 offset1:49\n\t"
"v_mfma_f32_16x16x32_bf16 v[112:115], a[224:227], a[180:183], v[112:115]\n\t"
"v_mfma_f32_16x16x32_bf16 v[112:115], a[228:231], a[184:187], v[112:115]\n\t"
"ds_read_b64_tr_b16 v[232:233], v246 offset:32768\n\t"
"v_mfma_f32_16x16x32_bf16 v[112:115], v[236:239], a[188:191], v[112:115]\n\t"
"ds_write2st64_b64 v245, v[224:225], v[226:227] offset0:50 offset1:51\n\t"
"v_mfma_f32_16x16x32_bf16 v[116:119], v[236:239], a[192:195], v[116:119]\n\t"
"v_mfma_f32_16x16x32_bf16 v[116:119], a[228:231], a[188:191], v[116:119]\n\t"
"ds_read_b64_tr_b16 v[234:235], v246 offset:35840\n\t"
"v_mfma_f32_16x16x32_bf16 v[116:119], a[224:227], a[184:187], v[116:119]\n\t"
"ds_write_b64 v245, v[228:229] offset:26624\n\t"
"v_mfma_f32_16x16x32_bf16 v[120:123], a[224:227], a[188:191], v[120:123]\n\t"
"ds_read_b64_tr_b16 v[224:225], v246 offset:40448\n\t"
"v_mfma_f32_16x16x32_bf16 v[120:123], a[228:231], a[192:195], v[120:123]\n\t"
"ds_read_b64_tr_b16 v[226:227], v246 offset:43520\n\t"
"v_mfma_f32_16x16x32_bf16 v[120:123], v[236:239], a[196:199], v[120:123]\n\t"
"ds_read_b64_tr_b16 v[228:229], v246 offset:46592\n\t"
"v_mfma_f32_16x16x32_bf16 v[124:127], a[228:231], a[196:199], v[124:127]\n\t"
"s_waitcnt lgkmcnt(4)\n\t"
"ds_read_b64_tr_b16 v[230:231], v246 offset:49664\n\t"
"v_mfma_f32_16x16x32_bf16 v[124:127], a[224:227], a[192:195], v[124:127]\n\t"
"global_load_dwordx4 v[240:243], v252, s[48:49] offset:2048\n\t"
"v_mfma_f32_16x16x32_bf16 v[124:127], v[236:239], a[200:203], v[124:127]\n\t"
"global_load_dwordx4 a[208:211], v252, s[46:47] offset:1024\n\t"
"v_mfma_f32_16x16x32_bf16 v[232:235], v[232:235], v[220:223], 0\n\t"
"s_waitcnt lgkmcnt(2)\n\t"
"v_mfma_f32_16x16x32_bf16 v[224:227], v[224:227], v[220:223], 0\n\t"
"v_mfma_f32_16x16x32_bf16 v[128:131], a[228:231], a[200:203], v[128:131]\n\t"
"buffer_load_dwordx4 a[228:231], off, s[80:83], s20 offset:2048 sc1\n\t"
"v_mfma_f32_16x16x32_bf16 v[128:131], a[224:227], a[196:199], v[128:131]\n\t"
"s_waitcnt vmcnt(11) lgkmcnt(0)\n\t"
"s_mov_b32 s20, 1019904\n\t"
"v_mfma_f32_16x16x32_bf16 v[128:131], v[236:239], a[204:207], v[128:131]\n\t"
"v_mfma_f32_16x16x32_bf16 v[228:231], v[228:231], v[220:223], 0\n\t"
"buffer_load_dwordx4 a[224:227], off, s[80:83], s20 offset:0 sc1\n\t"
"v_mfma_f32_16x16x32_bf16 v[132:135], v[248:251], a[168:171], v[132:135]\n\t"
"s_add_u32 m0, s24, 46848\n\t"
"s_add_u32 s28, s21, 1376256\n\t"
"v_cvt_pk_bf16_f32 v232, v232, v233\n\t"
"v_mfma_f32_16x16x32_bf16 v[132:135], a[236:239], a[176:179], v[132:135]\n\t"
"v_cvt_pk_bf16_f32 v233, v234, v235\n\t"
"v_cvt_pk_bf16_f32 v234, v224, v225\n\t"
"v_mfma_f32_16x16x32_bf16 v[132:135], a[232:235], a[172:175], v[132:135]\n\t"
"v_cvt_pk_bf16_f32 v235, v226, v227\n\t"
"v_mfma_f32_16x16x32_bf16 v[136:139], v[248:251], a[172:175], v[136:139]\n\t"
"buffer_load_dwordx4 v247, s[12:15], s28 offen offset:256 nt lds\n\t"
"v_mfma_f32_16x16x32_bf16 v[136:139], a[232:235], a[176:179], v[136:139]\n\t"
"s_add_u32 m0, s24, 52992\n\t"
"s_add_u32 s28, s21, 1572864\n\t"
"ds_read_b64_tr_b16 v[224:225], v246 offset:52736\n\t"
"v_mfma_f32_16x16x32_bf16 v[136:139], a[236:239], a[180:183], v[136:139]\n\t"
"buffer_load_dwordx4 v247, s[12:15], s28 offen offset:256 nt lds\n\t"
"v_mfma_f32_16x16x32_bf16 v[140:143], a[236:239], a[184:187], v[140:143]\n\t"
"s_add_u32 m0, s24, 92928\n\t"
"s_add_u32 s28, s21, 2752512\n\t"
"ds_read_b64_tr_b16 v[226:227], v246 offset:55808\n\t"
"v_mfma_f32_16x16x32_bf16 v[140:143], a[232:235], a[180:183], v[140:143]\n\t"
"buffer_load_dwordx4 v247, s[12:15], s28 offen offset:256 nt lds\n\t"
"v_mfma_f32_16x16x32_bf16 v[140:143], v[248:251], a[176:179], v[140:143]\n\t"
"s_add_u32 m0, s24, 100608\n\t"
"s_add_u32 s28, s21, 2949120\n\t"
"ds_write2st64_b64 v245, v[232:233], v[234:235] offset0:53 offset1:54\n\t"
"v_cvt_pk_bf16_f32 v228, v228, v229\n\t"
"v_cvt_pk_bf16_f32 v229, v230, v231\n\t"
"v_mfma_f32_16x16x32_bf16 v[144:147], v[248:251], a[180:183], v[144:147]\n\t"
"v_mfma_f32_16x16x32_bf16 v[144:147], a[232:235], a[184:187], v[144:147]\n\t"
"buffer_load_dwordx4 v247, s[12:15], s28 offen offset:256 nt lds\n\t"
"v_mfma_f32_16x16x32_bf16 v[144:147], a[236:239], a[188:191], v[144:147]\n\t"
"s_add_u32 m0, s24, 106752\n\t"
"s_add_u32 s28, s21, 3145728\n\t"
"v_mfma_f32_16x16x32_bf16 v[148:151], a[232:235], a[188:191], v[148:151]\n\t"
"buffer_load_dwordx4 v247, s[12:15], s28 offen offset:256 nt lds\n\t"
"v_mfma_f32_16x16x32_bf16 v[148:151], a[236:239], a[192:195], v[148:151]\n\t"
"s_waitcnt lgkmcnt(1)\n\t"
"s_add_u32 m0, s24, 112896\n\t"
"s_add_u32 s28, s21, 3342336\n\t"
"v_mfma_f32_16x16x32_bf16 v[148:151], v[248:251], a[184:187], v[148:151]\n\t"
"buffer_load_dwordx4 v247, s[12:15], s28 offen offset:256 nt lds\n\t"
"s_add_u32 m0, s25, 768\n\t"
"v_mfma_f32_16x16x32_bf16 v[224:227], v[224:227], v[220:223], 0\n\t"
"buffer_load_dwordx4 v254, s[12:15], 0 offen offset:256 nt lds\n\t"
"v_mfma_f32_16x16x32_bf16 v[152:155], v[248:251], a[188:191], v[152:155]\n\t"
"s_add_u32 m0, s26, 768\n\t"
"v_mfma_f32_16x16x32_bf16 v[152:155], a[236:239], a[196:199], v[152:155]\n\t"
"v_mfma_f32_16x16x32_bf16 v[152:155], a[232:235], a[192:195], v[152:155]\n\t"
"s_mov_b64 exec, s[36:37]\n\t"
"buffer_load_dwordx4 v254, s[12:15], s27 offen offset:256 nt lds\n\t"
"s_mov_b64 exec, -1\n\t"
"v_mfma_f32_16x16x32_bf16 v[156:159], v[248:251], a[192:195], v[156:159]\n\t"
"v_mfma_f32_16x16x32_bf16 v[156:159], a[232:235], a[196:199], v[156:159]\n\t"
"v_cvt_pk_bf16_f32 v230, v224, v225\n\t"
"v_cvt_pk_bf16_f32 v231, v226, v227\n\t"
"v_mfma_f32_16x16x32_bf16 v[156:159], a[236:239], a[200:203], v[156:159]\n\t"
"ds_write2st64_b64 v245, v[228:229], v[230:231] offset0:55 offset1:56\n\t"
"v_mfma_f32_16x16x32_bf16 v[160:163], a[232:235], a[200:203], v[160:163]\n\t"
"s_waitcnt vmcnt(17)\n\t"
"v_mfma_f32_16x16x32_bf16 v[160:163], v[248:251], a[196:199], v[160:163]\n\t"
"s_waitcnt vmcnt(11)\n\t"
"ds_read_b64_tr_b16 v[236:237], v240 offset:512\n\t"
"v_mfma_f32_16x16x32_bf16 v[160:163], a[236:239], a[204:207], v[160:163]\n\t"
"ds_read_b64_tr_b16 v[238:239], v240 offset:2048\n\t"
"v_mfma_f32_16x16x32_bf16 v[164:167], a[240:243], a[168:171], v[164:167]\n\t"
"ds_read_b64_tr_b16 v[232:233], v241 offset:512\n\t"
"v_mfma_f32_16x16x32_bf16 v[164:167], a[248:251], a[176:179], v[164:167]\n\t"
"ds_read_b64_tr_b16 v[234:235], v241 offset:2048\n\t"
"v_mfma_f32_16x16x32_bf16 v[164:167], a[244:247], a[172:175], v[164:167]\n\t"
"s_waitcnt lgkmcnt(4)\n\t"
"s_barrier\n\t"
"v_mfma_f32_16x16x32_bf16 v[168:171], a[240:243], a[172:175], v[168:171]\n\t"
"ds_read_b64_tr_b16 v[224:225], v240 offset:20480\n\t"
"v_mfma_f32_16x16x32_bf16 v[168:171], a[244:247], a[176:179], v[168:171]\n\t"
"ds_read_b64_tr_b16 v[226:227], v240 offset:22016\n\t"
"v_mfma_f32_16x16x32_bf16 v[168:171], a[248:251], a[180:183], v[168:171]\n\t"
"ds_read_b64_tr_b16 v[228:229], v241 offset:20480\n\t"
"v_mfma_f32_16x16x32_bf16 v[172:175], a[244:247], a[180:183], v[172:175]\n\t"
"ds_read_b64_tr_b16 v[230:231], v241 offset:22016\n\t"
"v_mfma_f32_16x16x32_bf16 v[172:175], a[240:243], a[176:179], v[172:175]\n\t"
"ds_read_b128 a[168:171], v244 offset:24576\n\t"
"v_mfma_f32_16x16x32_bf16 v[172:175], a[248:251], a[184:187], v[172:175]\n\t"
"ds_read_b128 a[172:175], v244 offset:25088\n\t"
"v_mfma_f32_16x16x32_bf16 v[176:179], a[240:243], a[180:183], v[176:179]\n\t"
"s_waitcnt vmcnt(10) lgkmcnt(8)\n\t"
"ds_read_b128 a[176:179], v244 offset:25600\n\t"
"v_mfma_f32_16x16x32_bf16 v[176:179], a[248:251], a[188:191], v[176:179]\n\t"
"s_waitcnt lgkmcnt(7)\n\t"
"ds_read_b128 a[180:183], v244 offset:26112\n\t"
"v_mfma_f32_16x16x32_bf16 v[176:179], a[244:247], a[184:187], v[176:179]\n\t"
"s_waitcnt lgkmcnt(6)\n\t"
"v_mfma_f32_16x16x32_bf16 v[236:239], v[236:239], a[208:211], 0\n\t"
"s_waitcnt lgkmcnt(4)\n\t"
"v_mfma_f32_16x16x32_bf16 v[232:235], v[232:235], a[208:211], 0\n\t"
"v_mfma_f32_16x16x32_bf16 v[224:227], v[224:227], a[208:211], 0\n\t"
"v_mfma_f32_16x16x32_bf16 v[228:231], v[228:231], a[208:211], 0\n\t"
"v_mfma_f32_16x16x32_bf16 v[180:183], a[240:243], a[184:187], v[180:183]\n\t"
"ds_read_b128 a[184:187], v244 offset:26624\n\t"
"v_mfma_f32_16x16x32_bf16 v[180:183], a[248:251], a[192:195], v[180:183]\n\t"
"v_cvt_pk_bf16_f32 v236, v236, v237\n\t"
"v_cvt_pk_bf16_f32 v237, v238, v239\n\t"
"v_mfma_f32_16x16x32_bf16 v[180:183], a[244:247], a[188:191], v[180:183]\n\t"
"v_cvt_pk_bf16_f32 v238, v232, v233\n\t"
"v_cvt_pk_bf16_f32 v239, v234, v235\n\t"
"v_mfma_f32_16x16x32_bf16 v[184:187], a[240:243], a[188:191], v[184:187]\n\t"
"ds_read_b128 a[188:191], v244 offset:27136\n\t"
"v_cvt_pk_bf16_f32 v224, v224, v225\n\t"
"v_mfma_f32_16x16x32_bf16 v[184:187], a[244:247], a[192:195], v[184:187]\n\t"
"ds_read_b64_tr_b16 v[232:233], v240 offset:40448\n\t"
"v_cvt_pk_bf16_f32 v225, v226, v227\n\t"
"v_mfma_f32_16x16x32_bf16 v[184:187], a[248:251], a[196:199], v[184:187]\n\t"
"ds_read_b64_tr_b16 v[234:235], v240 offset:41984\n\t"
"v_cvt_pk_bf16_f32 v226, v228, v229\n\t"
"v_mfma_f32_16x16x32_bf16 v[188:191], a[248:251], a[200:203], v[188:191]\n\t"
"v_cvt_pk_bf16_f32 v227, v230, v231\n\t"
"v_mfma_f32_16x16x32_bf16 v[188:191], a[244:247], a[196:199], v[188:191]\n\t"
"ds_read_b64_tr_b16 v[228:229], v241 offset:40448\n\t"
"v_mfma_f32_16x16x32_bf16 v[188:191], a[240:243], a[192:195], v[188:191]\n\t"
"ds_read_b64_tr_b16 v[230:231], v241 offset:41984\n\t"
"v_mfma_f32_16x16x32_bf16 v[192:195], a[240:243], a[196:199], v[192:195]\n\t"
"s_waitcnt lgkmcnt(7)\n\t"
"ds_read_b128 a[192:195], v244 offset:27648\n\t"
"v_mfma_f32_16x16x32_bf16 v[192:195], a[244:247], a[200:203], v[192:195]\n\t"
"ds_read_b128 a[196:199], v244 offset:28160\n\t"
"v_mfma_f32_16x16x32_bf16 v[192:195], a[248:251], a[204:207], v[192:195]\n\t"
"ds_read_b128 a[200:203], v244 offset:28672\n\t"
"Lst27_%=:\n\t"
"v_mfma_f32_16x16x32_bf16 v[196:199], a[212:215], a[168:171], v[196:199]\n\t"
"s_mov_b64 vcc, -1\n\t"
"s_bitcmp1_b32 s73, 1\n\t"
"s_cbranch_scc1 Lvh4_%=\n\t"
"s_bitcmp1_b32 s73, 0\n\t"
"s_cbranch_scc1 Lvr4_%=\n\t"
"s_branch Lpg25_%=\n\t"
"Lvh4_%=:\n\t"
"s_bitcmp1_b32 s73, 0\n\t"
"s_cbranch_scc1 Lvr4_%=\n\t"
"s_branch Lvr4_%=\n\t"
"Lvr4_%=:\n\t"
"s_mov_b64 vcc, 0\n\t"
"s_waitcnt lgkmcnt(9)\n\t"
"ds_read_b128 a[204:207], v244 offset:29184\n\t"
"v_mfma_f32_16x16x32_bf16 v[196:199], a[252:255], a[176:179], v[196:199]\n\t"
"v_mfma_f32_16x16x32_bf16 v[196:199], a[216:219], a[172:175], v[196:199]\n\t"
"buffer_load_dwordx4 v[248:251], off, s[80:83], s20 offset:1024 sc1\n\t"
"v_mfma_f32_16x16x32_bf16 v[204:207], a[212:215], a[172:175], v[204:207]\n\t"
"s_waitcnt lgkmcnt(6)\n\t"
"v_mfma_f32_16x16x32_bf16 v[204:207], a[216:219], a[176:179], v[204:207]\n\t"
"s_waitcnt lgkmcnt(4)\n\t"
"v_mfma_f32_16x16x32_bf16 v[204:207], a[252:255], a[180:183], v[204:207]\n\t"
"v_mfma_f32_16x16x32_bf16 v[232:235], v[232:235], a[208:211], 0\n\t"
"v_add_u32 v240, 59904, v240\n\t"
"v_add_u32 v241, 59904, v241\n\t"
"v_mfma_f32_16x16x32_bf16 v[228:231], v[228:231], a[208:211], 0\n\t"
"v_mfma_f32_16x16x32_bf16 v[212:215], a[216:219], a[180:183], v[212:215]\n\t"
"v_mfma_f32_16x16x32_bf16 v[212:215], a[212:215], a[176:179], v[212:215]\n\t"
"v_mfma_f32_16x16x32_bf16 v[212:215], a[252:255], a[184:187], v[212:215]\n\t"
"s_barrier\n\t"
"v_mfma_f32_16x16x32_bf16 a[0:3], a[212:215], a[180:183], a[0:3]\n\t"
"s_waitcnt lgkmcnt(3)\n\t"
"v_cvt_pk_bf16_f32 v232, v232, v233\n\t"
"v_mfma_f32_16x16x32_bf16 a[0:3], a[252:255], a[188:191], a[0:3]\n\t"
"v_cvt_pk_bf16_f32 v233, v234, v235\n\t"
"v_cvt_pk_bf16_f32 v234, v228, v229\n\t"
"v_mfma_f32_16x16x32_bf16 a[0:3], a[216:219], a[184:187], a[0:3]\n\t"
"ds_write_b128 v243, v[236:239] offset:512\n\t"
"v_mfma_f32_16x16x32_bf16 a[8:11], a[212:215], a[184:187], a[8:11]\n\t"
"s_waitcnt lgkmcnt(3)\n\t"
"v_cvt_pk_bf16_f32 v235, v230, v231\n\t"
"v_mfma_f32_16x16x32_bf16 a[8:11], a[216:219], a[188:191], a[8:11]\n\t"
"ds_read_b64_tr_b16 v[236:237], v240 offset:512\n\t"
"v_mfma_f32_16x16x32_bf16 a[8:11], a[252:255], a[192:195], a[8:11]\n\t"
"ds_write_b128 v243, v[224:227] offset:9728\n\t"
"v_mfma_f32_16x16x32_bf16 a[16:19], a[252:255], a[196:199], a[16:19]\n\t"
"buffer_load_dwordx4 a[232:235], off, s[80:83], s20 offset:2048 sc1\n\t"
"v_mfma_f32_16x16x32_bf16 a[16:19], a[216:219], a[192:195], a[16:19]\n\t"
"s_waitcnt lgkmcnt(4)\n\t"
"ds_read_b64_tr_b16 v[238:239], v240 offset:2048\n\t"
"v_mfma_f32_16x16x32_bf16 a[16:19], a[212:215], a[188:191], a[16:19]\n\t"
"s_mov_b32 s20, 1032192\n\t"
"ds_write_b128 v243, v[232:235] offset:18944\n\t"
"v_mfma_f32_16x16x32_bf16 a[24:27], a[212:215], a[192:195], a[24:27]\n\t"
"buffer_load_dwordx4 a[236:239], off, s[80:83], s20 offset:0 sc1\n\t"
"v_mfma_f32_16x16x32_bf16 a[24:27], a[216:219], a[196:199], a[24:27]\n\t"
"s_waitcnt lgkmcnt(5)\n\t"
"ds_read_b64_tr_b16 v[224:225], v241 offset:512\n\t"
"v_mfma_f32_16x16x32_bf16 a[24:27], a[252:255], a[200:203], a[24:27]\n\t"
"ds_read_b64_tr_b16 v[226:227], v241 offset:2048\n\t"
"v_mfma_f32_16x16x32_bf16 a[32:35], a[216:219], a[200:203], a[32:35]\n\t"
"s_waitcnt lgkmcnt(3)\n\t"
"ds_read_b64_tr_b16 v[228:229], v240 offset:20480\n\t"
"v_mfma_f32_16x16x32_bf16 a[32:35], a[212:215], a[196:199], a[32:35]\n\t"
"ds_read_b64_tr_b16 v[230:231], v240 offset:22016\n\t"
"v_mfma_f32_16x16x32_bf16 a[32:35], a[252:255], a[204:207], a[32:35]\n\t"
"ds_read_b64_tr_b16 v[232:233], v241 offset:20480\n\t"
"ds_read_b64_tr_b16 v[234:235], v241 offset:22016\n\t"
"v_mfma_f32_16x16x32_bf16 v[236:239], v[236:239], a[208:211], 0\n\t"
"s_waitcnt lgkmcnt(4)\n\t"
"v_mfma_f32_16x16x32_bf16 v[224:227], v[224:227], a[208:211], 0\n\t"
"s_waitcnt vmcnt(12) lgkmcnt(2)\n\t"
"buffer_load_dwordx4 a[212:215], off, s[80:83], s20 offset:1024 sc1\n\t"
"v_mfma_f32_16x16x32_bf16 v[228:231], v[228:231], a[208:211], 0\n\t"
"s_waitcnt lgkmcnt(0)\n\t"
"v_mfma_f32_16x16x32_bf16 v[232:235], v[232:235], a[208:211], 0\n\t"
"buffer_load_dwordx4 a[216:219], off, s[80:83], s20 offset:2048 sc1\n\t"
"v_mfma_f32_16x16x32_bf16 a[40:43], a[220:223], a[168:171], a[40:43]\n\t"
"s_mov_b32 s20, 1044480\n\t"
"v_mfma_f32_16x16x32_bf16 a[40:43], a[228:231], a[176:179], a[40:43]\n\t"
"v_cvt_pk_bf16_f32 v236, v236, v237\n\t"
"v_cvt_pk_bf16_f32 v237, v238, v239\n\t"
"v_mfma_f32_16x16x32_bf16 a[40:43], v[96:99], a[172:175], a[40:43]\n\t"
"v_cvt_pk_bf16_f32 v238, v224, v225\n\t"
"v_cvt_pk_bf16_f32 v239, v226, v227\n\t"
"v_mfma_f32_16x16x32_bf16 a[48:51], a[220:223], a[172:175], a[48:51]\n\t"
"v_cvt_pk_bf16_f32 v228, v228, v229\n\t"
"v_cvt_pk_bf16_f32 v229, v230, v231\n\t"
"v_mfma_f32_16x16x32_bf16 a[48:51], v[96:99], a[176:179], a[48:51]\n\t"
"v_cvt_pk_bf16_f32 v230, v232, v233\n\t"
"v_cvt_pk_bf16_f32 v231, v234, v235\n\t"
"v_mfma_f32_16x16x32_bf16 a[48:51], a[228:231], a[180:183], a[48:51]\n\t"
"buffer_load_dwordx4 a[240:243], off, s[80:83], s20 offset:0 sc1\n\t"
"v_mfma_f32_16x16x32_bf16 a[56:59], a[228:231], a[184:187], a[56:59]\n\t"
"buffer_load_dwordx4 a[244:247], off, s[80:83], s20 offset:1024 sc1\n\t"
"v_mfma_f32_16x16x32_bf16 a[56:59], v[96:99], a[180:183], a[56:59]\n\t"
"ds_read_b64_tr_b16 v[224:225], v240 offset:40448\n\t"
"v_mfma_f32_16x16x32_bf16 a[56:59], a[220:223], a[176:179], a[56:59]\n\t"
"ds_write_b128 v243, v[236:239] offset:28160\n\t"
"v_mfma_f32_16x16x32_bf16 a[64:67], a[220:223], a[180:183], a[64:67]\n\t"
"v_mfma_f32_16x16x32_bf16 a[64:67], v[96:99], a[184:187], a[64:67]\n\t"
"ds_read_b64_tr_b16 v[226:227], v240 offset:41984\n\t"
"v_mfma_f32_16x16x32_bf16 a[64:67], a[228:231], a[188:191], a[64:67]\n\t"
"ds_write_b128 v243, v[228:231] offset:37376\n\t"
"v_mfma_f32_16x16x32_bf16 a[72:75], a[228:231], a[192:195], a[72:75]\n\t"
"buffer_load_dwordx4 a[248:251], off, s[80:83], s20 offset:2048 sc1\n\t"
"v_mfma_f32_16x16x32_bf16 a[72:75], v[96:99], a[188:191], a[72:75]\n\t"
"s_mov_b32 s20, 1056768\n\t"
"ds_read_b64_tr_b16 v[232:233], v241 offset:40448\n\t"
"v_mfma_f32_16x16x32_bf16 a[72:75], a[220:223], a[184:187], a[72:75]\n\t"
"ds_read_b64_tr_b16 v[234:235], v241 offset:41984\n\t"
"v_mfma_f32_16x16x32_bf16 a[80:83], a[220:223], a[188:191], a[80:83]\n\t"
"buffer_load_dwordx4 a[252:255], off, s[80:83], s20 offset:0 sc1\n\t"
"v_mfma_f32_16x16x32_bf16 a[80:83], v[96:99], a[192:195], a[80:83]\n\t"
"v_mfma_f32_16x16x32_bf16 a[80:83], a[228:231], a[196:199], a[80:83]\n\t"
"v_mfma_f32_16x16x32_bf16 a[88:91], v[96:99], a[196:199], a[88:91]\n\t"
"s_waitcnt lgkmcnt(3)\n\t"
"v_mfma_f32_16x16x32_bf16 a[88:91], a[220:223], a[192:195], a[88:91]\n\t"
"s_waitcnt lgkmcnt(0)\n\t"
"v_mfma_f32_16x16x32_bf16 a[88:91], a[228:231], a[200:203], a[88:91]\n\t"
"v_mfma_f32_16x16x32_bf16 v[224:227], v[224:227], a[208:211], 0\n\t"
"v_mfma_f32_16x16x32_bf16 v[232:235], v[232:235], a[208:211], 0\n\t"
"v_mfma_f32_16x16x32_bf16 a[96:99], v[96:99], a[200:203], a[96:99]\n\t"
"buffer_load_dwordx4 v[96:99], off, s[80:83], s20 offset:1024 sc1\n\t"
"v_mfma_f32_16x16x32_bf16 a[96:99], a[220:223], a[196:199], a[96:99]\n\t"
"s_waitcnt vmcnt(8)\n\t"
"v_mfma_f32_16x16x32_bf16 a[96:99], a[228:231], a[204:207], a[96:99]\n\t"
"ds_read_b64_tr_b16 v[236:237], v246 offset:1024\n\t"
"v_mfma_f32_16x16x32_bf16 a[104:107], a[224:227], a[168:171], a[104:107]\n\t"
"buffer_load_dwordx4 a[220:223], off, s[80:83], s20 offset:2048 sc1\n\t"
"v_mfma_f32_16x16x32_bf16 a[104:107], a[232:235], a[176:179], a[104:107]\n\t"
"s_mov_b32 s20, 1290240\n\t"
"ds_read_b64_tr_b16 v[238:239], v246 offset:4096\n\t"
"v_cvt_pk_bf16_f32 v224, v224, v225\n\t"
"v_mfma_f32_16x16x32_bf16 a[104:107], v[248:251], a[172:175], a[104:107]\n\t"
"ds_read_b64_tr_b16 v[228:229], v246 offset:7168\n\t"
"v_cvt_pk_bf16_f32 v225, v226, v227\n\t"
"v_mfma_f32_16x16x32_bf16 a[112:115], a[224:227], a[172:175], a[112:115]\n\t"
"ds_read_b64_tr_b16 v[230:231], v246 offset:10240\n\t"
"v_cvt_pk_bf16_f32 v226, v232, v233\n\t"
"v_mfma_f32_16x16x32_bf16 a[112:115], v[248:251], a[176:179], a[112:115]\n\t"
"ds_read_b128 a[168:171], v244 offset:33792\n\t"
"v_cvt_pk_bf16_f32 v227, v234, v235\n\t"
"v_mfma_f32_16x16x32_bf16 a[112:115], a[232:235], a[180:183], a[112:115]\n\t"
"ds_write_b128 v243, v[224:227] offset:46592\n\t"
"v_mfma_f32_16x16x32_bf16 a[120:123], a[232:235], a[184:187], a[120:123]\n\t"
"buffer_load_dwordx4 a[228:231], off, s[80:83], s20 offset:0 sc1\n\t"
"v_mfma_f32_16x16x32_bf16 a[120:123], v[248:251], a[180:183], a[120:123]\n\t"
"ds_read_b64_tr_b16 v[232:233], v246 offset:13312\n\t"
"v_mfma_f32_16x16x32_bf16 a[120:123], a[224:227], a[176:179], a[120:123]\n\t"
"ds_read_b64_tr_b16 v[234:235], v246 offset:16384\n\t"
"v_mfma_f32_16x16x32_bf16 a[128:131], a[224:227], a[180:183], a[128:131]\n\t"
"ds_read_b64_tr_b16 v[224:225], v246 offset:20992\n\t"
"v_mfma_f32_16x16x32_bf16 a[128:131], v[248:251], a[184:187], a[128:131]\n\t"
"ds_read_b64_tr_b16 v[226:227], v246 offset:24064\n\t"
"v_mfma_f32_16x16x32_bf16 a[128:131], a[232:235], a[188:191], a[128:131]\n\t"
"ds_read_b128 a[172:175], v244 offset:34304\n\t"
"v_mfma_f32_16x16x32_bf16 a[136:139], a[224:227], a[184:187], a[136:139]\n\t"
"s_waitcnt lgkmcnt(9)\n\t"
"ds_read_b128 a[176:179], v244 offset:34816\n\t"
"v_mfma_f32_16x16x32_bf16 a[136:139], a[232:235], a[192:195], a[136:139]\n\t"
"s_waitcnt lgkmcnt(8)\n\t"
"ds_read_b128 a[180:183], v244 offset:35328\n\t"
"v_mfma_f32_16x16x32_bf16 a[136:139], v[248:251], a[188:191], a[136:139]\n\t"
"s_waitcnt lgkmcnt(5)\n\t"
"ds_read_b128 a[184:187], v244 offset:35840\n\t"
"v_mfma_f32_16x16x32_bf16 v[236:239], v[236:239], v[220:223], 0\n\t"
"s_waitcnt lgkmcnt(4)\n\t"
"v_mfma_f32_16x16x32_bf16 v[228:231], v[228:231], v[220:223], 0\n\t"
"v_mfma_f32_16x16x32_bf16 v[232:235], v[232:235], v[220:223], 0\n\t"
"v_mfma_f32_16x16x32_bf16 v[224:227], v[224:227], v[220:223], 0\n\t"
"v_mfma_f32_16x16x32_bf16 a[144:147], a[224:227], a[188:191], a[144:147]\n\t"
"ds_read_b128 a[188:191], v244 offset:36352\n\t"
"v_mfma_f32_16x16x32_bf16 a[144:147], a[232:235], a[196:199], a[144:147]\n\t"
"v_cvt_pk_bf16_f32 v236, v236, v237\n\t"
"v_cvt_pk_bf16_f32 v237, v238, v239\n\t"
"v_mfma_f32_16x16x32_bf16 a[144:147], v[248:251], a[192:195], a[144:147]\n\t"
"v_cvt_pk_bf16_f32 v238, v228, v229\n\t"
"v_cvt_pk_bf16_f32 v239, v230, v231\n\t"
"v_mfma_f32_16x16x32_bf16 a[152:155], a[224:227], a[192:195], a[152:155]\n\t"
"ds_read_b64_tr_b16 v[228:229], v246 offset:27136\n\t"
"v_cvt_pk_bf16_f32 v232, v232, v233\n\t"
"v_mfma_f32_16x16x32_bf16 a[152:155], v[248:251], a[196:199], a[152:155]\n\t"
"ds_read_b64_tr_b16 v[230:231], v246 offset:30208\n\t"
"v_cvt_pk_bf16_f32 v233, v234, v235\n\t"
"v_mfma_f32_16x16x32_bf16 a[152:155], a[232:235], a[200:203], a[152:155]\n\t"
"ds_read_b128 a[192:195], v244 offset:36864\n\t"
"v_cvt_pk_bf16_f32 v234, v224, v225\n\t"
"v_mfma_f32_16x16x32_bf16 a[160:163], v[248:251], a[200:203], a[160:163]\n\t"
"s_waitcnt vmcnt(7) lgkmcnt(6)\n\t"
"ds_read_b128 a[200:203], v244 offset:37888\n\t"
"v_cvt_pk_bf16_f32 v235, v226, v227\n\t"
"v_mfma_f32_16x16x32_bf16 a[160:163], a[224:227], a[196:199], a[160:163]\n\t"
"ds_read_b128 a[196:199], v244 offset:37376\n\t"
"v_mfma_f32_16x16x32_bf16 a[160:163], a[232:235], a[204:207], a[160:163]\n\t"
"buffer_load_dwordx4 v[248:251], off, s[80:83], s20 offset:1024 sc1\n\t"
"v_mfma_f32_16x16x32_bf16 v[200:203], a[236:239], a[168:171], v[200:203]\n\t"
"s_waitcnt lgkmcnt(3)\n\t"
"ds_read_b128 a[204:207], v244 offset:38400\n\t"
"v_mfma_f32_16x16x32_bf16 v[200:203], a[216:219], a[176:179], v[200:203]\n\t"
"buffer_load_dwordx4 a[224:227], off, s[80:83], s20 offset:2048 sc1\n\t"
"v_mfma_f32_16x16x32_bf16 v[200:203], a[212:215], a[172:175], v[200:203]\n\t"
"s_mov_b32 s20, 1302528\n\t"
"v_mfma_f32_16x16x32_bf16 v[228:231], v[228:231], v[220:223], 0\n\t"
"buffer_load_dwordx4 a[232:235], off, s[80:83], s20 offset:0 sc1\n\t"
"v_mfma_f32_16x16x32_bf16 v[208:211], a[236:239], a[172:175], v[208:211]\n\t"
"v_mfma_f32_16x16x32_bf16 v[208:211], a[216:219], a[180:183], v[208:211]\n\t"
"v_mfma_f32_16x16x32_bf16 v[208:211], a[212:215], a[176:179], v[208:211]\n\t"
"s_waitcnt lgkmcnt(0)\n\t"
"s_barrier\n\t"
"v_mfma_f32_16x16x32_bf16 v[216:219], a[236:239], a[176:179], v[216:219]\n\t"
"ds_read_b64_tr_b16 v[224:225], v246 offset:33280\n\t"
"v_mfma_f32_16x16x32_bf16 v[216:219], a[212:215], a[180:183], v[216:219]\n\t"
"ds_read_b64_tr_b16 v[226:227], v246 offset:36352\n\t"
"v_cvt_pk_bf16_f32 v228, v228, v229\n\t"
"v_mfma_f32_16x16x32_bf16 v[216:219], a[216:219], a[184:187], v[216:219]\n\t"
"ds_write2st64_b64 v245, v[236:237], v[238:239] offset0:48 offset1:49\n\t"
"v_mfma_f32_16x16x32_bf16 a[4:7], a[216:219], a[188:191], a[4:7]\n\t"
"v_cvt_pk_bf16_f32 v229, v230, v231\n\t"
"v_mfma_f32_16x16x32_bf16 a[4:7], a[212:215], a[184:187], a[4:7]\n\t"
"v_mfma_f32_16x16x32_bf16 a[4:7], a[236:239], a[180:183], a[4:7]\n\t"
"ds_write2st64_b64 v245, v[232:233], v[234:235] offset0:50 offset1:51\n\t"
"v_mfma_f32_16x16x32_bf16 a[12:15], a[236:239], a[184:187], a[12:15]\n\t"
"v_mfma_f32_16x16x32_bf16 a[12:15], a[212:215], a[188:191], a[12:15]\n\t"
"ds_read_b64_tr_b16 v[232:233], v246 offset:40960\n\t"
"v_mfma_f32_16x16x32_bf16 a[12:15], a[216:219], a[192:195], a[12:15]\n\t"
"ds_write_b64 v245, v[228:229] offset:26624\n\t"
"v_mfma_f32_16x16x32_bf16 a[20:23], a[216:219], a[196:199], a[20:23]\n\t"
"s_waitcnt lgkmcnt(4)\n\t"
"ds_read_b64_tr_b16 v[234:235], v246 offset:44032\n\t"
"v_mfma_f32_16x16x32_bf16 a[20:23], a[212:215], a[192:195], a[20:23]\n\t"
"ds_read_b64_tr_b16 v[228:229], v246 offset:47104\n\t"
"v_mfma_f32_16x16x32_bf16 a[20:23], a[236:239], a[188:191], a[20:23]\n\t"
"Lpg20_%=:\n\t"
"s_cbranch_vccnz Lvr2_%=\n\t"
"ds_read_b64_tr_b16 v[230:231], v246 offset:50176\n\t"
"v_mfma_f32_16x16x32_bf16 a[28:31], a[236:239], a[192:195], a[28:31]\n\t"
"v_mfma_f32_16x16x32_bf16 a[28:31], a[212:215], a[196:199], a[28:31]\n\t"
"v_mfma_f32_16x16x32_bf16 a[28:31], a[216:219], a[200:203], a[28:31]\n\t"
"s_waitcnt lgkmcnt(2)\n\t"
"v_mfma_f32_16x16x32_bf16 v[224:227], v[224:227], v[220:223], 0\n\t"
"v_mfma_f32_16x16x32_bf16 v[232:235], v[232:235], v[220:223], 0\n\t"
"v_mfma_f32_16x16x32_bf16 a[36:39], a[212:215], a[200:203], a[36:39]\n\t"
"buffer_load_dwordx4 a[212:215], off, s[80:83], s20 offset:1024 sc1\n\t"
"v_mfma_f32_16x16x32_bf16 a[36:39], a[236:239], a[196:199], a[36:39]\n\t"
"s_waitcnt vmcnt(8) lgkmcnt(0)\n\t"
"v_mfma_f32_16x16x32_bf16 a[36:39], a[216:219], a[204:207], a[36:39]\n\t"
"v_mfma_f32_16x16x32_bf16 v[228:231], v[228:231], v[220:223], 0\n\t"
"buffer_load_dwordx4 a[216:219], off, s[80:83], s20 offset:2048 sc1\n\t"
"v_mfma_f32_16x16x32_bf16 a[44:47], a[240:243], a[168:171], a[44:47]\n\t"
"s_mov_b32 s20, 1314816\n\t"
"v_cvt_pk_bf16_f32 v224, v224, v225\n\t"
"v_cvt_pk_bf16_f32 v225, v226, v227\n\t"
"v_mfma_f32_16x16x32_bf16 a[44:47], a[248:251], a[176:179], a[44:47]\n\t"
"v_cvt_pk_bf16_f32 v226, v232, v233\n\t"
"v_cvt_pk_bf16_f32 v227, v234, v235\n\t"
"v_mfma_f32_16x16x32_bf16 a[44:47], a[244:247], a[172:175], a[44:47]\n\t"
"ds_write2st64_b64 v245, v[224:225], v[226:227] offset0:53 offset1:54\n\t"
"v_mfma_f32_16x16x32_bf16 a[52:55], a[240:243], a[172:175], a[52:55]\n\t"
"buffer_load_dwordx4 a[236:239], off, s[80:83], s20 offset:0 sc1\n\t"
"v_mfma_f32_16x16x32_bf16 a[52:55], a[244:247], a[176:179], a[52:55]\n\t"
"ds_read_b64_tr_b16 v[232:233], v246 offset:53248\n\t"
"v_mfma_f32_16x16x32_bf16 a[52:55], a[248:251], a[180:183], a[52:55]\n\t"
"ds_read_b64_tr_b16 v[234:235], v246 offset:56320\n\t"
"v_cvt_pk_bf16_f32 v228, v228, v229\n\t"
"v_mfma_f32_16x16x32_bf16 a[60:63], a[248:251], a[184:187], a[60:63]\n\t"
"v_cvt_pk_bf16_f32 v229, v230, v231\n\t"
"v_mfma_f32_16x16x32_bf16 a[60:63], a[244:247], a[180:183], a[60:63]\n\t"
"v_mfma_f32_16x16x32_bf16 a[60:63], a[240:243], a[176:179], a[60:63]\n\t"
"v_mfma_f32_16x16x32_bf16 a[68:71], a[240:243], a[180:183], a[68:71]\n\t"
"v_mfma_f32_16x16x32_bf16 a[68:71], a[244:247], a[184:187], a[68:71]\n\t"
"v_add_u32 v243, 55296, v243\n\t"
"v_mfma_f32_16x16x32_bf16 a[68:71], a[248:251], a[188:191], a[68:71]\n\t"
"v_mfma_f32_16x16x32_bf16 a[76:79], a[244:247], a[188:191], a[76:79]\n\t"
"s_waitcnt lgkmcnt(0)\n\t"
"v_mfma_f32_16x16x32_bf16 a[76:79], a[248:251], a[192:195], a[76:79]\n\t"
"v_mfma_f32_16x16x32_bf16 a[76:79], a[240:243], a[184:187], a[76:79]\n\t"
"v_mfma_f32_16x16x32_bf16 v[232:235], v[232:235], v[220:223], 0\n\t"
"v_mfma_f32_16x16x32_bf16 a[84:87], a[240:243], a[188:191], a[84:87]\n\t"
"v_mfma_f32_16x16x32_bf16 a[84:87], a[248:251], a[196:199], a[84:87]\n\t"
"v_mfma_f32_16x16x32_bf16 a[84:87], a[244:247], a[192:195], a[84:87]\n\t"
"v_mfma_f32_16x16x32_bf16 a[92:95], a[240:243], a[192:195], a[92:95]\n\t"
"v_mfma_f32_16x16x32_bf16 a[92:95], a[244:247], a[196:199], a[92:95]\n\t"
"v_cvt_pk_bf16_f32 v230, v232, v233\n\t"
"v_cvt_pk_bf16_f32 v231, v234, v235\n\t"
"v_mfma_f32_16x16x32_bf16 a[92:95], a[248:251], a[200:203], a[92:95]\n\t"
"ds_write2st64_b64 v245, v[228:229], v[230:231] offset0:55 offset1:56\n\t"
"v_mfma_f32_16x16x32_bf16 a[100:103], a[244:247], a[200:203], a[100:103]\n\t"
"buffer_load_dwordx4 a[244:247], off, s[80:83], s20 offset:1024 sc1\n\t"
"v_mfma_f32_16x16x32_bf16 a[100:103], a[240:243], a[196:199], a[100:103]\n\t"
"s_waitcnt vmcnt(8)\n\t"
"v_mfma_f32_16x16x32_bf16 a[100:103], a[248:251], a[204:207], a[100:103]\n\t"
"v_mfma_f32_16x16x32_bf16 a[108:111], a[252:255], a[168:171], a[108:111]\n\t"
"buffer_load_dwordx4 a[240:243], off, s[80:83], s20 offset:2048 sc1\n\t"
"v_mfma_f32_16x16x32_bf16 a[108:111], a[220:223], a[176:179], a[108:111]\n\t"
"s_mov_b32 s20, 1327104\n\t"
"v_mfma_f32_16x16x32_bf16 a[108:111], v[96:99], a[172:175], a[108:111]\n\t"
"s_waitcnt lgkmcnt(0)\n\t"
"s_barrier\n\t"
"v_mfma_f32_16x16x32_bf16 a[116:119], a[252:255], a[172:175], a[116:119]\n\t"
"ds_read_b64_tr_b16 v[236:237], v240 offset:1024\n\t"
"v_mfma_f32_16x16x32_bf16 a[116:119], v[96:99], a[176:179], a[116:119]\n\t"
"buffer_load_dwordx4 a[248:251], off, s[80:83], s20 offset:0 sc1\n\t"
"v_mfma_f32_16x16x32_bf16 a[116:119], a[220:223], a[180:183], a[116:119]\n\t"
"ds_read_b64_tr_b16 v[238:239], v240 offset:2560\n\t"
"v_mfma_f32_16x16x32_bf16 a[124:127], a[220:223], a[184:187], a[124:127]\n\t"
"ds_read_b64_tr_b16 v[224:225], v241 offset:1024\n\t"
"v_mfma_f32_16x16x32_bf16 a[124:127], v[96:99], a[180:183], a[124:127]\n\t"
"ds_read_b64_tr_b16 v[226:227], v241 offset:2560\n\t"
"v_mfma_f32_16x16x32_bf16 a[124:127], a[252:255], a[176:179], a[124:127]\n\t"
"ds_read_b64_tr_b16 v[232:233], v240 offset:20992\n\t"
"v_mfma_f32_16x16x32_bf16 a[132:135], a[252:255], a[180:183], a[132:135]\n\t"
"ds_read_b64_tr_b16 v[234:235], v240 offset:22528\n\t"
"v_mfma_f32_16x16x32_bf16 a[132:135], v[96:99], a[184:187], a[132:135]\n\t"
"ds_read_b64_tr_b16 v[228:229], v241 offset:20992\n\t"
"v_mfma_f32_16x16x32_bf16 a[132:135], a[220:223], a[188:191], a[132:135]\n\t"
"ds_read_b64_tr_b16 v[230:231], v241 offset:22528\n\t"
"v_mfma_f32_16x16x32_bf16 a[140:143], a[252:255], a[184:187], a[140:143]\n\t"
"s_waitcnt lgkmcnt(6)\n\t"
"ds_read_b128 a[168:171], v244 offset:24576\n\t"
"v_mfma_f32_16x16x32_bf16 a[140:143], a[220:223], a[192:195], a[140:143]\n\t"
"s_waitcnt lgkmcnt(5)\n\t"
"ds_read_b128 a[172:175], v244 offset:25088\n\t"
"v_mfma_f32_16x16x32_bf16 a[140:143], v[96:99], a[188:191], a[140:143]\n\t"
"s_waitcnt lgkmcnt(4)\n\t"
"ds_read_b128 a[176:179], v244 offset:25600\n\t"
"v_mfma_f32_16x16x32_bf16 v[236:239], v[236:239], a[208:211], 0\n\t"
"ds_read_b128 a[180:183], v244 offset:26112\n\t"
"v_mfma_f32_16x16x32_bf16 v[224:227], v[224:227], a[208:211], 0\n\t"
"s_waitcnt lgkmcnt(4)\n\t"
"ds_read_b128 a[184:187], v244 offset:26624\n\t"
"v_mfma_f32_16x16x32_bf16 v[232:235], v[232:235], a[208:211], 0\n\t"
"v_mfma_f32_16x16x32_bf16 v[228:231], v[228:231], a[208:211], 0\n\t"
"v_mfma_f32_16x16x32_bf16 a[148:151], a[252:255], a[188:191], a[148:151]\n\t"
"ds_read_b128 a[188:191], v244 offset:27136\n\t"
"v_mfma_f32_16x16x32_bf16 a[148:151], a[220:223], a[196:199], a[148:151]\n\t"
"v_cvt_pk_bf16_f32 v236, v236, v237\n\t"
"v_cvt_pk_bf16_f32 v237, v238, v239\n\t"
"v_mfma_f32_16x16x32_bf16 a[148:151], v[96:99], a[192:195], a[148:151]\n\t"
"v_cvt_pk_bf16_f32 v238, v224, v225\n\t"
"v_cvt_pk_bf16_f32 v239, v226, v227\n\t"
"v_mfma_f32_16x16x32_bf16 a[156:159], a[252:255], a[192:195], a[156:159]\n\t"
"ds_read_b128 a[192:195], v244 offset:27648\n\t"
"v_cvt_pk_bf16_f32 v232, v232, v233\n\t"
"v_mfma_f32_16x16x32_bf16 a[156:159], v[96:99], a[196:199], a[156:159]\n\t"
"v_cvt_pk_bf16_f32 v233, v234, v235\n\t"
"v_cvt_pk_bf16_f32 v234, v228, v229\n\t"
"v_mfma_f32_16x16x32_bf16 a[156:159], a[220:223], a[200:203], a[156:159]\n\t"
"ds_write_b128 v243, v[236:239] offset:28160\n\t"
"v_mfma_f32_16x16x32_bf16 a[164:167], v[96:99], a[200:203], a[164:167]\n\t"
"buffer_load_dwordx4 v[96:99], off, s[80:83], s20 offset:1024 sc1\n\t"
"v_mfma_f32_16x16x32_bf16 a[164:167], a[252:255], a[196:199], a[164:167]\n\t"
"s_waitcnt vmcnt(8) lgkmcnt(5)\n\t"
"ds_read_b64_tr_b16 v[224:225], v240 offset:40960\n\t"
"v_mfma_f32_16x16x32_bf16 a[164:167], a[220:223], a[204:207], a[164:167]\n\t"
"ds_read_b64_tr_b16 v[226:227], v240 offset:42496\n\t"
"v_cvt_pk_bf16_f32 v235, v230, v231\n\t"
"Lst28_%=:\n\t"
"v_mfma_f32_16x16x32_bf16 v[196:199], a[228:231], a[168:171], v[196:199]\n\t"
"s_waitcnt lgkmcnt(6)\n\t"
"ds_read_b64_tr_b16 v[228:229], v241 offset:40960\n\t"
"v_mfma_f32_16x16x32_bf16 v[196:199], a[224:227], a[176:179], v[196:199]\n\t"
"buffer_load_dwordx4 a[220:223], off, s[80:83], s20 offset:2048 sc1\n\t"
"v_mfma_f32_16x16x32_bf16 v[196:199], v[248:251], a[172:175], v[196:199]\n\t"
"s_mov_b32 s20, 1339392\n\t"
"ds_write_b128 v243, v[232:235] offset:37376\n\t"
"v_mfma_f32_16x16x32_bf16 v[204:207], a[228:231], a[172:175], v[204:207]\n\t"
"buffer_load_dwordx4 a[252:255], off, s[80:83], s20 offset:0 sc1\n\t"
"v_mfma_f32_16x16x32_bf16 v[204:207], v[248:251], a[176:179], v[204:207]\n\t"
"s_waitcnt lgkmcnt(7)\n\t"
"ds_read_b64_tr_b16 v[230:231], v241 offset:42496\n\t"
"v_mfma_f32_16x16x32_bf16 v[204:207], a[224:227], a[180:183], v[204:207]\n\t"
"ds_read_b128 a[196:199], v244 offset:28160\n\t"
"v_subrev_u32 v240, 59904, v240\n\t"
"v_subrev_u32 v241, 59904, v241\n\t"
"v_mfma_f32_16x16x32_bf16 v[212:215], v[248:251], a[180:183], v[212:215]\n\t"
"s_waitcnt lgkmcnt(8)\n\t"
"ds_read_b64_tr_b16 v[236:237], v240 offset:1024\n\t"
"v_mfma_f32_16x16x32_bf16 v[212:215], a[228:231], a[176:179], v[212:215]\n\t"
"ds_read_b64_tr_b16 v[238:239], v240 offset:2560\n\t"
"v_mfma_f32_16x16x32_bf16 v[212:215], a[224:227], a[184:187], v[212:215]\n\t"
"ds_read_b64_tr_b16 v[232:233], v241 offset:1024\n\t"
"v_mfma_f32_16x16x32_bf16 a[0:3], a[228:231], a[180:183], a[0:3]\n\t"
"s_waitcnt lgkmcnt(7)\n\t"
"ds_read_b64_tr_b16 v[234:235], v241 offset:2560\n\t"
"v_mfma_f32_16x16x32_bf16 a[0:3], a[224:227], a[188:191], a[0:3]\n\t"
"s_waitcnt lgkmcnt(5)\n\t"
"ds_read_b128 a[200:203], v244 offset:28672\n\t"
"v_mfma_f32_16x16x32_bf16 a[0:3], v[248:251], a[184:187], a[0:3]\n\t"
"ds_read_b128 a[204:207], v244 offset:29184\n\t"
"v_mfma_f32_16x16x32_bf16 v[224:227], v[224:227], a[208:211], 0\n\t"
"s_waitcnt lgkmcnt(4)\n\t"
"v_mfma_f32_16x16x32_bf16 v[228:231], v[228:231], a[208:211], 0\n\t"
"v_mfma_f32_16x16x32_bf16 v[236:239], v[236:239], a[208:211], 0\n\t"
"s_waitcnt lgkmcnt(2)\n\t"
"v_mfma_f32_16x16x32_bf16 v[232:235], v[232:235], a[208:211], 0\n\t"
"v_mfma_f32_16x16x32_bf16 a[8:11], a[228:231], a[184:187], a[8:11]\n\t"
"v_mfma_f32_16x16x32_bf16 a[8:11], a[224:227], a[192:195], a[8:11]\n\t"
"v_cvt_pk_bf16_f32 v224, v224, v225\n\t"
"v_cvt_pk_bf16_f32 v225, v226, v227\n\t"
"v_mfma_f32_16x16x32_bf16 a[8:11], v[248:251], a[188:191], a[8:11]\n\t"
"v_cvt_pk_bf16_f32 v226, v228, v229\n\t"
"v_cvt_pk_bf16_f32 v227, v230, v231\n\t"
"v_mfma_f32_16x16x32_bf16 a[16:19], a[228:231], a[188:191], a[16:19]\n\t"
"s_waitcnt lgkmcnt(1)\n\t"
"v_cvt_pk_bf16_f32 v236, v236, v237\n\t"
"v_cvt_pk_bf16_f32 v237, v238, v239\n\t"
"v_mfma_f32_16x16x32_bf16 a[16:19], v[248:251], a[192:195], a[16:19]\n\t"
"v_cvt_pk_bf16_f32 v238, v232, v233\n\t"
"v_cvt_pk_bf16_f32 v239, v234, v235\n\t"
"v_mfma_f32_16x16x32_bf16 a[16:19], a[224:227], a[196:199], a[16:19]\n\t"
"ds_write_b128 v243, v[224:227] offset:46592\n\t"
"v_mfma_f32_16x16x32_bf16 a[24:27], a[224:227], a[200:203], a[24:27]\n\t"
"s_waitcnt lgkmcnt(1)\n\t"
"v_mfma_f32_16x16x32_bf16 a[24:27], v[248:251], a[196:199], a[24:27]\n\t"
"ds_read_b64_tr_b16 v[228:229], v240 offset:20992\n\t"
"v_mfma_f32_16x16x32_bf16 a[24:27], a[228:231], a[192:195], a[24:27]\n\t"
"ds_write_b128 v243, v[236:239] offset:512\n\t"
"v_mfma_f32_16x16x32_bf16 a[32:35], a[228:231], a[196:199], a[32:35]\n\t"
"buffer_load_dwordx4 a[228:231], off, s[80:83], s20 offset:1024 sc1\n\t"
"v_mfma_f32_16x16x32_bf16 a[32:35], v[248:251], a[200:203], a[32:35]\n\t"
"s_waitcnt vmcnt(8)\n\t"
"ds_read_b64_tr_b16 v[230:231], v240 offset:22528\n\t"
"v_mfma_f32_16x16x32_bf16 a[32:35], a[224:227], a[204:207], a[32:35]\n\t"
"ds_read_b64_tr_b16 v[232:233], v241 offset:20992\n\t"
"v_mfma_f32_16x16x32_bf16 a[40:43], a[232:235], a[168:171], a[40:43]\n\t"
"ds_read_b64_tr_b16 v[234:235], v241 offset:22528\n\t"
"v_mfma_f32_16x16x32_bf16 a[40:43], a[216:219], a[176:179], a[40:43]\n\t"
"ds_read_b64_tr_b16 v[224:225], v240 offset:40960\n\t"
"v_mfma_f32_16x16x32_bf16 a[40:43], a[212:215], a[172:175], a[40:43]\n\t"
"ds_read_b64_tr_b16 v[226:227], v240 offset:42496\n\t"
"v_mfma_f32_16x16x32_bf16 a[48:51], a[232:235], a[172:175], a[48:51]\n\t"
"buffer_load_dwordx4 v[248:251], off, s[80:83], s20 offset:2048 sc1\n\t"
"v_mfma_f32_16x16x32_bf16 a[48:51], a[212:215], a[176:179], a[48:51]\n\t"
"s_mov_b32 s20, 1351680\n\t"
"ds_read_b64_tr_b16 v[236:237], v241 offset:40960\n\t"
"v_mfma_f32_16x16x32_bf16 a[48:51], a[216:219], a[180:183], a[48:51]\n\t"
"ds_read_b64_tr_b16 v[238:239], v241 offset:42496\n\t"
"v_mfma_f32_16x16x32_bf16 a[56:59], a[232:235], a[176:179], a[56:59]\n\t"
"buffer_load_dwordx4 a[224:227], off, s[80:83], s20 offset:0 sc1\n\t"
"v_mfma_f32_16x16x32_bf16 a[56:59], a[216:219], a[184:187], a[56:59]\n\t"
"s_waitcnt lgkmcnt(4)\n\t"
"v_mfma_f32_16x16x32_bf16 a[56:59], a[212:215], a[180:183], a[56:59]\n\t"
"s_waitcnt lgkmcnt(2)\n\t"
"v_mfma_f32_16x16x32_bf16 v[228:231], v[228:231], a[208:211], 0\n\t"
"v_mfma_f32_16x16x32_bf16 v[232:235], v[232:235], a[208:211], 0\n\t"
"s_waitcnt lgkmcnt(0)\n\t"
"v_xor_b32 v246, 0xc0, v246\n\t"
"v_mfma_f32_16x16x32_bf16 v[224:227], v[224:227], a[208:211], 0\n\t"
"v_mfma_f32_16x16x32_bf16 v[236:239], v[236:239], a[208:211], 0\n\t"
"v_mfma_f32_16x16x32_bf16 a[64:67], a[232:235], a[180:183], a[64:67]\n\t"
"v_mfma_f32_16x16x32_bf16 a[64:67], a[216:219], a[188:191], a[64:67]\n\t"
"v_cvt_pk_bf16_f32 v228, v228, v229\n\t"
"v_cvt_pk_bf16_f32 v229, v230, v231\n\t"
"v_mfma_f32_16x16x32_bf16 a[64:67], a[212:215], a[184:187], a[64:67]\n\t"
"v_cvt_pk_bf16_f32 v230, v232, v233\n\t"
"v_cvt_pk_bf16_f32 v231, v234, v235\n\t"
"v_mfma_f32_16x16x32_bf16 a[72:75], a[232:235], a[184:187], a[72:75]\n\t"
"v_cvt_pk_bf16_f32 v224, v224, v225\n\t"
"v_cvt_pk_bf16_f32 v225, v226, v227\n\t"
"v_mfma_f32_16x16x32_bf16 a[72:75], a[212:215], a[188:191], a[72:75]\n\t"
"v_cvt_pk_bf16_f32 v226, v236, v237\n\t"
"v_cvt_pk_bf16_f32 v227, v238, v239\n\t"
"v_mfma_f32_16x16x32_bf16 a[72:75], a[216:219], a[192:195], a[72:75]\n\t"
"ds_write_b128 v243, v[228:231] offset:9728\n\t"
"v_mfma_f32_16x16x32_bf16 a[80:83], a[216:219], a[196:199], a[80:83]\n\t"
"v_mfma_f32_16x16x32_bf16 a[80:83], a[212:215], a[192:195], a[80:83]\n\t"
"v_mfma_f32_16x16x32_bf16 a[80:83], a[232:235], a[188:191], a[80:83]\n\t"
"ds_write_b128 v243, v[224:227] offset:18944\n\t"
"v_mfma_f32_16x16x32_bf16 a[88:91], a[232:235], a[192:195], a[88:91]\n\t"
"v_mfma_f32_16x16x32_bf16 a[88:91], a[212:215], a[196:199], a[88:91]\n\t"
"v_mfma_f32_16x16x32_bf16 a[88:91], a[216:219], a[200:203], a[88:91]\n\t"
"ds_read_b64_tr_b16 v[232:233], v246 offset:1024\n\t"
"v_mfma_f32_16x16x32_bf16 a[96:99], a[212:215], a[200:203], a[96:99]\n\t"
"buffer_load_dwordx4 a[212:215], off, s[80:83], s20 offset:1024 sc1\n\t"
"v_mfma_f32_16x16x32_bf16 a[96:99], a[232:235], a[196:199], a[96:99]\n\t"
"s_waitcnt vmcnt(8)\n\t"
"ds_read_b64_tr_b16 v[234:235], v246 offset:4096\n\t"
"v_mfma_f32_16x16x32_bf16 a[96:99], a[216:219], a[204:207], a[96:99]\n\t"
"ds_read_b64_tr_b16 v[236:237], v246 offset:7168\n\t"
"v_mfma_f32_16x16x32_bf16 a[104:107], a[236:239], a[168:171], a[104:107]\n\t"
"buffer_load_dwordx4 a[216:219], off, s[80:83], s20 offset:2048 sc1\n\t"
"v_mfma_f32_16x16x32_bf16 a[104:107], a[240:243], a[176:179], a[104:107]\n\t"
"s_mov_b32 s20, 1216512\n\t"
"ds_read_b64_tr_b16 v[238:239], v246 offset:10240\n\t"
"v_mfma_f32_16x16x32_bf16 a[104:107], a[244:247], a[172:175], a[104:107]\n\t"
"ds_read_b64_tr_b16 v[228:229], v246 offset:13312\n\t"
"v_mfma_f32_16x16x32_bf16 a[112:115], a[236:239], a[172:175], a[112:115]\n\t"
"ds_read_b64_tr_b16 v[230:231], v246 offset:16384\n\t"
"v_mfma_f32_16x16x32_bf16 a[112:115], a[244:247], a[176:179], a[112:115]\n\t"
"buffer_load_dwordx4 a[232:235], off, s[80:83], s20 offset:0 sc1\n\t"
"v_mfma_f32_16x16x32_bf16 a[112:115], a[240:243], a[180:183], a[112:115]\n\t"
"ds_read_b64_tr_b16 v[224:225], v246 offset:20992\n\t"
"v_mfma_f32_16x16x32_bf16 a[120:123], a[244:247], a[180:183], a[120:123]\n\t"
"ds_read_b64_tr_b16 v[226:227], v246 offset:24064\n\t"
"v_mfma_f32_16x16x32_bf16 a[120:123], a[236:239], a[176:179], a[120:123]\n\t"
"ds_read_b128 a[168:171], v244 offset:33792\n\t"
"v_mfma_f32_16x16x32_bf16 a[120:123], a[240:243], a[184:187], a[120:123]\n\t"
"ds_read_b128 a[172:175], v244 offset:34304\n\t"
"v_mfma_f32_16x16x32_bf16 a[128:131], a[236:239], a[180:183], a[128:131]\n\t"
"s_waitcnt lgkmcnt(8)\n\t"
"ds_read_b128 a[176:179], v244 offset:34816\n\t"
"v_mfma_f32_16x16x32_bf16 a[128:131], a[240:243], a[188:191], a[128:131]\n\t"
"s_waitcnt lgkmcnt(7)\n\t"
"ds_read_b128 a[180:183], v244 offset:35328\n\t"
"v_mfma_f32_16x16x32_bf16 a[128:131], a[244:247], a[184:187], a[128:131]\n\t"
"s_waitcnt lgkmcnt(6)\n\t"
"v_mfma_f32_16x16x32_bf16 v[232:235], v[232:235], v[220:223], 0\n\t"
"s_waitcnt lgkmcnt(4)\n\t"
"v_mfma_f32_16x16x32_bf16 v[236:239], v[236:239], v[220:223], 0\n\t"
"v_mfma_f32_16x16x32_bf16 v[228:231], v[228:231], v[220:223], 0\n\t"
"v_mfma_f32_16x16x32_bf16 v[224:227], v[224:227], v[220:223], 0\n\t"
"v_mfma_f32_16x16x32_bf16 a[136:139], a[236:239], a[184:187], a[136:139]\n\t"
"ds_read_b128 a[184:187], v244 offset:35840\n\t"
"v_mfma_f32_16x16x32_bf16 a[136:139], a[240:243], a[192:195], a[136:139]\n\t"
"v_cvt_pk_bf16_f32 v232, v232, v233\n\t"
"v_cvt_pk_bf16_f32 v233, v234, v235\n\t"
"v_mfma_f32_16x16x32_bf16 a[136:139], a[244:247], a[188:191], a[136:139]\n\t"
"v_cvt_pk_bf16_f32 v234, v236, v237\n\t"
"v_cvt_pk_bf16_f32 v235, v238, v239\n\t"
"v_mfma_f32_16x16x32_bf16 a[144:147], a[236:239], a[188:191], a[144:147]\n\t"
"ds_read_b128 a[188:191], v244 offset:36352\n\t"
"v_cvt_pk_bf16_f32 v228, v228, v229\n\t"
"v_mfma_f32_16x16x32_bf16 a[144:147], a[244:247], a[192:195], a[144:147]\n\t"
"v_cvt_pk_bf16_f32 v229, v230, v231\n\t"
"v_cvt_pk_bf16_f32 v230, v224, v225\n\t"
"v_mfma_f32_16x16x32_bf16 a[144:147], a[240:243], a[196:199], a[144:147]\n\t"
"v_cvt_pk_bf16_f32 v231, v226, v227\n\t"
"v_mfma_f32_16x16x32_bf16 a[152:155], a[240:243], a[200:203], a[152:155]\n\t"
"ds_read_b64_tr_b16 v[224:225], v246 offset:27136\n\t"
"v_mfma_f32_16x16x32_bf16 a[152:155], a[244:247], a[196:199], a[152:155]\n\t"
"ds_read_b64_tr_b16 v[226:227], v246 offset:30208\n\t"
"v_mfma_f32_16x16x32_bf16 a[152:155], a[236:239], a[192:195], a[152:155]\n\t"
"ds_read_b128 a[192:195], v244 offset:36864\n\t"
"v_mfma_f32_16x16x32_bf16 a[160:163], a[236:239], a[196:199], a[160:163]\n\t"
"buffer_load_dwordx4 a[236:239], off, s[80:83], s20 offset:1024 sc1\n\t"
"v_mfma_f32_16x16x32_bf16 a[160:163], a[244:247], a[200:203], a[160:163]\n\t"
"s_waitcnt vmcnt(8) lgkmcnt(6)\n\t"
"ds_read_b128 a[196:199], v244 offset:37376\n\t"
"v_mfma_f32_16x16x32_bf16 a[160:163], a[240:243], a[204:207], a[160:163]\n\t"
"s_waitcnt lgkmcnt(2)\n\t"
"ds_read_b128 a[200:203], v244 offset:37888\n\t"
"v_mfma_f32_16x16x32_bf16 v[224:227], v[224:227], v[220:223], 0\n\t"
"ds_read_b128 a[204:207], v244 offset:38400\n\t"
"v_mfma_f32_16x16x32_bf16 v[200:203], a[248:251], a[168:171], v[200:203]\n\t"
"buffer_load_dwordx4 a[240:243], off, s[80:83], s20 offset:2048 sc1\n\t"
"v_mfma_f32_16x16x32_bf16 v[200:203], a[220:223], a[176:179], v[200:203]\n\t"
"s_mov_b32 s20, 1228800\n\t"
"v_mfma_f32_16x16x32_bf16 v[200:203], v[96:99], a[172:175], v[200:203]\n\t"
"buffer_load_dwordx4 a[244:247], off, s[80:83], s20 offset:0 sc1\n\t"
"v_mfma_f32_16x16x32_bf16 v[208:211], a[248:251], a[172:175], v[208:211]\n\t"
"v_mfma_f32_16x16x32_bf16 v[208:211], v[96:99], a[176:179], v[208:211]\n\t"
"v_cvt_pk_bf16_f32 v224, v224, v225\n\t"
"v_mfma_f32_16x16x32_bf16 v[208:211], a[220:223], a[180:183], v[208:211]\n\t"
"s_waitcnt lgkmcnt(0)\n\t"
"s_barrier\n\t"
"v_mfma_f32_16x16x32_bf16 v[216:219], a[220:223], a[184:187], v[216:219]\n\t"
"v_cvt_pk_bf16_f32 v225, v226, v227\n\t"
"v_mfma_f32_16x16x32_bf16 v[216:219], v[96:99], a[180:183], v[216:219]\n\t"
"v_mfma_f32_16x16x32_bf16 v[216:219], a[248:251], a[176:179], v[216:219]\n\t"
"ds_write2st64_b64 v245, v[232:233], v[234:235] offset0:48 offset1:49\n\t"
"v_mfma_f32_16x16x32_bf16 a[4:7], a[248:251], a[180:183], a[4:7]\n\t"
"v_mfma_f32_16x16x32_bf16 a[4:7], v[96:99], a[184:187], a[4:7]\n\t"
"ds_read_b64_tr_b16 v[232:233], v246 offset:33280\n\t"
"v_mfma_f32_16x16x32_bf16 a[4:7], a[220:223], a[188:191], a[4:7]\n\t"
"ds_write2st64_b64 v245, v[228:229], v[230:231] offset0:50 offset1:51\n\t"
"v_mfma_f32_16x16x32_bf16 a[12:15], a[220:223], a[192:195], a[12:15]\n\t"
"v_mfma_f32_16x16x32_bf16 a[12:15], v[96:99], a[188:191], a[12:15]\n\t"
"ds_read_b64_tr_b16 v[234:235], v246 offset:36352\n\t"
"v_mfma_f32_16x16x32_bf16 a[12:15], a[248:251], a[184:187], a[12:15]\n\t"
"ds_write_b64 v245, v[224:225] offset:26624\n\t"
"v_mfma_f32_16x16x32_bf16 a[20:23], a[248:251], a[188:191], a[20:23]\n\t"
"ds_read_b64_tr_b16 v[228:229], v246 offset:40960\n\t"
"v_mfma_f32_16x16x32_bf16 a[20:23], v[96:99], a[192:195], a[20:23]\n\t"
"ds_read_b64_tr_b16 v[230:231], v246 offset:44032\n\t"
"v_mfma_f32_16x16x32_bf16 a[20:23], a[220:223], a[196:199], a[20:23]\n\t"
"ds_read_b64_tr_b16 v[224:225], v246 offset:47104\n\t"
"v_mfma_f32_16x16x32_bf16 a[28:31], v[96:99], a[196:199], a[28:31]\n\t"
"s_waitcnt lgkmcnt(4)\n\t"
"ds_read_b64_tr_b16 v[226:227], v246 offset:50176\n\t"
"v_mfma_f32_16x16x32_bf16 a[28:31], a[248:251], a[192:195], a[28:31]\n\t"
"v_mfma_f32_16x16x32_bf16 a[28:31], a[220:223], a[200:203], a[28:31]\n\t"
"v_mfma_f32_16x16x32_bf16 v[232:235], v[232:235], v[220:223], 0\n\t"
"s_waitcnt lgkmcnt(2)\n\t"
"v_mfma_f32_16x16x32_bf16 v[228:231], v[228:231], v[220:223], 0\n\t"
"v_mfma_f32_16x16x32_bf16 a[36:39], v[96:99], a[200:203], a[36:39]\n\t"
"buffer_load_dwordx4 v[96:99], off, s[80:83], s20 offset:1024 sc1\n\t"
"v_mfma_f32_16x16x32_bf16 a[36:39], a[248:251], a[196:199], a[36:39]\n\t"
"s_waitcnt vmcnt(8) lgkmcnt(0)\n\t"
"v_mfma_f32_16x16x32_bf16 a[36:39], a[220:223], a[204:207], a[36:39]\n\t"
"v_mfma_f32_16x16x32_bf16 v[224:227], v[224:227], v[220:223], 0\n\t"
"buffer_load_dwordx4 a[220:223], off, s[80:83], s20 offset:2048 sc1\n\t"
"v_mfma_f32_16x16x32_bf16 a[44:47], a[252:255], a[168:171], a[44:47]\n\t"
"s_mov_b32 s20, 1241088\n\t"
"v_cvt_pk_bf16_f32 v232, v232, v233\n\t"
"v_cvt_pk_bf16_f32 v233, v234, v235\n\t"
"v_mfma_f32_16x16x32_bf16 a[44:47], v[248:251], a[176:179], a[44:47]\n\t"
"v_cvt_pk_bf16_f32 v234, v228, v229\n\t"
"v_cvt_pk_bf16_f32 v235, v230, v231\n\t"
"v_mfma_f32_16x16x32_bf16 a[44:47], a[228:231], a[172:175], a[44:47]\n\t"
"ds_write2st64_b64 v245, v[232:233], v[234:235] offset0:53 offset1:54\n\t"
"v_mfma_f32_16x16x32_bf16 a[52:55], a[252:255], a[172:175], a[52:55]\n\t"
"buffer_load_dwordx4 a[248:251], off, s[80:83], s20 offset:0 sc1\n\t"
"v_mfma_f32_16x16x32_bf16 a[52:55], a[228:231], a[176:179], a[52:55]\n\t"
"ds_read_b64_tr_b16 v[228:229], v246 offset:53248\n\t"
"v_mfma_f32_16x16x32_bf16 a[52:55], v[248:251], a[180:183], a[52:55]\n\t"
"ds_read_b64_tr_b16 v[230:231], v246 offset:56320\n\t"
"v_cvt_pk_bf16_f32 v224, v224, v225\n\t"
"v_mfma_f32_16x16x32_bf16 a[60:63], v[248:251], a[184:187], a[60:63]\n\t"
"v_cvt_pk_bf16_f32 v225, v226, v227\n\t"
"v_mfma_f32_16x16x32_bf16 a[60:63], a[228:231], a[180:183], a[60:63]\n\t"
"v_mfma_f32_16x16x32_bf16 a[60:63], a[252:255], a[176:179], a[60:63]\n\t"
"v_mfma_f32_16x16x32_bf16 a[68:71], a[252:255], a[180:183], a[68:71]\n\t"
"v_mfma_f32_16x16x32_bf16 a[68:71], a[228:231], a[184:187], a[68:71]\n\t"
"v_subrev_u32 v243, 55296, v243\n\t"
"v_mfma_f32_16x16x32_bf16 a[68:71], v[248:251], a[188:191], a[68:71]\n\t"
"v_mfma_f32_16x16x32_bf16 a[76:79], a[228:231], a[188:191], a[76:79]\n\t"
"s_waitcnt lgkmcnt(0)\n\t"
"v_mfma_f32_16x16x32_bf16 a[76:79], v[248:251], a[192:195], a[76:79]\n\t"
"v_mfma_f32_16x16x32_bf16 a[76:79], a[252:255], a[184:187], a[76:79]\n\t"
"v_mfma_f32_16x16x32_bf16 v[228:231], v[228:231], v[220:223], 0\n\t"
"v_mfma_f32_16x16x32_bf16 a[84:87], a[252:255], a[188:191], a[84:87]\n\t"
"v_mfma_f32_16x16x32_bf16 a[84:87], v[248:251], a[196:199], a[84:87]\n\t"
"v_mfma_f32_16x16x32_bf16 a[84:87], a[228:231], a[192:195], a[84:87]\n\t"
"v_mfma_f32_16x16x32_bf16 a[92:95], a[252:255], a[192:195], a[92:95]\n\t"
"v_mfma_f32_16x16x32_bf16 a[92:95], a[228:231], a[196:199], a[92:95]\n\t"
"v_cvt_pk_bf16_f32 v226, v228, v229\n\t"
"v_cvt_pk_bf16_f32 v227, v230, v231\n\t"
"v_mfma_f32_16x16x32_bf16 a[92:95], v[248:251], a[200:203], a[92:95]\n\t"
"ds_write2st64_b64 v245, v[224:225], v[226:227] offset0:55 offset1:56\n\t"
"v_mfma_f32_16x16x32_bf16 a[100:103], a[228:231], a[200:203], a[100:103]\n\t"
"buffer_load_dwordx4 a[228:231], off, s[80:83], s20 offset:1024 sc1\n\t"
"v_mfma_f32_16x16x32_bf16 a[100:103], a[252:255], a[196:199], a[100:103]\n\t"
"s_waitcnt vmcnt(8)\n\t"
"v_mfma_f32_16x16x32_bf16 a[100:103], v[248:251], a[204:207], a[100:103]\n\t"
"ds_read_b64_tr_b16 v[236:237], v240 offset:1536\n\t"
"v_mfma_f32_16x16x32_bf16 a[108:111], a[224:227], a[168:171], a[108:111]\n\t"
"buffer_load_dwordx4 v[248:251], off, s[80:83], s20 offset:2048 sc1\n\t"
"v_mfma_f32_16x16x32_bf16 a[108:111], a[216:219], a[176:179], a[108:111]\n\t"
"s_mov_b32 s20, 1253376\n\t"
"ds_read_b64_tr_b16 v[238:239], v240 offset:3072\n\t"
"v_mfma_f32_16x16x32_bf16 a[108:111], a[212:215], a[172:175], a[108:111]\n\t"
"s_waitcnt lgkmcnt(2)\n\t"
"s_barrier\n\t"
"v_mfma_f32_16x16x32_bf16 a[116:119], a[224:227], a[172:175], a[116:119]\n\t"
"ds_read_b64_tr_b16 v[232:233], v241 offset:1536\n\t"
"v_mfma_f32_16x16x32_bf16 a[116:119], a[212:215], a[176:179], a[116:119]\n\t"
"buffer_load_dwordx4 a[252:255], off, s[80:83], s20 offset:0 sc1\n\t"
"v_mfma_f32_16x16x32_bf16 a[116:119], a[216:219], a[180:183], a[116:119]\n\t"
"ds_read_b64_tr_b16 v[234:235], v241 offset:3072\n\t"
"v_mfma_f32_16x16x32_bf16 a[124:127], a[216:219], a[184:187], a[124:127]\n\t"
"ds_read_b64_tr_b16 v[228:229], v240 offset:21504\n\t"
"v_mfma_f32_16x16x32_bf16 a[124:127], a[212:215], a[180:183], a[124:127]\n\t"
"ds_read_b64_tr_b16 v[230:231], v240 offset:23040\n\t"
"v_mfma_f32_16x16x32_bf16 a[124:127], a[224:227], a[176:179], a[124:127]\n\t"
"ds_read_b64_tr_b16 v[224:225], v241 offset:21504\n\t"
"v_mfma_f32_16x16x32_bf16 a[132:135], a[224:227], a[180:183], a[132:135]\n\t"
"ds_read_b64_tr_b16 v[226:227], v241 offset:23040\n\t"
"v_mfma_f32_16x16x32_bf16 a[132:135], a[212:215], a[184:187], a[132:135]\n\t"
"ds_read_b128 a[168:171], v244 offset:24576\n\t"
"v_mfma_f32_16x16x32_bf16 a[132:135], a[216:219], a[188:191], a[132:135]\n\t"
"ds_read_b128 a[172:175], v244 offset:25088\n\t"
"v_mfma_f32_16x16x32_bf16 a[140:143], a[224:227], a[184:187], a[140:143]\n\t"
"s_waitcnt lgkmcnt(8)\n\t"
"ds_read_b128 a[176:179], v244 offset:25600\n\t"
"v_mfma_f32_16x16x32_bf16 a[140:143], a[216:219], a[192:195], a[140:143]\n\t"
"s_waitcnt lgkmcnt(7)\n\t"
"ds_read_b128 a[180:183], v244 offset:26112\n\t"
"v_mfma_f32_16x16x32_bf16 a[140:143], a[212:215], a[188:191], a[140:143]\n\t"
"s_waitcnt lgkmcnt(6)\n\t"
"ds_read_b128 a[184:187], v244 offset:26624\n\t"
"v_mfma_f32_16x16x32_bf16 v[236:239], v[236:239], a[208:211], 0\n\t"
"s_waitcnt lgkmcnt(5)\n\t"
"v_mfma_f32_16x16x32_bf16 v[232:235], v[232:235], a[208:211], 0\n\t"
"v_mfma_f32_16x16x32_bf16 v[228:231], v[228:231], a[208:211], 0\n\t"
"v_mfma_f32_16x16x32_bf16 v[224:227], v[224:227], a[208:211], 0\n\t"
"v_mfma_f32_16x16x32_bf16 a[148:151], a[224:227], a[188:191], a[148:151]\n\t"
"ds_read_b128 a[188:191], v244 offset:27136\n\t"
"v_mfma_f32_16x16x32_bf16 a[148:151], a[216:219], a[196:199], a[148:151]\n\t"
"v_cvt_pk_bf16_f32 v236, v236, v237\n\t"
"v_cvt_pk_bf16_f32 v237, v238, v239\n\t"
"v_mfma_f32_16x16x32_bf16 a[148:151], a[212:215], a[192:195], a[148:151]\n\t"
"v_cvt_pk_bf16_f32 v238, v232, v233\n\t"
"v_cvt_pk_bf16_f32 v239, v234, v235\n\t"
"v_mfma_f32_16x16x32_bf16 a[156:159], a[224:227], a[192:195], a[156:159]\n\t"
"ds_read_b128 a[192:195], v244 offset:27648\n\t"
"v_cvt_pk_bf16_f32 v228, v228, v229\n\t"
"v_mfma_f32_16x16x32_bf16 a[156:159], a[212:215], a[196:199], a[156:159]\n\t"
"v_cvt_pk_bf16_f32 v229, v230, v231\n\t"
"v_cvt_pk_bf16_f32 v230, v224, v225\n\t"
"v_mfma_f32_16x16x32_bf16 a[156:159], a[216:219], a[200:203], a[156:159]\n\t"
"ds_write_b128 v243, v[236:239] offset:1024\n\t"
"v_mfma_f32_16x16x32_bf16 a[164:167], a[212:215], a[200:203], a[164:167]\n\t"
"buffer_load_dwordx4 a[212:215], off, s[80:83], s20 offset:1024 sc1\n\t"
"v_mfma_f32_16x16x32_bf16 a[164:167], a[224:227], a[196:199], a[164:167]\n\t"
"s_waitcnt vmcnt(8) lgkmcnt(5)\n\t"
"ds_read_b64_tr_b16 v[232:233], v240 offset:41472\n\t"
"v_mfma_f32_16x16x32_bf16 a[164:167], a[216:219], a[204:207], a[164:167]\n\t"
"ds_read_b64_tr_b16 v[234:235], v240 offset:43008\n\t"
"v_cvt_pk_bf16_f32 v231, v226, v227\n\t"
"Lst29_%=:\n\t"
"v_mfma_f32_16x16x32_bf16 v[0:3], a[232:235], a[168:171], v[0:3]\n\t"
"s_waitcnt lgkmcnt(6)\n\t"
"ds_read_b64_tr_b16 v[224:225], v241 offset:41472\n\t"
"v_mfma_f32_16x16x32_bf16 v[0:3], a[240:243], a[176:179], v[0:3]\n\t"
"buffer_load_dwordx4 a[216:219], off, s[80:83], s20 offset:2048 sc1\n\t"
"v_mfma_f32_16x16x32_bf16 v[0:3], a[236:239], a[172:175], v[0:3]\n\t"
"s_mov_b32 s20, 1265664\n\t"
"ds_write_b128 v243, v[228:231] offset:10240\n\t"
"v_mfma_f32_16x16x32_bf16 v[4:7], a[232:235], a[172:175], v[4:7]\n\t"
"buffer_load_dwordx4 a[224:227], off, s[80:83], s20 offset:0 sc1\n\t"
"v_mfma_f32_16x16x32_bf16 v[4:7], a[236:239], a[176:179], v[4:7]\n\t"
"s_waitcnt lgkmcnt(7)\n\t"
"ds_read_b64_tr_b16 v[226:227], v241 offset:43008\n\t"
"v_mfma_f32_16x16x32_bf16 v[4:7], a[240:243], a[180:183], v[4:7]\n\t"
"ds_read_b128 a[196:199], v244 offset:28160\n\t"
"v_add_u32 v240, 59904, v240\n\t"
"v_add_u32 v241, 59904, v241\n\t"
"v_mfma_f32_16x16x32_bf16 v[8:11], a[236:239], a[180:183], v[8:11]\n\t"
"s_waitcnt lgkmcnt(8)\n\t"
"ds_read_b64_tr_b16 v[236:237], v240 offset:1536\n\t"
"v_mfma_f32_16x16x32_bf16 v[8:11], a[232:235], a[176:179], v[8:11]\n\t"
"ds_read_b64_tr_b16 v[238:239], v240 offset:3072\n\t"
"v_mfma_f32_16x16x32_bf16 v[8:11], a[240:243], a[184:187], v[8:11]\n\t"
"ds_read_b64_tr_b16 v[228:229], v241 offset:1536\n\t"
"v_mfma_f32_16x16x32_bf16 v[12:15], a[232:235], a[180:183], v[12:15]\n\t"
"s_waitcnt lgkmcnt(7)\n\t"
"ds_read_b64_tr_b16 v[230:231], v241 offset:3072\n\t"
"v_mfma_f32_16x16x32_bf16 v[12:15], a[240:243], a[188:191], v[12:15]\n\t"
"s_waitcnt lgkmcnt(5)\n\t"
"ds_read_b128 a[200:203], v244 offset:28672\n\t"
"v_mfma_f32_16x16x32_bf16 v[12:15], a[236:239], a[184:187], v[12:15]\n\t"
"ds_read_b128 a[204:207], v244 offset:29184\n\t"
"v_mfma_f32_16x16x32_bf16 v[232:235], v[232:235], a[208:211], 0\n\t"
"s_waitcnt lgkmcnt(4)\n\t"
"v_mfma_f32_16x16x32_bf16 v[224:227], v[224:227], a[208:211], 0\n\t"
"v_mfma_f32_16x16x32_bf16 v[236:239], v[236:239], a[208:211], 0\n\t"
"s_waitcnt lgkmcnt(2)\n\t"
"v_mfma_f32_16x16x32_bf16 v[228:231], v[228:231], a[208:211], 0\n\t"
"v_mfma_f32_16x16x32_bf16 v[16:19], a[232:235], a[184:187], v[16:19]\n\t"
"v_mfma_f32_16x16x32_bf16 v[16:19], a[240:243], a[192:195], v[16:19]\n\t"
"v_cvt_pk_bf16_f32 v232, v232, v233\n\t"
"v_cvt_pk_bf16_f32 v233, v234, v235\n\t"
"v_mfma_f32_16x16x32_bf16 v[16:19], a[236:239], a[188:191], v[16:19]\n\t"
"v_cvt_pk_bf16_f32 v234, v224, v225\n\t"
"v_cvt_pk_bf16_f32 v235, v226, v227\n\t"
"v_mfma_f32_16x16x32_bf16 v[20:23], a[232:235], a[188:191], v[20:23]\n\t"
"s_waitcnt lgkmcnt(1)\n\t"
"v_cvt_pk_bf16_f32 v236, v236, v237\n\t"
"v_cvt_pk_bf16_f32 v237, v238, v239\n\t"
"v_mfma_f32_16x16x32_bf16 v[20:23], a[236:239], a[192:195], v[20:23]\n\t"
"v_cvt_pk_bf16_f32 v238, v228, v229\n\t"
"v_cvt_pk_bf16_f32 v239, v230, v231\n\t"
"v_mfma_f32_16x16x32_bf16 v[20:23], a[240:243], a[196:199], v[20:23]\n\t"
"ds_write_b128 v243, v[232:235] offset:19456\n\t"
"v_mfma_f32_16x16x32_bf16 v[24:27], a[240:243], a[200:203], v[24:27]\n\t"
"s_waitcnt lgkmcnt(1)\n\t"
"v_mfma_f32_16x16x32_bf16 v[24:27], a[236:239], a[196:199], v[24:27]\n\t"
"ds_read_b64_tr_b16 v[224:225], v240 offset:21504\n\t"
"v_mfma_f32_16x16x32_bf16 v[24:27], a[232:235], a[192:195], v[24:27]\n\t"
"ds_write_b128 v243, v[236:239] offset:28672\n\t"
"v_mfma_f32_16x16x32_bf16 v[28:31], a[232:235], a[196:199], v[28:31]\n\t"
"buffer_load_dwordx4 a[232:235], off, s[80:83], s20 offset:1024 sc1\n\t"
"v_mfma_f32_16x16x32_bf16 v[28:31], a[236:239], a[200:203], v[28:31]\n\t"
"s_waitcnt vmcnt(8)\n\t"
"ds_read_b64_tr_b16 v[226:227], v240 offset:23040\n\t"
"v_mfma_f32_16x16x32_bf16 v[28:31], a[240:243], a[204:207], v[28:31]\n\t"
"ds_read_b64_tr_b16 v[228:229], v241 offset:21504\n\t"
"v_mfma_f32_16x16x32_bf16 v[32:35], a[244:247], a[168:171], v[32:35]\n\t"
"ds_read_b64_tr_b16 v[230:231], v241 offset:23040\n\t"
"v_mfma_f32_16x16x32_bf16 v[32:35], a[220:223], a[176:179], v[32:35]\n\t"
"ds_read_b64_tr_b16 v[232:233], v240 offset:41472\n\t"
"v_mfma_f32_16x16x32_bf16 v[32:35], v[96:99], a[172:175], v[32:35]\n\t"
"ds_read_b64_tr_b16 v[234:235], v240 offset:43008\n\t"
"v_mfma_f32_16x16x32_bf16 v[36:39], a[244:247], a[172:175], v[36:39]\n\t"
"buffer_load_dwordx4 a[236:239], off, s[80:83], s20 offset:2048 sc1\n\t"
"v_mfma_f32_16x16x32_bf16 v[36:39], v[96:99], a[176:179], v[36:39]\n\t"
"Lpg21_%=:\n\t"
"s_cbranch_vccnz Lvr3_%=\n\t"
"ds_read_b64_tr_b16 v[236:237], v241 offset:41472\n\t"
"s_mov_b32 s20, 1277952\n\t"
"v_mfma_f32_16x16x32_bf16 v[36:39], a[220:223], a[180:183], v[36:39]\n\t"
"ds_read_b64_tr_b16 v[238:239], v241 offset:43008\n\t"
"v_mfma_f32_16x16x32_bf16 v[40:43], a[244:247], a[176:179], v[40:43]\n\t"
"buffer_load_dwordx4 a[240:243], off, s[80:83], s20 offset:0 sc1\n\t"
"v_mfma_f32_16x16x32_bf16 v[40:43], a[220:223], a[184:187], v[40:43]\n\t"
"s_waitcnt lgkmcnt(4)\n\t"
"v_mfma_f32_16x16x32_bf16 v[40:43], v[96:99], a[180:183], v[40:43]\n\t"
"s_waitcnt lgkmcnt(2)\n\t"
"v_mfma_f32_16x16x32_bf16 v[224:227], v[224:227], a[208:211], 0\n\t"
"v_mfma_f32_16x16x32_bf16 v[228:231], v[228:231], a[208:211], 0\n\t"
"s_waitcnt lgkmcnt(0)\n\t"
"v_mfma_f32_16x16x32_bf16 v[232:235], v[232:235], a[208:211], 0\n\t"
"v_mfma_f32_16x16x32_bf16 v[236:239], v[236:239], a[208:211], 0\n\t"
"v_mfma_f32_16x16x32_bf16 v[44:47], a[244:247], a[180:183], v[44:47]\n\t"
"buffer_load_dwordx4 a[208:211], off, s[80:83], s20 offset:1024 sc1\n\t"
"v_mfma_f32_16x16x32_bf16 v[44:47], a[220:223], a[188:191], v[44:47]\n\t"
"v_cvt_pk_bf16_f32 v224, v224, v225\n\t"
"v_cvt_pk_bf16_f32 v225, v226, v227\n\t"
"v_mfma_f32_16x16x32_bf16 v[44:47], v[96:99], a[184:187], v[44:47]\n\t"
"v_cvt_pk_bf16_f32 v226, v228, v229\n\t"
"v_cvt_pk_bf16_f32 v227, v230, v231\n\t"
"v_mfma_f32_16x16x32_bf16 v[48:51], a[244:247], a[184:187], v[48:51]\n\t"
"v_cvt_pk_bf16_f32 v232, v232, v233\n\t"
"v_cvt_pk_bf16_f32 v233, v234, v235\n\t"
"v_mfma_f32_16x16x32_bf16 v[48:51], v[96:99], a[188:191], v[48:51]\n\t"
"v_cvt_pk_bf16_f32 v234, v236, v237\n\t"
"v_cvt_pk_bf16_f32 v235, v238, v239\n\t"
"v_mfma_f32_16x16x32_bf16 v[48:51], a[220:223], a[192:195], v[48:51]\n\t"
"ds_write_b128 v243, v[224:227] offset:37888\n\t"
"v_mfma_f32_16x16x32_bf16 v[52:55], a[220:223], a[196:199], v[52:55]\n\t"
"v_mfma_f32_16x16x32_bf16 v[52:55], v[96:99], a[192:195], v[52:55]\n\t"
"v_mfma_f32_16x16x32_bf16 v[52:55], a[244:247], a[188:191], v[52:55]\n\t"
"ds_write_b128 v243, v[232:235] offset:47104\n\t"
"v_mfma_f32_16x16x32_bf16 v[56:59], a[244:247], a[192:195], v[56:59]\n\t"
"v_mfma_f32_16x16x32_bf16 v[56:59], v[96:99], a[196:199], v[56:59]\n\t"
"v_mfma_f32_16x16x32_bf16 v[56:59], a[220:223], a[200:203], v[56:59]\n\t"
"ds_read_b64_tr_b16 v[228:229], v246 offset:1536\n\t"
"v_mfma_f32_16x16x32_bf16 v[60:63], v[96:99], a[200:203], v[60:63]\n\t"
"buffer_load_dwordx4 v[96:99], off, s[80:83], s20 offset:2048 sc1\n\t"
"v_mfma_f32_16x16x32_bf16 v[60:63], a[244:247], a[196:199], v[60:63]\n\t"
"s_waitcnt vmcnt(9)\n\t"
"ds_read_b64_tr_b16 v[230:231], v246 offset:4608\n\t"
"s_mov_b32 s20, 1511424\n\t"
"v_mfma_f32_16x16x32_bf16 v[60:63], a[220:223], a[204:207], v[60:63]\n\t"
"ds_read_b64_tr_b16 v[236:237], v246 offset:7680\n\t"
"v_mfma_f32_16x16x32_bf16 v[64:67], a[248:251], a[168:171], v[64:67]\n\t"
"buffer_load_dwordx4 a[220:223], off, s[80:83], s20 offset:0 sc1\n\t"
"v_mfma_f32_16x16x32_bf16 v[64:67], v[248:251], a[176:179], v[64:67]\n\t"
"ds_read_b64_tr_b16 v[238:239], v246 offset:10752\n\t"
"v_mfma_f32_16x16x32_bf16 v[64:67], a[228:231], a[172:175], v[64:67]\n\t"
"ds_read_b64_tr_b16 v[224:225], v246 offset:13824\n\t"
"v_mfma_f32_16x16x32_bf16 v[68:71], a[248:251], a[172:175], v[68:71]\n\t"
"ds_read_b64_tr_b16 v[226:227], v246 offset:16896\n\t"
"v_mfma_f32_16x16x32_bf16 v[68:71], a[228:231], a[176:179], v[68:71]\n\t"
"buffer_load_dwordx4 a[244:247], off, s[80:83], s20 offset:1024 sc1\n\t"
"v_mfma_f32_16x16x32_bf16 v[68:71], v[248:251], a[180:183], v[68:71]\n\t"
"ds_read_b64_tr_b16 v[232:233], v246 offset:21504\n\t"
"v_mfma_f32_16x16x32_bf16 v[72:75], a[228:231], a[180:183], v[72:75]\n\t"
"ds_read_b64_tr_b16 v[234:235], v246 offset:24576\n\t"
"v_mfma_f32_16x16x32_bf16 v[72:75], a[248:251], a[176:179], v[72:75]\n\t"
"buffer_load_dwordx4 v[240:243], off, s[80:83], s20 offset:2048 sc1\n\t"
"v_mfma_f32_16x16x32_bf16 v[72:75], v[248:251], a[184:187], v[72:75]\n\t"
"ds_read_b128 a[168:171], v244 offset:33792\n\t"
"v_mfma_f32_16x16x32_bf16 v[76:79], a[248:251], a[180:183], v[76:79]\n\t"
"s_waitcnt lgkmcnt(7)\n\t"
"ds_read_b128 a[172:175], v244 offset:34304\n\t"
"v_mfma_f32_16x16x32_bf16 v[76:79], v[248:251], a[188:191], v[76:79]\n\t"
"s_waitcnt lgkmcnt(6)\n\t"
"ds_read_b128 a[176:179], v244 offset:34816\n\t"
"v_mfma_f32_16x16x32_bf16 v[76:79], a[228:231], a[184:187], v[76:79]\n\t"
"s_waitcnt lgkmcnt(5)\n\t"
"ds_read_b128 a[180:183], v244 offset:35328\n\t"
"v_mfma_f32_16x16x32_bf16 v[228:231], v[228:231], v[220:223], 0\n\t"
"s_waitcnt lgkmcnt(4)\n\t"
"v_mfma_f32_16x16x32_bf16 v[236:239], v[236:239], v[220:223], 0\n\t"
"v_mfma_f32_16x16x32_bf16 v[224:227], v[224:227], v[220:223], 0\n\t"
"v_mfma_f32_16x16x32_bf16 v[232:235], v[232:235], v[220:223], 0\n\t"
"v_mfma_f32_16x16x32_bf16 v[80:83], a[248:251], a[184:187], v[80:83]\n\t"
"s_mov_b32 s20, 1523712\n\t"
"ds_read_b128 a[184:187], v244 offset:35840\n\t"
"v_mfma_f32_16x16x32_bf16 v[80:83], v[248:251], a[192:195], v[80:83]\n\t"
"v_cvt_pk_bf16_f32 v228, v228, v229\n\t"
"v_cvt_pk_bf16_f32 v229, v230, v231\n\t"
"v_mfma_f32_16x16x32_bf16 v[80:83], a[228:231], a[188:191], v[80:83]\n\t"
"v_cvt_pk_bf16_f32 v230, v236, v237\n\t"
"v_cvt_pk_bf16_f32 v231, v238, v239\n\t"
"v_mfma_f32_16x16x32_bf16 v[84:87], a[248:251], a[188:191], v[84:87]\n\t"
"ds_read_b128 a[188:191], v244 offset:36352\n\t"
"v_cvt_pk_bf16_f32 v224, v224, v225\n\t"
"v_mfma_f32_16x16x32_bf16 v[84:87], a[228:231], a[192:195], v[84:87]\n\t"
"v_cvt_pk_bf16_f32 v225, v226, v227\n\t"
"v_cvt_pk_bf16_f32 v226, v232, v233\n\t"
"v_mfma_f32_16x16x32_bf16 v[84:87], v[248:251], a[196:199], v[84:87]\n\t"
"v_cvt_pk_bf16_f32 v227, v234, v235\n\t"
"v_mfma_f32_16x16x32_bf16 v[88:91], v[248:251], a[200:203], v[88:91]\n\t"
"ds_read_b64_tr_b16 v[232:233], v246 offset:27648\n\t"
"v_mfma_f32_16x16x32_bf16 v[88:91], a[228:231], a[196:199], v[88:91]\n\t"
"ds_read_b64_tr_b16 v[234:235], v246 offset:30720\n\t"
"v_mfma_f32_16x16x32_bf16 v[88:91], a[248:251], a[192:195], v[88:91]\n\t"
"buffer_load_dwordx4 v[236:239], off, s[80:83], s20 offset:0 sc1\n\t"
"v_mfma_f32_16x16x32_bf16 v[92:95], a[248:251], a[196:199], v[92:95]\n\t"
"ds_read_b128 a[192:195], v244 offset:36864\n\t"
"v_mfma_f32_16x16x32_bf16 v[92:95], a[228:231], a[200:203], v[92:95]\n\t"
"buffer_load_dwordx4 a[248:251], off, s[80:83], s20 offset:1024 sc1\n\t"
"v_mfma_f32_16x16x32_bf16 v[92:95], v[248:251], a[204:207], v[92:95]\n\t"
"s_waitcnt vmcnt(11) lgkmcnt(1)\n\t"
"ds_read_b128 a[196:199], v244 offset:37376\n\t"
"v_mfma_f32_16x16x32_bf16 v[232:235], v[232:235], v[220:223], 0\n\t"
"ds_read_b128 a[200:203], v244 offset:37888\n\t"
"v_mfma_f32_16x16x32_bf16 v[100:103], a[252:255], a[168:171], v[100:103]\n\t"
"ds_read_b128 a[204:207], v244 offset:38400\n\t"
"v_mfma_f32_16x16x32_bf16 v[100:103], a[216:219], a[176:179], v[100:103]\n\t"
"buffer_load_dwordx4 v[248:251], off, s[80:83], s20 offset:2048 sc1\n\t"
"v_mfma_f32_16x16x32_bf16 v[100:103], a[212:215], a[172:175], v[100:103]\n\t"
"s_mov_b32 s20, 1536000\n\t"
"v_mfma_f32_16x16x32_bf16 v[104:107], a[252:255], a[172:175], v[104:107]\n\t"
"buffer_load_dwordx4 a[228:231], off, s[80:83], s20 offset:0 sc1\n\t"
"v_mfma_f32_16x16x32_bf16 v[104:107], a[212:215], a[176:179], v[104:107]\n\t"
"v_cvt_pk_bf16_f32 v232, v232, v233\n\t"
"v_mfma_f32_16x16x32_bf16 v[104:107], a[216:219], a[180:183], v[104:107]\n\t"
"s_waitcnt lgkmcnt(0)\n\t"
"s_barrier\n\t"
"v_mfma_f32_16x16x32_bf16 v[108:111], a[216:219], a[184:187], v[108:111]\n\t"
"v_cvt_pk_bf16_f32 v233, v234, v235\n\t"
"v_mfma_f32_16x16x32_bf16 v[108:111], a[212:215], a[180:183], v[108:111]\n\t"
"v_mfma_f32_16x16x32_bf16 v[108:111], a[252:255], a[176:179], v[108:111]\n\t"
"ds_write2st64_b64 v245, v[228:229], v[230:231] offset0:48 offset1:49\n\t"
"v_mfma_f32_16x16x32_bf16 v[112:115], a[252:255], a[180:183], v[112:115]\n\t"
"v_mfma_f32_16x16x32_bf16 v[112:115], a[212:215], a[184:187], v[112:115]\n\t"
"ds_read_b64_tr_b16 v[228:229], v246 offset:33792\n\t"
"v_mfma_f32_16x16x32_bf16 v[112:115], a[216:219], a[188:191], v[112:115]\n\t"
"ds_write2st64_b64 v245, v[224:225], v[226:227] offset0:50 offset1:51\n\t"
"v_mfma_f32_16x16x32_bf16 v[116:119], a[216:219], a[192:195], v[116:119]\n\t"
"v_mfma_f32_16x16x32_bf16 v[116:119], a[212:215], a[188:191], v[116:119]\n\t"
"ds_read_b64_tr_b16 v[230:231], v246 offset:36864\n\t"
"v_mfma_f32_16x16x32_bf16 v[116:119], a[252:255], a[184:187], v[116:119]\n\t"
"ds_write_b64 v245, v[232:233] offset:26624\n\t"
"v_mfma_f32_16x16x32_bf16 v[120:123], a[252:255], a[188:191], v[120:123]\n\t"
"ds_read_b64_tr_b16 v[224:225], v246 offset:41472\n\t"
"v_mfma_f32_16x16x32_bf16 v[120:123], a[212:215], a[192:195], v[120:123]\n\t"
"ds_read_b64_tr_b16 v[226:227], v246 offset:44544\n\t"
"v_mfma_f32_16x16x32_bf16 v[120:123], a[216:219], a[196:199], v[120:123]\n\t"
"ds_read_b64_tr_b16 v[232:233], v246 offset:47616\n\t"
"v_mfma_f32_16x16x32_bf16 v[124:127], a[212:215], a[196:199], v[124:127]\n\t"
"s_waitcnt lgkmcnt(4)\n\t"
"ds_read_b64_tr_b16 v[234:235], v246 offset:50688\n\t"
"v_mfma_f32_16x16x32_bf16 v[124:127], a[252:255], a[192:195], v[124:127]\n\t"
"v_mfma_f32_16x16x32_bf16 v[124:127], a[216:219], a[200:203], v[124:127]\n\t"
"v_mfma_f32_16x16x32_bf16 v[228:231], v[228:231], v[220:223], 0\n\t"
"s_waitcnt lgkmcnt(2)\n\t"
"v_mfma_f32_16x16x32_bf16 v[224:227], v[224:227], v[220:223], 0\n\t"
"v_mfma_f32_16x16x32_bf16 v[128:131], a[212:215], a[200:203], v[128:131]\n\t"
"buffer_load_dwordx4 a[212:215], off, s[80:83], s20 offset:1024 sc1\n\t"
"v_mfma_f32_16x16x32_bf16 v[128:131], a[252:255], a[196:199], v[128:131]\n\t"
"s_waitcnt vmcnt(11) lgkmcnt(0)\n\t"
"v_mfma_f32_16x16x32_bf16 v[128:131], a[216:219], a[204:207], v[128:131]\n\t"
"v_mfma_f32_16x16x32_bf16 v[232:235], v[232:235], v[220:223], 0\n\t"
"buffer_load_dwordx4 a[216:219], off, s[80:83], s20 offset:2048 sc1\n\t"
"v_mfma_f32_16x16x32_bf16 v[132:135], a[224:227], a[168:171], v[132:135]\n\t"
"s_mov_b32 s20, 1548288\n\t"
"v_cvt_pk_bf16_f32 v228, v228, v229\n\t"
"v_cvt_pk_bf16_f32 v229, v230, v231\n\t"
"v_mfma_f32_16x16x32_bf16 v[132:135], a[236:239], a[176:179], v[132:135]\n\t"
"v_cvt_pk_bf16_f32 v230, v224, v225\n\t"
"v_cvt_pk_bf16_f32 v231, v226, v227\n\t"
"v_mfma_f32_16x16x32_bf16 v[132:135], a[232:235], a[172:175], v[132:135]\n\t"
"ds_write2st64_b64 v245, v[228:229], v[230:231] offset0:53 offset1:54\n\t"
"v_mfma_f32_16x16x32_bf16 v[136:139], a[224:227], a[172:175], v[136:139]\n\t"
"buffer_load_dwordx4 a[252:255], off, s[80:83], s20 offset:0 sc1\n\t"
"v_mfma_f32_16x16x32_bf16 v[136:139], a[232:235], a[176:179], v[136:139]\n\t"
"ds_read_b64_tr_b16 v[224:225], v246 offset:53760\n\t"
"v_mfma_f32_16x16x32_bf16 v[136:139], a[236:239], a[180:183], v[136:139]\n\t"
"ds_read_b64_tr_b16 v[226:227], v246 offset:56832\n\t"
"v_cvt_pk_bf16_f32 v232, v232, v233\n\t"
"v_mfma_f32_16x16x32_bf16 v[140:143], a[236:239], a[184:187], v[140:143]\n\t"
"v_cvt_pk_bf16_f32 v233, v234, v235\n\t"
"v_mfma_f32_16x16x32_bf16 v[140:143], a[232:235], a[180:183], v[140:143]\n\t"
"v_mfma_f32_16x16x32_bf16 v[140:143], a[224:227], a[176:179], v[140:143]\n\t"
"v_mfma_f32_16x16x32_bf16 v[144:147], a[224:227], a[180:183], v[144:147]\n\t"
"v_mfma_f32_16x16x32_bf16 v[144:147], a[232:235], a[184:187], v[144:147]\n\t"
"v_mfma_f32_16x16x32_bf16 v[144:147], a[236:239], a[188:191], v[144:147]\n\t"
"v_mfma_f32_16x16x32_bf16 v[148:151], a[232:235], a[188:191], v[148:151]\n\t"
"s_waitcnt lgkmcnt(0)\n\t"
"v_mfma_f32_16x16x32_bf16 v[148:151], a[236:239], a[192:195], v[148:151]\n\t"
"v_mfma_f32_16x16x32_bf16 v[148:151], a[224:227], a[184:187], v[148:151]\n\t"
"v_mfma_f32_16x16x32_bf16 v[224:227], v[224:227], v[220:223], 0\n\t"
"v_mfma_f32_16x16x32_bf16 v[152:155], a[224:227], a[188:191], v[152:155]\n\t"
"v_mfma_f32_16x16x32_bf16 v[152:155], a[236:239], a[196:199], v[152:155]\n\t"
"v_mfma_f32_16x16x32_bf16 v[152:155], a[232:235], a[192:195], v[152:155]\n\t"
"v_mfma_f32_16x16x32_bf16 v[156:159], a[224:227], a[192:195], v[156:159]\n\t"
"v_mfma_f32_16x16x32_bf16 v[156:159], a[232:235], a[196:199], v[156:159]\n\t"
"v_cvt_pk_bf16_f32 v234, v224, v225\n\t"
"v_cvt_pk_bf16_f32 v235, v226, v227\n\t"
"v_mfma_f32_16x16x32_bf16 v[156:159], a[236:239], a[200:203], v[156:159]\n\t"
"ds_write2st64_b64 v245, v[232:233], v[234:235] offset0:55 offset1:56\n\t"
"v_mfma_f32_16x16x32_bf16 v[160:163], a[232:235], a[200:203], v[160:163]\n\t"
"buffer_load_dwordx4 a[232:235], off, s[80:83], s20 offset:1024 sc1\n\t"
"v_mfma_f32_16x16x32_bf16 v[160:163], a[224:227], a[196:199], v[160:163]\n\t"
"s_waitcnt vmcnt(11)\n\t"
"v_mfma_f32_16x16x32_bf16 v[160:163], a[236:239], a[204:207], v[160:163]\n\t"
"v_mfma_f32_16x16x32_bf16 v[164:167], a[240:243], a[168:171], v[164:167]\n\t"
"buffer_load_dwordx4 a[224:227], off, s[80:83], s20 offset:2048 sc1\n\t"
"v_mfma_f32_16x16x32_bf16 v[164:167], v[96:99], a[176:179], v[164:167]\n\t"
"s_mov_b32 s20, 1560576\n\t"
"v_mfma_f32_16x16x32_bf16 v[164:167], a[208:211], a[172:175], v[164:167]\n\t"
"s_waitcnt lgkmcnt(0)\n\t"
"s_barrier\n\t"
"v_mfma_f32_16x16x32_bf16 v[168:171], a[240:243], a[172:175], v[168:171]\n\t"
"ds_read_b128 a[168:171], v244 offset:24576\n\t"
"v_mfma_f32_16x16x32_bf16 v[168:171], a[208:211], a[176:179], v[168:171]\n\t"
"buffer_load_dwordx4 a[236:239], off, s[80:83], s20 offset:0 sc1\n\t"
"v_mfma_f32_16x16x32_bf16 v[168:171], v[96:99], a[180:183], v[168:171]\n\t"
"ds_read_b128 a[172:175], v244 offset:25088\n\t"
"v_mfma_f32_16x16x32_bf16 v[172:175], v[96:99], a[184:187], v[172:175]\n\t"
"v_mfma_f32_16x16x32_bf16 v[172:175], a[208:211], a[180:183], v[172:175]\n\t"
"v_mfma_f32_16x16x32_bf16 v[172:175], a[240:243], a[176:179], v[172:175]\n\t"
"ds_read_b128 a[176:179], v244 offset:25600\n\t"
"v_mfma_f32_16x16x32_bf16 v[176:179], a[240:243], a[180:183], v[176:179]\n\t"
"ds_read_b128 a[180:183], v244 offset:26112\n\t"
"v_mfma_f32_16x16x32_bf16 v[176:179], a[208:211], a[184:187], v[176:179]\n\t"
"v_mfma_f32_16x16x32_bf16 v[176:179], v[96:99], a[188:191], v[176:179]\n\t"
"v_mfma_f32_16x16x32_bf16 v[180:183], v[96:99], a[192:195], v[180:183]\n\t"
"v_mfma_f32_16x16x32_bf16 v[180:183], a[208:211], a[188:191], v[180:183]\n\t"
"v_mfma_f32_16x16x32_bf16 v[180:183], a[240:243], a[184:187], v[180:183]\n\t"
"ds_read_b128 a[184:187], v244 offset:26624\n\t"
"v_mfma_f32_16x16x32_bf16 v[184:187], a[240:243], a[188:191], v[184:187]\n\t"
"ds_read_b128 a[188:191], v244 offset:27136\n\t"
"v_mfma_f32_16x16x32_bf16 v[184:187], a[208:211], a[192:195], v[184:187]\n\t"
"v_mfma_f32_16x16x32_bf16 v[184:187], v[96:99], a[196:199], v[184:187]\n\t"
"v_mfma_f32_16x16x32_bf16 v[188:191], v[96:99], a[200:203], v[188:191]\n\t"
"v_xor_b32 v246, 0xc0, v246\n\t"
"v_mfma_f32_16x16x32_bf16 v[188:191], a[208:211], a[196:199], v[188:191]\n\t"
"v_mfma_f32_16x16x32_bf16 v[188:191], a[240:243], a[192:195], v[188:191]\n\t"
"ds_read_b128 a[192:195], v244 offset:27648\n\t"
"v_mfma_f32_16x16x32_bf16 v[192:195], a[240:243], a[196:199], v[192:195]\n\t"
"buffer_load_dwordx4 a[240:243], off, s[80:83], s20 offset:1024 sc1\n\t"
"v_mfma_f32_16x16x32_bf16 v[192:195], a[208:211], a[200:203], v[192:195]\n\t"
"s_waitcnt vmcnt(11) lgkmcnt(4)\n\t"
"ds_read_b128 a[196:199], v244 offset:28160\n\t"
"v_mfma_f32_16x16x32_bf16 v[192:195], v[96:99], a[204:207], v[192:195]\n\t"
"ds_read_b128 a[200:203], v244 offset:28672\n\t"
"Lst30_%=:\n\t"
"v_mfma_f32_16x16x32_bf16 v[0:3], a[220:223], a[168:171], v[0:3]\n\t"
"buffer_load_dwordx4 v[96:99], off, s[80:83], s20 offset:2048 sc1\n\t"
"v_mfma_f32_16x16x32_bf16 v[0:3], v[240:243], a[176:179], v[0:3]\n\t"
"s_waitcnt lgkmcnt(5)\n\t"
"ds_read_b128 a[204:207], v244 offset:29184\n\t"
"s_mov_b32 s20, 1572864\n\t"
"v_mfma_f32_16x16x32_bf16 v[0:3], a[244:247], a[172:175], v[0:3]\n\t"
"v_mfma_f32_16x16x32_bf16 v[4:7], a[220:223], a[172:175], v[4:7]\n\t"
"buffer_load_dwordx4 a[208:211], off, s[80:83], s20 offset:0 sc1\n\t"
"v_mfma_f32_16x16x32_bf16 v[4:7], a[244:247], a[176:179], v[4:7]\n\t"
"s_waitcnt lgkmcnt(5)\n\t"
"v_mfma_f32_16x16x32_bf16 v[4:7], v[240:243], a[180:183], v[4:7]\n\t"
"v_mfma_f32_16x16x32_bf16 v[8:11], v[240:243], a[184:187], v[8:11]\n\t"
"s_waitcnt lgkmcnt(4)\n\t"
"v_mfma_f32_16x16x32_bf16 v[8:11], a[244:247], a[180:183], v[8:11]\n\t"
"v_mfma_f32_16x16x32_bf16 v[8:11], a[220:223], a[176:179], v[8:11]\n\t"
"ds_read_b64_tr_b16 v[228:229], v246 offset:1536\n\t"
"v_mfma_f32_16x16x32_bf16 v[12:15], a[220:223], a[180:183], v[12:15]\n\t"
"s_waitcnt lgkmcnt(4)\n\t"
"ds_read_b64_tr_b16 v[230:231], v246 offset:4608\n\t"
"v_mfma_f32_16x16x32_bf16 v[12:15], a[244:247], a[184:187], v[12:15]\n\t"
"ds_read_b64_tr_b16 v[224:225], v246 offset:7680\n\t"
"v_mfma_f32_16x16x32_bf16 v[12:15], v[240:243], a[188:191], v[12:15]\n\t"
"ds_read_b64_tr_b16 v[226:227], v246 offset:10752\n\t"
"v_mfma_f32_16x16x32_bf16 v[16:19], v[240:243], a[192:195], v[16:19]\n\t"
"s_waitcnt lgkmcnt(6)\n\t"
"v_mfma_f32_16x16x32_bf16 v[16:19], a[244:247], a[188:191], v[16:19]\n\t"
"v_mfma_f32_16x16x32_bf16 v[16:19], a[220:223], a[184:187], v[16:19]\n\t"
"v_mfma_f32_16x16x32_bf16 v[20:23], a[220:223], a[188:191], v[20:23]\n\t"
"s_waitcnt lgkmcnt(5)\n\t"
"v_mfma_f32_16x16x32_bf16 v[20:23], a[244:247], a[192:195], v[20:23]\n\t"
"v_mfma_f32_16x16x32_bf16 v[20:23], v[240:243], a[196:199], v[20:23]\n\t"
"v_mfma_f32_16x16x32_bf16 v[24:27], v[240:243], a[200:203], v[24:27]\n\t"
"s_waitcnt lgkmcnt(4)\n\t"
"v_mfma_f32_16x16x32_bf16 v[24:27], a[244:247], a[196:199], v[24:27]\n\t"
"v_mfma_f32_16x16x32_bf16 v[24:27], a[220:223], a[192:195], v[24:27]\n\t"
"v_mfma_f32_16x16x32_bf16 v[28:31], a[220:223], a[196:199], v[28:31]\n\t"
"buffer_load_dwordx4 a[220:223], off, s[80:83], s20 offset:1024 sc1\n\t"
"v_mfma_f32_16x16x32_bf16 v[28:31], a[244:247], a[200:203], v[28:31]\n\t"
"s_waitcnt lgkmcnt(0)\n\t"
"v_mfma_f32_16x16x32_bf16 v[28:31], v[240:243], a[204:207], v[28:31]\n\t"
"s_waitcnt vmcnt(11)\n\t"
"v_mfma_f32_16x16x32_bf16 v[228:231], v[228:231], v[220:223], 0\n\t"
"buffer_load_dwordx4 a[244:247], off, s[80:83], s20 offset:2048 sc1\n\t"
"v_mfma_f32_16x16x32_bf16 v[224:227], v[224:227], v[220:223], 0\n\t"
"s_mov_b32 s20, 1585152\n\t"
"v_mfma_f32_16x16x32_bf16 v[32:35], v[236:239], a[168:171], v[32:35]\n\t"
"v_mfma_f32_16x16x32_bf16 v[32:35], v[248:251], a[176:179], v[32:35]\n\t"
"buffer_load_dwordx4 v[240:243], off, s[80:83], s20 offset:0 sc1\n\t"
"v_mfma_f32_16x16x32_bf16 v[32:35], a[248:251], a[172:175], v[32:35]\n\t"
"v_mfma_f32_16x16x32_bf16 v[36:39], v[236:239], a[172:175], v[36:39]\n\t"
"v_cvt_pk_bf16_f32 v228, v228, v229\n\t"
"v_cvt_pk_bf16_f32 v229, v230, v231\n\t"
"v_mfma_f32_16x16x32_bf16 v[36:39], a[248:251], a[176:179], v[36:39]\n\t"
"v_cvt_pk_bf16_f32 v230, v224, v225\n\t"
"v_cvt_pk_bf16_f32 v231, v226, v227\n\t"
"v_mfma_f32_16x16x32_bf16 v[36:39], v[248:251], a[180:183], v[36:39]\n\t"
"v_mfma_f32_16x16x32_bf16 v[40:43], v[248:251], a[184:187], v[40:43]\n\t"
"v_mfma_f32_16x16x32_bf16 v[40:43], a[248:251], a[180:183], v[40:43]\n\t"
"v_mfma_f32_16x16x32_bf16 v[40:43], v[236:239], a[176:179], v[40:43]\n\t"
"ds_read_b64_tr_b16 v[232:233], v246 offset:13824\n\t"
"v_mfma_f32_16x16x32_bf16 v[44:47], v[236:239], a[180:183], v[44:47]\n\t"
"ds_read_b64_tr_b16 v[234:235], v246 offset:16896\n\t"
"v_mfma_f32_16x16x32_bf16 v[44:47], a[248:251], a[184:187], v[44:47]\n\t"
"ds_read_b64_tr_b16 v[224:225], v246 offset:21504\n\t"
"v_mfma_f32_16x16x32_bf16 v[44:47], v[248:251], a[188:191], v[44:47]\n\t"
"ds_read_b64_tr_b16 v[226:227], v246 offset:24576\n\t"
"v_mfma_f32_16x16x32_bf16 v[48:51], v[248:251], a[192:195], v[48:51]\n\t"
"v_mfma_f32_16x16x32_bf16 v[48:51], a[248:251], a[188:191], v[48:51]\n\t"
"v_mfma_f32_16x16x32_bf16 v[48:51], v[236:239], a[184:187], v[48:51]\n\t"
"v_mfma_f32_16x16x32_bf16 v[52:55], v[236:239], a[188:191], v[52:55]\n\t"
"v_mfma_f32_16x16x32_bf16 v[52:55], a[248:251], a[192:195], v[52:55]\n\t"
"v_mfma_f32_16x16x32_bf16 v[52:55], v[248:251], a[196:199], v[52:55]\n\t"
"v_mfma_f32_16x16x32_bf16 v[56:59], v[248:251], a[200:203], v[56:59]\n\t"
"v_mfma_f32_16x16x32_bf16 v[56:59], a[248:251], a[196:199], v[56:59]\n\t"
"v_mfma_f32_16x16x32_bf16 v[56:59], v[236:239], a[192:195], v[56:59]\n\t"
"v_mfma_f32_16x16x32_bf16 v[60:63], v[236:239], a[196:199], v[60:63]\n\t"
"buffer_load_dwordx4 v[236:239], off, s[80:83], s20 offset:1024 sc1\n\t"
"v_mfma_f32_16x16x32_bf16 v[60:63], a[248:251], a[200:203], v[60:63]\n\t"
"s_waitcnt lgkmcnt(0)\n\t"
"v_mfma_f32_16x16x32_bf16 v[60:63], v[248:251], a[204:207], v[60:63]\n\t"
"s_waitcnt vmcnt(11)\n\t"
"v_mfma_f32_16x16x32_bf16 v[232:235], v[232:235], v[220:223], 0\n\t"
"buffer_load_dwordx4 v[248:251], off, s[80:83], s20 offset:2048 sc1\n\t"
"v_mfma_f32_16x16x32_bf16 v[224:227], v[224:227], v[220:223], 0\n\t"
"s_mov_b32 s20, 1597440\n\t"
"v_mfma_f32_16x16x32_bf16 v[64:67], a[228:231], a[168:171], v[64:67]\n\t"
"ds_read_b128 a[168:171], v244 offset:33792\n\t"
"v_mfma_f32_16x16x32_bf16 v[64:67], a[216:219], a[176:179], v[64:67]\n\t"
"buffer_load_dwordx4 a[248:251], off, s[80:83], s20 offset:0 sc1\n\t"
"v_mfma_f32_16x16x32_bf16 v[64:67], a[212:215], a[172:175], v[64:67]\n\t"
"v_mfma_f32_16x16x32_bf16 v[68:71], a[228:231], a[172:175], v[68:71]\n\t"
"ds_read_b128 a[172:175], v244 offset:34304\n\t"
"v_cvt_pk_bf16_f32 v232, v232, v233\n\t"
"v_mfma_f32_16x16x32_bf16 v[68:71], a[212:215], a[176:179], v[68:71]\n\t"
"v_cvt_pk_bf16_f32 v233, v234, v235\n\t"
"v_cvt_pk_bf16_f32 v234, v224, v225\n\t"
"v_mfma_f32_16x16x32_bf16 v[68:71], a[216:219], a[180:183], v[68:71]\n\t"
"v_cvt_pk_bf16_f32 v235, v226, v227\n\t"
"v_mfma_f32_16x16x32_bf16 v[72:75], a[216:219], a[184:187], v[72:75]\n\t"
"v_mfma_f32_16x16x32_bf16 v[72:75], a[212:215], a[180:183], v[72:75]\n\t"
"v_mfma_f32_16x16x32_bf16 v[72:75], a[228:231], a[176:179], v[72:75]\n\t"
"ds_read_b128 a[176:179], v244 offset:34816\n\t"
"v_mfma_f32_16x16x32_bf16 v[76:79], a[228:231], a[180:183], v[76:79]\n\t"
"ds_read_b128 a[180:183], v244 offset:35328\n\t"
"v_mfma_f32_16x16x32_bf16 v[76:79], a[212:215], a[184:187], v[76:79]\n\t"
"ds_read_b64_tr_b16 v[224:225], v246 offset:27648\n\t"
"v_mfma_f32_16x16x32_bf16 v[76:79], a[216:219], a[188:191], v[76:79]\n\t"
"ds_read_b64_tr_b16 v[226:227], v246 offset:30720\n\t"
"v_mfma_f32_16x16x32_bf16 v[80:83], a[216:219], a[192:195], v[80:83]\n\t"
"v_mfma_f32_16x16x32_bf16 v[80:83], a[212:215], a[188:191], v[80:83]\n\t"
"v_mfma_f32_16x16x32_bf16 v[80:83], a[228:231], a[184:187], v[80:83]\n\t"
"ds_read_b128 a[184:187], v244 offset:35840\n\t"
"v_mfma_f32_16x16x32_bf16 v[84:87], a[228:231], a[188:191], v[84:87]\n\t"
"ds_read_b128 a[188:191], v244 offset:36352\n\t"
"v_mfma_f32_16x16x32_bf16 v[84:87], a[212:215], a[192:195], v[84:87]\n\t"
"v_mfma_f32_16x16x32_bf16 v[84:87], a[216:219], a[196:199], v[84:87]\n\t"
"v_mfma_f32_16x16x32_bf16 v[88:91], a[216:219], a[200:203], v[88:91]\n\t"
"v_mfma_f32_16x16x32_bf16 v[88:91], a[212:215], a[196:199], v[88:91]\n\t"
"v_mfma_f32_16x16x32_bf16 v[88:91], a[228:231], a[192:195], v[88:91]\n\t"
"ds_read_b128 a[192:195], v244 offset:36864\n\t"
"v_mfma_f32_16x16x32_bf16 v[92:95], a[228:231], a[196:199], v[92:95]\n\t"
"buffer_load_dwordx4 a[228:231], off, s[80:83], s20 offset:1024 sc1\n\t"
"v_mfma_f32_16x16x32_bf16 v[92:95], a[212:215], a[200:203], v[92:95]\n\t"
"s_waitcnt vmcnt(11) lgkmcnt(3)\n\t"
"ds_read_b128 a[196:199], v244 offset:37376\n\t"
"v_mfma_f32_16x16x32_bf16 v[92:95], a[216:219], a[204:207], v[92:95]\n\t"
"ds_read_b128 a[200:203], v244 offset:37888\n\t"
"v_mfma_f32_16x16x32_bf16 v[224:227], v[224:227], v[220:223], 0\n\t"
"ds_read_b128 a[204:207], v244 offset:38400\n\t"
"v_mfma_f32_16x16x32_bf16 v[100:103], a[252:255], a[168:171], v[100:103]\n\t"
"buffer_load_dwordx4 a[212:215], off, s[80:83], s20 offset:2048 sc1\n\t"
"v_mfma_f32_16x16x32_bf16 v[100:103], a[224:227], a[176:179], v[100:103]\n\t"
"s_mov_b32 s20, 1609728\n\t"
"v_mfma_f32_16x16x32_bf16 v[100:103], a[232:235], a[172:175], v[100:103]\n\t"
"buffer_load_dwordx4 a[216:219], off, s[80:83], s20 offset:0 sc1\n\t"
"v_mfma_f32_16x16x32_bf16 v[104:107], a[252:255], a[172:175], v[104:107]\n\t"
"s_waitcnt lgkmcnt(5)\n\t"
"v_mfma_f32_16x16x32_bf16 v[104:107], a[232:235], a[176:179], v[104:107]\n\t"
"v_cvt_pk_bf16_f32 v224, v224, v225\n\t"
"v_mfma_f32_16x16x32_bf16 v[104:107], a[224:227], a[180:183], v[104:107]\n\t"
"s_waitcnt lgkmcnt(0)\n\t"
"s_barrier\n\t"
"v_mfma_f32_16x16x32_bf16 v[108:111], a[224:227], a[184:187], v[108:111]\n\t"
"v_cvt_pk_bf16_f32 v225, v226, v227\n\t"
"v_mfma_f32_16x16x32_bf16 v[108:111], a[232:235], a[180:183], v[108:111]\n\t"
"v_fma_f32 v0, v100, s52, v0\n\t"
"v_fma_f32 v1, v101, s52, v1\n\t"
"v_mfma_f32_16x16x32_bf16 v[108:111], a[252:255], a[176:179], v[108:111]\n\t"
"ds_write2st64_b64 v245, v[228:229], v[230:231] offset0:48 offset1:49\n\t"
"v_mfma_f32_16x16x32_bf16 v[112:115], a[252:255], a[180:183], v[112:115]\n\t"
"v_fma_f32 v2, v102, s52, v2\n\t"
"v_fma_f32 v3, v103, s52, v3\n\t"
"v_mfma_f32_16x16x32_bf16 v[112:115], a[232:235], a[184:187], v[112:115]\n\t"
"ds_read_b64_tr_b16 v[228:229], v246 offset:33792\n\t"
"v_mfma_f32_16x16x32_bf16 v[112:115], a[224:227], a[188:191], v[112:115]\n\t"
"ds_write2st64_b64 v245, v[232:233], v[234:235] offset0:50 offset1:51\n\t"
"v_mfma_f32_16x16x32_bf16 v[116:119], a[224:227], a[192:195], v[116:119]\n\t"
"v_fma_f32 v100, v0, s54, -v100\n\t"
"v_fma_f32 v101, v1, s54, -v101\n\t"
"v_mfma_f32_16x16x32_bf16 v[116:119], a[232:235], a[188:191], v[116:119]\n\t"
"ds_read_b64_tr_b16 v[230:231], v246 offset:36864\n\t"
"v_mfma_f32_16x16x32_bf16 v[116:119], a[252:255], a[184:187], v[116:119]\n\t"
"ds_write_b64 v245, v[224:225] offset:26624\n\t"
"v_mfma_f32_16x16x32_bf16 v[120:123], a[252:255], a[188:191], v[120:123]\n\t"
"ds_read_b64_tr_b16 v[232:233], v246 offset:41472\n\t"
"v_fma_f32 v102, v2, s54, -v102\n\t"
"v_mfma_f32_16x16x32_bf16 v[120:123], a[232:235], a[192:195], v[120:123]\n\t"
"ds_read_b64_tr_b16 v[234:235], v246 offset:44544\n\t"
"v_fma_f32 v103, v3, s54, -v103\n\t"
"v_mfma_f32_16x16x32_bf16 v[120:123], a[224:227], a[196:199], v[120:123]\n\t"
"ds_read_b64_tr_b16 v[224:225], v246 offset:47616\n\t"
"v_fma_f32 v4, v104, s52, v4\n\t"
"v_mfma_f32_16x16x32_bf16 v[124:127], a[232:235], a[196:199], v[124:127]\n\t"
"s_waitcnt lgkmcnt(4)\n\t"
"ds_read_b64_tr_b16 v[226:227], v246 offset:50688\n\t"
"v_fma_f32 v5, v105, s52, v5\n\t"
"v_mfma_f32_16x16x32_bf16 v[124:127], a[252:255], a[192:195], v[124:127]\n\t"
"v_fma_f32 v6, v106, s52, v6\n\t"
"v_fma_f32 v7, v107, s52, v7\n\t"
"v_mfma_f32_16x16x32_bf16 v[124:127], a[224:227], a[200:203], v[124:127]\n\t"
"v_fma_f32 v104, v4, s54, -v104\n\t"
"v_fma_f32 v105, v5, s54, -v105\n\t"
"v_mfma_f32_16x16x32_bf16 v[228:231], v[228:231], v[220:223], 0\n\t"
"s_waitcnt lgkmcnt(2)\n\t"
"v_fma_f32 v106, v6, s54, -v106\n\t"
"v_fma_f32 v107, v7, s54, -v107\n\t"
"v_mfma_f32_16x16x32_bf16 v[232:235], v[232:235], v[220:223], 0\n\t"
"v_fma_f32 v8, v108, s52, v8\n\t"
"v_fma_f32 v9, v109, s52, v9\n\t"
"v_mfma_f32_16x16x32_bf16 v[128:131], a[232:235], a[200:203], v[128:131]\n\t"
"s_waitcnt lgkmcnt(0)\n\t"
"v_fma_f32 v10, v110, s52, v10\n\t"
"v_fma_f32 v11, v111, s52, v11\n\t"
"v_mfma_f32_16x16x32_bf16 v[128:131], a[252:255], a[196:199], v[128:131]\n\t"
"s_waitcnt vmcnt(10)\n\t"
"v_fma_f32 v108, v8, s54, -v108\n\t"
"v_fma_f32 v109, v9, s54, -v109\n\t"
"v_mfma_f32_16x16x32_bf16 v[128:131], a[224:227], a[204:207], v[128:131]\n\t"
"v_fma_f32 v110, v10, s54, -v110\n\t"
"v_fma_f32 v111, v11, s54, -v111\n\t"
"v_mfma_f32_16x16x32_bf16 v[224:227], v[224:227], v[220:223], 0\n\t"
"buffer_load_dwordx4 a[232:235], off, s[80:83], s20 offset:1024 sc1\n\t"
"v_mfma_f32_16x16x32_bf16 v[132:135], a[236:239], a[168:171], v[132:135]\n\t"
"buffer_load_dwordx4 a[224:227], off, s[80:83], s20 offset:2048 sc1\n\t"
"v_mfma_f32_16x16x32_bf16 v[132:135], v[96:99], a[176:179], v[132:135]\n\t"
"s_mov_b32 s20, 1622016\n\t"
"v_cvt_pk_bf16_f32 v228, v228, v229\n\t"
"v_cvt_pk_bf16_f32 v229, v230, v231\n\t"
"v_mfma_f32_16x16x32_bf16 v[132:135], a[240:243], a[172:175], v[132:135]\n\t"
"v_cvt_pk_bf16_f32 v230, v232, v233\n\t"
"v_cvt_pk_bf16_f32 v231, v234, v235\n\t"
"v_mfma_f32_16x16x32_bf16 v[136:139], a[236:239], a[172:175], v[136:139]\n\t"
"ds_read_b64_tr_b16 v[232:233], v246 offset:53760\n\t"
"v_fma_f32 v12, v112, s52, v12\n\t"
"v_mfma_f32_16x16x32_bf16 v[136:139], a[240:243], a[176:179], v[136:139]\n\t"
"ds_read_b64_tr_b16 v[234:235], v246 offset:56832\n\t"
"v_cvt_pk_bf16_f32 v224, v224, v225\n\t"
"v_mfma_f32_16x16x32_bf16 v[136:139], v[96:99], a[180:183], v[136:139]\n\t"
"buffer_load_dwordx4 a[252:255], off, s[80:83], s20 offset:0 sc1\n\t"
"v_mfma_f32_16x16x32_bf16 v[140:143], v[96:99], a[184:187], v[140:143]\n\t"
"v_cvt_pk_bf16_f32 v225, v226, v227\n\t"
"v_fma_f32 v13, v113, s52, v13\n\t"
"v_mfma_f32_16x16x32_bf16 v[140:143], a[240:243], a[180:183], v[140:143]\n\t"
"v_fma_f32 v14, v114, s52, v14\n\t"
"v_fma_f32 v15, v115, s52, v15\n\t"
"v_mfma_f32_16x16x32_bf16 v[140:143], a[236:239], a[176:179], v[140:143]\n\t"
"ds_write2st64_b64 v245, v[228:229], v[230:231] offset0:53 offset1:54\n\t"
"v_mfma_f32_16x16x32_bf16 v[144:147], a[236:239], a[180:183], v[144:147]\n\t"
"v_fma_f32 v112, v12, s54, -v112\n\t"
"v_fma_f32 v113, v13, s54, -v113\n\t"
"v_mfma_f32_16x16x32_bf16 v[144:147], a[240:243], a[184:187], v[144:147]\n\t"
"v_fma_f32 v114, v14, s54, -v114\n\t"
"v_fma_f32 v115, v15, s54, -v115\n\t"
"v_mfma_f32_16x16x32_bf16 v[144:147], v[96:99], a[188:191], v[144:147]\n\t"
"v_fma_f32 v16, v116, s52, v16\n\t"
"v_fma_f32 v17, v117, s52, v17\n\t"
"v_mfma_f32_16x16x32_bf16 v[148:151], a[240:243], a[188:191], v[148:151]\n\t"
"s_waitcnt lgkmcnt(1)\n\t"
"v_fma_f32 v18, v118, s52, v18\n\t"
"v_fma_f32 v19, v119, s52, v19\n\t"
"v_mfma_f32_16x16x32_bf16 v[148:151], v[96:99], a[192:195], v[148:151]\n\t"
"v_fma_f32 v116, v16, s54, -v116\n\t"
"v_fma_f32 v117, v17, s54, -v117\n\t"
"v_mfma_f32_16x16x32_bf16 v[148:151], a[236:239], a[184:187], v[148:151]\n\t"
"v_fma_f32 v118, v18, s54, -v118\n\t"
"v_fma_f32 v119, v19, s54, -v119\n\t"
"v_mfma_f32_16x16x32_bf16 v[232:235], v[232:235], v[220:223], 0\n\t"
"v_fma_f32 v20, v120, s52, v20\n\t"
"v_fma_f32 v21, v121, s52, v21\n\t"
"v_mfma_f32_16x16x32_bf16 v[152:155], a[236:239], a[188:191], v[152:155]\n\t"
"v_fma_f32 v22, v122, s52, v22\n\t"
"v_fma_f32 v23, v123, s52, v23\n\t"
"v_mfma_f32_16x16x32_bf16 v[152:155], v[96:99], a[196:199], v[152:155]\n\t"
"v_fma_f32 v120, v20, s54, -v120\n\t"
"v_fma_f32 v121, v21, s54, -v121\n\t"
"v_mfma_f32_16x16x32_bf16 v[152:155], a[240:243], a[192:195], v[152:155]\n\t"
"v_fma_f32 v122, v22, s54, -v122\n\t"
"v_fma_f32 v123, v23, s54, -v123\n\t"
"v_mfma_f32_16x16x32_bf16 v[156:159], a[236:239], a[192:195], v[156:159]\n\t"
"v_fma_f32 v24, v124, s52, v24\n\t"
"v_fma_f32 v25, v125, s52, v25\n\t"
"v_mfma_f32_16x16x32_bf16 v[156:159], a[240:243], a[196:199], v[156:159]\n\t"
"v_cvt_pk_bf16_f32 v226, v232, v233\n\t"
"v_cvt_pk_bf16_f32 v227, v234, v235\n\t"
"v_mfma_f32_16x16x32_bf16 v[156:159], v[96:99], a[200:203], v[156:159]\n\t"
"ds_write2st64_b64 v245, v[224:225], v[226:227] offset0:55 offset1:56\n\t"
"v_mfma_f32_16x16x32_bf16 v[160:163], a[240:243], a[200:203], v[160:163]\n\t"
"buffer_load_dwordx4 a[240:243], off, s[80:83], s20 offset:1024 sc1\n\t"
"v_mfma_f32_16x16x32_bf16 v[160:163], a[236:239], a[196:199], v[160:163]\n\t"
"s_waitcnt vmcnt(11)\n\t"
"v_fma_f32 v26, v126, s52, v26\n\t"
"v_fma_f32 v27, v127, s52, v27\n\t"
"v_mfma_f32_16x16x32_bf16 v[160:163], v[96:99], a[204:207], v[160:163]\n\t"
"v_fma_f32 v124, v24, s54, -v124\n\t"
"v_fma_f32 v125, v25, s54, -v125\n\t"
"v_mfma_f32_16x16x32_bf16 v[164:167], a[208:211], a[168:171], v[164:167]\n\t"
"v_fma_f32 v126, v26, s54, -v126\n\t"
"v_fma_f32 v127, v27, s54, -v127\n\t"
"v_mfma_f32_16x16x32_bf16 v[164:167], a[244:247], a[176:179], v[164:167]\n\t"
"v_fma_f32 v28, v128, s52, v28\n\t"
"v_mfma_f32_16x16x32_bf16 v[164:167], a[220:223], a[172:175], v[164:167]\n\t"
"s_waitcnt lgkmcnt(0)\n\t"
"s_barrier\n\t"
"v_mfma_f32_16x16x32_bf16 v[168:171], a[208:211], a[172:175], v[168:171]\n\t"
"ds_read_b128 a[168:171], v244 offset:24576\n\t"
"v_mfma_f32_16x16x32_bf16 v[168:171], a[220:223], a[176:179], v[168:171]\n\t"
"buffer_load_dwordx4 a[236:239], off, s[80:83], s20 offset:2048 sc1\n\t"
"v_mfma_f32_16x16x32_bf16 v[168:171], a[244:247], a[180:183], v[168:171]\n\t"
"Lpg22_%=:\n\t"
"s_cbranch_vccnz Lvr3_%=\n\t"
"ds_read_b128 a[172:175], v244 offset:25088\n\t"
"v_fma_f32 v29, v129, s52, v29\n\t"
"v_mfma_f32_16x16x32_bf16 v[172:175], a[244:247], a[184:187], v[172:175]\n\t"
"v_fma_f32 v30, v130, s52, v30\n\t"
"v_fma_f32 v31, v131, s52, v31\n\t"
"v_mfma_f32_16x16x32_bf16 v[172:175], a[220:223], a[180:183], v[172:175]\n\t"
"v_fma_f32 v128, v28, s54, -v128\n\t"
"v_fma_f32 v129, v29, s54, -v129\n\t"
"v_mfma_f32_16x16x32_bf16 v[172:175], a[208:211], a[176:179], v[172:175]\n\t"
"ds_read_b128 a[176:179], v244 offset:25600\n\t"
"v_fma_f32 v130, v30, s54, -v130\n\t"
"v_mfma_f32_16x16x32_bf16 v[176:179], a[208:211], a[180:183], v[176:179]\n\t"
"ds_read_b128 a[180:183], v244 offset:26112\n\t"
"v_fma_f32 v131, v31, s54, -v131\n\t"
"v_mfma_f32_16x16x32_bf16 v[176:179], a[220:223], a[184:187], v[176:179]\n\t"
"v_fma_f32 v32, v132, s52, v32\n\t"
"v_fma_f32 v33, v133, s52, v33\n\t"
"v_mfma_f32_16x16x32_bf16 v[176:179], a[244:247], a[188:191], v[176:179]\n\t"
"v_fma_f32 v34, v134, s52, v34\n\t"
"v_fma_f32 v35, v135, s52, v35\n\t"
"v_mfma_f32_16x16x32_bf16 v[180:183], a[244:247], a[192:195], v[180:183]\n\t"
"v_fma_f32 v132, v32, s54, -v132\n\t"
"v_fma_f32 v133, v33, s54, -v133\n\t"
"v_mfma_f32_16x16x32_bf16 v[180:183], a[220:223], a[188:191], v[180:183]\n\t"
"v_fma_f32 v134, v34, s54, -v134\n\t"
"v_fma_f32 v135, v35, s54, -v135\n\t"
"v_mfma_f32_16x16x32_bf16 v[180:183], a[208:211], a[184:187], v[180:183]\n\t"
"ds_read_b128 a[184:187], v244 offset:26624\n\t"
"v_fma_f32 v36, v136, s52, v36\n\t"
"v_mfma_f32_16x16x32_bf16 v[184:187], a[208:211], a[188:191], v[184:187]\n\t"
"s_mov_b32 s20, 1634304\n\t"
"ds_read_b128 a[188:191], v244 offset:27136\n\t"
"v_fma_f32 v37, v137, s52, v37\n\t"
"v_mfma_f32_16x16x32_bf16 v[184:187], a[220:223], a[192:195], v[184:187]\n\t"
"v_fma_f32 v38, v138, s52, v38\n\t"
"v_fma_f32 v39, v139, s52, v39\n\t"
"v_mfma_f32_16x16x32_bf16 v[184:187], a[244:247], a[196:199], v[184:187]\n\t"
"v_fma_f32 v136, v36, s54, -v136\n\t"
"v_fma_f32 v137, v37, s54, -v137\n\t"
"v_mfma_f32_16x16x32_bf16 v[188:191], a[244:247], a[200:203], v[188:191]\n\t"
"v_fma_f32 v138, v38, s54, -v138\n\t"
"v_fma_f32 v139, v39, s54, -v139\n\t"
"v_mfma_f32_16x16x32_bf16 v[188:191], a[220:223], a[196:199], v[188:191]\n\t"
"v_fma_f32 v40, v140, s52, v40\n\t"
"v_fma_f32 v41, v141, s52, v41\n\t"
"v_mfma_f32_16x16x32_bf16 v[188:191], a[208:211], a[192:195], v[188:191]\n\t"
"ds_read_b128 a[192:195], v244 offset:27648\n\t"
"v_fma_f32 v42, v142, s52, v42\n\t"
"v_mfma_f32_16x16x32_bf16 v[192:195], a[208:211], a[196:199], v[192:195]\n\t"
"s_waitcnt vmcnt(9) lgkmcnt(4)\n\t"
"ds_read_b128 a[196:199], v244 offset:28160\n\t"
"v_fma_f32 v43, v143, s52, v43\n\t"
"v_mfma_f32_16x16x32_bf16 v[192:195], a[220:223], a[200:203], v[192:195]\n\t"
"ds_read_b128 a[200:203], v244 offset:28672\n\t"
"v_fma_f32 v140, v40, s54, -v140\n\t"
"v_mfma_f32_16x16x32_bf16 v[192:195], a[244:247], a[204:207], v[192:195]\n\t"
"ds_read_b128 a[204:207], v244 offset:29184\n\t"
"v_fma_f32 v141, v41, s54, -v141\n\t"
"Lst31_%=:\n\t"
"v_mfma_f32_16x16x32_bf16 v[196:199], v[240:243], a[168:171], v[196:199]\n\t"
"s_waitcnt lgkmcnt(6)\n\t"
"v_fma_f32 v142, v42, s54, -v142\n\t"
"v_fma_f32 v143, v43, s54, -v143\n\t"
"v_mfma_f32_16x16x32_bf16 v[196:199], v[248:251], a[176:179], v[196:199]\n\t"
"v_fma_f32 v44, v144, s52, v44\n\t"
"v_fma_f32 v45, v145, s52, v45\n\t"
"v_mfma_f32_16x16x32_bf16 v[196:199], v[236:239], a[172:175], v[196:199]\n\t"
"buffer_load_dwordx4 a[208:211], off, s[80:83], s20 offset:0 sc1\n\t"
"v_mfma_f32_16x16x32_bf16 v[204:207], v[240:243], a[172:175], v[204:207]\n\t"
"buffer_load_dwordx4 a[220:223], off, s[80:83], s20 offset:1024 sc1\n\t"
"v_mfma_f32_16x16x32_bf16 v[204:207], v[236:239], a[176:179], v[204:207]\n\t"
"buffer_load_dwordx4 a[244:247], off, s[80:83], s20 offset:2048 sc1\n\t"
"v_mfma_f32_16x16x32_bf16 v[204:207], v[248:251], a[180:183], v[204:207]\n\t"
"s_waitcnt lgkmcnt(5)\n\t"
"v_fma_f32 v46, v146, s52, v46\n\t"
"v_fma_f32 v47, v147, s52, v47\n\t"
"v_mfma_f32_16x16x32_bf16 v[212:215], v[248:251], a[184:187], v[212:215]\n\t"
"s_waitcnt lgkmcnt(4)\n\t"
"v_fma_f32 v144, v44, s54, -v144\n\t"
"v_fma_f32 v145, v45, s54, -v145\n\t"
"v_mfma_f32_16x16x32_bf16 v[212:215], v[236:239], a[180:183], v[212:215]\n\t"
"v_fma_f32 v146, v46, s54, -v146\n\t"
"v_fma_f32 v147, v47, s54, -v147\n\t"
"v_mfma_f32_16x16x32_bf16 v[212:215], v[240:243], a[176:179], v[212:215]\n\t"
"v_fma_f32 v48, v148, s52, v48\n\t"
"v_fma_f32 v49, v149, s52, v49\n\t"
"v_mfma_f32_16x16x32_bf16 a[0:3], v[240:243], a[180:183], a[0:3]\n\t"
"s_waitcnt lgkmcnt(3)\n\t"
"v_fma_f32 v50, v150, s52, v50\n\t"
"v_fma_f32 v51, v151, s52, v51\n\t"
"v_mfma_f32_16x16x32_bf16 a[0:3], v[236:239], a[184:187], a[0:3]\n\t"
"v_fma_f32 v148, v48, s54, -v148\n\t"
"v_fma_f32 v149, v49, s54, -v149\n\t"
"v_mfma_f32_16x16x32_bf16 a[0:3], v[248:251], a[188:191], a[0:3]\n\t"
"v_fma_f32 v150, v50, s54, -v150\n\t"
"v_fma_f32 v151, v51, s54, -v151\n\t"
"v_mfma_f32_16x16x32_bf16 a[8:11], v[248:251], a[192:195], a[8:11]\n\t"
"s_waitcnt lgkmcnt(2)\n\t"
"v_fma_f32 v52, v152, s52, v52\n\t"
"v_fma_f32 v53, v153, s52, v53\n\t"
"v_mfma_f32_16x16x32_bf16 a[8:11], v[236:239], a[188:191], a[8:11]\n\t"
"v_fma_f32 v54, v154, s52, v54\n\t"
"v_fma_f32 v55, v155, s52, v55\n\t"
"v_mfma_f32_16x16x32_bf16 a[8:11], v[240:243], a[184:187], a[8:11]\n\t"
"v_fma_f32 v152, v52, s54, -v152\n\t"
"v_fma_f32 v153, v53, s54, -v153\n\t"
"v_mfma_f32_16x16x32_bf16 a[16:19], v[240:243], a[188:191], a[16:19]\n\t"
"s_waitcnt lgkmcnt(1)\n\t"
"v_fma_f32 v154, v54, s54, -v154\n\t"
"v_fma_f32 v155, v55, s54, -v155\n\t"
"v_mfma_f32_16x16x32_bf16 a[16:19], v[236:239], a[192:195], a[16:19]\n\t"
"s_mov_b32 s20, 1646592\n\t"
"v_fma_f32 v56, v156, s52, v56\n\t"
"v_fma_f32 v57, v157, s52, v57\n\t"
"v_mfma_f32_16x16x32_bf16 a[16:19], v[248:251], a[196:199], a[16:19]\n\t"
"v_fma_f32 v58, v158, s52, v58\n\t"
"v_fma_f32 v59, v159, s52, v59\n\t"
"v_mfma_f32_16x16x32_bf16 a[24:27], v[248:251], a[200:203], a[24:27]\n\t"
"s_waitcnt lgkmcnt(0)\n\t"
"v_fma_f32 v156, v56, s54, -v156\n\t"
"v_fma_f32 v157, v57, s54, -v157\n\t"
"v_mfma_f32_16x16x32_bf16 a[24:27], v[236:239], a[196:199], a[24:27]\n\t"
"v_fma_f32 v158, v58, s54, -v158\n\t"
"v_fma_f32 v159, v59, s54, -v159\n\t"
"v_mfma_f32_16x16x32_bf16 a[24:27], v[240:243], a[192:195], a[24:27]\n\t"
"v_fma_f32 v60, v160, s52, v60\n\t"
"v_fma_f32 v61, v161, s52, v61\n\t"
"v_mfma_f32_16x16x32_bf16 a[32:35], v[240:243], a[196:199], a[32:35]\n\t"
"s_waitcnt vmcnt(9)\n\t"
"v_fma_f32 v62, v162, s52, v62\n\t"
"v_fma_f32 v63, v163, s52, v63\n\t"
"v_mfma_f32_16x16x32_bf16 a[32:35], v[236:239], a[200:203], a[32:35]\n\t"
"v_fma_f32 v160, v60, s54, -v160\n\t"
"v_fma_f32 v161, v61, s54, -v161\n\t"
"v_mfma_f32_16x16x32_bf16 a[32:35], v[248:251], a[204:207], a[32:35]\n\t"
"v_fma_f32 v162, v62, s54, -v162\n\t"
"v_fma_f32 v163, v63, s54, -v163\n\t"
"v_mfma_f32_16x16x32_bf16 a[40:43], a[248:251], a[168:171], a[40:43]\n\t"
"v_fma_f32 v64, v164, s52, v64\n\t"
"v_fma_f32 v65, v165, s52, v65\n\t"
"v_mfma_f32_16x16x32_bf16 a[40:43], a[212:215], a[176:179], a[40:43]\n\t"
"v_fma_f32 v66, v166, s52, v66\n\t"
"v_fma_f32 v67, v167, s52, v67\n\t"
"v_mfma_f32_16x16x32_bf16 a[40:43], a[228:231], a[172:175], a[40:43]\n\t"
"buffer_load_dwordx4 v[240:243], off, s[80:83], s20 offset:0 sc1\n\t"
"v_mfma_f32_16x16x32_bf16 a[48:51], a[248:251], a[172:175], a[48:51]\n\t"
"v_fma_f32 v164, v64, s54, -v164\n\t"
"v_fma_f32 v165, v65, s54, -v165\n\t"
"v_mfma_f32_16x16x32_bf16 a[48:51], a[228:231], a[176:179], a[48:51]\n\t"
"v_fma_f32 v166, v66, s54, -v166\n\t"
"v_fma_f32 v167, v67, s54, -v167\n\t"
"v_mfma_f32_16x16x32_bf16 a[48:51], a[212:215], a[180:183], a[48:51]\n\t"
"v_fma_f32 v68, v168, s52, v68\n\t"
"v_fma_f32 v69, v169, s52, v69\n\t"
"v_mfma_f32_16x16x32_bf16 a[56:59], a[212:215], a[184:187], a[56:59]\n\t"
"v_fma_f32 v70, v170, s52, v70\n\t"
"v_fma_f32 v71, v171, s52, v71\n\t"
"v_mfma_f32_16x16x32_bf16 a[56:59], a[228:231], a[180:183], a[56:59]\n\t"
"v_fma_f32 v168, v68, s54, -v168\n\t"
"v_fma_f32 v169, v69, s54, -v169\n\t"
"v_mfma_f32_16x16x32_bf16 a[56:59], a[248:251], a[176:179], a[56:59]\n\t"
"v_fma_f32 v170, v70, s54, -v170\n\t"
"v_fma_f32 v171, v71, s54, -v171\n\t"
"v_mfma_f32_16x16x32_bf16 a[64:67], a[248:251], a[180:183], a[64:67]\n\t"
"v_fma_f32 v72, v172, s52, v72\n\t"
"v_fma_f32 v73, v173, s52, v73\n\t"
"v_mfma_f32_16x16x32_bf16 a[64:67], a[228:231], a[184:187], a[64:67]\n\t"
"v_fma_f32 v74, v174, s52, v74\n\t"
"v_fma_f32 v75, v175, s52, v75\n\t"
"v_mfma_f32_16x16x32_bf16 a[64:67], a[212:215], a[188:191], a[64:67]\n\t"
"v_fma_f32 v172, v72, s54, -v172\n\t"
"v_fma_f32 v173, v73, s54, -v173\n\t"
"v_mfma_f32_16x16x32_bf16 a[72:75], a[212:215], a[192:195], a[72:75]\n\t"
"v_fma_f32 v174, v74, s54, -v174\n\t"
"v_fma_f32 v175, v75, s54, -v175\n\t"
"v_mfma_f32_16x16x32_bf16 a[72:75], a[228:231], a[188:191], a[72:75]\n\t"
"v_fma_f32 v76, v176, s52, v76\n\t"
"v_fma_f32 v77, v177, s52, v77\n\t"
"v_mfma_f32_16x16x32_bf16 a[72:75], a[248:251], a[184:187], a[72:75]\n\t"
"v_fma_f32 v78, v178, s52, v78\n\t"
"v_fma_f32 v79, v179, s52, v79\n\t"
"v_mfma_f32_16x16x32_bf16 a[80:83], a[248:251], a[188:191], a[80:83]\n\t"
"v_fma_f32 v176, v76, s54, -v176\n\t"
"v_fma_f32 v177, v77, s54, -v177\n\t"
"v_mfma_f32_16x16x32_bf16 a[80:83], a[228:231], a[192:195], a[80:83]\n\t"
"v_fma_f32 v178, v78, s54, -v178\n\t"
"v_fma_f32 v179, v79, s54, -v179\n\t"
"v_mfma_f32_16x16x32_bf16 a[80:83], a[212:215], a[196:199], a[80:83]\n\t"
"v_fma_f32 v80, v180, s52, v80\n\t"
"v_fma_f32 v81, v181, s52, v81\n\t"
"v_mfma_f32_16x16x32_bf16 a[88:91], a[212:215], a[200:203], a[88:91]\n\t"
"v_fma_f32 v82, v182, s52, v82\n\t"
"v_fma_f32 v83, v183, s52, v83\n\t"
"v_mfma_f32_16x16x32_bf16 a[88:91], a[228:231], a[196:199], a[88:91]\n\t"
"v_fma_f32 v180, v80, s54, -v180\n\t"
"v_fma_f32 v181, v81, s54, -v181\n\t"
"v_mfma_f32_16x16x32_bf16 a[88:91], a[248:251], a[192:195], a[88:91]\n\t"
"v_fma_f32 v182, v82, s54, -v182\n\t"
"v_fma_f32 v183, v83, s54, -v183\n\t"
"v_mfma_f32_16x16x32_bf16 a[96:99], a[248:251], a[196:199], a[96:99]\n\t"
"s_waitcnt vmcnt(7)\n\t"
"v_fma_f32 v84, v184, s52, v84\n\t"
"v_fma_f32 v85, v185, s52, v85\n\t"
"v_mfma_f32_16x16x32_bf16 a[96:99], a[228:231], a[200:203], a[96:99]\n\t"
"v_fma_f32 v86, v186, s52, v86\n\t"
"v_fma_f32 v87, v187, s52, v87\n\t"
"v_mfma_f32_16x16x32_bf16 a[96:99], a[212:215], a[204:207], a[96:99]\n\t"
"v_fma_f32 v184, v84, s54, -v184\n\t"
"v_fma_f32 v185, v85, s54, -v185\n\t"
"v_mfma_f32_16x16x32_bf16 a[104:107], a[216:219], a[168:171], a[104:107]\n\t"
"ds_read_b128 a[168:171], v244 offset:33792\n\t"
"v_fma_f32 v186, v86, s54, -v186\n\t"
"v_mfma_f32_16x16x32_bf16 a[104:107], a[224:227], a[176:179], a[104:107]\n\t"
"v_fma_f32 v187, v87, s54, -v187\n\t"
"v_fma_f32 v88, v188, s52, v88\n\t"
"v_mfma_f32_16x16x32_bf16 a[104:107], a[232:235], a[172:175], a[104:107]\n\t"
"buffer_load_dwordx4 a[248:251], off, s[80:83], s20 offset:1024 sc1\n\t"
"v_mfma_f32_16x16x32_bf16 a[112:115], a[216:219], a[172:175], a[112:115]\n\t"
"buffer_load_dwordx4 a[212:215], off, s[80:83], s20 offset:2048 sc1\n\t"
"v_mfma_f32_16x16x32_bf16 a[112:115], a[232:235], a[176:179], a[112:115]\n\t"
"s_mov_b32 s20, 1179648\n\t"
"ds_read_b128 a[172:175], v244 offset:34304\n\t"
"v_fma_f32 v89, v189, s52, v89\n\t"
"v_mfma_f32_16x16x32_bf16 a[112:115], a[224:227], a[180:183], a[112:115]\n\t"
"v_fma_f32 v90, v190, s52, v90\n\t"
"v_fma_f32 v91, v191, s52, v91\n\t"
"v_mfma_f32_16x16x32_bf16 a[120:123], a[224:227], a[184:187], a[120:123]\n\t"
"v_fma_f32 v188, v88, s54, -v188\n\t"
"v_fma_f32 v189, v89, s54, -v189\n\t"
"v_mfma_f32_16x16x32_bf16 a[120:123], a[232:235], a[180:183], a[120:123]\n\t"
"v_fma_f32 v190, v90, s54, -v190\n\t"
"v_fma_f32 v191, v91, s54, -v191\n\t"
"v_mfma_f32_16x16x32_bf16 a[120:123], a[216:219], a[176:179], a[120:123]\n\t"
"buffer_load_dwordx4 a[228:231], off, s[80:83], s20 offset:0 sc1\n\t"
"v_mfma_f32_16x16x32_bf16 a[128:131], a[216:219], a[180:183], a[128:131]\n\t"
"ds_read_b128 a[176:179], v244 offset:34816\n\t"
"v_fma_f32 v92, v192, s52, v92\n\t"
"v_mfma_f32_16x16x32_bf16 a[128:131], a[232:235], a[184:187], a[128:131]\n\t"
"ds_read_b128 a[180:183], v244 offset:35328\n\t"
"v_fma_f32 v93, v193, s52, v93\n\t"
"v_mfma_f32_16x16x32_bf16 a[128:131], a[224:227], a[188:191], a[128:131]\n\t"
"v_fma_f32 v94, v194, s52, v94\n\t"
"v_fma_f32 v95, v195, s52, v95\n\t"
"v_mfma_f32_16x16x32_bf16 a[136:139], a[224:227], a[192:195], a[136:139]\n\t"
"v_fma_f32 v192, v92, s54, -v192\n\t"
"v_fma_f32 v193, v93, s54, -v193\n\t"
"v_mfma_f32_16x16x32_bf16 a[136:139], a[232:235], a[188:191], a[136:139]\n\t"
"v_fma_f32 v194, v94, s54, -v194\n\t"
"v_fma_f32 v195, v95, s54, -v195\n\t"
"v_mfma_f32_16x16x32_bf16 a[136:139], a[216:219], a[184:187], a[136:139]\n\t"
"ds_read_b128 a[184:187], v244 offset:35840\n\t"
"v_mfma_f32_16x16x32_bf16 a[144:147], a[216:219], a[188:191], a[144:147]\n\t"
"ds_read_b128 a[188:191], v244 offset:36352\n\t"
"v_mfma_f32_16x16x32_bf16 a[144:147], a[232:235], a[192:195], a[144:147]\n\t"
"v_mfma_f32_16x16x32_bf16 a[144:147], a[224:227], a[196:199], a[144:147]\n\t"
"v_mfma_f32_16x16x32_bf16 a[152:155], a[224:227], a[200:203], a[152:155]\n\t"
"v_mfma_f32_16x16x32_bf16 a[152:155], a[232:235], a[196:199], a[152:155]\n\t"
"v_mfma_f32_16x16x32_bf16 a[152:155], a[216:219], a[192:195], a[152:155]\n\t"
"ds_read_b128 a[192:195], v244 offset:36864\n\t"
"v_mfma_f32_16x16x32_bf16 a[160:163], a[216:219], a[196:199], a[160:163]\n\t"
"buffer_load_dwordx4 a[216:219], off, s[80:83], s20 offset:1024 sc1\n\t"
"v_mfma_f32_16x16x32_bf16 a[160:163], a[232:235], a[200:203], a[160:163]\n\t"
"s_waitcnt vmcnt(8) lgkmcnt(4)\n\t"
"ds_read_b128 a[196:199], v244 offset:37376\n\t"
"v_mfma_f32_16x16x32_bf16 a[160:163], a[224:227], a[204:207], a[160:163]\n\t"
"ds_read_b128 a[200:203], v244 offset:37888\n\t"
"v_mfma_f32_16x16x32_bf16 v[200:203], a[252:255], a[168:171], v[200:203]\n\t"
"s_waitcnt lgkmcnt(5)\n\t"
"ds_read_b128 a[204:207], v244 offset:38400\n\t"
"v_mfma_f32_16x16x32_bf16 v[200:203], a[236:239], a[176:179], v[200:203]\n\t"
"buffer_load_dwordx4 a[224:227], off, s[80:83], s20 offset:2048 sc1\n\t"
"v_mfma_f32_16x16x32_bf16 v[200:203], a[240:243], a[172:175], v[200:203]\n\t"
"s_mov_b32 s20, 1191936\n\t"
"v_mfma_f32_16x16x32_bf16 v[208:211], a[252:255], a[172:175], v[208:211]\n\t"
"buffer_load_dwordx4 a[232:235], off, s[80:83], s20 offset:0 sc1\n\t"
"v_mfma_f32_16x16x32_bf16 v[208:211], a[240:243], a[176:179], v[208:211]\n\t"
"s_waitcnt lgkmcnt(5)\n\t"
"v_mfma_f32_16x16x32_bf16 v[208:211], a[236:239], a[180:183], v[208:211]\n\t"
"s_waitcnt lgkmcnt(0)\n\t"
"s_barrier\n\t"
"v_mfma_f32_16x16x32_bf16 v[216:219], a[236:239], a[184:187], v[216:219]\n\t"
"v_mfma_f32_16x16x32_bf16 v[216:219], a[240:243], a[180:183], v[216:219]\n\t"
"v_mfma_f32_16x16x32_bf16 v[216:219], a[252:255], a[176:179], v[216:219]\n\t"
"v_pk_fma_f32 v[196:197], v[200:201], s[56:57], v[196:197]\n\t"
"v_pk_fma_f32 v[200:201], v[196:197], s[58:59], v[200:201] neg_lo:[0,0,1] neg_hi:[0,0,1]\n\t"
"v_pk_fma_f32 v[228:229], v[0:1], s[60:61], v[196:197]\n\t"
"v_pk_fma_f32 v[0:1], v[196:197], s[60:61], v[0:1]\n\t"
"v_pk_fma_f32 v[196:197], v[100:101], s[62:63], v[200:201]\n\t"
"v_pk_fma_f32 v[100:101], v[200:201], s[62:63], v[100:101]\n\t"
"v_pk_fma_f32 v[198:199], v[202:203], s[56:57], v[198:199]\n\t"
"v_pk_fma_f32 v[202:203], v[198:199], s[58:59], v[202:203] neg_lo:[0,0,1] neg_hi:[0,0,1]\n\t"
"v_pk_fma_f32 v[234:235], v[2:3], s[60:61], v[198:199]\n\t"
"v_pk_fma_f32 v[2:3], v[198:199], s[60:61], v[2:3]\n\t"
"v_pk_fma_f32 v[198:199], v[102:103], s[62:63], v[202:203]\n\t"
"v_pk_fma_f32 v[102:103], v[202:203], s[62:63], v[102:103]\n\t"
"v_mfma_f32_16x16x32_bf16 a[4:7], a[252:255], a[180:183], a[4:7]\n\t"
"v_cvt_pk_bf16_f32 v196, v196, v197\n\t"
"v_cvt_pk_bf16_f32 v200, v228, v229\n\t"
"v_mfma_f32_16x16x32_bf16 a[4:7], a[240:243], a[184:187], a[4:7]\n\t"
"v_cvt_pk_bf16_f32 v197, v198, v199\n\t"
"v_cvt_pk_bf16_f32 v201, v234, v235\n\t"
"v_mfma_f32_16x16x32_bf16 a[4:7], a[236:239], a[188:191], a[4:7]\n\t"
"v_pk_fma_f32 v[204:205], v[208:209], s[56:57], v[204:205]\n\t"
"v_pk_fma_f32 v[208:209], v[204:205], s[58:59], v[208:209] neg_lo:[0,0,1] neg_hi:[0,0,1]\n\t"
"v_pk_fma_f32 v[98:99], v[4:5], s[60:61], v[204:205]\n\t"
"v_pk_fma_f32 v[4:5], v[204:205], s[60:61], v[4:5]\n\t"
"v_pk_fma_f32 v[204:205], v[104:105], s[62:63], v[208:209]\n\t"
"v_pk_fma_f32 v[104:105], v[208:209], s[62:63], v[104:105]\n\t"
"v_mfma_f32_16x16x32_bf16 a[12:15], a[236:239], a[192:195], a[12:15]\n\t"
"v_cvt_pk_bf16_f32 v204, v204, v205\n\t"
"v_cvt_pk_bf16_f32 v208, v98, v99\n\t"
"v_mfma_f32_16x16x32_bf16 a[12:15], a[240:243], a[188:191], a[12:15]\n\t"
"v_mfma_f32_16x16x32_bf16 a[12:15], a[252:255], a[184:187], a[12:15]\n\t"
"v_pk_fma_f32 v[206:207], v[210:211], s[56:57], v[206:207]\n\t"
"v_pk_fma_f32 v[210:211], v[206:207], s[58:59], v[210:211] neg_lo:[0,0,1] neg_hi:[0,0,1]\n\t"
"v_pk_fma_f32 v[228:229], v[6:7], s[60:61], v[206:207]\n\t"
"v_pk_fma_f32 v[6:7], v[206:207], s[60:61], v[6:7]\n\t"
"v_pk_fma_f32 v[206:207], v[106:107], s[62:63], v[210:211]\n\t"
"v_pk_fma_f32 v[106:107], v[210:211], s[62:63], v[106:107]\n\t"
"v_mfma_f32_16x16x32_bf16 a[20:23], a[252:255], a[188:191], a[20:23]\n\t"
"v_cvt_pk_bf16_f32 v205, v206, v207\n\t"
"v_cvt_pk_bf16_f32 v209, v228, v229\n\t"
"v_mfma_f32_16x16x32_bf16 a[20:23], a[240:243], a[192:195], a[20:23]\n\t"
"v_accvgpr_read_b32 v224, a0\n\t"
"v_accvgpr_read_b32 v226, a4\n\t"
"v_mfma_f32_16x16x32_bf16 a[20:23], a[236:239], a[196:199], a[20:23]\n\t"
"v_accvgpr_read_b32 v225, a1\n\t"
"v_accvgpr_read_b32 v227, a5\n\t"
"v_pk_fma_f32 v[212:213], v[216:217], s[56:57], v[212:213]\n\t"
"v_pk_fma_f32 v[216:217], v[212:213], s[58:59], v[216:217] neg_lo:[0,0,1] neg_hi:[0,0,1]\n\t"
"v_pk_fma_f32 v[234:235], v[8:9], s[60:61], v[212:213]\n\t"
"v_pk_fma_f32 v[8:9], v[212:213], s[60:61], v[8:9]\n\t"
"v_pk_fma_f32 v[212:213], v[108:109], s[62:63], v[216:217]\n\t"
"v_pk_fma_f32 v[108:109], v[216:217], s[62:63], v[108:109]\n\t"
"v_mfma_f32_16x16x32_bf16 a[28:31], a[236:239], a[200:203], a[28:31]\n\t"
"v_cvt_pk_bf16_f32 v212, v212, v213\n\t"
"v_cvt_pk_bf16_f32 v216, v234, v235\n\t"
"v_mfma_f32_16x16x32_bf16 a[28:31], a[240:243], a[196:199], a[28:31]\n\t"
"v_accvgpr_read_b32 v230, a2\n\t"
"v_accvgpr_read_b32 v232, a6\n\t"
"v_mfma_f32_16x16x32_bf16 a[28:31], a[252:255], a[192:195], a[28:31]\n\t"
"v_accvgpr_read_b32 v231, a3\n\t"
"v_accvgpr_read_b32 v233, a7\n\t"
"v_pk_fma_f32 v[214:215], v[218:219], s[56:57], v[214:215]\n\t"
"v_pk_fma_f32 v[218:219], v[214:215], s[58:59], v[218:219] neg_lo:[0,0,1] neg_hi:[0,0,1]\n\t"
"v_pk_fma_f32 v[98:99], v[10:11], s[60:61], v[214:215]\n\t"
"v_pk_fma_f32 v[10:11], v[214:215], s[60:61], v[10:11]\n\t"
"v_pk_fma_f32 v[214:215], v[110:111], s[62:63], v[218:219]\n\t"
"v_pk_fma_f32 v[110:111], v[218:219], s[62:63], v[110:111]\n\t"
"v_mfma_f32_16x16x32_bf16 a[36:39], a[252:255], a[196:199], a[36:39]\n\t"
"s_waitcnt vmcnt(7)\n\t"
"v_cvt_pk_bf16_f32 v213, v214, v215\n\t"
"v_cvt_pk_bf16_f32 v217, v98, v99\n\t"
"v_mfma_f32_16x16x32_bf16 a[36:39], a[240:243], a[200:203], a[36:39]\n\t"
"v_accvgpr_read_b32 v236, a8\n\t"
"v_accvgpr_read_b32 v238, a12\n\t"
"v_mfma_f32_16x16x32_bf16 a[36:39], a[236:239], a[204:207], a[36:39]\n\t"
"buffer_load_dwordx4 a[252:255], off, s[80:83], s20 offset:1024 sc1\n\t"
"v_accvgpr_read_b32 v237, a9\n\t"
"v_accvgpr_read_b32 v239, a13\n\t"
"v_pk_fma_f32 v[224:225], v[226:227], s[56:57], v[224:225]\n\t"
"v_pk_fma_f32 v[226:227], v[224:225], s[58:59], v[226:227] neg_lo:[0,0,1] neg_hi:[0,0,1]\n\t"
"v_pk_fma_f32 v[228:229], v[12:13], s[60:61], v[224:225]\n\t"
"v_pk_fma_f32 v[12:13], v[224:225], s[60:61], v[12:13]\n\t"
"v_pk_fma_f32 v[224:225], v[112:113], s[62:63], v[226:227]\n\t"
"v_pk_fma_f32 v[112:113], v[226:227], s[62:63], v[112:113]\n\t"
"v_mfma_f32_16x16x32_bf16 a[44:47], a[208:211], a[168:171], a[44:47]\n\t"
"v_cvt_pk_bf16_f32 v224, v224, v225\n\t"
"v_cvt_pk_bf16_f32 v228, v228, v229\n\t"
"v_mfma_f32_16x16x32_bf16 a[44:47], a[244:247], a[176:179], a[44:47]\n\t"
"v_accvgpr_write_b32 a0, v224\n\t"
"v_accvgpr_write_b32 a4, v228\n\t"
"v_mfma_f32_16x16x32_bf16 a[44:47], a[220:223], a[172:175], a[44:47]\n\t"
"buffer_load_dwordx4 a[236:239], off, s[80:83], s20 offset:2048 sc1\n\t"
"v_accvgpr_read_b32 v224, a10\n\t"
"v_accvgpr_read_b32 v226, a14\n\t"
"s_mov_b32 s20, 1204224\n\t"
"v_pk_fma_f32 v[230:231], v[232:233], s[56:57], v[230:231]\n\t"
"v_pk_fma_f32 v[232:233], v[230:231], s[58:59], v[232:233] neg_lo:[0,0,1] neg_hi:[0,0,1]\n\t"
"v_pk_fma_f32 v[234:235], v[14:15], s[60:61], v[230:231]\n\t"
"v_pk_fma_f32 v[14:15], v[230:231], s[60:61], v[14:15]\n\t"
"v_pk_fma_f32 v[230:231], v[114:115], s[62:63], v[232:233]\n\t"
"v_pk_fma_f32 v[114:115], v[232:233], s[62:63], v[114:115]\n\t"
"v_mfma_f32_16x16x32_bf16 a[52:55], a[208:211], a[172:175], a[52:55]\n\t"
"v_cvt_pk_bf16_f32 v230, v230, v231\n\t"
"v_cvt_pk_bf16_f32 v234, v234, v235\n\t"
"v_mfma_f32_16x16x32_bf16 a[52:55], a[220:223], a[176:179], a[52:55]\n\t"
"v_accvgpr_write_b32 a1, v230\n\t"
"v_accvgpr_write_b32 a5, v234\n\t"
"v_mfma_f32_16x16x32_bf16 a[52:55], a[244:247], a[180:183], a[52:55]\n\t"
"buffer_load_dwordx4 a[240:243], off, s[80:83], s20 offset:0 sc1\n\t"
"v_accvgpr_read_b32 v225, a11\n\t"
"v_accvgpr_read_b32 v227, a15\n\t"
"v_pk_fma_f32 v[236:237], v[238:239], s[56:57], v[236:237]\n\t"
"v_pk_fma_f32 v[238:239], v[236:237], s[58:59], v[238:239] neg_lo:[0,0,1] neg_hi:[0,0,1]\n\t"
"v_pk_fma_f32 v[98:99], v[16:17], s[60:61], v[236:237]\n\t"
"v_pk_fma_f32 v[16:17], v[236:237], s[60:61], v[16:17]\n\t"
"v_pk_fma_f32 v[236:237], v[116:117], s[62:63], v[238:239]\n\t"
"v_pk_fma_f32 v[116:117], v[238:239], s[62:63], v[116:117]\n\t"
"v_mfma_f32_16x16x32_bf16 a[60:63], a[244:247], a[184:187], a[60:63]\n\t"
"v_accvgpr_read_b32 v230, a16\n\t"
"v_accvgpr_read_b32 v232, a20\n\t"
"v_mfma_f32_16x16x32_bf16 a[60:63], a[220:223], a[180:183], a[60:63]\n\t"
"v_accvgpr_read_b32 v231, a17\n\t"
"v_accvgpr_read_b32 v233, a21\n\t"
"v_mfma_f32_16x16x32_bf16 a[60:63], a[208:211], a[176:179], a[60:63]\n\t"
"v_cvt_pk_bf16_f32 v236, v236, v237\n\t"
"v_cvt_pk_bf16_f32 v98, v98, v99\n\t"
"v_accvgpr_write_b32 a8, v236\n\t"
"v_accvgpr_write_b32 a12, v98\n\t"
"v_accvgpr_read_b32 v236, a18\n\t"
"v_pk_fma_f32 v[224:225], v[226:227], s[56:57], v[224:225]\n\t"
"v_pk_fma_f32 v[226:227], v[224:225], s[58:59], v[226:227] neg_lo:[0,0,1] neg_hi:[0,0,1]\n\t"
"v_pk_fma_f32 v[228:229], v[18:19], s[60:61], v[224:225]\n\t"
"v_pk_fma_f32 v[18:19], v[224:225], s[60:61], v[18:19]\n\t"
"v_pk_fma_f32 v[224:225], v[118:119], s[62:63], v[226:227]\n\t"
"v_pk_fma_f32 v[118:119], v[226:227], s[62:63], v[118:119]\n\t"
"v_pk_fma_f32 v[230:231], v[232:233], s[56:57], v[230:231]\n\t"
"v_pk_fma_f32 v[232:233], v[230:231], s[58:59], v[232:233] neg_lo:[0,0,1] neg_hi:[0,0,1]\n\t"
"v_pk_fma_f32 v[234:235], v[20:21], s[60:61], v[230:231]\n\t"
"v_pk_fma_f32 v[20:21], v[230:231], s[60:61], v[20:21]\n\t"
"v_pk_fma_f32 v[230:231], v[120:121], s[62:63], v[232:233]\n\t"
"v_pk_fma_f32 v[120:121], v[232:233], s[62:63], v[120:121]\n\t"
"v_mfma_f32_16x16x32_bf16 a[68:71], a[208:211], a[180:183], a[68:71]\n\t"
"v_accvgpr_read_b32 v238, a22\n\t"
"v_accvgpr_read_b32 v237, a19\n\t"
"v_mfma_f32_16x16x32_bf16 a[68:71], a[220:223], a[184:187], a[68:71]\n\t"
"v_accvgpr_read_b32 v239, a23\n\t"
"v_cvt_pk_bf16_f32 v224, v224, v225\n\t"
"v_mfma_f32_16x16x32_bf16 a[68:71], a[244:247], a[188:191], a[68:71]\n\t"
"v_cvt_pk_bf16_f32 v228, v228, v229\n\t"
"v_cvt_pk_bf16_f32 v230, v230, v231\n\t"
"v_cvt_pk_bf16_f32 v234, v234, v235\n\t"
"v_accvgpr_write_b32 a9, v224\n\t"
"v_accvgpr_write_b32 a13, v228\n\t"
"v_pk_fma_f32 v[236:237], v[238:239], s[56:57], v[236:237]\n\t"
"v_pk_fma_f32 v[238:239], v[236:237], s[58:59], v[238:239] neg_lo:[0,0,1] neg_hi:[0,0,1]\n\t"
"v_pk_fma_f32 v[98:99], v[22:23], s[60:61], v[236:237]\n\t"
"v_pk_fma_f32 v[22:23], v[236:237], s[60:61], v[22:23]\n\t"
"v_pk_fma_f32 v[236:237], v[122:123], s[62:63], v[238:239]\n\t"
"v_pk_fma_f32 v[122:123], v[238:239], s[62:63], v[122:123]\n\t"
"v_mfma_f32_16x16x32_bf16 a[76:79], a[244:247], a[192:195], a[76:79]\n\t"
"v_accvgpr_write_b32 a16, v230\n\t"
"v_accvgpr_write_b32 a20, v234\n\t"
"v_mfma_f32_16x16x32_bf16 a[76:79], a[220:223], a[188:191], a[76:79]\n\t"
"v_accvgpr_read_b32 v224, a24\n\t"
"v_accvgpr_read_b32 v226, a28\n\t"
"v_mfma_f32_16x16x32_bf16 a[76:79], a[208:211], a[184:187], a[76:79]\n\t"
"v_accvgpr_read_b32 v225, a25\n\t"
"v_accvgpr_read_b32 v227, a29\n\t"
"v_cvt_pk_bf16_f32 v236, v236, v237\n\t"
"v_cvt_pk_bf16_f32 v98, v98, v99\n\t"
"v_accvgpr_write_b32 a17, v236\n\t"
"v_pk_fma_f32 v[224:225], v[226:227], s[56:57], v[224:225]\n\t"
"v_pk_fma_f32 v[226:227], v[224:225], s[58:59], v[226:227] neg_lo:[0,0,1] neg_hi:[0,0,1]\n\t"
"v_pk_fma_f32 v[228:229], v[24:25], s[60:61], v[224:225]\n\t"
"v_pk_fma_f32 v[24:25], v[224:225], s[60:61], v[24:25]\n\t"
"v_pk_fma_f32 v[224:225], v[124:125], s[62:63], v[226:227]\n\t"
"v_pk_fma_f32 v[124:125], v[226:227], s[62:63], v[124:125]\n\t"
"v_mfma_f32_16x16x32_bf16 a[84:87], a[208:211], a[188:191], a[84:87]\n\t"
"v_accvgpr_write_b32 a21, v98\n\t"
"v_accvgpr_read_b32 v230, a26\n\t"
"v_mfma_f32_16x16x32_bf16 a[84:87], a[220:223], a[192:195], a[84:87]\n\t"
"v_accvgpr_read_b32 v232, a30\n\t"
"v_accvgpr_read_b32 v231, a27\n\t"
"v_mfma_f32_16x16x32_bf16 a[84:87], a[244:247], a[196:199], a[84:87]\n\t"
"v_accvgpr_read_b32 v233, a31\n\t"
"v_cvt_pk_bf16_f32 v224, v224, v225\n\t"
"v_cvt_pk_bf16_f32 v228, v228, v229\n\t"
"v_accvgpr_write_b32 a24, v224\n\t"
"v_accvgpr_write_b32 a28, v228\n\t"
"v_pk_fma_f32 v[230:231], v[232:233], s[56:57], v[230:231]\n\t"
"v_pk_fma_f32 v[232:233], v[230:231], s[58:59], v[232:233] neg_lo:[0,0,1] neg_hi:[0,0,1]\n\t"
"v_pk_fma_f32 v[234:235], v[26:27], s[60:61], v[230:231]\n\t"
"v_pk_fma_f32 v[26:27], v[230:231], s[60:61], v[26:27]\n\t"
"v_pk_fma_f32 v[230:231], v[126:127], s[62:63], v[232:233]\n\t"
"v_pk_fma_f32 v[126:127], v[232:233], s[62:63], v[126:127]\n\t"
"v_mfma_f32_16x16x32_bf16 a[92:95], a[244:247], a[200:203], a[92:95]\n\t"
"v_accvgpr_read_b32 v236, a32\n\t"
"v_accvgpr_read_b32 v238, a36\n\t"
"v_mfma_f32_16x16x32_bf16 a[92:95], a[220:223], a[196:199], a[92:95]\n\t"
"v_accvgpr_read_b32 v237, a33\n\t"
"v_accvgpr_read_b32 v239, a37\n\t"
"v_mfma_f32_16x16x32_bf16 a[92:95], a[208:211], a[192:195], a[92:95]\n\t"
"v_cvt_pk_bf16_f32 v230, v230, v231\n\t"
"v_cvt_pk_bf16_f32 v234, v234, v235\n\t"
"v_accvgpr_write_b32 a25, v230\n\t"
"v_accvgpr_write_b32 a29, v234\n\t"
"v_accvgpr_read_b32 v224, a34\n\t"
"v_pk_fma_f32 v[236:237], v[238:239], s[56:57], v[236:237]\n\t"
"v_pk_fma_f32 v[238:239], v[236:237], s[58:59], v[238:239] neg_lo:[0,0,1] neg_hi:[0,0,1]\n\t"
"v_pk_fma_f32 v[98:99], v[28:29], s[60:61], v[236:237]\n\t"
"v_pk_fma_f32 v[28:29], v[236:237], s[60:61], v[28:29]\n\t"
"v_pk_fma_f32 v[236:237], v[128:129], s[62:63], v[238:239]\n\t"
"v_pk_fma_f32 v[128:129], v[238:239], s[62:63], v[128:129]\n\t"
"v_mfma_f32_16x16x32_bf16 a[100:103], a[208:211], a[196:199], a[100:103]\n\t"
"s_waitcnt vmcnt(7)\n\t"
"v_accvgpr_read_b32 v226, a38\n\t"
"v_accvgpr_read_b32 v225, a35\n\t"
"v_mfma_f32_16x16x32_bf16 a[100:103], a[220:223], a[200:203], a[100:103]\n\t"
"v_accvgpr_read_b32 v227, a39\n\t"
"v_cvt_pk_bf16_f32 v236, v236, v237\n\t"
"v_mfma_f32_16x16x32_bf16 a[100:103], a[244:247], a[204:207], a[100:103]\n\t"
"buffer_load_dwordx4 a[208:211], off, s[80:83], s20 offset:1024 sc1\n\t"
"v_cvt_pk_bf16_f32 v98, v98, v99\n\t"
"v_accvgpr_write_b32 a32, v236\n\t"
"v_pk_fma_f32 v[224:225], v[226:227], s[56:57], v[224:225]\n\t"
"v_pk_fma_f32 v[226:227], v[224:225], s[58:59], v[226:227] neg_lo:[0,0,1] neg_hi:[0,0,1]\n\t"
"v_pk_fma_f32 v[228:229], v[30:31], s[60:61], v[224:225]\n\t"
"v_pk_fma_f32 v[30:31], v[224:225], s[60:61], v[30:31]\n\t"
"v_pk_fma_f32 v[224:225], v[130:131], s[62:63], v[226:227]\n\t"
"v_pk_fma_f32 v[130:131], v[226:227], s[62:63], v[130:131]\n\t"
"v_mfma_f32_16x16x32_bf16 a[108:111], v[240:243], a[168:171], a[108:111]\n\t"
"ds_read_b128 a[168:171], v255 offset:512\n\t"
"v_accvgpr_write_b32 a36, v98\n\t"
"v_mfma_f32_16x16x32_bf16 a[108:111], a[212:215], a[176:179], a[108:111]\n\t"
"v_accvgpr_read_b32 v230, a40\n\t"
"v_accvgpr_read_b32 v232, a44\n\t"
"v_mfma_f32_16x16x32_bf16 a[108:111], a[248:251], a[172:175], a[108:111]\n\t"
"buffer_load_dwordx4 a[220:223], off, s[80:83], s20 offset:2048 sc1\n\t"
"v_accvgpr_read_b32 v231, a41\n\t"
"v_accvgpr_read_b32 v233, a45\n\t"
"v_cvt_pk_bf16_f32 v224, v224, v225\n\t"
"v_cvt_pk_bf16_f32 v228, v228, v229\n\t"
"s_mov_b32 s20, 1363968\n\t"
"Lpg23_%=:\n\t"
"s_cbranch_vccnz Lvr3_%=\n\t"
"v_pk_fma_f32 v[230:231], v[232:233], s[56:57], v[230:231]\n\t"
"v_pk_fma_f32 v[232:233], v[230:231], s[58:59], v[232:233] neg_lo:[0,0,1] neg_hi:[0,0,1]\n\t"
"v_pk_fma_f32 v[234:235], v[32:33], s[60:61], v[230:231]\n\t"
"v_pk_fma_f32 v[32:33], v[230:231], s[60:61], v[32:33]\n\t"
"v_pk_fma_f32 v[230:231], v[132:133], s[62:63], v[232:233]\n\t"
"v_pk_fma_f32 v[132:133], v[232:233], s[62:63], v[132:133]\n\t"
"v_mfma_f32_16x16x32_bf16 a[116:119], v[240:243], a[172:175], a[116:119]\n\t"
"ds_read_b128 a[172:175], v255 offset:3584\n\t"
"v_accvgpr_write_b32 a33, v224\n\t"
"v_mfma_f32_16x16x32_bf16 a[116:119], a[248:251], a[176:179], a[116:119]\n\t"
"v_accvgpr_write_b32 a37, v228\n\t"
"v_accvgpr_read_b32 v236, a42\n\t"
"v_mfma_f32_16x16x32_bf16 a[116:119], a[212:215], a[180:183], a[116:119]\n\t"
"buffer_load_dwordx4 a[244:247], off, s[80:83], s20 offset:0 sc1\n\t"
"v_accvgpr_read_b32 v238, a46\n\t"
"v_accvgpr_read_b32 v237, a43\n\t"
"v_accvgpr_read_b32 v239, a47\n\t"
"v_pk_fma_f32 v[236:237], v[238:239], s[56:57], v[236:237]\n\t"
"v_pk_fma_f32 v[238:239], v[236:237], s[58:59], v[238:239] neg_lo:[0,0,1] neg_hi:[0,0,1]\n\t"
"v_pk_fma_f32 v[98:99], v[34:35], s[60:61], v[236:237]\n\t"
"v_pk_fma_f32 v[34:35], v[236:237], s[60:61], v[34:35]\n\t"
"v_pk_fma_f32 v[236:237], v[134:135], s[62:63], v[238:239]\n\t"
"v_pk_fma_f32 v[134:135], v[238:239], s[62:63], v[134:135]\n\t"
"v_mfma_f32_16x16x32_bf16 a[124:127], a[212:215], a[184:187], a[124:127]\n\t"
"v_cvt_pk_bf16_f32 v198, v230, v231\n\t"
"v_cvt_pk_bf16_f32 v202, v234, v235\n\t"
"v_mfma_f32_16x16x32_bf16 a[124:127], a[248:251], a[180:183], a[124:127]\n\t"
"v_accvgpr_read_b32 v224, a48\n\t"
"v_accvgpr_read_b32 v226, a52\n\t"
"v_mfma_f32_16x16x32_bf16 a[124:127], v[240:243], a[176:179], a[124:127]\n\t"
"global_load_dwordx4 v[248:251], v252, s[48:49] offset:0\n\t"
"ds_read_b128 a[176:179], v255 offset:6656\n\t"
"v_accvgpr_read_b32 v225, a49\n\t"
"v_accvgpr_read_b32 v227, a53\n\t"
"v_accvgpr_read_b32 v230, a50\n\t"
"v_accvgpr_read_b32 v232, a54\n\t"
"v_accvgpr_read_b32 v231, a51\n\t"
"v_accvgpr_read_b32 v233, a55\n\t"
"v_pk_fma_f32 v[224:225], v[226:227], s[56:57], v[224:225]\n\t"
"v_pk_fma_f32 v[226:227], v[224:225], s[58:59], v[226:227] neg_lo:[0,0,1] neg_hi:[0,0,1]\n\t"
"v_pk_fma_f32 v[228:229], v[36:37], s[60:61], v[224:225]\n\t"
"v_pk_fma_f32 v[36:37], v[224:225], s[60:61], v[36:37]\n\t"
"v_pk_fma_f32 v[224:225], v[136:137], s[62:63], v[226:227]\n\t"
"v_pk_fma_f32 v[136:137], v[226:227], s[62:63], v[136:137]\n\t"
"v_pk_fma_f32 v[230:231], v[232:233], s[56:57], v[230:231]\n\t"
"v_pk_fma_f32 v[232:233], v[230:231], s[58:59], v[232:233] neg_lo:[0,0,1] neg_hi:[0,0,1]\n\t"
"v_pk_fma_f32 v[234:235], v[38:39], s[60:61], v[230:231]\n\t"
"v_pk_fma_f32 v[38:39], v[230:231], s[60:61], v[38:39]\n\t"
"v_pk_fma_f32 v[230:231], v[138:139], s[62:63], v[232:233]\n\t"
"v_pk_fma_f32 v[138:139], v[232:233], s[62:63], v[138:139]\n\t"
"v_mfma_f32_16x16x32_bf16 a[132:135], v[240:243], a[180:183], a[132:135]\n\t"
"ds_read_b128 a[180:183], v255 offset:9728\n\t"
"v_cvt_pk_bf16_f32 v199, v236, v237\n\t"
"v_mfma_f32_16x16x32_bf16 a[132:135], a[248:251], a[184:187], a[132:135]\n\t"
"v_cvt_pk_bf16_f32 v203, v98, v99\n\t"
"v_accvgpr_read_b32 v236, a56\n\t"
"v_mfma_f32_16x16x32_bf16 a[132:135], a[212:215], a[188:191], a[132:135]\n\t"
"buffer_load_dwordx4 a[52:55], off, s[80:83], s20 offset:1024 sc1\n\t"
"v_accvgpr_read_b32 v238, a60\n\t"
"v_accvgpr_read_b32 v237, a57\n\t"
"v_accvgpr_read_b32 v239, a61\n\t"
"v_pk_fma_f32 v[236:237], v[238:239], s[56:57], v[236:237]\n\t"
"v_pk_fma_f32 v[238:239], v[236:237], s[58:59], v[238:239] neg_lo:[0,0,1] neg_hi:[0,0,1]\n\t"
"v_pk_fma_f32 v[98:99], v[40:41], s[60:61], v[236:237]\n\t"
"v_pk_fma_f32 v[40:41], v[236:237], s[60:61], v[40:41]\n\t"
"v_pk_fma_f32 v[236:237], v[140:141], s[62:63], v[238:239]\n\t"
"v_pk_fma_f32 v[140:141], v[238:239], s[62:63], v[140:141]\n\t"
"v_mfma_f32_16x16x32_bf16 a[140:143], a[212:215], a[192:195], a[140:143]\n\t"
"v_cvt_pk_bf16_f32 v206, v224, v225\n\t"
"v_cvt_pk_bf16_f32 v210, v228, v229\n\t"
"v_mfma_f32_16x16x32_bf16 a[140:143], a[248:251], a[188:191], a[140:143]\n\t"
"v_cvt_pk_bf16_f32 v207, v230, v231\n\t"
"v_cvt_pk_bf16_f32 v211, v234, v235\n\t"
"v_mfma_f32_16x16x32_bf16 a[140:143], v[240:243], a[184:187], a[140:143]\n\t"
"ds_read_b128 a[184:187], v255 offset:12800\n\t"
"v_accvgpr_read_b32 v224, a58\n\t"
"v_accvgpr_read_b32 v226, a62\n\t"
"v_accvgpr_read_b32 v225, a59\n\t"
"v_accvgpr_read_b32 v227, a63\n\t"
"v_pk_fma_f32 v[224:225], v[226:227], s[56:57], v[224:225]\n\t"
"v_pk_fma_f32 v[226:227], v[224:225], s[58:59], v[226:227] neg_lo:[0,0,1] neg_hi:[0,0,1]\n\t"
"v_pk_fma_f32 v[228:229], v[42:43], s[60:61], v[224:225]\n\t"
"v_pk_fma_f32 v[42:43], v[224:225], s[60:61], v[42:43]\n\t"
"v_pk_fma_f32 v[224:225], v[142:143], s[62:63], v[226:227]\n\t"
"v_pk_fma_f32 v[142:143], v[226:227], s[62:63], v[142:143]\n\t"
"v_mfma_f32_16x16x32_bf16 a[148:151], v[240:243], a[188:191], a[148:151]\n\t"
"ds_read_b128 a[188:191], v255 offset:15872\n\t"
"v_cvt_pk_bf16_f32 v214, v236, v237\n\t"
"v_mfma_f32_16x16x32_bf16 a[148:151], a[248:251], a[192:195], a[148:151]\n\t"
"v_cvt_pk_bf16_f32 v218, v98, v99\n\t"
"v_accvgpr_read_b32 v230, a64\n\t"
"v_mfma_f32_16x16x32_bf16 a[148:151], a[212:215], a[196:199], a[148:151]\n\t"
"buffer_load_dwordx4 a[56:59], off, s[80:83], s20 offset:2048 sc1\n\t"
"v_accvgpr_read_b32 v232, a68\n\t"
"v_accvgpr_read_b32 v231, a65\n\t"
"v_accvgpr_read_b32 v233, a69\n\t"
"v_pk_fma_f32 v[230:231], v[232:233], s[56:57], v[230:231]\n\t"
"v_pk_fma_f32 v[232:233], v[230:231], s[58:59], v[232:233] neg_lo:[0,0,1] neg_hi:[0,0,1]\n\t"
"v_pk_fma_f32 v[234:235], v[44:45], s[60:61], v[230:231]\n\t"
"v_pk_fma_f32 v[44:45], v[230:231], s[60:61], v[44:45]\n\t"
"v_pk_fma_f32 v[230:231], v[144:145], s[62:63], v[232:233]\n\t"
"v_pk_fma_f32 v[144:145], v[232:233], s[62:63], v[144:145]\n\t"
"v_mfma_f32_16x16x32_bf16 a[156:159], a[212:215], a[200:203], a[156:159]\n\t"
"s_mov_b32 s20, 1376256\n\t"
"v_cvt_pk_bf16_f32 v215, v224, v225\n\t"
"v_cvt_pk_bf16_f32 v219, v228, v229\n\t"
"v_mfma_f32_16x16x32_bf16 a[156:159], a[248:251], a[196:199], a[156:159]\n\t"
"v_accvgpr_read_b32 v236, a66\n\t"
"v_accvgpr_read_b32 v238, a70\n\t"
"v_mfma_f32_16x16x32_bf16 a[156:159], v[240:243], a[192:195], a[156:159]\n\t"
"ds_read_b128 a[192:195], v255 offset:18944\n\t"
"buffer_load_dwordx4 a[60:63], off, s[80:83], s20 offset:0 sc1\n\t"
"v_accvgpr_read_b32 v237, a67\n\t"
"v_accvgpr_read_b32 v239, a71\n\t"
"v_cvt_pk_bf16_f32 v230, v230, v231\n\t"
"v_cvt_pk_bf16_f32 v234, v234, v235\n\t"
"v_pk_fma_f32 v[236:237], v[238:239], s[56:57], v[236:237]\n\t"
"v_pk_fma_f32 v[238:239], v[236:237], s[58:59], v[238:239] neg_lo:[0,0,1] neg_hi:[0,0,1]\n\t"
"v_pk_fma_f32 v[98:99], v[46:47], s[60:61], v[236:237]\n\t"
"v_pk_fma_f32 v[46:47], v[236:237], s[60:61], v[46:47]\n\t"
"v_pk_fma_f32 v[236:237], v[146:147], s[62:63], v[238:239]\n\t"
"v_pk_fma_f32 v[146:147], v[238:239], s[62:63], v[146:147]\n\t"
"v_mfma_f32_16x16x32_bf16 a[164:167], v[240:243], a[196:199], a[164:167]\n\t"
"s_waitcnt vmcnt(11) lgkmcnt(4)\n\t"
"ds_read_b128 a[196:199], v255 offset:22016\n\t"
"v_accvgpr_write_b32 a2, v230\n\t"
"v_mfma_f32_16x16x32_bf16 a[164:167], a[248:251], a[200:203], a[164:167]\n\t"
"ds_read_b128 a[200:203], v255 offset:25088\n\t"
"v_accvgpr_write_b32 a6, v234\n\t"
"v_mfma_f32_16x16x32_bf16 a[164:167], a[212:215], a[204:207], a[164:167]\n\t"
"buffer_load_dwordx4 a[248:251], off, s[80:83], s20 offset:1024 sc1\n\t"
"ds_read_b128 a[204:207], v255 offset:28160\n\t"
"v_accvgpr_read_b32 v224, a72\n\t"
"v_accvgpr_read_b32 v226, a76\n\t"
"v_accvgpr_read_b32 v225, a73\n\t"
"v_accvgpr_read_b32 v227, a77\n\t"
"v_cvt_pk_bf16_f32 v236, v236, v237\n\t"
"v_cvt_pk_bf16_f32 v98, v98, v99\n\t"
"v_pk_fma_f32 v[224:225], v[226:227], s[56:57], v[224:225]\n\t"
"v_pk_fma_f32 v[226:227], v[224:225], s[58:59], v[226:227] neg_lo:[0,0,1] neg_hi:[0,0,1]\n\t"
"v_pk_fma_f32 v[228:229], v[48:49], s[60:61], v[224:225]\n\t"
"v_pk_fma_f32 v[48:49], v[224:225], s[60:61], v[48:49]\n\t"
"v_pk_fma_f32 v[224:225], v[148:149], s[62:63], v[226:227]\n\t"
"v_pk_fma_f32 v[148:149], v[226:227], s[62:63], v[148:149]\n\t"
"Lst32_%=:\n\t"
"v_mfma_f32_16x16x32_bf16 v[0:3], a[228:231], a[168:171], v[0:3]\n\t"
"s_waitcnt lgkmcnt(6)\n\t"
"v_accvgpr_write_b32 a3, v236\n\t"
"v_accvgpr_write_b32 a7, v98\n\t"
"v_mfma_f32_16x16x32_bf16 v[0:3], a[224:227], a[176:179], v[0:3]\n\t"
"v_accvgpr_read_b32 v230, a74\n\t"
"v_accvgpr_read_b32 v232, a78\n\t"
"v_mfma_f32_16x16x32_bf16 v[0:3], a[216:219], a[172:175], v[0:3]\n\t"
"buffer_load_dwordx4 a[212:215], off, s[80:83], s20 offset:2048 sc1\n\t"
"v_accvgpr_read_b32 v231, a75\n\t"
"v_accvgpr_read_b32 v233, a79\n\t"
"v_cvt_pk_bf16_f32 v224, v224, v225\n\t"
"v_cvt_pk_bf16_f32 v228, v228, v229\n\t"
"v_pk_fma_f32 v[230:231], v[232:233], s[56:57], v[230:231]\n\t"
"v_pk_fma_f32 v[232:233], v[230:231], s[58:59], v[232:233] neg_lo:[0,0,1] neg_hi:[0,0,1]\n\t"
"v_pk_fma_f32 v[234:235], v[50:51], s[60:61], v[230:231]\n\t"
"v_pk_fma_f32 v[50:51], v[230:231], s[60:61], v[50:51]\n\t"
"v_pk_fma_f32 v[230:231], v[150:151], s[62:63], v[232:233]\n\t"
"v_pk_fma_f32 v[150:151], v[232:233], s[62:63], v[150:151]\n\t"
"v_mfma_f32_16x16x32_bf16 v[4:7], a[228:231], a[172:175], v[4:7]\n\t"
"s_waitcnt lgkmcnt(5)\n\t"
"s_mov_b32 s20, 1388544\n\t"
"v_accvgpr_write_b32 a10, v224\n\t"
"v_mfma_f32_16x16x32_bf16 v[4:7], a[216:219], a[176:179], v[4:7]\n\t"
"v_accvgpr_write_b32 a14, v228\n\t"
"v_accvgpr_read_b32 v236, a80\n\t"
"v_mfma_f32_16x16x32_bf16 v[4:7], a[224:227], a[180:183], v[4:7]\n\t"
"buffer_load_dwordx4 a[64:67], off, s[80:83], s20 offset:0 sc1\n\t"
"v_accvgpr_read_b32 v238, a84\n\t"
"v_accvgpr_read_b32 v237, a81\n\t"
"v_accvgpr_read_b32 v239, a85\n\t"
"v_cvt_pk_bf16_f32 v230, v230, v231\n\t"
"v_cvt_pk_bf16_f32 v234, v234, v235\n\t"
"v_pk_fma_f32 v[236:237], v[238:239], s[56:57], v[236:237]\n\t"
"v_pk_fma_f32 v[238:239], v[236:237], s[58:59], v[238:239] neg_lo:[0,0,1] neg_hi:[0,0,1]\n\t"
"v_pk_fma_f32 v[98:99], v[52:53], s[60:61], v[236:237]\n\t"
"v_pk_fma_f32 v[52:53], v[236:237], s[60:61], v[52:53]\n\t"
"v_pk_fma_f32 v[236:237], v[152:153], s[62:63], v[238:239]\n\t"
"v_pk_fma_f32 v[152:153], v[238:239], s[62:63], v[152:153]\n\t"
"v_mfma_f32_16x16x32_bf16 v[8:11], a[224:227], a[184:187], v[8:11]\n\t"
"s_waitcnt lgkmcnt(4)\n\t"
"v_accvgpr_write_b32 a11, v230\n\t"
"v_accvgpr_write_b32 a15, v234\n\t"
"v_mfma_f32_16x16x32_bf16 v[8:11], a[216:219], a[180:183], v[8:11]\n\t"
"v_accvgpr_read_b32 v224, a82\n\t"
"v_accvgpr_read_b32 v226, a86\n\t"
"v_mfma_f32_16x16x32_bf16 v[8:11], a[228:231], a[176:179], v[8:11]\n\t"
"buffer_load_dwordx4 a[68:71], off, s[80:83], s20 offset:1024 sc1\n\t"
"v_accvgpr_read_b32 v225, a83\n\t"
"v_accvgpr_read_b32 v227, a87\n\t"
"v_cvt_pk_bf16_f32 v236, v236, v237\n\t"
"v_cvt_pk_bf16_f32 v98, v98, v99\n\t"
"v_pk_fma_f32 v[224:225], v[226:227], s[56:57], v[224:225]\n\t"
"v_pk_fma_f32 v[226:227], v[224:225], s[58:59], v[226:227] neg_lo:[0,0,1] neg_hi:[0,0,1]\n\t"
"v_pk_fma_f32 v[228:229], v[54:55], s[60:61], v[224:225]\n\t"
"v_pk_fma_f32 v[54:55], v[224:225], s[60:61], v[54:55]\n\t"
"v_pk_fma_f32 v[224:225], v[154:155], s[62:63], v[226:227]\n\t"
"v_pk_fma_f32 v[154:155], v[226:227], s[62:63], v[154:155]\n\t"
"v_mfma_f32_16x16x32_bf16 v[12:15], a[228:231], a[180:183], v[12:15]\n\t"
"s_waitcnt lgkmcnt(3)\n\t"
"v_accvgpr_write_b32 a18, v236\n\t"
"v_accvgpr_write_b32 a22, v98\n\t"
"v_mfma_f32_16x16x32_bf16 v[12:15], a[216:219], a[184:187], v[12:15]\n\t"
"v_accvgpr_read_b32 v230, a88\n\t"
"v_accvgpr_read_b32 v232, a92\n\t"
"v_mfma_f32_16x16x32_bf16 v[12:15], a[224:227], a[188:191], v[12:15]\n\t"
"buffer_load_dwordx4 a[72:75], off, s[80:83], s20 offset:2048 sc1\n\t"
"v_accvgpr_read_b32 v231, a89\n\t"
"v_accvgpr_read_b32 v233, a93\n\t"
"v_accvgpr_read_b32 v236, a90\n\t"
"v_accvgpr_read_b32 v238, a94\n\t"
"v_accvgpr_read_b32 v237, a91\n\t"
"v_accvgpr_read_b32 v239, a95\n\t"
"v_cvt_pk_bf16_f32 v224, v224, v225\n\t"
"v_cvt_pk_bf16_f32 v228, v228, v229\n\t"
"v_pk_fma_f32 v[230:231], v[232:233], s[56:57], v[230:231]\n\t"
"v_pk_fma_f32 v[232:233], v[230:231], s[58:59], v[232:233] neg_lo:[0,0,1] neg_hi:[0,0,1]\n\t"
"v_pk_fma_f32 v[234:235], v[56:57], s[60:61], v[230:231]\n\t"
"v_pk_fma_f32 v[56:57], v[230:231], s[60:61], v[56:57]\n\t"
"v_pk_fma_f32 v[230:231], v[156:157], s[62:63], v[232:233]\n\t"
"v_pk_fma_f32 v[156:157], v[232:233], s[62:63], v[156:157]\n\t"
"v_pk_fma_f32 v[236:237], v[238:239], s[56:57], v[236:237]\n\t"
"v_pk_fma_f32 v[238:239], v[236:237], s[58:59], v[238:239] neg_lo:[0,0,1] neg_hi:[0,0,1]\n\t"
"v_pk_fma_f32 v[98:99], v[58:59], s[60:61], v[236:237]\n\t"
"v_pk_fma_f32 v[58:59], v[236:237], s[60:61], v[58:59]\n\t"
"v_pk_fma_f32 v[236:237], v[158:159], s[62:63], v[238:239]\n\t"
"v_pk_fma_f32 v[158:159], v[238:239], s[62:63], v[158:159]\n\t"
"v_mfma_f32_16x16x32_bf16 v[16:19], a[224:227], a[192:195], v[16:19]\n\t"
"s_waitcnt lgkmcnt(2)\n\t"
"s_mov_b32 s20, 589824\n\t"
"v_accvgpr_write_b32 a19, v224\n\t"
"v_mfma_f32_16x16x32_bf16 v[16:19], a[216:219], a[188:191], v[16:19]\n\t"
"v_accvgpr_write_b32 a23, v228\n\t"
"v_accvgpr_read_b32 v224, a96\n\t"
"v_mfma_f32_16x16x32_bf16 v[16:19], a[228:231], a[184:187], v[16:19]\n\t"
"buffer_load_dwordx4 a[76:79], off, s[80:83], s20 offset:0 sc1\n\t"
"v_accvgpr_read_b32 v226, a100\n\t"
"v_accvgpr_read_b32 v225, a97\n\t"
"v_accvgpr_read_b32 v227, a101\n\t"
"v_cvt_pk_bf16_f32 v230, v230, v231\n\t"
"v_cvt_pk_bf16_f32 v234, v234, v235\n\t"
"v_pk_fma_f32 v[224:225], v[226:227], s[56:57], v[224:225]\n\t"
"v_pk_fma_f32 v[226:227], v[224:225], s[58:59], v[226:227] neg_lo:[0,0,1] neg_hi:[0,0,1]\n\t"
"v_pk_fma_f32 v[228:229], v[60:61], s[60:61], v[224:225]\n\t"
"v_pk_fma_f32 v[60:61], v[224:225], s[60:61], v[60:61]\n\t"
"v_pk_fma_f32 v[224:225], v[160:161], s[62:63], v[226:227]\n\t"
"v_pk_fma_f32 v[160:161], v[226:227], s[62:63], v[160:161]\n\t"
"v_mfma_f32_16x16x32_bf16 v[20:23], a[228:231], a[188:191], v[20:23]\n\t"
"s_waitcnt lgkmcnt(1)\n\t"
"v_accvgpr_write_b32 a26, v230\n\t"
"v_cvt_pk_bf16_f32 v236, v236, v237\n\t"
"v_mfma_f32_16x16x32_bf16 v[20:23], a[216:219], a[192:195], v[20:23]\n\t"
"v_accvgpr_write_b32 a30, v234\n\t"
"v_cvt_pk_bf16_f32 v98, v98, v99\n\t"
"v_mfma_f32_16x16x32_bf16 v[20:23], a[224:227], a[196:199], v[20:23]\n\t"
"buffer_load_dwordx4 a[80:83], off, s[80:83], s20 offset:1024 sc1\n\t"
"v_accvgpr_read_b32 v230, a98\n\t"
"v_accvgpr_read_b32 v232, a102\n\t"
"v_accvgpr_read_b32 v231, a99\n\t"
"v_accvgpr_read_b32 v233, a103\n\t"
"v_accvgpr_write_b32 a27, v236\n\t"
"v_accvgpr_write_b32 a31, v98\n\t"
"v_cvt_pk_bf16_f32 v224, v224, v225\n\t"
"v_cvt_pk_bf16_f32 v228, v228, v229\n\t"
"v_pk_fma_f32 v[230:231], v[232:233], s[56:57], v[230:231]\n\t"
"v_pk_fma_f32 v[232:233], v[230:231], s[58:59], v[232:233] neg_lo:[0,0,1] neg_hi:[0,0,1]\n\t"
"v_pk_fma_f32 v[234:235], v[62:63], s[60:61], v[230:231]\n\t"
"v_pk_fma_f32 v[62:63], v[230:231], s[60:61], v[62:63]\n\t"
"v_pk_fma_f32 v[230:231], v[162:163], s[62:63], v[232:233]\n\t"
"v_pk_fma_f32 v[162:163], v[232:233], s[62:63], v[162:163]\n\t"
"v_mfma_f32_16x16x32_bf16 v[24:27], a[224:227], a[200:203], v[24:27]\n\t"
"s_waitcnt lgkmcnt(0)\n\t"
"v_accvgpr_write_b32 a34, v224\n\t"
"v_accvgpr_write_b32 a38, v228\n\t"
"v_mfma_f32_16x16x32_bf16 v[24:27], a[216:219], a[196:199], v[24:27]\n\t"
"v_accvgpr_read_b32 v236, a104\n\t"
"v_accvgpr_read_b32 v238, a108\n\t"
"v_mfma_f32_16x16x32_bf16 v[24:27], a[228:231], a[192:195], v[24:27]\n\t"
"buffer_load_dwordx4 a[84:87], off, s[80:83], s20 offset:2048 sc1\n\t"
"v_accvgpr_read_b32 v237, a105\n\t"
"v_accvgpr_read_b32 v239, a109\n\t"
"v_cvt_pk_bf16_f32 v230, v230, v231\n\t"
"v_cvt_pk_bf16_f32 v234, v234, v235\n\t"
"v_pk_fma_f32 v[236:237], v[238:239], s[56:57], v[236:237]\n\t"
"v_pk_fma_f32 v[238:239], v[236:237], s[58:59], v[238:239] neg_lo:[0,0,1] neg_hi:[0,0,1]\n\t"
"v_pk_fma_f32 v[98:99], v[64:65], s[60:61], v[236:237]\n\t"
"v_pk_fma_f32 v[64:65], v[236:237], s[60:61], v[64:65]\n\t"
"v_pk_fma_f32 v[236:237], v[164:165], s[62:63], v[238:239]\n\t"
"v_pk_fma_f32 v[164:165], v[238:239], s[62:63], v[164:165]\n\t"
"v_mfma_f32_16x16x32_bf16 v[28:31], a[228:231], a[196:199], v[28:31]\n\t"
"s_waitcnt vmcnt(16)\n\t"
"s_mov_b32 s20, 602112\n\t"
"v_accvgpr_write_b32 a35, v230\n\t"
"v_mfma_f32_16x16x32_bf16 v[28:31], a[216:219], a[200:203], v[28:31]\n\t"
"v_accvgpr_write_b32 a39, v234\n\t"
"v_accvgpr_read_b32 v224, a106\n\t"
"v_mfma_f32_16x16x32_bf16 v[28:31], a[224:227], a[204:207], v[28:31]\n\t"
"buffer_load_dwordx4 a[216:219], off, s[80:83], s20 offset:0 sc1\n\t"
"v_accvgpr_read_b32 v226, a110\n\t"
"v_accvgpr_read_b32 v225, a107\n\t"
"v_accvgpr_read_b32 v227, a111\n\t"
"v_cvt_pk_bf16_f32 v236, v236, v237\n\t"
"v_cvt_pk_bf16_f32 v98, v98, v99\n\t"
"v_pk_fma_f32 v[224:225], v[226:227], s[56:57], v[224:225]\n\t"
"v_pk_fma_f32 v[226:227], v[224:225], s[58:59], v[226:227] neg_lo:[0,0,1] neg_hi:[0,0,1]\n\t"
"v_pk_fma_f32 v[228:229], v[66:67], s[60:61], v[224:225]\n\t"
"v_pk_fma_f32 v[66:67], v[224:225], s[60:61], v[66:67]\n\t"
"v_pk_fma_f32 v[224:225], v[166:167], s[62:63], v[226:227]\n\t"
"v_pk_fma_f32 v[166:167], v[226:227], s[62:63], v[166:167]\n\t"
"v_mfma_f32_16x16x32_bf16 v[32:35], a[232:235], a[168:171], v[32:35]\n\t"
"v_accvgpr_write_b32 a104, v236\n\t"
"v_accvgpr_write_b32 a106, v98\n\t"
"v_mfma_f32_16x16x32_bf16 v[32:35], a[236:239], a[176:179], v[32:35]\n\t"
"v_accvgpr_read_b32 v230, a112\n\t"
"v_accvgpr_read_b32 v232, a116\n\t"
"v_mfma_f32_16x16x32_bf16 v[32:35], a[252:255], a[172:175], v[32:35]\n\t"
"buffer_load_dwordx4 a[224:227], off, s[80:83], s20 offset:1024 sc1\n\t"
"v_accvgpr_read_b32 v231, a113\n\t"
"v_accvgpr_read_b32 v233, a117\n\t"
"v_cvt_pk_bf16_f32 v224, v224, v225\n\t"
"v_cvt_pk_bf16_f32 v228, v228, v229\n\t"
"v_pk_fma_f32 v[230:231], v[232:233], s[56:57], v[230:231]\n\t"
"v_pk_fma_f32 v[232:233], v[230:231], s[58:59], v[232:233] neg_lo:[0,0,1] neg_hi:[0,0,1]\n\t"
"v_pk_fma_f32 v[234:235], v[68:69], s[60:61], v[230:231]\n\t"
"v_pk_fma_f32 v[68:69], v[230:231], s[60:61], v[68:69]\n\t"
"v_pk_fma_f32 v[230:231], v[168:169], s[62:63], v[232:233]\n\t"
"v_pk_fma_f32 v[168:169], v[232:233], s[62:63], v[168:169]\n\t"
"v_mfma_f32_16x16x32_bf16 v[36:39], a[232:235], a[172:175], v[36:39]\n\t"
"v_accvgpr_write_b32 a105, v224\n\t"
"v_accvgpr_write_b32 a107, v228\n\t"
"v_mfma_f32_16x16x32_bf16 v[36:39], a[252:255], a[176:179], v[36:39]\n\t"
"v_accvgpr_read_b32 v236, a114\n\t"
"v_accvgpr_read_b32 v238, a118\n\t"
"v_mfma_f32_16x16x32_bf16 v[36:39], a[236:239], a[180:183], v[36:39]\n\t"
"buffer_load_dwordx4 a[228:231], off, s[80:83], s20 offset:2048 sc1\n\t"
"v_accvgpr_read_b32 v237, a115\n\t"
"v_accvgpr_read_b32 v239, a119\n\t"
"v_cvt_pk_bf16_f32 v230, v230, v231\n\t"
"v_cvt_pk_bf16_f32 v234, v234, v235\n\t"
"v_pk_fma_f32 v[236:237], v[238:239], s[56:57], v[236:237]\n\t"
"v_pk_fma_f32 v[238:239], v[236:237], s[58:59], v[238:239] neg_lo:[0,0,1] neg_hi:[0,0,1]\n\t"
"v_pk_fma_f32 v[98:99], v[70:71], s[60:61], v[236:237]\n\t"
"v_pk_fma_f32 v[70:71], v[236:237], s[60:61], v[70:71]\n\t"
"v_pk_fma_f32 v[236:237], v[170:171], s[62:63], v[238:239]\n\t"
"v_pk_fma_f32 v[170:171], v[238:239], s[62:63], v[170:171]\n\t"
"v_mfma_f32_16x16x32_bf16 v[40:43], a[236:239], a[184:187], v[40:43]\n\t"
"s_mov_b32 s20, 614400\n\t"
"v_accvgpr_write_b32 a112, v230\n\t"
"v_accvgpr_write_b32 a114, v234\n\t"
"v_mfma_f32_16x16x32_bf16 v[40:43], a[252:255], a[180:183], v[40:43]\n\t"
"v_accvgpr_read_b32 v224, a120\n\t"
"v_accvgpr_read_b32 v226, a124\n\t"
"v_mfma_f32_16x16x32_bf16 v[40:43], a[232:235], a[176:179], v[40:43]\n\t"
"buffer_load_dwordx4 a[88:91], off, s[80:83], s20 offset:0 sc1\n\t"
"v_accvgpr_read_b32 v225, a121\n\t"
"v_accvgpr_read_b32 v227, a125\n\t"
"v_cvt_pk_bf16_f32 v236, v236, v237\n\t"
"v_cvt_pk_bf16_f32 v98, v98, v99\n\t"
"v_pk_fma_f32 v[224:225], v[226:227], s[56:57], v[224:225]\n\t"
"v_pk_fma_f32 v[226:227], v[224:225], s[58:59], v[226:227] neg_lo:[0,0,1] neg_hi:[0,0,1]\n\t"
"v_pk_fma_f32 v[228:229], v[72:73], s[60:61], v[224:225]\n\t"
"v_pk_fma_f32 v[72:73], v[224:225], s[60:61], v[72:73]\n\t"
"v_pk_fma_f32 v[224:225], v[172:173], s[62:63], v[226:227]\n\t"
"v_pk_fma_f32 v[172:173], v[226:227], s[62:63], v[172:173]\n\t"
"v_mfma_f32_16x16x32_bf16 v[44:47], a[232:235], a[180:183], v[44:47]\n\t"
"v_accvgpr_write_b32 a113, v236\n\t"
"v_accvgpr_write_b32 a115, v98\n\t"
"v_mfma_f32_16x16x32_bf16 v[44:47], a[252:255], a[184:187], v[44:47]\n\t"
"v_accvgpr_read_b32 v230, a122\n\t"
"v_accvgpr_read_b32 v232, a126\n\t"
"v_mfma_f32_16x16x32_bf16 v[44:47], a[236:239], a[188:191], v[44:47]\n\t"
"buffer_load_dwordx4 a[92:95], off, s[80:83], s20 offset:1024 sc1\n\t"
"v_accvgpr_read_b32 v231, a123\n\t"
"v_accvgpr_read_b32 v233, a127\n\t"
"v_accvgpr_read_b32 v236, a128\n\t"
"v_accvgpr_read_b32 v238, a132\n\t"
"v_accvgpr_read_b32 v237, a129\n\t"
"v_accvgpr_read_b32 v239, a133\n\t"
"v_cvt_pk_bf16_f32 v224, v224, v225\n\t"
"v_cvt_pk_bf16_f32 v228, v228, v229\n\t"
"v_pk_fma_f32 v[230:231], v[232:233], s[56:57], v[230:231]\n\t"
"v_pk_fma_f32 v[232:233], v[230:231], s[58:59], v[232:233] neg_lo:[0,0,1] neg_hi:[0,0,1]\n\t"
"v_pk_fma_f32 v[234:235], v[74:75], s[60:61], v[230:231]\n\t"
"v_pk_fma_f32 v[74:75], v[230:231], s[60:61], v[74:75]\n\t"
"v_pk_fma_f32 v[230:231], v[174:175], s[62:63], v[232:233]\n\t"
"v_pk_fma_f32 v[174:175], v[232:233], s[62:63], v[174:175]\n\t"
"v_pk_fma_f32 v[236:237], v[238:239], s[56:57], v[236:237]\n\t"
"v_pk_fma_f32 v[238:239], v[236:237], s[58:59], v[238:239] neg_lo:[0,0,1] neg_hi:[0,0,1]\n\t"
"v_pk_fma_f32 v[98:99], v[76:77], s[60:61], v[236:237]\n\t"
"v_pk_fma_f32 v[76:77], v[236:237], s[60:61], v[76:77]\n\t"
"v_pk_fma_f32 v[236:237], v[176:177], s[62:63], v[238:239]\n\t"
"v_pk_fma_f32 v[176:177], v[238:239], s[62:63], v[176:177]\n\t"
"v_mfma_f32_16x16x32_bf16 v[48:51], a[236:239], a[192:195], v[48:51]\n\t"
"v_accvgpr_write_b32 a120, v224\n\t"
"v_accvgpr_write_b32 a122, v228\n\t"
"v_mfma_f32_16x16x32_bf16 v[48:51], a[252:255], a[188:191], v[48:51]\n\t"
"v_accvgpr_read_b32 v224, a130\n\t"
"v_accvgpr_read_b32 v226, a134\n\t"
"v_mfma_f32_16x16x32_bf16 v[48:51], a[232:235], a[184:187], v[48:51]\n\t"
"buffer_load_dwordx4 a[96:99], off, s[80:83], s20 offset:2048 sc1\n\t"
"v_accvgpr_read_b32 v225, a131\n\t"
"v_accvgpr_read_b32 v227, a135\n\t"
"v_cvt_pk_bf16_f32 v230, v230, v231\n\t"
"v_cvt_pk_bf16_f32 v234, v234, v235\n\t"
"v_cvt_pk_bf16_f32 v236, v236, v237\n\t"
"v_cvt_pk_bf16_f32 v98, v98, v99\n\t"
"v_pk_fma_f32 v[224:225], v[226:227], s[56:57], v[224:225]\n\t"
"v_pk_fma_f32 v[226:227], v[224:225], s[58:59], v[226:227] neg_lo:[0,0,1] neg_hi:[0,0,1]\n\t"
"v_pk_fma_f32 v[228:229], v[78:79], s[60:61], v[224:225]\n\t"
"v_pk_fma_f32 v[78:79], v[224:225], s[60:61], v[78:79]\n\t"
"v_pk_fma_f32 v[224:225], v[178:179], s[62:63], v[226:227]\n\t"
"v_pk_fma_f32 v[178:179], v[226:227], s[62:63], v[178:179]\n\t"
"v_mfma_f32_16x16x32_bf16 v[52:55], a[232:235], a[188:191], v[52:55]\n\t"
"s_mov_b32 s20, 774144\n\t"
"v_accvgpr_write_b32 a121, v230\n\t"
"v_accvgpr_write_b32 a123, v234\n\t"
"v_mfma_f32_16x16x32_bf16 v[52:55], a[252:255], a[192:195], v[52:55]\n\t"
"v_accvgpr_write_b32 a128, v236\n\t"
"v_accvgpr_write_b32 a130, v98\n\t"
"v_mfma_f32_16x16x32_bf16 v[52:55], a[236:239], a[196:199], v[52:55]\n\t"
"s_waitcnt vmcnt(17)\n\t"
"ds_write_b128 v249, v[196:199] offset:18432\n\t"
"v_accvgpr_read_b32 v230, a136\n\t"
"v_accvgpr_read_b32 v232, a140\n\t"
"v_accvgpr_read_b32 v231, a137\n\t"
"v_accvgpr_read_b32 v233, a141\n\t"
"v_cvt_pk_bf16_f32 v224, v224, v225\n\t"
"v_cvt_pk_bf16_f32 v228, v228, v229\n\t"
"v_pk_fma_f32 v[230:231], v[232:233], s[56:57], v[230:231]\n\t"
"v_pk_fma_f32 v[232:233], v[230:231], s[58:59], v[232:233] neg_lo:[0,0,1] neg_hi:[0,0,1]\n\t"
"v_pk_fma_f32 v[234:235], v[80:81], s[60:61], v[230:231]\n\t"
"v_pk_fma_f32 v[80:81], v[230:231], s[60:61], v[80:81]\n\t"
"v_pk_fma_f32 v[230:231], v[180:181], s[62:63], v[232:233]\n\t"
"v_pk_fma_f32 v[180:181], v[232:233], s[62:63], v[180:181]\n\t"
"v_mfma_f32_16x16x32_bf16 v[56:59], a[236:239], a[200:203], v[56:59]\n\t"
"v_accvgpr_write_b32 a129, v224\n\t"
"v_accvgpr_write_b32 a131, v228\n\t"
"v_mfma_f32_16x16x32_bf16 v[56:59], a[252:255], a[196:199], v[56:59]\n\t"
"v_accvgpr_read_b32 v236, a138\n\t"
"v_accvgpr_read_b32 v238, a142\n\t"
"v_mfma_f32_16x16x32_bf16 v[56:59], a[232:235], a[192:195], v[56:59]\n\t"
"buffer_load_dwordx4 a[100:103], off, s[80:83], s20 offset:0 sc1\n\t"
"v_accvgpr_read_b32 v237, a139\n\t"
"v_accvgpr_read_b32 v239, a143\n\t"
"v_cvt_pk_bf16_f32 v230, v230, v231\n\t"
"v_cvt_pk_bf16_f32 v234, v234, v235\n\t"
"v_pk_fma_f32 v[236:237], v[238:239], s[56:57], v[236:237]\n\t"
"v_pk_fma_f32 v[238:239], v[236:237], s[58:59], v[238:239] neg_lo:[0,0,1] neg_hi:[0,0,1]\n\t"
"v_pk_fma_f32 v[98:99], v[82:83], s[60:61], v[236:237]\n\t"
"v_pk_fma_f32 v[82:83], v[236:237], s[60:61], v[82:83]\n\t"
"v_pk_fma_f32 v[236:237], v[182:183], s[62:63], v[238:239]\n\t"
"v_pk_fma_f32 v[182:183], v[238:239], s[62:63], v[182:183]\n\t"
"v_mfma_f32_16x16x32_bf16 v[60:63], a[232:235], a[196:199], v[60:63]\n\t"
"v_accvgpr_write_b32 a136, v230\n\t"
"v_accvgpr_write_b32 a138, v234\n\t"
"v_mfma_f32_16x16x32_bf16 v[60:63], a[252:255], a[200:203], v[60:63]\n\t"
"v_accvgpr_read_b32 v224, a144\n\t"
"v_accvgpr_read_b32 v226, a148\n\t"
"v_mfma_f32_16x16x32_bf16 v[60:63], a[236:239], a[204:207], v[60:63]\n\t"
"buffer_load_dwordx4 a[108:111], off, s[80:83], s20 offset:1024 sc1\n\t"
"v_accvgpr_read_b32 v225, a145\n\t"
"v_accvgpr_read_b32 v227, a149\n\t"
"v_cvt_pk_bf16_f32 v236, v236, v237\n\t"
"v_cvt_pk_bf16_f32 v98, v98, v99\n\t"
"v_pk_fma_f32 v[224:225], v[226:227], s[56:57], v[224:225]\n\t"
"v_pk_fma_f32 v[226:227], v[224:225], s[58:59], v[226:227] neg_lo:[0,0,1] neg_hi:[0,0,1]\n\t"
"v_pk_fma_f32 v[228:229], v[84:85], s[60:61], v[224:225]\n\t"
"v_pk_fma_f32 v[84:85], v[224:225], s[60:61], v[84:85]\n\t"
"v_pk_fma_f32 v[224:225], v[184:185], s[62:63], v[226:227]\n\t"
"v_pk_fma_f32 v[184:185], v[226:227], s[62:63], v[184:185]\n\t"
"v_mfma_f32_16x16x32_bf16 v[64:67], a[240:243], a[168:171], v[64:67]\n\t"
"ds_read_b128 a[168:171], v255 offset:2048\n\t"
"v_accvgpr_write_b32 a137, v236\n\t"
"v_mfma_f32_16x16x32_bf16 v[64:67], a[220:223], a[176:179], v[64:67]\n\t"
"v_accvgpr_write_b32 a139, v98\n\t"
"v_accvgpr_read_b32 v230, a146\n\t"
"v_mfma_f32_16x16x32_bf16 v[64:67], a[208:211], a[172:175], v[64:67]\n\t"
"ds_write_b64 v250, a[104:105] offset:18432\n\t"
"v_accvgpr_read_b32 v232, a150\n\t"
"v_accvgpr_read_b32 v231, a147\n\t"
"v_accvgpr_read_b32 v233, a151\n\t"
"v_cvt_pk_bf16_f32 v224, v224, v225\n\t"
"v_cvt_pk_bf16_f32 v228, v228, v229\n\t"
"v_pk_fma_f32 v[230:231], v[232:233], s[56:57], v[230:231]\n\t"
"v_pk_fma_f32 v[232:233], v[230:231], s[58:59], v[232:233] neg_lo:[0,0,1] neg_hi:[0,0,1]\n\t"
"v_pk_fma_f32 v[234:235], v[86:87], s[60:61], v[230:231]\n\t"
"v_pk_fma_f32 v[86:87], v[230:231], s[60:61], v[86:87]\n\t"
"v_pk_fma_f32 v[230:231], v[186:187], s[62:63], v[232:233]\n\t"
"v_pk_fma_f32 v[186:187], v[232:233], s[62:63], v[186:187]\n\t"
"v_mfma_f32_16x16x32_bf16 v[68:71], a[240:243], a[172:175], v[68:71]\n\t"
"ds_read_b128 a[172:175], v255 offset:5120\n\t"
"v_accvgpr_write_b32 a144, v224\n\t"
"v_mfma_f32_16x16x32_bf16 v[68:71], a[208:211], a[176:179], v[68:71]\n\t"
"v_accvgpr_write_b32 a146, v228\n\t"
"v_accvgpr_read_b32 v236, a152\n\t"
"v_mfma_f32_16x16x32_bf16 v[68:71], a[220:223], a[180:183], v[68:71]\n\t"
"buffer_load_dwordx4 a[232:235], off, s[80:83], s20 offset:2048 sc1\n\t"
"v_accvgpr_read_b32 v238, a156\n\t"
"v_accvgpr_read_b32 v237, a153\n\t"
"v_accvgpr_read_b32 v239, a157\n\t"
"v_cvt_pk_bf16_f32 v230, v230, v231\n\t"
"v_cvt_pk_bf16_f32 v234, v234, v235\n\t"
"s_mov_b32 s20, 786432\n\t"
"v_pk_fma_f32 v[236:237], v[238:239], s[56:57], v[236:237]\n\t"
"v_pk_fma_f32 v[238:239], v[236:237], s[58:59], v[238:239] neg_lo:[0,0,1] neg_hi:[0,0,1]\n\t"
"v_pk_fma_f32 v[98:99], v[88:89], s[60:61], v[236:237]\n\t"
"v_pk_fma_f32 v[88:89], v[236:237], s[60:61], v[88:89]\n\t"
"v_pk_fma_f32 v[236:237], v[188:189], s[62:63], v[238:239]\n\t"
"v_pk_fma_f32 v[188:189], v[238:239], s[62:63], v[188:189]\n\t"
"v_mfma_f32_16x16x32_bf16 v[72:75], a[220:223], a[184:187], v[72:75]\n\t"
"Lpg24_%=:\n\t"
"s_cbranch_vccnz Lvr3_%=\n\t"
"v_accvgpr_write_b32 a145, v230\n\t"
"v_accvgpr_write_b32 a147, v234\n\t"
"v_mfma_f32_16x16x32_bf16 v[72:75], a[208:211], a[180:183], v[72:75]\n\t"
"v_accvgpr_read_b32 v224, a154\n\t"
"v_accvgpr_read_b32 v226, a158\n\t"
"v_mfma_f32_16x16x32_bf16 v[72:75], a[240:243], a[176:179], v[72:75]\n\t"
"buffer_load_dwordx4 a[236:239], off, s[80:83], s20 offset:0 sc1\n\t"
"ds_read_b128 a[176:179], v255 offset:8192\n\t"
"v_accvgpr_read_b32 v225, a155\n\t"
"v_accvgpr_read_b32 v227, a159\n\t"
"v_cvt_pk_bf16_f32 v236, v236, v237\n\t"
"v_cvt_pk_bf16_f32 v98, v98, v99\n\t"
"v_pk_fma_f32 v[224:225], v[226:227], s[56:57], v[224:225]\n\t"
"v_pk_fma_f32 v[226:227], v[224:225], s[58:59], v[226:227] neg_lo:[0,0,1] neg_hi:[0,0,1]\n\t"
"v_pk_fma_f32 v[228:229], v[90:91], s[60:61], v[224:225]\n\t"
"v_pk_fma_f32 v[90:91], v[224:225], s[60:61], v[90:91]\n\t"
"v_pk_fma_f32 v[224:225], v[190:191], s[62:63], v[226:227]\n\t"
"v_pk_fma_f32 v[190:191], v[226:227], s[62:63], v[190:191]\n\t"
"v_mfma_f32_16x16x32_bf16 v[76:79], a[240:243], a[180:183], v[76:79]\n\t"
"ds_read_b128 a[180:183], v255 offset:11264\n\t"
"v_accvgpr_write_b32 a152, v236\n\t"
"v_mfma_f32_16x16x32_bf16 v[76:79], a[208:211], a[184:187], v[76:79]\n\t"
"v_accvgpr_write_b32 a154, v98\n\t"
"v_accvgpr_read_b32 v230, a160\n\t"
"v_mfma_f32_16x16x32_bf16 v[76:79], a[220:223], a[188:191], v[76:79]\n\t"
"buffer_load_dwordx4 a[252:255], off, s[80:83], s20 offset:1024 sc1\n\t"
"v_accvgpr_read_b32 v232, a164\n\t"
"v_accvgpr_read_b32 v231, a161\n\t"
"v_accvgpr_read_b32 v233, a165\n\t"
"v_cvt_pk_bf16_f32 v224, v224, v225\n\t"
"v_cvt_pk_bf16_f32 v228, v228, v229\n\t"
"v_pk_fma_f32 v[230:231], v[232:233], s[56:57], v[230:231]\n\t"
"v_pk_fma_f32 v[232:233], v[230:231], s[58:59], v[232:233] neg_lo:[0,0,1] neg_hi:[0,0,1]\n\t"
"v_pk_fma_f32 v[234:235], v[92:93], s[60:61], v[230:231]\n\t"
"v_pk_fma_f32 v[92:93], v[230:231], s[60:61], v[92:93]\n\t"
"v_pk_fma_f32 v[230:231], v[192:193], s[62:63], v[232:233]\n\t"
"v_pk_fma_f32 v[192:193], v[232:233], s[62:63], v[192:193]\n\t"
"v_mfma_f32_16x16x32_bf16 v[80:83], a[220:223], a[192:195], v[80:83]\n\t"
"v_accvgpr_write_b32 a153, v224\n\t"
"v_accvgpr_write_b32 a155, v228\n\t"
"v_mfma_f32_16x16x32_bf16 v[80:83], a[208:211], a[188:191], v[80:83]\n\t"
"v_accvgpr_read_b32 v236, a162\n\t"
"v_accvgpr_read_b32 v238, a166\n\t"
"v_mfma_f32_16x16x32_bf16 v[80:83], a[240:243], a[184:187], v[80:83]\n\t"
"buffer_load_dwordx4 a[116:119], off, s[80:83], s20 offset:2048 sc1\n\t"
"ds_read_b128 a[184:187], v255 offset:14336\n\t"
"v_accvgpr_read_b32 v237, a163\n\t"
"v_accvgpr_read_b32 v239, a167\n\t"
"v_cvt_pk_bf16_f32 v230, v230, v231\n\t"
"v_cvt_pk_bf16_f32 v234, v234, v235\n\t"
"s_mov_b32 s20, 798720\n\t"
"v_pk_fma_f32 v[236:237], v[238:239], s[56:57], v[236:237]\n\t"
"v_pk_fma_f32 v[238:239], v[236:237], s[58:59], v[238:239] neg_lo:[0,0,1] neg_hi:[0,0,1]\n\t"
"v_pk_fma_f32 v[98:99], v[94:95], s[60:61], v[236:237]\n\t"
"v_pk_fma_f32 v[94:95], v[236:237], s[60:61], v[94:95]\n\t"
"v_pk_fma_f32 v[236:237], v[194:195], s[62:63], v[238:239]\n\t"
"v_pk_fma_f32 v[194:195], v[238:239], s[62:63], v[194:195]\n\t"
"v_mfma_f32_16x16x32_bf16 v[84:87], a[240:243], a[188:191], v[84:87]\n\t"
"ds_read_b128 a[188:191], v255 offset:17408\n\t"
"v_accvgpr_write_b32 a160, v230\n\t"
"v_mfma_f32_16x16x32_bf16 v[84:87], a[208:211], a[192:195], v[84:87]\n\t"
"v_accvgpr_write_b32 a162, v234\n\t"
"v_cvt_pk_bf16_f32 v236, v236, v237\n\t"
"v_mfma_f32_16x16x32_bf16 v[84:87], a[220:223], a[196:199], v[84:87]\n\t"
"buffer_load_dwordx4 a[124:127], off, s[80:83], s20 offset:0 sc1\n\t"
"v_cvt_pk_bf16_f32 v98, v98, v99\n\t"
"v_mfma_f32_16x16x32_bf16 v[88:91], a[220:223], a[200:203], v[88:91]\n\t"
"v_accvgpr_write_b32 a161, v236\n\t"
"v_accvgpr_write_b32 a163, v98\n\t"
"v_mfma_f32_16x16x32_bf16 v[88:91], a[208:211], a[196:199], v[88:91]\n\t"
"v_mfma_f32_16x16x32_bf16 v[88:91], a[240:243], a[192:195], v[88:91]\n\t"
"ds_write_b128 v249, v[200:203] offset:24576\n\t"
"buffer_load_dwordx4 a[132:135], off, s[80:83], s20 offset:1024 sc1\n\t"
"v_mfma_f32_16x16x32_bf16 v[92:95], a[240:243], a[196:199], v[92:95]\n\t"
"buffer_load_dwordx4 a[140:143], off, s[80:83], s20 offset:2048 sc1\n\t"
"v_mfma_f32_16x16x32_bf16 v[92:95], a[208:211], a[200:203], v[92:95]\n\t"
"s_waitcnt vmcnt(24) lgkmcnt(4)\n\t"
"ds_read_b128 a[192:195], v255 offset:20480\n\t"
"s_mov_b32 s20, 884736\n\t"
"v_mfma_f32_16x16x32_bf16 v[92:95], a[220:223], a[204:207], v[92:95]\n\t"
"ds_write_b64 v250, a[106:107] offset:24576\n\t"
"v_mfma_f32_16x16x32_bf16 v[100:103], a[244:247], a[168:171], v[100:103]\n\t"
"buffer_load_dwordx4 a[148:151], off, s[80:83], s20 offset:0 sc1\n\t"
"v_mfma_f32_16x16x32_bf16 v[100:103], a[56:59], a[176:179], v[100:103]\n\t"
"s_waitcnt lgkmcnt(5)\n\t"
"ds_read_b128 a[196:199], v255 offset:23552\n\t"
"v_mfma_f32_16x16x32_bf16 v[100:103], a[52:55], a[172:175], v[100:103]\n\t"
"ds_read_b128 a[200:203], v255 offset:26624\n\t"
"v_mfma_f32_16x16x32_bf16 v[104:107], a[244:247], a[172:175], v[104:107]\n\t"
"s_waitcnt lgkmcnt(6)\n\t"
"ds_read_b128 a[204:207], v255 offset:29696\n\t"
"v_mfma_f32_16x16x32_bf16 v[104:107], a[52:55], a[176:179], v[104:107]\n\t"
"v_mfma_f32_16x16x32_bf16 v[104:107], a[56:59], a[180:183], v[104:107]\n\t"
"s_waitcnt lgkmcnt(0)\n\t"
"s_barrier\n\t"
"v_mfma_f32_16x16x32_bf16 v[108:111], a[56:59], a[184:187], v[108:111]\n\t"
"ds_read_b128 a[40:43], v252 offset:18432\n\t"
"v_mfma_f32_16x16x32_bf16 v[108:111], a[52:55], a[180:183], v[108:111]\n\t"
"buffer_load_dwordx4 a[208:211], off, s[80:83], s20 offset:1024 sc1\n\t"
"v_mfma_f32_16x16x32_bf16 v[108:111], a[244:247], a[176:179], v[108:111]\n\t"
"buffer_load_dwordx4 a[220:223], off, s[80:83], s20 offset:2048 sc1\n\t"
"s_mov_b32 s20, 1474560\n\t"
"v_mfma_f32_16x16x32_bf16 v[112:115], a[244:247], a[180:183], v[112:115]\n\t"
"buffer_load_dwordx4 a[240:243], off, s[80:83], s20 offset:0 sc1\n\t"
"v_mfma_f32_16x16x32_bf16 v[112:115], a[52:55], a[184:187], v[112:115]\n\t"
"s_add_u32 s29, s22, 384\n\t"
"ds_read_b128 a[44:47], v252 offset:19456\n\t"
"v_add_u32 v224, 98304, v249\n\t"
"v_mfma_f32_16x16x32_bf16 v[112:115], a[56:59], a[188:191], v[112:115]\n\t"
"buffer_load_dwordx4 a[104:107], off, s[80:83], s20 offset:1024 sc1\n\t"
"v_add_u32 v225, 98304, v250\n\t"
"v_add_u32 v226, 98304, v252\n\t"
"v_mfma_f32_16x16x32_bf16 v[116:119], a[56:59], a[192:195], v[116:119]\n\t"
"s_waitcnt lgkmcnt(1)\n\t"
"ds_read_b128 a[48:51], v252 offset:20480\n\t"
"v_mfma_f32_16x16x32_bf16 v[116:119], a[52:55], a[188:191], v[116:119]\n\t"
"buffer_load_dwordx4 a[156:159], off, s[80:83], s20 offset:2048 sc1\n\t"
"v_mfma_f32_16x16x32_bf16 v[116:119], a[244:247], a[184:187], v[116:119]\n\t"
"s_mov_b32 s20, 1486848\n\t"
"ds_write_b128 v249, v[204:207] offset:30720\n\t"
"v_mfma_f32_16x16x32_bf16 v[120:123], a[244:247], a[188:191], v[120:123]\n\t"
"buffer_load_dwordx4 a[164:167], off, s[80:83], s20 offset:0 sc1\n\t"
"v_mfma_f32_16x16x32_bf16 v[120:123], a[52:55], a[192:195], v[120:123]\n\t"
"buffer_load_dwordx4 v[240:243], off, s[80:83], s20 offset:1024 sc1\n\t"
"v_mfma_f32_16x16x32_bf16 v[120:123], a[56:59], a[196:199], v[120:123]\n\t"
"s_waitcnt lgkmcnt(2)\n\t"
"ds_write_b128 v224, v[212:215] offset:0\n\t"
"v_mfma_f32_16x16x32_bf16 v[124:127], a[56:59], a[200:203], v[124:127]\n\t"
"s_waitcnt lgkmcnt(2)\n\t"
"v_mfma_f32_16x16x32_bf16 v[124:127], a[52:55], a[196:199], v[124:127]\n\t"
"buffer_load_dwordx4 v[96:99], off, s[80:83], s20 offset:2048 sc1\n\t"
"v_mfma_f32_16x16x32_bf16 v[124:127], a[244:247], a[192:195], v[124:127]\n\t"
"s_mov_b32 s20, 897024\n\t"
"ds_write_b128 v224, a[0:3] offset:12288\n\t"
"v_mfma_f32_16x16x32_bf16 v[128:131], a[244:247], a[196:199], v[128:131]\n\t"
"s_waitcnt vmcnt(30)\n\t"
"v_mfma_f32_16x16x32_bf16 v[128:131], a[52:55], a[200:203], v[128:131]\n\t"
"buffer_load_dwordx4 a[244:247], off, s[80:83], s20 offset:0 sc1\n\t"
"v_mfma_f32_16x16x32_bf16 v[128:131], a[56:59], a[204:207], v[128:131]\n\t"
"ds_write_b128 v224, a[8:11] offset:24576\n\t"
"v_mfma_f32_16x16x32_bf16 v[132:135], a[60:63], a[168:171], v[132:135]\n\t"
"v_mfma_f32_16x16x32_bf16 v[132:135], a[212:215], a[176:179], v[132:135]\n\t"
"buffer_load_dwordx4 a[52:55], off, s[80:83], s20 offset:1024 sc1\n\t"
"v_mfma_f32_16x16x32_bf16 v[132:135], a[248:251], a[172:175], v[132:135]\n\t"
"ds_write_b128 v224, a[16:19] offset:36864\n\t"
"ds_write_b128 v224, a[24:27] offset:49152\n\t"
"buffer_store_dwordx4 a[40:43], v248, s[16:19], s29 offen offset:0 nt\n\t"
"v_mfma_f32_16x16x32_bf16 v[136:139], a[60:63], a[172:175], v[136:139]\n\t"
"v_mfma_f32_16x16x32_bf16 v[136:139], a[248:251], a[176:179], v[136:139]\n\t"
"buffer_load_dwordx4 a[56:59], off, s[80:83], s20 offset:2048 sc1\n\t"
"v_mfma_f32_16x16x32_bf16 v[136:139], a[212:215], a[180:183], v[136:139]\n\t"
"buffer_store_dwordx4 a[44:47], v248, s[16:19], s29 offen offset:128 nt\n\t"
"ds_write_b64 v250, a[112:113] offset:30720\n\t"
"s_mov_b32 s20, 909312\n\t"
"v_mfma_f32_16x16x32_bf16 v[140:143], a[212:215], a[184:187], v[140:143]\n\t"
"v_mfma_f32_16x16x32_bf16 v[140:143], a[248:251], a[180:183], v[140:143]\n\t"
"v_mfma_f32_16x16x32_bf16 v[140:143], a[60:63], a[176:179], v[140:143]\n\t"
"buffer_store_dwordx4 a[48:51], v248, s[16:19], s29 offen offset:256 nt\n\t"
"ds_write_b64 v225, a[120:121] offset:0\n\t"
"v_mfma_f32_16x16x32_bf16 v[144:147], a[60:63], a[180:183], v[144:147]\n\t"
"v_mfma_f32_16x16x32_bf16 v[144:147], a[248:251], a[184:187], v[144:147]\n\t"
"buffer_load_dwordx4 a[48:51], off, s[80:83], s20 offset:0 sc1\n\t"
"v_mfma_f32_16x16x32_bf16 v[144:147], a[212:215], a[188:191], v[144:147]\n\t"
"ds_write_b64 v225, a[128:129] offset:12288\n\t"
"ds_write_b64 v225, a[136:137] offset:24576\n\t"
"ds_write_b64 v225, a[144:145] offset:36864\n\t"
"ds_write_b64 v225, a[152:153] offset:49152\n\t"
"v_mfma_f32_16x16x32_bf16 v[148:151], a[212:215], a[192:195], v[148:151]\n\t"
"buffer_load_dwordx4 v[236:239], off, s[80:83], s20 offset:1024 sc1\n\t"
"v_mfma_f32_16x16x32_bf16 v[148:151], a[248:251], a[188:191], v[148:151]\n\t"
"v_mfma_f32_16x16x32_bf16 v[148:151], a[60:63], a[184:187], v[148:151]\n\t"
"ds_write_b128 v249, v[208:211] offset:36864\n\t"
"v_mfma_f32_16x16x32_bf16 v[152:155], a[60:63], a[188:191], v[152:155]\n\t"
"v_mfma_f32_16x16x32_bf16 v[152:155], a[248:251], a[192:195], v[152:155]\n\t"
"v_mfma_f32_16x16x32_bf16 v[152:155], a[212:215], a[196:199], v[152:155]\n\t"
"ds_write_b128 v224, v[216:219] offset:6144\n\t"
"v_mfma_f32_16x16x32_bf16 v[156:159], a[212:215], a[200:203], v[156:159]\n\t"
"v_mfma_f32_16x16x32_bf16 v[156:159], a[248:251], a[196:199], v[156:159]\n\t"
"v_mfma_f32_16x16x32_bf16 v[156:159], a[60:63], a[192:195], v[156:159]\n\t"
"ds_write_b128 v224, a[4:7] offset:18432\n\t"
"v_mfma_f32_16x16x32_bf16 v[160:163], a[60:63], a[196:199], v[160:163]\n\t"
"buffer_load_dwordx4 a[60:63], off, s[80:83], s20 offset:2048 sc1\n\t"
"v_mfma_f32_16x16x32_bf16 v[160:163], a[248:251], a[200:203], v[160:163]\n\t"
"s_waitcnt vmcnt(36)\n\t"
"s_mov_b32 s20, 1499136\n\t"
"v_mfma_f32_16x16x32_bf16 v[160:163], a[212:215], a[204:207], v[160:163]\n\t"
"ds_write_b128 v224, a[12:15] offset:30720\n\t"
"ds_write_b128 v224, a[20:23] offset:43008\n\t"
"ds_write_b128 v224, a[28:31] offset:55296\n\t"
"v_mfma_f32_16x16x32_bf16 v[164:167], a[64:67], a[168:171], v[164:167]\n\t"
"buffer_load_dwordx4 a[212:215], off, s[80:83], s20 offset:0 sc1\n\t"
"v_mfma_f32_16x16x32_bf16 v[164:167], a[72:75], a[176:179], v[164:167]\n\t"
"ds_read_b128 a[168:171], v244 offset:0\n\t"
"v_mfma_f32_16x16x32_bf16 v[164:167], a[68:71], a[172:175], v[164:167]\n\t"
"ds_write_b64 v250, a[114:115] offset:36864\n\t"
"v_mfma_f32_16x16x32_bf16 v[168:171], a[64:67], a[172:175], v[168:171]\n\t"
"buffer_load_dwordx4 a[248:251], off, s[80:83], s20 offset:1024 sc1\n\t"
"v_mfma_f32_16x16x32_bf16 v[168:171], a[68:71], a[176:179], v[168:171]\n\t"
"ds_read_b128 a[172:175], v244 offset:512\n\t"
"v_mfma_f32_16x16x32_bf16 v[168:171], a[72:75], a[180:183], v[168:171]\n\t"
"ds_write_b64 v225, a[122:123] offset:6144\n\t"
"v_mfma_f32_16x16x32_bf16 v[172:175], a[72:75], a[184:187], v[172:175]\n\t"
"v_mfma_f32_16x16x32_bf16 v[172:175], a[68:71], a[180:183], v[172:175]\n\t"
"v_mfma_f32_16x16x32_bf16 v[172:175], a[64:67], a[176:179], v[172:175]\n\t"
"ds_write_b64 v225, a[130:131] offset:18432\n\t"
"ds_write_b64 v225, a[138:139] offset:30720\n\t"
"ds_write_b64 v225, a[146:147] offset:43008\n\t"
"ds_write_b64 v225, a[154:155] offset:55296\n\t"
"v_mfma_f32_16x16x32_bf16 v[176:179], a[64:67], a[180:183], v[176:179]\n\t"
"ds_read_b128 a[176:179], v244 offset:1024\n\t"
"v_mfma_f32_16x16x32_bf16 v[176:179], a[68:71], a[184:187], v[176:179]\n\t"
"ds_read_b128 a[180:183], v244 offset:1536\n\t"
"v_mfma_f32_16x16x32_bf16 v[176:179], a[72:75], a[188:191], v[176:179]\n\t"
"s_waitcnt lgkmcnt(2)\n\t"
"s_barrier\n\t"
"v_mfma_f32_16x16x32_bf16 v[180:183], a[72:75], a[192:195], v[180:183]\n\t"
"ds_read_b128 v[196:199], v252 offset:30720\n\t"
"v_mfma_f32_16x16x32_bf16 v[180:183], a[68:71], a[188:191], v[180:183]\n\t"
"ds_read_b128 v[200:203], v252 offset:31744\n\t"
"v_mfma_f32_16x16x32_bf16 v[180:183], a[64:67], a[184:187], v[180:183]\n\t"
"ds_read_b128 a[184:187], v244 offset:2048\n\t"
"v_mfma_f32_16x16x32_bf16 v[184:187], a[64:67], a[188:191], v[184:187]\n\t"
"buffer_load_dwordx4 a[28:31], off, s[80:83], s20 offset:2048 sc1\n\t"
"v_mfma_f32_16x16x32_bf16 v[184:187], a[68:71], a[192:195], v[184:187]\n\t"
"s_mov_b32 s20, 1069056\n\t"
"ds_read_b128 a[40:43], v252 offset:32768\n\t"
"s_add_u32 s29, s22, 196992\n\t"
"v_mfma_f32_16x16x32_bf16 v[184:187], a[72:75], a[196:199], v[184:187]\n\t"
"ds_read_b128 a[188:191], v244 offset:2560\n\t"
"v_mfma_f32_16x16x32_bf16 v[188:191], a[72:75], a[200:203], v[188:191]\n\t"
"buffer_load_dwordx4 a[112:115], off, s[80:83], s20 offset:0 sc1\n\t"
"v_mfma_f32_16x16x32_bf16 v[188:191], a[68:71], a[196:199], v[188:191]\n\t"
"s_waitcnt lgkmcnt(4)\n\t"
"ds_read_b128 a[44:47], v226 offset:0\n\t"
"v_mfma_f32_16x16x32_bf16 v[188:191], a[64:67], a[192:195], v[188:191]\n\t"
"ds_read_b128 a[192:195], v244 offset:3072\n\t"
"v_mfma_f32_16x16x32_bf16 v[192:195], a[64:67], a[196:199], v[192:195]\n\t"
"buffer_load_dwordx4 a[120:123], off, s[80:83], s20 offset:1024 sc1\n\t"
"v_mfma_f32_16x16x32_bf16 v[192:195], a[68:71], a[200:203], v[192:195]\n\t"
"s_waitcnt vmcnt(38)\n\t"
"ds_read_b128 v[204:207], v226 offset:1024\n\t"
"s_waitcnt lgkmcnt(6)\n\t"
"v_mfma_f32_16x16x32_bf16 v[192:195], a[72:75], a[204:207], v[192:195]\n\t"
"ds_write_b128 v249, a[32:35] offset:18432\n\t"
"Lst33_%=:\n\t"
"v_mfma_f32_16x16x32_bf16 v[0:3], a[76:79], a[168:171], v[0:3]\n\t"
"buffer_load_dwordx4 a[64:67], off, s[80:83], s20 offset:2048 sc1\n\t"
"v_mfma_f32_16x16x32_bf16 v[0:3], a[84:87], a[176:179], v[0:3]\n\t"
"s_mov_b32 s20, 1658880\n\t"
"ds_read_b128 a[196:199], v244 offset:3584\n\t"
"v_mfma_f32_16x16x32_bf16 v[0:3], a[80:83], a[172:175], v[0:3]\n\t"
"buffer_store_dwordx4 v[196:199], v248, s[16:19], s29 offen offset:0 nt\n\t"
"ds_read_b128 v[208:211], v226 offset:2048\n\t"
"ds_read_b128 a[200:203], v244 offset:4096\n\t"
"s_waitcnt lgkmcnt(8)\n\t"
"v_mfma_f32_16x16x32_bf16 v[4:7], a[76:79], a[172:175], v[4:7]\n\t"
"s_waitcnt lgkmcnt(6)\n\t"
"ds_read_b128 v[212:215], v226 offset:12288\n\t"
"v_mfma_f32_16x16x32_bf16 v[4:7], a[80:83], a[176:179], v[4:7]\n\t"
"buffer_load_dwordx4 a[68:71], off, s[80:83], s20 offset:0 sc1\n\t"
"v_mfma_f32_16x16x32_bf16 v[4:7], a[84:87], a[180:183], v[4:7]\n\t"
"buffer_store_dwordx4 v[200:203], v248, s[16:19], s29 offen offset:128 nt\n\t"
"ds_read_b128 a[204:207], v244 offset:4608\n\t"
"buffer_load_dwordx4 a[72:75], off, s[80:83], s20 offset:1024 sc1\n\t"
"v_mfma_f32_16x16x32_bf16 v[8:11], a[84:87], a[184:187], v[8:11]\n\t"
"s_waitcnt lgkmcnt(6)\n\t"
"ds_read_b128 v[216:219], v226 offset:13312\n\t"
"v_mfma_f32_16x16x32_bf16 v[8:11], a[80:83], a[180:183], v[8:11]\n\t"
"v_mfma_f32_16x16x32_bf16 v[8:11], a[76:79], a[176:179], v[8:11]\n\t"
"ds_write_b64 v250, a[160:161] offset:18432\n\t"
"v_mfma_f32_16x16x32_bf16 v[12:15], a[76:79], a[180:183], v[12:15]\n\t"
"s_waitcnt lgkmcnt(5)\n\t"
"ds_read_b128 v[196:199], v226 offset:14336\n\t"
"v_mfma_f32_16x16x32_bf16 v[12:15], a[80:83], a[184:187], v[12:15]\n\t"
"v_mfma_f32_16x16x32_bf16 v[12:15], a[84:87], a[188:191], v[12:15]\n\t"
"buffer_store_dwordx4 a[40:43], v248, s[16:19], s29 offen offset:256 nt\n\t"
"ds_write_b64 v250, a[162:163] offset:24576\n\t"
"s_add_u32 s29, s22, 393600\n\t"
"v_mfma_f32_16x16x32_bf16 v[16:19], a[84:87], a[192:195], v[16:19]\n\t"
"s_waitcnt lgkmcnt(5)\n\t"
"v_mfma_f32_16x16x32_bf16 v[16:19], a[80:83], a[188:191], v[16:19]\n\t"
"v_mfma_f32_16x16x32_bf16 v[16:19], a[76:79], a[184:187], v[16:19]\n\t"
"buffer_store_dwordx4 a[44:47], v248, s[16:19], s29 offen offset:0 nt\n\t"
"ds_read_b128 v[200:203], v226 offset:24576\n\t"
"ds_read_b128 a[4:7], v226 offset:36864\n\t"
"v_mfma_f32_16x16x32_bf16 v[20:23], a[76:79], a[188:191], v[20:23]\n\t"
"s_waitcnt lgkmcnt(5)\n\t"
"ds_read_b128 a[16:19], v226 offset:49152\n\t"
"v_mfma_f32_16x16x32_bf16 v[20:23], a[80:83], a[192:195], v[20:23]\n\t"
"ds_read_b128 a[0:3], v226 offset:25600\n\t"
"v_mfma_f32_16x16x32_bf16 v[20:23], a[84:87], a[196:199], v[20:23]\n\t"
"buffer_store_dwordx4 v[204:207], v248, s[16:19], s29 offen offset:128 nt\n\t"
"ds_read_b128 a[8:11], v226 offset:37888\n\t"
"ds_read_b128 a[20:23], v226 offset:50176\n\t"
"v_mfma_f32_16x16x32_bf16 v[24:27], a[84:87], a[200:203], v[24:27]\n\t"
"ds_read_b128 v[204:207], v226 offset:26624\n\t"
"v_mfma_f32_16x16x32_bf16 v[24:27], a[80:83], a[196:199], v[24:27]\n\t"
"ds_read_b128 a[12:15], v226 offset:38912\n\t"
"v_mfma_f32_16x16x32_bf16 v[24:27], a[76:79], a[192:195], v[24:27]\n\t"
"buffer_store_dwordx4 v[208:211], v248, s[16:19], s29 offen offset:256 nt\n\t"
"ds_read_b128 a[24:27], v226 offset:51200\n\t"
"s_add_u32 s29, s22, 590208\n\t"
"v_mfma_f32_16x16x32_bf16 v[28:31], a[76:79], a[196:199], v[28:31]\n\t"
"s_waitcnt vmcnt(44)\n\t"
"v_mfma_f32_16x16x32_bf16 v[28:31], a[80:83], a[200:203], v[28:31]\n\t"
"v_mfma_f32_16x16x32_bf16 v[28:31], a[84:87], a[204:207], v[28:31]\n\t"
"ds_write_b128 v249, a[36:39] offset:24576\n\t"
"v_mfma_f32_16x16x32_bf16 v[32:35], a[216:219], a[168:171], v[32:35]\n\t"
"s_waitcnt lgkmcnt(11)\n\t"
"v_mfma_f32_16x16x32_bf16 v[32:35], a[228:231], a[176:179], v[32:35]\n\t"
"buffer_load_dwordx4 a[40:43], off, s[80:83], s20 offset:2048 sc1\n\t"
"v_mfma_f32_16x16x32_bf16 v[32:35], a[224:227], a[172:175], v[32:35]\n\t"
"buffer_store_dwordx4 v[212:215], v248, s[16:19], s29 offen offset:0 nt\n\t"
"v_mfma_f32_16x16x32_bf16 v[36:39], a[216:219], a[172:175], v[36:39]\n\t"
"s_mov_b32 s20, 1671168\n\t"
"v_mfma_f32_16x16x32_bf16 v[36:39], a[224:227], a[176:179], v[36:39]\n\t"
"v_mfma_f32_16x16x32_bf16 v[36:39], a[228:231], a[180:183], v[36:39]\n\t"
"buffer_store_dwordx4 v[216:219], v248, s[16:19], s29 offen offset:128 nt\n\t"
"v_mfma_f32_16x16x32_bf16 v[40:43], a[228:231], a[184:187], v[40:43]\n\t"
"v_mfma_f32_16x16x32_bf16 v[40:43], a[224:227], a[180:183], v[40:43]\n\t"
"v_mfma_f32_16x16x32_bf16 v[40:43], a[216:219], a[176:179], v[40:43]\n\t"
"buffer_store_dwordx4 v[196:199], v248, s[16:19], s29 offen offset:256 nt\n\t"
"s_add_u32 s29, s22, 786816\n\t"
"v_mfma_f32_16x16x32_bf16 v[44:47], a[216:219], a[180:183], v[44:47]\n\t"
"s_waitcnt lgkmcnt(9)\n\t"
"v_mfma_f32_16x16x32_bf16 v[44:47], a[224:227], a[184:187], v[44:47]\n\t"
"v_mfma_f32_16x16x32_bf16 v[44:47], a[228:231], a[188:191], v[44:47]\n\t"
"s_waitcnt lgkmcnt(0)\n\t"
"s_barrier\n\t"
"v_mfma_f32_16x16x32_bf16 v[48:51], a[228:231], a[192:195], v[48:51]\n\t"
"v_mfma_f32_16x16x32_bf16 v[48:51], a[224:227], a[188:191], v[48:51]\n\t"
"buffer_load_dwordx4 a[44:47], off, s[80:83], s20 offset:0 sc1\n\t"
"v_mfma_f32_16x16x32_bf16 v[48:51], a[216:219], a[184:187], v[48:51]\n\t"
"buffer_load_dwordx4 a[32:35], off, s[80:83], s20 offset:1024 sc1\n\t"
"v_mfma_f32_16x16x32_bf16 v[52:55], a[216:219], a[188:191], v[52:55]\n\t"
"v_mfma_f32_16x16x32_bf16 v[52:55], a[224:227], a[192:195], v[52:55]\n\t"
"v_mfma_f32_16x16x32_bf16 v[52:55], a[228:231], a[196:199], v[52:55]\n\t"
"buffer_store_dwordx4 v[200:203], v248, s[16:19], s29 offen offset:0 nt\n\t"
"v_mfma_f32_16x16x32_bf16 v[56:59], a[228:231], a[200:203], v[56:59]\n\t"
"v_mfma_f32_16x16x32_bf16 v[56:59], a[224:227], a[196:199], v[56:59]\n\t"
"buffer_load_dwordx4 a[36:39], off, s[80:83], s20 offset:2048 sc1\n\t"
"v_mfma_f32_16x16x32_bf16 v[56:59], a[216:219], a[192:195], v[56:59]\n\t"
"s_mov_b32 s20, 1081344\n\t"
"v_mfma_f32_16x16x32_bf16 v[60:63], a[216:219], a[196:199], v[60:63]\n\t"
"s_waitcnt vmcnt(49)\n\t"
"v_mfma_f32_16x16x32_bf16 v[60:63], a[224:227], a[200:203], v[60:63]\n\t"
"v_mfma_f32_16x16x32_bf16 v[60:63], a[228:231], a[204:207], v[60:63]\n\t"
"buffer_store_dwordx4 a[0:3], v248, s[16:19], s29 offen offset:128 nt\n\t"
"v_mfma_f32_16x16x32_bf16 v[64:67], a[88:91], a[168:171], v[64:67]\n\t"
"ds_read_b128 a[168:171], v244 offset:9216\n\t"
"v_mfma_f32_16x16x32_bf16 v[64:67], a[96:99], a[176:179], v[64:67]\n\t"
"v_mfma_f32_16x16x32_bf16 v[64:67], a[92:95], a[172:175], v[64:67]\n\t"
"buffer_load_dwordx4 a[216:219], off, s[80:83], s20 offset:0 sc1\n\t"
"v_mfma_f32_16x16x32_bf16 v[68:71], a[88:91], a[172:175], v[68:71]\n\t"
"ds_read_b128 a[172:175], v244 offset:9728\n\t"
"v_mfma_f32_16x16x32_bf16 v[68:71], a[92:95], a[176:179], v[68:71]\n\t"
"v_mfma_f32_16x16x32_bf16 v[68:71], a[96:99], a[180:183], v[68:71]\n\t"
"v_mfma_f32_16x16x32_bf16 v[72:75], a[96:99], a[184:187], v[72:75]\n\t"
"v_mfma_f32_16x16x32_bf16 v[72:75], a[92:95], a[180:183], v[72:75]\n\t"
"v_mfma_f32_16x16x32_bf16 v[72:75], a[88:91], a[176:179], v[72:75]\n\t"
"ds_read_b128 a[176:179], v244 offset:10240\n\t"
"v_mfma_f32_16x16x32_bf16 v[76:79], a[88:91], a[180:183], v[76:79]\n\t"
"buffer_load_dwordx4 a[224:227], off, s[80:83], s20 offset:1024 sc1\n\t"
"v_mfma_f32_16x16x32_bf16 v[76:79], a[92:95], a[184:187], v[76:79]\n\t"
"ds_read_b128 a[180:183], v244 offset:10752\n\t"
"v_mfma_f32_16x16x32_bf16 v[76:79], a[96:99], a[188:191], v[76:79]\n\t"
"buffer_store_dwordx4 v[204:207], v248, s[16:19], s29 offen offset:256 nt\n\t"
"ds_read_b128 v[196:199], v252 offset:18432\n\t"
"ds_read_b128 v[200:203], v252 offset:19456\n\t"
"s_add_u32 s29, s22, 983424\n\t"
"v_mfma_f32_16x16x32_bf16 v[80:83], a[96:99], a[192:195], v[80:83]\n\t"
"ds_read_b128 v[208:211], v252 offset:20480\n\t"
"v_mfma_f32_16x16x32_bf16 v[80:83], a[92:95], a[188:191], v[80:83]\n\t"
"buffer_load_dwordx4 a[228:231], off, s[80:83], s20 offset:2048 sc1\n\t"
"v_mfma_f32_16x16x32_bf16 v[80:83], a[88:91], a[184:187], v[80:83]\n\t"
"ds_read_b128 a[184:187], v244 offset:11264\n\t"
"v_mfma_f32_16x16x32_bf16 v[84:87], a[88:91], a[188:191], v[84:87]\n\t"
"s_mov_b32 s20, 1093632\n\t"
"ds_read_b128 a[188:191], v244 offset:11776\n\t"
"v_mfma_f32_16x16x32_bf16 v[84:87], a[92:95], a[192:195], v[84:87]\n\t"
"v_mfma_f32_16x16x32_bf16 v[84:87], a[96:99], a[196:199], v[84:87]\n\t"
"buffer_store_dwordx4 a[4:7], v248, s[16:19], s29 offen offset:0 nt\n\t"
"v_mfma_f32_16x16x32_bf16 v[88:91], a[96:99], a[200:203], v[88:91]\n\t"
"v_mfma_f32_16x16x32_bf16 v[88:91], a[92:95], a[196:199], v[88:91]\n\t"
"v_mfma_f32_16x16x32_bf16 v[88:91], a[88:91], a[192:195], v[88:91]\n\t"
"ds_read_b128 a[192:195], v244 offset:12288\n\t"
"v_mfma_f32_16x16x32_bf16 v[92:95], a[88:91], a[196:199], v[92:95]\n\t"
"buffer_load_dwordx4 a[76:79], off, s[80:83], s20 offset:0 sc1\n\t"
"v_mfma_f32_16x16x32_bf16 v[92:95], a[92:95], a[200:203], v[92:95]\n\t"
"s_waitcnt vmcnt(53) lgkmcnt(7)\n\t"
"ds_read_b128 a[196:199], v244 offset:12800\n\t"
"v_mfma_f32_16x16x32_bf16 v[92:95], a[96:99], a[204:207], v[92:95]\n\t"
"ds_read_b128 a[200:203], v244 offset:13312\n\t"
"v_mfma_f32_16x16x32_bf16 v[100:103], a[100:103], a[168:171], v[100:103]\n\t"
"s_waitcnt lgkmcnt(8)\n\t"
"ds_read_b128 a[204:207], v244 offset:13824\n\t"
"v_mfma_f32_16x16x32_bf16 v[100:103], a[232:235], a[176:179], v[100:103]\n\t"
"v_mfma_f32_16x16x32_bf16 v[100:103], a[108:111], a[172:175], v[100:103]\n\t"
"buffer_store_dwordx4 a[8:11], v248, s[16:19], s29 offen offset:128 nt\n\t"
"v_mfma_f32_16x16x32_bf16 v[104:107], a[100:103], a[172:175], v[104:107]\n\t"
"s_waitcnt lgkmcnt(5)\n\t"
"v_mfma_f32_16x16x32_bf16 v[104:107], a[108:111], a[176:179], v[104:107]\n\t"
"v_mfma_f32_16x16x32_bf16 v[104:107], a[232:235], a[180:183], v[104:107]\n\t"
"s_waitcnt lgkmcnt(0)\n\t"
"s_barrier\n\t"
"v_mfma_f32_16x16x32_bf16 v[108:111], a[232:235], a[184:187], v[108:111]\n\t"
"v_mfma_f32_16x16x32_bf16 v[108:111], a[108:111], a[180:183], v[108:111]\n\t"
"buffer_load_dwordx4 a[4:7], off, s[80:83], s20 offset:1024 sc1\n\t"
"v_mfma_f32_16x16x32_bf16 v[108:111], a[100:103], a[176:179], v[108:111]\n\t"
"buffer_load_dwordx4 a[80:83], off, s[80:83], s20 offset:2048 sc1\n\t"
"v_mfma_f32_16x16x32_bf16 v[112:115], a[100:103], a[180:183], v[112:115]\n\t"
"s_mov_b32 s20, 1683456\n\t"
"v_mfma_f32_16x16x32_bf16 v[112:115], a[108:111], a[184:187], v[112:115]\n\t"
"v_mfma_f32_16x16x32_bf16 v[112:115], a[232:235], a[188:191], v[112:115]\n\t"
"buffer_store_dwordx4 a[12:15], v248, s[16:19], s29 offen offset:256 nt\n\t"
"v_mfma_f32_16x16x32_bf16 v[116:119], a[232:235], a[192:195], v[116:119]\n\t"
"s_add_u32 s29, s22, 1180032\n\t"
"v_mfma_f32_16x16x32_bf16 v[116:119], a[108:111], a[188:191], v[116:119]\n\t"
"v_mfma_f32_16x16x32_bf16 v[116:119], a[100:103], a[184:187], v[116:119]\n\t"
"buffer_load_dwordx4 a[8:11], off, s[80:83], s20 offset:0 sc1\n\t"
"v_mfma_f32_16x16x32_bf16 v[120:123], a[100:103], a[188:191], v[120:123]\n\t"
"v_mfma_f32_16x16x32_bf16 v[120:123], a[108:111], a[192:195], v[120:123]\n\t"
"v_mfma_f32_16x16x32_bf16 v[120:123], a[232:235], a[196:199], v[120:123]\n\t"
"v_mfma_f32_16x16x32_bf16 v[124:127], a[232:235], a[200:203], v[124:127]\n\t"
"v_mfma_f32_16x16x32_bf16 v[124:127], a[108:111], a[196:199], v[124:127]\n\t"
"v_mfma_f32_16x16x32_bf16 v[124:127], a[100:103], a[192:195], v[124:127]\n\t"
"buffer_store_dwordx4 a[16:19], v248, s[16:19], s29 offen offset:0 nt\n\t"
"v_mfma_f32_16x16x32_bf16 v[128:131], a[100:103], a[196:199], v[128:131]\n\t"
"s_waitcnt vmcnt(56)\n\t"
"v_mfma_f32_16x16x32_bf16 v[128:131], a[108:111], a[200:203], v[128:131]\n\t"
"buffer_load_dwordx4 a[12:15], off, s[80:83], s20 offset:1024 sc1\n\t"
"v_mfma_f32_16x16x32_bf16 v[128:131], a[232:235], a[204:207], v[128:131]\n\t"
"v_mfma_f32_16x16x32_bf16 v[132:135], a[236:239], a[168:171], v[132:135]\n\t"
"v_mfma_f32_16x16x32_bf16 v[132:135], a[116:119], a[176:179], v[132:135]\n\t"
"buffer_load_dwordx4 a[232:235], off, s[80:83], s20 offset:2048 sc1\n\t"
"v_mfma_f32_16x16x32_bf16 v[132:135], a[252:255], a[172:175], v[132:135]\n\t"
"v_mfma_f32_16x16x32_bf16 v[136:139], a[236:239], a[172:175], v[136:139]\n\t"
"v_mfma_f32_16x16x32_bf16 v[136:139], a[252:255], a[176:179], v[136:139]\n\t"
"v_subrev_u32 v255, 55296, v255\n\t"
"v_mfma_f32_16x16x32_bf16 v[136:139], a[116:119], a[180:183], v[136:139]\n\t"
"buffer_store_dwordx4 a[20:23], v248, s[16:19], s29 offen offset:128 nt\n\t"
"v_mfma_f32_16x16x32_bf16 v[140:143], a[116:119], a[184:187], v[140:143]\n\t"
"v_mfma_f32_16x16x32_bf16 v[140:143], a[252:255], a[180:183], v[140:143]\n\t"
"v_mfma_f32_16x16x32_bf16 v[140:143], a[236:239], a[176:179], v[140:143]\n\t"
"v_mfma_f32_16x16x32_bf16 v[144:147], a[236:239], a[180:183], v[144:147]\n\t"
"v_mfma_f32_16x16x32_bf16 v[144:147], a[252:255], a[184:187], v[144:147]\n\t"
"v_mfma_f32_16x16x32_bf16 v[144:147], a[116:119], a[188:191], v[144:147]\n\t"
"v_mfma_f32_16x16x32_bf16 v[148:151], a[116:119], a[192:195], v[148:151]\n\t"
"v_mfma_f32_16x16x32_bf16 v[148:151], a[252:255], a[188:191], v[148:151]\n\t"
"v_mfma_f32_16x16x32_bf16 v[148:151], a[236:239], a[184:187], v[148:151]\n\t"
"buffer_store_dwordx4 a[24:27], v248, s[16:19], s29 offen offset:256 nt\n\t"
"v_mfma_f32_16x16x32_bf16 v[152:155], a[236:239], a[188:191], v[152:155]\n\t"
"s_add_u32 s29, s22, 1376640\n\t"
"v_mfma_f32_16x16x32_bf16 v[152:155], a[252:255], a[192:195], v[152:155]\n\t"
"v_mfma_f32_16x16x32_bf16 v[152:155], a[116:119], a[196:199], v[152:155]\n\t"
"v_mfma_f32_16x16x32_bf16 v[156:159], a[116:119], a[200:203], v[156:159]\n\t"
"v_mfma_f32_16x16x32_bf16 v[156:159], a[252:255], a[196:199], v[156:159]\n\t"
"v_mfma_f32_16x16x32_bf16 v[156:159], a[236:239], a[192:195], v[156:159]\n\t"
"v_mfma_f32_16x16x32_bf16 v[160:163], a[236:239], a[196:199], v[160:163]\n\t"
"s_waitcnt vmcnt(57)\n\t"
"v_mfma_f32_16x16x32_bf16 v[160:163], a[252:255], a[200:203], v[160:163]\n\t"
"v_mfma_f32_16x16x32_bf16 v[160:163], a[116:119], a[204:207], v[160:163]\n\t"
"buffer_store_dwordx4 v[196:199], v248, s[16:19], s29 offen offset:0 nt\n\t"
"v_mfma_f32_16x16x32_bf16 v[164:167], a[124:127], a[168:171], v[164:167]\n\t"
"ds_read_b128 a[168:171], v255 offset:512\n\t"
"v_mfma_f32_16x16x32_bf16 v[164:167], a[140:143], a[176:179], v[164:167]\n\t"
"ds_read_b128 v[204:207], v255 offset:3584\n\t"
"v_mfma_f32_16x16x32_bf16 v[164:167], a[132:135], a[172:175], v[164:167]\n\t"
"ds_read_b128 v[212:215], v255 offset:6656\n\t"
"v_mfma_f32_16x16x32_bf16 v[168:171], a[124:127], a[172:175], v[168:171]\n\t"
"ds_read_b128 a[172:175], v255 offset:1024\n\t"
"v_mfma_f32_16x16x32_bf16 v[168:171], a[132:135], a[176:179], v[168:171]\n\t"
"ds_read_b128 v[216:219], v255 offset:4096\n\t"
"v_mfma_f32_16x16x32_bf16 v[168:171], a[140:143], a[180:183], v[168:171]\n\t"
"ds_read_b128 a[0:3], v255 offset:7168\n\t"
"v_mfma_f32_16x16x32_bf16 v[172:175], a[140:143], a[184:187], v[172:175]\n\t"
"v_mfma_f32_16x16x32_bf16 v[172:175], a[132:135], a[180:183], v[172:175]\n\t"
"v_mfma_f32_16x16x32_bf16 v[172:175], a[124:127], a[176:179], v[172:175]\n\t"
"buffer_store_dwordx4 v[200:203], v248, s[16:19], s29 offen offset:128 nt\n\t"
"v_mfma_f32_16x16x32_bf16 v[176:179], a[124:127], a[180:183], v[176:179]\n\t"
"v_mfma_f32_16x16x32_bf16 v[176:179], a[132:135], a[184:187], v[176:179]\n\t"
"v_mfma_f32_16x16x32_bf16 v[176:179], a[140:143], a[188:191], v[176:179]\n\t"
"s_barrier\n\t"
"v_mfma_f32_16x16x32_bf16 v[180:183], a[140:143], a[192:195], v[180:183]\n\t"
"v_mfma_f32_16x16x32_bf16 v[180:183], a[132:135], a[188:191], v[180:183]\n\t"
"v_mfma_f32_16x16x32_bf16 v[180:183], a[124:127], a[184:187], v[180:183]\n\t"
"v_mfma_f32_16x16x32_bf16 v[184:187], a[124:127], a[188:191], v[184:187]\n\t"
"v_mfma_f32_16x16x32_bf16 v[184:187], a[132:135], a[192:195], v[184:187]\n\t"
"v_mfma_f32_16x16x32_bf16 v[184:187], a[140:143], a[196:199], v[184:187]\n\t"
"buffer_store_dwordx4 v[208:211], v248, s[16:19], s29 offen offset:256 nt\n\t"
"v_mfma_f32_16x16x32_bf16 v[188:191], a[140:143], a[200:203], v[188:191]\n\t"
"ds_read_b128 a[176:179], v255 offset:2048\n\t"
"v_mfma_f32_16x16x32_bf16 v[188:191], a[132:135], a[196:199], v[188:191]\n\t"
"ds_read_b128 a[180:183], v255 offset:5120\n\t"
"v_mfma_f32_16x16x32_bf16 v[188:191], a[124:127], a[192:195], v[188:191]\n\t"
"ds_read_b128 a[184:187], v255 offset:8192\n\t"
"v_mfma_f32_16x16x32_bf16 v[192:195], a[124:127], a[196:199], v[192:195]\n\t"
"s_waitcnt vmcnt(57) lgkmcnt(6)\n\t"
"ds_read_b128 a[188:191], v255 offset:2560\n\t"
"v_mfma_f32_16x16x32_bf16 v[192:195], a[132:135], a[200:203], v[192:195]\n\t"
"ds_read_b128 a[192:195], v255 offset:5632\n\t"
"v_mfma_f32_16x16x32_bf16 v[192:195], a[140:143], a[204:207], v[192:195]\n\t"
"ds_read_b128 a[196:199], v255 offset:8704\n\t"
"Lst34_%=:\n\t"
"v_mfma_f32_16x16x32_bf16 v[0:3], a[148:151], a[168:171], v[0:3]\n\t"
"s_waitcnt vmcnt(54) lgkmcnt(6)\n\t"
"v_mfma_f32_16x16x32_bf16 v[0:3], a[208:211], v[204:207], v[0:3]\n\t"
"v_mfma_f32_16x16x32_bf16 v[0:3], a[220:223], v[212:215], v[0:3]\n\t"
"v_mfma_f32_16x16x32_bf16 v[0:3], a[240:243], a[172:175], v[0:3]\n\t"
"s_waitcnt vmcnt(51)\n\t"
"v_mfma_f32_16x16x32_bf16 v[0:3], a[104:107], v[216:219], v[0:3]\n\t"
"v_mfma_f32_16x16x32_bf16 v[0:3], a[156:159], a[0:3], v[0:3]\n\t"
"v_mfma_f32_16x16x32_bf16 v[32:35], v[240:243], v[216:219], v[32:35]\n\t"
"s_waitcnt vmcnt(47)\n\t"
"v_mfma_f32_16x16x32_bf16 v[32:35], v[96:99], a[0:3], v[32:35]\n\t"
"v_mfma_f32_16x16x32_bf16 v[32:35], a[164:167], a[172:175], v[32:35]\n\t"
"v_mfma_f32_16x16x32_bf16 v[32:35], a[244:247], a[168:171], v[32:35]\n\t"
"s_waitcnt vmcnt(42)\n\t"
"v_cvt_pk_bf16_f32 v0, v0, v1\n\t"
"v_cvt_pk_bf16_f32 v1, v2, v3\n\t"
"v_mfma_f32_16x16x32_bf16 v[32:35], a[52:55], v[204:207], v[32:35]\n\t"
"v_mfma_f32_16x16x32_bf16 v[32:35], a[56:59], v[212:215], v[32:35]\n\t"
"v_mfma_f32_16x16x32_bf16 v[64:67], v[236:239], v[204:207], v[64:67]\n\t"
"s_waitcnt vmcnt(39)\n\t"
"ds_read_b128 a[200:203], v255 offset:9728\n\t"
"v_mfma_f32_16x16x32_bf16 v[64:67], a[60:63], v[212:215], v[64:67]\n\t"
"v_mfma_f32_16x16x32_bf16 v[64:67], a[48:51], a[168:171], v[64:67]\n\t"
"s_barrier\n\t"
"s_cmp_lt_u32 s69, 216\n\t"
"s_cbranch_scc0 Lna2_%=\n\t"
"s_add_u32 s71, s71, 1\n\t"
"v_mov_b32 v228, s71\n\t"
"s_mov_b64 exec, 1\n\t"
"buffer_store_dword v228, off, s[8:11], s70 sc1\n\t"
"s_mov_b64 exec, -1\n\t"
"Lna2_%=:\n\t"
"v_mfma_f32_16x16x32_bf16 v[64:67], a[212:215], a[172:175], v[64:67]\n\t"
"Lpg25_%=:\n\t"
"s_cbranch_vccnz Lvr4_%=\n\t"
"s_waitcnt vmcnt(36) lgkmcnt(4)\n\t"
"ds_read_b128 a[168:171], v255 offset:10240\n\t"
"v_mfma_f32_16x16x32_bf16 v[64:67], a[248:251], v[216:219], v[64:67]\n\t"
"v_cvt_pk_bf16_f32 v2, v32, v33\n\t"
"v_cvt_pk_bf16_f32 v3, v34, v35\n\t"
"v_mfma_f32_16x16x32_bf16 v[64:67], a[28:31], a[0:3], v[64:67]\n\t"
"ds_write_b128 v249, v[0:3] offset:18432\n\t"
"v_mfma_f32_16x16x32_bf16 v[100:103], a[112:115], a[176:179], v[100:103]\n\t"
"s_waitcnt vmcnt(27) lgkmcnt(3)\n\t"
"v_mfma_f32_16x16x32_bf16 v[100:103], a[120:123], a[180:183], v[100:103]\n\t"
"v_mfma_f32_16x16x32_bf16 v[100:103], a[64:67], a[184:187], v[100:103]\n\t"
"v_mfma_f32_16x16x32_bf16 v[100:103], a[68:71], a[188:191], v[100:103]\n\t"
"s_waitcnt vmcnt(20)\n\t"
"v_cvt_pk_bf16_f32 v64, v64, v65\n\t"
"v_cvt_pk_bf16_f32 v65, v66, v67\n\t"
"v_mfma_f32_16x16x32_bf16 v[100:103], a[72:75], a[192:195], v[100:103]\n\t"
"v_mfma_f32_16x16x32_bf16 v[100:103], a[40:43], a[196:199], v[100:103]\n\t"
"ds_write_b64 v250, v[64:65] offset:18432\n\t"
"v_mfma_f32_16x16x32_bf16 v[132:135], a[32:35], a[192:195], v[132:135]\n\t"
"s_waitcnt vmcnt(15)\n\t"
"v_mfma_f32_16x16x32_bf16 v[132:135], a[36:39], a[196:199], v[132:135]\n\t"
"v_mfma_f32_16x16x32_bf16 v[132:135], a[44:47], a[188:191], v[132:135]\n\t"
"v_mfma_f32_16x16x32_bf16 v[132:135], a[216:219], a[176:179], v[132:135]\n\t"
"s_waitcnt vmcnt(10)\n\t"
"v_cvt_pk_bf16_f32 v100, v100, v101\n\t"
"v_cvt_pk_bf16_f32 v101, v102, v103\n\t"
"v_mfma_f32_16x16x32_bf16 v[132:135], a[224:227], a[180:183], v[132:135]\n\t"
"v_mfma_f32_16x16x32_bf16 v[132:135], a[228:231], a[184:187], v[132:135]\n\t"
"v_mfma_f32_16x16x32_bf16 v[164:167], a[4:7], a[180:183], v[164:167]\n\t"
"s_waitcnt vmcnt(5)\n\t"
"ds_read_b128 a[172:175], v255 offset:11264\n\t"
"v_mfma_f32_16x16x32_bf16 v[164:167], a[80:83], a[184:187], v[164:167]\n\t"
"v_mfma_f32_16x16x32_bf16 v[164:167], a[76:79], a[176:179], v[164:167]\n\t"
"v_mfma_f32_16x16x32_bf16 v[164:167], a[8:11], a[188:191], v[164:167]\n\t"
"s_waitcnt lgkmcnt(4)\n\t"
"ds_read_b128 a[176:179], v255 offset:11776\n\t"
"v_cvt_pk_bf16_f32 v102, v132, v133\n\t"
"v_mfma_f32_16x16x32_bf16 v[164:167], a[12:15], a[192:195], v[164:167]\n\t"
"v_cvt_pk_bf16_f32 v103, v134, v135\n\t"
"v_mfma_f32_16x16x32_bf16 v[164:167], a[232:235], a[196:199], v[164:167]\n\t"
"ds_write_b128 v249, v[100:103] offset:24576\n\t"
"v_mfma_f32_16x16x32_bf16 v[4:7], a[148:151], v[204:207], v[4:7]\n\t"
"s_waitcnt lgkmcnt(5)\n\t"
"v_mfma_f32_16x16x32_bf16 v[4:7], a[208:211], v[212:215], v[4:7]\n\t"
"v_mfma_f32_16x16x32_bf16 v[4:7], a[220:223], a[200:203], v[4:7]\n\t"
"v_mfma_f32_16x16x32_bf16 v[4:7], a[240:243], v[216:219], v[4:7]\n\t"
"v_cvt_pk_bf16_f32 v164, v164, v165\n\t"
"v_cvt_pk_bf16_f32 v165, v166, v167\n\t"
"v_mfma_f32_16x16x32_bf16 v[4:7], a[104:107], a[0:3], v[4:7]\n\t"
"v_mfma_f32_16x16x32_bf16 v[4:7], a[156:159], a[168:171], v[4:7]\n\t"
"ds_write_b64 v250, v[164:165] offset:24576\n\t"
"v_mfma_f32_16x16x32_bf16 v[36:39], v[240:243], a[0:3], v[36:39]\n\t"
"v_mfma_f32_16x16x32_bf16 v[36:39], v[96:99], a[168:171], v[36:39]\n\t"
"v_mfma_f32_16x16x32_bf16 v[36:39], a[164:167], v[216:219], v[36:39]\n\t"
"v_mfma_f32_16x16x32_bf16 v[36:39], a[244:247], v[204:207], v[36:39]\n\t"
"v_cvt_pk_bf16_f32 v4, v4, v5\n\t"
"v_cvt_pk_bf16_f32 v5, v6, v7\n\t"
"v_mfma_f32_16x16x32_bf16 v[36:39], a[52:55], v[212:215], v[36:39]\n\t"
"v_mfma_f32_16x16x32_bf16 v[36:39], a[56:59], a[200:203], v[36:39]\n\t"
"v_mfma_f32_16x16x32_bf16 v[68:71], v[236:239], v[212:215], v[68:71]\n\t"
"ds_read_b128 a[188:191], v255 offset:12800\n\t"
"v_mfma_f32_16x16x32_bf16 v[68:71], a[60:63], a[200:203], v[68:71]\n\t"
"v_mfma_f32_16x16x32_bf16 v[68:71], a[48:51], v[204:207], v[68:71]\n\t"
"s_waitcnt lgkmcnt(1)\n\t"
"s_barrier\n\t"
"v_mfma_f32_16x16x32_bf16 v[68:71], a[212:215], v[216:219], v[68:71]\n\t"
"ds_read_b128 v[196:199], v252 offset:18432\n\t"
"v_mfma_f32_16x16x32_bf16 v[68:71], a[248:251], a[0:3], v[68:71]\n\t"
"ds_read_b128 v[200:203], v252 offset:19456\n\t"
"v_cvt_pk_bf16_f32 v6, v36, v37\n\t"
"v_mfma_f32_16x16x32_bf16 v[68:71], a[28:31], a[168:171], v[68:71]\n\t"
"ds_read_b128 v[204:207], v252 offset:20480\n\t"
"v_cvt_pk_bf16_f32 v7, v38, v39\n\t"
"v_mfma_f32_16x16x32_bf16 v[104:107], a[112:115], a[180:183], v[104:107]\n\t"
"s_add_u32 s29, s23, 0\n\t"
"ds_read_b128 a[204:207], v255 offset:13312\n\t"
"v_mfma_f32_16x16x32_bf16 v[104:107], a[120:123], a[184:187], v[104:107]\n\t"
"v_mfma_f32_16x16x32_bf16 v[104:107], a[64:67], a[172:175], v[104:107]\n\t"
"ds_write_b128 v249, v[4:7] offset:30720\n\t"
"v_mfma_f32_16x16x32_bf16 v[104:107], a[68:71], a[192:195], v[104:107]\n\t"
"s_waitcnt lgkmcnt(4)\n\t"
"v_cvt_pk_bf16_f32 v68, v68, v69\n\t"
"v_cvt_pk_bf16_f32 v69, v70, v71\n\t"
"v_mfma_f32_16x16x32_bf16 v[104:107], a[72:75], a[196:199], v[104:107]\n\t"
"v_mfma_f32_16x16x32_bf16 v[104:107], a[40:43], a[176:179], v[104:107]\n\t"
"ds_write_b64 v250, v[68:69] offset:30720\n\t"
"v_mfma_f32_16x16x32_bf16 v[136:139], a[32:35], a[196:199], v[136:139]\n\t"
"s_waitcnt lgkmcnt(4)\n\t"
"v_mfma_f32_16x16x32_bf16 v[136:139], a[36:39], a[176:179], v[136:139]\n\t"
"v_mfma_f32_16x16x32_bf16 v[136:139], a[44:47], a[192:195], v[136:139]\n\t"
"buffer_store_dwordx4 v[196:199], v248, s[16:19], s29 offen offset:0 sc1\n\t"
"v_mfma_f32_16x16x32_bf16 v[136:139], a[216:219], a[180:183], v[136:139]\n\t"
"s_waitcnt lgkmcnt(3)\n\t"
"v_cvt_pk_bf16_f32 v104, v104, v105\n\t"
"v_cvt_pk_bf16_f32 v105, v106, v107\n\t"
"v_mfma_f32_16x16x32_bf16 v[136:139], a[224:227], a[184:187], v[136:139]\n\t"
"v_mfma_f32_16x16x32_bf16 v[136:139], a[228:231], a[172:175], v[136:139]\n\t"
"buffer_store_dwordx4 v[200:203], v248, s[16:19], s29 offen offset:128 sc1\n\t"
"v_mfma_f32_16x16x32_bf16 v[168:171], a[4:7], a[184:187], v[168:171]\n\t"
"ds_read_b128 v[208:211], v255 offset:14336\n\t"
"v_mfma_f32_16x16x32_bf16 v[168:171], a[80:83], a[172:175], v[168:171]\n\t"
"v_mfma_f32_16x16x32_bf16 v[168:171], a[76:79], a[180:183], v[168:171]\n\t"
"buffer_store_dwordx4 v[204:207], v248, s[16:19], s29 offen offset:256 sc1\n\t"
"v_mfma_f32_16x16x32_bf16 v[168:171], a[8:11], a[192:195], v[168:171]\n\t"
"ds_read_b128 a[180:183], v255 offset:14848\n\t"
"v_cvt_pk_bf16_f32 v106, v136, v137\n\t"
"v_mfma_f32_16x16x32_bf16 v[168:171], a[12:15], a[196:199], v[168:171]\n\t"
"v_cvt_pk_bf16_f32 v107, v138, v139\n\t"
"v_mfma_f32_16x16x32_bf16 v[168:171], a[232:235], a[176:179], v[168:171]\n\t"
"ds_write_b128 v249, v[104:107] offset:36864\n\t"
"v_mfma_f32_16x16x32_bf16 v[8:11], a[148:151], v[212:215], v[8:11]\n\t"
"s_waitcnt lgkmcnt(5)\n\t"
"v_mfma_f32_16x16x32_bf16 v[8:11], a[208:211], a[200:203], v[8:11]\n\t"
"v_mfma_f32_16x16x32_bf16 v[8:11], a[220:223], a[188:191], v[8:11]\n\t"
"v_mfma_f32_16x16x32_bf16 v[8:11], a[240:243], a[0:3], v[8:11]\n\t"
"v_cvt_pk_bf16_f32 v168, v168, v169\n\t"
"v_cvt_pk_bf16_f32 v169, v170, v171\n\t"
"v_mfma_f32_16x16x32_bf16 v[8:11], a[104:107], a[168:171], v[8:11]\n\t"
"v_mfma_f32_16x16x32_bf16 v[8:11], a[156:159], a[204:207], v[8:11]\n\t"
"ds_write_b64 v250, v[168:169] offset:36864\n\t"
"v_mfma_f32_16x16x32_bf16 v[40:43], v[240:243], a[168:171], v[40:43]\n\t"
"v_mfma_f32_16x16x32_bf16 v[40:43], v[96:99], a[204:207], v[40:43]\n\t"
"v_mfma_f32_16x16x32_bf16 v[40:43], a[164:167], a[0:3], v[40:43]\n\t"
"v_mfma_f32_16x16x32_bf16 v[40:43], a[244:247], v[212:215], v[40:43]\n\t"
"v_cvt_pk_bf16_f32 v8, v8, v9\n\t"
"v_cvt_pk_bf16_f32 v9, v10, v11\n\t"
"v_mfma_f32_16x16x32_bf16 v[40:43], a[52:55], a[200:203], v[40:43]\n\t"
"v_mfma_f32_16x16x32_bf16 v[40:43], a[56:59], a[188:191], v[40:43]\n\t"
"v_mfma_f32_16x16x32_bf16 v[72:75], v[236:239], a[200:203], v[72:75]\n\t"
"ds_read_b128 a[192:195], v255 offset:15872\n\t"
"v_mfma_f32_16x16x32_bf16 v[72:75], a[60:63], a[188:191], v[72:75]\n\t"
"v_mfma_f32_16x16x32_bf16 v[72:75], a[48:51], v[212:215], v[72:75]\n\t"
"s_waitcnt lgkmcnt(1)\n\t"
"s_barrier\n\t"
"v_mfma_f32_16x16x32_bf16 v[72:75], a[212:215], a[0:3], v[72:75]\n\t"
"ds_read_b128 v[196:199], v252 offset:30720\n\t"
"v_mfma_f32_16x16x32_bf16 v[72:75], a[248:251], a[168:171], v[72:75]\n\t"
"ds_read_b128 v[200:203], v252 offset:31744\n\t"
"v_cvt_pk_bf16_f32 v10, v40, v41\n\t"
"v_mfma_f32_16x16x32_bf16 v[72:75], a[28:31], a[204:207], v[72:75]\n\t"
"ds_read_b128 v[204:207], v252 offset:32768\n\t"
"v_cvt_pk_bf16_f32 v11, v42, v43\n\t"
"v_mfma_f32_16x16x32_bf16 v[108:111], a[112:115], a[184:187], v[108:111]\n\t"
"s_add_u32 s29, s23, 196608\n\t"
"ds_read_b128 v[212:215], v255 offset:16384\n\t"
"v_mfma_f32_16x16x32_bf16 v[108:111], a[120:123], a[172:175], v[108:111]\n\t"
"v_mfma_f32_16x16x32_bf16 v[108:111], a[64:67], v[208:211], v[108:111]\n\t"
"ds_write_b128 v249, v[8:11] offset:18432\n\t"
"v_mfma_f32_16x16x32_bf16 v[108:111], a[68:71], a[196:199], v[108:111]\n\t"
"s_waitcnt lgkmcnt(4)\n\t"
"v_cvt_pk_bf16_f32 v72, v72, v73\n\t"
"v_cvt_pk_bf16_f32 v73, v74, v75\n\t"
"v_mfma_f32_16x16x32_bf16 v[108:111], a[72:75], a[176:179], v[108:111]\n\t"
"v_mfma_f32_16x16x32_bf16 v[108:111], a[40:43], a[180:183], v[108:111]\n\t"
"ds_write_b64 v250, v[72:73] offset:18432\n\t"
"v_mfma_f32_16x16x32_bf16 v[140:143], a[32:35], a[176:179], v[140:143]\n\t"
"s_waitcnt lgkmcnt(4)\n\t"
"v_mfma_f32_16x16x32_bf16 v[140:143], a[36:39], a[180:183], v[140:143]\n\t"
"v_mfma_f32_16x16x32_bf16 v[140:143], a[44:47], a[196:199], v[140:143]\n\t"
"buffer_store_dwordx4 v[196:199], v248, s[16:19], s29 offen offset:0 sc1\n\t"
"v_mfma_f32_16x16x32_bf16 v[140:143], a[216:219], a[184:187], v[140:143]\n\t"
"s_waitcnt lgkmcnt(3)\n\t"
"v_cvt_pk_bf16_f32 v108, v108, v109\n\t"
"v_cvt_pk_bf16_f32 v109, v110, v111\n\t"
"v_mfma_f32_16x16x32_bf16 v[140:143], a[224:227], a[172:175], v[140:143]\n\t"
"v_mfma_f32_16x16x32_bf16 v[140:143], a[228:231], v[208:211], v[140:143]\n\t"
"buffer_store_dwordx4 v[200:203], v248, s[16:19], s29 offen offset:128 sc1\n\t"
"v_mfma_f32_16x16x32_bf16 v[172:175], a[4:7], a[172:175], v[172:175]\n\t"
"ds_read_b128 v[216:219], v255 offset:17408\n\t"
"v_mfma_f32_16x16x32_bf16 v[172:175], a[80:83], v[208:211], v[172:175]\n\t"
"v_mfma_f32_16x16x32_bf16 v[172:175], a[76:79], a[184:187], v[172:175]\n\t"
"buffer_store_dwordx4 v[204:207], v248, s[16:19], s29 offen offset:256 sc1\n\t"
"v_mfma_f32_16x16x32_bf16 v[172:175], a[8:11], a[196:199], v[172:175]\n\t"
"ds_read_b128 a[184:187], v255 offset:17920\n\t"
"v_cvt_pk_bf16_f32 v110, v140, v141\n\t"
"v_mfma_f32_16x16x32_bf16 v[172:175], a[12:15], a[176:179], v[172:175]\n\t"
"v_cvt_pk_bf16_f32 v111, v142, v143\n\t"
"v_mfma_f32_16x16x32_bf16 v[172:175], a[232:235], a[180:183], v[172:175]\n\t"
"ds_write_b128 v249, v[108:111] offset:24576\n\t"
"v_mfma_f32_16x16x32_bf16 v[12:15], a[148:151], a[200:203], v[12:15]\n\t"
"s_waitcnt lgkmcnt(5)\n\t"
"v_mfma_f32_16x16x32_bf16 v[12:15], a[208:211], a[188:191], v[12:15]\n\t"
"v_mfma_f32_16x16x32_bf16 v[12:15], a[220:223], a[192:195], v[12:15]\n\t"
"v_mfma_f32_16x16x32_bf16 v[12:15], a[240:243], a[168:171], v[12:15]\n\t"
"v_cvt_pk_bf16_f32 v172, v172, v173\n\t"
"v_cvt_pk_bf16_f32 v173, v174, v175\n\t"
"v_mfma_f32_16x16x32_bf16 v[12:15], a[104:107], a[204:207], v[12:15]\n\t"
"v_mfma_f32_16x16x32_bf16 v[12:15], a[156:159], v[212:215], v[12:15]\n\t"
"ds_write_b64 v250, v[172:173] offset:24576\n\t"
"v_mfma_f32_16x16x32_bf16 v[44:47], v[240:243], a[204:207], v[44:47]\n\t"
"v_mfma_f32_16x16x32_bf16 v[44:47], v[96:99], v[212:215], v[44:47]\n\t"
"v_mfma_f32_16x16x32_bf16 v[44:47], a[164:167], a[168:171], v[44:47]\n\t"
"v_mfma_f32_16x16x32_bf16 v[44:47], a[244:247], a[200:203], v[44:47]\n\t"
"v_cvt_pk_bf16_f32 v12, v12, v13\n\t"
"v_cvt_pk_bf16_f32 v13, v14, v15\n\t"
"v_mfma_f32_16x16x32_bf16 v[44:47], a[52:55], a[188:191], v[44:47]\n\t"
"v_mfma_f32_16x16x32_bf16 v[44:47], a[56:59], a[192:195], v[44:47]\n\t"
"v_mfma_f32_16x16x32_bf16 v[76:79], v[236:239], a[188:191], v[76:79]\n\t"
"ds_read_b128 a[196:199], v255 offset:18944\n\t"
"v_mfma_f32_16x16x32_bf16 v[76:79], a[60:63], a[192:195], v[76:79]\n\t"
"v_mfma_f32_16x16x32_bf16 v[76:79], a[48:51], a[200:203], v[76:79]\n\t"
"s_waitcnt lgkmcnt(1)\n\t"
"s_barrier\n\t"
"v_mfma_f32_16x16x32_bf16 v[76:79], a[212:215], a[168:171], v[76:79]\n\t"
"ds_read_b128 v[196:199], v252 offset:18432\n\t"
"v_mfma_f32_16x16x32_bf16 v[76:79], a[248:251], a[204:207], v[76:79]\n\t"
"ds_read_b128 v[200:203], v252 offset:19456\n\t"
"v_cvt_pk_bf16_f32 v14, v44, v45\n\t"
"v_mfma_f32_16x16x32_bf16 v[76:79], a[28:31], v[212:215], v[76:79]\n\t"
"ds_read_b128 v[204:207], v252 offset:20480\n\t"
"v_cvt_pk_bf16_f32 v15, v46, v47\n\t"
"v_mfma_f32_16x16x32_bf16 v[112:115], a[112:115], a[172:175], v[112:115]\n\t"
"s_add_u32 s29, s23, 393216\n\t"
"ds_read_b128 a[168:171], v255 offset:19456\n\t"
"v_mfma_f32_16x16x32_bf16 v[112:115], a[120:123], v[208:211], v[112:115]\n\t"
"v_mfma_f32_16x16x32_bf16 v[112:115], a[64:67], v[216:219], v[112:115]\n\t"
"ds_write_b128 v249, v[12:15] offset:30720\n\t"
"v_mfma_f32_16x16x32_bf16 v[112:115], a[68:71], a[176:179], v[112:115]\n\t"
"s_waitcnt lgkmcnt(4)\n\t"
"v_cvt_pk_bf16_f32 v76, v76, v77\n\t"
"v_cvt_pk_bf16_f32 v77, v78, v79\n\t"
"v_mfma_f32_16x16x32_bf16 v[112:115], a[72:75], a[180:183], v[112:115]\n\t"
"v_mfma_f32_16x16x32_bf16 v[112:115], a[40:43], a[184:187], v[112:115]\n\t"
"ds_write_b64 v250, v[76:77] offset:30720\n\t"
"v_mfma_f32_16x16x32_bf16 v[144:147], a[32:35], a[180:183], v[144:147]\n\t"
"s_waitcnt lgkmcnt(4)\n\t"
"v_mfma_f32_16x16x32_bf16 v[144:147], a[36:39], a[184:187], v[144:147]\n\t"
"v_mfma_f32_16x16x32_bf16 v[144:147], a[44:47], a[176:179], v[144:147]\n\t"
"buffer_store_dwordx4 v[196:199], v248, s[16:19], s29 offen offset:0 sc1\n\t"
"v_mfma_f32_16x16x32_bf16 v[144:147], a[216:219], a[172:175], v[144:147]\n\t"
"s_waitcnt lgkmcnt(3)\n\t"
"v_cvt_pk_bf16_f32 v112, v112, v113\n\t"
"v_cvt_pk_bf16_f32 v113, v114, v115\n\t"
"v_mfma_f32_16x16x32_bf16 v[144:147], a[224:227], v[208:211], v[144:147]\n\t"
"v_mfma_f32_16x16x32_bf16 v[144:147], a[228:231], v[216:219], v[144:147]\n\t"
"buffer_store_dwordx4 v[200:203], v248, s[16:19], s29 offen offset:128 sc1\n\t"
"v_mfma_f32_16x16x32_bf16 v[176:179], a[4:7], v[208:211], v[176:179]\n\t"
"ds_read_b128 a[200:203], v255 offset:20480\n\t"
"v_mfma_f32_16x16x32_bf16 v[176:179], a[80:83], v[216:219], v[176:179]\n\t"
"v_mfma_f32_16x16x32_bf16 v[176:179], a[76:79], a[172:175], v[176:179]\n\t"
"buffer_store_dwordx4 v[204:207], v248, s[16:19], s29 offen offset:256 sc1\n\t"
"v_mfma_f32_16x16x32_bf16 v[176:179], a[8:11], a[176:179], v[176:179]\n\t"
"ds_read_b128 a[172:175], v255 offset:20992\n\t"
"v_cvt_pk_bf16_f32 v114, v144, v145\n\t"
"v_mfma_f32_16x16x32_bf16 v[176:179], a[12:15], a[180:183], v[176:179]\n\t"
"v_cvt_pk_bf16_f32 v115, v146, v147\n\t"
"v_mfma_f32_16x16x32_bf16 v[176:179], a[232:235], a[184:187], v[176:179]\n\t"
"ds_write_b128 v249, v[112:115] offset:36864\n\t"
"Lst35_%=:\n\t"
"v_mfma_f32_16x16x32_bf16 v[16:19], a[148:151], a[188:191], v[16:19]\n\t"
"s_waitcnt lgkmcnt(5)\n\t"
"v_mfma_f32_16x16x32_bf16 v[16:19], a[208:211], a[192:195], v[16:19]\n\t"
"v_mfma_f32_16x16x32_bf16 v[16:19], a[220:223], a[196:199], v[16:19]\n\t"
"v_mfma_f32_16x16x32_bf16 v[16:19], a[240:243], a[204:207], v[16:19]\n\t"
"v_cvt_pk_bf16_f32 v176, v176, v177\n\t"
"v_cvt_pk_bf16_f32 v177, v178, v179\n\t"
"v_mfma_f32_16x16x32_bf16 v[16:19], a[104:107], v[212:215], v[16:19]\n\t"
"v_mfma_f32_16x16x32_bf16 v[16:19], a[156:159], a[168:171], v[16:19]\n\t"
"ds_write_b64 v250, v[176:177] offset:36864\n\t"
"v_mfma_f32_16x16x32_bf16 v[48:51], v[240:243], v[212:215], v[48:51]\n\t"
"v_mfma_f32_16x16x32_bf16 v[48:51], v[96:99], a[168:171], v[48:51]\n\t"
"v_mfma_f32_16x16x32_bf16 v[48:51], a[164:167], a[204:207], v[48:51]\n\t"
"v_mfma_f32_16x16x32_bf16 v[48:51], a[244:247], a[188:191], v[48:51]\n\t"
"v_cvt_pk_bf16_f32 v16, v16, v17\n\t"
"v_cvt_pk_bf16_f32 v17, v18, v19\n\t"
"v_mfma_f32_16x16x32_bf16 v[48:51], a[52:55], a[192:195], v[48:51]\n\t"
"v_mfma_f32_16x16x32_bf16 v[48:51], a[56:59], a[196:199], v[48:51]\n\t"
"v_mfma_f32_16x16x32_bf16 v[80:83], v[236:239], a[192:195], v[80:83]\n\t"
"ds_read_b128 a[176:179], v255 offset:22016\n\t"
"v_mfma_f32_16x16x32_bf16 v[80:83], a[60:63], a[196:199], v[80:83]\n\t"
"v_mfma_f32_16x16x32_bf16 v[80:83], a[48:51], a[188:191], v[80:83]\n\t"
"s_waitcnt lgkmcnt(1)\n\t"
"s_barrier\n\t"
"v_mfma_f32_16x16x32_bf16 v[80:83], a[212:215], a[204:207], v[80:83]\n\t"
"ds_read_b128 v[196:199], v252 offset:30720\n\t"
"v_mfma_f32_16x16x32_bf16 v[80:83], a[248:251], v[212:215], v[80:83]\n\t"
"ds_read_b128 v[200:203], v252 offset:31744\n\t"
"v_cvt_pk_bf16_f32 v18, v48, v49\n\t"
"v_mfma_f32_16x16x32_bf16 v[80:83], a[28:31], a[168:171], v[80:83]\n\t"
"ds_read_b128 v[204:207], v252 offset:32768\n\t"
"v_cvt_pk_bf16_f32 v19, v50, v51\n\t"
"v_mfma_f32_16x16x32_bf16 v[116:119], a[112:115], v[208:211], v[116:119]\n\t"
"s_add_u32 s29, s23, 589824\n\t"
"ds_read_b128 a[188:191], v255 offset:22528\n\t"
"v_mfma_f32_16x16x32_bf16 v[116:119], a[120:123], v[216:219], v[116:119]\n\t"
"v_mfma_f32_16x16x32_bf16 v[116:119], a[64:67], a[200:203], v[116:119]\n\t"
"ds_write_b128 v249, v[16:19] offset:18432\n\t"
"v_mfma_f32_16x16x32_bf16 v[116:119], a[68:71], a[180:183], v[116:119]\n\t"
"s_waitcnt lgkmcnt(4)\n\t"
"v_cvt_pk_bf16_f32 v80, v80, v81\n\t"
"v_cvt_pk_bf16_f32 v81, v82, v83\n\t"
"v_mfma_f32_16x16x32_bf16 v[116:119], a[72:75], a[184:187], v[116:119]\n\t"
"v_mfma_f32_16x16x32_bf16 v[116:119], a[40:43], a[172:175], v[116:119]\n\t"
"ds_write_b64 v250, v[80:81] offset:18432\n\t"
"v_mfma_f32_16x16x32_bf16 v[148:151], a[32:35], a[184:187], v[148:151]\n\t"
"s_waitcnt lgkmcnt(4)\n\t"
"v_mfma_f32_16x16x32_bf16 v[148:151], a[36:39], a[172:175], v[148:151]\n\t"
"v_mfma_f32_16x16x32_bf16 v[148:151], a[44:47], a[180:183], v[148:151]\n\t"
"buffer_store_dwordx4 v[196:199], v248, s[16:19], s29 offen offset:0 sc1\n\t"
"v_mfma_f32_16x16x32_bf16 v[148:151], a[216:219], v[208:211], v[148:151]\n\t"
"s_waitcnt lgkmcnt(3)\n\t"
"v_cvt_pk_bf16_f32 v116, v116, v117\n\t"
"v_cvt_pk_bf16_f32 v117, v118, v119\n\t"
"v_mfma_f32_16x16x32_bf16 v[148:151], a[224:227], v[216:219], v[148:151]\n\t"
"v_mfma_f32_16x16x32_bf16 v[148:151], a[228:231], a[200:203], v[148:151]\n\t"
"buffer_store_dwordx4 v[200:203], v248, s[16:19], s29 offen offset:128 sc1\n\t"
"v_mfma_f32_16x16x32_bf16 v[180:183], a[4:7], v[216:219], v[180:183]\n\t"
"ds_read_b128 a[204:207], v255 offset:23552\n\t"
"v_mfma_f32_16x16x32_bf16 v[180:183], a[80:83], a[200:203], v[180:183]\n\t"
"v_mfma_f32_16x16x32_bf16 v[180:183], a[76:79], v[208:211], v[180:183]\n\t"
"buffer_store_dwordx4 v[204:207], v248, s[16:19], s29 offen offset:256 sc1\n\t"
"v_mfma_f32_16x16x32_bf16 v[180:183], a[8:11], a[180:183], v[180:183]\n\t"
"ds_read_b128 a[180:183], v255 offset:24064\n\t"
"v_cvt_pk_bf16_f32 v118, v148, v149\n\t"
"v_mfma_f32_16x16x32_bf16 v[180:183], a[12:15], a[184:187], v[180:183]\n\t"
"v_cvt_pk_bf16_f32 v119, v150, v151\n\t"
"v_mfma_f32_16x16x32_bf16 v[180:183], a[232:235], a[172:175], v[180:183]\n\t"
"ds_write_b128 v249, v[116:119] offset:24576\n\t"
"v_mfma_f32_16x16x32_bf16 v[20:23], a[148:151], a[192:195], v[20:23]\n\t"
"s_waitcnt lgkmcnt(5)\n\t"
"v_mfma_f32_16x16x32_bf16 v[20:23], a[208:211], a[196:199], v[20:23]\n\t"
"v_mfma_f32_16x16x32_bf16 v[20:23], a[220:223], a[176:179], v[20:23]\n\t"
"v_mfma_f32_16x16x32_bf16 v[20:23], a[240:243], v[212:215], v[20:23]\n\t"
"v_cvt_pk_bf16_f32 v180, v180, v181\n\t"
"v_cvt_pk_bf16_f32 v181, v182, v183\n\t"
"v_mfma_f32_16x16x32_bf16 v[20:23], a[104:107], a[168:171], v[20:23]\n\t"
"v_mfma_f32_16x16x32_bf16 v[20:23], a[156:159], a[188:191], v[20:23]\n\t"
"ds_write_b64 v250, v[180:181] offset:24576\n\t"
"v_mfma_f32_16x16x32_bf16 v[52:55], v[240:243], a[168:171], v[52:55]\n\t"
"v_mfma_f32_16x16x32_bf16 v[52:55], v[96:99], a[188:191], v[52:55]\n\t"
"v_mfma_f32_16x16x32_bf16 v[52:55], a[164:167], v[212:215], v[52:55]\n\t"
"v_mfma_f32_16x16x32_bf16 v[52:55], a[244:247], a[192:195], v[52:55]\n\t"
"v_cvt_pk_bf16_f32 v20, v20, v21\n\t"
"v_cvt_pk_bf16_f32 v21, v22, v23\n\t"
"v_mfma_f32_16x16x32_bf16 v[52:55], a[52:55], a[196:199], v[52:55]\n\t"
"v_mfma_f32_16x16x32_bf16 v[52:55], a[56:59], a[176:179], v[52:55]\n\t"
"v_mfma_f32_16x16x32_bf16 v[84:87], v[236:239], a[196:199], v[84:87]\n\t"
"ds_read_b128 v[208:211], v255 offset:25088\n\t"
"v_mfma_f32_16x16x32_bf16 v[84:87], a[60:63], a[176:179], v[84:87]\n\t"
"v_mfma_f32_16x16x32_bf16 v[84:87], a[48:51], a[192:195], v[84:87]\n\t"
"s_waitcnt lgkmcnt(1)\n\t"
"s_barrier\n\t"
"v_mfma_f32_16x16x32_bf16 v[84:87], a[212:215], v[212:215], v[84:87]\n\t"
"ds_read_b128 v[196:199], v252 offset:18432\n\t"
"v_mfma_f32_16x16x32_bf16 v[84:87], a[248:251], a[168:171], v[84:87]\n\t"
"ds_read_b128 v[200:203], v252 offset:19456\n\t"
"v_cvt_pk_bf16_f32 v22, v52, v53\n\t"
"v_mfma_f32_16x16x32_bf16 v[84:87], a[28:31], a[188:191], v[84:87]\n\t"
"ds_read_b128 v[204:207], v252 offset:20480\n\t"
"v_cvt_pk_bf16_f32 v23, v54, v55\n\t"
"v_mfma_f32_16x16x32_bf16 v[120:123], a[112:115], v[216:219], v[120:123]\n\t"
"s_add_u32 s29, s23, 786432\n\t"
"ds_read_b128 a[192:195], v255 offset:25600\n\t"
"v_mfma_f32_16x16x32_bf16 v[120:123], a[120:123], a[200:203], v[120:123]\n\t"
"v_mfma_f32_16x16x32_bf16 v[120:123], a[64:67], a[204:207], v[120:123]\n\t"
"ds_write_b128 v249, v[20:23] offset:30720\n\t"
"v_mfma_f32_16x16x32_bf16 v[120:123], a[68:71], a[184:187], v[120:123]\n\t"
"s_waitcnt lgkmcnt(4)\n\t"
"v_cvt_pk_bf16_f32 v84, v84, v85\n\t"
"v_cvt_pk_bf16_f32 v85, v86, v87\n\t"
"v_mfma_f32_16x16x32_bf16 v[120:123], a[72:75], a[172:175], v[120:123]\n\t"
"v_mfma_f32_16x16x32_bf16 v[120:123], a[40:43], a[180:183], v[120:123]\n\t"
"ds_write_b64 v250, v[84:85] offset:30720\n\t"
"v_mfma_f32_16x16x32_bf16 v[152:155], a[32:35], a[172:175], v[152:155]\n\t"
"s_waitcnt lgkmcnt(4)\n\t"
"v_mfma_f32_16x16x32_bf16 v[152:155], a[36:39], a[180:183], v[152:155]\n\t"
"v_mfma_f32_16x16x32_bf16 v[152:155], a[44:47], a[184:187], v[152:155]\n\t"
"buffer_store_dwordx4 v[196:199], v248, s[16:19], s29 offen offset:0 sc1\n\t"
"v_mfma_f32_16x16x32_bf16 v[152:155], a[216:219], v[216:219], v[152:155]\n\t"
"s_waitcnt lgkmcnt(3)\n\t"
"v_cvt_pk_bf16_f32 v120, v120, v121\n\t"
"v_cvt_pk_bf16_f32 v121, v122, v123\n\t"
"v_mfma_f32_16x16x32_bf16 v[152:155], a[224:227], a[200:203], v[152:155]\n\t"
"v_mfma_f32_16x16x32_bf16 v[152:155], a[228:231], a[204:207], v[152:155]\n\t"
"buffer_store_dwordx4 v[200:203], v248, s[16:19], s29 offen offset:128 sc1\n\t"
"v_mfma_f32_16x16x32_bf16 v[184:187], a[4:7], a[200:203], v[184:187]\n\t"
"ds_read_b128 v[212:215], v255 offset:26624\n\t"
"v_mfma_f32_16x16x32_bf16 v[184:187], a[80:83], a[204:207], v[184:187]\n\t"
"v_mfma_f32_16x16x32_bf16 v[184:187], a[76:79], v[216:219], v[184:187]\n\t"
"buffer_store_dwordx4 v[204:207], v248, s[16:19], s29 offen offset:256 sc1\n\t"
"v_mfma_f32_16x16x32_bf16 v[184:187], a[8:11], a[184:187], v[184:187]\n\t"
"ds_read_b128 a[184:187], v255 offset:27136\n\t"
"v_cvt_pk_bf16_f32 v122, v152, v153\n\t"
"v_mfma_f32_16x16x32_bf16 v[184:187], a[12:15], a[172:175], v[184:187]\n\t"
"v_cvt_pk_bf16_f32 v123, v154, v155\n\t"
"v_mfma_f32_16x16x32_bf16 v[184:187], a[232:235], a[180:183], v[184:187]\n\t"
"ds_write_b128 v249, v[120:123] offset:36864\n\t"
"v_mfma_f32_16x16x32_bf16 v[24:27], a[148:151], a[196:199], v[24:27]\n\t"
"s_waitcnt lgkmcnt(5)\n\t"
"v_mfma_f32_16x16x32_bf16 v[24:27], a[208:211], a[176:179], v[24:27]\n\t"
"v_mfma_f32_16x16x32_bf16 v[24:27], a[220:223], v[208:211], v[24:27]\n\t"
"v_mfma_f32_16x16x32_bf16 v[24:27], a[240:243], a[168:171], v[24:27]\n\t"
"v_cvt_pk_bf16_f32 v184, v184, v185\n\t"
"v_cvt_pk_bf16_f32 v185, v186, v187\n\t"
"v_mfma_f32_16x16x32_bf16 v[24:27], a[104:107], a[188:191], v[24:27]\n\t"
"v_mfma_f32_16x16x32_bf16 v[24:27], a[156:159], a[192:195], v[24:27]\n\t"
"ds_write_b64 v250, v[184:185] offset:36864\n\t"
"v_mfma_f32_16x16x32_bf16 v[56:59], v[240:243], a[188:191], v[56:59]\n\t"
"v_mfma_f32_16x16x32_bf16 v[56:59], v[96:99], a[192:195], v[56:59]\n\t"
"v_mfma_f32_16x16x32_bf16 v[56:59], a[164:167], a[168:171], v[56:59]\n\t"
"v_mfma_f32_16x16x32_bf16 v[56:59], a[244:247], a[196:199], v[56:59]\n\t"
"v_cvt_pk_bf16_f32 v24, v24, v25\n\t"
"v_cvt_pk_bf16_f32 v25, v26, v27\n\t"
"v_mfma_f32_16x16x32_bf16 v[56:59], a[52:55], a[176:179], v[56:59]\n\t"
"v_mfma_f32_16x16x32_bf16 v[56:59], a[56:59], v[208:211], v[56:59]\n\t"
"v_mfma_f32_16x16x32_bf16 v[88:91], v[236:239], a[176:179], v[88:91]\n\t"
"ds_read_b128 v[216:219], v255 offset:28160\n\t"
"v_mfma_f32_16x16x32_bf16 v[88:91], a[60:63], v[208:211], v[88:91]\n\t"
"v_mfma_f32_16x16x32_bf16 v[88:91], a[48:51], a[196:199], v[88:91]\n\t"
"s_waitcnt lgkmcnt(1)\n\t"
"s_barrier\n\t"
"v_mfma_f32_16x16x32_bf16 v[88:91], a[212:215], a[168:171], v[88:91]\n\t"
"ds_read_b128 v[196:199], v252 offset:30720\n\t"
"v_mfma_f32_16x16x32_bf16 v[88:91], a[248:251], a[188:191], v[88:91]\n\t"
"ds_read_b128 v[200:203], v252 offset:31744\n\t"
"v_cvt_pk_bf16_f32 v26, v56, v57\n\t"
"v_mfma_f32_16x16x32_bf16 v[88:91], a[28:31], a[192:195], v[88:91]\n\t"
"ds_read_b128 v[204:207], v252 offset:32768\n\t"
"v_cvt_pk_bf16_f32 v27, v58, v59\n\t"
"v_mfma_f32_16x16x32_bf16 v[124:127], a[112:115], a[200:203], v[124:127]\n\t"
"s_add_u32 s29, s23, 983040\n\t"
"ds_read_b128 a[168:171], v255 offset:28672\n\t"
"v_mfma_f32_16x16x32_bf16 v[124:127], a[120:123], a[204:207], v[124:127]\n\t"
"v_mfma_f32_16x16x32_bf16 v[124:127], a[64:67], v[212:215], v[124:127]\n\t"
"ds_write_b128 v249, v[24:27] offset:18432\n\t"
"v_mfma_f32_16x16x32_bf16 v[124:127], a[68:71], a[172:175], v[124:127]\n\t"
"s_waitcnt lgkmcnt(4)\n\t"
"v_cvt_pk_bf16_f32 v88, v88, v89\n\t"
"v_cvt_pk_bf16_f32 v89, v90, v91\n\t"
"v_mfma_f32_16x16x32_bf16 v[124:127], a[72:75], a[180:183], v[124:127]\n\t"
"v_mfma_f32_16x16x32_bf16 v[124:127], a[40:43], a[184:187], v[124:127]\n\t"
"ds_write_b64 v250, v[88:89] offset:18432\n\t"
"v_mfma_f32_16x16x32_bf16 v[156:159], a[32:35], a[180:183], v[156:159]\n\t"
"s_waitcnt lgkmcnt(4)\n\t"
"v_mfma_f32_16x16x32_bf16 v[156:159], a[36:39], a[184:187], v[156:159]\n\t"
"v_mfma_f32_16x16x32_bf16 v[156:159], a[44:47], a[172:175], v[156:159]\n\t"
"buffer_store_dwordx4 v[196:199], v248, s[16:19], s29 offen offset:0 sc1\n\t"
"v_mfma_f32_16x16x32_bf16 v[156:159], a[216:219], a[200:203], v[156:159]\n\t"
"s_waitcnt lgkmcnt(3)\n\t"
"v_cvt_pk_bf16_f32 v124, v124, v125\n\t"
"v_cvt_pk_bf16_f32 v125, v126, v127\n\t"
"v_mfma_f32_16x16x32_bf16 v[156:159], a[224:227], a[204:207], v[156:159]\n\t"
"v_mfma_f32_16x16x32_bf16 v[156:159], a[228:231], v[212:215], v[156:159]\n\t"
"buffer_store_dwordx4 v[200:203], v248, s[16:19], s29 offen offset:128 sc1\n\t"
"v_mfma_f32_16x16x32_bf16 v[188:191], a[4:7], a[204:207], v[188:191]\n\t"
"ds_read_b128 a[196:199], v255 offset:29696\n\t"
"v_mfma_f32_16x16x32_bf16 v[188:191], a[80:83], v[212:215], v[188:191]\n\t"
"v_mfma_f32_16x16x32_bf16 v[188:191], a[76:79], a[200:203], v[188:191]\n\t"
"buffer_store_dwordx4 v[204:207], v248, s[16:19], s29 offen offset:256 sc1\n\t"
"v_mfma_f32_16x16x32_bf16 v[188:191], a[8:11], a[172:175], v[188:191]\n\t"
"ds_read_b128 a[172:175], v255 offset:30208\n\t"
"v_cvt_pk_bf16_f32 v126, v156, v157\n\t"
"v_mfma_f32_16x16x32_bf16 v[188:191], a[12:15], a[180:183], v[188:191]\n\t"
"v_cvt_pk_bf16_f32 v127, v158, v159\n\t"
"v_mfma_f32_16x16x32_bf16 v[188:191], a[232:235], a[184:187], v[188:191]\n\t"
"ds_write_b128 v249, v[124:127] offset:24576\n\t"
"v_mfma_f32_16x16x32_bf16 v[28:31], a[148:151], a[176:179], v[28:31]\n\t"
"s_waitcnt lgkmcnt(5)\n\t"
"v_mfma_f32_16x16x32_bf16 v[28:31], a[208:211], v[208:211], v[28:31]\n\t"
"v_mfma_f32_16x16x32_bf16 v[28:31], a[220:223], v[216:219], v[28:31]\n\t"
"v_mfma_f32_16x16x32_bf16 v[28:31], a[240:243], a[188:191], v[28:31]\n\t"
"v_cvt_pk_bf16_f32 v188, v188, v189\n\t"
"v_cvt_pk_bf16_f32 v189, v190, v191\n\t"
"v_mfma_f32_16x16x32_bf16 v[28:31], a[104:107], a[192:195], v[28:31]\n\t"
"v_mfma_f32_16x16x32_bf16 v[28:31], a[156:159], a[168:171], v[28:31]\n\t"
"ds_write_b64 v250, v[188:189] offset:24576\n\t"
"v_mfma_f32_16x16x32_bf16 v[60:63], v[240:243], a[192:195], v[60:63]\n\t"
"v_mfma_f32_16x16x32_bf16 v[60:63], v[96:99], a[168:171], v[60:63]\n\t"
"v_mfma_f32_16x16x32_bf16 v[60:63], a[164:167], a[188:191], v[60:63]\n\t"
"v_mfma_f32_16x16x32_bf16 v[60:63], a[244:247], a[176:179], v[60:63]\n\t"
"v_cvt_pk_bf16_f32 v28, v28, v29\n\t"
"v_cvt_pk_bf16_f32 v29, v30, v31\n\t"
"v_mfma_f32_16x16x32_bf16 v[60:63], a[52:55], v[208:211], v[60:63]\n\t"
"v_mfma_f32_16x16x32_bf16 v[60:63], a[56:59], v[216:219], v[60:63]\n\t"
"v_mfma_f32_16x16x32_bf16 v[92:95], v[236:239], v[208:211], v[92:95]\n\t"
"v_mfma_f32_16x16x32_bf16 v[92:95], a[60:63], v[216:219], v[92:95]\n\t"
"v_mfma_f32_16x16x32_bf16 v[92:95], a[48:51], a[176:179], v[92:95]\n\t"
"s_waitcnt lgkmcnt(0)\n\t"
"s_barrier\n\t"
"v_mfma_f32_16x16x32_bf16 v[92:95], a[212:215], a[188:191], v[92:95]\n\t"
"ds_read_b128 v[196:199], v252 offset:18432\n\t"
"v_mfma_f32_16x16x32_bf16 v[92:95], a[248:251], a[192:195], v[92:95]\n\t"
"ds_read_b128 v[200:203], v252 offset:19456\n\t"
"v_cvt_pk_bf16_f32 v30, v60, v61\n\t"
"v_mfma_f32_16x16x32_bf16 v[92:95], a[28:31], a[168:171], v[92:95]\n\t"
"ds_read_b128 v[204:207], v252 offset:20480\n\t"
"v_cvt_pk_bf16_f32 v31, v62, v63\n\t"
"v_mfma_f32_16x16x32_bf16 v[128:131], a[112:115], a[204:207], v[128:131]\n\t"
"s_add_u32 s29, s23, 1179648\n\t"
"v_mfma_f32_16x16x32_bf16 v[128:131], a[120:123], v[212:215], v[128:131]\n\t"
"v_mfma_f32_16x16x32_bf16 v[128:131], a[64:67], a[196:199], v[128:131]\n\t"
"ds_write_b128 v249, v[28:31] offset:30720\n\t"
"v_mfma_f32_16x16x32_bf16 v[128:131], a[68:71], a[180:183], v[128:131]\n\t"
"s_waitcnt lgkmcnt(3)\n\t"
"v_cvt_pk_bf16_f32 v92, v92, v93\n\t"
"v_cvt_pk_bf16_f32 v93, v94, v95\n\t"
"v_mfma_f32_16x16x32_bf16 v[128:131], a[72:75], a[184:187], v[128:131]\n\t"
"v_mfma_f32_16x16x32_bf16 v[128:131], a[40:43], a[172:175], v[128:131]\n\t"
"ds_write_b64 v250, v[92:93] offset:30720\n\t"
"v_mfma_f32_16x16x32_bf16 v[160:163], a[32:35], a[184:187], v[160:163]\n\t"
"s_waitcnt lgkmcnt(3)\n\t"
"v_mfma_f32_16x16x32_bf16 v[160:163], a[36:39], a[172:175], v[160:163]\n\t"
"v_mfma_f32_16x16x32_bf16 v[160:163], a[44:47], a[180:183], v[160:163]\n\t"
"buffer_store_dwordx4 v[196:199], v248, s[16:19], s29 offen offset:0 sc1\n\t"
"v_mfma_f32_16x16x32_bf16 v[160:163], a[216:219], a[204:207], v[160:163]\n\t"
"s_waitcnt lgkmcnt(2)\n\t"
"v_cvt_pk_bf16_f32 v128, v128, v129\n\t"
"v_cvt_pk_bf16_f32 v129, v130, v131\n\t"
"v_mfma_f32_16x16x32_bf16 v[160:163], a[224:227], v[212:215], v[160:163]\n\t"
"v_mfma_f32_16x16x32_bf16 v[160:163], a[228:231], a[196:199], v[160:163]\n\t"
"buffer_store_dwordx4 v[200:203], v248, s[16:19], s29 offen offset:128 sc1\n\t"
"v_mfma_f32_16x16x32_bf16 v[192:195], a[4:7], v[212:215], v[192:195]\n\t"
"v_mfma_f32_16x16x32_bf16 v[192:195], a[80:83], a[196:199], v[192:195]\n\t"
"v_mfma_f32_16x16x32_bf16 v[192:195], a[76:79], a[204:207], v[192:195]\n\t"
"buffer_store_dwordx4 v[204:207], v248, s[16:19], s29 offen offset:256 sc1\n\t"
"v_mfma_f32_16x16x32_bf16 v[192:195], a[8:11], a[180:183], v[192:195]\n\t"
"v_cvt_pk_bf16_f32 v130, v160, v161\n\t"
"v_cvt_pk_bf16_f32 v131, v162, v163\n\t"
"v_mfma_f32_16x16x32_bf16 v[192:195], a[12:15], a[184:187], v[192:195]\n\t"
"v_mfma_f32_16x16x32_bf16 v[192:195], a[232:235], a[172:175], v[192:195]\n\t"
"ds_write_b128 v249, v[128:131] offset:36864\n\t"
"s_nop 6\n\t"
"v_cvt_pk_bf16_f32 v192, v192, v193\n\t"
"v_cvt_pk_bf16_f32 v193, v194, v195\n\t"
"ds_write_b64 v250, v[192:193] offset:36864\n\t"
"s_waitcnt lgkmcnt(0)\n\t"
"s_barrier\n\t"
"ds_read_b128 v[196:199], v252 offset:30720\n\t"
"ds_read_b128 v[200:203], v252 offset:31744\n\t"
"ds_read_b128 v[208:211], v252 offset:32768\n\t"
"s_waitcnt lgkmcnt(2)\n\t"
"s_add_u32 s29, s23, 1376256\n\t"
"buffer_store_dwordx4 v[196:199], v248, s[16:19], s29 offen offset:0 sc1\n\t"
"s_waitcnt lgkmcnt(1)\n\t"
"buffer_store_dwordx4 v[200:203], v248, s[16:19], s29 offen offset:128 sc1\n\t"
"s_waitcnt lgkmcnt(0)\n\t"
"buffer_store_dwordx4 v[208:211], v248, s[16:19], s29 offen offset:256 sc1\n\t"
"s_waitcnt vmcnt(0)\n\t"
"Lwino_end:\n\t"
  : "+{v205}"(tid)
  : "s"((unsigned long long)__builtin_amdgcn_kernarg_segment_ptr()), "s"(blockIdx.x), "s"((unsigned long long)lane_tab), "s"((unsigned)(size_t)(__attribute__((address_space(3))) char*)smem), "s"((unsigned)(size_t)__builtin_amdgcn_dispatch_ptr())
  : "v0", "v1", "v2", "v3", "v4", "v5", "v6", "v7", "v8", "v9", "v10", "v11", "v12", "v13", "v14", "v15", "v16", "v17", "v18", "v19", "v20", "v21", "v22", "v23", "v24", "v25", "v26", "v27", "v28", "v29", "v30", "v31", "v32", "v33", "v34", "v35", "v36", "v37", "v38", "v39", "v40", "v41", "v42", "v43", "v44", "v45", "v46", "v47", "v48", "v49", "v50", "v51", "v52", "v53", "v54", "v55", "v56", "v57", "v58", "v59", "v60", "v61", "v62", "v63", "v64", "v65", "v66", "v67", "v68", "v69", "v70", "v71", "v72", "v73", "v74", "v75", "v76", "v77", "v78", "v79", "v80", "v81", "v82", "v83", "v84", "v85", "v86", "v87", "v88", "v89", "v90", "v91", "v92", "v93", "v94", "v95", "v96", "v97", "v98", "v99", "v100", "v101", "v102", "v103", "v104", "v105", "v106", "v107", "v108", "v109", "v110", "v111", "v112", "v113", "v114", "v115", "v116", "v117", "v118", "v119", "v120", "v121", "v122", "v123", "v124", "v125", "v126", "v127", "v128", "v129", "v130", "v131", "v132", "v133", "v134", "v135", "v136", "v137", "v138", "v139", "v140", "v141", "v142", "v143", "v144", "v145", "v146", "v147", "v148", "v149", "v150", "v151", "v152", "v153", "v154", "v155", "v156", "v157", "v158", "v159", "v160", "v161", "v162", "v163", "v164", "v165", "v166", "v167", "v168", "v169", "v170", "v171", "v172", "v173", "v174", "v175", "v176", "v177", "v178", "v179", "v180", "v181", "v182", "v183", "v184", "v185", "v186", "v187", "v188", "v189", "v190", "v191", "v192", "v193", "v194", "v195", "v196", "v197", "v198", "v199", "v200", "v201", "v202", "v203", "v204", "v206", "v207", "v208", "v209", "v210", "v211", "v212", "v213", "v214", "v215", "v216", "v217", "v218", "v219", "v220", "v221", "v222", "v223", "v224", "v225", "v226", "v227", "v228", "v229", "v230", "v231", "v232", "v233", "v234", "v235", "v236", "v237", "v238", "v239", "v240", "v241", "v242", "v243", "v244", "v245", "v246", "v247", "v248", "v249", "v250", "v251", "v252", "v253", "v254", "v255", "a0", "a1", "a2", "a3", "a4", "a5", "a6", "a7", "a8", "a9", "a10", "a11", "a12", "a13", "a14", "a15", "a16", "a17", "a18", "a19", "a20", "a21", "a22", "a23", "a24", "a25", "a26", "a27", "a28", "a29", "a30", "a31", "a32", "a33", "a34", "a35", "a36", "a37", "a38", "a39", "a40", "a41", "a42", "a43", "a44", "a45", "a46", "a47", "a48", "a49", "a50", "a51", "a52", "a53", "a54", "a55", "a56", "a57", "a58", "a59", "a60", "a61", "a62", "a63", "a64", "a65", "a66", "a67", "a68", "a69", "a70", "a71", "a72", "a73", "a74", "a75", "a76", "a77", "a78", "a79", "a80", "a81", "a82", "a83", "a84", "a85", "a86", "a87", "a88", "a89", "a90", "a91", "a92", "a93", "a94", "a95", "a96", "a97", "a98", "a99", "a100", "a101", "a102", "a103", "a104", "a105", "a106", "a107", "a108", "a109", "a110", "a111", "a112", "a113", "a114", "a115", "a116", "a117", "a118", "a119", "a120", "a121", "a122", "a123", "a124", "a125", "a126", "a127", "a128", "a129", "a130", "a131", "a132", "a133", "a134", "a135", "a136", "a137", "a138", "a139", "a140", "a141", "a142", "a143", "a144", "a145", "a146", "a147", "a148", "a149", "a150", "a151", "a152", "a153", "a154", "a155", "a156", "a157", "a158", "a159", "a160", "a161", "a162", "a163", "a164", "a165", "a166", "a167", "a168", "a169", "a170", "a171", "a172", "a173", "a174", "a175", "a176", "a177", "a178", "a179", "a180", "a181", "a182", "a183", "a184", "a185", "a186", "a187", "a188", "a189", "a190", "a191", "a192", "a193", "a194", "a195", "a196", "a197", "a198", "a199", "a200", "a201", "a202", "a203", "a204", "a205", "a206", "a207", "a208", "a209", "a210", "a211", "a212", "a213", "a214", "a215", "a216", "a217", "a218", "a219", "a220", "a221", "a222", "a223", "a224", "a225", "a226", "a227", "a228", "a229", "a230", "a231", "a232", "a233", "a234", "a235", "a236", "a237", "a238", "a239", "a240", "a241", "a242", "a243", "a244", "a245", "a246", "a247", "a248", "a249", "a250", "a251", "a252", "a253", "a254", "a255", "s8", "s9", "s10", "s11", "s12", "s13", "s14", "s15", "s16", "s17", "s18", "s19", "s20", "s21", "s22", "s23", "s24", "s25", "s26", "s27", "s28", "s29", "s30", "s31", "s32", "s33", "s34", "s35", "s36", "s37", "s38", "s39", "s40", "s41", "s42", "s43", "s44", "s45", "s46", "s47", "s48", "s49", "s50", "s51", "s52", "s53", "s54", "s55", "s56", "s57", "s58", "s59", "s60", "s61", "s62", "s63", "s64", "s65", "s66", "s67", "s68", "s69", "s70", "s71", "s72", "s73", "s74", "s75", "s76", "s77", "s78", "s79", "s80", "s81", "s82", "s83", "scc", "vcc", "memory");
}

extern "C" void kernel_launch(void* const* d_in, const int* in_sizes, int n_in, void* d_out, int out_size, void* d_ws,
                              size_t ws_size, hipStream_t stream) {
  wino_main_kernel<<<256, 256, 0, stream>>>((const char*)d_in[0], (char*)d_ws, (char*)d_out, (const char*)d_in[1],
                                            ws_size >= (36ull << 20) ? ~0ull : 0ull);
}
```

```cpp
#include <hip/hip_runtime.h>
#include <hip/hip_bf16.h>
#include <stdint.h>

#define IMG_W 512
#define CH 192
#define PXB 384
#define ROWB (IMG_W * PXB)
#define U_BYTES 1769472

__device__ const __attribute__((aligned(1024))) unsigned lane_tab[6144] = {
  0x0,0x0,0x0,0x0,0x0,0x0,0x0,0x0,0x0,0x0,0x0,0x0,0x0,0x0,0x0,0x0,
  0x0,0x0,0x0,0x0,0x0,0x0,0x0,0x0,0x0,0x0,0x0,0x0,0x0,0x0,0x0,0x0,
  0x0,0x0,0x0,0x0,0x0,0x0,0x0,0x0,0x0,0x0,0x0,0x0,0x0,0x0,0x0,0x0,
  0x0,0x0,0x0,0x0,0x0,0x0,0x0,0x0,0x0,0x0,0x0,0x0,0x0,0x0,0x0,0x0,
  0x0,0x0,0x0,0x0,0x0,0x0,0x0,0x0,0x0,0x0,0x0,0x0,0x0,0x0,0x0,0x0,
  0x0,0x0,0x0,0x0,0x0,0x0,0x0,0x0,0x0,0x0,0x0,0x0,0x0,0x0,0x0,0x0,
  0x0,0x0,0x0,0x0,0x0,0x0,0x0,0x0,0x0,0x0,0x0,0x0,0x0,0x0,0x0,0x0,
  0x0,0x0,0x0,0x0,0x0,0x0,0x0,0x0,0x0,0x0,0x0,0x0,0x0,0x0,0x0,0x0,
  0x0,0x0,0x0,0x0,0x0,0x0,0x0,0x0,0x0,0x0,0x0,0x0,0x0,0x0,0x0,0x0,
  0x0,0x0,0x0,0x0,0x0,0x0,0x0,0x0,0x0,0x0,0x0,0x0,0x0,0x0,0x0,0x0,
  0x0,0x0,0x0,0x0,0x0,0x0,0x0,0x0,0x0,0x0,0x0,0x0,0x0,0x0,0x0,0x0,
  0x0,0x0,0x0,0x0,0x0,0x0,0x0,0x0,0x0,0x0,0x0,0x0,0x0,0x0,0x0,0x0,
  0x0,0x0,0x0,0x0,0x0,0x0,0x0,0x0,0x0,0x0,0x0,0x0,0x0,0x0,0x0,0x0,
  0x0,0x0,0x0,0x0,0x0,0x0,0x0,0x0,0x0,0x0,0x0,0x0,0x0,0x0,0x0,0x0,
  0x0,0x0,0x0,0x0,0x0,0x0,0x0,0x0,0x0,0x0,0x0,0x0,0x0,0x0,0x0,0x0,
  0x0,0x0,0x0,0x0,0x0,0x0,0x0,0x0,0x0,0x0,0x0,0x0,0x0,0x0,0x0,0x0,
  0x0,0xc042,0x0,0x0,0x0,0xc0420000,0x0,0x0,0x0,0x0,0x0,0xc042,0x0,0x0,0x0,0xc0420000,
  0x3f903f90,0x0,0x0,0x0,0x3f900000,0x0,0x3f90,0x0,0x0,0x0,0x3f903f90,0x0,0x0,0x0,0x3f900000,0x0,
  0x0,0x0,0x0,0x0,0x0,0x0,0x0,0x0,0x0,0x0,0x0,0x0,0x0,0x0,0x0,0x0,
  0x0,0x0,0x0,0x0,0x0,0x0,0x0,0x0,0x0,0x0,0x0,0x0,0x0,0x0,0x0,0x0,
  0x0,0x0,0x3f90,0x0,0x0,0x0,0x0,0x0,0x0,0x0,0x0,0x0,0x0,0x0,0x0,0x0,
  0x0,0x0,0x0,0x0,0xc0420000,0x0,0x0,0x0,0x0,0x0,0x0,0x0,0x0,0x0,0xc0420000,0x0,
  0x0,0x3f90,0x0,0x0,0x0,0x3f903f90,0x0,0x0,0x0,0x3f900000,0x0,0x3f90,0x0,0x0,0x0,0x3f903f90,
  0x0,0x3f90,0x0,0x0,0x0,0x3f903f90,0x0,0x0,0x0,0x3f900000,0x0,0x3f90,0x0,0x0,0x0,0x3f903f90,
  0x0,0x0,0x0,0x0,0x0,0x0,0x0,0x0,0x0,0x0,0x0,0x0,0x0,0x0,0x0,0x0,
  0x0,0x0,0x0,0x0,0x0,0x0,0x0,0x0,0x0,0x0,0x0,0x0,0x0,0x0,0x3f90,0x0,
  0x0,0xc042,0x3f900000,0x0,0x0,0xc0420000,0x0,0x0,0x0,0x0,0x0,0xc042,0x0,0x0,0x0,0xc0420000,
  0x0,0xc042,0x3f900000,0x0,0x0,0xc0420000,0x0,0x0,0x0,0x0,0x0,0xc042,0x0,0x0,0x0,0xc0420000,
  0x3f900000,0x0,0x0,0x0,0x3f900000,0x3f90,0x0,0x0,0x0,0x3f90,0x3f900000,0x0,0x0,0x0,0x3f900000,0x3f90,
  0xc042,0x0,0x0,0x0,0x0,0x0,0x0,0x0,0x0,0x0,0xc042,0x0,0x0,0x0,0x0,0x0,
  0x0,0x0,0x0,0x0,0x0,0x0,0x0,0x0,0x0,0x0,0x0,0x0,0x0,0x0,0x0,0x0,
  0x0,0x0,0x0,0x0,0x0,0x0,0x0,0x0,0x0,0x0,0x0,0x0,0x0,0x0,0x0,0x0,
  0xa8000000,0xae0,0xa800,0x60,0xa8080040,0x400af1,0xa920,0x600000,0xa8100090,0x900a82,0xa808,0x6000,0xa81800d0,0xd00a93,0xa928,0x60000000,
  0xa9200120,0x24000ae4,0xa840,0x40004,0xa9280160,0x24400af5,0xa960,0x40400,0xa93001b0,0x24900a86,0xa848,0x4000400,0xa93801f0,0x24d00a97,0xa968,0x4000000,
  0xa8401000,0x2000ae1,0xa880,0x0,0xa8481040,0x2400af0,0xa9a0,0x0,0xa8501090,0x2900a83,0xa888,0x0,0xa85810d0,0x2d00a92,0xa9a8,0x0,
  0xa9601120,0x2000ae5,0xa8c0,0x0,0xa9681160,0x2400af4,0xa9e0,0x0,0xa97011b0,0x2900a87,0xa8c8,0x0,0xa97811f0,0x2d00a96,0xa9e8,0x0,
  0xea800010,0x100ae2,0xa810,0x400,0xea880050,0x500af3,0xa930,0x0,0xea900080,0x800a80,0xa818,0x0,0xea9800c0,0xc00a91,0xa938,0x0,
  0xa9a00130,0x24100ae6,0xa850,0x0,0xa9a80170,0x24500af7,0xa970,0x60000,0xa9b001a0,0x24800a84,0xa858,0x0,0xa9b801e0,0x24c00a95,0xa978,0x6000000,
  0xc0c01010,0x2100ae3,0xa890,0x40,0xc0c81050,0x2500af2,0xa9b0,0x400040,0xc0d01080,0x2800a81,0xa898,0x404000,0xc0d810c0,0x2c00a90,0xa9b8,0x40004000,
  0xc1e01130,0x2100ae7,0xa8d0,0x40,0xc1e81170,0x2500af6,0xa9f0,0x400040,0xc1f011a0,0x2800a85,0xa8d8,0x404000,0xc1f811e0,0x2c00a94,0xa9f8,0x40004000,
  0xae000020,0x200ae9,0xae00,0x0,0xae080060,0x600af8,0xaf20,0x0,0xae1000b0,0xb00a8b,0xae08,0x0,0xae1800f0,0xf00a9a,0xaf28,0x0,
  0xeb200100,0x24200aed,0xae40,0x0,0xeb280140,0x24600afc,0xaf60,0x0,0xeb300190,0x24b00a8f,0xae48,0x0,0xeb3801d0,0x24f00a9e,0xaf68,0x400,
  0xc0401020,0x2200ae8,0xae80,0x4000060,0xc0481060,0x2600af9,0xafa0,0x600000,0xc05010b0,0x2b00a8a,0xae88,0x6000,0xc05810f0,0x2f00a9b,0xafa8,0x60000000,
  0xc1601100,0x2200aec,0xaec0,0x4000060,0xc1681140,0x2600afd,0xafe0,0x600000,0xc1701190,0x2b00a8e,0xaec8,0x6000,0xc17811d0,0x2f00a9f,0xafe8,0x60000000,
  0xa8800030,0x300aeb,0xae10,0x40000,0xa8880070,0x700afa,0xaf30,0x40040,0xa89000a0,0xa00a89,0xae18,0x4000040,0xa89800e0,0xe00a98,0xaf38,0x4004000,
  0xa8c00110,0x24300aef,0xae50,0x6,0xa8c80150,0x24700afe,0xaf70,0x0,0xa8d00180,0x24a00a8d,0xae58,0x600,0xa8d801c0,0x24e00a9c,0xaf78,0x0,
  0xa9e01030,0x2300aea,0xae90,0x0,0xa9e81070,0x2700afb,0xafb0,0x0,0xa9f010a0,0x2a00a88,0xae98,0x0,0xa9f810e0,0x2e00a99,0xafb8,0x0,
  0xae001110,0x2300aee,0xaed0,0x0,0xae081150,0x2700aff,0xaff0,0x0,0xae101180,0x2a00a8c,0xaed8,0x0,0xae1811c0,0x2e00a9d,0xaff8,0x0,
  0x0,0x0,0x0,0x0,0x0,0x0,0x0,0x0,0x0,0x0,0x0,0x0,0x0,0x0,0x0,0x0,
  0x0,0x0,0x0,0x0,0x0,0x0,0x0,0x0,0x0,0x0,0x0,0x0,0x0,0x0,0x0,0x0,
  0x0,0x0,0x0,0x0,0x0,0x0,0x0,0x0,0x0,0x0,0x0,0x0,0x0,0x0,0x0,0x0,
  0x0,0x0,0x0,0x0,0x0,0x0,0x0,0x0,0x0,0x0,0x0,0x0,0x0,0x0,0x0,0x0,
  0x0,0x0,0x0,0x0,0x0,0x0,0x0,0x0,0x0,0x0,0x0,0x0,0x0,0x0,0x0,0x0,
  0x0,0x0,0x0,0x0,0x0,0x0,0x0,0x0,0x0,0x0,0x0,0x0,0x0,0x0,0x0,0x0,
  0x0,0x0,0x0,0x0,0x0,0x0,0x0,0x0,0x0,0x0,0x0,0x0,0x0,0x0,0x0,0x0,
  0x0,0x0,0x0,0x0,0x0,0x0,0x0,0x0,0x0,0x0,0x0,0x0,0x0,0x0,0x0,0x0,
  0x0,0x0,0x0,0x0,0x0,0x0,0x0,0x0,0x0,0x0,0x0,0x0,0x0,0x0,0x0,0x0,
  0x0,0x0,0x0,0x0,0x0,0x0,0x0,0x0,0x0,0x0,0x0,0x0,0x0,0x0,0x0,0x0,
  0x0,0x0,0x0,0x0,0x0,0x0,0x0,0x0,0x0,0x0,0x0,0x0,0x0,0x0,0x0,0x0,
  0x0,0x0,0x0,0x0,0x0,0x0,0x0,0x0,0x0,0x0,0x0,0x0,0x0,0x0,0x0,0x0,
  0x0,0x0,0x0,0x0,0x0,0x0,0x0,0x0,0x0,0x0,0x0,0x0,0x0,0x0,0x0,0x0,
  0x0,0x0,0x0,0x0,0x0,0x0,0x0,0x0,0x0,0x0,0x0,0x0,0x0,0x0,0x0,0x0,
  0x0,0x0,0x0,0x0,0x0,0x0,0x0,0x0,0x0,0x0,0x0,0x0,0x0,0x0,0x0,0x0,
  0x0,0x0,0x0,0x0,0x0,0x0,0x0,0x0,0x0,0x0,0x0,0x0,0x0,0x0,0x0,0x0,
  0xc0420000,0x0,0x0,0x0,0x0,0x0,0x0,0x0,0x0,0x0,0xc0420000,0x0,0x0,0x0,0x0,0x0,
  0x3f90,0x0,0x0,0x0,0x0,0x0,0x3f90,0x0,0x0,0x0,0x3f90,0x0,0x0,0x0,0x0,0x0,
  0x0,0xc042,0x0,0x0,0x0,0xc0420000,0x0,0x0,0x0,0x0,0x0,0xc042,0x0,0x0,0x0,0xc0420000,
  0x0,0xc042,0x0,0x0,0x0,0xc0420000,0x0,0x0,0x0,0x0,0x0,0xc042,0x0,0x0,0x0,0xc0420000,
  0x0,0x0,0x0,0x3f90,0x0,0x0,0x0,0x0,0x0,0x0,0x0,0x0,0x0,0x0,0x0,0x0,
  0x0,0x0,0x0,0x0,0xc042,0x0,0x0,0x0,0x0,0x0,0x0,0x0,0x0,0x0,0xc042,0x0,
  0x3f900000,0x0,0x0,0x3f900000,0x3f900000,0x0,0x0,0x0,0x0,0x0,0x3f900000,0x0,0x0,0x0,0x3f900000,0x0,
  0x3f900000,0x0,0x0,0x3f900000,0x3f900000,0x0,0x0,0x0,0x0,0x0,0x3f900000,0x0,0x0,0x0,0x3f900000,0x0,
  0x0,0x0,0x0,0x0,0x0,0xc042,0x0,0x0,0x0,0x0,0x0,0x0,0x0,0x0,0x0,0xc042,
  0x3f90,0xc0420000,0x0,0x0,0x3f90,0x0,0x0,0x0,0x0,0x0,0x3f90,0xc0420000,0x0,0x0,0x3f903f90,0x0,
  0x0,0x0,0x0,0x0,0x0,0x0,0x0,0x0,0x0,0x0,0x0,0x0,0x0,0x0,0x0,0x0,
  0x0,0x0,0x0,0x0,0x0,0x0,0x0,0x0,0x0,0x0,0x0,0x0,0x0,0x0,0x0,0x0,
  0x3f90,0x0,0x0,0x0,0x3f903f90,0x0,0x0,0x0,0x3f900000,0x0,0x3f90,0x0,0x0,0x0,0x3f903f90,0x0,
  0x0,0x0,0x0,0x0,0x0,0x0,0x0,0x0,0x0,0x0,0x0,0x0,0x0,0x0,0x0,0x0,
  0x0,0x0,0x0,0x0,0x0,0x3f90,0x0,0x0,0x0,0x3f90,0x0,0x0,0x0,0x0,0x0,0x3f90,
  0x0,0x0,0x0,0x0,0x0,0x3f90,0x0,0x0,0x0,0x3f90,0x0,0x0,0x0,0x0,0x0,0x3f90,
  0xb4000000,0x1200ba2,0xb400,0x60000,0xb4080040,0x1600bb3,0xb520,0x0,0xb4100090,0x1b00b40,0xb408,0x6000000,0xb41800d0,0x1f00b51,0xb528,0x0,
  0xb4400120,0x25200ba6,0xb440,0x4,0xb4480160,0x25600bb7,0xb560,0x400,0xb45001b0,0x25b00b44,0xb448,0x400,0xb45801f0,0x25f00b55,0xb568,0x0,
  0xcc401000,0x3200ba3,0xb480,0x60,0xcc481040,0x3600bb2,0xb5a0,0x600000,0xcc501090,0x3b00b41,0xb488,0x6000,0xcc5810d0,0x3f00b50,0xb5a8,0x60000000,
  0xcd601120,0x3200ba7,0xb4c0,0x60,0xcd681160,0x3600bb6,0xb5e0,0x600000,0xcd7011b0,0x3b00b45,0xb4c8,0x6000,0xcd7811f0,0x3f00b54,0xb5e8,0x60000000,
  0xb5a00010,0x1300ba0,0xb410,0x4000,0xb5a80050,0x1700bb1,0xb530,0x0,0xb5b00080,0x1a00b42,0xb418,0x0,0xb5b800c0,0x1e00b53,0xb538,0x0,
  0xccc00130,0x25300ba4,0xb450,0x0,0xccc80170,0x25700bb5,0xb570,0x6,0xccd001a0,0x25a00b46,0xb458,0x0,0xccd801e0,0x25e00b57,0xb578,0x600,
  0xa9e01010,0x3300ba1,0xb490,0x40040000,0xa9e81050,0x3700bb0,0xb5b0,0x40000,0xa9f01080,0x3a00b43,0xb498,0x4000000,0xa9f810c0,0x3e00b52,0xb5b8,0x4000000,
  0xc1e01130,0x3300ba5,0xb4d0,0x40040000,0xc1e81170,0x3700bb4,0xb5f0,0x40000,0xc1f011a0,0x3a00b47,0xb4d8,0x4000000,0xc1f811e0,0x3e00b56,0xb5f8,0x4000000,
  0xb5200020,0x1000bab,0xba00,0x0,0xb5280060,0x1400bba,0xbb20,0x60,0xb53000b0,0x1900b49,0xba08,0x0,0xb53800f0,0x1d00b58,0xbb28,0x6000,
  0xea400100,0x25000baf,0xba40,0x600004,0xea480140,0x25400bbe,0xbb60,0x4,0xea500190,0x25900b4d,0xba48,0x60000400,0xea5801d0,0x25d00b5c,0xbb68,0x4000400,
  0xb5601020,0x3000baa,0xba80,0x0,0xb5681060,0x3400bbb,0xbba0,0x0,0xb57010b0,0x3900b48,0xba88,0x0,0xb57810f0,0x3d00b59,0xbba8,0x0,
  0xb4801100,0x3000bae,0xbac0,0x0,0xb4881140,0x3400bbf,0xbbe0,0x0,0xb4901190,0x3900b4c,0xbac8,0x0,0xb49811d0,0x3d00b5d,0xbbe8,0x0,
  0xb4c00030,0x1100ba9,0xba10,0x4,0xb4c80070,0x1500bb8,0xbb30,0x40004,0xb4d000a0,0x1800b4b,0xba18,0x40400,0xb4d800e0,0x1c00b5a,0xbb38,0x4000400,
  0xb5e00110,0x25100bad,0xba50,0x0,0xb5e80150,0x25500bbc,0xbb70,0x0,0xb5f00180,0x25800b4f,0xba58,0x0,0xb5f801c0,0x25c00b5e,0xbb78,0x0,
  0xcde01030,0x3100ba8,0xba90,0x0,0xcde81070,0x3500bb9,0xbbb0,0x40,0xcdf010a0,0x3800b4a,0xba98,0x40,0xcdf810e0,0x3c00b5b,0xbbb8,0x4000,
  0xb5201110,0x3100bac,0xbad0,0x0,0xb5281150,0x3500bbd,0xbbf0,0x40,0xb5301180,0x3800b4e,0xbad8,0x40,0xb53811c0,0x3c00b5f,0xbbf8,0x4000,
  0x0,0x0,0x0,0x0,0x0,0x0,0x0,0x0,0x0,0x0,0x0,0x0,0x0,0x0,0x0,0x0,
  0x0,0x0,0x0,0x0,0x0,0x0,0x0,0x0,0x0,0x0,0x0,0x0,0x0,0x0,0x0,0x0,
  0x0,0x0,0x0,0x0,0x0,0x0,0x0,0x0,0x0,0x0,0x0,0x0,0x0,0x0,0x0,0x0,
  0x0,0x0,0x0,0x0,0x0,0x0,0x0,0x0,0x0,0x0,0x0,0x0,0x0,0x0,0x0,0x0,
  0x0,0x0,0x0,0x0,0x0,0x0,0x0,0x0,0x0,0x0,0x0,0x0,0x0,0x0,0x0,0x0,
  0x0,0x0,0x0,0x0,0x0,0x0,0x0,0x0,0x0,0x0,0x0,0x0,0x0,0x0,0x0,0x0,
  0x0,0x0,0x0,0x0,0x0,0x0,0x0,0x0,0x0,0x0,0x0,0x0,0x0,0x0,0x0,0x0,
  0x0,0x0,0x0,0x0,0x0,0x0,0x0,0x0,0x0,0x0,0x0,0x0,0x0,0x0,0x0,0x0,
  0x0,0x0,0x0,0x0,0x0,0x0,0x0,0x0,0x0,0x0,0x0,0x0,0x0,0x0,0x0,0x0,
  0x0,0x0,0x0,0x0,0x0,0x0,0x0,0x0,0x0,0x0,0x0,0x0,0x0,0x0,0x0,0x0,
  0x0,0x0,0x0,0x0,0x0,0x0,0x0,0x0,0x0,0x0,0x0,0x0,0x0,0x0,0x0,0x0,
  0x0,0x0,0x0,0x0,0x0,0x0,0x0,0x0,0x0,0x0,0x0,0x0,0x0,0x0,0x0,0x0,
  0x0,0x0,0x0,0x0,0x0,0x0,0x0,0x0,0x0,0x0,0x0,0x0,0x0,0x0,0x0,0x0,
  0x0,0x0,0x0,0x0,0x0,0x0,0x0,0x0,0x0,0x0,0x0,0x0,0x0,0x0,0x0,0x0,
  0x0,0x0,0x0,0x0,0x0,0x0,0x0,0x0,0x0,0x0,0x0,0x0,0x0,0x0,0x0,0x0,
  0x0,0x0,0x0,0x0,0x0,0x0,0x0,0x0,0x0,0x0,0x0,0x0,0x0,0x0,0x0,0x0,
  0x3f90,0x3f90,0x0,0x0,0x0,0x3f90,0x3f90,0x0,0x0,0x0,0x3f90,0x3f90,0x0,0x0,0x0,0x3f90,
  0x0,0xc0420000,0x0,0x0,0x0,0x0,0x0,0x0,0x0,0x0,0x0,0xc0420000,0x0,0x0,0x0,0x0,
  0x3f900000,0x0,0x0,0x0,0x0,0x0,0x3f900000,0x0,0x0,0x0,0x3f900000,0x0,0x0,0x0,0x0,0x0,
  0x3f900000,0x0,0x0,0x0,0x0,0x0,0x3f900000,0x0,0x0,0x0,0x3f900000,0x0,0x0,0x0,0x0,0x0,
  0xc042,0x0,0x0,0x0,0xc0420000,0x0,0x0,0x0,0x0,0x0,0xc042,0x0,0x0,0x0,0xc0420000,0x0,
  0x0,0x0,0x0,0x0,0x0,0x3f900000,0x0,0x0,0x0,0x3f900000,0x0,0x0,0x0,0x0,0x0,0x3f900000,
  0x0,0x0,0x0,0x0,0x0,0xc042,0x0,0x0,0x0,0x0,0x0,0x0,0x0,0x0,0x0,0xc042,
  0x0,0x0,0x0,0x0,0x0,0xc042,0x0,0x0,0x0,0x0,0x0,0x0,0x0,0x0,0x0,0xc042,
  0x0,0x0,0x0,0x0,0x0,0x0,0x0,0x0,0x0,0x0,0x0,0x0,0x0,0x0,0x3f90,0x0,
  0x0,0x0,0x0,0x0,0x0,0xc0420000,0x0,0x0,0x0,0x0,0x0,0x0,0x0,0x0,0x0,0xc0420000,
  0x0,0x3f90,0x0,0x0,0x0,0x3f90,0x0,0x0,0x0,0x0,0x0,0x3f90,0x0,0x0,0x3f900000,0x3f90,
  0x0,0x3f90,0x0,0x0,0x0,0x3f90,0x0,0x0,0x0,0x0,0x0,0x3f90,0x0,0x0,0x3f900000,0x3f90,
  0x0,0x0,0x0,0x0,0x0,0x0,0x0,0x0,0x0,0x0,0x0,0x0,0x0,0x0,0x0,0x0,
  0x0,0x3f90,0x3f900000,0x0,0x0,0x3f90,0x0,0x0,0x0,0x0,0x0,0x3f90,0x0,0x0,0x0,0x3f90,
  0xc042,0x0,0x0,0x0,0x0,0x0,0x0,0x0,0x0,0x0,0xc042,0x0,0x0,0x0,0x0,0x0,
  0xc042,0x0,0x0,0x0,0x0,0x0,0x0,0x0,0x0,0x0,0xc042,0x0,0x0,0x0,0x0,0x0,
  0xc0000000,0x26001980,0x19200,0x44,0xc0080040,0x26401991,0x19320,0x440,0xc0100090,0x26901922,0x19208,0x4400,0xc01800d0,0x26d01933,0x19328,0x4000,
  0xd8000120,0x4001984,0x19240,0x600000,0xd8080160,0x4401995,0x19360,0x0,0xd81001b0,0x4901926,0x19248,0x60000000,0xd81801f0,0x4d01937,0x19368,0x0,
  0xc1201000,0x28001981,0x19280,0x40000,0xc1281040,0x28401990,0x193a0,0x4000000,0xc1301090,0x28901923,0x19288,0x4000000,0xc13810d0,0x28d01932,0x193a8,0x0,
  0xd8401120,0x28001985,0x192c0,0x40000,0xd8481160,0x28401994,0x193e0,0x4000000,0xd85011b0,0x28901927,0x192c8,0x4000000,0xd85811f0,0x28d01936,0x193e8,0x0,
  0xc0800010,0x26101982,0x19210,0x6,0xc0880050,0x26501993,0x19330,0x60000,0xc0900080,0x26801920,0x19218,0x600,0xc09800c0,0x26c01931,0x19338,0x6000000,
  0xc1a00130,0x4101986,0x19250,0x0,0xc1a80170,0x4501997,0x19370,0x400000,0xc1b001a0,0x4801924,0x19258,0x400000,0xc1b801e0,0x4c01935,0x19378,0x40000000,
  0xd9a01010,0x28101983,0x19290,0x0,0xd9a81050,0x28501992,0x193b0,0x60,0xd9b01080,0x28801921,0x19298,0x0,0xd9b810c0,0x28c01930,0x193b8,0x6000,
  0xd9e01130,0x28101987,0x192d0,0x0,0xd9e81170,0x28501996,0x193f0,0x60,0xd9f011a0,0x28801925,0x192d8,0x0,0xd9f811e0,0x28c01934,0x193f8,0x6000,
  0xc6000020,0x26201989,0x19800,0x0,0xc6080060,0x26601998,0x19920,0x0,0xc61000b0,0x26b0192b,0x19808,0x0,0xc61800f0,0x26f0193a,0x19928,0x400,
  0xde000100,0x420198d,0x19840,0x0,0xde080140,0x460199c,0x19960,0x600000,0xde100190,0x4b0192f,0x19848,0x0,0xde1801d0,0x4f0193e,0x19968,0x60000000,
  0xd9201020,0x28201988,0x19880,0x40,0xd9281060,0x28601999,0x199a0,0x40,0xd93010b0,0x28b0192a,0x19888,0x4000,0xd93810f0,0x28f0193b,0x199a8,0x4004000,
  0xd9601100,0x2820198c,0x198c0,0x40,0xd9681140,0x2860199d,0x199e0,0x40,0xd9701190,0x28b0192e,0x198c8,0x4000,0xd97811d0,0x28f0193f,0x199e8,0x4004000,
  0xd8800030,0x2630198b,0x19810,0x0,0xd8880070,0x2670199a,0x19930,0x0,0xd89000a0,0x26a01929,0x19818,0x0,0xd89800e0,0x26e01938,0x19938,0x0,
  0xeba00110,0x430198f,0x19850,0x4000040,0xeba80150,0x470199e,0x19970,0x40,0xebb00180,0x4a0192d,0x19858,0x4000,0xebb801c0,0x4e0193c,0x19978,0x4000,
  0xd8c01030,0x2830198a,0x19890,0x6,0xd8c81070,0x2870199b,0x199b0,0x0,0xd8d010a0,0x28a01928,0x19898,0x600,0xd8d810e0,0x28e01939,0x199b8,0x0,
  0xc6001110,0x2830198e,0x198d0,0x6,0xc6081150,0x2870199f,0x199f0,0x0,0xc6101180,0x28a0192c,0x198d8,0x600,0xc61811c0,0x28e0193d,0x199f8,0x0,
  0x0,0x0,0x0,0x0,0x0,0x0,0x0,0x0,0x0,0x0,0x0,0x0,0x0,0x0,0x0,0x0,
  0x0,0x0,0x0,0x0,0x0,0x0,0x0,0x0,0x0,0x0,0x0,0x0,0x0,0x0,0x0,0x0,
  0x0,0x0,0x0,0x0,0x0,0x0,0x0,0x0,0x0,0x0,0x0,0x0,0x0,0x0,0x0,0x0,
  0x0,0x0,0x0,0x0,0x0,0x0,0x0,0x0,0x0,0x0,0x0,0x0,0x0,0x0,0x0,0x0,
  0x0,0x0,0x0,0x0,0x0,0x0,0x0,0x0,0x0,0x0,0x0,0x0,0x0,0x0,0x0,0x0,
  0x0,0x0,0x0,0x0,0x0,0x0,0x0,0x0,0x0,0x0,0x0,0x0,0x0,0x0,0x0,0x0,
  0x0,0x0,0x0,0x0,0x0,0x0,0x0,0x0,0x0,0x0,0x0,0x0,0x0,0x0,0x0,0x0,
  0x0,0x0,0x0,0x0,0x0,0x0,0x0,0x0,0x0,0x0,0x0,0x0,0x0,0x0,0x0,0x0,
  0x0,0x0,0x0,0x0,0x0,0x0,0x0,0x0,0x0,0x0,0x0,0x0,0x0,0x0,0x0,0x0,
  0x0,0x0,0x0,0x0,0x0,0x0,0x0,0x0,0x0,0x0,0x0,0x0,0x0,0x0,0x0,0x0,
  0x0,0x0,0x0,0x0,0x0,0x0,0x0,0x0,0x0,0x0,0x0,0x0,0x0,0x0,0x0,0x0,
  0x0,0x0,0x0,0x0,0x0,0x0,0x0,0x0,0x0,0x0,0x0,0x0,0x0,0x0,0x0,0x0,
  0x0,0x0,0x0,0x0,0x0,0x0,0x0,0x0,0x0,0x0,0x0,0x0,0x0,0x0,0x0,0x0,
  0x0,0x0,0x0,0x0,0x0,0x0,0x0,0x0,0x0,0x0,0x0,0x0,0x0,0x0,0x0,0x0,
  0x0,0x0,0x0,0x0,0x0,0x0,0x0,0x0,0x0,0x0,0x0,0x0,0x0,0x0,0x0,0x0,
  0x0,0x0,0x0,0x0,0x0,0x0,0x0,0x0,0x0,0x0,0x0,0x0,0x0,0x0,0x0,0x0,
  0x3f903f90,0x0,0x0,0x0,0x3f900000,0x0,0x3f90,0x0,0x0,0x0,0x3f903f90,0x0,0x0,0x0,0x3f900000,0x0,
  0x0,0xc042,0x0,0x0,0x0,0x0,0x0,0x0,0x0,0x0,0x0,0xc042,0x0,0x0,0x0,0x0,
  0x0,0x0,0x0,0x0,0x0,0x0,0x0,0x0,0x0,0x0,0x0,0x0,0x0,0x0,0x0,0x3f900000,
  0x0,0x0,0x0,0x0,0x0,0x0,0x0,0x0,0x0,0x0,0x0,0x0,0x0,0x0,0x0,0x3f900000,
  0x0,0x0,0x0,0x0,0x0,0x0,0x0,0x0,0x0,0x0,0x0,0x0,0x0,0x0,0x0,0x0,
  0x0,0x3f90,0x0,0x3f900000,0x0,0x3f90,0x0,0x0,0x0,0x0,0x0,0x3f90,0x0,0x0,0x0,0x3f90,
  0xc042,0x0,0x0,0x0,0xc0420000,0x0,0x0,0x0,0x0,0x0,0xc042,0x0,0x0,0x0,0xc0420000,0x0,
  0xc042,0x0,0x0,0x0,0xc0420000,0x0,0x0,0x0,0x0,0x0,0xc042,0x0,0x0,0x0,0xc0420000,0x0,
  0x0,0xc0420000,0x0,0x0,0x0,0x0,0x0,0x0,0x0,0x0,0x0,0xc0420000,0x0,0x0,0x0,0x0,
  0x0,0x0,0x0,0x0,0x0,0xc042,0x0,0x0,0x0,0x0,0x0,0x0,0x0,0x0,0x0,0xc042,
  0x3f903f90,0x0,0x0,0x0,0x3f900000,0x0,0x3f90,0x0,0x0,0x0,0x3f903f90,0x0,0x0,0x0,0x3f900000,0x0,
  0x3f903f90,0x0,0x0,0x0,0x3f900000,0x0,0x3f90,0x0,0x0,0x0,0x3f903f90,0x0,0x0,0x0,0x3f900000,0x0,
  0x0,0x0,0x0,0x0,0xc042,0x0,0x0,0x0,0x0,0x0,0x0,0x0,0x0,0x0,0x3f90c042,0x0,
  0x0,0x0,0x0,0x0,0x0,0x3f90,0x0,0x0,0x0,0x3f90,0x0,0x0,0x0,0x0,0x0,0x3f90,
  0x0,0x0,0x0,0x0,0x0,0x0,0x0,0x0,0x0,0x0,0x0,0x0,0x0,0x0,0x0,0x0,
  0x0,0x0,0x0,0x0,0x0,0x0,0x0,0x0,0x0,0x0,0x0,0x0,0x0,0x0,0x0,0x0,
  0xcc000000,0x27201a42,0x19e00,0x40004,0xcc080040,0x27601a53,0x19f20,0x40400,0xcc100090,0x27b019e0,0x19e08,0x4000400,0xcc1800d0,0x27f019f1,0x19f28,0x4000000,
  0xcd200120,0x5201a46,0x19e40,0x60,0xcd280160,0x5601a57,0x19f60,0x0,0xcd3001b0,0x5b019e4,0x19e48,0x6000,0xcd3801f0,0x5f019f5,0x19f68,0x0,
  0xe4401000,0x29201a43,0x19e80,0x0,0xe4481040,0x29601a52,0x19fa0,0x0,0xe4501090,0x29b019e1,0x19e88,0x0,0xe45810d0,0x29f019f0,0x19fa8,0x40000000,
  0xeb601120,0x29201a47,0x19ec0,0x0,0xeb681160,0x29601a56,0x19fe0,0x0,0xeb7011b0,0x29b019e5,0x19ec8,0x0,0xeb7811f0,0x29f019f4,0x19fe8,0x40000000,
  0xe4800010,0x27301a40,0x19e10,0x0,0xe4880050,0x27701a51,0x19f30,0x0,0xe4900080,0x27a019e2,0x19e18,0x0,0xe49800c0,0x27e019f3,0x19f38,0x0,
  0xe5a00130,0x5301a44,0x19e50,0x40000040,0xe5a80170,0x5701a55,0x19f70,0x40,0xe5b001a0,0x5a019e6,0x19e58,0x4000,0xe5b801e0,0x5e019f7,0x19f78,0x4000,
  0xe4c01010,0x29301a41,0x19e90,0x6,0xe4c81050,0x29701a50,0x19fb0,0x60000,0xe4d01080,0x29a019e3,0x19e98,0x600,0xe4d810c0,0x29e019f2,0x19fb8,0x6000000,
  0xd9e01130,0x29301a45,0x19ed0,0x6,0xd9e81170,0x29701a54,0x19ff0,0x60000,0xd9f011a0,0x29a019e7,0x19ed8,0x600,0xd9f811e0,0x29e019f6,0x19ff8,0x6000000,
  0xe4000020,0x27001a4b,0x1a400,0x600000,0xe4080060,0x27401a5a,0x1a520,0x0,0xe41000b0,0x279019e9,0x1a408,0x60000000,0xe41800f0,0x27d019f8,0x1a528,0x0,
  0xe5200100,0x5001a4f,0x1a440,0x0,0xe5280140,0x5401a5e,0x1a560,0x60,0xe5300190,0x59019ed,0x1a448,0x0,0xe53801d0,0x5d019fc,0x1a568,0x6000,
  0xe5601020,0x29001a4a,0x1a480,0x40004,0xe5681060,0x29401a5b,0x1a5a0,0x40400,0xe57010b0,0x299019e8,0x1a488,0x4000400,0xe57810f0,0x29d019f9,0x1a5a8,0x4000000,
  0xcc801100,0x29001a4e,0x1a4c0,0x40004,0xcc881140,0x29401a5f,0x1a5e0,0x40400,0xcc901190,0x299019ec,0x1a4c8,0x4000400,0xcc9811d0,0x29d019fd,0x1a5e8,0x4000000,
  0xcda00030,0x27101a49,0x1a410,0x0,0xcda80070,0x27501a58,0x1a530,0x6,0xcdb000a0,0x278019eb,0x1a418,0x0,0xcdb800e0,0x27c019fa,0x1a538,0x4000600,
  0xeac00110,0x5101a4d,0x1a450,0x0,0xeac80150,0x5501a5c,0x1a570,0x40,0xead00180,0x58019ef,0x1a458,0x40,0xead801c0,0x5c019fe,0x1a578,0x4000,
  0xe5e01030,0x29101a48,0x1a490,0x0,0xe5e81070,0x29501a59,0x1a5b0,0x0,0xe5f010a0,0x298019ea,0x1a498,0x0,0xe5f810e0,0x29c019fb,0x1a5b8,0x0,
  0xe4001110,0x29101a4c,0x1a4d0,0x0,0xe4081150,0x29501a5d,0x1a5f0,0x0,0xe4101180,0x298019ee,0x1a4d8,0x0,0xe41811c0,0x29c019ff,0x1a5f8,0x0,
  0x0,0x0,0x800,0x10000000,0x10,0x90,0x890,0x10000000,0x20,0x120,0x920,0x10000000,0x30,0x1b0,0x9b0,0x10000000,
  0x40,0x240,0xa40,0x20003180,0x50,0x2d0,0xad0,0x20003190,0x60,0x360,0xb60,0x200031a0,0x70,0x3f0,0xbf0,0x200031b0,
  0x610,0xc00,0x1400,0x40000000,0x600,0xc90,0x1490,0x40000010,0x630,0xd20,0x1520,0x40000020,0x620,0xdb0,0x15b0,0x40000030,
  0x650,0xe40,0x1640,0x20033180,0x640,0xed0,0x16d0,0x20033190,0x670,0xf60,0x1760,0x200331a0,0x660,0xff0,0x17f0,0x200331b0,
  0xc20,0x10,0x808,0x40030020,0xc30,0x80,0x898,0x40030030,0xc00,0x130,0x928,0x40030000,0xc10,0x1a0,0x9b8,0x40030010,
  0xc60,0x250,0xa48,0x200631a0,0xc70,0x2c0,0xad8,0x200631b0,0xc40,0x370,0xb68,0x20063180,0xc50,0x3e0,0xbf8,0x20063190,
  0x1230,0xc10,0x1408,0x40060020,0x1220,0xc80,0x1498,0x40060030,0x1210,0xd30,0x1528,0x40060000,0x1200,0xda0,0x15b8,0x40060010,
  0x1270,0xe50,0x1648,0x10000000,0x1260,0xec0,0x16d8,0x10000000,0x1250,0xf70,0x1768,0x10000000,0x1240,0xfe0,0x17f8,0x10000000,
  0x1840,0x20,0x810,0x10000000,0x1850,0xb0,0x880,0x10000000,0x1860,0x100,0x930,0x10000000,0x1870,0x190,0x9a0,0x10000000,
  0x1800,0x260,0xa50,0x200031c0,0x1810,0x2f0,0xac0,0x200031d0,0x1820,0x340,0xb70,0x200031e0,0x1830,0x3d0,0xbe0,0x200031f0,
  0x1e50,0xc20,0x1410,0x40000040,0x1e40,0xcb0,0x1480,0x40000050,0x1e70,0xd00,0x1530,0x40000060,0x1e60,0xd90,0x15a0,0x40000070,
  0x1e10,0xe60,0x1650,0x200331c0,0x1e00,0xef0,0x16c0,0x200331d0,0x1e30,0xf40,0x1770,0x200331e0,0x1e20,0xfd0,0x17e0,0x200331f0,
  0x2460,0x30,0x818,0x40030060,0x2470,0xa0,0x888,0x40030070,0x2440,0x110,0x938,0x40030040,0x2450,0x180,0x9a8,0x40030050,
  0x2420,0x270,0xa58,0x200631e0,0x2430,0x2e0,0xac8,0x200631f0,0x2400,0x350,0xb78,0x200631c0,0x2410,0x3c0,0xbe8,0x200631d0,
  0x2a70,0xc30,0x1418,0x40060060,0x2a60,0xca0,0x1488,0x40060070,0x2a50,0xd10,0x1538,0x40060040,0x2a40,0xd80,0x15a8,0x40060050,
  0x2a30,0xe70,0x1658,0x10000000,0x2a20,0xee0,0x16c8,0x10000000,0x2a10,0xf50,0x1778,0x10000000,0x2a00,0xfc0,0x17e8,0x10000000,
  0xbfd8bf90,0x3f403f00,0xa800,0xa800,0xbfd83f90,0x3f40bf00,0xa840,0xa840,0x3f003f40,0xbf90bfd8,0xa890,0xa890,0xbf003f40,0x3f90bfd8,0xa8d0,0xa8d0,
  0xbfd8bf90,0x3f403f00,0xb420,0xb420,0xbfd83f90,0x3f40bf00,0xb460,0xb460,0x3f003f40,0xbf90bfd8,0xb4b0,0xb4b0,0xbf003f40,0x3f90bfd8,0xb4f0,0xb4f0,
  0xbfd8bf90,0x3f403f00,0x17400,0x17a00,0xbfd83f90,0x3f40bf00,0x17440,0x17a40,0x3f003f40,0xbf90bfd8,0x17490,0x17a90,0xbf003f40,0x3f90bfd8,0x174d0,0x17ad0,
  0xbfd8bf90,0x3f403f00,0x18020,0x18620,0xbfd83f90,0x3f40bf00,0x18060,0x18660,0x3f003f40,0xbf90bfd8,0x180b0,0x186b0,0xbf003f40,0x3f90bfd8,0x180f0,0x186f0,
  0xbfd8bf90,0x3f403f00,0xa810,0xa810,0xbfd83f90,0x3f40bf00,0xa850,0xa850,0x3f003f40,0xbf90bfd8,0xa880,0xa880,0xbf003f40,0x3f90bfd8,0xa8c0,0xa8c0,
  0xbfd8bf90,0x3f403f00,0xb430,0xb430,0xbfd83f90,0x3f40bf00,0xb470,0xb470,0x3f003f40,0xbf90bfd8,0xb4a0,0xb4a0,0xbf003f40,0x3f90bfd8,0xb4e0,0xb4e0,
  0xbfd8bf90,0x3f403f00,0x17410,0x17a10,0xbfd83f90,0x3f40bf00,0x17450,0x17a50,0x3f003f40,0xbf90bfd8,0x17480,0x17a80,0xbf003f40,0x3f90bfd8,0x174c0,0x17ac0,
  0xbfd8bf90,0x3f403f00,0x18030,0x18630,0xbfd83f90,0x3f40bf00,0x18070,0x18670,0x3f003f40,0xbf90bfd8,0x180a0,0x186a0,0xbf003f40,0x3f90bfd8,0x180e0,0x186e0,
  0xbfd8bf90,0x3f403f00,0xa820,0xa820,0xbfd83f90,0x3f40bf00,0xa860,0xa860,0x3f003f40,0xbf90bfd8,0xa8b0,0xa8b0,0xbf003f40,0x3f90bfd8,0xa8f0,0xa8f0,
  0xbfd8bf90,0x3f403f00,0xb400,0xb400,0xbfd83f90,0x3f40bf00,0xb440,0xb440,0x3f003f40,0xbf90bfd8,0xb490,0xb490,0xbf003f40,0x3f90bfd8,0xb4d0,0xb4d0,
  0xbfd8bf90,0x3f403f00,0x17420,0x17a20,0xbfd83f90,0x3f40bf00,0x17460,0x17a60,0x3f003f40,0xbf90bfd8,0x174b0,0x17ab0,0xbf003f40,0x3f90bfd8,0x174f0,0x17af0,
  0xbfd8bf90,0x3f403f00,0x18000,0x18600,0xbfd83f90,0x3f40bf00,0x18040,0x18640,0x3f003f40,0xbf90bfd8,0x18090,0x18690,0xbf003f40,0x3f90bfd8,0x180d0,0x186d0,
  0xbfd8bf90,0x3f403f00,0xa830,0xa830,0xbfd83f90,0x3f40bf00,0xa870,0xa870,0x3f003f40,0xbf90bfd8,0xa8a0,0xa8a0,0xbf003f40,0x3f90bfd8,0xa8e0,0xa8e0,
  0xbfd8bf90,0x3f403f00,0xb410,0xb410,0xbfd83f90,0x3f40bf00,0xb450,0xb450,0x3f003f40,0xbf90bfd8,0xb480,0xb480,0xbf003f40,0x3f90bfd8,0xb4c0,0xb4c0,
  0xbfd8bf90,0x3f403f00,0x17430,0x17a30,0xbfd83f90,0x3f40bf00,0x17470,0x17a70,0x3f003f40,0xbf90bfd8,0x174a0,0x17aa0,0xbf003f40,0x3f90bfd8,0x174e0,0x17ae0,
  0xbfd8bf90,0x3f403f00,0x18010,0x18610,0xbfd83f90,0x3f40bf00,0x18050,0x18650,0x3f003f40,0xbf90bfd8,0x18080,0x18680,0xbf003f40,0x3f90bfd8,0x180c0,0x186c0,
  0xa800,0xa820,0x0,0xa800,0xa808,0xa828,0x40,0xa840,0xa810,0xa830,0x90,0xa890,0xa818,0xa838,0xd0,0xa8d0,
  0xa920,0xa900,0x2400,0xa900,0xa928,0xa908,0x2440,0xa940,0xa930,0xa910,0x2490,0xa990,0xa938,0xa918,0x24d0,0xa9d0,
  0xa840,0xa860,0x200,0xc000,0xa848,0xa868,0x240,0xc040,0xa850,0xa870,0x290,0xc090,0xa858,0xa878,0x2d0,0xc0d0,
  0xa960,0xa940,0x200,0xc000,0xa968,0xa948,0x240,0xc040,0xa970,0xa950,0x290,0xc090,0xa978,0xa958,0x2d0,0xc0d0,
  0xea80,0xeaa0,0x10,0xa810,0xea88,0xeaa8,0x50,0xa850,0xea90,0xeab0,0x80,0xa880,0xea98,0xeab8,0xc0,0xa8c0,
  0xa9a0,0xa980,0x2410,0xa910,0xa9a8,0xa988,0x2450,0xa950,0xa9b0,0xa990,0x2480,0xa980,0xa9b8,0xa998,0x24c0,0xa9c0,
  0xc0c0,0xc0e0,0x210,0xc010,0xc0c8,0xc0e8,0x250,0xc050,0xc0d0,0xc0f0,0x280,0xc080,0xc0d8,0xc0f8,0x2c0,0xc0c0,
  0xc1e0,0xc1c0,0x210,0xc010,0xc1e8,0xc1c8,0x250,0xc050,0xc1f0,0xc1d0,0x280,0xc080,0xc1f8,0xc1d8,0x2c0,0xc0c0,
  0xae00,0xae20,0x20,0xa820,0xae08,0xae28,0x60,0xa860,0xae10,0xae30,0xb0,0xa8b0,0xae18,0xae38,0xf0,0xa8f0,
  0xeb20,0xeb00,0x2420,0xa920,0xeb28,0xeb08,0x2460,0xa960,0xeb30,0xeb10,0x24b0,0xa9b0,0xeb38,0xeb18,0x24f0,0xa9f0,
  0xc040,0xc060,0x220,0xc020,0xc048,0xc068,0x260,0xc060,0xc050,0xc070,0x2b0,0xc0b0,0xc058,0xc078,0x2f0,0xc0f0,
  0xc160,0xc140,0x220,0xc020,0xc168,0xc148,0x260,0xc060,0xc170,0xc150,0x2b0,0xc0b0,0xc178,0xc158,0x2f0,0xc0f0,
  0xa880,0xa8a0,0x30,0xa830,0xa888,0xa8a8,0x70,0xa870,0xa890,0xa8b0,0xa0,0xa8a0,0xa898,0xa8b8,0xe0,0xa8e0,
  0xa8c0,0xa8e0,0x2430,0xa930,0xa8c8,0xa8e8,0x2470,0xa970,0xa8d0,0xa8f0,0x24a0,0xa9a0,0xa8d8,0xa8f8,0x24e0,0xa9e0,
  0xa9e0,0xa9c0,0x230,0xc030,0xa9e8,0xa9c8,0x270,0xc070,0xa9f0,0xa9d0,0x2a0,0xc0a0,0xa9f8,0xa9d8,0x2e0,0xc0e0,
  0xae00,0xae20,0x230,0xc030,0xae08,0xae28,0x270,0xc070,0xae10,0xae30,0x2a0,0xc0a0,0xae18,0xae38,0x2e0,0xc0e0,
  0x180000,0x40,0x820,0x10000000,0x180010,0xd0,0x8b0,0x10000000,0x180020,0x160,0x900,0x10000000,0x180030,0x1f0,0x990,0x10000000,
  0x180040,0x200,0xa60,0x20093180,0x180050,0x290,0xaf0,0x20093190,0x180060,0x320,0xb40,0x200931a0,0x180070,0x3b0,0xbd0,0x200931b0,
  0x180610,0xc40,0x1420,0x40090000,0x180600,0xcd0,0x14b0,0x40090010,0x180630,0xd60,0x1500,0x40090020,0x180620,0xdf0,0x1590,0x40090030,
  0x180650,0xe00,0x1660,0x200c3180,0x180640,0xe90,0x16f0,0x200c3190,0x180670,0xf20,0x1740,0x200c31a0,0x180660,0xfb0,0x17d0,0x200c31b0,
  0x180c20,0x50,0x828,0x400c0020,0x180c30,0xc0,0x8b8,0x400c0030,0x180c00,0x170,0x908,0x400c0000,0x180c10,0x1e0,0x998,0x400c0010,
  0x180c60,0x210,0xa68,0x200f31a0,0x180c70,0x280,0xaf8,0x200f31b0,0x180c40,0x330,0xb48,0x200f3180,0x180c50,0x3a0,0xbd8,0x200f3190,
  0x181230,0xc50,0x1428,0x400f0020,0x181220,0xcc0,0x14b8,0x400f0030,0x181210,0xd70,0x1508,0x400f0000,0x181200,0xde0,0x1598,0x400f0010,
  0x181270,0xe10,0x1668,0x10000000,0x181260,0xe80,0x16f8,0x10000000,0x181250,0xf30,0x1748,0x10000000,0x181240,0xfa0,0x17d8,0x10000000,
  0x181840,0x60,0x830,0x10000000,0x181850,0xf0,0x8a0,0x10000000,0x181860,0x140,0x910,0x10000000,0x181870,0x1d0,0x980,0x10000000,
  0x181800,0x220,0xa70,0x200931c0,0x181810,0x2b0,0xae0,0x200931d0,0x181820,0x300,0xb50,0x200931e0,0x181830,0x390,0xbc0,0x200931f0,
  0x181e50,0xc60,0x1430,0x40090040,0x181e40,0xcf0,0x14a0,0x40090050,0x181e70,0xd40,0x1510,0x40090060,0x181e60,0xdd0,0x1580,0x40090070,
  0x181e10,0xe20,0x1670,0x200c31c0,0x181e00,0xeb0,0x16e0,0x200c31d0,0x181e30,0xf00,0x1750,0x200c31e0,0x181e20,0xf90,0x17c0,0x200c31f0,
  0x182460,0x70,0x838,0x400c0060,0x182470,0xe0,0x8a8,0x400c0070,0x182440,0x150,0x918,0x400c0040,0x182450,0x1c0,0x988,0x400c0050,
  0x182420,0x230,0xa78,0x200f31e0,0x182430,0x2a0,0xae8,0x200f31f0,0x182400,0x310,0xb58,0x200f31c0,0x182410,0x380,0xbc8,0x200f31d0,
  0x182a70,0xc70,0x1438,0x400f0060,0x182a60,0xce0,0x14a8,0x400f0070,0x182a50,0xd50,0x1518,0x400f0040,0x182a40,0xdc0,0x1588,0x400f0050,
  0x182a30,0xe30,0x1678,0x10000000,0x182a20,0xea0,0x16e8,0x10000000,0x182a10,0xf10,0x1758,0x10000000,0x182a00,0xf80,0x17c8,0x10000000,
  0xbfd8bf90,0x3f403f00,0xa800,0xa800,0xbfd83f90,0x3f40bf00,0xa840,0xa840,0x3f003f40,0xbf90bfd8,0xa890,0xa890,0xbf003f40,0x3f90bfd8,0xa8d0,0xa8d0,
  0xbfd8bf90,0x3f403f00,0xb420,0xb420,0xbfd83f90,0x3f40bf00,0xb460,0xb460,0x3f003f40,0xbf90bfd8,0xb4b0,0xb4b0,0xbf003f40,0x3f90bfd8,0xb4f0,0xb4f0,
  0xbfd8bf90,0x3f403f00,0x17400,0x17a00,0xbfd83f90,0x3f40bf00,0x17440,0x17a40,0x3f003f40,0xbf90bfd8,0x17490,0x17a90,0xbf003f40,0x3f90bfd8,0x174d0,0x17ad0,
  0xbfd8bf90,0x3f403f00,0x18020,0x18620,0xbfd83f90,0x3f40bf00,0x18060,0x18660,0x3f003f40,0xbf90bfd8,0x180b0,0x186b0,0xbf003f40,0x3f90bfd8,0x180f0,0x186f0,
  0xbfd8bf90,0x3f403f00,0xa810,0xa810,0xbfd83f90,0x3f40bf00,0xa850,0xa850,0x3f003f40,0xbf90bfd8,0xa880,0xa880,0xbf003f40,0x3f90bfd8,0xa8c0,0xa8c0,
  0xbfd8bf90,0x3f403f00,0xb430,0xb430,0xbfd83f90,0x3f40bf00,0xb470,0xb470,0x3f003f40,0xbf90bfd8,0xb4a0,0xb4a0,0xbf003f40,0x3f90bfd8,0xb4e0,0xb4e0,
  0xbfd8bf90,0x3f403f00,0x17410,0x17a10,0xbfd83f90,0x3f40bf00,0x17450,0x17a50,0x3f003f40,0xbf90bfd8,0x17480,0x17a80,0xbf003f40,0x3f90bfd8,0x174c0,0x17ac0,
  0xbfd8bf90,0x3f403f00,0x18030,0x18630,0xbfd83f90,0x3f40bf00,0x18070,0x18670,0x3f003f40,0xbf90bfd8,0x180a0,0x186a0,0xbf003f40,0x3f90bfd8,0x180e0,0x186e0,
  0xbfd8bf90,0x3f403f00,0xa820,0xa820,0xbfd83f90,0x3f40bf00,0xa860,0xa860,0x3f003f40,0xbf90bfd8,0xa8b0,0xa8b0,0xbf003f40,0x3f90bfd8,0xa8f0,0xa8f0,
  0xbfd8bf90,0x3f403f00,0xb400,0xb400,0xbfd83f90,0x3f40bf00,0xb440,0xb440,0x3f003f40,0xbf90bfd8,0xb490,0xb490,0xbf003f40,0x3f90bfd8,0xb4d0,0xb4d0,
  0xbfd8bf90,0x3f403f00,0x17420,0x17a20,0xbfd83f90,0x3f40bf00,0x17460,0x17a60,0x3f003f40,0xbf90bfd8,0x174b0,0x17ab0,0xbf003f40,0x3f90bfd8,0x174f0,0x17af0,
  0xbfd8bf90,0x3f403f00,0x18000,0x18600,0xbfd83f90,0x3f40bf00,0x18040,0x18640,0x3f003f40,0xbf90bfd8,0x18090,0x18690,0xbf003f40,0x3f90bfd8,0x180d0,0x186d0,
  0xbfd8bf90,0x3f403f00,0xa830,0xa830,0xbfd83f90,0x3f40bf00,0xa870,0xa870,0x3f003f40,0xbf90bfd8,0xa8a0,0xa8a0,0xbf003f40,0x3f90bfd8,0xa8e0,0xa8e0,
  0xbfd8bf90,0x3f403f00,0xb410,0xb410,0xbfd83f90,0x3f40bf00,0xb450,0xb450,0x3f003f40,0xbf90bfd8,0xb480,0xb480,0xbf003f40,0x3f90bfd8,0xb4c0,0xb4c0,
  0xbfd8bf90,0x3f403f00,0x17430,0x17a30,0xbfd83f90,0x3f40bf00,0x17470,0x17a70,0x3f003f40,0xbf90bfd8,0x174a0,0x17aa0,0xbf003f40,0x3f90bfd8,0x174e0,0x17ae0,
  0xbfd8bf90,0x3f403f00,0x18010,0x18610,0xbfd83f90,0x3f40bf00,0x18050,0x18650,0x3f003f40,0xbf90bfd8,0x18080,0x18680,0xbf003f40,0x3f90bfd8,0x180c0,0x186c0,
  0xb400,0xb420,0x120,0xb420,0xb408,0xb428,0x160,0xb460,0xb410,0xb430,0x1b0,0xb4b0,0xb418,0xb438,0x1f0,0xb4f0,
  0xb440,0xb460,0x2520,0xb520,0xb448,0xb468,0x2560,0xb560,0xb450,0xb470,0x25b0,0xb5b0,0xb458,0xb478,0x25f0,0xb5f0,
  0xcc40,0xcc60,0x320,0xcc20,0xcc48,0xcc68,0x360,0xcc60,0xcc50,0xcc70,0x3b0,0xccb0,0xcc58,0xcc78,0x3f0,0xccf0,
  0xcd60,0xcd40,0x320,0xcc20,0xcd68,0xcd48,0x360,0xcc60,0xcd70,0xcd50,0x3b0,0xccb0,0xcd78,0xcd58,0x3f0,0xccf0,
  0xb5a0,0xb580,0x130,0xb430,0xb5a8,0xb588,0x170,0xb470,0xb5b0,0xb590,0x1a0,0xb4a0,0xb5b8,0xb598,0x1e0,0xb4e0,
  0xccc0,0xcce0,0x2530,0xb530,0xccc8,0xcce8,0x2570,0xb570,0xccd0,0xccf0,0x25a0,0xb5a0,0xccd8,0xccf8,0x25e0,0xb5e0,
  0xa9e0,0xa9c0,0x330,0xcc30,0xa9e8,0xa9c8,0x370,0xcc70,0xa9f0,0xa9d0,0x3a0,0xcca0,0xa9f8,0xa9d8,0x3e0,0xcce0,
  0xc1e0,0xc1c0,0x330,0xcc30,0xc1e8,0xc1c8,0x370,0xcc70,0xc1f0,0xc1d0,0x3a0,0xcca0,0xc1f8,0xc1d8,0x3e0,0xcce0,
  0xb520,0xb500,0x100,0xb400,0xb528,0xb508,0x140,0xb440,0xb530,0xb510,0x190,0xb490,0xb538,0xb518,0x1d0,0xb4d0,
  0xea40,0xea60,0x2500,0xb500,0xea48,0xea68,0x2540,0xb540,0xea50,0xea70,0x2590,0xb590,0xea58,0xea78,0x25d0,0xb5d0,
  0xb560,0xb540,0x300,0xcc00,0xb568,0xb548,0x340,0xcc40,0xb570,0xb550,0x390,0xcc90,0xb578,0xb558,0x3d0,0xccd0,
  0xb480,0xb4a0,0x300,0xcc00,0xb488,0xb4a8,0x340,0xcc40,0xb490,0xb4b0,0x390,0xcc90,0xb498,0xb4b8,0x3d0,0xccd0,
  0xb4c0,0xb4e0,0x110,0xb410,0xb4c8,0xb4e8,0x150,0xb450,0xb4d0,0xb4f0,0x180,0xb480,0xb4d8,0xb4f8,0x1c0,0xb4c0,
  0xb5e0,0xb5c0,0x2510,0xb510,0xb5e8,0xb5c8,0x2550,0xb550,0xb5f0,0xb5d0,0x2580,0xb580,0xb5f8,0xb5d8,0x25c0,0xb5c0,
  0xcde0,0xcdc0,0x310,0xcc10,0xcde8,0xcdc8,0x350,0xcc50,0xcdf0,0xcdd0,0x380,0xcc80,0xcdf8,0xcdd8,0x3c0,0xccc0,
  0xb520,0xb500,0x310,0xcc10,0xb528,0xb508,0x350,0xcc50,0xb530,0xb510,0x380,0xcc80,0xb538,0xb518,0x3c0,0xccc0,
  0x0,0x400,0x840,0x10000000,0x10,0x490,0x8d0,0x10000000,0x20,0x520,0x960,0x10000000,0x30,0x5b0,0x9f0,0x10000000,
  0x40,0x640,0xa00,0x20123180,0x50,0x6d0,0xa90,0x20123190,0x60,0x760,0xb20,0x201231a0,0x70,0x7f0,0xbb0,0x201231b0,
  0x610,0x1000,0x1440,0x40120000,0x600,0x1090,0x14d0,0x40120010,0x630,0x1120,0x1560,0x40120020,0x620,0x11b0,0x15f0,0x40120030,
  0x650,0x1240,0x1600,0x20153180,0x640,0x12d0,0x1690,0x20153190,0x670,0x1360,0x1720,0x201531a0,0x660,0x13f0,0x17b0,0x201531b0,
  0xc20,0x410,0x848,0x40150020,0xc30,0x480,0x8d8,0x40150030,0xc00,0x530,0x968,0x40150000,0xc10,0x5a0,0x9f8,0x40150010,
  0xc60,0x650,0xa08,0x201831a0,0xc70,0x6c0,0xa98,0x201831b0,0xc40,0x770,0xb28,0x20183180,0xc50,0x7e0,0xbb8,0x20183190,
  0x1230,0x1010,0x1448,0x40180020,0x1220,0x1080,0x14d8,0x40180030,0x1210,0x1130,0x1568,0x40180000,0x1200,0x11a0,0x15f8,0x40180010,
  0x1270,0x1250,0x1608,0x10000000,0x1260,0x12c0,0x1698,0x10000000,0x1250,0x1370,0x1728,0x10000000,0x1240,0x13e0,0x17b8,0x10000000,
  0x1840,0x420,0x850,0x10000000,0x1850,0x4b0,0x8c0,0x10000000,0x1860,0x500,0x970,0x10000000,0x1870,0x590,0x9e0,0x10000000,
  0x1800,0x660,0xa10,0x201231c0,0x1810,0x6f0,0xa80,0x201231d0,0x1820,0x740,0xb30,0x201231e0,0x1830,0x7d0,0xba0,0x201231f0,
  0x1e50,0x1020,0x1450,0x40120040,0x1e40,0x10b0,0x14c0,0x40120050,0x1e70,0x1100,0x1570,0x40120060,0x1e60,0x1190,0x15e0,0x40120070,
  0x1e10,0x1260,0x1610,0x201531c0,0x1e00,0x12f0,0x1680,0x201531d0,0x1e30,0x1340,0x1730,0x201531e0,0x1e20,0x13d0,0x17a0,0x201531f0,
  0x2460,0x430,0x858,0x40150060,0x2470,0x4a0,0x8c8,0x40150070,0x2440,0x510,0x978,0x40150040,0x2450,0x580,0x9e8,0x40150050,
  0x2420,0x670,0xa18,0x201831e0,0x2430,0x6e0,0xa88,0x201831f0,0x2400,0x750,0xb38,0x201831c0,0x2410,0x7c0,0xba8,0x201831d0,
  0x2a70,0x1030,0x1458,0x40180060,0x2a60,0x10a0,0x14c8,0x40180070,0x2a50,0x1110,0x1578,0x40180040,0x2a40,0x1180,0x15e8,0x40180050,
  0x2a30,0x1270,0x1618,0x10000000,0x2a20,0x12e0,0x1688,0x10000000,0x2a10,0x1350,0x1738,0x10000000,0x2a00,0x13c0,0x17a8,0x10000000,
  0xbfd8bf90,0x3f403f00,0xa800,0xa800,0xbfd83f90,0x3f40bf00,0xa840,0xa840,0x3f003f40,0xbf90bfd8,0xa890,0xa890,0xbf003f40,0x3f90bfd8,0xa8d0,0xa8d0,
  0xbfd8bf90,0x3f403f00,0xb420,0xb420,0xbfd83f90,0x3f40bf00,0xb460,0xb460,0x3f003f40,0xbf90bfd8,0xb4b0,0xb4b0,0xbf003f40,0x3f90bfd8,0xb4f0,0xb4f0,
  0xbfd8bf90,0x3f403f00,0x17400,0x17a00,0xbfd83f90,0x3f40bf00,0x17440,0x17a40,0x3f003f40,0xbf90bfd8,0x17490,0x17a90,0xbf003f40,0x3f90bfd8,0x174d0,0x17ad0,
  0xbfd8bf90,0x3f403f00,0x18020,0x18620,0xbfd83f90,0x3f40bf00,0x18060,0x18660,0x3f003f40,0xbf90bfd8,0x180b0,0x186b0,0xbf003f40,0x3f90bfd8,0x180f0,0x186f0,
  0xbfd8bf90,0x3f403f00,0xa810,0xa810,0xbfd83f90,0x3f40bf00,0xa850,0xa850,0x3f003f40,0xbf90bfd8,0xa880,0xa880,0xbf003f40,0x3f90bfd8,0xa8c0,0xa8c0,
  0xbfd8bf90,0x3f403f00,0xb430,0xb430,0xbfd83f90,0x3f40bf00,0xb470,0xb470,0x3f003f40,0xbf90bfd8,0xb4a0,0xb4a0,0xbf003f40,0x3f90bfd8,0xb4e0,0xb4e0,
  0xbfd8bf90,0x3f403f00,0x17410,0x17a10,0xbfd83f90,0x3f40bf00,0x17450,0x17a50,0x3f003f40,0xbf90bfd8,0x17480,0x17a80,0xbf003f40,0x3f90bfd8,0x174c0,0x17ac0,
  0xbfd8bf90,0x3f403f00,0x18030,0x18630,0xbfd83f90,0x3f40bf00,0x18070,0x18670,0x3f003f40,0xbf90bfd8,0x180a0,0x186a0,0xbf003f40,0x3f90bfd8,0x180e0,0x186e0,
  0xbfd8bf90,0x3f403f00,0xa820,0xa820,0xbfd83f90,0x3f40bf00,0xa860,0xa860,0x3f003f40,0xbf90bfd8,0xa8b0,0xa8b0,0xbf003f40,0x3f90bfd8,0xa8f0,0xa8f0,
  0xbfd8bf90,0x3f403f00,0xb400,0xb400,0xbfd83f90,0x3f40bf00,0xb440,0xb440,0x3f003f40,0xbf90bfd8,0xb490,0xb490,0xbf003f40,0x3f90bfd8,0xb4d0,0xb4d0,
  0xbfd8bf90,0x3f403f00,0x17420,0x17a20,0xbfd83f90,0x3f40bf00,0x17460,0x17a60,0x3f003f40,0xbf90bfd8,0x174b0,0x17ab0,0xbf003f40,0x3f90bfd8,0x174f0,0x17af0,
  0xbfd8bf90,0x3f403f00,0x18000,0x18600,0xbfd83f90,0x3f40bf00,0x18040,0x18640,0x3f003f40,0xbf90bfd8,0x18090,0x18690,0xbf003f40,0x3f90bfd8,0x180d0,0x186d0,
  0xbfd8bf90,0x3f403f00,0xa830,0xa830,0xbfd83f90,0x3f40bf00,0xa870,0xa870,0x3f003f40,0xbf90bfd8,0xa8a0,0xa8a0,0xbf003f40,0x3f90bfd8,0xa8e0,0xa8e0,
  0xbfd8bf90,0x3f403f00,0xb410,0xb410,0xbfd83f90,0x3f40bf00,0xb450,0xb450,0x3f003f40,0xbf90bfd8,0xb480,0xb480,0xbf003f40,0x3f90bfd8,0xb4c0,0xb4c0,
  0xbfd8bf90,0x3f403f00,0x17430,0x17a30,0xbfd83f90,0x3f40bf00,0x17470,0x17a70,0x3f003f40,0xbf90bfd8,0x174a0,0x17aa0,0xbf003f40,0x3f90bfd8,0x174e0,0x17ae0,
  0xbfd8bf90,0x3f403f00,0x18010,0x18610,0xbfd83f90,0x3f40bf00,0x18050,0x18650,0x3f003f40,0xbf90bfd8,0x18080,0x18680,0xbf003f40,0x3f90bfd8,0x180c0,0x186c0,
  0xc000,0xc020,0x2600,0xc100,0xc008,0xc028,0x2640,0xc140,0xc010,0xc030,0x2690,0xc190,0xc018,0xc038,0x26d0,0xc1d0,
  0xd800,0xd820,0x400,0xd800,0xd808,0xd828,0x440,0xd840,0xd810,0xd830,0x490,0xd890,0xd818,0xd838,0x4d0,0xd8d0,
  0xc120,0xc100,0x2800,0xd900,0xc128,0xc108,0x2840,0xd940,0xc130,0xc110,0x2890,0xd990,0xc138,0xc118,0x28d0,0xd9d0,
  0xd840,0xd860,0x2800,0xd900,0xd848,0xd868,0x2840,0xd940,0xd850,0xd870,0x2890,0xd990,0xd858,0xd878,0x28d0,0xd9d0,
  0xc080,0xc0a0,0x2610,0xc110,0xc088,0xc0a8,0x2650,0xc150,0xc090,0xc0b0,0x2680,0xc180,0xc098,0xc0b8,0x26c0,0xc1c0,
  0xc1a0,0xc180,0x410,0xd810,0xc1a8,0xc188,0x450,0xd850,0xc1b0,0xc190,0x480,0xd880,0xc1b8,0xc198,0x4c0,0xd8c0,
  0xd9a0,0xd980,0x2810,0xd910,0xd9a8,0xd988,0x2850,0xd950,0xd9b0,0xd990,0x2880,0xd980,0xd9b8,0xd998,0x28c0,0xd9c0,
  0xd9e0,0xd9c0,0x2810,0xd910,0xd9e8,0xd9c8,0x2850,0xd950,0xd9f0,0xd9d0,0x2880,0xd980,0xd9f8,0xd9d8,0x28c0,0xd9c0,
  0xc600,0xc620,0x2620,0xc120,0xc608,0xc628,0x2660,0xc160,0xc610,0xc630,0x26b0,0xc1b0,0xc618,0xc638,0x26f0,0xc1f0,
  0xde00,0xde20,0x420,0xd820,0xde08,0xde28,0x460,0xd860,0xde10,0xde30,0x4b0,0xd8b0,0xde18,0xde38,0x4f0,0xd8f0,
  0xd920,0xd900,0x2820,0xd920,0xd928,0xd908,0x2860,0xd960,0xd930,0xd910,0x28b0,0xd9b0,0xd938,0xd918,0x28f0,0xd9f0,
  0xd960,0xd940,0x2820,0xd920,0xd968,0xd948,0x2860,0xd960,0xd970,0xd950,0x28b0,0xd9b0,0xd978,0xd958,0x28f0,0xd9f0,
  0xd880,0xd8a0,0x2630,0xc130,0xd888,0xd8a8,0x2670,0xc170,0xd890,0xd8b0,0x26a0,0xc1a0,0xd898,0xd8b8,0x26e0,0xc1e0,
  0xeba0,0xeb80,0x430,0xd830,0xeba8,0xeb88,0x470,0xd870,0xebb0,0xeb90,0x4a0,0xd8a0,0xebb8,0xeb98,0x4e0,0xd8e0,
  0xd8c0,0xd8e0,0x2830,0xd930,0xd8c8,0xd8e8,0x2870,0xd970,0xd8d0,0xd8f0,0x28a0,0xd9a0,0xd8d8,0xd8f8,0x28e0,0xd9e0,
  0xc600,0xc620,0x2830,0xd930,0xc608,0xc628,0x2870,0xd970,0xc610,0xc630,0x28a0,0xd9a0,0xc618,0xc638,0x28e0,0xd9e0,
  0x180000,0x440,0x860,0x10000000,0x180010,0x4d0,0x8f0,0x10000000,0x180020,0x560,0x940,0x10000000,0x180030,0x5f0,0x9d0,0x10000000,
  0x180040,0x600,0xa20,0x201b3180,0x180050,0x690,0xab0,0x201b3190,0x180060,0x720,0xb00,0x201b31a0,0x180070,0x7b0,0xb90,0x201b31b0,
  0x180610,0x1040,0x1460,0x401b0000,0x180600,0x10d0,0x14f0,0x401b0010,0x180630,0x1160,0x1540,0x401b0020,0x180620,0x11f0,0x15d0,0x401b0030,
  0x180650,0x1200,0x1620,0x201e3180,0x180640,0x1290,0x16b0,0x201e3190,0x180670,0x1320,0x1700,0x201e31a0,0x180660,0x13b0,0x1790,0x201e31b0,
  0x180c20,0x450,0x868,0x401e0020,0x180c30,0x4c0,0x8f8,0x401e0030,0x180c00,0x570,0x948,0x401e0000,0x180c10,0x5e0,0x9d8,0x401e0010,
  0x180c60,0x610,0xa28,0x202131a0,0x180c70,0x680,0xab8,0x202131b0,0x180c40,0x730,0xb08,0x20213180,0x180c50,0x7a0,0xb98,0x20213190,
  0x181230,0x1050,0x1468,0x40210020,0x181220,0x10c0,0x14f8,0x40210030,0x181210,0x1170,0x1548,0x40210000,0x181200,0x11e0,0x15d8,0x40210010,
  0x181270,0x1210,0x1628,0x10000000,0x181260,0x1280,0x16b8,0x10000000,0x181250,0x1330,0x1708,0x10000000,0x181240,0x13a0,0x1798,0x10000000,
  0x181840,0x460,0x870,0x10000000,0x181850,0x4f0,0x8e0,0x10000000,0x181860,0x540,0x950,0x10000000,0x181870,0x5d0,0x9c0,0x10000000,
  0x181800,0x620,0xa30,0x201b31c0,0x181810,0x6b0,0xaa0,0x201b31d0,0x181820,0x700,0xb10,0x201b31e0,0x181830,0x790,0xb80,0x201b31f0,
  0x181e50,0x1060,0x1470,0x401b0040,0x181e40,0x10f0,0x14e0,0x401b0050,0x181e70,0x1140,0x1550,0x401b0060,0x181e60,0x11d0,0x15c0,0x401b0070,
  0x181e10,0x1220,0x1630,0x201e31c0,0x181e00,0x12b0,0x16a0,0x201e31d0,0x181e30,0x1300,0x1710,0x201e31e0,0x181e20,0x1390,0x1780,0x201e31f0,
  0x182460,0x470,0x878,0x401e0060,0x182470,0x4e0,0x8e8,0x401e0070,0x182440,0x550,0x958,0x401e0040,0x182450,0x5c0,0x9c8,0x401e0050,
  0x182420,0x630,0xa38,0x202131e0,0x182430,0x6a0,0xaa8,0x202131f0,0x182400,0x710,0xb18,0x202131c0,0x182410,0x780,0xb88,0x202131d0,
  0x182a70,0x1070,0x1478,0x40210060,0x182a60,0x10e0,0x14e8,0x40210070,0x182a50,0x1150,0x1558,0x40210040,0x182a40,0x11c0,0x15c8,0x40210050,
  0x182a30,0x1230,0x1638,0x10000000,0x182a20,0x12a0,0x16a8,0x10000000,0x182a10,0x1310,0x1718,0x10000000,0x182a00,0x1380,0x1788,0x10000000,
  0xbfd8bf90,0x3f403f00,0xa800,0xa800,0xbfd83f90,0x3f40bf00,0xa840,0xa840,0x3f003f40,0xbf90bfd8,0xa890,0xa890,0xbf003f40,0x3f90bfd8,0xa8d0,0xa8d0,
  0xbfd8bf90,0x3f403f00,0xb420,0xb420,0xbfd83f90,0x3f40bf00,0xb460,0xb460,0x3f003f40,0xbf90bfd8,0xb4b0,0xb4b0,0xbf003f40,0x3f90bfd8,0xb4f0,0xb4f0,
  0xbfd8bf90,0x3f403f00,0x17400,0x17a00,0xbfd83f90,0x3f40bf00,0x17440,0x17a40,0x3f003f40,0xbf90bfd8,0x17490,0x17a90,0xbf003f40,0x3f90bfd8,0x174d0,0x17ad0,
  0xbfd8bf90,0x3f403f00,0x18020,0x18620,0xbfd83f90,0x3f40bf00,0x18060,0x18660,0x3f003f40,0xbf90bfd8,0x180b0,0x186b0,0xbf003f40,0x3f90bfd8,0x180f0,0x186f0,
  0xbfd8bf90,0x3f403f00,0xa810,0xa810,0xbfd83f90,0x3f40bf00,0xa850,0xa850,0x3f003f40,0xbf90bfd8,0xa880,0xa880,0xbf003f40,0x3f90bfd8,0xa8c0,0xa8c0,
  0xbfd8bf90,0x3f403f00,0xb430,0xb430,0xbfd83f90,0x3f40bf00,0xb470,0xb470,0x3f003f40,0xbf90bfd8,0xb4a0,0xb4a0,0xbf003f40,0x3f90bfd8,0xb4e0,0xb4e0,
  0xbfd8bf90,0x3f403f00,0x17410,0x17a10,0xbfd83f90,0x3f40bf00,0x17450,0x17a50,0x3f003f40,0xbf90bfd8,0x17480,0x17a80,0xbf003f40,0x3f90bfd8,0x174c0,0x17ac0,
  0xbfd8bf90,0x3f403f00,0x18030,0x18630,0xbfd83f90,0x3f40bf00,0x18070,0x18670,0x3f003f40,0xbf90bfd8,0x180a0,0x186a0,0xbf003f40,0x3f90bfd8,0x180e0,0x186e0,
  0xbfd8bf90,0x3f403f00,0xa820,0xa820,0xbfd83f90,0x3f40bf00,0xa860,0xa860,0x3f003f40,0xbf90bfd8,0xa8b0,0xa8b0,0xbf003f40,0x3f90bfd8,0xa8f0,0xa8f0,
  0xbfd8bf90,0x3f403f00,0xb400,0xb400,0xbfd83f90,0x3f40bf00,0xb440,0xb440,0x3f003f40,0xbf90bfd8,0xb490,0xb490,0xbf003f40,0x3f90bfd8,0xb4d0,0xb4d0,
  0xbfd8bf90,0x3f403f00,0x17420,0x17a20,0xbfd83f90,0x3f40bf00,0x17460,0x17a60,0x3f003f40,0xbf90bfd8,0x174b0,0x17ab0,0xbf003f40,0x3f90bfd8,0x174f0,0x17af0,
  0xbfd8bf90,0x3f403f00,0x18000,0x18600,0xbfd83f90,0x3f40bf00,0x18040,0x18640,0x3f003f40,0xbf90bfd8,0x18090,0x18690,0xbf003f40,0x3f90bfd8,0x180d0,0x186d0,
  0xbfd8bf90,0x3f403f00,0xa830,0xa830,0xbfd83f90,0x3f40bf00,0xa870,0xa870,0x3f003f40,0xbf90bfd8,0xa8a0,0xa8a0,0xbf003f40,0x3f90bfd8,0xa8e0,0xa8e0,
  0xbfd8bf90,0x3f403f00,0xb410,0xb410,0xbfd83f90,0x3f40bf00,0xb450,0xb450,0x3f003f40,0xbf90bfd8,0xb480,0xb480,0xbf003f40,0x3f90bfd8,0xb4c0,0xb4c0,
  0xbfd8bf90,0x3f403f00,0x17430,0x17a30,0xbfd83f90,0x3f40bf00,0x17470,0x17a70,0x3f003f40,0xbf90bfd8,0x174a0,0x17aa0,0xbf003f40,0x3f90bfd8,0x174e0,0x17ae0,
  0xbfd8bf90,0x3f403f00,0x18010,0x18610,0xbfd83f90,0x3f40bf00,0x18050,0x18650,0x3f003f40,0xbf90bfd8,0x18080,0x18680,0xbf003f40,0x3f90bfd8,0x180c0,0x186c0,
  0xcc00,0xcc20,0x2720,0xcd20,0xcc08,0xcc28,0x2760,0xcd60,0xcc10,0xcc30,0x27b0,0xcdb0,0xcc18,0xcc38,0x27f0,0xcdf0,
  0xcd20,0xcd00,0x520,0xe420,0xcd28,0xcd08,0x560,0xe460,0xcd30,0xcd10,0x5b0,0xe4b0,0xcd38,0xcd18,0x5f0,0xe4f0,
  0xe440,0xe460,0x2920,0xe520,0xe448,0xe468,0x2960,0xe560,0xe450,0xe470,0x29b0,0xe5b0,0xe458,0xe478,0x29f0,0xe5f0,
  0xeb60,0xeb40,0x2920,0xe520,0xeb68,0xeb48,0x2960,0xe560,0xeb70,0xeb50,0x29b0,0xe5b0,0xeb78,0xeb58,0x29f0,0xe5f0,
  0xe480,0xe4a0,0x2730,0xcd30,0xe488,0xe4a8,0x2770,0xcd70,0xe490,0xe4b0,0x27a0,0xcda0,0xe498,0xe4b8,0x27e0,0xcde0,
  0xe5a0,0xe580,0x530,0xe430,0xe5a8,0xe588,0x570,0xe470,0xe5b0,0xe590,0x5a0,0xe4a0,0xe5b8,0xe598,0x5e0,0xe4e0,
  0xe4c0,0xe4e0,0x2930,0xe530,0xe4c8,0xe4e8,0x2970,0xe570,0xe4d0,0xe4f0,0x29a0,0xe5a0,0xe4d8,0xe4f8,0x29e0,0xe5e0,
  0xd9e0,0xd9c0,0x2930,0xe530,0xd9e8,0xd9c8,0x2970,0xe570,0xd9f0,0xd9d0,0x29a0,0xe5a0,0xd9f8,0xd9d8,0x29e0,0xe5e0,
  0xe400,0xe420,0x2700,0xcd00,0xe408,0xe428,0x2740,0xcd40,0xe410,0xe430,0x2790,0xcd90,0xe418,0xe438,0x27d0,0xcdd0,
  0xe520,0xe500,0x500,0xe400,0xe528,0xe508,0x540,0xe440,0xe530,0xe510,0x590,0xe490,0xe538,0xe518,0x5d0,0xe4d0,
  0xe560,0xe540,0x2900,0xe500,0xe568,0xe548,0x2940,0xe540,0xe570,0xe550,0x2990,0xe590,0xe578,0xe558,0x29d0,0xe5d0,
  0xcc80,0xcca0,0x2900,0xe500,0xcc88,0xcca8,0x2940,0xe540,0xcc90,0xccb0,0x2990,0xe590,0xcc98,0xccb8,0x29d0,0xe5d0,
  0xcda0,0xcd80,0x2710,0xcd10,0xcda8,0xcd88,0x2750,0xcd50,0xcdb0,0xcd90,0x2780,0xcd80,0xcdb8,0xcd98,0x27c0,0xcdc0,
  0xeac0,0xeae0,0x510,0xe410,0xeac8,0xeae8,0x550,0xe450,0xead0,0xeaf0,0x580,0xe480,0xead8,0xeaf8,0x5c0,0xe4c0,
  0xe5e0,0xe5c0,0x2910,0xe510,0xe5e8,0xe5c8,0x2950,0xe550,0xe5f0,0xe5d0,0x2980,0xe580,0xe5f8,0xe5d8,0x29c0,0xe5c0,
  0xe400,0xe420,0x2910,0xe510,0xe408,0xe428,0x2950,0xe550,0xe410,0xe430,0x2980,0xe580,0xe418,0xe438,0x29c0,0xe5c0,
};

__global__ __launch_bounds__(256, 1) void wino_main_kernel(const char* __restrict__ x, char* __restrict__ Uq, char* __restrict__ out, const char* __restrict__ wt,
                                                           unsigned long long region_mask) {
  __shared__ __attribute__((aligned(1024))) char smem[163840];
  unsigned tid = threadIdx.x;
  asm volatile(
"s_load_dwordx2 s[12:13], %1, 0\n\t"
"s_load_dwordx2 s[52:53], %1, 24\n\t"
"s_load_dwordx2 s[8:9], %1, 8\n\t"
"s_load_dwordx2 s[16:17], %1, 16\n\t"
"s_load_dword s76, %1, 32\n\t"
"s_getpc_b64 s[40:41]\n\t"
"Lwino_pc:\n\t"
"s_bfe_u32 s42, %2, 0x50003\n\t"
"s_lshl_b32 s42, s42, 12\n\t"
"v_lshl_add_u32 v203, v205, 4, s42\n\t"
"v_min_u32 v203, (Lwino_end-Lwino_pc-16)&-16, v203\n\t"
"global_load_dwordx4 v[100:103], v203, s[40:41]\n\t"
"v_readfirstlane_b32 s73, v205\n\t"
"s_lshl2_add_u32 s69, s73, %2\n\t"
"s_and_b32 s60, %2, 7\n\t"
"s_lshr_b32 s61, %2, 7\n\t"
"s_lshl2_add_u32 s60, s60, s61\n\t"
"s_bfe_u32 s61, %2, 0x40003\n\t"
"s_lshl_b32 s60, s60, 8\n\t"
"s_add_u32 s60, s60, s61\n\t"
"s_sub_u32 s60, s60, 16\n\t"
"s_mul_i32 s21, s60, 12288\n\t"
"s_lshr_b32 s73, s73, 6\n\t"
"s_add_u32 s24, s73, 28\n\t"
"s_mulk_i32 s24, 1536\n\t"
"v_bfe_u32 v1, v205, 2, 3\n\t"
"v_lshrrev_b32 v2, 3, v205\n\t"
"v_and_or_b32 v1, v2, 24, v1\n\t"
"v_and_b32 v3, 6, v2\n\t"
"v_and_b32 v4, 3, v205\n\t"
"v_xor_b32 v4, v3, v4\n\t"
"v_mul_u32_u24 v1, 384, v1\n\t"
"v_lshl_add_u32 v28, v4, 4, v1\n\t"
"s_mov_b32 s14, 100663296\n\t"
"s_mov_b32 s15, 0x20000\n\t"
"s_mov_b32 s54, 663552\n\t"
"s_mov_b32 s55, s15\n\t"
"s_mov_b32 s10, 0x200000\n\t"
"s_mov_b32 s11, s15\n\t"
"s_lshl_b32 s60, s73, 11\n\t"
"v_lshl_add_u32 v252, v205, 4, s60\n\t"
"s_lshr_b32 s71, %5, 6\n\t"
"s_mul_i32 s71, s71, 0x9e3779b1\n\t"
"s_lshr_b32 s71, s71, 28\n\t"
"s_add_u32 s71, s71, 2\n\t"
"s_lshl_b32 s71, s71, 21\n\t"
"s_mov_b64 s[46:47], %3\n\t"
"s_add_u32 s48, s46, 12288\n\t"
"s_addc_u32 s49, s47, 0\n\t"
"global_load_dwordx4 v[244:247], v252, s[46:47] offset:2048\n\t"
"s_and_b32 s60, s69, 3\n\t"
"s_lshr_b32 s61, s69, 2\n\t"
"s_mul_i32 s62, s61, 171\n\t"
"s_lshr_b32 s62, s62, 9\n\t"
"s_mul_i32 s63, s62, 3\n\t"
"s_sub_u32 s61, s61, s63\n\t"
"s_mul_i32 s63, s62, 43\n\t"
"s_lshr_b32 s63, s63, 7\n\t"
"s_mul_i32 s64, s63, 3\n\t"
"s_sub_u32 s62, s62, s64\n\t"
"s_mul_i32 s74, s62, 18\n\t"
"s_add_u32 s74, s74, s63\n\t"
"s_lshl3_add_u32 s74, s74, s60\n\t"
"s_mul_i32 s74, s74, 12\n\t"
"s_add_u32 s74, s74, s61\n\t"
"s_lshl_b32 s74, s74, 7\n\t"
"v_bfe_u32 v96, v205, 3, 2\n\t"
"v_and_b32 v97, 32, v205\n\t"
"v_lshl_or_b32 v96, v96, 1, v97\n\t"
"v_mul_u32_u24 v96, 192, v96\n\t"
"v_and_b32 v97, 7, v205\n\t"
"v_lshl_add_u32 v96, v97, 4, v96\n\t"
"v_and_b32 v4, 7, v205\n\t"
"v_xor_b32 v4, v3, v4\n\t"
"v_and_b32 v2, 7, v2\n\t"
"v_mul_u32_u24 v2, 384, v2\n\t"
"v_lshl_add_u32 v29, v4, 4, v2\n\t"
"s_sub_u32 s58, s73, 1\n\t"
"s_lshl_b32 s59, s58, 10\n\t"
"s_lshl_b32 s58, s58, 7\n\t"
"s_waitcnt lgkmcnt(0)\n\t"
"s_and_b32 s13, s13, 0xffff\n\t"
"s_and_b32 s53, s53, 0xffff\n\t"
"s_and_b32 s71, s71, s76\n\t"
"s_add_u32 s8, s8, s71\n\t"
"s_addc_u32 s9, s9, 0\n\t"
"s_and_b32 s9, s9, 0xffff\n\t"
"s_cmp_lg_u32 s73, 0\n\t"
"s_cbranch_scc1 Lpb_%=\n\t"
"s_cmp_lt_u32 s69, 216\n\t"
"s_cbranch_scc0 Lnf1_%=\n\t"
"s_mov_b32 m0, 95232\n\t"
"s_add_u32 s64, s74, 0\n\t"
"buffer_load_dwordx4 v96, s[52:55], s64 offen lds\n\t"
"s_mov_b32 m0, 96256\n\t"
"s_add_u32 s64, s74, 73728\n\t"
"buffer_load_dwordx4 v96, s[52:55], s64 offen lds\n\t"
"s_mov_b32 m0, 97280\n\t"
"s_add_u32 s64, s74, 147456\n\t"
"buffer_load_dwordx4 v96, s[52:55], s64 offen lds\n\t"
"Lnf1_%=:\n\t"
"s_mov_b32 s72, 1900544\n\t"
"s_lshl2_add_u32 s70, %2, s72\n\t"
"s_mov_b32 s68, 3000\n\t"
"buffer_load_dwordx4 v[220:223], v252, s[8:11], s72 offen sc1\n\t"
"s_branch Lpa_%=\n\t"
"Lpb_%=:\n\t"
"s_lshr_b32 s56, %2, 3\n\t"
"s_bitcmp1_b32 0xaaaaaaaa, s56\n\t"
"s_cbranch_scc1 Lpc_%=\n\t"
"s_add_u32 m0, s59, 24576\n\t"
"s_add_u32 s57, s58, 0\n\t"
"buffer_load_dwordx4 v29, s[52:55], s57 offen offset:0 lds\n\t"
"buffer_load_dwordx4 v29, s[52:55], s57 offen offset:3072 lds\n\t"
"s_add_u32 m0, s59, 30720\n\t"
"s_add_u32 s57, s58, 6144\n\t"
"buffer_load_dwordx4 v29, s[52:55], s57 offen offset:0 lds\n\t"
"buffer_load_dwordx4 v29, s[52:55], s57 offen offset:3072 lds\n\t"
"s_add_u32 m0, s59, 36864\n\t"
"s_add_u32 s57, s58, 221184\n\t"
"buffer_load_dwordx4 v29, s[52:55], s57 offen offset:0 lds\n\t"
"buffer_load_dwordx4 v29, s[52:55], s57 offen offset:3072 lds\n\t"
"s_add_u32 m0, s59, 82944\n\t"
"s_add_u32 s57, s58, 227328\n\t"
"buffer_load_dwordx4 v29, s[52:55], s57 offen offset:0 lds\n\t"
"buffer_load_dwordx4 v29, s[52:55], s57 offen offset:3072 lds\n\t"
"s_add_u32 m0, s59, 89088\n\t"
"s_add_u32 s57, s58, 442368\n\t"
"buffer_load_dwordx4 v29, s[52:55], s57 offen offset:0 lds\n\t"
"buffer_load_dwordx4 v29, s[52:55], s57 offen offset:3072 lds\n\t"
"s_add_u32 m0, s59, 142848\n\t"
"s_add_u32 s57, s58, 448512\n\t"
"buffer_load_dwordx4 v29, s[52:55], s57 offen offset:0 lds\n\t"
"buffer_load_dwordx4 v29, s[52:55], s57 offen offset:3072 lds\n\t"
"s_branch Lpro_%=\n\t"
"Lpa_%=:\n\t"
"s_mul_i32 s75, s63, 288\n\t"
"s_add_u32 s75, s75, s62\n\t"
"s_lshl2_add_u32 s75, s75, s60\n\t"
"s_cmp_lt_u32 s61, 2\n\t"
"s_cselect_b64 vcc, -1, 0\n\t"
"s_cselect_b32 s61, s61, 4\n\t"
"s_mulk_i32 s61, 24\n\t"
"s_add_u32 s75, s75, s61\n\t"
"s_lshl_b32 s75, s75, 8\n\t"
"v_and_b32 v99, 12, v205\n\t"
"v_and_b32 v60, 35, v205\n\t"
"v_lshl_or_b32 v60, v99, 1, v60\n\t"
"v_bfe_u32 v99, v205, 4, 1\n\t"
"v_lshl_or_b32 v60, v99, 2, v60\n\t"
"v_cndmask_b32 v60, v205, v60, vcc\n\t"
"v_lshlrev_b32 v60, 1, v60\n\t"
"v_add_u32 v60, 95232, v60\n\t"
"v_lshrrev_b32 v97, 5, v205\n\t"
"v_lshl_or_b32 v97, v99, 2, v97\n\t"
"v_lshrrev_b32 v99, 4, v205\n\t"
"v_cndmask_b32 v97, v99, v97, vcc\n\t"
"v_mul_u32_u24 v97, 3072, v97\n\t"
"v_and_b32 v99, 15, v205\n\t"
"v_lshl_add_u32 v97, v99, 4, v97\n\t"
"s_cmp_lt_u32 s63, 2\n\t"
"s_mov_b32 s56, 0x3f2aaaab\n\t"
"s_mov_b32 s57, 0x3fc00000\n\t"
"s_mov_b32 s58, 0x3ee38e39\n\t"
"s_mov_b32 s59, 0x40100000\n\t"
"s_mov_b32 s62, 0xbf54ad4b\n\t"
"s_mov_b32 s63, 0xbefc0fc1\n\t"
"s_mov_b32 s64, 0xbe7c0fc1\n\t"
"s_mov_b32 s65, 0x3ea80a81\n\t"
"s_mov_b32 s38, 0x3f638e39\n\t"
"s_mov_b32 s39, 0x3f638e39\n\t"
"s_cselect_b32 s38, 0x3ea80a81, s38\n\t"
"s_cselect_b32 s39, 0xbebd0bd1, s39\n\t"
"v_mov_b64 v[32:33], 0\n\t"
"v_mov_b64 v[34:35], 0\n\t"
"v_mov_b64 v[36:37], 0\n\t"
"v_mov_b64 v[38:39], 0\n\t"
"v_mov_b64 v[40:41], 0\n\t"
"v_mov_b64 v[42:43], 0\n\t"
"v_mov_b64 v[44:45], 0\n\t"
"v_mov_b64 v[46:47], 0\n\t"
"v_mov_b64 v[48:49], 0\n\t"
"v_mov_b64 v[50:51], 0\n\t"
"v_mov_b64 v[52:53], 0\n\t"
"v_mov_b64 v[54:55], 0\n\t"
"s_mul_i32 s25, s73, 19968\n\t"
"s_add_u32 s25, s25, 61440\n\t"
"s_bfe_u32 s41, %2, 0x40003\n\t"
"s_cmp_eq_u32 s41, 0\n\t"
"s_cselect_b32 s40, -1, 0xbfffffff\n\t"
"s_cmp_eq_u32 s41, 15\n\t"
"s_cselect_b32 s41, -1, 0xdfffffff\n\t"
"s_and_b32 s40, s40, s41\n\t"
"s_sub_u32 s42, s21, 384\n\t"
"s_mul_i32 s41, s73, 589824\n\t"
"s_add_u32 s42, s42, s41\n\t"
"s_and_b32 s41, s40, 536883584\n\t"
"s_and_b32 s40, s40, 2.0\n\t"
"v_bfe_u32 v26, v205, 2, 3\n\t"
"v_add_u32 v27, -1, v26\n\t"
"v_cmp_gt_u32 vcc, 6, v27\n\t"
"v_lshrrev_b32 v27, 1, v27\n\t"
"v_mul_u32_u24 v27, 196608, v27\n\t"
"v_and_b32 v20, 0x70, v28\n\t"
"v_add3_u32 v27, v27, v20, s42\n\t"
"v_bfe_i32 v20, v205, 2, 1\n\t"
"v_mov_b32 v21, s40\n\t"
"v_bfi_b32 v20, v20, s41, v21\n\t"
"v_add_u32 v27, v27, v20\n\t"
"v_bfrev_b32 v20, 8\n\t"
"v_cndmask_b32 v254, v20, v27, vcc\n\t"
"v_bfe_u32 v26, v205, 2, 3\n\t"
"v_bfe_u32 v27, v205, 5, 1\n\t"
"v_mul_u32_u24 v27, 3072, v27\n\t"
"v_lshl_add_u32 v27, v26, 7, v27\n\t"
"v_and_b32 v26, 6, v26\n\t"
"v_lshrrev_b32 v20, 4, v205\n\t"
"v_and_b32 v20, 4, v20\n\t"
"v_and_or_b32 v20, v205, 3, v20\n\t"
"v_xor_b32 v20, v20, v26\n\t"
"v_lshl_add_u32 v20, v20, 4, v27\n\t"
"v_lshrrev_b32 v21, 7, v205\n\t"
"v_lshl_add_u32 v20, v21, 10, v20\n\t"
"v_lshrrev_b32 v21, 5, v205\n\t"
"v_and_b32 v21, 6, v21\n\t"
"v_bfe_u32 v22, v205, 1, 1\n\t"
"v_or_b32 v21, v21, v22\n\t"
"v_xor_b32 v21, v21, v26\n\t"
"v_lshl_add_u32 v21, v21, 4, v27\n\t"
"v_and_b32 v22, 1, v205\n\t"
"v_lshl_add_u32 v21, v22, 3, v21\n\t"
"v_add_u32 v22, 142848, v20\n\t"
"v_add_u32 v23, 142848, v21\n\t"
"v_add_u32 v24, 82944, v20\n\t"
"v_add_u32 v25, 82944, v21\n\t"
"s_and_b32 s17, s17, 0xffff\n\t"
"s_mov_b32 s18, 100663296\n\t"
"s_mov_b32 s19, 0x20000\n\t"
"s_lshr_b32 s41, s73, 1\n\t"
"s_add_u32 s42, s21, 196608\n\t"
"s_mul_i32 s43, s41, 384\n\t"
"s_add_u32 s22, s42, s43\n\t"
"s_lshl1_add_u32 s40, s43, s22\n\t"
"s_mul_i32 s43, s41, 196608\n\t"
"s_add_u32 s23, s42, s43\n\t"
"s_mov_b32 s44, 0x0fff0fff\n\t"
"s_mov_b32 s45, 0x0fff0fff\n\t"
"s_mul_i32 s26, s41, 19968\n\t"
"s_add_u32 s26, s26, 141312\n\t"
"s_sub_u32 s43, s41, s73\n\t"
"s_add_u32 s43, s43, 4\n\t"
"s_mul_i32 s27, s43, 589824\n\t"
"s_bitcmp1_b32 s73, 0\n\t"
"s_mov_b32 s36, 0x0fff0000\n\t"
"s_cselect_b32 s36, 0xfff0, s36\n\t"
"s_mov_b32 s37, s36\n\t"
"s_mul_i32 s80, s73, 3072\n\t"
"s_add_u32 s80, s80, s8\n\t"
"s_addc_u32 s81, s9, 0x100000\n\t"
"s_mov_b32 s82, 0x1c0000\n\t"
"s_mov_b32 s83, 0x800000\n\t"
"s_cmp_lt_u32 s69, 216\n\t"
"s_cbranch_scc0 Lnf3_%=\n\t"
"s_waitcnt vmcnt(1)\n\t"
"ds_read_u16_d16_hi v32, v60 offset:0\n\t"
"ds_read_u16_d16_hi v33, v60 offset:128\n\t"
"ds_read_u16_d16_hi v40, v60 offset:1024\n\t"
"ds_read_u16_d16_hi v41, v60 offset:1152\n\t"
"ds_read_u16_d16_hi v48, v60 offset:2048\n\t"
"ds_read_u16_d16_hi v49, v60 offset:2176\n\t"
"ds_read_u16_d16_hi v34, v60 offset:256\n\t"
"ds_read_u16_d16_hi v35, v60 offset:384\n\t"
"ds_read_u16_d16_hi v42, v60 offset:1280\n\t"
"ds_read_u16_d16_hi v43, v60 offset:1408\n\t"
"ds_read_u16_d16_hi v50, v60 offset:2304\n\t"
"ds_read_u16_d16_hi v51, v60 offset:2432\n\t"
"ds_read_u16_d16_hi v36, v60 offset:512\n\t"
"ds_read_u16_d16_hi v37, v60 offset:640\n\t"
"ds_read_u16_d16_hi v44, v60 offset:1536\n\t"
"ds_read_u16_d16_hi v45, v60 offset:1664\n\t"
"ds_read_u16_d16_hi v52, v60 offset:2560\n\t"
"ds_read_u16_d16_hi v53, v60 offset:2688\n\t"
"ds_read_u16_d16_hi v38, v60 offset:768\n\t"
"ds_read_u16_d16_hi v39, v60 offset:896\n\t"
"ds_read_u16_d16_hi v46, v60 offset:1792\n\t"
"ds_read_u16_d16_hi v47, v60 offset:1920\n\t"
"ds_read_u16_d16_hi v54, v60 offset:2816\n\t"
"ds_read_u16_d16_hi v55, v60 offset:2944\n\t"
"s_waitcnt lgkmcnt(15)\n\t"
"v_pk_fma_f32 v[56:57], v[48:49], s[58:59], v[32:33] op_sel:[0,0,0] op_sel_hi:[1,0,1]\n\t"
"v_pk_fma_f32 v[58:59], v[48:49], s[58:59], v[32:33] op_sel:[0,1,0] op_sel_hi:[1,1,1]\n\t"
"v_pk_mul_f32 v[104:105], v[32:33], s[38:39] op_sel:[0,0] op_sel_hi:[1,0]\n\t"
"v_pk_mul_f32 v[114:115], v[48:49], s[38:39] op_sel:[0,1] op_sel_hi:[1,1]\n\t"
"v_pk_fma_f32 v[106:107], v[40:41], s[56:57], v[56:57] op_sel:[0,0,0] op_sel_hi:[1,0,1]\n\t"
"v_pk_fma_f32 v[108:109], v[40:41], s[56:57], v[56:57] op_sel:[0,0,0] op_sel_hi:[1,0,1] neg_lo:[1,0,0] neg_hi:[1,0,0]\n\t"
"v_pk_fma_f32 v[110:111], v[40:41], s[56:57], v[58:59] op_sel:[0,1,0] op_sel_hi:[1,1,1]\n\t"
"v_pk_fma_f32 v[112:113], v[40:41], s[56:57], v[58:59] op_sel:[0,1,0] op_sel_hi:[1,1,1] neg_lo:[1,0,0] neg_hi:[1,0,0]\n\t"
"v_pk_mul_f32 v[106:107], v[106:107], s[62:63] op_sel:[0,0] op_sel_hi:[1,0]\n\t"
"v_pk_mul_f32 v[108:109], v[108:109], s[62:63] op_sel:[0,1] op_sel_hi:[1,1]\n\t"
"v_pk_mul_f32 v[110:111], v[110:111], s[64:65] op_sel:[0,0] op_sel_hi:[1,0]\n\t"
"v_pk_mul_f32 v[112:113], v[112:113], s[64:65] op_sel:[0,1] op_sel_hi:[1,1]\n\t"
"v_cvt_pk_bf16_f32 v64, v104, v105\n\t"
"v_cvt_pk_bf16_f32 v84, v114, v115\n\t"
"v_cvt_pk_bf16_f32 v68, v106, v107\n\t"
"v_cvt_pk_bf16_f32 v72, v108, v109\n\t"
"v_cvt_pk_bf16_f32 v76, v110, v111\n\t"
"v_cvt_pk_bf16_f32 v80, v112, v113\n\t"
"s_waitcnt lgkmcnt(12)\n\t"
"v_pk_fma_f32 v[56:57], v[50:51], s[58:59], v[34:35] op_sel:[0,0,0] op_sel_hi:[1,0,1]\n\t"
"v_pk_fma_f32 v[58:59], v[50:51], s[58:59], v[34:35] op_sel:[0,1,0] op_sel_hi:[1,1,1]\n\t"
"v_pk_mul_f32 v[104:105], v[34:35], s[38:39] op_sel:[0,0] op_sel_hi:[1,0]\n\t"
"v_pk_mul_f32 v[114:115], v[50:51], s[38:39] op_sel:[0,1] op_sel_hi:[1,1]\n\t"
"v_pk_fma_f32 v[106:107], v[42:43], s[56:57], v[56:57] op_sel:[0,0,0] op_sel_hi:[1,0,1]\n\t"
"v_pk_fma_f32 v[108:109], v[42:43], s[56:57], v[56:57] op_sel:[0,0,0] op_sel_hi:[1,0,1] neg_lo:[1,0,0] neg_hi:[1,0,0]\n\t"
"v_pk_fma_f32 v[110:111], v[42:43], s[56:57], v[58:59] op_sel:[0,1,0] op_sel_hi:[1,1,1]\n\t"
"v_pk_fma_f32 v[112:113], v[42:43], s[56:57], v[58:59] op_sel:[0,1,0] op_sel_hi:[1,1,1] neg_lo:[1,0,0] neg_hi:[1,0,0]\n\t"
"v_pk_mul_f32 v[106:107], v[106:107], s[62:63] op_sel:[0,0] op_sel_hi:[1,0]\n\t"
"v_pk_mul_f32 v[108:109], v[108:109], s[62:63] op_sel:[0,1] op_sel_hi:[1,1]\n\t"
"v_pk_mul_f32 v[110:111], v[110:111], s[64:65] op_sel:[0,0] op_sel_hi:[1,0]\n\t"
"v_pk_mul_f32 v[112:113], v[112:113], s[64:65] op_sel:[0,1] op_sel_hi:[1,1]\n\t"
"v_cvt_pk_bf16_f32 v65, v104, v105\n\t"
"v_cvt_pk_bf16_f32 v85, v114, v115\n\t"
"v_cvt_pk_bf16_f32 v69, v106, v107\n\t"
"v_cvt_pk_bf16_f32 v73, v108, v109\n\t"
"v_cvt_pk_bf16_f32 v77, v110, v111\n\t"
"v_cvt_pk_bf16_f32 v81, v112, v113\n\t"
"s_waitcnt lgkmcnt(6)\n\t"
"v_pk_fma_f32 v[56:57], v[52:53], s[58:59], v[36:37] op_sel:[0,0,0] op_sel_hi:[1,0,1]\n\t"
"v_pk_fma_f32 v[58:59], v[52:53], s[58:59], v[36:37] op_sel:[0,1,0] op_sel_hi:[1,1,1]\n\t"
"v_pk_mul_f32 v[104:105], v[36:37], s[38:39] op_sel:[0,0] op_sel_hi:[1,0]\n\t"
"v_pk_mul_f32 v[114:115], v[52:53], s[38:39] op_sel:[0,1] op_sel_hi:[1,1]\n\t"
"v_pk_fma_f32 v[106:107], v[44:45], s[56:57], v[56:57] op_sel:[0,0,0] op_sel_hi:[1,0,1]\n\t"
"v_pk_fma_f32 v[108:109], v[44:45], s[56:57], v[56:57] op_sel:[0,0,0] op_sel_hi:[1,0,1] neg_lo:[1,0,0] neg_hi:[1,0,0]\n\t"
"v_pk_fma_f32 v[110:111], v[44:45], s[56:57], v[58:59] op_sel:[0,1,0] op_sel_hi:[1,1,1]\n\t"
"v_pk_fma_f32 v[112:113], v[44:45], s[56:57], v[58:59] op_sel:[0,1,0] op_sel_hi:[1,1,1] neg_lo:[1,0,0] neg_hi:[1,0,0]\n\t"
"v_pk_mul_f32 v[106:107], v[106:107], s[62:63] op_sel:[0,0] op_sel_hi:[1,0]\n\t"
"v_pk_mul_f32 v[108:109], v[108:109], s[62:63] op_sel:[0,1] op_sel_hi:[1,1]\n\t"
"v_pk_mul_f32 v[110:111], v[110:111], s[64:65] op_sel:[0,0] op_sel_hi:[1,0]\n\t"
"v_pk_mul_f32 v[112:113], v[112:113], s[64:65] op_sel:[0,1] op_sel_hi:[1,1]\n\t"
"v_cvt_pk_bf16_f32 v66, v104, v105\n\t"
"v_cvt_pk_bf16_f32 v86, v114, v115\n\t"
"v_cvt_pk_bf16_f32 v70, v106, v107\n\t"
"v_cvt_pk_bf16_f32 v74, v108, v109\n\t"
"v_cvt_pk_bf16_f32 v78, v110, v111\n\t"
"v_cvt_pk_bf16_f32 v82, v112, v113\n\t"
"s_waitcnt lgkmcnt(0)\n\t"
"v_pk_fma_f32 v[56:57], v[54:55], s[58:59], v[38:39] op_sel:[0,0,0] op_sel_hi:[1,0,1]\n\t"
"v_pk_fma_f32 v[58:59], v[54:55], s[58:59], v[38:39] op_sel:[0,1,0] op_sel_hi:[1,1,1]\n\t"
"v_pk_mul_f32 v[104:105], v[38:39], s[38:39] op_sel:[0,0] op_sel_hi:[1,0]\n\t"
"v_pk_mul_f32 v[114:115], v[54:55], s[38:39] op_sel:[0,1] op_sel_hi:[1,1]\n\t"
"v_pk_fma_f32 v[106:107], v[46:47], s[56:57], v[56:57] op_sel:[0,0,0] op_sel_hi:[1,0,1]\n\t"
"v_pk_fma_f32 v[108:109], v[46:47], s[56:57], v[56:57] op_sel:[0,0,0] op_sel_hi:[1,0,1] neg_lo:[1,0,0] neg_hi:[1,0,0]\n\t"
"v_pk_fma_f32 v[110:111], v[46:47], s[56:57], v[58:59] op_sel:[0,1,0] op_sel_hi:[1,1,1]\n\t"
"v_pk_fma_f32 v[112:113], v[46:47], s[56:57], v[58:59] op_sel:[0,1,0] op_sel_hi:[1,1,1] neg_lo:[1,0,0] neg_hi:[1,0,0]\n\t"
"v_pk_mul_f32 v[106:107], v[106:107], s[62:63] op_sel:[0,0] op_sel_hi:[1,0]\n\t"
"v_pk_mul_f32 v[108:109], v[108:109], s[62:63] op_sel:[0,1] op_sel_hi:[1,1]\n\t"
"v_pk_mul_f32 v[110:111], v[110:111], s[64:65] op_sel:[0,0] op_sel_hi:[1,0]\n\t"
"v_pk_mul_f32 v[112:113], v[112:113], s[64:65] op_sel:[0,1] op_sel_hi:[1,1]\n\t"
"v_cvt_pk_bf16_f32 v67, v104, v105\n\t"
"v_cvt_pk_bf16_f32 v87, v114, v115\n\t"
"v_cvt_pk_bf16_f32 v71, v106, v107\n\t"
"v_cvt_pk_bf16_f32 v75, v108, v109\n\t"
"v_cvt_pk_bf16_f32 v79, v110, v111\n\t"
"v_cvt_pk_bf16_f32 v83, v112, v113\n\t"
"s_add_u32 s60, s75, 0\n\t"
"buffer_store_dwordx4 v[64:67], v97, s[8:11], s60 offen sc1\n\t"
"s_add_u32 s60, s75, 184320\n\t"
"buffer_store_dwordx4 v[84:87], v97, s[8:11], s60 offen sc1\n\t"
"s_add_u32 s60, s75, 36864\n\t"
"buffer_store_dwordx4 v[68:71], v97, s[8:11], s60 offen sc1\n\t"
"s_add_u32 s60, s75, 73728\n\t"
"s_xor_b32 s60, s60, 256\n\t"
"buffer_store_dwordx4 v[72:75], v97, s[8:11], s60 offen sc1\n\t"
"s_add_u32 s60, s75, 110592\n\t"
"s_xor_b32 s60, s60, 512\n\t"
"buffer_store_dwordx4 v[76:79], v97, s[8:11], s60 offen sc1\n\t"
"s_add_u32 s60, s75, 147456\n\t"
"s_xor_b32 s60, s60, 768\n\t"
"buffer_store_dwordx4 v[80:83], v97, s[8:11], s60 offen sc1\n\t"
"s_waitcnt vmcnt(6)\n\t"
"s_branch Lnf4_%=\n\t"
"Lnf3_%=:\n\t"
"s_waitcnt vmcnt(0)\n\t"
"Lnf4_%=:\n\t"
"s_lshr_b32 s60, %2, 2\n\t"
"v_readlane_b32 s61, v220, s60\n\t"
"v_readlane_b32 s62, v221, s60\n\t"
"v_readlane_b32 s63, v222, s60\n\t"
"v_readlane_b32 s64, v223, s60\n\t"
"s_bitcmp1_b32 %2, 0\n\t"
"s_cselect_b32 s61, s62, s61\n\t"
"s_cselect_b32 s63, s64, s63\n\t"
"s_bitcmp1_b32 %2, 1\n\t"
"s_cselect_b32 s71, s63, s61\n\t"
"s_branch Ljoin_%=\n\t"
"Lpc_%=:\n\t"
"s_add_u32 m0, s24, 59904\n\t"
"s_add_u32 s28, s21, 1769472\n\t"
"buffer_load_dwordx4 v28, s[12:15], s28 offen offset:0 nt lds\n\t"
"s_add_u32 m0, s24, 66048\n\t"
"s_add_u32 s28, s21, 1966080\n\t"
"buffer_load_dwordx4 v28, s[12:15], s28 offen offset:0 nt lds\n\t"
"s_add_u32 m0, s24, 72192\n\t"
"s_add_u32 s28, s21, 2162688\n\t"
"buffer_load_dwordx4 v28, s[12:15], s28 offen offset:0 nt lds\n\t"
"s_mul_i32 s60, s59, 6\n\t"
"s_mul_i32 s61, s59, 192\n\t"
"s_add_u32 s61, s61, s21\n\t"
"s_mul_i32 s62, s73, 3072\n\t"
"v_subrev_u32 v30, s62, v28\n\t"
"s_add_u32 m0, s60, 102912\n\t"
"s_add_u32 s28, s61, 1769472\n\t"
"buffer_load_dwordx4 v30, s[12:15], s28 offen nt lds\n\t"
"s_add_u32 m0, s24, 79872\n\t"
"s_add_u32 s28, s21, 2359296\n\t"
"buffer_load_dwordx4 v28, s[12:15], s28 offen offset:0 nt lds\n\t"
"s_add_u32 m0, s24, 86016\n\t"
"s_add_u32 s28, s21, 2555904\n\t"
"buffer_load_dwordx4 v28, s[12:15], s28 offen offset:0 nt lds\n\t"
"s_add_u32 m0, s24, 92160\n\t"
"s_add_u32 s28, s21, 2752512\n\t"
"buffer_load_dwordx4 v28, s[12:15], s28 offen offset:0 nt lds\n\t"
"s_add_u32 m0, s60, 122880\n\t"
"s_add_u32 s28, s61, 2359296\n\t"
"buffer_load_dwordx4 v30, s[12:15], s28 offen nt lds\n\t"
"s_mul_i32 s25, s73, 19968\n\t"
"s_add_u32 s25, s25, 61440\n\t"
"s_bfe_u32 s41, %2, 0x40003\n\t"
"s_cmp_eq_u32 s41, 0\n\t"
"s_cselect_b32 s40, -1, 0xbfffffff\n\t"
"s_cmp_eq_u32 s41, 15\n\t"
"s_cselect_b32 s41, -1, 0xdfffffff\n\t"
"s_and_b32 s40, s40, s41\n\t"
"s_sub_u32 s42, s21, 384\n\t"
"s_mul_i32 s41, s73, 589824\n\t"
"s_add_u32 s42, s42, s41\n\t"
"s_and_b32 s41, s40, 536883584\n\t"
"s_and_b32 s40, s40, 2.0\n\t"
"v_bfe_u32 v26, v205, 2, 3\n\t"
"v_add_u32 v27, -1, v26\n\t"
"v_cmp_gt_u32 vcc, 6, v27\n\t"
"v_lshrrev_b32 v27, 1, v27\n\t"
"v_mul_u32_u24 v27, 196608, v27\n\t"
"v_and_b32 v20, 0x70, v28\n\t"
"v_add3_u32 v27, v27, v20, s42\n\t"
"v_bfe_i32 v20, v205, 2, 1\n\t"
"v_mov_b32 v21, s40\n\t"
"v_bfi_b32 v20, v20, s41, v21\n\t"
"v_add_u32 v27, v27, v20\n\t"
"v_bfrev_b32 v20, 8\n\t"
"v_cndmask_b32 v254, v20, v27, vcc\n\t"
"s_add_u32 m0, s25, 0\n\t"
"s_nop 0\n\t"
"buffer_load_dwordx4 v254, s[12:15], 0 offen nt lds\n\t"
"s_cmp_eq_u32 s73, 3\n\t"
"s_cselect_b32 s62, -1, 1\n\t"
"s_mul_i32 s63, s62, 59904\n\t"
"s_mul_i32 s62, s62, 1769472\n\t"
"v_add_u32 v19, s62, v254\n\t"
"s_add_u32 m0, s25, s63\n\t"
"s_nop 0\n\t"
"buffer_load_dwordx4 v19, s[12:15], 0 offen nt lds\n\t"
"v_bfe_u32 v26, v205, 2, 3\n\t"
"v_bfe_u32 v27, v205, 5, 1\n\t"
"v_mul_u32_u24 v27, 3072, v27\n\t"
"v_lshl_add_u32 v27, v26, 7, v27\n\t"
"v_and_b32 v26, 6, v26\n\t"
"v_lshrrev_b32 v20, 4, v205\n\t"
"v_and_b32 v20, 4, v20\n\t"
"v_and_or_b32 v20, v205, 3, v20\n\t"
"v_xor_b32 v20, v20, v26\n\t"
"v_lshl_add_u32 v20, v20, 4, v27\n\t"
"v_lshrrev_b32 v21, 7, v205\n\t"
"v_lshl_add_u32 v20, v21, 10, v20\n\t"
"v_lshrrev_b32 v21, 5, v205\n\t"
"v_and_b32 v21, 6, v21\n\t"
"v_bfe_u32 v22, v205, 1, 1\n\t"
"v_or_b32 v21, v21, v22\n\t"
"v_xor_b32 v21, v21, v26\n\t"
"v_lshl_add_u32 v21, v21, 4, v27\n\t"
"v_and_b32 v22, 1, v205\n\t"
"v_lshl_add_u32 v21, v22, 3, v21\n\t"
"v_add_u32 v22, 142848, v20\n\t"
"v_add_u32 v23, 142848, v21\n\t"
"v_add_u32 v24, 82944, v20\n\t"
"v_add_u32 v25, 82944, v21\n\t"
"s_and_b32 s17, s17, 0xffff\n\t"
"s_mov_b32 s18, 100663296\n\t"
"s_mov_b32 s19, 0x20000\n\t"
"s_lshr_b32 s41, s73, 1\n\t"
"s_add_u32 s42, s21, 196608\n\t"
"s_mul_i32 s43, s41, 384\n\t"
"s_add_u32 s22, s42, s43\n\t"
"s_lshl1_add_u32 s40, s43, s22\n\t"
"s_mul_i32 s43, s41, 196608\n\t"
"s_add_u32 s23, s42, s43\n\t"
"s_mov_b32 s44, 0x0fff0fff\n\t"
"s_mov_b32 s45, 0x0fff0fff\n\t"
"s_mul_i32 s26, s41, 19968\n\t"
"s_add_u32 s26, s26, 141312\n\t"
"s_sub_u32 s43, s41, s73\n\t"
"s_add_u32 s43, s43, 4\n\t"
"s_mul_i32 s27, s43, 589824\n\t"
"s_bitcmp1_b32 s73, 0\n\t"
"s_mov_b32 s36, 0x0fff0000\n\t"
"s_cselect_b32 s36, 0xfff0, s36\n\t"
"s_mov_b32 s37, s36\n\t"
"s_mul_i32 s80, s73, 3072\n\t"
"s_add_u32 s80, s80, s8\n\t"
"s_addc_u32 s81, s9, 0x100000\n\t"
"s_mov_b32 s82, 0x1c0000\n\t"
"s_mov_b32 s83, 0x800000\n\t"
"s_add_u32 m0, s59, 24576\n\t"
"s_add_u32 s57, s58, 0\n\t"
"buffer_load_dwordx4 v29, s[52:55], s57 offen offset:0 lds\n\t"
"buffer_load_dwordx4 v29, s[52:55], s57 offen offset:3072 lds\n\t"
"s_add_u32 m0, s59, 30720\n\t"
"s_add_u32 s57, s58, 6144\n\t"
"buffer_load_dwordx4 v29, s[52:55], s57 offen offset:0 lds\n\t"
"buffer_load_dwordx4 v29, s[52:55], s57 offen offset:3072 lds\n\t"
"s_add_u32 m0, s59, 36864\n\t"
"s_add_u32 s57, s58, 221184\n\t"
"buffer_load_dwordx4 v29, s[52:55], s57 offen offset:0 lds\n\t"
"buffer_load_dwordx4 v29, s[52:55], s57 offen offset:3072 lds\n\t"
"s_add_u32 m0, s59, 82944\n\t"
"s_add_u32 s57, s58, 227328\n\t"
"buffer_load_dwordx4 v29, s[52:55], s57 offen offset:0 lds\n\t"
"buffer_load_dwordx4 v29, s[52:55], s57 offen offset:3072 lds\n\t"
"s_add_u32 m0, s59, 89088\n\t"
"s_add_u32 s57, s58, 442368\n\t"
"buffer_load_dwordx4 v29, s[52:55], s57 offen offset:0 lds\n\t"
"buffer_load_dwordx4 v29, s[52:55], s57 offen offset:3072 lds\n\t"
"s_add_u32 m0, s59, 142848\n\t"
"s_add_u32 s57, s58, 448512\n\t"
"buffer_load_dwordx4 v29, s[52:55], s57 offen offset:0 lds\n\t"
"buffer_load_dwordx4 v29, s[52:55], s57 offen offset:3072 lds\n\t"
"s_add_u32 m0, s59, 148992\n\t"
"s_add_u32 s57, s58, 147456\n\t"
"buffer_load_dwordx4 v29, s[52:55], s57 offen offset:0 lds\n\t"
"buffer_load_dwordx4 v29, s[52:55], s57 offen offset:3072 lds\n\t"
"s_add_u32 m0, s59, 155136\n\t"
"s_add_u32 s57, s58, 153600\n\t"
"buffer_load_dwordx4 v29, s[52:55], s57 offen offset:0 lds\n\t"
"buffer_load_dwordx4 v29, s[52:55], s57 offen offset:3072 lds\n\t"
"s_add_u32 m0, s59, 0\n\t"
"s_add_u32 s57, s58, 368640\n\t"
"buffer_load_dwordx4 v29, s[52:55], s57 offen offset:0 lds\n\t"
"buffer_load_dwordx4 v29, s[52:55], s57 offen offset:3072 lds\n\t"
"s_add_u32 m0, s59, 6144\n\t"
"s_add_u32 s57, s58, 374784\n\t"
"buffer_load_dwordx4 v29, s[52:55], s57 offen offset:0 lds\n\t"
"buffer_load_dwordx4 v29, s[52:55], s57 offen offset:3072 lds\n\t"
"s_add_u32 m0, s59, 12288\n\t"
"s_add_u32 s57, s58, 589824\n\t"
"buffer_load_dwordx4 v29, s[52:55], s57 offen offset:0 lds\n\t"
"buffer_load_dwordx4 v29, s[52:55], s57 offen offset:3072 lds\n\t"
"s_add_u32 m0, s59, 18432\n\t"
"s_add_u32 s57, s58, 595968\n\t"
"buffer_load_dwordx4 v29, s[52:55], s57 offen offset:0 lds\n\t"
"buffer_load_dwordx4 v29, s[52:55], s57 offen offset:3072 lds\n\t"
"s_add_u32 m0, s24, 19968\n\t"
"s_add_u32 s28, s21, 589824\n\t"
"buffer_load_dwordx4 v28, s[12:15], s28 offen offset:0 nt lds\n\t"
"s_add_u32 m0, s24, 26112\n\t"
"s_add_u32 s28, s21, 786432\n\t"
"buffer_load_dwordx4 v28, s[12:15], s28 offen offset:0 nt lds\n\t"
"s_add_u32 m0, s24, 32256\n\t"
"s_add_u32 s28, s21, 983040\n\t"
"buffer_load_dwordx4 v28, s[12:15], s28 offen offset:0 nt lds\n\t"
"s_add_u32 m0, s60, 62976\n\t"
"s_add_u32 s28, s61, 589824\n\t"
"buffer_load_dwordx4 v30, s[12:15], s28 offen nt lds\n\t"
"s_add_u32 m0, s24, 0\n\t"
"s_add_u32 s28, s21, 0\n\t"
"buffer_load_dwordx4 v28, s[12:15], s28 offen offset:0 nt lds\n\t"
"s_add_u32 m0, s24, 6144\n\t"
"s_add_u32 s28, s21, 196608\n\t"
"buffer_load_dwordx4 v28, s[12:15], s28 offen offset:0 nt lds\n\t"
"s_add_u32 m0, s24, 12288\n\t"
"s_add_u32 s28, s21, 393216\n\t"
"buffer_load_dwordx4 v28, s[12:15], s28 offen offset:0 nt lds\n\t"
"s_add_u32 m0, s60, 43008\n\t"
"s_add_u32 s28, s61, 0\n\t"
"buffer_load_dwordx4 v30, s[12:15], s28 offen nt lds\n\t"
"s_waitcnt vmcnt(42)\n\t"
"s_branch Ljoin_%=\n\t"
"Lfb_%=:\n\t"
"s_waitcnt vmcnt(0) lgkmcnt(0)\n\t"
"s_nop 15\n\t"
"v_lshrrev_b32 v255, 9, v252\n\t"
"v_lshl_add_u32 v255, v255, 10, v252\n\t"
"v_add_u32 v255, 44032, v255\n\t"
"ds_write_b128 v255, v[32:35] offset:0\n\t"
"ds_write_b128 v255, v[36:39] offset:3072\n\t"
"ds_write_b128 v255, v[40:43] offset:6144\n\t"
"ds_write_b128 v255, v[44:47] offset:9216\n\t"
"ds_write_b128 v255, v[48:51] offset:12288\n\t"
"ds_write_b128 v255, v[52:55] offset:15360\n\t"
"ds_write_b128 v255, v[56:59] offset:18432\n\t"
"ds_write_b128 v255, v[60:63] offset:21504\n\t"
"ds_write_b128 v255, v[64:67] offset:24576\n\t"
"ds_write_b128 v255, v[68:71] offset:27648\n\t"
"ds_write_b128 v255, v[72:75] offset:30720\n\t"
"ds_write_b128 v255, v[76:79] offset:33792\n\t"
"ds_write_b128 v255, v[80:83] offset:36864\n\t"
"ds_write_b128 v255, v[84:87] offset:39936\n\t"
"ds_write_b128 v255, v[88:91] offset:43008\n\t"
"ds_write_b128 v255, v[92:95] offset:46080\n\t"
"ds_write_b128 v255, v[96:99] offset:49152\n\t"
"ds_write_b128 v255, v[100:103] offset:52224\n\t"
"ds_write_b128 v255, v[104:107] offset:55296\n\t"
"ds_write_b128 v255, v[108:111] offset:58368\n\t"
"ds_write_b128 v255, v[112:115] offset:61440\n\t"
"ds_write_b128 v255, v[116:119] offset:64512\n\t"
"s_waitcnt lgkmcnt(0)\n\t"
"v_mov_b64 v[32:33], 0\n\t"
"v_mov_b64 v[34:35], 0\n\t"
"v_mov_b64 v[36:37], 0\n\t"
"v_mov_b64 v[38:39], 0\n\t"
"v_mov_b64 v[40:41], 0\n\t"
"v_mov_b64 v[42:43], 0\n\t"
"v_mov_b64 v[44:45], 0\n\t"
"v_mov_b64 v[46:47], 0\n\t"
"v_mov_b64 v[48:49], 0\n\t"
"v_mov_b64 v[50:51], 0\n\t"
"v_mov_b64 v[52:53], 0\n\t"
"v_mov_b64 v[54:55], 0\n\t"
"v_lshrrev_b32 v98, 4, v252\n\t"
"v_bfe_u32 v96, v98, 3, 2\n\t"
"v_and_b32 v97, 32, v98\n\t"
"v_lshl_or_b32 v96, v96, 1, v97\n\t"
"v_mul_u32_u24 v96, 192, v96\n\t"
"v_and_b32 v97, 7, v98\n\t"
"v_lshl_add_u32 v96, v97, 4, v96\n\t"
"s_mov_b32 s69, 0\n\t"
"Lfb1_%=:\n\t"
"s_and_b32 s60, s69, 3\n\t"
"s_lshr_b32 s61, s69, 2\n\t"
"s_mul_i32 s62, s61, 171\n\t"
"s_lshr_b32 s62, s62, 9\n\t"
"s_mul_i32 s63, s62, 3\n\t"
"s_sub_u32 s61, s61, s63\n\t"
"s_mul_i32 s63, s62, 43\n\t"
"s_lshr_b32 s63, s63, 7\n\t"
"s_mul_i32 s64, s63, 3\n\t"
"s_sub_u32 s62, s62, s64\n\t"
"s_mul_i32 s74, s62, 18\n\t"
"s_add_u32 s74, s74, s63\n\t"
"s_lshl3_add_u32 s74, s74, s60\n\t"
"s_mul_i32 s74, s74, 12\n\t"
"s_add_u32 s74, s74, s61\n\t"
"s_lshl_b32 s74, s74, 7\n\t"
"s_mov_b32 m0, 18432\n\t"
"s_add_u32 s64, s74, 0\n\t"
"buffer_load_dwordx4 v96, s[52:55], s64 offen lds\n\t"
"s_mov_b32 m0, 19456\n\t"
"s_add_u32 s64, s74, 73728\n\t"
"buffer_load_dwordx4 v96, s[52:55], s64 offen lds\n\t"
"s_mov_b32 m0, 20480\n\t"
"s_add_u32 s64, s74, 147456\n\t"
"buffer_load_dwordx4 v96, s[52:55], s64 offen lds\n\t"
"s_mul_i32 s75, s63, 288\n\t"
"s_add_u32 s75, s75, s62\n\t"
"s_lshl2_add_u32 s75, s75, s60\n\t"
"s_cmp_lt_u32 s61, 2\n\t"
"s_cselect_b64 vcc, -1, 0\n\t"
"s_cselect_b32 s61, s61, 4\n\t"
"s_mulk_i32 s61, 24\n\t"
"s_add_u32 s75, s75, s61\n\t"
"s_lshl_b32 s75, s75, 8\n\t"
"v_and_b32 v99, 12, v98\n\t"
"v_and_b32 v60, 35, v98\n\t"
"v_lshl_or_b32 v60, v99, 1, v60\n\t"
"v_bfe_u32 v99, v98, 4, 1\n\t"
"v_lshl_or_b32 v60, v99, 2, v60\n\t"
"v_cndmask_b32 v60, v98, v60, vcc\n\t"
"v_lshlrev_b32 v60, 1, v60\n\t"
"v_add_u32 v60, 18432, v60\n\t"
"v_lshrrev_b32 v97, 5, v98\n\t"
"v_lshl_or_b32 v97, v99, 2, v97\n\t"
"v_lshrrev_b32 v99, 4, v98\n\t"
"v_cndmask_b32 v97, v99, v97, vcc\n\t"
"v_mul_u32_u24 v97, 3072, v97\n\t"
"v_and_b32 v99, 15, v98\n\t"
"v_lshl_add_u32 v97, v99, 4, v97\n\t"
"s_cmp_lt_u32 s63, 2\n\t"
"s_mov_b32 s56, 0x3f2aaaab\n\t"
"s_mov_b32 s57, 0x3fc00000\n\t"
"s_mov_b32 s58, 0x3ee38e39\n\t"
"s_mov_b32 s59, 0x40100000\n\t"
"s_mov_b32 s62, 0xbf54ad4b\n\t"
"s_mov_b32 s63, 0xbefc0fc1\n\t"
"s_mov_b32 s64, 0xbe7c0fc1\n\t"
"s_mov_b32 s65, 0x3ea80a81\n\t"
"s_mov_b32 s38, 0x3f638e39\n\t"
"s_mov_b32 s39, 0x3f638e39\n\t"
"s_cselect_b32 s38, 0x3ea80a81, s38\n\t"
"s_cselect_b32 s39, 0xbebd0bd1, s39\n\t"
"s_waitcnt vmcnt(0)\n\t"
"ds_read_u16_d16_hi v32, v60 offset:0\n\t"
"ds_read_u16_d16_hi v33, v60 offset:128\n\t"
"ds_read_u16_d16_hi v40, v60 offset:1024\n\t"
"ds_read_u16_d16_hi v41, v60 offset:1152\n\t"
"ds_read_u16_d16_hi v48, v60 offset:2048\n\t"
"ds_read_u16_d16_hi v49, v60 offset:2176\n\t"
"ds_read_u16_d16_hi v34, v60 offset:256\n\t"
"ds_read_u16_d16_hi v35, v60 offset:384\n\t"
"ds_read_u16_d16_hi v42, v60 offset:1280\n\t"
"ds_read_u16_d16_hi v43, v60 offset:1408\n\t"
"ds_read_u16_d16_hi v50, v60 offset:2304\n\t"
"ds_read_u16_d16_hi v51, v60 offset:2432\n\t"
"ds_read_u16_d16_hi v36, v60 offset:512\n\t"
"ds_read_u16_d16_hi v37, v60 offset:640\n\t"
"ds_read_u16_d16_hi v44, v60 offset:1536\n\t"
"ds_read_u16_d16_hi v45, v60 offset:1664\n\t"
"ds_read_u16_d16_hi v52, v60 offset:2560\n\t"
"ds_read_u16_d16_hi v53, v60 offset:2688\n\t"
"ds_read_u16_d16_hi v38, v60 offset:768\n\t"
"ds_read_u16_d16_hi v39, v60 offset:896\n\t"
"ds_read_u16_d16_hi v46, v60 offset:1792\n\t"
"ds_read_u16_d16_hi v47, v60 offset:1920\n\t"
"ds_read_u16_d16_hi v54, v60 offset:2816\n\t"
"ds_read_u16_d16_hi v55, v60 offset:2944\n\t"
"s_waitcnt lgkmcnt(15)\n\t"
"v_pk_fma_f32 v[56:57], v[48:49], s[58:59], v[32:33] op_sel:[0,0,0] op_sel_hi:[1,0,1]\n\t"
"v_pk_fma_f32 v[58:59], v[48:49], s[58:59], v[32:33] op_sel:[0,1,0] op_sel_hi:[1,1,1]\n\t"
"v_pk_mul_f32 v[104:105], v[32:33], s[38:39] op_sel:[0,0] op_sel_hi:[1,0]\n\t"
"v_pk_mul_f32 v[114:115], v[48:49], s[38:39] op_sel:[0,1] op_sel_hi:[1,1]\n\t"
"v_pk_fma_f32 v[106:107], v[40:41], s[56:57], v[56:57] op_sel:[0,0,0] op_sel_hi:[1,0,1]\n\t"
"v_pk_fma_f32 v[108:109], v[40:41], s[56:57], v[56:57] op_sel:[0,0,0] op_sel_hi:[1,0,1] neg_lo:[1,0,0] neg_hi:[1,0,0]\n\t"
"v_pk_fma_f32 v[110:111], v[40:41], s[56:57], v[58:59] op_sel:[0,1,0] op_sel_hi:[1,1,1]\n\t"
"v_pk_fma_f32 v[112:113], v[40:41], s[56:57], v[58:59] op_sel:[0,1,0] op_sel_hi:[1,1,1] neg_lo:[1,0,0] neg_hi:[1,0,0]\n\t"
"v_pk_mul_f32 v[106:107], v[106:107], s[62:63] op_sel:[0,0] op_sel_hi:[1,0]\n\t"
"v_pk_mul_f32 v[108:109], v[108:109], s[62:63] op_sel:[0,1] op_sel_hi:[1,1]\n\t"
"v_pk_mul_f32 v[110:111], v[110:111], s[64:65] op_sel:[0,0] op_sel_hi:[1,0]\n\t"
"v_pk_mul_f32 v[112:113], v[112:113], s[64:65] op_sel:[0,1] op_sel_hi:[1,1]\n\t"
"v_cvt_pk_bf16_f32 v64, v104, v105\n\t"
"v_cvt_pk_bf16_f32 v84, v114, v115\n\t"
"v_cvt_pk_bf16_f32 v68, v106, v107\n\t"
"v_cvt_pk_bf16_f32 v72, v108, v109\n\t"
"v_cvt_pk_bf16_f32 v76, v110, v111\n\t"
"v_cvt_pk_bf16_f32 v80, v112, v113\n\t"
"s_waitcnt lgkmcnt(12)\n\t"
"v_pk_fma_f32 v[56:57], v[50:51], s[58:59], v[34:35] op_sel:[0,0,0] op_sel_hi:[1,0,1]\n\t"
"v_pk_fma_f32 v[58:59], v[50:51], s[58:59], v[34:35] op_sel:[0,1,0] op_sel_hi:[1,1,1]\n\t"
"v_pk_mul_f32 v[104:105], v[34:35], s[38:39] op_sel:[0,0] op_sel_hi:[1,0]\n\t"
"v_pk_mul_f32 v[114:115], v[50:51], s[38:39] op_sel:[0,1] op_sel_hi:[1,1]\n\t"
"v_pk_fma_f32 v[106:107], v[42:43], s[56:57], v[56:57] op_sel:[0,0,0] op_sel_hi:[1,0,1]\n\t"
"v_pk_fma_f32 v[108:109], v[42:43], s[56:57], v[56:57] op_sel:[0,0,0] op_sel_hi:[1,0,1] neg_lo:[1,0,0] neg_hi:[1,0,0]\n\t"
"v_pk_fma_f32 v[110:111], v[42:43], s[56:57], v[58:59] op_sel:[0,1,0] op_sel_hi:[1,1,1]\n\t"
"v_pk_fma_f32 v[112:113], v[42:43], s[56:57], v[58:59] op_sel:[0,1,0] op_sel_hi:[1,1,1] neg_lo:[1,0,0] neg_hi:[1,0,0]\n\t"
"v_pk_mul_f32 v[106:107], v[106:107], s[62:63] op_sel:[0,0] op_sel_hi:[1,0]\n\t"
"v_pk_mul_f32 v[108:109], v[108:109], s[62:63] op_sel:[0,1] op_sel_hi:[1,1]\n\t"
"v_pk_mul_f32 v[110:111], v[110:111], s[64:65] op_sel:[0,0] op_sel_hi:[1,0]\n\t"
"v_pk_mul_f32 v[112:113], v[112:113], s[64:65] op_sel:[0,1] op_sel_hi:[1,1]\n\t"
"v_cvt_pk_bf16_f32 v65, v104, v105\n\t"
"v_cvt_pk_bf16_f32 v85, v114, v115\n\t"
"v_cvt_pk_bf16_f32 v69, v106, v107\n\t"
"v_cvt_pk_bf16_f32 v73, v108, v109\n\t"
"v_cvt_pk_bf16_f32 v77, v110, v111\n\t"
"v_cvt_pk_bf16_f32 v81, v112, v113\n\t"
"s_waitcnt lgkmcnt(6)\n\t"
"v_pk_fma_f32 v[56:57], v[52:53], s[58:59], v[36:37] op_sel:[0,0,0] op_sel_hi:[1,0,1]\n\t"
"v_pk_fma_f32 v[58:59], v[52:53], s[58:59], v[36:37] op_sel:[0,1,0] op_sel_hi:[1,1,1]\n\t"
"v_pk_mul_f32 v[104:105], v[36:37], s[38:39] op_sel:[0,0] op_sel_hi:[1,0]\n\t"
"v_pk_mul_f32 v[114:115], v[52:53], s[38:39] op_sel:[0,1] op_sel_hi:[1,1]\n\t"
"v_pk_fma_f32 v[106:107], v[44:45], s[56:57], v[56:57] op_sel:[0,0,0] op_sel_hi:[1,0,1]\n\t"
"v_pk_fma_f32 v[108:109], v[44:45], s[56:57], v[56:57] op_sel:[0,0,0] op_sel_hi:[1,0,1] neg_lo:[1,0,0] neg_hi:[1,0,0]\n\t"
"v_pk_fma_f32 v[110:111], v[44:45], s[56:57], v[58:59] op_sel:[0,1,0] op_sel_hi:[1,1,1]\n\t"
"v_pk_fma_f32 v[112:113], v[44:45], s[56:57], v[58:59] op_sel:[0,1,0] op_sel_hi:[1,1,1] neg_lo:[1,0,0] neg_hi:[1,0,0]\n\t"
"v_pk_mul_f32 v[106:107], v[106:107], s[62:63] op_sel:[0,0] op_sel_hi:[1,0]\n\t"
"v_pk_mul_f32 v[108:109], v[108:109], s[62:63] op_sel:[0,1] op_sel_hi:[1,1]\n\t"
"v_pk_mul_f32 v[110:111], v[110:111], s[64:65] op_sel:[0,0] op_sel_hi:[1,0]\n\t"
"v_pk_mul_f32 v[112:113], v[112:113], s[64:65] op_sel:[0,1] op_sel_hi:[1,1]\n\t"
"v_cvt_pk_bf16_f32 v66, v104, v105\n\t"
"v_cvt_pk_bf16_f32 v86, v114, v115\n\t"
"v_cvt_pk_bf16_f32 v70, v106, v107\n\t"
"v_cvt_pk_bf16_f32 v74, v108, v109\n\t"
"v_cvt_pk_bf16_f32 v78, v110, v111\n\t"
"v_cvt_pk_bf16_f32 v82, v112, v113\n\t"
"s_waitcnt lgkmcnt(0)\n\t"
"v_pk_fma_f32 v[56:57], v[54:55], s[58:59], v[38:39] op_sel:[0,0,0] op_sel_hi:[1,0,1]\n\t"
"v_pk_fma_f32 v[58:59], v[54:55], s[58:59], v[38:39] op_sel:[0,1,0] op_sel_hi:[1,1,1]\n\t"
"v_pk_mul_f32 v[104:105], v[38:39], s[38:39] op_sel:[0,0] op_sel_hi:[1,0]\n\t"
"v_pk_mul_f32 v[114:115], v[54:55], s[38:39] op_sel:[0,1] op_sel_hi:[1,1]\n\t"
"v_pk_fma_f32 v[106:107], v[46:47], s[56:57], v[56:57] op_sel:[0,0,0] op_sel_hi:[1,0,1]\n\t"
"v_pk_fma_f32 v[108:109], v[46:47], s[56:57], v[56:57] op_sel:[0,0,0] op_sel_hi:[1,0,1] neg_lo:[1,0,0] neg_hi:[1,0,0]\n\t"
"v_pk_fma_f32 v[110:111], v[46:47], s[56:57], v[58:59] op_sel:[0,1,0] op_sel_hi:[1,1,1]\n\t"
"v_pk_fma_f32 v[112:113], v[46:47], s[56:57], v[58:59] op_sel:[0,1,0] op_sel_hi:[1,1,1] neg_lo:[1,0,0] neg_hi:[1,0,0]\n\t"
"v_pk_mul_f32 v[106:107], v[106:107], s[62:63] op_sel:[0,0] op_sel_hi:[1,0]\n\t"
"v_pk_mul_f32 v[108:109], v[108:109], s[62:63] op_sel:[0,1] op_sel_hi:[1,1]\n\t"
"v_pk_mul_f32 v[110:111], v[110:111], s[64:65] op_sel:[0,0] op_sel_hi:[1,0]\n\t"
"v_pk_mul_f32 v[112:113], v[112:113], s[64:65] op_sel:[0,1] op_sel_hi:[1,1]\n\t"
"v_cvt_pk_bf16_f32 v67, v104, v105\n\t"
"v_cvt_pk_bf16_f32 v87, v114, v115\n\t"
"v_cvt_pk_bf16_f32 v71, v106, v107\n\t"
"v_cvt_pk_bf16_f32 v75, v108, v109\n\t"
"v_cvt_pk_bf16_f32 v79, v110, v111\n\t"
"v_cvt_pk_bf16_f32 v83, v112, v113\n\t"
"s_add_u32 s60, s75, 0\n\t"
"buffer_store_dwordx4 v[64:67], v97, s[8:11], s60 offen sc1\n\t"
"s_add_u32 s60, s75, 184320\n\t"
"buffer_store_dwordx4 v[84:87], v97, s[8:11], s60 offen sc1\n\t"
"s_add_u32 s60, s75, 36864\n\t"
"buffer_store_dwordx4 v[68:71], v97, s[8:11], s60 offen sc1\n\t"
"s_add_u32 s60, s75, 73728\n\t"
"s_xor_b32 s60, s60, 256\n\t"
"buffer_store_dwordx4 v[72:75], v97, s[8:11], s60 offen sc1\n\t"
"s_add_u32 s60, s75, 110592\n\t"
"s_xor_b32 s60, s60, 512\n\t"
"buffer_store_dwordx4 v[76:79], v97, s[8:11], s60 offen sc1\n\t"
"s_add_u32 s60, s75, 147456\n\t"
"s_xor_b32 s60, s60, 768\n\t"
"buffer_store_dwordx4 v[80:83], v97, s[8:11], s60 offen sc1\n\t"
"s_waitcnt vmcnt(0)\n\t"
"s_add_u32 s69, s69, 1\n\t"
"s_cmp_lt_u32 s69, 216\n\t"
"s_cbranch_scc1 Lfb1_%=\n\t"
"s_mov_b32 s69, %2\n\t"
"s_mov_b64 vcc, 0\n\t"
"ds_read_b128 v[32:35], v255 offset:0\n\t"
"ds_read_b128 v[36:39], v255 offset:3072\n\t"
"ds_read_b128 v[40:43], v255 offset:6144\n\t"
"ds_read_b128 v[44:47], v255 offset:9216\n\t"
"ds_read_b128 v[48:51], v255 offset:12288\n\t"
"ds_read_b128 v[52:55], v255 offset:15360\n\t"
"ds_read_b128 v[56:59], v255 offset:18432\n\t"
"ds_read_b128 v[60:63], v255 offset:21504\n\t"
"ds_read_b128 v[64:67], v255 offset:24576\n\t"
"ds_read_b128 v[68:71], v255 offset:27648\n\t"
"ds_read_b128 v[72:75], v255 offset:30720\n\t"
"ds_read_b128 v[76:79], v255 offset:33792\n\t"
"ds_read_b128 v[80:83], v255 offset:36864\n\t"
"ds_read_b128 v[84:87], v255 offset:39936\n\t"
"ds_read_b128 v[88:91], v255 offset:43008\n\t"
"ds_read_b128 v[92:95], v255 offset:46080\n\t"
"ds_read_b128 v[96:99], v255 offset:49152\n\t"
"ds_read_b128 v[100:103], v255 offset:52224\n\t"
"ds_read_b128 v[104:107], v255 offset:55296\n\t"
"ds_read_b128 v[108:111], v255 offset:58368\n\t"
"ds_read_b128 v[112:115], v255 offset:61440\n\t"
"ds_read_b128 v[116:119], v255 offset:64512\n\t"
"s_waitcnt lgkmcnt(0)\n\t"
"s_branch Lgo_%=\n\t"
".p2align 12\n\t"
"Lpro_%=:\n\t"
"s_mul_i32 s25, s73, 19968\n\t"
"s_add_u32 s25, s25, 61440\n\t"
"s_bfe_u32 s41, %2, 0x40003\n\t"
"s_cmp_eq_u32 s41, 0\n\t"
"s_cselect_b32 s40, -1, 0xbfffffff\n\t"
"s_cmp_eq_u32 s41, 15\n\t"
"s_cselect_b32 s41, -1, 0xdfffffff\n\t"
"s_and_b32 s40, s40, s41\n\t"
"s_sub_u32 s42, s21, 384\n\t"
"s_mul_i32 s41, s73, 589824\n\t"
"s_add_u32 s42, s42, s41\n\t"
"s_and_b32 s41, s40, 536883584\n\t"
"s_and_b32 s40, s40, 2.0\n\t"
"v_bfe_u32 v26, v205, 2, 3\n\t"
"v_add_u32 v27, -1, v26\n\t"
"v_cmp_gt_u32 vcc, 6, v27\n\t"
"v_lshrrev_b32 v27, 1, v27\n\t"
"v_mul_u32_u24 v27, 196608, v27\n\t"
"v_and_b32 v20, 0x70, v28\n\t"
"v_add3_u32 v27, v27, v20, s42\n\t"
"v_bfe_i32 v20, v205, 2, 1\n\t"
"v_mov_b32 v21, s40\n\t"
"v_bfi_b32 v20, v20, s41, v21\n\t"
"v_add_u32 v27, v27, v20\n\t"
"v_bfrev_b32 v20, 8\n\t"
"v_cndmask_b32 v254, v20, v27, vcc\n\t"
"v_bfe_u32 v26, v205, 2, 3\n\t"
"v_bfe_u32 v27, v205, 5, 1\n\t"
"v_mul_u32_u24 v27, 3072, v27\n\t"
"v_lshl_add_u32 v27, v26, 7, v27\n\t"
"v_and_b32 v26, 6, v26\n\t"
"v_lshrrev_b32 v20, 4, v205\n\t"
"v_and_b32 v20, 4, v20\n\t"
"v_and_or_b32 v20, v205, 3, v20\n\t"
"v_xor_b32 v20, v20, v26\n\t"
"v_lshl_add_u32 v20, v20, 4, v27\n\t"
"v_lshrrev_b32 v21, 7, v205\n\t"
"v_lshl_add_u32 v20, v21, 10, v20\n\t"
"v_lshrrev_b32 v21, 5, v205\n\t"
"v_and_b32 v21, 6, v21\n\t"
"v_bfe_u32 v22, v205, 1, 1\n\t"
"v_or_b32 v21, v21, v22\n\t"
"v_xor_b32 v21, v21, v26\n\t"
"v_lshl_add_u32 v21, v21, 4, v27\n\t"
"v_and_b32 v22, 1, v205\n\t"
"v_lshl_add_u32 v21, v22, 3, v21\n\t"
"v_add_u32 v22, 142848, v20\n\t"
"v_add_u32 v23, 142848, v21\n\t"
"v_add_u32 v24, 82944, v20\n\t"
"v_add_u32 v25, 82944, v21\n\t"
"s_and_b32 s17, s17, 0xffff\n\t"
"s_mov_b32 s18, 100663296\n\t"
"s_mov_b32 s19, 0x20000\n\t"
"s_lshr_b32 s41, s73, 1\n\t"
"s_add_u32 s42, s21, 196608\n\t"
"s_mul_i32 s43, s41, 384\n\t"
"s_add_u32 s22, s42, s43\n\t"
"s_lshl1_add_u32 s40, s43, s22\n\t"
"s_mul_i32 s43, s41, 196608\n\t"
"s_add_u32 s23, s42, s43\n\t"
"s_mov_b32 s44, 0x0fff0fff\n\t"
"s_mov_b32 s45, 0x0fff0fff\n\t"
"s_mul_i32 s26, s41, 19968\n\t"
"s_add_u32 s26, s26, 141312\n\t"
"s_sub_u32 s43, s41, s73\n\t"
"s_add_u32 s43, s43, 4\n\t"
"s_mul_i32 s27, s43, 589824\n\t"
"s_bitcmp1_b32 s73, 0\n\t"
"s_mov_b32 s36, 0x0fff0000\n\t"
"s_cselect_b32 s36, 0xfff0, s36\n\t"
"s_mov_b32 s37, s36\n\t"
"s_mul_i32 s80, s73, 3072\n\t"
"s_add_u32 s80, s80, s8\n\t"
"s_addc_u32 s81, s9, 0x100000\n\t"
"s_mov_b32 s82, 0x1c0000\n\t"
"s_mov_b32 s83, 0x800000\n\t"
"s_add_u32 m0, s59, 148992\n\t"
"s_add_u32 s57, s58, 147456\n\t"
"buffer_load_dwordx4 v29, s[52:55], s57 offen offset:0 lds\n\t"
"buffer_load_dwordx4 v29, s[52:55], s57 offen offset:3072 lds\n\t"
"s_add_u32 m0, s59, 155136\n\t"
"s_add_u32 s57, s58, 153600\n\t"
"buffer_load_dwordx4 v29, s[52:55], s57 offen offset:0 lds\n\t"
"buffer_load_dwordx4 v29, s[52:55], s57 offen offset:3072 lds\n\t"
"s_add_u32 m0, s59, 0\n\t"
"s_add_u32 s57, s58, 368640\n\t"
"buffer_load_dwordx4 v29, s[52:55], s57 offen offset:0 lds\n\t"
"buffer_load_dwordx4 v29, s[52:55], s57 offen offset:3072 lds\n\t"
"s_add_u32 m0, s59, 6144\n\t"
"s_add_u32 s57, s58, 374784\n\t"
"buffer_load_dwordx4 v29, s[52:55], s57 offen offset:0 lds\n\t"
"buffer_load_dwordx4 v29, s[52:55], s57 offen offset:3072 lds\n\t"
"s_add_u32 m0, s59, 12288\n\t"
"s_add_u32 s57, s58, 589824\n\t"
"buffer_load_dwordx4 v29, s[52:55], s57 offen offset:0 lds\n\t"
"buffer_load_dwordx4 v29, s[52:55], s57 offen offset:3072 lds\n\t"
"s_add_u32 m0, s59, 18432\n\t"
"s_add_u32 s57, s58, 595968\n\t"
"buffer_load_dwordx4 v29, s[52:55], s57 offen offset:0 lds\n\t"
"buffer_load_dwordx4 v29, s[52:55], s57 offen offset:3072 lds\n\t"
"s_add_u32 m0, s25, 0\n\t"
"s_nop 0\n\t"
"buffer_load_dwordx4 v254, s[12:15], 0 offen nt lds\n\t"
"s_cmp_eq_u32 s73, 3\n\t"
"s_cselect_b32 s62, -1, 1\n\t"
"s_mul_i32 s63, s62, 59904\n\t"
"s_mul_i32 s62, s62, 1769472\n\t"
"v_add_u32 v19, s62, v254\n\t"
"s_add_u32 m0, s25, s63\n\t"
"s_nop 0\n\t"
"buffer_load_dwordx4 v19, s[12:15], 0 offen nt lds\n\t"
"s_add_u32 m0, s24, 59904\n\t"
"s_add_u32 s28, s21, 1769472\n\t"
"buffer_load_dwordx4 v28, s[12:15], s28 offen offset:0 nt lds\n\t"
"s_add_u32 m0, s24, 66048\n\t"
"s_add_u32 s28, s21, 1966080\n\t"
"buffer_load_dwordx4 v28, s[12:15], s28 offen offset:0 nt lds\n\t"
"s_add_u32 m0, s24, 72192\n\t"
"s_add_u32 s28, s21, 2162688\n\t"
"buffer_load_dwordx4 v28, s[12:15], s28 offen offset:0 nt lds\n\t"
"s_mul_i32 s60, s59, 6\n\t"
"s_mul_i32 s61, s59, 192\n\t"
"s_add_u32 s61, s61, s21\n\t"
"s_mul_i32 s62, s73, 3072\n\t"
"v_subrev_u32 v30, s62, v28\n\t"
"s_add_u32 m0, s60, 102912\n\t"
"s_add_u32 s28, s61, 1769472\n\t"
"buffer_load_dwordx4 v30, s[12:15], s28 offen nt lds\n\t"
"s_add_u32 m0, s24, 79872\n\t"
"s_add_u32 s28, s21, 2359296\n\t"
"buffer_load_dwordx4 v28, s[12:15], s28 offen offset:0 nt lds\n\t"
"s_add_u32 m0, s24, 86016\n\t"
"s_add_u32 s28, s21, 2555904\n\t"
"buffer_load_dwordx4 v28, s[12:15], s28 offen offset:0 nt lds\n\t"
"s_add_u32 m0, s24, 92160\n\t"
"s_add_u32 s28, s21, 2752512\n\t"
"buffer_load_dwordx4 v28, s[12:15], s28 offen offset:0 nt lds\n\t"
"s_add_u32 m0, s60, 122880\n\t"
"s_add_u32 s28, s61, 2359296\n\t"
"buffer_load_dwordx4 v30, s[12:15], s28 offen nt lds\n\t"
"s_add_u32 m0, s24, 19968\n\t"
"s_add_u32 s28, s21, 589824\n\t"
"buffer_load_dwordx4 v28, s[12:15], s28 offen offset:0 nt lds\n\t"
"s_add_u32 m0, s24, 26112\n\t"
"s_add_u32 s28, s21, 786432\n\t"
"buffer_load_dwordx4 v28, s[12:15], s28 offen offset:0 nt lds\n\t"
"s_add_u32 m0, s24, 32256\n\t"
"s_add_u32 s28, s21, 983040\n\t"
"buffer_load_dwordx4 v28, s[12:15], s28 offen offset:0 nt lds\n\t"
"s_add_u32 m0, s60, 62976\n\t"
"s_add_u32 s28, s61, 589824\n\t"
"buffer_load_dwordx4 v30, s[12:15], s28 offen nt lds\n\t"
"s_add_u32 m0, s24, 0\n\t"
"s_add_u32 s28, s21, 0\n\t"
"buffer_load_dwordx4 v28, s[12:15], s28 offen offset:0 nt lds\n\t"
"s_add_u32 m0, s24, 6144\n\t"
"s_add_u32 s28, s21, 196608\n\t"
"buffer_load_dwordx4 v28, s[12:15], s28 offen offset:0 nt lds\n\t"
"s_add_u32 m0, s24, 12288\n\t"
"s_add_u32 s28, s21, 393216\n\t"
"buffer_load_dwordx4 v28, s[12:15], s28 offen offset:0 nt lds\n\t"
"s_add_u32 m0, s60, 43008\n\t"
"s_add_u32 s28, s61, 0\n\t"
"buffer_load_dwordx4 v30, s[12:15], s28 offen nt lds\n\t"
"s_waitcnt vmcnt(42)\n\t"
"Ljoin_%=:\n\t"
"s_mov_b32 s41, 0xc0423f90\n\t"
"s_movk_i32 s43, 0x101\n\t"
"v_mov_b32 v6, 0x01000100\n\t"
"v_mov_b32 v7, 0xbebd0bd1\n\t"
"v_lshrrev_b32 v240, 16, v244\n\t"
"v_lshrrev_b32 v242, 16, v245\n\t"
"v_xor_b32 v241, 32, v240\n\t"
"v_and_b32 v244, 0xffff, v244\n\t"
"v_and_b32 v245, 0xffff, v245\n\t"
"v_cmp_le_u32 vcc, 9216, v242\n\t"
"v_mov_b32 v243, 0x3ea80a81\n\t"
"v_cndmask_b32 v243, v243, v7, vcc\n\t"
"v_and_b32 v8, 0xf000f, v247\n\t"
"v_mad_u32_u24 v8, v8, s43, v6\n\t"
"v_perm_b32 v196, s41, 0, v8\n\t"
"v_lshrrev_b32 v9, 4, v247\n\t"
"v_and_b32 v9, 0xf000f, v9\n\t"
"v_mad_u32_u24 v9, v9, s43, v6\n\t"
"v_perm_b32 v197, s41, 0, v9\n\t"
"v_lshrrev_b32 v10, 8, v247\n\t"
"v_and_b32 v10, 0xf000f, v10\n\t"
"v_mad_u32_u24 v10, v10, s43, v6\n\t"
"v_perm_b32 v198, s41, 0, v10\n\t"
"v_lshrrev_b32 v11, 12, v247\n\t"
"v_and_b32 v11, 0xf000f, v11\n\t"
"v_mad_u32_u24 v11, v11, s43, v6\n\t"
"v_perm_b32 v199, s41, 0, v11\n\t"
"v_mov_b32 v247, v28\n\t"
"s_mov_b64 vcc, -1\n\t"
"s_bitcmp1_b32 s73, 1\n\t"
"s_cbranch_scc1 Lvh0_%=\n\t"
"s_bitcmp1_b32 s73, 0\n\t"
"s_cbranch_scc1 Lpg2_%=\n\t"
"s_branch Lpg1_%=\n\t"
"Lvh0_%=:\n\t"
"s_bitcmp1_b32 s73, 0\n\t"
"s_cbranch_scc1 Lpg4_%=\n\t"
"s_branch Lpg3_%=\n\t"
"Lvr0_%=:\n\t"
"s_mov_b64 vcc, 0\n\t"
"v_add_u32 v240, 59904, v240\n\t"
"v_add_u32 v241, 59904, v241\n\t"
"s_cmp_lt_u32 s69, 216\n\t"
"s_cbranch_scc0 Lna1_%=\n\t"
"s_waitcnt vmcnt(0)\n\t"
"s_add_u32 s71, s71, 1\n\t"
"v_mov_b32 v31, s71\n\t"
"s_mov_b64 exec, 1\n\t"
"buffer_store_dword v31, off, s[8:11], s70 sc1\n\t"
"s_mov_b64 exec, -1\n\t"
"Lna1_%=:\n\t"
"s_cmp_eq_u32 s73, 0\n\t"
"s_cbranch_scc1 Lsk0_%=\n\t"
"s_lshr_b32 s56, %2, 3\n\t"
"s_bitcmp1_b32 0xaaaaaaaa, s56\n\t"
"s_cbranch_scc1 Lkc2_%=\n\t"
"s_waitcnt vmcnt(8)\n\t"
"s_branch Lke2_%=\n\t"
"Lkc2_%=:\n\t"
"s_waitcnt vmcnt(32)\n\t"
"Lke2_%=:\n\t"
"Lsk0_%=:\n\t"
"s_barrier\n\t"
"ds_read_b64_tr_b16 v[120:121], v240 offset:0\n\t"
"ds_read_b64_tr_b16 v[122:123], v240 offset:1536\n\t"
"ds_read_b64_tr_b16 v[124:125], v241 offset:0\n\t"
"ds_read_b64_tr_b16 v[126:127], v241 offset:1536\n\t"
"ds_read_b64_tr_b16 v[128:129], v240 offset:19968\n\t"
"ds_read_b64_tr_b16 v[130:131], v240 offset:21504\n\t"
"ds_read_b64_tr_b16 v[132:133], v241 offset:19968\n\t"
"ds_read_b64_tr_b16 v[134:135], v241 offset:21504\n\t"
"s_waitcnt lgkmcnt(6)\n\t"
"v_mfma_f32_16x16x32_bf16 v[120:123], v[120:123], v[196:199], 0\n\t"
"s_waitcnt lgkmcnt(4)\n\t"
"v_mfma_f32_16x16x32_bf16 v[124:127], v[124:127], v[196:199], 0\n\t"
"s_waitcnt lgkmcnt(2)\n\t"
"v_mfma_f32_16x16x32_bf16 v[128:131], v[128:131], v[196:199], 0\n\t"
"s_waitcnt lgkmcnt(0)\n\t"
"v_mfma_f32_16x16x32_bf16 v[132:135], v[132:135], v[196:199], 0\n\t"
"v_pk_mul_f32 v[120:121], v[120:121], v[242:243] op_sel:[0,1] op_sel_hi:[1,1]\n\t"
"v_pk_mul_f32 v[122:123], v[122:123], v[242:243] op_sel:[0,1] op_sel_hi:[1,1]\n\t"
"v_cvt_pk_bf16_f32 v120, v120, v121\n\t"
"v_cvt_pk_bf16_f32 v121, v122, v123\n\t"
"v_pk_mul_f32 v[124:125], v[124:125], v[242:243] op_sel:[0,1] op_sel_hi:[1,1]\n\t"
"v_pk_mul_f32 v[126:127], v[126:127], v[242:243] op_sel:[0,1] op_sel_hi:[1,1]\n\t"
"v_cvt_pk_bf16_f32 v122, v124, v125\n\t"
"v_cvt_pk_bf16_f32 v123, v126, v127\n\t"
"v_pk_mul_f32 v[128:129], v[128:129], v[242:243] op_sel:[0,1] op_sel_hi:[1,1]\n\t"
"v_pk_mul_f32 v[130:131], v[130:131], v[242:243] op_sel:[0,1] op_sel_hi:[1,1]\n\t"
"v_cvt_pk_bf16_f32 v128, v128, v129\n\t"
"v_cvt_pk_bf16_f32 v129, v130, v131\n\t"
"v_pk_mul_f32 v[132:133], v[132:133], v[242:243] op_sel:[0,1] op_sel_hi:[1,1]\n\t"
"v_pk_mul_f32 v[134:135], v[134:135], v[242:243] op_sel:[0,1] op_sel_hi:[1,1]\n\t"
"v_cvt_pk_bf16_f32 v130, v132, v133\n\t"
"v_cvt_pk_bf16_f32 v131, v134, v135\n\t"
"s_cmp_eq_u32 s73, 0\n\t"
"s_cbranch_scc1 Lsk1_%=\n\t"
"s_lshr_b32 s56, %2, 3\n\t"
"s_bitcmp1_b32 0xaaaaaaaa, s56\n\t"
"s_cbranch_scc1 Lkc3_%=\n\t"
"s_branch Lke3_%=\n\t"
"Lkc3_%=:\n\t"
"s_waitcnt vmcnt(8)\n\t"
"Lke3_%=:\n\t"
"Lsk1_%=:\n\t"
"s_barrier\n\t"
"ds_read_b64_tr_b16 a[0:1], v20 offset:24576\n\t"
"ds_read_b64_tr_b16 a[2:3], v20 offset:30720\n\t"
"ds_read_b64_tr_b16 a[24:25], v20 offset:24584\n\t"
"ds_read_b64_tr_b16 a[26:27], v20 offset:30728\n\t"
"ds_read_b64_tr_b16 a[48:49], v21 offset:26624\n\t"
"ds_read_b64_tr_b16 a[50:51], v21 offset:32768\n\t"
"ds_read_b64_tr_b16 a[8:9], v20 offset:36864\n\t"
"ds_read_b64_tr_b16 a[10:11], v24 offset:0\n\t"
"ds_read_b64_tr_b16 a[32:33], v20 offset:36872\n\t"
"ds_read_b64_tr_b16 a[34:35], v24 offset:8\n\t"
"ds_read_b64_tr_b16 a[56:57], v21 offset:38912\n\t"
"ds_read_b64_tr_b16 a[58:59], v25 offset:2048\n\t"
"ds_read_b64_tr_b16 a[16:17], v24 offset:6144\n\t"
"ds_read_b64_tr_b16 a[18:19], v22 offset:0\n\t"
"ds_read_b64_tr_b16 a[40:41], v24 offset:6152\n\t"
"ds_read_b64_tr_b16 a[42:43], v22 offset:8\n\t"
"ds_read_b64_tr_b16 a[64:65], v25 offset:8192\n\t"
"ds_read_b64_tr_b16 a[66:67], v23 offset:2048\n\t"
"ds_read_b64_tr_b16 a[72:73], v22 offset:6144\n\t"
"ds_read_b64_tr_b16 a[74:75], v22 offset:12288\n\t"
"ds_read_b64_tr_b16 a[96:97], v22 offset:6152\n\t"
"ds_read_b64_tr_b16 a[98:99], v22 offset:12296\n\t"
"ds_read_b64_tr_b16 a[120:121], v23 offset:8192\n\t"
"ds_read_b64_tr_b16 a[122:123], v23 offset:14336\n\t"
"s_waitcnt lgkmcnt(0)\n\t"
"s_barrier\n\t"
"s_cmp_eq_u32 s73, 0\n\t"
"s_cbranch_scc1 Lsk2_%=\n\t"
"s_add_u32 m0, s24, 39936\n\t"
"s_add_u32 s28, s21, 1179648\n\t"
"buffer_load_dwordx4 v28, s[12:15], s28 offen offset:0 nt lds\n\t"
"s_add_u32 m0, s24, 46080\n\t"
"s_add_u32 s28, s21, 1376256\n\t"
"buffer_load_dwordx4 v28, s[12:15], s28 offen offset:0 nt lds\n\t"
"s_add_u32 m0, s24, 52224\n\t"
"s_add_u32 s28, s21, 1572864\n\t"
"buffer_load_dwordx4 v28, s[12:15], s28 offen offset:0 nt lds\n\t"
"s_add_u32 m0, s60, 82944\n\t"
"s_add_u32 s28, s61, 1179648\n\t"
"buffer_load_dwordx4 v30, s[12:15], s28 offen nt lds\n\t"
"Lsk2_%=:\n\t"
"ds_read_b64_tr_b16 a[80:81], v20 offset:0\n\t"
"ds_read_b64_tr_b16 a[82:83], v20 offset:6144\n\t"
"ds_read_b64_tr_b16 a[104:105], v20 offset:8\n\t"
"ds_read_b64_tr_b16 a[106:107], v20 offset:6152\n\t"
"ds_read_b64_tr_b16 a[128:129], v21 offset:2048\n\t"
"ds_read_b64_tr_b16 a[130:131], v21 offset:8192\n\t"
"ds_read_b64_tr_b16 a[88:89], v20 offset:12288\n\t"
"ds_read_b64_tr_b16 a[90:91], v20 offset:18432\n\t"
"ds_read_b64_tr_b16 a[112:113], v20 offset:12296\n\t"
"ds_read_b64_tr_b16 a[114:115], v20 offset:18440\n\t"
"ds_read_b64_tr_b16 a[136:137], v21 offset:14336\n\t"
"ds_read_b64_tr_b16 a[138:139], v21 offset:20480\n\t"
"ds_write_b128 v242, v[120:123] offset:29184\n\t"
"ds_write_b128 v242, v[128:131] offset:30720\n\t"
"s_waitcnt lgkmcnt(0)\n\t"
"v_subrev_u32 v240, 59904, v240\n\t"
"v_subrev_u32 v241, 59904, v241\n\t"
"s_cmp_eq_u32 s73, 0\n\t"
"s_cbranch_scc1 Lsk3_%=\n\t"
"s_waitcnt vmcnt(4)\n\t"
"Lsk3_%=:\n\t"
"s_barrier\n\t"
"ds_read_b64_tr_b16 v[136:137], v240 offset:19968\n\t"
"ds_read_b64_tr_b16 v[138:139], v240 offset:21504\n\t"
"ds_read_b64_tr_b16 v[140:141], v241 offset:19968\n\t"
"ds_read_b64_tr_b16 v[142:143], v241 offset:21504\n\t"
"ds_read_b64_tr_b16 v[144:145], v240 offset:0\n\t"
"ds_read_b64_tr_b16 v[146:147], v240 offset:1536\n\t"
"ds_read_b64_tr_b16 v[148:149], v241 offset:0\n\t"
"ds_read_b64_tr_b16 v[150:151], v241 offset:1536\n\t"
"s_cmp_eq_u32 s73, 0\n\t"
"s_cbranch_scc1 Lsk4_%=\n\t"
"s_add_u32 m0, s24, 99840\n\t"
"s_add_u32 s28, s21, 2949120\n\t"
"buffer_load_dwordx4 v28, s[12:15], s28 offen offset:0 nt lds\n\t"
"s_add_u32 m0, s24, 105984\n\t"
"s_add_u32 s28, s21, 3145728\n\t"
"buffer_load_dwordx4 v28, s[12:15], s28 offen offset:0 nt lds\n\t"
"s_add_u32 m0, s24, 112128\n\t"
"s_add_u32 s28, s21, 3342336\n\t"
"buffer_load_dwordx4 v28, s[12:15], s28 offen offset:0 nt lds\n\t"
"s_add_u32 m0, s60, 142848\n\t"
"s_add_u32 s28, s61, 2949120\n\t"
"buffer_load_dwordx4 v30, s[12:15], s28 offen nt lds\n\t"
"Lsk4_%=:\n\t"
"s_waitcnt lgkmcnt(6)\n\t"
"v_mfma_f32_16x16x32_bf16 v[136:139], v[136:139], v[196:199], 0\n\t"
"s_waitcnt lgkmcnt(4)\n\t"
"v_mfma_f32_16x16x32_bf16 v[140:143], v[140:143], v[196:199], 0\n\t"
"s_waitcnt lgkmcnt(2)\n\t"
"v_mfma_f32_16x16x32_bf16 v[144:147], v[144:147], v[196:199], 0\n\t"
"s_waitcnt lgkmcnt(0)\n\t"
"v_mfma_f32_16x16x32_bf16 v[148:151], v[148:151], v[196:199], 0\n\t"
"v_pk_mul_f32 v[136:137], v[136:137], v[242:243] op_sel:[0,1] op_sel_hi:[1,1]\n\t"
"v_pk_mul_f32 v[138:139], v[138:139], v[242:243] op_sel:[0,1] op_sel_hi:[1,1]\n\t"
"v_cvt_pk_bf16_f32 v136, v136, v137\n\t"
"v_cvt_pk_bf16_f32 v137, v138, v139\n\t"
"v_pk_mul_f32 v[140:141], v[140:141], v[242:243] op_sel:[0,1] op_sel_hi:[1,1]\n\t"
"v_pk_mul_f32 v[142:143], v[142:143], v[242:243] op_sel:[0,1] op_sel_hi:[1,1]\n\t"
"v_cvt_pk_bf16_f32 v138, v140, v141\n\t"
"v_cvt_pk_bf16_f32 v139, v142, v143\n\t"
"ds_write_b128 v242, v[136:139] offset:26112\n\t"
"v_pk_mul_f32 v[144:145], v[144:145], v[242:243] op_sel:[0,1] op_sel_hi:[1,1]\n\t"
"v_pk_mul_f32 v[146:147], v[146:147], v[242:243] op_sel:[0,1] op_sel_hi:[1,1]\n\t"
"v_cvt_pk_bf16_f32 v144, v144, v145\n\t"
"v_cvt_pk_bf16_f32 v145, v146, v147\n\t"
"v_pk_mul_f32 v[148:149], v[148:149], v[242:243] op_sel:[0,1] op_sel_hi:[1,1]\n\t"
"v_pk_mul_f32 v[150:151], v[150:151], v[242:243] op_sel:[0,1] op_sel_hi:[1,1]\n\t"
"v_cvt_pk_bf16_f32 v146, v148, v149\n\t"
"v_cvt_pk_bf16_f32 v147, v150, v151\n\t"
"ds_write_b128 v242, v[144:147] offset:24576\n\t"
"s_mov_b64 vcc, 0\n\t"
"s_waitcnt lgkmcnt(0)\n\t"
"s_barrier\n\t"
"ds_read_b128 a[168:171], v244 offset:26112\n\t"
"ds_read_b128 a[172:175], v244 offset:26624\n\t"
"ds_read_b128 a[176:179], v244 offset:27136\n\t"
"ds_read_b128 a[180:183], v244 offset:35328\n\t"
"ds_read_b128 a[184:187], v244 offset:35840\n\t"
"ds_read_b128 a[188:191], v244 offset:36352\n\t"
"ds_read_b128 a[192:195], v244 offset:25600\n\t"
"s_waitcnt lgkmcnt(4)\n\t"
"v_mfma_f32_16x16x32_bf16 v[14:17], a[0:3], a[168:171], 0\n\t"
"ds_read_b64_tr_b16 v[224:225], v240 offset:512\n\t"
"v_mfma_f32_16x16x32_bf16 v[14:17], a[8:11], a[172:175], v[14:17]\n\t"
"ds_read_b64_tr_b16 v[226:227], v240 offset:2048\n\t"
"v_mfma_f32_16x16x32_bf16 v[14:17], a[16:19], a[176:179], v[14:17]\n\t"
"ds_read_b64_tr_b16 v[228:229], v241 offset:512\n\t"
"v_mfma_f32_16x16x32_bf16 v[46:49], a[32:35], a[172:175], 0\n\t"
"ds_read_b64_tr_b16 v[230:231], v241 offset:2048\n\t"
"v_mfma_f32_16x16x32_bf16 v[46:49], a[40:43], a[176:179], v[46:49]\n\t"
"v_mfma_f32_16x16x32_bf16 v[46:49], a[24:27], a[168:171], v[46:49]\n\t"
"v_mfma_f32_16x16x32_bf16 v[78:81], a[48:51], a[168:171], 0\n\t"
"s_waitcnt lgkmcnt(5)\n\t"
"v_mfma_f32_16x16x32_bf16 v[78:81], a[64:67], a[176:179], v[78:81]\n\t"
"ds_read_b64_tr_b16 v[232:233], v240 offset:20480\n\t"
"v_mfma_f32_16x16x32_bf16 v[78:81], a[56:59], a[172:175], v[78:81]\n\t"
"ds_read_b64_tr_b16 v[234:235], v240 offset:22016\n\t"
"v_mfma_f32_16x16x32_bf16 v[112:115], a[72:75], a[180:183], 0\n\t"
"s_waitcnt lgkmcnt(4)\n\t"
"ds_read_b64_tr_b16 v[236:237], v241 offset:20480\n\t"
"v_mfma_f32_16x16x32_bf16 v[112:115], a[80:83], a[184:187], v[112:115]\n\t"
"s_waitcnt lgkmcnt(3)\n\t"
"ds_read_b64_tr_b16 v[238:239], v241 offset:22016\n\t"
"v_mfma_f32_16x16x32_bf16 v[112:115], a[88:91], a[188:191], v[112:115]\n\t"
"ds_read_b128 a[176:179], v244 offset:34816\n\t"
"v_mfma_f32_16x16x32_bf16 v[224:227], v[224:227], v[196:199], 0\n\t"
"v_mfma_f32_16x16x32_bf16 v[228:231], v[228:231], v[196:199], 0\n\t"
"v_mfma_f32_16x16x32_bf16 v[144:147], a[112:115], a[188:191], 0\n\t"
"v_mfma_f32_16x16x32_bf16 v[144:147], a[96:99], a[180:183], v[144:147]\n\t"
"v_mfma_f32_16x16x32_bf16 v[144:147], a[104:107], a[184:187], v[144:147]\n\t"
"v_mfma_f32_16x16x32_bf16 v[176:179], a[120:123], a[180:183], 0\n\t"
"v_cvt_pk_bf16_f32 v224, v224, v225\n\t"
"v_cvt_pk_bf16_f32 v225, v226, v227\n\t"
"v_mfma_f32_16x16x32_bf16 v[176:179], a[128:131], a[184:187], v[176:179]\n\t"
"ds_read_b128 a[196:199], v244 offset:25088\n\t"
"v_cvt_pk_bf16_f32 v226, v228, v229\n\t"
"v_mfma_f32_16x16x32_bf16 v[176:179], a[136:139], a[188:191], v[176:179]\n\t"
"v_cvt_pk_bf16_f32 v227, v230, v231\n\t"
"v_mfma_f32_16x16x32_bf16 v[10:13], a[0:3], a[192:195], 0\n\t"
"s_waitcnt lgkmcnt(4)\n\t"
"v_mfma_f32_16x16x32_bf16 v[10:13], a[16:19], a[172:175], v[10:13]\n\t"
"s_waitcnt lgkmcnt(2)\n\t"
"v_mfma_f32_16x16x32_bf16 v[10:13], a[8:11], a[168:171], v[10:13]\n\t"
"v_mfma_f32_16x16x32_bf16 v[232:235], v[232:235], v[196:199], 0\n\t"
"v_mfma_f32_16x16x32_bf16 v[236:239], v[236:239], v[196:199], 0\n\t"
"s_waitcnt vmcnt(4)\n\t"
"s_barrier\n\t"
"v_mfma_f32_16x16x32_bf16 v[42:45], a[32:35], a[168:171], 0\n\t"
"ds_read_b64_tr_b16 v[228:229], v240 offset:39936\n\t"
"v_mfma_f32_16x16x32_bf16 v[42:45], a[24:27], a[192:195], v[42:45]\n\t"
"ds_read_b64_tr_b16 v[230:231], v240 offset:41472\n\t"
"v_mfma_f32_16x16x32_bf16 v[42:45], a[40:43], a[172:175], v[42:45]\n\t"
"ds_write_b128 v242, v[224:227] offset:0\n\t"
"v_mfma_f32_16x16x32_bf16 v[74:77], a[48:51], a[192:195], 0\n\t"
"s_waitcnt lgkmcnt(4)\n\t"
"v_cvt_pk_bf16_f32 v232, v232, v233\n\t"
"v_cvt_pk_bf16_f32 v233, v234, v235\n\t"
"v_mfma_f32_16x16x32_bf16 v[74:77], a[64:67], a[172:175], v[74:77]\n\t"
"ds_read_b64_tr_b16 v[224:225], v241 offset:39936\n\t"
"v_mfma_f32_16x16x32_bf16 v[74:77], a[56:59], a[168:171], v[74:77]\n\t"
"ds_read_b64_tr_b16 v[226:227], v241 offset:41472\n\t"
"v_cvt_pk_bf16_f32 v234, v236, v237\n\t"
"v_mfma_f32_16x16x32_bf16 v[108:111], a[72:75], a[176:179], 0\n\t"
"s_waitcnt lgkmcnt(3)\n\t"
"ds_read_b128 a[172:175], v244 offset:34304\n\t"
"v_cvt_pk_bf16_f32 v235, v238, v239\n\t"
"v_mfma_f32_16x16x32_bf16 v[108:111], a[88:91], a[184:187], v[108:111]\n\t"
"ds_read_b64_tr_b16 v[236:237], v240 offset:40448\n\t"
"v_mfma_f32_16x16x32_bf16 v[108:111], a[80:83], a[180:183], v[108:111]\n\t"
"ds_write_b128 v242, v[232:235] offset:1536\n\t"
"v_mfma_f32_16x16x32_bf16 v[228:231], v[228:231], v[196:199], 0\n\t"
"s_waitcnt lgkmcnt(3)\n\t"
"v_mfma_f32_16x16x32_bf16 v[224:227], v[224:227], v[196:199], 0\n\t"
"ds_read_b64_tr_b16 v[238:239], v240 offset:41984\n\t"
"v_mfma_f32_16x16x32_bf16 v[140:143], a[104:107], a[180:183], 0\n\t"
"ds_read_b64_tr_b16 v[232:233], v241 offset:40448\n\t"
"v_mfma_f32_16x16x32_bf16 v[140:143], a[96:99], a[176:179], v[140:143]\n\t"
"ds_read_b64_tr_b16 v[234:235], v241 offset:41984\n\t"
"v_mfma_f32_16x16x32_bf16 v[140:143], a[112:115], a[184:187], v[140:143]\n\t"
"v_pk_mul_f32 v[228:229], v[228:229], v[242:243] op_sel:[0,1] op_sel_hi:[1,1]\n\t"
"v_pk_mul_f32 v[230:231], v[230:231], v[242:243] op_sel:[0,1] op_sel_hi:[1,1]\n\t"
"v_mfma_f32_16x16x32_bf16 v[172:175], a[120:123], a[176:179], 0\n\t"
"v_cvt_pk_bf16_f32 v228, v228, v229\n\t"
"v_cvt_pk_bf16_f32 v229, v230, v231\n\t"
"v_mfma_f32_16x16x32_bf16 v[172:175], a[136:139], a[184:187], v[172:175]\n\t"
"v_mfma_f32_16x16x32_bf16 v[172:175], a[128:131], a[180:183], v[172:175]\n\t"
"v_pk_mul_f32 v[224:225], v[224:225], v[242:243] op_sel:[0,1] op_sel_hi:[1,1]\n\t"
"v_pk_mul_f32 v[226:227], v[226:227], v[242:243] op_sel:[0,1] op_sel_hi:[1,1]\n\t"
"v_mfma_f32_16x16x32_bf16 v[6:9], a[0:3], a[196:199], 0\n\t"
"v_cvt_pk_bf16_f32 v230, v224, v225\n\t"
"v_cvt_pk_bf16_f32 v231, v226, v227\n\t"
"v_mfma_f32_16x16x32_bf16 v[6:9], a[8:11], a[192:195], v[6:9]\n\t"
"v_add_u32 v240, 59904, v240\n\t"
"v_add_u32 v241, 59904, v241\n\t"
"v_mfma_f32_16x16x32_bf16 v[6:9], a[16:19], a[168:171], v[6:9]\n\t"
"ds_write_b128 v242, v[228:231] offset:27648\n\t"
"v_mfma_f32_16x16x32_bf16 v[38:41], a[32:35], a[192:195], 0\n\t"
"s_waitcnt lgkmcnt(3)\n\t"
"v_mfma_f32_16x16x32_bf16 v[38:41], a[40:43], a[168:171], v[38:41]\n\t"
"s_waitcnt lgkmcnt(1)\n\t"
"ds_read_b64_tr_b16 v[224:225], v240 offset:512\n\t"
"v_mfma_f32_16x16x32_bf16 v[38:41], a[24:27], a[196:199], v[38:41]\n\t"
"ds_read_b64_tr_b16 v[226:227], v240 offset:2048\n\t"
"v_mfma_f32_16x16x32_bf16 v[236:239], v[236:239], v[196:199], 0\n\t"
"ds_read_b64_tr_b16 v[228:229], v241 offset:512\n\t"
"v_mfma_f32_16x16x32_bf16 v[232:235], v[232:235], v[196:199], 0\n\t"
"ds_read_b64_tr_b16 v[230:231], v241 offset:2048\n\t"
"v_mfma_f32_16x16x32_bf16 v[70:73], a[48:51], a[196:199], 0\n\t"
"v_mfma_f32_16x16x32_bf16 v[70:73], a[56:59], a[192:195], v[70:73]\n\t"
"v_mfma_f32_16x16x32_bf16 v[70:73], a[64:67], a[168:171], v[70:73]\n\t"
"v_mfma_f32_16x16x32_bf16 v[104:107], a[72:75], a[172:175], 0\n\t"
"v_cvt_pk_bf16_f32 v236, v236, v237\n\t"
"v_cvt_pk_bf16_f32 v237, v238, v239\n\t"
"v_mfma_f32_16x16x32_bf16 v[104:107], a[80:83], a[176:179], v[104:107]\n\t"
"v_cvt_pk_bf16_f32 v238, v232, v233\n\t"
"v_mfma_f32_16x16x32_bf16 v[104:107], a[88:91], a[180:183], v[104:107]\n\t"
"s_waitcnt lgkmcnt(4)\n\t"
"s_barrier\n\t"
"v_mfma_f32_16x16x32_bf16 v[136:139], a[104:107], a[176:179], 0\n\t"
"ds_read_b128 a[168:171], v244 offset:24576\n\t"
"v_mfma_f32_16x16x32_bf16 v[136:139], a[112:115], a[180:183], v[136:139]\n\t"
"ds_read_b128 a[184:187], v244 offset:33792\n\t"
"v_cvt_pk_bf16_f32 v239, v234, v235\n\t"
"v_mfma_f32_16x16x32_bf16 v[136:139], a[96:99], a[172:175], v[136:139]\n\t"
"ds_write_b128 v242, v[236:239] offset:3072\n\t"
"v_mfma_f32_16x16x32_bf16 v[168:171], a[120:123], a[172:175], 0\n\t"
"s_waitcnt lgkmcnt(5)\n\t"
"v_mfma_f32_16x16x32_bf16 v[168:171], a[136:139], a[180:183], v[168:171]\n\t"
"s_waitcnt lgkmcnt(3)\n\t"
"ds_read_b128 a[180:183], v244 offset:26624\n\t"
"v_mfma_f32_16x16x32_bf16 v[168:171], a[128:131], a[176:179], v[168:171]\n\t"
"s_waitcnt lgkmcnt(3)\n\t"
"ds_read_b128 a[188:191], v244 offset:27136\n\t"
"v_mfma_f32_16x16x32_bf16 v[224:227], v[224:227], v[196:199], 0\n\t"
"ds_read_b128 a[200:203], v244 offset:27648\n\t"
"v_mfma_f32_16x16x32_bf16 v[228:231], v[228:231], v[196:199], 0\n\t"
"v_mfma_f32_16x16x32_bf16 v[2:5], a[0:3], a[168:171], 0\n\t"
"v_mfma_f32_16x16x32_bf16 v[2:5], a[8:11], a[196:199], v[2:5]\n\t"
"v_mfma_f32_16x16x32_bf16 v[2:5], a[16:19], a[192:195], v[2:5]\n\t"
"s_waitcnt vmcnt(0)\n\t"
"s_barrier\n\t"
"v_mfma_f32_16x16x32_bf16 v[34:37], a[32:35], a[196:199], 0\n\t"
"ds_read_b64_tr_b16 v[232:233], v240 offset:39936\n\t"
"v_mfma_f32_16x16x32_bf16 v[34:37], a[40:43], a[192:195], v[34:37]\n\t"
"ds_read_b64_tr_b16 v[234:235], v240 offset:41472\n\t"
"v_cvt_pk_bf16_f32 v224, v224, v225\n\t"
"v_mfma_f32_16x16x32_bf16 v[34:37], a[24:27], a[168:171], v[34:37]\n\t"
"ds_read_b64_tr_b16 v[236:237], v241 offset:39936\n\t"
"v_cvt_pk_bf16_f32 v225, v226, v227\n\t"
"v_mfma_f32_16x16x32_bf16 v[66:69], a[48:51], a[168:171], 0\n\t"
"s_waitcnt lgkmcnt(7)\n\t"
"ds_read_b64_tr_b16 v[238:239], v241 offset:41472\n\t"
"v_cvt_pk_bf16_f32 v226, v228, v229\n\t"
"v_mfma_f32_16x16x32_bf16 v[66:69], a[64:67], a[192:195], v[66:69]\n\t"
"ds_read_b128 a[168:171], v244 offset:35840\n\t"
"v_cvt_pk_bf16_f32 v227, v230, v231\n\t"
"v_mfma_f32_16x16x32_bf16 v[66:69], a[56:59], a[196:199], v[66:69]\n\t"
"s_cmp_lg_u32 s73, 0\n\t"
"s_cbranch_scc1 Lnp_%=\n\t"
"buffer_load_dwordx4 v[248:251], v252, s[8:11], s72 offen sc1\n\t"
"Lnp_%=:\n\t"
"ds_write_b128 v242, v[224:227] offset:4608\n\t"
"v_mfma_f32_16x16x32_bf16 v[100:103], a[72:75], a[184:187], 0\n\t"
"s_waitcnt lgkmcnt(4)\n\t"
"v_mfma_f32_16x16x32_bf16 v[100:103], a[80:83], a[172:175], v[100:103]\n\t"
"ds_read_b128 a[192:195], v244 offset:36352\n\t"
"v_mfma_f32_16x16x32_bf16 v[100:103], a[88:91], a[176:179], v[100:103]\n\t"
"s_waitcnt lgkmcnt(3)\n\t"
"ds_read_b128 a[204:207], v244 offset:36864\n\t"
"v_mfma_f32_16x16x32_bf16 v[232:235], v[232:235], v[196:199], 0\n\t"
"ds_read_b64_tr_b16 v[228:229], v240 offset:20480\n\t"
"v_mfma_f32_16x16x32_bf16 v[236:239], v[236:239], v[196:199], 0\n\t"
"ds_read_b64_tr_b16 v[230:231], v240 offset:22016\n\t"
"v_mfma_f32_16x16x32_bf16 v[132:135], a[112:115], a[176:179], 0\n\t"
"ds_read_b64_tr_b16 v[224:225], v241 offset:20480\n\t"
"v_mfma_f32_16x16x32_bf16 v[132:135], a[96:99], a[184:187], v[132:135]\n\t"
"ds_read_b64_tr_b16 v[226:227], v241 offset:22016\n\t"
"v_mfma_f32_16x16x32_bf16 v[132:135], a[104:107], a[172:175], v[132:135]\n\t"
"v_pk_mul_f32 v[232:233], v[232:233], v[242:243] op_sel:[0,1] op_sel_hi:[1,1]\n\t"
"v_pk_mul_f32 v[234:235], v[234:235], v[242:243] op_sel:[0,1] op_sel_hi:[1,1]\n\t"
"v_mfma_f32_16x16x32_bf16 v[164:167], a[120:123], a[184:187], 0\n\t"
"v_cvt_pk_bf16_f32 v232, v232, v233\n\t"
"v_cvt_pk_bf16_f32 v233, v234, v235\n\t"
"v_mfma_f32_16x16x32_bf16 v[164:167], a[128:131], a[172:175], v[164:167]\n\t"
"v_mfma_f32_16x16x32_bf16 v[164:167], a[136:139], a[176:179], v[164:167]\n\t"
"v_pk_mul_f32 v[236:237], v[236:237], v[242:243] op_sel:[0,1] op_sel_hi:[1,1]\n\t"
"v_pk_mul_f32 v[238:239], v[238:239], v[242:243] op_sel:[0,1] op_sel_hi:[1,1]\n\t"
"v_mfma_f32_16x16x32_bf16 v[18:21], a[0:3], a[180:183], 0\n\t"
"v_cvt_pk_bf16_f32 v234, v236, v237\n\t"
"v_cvt_pk_bf16_f32 v235, v238, v239\n\t"
"v_mfma_f32_16x16x32_bf16 v[18:21], a[8:11], a[188:191], v[18:21]\n\t"
"ds_read_b64_tr_b16 v[236:237], v240 offset:40448\n\t"
"v_mfma_f32_16x16x32_bf16 v[18:21], a[16:19], a[200:203], v[18:21]\n\t"
"ds_write_b128 v242, v[232:235] offset:32256\n\t"
"v_mfma_f32_16x16x32_bf16 v[50:53], a[32:35], a[188:191], 0\n\t"
"s_waitcnt lgkmcnt(2)\n\t"
"v_mfma_f32_16x16x32_bf16 v[50:53], a[40:43], a[200:203], v[50:53]\n\t"
"ds_read_b64_tr_b16 v[238:239], v240 offset:41984\n\t"
"v_mfma_f32_16x16x32_bf16 v[50:53], a[24:27], a[180:183], v[50:53]\n\t"
"ds_read_b64_tr_b16 v[232:233], v241 offset:40448\n\t"
"v_mfma_f32_16x16x32_bf16 v[228:231], v[228:231], v[196:199], 0\n\t"
"Lpg1_%=:\n\t"
"s_cbranch_vccnz Lpg5_%=\n\t"
"ds_read_b64_tr_b16 v[234:235], v241 offset:41984\n\t"
"v_mfma_f32_16x16x32_bf16 v[224:227], v[224:227], v[196:199], 0\n\t"
"v_mfma_f32_16x16x32_bf16 v[82:85], a[48:51], a[180:183], 0\n\t"
"v_mfma_f32_16x16x32_bf16 v[82:85], a[56:59], a[188:191], v[82:85]\n\t"
"v_mfma_f32_16x16x32_bf16 v[82:85], a[64:67], a[200:203], v[82:85]\n\t"
"v_mfma_f32_16x16x32_bf16 v[116:119], a[72:75], a[168:171], 0\n\t"
"v_cvt_pk_bf16_f32 v228, v228, v229\n\t"
"v_cvt_pk_bf16_f32 v229, v230, v231\n\t"
"v_mfma_f32_16x16x32_bf16 v[116:119], a[80:83], a[192:195], v[116:119]\n\t"
"v_cvt_pk_bf16_f32 v230, v224, v225\n\t"
"v_mfma_f32_16x16x32_bf16 v[116:119], a[88:91], a[204:207], v[116:119]\n\t"
"s_waitcnt lgkmcnt(3)\n\t"
"s_barrier\n\t"
"v_mfma_f32_16x16x32_bf16 v[148:151], a[104:107], a[192:195], 0\n\t"
"s_waitcnt lgkmcnt(2)\n\t"
"ds_read_b128 a[184:187], v244 offset:28160\n\t"
"v_mfma_f32_16x16x32_bf16 v[148:151], a[112:115], a[204:207], v[148:151]\n\t"
"s_waitcnt lgkmcnt(1)\n\t"
"ds_read_b128 a[196:199], v244 offset:37376\n\t"
"v_cvt_pk_bf16_f32 v231, v226, v227\n\t"
"v_mfma_f32_16x16x32_bf16 v[148:151], a[96:99], a[168:171], v[148:151]\n\t"
"ds_write_b128 v242, v[228:231] offset:6144\n\t"
"v_mfma_f32_16x16x32_bf16 v[236:239], v[236:239], v[196:199], 0\n\t"
"v_mfma_f32_16x16x32_bf16 v[232:235], v[232:235], v[196:199], 0\n\t"
"ds_read_b128 v[204:207], v244 offset:28672\n\t"
"v_mfma_f32_16x16x32_bf16 v[180:183], a[120:123], a[168:171], 0\n\t"
"s_waitcnt lgkmcnt(3)\n\t"
"v_mfma_f32_16x16x32_bf16 v[180:183], a[128:131], a[192:195], v[180:183]\n\t"
"v_mfma_f32_16x16x32_bf16 v[180:183], a[136:139], a[204:207], v[180:183]\n\t"
"v_mfma_f32_16x16x32_bf16 v[22:25], a[0:3], a[188:191], 0\n\t"
"v_cvt_pk_bf16_f32 v236, v236, v237\n\t"
"v_cvt_pk_bf16_f32 v237, v238, v239\n\t"
"v_mfma_f32_16x16x32_bf16 v[22:25], a[8:11], a[200:203], v[22:25]\n\t"
"v_cvt_pk_bf16_f32 v238, v232, v233\n\t"
"v_cvt_pk_bf16_f32 v239, v234, v235\n\t"
"v_mfma_f32_16x16x32_bf16 v[22:25], a[16:19], a[184:187], v[22:25]\n\t"
"ds_write_b128 v242, v[236:239] offset:7680\n\t"
"v_mfma_f32_16x16x32_bf16 v[54:57], a[32:35], a[200:203], 0\n\t"
"v_mfma_f32_16x16x32_bf16 v[54:57], a[40:43], a[184:187], v[54:57]\n\t"
"ds_read_b128 v[196:199], v244 offset:37888\n\t"
"v_mfma_f32_16x16x32_bf16 v[54:57], a[24:27], a[188:191], v[54:57]\n\t"
"v_mfma_f32_16x16x32_bf16 v[86:89], a[48:51], a[188:191], 0\n\t"
"s_waitcnt lgkmcnt(4)\n\t"
"v_mfma_f32_16x16x32_bf16 v[86:89], a[64:67], a[184:187], v[86:89]\n\t"
"v_mfma_f32_16x16x32_bf16 v[86:89], a[56:59], a[200:203], v[86:89]\n\t"
"v_mfma_f32_16x16x32_bf16 v[120:123], a[72:75], a[192:195], 0\n\t"
"s_mov_b32 s20, 294912\n\t"
"v_mfma_f32_16x16x32_bf16 v[120:123], a[80:83], a[204:207], v[120:123]\n\t"
"v_mfma_f32_16x16x32_bf16 v[120:123], a[88:91], a[196:199], v[120:123]\n\t"
"v_mfma_f32_16x16x32_bf16 v[152:155], a[104:107], a[204:207], 0\n\t"
"ds_read_b128 a[188:191], v244 offset:29184\n\t"
"v_mfma_f32_16x16x32_bf16 v[152:155], a[112:115], a[196:199], v[152:155]\n\t"
"v_mfma_f32_16x16x32_bf16 v[152:155], a[96:99], a[192:195], v[152:155]\n\t"
"s_waitcnt lgkmcnt(2)\n\t"
"s_cmp_lg_u32 s73, 0\n\t"
"s_cbranch_scc1 Lgo_%=\n\t"
"Lchk_%=:\n\t"
"s_waitcnt vmcnt(0)\n\t"
"v_cmpx_ne_u32_e64 s[60:61], v220, v248\n\t"
"v_cmpx_ne_u32_e64 s[60:61], v221, v249\n\t"
"v_cmpx_ne_u32_e64 s[60:61], v222, v250\n\t"
"v_cmpx_ne_u32_e64 s[60:61], v223, v251\n\t"
"s_not_b64 s[60:61], s[60:61]\n\t"
"s_mov_b64 exec, -1\n\t"
"s_lshl_b64 s[60:61], s[60:61], 10\n\t"
"s_cbranch_scc0 Lgo_%=\n\t"
"s_sub_u32 s68, s68, 1\n\t"
"s_cbranch_scc1 Lfb_%=\n\t"
"buffer_load_dwordx4 v[248:251], v252, s[8:11], s72 offen sc1\n\t"
"s_branch Lchk_%=\n\t"
"Lgo_%=:\n\t"
"s_barrier\n\t"
"v_mfma_f32_16x16x32_bf16 v[184:187], a[120:123], a[192:195], 0\n\t"
"s_lshr_b32 s28, s73, 1\n\t"
"s_mul_i32 s29, s28, 1757184\n\t"
"v_mfma_f32_16x16x32_bf16 v[184:187], a[136:139], a[196:199], v[184:187]\n\t"
"buffer_load_dwordx4 a[144:147], off, s[80:83], s20 offset:0 sc1\n\t"
"v_mfma_f32_16x16x32_bf16 v[184:187], a[128:131], a[204:207], v[184:187]\n\t"
"global_load_dwordx4 v[220:223], v252, s[48:49] offset:1024\n\t"
"v_mfma_f32_16x16x32_bf16 v[26:29], a[0:3], a[200:203], 0\n\t"
"buffer_load_dwordx4 a[152:155], off, s[80:83], s20 offset:1024 sc1\n\t"
"v_mfma_f32_16x16x32_bf16 v[26:29], a[8:11], a[184:187], v[26:29]\n\t"
"s_add_u32 s21, s21, s29\n\t"
"s_mul_i32 s29, s73, 3072\n\t"
"v_lshlrev_b32 v245, 4, v245\n\t"
"v_mfma_f32_16x16x32_bf16 v[26:29], a[16:19], v[204:207], v[26:29]\n\t"
"s_add_u32 s21, s21, s29\n\t"
"s_mul_i32 s28, s28, 53760\n\t"
"v_xor_b32 v255, 32, v246\n\t"
"v_mfma_f32_16x16x32_bf16 v[58:61], a[32:35], a[184:187], 0\n\t"
"s_lshl1_add_u32 s24, s24, s28\n\t"
"ds_read_b128 v[212:215], v244 offset:38400\n\t"
"s_sub_u32 s24, s24, 43008\n\t"
"v_mfma_f32_16x16x32_bf16 v[58:61], a[40:43], v[204:207], v[58:61]\n\t"
"buffer_load_dwordx4 a[160:163], off, s[80:83], s20 offset:2048 sc1\n\t"
"v_mfma_f32_16x16x32_bf16 v[58:61], a[24:27], a[200:203], v[58:61]\n\t"
"s_mov_b32 s20, 307200\n\t"
"s_mov_b32 s52, 0x3fd80000\n\t"
"s_mov_b32 s53, s52\n\t"
"v_mfma_f32_16x16x32_bf16 v[90:93], a[48:51], a[200:203], 0\n\t"
"buffer_load_dwordx4 v[200:203], off, s[80:83], s20 offset:0 sc1\n\t"
"v_mfma_f32_16x16x32_bf16 v[90:93], a[64:67], v[204:207], v[90:93]\n\t"
"s_waitcnt lgkmcnt(2)\n\t"
"s_mov_b32 s54, 0x3e97b426\n\t"
"s_mov_b32 s55, s54\n\t"
"v_mfma_f32_16x16x32_bf16 v[90:93], a[56:59], a[184:187], v[90:93]\n\t"
"s_mov_b32 s56, 0x3f400000\n\t"
"s_mov_b32 s57, s56\n\t"
"s_mov_b32 s58, 0x3f2aaaab\n\t"
"v_mfma_f32_16x16x32_bf16 v[124:127], a[72:75], a[204:207], 0\n\t"
"buffer_load_dwordx4 v[208:211], off, s[80:83], s20 offset:1024 sc1\n\t"
"v_mfma_f32_16x16x32_bf16 v[124:127], a[80:83], a[196:199], v[124:127]\n\t"
"s_mov_b32 s59, s58\n\t"
"s_mov_b32 s60, 0x3ee38e39\n\t"
"s_mov_b32 s61, s60\n\t"
"v_mfma_f32_16x16x32_bf16 v[124:127], a[88:91], v[196:199], v[124:127]\n\t"
"s_mov_b32 s62, 0x40100000\n\t"
"s_mov_b32 s63, s62\n\t"
"s_mov_b32 s64, 0x3faaaaab\n\t"
"v_mfma_f32_16x16x32_bf16 v[156:159], a[104:107], a[196:199], 0\n\t"
"buffer_load_dwordx4 v[216:219], off, s[80:83], s20 offset:2048 sc1\n\t"
"v_mfma_f32_16x16x32_bf16 v[156:159], a[112:115], v[196:199], v[156:159]\n\t"
"s_mov_b32 s20, 319488\n\t"
"s_mov_b32 s65, s64\n\t"
"s_mov_b32 s66, 0x3f17b426\n\t"
"v_mfma_f32_16x16x32_bf16 v[156:159], a[96:99], a[204:207], v[156:159]\n\t"
"s_mov_b32 s67, s66\n\t"
"v_mfma_f32_16x16x32_bf16 v[188:191], a[120:123], a[204:207], 0\n\t"
"buffer_load_dwordx4 a[4:7], off, s[80:83], s20 offset:0 sc1\n\t"
"v_mfma_f32_16x16x32_bf16 v[188:191], a[136:139], v[196:199], v[188:191]\n\t"
"s_waitcnt lgkmcnt(1)\n\t"
"v_mfma_f32_16x16x32_bf16 v[188:191], a[128:131], a[196:199], v[188:191]\n\t"
"v_mfma_f32_16x16x32_bf16 v[30:33], a[0:3], a[184:187], 0\n\t"
"buffer_load_dwordx4 a[0:3], off, s[80:83], s20 offset:1024 sc1\n\t"
"v_mfma_f32_16x16x32_bf16 v[30:33], a[8:11], v[204:207], v[30:33]\n\t"
"v_mfma_f32_16x16x32_bf16 v[30:33], a[16:19], a[188:191], v[30:33]\n\t"
"ds_read_b128 a[168:171], v244 offset:0\n\t"
"v_mfma_f32_16x16x32_bf16 v[62:65], a[32:35], v[204:207], 0\n\t"
"buffer_load_dwordx4 a[8:11], off, s[80:83], s20 offset:2048 sc1\n\t"
"v_mfma_f32_16x16x32_bf16 v[62:65], a[40:43], a[188:191], v[62:65]\n\t"
"s_mov_b32 s20, 479232\n\t"
"ds_read_b128 a[172:175], v244 offset:512\n\t"
"v_mfma_f32_16x16x32_bf16 v[62:65], a[24:27], a[184:187], v[62:65]\n\t"
"ds_read_b128 a[176:179], v244 offset:1024\n\t"
"v_mfma_f32_16x16x32_bf16 v[94:97], a[48:51], a[184:187], 0\n\t"
"buffer_load_dwordx4 a[16:19], off, s[80:83], s20 offset:0 sc1\n\t"
"v_mfma_f32_16x16x32_bf16 v[94:97], a[64:67], a[188:191], v[94:97]\n\t"
"s_waitcnt lgkmcnt(3)\n\t"
"ds_read_b64_tr_b16 v[224:225], v246 offset:0\n\t"
"v_mfma_f32_16x16x32_bf16 v[94:97], a[56:59], v[204:207], v[94:97]\n\t"
"ds_read_b64_tr_b16 v[228:229], v255 offset:0\n\t"
"v_mfma_f32_16x16x32_bf16 v[128:131], a[72:75], a[196:199], 0\n\t"
"ds_read_b128 a[180:183], v244 offset:1536\n\t"
"v_mfma_f32_16x16x32_bf16 v[128:131], a[80:83], v[196:199], v[128:131]\n\t"
"ds_read_b64_tr_b16 v[232:233], v246 offset:6144\n\t"
"v_mfma_f32_16x16x32_bf16 v[128:131], a[88:91], v[212:215], v[128:131]\n\t"
"buffer_load_dwordx4 v[204:207], off, s[80:83], s20 offset:1024 sc1\n\t"
"v_mfma_f32_16x16x32_bf16 v[160:163], a[104:107], v[196:199], 0\n\t"
"ds_read_b64_tr_b16 v[236:237], v255 offset:6144\n\t"
"v_mfma_f32_16x16x32_bf16 v[160:163], a[112:115], v[212:215], v[160:163]\n\t"
"ds_read_b128 a[184:187], v244 offset:2048\n\t"
"v_mfma_f32_16x16x32_bf16 v[160:163], a[96:99], a[196:199], v[160:163]\n\t"
"buffer_load_dwordx4 a[24:27], off, s[80:83], s20 offset:2048 sc1\n\t"
"s_mov_b32 s20, 491520\n\t"
"v_mfma_f32_16x16x32_bf16 v[192:195], a[120:123], a[196:199], 0\n\t"
"s_waitcnt vmcnt(11) lgkmcnt(5)\n\t"
"ds_read_b128 a[188:191], v244 offset:2560\n\t"
"v_mfma_f32_16x16x32_bf16 v[192:195], a[136:139], v[212:215], v[192:195]\n\t"
"buffer_load_dwordx4 a[32:35], off, s[80:83], s20 offset:0 sc1\n\t"
"v_mfma_f32_16x16x32_bf16 v[192:195], a[128:131], v[196:199], v[192:195]\n\t"
"s_waitcnt lgkmcnt(3)\n\t"
"ds_read_b128 a[192:195], v244 offset:3072\n\t"
"v_mfma_f32_4x4x4_16b_bf16 v[224:227], v[224:225], v[220:221], 0\n\t"
"s_waitcnt lgkmcnt(3)\n\t"
"v_mfma_f32_4x4x4_16b_bf16 v[228:231], v[228:229], v[220:221], 0\n\t"
"s_waitcnt vmcnt(10)\n\t"
"v_mfma_f32_4x4x4_16b_bf16 v[232:235], v[232:233], v[220:221], 0\n\t"
"v_mfma_f32_4x4x4_16b_bf16 v[236:239], v[236:237], v[220:221], 0\n\t"
"buffer_load_dwordx4 a[40:43], off, s[80:83], s20 offset:1024 sc1\n\t"
"Lst1_%=:\n\t"
"v_mfma_f32_16x16x32_bf16 v[2:5], a[144:147], a[168:171], v[2:5]\n\t"
"ds_read_b128 a[196:199], v244 offset:3584\n\t"
"v_cvt_pk_bf16_f32 v224, v224, v225\n\t"
"v_mfma_f32_16x16x32_bf16 v[2:5], a[160:163], a[176:179], v[2:5]\n\t"
"ds_read_b128 a[200:203], v244 offset:4096\n\t"
"v_cvt_pk_bf16_f32 v225, v226, v227\n\t"
"v_mfma_f32_16x16x32_bf16 v[2:5], a[152:155], a[172:175], v[2:5]\n\t"
"buffer_load_dwordx4 a[48:51], off, s[80:83], s20 offset:2048 sc1\n\t"
"s_mov_b32 s20, 503808\n\t"
"v_mfma_f32_16x16x32_bf16 v[6:9], a[144:147], a[172:175], v[6:9]\n\t"
"buffer_load_dwordx4 a[80:83], off, s[80:83], s20 offset:0 sc1\n\t"
"v_mfma_f32_16x16x32_bf16 v[6:9], a[152:155], a[176:179], v[6:9]\n\t"
"s_waitcnt lgkmcnt(4)\n\t"
"ds_read_b128 a[204:207], v244 offset:4608\n\t"
"v_cvt_pk_bf16_f32 v226, v228, v229\n\t"
"v_mfma_f32_16x16x32_bf16 v[6:9], a[160:163], a[180:183], v[6:9]\n\t"
"v_cvt_pk_bf16_f32 v227, v230, v231\n\t"
"v_cvt_pk_bf16_f32 v232, v232, v233\n\t"
"v_cvt_pk_bf16_f32 v233, v234, v235\n\t"
"v_cvt_pk_bf16_f32 v234, v236, v237\n\t"
"v_cvt_pk_bf16_f32 v235, v238, v239\n\t"
"v_mfma_f32_16x16x32_bf16 v[10:13], a[160:163], a[184:187], v[10:13]\n\t"
"s_waitcnt lgkmcnt(4)\n\t"
"v_mfma_f32_16x16x32_bf16 v[10:13], a[152:155], a[180:183], v[10:13]\n\t"
"buffer_load_dwordx4 a[88:91], off, s[80:83], s20 offset:1024 sc1\n\t"
"v_mfma_f32_16x16x32_bf16 v[10:13], a[144:147], a[176:179], v[10:13]\n\t"
"ds_write_b128 v245, v[224:227] offset:0\n\t"
"v_mfma_f32_16x16x32_bf16 v[14:17], a[144:147], a[180:183], v[14:17]\n\t"
"buffer_load_dwordx4 a[96:99], off, s[80:83], s20 offset:2048 sc1\n\t"
"v_mfma_f32_16x16x32_bf16 v[14:17], a[152:155], a[184:187], v[14:17]\n\t"
"s_waitcnt lgkmcnt(4)\n\t"
"ds_read_b64_tr_b16 v[228:229], v246 offset:12288\n\t"
"v_mfma_f32_16x16x32_bf16 v[14:17], a[160:163], a[188:191], v[14:17]\n\t"
"s_mov_b32 s20, 110592\n\t"
"ds_write_b128 v245, v[232:235] offset:6144\n\t"
"v_mfma_f32_16x16x32_bf16 v[18:21], a[160:163], a[192:195], v[18:21]\n\t"
"buffer_load_dwordx4 a[12:15], off, s[80:83], s20 offset:0 sc1\n\t"
"v_mfma_f32_16x16x32_bf16 v[18:21], a[152:155], a[188:191], v[18:21]\n\t"
"s_waitcnt lgkmcnt(5)\n\t"
"ds_read_b64_tr_b16 v[236:237], v255 offset:12288\n\t"
"v_mfma_f32_16x16x32_bf16 v[18:21], a[144:147], a[184:187], v[18:21]\n\t"
"buffer_load_dwordx4 a[20:23], off, s[80:83], s20 offset:1024 sc1\n\t"
"v_mfma_f32_16x16x32_bf16 v[22:25], a[144:147], a[188:191], v[22:25]\n\t"
"s_waitcnt lgkmcnt(5)\n\t"
"ds_read_b64_tr_b16 v[224:225], v246 offset:19968\n\t"
"v_mfma_f32_16x16x32_bf16 v[22:25], a[152:155], a[192:195], v[22:25]\n\t"
"ds_read_b64_tr_b16 v[232:233], v255 offset:19968\n\t"
"v_mfma_f32_16x16x32_bf16 v[22:25], a[160:163], a[196:199], v[22:25]\n\t"
"buffer_load_dwordx4 a[28:31], off, s[80:83], s20 offset:2048 sc1\n\t"
"v_mfma_f32_16x16x32_bf16 v[26:29], a[160:163], a[200:203], v[26:29]\n\t"
"s_waitcnt lgkmcnt(6)\n\t"
"s_mov_b32 s20, 122880\n\t"
"v_mfma_f32_16x16x32_bf16 v[26:29], a[152:155], a[196:199], v[26:29]\n\t"
"v_mfma_f32_16x16x32_bf16 v[26:29], a[144:147], a[192:195], v[26:29]\n\t"
"buffer_load_dwordx4 a[36:39], off, s[80:83], s20 offset:0 sc1\n\t"
"v_mfma_f32_16x16x32_bf16 v[30:33], a[144:147], a[196:199], v[30:33]\n\t"
"s_waitcnt lgkmcnt(4)\n\t"
"v_mfma_f32_16x16x32_bf16 v[30:33], a[152:155], a[200:203], v[30:33]\n\t"
"s_waitcnt lgkmcnt(2)\n\t"
"v_mfma_f32_16x16x32_bf16 v[30:33], a[160:163], a[204:207], v[30:33]\n\t"
"buffer_load_dwordx4 a[44:47], off, s[80:83], s20 offset:1024 sc1\n\t"
"v_mfma_f32_4x4x4_16b_bf16 v[228:231], v[228:229], v[220:221], 0\n\t"
"s_waitcnt lgkmcnt(0)\n\t"
"v_mfma_f32_4x4x4_16b_bf16 v[236:239], v[236:237], v[220:221], 0\n\t"
"s_waitcnt vmcnt(17)\n\t"
"v_mfma_f32_4x4x4_16b_bf16 v[224:227], v[224:225], v[220:221], 0\n\t"
"v_mfma_f32_4x4x4_16b_bf16 v[232:235], v[232:233], v[220:221], 0\n\t"
"v_mfma_f32_16x16x32_bf16 v[34:37], v[200:203], a[168:171], v[34:37]\n\t"
"buffer_load_dwordx4 a[52:55], off, s[80:83], s20 offset:2048 sc1\n\t"
"v_mfma_f32_16x16x32_bf16 v[34:37], v[216:219], a[176:179], v[34:37]\n\t"
"s_mov_b32 s20, 135168\n\t"
"v_cvt_pk_bf16_f32 v228, v228, v229\n\t"
"v_cvt_pk_bf16_f32 v229, v230, v231\n\t"
"v_mfma_f32_16x16x32_bf16 v[34:37], v[208:211], a[172:175], v[34:37]\n\t"
"v_cvt_pk_bf16_f32 v230, v236, v237\n\t"
"v_cvt_pk_bf16_f32 v231, v238, v239\n\t"
"v_mfma_f32_16x16x32_bf16 v[38:41], v[200:203], a[172:175], v[38:41]\n\t"
"v_cvt_pk_bf16_f32 v224, v224, v225\n\t"
"v_cvt_pk_bf16_f32 v225, v226, v227\n\t"
"v_mfma_f32_16x16x32_bf16 v[38:41], v[208:211], a[176:179], v[38:41]\n\t"
"v_cvt_pk_bf16_f32 v226, v232, v233\n\t"
"v_cvt_pk_bf16_f32 v227, v234, v235\n\t"
"v_mfma_f32_16x16x32_bf16 v[38:41], v[216:219], a[180:183], v[38:41]\n\t"
"buffer_load_dwordx4 a[84:87], off, s[80:83], s20 offset:0 sc1\n\t"
"v_mfma_f32_16x16x32_bf16 v[42:45], v[216:219], a[184:187], v[42:45]\n\t"
"buffer_load_dwordx4 a[92:95], off, s[80:83], s20 offset:1024 sc1\n\t"
"v_mfma_f32_16x16x32_bf16 v[42:45], v[208:211], a[180:183], v[42:45]\n\t"
"buffer_load_dwordx4 a[100:103], off, s[80:83], s20 offset:2048 sc1\n\t"
"v_mfma_f32_16x16x32_bf16 v[42:45], v[200:203], a[176:179], v[42:45]\n\t"
"s_mov_b32 s20, 147456\n\t"
"ds_write_b128 v245, v[228:231] offset:12288\n\t"
"v_mfma_f32_16x16x32_bf16 v[46:49], v[200:203], a[180:183], v[46:49]\n\t"
"v_mfma_f32_16x16x32_bf16 v[46:49], v[208:211], a[184:187], v[46:49]\n\t"
"buffer_load_dwordx4 a[140:143], off, s[80:83], s20 offset:0 sc1\n\t"
"v_mfma_f32_16x16x32_bf16 v[46:49], v[216:219], a[188:191], v[46:49]\n\t"
"ds_write_b128 v245, v[224:227] offset:19968\n\t"
"v_mfma_f32_16x16x32_bf16 v[50:53], v[216:219], a[192:195], v[50:53]\n\t"
"v_mfma_f32_16x16x32_bf16 v[50:53], v[208:211], a[188:191], v[50:53]\n\t"
"ds_read_b64_tr_b16 v[236:237], v246 offset:26112\n\t"
"v_mfma_f32_16x16x32_bf16 v[50:53], v[200:203], a[184:187], v[50:53]\n\t"
"ds_read_b64_tr_b16 v[232:233], v255 offset:26112\n\t"
"v_mfma_f32_16x16x32_bf16 v[54:57], v[200:203], a[188:191], v[54:57]\n\t"
"buffer_load_dwordx4 a[148:151], off, s[80:83], s20 offset:1024 sc1\n\t"
"v_mfma_f32_16x16x32_bf16 v[54:57], v[208:211], a[192:195], v[54:57]\n\t"
"ds_read_b64_tr_b16 v[228:229], v246 offset:32256\n\t"
"v_mfma_f32_16x16x32_bf16 v[54:57], v[216:219], a[196:199], v[54:57]\n\t"
"ds_read_b64_tr_b16 v[224:225], v255 offset:32256\n\t"
"v_mfma_f32_16x16x32_bf16 v[58:61], v[216:219], a[200:203], v[58:61]\n\t"
"buffer_load_dwordx4 a[156:159], off, s[80:83], s20 offset:2048 sc1\n\t"
"v_mfma_f32_16x16x32_bf16 v[58:61], v[208:211], a[196:199], v[58:61]\n\t"
"s_mov_b32 s20, 159744\n\t"
"v_mfma_f32_16x16x32_bf16 v[58:61], v[200:203], a[192:195], v[58:61]\n\t"
"v_mfma_f32_16x16x32_bf16 v[62:65], v[200:203], a[196:199], v[62:65]\n\t"
"s_waitcnt lgkmcnt(3)\n\t"
"v_mfma_f32_16x16x32_bf16 v[62:65], v[208:211], a[200:203], v[62:65]\n\t"
"buffer_load_dwordx4 v[240:243], off, s[80:83], s20 offset:0 sc1\n\t"
"v_mfma_f32_16x16x32_bf16 v[62:65], v[216:219], a[204:207], v[62:65]\n\t"
"s_waitcnt lgkmcnt(1)\n\t"
"v_mfma_f32_4x4x4_16b_bf16 v[236:239], v[236:237], v[220:221], 0\n\t"
"s_waitcnt lgkmcnt(0)\n\t"
"v_mfma_f32_4x4x4_16b_bf16 v[232:235], v[232:233], v[220:221], 0\n\t"
"s_waitcnt vmcnt(22)\n\t"
"v_mfma_f32_4x4x4_16b_bf16 v[228:231], v[228:229], v[220:221], 0\n\t"
"v_mfma_f32_4x4x4_16b_bf16 v[224:227], v[224:225], v[220:221], 0\n\t"
"v_mfma_f32_16x16x32_bf16 v[66:69], a[4:7], a[168:171], v[66:69]\n\t"
"buffer_load_dwordx4 a[208:211], off, s[80:83], s20 offset:1024 sc1\n\t"
"v_mfma_f32_16x16x32_bf16 v[66:69], a[8:11], a[176:179], v[66:69]\n\t"
"ds_read_b128 a[168:171], v244 offset:9216\n\t"
"v_cvt_pk_bf16_f32 v236, v236, v237\n\t"
"v_mfma_f32_16x16x32_bf16 v[66:69], a[0:3], a[172:175], v[66:69]\n\t"
"v_cvt_pk_bf16_f32 v237, v238, v239\n\t"
"v_cvt_pk_bf16_f32 v238, v232, v233\n\t"
"v_mfma_f32_16x16x32_bf16 v[70:73], a[4:7], a[172:175], v[70:73]\n\t"
"ds_read_b128 a[172:175], v244 offset:9728\n\t"
"v_cvt_pk_bf16_f32 v239, v234, v235\n\t"
"v_mfma_f32_16x16x32_bf16 v[70:73], a[0:3], a[176:179], v[70:73]\n\t"
"v_cvt_pk_bf16_f32 v228, v228, v229\n\t"
"v_cvt_pk_bf16_f32 v229, v230, v231\n\t"
"v_mfma_f32_16x16x32_bf16 v[70:73], a[8:11], a[180:183], v[70:73]\n\t"
"buffer_load_dwordx4 v[248:251], off, s[80:83], s20 offset:2048 sc1\n\t"
"v_cvt_pk_bf16_f32 v230, v224, v225\n\t"
"v_cvt_pk_bf16_f32 v231, v226, v227\n\t"
"s_mov_b32 s20, 172032\n\t"
"v_mfma_f32_16x16x32_bf16 v[74:77], a[8:11], a[184:187], v[74:77]\n\t"
"buffer_load_dwordx4 a[212:215], off, s[80:83], s20 offset:0 sc1\n\t"
"v_mfma_f32_16x16x32_bf16 v[74:77], a[0:3], a[180:183], v[74:77]\n\t"
"v_mfma_f32_16x16x32_bf16 v[74:77], a[4:7], a[176:179], v[74:77]\n\t"
"ds_write_b128 v245, v[236:239] offset:26112\n\t"
"v_mfma_f32_16x16x32_bf16 v[78:81], a[4:7], a[180:183], v[78:81]\n\t"
"v_mfma_f32_16x16x32_bf16 v[78:81], a[0:3], a[184:187], v[78:81]\n\t"
"buffer_load_dwordx4 a[216:219], off, s[80:83], s20 offset:1024 sc1\n\t"
"v_mfma_f32_16x16x32_bf16 v[78:81], a[8:11], a[188:191], v[78:81]\n\t"
"ds_write_b128 v245, v[228:231] offset:32256\n\t"
"v_mfma_f32_16x16x32_bf16 v[82:85], a[8:11], a[192:195], v[82:85]\n\t"
"v_mfma_f32_16x16x32_bf16 v[82:85], a[0:3], a[188:191], v[82:85]\n\t"
"ds_read_b128 a[176:179], v244 offset:10240\n\t"
"v_mfma_f32_16x16x32_bf16 v[82:85], a[4:7], a[184:187], v[82:85]\n\t"
"ds_read_b64_tr_b16 v[232:233], v246 offset:39936\n\t"
"v_mfma_f32_16x16x32_bf16 v[86:89], a[4:7], a[188:191], v[86:89]\n\t"
"ds_read_b64_tr_b16 v[224:225], v255 offset:39936\n\t"
"v_mfma_f32_16x16x32_bf16 v[86:89], a[0:3], a[192:195], v[86:89]\n\t"
"ds_read_b128 a[180:183], v244 offset:10752\n\t"
"v_mfma_f32_16x16x32_bf16 v[86:89], a[8:11], a[196:199], v[86:89]\n\t"
"buffer_load_dwordx4 a[220:223], off, s[80:83], s20 offset:2048 sc1\n\t"
"s_mov_b32 s20, 36864\n\t"
"v_mfma_f32_16x16x32_bf16 v[90:93], a[8:11], a[200:203], v[90:93]\n\t"
"ds_read_b64_tr_b16 v[236:237], v246 offset:46080\n\t"
"v_mfma_f32_16x16x32_bf16 v[90:93], a[0:3], a[196:199], v[90:93]\n\t"
"ds_read_b64_tr_b16 v[228:229], v255 offset:46080\n\t"
"v_mfma_f32_16x16x32_bf16 v[90:93], a[4:7], a[192:195], v[90:93]\n\t"
"ds_read_b128 a[184:187], v244 offset:11264\n\t"
"v_mfma_f32_16x16x32_bf16 v[94:97], a[4:7], a[196:199], v[94:97]\n\t"
"buffer_load_dwordx4 a[224:227], off, s[80:83], s20 offset:0 sc1\n\t"
"v_mfma_f32_16x16x32_bf16 v[94:97], a[0:3], a[200:203], v[94:97]\n\t"
"s_waitcnt lgkmcnt(4)\n\t"
"ds_read_b128 a[188:191], v244 offset:11776\n\t"
"v_mfma_f32_16x16x32_bf16 v[94:97], a[8:11], a[204:207], v[94:97]\n\t"
"buffer_load_dwordx4 a[228:231], off, s[80:83], s20 offset:1024 sc1\n\t"
"v_mfma_f32_4x4x4_16b_bf16 v[232:235], v[232:233], v[220:221], 0\n\t"
"s_waitcnt lgkmcnt(2)\n\t"
"v_mfma_f32_4x4x4_16b_bf16 v[224:227], v[224:225], v[220:221], 0\n\t"
"s_waitcnt vmcnt(26)\n\t"
"v_mfma_f32_4x4x4_16b_bf16 v[236:239], v[236:237], v[220:221], 0\n\t"
"ds_read_b128 a[192:195], v244 offset:12288\n\t"
"v_mfma_f32_4x4x4_16b_bf16 v[228:231], v[228:229], v[220:221], 0\n\t"
"v_mfma_f32_16x16x32_bf16 v[100:103], a[16:19], a[168:171], v[100:103]\n\t"
"ds_read_b128 a[196:199], v244 offset:12800\n\t"
"v_cvt_pk_bf16_f32 v232, v232, v233\n\t"
"v_mfma_f32_16x16x32_bf16 v[100:103], a[24:27], a[176:179], v[100:103]\n\t"
"ds_read_b128 a[200:203], v244 offset:13312\n\t"
"v_cvt_pk_bf16_f32 v233, v234, v235\n\t"
"v_mfma_f32_16x16x32_bf16 v[100:103], v[204:207], a[172:175], v[100:103]\n\t"
"buffer_load_dwordx4 a[232:235], off, s[80:83], s20 offset:2048 sc1\n\t"
"s_mov_b32 s20, 49152\n\t"
"v_mfma_f32_16x16x32_bf16 v[104:107], a[16:19], a[172:175], v[104:107]\n\t"
"buffer_load_dwordx4 a[236:239], off, s[80:83], s20 offset:0 sc1\n\t"
"v_mfma_f32_16x16x32_bf16 v[104:107], v[204:207], a[176:179], v[104:107]\n\t"
"s_waitcnt lgkmcnt(4)\n\t"
"ds_read_b128 a[204:207], v244 offset:13824\n\t"
"v_cvt_pk_bf16_f32 v234, v224, v225\n\t"
"v_mfma_f32_16x16x32_bf16 v[104:107], a[24:27], a[180:183], v[104:107]\n\t"
"v_cvt_pk_bf16_f32 v235, v226, v227\n\t"
"v_cvt_pk_bf16_f32 v236, v236, v237\n\t"
"v_cvt_pk_bf16_f32 v237, v238, v239\n\t"
"v_cvt_pk_bf16_f32 v238, v228, v229\n\t"
"v_cvt_pk_bf16_f32 v239, v230, v231\n\t"
"v_mfma_f32_16x16x32_bf16 v[108:111], a[24:27], a[184:187], v[108:111]\n\t"
"s_waitcnt lgkmcnt(4)\n\t"
"v_fma_f32 v196, -v2, s56, -v100\n\t"
"v_fma_f32 v197, -v3, s56, -v101\n\t"
"v_mfma_f32_16x16x32_bf16 v[108:111], v[204:207], a[180:183], v[108:111]\n\t"
"v_fma_f32 v198, -v4, s56, -v102\n\t"
"v_fma_f32 v199, -v5, s56, -v103\n\t"
"v_mfma_f32_16x16x32_bf16 v[108:111], a[16:19], a[176:179], v[108:111]\n\t"
"ds_write_b128 v245, v[232:235] offset:39936\n\t"
"v_mfma_f32_16x16x32_bf16 v[112:115], a[16:19], a[180:183], v[112:115]\n\t"
"buffer_load_dwordx4 a[240:243], off, s[80:83], s20 offset:1024 sc1\n\t"
"v_mfma_f32_16x16x32_bf16 v[112:115], v[204:207], a[184:187], v[112:115]\n\t"
"buffer_load_dwordx4 a[244:247], off, s[80:83], s20 offset:2048 sc1\n\t"
"v_mfma_f32_16x16x32_bf16 v[112:115], a[24:27], a[188:191], v[112:115]\n\t"
"s_waitcnt lgkmcnt(4)\n\t"
"ds_write_b128 v245, v[236:239] offset:46080\n\t"
"s_mov_b32 s20, 61440\n\t"
"v_mfma_f32_16x16x32_bf16 v[116:119], a[24:27], a[192:195], v[116:119]\n\t"
"buffer_load_dwordx4 a[248:251], off, s[80:83], s20 offset:0 sc1\n\t"
"v_mfma_f32_16x16x32_bf16 v[116:119], v[204:207], a[188:191], v[116:119]\n\t"
"s_waitcnt lgkmcnt(4)\n\t"
"ds_read_b64_tr_b16 v[224:225], v246 offset:52224\n\t"
"v_fma_f32 v212, -v10, s56, -v108\n\t"
"v_mfma_f32_16x16x32_bf16 v[116:119], a[16:19], a[184:187], v[116:119]\n\t"
"ds_read_b64_tr_b16 v[228:229], v255 offset:52224\n\t"
"v_fma_f32 v213, -v11, s56, -v109\n\t"
"v_mfma_f32_16x16x32_bf16 v[120:123], a[16:19], a[188:191], v[120:123]\n\t"
"s_waitcnt lgkmcnt(5)\n\t"
"v_fma_f32 v214, -v12, s56, -v110\n\t"
"v_fma_f32 v215, -v13, s56, -v111\n\t"
"v_mfma_f32_16x16x32_bf16 v[120:123], v[204:207], a[192:195], v[120:123]\n\t"
"v_fma_f32 v98, -v14, s56, -v112\n\t"
"v_fma_f32 v99, -v15, s56, -v113\n\t"
"v_mfma_f32_16x16x32_bf16 v[120:123], a[24:27], a[196:199], v[120:123]\n\t"
"buffer_load_dwordx4 a[252:255], off, s[80:83], s20 offset:1024 sc1\n\t"
"v_mfma_f32_16x16x32_bf16 v[124:127], a[24:27], a[200:203], v[124:127]\n\t"
"s_waitcnt lgkmcnt(4)\n\t"
"v_accvgpr_write_b32 a0, v98\n\t"
"v_fma_f32 v98, -v16, s56, -v114\n\t"
"v_mfma_f32_16x16x32_bf16 v[124:127], v[204:207], a[196:199], v[124:127]\n\t"
"v_accvgpr_write_b32 a1, v99\n\t"
"v_fma_f32 v99, -v17, s56, -v115\n\t"
"v_mfma_f32_16x16x32_bf16 v[124:127], a[16:19], a[192:195], v[124:127]\n\t"
"v_accvgpr_write_b32 a2, v98\n\t"
"v_accvgpr_write_b32 a3, v99\n\t"
"v_mfma_f32_16x16x32_bf16 v[128:131], a[16:19], a[196:199], v[128:131]\n\t"
"s_waitcnt lgkmcnt(1)\n\t"
"v_fma_f32 v98, -v18, s56, -v116\n\t"
"v_fma_f32 v99, -v19, s56, -v117\n\t"
"v_mfma_f32_16x16x32_bf16 v[128:131], v[204:207], a[200:203], v[128:131]\n\t"
"s_waitcnt lgkmcnt(0)\n\t"
"v_fma_f32 v204, -v6, s56, -v104\n\t"
"v_fma_f32 v205, -v7, s56, -v105\n\t"
"v_mfma_f32_16x16x32_bf16 v[128:131], a[24:27], a[204:207], v[128:131]\n\t"
"s_waitcnt vmcnt(29)\n\t"
"v_fma_f32 v206, -v8, s56, -v106\n\t"
"v_fma_f32 v207, -v9, s56, -v107\n\t"
"v_mfma_f32_4x4x4_16b_bf16 v[224:227], v[224:225], v[220:221], 0\n\t"
"v_mfma_f32_4x4x4_16b_bf16 v[228:231], v[228:229], v[220:221], 0\n\t"
"v_mfma_f32_16x16x32_bf16 v[132:135], a[32:35], a[168:171], v[132:135]\n\t"
"v_accvgpr_write_b32 a8, v98\n\t"
"v_fma_f32 v98, -v20, s56, -v118\n\t"
"v_mfma_f32_16x16x32_bf16 v[132:135], a[48:51], a[176:179], v[132:135]\n\t"
"v_cvt_pk_bf16_f32 v224, v224, v225\n\t"
"v_cvt_pk_bf16_f32 v225, v226, v227\n\t"
"v_mfma_f32_16x16x32_bf16 v[132:135], a[40:43], a[172:175], v[132:135]\n\t"
"v_cvt_pk_bf16_f32 v226, v228, v229\n\t"
"v_cvt_pk_bf16_f32 v227, v230, v231\n\t"
"v_mfma_f32_16x16x32_bf16 v[136:139], a[32:35], a[172:175], v[136:139]\n\t"
"v_accvgpr_write_b32 a9, v99\n\t"
"v_fma_f32 v99, -v21, s56, -v119\n\t"
"v_mfma_f32_16x16x32_bf16 v[136:139], a[40:43], a[176:179], v[136:139]\n\t"
"v_accvgpr_write_b32 a10, v98\n\t"
"v_accvgpr_write_b32 a11, v99\n\t"
"v_mfma_f32_16x16x32_bf16 v[136:139], a[48:51], a[180:183], v[136:139]\n\t"
"ds_write_b128 v245, v[224:227] offset:52224\n\t"
"v_mfma_f32_16x16x32_bf16 v[140:143], a[48:51], a[184:187], v[140:143]\n\t"
"v_fma_f32 v98, -v22, s56, -v120\n\t"
"v_fma_f32 v99, -v23, s56, -v121\n\t"
"v_mfma_f32_16x16x32_bf16 v[140:143], a[40:43], a[180:183], v[140:143]\n\t"
"v_accvgpr_write_b32 a16, v98\n\t"
"v_fma_f32 v98, -v24, s56, -v122\n\t"
"v_mfma_f32_16x16x32_bf16 v[140:143], a[32:35], a[176:179], v[140:143]\n\t"
"v_accvgpr_write_b32 a17, v99\n\t"
"v_fma_f32 v99, -v25, s56, -v123\n\t"
"v_mfma_f32_16x16x32_bf16 v[144:147], a[32:35], a[180:183], v[144:147]\n\t"
"v_accvgpr_write_b32 a18, v98\n\t"
"v_accvgpr_write_b32 a19, v99\n\t"
"v_mfma_f32_16x16x32_bf16 v[144:147], a[40:43], a[184:187], v[144:147]\n\t"
"v_fma_f32 v98, -v26, s56, -v124\n\t"
"v_fma_f32 v99, -v27, s56, -v125\n\t"
"v_mfma_f32_16x16x32_bf16 v[144:147], a[48:51], a[188:191], v[144:147]\n\t"
"v_accvgpr_write_b32 a24, v98\n\t"
"v_fma_f32 v98, -v28, s56, -v126\n\t"
"v_mfma_f32_16x16x32_bf16 v[148:151], a[48:51], a[192:195], v[148:151]\n\t"
"v_accvgpr_write_b32 a25, v99\n\t"
"v_fma_f32 v99, -v29, s56, -v127\n\t"
"v_mfma_f32_16x16x32_bf16 v[148:151], a[40:43], a[188:191], v[148:151]\n\t"
"v_accvgpr_write_b32 a26, v98\n\t"
"v_accvgpr_write_b32 a27, v99\n\t"
"v_mfma_f32_16x16x32_bf16 v[148:151], a[32:35], a[184:187], v[148:151]\n\t"
"v_fma_f32 v200, v100, s64, -v2\n\t"
"v_fma_f32 v201, v101, s64, -v3\n\t"
"v_mfma_f32_16x16x32_bf16 v[152:155], a[32:35], a[188:191], v[152:155]\n\t"
"v_fma_f32 v202, v102, s64, -v4\n\t"
"v_fma_f32 v203, v103, s64, -v5\n\t"
"v_mfma_f32_16x16x32_bf16 v[152:155], a[40:43], a[192:195], v[152:155]\n\t"
"v_fma_f32 v208, v104, s64, -v6\n\t"
"v_fma_f32 v209, v105, s64, -v7\n\t"
"v_mfma_f32_16x16x32_bf16 v[152:155], a[48:51], a[196:199], v[152:155]\n\t"
"v_fma_f32 v210, v106, s64, -v8\n\t"
"v_fma_f32 v211, v107, s64, -v9\n\t"
"v_mfma_f32_16x16x32_bf16 v[156:159], a[48:51], a[200:203], v[156:159]\n\t"
"v_fma_f32 v216, v108, s64, -v10\n\t"
"v_fma_f32 v217, v109, s64, -v11\n\t"
"v_mfma_f32_16x16x32_bf16 v[156:159], a[40:43], a[196:199], v[156:159]\n\t"
"v_fma_f32 v218, v110, s64, -v12\n\t"
"v_fma_f32 v219, v111, s64, -v13\n\t"
"v_mfma_f32_16x16x32_bf16 v[156:159], a[32:35], a[192:195], v[156:159]\n\t"
"v_fma_f32 v0, v2, s52, v100\n\t"
"v_fma_f32 v1, v3, s52, v101\n\t"
"v_mfma_f32_16x16x32_bf16 v[160:163], a[32:35], a[196:199], v[160:163]\n\t"
"s_waitcnt vmcnt(26)\n\t"
"v_fma_f32 v98, -v30, s56, -v128\n\t"
"v_fma_f32 v99, -v31, s56, -v129\n\t"
"v_mfma_f32_16x16x32_bf16 v[160:163], a[40:43], a[200:203], v[160:163]\n\t"
"v_accvgpr_write_b32 a32, v98\n\t"
"v_fma_f32 v98, -v32, s56, -v130\n\t"
"v_mfma_f32_16x16x32_bf16 v[160:163], a[48:51], a[204:207], v[160:163]\n\t"
"v_accvgpr_write_b32 a33, v99\n\t"
"v_fma_f32 v99, -v33, s56, -v131\n\t"
"v_mfma_f32_16x16x32_bf16 v[164:167], a[80:83], a[168:171], v[164:167]\n\t"
"v_accvgpr_write_b32 a34, v98\n\t"
"v_accvgpr_write_b32 a35, v99\n\t"
"v_mfma_f32_16x16x32_bf16 v[164:167], a[96:99], a[176:179], v[164:167]\n\t"
"v_fma_f32 v98, -v34, s56, -v132\n\t"
"v_mfma_f32_16x16x32_bf16 v[164:167], a[88:91], a[172:175], v[164:167]\n\t"
"s_waitcnt lgkmcnt(0)\n\t"
"s_barrier\n\t"
"v_mfma_f32_16x16x32_bf16 v[168:171], a[80:83], a[172:175], v[168:171]\n\t"
"ds_read_b128 a[168:171], v222 offset:0\n\t"
"v_mfma_f32_16x16x32_bf16 v[168:171], a[88:91], a[176:179], v[168:171]\n\t"
"ds_read_b128 a[172:175], v223 offset:6144\n\t"
"v_fma_f32 v99, -v35, s56, -v133\n\t"
"v_mfma_f32_16x16x32_bf16 v[168:171], a[96:99], a[180:183], v[168:171]\n\t"
"v_accvgpr_write_b32 a40, v98\n\t"
"v_fma_f32 v98, -v36, s56, -v134\n\t"
"v_mfma_f32_16x16x32_bf16 v[172:175], a[96:99], a[184:187], v[172:175]\n\t"
"v_accvgpr_write_b32 a41, v99\n\t"
"v_fma_f32 v99, -v37, s56, -v135\n\t"
"v_mfma_f32_16x16x32_bf16 v[172:175], a[88:91], a[180:183], v[172:175]\n\t"
"v_accvgpr_write_b32 a42, v98\n\t"
"v_accvgpr_write_b32 a43, v99\n\t"
"v_mfma_f32_16x16x32_bf16 v[172:175], a[80:83], a[176:179], v[172:175]\n\t"
"ds_read_b128 a[176:179], v223 offset:12288\n\t"
"v_fma_f32 v98, -v38, s56, -v136\n\t"
"v_mfma_f32_16x16x32_bf16 v[176:179], a[80:83], a[180:183], v[176:179]\n\t"
"ds_read_b128 a[180:183], v222 offset:19968\n\t"
"v_fma_f32 v99, -v39, s56, -v137\n\t"
"v_mfma_f32_16x16x32_bf16 v[176:179], a[88:91], a[184:187], v[176:179]\n\t"
"v_accvgpr_write_b32 a48, v98\n\t"
"v_fma_f32 v98, -v40, s56, -v138\n\t"
"v_mfma_f32_16x16x32_bf16 v[176:179], a[96:99], a[188:191], v[176:179]\n\t"
"Lpg2_%=:\n\t"
"s_cbranch_vccnz Lpg6_%=\n\t"
"v_accvgpr_write_b32 a49, v99\n\t"
"v_fma_f32 v99, -v41, s56, -v139\n\t"
"v_mfma_f32_16x16x32_bf16 v[180:183], a[96:99], a[192:195], v[180:183]\n\t"
"v_accvgpr_write_b32 a50, v98\n\t"
"v_accvgpr_write_b32 a51, v99\n\t"
"v_mfma_f32_16x16x32_bf16 v[180:183], a[88:91], a[188:191], v[180:183]\n\t"
"v_fma_f32 v98, -v42, s56, -v140\n\t"
"v_fma_f32 v99, -v43, s56, -v141\n\t"
"v_mfma_f32_16x16x32_bf16 v[180:183], a[80:83], a[184:187], v[180:183]\n\t"
"ds_read_b128 a[184:187], v223 offset:26112\n\t"
"v_accvgpr_write_b32 a56, v98\n\t"
"v_mfma_f32_16x16x32_bf16 v[184:187], a[80:83], a[188:191], v[184:187]\n\t"
"ds_read_b128 a[188:191], v223 offset:32256\n\t"
"v_fma_f32 v98, -v44, s56, -v142\n\t"
"v_mfma_f32_16x16x32_bf16 v[184:187], a[88:91], a[192:195], v[184:187]\n\t"
"v_accvgpr_write_b32 a57, v99\n\t"
"v_fma_f32 v99, -v45, s56, -v143\n\t"
"v_mfma_f32_16x16x32_bf16 v[184:187], a[96:99], a[196:199], v[184:187]\n\t"
"v_accvgpr_write_b32 a58, v98\n\t"
"v_accvgpr_write_b32 a59, v99\n\t"
"v_mfma_f32_16x16x32_bf16 v[188:191], a[96:99], a[200:203], v[188:191]\n\t"
"v_fma_f32 v98, -v46, s56, -v144\n\t"
"v_fma_f32 v99, -v47, s56, -v145\n\t"
"v_mfma_f32_16x16x32_bf16 v[188:191], a[88:91], a[196:199], v[188:191]\n\t"
"v_accvgpr_write_b32 a64, v98\n\t"
"v_fma_f32 v98, -v48, s56, -v146\n\t"
"v_mfma_f32_16x16x32_bf16 v[188:191], a[80:83], a[192:195], v[188:191]\n\t"
"ds_read_b128 a[192:195], v222 offset:39936\n\t"
"v_accvgpr_write_b32 a65, v99\n\t"
"v_mfma_f32_16x16x32_bf16 v[192:195], a[80:83], a[196:199], v[192:195]\n\t"
"s_waitcnt vmcnt(23) lgkmcnt(4)\n\t"
"ds_read_b128 a[196:199], v223 offset:46080\n\t"
"v_fma_f32 v99, -v49, s56, -v147\n\t"
"v_mfma_f32_16x16x32_bf16 v[192:195], a[88:91], a[200:203], v[192:195]\n\t"
"ds_read_b128 a[200:203], v223 offset:52224\n\t"
"v_accvgpr_write_b32 a66, v98\n\t"
"v_mfma_f32_16x16x32_bf16 v[192:195], a[96:99], a[204:207], v[192:195]\n\t"
"ds_read_b128 a[204:207], v222 offset:59904\n\t"
"v_accvgpr_write_b32 a67, v99\n\t"
"Lst2_%=:\n\t"
"v_mfma_f32_16x16x32_bf16 v[196:199], a[12:15], a[168:171], v[196:199]\n\t"
"s_waitcnt lgkmcnt(6)\n\t"
"v_fma_f32 v98, -v50, s56, -v148\n\t"
"v_fma_f32 v99, -v51, s56, -v149\n\t"
"v_mfma_f32_16x16x32_bf16 v[196:199], a[28:31], a[176:179], v[196:199]\n\t"
"v_accvgpr_write_b32 a72, v98\n\t"
"v_fma_f32 v98, -v52, s56, -v150\n\t"
"v_mfma_f32_16x16x32_bf16 v[196:199], a[20:23], a[172:175], v[196:199]\n\t"
"v_accvgpr_write_b32 a73, v99\n\t"
"v_fma_f32 v99, -v53, s56, -v151\n\t"
"v_mfma_f32_16x16x32_bf16 v[204:207], a[12:15], a[172:175], v[204:207]\n\t"
"s_waitcnt lgkmcnt(5)\n\t"
"v_accvgpr_write_b32 a74, v98\n\t"
"v_accvgpr_write_b32 a75, v99\n\t"
"v_mfma_f32_16x16x32_bf16 v[204:207], a[20:23], a[176:179], v[204:207]\n\t"
"v_fma_f32 v98, -v54, s56, -v152\n\t"
"v_fma_f32 v99, -v55, s56, -v153\n\t"
"v_mfma_f32_16x16x32_bf16 v[204:207], a[28:31], a[180:183], v[204:207]\n\t"
"v_accvgpr_write_b32 a80, v98\n\t"
"v_fma_f32 v98, -v56, s56, -v154\n\t"
"v_mfma_f32_16x16x32_bf16 v[212:215], a[28:31], a[184:187], v[212:215]\n\t"
"s_waitcnt lgkmcnt(4)\n\t"
"v_accvgpr_write_b32 a81, v99\n\t"
"v_fma_f32 v99, -v57, s56, -v155\n\t"
"v_mfma_f32_16x16x32_bf16 v[212:215], a[20:23], a[180:183], v[212:215]\n\t"
"v_accvgpr_write_b32 a82, v98\n\t"
"v_accvgpr_write_b32 a83, v99\n\t"
"v_mfma_f32_16x16x32_bf16 v[212:215], a[12:15], a[176:179], v[212:215]\n\t"
"v_fma_f32 v98, -v58, s56, -v156\n\t"
"v_fma_f32 v99, -v59, s56, -v157\n\t"
"v_mfma_f32_16x16x32_bf16 a[0:3], a[12:15], a[180:183], a[0:3]\n\t"
"s_waitcnt lgkmcnt(3)\n\t"
"v_accvgpr_write_b32 a88, v98\n\t"
"v_fma_f32 v98, -v60, s56, -v158\n\t"
"v_mfma_f32_16x16x32_bf16 a[0:3], a[20:23], a[184:187], a[0:3]\n\t"
"v_accvgpr_write_b32 a89, v99\n\t"
"v_fma_f32 v99, -v61, s56, -v159\n\t"
"v_mfma_f32_16x16x32_bf16 a[0:3], a[28:31], a[188:191], a[0:3]\n\t"
"v_accvgpr_write_b32 a90, v98\n\t"
"v_accvgpr_write_b32 a91, v99\n\t"
"v_mfma_f32_16x16x32_bf16 a[8:11], a[28:31], a[192:195], a[8:11]\n\t"
"s_waitcnt lgkmcnt(2)\n\t"
"v_fma_f32 v98, -v62, s56, -v160\n\t"
"v_fma_f32 v99, -v63, s56, -v161\n\t"
"v_mfma_f32_16x16x32_bf16 a[8:11], a[20:23], a[188:191], a[8:11]\n\t"
"v_accvgpr_write_b32 a96, v98\n\t"
"v_fma_f32 v98, -v64, s56, -v162\n\t"
"v_mfma_f32_16x16x32_bf16 a[8:11], a[12:15], a[184:187], a[8:11]\n\t"
"v_accvgpr_write_b32 a97, v99\n\t"
"v_fma_f32 v99, -v65, s56, -v163\n\t"
"v_mfma_f32_16x16x32_bf16 a[16:19], a[12:15], a[188:191], a[16:19]\n\t"
"s_waitcnt lgkmcnt(1)\n\t"
"v_accvgpr_write_b32 a98, v98\n\t"
"v_accvgpr_write_b32 a99, v99\n\t"
"v_mfma_f32_16x16x32_bf16 a[16:19], a[20:23], a[192:195], a[16:19]\n\t"
"v_fma_f32 v98, -v66, s56, -v164\n\t"
"v_fma_f32 v99, -v67, s56, -v165\n\t"
"v_mfma_f32_16x16x32_bf16 a[16:19], a[28:31], a[196:199], a[16:19]\n\t"
"v_accvgpr_write_b32 a104, v98\n\t"
"v_fma_f32 v98, -v68, s56, -v166\n\t"
"v_mfma_f32_16x16x32_bf16 a[24:27], a[28:31], a[200:203], a[24:27]\n\t"
"s_waitcnt lgkmcnt(0)\n\t"
"v_accvgpr_write_b32 a105, v99\n\t"
"v_fma_f32 v99, -v69, s56, -v167\n\t"
"v_mfma_f32_16x16x32_bf16 a[24:27], a[20:23], a[196:199], a[24:27]\n\t"
"v_accvgpr_write_b32 a106, v98\n\t"
"v_accvgpr_write_b32 a107, v99\n\t"
"v_mfma_f32_16x16x32_bf16 a[24:27], a[12:15], a[192:195], a[24:27]\n\t"
"v_fma_f32 v98, -v70, s56, -v168\n\t"
"v_fma_f32 v99, -v71, s56, -v169\n\t"
"v_mfma_f32_16x16x32_bf16 a[32:35], a[12:15], a[196:199], a[32:35]\n\t"
"s_waitcnt vmcnt(20)\n\t"
"v_accvgpr_write_b32 a112, v98\n\t"
"v_fma_f32 v98, -v72, s56, -v170\n\t"
"v_mfma_f32_16x16x32_bf16 a[32:35], a[20:23], a[200:203], a[32:35]\n\t"
"v_accvgpr_write_b32 a113, v99\n\t"
"v_fma_f32 v99, -v73, s56, -v171\n\t"
"v_mfma_f32_16x16x32_bf16 a[32:35], a[28:31], a[204:207], a[32:35]\n\t"
"v_accvgpr_write_b32 a114, v98\n\t"
"v_accvgpr_write_b32 a115, v99\n\t"
"v_mfma_f32_16x16x32_bf16 a[40:43], a[36:39], a[168:171], a[40:43]\n\t"
"v_fma_f32 v98, -v74, s56, -v172\n\t"
"v_fma_f32 v99, -v75, s56, -v173\n\t"
"v_mfma_f32_16x16x32_bf16 a[40:43], a[52:55], a[176:179], a[40:43]\n\t"
"v_accvgpr_write_b32 a120, v98\n\t"
"v_fma_f32 v98, -v76, s56, -v174\n\t"
"v_mfma_f32_16x16x32_bf16 a[40:43], a[44:47], a[172:175], a[40:43]\n\t"
"v_accvgpr_write_b32 a121, v99\n\t"
"v_fma_f32 v99, -v77, s56, -v175\n\t"
"v_mfma_f32_16x16x32_bf16 a[48:51], a[36:39], a[172:175], a[48:51]\n\t"
"v_accvgpr_write_b32 a122, v98\n\t"
"v_accvgpr_write_b32 a123, v99\n\t"
"v_mfma_f32_16x16x32_bf16 a[48:51], a[44:47], a[176:179], a[48:51]\n\t"
"v_fma_f32 v98, -v78, s56, -v176\n\t"
"v_fma_f32 v99, -v79, s56, -v177\n\t"
"v_mfma_f32_16x16x32_bf16 a[48:51], a[52:55], a[180:183], a[48:51]\n\t"
"v_accvgpr_write_b32 a128, v98\n\t"
"v_fma_f32 v98, -v80, s56, -v178\n\t"
"v_mfma_f32_16x16x32_bf16 a[56:59], a[52:55], a[184:187], a[56:59]\n\t"
"v_accvgpr_write_b32 a129, v99\n\t"
"v_fma_f32 v99, -v81, s56, -v179\n\t"
"v_mfma_f32_16x16x32_bf16 a[56:59], a[44:47], a[180:183], a[56:59]\n\t"
"v_accvgpr_write_b32 a130, v98\n\t"
"v_accvgpr_write_b32 a131, v99\n\t"
"v_mfma_f32_16x16x32_bf16 a[56:59], a[36:39], a[176:179], a[56:59]\n\t"
"v_fma_f32 v98, -v82, s56, -v180\n\t"
"v_fma_f32 v99, -v83, s56, -v181\n\t"
"v_mfma_f32_16x16x32_bf16 a[64:67], a[36:39], a[180:183], a[64:67]\n\t"
"v_accvgpr_write_b32 a136, v98\n\t"
"v_fma_f32 v98, -v84, s56, -v182\n\t"
"v_mfma_f32_16x16x32_bf16 a[64:67], a[44:47], a[184:187], a[64:67]\n\t"
"v_accvgpr_write_b32 a137, v99\n\t"
"v_fma_f32 v99, -v85, s56, -v183\n\t"
"v_mfma_f32_16x16x32_bf16 a[64:67], a[52:55], a[188:191], a[64:67]\n\t"
"v_accvgpr_write_b32 a138, v98\n\t"
"v_accvgpr_write_b32 a139, v99\n\t"
"v_mfma_f32_16x16x32_bf16 a[72:75], a[52:55], a[192:195], a[72:75]\n\t"
"v_fma_f32 v98, -v86, s56, -v184\n\t"
"v_fma_f32 v99, -v87, s56, -v185\n\t"
"v_mfma_f32_16x16x32_bf16 a[72:75], a[44:47], a[188:191], a[72:75]\n\t"
"v_accvgpr_write_b32 a144, v98\n\t"
"v_fma_f32 v98, -v88, s56, -v186\n\t"
"v_mfma_f32_16x16x32_bf16 a[72:75], a[36:39], a[184:187], a[72:75]\n\t"
"v_accvgpr_write_b32 a145, v99\n\t"
"v_fma_f32 v99, -v89, s56, -v187\n\t"
"v_mfma_f32_16x16x32_bf16 a[80:83], a[36:39], a[188:191], a[80:83]\n\t"
"v_accvgpr_write_b32 a146, v98\n\t"
"v_accvgpr_write_b32 a147, v99\n\t"
"v_mfma_f32_16x16x32_bf16 a[80:83], a[44:47], a[192:195], a[80:83]\n\t"
"v_fma_f32 v98, -v90, s56, -v188\n\t"
"v_fma_f32 v99, -v91, s56, -v189\n\t"
"v_mfma_f32_16x16x32_bf16 a[80:83], a[52:55], a[196:199], a[80:83]\n\t"
"v_accvgpr_write_b32 a152, v98\n\t"
"v_fma_f32 v98, -v92, s56, -v190\n\t"
"v_mfma_f32_16x16x32_bf16 a[88:91], a[52:55], a[200:203], a[88:91]\n\t"
"v_accvgpr_write_b32 a153, v99\n\t"
"v_fma_f32 v99, -v93, s56, -v191\n\t"
"v_mfma_f32_16x16x32_bf16 a[88:91], a[44:47], a[196:199], a[88:91]\n\t"
"v_accvgpr_write_b32 a154, v98\n\t"
"v_accvgpr_write_b32 a155, v99\n\t"
"v_mfma_f32_16x16x32_bf16 a[88:91], a[36:39], a[192:195], a[88:91]\n\t"
"v_fma_f32 v98, -v94, s56, -v192\n\t"
"v_fma_f32 v99, -v95, s56, -v193\n\t"
"v_mfma_f32_16x16x32_bf16 a[96:99], a[36:39], a[196:199], a[96:99]\n\t"
"s_waitcnt vmcnt(17)\n\t"
"v_accvgpr_write_b32 a160, v98\n\t"
"v_fma_f32 v98, -v96, s56, -v194\n\t"
"v_mfma_f32_16x16x32_bf16 a[96:99], a[44:47], a[200:203], a[96:99]\n\t"
"v_accvgpr_write_b32 a161, v99\n\t"
"v_fma_f32 v99, -v97, s56, -v195\n\t"
"v_mfma_f32_16x16x32_bf16 a[96:99], a[52:55], a[204:207], a[96:99]\n\t"
"v_accvgpr_write_b32 a162, v98\n\t"
"v_accvgpr_write_b32 a163, v99\n\t"
"v_mfma_f32_16x16x32_bf16 a[104:107], a[84:87], a[168:171], a[104:107]\n\t"
"ds_read_b128 a[168:171], v222 offset:256\n\t"
"v_fma_f32 v98, v112, s64, -v14\n\t"
"v_mfma_f32_16x16x32_bf16 a[104:107], a[100:103], a[176:179], a[104:107]\n\t"
"v_fma_f32 v99, v113, s64, -v15\n\t"
"v_accvgpr_write_b32 a4, v98\n\t"
"v_mfma_f32_16x16x32_bf16 a[104:107], a[92:95], a[172:175], a[104:107]\n\t"
"v_fma_f32 v98, v114, s64, -v16\n\t"
"v_accvgpr_write_b32 a5, v99\n\t"
"v_mfma_f32_16x16x32_bf16 a[112:115], a[84:87], a[172:175], a[112:115]\n\t"
"ds_read_b128 a[172:175], v223 offset:6400\n\t"
"v_fma_f32 v99, v115, s64, -v17\n\t"
"v_mfma_f32_16x16x32_bf16 a[112:115], a[92:95], a[176:179], a[112:115]\n\t"
"v_accvgpr_write_b32 a6, v98\n\t"
"v_accvgpr_write_b32 a7, v99\n\t"
"v_mfma_f32_16x16x32_bf16 a[112:115], a[100:103], a[180:183], a[112:115]\n\t"
"v_fma_f32 v98, v116, s64, -v18\n\t"
"v_fma_f32 v99, v117, s64, -v19\n\t"
"v_mfma_f32_16x16x32_bf16 a[120:123], a[100:103], a[184:187], a[120:123]\n\t"
"v_accvgpr_write_b32 a12, v98\n\t"
"v_fma_f32 v98, v118, s64, -v20\n\t"
"v_mfma_f32_16x16x32_bf16 a[120:123], a[92:95], a[180:183], a[120:123]\n\t"
"v_accvgpr_write_b32 a13, v99\n\t"
"v_fma_f32 v99, v119, s64, -v21\n\t"
"v_mfma_f32_16x16x32_bf16 a[120:123], a[84:87], a[176:179], a[120:123]\n\t"
"ds_read_b128 a[176:179], v223 offset:12544\n\t"
"v_accvgpr_write_b32 a14, v98\n\t"
"v_mfma_f32_16x16x32_bf16 a[128:131], a[84:87], a[180:183], a[128:131]\n\t"
"ds_read_b128 a[180:183], v222 offset:20224\n\t"
"v_accvgpr_write_b32 a15, v99\n\t"
"v_mfma_f32_16x16x32_bf16 a[128:131], a[92:95], a[184:187], a[128:131]\n\t"
"v_fma_f32 v98, v120, s64, -v22\n\t"
"v_fma_f32 v99, v121, s64, -v23\n\t"
"v_mfma_f32_16x16x32_bf16 a[128:131], a[100:103], a[188:191], a[128:131]\n\t"
"v_accvgpr_write_b32 a20, v98\n\t"
"v_fma_f32 v98, v122, s64, -v24\n\t"
"v_mfma_f32_16x16x32_bf16 a[136:139], a[100:103], a[192:195], a[136:139]\n\t"
"v_accvgpr_write_b32 a21, v99\n\t"
"v_fma_f32 v99, v123, s64, -v25\n\t"
"v_mfma_f32_16x16x32_bf16 a[136:139], a[92:95], a[188:191], a[136:139]\n\t"
"v_accvgpr_write_b32 a22, v98\n\t"
"v_accvgpr_write_b32 a23, v99\n\t"
"v_mfma_f32_16x16x32_bf16 a[136:139], a[84:87], a[184:187], a[136:139]\n\t"
"ds_read_b128 a[184:187], v223 offset:26368\n\t"
"v_fma_f32 v98, v124, s64, -v26\n\t"
"v_mfma_f32_16x16x32_bf16 a[144:147], a[84:87], a[188:191], a[144:147]\n\t"
"ds_read_b128 a[188:191], v223 offset:32512\n\t"
"v_fma_f32 v99, v125, s64, -v27\n\t"
"v_mfma_f32_16x16x32_bf16 a[144:147], a[92:95], a[192:195], a[144:147]\n\t"
"v_accvgpr_write_b32 a28, v98\n\t"
"v_fma_f32 v98, v126, s64, -v28\n\t"
"v_mfma_f32_16x16x32_bf16 a[144:147], a[100:103], a[196:199], a[144:147]\n\t"
"v_accvgpr_write_b32 a29, v99\n\t"
"v_fma_f32 v99, v127, s64, -v29\n\t"
"v_mfma_f32_16x16x32_bf16 a[152:155], a[100:103], a[200:203], a[152:155]\n\t"
"v_accvgpr_write_b32 a30, v98\n\t"
"v_accvgpr_write_b32 a31, v99\n\t"
"v_mfma_f32_16x16x32_bf16 a[152:155], a[92:95], a[196:199], a[152:155]\n\t"
"v_fma_f32 v98, v128, s64, -v30\n\t"
"v_fma_f32 v99, v129, s64, -v31\n\t"
"v_mfma_f32_16x16x32_bf16 a[152:155], a[84:87], a[192:195], a[152:155]\n\t"
"ds_read_b128 a[192:195], v222 offset:40192\n\t"
"v_accvgpr_write_b32 a36, v98\n\t"
"v_mfma_f32_16x16x32_bf16 a[160:163], a[84:87], a[196:199], a[160:163]\n\t"
"s_waitcnt vmcnt(14) lgkmcnt(4)\n\t"
"ds_read_b128 a[196:199], v223 offset:46336\n\t"
"v_fma_f32 v98, v130, s64, -v32\n\t"
"v_mfma_f32_16x16x32_bf16 a[160:163], a[92:95], a[200:203], a[160:163]\n\t"
"ds_read_b128 a[200:203], v223 offset:52480\n\t"
"v_accvgpr_write_b32 a37, v99\n\t"
"v_mfma_f32_16x16x32_bf16 a[160:163], a[100:103], a[204:207], a[160:163]\n\t"
"ds_read_b128 a[204:207], v222 offset:60160\n\t"
"v_fma_f32 v99, v131, s64, -v33\n\t"
"v_mfma_f32_16x16x32_bf16 v[200:203], a[140:143], a[168:171], v[200:203]\n\t"
"s_waitcnt lgkmcnt(6)\n\t"
"v_accvgpr_write_b32 a38, v98\n\t"
"v_accvgpr_write_b32 a39, v99\n\t"
"v_mfma_f32_16x16x32_bf16 v[200:203], a[156:159], a[176:179], v[200:203]\n\t"
"v_fma_f32 v98, v132, s64, -v34\n\t"
"v_fma_f32 v99, v133, s64, -v35\n\t"
"v_mfma_f32_16x16x32_bf16 v[200:203], a[148:151], a[172:175], v[200:203]\n\t"
"s_add_u32 m0, s25, 896\n\t"
"v_accvgpr_write_b32 a44, v98\n\t"
"v_fma_f32 v98, v134, s64, -v36\n\t"
"v_mfma_f32_16x16x32_bf16 v[208:211], a[140:143], a[172:175], v[208:211]\n\t"
"s_waitcnt lgkmcnt(5)\n\t"
"v_accvgpr_write_b32 a45, v99\n\t"
"v_fma_f32 v99, v135, s64, -v37\n\t"
"v_mfma_f32_16x16x32_bf16 v[208:211], a[148:151], a[176:179], v[208:211]\n\t"
"v_accvgpr_write_b32 a46, v98\n\t"
"v_mfma_f32_16x16x32_bf16 v[208:211], a[156:159], a[180:183], v[208:211]\n\t"
"s_waitcnt lgkmcnt(0)\n\t"
"s_barrier\n\t"
"v_mfma_f32_16x16x32_bf16 v[216:219], a[156:159], a[184:187], v[216:219]\n\t"
"v_accvgpr_write_b32 a47, v99\n\t"
"v_mfma_f32_16x16x32_bf16 v[216:219], a[148:151], a[180:183], v[216:219]\n\t"
"buffer_load_dwordx4 v254, s[12:15], 0 offen offset:128 nt lds\n\t"
"v_mfma_f32_16x16x32_bf16 v[216:219], a[140:143], a[176:179], v[216:219]\n\t"
"s_add_u32 m0, s26, 896\n\t"
"ds_read_b64_tr_b16 v[232:233], v246 offset:512\n\t"
"v_fma_f32 v98, v136, s64, -v38\n\t"
"v_mfma_f32_16x16x32_bf16 a[4:7], a[140:143], a[180:183], a[4:7]\n\t"
"ds_read_b64_tr_b16 v[236:237], v255 offset:512\n\t"
"v_fma_f32 v99, v137, s64, -v39\n\t"
"v_mfma_f32_16x16x32_bf16 a[4:7], a[148:151], a[184:187], a[4:7]\n\t"
"v_accvgpr_write_b32 a52, v98\n\t"
"v_fma_f32 v98, v138, s64, -v40\n\t"
"v_mfma_f32_16x16x32_bf16 a[4:7], a[156:159], a[188:191], a[4:7]\n\t"
"s_mov_b64 exec, s[36:37]\n\t"
"buffer_load_dwordx4 v254, s[12:15], s27 offen offset:128 nt lds\n\t"
"s_mov_b64 exec, -1\n\t"
"s_add_u32 m0, s24, 2432\n\t"
"s_add_u32 s28, s21, 3072\n\t"
"v_mfma_f32_16x16x32_bf16 a[12:15], a[156:159], a[192:195], a[12:15]\n\t"
"buffer_load_dwordx4 v247, s[12:15], s28 offen offset:128 nt lds\n\t"
"v_mfma_f32_16x16x32_bf16 a[12:15], a[148:151], a[188:191], a[12:15]\n\t"
"s_add_u32 m0, s24, 8576\n\t"
"s_add_u32 s28, s21, 199680\n\t"
"v_accvgpr_write_b32 a53, v99\n\t"
"v_mfma_f32_16x16x32_bf16 a[12:15], a[140:143], a[184:187], a[12:15]\n\t"
"buffer_load_dwordx4 v247, s[12:15], s28 offen offset:128 nt lds\n\t"
"s_add_u32 m0, s24, 14720\n\t"
"s_add_u32 s28, s21, 396288\n\t"
"v_mfma_f32_16x16x32_bf16 a[20:23], a[140:143], a[188:191], a[20:23]\n\t"
"buffer_load_dwordx4 v247, s[12:15], s28 offen offset:128 nt lds\n\t"
"v_mfma_f32_16x16x32_bf16 a[20:23], a[148:151], a[192:195], a[20:23]\n\t"
"s_add_u32 m0, s24, 22400\n\t"
"s_add_u32 s28, s21, 592896\n\t"
"v_fma_f32 v99, v139, s64, -v41\n\t"
"v_mfma_f32_16x16x32_bf16 a[20:23], a[156:159], a[196:199], a[20:23]\n\t"
"buffer_load_dwordx4 v247, s[12:15], s28 offen offset:128 nt lds\n\t"
"s_add_u32 m0, s24, 28544\n\t"
"s_add_u32 s28, s21, 789504\n\t"
"v_mfma_f32_16x16x32_bf16 a[28:31], a[156:159], a[200:203], a[28:31]\n\t"
"buffer_load_dwordx4 v247, s[12:15], s28 offen offset:128 nt lds\n\t"
"v_mfma_f32_16x16x32_bf16 a[28:31], a[148:151], a[196:199], a[28:31]\n\t"
"s_add_u32 m0, s24, 34688\n\t"
"s_add_u32 s28, s21, 986112\n\t"
"v_accvgpr_write_b32 a54, v98\n\t"
"v_mfma_f32_16x16x32_bf16 a[28:31], a[140:143], a[192:195], a[28:31]\n\t"
"v_accvgpr_write_b32 a55, v99\n\t"
"v_fma_f32 v98, v140, s64, -v42\n\t"
"v_mfma_f32_16x16x32_bf16 a[36:39], a[140:143], a[196:199], a[36:39]\n\t"
"s_waitcnt lgkmcnt(1)\n\t"
"v_fma_f32 v99, v141, s64, -v43\n\t"
"v_accvgpr_write_b32 a60, v98\n\t"
"v_mfma_f32_16x16x32_bf16 a[36:39], a[148:151], a[200:203], a[36:39]\n\t"
"s_waitcnt lgkmcnt(0)\n\t"
"v_fma_f32 v98, v142, s64, -v44\n\t"
"v_accvgpr_write_b32 a61, v99\n\t"
"v_mfma_f32_16x16x32_bf16 a[36:39], a[156:159], a[204:207], a[36:39]\n\t"
"buffer_load_dwordx4 v247, s[12:15], s28 offen offset:128 nt lds\n\t"
"v_mfma_f32_4x4x4_16b_bf16 v[232:235], v[232:233], v[220:221], 0\n\t"
"s_waitcnt vmcnt(19)\n\t"
"v_mfma_f32_4x4x4_16b_bf16 v[236:239], v[236:237], v[220:221], 0\n\t"
"v_mfma_f32_16x16x32_bf16 a[44:47], v[240:243], a[168:171], a[44:47]\n\t"
"v_fma_f32 v99, v143, s64, -v45\n\t"
"v_accvgpr_write_b32 a62, v98\n\t"
"v_mfma_f32_16x16x32_bf16 a[44:47], v[248:251], a[176:179], a[44:47]\n\t"
"v_accvgpr_write_b32 a63, v99\n\t"
"v_fma_f32 v98, v144, s64, -v46\n\t"
"v_mfma_f32_16x16x32_bf16 a[44:47], a[208:211], a[172:175], a[44:47]\n\t"
"v_fma_f32 v99, v145, s64, -v47\n\t"
"v_cvt_pk_bf16_f32 v232, v232, v233\n\t"
"v_mfma_f32_16x16x32_bf16 a[52:55], v[240:243], a[172:175], a[52:55]\n\t"
"v_cvt_pk_bf16_f32 v233, v234, v235\n\t"
"v_cvt_pk_bf16_f32 v234, v236, v237\n\t"
"v_mfma_f32_16x16x32_bf16 a[52:55], a[208:211], a[176:179], a[52:55]\n\t"
"v_cvt_pk_bf16_f32 v235, v238, v239\n\t"
"v_accvgpr_write_b32 a68, v98\n\t"
"v_mfma_f32_16x16x32_bf16 a[52:55], v[248:251], a[180:183], a[52:55]\n\t"
"ds_write_b128 v245, v[232:235] offset:512\n\t"
"v_fma_f32 v98, v146, s64, -v48\n\t"
"v_mfma_f32_16x16x32_bf16 a[60:63], v[248:251], a[184:187], a[60:63]\n\t"
"v_accvgpr_write_b32 a69, v99\n\t"
"v_fma_f32 v99, v147, s64, -v49\n\t"
"v_mfma_f32_16x16x32_bf16 a[60:63], a[208:211], a[180:183], a[60:63]\n\t"
"v_accvgpr_write_b32 a70, v98\n\t"
"v_accvgpr_write_b32 a71, v99\n\t"
"v_mfma_f32_16x16x32_bf16 a[60:63], v[240:243], a[176:179], a[60:63]\n\t"
"ds_read_b64_tr_b16 v[228:229], v246 offset:6656\n\t"
"v_fma_f32 v98, v148, s64, -v50\n\t"
"v_fma_f32 v99, v149, s64, -v51\n\t"
"v_mfma_f32_16x16x32_bf16 a[68:71], v[240:243], a[180:183], a[68:71]\n\t"
"ds_read_b64_tr_b16 v[224:225], v255 offset:6656\n\t"
"v_accvgpr_write_b32 a76, v98\n\t"
"v_mfma_f32_16x16x32_bf16 a[68:71], a[208:211], a[184:187], a[68:71]\n\t"
"v_fma_f32 v98, v150, s64, -v52\n\t"
"v_accvgpr_write_b32 a77, v99\n\t"
"v_mfma_f32_16x16x32_bf16 a[68:71], v[248:251], a[188:191], a[68:71]\n\t"
"v_fma_f32 v99, v151, s64, -v53\n\t"
"v_accvgpr_write_b32 a78, v98\n\t"
"v_accvgpr_write_b32 a79, v99\n\t"
"v_fma_f32 v98, v152, s64, -v54\n\t"
"v_fma_f32 v99, v153, s64, -v55\n\t"
"v_mfma_f32_16x16x32_bf16 a[76:79], v[248:251], a[192:195], a[76:79]\n\t"
"v_accvgpr_write_b32 a84, v98\n\t"
"v_fma_f32 v98, v154, s64, -v56\n\t"
"v_mfma_f32_16x16x32_bf16 a[76:79], a[208:211], a[188:191], a[76:79]\n\t"
"v_accvgpr_write_b32 a85, v99\n\t"
"v_fma_f32 v99, v155, s64, -v57\n\t"
"v_mfma_f32_16x16x32_bf16 a[76:79], v[240:243], a[184:187], a[76:79]\n\t"
"v_accvgpr_write_b32 a86, v98\n\t"
"v_accvgpr_write_b32 a87, v99\n\t"
"v_fma_f32 v98, v156, s64, -v58\n\t"
"v_fma_f32 v99, v157, s64, -v59\n\t"
"v_mfma_f32_16x16x32_bf16 a[84:87], v[240:243], a[188:191], a[84:87]\n\t"
"v_accvgpr_write_b32 a92, v98\n\t"
"v_fma_f32 v98, v158, s64, -v60\n\t"
"v_mfma_f32_16x16x32_bf16 a[84:87], a[208:211], a[192:195], a[84:87]\n\t"
"v_accvgpr_write_b32 a93, v99\n\t"
"v_fma_f32 v99, v159, s64, -v61\n\t"
"v_mfma_f32_16x16x32_bf16 a[84:87], v[248:251], a[196:199], a[84:87]\n\t"
"v_accvgpr_write_b32 a94, v98\n\t"
"v_accvgpr_write_b32 a95, v99\n\t"
"v_fma_f32 v98, v160, s64, -v62\n\t"
"v_fma_f32 v99, v161, s64, -v63\n\t"
"v_mfma_f32_16x16x32_bf16 a[92:95], v[248:251], a[200:203], a[92:95]\n\t"
"v_accvgpr_write_b32 a100, v98\n\t"
"v_fma_f32 v98, v162, s64, -v64\n\t"
"v_mfma_f32_16x16x32_bf16 a[92:95], a[208:211], a[196:199], a[92:95]\n\t"
"v_accvgpr_write_b32 a101, v99\n\t"
"v_fma_f32 v99, v163, s64, -v65\n\t"
"v_mfma_f32_16x16x32_bf16 a[92:95], v[240:243], a[192:195], a[92:95]\n\t"
"v_accvgpr_write_b32 a102, v98\n\t"
"v_accvgpr_write_b32 a103, v99\n\t"
"s_nop 1\n\t"
"v_mfma_f32_16x16x32_bf16 a[100:103], v[240:243], a[196:199], a[100:103]\n\t"
"buffer_load_dwordx4 v[240:243], off, s[80:83], s20 offset:2048 sc1\n\t"
"v_mfma_f32_16x16x32_bf16 a[100:103], a[208:211], a[200:203], a[100:103]\n\t"
"s_waitcnt lgkmcnt(0)\n\t"
"v_fma_f32 v98, v164, s64, -v66\n\t"
"v_fma_f32 v99, v165, s64, -v67\n\t"
"v_mfma_f32_16x16x32_bf16 a[100:103], v[248:251], a[204:207], a[100:103]\n\t"
"v_accvgpr_write_b32 a108, v98\n\t"
"v_fma_f32 v98, v166, s64, -v68\n\t"
"v_accvgpr_write_b32 a109, v99\n\t"
"v_fma_f32 v99, v167, s64, -v69\n\t"
"v_accvgpr_write_b32 a110, v98\n\t"
"v_accvgpr_write_b32 a111, v99\n\t"
"v_mfma_f32_4x4x4_16b_bf16 v[228:231], v[228:229], v[220:221], 0\n\t"
"s_waitcnt vmcnt(17)\n\t"
"v_fma_f32 v98, v168, s64, -v70\n\t"
"v_fma_f32 v99, v169, s64, -v71\n\t"
"v_mfma_f32_4x4x4_16b_bf16 v[224:227], v[224:225], v[220:221], 0\n\t"
"v_mfma_f32_16x16x32_bf16 a[108:111], a[212:215], a[168:171], a[108:111]\n\t"
"ds_read_b128 a[168:171], v222 offset:1536\n\t"
"v_accvgpr_write_b32 a116, v98\n\t"
"v_mfma_f32_16x16x32_bf16 a[108:111], a[220:223], a[176:179], a[108:111]\n\t"
"v_fma_f32 v98, v170, s64, -v72\n\t"
"v_accvgpr_write_b32 a117, v99\n\t"
"v_mfma_f32_16x16x32_bf16 a[108:111], a[216:219], a[172:175], a[108:111]\n\t"
"v_fma_f32 v99, v171, s64, -v73\n\t"
"v_accvgpr_write_b32 a118, v98\n\t"
"v_accvgpr_write_b32 a119, v99\n\t"
"v_fma_f32 v98, v172, s64, -v74\n\t"
"v_fma_f32 v99, v173, s64, -v75\n\t"
"v_mfma_f32_16x16x32_bf16 a[116:119], a[212:215], a[172:175], a[116:119]\n\t"
"ds_read_b128 a[172:175], v223 offset:7680\n\t"
"v_accvgpr_write_b32 a124, v98\n\t"
"v_mfma_f32_16x16x32_bf16 a[116:119], a[216:219], a[176:179], a[116:119]\n\t"
"v_fma_f32 v98, v174, s64, -v76\n\t"
"v_accvgpr_write_b32 a125, v99\n\t"
"v_mfma_f32_16x16x32_bf16 a[116:119], a[220:223], a[180:183], a[116:119]\n\t"
"v_fma_f32 v99, v175, s64, -v77\n\t"
"v_accvgpr_write_b32 a126, v98\n\t"
"v_accvgpr_write_b32 a127, v99\n\t"
"v_fma_f32 v98, v176, s64, -v78\n\t"
"v_fma_f32 v99, v177, s64, -v79\n\t"
"v_cvt_pk_bf16_f32 v228, v228, v229\n\t"
"v_cvt_pk_bf16_f32 v229, v230, v231\n\t"
"v_cvt_pk_bf16_f32 v230, v224, v225\n\t"
"v_cvt_pk_bf16_f32 v231, v226, v227\n\t"
"v_mfma_f32_16x16x32_bf16 a[124:127], a[220:223], a[184:187], a[124:127]\n\t"
"v_accvgpr_write_b32 a132, v98\n\t"
"v_fma_f32 v98, v178, s64, -v80\n\t"
"v_mfma_f32_16x16x32_bf16 a[124:127], a[216:219], a[180:183], a[124:127]\n\t"
"v_accvgpr_write_b32 a133, v99\n\t"
"v_fma_f32 v99, v179, s64, -v81\n\t"
"v_mfma_f32_16x16x32_bf16 a[124:127], a[212:215], a[176:179], a[124:127]\n\t"
"ds_write_b128 v245, v[228:231] offset:6656\n\t"
"v_accvgpr_write_b32 a134, v98\n\t"
"v_accvgpr_write_b32 a135, v99\n\t"
"v_fma_f32 v98, v180, s64, -v82\n\t"
"v_fma_f32 v99, v181, s64, -v83\n\t"
"v_mfma_f32_16x16x32_bf16 a[132:135], a[212:215], a[180:183], a[132:135]\n\t"
"ds_read_b128 a[176:179], v223 offset:13824\n\t"
"v_mfma_f32_16x16x32_bf16 a[132:135], a[216:219], a[184:187], a[132:135]\n\t"
"ds_read_b128 a[180:183], v222 offset:21504\n\t"
"v_accvgpr_write_b32 a140, v98\n\t"
"v_mfma_f32_16x16x32_bf16 a[132:135], a[220:223], a[188:191], a[132:135]\n\t"
"ds_read_b64_tr_b16 v[236:237], v246 offset:12800\n\t"
"v_fma_f32 v98, v182, s64, -v84\n\t"
"v_accvgpr_write_b32 a141, v99\n\t"
"v_fma_f32 v99, v183, s64, -v85\n\t"
"v_accvgpr_write_b32 a142, v98\n\t"
"v_accvgpr_write_b32 a143, v99\n\t"
"v_fma_f32 v98, v184, s64, -v86\n\t"
"v_fma_f32 v99, v185, s64, -v87\n\t"
"v_mfma_f32_16x16x32_bf16 a[140:143], a[220:223], a[192:195], a[140:143]\n\t"
"s_mov_b32 s20, 73728\n\t"
"ds_read_b64_tr_b16 v[232:233], v255 offset:12800\n\t"
"v_accvgpr_write_b32 a148, v98\n\t"
"v_mfma_f32_16x16x32_bf16 a[140:143], a[216:219], a[188:191], a[140:143]\n\t"
"v_fma_f32 v98, v186, s64, -v88\n\t"
"v_accvgpr_write_b32 a149, v99\n\t"
"v_mfma_f32_16x16x32_bf16 a[140:143], a[212:215], a[184:187], a[140:143]\n\t"
"ds_read_b128 a[184:187], v223 offset:27648\n\t"
"v_fma_f32 v99, v187, s64, -v89\n\t"
"v_accvgpr_write_b32 a150, v98\n\t"
"v_accvgpr_write_b32 a151, v99\n\t"
"v_fma_f32 v98, v188, s64, -v90\n\t"
"v_fma_f32 v99, v189, s64, -v91\n\t"
"v_mfma_f32_16x16x32_bf16 a[148:151], a[212:215], a[188:191], a[148:151]\n\t"
"ds_read_b128 a[188:191], v223 offset:33792\n\t"
"v_accvgpr_write_b32 a156, v98\n\t"
"v_mfma_f32_16x16x32_bf16 a[148:151], a[216:219], a[192:195], a[148:151]\n\t"
"v_fma_f32 v98, v190, s64, -v92\n\t"
"v_accvgpr_write_b32 a157, v99\n\t"
"v_mfma_f32_16x16x32_bf16 a[148:151], a[220:223], a[196:199], a[148:151]\n\t"
"v_fma_f32 v99, v191, s64, -v93\n\t"
"v_accvgpr_write_b32 a158, v98\n\t"
"v_accvgpr_write_b32 a159, v99\n\t"
"v_fma_f32 v98, v192, s64, -v94\n\t"
"v_fma_f32 v99, v193, s64, -v95\n\t"
"v_mfma_f32_16x16x32_bf16 a[156:159], a[220:223], a[200:203], a[156:159]\n\t"
"s_waitcnt lgkmcnt(2)\n\t"
"v_accvgpr_write_b32 a164, v98\n\t"
"v_fma_f32 v98, v194, s64, -v96\n\t"
"v_mfma_f32_16x16x32_bf16 a[156:159], a[216:219], a[196:199], a[156:159]\n\t"
"Lpg3_%=:\n\t"
"s_cbranch_vccnz Lpg7_%=\n\t"
"v_accvgpr_write_b32 a165, v99\n\t"
"v_fma_f32 v99, v195, s64, -v97\n\t"
"v_mfma_f32_16x16x32_bf16 a[156:159], a[212:215], a[192:195], a[156:159]\n\t"
"ds_read_b128 a[192:195], v222 offset:41472\n\t"
"v_accvgpr_write_b32 a166, v98\n\t"
"v_accvgpr_write_b32 a167, v99\n\t"
"s_nop 1\n\t"
"v_mfma_f32_16x16x32_bf16 a[164:167], a[212:215], a[196:199], a[164:167]\n\t"
"buffer_load_dwordx4 a[208:211], off, s[80:83], s20 offset:0 sc1\n\t"
"v_mfma_f32_16x16x32_bf16 a[164:167], a[216:219], a[200:203], a[164:167]\n\t"
"buffer_load_dwordx4 v[248:251], off, s[80:83], s20 offset:1024 sc1\n\t"
"v_mfma_f32_16x16x32_bf16 a[164:167], a[220:223], a[204:207], a[164:167]\n\t"
"ds_read_b128 a[196:199], v223 offset:47616\n\t"
"v_fma_f32 v100, -v100, s66, v2\n\t"
"v_fma_f32 v2, v4, s52, v102\n\t"
"v_fma_f32 v101, -v101, s66, v3\n\t"
"v_fma_f32 v3, v5, s52, v103\n\t"
"v_fma_f32 v102, -v102, s66, v4\n\t"
"v_fma_f32 v103, -v103, s66, v5\n\t"
"v_mfma_f32_4x4x4_16b_bf16 v[236:239], v[236:237], v[220:221], 0\n\t"
"s_waitcnt vmcnt(16)\n\t"
"v_mfma_f32_4x4x4_16b_bf16 v[232:235], v[232:233], v[220:221], 0\n\t"
"v_fma_f32 v4, v6, s52, v104\n\t"
"v_fma_f32 v5, v7, s52, v105\n\t"
"Lst3_%=:\n\t"
"v_mfma_f32_16x16x32_bf16 v[0:3], a[224:227], a[168:171], v[0:3]\n\t"
"ds_read_b128 a[200:203], v223 offset:53760\n\t"
"v_fma_f32 v104, -v104, s66, v6\n\t"
"v_mfma_f32_16x16x32_bf16 v[0:3], a[232:235], a[176:179], v[0:3]\n\t"
"ds_read_b128 a[204:207], v222 offset:61440\n\t"
"v_fma_f32 v6, v8, s52, v106\n\t"
"v_mfma_f32_16x16x32_bf16 v[0:3], a[228:231], a[172:175], v[0:3]\n\t"
"buffer_load_dwordx4 a[212:215], off, s[80:83], s20 offset:2048 sc1\n\t"
"v_fma_f32 v105, -v105, s66, v7\n\t"
"v_fma_f32 v7, v9, s52, v107\n\t"
"v_fma_f32 v106, -v106, s66, v8\n\t"
"v_fma_f32 v107, -v107, s66, v9\n\t"
"v_fma_f32 v8, v10, s52, v108\n\t"
"v_fma_f32 v9, v11, s52, v109\n\t"
"s_mov_b32 s20, 86016\n\t"
"v_mfma_f32_16x16x32_bf16 v[4:7], a[224:227], a[172:175], v[4:7]\n\t"
"buffer_load_dwordx4 a[216:219], off, s[80:83], s20 offset:0 sc1\n\t"
"v_mfma_f32_16x16x32_bf16 v[4:7], a[228:231], a[176:179], v[4:7]\n\t"
"buffer_load_dwordx4 a[220:223], off, s[80:83], s20 offset:1024 sc1\n\t"
"v_mfma_f32_16x16x32_bf16 v[4:7], a[232:235], a[180:183], v[4:7]\n\t"
"v_fma_f32 v108, -v108, s66, v10\n\t"
"v_fma_f32 v10, v12, s52, v110\n\t"
"v_fma_f32 v109, -v109, s66, v11\n\t"
"v_fma_f32 v11, v13, s52, v111\n\t"
"v_fma_f32 v110, -v110, s66, v12\n\t"
"v_fma_f32 v111, -v111, s66, v13\n\t"
"v_cvt_pk_bf16_f32 v236, v236, v237\n\t"
"v_cvt_pk_bf16_f32 v237, v238, v239\n\t"
"v_fma_f32 v12, v14, s52, v112\n\t"
"v_fma_f32 v13, v15, s52, v113\n\t"
"v_cvt_pk_bf16_f32 v238, v232, v233\n\t"
"v_cvt_pk_bf16_f32 v239, v234, v235\n\t"
"s_waitcnt lgkmcnt(5)\n\t"
"v_mfma_f32_16x16x32_bf16 v[8:11], a[232:235], a[184:187], v[8:11]\n\t"
"v_fma_f32 v112, -v112, s66, v14\n\t"
"v_fma_f32 v14, v16, s52, v114\n\t"
"v_mfma_f32_16x16x32_bf16 v[8:11], a[228:231], a[180:183], v[8:11]\n\t"
"v_fma_f32 v113, -v113, s66, v15\n\t"
"v_fma_f32 v15, v17, s52, v115\n\t"
"v_mfma_f32_16x16x32_bf16 v[8:11], a[224:227], a[176:179], v[8:11]\n\t"
"ds_write_b128 v245, v[236:239] offset:12800\n\t"
"v_fma_f32 v114, -v114, s66, v16\n\t"
"v_fma_f32 v115, -v115, s66, v17\n\t"
"v_fma_f32 v16, v18, s52, v116\n\t"
"v_fma_f32 v17, v19, s52, v117\n\t"
"s_waitcnt lgkmcnt(5)\n\t"
"v_mfma_f32_16x16x32_bf16 v[12:15], a[224:227], a[180:183], v[12:15]\n\t"
"ds_read_b64_tr_b16 v[224:225], v246 offset:20480\n\t"
"v_fma_f32 v116, -v116, s66, v18\n\t"
"v_mfma_f32_16x16x32_bf16 v[12:15], a[228:231], a[184:187], v[12:15]\n\t"
"ds_read_b64_tr_b16 v[228:229], v255 offset:20480\n\t"
"v_fma_f32 v18, v20, s52, v118\n\t"
"v_mfma_f32_16x16x32_bf16 v[12:15], a[232:235], a[188:191], v[12:15]\n\t"
"v_fma_f32 v117, -v117, s66, v19\n\t"
"v_fma_f32 v19, v21, s52, v119\n\t"
"v_fma_f32 v118, -v118, s66, v20\n\t"
"v_fma_f32 v119, -v119, s66, v21\n\t"
"v_fma_f32 v20, v22, s52, v120\n\t"
"v_fma_f32 v21, v23, s52, v121\n\t"
"s_waitcnt lgkmcnt(6)\n\t"
"v_mfma_f32_16x16x32_bf16 v[16:19], a[232:235], a[192:195], v[16:19]\n\t"
"v_fma_f32 v120, -v120, s66, v22\n\t"
"v_fma_f32 v22, v24, s52, v122\n\t"
"v_mfma_f32_16x16x32_bf16 v[16:19], a[228:231], a[188:191], v[16:19]\n\t"
"v_fma_f32 v121, -v121, s66, v23\n\t"
"v_fma_f32 v23, v25, s52, v123\n\t"
"v_mfma_f32_16x16x32_bf16 v[16:19], a[224:227], a[184:187], v[16:19]\n\t"
"v_fma_f32 v122, -v122, s66, v24\n\t"
"v_fma_f32 v123, -v123, s66, v25\n\t"
"v_fma_f32 v24, v26, s52, v124\n\t"
"v_fma_f32 v25, v27, s52, v125\n\t"
"s_waitcnt lgkmcnt(5)\n\t"
"v_mfma_f32_16x16x32_bf16 v[20:23], a[224:227], a[188:191], v[20:23]\n\t"
"v_fma_f32 v124, -v124, s66, v26\n\t"
"v_fma_f32 v26, v28, s52, v126\n\t"
"v_mfma_f32_16x16x32_bf16 v[20:23], a[228:231], a[192:195], v[20:23]\n\t"
"v_fma_f32 v125, -v125, s66, v27\n\t"
"v_fma_f32 v27, v29, s52, v127\n\t"
"v_mfma_f32_16x16x32_bf16 v[20:23], a[232:235], a[196:199], v[20:23]\n\t"
"v_fma_f32 v126, -v126, s66, v28\n\t"
"v_fma_f32 v127, -v127, s66, v29\n\t"
"v_fma_f32 v28, v30, s52, v128\n\t"
"v_fma_f32 v29, v31, s52, v129\n\t"
"s_waitcnt lgkmcnt(4)\n\t"
"v_mfma_f32_16x16x32_bf16 v[24:27], a[232:235], a[200:203], v[24:27]\n\t"
"v_fma_f32 v128, -v128, s66, v30\n\t"
"v_fma_f32 v30, v32, s52, v130\n\t"
"v_mfma_f32_16x16x32_bf16 v[24:27], a[228:231], a[196:199], v[24:27]\n\t"
"v_fma_f32 v129, -v129, s66, v31\n\t"
"v_fma_f32 v31, v33, s52, v131\n\t"
"v_mfma_f32_16x16x32_bf16 v[24:27], a[224:227], a[192:195], v[24:27]\n\t"
"v_fma_f32 v130, -v130, s66, v32\n\t"
"v_fma_f32 v131, -v131, s66, v33\n\t"
"s_waitcnt lgkmcnt(3)\n\t"
"v_mfma_f32_16x16x32_bf16 v[28:31], a[224:227], a[196:199], v[28:31]\n\t"
"buffer_load_dwordx4 a[224:227], off, s[80:83], s20 offset:2048 sc1\n\t"
"v_mfma_f32_16x16x32_bf16 v[28:31], a[228:231], a[200:203], v[28:31]\n\t"
"s_waitcnt lgkmcnt(0)\n\t"
"s_mov_b32 s20, 98304\n\t"
"v_fma_f32 v32, v34, s52, v132\n\t"
"v_mfma_f32_16x16x32_bf16 v[28:31], a[232:235], a[204:207], v[28:31]\n\t"
"v_fma_f32 v33, v35, s52, v133\n\t"
"v_fma_f32 v132, -v132, s66, v34\n\t"
"v_fma_f32 v34, v36, s52, v134\n\t"
"v_fma_f32 v133, -v133, s66, v35\n\t"
"v_fma_f32 v35, v37, s52, v135\n\t"
"v_fma_f32 v134, -v134, s66, v36\n\t"
"v_fma_f32 v135, -v135, s66, v37\n\t"
"v_mfma_f32_4x4x4_16b_bf16 v[224:227], v[224:225], v[220:221], 0\n\t"
"s_waitcnt vmcnt(17)\n\t"
"v_mfma_f32_4x4x4_16b_bf16 v[228:231], v[228:229], v[220:221], 0\n\t"
"v_fma_f32 v36, v38, s52, v136\n\t"
"v_fma_f32 v37, v39, s52, v137\n\t"
"v_mfma_f32_16x16x32_bf16 v[32:35], a[236:239], a[168:171], v[32:35]\n\t"
"v_fma_f32 v136, -v136, s66, v38\n\t"
"v_fma_f32 v38, v40, s52, v138\n\t"
"v_mfma_f32_16x16x32_bf16 v[32:35], a[244:247], a[176:179], v[32:35]\n\t"
"v_fma_f32 v137, -v137, s66, v39\n\t"
"v_fma_f32 v39, v41, s52, v139\n\t"
"v_mfma_f32_16x16x32_bf16 v[32:35], a[240:243], a[172:175], v[32:35]\n\t"
"buffer_load_dwordx4 a[228:231], off, s[80:83], s20 offset:0 sc1\n\t"
"v_fma_f32 v138, -v138, s66, v40\n\t"
"v_fma_f32 v139, -v139, s66, v41\n\t"
"v_fma_f32 v40, v42, s52, v140\n\t"
"v_fma_f32 v41, v43, s52, v141\n\t"
"v_mfma_f32_16x16x32_bf16 v[36:39], a[236:239], a[172:175], v[36:39]\n\t"
"v_fma_f32 v140, -v140, s66, v42\n\t"
"v_fma_f32 v42, v44, s52, v142\n\t"
"v_mfma_f32_16x16x32_bf16 v[36:39], a[240:243], a[176:179], v[36:39]\n\t"
"v_fma_f32 v141, -v141, s66, v43\n\t"
"v_fma_f32 v43, v45, s52, v143\n\t"
"v_mfma_f32_16x16x32_bf16 v[36:39], a[244:247], a[180:183], v[36:39]\n\t"
"buffer_load_dwordx4 a[232:235], off, s[80:83], s20 offset:1024 sc1\n\t"
"v_fma_f32 v142, -v142, s66, v44\n\t"
"v_fma_f32 v143, -v143, s66, v45\n\t"
"v_cvt_pk_bf16_f32 v224, v224, v225\n\t"
"v_cvt_pk_bf16_f32 v225, v226, v227\n\t"
"v_fma_f32 v44, v46, s52, v144\n\t"
"v_fma_f32 v45, v47, s52, v145\n\t"
"v_cvt_pk_bf16_f32 v226, v228, v229\n\t"
"v_cvt_pk_bf16_f32 v227, v230, v231\n\t"
"v_mfma_f32_16x16x32_bf16 v[40:43], a[244:247], a[184:187], v[40:43]\n\t"
"v_fma_f32 v144, -v144, s66, v46\n\t"
"v_fma_f32 v46, v48, s52, v146\n\t"
"v_mfma_f32_16x16x32_bf16 v[40:43], a[240:243], a[180:183], v[40:43]\n\t"
"v_fma_f32 v145, -v145, s66, v47\n\t"
"v_fma_f32 v47, v49, s52, v147\n\t"
"v_mfma_f32_16x16x32_bf16 v[40:43], a[236:239], a[176:179], v[40:43]\n\t"
"ds_write_b128 v245, v[224:227] offset:20480\n\t"
"v_fma_f32 v146, -v146, s66, v48\n\t"
"v_fma_f32 v147, -v147, s66, v49\n\t"
"v_fma_f32 v48, v50, s52, v148\n\t"
"v_fma_f32 v49, v51, s52, v149\n\t"
"v_mfma_f32_16x16x32_bf16 v[44:47], a[236:239], a[180:183], v[44:47]\n\t"
"ds_read_b64_tr_b16 v[232:233], v246 offset:26624\n\t"
"v_mfma_f32_16x16x32_bf16 v[44:47], a[240:243], a[184:187], v[44:47]\n\t"
"ds_read_b64_tr_b16 v[236:237], v255 offset:26624\n\t"
"v_fma_f32 v148, -v148, s66, v50\n\t"
"v_mfma_f32_16x16x32_bf16 v[44:47], a[244:247], a[188:191], v[44:47]\n\t"
"v_fma_f32 v50, v52, s52, v150\n\t"
"v_fma_f32 v149, -v149, s66, v51\n\t"
"v_fma_f32 v51, v53, s52, v151\n\t"
"v_fma_f32 v150, -v150, s66, v52\n\t"
"v_fma_f32 v151, -v151, s66, v53\n\t"
"v_fma_f32 v52, v54, s52, v152\n\t"
"v_fma_f32 v53, v55, s52, v153\n\t"
"v_mfma_f32_16x16x32_bf16 v[48:51], a[244:247], a[192:195], v[48:51]\n\t"
"v_fma_f32 v152, -v152, s66, v54\n\t"
"v_fma_f32 v54, v56, s52, v154\n\t"
"v_mfma_f32_16x16x32_bf16 v[48:51], a[240:243], a[188:191], v[48:51]\n\t"
"v_fma_f32 v153, -v153, s66, v55\n\t"
"v_fma_f32 v55, v57, s52, v155\n\t"
"v_mfma_f32_16x16x32_bf16 v[48:51], a[236:239], a[184:187], v[48:51]\n\t"
"v_fma_f32 v154, -v154, s66, v56\n\t"
"v_fma_f32 v155, -v155, s66, v57\n\t"
"v_fma_f32 v56, v58, s52, v156\n\t"
"v_fma_f32 v57, v59, s52, v157\n\t"
"v_mfma_f32_16x16x32_bf16 v[52:55], a[236:239], a[188:191], v[52:55]\n\t"
"v_fma_f32 v156, -v156, s66, v58\n\t"
"v_fma_f32 v58, v60, s52, v158\n\t"
"v_mfma_f32_16x16x32_bf16 v[52:55], a[240:243], a[192:195], v[52:55]\n\t"
"v_fma_f32 v157, -v157, s66, v59\n\t"
"v_fma_f32 v59, v61, s52, v159\n\t"
"v_mfma_f32_16x16x32_bf16 v[52:55], a[244:247], a[196:199], v[52:55]\n\t"
"v_fma_f32 v158, -v158, s66, v60\n\t"
"v_fma_f32 v159, -v159, s66, v61\n\t"
"v_fma_f32 v60, v62, s52, v160\n\t"
"v_fma_f32 v61, v63, s52, v161\n\t"
"v_mfma_f32_16x16x32_bf16 v[56:59], a[244:247], a[200:203], v[56:59]\n\t"
"v_fma_f32 v160, -v160, s66, v62\n\t"
"v_fma_f32 v62, v64, s52, v162\n\t"
"v_mfma_f32_16x16x32_bf16 v[56:59], a[240:243], a[196:199], v[56:59]\n\t"
"v_fma_f32 v161, -v161, s66, v63\n\t"
"v_fma_f32 v63, v65, s52, v163\n\t"
"v_mfma_f32_16x16x32_bf16 v[56:59], a[236:239], a[192:195], v[56:59]\n\t"
"v_fma_f32 v162, -v162, s66, v64\n\t"
"v_fma_f32 v163, -v163, s66, v65\n\t"
"v_mfma_f32_16x16x32_bf16 v[60:63], a[236:239], a[196:199], v[60:63]\n\t"
"s_waitcnt lgkmcnt(1)\n\t"
"v_fma_f32 v64, v66, s52, v164\n\t"
"v_fma_f32 v65, v67, s52, v165\n\t"
"v_mfma_f32_16x16x32_bf16 v[60:63], a[240:243], a[200:203], v[60:63]\n\t"
"s_waitcnt lgkmcnt(0)\n\t"
"v_fma_f32 v164, -v164, s66, v66\n\t"
"v_fma_f32 v66, v68, s52, v166\n\t"
"v_mfma_f32_16x16x32_bf16 v[60:63], a[244:247], a[204:207], v[60:63]\n\t"
"v_fma_f32 v165, -v165, s66, v67\n\t"
"v_fma_f32 v67, v69, s52, v167\n\t"
"v_fma_f32 v166, -v166, s66, v68\n\t"
"v_fma_f32 v167, -v167, s66, v69\n\t"
"v_mfma_f32_4x4x4_16b_bf16 v[232:235], v[232:233], v[220:221], 0\n\t"
"buffer_load_dwordx4 a[236:239], off, s[80:83], s20 offset:2048 sc1\n\t"
"s_mov_b32 s20, 331776\n\t"
"v_mfma_f32_4x4x4_16b_bf16 v[236:239], v[236:237], v[220:221], 0\n\t"
"s_waitcnt vmcnt(9)\n\t"
"v_fma_f32 v68, v70, s52, v168\n\t"
"v_fma_f32 v69, v71, s52, v169\n\t"
"v_mfma_f32_16x16x32_bf16 v[64:67], a[248:251], a[168:171], v[64:67]\n\t"
"ds_read_b128 a[168:171], v222 offset:1792\n\t"
"v_fma_f32 v168, -v168, s66, v70\n\t"
"v_mfma_f32_16x16x32_bf16 v[64:67], v[240:243], a[176:179], v[64:67]\n\t"
"v_fma_f32 v70, v72, s52, v170\n\t"
"v_fma_f32 v169, -v169, s66, v71\n\t"
"v_mfma_f32_16x16x32_bf16 v[64:67], a[252:255], a[172:175], v[64:67]\n\t"
"buffer_load_dwordx4 a[240:243], off, s[80:83], s20 offset:0 sc1\n\t"
"v_fma_f32 v71, v73, s52, v171\n\t"
"v_fma_f32 v170, -v170, s66, v72\n\t"
"v_fma_f32 v171, -v171, s66, v73\n\t"
"v_fma_f32 v72, v74, s52, v172\n\t"
"v_fma_f32 v73, v75, s52, v173\n\t"
"v_mfma_f32_16x16x32_bf16 v[68:71], a[248:251], a[172:175], v[68:71]\n\t"
"ds_read_b128 a[172:175], v223 offset:7936\n\t"
"v_fma_f32 v172, -v172, s66, v74\n\t"
"v_mfma_f32_16x16x32_bf16 v[68:71], a[252:255], a[176:179], v[68:71]\n\t"
"v_fma_f32 v74, v76, s52, v174\n\t"
"v_fma_f32 v173, -v173, s66, v75\n\t"
"v_mfma_f32_16x16x32_bf16 v[68:71], v[240:243], a[180:183], v[68:71]\n\t"
"buffer_load_dwordx4 a[244:247], off, s[80:83], s20 offset:1024 sc1\n\t"
"v_fma_f32 v75, v77, s52, v175\n\t"
"v_fma_f32 v174, -v174, s66, v76\n\t"
"v_fma_f32 v175, -v175, s66, v77\n\t"
"v_cvt_pk_bf16_f32 v232, v232, v233\n\t"
"v_cvt_pk_bf16_f32 v233, v234, v235\n\t"
"v_fma_f32 v76, v78, s52, v176\n\t"
"v_fma_f32 v77, v79, s52, v177\n\t"
"v_cvt_pk_bf16_f32 v234, v236, v237\n\t"
"v_cvt_pk_bf16_f32 v235, v238, v239\n\t"
"v_mfma_f32_16x16x32_bf16 v[72:75], v[240:243], a[184:187], v[72:75]\n\t"
"v_fma_f32 v176, -v176, s66, v78\n\t"
"v_fma_f32 v78, v80, s52, v178\n\t"
"v_mfma_f32_16x16x32_bf16 v[72:75], a[252:255], a[180:183], v[72:75]\n\t"
"v_fma_f32 v177, -v177, s66, v79\n\t"
"v_fma_f32 v79, v81, s52, v179\n\t"
"v_mfma_f32_16x16x32_bf16 v[72:75], a[248:251], a[176:179], v[72:75]\n\t"
"ds_write_b128 v245, v[232:235] offset:26624\n\t"
"v_fma_f32 v178, -v178, s66, v80\n\t"
"v_fma_f32 v179, -v179, s66, v81\n\t"
"v_fma_f32 v80, v82, s52, v180\n\t"
"v_fma_f32 v81, v83, s52, v181\n\t"
"v_mfma_f32_16x16x32_bf16 v[76:79], a[248:251], a[180:183], v[76:79]\n\t"
"ds_read_b128 a[176:179], v223 offset:14080\n\t"
"v_mfma_f32_16x16x32_bf16 v[76:79], a[252:255], a[184:187], v[76:79]\n\t"
"ds_read_b64_tr_b16 v[228:229], v246 offset:32768\n\t"
"v_fma_f32 v180, -v180, s66, v82\n\t"
"v_mfma_f32_16x16x32_bf16 v[76:79], v[240:243], a[188:191], v[76:79]\n\t"
"ds_read_b64_tr_b16 v[224:225], v255 offset:32768\n\t"
"v_fma_f32 v82, v84, s52, v182\n\t"
"v_fma_f32 v181, -v181, s66, v83\n\t"
"v_fma_f32 v83, v85, s52, v183\n\t"
"v_fma_f32 v182, -v182, s66, v84\n\t"
"v_fma_f32 v183, -v183, s66, v85\n\t"
"v_fma_f32 v84, v86, s52, v184\n\t"
"v_fma_f32 v85, v87, s52, v185\n\t"
"v_mfma_f32_16x16x32_bf16 v[80:83], v[240:243], a[192:195], v[80:83]\n\t"
"ds_read_b128 a[180:183], v222 offset:21760\n\t"
"v_fma_f32 v184, -v184, s66, v86\n\t"
"v_mfma_f32_16x16x32_bf16 v[80:83], a[252:255], a[188:191], v[80:83]\n\t"
"ds_read_b64_tr_b16 v[236:237], v246 offset:40448\n\t"
"v_fma_f32 v86, v88, s52, v186\n\t"
"v_mfma_f32_16x16x32_bf16 v[80:83], a[248:251], a[184:187], v[80:83]\n\t"
"ds_read_b64_tr_b16 v[232:233], v255 offset:40448\n\t"
"v_fma_f32 v185, -v185, s66, v87\n\t"
"v_fma_f32 v87, v89, s52, v187\n\t"
"v_fma_f32 v186, -v186, s66, v88\n\t"
"v_fma_f32 v187, -v187, s66, v89\n\t"
"v_fma_f32 v88, v90, s52, v188\n\t"
"v_fma_f32 v89, v91, s52, v189\n\t"
"v_mfma_f32_16x16x32_bf16 v[84:87], a[248:251], a[188:191], v[84:87]\n\t"
"ds_read_b128 a[184:187], v223 offset:27904\n\t"
"v_fma_f32 v188, -v188, s66, v90\n\t"
"v_mfma_f32_16x16x32_bf16 v[84:87], a[252:255], a[192:195], v[84:87]\n\t"
"ds_read_b128 a[188:191], v223 offset:34048\n\t"
"v_fma_f32 v90, v92, s52, v190\n\t"
"v_mfma_f32_16x16x32_bf16 v[84:87], v[240:243], a[196:199], v[84:87]\n\t"
"v_fma_f32 v189, -v189, s66, v91\n\t"
"v_fma_f32 v91, v93, s52, v191\n\t"
"v_fma_f32 v190, -v190, s66, v92\n\t"
"v_fma_f32 v191, -v191, s66, v93\n\t"
"v_fma_f32 v92, v94, s52, v192\n\t"
"v_fma_f32 v93, v95, s52, v193\n\t"
"v_mfma_f32_16x16x32_bf16 v[88:91], v[240:243], a[200:203], v[88:91]\n\t"
"v_fma_f32 v192, -v192, s66, v94\n\t"
"v_fma_f32 v94, v96, s52, v194\n\t"
"v_mfma_f32_16x16x32_bf16 v[88:91], a[252:255], a[196:199], v[88:91]\n\t"
"v_fma_f32 v193, -v193, s66, v95\n\t"
"v_fma_f32 v95, v97, s52, v195\n\t"
"v_mfma_f32_16x16x32_bf16 v[88:91], a[248:251], a[192:195], v[88:91]\n\t"
"ds_read_b128 a[192:195], v222 offset:41728\n\t"
"v_fma_f32 v194, -v194, s66, v96\n\t"
"v_fma_f32 v195, -v195, s66, v97\n\t"
"v_mfma_f32_16x16x32_bf16 v[92:95], a[248:251], a[196:199], v[92:95]\n\t"
"s_waitcnt lgkmcnt(7)\n\t"
"ds_read_b128 a[196:199], v223 offset:47872\n\t"
"v_mfma_f32_16x16x32_bf16 v[92:95], a[252:255], a[200:203], v[92:95]\n\t"
"buffer_load_dwordx4 v[96:99], off, s[80:83], s20 offset:2048 sc1\n\t"
"v_mfma_f32_16x16x32_bf16 v[92:95], v[240:243], a[204:207], v[92:95]\n\t"
"s_waitcnt lgkmcnt(5)\n\t"
"ds_read_b128 a[200:203], v223 offset:54016\n\t"
"s_mov_b32 s20, 344064\n\t"
"v_mfma_f32_4x4x4_16b_bf16 v[228:231], v[228:229], v[220:221], 0\n\t"
"s_waitcnt lgkmcnt(5)\n\t"
"v_mfma_f32_4x4x4_16b_bf16 v[224:227], v[224:225], v[220:221], 0\n\t"
"buffer_load_dwordx4 a[248:251], off, s[80:83], s20 offset:0 sc1\n\t"
"v_mfma_f32_4x4x4_16b_bf16 v[236:239], v[236:237], v[220:221], 0\n\t"
"s_waitcnt vmcnt(10)\n\t"
"v_mfma_f32_4x4x4_16b_bf16 v[232:235], v[232:233], v[220:221], 0\n\t"
"v_mfma_f32_16x16x32_bf16 v[100:103], a[208:211], a[168:171], v[100:103]\n\t"
"buffer_load_dwordx4 v[240:243], off, s[80:83], s20 offset:1024 sc1\n\t"
"v_mfma_f32_16x16x32_bf16 v[100:103], a[212:215], a[176:179], v[100:103]\n\t"
"ds_read_b128 a[204:207], v222 offset:61696\n\t"
"v_cvt_pk_bf16_f32 v228, v228, v229\n\t"
"v_mfma_f32_16x16x32_bf16 v[100:103], v[248:251], a[172:175], v[100:103]\n\t"
"s_add_u32 m0, s24, 896\n\t"
"s_add_u32 s28, s21, 0\n\t"
"v_cvt_pk_bf16_f32 v229, v230, v231\n\t"
"v_mfma_f32_16x16x32_bf16 v[104:107], a[208:211], a[172:175], v[104:107]\n\t"
"s_waitcnt lgkmcnt(5)\n\t"
"v_cvt_pk_bf16_f32 v230, v224, v225\n\t"
"v_cvt_pk_bf16_f32 v231, v226, v227\n\t"
"v_mfma_f32_16x16x32_bf16 v[104:107], v[248:251], a[176:179], v[104:107]\n\t"
"v_cvt_pk_bf16_f32 v236, v236, v237\n\t"
"v_mfma_f32_16x16x32_bf16 v[104:107], a[212:215], a[180:183], v[104:107]\n\t"
"s_waitcnt lgkmcnt(0)\n\t"
"s_barrier\n\t"
"v_mfma_f32_16x16x32_bf16 v[108:111], a[212:215], a[184:187], v[108:111]\n\t"
"v_cvt_pk_bf16_f32 v237, v238, v239\n\t"
"v_mfma_f32_16x16x32_bf16 v[108:111], v[248:251], a[180:183], v[108:111]\n\t"
"buffer_load_dwordx4 a[252:255], off, s[80:83], s20 offset:2048 sc1\n\t"
"v_mfma_f32_16x16x32_bf16 v[108:111], a[208:211], a[176:179], v[108:111]\n\t"
"ds_write_b128 v245, v[228:231] offset:32768\n\t"
"v_cvt_pk_bf16_f32 v238, v232, v233\n\t"
"v_cvt_pk_bf16_f32 v239, v234, v235\n\t"
"v_mfma_f32_16x16x32_bf16 v[112:115], a[208:211], a[180:183], v[112:115]\n\t"
"v_mfma_f32_16x16x32_bf16 v[112:115], v[248:251], a[184:187], v[112:115]\n\t"
"buffer_load_dwordx4 v247, s[12:15], s28 offen offset:128 nt lds\n\t"
"v_mfma_f32_16x16x32_bf16 v[112:115], a[212:215], a[188:191], v[112:115]\n\t"
"s_add_u32 m0, s24, 7040\n\t"
"s_add_u32 s28, s21, 196608\n\t"
"ds_write_b128 v245, v[236:239] offset:40448\n\t"
"v_mfma_f32_16x16x32_bf16 v[116:119], a[212:215], a[192:195], v[116:119]\n\t"
"buffer_load_dwordx4 v247, s[12:15], s28 offen offset:128 nt lds\n\t"
"v_mfma_f32_16x16x32_bf16 v[116:119], v[248:251], a[188:191], v[116:119]\n\t"
"s_add_u32 m0, s24, 13184\n\t"
"s_add_u32 s28, s21, 393216\n\t"
"ds_read_b64_tr_b16 v[224:225], v246 offset:46592\n\t"
"v_mfma_f32_16x16x32_bf16 v[116:119], a[208:211], a[184:187], v[116:119]\n\t"
"buffer_load_dwordx4 v247, s[12:15], s28 offen offset:128 nt lds\n\t"
"s_add_u32 m0, s24, 20864\n\t"
"s_add_u32 s28, s21, 589824\n\t"
"v_mfma_f32_16x16x32_bf16 v[120:123], a[208:211], a[188:191], v[120:123]\n\t"
"buffer_load_dwordx4 v247, s[12:15], s28 offen offset:128 nt lds\n\t"
"v_mfma_f32_16x16x32_bf16 v[120:123], v[248:251], a[192:195], v[120:123]\n\t"
"s_add_u32 m0, s24, 27008\n\t"
"s_add_u32 s28, s21, 786432\n\t"
"ds_read_b64_tr_b16 v[232:233], v255 offset:46592\n\t"
"v_mfma_f32_16x16x32_bf16 v[120:123], a[212:215], a[196:199], v[120:123]\n\t"
"buffer_load_dwordx4 v247, s[12:15], s28 offen offset:128 nt lds\n\t"
"s_add_u32 m0, s24, 33152\n\t"
"s_add_u32 s28, s21, 983040\n\t"
"v_mfma_f32_16x16x32_bf16 v[124:127], a[212:215], a[200:203], v[124:127]\n\t"
"buffer_load_dwordx4 v247, s[12:15], s28 offen offset:128 nt lds\n\t"
"v_mfma_f32_16x16x32_bf16 v[124:127], v[248:251], a[196:199], v[124:127]\n\t"
"ds_read_b64_tr_b16 v[228:229], v246 offset:52736\n\t"
"v_mfma_f32_16x16x32_bf16 v[124:127], a[208:211], a[192:195], v[124:127]\n\t"
"ds_read_b64_tr_b16 v[236:237], v255 offset:52736\n\t"
"v_mfma_f32_16x16x32_bf16 v[128:131], a[208:211], a[196:199], v[128:131]\n\t"
"s_waitcnt lgkmcnt(3)\n\t"
"v_mfma_f32_16x16x32_bf16 v[128:131], v[248:251], a[200:203], v[128:131]\n\t"
"s_waitcnt lgkmcnt(2)\n\t"
"v_mfma_f32_16x16x32_bf16 v[128:131], a[212:215], a[204:207], v[128:131]\n\t"
"v_mfma_f32_4x4x4_16b_bf16 v[224:227], v[224:225], v[220:221], 0\n\t"
"s_waitcnt lgkmcnt(1)\n\t"
"v_mfma_f32_4x4x4_16b_bf16 v[232:235], v[232:233], v[220:221], 0\n\t"
"s_waitcnt vmcnt(15) lgkmcnt(0)\n\t"
"v_mfma_f32_4x4x4_16b_bf16 v[228:231], v[228:229], v[220:221], 0\n\t"
"v_mfma_f32_4x4x4_16b_bf16 v[236:239], v[236:237], v[220:221], 0\n\t"
"v_mfma_f32_16x16x32_bf16 v[132:135], a[216:219], a[168:171], v[132:135]\n\t"
"v_cvt_pk_bf16_f32 v224, v224, v225\n\t"
"v_cvt_pk_bf16_f32 v225, v226, v227\n\t"
"v_mfma_f32_16x16x32_bf16 v[132:135], a[224:227], a[176:179], v[132:135]\n\t"
"v_cvt_pk_bf16_f32 v226, v232, v233\n\t"
"v_cvt_pk_bf16_f32 v227, v234, v235\n\t"
"v_mfma_f32_16x16x32_bf16 v[132:135], a[220:223], a[172:175], v[132:135]\n\t"
"ds_write_b128 v245, v[224:227] offset:46592\n\t"
"v_mfma_f32_16x16x32_bf16 v[136:139], a[216:219], a[172:175], v[136:139]\n\t"
"v_cvt_pk_bf16_f32 v228, v228, v229\n\t"
"v_cvt_pk_bf16_f32 v229, v230, v231\n\t"
"v_mfma_f32_16x16x32_bf16 v[136:139], a[220:223], a[176:179], v[136:139]\n\t"
"v_cvt_pk_bf16_f32 v230, v236, v237\n\t"
"v_cvt_pk_bf16_f32 v231, v238, v239\n\t"
"v_mfma_f32_16x16x32_bf16 v[136:139], a[224:227], a[180:183], v[136:139]\n\t"
"ds_write_b128 v245, v[228:231] offset:52736\n\t"
"v_mfma_f32_16x16x32_bf16 v[140:143], a[224:227], a[184:187], v[140:143]\n\t"
"s_mov_b32 s20, 356352\n\t"
"v_mfma_f32_16x16x32_bf16 v[140:143], a[220:223], a[180:183], v[140:143]\n\t"
"v_mfma_f32_16x16x32_bf16 v[140:143], a[216:219], a[176:179], v[140:143]\n\t"
"v_mfma_f32_16x16x32_bf16 v[144:147], a[216:219], a[180:183], v[144:147]\n\t"
"v_mfma_f32_16x16x32_bf16 v[144:147], a[220:223], a[184:187], v[144:147]\n\t"
"v_mfma_f32_16x16x32_bf16 v[144:147], a[224:227], a[188:191], v[144:147]\n\t"
"v_mfma_f32_16x16x32_bf16 v[148:151], a[224:227], a[192:195], v[148:151]\n\t"
"buffer_load_dwordx4 a[208:211], off, s[80:83], s20 offset:0 sc1\n\t"
"v_mfma_f32_16x16x32_bf16 v[148:151], a[220:223], a[188:191], v[148:151]\n\t"
"v_mfma_f32_16x16x32_bf16 v[148:151], a[216:219], a[184:187], v[148:151]\n\t"
"v_mfma_f32_16x16x32_bf16 v[152:155], a[216:219], a[188:191], v[152:155]\n\t"
"v_mfma_f32_16x16x32_bf16 v[152:155], a[220:223], a[192:195], v[152:155]\n\t"
"v_mfma_f32_16x16x32_bf16 v[152:155], a[224:227], a[196:199], v[152:155]\n\t"
"v_mfma_f32_16x16x32_bf16 v[156:159], a[224:227], a[200:203], v[156:159]\n\t"
"v_mfma_f32_16x16x32_bf16 v[156:159], a[220:223], a[196:199], v[156:159]\n\t"
"v_mfma_f32_16x16x32_bf16 v[156:159], a[216:219], a[192:195], v[156:159]\n\t"
"v_mfma_f32_16x16x32_bf16 v[160:163], a[216:219], a[196:199], v[160:163]\n\t"
"s_waitcnt vmcnt(13)\n\t"
"v_mfma_f32_16x16x32_bf16 v[160:163], a[220:223], a[200:203], v[160:163]\n\t"
"v_mfma_f32_16x16x32_bf16 v[160:163], a[224:227], a[204:207], v[160:163]\n\t"
"v_mfma_f32_16x16x32_bf16 v[164:167], a[228:231], a[168:171], v[164:167]\n\t"
"v_mfma_f32_16x16x32_bf16 v[164:167], a[236:239], a[176:179], v[164:167]\n\t"
"v_mfma_f32_16x16x32_bf16 v[164:167], a[232:235], a[172:175], v[164:167]\n\t"
"s_waitcnt lgkmcnt(0)\n\t"
"s_barrier\n\t"
"v_mfma_f32_16x16x32_bf16 v[168:171], a[228:231], a[172:175], v[168:171]\n\t"
"ds_read_b128 a[168:171], v222 offset:2048\n\t"
"v_mfma_f32_16x16x32_bf16 v[168:171], a[232:235], a[176:179], v[168:171]\n\t"
"ds_read_b128 a[172:175], v223 offset:8192\n\t"
"v_mfma_f32_16x16x32_bf16 v[168:171], a[236:239], a[180:183], v[168:171]\n\t"
"v_mfma_f32_16x16x32_bf16 v[172:175], a[236:239], a[184:187], v[172:175]\n\t"
"v_mfma_f32_16x16x32_bf16 v[172:175], a[232:235], a[180:183], v[172:175]\n\t"
"v_mfma_f32_16x16x32_bf16 v[172:175], a[228:231], a[176:179], v[172:175]\n\t"
"ds_read_b128 a[176:179], v223 offset:14336\n\t"
"v_mfma_f32_16x16x32_bf16 v[176:179], a[228:231], a[180:183], v[176:179]\n\t"
"ds_read_b128 a[180:183], v222 offset:22016\n\t"
"v_mfma_f32_16x16x32_bf16 v[176:179], a[232:235], a[184:187], v[176:179]\n\t"
"v_mfma_f32_16x16x32_bf16 v[176:179], a[236:239], a[188:191], v[176:179]\n\t"
"v_mfma_f32_16x16x32_bf16 v[180:183], a[236:239], a[192:195], v[180:183]\n\t"
"v_mfma_f32_16x16x32_bf16 v[180:183], a[232:235], a[188:191], v[180:183]\n\t"
"v_mfma_f32_16x16x32_bf16 v[180:183], a[228:231], a[184:187], v[180:183]\n\t"
"ds_read_b128 a[184:187], v223 offset:28160\n\t"
"v_mfma_f32_16x16x32_bf16 v[184:187], a[228:231], a[188:191], v[184:187]\n\t"
"ds_read_b128 a[188:191], v223 offset:34304\n\t"
"v_mfma_f32_16x16x32_bf16 v[184:187], a[232:235], a[192:195], v[184:187]\n\t"
"v_mfma_f32_16x16x32_bf16 v[184:187], a[236:239], a[196:199], v[184:187]\n\t"
"v_mfma_f32_16x16x32_bf16 v[188:191], a[236:239], a[200:203], v[188:191]\n\t"
"v_mfma_f32_16x16x32_bf16 v[188:191], a[232:235], a[196:199], v[188:191]\n\t"
"v_mfma_f32_16x16x32_bf16 v[188:191], a[228:231], a[192:195], v[188:191]\n\t"
"ds_read_b128 a[192:195], v222 offset:41984\n\t"
"v_mfma_f32_16x16x32_bf16 v[192:195], a[228:231], a[196:199], v[192:195]\n\t"
"buffer_load_dwordx4 v[236:239], off, s[80:83], s20 offset:1024 sc1\n\t"
"v_mfma_f32_16x16x32_bf16 v[192:195], a[232:235], a[200:203], v[192:195]\n\t"
"s_waitcnt vmcnt(11) lgkmcnt(4)\n\t"
"ds_read_b128 a[196:199], v223 offset:48128\n\t"
"v_mfma_f32_16x16x32_bf16 v[192:195], a[236:239], a[204:207], v[192:195]\n\t"
"ds_read_b128 a[200:203], v223 offset:54272\n\t"
"Lst4_%=:\n\t"
"v_mfma_f32_16x16x32_bf16 v[0:3], a[240:243], a[168:171], v[0:3]\n\t"
"buffer_load_dwordx4 v[248:251], off, s[80:83], s20 offset:2048 sc1\n\t"
"v_mfma_f32_16x16x32_bf16 v[0:3], v[96:99], a[176:179], v[0:3]\n\t"
"s_waitcnt lgkmcnt(5)\n\t"
"ds_read_b128 a[204:207], v222 offset:61952\n\t"
"s_mov_b32 s20, 368640\n\t"
"v_mfma_f32_16x16x32_bf16 v[0:3], a[244:247], a[172:175], v[0:3]\n\t"
"v_mfma_f32_16x16x32_bf16 v[4:7], a[240:243], a[172:175], v[4:7]\n\t"
"s_waitcnt lgkmcnt(5)\n\t"
"v_mfma_f32_16x16x32_bf16 v[4:7], a[244:247], a[176:179], v[4:7]\n\t"
"buffer_load_dwordx4 a[212:215], off, s[80:83], s20 offset:0 sc1\n\t"
"v_mfma_f32_16x16x32_bf16 v[4:7], v[96:99], a[180:183], v[4:7]\n\t"
"v_mfma_f32_16x16x32_bf16 v[8:11], v[96:99], a[184:187], v[8:11]\n\t"
"s_waitcnt lgkmcnt(4)\n\t"
"v_mfma_f32_16x16x32_bf16 v[8:11], a[244:247], a[180:183], v[8:11]\n\t"
"v_mfma_f32_16x16x32_bf16 v[8:11], a[240:243], a[176:179], v[8:11]\n\t"
"buffer_load_dwordx4 a[216:219], off, s[80:83], s20 offset:1024 sc1\n\t"
"v_mfma_f32_16x16x32_bf16 v[12:15], a[240:243], a[180:183], v[12:15]\n\t"
"s_waitcnt lgkmcnt(3)\n\t"
"v_mfma_f32_16x16x32_bf16 v[12:15], a[244:247], a[184:187], v[12:15]\n\t"
"v_mfma_f32_16x16x32_bf16 v[12:15], v[96:99], a[188:191], v[12:15]\n\t"
"v_mfma_f32_16x16x32_bf16 v[16:19], v[96:99], a[192:195], v[16:19]\n\t"
"buffer_load_dwordx4 a[220:223], off, s[80:83], s20 offset:2048 sc1\n\t"
"v_mfma_f32_16x16x32_bf16 v[16:19], a[244:247], a[188:191], v[16:19]\n\t"
"s_waitcnt lgkmcnt(2)\n\t"
"s_mov_b32 s20, 380928\n\t"
"v_mfma_f32_16x16x32_bf16 v[16:19], a[240:243], a[184:187], v[16:19]\n\t"
"v_mfma_f32_16x16x32_bf16 v[20:23], a[240:243], a[188:191], v[20:23]\n\t"
"s_waitcnt lgkmcnt(1)\n\t"
"v_mfma_f32_16x16x32_bf16 v[20:23], a[244:247], a[192:195], v[20:23]\n\t"
"buffer_load_dwordx4 a[224:227], off, s[80:83], s20 offset:0 sc1\n\t"
"v_mfma_f32_16x16x32_bf16 v[20:23], v[96:99], a[196:199], v[20:23]\n\t"
"s_waitcnt vmcnt(13) lgkmcnt(0)\n\t"
"v_mfma_f32_16x16x32_bf16 v[24:27], v[96:99], a[200:203], v[24:27]\n\t"
"v_mfma_f32_16x16x32_bf16 v[24:27], a[244:247], a[196:199], v[24:27]\n\t"
"v_mfma_f32_16x16x32_bf16 v[24:27], a[240:243], a[192:195], v[24:27]\n\t"
"Lpg4_%=:\n\t"
"s_cbranch_vccnz Lpg8_%=\n\t"
"v_mfma_f32_16x16x32_bf16 v[28:31], a[240:243], a[196:199], v[28:31]\n\t"
"buffer_load_dwordx4 a[228:231], off, s[80:83], s20 offset:1024 sc1\n\t"
"v_mfma_f32_16x16x32_bf16 v[28:31], a[244:247], a[200:203], v[28:31]\n\t"
"v_mfma_f32_16x16x32_bf16 v[28:31], v[96:99], a[204:207], v[28:31]\n\t"
"v_mfma_f32_16x16x32_bf16 v[32:35], a[248:251], a[168:171], v[32:35]\n\t"
"buffer_load_dwordx4 v[96:99], off, s[80:83], s20 offset:2048 sc1\n\t"
"v_mfma_f32_16x16x32_bf16 v[32:35], a[252:255], a[176:179], v[32:35]\n\t"
"s_mov_b32 s20, 393216\n\t"
"v_mfma_f32_16x16x32_bf16 v[32:35], v[240:243], a[172:175], v[32:35]\n\t"
"v_mfma_f32_16x16x32_bf16 v[36:39], a[248:251], a[172:175], v[36:39]\n\t"
"v_mfma_f32_16x16x32_bf16 v[36:39], v[240:243], a[176:179], v[36:39]\n\t"
"buffer_load_dwordx4 a[232:235], off, s[80:83], s20 offset:0 sc1\n\t"
"v_mfma_f32_16x16x32_bf16 v[36:39], a[252:255], a[180:183], v[36:39]\n\t"
"v_mfma_f32_16x16x32_bf16 v[40:43], a[252:255], a[184:187], v[40:43]\n\t"
"v_mfma_f32_16x16x32_bf16 v[40:43], v[240:243], a[180:183], v[40:43]\n\t"
"v_mfma_f32_16x16x32_bf16 v[40:43], a[248:251], a[176:179], v[40:43]\n\t"
"buffer_load_dwordx4 a[236:239], off, s[80:83], s20 offset:1024 sc1\n\t"
"v_mfma_f32_16x16x32_bf16 v[44:47], a[248:251], a[180:183], v[44:47]\n\t"
"v_mfma_f32_16x16x32_bf16 v[44:47], v[240:243], a[184:187], v[44:47]\n\t"
"v_mfma_f32_16x16x32_bf16 v[44:47], a[252:255], a[188:191], v[44:47]\n\t"
"v_mfma_f32_16x16x32_bf16 v[48:51], a[252:255], a[192:195], v[48:51]\n\t"
"buffer_load_dwordx4 a[240:243], off, s[80:83], s20 offset:2048 sc1\n\t"
"v_mfma_f32_16x16x32_bf16 v[48:51], v[240:243], a[188:191], v[48:51]\n\t"
"s_mov_b32 s20, 405504\n\t"
"v_mfma_f32_16x16x32_bf16 v[48:51], a[248:251], a[184:187], v[48:51]\n\t"
"v_mfma_f32_16x16x32_bf16 v[52:55], a[248:251], a[188:191], v[52:55]\n\t"
"v_mfma_f32_16x16x32_bf16 v[52:55], v[240:243], a[192:195], v[52:55]\n\t"
"buffer_load_dwordx4 a[244:247], off, s[80:83], s20 offset:0 sc1\n\t"
"v_mfma_f32_16x16x32_bf16 v[52:55], a[252:255], a[196:199], v[52:55]\n\t"
"v_mfma_f32_16x16x32_bf16 v[56:59], a[252:255], a[200:203], v[56:59]\n\t"
"v_mfma_f32_16x16x32_bf16 v[56:59], v[240:243], a[196:199], v[56:59]\n\t"
"v_mfma_f32_16x16x32_bf16 v[56:59], a[248:251], a[192:195], v[56:59]\n\t"
"v_mfma_f32_16x16x32_bf16 v[60:63], a[248:251], a[196:199], v[60:63]\n\t"
"buffer_load_dwordx4 a[248:251], off, s[80:83], s20 offset:1024 sc1\n\t"
"v_mfma_f32_16x16x32_bf16 v[60:63], v[240:243], a[200:203], v[60:63]\n\t"
"s_waitcnt vmcnt(11)\n\t"
"v_mfma_f32_16x16x32_bf16 v[60:63], a[252:255], a[204:207], v[60:63]\n\t"
"v_mfma_f32_16x16x32_bf16 v[64:67], a[208:211], a[168:171], v[64:67]\n\t"
"buffer_load_dwordx4 a[252:255], off, s[80:83], s20 offset:2048 sc1\n\t"
"v_mfma_f32_16x16x32_bf16 v[64:67], v[248:251], a[176:179], v[64:67]\n\t"
"ds_read_b128 a[168:171], v222 offset:2304\n\t"
"v_mfma_f32_16x16x32_bf16 v[64:67], v[236:239], a[172:175], v[64:67]\n\t"
"v_mfma_f32_16x16x32_bf16 v[68:71], a[208:211], a[172:175], v[68:71]\n\t"
"ds_read_b128 a[172:175], v223 offset:8448\n\t"
"v_mfma_f32_16x16x32_bf16 v[68:71], v[236:239], a[176:179], v[68:71]\n\t"
"v_mfma_f32_16x16x32_bf16 v[68:71], v[248:251], a[180:183], v[68:71]\n\t"
"v_mfma_f32_16x16x32_bf16 v[72:75], v[248:251], a[184:187], v[72:75]\n\t"
"v_mfma_f32_16x16x32_bf16 v[72:75], v[236:239], a[180:183], v[72:75]\n\t"
"v_mfma_f32_16x16x32_bf16 v[72:75], a[208:211], a[176:179], v[72:75]\n\t"
"ds_read_b128 a[176:179], v223 offset:14592\n\t"
"v_mfma_f32_16x16x32_bf16 v[76:79], a[208:211], a[180:183], v[76:79]\n\t"
"ds_read_b128 a[180:183], v222 offset:22272\n\t"
"v_mfma_f32_16x16x32_bf16 v[76:79], v[236:239], a[184:187], v[76:79]\n\t"
"v_mfma_f32_16x16x32_bf16 v[76:79], v[248:251], a[188:191], v[76:79]\n\t"
"v_mfma_f32_16x16x32_bf16 v[80:83], v[248:251], a[192:195], v[80:83]\n\t"
"v_mfma_f32_16x16x32_bf16 v[80:83], v[236:239], a[188:191], v[80:83]\n\t"
"v_mfma_f32_16x16x32_bf16 v[80:83], a[208:211], a[184:187], v[80:83]\n\t"
"ds_read_b128 a[184:187], v223 offset:28416\n\t"
"v_mfma_f32_16x16x32_bf16 v[84:87], a[208:211], a[188:191], v[84:87]\n\t"
"ds_read_b128 a[188:191], v223 offset:34560\n\t"
"v_mfma_f32_16x16x32_bf16 v[84:87], v[236:239], a[192:195], v[84:87]\n\t"
"v_mfma_f32_16x16x32_bf16 v[84:87], v[248:251], a[196:199], v[84:87]\n\t"
"s_mov_b32 s20, 417792\n\t"
"v_mfma_f32_16x16x32_bf16 v[88:91], v[248:251], a[200:203], v[88:91]\n\t"
"v_mfma_f32_16x16x32_bf16 v[88:91], v[236:239], a[196:199], v[88:91]\n\t"
"v_mfma_f32_16x16x32_bf16 v[88:91], a[208:211], a[192:195], v[88:91]\n\t"
"ds_read_b128 a[192:195], v222 offset:42240\n\t"
"v_mfma_f32_16x16x32_bf16 v[92:95], a[208:211], a[196:199], v[92:95]\n\t"
"s_waitcnt vmcnt(9) lgkmcnt(4)\n\t"
"ds_read_b128 a[196:199], v223 offset:48384\n\t"
"v_mfma_f32_16x16x32_bf16 v[92:95], v[236:239], a[200:203], v[92:95]\n\t"
"global_load_dwordx4 v[240:243], v252, s[48:49] offset:2048\n\t"
"v_mfma_f32_16x16x32_bf16 v[92:95], v[248:251], a[204:207], v[92:95]\n\t"
"buffer_load_dwordx4 v[248:251], off, s[80:83], s20 offset:0 sc1\n\t"
"v_mfma_f32_16x16x32_bf16 v[100:103], a[212:215], a[168:171], v[100:103]\n\t"
"s_waitcnt lgkmcnt(4)\n\t"
"ds_read_b128 a[200:203], v223 offset:54528\n\t"
"v_mfma_f32_16x16x32_bf16 v[100:103], a[220:223], a[176:179], v[100:103]\n\t"
"ds_read_b128 a[204:207], v222 offset:62208\n\t"
"v_mfma_f32_16x16x32_bf16 v[100:103], a[216:219], a[172:175], v[100:103]\n\t"
"v_mfma_f32_16x16x32_bf16 v[104:107], a[212:215], a[172:175], v[104:107]\n\t"
"s_waitcnt lgkmcnt(5)\n\t"
"v_mfma_f32_16x16x32_bf16 v[104:107], a[216:219], a[176:179], v[104:107]\n\t"
"global_load_dwordx4 a[208:211], v252, s[46:47] offset:1024\n\t"
"v_mfma_f32_16x16x32_bf16 v[104:107], a[220:223], a[180:183], v[104:107]\n\t"
"v_mfma_f32_16x16x32_bf16 v[108:111], a[220:223], a[184:187], v[108:111]\n\t"
"s_waitcnt lgkmcnt(4)\n\t"
"v_mfma_f32_16x16x32_bf16 v[108:111], a[216:219], a[180:183], v[108:111]\n\t"
"v_mfma_f32_16x16x32_bf16 v[108:111], a[212:215], a[176:179], v[108:111]\n\t"
"v_mfma_f32_16x16x32_bf16 v[112:115], a[212:215], a[180:183], v[112:115]\n\t"
"s_waitcnt lgkmcnt(3)\n\t"
"v_mfma_f32_16x16x32_bf16 v[112:115], a[216:219], a[184:187], v[112:115]\n\t"
"v_mfma_f32_16x16x32_bf16 v[112:115], a[220:223], a[188:191], v[112:115]\n\t"
"v_mfma_f32_16x16x32_bf16 v[116:119], a[220:223], a[192:195], v[116:119]\n\t"
"s_waitcnt lgkmcnt(2)\n\t"
"v_mfma_f32_16x16x32_bf16 v[116:119], a[216:219], a[188:191], v[116:119]\n\t"
"v_mfma_f32_16x16x32_bf16 v[116:119], a[212:215], a[184:187], v[116:119]\n\t"
"v_mfma_f32_16x16x32_bf16 v[120:123], a[212:215], a[188:191], v[120:123]\n\t"
"s_waitcnt lgkmcnt(1)\n\t"
"v_mfma_f32_16x16x32_bf16 v[120:123], a[216:219], a[192:195], v[120:123]\n\t"
"v_mfma_f32_16x16x32_bf16 v[120:123], a[220:223], a[196:199], v[120:123]\n\t"
"v_mfma_f32_16x16x32_bf16 v[124:127], a[220:223], a[200:203], v[124:127]\n\t"
"s_waitcnt lgkmcnt(0)\n\t"
"v_mfma_f32_16x16x32_bf16 v[124:127], a[216:219], a[196:199], v[124:127]\n\t"
"s_add_u32 m0, s24, 42368\n\t"
"s_add_u32 s28, s21, 1182720\n\t"
"v_mfma_f32_16x16x32_bf16 v[124:127], a[212:215], a[192:195], v[124:127]\n\t"
"v_mfma_f32_16x16x32_bf16 v[128:131], a[212:215], a[196:199], v[128:131]\n\t"
"buffer_load_dwordx4 a[212:215], off, s[80:83], s20 offset:1024 sc1\n\t"
"v_mfma_f32_16x16x32_bf16 v[128:131], a[216:219], a[200:203], v[128:131]\n\t"
"s_waitcnt vmcnt(10)\n\t"
"v_mfma_f32_16x16x32_bf16 v[128:131], a[220:223], a[204:207], v[128:131]\n\t"
"buffer_load_dwordx4 v247, s[12:15], s28 offen offset:128 nt lds\n\t"
"v_mfma_f32_16x16x32_bf16 v[132:135], a[224:227], a[168:171], v[132:135]\n\t"
"s_add_u32 m0, s24, 48512\n\t"
"s_add_u32 s28, s21, 1379328\n\t"
"v_mfma_f32_16x16x32_bf16 v[132:135], v[96:99], a[176:179], v[132:135]\n\t"
"buffer_load_dwordx4 v247, s[12:15], s28 offen offset:128 nt lds\n\t"
"v_mfma_f32_16x16x32_bf16 v[132:135], a[228:231], a[172:175], v[132:135]\n\t"
"s_add_u32 m0, s24, 54656\n\t"
"s_add_u32 s28, s21, 1575936\n\t"
"v_mfma_f32_16x16x32_bf16 v[136:139], a[224:227], a[172:175], v[136:139]\n\t"
"buffer_load_dwordx4 v247, s[12:15], s28 offen offset:128 nt lds\n\t"
"v_mfma_f32_16x16x32_bf16 v[136:139], a[228:231], a[176:179], v[136:139]\n\t"
"s_add_u32 m0, s24, 40832\n\t"
"s_add_u32 s28, s21, 1179648\n\t"
"v_mfma_f32_16x16x32_bf16 v[136:139], v[96:99], a[180:183], v[136:139]\n\t"
"s_barrier\n\t"
"v_mfma_f32_16x16x32_bf16 v[140:143], v[96:99], a[184:187], v[140:143]\n\t"
"v_mfma_f32_16x16x32_bf16 v[140:143], a[228:231], a[180:183], v[140:143]\n\t"
"buffer_load_dwordx4 v247, s[12:15], s28 offen offset:128 nt lds\n\t"
"v_mfma_f32_16x16x32_bf16 v[140:143], a[224:227], a[176:179], v[140:143]\n\t"
"s_add_u32 m0, s24, 46976\n\t"
"s_add_u32 s28, s21, 1376256\n\t"
"s_waitcnt vmcnt(7)\n\t"
"ds_read_b64_tr_b16 v[232:233], v240 offset:1024\n\t"
"v_mfma_f32_16x16x32_bf16 v[144:147], a[224:227], a[180:183], v[144:147]\n\t"
"buffer_load_dwordx4 v247, s[12:15], s28 offen offset:128 nt lds\n\t"
"v_mfma_f32_16x16x32_bf16 v[144:147], a[228:231], a[184:187], v[144:147]\n\t"
"s_add_u32 m0, s24, 53120\n\t"
"s_add_u32 s28, s21, 1572864\n\t"
"ds_read_b64_tr_b16 v[234:235], v240 offset:2560\n\t"
"v_mfma_f32_16x16x32_bf16 v[144:147], v[96:99], a[188:191], v[144:147]\n\t"
"buffer_load_dwordx4 v247, s[12:15], s28 offen offset:128 nt lds\n\t"
"v_mfma_f32_16x16x32_bf16 v[148:151], v[96:99], a[192:195], v[148:151]\n\t"
"ds_read_b64_tr_b16 v[236:237], v241 offset:1024\n\t"
"v_mfma_f32_16x16x32_bf16 v[148:151], a[228:231], a[188:191], v[148:151]\n\t"
"ds_read_b64_tr_b16 v[238:239], v241 offset:2560\n\t"
"v_mfma_f32_16x16x32_bf16 v[148:151], a[224:227], a[184:187], v[148:151]\n\t"
"ds_read_b64_tr_b16 v[224:225], v240 offset:20992\n\t"
"v_mfma_f32_16x16x32_bf16 v[152:155], a[224:227], a[188:191], v[152:155]\n\t"
"ds_read_b64_tr_b16 v[226:227], v240 offset:22528\n\t"
"v_mfma_f32_16x16x32_bf16 v[152:155], a[228:231], a[192:195], v[152:155]\n\t"
"ds_read_b64_tr_b16 v[228:229], v241 offset:20992\n\t"
"v_mfma_f32_16x16x32_bf16 v[152:155], v[96:99], a[196:199], v[152:155]\n\t"
"ds_read_b64_tr_b16 v[230:231], v241 offset:22528\n\t"
"v_mfma_f32_16x16x32_bf16 v[156:159], v[96:99], a[200:203], v[156:159]\n\t"
"v_mfma_f32_16x16x32_bf16 v[156:159], a[228:231], a[196:199], v[156:159]\n\t"
"v_mfma_f32_16x16x32_bf16 v[156:159], a[224:227], a[192:195], v[156:159]\n\t"
"v_mfma_f32_16x16x32_bf16 v[160:163], a[224:227], a[196:199], v[160:163]\n\t"
"s_waitcnt vmcnt(7) lgkmcnt(6)\n\t"
"v_mfma_f32_16x16x32_bf16 v[160:163], a[228:231], a[200:203], v[160:163]\n\t"
"s_waitcnt lgkmcnt(4)\n\t"
"v_mfma_f32_16x16x32_bf16 v[160:163], v[96:99], a[204:207], v[160:163]\n\t"
"s_waitcnt lgkmcnt(2)\n\t"
"v_mfma_f32_16x16x32_bf16 v[232:235], v[232:235], a[208:211], 0\n\t"
"s_waitcnt lgkmcnt(0)\n\t"
"v_mfma_f32_16x16x32_bf16 v[236:239], v[236:239], a[208:211], 0\n\t"
"v_mfma_f32_16x16x32_bf16 v[224:227], v[224:227], a[208:211], 0\n\t"
"v_mfma_f32_16x16x32_bf16 v[228:231], v[228:231], a[208:211], 0\n\t"
"v_mfma_f32_16x16x32_bf16 v[164:167], a[232:235], a[168:171], v[164:167]\n\t"
"ds_read_b128 a[168:171], v222 offset:512\n\t"
"v_mfma_f32_16x16x32_bf16 v[164:167], a[240:243], a[176:179], v[164:167]\n\t"
"v_cvt_pk_bf16_f32 v232, v232, v233\n\t"
"v_cvt_pk_bf16_f32 v233, v234, v235\n\t"
"v_mfma_f32_16x16x32_bf16 v[164:167], a[236:239], a[172:175], v[164:167]\n\t"
"v_cvt_pk_bf16_f32 v234, v236, v237\n\t"
"v_cvt_pk_bf16_f32 v235, v238, v239\n\t"
"v_mfma_f32_16x16x32_bf16 v[168:171], a[232:235], a[172:175], v[168:171]\n\t"
"ds_read_b128 a[172:175], v223 offset:6656\n\t"
"v_cvt_pk_bf16_f32 v224, v224, v225\n\t"
"v_mfma_f32_16x16x32_bf16 v[168:171], a[236:239], a[176:179], v[168:171]\n\t"
"v_cvt_pk_bf16_f32 v225, v226, v227\n\t"
"v_cvt_pk_bf16_f32 v226, v228, v229\n\t"
"v_mfma_f32_16x16x32_bf16 v[168:171], a[240:243], a[180:183], v[168:171]\n\t"
"ds_write_b128 v242, v[232:235] offset:24576\n\t"
"v_mfma_f32_16x16x32_bf16 v[172:175], a[240:243], a[184:187], v[172:175]\n\t"
"v_cvt_pk_bf16_f32 v227, v230, v231\n\t"
"v_add_u32 v240, 59904, v240\n\t"
"v_mfma_f32_16x16x32_bf16 v[172:175], a[236:239], a[180:183], v[172:175]\n\t"
"v_add_u32 v241, 59904, v241\n\t"
"v_mfma_f32_16x16x32_bf16 v[172:175], a[232:235], a[176:179], v[172:175]\n\t"
"ds_write_b128 v242, v[224:227] offset:26112\n\t"
"v_mfma_f32_16x16x32_bf16 v[176:179], a[232:235], a[180:183], v[176:179]\n\t"
"buffer_load_dwordx4 v[96:99], off, s[80:83], s20 offset:2048 sc1\n\t"
"v_mfma_f32_16x16x32_bf16 v[176:179], a[236:239], a[184:187], v[176:179]\n\t"
"ds_read_b128 a[176:179], v223 offset:12800\n\t"
"v_mfma_f32_16x16x32_bf16 v[176:179], a[240:243], a[188:191], v[176:179]\n\t"
"ds_read_b64_tr_b16 v[236:237], v240 offset:1024\n\t"
"v_mfma_f32_16x16x32_bf16 v[180:183], a[240:243], a[192:195], v[180:183]\n\t"
"ds_read_b64_tr_b16 v[238:239], v240 offset:2560\n\t"
"v_mfma_f32_16x16x32_bf16 v[180:183], a[236:239], a[188:191], v[180:183]\n\t"
"ds_read_b64_tr_b16 v[228:229], v241 offset:1024\n\t"
"v_mfma_f32_16x16x32_bf16 v[180:183], a[232:235], a[184:187], v[180:183]\n\t"
"ds_read_b64_tr_b16 v[230:231], v241 offset:2560\n\t"
"v_mfma_f32_16x16x32_bf16 v[184:187], a[232:235], a[188:191], v[184:187]\n\t"
"ds_read_b128 a[180:183], v222 offset:20480\n\t"
"v_mfma_f32_16x16x32_bf16 v[184:187], a[236:239], a[192:195], v[184:187]\n\t"
"ds_read_b64_tr_b16 v[232:233], v240 offset:20992\n\t"
"v_mfma_f32_16x16x32_bf16 v[184:187], a[240:243], a[196:199], v[184:187]\n\t"
"ds_read_b64_tr_b16 v[234:235], v240 offset:22528\n\t"
"v_mfma_f32_16x16x32_bf16 v[188:191], a[240:243], a[200:203], v[188:191]\n\t"
"ds_read_b64_tr_b16 v[224:225], v241 offset:20992\n\t"
"v_mfma_f32_16x16x32_bf16 v[188:191], a[236:239], a[196:199], v[188:191]\n\t"
"ds_read_b64_tr_b16 v[226:227], v241 offset:22528\n\t"
"v_mfma_f32_16x16x32_bf16 v[188:191], a[232:235], a[192:195], v[188:191]\n\t"
"ds_read_b128 a[184:187], v223 offset:26624\n\t"
"v_mfma_f32_16x16x32_bf16 v[192:195], a[232:235], a[196:199], v[192:195]\n\t"
"s_waitcnt lgkmcnt(8)\n\t"
"ds_read_b128 a[188:191], v223 offset:32768\n\t"
"v_mfma_f32_16x16x32_bf16 v[192:195], a[236:239], a[200:203], v[192:195]\n\t"
"s_waitcnt lgkmcnt(7)\n\t"
"ds_read_b128 a[192:195], v222 offset:40448\n\t"
"v_mfma_f32_16x16x32_bf16 v[192:195], a[240:243], a[204:207], v[192:195]\n\t"
"s_waitcnt lgkmcnt(5)\n\t"
"ds_read_b128 a[196:199], v223 offset:46592\n\t"
"v_mfma_f32_16x16x32_bf16 v[236:239], v[236:239], a[208:211], 0\n\t"
"s_waitcnt lgkmcnt(4)\n\t"
"ds_read_b128 a[200:203], v223 offset:52736\n\t"
"v_mfma_f32_16x16x32_bf16 v[228:231], v[228:231], a[208:211], 0\n\t"
"ds_read_b128 a[204:207], v222 offset:60416\n\t"
"v_mfma_f32_16x16x32_bf16 v[232:235], v[232:235], a[208:211], 0\n\t"
"v_mfma_f32_16x16x32_bf16 v[224:227], v[224:227], a[208:211], 0\n\t"
"Lst5_%=:\n\t"
"v_mfma_f32_16x16x32_bf16 v[196:199], a[244:247], a[168:171], v[196:199]\n\t"
"v_mfma_f32_16x16x32_bf16 v[196:199], a[252:255], a[176:179], v[196:199]\n\t"
"v_cvt_pk_bf16_f32 v236, v236, v237\n\t"
"v_cvt_pk_bf16_f32 v237, v238, v239\n\t"
"v_mfma_f32_16x16x32_bf16 v[196:199], a[248:251], a[172:175], v[196:199]\n\t"
"v_cvt_pk_bf16_f32 v238, v228, v229\n\t"
"v_cvt_pk_bf16_f32 v239, v230, v231\n\t"
"v_mfma_f32_16x16x32_bf16 v[204:207], a[244:247], a[172:175], v[204:207]\n\t"
"s_waitcnt lgkmcnt(5)\n\t"
"v_cvt_pk_bf16_f32 v232, v232, v233\n\t"
"v_cvt_pk_bf16_f32 v233, v234, v235\n\t"
"v_mfma_f32_16x16x32_bf16 v[204:207], a[248:251], a[176:179], v[204:207]\n\t"
"v_cvt_pk_bf16_f32 v234, v224, v225\n\t"
"v_cvt_pk_bf16_f32 v235, v226, v227\n\t"
"v_mfma_f32_16x16x32_bf16 v[204:207], a[252:255], a[180:183], v[204:207]\n\t"
"ds_write_b128 v242, v[236:239] offset:29184\n\t"
"v_mfma_f32_16x16x32_bf16 v[212:215], a[252:255], a[184:187], v[212:215]\n\t"
"s_waitcnt lgkmcnt(5)\n\t"
"s_mov_b32 s20, 430080\n\t"
"v_mfma_f32_16x16x32_bf16 v[212:215], a[248:251], a[180:183], v[212:215]\n\t"
"v_mfma_f32_16x16x32_bf16 v[212:215], a[244:247], a[176:179], v[212:215]\n\t"
"ds_write_b128 v242, v[232:235] offset:30720\n\t"
"v_mfma_f32_16x16x32_bf16 a[0:3], a[244:247], a[180:183], a[0:3]\n\t"
"s_waitcnt lgkmcnt(5)\n\t"
"v_mfma_f32_16x16x32_bf16 a[0:3], a[248:251], a[184:187], a[0:3]\n\t"
"v_mfma_f32_16x16x32_bf16 a[0:3], a[252:255], a[188:191], a[0:3]\n\t"
"s_barrier\n\t"
"v_mfma_f32_16x16x32_bf16 a[8:11], a[252:255], a[192:195], a[8:11]\n\t"
"s_waitcnt lgkmcnt(4)\n\t"
"ds_read_b64_tr_b16 v[228:229], v246 offset:1024\n\t"
"v_mfma_f32_16x16x32_bf16 a[8:11], a[248:251], a[188:191], a[8:11]\n\t"
"buffer_load_dwordx4 a[216:219], off, s[80:83], s20 offset:0 sc1\n\t"
"v_mfma_f32_16x16x32_bf16 a[8:11], a[244:247], a[184:187], a[8:11]\n\t"
"ds_read_b64_tr_b16 v[224:225], v255 offset:1024\n\t"
"v_mfma_f32_16x16x32_bf16 a[16:19], a[244:247], a[188:191], a[16:19]\n\t"
"s_waitcnt lgkmcnt(5)\n\t"
"ds_read_b64_tr_b16 v[236:237], v246 offset:7168\n\t"
"v_mfma_f32_16x16x32_bf16 a[16:19], a[248:251], a[192:195], a[16:19]\n\t"
"ds_read_b64_tr_b16 v[232:233], v255 offset:7168\n\t"
"v_mfma_f32_16x16x32_bf16 a[16:19], a[252:255], a[196:199], a[16:19]\n\t"
"buffer_load_dwordx4 a[220:223], off, s[80:83], s20 offset:1024 sc1\n\t"
"v_mfma_f32_16x16x32_bf16 a[24:27], a[252:255], a[200:203], a[24:27]\n\t"
"s_waitcnt lgkmcnt(6)\n\t"
"v_mfma_f32_16x16x32_bf16 a[24:27], a[248:251], a[196:199], a[24:27]\n\t"
"buffer_load_dwordx4 a[224:227], off, s[80:83], s20 offset:2048 sc1\n\t"
"v_mfma_f32_16x16x32_bf16 a[24:27], a[244:247], a[192:195], a[24:27]\n\t"
"s_mov_b32 s20, 442368\n\t"
"v_mfma_f32_16x16x32_bf16 a[32:35], a[244:247], a[196:199], a[32:35]\n\t"
"s_waitcnt lgkmcnt(3)\n\t"
"v_mfma_f32_16x16x32_bf16 a[32:35], a[248:251], a[200:203], a[32:35]\n\t"
"s_waitcnt lgkmcnt(2)\n\t"
"v_mfma_f32_16x16x32_bf16 a[32:35], a[252:255], a[204:207], a[32:35]\n\t"
"buffer_load_dwordx4 a[228:231], off, s[80:83], s20 offset:0 sc1\n\t"
"v_mfma_f32_4x4x4_16b_bf16 v[228:231], v[228:229], v[220:221], 0\n\t"
"s_waitcnt lgkmcnt(0)\n\t"
"v_mfma_f32_4x4x4_16b_bf16 v[224:227], v[224:225], v[220:221], 0\n\t"
"s_waitcnt vmcnt(4)\n\t"
"v_mfma_f32_4x4x4_16b_bf16 v[236:239], v[236:237], v[220:221], 0\n\t"
"v_mfma_f32_4x4x4_16b_bf16 v[232:235], v[232:233], v[220:221], 0\n\t"
"v_mfma_f32_16x16x32_bf16 a[40:43], v[248:251], a[168:171], a[40:43]\n\t"
"buffer_load_dwordx4 a[232:235], off, s[80:83], s20 offset:1024 sc1\n\t"
"v_mfma_f32_16x16x32_bf16 a[40:43], v[96:99], a[176:179], a[40:43]\n\t"
"v_cvt_pk_bf16_f32 v228, v228, v229\n\t"
"v_cvt_pk_bf16_f32 v229, v230, v231\n\t"
"v_mfma_f32_16x16x32_bf16 a[40:43], a[212:215], a[172:175], a[40:43]\n\t"
"v_cvt_pk_bf16_f32 v230, v224, v225\n\t"
"v_cvt_pk_bf16_f32 v231, v226, v227\n\t"
"v_mfma_f32_16x16x32_bf16 a[48:51], v[248:251], a[172:175], a[48:51]\n\t"
"ds_read_b64_tr_b16 v[224:225], v246 offset:13312\n\t"
"v_cvt_pk_bf16_f32 v236, v236, v237\n\t"
"v_mfma_f32_16x16x32_bf16 a[48:51], a[212:215], a[176:179], a[48:51]\n\t"
"v_cvt_pk_bf16_f32 v237, v238, v239\n\t"
"v_cvt_pk_bf16_f32 v238, v232, v233\n\t"
"v_mfma_f32_16x16x32_bf16 a[48:51], v[96:99], a[180:183], a[48:51]\n\t"
"buffer_load_dwordx4 a[236:239], off, s[80:83], s20 offset:2048 sc1\n\t"
"v_cvt_pk_bf16_f32 v239, v234, v235\n\t"
"s_mov_b32 s20, 454656\n\t"
"v_mfma_f32_16x16x32_bf16 a[56:59], v[96:99], a[184:187], a[56:59]\n\t"
"ds_read_b64_tr_b16 v[232:233], v255 offset:13312\n\t"
"v_mfma_f32_16x16x32_bf16 a[56:59], a[212:215], a[180:183], a[56:59]\n\t"
"buffer_load_dwordx4 a[240:243], off, s[80:83], s20 offset:0 sc1\n\t"
"v_mfma_f32_16x16x32_bf16 a[56:59], v[248:251], a[176:179], a[56:59]\n\t"
"ds_write_b128 v245, v[228:231] offset:1024\n\t"
"v_mfma_f32_16x16x32_bf16 a[64:67], v[248:251], a[180:183], a[64:67]\n\t"
"buffer_load_dwordx4 a[244:247], off, s[80:83], s20 offset:1024 sc1\n\t"
"v_mfma_f32_16x16x32_bf16 a[64:67], a[212:215], a[184:187], a[64:67]\n\t"
"ds_read_b64_tr_b16 v[228:229], v246 offset:20992\n\t"
"v_mfma_f32_16x16x32_bf16 a[64:67], v[96:99], a[188:191], a[64:67]\n\t"
"ds_write_b128 v245, v[236:239] offset:7168\n\t"
"v_mfma_f32_16x16x32_bf16 a[72:75], v[248:251], a[184:187], a[72:75]\n\t"
"buffer_load_dwordx4 a[248:251], off, s[80:83], s20 offset:2048 sc1\n\t"
"v_mfma_f32_16x16x32_bf16 a[72:75], v[96:99], a[192:195], a[72:75]\n\t"
"s_waitcnt lgkmcnt(3)\n\t"
"ds_read_b64_tr_b16 v[236:237], v255 offset:20992\n\t"
"v_mfma_f32_16x16x32_bf16 a[72:75], a[212:215], a[188:191], a[72:75]\n\t"
"s_mov_b32 s20, 466944\n\t"
"v_mfma_f32_4x4x4_16b_bf16 v[224:227], v[224:225], v[220:221], 0\n\t"
"s_waitcnt lgkmcnt(2)\n\t"
"v_mfma_f32_4x4x4_16b_bf16 v[232:235], v[232:233], v[220:221], 0\n\t"
"v_mfma_f32_4x4x4_16b_bf16 v[228:231], v[228:229], v[220:221], 0\n\t"
"s_waitcnt lgkmcnt(0)\n\t"
"buffer_load_dwordx4 a[252:255], off, s[80:83], s20 offset:0 sc1\n\t"
"v_mfma_f32_4x4x4_16b_bf16 v[236:239], v[236:237], v[220:221], 0\n\t"
"v_mfma_f32_16x16x32_bf16 a[80:83], v[248:251], a[188:191], a[80:83]\n\t"
"v_cvt_pk_bf16_f32 v224, v224, v225\n\t"
"v_cvt_pk_bf16_f32 v225, v226, v227\n\t"
"v_mfma_f32_16x16x32_bf16 a[80:83], v[96:99], a[196:199], a[80:83]\n\t"
"v_cvt_pk_bf16_f32 v226, v232, v233\n\t"
"v_cvt_pk_bf16_f32 v227, v234, v235\n\t"
"v_mfma_f32_16x16x32_bf16 a[80:83], a[212:215], a[192:195], a[80:83]\n\t"
"ds_write_b128 v245, v[224:227] offset:13312\n\t"
"v_mfma_f32_16x16x32_bf16 a[88:91], v[248:251], a[192:195], a[88:91]\n\t"
"v_cvt_pk_bf16_f32 v228, v228, v229\n\t"
"v_cvt_pk_bf16_f32 v229, v230, v231\n\t"
"v_mfma_f32_16x16x32_bf16 a[88:91], a[212:215], a[196:199], a[88:91]\n\t"
"ds_read_b64_tr_b16 v[232:233], v246 offset:27136\n\t"
"v_mfma_f32_16x16x32_bf16 a[88:91], v[96:99], a[200:203], a[88:91]\n\t"
"v_cvt_pk_bf16_f32 v230, v236, v237\n\t"
"v_cvt_pk_bf16_f32 v231, v238, v239\n\t"
"v_mfma_f32_16x16x32_bf16 a[96:99], a[212:215], a[200:203], a[96:99]\n\t"
"s_waitcnt vmcnt(7)\n\t"
"ds_read_b64_tr_b16 v[236:237], v255 offset:27136\n\t"
"v_mfma_f32_16x16x32_bf16 a[96:99], v[248:251], a[196:199], a[96:99]\n\t"
"ds_read_b64_tr_b16 v[224:225], v246 offset:33280\n\t"
"v_mfma_f32_16x16x32_bf16 a[96:99], v[96:99], a[204:207], a[96:99]\n\t"
"ds_write_b128 v245, v[228:231] offset:20992\n\t"
"v_mfma_f32_16x16x32_bf16 a[104:107], a[216:219], a[168:171], a[104:107]\n\t"
"buffer_load_dwordx4 a[212:215], off, s[80:83], s20 offset:1024 sc1\n\t"
"v_mfma_f32_16x16x32_bf16 a[104:107], a[224:227], a[176:179], a[104:107]\n\t"
"ds_read_b64_tr_b16 v[228:229], v255 offset:33280\n\t"
"v_mfma_f32_16x16x32_bf16 a[104:107], a[220:223], a[172:175], a[104:107]\n\t"
"buffer_load_dwordx4 v[96:99], off, s[80:83], s20 offset:2048 sc1\n\t"
"s_mov_b32 s20, 700416\n\t"
"v_mfma_f32_16x16x32_bf16 a[112:115], a[216:219], a[172:175], a[112:115]\n\t"
"buffer_load_dwordx4 v[248:251], off, s[80:83], s20 offset:0 sc1\n\t"
"v_mfma_f32_16x16x32_bf16 a[112:115], a[220:223], a[176:179], a[112:115]\n\t"
"s_waitcnt lgkmcnt(3)\n\t"
"ds_read_b128 a[168:171], v222 offset:768\n\t"
"v_mfma_f32_16x16x32_bf16 a[112:115], a[224:227], a[180:183], a[112:115]\n\t"
"s_waitcnt lgkmcnt(3)\n\t"
"ds_read_b128 a[172:175], v223 offset:6912\n\t"
"v_mfma_f32_4x4x4_16b_bf16 v[232:235], v[232:233], v[220:221], 0\n\t"
"s_waitcnt lgkmcnt(2)\n\t"
"v_mfma_f32_4x4x4_16b_bf16 v[236:239], v[236:237], v[220:221], 0\n\t"
"v_mfma_f32_4x4x4_16b_bf16 v[224:227], v[224:225], v[220:221], 0\n\t"
"v_mfma_f32_4x4x4_16b_bf16 v[228:231], v[228:229], v[220:221], 0\n\t"
"v_mfma_f32_16x16x32_bf16 a[120:123], a[220:223], a[180:183], a[120:123]\n\t"
"v_cvt_pk_bf16_f32 v232, v232, v233\n\t"
"v_cvt_pk_bf16_f32 v233, v234, v235\n\t"
"v_mfma_f32_16x16x32_bf16 a[120:123], a[216:219], a[176:179], a[120:123]\n\t"
"ds_read_b128 a[176:179], v223 offset:13056\n\t"
"v_cvt_pk_bf16_f32 v234, v236, v237\n\t"
"v_mfma_f32_16x16x32_bf16 a[120:123], a[224:227], a[184:187], a[120:123]\n\t"
"v_cvt_pk_bf16_f32 v235, v238, v239\n\t"
"v_cvt_pk_bf16_f32 v224, v224, v225\n\t"
"v_mfma_f32_16x16x32_bf16 a[128:131], a[216:219], a[180:183], a[128:131]\n\t"
"ds_read_b128 a[180:183], v222 offset:20736\n\t"
"v_cvt_pk_bf16_f32 v225, v226, v227\n\t"
"v_mfma_f32_16x16x32_bf16 a[128:131], a[224:227], a[188:191], a[128:131]\n\t"
"v_cvt_pk_bf16_f32 v226, v228, v229\n\t"
"v_cvt_pk_bf16_f32 v227, v230, v231\n\t"
"v_mfma_f32_16x16x32_bf16 a[128:131], a[220:223], a[184:187], a[128:131]\n\t"
"ds_write_b128 v245, v[232:235] offset:27136\n\t"
"v_mfma_f32_16x16x32_bf16 a[136:139], a[216:219], a[184:187], a[136:139]\n\t"
"v_mfma_f32_16x16x32_bf16 a[136:139], a[220:223], a[188:191], a[136:139]\n\t"
"ds_read_b128 a[184:187], v223 offset:26880\n\t"
"v_mfma_f32_16x16x32_bf16 a[136:139], a[224:227], a[192:195], a[136:139]\n\t"
"ds_write_b128 v245, v[224:227] offset:33280\n\t"
"v_mfma_f32_16x16x32_bf16 a[144:147], a[224:227], a[196:199], a[144:147]\n\t"
"v_mfma_f32_16x16x32_bf16 a[144:147], a[220:223], a[192:195], a[144:147]\n\t"
"v_mfma_f32_16x16x32_bf16 a[144:147], a[216:219], a[188:191], a[144:147]\n\t"
"ds_read_b128 a[188:191], v223 offset:33024\n\t"
"v_mfma_f32_16x16x32_bf16 a[152:155], a[216:219], a[192:195], a[152:155]\n\t"
"ds_read_b128 a[192:195], v222 offset:40704\n\t"
"v_mfma_f32_16x16x32_bf16 a[152:155], a[220:223], a[196:199], a[152:155]\n\t"
"v_mfma_f32_16x16x32_bf16 a[152:155], a[224:227], a[200:203], a[152:155]\n\t"
"s_barrier\n\t"
"v_mfma_f32_16x16x32_bf16 a[160:163], a[220:223], a[200:203], a[160:163]\n\t"
"s_waitcnt vmcnt(7) lgkmcnt(6)\n\t"
"ds_read_b64_tr_b16 v[236:237], v240 offset:40960\n\t"
"v_mfma_f32_16x16x32_bf16 a[160:163], a[216:219], a[196:199], a[160:163]\n\t"
"ds_read_b64_tr_b16 v[238:239], v240 offset:42496\n\t"
"v_mfma_f32_16x16x32_bf16 a[160:163], a[224:227], a[204:207], a[160:163]\n\t"
"ds_read_b64_tr_b16 v[228:229], v241 offset:40960\n\t"
"v_mfma_f32_16x16x32_bf16 v[200:203], a[228:231], a[168:171], v[200:203]\n\t"
"s_waitcnt lgkmcnt(8)\n\t"
"ds_read_b64_tr_b16 v[230:231], v241 offset:42496\n\t"
"v_subrev_u32 v240, 59904, v240\n\t"
"v_mfma_f32_16x16x32_bf16 v[200:203], a[236:239], a[176:179], v[200:203]\n\t"
"ds_read_b128 a[196:199], v223 offset:46848\n\t"
"v_subrev_u32 v241, 59904, v241\n\t"
"v_mfma_f32_16x16x32_bf16 v[200:203], a[232:235], a[172:175], v[200:203]\n\t"
"ds_read_b64_tr_b16 v[232:233], v240 offset:40960\n\t"
"v_mfma_f32_16x16x32_bf16 v[208:211], a[228:231], a[172:175], v[208:211]\n\t"
"s_waitcnt lgkmcnt(4)\n\t"
"ds_read_b64_tr_b16 v[234:235], v240 offset:42496\n\t"
"v_mfma_f32_16x16x32_bf16 v[208:211], a[232:235], a[176:179], v[208:211]\n\t"
"buffer_load_dwordx4 a[220:223], off, s[80:83], s20 offset:1024 sc1\n\t"
"v_mfma_f32_16x16x32_bf16 v[208:211], a[236:239], a[180:183], v[208:211]\n\t"
"s_waitcnt lgkmcnt(3)\n\t"
"ds_read_b64_tr_b16 v[224:225], v241 offset:40960\n\t"
"ds_read_b64_tr_b16 v[226:227], v241 offset:42496\n\t"
"v_mfma_f32_16x16x32_bf16 v[236:239], v[236:239], a[208:211], 0\n\t"
"buffer_load_dwordx4 a[216:219], off, s[80:83], s20 offset:2048 sc1\n\t"
"s_mov_b32 s20, 712704\n\t"
"v_mfma_f32_16x16x32_bf16 v[228:231], v[228:231], a[208:211], 0\n\t"
"s_waitcnt lgkmcnt(2)\n\t"
"buffer_load_dwordx4 a[224:227], off, s[80:83], s20 offset:0 sc1\n\t"
"v_mfma_f32_16x16x32_bf16 v[232:235], v[232:235], a[208:211], 0\n\t"
"s_waitcnt lgkmcnt(0)\n\t"
"ds_read_b128 a[200:203], v223 offset:52992\n\t"
"v_mfma_f32_16x16x32_bf16 v[224:227], v[224:227], a[208:211], 0\n\t"
"ds_read_b128 a[204:207], v222 offset:60672\n\t"
"v_mfma_f32_16x16x32_bf16 v[216:219], a[232:235], a[180:183], v[216:219]\n\t"
"v_mfma_f32_16x16x32_bf16 v[216:219], a[228:231], a[176:179], v[216:219]\n\t"
"v_cvt_pk_bf16_f32 v236, v236, v237\n\t"
"v_cvt_pk_bf16_f32 v237, v238, v239\n\t"
"v_mfma_f32_16x16x32_bf16 v[216:219], a[236:239], a[184:187], v[216:219]\n\t"
"v_cvt_pk_bf16_f32 v238, v228, v229\n\t"
"v_cvt_pk_bf16_f32 v239, v230, v231\n\t"
"v_mfma_f32_16x16x32_bf16 a[4:7], a[228:231], a[180:183], a[4:7]\n\t"
"ds_read_b64_tr_b16 v[228:229], v246 offset:40960\n\t"
"v_cvt_pk_bf16_f32 v232, v232, v233\n\t"
"v_mfma_f32_16x16x32_bf16 a[4:7], a[236:239], a[188:191], a[4:7]\n\t"
"v_cvt_pk_bf16_f32 v233, v234, v235\n\t"
"v_cvt_pk_bf16_f32 v234, v224, v225\n\t"
"v_mfma_f32_16x16x32_bf16 a[4:7], a[232:235], a[184:187], a[4:7]\n\t"
"ds_write_b128 v242, v[236:239] offset:32256\n\t"
"v_mfma_f32_16x16x32_bf16 a[12:15], a[228:231], a[184:187], a[12:15]\n\t"
"v_cvt_pk_bf16_f32 v235, v226, v227\n\t"
"v_mfma_f32_16x16x32_bf16 a[12:15], a[232:235], a[188:191], a[12:15]\n\t"
"ds_read_b64_tr_b16 v[224:225], v255 offset:40960\n\t"
"v_mfma_f32_16x16x32_bf16 a[12:15], a[236:239], a[192:195], a[12:15]\n\t"
"ds_write_b128 v242, v[232:235] offset:27648\n\t"
"v_mfma_f32_16x16x32_bf16 a[20:23], a[236:239], a[196:199], a[20:23]\n\t"
"s_waitcnt lgkmcnt(5)\n\t"
"v_mfma_f32_16x16x32_bf16 a[20:23], a[232:235], a[192:195], a[20:23]\n\t"
"ds_read_b64_tr_b16 v[236:237], v246 offset:47104\n\t"
"v_mfma_f32_16x16x32_bf16 a[20:23], a[228:231], a[188:191], a[20:23]\n\t"
"ds_read_b64_tr_b16 v[232:233], v255 offset:47104\n\t"
"v_mfma_f32_16x16x32_bf16 a[28:31], a[228:231], a[192:195], a[28:31]\n\t"
"s_waitcnt lgkmcnt(6)\n\t"
"v_mfma_f32_16x16x32_bf16 a[28:31], a[232:235], a[196:199], a[28:31]\n\t"
"v_mfma_f32_16x16x32_bf16 a[28:31], a[236:239], a[200:203], a[28:31]\n\t"
"s_barrier\n\t"
"v_mfma_f32_16x16x32_bf16 a[36:39], a[232:235], a[200:203], a[36:39]\n\t"
"s_waitcnt lgkmcnt(5)\n\t"
"v_mfma_f32_16x16x32_bf16 a[36:39], a[228:231], a[196:199], a[36:39]\n\t"
"buffer_load_dwordx4 a[232:235], off, s[80:83], s20 offset:1024 sc1\n\t"
"v_mfma_f32_16x16x32_bf16 a[36:39], a[236:239], a[204:207], a[36:39]\n\t"
"s_waitcnt lgkmcnt(1)\n\t"
"v_mfma_f32_4x4x4_16b_bf16 v[228:231], v[228:229], v[220:221], 0\n\t"
"s_waitcnt lgkmcnt(0)\n\t"
"v_mfma_f32_4x4x4_16b_bf16 v[224:227], v[224:225], v[220:221], 0\n\t"
"s_waitcnt vmcnt(8)\n\t"
"v_mfma_f32_4x4x4_16b_bf16 v[236:239], v[236:237], v[220:221], 0\n\t"
"v_mfma_f32_4x4x4_16b_bf16 v[232:235], v[232:233], v[220:221], 0\n\t"
"buffer_load_dwordx4 a[228:231], off, s[80:83], s20 offset:2048 sc1\n\t"
"s_mov_b32 s20, 724992\n\t"
"v_mfma_f32_16x16x32_bf16 a[44:47], a[240:243], a[168:171], a[44:47]\n\t"
"buffer_load_dwordx4 a[236:239], off, s[80:83], s20 offset:0 sc1\n\t"
"v_mfma_f32_16x16x32_bf16 a[44:47], a[248:251], a[176:179], a[44:47]\n\t"
"v_cvt_pk_bf16_f32 v228, v228, v229\n\t"
"v_cvt_pk_bf16_f32 v229, v230, v231\n\t"
"v_mfma_f32_16x16x32_bf16 a[44:47], a[244:247], a[172:175], a[44:47]\n\t"
"v_cvt_pk_bf16_f32 v230, v224, v225\n\t"
"v_cvt_pk_bf16_f32 v231, v226, v227\n\t"
"v_mfma_f32_16x16x32_bf16 a[52:55], a[240:243], a[172:175], a[52:55]\n\t"
"ds_read_b64_tr_b16 v[224:225], v246 offset:53248\n\t"
"v_cvt_pk_bf16_f32 v236, v236, v237\n\t"
"v_mfma_f32_16x16x32_bf16 a[52:55], a[244:247], a[176:179], a[52:55]\n\t"
"v_cvt_pk_bf16_f32 v237, v238, v239\n\t"
"v_cvt_pk_bf16_f32 v238, v232, v233\n\t"
"v_mfma_f32_16x16x32_bf16 a[52:55], a[248:251], a[180:183], a[52:55]\n\t"
"ds_write_b128 v245, v[228:231] offset:40960\n\t"
"v_cvt_pk_bf16_f32 v239, v234, v235\n\t"
"v_mfma_f32_16x16x32_bf16 a[60:63], a[248:251], a[184:187], a[60:63]\n\t"
"v_mfma_f32_16x16x32_bf16 a[60:63], a[244:247], a[180:183], a[60:63]\n\t"
"ds_read_b64_tr_b16 v[232:233], v255 offset:53248\n\t"
"v_mfma_f32_16x16x32_bf16 a[60:63], a[240:243], a[176:179], a[60:63]\n\t"
"ds_write_b128 v245, v[236:239] offset:47104\n\t"
"v_mfma_f32_16x16x32_bf16 a[68:71], a[240:243], a[180:183], a[68:71]\n\t"
"v_mfma_f32_16x16x32_bf16 a[68:71], a[244:247], a[184:187], a[68:71]\n\t"
"v_mfma_f32_16x16x32_bf16 a[68:71], a[248:251], a[188:191], a[68:71]\n\t"
"v_mfma_f32_16x16x32_bf16 a[76:79], a[240:243], a[184:187], a[76:79]\n\t"
"s_waitcnt lgkmcnt(3)\n\t"
"v_mfma_f32_16x16x32_bf16 a[76:79], a[248:251], a[192:195], a[76:79]\n\t"
"s_waitcnt lgkmcnt(1)\n\t"
"v_mfma_f32_16x16x32_bf16 a[76:79], a[244:247], a[188:191], a[76:79]\n\t"
"v_mfma_f32_4x4x4_16b_bf16 v[224:227], v[224:225], v[220:221], 0\n\t"
"v_mfma_f32_4x4x4_16b_bf16 v[232:235], v[232:233], v[220:221], 0\n\t"
"v_mfma_f32_16x16x32_bf16 a[84:87], a[240:243], a[188:191], a[84:87]\n\t"
"v_mfma_f32_16x16x32_bf16 a[84:87], a[248:251], a[196:199], a[84:87]\n\t"
"v_cvt_pk_bf16_f32 v224, v224, v225\n\t"
"v_cvt_pk_bf16_f32 v225, v226, v227\n\t"
"v_mfma_f32_16x16x32_bf16 a[84:87], a[244:247], a[192:195], a[84:87]\n\t"
"v_cvt_pk_bf16_f32 v226, v232, v233\n\t"
"v_cvt_pk_bf16_f32 v227, v234, v235\n\t"
"v_mfma_f32_16x16x32_bf16 a[92:95], a[240:243], a[192:195], a[92:95]\n\t"
"v_mfma_f32_16x16x32_bf16 a[92:95], a[244:247], a[196:199], a[92:95]\n\t"
"s_waitcnt vmcnt(7)\n\t"
"v_mfma_f32_16x16x32_bf16 a[92:95], a[248:251], a[200:203], a[92:95]\n\t"
"ds_write_b128 v245, v[224:227] offset:53248\n\t"
"v_mfma_f32_16x16x32_bf16 a[100:103], a[244:247], a[200:203], a[100:103]\n\t"
"buffer_load_dwordx4 a[244:247], off, s[80:83], s20 offset:1024 sc1\n\t"
"v_mfma_f32_16x16x32_bf16 a[100:103], a[240:243], a[196:199], a[100:103]\n\t"
"Lpg5_%=:\n\t"
"s_cbranch_vccnz Lpg9_%=\n\t"
"v_mfma_f32_16x16x32_bf16 a[100:103], a[248:251], a[204:207], a[100:103]\n\t"
"v_mfma_f32_16x16x32_bf16 a[108:111], a[252:255], a[168:171], a[108:111]\n\t"
"buffer_load_dwordx4 a[240:243], off, s[80:83], s20 offset:2048 sc1\n\t"
"v_mfma_f32_16x16x32_bf16 a[108:111], v[96:99], a[176:179], a[108:111]\n\t"
"s_mov_b32 s20, 737280\n\t"
"v_mfma_f32_16x16x32_bf16 a[108:111], a[212:215], a[172:175], a[108:111]\n\t"
"s_waitcnt lgkmcnt(0)\n\t"
"s_barrier\n\t"
"v_mfma_f32_16x16x32_bf16 a[116:119], a[252:255], a[172:175], a[116:119]\n\t"
"ds_read_b128 a[168:171], v222 offset:1024\n\t"
"v_mfma_f32_16x16x32_bf16 a[116:119], a[212:215], a[176:179], a[116:119]\n\t"
"buffer_load_dwordx4 a[248:251], off, s[80:83], s20 offset:0 sc1\n\t"
"v_mfma_f32_16x16x32_bf16 a[116:119], v[96:99], a[180:183], a[116:119]\n\t"
"ds_read_b128 a[172:175], v223 offset:7168\n\t"
"v_mfma_f32_16x16x32_bf16 a[124:127], v[96:99], a[184:187], a[124:127]\n\t"
"v_mfma_f32_16x16x32_bf16 a[124:127], a[212:215], a[180:183], a[124:127]\n\t"
"v_mfma_f32_16x16x32_bf16 a[124:127], a[252:255], a[176:179], a[124:127]\n\t"
"ds_read_b128 a[176:179], v223 offset:13312\n\t"
"v_mfma_f32_16x16x32_bf16 a[132:135], a[252:255], a[180:183], a[132:135]\n\t"
"ds_read_b64_tr_b16 v[228:229], v240 offset:1536\n\t"
"v_mfma_f32_16x16x32_bf16 a[132:135], a[212:215], a[184:187], a[132:135]\n\t"
"ds_read_b64_tr_b16 v[230:231], v240 offset:3072\n\t"
"v_mfma_f32_16x16x32_bf16 a[132:135], v[96:99], a[188:191], a[132:135]\n\t"
"ds_read_b64_tr_b16 v[236:237], v241 offset:1536\n\t"
"v_mfma_f32_16x16x32_bf16 a[140:143], v[96:99], a[192:195], a[140:143]\n\t"
"ds_read_b64_tr_b16 v[238:239], v241 offset:3072\n\t"
"v_mfma_f32_16x16x32_bf16 a[140:143], a[212:215], a[188:191], a[140:143]\n\t"
"ds_read_b128 a[180:183], v222 offset:20992\n\t"
"v_mfma_f32_16x16x32_bf16 a[140:143], a[252:255], a[184:187], a[140:143]\n\t"
"ds_read_b64_tr_b16 v[232:233], v240 offset:21504\n\t"
"v_mfma_f32_16x16x32_bf16 a[148:151], a[252:255], a[188:191], a[148:151]\n\t"
"ds_read_b64_tr_b16 v[234:235], v240 offset:23040\n\t"
"v_mfma_f32_16x16x32_bf16 a[148:151], a[212:215], a[192:195], a[148:151]\n\t"
"ds_read_b64_tr_b16 v[224:225], v241 offset:21504\n\t"
"v_mfma_f32_16x16x32_bf16 a[148:151], v[96:99], a[196:199], a[148:151]\n\t"
"ds_read_b64_tr_b16 v[226:227], v241 offset:23040\n\t"
"v_mfma_f32_16x16x32_bf16 a[156:159], v[96:99], a[200:203], a[156:159]\n\t"
"ds_read_b128 a[184:187], v223 offset:27136\n\t"
"v_mfma_f32_16x16x32_bf16 a[156:159], a[212:215], a[196:199], a[156:159]\n\t"
"ds_read_b128 a[188:191], v223 offset:33280\n\t"
"v_mfma_f32_16x16x32_bf16 a[156:159], a[252:255], a[192:195], a[156:159]\n\t"
"ds_read_b128 a[192:195], v222 offset:40960\n\t"
"v_mfma_f32_16x16x32_bf16 a[164:167], a[252:255], a[196:199], a[164:167]\n\t"
"buffer_load_dwordx4 a[252:255], off, s[80:83], s20 offset:1024 sc1\n\t"
"v_mfma_f32_16x16x32_bf16 a[164:167], a[212:215], a[200:203], a[164:167]\n\t"
"s_waitcnt lgkmcnt(8)\n\t"
"ds_read_b128 a[196:199], v223 offset:47104\n\t"
"v_mfma_f32_16x16x32_bf16 a[164:167], v[96:99], a[204:207], a[164:167]\n\t"
"s_waitcnt lgkmcnt(6)\n\t"
"ds_read_b128 a[200:203], v223 offset:53248\n\t"
"v_mfma_f32_16x16x32_bf16 v[228:231], v[228:231], a[208:211], 0\n\t"
"buffer_load_dwordx4 v[96:99], off, s[80:83], s20 offset:2048 sc1\n\t"
"v_mfma_f32_16x16x32_bf16 v[236:239], v[236:239], a[208:211], 0\n\t"
"s_waitcnt vmcnt(9) lgkmcnt(5)\n\t"
"ds_read_b128 a[204:207], v222 offset:60928\n\t"
"s_mov_b32 s20, 749568\n\t"
"v_mfma_f32_16x16x32_bf16 v[232:235], v[232:235], a[208:211], 0\n\t"
"v_mfma_f32_16x16x32_bf16 v[224:227], v[224:227], a[208:211], 0\n\t"
"buffer_load_dwordx4 a[212:215], off, s[80:83], s20 offset:0 sc1\n\t"
"Lst6_%=:\n\t"
"v_mfma_f32_16x16x32_bf16 v[196:199], v[248:251], a[168:171], v[196:199]\n\t"
"v_mfma_f32_16x16x32_bf16 v[196:199], a[216:219], a[176:179], v[196:199]\n\t"
"v_cvt_pk_bf16_f32 v228, v228, v229\n\t"
"v_cvt_pk_bf16_f32 v229, v230, v231\n\t"
"v_mfma_f32_16x16x32_bf16 v[196:199], a[220:223], a[172:175], v[196:199]\n\t"
"v_cvt_pk_bf16_f32 v230, v236, v237\n\t"
"v_cvt_pk_bf16_f32 v231, v238, v239\n\t"
"v_mfma_f32_16x16x32_bf16 v[204:207], v[248:251], a[172:175], v[204:207]\n\t"
"s_waitcnt lgkmcnt(5)\n\t"
"v_cvt_pk_bf16_f32 v232, v232, v233\n\t"
"v_cvt_pk_bf16_f32 v233, v234, v235\n\t"
"v_mfma_f32_16x16x32_bf16 v[204:207], a[220:223], a[176:179], v[204:207]\n\t"
"v_cvt_pk_bf16_f32 v234, v224, v225\n\t"
"v_cvt_pk_bf16_f32 v235, v226, v227\n\t"
"v_mfma_f32_16x16x32_bf16 v[204:207], a[216:219], a[180:183], v[204:207]\n\t"
"ds_write_b128 v242, v[228:231] offset:0\n\t"
"v_mfma_f32_16x16x32_bf16 v[212:215], a[216:219], a[184:187], v[212:215]\n\t"
"s_waitcnt lgkmcnt(5)\n\t"
"v_mfma_f32_16x16x32_bf16 v[212:215], a[220:223], a[180:183], v[212:215]\n\t"
"v_mfma_f32_16x16x32_bf16 v[212:215], v[248:251], a[176:179], v[212:215]\n\t"
"ds_write_b128 v242, v[232:235] offset:1536\n\t"
"v_mfma_f32_16x16x32_bf16 a[0:3], v[248:251], a[180:183], a[0:3]\n\t"
"s_waitcnt lgkmcnt(5)\n\t"
"v_mfma_f32_16x16x32_bf16 a[0:3], a[220:223], a[184:187], a[0:3]\n\t"
"ds_read_b64_tr_b16 v[236:237], v240 offset:41472\n\t"
"v_mfma_f32_16x16x32_bf16 a[0:3], a[216:219], a[188:191], a[0:3]\n\t"
"ds_read_b64_tr_b16 v[238:239], v240 offset:43008\n\t"
"v_mfma_f32_16x16x32_bf16 a[8:11], a[216:219], a[192:195], a[8:11]\n\t"
"s_waitcnt lgkmcnt(6)\n\t"
"ds_read_b64_tr_b16 v[224:225], v241 offset:41472\n\t"
"v_mfma_f32_16x16x32_bf16 a[8:11], a[220:223], a[188:191], a[8:11]\n\t"
"ds_read_b64_tr_b16 v[226:227], v241 offset:43008\n\t"
"v_add_u32 v240, 59904, v240\n\t"
"v_mfma_f32_16x16x32_bf16 a[8:11], v[248:251], a[184:187], a[8:11]\n\t"
"v_add_u32 v241, 59904, v241\n\t"
"v_mfma_f32_16x16x32_bf16 a[16:19], v[248:251], a[188:191], a[16:19]\n\t"
"s_waitcnt lgkmcnt(7)\n\t"
"ds_read_b64_tr_b16 v[228:229], v240 offset:1536\n\t"
"v_mfma_f32_16x16x32_bf16 a[16:19], a[220:223], a[192:195], a[16:19]\n\t"
"ds_read_b64_tr_b16 v[230:231], v240 offset:3072\n\t"
"v_mfma_f32_16x16x32_bf16 a[16:19], a[216:219], a[196:199], a[16:19]\n\t"
"ds_read_b64_tr_b16 v[232:233], v241 offset:1536\n\t"
"v_mfma_f32_16x16x32_bf16 a[24:27], a[216:219], a[200:203], a[24:27]\n\t"
"s_waitcnt lgkmcnt(9)\n\t"
"ds_read_b64_tr_b16 v[234:235], v241 offset:3072\n\t"
"v_mfma_f32_16x16x32_bf16 a[24:27], a[220:223], a[196:199], a[24:27]\n\t"
"v_mfma_f32_16x16x32_bf16 a[24:27], v[248:251], a[192:195], a[24:27]\n\t"
"v_mfma_f32_16x16x32_bf16 a[32:35], v[248:251], a[196:199], a[32:35]\n\t"
"buffer_load_dwordx4 v[248:251], off, s[80:83], s20 offset:1024 sc1\n\t"
"v_mfma_f32_16x16x32_bf16 a[32:35], a[220:223], a[200:203], a[32:35]\n\t"
"s_waitcnt lgkmcnt(4)\n\t"
"v_mfma_f32_16x16x32_bf16 a[32:35], a[216:219], a[204:207], a[32:35]\n\t"
"s_waitcnt lgkmcnt(2)\n\t"
"v_mfma_f32_16x16x32_bf16 v[236:239], v[236:239], a[208:211], 0\n\t"
"buffer_load_dwordx4 a[216:219], off, s[80:83], s20 offset:2048 sc1\n\t"
"v_mfma_f32_16x16x32_bf16 v[224:227], v[224:227], a[208:211], 0\n\t"
"s_waitcnt vmcnt(9) lgkmcnt(0)\n\t"
"s_mov_b32 s20, 761856\n\t"
"v_mfma_f32_16x16x32_bf16 v[228:231], v[228:231], a[208:211], 0\n\t"
"v_mfma_f32_16x16x32_bf16 v[232:235], v[232:235], a[208:211], 0\n\t"
"buffer_load_dwordx4 a[220:223], off, s[80:83], s20 offset:0 sc1\n\t"
"v_mfma_f32_16x16x32_bf16 a[40:43], a[224:227], a[168:171], a[40:43]\n\t"
"v_mfma_f32_16x16x32_bf16 a[40:43], a[228:231], a[176:179], a[40:43]\n\t"
"v_cvt_pk_bf16_f32 v236, v236, v237\n\t"
"v_cvt_pk_bf16_f32 v237, v238, v239\n\t"
"v_mfma_f32_16x16x32_bf16 a[40:43], a[232:235], a[172:175], a[40:43]\n\t"
"v_cvt_pk_bf16_f32 v238, v224, v225\n\t"
"v_cvt_pk_bf16_f32 v239, v226, v227\n\t"
"v_mfma_f32_16x16x32_bf16 a[48:51], a[224:227], a[172:175], a[48:51]\n\t"
"v_cvt_pk_bf16_f32 v228, v228, v229\n\t"
"v_cvt_pk_bf16_f32 v229, v230, v231\n\t"
"v_mfma_f32_16x16x32_bf16 a[48:51], a[232:235], a[176:179], a[48:51]\n\t"
"v_cvt_pk_bf16_f32 v230, v232, v233\n\t"
"v_cvt_pk_bf16_f32 v231, v234, v235\n\t"
"v_mfma_f32_16x16x32_bf16 a[48:51], a[228:231], a[180:183], a[48:51]\n\t"
"ds_write_b128 v242, v[236:239] offset:3072\n\t"
"v_mfma_f32_16x16x32_bf16 a[56:59], a[228:231], a[184:187], a[56:59]\n\t"
"v_mfma_f32_16x16x32_bf16 a[56:59], a[232:235], a[180:183], a[56:59]\n\t"
"v_mfma_f32_16x16x32_bf16 a[56:59], a[224:227], a[176:179], a[56:59]\n\t"
"ds_write_b128 v242, v[228:231] offset:4608\n\t"
"v_mfma_f32_16x16x32_bf16 a[64:67], a[224:227], a[180:183], a[64:67]\n\t"
"v_mfma_f32_16x16x32_bf16 a[64:67], a[232:235], a[184:187], a[64:67]\n\t"
"ds_read_b64_tr_b16 v[224:225], v240 offset:21504\n\t"
"v_mfma_f32_16x16x32_bf16 a[64:67], a[228:231], a[188:191], a[64:67]\n\t"
"ds_read_b64_tr_b16 v[226:227], v240 offset:23040\n\t"
"v_mfma_f32_16x16x32_bf16 a[72:75], a[228:231], a[192:195], a[72:75]\n\t"
"ds_read_b64_tr_b16 v[232:233], v241 offset:21504\n\t"
"v_mfma_f32_16x16x32_bf16 a[72:75], a[232:235], a[188:191], a[72:75]\n\t"
"ds_read_b64_tr_b16 v[234:235], v241 offset:23040\n\t"
"v_mfma_f32_16x16x32_bf16 a[72:75], a[224:227], a[184:187], a[72:75]\n\t"
"ds_read_b64_tr_b16 v[236:237], v240 offset:41472\n\t"
"v_mfma_f32_16x16x32_bf16 a[80:83], a[224:227], a[188:191], a[80:83]\n\t"
"ds_read_b64_tr_b16 v[238:239], v240 offset:43008\n\t"
"v_mfma_f32_16x16x32_bf16 a[80:83], a[232:235], a[192:195], a[80:83]\n\t"
"ds_read_b64_tr_b16 v[228:229], v241 offset:41472\n\t"
"v_mfma_f32_16x16x32_bf16 a[80:83], a[228:231], a[196:199], a[80:83]\n\t"
"ds_read_b64_tr_b16 v[230:231], v241 offset:43008\n\t"
"v_mfma_f32_16x16x32_bf16 a[88:91], a[228:231], a[200:203], a[88:91]\n\t"
"v_mfma_f32_16x16x32_bf16 a[88:91], a[232:235], a[196:199], a[88:91]\n\t"
"v_mfma_f32_16x16x32_bf16 a[88:91], a[224:227], a[192:195], a[88:91]\n\t"
"v_mfma_f32_16x16x32_bf16 a[96:99], a[224:227], a[196:199], a[96:99]\n\t"
"buffer_load_dwordx4 a[224:227], off, s[80:83], s20 offset:1024 sc1\n\t"
"v_mfma_f32_16x16x32_bf16 a[96:99], a[232:235], a[200:203], a[96:99]\n\t"
"s_waitcnt lgkmcnt(4)\n\t"
"v_mfma_f32_16x16x32_bf16 a[96:99], a[228:231], a[204:207], a[96:99]\n\t"
"s_waitcnt lgkmcnt(2)\n\t"
"v_mfma_f32_16x16x32_bf16 v[224:227], v[224:227], a[208:211], 0\n\t"
"buffer_load_dwordx4 a[228:231], off, s[80:83], s20 offset:2048 sc1\n\t"
"v_mfma_f32_16x16x32_bf16 v[232:235], v[232:235], a[208:211], 0\n\t"
"s_waitcnt vmcnt(9) lgkmcnt(0)\n\t"
"s_mov_b32 s20, 626688\n\t"
"v_mfma_f32_16x16x32_bf16 v[236:239], v[236:239], a[208:211], 0\n\t"
"v_mfma_f32_16x16x32_bf16 v[228:231], v[228:231], a[208:211], 0\n\t"
"buffer_load_dwordx4 a[232:235], off, s[80:83], s20 offset:0 sc1\n\t"
"v_mfma_f32_16x16x32_bf16 a[104:107], a[236:239], a[168:171], a[104:107]\n\t"
"ds_read_b128 a[168:171], v222 offset:1280\n\t"
"v_mfma_f32_16x16x32_bf16 a[104:107], a[240:243], a[176:179], a[104:107]\n\t"
"v_cvt_pk_bf16_f32 v224, v224, v225\n\t"
"v_cvt_pk_bf16_f32 v225, v226, v227\n\t"
"v_mfma_f32_16x16x32_bf16 a[104:107], a[244:247], a[172:175], a[104:107]\n\t"
"v_cvt_pk_bf16_f32 v226, v232, v233\n\t"
"v_cvt_pk_bf16_f32 v227, v234, v235\n\t"
"v_mfma_f32_16x16x32_bf16 a[112:115], a[236:239], a[172:175], a[112:115]\n\t"
"ds_read_b128 a[172:175], v223 offset:7424\n\t"
"v_cvt_pk_bf16_f32 v236, v236, v237\n\t"
"v_mfma_f32_16x16x32_bf16 a[112:115], a[244:247], a[176:179], a[112:115]\n\t"
"v_cvt_pk_bf16_f32 v237, v238, v239\n\t"
"v_cvt_pk_bf16_f32 v238, v228, v229\n\t"
"v_mfma_f32_16x16x32_bf16 a[112:115], a[240:243], a[180:183], a[112:115]\n\t"
"buffer_load_dwordx4 a[208:211], off, s[80:83], s20 offset:1024 sc1\n\t"
"v_mfma_f32_16x16x32_bf16 a[120:123], a[240:243], a[184:187], a[120:123]\n\t"
"v_cvt_pk_bf16_f32 v239, v230, v231\n\t"
"v_mfma_f32_16x16x32_bf16 a[120:123], a[244:247], a[180:183], a[120:123]\n\t"
"v_mfma_f32_16x16x32_bf16 a[120:123], a[236:239], a[176:179], a[120:123]\n\t"
"ds_write_b128 v242, v[224:227] offset:6144\n\t"
"v_mfma_f32_16x16x32_bf16 a[128:131], a[236:239], a[180:183], a[128:131]\n\t"
"v_mfma_f32_16x16x32_bf16 a[128:131], a[244:247], a[184:187], a[128:131]\n\t"
"ds_read_b128 a[176:179], v223 offset:13568\n\t"
"v_mfma_f32_16x16x32_bf16 a[128:131], a[240:243], a[188:191], a[128:131]\n\t"
"ds_write_b128 v242, v[236:239] offset:7680\n\t"
"v_mfma_f32_16x16x32_bf16 a[136:139], a[240:243], a[192:195], a[136:139]\n\t"
"buffer_load_dwordx4 v[240:243], off, s[80:83], s20 offset:2048 sc1\n\t"
"v_mfma_f32_16x16x32_bf16 a[136:139], a[244:247], a[188:191], a[136:139]\n\t"
"ds_read_b128 a[180:183], v222 offset:21248\n\t"
"v_mfma_f32_16x16x32_bf16 a[136:139], a[236:239], a[184:187], a[136:139]\n\t"
"ds_read_b128 a[184:187], v223 offset:27392\n\t"
"v_mfma_f32_16x16x32_bf16 a[144:147], a[236:239], a[188:191], a[144:147]\n\t"
"s_mov_b32 s20, 638976\n\t"
"ds_read_b128 a[188:191], v223 offset:33536\n\t"
"v_mfma_f32_16x16x32_bf16 a[144:147], a[244:247], a[192:195], a[144:147]\n\t"
"v_mfma_f32_16x16x32_bf16 a[144:147], a[240:243], a[196:199], a[144:147]\n\t"
"v_mfma_f32_16x16x32_bf16 a[152:155], a[240:243], a[200:203], a[152:155]\n\t"
"v_mfma_f32_16x16x32_bf16 a[152:155], a[244:247], a[196:199], a[152:155]\n\t"
"v_mfma_f32_16x16x32_bf16 a[152:155], a[236:239], a[192:195], a[152:155]\n\t"
"ds_read_b128 a[192:195], v222 offset:41216\n\t"
"v_mfma_f32_16x16x32_bf16 a[160:163], a[236:239], a[196:199], a[160:163]\n\t"
"buffer_load_dwordx4 a[236:239], off, s[80:83], s20 offset:0 sc1\n\t"
"v_mfma_f32_16x16x32_bf16 a[160:163], a[244:247], a[200:203], a[160:163]\n\t"
"s_waitcnt vmcnt(10) lgkmcnt(5)\n\t"
"ds_read_b128 a[196:199], v223 offset:47360\n\t"
"v_mfma_f32_16x16x32_bf16 a[160:163], a[240:243], a[204:207], a[160:163]\n\t"
"ds_read_b128 a[200:203], v223 offset:53504\n\t"
"v_mfma_f32_16x16x32_bf16 v[200:203], a[248:251], a[168:171], v[200:203]\n\t"
"s_waitcnt lgkmcnt(5)\n\t"
"ds_read_b128 a[204:207], v222 offset:61184\n\t"
"v_mfma_f32_16x16x32_bf16 v[200:203], v[96:99], a[176:179], v[200:203]\n\t"
"buffer_load_dwordx4 a[240:243], off, s[80:83], s20 offset:1024 sc1\n\t"
"v_mfma_f32_16x16x32_bf16 v[200:203], a[252:255], a[172:175], v[200:203]\n\t"
"s_add_u32 m0, s25, 256\n\t"
"v_mfma_f32_16x16x32_bf16 v[208:211], a[248:251], a[172:175], v[208:211]\n\t"
"buffer_load_dwordx4 a[244:247], off, s[80:83], s20 offset:2048 sc1\n\t"
"v_mfma_f32_16x16x32_bf16 v[208:211], a[252:255], a[176:179], v[208:211]\n\t"
"s_waitcnt lgkmcnt(5)\n\t"
"v_mfma_f32_16x16x32_bf16 v[208:211], v[96:99], a[180:183], v[208:211]\n\t"
"s_waitcnt lgkmcnt(0)\n\t"
"s_barrier\n\t"
"v_mfma_f32_16x16x32_bf16 v[216:219], v[96:99], a[184:187], v[216:219]\n\t"
"v_mfma_f32_16x16x32_bf16 v[216:219], a[252:255], a[180:183], v[216:219]\n\t"
"buffer_load_dwordx4 v254, s[12:15], 0 offen offset:256 nt lds\n\t"
"v_mfma_f32_16x16x32_bf16 v[216:219], a[248:251], a[176:179], v[216:219]\n\t"
"s_add_u32 m0, s26, 256\n\t"
"ds_read_b64_tr_b16 v[232:233], v246 offset:1536\n\t"
"v_mfma_f32_16x16x32_bf16 a[4:7], a[248:251], a[180:183], a[4:7]\n\t"
"ds_read_b64_tr_b16 v[228:229], v255 offset:1536\n\t"
"v_mfma_f32_16x16x32_bf16 a[4:7], a[252:255], a[184:187], a[4:7]\n\t"
"ds_read_b64_tr_b16 v[224:225], v246 offset:7680\n\t"
"v_mfma_f32_16x16x32_bf16 a[4:7], v[96:99], a[188:191], a[4:7]\n\t"
"s_mov_b64 exec, s[36:37]\n\t"
"buffer_load_dwordx4 v254, s[12:15], s27 offen offset:256 nt lds\n\t"
"s_mov_b64 exec, -1\n\t"
"s_add_u32 m0, s24, 256\n\t"
"s_add_u32 s28, s21, 0\n\t"
"v_mfma_f32_16x16x32_bf16 a[12:15], v[96:99], a[192:195], a[12:15]\n\t"
"buffer_load_dwordx4 v247, s[12:15], s28 offen offset:256 nt lds\n\t"
"v_mfma_f32_16x16x32_bf16 a[12:15], a[252:255], a[188:191], a[12:15]\n\t"
"s_add_u32 m0, s24, 6400\n\t"
"s_add_u32 s28, s21, 196608\n\t"
"ds_read_b64_tr_b16 v[236:237], v255 offset:7680\n\t"
"v_mfma_f32_16x16x32_bf16 a[12:15], a[248:251], a[184:187], a[12:15]\n\t"
"buffer_load_dwordx4 v247, s[12:15], s28 offen offset:256 nt lds\n\t"
"s_add_u32 m0, s24, 12544\n\t"
"s_add_u32 s28, s21, 393216\n\t"
"v_mfma_f32_16x16x32_bf16 a[20:23], a[248:251], a[188:191], a[20:23]\n\t"
"buffer_load_dwordx4 v247, s[12:15], s28 offen offset:256 nt lds\n\t"
"v_mfma_f32_16x16x32_bf16 a[20:23], a[252:255], a[192:195], a[20:23]\n\t"
"s_add_u32 m0, s24, 20224\n\t"
"s_add_u32 s28, s21, 589824\n\t"
"v_mfma_f32_16x16x32_bf16 a[20:23], v[96:99], a[196:199], a[20:23]\n\t"
"buffer_load_dwordx4 v247, s[12:15], s28 offen offset:256 nt lds\n\t"
"s_add_u32 m0, s24, 26368\n\t"
"s_add_u32 s28, s21, 786432\n\t"
"v_mfma_f32_16x16x32_bf16 a[28:31], v[96:99], a[200:203], a[28:31]\n\t"
"buffer_load_dwordx4 v247, s[12:15], s28 offen offset:256 nt lds\n\t"
"v_mfma_f32_16x16x32_bf16 a[28:31], a[252:255], a[196:199], a[28:31]\n\t"
"s_add_u32 m0, s24, 32512\n\t"
"s_add_u32 s28, s21, 983040\n\t"
"v_mfma_f32_16x16x32_bf16 a[28:31], a[248:251], a[192:195], a[28:31]\n\t"
"buffer_load_dwordx4 v247, s[12:15], s28 offen offset:256 nt lds\n\t"
"v_mfma_f32_16x16x32_bf16 a[36:39], a[248:251], a[196:199], a[36:39]\n\t"
"s_waitcnt lgkmcnt(3)\n\t"
"v_mfma_f32_16x16x32_bf16 a[36:39], a[252:255], a[200:203], a[36:39]\n\t"
"s_waitcnt lgkmcnt(2)\n\t"
"v_mfma_f32_16x16x32_bf16 a[36:39], v[96:99], a[204:207], a[36:39]\n\t"
"s_waitcnt lgkmcnt(1)\n\t"
"v_mfma_f32_4x4x4_16b_bf16 v[232:235], v[232:233], v[220:221], 0\n\t"
"s_waitcnt lgkmcnt(0)\n\t"
"v_mfma_f32_4x4x4_16b_bf16 v[228:231], v[228:229], v[220:221], 0\n\t"
"s_waitcnt vmcnt(17)\n\t"
"v_mfma_f32_4x4x4_16b_bf16 v[224:227], v[224:225], v[220:221], 0\n\t"
"v_mfma_f32_4x4x4_16b_bf16 v[236:239], v[236:237], v[220:221], 0\n\t"
"v_mfma_f32_16x16x32_bf16 a[44:47], a[212:215], a[168:171], a[44:47]\n\t"
"v_cvt_pk_bf16_f32 v232, v232, v233\n\t"
"v_cvt_pk_bf16_f32 v233, v234, v235\n\t"
"v_mfma_f32_16x16x32_bf16 a[44:47], a[216:219], a[176:179], a[44:47]\n\t"
"v_cvt_pk_bf16_f32 v234, v228, v229\n\t"
"v_cvt_pk_bf16_f32 v235, v230, v231\n\t"
"v_mfma_f32_16x16x32_bf16 a[44:47], v[248:251], a[172:175], a[44:47]\n\t"
"ds_write_b128 v245, v[232:235] offset:1536\n\t"
"v_mfma_f32_16x16x32_bf16 a[52:55], a[212:215], a[172:175], a[52:55]\n\t"
"v_cvt_pk_bf16_f32 v224, v224, v225\n\t"
"v_cvt_pk_bf16_f32 v225, v226, v227\n\t"
"v_mfma_f32_16x16x32_bf16 a[52:55], v[248:251], a[176:179], a[52:55]\n\t"
"v_cvt_pk_bf16_f32 v226, v236, v237\n\t"
"v_cvt_pk_bf16_f32 v227, v238, v239\n\t"
"v_mfma_f32_16x16x32_bf16 a[52:55], a[216:219], a[180:183], a[52:55]\n\t"
"ds_write_b128 v245, v[224:227] offset:7680\n\t"
"v_mfma_f32_16x16x32_bf16 a[60:63], a[216:219], a[184:187], a[60:63]\n\t"
"v_mfma_f32_16x16x32_bf16 a[60:63], v[248:251], a[180:183], a[60:63]\n\t"
"v_mfma_f32_16x16x32_bf16 a[60:63], a[212:215], a[176:179], a[60:63]\n\t"
"ds_read_b64_tr_b16 v[228:229], v246 offset:13824\n\t"
"v_mfma_f32_16x16x32_bf16 a[68:71], a[212:215], a[180:183], a[68:71]\n\t"
"ds_read_b64_tr_b16 v[236:237], v255 offset:13824\n\t"
"v_mfma_f32_16x16x32_bf16 a[68:71], v[248:251], a[184:187], a[68:71]\n\t"
"ds_read_b64_tr_b16 v[232:233], v246 offset:21504\n\t"
"v_mfma_f32_16x16x32_bf16 a[68:71], a[216:219], a[188:191], a[68:71]\n\t"
"ds_read_b64_tr_b16 v[224:225], v255 offset:21504\n\t"
"v_mfma_f32_16x16x32_bf16 a[76:79], a[216:219], a[192:195], a[76:79]\n\t"
"v_mfma_f32_16x16x32_bf16 a[76:79], v[248:251], a[188:191], a[76:79]\n\t"
"v_mfma_f32_16x16x32_bf16 a[76:79], a[212:215], a[184:187], a[76:79]\n\t"
"v_mfma_f32_16x16x32_bf16 a[84:87], a[212:215], a[188:191], a[84:87]\n\t"
"v_mfma_f32_16x16x32_bf16 a[84:87], v[248:251], a[192:195], a[84:87]\n\t"
"v_mfma_f32_16x16x32_bf16 a[84:87], a[216:219], a[196:199], a[84:87]\n\t"
"v_mfma_f32_16x16x32_bf16 a[92:95], a[216:219], a[200:203], a[92:95]\n\t"
"v_mfma_f32_16x16x32_bf16 a[92:95], v[248:251], a[196:199], a[92:95]\n\t"
"v_mfma_f32_16x16x32_bf16 a[92:95], a[212:215], a[192:195], a[92:95]\n\t"
"v_mfma_f32_16x16x32_bf16 a[100:103], a[212:215], a[196:199], a[100:103]\n\t"
"s_waitcnt lgkmcnt(3)\n\t"
"v_mfma_f32_16x16x32_bf16 a[100:103], v[248:251], a[200:203], a[100:103]\n\t"
"s_waitcnt lgkmcnt(2)\n\t"
"v_mfma_f32_16x16x32_bf16 a[100:103], a[216:219], a[204:207], a[100:103]\n\t"
"s_waitcnt lgkmcnt(1)\n\t"
"v_mfma_f32_4x4x4_16b_bf16 v[228:231], v[228:229], v[220:221], 0\n\t"
"s_waitcnt lgkmcnt(0)\n\t"
"v_mfma_f32_4x4x4_16b_bf16 v[236:239], v[236:237], v[220:221], 0\n\t"
"s_waitcnt vmcnt(14)\n\t"
"v_mfma_f32_4x4x4_16b_bf16 v[232:235], v[232:233], v[220:221], 0\n\t"
"s_mov_b32 s20, 651264\n\t"
"v_mfma_f32_4x4x4_16b_bf16 v[224:227], v[224:225], v[220:221], 0\n\t"
"v_mfma_f32_16x16x32_bf16 a[108:111], a[220:223], a[168:171], a[108:111]\n\t"
"ds_read_b128 a[168:171], v222 offset:2560\n\t"
"v_cvt_pk_bf16_f32 v228, v228, v229\n\t"
"v_mfma_f32_16x16x32_bf16 a[108:111], a[228:231], a[176:179], a[108:111]\n\t"
"v_cvt_pk_bf16_f32 v229, v230, v231\n\t"
"v_cvt_pk_bf16_f32 v230, v236, v237\n\t"
"v_mfma_f32_16x16x32_bf16 a[108:111], a[224:227], a[172:175], a[108:111]\n\t"
"v_cvt_pk_bf16_f32 v231, v238, v239\n\t"
"v_cvt_pk_bf16_f32 v232, v232, v233\n\t"
"v_mfma_f32_16x16x32_bf16 a[116:119], a[220:223], a[172:175], a[116:119]\n\t"
"ds_read_b128 a[172:175], v223 offset:8704\n\t"
"v_cvt_pk_bf16_f32 v233, v234, v235\n\t"
"v_mfma_f32_16x16x32_bf16 a[116:119], a[224:227], a[176:179], a[116:119]\n\t"
"v_cvt_pk_bf16_f32 v234, v224, v225\n\t"
"v_cvt_pk_bf16_f32 v235, v226, v227\n\t"
"v_mfma_f32_16x16x32_bf16 a[116:119], a[228:231], a[180:183], a[116:119]\n\t"
"ds_write_b128 v245, v[228:231] offset:13824\n\t"
"v_mfma_f32_16x16x32_bf16 a[124:127], a[228:231], a[184:187], a[124:127]\n\t"
"buffer_load_dwordx4 v[96:99], off, s[80:83], s20 offset:0 sc1\n\t"
"v_mfma_f32_16x16x32_bf16 a[124:127], a[224:227], a[180:183], a[124:127]\n\t"
"v_mfma_f32_16x16x32_bf16 a[124:127], a[220:223], a[176:179], a[124:127]\n\t"
"ds_write_b128 v245, v[232:235] offset:21504\n\t"
"v_mfma_f32_16x16x32_bf16 a[132:135], a[220:223], a[180:183], a[132:135]\n\t"
"v_mfma_f32_16x16x32_bf16 a[132:135], a[224:227], a[184:187], a[132:135]\n\t"
"ds_read_b128 a[176:179], v223 offset:14848\n\t"
"v_mfma_f32_16x16x32_bf16 a[132:135], a[228:231], a[188:191], a[132:135]\n\t"
"ds_read_b64_tr_b16 v[236:237], v246 offset:27648\n\t"
"v_mfma_f32_16x16x32_bf16 a[140:143], a[228:231], a[192:195], a[140:143]\n\t"
"ds_read_b64_tr_b16 v[224:225], v255 offset:27648\n\t"
"v_mfma_f32_16x16x32_bf16 a[140:143], a[224:227], a[188:191], a[140:143]\n\t"
"ds_read_b128 a[180:183], v222 offset:22528\n\t"
"v_mfma_f32_16x16x32_bf16 a[140:143], a[220:223], a[184:187], a[140:143]\n\t"
"ds_read_b64_tr_b16 v[228:229], v246 offset:33792\n\t"
"v_mfma_f32_16x16x32_bf16 a[148:151], a[220:223], a[188:191], a[148:151]\n\t"
"ds_read_b64_tr_b16 v[232:233], v255 offset:33792\n\t"
"v_mfma_f32_16x16x32_bf16 a[148:151], a[224:227], a[192:195], a[148:151]\n\t"
"ds_read_b128 a[184:187], v223 offset:28672\n\t"
"v_mfma_f32_16x16x32_bf16 a[148:151], a[228:231], a[196:199], a[148:151]\n\t"
"ds_read_b128 a[188:191], v223 offset:34816\n\t"
"v_mfma_f32_16x16x32_bf16 a[156:159], a[228:231], a[200:203], a[156:159]\n\t"
"v_mfma_f32_16x16x32_bf16 a[156:159], a[224:227], a[196:199], a[156:159]\n\t"
"v_mfma_f32_16x16x32_bf16 a[156:159], a[220:223], a[192:195], a[156:159]\n\t"
"ds_read_b128 a[192:195], v222 offset:42496\n\t"
"v_mfma_f32_16x16x32_bf16 a[164:167], a[220:223], a[196:199], a[164:167]\n\t"
"s_waitcnt lgkmcnt(7)\n\t"
"ds_read_b128 a[196:199], v223 offset:48640\n\t"
"v_mfma_f32_16x16x32_bf16 a[164:167], a[224:227], a[200:203], a[164:167]\n\t"
"s_waitcnt lgkmcnt(7)\n\t"
"ds_read_b128 a[200:203], v223 offset:54784\n\t"
"v_mfma_f32_16x16x32_bf16 a[164:167], a[228:231], a[204:207], a[164:167]\n\t"
"s_waitcnt lgkmcnt(6)\n\t"
"ds_read_b128 a[204:207], v222 offset:62464\n\t"
"v_mfma_f32_4x4x4_16b_bf16 v[236:239], v[236:237], v[220:221], 0\n\t"
"s_waitcnt lgkmcnt(6)\n\t"
"v_mfma_f32_4x4x4_16b_bf16 v[224:227], v[224:225], v[220:221], 0\n\t"
"s_waitcnt vmcnt(12)\n\t"
"v_mfma_f32_4x4x4_16b_bf16 v[228:231], v[228:229], v[220:221], 0\n\t"
"v_mfma_f32_4x4x4_16b_bf16 v[232:235], v[232:233], v[220:221], 0\n\t"
"Lst7_%=:\n\t"
"v_mfma_f32_16x16x32_bf16 v[0:3], a[232:235], a[168:171], v[0:3]\n\t"
"v_cvt_pk_bf16_f32 v236, v236, v237\n\t"
"v_cvt_pk_bf16_f32 v237, v238, v239\n\t"
"v_mfma_f32_16x16x32_bf16 v[0:3], v[240:243], a[176:179], v[0:3]\n\t"
"v_cvt_pk_bf16_f32 v238, v224, v225\n\t"
"v_cvt_pk_bf16_f32 v239, v226, v227\n\t"
"v_mfma_f32_16x16x32_bf16 v[0:3], a[208:211], a[172:175], v[0:3]\n\t"
"ds_write_b128 v245, v[236:239] offset:27648\n\t"
"v_mfma_f32_16x16x32_bf16 v[4:7], a[232:235], a[172:175], v[4:7]\n\t"
"s_waitcnt lgkmcnt(6)\n\t"
"v_cvt_pk_bf16_f32 v228, v228, v229\n\t"
"v_cvt_pk_bf16_f32 v229, v230, v231\n\t"
"v_mfma_f32_16x16x32_bf16 v[4:7], a[208:211], a[176:179], v[4:7]\n\t"
"v_cvt_pk_bf16_f32 v230, v232, v233\n\t"
"v_cvt_pk_bf16_f32 v231, v234, v235\n\t"
"v_mfma_f32_16x16x32_bf16 v[4:7], v[240:243], a[180:183], v[4:7]\n\t"
"ds_write_b128 v245, v[228:231] offset:33792\n\t"
"v_mfma_f32_16x16x32_bf16 v[8:11], v[240:243], a[184:187], v[8:11]\n\t"
"s_waitcnt lgkmcnt(6)\n\t"
"v_mfma_f32_16x16x32_bf16 v[8:11], a[208:211], a[180:183], v[8:11]\n\t"
"v_mfma_f32_16x16x32_bf16 v[8:11], a[232:235], a[176:179], v[8:11]\n\t"
"ds_read_b64_tr_b16 v[224:225], v246 offset:41472\n\t"
"v_mfma_f32_16x16x32_bf16 v[12:15], a[232:235], a[180:183], v[12:15]\n\t"
"s_waitcnt lgkmcnt(6)\n\t"
"ds_read_b64_tr_b16 v[232:233], v255 offset:41472\n\t"
"v_mfma_f32_16x16x32_bf16 v[12:15], a[208:211], a[184:187], v[12:15]\n\t"
"ds_read_b64_tr_b16 v[236:237], v246 offset:47616\n\t"
"v_mfma_f32_16x16x32_bf16 v[12:15], v[240:243], a[188:191], v[12:15]\n\t"
"buffer_load_dwordx4 v[248:251], off, s[80:83], s20 offset:1024 sc1\n\t"
"v_mfma_f32_16x16x32_bf16 v[16:19], v[240:243], a[192:195], v[16:19]\n\t"
"s_waitcnt lgkmcnt(7)\n\t"
"ds_read_b64_tr_b16 v[228:229], v255 offset:47616\n\t"
"v_mfma_f32_16x16x32_bf16 v[16:19], a[208:211], a[188:191], v[16:19]\n\t"
"v_mfma_f32_16x16x32_bf16 v[16:19], a[232:235], a[184:187], v[16:19]\n\t"
"buffer_load_dwordx4 a[212:215], off, s[80:83], s20 offset:2048 sc1\n\t"
"v_mfma_f32_16x16x32_bf16 v[20:23], a[232:235], a[188:191], v[20:23]\n\t"
"s_waitcnt lgkmcnt(7)\n\t"
"s_mov_b32 s20, 663552\n\t"
"v_mfma_f32_16x16x32_bf16 v[20:23], a[208:211], a[192:195], v[20:23]\n\t"
"v_mfma_f32_16x16x32_bf16 v[20:23], v[240:243], a[196:199], v[20:23]\n\t"
"v_mfma_f32_16x16x32_bf16 v[24:27], v[240:243], a[200:203], v[24:27]\n\t"
"buffer_load_dwordx4 a[216:219], off, s[80:83], s20 offset:0 sc1\n\t"
"v_mfma_f32_16x16x32_bf16 v[24:27], a[208:211], a[196:199], v[24:27]\n\t"
"s_waitcnt lgkmcnt(6)\n\t"
"v_mfma_f32_16x16x32_bf16 v[24:27], a[232:235], a[192:195], v[24:27]\n\t"
"v_mfma_f32_16x16x32_bf16 v[28:31], a[232:235], a[196:199], v[28:31]\n\t"
"s_waitcnt lgkmcnt(3)\n\t"
"v_mfma_f32_16x16x32_bf16 v[28:31], a[208:211], a[200:203], v[28:31]\n\t"
"buffer_load_dwordx4 a[208:211], off, s[80:83], s20 offset:1024 sc1\n\t"
"v_mfma_f32_16x16x32_bf16 v[28:31], v[240:243], a[204:207], v[28:31]\n\t"
"s_waitcnt lgkmcnt(1)\n\t"
"v_mfma_f32_4x4x4_16b_bf16 v[224:227], v[224:225], v[220:221], 0\n\t"
"s_waitcnt lgkmcnt(0)\n\t"
"v_mfma_f32_4x4x4_16b_bf16 v[232:235], v[232:233], v[220:221], 0\n\t"
"s_waitcnt vmcnt(13)\n\t"
"v_mfma_f32_4x4x4_16b_bf16 v[236:239], v[236:237], v[220:221], 0\n\t"
"v_mfma_f32_4x4x4_16b_bf16 v[228:231], v[228:229], v[220:221], 0\n\t"
"v_mfma_f32_16x16x32_bf16 v[32:35], a[236:239], a[168:171], v[32:35]\n\t"
"buffer_load_dwordx4 v[240:243], off, s[80:83], s20 offset:2048 sc1\n\t"
"v_mfma_f32_16x16x32_bf16 v[32:35], a[244:247], a[176:179], v[32:35]\n\t"
"s_mov_b32 s20, 675840\n\t"
"v_cvt_pk_bf16_f32 v224, v224, v225\n\t"
"v_cvt_pk_bf16_f32 v225, v226, v227\n\t"
"v_mfma_f32_16x16x32_bf16 v[32:35], a[240:243], a[172:175], v[32:35]\n\t"
"v_cvt_pk_bf16_f32 v226, v232, v233\n\t"
"v_cvt_pk_bf16_f32 v227, v234, v235\n\t"
"v_mfma_f32_16x16x32_bf16 v[36:39], a[236:239], a[172:175], v[36:39]\n\t"
"v_cvt_pk_bf16_f32 v236, v236, v237\n\t"
"v_cvt_pk_bf16_f32 v237, v238, v239\n\t"
"v_mfma_f32_16x16x32_bf16 v[36:39], a[240:243], a[176:179], v[36:39]\n\t"
"v_cvt_pk_bf16_f32 v238, v228, v229\n\t"
"v_cvt_pk_bf16_f32 v239, v230, v231\n\t"
"v_mfma_f32_16x16x32_bf16 v[36:39], a[244:247], a[180:183], v[36:39]\n\t"
"buffer_load_dwordx4 a[220:223], off, s[80:83], s20 offset:0 sc1\n\t"
"v_mfma_f32_16x16x32_bf16 v[40:43], a[244:247], a[184:187], v[40:43]\n\t"
"buffer_load_dwordx4 a[224:227], off, s[80:83], s20 offset:1024 sc1\n\t"
"v_mfma_f32_16x16x32_bf16 v[40:43], a[240:243], a[180:183], v[40:43]\n\t"
"v_mfma_f32_16x16x32_bf16 v[40:43], a[236:239], a[176:179], v[40:43]\n\t"
"ds_write_b128 v245, v[224:227] offset:41472\n\t"
"v_mfma_f32_16x16x32_bf16 v[44:47], a[236:239], a[180:183], v[44:47]\n\t"
"v_mfma_f32_16x16x32_bf16 v[44:47], a[240:243], a[184:187], v[44:47]\n\t"
"ds_read_b64_tr_b16 v[232:233], v246 offset:53760\n\t"
"v_mfma_f32_16x16x32_bf16 v[44:47], a[244:247], a[188:191], v[44:47]\n\t"
"ds_write_b128 v245, v[236:239] offset:47616\n\t"
"v_mfma_f32_16x16x32_bf16 v[48:51], a[244:247], a[192:195], v[48:51]\n\t"
"buffer_load_dwordx4 v[236:239], off, s[80:83], s20 offset:2048 sc1\n\t"
"v_mfma_f32_16x16x32_bf16 v[48:51], a[240:243], a[188:191], v[48:51]\n\t"
"s_mov_b32 s20, 688128\n\t"
"ds_read_b64_tr_b16 v[228:229], v255 offset:53760\n\t"
"v_mfma_f32_16x16x32_bf16 v[48:51], a[236:239], a[184:187], v[48:51]\n\t"
"buffer_load_dwordx4 a[228:231], off, s[80:83], s20 offset:0 sc1\n\t"
"v_mfma_f32_16x16x32_bf16 v[52:55], a[236:239], a[188:191], v[52:55]\n\t"
"v_mfma_f32_16x16x32_bf16 v[52:55], a[240:243], a[192:195], v[52:55]\n\t"
"v_mfma_f32_16x16x32_bf16 v[52:55], a[244:247], a[196:199], v[52:55]\n\t"
"v_mfma_f32_16x16x32_bf16 v[56:59], a[244:247], a[200:203], v[56:59]\n\t"
"buffer_load_dwordx4 a[232:235], off, s[80:83], s20 offset:1024 sc1\n\t"
"v_mfma_f32_16x16x32_bf16 v[56:59], a[240:243], a[196:199], v[56:59]\n\t"
"v_mfma_f32_16x16x32_bf16 v[56:59], a[236:239], a[192:195], v[56:59]\n\t"
"v_mfma_f32_16x16x32_bf16 v[60:63], a[236:239], a[196:199], v[60:63]\n\t"
"s_waitcnt lgkmcnt(2)\n\t"
"v_mfma_f32_16x16x32_bf16 v[60:63], a[240:243], a[200:203], v[60:63]\n\t"
"buffer_load_dwordx4 a[236:239], off, s[80:83], s20 offset:2048 sc1\n\t"
"v_mfma_f32_16x16x32_bf16 v[60:63], a[244:247], a[204:207], v[60:63]\n\t"
"s_waitcnt vmcnt(9) lgkmcnt(0)\n\t"
"s_mov_b32 s20, 921600\n\t"
"v_mfma_f32_4x4x4_16b_bf16 v[232:235], v[232:233], v[220:221], 0\n\t"
"v_mfma_f32_4x4x4_16b_bf16 v[228:231], v[228:229], v[220:221], 0\n\t"
"v_mfma_f32_16x16x32_bf16 v[64:67], v[96:99], a[168:171], v[64:67]\n\t"
"buffer_load_dwordx4 a[240:243], off, s[80:83], s20 offset:0 sc1\n\t"
"v_mfma_f32_16x16x32_bf16 v[64:67], a[212:215], a[176:179], v[64:67]\n\t"
"ds_read_b128 a[168:171], v222 offset:2816\n\t"
"v_cvt_pk_bf16_f32 v232, v232, v233\n\t"
"v_mfma_f32_16x16x32_bf16 v[64:67], v[248:251], a[172:175], v[64:67]\n\t"
"v_cvt_pk_bf16_f32 v233, v234, v235\n\t"
"v_cvt_pk_bf16_f32 v234, v228, v229\n\t"
"v_mfma_f32_16x16x32_bf16 v[68:71], v[96:99], a[172:175], v[68:71]\n\t"
"ds_read_b128 a[172:175], v223 offset:8960\n\t"
"v_cvt_pk_bf16_f32 v235, v230, v231\n\t"
"v_mfma_f32_16x16x32_bf16 v[68:71], v[248:251], a[176:179], v[68:71]\n\t"
"v_mfma_f32_16x16x32_bf16 v[68:71], a[212:215], a[180:183], v[68:71]\n\t"
"buffer_load_dwordx4 a[244:247], off, s[80:83], s20 offset:1024 sc1\n\t"
"v_mfma_f32_16x16x32_bf16 v[72:75], a[212:215], a[184:187], v[72:75]\n\t"
"buffer_load_dwordx4 a[248:251], off, s[80:83], s20 offset:2048 sc1\n\t"
"v_mfma_f32_16x16x32_bf16 v[72:75], v[248:251], a[180:183], v[72:75]\n\t"
"s_mov_b32 s20, 933888\n\t"
"v_mfma_f32_16x16x32_bf16 v[72:75], v[96:99], a[176:179], v[72:75]\n\t"
"ds_write_b128 v245, v[232:235] offset:53760\n\t"
"v_mfma_f32_16x16x32_bf16 v[76:79], v[96:99], a[180:183], v[76:79]\n\t"
"buffer_load_dwordx4 a[252:255], off, s[80:83], s20 offset:0 sc1\n\t"
"v_mfma_f32_16x16x32_bf16 v[76:79], v[248:251], a[184:187], v[76:79]\n\t"
"ds_read_b128 a[176:179], v223 offset:15104\n\t"
"v_mfma_f32_16x16x32_bf16 v[76:79], a[212:215], a[188:191], v[76:79]\n\t"
"ds_read_b128 a[180:183], v222 offset:22784\n\t"
"v_mfma_f32_16x16x32_bf16 v[80:83], a[212:215], a[192:195], v[80:83]\n\t"
"v_mfma_f32_16x16x32_bf16 v[80:83], v[248:251], a[188:191], v[80:83]\n\t"
"v_mfma_f32_16x16x32_bf16 v[80:83], v[96:99], a[184:187], v[80:83]\n\t"
"ds_read_b128 a[184:187], v223 offset:28928\n\t"
"v_mfma_f32_16x16x32_bf16 v[84:87], v[96:99], a[188:191], v[84:87]\n\t"
"s_waitcnt vmcnt(10) lgkmcnt(2)\n\t"
"ds_read_b128 a[188:191], v223 offset:35072\n\t"
"v_mfma_f32_16x16x32_bf16 v[84:87], v[248:251], a[192:195], v[84:87]\n\t"
"v_mfma_f32_16x16x32_bf16 v[84:87], a[212:215], a[196:199], v[84:87]\n\t"
"s_waitcnt lgkmcnt(2)\n\t"
"v_mfma_f32_16x16x32_bf16 v[88:91], a[212:215], a[200:203], v[88:91]\n\t"
"Lpg6_%=:\n\t"
"s_cbranch_vccnz Lpg10_%=\n\t"
"v_mfma_f32_16x16x32_bf16 v[88:91], v[248:251], a[196:199], v[88:91]\n\t"
"v_mfma_f32_16x16x32_bf16 v[88:91], v[96:99], a[192:195], v[88:91]\n\t"
"ds_read_b128 a[192:195], v222 offset:42752\n\t"
"v_mfma_f32_16x16x32_bf16 v[92:95], v[96:99], a[196:199], v[92:95]\n\t"
"buffer_load_dwordx4 v[96:99], off, s[80:83], s20 offset:1024 sc1\n\t"
"v_mfma_f32_16x16x32_bf16 v[92:95], v[248:251], a[200:203], v[92:95]\n\t"
"ds_read_b128 a[196:199], v223 offset:48896\n\t"
"v_mfma_f32_16x16x32_bf16 v[92:95], a[212:215], a[204:207], v[92:95]\n\t"
"ds_read_b128 a[200:203], v223 offset:55040\n\t"
"v_mfma_f32_16x16x32_bf16 v[100:103], a[216:219], a[168:171], v[100:103]\n\t"
"ds_read_b128 a[204:207], v222 offset:62720\n\t"
"v_mfma_f32_16x16x32_bf16 v[100:103], v[240:243], a[176:179], v[100:103]\n\t"
"buffer_load_dwordx4 v[248:251], off, s[80:83], s20 offset:2048 sc1\n\t"
"v_mfma_f32_16x16x32_bf16 v[100:103], a[208:211], a[172:175], v[100:103]\n\t"
"s_mov_b32 s20, 946176\n\t"
"s_add_u32 m0, s24, 1792\n\t"
"s_add_u32 s28, s21, 3072\n\t"
"v_mfma_f32_16x16x32_bf16 v[104:107], a[216:219], a[172:175], v[104:107]\n\t"
"buffer_load_dwordx4 a[212:215], off, s[80:83], s20 offset:0 sc1\n\t"
"v_mfma_f32_16x16x32_bf16 v[104:107], a[208:211], a[176:179], v[104:107]\n\t"
"s_waitcnt lgkmcnt(5)\n\t"
"v_mfma_f32_16x16x32_bf16 v[104:107], v[240:243], a[180:183], v[104:107]\n\t"
"s_waitcnt lgkmcnt(0)\n\t"
"s_barrier\n\t"
"v_mfma_f32_16x16x32_bf16 v[108:111], v[240:243], a[184:187], v[108:111]\n\t"
"v_mfma_f32_16x16x32_bf16 v[108:111], a[208:211], a[180:183], v[108:111]\n\t"
"buffer_load_dwordx4 v247, s[12:15], s28 offen offset:256 nt lds\n\t"
"v_mfma_f32_16x16x32_bf16 v[108:111], a[216:219], a[176:179], v[108:111]\n\t"
"s_add_u32 m0, s24, 7936\n\t"
"s_add_u32 s28, s21, 199680\n\t"
"v_mfma_f32_16x16x32_bf16 v[112:115], a[216:219], a[180:183], v[112:115]\n\t"
"buffer_load_dwordx4 v247, s[12:15], s28 offen offset:256 nt lds\n\t"
"v_mfma_f32_16x16x32_bf16 v[112:115], a[208:211], a[184:187], v[112:115]\n\t"
"s_add_u32 m0, s24, 14080\n\t"
"s_add_u32 s28, s21, 396288\n\t"
"v_mfma_f32_16x16x32_bf16 v[112:115], v[240:243], a[188:191], v[112:115]\n\t"
"buffer_load_dwordx4 v247, s[12:15], s28 offen offset:256 nt lds\n\t"
"s_add_u32 m0, s24, 21760\n\t"
"s_add_u32 s28, s21, 592896\n\t"
"v_mfma_f32_16x16x32_bf16 v[116:119], v[240:243], a[192:195], v[116:119]\n\t"
"buffer_load_dwordx4 v247, s[12:15], s28 offen offset:256 nt lds\n\t"
"v_mfma_f32_16x16x32_bf16 v[116:119], a[208:211], a[188:191], v[116:119]\n\t"
"s_add_u32 m0, s24, 27904\n\t"
"s_add_u32 s28, s21, 789504\n\t"
"v_mfma_f32_16x16x32_bf16 v[116:119], a[216:219], a[184:187], v[116:119]\n\t"
"buffer_load_dwordx4 v247, s[12:15], s28 offen offset:256 nt lds\n\t"
"v_mfma_f32_16x16x32_bf16 v[120:123], a[216:219], a[188:191], v[120:123]\n\t"
"s_add_u32 m0, s24, 34048\n\t"
"s_add_u32 s28, s21, 986112\n\t"
"v_mfma_f32_16x16x32_bf16 v[120:123], a[208:211], a[192:195], v[120:123]\n\t"
"buffer_load_dwordx4 v247, s[12:15], s28 offen offset:256 nt lds\n\t"
"v_mfma_f32_16x16x32_bf16 v[120:123], v[240:243], a[196:199], v[120:123]\n\t"
"v_mfma_f32_16x16x32_bf16 v[124:127], v[240:243], a[200:203], v[124:127]\n\t"
"v_mfma_f32_16x16x32_bf16 v[124:127], a[208:211], a[196:199], v[124:127]\n\t"
"v_mfma_f32_16x16x32_bf16 v[124:127], a[216:219], a[192:195], v[124:127]\n\t"
"v_mfma_f32_16x16x32_bf16 v[128:131], a[216:219], a[196:199], v[128:131]\n\t"
"s_waitcnt vmcnt(16)\n\t"
"v_mfma_f32_16x16x32_bf16 v[128:131], a[208:211], a[200:203], v[128:131]\n\t"
"v_mfma_f32_16x16x32_bf16 v[128:131], v[240:243], a[204:207], v[128:131]\n\t"
"v_mfma_f32_16x16x32_bf16 v[132:135], a[220:223], a[168:171], v[132:135]\n\t"
"v_mfma_f32_16x16x32_bf16 v[132:135], v[236:239], a[176:179], v[132:135]\n\t"
"v_mfma_f32_16x16x32_bf16 v[132:135], a[224:227], a[172:175], v[132:135]\n\t"
"v_mfma_f32_16x16x32_bf16 v[136:139], a[220:223], a[172:175], v[136:139]\n\t"
"v_mfma_f32_16x16x32_bf16 v[136:139], a[224:227], a[176:179], v[136:139]\n\t"
"v_mfma_f32_16x16x32_bf16 v[136:139], v[236:239], a[180:183], v[136:139]\n\t"
"v_mfma_f32_16x16x32_bf16 v[140:143], v[236:239], a[184:187], v[140:143]\n\t"
"v_mfma_f32_16x16x32_bf16 v[140:143], a[224:227], a[180:183], v[140:143]\n\t"
"v_mfma_f32_16x16x32_bf16 v[140:143], a[220:223], a[176:179], v[140:143]\n\t"
"v_mfma_f32_16x16x32_bf16 v[144:147], a[220:223], a[180:183], v[144:147]\n\t"
"v_mfma_f32_16x16x32_bf16 v[144:147], a[224:227], a[184:187], v[144:147]\n\t"
"v_mfma_f32_16x16x32_bf16 v[144:147], v[236:239], a[188:191], v[144:147]\n\t"
"v_mfma_f32_16x16x32_bf16 v[148:151], v[236:239], a[192:195], v[148:151]\n\t"
"v_mfma_f32_16x16x32_bf16 v[148:151], a[224:227], a[188:191], v[148:151]\n\t"
"v_mfma_f32_16x16x32_bf16 v[148:151], a[220:223], a[184:187], v[148:151]\n\t"
"v_mfma_f32_16x16x32_bf16 v[152:155], a[220:223], a[188:191], v[152:155]\n\t"
"v_mfma_f32_16x16x32_bf16 v[152:155], a[224:227], a[192:195], v[152:155]\n\t"
"v_mfma_f32_16x16x32_bf16 v[152:155], v[236:239], a[196:199], v[152:155]\n\t"
"v_mfma_f32_16x16x32_bf16 v[156:159], v[236:239], a[200:203], v[156:159]\n\t"
"v_mfma_f32_16x16x32_bf16 v[156:159], a[224:227], a[196:199], v[156:159]\n\t"
"v_mfma_f32_16x16x32_bf16 v[156:159], a[220:223], a[192:195], v[156:159]\n\t"
"v_mfma_f32_16x16x32_bf16 v[160:163], a[220:223], a[196:199], v[160:163]\n\t"
"s_waitcnt vmcnt(13)\n\t"
"v_mfma_f32_16x16x32_bf16 v[160:163], a[224:227], a[200:203], v[160:163]\n\t"
"v_mfma_f32_16x16x32_bf16 v[160:163], v[236:239], a[204:207], v[160:163]\n\t"
"v_mfma_f32_16x16x32_bf16 v[164:167], a[228:231], a[168:171], v[164:167]\n\t"
"ds_read_b128 a[168:171], v222 offset:3072\n\t"
"v_mfma_f32_16x16x32_bf16 v[164:167], a[236:239], a[176:179], v[164:167]\n\t"
"v_mfma_f32_16x16x32_bf16 v[164:167], a[232:235], a[172:175], v[164:167]\n\t"
"buffer_load_dwordx4 v[240:243], off, s[80:83], s20 offset:1024 sc1\n\t"
"v_mfma_f32_16x16x32_bf16 v[168:171], a[228:231], a[172:175], v[168:171]\n\t"
"ds_read_b128 a[172:175], v223 offset:9216\n\t"
"v_mfma_f32_16x16x32_bf16 v[168:171], a[232:235], a[176:179], v[168:171]\n\t"
"v_mfma_f32_16x16x32_bf16 v[168:171], a[236:239], a[180:183], v[168:171]\n\t"
"v_mfma_f32_16x16x32_bf16 v[172:175], a[236:239], a[184:187], v[172:175]\n\t"
"v_mfma_f32_16x16x32_bf16 v[172:175], a[232:235], a[180:183], v[172:175]\n\t"
"v_mfma_f32_16x16x32_bf16 v[172:175], a[228:231], a[176:179], v[172:175]\n\t"
"ds_read_b128 a[176:179], v223 offset:15360\n\t"
"v_mfma_f32_16x16x32_bf16 v[176:179], a[228:231], a[180:183], v[176:179]\n\t"
"ds_read_b128 a[180:183], v222 offset:23040\n\t"
"v_mfma_f32_16x16x32_bf16 v[176:179], a[232:235], a[184:187], v[176:179]\n\t"
"v_mfma_f32_16x16x32_bf16 v[176:179], a[236:239], a[188:191], v[176:179]\n\t"
"v_mfma_f32_16x16x32_bf16 v[180:183], a[236:239], a[192:195], v[180:183]\n\t"
"v_mfma_f32_16x16x32_bf16 v[180:183], a[232:235], a[188:191], v[180:183]\n\t"
"v_mfma_f32_16x16x32_bf16 v[180:183], a[228:231], a[184:187], v[180:183]\n\t"
"ds_read_b128 a[184:187], v223 offset:29184\n\t"
"v_mfma_f32_16x16x32_bf16 v[184:187], a[228:231], a[188:191], v[184:187]\n\t"
"ds_read_b128 a[188:191], v223 offset:35328\n\t"
"v_mfma_f32_16x16x32_bf16 v[184:187], a[232:235], a[192:195], v[184:187]\n\t"
"v_mfma_f32_16x16x32_bf16 v[184:187], a[236:239], a[196:199], v[184:187]\n\t"
"v_mfma_f32_16x16x32_bf16 v[188:191], a[236:239], a[200:203], v[188:191]\n\t"
"v_mfma_f32_16x16x32_bf16 v[188:191], a[232:235], a[196:199], v[188:191]\n\t"
"v_mfma_f32_16x16x32_bf16 v[188:191], a[228:231], a[192:195], v[188:191]\n\t"
"ds_read_b128 a[192:195], v222 offset:43008\n\t"
"v_mfma_f32_16x16x32_bf16 v[192:195], a[228:231], a[196:199], v[192:195]\n\t"
"s_waitcnt vmcnt(11) lgkmcnt(4)\n\t"
"ds_read_b128 a[196:199], v223 offset:49152\n\t"
"v_mfma_f32_16x16x32_bf16 v[192:195], a[232:235], a[200:203], v[192:195]\n\t"
"ds_read_b128 a[200:203], v223 offset:55296\n\t"
"v_mfma_f32_16x16x32_bf16 v[192:195], a[236:239], a[204:207], v[192:195]\n\t"
"ds_read_b128 a[204:207], v222 offset:62976\n\t"
"Lst8_%=:\n\t"
"v_mfma_f32_16x16x32_bf16 v[0:3], a[240:243], a[168:171], v[0:3]\n\t"
"s_mov_b64 vcc, -1\n\t"
"s_bitcmp1_b32 s73, 1\n\t"
"s_cbranch_scc1 Lvh1_%=\n\t"
"s_bitcmp1_b32 s73, 0\n\t"
"s_cbranch_scc1 Lpg14_%=\n\t"
"s_branch Lpg13_%=\n\t"
"Lvh1_%=:\n\t"
"s_bitcmp1_b32 s73, 0\n\t"
"s_cbranch_scc1 Lpg16_%=\n\t"
"s_branch Lpg15_%=\n\t"
"Lvr1_%=:\n\t"
"s_mov_b64 vcc, 0\n\t"
"s_waitcnt lgkmcnt(6)\n\t"
"v_mfma_f32_16x16x32_bf16 v[0:3], a[248:251], a[176:179], v[0:3]\n\t"
"v_mfma_f32_16x16x32_bf16 v[0:3], a[244:247], a[172:175], v[0:3]\n\t"
"v_mfma_f32_16x16x32_bf16 v[4:7], a[240:243], a[172:175], v[4:7]\n\t"
"s_waitcnt lgkmcnt(5)\n\t"
"v_mfma_f32_16x16x32_bf16 v[4:7], a[244:247], a[176:179], v[4:7]\n\t"
"v_mfma_f32_16x16x32_bf16 v[4:7], a[248:251], a[180:183], v[4:7]\n\t"
"v_mfma_f32_16x16x32_bf16 v[8:11], a[248:251], a[184:187], v[8:11]\n\t"
"s_waitcnt lgkmcnt(4)\n\t"
"v_mfma_f32_16x16x32_bf16 v[8:11], a[244:247], a[180:183], v[8:11]\n\t"
"v_mfma_f32_16x16x32_bf16 v[8:11], a[240:243], a[176:179], v[8:11]\n\t"
"buffer_load_dwordx4 a[208:211], off, s[80:83], s20 offset:2048 sc1\n\t"
"v_mfma_f32_16x16x32_bf16 v[12:15], a[240:243], a[180:183], v[12:15]\n\t"
"s_waitcnt lgkmcnt(3)\n\t"
"s_mov_b32 s20, 958464\n\t"
"v_mfma_f32_16x16x32_bf16 v[12:15], a[244:247], a[184:187], v[12:15]\n\t"
"v_mfma_f32_16x16x32_bf16 v[12:15], a[248:251], a[188:191], v[12:15]\n\t"
"v_mfma_f32_16x16x32_bf16 v[16:19], a[248:251], a[192:195], v[16:19]\n\t"
"buffer_load_dwordx4 v[236:239], off, s[80:83], s20 offset:0 sc1\n\t"
"v_mfma_f32_16x16x32_bf16 v[16:19], a[244:247], a[188:191], v[16:19]\n\t"
"s_waitcnt lgkmcnt(2)\n\t"
"v_mfma_f32_16x16x32_bf16 v[16:19], a[240:243], a[184:187], v[16:19]\n\t"
"v_mfma_f32_16x16x32_bf16 v[20:23], a[240:243], a[188:191], v[20:23]\n\t"
"s_waitcnt lgkmcnt(1)\n\t"
"v_mfma_f32_16x16x32_bf16 v[20:23], a[244:247], a[192:195], v[20:23]\n\t"
"buffer_load_dwordx4 a[216:219], off, s[80:83], s20 offset:1024 sc1\n\t"
"v_mfma_f32_16x16x32_bf16 v[20:23], a[248:251], a[196:199], v[20:23]\n\t"
"v_mfma_f32_16x16x32_bf16 v[24:27], a[248:251], a[200:203], v[24:27]\n\t"
"s_waitcnt lgkmcnt(0)\n\t"
"v_mfma_f32_16x16x32_bf16 v[24:27], a[244:247], a[196:199], v[24:27]\n\t"
"v_mfma_f32_16x16x32_bf16 v[24:27], a[240:243], a[192:195], v[24:27]\n\t"
"buffer_load_dwordx4 a[220:223], off, s[80:83], s20 offset:2048 sc1\n\t"
"v_mfma_f32_16x16x32_bf16 v[28:31], a[240:243], a[196:199], v[28:31]\n\t"
"s_waitcnt vmcnt(12)\n\t"
"s_mov_b32 s20, 970752\n\t"
"v_mfma_f32_16x16x32_bf16 v[28:31], a[244:247], a[200:203], v[28:31]\n\t"
"v_mfma_f32_16x16x32_bf16 v[28:31], a[248:251], a[204:207], v[28:31]\n\t"
"v_mfma_f32_16x16x32_bf16 v[32:35], a[252:255], a[168:171], v[32:35]\n\t"
"buffer_load_dwordx4 a[224:227], off, s[80:83], s20 offset:0 sc1\n\t"
"v_mfma_f32_16x16x32_bf16 v[32:35], v[248:251], a[176:179], v[32:35]\n\t"
"v_mfma_f32_16x16x32_bf16 v[32:35], v[96:99], a[172:175], v[32:35]\n\t"
"v_mfma_f32_16x16x32_bf16 v[36:39], a[252:255], a[172:175], v[36:39]\n\t"
"v_mfma_f32_16x16x32_bf16 v[36:39], v[96:99], a[176:179], v[36:39]\n\t"
"buffer_load_dwordx4 a[228:231], off, s[80:83], s20 offset:1024 sc1\n\t"
"v_mfma_f32_16x16x32_bf16 v[36:39], v[248:251], a[180:183], v[36:39]\n\t"
"v_mfma_f32_16x16x32_bf16 v[40:43], v[248:251], a[184:187], v[40:43]\n\t"
"v_mfma_f32_16x16x32_bf16 v[40:43], v[96:99], a[180:183], v[40:43]\n\t"
"v_mfma_f32_16x16x32_bf16 v[40:43], a[252:255], a[176:179], v[40:43]\n\t"
"buffer_load_dwordx4 a[232:235], off, s[80:83], s20 offset:2048 sc1\n\t"
"v_mfma_f32_16x16x32_bf16 v[44:47], a[252:255], a[180:183], v[44:47]\n\t"
"s_mov_b32 s20, 983040\n\t"
"v_mfma_f32_16x16x32_bf16 v[44:47], v[96:99], a[184:187], v[44:47]\n\t"
"v_mfma_f32_16x16x32_bf16 v[44:47], v[248:251], a[188:191], v[44:47]\n\t"
"v_mfma_f32_16x16x32_bf16 v[48:51], v[248:251], a[192:195], v[48:51]\n\t"
"buffer_load_dwordx4 a[236:239], off, s[80:83], s20 offset:0 sc1\n\t"
"v_mfma_f32_16x16x32_bf16 v[48:51], v[96:99], a[188:191], v[48:51]\n\t"
"v_mfma_f32_16x16x32_bf16 v[48:51], a[252:255], a[184:187], v[48:51]\n\t"
"v_mfma_f32_16x16x32_bf16 v[52:55], a[252:255], a[188:191], v[52:55]\n\t"
"v_mfma_f32_16x16x32_bf16 v[52:55], v[96:99], a[192:195], v[52:55]\n\t"
"buffer_load_dwordx4 a[240:243], off, s[80:83], s20 offset:1024 sc1\n\t"
"v_mfma_f32_16x16x32_bf16 v[52:55], v[248:251], a[196:199], v[52:55]\n\t"
"v_mfma_f32_16x16x32_bf16 v[56:59], v[248:251], a[200:203], v[56:59]\n\t"
"v_mfma_f32_16x16x32_bf16 v[56:59], v[96:99], a[196:199], v[56:59]\n\t"
"v_mfma_f32_16x16x32_bf16 v[56:59], a[252:255], a[192:195], v[56:59]\n\t"
"buffer_load_dwordx4 a[244:247], off, s[80:83], s20 offset:2048 sc1\n\t"
"v_mfma_f32_16x16x32_bf16 v[60:63], a[252:255], a[196:199], v[60:63]\n\t"
"s_waitcnt vmcnt(9)\n\t"
"s_mov_b32 s20, 995328\n\t"
"v_mfma_f32_16x16x32_bf16 v[60:63], v[96:99], a[200:203], v[60:63]\n\t"
"v_mfma_f32_16x16x32_bf16 v[60:63], v[248:251], a[204:207], v[60:63]\n\t"
"v_mfma_f32_16x16x32_bf16 v[64:67], a[212:215], a[168:171], v[64:67]\n\t"
"buffer_load_dwordx4 v[96:99], off, s[80:83], s20 offset:0 sc1\n\t"
"v_mfma_f32_16x16x32_bf16 v[64:67], a[208:211], a[176:179], v[64:67]\n\t"
"ds_read_b128 a[168:171], v222 offset:3328\n\t"
"v_mfma_f32_16x16x32_bf16 v[64:67], v[240:243], a[172:175], v[64:67]\n\t"
"v_mfma_f32_16x16x32_bf16 v[68:71], a[212:215], a[172:175], v[68:71]\n\t"
"buffer_load_dwordx4 v[248:251], off, s[80:83], s20 offset:1024 sc1\n\t"
"v_mfma_f32_16x16x32_bf16 v[68:71], v[240:243], a[176:179], v[68:71]\n\t"
"ds_read_b128 a[172:175], v223 offset:9472\n\t"
"v_mfma_f32_16x16x32_bf16 v[68:71], a[208:211], a[180:183], v[68:71]\n\t"
"v_mfma_f32_16x16x32_bf16 v[72:75], a[208:211], a[184:187], v[72:75]\n\t"
"buffer_load_dwordx4 a[248:251], off, s[80:83], s20 offset:2048 sc1\n\t"
"v_mfma_f32_16x16x32_bf16 v[72:75], v[240:243], a[180:183], v[72:75]\n\t"
"s_mov_b32 s20, 1007616\n\t"
"v_mfma_f32_16x16x32_bf16 v[72:75], a[212:215], a[176:179], v[72:75]\n\t"
"ds_read_b128 a[176:179], v223 offset:15616\n\t"
"v_mfma_f32_16x16x32_bf16 v[76:79], a[212:215], a[180:183], v[76:79]\n\t"
"buffer_load_dwordx4 a[252:255], off, s[80:83], s20 offset:0 sc1\n\t"
"v_mfma_f32_16x16x32_bf16 v[76:79], v[240:243], a[184:187], v[76:79]\n\t"
"ds_read_b128 a[180:183], v222 offset:23296\n\t"
"v_mfma_f32_16x16x32_bf16 v[76:79], a[208:211], a[188:191], v[76:79]\n\t"
"v_mfma_f32_16x16x32_bf16 v[80:83], a[208:211], a[192:195], v[80:83]\n\t"
"v_mfma_f32_16x16x32_bf16 v[80:83], v[240:243], a[188:191], v[80:83]\n\t"
"v_mfma_f32_16x16x32_bf16 v[80:83], a[212:215], a[184:187], v[80:83]\n\t"
"ds_read_b128 a[184:187], v223 offset:29440\n\t"
"v_mfma_f32_16x16x32_bf16 v[84:87], a[212:215], a[188:191], v[84:87]\n\t"
"ds_read_b128 a[188:191], v223 offset:35584\n\t"
"v_mfma_f32_16x16x32_bf16 v[84:87], v[240:243], a[192:195], v[84:87]\n\t"
"v_mfma_f32_16x16x32_bf16 v[84:87], a[208:211], a[196:199], v[84:87]\n\t"
"v_mfma_f32_16x16x32_bf16 v[88:91], a[208:211], a[200:203], v[88:91]\n\t"
"v_mfma_f32_16x16x32_bf16 v[88:91], v[240:243], a[196:199], v[88:91]\n\t"
"v_mfma_f32_16x16x32_bf16 v[88:91], a[212:215], a[192:195], v[88:91]\n\t"
"ds_read_b128 a[192:195], v222 offset:43264\n\t"
"v_mfma_f32_16x16x32_bf16 v[92:95], a[212:215], a[196:199], v[92:95]\n\t"
"buffer_load_dwordx4 a[212:215], off, s[80:83], s20 offset:1024 sc1\n\t"
"v_mfma_f32_16x16x32_bf16 v[92:95], v[240:243], a[200:203], v[92:95]\n\t"
"s_waitcnt vmcnt(11) lgkmcnt(4)\n\t"
"ds_read_b128 a[196:199], v223 offset:49408\n\t"
"v_mfma_f32_16x16x32_bf16 v[92:95], a[208:211], a[204:207], v[92:95]\n\t"
"global_load_dwordx4 v[240:243], v252, s[48:49] offset:2048\n\t"
"v_mfma_f32_16x16x32_bf16 v[100:103], v[236:239], a[168:171], v[100:103]\n\t"
"s_waitcnt lgkmcnt(4)\n\t"
"ds_read_b128 a[200:203], v223 offset:55552\n\t"
"v_mfma_f32_16x16x32_bf16 v[100:103], a[220:223], a[176:179], v[100:103]\n\t"
"ds_read_b128 a[204:207], v222 offset:63232\n\t"
"v_mfma_f32_16x16x32_bf16 v[100:103], a[216:219], a[172:175], v[100:103]\n\t"
"v_mfma_f32_16x16x32_bf16 v[104:107], v[236:239], a[172:175], v[104:107]\n\t"
"s_waitcnt lgkmcnt(5)\n\t"
"v_mfma_f32_16x16x32_bf16 v[104:107], a[216:219], a[176:179], v[104:107]\n\t"
"global_load_dwordx4 a[208:211], v252, s[46:47] offset:1024\n\t"
"v_mfma_f32_16x16x32_bf16 v[104:107], a[220:223], a[180:183], v[104:107]\n\t"
"v_mfma_f32_16x16x32_bf16 v[108:111], a[220:223], a[184:187], v[108:111]\n\t"
"s_waitcnt lgkmcnt(4)\n\t"
"v_mfma_f32_16x16x32_bf16 v[108:111], a[216:219], a[180:183], v[108:111]\n\t"
"v_mfma_f32_16x16x32_bf16 v[108:111], v[236:239], a[176:179], v[108:111]\n\t"
"v_mfma_f32_16x16x32_bf16 v[112:115], v[236:239], a[180:183], v[112:115]\n\t"
"s_waitcnt lgkmcnt(3)\n\t"
"v_mfma_f32_16x16x32_bf16 v[112:115], a[216:219], a[184:187], v[112:115]\n\t"
"v_mfma_f32_16x16x32_bf16 v[112:115], a[220:223], a[188:191], v[112:115]\n\t"
"v_mfma_f32_16x16x32_bf16 v[116:119], a[220:223], a[192:195], v[116:119]\n\t"
"s_waitcnt lgkmcnt(2)\n\t"
"v_mfma_f32_16x16x32_bf16 v[116:119], a[216:219], a[188:191], v[116:119]\n\t"
"v_mfma_f32_16x16x32_bf16 v[116:119], v[236:239], a[184:187], v[116:119]\n\t"
"v_mfma_f32_16x16x32_bf16 v[120:123], v[236:239], a[188:191], v[120:123]\n\t"
"s_waitcnt lgkmcnt(1)\n\t"
"v_mfma_f32_16x16x32_bf16 v[120:123], a[216:219], a[192:195], v[120:123]\n\t"
"v_mfma_f32_16x16x32_bf16 v[120:123], a[220:223], a[196:199], v[120:123]\n\t"
"v_mfma_f32_16x16x32_bf16 v[124:127], a[220:223], a[200:203], v[124:127]\n\t"
"s_waitcnt lgkmcnt(0)\n\t"
"v_mfma_f32_16x16x32_bf16 v[124:127], a[216:219], a[196:199], v[124:127]\n\t"
"v_mfma_f32_16x16x32_bf16 v[124:127], v[236:239], a[192:195], v[124:127]\n\t"
"v_mfma_f32_16x16x32_bf16 v[128:131], v[236:239], a[196:199], v[128:131]\n\t"
"s_waitcnt vmcnt(10)\n\t"
"v_mfma_f32_16x16x32_bf16 v[128:131], a[216:219], a[200:203], v[128:131]\n\t"
"buffer_load_dwordx4 a[216:219], off, s[80:83], s20 offset:2048 sc1\n\t"
"v_mfma_f32_16x16x32_bf16 v[128:131], a[220:223], a[204:207], v[128:131]\n\t"
"s_mov_b32 s20, 1019904\n\t"
"s_add_u32 m0, s24, 40192\n\t"
"s_add_u32 s28, s21, 1179648\n\t"
"v_mfma_f32_16x16x32_bf16 v[132:135], a[224:227], a[168:171], v[132:135]\n\t"
"v_mfma_f32_16x16x32_bf16 v[132:135], a[232:235], a[176:179], v[132:135]\n\t"
"buffer_load_dwordx4 a[220:223], off, s[80:83], s20 offset:0 sc1\n\t"
"v_mfma_f32_16x16x32_bf16 v[132:135], a[228:231], a[172:175], v[132:135]\n\t"
"v_mfma_f32_16x16x32_bf16 v[136:139], a[224:227], a[172:175], v[136:139]\n\t"
"buffer_load_dwordx4 v247, s[12:15], s28 offen offset:256 nt lds\n\t"
"v_mfma_f32_16x16x32_bf16 v[136:139], a[228:231], a[176:179], v[136:139]\n\t"
"s_add_u32 m0, s24, 46336\n\t"
"s_add_u32 s28, s21, 1376256\n\t"
"v_mfma_f32_16x16x32_bf16 v[136:139], a[232:235], a[180:183], v[136:139]\n\t"
"s_barrier\n\t"
"v_mfma_f32_16x16x32_bf16 v[140:143], a[232:235], a[184:187], v[140:143]\n\t"
"v_mfma_f32_16x16x32_bf16 v[140:143], a[228:231], a[180:183], v[140:143]\n\t"
"buffer_load_dwordx4 v247, s[12:15], s28 offen offset:256 nt lds\n\t"
"v_mfma_f32_16x16x32_bf16 v[140:143], a[224:227], a[176:179], v[140:143]\n\t"
"s_add_u32 m0, s24, 52480\n\t"
"s_add_u32 s28, s21, 1572864\n\t"
"s_waitcnt vmcnt(5)\n\t"
"ds_read_b64_tr_b16 v[224:225], v240 offset:512\n\t"
"v_mfma_f32_16x16x32_bf16 v[144:147], a[224:227], a[180:183], v[144:147]\n\t"
"buffer_load_dwordx4 v247, s[12:15], s28 offen offset:256 nt lds\n\t"
"v_mfma_f32_16x16x32_bf16 v[144:147], a[228:231], a[184:187], v[144:147]\n\t"
"s_add_u32 m0, s24, 41728\n\t"
"s_add_u32 s28, s21, 1182720\n\t"
"ds_read_b64_tr_b16 v[226:227], v240 offset:2048\n\t"
"v_mfma_f32_16x16x32_bf16 v[144:147], a[232:235], a[188:191], v[144:147]\n\t"
"buffer_load_dwordx4 v247, s[12:15], s28 offen offset:256 nt lds\n\t"
"s_add_u32 m0, s24, 47872\n\t"
"s_add_u32 s28, s21, 1379328\n\t"
"v_mfma_f32_16x16x32_bf16 v[148:151], a[232:235], a[192:195], v[148:151]\n\t"
"buffer_load_dwordx4 v247, s[12:15], s28 offen offset:256 nt lds\n\t"
"v_mfma_f32_16x16x32_bf16 v[148:151], a[228:231], a[188:191], v[148:151]\n\t"
"s_add_u32 m0, s24, 54016\n\t"
"s_add_u32 s28, s21, 1575936\n\t"
"ds_read_b64_tr_b16 v[236:237], v241 offset:512\n\t"
"v_mfma_f32_16x16x32_bf16 v[148:151], a[224:227], a[184:187], v[148:151]\n\t"
"buffer_load_dwordx4 v247, s[12:15], s28 offen offset:256 nt lds\n\t"
"v_mfma_f32_16x16x32_bf16 v[152:155], a[224:227], a[188:191], v[152:155]\n\t"
"ds_read_b64_tr_b16 v[238:239], v241 offset:2048\n\t"
"v_mfma_f32_16x16x32_bf16 v[152:155], a[228:231], a[192:195], v[152:155]\n\t"
"ds_read_b64_tr_b16 v[228:229], v240 offset:20480\n\t"
"v_mfma_f32_16x16x32_bf16 v[152:155], a[232:235], a[196:199], v[152:155]\n\t"
"ds_read_b64_tr_b16 v[230:231], v240 offset:22016\n\t"
"v_mfma_f32_16x16x32_bf16 v[156:159], a[232:235], a[200:203], v[156:159]\n\t"
"ds_read_b64_tr_b16 v[232:233], v241 offset:20480\n\t"
"v_mfma_f32_16x16x32_bf16 v[156:159], a[228:231], a[196:199], v[156:159]\n\t"
"ds_read_b64_tr_b16 v[234:235], v241 offset:22016\n\t"
"v_mfma_f32_16x16x32_bf16 v[156:159], a[224:227], a[192:195], v[156:159]\n\t"
"v_mfma_f32_16x16x32_bf16 v[160:163], a[224:227], a[196:199], v[160:163]\n\t"
"s_waitcnt vmcnt(8) lgkmcnt(6)\n\t"
"v_mfma_f32_16x16x32_bf16 v[160:163], a[228:231], a[200:203], v[160:163]\n\t"
"s_waitcnt lgkmcnt(4)\n\t"
"v_mfma_f32_16x16x32_bf16 v[160:163], a[232:235], a[204:207], v[160:163]\n\t"
"s_waitcnt lgkmcnt(2)\n\t"
"v_mfma_f32_16x16x32_bf16 v[224:227], v[224:227], a[208:211], 0\n\t"
"s_waitcnt lgkmcnt(0)\n\t"
"v_mfma_f32_16x16x32_bf16 v[236:239], v[236:239], a[208:211], 0\n\t"
"v_mfma_f32_16x16x32_bf16 v[228:231], v[228:231], a[208:211], 0\n\t"
"v_mfma_f32_16x16x32_bf16 v[232:235], v[232:235], a[208:211], 0\n\t"
"v_mfma_f32_16x16x32_bf16 v[164:167], a[236:239], a[168:171], v[164:167]\n\t"
"ds_read_b128 a[168:171], v222 offset:1536\n\t"
"v_mfma_f32_16x16x32_bf16 v[164:167], a[244:247], a[176:179], v[164:167]\n\t"
"v_cvt_pk_bf16_f32 v224, v224, v225\n\t"
"v_cvt_pk_bf16_f32 v225, v226, v227\n\t"
"v_mfma_f32_16x16x32_bf16 v[164:167], a[240:243], a[172:175], v[164:167]\n\t"
"v_cvt_pk_bf16_f32 v226, v236, v237\n\t"
"v_cvt_pk_bf16_f32 v227, v238, v239\n\t"
"v_mfma_f32_16x16x32_bf16 v[168:171], a[236:239], a[172:175], v[168:171]\n\t"
"ds_read_b128 a[172:175], v223 offset:7680\n\t"
"v_cvt_pk_bf16_f32 v228, v228, v229\n\t"
"v_mfma_f32_16x16x32_bf16 v[168:171], a[240:243], a[176:179], v[168:171]\n\t"
"v_cvt_pk_bf16_f32 v229, v230, v231\n\t"
"v_cvt_pk_bf16_f32 v230, v232, v233\n\t"
"v_mfma_f32_16x16x32_bf16 v[168:171], a[244:247], a[180:183], v[168:171]\n\t"
"ds_write_b128 v243, v[224:227] offset:3072\n\t"
"v_mfma_f32_16x16x32_bf16 v[172:175], a[244:247], a[184:187], v[172:175]\n\t"
"v_cvt_pk_bf16_f32 v231, v234, v235\n\t"
"v_add_u32 v240, 59904, v240\n\t"
"v_mfma_f32_16x16x32_bf16 v[172:175], a[240:243], a[180:183], v[172:175]\n\t"
"v_add_u32 v241, 59904, v241\n\t"
"v_mfma_f32_16x16x32_bf16 v[172:175], a[236:239], a[176:179], v[172:175]\n\t"
"ds_write_b128 v243, v[228:231] offset:23040\n\t"
"v_mfma_f32_16x16x32_bf16 v[176:179], a[236:239], a[180:183], v[176:179]\n\t"
"v_add_u32 v243, 59904, v243\n\t"
"v_mfma_f32_16x16x32_bf16 v[176:179], a[240:243], a[184:187], v[176:179]\n\t"
"ds_read_b128 a[176:179], v223 offset:13824\n\t"
"v_mfma_f32_16x16x32_bf16 v[176:179], a[244:247], a[188:191], v[176:179]\n\t"
"ds_read_b64_tr_b16 v[236:237], v240 offset:512\n\t"
"v_mfma_f32_16x16x32_bf16 v[180:183], a[244:247], a[192:195], v[180:183]\n\t"
"ds_read_b64_tr_b16 v[238:239], v240 offset:2048\n\t"
"v_mfma_f32_16x16x32_bf16 v[180:183], a[240:243], a[188:191], v[180:183]\n\t"
"buffer_load_dwordx4 a[224:227], off, s[80:83], s20 offset:1024 sc1\n\t"
"v_mfma_f32_16x16x32_bf16 v[180:183], a[236:239], a[184:187], v[180:183]\n\t"
"ds_read_b64_tr_b16 v[232:233], v241 offset:512\n\t"
"v_mfma_f32_16x16x32_bf16 v[184:187], a[236:239], a[188:191], v[184:187]\n\t"
"ds_read_b64_tr_b16 v[234:235], v241 offset:2048\n\t"
"v_mfma_f32_16x16x32_bf16 v[184:187], a[240:243], a[192:195], v[184:187]\n\t"
"ds_read_b128 a[180:183], v222 offset:21504\n\t"
"v_mfma_f32_16x16x32_bf16 v[184:187], a[244:247], a[196:199], v[184:187]\n\t"
"ds_read_b64_tr_b16 v[224:225], v240 offset:20480\n\t"
"v_mfma_f32_16x16x32_bf16 v[188:191], a[244:247], a[200:203], v[188:191]\n\t"
"ds_read_b64_tr_b16 v[226:227], v240 offset:22016\n\t"
"v_mfma_f32_16x16x32_bf16 v[188:191], a[240:243], a[196:199], v[188:191]\n\t"
"ds_read_b64_tr_b16 v[228:229], v241 offset:20480\n\t"
"v_mfma_f32_16x16x32_bf16 v[188:191], a[236:239], a[192:195], v[188:191]\n\t"
"ds_read_b64_tr_b16 v[230:231], v241 offset:22016\n\t"
"v_mfma_f32_16x16x32_bf16 v[192:195], a[236:239], a[196:199], v[192:195]\n\t"
"s_waitcnt lgkmcnt(7)\n\t"
"ds_read_b128 a[184:187], v223 offset:27648\n\t"
"v_mfma_f32_16x16x32_bf16 v[192:195], a[240:243], a[200:203], v[192:195]\n\t"
"s_waitcnt lgkmcnt(6)\n\t"
"ds_read_b128 a[188:191], v223 offset:33792\n\t"
"v_mfma_f32_16x16x32_bf16 v[192:195], a[244:247], a[204:207], v[192:195]\n\t"
"s_waitcnt lgkmcnt(4)\n\t"
"ds_read_b128 a[192:195], v222 offset:41472\n\t"
"v_mfma_f32_16x16x32_bf16 v[236:239], v[236:239], a[208:211], 0\n\t"
"ds_read_b128 a[196:199], v223 offset:47616\n\t"
"v_mfma_f32_16x16x32_bf16 v[232:235], v[232:235], a[208:211], 0\n\t"
"s_waitcnt lgkmcnt(4)\n\t"
"ds_read_b128 a[200:203], v223 offset:53760\n\t"
"v_mfma_f32_16x16x32_bf16 v[224:227], v[224:227], a[208:211], 0\n\t"
"ds_read_b128 a[204:207], v222 offset:61440\n\t"
"v_mfma_f32_16x16x32_bf16 v[228:231], v[228:231], a[208:211], 0\n\t"
"Lst9_%=:\n\t"
"v_mfma_f32_16x16x32_bf16 v[196:199], v[96:99], a[168:171], v[196:199]\n\t"
"s_mov_b64 vcc, -1\n\t"
"s_bitcmp1_b32 s73, 1\n\t"
"s_cbranch_scc1 Lvh2_%=\n\t"
"s_bitcmp1_b32 s73, 0\n\t"
"s_cbranch_scc1 Lpg18_%=\n\t"
"s_branch Lpg17_%=\n\t"
"Lvh2_%=:\n\t"
"s_bitcmp1_b32 s73, 0\n\t"
"s_cbranch_scc1 Lpg20_%=\n\t"
"s_branch Lpg19_%=\n\t"
"Lvr2_%=:\n\t"
"s_mov_b64 vcc, 0\n\t"
"v_mfma_f32_16x16x32_bf16 v[196:199], a[248:251], a[176:179], v[196:199]\n\t"
"v_cvt_pk_bf16_f32 v236, v236, v237\n\t"
"v_cvt_pk_bf16_f32 v237, v238, v239\n\t"
"v_mfma_f32_16x16x32_bf16 v[196:199], v[248:251], a[172:175], v[196:199]\n\t"
"v_cvt_pk_bf16_f32 v238, v232, v233\n\t"
"v_cvt_pk_bf16_f32 v239, v234, v235\n\t"
"v_mfma_f32_16x16x32_bf16 v[204:207], v[96:99], a[172:175], v[204:207]\n\t"
"s_waitcnt lgkmcnt(5)\n\t"
"v_cvt_pk_bf16_f32 v224, v224, v225\n\t"
"v_cvt_pk_bf16_f32 v225, v226, v227\n\t"
"v_mfma_f32_16x16x32_bf16 v[204:207], v[248:251], a[176:179], v[204:207]\n\t"
"v_cvt_pk_bf16_f32 v226, v228, v229\n\t"
"v_cvt_pk_bf16_f32 v227, v230, v231\n\t"
"v_mfma_f32_16x16x32_bf16 v[204:207], a[248:251], a[180:183], v[204:207]\n\t"
"ds_write_b128 v243, v[236:239] offset:3072\n\t"
"v_mfma_f32_16x16x32_bf16 v[212:215], a[248:251], a[184:187], v[212:215]\n\t"
"s_waitcnt lgkmcnt(5)\n\t"
"v_mfma_f32_16x16x32_bf16 v[212:215], v[248:251], a[180:183], v[212:215]\n\t"
"v_mfma_f32_16x16x32_bf16 v[212:215], v[96:99], a[176:179], v[212:215]\n\t"
"ds_write_b128 v243, v[224:227] offset:23040\n\t"
"v_mfma_f32_16x16x32_bf16 a[0:3], v[96:99], a[180:183], a[0:3]\n\t"
"s_waitcnt lgkmcnt(5)\n\t"
"v_mfma_f32_16x16x32_bf16 a[0:3], v[248:251], a[184:187], a[0:3]\n\t"
"v_mfma_f32_16x16x32_bf16 a[0:3], a[248:251], a[188:191], a[0:3]\n\t"
"s_barrier\n\t"
"v_mfma_f32_16x16x32_bf16 a[8:11], a[248:251], a[192:195], a[8:11]\n\t"
"s_waitcnt lgkmcnt(4)\n\t"
"ds_read_b64_tr_b16 v[232:233], v246 offset:512\n\t"
"v_mfma_f32_16x16x32_bf16 a[8:11], v[248:251], a[188:191], a[8:11]\n\t"
"ds_read_b64_tr_b16 v[228:229], v255 offset:512\n\t"
"v_mfma_f32_16x16x32_bf16 a[8:11], v[96:99], a[184:187], a[8:11]\n\t"
"ds_read_b64_tr_b16 v[236:237], v246 offset:6656\n\t"
"v_mfma_f32_16x16x32_bf16 a[16:19], v[96:99], a[188:191], a[16:19]\n\t"
"s_waitcnt lgkmcnt(6)\n\t"
"ds_read_b64_tr_b16 v[224:225], v255 offset:6656\n\t"
"v_mfma_f32_16x16x32_bf16 a[16:19], v[248:251], a[192:195], a[16:19]\n\t"
"buffer_load_dwordx4 a[228:231], off, s[80:83], s20 offset:2048 sc1\n\t"
"v_mfma_f32_16x16x32_bf16 a[16:19], a[248:251], a[196:199], a[16:19]\n\t"
"s_mov_b32 s20, 1032192\n\t"
"v_mfma_f32_16x16x32_bf16 a[24:27], a[248:251], a[200:203], a[24:27]\n\t"
"s_waitcnt lgkmcnt(6)\n\t"
"v_mfma_f32_16x16x32_bf16 a[24:27], v[248:251], a[196:199], a[24:27]\n\t"
"v_mfma_f32_16x16x32_bf16 a[24:27], v[96:99], a[192:195], a[24:27]\n\t"
"buffer_load_dwordx4 a[232:235], off, s[80:83], s20 offset:0 sc1\n\t"
"v_mfma_f32_16x16x32_bf16 a[32:35], v[96:99], a[196:199], a[32:35]\n\t"
"s_waitcnt lgkmcnt(3)\n\t"
"v_mfma_f32_16x16x32_bf16 a[32:35], v[248:251], a[200:203], a[32:35]\n\t"
"s_waitcnt lgkmcnt(2)\n\t"
"v_mfma_f32_16x16x32_bf16 a[32:35], a[248:251], a[204:207], a[32:35]\n\t"
"s_waitcnt lgkmcnt(1)\n\t"
"v_mfma_f32_4x4x4_16b_bf16 v[232:235], v[232:233], v[220:221], 0\n\t"
"s_waitcnt lgkmcnt(0)\n\t"
"v_mfma_f32_4x4x4_16b_bf16 v[228:231], v[228:229], v[220:221], 0\n\t"
"s_waitcnt vmcnt(10)\n\t"
"v_mfma_f32_4x4x4_16b_bf16 v[236:239], v[236:237], v[220:221], 0\n\t"
"v_mfma_f32_4x4x4_16b_bf16 v[224:227], v[224:225], v[220:221], 0\n\t"
"buffer_load_dwordx4 v[96:99], off, s[80:83], s20 offset:1024 sc1\n\t"
"v_mfma_f32_16x16x32_bf16 a[40:43], a[252:255], a[168:171], a[40:43]\n\t"
"buffer_load_dwordx4 v[248:251], off, s[80:83], s20 offset:2048 sc1\n\t"
"v_mfma_f32_16x16x32_bf16 a[40:43], a[216:219], a[176:179], a[40:43]\n\t"
"s_mov_b32 s20, 1044480\n\t"
"v_cvt_pk_bf16_f32 v232, v232, v233\n\t"
"v_cvt_pk_bf16_f32 v233, v234, v235\n\t"
"v_mfma_f32_16x16x32_bf16 a[40:43], a[212:215], a[172:175], a[40:43]\n\t"
"v_cvt_pk_bf16_f32 v234, v228, v229\n\t"
"v_cvt_pk_bf16_f32 v235, v230, v231\n\t"
"v_mfma_f32_16x16x32_bf16 a[48:51], a[252:255], a[172:175], a[48:51]\n\t"
"ds_read_b64_tr_b16 v[228:229], v246 offset:12800\n\t"
"v_cvt_pk_bf16_f32 v236, v236, v237\n\t"
"v_mfma_f32_16x16x32_bf16 a[48:51], a[212:215], a[176:179], a[48:51]\n\t"
"v_cvt_pk_bf16_f32 v237, v238, v239\n\t"
"v_cvt_pk_bf16_f32 v238, v224, v225\n\t"
"v_mfma_f32_16x16x32_bf16 a[48:51], a[216:219], a[180:183], a[48:51]\n\t"
"ds_write_b128 v245, v[232:235] offset:512\n\t"
"v_cvt_pk_bf16_f32 v239, v226, v227\n\t"
"v_mfma_f32_16x16x32_bf16 a[56:59], a[216:219], a[184:187], a[56:59]\n\t"
"v_mfma_f32_16x16x32_bf16 a[56:59], a[212:215], a[180:183], a[56:59]\n\t"
"buffer_load_dwordx4 a[236:239], off, s[80:83], s20 offset:0 sc1\n\t"
"v_mfma_f32_16x16x32_bf16 a[56:59], a[252:255], a[176:179], a[56:59]\n\t"
"ds_read_b64_tr_b16 v[224:225], v255 offset:12800\n\t"
"v_mfma_f32_16x16x32_bf16 a[64:67], a[252:255], a[180:183], a[64:67]\n\t"
"ds_read_b64_tr_b16 v[232:233], v246 offset:20480\n\t"
"v_mfma_f32_16x16x32_bf16 a[64:67], a[212:215], a[184:187], a[64:67]\n\t"
"buffer_load_dwordx4 a[240:243], off, s[80:83], s20 offset:1024 sc1\n\t"
"v_mfma_f32_16x16x32_bf16 a[64:67], a[216:219], a[188:191], a[64:67]\n\t"
"ds_write_b128 v245, v[236:239] offset:6656\n\t"
"v_mfma_f32_16x16x32_bf16 a[72:75], a[252:255], a[184:187], a[72:75]\n\t"
"buffer_load_dwordx4 a[244:247], off, s[80:83], s20 offset:2048 sc1\n\t"
"v_mfma_f32_16x16x32_bf16 a[72:75], a[216:219], a[192:195], a[72:75]\n\t"
"s_waitcnt lgkmcnt(2)\n\t"
"ds_read_b64_tr_b16 v[236:237], v255 offset:20480\n\t"
"v_mfma_f32_16x16x32_bf16 a[72:75], a[212:215], a[188:191], a[72:75]\n\t"
"s_waitcnt lgkmcnt(2)\n\t"
"s_mov_b32 s20, 1056768\n\t"
"v_mfma_f32_4x4x4_16b_bf16 v[228:231], v[228:229], v[220:221], 0\n\t"
"v_mfma_f32_4x4x4_16b_bf16 v[224:227], v[224:225], v[220:221], 0\n\t"
"v_mfma_f32_4x4x4_16b_bf16 v[232:235], v[232:233], v[220:221], 0\n\t"
"s_waitcnt lgkmcnt(0)\n\t"
"v_mfma_f32_4x4x4_16b_bf16 v[236:239], v[236:237], v[220:221], 0\n\t"
"v_mfma_f32_16x16x32_bf16 a[80:83], a[252:255], a[188:191], a[80:83]\n\t"
"buffer_load_dwordx4 a[248:251], off, s[80:83], s20 offset:0 sc1\n\t"
"v_mfma_f32_16x16x32_bf16 a[80:83], a[216:219], a[196:199], a[80:83]\n\t"
"v_cvt_pk_bf16_f32 v228, v228, v229\n\t"
"v_cvt_pk_bf16_f32 v229, v230, v231\n\t"
"v_mfma_f32_16x16x32_bf16 a[80:83], a[212:215], a[192:195], a[80:83]\n\t"
"v_cvt_pk_bf16_f32 v230, v224, v225\n\t"
"v_cvt_pk_bf16_f32 v231, v226, v227\n\t"
"v_mfma_f32_16x16x32_bf16 a[88:91], a[252:255], a[192:195], a[88:91]\n\t"
"ds_read_b64_tr_b16 v[224:225], v246 offset:26624\n\t"
"v_cvt_pk_bf16_f32 v232, v232, v233\n\t"
"v_mfma_f32_16x16x32_bf16 a[88:91], a[212:215], a[196:199], a[88:91]\n\t"
"v_cvt_pk_bf16_f32 v233, v234, v235\n\t"
"v_cvt_pk_bf16_f32 v234, v236, v237\n\t"
"v_mfma_f32_16x16x32_bf16 a[88:91], a[216:219], a[200:203], a[88:91]\n\t"
"ds_write_b128 v245, v[228:231] offset:12800\n\t"
"v_cvt_pk_bf16_f32 v235, v238, v239\n\t"
"v_mfma_f32_16x16x32_bf16 a[96:99], a[212:215], a[200:203], a[96:99]\n\t"
"s_waitcnt vmcnt(7)\n\t"
"v_mfma_f32_16x16x32_bf16 a[96:99], a[252:255], a[196:199], a[96:99]\n\t"
"ds_read_b64_tr_b16 v[236:237], v255 offset:26624\n\t"
"v_mfma_f32_16x16x32_bf16 a[96:99], a[216:219], a[204:207], a[96:99]\n\t"
"ds_write_b128 v245, v[232:235] offset:20480\n\t"
"v_mfma_f32_16x16x32_bf16 a[104:107], a[220:223], a[168:171], a[104:107]\n\t"
"buffer_load_dwordx4 a[212:215], off, s[80:83], s20 offset:1024 sc1\n\t"
"v_mfma_f32_16x16x32_bf16 a[104:107], a[228:231], a[176:179], a[104:107]\n\t"
"ds_read_b64_tr_b16 v[228:229], v246 offset:32768\n\t"
"v_mfma_f32_16x16x32_bf16 a[104:107], a[224:227], a[172:175], a[104:107]\n\t"
"s_waitcnt lgkmcnt(4)\n\t"
"ds_read_b64_tr_b16 v[232:233], v255 offset:32768\n\t"
"v_mfma_f32_16x16x32_bf16 a[112:115], a[220:223], a[172:175], a[112:115]\n\t"
"buffer_load_dwordx4 a[216:219], off, s[80:83], s20 offset:2048 sc1\n\t"
"v_mfma_f32_16x16x32_bf16 a[112:115], a[224:227], a[176:179], a[112:115]\n\t"
"Lpg7_%=:\n\t"
"s_cbranch_vccnz Lpg11_%=\n\t"
"s_waitcnt lgkmcnt(3)\n\t"
"ds_read_b128 a[168:171], v222 offset:1792\n\t"
"v_mfma_f32_16x16x32_bf16 a[112:115], a[228:231], a[180:183], a[112:115]\n\t"
"s_mov_b32 s20, 1290240\n\t"
"ds_read_b128 a[172:175], v223 offset:7936\n\t"
"v_mfma_f32_4x4x4_16b_bf16 v[224:227], v[224:225], v[220:221], 0\n\t"
"buffer_load_dwordx4 a[252:255], off, s[80:83], s20 offset:0 sc1\n\t"
"v_mfma_f32_4x4x4_16b_bf16 v[236:239], v[236:237], v[220:221], 0\n\t"
"s_waitcnt lgkmcnt(2)\n\t"
"v_mfma_f32_4x4x4_16b_bf16 v[228:231], v[228:229], v[220:221], 0\n\t"
"v_mfma_f32_4x4x4_16b_bf16 v[232:235], v[232:233], v[220:221], 0\n\t"
"v_mfma_f32_16x16x32_bf16 a[120:123], a[224:227], a[180:183], a[120:123]\n\t"
"v_cvt_pk_bf16_f32 v224, v224, v225\n\t"
"v_cvt_pk_bf16_f32 v225, v226, v227\n\t"
"v_mfma_f32_16x16x32_bf16 a[120:123], a[220:223], a[176:179], a[120:123]\n\t"
"ds_read_b128 a[176:179], v223 offset:14080\n\t"
"v_cvt_pk_bf16_f32 v226, v236, v237\n\t"
"v_mfma_f32_16x16x32_bf16 a[120:123], a[228:231], a[184:187], a[120:123]\n\t"
"v_cvt_pk_bf16_f32 v227, v238, v239\n\t"
"v_cvt_pk_bf16_f32 v228, v228, v229\n\t"
"v_mfma_f32_16x16x32_bf16 a[128:131], a[220:223], a[180:183], a[128:131]\n\t"
"ds_read_b128 a[180:183], v222 offset:21760\n\t"
"v_cvt_pk_bf16_f32 v229, v230, v231\n\t"
"v_mfma_f32_16x16x32_bf16 a[128:131], a[228:231], a[188:191], a[128:131]\n\t"
"v_cvt_pk_bf16_f32 v230, v232, v233\n\t"
"v_cvt_pk_bf16_f32 v231, v234, v235\n\t"
"v_mfma_f32_16x16x32_bf16 a[128:131], a[224:227], a[184:187], a[128:131]\n\t"
"ds_write_b128 v245, v[224:227] offset:26624\n\t"
"v_mfma_f32_16x16x32_bf16 a[136:139], a[220:223], a[184:187], a[136:139]\n\t"
"v_mfma_f32_16x16x32_bf16 a[136:139], a[224:227], a[188:191], a[136:139]\n\t"
"ds_read_b128 a[184:187], v223 offset:27904\n\t"
"v_mfma_f32_16x16x32_bf16 a[136:139], a[228:231], a[192:195], a[136:139]\n\t"
"ds_write_b128 v245, v[228:231] offset:32768\n\t"
"v_mfma_f32_16x16x32_bf16 a[144:147], a[228:231], a[196:199], a[144:147]\n\t"
"v_mfma_f32_16x16x32_bf16 a[144:147], a[224:227], a[192:195], a[144:147]\n\t"
"v_mfma_f32_16x16x32_bf16 a[144:147], a[220:223], a[188:191], a[144:147]\n\t"
"ds_read_b128 a[188:191], v223 offset:34048\n\t"
"v_mfma_f32_16x16x32_bf16 a[152:155], a[220:223], a[192:195], a[152:155]\n\t"
"ds_read_b128 a[192:195], v222 offset:41728\n\t"
"v_mfma_f32_16x16x32_bf16 a[152:155], a[224:227], a[196:199], a[152:155]\n\t"
"v_mfma_f32_16x16x32_bf16 a[152:155], a[228:231], a[200:203], a[152:155]\n\t"
"s_barrier\n\t"
"v_mfma_f32_16x16x32_bf16 a[160:163], a[224:227], a[200:203], a[160:163]\n\t"
"s_waitcnt vmcnt(7) lgkmcnt(6)\n\t"
"ds_read_b64_tr_b16 v[236:237], v240 offset:40448\n\t"
"v_mfma_f32_16x16x32_bf16 a[160:163], a[220:223], a[196:199], a[160:163]\n\t"
"ds_read_b64_tr_b16 v[238:239], v240 offset:41984\n\t"
"v_mfma_f32_16x16x32_bf16 a[160:163], a[228:231], a[204:207], a[160:163]\n\t"
"ds_read_b64_tr_b16 v[232:233], v241 offset:40448\n\t"
"v_mfma_f32_16x16x32_bf16 v[200:203], a[232:235], a[168:171], v[200:203]\n\t"
"s_waitcnt lgkmcnt(8)\n\t"
"ds_read_b64_tr_b16 v[234:235], v241 offset:41984\n\t"
"v_subrev_u32 v240, 59904, v240\n\t"
"v_mfma_f32_16x16x32_bf16 v[200:203], v[248:251], a[176:179], v[200:203]\n\t"
"ds_read_b128 a[196:199], v223 offset:47872\n\t"
"v_subrev_u32 v241, 59904, v241\n\t"
"v_mfma_f32_16x16x32_bf16 v[200:203], v[96:99], a[172:175], v[200:203]\n\t"
"ds_read_b64_tr_b16 v[224:225], v240 offset:40448\n\t"
"v_mfma_f32_16x16x32_bf16 v[208:211], a[232:235], a[172:175], v[208:211]\n\t"
"s_waitcnt lgkmcnt(4)\n\t"
"ds_read_b64_tr_b16 v[226:227], v240 offset:41984\n\t"
"v_mfma_f32_16x16x32_bf16 v[208:211], v[96:99], a[176:179], v[208:211]\n\t"
"buffer_load_dwordx4 a[224:227], off, s[80:83], s20 offset:1024 sc1\n\t"
"v_mfma_f32_16x16x32_bf16 v[208:211], v[248:251], a[180:183], v[208:211]\n\t"
"s_waitcnt lgkmcnt(3)\n\t"
"ds_read_b64_tr_b16 v[228:229], v241 offset:40448\n\t"
"ds_read_b64_tr_b16 v[230:231], v241 offset:41984\n\t"
"v_mfma_f32_16x16x32_bf16 v[236:239], v[236:239], a[208:211], 0\n\t"
"buffer_load_dwordx4 a[220:223], off, s[80:83], s20 offset:2048 sc1\n\t"
"s_mov_b32 s20, 1302528\n\t"
"v_mfma_f32_16x16x32_bf16 v[232:235], v[232:235], a[208:211], 0\n\t"
"s_waitcnt lgkmcnt(2)\n\t"
"buffer_load_dwordx4 a[228:231], off, s[80:83], s20 offset:0 sc1\n\t"
"v_mfma_f32_16x16x32_bf16 v[224:227], v[224:227], a[208:211], 0\n\t"
"s_waitcnt lgkmcnt(0)\n\t"
"ds_read_b128 a[200:203], v223 offset:54016\n\t"
"v_mfma_f32_16x16x32_bf16 v[228:231], v[228:231], a[208:211], 0\n\t"
"ds_read_b128 a[204:207], v222 offset:61696\n\t"
"v_mfma_f32_16x16x32_bf16 v[216:219], v[96:99], a[180:183], v[216:219]\n\t"
"v_mfma_f32_16x16x32_bf16 v[216:219], a[232:235], a[176:179], v[216:219]\n\t"
"v_cvt_pk_bf16_f32 v236, v236, v237\n\t"
"v_cvt_pk_bf16_f32 v237, v238, v239\n\t"
"v_mfma_f32_16x16x32_bf16 v[216:219], v[248:251], a[184:187], v[216:219]\n\t"
"v_cvt_pk_bf16_f32 v238, v232, v233\n\t"
"v_cvt_pk_bf16_f32 v239, v234, v235\n\t"
"v_mfma_f32_16x16x32_bf16 a[4:7], a[232:235], a[180:183], a[4:7]\n\t"
"ds_read_b64_tr_b16 v[232:233], v246 offset:40448\n\t"
"v_cvt_pk_bf16_f32 v224, v224, v225\n\t"
"v_mfma_f32_16x16x32_bf16 a[4:7], v[248:251], a[188:191], a[4:7]\n\t"
"v_cvt_pk_bf16_f32 v225, v226, v227\n\t"
"v_cvt_pk_bf16_f32 v226, v228, v229\n\t"
"v_mfma_f32_16x16x32_bf16 a[4:7], v[96:99], a[184:187], a[4:7]\n\t"
"ds_write_b128 v243, v[236:239] offset:43008\n\t"
"v_mfma_f32_16x16x32_bf16 a[12:15], a[232:235], a[184:187], a[12:15]\n\t"
"v_cvt_pk_bf16_f32 v227, v230, v231\n\t"
"v_subrev_u32 v243, 59904, v243\n\t"
"v_mfma_f32_16x16x32_bf16 a[12:15], v[96:99], a[188:191], a[12:15]\n\t"
"ds_read_b64_tr_b16 v[228:229], v255 offset:40448\n\t"
"v_mfma_f32_16x16x32_bf16 a[12:15], v[248:251], a[192:195], a[12:15]\n\t"
"ds_write_b128 v243, v[224:227] offset:43008\n\t"
"v_mfma_f32_16x16x32_bf16 a[20:23], v[248:251], a[196:199], a[20:23]\n\t"
"s_waitcnt lgkmcnt(5)\n\t"
"v_mfma_f32_16x16x32_bf16 a[20:23], v[96:99], a[192:195], a[20:23]\n\t"
"ds_read_b64_tr_b16 v[236:237], v246 offset:46592\n\t"
"v_mfma_f32_16x16x32_bf16 a[20:23], a[232:235], a[188:191], a[20:23]\n\t"
"ds_read_b64_tr_b16 v[224:225], v255 offset:46592\n\t"
"v_mfma_f32_16x16x32_bf16 a[28:31], a[232:235], a[192:195], a[28:31]\n\t"
"s_waitcnt lgkmcnt(6)\n\t"
"v_mfma_f32_16x16x32_bf16 a[28:31], v[96:99], a[196:199], a[28:31]\n\t"
"v_mfma_f32_16x16x32_bf16 a[28:31], v[248:251], a[200:203], a[28:31]\n\t"
"s_barrier\n\t"
"v_mfma_f32_16x16x32_bf16 a[36:39], v[96:99], a[200:203], a[36:39]\n\t"
"s_waitcnt lgkmcnt(5)\n\t"
"v_mfma_f32_16x16x32_bf16 a[36:39], a[232:235], a[196:199], a[36:39]\n\t"
"buffer_load_dwordx4 v[96:99], off, s[80:83], s20 offset:1024 sc1\n\t"
"v_mfma_f32_16x16x32_bf16 a[36:39], v[248:251], a[204:207], a[36:39]\n\t"
"s_waitcnt lgkmcnt(1)\n\t"
"v_mfma_f32_4x4x4_16b_bf16 v[232:235], v[232:233], v[220:221], 0\n\t"
"s_waitcnt lgkmcnt(0)\n\t"
"v_mfma_f32_4x4x4_16b_bf16 v[228:231], v[228:229], v[220:221], 0\n\t"
"s_waitcnt vmcnt(8)\n\t"
"v_mfma_f32_4x4x4_16b_bf16 v[236:239], v[236:237], v[220:221], 0\n\t"
"v_mfma_f32_4x4x4_16b_bf16 v[224:227], v[224:225], v[220:221], 0\n\t"
"buffer_load_dwordx4 v[248:251], off, s[80:83], s20 offset:2048 sc1\n\t"
"s_mov_b32 s20, 1314816\n\t"
"v_mfma_f32_16x16x32_bf16 a[44:47], a[236:239], a[168:171], a[44:47]\n\t"
"buffer_load_dwordx4 a[232:235], off, s[80:83], s20 offset:0 sc1\n\t"
"v_mfma_f32_16x16x32_bf16 a[44:47], a[244:247], a[176:179], a[44:47]\n\t"
"v_cvt_pk_bf16_f32 v232, v232, v233\n\t"
"v_cvt_pk_bf16_f32 v233, v234, v235\n\t"
"v_mfma_f32_16x16x32_bf16 a[44:47], a[240:243], a[172:175], a[44:47]\n\t"
"v_cvt_pk_bf16_f32 v234, v228, v229\n\t"
"v_cvt_pk_bf16_f32 v235, v230, v231\n\t"
"v_mfma_f32_16x16x32_bf16 a[52:55], a[236:239], a[172:175], a[52:55]\n\t"
"ds_read_b64_tr_b16 v[228:229], v246 offset:52736\n\t"
"v_cvt_pk_bf16_f32 v236, v236, v237\n\t"
"v_mfma_f32_16x16x32_bf16 a[52:55], a[240:243], a[176:179], a[52:55]\n\t"
"v_cvt_pk_bf16_f32 v237, v238, v239\n\t"
"v_cvt_pk_bf16_f32 v238, v224, v225\n\t"
"v_mfma_f32_16x16x32_bf16 a[52:55], a[244:247], a[180:183], a[52:55]\n\t"
"ds_write_b128 v245, v[232:235] offset:40448\n\t"
"v_cvt_pk_bf16_f32 v239, v226, v227\n\t"
"v_mfma_f32_16x16x32_bf16 a[60:63], a[244:247], a[184:187], a[60:63]\n\t"
"v_mfma_f32_16x16x32_bf16 a[60:63], a[240:243], a[180:183], a[60:63]\n\t"
"ds_read_b64_tr_b16 v[224:225], v255 offset:52736\n\t"
"v_mfma_f32_16x16x32_bf16 a[60:63], a[236:239], a[176:179], a[60:63]\n\t"
"ds_write_b128 v245, v[236:239] offset:46592\n\t"
"v_mfma_f32_16x16x32_bf16 a[68:71], a[236:239], a[180:183], a[68:71]\n\t"
"v_mfma_f32_16x16x32_bf16 a[68:71], a[240:243], a[184:187], a[68:71]\n\t"
"v_mfma_f32_16x16x32_bf16 a[68:71], a[244:247], a[188:191], a[68:71]\n\t"
"v_mfma_f32_16x16x32_bf16 a[76:79], a[236:239], a[184:187], a[76:79]\n\t"
"s_waitcnt lgkmcnt(3)\n\t"
"v_mfma_f32_16x16x32_bf16 a[76:79], a[244:247], a[192:195], a[76:79]\n\t"
"s_waitcnt lgkmcnt(1)\n\t"
"v_mfma_f32_16x16x32_bf16 a[76:79], a[240:243], a[188:191], a[76:79]\n\t"
"v_mfma_f32_4x4x4_16b_bf16 v[228:231], v[228:229], v[220:221], 0\n\t"
"v_mfma_f32_4x4x4_16b_bf16 v[224:227], v[224:225], v[220:221], 0\n\t"
"v_mfma_f32_16x16x32_bf16 a[84:87], a[236:239], a[188:191], a[84:87]\n\t"
"v_mfma_f32_16x16x32_bf16 a[84:87], a[244:247], a[196:199], a[84:87]\n\t"
"v_cvt_pk_bf16_f32 v228, v228, v229\n\t"
"v_cvt_pk_bf16_f32 v229, v230, v231\n\t"
"v_mfma_f32_16x16x32_bf16 a[84:87], a[240:243], a[192:195], a[84:87]\n\t"
"v_cvt_pk_bf16_f32 v230, v224, v225\n\t"
"v_cvt_pk_bf16_f32 v231, v226, v227\n\t"
"v_mfma_f32_16x16x32_bf16 a[92:95], a[236:239], a[192:195], a[92:95]\n\t"
"v_mfma_f32_16x16x32_bf16 a[92:95], a[240:243], a[196:199], a[92:95]\n\t"
"v_mfma_f32_16x16x32_bf16 a[92:95], a[244:247], a[200:203], a[92:95]\n\t"
"ds_write_b128 v245, v[228:231] offset:52736\n\t"
"v_mfma_f32_16x16x32_bf16 a[100:103], a[240:243], a[200:203], a[100:103]\n\t"
"buffer_load_dwordx4 a[240:243], off, s[80:83], s20 offset:1024 sc1\n\t"
"v_mfma_f32_16x16x32_bf16 a[100:103], a[236:239], a[196:199], a[100:103]\n\t"
"s_waitcnt vmcnt(8)\n\t"
"v_mfma_f32_16x16x32_bf16 a[100:103], a[244:247], a[204:207], a[100:103]\n\t"
"v_mfma_f32_16x16x32_bf16 a[108:111], a[248:251], a[168:171], a[108:111]\n\t"
"buffer_load_dwordx4 a[236:239], off, s[80:83], s20 offset:2048 sc1\n\t"
"v_mfma_f32_16x16x32_bf16 a[108:111], a[216:219], a[176:179], a[108:111]\n\t"
"s_mov_b32 s20, 1327104\n\t"
"v_mfma_f32_16x16x32_bf16 a[108:111], a[212:215], a[172:175], a[108:111]\n\t"
"s_waitcnt lgkmcnt(0)\n\t"
"s_barrier\n\t"
"v_mfma_f32_16x16x32_bf16 a[116:119], a[248:251], a[172:175], a[116:119]\n\t"
"ds_read_b128 a[168:171], v222 offset:512\n\t"
"v_mfma_f32_16x16x32_bf16 a[116:119], a[212:215], a[176:179], a[116:119]\n\t"
"buffer_load_dwordx4 a[244:247], off, s[80:83], s20 offset:0 sc1\n\t"
"v_mfma_f32_16x16x32_bf16 a[116:119], a[216:219], a[180:183], a[116:119]\n\t"
"ds_read_b128 a[172:175], v223 offset:6656\n\t"
"v_mfma_f32_16x16x32_bf16 a[124:127], a[216:219], a[184:187], a[124:127]\n\t"
"v_mfma_f32_16x16x32_bf16 a[124:127], a[212:215], a[180:183], a[124:127]\n\t"
"v_mfma_f32_16x16x32_bf16 a[124:127], a[248:251], a[176:179], a[124:127]\n\t"
"ds_read_b128 a[176:179], v223 offset:12800\n\t"
"v_mfma_f32_16x16x32_bf16 a[132:135], a[248:251], a[180:183], a[132:135]\n\t"
"ds_read_b64_tr_b16 v[232:233], v240 offset:1024\n\t"
"v_mfma_f32_16x16x32_bf16 a[132:135], a[212:215], a[184:187], a[132:135]\n\t"
"ds_read_b64_tr_b16 v[234:235], v240 offset:2560\n\t"
"v_mfma_f32_16x16x32_bf16 a[132:135], a[216:219], a[188:191], a[132:135]\n\t"
"ds_read_b64_tr_b16 v[236:237], v241 offset:1024\n\t"
"v_mfma_f32_16x16x32_bf16 a[140:143], a[216:219], a[192:195], a[140:143]\n\t"
"ds_read_b64_tr_b16 v[238:239], v241 offset:2560\n\t"
"v_mfma_f32_16x16x32_bf16 a[140:143], a[212:215], a[188:191], a[140:143]\n\t"
"ds_read_b128 a[180:183], v222 offset:20480\n\t"
"v_mfma_f32_16x16x32_bf16 a[140:143], a[248:251], a[184:187], a[140:143]\n\t"
"ds_read_b64_tr_b16 v[224:225], v240 offset:20992\n\t"
"v_mfma_f32_16x16x32_bf16 a[148:151], a[248:251], a[188:191], a[148:151]\n\t"
"ds_read_b64_tr_b16 v[226:227], v240 offset:22528\n\t"
"v_mfma_f32_16x16x32_bf16 a[148:151], a[212:215], a[192:195], a[148:151]\n\t"
"ds_read_b64_tr_b16 v[228:229], v241 offset:20992\n\t"
"v_mfma_f32_16x16x32_bf16 a[148:151], a[216:219], a[196:199], a[148:151]\n\t"
"ds_read_b64_tr_b16 v[230:231], v241 offset:22528\n\t"
"v_mfma_f32_16x16x32_bf16 a[156:159], a[216:219], a[200:203], a[156:159]\n\t"
"ds_read_b128 a[184:187], v223 offset:26624\n\t"
"v_mfma_f32_16x16x32_bf16 a[156:159], a[212:215], a[196:199], a[156:159]\n\t"
"ds_read_b128 a[188:191], v223 offset:32768\n\t"
"v_mfma_f32_16x16x32_bf16 a[156:159], a[248:251], a[192:195], a[156:159]\n\t"
"ds_read_b128 a[192:195], v222 offset:40448\n\t"
"v_mfma_f32_16x16x32_bf16 a[164:167], a[248:251], a[196:199], a[164:167]\n\t"
"buffer_load_dwordx4 a[248:251], off, s[80:83], s20 offset:1024 sc1\n\t"
"v_mfma_f32_16x16x32_bf16 a[164:167], a[212:215], a[200:203], a[164:167]\n\t"
"s_waitcnt lgkmcnt(8)\n\t"
"ds_read_b128 a[196:199], v223 offset:46592\n\t"
"v_mfma_f32_16x16x32_bf16 a[164:167], a[216:219], a[204:207], a[164:167]\n\t"
"s_waitcnt lgkmcnt(6)\n\t"
"ds_read_b128 a[200:203], v223 offset:52736\n\t"
"v_mfma_f32_16x16x32_bf16 v[232:235], v[232:235], a[208:211], 0\n\t"
"buffer_load_dwordx4 a[212:215], off, s[80:83], s20 offset:2048 sc1\n\t"
"v_mfma_f32_16x16x32_bf16 v[236:239], v[236:239], a[208:211], 0\n\t"
"s_waitcnt vmcnt(9) lgkmcnt(5)\n\t"
"ds_read_b128 a[204:207], v222 offset:60416\n\t"
"s_mov_b32 s20, 1339392\n\t"
"v_mfma_f32_16x16x32_bf16 v[224:227], v[224:227], a[208:211], 0\n\t"
"v_mfma_f32_16x16x32_bf16 v[228:231], v[228:231], a[208:211], 0\n\t"
"buffer_load_dwordx4 a[216:219], off, s[80:83], s20 offset:0 sc1\n\t"
"Lst10_%=:\n\t"
"v_mfma_f32_16x16x32_bf16 v[196:199], a[252:255], a[168:171], v[196:199]\n\t"
"v_mfma_f32_16x16x32_bf16 v[196:199], a[220:223], a[176:179], v[196:199]\n\t"
"v_cvt_pk_bf16_f32 v232, v232, v233\n\t"
"v_cvt_pk_bf16_f32 v233, v234, v235\n\t"
"v_mfma_f32_16x16x32_bf16 v[196:199], a[224:227], a[172:175], v[196:199]\n\t"
"v_cvt_pk_bf16_f32 v234, v236, v237\n\t"
"v_cvt_pk_bf16_f32 v235, v238, v239\n\t"
"v_mfma_f32_16x16x32_bf16 v[204:207], a[252:255], a[172:175], v[204:207]\n\t"
"s_waitcnt lgkmcnt(5)\n\t"
"v_cvt_pk_bf16_f32 v224, v224, v225\n\t"
"v_cvt_pk_bf16_f32 v225, v226, v227\n\t"
"v_mfma_f32_16x16x32_bf16 v[204:207], a[224:227], a[176:179], v[204:207]\n\t"
"v_cvt_pk_bf16_f32 v226, v228, v229\n\t"
"v_cvt_pk_bf16_f32 v227, v230, v231\n\t"
"v_mfma_f32_16x16x32_bf16 v[204:207], a[220:223], a[180:183], v[204:207]\n\t"
"ds_write_b128 v243, v[232:235] offset:1536\n\t"
"v_mfma_f32_16x16x32_bf16 v[212:215], a[220:223], a[184:187], v[212:215]\n\t"
"s_waitcnt lgkmcnt(5)\n\t"
"v_mfma_f32_16x16x32_bf16 v[212:215], a[224:227], a[180:183], v[212:215]\n\t"
"v_mfma_f32_16x16x32_bf16 v[212:215], a[252:255], a[176:179], v[212:215]\n\t"
"ds_write_b128 v243, v[224:227] offset:21504\n\t"
"v_mfma_f32_16x16x32_bf16 a[0:3], a[252:255], a[180:183], a[0:3]\n\t"
"s_waitcnt lgkmcnt(5)\n\t"
"v_mfma_f32_16x16x32_bf16 a[0:3], a[224:227], a[184:187], a[0:3]\n\t"
"ds_read_b64_tr_b16 v[236:237], v240 offset:40960\n\t"
"v_mfma_f32_16x16x32_bf16 a[0:3], a[220:223], a[188:191], a[0:3]\n\t"
"ds_read_b64_tr_b16 v[238:239], v240 offset:42496\n\t"
"v_mfma_f32_16x16x32_bf16 a[8:11], a[220:223], a[192:195], a[8:11]\n\t"
"s_waitcnt lgkmcnt(6)\n\t"
"ds_read_b64_tr_b16 v[228:229], v241 offset:40960\n\t"
"v_mfma_f32_16x16x32_bf16 a[8:11], a[224:227], a[188:191], a[8:11]\n\t"
"ds_read_b64_tr_b16 v[230:231], v241 offset:42496\n\t"
"v_add_u32 v240, 59904, v240\n\t"
"v_mfma_f32_16x16x32_bf16 a[8:11], a[252:255], a[184:187], a[8:11]\n\t"
"v_add_u32 v241, 59904, v241\n\t"
"v_mfma_f32_16x16x32_bf16 a[16:19], a[252:255], a[188:191], a[16:19]\n\t"
"s_waitcnt lgkmcnt(7)\n\t"
"ds_read_b64_tr_b16 v[232:233], v240 offset:1024\n\t"
"v_mfma_f32_16x16x32_bf16 a[16:19], a[224:227], a[192:195], a[16:19]\n\t"
"ds_read_b64_tr_b16 v[234:235], v240 offset:2560\n\t"
"v_mfma_f32_16x16x32_bf16 a[16:19], a[220:223], a[196:199], a[16:19]\n\t"
"ds_read_b64_tr_b16 v[224:225], v241 offset:1024\n\t"
"v_mfma_f32_16x16x32_bf16 a[24:27], a[220:223], a[200:203], a[24:27]\n\t"
"s_waitcnt lgkmcnt(9)\n\t"
"ds_read_b64_tr_b16 v[226:227], v241 offset:2560\n\t"
"v_mfma_f32_16x16x32_bf16 a[24:27], a[224:227], a[196:199], a[24:27]\n\t"
"v_mfma_f32_16x16x32_bf16 a[24:27], a[252:255], a[192:195], a[24:27]\n\t"
"v_mfma_f32_16x16x32_bf16 a[32:35], a[252:255], a[196:199], a[32:35]\n\t"
"buffer_load_dwordx4 a[252:255], off, s[80:83], s20 offset:1024 sc1\n\t"
"v_mfma_f32_16x16x32_bf16 a[32:35], a[224:227], a[200:203], a[32:35]\n\t"
"s_waitcnt lgkmcnt(4)\n\t"
"v_mfma_f32_16x16x32_bf16 a[32:35], a[220:223], a[204:207], a[32:35]\n\t"
"s_waitcnt lgkmcnt(2)\n\t"
"v_mfma_f32_16x16x32_bf16 v[236:239], v[236:239], a[208:211], 0\n\t"
"buffer_load_dwordx4 a[220:223], off, s[80:83], s20 offset:2048 sc1\n\t"
"v_mfma_f32_16x16x32_bf16 v[228:231], v[228:231], a[208:211], 0\n\t"
"s_waitcnt vmcnt(9) lgkmcnt(0)\n\t"
"s_mov_b32 s20, 1351680\n\t"
"v_mfma_f32_16x16x32_bf16 v[232:235], v[232:235], a[208:211], 0\n\t"
"v_mfma_f32_16x16x32_bf16 v[224:227], v[224:227], a[208:211], 0\n\t"
"buffer_load_dwordx4 a[224:227], off, s[80:83], s20 offset:0 sc1\n\t"
"v_mfma_f32_16x16x32_bf16 a[40:43], a[228:231], a[168:171], a[40:43]\n\t"
"v_mfma_f32_16x16x32_bf16 a[40:43], v[248:251], a[176:179], a[40:43]\n\t"
"v_cvt_pk_bf16_f32 v236, v236, v237\n\t"
"v_cvt_pk_bf16_f32 v237, v238, v239\n\t"
"v_mfma_f32_16x16x32_bf16 a[40:43], v[96:99], a[172:175], a[40:43]\n\t"
"v_cvt_pk_bf16_f32 v238, v228, v229\n\t"
"v_cvt_pk_bf16_f32 v239, v230, v231\n\t"
"v_mfma_f32_16x16x32_bf16 a[48:51], a[228:231], a[172:175], a[48:51]\n\t"
"v_cvt_pk_bf16_f32 v232, v232, v233\n\t"
"v_cvt_pk_bf16_f32 v233, v234, v235\n\t"
"v_mfma_f32_16x16x32_bf16 a[48:51], v[96:99], a[176:179], a[48:51]\n\t"
"v_cvt_pk_bf16_f32 v234, v224, v225\n\t"
"v_cvt_pk_bf16_f32 v235, v226, v227\n\t"
"v_mfma_f32_16x16x32_bf16 a[48:51], v[248:251], a[180:183], a[48:51]\n\t"
"ds_write_b128 v243, v[236:239] offset:41472\n\t"
"v_mfma_f32_16x16x32_bf16 a[56:59], v[248:251], a[184:187], a[56:59]\n\t"
"v_add_u32 v243, 59904, v243\n\t"
"v_mfma_f32_16x16x32_bf16 a[56:59], v[96:99], a[180:183], a[56:59]\n\t"
"v_mfma_f32_16x16x32_bf16 a[56:59], a[228:231], a[176:179], a[56:59]\n\t"
"ds_write_b128 v243, v[232:235] offset:1536\n\t"
"v_mfma_f32_16x16x32_bf16 a[64:67], a[228:231], a[180:183], a[64:67]\n\t"
"v_mfma_f32_16x16x32_bf16 a[64:67], v[96:99], a[184:187], a[64:67]\n\t"
"ds_read_b64_tr_b16 v[228:229], v240 offset:20992\n\t"
"v_mfma_f32_16x16x32_bf16 a[64:67], v[248:251], a[188:191], a[64:67]\n\t"
"ds_read_b64_tr_b16 v[230:231], v240 offset:22528\n\t"
"v_mfma_f32_16x16x32_bf16 a[72:75], v[248:251], a[192:195], a[72:75]\n\t"
"ds_read_b64_tr_b16 v[224:225], v241 offset:20992\n\t"
"v_mfma_f32_16x16x32_bf16 a[72:75], v[96:99], a[188:191], a[72:75]\n\t"
"ds_read_b64_tr_b16 v[226:227], v241 offset:22528\n\t"
"v_mfma_f32_16x16x32_bf16 a[72:75], a[228:231], a[184:187], a[72:75]\n\t"
"ds_read_b64_tr_b16 v[236:237], v240 offset:40960\n\t"
"v_mfma_f32_16x16x32_bf16 a[80:83], a[228:231], a[188:191], a[80:83]\n\t"
"ds_read_b64_tr_b16 v[238:239], v240 offset:42496\n\t"
"v_mfma_f32_16x16x32_bf16 a[80:83], v[96:99], a[192:195], a[80:83]\n\t"
"ds_read_b64_tr_b16 v[232:233], v241 offset:40960\n\t"
"v_mfma_f32_16x16x32_bf16 a[80:83], v[248:251], a[196:199], a[80:83]\n\t"
"ds_read_b64_tr_b16 v[234:235], v241 offset:42496\n\t"
"v_mfma_f32_16x16x32_bf16 a[88:91], v[248:251], a[200:203], a[88:91]\n\t"
"v_mfma_f32_16x16x32_bf16 a[88:91], v[96:99], a[196:199], a[88:91]\n\t"
"v_mfma_f32_16x16x32_bf16 a[88:91], a[228:231], a[192:195], a[88:91]\n\t"
"v_mfma_f32_16x16x32_bf16 a[96:99], a[228:231], a[196:199], a[96:99]\n\t"
"buffer_load_dwordx4 a[228:231], off, s[80:83], s20 offset:1024 sc1\n\t"
"v_mfma_f32_16x16x32_bf16 a[96:99], v[96:99], a[200:203], a[96:99]\n\t"
"s_waitcnt lgkmcnt(4)\n\t"
"v_mfma_f32_16x16x32_bf16 a[96:99], v[248:251], a[204:207], a[96:99]\n\t"
"s_waitcnt lgkmcnt(2)\n\t"
"v_mfma_f32_16x16x32_bf16 v[228:231], v[228:231], a[208:211], 0\n\t"
"buffer_load_dwordx4 v[96:99], off, s[80:83], s20 offset:2048 sc1\n\t"
"v_mfma_f32_16x16x32_bf16 v[224:227], v[224:227], a[208:211], 0\n\t"
"s_waitcnt vmcnt(9) lgkmcnt(0)\n\t"
"s_mov_b32 s20, 1216512\n\t"
"v_mfma_f32_16x16x32_bf16 v[236:239], v[236:239], a[208:211], 0\n\t"
"v_mfma_f32_16x16x32_bf16 v[232:235], v[232:235], a[208:211], 0\n\t"
"buffer_load_dwordx4 v[248:251], off, s[80:83], s20 offset:0 sc1\n\t"
"v_mfma_f32_16x16x32_bf16 a[104:107], a[232:235], a[168:171], a[104:107]\n\t"
"ds_read_b128 a[168:171], v222 offset:768\n\t"
"v_mfma_f32_16x16x32_bf16 a[104:107], a[236:239], a[176:179], a[104:107]\n\t"
"v_cvt_pk_bf16_f32 v228, v228, v229\n\t"
"v_cvt_pk_bf16_f32 v229, v230, v231\n\t"
"v_mfma_f32_16x16x32_bf16 a[104:107], a[240:243], a[172:175], a[104:107]\n\t"
"v_cvt_pk_bf16_f32 v230, v224, v225\n\t"
"v_cvt_pk_bf16_f32 v231, v226, v227\n\t"
"v_mfma_f32_16x16x32_bf16 a[112:115], a[232:235], a[172:175], a[112:115]\n\t"
"ds_read_b128 a[172:175], v223 offset:6912\n\t"
"v_cvt_pk_bf16_f32 v236, v236, v237\n\t"
"v_mfma_f32_16x16x32_bf16 a[112:115], a[240:243], a[176:179], a[112:115]\n\t"
"v_cvt_pk_bf16_f32 v237, v238, v239\n\t"
"v_cvt_pk_bf16_f32 v238, v232, v233\n\t"
"v_mfma_f32_16x16x32_bf16 a[112:115], a[236:239], a[180:183], a[112:115]\n\t"
"buffer_load_dwordx4 a[208:211], off, s[80:83], s20 offset:1024 sc1\n\t"
"v_mfma_f32_16x16x32_bf16 a[120:123], a[236:239], a[184:187], a[120:123]\n\t"
"v_cvt_pk_bf16_f32 v239, v234, v235\n\t"
"v_mfma_f32_16x16x32_bf16 a[120:123], a[240:243], a[180:183], a[120:123]\n\t"
"v_mfma_f32_16x16x32_bf16 a[120:123], a[232:235], a[176:179], a[120:123]\n\t"
"ds_write_b128 v243, v[228:231] offset:21504\n\t"
"v_mfma_f32_16x16x32_bf16 a[128:131], a[232:235], a[180:183], a[128:131]\n\t"
"v_mfma_f32_16x16x32_bf16 a[128:131], a[240:243], a[184:187], a[128:131]\n\t"
"ds_read_b128 a[176:179], v223 offset:13056\n\t"
"v_mfma_f32_16x16x32_bf16 a[128:131], a[236:239], a[188:191], a[128:131]\n\t"
"ds_write_b128 v243, v[236:239] offset:41472\n\t"
"v_mfma_f32_16x16x32_bf16 a[136:139], a[236:239], a[192:195], a[136:139]\n\t"
"buffer_load_dwordx4 v[240:243], off, s[80:83], s20 offset:2048 sc1\n\t"
"v_mfma_f32_16x16x32_bf16 a[136:139], a[240:243], a[188:191], a[136:139]\n\t"
"ds_read_b128 a[180:183], v222 offset:20736\n\t"
"v_mfma_f32_16x16x32_bf16 a[136:139], a[232:235], a[184:187], a[136:139]\n\t"
"ds_read_b128 a[184:187], v223 offset:26880\n\t"
"v_mfma_f32_16x16x32_bf16 a[144:147], a[232:235], a[188:191], a[144:147]\n\t"
"s_mov_b32 s20, 1228800\n\t"
"ds_read_b128 a[188:191], v223 offset:33024\n\t"
"v_mfma_f32_16x16x32_bf16 a[144:147], a[240:243], a[192:195], a[144:147]\n\t"
"v_mfma_f32_16x16x32_bf16 a[144:147], a[236:239], a[196:199], a[144:147]\n\t"
"v_mfma_f32_16x16x32_bf16 a[152:155], a[236:239], a[200:203], a[152:155]\n\t"
"v_mfma_f32_16x16x32_bf16 a[152:155], a[240:243], a[196:199], a[152:155]\n\t"
"v_mfma_f32_16x16x32_bf16 a[152:155], a[232:235], a[192:195], a[152:155]\n\t"
"ds_read_b128 a[192:195], v222 offset:40704\n\t"
"v_mfma_f32_16x16x32_bf16 a[160:163], a[232:235], a[196:199], a[160:163]\n\t"
"buffer_load_dwordx4 a[232:235], off, s[80:83], s20 offset:0 sc1\n\t"
"v_mfma_f32_16x16x32_bf16 a[160:163], a[240:243], a[200:203], a[160:163]\n\t"
"s_waitcnt vmcnt(10) lgkmcnt(5)\n\t"
"ds_read_b128 a[196:199], v223 offset:46848\n\t"
"v_mfma_f32_16x16x32_bf16 a[160:163], a[236:239], a[204:207], a[160:163]\n\t"
"ds_read_b128 a[200:203], v223 offset:52992\n\t"
"v_mfma_f32_16x16x32_bf16 v[200:203], a[244:247], a[168:171], v[200:203]\n\t"
"s_waitcnt lgkmcnt(5)\n\t"
"ds_read_b128 a[204:207], v222 offset:60672\n\t"
"v_mfma_f32_16x16x32_bf16 v[200:203], a[212:215], a[176:179], v[200:203]\n\t"
"buffer_load_dwordx4 a[236:239], off, s[80:83], s20 offset:1024 sc1\n\t"
"v_mfma_f32_16x16x32_bf16 v[200:203], a[248:251], a[172:175], v[200:203]\n\t"
"v_mfma_f32_16x16x32_bf16 v[208:211], a[244:247], a[172:175], v[208:211]\n\t"
"buffer_load_dwordx4 a[240:243], off, s[80:83], s20 offset:2048 sc1\n\t"
"v_mfma_f32_16x16x32_bf16 v[208:211], a[248:251], a[176:179], v[208:211]\n\t"
"s_waitcnt lgkmcnt(5)\n\t"
"v_mfma_f32_16x16x32_bf16 v[208:211], a[212:215], a[180:183], v[208:211]\n\t"
"s_waitcnt lgkmcnt(0)\n\t"
"s_barrier\n\t"
"v_mfma_f32_16x16x32_bf16 v[216:219], a[212:215], a[184:187], v[216:219]\n\t"
"v_mfma_f32_16x16x32_bf16 v[216:219], a[248:251], a[180:183], v[216:219]\n\t"
"v_mfma_f32_16x16x32_bf16 v[216:219], a[244:247], a[176:179], v[216:219]\n\t"
"ds_read_b64_tr_b16 v[224:225], v246 offset:1024\n\t"
"v_mfma_f32_16x16x32_bf16 a[4:7], a[244:247], a[180:183], a[4:7]\n\t"
"ds_read_b64_tr_b16 v[232:233], v255 offset:1024\n\t"
"v_mfma_f32_16x16x32_bf16 a[4:7], a[248:251], a[184:187], a[4:7]\n\t"
"ds_read_b64_tr_b16 v[228:229], v246 offset:7168\n\t"
"v_mfma_f32_16x16x32_bf16 a[4:7], a[212:215], a[188:191], a[4:7]\n\t"
"ds_read_b64_tr_b16 v[236:237], v255 offset:7168\n\t"
"v_mfma_f32_16x16x32_bf16 a[12:15], a[212:215], a[192:195], a[12:15]\n\t"
"v_mfma_f32_16x16x32_bf16 a[12:15], a[248:251], a[188:191], a[12:15]\n\t"
"v_mfma_f32_16x16x32_bf16 a[12:15], a[244:247], a[184:187], a[12:15]\n\t"
"v_mfma_f32_16x16x32_bf16 a[20:23], a[244:247], a[188:191], a[20:23]\n\t"
"s_mov_b32 s20, 1241088\n\t"
"v_mfma_f32_16x16x32_bf16 a[20:23], a[248:251], a[192:195], a[20:23]\n\t"
"v_mfma_f32_16x16x32_bf16 a[20:23], a[212:215], a[196:199], a[20:23]\n\t"
"v_mfma_f32_16x16x32_bf16 a[28:31], a[212:215], a[200:203], a[28:31]\n\t"
"v_mfma_f32_16x16x32_bf16 a[28:31], a[248:251], a[196:199], a[28:31]\n\t"
"v_mfma_f32_16x16x32_bf16 a[28:31], a[244:247], a[192:195], a[28:31]\n\t"
"v_mfma_f32_16x16x32_bf16 a[36:39], a[244:247], a[196:199], a[36:39]\n\t"
"buffer_load_dwordx4 a[244:247], off, s[80:83], s20 offset:0 sc1\n\t"
"v_mfma_f32_16x16x32_bf16 a[36:39], a[248:251], a[200:203], a[36:39]\n\t"
"s_waitcnt lgkmcnt(2)\n\t"
"v_mfma_f32_16x16x32_bf16 a[36:39], a[212:215], a[204:207], a[36:39]\n\t"
"s_waitcnt lgkmcnt(1)\n\t"
"v_mfma_f32_4x4x4_16b_bf16 v[224:227], v[224:225], v[220:221], 0\n\t"
"s_waitcnt lgkmcnt(0)\n\t"
"v_mfma_f32_4x4x4_16b_bf16 v[232:235], v[232:233], v[220:221], 0\n\t"
"s_waitcnt vmcnt(10)\n\t"
"v_mfma_f32_4x4x4_16b_bf16 v[228:231], v[228:229], v[220:221], 0\n\t"
"v_mfma_f32_4x4x4_16b_bf16 v[236:239], v[236:237], v[220:221], 0\n\t"
"buffer_load_dwordx4 a[212:215], off, s[80:83], s20 offset:1024 sc1\n\t"
"v_mfma_f32_16x16x32_bf16 a[44:47], a[216:219], a[168:171], a[44:47]\n\t"
"buffer_load_dwordx4 a[248:251], off, s[80:83], s20 offset:2048 sc1\n\t"
"v_mfma_f32_16x16x32_bf16 a[44:47], a[220:223], a[176:179], a[44:47]\n\t"
"v_cvt_pk_bf16_f32 v224, v224, v225\n\t"
"v_cvt_pk_bf16_f32 v225, v226, v227\n\t"
"v_mfma_f32_16x16x32_bf16 a[44:47], a[252:255], a[172:175], a[44:47]\n\t"
"v_cvt_pk_bf16_f32 v226, v232, v233\n\t"
"v_cvt_pk_bf16_f32 v227, v234, v235\n\t"
"v_mfma_f32_16x16x32_bf16 a[52:55], a[216:219], a[172:175], a[52:55]\n\t"
"v_cvt_pk_bf16_f32 v228, v228, v229\n\t"
"v_cvt_pk_bf16_f32 v229, v230, v231\n\t"
"v_mfma_f32_16x16x32_bf16 a[52:55], a[252:255], a[176:179], a[52:55]\n\t"
"v_cvt_pk_bf16_f32 v230, v236, v237\n\t"
"v_cvt_pk_bf16_f32 v231, v238, v239\n\t"
"v_mfma_f32_16x16x32_bf16 a[52:55], a[220:223], a[180:183], a[52:55]\n\t"
"ds_write_b128 v245, v[224:227] offset:1024\n\t"
"v_mfma_f32_16x16x32_bf16 a[60:63], a[220:223], a[184:187], a[60:63]\n\t"
"v_mfma_f32_16x16x32_bf16 a[60:63], a[252:255], a[180:183], a[60:63]\n\t"
"v_mfma_f32_16x16x32_bf16 a[60:63], a[216:219], a[176:179], a[60:63]\n\t"
"ds_write_b128 v245, v[228:231] offset:7168\n\t"
"v_mfma_f32_16x16x32_bf16 a[68:71], a[216:219], a[180:183], a[68:71]\n\t"
"v_mfma_f32_16x16x32_bf16 a[68:71], a[252:255], a[184:187], a[68:71]\n\t"
"ds_read_b64_tr_b16 v[232:233], v246 offset:13312\n\t"
"v_mfma_f32_16x16x32_bf16 a[68:71], a[220:223], a[188:191], a[68:71]\n\t"
"ds_read_b64_tr_b16 v[236:237], v255 offset:13312\n\t"
"v_mfma_f32_16x16x32_bf16 a[76:79], a[220:223], a[192:195], a[76:79]\n\t"
"v_mfma_f32_16x16x32_bf16 a[76:79], a[252:255], a[188:191], a[76:79]\n\t"
"v_mfma_f32_16x16x32_bf16 a[76:79], a[216:219], a[184:187], a[76:79]\n\t"
"v_mfma_f32_16x16x32_bf16 a[84:87], a[216:219], a[188:191], a[84:87]\n\t"
"s_mov_b32 s20, 1253376\n\t"
"v_mfma_f32_16x16x32_bf16 a[84:87], a[252:255], a[192:195], a[84:87]\n\t"
"v_mfma_f32_16x16x32_bf16 a[84:87], a[220:223], a[196:199], a[84:87]\n\t"
"v_mfma_f32_16x16x32_bf16 a[92:95], a[220:223], a[200:203], a[92:95]\n\t"
"v_mfma_f32_16x16x32_bf16 a[92:95], a[252:255], a[196:199], a[92:95]\n\t"
"v_mfma_f32_16x16x32_bf16 a[92:95], a[216:219], a[192:195], a[92:95]\n\t"
"v_mfma_f32_16x16x32_bf16 a[100:103], a[216:219], a[196:199], a[100:103]\n\t"
"buffer_load_dwordx4 a[216:219], off, s[80:83], s20 offset:0 sc1\n\t"
"v_mfma_f32_16x16x32_bf16 a[100:103], a[252:255], a[200:203], a[100:103]\n\t"
"s_waitcnt lgkmcnt(0)\n\t"
"v_mfma_f32_16x16x32_bf16 a[100:103], a[220:223], a[204:207], a[100:103]\n\t"
"s_waitcnt vmcnt(10)\n\t"
"v_mfma_f32_4x4x4_16b_bf16 v[232:235], v[232:233], v[220:221], 0\n\t"
"v_mfma_f32_4x4x4_16b_bf16 v[236:239], v[236:237], v[220:221], 0\n\t"
"v_mfma_f32_16x16x32_bf16 a[108:111], a[224:227], a[168:171], a[108:111]\n\t"
"buffer_load_dwordx4 a[220:223], off, s[80:83], s20 offset:1024 sc1\n\t"
"v_mfma_f32_16x16x32_bf16 a[108:111], v[96:99], a[176:179], a[108:111]\n\t"
"buffer_load_dwordx4 a[252:255], off, s[80:83], s20 offset:2048 sc1\n\t"
"v_mfma_f32_16x16x32_bf16 a[108:111], a[228:231], a[172:175], a[108:111]\n\t"
"ds_read_b128 a[168:171], v222 offset:2048\n\t"
"v_cvt_pk_bf16_f32 v232, v232, v233\n\t"
"v_mfma_f32_16x16x32_bf16 a[116:119], a[224:227], a[172:175], a[116:119]\n\t"
"ds_read_b128 a[172:175], v223 offset:8192\n\t"
"v_cvt_pk_bf16_f32 v233, v234, v235\n\t"
"v_mfma_f32_16x16x32_bf16 a[116:119], a[228:231], a[176:179], a[116:119]\n\t"
"v_cvt_pk_bf16_f32 v234, v236, v237\n\t"
"v_cvt_pk_bf16_f32 v235, v238, v239\n\t"
"v_mfma_f32_16x16x32_bf16 a[116:119], v[96:99], a[180:183], a[116:119]\n\t"
"ds_write_b128 v245, v[232:235] offset:13312\n\t"
"v_mfma_f32_16x16x32_bf16 a[124:127], v[96:99], a[184:187], a[124:127]\n\t"
"v_mfma_f32_16x16x32_bf16 a[124:127], a[228:231], a[180:183], a[124:127]\n\t"
"v_mfma_f32_16x16x32_bf16 a[124:127], a[224:227], a[176:179], a[124:127]\n\t"
"ds_read_b128 a[176:179], v223 offset:14336\n\t"
"v_mfma_f32_16x16x32_bf16 a[132:135], a[224:227], a[180:183], a[132:135]\n\t"
"ds_read_b128 a[180:183], v222 offset:22016\n\t"
"v_mfma_f32_16x16x32_bf16 a[132:135], a[228:231], a[184:187], a[132:135]\n\t"
"ds_read_b64_tr_b16 v[224:225], v246 offset:20992\n\t"
"v_mfma_f32_16x16x32_bf16 a[132:135], v[96:99], a[188:191], a[132:135]\n\t"
"ds_read_b64_tr_b16 v[228:229], v255 offset:20992\n\t"
"v_mfma_f32_16x16x32_bf16 a[140:143], v[96:99], a[192:195], a[140:143]\n\t"
"v_mfma_f32_16x16x32_bf16 a[140:143], a[228:231], a[188:191], a[140:143]\n\t"
"v_mfma_f32_16x16x32_bf16 a[140:143], a[224:227], a[184:187], a[140:143]\n\t"
"ds_read_b128 a[184:187], v223 offset:28160\n\t"
"v_mfma_f32_16x16x32_bf16 a[148:151], a[224:227], a[188:191], a[148:151]\n\t"
"s_mov_b32 s20, 1265664\n\t"
"ds_read_b128 a[188:191], v223 offset:34304\n\t"
"v_mfma_f32_16x16x32_bf16 a[148:151], a[228:231], a[192:195], a[148:151]\n\t"
"v_mfma_f32_16x16x32_bf16 a[148:151], v[96:99], a[196:199], a[148:151]\n\t"
"v_mfma_f32_16x16x32_bf16 a[156:159], v[96:99], a[200:203], a[156:159]\n\t"
"v_mfma_f32_16x16x32_bf16 a[156:159], a[228:231], a[196:199], a[156:159]\n\t"
"v_mfma_f32_16x16x32_bf16 a[156:159], a[224:227], a[192:195], a[156:159]\n\t"
"ds_read_b128 a[192:195], v222 offset:41984\n\t"
"v_mfma_f32_16x16x32_bf16 a[164:167], a[224:227], a[196:199], a[164:167]\n\t"
"buffer_load_dwordx4 a[224:227], off, s[80:83], s20 offset:0 sc1\n\t"
"v_mfma_f32_16x16x32_bf16 a[164:167], a[228:231], a[200:203], a[164:167]\n\t"
"s_waitcnt lgkmcnt(3)\n\t"
"ds_read_b128 a[196:199], v223 offset:48128\n\t"
"v_mfma_f32_16x16x32_bf16 a[164:167], v[96:99], a[204:207], a[164:167]\n\t"
"s_waitcnt vmcnt(10)\n\t"
"ds_read_b128 a[200:203], v223 offset:54272\n\t"
"v_mfma_f32_4x4x4_16b_bf16 v[224:227], v[224:225], v[220:221], 0\n\t"
"v_mfma_f32_4x4x4_16b_bf16 v[228:231], v[228:229], v[220:221], 0\n\t"
"s_waitcnt lgkmcnt(3)\n\t"
"Lst11_%=:\n\t"
"v_mfma_f32_16x16x32_bf16 v[0:3], v[248:251], a[168:171], v[0:3]\n\t"
"buffer_load_dwordx4 v[96:99], off, s[80:83], s20 offset:1024 sc1\n\t"
"v_mfma_f32_16x16x32_bf16 v[0:3], v[240:243], a[176:179], v[0:3]\n\t"
"buffer_load_dwordx4 a[228:231], off, s[80:83], s20 offset:2048 sc1\n\t"
"v_mfma_f32_16x16x32_bf16 v[0:3], a[208:211], a[172:175], v[0:3]\n\t"
"Lpg8_%=:\n\t"
"s_cbranch_vccnz Lpg12_%=\n\t"
"ds_read_b128 a[204:207], v222 offset:61952\n\t"
"v_cvt_pk_bf16_f32 v224, v224, v225\n\t"
"v_mfma_f32_16x16x32_bf16 v[4:7], v[248:251], a[172:175], v[4:7]\n\t"
"v_cvt_pk_bf16_f32 v225, v226, v227\n\t"
"v_cvt_pk_bf16_f32 v226, v228, v229\n\t"
"v_mfma_f32_16x16x32_bf16 v[4:7], a[208:211], a[176:179], v[4:7]\n\t"
"v_cvt_pk_bf16_f32 v227, v230, v231\n\t"
"v_mfma_f32_16x16x32_bf16 v[4:7], v[240:243], a[180:183], v[4:7]\n\t"
"ds_write_b128 v245, v[224:227] offset:20992\n\t"
"v_mfma_f32_16x16x32_bf16 v[8:11], v[240:243], a[184:187], v[8:11]\n\t"
"v_mfma_f32_16x16x32_bf16 v[8:11], a[208:211], a[180:183], v[8:11]\n\t"
"v_mfma_f32_16x16x32_bf16 v[8:11], v[248:251], a[176:179], v[8:11]\n\t"
"ds_read_b64_tr_b16 v[236:237], v246 offset:27136\n\t"
"v_mfma_f32_16x16x32_bf16 v[12:15], v[248:251], a[180:183], v[12:15]\n\t"
"s_waitcnt lgkmcnt(5)\n\t"
"ds_read_b64_tr_b16 v[232:233], v255 offset:27136\n\t"
"v_mfma_f32_16x16x32_bf16 v[12:15], a[208:211], a[184:187], v[12:15]\n\t"
"v_mfma_f32_16x16x32_bf16 v[12:15], v[240:243], a[188:191], v[12:15]\n\t"
"v_mfma_f32_16x16x32_bf16 v[16:19], v[240:243], a[192:195], v[16:19]\n\t"
"s_waitcnt lgkmcnt(5)\n\t"
"v_mfma_f32_16x16x32_bf16 v[16:19], a[208:211], a[188:191], v[16:19]\n\t"
"v_mfma_f32_16x16x32_bf16 v[16:19], v[248:251], a[184:187], v[16:19]\n\t"
"v_mfma_f32_16x16x32_bf16 v[20:23], v[248:251], a[188:191], v[20:23]\n\t"
"s_waitcnt lgkmcnt(4)\n\t"
"s_mov_b32 s20, 1277952\n\t"
"v_mfma_f32_16x16x32_bf16 v[20:23], a[208:211], a[192:195], v[20:23]\n\t"
"v_mfma_f32_16x16x32_bf16 v[20:23], v[240:243], a[196:199], v[20:23]\n\t"
"v_mfma_f32_16x16x32_bf16 v[24:27], v[240:243], a[200:203], v[24:27]\n\t"
"s_waitcnt lgkmcnt(3)\n\t"
"v_mfma_f32_16x16x32_bf16 v[24:27], a[208:211], a[196:199], v[24:27]\n\t"
"v_mfma_f32_16x16x32_bf16 v[24:27], v[248:251], a[192:195], v[24:27]\n\t"
"v_mfma_f32_16x16x32_bf16 v[28:31], v[248:251], a[196:199], v[28:31]\n\t"
"buffer_load_dwordx4 v[248:251], off, s[80:83], s20 offset:0 sc1\n\t"
"v_mfma_f32_16x16x32_bf16 v[28:31], a[208:211], a[200:203], v[28:31]\n\t"
"s_waitcnt lgkmcnt(0)\n\t"
"v_mfma_f32_16x16x32_bf16 v[28:31], v[240:243], a[204:207], v[28:31]\n\t"
"s_waitcnt vmcnt(10)\n\t"
"v_mfma_f32_4x4x4_16b_bf16 v[236:239], v[236:237], v[220:221], 0\n\t"
"v_mfma_f32_4x4x4_16b_bf16 v[232:235], v[232:233], v[220:221], 0\n\t"
"v_mfma_f32_16x16x32_bf16 v[32:35], a[232:235], a[168:171], v[32:35]\n\t"
"buffer_load_dwordx4 v[240:243], off, s[80:83], s20 offset:1024 sc1\n\t"
"v_mfma_f32_16x16x32_bf16 v[32:35], a[240:243], a[176:179], v[32:35]\n\t"
"buffer_load_dwordx4 a[208:211], off, s[80:83], s20 offset:2048 sc1\n\t"
"v_mfma_f32_16x16x32_bf16 v[32:35], a[236:239], a[172:175], v[32:35]\n\t"
"v_cvt_pk_bf16_f32 v236, v236, v237\n\t"
"v_cvt_pk_bf16_f32 v237, v238, v239\n\t"
"v_mfma_f32_16x16x32_bf16 v[36:39], a[232:235], a[172:175], v[36:39]\n\t"
"v_cvt_pk_bf16_f32 v238, v232, v233\n\t"
"v_cvt_pk_bf16_f32 v239, v234, v235\n\t"
"v_mfma_f32_16x16x32_bf16 v[36:39], a[236:239], a[176:179], v[36:39]\n\t"
"v_mfma_f32_16x16x32_bf16 v[36:39], a[240:243], a[180:183], v[36:39]\n\t"
"ds_write_b128 v245, v[236:239] offset:27136\n\t"
"v_mfma_f32_16x16x32_bf16 v[40:43], a[240:243], a[184:187], v[40:43]\n\t"
"v_mfma_f32_16x16x32_bf16 v[40:43], a[236:239], a[180:183], v[40:43]\n\t"
"v_mfma_f32_16x16x32_bf16 v[40:43], a[232:235], a[176:179], v[40:43]\n\t"
"ds_read_b64_tr_b16 v[228:229], v246 offset:33280\n\t"
"v_mfma_f32_16x16x32_bf16 v[44:47], a[232:235], a[180:183], v[44:47]\n\t"
"ds_read_b64_tr_b16 v[224:225], v255 offset:33280\n\t"
"v_mfma_f32_16x16x32_bf16 v[44:47], a[236:239], a[184:187], v[44:47]\n\t"
"v_mfma_f32_16x16x32_bf16 v[44:47], a[240:243], a[188:191], v[44:47]\n\t"
"v_mfma_f32_16x16x32_bf16 v[48:51], a[240:243], a[192:195], v[48:51]\n\t"
"v_mfma_f32_16x16x32_bf16 v[48:51], a[236:239], a[188:191], v[48:51]\n\t"
"v_mfma_f32_16x16x32_bf16 v[48:51], a[232:235], a[184:187], v[48:51]\n\t"
"v_mfma_f32_16x16x32_bf16 v[52:55], a[232:235], a[188:191], v[52:55]\n\t"
"s_mov_b32 s20, 1511424\n\t"
"v_mfma_f32_16x16x32_bf16 v[52:55], a[236:239], a[192:195], v[52:55]\n\t"
"v_mfma_f32_16x16x32_bf16 v[52:55], a[240:243], a[196:199], v[52:55]\n\t"
"v_mfma_f32_16x16x32_bf16 v[56:59], a[240:243], a[200:203], v[56:59]\n\t"
"v_mfma_f32_16x16x32_bf16 v[56:59], a[236:239], a[196:199], v[56:59]\n\t"
"v_mfma_f32_16x16x32_bf16 v[56:59], a[232:235], a[192:195], v[56:59]\n\t"
"v_mfma_f32_16x16x32_bf16 v[60:63], a[232:235], a[196:199], v[60:63]\n\t"
"buffer_load_dwordx4 a[232:235], off, s[80:83], s20 offset:0 sc1\n\t"
"v_mfma_f32_16x16x32_bf16 v[60:63], a[236:239], a[200:203], v[60:63]\n\t"
"s_waitcnt lgkmcnt(0)\n\t"
"v_mfma_f32_16x16x32_bf16 v[60:63], a[240:243], a[204:207], v[60:63]\n\t"
"s_waitcnt vmcnt(10)\n\t"
"v_mfma_f32_4x4x4_16b_bf16 v[228:231], v[228:229], v[220:221], 0\n\t"
"v_mfma_f32_4x4x4_16b_bf16 v[224:227], v[224:225], v[220:221], 0\n\t"
"v_mfma_f32_16x16x32_bf16 v[64:67], a[244:247], a[168:171], v[64:67]\n\t"
"buffer_load_dwordx4 a[236:239], off, s[80:83], s20 offset:1024 sc1\n\t"
"v_mfma_f32_16x16x32_bf16 v[64:67], a[248:251], a[176:179], v[64:67]\n\t"
"buffer_load_dwordx4 a[240:243], off, s[80:83], s20 offset:2048 sc1\n\t"
"v_mfma_f32_16x16x32_bf16 v[64:67], a[212:215], a[172:175], v[64:67]\n\t"
"ds_read_b128 a[168:171], v222 offset:2304\n\t"
"v_cvt_pk_bf16_f32 v228, v228, v229\n\t"
"v_mfma_f32_16x16x32_bf16 v[68:71], a[244:247], a[172:175], v[68:71]\n\t"
"ds_read_b128 a[172:175], v223 offset:8448\n\t"
"v_cvt_pk_bf16_f32 v229, v230, v231\n\t"
"v_mfma_f32_16x16x32_bf16 v[68:71], a[212:215], a[176:179], v[68:71]\n\t"
"v_cvt_pk_bf16_f32 v230, v224, v225\n\t"
"v_cvt_pk_bf16_f32 v231, v226, v227\n\t"
"v_mfma_f32_16x16x32_bf16 v[68:71], a[248:251], a[180:183], v[68:71]\n\t"
"ds_write_b128 v245, v[228:231] offset:33280\n\t"
"v_mfma_f32_16x16x32_bf16 v[72:75], a[248:251], a[184:187], v[72:75]\n\t"
"v_mfma_f32_16x16x32_bf16 v[72:75], a[212:215], a[180:183], v[72:75]\n\t"
"v_mfma_f32_16x16x32_bf16 v[72:75], a[244:247], a[176:179], v[72:75]\n\t"
"ds_read_b128 a[176:179], v223 offset:14592\n\t"
"v_mfma_f32_16x16x32_bf16 v[76:79], a[244:247], a[180:183], v[76:79]\n\t"
"ds_read_b128 a[180:183], v222 offset:22272\n\t"
"v_mfma_f32_16x16x32_bf16 v[76:79], a[212:215], a[184:187], v[76:79]\n\t"
"ds_read_b64_tr_b16 v[232:233], v246 offset:40960\n\t"
"v_mfma_f32_16x16x32_bf16 v[76:79], a[248:251], a[188:191], v[76:79]\n\t"
"ds_read_b64_tr_b16 v[236:237], v255 offset:40960\n\t"
"v_mfma_f32_16x16x32_bf16 v[80:83], a[248:251], a[192:195], v[80:83]\n\t"
"v_mfma_f32_16x16x32_bf16 v[80:83], a[212:215], a[188:191], v[80:83]\n\t"
"v_mfma_f32_16x16x32_bf16 v[80:83], a[244:247], a[184:187], v[80:83]\n\t"
"ds_read_b128 a[184:187], v223 offset:28416\n\t"
"v_mfma_f32_16x16x32_bf16 v[84:87], a[244:247], a[188:191], v[84:87]\n\t"
"s_mov_b32 s20, 1523712\n\t"
"ds_read_b128 a[188:191], v223 offset:34560\n\t"
"v_mfma_f32_16x16x32_bf16 v[84:87], a[212:215], a[192:195], v[84:87]\n\t"
"v_mfma_f32_16x16x32_bf16 v[84:87], a[248:251], a[196:199], v[84:87]\n\t"
"v_mfma_f32_16x16x32_bf16 v[88:91], a[248:251], a[200:203], v[88:91]\n\t"
"v_mfma_f32_16x16x32_bf16 v[88:91], a[212:215], a[196:199], v[88:91]\n\t"
"v_mfma_f32_16x16x32_bf16 v[88:91], a[244:247], a[192:195], v[88:91]\n\t"
"ds_read_b128 a[192:195], v222 offset:42240\n\t"
"v_mfma_f32_16x16x32_bf16 v[92:95], a[244:247], a[196:199], v[92:95]\n\t"
"buffer_load_dwordx4 a[244:247], off, s[80:83], s20 offset:0 sc1\n\t"
"v_mfma_f32_16x16x32_bf16 v[92:95], a[212:215], a[200:203], v[92:95]\n\t"
"s_waitcnt lgkmcnt(3)\n\t"
"ds_read_b128 a[196:199], v223 offset:48384\n\t"
"v_mfma_f32_16x16x32_bf16 v[92:95], a[248:251], a[204:207], v[92:95]\n\t"
"s_waitcnt vmcnt(10)\n\t"
"ds_read_b128 a[200:203], v223 offset:54528\n\t"
"v_mfma_f32_4x4x4_16b_bf16 v[232:235], v[232:233], v[220:221], 0\n\t"
"v_mfma_f32_4x4x4_16b_bf16 v[236:239], v[236:237], v[220:221], 0\n\t"
"v_mfma_f32_16x16x32_bf16 v[100:103], a[216:219], a[168:171], v[100:103]\n\t"
"buffer_load_dwordx4 a[212:215], off, s[80:83], s20 offset:1024 sc1\n\t"
"v_mfma_f32_16x16x32_bf16 v[100:103], a[252:255], a[176:179], v[100:103]\n\t"
"buffer_load_dwordx4 a[248:251], off, s[80:83], s20 offset:2048 sc1\n\t"
"v_mfma_f32_16x16x32_bf16 v[100:103], a[220:223], a[172:175], v[100:103]\n\t"
"ds_read_b128 a[204:207], v222 offset:62208\n\t"
"v_cvt_pk_bf16_f32 v232, v232, v233\n\t"
"v_mfma_f32_16x16x32_bf16 v[104:107], a[216:219], a[172:175], v[104:107]\n\t"
"s_waitcnt lgkmcnt(5)\n\t"
"v_cvt_pk_bf16_f32 v233, v234, v235\n\t"
"v_cvt_pk_bf16_f32 v234, v236, v237\n\t"
"v_mfma_f32_16x16x32_bf16 v[104:107], a[220:223], a[176:179], v[104:107]\n\t"
"v_cvt_pk_bf16_f32 v235, v238, v239\n\t"
"v_mfma_f32_16x16x32_bf16 v[104:107], a[252:255], a[180:183], v[104:107]\n\t"
"ds_write_b128 v245, v[232:235] offset:40960\n\t"
"v_mfma_f32_16x16x32_bf16 v[108:111], a[252:255], a[184:187], v[108:111]\n\t"
"s_waitcnt lgkmcnt(5)\n\t"
"v_mfma_f32_16x16x32_bf16 v[108:111], a[220:223], a[180:183], v[108:111]\n\t"
"v_mfma_f32_16x16x32_bf16 v[108:111], a[216:219], a[176:179], v[108:111]\n\t"
"ds_read_b64_tr_b16 v[224:225], v246 offset:47104\n\t"
"v_mfma_f32_16x16x32_bf16 v[112:115], a[216:219], a[180:183], v[112:115]\n\t"
"s_waitcnt lgkmcnt(5)\n\t"
"ds_read_b64_tr_b16 v[228:229], v255 offset:47104\n\t"
"v_mfma_f32_16x16x32_bf16 v[112:115], a[220:223], a[184:187], v[112:115]\n\t"
"ds_read_b64_tr_b16 v[236:237], v246 offset:53248\n\t"
"v_mfma_f32_16x16x32_bf16 v[112:115], a[252:255], a[188:191], v[112:115]\n\t"
"ds_read_b64_tr_b16 v[232:233], v255 offset:53248\n\t"
"v_mfma_f32_16x16x32_bf16 v[116:119], a[252:255], a[192:195], v[116:119]\n\t"
"s_waitcnt lgkmcnt(7)\n\t"
"v_mfma_f32_16x16x32_bf16 v[116:119], a[220:223], a[188:191], v[116:119]\n\t"
"v_mfma_f32_16x16x32_bf16 v[116:119], a[216:219], a[184:187], v[116:119]\n\t"
"v_mfma_f32_16x16x32_bf16 v[120:123], a[216:219], a[188:191], v[120:123]\n\t"
"s_waitcnt lgkmcnt(6)\n\t"
"s_mov_b32 s20, 1536000\n\t"
"v_mfma_f32_16x16x32_bf16 v[120:123], a[220:223], a[192:195], v[120:123]\n\t"
"v_mfma_f32_16x16x32_bf16 v[120:123], a[252:255], a[196:199], v[120:123]\n\t"
"v_mfma_f32_16x16x32_bf16 v[124:127], a[252:255], a[200:203], v[124:127]\n\t"
"s_waitcnt lgkmcnt(5)\n\t"
"v_mfma_f32_16x16x32_bf16 v[124:127], a[220:223], a[196:199], v[124:127]\n\t"
"v_mfma_f32_16x16x32_bf16 v[124:127], a[216:219], a[192:195], v[124:127]\n\t"
"v_mfma_f32_16x16x32_bf16 v[128:131], a[216:219], a[196:199], v[128:131]\n\t"
"buffer_load_dwordx4 a[216:219], off, s[80:83], s20 offset:0 sc1\n\t"
"v_mfma_f32_16x16x32_bf16 v[128:131], a[220:223], a[200:203], v[128:131]\n\t"
"s_waitcnt lgkmcnt(2)\n\t"
"v_mfma_f32_16x16x32_bf16 v[128:131], a[252:255], a[204:207], v[128:131]\n\t"
"s_waitcnt lgkmcnt(1)\n\t"
"v_mfma_f32_4x4x4_16b_bf16 v[224:227], v[224:225], v[220:221], 0\n\t"
"s_waitcnt lgkmcnt(0)\n\t"
"v_mfma_f32_4x4x4_16b_bf16 v[228:231], v[228:229], v[220:221], 0\n\t"
"s_waitcnt vmcnt(10)\n\t"
"v_mfma_f32_4x4x4_16b_bf16 v[236:239], v[236:237], v[220:221], 0\n\t"
"v_mfma_f32_4x4x4_16b_bf16 v[232:235], v[232:233], v[220:221], 0\n\t"
"buffer_load_dwordx4 a[220:223], off, s[80:83], s20 offset:1024 sc1\n\t"
"v_mfma_f32_16x16x32_bf16 v[132:135], a[224:227], a[168:171], v[132:135]\n\t"
"buffer_load_dwordx4 a[252:255], off, s[80:83], s20 offset:2048 sc1\n\t"
"v_mfma_f32_16x16x32_bf16 v[132:135], a[228:231], a[176:179], v[132:135]\n\t"
"v_cvt_pk_bf16_f32 v224, v224, v225\n\t"
"v_cvt_pk_bf16_f32 v225, v226, v227\n\t"
"v_mfma_f32_16x16x32_bf16 v[132:135], v[96:99], a[172:175], v[132:135]\n\t"
"v_cvt_pk_bf16_f32 v226, v228, v229\n\t"
"v_cvt_pk_bf16_f32 v227, v230, v231\n\t"
"v_mfma_f32_16x16x32_bf16 v[136:139], a[224:227], a[172:175], v[136:139]\n\t"
"v_cvt_pk_bf16_f32 v236, v236, v237\n\t"
"v_cvt_pk_bf16_f32 v237, v238, v239\n\t"
"v_mfma_f32_16x16x32_bf16 v[136:139], v[96:99], a[176:179], v[136:139]\n\t"
"s_mov_b32 s20, 1548288\n\t"
"v_cvt_pk_bf16_f32 v238, v232, v233\n\t"
"v_cvt_pk_bf16_f32 v239, v234, v235\n\t"
"v_mfma_f32_16x16x32_bf16 v[136:139], a[228:231], a[180:183], v[136:139]\n\t"
"ds_write_b128 v245, v[224:227] offset:47104\n\t"
"v_mfma_f32_16x16x32_bf16 v[140:143], a[228:231], a[184:187], v[140:143]\n\t"
"v_mfma_f32_16x16x32_bf16 v[140:143], v[96:99], a[180:183], v[140:143]\n\t"
"v_mfma_f32_16x16x32_bf16 v[140:143], a[224:227], a[176:179], v[140:143]\n\t"
"ds_write_b128 v245, v[236:239] offset:53248\n\t"
"v_mfma_f32_16x16x32_bf16 v[144:147], a[224:227], a[180:183], v[144:147]\n\t"
"v_mfma_f32_16x16x32_bf16 v[144:147], v[96:99], a[184:187], v[144:147]\n\t"
"buffer_load_dwordx4 v[236:239], off, s[80:83], s20 offset:0 sc1\n\t"
"v_mfma_f32_16x16x32_bf16 v[144:147], a[228:231], a[188:191], v[144:147]\n\t"
"v_mfma_f32_16x16x32_bf16 v[148:151], a[228:231], a[192:195], v[148:151]\n\t"
"v_mfma_f32_16x16x32_bf16 v[148:151], v[96:99], a[188:191], v[148:151]\n\t"
"v_mfma_f32_16x16x32_bf16 v[148:151], a[224:227], a[184:187], v[148:151]\n\t"
"v_mfma_f32_16x16x32_bf16 v[152:155], a[224:227], a[188:191], v[152:155]\n\t"
"v_mfma_f32_16x16x32_bf16 v[152:155], v[96:99], a[192:195], v[152:155]\n\t"
"v_mfma_f32_16x16x32_bf16 v[152:155], a[228:231], a[196:199], v[152:155]\n\t"
"v_mfma_f32_16x16x32_bf16 v[156:159], a[228:231], a[200:203], v[156:159]\n\t"
"v_mfma_f32_16x16x32_bf16 v[156:159], v[96:99], a[196:199], v[156:159]\n\t"
"v_mfma_f32_16x16x32_bf16 v[156:159], a[224:227], a[192:195], v[156:159]\n\t"
"v_mfma_f32_16x16x32_bf16 v[160:163], a[224:227], a[196:199], v[160:163]\n\t"
"buffer_load_dwordx4 a[224:227], off, s[80:83], s20 offset:1024 sc1\n\t"
"v_mfma_f32_16x16x32_bf16 v[160:163], v[96:99], a[200:203], v[160:163]\n\t"
"s_waitcnt vmcnt(11)\n\t"
"v_mfma_f32_16x16x32_bf16 v[160:163], a[228:231], a[204:207], v[160:163]\n\t"
"v_mfma_f32_16x16x32_bf16 v[164:167], v[248:251], a[168:171], v[164:167]\n\t"
"buffer_load_dwordx4 v[96:99], off, s[80:83], s20 offset:2048 sc1\n\t"
"v_mfma_f32_16x16x32_bf16 v[164:167], a[208:211], a[176:179], v[164:167]\n\t"
"s_mov_b32 s20, 1560576\n\t"
"v_mfma_f32_16x16x32_bf16 v[164:167], v[240:243], a[172:175], v[164:167]\n\t"
"s_waitcnt lgkmcnt(0)\n\t"
"s_barrier\n\t"
"v_mfma_f32_16x16x32_bf16 v[168:171], v[248:251], a[172:175], v[168:171]\n\t"
"ds_read_b128 a[168:171], v222 offset:2560\n\t"
"v_mfma_f32_16x16x32_bf16 v[168:171], v[240:243], a[176:179], v[168:171]\n\t"
"buffer_load_dwordx4 a[228:231], off, s[80:83], s20 offset:0 sc1\n\t"
"v_mfma_f32_16x16x32_bf16 v[168:171], a[208:211], a[180:183], v[168:171]\n\t"
"ds_read_b128 a[172:175], v223 offset:8704\n\t"
"v_mfma_f32_16x16x32_bf16 v[172:175], a[208:211], a[184:187], v[172:175]\n\t"
"v_mfma_f32_16x16x32_bf16 v[172:175], v[240:243], a[180:183], v[172:175]\n\t"
"v_mfma_f32_16x16x32_bf16 v[172:175], v[248:251], a[176:179], v[172:175]\n\t"
"ds_read_b128 a[176:179], v223 offset:14848\n\t"
"v_mfma_f32_16x16x32_bf16 v[176:179], v[248:251], a[180:183], v[176:179]\n\t"
"ds_read_b128 a[180:183], v222 offset:22528\n\t"
"v_mfma_f32_16x16x32_bf16 v[176:179], v[240:243], a[184:187], v[176:179]\n\t"
"v_mfma_f32_16x16x32_bf16 v[176:179], a[208:211], a[188:191], v[176:179]\n\t"
"v_mfma_f32_16x16x32_bf16 v[180:183], a[208:211], a[192:195], v[180:183]\n\t"
"v_mfma_f32_16x16x32_bf16 v[180:183], v[240:243], a[188:191], v[180:183]\n\t"
"v_mfma_f32_16x16x32_bf16 v[180:183], v[248:251], a[184:187], v[180:183]\n\t"
"ds_read_b128 a[184:187], v223 offset:28672\n\t"
"v_mfma_f32_16x16x32_bf16 v[184:187], v[248:251], a[188:191], v[184:187]\n\t"
"ds_read_b128 a[188:191], v223 offset:34816\n\t"
"v_mfma_f32_16x16x32_bf16 v[184:187], v[240:243], a[192:195], v[184:187]\n\t"
"v_mfma_f32_16x16x32_bf16 v[184:187], a[208:211], a[196:199], v[184:187]\n\t"
"v_mfma_f32_16x16x32_bf16 v[188:191], a[208:211], a[200:203], v[188:191]\n\t"
"v_mfma_f32_16x16x32_bf16 v[188:191], v[240:243], a[196:199], v[188:191]\n\t"
"v_mfma_f32_16x16x32_bf16 v[188:191], v[248:251], a[192:195], v[188:191]\n\t"
"ds_read_b128 a[192:195], v222 offset:42496\n\t"
"v_mfma_f32_16x16x32_bf16 v[192:195], v[248:251], a[196:199], v[192:195]\n\t"
"buffer_load_dwordx4 v[248:251], off, s[80:83], s20 offset:1024 sc1\n\t"
"v_mfma_f32_16x16x32_bf16 v[192:195], v[240:243], a[200:203], v[192:195]\n\t"
"s_waitcnt vmcnt(11) lgkmcnt(4)\n\t"
"ds_read_b128 a[196:199], v223 offset:48640\n\t"
"v_mfma_f32_16x16x32_bf16 v[192:195], a[208:211], a[204:207], v[192:195]\n\t"
"ds_read_b128 a[200:203], v223 offset:54784\n\t"
"Lst12_%=:\n\t"
"v_mfma_f32_16x16x32_bf16 v[0:3], a[232:235], a[168:171], v[0:3]\n\t"
"buffer_load_dwordx4 v[240:243], off, s[80:83], s20 offset:2048 sc1\n\t"
"v_mfma_f32_16x16x32_bf16 v[0:3], a[240:243], a[176:179], v[0:3]\n\t"
"s_waitcnt lgkmcnt(5)\n\t"
"ds_read_b128 a[204:207], v222 offset:62464\n\t"
"s_mov_b32 s20, 1572864\n\t"
"v_mfma_f32_16x16x32_bf16 v[0:3], a[236:239], a[172:175], v[0:3]\n\t"
"v_mfma_f32_16x16x32_bf16 v[4:7], a[232:235], a[172:175], v[4:7]\n\t"
"buffer_load_dwordx4 a[208:211], off, s[80:83], s20 offset:0 sc1\n\t"
"v_mfma_f32_16x16x32_bf16 v[4:7], a[236:239], a[176:179], v[4:7]\n\t"
"s_waitcnt lgkmcnt(5)\n\t"
"v_mfma_f32_16x16x32_bf16 v[4:7], a[240:243], a[180:183], v[4:7]\n\t"
"v_mfma_f32_16x16x32_bf16 v[8:11], a[240:243], a[184:187], v[8:11]\n\t"
"s_waitcnt lgkmcnt(4)\n\t"
"v_mfma_f32_16x16x32_bf16 v[8:11], a[236:239], a[180:183], v[8:11]\n\t"
"v_mfma_f32_16x16x32_bf16 v[8:11], a[232:235], a[176:179], v[8:11]\n\t"
"v_mfma_f32_16x16x32_bf16 v[12:15], a[232:235], a[180:183], v[12:15]\n\t"
"s_waitcnt lgkmcnt(3)\n\t"
"v_mfma_f32_16x16x32_bf16 v[12:15], a[236:239], a[184:187], v[12:15]\n\t"
"v_mfma_f32_16x16x32_bf16 v[12:15], a[240:243], a[188:191], v[12:15]\n\t"
"v_mfma_f32_16x16x32_bf16 v[16:19], a[240:243], a[192:195], v[16:19]\n\t"
"s_waitcnt lgkmcnt(2)\n\t"
"v_mfma_f32_16x16x32_bf16 v[16:19], a[236:239], a[188:191], v[16:19]\n\t"
"v_mfma_f32_16x16x32_bf16 v[16:19], a[232:235], a[184:187], v[16:19]\n\t"
"v_mfma_f32_16x16x32_bf16 v[20:23], a[232:235], a[188:191], v[20:23]\n\t"
"s_waitcnt lgkmcnt(1)\n\t"
"v_mfma_f32_16x16x32_bf16 v[20:23], a[236:239], a[192:195], v[20:23]\n\t"
"v_mfma_f32_16x16x32_bf16 v[20:23], a[240:243], a[196:199], v[20:23]\n\t"
"v_mfma_f32_16x16x32_bf16 v[24:27], a[240:243], a[200:203], v[24:27]\n\t"
"s_waitcnt lgkmcnt(0)\n\t"
"s_add_u32 s21, s21, 6291456\n\t"
"v_add_u32 v254, 6291456, v254\n\t"
"v_mfma_f32_16x16x32_bf16 v[24:27], a[236:239], a[196:199], v[24:27]\n\t"
"v_mfma_f32_16x16x32_bf16 v[24:27], a[232:235], a[192:195], v[24:27]\n\t"
"v_mfma_f32_16x16x32_bf16 v[28:31], a[232:235], a[196:199], v[28:31]\n\t"
"buffer_load_dwordx4 a[232:235], off, s[80:83], s20 offset:1024 sc1\n\t"
"v_mfma_f32_16x16x32_bf16 v[28:31], a[236:239], a[200:203], v[28:31]\n\t"
"s_waitcnt vmcnt(11)\n\t"
"v_mfma_f32_16x16x32_bf16 v[28:31], a[240:243], a[204:207], v[28:31]\n\t"
"v_mfma_f32_16x16x32_bf16 v[32:35], a[244:247], a[168:171], v[32:35]\n\t"
"buffer_load_dwordx4 a[236:239], off, s[80:83], s20 offset:2048 sc1\n\t"
"v_mfma_f32_16x16x32_bf16 v[32:35], a[248:251], a[176:179], v[32:35]\n\t"
"s_mov_b32 s20, 1585152\n\t"
"v_mfma_f32_16x16x32_bf16 v[32:35], a[212:215], a[172:175], v[32:35]\n\t"
"v_mfma_f32_16x16x32_bf16 v[36:39], a[244:247], a[172:175], v[36:39]\n\t"
"buffer_load_dwordx4 a[240:243], off, s[80:83], s20 offset:0 sc1\n\t"
"v_mfma_f32_16x16x32_bf16 v[36:39], a[212:215], a[176:179], v[36:39]\n\t"
"v_mfma_f32_16x16x32_bf16 v[36:39], a[248:251], a[180:183], v[36:39]\n\t"
"v_mfma_f32_16x16x32_bf16 v[40:43], a[248:251], a[184:187], v[40:43]\n\t"
"v_mfma_f32_16x16x32_bf16 v[40:43], a[212:215], a[180:183], v[40:43]\n\t"
"v_mfma_f32_16x16x32_bf16 v[40:43], a[244:247], a[176:179], v[40:43]\n\t"
"v_mfma_f32_16x16x32_bf16 v[44:47], a[244:247], a[180:183], v[44:47]\n\t"
"v_mfma_f32_16x16x32_bf16 v[44:47], a[212:215], a[184:187], v[44:47]\n\t"
"v_mfma_f32_16x16x32_bf16 v[44:47], a[248:251], a[188:191], v[44:47]\n\t"
"v_mfma_f32_16x16x32_bf16 v[48:51], a[248:251], a[192:195], v[48:51]\n\t"
"v_mfma_f32_16x16x32_bf16 v[48:51], a[212:215], a[188:191], v[48:51]\n\t"
"v_mfma_f32_16x16x32_bf16 v[48:51], a[244:247], a[184:187], v[48:51]\n\t"
"v_mfma_f32_16x16x32_bf16 v[52:55], a[244:247], a[188:191], v[52:55]\n\t"
"v_mfma_f32_16x16x32_bf16 v[52:55], a[212:215], a[192:195], v[52:55]\n\t"
"v_mfma_f32_16x16x32_bf16 v[52:55], a[248:251], a[196:199], v[52:55]\n\t"
"v_mfma_f32_16x16x32_bf16 v[56:59], a[248:251], a[200:203], v[56:59]\n\t"
"v_mfma_f32_16x16x32_bf16 v[56:59], a[212:215], a[196:199], v[56:59]\n\t"
"v_mfma_f32_16x16x32_bf16 v[56:59], a[244:247], a[192:195], v[56:59]\n\t"
"v_mfma_f32_16x16x32_bf16 v[60:63], a[244:247], a[196:199], v[60:63]\n\t"
"buffer_load_dwordx4 a[244:247], off, s[80:83], s20 offset:1024 sc1\n\t"
"v_mfma_f32_16x16x32_bf16 v[60:63], a[212:215], a[200:203], v[60:63]\n\t"
"s_waitcnt vmcnt(11)\n\t"
"v_mfma_f32_16x16x32_bf16 v[60:63], a[248:251], a[204:207], v[60:63]\n\t"
"v_mfma_f32_16x16x32_bf16 v[64:67], a[216:219], a[168:171], v[64:67]\n\t"
"buffer_load_dwordx4 a[212:215], off, s[80:83], s20 offset:2048 sc1\n\t"
"v_mfma_f32_16x16x32_bf16 v[64:67], a[252:255], a[176:179], v[64:67]\n\t"
"s_mov_b32 s20, 1597440\n\t"
"ds_read_b128 a[168:171], v222 offset:2816\n\t"
"v_mfma_f32_16x16x32_bf16 v[64:67], a[220:223], a[172:175], v[64:67]\n\t"
"v_mfma_f32_16x16x32_bf16 v[68:71], a[216:219], a[172:175], v[68:71]\n\t"
"buffer_load_dwordx4 a[248:251], off, s[80:83], s20 offset:0 sc1\n\t"
"v_mfma_f32_16x16x32_bf16 v[68:71], a[220:223], a[176:179], v[68:71]\n\t"
"ds_read_b128 a[172:175], v223 offset:8960\n\t"
"v_mfma_f32_16x16x32_bf16 v[68:71], a[252:255], a[180:183], v[68:71]\n\t"
"v_mfma_f32_16x16x32_bf16 v[72:75], a[252:255], a[184:187], v[72:75]\n\t"
"v_mfma_f32_16x16x32_bf16 v[72:75], a[220:223], a[180:183], v[72:75]\n\t"
"v_mfma_f32_16x16x32_bf16 v[72:75], a[216:219], a[176:179], v[72:75]\n\t"
"ds_read_b128 a[176:179], v223 offset:15104\n\t"
"v_mfma_f32_16x16x32_bf16 v[76:79], a[216:219], a[180:183], v[76:79]\n\t"
"ds_read_b128 a[180:183], v222 offset:22784\n\t"
"v_mfma_f32_16x16x32_bf16 v[76:79], a[220:223], a[184:187], v[76:79]\n\t"
"v_mfma_f32_16x16x32_bf16 v[76:79], a[252:255], a[188:191], v[76:79]\n\t"
"v_mfma_f32_16x16x32_bf16 v[80:83], a[252:255], a[192:195], v[80:83]\n\t"
"v_mfma_f32_16x16x32_bf16 v[80:83], a[220:223], a[188:191], v[80:83]\n\t"
"v_mfma_f32_16x16x32_bf16 v[80:83], a[216:219], a[184:187], v[80:83]\n\t"
"ds_read_b128 a[184:187], v223 offset:28928\n\t"
"v_mfma_f32_16x16x32_bf16 v[84:87], a[216:219], a[188:191], v[84:87]\n\t"
"ds_read_b128 a[188:191], v223 offset:35072\n\t"
"v_mfma_f32_16x16x32_bf16 v[84:87], a[220:223], a[192:195], v[84:87]\n\t"
"v_mfma_f32_16x16x32_bf16 v[84:87], a[252:255], a[196:199], v[84:87]\n\t"
"v_mfma_f32_16x16x32_bf16 v[88:91], a[252:255], a[200:203], v[88:91]\n\t"
"v_mfma_f32_16x16x32_bf16 v[88:91], a[220:223], a[196:199], v[88:91]\n\t"
"v_mfma_f32_16x16x32_bf16 v[88:91], a[216:219], a[192:195], v[88:91]\n\t"
"ds_read_b128 a[192:195], v222 offset:42752\n\t"
"v_mfma_f32_16x16x32_bf16 v[92:95], a[216:219], a[196:199], v[92:95]\n\t"
"buffer_load_dwordx4 a[216:219], off, s[80:83], s20 offset:1024 sc1\n\t"
"v_mfma_f32_16x16x32_bf16 v[92:95], a[220:223], a[200:203], v[92:95]\n\t"
"s_waitcnt vmcnt(11) lgkmcnt(4)\n\t"
"ds_read_b128 a[196:199], v223 offset:48896\n\t"
"v_mfma_f32_16x16x32_bf16 v[92:95], a[252:255], a[204:207], v[92:95]\n\t"
"ds_read_b128 a[200:203], v223 offset:55040\n\t"
"v_mfma_f32_16x16x32_bf16 v[100:103], v[236:239], a[168:171], v[100:103]\n\t"
"s_waitcnt lgkmcnt(5)\n\t"
"ds_read_b128 a[204:207], v222 offset:62720\n\t"
"v_mfma_f32_16x16x32_bf16 v[100:103], v[96:99], a[176:179], v[100:103]\n\t"
"buffer_load_dwordx4 a[220:223], off, s[80:83], s20 offset:2048 sc1\n\t"
"v_mfma_f32_16x16x32_bf16 v[100:103], a[224:227], a[172:175], v[100:103]\n\t"
"s_mov_b32 s20, 1609728\n\t"
"s_add_u32 m0, s26, 512\n\t"
"v_mfma_f32_16x16x32_bf16 v[104:107], v[236:239], a[172:175], v[104:107]\n\t"
"buffer_load_dwordx4 a[252:255], off, s[80:83], s20 offset:0 sc1\n\t"
"v_mfma_f32_16x16x32_bf16 v[104:107], a[224:227], a[176:179], v[104:107]\n\t"
"s_waitcnt lgkmcnt(5)\n\t"
"v_mfma_f32_16x16x32_bf16 v[104:107], v[96:99], a[180:183], v[104:107]\n\t"
"s_waitcnt lgkmcnt(0)\n\t"
"s_barrier\n\t"
"v_mfma_f32_16x16x32_bf16 v[108:111], v[96:99], a[184:187], v[108:111]\n\t"
"v_fma_f32 v0, v100, s52, v0\n\t"
"v_mfma_f32_16x16x32_bf16 v[108:111], a[224:227], a[180:183], v[108:111]\n\t"
"v_fma_f32 v1, v101, s52, v1\n\t"
"v_fma_f32 v2, v102, s52, v2\n\t"
"v_mfma_f32_16x16x32_bf16 v[108:111], v[236:239], a[176:179], v[108:111]\n\t"
"s_mov_b64 exec, s[36:37]\n\t"
"buffer_load_dwordx4 v254, s[12:15], s27 offen offset:0 nt lds\n\t"
"s_mov_b64 exec, -1\n\t"
"s_add_u32 m0, s24, 22016\n\t"
"s_add_u32 s28, s21, 592896\n\t"
"v_mfma_f32_16x16x32_bf16 v[112:115], v[236:239], a[180:183], v[112:115]\n\t"
"buffer_load_dwordx4 v247, s[12:15], s28 offen offset:0 nt lds\n\t"
"v_mfma_f32_16x16x32_bf16 v[112:115], a[224:227], a[184:187], v[112:115]\n\t"
"s_add_u32 m0, s24, 28160\n\t"
"s_add_u32 s28, s21, 789504\n\t"
"v_fma_f32 v3, v103, s52, v3\n\t"
"v_mfma_f32_16x16x32_bf16 v[112:115], v[96:99], a[188:191], v[112:115]\n\t"
"buffer_load_dwordx4 v247, s[12:15], s28 offen offset:0 nt lds\n\t"
"s_add_u32 m0, s24, 34304\n\t"
"s_add_u32 s28, s21, 986112\n\t"
"v_mfma_f32_16x16x32_bf16 v[116:119], v[96:99], a[192:195], v[116:119]\n\t"
"buffer_load_dwordx4 v247, s[12:15], s28 offen offset:0 nt lds\n\t"
"v_mfma_f32_16x16x32_bf16 v[116:119], a[224:227], a[188:191], v[116:119]\n\t"
"s_add_u32 m0, s24, 41984\n\t"
"s_add_u32 s28, s21, 1182720\n\t"
"v_fma_f32 v100, v0, s54, -v100\n\t"
"v_mfma_f32_16x16x32_bf16 v[116:119], v[236:239], a[184:187], v[116:119]\n\t"
"v_fma_f32 v101, v1, s54, -v101\n\t"
"v_fma_f32 v102, v2, s54, -v102\n\t"
"v_mfma_f32_16x16x32_bf16 v[120:123], v[236:239], a[188:191], v[120:123]\n\t"
"v_fma_f32 v103, v3, s54, -v103\n\t"
"v_fma_f32 v4, v104, s52, v4\n\t"
"v_mfma_f32_16x16x32_bf16 v[120:123], a[224:227], a[192:195], v[120:123]\n\t"
"v_fma_f32 v5, v105, s52, v5\n\t"
"v_fma_f32 v6, v106, s52, v6\n\t"
"v_mfma_f32_16x16x32_bf16 v[120:123], v[96:99], a[196:199], v[120:123]\n\t"
"buffer_load_dwordx4 v247, s[12:15], s28 offen offset:0 nt lds\n\t"
"s_add_u32 m0, s24, 48128\n\t"
"s_add_u32 s28, s21, 1379328\n\t"
"v_mfma_f32_16x16x32_bf16 v[124:127], v[96:99], a[200:203], v[124:127]\n\t"
"buffer_load_dwordx4 v247, s[12:15], s28 offen offset:0 nt lds\n\t"
"v_mfma_f32_16x16x32_bf16 v[124:127], a[224:227], a[196:199], v[124:127]\n\t"
"v_fma_f32 v7, v107, s52, v7\n\t"
"v_fma_f32 v104, v4, s54, -v104\n\t"
"v_mfma_f32_16x16x32_bf16 v[124:127], v[236:239], a[192:195], v[124:127]\n\t"
"v_fma_f32 v105, v5, s54, -v105\n\t"
"v_fma_f32 v106, v6, s54, -v106\n\t"
"v_mfma_f32_16x16x32_bf16 v[128:131], v[236:239], a[196:199], v[128:131]\n\t"
"s_waitcnt vmcnt(16)\n\t"
"v_fma_f32 v107, v7, s54, -v107\n\t"
"v_fma_f32 v8, v108, s52, v8\n\t"
"v_mfma_f32_16x16x32_bf16 v[128:131], a[224:227], a[200:203], v[128:131]\n\t"
"v_fma_f32 v9, v109, s52, v9\n\t"
"v_fma_f32 v10, v110, s52, v10\n\t"
"v_mfma_f32_16x16x32_bf16 v[128:131], v[96:99], a[204:207], v[128:131]\n\t"
"v_fma_f32 v11, v111, s52, v11\n\t"
"v_fma_f32 v108, v8, s54, -v108\n\t"
"v_mfma_f32_16x16x32_bf16 v[132:135], a[228:231], a[168:171], v[132:135]\n\t"
"v_fma_f32 v109, v9, s54, -v109\n\t"
"v_fma_f32 v110, v10, s54, -v110\n\t"
"v_mfma_f32_16x16x32_bf16 v[132:135], v[240:243], a[176:179], v[132:135]\n\t"
"v_fma_f32 v111, v11, s54, -v111\n\t"
"v_fma_f32 v12, v112, s52, v12\n\t"
"v_mfma_f32_16x16x32_bf16 v[132:135], v[248:251], a[172:175], v[132:135]\n\t"
"v_fma_f32 v13, v113, s52, v13\n\t"
"v_fma_f32 v14, v114, s52, v14\n\t"
"v_mfma_f32_16x16x32_bf16 v[136:139], a[228:231], a[172:175], v[136:139]\n\t"
"v_fma_f32 v15, v115, s52, v15\n\t"
"v_fma_f32 v112, v12, s54, -v112\n\t"
"v_mfma_f32_16x16x32_bf16 v[136:139], v[248:251], a[176:179], v[136:139]\n\t"
"v_fma_f32 v113, v13, s54, -v113\n\t"
"v_fma_f32 v114, v14, s54, -v114\n\t"
"v_mfma_f32_16x16x32_bf16 v[136:139], v[240:243], a[180:183], v[136:139]\n\t"
"v_fma_f32 v115, v15, s54, -v115\n\t"
"v_fma_f32 v16, v116, s52, v16\n\t"
"v_mfma_f32_16x16x32_bf16 v[140:143], v[240:243], a[184:187], v[140:143]\n\t"
"v_fma_f32 v17, v117, s52, v17\n\t"
"v_fma_f32 v18, v118, s52, v18\n\t"
"v_mfma_f32_16x16x32_bf16 v[140:143], v[248:251], a[180:183], v[140:143]\n\t"
"v_fma_f32 v19, v119, s52, v19\n\t"
"v_fma_f32 v116, v16, s54, -v116\n\t"
"v_mfma_f32_16x16x32_bf16 v[140:143], a[228:231], a[176:179], v[140:143]\n\t"
"v_fma_f32 v117, v17, s54, -v117\n\t"
"v_fma_f32 v118, v18, s54, -v118\n\t"
"v_mfma_f32_16x16x32_bf16 v[144:147], a[228:231], a[180:183], v[144:147]\n\t"
"v_fma_f32 v119, v19, s54, -v119\n\t"
"v_fma_f32 v20, v120, s52, v20\n\t"
"v_mfma_f32_16x16x32_bf16 v[144:147], v[248:251], a[184:187], v[144:147]\n\t"
"v_fma_f32 v21, v121, s52, v21\n\t"
"v_fma_f32 v22, v122, s52, v22\n\t"
"v_mfma_f32_16x16x32_bf16 v[144:147], v[240:243], a[188:191], v[144:147]\n\t"
"v_fma_f32 v23, v123, s52, v23\n\t"
"v_fma_f32 v120, v20, s54, -v120\n\t"
"v_mfma_f32_16x16x32_bf16 v[148:151], v[240:243], a[192:195], v[148:151]\n\t"
"v_fma_f32 v121, v21, s54, -v121\n\t"
"v_fma_f32 v122, v22, s54, -v122\n\t"
"v_mfma_f32_16x16x32_bf16 v[148:151], v[248:251], a[188:191], v[148:151]\n\t"
"v_fma_f32 v123, v23, s54, -v123\n\t"
"v_fma_f32 v24, v124, s52, v24\n\t"
"v_mfma_f32_16x16x32_bf16 v[148:151], a[228:231], a[184:187], v[148:151]\n\t"
"v_fma_f32 v25, v125, s52, v25\n\t"
"v_fma_f32 v26, v126, s52, v26\n\t"
"v_mfma_f32_16x16x32_bf16 v[152:155], a[228:231], a[188:191], v[152:155]\n\t"
"v_fma_f32 v27, v127, s52, v27\n\t"
"v_fma_f32 v124, v24, s54, -v124\n\t"
"v_mfma_f32_16x16x32_bf16 v[152:155], v[248:251], a[192:195], v[152:155]\n\t"
"v_fma_f32 v125, v25, s54, -v125\n\t"
"v_fma_f32 v126, v26, s54, -v126\n\t"
"v_mfma_f32_16x16x32_bf16 v[152:155], v[240:243], a[196:199], v[152:155]\n\t"
"v_fma_f32 v127, v27, s54, -v127\n\t"
"v_fma_f32 v28, v128, s52, v28\n\t"
"v_mfma_f32_16x16x32_bf16 v[156:159], v[240:243], a[200:203], v[156:159]\n\t"
"v_fma_f32 v29, v129, s52, v29\n\t"
"v_fma_f32 v30, v130, s52, v30\n\t"
"v_mfma_f32_16x16x32_bf16 v[156:159], v[248:251], a[196:199], v[156:159]\n\t"
"v_fma_f32 v31, v131, s52, v31\n\t"
"v_fma_f32 v128, v28, s54, -v128\n\t"
"v_mfma_f32_16x16x32_bf16 v[156:159], a[228:231], a[192:195], v[156:159]\n\t"
"v_fma_f32 v129, v29, s54, -v129\n\t"
"v_fma_f32 v130, v30, s54, -v130\n\t"
"v_mfma_f32_16x16x32_bf16 v[160:163], a[228:231], a[196:199], v[160:163]\n\t"
"s_waitcnt vmcnt(13)\n\t"
"v_fma_f32 v131, v31, s54, -v131\n\t"
"v_fma_f32 v32, v132, s52, v32\n\t"
"v_mfma_f32_16x16x32_bf16 v[160:163], v[248:251], a[200:203], v[160:163]\n\t"
"v_fma_f32 v33, v133, s52, v33\n\t"
"v_fma_f32 v34, v134, s52, v34\n\t"
"v_mfma_f32_16x16x32_bf16 v[160:163], v[240:243], a[204:207], v[160:163]\n\t"
"buffer_load_dwordx4 v[96:99], off, s[80:83], s20 offset:1024 sc1\n\t"
"v_mfma_f32_16x16x32_bf16 v[164:167], a[208:211], a[168:171], v[164:167]\n\t"
"ds_read_b128 a[168:171], v222 offset:1024\n\t"
"v_fma_f32 v35, v135, s52, v35\n\t"
"v_mfma_f32_16x16x32_bf16 v[164:167], a[236:239], a[176:179], v[164:167]\n\t"
"v_fma_f32 v132, v32, s54, -v132\n\t"
"v_fma_f32 v133, v33, s54, -v133\n\t"
"v_mfma_f32_16x16x32_bf16 v[164:167], a[232:235], a[172:175], v[164:167]\n\t"
"v_fma_f32 v134, v34, s54, -v134\n\t"
"v_fma_f32 v135, v35, s54, -v135\n\t"
"v_mfma_f32_16x16x32_bf16 v[168:171], a[208:211], a[172:175], v[168:171]\n\t"
"ds_read_b128 a[172:175], v223 offset:7168\n\t"
"v_fma_f32 v36, v136, s52, v36\n\t"
"v_mfma_f32_16x16x32_bf16 v[168:171], a[232:235], a[176:179], v[168:171]\n\t"
"v_fma_f32 v37, v137, s52, v37\n\t"
"v_fma_f32 v38, v138, s52, v38\n\t"
"v_mfma_f32_16x16x32_bf16 v[168:171], a[236:239], a[180:183], v[168:171]\n\t"
"v_fma_f32 v39, v139, s52, v39\n\t"
"v_fma_f32 v136, v36, s54, -v136\n\t"
"v_mfma_f32_16x16x32_bf16 v[172:175], a[236:239], a[184:187], v[172:175]\n\t"
"v_fma_f32 v137, v37, s54, -v137\n\t"
"v_fma_f32 v138, v38, s54, -v138\n\t"
"v_mfma_f32_16x16x32_bf16 v[172:175], a[232:235], a[180:183], v[172:175]\n\t"
"v_fma_f32 v139, v39, s54, -v139\n\t"
"v_fma_f32 v40, v140, s52, v40\n\t"
"v_mfma_f32_16x16x32_bf16 v[172:175], a[208:211], a[176:179], v[172:175]\n\t"
"Lpg9_%=:\n\t"
"s_cbranch_vccnz Lvr0_%=\n\t"
"ds_read_b128 a[176:179], v223 offset:13312\n\t"
"v_fma_f32 v41, v141, s52, v41\n\t"
"v_mfma_f32_16x16x32_bf16 v[176:179], a[208:211], a[180:183], v[176:179]\n\t"
"ds_read_b128 a[180:183], v222 offset:20992\n\t"
"v_fma_f32 v42, v142, s52, v42\n\t"
"v_mfma_f32_16x16x32_bf16 v[176:179], a[232:235], a[184:187], v[176:179]\n\t"
"v_fma_f32 v43, v143, s52, v43\n\t"
"v_fma_f32 v140, v40, s54, -v140\n\t"
"v_mfma_f32_16x16x32_bf16 v[176:179], a[236:239], a[188:191], v[176:179]\n\t"
"v_fma_f32 v141, v41, s54, -v141\n\t"
"v_fma_f32 v142, v42, s54, -v142\n\t"
"v_mfma_f32_16x16x32_bf16 v[180:183], a[236:239], a[192:195], v[180:183]\n\t"
"v_fma_f32 v143, v43, s54, -v143\n\t"
"v_fma_f32 v44, v144, s52, v44\n\t"
"v_mfma_f32_16x16x32_bf16 v[180:183], a[232:235], a[188:191], v[180:183]\n\t"
"v_fma_f32 v45, v145, s52, v45\n\t"
"v_fma_f32 v46, v146, s52, v46\n\t"
"v_mfma_f32_16x16x32_bf16 v[180:183], a[208:211], a[184:187], v[180:183]\n\t"
"ds_read_b128 a[184:187], v223 offset:27136\n\t"
"v_fma_f32 v47, v147, s52, v47\n\t"
"v_mfma_f32_16x16x32_bf16 v[184:187], a[208:211], a[188:191], v[184:187]\n\t"
"ds_read_b128 a[188:191], v223 offset:33280\n\t"
"v_fma_f32 v144, v44, s54, -v144\n\t"
"v_mfma_f32_16x16x32_bf16 v[184:187], a[232:235], a[192:195], v[184:187]\n\t"
"v_fma_f32 v145, v45, s54, -v145\n\t"
"v_fma_f32 v146, v46, s54, -v146\n\t"
"v_mfma_f32_16x16x32_bf16 v[184:187], a[236:239], a[196:199], v[184:187]\n\t"
"v_fma_f32 v147, v47, s54, -v147\n\t"
"v_fma_f32 v48, v148, s52, v48\n\t"
"v_mfma_f32_16x16x32_bf16 v[188:191], a[236:239], a[200:203], v[188:191]\n\t"
"v_fma_f32 v49, v149, s52, v49\n\t"
"v_fma_f32 v50, v150, s52, v50\n\t"
"v_mfma_f32_16x16x32_bf16 v[188:191], a[232:235], a[196:199], v[188:191]\n\t"
"v_fma_f32 v51, v151, s52, v51\n\t"
"v_fma_f32 v148, v48, s54, -v148\n\t"
"v_mfma_f32_16x16x32_bf16 v[188:191], a[208:211], a[192:195], v[188:191]\n\t"
"ds_read_b128 a[192:195], v222 offset:40960\n\t"
"v_fma_f32 v149, v49, s54, -v149\n\t"
"v_mfma_f32_16x16x32_bf16 v[192:195], a[208:211], a[196:199], v[192:195]\n\t"
"s_waitcnt vmcnt(11) lgkmcnt(4)\n\t"
"ds_read_b128 a[196:199], v223 offset:47104\n\t"
"v_fma_f32 v150, v50, s54, -v150\n\t"
"v_mfma_f32_16x16x32_bf16 v[192:195], a[232:235], a[200:203], v[192:195]\n\t"
"ds_read_b128 a[200:203], v223 offset:53248\n\t"
"v_fma_f32 v151, v51, s54, -v151\n\t"
"v_mfma_f32_16x16x32_bf16 v[192:195], a[236:239], a[204:207], v[192:195]\n\t"
"ds_read_b128 a[204:207], v222 offset:60928\n\t"
"v_fma_f32 v52, v152, s52, v52\n\t"
"Lst13_%=:\n\t"
"v_mfma_f32_16x16x32_bf16 v[196:199], a[240:243], a[168:171], v[196:199]\n\t"
"s_waitcnt lgkmcnt(6)\n\t"
"v_fma_f32 v53, v153, s52, v53\n\t"
"v_fma_f32 v54, v154, s52, v54\n\t"
"v_mfma_f32_16x16x32_bf16 v[196:199], a[212:215], a[176:179], v[196:199]\n\t"
"v_fma_f32 v55, v155, s52, v55\n\t"
"v_fma_f32 v152, v52, s54, -v152\n\t"
"v_mfma_f32_16x16x32_bf16 v[196:199], a[244:247], a[172:175], v[196:199]\n\t"
"v_fma_f32 v153, v53, s54, -v153\n\t"
"v_fma_f32 v154, v54, s54, -v154\n\t"
"v_mfma_f32_16x16x32_bf16 v[204:207], a[240:243], a[172:175], v[204:207]\n\t"
"s_waitcnt lgkmcnt(5)\n\t"
"v_fma_f32 v155, v55, s54, -v155\n\t"
"v_fma_f32 v56, v156, s52, v56\n\t"
"v_mfma_f32_16x16x32_bf16 v[204:207], a[244:247], a[176:179], v[204:207]\n\t"
"v_fma_f32 v57, v157, s52, v57\n\t"
"v_fma_f32 v58, v158, s52, v58\n\t"
"v_mfma_f32_16x16x32_bf16 v[204:207], a[212:215], a[180:183], v[204:207]\n\t"
"v_fma_f32 v59, v159, s52, v59\n\t"
"v_fma_f32 v156, v56, s54, -v156\n\t"
"v_mfma_f32_16x16x32_bf16 v[212:215], a[212:215], a[184:187], v[212:215]\n\t"
"s_waitcnt lgkmcnt(4)\n\t"
"v_fma_f32 v157, v57, s54, -v157\n\t"
"v_fma_f32 v158, v58, s54, -v158\n\t"
"v_mfma_f32_16x16x32_bf16 v[212:215], a[244:247], a[180:183], v[212:215]\n\t"
"v_fma_f32 v159, v59, s54, -v159\n\t"
"v_fma_f32 v60, v160, s52, v60\n\t"
"v_mfma_f32_16x16x32_bf16 v[212:215], a[240:243], a[176:179], v[212:215]\n\t"
"v_fma_f32 v61, v161, s52, v61\n\t"
"v_fma_f32 v62, v162, s52, v62\n\t"
"v_mfma_f32_16x16x32_bf16 a[0:3], a[240:243], a[180:183], a[0:3]\n\t"
"s_waitcnt lgkmcnt(3)\n\t"
"v_fma_f32 v63, v163, s52, v63\n\t"
"v_fma_f32 v160, v60, s54, -v160\n\t"
"v_mfma_f32_16x16x32_bf16 a[0:3], a[244:247], a[184:187], a[0:3]\n\t"
"v_fma_f32 v161, v61, s54, -v161\n\t"
"v_fma_f32 v162, v62, s54, -v162\n\t"
"v_mfma_f32_16x16x32_bf16 a[0:3], a[212:215], a[188:191], a[0:3]\n\t"
"buffer_load_dwordx4 v[236:239], off, s[80:83], s20 offset:2048 sc1\n\t"
"s_mov_b32 s20, 1622016\n\t"
"v_mfma_f32_16x16x32_bf16 a[8:11], a[212:215], a[192:195], a[8:11]\n\t"
"buffer_load_dwordx4 v[248:251], off, s[80:83], s20 offset:0 sc1\n\t"
"v_mfma_f32_16x16x32_bf16 a[8:11], a[244:247], a[188:191], a[8:11]\n\t"
"s_waitcnt lgkmcnt(2)\n\t"
"v_fma_f32 v163, v63, s54, -v163\n\t"
"v_fma_f32 v64, v164, s52, v64\n\t"
"v_mfma_f32_16x16x32_bf16 a[8:11], a[240:243], a[184:187], a[8:11]\n\t"
"v_fma_f32 v65, v165, s52, v65\n\t"
"v_fma_f32 v66, v166, s52, v66\n\t"
"v_mfma_f32_16x16x32_bf16 a[16:19], a[240:243], a[188:191], a[16:19]\n\t"
"s_waitcnt lgkmcnt(1)\n\t"
"v_fma_f32 v67, v167, s52, v67\n\t"
"v_fma_f32 v164, v64, s54, -v164\n\t"
"v_mfma_f32_16x16x32_bf16 a[16:19], a[244:247], a[192:195], a[16:19]\n\t"
"v_fma_f32 v165, v65, s54, -v165\n\t"
"v_fma_f32 v166, v66, s54, -v166\n\t"
"v_mfma_f32_16x16x32_bf16 a[16:19], a[212:215], a[196:199], a[16:19]\n\t"
"buffer_load_dwordx4 v[240:243], off, s[80:83], s20 offset:1024 sc1\n\t"
"v_mfma_f32_16x16x32_bf16 a[24:27], a[212:215], a[200:203], a[24:27]\n\t"
"buffer_load_dwordx4 a[208:211], off, s[80:83], s20 offset:2048 sc1\n\t"
"v_mfma_f32_16x16x32_bf16 a[24:27], a[244:247], a[196:199], a[24:27]\n\t"
"s_waitcnt lgkmcnt(0)\n\t"
"v_fma_f32 v167, v67, s54, -v167\n\t"
"v_fma_f32 v68, v168, s52, v68\n\t"
"v_mfma_f32_16x16x32_bf16 a[24:27], a[240:243], a[192:195], a[24:27]\n\t"
"s_mov_b32 s20, 1634304\n\t"
"v_fma_f32 v69, v169, s52, v69\n\t"
"v_fma_f32 v70, v170, s52, v70\n\t"
"v_mfma_f32_16x16x32_bf16 a[32:35], a[240:243], a[196:199], a[32:35]\n\t"
"s_waitcnt vmcnt(12)\n\t"
"v_fma_f32 v71, v171, s52, v71\n\t"
"v_fma_f32 v168, v68, s54, -v168\n\t"
"v_mfma_f32_16x16x32_bf16 a[32:35], a[244:247], a[200:203], a[32:35]\n\t"
"v_fma_f32 v169, v69, s54, -v169\n\t"
"v_fma_f32 v170, v70, s54, -v170\n\t"
"v_mfma_f32_16x16x32_bf16 a[32:35], a[212:215], a[204:207], a[32:35]\n\t"
"v_fma_f32 v171, v71, s54, -v171\n\t"
"v_fma_f32 v72, v172, s52, v72\n\t"
"v_mfma_f32_16x16x32_bf16 a[40:43], a[248:251], a[168:171], a[40:43]\n\t"
"v_fma_f32 v73, v173, s52, v73\n\t"
"v_fma_f32 v74, v174, s52, v74\n\t"
"v_mfma_f32_16x16x32_bf16 a[40:43], a[220:223], a[176:179], a[40:43]\n\t"
"v_fma_f32 v75, v175, s52, v75\n\t"
"v_fma_f32 v172, v72, s54, -v172\n\t"
"v_mfma_f32_16x16x32_bf16 a[40:43], a[216:219], a[172:175], a[40:43]\n\t"
"buffer_load_dwordx4 a[224:227], off, s[80:83], s20 offset:0 sc1\n\t"
"v_mfma_f32_16x16x32_bf16 a[48:51], a[248:251], a[172:175], a[48:51]\n\t"
"buffer_load_dwordx4 a[212:215], off, s[80:83], s20 offset:1024 sc1\n\t"
"v_mfma_f32_16x16x32_bf16 a[48:51], a[216:219], a[176:179], a[48:51]\n\t"
"v_fma_f32 v173, v73, s54, -v173\n\t"
"v_fma_f32 v174, v74, s54, -v174\n\t"
"v_mfma_f32_16x16x32_bf16 a[48:51], a[220:223], a[180:183], a[48:51]\n\t"
"v_fma_f32 v175, v75, s54, -v175\n\t"
"v_fma_f32 v76, v176, s52, v76\n\t"
"v_mfma_f32_16x16x32_bf16 a[56:59], a[220:223], a[184:187], a[56:59]\n\t"
"v_fma_f32 v77, v177, s52, v77\n\t"
"v_fma_f32 v78, v178, s52, v78\n\t"
"v_mfma_f32_16x16x32_bf16 a[56:59], a[216:219], a[180:183], a[56:59]\n\t"
"v_fma_f32 v79, v179, s52, v79\n\t"
"v_fma_f32 v176, v76, s54, -v176\n\t"
"v_mfma_f32_16x16x32_bf16 a[56:59], a[248:251], a[176:179], a[56:59]\n\t"
"v_fma_f32 v177, v77, s54, -v177\n\t"
"v_fma_f32 v178, v78, s54, -v178\n\t"
"v_mfma_f32_16x16x32_bf16 a[64:67], a[248:251], a[180:183], a[64:67]\n\t"
"v_fma_f32 v179, v79, s54, -v179\n\t"
"v_fma_f32 v80, v180, s52, v80\n\t"
"v_mfma_f32_16x16x32_bf16 a[64:67], a[216:219], a[184:187], a[64:67]\n\t"
"v_fma_f32 v81, v181, s52, v81\n\t"
"v_fma_f32 v82, v182, s52, v82\n\t"
"v_mfma_f32_16x16x32_bf16 a[64:67], a[220:223], a[188:191], a[64:67]\n\t"
"buffer_load_dwordx4 a[228:231], off, s[80:83], s20 offset:2048 sc1\n\t"
"s_mov_b32 s20, 1646592\n\t"
"v_mfma_f32_16x16x32_bf16 a[72:75], a[220:223], a[192:195], a[72:75]\n\t"
"buffer_load_dwordx4 a[232:235], off, s[80:83], s20 offset:0 sc1\n\t"
"v_mfma_f32_16x16x32_bf16 a[72:75], a[216:219], a[188:191], a[72:75]\n\t"
"v_fma_f32 v83, v183, s52, v83\n\t"
"v_fma_f32 v180, v80, s54, -v180\n\t"
"v_mfma_f32_16x16x32_bf16 a[72:75], a[248:251], a[184:187], a[72:75]\n\t"
"v_fma_f32 v181, v81, s54, -v181\n\t"
"v_fma_f32 v182, v82, s54, -v182\n\t"
"v_mfma_f32_16x16x32_bf16 a[80:83], a[248:251], a[188:191], a[80:83]\n\t"
"v_fma_f32 v183, v83, s54, -v183\n\t"
"v_fma_f32 v84, v184, s52, v84\n\t"
"v_mfma_f32_16x16x32_bf16 a[80:83], a[216:219], a[192:195], a[80:83]\n\t"
"v_fma_f32 v85, v185, s52, v85\n\t"
"v_fma_f32 v86, v186, s52, v86\n\t"
"v_mfma_f32_16x16x32_bf16 a[80:83], a[220:223], a[196:199], a[80:83]\n\t"
"buffer_load_dwordx4 a[236:239], off, s[80:83], s20 offset:1024 sc1\n\t"
"v_mfma_f32_16x16x32_bf16 a[88:91], a[220:223], a[200:203], a[88:91]\n\t"
"buffer_load_dwordx4 a[240:243], off, s[80:83], s20 offset:2048 sc1\n\t"
"v_mfma_f32_16x16x32_bf16 a[88:91], a[216:219], a[196:199], a[88:91]\n\t"
"s_mov_b32 s20, 884736\n\t"
"v_fma_f32 v87, v187, s52, v87\n\t"
"v_fma_f32 v184, v84, s54, -v184\n\t"
"v_mfma_f32_16x16x32_bf16 a[88:91], a[248:251], a[192:195], a[88:91]\n\t"
"v_fma_f32 v185, v85, s54, -v185\n\t"
"v_fma_f32 v186, v86, s54, -v186\n\t"
"v_mfma_f32_16x16x32_bf16 a[96:99], a[248:251], a[196:199], a[96:99]\n\t"
"s_waitcnt vmcnt(9)\n\t"
"v_fma_f32 v187, v87, s54, -v187\n\t"
"v_fma_f32 v88, v188, s52, v88\n\t"
"v_mfma_f32_16x16x32_bf16 a[96:99], a[216:219], a[200:203], a[96:99]\n\t"
"v_fma_f32 v89, v189, s52, v89\n\t"
"v_fma_f32 v90, v190, s52, v90\n\t"
"v_mfma_f32_16x16x32_bf16 a[96:99], a[220:223], a[204:207], a[96:99]\n\t"
"v_fma_f32 v91, v191, s52, v91\n\t"
"v_fma_f32 v188, v88, s54, -v188\n\t"
"v_mfma_f32_16x16x32_bf16 a[104:107], a[252:255], a[168:171], a[104:107]\n\t"
"ds_read_b128 a[168:171], v222 offset:1280\n\t"
"v_fma_f32 v189, v89, s54, -v189\n\t"
"v_mfma_f32_16x16x32_bf16 a[104:107], v[236:239], a[176:179], a[104:107]\n\t"
"v_fma_f32 v190, v90, s54, -v190\n\t"
"v_fma_f32 v191, v91, s54, -v191\n\t"
"v_mfma_f32_16x16x32_bf16 a[104:107], v[96:99], a[172:175], a[104:107]\n\t"
"buffer_load_dwordx4 a[216:219], off, s[80:83], s20 offset:0 sc1\n\t"
"v_mfma_f32_16x16x32_bf16 a[112:115], a[252:255], a[172:175], a[112:115]\n\t"
"buffer_load_dwordx4 a[220:223], off, s[80:83], s20 offset:1024 sc1\n\t"
"v_mfma_f32_16x16x32_bf16 a[112:115], v[96:99], a[176:179], a[112:115]\n\t"
"buffer_load_dwordx4 a[244:247], off, s[80:83], s20 offset:2048 sc1\n\t"
"v_mfma_f32_16x16x32_bf16 a[112:115], v[236:239], a[180:183], a[112:115]\n\t"
"s_mov_b32 s20, 897024\n\t"
"ds_read_b128 a[172:175], v223 offset:7424\n\t"
"v_fma_f32 v92, v192, s52, v92\n\t"
"v_mfma_f32_16x16x32_bf16 a[120:123], v[236:239], a[184:187], a[120:123]\n\t"
"v_fma_f32 v93, v193, s52, v93\n\t"
"v_fma_f32 v94, v194, s52, v94\n\t"
"v_mfma_f32_16x16x32_bf16 a[120:123], v[96:99], a[180:183], a[120:123]\n\t"
"v_fma_f32 v95, v195, s52, v95\n\t"
"v_fma_f32 v192, v92, s54, -v192\n\t"
"v_mfma_f32_16x16x32_bf16 a[120:123], a[252:255], a[176:179], a[120:123]\n\t"
"ds_read_b128 a[176:179], v223 offset:13568\n\t"
"v_fma_f32 v193, v93, s54, -v193\n\t"
"v_mfma_f32_16x16x32_bf16 a[128:131], a[252:255], a[180:183], a[128:131]\n\t"
"ds_read_b128 a[180:183], v222 offset:21248\n\t"
"v_fma_f32 v194, v94, s54, -v194\n\t"
"v_mfma_f32_16x16x32_bf16 a[128:131], v[96:99], a[184:187], a[128:131]\n\t"
"v_fma_f32 v195, v95, s54, -v195\n\t"
"v_mfma_f32_16x16x32_bf16 a[128:131], v[236:239], a[188:191], a[128:131]\n\t"
"buffer_load_dwordx4 a[248:251], off, s[80:83], s20 offset:0 sc1\n\t"
"v_mfma_f32_16x16x32_bf16 a[136:139], v[236:239], a[192:195], a[136:139]\n\t"
"v_mfma_f32_16x16x32_bf16 a[136:139], v[96:99], a[188:191], a[136:139]\n\t"
"v_mfma_f32_16x16x32_bf16 a[136:139], a[252:255], a[184:187], a[136:139]\n\t"
"ds_read_b128 a[184:187], v223 offset:27392\n\t"
"v_mfma_f32_16x16x32_bf16 a[144:147], a[252:255], a[188:191], a[144:147]\n\t"
"ds_read_b128 a[188:191], v223 offset:33536\n\t"
"v_mfma_f32_16x16x32_bf16 a[144:147], v[96:99], a[192:195], a[144:147]\n\t"
"v_mfma_f32_16x16x32_bf16 a[144:147], v[236:239], a[196:199], a[144:147]\n\t"
"v_mfma_f32_16x16x32_bf16 a[152:155], v[236:239], a[200:203], a[152:155]\n\t"
"v_mfma_f32_16x16x32_bf16 a[152:155], v[96:99], a[196:199], a[152:155]\n\t"
"v_mfma_f32_16x16x32_bf16 a[152:155], a[252:255], a[192:195], a[152:155]\n\t"
"ds_read_b128 a[192:195], v222 offset:41216\n\t"
"v_mfma_f32_16x16x32_bf16 a[160:163], a[252:255], a[196:199], a[160:163]\n\t"
"buffer_load_dwordx4 a[252:255], off, s[80:83], s20 offset:1024 sc1\n\t"
"v_mfma_f32_16x16x32_bf16 a[160:163], v[96:99], a[200:203], a[160:163]\n\t"
"s_waitcnt vmcnt(11) lgkmcnt(4)\n\t"
"ds_read_b128 a[196:199], v223 offset:47360\n\t"
"v_mfma_f32_16x16x32_bf16 a[160:163], v[236:239], a[204:207], a[160:163]\n\t"
"ds_read_b128 a[200:203], v223 offset:53504\n\t"
"v_mfma_f32_16x16x32_bf16 v[200:203], v[248:251], a[168:171], v[200:203]\n\t"
"s_waitcnt lgkmcnt(5)\n\t"
"ds_read_b128 a[204:207], v222 offset:61184\n\t"
"v_mfma_f32_16x16x32_bf16 v[200:203], a[208:211], a[176:179], v[200:203]\n\t"
"v_mfma_f32_16x16x32_bf16 v[200:203], v[240:243], a[172:175], v[200:203]\n\t"
"v_mfma_f32_16x16x32_bf16 v[208:211], v[248:251], a[172:175], v[208:211]\n\t"
"s_waitcnt lgkmcnt(5)\n\t"
"v_mfma_f32_16x16x32_bf16 v[208:211], v[240:243], a[176:179], v[208:211]\n\t"
"v_mfma_f32_16x16x32_bf16 v[208:211], a[208:211], a[180:183], v[208:211]\n\t"
"s_waitcnt lgkmcnt(0)\n\t"
"s_barrier\n\t"
"v_mfma_f32_16x16x32_bf16 v[216:219], a[208:211], a[184:187], v[216:219]\n\t"
"v_mfma_f32_16x16x32_bf16 v[216:219], v[240:243], a[180:183], v[216:219]\n\t"
"v_mfma_f32_16x16x32_bf16 v[216:219], v[248:251], a[176:179], v[216:219]\n\t"
"v_pk_fma_f32 v[196:197], v[200:201], s[56:57], v[196:197]\n\t"
"v_pk_fma_f32 v[200:201], v[196:197], s[58:59], v[200:201] neg_lo:[0,0,1] neg_hi:[0,0,1]\n\t"
"v_pk_fma_f32 v[228:229], v[0:1], s[60:61], v[196:197]\n\t"
"v_pk_fma_f32 v[0:1], v[196:197], s[60:61], v[0:1]\n\t"
"v_pk_fma_f32 v[196:197], v[100:101], s[62:63], v[200:201]\n\t"
"v_pk_fma_f32 v[100:101], v[200:201], s[62:63], v[100:101]\n\t"
"v_pk_fma_f32 v[198:199], v[202:203], s[56:57], v[198:199]\n\t"
"v_pk_fma_f32 v[202:203], v[198:199], s[58:59], v[202:203] neg_lo:[0,0,1] neg_hi:[0,0,1]\n\t"
"v_pk_fma_f32 v[234:235], v[2:3], s[60:61], v[198:199]\n\t"
"v_pk_fma_f32 v[2:3], v[198:199], s[60:61], v[2:3]\n\t"
"v_pk_fma_f32 v[198:199], v[102:103], s[62:63], v[202:203]\n\t"
"v_pk_fma_f32 v[102:103], v[202:203], s[62:63], v[102:103]\n\t"
"v_mfma_f32_16x16x32_bf16 a[4:7], v[248:251], a[180:183], a[4:7]\n\t"
"v_cvt_pk_bf16_f32 v196, v196, v197\n\t"
"v_cvt_pk_bf16_f32 v200, v228, v229\n\t"
"v_mfma_f32_16x16x32_bf16 a[4:7], v[240:243], a[184:187], a[4:7]\n\t"
"v_cvt_pk_bf16_f32 v197, v198, v199\n\t"
"v_cvt_pk_bf16_f32 v201, v234, v235\n\t"
"v_mfma_f32_16x16x32_bf16 a[4:7], a[208:211], a[188:191], a[4:7]\n\t"
"v_pk_fma_f32 v[204:205], v[208:209], s[56:57], v[204:205]\n\t"
"v_pk_fma_f32 v[208:209], v[204:205], s[58:59], v[208:209] neg_lo:[0,0,1] neg_hi:[0,0,1]\n\t"
"v_pk_fma_f32 v[98:99], v[4:5], s[60:61], v[204:205]\n\t"
"v_pk_fma_f32 v[4:5], v[204:205], s[60:61], v[4:5]\n\t"
"v_pk_fma_f32 v[204:205], v[104:105], s[62:63], v[208:209]\n\t"
"v_pk_fma_f32 v[104:105], v[208:209], s[62:63], v[104:105]\n\t"
"v_mfma_f32_16x16x32_bf16 a[12:15], a[208:211], a[192:195], a[12:15]\n\t"
"v_cvt_pk_bf16_f32 v204, v204, v205\n\t"
"v_cvt_pk_bf16_f32 v208, v98, v99\n\t"
"v_mfma_f32_16x16x32_bf16 a[12:15], v[240:243], a[188:191], a[12:15]\n\t"
"v_mfma_f32_16x16x32_bf16 a[12:15], v[248:251], a[184:187], a[12:15]\n\t"
"v_pk_fma_f32 v[206:207], v[210:211], s[56:57], v[206:207]\n\t"
"v_pk_fma_f32 v[210:211], v[206:207], s[58:59], v[210:211] neg_lo:[0,0,1] neg_hi:[0,0,1]\n\t"
"v_pk_fma_f32 v[228:229], v[6:7], s[60:61], v[206:207]\n\t"
"v_pk_fma_f32 v[6:7], v[206:207], s[60:61], v[6:7]\n\t"
"v_pk_fma_f32 v[206:207], v[106:107], s[62:63], v[210:211]\n\t"
"v_pk_fma_f32 v[106:107], v[210:211], s[62:63], v[106:107]\n\t"
"v_mfma_f32_16x16x32_bf16 a[20:23], v[248:251], a[188:191], a[20:23]\n\t"
"v_cvt_pk_bf16_f32 v205, v206, v207\n\t"
"v_cvt_pk_bf16_f32 v209, v228, v229\n\t"
"v_mfma_f32_16x16x32_bf16 a[20:23], v[240:243], a[192:195], a[20:23]\n\t"
"v_accvgpr_read_b32 v224, a0\n\t"
"v_accvgpr_read_b32 v226, a4\n\t"
"v_mfma_f32_16x16x32_bf16 a[20:23], a[208:211], a[196:199], a[20:23]\n\t"
"v_accvgpr_read_b32 v225, a1\n\t"
"v_accvgpr_read_b32 v227, a5\n\t"
"v_pk_fma_f32 v[212:213], v[216:217], s[56:57], v[212:213]\n\t"
"v_pk_fma_f32 v[216:217], v[212:213], s[58:59], v[216:217] neg_lo:[0,0,1] neg_hi:[0,0,1]\n\t"
"v_pk_fma_f32 v[234:235], v[8:9], s[60:61], v[212:213]\n\t"
"v_pk_fma_f32 v[8:9], v[212:213], s[60:61], v[8:9]\n\t"
"v_pk_fma_f32 v[212:213], v[108:109], s[62:63], v[216:217]\n\t"
"v_pk_fma_f32 v[108:109], v[216:217], s[62:63], v[108:109]\n\t"
"v_mfma_f32_16x16x32_bf16 a[28:31], a[208:211], a[200:203], a[28:31]\n\t"
"v_cvt_pk_bf16_f32 v212, v212, v213\n\t"
"v_cvt_pk_bf16_f32 v216, v234, v235\n\t"
"v_mfma_f32_16x16x32_bf16 a[28:31], v[240:243], a[196:199], a[28:31]\n\t"
"v_accvgpr_read_b32 v230, a2\n\t"
"v_accvgpr_read_b32 v232, a6\n\t"
"v_mfma_f32_16x16x32_bf16 a[28:31], v[248:251], a[192:195], a[28:31]\n\t"
"v_accvgpr_read_b32 v231, a3\n\t"
"v_accvgpr_read_b32 v233, a7\n\t"
"v_pk_fma_f32 v[214:215], v[218:219], s[56:57], v[214:215]\n\t"
"v_pk_fma_f32 v[218:219], v[214:215], s[58:59], v[218:219] neg_lo:[0,0,1] neg_hi:[0,0,1]\n\t"
"v_pk_fma_f32 v[98:99], v[10:11], s[60:61], v[214:215]\n\t"
"v_pk_fma_f32 v[10:11], v[214:215], s[60:61], v[10:11]\n\t"
"v_pk_fma_f32 v[214:215], v[110:111], s[62:63], v[218:219]\n\t"
"v_pk_fma_f32 v[110:111], v[218:219], s[62:63], v[110:111]\n\t"
"v_mfma_f32_16x16x32_bf16 a[36:39], v[248:251], a[196:199], a[36:39]\n\t"
"s_waitcnt vmcnt(8)\n\t"
"v_cvt_pk_bf16_f32 v213, v214, v215\n\t"
"v_cvt_pk_bf16_f32 v217, v98, v99\n\t"
"v_mfma_f32_16x16x32_bf16 a[36:39], v[240:243], a[200:203], a[36:39]\n\t"
"v_accvgpr_read_b32 v236, a8\n\t"
"v_accvgpr_read_b32 v238, a12\n\t"
"v_mfma_f32_16x16x32_bf16 a[36:39], a[208:211], a[204:207], a[36:39]\n\t"
"buffer_load_dwordx4 v[240:243], off, s[80:83], s20 offset:2048 sc1\n\t"
"v_accvgpr_read_b32 v237, a9\n\t"
"v_accvgpr_read_b32 v239, a13\n\t"
"v_pk_fma_f32 v[224:225], v[226:227], s[56:57], v[224:225]\n\t"
"v_pk_fma_f32 v[226:227], v[224:225], s[58:59], v[226:227] neg_lo:[0,0,1] neg_hi:[0,0,1]\n\t"
"v_pk_fma_f32 v[228:229], v[12:13], s[60:61], v[224:225]\n\t"
"v_pk_fma_f32 v[12:13], v[224:225], s[60:61], v[12:13]\n\t"
"v_pk_fma_f32 v[224:225], v[112:113], s[62:63], v[226:227]\n\t"
"v_pk_fma_f32 v[112:113], v[226:227], s[62:63], v[112:113]\n\t"
"v_mfma_f32_16x16x32_bf16 a[44:47], a[224:227], a[168:171], a[44:47]\n\t"
"s_mov_b32 s20, 909312\n\t"
"v_cvt_pk_bf16_f32 v224, v224, v225\n\t"
"v_cvt_pk_bf16_f32 v228, v228, v229\n\t"
"v_mfma_f32_16x16x32_bf16 a[44:47], a[228:231], a[176:179], a[44:47]\n\t"
"v_accvgpr_write_b32 a0, v224\n\t"
"v_accvgpr_write_b32 a4, v228\n\t"
"v_mfma_f32_16x16x32_bf16 a[44:47], a[212:215], a[172:175], a[44:47]\n\t"
"buffer_load_dwordx4 a[208:211], off, s[80:83], s20 offset:0 sc1\n\t"
"v_accvgpr_read_b32 v224, a10\n\t"
"v_accvgpr_read_b32 v226, a14\n\t"
"v_pk_fma_f32 v[230:231], v[232:233], s[56:57], v[230:231]\n\t"
"v_pk_fma_f32 v[232:233], v[230:231], s[58:59], v[232:233] neg_lo:[0,0,1] neg_hi:[0,0,1]\n\t"
"v_pk_fma_f32 v[234:235], v[14:15], s[60:61], v[230:231]\n\t"
"v_pk_fma_f32 v[14:15], v[230:231], s[60:61], v[14:15]\n\t"
"v_pk_fma_f32 v[230:231], v[114:115], s[62:63], v[232:233]\n\t"
"v_pk_fma_f32 v[114:115], v[232:233], s[62:63], v[114:115]\n\t"
"v_mfma_f32_16x16x32_bf16 a[52:55], a[224:227], a[172:175], a[52:55]\n\t"
"v_cvt_pk_bf16_f32 v230, v230, v231\n\t"
"v_cvt_pk_bf16_f32 v234, v234, v235\n\t"
"v_mfma_f32_16x16x32_bf16 a[52:55], a[212:215], a[176:179], a[52:55]\n\t"
"v_accvgpr_write_b32 a1, v230\n\t"
"v_accvgpr_write_b32 a5, v234\n\t"
"v_mfma_f32_16x16x32_bf16 a[52:55], a[228:231], a[180:183], a[52:55]\n\t"
"v_accvgpr_read_b32 v225, a11\n\t"
"v_accvgpr_read_b32 v227, a15\n\t"
"v_accvgpr_read_b32 v230, a16\n\t"
"v_accvgpr_read_b32 v232, a20\n\t"
"v_accvgpr_read_b32 v231, a17\n\t"
"v_pk_fma_f32 v[236:237], v[238:239], s[56:57], v[236:237]\n\t"
"v_pk_fma_f32 v[238:239], v[236:237], s[58:59], v[238:239] neg_lo:[0,0,1] neg_hi:[0,0,1]\n\t"
"v_pk_fma_f32 v[98:99], v[16:17], s[60:61], v[236:237]\n\t"
"v_pk_fma_f32 v[16:17], v[236:237], s[60:61], v[16:17]\n\t"
"v_pk_fma_f32 v[236:237], v[116:117], s[62:63], v[238:239]\n\t"
"v_pk_fma_f32 v[116:117], v[238:239], s[62:63], v[116:117]\n\t"
"v_mfma_f32_16x16x32_bf16 a[60:63], a[228:231], a[184:187], a[60:63]\n\t"
"v_accvgpr_read_b32 v233, a21\n\t"
"v_cvt_pk_bf16_f32 v236, v236, v237\n\t"
"v_mfma_f32_16x16x32_bf16 a[60:63], a[212:215], a[180:183], a[60:63]\n\t"
"v_cvt_pk_bf16_f32 v98, v98, v99\n\t"
"v_accvgpr_write_b32 a8, v236\n\t"
"v_mfma_f32_16x16x32_bf16 a[60:63], a[224:227], a[176:179], a[60:63]\n\t"
"v_accvgpr_write_b32 a12, v98\n\t"
"v_accvgpr_read_b32 v236, a18\n\t"
"v_accvgpr_read_b32 v238, a22\n\t"
"v_accvgpr_read_b32 v237, a19\n\t"
"v_accvgpr_read_b32 v239, a23\n\t"
"v_pk_fma_f32 v[224:225], v[226:227], s[56:57], v[224:225]\n\t"
"v_pk_fma_f32 v[226:227], v[224:225], s[58:59], v[226:227] neg_lo:[0,0,1] neg_hi:[0,0,1]\n\t"
"v_pk_fma_f32 v[228:229], v[18:19], s[60:61], v[224:225]\n\t"
"v_pk_fma_f32 v[18:19], v[224:225], s[60:61], v[18:19]\n\t"
"v_pk_fma_f32 v[224:225], v[118:119], s[62:63], v[226:227]\n\t"
"v_pk_fma_f32 v[118:119], v[226:227], s[62:63], v[118:119]\n\t"
"v_pk_fma_f32 v[230:231], v[232:233], s[56:57], v[230:231]\n\t"
"v_pk_fma_f32 v[232:233], v[230:231], s[58:59], v[232:233] neg_lo:[0,0,1] neg_hi:[0,0,1]\n\t"
"v_pk_fma_f32 v[234:235], v[20:21], s[60:61], v[230:231]\n\t"
"v_pk_fma_f32 v[20:21], v[230:231], s[60:61], v[20:21]\n\t"
"v_pk_fma_f32 v[230:231], v[120:121], s[62:63], v[232:233]\n\t"
"v_pk_fma_f32 v[120:121], v[232:233], s[62:63], v[120:121]\n\t"
"v_mfma_f32_16x16x32_bf16 a[68:71], a[224:227], a[180:183], a[68:71]\n\t"
"v_cvt_pk_bf16_f32 v224, v224, v225\n\t"
"v_cvt_pk_bf16_f32 v228, v228, v229\n\t"
"v_mfma_f32_16x16x32_bf16 a[68:71], a[212:215], a[184:187], a[68:71]\n\t"
"v_cvt_pk_bf16_f32 v230, v230, v231\n\t"
"v_cvt_pk_bf16_f32 v234, v234, v235\n\t"
"v_mfma_f32_16x16x32_bf16 a[68:71], a[228:231], a[188:191], a[68:71]\n\t"
"v_accvgpr_write_b32 a9, v224\n\t"
"v_accvgpr_write_b32 a13, v228\n\t"
"v_accvgpr_write_b32 a16, v230\n\t"
"v_accvgpr_write_b32 a20, v234\n\t"
"v_accvgpr_read_b32 v224, a24\n\t"
"v_pk_fma_f32 v[236:237], v[238:239], s[56:57], v[236:237]\n\t"
"v_pk_fma_f32 v[238:239], v[236:237], s[58:59], v[238:239] neg_lo:[0,0,1] neg_hi:[0,0,1]\n\t"
"v_pk_fma_f32 v[98:99], v[22:23], s[60:61], v[236:237]\n\t"
"v_pk_fma_f32 v[22:23], v[236:237], s[60:61], v[22:23]\n\t"
"v_pk_fma_f32 v[236:237], v[122:123], s[62:63], v[238:239]\n\t"
"v_pk_fma_f32 v[122:123], v[238:239], s[62:63], v[122:123]\n\t"
"v_mfma_f32_16x16x32_bf16 a[76:79], a[228:231], a[192:195], a[76:79]\n\t"
"v_accvgpr_read_b32 v226, a28\n\t"
"v_accvgpr_read_b32 v225, a25\n\t"
"v_mfma_f32_16x16x32_bf16 a[76:79], a[212:215], a[188:191], a[76:79]\n\t"
"v_accvgpr_read_b32 v227, a29\n\t"
"v_cvt_pk_bf16_f32 v236, v236, v237\n\t"
"v_mfma_f32_16x16x32_bf16 a[76:79], a[224:227], a[184:187], a[76:79]\n\t"
"v_cvt_pk_bf16_f32 v98, v98, v99\n\t"
"v_accvgpr_write_b32 a17, v236\n\t"
"v_accvgpr_write_b32 a21, v98\n\t"
"v_accvgpr_read_b32 v230, a26\n\t"
"v_accvgpr_read_b32 v232, a30\n\t"
"v_pk_fma_f32 v[224:225], v[226:227], s[56:57], v[224:225]\n\t"
"v_pk_fma_f32 v[226:227], v[224:225], s[58:59], v[226:227] neg_lo:[0,0,1] neg_hi:[0,0,1]\n\t"
"v_pk_fma_f32 v[228:229], v[24:25], s[60:61], v[224:225]\n\t"
"v_pk_fma_f32 v[24:25], v[224:225], s[60:61], v[24:25]\n\t"
"v_pk_fma_f32 v[224:225], v[124:125], s[62:63], v[226:227]\n\t"
"v_pk_fma_f32 v[124:125], v[226:227], s[62:63], v[124:125]\n\t"
"v_mfma_f32_16x16x32_bf16 a[84:87], a[224:227], a[188:191], a[84:87]\n\t"
"v_accvgpr_read_b32 v231, a27\n\t"
"v_accvgpr_read_b32 v233, a31\n\t"
"v_mfma_f32_16x16x32_bf16 a[84:87], a[212:215], a[192:195], a[84:87]\n\t"
"v_cvt_pk_bf16_f32 v224, v224, v225\n\t"
"v_cvt_pk_bf16_f32 v228, v228, v229\n\t"
"v_mfma_f32_16x16x32_bf16 a[84:87], a[228:231], a[196:199], a[84:87]\n\t"
"v_accvgpr_write_b32 a24, v224\n\t"
"v_accvgpr_write_b32 a28, v228\n\t"
"v_accvgpr_read_b32 v236, a32\n\t"
"v_accvgpr_read_b32 v238, a36\n\t"
"v_accvgpr_read_b32 v237, a33\n\t"
"v_pk_fma_f32 v[230:231], v[232:233], s[56:57], v[230:231]\n\t"
"v_pk_fma_f32 v[232:233], v[230:231], s[58:59], v[232:233] neg_lo:[0,0,1] neg_hi:[0,0,1]\n\t"
"v_pk_fma_f32 v[234:235], v[26:27], s[60:61], v[230:231]\n\t"
"v_pk_fma_f32 v[26:27], v[230:231], s[60:61], v[26:27]\n\t"
"v_pk_fma_f32 v[230:231], v[126:127], s[62:63], v[232:233]\n\t"
"v_pk_fma_f32 v[126:127], v[232:233], s[62:63], v[126:127]\n\t"
"v_mfma_f32_16x16x32_bf16 a[92:95], a[228:231], a[200:203], a[92:95]\n\t"
"v_accvgpr_read_b32 v239, a37\n\t"
"v_cvt_pk_bf16_f32 v230, v230, v231\n\t"
"v_mfma_f32_16x16x32_bf16 a[92:95], a[212:215], a[196:199], a[92:95]\n\t"
"v_cvt_pk_bf16_f32 v234, v234, v235\n\t"
"v_accvgpr_write_b32 a25, v230\n\t"
"v_mfma_f32_16x16x32_bf16 a[92:95], a[224:227], a[192:195], a[92:95]\n\t"
"v_accvgpr_write_b32 a29, v234\n\t"
"v_accvgpr_read_b32 v224, a34\n\t"
"v_accvgpr_read_b32 v226, a38\n\t"
"v_accvgpr_read_b32 v225, a35\n\t"
"v_accvgpr_read_b32 v227, a39\n\t"
"v_pk_fma_f32 v[236:237], v[238:239], s[56:57], v[236:237]\n\t"
"v_pk_fma_f32 v[238:239], v[236:237], s[58:59], v[238:239] neg_lo:[0,0,1] neg_hi:[0,0,1]\n\t"
"v_pk_fma_f32 v[98:99], v[28:29], s[60:61], v[236:237]\n\t"
"v_pk_fma_f32 v[28:29], v[236:237], s[60:61], v[28:29]\n\t"
"v_pk_fma_f32 v[236:237], v[128:129], s[62:63], v[238:239]\n\t"
"v_pk_fma_f32 v[128:129], v[238:239], s[62:63], v[128:129]\n\t"
"v_mfma_f32_16x16x32_bf16 a[100:103], a[224:227], a[196:199], a[100:103]\n\t"
"s_waitcnt vmcnt(7)\n\t"
"v_cvt_pk_bf16_f32 v236, v236, v237\n\t"
"v_cvt_pk_bf16_f32 v98, v98, v99\n\t"
"v_mfma_f32_16x16x32_bf16 a[100:103], a[212:215], a[200:203], a[100:103]\n\t"
"v_accvgpr_write_b32 a32, v236\n\t"
"v_accvgpr_write_b32 a36, v98\n\t"
"v_mfma_f32_16x16x32_bf16 a[100:103], a[228:231], a[204:207], a[100:103]\n\t"
"buffer_load_dwordx4 a[224:227], off, s[80:83], s20 offset:1024 sc1\n\t"
"v_accvgpr_read_b32 v230, a40\n\t"
"v_accvgpr_read_b32 v232, a44\n\t"
"v_pk_fma_f32 v[224:225], v[226:227], s[56:57], v[224:225]\n\t"
"v_pk_fma_f32 v[226:227], v[224:225], s[58:59], v[226:227] neg_lo:[0,0,1] neg_hi:[0,0,1]\n\t"
"v_pk_fma_f32 v[228:229], v[30:31], s[60:61], v[224:225]\n\t"
"v_pk_fma_f32 v[30:31], v[224:225], s[60:61], v[30:31]\n\t"
"v_pk_fma_f32 v[224:225], v[130:131], s[62:63], v[226:227]\n\t"
"v_pk_fma_f32 v[130:131], v[226:227], s[62:63], v[130:131]\n\t"
"v_mfma_f32_16x16x32_bf16 a[108:111], a[232:235], a[168:171], a[108:111]\n\t"
"ds_read_b128 a[168:171], v244 offset:0\n\t"
"v_accvgpr_read_b32 v231, a41\n\t"
"v_mfma_f32_16x16x32_bf16 a[108:111], a[240:243], a[176:179], a[108:111]\n\t"
"v_accvgpr_read_b32 v233, a45\n\t"
"v_cvt_pk_bf16_f32 v224, v224, v225\n\t"
"v_mfma_f32_16x16x32_bf16 a[108:111], a[236:239], a[172:175], a[108:111]\n\t"
"buffer_load_dwordx4 a[212:215], off, s[80:83], s20 offset:2048 sc1\n\t"
"v_cvt_pk_bf16_f32 v228, v228, v229\n\t"
"v_accvgpr_write_b32 a33, v224\n\t"
"s_mov_b32 s20, 1069056\n\t"
"v_pk_fma_f32 v[230:231], v[232:233], s[56:57], v[230:231]\n\t"
"v_pk_fma_f32 v[232:233], v[230:231], s[58:59], v[232:233] neg_lo:[0,0,1] neg_hi:[0,0,1]\n\t"
"v_pk_fma_f32 v[234:235], v[32:33], s[60:61], v[230:231]\n\t"
"v_pk_fma_f32 v[32:33], v[230:231], s[60:61], v[32:33]\n\t"
"v_pk_fma_f32 v[230:231], v[132:133], s[62:63], v[232:233]\n\t"
"v_pk_fma_f32 v[132:133], v[232:233], s[62:63], v[132:133]\n\t"
"v_mfma_f32_16x16x32_bf16 a[116:119], a[232:235], a[172:175], a[116:119]\n\t"
"ds_read_b128 a[172:175], v244 offset:512\n\t"
"v_accvgpr_write_b32 a37, v228\n\t"
"v_mfma_f32_16x16x32_bf16 a[116:119], a[236:239], a[176:179], a[116:119]\n\t"
"v_accvgpr_read_b32 v236, a42\n\t"
"v_accvgpr_read_b32 v238, a46\n\t"
"v_mfma_f32_16x16x32_bf16 a[116:119], a[240:243], a[180:183], a[116:119]\n\t"
"buffer_load_dwordx4 a[228:231], off, s[80:83], s20 offset:0 sc1\n\t"
"v_accvgpr_read_b32 v237, a43\n\t"
"v_accvgpr_read_b32 v239, a47\n\t"
"Lpg10_%=:\n\t"
"s_cbranch_vccnz Lvr0_%=\n\t"
"v_pk_fma_f32 v[236:237], v[238:239], s[56:57], v[236:237]\n\t"
"v_pk_fma_f32 v[238:239], v[236:237], s[58:59], v[238:239] neg_lo:[0,0,1] neg_hi:[0,0,1]\n\t"
"v_pk_fma_f32 v[98:99], v[34:35], s[60:61], v[236:237]\n\t"
"v_pk_fma_f32 v[34:35], v[236:237], s[60:61], v[34:35]\n\t"
"v_pk_fma_f32 v[236:237], v[134:135], s[62:63], v[238:239]\n\t"
"v_pk_fma_f32 v[134:135], v[238:239], s[62:63], v[134:135]\n\t"
"v_mfma_f32_16x16x32_bf16 a[124:127], a[240:243], a[184:187], a[124:127]\n\t"
"v_cvt_pk_bf16_f32 v198, v230, v231\n\t"
"v_cvt_pk_bf16_f32 v202, v234, v235\n\t"
"v_mfma_f32_16x16x32_bf16 a[124:127], a[236:239], a[180:183], a[124:127]\n\t"
"v_accvgpr_read_b32 v224, a48\n\t"
"v_accvgpr_read_b32 v226, a52\n\t"
"v_mfma_f32_16x16x32_bf16 a[124:127], a[232:235], a[176:179], a[124:127]\n\t"
"global_load_dwordx4 v[248:251], v252, s[48:49] offset:0\n\t"
"ds_read_b128 a[176:179], v244 offset:1024\n\t"
"v_accvgpr_read_b32 v225, a49\n\t"
"v_accvgpr_read_b32 v227, a53\n\t"
"v_accvgpr_read_b32 v230, a50\n\t"
"v_accvgpr_read_b32 v232, a54\n\t"
"v_accvgpr_read_b32 v231, a51\n\t"
"v_accvgpr_read_b32 v233, a55\n\t"
"v_pk_fma_f32 v[224:225], v[226:227], s[56:57], v[224:225]\n\t"
"v_pk_fma_f32 v[226:227], v[224:225], s[58:59], v[226:227] neg_lo:[0,0,1] neg_hi:[0,0,1]\n\t"
"v_pk_fma_f32 v[228:229], v[36:37], s[60:61], v[224:225]\n\t"
"v_pk_fma_f32 v[36:37], v[224:225], s[60:61], v[36:37]\n\t"
"v_pk_fma_f32 v[224:225], v[136:137], s[62:63], v[226:227]\n\t"
"v_pk_fma_f32 v[136:137], v[226:227], s[62:63], v[136:137]\n\t"
"v_pk_fma_f32 v[230:231], v[232:233], s[56:57], v[230:231]\n\t"
"v_pk_fma_f32 v[232:233], v[230:231], s[58:59], v[232:233] neg_lo:[0,0,1] neg_hi:[0,0,1]\n\t"
"v_pk_fma_f32 v[234:235], v[38:39], s[60:61], v[230:231]\n\t"
"v_pk_fma_f32 v[38:39], v[230:231], s[60:61], v[38:39]\n\t"
"v_pk_fma_f32 v[230:231], v[138:139], s[62:63], v[232:233]\n\t"
"v_pk_fma_f32 v[138:139], v[232:233], s[62:63], v[138:139]\n\t"
"v_mfma_f32_16x16x32_bf16 a[132:135], a[232:235], a[180:183], a[132:135]\n\t"
"ds_read_b128 a[180:183], v244 offset:1536\n\t"
"v_cvt_pk_bf16_f32 v199, v236, v237\n\t"
"v_mfma_f32_16x16x32_bf16 a[132:135], a[236:239], a[184:187], a[132:135]\n\t"
"v_cvt_pk_bf16_f32 v203, v98, v99\n\t"
"v_accvgpr_read_b32 v236, a56\n\t"
"v_mfma_f32_16x16x32_bf16 a[132:135], a[240:243], a[188:191], a[132:135]\n\t"
"buffer_load_dwordx4 a[40:43], off, s[80:83], s20 offset:1024 sc1\n\t"
"v_accvgpr_read_b32 v238, a60\n\t"
"v_accvgpr_read_b32 v237, a57\n\t"
"v_accvgpr_read_b32 v239, a61\n\t"
"v_pk_fma_f32 v[236:237], v[238:239], s[56:57], v[236:237]\n\t"
"v_pk_fma_f32 v[238:239], v[236:237], s[58:59], v[238:239] neg_lo:[0,0,1] neg_hi:[0,0,1]\n\t"
"v_pk_fma_f32 v[98:99], v[40:41], s[60:61], v[236:237]\n\t"
"v_pk_fma_f32 v[40:41], v[236:237], s[60:61], v[40:41]\n\t"
"v_pk_fma_f32 v[236:237], v[140:141], s[62:63], v[238:239]\n\t"
"v_pk_fma_f32 v[140:141], v[238:239], s[62:63], v[140:141]\n\t"
"v_mfma_f32_16x16x32_bf16 a[140:143], a[240:243], a[192:195], a[140:143]\n\t"
"v_cvt_pk_bf16_f32 v206, v224, v225\n\t"
"v_cvt_pk_bf16_f32 v210, v228, v229\n\t"
"v_mfma_f32_16x16x32_bf16 a[140:143], a[236:239], a[188:191], a[140:143]\n\t"
"v_cvt_pk_bf16_f32 v207, v230, v231\n\t"
"v_cvt_pk_bf16_f32 v211, v234, v235\n\t"
"v_mfma_f32_16x16x32_bf16 a[140:143], a[232:235], a[184:187], a[140:143]\n\t"
"buffer_load_dwordx4 a[48:51], off, s[80:83], s20 offset:2048 sc1\n\t"
"ds_read_b128 a[184:187], v244 offset:2048\n\t"
"v_accvgpr_read_b32 v224, a58\n\t"
"v_accvgpr_read_b32 v226, a62\n\t"
"v_accvgpr_read_b32 v225, a59\n\t"
"v_accvgpr_read_b32 v227, a63\n\t"
"s_mov_b32 s20, 1081344\n\t"
"v_pk_fma_f32 v[224:225], v[226:227], s[56:57], v[224:225]\n\t"
"v_pk_fma_f32 v[226:227], v[224:225], s[58:59], v[226:227] neg_lo:[0,0,1] neg_hi:[0,0,1]\n\t"
"v_pk_fma_f32 v[228:229], v[42:43], s[60:61], v[224:225]\n\t"
"v_pk_fma_f32 v[42:43], v[224:225], s[60:61], v[42:43]\n\t"
"v_pk_fma_f32 v[224:225], v[142:143], s[62:63], v[226:227]\n\t"
"v_pk_fma_f32 v[142:143], v[226:227], s[62:63], v[142:143]\n\t"
"v_mfma_f32_16x16x32_bf16 a[148:151], a[232:235], a[188:191], a[148:151]\n\t"
"ds_read_b128 a[188:191], v244 offset:2560\n\t"
"v_cvt_pk_bf16_f32 v214, v236, v237\n\t"
"v_mfma_f32_16x16x32_bf16 a[148:151], a[236:239], a[192:195], a[148:151]\n\t"
"v_cvt_pk_bf16_f32 v218, v98, v99\n\t"
"v_accvgpr_read_b32 v230, a64\n\t"
"v_mfma_f32_16x16x32_bf16 a[148:151], a[240:243], a[196:199], a[148:151]\n\t"
"buffer_load_dwordx4 a[52:55], off, s[80:83], s20 offset:0 sc1\n\t"
"v_accvgpr_read_b32 v232, a68\n\t"
"v_accvgpr_read_b32 v231, a65\n\t"
"v_accvgpr_read_b32 v233, a69\n\t"
"v_pk_fma_f32 v[230:231], v[232:233], s[56:57], v[230:231]\n\t"
"v_pk_fma_f32 v[232:233], v[230:231], s[58:59], v[232:233] neg_lo:[0,0,1] neg_hi:[0,0,1]\n\t"
"v_pk_fma_f32 v[234:235], v[44:45], s[60:61], v[230:231]\n\t"
"v_pk_fma_f32 v[44:45], v[230:231], s[60:61], v[44:45]\n\t"
"v_pk_fma_f32 v[230:231], v[144:145], s[62:63], v[232:233]\n\t"
"v_pk_fma_f32 v[144:145], v[232:233], s[62:63], v[144:145]\n\t"
"v_mfma_f32_16x16x32_bf16 a[156:159], a[240:243], a[200:203], a[156:159]\n\t"
"v_cvt_pk_bf16_f32 v215, v224, v225\n\t"
"v_cvt_pk_bf16_f32 v219, v228, v229\n\t"
"v_mfma_f32_16x16x32_bf16 a[156:159], a[236:239], a[196:199], a[156:159]\n\t"
"v_accvgpr_read_b32 v236, a66\n\t"
"v_accvgpr_read_b32 v238, a70\n\t"
"v_mfma_f32_16x16x32_bf16 a[156:159], a[232:235], a[192:195], a[156:159]\n\t"
"buffer_load_dwordx4 a[56:59], off, s[80:83], s20 offset:1024 sc1\n\t"
"ds_read_b128 a[192:195], v244 offset:3072\n\t"
"v_accvgpr_read_b32 v237, a67\n\t"
"v_accvgpr_read_b32 v239, a71\n\t"
"v_cvt_pk_bf16_f32 v230, v230, v231\n\t"
"v_cvt_pk_bf16_f32 v234, v234, v235\n\t"
"v_pk_fma_f32 v[236:237], v[238:239], s[56:57], v[236:237]\n\t"
"v_pk_fma_f32 v[238:239], v[236:237], s[58:59], v[238:239] neg_lo:[0,0,1] neg_hi:[0,0,1]\n\t"
"v_pk_fma_f32 v[98:99], v[46:47], s[60:61], v[236:237]\n\t"
"v_pk_fma_f32 v[46:47], v[236:237], s[60:61], v[46:47]\n\t"
"v_pk_fma_f32 v[236:237], v[146:147], s[62:63], v[238:239]\n\t"
"v_pk_fma_f32 v[146:147], v[238:239], s[62:63], v[146:147]\n\t"
"v_mfma_f32_16x16x32_bf16 a[164:167], a[232:235], a[196:199], a[164:167]\n\t"
"s_waitcnt vmcnt(12) lgkmcnt(4)\n\t"
"ds_read_b128 a[196:199], v244 offset:3584\n\t"
"v_accvgpr_write_b32 a2, v230\n\t"
"v_mfma_f32_16x16x32_bf16 a[164:167], a[236:239], a[200:203], a[164:167]\n\t"
"ds_read_b128 a[200:203], v244 offset:4096\n\t"
"v_accvgpr_write_b32 a6, v234\n\t"
"v_mfma_f32_16x16x32_bf16 a[164:167], a[240:243], a[204:207], a[164:167]\n\t"
"buffer_load_dwordx4 a[44:47], off, s[80:83], s20 offset:2048 sc1\n\t"
"ds_read_b128 a[204:207], v244 offset:4608\n\t"
"v_accvgpr_read_b32 v224, a72\n\t"
"v_accvgpr_read_b32 v226, a76\n\t"
"v_accvgpr_read_b32 v225, a73\n\t"
"v_accvgpr_read_b32 v227, a77\n\t"
"v_cvt_pk_bf16_f32 v236, v236, v237\n\t"
"v_cvt_pk_bf16_f32 v98, v98, v99\n\t"
"s_mov_b32 s20, 1093632\n\t"
"v_pk_fma_f32 v[224:225], v[226:227], s[56:57], v[224:225]\n\t"
"v_pk_fma_f32 v[226:227], v[224:225], s[58:59], v[226:227] neg_lo:[0,0,1] neg_hi:[0,0,1]\n\t"
"v_pk_fma_f32 v[228:229], v[48:49], s[60:61], v[224:225]\n\t"
"v_pk_fma_f32 v[48:49], v[224:225], s[60:61], v[48:49]\n\t"
"v_pk_fma_f32 v[224:225], v[148:149], s[62:63], v[226:227]\n\t"
"v_pk_fma_f32 v[148:149], v[226:227], s[62:63], v[148:149]\n\t"
"Lst14_%=:\n\t"
"v_mfma_f32_16x16x32_bf16 v[0:3], a[216:219], a[168:171], v[0:3]\n\t"
"s_waitcnt lgkmcnt(6)\n\t"
"v_accvgpr_write_b32 a3, v236\n\t"
"v_accvgpr_write_b32 a7, v98\n\t"
"v_mfma_f32_16x16x32_bf16 v[0:3], a[244:247], a[176:179], v[0:3]\n\t"
"v_accvgpr_read_b32 v230, a74\n\t"
"v_accvgpr_read_b32 v232, a78\n\t"
"v_mfma_f32_16x16x32_bf16 v[0:3], a[220:223], a[172:175], v[0:3]\n\t"
"buffer_load_dwordx4 a[64:67], off, s[80:83], s20 offset:0 sc1\n\t"
"v_accvgpr_read_b32 v231, a75\n\t"
"v_accvgpr_read_b32 v233, a79\n\t"
"v_cvt_pk_bf16_f32 v224, v224, v225\n\t"
"v_cvt_pk_bf16_f32 v228, v228, v229\n\t"
"v_pk_fma_f32 v[230:231], v[232:233], s[56:57], v[230:231]\n\t"
"v_pk_fma_f32 v[232:233], v[230:231], s[58:59], v[232:233] neg_lo:[0,0,1] neg_hi:[0,0,1]\n\t"
"v_pk_fma_f32 v[234:235], v[50:51], s[60:61], v[230:231]\n\t"
"v_pk_fma_f32 v[50:51], v[230:231], s[60:61], v[50:51]\n\t"
"v_pk_fma_f32 v[230:231], v[150:151], s[62:63], v[232:233]\n\t"
"v_pk_fma_f32 v[150:151], v[232:233], s[62:63], v[150:151]\n\t"
"v_mfma_f32_16x16x32_bf16 v[4:7], a[216:219], a[172:175], v[4:7]\n\t"
"s_waitcnt lgkmcnt(5)\n\t"
"v_accvgpr_write_b32 a10, v224\n\t"
"v_accvgpr_write_b32 a14, v228\n\t"
"v_mfma_f32_16x16x32_bf16 v[4:7], a[220:223], a[176:179], v[4:7]\n\t"
"v_accvgpr_read_b32 v236, a80\n\t"
"v_accvgpr_read_b32 v238, a84\n\t"
"v_mfma_f32_16x16x32_bf16 v[4:7], a[244:247], a[180:183], v[4:7]\n\t"
"buffer_load_dwordx4 a[60:63], off, s[80:83], s20 offset:1024 sc1\n\t"
"v_accvgpr_read_b32 v237, a81\n\t"
"v_accvgpr_read_b32 v239, a85\n\t"
"v_cvt_pk_bf16_f32 v230, v230, v231\n\t"
"v_cvt_pk_bf16_f32 v234, v234, v235\n\t"
"v_pk_fma_f32 v[236:237], v[238:239], s[56:57], v[236:237]\n\t"
"v_pk_fma_f32 v[238:239], v[236:237], s[58:59], v[238:239] neg_lo:[0,0,1] neg_hi:[0,0,1]\n\t"
"v_pk_fma_f32 v[98:99], v[52:53], s[60:61], v[236:237]\n\t"
"v_pk_fma_f32 v[52:53], v[236:237], s[60:61], v[52:53]\n\t"
"v_pk_fma_f32 v[236:237], v[152:153], s[62:63], v[238:239]\n\t"
"v_pk_fma_f32 v[152:153], v[238:239], s[62:63], v[152:153]\n\t"
"v_mfma_f32_16x16x32_bf16 v[8:11], a[244:247], a[184:187], v[8:11]\n\t"
"s_waitcnt lgkmcnt(4)\n\t"
"v_accvgpr_write_b32 a11, v230\n\t"
"v_accvgpr_write_b32 a15, v234\n\t"
"v_mfma_f32_16x16x32_bf16 v[8:11], a[220:223], a[180:183], v[8:11]\n\t"
"v_accvgpr_read_b32 v224, a82\n\t"
"v_accvgpr_read_b32 v226, a86\n\t"
"v_mfma_f32_16x16x32_bf16 v[8:11], a[216:219], a[176:179], v[8:11]\n\t"
"buffer_load_dwordx4 a[72:75], off, s[80:83], s20 offset:2048 sc1\n\t"
"v_accvgpr_read_b32 v225, a83\n\t"
"v_accvgpr_read_b32 v227, a87\n\t"
"v_cvt_pk_bf16_f32 v236, v236, v237\n\t"
"v_cvt_pk_bf16_f32 v98, v98, v99\n\t"
"s_mov_b32 s20, 589824\n\t"
"v_pk_fma_f32 v[224:225], v[226:227], s[56:57], v[224:225]\n\t"
"v_pk_fma_f32 v[226:227], v[224:225], s[58:59], v[226:227] neg_lo:[0,0,1] neg_hi:[0,0,1]\n\t"
"v_pk_fma_f32 v[228:229], v[54:55], s[60:61], v[224:225]\n\t"
"v_pk_fma_f32 v[54:55], v[224:225], s[60:61], v[54:55]\n\t"
"v_pk_fma_f32 v[224:225], v[154:155], s[62:63], v[226:227]\n\t"
"v_pk_fma_f32 v[154:155], v[226:227], s[62:63], v[154:155]\n\t"
"v_mfma_f32_16x16x32_bf16 v[12:15], a[216:219], a[180:183], v[12:15]\n\t"
"s_waitcnt lgkmcnt(3)\n\t"
"v_accvgpr_write_b32 a18, v236\n\t"
"v_accvgpr_write_b32 a22, v98\n\t"
"v_mfma_f32_16x16x32_bf16 v[12:15], a[220:223], a[184:187], v[12:15]\n\t"
"v_accvgpr_read_b32 v230, a88\n\t"
"v_accvgpr_read_b32 v232, a92\n\t"
"v_mfma_f32_16x16x32_bf16 v[12:15], a[244:247], a[188:191], v[12:15]\n\t"
"buffer_load_dwordx4 a[68:71], off, s[80:83], s20 offset:0 sc1\n\t"
"v_accvgpr_read_b32 v231, a89\n\t"
"v_accvgpr_read_b32 v233, a93\n\t"
"v_accvgpr_read_b32 v236, a90\n\t"
"v_accvgpr_read_b32 v238, a94\n\t"
"v_accvgpr_read_b32 v237, a91\n\t"
"v_accvgpr_read_b32 v239, a95\n\t"
"v_cvt_pk_bf16_f32 v224, v224, v225\n\t"
"v_cvt_pk_bf16_f32 v228, v228, v229\n\t"
"v_pk_fma_f32 v[230:231], v[232:233], s[56:57], v[230:231]\n\t"
"v_pk_fma_f32 v[232:233], v[230:231], s[58:59], v[232:233] neg_lo:[0,0,1] neg_hi:[0,0,1]\n\t"
"v_pk_fma_f32 v[234:235], v[56:57], s[60:61], v[230:231]\n\t"
"v_pk_fma_f32 v[56:57], v[230:231], s[60:61], v[56:57]\n\t"
"v_pk_fma_f32 v[230:231], v[156:157], s[62:63], v[232:233]\n\t"
"v_pk_fma_f32 v[156:157], v[232:233], s[62:63], v[156:157]\n\t"
"v_pk_fma_f32 v[236:237], v[238:239], s[56:57], v[236:237]\n\t"
"v_pk_fma_f32 v[238:239], v[236:237], s[58:59], v[238:239] neg_lo:[0,0,1] neg_hi:[0,0,1]\n\t"
"v_pk_fma_f32 v[98:99], v[58:59], s[60:61], v[236:237]\n\t"
"v_pk_fma_f32 v[58:59], v[236:237], s[60:61], v[58:59]\n\t"
"v_pk_fma_f32 v[236:237], v[158:159], s[62:63], v[238:239]\n\t"
"v_pk_fma_f32 v[158:159], v[238:239], s[62:63], v[158:159]\n\t"
"v_mfma_f32_16x16x32_bf16 v[16:19], a[244:247], a[192:195], v[16:19]\n\t"
"s_waitcnt lgkmcnt(2)\n\t"
"v_accvgpr_write_b32 a19, v224\n\t"
"v_accvgpr_write_b32 a23, v228\n\t"
"v_mfma_f32_16x16x32_bf16 v[16:19], a[220:223], a[188:191], v[16:19]\n\t"
"v_accvgpr_read_b32 v224, a96\n\t"
"v_accvgpr_read_b32 v226, a100\n\t"
"v_mfma_f32_16x16x32_bf16 v[16:19], a[216:219], a[184:187], v[16:19]\n\t"
"buffer_load_dwordx4 a[80:83], off, s[80:83], s20 offset:1024 sc1\n\t"
"v_accvgpr_read_b32 v225, a97\n\t"
"v_accvgpr_read_b32 v227, a101\n\t"
"v_cvt_pk_bf16_f32 v230, v230, v231\n\t"
"v_cvt_pk_bf16_f32 v234, v234, v235\n\t"
"v_pk_fma_f32 v[224:225], v[226:227], s[56:57], v[224:225]\n\t"
"v_pk_fma_f32 v[226:227], v[224:225], s[58:59], v[226:227] neg_lo:[0,0,1] neg_hi:[0,0,1]\n\t"
"v_pk_fma_f32 v[228:229], v[60:61], s[60:61], v[224:225]\n\t"
"v_pk_fma_f32 v[60:61], v[224:225], s[60:61], v[60:61]\n\t"
"v_pk_fma_f32 v[224:225], v[160:161], s[62:63], v[226:227]\n\t"
"v_pk_fma_f32 v[160:161], v[226:227], s[62:63], v[160:161]\n\t"
"v_mfma_f32_16x16x32_bf16 v[20:23], a[216:219], a[188:191], v[20:23]\n\t"
"s_waitcnt lgkmcnt(1)\n\t"
"v_accvgpr_write_b32 a26, v230\n\t"
"v_cvt_pk_bf16_f32 v236, v236, v237\n\t"
"v_mfma_f32_16x16x32_bf16 v[20:23], a[220:223], a[192:195], v[20:23]\n\t"
"v_accvgpr_write_b32 a30, v234\n\t"
"v_cvt_pk_bf16_f32 v98, v98, v99\n\t"
"v_mfma_f32_16x16x32_bf16 v[20:23], a[244:247], a[196:199], v[20:23]\n\t"
"buffer_load_dwordx4 a[88:91], off, s[80:83], s20 offset:2048 sc1\n\t"
"v_accvgpr_read_b32 v230, a98\n\t"
"v_accvgpr_read_b32 v232, a102\n\t"
"v_accvgpr_read_b32 v231, a99\n\t"
"v_accvgpr_read_b32 v233, a103\n\t"
"v_accvgpr_write_b32 a27, v236\n\t"
"v_accvgpr_write_b32 a31, v98\n\t"
"v_cvt_pk_bf16_f32 v224, v224, v225\n\t"
"v_cvt_pk_bf16_f32 v228, v228, v229\n\t"
"s_mov_b32 s20, 602112\n\t"
"v_pk_fma_f32 v[230:231], v[232:233], s[56:57], v[230:231]\n\t"
"v_pk_fma_f32 v[232:233], v[230:231], s[58:59], v[232:233] neg_lo:[0,0,1] neg_hi:[0,0,1]\n\t"
"v_pk_fma_f32 v[234:235], v[62:63], s[60:61], v[230:231]\n\t"
"v_pk_fma_f32 v[62:63], v[230:231], s[60:61], v[62:63]\n\t"
"v_pk_fma_f32 v[230:231], v[162:163], s[62:63], v[232:233]\n\t"
"v_pk_fma_f32 v[162:163], v[232:233], s[62:63], v[162:163]\n\t"
"v_mfma_f32_16x16x32_bf16 v[24:27], a[244:247], a[200:203], v[24:27]\n\t"
"s_waitcnt lgkmcnt(0)\n\t"
"v_accvgpr_write_b32 a34, v224\n\t"
"v_accvgpr_write_b32 a38, v228\n\t"
"v_mfma_f32_16x16x32_bf16 v[24:27], a[220:223], a[196:199], v[24:27]\n\t"
"v_accvgpr_read_b32 v236, a104\n\t"
"v_accvgpr_read_b32 v238, a108\n\t"
"v_mfma_f32_16x16x32_bf16 v[24:27], a[216:219], a[192:195], v[24:27]\n\t"
"buffer_load_dwordx4 a[84:87], off, s[80:83], s20 offset:0 sc1\n\t"
"v_accvgpr_read_b32 v237, a105\n\t"
"v_accvgpr_read_b32 v239, a109\n\t"
"v_cvt_pk_bf16_f32 v230, v230, v231\n\t"
"v_cvt_pk_bf16_f32 v234, v234, v235\n\t"
"v_pk_fma_f32 v[236:237], v[238:239], s[56:57], v[236:237]\n\t"
"v_pk_fma_f32 v[238:239], v[236:237], s[58:59], v[238:239] neg_lo:[0,0,1] neg_hi:[0,0,1]\n\t"
"v_pk_fma_f32 v[98:99], v[64:65], s[60:61], v[236:237]\n\t"
"v_pk_fma_f32 v[64:65], v[236:237], s[60:61], v[64:65]\n\t"
"v_pk_fma_f32 v[236:237], v[164:165], s[62:63], v[238:239]\n\t"
"v_pk_fma_f32 v[164:165], v[238:239], s[62:63], v[164:165]\n\t"
"v_mfma_f32_16x16x32_bf16 v[28:31], a[216:219], a[196:199], v[28:31]\n\t"
"s_waitcnt vmcnt(17)\n\t"
"v_accvgpr_write_b32 a35, v230\n\t"
"v_accvgpr_write_b32 a39, v234\n\t"
"v_mfma_f32_16x16x32_bf16 v[28:31], a[220:223], a[200:203], v[28:31]\n\t"
"v_accvgpr_read_b32 v224, a106\n\t"
"v_accvgpr_read_b32 v226, a110\n\t"
"v_mfma_f32_16x16x32_bf16 v[28:31], a[244:247], a[204:207], v[28:31]\n\t"
"buffer_load_dwordx4 a[96:99], off, s[80:83], s20 offset:1024 sc1\n\t"
"v_accvgpr_read_b32 v225, a107\n\t"
"v_accvgpr_read_b32 v227, a111\n\t"
"v_cvt_pk_bf16_f32 v236, v236, v237\n\t"
"v_cvt_pk_bf16_f32 v98, v98, v99\n\t"
"v_pk_fma_f32 v[224:225], v[226:227], s[56:57], v[224:225]\n\t"
"v_pk_fma_f32 v[226:227], v[224:225], s[58:59], v[226:227] neg_lo:[0,0,1] neg_hi:[0,0,1]\n\t"
"v_pk_fma_f32 v[228:229], v[66:67], s[60:61], v[224:225]\n\t"
"v_pk_fma_f32 v[66:67], v[224:225], s[60:61], v[66:67]\n\t"
"v_pk_fma_f32 v[224:225], v[166:167], s[62:63], v[226:227]\n\t"
"v_pk_fma_f32 v[166:167], v[226:227], s[62:63], v[166:167]\n\t"
"v_mfma_f32_16x16x32_bf16 v[32:35], a[248:251], a[168:171], v[32:35]\n\t"
"v_accvgpr_write_b32 a104, v236\n\t"
"v_accvgpr_write_b32 a106, v98\n\t"
"v_mfma_f32_16x16x32_bf16 v[32:35], v[240:243], a[176:179], v[32:35]\n\t"
"v_accvgpr_read_b32 v230, a112\n\t"
"v_accvgpr_read_b32 v232, a116\n\t"
"v_mfma_f32_16x16x32_bf16 v[32:35], a[252:255], a[172:175], v[32:35]\n\t"
"buffer_load_dwordx4 a[76:79], off, s[80:83], s20 offset:2048 sc1\n\t"
"v_accvgpr_read_b32 v231, a113\n\t"
"v_accvgpr_read_b32 v233, a117\n\t"
"v_cvt_pk_bf16_f32 v224, v224, v225\n\t"
"v_cvt_pk_bf16_f32 v228, v228, v229\n\t"
"s_mov_b32 s20, 614400\n\t"
"v_pk_fma_f32 v[230:231], v[232:233], s[56:57], v[230:231]\n\t"
"v_pk_fma_f32 v[232:233], v[230:231], s[58:59], v[232:233] neg_lo:[0,0,1] neg_hi:[0,0,1]\n\t"
"v_pk_fma_f32 v[234:235], v[68:69], s[60:61], v[230:231]\n\t"
"v_pk_fma_f32 v[68:69], v[230:231], s[60:61], v[68:69]\n\t"
"v_pk_fma_f32 v[230:231], v[168:169], s[62:63], v[232:233]\n\t"
"v_pk_fma_f32 v[168:169], v[232:233], s[62:63], v[168:169]\n\t"
"v_mfma_f32_16x16x32_bf16 v[36:39], a[248:251], a[172:175], v[36:39]\n\t"
"v_accvgpr_write_b32 a105, v224\n\t"
"v_accvgpr_write_b32 a107, v228\n\t"
"v_mfma_f32_16x16x32_bf16 v[36:39], a[252:255], a[176:179], v[36:39]\n\t"
"v_accvgpr_read_b32 v236, a114\n\t"
"v_accvgpr_read_b32 v238, a118\n\t"
"v_mfma_f32_16x16x32_bf16 v[36:39], v[240:243], a[180:183], v[36:39]\n\t"
"buffer_load_dwordx4 a[108:111], off, s[80:83], s20 offset:0 sc1\n\t"
"v_accvgpr_read_b32 v237, a115\n\t"
"v_accvgpr_read_b32 v239, a119\n\t"
"v_cvt_pk_bf16_f32 v230, v230, v231\n\t"
"v_cvt_pk_bf16_f32 v234, v234, v235\n\t"
"v_pk_fma_f32 v[236:237], v[238:239], s[56:57], v[236:237]\n\t"
"v_pk_fma_f32 v[238:239], v[236:237], s[58:59], v[238:239] neg_lo:[0,0,1] neg_hi:[0,0,1]\n\t"
"v_pk_fma_f32 v[98:99], v[70:71], s[60:61], v[236:237]\n\t"
"v_pk_fma_f32 v[70:71], v[236:237], s[60:61], v[70:71]\n\t"
"v_pk_fma_f32 v[236:237], v[170:171], s[62:63], v[238:239]\n\t"
"v_pk_fma_f32 v[170:171], v[238:239], s[62:63], v[170:171]\n\t"
"v_mfma_f32_16x16x32_bf16 v[40:43], v[240:243], a[184:187], v[40:43]\n\t"
"v_accvgpr_write_b32 a112, v230\n\t"
"v_accvgpr_write_b32 a114, v234\n\t"
"v_mfma_f32_16x16x32_bf16 v[40:43], a[252:255], a[180:183], v[40:43]\n\t"
"v_accvgpr_read_b32 v224, a120\n\t"
"v_accvgpr_read_b32 v226, a124\n\t"
"v_mfma_f32_16x16x32_bf16 v[40:43], a[248:251], a[176:179], v[40:43]\n\t"
"buffer_load_dwordx4 a[92:95], off, s[80:83], s20 offset:1024 sc1\n\t"
"v_accvgpr_read_b32 v225, a121\n\t"
"v_accvgpr_read_b32 v227, a125\n\t"
"v_cvt_pk_bf16_f32 v236, v236, v237\n\t"
"v_cvt_pk_bf16_f32 v98, v98, v99\n\t"
"v_pk_fma_f32 v[224:225], v[226:227], s[56:57], v[224:225]\n\t"
"v_pk_fma_f32 v[226:227], v[224:225], s[58:59], v[226:227] neg_lo:[0,0,1] neg_hi:[0,0,1]\n\t"
"v_pk_fma_f32 v[228:229], v[72:73], s[60:61], v[224:225]\n\t"
"v_pk_fma_f32 v[72:73], v[224:225], s[60:61], v[72:73]\n\t"
"v_pk_fma_f32 v[224:225], v[172:173], s[62:63], v[226:227]\n\t"
"v_pk_fma_f32 v[172:173], v[226:227], s[62:63], v[172:173]\n\t"
"v_mfma_f32_16x16x32_bf16 v[44:47], a[248:251], a[180:183], v[44:47]\n\t"
"v_accvgpr_write_b32 a113, v236\n\t"
"v_accvgpr_write_b32 a115, v98\n\t"
"v_mfma_f32_16x16x32_bf16 v[44:47], a[252:255], a[184:187], v[44:47]\n\t"
"v_accvgpr_read_b32 v230, a122\n\t"
"v_accvgpr_read_b32 v232, a126\n\t"
"v_mfma_f32_16x16x32_bf16 v[44:47], v[240:243], a[188:191], v[44:47]\n\t"
"buffer_load_dwordx4 a[116:119], off, s[80:83], s20 offset:2048 sc1\n\t"
"v_accvgpr_read_b32 v231, a123\n\t"
"v_accvgpr_read_b32 v233, a127\n\t"
"v_accvgpr_read_b32 v236, a128\n\t"
"v_accvgpr_read_b32 v238, a132\n\t"
"v_accvgpr_read_b32 v237, a129\n\t"
"v_accvgpr_read_b32 v239, a133\n\t"
"v_cvt_pk_bf16_f32 v224, v224, v225\n\t"
"v_cvt_pk_bf16_f32 v228, v228, v229\n\t"
"s_mov_b32 s20, 774144\n\t"
"v_pk_fma_f32 v[230:231], v[232:233], s[56:57], v[230:231]\n\t"
"v_pk_fma_f32 v[232:233], v[230:231], s[58:59], v[232:233] neg_lo:[0,0,1] neg_hi:[0,0,1]\n\t"
"v_pk_fma_f32 v[234:235], v[74:75], s[60:61], v[230:231]\n\t"
"v_pk_fma_f32 v[74:75], v[230:231], s[60:61], v[74:75]\n\t"
"v_pk_fma_f32 v[230:231], v[174:175], s[62:63], v[232:233]\n\t"
"v_pk_fma_f32 v[174:175], v[232:233], s[62:63], v[174:175]\n\t"
"v_pk_fma_f32 v[236:237], v[238:239], s[56:57], v[236:237]\n\t"
"v_pk_fma_f32 v[238:239], v[236:237], s[58:59], v[238:239] neg_lo:[0,0,1] neg_hi:[0,0,1]\n\t"
"v_pk_fma_f32 v[98:99], v[76:77], s[60:61], v[236:237]\n\t"
"v_pk_fma_f32 v[76:77], v[236:237], s[60:61], v[76:77]\n\t"
"v_pk_fma_f32 v[236:237], v[176:177], s[62:63], v[238:239]\n\t"
"v_pk_fma_f32 v[176:177], v[238:239], s[62:63], v[176:177]\n\t"
"v_mfma_f32_16x16x32_bf16 v[48:51], v[240:243], a[192:195], v[48:51]\n\t"
"v_accvgpr_write_b32 a120, v224\n\t"
"v_accvgpr_write_b32 a122, v228\n\t"
"v_mfma_f32_16x16x32_bf16 v[48:51], a[252:255], a[188:191], v[48:51]\n\t"
"v_accvgpr_read_b32 v224, a130\n\t"
"v_accvgpr_read_b32 v226, a134\n\t"
"v_mfma_f32_16x16x32_bf16 v[48:51], a[248:251], a[184:187], v[48:51]\n\t"
"buffer_load_dwordx4 a[100:103], off, s[80:83], s20 offset:0 sc1\n\t"
"v_accvgpr_read_b32 v225, a131\n\t"
"v_accvgpr_read_b32 v227, a135\n\t"
"v_cvt_pk_bf16_f32 v230, v230, v231\n\t"
"v_cvt_pk_bf16_f32 v234, v234, v235\n\t"
"v_cvt_pk_bf16_f32 v236, v236, v237\n\t"
"v_cvt_pk_bf16_f32 v98, v98, v99\n\t"
"v_pk_fma_f32 v[224:225], v[226:227], s[56:57], v[224:225]\n\t"
"v_pk_fma_f32 v[226:227], v[224:225], s[58:59], v[226:227] neg_lo:[0,0,1] neg_hi:[0,0,1]\n\t"
"v_pk_fma_f32 v[228:229], v[78:79], s[60:61], v[224:225]\n\t"
"v_pk_fma_f32 v[78:79], v[224:225], s[60:61], v[78:79]\n\t"
"v_pk_fma_f32 v[224:225], v[178:179], s[62:63], v[226:227]\n\t"
"v_pk_fma_f32 v[178:179], v[226:227], s[62:63], v[178:179]\n\t"
"v_mfma_f32_16x16x32_bf16 v[52:55], a[248:251], a[188:191], v[52:55]\n\t"
"v_accvgpr_write_b32 a121, v230\n\t"
"v_accvgpr_write_b32 a123, v234\n\t"
"v_mfma_f32_16x16x32_bf16 v[52:55], a[252:255], a[192:195], v[52:55]\n\t"
"v_accvgpr_write_b32 a128, v236\n\t"
"v_accvgpr_write_b32 a130, v98\n\t"
"v_mfma_f32_16x16x32_bf16 v[52:55], v[240:243], a[196:199], v[52:55]\n\t"
"buffer_load_dwordx4 a[124:127], off, s[80:83], s20 offset:1024 sc1\n\t"
"v_accvgpr_read_b32 v230, a136\n\t"
"v_accvgpr_read_b32 v232, a140\n\t"
"v_accvgpr_read_b32 v231, a137\n\t"
"v_accvgpr_read_b32 v233, a141\n\t"
"v_cvt_pk_bf16_f32 v224, v224, v225\n\t"
"v_cvt_pk_bf16_f32 v228, v228, v229\n\t"
"v_pk_fma_f32 v[230:231], v[232:233], s[56:57], v[230:231]\n\t"
"v_pk_fma_f32 v[232:233], v[230:231], s[58:59], v[232:233] neg_lo:[0,0,1] neg_hi:[0,0,1]\n\t"
"v_pk_fma_f32 v[234:235], v[80:81], s[60:61], v[230:231]\n\t"
"v_pk_fma_f32 v[80:81], v[230:231], s[60:61], v[80:81]\n\t"
"v_pk_fma_f32 v[230:231], v[180:181], s[62:63], v[232:233]\n\t"
"v_pk_fma_f32 v[180:181], v[232:233], s[62:63], v[180:181]\n\t"
"v_mfma_f32_16x16x32_bf16 v[56:59], v[240:243], a[200:203], v[56:59]\n\t"
"v_accvgpr_write_b32 a129, v224\n\t"
"v_accvgpr_write_b32 a131, v228\n\t"
"v_mfma_f32_16x16x32_bf16 v[56:59], a[252:255], a[196:199], v[56:59]\n\t"
"v_accvgpr_read_b32 v236, a138\n\t"
"v_accvgpr_read_b32 v238, a142\n\t"
"v_mfma_f32_16x16x32_bf16 v[56:59], a[248:251], a[192:195], v[56:59]\n\t"
"buffer_load_dwordx4 a[132:135], off, s[80:83], s20 offset:2048 sc1\n\t"
"v_accvgpr_read_b32 v237, a139\n\t"
"v_accvgpr_read_b32 v239, a143\n\t"
"v_cvt_pk_bf16_f32 v230, v230, v231\n\t"
"v_cvt_pk_bf16_f32 v234, v234, v235\n\t"
"s_mov_b32 s20, 786432\n\t"
"v_pk_fma_f32 v[236:237], v[238:239], s[56:57], v[236:237]\n\t"
"v_pk_fma_f32 v[238:239], v[236:237], s[58:59], v[238:239] neg_lo:[0,0,1] neg_hi:[0,0,1]\n\t"
"v_pk_fma_f32 v[98:99], v[82:83], s[60:61], v[236:237]\n\t"
"v_pk_fma_f32 v[82:83], v[236:237], s[60:61], v[82:83]\n\t"
"v_pk_fma_f32 v[236:237], v[182:183], s[62:63], v[238:239]\n\t"
"v_pk_fma_f32 v[182:183], v[238:239], s[62:63], v[182:183]\n\t"
"v_mfma_f32_16x16x32_bf16 v[60:63], a[248:251], a[196:199], v[60:63]\n\t"
"s_waitcnt vmcnt(22)\n\t"
"v_accvgpr_write_b32 a136, v230\n\t"
"v_accvgpr_write_b32 a138, v234\n\t"
"v_mfma_f32_16x16x32_bf16 v[60:63], a[252:255], a[200:203], v[60:63]\n\t"
"v_accvgpr_read_b32 v224, a144\n\t"
"v_accvgpr_read_b32 v226, a148\n\t"
"v_mfma_f32_16x16x32_bf16 v[60:63], v[240:243], a[204:207], v[60:63]\n\t"
"buffer_load_dwordx4 a[216:219], off, s[80:83], s20 offset:0 sc1\n\t"
"v_accvgpr_read_b32 v225, a145\n\t"
"v_accvgpr_read_b32 v227, a149\n\t"
"v_cvt_pk_bf16_f32 v236, v236, v237\n\t"
"v_cvt_pk_bf16_f32 v98, v98, v99\n\t"
"v_pk_fma_f32 v[224:225], v[226:227], s[56:57], v[224:225]\n\t"
"v_pk_fma_f32 v[226:227], v[224:225], s[58:59], v[226:227] neg_lo:[0,0,1] neg_hi:[0,0,1]\n\t"
"v_pk_fma_f32 v[228:229], v[84:85], s[60:61], v[224:225]\n\t"
"v_pk_fma_f32 v[84:85], v[224:225], s[60:61], v[84:85]\n\t"
"v_pk_fma_f32 v[224:225], v[184:185], s[62:63], v[226:227]\n\t"
"v_pk_fma_f32 v[184:185], v[226:227], s[62:63], v[184:185]\n\t"
"v_mfma_f32_16x16x32_bf16 v[64:67], a[208:211], a[168:171], v[64:67]\n\t"
"ds_read_b128 a[168:171], v244 offset:9216\n\t"
"v_accvgpr_write_b32 a137, v236\n\t"
"v_mfma_f32_16x16x32_bf16 v[64:67], a[212:215], a[176:179], v[64:67]\n\t"
"v_accvgpr_write_b32 a139, v98\n\t"
"v_accvgpr_read_b32 v230, a146\n\t"
"v_mfma_f32_16x16x32_bf16 v[64:67], a[224:227], a[172:175], v[64:67]\n\t"
"buffer_load_dwordx4 a[140:143], off, s[80:83], s20 offset:1024 sc1\n\t"
"v_accvgpr_read_b32 v232, a150\n\t"
"v_accvgpr_read_b32 v231, a147\n\t"
"v_accvgpr_read_b32 v233, a151\n\t"
"v_cvt_pk_bf16_f32 v224, v224, v225\n\t"
"v_cvt_pk_bf16_f32 v228, v228, v229\n\t"
"v_pk_fma_f32 v[230:231], v[232:233], s[56:57], v[230:231]\n\t"
"v_pk_fma_f32 v[232:233], v[230:231], s[58:59], v[232:233] neg_lo:[0,0,1] neg_hi:[0,0,1]\n\t"
"v_pk_fma_f32 v[234:235], v[86:87], s[60:61], v[230:231]\n\t"
"v_pk_fma_f32 v[86:87], v[230:231], s[60:61], v[86:87]\n\t"
"v_pk_fma_f32 v[230:231], v[186:187], s[62:63], v[232:233]\n\t"
"v_pk_fma_f32 v[186:187], v[232:233], s[62:63], v[186:187]\n\t"
"v_mfma_f32_16x16x32_bf16 v[68:71], a[208:211], a[172:175], v[68:71]\n\t"
"ds_read_b128 a[172:175], v244 offset:9728\n\t"
"v_accvgpr_write_b32 a144, v224\n\t"
"v_mfma_f32_16x16x32_bf16 v[68:71], a[224:227], a[176:179], v[68:71]\n\t"
"v_accvgpr_write_b32 a146, v228\n\t"
"v_accvgpr_read_b32 v236, a152\n\t"
"v_mfma_f32_16x16x32_bf16 v[68:71], a[212:215], a[180:183], v[68:71]\n\t"
"buffer_load_dwordx4 v[240:243], off, s[80:83], s20 offset:2048 sc1\n\t"
"v_accvgpr_read_b32 v238, a156\n\t"
"v_accvgpr_read_b32 v237, a153\n\t"
"v_accvgpr_read_b32 v239, a157\n\t"
"v_cvt_pk_bf16_f32 v230, v230, v231\n\t"
"v_cvt_pk_bf16_f32 v234, v234, v235\n\t"
"s_mov_b32 s20, 798720\n\t"
"v_pk_fma_f32 v[236:237], v[238:239], s[56:57], v[236:237]\n\t"
"v_pk_fma_f32 v[238:239], v[236:237], s[58:59], v[238:239] neg_lo:[0,0,1] neg_hi:[0,0,1]\n\t"
"v_pk_fma_f32 v[98:99], v[88:89], s[60:61], v[236:237]\n\t"
"v_pk_fma_f32 v[88:89], v[236:237], s[60:61], v[88:89]\n\t"
"v_pk_fma_f32 v[236:237], v[188:189], s[62:63], v[238:239]\n\t"
"v_pk_fma_f32 v[188:189], v[238:239], s[62:63], v[188:189]\n\t"
"v_mfma_f32_16x16x32_bf16 v[72:75], a[212:215], a[184:187], v[72:75]\n\t"
"v_accvgpr_write_b32 a145, v230\n\t"
"v_accvgpr_write_b32 a147, v234\n\t"
"v_mfma_f32_16x16x32_bf16 v[72:75], a[224:227], a[180:183], v[72:75]\n\t"
"v_accvgpr_read_b32 v224, a154\n\t"
"v_accvgpr_read_b32 v226, a158\n\t"
"v_mfma_f32_16x16x32_bf16 v[72:75], a[208:211], a[176:179], v[72:75]\n\t"
"buffer_load_dwordx4 a[148:151], off, s[80:83], s20 offset:0 sc1\n\t"
"ds_read_b128 a[176:179], v244 offset:10240\n\t"
"v_accvgpr_read_b32 v225, a155\n\t"
"v_accvgpr_read_b32 v227, a159\n\t"
"v_cvt_pk_bf16_f32 v236, v236, v237\n\t"
"v_cvt_pk_bf16_f32 v98, v98, v99\n\t"
"v_pk_fma_f32 v[224:225], v[226:227], s[56:57], v[224:225]\n\t"
"v_pk_fma_f32 v[226:227], v[224:225], s[58:59], v[226:227] neg_lo:[0,0,1] neg_hi:[0,0,1]\n\t"
"v_pk_fma_f32 v[228:229], v[90:91], s[60:61], v[224:225]\n\t"
"v_pk_fma_f32 v[90:91], v[224:225], s[60:61], v[90:91]\n\t"
"v_pk_fma_f32 v[224:225], v[190:191], s[62:63], v[226:227]\n\t"
"v_pk_fma_f32 v[190:191], v[226:227], s[62:63], v[190:191]\n\t"
"v_mfma_f32_16x16x32_bf16 v[76:79], a[208:211], a[180:183], v[76:79]\n\t"
"Lpg11_%=:\n\t"
"s_cbranch_vccnz Lvr0_%=\n\t"
"ds_read_b128 a[180:183], v244 offset:10752\n\t"
"v_accvgpr_write_b32 a152, v236\n\t"
"v_mfma_f32_16x16x32_bf16 v[76:79], a[224:227], a[184:187], v[76:79]\n\t"
"v_accvgpr_write_b32 a154, v98\n\t"
"v_accvgpr_read_b32 v230, a160\n\t"
"v_mfma_f32_16x16x32_bf16 v[76:79], a[212:215], a[188:191], v[76:79]\n\t"
"buffer_load_dwordx4 a[220:223], off, s[80:83], s20 offset:1024 sc1\n\t"
"v_accvgpr_read_b32 v232, a164\n\t"
"v_accvgpr_read_b32 v231, a161\n\t"
"v_accvgpr_read_b32 v233, a165\n\t"
"v_cvt_pk_bf16_f32 v224, v224, v225\n\t"
"v_cvt_pk_bf16_f32 v228, v228, v229\n\t"
"v_pk_fma_f32 v[230:231], v[232:233], s[56:57], v[230:231]\n\t"
"v_pk_fma_f32 v[232:233], v[230:231], s[58:59], v[232:233] neg_lo:[0,0,1] neg_hi:[0,0,1]\n\t"
"v_pk_fma_f32 v[234:235], v[92:93], s[60:61], v[230:231]\n\t"
"v_pk_fma_f32 v[92:93], v[230:231], s[60:61], v[92:93]\n\t"
"v_pk_fma_f32 v[230:231], v[192:193], s[62:63], v[232:233]\n\t"
"v_pk_fma_f32 v[192:193], v[232:233], s[62:63], v[192:193]\n\t"
"v_mfma_f32_16x16x32_bf16 v[80:83], a[212:215], a[192:195], v[80:83]\n\t"
"v_accvgpr_write_b32 a153, v224\n\t"
"v_accvgpr_write_b32 a155, v228\n\t"
"v_mfma_f32_16x16x32_bf16 v[80:83], a[224:227], a[188:191], v[80:83]\n\t"
"v_accvgpr_read_b32 v236, a162\n\t"
"v_accvgpr_read_b32 v238, a166\n\t"
"v_mfma_f32_16x16x32_bf16 v[80:83], a[208:211], a[184:187], v[80:83]\n\t"
"buffer_load_dwordx4 a[156:159], off, s[80:83], s20 offset:2048 sc1\n\t"
"ds_read_b128 a[184:187], v244 offset:11264\n\t"
"v_accvgpr_read_b32 v237, a163\n\t"
"v_accvgpr_read_b32 v239, a167\n\t"
"v_cvt_pk_bf16_f32 v230, v230, v231\n\t"
"v_cvt_pk_bf16_f32 v234, v234, v235\n\t"
"s_mov_b32 s20, 1179648\n\t"
"v_pk_fma_f32 v[236:237], v[238:239], s[56:57], v[236:237]\n\t"
"v_pk_fma_f32 v[238:239], v[236:237], s[58:59], v[238:239] neg_lo:[0,0,1] neg_hi:[0,0,1]\n\t"
"v_pk_fma_f32 v[98:99], v[94:95], s[60:61], v[236:237]\n\t"
"v_pk_fma_f32 v[94:95], v[236:237], s[60:61], v[94:95]\n\t"
"v_pk_fma_f32 v[236:237], v[194:195], s[62:63], v[238:239]\n\t"
"v_pk_fma_f32 v[194:195], v[238:239], s[62:63], v[194:195]\n\t"
"v_mfma_f32_16x16x32_bf16 v[84:87], a[208:211], a[188:191], v[84:87]\n\t"
"ds_read_b128 a[188:191], v244 offset:11776\n\t"
"v_accvgpr_write_b32 a160, v230\n\t"
"v_mfma_f32_16x16x32_bf16 v[84:87], a[224:227], a[192:195], v[84:87]\n\t"
"v_accvgpr_write_b32 a162, v234\n\t"
"v_cvt_pk_bf16_f32 v236, v236, v237\n\t"
"v_mfma_f32_16x16x32_bf16 v[84:87], a[212:215], a[196:199], v[84:87]\n\t"
"buffer_load_dwordx4 a[232:235], off, s[80:83], s20 offset:0 sc1\n\t"
"v_cvt_pk_bf16_f32 v98, v98, v99\n\t"
"v_mfma_f32_16x16x32_bf16 v[88:91], a[212:215], a[200:203], v[88:91]\n\t"
"v_accvgpr_write_b32 a161, v236\n\t"
"v_accvgpr_write_b32 a163, v98\n\t"
"v_mfma_f32_16x16x32_bf16 v[88:91], a[224:227], a[196:199], v[88:91]\n\t"
"v_mfma_f32_16x16x32_bf16 v[88:91], a[208:211], a[192:195], v[88:91]\n\t"
"buffer_load_dwordx4 a[164:167], off, s[80:83], s20 offset:1024 sc1\n\t"
"v_mfma_f32_16x16x32_bf16 v[92:95], a[208:211], a[196:199], v[92:95]\n\t"
"s_waitcnt vmcnt(26) lgkmcnt(3)\n\t"
"ds_read_b128 a[192:195], v244 offset:12288\n\t"
"v_mfma_f32_16x16x32_bf16 v[92:95], a[224:227], a[200:203], v[92:95]\n\t"
"buffer_load_dwordx4 a[236:239], off, s[80:83], s20 offset:2048 sc1\n\t"
"v_mfma_f32_16x16x32_bf16 v[92:95], a[212:215], a[204:207], v[92:95]\n\t"
"s_mov_b32 s20, 1191936\n\t"
"ds_read_b128 a[196:199], v244 offset:12800\n\t"
"v_mfma_f32_16x16x32_bf16 v[100:103], a[228:231], a[168:171], v[100:103]\n\t"
"s_waitcnt lgkmcnt(4)\n\t"
"ds_read_b128 a[200:203], v244 offset:13312\n\t"
"v_mfma_f32_16x16x32_bf16 v[100:103], a[48:51], a[176:179], v[100:103]\n\t"
"ds_read_b128 a[204:207], v244 offset:13824\n\t"
"v_mfma_f32_16x16x32_bf16 v[100:103], a[40:43], a[172:175], v[100:103]\n\t"
"buffer_load_dwordx4 v[236:239], off, s[80:83], s20 offset:0 sc1\n\t"
"v_mfma_f32_16x16x32_bf16 v[104:107], a[228:231], a[172:175], v[104:107]\n\t"
"buffer_load_dwordx4 v[96:99], off, s[80:83], s20 offset:1024 sc1\n\t"
"v_mfma_f32_16x16x32_bf16 v[104:107], a[40:43], a[176:179], v[104:107]\n\t"
"s_waitcnt lgkmcnt(5)\n\t"
"v_mfma_f32_16x16x32_bf16 v[104:107], a[48:51], a[180:183], v[104:107]\n\t"
"s_waitcnt lgkmcnt(0)\n\t"
"s_barrier\n\t"
"v_mfma_f32_16x16x32_bf16 v[108:111], a[48:51], a[184:187], v[108:111]\n\t"
"v_mfma_f32_16x16x32_bf16 v[108:111], a[40:43], a[180:183], v[108:111]\n\t"
"buffer_load_dwordx4 a[208:211], off, s[80:83], s20 offset:2048 sc1\n\t"
"v_mfma_f32_16x16x32_bf16 v[108:111], a[228:231], a[176:179], v[108:111]\n\t"
"s_mov_b32 s20, 1204224\n\t"
"ds_write_b128 v249, v[196:199] offset:0\n\t"
"v_mfma_f32_16x16x32_bf16 v[112:115], a[228:231], a[180:183], v[112:115]\n\t"
"buffer_load_dwordx4 a[212:215], off, s[80:83], s20 offset:0 sc1\n\t"
"v_mfma_f32_16x16x32_bf16 v[112:115], a[40:43], a[184:187], v[112:115]\n\t"
"buffer_load_dwordx4 a[224:227], off, s[80:83], s20 offset:1024 sc1\n\t"
"v_mfma_f32_16x16x32_bf16 v[112:115], a[48:51], a[188:191], v[112:115]\n\t"
"s_add_u32 m0, s24, 54272\n\t"
"s_add_u32 s28, s21, 1575936\n\t"
"ds_write_b64 v250, a[104:105] offset:0\n\t"
"v_mfma_f32_16x16x32_bf16 v[116:119], a[48:51], a[192:195], v[116:119]\n\t"
"buffer_load_dwordx4 v247, s[12:15], s28 offen offset:0 nt lds\n\t"
"v_mfma_f32_16x16x32_bf16 v[116:119], a[40:43], a[188:191], v[116:119]\n\t"
"s_add_u32 m0, s25, 512\n\t"
"v_mfma_f32_16x16x32_bf16 v[116:119], a[228:231], a[184:187], v[116:119]\n\t"
"buffer_load_dwordx4 v254, s[12:15], 0 offen offset:0 nt lds\n\t"
"v_mfma_f32_16x16x32_bf16 v[120:123], a[228:231], a[188:191], v[120:123]\n\t"
"s_add_u32 m0, s24, 2048\n\t"
"s_add_u32 s28, s21, 3072\n\t"
"v_mfma_f32_16x16x32_bf16 v[120:123], a[40:43], a[192:195], v[120:123]\n\t"
"buffer_load_dwordx4 v247, s[12:15], s28 offen offset:0 nt lds\n\t"
"v_mfma_f32_16x16x32_bf16 v[120:123], a[48:51], a[196:199], v[120:123]\n\t"
"s_add_u32 m0, s24, 8192\n\t"
"s_add_u32 s28, s21, 199680\n\t"
"ds_write_b128 v249, v[200:203] offset:6144\n\t"
"v_mfma_f32_16x16x32_bf16 v[124:127], a[48:51], a[200:203], v[124:127]\n\t"
"buffer_load_dwordx4 v247, s[12:15], s28 offen offset:0 nt lds\n\t"
"v_mfma_f32_16x16x32_bf16 v[124:127], a[40:43], a[196:199], v[124:127]\n\t"
"s_add_u32 m0, s24, 14336\n\t"
"s_add_u32 s28, s21, 396288\n\t"
"v_mfma_f32_16x16x32_bf16 v[124:127], a[228:231], a[192:195], v[124:127]\n\t"
"buffer_load_dwordx4 v247, s[12:15], s28 offen offset:0 nt lds\n\t"
"v_mfma_f32_16x16x32_bf16 v[128:131], a[228:231], a[196:199], v[128:131]\n\t"
"s_waitcnt vmcnt(34)\n\t"
"s_add_u32 m0, s24, 20480\n\t"
"s_add_u32 s28, s21, 589824\n\t"
"v_mfma_f32_16x16x32_bf16 v[128:131], a[40:43], a[200:203], v[128:131]\n\t"
"buffer_load_dwordx4 v247, s[12:15], s28 offen offset:0 nt lds\n\t"
"v_mfma_f32_16x16x32_bf16 v[128:131], a[48:51], a[204:207], v[128:131]\n\t"
"s_add_u32 m0, s24, 26624\n\t"
"s_add_u32 s28, s21, 786432\n\t"
"ds_write_b64 v250, a[106:107] offset:6144\n\t"
"v_mfma_f32_16x16x32_bf16 v[132:135], a[52:55], a[168:171], v[132:135]\n\t"
"buffer_load_dwordx4 v247, s[12:15], s28 offen offset:0 nt lds\n\t"
"v_mfma_f32_16x16x32_bf16 v[132:135], a[44:47], a[176:179], v[132:135]\n\t"
"v_mfma_f32_16x16x32_bf16 v[132:135], a[56:59], a[172:175], v[132:135]\n\t"
"v_mfma_f32_16x16x32_bf16 v[136:139], a[52:55], a[172:175], v[136:139]\n\t"
"v_mfma_f32_16x16x32_bf16 v[136:139], a[56:59], a[176:179], v[136:139]\n\t"
"v_mfma_f32_16x16x32_bf16 v[136:139], a[44:47], a[180:183], v[136:139]\n\t"
"v_mfma_f32_16x16x32_bf16 v[140:143], a[44:47], a[184:187], v[140:143]\n\t"
"v_mfma_f32_16x16x32_bf16 v[140:143], a[56:59], a[180:183], v[140:143]\n\t"
"v_mfma_f32_16x16x32_bf16 v[140:143], a[52:55], a[176:179], v[140:143]\n\t"
"v_mfma_f32_16x16x32_bf16 v[144:147], a[52:55], a[180:183], v[144:147]\n\t"
"v_mfma_f32_16x16x32_bf16 v[144:147], a[56:59], a[184:187], v[144:147]\n\t"
"v_mfma_f32_16x16x32_bf16 v[144:147], a[44:47], a[188:191], v[144:147]\n\t"
"v_mfma_f32_16x16x32_bf16 v[148:151], a[44:47], a[192:195], v[148:151]\n\t"
"v_mfma_f32_16x16x32_bf16 v[148:151], a[56:59], a[188:191], v[148:151]\n\t"
"v_mfma_f32_16x16x32_bf16 v[148:151], a[52:55], a[184:187], v[148:151]\n\t"
"v_mfma_f32_16x16x32_bf16 v[152:155], a[52:55], a[188:191], v[152:155]\n\t"
"v_mfma_f32_16x16x32_bf16 v[152:155], a[56:59], a[192:195], v[152:155]\n\t"
"v_mfma_f32_16x16x32_bf16 v[152:155], a[44:47], a[196:199], v[152:155]\n\t"
"v_mfma_f32_16x16x32_bf16 v[156:159], a[44:47], a[200:203], v[156:159]\n\t"
"v_mfma_f32_16x16x32_bf16 v[156:159], a[56:59], a[196:199], v[156:159]\n\t"
"v_mfma_f32_16x16x32_bf16 v[156:159], a[52:55], a[192:195], v[156:159]\n\t"
"v_mfma_f32_16x16x32_bf16 v[160:163], a[52:55], a[196:199], v[160:163]\n\t"
"s_waitcnt vmcnt(33)\n\t"
"v_mfma_f32_16x16x32_bf16 v[160:163], a[56:59], a[200:203], v[160:163]\n\t"
"v_mfma_f32_16x16x32_bf16 v[160:163], a[44:47], a[204:207], v[160:163]\n\t"
"v_mfma_f32_16x16x32_bf16 v[164:167], a[64:67], a[168:171], v[164:167]\n\t"
"ds_read_b128 a[168:171], v244 offset:24576\n\t"
"v_mfma_f32_16x16x32_bf16 v[164:167], a[72:75], a[176:179], v[164:167]\n\t"
"v_mfma_f32_16x16x32_bf16 v[164:167], a[60:63], a[172:175], v[164:167]\n\t"
"v_mfma_f32_16x16x32_bf16 v[168:171], a[64:67], a[172:175], v[168:171]\n\t"
"ds_read_b128 a[172:175], v244 offset:25088\n\t"
"v_mfma_f32_16x16x32_bf16 v[168:171], a[60:63], a[176:179], v[168:171]\n\t"
"v_mfma_f32_16x16x32_bf16 v[168:171], a[72:75], a[180:183], v[168:171]\n\t"
"v_mfma_f32_16x16x32_bf16 v[172:175], a[72:75], a[184:187], v[172:175]\n\t"
"v_mfma_f32_16x16x32_bf16 v[172:175], a[60:63], a[180:183], v[172:175]\n\t"
"v_mfma_f32_16x16x32_bf16 v[172:175], a[64:67], a[176:179], v[172:175]\n\t"
"ds_read_b128 a[176:179], v244 offset:25600\n\t"
"v_mfma_f32_16x16x32_bf16 v[176:179], a[64:67], a[180:183], v[176:179]\n\t"
"ds_read_b128 a[180:183], v244 offset:26112\n\t"
"v_mfma_f32_16x16x32_bf16 v[176:179], a[60:63], a[184:187], v[176:179]\n\t"
"s_add_u32 m0, s24, 32768\n\t"
"s_add_u32 s28, s21, 983040\n\t"
"v_mfma_f32_16x16x32_bf16 v[176:179], a[72:75], a[188:191], v[176:179]\n\t"
"s_waitcnt lgkmcnt(4)\n\t"
"s_barrier\n\t"
"v_mfma_f32_16x16x32_bf16 v[180:183], a[72:75], a[192:195], v[180:183]\n\t"
"v_mfma_f32_16x16x32_bf16 v[180:183], a[60:63], a[188:191], v[180:183]\n\t"
"buffer_load_dwordx4 a[44:47], off, s[80:83], s20 offset:2048 sc1\n\t"
"v_mfma_f32_16x16x32_bf16 v[180:183], a[64:67], a[184:187], v[180:183]\n\t"
"buffer_load_dwordx4 v247, s[12:15], s28 offen offset:0 nt lds\n\t"
"v_mfma_f32_16x16x32_bf16 v[184:187], a[64:67], a[188:191], v[184:187]\n\t"
"s_add_u32 m0, s24, 40448\n\t"
"s_add_u32 s28, s21, 1179648\n\t"
"ds_read_b128 a[184:187], v244 offset:26624\n\t"
"v_mfma_f32_16x16x32_bf16 v[184:187], a[60:63], a[192:195], v[184:187]\n\t"
"buffer_load_dwordx4 v247, s[12:15], s28 offen offset:0 nt lds\n\t"
"v_mfma_f32_16x16x32_bf16 v[184:187], a[72:75], a[196:199], v[184:187]\n\t"
"s_add_u32 m0, s24, 46592\n\t"
"s_add_u32 s28, s21, 1376256\n\t"
"ds_read_b128 a[188:191], v244 offset:27136\n\t"
"v_mfma_f32_16x16x32_bf16 v[188:191], a[72:75], a[200:203], v[188:191]\n\t"
"buffer_load_dwordx4 v247, s[12:15], s28 offen offset:0 nt lds\n\t"
"v_mfma_f32_16x16x32_bf16 v[188:191], a[60:63], a[196:199], v[188:191]\n\t"
"s_add_u32 m0, s24, 52736\n\t"
"s_add_u32 s28, s21, 1572864\n\t"
"v_mfma_f32_16x16x32_bf16 v[188:191], a[64:67], a[192:195], v[188:191]\n\t"
"buffer_load_dwordx4 v247, s[12:15], s28 offen offset:0 nt lds\n\t"
"v_mfma_f32_16x16x32_bf16 v[192:195], a[64:67], a[196:199], v[192:195]\n\t"
"s_waitcnt vmcnt(35) lgkmcnt(3)\n\t"
"ds_read_b128 a[192:195], v244 offset:27648\n\t"
"v_mfma_f32_16x16x32_bf16 v[192:195], a[60:63], a[200:203], v[192:195]\n\t"
"s_add_u32 m0, s24, 512\n\t"
"s_add_u32 s28, s21, 0\n\t"
"ds_read_b128 a[196:199], v244 offset:28160\n\t"
"v_mfma_f32_16x16x32_bf16 v[192:195], a[72:75], a[204:207], v[192:195]\n\t"
"buffer_load_dwordx4 v247, s[12:15], s28 offen offset:0 nt lds\n\t"
"s_add_u32 m0, s24, 6656\n\t"
"s_add_u32 s28, s21, 196608\n\t"
"Lst15_%=:\n\t"
"v_mfma_f32_16x16x32_bf16 v[0:3], a[68:71], a[168:171], v[0:3]\n\t"
"buffer_load_dwordx4 v247, s[12:15], s28 offen offset:0 nt lds\n\t"
"v_mfma_f32_16x16x32_bf16 v[0:3], a[88:91], a[176:179], v[0:3]\n\t"
"s_waitcnt lgkmcnt(4)\n\t"
"ds_read_b128 a[200:203], v244 offset:28672\n\t"
"v_mfma_f32_16x16x32_bf16 v[0:3], a[80:83], a[172:175], v[0:3]\n\t"
"s_add_u32 m0, s24, 12800\n\t"
"s_add_u32 s28, s21, 393216\n\t"
"ds_write_b128 v249, v[204:207] offset:12288\n\t"
"v_mfma_f32_16x16x32_bf16 v[4:7], a[68:71], a[172:175], v[4:7]\n\t"
"buffer_load_dwordx4 v247, s[12:15], s28 offen offset:0 nt lds\n\t"
"v_mfma_f32_16x16x32_bf16 v[4:7], a[80:83], a[176:179], v[4:7]\n\t"
"s_waitcnt lgkmcnt(5)\n\t"
"ds_read_b128 a[204:207], v244 offset:29184\n\t"
"v_mfma_f32_16x16x32_bf16 v[4:7], a[88:91], a[180:183], v[4:7]\n\t"
"ds_write_b64 v250, a[112:113] offset:12288\n\t"
"v_mfma_f32_16x16x32_bf16 v[8:11], a[88:91], a[184:187], v[8:11]\n\t"
"s_waitcnt lgkmcnt(6)\n\t"
"v_mfma_f32_16x16x32_bf16 v[8:11], a[80:83], a[180:183], v[8:11]\n\t"
"v_mfma_f32_16x16x32_bf16 v[8:11], a[68:71], a[176:179], v[8:11]\n\t"
"ds_write_b128 v249, v[208:211] offset:18432\n\t"
"v_mfma_f32_16x16x32_bf16 v[12:15], a[68:71], a[180:183], v[12:15]\n\t"
"s_waitcnt lgkmcnt(6)\n\t"
"v_mfma_f32_16x16x32_bf16 v[12:15], a[80:83], a[184:187], v[12:15]\n\t"
"v_mfma_f32_16x16x32_bf16 v[12:15], a[88:91], a[188:191], v[12:15]\n\t"
"ds_write_b64 v250, a[114:115] offset:18432\n\t"
"v_mfma_f32_16x16x32_bf16 v[16:19], a[88:91], a[192:195], v[16:19]\n\t"
"s_waitcnt lgkmcnt(6)\n\t"
"v_mfma_f32_16x16x32_bf16 v[16:19], a[80:83], a[188:191], v[16:19]\n\t"
"v_mfma_f32_16x16x32_bf16 v[16:19], a[68:71], a[184:187], v[16:19]\n\t"
"ds_read_b128 a[40:43], v252 offset:0\n\t"
"v_mfma_f32_16x16x32_bf16 v[20:23], a[68:71], a[188:191], v[20:23]\n\t"
"s_waitcnt lgkmcnt(6)\n\t"
"v_mfma_f32_16x16x32_bf16 v[20:23], a[80:83], a[192:195], v[20:23]\n\t"
"ds_read_b128 a[48:51], v252 offset:1024\n\t"
"v_mfma_f32_16x16x32_bf16 v[20:23], a[88:91], a[196:199], v[20:23]\n\t"
"v_mfma_f32_16x16x32_bf16 v[24:27], a[88:91], a[200:203], v[24:27]\n\t"
"s_waitcnt lgkmcnt(5)\n\t"
"ds_read_b128 a[56:59], v252 offset:2048\n\t"
"v_mfma_f32_16x16x32_bf16 v[24:27], a[80:83], a[196:199], v[24:27]\n\t"
"v_mfma_f32_16x16x32_bf16 v[24:27], a[68:71], a[192:195], v[24:27]\n\t"
"v_mfma_f32_16x16x32_bf16 v[28:31], a[68:71], a[196:199], v[28:31]\n\t"
"s_waitcnt vmcnt(35)\n\t"
"v_mfma_f32_16x16x32_bf16 v[28:31], a[80:83], a[200:203], v[28:31]\n\t"
"v_mfma_f32_16x16x32_bf16 v[28:31], a[88:91], a[204:207], v[28:31]\n\t"
"v_mfma_f32_16x16x32_bf16 v[32:35], a[84:87], a[168:171], v[32:35]\n\t"
"v_mfma_f32_16x16x32_bf16 v[32:35], a[76:79], a[176:179], v[32:35]\n\t"
"v_mfma_f32_16x16x32_bf16 v[32:35], a[96:99], a[172:175], v[32:35]\n\t"
"v_mfma_f32_16x16x32_bf16 v[36:39], a[84:87], a[172:175], v[36:39]\n\t"
"v_mfma_f32_16x16x32_bf16 v[36:39], a[96:99], a[176:179], v[36:39]\n\t"
"v_mfma_f32_16x16x32_bf16 v[36:39], a[76:79], a[180:183], v[36:39]\n\t"
"v_mfma_f32_16x16x32_bf16 v[40:43], a[76:79], a[184:187], v[40:43]\n\t"
"v_mfma_f32_16x16x32_bf16 v[40:43], a[96:99], a[180:183], v[40:43]\n\t"
"v_mfma_f32_16x16x32_bf16 v[40:43], a[84:87], a[176:179], v[40:43]\n\t"
"v_mfma_f32_16x16x32_bf16 v[44:47], a[84:87], a[180:183], v[44:47]\n\t"
"v_mfma_f32_16x16x32_bf16 v[44:47], a[96:99], a[184:187], v[44:47]\n\t"
"v_mfma_f32_16x16x32_bf16 v[44:47], a[76:79], a[188:191], v[44:47]\n\t"
"s_waitcnt lgkmcnt(0)\n\t"
"s_barrier\n\t"
"v_mfma_f32_16x16x32_bf16 v[48:51], a[76:79], a[192:195], v[48:51]\n\t"
"v_mfma_f32_16x16x32_bf16 v[48:51], a[96:99], a[188:191], v[48:51]\n\t"
"v_mfma_f32_16x16x32_bf16 v[48:51], a[84:87], a[184:187], v[48:51]\n\t"
"ds_write_b128 v249, v[212:215] offset:0\n\t"
"v_mfma_f32_16x16x32_bf16 v[52:55], a[84:87], a[188:191], v[52:55]\n\t"
"v_mfma_f32_16x16x32_bf16 v[52:55], a[96:99], a[192:195], v[52:55]\n\t"
"v_mfma_f32_16x16x32_bf16 v[52:55], a[76:79], a[196:199], v[52:55]\n\t"
"ds_write_b64 v250, a[120:121] offset:0\n\t"
"v_mfma_f32_16x16x32_bf16 v[56:59], a[76:79], a[200:203], v[56:59]\n\t"
"v_mfma_f32_16x16x32_bf16 v[56:59], a[96:99], a[196:199], v[56:59]\n\t"
"v_mfma_f32_16x16x32_bf16 v[56:59], a[84:87], a[192:195], v[56:59]\n\t"
"ds_write_b128 v249, v[216:219] offset:6144\n\t"
"v_mfma_f32_16x16x32_bf16 v[60:63], a[84:87], a[196:199], v[60:63]\n\t"
"s_waitcnt vmcnt(32)\n\t"
"v_mfma_f32_16x16x32_bf16 v[60:63], a[96:99], a[200:203], v[60:63]\n\t"
"v_mfma_f32_16x16x32_bf16 v[60:63], a[76:79], a[204:207], v[60:63]\n\t"
"ds_write_b64 v250, a[122:123] offset:6144\n\t"
"v_mfma_f32_16x16x32_bf16 v[64:67], a[108:111], a[168:171], v[64:67]\n\t"
"ds_read_b128 a[168:171], v244 offset:33792\n\t"
"v_mfma_f32_16x16x32_bf16 v[64:67], a[116:119], a[176:179], v[64:67]\n\t"
"v_mfma_f32_16x16x32_bf16 v[64:67], a[92:95], a[172:175], v[64:67]\n\t"
"v_mfma_f32_16x16x32_bf16 v[68:71], a[108:111], a[172:175], v[68:71]\n\t"
"ds_read_b128 a[172:175], v244 offset:34304\n\t"
"v_mfma_f32_16x16x32_bf16 v[68:71], a[92:95], a[176:179], v[68:71]\n\t"
"v_mfma_f32_16x16x32_bf16 v[68:71], a[116:119], a[180:183], v[68:71]\n\t"
"v_mfma_f32_16x16x32_bf16 v[72:75], a[116:119], a[184:187], v[72:75]\n\t"
"v_mfma_f32_16x16x32_bf16 v[72:75], a[92:95], a[180:183], v[72:75]\n\t"
"v_mfma_f32_16x16x32_bf16 v[72:75], a[108:111], a[176:179], v[72:75]\n\t"
"ds_read_b128 a[176:179], v244 offset:34816\n\t"
"v_mfma_f32_16x16x32_bf16 v[76:79], a[108:111], a[180:183], v[76:79]\n\t"
"ds_read_b128 a[180:183], v244 offset:35328\n\t"
"v_mfma_f32_16x16x32_bf16 v[76:79], a[92:95], a[184:187], v[76:79]\n\t"
"ds_read_b128 a[64:67], v252 offset:12288\n\t"
"v_mfma_f32_16x16x32_bf16 v[76:79], a[116:119], a[188:191], v[76:79]\n\t"
"ds_read_b128 a[72:75], v252 offset:13312\n\t"
"v_mfma_f32_16x16x32_bf16 v[80:83], a[116:119], a[192:195], v[80:83]\n\t"
"ds_read_b128 a[80:83], v252 offset:14336\n\t"
"v_mfma_f32_16x16x32_bf16 v[80:83], a[92:95], a[188:191], v[80:83]\n\t"
"v_mfma_f32_16x16x32_bf16 v[80:83], a[108:111], a[184:187], v[80:83]\n\t"
"ds_read_b128 a[184:187], v244 offset:35840\n\t"
"v_mfma_f32_16x16x32_bf16 v[84:87], a[108:111], a[188:191], v[84:87]\n\t"
"ds_read_b128 a[188:191], v244 offset:36352\n\t"
"v_mfma_f32_16x16x32_bf16 v[84:87], a[92:95], a[192:195], v[84:87]\n\t"
"v_mfma_f32_16x16x32_bf16 v[84:87], a[116:119], a[196:199], v[84:87]\n\t"
"v_mfma_f32_16x16x32_bf16 v[88:91], a[116:119], a[200:203], v[88:91]\n\t"
"v_mfma_f32_16x16x32_bf16 v[88:91], a[92:95], a[196:199], v[88:91]\n\t"
"v_mfma_f32_16x16x32_bf16 v[88:91], a[108:111], a[192:195], v[88:91]\n\t"
"ds_read_b128 a[192:195], v244 offset:36864\n\t"
"v_mfma_f32_16x16x32_bf16 v[92:95], a[108:111], a[196:199], v[92:95]\n\t"
"s_waitcnt vmcnt(29) lgkmcnt(7)\n\t"
"ds_read_b128 a[196:199], v244 offset:37376\n\t"
"v_mfma_f32_16x16x32_bf16 v[92:95], a[92:95], a[200:203], v[92:95]\n\t"
"ds_read_b128 a[200:203], v244 offset:37888\n\t"
"v_mfma_f32_16x16x32_bf16 v[92:95], a[116:119], a[204:207], v[92:95]\n\t"
"ds_read_b128 a[204:207], v244 offset:38400\n\t"
"v_mfma_f32_16x16x32_bf16 v[100:103], a[100:103], a[168:171], v[100:103]\n\t"
"s_waitcnt lgkmcnt(9)\n\t"
"v_mfma_f32_16x16x32_bf16 v[100:103], a[132:135], a[176:179], v[100:103]\n\t"
"v_mfma_f32_16x16x32_bf16 v[100:103], a[124:127], a[172:175], v[100:103]\n\t"
"v_mfma_f32_16x16x32_bf16 v[104:107], a[100:103], a[172:175], v[104:107]\n\t"
"s_waitcnt lgkmcnt(5)\n\t"
"v_mfma_f32_16x16x32_bf16 v[104:107], a[124:127], a[176:179], v[104:107]\n\t"
"v_mfma_f32_16x16x32_bf16 v[104:107], a[132:135], a[180:183], v[104:107]\n\t"
"s_waitcnt lgkmcnt(0)\n\t"
"s_barrier\n\t"
"v_mfma_f32_16x16x32_bf16 v[108:111], a[132:135], a[184:187], v[108:111]\n\t"
"v_mfma_f32_16x16x32_bf16 v[108:111], a[124:127], a[180:183], v[108:111]\n\t"
"v_mfma_f32_16x16x32_bf16 v[108:111], a[100:103], a[176:179], v[108:111]\n\t"
"ds_write_b128 v249, a[0:3] offset:12288\n\t"
"v_mfma_f32_16x16x32_bf16 v[112:115], a[100:103], a[180:183], v[112:115]\n\t"
"v_mfma_f32_16x16x32_bf16 v[112:115], a[124:127], a[184:187], v[112:115]\n\t"
"v_mfma_f32_16x16x32_bf16 v[112:115], a[132:135], a[188:191], v[112:115]\n\t"
"s_mov_b32 s20, 1363968\n\t"
"ds_write_b64 v250, a[128:129] offset:12288\n\t"
"v_mfma_f32_16x16x32_bf16 v[116:119], a[132:135], a[192:195], v[116:119]\n\t"
"v_mfma_f32_16x16x32_bf16 v[116:119], a[124:127], a[188:191], v[116:119]\n\t"
"v_mfma_f32_16x16x32_bf16 v[116:119], a[100:103], a[184:187], v[116:119]\n\t"
"ds_write_b128 v249, a[4:7] offset:18432\n\t"
"v_mfma_f32_16x16x32_bf16 v[120:123], a[100:103], a[188:191], v[120:123]\n\t"
"v_mfma_f32_16x16x32_bf16 v[120:123], a[124:127], a[192:195], v[120:123]\n\t"
"v_mfma_f32_16x16x32_bf16 v[120:123], a[132:135], a[196:199], v[120:123]\n\t"
"buffer_load_dwordx4 v[204:207], off, s[80:83], s20 offset:0 sc1\n\t"
"v_mfma_f32_16x16x32_bf16 v[124:127], a[132:135], a[200:203], v[124:127]\n\t"
"v_mfma_f32_16x16x32_bf16 v[124:127], a[124:127], a[196:199], v[124:127]\n\t"
"v_mfma_f32_16x16x32_bf16 v[124:127], a[100:103], a[192:195], v[124:127]\n\t"
"ds_write_b64 v250, a[130:131] offset:18432\n\t"
"v_mfma_f32_16x16x32_bf16 v[128:131], a[100:103], a[196:199], v[128:131]\n\t"
"s_waitcnt vmcnt(27)\n\t"
"v_mfma_f32_16x16x32_bf16 v[128:131], a[124:127], a[200:203], v[128:131]\n\t"
"v_mfma_f32_16x16x32_bf16 v[128:131], a[132:135], a[204:207], v[128:131]\n\t"
"v_mfma_f32_16x16x32_bf16 v[132:135], a[216:219], a[168:171], v[132:135]\n\t"
"v_mfma_f32_16x16x32_bf16 v[132:135], v[240:243], a[176:179], v[132:135]\n\t"
"v_mfma_f32_16x16x32_bf16 v[132:135], a[140:143], a[172:175], v[132:135]\n\t"
"v_mfma_f32_16x16x32_bf16 v[136:139], a[216:219], a[172:175], v[136:139]\n\t"
"v_mfma_f32_16x16x32_bf16 v[136:139], a[140:143], a[176:179], v[136:139]\n\t"
"v_mfma_f32_16x16x32_bf16 v[136:139], v[240:243], a[180:183], v[136:139]\n\t"
"v_mfma_f32_16x16x32_bf16 v[140:143], v[240:243], a[184:187], v[140:143]\n\t"
"v_mfma_f32_16x16x32_bf16 v[140:143], a[140:143], a[180:183], v[140:143]\n\t"
"v_mfma_f32_16x16x32_bf16 v[140:143], a[216:219], a[176:179], v[140:143]\n\t"
"v_mfma_f32_16x16x32_bf16 v[144:147], a[216:219], a[180:183], v[144:147]\n\t"
"v_mfma_f32_16x16x32_bf16 v[144:147], a[140:143], a[184:187], v[144:147]\n\t"
"v_mfma_f32_16x16x32_bf16 v[144:147], v[240:243], a[188:191], v[144:147]\n\t"
"v_mfma_f32_16x16x32_bf16 v[148:151], v[240:243], a[192:195], v[148:151]\n\t"
"v_mfma_f32_16x16x32_bf16 v[148:151], a[140:143], a[188:191], v[148:151]\n\t"
"v_mfma_f32_16x16x32_bf16 v[148:151], a[216:219], a[184:187], v[148:151]\n\t"
"ds_read_b128 a[88:91], v252 offset:0\n\t"
"v_mfma_f32_16x16x32_bf16 v[152:155], a[216:219], a[188:191], v[152:155]\n\t"
"v_mfma_f32_16x16x32_bf16 v[152:155], a[140:143], a[192:195], v[152:155]\n\t"
"ds_read_b128 a[96:99], v252 offset:1024\n\t"
"v_mfma_f32_16x16x32_bf16 v[152:155], v[240:243], a[196:199], v[152:155]\n\t"
"v_mfma_f32_16x16x32_bf16 v[156:159], v[240:243], a[200:203], v[156:159]\n\t"
"ds_read_b128 a[108:111], v252 offset:2048\n\t"
"v_mfma_f32_16x16x32_bf16 v[156:159], a[140:143], a[196:199], v[156:159]\n\t"
"v_mfma_f32_16x16x32_bf16 v[156:159], a[216:219], a[192:195], v[156:159]\n\t"
"v_mfma_f32_16x16x32_bf16 v[160:163], a[216:219], a[196:199], v[160:163]\n\t"
"s_waitcnt vmcnt(24)\n\t"
"v_mfma_f32_16x16x32_bf16 v[160:163], a[140:143], a[200:203], v[160:163]\n\t"
"v_mfma_f32_16x16x32_bf16 v[160:163], v[240:243], a[204:207], v[160:163]\n\t"
"v_mfma_f32_16x16x32_bf16 v[164:167], a[148:151], a[168:171], v[164:167]\n\t"
"ds_read_b128 a[168:171], v222 offset:3072\n\t"
"v_mfma_f32_16x16x32_bf16 v[164:167], a[156:159], a[176:179], v[164:167]\n\t"
"v_mfma_f32_16x16x32_bf16 v[164:167], a[220:223], a[172:175], v[164:167]\n\t"
"v_mfma_f32_16x16x32_bf16 v[168:171], a[148:151], a[172:175], v[168:171]\n\t"
"ds_read_b128 a[172:175], v223 offset:9216\n\t"
"v_mfma_f32_16x16x32_bf16 v[168:171], a[220:223], a[176:179], v[168:171]\n\t"
"buffer_load_dwordx4 v[212:215], off, s[80:83], s20 offset:1024 sc1\n\t"
"v_mfma_f32_16x16x32_bf16 v[168:171], a[156:159], a[180:183], v[168:171]\n\t"
"v_mfma_f32_16x16x32_bf16 v[172:175], a[156:159], a[184:187], v[172:175]\n\t"
"v_mfma_f32_16x16x32_bf16 v[172:175], a[220:223], a[180:183], v[172:175]\n\t"
"v_mfma_f32_16x16x32_bf16 v[172:175], a[148:151], a[176:179], v[172:175]\n\t"
"ds_read_b128 a[176:179], v223 offset:15360\n\t"
"v_mfma_f32_16x16x32_bf16 v[176:179], a[148:151], a[180:183], v[176:179]\n\t"
"buffer_load_dwordx4 v[240:243], off, s[80:83], s20 offset:2048 sc1\n\t"
"v_mfma_f32_16x16x32_bf16 v[176:179], a[220:223], a[184:187], v[176:179]\n\t"
"s_mov_b32 s20, 1376256\n\t"
"ds_read_b128 a[180:183], v222 offset:23040\n\t"
"v_mfma_f32_16x16x32_bf16 v[176:179], a[156:159], a[188:191], v[176:179]\n\t"
"s_waitcnt lgkmcnt(4)\n\t"
"s_barrier\n\t"
"v_mfma_f32_16x16x32_bf16 v[180:183], a[156:159], a[192:195], v[180:183]\n\t"
"v_mfma_f32_16x16x32_bf16 v[180:183], a[220:223], a[188:191], v[180:183]\n\t"
"buffer_load_dwordx4 a[104:107], off, s[80:83], s20 offset:0 sc1\n\t"
"v_mfma_f32_16x16x32_bf16 v[180:183], a[148:151], a[184:187], v[180:183]\n\t"
"ds_read_b128 a[184:187], v223 offset:29184\n\t"
"v_mfma_f32_16x16x32_bf16 v[184:187], a[148:151], a[188:191], v[184:187]\n\t"
"buffer_load_dwordx4 a[128:131], off, s[80:83], s20 offset:1024 sc1\n\t"
"v_mfma_f32_16x16x32_bf16 v[184:187], a[220:223], a[192:195], v[184:187]\n\t"
"ds_read_b128 a[188:191], v223 offset:35328\n\t"
"v_mfma_f32_16x16x32_bf16 v[184:187], a[156:159], a[196:199], v[184:187]\n\t"
"ds_write_b128 v249, a[8:11] offset:0\n\t"
"v_mfma_f32_16x16x32_bf16 v[188:191], a[156:159], a[200:203], v[188:191]\n\t"
"buffer_load_dwordx4 a[112:115], off, s[80:83], s20 offset:2048 sc1\n\t"
"v_mfma_f32_16x16x32_bf16 v[188:191], a[220:223], a[196:199], v[188:191]\n\t"
"s_mov_b32 s20, 1388544\n\t"
"v_mfma_f32_16x16x32_bf16 v[188:191], a[148:151], a[192:195], v[188:191]\n\t"
"ds_read_b128 a[192:195], v222 offset:43008\n\t"
"v_mfma_f32_16x16x32_bf16 v[192:195], a[148:151], a[196:199], v[192:195]\n\t"
"buffer_load_dwordx4 a[132:135], off, s[80:83], s20 offset:0 sc1\n\t"
"v_mfma_f32_16x16x32_bf16 v[192:195], a[220:223], a[200:203], v[192:195]\n\t"
"s_waitcnt vmcnt(27) lgkmcnt(5)\n\t"
"ds_read_b128 a[196:199], v223 offset:49152\n\t"
"v_mfma_f32_16x16x32_bf16 v[192:195], a[156:159], a[204:207], v[192:195]\n\t"
"ds_read_b128 a[200:203], v223 offset:55296\n\t"
"Lst16_%=:\n\t"
"v_mfma_f32_16x16x32_bf16 v[0:3], a[232:235], a[168:171], v[0:3]\n\t"
"s_waitcnt lgkmcnt(6)\n\t"
"ds_read_b128 a[204:207], v222 offset:62976\n\t"
"v_mfma_f32_16x16x32_bf16 v[0:3], a[236:239], a[176:179], v[0:3]\n\t"
"buffer_load_dwordx4 a[140:143], off, s[80:83], s20 offset:1024 sc1\n\t"
"v_mfma_f32_16x16x32_bf16 v[0:3], a[164:167], a[172:175], v[0:3]\n\t"
"ds_write_b64 v250, a[136:137] offset:0\n\t"
"v_mfma_f32_16x16x32_bf16 v[4:7], a[232:235], a[172:175], v[4:7]\n\t"
"s_waitcnt lgkmcnt(7)\n\t"
"v_mfma_f32_16x16x32_bf16 v[4:7], a[164:167], a[176:179], v[4:7]\n\t"
"v_mfma_f32_16x16x32_bf16 v[4:7], a[236:239], a[180:183], v[4:7]\n\t"
"buffer_load_dwordx4 a[148:151], off, s[80:83], s20 offset:2048 sc1\n\t"
"v_mfma_f32_16x16x32_bf16 v[8:11], a[236:239], a[184:187], v[8:11]\n\t"
"s_waitcnt lgkmcnt(6)\n\t"
"s_mov_b32 s20, 1474560\n\t"
"v_mfma_f32_16x16x32_bf16 v[8:11], a[164:167], a[180:183], v[8:11]\n\t"
"v_mfma_f32_16x16x32_bf16 v[8:11], a[232:235], a[176:179], v[8:11]\n\t"
"ds_write_b128 v249, a[12:15] offset:6144\n\t"
"v_mfma_f32_16x16x32_bf16 v[12:15], a[232:235], a[180:183], v[12:15]\n\t"
"buffer_load_dwordx4 a[60:63], off, s[80:83], s20 offset:0 sc1\n\t"
"v_mfma_f32_16x16x32_bf16 v[12:15], a[164:167], a[184:187], v[12:15]\n\t"
"s_waitcnt lgkmcnt(5)\n\t"
"v_mfma_f32_16x16x32_bf16 v[12:15], a[236:239], a[188:191], v[12:15]\n\t"
"ds_write_b64 v250, a[138:139] offset:6144\n\t"
"v_mfma_f32_16x16x32_bf16 v[16:19], a[236:239], a[192:195], v[16:19]\n\t"
"s_waitcnt lgkmcnt(5)\n\t"
"v_mfma_f32_16x16x32_bf16 v[16:19], a[164:167], a[188:191], v[16:19]\n\t"
"buffer_load_dwordx4 a[68:71], off, s[80:83], s20 offset:1024 sc1\n\t"
"v_mfma_f32_16x16x32_bf16 v[16:19], a[232:235], a[184:187], v[16:19]\n\t"
"ds_read_b128 a[116:119], v252 offset:12288\n\t"
"v_mfma_f32_16x16x32_bf16 v[20:23], a[232:235], a[188:191], v[20:23]\n\t"
"s_waitcnt lgkmcnt(5)\n\t"
"v_mfma_f32_16x16x32_bf16 v[20:23], a[164:167], a[192:195], v[20:23]\n\t"
"ds_read_b128 a[124:127], v252 offset:13312\n\t"
"v_mfma_f32_16x16x32_bf16 v[20:23], a[236:239], a[196:199], v[20:23]\n\t"
"buffer_load_dwordx4 a[76:79], off, s[80:83], s20 offset:2048 sc1\n\t"
"v_mfma_f32_16x16x32_bf16 v[24:27], a[236:239], a[200:203], v[24:27]\n\t"
"s_waitcnt lgkmcnt(5)\n\t"
"ds_read_b128 a[156:159], v252 offset:14336\n\t"
"s_mov_b32 s20, 1486848\n\t"
"v_mfma_f32_16x16x32_bf16 v[24:27], a[164:167], a[196:199], v[24:27]\n\t"
"v_mfma_f32_16x16x32_bf16 v[24:27], a[232:235], a[192:195], v[24:27]\n\t"
"v_mfma_f32_16x16x32_bf16 v[28:31], a[232:235], a[196:199], v[28:31]\n\t"
"buffer_load_dwordx4 a[92:95], off, s[80:83], s20 offset:0 sc1\n\t"
"v_mfma_f32_16x16x32_bf16 v[28:31], a[164:167], a[200:203], v[28:31]\n\t"
"s_waitcnt vmcnt(30)\n\t"
"v_mfma_f32_16x16x32_bf16 v[28:31], a[236:239], a[204:207], v[28:31]\n\t"
"v_mfma_f32_16x16x32_bf16 v[32:35], v[236:239], a[168:171], v[32:35]\n\t"
"v_mfma_f32_16x16x32_bf16 v[32:35], a[208:211], a[176:179], v[32:35]\n\t"
"buffer_load_dwordx4 a[164:167], off, s[80:83], s20 offset:1024 sc1\n\t"
"v_mfma_f32_16x16x32_bf16 v[32:35], v[96:99], a[172:175], v[32:35]\n\t"
"v_mfma_f32_16x16x32_bf16 v[36:39], v[236:239], a[172:175], v[36:39]\n\t"
"v_mfma_f32_16x16x32_bf16 v[36:39], v[96:99], a[176:179], v[36:39]\n\t"
"v_mfma_f32_16x16x32_bf16 v[36:39], a[208:211], a[180:183], v[36:39]\n\t"
"buffer_load_dwordx4 a[100:103], off, s[80:83], s20 offset:2048 sc1\n\t"
"v_mfma_f32_16x16x32_bf16 v[40:43], a[208:211], a[184:187], v[40:43]\n\t"
"s_mov_b32 s20, 1499136\n\t"
"v_mfma_f32_16x16x32_bf16 v[40:43], v[96:99], a[180:183], v[40:43]\n\t"
"v_mfma_f32_16x16x32_bf16 v[40:43], v[236:239], a[176:179], v[40:43]\n\t"
"v_mfma_f32_16x16x32_bf16 v[44:47], v[236:239], a[180:183], v[44:47]\n\t"
"buffer_load_dwordx4 v[200:203], off, s[80:83], s20 offset:0 sc1\n\t"
"v_mfma_f32_16x16x32_bf16 v[44:47], v[96:99], a[184:187], v[44:47]\n\t"
"v_mfma_f32_16x16x32_bf16 v[44:47], a[208:211], a[188:191], v[44:47]\n\t"
"s_waitcnt lgkmcnt(0)\n\t"
"s_barrier\n\t"
"v_mfma_f32_16x16x32_bf16 v[48:51], a[208:211], a[192:195], v[48:51]\n\t"
"v_mfma_f32_16x16x32_bf16 v[48:51], v[96:99], a[188:191], v[48:51]\n\t"
"buffer_load_dwordx4 a[120:123], off, s[80:83], s20 offset:1024 sc1\n\t"
"v_mfma_f32_16x16x32_bf16 v[48:51], v[236:239], a[184:187], v[48:51]\n\t"
"ds_write_b128 v249, a[16:19] offset:12288\n\t"
"v_mfma_f32_16x16x32_bf16 v[52:55], v[236:239], a[188:191], v[52:55]\n\t"
"buffer_load_dwordx4 a[12:15], off, s[80:83], s20 offset:2048 sc1\n\t"
"v_mfma_f32_16x16x32_bf16 v[52:55], v[96:99], a[192:195], v[52:55]\n\t"
"s_mov_b32 s20, 1658880\n\t"
"v_mfma_f32_16x16x32_bf16 v[52:55], a[208:211], a[196:199], v[52:55]\n\t"
"ds_write_b64 v250, a[144:145] offset:12288\n\t"
"v_mfma_f32_16x16x32_bf16 v[56:59], a[208:211], a[200:203], v[56:59]\n\t"
"buffer_load_dwordx4 a[136:139], off, s[80:83], s20 offset:0 sc1\n\t"
"v_mfma_f32_16x16x32_bf16 v[56:59], v[96:99], a[196:199], v[56:59]\n\t"
"v_mfma_f32_16x16x32_bf16 v[56:59], v[236:239], a[192:195], v[56:59]\n\t"
"ds_write_b128 v249, a[20:23] offset:18432\n\t"
"v_mfma_f32_16x16x32_bf16 v[60:63], v[236:239], a[196:199], v[60:63]\n\t"
"s_waitcnt vmcnt(26)\n\t"
"v_mfma_f32_16x16x32_bf16 v[60:63], v[96:99], a[200:203], v[60:63]\n\t"
"buffer_load_dwordx4 v[96:99], off, s[80:83], s20 offset:1024 sc1\n\t"
"v_mfma_f32_16x16x32_bf16 v[60:63], a[208:211], a[204:207], v[60:63]\n\t"
"ds_write_b64 v250, a[146:147] offset:18432\n\t"
"v_mfma_f32_16x16x32_bf16 v[64:67], a[212:215], a[168:171], v[64:67]\n\t"
"ds_read_b128 a[168:171], v222 offset:3328\n\t"
"v_mfma_f32_16x16x32_bf16 v[64:67], a[44:47], a[176:179], v[64:67]\n\t"
"v_mfma_f32_16x16x32_bf16 v[64:67], a[224:227], a[172:175], v[64:67]\n\t"
"buffer_load_dwordx4 v[208:211], off, s[80:83], s20 offset:2048 sc1\n\t"
"v_mfma_f32_16x16x32_bf16 v[68:71], a[212:215], a[172:175], v[68:71]\n\t"
"s_mov_b32 s20, 1671168\n\t"
"ds_read_b128 a[172:175], v223 offset:9472\n\t"
"v_mfma_f32_16x16x32_bf16 v[68:71], a[224:227], a[176:179], v[68:71]\n\t"
"v_mfma_f32_16x16x32_bf16 v[68:71], a[44:47], a[180:183], v[68:71]\n\t"
"v_mfma_f32_16x16x32_bf16 v[72:75], a[44:47], a[184:187], v[72:75]\n\t"
"buffer_load_dwordx4 v[216:219], off, s[80:83], s20 offset:0 sc1\n\t"
"v_mfma_f32_16x16x32_bf16 v[72:75], a[224:227], a[180:183], v[72:75]\n\t"
"v_mfma_f32_16x16x32_bf16 v[72:75], a[212:215], a[176:179], v[72:75]\n\t"
"ds_read_b128 a[176:179], v223 offset:15616\n\t"
"v_mfma_f32_16x16x32_bf16 v[76:79], a[212:215], a[180:183], v[76:79]\n\t"
"ds_read_b128 a[180:183], v222 offset:23296\n\t"
"v_mfma_f32_16x16x32_bf16 v[76:79], a[224:227], a[184:187], v[76:79]\n\t"
"buffer_load_dwordx4 a[4:7], off, s[80:83], s20 offset:1024 sc1\n\t"
"v_mfma_f32_16x16x32_bf16 v[76:79], a[44:47], a[188:191], v[76:79]\n\t"
"ds_read_b128 a[52:55], v252 offset:0\n\t"
"v_mfma_f32_16x16x32_bf16 v[80:83], a[44:47], a[192:195], v[80:83]\n\t"
"ds_read_b128 a[84:87], v252 offset:1024\n\t"
"v_mfma_f32_16x16x32_bf16 v[80:83], a[224:227], a[188:191], v[80:83]\n\t"
"Lpg12_%=:\n\t"
"s_cbranch_vccnz Lvr0_%=\n\t"
"ds_read_b128 v[196:199], v252 offset:2048\n\t"
"v_mfma_f32_16x16x32_bf16 v[80:83], a[212:215], a[184:187], v[80:83]\n\t"
"ds_read_b128 a[184:187], v223 offset:29440\n\t"
"v_mfma_f32_16x16x32_bf16 v[84:87], a[212:215], a[188:191], v[84:87]\n\t"
"buffer_load_dwordx4 a[208:211], off, s[80:83], s20 offset:2048 sc1\n\t"
"v_mfma_f32_16x16x32_bf16 v[84:87], a[224:227], a[192:195], v[84:87]\n\t"
"s_mov_b32 s20, 1683456\n\t"
"ds_read_b128 a[188:191], v223 offset:35584\n\t"
"v_mfma_f32_16x16x32_bf16 v[84:87], a[44:47], a[196:199], v[84:87]\n\t"
"v_mfma_f32_16x16x32_bf16 v[88:91], a[44:47], a[200:203], v[88:91]\n\t"
"buffer_load_dwordx4 a[216:219], off, s[80:83], s20 offset:0 sc1\n\t"
"v_mfma_f32_16x16x32_bf16 v[88:91], a[224:227], a[196:199], v[88:91]\n\t"
"s_add_u32 s29, s22, 384\n\t"
"v_mfma_f32_16x16x32_bf16 v[88:91], a[212:215], a[192:195], v[88:91]\n\t"
"ds_read_b128 a[192:195], v222 offset:43264\n\t"
"v_mfma_f32_16x16x32_bf16 v[92:95], a[212:215], a[196:199], v[92:95]\n\t"
"s_waitcnt vmcnt(22) lgkmcnt(7)\n\t"
"ds_read_b128 a[196:199], v223 offset:49408\n\t"
"v_mfma_f32_16x16x32_bf16 v[92:95], a[224:227], a[200:203], v[92:95]\n\t"
"ds_read_b128 a[200:203], v223 offset:55552\n\t"
"v_mfma_f32_16x16x32_bf16 v[92:95], a[44:47], a[204:207], v[92:95]\n\t"
"ds_read_b128 a[204:207], v222 offset:63232\n\t"
"v_mfma_f32_16x16x32_bf16 v[100:103], v[204:207], a[168:171], v[100:103]\n\t"
"buffer_load_dwordx4 a[212:215], off, s[80:83], s20 offset:1024 sc1\n\t"
"v_mfma_f32_16x16x32_bf16 v[100:103], v[240:243], a[176:179], v[100:103]\n\t"
"s_waitcnt lgkmcnt(9)\n\t"
"v_mfma_f32_16x16x32_bf16 v[100:103], v[212:215], a[172:175], v[100:103]\n\t"
"buffer_store_dwordx4 a[40:43], v248, s[16:19], s29 offen offset:0 nt\n\t"
"v_mfma_f32_16x16x32_bf16 v[104:107], v[204:207], a[172:175], v[104:107]\n\t"
"s_waitcnt lgkmcnt(5)\n\t"
"v_mfma_f32_16x16x32_bf16 v[104:107], v[212:215], a[176:179], v[104:107]\n\t"
"v_mfma_f32_16x16x32_bf16 v[104:107], v[240:243], a[180:183], v[104:107]\n\t"
"s_waitcnt lgkmcnt(0)\n\t"
"s_barrier\n\t"
"v_mfma_f32_16x16x32_bf16 v[108:111], v[240:243], a[184:187], v[108:111]\n\t"
"v_mfma_f32_16x16x32_bf16 v[108:111], v[212:215], a[180:183], v[108:111]\n\t"
"buffer_load_dwordx4 a[220:223], off, s[80:83], s20 offset:2048 sc1\n\t"
"v_mfma_f32_16x16x32_bf16 v[108:111], v[204:207], a[176:179], v[108:111]\n\t"
"s_mov_b32 s20, 0\n\t"
"ds_write_b128 v249, a[24:27] offset:0\n\t"
"v_mfma_f32_16x16x32_bf16 v[112:115], v[204:207], a[180:183], v[112:115]\n\t"
"buffer_load_dwordx4 a[144:147], off, s[80:83], s20 offset:0 sc1\n\t"
"v_mfma_f32_16x16x32_bf16 v[112:115], v[212:215], a[184:187], v[112:115]\n\t"
"v_mfma_f32_16x16x32_bf16 v[112:115], v[240:243], a[188:191], v[112:115]\n\t"
"buffer_load_dwordx4 a[20:23], off, s[80:83], s20 offset:1024 sc1\n\t"
"v_mfma_f32_16x16x32_bf16 v[116:119], v[240:243], a[192:195], v[116:119]\n\t"
"v_mfma_f32_16x16x32_bf16 v[116:119], v[212:215], a[188:191], v[116:119]\n\t"
"v_mfma_f32_16x16x32_bf16 v[116:119], v[204:207], a[184:187], v[116:119]\n\t"
"buffer_store_dwordx4 a[48:51], v248, s[16:19], s29 offen offset:128 nt\n\t"
"ds_write_b64 v250, a[152:153] offset:0\n\t"
"v_mfma_f32_16x16x32_bf16 v[120:123], v[204:207], a[188:191], v[120:123]\n\t"
"v_mfma_f32_16x16x32_bf16 v[120:123], v[212:215], a[192:195], v[120:123]\n\t"
"v_mfma_f32_16x16x32_bf16 v[120:123], v[240:243], a[196:199], v[120:123]\n\t"
"ds_write_b128 v249, a[28:31] offset:6144\n\t"
"v_mfma_f32_16x16x32_bf16 v[124:127], v[240:243], a[200:203], v[124:127]\n\t"
"v_mfma_f32_16x16x32_bf16 v[124:127], v[212:215], a[196:199], v[124:127]\n\t"
"buffer_load_dwordx4 a[224:227], off, s[80:83], s20 offset:2048 sc1\n\t"
"v_mfma_f32_16x16x32_bf16 v[124:127], v[204:207], a[192:195], v[124:127]\n\t"
"s_mov_b32 s20, 12288\n\t"
"ds_write_b64 v250, a[154:155] offset:6144\n\t"
"v_mfma_f32_16x16x32_bf16 v[128:131], v[204:207], a[196:199], v[128:131]\n\t"
"s_waitcnt vmcnt(26)\n\t"
"v_mfma_f32_16x16x32_bf16 v[128:131], v[212:215], a[200:203], v[128:131]\n\t"
"buffer_load_dwordx4 a[228:231], off, s[80:83], s20 offset:0 sc1\n\t"
"v_mfma_f32_16x16x32_bf16 v[128:131], v[240:243], a[204:207], v[128:131]\n\t"
"v_mfma_f32_16x16x32_bf16 v[132:135], a[104:107], a[168:171], v[132:135]\n\t"
"v_mfma_f32_16x16x32_bf16 v[132:135], a[112:115], a[176:179], v[132:135]\n\t"
"v_mfma_f32_16x16x32_bf16 v[132:135], a[128:131], a[172:175], v[132:135]\n\t"
"buffer_store_dwordx4 a[56:59], v248, s[16:19], s29 offen offset:256 nt\n\t"
"v_mfma_f32_16x16x32_bf16 v[136:139], a[104:107], a[172:175], v[136:139]\n\t"
"v_mfma_f32_16x16x32_bf16 v[136:139], a[128:131], a[176:179], v[136:139]\n\t"
"v_mfma_f32_16x16x32_bf16 v[136:139], a[112:115], a[180:183], v[136:139]\n\t"
"v_mfma_f32_16x16x32_bf16 v[140:143], a[112:115], a[184:187], v[140:143]\n\t"
"buffer_load_dwordx4 a[232:235], off, s[80:83], s20 offset:1024 sc1\n\t"
"v_mfma_f32_16x16x32_bf16 v[140:143], a[128:131], a[180:183], v[140:143]\n\t"
"v_mfma_f32_16x16x32_bf16 v[140:143], a[104:107], a[176:179], v[140:143]\n\t"
"s_add_u32 s29, s22, 196992\n\t"
"v_mfma_f32_16x16x32_bf16 v[144:147], a[104:107], a[180:183], v[144:147]\n\t"
"v_mfma_f32_16x16x32_bf16 v[144:147], a[128:131], a[184:187], v[144:147]\n\t"
"buffer_load_dwordx4 a[236:239], off, s[80:83], s20 offset:2048 sc1\n\t"
"v_mfma_f32_16x16x32_bf16 v[144:147], a[112:115], a[188:191], v[144:147]\n\t"
"v_mfma_f32_16x16x32_bf16 v[148:151], a[112:115], a[192:195], v[148:151]\n\t"
"s_mov_b32 s20, 24576\n\t"
"v_mfma_f32_16x16x32_bf16 v[148:151], a[128:131], a[188:191], v[148:151]\n\t"
"v_mfma_f32_16x16x32_bf16 v[148:151], a[104:107], a[184:187], v[148:151]\n\t"
"buffer_store_dwordx4 a[64:67], v248, s[16:19], s29 offen offset:0 nt\n\t"
"ds_read_b128 v[204:207], v252 offset:12288\n\t"
"v_mfma_f32_16x16x32_bf16 v[152:155], a[104:107], a[188:191], v[152:155]\n\t"
"v_mfma_f32_16x16x32_bf16 v[152:155], a[128:131], a[192:195], v[152:155]\n\t"
"ds_read_b128 v[212:215], v252 offset:13312\n\t"
"v_mfma_f32_16x16x32_bf16 v[152:155], a[112:115], a[196:199], v[152:155]\n\t"
"v_mfma_f32_16x16x32_bf16 v[156:159], a[112:115], a[200:203], v[156:159]\n\t"
"buffer_load_dwordx4 a[240:243], off, s[80:83], s20 offset:0 sc1\n\t"
"v_mfma_f32_16x16x32_bf16 v[156:159], a[128:131], a[196:199], v[156:159]\n\t"
"ds_read_b128 a[0:3], v252 offset:14336\n\t"
"v_mfma_f32_16x16x32_bf16 v[156:159], a[104:107], a[192:195], v[156:159]\n\t"
"v_mfma_f32_16x16x32_bf16 v[160:163], a[104:107], a[196:199], v[160:163]\n\t"
"s_waitcnt vmcnt(29)\n\t"
"v_mfma_f32_16x16x32_bf16 v[160:163], a[128:131], a[200:203], v[160:163]\n\t"
"buffer_load_dwordx4 a[244:247], off, s[80:83], s20 offset:1024 sc1\n\t"
"v_mfma_f32_16x16x32_bf16 v[160:163], a[112:115], a[204:207], v[160:163]\n\t"
"global_load_dwordx4 v[240:243], v252, s[48:49] offset:2048\n\t"
"v_mfma_f32_16x16x32_bf16 v[164:167], a[132:135], a[168:171], v[164:167]\n\t"
"ds_read_b128 a[168:171], v222 offset:1536\n\t"
"v_mfma_f32_16x16x32_bf16 v[164:167], a[148:151], a[176:179], v[164:167]\n\t"
"buffer_load_dwordx4 a[248:251], off, s[80:83], s20 offset:2048 sc1\n\t"
"v_mfma_f32_16x16x32_bf16 v[164:167], a[140:143], a[172:175], v[164:167]\n\t"
"buffer_store_dwordx4 a[72:75], v248, s[16:19], s29 offen offset:128 nt\n\t"
"v_mfma_f32_16x16x32_bf16 v[168:171], a[132:135], a[172:175], v[168:171]\n\t"
"s_mov_b32 s20, 184320\n\t"
"ds_read_b128 a[172:175], v223 offset:7680\n\t"
"v_mfma_f32_16x16x32_bf16 v[168:171], a[140:143], a[176:179], v[168:171]\n\t"
"global_load_dwordx4 a[128:131], v252, s[46:47] offset:1024\n\t"
"v_mfma_f32_16x16x32_bf16 v[168:171], a[148:151], a[180:183], v[168:171]\n\t"
"v_mfma_f32_16x16x32_bf16 v[172:175], a[148:151], a[184:187], v[172:175]\n\t"
"v_mfma_f32_16x16x32_bf16 v[172:175], a[140:143], a[180:183], v[172:175]\n\t"
"v_mfma_f32_16x16x32_bf16 v[172:175], a[132:135], a[176:179], v[172:175]\n\t"
"ds_read_b128 a[176:179], v223 offset:13824\n\t"
"v_mfma_f32_16x16x32_bf16 v[176:179], a[132:135], a[180:183], v[176:179]\n\t"
"buffer_load_dwordx4 a[252:255], off, s[80:83], s20 offset:0 sc1\n\t"
"v_mfma_f32_16x16x32_bf16 v[176:179], a[140:143], a[184:187], v[176:179]\n\t"
"ds_read_b128 a[180:183], v222 offset:21504\n\t"
"v_mfma_f32_16x16x32_bf16 v[176:179], a[148:151], a[188:191], v[176:179]\n\t"
"s_waitcnt lgkmcnt(4)\n\t"
"s_barrier\n\t"
"v_mfma_f32_16x16x32_bf16 v[180:183], a[148:151], a[192:195], v[180:183]\n\t"
"v_mfma_f32_16x16x32_bf16 v[180:183], a[140:143], a[188:191], v[180:183]\n\t"
"buffer_load_dwordx4 a[152:155], off, s[80:83], s20 offset:1024 sc1\n\t"
"v_mfma_f32_16x16x32_bf16 v[180:183], a[132:135], a[184:187], v[180:183]\n\t"
"ds_read_b128 a[184:187], v223 offset:27648\n\t"
"v_mfma_f32_16x16x32_bf16 v[184:187], a[132:135], a[188:191], v[184:187]\n\t"
"ds_read_b128 a[188:191], v223 offset:33792\n\t"
"v_mfma_f32_16x16x32_bf16 v[184:187], a[140:143], a[192:195], v[184:187]\n\t"
"v_mfma_f32_16x16x32_bf16 v[184:187], a[148:151], a[196:199], v[184:187]\n\t"
"buffer_store_dwordx4 a[80:83], v248, s[16:19], s29 offen offset:256 nt\n\t"
"ds_write_b64 v250, a[160:161] offset:12288\n\t"
"v_mfma_f32_16x16x32_bf16 v[188:191], a[148:151], a[200:203], v[188:191]\n\t"
"v_mfma_f32_16x16x32_bf16 v[188:191], a[140:143], a[196:199], v[188:191]\n\t"
"buffer_load_dwordx4 a[28:31], off, s[80:83], s20 offset:2048 sc1\n\t"
"v_mfma_f32_16x16x32_bf16 v[188:191], a[132:135], a[192:195], v[188:191]\n\t"
"ds_read_b128 a[192:195], v222 offset:41472\n\t"
"v_mfma_f32_16x16x32_bf16 v[192:195], a[132:135], a[196:199], v[192:195]\n\t"
"s_waitcnt vmcnt(35) lgkmcnt(5)\n\t"
"ds_read_b128 a[196:199], v223 offset:47616\n\t"
"v_mfma_f32_16x16x32_bf16 v[192:195], a[140:143], a[200:203], v[192:195]\n\t"
"s_add_u32 s29, s22, 393600\n\t"
"ds_read_b128 a[200:203], v223 offset:53760\n\t"
"v_mfma_f32_16x16x32_bf16 v[192:195], a[148:151], a[204:207], v[192:195]\n\t"
"ds_write_b128 v249, a[32:35] offset:12288\n\t"
"Lst17_%=:\n\t"
"v_mfma_f32_16x16x32_bf16 v[0:3], a[60:63], a[168:171], v[0:3]\n\t"
"s_waitcnt lgkmcnt(7)\n\t"
"v_mfma_f32_16x16x32_bf16 v[0:3], a[76:79], a[176:179], v[0:3]\n\t"
"ds_read_b128 a[204:207], v222 offset:61440\n\t"
"v_mfma_f32_16x16x32_bf16 v[0:3], a[68:71], a[172:175], v[0:3]\n\t"
"ds_write_b128 v249, a[36:39] offset:18432\n\t"
"v_mfma_f32_16x16x32_bf16 v[4:7], a[60:63], a[172:175], v[4:7]\n\t"
"s_waitcnt lgkmcnt(8)\n\t"
"v_mfma_f32_16x16x32_bf16 v[4:7], a[68:71], a[176:179], v[4:7]\n\t"
"s_waitcnt vmcnt(7)\n\t"
"v_add_u32 v240, 59904, v240\n\t"
"v_add_u32 v241, 59904, v241\n\t"
"v_mfma_f32_16x16x32_bf16 v[4:7], a[76:79], a[180:183], v[4:7]\n\t"
"ds_write_b64 v250, a[162:163] offset:18432\n\t"
"v_mfma_f32_16x16x32_bf16 v[8:11], a[76:79], a[184:187], v[8:11]\n\t"
"s_waitcnt lgkmcnt(8)\n\t"
"v_cvt_pk_bf16_f32 v0, v0, v1\n\t"
"v_cvt_pk_bf16_f32 v2, v2, v3\n\t"
"v_mfma_f32_16x16x32_bf16 v[8:11], a[68:71], a[180:183], v[8:11]\n\t"
"v_accvgpr_write_b32 a40, v0\n\t"
"v_accvgpr_write_b32 a41, v2\n\t"
"v_mfma_f32_16x16x32_bf16 v[8:11], a[60:63], a[176:179], v[8:11]\n\t"
"ds_read_b64_tr_b16 v[228:229], v240 offset:20480\n\t"
"v_mfma_f32_16x16x32_bf16 v[12:15], a[60:63], a[180:183], v[12:15]\n\t"
"s_waitcnt lgkmcnt(7)\n\t"
"ds_read_b64_tr_b16 v[230:231], v240 offset:22016\n\t"
"v_cvt_pk_bf16_f32 v4, v4, v5\n\t"
"v_mfma_f32_16x16x32_bf16 v[12:15], a[68:71], a[184:187], v[12:15]\n\t"
"ds_read_b64_tr_b16 v[232:233], v241 offset:20480\n\t"
"v_cvt_pk_bf16_f32 v6, v6, v7\n\t"
"v_mfma_f32_16x16x32_bf16 v[12:15], a[76:79], a[188:191], v[12:15]\n\t"
"buffer_store_dwordx4 a[88:91], v248, s[16:19], s29 offen offset:0 nt\n\t"
"ds_read_b64_tr_b16 v[234:235], v241 offset:22016\n\t"
"ds_read_b64_tr_b16 v[224:225], v240 offset:40448\n\t"
"v_accvgpr_write_b32 a48, v4\n\t"
"v_mfma_f32_16x16x32_bf16 v[16:19], a[76:79], a[192:195], v[16:19]\n\t"
"s_waitcnt lgkmcnt(10)\n\t"
"ds_read_b64_tr_b16 v[226:227], v240 offset:41984\n\t"
"v_cvt_pk_bf16_f32 v8, v8, v9\n\t"
"v_mfma_f32_16x16x32_bf16 v[16:19], a[68:71], a[188:191], v[16:19]\n\t"
"ds_read_b64_tr_b16 v[236:237], v241 offset:40448\n\t"
"v_cvt_pk_bf16_f32 v10, v10, v11\n\t"
"v_mfma_f32_16x16x32_bf16 v[16:19], a[60:63], a[184:187], v[16:19]\n\t"
"ds_read_b64_tr_b16 v[238:239], v241 offset:41984\n\t"
"v_accvgpr_write_b32 a49, v6\n\t"
"v_mfma_f32_16x16x32_bf16 v[20:23], a[60:63], a[188:191], v[20:23]\n\t"
"s_waitcnt lgkmcnt(12)\n\t"
"v_cvt_pk_bf16_f32 v12, v12, v13\n\t"
"v_cvt_pk_bf16_f32 v14, v14, v15\n\t"
"v_mfma_f32_16x16x32_bf16 v[20:23], a[68:71], a[192:195], v[20:23]\n\t"
"v_accvgpr_write_b32 a56, v8\n\t"
"v_accvgpr_write_b32 a57, v10\n\t"
"v_mfma_f32_16x16x32_bf16 v[20:23], a[76:79], a[196:199], v[20:23]\n\t"
"v_accvgpr_write_b32 a64, v12\n\t"
"v_accvgpr_write_b32 a65, v14\n\t"
"v_mfma_f32_16x16x32_bf16 v[24:27], a[76:79], a[200:203], v[24:27]\n\t"
"s_waitcnt lgkmcnt(10)\n\t"
"ds_read_b128 a[8:11], v252 offset:0\n\t"
"v_cvt_pk_bf16_f32 v16, v16, v17\n\t"
"v_mfma_f32_16x16x32_bf16 v[24:27], a[68:71], a[196:199], v[24:27]\n\t"
"v_cvt_pk_bf16_f32 v18, v18, v19\n\t"
"v_accvgpr_write_b32 a72, v16\n\t"
"v_mfma_f32_16x16x32_bf16 v[24:27], a[60:63], a[192:195], v[24:27]\n\t"
"ds_read_b128 a[16:19], v252 offset:1024\n\t"
"v_accvgpr_write_b32 a73, v18\n\t"
"v_mfma_f32_16x16x32_bf16 v[28:31], a[60:63], a[196:199], v[28:31]\n\t"
"s_waitcnt vmcnt(5) lgkmcnt(8)\n\t"
"v_cvt_pk_bf16_f32 v20, v20, v21\n\t"
"v_cvt_pk_bf16_f32 v22, v22, v23\n\t"
"v_mfma_f32_16x16x32_bf16 v[28:31], a[68:71], a[200:203], v[28:31]\n\t"
"s_waitcnt lgkmcnt(6)\n\t"
"ds_read_b128 a[24:27], v252 offset:2048\n\t"
"v_accvgpr_write_b32 a80, v20\n\t"
"v_mfma_f32_16x16x32_bf16 v[28:31], a[76:79], a[204:207], v[28:31]\n\t"
"buffer_store_dwordx4 a[96:99], v248, s[16:19], s29 offen offset:128 nt\n\t"
"v_accvgpr_write_b32 a81, v22\n\t"
"v_mfma_f32_16x16x32_bf16 v[228:231], v[228:231], a[128:131], 0\n\t"
"s_waitcnt lgkmcnt(3)\n\t"
"v_cvt_pk_bf16_f32 v24, v24, v25\n\t"
"v_cvt_pk_bf16_f32 v26, v26, v27\n\t"
"v_mfma_f32_16x16x32_bf16 v[232:235], v[232:235], a[128:131], 0\n\t"
"v_accvgpr_write_b32 a88, v24\n\t"
"v_accvgpr_write_b32 a89, v26\n\t"
"v_mfma_f32_16x16x32_bf16 v[224:227], v[224:227], a[128:131], 0\n\t"
"v_mfma_f32_16x16x32_bf16 v[236:239], v[236:239], a[128:131], 0\n\t"
"v_cvt_pk_bf16_f32 v28, v28, v29\n\t"
"v_cvt_pk_bf16_f32 v30, v30, v31\n\t"
"v_mfma_f32_16x16x32_bf16 v[32:35], a[92:95], a[168:171], v[32:35]\n\t"
"v_accvgpr_write_b32 a96, v28\n\t"
"v_accvgpr_write_b32 a97, v30\n\t"
"v_mfma_f32_16x16x32_bf16 v[32:35], a[100:103], a[176:179], v[32:35]\n\t"
"v_cvt_pk_bf16_f32 v228, v228, v229\n\t"
"v_cvt_pk_bf16_f32 v229, v230, v231\n\t"
"v_mfma_f32_16x16x32_bf16 v[32:35], a[164:167], a[172:175], v[32:35]\n\t"
"v_cvt_pk_bf16_f32 v230, v232, v233\n\t"
"v_cvt_pk_bf16_f32 v231, v234, v235\n\t"
"v_mfma_f32_16x16x32_bf16 v[36:39], a[92:95], a[172:175], v[36:39]\n\t"
"v_cvt_pk_bf16_f32 v224, v224, v225\n\t"
"v_cvt_pk_bf16_f32 v225, v226, v227\n\t"
"v_mfma_f32_16x16x32_bf16 v[36:39], a[164:167], a[176:179], v[36:39]\n\t"
"v_cvt_pk_bf16_f32 v226, v236, v237\n\t"
"v_cvt_pk_bf16_f32 v227, v238, v239\n\t"
"v_mfma_f32_16x16x32_bf16 v[36:39], a[100:103], a[180:183], v[36:39]\n\t"
"ds_write_b128 v242, v[228:231] offset:30720\n\t"
"v_mfma_f32_16x16x32_bf16 v[40:43], a[100:103], a[184:187], v[40:43]\n\t"
"v_cvt_pk_bf16_f32 v32, v32, v33\n\t"
"v_cvt_pk_bf16_f32 v34, v34, v35\n\t"
"v_mfma_f32_16x16x32_bf16 v[40:43], a[164:167], a[180:183], v[40:43]\n\t"
"s_mov_b32 s20, 196608\n\t"
"v_accvgpr_write_b32 a42, v32\n\t"
"v_accvgpr_write_b32 a43, v34\n\t"
"v_mfma_f32_16x16x32_bf16 v[40:43], a[92:95], a[176:179], v[40:43]\n\t"
"buffer_store_dwordx4 a[108:111], v248, s[16:19], s29 offen offset:256 nt\n\t"
"s_add_u32 s29, s22, 590208\n\t"
"v_mfma_f32_16x16x32_bf16 v[44:47], a[92:95], a[180:183], v[44:47]\n\t"
"v_cvt_pk_bf16_f32 v36, v36, v37\n\t"
"v_cvt_pk_bf16_f32 v38, v38, v39\n\t"
"v_mfma_f32_16x16x32_bf16 v[44:47], a[164:167], a[184:187], v[44:47]\n\t"
"v_accvgpr_write_b32 a50, v36\n\t"
"v_mfma_f32_16x16x32_bf16 v[44:47], a[100:103], a[188:191], v[44:47]\n\t"
"s_waitcnt lgkmcnt(1)\n\t"
"s_barrier\n\t"
"v_mfma_f32_16x16x32_bf16 v[48:51], a[100:103], a[192:195], v[48:51]\n\t"
"ds_read_b64_tr_b16 v[232:233], v240 offset:512\n\t"
"v_mfma_f32_16x16x32_bf16 v[48:51], a[164:167], a[188:191], v[48:51]\n\t"
"ds_read_b64_tr_b16 v[234:235], v240 offset:2048\n\t"
"v_cvt_pk_bf16_f32 v40, v40, v41\n\t"
"v_mfma_f32_16x16x32_bf16 v[48:51], a[92:95], a[184:187], v[48:51]\n\t"
"ds_write_b128 v242, v[224:227] offset:32256\n\t"
"v_mfma_f32_16x16x32_bf16 v[52:55], a[92:95], a[188:191], v[52:55]\n\t"
"buffer_load_dwordx4 a[160:163], off, s[80:83], s20 offset:0 sc1\n\t"
"v_mfma_f32_16x16x32_bf16 v[52:55], a[164:167], a[192:195], v[52:55]\n\t"
"ds_read_b64_tr_b16 v[236:237], v241 offset:512\n\t"
"v_mfma_f32_16x16x32_bf16 v[52:55], a[100:103], a[196:199], v[52:55]\n\t"
"ds_read_b64_tr_b16 v[238:239], v241 offset:2048\n\t"
"v_cvt_pk_bf16_f32 v42, v42, v43\n\t"
"v_mfma_f32_16x16x32_bf16 v[56:59], a[100:103], a[200:203], v[56:59]\n\t"
"v_accvgpr_write_b32 a51, v38\n\t"
"v_cvt_pk_bf16_f32 v44, v44, v45\n\t"
"v_mfma_f32_16x16x32_bf16 v[56:59], a[164:167], a[196:199], v[56:59]\n\t"
"v_cvt_pk_bf16_f32 v46, v46, v47\n\t"
"v_accvgpr_write_b32 a58, v40\n\t"
"v_mfma_f32_16x16x32_bf16 v[56:59], a[92:95], a[192:195], v[56:59]\n\t"
"buffer_load_dwordx4 a[36:39], off, s[80:83], s20 offset:1024 sc1\n\t"
"v_mfma_f32_16x16x32_bf16 v[60:63], a[92:95], a[196:199], v[60:63]\n\t"
"s_waitcnt lgkmcnt(3)\n\t"
"v_accvgpr_write_b32 a59, v42\n\t"
"v_cvt_pk_bf16_f32 v48, v48, v49\n\t"
"v_mfma_f32_16x16x32_bf16 v[60:63], a[164:167], a[200:203], v[60:63]\n\t"
"s_waitcnt lgkmcnt(0)\n\t"
"v_cvt_pk_bf16_f32 v50, v50, v51\n\t"
"v_accvgpr_write_b32 a66, v44\n\t"
"v_mfma_f32_16x16x32_bf16 v[60:63], a[100:103], a[204:207], v[60:63]\n\t"
"buffer_store_dwordx4 a[116:119], v248, s[16:19], s29 offen offset:0 nt\n\t"
"v_accvgpr_write_b32 a67, v46\n\t"
"v_cvt_pk_bf16_f32 v52, v52, v53\n\t"
"v_cvt_pk_bf16_f32 v54, v54, v55\n\t"
"v_accvgpr_write_b32 a74, v48\n\t"
"v_mfma_f32_16x16x32_bf16 v[232:235], v[232:235], a[128:131], 0\n\t"
"v_accvgpr_write_b32 a75, v50\n\t"
"v_cvt_pk_bf16_f32 v56, v56, v57\n\t"
"v_mfma_f32_16x16x32_bf16 v[236:239], v[236:239], a[128:131], 0\n\t"
"v_cvt_pk_bf16_f32 v58, v58, v59\n\t"
"v_accvgpr_write_b32 a82, v52\n\t"
"v_mfma_f32_16x16x32_bf16 v[64:67], v[200:203], a[168:171], v[64:67]\n\t"
"ds_read_b128 a[168:171], v222 offset:1792\n\t"
"v_accvgpr_write_b32 a83, v54\n\t"
"v_mfma_f32_16x16x32_bf16 v[64:67], a[12:15], a[176:179], v[64:67]\n\t"
"v_cvt_pk_bf16_f32 v60, v60, v61\n\t"
"v_cvt_pk_bf16_f32 v62, v62, v63\n\t"
"v_mfma_f32_16x16x32_bf16 v[64:67], a[120:123], a[172:175], v[64:67]\n\t"
"v_accvgpr_write_b32 a90, v56\n\t"
"v_accvgpr_write_b32 a91, v58\n\t"
"v_mfma_f32_16x16x32_bf16 v[68:71], v[200:203], a[172:175], v[68:71]\n\t"
"ds_read_b128 a[172:175], v223 offset:7936\n\t"
"v_cvt_pk_bf16_f32 v232, v232, v233\n\t"
"v_mfma_f32_16x16x32_bf16 v[68:71], a[120:123], a[176:179], v[68:71]\n\t"
"v_cvt_pk_bf16_f32 v233, v234, v235\n\t"
"v_cvt_pk_bf16_f32 v234, v236, v237\n\t"
"v_mfma_f32_16x16x32_bf16 v[68:71], a[12:15], a[180:183], v[68:71]\n\t"
"v_cvt_pk_bf16_f32 v235, v238, v239\n\t"
"v_subrev_u32 v240, 59904, v240\n\t"
"v_mfma_f32_16x16x32_bf16 v[72:75], a[12:15], a[184:187], v[72:75]\n\t"
"v_subrev_u32 v241, 59904, v241\n\t"
"v_accvgpr_write_b32 a98, v60\n\t"
"v_mfma_f32_16x16x32_bf16 v[72:75], a[120:123], a[180:183], v[72:75]\n\t"
"v_accvgpr_write_b32 a99, v62\n\t"
"v_cvt_pk_bf16_f32 v64, v64, v65\n\t"
"v_mfma_f32_16x16x32_bf16 v[72:75], v[200:203], a[176:179], v[72:75]\n\t"
"buffer_store_dwordx4 a[124:127], v248, s[16:19], s29 offen offset:128 nt\n\t"
"ds_read_b128 a[176:179], v223 offset:14080\n\t"
"ds_read_b64_tr_b16 v[228:229], v240 offset:512\n\t"
"v_cvt_pk_bf16_f32 v66, v66, v67\n\t"
"v_mfma_f32_16x16x32_bf16 v[76:79], v[200:203], a[180:183], v[76:79]\n\t"
"ds_read_b128 a[180:183], v222 offset:21760\n\t"
"v_cvt_pk_bf16_f32 v68, v68, v69\n\t"
"v_mfma_f32_16x16x32_bf16 v[76:79], a[120:123], a[184:187], v[76:79]\n\t"
"ds_read_b64_tr_b16 v[230:231], v240 offset:2048\n\t"
"v_cvt_pk_bf16_f32 v70, v70, v71\n\t"
"v_mfma_f32_16x16x32_bf16 v[76:79], a[12:15], a[188:191], v[76:79]\n\t"
"ds_write_b128 v242, v[232:235] offset:29184\n\t"
"v_mfma_f32_16x16x32_bf16 v[80:83], a[12:15], a[192:195], v[80:83]\n\t"
"v_accvgpr_write_b32 a108, v64\n\t"
"v_accvgpr_write_b32 a109, v66\n\t"
"v_mfma_f32_16x16x32_bf16 v[80:83], a[120:123], a[188:191], v[80:83]\n\t"
"ds_read_b64_tr_b16 v[224:225], v241 offset:512\n\t"
"v_mfma_f32_16x16x32_bf16 v[80:83], v[200:203], a[184:187], v[80:83]\n\t"
"ds_read_b64_tr_b16 v[226:227], v241 offset:2048\n\t"
"v_cvt_pk_bf16_f32 v72, v72, v73\n\t"
"v_mfma_f32_16x16x32_bf16 v[84:87], v[200:203], a[188:191], v[84:87]\n\t"
"ds_read_b128 a[184:187], v223 offset:27904\n\t"
"v_cvt_pk_bf16_f32 v74, v74, v75\n\t"
"v_mfma_f32_16x16x32_bf16 v[84:87], a[120:123], a[192:195], v[84:87]\n\t"
"ds_read_b128 a[32:35], v252 offset:12288\n\t"
"v_accvgpr_write_b32 a116, v68\n\t"
"v_mfma_f32_16x16x32_bf16 v[84:87], a[12:15], a[196:199], v[84:87]\n\t"
"ds_read_b128 a[188:191], v223 offset:34048\n\t"
"v_accvgpr_write_b32 a117, v70\n\t"
"v_mfma_f32_16x16x32_bf16 v[88:91], a[12:15], a[200:203], v[88:91]\n\t"
"ds_read_b128 a[104:107], v252 offset:13312\n\t"
"v_cvt_pk_bf16_f32 v76, v76, v77\n\t"
"v_mfma_f32_16x16x32_bf16 v[88:91], a[120:123], a[196:199], v[88:91]\n\t"
"ds_read_b128 a[112:115], v252 offset:14336\n\t"
"v_cvt_pk_bf16_f32 v78, v78, v79\n\t"
"v_mfma_f32_16x16x32_bf16 v[88:91], v[200:203], a[192:195], v[88:91]\n\t"
"buffer_store_dwordx4 a[156:159], v248, s[16:19], s29 offen offset:256 nt\n\t"
"ds_read_b128 a[192:195], v222 offset:41728\n\t"
"v_accvgpr_write_b32 a124, v72\n\t"
"v_accvgpr_write_b32 a125, v74\n\t"
"v_cvt_pk_bf16_f32 v80, v80, v81\n\t"
"v_mfma_f32_16x16x32_bf16 v[92:95], v[200:203], a[196:199], v[92:95]\n\t"
"s_waitcnt lgkmcnt(9)\n\t"
"ds_read_b128 a[196:199], v223 offset:47872\n\t"
"v_cvt_pk_bf16_f32 v82, v82, v83\n\t"
"v_mfma_f32_16x16x32_bf16 v[92:95], a[120:123], a[200:203], v[92:95]\n\t"
"s_waitcnt lgkmcnt(7)\n\t"
"ds_read_b128 a[200:203], v223 offset:54016\n\t"
"v_accvgpr_write_b32 a132, v76\n\t"
"v_mfma_f32_16x16x32_bf16 v[92:95], a[12:15], a[204:207], v[92:95]\n\t"
"ds_read_b128 a[204:207], v222 offset:61696\n\t"
"v_accvgpr_write_b32 a133, v78\n\t"
"v_mfma_f32_16x16x32_bf16 v[228:231], v[228:231], a[128:131], 0\n\t"
"buffer_load_dwordx4 v[200:203], off, s[80:83], s20 offset:2048 sc1\n\t"
"s_mov_b32 s20, 208896\n\t"
"v_mfma_f32_16x16x32_bf16 v[224:227], v[224:227], a[128:131], 0\n\t"
"buffer_load_dwordx4 a[120:123], off, s[80:83], s20 offset:0 sc1\n\t"
"s_add_u32 s29, s22, 786816\n\t"
"v_mfma_f32_16x16x32_bf16 v[100:103], a[136:139], a[168:171], v[100:103]\n\t"
"v_cvt_pk_bf16_f32 v84, v84, v85\n\t"
"v_cvt_pk_bf16_f32 v86, v86, v87\n\t"
"v_mfma_f32_16x16x32_bf16 v[100:103], v[208:211], a[176:179], v[100:103]\n\t"
"v_accvgpr_write_b32 a140, v80\n\t"
"v_accvgpr_write_b32 a141, v82\n\t"
"v_mfma_f32_16x16x32_bf16 v[100:103], v[96:99], a[172:175], v[100:103]\n\t"
"v_cvt_pk_bf16_f32 v88, v88, v89\n\t"
"v_cvt_pk_bf16_f32 v90, v90, v91\n\t"
"v_mfma_f32_16x16x32_bf16 v[104:107], a[136:139], a[172:175], v[104:107]\n\t"
"s_waitcnt lgkmcnt(8)\n\t"
"v_cvt_pk_bf16_f32 v228, v228, v229\n\t"
"v_cvt_pk_bf16_f32 v229, v230, v231\n\t"
"v_mfma_f32_16x16x32_bf16 v[104:107], v[96:99], a[176:179], v[104:107]\n\t"
"v_cvt_pk_bf16_f32 v230, v224, v225\n\t"
"v_mfma_f32_16x16x32_bf16 v[104:107], v[208:211], a[180:183], v[104:107]\n\t"
"s_waitcnt lgkmcnt(0)\n\t"
"s_barrier\n\t"
"v_mfma_f32_16x16x32_bf16 v[108:111], v[208:211], a[184:187], v[108:111]\n\t"
"v_cvt_pk_bf16_f32 v231, v226, v227\n\t"
"v_mfma_f32_16x16x32_bf16 v[108:111], v[96:99], a[180:183], v[108:111]\n\t"
"buffer_load_dwordx4 a[12:15], off, s[80:83], s20 offset:1024 sc1\n\t"
"v_mfma_f32_16x16x32_bf16 v[108:111], a[136:139], a[176:179], v[108:111]\n\t"
"buffer_store_dwordx4 a[52:55], v248, s[16:19], s29 offen offset:0 nt\n\t"
"ds_read_b64_tr_b16 v[236:237], v240 offset:20480\n\t"
"ds_read_b64_tr_b16 v[238:239], v240 offset:22016\n\t"
"v_accvgpr_write_b32 a148, v84\n\t"
"v_mfma_f32_16x16x32_bf16 v[112:115], a[136:139], a[180:183], v[112:115]\n\t"
"ds_read_b64_tr_b16 v[232:233], v241 offset:20480\n\t"
"v_accvgpr_write_b32 a149, v86\n\t"
"v_mfma_f32_16x16x32_bf16 v[112:115], v[96:99], a[184:187], v[112:115]\n\t"
"ds_read_b64_tr_b16 v[234:235], v241 offset:22016\n\t"
"v_cvt_pk_bf16_f32 v92, v92, v93\n\t"
"v_mfma_f32_16x16x32_bf16 v[112:115], v[208:211], a[188:191], v[112:115]\n\t"
"ds_write_b128 v242, v[228:231] offset:24576\n\t"
"v_mfma_f32_16x16x32_bf16 v[116:119], v[208:211], a[192:195], v[116:119]\n\t"
"v_cvt_pk_bf16_f32 v94, v94, v95\n\t"
"v_accvgpr_write_b32 a156, v88\n\t"
"v_mfma_f32_16x16x32_bf16 v[116:119], v[96:99], a[188:191], v[116:119]\n\t"
"ds_read_b64_tr_b16 v[224:225], v240 offset:40448\n\t"
"v_mfma_f32_16x16x32_bf16 v[116:119], a[136:139], a[184:187], v[116:119]\n\t"
"ds_read_b64_tr_b16 v[226:227], v240 offset:41984\n\t"
"v_accvgpr_write_b32 a157, v90\n\t"
"v_mfma_f32_16x16x32_bf16 v[120:123], a[136:139], a[188:191], v[120:123]\n\t"
"ds_read_b64_tr_b16 v[228:229], v241 offset:40448\n\t"
"v_accvgpr_write_b32 a164, v92\n\t"
"v_mfma_f32_16x16x32_bf16 v[120:123], v[96:99], a[192:195], v[120:123]\n\t"
"ds_read_b64_tr_b16 v[230:231], v241 offset:41984\n\t"
"v_accvgpr_write_b32 a165, v94\n\t"
"v_mfma_f32_16x16x32_bf16 v[120:123], v[208:211], a[196:199], v[120:123]\n\t"
"ds_write_b128 v249, a[40:43] offset:0\n\t"
"v_mfma_f32_16x16x32_bf16 v[124:127], v[208:211], a[200:203], v[124:127]\n\t"
"v_cvt_pk_bf16_f32 v100, v100, v101\n\t"
"v_cvt_pk_bf16_f32 v102, v102, v103\n\t"
"v_mfma_f32_16x16x32_bf16 v[124:127], v[96:99], a[196:199], v[124:127]\n\t"
"v_cvt_pk_bf16_f32 v104, v104, v105\n\t"
"v_cvt_pk_bf16_f32 v106, v106, v107\n\t"
"v_mfma_f32_16x16x32_bf16 v[124:127], a[136:139], a[192:195], v[124:127]\n\t"
"buffer_store_dwordx4 a[84:87], v248, s[16:19], s29 offen offset:128 nt\n\t"
"ds_write_b64 v250, a[108:109] offset:0\n\t"
"v_accvgpr_write_b32 a44, v100\n\t"
"v_mfma_f32_16x16x32_bf16 v[128:131], a[136:139], a[196:199], v[128:131]\n\t"
"s_waitcnt lgkmcnt(9)\n\t"
"v_accvgpr_write_b32 a45, v102\n\t"
"v_cvt_pk_bf16_f32 v108, v108, v109\n\t"
"v_mfma_f32_16x16x32_bf16 v[128:131], v[96:99], a[200:203], v[128:131]\n\t"
"s_waitcnt lgkmcnt(7)\n\t"
"v_cvt_pk_bf16_f32 v110, v110, v111\n\t"
"v_accvgpr_write_b32 a52, v104\n\t"
"v_mfma_f32_16x16x32_bf16 v[128:131], v[208:211], a[204:207], v[128:131]\n\t"
"s_waitcnt lgkmcnt(4)\n\t"
"ds_write_b128 v249, a[56:59] offset:12288\n\t"
"v_mfma_f32_16x16x32_bf16 v[236:239], v[236:239], a[128:131], 0\n\t"
"buffer_load_dwordx4 a[136:139], off, s[80:83], s20 offset:2048 sc1\n\t"
"s_mov_b32 s20, 294912\n\t"
"v_mfma_f32_16x16x32_bf16 v[232:235], v[232:235], a[128:131], 0\n\t"
"buffer_load_dwordx4 v[208:211], off, s[80:83], s20 offset:0 sc1\n\t"
"v_mfma_f32_16x16x32_bf16 v[224:227], v[224:227], a[128:131], 0\n\t"
"s_waitcnt lgkmcnt(3)\n\t"
"ds_write_b64 v250, a[124:125] offset:12288\n\t"
"v_mfma_f32_16x16x32_bf16 v[228:231], v[228:231], a[128:131], 0\n\t"
"buffer_store_dwordx4 v[196:199], v248, s[16:19], s29 offen offset:256 nt\n\t"
"ds_write_b64 v250, a[116:117] offset:6144\n\t"
"v_accvgpr_write_b32 a53, v106\n\t"
"v_mfma_f32_16x16x32_bf16 v[132:135], v[216:219], a[168:171], v[132:135]\n\t"
"v_cvt_pk_bf16_f32 v112, v112, v113\n\t"
"v_cvt_pk_bf16_f32 v114, v114, v115\n\t"
"v_mfma_f32_16x16x32_bf16 v[132:135], a[208:211], a[176:179], v[132:135]\n\t"
"v_cvt_pk_bf16_f32 v236, v236, v237\n\t"
"v_cvt_pk_bf16_f32 v237, v238, v239\n\t"
"v_mfma_f32_16x16x32_bf16 v[132:135], a[4:7], a[172:175], v[132:135]\n\t"
"ds_write_b128 v249, a[48:51] offset:6144\n\t"
"v_mfma_f32_16x16x32_bf16 v[136:139], v[216:219], a[172:175], v[136:139]\n\t"
"v_accvgpr_write_b32 a60, v108\n\t"
"v_accvgpr_write_b32 a61, v110\n\t"
"v_mfma_f32_16x16x32_bf16 v[136:139], a[4:7], a[176:179], v[136:139]\n\t"
"s_add_u32 s29, s22, 983424\n\t"
"v_cvt_pk_bf16_f32 v238, v232, v233\n\t"
"v_cvt_pk_bf16_f32 v239, v234, v235\n\t"
"v_mfma_f32_16x16x32_bf16 v[136:139], a[208:211], a[180:183], v[136:139]\n\t"
"ds_write_b128 v242, v[236:239] offset:26112\n\t"
"v_mfma_f32_16x16x32_bf16 v[140:143], a[208:211], a[184:187], v[140:143]\n\t"
"v_cvt_pk_bf16_f32 v224, v224, v225\n\t"
"v_cvt_pk_bf16_f32 v225, v226, v227\n\t"
"v_mfma_f32_16x16x32_bf16 v[140:143], a[4:7], a[180:183], v[140:143]\n\t"
"v_cvt_pk_bf16_f32 v116, v116, v117\n\t"
"v_cvt_pk_bf16_f32 v118, v118, v119\n\t"
"v_mfma_f32_16x16x32_bf16 v[140:143], v[216:219], a[176:179], v[140:143]\n\t"
"ds_write_b128 v249, a[64:67] offset:18432\n\t"
"v_cvt_pk_bf16_f32 v226, v228, v229\n\t"
"v_cvt_pk_bf16_f32 v227, v230, v231\n\t"
"v_accvgpr_write_b32 a68, v112\n\t"
"v_accvgpr_write_b32 a69, v114\n\t"
"v_mfma_f32_16x16x32_bf16 v[144:147], v[216:219], a[180:183], v[144:147]\n\t"
"v_cvt_pk_bf16_f32 v120, v120, v121\n\t"
"v_cvt_pk_bf16_f32 v122, v122, v123\n\t"
"v_mfma_f32_16x16x32_bf16 v[144:147], a[4:7], a[184:187], v[144:147]\n\t"
"v_accvgpr_write_b32 a76, v116\n\t"
"v_accvgpr_write_b32 a77, v118\n\t"
"v_mfma_f32_16x16x32_bf16 v[144:147], a[208:211], a[188:191], v[144:147]\n\t"
"ds_write_b128 v242, v[224:227] offset:27648\n\t"
"v_mfma_f32_16x16x32_bf16 v[148:151], a[208:211], a[192:195], v[148:151]\n\t"
"v_cvt_pk_bf16_f32 v124, v124, v125\n\t"
"v_cvt_pk_bf16_f32 v126, v126, v127\n\t"
"v_mfma_f32_16x16x32_bf16 v[148:151], a[4:7], a[188:191], v[148:151]\n\t"
"v_accvgpr_write_b32 a84, v120\n\t"
"v_accvgpr_write_b32 a85, v122\n\t"
"v_mfma_f32_16x16x32_bf16 v[148:151], v[216:219], a[184:187], v[148:151]\n\t"
"ds_write_b64 v250, a[132:133] offset:18432\n\t"
"v_mfma_f32_16x16x32_bf16 v[152:155], v[216:219], a[188:191], v[152:155]\n\t"
"v_cvt_pk_bf16_f32 v128, v128, v129\n\t"
"v_cvt_pk_bf16_f32 v130, v130, v131\n\t"
"v_mfma_f32_16x16x32_bf16 v[152:155], a[4:7], a[192:195], v[152:155]\n\t"
"Lpg13_%=:\n\t"
"s_cbranch_vccnz Lvr1_%=\n\t"
"v_accvgpr_write_b32 a92, v124\n\t"
"v_accvgpr_write_b32 a93, v126\n\t"
"v_mfma_f32_16x16x32_bf16 v[152:155], a[208:211], a[196:199], v[152:155]\n\t"
"buffer_store_dwordx4 v[204:207], v248, s[16:19], s29 offen offset:0 nt\n\t"
"v_accvgpr_write_b32 a100, v128\n\t"
"v_accvgpr_write_b32 a101, v130\n\t"
"v_cvt_pk_bf16_f32 v132, v132, v133\n\t"
"v_cvt_pk_bf16_f32 v134, v134, v135\n\t"
"v_mfma_f32_16x16x32_bf16 v[156:159], a[208:211], a[200:203], v[156:159]\n\t"
"v_cvt_pk_bf16_f32 v136, v136, v137\n\t"
"v_cvt_pk_bf16_f32 v138, v138, v139\n\t"
"v_mfma_f32_16x16x32_bf16 v[156:159], a[4:7], a[196:199], v[156:159]\n\t"
"v_accvgpr_write_b32 a46, v132\n\t"
"v_accvgpr_write_b32 a47, v134\n\t"
"v_mfma_f32_16x16x32_bf16 v[156:159], v[216:219], a[192:195], v[156:159]\n\t"
"v_cvt_pk_bf16_f32 v140, v140, v141\n\t"
"v_cvt_pk_bf16_f32 v142, v142, v143\n\t"
"v_mfma_f32_16x16x32_bf16 v[160:163], v[216:219], a[196:199], v[160:163]\n\t"
"v_accvgpr_write_b32 a54, v136\n\t"
"v_accvgpr_write_b32 a55, v138\n\t"
"v_mfma_f32_16x16x32_bf16 v[160:163], a[4:7], a[200:203], v[160:163]\n\t"
"v_cvt_pk_bf16_f32 v144, v144, v145\n\t"
"v_cvt_pk_bf16_f32 v146, v146, v147\n\t"
"v_mfma_f32_16x16x32_bf16 v[160:163], a[208:211], a[204:207], v[160:163]\n\t"
"v_accvgpr_write_b32 a62, v140\n\t"
"v_accvgpr_write_b32 a63, v142\n\t"
"v_mfma_f32_16x16x32_bf16 v[164:167], a[216:219], a[168:171], v[164:167]\n\t"
"v_cvt_pk_bf16_f32 v148, v148, v149\n\t"
"v_cvt_pk_bf16_f32 v150, v150, v151\n\t"
"v_mfma_f32_16x16x32_bf16 v[164:167], a[220:223], a[176:179], v[164:167]\n\t"
"v_accvgpr_write_b32 a70, v144\n\t"
"v_accvgpr_write_b32 a71, v146\n\t"
"v_mfma_f32_16x16x32_bf16 v[164:167], a[212:215], a[172:175], v[164:167]\n\t"
"buffer_load_dwordx4 v[216:219], off, s[80:83], s20 offset:1024 sc1\n\t"
"v_mfma_f32_16x16x32_bf16 v[168:171], a[216:219], a[172:175], v[168:171]\n\t"
"buffer_load_dwordx4 a[4:7], off, s[80:83], s20 offset:2048 sc1\n\t"
"v_mfma_f32_16x16x32_bf16 v[168:171], a[212:215], a[176:179], v[168:171]\n\t"
"s_mov_b32 s20, 307200\n\t"
"v_cvt_pk_bf16_f32 v152, v152, v153\n\t"
"v_cvt_pk_bf16_f32 v154, v154, v155\n\t"
"v_mfma_f32_16x16x32_bf16 v[168:171], a[220:223], a[180:183], v[168:171]\n\t"
"buffer_store_dwordx4 v[212:215], v248, s[16:19], s29 offen offset:128 nt\n\t"
"buffer_load_dwordx4 a[208:211], off, s[80:83], s20 offset:0 sc1\n\t"
"v_accvgpr_write_b32 a78, v148\n\t"
"v_mfma_f32_16x16x32_bf16 v[172:175], a[220:223], a[184:187], v[172:175]\n\t"
"v_accvgpr_write_b32 a79, v150\n\t"
"v_cvt_pk_bf16_f32 v156, v156, v157\n\t"
"v_mfma_f32_16x16x32_bf16 v[172:175], a[212:215], a[180:183], v[172:175]\n\t"
"v_cvt_pk_bf16_f32 v158, v158, v159\n\t"
"v_accvgpr_write_b32 a86, v152\n\t"
"v_mfma_f32_16x16x32_bf16 v[172:175], a[216:219], a[176:179], v[172:175]\n\t"
"v_accvgpr_write_b32 a87, v154\n\t"
"v_cvt_pk_bf16_f32 v160, v160, v161\n\t"
"v_mfma_f32_16x16x32_bf16 v[176:179], a[216:219], a[180:183], v[176:179]\n\t"
"v_cvt_pk_bf16_f32 v162, v162, v163\n\t"
"v_accvgpr_write_b32 a94, v156\n\t"
"v_mfma_f32_16x16x32_bf16 v[176:179], a[212:215], a[184:187], v[176:179]\n\t"
"v_accvgpr_write_b32 a95, v158\n\t"
"v_mfma_f32_16x16x32_bf16 v[176:179], a[220:223], a[188:191], v[176:179]\n\t"
"s_waitcnt lgkmcnt(0)\n\t"
"s_barrier\n\t"
"v_mfma_f32_16x16x32_bf16 v[180:183], a[220:223], a[192:195], v[180:183]\n\t"
"ds_read_b128 a[168:171], v244 offset:24576\n\t"
"v_mfma_f32_16x16x32_bf16 v[180:183], a[212:215], a[188:191], v[180:183]\n\t"
"ds_read_b128 a[172:175], v244 offset:25088\n\t"
"v_cvt_pk_bf16_f32 v164, v164, v165\n\t"
"v_mfma_f32_16x16x32_bf16 v[180:183], a[216:219], a[184:187], v[180:183]\n\t"
"ds_read_b128 a[176:179], v244 offset:25600\n\t"
"v_cvt_pk_bf16_f32 v166, v166, v167\n\t"
"v_mfma_f32_16x16x32_bf16 v[184:187], a[216:219], a[188:191], v[184:187]\n\t"
"ds_read_b128 a[180:183], v244 offset:26112\n\t"
"v_accvgpr_write_b32 a102, v160\n\t"
"v_mfma_f32_16x16x32_bf16 v[184:187], a[212:215], a[192:195], v[184:187]\n\t"
"ds_read_b128 a[184:187], v244 offset:26624\n\t"
"v_accvgpr_write_b32 a103, v162\n\t"
"v_mfma_f32_16x16x32_bf16 v[184:187], a[220:223], a[196:199], v[184:187]\n\t"
"buffer_load_dwordx4 a[64:67], off, s[80:83], s20 offset:1024 sc1\n\t"
"v_mfma_f32_16x16x32_bf16 v[188:191], a[220:223], a[200:203], v[188:191]\n\t"
"ds_read_b128 a[188:191], v244 offset:27136\n\t"
"v_cvt_pk_bf16_f32 v168, v168, v169\n\t"
"v_mfma_f32_16x16x32_bf16 v[188:191], a[212:215], a[196:199], v[188:191]\n\t"
"v_cvt_pk_bf16_f32 v170, v170, v171\n\t"
"v_accvgpr_write_b32 a110, v164\n\t"
"v_mfma_f32_16x16x32_bf16 v[188:191], a[216:219], a[192:195], v[188:191]\n\t"
"buffer_store_dwordx4 a[0:3], v248, s[16:19], s29 offen offset:256 nt\n\t"
"buffer_load_dwordx4 a[48:51], off, s[80:83], s20 offset:2048 sc1\n\t"
"ds_read_b128 a[192:195], v244 offset:27648\n\t"
"s_mov_b32 s20, 319488\n\t"
"v_mfma_f32_16x16x32_bf16 v[192:195], a[216:219], a[196:199], v[192:195]\n\t"
"s_waitcnt lgkmcnt(4)\n\t"
"ds_read_b128 a[196:199], v244 offset:28160\n\t"
"v_accvgpr_write_b32 a111, v166\n\t"
"v_mfma_f32_16x16x32_bf16 v[192:195], a[212:215], a[200:203], v[192:195]\n\t"
"ds_read_b128 a[200:203], v244 offset:28672\n\t"
"v_cvt_pk_bf16_f32 v172, v172, v173\n\t"
"v_mfma_f32_16x16x32_bf16 v[192:195], a[220:223], a[204:207], v[192:195]\n\t"
"buffer_load_dwordx4 a[56:59], off, s[80:83], s20 offset:0 sc1\n\t"
"s_add_u32 s29, s22, 1180032\n\t"
"Lst18_%=:\n\t"
"v_mfma_f32_16x16x32_bf16 v[2:5], a[144:147], a[168:171], 0\n\t"
"buffer_load_dwordx4 a[212:215], off, s[80:83], s20 offset:1024 sc1\n\t"
"v_mfma_f32_16x16x32_bf16 v[2:5], a[224:227], a[176:179], v[2:5]\n\t"
"buffer_load_dwordx4 a[216:219], off, s[80:83], s20 offset:2048 sc1\n\t"
"v_mfma_f32_16x16x32_bf16 v[2:5], a[20:23], a[172:175], v[2:5]\n\t"
"s_waitcnt lgkmcnt(5)\n\t"
"ds_read_b128 a[204:207], v244 offset:29184\n\t"
"v_cvt_pk_bf16_f32 v174, v174, v175\n\t"
"v_mfma_f32_16x16x32_bf16 v[6:9], a[144:147], a[172:175], 0\n\t"
"s_waitcnt lgkmcnt(5)\n\t"
"s_mov_b32 s20, 479232\n\t"
"v_accvgpr_write_b32 a118, v168\n\t"
"v_mfma_f32_16x16x32_bf16 v[6:9], a[20:23], a[176:179], v[6:9]\n\t"
"v_accvgpr_write_b32 a119, v170\n\t"
"v_cvt_pk_bf16_f32 v176, v176, v177\n\t"
"v_mfma_f32_16x16x32_bf16 v[6:9], a[224:227], a[180:183], v[6:9]\n\t"
"v_cvt_pk_bf16_f32 v178, v178, v179\n\t"
"v_accvgpr_write_b32 a126, v172\n\t"
"v_mfma_f32_16x16x32_bf16 v[10:13], a[224:227], a[184:187], 0\n\t"
"s_waitcnt lgkmcnt(4)\n\t"
"v_accvgpr_write_b32 a127, v174\n\t"
"v_cvt_pk_bf16_f32 v180, v180, v181\n\t"
"v_mfma_f32_16x16x32_bf16 v[10:13], a[20:23], a[180:183], v[10:13]\n\t"
"v_cvt_pk_bf16_f32 v182, v182, v183\n\t"
"v_accvgpr_write_b32 a134, v176\n\t"
"v_mfma_f32_16x16x32_bf16 v[10:13], a[144:147], a[176:179], v[10:13]\n\t"
"buffer_store_dwordx4 a[8:11], v248, s[16:19], s29 offen offset:0 nt\n\t"
"buffer_load_dwordx4 a[220:223], off, s[80:83], s20 offset:0 sc1\n\t"
"v_accvgpr_write_b32 a135, v178\n\t"
"v_mfma_f32_16x16x32_bf16 v[14:17], a[144:147], a[180:183], 0\n\t"
"s_waitcnt lgkmcnt(3)\n\t"
"v_cvt_pk_bf16_f32 v184, v184, v185\n\t"
"v_cvt_pk_bf16_f32 v186, v186, v187\n\t"
"v_mfma_f32_16x16x32_bf16 v[14:17], a[20:23], a[184:187], v[14:17]\n\t"
"v_accvgpr_write_b32 a142, v180\n\t"
"v_accvgpr_write_b32 a143, v182\n\t"
"v_mfma_f32_16x16x32_bf16 v[14:17], a[224:227], a[188:191], v[14:17]\n\t"
"v_cvt_pk_bf16_f32 v188, v188, v189\n\t"
"v_cvt_pk_bf16_f32 v190, v190, v191\n\t"
"v_mfma_f32_16x16x32_bf16 v[18:21], a[224:227], a[192:195], 0\n\t"
"s_waitcnt lgkmcnt(2)\n\t"
"v_accvgpr_write_b32 a150, v184\n\t"
"v_accvgpr_write_b32 a151, v186\n\t"
"v_mfma_f32_16x16x32_bf16 v[18:21], a[20:23], a[188:191], v[18:21]\n\t"
"v_cvt_pk_bf16_f32 v192, v192, v193\n\t"
"v_cvt_pk_bf16_f32 v194, v194, v195\n\t"
"v_mfma_f32_16x16x32_bf16 v[18:21], a[144:147], a[184:187], v[18:21]\n\t"
"ds_read_b128 v[196:199], v252 offset:0\n\t"
"v_accvgpr_write_b32 a158, v188\n\t"
"v_mfma_f32_16x16x32_bf16 v[22:25], a[144:147], a[188:191], 0\n\t"
"s_waitcnt lgkmcnt(2)\n\t"
"ds_read_b128 a[0:3], v252 offset:12288\n\t"
"v_accvgpr_write_b32 a159, v190\n\t"
"v_mfma_f32_16x16x32_bf16 v[22:25], a[20:23], a[192:195], v[22:25]\n\t"
"ds_read_b128 v[204:207], v252 offset:1024\n\t"
"v_accvgpr_write_b32 a166, v192\n\t"
"v_mfma_f32_16x16x32_bf16 v[22:25], a[224:227], a[196:199], v[22:25]\n\t"
"ds_read_b128 a[8:11], v252 offset:13312\n\t"
"v_accvgpr_write_b32 a167, v194\n\t"
"v_mfma_f32_16x16x32_bf16 v[26:29], a[224:227], a[200:203], 0\n\t"
"s_waitcnt lgkmcnt(4)\n\t"
"ds_read_b128 v[212:215], v252 offset:2048\n\t"
"v_mfma_f32_16x16x32_bf16 v[26:29], a[20:23], a[196:199], v[26:29]\n\t"
"ds_read_b128 a[40:43], v252 offset:14336\n\t"
"v_mfma_f32_16x16x32_bf16 v[26:29], a[144:147], a[192:195], v[26:29]\n\t"
"buffer_store_dwordx4 a[16:19], v248, s[16:19], s29 offen offset:128 nt\n\t"
"v_mfma_f32_16x16x32_bf16 v[30:33], a[144:147], a[196:199], 0\n\t"
"v_mfma_f32_16x16x32_bf16 v[30:33], a[20:23], a[200:203], v[30:33]\n\t"
"buffer_load_dwordx4 a[144:147], off, s[80:83], s20 offset:1024 sc1\n\t"
"v_mfma_f32_16x16x32_bf16 v[30:33], a[224:227], a[204:207], v[30:33]\n\t"
"v_mfma_f32_16x16x32_bf16 v[34:37], a[228:231], a[168:171], 0\n\t"
"buffer_load_dwordx4 a[20:23], off, s[80:83], s20 offset:2048 sc1\n\t"
"v_mfma_f32_16x16x32_bf16 v[34:37], a[236:239], a[176:179], v[34:37]\n\t"
"s_mov_b32 s20, 491520\n\t"
"v_mfma_f32_16x16x32_bf16 v[34:37], a[232:235], a[172:175], v[34:37]\n\t"
"buffer_store_dwordx4 a[24:27], v248, s[16:19], s29 offen offset:256 nt\n\t"
"v_mfma_f32_16x16x32_bf16 v[38:41], a[228:231], a[172:175], 0\n\t"
"v_mfma_f32_16x16x32_bf16 v[38:41], a[232:235], a[176:179], v[38:41]\n\t"
"buffer_load_dwordx4 a[224:227], off, s[80:83], s20 offset:0 sc1\n\t"
"v_mfma_f32_16x16x32_bf16 v[38:41], a[236:239], a[180:183], v[38:41]\n\t"
"v_mfma_f32_16x16x32_bf16 v[42:45], a[236:239], a[184:187], 0\n\t"
"v_mfma_f32_16x16x32_bf16 v[42:45], a[232:235], a[180:183], v[42:45]\n\t"
"v_mfma_f32_16x16x32_bf16 v[42:45], a[228:231], a[176:179], v[42:45]\n\t"
"s_add_u32 s29, s22, 1376640\n\t"
"ds_read_b64_tr_b16 v[232:233], v240 offset:1024\n\t"
"v_mfma_f32_16x16x32_bf16 v[46:49], a[228:231], a[180:183], 0\n\t"
"ds_read_b64_tr_b16 v[234:235], v240 offset:2560\n\t"
"v_mfma_f32_16x16x32_bf16 v[46:49], a[232:235], a[184:187], v[46:49]\n\t"
"ds_read_b64_tr_b16 v[228:229], v241 offset:1024\n\t"
"v_mfma_f32_16x16x32_bf16 v[46:49], a[236:239], a[188:191], v[46:49]\n\t"
"s_waitcnt lgkmcnt(3)\n\t"
"s_barrier\n\t"
"v_mfma_f32_16x16x32_bf16 v[50:53], a[236:239], a[192:195], 0\n\t"
"ds_read_b64_tr_b16 v[230:231], v241 offset:2560\n\t"
"v_mfma_f32_16x16x32_bf16 v[50:53], a[232:235], a[188:191], v[50:53]\n\t"
"ds_read_b64_tr_b16 v[236:237], v240 offset:20992\n\t"
"v_mfma_f32_16x16x32_bf16 v[50:53], a[228:231], a[184:187], v[50:53]\n\t"
"ds_read_b64_tr_b16 v[238:239], v240 offset:22528\n\t"
"v_mfma_f32_16x16x32_bf16 v[54:57], a[228:231], a[188:191], 0\n\t"
"ds_read_b64_tr_b16 v[224:225], v241 offset:20992\n\t"
"v_mfma_f32_16x16x32_bf16 v[54:57], a[232:235], a[192:195], v[54:57]\n\t"
"ds_read_b64_tr_b16 v[226:227], v241 offset:22528\n\t"
"v_mfma_f32_16x16x32_bf16 v[54:57], a[236:239], a[196:199], v[54:57]\n\t"
"buffer_store_dwordx4 a[32:35], v248, s[16:19], s29 offen offset:0 nt\n\t"
"ds_write_b64 v250, a[140:141] offset:0\n\t"
"v_mfma_f32_16x16x32_bf16 v[58:61], a[236:239], a[200:203], 0\n\t"
"v_mfma_f32_16x16x32_bf16 v[58:61], a[232:235], a[196:199], v[58:61]\n\t"
"v_mfma_f32_16x16x32_bf16 v[58:61], a[228:231], a[192:195], v[58:61]\n\t"
"ds_write_b128 v249, a[72:75] offset:0\n\t"
"v_mfma_f32_16x16x32_bf16 v[62:65], a[228:231], a[196:199], 0\n\t"
"buffer_load_dwordx4 a[228:231], off, s[80:83], s20 offset:1024 sc1\n\t"
"v_mfma_f32_16x16x32_bf16 v[62:65], a[232:235], a[200:203], v[62:65]\n\t"
"s_waitcnt lgkmcnt(6)\n\t"
"v_mfma_f32_16x16x32_bf16 v[62:65], a[236:239], a[204:207], v[62:65]\n\t"
"s_waitcnt lgkmcnt(4)\n\t"
"ds_write_b128 v249, a[88:91] offset:12288\n\t"
"v_mfma_f32_16x16x32_bf16 v[232:235], v[232:235], a[128:131], 0\n\t"
"buffer_load_dwordx4 a[232:235], off, s[80:83], s20 offset:2048 sc1\n\t"
"v_mfma_f32_16x16x32_bf16 v[228:231], v[228:231], a[128:131], 0\n\t"
"s_waitcnt lgkmcnt(3)\n\t"
"s_mov_b32 s20, 503808\n\t"
"v_mfma_f32_16x16x32_bf16 v[236:239], v[236:239], a[128:131], 0\n\t"
"ds_write_b64 v250, a[156:157] offset:12288\n\t"
"v_mfma_f32_16x16x32_bf16 v[224:227], v[224:227], a[128:131], 0\n\t"
"buffer_load_dwordx4 a[236:239], off, s[80:83], s20 offset:0 sc1\n\t"
"v_mfma_f32_16x16x32_bf16 v[66:69], a[240:243], a[168:171], 0\n\t"
"ds_read_b128 a[168:171], v244 offset:33792\n\t"
"v_mfma_f32_16x16x32_bf16 v[66:69], a[248:251], a[176:179], v[66:69]\n\t"
"v_cvt_pk_bf16_f32 v232, v232, v233\n\t"
"v_cvt_pk_bf16_f32 v233, v234, v235\n\t"
"v_mfma_f32_16x16x32_bf16 v[66:69], a[244:247], a[172:175], v[66:69]\n\t"
"ds_write_b128 v249, a[80:83] offset:6144\n\t"
"v_mfma_f32_16x16x32_bf16 v[70:73], a[240:243], a[172:175], 0\n\t"
"v_cvt_pk_bf16_f32 v234, v228, v229\n\t"
"v_cvt_pk_bf16_f32 v235, v230, v231\n\t"
"v_mfma_f32_16x16x32_bf16 v[70:73], a[244:247], a[176:179], v[70:73]\n\t"
"ds_read_b128 a[172:175], v244 offset:34304\n\t"
"v_mfma_f32_16x16x32_bf16 v[70:73], a[248:251], a[180:183], v[70:73]\n\t"
"buffer_store_dwordx4 a[104:107], v248, s[16:19], s29 offen offset:128 nt\n\t"
"ds_write_b64 v250, a[148:149] offset:6144\n\t"
"v_cvt_pk_bf16_f32 v236, v236, v237\n\t"
"v_mfma_f32_16x16x32_bf16 v[74:77], a[248:251], a[184:187], 0\n\t"
"v_cvt_pk_bf16_f32 v237, v238, v239\n\t"
"v_cvt_pk_bf16_f32 v238, v224, v225\n\t"
"v_mfma_f32_16x16x32_bf16 v[74:77], a[244:247], a[180:183], v[74:77]\n\t"
"v_cvt_pk_bf16_f32 v239, v226, v227\n\t"
"v_mfma_f32_16x16x32_bf16 v[74:77], a[240:243], a[176:179], v[74:77]\n\t"
"ds_read_b128 a[176:179], v244 offset:34816\n\t"
"v_mfma_f32_16x16x32_bf16 v[78:81], a[240:243], a[180:183], 0\n\t"
"buffer_load_dwordx4 a[104:107], off, s[80:83], s20 offset:1024 sc1\n\t"
"v_mfma_f32_16x16x32_bf16 v[78:81], a[244:247], a[184:187], v[78:81]\n\t"
"ds_read_b128 a[180:183], v244 offset:35328\n\t"
"v_mfma_f32_16x16x32_bf16 v[78:81], a[248:251], a[188:191], v[78:81]\n\t"
"ds_write_b128 v249, a[96:99] offset:18432\n\t"
"v_mfma_f32_16x16x32_bf16 v[82:85], a[248:251], a[192:195], 0\n\t"
"v_mfma_f32_16x16x32_bf16 v[82:85], a[244:247], a[188:191], v[82:85]\n\t"
"v_mfma_f32_16x16x32_bf16 v[82:85], a[240:243], a[184:187], v[82:85]\n\t"
"ds_read_b128 a[184:187], v244 offset:35840\n\t"
"v_mfma_f32_16x16x32_bf16 v[86:89], a[240:243], a[188:191], 0\n\t"
"ds_read_b128 a[188:191], v244 offset:36352\n\t"
"v_mfma_f32_16x16x32_bf16 v[86:89], a[244:247], a[192:195], v[86:89]\n\t"
"v_mfma_f32_16x16x32_bf16 v[86:89], a[248:251], a[196:199], v[86:89]\n\t"
"buffer_store_dwordx4 a[112:115], v248, s[16:19], s29 offen offset:256 nt\n\t"
"ds_write_b64 v250, a[164:165] offset:18432\n\t"
"v_mfma_f32_16x16x32_bf16 v[90:93], a[248:251], a[200:203], 0\n\t"
"v_mfma_f32_16x16x32_bf16 v[90:93], a[244:247], a[196:199], v[90:93]\n\t"
"v_mfma_f32_16x16x32_bf16 v[90:93], a[240:243], a[192:195], v[90:93]\n\t"
"ds_read_b128 a[192:195], v244 offset:36864\n\t"
"v_mfma_f32_16x16x32_bf16 v[94:97], a[240:243], a[196:199], 0\n\t"
"buffer_load_dwordx4 a[112:115], off, s[80:83], s20 offset:2048 sc1\n\t"
"v_mfma_f32_16x16x32_bf16 v[94:97], a[244:247], a[200:203], v[94:97]\n\t"
"s_waitcnt vmcnt(42) lgkmcnt(6)\n\t"
"ds_read_b128 a[196:199], v244 offset:37376\n\t"
"s_mov_b32 s20, 110592\n\t"
"v_mfma_f32_16x16x32_bf16 v[94:97], a[248:251], a[204:207], v[94:97]\n\t"
"ds_read_b128 a[200:203], v244 offset:37888\n\t"
"v_mfma_f32_16x16x32_bf16 v[100:103], a[252:255], a[168:171], 0\n\t"
"s_waitcnt lgkmcnt(7)\n\t"
"ds_read_b128 a[204:207], v244 offset:38400\n\t"
"v_mfma_f32_16x16x32_bf16 v[100:103], a[28:31], a[176:179], v[100:103]\n\t"
"buffer_load_dwordx4 a[240:243], off, s[80:83], s20 offset:0 sc1\n\t"
"v_mfma_f32_16x16x32_bf16 v[100:103], a[152:155], a[172:175], v[100:103]\n\t"
"buffer_load_dwordx4 a[244:247], off, s[80:83], s20 offset:1024 sc1\n\t"
"v_mfma_f32_16x16x32_bf16 v[104:107], a[252:255], a[172:175], 0\n\t"
"s_waitcnt lgkmcnt(6)\n\t"
"v_mfma_f32_16x16x32_bf16 v[104:107], a[152:155], a[176:179], v[104:107]\n\t"
"v_mfma_f32_16x16x32_bf16 v[104:107], a[28:31], a[180:183], v[104:107]\n\t"
"s_waitcnt lgkmcnt(0)\n\t"
"s_barrier\n\t"
"v_mfma_f32_16x16x32_bf16 v[108:111], a[28:31], a[184:187], 0\n\t"
"s_add_u32 s29, s23, 0\n\t"
"ds_read_b64_tr_b16 v[228:229], v240 offset:40960\n\t"
"v_mfma_f32_16x16x32_bf16 v[108:111], a[152:155], a[180:183], v[108:111]\n\t"
"buffer_load_dwordx4 a[248:251], off, s[80:83], s20 offset:2048 sc1\n\t"
"v_mfma_f32_16x16x32_bf16 v[108:111], a[252:255], a[176:179], v[108:111]\n\t"
"ds_write_b128 v242, v[232:235] offset:24576\n\t"
"v_mfma_f32_16x16x32_bf16 v[112:115], a[252:255], a[180:183], 0\n\t"
"v_mfma_f32_16x16x32_bf16 v[112:115], a[152:155], a[184:187], v[112:115]\n\t"
"ds_read_b64_tr_b16 v[230:231], v240 offset:42496\n\t"
"v_mfma_f32_16x16x32_bf16 v[112:115], a[28:31], a[188:191], v[112:115]\n\t"
"ds_write_b128 v242, v[236:239] offset:26112\n\t"
"v_mfma_f32_16x16x32_bf16 v[116:119], a[28:31], a[192:195], 0\n\t"
"v_mfma_f32_16x16x32_bf16 v[116:119], a[152:155], a[188:191], v[116:119]\n\t"
"ds_read_b64_tr_b16 v[224:225], v241 offset:40960\n\t"
"v_mfma_f32_16x16x32_bf16 v[116:119], a[252:255], a[184:187], v[116:119]\n\t"
"ds_read_b64_tr_b16 v[226:227], v241 offset:42496\n\t"
"v_add_u32 v240, 59904, v240\n\t"
"v_add_u32 v241, 59904, v241\n\t"
"v_mfma_f32_16x16x32_bf16 v[120:123], a[252:255], a[188:191], 0\n\t"
"ds_read_b64_tr_b16 v[232:233], v240 offset:1024\n\t"
"v_mfma_f32_16x16x32_bf16 v[120:123], a[152:155], a[192:195], v[120:123]\n\t"
"ds_read_b64_tr_b16 v[234:235], v240 offset:2560\n\t"
"v_mfma_f32_16x16x32_bf16 v[120:123], a[28:31], a[196:199], v[120:123]\n\t"
"ds_read_b64_tr_b16 v[236:237], v241 offset:1024\n\t"
"v_mfma_f32_16x16x32_bf16 v[124:127], a[152:155], a[196:199], 0\n\t"
"s_waitcnt lgkmcnt(6)\n\t"
"ds_read_b64_tr_b16 v[238:239], v241 offset:2560\n\t"
"v_mfma_f32_16x16x32_bf16 v[124:127], a[252:255], a[192:195], v[124:127]\n\t"
"s_waitcnt lgkmcnt(4)\n\t"
"v_mfma_f32_16x16x32_bf16 v[124:127], a[28:31], a[200:203], v[124:127]\n\t"
"buffer_store_dwordx4 v[196:199], v248, s[16:19], s29 offen offset:0 nt\n\t"
"s_mov_b32 s20, 122880\n\t"
"v_mfma_f32_16x16x32_bf16 v[228:231], v[228:231], a[128:131], 0\n\t"
"s_waitcnt lgkmcnt(2)\n\t"
"v_mfma_f32_16x16x32_bf16 v[224:227], v[224:227], a[128:131], 0\n\t"
"v_mfma_f32_16x16x32_bf16 v[232:235], v[232:235], a[128:131], 0\n\t"
"s_waitcnt lgkmcnt(0)\n\t"
"v_mfma_f32_16x16x32_bf16 v[236:239], v[236:239], a[128:131], 0\n\t"
"buffer_store_dwordx4 v[204:207], v248, s[16:19], s29 offen offset:128 nt\n\t"
"v_mfma_f32_16x16x32_bf16 v[128:131], a[152:155], a[200:203], 0\n\t"
"s_waitcnt vmcnt(38)\n\t"
"v_mfma_f32_16x16x32_bf16 v[128:131], a[252:255], a[196:199], v[128:131]\n\t"
"buffer_load_dwordx4 a[252:255], off, s[80:83], s20 offset:0 sc1\n\t"
"v_mfma_f32_16x16x32_bf16 v[128:131], a[28:31], a[204:207], v[128:131]\n\t"
"v_cvt_pk_bf16_f32 v228, v228, v229\n\t"
"v_cvt_pk_bf16_f32 v229, v230, v231\n\t"
"v_mfma_f32_16x16x32_bf16 v[132:135], a[160:163], a[168:171], 0\n\t"
"v_cvt_pk_bf16_f32 v230, v224, v225\n\t"
"v_cvt_pk_bf16_f32 v231, v226, v227\n\t"
"v_mfma_f32_16x16x32_bf16 v[132:135], v[200:203], a[176:179], v[132:135]\n\t"
"ds_read_b64_tr_b16 v[224:225], v240 offset:20992\n\t"
"v_cvt_pk_bf16_f32 v232, v232, v233\n\t"
"v_mfma_f32_16x16x32_bf16 v[132:135], a[36:39], a[172:175], v[132:135]\n\t"
"ds_write_b128 v242, v[228:231] offset:27648\n\t"
"v_mfma_f32_16x16x32_bf16 v[136:139], a[160:163], a[172:175], 0\n\t"
"buffer_load_dwordx4 a[28:31], off, s[80:83], s20 offset:1024 sc1\n\t"
"v_mfma_f32_16x16x32_bf16 v[136:139], a[36:39], a[176:179], v[136:139]\n\t"
"ds_read_b64_tr_b16 v[226:227], v240 offset:22528\n\t"
"v_mfma_f32_16x16x32_bf16 v[136:139], v[200:203], a[180:183], v[136:139]\n\t"
"v_cvt_pk_bf16_f32 v233, v234, v235\n\t"
"v_cvt_pk_bf16_f32 v234, v236, v237\n\t"
"v_cvt_pk_bf16_f32 v235, v238, v239\n\t"
"v_mfma_f32_16x16x32_bf16 v[140:143], v[200:203], a[184:187], 0\n\t"
"ds_read_b64_tr_b16 v[236:237], v241 offset:20992\n\t"
"v_mfma_f32_16x16x32_bf16 v[140:143], a[36:39], a[180:183], v[140:143]\n\t"
"ds_read_b64_tr_b16 v[238:239], v241 offset:22528\n\t"
"v_mfma_f32_16x16x32_bf16 v[140:143], a[160:163], a[176:179], v[140:143]\n\t"
"ds_write_b128 v242, v[232:235] offset:29184\n\t"
"v_mfma_f32_16x16x32_bf16 v[144:147], a[160:163], a[180:183], 0\n\t"
"v_mfma_f32_16x16x32_bf16 v[144:147], a[36:39], a[184:187], v[144:147]\n\t"
"ds_read_b64_tr_b16 v[228:229], v240 offset:40960\n\t"
"v_mfma_f32_16x16x32_bf16 v[144:147], v[200:203], a[188:191], v[144:147]\n\t"
"ds_read_b64_tr_b16 v[230:231], v240 offset:42496\n\t"
"v_mfma_f32_16x16x32_bf16 v[148:151], a[160:163], a[184:187], 0\n\t"
"s_waitcnt lgkmcnt(5)\n\t"
"ds_read_b64_tr_b16 v[232:233], v241 offset:40960\n\t"
"v_mfma_f32_16x16x32_bf16 v[148:151], v[200:203], a[192:195], v[148:151]\n\t"
"s_waitcnt lgkmcnt(4)\n\t"
"ds_read_b64_tr_b16 v[234:235], v241 offset:42496\n\t"
"v_mfma_f32_16x16x32_bf16 v[148:151], a[36:39], a[188:191], v[148:151]\n\t"
"buffer_store_dwordx4 v[212:215], v248, s[16:19], s29 offen offset:256 nt\n\t"
"v_mfma_f32_16x16x32_bf16 v[224:227], v[224:227], a[128:131], 0\n\t"
"v_mfma_f32_16x16x32_bf16 v[236:239], v[236:239], a[128:131], 0\n\t"
"s_waitcnt lgkmcnt(2)\n\t"
"s_add_u32 s29, s23, 393216\n\t"
"v_mfma_f32_16x16x32_bf16 v[228:231], v[228:231], a[128:131], 0\n\t"
"s_waitcnt lgkmcnt(0)\n\t"
"v_mfma_f32_16x16x32_bf16 v[232:235], v[232:235], a[128:131], 0\n\t"
"ds_read_b128 v[196:199], v252 offset:0\n\t"
"v_mfma_f32_16x16x32_bf16 v[152:155], a[160:163], a[188:191], 0\n\t"
"ds_read_b128 a[16:19], v252 offset:12288\n\t"
"v_mfma_f32_16x16x32_bf16 v[152:155], v[200:203], a[196:199], v[152:155]\n\t"
"ds_read_b128 v[204:207], v252 offset:1024\n\t"
"v_cvt_pk_bf16_f32 v224, v224, v225\n\t"
"v_mfma_f32_16x16x32_bf16 v[152:155], a[36:39], a[192:195], v[152:155]\n\t"
"ds_read_b128 a[24:27], v252 offset:13312\n\t"
"v_cvt_pk_bf16_f32 v225, v226, v227\n\t"
"v_mfma_f32_16x16x32_bf16 v[156:159], a[160:163], a[192:195], 0\n\t"
"ds_read_b128 v[212:215], v252 offset:2048\n\t"
"v_cvt_pk_bf16_f32 v226, v236, v237\n\t"
"v_mfma_f32_16x16x32_bf16 v[156:159], a[36:39], a[196:199], v[156:159]\n\t"
"ds_read_b128 a[32:35], v252 offset:14336\n\t"
"v_cvt_pk_bf16_f32 v227, v238, v239\n\t"
"v_mfma_f32_16x16x32_bf16 v[156:159], v[200:203], a[200:203], v[156:159]\n\t"
"ds_write_b128 v242, v[224:227] offset:30720\n\t"
"v_mfma_f32_16x16x32_bf16 v[160:163], a[36:39], a[200:203], 0\n\t"
"buffer_load_dwordx4 a[36:39], off, s[80:83], s20 offset:2048 sc1\n\t"
"v_mfma_f32_16x16x32_bf16 v[160:163], a[160:163], a[196:199], v[160:163]\n\t"
"s_waitcnt vmcnt(37)\n\t"
"v_cvt_pk_bf16_f32 v228, v228, v229\n\t"
"v_cvt_pk_bf16_f32 v229, v230, v231\n\t"
"v_mfma_f32_16x16x32_bf16 v[160:163], v[200:203], a[204:207], v[160:163]\n\t"
"s_mov_b32 s20, 135168\n\t"
"v_cvt_pk_bf16_f32 v230, v232, v233\n\t"
"v_cvt_pk_bf16_f32 v231, v234, v235\n\t"
"v_mfma_f32_16x16x32_bf16 v[164:167], a[120:123], a[168:171], 0\n\t"
"v_mfma_f32_16x16x32_bf16 v[164:167], a[136:139], a[176:179], v[164:167]\n\t"
"v_mfma_f32_16x16x32_bf16 v[164:167], a[12:15], a[172:175], v[164:167]\n\t"
"buffer_store_dwordx4 a[0:3], v248, s[16:19], s29 offen offset:0 nt\n\t"
"v_mfma_f32_16x16x32_bf16 v[168:171], a[120:123], a[172:175], 0\n\t"
"v_mfma_f32_16x16x32_bf16 v[168:171], a[12:15], a[176:179], v[168:171]\n\t"
"v_mfma_f32_16x16x32_bf16 v[168:171], a[136:139], a[180:183], v[168:171]\n\t"
"ds_write_b128 v242, v[228:231] offset:32256\n\t"
"v_mfma_f32_16x16x32_bf16 v[172:175], a[136:139], a[184:187], 0\n\t"
"buffer_load_dwordx4 v[240:243], off, s[80:83], s20 offset:0 sc1\n\t"
"v_mfma_f32_16x16x32_bf16 v[172:175], a[12:15], a[180:183], v[172:175]\n\t"
"v_mfma_f32_16x16x32_bf16 v[172:175], a[120:123], a[176:179], v[172:175]\n\t"
"buffer_store_dwordx4 a[8:11], v248, s[16:19], s29 offen offset:128 nt\n\t"
"ds_read_b64_tr_b16 v[236:237], v246 offset:512\n\t"
"v_mfma_f32_16x16x32_bf16 v[176:179], a[120:123], a[180:183], 0\n\t"
"ds_read_b64_tr_b16 v[232:233], v255 offset:512\n\t"
"v_mfma_f32_16x16x32_bf16 v[176:179], a[12:15], a[184:187], v[176:179]\n\t"
"ds_read_b64_tr_b16 v[224:225], v246 offset:6656\n\t"
"v_mfma_f32_16x16x32_bf16 v[176:179], a[136:139], a[188:191], v[176:179]\n\t"
"s_waitcnt lgkmcnt(3)\n\t"
"s_barrier\n\t"
"v_mfma_f32_16x16x32_bf16 v[180:183], a[136:139], a[192:195], 0\n\t"
"ds_read_b128 a[168:171], v244 offset:24576\n\t"
"v_mfma_f32_16x16x32_bf16 v[180:183], a[12:15], a[188:191], v[180:183]\n\t"
"ds_read_b128 a[172:175], v244 offset:25088\n\t"
"v_mfma_f32_16x16x32_bf16 v[180:183], a[120:123], a[184:187], v[180:183]\n\t"
"ds_read_b128 a[176:179], v244 offset:25600\n\t"
"v_mfma_f32_16x16x32_bf16 v[184:187], a[120:123], a[188:191], 0\n\t"
"ds_read_b128 a[180:183], v244 offset:26112\n\t"
"v_mfma_f32_16x16x32_bf16 v[184:187], a[12:15], a[192:195], v[184:187]\n\t"
"ds_read_b64_tr_b16 v[228:229], v255 offset:6656\n\t"
"v_mfma_f32_16x16x32_bf16 v[184:187], a[136:139], a[196:199], v[184:187]\n\t"
"ds_read_b128 a[184:187], v244 offset:26624\n\t"
"v_mfma_f32_16x16x32_bf16 v[188:191], a[136:139], a[200:203], 0\n\t"
"ds_read_b128 a[188:191], v244 offset:27136\n\t"
"v_mfma_f32_16x16x32_bf16 v[188:191], a[12:15], a[196:199], v[188:191]\n\t"
"v_mfma_f32_16x16x32_bf16 v[188:191], a[120:123], a[192:195], v[188:191]\n\t"
"ds_read_b128 a[192:195], v244 offset:27648\n\t"
"v_mfma_f32_16x16x32_bf16 v[192:195], a[120:123], a[196:199], 0\n\t"
"s_waitcnt lgkmcnt(10)\n\t"
"ds_read_b128 a[196:199], v244 offset:28160\n\t"
"v_mfma_f32_16x16x32_bf16 v[192:195], a[12:15], a[200:203], v[192:195]\n\t"
"s_waitcnt lgkmcnt(10)\n\t"
"ds_read_b128 a[200:203], v244 offset:28672\n\t"
"v_mfma_f32_16x16x32_bf16 v[192:195], a[136:139], a[204:207], v[192:195]\n\t"
"buffer_store_dwordx4 a[40:43], v248, s[16:19], s29 offen offset:256 nt\n\t"
"ds_read_b128 a[204:207], v244 offset:29184\n\t"
"v_mfma_f32_4x4x4_16b_bf16 v[236:239], v[236:237], v[220:221], 0\n\t"
"s_waitcnt lgkmcnt(6)\n\t"
"ds_write_b128 v249, a[44:47] offset:0\n\t"
"v_mfma_f32_4x4x4_16b_bf16 v[232:235], v[232:233], v[220:221], 0\n\t"
"s_waitcnt vmcnt(36)\n\t"
"v_mfma_f32_4x4x4_16b_bf16 v[224:227], v[224:225], v[220:221], 0\n\t"
"v_mfma_f32_4x4x4_16b_bf16 v[228:231], v[228:229], v[220:221], 0\n\t"
"Lst19_%=:\n\t"
"v_mfma_f32_16x16x32_bf16 v[2:5], v[208:211], a[168:171], v[2:5]\n\t"
"s_add_u32 s29, s23, 786432\n\t"
"v_cvt_pk_bf16_f32 v236, v236, v237\n\t"
"v_cvt_pk_bf16_f32 v237, v238, v239\n\t"
"v_mfma_f32_16x16x32_bf16 v[2:5], a[4:7], a[176:179], v[2:5]\n\t"
"v_cvt_pk_bf16_f32 v238, v232, v233\n\t"
"v_cvt_pk_bf16_f32 v239, v234, v235\n\t"
"v_mfma_f32_16x16x32_bf16 v[2:5], v[216:219], a[172:175], v[2:5]\n\t"
"ds_write_b128 v249, a[60:63] offset:12288\n\t"
"v_mfma_f32_16x16x32_bf16 v[6:9], v[208:211], a[172:175], v[6:9]\n\t"
"s_waitcnt lgkmcnt(7)\n\t"
"v_cvt_pk_bf16_f32 v224, v224, v225\n\t"
"v_cvt_pk_bf16_f32 v225, v226, v227\n\t"
"v_mfma_f32_16x16x32_bf16 v[6:9], v[216:219], a[176:179], v[6:9]\n\t"
"v_cvt_pk_bf16_f32 v226, v228, v229\n\t"
"v_cvt_pk_bf16_f32 v227, v230, v231\n\t"
"v_mfma_f32_16x16x32_bf16 v[6:9], a[4:7], a[180:183], v[6:9]\n\t"
"ds_write_b128 v245, v[236:239] offset:512\n\t"
"v_mfma_f32_16x16x32_bf16 v[10:13], a[4:7], a[184:187], v[10:13]\n\t"
"s_waitcnt lgkmcnt(7)\n\t"
"v_mfma_f32_16x16x32_bf16 v[10:13], v[216:219], a[180:183], v[10:13]\n\t"
"v_mfma_f32_16x16x32_bf16 v[10:13], v[208:211], a[176:179], v[10:13]\n\t"
"ds_write_b64 v250, a[110:111] offset:0\n\t"
"v_mfma_f32_16x16x32_bf16 v[14:17], v[208:211], a[180:183], v[14:17]\n\t"
"s_waitcnt lgkmcnt(7)\n\t"
"ds_read_b64_tr_b16 v[232:233], v246 offset:12800\n\t"
"v_mfma_f32_16x16x32_bf16 v[14:17], v[216:219], a[184:187], v[14:17]\n\t"
"ds_read_b64_tr_b16 v[228:229], v255 offset:12800\n\t"
"v_mfma_f32_16x16x32_bf16 v[14:17], a[4:7], a[188:191], v[14:17]\n\t"
"ds_write_b64 v250, a[126:127] offset:12288\n\t"
"v_mfma_f32_16x16x32_bf16 v[18:21], a[4:7], a[192:195], v[18:21]\n\t"
"s_waitcnt lgkmcnt(9)\n\t"
"ds_read_b64_tr_b16 v[236:237], v246 offset:20480\n\t"
"v_mfma_f32_16x16x32_bf16 v[18:21], v[216:219], a[188:191], v[18:21]\n\t"
"v_mfma_f32_16x16x32_bf16 v[18:21], v[208:211], a[184:187], v[18:21]\n\t"
"ds_write_b128 v245, v[224:227] offset:6656\n\t"
"v_mfma_f32_16x16x32_bf16 v[22:25], v[208:211], a[188:191], v[22:25]\n\t"
"s_waitcnt lgkmcnt(10)\n\t"
"v_mfma_f32_16x16x32_bf16 v[22:25], v[216:219], a[192:195], v[22:25]\n\t"
"ds_read_b64_tr_b16 v[224:225], v255 offset:20480\n\t"
"v_mfma_f32_16x16x32_bf16 v[22:25], a[4:7], a[196:199], v[22:25]\n\t"
"buffer_store_dwordx4 v[196:199], v248, s[16:19], s29 offen offset:0 nt\n\t"
"ds_write_b64 v250, a[118:119] offset:6144\n\t"
"v_mfma_f32_16x16x32_bf16 v[26:29], a[4:7], a[200:203], v[26:29]\n\t"
"s_waitcnt lgkmcnt(11)\n\t"
"v_mfma_f32_16x16x32_bf16 v[26:29], v[216:219], a[196:199], v[26:29]\n\t"
"v_mfma_f32_16x16x32_bf16 v[26:29], v[208:211], a[192:195], v[26:29]\n\t"
"ds_write_b128 v249, a[52:55] offset:6144\n\t"
"v_mfma_f32_16x16x32_bf16 v[30:33], v[208:211], a[196:199], v[30:33]\n\t"
"s_waitcnt lgkmcnt(7)\n\t"
"v_mfma_f32_16x16x32_bf16 v[30:33], v[216:219], a[200:203], v[30:33]\n\t"
"s_waitcnt lgkmcnt(6)\n\t"
"v_mfma_f32_16x16x32_bf16 v[30:33], a[4:7], a[204:207], v[30:33]\n\t"
"s_waitcnt lgkmcnt(4)\n\t"
"ds_write_b128 v249, a[68:71] offset:18432\n\t"
"v_mfma_f32_4x4x4_16b_bf16 v[232:235], v[232:233], v[220:221], 0\n\t"
"buffer_store_dwordx4 v[204:207], v248, s[16:19], s29 offen offset:128 nt\n\t"
"v_mfma_f32_4x4x4_16b_bf16 v[228:231], v[228:229], v[220:221], 0\n\t"
"s_waitcnt vmcnt(33) lgkmcnt(3)\n\t"
"ds_write_b64 v250, a[134:135] offset:18432\n\t"
"v_mfma_f32_4x4x4_16b_bf16 v[236:239], v[236:237], v[220:221], 0\n\t"
"v_mfma_f32_4x4x4_16b_bf16 v[224:227], v[224:225], v[220:221], 0\n\t"
"v_mfma_f32_16x16x32_bf16 v[34:37], a[208:211], a[168:171], v[34:37]\n\t"
"v_cvt_pk_bf16_f32 v232, v232, v233\n\t"
"v_cvt_pk_bf16_f32 v233, v234, v235\n\t"
"v_mfma_f32_16x16x32_bf16 v[34:37], a[48:51], a[176:179], v[34:37]\n\t"
"v_cvt_pk_bf16_f32 v234, v228, v229\n\t"
"v_cvt_pk_bf16_f32 v235, v230, v231\n\t"
"v_mfma_f32_16x16x32_bf16 v[34:37], a[64:67], a[172:175], v[34:37]\n\t"
"ds_write_b128 v245, v[232:235] offset:12800\n\t"
"v_mfma_f32_16x16x32_bf16 v[38:41], a[208:211], a[172:175], v[38:41]\n\t"
"v_cvt_pk_bf16_f32 v236, v236, v237\n\t"
"v_cvt_pk_bf16_f32 v237, v238, v239\n\t"
"v_mfma_f32_16x16x32_bf16 v[38:41], a[64:67], a[176:179], v[38:41]\n\t"
"v_cvt_pk_bf16_f32 v238, v224, v225\n\t"
"v_cvt_pk_bf16_f32 v239, v226, v227\n\t"
"v_mfma_f32_16x16x32_bf16 v[38:41], a[48:51], a[180:183], v[38:41]\n\t"
"Lpg14_%=:\n\t"
"s_cbranch_vccnz Lvr1_%=\n\t"
"ds_write_b128 v245, v[236:239] offset:20480\n\t"
"v_mfma_f32_16x16x32_bf16 v[42:45], a[48:51], a[184:187], v[42:45]\n\t"
"v_mfma_f32_16x16x32_bf16 v[42:45], a[64:67], a[180:183], v[42:45]\n\t"
"v_mfma_f32_16x16x32_bf16 v[42:45], a[208:211], a[176:179], v[42:45]\n\t"
"buffer_store_dwordx4 v[212:215], v248, s[16:19], s29 offen offset:256 nt\n\t"
"ds_read_b64_tr_b16 v[228:229], v246 offset:26624\n\t"
"v_mfma_f32_16x16x32_bf16 v[46:49], a[208:211], a[180:183], v[46:49]\n\t"
"ds_read_b64_tr_b16 v[224:225], v255 offset:26624\n\t"
"v_mfma_f32_16x16x32_bf16 v[46:49], a[64:67], a[184:187], v[46:49]\n\t"
"ds_read_b64_tr_b16 v[232:233], v246 offset:32768\n\t"
"v_mfma_f32_16x16x32_bf16 v[46:49], a[48:51], a[188:191], v[46:49]\n\t"
"s_waitcnt lgkmcnt(5)\n\t"
"s_barrier\n\t"
"v_mfma_f32_16x16x32_bf16 v[50:53], a[48:51], a[192:195], v[50:53]\n\t"
"s_add_u32 s29, s23, 1179648\n\t"
"ds_read_b64_tr_b16 v[236:237], v255 offset:32768\n\t"
"v_mfma_f32_16x16x32_bf16 v[50:53], a[64:67], a[188:191], v[50:53]\n\t"
"buffer_load_dwordx4 a[116:119], off, s[80:83], s20 offset:1024 sc1\n\t"
"v_mfma_f32_16x16x32_bf16 v[50:53], a[208:211], a[184:187], v[50:53]\n\t"
"v_mfma_f32_16x16x32_bf16 v[54:57], a[208:211], a[188:191], v[54:57]\n\t"
"v_mfma_f32_16x16x32_bf16 v[54:57], a[64:67], a[192:195], v[54:57]\n\t"
"buffer_load_dwordx4 a[124:127], off, s[80:83], s20 offset:2048 sc1\n\t"
"v_mfma_f32_16x16x32_bf16 v[54:57], a[48:51], a[196:199], v[54:57]\n\t"
"buffer_store_dwordx4 a[16:19], v248, s[16:19], s29 offen offset:0 nt\n\t"
"s_mov_b32 s20, 147456\n\t"
"v_mfma_f32_16x16x32_bf16 v[58:61], a[48:51], a[200:203], v[58:61]\n\t"
"v_mfma_f32_16x16x32_bf16 v[58:61], a[64:67], a[196:199], v[58:61]\n\t"
"v_mfma_f32_16x16x32_bf16 v[58:61], a[208:211], a[192:195], v[58:61]\n\t"
"v_mfma_f32_16x16x32_bf16 v[62:65], a[208:211], a[196:199], v[62:65]\n\t"
"buffer_load_dwordx4 a[208:211], off, s[80:83], s20 offset:0 sc1\n\t"
"v_mfma_f32_16x16x32_bf16 v[62:65], a[64:67], a[200:203], v[62:65]\n\t"
"s_waitcnt lgkmcnt(2)\n\t"
"v_mfma_f32_16x16x32_bf16 v[62:65], a[48:51], a[204:207], v[62:65]\n\t"
"s_waitcnt lgkmcnt(1)\n\t"
"v_mfma_f32_4x4x4_16b_bf16 v[228:231], v[228:229], v[220:221], 0\n\t"
"s_waitcnt lgkmcnt(0)\n\t"
"v_mfma_f32_4x4x4_16b_bf16 v[224:227], v[224:225], v[220:221], 0\n\t"
"s_waitcnt vmcnt(35)\n\t"
"v_mfma_f32_4x4x4_16b_bf16 v[232:235], v[232:233], v[220:221], 0\n\t"
"v_mfma_f32_4x4x4_16b_bf16 v[236:239], v[236:237], v[220:221], 0\n\t"
"v_mfma_f32_16x16x32_bf16 v[66:69], a[56:59], a[168:171], v[66:69]\n\t"
"ds_read_b128 a[168:171], v244 offset:33792\n\t"
"v_cvt_pk_bf16_f32 v228, v228, v229\n\t"
"v_mfma_f32_16x16x32_bf16 v[66:69], a[216:219], a[176:179], v[66:69]\n\t"
"v_cvt_pk_bf16_f32 v229, v230, v231\n\t"
"v_cvt_pk_bf16_f32 v230, v224, v225\n\t"
"v_mfma_f32_16x16x32_bf16 v[66:69], a[212:215], a[172:175], v[66:69]\n\t"
"v_cvt_pk_bf16_f32 v231, v226, v227\n\t"
"v_cvt_pk_bf16_f32 v232, v232, v233\n\t"
"v_mfma_f32_16x16x32_bf16 v[70:73], a[56:59], a[172:175], v[70:73]\n\t"
"ds_read_b128 a[172:175], v244 offset:34304\n\t"
"v_cvt_pk_bf16_f32 v233, v234, v235\n\t"
"v_mfma_f32_16x16x32_bf16 v[70:73], a[212:215], a[176:179], v[70:73]\n\t"
"v_cvt_pk_bf16_f32 v234, v236, v237\n\t"
"v_cvt_pk_bf16_f32 v235, v238, v239\n\t"
"v_mfma_f32_16x16x32_bf16 v[70:73], a[216:219], a[180:183], v[70:73]\n\t"
"ds_write_b128 v245, v[228:231] offset:26624\n\t"
"v_mfma_f32_16x16x32_bf16 v[74:77], a[216:219], a[184:187], v[74:77]\n\t"
"v_mfma_f32_16x16x32_bf16 v[74:77], a[212:215], a[180:183], v[74:77]\n\t"
"v_mfma_f32_16x16x32_bf16 v[74:77], a[56:59], a[176:179], v[74:77]\n\t"
"ds_write_b128 v245, v[232:235] offset:32768\n\t"
"v_mfma_f32_16x16x32_bf16 v[78:81], a[56:59], a[180:183], v[78:81]\n\t"
"v_mfma_f32_16x16x32_bf16 v[78:81], a[212:215], a[184:187], v[78:81]\n\t"
"ds_read_b128 a[176:179], v244 offset:34816\n\t"
"v_mfma_f32_16x16x32_bf16 v[78:81], a[216:219], a[188:191], v[78:81]\n\t"
"buffer_store_dwordx4 a[24:27], v248, s[16:19], s29 offen offset:128 nt\n\t"
"ds_read_b64_tr_b16 v[224:225], v246 offset:40448\n\t"
"ds_read_b64_tr_b16 v[236:237], v255 offset:40448\n\t"
"v_mfma_f32_16x16x32_bf16 v[82:85], a[216:219], a[192:195], v[82:85]\n\t"
"ds_read_b128 a[180:183], v244 offset:35328\n\t"
"v_mfma_f32_16x16x32_bf16 v[82:85], a[212:215], a[188:191], v[82:85]\n\t"
"ds_read_b64_tr_b16 v[228:229], v246 offset:46592\n\t"
"v_mfma_f32_16x16x32_bf16 v[82:85], a[56:59], a[184:187], v[82:85]\n\t"
"ds_read_b64_tr_b16 v[232:233], v255 offset:46592\n\t"
"v_mfma_f32_16x16x32_bf16 v[86:89], a[56:59], a[188:191], v[86:89]\n\t"
"ds_read_b128 a[184:187], v244 offset:35840\n\t"
"v_mfma_f32_16x16x32_bf16 v[86:89], a[212:215], a[192:195], v[86:89]\n\t"
"ds_read_b128 v[196:199], v252 offset:0\n\t"
"v_mfma_f32_16x16x32_bf16 v[86:89], a[216:219], a[196:199], v[86:89]\n\t"
"buffer_store_dwordx4 a[32:35], v248, s[16:19], s29 offen offset:256 nt\n\t"
"ds_read_b128 a[24:27], v252 offset:12288\n\t"
"ds_read_b128 a[188:191], v244 offset:36352\n\t"
"v_mfma_f32_16x16x32_bf16 v[90:93], a[216:219], a[200:203], v[90:93]\n\t"
"ds_read_b128 v[204:207], v252 offset:1024\n\t"
"v_mfma_f32_16x16x32_bf16 v[90:93], a[212:215], a[196:199], v[90:93]\n\t"
"ds_read_b128 a[64:67], v252 offset:13312\n\t"
"v_mfma_f32_16x16x32_bf16 v[90:93], a[56:59], a[192:195], v[90:93]\n\t"
"ds_read_b128 v[212:215], v252 offset:2048\n\t"
"v_mfma_f32_16x16x32_bf16 v[94:97], a[56:59], a[196:199], v[94:97]\n\t"
"s_waitcnt lgkmcnt(11)\n\t"
"ds_read_b128 a[96:99], v252 offset:14336\n\t"
"v_mfma_f32_16x16x32_bf16 v[94:97], a[212:215], a[200:203], v[94:97]\n\t"
"buffer_load_dwordx4 a[212:215], off, s[80:83], s20 offset:1024 sc1\n\t"
"v_mfma_f32_16x16x32_bf16 v[94:97], a[216:219], a[204:207], v[94:97]\n\t"
"s_waitcnt lgkmcnt(9)\n\t"
"ds_read_b128 a[192:195], v244 offset:36864\n\t"
"s_add_u32 s29, s23, 1152\n\t"
"v_mfma_f32_4x4x4_16b_bf16 v[224:227], v[224:225], v[220:221], 0\n\t"
"s_waitcnt lgkmcnt(9)\n\t"
"v_mfma_f32_4x4x4_16b_bf16 v[236:239], v[236:237], v[220:221], 0\n\t"
"s_waitcnt vmcnt(33)\n\t"
"v_mfma_f32_4x4x4_16b_bf16 v[228:231], v[228:229], v[220:221], 0\n\t"
"s_waitcnt lgkmcnt(7)\n\t"
"v_mfma_f32_4x4x4_16b_bf16 v[232:235], v[232:233], v[220:221], 0\n\t"
"v_mfma_f32_16x16x32_bf16 v[100:103], a[220:223], a[168:171], v[100:103]\n\t"
"buffer_load_dwordx4 a[216:219], off, s[80:83], s20 offset:2048 sc1\n\t"
"v_mfma_f32_16x16x32_bf16 v[100:103], a[20:23], a[176:179], v[100:103]\n\t"
"ds_read_b128 a[196:199], v244 offset:37376\n\t"
"v_cvt_pk_bf16_f32 v224, v224, v225\n\t"
"v_mfma_f32_16x16x32_bf16 v[100:103], a[144:147], a[172:175], v[100:103]\n\t"
"ds_read_b128 a[200:203], v244 offset:37888\n\t"
"v_cvt_pk_bf16_f32 v225, v226, v227\n\t"
"v_mfma_f32_16x16x32_bf16 v[104:107], a[220:223], a[172:175], v[104:107]\n\t"
"ds_read_b128 a[204:207], v244 offset:38400\n\t"
"v_cvt_pk_bf16_f32 v226, v236, v237\n\t"
"v_mfma_f32_16x16x32_bf16 v[104:107], a[144:147], a[176:179], v[104:107]\n\t"
"v_cvt_pk_bf16_f32 v227, v238, v239\n\t"
"v_mfma_f32_16x16x32_bf16 v[104:107], a[20:23], a[180:183], v[104:107]\n\t"
"s_waitcnt lgkmcnt(0)\n\t"
"s_barrier\n\t"
"v_mfma_f32_16x16x32_bf16 v[108:111], a[20:23], a[184:187], v[108:111]\n\t"
"v_cvt_pk_bf16_f32 v228, v228, v229\n\t"
"v_mfma_f32_16x16x32_bf16 v[108:111], a[144:147], a[180:183], v[108:111]\n\t"
"v_cvt_pk_bf16_f32 v229, v230, v231\n\t"
"v_cvt_pk_bf16_f32 v230, v232, v233\n\t"
"v_mfma_f32_16x16x32_bf16 v[108:111], a[220:223], a[176:179], v[108:111]\n\t"
"ds_write_b128 v245, v[224:227] offset:40448\n\t"
"v_cvt_pk_bf16_f32 v231, v234, v235\n\t"
"v_mfma_f32_16x16x32_bf16 v[112:115], a[220:223], a[180:183], v[112:115]\n\t"
"v_fma_f32 v208, v104, s64, -v6\n\t"
"v_fma_f32 v209, v105, s64, -v7\n\t"
"v_mfma_f32_16x16x32_bf16 v[112:115], a[144:147], a[184:187], v[112:115]\n\t"
"ds_read_b64_tr_b16 v[236:237], v246 offset:52736\n\t"
"v_fma_f32 v210, v106, s64, -v8\n\t"
"v_mfma_f32_16x16x32_bf16 v[112:115], a[20:23], a[188:191], v[112:115]\n\t"
"buffer_store_dwordx4 v[196:199], v248, s[16:19], s29 offen offset:0 nt\n\t"
"ds_read_b64_tr_b16 v[232:233], v255 offset:52736\n\t"
"v_fma_f32 v211, v107, s64, -v9\n\t"
"v_fma_f32 v0, v2, s52, v100\n\t"
"v_fma_f32 v1, v3, s52, v101\n\t"
"v_mfma_f32_16x16x32_bf16 v[116:119], a[20:23], a[192:195], v[116:119]\n\t"
"v_fma_f32 v196, -v2, s56, -v100\n\t"
"v_fma_f32 v197, -v3, s56, -v101\n\t"
"v_mfma_f32_16x16x32_bf16 v[116:119], a[144:147], a[188:191], v[116:119]\n\t"
"v_fma_f32 v198, -v4, s56, -v102\n\t"
"v_fma_f32 v199, -v5, s56, -v103\n\t"
"v_mfma_f32_16x16x32_bf16 v[116:119], a[220:223], a[184:187], v[116:119]\n\t"
"ds_write_b128 v245, v[228:231] offset:46592\n\t"
"v_mfma_f32_16x16x32_bf16 v[120:123], a[220:223], a[188:191], v[120:123]\n\t"
"s_mov_b32 s20, 159744\n\t"
"v_fma_f32 v98, -v14, s56, -v112\n\t"
"v_fma_f32 v99, -v15, s56, -v113\n\t"
"v_mfma_f32_16x16x32_bf16 v[120:123], a[144:147], a[192:195], v[120:123]\n\t"
"v_accvgpr_write_b32 a0, v98\n\t"
"v_fma_f32 v98, -v16, s56, -v114\n\t"
"v_mfma_f32_16x16x32_bf16 v[120:123], a[20:23], a[196:199], v[120:123]\n\t"
"ds_write_b128 v249, a[76:79] offset:0\n\t"
"v_mfma_f32_16x16x32_bf16 v[124:127], a[20:23], a[200:203], v[124:127]\n\t"
"v_accvgpr_write_b32 a1, v99\n\t"
"v_fma_f32 v99, -v17, s56, -v115\n\t"
"v_mfma_f32_16x16x32_bf16 v[124:127], a[144:147], a[196:199], v[124:127]\n\t"
"v_accvgpr_write_b32 a2, v98\n\t"
"v_accvgpr_write_b32 a3, v99\n\t"
"v_mfma_f32_16x16x32_bf16 v[124:127], a[220:223], a[192:195], v[124:127]\n\t"
"ds_write_b128 v249, a[92:95] offset:12288\n\t"
"v_mfma_f32_16x16x32_bf16 v[128:131], a[220:223], a[196:199], v[128:131]\n\t"
"buffer_load_dwordx4 a[220:223], off, s[80:83], s20 offset:0 sc1\n\t"
"v_mfma_f32_16x16x32_bf16 v[128:131], a[144:147], a[200:203], v[128:131]\n\t"
"s_waitcnt lgkmcnt(3)\n\t"
"v_fma_f32 v98, -v18, s56, -v116\n\t"
"v_fma_f32 v99, -v19, s56, -v117\n\t"
"v_mfma_f32_16x16x32_bf16 v[128:131], a[20:23], a[204:207], v[128:131]\n\t"
"buffer_store_dwordx4 v[204:207], v248, s[16:19], s29 offen offset:128 nt\n\t"
"ds_write_b64 v250, a[142:143] offset:0\n\t"
"v_accvgpr_write_b32 a8, v98\n\t"
"v_mfma_f32_4x4x4_16b_bf16 v[236:239], v[236:237], v[220:221], 0\n\t"
"s_waitcnt vmcnt(32)\n\t"
"ds_write_b64 v250, a[158:159] offset:12288\n\t"
"v_mfma_f32_4x4x4_16b_bf16 v[232:235], v[232:233], v[220:221], 0\n\t"
"v_mfma_f32_16x16x32_bf16 v[132:135], a[224:227], a[168:171], v[132:135]\n\t"
"v_fma_f32 v204, -v6, s56, -v104\n\t"
"v_fma_f32 v205, -v7, s56, -v105\n\t"
"v_mfma_f32_16x16x32_bf16 v[132:135], a[232:235], a[176:179], v[132:135]\n\t"
"v_cvt_pk_bf16_f32 v236, v236, v237\n\t"
"v_cvt_pk_bf16_f32 v237, v238, v239\n\t"
"v_mfma_f32_16x16x32_bf16 v[132:135], a[228:231], a[172:175], v[132:135]\n\t"
"ds_write_b128 v249, a[84:87] offset:6144\n\t"
"v_mfma_f32_16x16x32_bf16 v[136:139], a[224:227], a[172:175], v[136:139]\n\t"
"v_cvt_pk_bf16_f32 v238, v232, v233\n\t"
"v_cvt_pk_bf16_f32 v239, v234, v235\n\t"
"v_mfma_f32_16x16x32_bf16 v[136:139], a[228:231], a[176:179], v[136:139]\n\t"
"v_fma_f32 v206, -v8, s56, -v106\n\t"
"v_fma_f32 v207, -v9, s56, -v107\n\t"
"v_mfma_f32_16x16x32_bf16 v[136:139], a[232:235], a[180:183], v[136:139]\n\t"
"ds_write_b128 v245, v[236:239] offset:52736\n\t"
"v_mfma_f32_16x16x32_bf16 v[140:143], a[232:235], a[184:187], v[140:143]\n\t"
"v_fma_f32 v98, -v20, s56, -v118\n\t"
"v_accvgpr_write_b32 a9, v99\n\t"
"v_mfma_f32_16x16x32_bf16 v[140:143], a[228:231], a[180:183], v[140:143]\n\t"
"v_fma_f32 v99, -v21, s56, -v119\n\t"
"v_accvgpr_write_b32 a10, v98\n\t"
"v_mfma_f32_16x16x32_bf16 v[140:143], a[224:227], a[176:179], v[140:143]\n\t"
"buffer_store_dwordx4 v[212:215], v248, s[16:19], s29 offen offset:256 nt\n\t"
"ds_write_b64 v250, a[150:151] offset:6144\n\t"
"v_accvgpr_write_b32 a11, v99\n\t"
"v_mfma_f32_16x16x32_bf16 v[144:147], a[224:227], a[180:183], v[144:147]\n\t"
"s_add_u32 s29, s23, 394368\n\t"
"v_fma_f32 v98, -v22, s56, -v120\n\t"
"v_fma_f32 v99, -v23, s56, -v121\n\t"
"v_mfma_f32_16x16x32_bf16 v[144:147], a[228:231], a[184:187], v[144:147]\n\t"
"v_fma_f32 v212, -v10, s56, -v108\n\t"
"v_fma_f32 v213, -v11, s56, -v109\n\t"
"v_mfma_f32_16x16x32_bf16 v[144:147], a[232:235], a[188:191], v[144:147]\n\t"
"ds_write_b128 v249, a[100:103] offset:18432\n\t"
"v_mfma_f32_16x16x32_bf16 v[148:151], a[232:235], a[192:195], v[148:151]\n\t"
"v_fma_f32 v214, -v12, s56, -v110\n\t"
"v_fma_f32 v215, -v13, s56, -v111\n\t"
"v_mfma_f32_16x16x32_bf16 v[148:151], a[228:231], a[188:191], v[148:151]\n\t"
"v_accvgpr_write_b32 a16, v98\n\t"
"v_fma_f32 v98, -v24, s56, -v122\n\t"
"v_mfma_f32_16x16x32_bf16 v[148:151], a[224:227], a[184:187], v[148:151]\n\t"
"ds_write_b64 v250, a[166:167] offset:18432\n\t"
"v_mfma_f32_16x16x32_bf16 v[152:155], a[224:227], a[188:191], v[152:155]\n\t"
"v_accvgpr_write_b32 a17, v99\n\t"
"v_fma_f32 v99, -v25, s56, -v123\n\t"
"v_mfma_f32_16x16x32_bf16 v[152:155], a[228:231], a[192:195], v[152:155]\n\t"
"v_accvgpr_write_b32 a18, v98\n\t"
"v_accvgpr_write_b32 a19, v99\n\t"
"v_mfma_f32_16x16x32_bf16 v[152:155], a[232:235], a[196:199], v[152:155]\n\t"
"v_fma_f32 v216, v108, s64, -v10\n\t"
"v_fma_f32 v217, v109, s64, -v11\n\t"
"v_mfma_f32_16x16x32_bf16 v[156:159], a[232:235], a[200:203], v[156:159]\n\t"
"v_fma_f32 v218, v110, s64, -v12\n\t"
"v_fma_f32 v219, v111, s64, -v13\n\t"
"v_mfma_f32_16x16x32_bf16 v[156:159], a[228:231], a[196:199], v[156:159]\n\t"
"v_mfma_f32_16x16x32_bf16 v[156:159], a[224:227], a[192:195], v[156:159]\n\t"
"buffer_store_dwordx4 a[24:27], v248, s[16:19], s29 offen offset:0 nt\n\t"
"v_mfma_f32_16x16x32_bf16 v[160:163], a[224:227], a[196:199], v[160:163]\n\t"
"s_waitcnt vmcnt(29)\n\t"
"v_mfma_f32_16x16x32_bf16 v[160:163], a[228:231], a[200:203], v[160:163]\n\t"
"buffer_load_dwordx4 a[224:227], off, s[80:83], s20 offset:1024 sc1\n\t"
"v_mfma_f32_16x16x32_bf16 v[160:163], a[232:235], a[204:207], v[160:163]\n\t"
"v_mfma_f32_16x16x32_bf16 v[164:167], a[236:239], a[168:171], v[164:167]\n\t"
"buffer_load_dwordx4 a[228:231], off, s[80:83], s20 offset:2048 sc1\n\t"
"v_mfma_f32_16x16x32_bf16 v[164:167], a[112:115], a[176:179], v[164:167]\n\t"
"s_mov_b32 s20, 172032\n\t"
"v_fma_f32 v98, -v26, s56, -v124\n\t"
"v_mfma_f32_16x16x32_bf16 v[164:167], a[104:107], a[172:175], v[164:167]\n\t"
"s_waitcnt lgkmcnt(0)\n\t"
"s_barrier\n\t"
"v_mfma_f32_16x16x32_bf16 v[168:171], a[236:239], a[172:175], v[168:171]\n\t"
"ds_read_b128 a[168:171], v222 offset:512\n\t"
"v_mfma_f32_16x16x32_bf16 v[168:171], a[104:107], a[176:179], v[168:171]\n\t"
"buffer_load_dwordx4 a[232:235], off, s[80:83], s20 offset:0 sc1\n\t"
"v_mfma_f32_16x16x32_bf16 v[168:171], a[112:115], a[180:183], v[168:171]\n\t"
"buffer_store_dwordx4 a[64:67], v248, s[16:19], s29 offen offset:128 nt\n\t"
"ds_read_b128 a[172:175], v223 offset:6656\n\t"
"v_fma_f32 v99, -v27, s56, -v125\n\t"
"v_accvgpr_write_b32 a24, v98\n\t"
"v_fma_f32 v98, -v28, s56, -v126\n\t"
"v_mfma_f32_16x16x32_bf16 v[172:175], a[112:115], a[184:187], v[172:175]\n\t"
"v_accvgpr_write_b32 a25, v99\n\t"
"v_fma_f32 v99, -v29, s56, -v127\n\t"
"v_mfma_f32_16x16x32_bf16 v[172:175], a[104:107], a[180:183], v[172:175]\n\t"
"v_accvgpr_write_b32 a26, v98\n\t"
"v_accvgpr_write_b32 a27, v99\n\t"
"v_mfma_f32_16x16x32_bf16 v[172:175], a[236:239], a[176:179], v[172:175]\n\t"
"ds_read_b128 a[176:179], v223 offset:12800\n\t"
"v_fma_f32 v98, -v30, s56, -v128\n\t"
"v_mfma_f32_16x16x32_bf16 v[176:179], a[236:239], a[180:183], v[176:179]\n\t"
"ds_read_b128 a[180:183], v222 offset:20480\n\t"
"v_fma_f32 v99, -v31, s56, -v129\n\t"
"v_mfma_f32_16x16x32_bf16 v[176:179], a[104:107], a[184:187], v[176:179]\n\t"
"v_accvgpr_write_b32 a32, v98\n\t"
"v_fma_f32 v98, -v32, s56, -v130\n\t"
"v_mfma_f32_16x16x32_bf16 v[176:179], a[112:115], a[188:191], v[176:179]\n\t"
"v_accvgpr_write_b32 a33, v99\n\t"
"v_fma_f32 v99, -v33, s56, -v131\n\t"
"v_mfma_f32_16x16x32_bf16 v[180:183], a[112:115], a[192:195], v[180:183]\n\t"
"ds_read_b128 a[128:131], v252 offset:0\n\t"
"v_accvgpr_write_b32 a34, v98\n\t"
"v_mfma_f32_16x16x32_bf16 v[180:183], a[104:107], a[188:191], v[180:183]\n\t"
"v_accvgpr_write_b32 a35, v99\n\t"
"v_fma_f32 v98, -v34, s56, -v132\n\t"
"v_mfma_f32_16x16x32_bf16 v[180:183], a[236:239], a[184:187], v[180:183]\n\t"
"ds_read_b128 a[184:187], v223 offset:26624\n\t"
"v_fma_f32 v99, -v35, s56, -v133\n\t"
"v_mfma_f32_16x16x32_bf16 v[184:187], a[236:239], a[188:191], v[184:187]\n\t"
"ds_read_b128 a[188:191], v223 offset:32768\n\t"
"v_accvgpr_write_b32 a40, v98\n\t"
"v_mfma_f32_16x16x32_bf16 v[184:187], a[104:107], a[192:195], v[184:187]\n\t"
"v_fma_f32 v98, -v36, s56, -v134\n\t"
"v_accvgpr_write_b32 a41, v99\n\t"
"v_mfma_f32_16x16x32_bf16 v[184:187], a[112:115], a[196:199], v[184:187]\n\t"
"v_fma_f32 v99, -v37, s56, -v135\n\t"
"v_accvgpr_write_b32 a42, v98\n\t"
"v_mfma_f32_16x16x32_bf16 v[188:191], a[112:115], a[200:203], v[188:191]\n\t"
"v_accvgpr_write_b32 a43, v99\n\t"
"v_fma_f32 v98, -v38, s56, -v136\n\t"
"v_mfma_f32_16x16x32_bf16 v[188:191], a[104:107], a[196:199], v[188:191]\n\t"
"v_fma_f32 v99, -v39, s56, -v137\n\t"
"v_accvgpr_write_b32 a48, v98\n\t"
"v_mfma_f32_16x16x32_bf16 v[188:191], a[236:239], a[192:195], v[188:191]\n\t"
"ds_read_b128 a[192:195], v222 offset:40448\n\t"
"v_fma_f32 v98, -v40, s56, -v138\n\t"
"v_mfma_f32_16x16x32_bf16 v[192:195], a[236:239], a[196:199], v[192:195]\n\t"
"s_waitcnt vmcnt(30) lgkmcnt(5)\n\t"
"ds_read_b128 a[196:199], v223 offset:46592\n\t"
"v_accvgpr_write_b32 a49, v99\n\t"
"v_mfma_f32_16x16x32_bf16 v[192:195], a[104:107], a[200:203], v[192:195]\n\t"
"ds_read_b128 a[200:203], v223 offset:52736\n\t"
"v_fma_f32 v99, -v41, s56, -v139\n\t"
"v_mfma_f32_16x16x32_bf16 v[192:195], a[112:115], a[204:207], v[192:195]\n\t"
"buffer_store_dwordx4 a[96:99], v248, s[16:19], s29 offen offset:256 nt\n\t"
"buffer_load_dwordx4 a[236:239], off, s[80:83], s20 offset:1024 sc1\n\t"
"ds_read_b128 a[204:207], v222 offset:60416\n\t"
"Lst20_%=:\n\t"
"v_mfma_f32_16x16x32_bf16 v[196:199], a[240:243], a[168:171], v[196:199]\n\t"
"s_waitcnt lgkmcnt(7)\n\t"
"v_accvgpr_write_b32 a50, v98\n\t"
"v_accvgpr_write_b32 a51, v99\n\t"
"v_mfma_f32_16x16x32_bf16 v[196:199], a[248:251], a[176:179], v[196:199]\n\t"
"s_add_u32 s29, s23, 787584\n\t"
"ds_read_b128 v[200:203], v252 offset:1024\n\t"
"v_fma_f32 v98, -v42, s56, -v140\n\t"
"v_mfma_f32_16x16x32_bf16 v[196:199], a[244:247], a[172:175], v[196:199]\n\t"
"v_fma_f32 v99, -v43, s56, -v141\n\t"
"v_accvgpr_write_b32 a56, v98\n\t"
"v_mfma_f32_16x16x32_bf16 v[204:207], a[240:243], a[172:175], v[204:207]\n\t"
"s_waitcnt lgkmcnt(6)\n\t"
"v_fma_f32 v98, -v44, s56, -v142\n\t"
"v_accvgpr_write_b32 a57, v99\n\t"
"v_mfma_f32_16x16x32_bf16 v[204:207], a[244:247], a[176:179], v[204:207]\n\t"
"v_fma_f32 v99, -v45, s56, -v143\n\t"
"v_accvgpr_write_b32 a58, v98\n\t"
"v_mfma_f32_16x16x32_bf16 v[204:207], a[248:251], a[180:183], v[204:207]\n\t"
"v_accvgpr_write_b32 a59, v99\n\t"
"v_fma_f32 v98, -v46, s56, -v144\n\t"
"v_mfma_f32_16x16x32_bf16 v[212:215], a[248:251], a[184:187], v[212:215]\n\t"
"s_waitcnt lgkmcnt(5)\n\t"
"v_fma_f32 v99, -v47, s56, -v145\n\t"
"v_accvgpr_write_b32 a64, v98\n\t"
"v_mfma_f32_16x16x32_bf16 v[212:215], a[244:247], a[180:183], v[212:215]\n\t"
"v_fma_f32 v98, -v48, s56, -v146\n\t"
"v_accvgpr_write_b32 a65, v99\n\t"
"v_mfma_f32_16x16x32_bf16 v[212:215], a[240:243], a[176:179], v[212:215]\n\t"
"v_fma_f32 v99, -v49, s56, -v147\n\t"
"v_accvgpr_write_b32 a66, v98\n\t"
"v_mfma_f32_16x16x32_bf16 a[0:3], a[240:243], a[180:183], a[0:3]\n\t"
"s_waitcnt lgkmcnt(4)\n\t"
"v_accvgpr_write_b32 a67, v99\n\t"
"v_fma_f32 v98, -v50, s56, -v148\n\t"
"v_mfma_f32_16x16x32_bf16 a[0:3], a[244:247], a[184:187], a[0:3]\n\t"
"v_fma_f32 v99, -v51, s56, -v149\n\t"
"v_accvgpr_write_b32 a72, v98\n\t"
"v_mfma_f32_16x16x32_bf16 a[0:3], a[248:251], a[188:191], a[0:3]\n\t"
"v_fma_f32 v98, -v52, s56, -v150\n\t"
"v_accvgpr_write_b32 a73, v99\n\t"
"v_mfma_f32_16x16x32_bf16 a[8:11], a[248:251], a[192:195], a[8:11]\n\t"
"s_waitcnt lgkmcnt(3)\n\t"
"v_fma_f32 v99, -v53, s56, -v151\n\t"
"v_accvgpr_write_b32 a74, v98\n\t"
"v_mfma_f32_16x16x32_bf16 a[8:11], a[244:247], a[188:191], a[8:11]\n\t"
"v_accvgpr_write_b32 a75, v99\n\t"
"v_fma_f32 v98, -v54, s56, -v152\n\t"
"v_mfma_f32_16x16x32_bf16 a[8:11], a[240:243], a[184:187], a[8:11]\n\t"
"buffer_store_dwordx4 a[128:131], v248, s[16:19], s29 offen offset:0 nt\n\t"
"ds_read_b128 a[44:47], v252 offset:12288\n\t"
"v_fma_f32 v99, -v55, s56, -v153\n\t"
"v_accvgpr_write_b32 a80, v98\n\t"
"v_fma_f32 v98, -v56, s56, -v154\n\t"
"v_mfma_f32_16x16x32_bf16 a[16:19], a[240:243], a[188:191], a[16:19]\n\t"
"s_waitcnt lgkmcnt(3)\n\t"
"ds_read_b128 a[12:15], v252 offset:2048\n\t"
"v_accvgpr_write_b32 a81, v99\n\t"
"v_mfma_f32_16x16x32_bf16 a[16:19], a[244:247], a[192:195], a[16:19]\n\t"
"s_waitcnt lgkmcnt(2)\n\t"
"ds_read_b128 a[76:79], v252 offset:13312\n\t"
"v_fma_f32 v99, -v57, s56, -v155\n\t"
"v_mfma_f32_16x16x32_bf16 a[16:19], a[248:251], a[196:199], a[16:19]\n\t"
"v_accvgpr_write_b32 a82, v98\n\t"
"v_accvgpr_write_b32 a83, v99\n\t"
"v_mfma_f32_16x16x32_bf16 a[24:27], a[248:251], a[200:203], a[24:27]\n\t"
"ds_read_b128 a[108:111], v252 offset:14336\n\t"
"v_fma_f32 v98, -v58, s56, -v156\n\t"
"v_mfma_f32_16x16x32_bf16 a[24:27], a[244:247], a[196:199], a[24:27]\n\t"
"v_fma_f32 v99, -v59, s56, -v157\n\t"
"v_accvgpr_write_b32 a88, v98\n\t"
"v_mfma_f32_16x16x32_bf16 a[24:27], a[240:243], a[192:195], a[24:27]\n\t"
"v_fma_f32 v98, -v60, s56, -v158\n\t"
"v_accvgpr_write_b32 a89, v99\n\t"
"v_mfma_f32_16x16x32_bf16 a[32:35], a[240:243], a[196:199], a[32:35]\n\t"
"s_waitcnt vmcnt(27)\n\t"
"v_fma_f32 v99, -v61, s56, -v159\n\t"
"v_accvgpr_write_b32 a90, v98\n\t"
"v_mfma_f32_16x16x32_bf16 a[32:35], a[244:247], a[200:203], a[32:35]\n\t"
"v_accvgpr_write_b32 a91, v99\n\t"
"v_fma_f32 v98, -v62, s56, -v160\n\t"
"v_mfma_f32_16x16x32_bf16 a[32:35], a[248:251], a[204:207], a[32:35]\n\t"
"v_fma_f32 v99, -v63, s56, -v161\n\t"
"v_accvgpr_write_b32 a96, v98\n\t"
"v_mfma_f32_16x16x32_bf16 a[40:43], a[252:255], a[168:171], a[40:43]\n\t"
"v_fma_f32 v98, -v64, s56, -v162\n\t"
"v_accvgpr_write_b32 a97, v99\n\t"
"v_mfma_f32_16x16x32_bf16 a[40:43], a[36:39], a[176:179], a[40:43]\n\t"
"v_fma_f32 v99, -v65, s56, -v163\n\t"
"v_accvgpr_write_b32 a98, v98\n\t"
"v_mfma_f32_16x16x32_bf16 a[40:43], a[28:31], a[172:175], a[40:43]\n\t"
"buffer_load_dwordx4 a[240:243], off, s[80:83], s20 offset:2048 sc1\n\t"
"s_mov_b32 s20, 36864\n\t"
"v_mfma_f32_16x16x32_bf16 a[48:51], a[252:255], a[172:175], a[48:51]\n\t"
"buffer_load_dwordx4 a[244:247], off, s[80:83], s20 offset:0 sc1\n\t"
"v_mfma_f32_16x16x32_bf16 a[48:51], a[28:31], a[176:179], a[48:51]\n\t"
"buffer_load_dwordx4 a[248:251], off, s[80:83], s20 offset:1024 sc1\n\t"
"v_mfma_f32_16x16x32_bf16 a[48:51], a[36:39], a[180:183], a[48:51]\n\t"
"buffer_store_dwordx4 v[200:203], v248, s[16:19], s29 offen offset:128 nt\n\t"
"v_accvgpr_write_b32 a99, v99\n\t"
"v_fma_f32 v98, -v66, s56, -v164\n\t"
"v_fma_f32 v99, -v67, s56, -v165\n\t"
"v_accvgpr_write_b32 a104, v98\n\t"
"v_mfma_f32_16x16x32_bf16 a[56:59], a[36:39], a[184:187], a[56:59]\n\t"
"s_waitcnt lgkmcnt(2)\n\t"
"v_fma_f32 v98, -v68, s56, -v166\n\t"
"v_accvgpr_write_b32 a105, v99\n\t"
"v_mfma_f32_16x16x32_bf16 a[56:59], a[28:31], a[180:183], a[56:59]\n\t"
"v_fma_f32 v99, -v69, s56, -v167\n\t"
"v_accvgpr_write_b32 a106, v98\n\t"
"v_mfma_f32_16x16x32_bf16 a[56:59], a[252:255], a[176:179], a[56:59]\n\t"
"v_accvgpr_write_b32 a107, v99\n\t"
"v_fma_f32 v98, -v70, s56, -v168\n\t"
"v_mfma_f32_16x16x32_bf16 a[64:67], a[252:255], a[180:183], a[64:67]\n\t"
"v_fma_f32 v99, -v71, s56, -v169\n\t"
"v_accvgpr_write_b32 a112, v98\n\t"
"v_mfma_f32_16x16x32_bf16 a[64:67], a[28:31], a[184:187], a[64:67]\n\t"
"v_fma_f32 v98, -v72, s56, -v170\n\t"
"v_mfma_f32_16x16x32_bf16 a[64:67], a[36:39], a[188:191], a[64:67]\n\t"
"s_waitcnt lgkmcnt(0)\n\t"
"s_barrier\n\t"
"v_mfma_f32_16x16x32_bf16 a[72:75], a[36:39], a[192:195], a[72:75]\n\t"
"v_accvgpr_write_b32 a113, v99\n\t"
"v_mfma_f32_16x16x32_bf16 a[72:75], a[28:31], a[188:191], a[72:75]\n\t"
"v_fma_f32 v99, -v73, s56, -v171\n\t"
"v_accvgpr_write_b32 a114, v98\n\t"
"v_mfma_f32_16x16x32_bf16 a[72:75], a[252:255], a[184:187], a[72:75]\n\t"
"v_accvgpr_write_b32 a115, v99\n\t"
"v_fma_f32 v98, -v74, s56, -v172\n\t"
"v_mfma_f32_16x16x32_bf16 a[80:83], a[252:255], a[188:191], a[80:83]\n\t"
"v_fma_f32 v99, -v75, s56, -v173\n\t"
"v_accvgpr_write_b32 a120, v98\n\t"
"v_mfma_f32_16x16x32_bf16 a[80:83], a[28:31], a[192:195], a[80:83]\n\t"
"v_fma_f32 v98, -v76, s56, -v174\n\t"
"v_accvgpr_write_b32 a121, v99\n\t"
"v_mfma_f32_16x16x32_bf16 a[80:83], a[36:39], a[196:199], a[80:83]\n\t"
"buffer_store_dwordx4 a[12:15], v248, s[16:19], s29 offen offset:256 nt\n\t"
"v_fma_f32 v99, -v77, s56, -v175\n\t"
"v_accvgpr_write_b32 a122, v98\n\t"
"v_accvgpr_write_b32 a123, v99\n\t"
"v_fma_f32 v98, -v78, s56, -v176\n\t"
"v_mfma_f32_16x16x32_bf16 a[88:91], a[36:39], a[200:203], a[88:91]\n\t"
"s_add_u32 s29, s23, 1180800\n\t"
"v_fma_f32 v99, -v79, s56, -v177\n\t"
"v_accvgpr_write_b32 a128, v98\n\t"
"v_mfma_f32_16x16x32_bf16 a[88:91], a[28:31], a[196:199], a[88:91]\n\t"
"v_fma_f32 v98, -v80, s56, -v178\n\t"
"v_accvgpr_write_b32 a129, v99\n\t"
"v_mfma_f32_16x16x32_bf16 a[88:91], a[252:255], a[192:195], a[88:91]\n\t"
"v_fma_f32 v99, -v81, s56, -v179\n\t"
"v_accvgpr_write_b32 a130, v98\n\t"
"v_mfma_f32_16x16x32_bf16 a[96:99], a[252:255], a[196:199], a[96:99]\n\t"
"s_waitcnt vmcnt(23)\n\t"
"v_accvgpr_write_b32 a131, v99\n\t"
"v_fma_f32 v98, -v82, s56, -v180\n\t"
"v_mfma_f32_16x16x32_bf16 a[96:99], a[28:31], a[200:203], a[96:99]\n\t"
"v_fma_f32 v99, -v83, s56, -v181\n\t"
"v_accvgpr_write_b32 a136, v98\n\t"
"v_mfma_f32_16x16x32_bf16 a[96:99], a[36:39], a[204:207], a[96:99]\n\t"
"v_fma_f32 v98, -v84, s56, -v182\n\t"
"v_accvgpr_write_b32 a137, v99\n\t"
"v_mfma_f32_16x16x32_bf16 a[104:107], v[240:243], a[168:171], a[104:107]\n\t"
"ds_read_b128 a[168:171], v222 offset:768\n\t"
"v_fma_f32 v99, -v85, s56, -v183\n\t"
"v_mfma_f32_16x16x32_bf16 a[104:107], a[124:127], a[176:179], a[104:107]\n\t"
"v_accvgpr_write_b32 a138, v98\n\t"
"v_accvgpr_write_b32 a139, v99\n\t"
"v_mfma_f32_16x16x32_bf16 a[104:107], a[116:119], a[172:175], a[104:107]\n\t"
"buffer_load_dwordx4 a[252:255], off, s[80:83], s20 offset:2048 sc1\n\t"
"v_mfma_f32_16x16x32_bf16 a[112:115], v[240:243], a[172:175], a[112:115]\n\t"
"ds_read_b128 a[172:175], v223 offset:6912\n\t"
"v_fma_f32 v98, -v86, s56, -v184\n\t"
"v_mfma_f32_16x16x32_bf16 a[112:115], a[116:119], a[176:179], a[112:115]\n\t"
"v_fma_f32 v99, -v87, s56, -v185\n\t"
"v_accvgpr_write_b32 a144, v98\n\t"
"v_mfma_f32_16x16x32_bf16 a[112:115], a[124:127], a[180:183], a[112:115]\n\t"
"buffer_store_dwordx4 a[44:47], v248, s[16:19], s29 offen offset:0 nt\n\t"
"v_fma_f32 v98, -v88, s56, -v186\n\t"
"v_accvgpr_write_b32 a145, v99\n\t"
"v_fma_f32 v99, -v89, s56, -v187\n\t"
"v_accvgpr_write_b32 a146, v98\n\t"
"v_mfma_f32_16x16x32_bf16 a[120:123], a[124:127], a[184:187], a[120:123]\n\t"
"v_accvgpr_write_b32 a147, v99\n\t"
"v_fma_f32 v98, -v90, s56, -v188\n\t"
"v_mfma_f32_16x16x32_bf16 a[120:123], a[116:119], a[180:183], a[120:123]\n\t"
"v_fma_f32 v99, -v91, s56, -v189\n\t"
"v_accvgpr_write_b32 a152, v98\n\t"
"v_mfma_f32_16x16x32_bf16 a[120:123], v[240:243], a[176:179], a[120:123]\n\t"
"ds_read_b128 a[176:179], v223 offset:13056\n\t"
"v_fma_f32 v98, -v92, s56, -v190\n\t"
"v_mfma_f32_16x16x32_bf16 a[128:131], v[240:243], a[180:183], a[128:131]\n\t"
"ds_read_b128 a[180:183], v222 offset:20736\n\t"
"v_accvgpr_write_b32 a153, v99\n\t"
"v_mfma_f32_16x16x32_bf16 a[128:131], a[116:119], a[184:187], a[128:131]\n\t"
"v_fma_f32 v99, -v93, s56, -v191\n\t"
"v_accvgpr_write_b32 a154, v98\n\t"
"v_mfma_f32_16x16x32_bf16 a[128:131], a[124:127], a[188:191], a[128:131]\n\t"
"v_accvgpr_write_b32 a155, v99\n\t"
"v_fma_f32 v98, -v94, s56, -v192\n\t"
"v_mfma_f32_16x16x32_bf16 a[136:139], a[124:127], a[192:195], a[136:139]\n\t"
"v_fma_f32 v99, -v95, s56, -v193\n\t"
"v_accvgpr_write_b32 a160, v98\n\t"
"v_mfma_f32_16x16x32_bf16 a[136:139], a[116:119], a[188:191], a[136:139]\n\t"
"v_fma_f32 v98, -v96, s56, -v194\n\t"
"v_accvgpr_write_b32 a161, v99\n\t"
"v_mfma_f32_16x16x32_bf16 a[136:139], v[240:243], a[184:187], a[136:139]\n\t"
"ds_read_b128 a[184:187], v223 offset:26880\n\t"
"v_fma_f32 v99, -v97, s56, -v195\n\t"
"v_mfma_f32_16x16x32_bf16 a[144:147], v[240:243], a[188:191], a[144:147]\n\t"
"s_mov_b32 s20, 49152\n\t"
"ds_read_b128 a[188:191], v223 offset:33024\n\t"
"v_accvgpr_write_b32 a162, v98\n\t"
"v_mfma_f32_16x16x32_bf16 a[144:147], a[116:119], a[192:195], a[144:147]\n\t"
"Lpg15_%=:\n\t"
"s_cbranch_vccnz Lvr1_%=\n\t"
"v_accvgpr_write_b32 a163, v99\n\t"
"v_fma_f32 v200, v100, s64, -v2\n\t"
"v_mfma_f32_16x16x32_bf16 a[144:147], a[124:127], a[196:199], a[144:147]\n\t"
"buffer_store_dwordx4 a[76:79], v248, s[16:19], s29 offen offset:128 nt\n\t"
"v_fma_f32 v201, v101, s64, -v3\n\t"
"v_fma_f32 v202, v102, s64, -v4\n\t"
"v_fma_f32 v203, v103, s64, -v5\n\t"
"v_fma_f32 v98, v112, s64, -v14\n\t"
"v_mfma_f32_16x16x32_bf16 a[152:155], a[124:127], a[200:203], a[152:155]\n\t"
"v_fma_f32 v99, v113, s64, -v15\n\t"
"v_accvgpr_write_b32 a4, v98\n\t"
"v_mfma_f32_16x16x32_bf16 a[152:155], a[116:119], a[196:199], a[152:155]\n\t"
"v_fma_f32 v98, v114, s64, -v16\n\t"
"v_accvgpr_write_b32 a5, v99\n\t"
"v_mfma_f32_16x16x32_bf16 a[152:155], v[240:243], a[192:195], a[152:155]\n\t"
"ds_read_b128 a[192:195], v222 offset:40704\n\t"
"v_fma_f32 v99, v115, s64, -v17\n\t"
"v_mfma_f32_16x16x32_bf16 a[160:163], v[240:243], a[196:199], a[160:163]\n\t"
"s_waitcnt vmcnt(20) lgkmcnt(4)\n\t"
"ds_read_b128 a[196:199], v223 offset:46848\n\t"
"v_accvgpr_write_b32 a6, v98\n\t"
"v_mfma_f32_16x16x32_bf16 a[160:163], a[116:119], a[200:203], a[160:163]\n\t"
"ds_read_b128 a[200:203], v223 offset:52992\n\t"
"v_accvgpr_write_b32 a7, v99\n\t"
"v_mfma_f32_16x16x32_bf16 a[160:163], a[124:127], a[204:207], a[160:163]\n\t"
"ds_read_b128 a[204:207], v222 offset:60672\n\t"
"v_fma_f32 v98, v116, s64, -v18\n\t"
"v_mfma_f32_16x16x32_bf16 v[200:203], a[208:211], a[168:171], v[200:203]\n\t"
"s_waitcnt lgkmcnt(6)\n\t"
"v_fma_f32 v99, v117, s64, -v19\n\t"
"v_accvgpr_write_b32 a12, v98\n\t"
"v_mfma_f32_16x16x32_bf16 v[200:203], a[216:219], a[176:179], v[200:203]\n\t"
"v_fma_f32 v98, v118, s64, -v20\n\t"
"v_accvgpr_write_b32 a13, v99\n\t"
"v_mfma_f32_16x16x32_bf16 v[200:203], a[212:215], a[172:175], v[200:203]\n\t"
"buffer_load_dwordx4 v[240:243], off, s[80:83], s20 offset:0 sc1\n\t"
"v_mfma_f32_16x16x32_bf16 v[208:211], a[208:211], a[172:175], v[208:211]\n\t"
"s_waitcnt lgkmcnt(5)\n\t"
"v_fma_f32 v99, v119, s64, -v21\n\t"
"v_accvgpr_write_b32 a14, v98\n\t"
"v_mfma_f32_16x16x32_bf16 v[208:211], a[212:215], a[176:179], v[208:211]\n\t"
"v_accvgpr_write_b32 a15, v99\n\t"
"v_mfma_f32_16x16x32_bf16 v[208:211], a[216:219], a[180:183], v[208:211]\n\t"
"s_waitcnt lgkmcnt(0)\n\t"
"s_barrier\n\t"
"v_mfma_f32_16x16x32_bf16 v[216:219], a[216:219], a[184:187], v[216:219]\n\t"
"v_fma_f32 v98, v120, s64, -v22\n\t"
"v_mfma_f32_16x16x32_bf16 v[216:219], a[212:215], a[180:183], v[216:219]\n\t"
"v_fma_f32 v99, v121, s64, -v23\n\t"
"v_accvgpr_write_b32 a20, v98\n\t"
"v_mfma_f32_16x16x32_bf16 v[216:219], a[208:211], a[176:179], v[216:219]\n\t"
"buffer_store_dwordx4 a[108:111], v248, s[16:19], s29 offen offset:256 nt\n\t"
"ds_read_b64_tr_b16 v[224:225], v246 offset:1024\n\t"
"v_fma_f32 v98, v122, s64, -v24\n\t"
"v_accvgpr_write_b32 a21, v99\n\t"
"v_fma_f32 v99, v123, s64, -v25\n\t"
"v_mfma_f32_16x16x32_bf16 a[4:7], a[208:211], a[180:183], a[4:7]\n\t"
"ds_read_b64_tr_b16 v[228:229], v255 offset:1024\n\t"
"v_accvgpr_write_b32 a22, v98\n\t"
"v_mfma_f32_16x16x32_bf16 a[4:7], a[212:215], a[184:187], a[4:7]\n\t"
"v_accvgpr_write_b32 a23, v99\n\t"
"v_fma_f32 v98, v124, s64, -v26\n\t"
"v_mfma_f32_16x16x32_bf16 a[4:7], a[216:219], a[188:191], a[4:7]\n\t"
"buffer_load_dwordx4 v[248:251], off, s[80:83], s20 offset:1024 sc1\n\t"
"v_mfma_f32_16x16x32_bf16 a[12:15], a[216:219], a[192:195], a[12:15]\n\t"
"v_fma_f32 v99, v125, s64, -v27\n\t"
"v_accvgpr_write_b32 a28, v98\n\t"
"v_mfma_f32_16x16x32_bf16 a[12:15], a[212:215], a[188:191], a[12:15]\n\t"
"v_fma_f32 v98, v126, s64, -v28\n\t"
"v_accvgpr_write_b32 a29, v99\n\t"
"v_mfma_f32_16x16x32_bf16 a[12:15], a[208:211], a[184:187], a[12:15]\n\t"
"v_fma_f32 v99, v127, s64, -v29\n\t"
"v_accvgpr_write_b32 a30, v98\n\t"
"v_mfma_f32_16x16x32_bf16 a[20:23], a[208:211], a[188:191], a[20:23]\n\t"
"v_accvgpr_write_b32 a31, v99\n\t"
"v_fma_f32 v98, v128, s64, -v30\n\t"
"v_mfma_f32_16x16x32_bf16 a[20:23], a[212:215], a[192:195], a[20:23]\n\t"
"v_fma_f32 v99, v129, s64, -v31\n\t"
"v_accvgpr_write_b32 a36, v98\n\t"
"v_mfma_f32_16x16x32_bf16 a[20:23], a[216:219], a[196:199], a[20:23]\n\t"
"v_fma_f32 v98, v130, s64, -v32\n\t"
"v_accvgpr_write_b32 a37, v99\n\t"
"v_fma_f32 v99, v131, s64, -v33\n\t"
"v_mfma_f32_16x16x32_bf16 a[28:31], a[216:219], a[200:203], a[28:31]\n\t"
"v_accvgpr_write_b32 a38, v98\n\t"
"v_accvgpr_write_b32 a39, v99\n\t"
"v_mfma_f32_16x16x32_bf16 a[28:31], a[212:215], a[196:199], a[28:31]\n\t"
"v_fma_f32 v98, v132, s64, -v34\n\t"
"v_fma_f32 v99, v133, s64, -v35\n\t"
"v_mfma_f32_16x16x32_bf16 a[28:31], a[208:211], a[192:195], a[28:31]\n\t"
"v_accvgpr_write_b32 a44, v98\n\t"
"v_fma_f32 v98, v134, s64, -v36\n\t"
"v_mfma_f32_16x16x32_bf16 a[36:39], a[208:211], a[196:199], a[36:39]\n\t"
"s_waitcnt lgkmcnt(1)\n\t"
"v_accvgpr_write_b32 a45, v99\n\t"
"v_fma_f32 v99, v135, s64, -v37\n\t"
"v_mfma_f32_16x16x32_bf16 a[36:39], a[212:215], a[200:203], a[36:39]\n\t"
"s_waitcnt lgkmcnt(0)\n\t"
"v_accvgpr_write_b32 a46, v98\n\t"
"v_accvgpr_write_b32 a47, v99\n\t"
"v_mfma_f32_16x16x32_bf16 a[36:39], a[216:219], a[204:207], a[36:39]\n\t"
"s_waitcnt vmcnt(16)\n\t"
"v_fma_f32 v98, v136, s64, -v38\n\t"
"v_fma_f32 v99, v137, s64, -v39\n\t"
"v_mfma_f32_4x4x4_16b_bf16 v[224:227], v[224:225], v[220:221], 0\n\t"
"buffer_load_dwordx4 a[208:211], off, s[80:83], s20 offset:2048 sc1\n\t"
"s_mov_b32 s20, 61440\n\t"
"v_mfma_f32_4x4x4_16b_bf16 v[228:231], v[228:229], v[220:221], 0\n\t"
"v_mfma_f32_16x16x32_bf16 a[44:47], a[220:223], a[168:171], a[44:47]\n\t"
"buffer_load_dwordx4 a[212:215], off, s[80:83], s20 offset:0 sc1\n\t"
"v_mfma_f32_16x16x32_bf16 a[44:47], a[228:231], a[176:179], a[44:47]\n\t"
"buffer_load_dwordx4 a[216:219], off, s[80:83], s20 offset:1024 sc1\n\t"
"v_mfma_f32_16x16x32_bf16 a[44:47], a[224:227], a[172:175], a[44:47]\n\t"
"v_accvgpr_write_b32 a52, v98\n\t"
"v_fma_f32 v98, v138, s64, -v40\n\t"
"v_accvgpr_write_b32 a53, v99\n\t"
"v_fma_f32 v99, v139, s64, -v41\n\t"
"v_accvgpr_write_b32 a54, v98\n\t"
"v_accvgpr_write_b32 a55, v99\n\t"
"v_fma_f32 v98, v140, s64, -v42\n\t"
"v_fma_f32 v99, v141, s64, -v43\n\t"
"v_mfma_f32_16x16x32_bf16 a[52:55], a[220:223], a[172:175], a[52:55]\n\t"
"v_accvgpr_write_b32 a60, v98\n\t"
"v_fma_f32 v98, v142, s64, -v44\n\t"
"v_mfma_f32_16x16x32_bf16 a[52:55], a[224:227], a[176:179], a[52:55]\n\t"
"v_accvgpr_write_b32 a61, v99\n\t"
"v_fma_f32 v99, v143, s64, -v45\n\t"
"v_mfma_f32_16x16x32_bf16 a[52:55], a[228:231], a[180:183], a[52:55]\n\t"
"v_accvgpr_write_b32 a62, v98\n\t"
"v_accvgpr_write_b32 a63, v99\n\t"
"v_fma_f32 v98, v144, s64, -v46\n\t"
"v_fma_f32 v99, v145, s64, -v47\n\t"
"v_cvt_pk_bf16_f32 v224, v224, v225\n\t"
"v_cvt_pk_bf16_f32 v225, v226, v227\n\t"
"v_cvt_pk_bf16_f32 v226, v228, v229\n\t"
"v_cvt_pk_bf16_f32 v227, v230, v231\n\t"
"v_mfma_f32_16x16x32_bf16 a[60:63], a[228:231], a[184:187], a[60:63]\n\t"
"v_accvgpr_write_b32 a68, v98\n\t"
"v_fma_f32 v98, v146, s64, -v48\n\t"
"v_mfma_f32_16x16x32_bf16 a[60:63], a[224:227], a[180:183], a[60:63]\n\t"
"v_accvgpr_write_b32 a69, v99\n\t"
"v_fma_f32 v99, v147, s64, -v49\n\t"
"v_mfma_f32_16x16x32_bf16 a[60:63], a[220:223], a[176:179], a[60:63]\n\t"
"ds_write_b128 v245, v[224:227] offset:1024\n\t"
"v_accvgpr_write_b32 a70, v98\n\t"
"v_accvgpr_write_b32 a71, v99\n\t"
"v_fma_f32 v98, v148, s64, -v50\n\t"
"v_fma_f32 v99, v149, s64, -v51\n\t"
"v_mfma_f32_16x16x32_bf16 a[68:71], a[220:223], a[180:183], a[68:71]\n\t"
"ds_read_b64_tr_b16 v[232:233], v246 offset:7168\n\t"
"v_mfma_f32_16x16x32_bf16 a[68:71], a[224:227], a[184:187], a[68:71]\n\t"
"ds_read_b64_tr_b16 v[236:237], v255 offset:7168\n\t"
"v_accvgpr_write_b32 a76, v98\n\t"
"v_mfma_f32_16x16x32_bf16 a[68:71], a[228:231], a[188:191], a[68:71]\n\t"
"v_fma_f32 v98, v150, s64, -v52\n\t"
"v_accvgpr_write_b32 a77, v99\n\t"
"v_fma_f32 v99, v151, s64, -v53\n\t"
"v_accvgpr_write_b32 a78, v98\n\t"
"v_accvgpr_write_b32 a79, v99\n\t"
"v_fma_f32 v98, v152, s64, -v54\n\t"
"v_fma_f32 v99, v153, s64, -v55\n\t"
"v_mfma_f32_16x16x32_bf16 a[76:79], a[228:231], a[192:195], a[76:79]\n\t"
"v_accvgpr_write_b32 a84, v98\n\t"
"v_fma_f32 v98, v154, s64, -v56\n\t"
"v_mfma_f32_16x16x32_bf16 a[76:79], a[224:227], a[188:191], a[76:79]\n\t"
"v_accvgpr_write_b32 a85, v99\n\t"
"v_fma_f32 v99, v155, s64, -v57\n\t"
"v_mfma_f32_16x16x32_bf16 a[76:79], a[220:223], a[184:187], a[76:79]\n\t"
"v_accvgpr_write_b32 a86, v98\n\t"
"v_accvgpr_write_b32 a87, v99\n\t"
"v_fma_f32 v98, v156, s64, -v58\n\t"
"v_fma_f32 v99, v157, s64, -v59\n\t"
"v_mfma_f32_16x16x32_bf16 a[84:87], a[220:223], a[188:191], a[84:87]\n\t"
"v_accvgpr_write_b32 a92, v98\n\t"
"v_fma_f32 v98, v158, s64, -v60\n\t"
"v_mfma_f32_16x16x32_bf16 a[84:87], a[224:227], a[192:195], a[84:87]\n\t"
"v_accvgpr_write_b32 a93, v99\n\t"
"v_fma_f32 v99, v159, s64, -v61\n\t"
"v_mfma_f32_16x16x32_bf16 a[84:87], a[228:231], a[196:199], a[84:87]\n\t"
"v_accvgpr_write_b32 a94, v98\n\t"
"v_accvgpr_write_b32 a95, v99\n\t"
"v_fma_f32 v98, v160, s64, -v62\n\t"
"v_fma_f32 v99, v161, s64, -v63\n\t"
"v_mfma_f32_16x16x32_bf16 a[92:95], a[228:231], a[200:203], a[92:95]\n\t"
"v_accvgpr_write_b32 a100, v98\n\t"
"v_fma_f32 v98, v162, s64, -v64\n\t"
"v_mfma_f32_16x16x32_bf16 a[92:95], a[224:227], a[196:199], a[92:95]\n\t"
"v_accvgpr_write_b32 a101, v99\n\t"
"v_fma_f32 v99, v163, s64, -v65\n\t"
"v_mfma_f32_16x16x32_bf16 a[92:95], a[220:223], a[192:195], a[92:95]\n\t"
"v_accvgpr_write_b32 a102, v98\n\t"
"v_accvgpr_write_b32 a103, v99\n\t"
"s_nop 1\n\t"
"v_mfma_f32_16x16x32_bf16 a[100:103], a[220:223], a[196:199], a[100:103]\n\t"
"buffer_load_dwordx4 a[220:223], off, s[80:83], s20 offset:2048 sc1\n\t"
"v_mfma_f32_16x16x32_bf16 a[100:103], a[224:227], a[200:203], a[100:103]\n\t"
"s_waitcnt lgkmcnt(0)\n\t"
"s_mov_b32 s20, 73728\n\t"
"v_fma_f32 v98, v164, s64, -v66\n\t"
"v_mfma_f32_16x16x32_bf16 a[100:103], a[228:231], a[204:207], a[100:103]\n\t"
"v_fma_f32 v99, v165, s64, -v67\n\t"
"v_accvgpr_write_b32 a108, v98\n\t"
"v_fma_f32 v98, v166, s64, -v68\n\t"
"v_accvgpr_write_b32 a109, v99\n\t"
"v_fma_f32 v99, v167, s64, -v69\n\t"
"v_accvgpr_write_b32 a110, v98\n\t"
"v_accvgpr_write_b32 a111, v99\n\t"
"v_mfma_f32_4x4x4_16b_bf16 v[232:235], v[232:233], v[220:221], 0\n\t"
"s_waitcnt vmcnt(14)\n\t"
"v_fma_f32 v98, v168, s64, -v70\n\t"
"v_fma_f32 v99, v169, s64, -v71\n\t"
"v_mfma_f32_4x4x4_16b_bf16 v[236:239], v[236:237], v[220:221], 0\n\t"
"v_mfma_f32_16x16x32_bf16 a[108:111], a[232:235], a[168:171], a[108:111]\n\t"
"buffer_load_dwordx4 a[224:227], off, s[80:83], s20 offset:0 sc1\n\t"
"v_mfma_f32_16x16x32_bf16 a[108:111], a[240:243], a[176:179], a[108:111]\n\t"
"buffer_load_dwordx4 a[228:231], off, s[80:83], s20 offset:1024 sc1\n\t"
"v_mfma_f32_16x16x32_bf16 a[108:111], a[236:239], a[172:175], a[108:111]\n\t"
"ds_read_b128 a[168:171], v222 offset:2048\n\t"
"v_accvgpr_write_b32 a116, v98\n\t"
"v_fma_f32 v98, v170, s64, -v72\n\t"
"v_accvgpr_write_b32 a117, v99\n\t"
"v_fma_f32 v99, v171, s64, -v73\n\t"
"v_accvgpr_write_b32 a118, v98\n\t"
"v_accvgpr_write_b32 a119, v99\n\t"
"v_fma_f32 v98, v172, s64, -v74\n\t"
"v_fma_f32 v99, v173, s64, -v75\n\t"
"v_mfma_f32_16x16x32_bf16 a[116:119], a[232:235], a[172:175], a[116:119]\n\t"
"ds_read_b128 a[172:175], v223 offset:8192\n\t"
"v_accvgpr_write_b32 a124, v98\n\t"
"v_mfma_f32_16x16x32_bf16 a[116:119], a[236:239], a[176:179], a[116:119]\n\t"
"v_fma_f32 v98, v174, s64, -v76\n\t"
"v_accvgpr_write_b32 a125, v99\n\t"
"v_mfma_f32_16x16x32_bf16 a[116:119], a[240:243], a[180:183], a[116:119]\n\t"
"v_fma_f32 v99, v175, s64, -v77\n\t"
"v_accvgpr_write_b32 a126, v98\n\t"
"v_accvgpr_write_b32 a127, v99\n\t"
"v_fma_f32 v98, v176, s64, -v78\n\t"
"v_fma_f32 v99, v177, s64, -v79\n\t"
"v_cvt_pk_bf16_f32 v232, v232, v233\n\t"
"v_cvt_pk_bf16_f32 v233, v234, v235\n\t"
"v_cvt_pk_bf16_f32 v234, v236, v237\n\t"
"v_cvt_pk_bf16_f32 v235, v238, v239\n\t"
"v_mfma_f32_16x16x32_bf16 a[124:127], a[240:243], a[184:187], a[124:127]\n\t"
"v_accvgpr_write_b32 a132, v98\n\t"
"v_fma_f32 v98, v178, s64, -v80\n\t"
"v_mfma_f32_16x16x32_bf16 a[124:127], a[236:239], a[180:183], a[124:127]\n\t"
"v_accvgpr_write_b32 a133, v99\n\t"
"v_fma_f32 v99, v179, s64, -v81\n\t"
"v_mfma_f32_16x16x32_bf16 a[124:127], a[232:235], a[176:179], a[124:127]\n\t"
"ds_write_b128 v245, v[232:235] offset:7168\n\t"
"v_accvgpr_write_b32 a134, v98\n\t"
"v_accvgpr_write_b32 a135, v99\n\t"
"v_fma_f32 v98, v180, s64, -v82\n\t"
"v_fma_f32 v99, v181, s64, -v83\n\t"
"v_mfma_f32_16x16x32_bf16 a[132:135], a[232:235], a[180:183], a[132:135]\n\t"
"ds_read_b128 a[176:179], v223 offset:14336\n\t"
"v_mfma_f32_16x16x32_bf16 a[132:135], a[236:239], a[184:187], a[132:135]\n\t"
"ds_read_b128 a[180:183], v222 offset:22016\n\t"
"v_accvgpr_write_b32 a140, v98\n\t"
"v_mfma_f32_16x16x32_bf16 a[132:135], a[240:243], a[188:191], a[132:135]\n\t"
"ds_read_b64_tr_b16 v[228:229], v246 offset:13312\n\t"
"v_fma_f32 v98, v182, s64, -v84\n\t"
"v_accvgpr_write_b32 a141, v99\n\t"
"v_fma_f32 v99, v183, s64, -v85\n\t"
"v_accvgpr_write_b32 a142, v98\n\t"
"v_accvgpr_write_b32 a143, v99\n\t"
"v_fma_f32 v98, v184, s64, -v86\n\t"
"v_fma_f32 v99, v185, s64, -v87\n\t"
"v_mfma_f32_16x16x32_bf16 a[140:143], a[240:243], a[192:195], a[140:143]\n\t"
"ds_read_b64_tr_b16 v[224:225], v255 offset:13312\n\t"
"v_accvgpr_write_b32 a148, v98\n\t"
"v_mfma_f32_16x16x32_bf16 a[140:143], a[236:239], a[188:191], a[140:143]\n\t"
"v_fma_f32 v98, v186, s64, -v88\n\t"
"v_accvgpr_write_b32 a149, v99\n\t"
"v_mfma_f32_16x16x32_bf16 a[140:143], a[232:235], a[184:187], a[140:143]\n\t"
"ds_read_b128 a[184:187], v223 offset:28160\n\t"
"v_fma_f32 v99, v187, s64, -v89\n\t"
"v_accvgpr_write_b32 a150, v98\n\t"
"v_accvgpr_write_b32 a151, v99\n\t"
"v_fma_f32 v98, v188, s64, -v90\n\t"
"v_fma_f32 v99, v189, s64, -v91\n\t"
"v_mfma_f32_16x16x32_bf16 a[148:151], a[232:235], a[188:191], a[148:151]\n\t"
"ds_read_b128 a[188:191], v223 offset:34304\n\t"
"v_accvgpr_write_b32 a156, v98\n\t"
"v_mfma_f32_16x16x32_bf16 a[148:151], a[236:239], a[192:195], a[148:151]\n\t"
"v_fma_f32 v98, v190, s64, -v92\n\t"
"v_accvgpr_write_b32 a157, v99\n\t"
"v_mfma_f32_16x16x32_bf16 a[148:151], a[240:243], a[196:199], a[148:151]\n\t"
"v_fma_f32 v99, v191, s64, -v93\n\t"
"v_accvgpr_write_b32 a158, v98\n\t"
"v_accvgpr_write_b32 a159, v99\n\t"
"v_fma_f32 v98, v192, s64, -v94\n\t"
"v_fma_f32 v99, v193, s64, -v95\n\t"
"v_mfma_f32_16x16x32_bf16 a[156:159], a[240:243], a[200:203], a[156:159]\n\t"
"v_accvgpr_write_b32 a164, v98\n\t"
"v_fma_f32 v98, v194, s64, -v96\n\t"
"v_mfma_f32_16x16x32_bf16 a[156:159], a[236:239], a[196:199], a[156:159]\n\t"
"v_accvgpr_write_b32 a165, v99\n\t"
"v_fma_f32 v99, v195, s64, -v97\n\t"
"v_mfma_f32_16x16x32_bf16 a[156:159], a[232:235], a[192:195], a[156:159]\n\t"
"s_add_u32 m0, s25, -128\n\t"
"ds_read_b128 a[192:195], v222 offset:41984\n\t"
"v_accvgpr_write_b32 a166, v98\n\t"
"v_accvgpr_write_b32 a167, v99\n\t"
"s_nop 1\n\t"
"v_mfma_f32_16x16x32_bf16 a[164:167], a[232:235], a[196:199], a[164:167]\n\t"
"buffer_load_dwordx4 a[232:235], off, s[80:83], s20 offset:2048 sc1\n\t"
"v_mfma_f32_16x16x32_bf16 a[164:167], a[236:239], a[200:203], a[164:167]\n\t"
"s_waitcnt lgkmcnt(3)\n\t"
"ds_read_b128 a[196:199], v223 offset:48128\n\t"
"v_fma_f32 v100, -v100, s66, v2\n\t"
"v_mfma_f32_16x16x32_bf16 a[164:167], a[240:243], a[204:207], a[164:167]\n\t"
"ds_read_b128 a[200:203], v223 offset:54272\n\t"
"v_fma_f32 v2, v4, s52, v102\n\t"
"v_fma_f32 v101, -v101, s66, v3\n\t"
"v_fma_f32 v3, v5, s52, v103\n\t"
"v_fma_f32 v102, -v102, s66, v4\n\t"
"v_fma_f32 v103, -v103, s66, v5\n\t"
"v_mfma_f32_4x4x4_16b_bf16 v[228:231], v[228:229], v[220:221], 0\n\t"
"s_waitcnt vmcnt(12)\n\t"
"v_mfma_f32_4x4x4_16b_bf16 v[224:227], v[224:225], v[220:221], 0\n\t"
"buffer_load_dwordx4 v254, s[12:15], 0 offen offset:128 nt lds\n\t"
"v_fma_f32 v4, v6, s52, v104\n\t"
"v_fma_f32 v5, v7, s52, v105\n\t"
"s_add_u32 m0, s26, -128\n\t"
"Lst21_%=:\n\t"
"v_mfma_f32_16x16x32_bf16 v[0:3], a[244:247], a[168:171], v[0:3]\n\t"
"ds_read_b128 a[204:207], v222 offset:61952\n\t"
"v_fma_f32 v104, -v104, s66, v6\n\t"
"v_mfma_f32_16x16x32_bf16 v[0:3], a[252:255], a[176:179], v[0:3]\n\t"
"v_fma_f32 v6, v8, s52, v106\n\t"
"v_fma_f32 v105, -v105, s66, v7\n\t"
"v_mfma_f32_16x16x32_bf16 v[0:3], a[248:251], a[172:175], v[0:3]\n\t"
"s_mov_b64 exec, s[36:37]\n\t"
"buffer_load_dwordx4 v254, s[12:15], s27 offen offset:128 nt lds\n\t"
"s_mov_b64 exec, -1\n\t"
"v_fma_f32 v7, v9, s52, v107\n\t"
"v_fma_f32 v106, -v106, s66, v8\n\t"
"v_fma_f32 v107, -v107, s66, v9\n\t"
"v_fma_f32 v8, v10, s52, v108\n\t"
"v_fma_f32 v9, v11, s52, v109\n\t"
"s_add_u32 m0, s24, -128\n\t"
"s_add_u32 s28, s21, 0\n\t"
"v_mfma_f32_16x16x32_bf16 v[4:7], a[244:247], a[172:175], v[4:7]\n\t"
"buffer_load_dwordx4 v247, s[12:15], s28 offen offset:128 nt lds\n\t"
"v_mfma_f32_16x16x32_bf16 v[4:7], a[248:251], a[176:179], v[4:7]\n\t"
"s_add_u32 m0, s24, 6016\n\t"
"s_add_u32 s28, s21, 196608\n\t"
"v_fma_f32 v108, -v108, s66, v10\n\t"
"v_mfma_f32_16x16x32_bf16 v[4:7], a[252:255], a[180:183], v[4:7]\n\t"
"buffer_load_dwordx4 v247, s[12:15], s28 offen offset:128 nt lds\n\t"
"v_fma_f32 v10, v12, s52, v110\n\t"
"v_fma_f32 v109, -v109, s66, v11\n\t"
"v_fma_f32 v11, v13, s52, v111\n\t"
"v_fma_f32 v110, -v110, s66, v12\n\t"
"v_fma_f32 v111, -v111, s66, v13\n\t"
"v_cvt_pk_bf16_f32 v228, v228, v229\n\t"
"v_cvt_pk_bf16_f32 v229, v230, v231\n\t"
"v_fma_f32 v12, v14, s52, v112\n\t"
"v_fma_f32 v13, v15, s52, v113\n\t"
"v_cvt_pk_bf16_f32 v230, v224, v225\n\t"
"v_cvt_pk_bf16_f32 v231, v226, v227\n\t"
"s_add_u32 m0, s24, 12160\n\t"
"s_add_u32 s28, s21, 393216\n\t"
"s_waitcnt lgkmcnt(5)\n\t"
"v_mfma_f32_16x16x32_bf16 v[8:11], a[252:255], a[184:187], v[8:11]\n\t"
"buffer_load_dwordx4 v247, s[12:15], s28 offen offset:128 nt lds\n\t"
"v_mfma_f32_16x16x32_bf16 v[8:11], a[248:251], a[180:183], v[8:11]\n\t"
"s_add_u32 m0, s24, 19840\n\t"
"s_add_u32 s28, s21, 589824\n\t"
"v_fma_f32 v112, -v112, s66, v14\n\t"
"v_mfma_f32_16x16x32_bf16 v[8:11], a[244:247], a[176:179], v[8:11]\n\t"
"ds_write_b128 v245, v[228:231] offset:13312\n\t"
"v_fma_f32 v14, v16, s52, v114\n\t"
"v_fma_f32 v113, -v113, s66, v15\n\t"
"v_fma_f32 v15, v17, s52, v115\n\t"
"v_fma_f32 v114, -v114, s66, v16\n\t"
"v_fma_f32 v115, -v115, s66, v17\n\t"
"v_fma_f32 v16, v18, s52, v116\n\t"
"v_fma_f32 v17, v19, s52, v117\n\t"
"s_waitcnt lgkmcnt(5)\n\t"
"v_mfma_f32_16x16x32_bf16 v[12:15], a[244:247], a[180:183], v[12:15]\n\t"
"buffer_load_dwordx4 v247, s[12:15], s28 offen offset:128 nt lds\n\t"
"v_mfma_f32_16x16x32_bf16 v[12:15], a[248:251], a[184:187], v[12:15]\n\t"
"s_add_u32 m0, s24, 25984\n\t"
"s_add_u32 s28, s21, 786432\n\t"
"ds_read_b64_tr_b16 v[236:237], v246 offset:20992\n\t"
"v_mfma_f32_16x16x32_bf16 v[12:15], a[252:255], a[188:191], v[12:15]\n\t"
"buffer_load_dwordx4 v247, s[12:15], s28 offen offset:128 nt lds\n\t"
"v_fma_f32 v116, -v116, s66, v18\n\t"
"v_fma_f32 v18, v20, s52, v118\n\t"
"v_fma_f32 v117, -v117, s66, v19\n\t"
"v_fma_f32 v19, v21, s52, v119\n\t"
"v_fma_f32 v118, -v118, s66, v20\n\t"
"v_fma_f32 v119, -v119, s66, v21\n\t"
"v_fma_f32 v20, v22, s52, v120\n\t"
"v_fma_f32 v21, v23, s52, v121\n\t"
"s_add_u32 m0, s24, 32128\n\t"
"s_add_u32 s28, s21, 983040\n\t"
"s_waitcnt lgkmcnt(5)\n\t"
"v_mfma_f32_16x16x32_bf16 v[16:19], a[252:255], a[192:195], v[16:19]\n\t"
"buffer_load_dwordx4 v247, s[12:15], s28 offen offset:128 nt lds\n\t"
"v_mfma_f32_16x16x32_bf16 v[16:19], a[248:251], a[188:191], v[16:19]\n\t"
"ds_read_b64_tr_b16 v[232:233], v255 offset:20992\n\t"
"v_fma_f32 v120, -v120, s66, v22\n\t"
"v_mfma_f32_16x16x32_bf16 v[16:19], a[244:247], a[184:187], v[16:19]\n\t"
"v_fma_f32 v22, v24, s52, v122\n\t"
"v_fma_f32 v121, -v121, s66, v23\n\t"
"v_fma_f32 v23, v25, s52, v123\n\t"
"v_fma_f32 v122, -v122, s66, v24\n\t"
"v_fma_f32 v123, -v123, s66, v25\n\t"
"v_fma_f32 v24, v26, s52, v124\n\t"
"v_fma_f32 v25, v27, s52, v125\n\t"
"s_waitcnt lgkmcnt(5)\n\t"
"v_mfma_f32_16x16x32_bf16 v[20:23], a[244:247], a[188:191], v[20:23]\n\t"
"v_fma_f32 v124, -v124, s66, v26\n\t"
"v_fma_f32 v26, v28, s52, v126\n\t"
"v_mfma_f32_16x16x32_bf16 v[20:23], a[248:251], a[192:195], v[20:23]\n\t"
"v_fma_f32 v125, -v125, s66, v27\n\t"
"v_fma_f32 v27, v29, s52, v127\n\t"
"v_mfma_f32_16x16x32_bf16 v[20:23], a[252:255], a[196:199], v[20:23]\n\t"
"v_fma_f32 v126, -v126, s66, v28\n\t"
"v_fma_f32 v127, -v127, s66, v29\n\t"
"v_fma_f32 v28, v30, s52, v128\n\t"
"v_fma_f32 v29, v31, s52, v129\n\t"
"s_waitcnt lgkmcnt(4)\n\t"
"v_mfma_f32_16x16x32_bf16 v[24:27], a[252:255], a[200:203], v[24:27]\n\t"
"v_fma_f32 v128, -v128, s66, v30\n\t"
"v_fma_f32 v30, v32, s52, v130\n\t"
"v_mfma_f32_16x16x32_bf16 v[24:27], a[248:251], a[196:199], v[24:27]\n\t"
"v_fma_f32 v129, -v129, s66, v31\n\t"
"v_fma_f32 v31, v33, s52, v131\n\t"
"v_mfma_f32_16x16x32_bf16 v[24:27], a[244:247], a[192:195], v[24:27]\n\t"
"v_fma_f32 v130, -v130, s66, v32\n\t"
"v_fma_f32 v131, -v131, s66, v33\n\t"
"s_waitcnt lgkmcnt(3)\n\t"
"v_mfma_f32_16x16x32_bf16 v[28:31], a[244:247], a[196:199], v[28:31]\n\t"
"s_waitcnt lgkmcnt(1)\n\t"
"v_fma_f32 v32, v34, s52, v132\n\t"
"v_fma_f32 v33, v35, s52, v133\n\t"
"v_mfma_f32_16x16x32_bf16 v[28:31], a[248:251], a[200:203], v[28:31]\n\t"
"s_waitcnt lgkmcnt(0)\n\t"
"v_fma_f32 v132, -v132, s66, v34\n\t"
"v_fma_f32 v34, v36, s52, v134\n\t"
"v_mfma_f32_16x16x32_bf16 v[28:31], a[252:255], a[204:207], v[28:31]\n\t"
"v_fma_f32 v133, -v133, s66, v35\n\t"
"v_fma_f32 v35, v37, s52, v135\n\t"
"v_fma_f32 v134, -v134, s66, v36\n\t"
"v_fma_f32 v135, -v135, s66, v37\n\t"
"v_mfma_f32_4x4x4_16b_bf16 v[236:239], v[236:237], v[220:221], 0\n\t"
"s_waitcnt vmcnt(14)\n\t"
"v_mfma_f32_4x4x4_16b_bf16 v[232:235], v[232:233], v[220:221], 0\n\t"
"v_fma_f32 v36, v38, s52, v136\n\t"
"v_fma_f32 v37, v39, s52, v137\n\t"
"v_mfma_f32_16x16x32_bf16 v[32:35], v[240:243], a[168:171], v[32:35]\n\t"
"v_fma_f32 v136, -v136, s66, v38\n\t"
"v_fma_f32 v38, v40, s52, v138\n\t"
"v_mfma_f32_16x16x32_bf16 v[32:35], a[208:211], a[176:179], v[32:35]\n\t"
"v_fma_f32 v137, -v137, s66, v39\n\t"
"v_fma_f32 v39, v41, s52, v139\n\t"
"v_mfma_f32_16x16x32_bf16 v[32:35], v[248:251], a[172:175], v[32:35]\n\t"
"v_fma_f32 v138, -v138, s66, v40\n\t"
"v_fma_f32 v139, -v139, s66, v41\n\t"
"v_fma_f32 v40, v42, s52, v140\n\t"
"v_fma_f32 v41, v43, s52, v141\n\t"
"v_mfma_f32_16x16x32_bf16 v[36:39], v[240:243], a[172:175], v[36:39]\n\t"
"v_fma_f32 v140, -v140, s66, v42\n\t"
"v_fma_f32 v42, v44, s52, v142\n\t"
"v_mfma_f32_16x16x32_bf16 v[36:39], v[248:251], a[176:179], v[36:39]\n\t"
"v_fma_f32 v141, -v141, s66, v43\n\t"
"v_fma_f32 v43, v45, s52, v143\n\t"
"v_mfma_f32_16x16x32_bf16 v[36:39], a[208:211], a[180:183], v[36:39]\n\t"
"v_fma_f32 v142, -v142, s66, v44\n\t"
"v_fma_f32 v143, -v143, s66, v45\n\t"
"v_cvt_pk_bf16_f32 v236, v236, v237\n\t"
"v_cvt_pk_bf16_f32 v237, v238, v239\n\t"
"v_fma_f32 v44, v46, s52, v144\n\t"
"v_fma_f32 v45, v47, s52, v145\n\t"
"v_cvt_pk_bf16_f32 v238, v232, v233\n\t"
"v_cvt_pk_bf16_f32 v239, v234, v235\n\t"
"v_mfma_f32_16x16x32_bf16 v[40:43], a[208:211], a[184:187], v[40:43]\n\t"
"s_mov_b32 s20, 86016\n\t"
"v_fma_f32 v144, -v144, s66, v46\n\t"
"v_fma_f32 v46, v48, s52, v146\n\t"
"v_mfma_f32_16x16x32_bf16 v[40:43], v[248:251], a[180:183], v[40:43]\n\t"
"v_fma_f32 v145, -v145, s66, v47\n\t"
"v_fma_f32 v47, v49, s52, v147\n\t"
"v_mfma_f32_16x16x32_bf16 v[40:43], v[240:243], a[176:179], v[40:43]\n\t"
"ds_write_b128 v245, v[236:239] offset:20992\n\t"
"v_fma_f32 v146, -v146, s66, v48\n\t"
"v_fma_f32 v147, -v147, s66, v49\n\t"
"v_fma_f32 v48, v50, s52, v148\n\t"
"v_fma_f32 v49, v51, s52, v149\n\t"
"v_mfma_f32_16x16x32_bf16 v[44:47], v[240:243], a[180:183], v[44:47]\n\t"
"ds_read_b64_tr_b16 v[224:225], v246 offset:27136\n\t"
"v_mfma_f32_16x16x32_bf16 v[44:47], v[248:251], a[184:187], v[44:47]\n\t"
"ds_read_b64_tr_b16 v[228:229], v255 offset:27136\n\t"
"v_fma_f32 v148, -v148, s66, v50\n\t"
"v_mfma_f32_16x16x32_bf16 v[44:47], a[208:211], a[188:191], v[44:47]\n\t"
"v_fma_f32 v50, v52, s52, v150\n\t"
"v_fma_f32 v149, -v149, s66, v51\n\t"
"v_fma_f32 v51, v53, s52, v151\n\t"
"v_fma_f32 v150, -v150, s66, v52\n\t"
"v_fma_f32 v151, -v151, s66, v53\n\t"
"v_fma_f32 v52, v54, s52, v152\n\t"
"v_fma_f32 v53, v55, s52, v153\n\t"
"v_mfma_f32_16x16x32_bf16 v[48:51], a[208:211], a[192:195], v[48:51]\n\t"
"v_fma_f32 v152, -v152, s66, v54\n\t"
"v_fma_f32 v54, v56, s52, v154\n\t"
"v_mfma_f32_16x16x32_bf16 v[48:51], v[248:251], a[188:191], v[48:51]\n\t"
"v_fma_f32 v153, -v153, s66, v55\n\t"
"v_fma_f32 v55, v57, s52, v155\n\t"
"v_mfma_f32_16x16x32_bf16 v[48:51], v[240:243], a[184:187], v[48:51]\n\t"
"v_fma_f32 v154, -v154, s66, v56\n\t"
"v_fma_f32 v155, -v155, s66, v57\n\t"
"v_fma_f32 v56, v58, s52, v156\n\t"
"v_fma_f32 v57, v59, s52, v157\n\t"
"v_mfma_f32_16x16x32_bf16 v[52:55], v[240:243], a[188:191], v[52:55]\n\t"
"s_waitcnt lgkmcnt(0)\n\t"
"v_fma_f32 v156, -v156, s66, v58\n\t"
"v_fma_f32 v58, v60, s52, v158\n\t"
"v_mfma_f32_16x16x32_bf16 v[52:55], v[248:251], a[192:195], v[52:55]\n\t"
"v_fma_f32 v157, -v157, s66, v59\n\t"
"v_fma_f32 v59, v61, s52, v159\n\t"
"v_mfma_f32_16x16x32_bf16 v[52:55], a[208:211], a[196:199], v[52:55]\n\t"
"Lpg16_%=:\n\t"
"s_cbranch_vccnz Lvr1_%=\n\t"
"buffer_load_dwordx4 a[236:239], off, s[80:83], s20 offset:0 sc1\n\t"
"v_fma_f32 v158, -v158, s66, v60\n\t"
"v_fma_f32 v159, -v159, s66, v61\n\t"
"v_fma_f32 v60, v62, s52, v160\n\t"
"v_fma_f32 v61, v63, s52, v161\n\t"
"v_mfma_f32_16x16x32_bf16 v[56:59], a[208:211], a[200:203], v[56:59]\n\t"
"v_fma_f32 v160, -v160, s66, v62\n\t"
"v_fma_f32 v62, v64, s52, v162\n\t"
"v_mfma_f32_16x16x32_bf16 v[56:59], v[248:251], a[196:199], v[56:59]\n\t"
"v_fma_f32 v161, -v161, s66, v63\n\t"
"v_fma_f32 v63, v65, s52, v163\n\t"
"v_mfma_f32_16x16x32_bf16 v[56:59], v[240:243], a[192:195], v[56:59]\n\t"
"v_fma_f32 v162, -v162, s66, v64\n\t"
"v_fma_f32 v163, -v163, s66, v65\n\t"
"v_mfma_f32_16x16x32_bf16 v[60:63], v[240:243], a[196:199], v[60:63]\n\t"
"v_fma_f32 v64, v66, s52, v164\n\t"
"v_fma_f32 v65, v67, s52, v165\n\t"
"v_mfma_f32_16x16x32_bf16 v[60:63], v[248:251], a[200:203], v[60:63]\n\t"
"v_fma_f32 v164, -v164, s66, v66\n\t"
"v_fma_f32 v66, v68, s52, v166\n\t"
"v_mfma_f32_16x16x32_bf16 v[60:63], a[208:211], a[204:207], v[60:63]\n\t"
"v_fma_f32 v165, -v165, s66, v67\n\t"
"v_fma_f32 v67, v69, s52, v167\n\t"
"v_fma_f32 v166, -v166, s66, v68\n\t"
"v_fma_f32 v167, -v167, s66, v69\n\t"
"v_mfma_f32_4x4x4_16b_bf16 v[224:227], v[224:225], v[220:221], 0\n\t"
"s_waitcnt vmcnt(12)\n\t"
"v_mfma_f32_4x4x4_16b_bf16 v[228:231], v[228:229], v[220:221], 0\n\t"
"v_fma_f32 v68, v70, s52, v168\n\t"
"v_fma_f32 v69, v71, s52, v169\n\t"
"v_mfma_f32_16x16x32_bf16 v[64:67], a[212:215], a[168:171], v[64:67]\n\t"
"ds_read_b128 a[168:171], v222 offset:2304\n\t"
"v_fma_f32 v168, -v168, s66, v70\n\t"
"v_mfma_f32_16x16x32_bf16 v[64:67], a[220:223], a[176:179], v[64:67]\n\t"
"v_fma_f32 v70, v72, s52, v170\n\t"
"v_fma_f32 v169, -v169, s66, v71\n\t"
"v_mfma_f32_16x16x32_bf16 v[64:67], a[216:219], a[172:175], v[64:67]\n\t"
"v_fma_f32 v71, v73, s52, v171\n\t"
"v_fma_f32 v170, -v170, s66, v72\n\t"
"v_fma_f32 v171, -v171, s66, v73\n\t"
"v_fma_f32 v72, v74, s52, v172\n\t"
"v_fma_f32 v73, v75, s52, v173\n\t"
"v_mfma_f32_16x16x32_bf16 v[68:71], a[212:215], a[172:175], v[68:71]\n\t"
"ds_read_b128 a[172:175], v223 offset:8448\n\t"
"v_fma_f32 v172, -v172, s66, v74\n\t"
"v_mfma_f32_16x16x32_bf16 v[68:71], a[216:219], a[176:179], v[68:71]\n\t"
"v_fma_f32 v74, v76, s52, v174\n\t"
"v_fma_f32 v173, -v173, s66, v75\n\t"
"v_mfma_f32_16x16x32_bf16 v[68:71], a[220:223], a[180:183], v[68:71]\n\t"
"v_fma_f32 v75, v77, s52, v175\n\t"
"v_fma_f32 v174, -v174, s66, v76\n\t"
"v_fma_f32 v175, -v175, s66, v77\n\t"
"v_cvt_pk_bf16_f32 v224, v224, v225\n\t"
"v_cvt_pk_bf16_f32 v225, v226, v227\n\t"
"v_fma_f32 v76, v78, s52, v176\n\t"
"v_fma_f32 v77, v79, s52, v177\n\t"
"v_cvt_pk_bf16_f32 v226, v228, v229\n\t"
"v_cvt_pk_bf16_f32 v227, v230, v231\n\t"
"v_mfma_f32_16x16x32_bf16 v[72:75], a[220:223], a[184:187], v[72:75]\n\t"
"v_fma_f32 v176, -v176, s66, v78\n\t"
"v_fma_f32 v78, v80, s52, v178\n\t"
"v_mfma_f32_16x16x32_bf16 v[72:75], a[216:219], a[180:183], v[72:75]\n\t"
"v_fma_f32 v177, -v177, s66, v79\n\t"
"v_fma_f32 v79, v81, s52, v179\n\t"
"v_mfma_f32_16x16x32_bf16 v[72:75], a[212:215], a[176:179], v[72:75]\n\t"
"ds_write_b128 v245, v[224:227] offset:27136\n\t"
"v_fma_f32 v178, -v178, s66, v80\n\t"
"v_fma_f32 v179, -v179, s66, v81\n\t"
"v_fma_f32 v80, v82, s52, v180\n\t"
"v_fma_f32 v81, v83, s52, v181\n\t"
"v_mfma_f32_16x16x32_bf16 v[76:79], a[212:215], a[180:183], v[76:79]\n\t"
"ds_read_b128 a[176:179], v223 offset:14592\n\t"
"v_mfma_f32_16x16x32_bf16 v[76:79], a[216:219], a[184:187], v[76:79]\n\t"
"ds_read_b64_tr_b16 v[232:233], v246 offset:33280\n\t"
"v_fma_f32 v180, -v180, s66, v82\n\t"
"v_mfma_f32_16x16x32_bf16 v[76:79], a[220:223], a[188:191], v[76:79]\n\t"
"ds_read_b64_tr_b16 v[236:237], v255 offset:33280\n\t"
"v_fma_f32 v82, v84, s52, v182\n\t"
"v_fma_f32 v181, -v181, s66, v83\n\t"
"v_fma_f32 v83, v85, s52, v183\n\t"
"v_fma_f32 v182, -v182, s66, v84\n\t"
"v_fma_f32 v183, -v183, s66, v85\n\t"
"v_fma_f32 v84, v86, s52, v184\n\t"
"v_fma_f32 v85, v87, s52, v185\n\t"
"v_mfma_f32_16x16x32_bf16 v[80:83], a[220:223], a[192:195], v[80:83]\n\t"
"ds_read_b128 a[180:183], v222 offset:22272\n\t"
"v_fma_f32 v184, -v184, s66, v86\n\t"
"v_mfma_f32_16x16x32_bf16 v[80:83], a[216:219], a[188:191], v[80:83]\n\t"
"ds_read_b64_tr_b16 v[228:229], v246 offset:40960\n\t"
"v_fma_f32 v86, v88, s52, v186\n\t"
"v_mfma_f32_16x16x32_bf16 v[80:83], a[212:215], a[184:187], v[80:83]\n\t"
"ds_read_b64_tr_b16 v[224:225], v255 offset:40960\n\t"
"v_fma_f32 v185, -v185, s66, v87\n\t"
"v_fma_f32 v87, v89, s52, v187\n\t"
"v_fma_f32 v186, -v186, s66, v88\n\t"
"v_fma_f32 v187, -v187, s66, v89\n\t"
"v_fma_f32 v88, v90, s52, v188\n\t"
"v_fma_f32 v89, v91, s52, v189\n\t"
"v_mfma_f32_16x16x32_bf16 v[84:87], a[212:215], a[188:191], v[84:87]\n\t"
"ds_read_b128 a[184:187], v223 offset:28416\n\t"
"v_fma_f32 v188, -v188, s66, v90\n\t"
"v_mfma_f32_16x16x32_bf16 v[84:87], a[216:219], a[192:195], v[84:87]\n\t"
"ds_read_b128 a[188:191], v223 offset:34560\n\t"
"v_fma_f32 v90, v92, s52, v190\n\t"
"v_mfma_f32_16x16x32_bf16 v[84:87], a[220:223], a[196:199], v[84:87]\n\t"
"buffer_load_dwordx4 v[240:243], off, s[80:83], s20 offset:1024 sc1\n\t"
"v_fma_f32 v189, -v189, s66, v91\n\t"
"v_fma_f32 v91, v93, s52, v191\n\t"
"v_fma_f32 v190, -v190, s66, v92\n\t"
"v_fma_f32 v191, -v191, s66, v93\n\t"
"v_fma_f32 v92, v94, s52, v192\n\t"
"v_fma_f32 v93, v95, s52, v193\n\t"
"v_mfma_f32_16x16x32_bf16 v[88:91], a[220:223], a[200:203], v[88:91]\n\t"
"v_fma_f32 v192, -v192, s66, v94\n\t"
"v_fma_f32 v94, v96, s52, v194\n\t"
"v_mfma_f32_16x16x32_bf16 v[88:91], a[216:219], a[196:199], v[88:91]\n\t"
"v_fma_f32 v193, -v193, s66, v95\n\t"
"v_fma_f32 v95, v97, s52, v195\n\t"
"v_mfma_f32_16x16x32_bf16 v[88:91], a[212:215], a[192:195], v[88:91]\n\t"
"buffer_load_dwordx4 a[208:211], off, s[80:83], s20 offset:2048 sc1\n\t"
"v_fma_f32 v194, -v194, s66, v96\n\t"
"v_fma_f32 v195, -v195, s66, v97\n\t"
"s_mov_b32 s20, 98304\n\t"
"v_mfma_f32_16x16x32_bf16 v[92:95], a[212:215], a[196:199], v[92:95]\n\t"
"buffer_load_dwordx4 v[248:251], off, s[80:83], s20 offset:0 sc1\n\t"
"v_mfma_f32_16x16x32_bf16 v[92:95], a[216:219], a[200:203], v[92:95]\n\t"
"buffer_load_dwordx4 v[96:99], off, s[80:83], s20 offset:1024 sc1\n\t"
"v_mfma_f32_16x16x32_bf16 v[92:95], a[220:223], a[204:207], v[92:95]\n\t"
"s_waitcnt lgkmcnt(3)\n\t"
"ds_read_b128 a[192:195], v222 offset:42240\n\t"
"v_mfma_f32_4x4x4_16b_bf16 v[232:235], v[232:233], v[220:221], 0\n\t"
"s_waitcnt lgkmcnt(3)\n\t"
"v_mfma_f32_4x4x4_16b_bf16 v[236:239], v[236:237], v[220:221], 0\n\t"
"s_waitcnt vmcnt(13)\n\t"
"v_mfma_f32_4x4x4_16b_bf16 v[228:231], v[228:229], v[220:221], 0\n\t"
"v_mfma_f32_4x4x4_16b_bf16 v[224:227], v[224:225], v[220:221], 0\n\t"
"buffer_load_dwordx4 a[212:215], off, s[80:83], s20 offset:2048 sc1\n\t"
"s_mov_b32 s20, 331776\n\t"
"v_mfma_f32_16x16x32_bf16 v[100:103], a[224:227], a[168:171], v[100:103]\n\t"
"ds_read_b128 a[196:199], v223 offset:48384\n\t"
"v_cvt_pk_bf16_f32 v232, v232, v233\n\t"
"v_mfma_f32_16x16x32_bf16 v[100:103], a[232:235], a[176:179], v[100:103]\n\t"
"ds_read_b128 a[200:203], v223 offset:54528\n\t"
"v_cvt_pk_bf16_f32 v233, v234, v235\n\t"
"v_mfma_f32_16x16x32_bf16 v[100:103], a[228:231], a[172:175], v[100:103]\n\t"
"ds_read_b128 a[204:207], v222 offset:62208\n\t"
"v_cvt_pk_bf16_f32 v234, v236, v237\n\t"
"v_mfma_f32_16x16x32_bf16 v[104:107], a[224:227], a[172:175], v[104:107]\n\t"
"s_waitcnt lgkmcnt(5)\n\t"
"v_cvt_pk_bf16_f32 v235, v238, v239\n\t"
"v_cvt_pk_bf16_f32 v228, v228, v229\n\t"
"v_mfma_f32_16x16x32_bf16 v[104:107], a[228:231], a[176:179], v[104:107]\n\t"
"v_cvt_pk_bf16_f32 v229, v230, v231\n\t"
"v_mfma_f32_16x16x32_bf16 v[104:107], a[232:235], a[180:183], v[104:107]\n\t"
"s_waitcnt lgkmcnt(0)\n\t"
"s_barrier\n\t"
"v_mfma_f32_16x16x32_bf16 v[108:111], a[232:235], a[184:187], v[108:111]\n\t"
"v_cvt_pk_bf16_f32 v230, v224, v225\n\t"
"v_mfma_f32_16x16x32_bf16 v[108:111], a[228:231], a[180:183], v[108:111]\n\t"
"buffer_load_dwordx4 a[216:219], off, s[80:83], s20 offset:0 sc1\n\t"
"v_mfma_f32_16x16x32_bf16 v[108:111], a[224:227], a[176:179], v[108:111]\n\t"
"buffer_load_dwordx4 a[220:223], off, s[80:83], s20 offset:1024 sc1\n\t"
"v_cvt_pk_bf16_f32 v231, v226, v227\n\t"
"v_mfma_f32_16x16x32_bf16 v[112:115], a[224:227], a[180:183], v[112:115]\n\t"
"ds_read_b64_tr_b16 v[236:237], v246 offset:47104\n\t"
"v_mfma_f32_16x16x32_bf16 v[112:115], a[228:231], a[184:187], v[112:115]\n\t"
"buffer_load_dwordx4 a[240:243], off, s[80:83], s20 offset:2048 sc1\n\t"
"v_mfma_f32_16x16x32_bf16 v[112:115], a[232:235], a[188:191], v[112:115]\n\t"
"s_mov_b32 s20, 344064\n\t"
"ds_write_b128 v245, v[232:235] offset:33280\n\t"
"v_mfma_f32_16x16x32_bf16 v[116:119], a[232:235], a[192:195], v[116:119]\n\t"
"buffer_load_dwordx4 a[244:247], off, s[80:83], s20 offset:0 sc1\n\t"
"v_mfma_f32_16x16x32_bf16 v[116:119], a[228:231], a[188:191], v[116:119]\n\t"
"buffer_load_dwordx4 a[248:251], off, s[80:83], s20 offset:1024 sc1\n\t"
"v_mfma_f32_16x16x32_bf16 v[116:119], a[224:227], a[184:187], v[116:119]\n\t"
"ds_write_b128 v245, v[228:231] offset:40960\n\t"
"v_mfma_f32_16x16x32_bf16 v[120:123], a[224:227], a[188:191], v[120:123]\n\t"
"v_mfma_f32_16x16x32_bf16 v[120:123], a[228:231], a[192:195], v[120:123]\n\t"
"ds_read_b64_tr_b16 v[224:225], v255 offset:47104\n\t"
"v_mfma_f32_16x16x32_bf16 v[120:123], a[232:235], a[196:199], v[120:123]\n\t"
"ds_read_b64_tr_b16 v[232:233], v246 offset:53248\n\t"
"v_mfma_f32_16x16x32_bf16 v[124:127], a[232:235], a[200:203], v[124:127]\n\t"
"ds_read_b64_tr_b16 v[228:229], v255 offset:53248\n\t"
"v_mfma_f32_16x16x32_bf16 v[124:127], a[228:231], a[196:199], v[124:127]\n\t"
"buffer_load_dwordx4 a[252:255], off, s[80:83], s20 offset:2048 sc1\n\t"
"v_mfma_f32_16x16x32_bf16 v[124:127], a[224:227], a[192:195], v[124:127]\n\t"
"s_mov_b32 s20, 356352\n\t"
"v_mfma_f32_16x16x32_bf16 v[128:131], a[224:227], a[196:199], v[128:131]\n\t"
"buffer_load_dwordx4 a[224:227], off, s[80:83], s20 offset:0 sc1\n\t"
"v_mfma_f32_16x16x32_bf16 v[128:131], a[228:231], a[200:203], v[128:131]\n\t"
"s_waitcnt lgkmcnt(2)\n\t"
"v_mfma_f32_16x16x32_bf16 v[128:131], a[232:235], a[204:207], v[128:131]\n\t"
"s_waitcnt lgkmcnt(1)\n\t"
"v_mfma_f32_4x4x4_16b_bf16 v[236:239], v[236:237], v[220:221], 0\n\t"
"s_waitcnt lgkmcnt(0)\n\t"
"v_mfma_f32_4x4x4_16b_bf16 v[224:227], v[224:225], v[220:221], 0\n\t"
"s_waitcnt vmcnt(10)\n\t"
"v_mfma_f32_4x4x4_16b_bf16 v[232:235], v[232:233], v[220:221], 0\n\t"
"v_mfma_f32_4x4x4_16b_bf16 v[228:231], v[228:229], v[220:221], 0\n\t"
"buffer_load_dwordx4 a[228:231], off, s[80:83], s20 offset:1024 sc1\n\t"
"v_mfma_f32_16x16x32_bf16 v[132:135], a[236:239], a[168:171], v[132:135]\n\t"
"buffer_load_dwordx4 a[232:235], off, s[80:83], s20 offset:2048 sc1\n\t"
"v_mfma_f32_16x16x32_bf16 v[132:135], a[208:211], a[176:179], v[132:135]\n\t"
"v_cvt_pk_bf16_f32 v236, v236, v237\n\t"
"v_cvt_pk_bf16_f32 v237, v238, v239\n\t"
"v_mfma_f32_16x16x32_bf16 v[132:135], v[240:243], a[172:175], v[132:135]\n\t"
"s_mov_b32 s20, 368640\n\t"
"v_cvt_pk_bf16_f32 v238, v224, v225\n\t"
"v_cvt_pk_bf16_f32 v239, v226, v227\n\t"
"v_mfma_f32_16x16x32_bf16 v[136:139], a[236:239], a[172:175], v[136:139]\n\t"
"v_cvt_pk_bf16_f32 v232, v232, v233\n\t"
"v_cvt_pk_bf16_f32 v233, v234, v235\n\t"
"v_mfma_f32_16x16x32_bf16 v[136:139], v[240:243], a[176:179], v[136:139]\n\t"
"v_cvt_pk_bf16_f32 v234, v228, v229\n\t"
"v_cvt_pk_bf16_f32 v235, v230, v231\n\t"
"v_mfma_f32_16x16x32_bf16 v[136:139], a[208:211], a[180:183], v[136:139]\n\t"
"ds_write_b128 v245, v[236:239] offset:47104\n\t"
"v_mfma_f32_16x16x32_bf16 v[140:143], a[208:211], a[184:187], v[140:143]\n\t"
"v_mfma_f32_16x16x32_bf16 v[140:143], v[240:243], a[180:183], v[140:143]\n\t"
"v_mfma_f32_16x16x32_bf16 v[140:143], a[236:239], a[176:179], v[140:143]\n\t"
"ds_write_b128 v245, v[232:235] offset:53248\n\t"
"v_mfma_f32_16x16x32_bf16 v[144:147], a[236:239], a[180:183], v[144:147]\n\t"
"buffer_load_dwordx4 v[236:239], off, s[80:83], s20 offset:0 sc1\n\t"
"v_mfma_f32_16x16x32_bf16 v[144:147], v[240:243], a[184:187], v[144:147]\n\t"
"v_mfma_f32_16x16x32_bf16 v[144:147], a[208:211], a[188:191], v[144:147]\n\t"
"v_mfma_f32_16x16x32_bf16 v[148:151], a[208:211], a[192:195], v[148:151]\n\t"
"v_mfma_f32_16x16x32_bf16 v[148:151], v[240:243], a[188:191], v[148:151]\n\t"
"v_mfma_f32_16x16x32_bf16 v[148:151], a[236:239], a[184:187], v[148:151]\n\t"
"s_add_u32 m0, s24, 1408\n\t"
"s_add_u32 s28, s21, 3072\n\t"
"v_mfma_f32_16x16x32_bf16 v[152:155], a[236:239], a[188:191], v[152:155]\n\t"
"v_mfma_f32_16x16x32_bf16 v[152:155], v[240:243], a[192:195], v[152:155]\n\t"
"v_mfma_f32_16x16x32_bf16 v[152:155], a[208:211], a[196:199], v[152:155]\n\t"
"v_mfma_f32_16x16x32_bf16 v[156:159], a[208:211], a[200:203], v[156:159]\n\t"
"buffer_load_dwordx4 v247, s[12:15], s28 offen offset:128 nt lds\n\t"
"v_mfma_f32_16x16x32_bf16 v[156:159], v[240:243], a[196:199], v[156:159]\n\t"
"s_add_u32 m0, s24, 7552\n\t"
"s_add_u32 s28, s21, 199680\n\t"
"v_mfma_f32_16x16x32_bf16 v[156:159], a[236:239], a[192:195], v[156:159]\n\t"
"buffer_load_dwordx4 v247, s[12:15], s28 offen offset:128 nt lds\n\t"
"v_mfma_f32_16x16x32_bf16 v[160:163], a[236:239], a[196:199], v[160:163]\n\t"
"s_waitcnt vmcnt(12)\n\t"
"s_add_u32 m0, s24, 13696\n\t"
"s_add_u32 s28, s21, 396288\n\t"
"v_mfma_f32_16x16x32_bf16 v[160:163], v[240:243], a[200:203], v[160:163]\n\t"
"buffer_load_dwordx4 v247, s[12:15], s28 offen offset:128 nt lds\n\t"
"v_mfma_f32_16x16x32_bf16 v[160:163], a[208:211], a[204:207], v[160:163]\n\t"
"s_add_u32 m0, s24, 21376\n\t"
"s_add_u32 s28, s21, 592896\n\t"
"v_mfma_f32_16x16x32_bf16 v[164:167], v[248:251], a[168:171], v[164:167]\n\t"
"buffer_load_dwordx4 v247, s[12:15], s28 offen offset:128 nt lds\n\t"
"v_mfma_f32_16x16x32_bf16 v[164:167], a[212:215], a[176:179], v[164:167]\n\t"
"s_add_u32 m0, s24, 27520\n\t"
"s_add_u32 s28, s21, 789504\n\t"
"v_mfma_f32_16x16x32_bf16 v[164:167], v[96:99], a[172:175], v[164:167]\n\t"
"s_waitcnt lgkmcnt(0)\n\t"
"s_barrier\n\t"
"v_mfma_f32_16x16x32_bf16 v[168:171], v[248:251], a[172:175], v[168:171]\n\t"
"ds_read_b128 a[168:171], v222 offset:2560\n\t"
"v_mfma_f32_16x16x32_bf16 v[168:171], v[96:99], a[176:179], v[168:171]\n\t"
"buffer_load_dwordx4 v247, s[12:15], s28 offen offset:128 nt lds\n\t"
"v_mfma_f32_16x16x32_bf16 v[168:171], a[212:215], a[180:183], v[168:171]\n\t"
"s_add_u32 m0, s24, 33664\n\t"
"s_add_u32 s28, s21, 986112\n\t"
"ds_read_b128 a[172:175], v223 offset:8704\n\t"
"v_mfma_f32_16x16x32_bf16 v[172:175], a[212:215], a[184:187], v[172:175]\n\t"
"buffer_load_dwordx4 v247, s[12:15], s28 offen offset:128 nt lds\n\t"
"v_mfma_f32_16x16x32_bf16 v[172:175], v[96:99], a[180:183], v[172:175]\n\t"
"v_mfma_f32_16x16x32_bf16 v[172:175], v[248:251], a[176:179], v[172:175]\n\t"
"ds_read_b128 a[176:179], v223 offset:14848\n\t"
"v_mfma_f32_16x16x32_bf16 v[176:179], v[248:251], a[180:183], v[176:179]\n\t"
"ds_read_b128 a[180:183], v222 offset:22528\n\t"
"v_mfma_f32_16x16x32_bf16 v[176:179], v[96:99], a[184:187], v[176:179]\n\t"
"v_mfma_f32_16x16x32_bf16 v[176:179], a[212:215], a[188:191], v[176:179]\n\t"
"v_mfma_f32_16x16x32_bf16 v[180:183], a[212:215], a[192:195], v[180:183]\n\t"
"v_mfma_f32_16x16x32_bf16 v[180:183], v[96:99], a[188:191], v[180:183]\n\t"
"v_mfma_f32_16x16x32_bf16 v[180:183], v[248:251], a[184:187], v[180:183]\n\t"
"ds_read_b128 a[184:187], v223 offset:28672\n\t"
"v_mfma_f32_16x16x32_bf16 v[184:187], v[248:251], a[188:191], v[184:187]\n\t"
"ds_read_b128 a[188:191], v223 offset:34816\n\t"
"v_mfma_f32_16x16x32_bf16 v[184:187], v[96:99], a[192:195], v[184:187]\n\t"
"v_mfma_f32_16x16x32_bf16 v[184:187], a[212:215], a[196:199], v[184:187]\n\t"
"v_mfma_f32_16x16x32_bf16 v[188:191], a[212:215], a[200:203], v[188:191]\n\t"
"v_mfma_f32_16x16x32_bf16 v[188:191], v[96:99], a[196:199], v[188:191]\n\t"
"v_mfma_f32_16x16x32_bf16 v[188:191], v[248:251], a[192:195], v[188:191]\n\t"
"ds_read_b128 a[192:195], v222 offset:42496\n\t"
"v_mfma_f32_16x16x32_bf16 v[192:195], v[248:251], a[196:199], v[192:195]\n\t"
"s_waitcnt vmcnt(13) lgkmcnt(4)\n\t"
"ds_read_b128 a[196:199], v223 offset:48640\n\t"
"v_mfma_f32_16x16x32_bf16 v[192:195], v[96:99], a[200:203], v[192:195]\n\t"
"ds_read_b128 a[200:203], v223 offset:54784\n\t"
"v_mfma_f32_16x16x32_bf16 v[192:195], a[212:215], a[204:207], v[192:195]\n\t"
"ds_read_b128 a[204:207], v222 offset:62464\n\t"
"Lst22_%=:\n\t"
"v_mfma_f32_16x16x32_bf16 v[0:3], a[216:219], a[168:171], v[0:3]\n\t"
"s_waitcnt lgkmcnt(6)\n\t"
"v_mfma_f32_16x16x32_bf16 v[0:3], a[240:243], a[176:179], v[0:3]\n\t"
"v_mfma_f32_16x16x32_bf16 v[0:3], a[220:223], a[172:175], v[0:3]\n\t"
"v_mfma_f32_16x16x32_bf16 v[4:7], a[216:219], a[172:175], v[4:7]\n\t"
"s_waitcnt lgkmcnt(5)\n\t"
"v_mfma_f32_16x16x32_bf16 v[4:7], a[220:223], a[176:179], v[4:7]\n\t"
"v_mfma_f32_16x16x32_bf16 v[4:7], a[240:243], a[180:183], v[4:7]\n\t"
"v_mfma_f32_16x16x32_bf16 v[8:11], a[240:243], a[184:187], v[8:11]\n\t"
"s_waitcnt lgkmcnt(4)\n\t"
"v_mfma_f32_16x16x32_bf16 v[8:11], a[220:223], a[180:183], v[8:11]\n\t"
"v_mfma_f32_16x16x32_bf16 v[8:11], a[216:219], a[176:179], v[8:11]\n\t"
"v_mfma_f32_16x16x32_bf16 v[12:15], a[216:219], a[180:183], v[12:15]\n\t"
"s_waitcnt lgkmcnt(3)\n\t"
"v_mfma_f32_16x16x32_bf16 v[12:15], a[220:223], a[184:187], v[12:15]\n\t"
"v_mfma_f32_16x16x32_bf16 v[12:15], a[240:243], a[188:191], v[12:15]\n\t"
"v_mfma_f32_16x16x32_bf16 v[16:19], a[240:243], a[192:195], v[16:19]\n\t"
"s_waitcnt lgkmcnt(2)\n\t"
"v_mfma_f32_16x16x32_bf16 v[16:19], a[220:223], a[188:191], v[16:19]\n\t"
"v_mfma_f32_16x16x32_bf16 v[16:19], a[216:219], a[184:187], v[16:19]\n\t"
"buffer_load_dwordx4 v[96:99], off, s[80:83], s20 offset:1024 sc1\n\t"
"v_mfma_f32_16x16x32_bf16 v[20:23], a[216:219], a[188:191], v[20:23]\n\t"
"s_waitcnt lgkmcnt(1)\n\t"
"v_mfma_f32_16x16x32_bf16 v[20:23], a[220:223], a[192:195], v[20:23]\n\t"
"v_mfma_f32_16x16x32_bf16 v[20:23], a[240:243], a[196:199], v[20:23]\n\t"
"v_mfma_f32_16x16x32_bf16 v[24:27], a[240:243], a[200:203], v[24:27]\n\t"
"s_waitcnt lgkmcnt(0)\n\t"
"v_mfma_f32_16x16x32_bf16 v[24:27], a[220:223], a[196:199], v[24:27]\n\t"
"v_mfma_f32_16x16x32_bf16 v[24:27], a[216:219], a[192:195], v[24:27]\n\t"
"v_mfma_f32_16x16x32_bf16 v[28:31], a[216:219], a[196:199], v[28:31]\n\t"
"s_waitcnt vmcnt(11)\n\t"
"v_mfma_f32_16x16x32_bf16 v[28:31], a[220:223], a[200:203], v[28:31]\n\t"
"v_mfma_f32_16x16x32_bf16 v[28:31], a[240:243], a[204:207], v[28:31]\n\t"
"v_mfma_f32_16x16x32_bf16 v[32:35], a[244:247], a[168:171], v[32:35]\n\t"
"v_mfma_f32_16x16x32_bf16 v[32:35], a[252:255], a[176:179], v[32:35]\n\t"
"v_mfma_f32_16x16x32_bf16 v[32:35], a[248:251], a[172:175], v[32:35]\n\t"
"v_mfma_f32_16x16x32_bf16 v[36:39], a[244:247], a[172:175], v[36:39]\n\t"
"v_mfma_f32_16x16x32_bf16 v[36:39], a[248:251], a[176:179], v[36:39]\n\t"
"v_mfma_f32_16x16x32_bf16 v[36:39], a[252:255], a[180:183], v[36:39]\n\t"
"v_mfma_f32_16x16x32_bf16 v[40:43], a[252:255], a[184:187], v[40:43]\n\t"
"v_mfma_f32_16x16x32_bf16 v[40:43], a[248:251], a[180:183], v[40:43]\n\t"
"v_mfma_f32_16x16x32_bf16 v[40:43], a[244:247], a[176:179], v[40:43]\n\t"
"buffer_load_dwordx4 v[248:251], off, s[80:83], s20 offset:2048 sc1\n\t"
"v_mfma_f32_16x16x32_bf16 v[44:47], a[244:247], a[180:183], v[44:47]\n\t"
"s_mov_b32 s20, 380928\n\t"
"v_mfma_f32_16x16x32_bf16 v[44:47], a[248:251], a[184:187], v[44:47]\n\t"
"v_mfma_f32_16x16x32_bf16 v[44:47], a[252:255], a[188:191], v[44:47]\n\t"
"v_mfma_f32_16x16x32_bf16 v[48:51], a[252:255], a[192:195], v[48:51]\n\t"
"buffer_load_dwordx4 a[212:215], off, s[80:83], s20 offset:0 sc1\n\t"
"v_mfma_f32_16x16x32_bf16 v[48:51], a[248:251], a[188:191], v[48:51]\n\t"
"v_mfma_f32_16x16x32_bf16 v[48:51], a[244:247], a[184:187], v[48:51]\n\t"
"v_mfma_f32_16x16x32_bf16 v[52:55], a[244:247], a[188:191], v[52:55]\n\t"
"v_mfma_f32_16x16x32_bf16 v[52:55], a[248:251], a[192:195], v[52:55]\n\t"
"buffer_load_dwordx4 a[216:219], off, s[80:83], s20 offset:1024 sc1\n\t"
"v_mfma_f32_16x16x32_bf16 v[52:55], a[252:255], a[196:199], v[52:55]\n\t"
"v_mfma_f32_16x16x32_bf16 v[56:59], a[252:255], a[200:203], v[56:59]\n\t"
"v_mfma_f32_16x16x32_bf16 v[56:59], a[248:251], a[196:199], v[56:59]\n\t"
"v_mfma_f32_16x16x32_bf16 v[56:59], a[244:247], a[192:195], v[56:59]\n\t"
"buffer_load_dwordx4 a[220:223], off, s[80:83], s20 offset:2048 sc1\n\t"
"v_mfma_f32_16x16x32_bf16 v[60:63], a[244:247], a[196:199], v[60:63]\n\t"
"s_waitcnt vmcnt(12)\n\t"
"s_mov_b32 s20, 393216\n\t"
"v_mfma_f32_16x16x32_bf16 v[60:63], a[248:251], a[200:203], v[60:63]\n\t"
"v_mfma_f32_16x16x32_bf16 v[60:63], a[252:255], a[204:207], v[60:63]\n\t"
"v_mfma_f32_16x16x32_bf16 v[64:67], a[224:227], a[168:171], v[64:67]\n\t"
"buffer_load_dwordx4 a[236:239], off, s[80:83], s20 offset:0 sc1\n\t"
"v_mfma_f32_16x16x32_bf16 v[64:67], a[232:235], a[176:179], v[64:67]\n\t"
"ds_read_b128 a[168:171], v222 offset:2816\n\t"
"v_mfma_f32_16x16x32_bf16 v[64:67], a[228:231], a[172:175], v[64:67]\n\t"
"v_mfma_f32_16x16x32_bf16 v[68:71], a[224:227], a[172:175], v[68:71]\n\t"
"ds_read_b128 a[172:175], v223 offset:8960\n\t"
"v_mfma_f32_16x16x32_bf16 v[68:71], a[228:231], a[176:179], v[68:71]\n\t"
"buffer_load_dwordx4 a[240:243], off, s[80:83], s20 offset:1024 sc1\n\t"
"v_mfma_f32_16x16x32_bf16 v[68:71], a[232:235], a[180:183], v[68:71]\n\t"
"v_mfma_f32_16x16x32_bf16 v[72:75], a[232:235], a[184:187], v[72:75]\n\t"
"v_mfma_f32_16x16x32_bf16 v[72:75], a[228:231], a[180:183], v[72:75]\n\t"
"v_mfma_f32_16x16x32_bf16 v[72:75], a[224:227], a[176:179], v[72:75]\n\t"
"ds_read_b128 a[176:179], v223 offset:15104\n\t"
"v_mfma_f32_16x16x32_bf16 v[76:79], a[224:227], a[180:183], v[76:79]\n\t"
"buffer_load_dwordx4 a[244:247], off, s[80:83], s20 offset:2048 sc1\n\t"
"v_mfma_f32_16x16x32_bf16 v[76:79], a[228:231], a[184:187], v[76:79]\n\t"
"s_mov_b32 s20, 405504\n\t"
"ds_read_b128 a[180:183], v222 offset:22784\n\t"
"v_mfma_f32_16x16x32_bf16 v[76:79], a[232:235], a[188:191], v[76:79]\n\t"
"v_mfma_f32_16x16x32_bf16 v[80:83], a[232:235], a[192:195], v[80:83]\n\t"
"buffer_load_dwordx4 a[248:251], off, s[80:83], s20 offset:0 sc1\n\t"
"v_mfma_f32_16x16x32_bf16 v[80:83], a[228:231], a[188:191], v[80:83]\n\t"
"v_mfma_f32_16x16x32_bf16 v[80:83], a[224:227], a[184:187], v[80:83]\n\t"
"ds_read_b128 a[184:187], v223 offset:28928\n\t"
"v_mfma_f32_16x16x32_bf16 v[84:87], a[224:227], a[188:191], v[84:87]\n\t"
"ds_read_b128 a[188:191], v223 offset:35072\n\t"
"v_mfma_f32_16x16x32_bf16 v[84:87], a[228:231], a[192:195], v[84:87]\n\t"
"buffer_load_dwordx4 a[252:255], off, s[80:83], s20 offset:1024 sc1\n\t"
"v_mfma_f32_16x16x32_bf16 v[84:87], a[232:235], a[196:199], v[84:87]\n\t"
"v_mfma_f32_16x16x32_bf16 v[88:91], a[232:235], a[200:203], v[88:91]\n\t"
"v_mfma_f32_16x16x32_bf16 v[88:91], a[228:231], a[196:199], v[88:91]\n\t"
"v_mfma_f32_16x16x32_bf16 v[88:91], a[224:227], a[192:195], v[88:91]\n\t"
"ds_read_b128 a[192:195], v222 offset:42752\n\t"
"v_mfma_f32_16x16x32_bf16 v[92:95], a[224:227], a[196:199], v[92:95]\n\t"
"buffer_load_dwordx4 a[224:227], off, s[80:83], s20 offset:2048 sc1\n\t"
"v_mfma_f32_16x16x32_bf16 v[92:95], a[228:231], a[200:203], v[92:95]\n\t"
"s_waitcnt vmcnt(9) lgkmcnt(4)\n\t"
"ds_read_b128 a[196:199], v223 offset:48896\n\t"
"s_mov_b32 s20, 417792\n\t"
"v_mfma_f32_16x16x32_bf16 v[92:95], a[232:235], a[204:207], v[92:95]\n\t"
"global_load_dwordx4 v[240:243], v252, s[48:49] offset:2048\n\t"
"v_mfma_f32_16x16x32_bf16 v[100:103], v[236:239], a[168:171], v[100:103]\n\t"
"buffer_load_dwordx4 a[228:231], off, s[80:83], s20 offset:0 sc1\n\t"
"v_mfma_f32_16x16x32_bf16 v[100:103], v[248:251], a[176:179], v[100:103]\n\t"
"s_waitcnt lgkmcnt(4)\n\t"
"ds_read_b128 a[200:203], v223 offset:55040\n\t"
"v_mfma_f32_16x16x32_bf16 v[100:103], v[96:99], a[172:175], v[100:103]\n\t"
"ds_read_b128 a[204:207], v222 offset:62720\n\t"
"v_mfma_f32_16x16x32_bf16 v[104:107], v[236:239], a[172:175], v[104:107]\n\t"
"buffer_load_dwordx4 a[232:235], off, s[80:83], s20 offset:1024 sc1\n\t"
"v_mfma_f32_16x16x32_bf16 v[104:107], v[96:99], a[176:179], v[104:107]\n\t"
"global_load_dwordx4 a[208:211], v252, s[46:47] offset:1024\n\t"
"v_mfma_f32_16x16x32_bf16 v[104:107], v[248:251], a[180:183], v[104:107]\n\t"
"s_waitcnt lgkmcnt(5)\n\t"
"v_mfma_f32_16x16x32_bf16 v[108:111], v[248:251], a[184:187], v[108:111]\n\t"
"s_waitcnt lgkmcnt(4)\n\t"
"v_mfma_f32_16x16x32_bf16 v[108:111], v[96:99], a[180:183], v[108:111]\n\t"
"v_mfma_f32_16x16x32_bf16 v[108:111], v[236:239], a[176:179], v[108:111]\n\t"
"v_mfma_f32_16x16x32_bf16 v[112:115], v[236:239], a[180:183], v[112:115]\n\t"
"s_waitcnt lgkmcnt(3)\n\t"
"v_mfma_f32_16x16x32_bf16 v[112:115], v[96:99], a[184:187], v[112:115]\n\t"
"v_mfma_f32_16x16x32_bf16 v[112:115], v[248:251], a[188:191], v[112:115]\n\t"
"v_mfma_f32_16x16x32_bf16 v[116:119], v[248:251], a[192:195], v[116:119]\n\t"
"s_waitcnt lgkmcnt(2)\n\t"
"v_mfma_f32_16x16x32_bf16 v[116:119], v[96:99], a[188:191], v[116:119]\n\t"
"v_mfma_f32_16x16x32_bf16 v[116:119], v[236:239], a[184:187], v[116:119]\n\t"
"v_mfma_f32_16x16x32_bf16 v[120:123], v[236:239], a[188:191], v[120:123]\n\t"
"s_waitcnt lgkmcnt(1)\n\t"
"v_mfma_f32_16x16x32_bf16 v[120:123], v[96:99], a[192:195], v[120:123]\n\t"
"v_mfma_f32_16x16x32_bf16 v[120:123], v[248:251], a[196:199], v[120:123]\n\t"
"v_mfma_f32_16x16x32_bf16 v[124:127], v[248:251], a[200:203], v[124:127]\n\t"
"s_waitcnt lgkmcnt(0)\n\t"
"v_mfma_f32_16x16x32_bf16 v[124:127], v[96:99], a[196:199], v[124:127]\n\t"
"v_mfma_f32_16x16x32_bf16 v[124:127], v[236:239], a[192:195], v[124:127]\n\t"
"v_mfma_f32_16x16x32_bf16 v[128:131], v[236:239], a[196:199], v[128:131]\n\t"
"s_waitcnt vmcnt(10)\n\t"
"v_mfma_f32_16x16x32_bf16 v[128:131], v[96:99], a[200:203], v[128:131]\n\t"
"buffer_load_dwordx4 v[96:99], off, s[80:83], s20 offset:2048 sc1\n\t"
"v_mfma_f32_16x16x32_bf16 v[128:131], v[248:251], a[204:207], v[128:131]\n\t"
"s_mov_b32 s20, 430080\n\t"
"v_mfma_f32_16x16x32_bf16 v[132:135], a[212:215], a[168:171], v[132:135]\n\t"
"v_mfma_f32_16x16x32_bf16 v[132:135], a[220:223], a[176:179], v[132:135]\n\t"
"buffer_load_dwordx4 v[248:251], off, s[80:83], s20 offset:0 sc1\n\t"
"v_mfma_f32_16x16x32_bf16 v[132:135], a[216:219], a[172:175], v[132:135]\n\t"
"v_mfma_f32_16x16x32_bf16 v[136:139], a[212:215], a[172:175], v[136:139]\n\t"
"v_mfma_f32_16x16x32_bf16 v[136:139], a[216:219], a[176:179], v[136:139]\n\t"
"v_mfma_f32_16x16x32_bf16 v[136:139], a[220:223], a[180:183], v[136:139]\n\t"
"s_barrier\n\t"
"v_mfma_f32_16x16x32_bf16 v[140:143], a[220:223], a[184:187], v[140:143]\n\t"
"s_add_u32 m0, s24, 39808\n\t"
"s_add_u32 s28, s21, 1179648\n\t"
"v_mfma_f32_16x16x32_bf16 v[140:143], a[216:219], a[180:183], v[140:143]\n\t"
"v_mfma_f32_16x16x32_bf16 v[140:143], a[212:215], a[176:179], v[140:143]\n\t"
"s_waitcnt vmcnt(5)\n\t"
"ds_read_b64_tr_b16 v[224:225], v240 offset:0\n\t"
"v_mfma_f32_16x16x32_bf16 v[144:147], a[212:215], a[180:183], v[144:147]\n\t"
"buffer_load_dwordx4 v247, s[12:15], s28 offen offset:128 nt lds\n\t"
"v_mfma_f32_16x16x32_bf16 v[144:147], a[216:219], a[184:187], v[144:147]\n\t"
"s_add_u32 m0, s24, 45952\n\t"
"s_add_u32 s28, s21, 1376256\n\t"
"ds_read_b64_tr_b16 v[226:227], v240 offset:1536\n\t"
"v_mfma_f32_16x16x32_bf16 v[144:147], a[220:223], a[188:191], v[144:147]\n\t"
"buffer_load_dwordx4 v247, s[12:15], s28 offen offset:128 nt lds\n\t"
"v_mfma_f32_16x16x32_bf16 v[148:151], a[220:223], a[192:195], v[148:151]\n\t"
"s_add_u32 m0, s24, 52096\n\t"
"s_add_u32 s28, s21, 1572864\n\t"
"ds_read_b64_tr_b16 v[228:229], v241 offset:0\n\t"
"v_mfma_f32_16x16x32_bf16 v[148:151], a[216:219], a[188:191], v[148:151]\n\t"
"buffer_load_dwordx4 v247, s[12:15], s28 offen offset:128 nt lds\n\t"
"v_mfma_f32_16x16x32_bf16 v[148:151], a[212:215], a[184:187], v[148:151]\n\t"
"s_add_u32 m0, s24, 41344\n\t"
"s_add_u32 s28, s21, 1182720\n\t"
"ds_read_b64_tr_b16 v[230:231], v241 offset:1536\n\t"
"v_mfma_f32_16x16x32_bf16 v[152:155], a[212:215], a[188:191], v[152:155]\n\t"
"buffer_load_dwordx4 v247, s[12:15], s28 offen offset:128 nt lds\n\t"
"v_mfma_f32_16x16x32_bf16 v[152:155], a[216:219], a[192:195], v[152:155]\n\t"
"s_add_u32 m0, s24, 47488\n\t"
"s_add_u32 s28, s21, 1379328\n\t"
"ds_read_b64_tr_b16 v[236:237], v240 offset:19968\n\t"
"v_mfma_f32_16x16x32_bf16 v[152:155], a[220:223], a[196:199], v[152:155]\n\t"
"ds_read_b64_tr_b16 v[238:239], v240 offset:21504\n\t"
"v_mfma_f32_16x16x32_bf16 v[156:159], a[220:223], a[200:203], v[156:159]\n\t"
"buffer_load_dwordx4 v247, s[12:15], s28 offen offset:128 nt lds\n\t"
"v_mfma_f32_16x16x32_bf16 v[156:159], a[216:219], a[196:199], v[156:159]\n\t"
"s_add_u32 m0, s24, 53632\n\t"
"s_add_u32 s28, s21, 1575936\n\t"
"ds_read_b64_tr_b16 v[232:233], v241 offset:19968\n\t"
"v_mfma_f32_16x16x32_bf16 v[156:159], a[212:215], a[192:195], v[156:159]\n\t"
"ds_read_b64_tr_b16 v[234:235], v241 offset:21504\n\t"
"v_mfma_f32_16x16x32_bf16 v[160:163], a[212:215], a[196:199], v[160:163]\n\t"
"buffer_load_dwordx4 v247, s[12:15], s28 offen offset:128 nt lds\n\t"
"v_mfma_f32_16x16x32_bf16 v[160:163], a[216:219], a[200:203], v[160:163]\n\t"
"s_waitcnt vmcnt(8) lgkmcnt(4)\n\t"
"v_mfma_f32_16x16x32_bf16 v[160:163], a[220:223], a[204:207], v[160:163]\n\t"
"s_waitcnt lgkmcnt(2)\n\t"
"v_mfma_f32_16x16x32_bf16 v[224:227], v[224:227], a[208:211], 0\n\t"
"v_mfma_f32_16x16x32_bf16 v[228:231], v[228:231], a[208:211], 0\n\t"
"s_waitcnt lgkmcnt(0)\n\t"
"v_mfma_f32_16x16x32_bf16 v[236:239], v[236:239], a[208:211], 0\n\t"
"v_mfma_f32_16x16x32_bf16 v[232:235], v[232:235], a[208:211], 0\n\t"
"v_mfma_f32_16x16x32_bf16 v[164:167], a[236:239], a[168:171], v[164:167]\n\t"
"ds_read_b128 a[168:171], v222 offset:1024\n\t"
"v_mfma_f32_16x16x32_bf16 v[164:167], a[244:247], a[176:179], v[164:167]\n\t"
"v_cvt_pk_bf16_f32 v224, v224, v225\n\t"
"v_cvt_pk_bf16_f32 v225, v226, v227\n\t"
"v_mfma_f32_16x16x32_bf16 v[164:167], a[240:243], a[172:175], v[164:167]\n\t"
"v_cvt_pk_bf16_f32 v226, v228, v229\n\t"
"v_cvt_pk_bf16_f32 v227, v230, v231\n\t"
"v_mfma_f32_16x16x32_bf16 v[168:171], a[236:239], a[172:175], v[168:171]\n\t"
"ds_read_b128 a[172:175], v223 offset:7168\n\t"
"v_cvt_pk_bf16_f32 v236, v236, v237\n\t"
"v_mfma_f32_16x16x32_bf16 v[168:171], a[240:243], a[176:179], v[168:171]\n\t"
"v_cvt_pk_bf16_f32 v237, v238, v239\n\t"
"v_cvt_pk_bf16_f32 v238, v232, v233\n\t"
"v_mfma_f32_16x16x32_bf16 v[168:171], a[244:247], a[180:183], v[168:171]\n\t"
"ds_write_b128 v242, v[224:227] offset:24576\n\t"
"v_mfma_f32_16x16x32_bf16 v[172:175], a[244:247], a[184:187], v[172:175]\n\t"
"v_cvt_pk_bf16_f32 v239, v234, v235\n\t"
"v_add_u32 v240, 59904, v240\n\t"
"v_mfma_f32_16x16x32_bf16 v[172:175], a[240:243], a[180:183], v[172:175]\n\t"
"v_add_u32 v241, 59904, v241\n\t"
"v_mfma_f32_16x16x32_bf16 v[172:175], a[236:239], a[176:179], v[172:175]\n\t"
"ds_write_b128 v242, v[236:239] offset:26112\n\t"
"v_mfma_f32_16x16x32_bf16 v[176:179], a[236:239], a[180:183], v[176:179]\n\t"
"v_mfma_f32_16x16x32_bf16 v[176:179], a[240:243], a[184:187], v[176:179]\n\t"
"ds_read_b128 a[176:179], v223 offset:13312\n\t"
"v_mfma_f32_16x16x32_bf16 v[176:179], a[244:247], a[188:191], v[176:179]\n\t"
"ds_read_b64_tr_b16 v[228:229], v240 offset:0\n\t"
"v_mfma_f32_16x16x32_bf16 v[180:183], a[244:247], a[192:195], v[180:183]\n\t"
"ds_read_b64_tr_b16 v[230:231], v240 offset:1536\n\t"
"v_mfma_f32_16x16x32_bf16 v[180:183], a[240:243], a[188:191], v[180:183]\n\t"
"Lpg17_%=:\n\t"
"s_cbranch_vccnz Lvr2_%=\n\t"
"ds_read_b64_tr_b16 v[232:233], v241 offset:0\n\t"
"v_mfma_f32_16x16x32_bf16 v[180:183], a[236:239], a[184:187], v[180:183]\n\t"
"ds_read_b64_tr_b16 v[234:235], v241 offset:1536\n\t"
"v_mfma_f32_16x16x32_bf16 v[184:187], a[236:239], a[188:191], v[184:187]\n\t"
"ds_read_b128 a[180:183], v222 offset:20992\n\t"
"v_mfma_f32_16x16x32_bf16 v[184:187], a[240:243], a[192:195], v[184:187]\n\t"
"ds_read_b64_tr_b16 v[224:225], v240 offset:19968\n\t"
"v_mfma_f32_16x16x32_bf16 v[184:187], a[244:247], a[196:199], v[184:187]\n\t"
"ds_read_b64_tr_b16 v[226:227], v240 offset:21504\n\t"
"v_mfma_f32_16x16x32_bf16 v[188:191], a[244:247], a[200:203], v[188:191]\n\t"
"ds_read_b64_tr_b16 v[236:237], v241 offset:19968\n\t"
"v_mfma_f32_16x16x32_bf16 v[188:191], a[240:243], a[196:199], v[188:191]\n\t"
"ds_read_b64_tr_b16 v[238:239], v241 offset:21504\n\t"
"v_mfma_f32_16x16x32_bf16 v[188:191], a[236:239], a[192:195], v[188:191]\n\t"
"ds_read_b128 a[184:187], v223 offset:27136\n\t"
"v_mfma_f32_16x16x32_bf16 v[192:195], a[236:239], a[196:199], v[192:195]\n\t"
"buffer_load_dwordx4 a[212:215], off, s[80:83], s20 offset:1024 sc1\n\t"
"v_mfma_f32_16x16x32_bf16 v[192:195], a[240:243], a[200:203], v[192:195]\n\t"
"s_waitcnt lgkmcnt(6)\n\t"
"ds_read_b128 a[188:191], v223 offset:33280\n\t"
"v_mfma_f32_16x16x32_bf16 v[192:195], a[244:247], a[204:207], v[192:195]\n\t"
"s_waitcnt lgkmcnt(4)\n\t"
"ds_read_b128 a[192:195], v222 offset:40960\n\t"
"v_mfma_f32_16x16x32_bf16 v[228:231], v[228:231], a[208:211], 0\n\t"
"s_waitcnt lgkmcnt(3)\n\t"
"ds_read_b128 a[196:199], v223 offset:47104\n\t"
"v_mfma_f32_16x16x32_bf16 v[232:235], v[232:235], a[208:211], 0\n\t"
"ds_read_b128 a[200:203], v223 offset:53248\n\t"
"v_mfma_f32_16x16x32_bf16 v[224:227], v[224:227], a[208:211], 0\n\t"
"ds_read_b128 a[204:207], v222 offset:60928\n\t"
"v_mfma_f32_16x16x32_bf16 v[236:239], v[236:239], a[208:211], 0\n\t"
"Lst23_%=:\n\t"
"v_mfma_f32_16x16x32_bf16 v[196:199], a[248:251], a[168:171], v[196:199]\n\t"
"s_mov_b64 vcc, -1\n\t"
"s_bitcmp1_b32 s73, 1\n\t"
"s_cbranch_scc1 Lvh3_%=\n\t"
"s_bitcmp1_b32 s73, 0\n\t"
"s_cbranch_scc1 Lpg22_%=\n\t"
"s_branch Lpg21_%=\n\t"
"Lvh3_%=:\n\t"
"s_bitcmp1_b32 s73, 0\n\t"
"s_cbranch_scc1 Lpg24_%=\n\t"
"s_branch Lpg23_%=\n\t"
"Lvr3_%=:\n\t"
"s_mov_b64 vcc, 0\n\t"
"v_mfma_f32_16x16x32_bf16 v[196:199], a[224:227], a[176:179], v[196:199]\n\t"
"v_cvt_pk_bf16_f32 v228, v228, v229\n\t"
"v_cvt_pk_bf16_f32 v229, v230, v231\n\t"
"v_mfma_f32_16x16x32_bf16 v[196:199], a[252:255], a[172:175], v[196:199]\n\t"
"v_cvt_pk_bf16_f32 v230, v232, v233\n\t"
"v_cvt_pk_bf16_f32 v231, v234, v235\n\t"
"v_mfma_f32_16x16x32_bf16 v[204:207], a[248:251], a[172:175], v[204:207]\n\t"
"s_waitcnt lgkmcnt(5)\n\t"
"v_cvt_pk_bf16_f32 v224, v224, v225\n\t"
"v_cvt_pk_bf16_f32 v225, v226, v227\n\t"
"v_mfma_f32_16x16x32_bf16 v[204:207], a[252:255], a[176:179], v[204:207]\n\t"
"v_cvt_pk_bf16_f32 v226, v236, v237\n\t"
"v_cvt_pk_bf16_f32 v227, v238, v239\n\t"
"v_mfma_f32_16x16x32_bf16 v[204:207], a[224:227], a[180:183], v[204:207]\n\t"
"ds_write_b128 v242, v[228:231] offset:29184\n\t"
"v_mfma_f32_16x16x32_bf16 v[212:215], a[224:227], a[184:187], v[212:215]\n\t"
"s_waitcnt lgkmcnt(5)\n\t"
"v_mfma_f32_16x16x32_bf16 v[212:215], a[252:255], a[180:183], v[212:215]\n\t"
"v_mfma_f32_16x16x32_bf16 v[212:215], a[248:251], a[176:179], v[212:215]\n\t"
"ds_write_b128 v242, v[224:227] offset:30720\n\t"
"v_mfma_f32_16x16x32_bf16 a[0:3], a[248:251], a[180:183], a[0:3]\n\t"
"s_waitcnt lgkmcnt(5)\n\t"
"v_mfma_f32_16x16x32_bf16 a[0:3], a[252:255], a[184:187], a[0:3]\n\t"
"v_mfma_f32_16x16x32_bf16 a[0:3], a[224:227], a[188:191], a[0:3]\n\t"
"s_barrier\n\t"
"v_mfma_f32_16x16x32_bf16 a[8:11], a[224:227], a[192:195], a[8:11]\n\t"
"s_waitcnt lgkmcnt(4)\n\t"
"ds_read_b64_tr_b16 v[232:233], v246 offset:0\n\t"
"v_mfma_f32_16x16x32_bf16 a[8:11], a[252:255], a[188:191], a[8:11]\n\t"
"ds_read_b64_tr_b16 v[236:237], v255 offset:0\n\t"
"v_mfma_f32_16x16x32_bf16 a[8:11], a[248:251], a[184:187], a[8:11]\n\t"
"ds_read_b64_tr_b16 v[228:229], v246 offset:6144\n\t"
"v_mfma_f32_16x16x32_bf16 a[16:19], a[248:251], a[188:191], a[16:19]\n\t"
"s_waitcnt lgkmcnt(6)\n\t"
"ds_read_b64_tr_b16 v[224:225], v255 offset:6144\n\t"
"v_mfma_f32_16x16x32_bf16 a[16:19], a[252:255], a[192:195], a[16:19]\n\t"
"buffer_load_dwordx4 a[216:219], off, s[80:83], s20 offset:2048 sc1\n\t"
"v_mfma_f32_16x16x32_bf16 a[16:19], a[224:227], a[196:199], a[16:19]\n\t"
"s_mov_b32 s20, 442368\n\t"
"v_mfma_f32_16x16x32_bf16 a[24:27], a[224:227], a[200:203], a[24:27]\n\t"
"s_waitcnt lgkmcnt(6)\n\t"
"v_mfma_f32_16x16x32_bf16 a[24:27], a[252:255], a[196:199], a[24:27]\n\t"
"v_mfma_f32_16x16x32_bf16 a[24:27], a[248:251], a[192:195], a[24:27]\n\t"
"buffer_load_dwordx4 a[220:223], off, s[80:83], s20 offset:0 sc1\n\t"
"v_mfma_f32_16x16x32_bf16 a[32:35], a[248:251], a[196:199], a[32:35]\n\t"
"s_waitcnt lgkmcnt(3)\n\t"
"v_mfma_f32_16x16x32_bf16 a[32:35], a[252:255], a[200:203], a[32:35]\n\t"
"s_waitcnt lgkmcnt(2)\n\t"
"v_mfma_f32_16x16x32_bf16 a[32:35], a[224:227], a[204:207], a[32:35]\n\t"
"s_waitcnt lgkmcnt(1)\n\t"
"v_mfma_f32_4x4x4_16b_bf16 v[232:235], v[232:233], v[220:221], 0\n\t"
"s_waitcnt lgkmcnt(0)\n\t"
"v_mfma_f32_4x4x4_16b_bf16 v[236:239], v[236:237], v[220:221], 0\n\t"
"s_waitcnt vmcnt(10)\n\t"
"v_mfma_f32_4x4x4_16b_bf16 v[228:231], v[228:229], v[220:221], 0\n\t"
"v_mfma_f32_4x4x4_16b_bf16 v[224:227], v[224:225], v[220:221], 0\n\t"
"buffer_load_dwordx4 a[224:227], off, s[80:83], s20 offset:1024 sc1\n\t"
"v_mfma_f32_16x16x32_bf16 a[40:43], a[228:231], a[168:171], a[40:43]\n\t"
"buffer_load_dwordx4 a[236:239], off, s[80:83], s20 offset:2048 sc1\n\t"
"v_mfma_f32_16x16x32_bf16 a[40:43], v[96:99], a[176:179], a[40:43]\n\t"
"s_mov_b32 s20, 454656\n\t"
"v_cvt_pk_bf16_f32 v232, v232, v233\n\t"
"v_cvt_pk_bf16_f32 v233, v234, v235\n\t"
"v_mfma_f32_16x16x32_bf16 a[40:43], a[232:235], a[172:175], a[40:43]\n\t"
"v_cvt_pk_bf16_f32 v234, v236, v237\n\t"
"v_cvt_pk_bf16_f32 v235, v238, v239\n\t"
"v_mfma_f32_16x16x32_bf16 a[48:51], a[228:231], a[172:175], a[48:51]\n\t"
"ds_read_b64_tr_b16 v[236:237], v246 offset:12288\n\t"
"v_cvt_pk_bf16_f32 v228, v228, v229\n\t"
"v_mfma_f32_16x16x32_bf16 a[48:51], a[232:235], a[176:179], a[48:51]\n\t"
"v_cvt_pk_bf16_f32 v229, v230, v231\n\t"
"v_cvt_pk_bf16_f32 v230, v224, v225\n\t"
"v_mfma_f32_16x16x32_bf16 a[48:51], v[96:99], a[180:183], a[48:51]\n\t"
"ds_write_b128 v245, v[232:235] offset:0\n\t"
"v_cvt_pk_bf16_f32 v231, v226, v227\n\t"
"v_mfma_f32_16x16x32_bf16 a[56:59], v[96:99], a[184:187], a[56:59]\n\t"
"v_mfma_f32_16x16x32_bf16 a[56:59], a[232:235], a[180:183], a[56:59]\n\t"
"buffer_load_dwordx4 a[240:243], off, s[80:83], s20 offset:0 sc1\n\t"
"v_mfma_f32_16x16x32_bf16 a[56:59], a[228:231], a[176:179], a[56:59]\n\t"
"ds_read_b64_tr_b16 v[224:225], v255 offset:12288\n\t"
"v_mfma_f32_16x16x32_bf16 a[64:67], a[228:231], a[180:183], a[64:67]\n\t"
"ds_read_b64_tr_b16 v[232:233], v246 offset:19968\n\t"
"v_mfma_f32_16x16x32_bf16 a[64:67], a[232:235], a[184:187], a[64:67]\n\t"
"buffer_load_dwordx4 a[244:247], off, s[80:83], s20 offset:1024 sc1\n\t"
"v_mfma_f32_16x16x32_bf16 a[64:67], v[96:99], a[188:191], a[64:67]\n\t"
"ds_write_b128 v245, v[228:231] offset:6144\n\t"
"v_mfma_f32_16x16x32_bf16 a[72:75], a[228:231], a[184:187], a[72:75]\n\t"
"buffer_load_dwordx4 a[248:251], off, s[80:83], s20 offset:2048 sc1\n\t"
"v_mfma_f32_16x16x32_bf16 a[72:75], v[96:99], a[192:195], a[72:75]\n\t"
"s_waitcnt lgkmcnt(2)\n\t"
"ds_read_b64_tr_b16 v[228:229], v255 offset:19968\n\t"
"v_mfma_f32_16x16x32_bf16 a[72:75], a[232:235], a[188:191], a[72:75]\n\t"
"s_waitcnt lgkmcnt(2)\n\t"
"s_mov_b32 s20, 466944\n\t"
"v_mfma_f32_4x4x4_16b_bf16 v[236:239], v[236:237], v[220:221], 0\n\t"
"v_mfma_f32_4x4x4_16b_bf16 v[224:227], v[224:225], v[220:221], 0\n\t"
"v_mfma_f32_4x4x4_16b_bf16 v[232:235], v[232:233], v[220:221], 0\n\t"
"s_waitcnt lgkmcnt(0)\n\t"
"v_mfma_f32_4x4x4_16b_bf16 v[228:231], v[228:229], v[220:221], 0\n\t"
"v_mfma_f32_16x16x32_bf16 a[80:83], a[228:231], a[188:191], a[80:83]\n\t"
"buffer_load_dwordx4 a[252:255], off, s[80:83], s20 offset:0 sc1\n\t"
"v_mfma_f32_16x16x32_bf16 a[80:83], v[96:99], a[196:199], a[80:83]\n\t"
"v_cvt_pk_bf16_f32 v236, v236, v237\n\t"
"v_cvt_pk_bf16_f32 v237, v238, v239\n\t"
"v_mfma_f32_16x16x32_bf16 a[80:83], a[232:235], a[192:195], a[80:83]\n\t"
"v_cvt_pk_bf16_f32 v238, v224, v225\n\t"
"v_cvt_pk_bf16_f32 v239, v226, v227\n\t"
"v_mfma_f32_16x16x32_bf16 a[88:91], a[228:231], a[192:195], a[88:91]\n\t"
"ds_read_b64_tr_b16 v[224:225], v246 offset:26112\n\t"
"v_cvt_pk_bf16_f32 v232, v232, v233\n\t"
"v_mfma_f32_16x16x32_bf16 a[88:91], a[232:235], a[196:199], a[88:91]\n\t"
"v_cvt_pk_bf16_f32 v233, v234, v235\n\t"
"v_cvt_pk_bf16_f32 v234, v228, v229\n\t"
"v_mfma_f32_16x16x32_bf16 a[88:91], v[96:99], a[200:203], a[88:91]\n\t"
"ds_write_b128 v245, v[236:239] offset:12288\n\t"
"v_cvt_pk_bf16_f32 v235, v230, v231\n\t"
"v_mfma_f32_16x16x32_bf16 a[96:99], a[232:235], a[200:203], a[96:99]\n\t"
"s_waitcnt vmcnt(7)\n\t"
"v_mfma_f32_16x16x32_bf16 a[96:99], a[228:231], a[196:199], a[96:99]\n\t"
"ds_read_b64_tr_b16 v[228:229], v255 offset:26112\n\t"
"v_mfma_f32_16x16x32_bf16 a[96:99], v[96:99], a[204:207], a[96:99]\n\t"
"ds_write_b128 v245, v[232:235] offset:19968\n\t"
"v_mfma_f32_16x16x32_bf16 a[104:107], v[248:251], a[168:171], a[104:107]\n\t"
"buffer_load_dwordx4 a[232:235], off, s[80:83], s20 offset:1024 sc1\n\t"
"v_mfma_f32_16x16x32_bf16 a[104:107], a[216:219], a[176:179], a[104:107]\n\t"
"ds_read_b64_tr_b16 v[236:237], v246 offset:32256\n\t"
"v_mfma_f32_16x16x32_bf16 a[104:107], a[212:215], a[172:175], a[104:107]\n\t"
"ds_read_b64_tr_b16 v[232:233], v255 offset:32256\n\t"
"v_mfma_f32_16x16x32_bf16 a[112:115], v[248:251], a[172:175], a[112:115]\n\t"
"buffer_load_dwordx4 v[96:99], off, s[80:83], s20 offset:2048 sc1\n\t"
"v_mfma_f32_16x16x32_bf16 a[112:115], a[212:215], a[176:179], a[112:115]\n\t"
"s_waitcnt lgkmcnt(3)\n\t"
"ds_read_b128 a[168:171], v222 offset:1280\n\t"
"s_mov_b32 s20, 700416\n\t"
"v_mfma_f32_16x16x32_bf16 a[112:115], a[216:219], a[180:183], a[112:115]\n\t"
"buffer_load_dwordx4 a[228:231], off, s[80:83], s20 offset:0 sc1\n\t"
"v_mfma_f32_4x4x4_16b_bf16 v[224:227], v[224:225], v[220:221], 0\n\t"
"s_waitcnt lgkmcnt(2)\n\t"
"v_mfma_f32_4x4x4_16b_bf16 v[228:231], v[228:229], v[220:221], 0\n\t"
"s_waitcnt lgkmcnt(1)\n\t"
"v_mfma_f32_4x4x4_16b_bf16 v[236:239], v[236:237], v[220:221], 0\n\t"
"v_mfma_f32_4x4x4_16b_bf16 v[232:235], v[232:233], v[220:221], 0\n\t"
"v_mfma_f32_16x16x32_bf16 a[120:123], a[212:215], a[180:183], a[120:123]\n\t"
"ds_read_b128 a[172:175], v223 offset:7424\n\t"
"v_cvt_pk_bf16_f32 v224, v224, v225\n\t"
"v_mfma_f32_16x16x32_bf16 a[120:123], v[248:251], a[176:179], a[120:123]\n\t"
"ds_read_b128 a[176:179], v223 offset:13568\n\t"
"v_cvt_pk_bf16_f32 v225, v226, v227\n\t"
"v_mfma_f32_16x16x32_bf16 a[120:123], a[216:219], a[184:187], a[120:123]\n\t"
"v_cvt_pk_bf16_f32 v226, v228, v229\n\t"
"v_cvt_pk_bf16_f32 v227, v230, v231\n\t"
"v_mfma_f32_16x16x32_bf16 a[128:131], v[248:251], a[180:183], a[128:131]\n\t"
"ds_read_b128 a[180:183], v222 offset:21248\n\t"
"v_cvt_pk_bf16_f32 v236, v236, v237\n\t"
"v_mfma_f32_16x16x32_bf16 a[128:131], a[216:219], a[188:191], a[128:131]\n\t"
"v_cvt_pk_bf16_f32 v237, v238, v239\n\t"
"v_cvt_pk_bf16_f32 v238, v232, v233\n\t"
"v_mfma_f32_16x16x32_bf16 a[128:131], a[212:215], a[184:187], a[128:131]\n\t"
"ds_write_b128 v245, v[224:227] offset:26112\n\t"
"v_cvt_pk_bf16_f32 v239, v234, v235\n\t"
"v_mfma_f32_16x16x32_bf16 a[136:139], v[248:251], a[184:187], a[136:139]\n\t"
"v_mfma_f32_16x16x32_bf16 a[136:139], a[212:215], a[188:191], a[136:139]\n\t"
"ds_read_b128 a[184:187], v223 offset:27392\n\t"
"v_mfma_f32_16x16x32_bf16 a[136:139], a[216:219], a[192:195], a[136:139]\n\t"
"ds_write_b128 v245, v[236:239] offset:32256\n\t"
"v_mfma_f32_16x16x32_bf16 a[144:147], a[216:219], a[196:199], a[144:147]\n\t"
"v_mfma_f32_16x16x32_bf16 a[144:147], a[212:215], a[192:195], a[144:147]\n\t"
"v_mfma_f32_16x16x32_bf16 a[144:147], v[248:251], a[188:191], a[144:147]\n\t"
"ds_read_b128 a[188:191], v223 offset:33536\n\t"
"v_mfma_f32_16x16x32_bf16 a[152:155], v[248:251], a[192:195], a[152:155]\n\t"
"ds_read_b128 a[192:195], v222 offset:41216\n\t"
"v_mfma_f32_16x16x32_bf16 a[152:155], a[212:215], a[196:199], a[152:155]\n\t"
"v_mfma_f32_16x16x32_bf16 a[152:155], a[216:219], a[200:203], a[152:155]\n\t"
"s_barrier\n\t"
"v_mfma_f32_16x16x32_bf16 a[160:163], a[212:215], a[200:203], a[160:163]\n\t"
"s_waitcnt vmcnt(7) lgkmcnt(6)\n\t"
"ds_read_b64_tr_b16 v[228:229], v240 offset:39936\n\t"
"v_mfma_f32_16x16x32_bf16 a[160:163], v[248:251], a[196:199], a[160:163]\n\t"
"ds_read_b64_tr_b16 v[230:231], v240 offset:41472\n\t"
"v_mfma_f32_16x16x32_bf16 a[160:163], a[216:219], a[204:207], a[160:163]\n\t"
"ds_read_b64_tr_b16 v[232:233], v241 offset:39936\n\t"
"v_mfma_f32_16x16x32_bf16 v[200:203], a[220:223], a[168:171], v[200:203]\n\t"
"s_waitcnt lgkmcnt(8)\n\t"
"ds_read_b64_tr_b16 v[234:235], v241 offset:41472\n\t"
"v_subrev_u32 v240, 59904, v240\n\t"
"v_mfma_f32_16x16x32_bf16 v[200:203], a[236:239], a[176:179], v[200:203]\n\t"
"ds_read_b128 a[196:199], v223 offset:47360\n\t"
"v_subrev_u32 v241, 59904, v241\n\t"
"v_mfma_f32_16x16x32_bf16 v[200:203], a[224:227], a[172:175], v[200:203]\n\t"
"ds_read_b64_tr_b16 v[224:225], v240 offset:39936\n\t"
"v_mfma_f32_16x16x32_bf16 v[208:211], a[220:223], a[172:175], v[208:211]\n\t"
"s_waitcnt lgkmcnt(4)\n\t"
"ds_read_b64_tr_b16 v[226:227], v240 offset:41472\n\t"
"v_mfma_f32_16x16x32_bf16 v[208:211], a[224:227], a[176:179], v[208:211]\n\t"
"buffer_load_dwordx4 a[212:215], off, s[80:83], s20 offset:1024 sc1\n\t"
"v_mfma_f32_16x16x32_bf16 v[208:211], a[236:239], a[180:183], v[208:211]\n\t"
"s_waitcnt lgkmcnt(3)\n\t"
"ds_read_b64_tr_b16 v[236:237], v241 offset:39936\n\t"
"ds_read_b64_tr_b16 v[238:239], v241 offset:41472\n\t"
"v_mfma_f32_16x16x32_bf16 v[228:231], v[228:231], a[208:211], 0\n\t"
"buffer_load_dwordx4 v[248:251], off, s[80:83], s20 offset:2048 sc1\n\t"
"s_mov_b32 s20, 712704\n\t"
"v_mfma_f32_16x16x32_bf16 v[232:235], v[232:235], a[208:211], 0\n\t"
"s_waitcnt lgkmcnt(2)\n\t"
"buffer_load_dwordx4 a[216:219], off, s[80:83], s20 offset:0 sc1\n\t"
"v_mfma_f32_16x16x32_bf16 v[224:227], v[224:227], a[208:211], 0\n\t"
"s_waitcnt lgkmcnt(0)\n\t"
"ds_read_b128 a[200:203], v223 offset:53504\n\t"
"v_mfma_f32_16x16x32_bf16 v[236:239], v[236:239], a[208:211], 0\n\t"
"ds_read_b128 a[204:207], v222 offset:61184\n\t"
"v_mfma_f32_16x16x32_bf16 v[216:219], a[224:227], a[180:183], v[216:219]\n\t"
"v_mfma_f32_16x16x32_bf16 v[216:219], a[220:223], a[176:179], v[216:219]\n\t"
"v_cvt_pk_bf16_f32 v228, v228, v229\n\t"
"v_cvt_pk_bf16_f32 v229, v230, v231\n\t"
"v_mfma_f32_16x16x32_bf16 v[216:219], a[236:239], a[184:187], v[216:219]\n\t"
"v_cvt_pk_bf16_f32 v230, v232, v233\n\t"
"v_cvt_pk_bf16_f32 v231, v234, v235\n\t"
"v_mfma_f32_16x16x32_bf16 a[4:7], a[220:223], a[180:183], a[4:7]\n\t"
"ds_read_b64_tr_b16 v[232:233], v246 offset:39936\n\t"
"v_cvt_pk_bf16_f32 v224, v224, v225\n\t"
"v_mfma_f32_16x16x32_bf16 a[4:7], a[236:239], a[188:191], a[4:7]\n\t"
"v_cvt_pk_bf16_f32 v225, v226, v227\n\t"
"v_cvt_pk_bf16_f32 v226, v236, v237\n\t"
"v_mfma_f32_16x16x32_bf16 a[4:7], a[224:227], a[184:187], a[4:7]\n\t"
"ds_write_b128 v242, v[228:231] offset:32256\n\t"
"v_mfma_f32_16x16x32_bf16 a[12:15], a[220:223], a[184:187], a[12:15]\n\t"
"v_cvt_pk_bf16_f32 v227, v238, v239\n\t"
"v_mfma_f32_16x16x32_bf16 a[12:15], a[224:227], a[188:191], a[12:15]\n\t"
"ds_read_b64_tr_b16 v[236:237], v255 offset:39936\n\t"
"v_mfma_f32_16x16x32_bf16 a[12:15], a[236:239], a[192:195], a[12:15]\n\t"
"ds_write_b128 v242, v[224:227] offset:27648\n\t"
"v_mfma_f32_16x16x32_bf16 a[20:23], a[236:239], a[196:199], a[20:23]\n\t"
"s_waitcnt lgkmcnt(5)\n\t"
"v_mfma_f32_16x16x32_bf16 a[20:23], a[224:227], a[192:195], a[20:23]\n\t"
"ds_read_b64_tr_b16 v[228:229], v246 offset:46080\n\t"
"v_mfma_f32_16x16x32_bf16 a[20:23], a[220:223], a[188:191], a[20:23]\n\t"
"ds_read_b64_tr_b16 v[224:225], v255 offset:46080\n\t"
"v_mfma_f32_16x16x32_bf16 a[28:31], a[220:223], a[192:195], a[28:31]\n\t"
"s_waitcnt lgkmcnt(6)\n\t"
"v_mfma_f32_16x16x32_bf16 a[28:31], a[224:227], a[196:199], a[28:31]\n\t"
"v_mfma_f32_16x16x32_bf16 a[28:31], a[236:239], a[200:203], a[28:31]\n\t"
"s_barrier\n\t"
"v_mfma_f32_16x16x32_bf16 a[36:39], a[224:227], a[200:203], a[36:39]\n\t"
"s_waitcnt lgkmcnt(5)\n\t"
"v_mfma_f32_16x16x32_bf16 a[36:39], a[220:223], a[196:199], a[36:39]\n\t"
"buffer_load_dwordx4 a[224:227], off, s[80:83], s20 offset:1024 sc1\n\t"
"v_mfma_f32_16x16x32_bf16 a[36:39], a[236:239], a[204:207], a[36:39]\n\t"
"s_waitcnt lgkmcnt(1)\n\t"
"v_mfma_f32_4x4x4_16b_bf16 v[232:235], v[232:233], v[220:221], 0\n\t"
"s_waitcnt lgkmcnt(0)\n\t"
"v_mfma_f32_4x4x4_16b_bf16 v[236:239], v[236:237], v[220:221], 0\n\t"
"s_waitcnt vmcnt(8)\n\t"
"v_mfma_f32_4x4x4_16b_bf16 v[228:231], v[228:229], v[220:221], 0\n\t"
"v_mfma_f32_4x4x4_16b_bf16 v[224:227], v[224:225], v[220:221], 0\n\t"
"buffer_load_dwordx4 a[220:223], off, s[80:83], s20 offset:2048 sc1\n\t"
"s_mov_b32 s20, 724992\n\t"
"v_mfma_f32_16x16x32_bf16 a[44:47], a[240:243], a[168:171], a[44:47]\n\t"
"buffer_load_dwordx4 a[236:239], off, s[80:83], s20 offset:0 sc1\n\t"
"v_mfma_f32_16x16x32_bf16 a[44:47], a[248:251], a[176:179], a[44:47]\n\t"
"v_cvt_pk_bf16_f32 v232, v232, v233\n\t"
"v_cvt_pk_bf16_f32 v233, v234, v235\n\t"
"v_mfma_f32_16x16x32_bf16 a[44:47], a[244:247], a[172:175], a[44:47]\n\t"
"v_cvt_pk_bf16_f32 v234, v236, v237\n\t"
"v_cvt_pk_bf16_f32 v235, v238, v239\n\t"
"v_mfma_f32_16x16x32_bf16 a[52:55], a[240:243], a[172:175], a[52:55]\n\t"
"ds_read_b64_tr_b16 v[236:237], v246 offset:52224\n\t"
"v_cvt_pk_bf16_f32 v228, v228, v229\n\t"
"v_mfma_f32_16x16x32_bf16 a[52:55], a[244:247], a[176:179], a[52:55]\n\t"
"v_cvt_pk_bf16_f32 v229, v230, v231\n\t"
"v_cvt_pk_bf16_f32 v230, v224, v225\n\t"
"v_mfma_f32_16x16x32_bf16 a[52:55], a[248:251], a[180:183], a[52:55]\n\t"
"ds_write_b128 v245, v[232:235] offset:39936\n\t"
"v_cvt_pk_bf16_f32 v231, v226, v227\n\t"
"v_mfma_f32_16x16x32_bf16 a[60:63], a[248:251], a[184:187], a[60:63]\n\t"
"v_mfma_f32_16x16x32_bf16 a[60:63], a[244:247], a[180:183], a[60:63]\n\t"
"ds_read_b64_tr_b16 v[224:225], v255 offset:52224\n\t"
"v_mfma_f32_16x16x32_bf16 a[60:63], a[240:243], a[176:179], a[60:63]\n\t"
"ds_write_b128 v245, v[228:231] offset:46080\n\t"
"v_mfma_f32_16x16x32_bf16 a[68:71], a[240:243], a[180:183], a[68:71]\n\t"
"v_mfma_f32_16x16x32_bf16 a[68:71], a[244:247], a[184:187], a[68:71]\n\t"
"v_mfma_f32_16x16x32_bf16 a[68:71], a[248:251], a[188:191], a[68:71]\n\t"
"v_mfma_f32_16x16x32_bf16 a[76:79], a[240:243], a[184:187], a[76:79]\n\t"
"s_waitcnt lgkmcnt(3)\n\t"
"v_mfma_f32_16x16x32_bf16 a[76:79], a[248:251], a[192:195], a[76:79]\n\t"
"s_waitcnt lgkmcnt(1)\n\t"
"v_mfma_f32_16x16x32_bf16 a[76:79], a[244:247], a[188:191], a[76:79]\n\t"
"v_mfma_f32_4x4x4_16b_bf16 v[236:239], v[236:237], v[220:221], 0\n\t"
"v_mfma_f32_4x4x4_16b_bf16 v[224:227], v[224:225], v[220:221], 0\n\t"
"v_mfma_f32_16x16x32_bf16 a[84:87], a[240:243], a[188:191], a[84:87]\n\t"
"v_mfma_f32_16x16x32_bf16 a[84:87], a[248:251], a[196:199], a[84:87]\n\t"
"v_cvt_pk_bf16_f32 v236, v236, v237\n\t"
"v_cvt_pk_bf16_f32 v237, v238, v239\n\t"
"v_mfma_f32_16x16x32_bf16 a[84:87], a[244:247], a[192:195], a[84:87]\n\t"
"v_cvt_pk_bf16_f32 v238, v224, v225\n\t"
"v_cvt_pk_bf16_f32 v239, v226, v227\n\t"
"v_mfma_f32_16x16x32_bf16 a[92:95], a[240:243], a[192:195], a[92:95]\n\t"
"v_mfma_f32_16x16x32_bf16 a[92:95], a[244:247], a[196:199], a[92:95]\n\t"
"v_mfma_f32_16x16x32_bf16 a[92:95], a[248:251], a[200:203], a[92:95]\n\t"
"ds_write_b128 v245, v[236:239] offset:52224\n\t"
"v_mfma_f32_16x16x32_bf16 a[100:103], a[244:247], a[200:203], a[100:103]\n\t"
"buffer_load_dwordx4 a[244:247], off, s[80:83], s20 offset:1024 sc1\n\t"
"v_mfma_f32_16x16x32_bf16 a[100:103], a[240:243], a[196:199], a[100:103]\n\t"
"s_waitcnt vmcnt(8)\n\t"
"v_mfma_f32_16x16x32_bf16 a[100:103], a[248:251], a[204:207], a[100:103]\n\t"
"v_mfma_f32_16x16x32_bf16 a[108:111], a[252:255], a[168:171], a[108:111]\n\t"
"buffer_load_dwordx4 a[240:243], off, s[80:83], s20 offset:2048 sc1\n\t"
"v_mfma_f32_16x16x32_bf16 a[108:111], v[96:99], a[176:179], a[108:111]\n\t"
"s_mov_b32 s20, 737280\n\t"
"v_mfma_f32_16x16x32_bf16 a[108:111], a[232:235], a[172:175], a[108:111]\n\t"
"s_waitcnt lgkmcnt(0)\n\t"
"s_barrier\n\t"
"v_mfma_f32_16x16x32_bf16 a[116:119], a[252:255], a[172:175], a[116:119]\n\t"
"ds_read_b128 a[168:171], v222 offset:0\n\t"
"v_mfma_f32_16x16x32_bf16 a[116:119], a[232:235], a[176:179], a[116:119]\n\t"
"buffer_load_dwordx4 a[248:251], off, s[80:83], s20 offset:0 sc1\n\t"
"v_mfma_f32_16x16x32_bf16 a[116:119], v[96:99], a[180:183], a[116:119]\n\t"
"ds_read_b128 a[172:175], v223 offset:6144\n\t"
"v_mfma_f32_16x16x32_bf16 a[124:127], v[96:99], a[184:187], a[124:127]\n\t"
"v_mfma_f32_16x16x32_bf16 a[124:127], a[232:235], a[180:183], a[124:127]\n\t"
"v_mfma_f32_16x16x32_bf16 a[124:127], a[252:255], a[176:179], a[124:127]\n\t"
"ds_read_b128 a[176:179], v223 offset:12288\n\t"
"v_mfma_f32_16x16x32_bf16 a[132:135], a[252:255], a[180:183], a[132:135]\n\t"
"ds_read_b64_tr_b16 v[232:233], v240 offset:512\n\t"
"v_mfma_f32_16x16x32_bf16 a[132:135], a[232:235], a[184:187], a[132:135]\n\t"
"ds_read_b64_tr_b16 v[234:235], v240 offset:2048\n\t"
"v_mfma_f32_16x16x32_bf16 a[132:135], v[96:99], a[188:191], a[132:135]\n\t"
"ds_read_b64_tr_b16 v[228:229], v241 offset:512\n\t"
"v_mfma_f32_16x16x32_bf16 a[140:143], v[96:99], a[192:195], a[140:143]\n\t"
"ds_read_b64_tr_b16 v[230:231], v241 offset:2048\n\t"
"v_mfma_f32_16x16x32_bf16 a[140:143], a[232:235], a[188:191], a[140:143]\n\t"
"ds_read_b128 a[180:183], v222 offset:19968\n\t"
"v_mfma_f32_16x16x32_bf16 a[140:143], a[252:255], a[184:187], a[140:143]\n\t"
"ds_read_b64_tr_b16 v[224:225], v240 offset:20480\n\t"
"v_mfma_f32_16x16x32_bf16 a[148:151], a[252:255], a[188:191], a[148:151]\n\t"
"ds_read_b64_tr_b16 v[226:227], v240 offset:22016\n\t"
"v_mfma_f32_16x16x32_bf16 a[148:151], a[232:235], a[192:195], a[148:151]\n\t"
"ds_read_b64_tr_b16 v[236:237], v241 offset:20480\n\t"
"v_mfma_f32_16x16x32_bf16 a[148:151], v[96:99], a[196:199], a[148:151]\n\t"
"ds_read_b64_tr_b16 v[238:239], v241 offset:22016\n\t"
"v_mfma_f32_16x16x32_bf16 a[156:159], v[96:99], a[200:203], a[156:159]\n\t"
"ds_read_b128 a[184:187], v223 offset:26112\n\t"
"v_mfma_f32_16x16x32_bf16 a[156:159], a[232:235], a[196:199], a[156:159]\n\t"
"ds_read_b128 a[188:191], v223 offset:32256\n\t"
"v_mfma_f32_16x16x32_bf16 a[156:159], a[252:255], a[192:195], a[156:159]\n\t"
"ds_read_b128 a[192:195], v222 offset:39936\n\t"
"v_mfma_f32_16x16x32_bf16 a[164:167], a[252:255], a[196:199], a[164:167]\n\t"
"buffer_load_dwordx4 a[252:255], off, s[80:83], s20 offset:1024 sc1\n\t"
"v_mfma_f32_16x16x32_bf16 a[164:167], a[232:235], a[200:203], a[164:167]\n\t"
"s_waitcnt lgkmcnt(8)\n\t"
"ds_read_b128 a[196:199], v223 offset:46080\n\t"
"v_mfma_f32_16x16x32_bf16 a[164:167], v[96:99], a[204:207], a[164:167]\n\t"
"s_waitcnt lgkmcnt(6)\n\t"
"ds_read_b128 a[200:203], v223 offset:52224\n\t"
"v_mfma_f32_16x16x32_bf16 v[232:235], v[232:235], a[208:211], 0\n\t"
"buffer_load_dwordx4 v[96:99], off, s[80:83], s20 offset:2048 sc1\n\t"
"v_mfma_f32_16x16x32_bf16 v[228:231], v[228:231], a[208:211], 0\n\t"
"s_waitcnt vmcnt(9) lgkmcnt(5)\n\t"
"ds_read_b128 a[204:207], v222 offset:59904\n\t"
"s_mov_b32 s20, 749568\n\t"
"v_mfma_f32_16x16x32_bf16 v[224:227], v[224:227], a[208:211], 0\n\t"
"v_mfma_f32_16x16x32_bf16 v[236:239], v[236:239], a[208:211], 0\n\t"
"buffer_load_dwordx4 a[232:235], off, s[80:83], s20 offset:0 sc1\n\t"
"Lst24_%=:\n\t"
"v_mfma_f32_16x16x32_bf16 v[196:199], a[228:231], a[168:171], v[196:199]\n\t"
"v_mfma_f32_16x16x32_bf16 v[196:199], v[248:251], a[176:179], v[196:199]\n\t"
"v_cvt_pk_bf16_f32 v232, v232, v233\n\t"
"v_cvt_pk_bf16_f32 v233, v234, v235\n\t"
"v_mfma_f32_16x16x32_bf16 v[196:199], a[212:215], a[172:175], v[196:199]\n\t"
"v_cvt_pk_bf16_f32 v234, v228, v229\n\t"
"v_cvt_pk_bf16_f32 v235, v230, v231\n\t"
"v_mfma_f32_16x16x32_bf16 v[204:207], a[228:231], a[172:175], v[204:207]\n\t"
"s_waitcnt lgkmcnt(5)\n\t"
"v_cvt_pk_bf16_f32 v224, v224, v225\n\t"
"v_cvt_pk_bf16_f32 v225, v226, v227\n\t"
"v_mfma_f32_16x16x32_bf16 v[204:207], a[212:215], a[176:179], v[204:207]\n\t"
"v_cvt_pk_bf16_f32 v226, v236, v237\n\t"
"v_cvt_pk_bf16_f32 v227, v238, v239\n\t"
"v_mfma_f32_16x16x32_bf16 v[204:207], v[248:251], a[180:183], v[204:207]\n\t"
"ds_write_b128 v242, v[232:235] offset:0\n\t"
"v_mfma_f32_16x16x32_bf16 v[212:215], v[248:251], a[184:187], v[212:215]\n\t"
"s_waitcnt lgkmcnt(5)\n\t"
"v_mfma_f32_16x16x32_bf16 v[212:215], a[212:215], a[180:183], v[212:215]\n\t"
"v_mfma_f32_16x16x32_bf16 v[212:215], a[228:231], a[176:179], v[212:215]\n\t"
"ds_write_b128 v242, v[224:227] offset:1536\n\t"
"v_mfma_f32_16x16x32_bf16 a[0:3], a[228:231], a[180:183], a[0:3]\n\t"
"s_waitcnt lgkmcnt(5)\n\t"
"v_mfma_f32_16x16x32_bf16 a[0:3], a[212:215], a[184:187], a[0:3]\n\t"
"ds_read_b64_tr_b16 v[228:229], v240 offset:40448\n\t"
"v_mfma_f32_16x16x32_bf16 a[0:3], v[248:251], a[188:191], a[0:3]\n\t"
"ds_read_b64_tr_b16 v[230:231], v240 offset:41984\n\t"
"v_mfma_f32_16x16x32_bf16 a[8:11], v[248:251], a[192:195], a[8:11]\n\t"
"s_waitcnt lgkmcnt(6)\n\t"
"ds_read_b64_tr_b16 v[236:237], v241 offset:40448\n\t"
"v_mfma_f32_16x16x32_bf16 a[8:11], a[212:215], a[188:191], a[8:11]\n\t"
"ds_read_b64_tr_b16 v[238:239], v241 offset:41984\n\t"
"v_add_u32 v240, 59904, v240\n\t"
"v_mfma_f32_16x16x32_bf16 a[8:11], a[228:231], a[184:187], a[8:11]\n\t"
"v_add_u32 v241, 59904, v241\n\t"
"v_mfma_f32_16x16x32_bf16 a[16:19], a[228:231], a[188:191], a[16:19]\n\t"
"s_waitcnt lgkmcnt(7)\n\t"
"ds_read_b64_tr_b16 v[232:233], v240 offset:512\n\t"
"v_mfma_f32_16x16x32_bf16 a[16:19], a[212:215], a[192:195], a[16:19]\n\t"
"ds_read_b64_tr_b16 v[234:235], v240 offset:2048\n\t"
"v_mfma_f32_16x16x32_bf16 a[16:19], v[248:251], a[196:199], a[16:19]\n\t"
"ds_read_b64_tr_b16 v[224:225], v241 offset:512\n\t"
"v_mfma_f32_16x16x32_bf16 a[24:27], v[248:251], a[200:203], a[24:27]\n\t"
"s_waitcnt lgkmcnt(9)\n\t"
"ds_read_b64_tr_b16 v[226:227], v241 offset:2048\n\t"
"v_mfma_f32_16x16x32_bf16 a[24:27], a[212:215], a[196:199], a[24:27]\n\t"
"v_mfma_f32_16x16x32_bf16 a[24:27], a[228:231], a[192:195], a[24:27]\n\t"
"v_mfma_f32_16x16x32_bf16 a[32:35], a[228:231], a[196:199], a[32:35]\n\t"
"buffer_load_dwordx4 a[228:231], off, s[80:83], s20 offset:1024 sc1\n\t"
"v_mfma_f32_16x16x32_bf16 a[32:35], a[212:215], a[200:203], a[32:35]\n\t"
"s_waitcnt lgkmcnt(4)\n\t"
"v_mfma_f32_16x16x32_bf16 a[32:35], v[248:251], a[204:207], a[32:35]\n\t"
"s_waitcnt lgkmcnt(2)\n\t"
"v_mfma_f32_16x16x32_bf16 v[228:231], v[228:231], a[208:211], 0\n\t"
"buffer_load_dwordx4 v[248:251], off, s[80:83], s20 offset:2048 sc1\n\t"
"v_mfma_f32_16x16x32_bf16 v[236:239], v[236:239], a[208:211], 0\n\t"
"s_waitcnt vmcnt(9) lgkmcnt(0)\n\t"
"s_mov_b32 s20, 761856\n\t"
"v_mfma_f32_16x16x32_bf16 v[232:235], v[232:235], a[208:211], 0\n\t"
"v_mfma_f32_16x16x32_bf16 v[224:227], v[224:227], a[208:211], 0\n\t"
"buffer_load_dwordx4 a[212:215], off, s[80:83], s20 offset:0 sc1\n\t"
"v_mfma_f32_16x16x32_bf16 a[40:43], a[216:219], a[168:171], a[40:43]\n\t"
"v_mfma_f32_16x16x32_bf16 a[40:43], a[220:223], a[176:179], a[40:43]\n\t"
"v_cvt_pk_bf16_f32 v228, v228, v229\n\t"
"v_cvt_pk_bf16_f32 v229, v230, v231\n\t"
"v_mfma_f32_16x16x32_bf16 a[40:43], a[224:227], a[172:175], a[40:43]\n\t"
"v_cvt_pk_bf16_f32 v230, v236, v237\n\t"
"v_cvt_pk_bf16_f32 v231, v238, v239\n\t"
"v_mfma_f32_16x16x32_bf16 a[48:51], a[216:219], a[172:175], a[48:51]\n\t"
"v_cvt_pk_bf16_f32 v232, v232, v233\n\t"
"v_cvt_pk_bf16_f32 v233, v234, v235\n\t"
"v_mfma_f32_16x16x32_bf16 a[48:51], a[224:227], a[176:179], a[48:51]\n\t"
"v_cvt_pk_bf16_f32 v234, v224, v225\n\t"
"v_cvt_pk_bf16_f32 v235, v226, v227\n\t"
"v_mfma_f32_16x16x32_bf16 a[48:51], a[220:223], a[180:183], a[48:51]\n\t"
"ds_write_b128 v242, v[228:231] offset:3072\n\t"
"v_mfma_f32_16x16x32_bf16 a[56:59], a[220:223], a[184:187], a[56:59]\n\t"
"v_mfma_f32_16x16x32_bf16 a[56:59], a[224:227], a[180:183], a[56:59]\n\t"
"v_mfma_f32_16x16x32_bf16 a[56:59], a[216:219], a[176:179], a[56:59]\n\t"
"ds_write_b128 v242, v[232:235] offset:4608\n\t"
"v_mfma_f32_16x16x32_bf16 a[64:67], a[216:219], a[180:183], a[64:67]\n\t"
"v_mfma_f32_16x16x32_bf16 a[64:67], a[224:227], a[184:187], a[64:67]\n\t"
"ds_read_b64_tr_b16 v[236:237], v240 offset:20480\n\t"
"v_mfma_f32_16x16x32_bf16 a[64:67], a[220:223], a[188:191], a[64:67]\n\t"
"ds_read_b64_tr_b16 v[238:239], v240 offset:22016\n\t"
"v_mfma_f32_16x16x32_bf16 a[72:75], a[220:223], a[192:195], a[72:75]\n\t"
"ds_read_b64_tr_b16 v[224:225], v241 offset:20480\n\t"
"v_mfma_f32_16x16x32_bf16 a[72:75], a[224:227], a[188:191], a[72:75]\n\t"
"ds_read_b64_tr_b16 v[226:227], v241 offset:22016\n\t"
"v_mfma_f32_16x16x32_bf16 a[72:75], a[216:219], a[184:187], a[72:75]\n\t"
"ds_read_b64_tr_b16 v[228:229], v240 offset:40448\n\t"
"v_mfma_f32_16x16x32_bf16 a[80:83], a[216:219], a[188:191], a[80:83]\n\t"
"ds_read_b64_tr_b16 v[230:231], v240 offset:41984\n\t"
"v_mfma_f32_16x16x32_bf16 a[80:83], a[224:227], a[192:195], a[80:83]\n\t"
"ds_read_b64_tr_b16 v[232:233], v241 offset:40448\n\t"
"v_mfma_f32_16x16x32_bf16 a[80:83], a[220:223], a[196:199], a[80:83]\n\t"
"ds_read_b64_tr_b16 v[234:235], v241 offset:41984\n\t"
"v_mfma_f32_16x16x32_bf16 a[88:91], a[220:223], a[200:203], a[88:91]\n\t"
"v_mfma_f32_16x16x32_bf16 a[88:91], a[224:227], a[196:199], a[88:91]\n\t"
"v_mfma_f32_16x16x32_bf16 a[88:91], a[216:219], a[192:195], a[88:91]\n\t"
"v_mfma_f32_16x16x32_bf16 a[96:99], a[216:219], a[196:199], a[96:99]\n\t"
"buffer_load_dwordx4 a[216:219], off, s[80:83], s20 offset:1024 sc1\n\t"
"v_mfma_f32_16x16x32_bf16 a[96:99], a[224:227], a[200:203], a[96:99]\n\t"
"s_waitcnt lgkmcnt(4)\n\t"
"v_mfma_f32_16x16x32_bf16 a[96:99], a[220:223], a[204:207], a[96:99]\n\t"
"s_waitcnt lgkmcnt(2)\n\t"
"v_mfma_f32_16x16x32_bf16 v[236:239], v[236:239], a[208:211], 0\n\t"
"buffer_load_dwordx4 a[220:223], off, s[80:83], s20 offset:2048 sc1\n\t"
"v_mfma_f32_16x16x32_bf16 v[224:227], v[224:227], a[208:211], 0\n\t"
"s_waitcnt vmcnt(9) lgkmcnt(0)\n\t"
"s_mov_b32 s20, 626688\n\t"
"v_mfma_f32_16x16x32_bf16 v[228:231], v[228:231], a[208:211], 0\n\t"
"v_mfma_f32_16x16x32_bf16 v[232:235], v[232:235], a[208:211], 0\n\t"
"buffer_load_dwordx4 a[224:227], off, s[80:83], s20 offset:0 sc1\n\t"
"v_mfma_f32_16x16x32_bf16 a[104:107], a[236:239], a[168:171], a[104:107]\n\t"
"ds_read_b128 a[168:171], v222 offset:256\n\t"
"v_mfma_f32_16x16x32_bf16 a[104:107], a[240:243], a[176:179], a[104:107]\n\t"
"v_cvt_pk_bf16_f32 v236, v236, v237\n\t"
"v_cvt_pk_bf16_f32 v237, v238, v239\n\t"
"v_mfma_f32_16x16x32_bf16 a[104:107], a[244:247], a[172:175], a[104:107]\n\t"
"v_cvt_pk_bf16_f32 v238, v224, v225\n\t"
"v_cvt_pk_bf16_f32 v239, v226, v227\n\t"
"v_mfma_f32_16x16x32_bf16 a[112:115], a[236:239], a[172:175], a[112:115]\n\t"
"ds_read_b128 a[172:175], v223 offset:6400\n\t"
"v_cvt_pk_bf16_f32 v228, v228, v229\n\t"
"v_mfma_f32_16x16x32_bf16 a[112:115], a[244:247], a[176:179], a[112:115]\n\t"
"v_cvt_pk_bf16_f32 v229, v230, v231\n\t"
"v_cvt_pk_bf16_f32 v230, v232, v233\n\t"
"v_mfma_f32_16x16x32_bf16 a[112:115], a[240:243], a[180:183], a[112:115]\n\t"
"buffer_load_dwordx4 a[208:211], off, s[80:83], s20 offset:1024 sc1\n\t"
"v_mfma_f32_16x16x32_bf16 a[120:123], a[240:243], a[184:187], a[120:123]\n\t"
"v_cvt_pk_bf16_f32 v231, v234, v235\n\t"
"v_mfma_f32_16x16x32_bf16 a[120:123], a[244:247], a[180:183], a[120:123]\n\t"
"v_mfma_f32_16x16x32_bf16 a[120:123], a[236:239], a[176:179], a[120:123]\n\t"
"ds_write_b128 v242, v[236:239] offset:6144\n\t"
"v_mfma_f32_16x16x32_bf16 a[128:131], a[236:239], a[180:183], a[128:131]\n\t"
"v_mfma_f32_16x16x32_bf16 a[128:131], a[244:247], a[184:187], a[128:131]\n\t"
"ds_read_b128 a[176:179], v223 offset:12544\n\t"
"v_mfma_f32_16x16x32_bf16 a[128:131], a[240:243], a[188:191], a[128:131]\n\t"
"ds_write_b128 v242, v[228:231] offset:7680\n\t"
"v_mfma_f32_16x16x32_bf16 a[136:139], a[240:243], a[192:195], a[136:139]\n\t"
"Lpg18_%=:\n\t"
"s_cbranch_vccnz Lvr2_%=\n\t"
"v_mfma_f32_16x16x32_bf16 a[136:139], a[244:247], a[188:191], a[136:139]\n\t"
"ds_read_b128 a[180:183], v222 offset:20224\n\t"
"v_mfma_f32_16x16x32_bf16 a[136:139], a[236:239], a[184:187], a[136:139]\n\t"
"buffer_load_dwordx4 v[240:243], off, s[80:83], s20 offset:2048 sc1\n\t"
"v_mfma_f32_16x16x32_bf16 a[144:147], a[236:239], a[188:191], a[144:147]\n\t"
"s_mov_b32 s20, 638976\n\t"
"ds_read_b128 a[184:187], v223 offset:26368\n\t"
"v_mfma_f32_16x16x32_bf16 a[144:147], a[244:247], a[192:195], a[144:147]\n\t"
"ds_read_b128 a[188:191], v223 offset:32512\n\t"
"v_mfma_f32_16x16x32_bf16 a[144:147], a[240:243], a[196:199], a[144:147]\n\t"
"v_mfma_f32_16x16x32_bf16 a[152:155], a[240:243], a[200:203], a[152:155]\n\t"
"v_mfma_f32_16x16x32_bf16 a[152:155], a[244:247], a[196:199], a[152:155]\n\t"
"v_mfma_f32_16x16x32_bf16 a[152:155], a[236:239], a[192:195], a[152:155]\n\t"
"ds_read_b128 a[192:195], v222 offset:40192\n\t"
"v_mfma_f32_16x16x32_bf16 a[160:163], a[236:239], a[196:199], a[160:163]\n\t"
"buffer_load_dwordx4 a[236:239], off, s[80:83], s20 offset:0 sc1\n\t"
"v_mfma_f32_16x16x32_bf16 a[160:163], a[244:247], a[200:203], a[160:163]\n\t"
"s_waitcnt vmcnt(10) lgkmcnt(5)\n\t"
"ds_read_b128 a[196:199], v223 offset:46336\n\t"
"v_mfma_f32_16x16x32_bf16 a[160:163], a[240:243], a[204:207], a[160:163]\n\t"
"ds_read_b128 a[200:203], v223 offset:52480\n\t"
"v_mfma_f32_16x16x32_bf16 v[200:203], a[248:251], a[168:171], v[200:203]\n\t"
"s_waitcnt lgkmcnt(5)\n\t"
"ds_read_b128 a[204:207], v222 offset:60160\n\t"
"v_mfma_f32_16x16x32_bf16 v[200:203], v[96:99], a[176:179], v[200:203]\n\t"
"buffer_load_dwordx4 a[240:243], off, s[80:83], s20 offset:1024 sc1\n\t"
"v_mfma_f32_16x16x32_bf16 v[200:203], a[252:255], a[172:175], v[200:203]\n\t"
"s_add_u32 m0, s25, 768\n\t"
"v_mfma_f32_16x16x32_bf16 v[208:211], a[248:251], a[172:175], v[208:211]\n\t"
"buffer_load_dwordx4 a[244:247], off, s[80:83], s20 offset:2048 sc1\n\t"
"v_mfma_f32_16x16x32_bf16 v[208:211], a[252:255], a[176:179], v[208:211]\n\t"
"s_waitcnt lgkmcnt(5)\n\t"
"v_mfma_f32_16x16x32_bf16 v[208:211], v[96:99], a[180:183], v[208:211]\n\t"
"s_waitcnt lgkmcnt(0)\n\t"
"s_barrier\n\t"
"v_mfma_f32_16x16x32_bf16 v[216:219], v[96:99], a[184:187], v[216:219]\n\t"
"v_mfma_f32_16x16x32_bf16 v[216:219], a[252:255], a[180:183], v[216:219]\n\t"
"buffer_load_dwordx4 v254, s[12:15], 0 offen offset:256 nt lds\n\t"
"v_mfma_f32_16x16x32_bf16 v[216:219], a[248:251], a[176:179], v[216:219]\n\t"
"s_add_u32 m0, s26, 768\n\t"
"ds_read_b64_tr_b16 v[224:225], v246 offset:512\n\t"
"v_mfma_f32_16x16x32_bf16 a[4:7], a[248:251], a[180:183], a[4:7]\n\t"
"ds_read_b64_tr_b16 v[232:233], v255 offset:512\n\t"
"v_mfma_f32_16x16x32_bf16 a[4:7], a[252:255], a[184:187], a[4:7]\n\t"
"ds_read_b64_tr_b16 v[236:237], v246 offset:6656\n\t"
"v_mfma_f32_16x16x32_bf16 a[4:7], v[96:99], a[188:191], a[4:7]\n\t"
"s_mov_b64 exec, s[36:37]\n\t"
"buffer_load_dwordx4 v254, s[12:15], s27 offen offset:256 nt lds\n\t"
"s_mov_b64 exec, -1\n\t"
"s_add_u32 m0, s24, 2304\n\t"
"s_add_u32 s28, s21, 3072\n\t"
"v_mfma_f32_16x16x32_bf16 a[12:15], v[96:99], a[192:195], a[12:15]\n\t"
"buffer_load_dwordx4 v247, s[12:15], s28 offen offset:256 nt lds\n\t"
"v_mfma_f32_16x16x32_bf16 a[12:15], a[252:255], a[188:191], a[12:15]\n\t"
"s_add_u32 m0, s24, 8448\n\t"
"s_add_u32 s28, s21, 199680\n\t"
"ds_read_b64_tr_b16 v[228:229], v255 offset:6656\n\t"
"v_mfma_f32_16x16x32_bf16 a[12:15], a[248:251], a[184:187], a[12:15]\n\t"
"buffer_load_dwordx4 v247, s[12:15], s28 offen offset:256 nt lds\n\t"
"s_add_u32 m0, s24, 14592\n\t"
"s_add_u32 s28, s21, 396288\n\t"
"v_mfma_f32_16x16x32_bf16 a[20:23], a[248:251], a[188:191], a[20:23]\n\t"
"buffer_load_dwordx4 v247, s[12:15], s28 offen offset:256 nt lds\n\t"
"v_mfma_f32_16x16x32_bf16 a[20:23], a[252:255], a[192:195], a[20:23]\n\t"
"s_add_u32 m0, s24, 22272\n\t"
"s_add_u32 s28, s21, 592896\n\t"
"v_mfma_f32_16x16x32_bf16 a[20:23], v[96:99], a[196:199], a[20:23]\n\t"
"buffer_load_dwordx4 v247, s[12:15], s28 offen offset:256 nt lds\n\t"
"s_add_u32 m0, s24, 28416\n\t"
"s_add_u32 s28, s21, 789504\n\t"
"v_mfma_f32_16x16x32_bf16 a[28:31], v[96:99], a[200:203], a[28:31]\n\t"
"buffer_load_dwordx4 v247, s[12:15], s28 offen offset:256 nt lds\n\t"
"v_mfma_f32_16x16x32_bf16 a[28:31], a[252:255], a[196:199], a[28:31]\n\t"
"s_add_u32 m0, s24, 34560\n\t"
"s_add_u32 s28, s21, 986112\n\t"
"v_mfma_f32_16x16x32_bf16 a[28:31], a[248:251], a[192:195], a[28:31]\n\t"
"buffer_load_dwordx4 v247, s[12:15], s28 offen offset:256 nt lds\n\t"
"v_mfma_f32_16x16x32_bf16 a[36:39], a[248:251], a[196:199], a[36:39]\n\t"
"s_waitcnt lgkmcnt(3)\n\t"
"v_mfma_f32_16x16x32_bf16 a[36:39], a[252:255], a[200:203], a[36:39]\n\t"
"s_waitcnt lgkmcnt(2)\n\t"
"v_mfma_f32_16x16x32_bf16 a[36:39], v[96:99], a[204:207], a[36:39]\n\t"
"s_waitcnt lgkmcnt(1)\n\t"
"v_mfma_f32_4x4x4_16b_bf16 v[224:227], v[224:225], v[220:221], 0\n\t"
"s_waitcnt lgkmcnt(0)\n\t"
"v_mfma_f32_4x4x4_16b_bf16 v[232:235], v[232:233], v[220:221], 0\n\t"
"s_waitcnt vmcnt(17)\n\t"
"v_mfma_f32_4x4x4_16b_bf16 v[236:239], v[236:237], v[220:221], 0\n\t"
"v_mfma_f32_4x4x4_16b_bf16 v[228:231], v[228:229], v[220:221], 0\n\t"
"v_mfma_f32_16x16x32_bf16 a[44:47], a[232:235], a[168:171], a[44:47]\n\t"
"v_cvt_pk_bf16_f32 v224, v224, v225\n\t"
"v_cvt_pk_bf16_f32 v225, v226, v227\n\t"
"v_mfma_f32_16x16x32_bf16 a[44:47], v[248:251], a[176:179], a[44:47]\n\t"
"v_cvt_pk_bf16_f32 v226, v232, v233\n\t"
"v_cvt_pk_bf16_f32 v227, v234, v235\n\t"
"v_mfma_f32_16x16x32_bf16 a[44:47], a[228:231], a[172:175], a[44:47]\n\t"
"ds_write_b128 v245, v[224:227] offset:512\n\t"
"v_mfma_f32_16x16x32_bf16 a[52:55], a[232:235], a[172:175], a[52:55]\n\t"
"v_cvt_pk_bf16_f32 v236, v236, v237\n\t"
"v_cvt_pk_bf16_f32 v237, v238, v239\n\t"
"v_mfma_f32_16x16x32_bf16 a[52:55], a[228:231], a[176:179], a[52:55]\n\t"
"v_cvt_pk_bf16_f32 v238, v228, v229\n\t"
"v_cvt_pk_bf16_f32 v239, v230, v231\n\t"
"v_mfma_f32_16x16x32_bf16 a[52:55], v[248:251], a[180:183], a[52:55]\n\t"
"ds_write_b128 v245, v[236:239] offset:6656\n\t"
"v_mfma_f32_16x16x32_bf16 a[60:63], v[248:251], a[184:187], a[60:63]\n\t"
"v_mfma_f32_16x16x32_bf16 a[60:63], a[228:231], a[180:183], a[60:63]\n\t"
"v_mfma_f32_16x16x32_bf16 a[60:63], a[232:235], a[176:179], a[60:63]\n\t"
"ds_read_b64_tr_b16 v[232:233], v246 offset:12800\n\t"
"v_mfma_f32_16x16x32_bf16 a[68:71], a[232:235], a[180:183], a[68:71]\n\t"
"ds_read_b64_tr_b16 v[228:229], v255 offset:12800\n\t"
"v_mfma_f32_16x16x32_bf16 a[68:71], a[228:231], a[184:187], a[68:71]\n\t"
"ds_read_b64_tr_b16 v[224:225], v246 offset:20480\n\t"
"v_mfma_f32_16x16x32_bf16 a[68:71], v[248:251], a[188:191], a[68:71]\n\t"
"ds_read_b64_tr_b16 v[236:237], v255 offset:20480\n\t"
"v_mfma_f32_16x16x32_bf16 a[76:79], v[248:251], a[192:195], a[76:79]\n\t"
"v_mfma_f32_16x16x32_bf16 a[76:79], a[228:231], a[188:191], a[76:79]\n\t"
"v_mfma_f32_16x16x32_bf16 a[76:79], a[232:235], a[184:187], a[76:79]\n\t"
"v_mfma_f32_16x16x32_bf16 a[84:87], a[232:235], a[188:191], a[84:87]\n\t"
"v_mfma_f32_16x16x32_bf16 a[84:87], a[228:231], a[192:195], a[84:87]\n\t"
"v_mfma_f32_16x16x32_bf16 a[84:87], v[248:251], a[196:199], a[84:87]\n\t"
"v_mfma_f32_16x16x32_bf16 a[92:95], v[248:251], a[200:203], a[92:95]\n\t"
"v_mfma_f32_16x16x32_bf16 a[92:95], a[228:231], a[196:199], a[92:95]\n\t"
"v_mfma_f32_16x16x32_bf16 a[92:95], a[232:235], a[192:195], a[92:95]\n\t"
"v_mfma_f32_16x16x32_bf16 a[100:103], a[232:235], a[196:199], a[100:103]\n\t"
"s_waitcnt lgkmcnt(3)\n\t"
"v_mfma_f32_16x16x32_bf16 a[100:103], a[228:231], a[200:203], a[100:103]\n\t"
"s_waitcnt lgkmcnt(2)\n\t"
"v_mfma_f32_16x16x32_bf16 a[100:103], v[248:251], a[204:207], a[100:103]\n\t"
"s_waitcnt lgkmcnt(1)\n\t"
"v_mfma_f32_4x4x4_16b_bf16 v[232:235], v[232:233], v[220:221], 0\n\t"
"s_waitcnt lgkmcnt(0)\n\t"
"v_mfma_f32_4x4x4_16b_bf16 v[228:231], v[228:229], v[220:221], 0\n\t"
"s_waitcnt vmcnt(14)\n\t"
"v_mfma_f32_4x4x4_16b_bf16 v[224:227], v[224:225], v[220:221], 0\n\t"
"s_mov_b32 s20, 651264\n\t"
"v_mfma_f32_4x4x4_16b_bf16 v[236:239], v[236:237], v[220:221], 0\n\t"
"v_mfma_f32_16x16x32_bf16 a[108:111], a[212:215], a[168:171], a[108:111]\n\t"
"ds_read_b128 a[168:171], v222 offset:1536\n\t"
"v_cvt_pk_bf16_f32 v232, v232, v233\n\t"
"v_mfma_f32_16x16x32_bf16 a[108:111], a[220:223], a[176:179], a[108:111]\n\t"
"v_cvt_pk_bf16_f32 v233, v234, v235\n\t"
"v_cvt_pk_bf16_f32 v234, v228, v229\n\t"
"v_mfma_f32_16x16x32_bf16 a[108:111], a[216:219], a[172:175], a[108:111]\n\t"
"v_cvt_pk_bf16_f32 v235, v230, v231\n\t"
"v_cvt_pk_bf16_f32 v224, v224, v225\n\t"
"v_mfma_f32_16x16x32_bf16 a[116:119], a[212:215], a[172:175], a[116:119]\n\t"
"ds_read_b128 a[172:175], v223 offset:7680\n\t"
"v_cvt_pk_bf16_f32 v225, v226, v227\n\t"
"v_mfma_f32_16x16x32_bf16 a[116:119], a[216:219], a[176:179], a[116:119]\n\t"
"v_cvt_pk_bf16_f32 v226, v236, v237\n\t"
"v_cvt_pk_bf16_f32 v227, v238, v239\n\t"
"v_mfma_f32_16x16x32_bf16 a[116:119], a[220:223], a[180:183], a[116:119]\n\t"
"ds_write_b128 v245, v[232:235] offset:12800\n\t"
"v_mfma_f32_16x16x32_bf16 a[124:127], a[220:223], a[184:187], a[124:127]\n\t"
"buffer_load_dwordx4 v[96:99], off, s[80:83], s20 offset:0 sc1\n\t"
"v_mfma_f32_16x16x32_bf16 a[124:127], a[216:219], a[180:183], a[124:127]\n\t"
"v_mfma_f32_16x16x32_bf16 a[124:127], a[212:215], a[176:179], a[124:127]\n\t"
"ds_write_b128 v245, v[224:227] offset:20480\n\t"
"v_mfma_f32_16x16x32_bf16 a[132:135], a[212:215], a[180:183], a[132:135]\n\t"
"v_mfma_f32_16x16x32_bf16 a[132:135], a[216:219], a[184:187], a[132:135]\n\t"
"ds_read_b128 a[176:179], v223 offset:13824\n\t"
"v_mfma_f32_16x16x32_bf16 a[132:135], a[220:223], a[188:191], a[132:135]\n\t"
"ds_read_b64_tr_b16 v[228:229], v246 offset:26624\n\t"
"v_mfma_f32_16x16x32_bf16 a[140:143], a[220:223], a[192:195], a[140:143]\n\t"
"ds_read_b64_tr_b16 v[236:237], v255 offset:26624\n\t"
"v_mfma_f32_16x16x32_bf16 a[140:143], a[216:219], a[188:191], a[140:143]\n\t"
"ds_read_b128 a[180:183], v222 offset:21504\n\t"
"v_mfma_f32_16x16x32_bf16 a[140:143], a[212:215], a[184:187], a[140:143]\n\t"
"ds_read_b64_tr_b16 v[232:233], v246 offset:32768\n\t"
"v_mfma_f32_16x16x32_bf16 a[148:151], a[212:215], a[188:191], a[148:151]\n\t"
"ds_read_b64_tr_b16 v[224:225], v255 offset:32768\n\t"
"v_mfma_f32_16x16x32_bf16 a[148:151], a[216:219], a[192:195], a[148:151]\n\t"
"ds_read_b128 a[184:187], v223 offset:27648\n\t"
"v_mfma_f32_16x16x32_bf16 a[148:151], a[220:223], a[196:199], a[148:151]\n\t"
"ds_read_b128 a[188:191], v223 offset:33792\n\t"
"v_mfma_f32_16x16x32_bf16 a[156:159], a[220:223], a[200:203], a[156:159]\n\t"
"v_mfma_f32_16x16x32_bf16 a[156:159], a[216:219], a[196:199], a[156:159]\n\t"
"v_mfma_f32_16x16x32_bf16 a[156:159], a[212:215], a[192:195], a[156:159]\n\t"
"ds_read_b128 a[192:195], v222 offset:41472\n\t"
"v_mfma_f32_16x16x32_bf16 a[164:167], a[212:215], a[196:199], a[164:167]\n\t"
"s_waitcnt lgkmcnt(7)\n\t"
"ds_read_b128 a[196:199], v223 offset:47616\n\t"
"v_mfma_f32_16x16x32_bf16 a[164:167], a[216:219], a[200:203], a[164:167]\n\t"
"s_waitcnt lgkmcnt(7)\n\t"
"ds_read_b128 a[200:203], v223 offset:53760\n\t"
"v_mfma_f32_16x16x32_bf16 a[164:167], a[220:223], a[204:207], a[164:167]\n\t"
"s_waitcnt lgkmcnt(6)\n\t"
"ds_read_b128 a[204:207], v222 offset:61440\n\t"
"v_mfma_f32_4x4x4_16b_bf16 v[228:231], v[228:229], v[220:221], 0\n\t"
"s_waitcnt lgkmcnt(6)\n\t"
"v_mfma_f32_4x4x4_16b_bf16 v[236:239], v[236:237], v[220:221], 0\n\t"
"s_waitcnt vmcnt(12)\n\t"
"v_mfma_f32_4x4x4_16b_bf16 v[232:235], v[232:233], v[220:221], 0\n\t"
"v_mfma_f32_4x4x4_16b_bf16 v[224:227], v[224:225], v[220:221], 0\n\t"
"Lst25_%=:\n\t"
"v_mfma_f32_16x16x32_bf16 v[0:3], a[224:227], a[168:171], v[0:3]\n\t"
"v_cvt_pk_bf16_f32 v228, v228, v229\n\t"
"v_cvt_pk_bf16_f32 v229, v230, v231\n\t"
"v_mfma_f32_16x16x32_bf16 v[0:3], v[240:243], a[176:179], v[0:3]\n\t"
"v_cvt_pk_bf16_f32 v230, v236, v237\n\t"
"v_cvt_pk_bf16_f32 v231, v238, v239\n\t"
"v_mfma_f32_16x16x32_bf16 v[0:3], a[208:211], a[172:175], v[0:3]\n\t"
"ds_write_b128 v245, v[228:231] offset:26624\n\t"
"v_mfma_f32_16x16x32_bf16 v[4:7], a[224:227], a[172:175], v[4:7]\n\t"
"s_waitcnt lgkmcnt(6)\n\t"
"v_cvt_pk_bf16_f32 v232, v232, v233\n\t"
"v_cvt_pk_bf16_f32 v233, v234, v235\n\t"
"v_mfma_f32_16x16x32_bf16 v[4:7], a[208:211], a[176:179], v[4:7]\n\t"
"v_cvt_pk_bf16_f32 v234, v224, v225\n\t"
"v_cvt_pk_bf16_f32 v235, v226, v227\n\t"
"v_mfma_f32_16x16x32_bf16 v[4:7], v[240:243], a[180:183], v[4:7]\n\t"
"ds_write_b128 v245, v[232:235] offset:32768\n\t"
"v_mfma_f32_16x16x32_bf16 v[8:11], v[240:243], a[184:187], v[8:11]\n\t"
"s_waitcnt lgkmcnt(6)\n\t"
"v_mfma_f32_16x16x32_bf16 v[8:11], a[208:211], a[180:183], v[8:11]\n\t"
"v_mfma_f32_16x16x32_bf16 v[8:11], a[224:227], a[176:179], v[8:11]\n\t"
"ds_read_b64_tr_b16 v[236:237], v246 offset:40448\n\t"
"v_mfma_f32_16x16x32_bf16 v[12:15], a[224:227], a[180:183], v[12:15]\n\t"
"s_waitcnt lgkmcnt(6)\n\t"
"ds_read_b64_tr_b16 v[224:225], v255 offset:40448\n\t"
"v_mfma_f32_16x16x32_bf16 v[12:15], a[208:211], a[184:187], v[12:15]\n\t"
"ds_read_b64_tr_b16 v[228:229], v246 offset:46592\n\t"
"v_mfma_f32_16x16x32_bf16 v[12:15], v[240:243], a[188:191], v[12:15]\n\t"
"buffer_load_dwordx4 v[248:251], off, s[80:83], s20 offset:1024 sc1\n\t"
"v_mfma_f32_16x16x32_bf16 v[16:19], v[240:243], a[192:195], v[16:19]\n\t"
"s_waitcnt lgkmcnt(7)\n\t"
"ds_read_b64_tr_b16 v[232:233], v255 offset:46592\n\t"
"v_mfma_f32_16x16x32_bf16 v[16:19], a[208:211], a[188:191], v[16:19]\n\t"
"v_mfma_f32_16x16x32_bf16 v[16:19], a[224:227], a[184:187], v[16:19]\n\t"
"buffer_load_dwordx4 a[212:215], off, s[80:83], s20 offset:2048 sc1\n\t"
"v_mfma_f32_16x16x32_bf16 v[20:23], a[224:227], a[188:191], v[20:23]\n\t"
"s_waitcnt lgkmcnt(7)\n\t"
"s_mov_b32 s20, 663552\n\t"
"v_mfma_f32_16x16x32_bf16 v[20:23], a[208:211], a[192:195], v[20:23]\n\t"
"v_mfma_f32_16x16x32_bf16 v[20:23], v[240:243], a[196:199], v[20:23]\n\t"
"v_mfma_f32_16x16x32_bf16 v[24:27], v[240:243], a[200:203], v[24:27]\n\t"
"buffer_load_dwordx4 a[216:219], off, s[80:83], s20 offset:0 sc1\n\t"
"v_mfma_f32_16x16x32_bf16 v[24:27], a[208:211], a[196:199], v[24:27]\n\t"
"s_waitcnt lgkmcnt(6)\n\t"
"v_mfma_f32_16x16x32_bf16 v[24:27], a[224:227], a[192:195], v[24:27]\n\t"
"v_mfma_f32_16x16x32_bf16 v[28:31], a[224:227], a[196:199], v[28:31]\n\t"
"s_waitcnt lgkmcnt(3)\n\t"
"v_mfma_f32_16x16x32_bf16 v[28:31], a[208:211], a[200:203], v[28:31]\n\t"
"buffer_load_dwordx4 a[208:211], off, s[80:83], s20 offset:1024 sc1\n\t"
"v_mfma_f32_16x16x32_bf16 v[28:31], v[240:243], a[204:207], v[28:31]\n\t"
"s_waitcnt lgkmcnt(1)\n\t"
"v_mfma_f32_4x4x4_16b_bf16 v[236:239], v[236:237], v[220:221], 0\n\t"
"s_waitcnt lgkmcnt(0)\n\t"
"v_mfma_f32_4x4x4_16b_bf16 v[224:227], v[224:225], v[220:221], 0\n\t"
"s_waitcnt vmcnt(13)\n\t"
"v_mfma_f32_4x4x4_16b_bf16 v[228:231], v[228:229], v[220:221], 0\n\t"
"v_mfma_f32_4x4x4_16b_bf16 v[232:235], v[232:233], v[220:221], 0\n\t"
"v_mfma_f32_16x16x32_bf16 v[32:35], a[236:239], a[168:171], v[32:35]\n\t"
"buffer_load_dwordx4 v[240:243], off, s[80:83], s20 offset:2048 sc1\n\t"
"v_mfma_f32_16x16x32_bf16 v[32:35], a[244:247], a[176:179], v[32:35]\n\t"
"s_mov_b32 s20, 675840\n\t"
"v_cvt_pk_bf16_f32 v236, v236, v237\n\t"
"v_cvt_pk_bf16_f32 v237, v238, v239\n\t"
"v_mfma_f32_16x16x32_bf16 v[32:35], a[240:243], a[172:175], v[32:35]\n\t"
"v_cvt_pk_bf16_f32 v238, v224, v225\n\t"
"v_cvt_pk_bf16_f32 v239, v226, v227\n\t"
"v_mfma_f32_16x16x32_bf16 v[36:39], a[236:239], a[172:175], v[36:39]\n\t"
"v_cvt_pk_bf16_f32 v228, v228, v229\n\t"
"v_cvt_pk_bf16_f32 v229, v230, v231\n\t"
"v_mfma_f32_16x16x32_bf16 v[36:39], a[240:243], a[176:179], v[36:39]\n\t"
"v_cvt_pk_bf16_f32 v230, v232, v233\n\t"
"v_cvt_pk_bf16_f32 v231, v234, v235\n\t"
"v_mfma_f32_16x16x32_bf16 v[36:39], a[244:247], a[180:183], v[36:39]\n\t"
"buffer_load_dwordx4 a[220:223], off, s[80:83], s20 offset:0 sc1\n\t"
"v_mfma_f32_16x16x32_bf16 v[40:43], a[244:247], a[184:187], v[40:43]\n\t"
"buffer_load_dwordx4 a[224:227], off, s[80:83], s20 offset:1024 sc1\n\t"
"v_mfma_f32_16x16x32_bf16 v[40:43], a[240:243], a[180:183], v[40:43]\n\t"
"v_mfma_f32_16x16x32_bf16 v[40:43], a[236:239], a[176:179], v[40:43]\n\t"
"ds_write_b128 v245, v[236:239] offset:40448\n\t"
"v_mfma_f32_16x16x32_bf16 v[44:47], a[236:239], a[180:183], v[44:47]\n\t"
"v_mfma_f32_16x16x32_bf16 v[44:47], a[240:243], a[184:187], v[44:47]\n\t"
"buffer_load_dwordx4 v[236:239], off, s[80:83], s20 offset:2048 sc1\n\t"
"v_mfma_f32_16x16x32_bf16 v[44:47], a[244:247], a[188:191], v[44:47]\n\t"
"s_mov_b32 s20, 688128\n\t"
"ds_write_b128 v245, v[228:231] offset:46592\n\t"
"v_mfma_f32_16x16x32_bf16 v[48:51], a[244:247], a[192:195], v[48:51]\n\t"
"v_mfma_f32_16x16x32_bf16 v[48:51], a[240:243], a[188:191], v[48:51]\n\t"
"ds_read_b64_tr_b16 v[224:225], v246 offset:52736\n\t"
"v_mfma_f32_16x16x32_bf16 v[48:51], a[236:239], a[184:187], v[48:51]\n\t"
"ds_read_b64_tr_b16 v[232:233], v255 offset:52736\n\t"
"v_mfma_f32_16x16x32_bf16 v[52:55], a[236:239], a[188:191], v[52:55]\n\t"
"buffer_load_dwordx4 a[228:231], off, s[80:83], s20 offset:0 sc1\n\t"
"v_mfma_f32_16x16x32_bf16 v[52:55], a[240:243], a[192:195], v[52:55]\n\t"
"v_mfma_f32_16x16x32_bf16 v[52:55], a[244:247], a[196:199], v[52:55]\n\t"
"v_mfma_f32_16x16x32_bf16 v[56:59], a[244:247], a[200:203], v[56:59]\n\t"
"buffer_load_dwordx4 a[232:235], off, s[80:83], s20 offset:1024 sc1\n\t"
"v_mfma_f32_16x16x32_bf16 v[56:59], a[240:243], a[196:199], v[56:59]\n\t"
"v_mfma_f32_16x16x32_bf16 v[56:59], a[236:239], a[192:195], v[56:59]\n\t"
"v_mfma_f32_16x16x32_bf16 v[60:63], a[236:239], a[196:199], v[60:63]\n\t"
"s_waitcnt lgkmcnt(1)\n\t"
"v_mfma_f32_16x16x32_bf16 v[60:63], a[240:243], a[200:203], v[60:63]\n\t"
"buffer_load_dwordx4 a[236:239], off, s[80:83], s20 offset:2048 sc1\n\t"
"v_mfma_f32_16x16x32_bf16 v[60:63], a[244:247], a[204:207], v[60:63]\n\t"
"s_waitcnt vmcnt(9) lgkmcnt(0)\n\t"
"s_mov_b32 s20, 921600\n\t"
"v_mfma_f32_4x4x4_16b_bf16 v[224:227], v[224:225], v[220:221], 0\n\t"
"v_mfma_f32_4x4x4_16b_bf16 v[232:235], v[232:233], v[220:221], 0\n\t"
"v_mfma_f32_16x16x32_bf16 v[64:67], v[96:99], a[168:171], v[64:67]\n\t"
"buffer_load_dwordx4 a[240:243], off, s[80:83], s20 offset:0 sc1\n\t"
"v_mfma_f32_16x16x32_bf16 v[64:67], a[212:215], a[176:179], v[64:67]\n\t"
"ds_read_b128 a[168:171], v222 offset:1792\n\t"
"v_cvt_pk_bf16_f32 v224, v224, v225\n\t"
"v_mfma_f32_16x16x32_bf16 v[64:67], v[248:251], a[172:175], v[64:67]\n\t"
"v_cvt_pk_bf16_f32 v225, v226, v227\n\t"
"v_cvt_pk_bf16_f32 v226, v232, v233\n\t"
"v_mfma_f32_16x16x32_bf16 v[68:71], v[96:99], a[172:175], v[68:71]\n\t"
"ds_read_b128 a[172:175], v223 offset:7936\n\t"
"v_cvt_pk_bf16_f32 v227, v234, v235\n\t"
"v_mfma_f32_16x16x32_bf16 v[68:71], v[248:251], a[176:179], v[68:71]\n\t"
"v_mfma_f32_16x16x32_bf16 v[68:71], a[212:215], a[180:183], v[68:71]\n\t"
"buffer_load_dwordx4 a[244:247], off, s[80:83], s20 offset:1024 sc1\n\t"
"v_mfma_f32_16x16x32_bf16 v[72:75], a[212:215], a[184:187], v[72:75]\n\t"
"buffer_load_dwordx4 a[248:251], off, s[80:83], s20 offset:2048 sc1\n\t"
"v_mfma_f32_16x16x32_bf16 v[72:75], v[248:251], a[180:183], v[72:75]\n\t"
"s_mov_b32 s20, 933888\n\t"
"v_mfma_f32_16x16x32_bf16 v[72:75], v[96:99], a[176:179], v[72:75]\n\t"
"ds_write_b128 v245, v[224:227] offset:52736\n\t"
"v_mfma_f32_16x16x32_bf16 v[76:79], v[96:99], a[180:183], v[76:79]\n\t"
"buffer_load_dwordx4 a[252:255], off, s[80:83], s20 offset:0 sc1\n\t"
"v_mfma_f32_16x16x32_bf16 v[76:79], v[248:251], a[184:187], v[76:79]\n\t"
"ds_read_b128 a[176:179], v223 offset:14080\n\t"
"v_mfma_f32_16x16x32_bf16 v[76:79], a[212:215], a[188:191], v[76:79]\n\t"
"ds_read_b128 a[180:183], v222 offset:21760\n\t"
"v_mfma_f32_16x16x32_bf16 v[80:83], a[212:215], a[192:195], v[80:83]\n\t"
"v_mfma_f32_16x16x32_bf16 v[80:83], v[248:251], a[188:191], v[80:83]\n\t"
"v_mfma_f32_16x16x32_bf16 v[80:83], v[96:99], a[184:187], v[80:83]\n\t"
"ds_read_b128 a[184:187], v223 offset:27904\n\t"
"v_mfma_f32_16x16x32_bf16 v[84:87], v[96:99], a[188:191], v[84:87]\n\t"
"ds_read_b128 a[188:191], v223 offset:34048\n\t"
"v_mfma_f32_16x16x32_bf16 v[84:87], v[248:251], a[192:195], v[84:87]\n\t"
"v_mfma_f32_16x16x32_bf16 v[84:87], a[212:215], a[196:199], v[84:87]\n\t"
"v_mfma_f32_16x16x32_bf16 v[88:91], a[212:215], a[200:203], v[88:91]\n\t"
"v_mfma_f32_16x16x32_bf16 v[88:91], v[248:251], a[196:199], v[88:91]\n\t"
"v_mfma_f32_16x16x32_bf16 v[88:91], v[96:99], a[192:195], v[88:91]\n\t"
"ds_read_b128 a[192:195], v222 offset:41728\n\t"
"v_mfma_f32_16x16x32_bf16 v[92:95], v[96:99], a[196:199], v[92:95]\n\t"
"buffer_load_dwordx4 v[96:99], off, s[80:83], s20 offset:1024 sc1\n\t"
"v_mfma_f32_16x16x32_bf16 v[92:95], v[248:251], a[200:203], v[92:95]\n\t"
"s_waitcnt vmcnt(11) lgkmcnt(4)\n\t"
"ds_read_b128 a[196:199], v223 offset:47872\n\t"
"v_mfma_f32_16x16x32_bf16 v[92:95], a[212:215], a[204:207], v[92:95]\n\t"
"ds_read_b128 a[200:203], v223 offset:54016\n\t"
"v_mfma_f32_16x16x32_bf16 v[100:103], a[216:219], a[168:171], v[100:103]\n\t"
"s_waitcnt lgkmcnt(5)\n\t"
"ds_read_b128 a[204:207], v222 offset:61696\n\t"
"v_mfma_f32_16x16x32_bf16 v[100:103], v[240:243], a[176:179], v[100:103]\n\t"
"buffer_load_dwordx4 v[248:251], off, s[80:83], s20 offset:2048 sc1\n\t"
"v_mfma_f32_16x16x32_bf16 v[100:103], a[208:211], a[172:175], v[100:103]\n\t"
"s_mov_b32 s20, 946176\n\t"
"s_add_u32 m0, s24, 768\n\t"
"s_add_u32 s28, s21, 0\n\t"
"v_mfma_f32_16x16x32_bf16 v[104:107], a[216:219], a[172:175], v[104:107]\n\t"
"buffer_load_dwordx4 a[212:215], off, s[80:83], s20 offset:0 sc1\n\t"
"v_mfma_f32_16x16x32_bf16 v[104:107], a[208:211], a[176:179], v[104:107]\n\t"
"s_waitcnt lgkmcnt(5)\n\t"
"v_mfma_f32_16x16x32_bf16 v[104:107], v[240:243], a[180:183], v[104:107]\n\t"
"s_waitcnt lgkmcnt(0)\n\t"
"s_barrier\n\t"
"v_mfma_f32_16x16x32_bf16 v[108:111], v[240:243], a[184:187], v[108:111]\n\t"
"v_mfma_f32_16x16x32_bf16 v[108:111], a[208:211], a[180:183], v[108:111]\n\t"
"buffer_load_dwordx4 v247, s[12:15], s28 offen offset:256 nt lds\n\t"
"v_mfma_f32_16x16x32_bf16 v[108:111], a[216:219], a[176:179], v[108:111]\n\t"
"s_add_u32 m0, s24, 6912\n\t"
"s_add_u32 s28, s21, 196608\n\t"
"v_mfma_f32_16x16x32_bf16 v[112:115], a[216:219], a[180:183], v[112:115]\n\t"
"buffer_load_dwordx4 v247, s[12:15], s28 offen offset:256 nt lds\n\t"
"v_mfma_f32_16x16x32_bf16 v[112:115], a[208:211], a[184:187], v[112:115]\n\t"
"s_add_u32 m0, s24, 13056\n\t"
"s_add_u32 s28, s21, 393216\n\t"
"v_mfma_f32_16x16x32_bf16 v[112:115], v[240:243], a[188:191], v[112:115]\n\t"
"buffer_load_dwordx4 v247, s[12:15], s28 offen offset:256 nt lds\n\t"
"s_add_u32 m0, s24, 20736\n\t"
"s_add_u32 s28, s21, 589824\n\t"
"v_mfma_f32_16x16x32_bf16 v[116:119], v[240:243], a[192:195], v[116:119]\n\t"
"buffer_load_dwordx4 v247, s[12:15], s28 offen offset:256 nt lds\n\t"
"v_mfma_f32_16x16x32_bf16 v[116:119], a[208:211], a[188:191], v[116:119]\n\t"
"s_add_u32 m0, s24, 26880\n\t"
"s_add_u32 s28, s21, 786432\n\t"
"v_mfma_f32_16x16x32_bf16 v[116:119], a[216:219], a[184:187], v[116:119]\n\t"
"buffer_load_dwordx4 v247, s[12:15], s28 offen offset:256 nt lds\n\t"
"v_mfma_f32_16x16x32_bf16 v[120:123], a[216:219], a[188:191], v[120:123]\n\t"
"s_add_u32 m0, s24, 33024\n\t"
"s_add_u32 s28, s21, 983040\n\t"
"v_mfma_f32_16x16x32_bf16 v[120:123], a[208:211], a[192:195], v[120:123]\n\t"
"buffer_load_dwordx4 v247, s[12:15], s28 offen offset:256 nt lds\n\t"
"v_mfma_f32_16x16x32_bf16 v[120:123], v[240:243], a[196:199], v[120:123]\n\t"
"v_mfma_f32_16x16x32_bf16 v[124:127], v[240:243], a[200:203], v[124:127]\n\t"
"v_mfma_f32_16x16x32_bf16 v[124:127], a[208:211], a[196:199], v[124:127]\n\t"
"v_mfma_f32_16x16x32_bf16 v[124:127], a[216:219], a[192:195], v[124:127]\n\t"
"v_mfma_f32_16x16x32_bf16 v[128:131], a[216:219], a[196:199], v[128:131]\n\t"
"s_waitcnt vmcnt(16)\n\t"
"v_mfma_f32_16x16x32_bf16 v[128:131], a[208:211], a[200:203], v[128:131]\n\t"
"v_mfma_f32_16x16x32_bf16 v[128:131], v[240:243], a[204:207], v[128:131]\n\t"
"v_mfma_f32_16x16x32_bf16 v[132:135], a[220:223], a[168:171], v[132:135]\n\t"
"v_mfma_f32_16x16x32_bf16 v[132:135], v[236:239], a[176:179], v[132:135]\n\t"
"v_mfma_f32_16x16x32_bf16 v[132:135], a[224:227], a[172:175], v[132:135]\n\t"
"v_mfma_f32_16x16x32_bf16 v[136:139], a[220:223], a[172:175], v[136:139]\n\t"
"v_mfma_f32_16x16x32_bf16 v[136:139], a[224:227], a[176:179], v[136:139]\n\t"
"v_mfma_f32_16x16x32_bf16 v[136:139], v[236:239], a[180:183], v[136:139]\n\t"
"v_mfma_f32_16x16x32_bf16 v[140:143], v[236:239], a[184:187], v[140:143]\n\t"
"v_mfma_f32_16x16x32_bf16 v[140:143], a[224:227], a[180:183], v[140:143]\n\t"
"v_mfma_f32_16x16x32_bf16 v[140:143], a[220:223], a[176:179], v[140:143]\n\t"
"v_mfma_f32_16x16x32_bf16 v[144:147], a[220:223], a[180:183], v[144:147]\n\t"
"v_mfma_f32_16x16x32_bf16 v[144:147], a[224:227], a[184:187], v[144:147]\n\t"
"v_mfma_f32_16x16x32_bf16 v[144:147], v[236:239], a[188:191], v[144:147]\n\t"
"v_mfma_f32_16x16x32_bf16 v[148:151], v[236:239], a[192:195], v[148:151]\n\t"
"v_mfma_f32_16x16x32_bf16 v[148:151], a[224:227], a[188:191], v[148:151]\n\t"
"v_mfma_f32_16x16x32_bf16 v[148:151], a[220:223], a[184:187], v[148:151]\n\t"
"v_mfma_f32_16x16x32_bf16 v[152:155], a[220:223], a[188:191], v[152:155]\n\t"
"v_mfma_f32_16x16x32_bf16 v[152:155], a[224:227], a[192:195], v[152:155]\n\t"
"v_mfma_f32_16x16x32_bf16 v[152:155], v[236:239], a[196:199], v[152:155]\n\t"
"v_mfma_f32_16x16x32_bf16 v[156:159], v[236:239], a[200:203], v[156:159]\n\t"
"v_mfma_f32_16x16x32_bf16 v[156:159], a[224:227], a[196:199], v[156:159]\n\t"
"v_mfma_f32_16x16x32_bf16 v[156:159], a[220:223], a[192:195], v[156:159]\n\t"
"v_mfma_f32_16x16x32_bf16 v[160:163], a[220:223], a[196:199], v[160:163]\n\t"
"s_waitcnt vmcnt(13)\n\t"
"v_mfma_f32_16x16x32_bf16 v[160:163], a[224:227], a[200:203], v[160:163]\n\t"
"v_mfma_f32_16x16x32_bf16 v[160:163], v[236:239], a[204:207], v[160:163]\n\t"
"v_mfma_f32_16x16x32_bf16 v[164:167], a[228:231], a[168:171], v[164:167]\n\t"
"ds_read_b128 a[168:171], v222 offset:2048\n\t"
"v_mfma_f32_16x16x32_bf16 v[164:167], a[236:239], a[176:179], v[164:167]\n\t"
"v_mfma_f32_16x16x32_bf16 v[164:167], a[232:235], a[172:175], v[164:167]\n\t"
"buffer_load_dwordx4 v[240:243], off, s[80:83], s20 offset:1024 sc1\n\t"
"v_mfma_f32_16x16x32_bf16 v[168:171], a[228:231], a[172:175], v[168:171]\n\t"
"ds_read_b128 a[172:175], v223 offset:8192\n\t"
"v_mfma_f32_16x16x32_bf16 v[168:171], a[232:235], a[176:179], v[168:171]\n\t"
"v_mfma_f32_16x16x32_bf16 v[168:171], a[236:239], a[180:183], v[168:171]\n\t"
"v_mfma_f32_16x16x32_bf16 v[172:175], a[236:239], a[184:187], v[172:175]\n\t"
"v_mfma_f32_16x16x32_bf16 v[172:175], a[232:235], a[180:183], v[172:175]\n\t"
"v_mfma_f32_16x16x32_bf16 v[172:175], a[228:231], a[176:179], v[172:175]\n\t"
"ds_read_b128 a[176:179], v223 offset:14336\n\t"
"v_mfma_f32_16x16x32_bf16 v[176:179], a[228:231], a[180:183], v[176:179]\n\t"
"ds_read_b128 a[180:183], v222 offset:22016\n\t"
"v_mfma_f32_16x16x32_bf16 v[176:179], a[232:235], a[184:187], v[176:179]\n\t"
"v_mfma_f32_16x16x32_bf16 v[176:179], a[236:239], a[188:191], v[176:179]\n\t"
"v_mfma_f32_16x16x32_bf16 v[180:183], a[236:239], a[192:195], v[180:183]\n\t"
"v_mfma_f32_16x16x32_bf16 v[180:183], a[232:235], a[188:191], v[180:183]\n\t"
"v_mfma_f32_16x16x32_bf16 v[180:183], a[228:231], a[184:187], v[180:183]\n\t"
"ds_read_b128 a[184:187], v223 offset:28160\n\t"
"v_mfma_f32_16x16x32_bf16 v[184:187], a[228:231], a[188:191], v[184:187]\n\t"
"ds_read_b128 a[188:191], v223 offset:34304\n\t"
"v_mfma_f32_16x16x32_bf16 v[184:187], a[232:235], a[192:195], v[184:187]\n\t"
"v_mfma_f32_16x16x32_bf16 v[184:187], a[236:239], a[196:199], v[184:187]\n\t"
"v_mfma_f32_16x16x32_bf16 v[188:191], a[236:239], a[200:203], v[188:191]\n\t"
"v_mfma_f32_16x16x32_bf16 v[188:191], a[232:235], a[196:199], v[188:191]\n\t"
"v_mfma_f32_16x16x32_bf16 v[188:191], a[228:231], a[192:195], v[188:191]\n\t"
"ds_read_b128 a[192:195], v222 offset:41984\n\t"
"v_mfma_f32_16x16x32_bf16 v[192:195], a[228:231], a[196:199], v[192:195]\n\t"
"s_waitcnt vmcnt(11) lgkmcnt(4)\n\t"
"ds_read_b128 a[196:199], v223 offset:48128\n\t"
"v_mfma_f32_16x16x32_bf16 v[192:195], a[232:235], a[200:203], v[192:195]\n\t"
"ds_read_b128 a[200:203], v223 offset:54272\n\t"
"v_mfma_f32_16x16x32_bf16 v[192:195], a[236:239], a[204:207], v[192:195]\n\t"
"ds_read_b128 a[204:207], v222 offset:61952\n\t"
"Lst26_%=:\n\t"
"v_mfma_f32_16x16x32_bf16 v[0:3], a[240:243], a[168:171], v[0:3]\n\t"
"s_waitcnt lgkmcnt(6)\n\t"
"s_add_u32 s23, s40, 6291456\n\t"
"s_add_u32 s22, s22, 6291456\n\t"
"v_mfma_f32_16x16x32_bf16 v[0:3], a[248:251], a[176:179], v[0:3]\n\t"
"v_mfma_f32_16x16x32_bf16 v[0:3], a[244:247], a[172:175], v[0:3]\n\t"
"v_mfma_f32_16x16x32_bf16 v[4:7], a[240:243], a[172:175], v[4:7]\n\t"
"s_waitcnt lgkmcnt(5)\n\t"
"v_mfma_f32_16x16x32_bf16 v[4:7], a[244:247], a[176:179], v[4:7]\n\t"
"v_mfma_f32_16x16x32_bf16 v[4:7], a[248:251], a[180:183], v[4:7]\n\t"
"v_mfma_f32_16x16x32_bf16 v[8:11], a[248:251], a[184:187], v[8:11]\n\t"
"s_waitcnt lgkmcnt(4)\n\t"
"v_mfma_f32_16x16x32_bf16 v[8:11], a[244:247], a[180:183], v[8:11]\n\t"
"v_mfma_f32_16x16x32_bf16 v[8:11], a[240:243], a[176:179], v[8:11]\n\t"
"buffer_load_dwordx4 a[208:211], off, s[80:83], s20 offset:2048 sc1\n\t"
"v_mfma_f32_16x16x32_bf16 v[12:15], a[240:243], a[180:183], v[12:15]\n\t"
"s_waitcnt lgkmcnt(3)\n\t"
"s_mov_b32 s20, 958464\n\t"
"v_mfma_f32_16x16x32_bf16 v[12:15], a[244:247], a[184:187], v[12:15]\n\t"
"v_mfma_f32_16x16x32_bf16 v[12:15], a[248:251], a[188:191], v[12:15]\n\t"
"v_mfma_f32_16x16x32_bf16 v[16:19], a[248:251], a[192:195], v[16:19]\n\t"
"buffer_load_dwordx4 v[236:239], off, s[80:83], s20 offset:0 sc1\n\t"
"v_mfma_f32_16x16x32_bf16 v[16:19], a[244:247], a[188:191], v[16:19]\n\t"
"s_waitcnt lgkmcnt(2)\n\t"
"v_mfma_f32_16x16x32_bf16 v[16:19], a[240:243], a[184:187], v[16:19]\n\t"
"v_mfma_f32_16x16x32_bf16 v[20:23], a[240:243], a[188:191], v[20:23]\n\t"
"s_waitcnt lgkmcnt(1)\n\t"
"v_mfma_f32_16x16x32_bf16 v[20:23], a[244:247], a[192:195], v[20:23]\n\t"
"buffer_load_dwordx4 a[216:219], off, s[80:83], s20 offset:1024 sc1\n\t"
"v_mfma_f32_16x16x32_bf16 v[20:23], a[248:251], a[196:199], v[20:23]\n\t"
"v_mfma_f32_16x16x32_bf16 v[24:27], a[248:251], a[200:203], v[24:27]\n\t"
"s_waitcnt lgkmcnt(0)\n\t"
"v_mfma_f32_16x16x32_bf16 v[24:27], a[244:247], a[196:199], v[24:27]\n\t"
"v_mfma_f32_16x16x32_bf16 v[24:27], a[240:243], a[192:195], v[24:27]\n\t"
"buffer_load_dwordx4 a[220:223], off, s[80:83], s20 offset:2048 sc1\n\t"
"v_mfma_f32_16x16x32_bf16 v[28:31], a[240:243], a[196:199], v[28:31]\n\t"
"s_waitcnt vmcnt(12)\n\t"
"s_mov_b32 s20, 970752\n\t"
"v_mfma_f32_16x16x32_bf16 v[28:31], a[244:247], a[200:203], v[28:31]\n\t"
"v_mfma_f32_16x16x32_bf16 v[28:31], a[248:251], a[204:207], v[28:31]\n\t"
"v_mfma_f32_16x16x32_bf16 v[32:35], a[252:255], a[168:171], v[32:35]\n\t"
"buffer_load_dwordx4 a[224:227], off, s[80:83], s20 offset:0 sc1\n\t"
"v_mfma_f32_16x16x32_bf16 v[32:35], v[248:251], a[176:179], v[32:35]\n\t"
"v_mfma_f32_16x16x32_bf16 v[32:35], v[96:99], a[172:175], v[32:35]\n\t"
"v_mfma_f32_16x16x32_bf16 v[36:39], a[252:255], a[172:175], v[36:39]\n\t"
"v_mfma_f32_16x16x32_bf16 v[36:39], v[96:99], a[176:179], v[36:39]\n\t"
"buffer_load_dwordx4 a[228:231], off, s[80:83], s20 offset:1024 sc1\n\t"
"v_mfma_f32_16x16x32_bf16 v[36:39], v[248:251], a[180:183], v[36:39]\n\t"
"v_mfma_f32_16x16x32_bf16 v[40:43], v[248:251], a[184:187], v[40:43]\n\t"
"v_mfma_f32_16x16x32_bf16 v[40:43], v[96:99], a[180:183], v[40:43]\n\t"
"v_mfma_f32_16x16x32_bf16 v[40:43], a[252:255], a[176:179], v[40:43]\n\t"
"buffer_load_dwordx4 a[232:235], off, s[80:83], s20 offset:2048 sc1\n\t"
"v_mfma_f32_16x16x32_bf16 v[44:47], a[252:255], a[180:183], v[44:47]\n\t"
"s_mov_b32 s20, 983040\n\t"
"v_mfma_f32_16x16x32_bf16 v[44:47], v[96:99], a[184:187], v[44:47]\n\t"
"v_mfma_f32_16x16x32_bf16 v[44:47], v[248:251], a[188:191], v[44:47]\n\t"
"v_mfma_f32_16x16x32_bf16 v[48:51], v[248:251], a[192:195], v[48:51]\n\t"
"buffer_load_dwordx4 a[236:239], off, s[80:83], s20 offset:0 sc1\n\t"
"v_mfma_f32_16x16x32_bf16 v[48:51], v[96:99], a[188:191], v[48:51]\n\t"
"v_mfma_f32_16x16x32_bf16 v[48:51], a[252:255], a[184:187], v[48:51]\n\t"
"v_mfma_f32_16x16x32_bf16 v[52:55], a[252:255], a[188:191], v[52:55]\n\t"
"v_mfma_f32_16x16x32_bf16 v[52:55], v[96:99], a[192:195], v[52:55]\n\t"
"buffer_load_dwordx4 a[240:243], off, s[80:83], s20 offset:1024 sc1\n\t"
"v_mfma_f32_16x16x32_bf16 v[52:55], v[248:251], a[196:199], v[52:55]\n\t"
"v_mfma_f32_16x16x32_bf16 v[56:59], v[248:251], a[200:203], v[56:59]\n\t"
"v_mfma_f32_16x16x32_bf16 v[56:59], v[96:99], a[196:199], v[56:59]\n\t"
"v_mfma_f32_16x16x32_bf16 v[56:59], a[252:255], a[192:195], v[56:59]\n\t"
"buffer_load_dwordx4 a[244:247], off, s[80:83], s20 offset:2048 sc1\n\t"
"v_mfma_f32_16x16x32_bf16 v[60:63], a[252:255], a[196:199], v[60:63]\n\t"
"s_waitcnt vmcnt(9)\n\t"
"s_mov_b32 s20, 995328\n\t"
"v_mfma_f32_16x16x32_bf16 v[60:63], v[96:99], a[200:203], v[60:63]\n\t"
"v_mfma_f32_16x16x32_bf16 v[60:63], v[248:251], a[204:207], v[60:63]\n\t"
"v_mfma_f32_16x16x32_bf16 v[64:67], a[212:215], a[168:171], v[64:67]\n\t"
"buffer_load_dwordx4 v[96:99], off, s[80:83], s20 offset:0 sc1\n\t"
"v_mfma_f32_16x16x32_bf16 v[64:67], a[208:211], a[176:179], v[64:67]\n\t"
"ds_read_b128 a[168:171], v222 offset:2304\n\t"
"v_mfma_f32_16x16x32_bf16 v[64:67], v[240:243], a[172:175], v[64:67]\n\t"
"Lpg19_%=:\n\t"
"s_cbranch_vccnz Lvr2_%=\n\t"
"v_mfma_f32_16x16x32_bf16 v[68:71], a[212:215], a[172:175], v[68:71]\n\t"
"buffer_load_dwordx4 v[248:251], off, s[80:83], s20 offset:1024 sc1\n\t"
"v_mfma_f32_16x16x32_bf16 v[68:71], v[240:243], a[176:179], v[68:71]\n\t"
"ds_read_b128 a[172:175], v223 offset:8448\n\t"
"v_mfma_f32_16x16x32_bf16 v[68:71], a[208:211], a[180:183], v[68:71]\n\t"
"v_mfma_f32_16x16x32_bf16 v[72:75], a[208:211], a[184:187], v[72:75]\n\t"
"buffer_load_dwordx4 a[248:251], off, s[80:83], s20 offset:2048 sc1\n\t"
"v_mfma_f32_16x16x32_bf16 v[72:75], v[240:243], a[180:183], v[72:75]\n\t"
"s_mov_b32 s20, 1007616\n\t"
"v_mfma_f32_16x16x32_bf16 v[72:75], a[212:215], a[176:179], v[72:75]\n\t"
"ds_read_b128 a[176:179], v223 offset:14592\n\t"
"v_mfma_f32_16x16x32_bf16 v[76:79], a[212:215], a[180:183], v[76:79]\n\t"
"buffer_load_dwordx4 a[252:255], off, s[80:83], s20 offset:0 sc1\n\t"
"v_mfma_f32_16x16x32_bf16 v[76:79], v[240:243], a[184:187], v[76:79]\n\t"
"ds_read_b128 a[180:183], v222 offset:22272\n\t"
"v_mfma_f32_16x16x32_bf16 v[76:79], a[208:211], a[188:191], v[76:79]\n\t"
"v_mfma_f32_16x16x32_bf16 v[80:83], a[208:211], a[192:195], v[80:83]\n\t"
"v_mfma_f32_16x16x32_bf16 v[80:83], v[240:243], a[188:191], v[80:83]\n\t"
"v_mfma_f32_16x16x32_bf16 v[80:83], a[212:215], a[184:187], v[80:83]\n\t"
"ds_read_b128 a[184:187], v223 offset:28416\n\t"
"v_mfma_f32_16x16x32_bf16 v[84:87], a[212:215], a[188:191], v[84:87]\n\t"
"ds_read_b128 a[188:191], v223 offset:34560\n\t"
"v_mfma_f32_16x16x32_bf16 v[84:87], v[240:243], a[192:195], v[84:87]\n\t"
"v_mfma_f32_16x16x32_bf16 v[84:87], a[208:211], a[196:199], v[84:87]\n\t"
"v_mfma_f32_16x16x32_bf16 v[88:91], a[208:211], a[200:203], v[88:91]\n\t"
"v_mfma_f32_16x16x32_bf16 v[88:91], v[240:243], a[196:199], v[88:91]\n\t"
"v_mfma_f32_16x16x32_bf16 v[88:91], a[212:215], a[192:195], v[88:91]\n\t"
"ds_read_b128 a[192:195], v222 offset:42240\n\t"
"v_mfma_f32_16x16x32_bf16 v[92:95], a[212:215], a[196:199], v[92:95]\n\t"
"buffer_load_dwordx4 a[212:215], off, s[80:83], s20 offset:1024 sc1\n\t"
"v_mfma_f32_16x16x32_bf16 v[92:95], v[240:243], a[200:203], v[92:95]\n\t"
"s_waitcnt vmcnt(11) lgkmcnt(4)\n\t"
"ds_read_b128 a[196:199], v223 offset:48384\n\t"
"v_mfma_f32_16x16x32_bf16 v[92:95], a[208:211], a[204:207], v[92:95]\n\t"
"global_load_dwordx4 v[240:243], v252, s[48:49] offset:2048\n\t"
"v_mfma_f32_16x16x32_bf16 v[100:103], v[236:239], a[168:171], v[100:103]\n\t"
"s_waitcnt lgkmcnt(4)\n\t"
"ds_read_b128 a[200:203], v223 offset:54528\n\t"
"v_mfma_f32_16x16x32_bf16 v[100:103], a[220:223], a[176:179], v[100:103]\n\t"
"ds_read_b128 a[204:207], v222 offset:62208\n\t"
"v_mfma_f32_16x16x32_bf16 v[100:103], a[216:219], a[172:175], v[100:103]\n\t"
"v_mfma_f32_16x16x32_bf16 v[104:107], v[236:239], a[172:175], v[104:107]\n\t"
"s_waitcnt lgkmcnt(5)\n\t"
"v_mfma_f32_16x16x32_bf16 v[104:107], a[216:219], a[176:179], v[104:107]\n\t"
"global_load_dwordx4 a[208:211], v252, s[46:47] offset:1024\n\t"
"v_mfma_f32_16x16x32_bf16 v[104:107], a[220:223], a[180:183], v[104:107]\n\t"
"v_mfma_f32_16x16x32_bf16 v[108:111], a[220:223], a[184:187], v[108:111]\n\t"
"s_waitcnt lgkmcnt(4)\n\t"
"v_mfma_f32_16x16x32_bf16 v[108:111], a[216:219], a[180:183], v[108:111]\n\t"
"v_mfma_f32_16x16x32_bf16 v[108:111], v[236:239], a[176:179], v[108:111]\n\t"
"v_mfma_f32_16x16x32_bf16 v[112:115], v[236:239], a[180:183], v[112:115]\n\t"
"s_waitcnt lgkmcnt(3)\n\t"
"v_mfma_f32_16x16x32_bf16 v[112:115], a[216:219], a[184:187], v[112:115]\n\t"
"v_mfma_f32_16x16x32_bf16 v[112:115], a[220:223], a[188:191], v[112:115]\n\t"
"v_mfma_f32_16x16x32_bf16 v[116:119], a[220:223], a[192:195], v[116:119]\n\t"
"s_waitcnt lgkmcnt(2)\n\t"
"v_mfma_f32_16x16x32_bf16 v[116:119], a[216:219], a[188:191], v[116:119]\n\t"
"v_mfma_f32_16x16x32_bf16 v[116:119], v[236:239], a[184:187], v[116:119]\n\t"
"v_mfma_f32_16x16x32_bf16 v[120:123], v[236:239], a[188:191], v[120:123]\n\t"
"s_waitcnt lgkmcnt(1)\n\t"
"v_mfma_f32_16x16x32_bf16 v[120:123], a[216:219], a[192:195], v[120:123]\n\t"
"v_mfma_f32_16x16x32_bf16 v[120:123], a[220:223], a[196:199], v[120:123]\n\t"
"v_mfma_f32_16x16x32_bf16 v[124:127], a[220:223], a[200:203], v[124:127]\n\t"
"s_waitcnt lgkmcnt(0)\n\t"
"v_mfma_f32_16x16x32_bf16 v[124:127], a[216:219], a[196:199], v[124:127]\n\t"
"v_mfma_f32_16x16x32_bf16 v[124:127], v[236:239], a[192:195], v[124:127]\n\t"
"v_mfma_f32_16x16x32_bf16 v[128:131], v[236:239], a[196:199], v[128:131]\n\t"
"s_waitcnt vmcnt(10)\n\t"
"v_mfma_f32_16x16x32_bf16 v[128:131], a[216:219], a[200:203], v[128:131]\n\t"
"buffer_load_dwordx4 a[216:219], off, s[80:83], s20 offset:2048 sc1\n\t"
"v_mfma_f32_16x16x32_bf16 v[128:131], a[220:223], a[204:207], v[128:131]\n\t"
"s_mov_b32 s20, 1019904\n\t"
"s_add_u32 m0, s24, 42240\n\t"
"s_add_u32 s28, s21, 1182720\n\t"
"v_mfma_f32_16x16x32_bf16 v[132:135], a[224:227], a[168:171], v[132:135]\n\t"
"v_mfma_f32_16x16x32_bf16 v[132:135], a[232:235], a[176:179], v[132:135]\n\t"
"buffer_load_dwordx4 a[220:223], off, s[80:83], s20 offset:0 sc1\n\t"
"v_mfma_f32_16x16x32_bf16 v[132:135], a[228:231], a[172:175], v[132:135]\n\t"
"v_mfma_f32_16x16x32_bf16 v[136:139], a[224:227], a[172:175], v[136:139]\n\t"
"buffer_load_dwordx4 v247, s[12:15], s28 offen offset:256 nt lds\n\t"
"v_mfma_f32_16x16x32_bf16 v[136:139], a[228:231], a[176:179], v[136:139]\n\t"
"s_add_u32 m0, s24, 48384\n\t"
"s_add_u32 s28, s21, 1379328\n\t"
"v_mfma_f32_16x16x32_bf16 v[136:139], a[232:235], a[180:183], v[136:139]\n\t"
"s_barrier\n\t"
"v_mfma_f32_16x16x32_bf16 v[140:143], a[232:235], a[184:187], v[140:143]\n\t"
"v_mfma_f32_16x16x32_bf16 v[140:143], a[228:231], a[180:183], v[140:143]\n\t"
"buffer_load_dwordx4 v247, s[12:15], s28 offen offset:256 nt lds\n\t"
"v_mfma_f32_16x16x32_bf16 v[140:143], a[224:227], a[176:179], v[140:143]\n\t"
"s_add_u32 m0, s24, 54528\n\t"
"s_add_u32 s28, s21, 1575936\n\t"
"s_waitcnt vmcnt(5)\n\t"
"ds_read_b64_tr_b16 v[236:237], v240 offset:1024\n\t"
"v_mfma_f32_16x16x32_bf16 v[144:147], a[224:227], a[180:183], v[144:147]\n\t"
"buffer_load_dwordx4 v247, s[12:15], s28 offen offset:256 nt lds\n\t"
"v_mfma_f32_16x16x32_bf16 v[144:147], a[228:231], a[184:187], v[144:147]\n\t"
"s_add_u32 m0, s24, 40704\n\t"
"s_add_u32 s28, s21, 1179648\n\t"
"ds_read_b64_tr_b16 v[238:239], v240 offset:2560\n\t"
"v_mfma_f32_16x16x32_bf16 v[144:147], a[232:235], a[188:191], v[144:147]\n\t"
"buffer_load_dwordx4 v247, s[12:15], s28 offen offset:256 nt lds\n\t"
"s_add_u32 m0, s24, 46848\n\t"
"s_add_u32 s28, s21, 1376256\n\t"
"v_mfma_f32_16x16x32_bf16 v[148:151], a[232:235], a[192:195], v[148:151]\n\t"
"buffer_load_dwordx4 v247, s[12:15], s28 offen offset:256 nt lds\n\t"
"v_mfma_f32_16x16x32_bf16 v[148:151], a[228:231], a[188:191], v[148:151]\n\t"
"s_add_u32 m0, s24, 52992\n\t"
"s_add_u32 s28, s21, 1572864\n\t"
"ds_read_b64_tr_b16 v[228:229], v241 offset:1024\n\t"
"v_mfma_f32_16x16x32_bf16 v[148:151], a[224:227], a[184:187], v[148:151]\n\t"
"buffer_load_dwordx4 v247, s[12:15], s28 offen offset:256 nt lds\n\t"
"v_mfma_f32_16x16x32_bf16 v[152:155], a[224:227], a[188:191], v[152:155]\n\t"
"ds_read_b64_tr_b16 v[230:231], v241 offset:2560\n\t"
"v_mfma_f32_16x16x32_bf16 v[152:155], a[228:231], a[192:195], v[152:155]\n\t"
"ds_read_b64_tr_b16 v[232:233], v240 offset:20992\n\t"
"v_mfma_f32_16x16x32_bf16 v[152:155], a[232:235], a[196:199], v[152:155]\n\t"
"ds_read_b64_tr_b16 v[234:235], v240 offset:22528\n\t"
"v_mfma_f32_16x16x32_bf16 v[156:159], a[232:235], a[200:203], v[156:159]\n\t"
"ds_read_b64_tr_b16 v[224:225], v241 offset:20992\n\t"
"v_mfma_f32_16x16x32_bf16 v[156:159], a[228:231], a[196:199], v[156:159]\n\t"
"ds_read_b64_tr_b16 v[226:227], v241 offset:22528\n\t"
"v_mfma_f32_16x16x32_bf16 v[156:159], a[224:227], a[192:195], v[156:159]\n\t"
"v_mfma_f32_16x16x32_bf16 v[160:163], a[224:227], a[196:199], v[160:163]\n\t"
"s_waitcnt vmcnt(8) lgkmcnt(6)\n\t"
"v_mfma_f32_16x16x32_bf16 v[160:163], a[228:231], a[200:203], v[160:163]\n\t"
"s_waitcnt lgkmcnt(4)\n\t"
"v_mfma_f32_16x16x32_bf16 v[160:163], a[232:235], a[204:207], v[160:163]\n\t"
"s_waitcnt lgkmcnt(2)\n\t"
"v_mfma_f32_16x16x32_bf16 v[236:239], v[236:239], a[208:211], 0\n\t"
"s_waitcnt lgkmcnt(0)\n\t"
"v_mfma_f32_16x16x32_bf16 v[228:231], v[228:231], a[208:211], 0\n\t"
"v_mfma_f32_16x16x32_bf16 v[232:235], v[232:235], a[208:211], 0\n\t"
"v_mfma_f32_16x16x32_bf16 v[224:227], v[224:227], a[208:211], 0\n\t"
"v_mfma_f32_16x16x32_bf16 v[164:167], a[236:239], a[168:171], v[164:167]\n\t"
"ds_read_b128 a[168:171], v222 offset:512\n\t"
"v_mfma_f32_16x16x32_bf16 v[164:167], a[244:247], a[176:179], v[164:167]\n\t"
"v_cvt_pk_bf16_f32 v236, v236, v237\n\t"
"v_cvt_pk_bf16_f32 v237, v238, v239\n\t"
"v_mfma_f32_16x16x32_bf16 v[164:167], a[240:243], a[172:175], v[164:167]\n\t"
"v_cvt_pk_bf16_f32 v238, v228, v229\n\t"
"v_cvt_pk_bf16_f32 v239, v230, v231\n\t"
"v_mfma_f32_16x16x32_bf16 v[168:171], a[236:239], a[172:175], v[168:171]\n\t"
"ds_read_b128 a[172:175], v223 offset:6656\n\t"
"v_cvt_pk_bf16_f32 v232, v232, v233\n\t"
"v_mfma_f32_16x16x32_bf16 v[168:171], a[240:243], a[176:179], v[168:171]\n\t"
"v_cvt_pk_bf16_f32 v233, v234, v235\n\t"
"v_cvt_pk_bf16_f32 v234, v224, v225\n\t"
"v_mfma_f32_16x16x32_bf16 v[168:171], a[244:247], a[180:183], v[168:171]\n\t"
"ds_write_b128 v243, v[236:239] offset:2048\n\t"
"v_mfma_f32_16x16x32_bf16 v[172:175], a[244:247], a[184:187], v[172:175]\n\t"
"v_cvt_pk_bf16_f32 v235, v226, v227\n\t"
"v_add_u32 v240, 59904, v240\n\t"
"v_mfma_f32_16x16x32_bf16 v[172:175], a[240:243], a[180:183], v[172:175]\n\t"
"v_add_u32 v241, 59904, v241\n\t"
"v_mfma_f32_16x16x32_bf16 v[172:175], a[236:239], a[176:179], v[172:175]\n\t"
"ds_write_b128 v243, v[232:235] offset:22016\n\t"
"v_mfma_f32_16x16x32_bf16 v[176:179], a[236:239], a[180:183], v[176:179]\n\t"
"v_add_u32 v243, 59904, v243\n\t"
"v_mfma_f32_16x16x32_bf16 v[176:179], a[240:243], a[184:187], v[176:179]\n\t"
"ds_read_b128 a[176:179], v223 offset:12800\n\t"
"v_mfma_f32_16x16x32_bf16 v[176:179], a[244:247], a[188:191], v[176:179]\n\t"
"ds_read_b64_tr_b16 v[228:229], v240 offset:1024\n\t"
"v_mfma_f32_16x16x32_bf16 v[180:183], a[244:247], a[192:195], v[180:183]\n\t"
"ds_read_b64_tr_b16 v[230:231], v240 offset:2560\n\t"
"v_mfma_f32_16x16x32_bf16 v[180:183], a[240:243], a[188:191], v[180:183]\n\t"
"buffer_load_dwordx4 a[224:227], off, s[80:83], s20 offset:1024 sc1\n\t"
"v_mfma_f32_16x16x32_bf16 v[180:183], a[236:239], a[184:187], v[180:183]\n\t"
"ds_read_b64_tr_b16 v[224:225], v241 offset:1024\n\t"
"v_mfma_f32_16x16x32_bf16 v[184:187], a[236:239], a[188:191], v[184:187]\n\t"
"ds_read_b64_tr_b16 v[226:227], v241 offset:2560\n\t"
"v_mfma_f32_16x16x32_bf16 v[184:187], a[240:243], a[192:195], v[184:187]\n\t"
"ds_read_b128 a[180:183], v222 offset:20480\n\t"
"v_mfma_f32_16x16x32_bf16 v[184:187], a[244:247], a[196:199], v[184:187]\n\t"
"ds_read_b64_tr_b16 v[236:237], v240 offset:20992\n\t"
"v_mfma_f32_16x16x32_bf16 v[188:191], a[244:247], a[200:203], v[188:191]\n\t"
"ds_read_b64_tr_b16 v[238:239], v240 offset:22528\n\t"
"v_mfma_f32_16x16x32_bf16 v[188:191], a[240:243], a[196:199], v[188:191]\n\t"
"ds_read_b64_tr_b16 v[232:233], v241 offset:20992\n\t"
"v_mfma_f32_16x16x32_bf16 v[188:191], a[236:239], a[192:195], v[188:191]\n\t"
"ds_read_b64_tr_b16 v[234:235], v241 offset:22528\n\t"
"v_mfma_f32_16x16x32_bf16 v[192:195], a[236:239], a[196:199], v[192:195]\n\t"
"s_waitcnt lgkmcnt(7)\n\t"
"ds_read_b128 a[184:187], v223 offset:26624\n\t"
"v_mfma_f32_16x16x32_bf16 v[192:195], a[240:243], a[200:203], v[192:195]\n\t"
"s_waitcnt lgkmcnt(6)\n\t"
"ds_read_b128 a[188:191], v223 offset:32768\n\t"
"v_mfma_f32_16x16x32_bf16 v[192:195], a[244:247], a[204:207], v[192:195]\n\t"
"s_waitcnt lgkmcnt(4)\n\t"
"ds_read_b128 a[192:195], v222 offset:40448\n\t"
"v_mfma_f32_16x16x32_bf16 v[228:231], v[228:231], a[208:211], 0\n\t"
"ds_read_b128 a[196:199], v223 offset:46592\n\t"
"v_mfma_f32_16x16x32_bf16 v[224:227], v[224:227], a[208:211], 0\n\t"
"s_waitcnt lgkmcnt(4)\n\t"
"ds_read_b128 a[200:203], v223 offset:52736\n\t"
"v_mfma_f32_16x16x32_bf16 v[236:239], v[236:239], a[208:211], 0\n\t"
"ds_read_b128 a[204:207], v222 offset:60416\n\t"
"v_mfma_f32_16x16x32_bf16 v[232:235], v[232:235], a[208:211], 0\n\t"
"Lst27_%=:\n\t"
"v_mfma_f32_16x16x32_bf16 v[196:199], v[96:99], a[168:171], v[196:199]\n\t"
"s_mov_b64 vcc, -1\n\t"
"s_bitcmp1_b32 s73, 1\n\t"
"s_cbranch_scc1 Lvh4_%=\n\t"
"s_bitcmp1_b32 s73, 0\n\t"
"s_cbranch_scc1 Lvr4_%=\n\t"
"s_branch Lpg25_%=\n\t"
"Lvh4_%=:\n\t"
"s_bitcmp1_b32 s73, 0\n\t"
"s_cbranch_scc1 Lvr4_%=\n\t"
"s_branch Lvr4_%=\n\t"
"Lvr4_%=:\n\t"
"s_mov_b64 vcc, 0\n\t"
"v_mfma_f32_16x16x32_bf16 v[196:199], a[248:251], a[176:179], v[196:199]\n\t"
"v_cvt_pk_bf16_f32 v228, v228, v229\n\t"
"v_cvt_pk_bf16_f32 v229, v230, v231\n\t"
"v_mfma_f32_16x16x32_bf16 v[196:199], v[248:251], a[172:175], v[196:199]\n\t"
"v_cvt_pk_bf16_f32 v230, v224, v225\n\t"
"v_cvt_pk_bf16_f32 v231, v226, v227\n\t"
"v_mfma_f32_16x16x32_bf16 v[204:207], v[96:99], a[172:175], v[204:207]\n\t"
"s_waitcnt lgkmcnt(5)\n\t"
"v_cvt_pk_bf16_f32 v236, v236, v237\n\t"
"v_cvt_pk_bf16_f32 v237, v238, v239\n\t"
"v_mfma_f32_16x16x32_bf16 v[204:207], v[248:251], a[176:179], v[204:207]\n\t"
"v_cvt_pk_bf16_f32 v238, v232, v233\n\t"
"v_cvt_pk_bf16_f32 v239, v234, v235\n\t"
"v_mfma_f32_16x16x32_bf16 v[204:207], a[248:251], a[180:183], v[204:207]\n\t"
"ds_write_b128 v243, v[228:231] offset:2048\n\t"
"v_mfma_f32_16x16x32_bf16 v[212:215], a[248:251], a[184:187], v[212:215]\n\t"
"s_waitcnt lgkmcnt(5)\n\t"
"v_mfma_f32_16x16x32_bf16 v[212:215], v[248:251], a[180:183], v[212:215]\n\t"
"v_mfma_f32_16x16x32_bf16 v[212:215], v[96:99], a[176:179], v[212:215]\n\t"
"ds_write_b128 v243, v[236:239] offset:22016\n\t"
"v_mfma_f32_16x16x32_bf16 a[0:3], v[96:99], a[180:183], a[0:3]\n\t"
"s_waitcnt lgkmcnt(5)\n\t"
"v_mfma_f32_16x16x32_bf16 a[0:3], v[248:251], a[184:187], a[0:3]\n\t"
"v_mfma_f32_16x16x32_bf16 a[0:3], a[248:251], a[188:191], a[0:3]\n\t"
"s_barrier\n\t"
"v_mfma_f32_16x16x32_bf16 a[8:11], a[248:251], a[192:195], a[8:11]\n\t"
"s_waitcnt lgkmcnt(4)\n\t"
"ds_read_b64_tr_b16 v[224:225], v246 offset:1024\n\t"
"v_mfma_f32_16x16x32_bf16 a[8:11], v[248:251], a[188:191], a[8:11]\n\t"
"ds_read_b64_tr_b16 v[232:233], v255 offset:1024\n\t"
"v_mfma_f32_16x16x32_bf16 a[8:11], v[96:99], a[184:187], a[8:11]\n\t"
"ds_read_b64_tr_b16 v[228:229], v246 offset:7168\n\t"
"v_mfma_f32_16x16x32_bf16 a[16:19], v[96:99], a[188:191], a[16:19]\n\t"
"s_waitcnt lgkmcnt(6)\n\t"
"ds_read_b64_tr_b16 v[236:237], v255 offset:7168\n\t"
"v_mfma_f32_16x16x32_bf16 a[16:19], v[248:251], a[192:195], a[16:19]\n\t"
"buffer_load_dwordx4 a[228:231], off, s[80:83], s20 offset:2048 sc1\n\t"
"v_mfma_f32_16x16x32_bf16 a[16:19], a[248:251], a[196:199], a[16:19]\n\t"
"s_mov_b32 s20, 1032192\n\t"
"v_mfma_f32_16x16x32_bf16 a[24:27], a[248:251], a[200:203], a[24:27]\n\t"
"s_waitcnt lgkmcnt(6)\n\t"
"v_mfma_f32_16x16x32_bf16 a[24:27], v[248:251], a[196:199], a[24:27]\n\t"
"v_mfma_f32_16x16x32_bf16 a[24:27], v[96:99], a[192:195], a[24:27]\n\t"
"buffer_load_dwordx4 a[232:235], off, s[80:83], s20 offset:0 sc1\n\t"
"v_mfma_f32_16x16x32_bf16 a[32:35], v[96:99], a[196:199], a[32:35]\n\t"
"s_waitcnt lgkmcnt(3)\n\t"
"v_mfma_f32_16x16x32_bf16 a[32:35], v[248:251], a[200:203], a[32:35]\n\t"
"s_waitcnt lgkmcnt(2)\n\t"
"v_mfma_f32_16x16x32_bf16 a[32:35], a[248:251], a[204:207], a[32:35]\n\t"
"s_waitcnt lgkmcnt(1)\n\t"
"v_mfma_f32_4x4x4_16b_bf16 v[224:227], v[224:225], v[220:221], 0\n\t"
"s_waitcnt lgkmcnt(0)\n\t"
"v_mfma_f32_4x4x4_16b_bf16 v[232:235], v[232:233], v[220:221], 0\n\t"
"s_waitcnt vmcnt(10)\n\t"
"v_mfma_f32_4x4x4_16b_bf16 v[228:231], v[228:229], v[220:221], 0\n\t"
"v_mfma_f32_4x4x4_16b_bf16 v[236:239], v[236:237], v[220:221], 0\n\t"
"buffer_load_dwordx4 v[96:99], off, s[80:83], s20 offset:1024 sc1\n\t"
"v_mfma_f32_16x16x32_bf16 a[40:43], a[252:255], a[168:171], a[40:43]\n\t"
"buffer_load_dwordx4 v[248:251], off, s[80:83], s20 offset:2048 sc1\n\t"
"v_mfma_f32_16x16x32_bf16 a[40:43], a[216:219], a[176:179], a[40:43]\n\t"
"s_mov_b32 s20, 1044480\n\t"
"v_cvt_pk_bf16_f32 v224, v224, v225\n\t"
"v_cvt_pk_bf16_f32 v225, v226, v227\n\t"
"v_mfma_f32_16x16x32_bf16 a[40:43], a[212:215], a[172:175], a[40:43]\n\t"
"v_cvt_pk_bf16_f32 v226, v232, v233\n\t"
"v_cvt_pk_bf16_f32 v227, v234, v235\n\t"
"v_mfma_f32_16x16x32_bf16 a[48:51], a[252:255], a[172:175], a[48:51]\n\t"
"ds_read_b64_tr_b16 v[232:233], v246 offset:13312\n\t"
"v_cvt_pk_bf16_f32 v228, v228, v229\n\t"
"v_mfma_f32_16x16x32_bf16 a[48:51], a[212:215], a[176:179], a[48:51]\n\t"
"v_cvt_pk_bf16_f32 v229, v230, v231\n\t"
"v_cvt_pk_bf16_f32 v230, v236, v237\n\t"
"v_mfma_f32_16x16x32_bf16 a[48:51], a[216:219], a[180:183], a[48:51]\n\t"
"ds_write_b128 v245, v[224:227] offset:1024\n\t"
"v_cvt_pk_bf16_f32 v231, v238, v239\n\t"
"v_mfma_f32_16x16x32_bf16 a[56:59], a[216:219], a[184:187], a[56:59]\n\t"
"v_mfma_f32_16x16x32_bf16 a[56:59], a[212:215], a[180:183], a[56:59]\n\t"
"buffer_load_dwordx4 a[236:239], off, s[80:83], s20 offset:0 sc1\n\t"
"v_mfma_f32_16x16x32_bf16 a[56:59], a[252:255], a[176:179], a[56:59]\n\t"
"ds_read_b64_tr_b16 v[236:237], v255 offset:13312\n\t"
"v_mfma_f32_16x16x32_bf16 a[64:67], a[252:255], a[180:183], a[64:67]\n\t"
"ds_read_b64_tr_b16 v[224:225], v246 offset:20992\n\t"
"v_mfma_f32_16x16x32_bf16 a[64:67], a[212:215], a[184:187], a[64:67]\n\t"
"buffer_load_dwordx4 a[240:243], off, s[80:83], s20 offset:1024 sc1\n\t"
"v_mfma_f32_16x16x32_bf16 a[64:67], a[216:219], a[188:191], a[64:67]\n\t"
"ds_write_b128 v245, v[228:231] offset:7168\n\t"
"v_mfma_f32_16x16x32_bf16 a[72:75], a[252:255], a[184:187], a[72:75]\n\t"
"buffer_load_dwordx4 a[244:247], off, s[80:83], s20 offset:2048 sc1\n\t"
"v_mfma_f32_16x16x32_bf16 a[72:75], a[216:219], a[192:195], a[72:75]\n\t"
"s_waitcnt lgkmcnt(2)\n\t"
"ds_read_b64_tr_b16 v[228:229], v255 offset:20992\n\t"
"v_mfma_f32_16x16x32_bf16 a[72:75], a[212:215], a[188:191], a[72:75]\n\t"
"s_waitcnt lgkmcnt(2)\n\t"
"s_mov_b32 s20, 1056768\n\t"
"v_mfma_f32_4x4x4_16b_bf16 v[232:235], v[232:233], v[220:221], 0\n\t"
"v_mfma_f32_4x4x4_16b_bf16 v[236:239], v[236:237], v[220:221], 0\n\t"
"v_mfma_f32_4x4x4_16b_bf16 v[224:227], v[224:225], v[220:221], 0\n\t"
"s_waitcnt lgkmcnt(0)\n\t"
"v_mfma_f32_4x4x4_16b_bf16 v[228:231], v[228:229], v[220:221], 0\n\t"
"v_mfma_f32_16x16x32_bf16 a[80:83], a[252:255], a[188:191], a[80:83]\n\t"
"buffer_load_dwordx4 a[248:251], off, s[80:83], s20 offset:0 sc1\n\t"
"v_mfma_f32_16x16x32_bf16 a[80:83], a[216:219], a[196:199], a[80:83]\n\t"
"v_cvt_pk_bf16_f32 v232, v232, v233\n\t"
"v_cvt_pk_bf16_f32 v233, v234, v235\n\t"
"v_mfma_f32_16x16x32_bf16 a[80:83], a[212:215], a[192:195], a[80:83]\n\t"
"v_cvt_pk_bf16_f32 v234, v236, v237\n\t"
"v_cvt_pk_bf16_f32 v235, v238, v239\n\t"
"v_mfma_f32_16x16x32_bf16 a[88:91], a[252:255], a[192:195], a[88:91]\n\t"
"ds_read_b64_tr_b16 v[236:237], v246 offset:27136\n\t"
"v_cvt_pk_bf16_f32 v224, v224, v225\n\t"
"v_mfma_f32_16x16x32_bf16 a[88:91], a[212:215], a[196:199], a[88:91]\n\t"
"v_cvt_pk_bf16_f32 v225, v226, v227\n\t"
"v_cvt_pk_bf16_f32 v226, v228, v229\n\t"
"v_mfma_f32_16x16x32_bf16 a[88:91], a[216:219], a[200:203], a[88:91]\n\t"
"ds_write_b128 v245, v[232:235] offset:13312\n\t"
"v_cvt_pk_bf16_f32 v227, v230, v231\n\t"
"v_mfma_f32_16x16x32_bf16 a[96:99], a[212:215], a[200:203], a[96:99]\n\t"
"s_waitcnt vmcnt(7)\n\t"
"v_mfma_f32_16x16x32_bf16 a[96:99], a[252:255], a[196:199], a[96:99]\n\t"
"ds_read_b64_tr_b16 v[228:229], v255 offset:27136\n\t"
"v_mfma_f32_16x16x32_bf16 a[96:99], a[216:219], a[204:207], a[96:99]\n\t"
"ds_write_b128 v245, v[224:227] offset:20992\n\t"
"v_mfma_f32_16x16x32_bf16 a[104:107], a[220:223], a[168:171], a[104:107]\n\t"
"buffer_load_dwordx4 a[212:215], off, s[80:83], s20 offset:1024 sc1\n\t"
"v_mfma_f32_16x16x32_bf16 a[104:107], a[228:231], a[176:179], a[104:107]\n\t"
"ds_read_b64_tr_b16 v[232:233], v246 offset:33280\n\t"
"v_mfma_f32_16x16x32_bf16 a[104:107], a[224:227], a[172:175], a[104:107]\n\t"
"ds_read_b64_tr_b16 v[224:225], v255 offset:33280\n\t"
"v_mfma_f32_16x16x32_bf16 a[112:115], a[220:223], a[172:175], a[112:115]\n\t"
"buffer_load_dwordx4 a[216:219], off, s[80:83], s20 offset:2048 sc1\n\t"
"v_mfma_f32_16x16x32_bf16 a[112:115], a[224:227], a[176:179], a[112:115]\n\t"
"s_waitcnt lgkmcnt(3)\n\t"
"ds_read_b128 a[168:171], v222 offset:768\n\t"
"s_mov_b32 s20, 1290240\n\t"
"v_mfma_f32_16x16x32_bf16 a[112:115], a[228:231], a[180:183], a[112:115]\n\t"
"buffer_load_dwordx4 a[252:255], off, s[80:83], s20 offset:0 sc1\n\t"
"v_mfma_f32_4x4x4_16b_bf16 v[236:239], v[236:237], v[220:221], 0\n\t"
"s_waitcnt lgkmcnt(2)\n\t"
"v_mfma_f32_4x4x4_16b_bf16 v[228:231], v[228:229], v[220:221], 0\n\t"
"s_waitcnt lgkmcnt(1)\n\t"
"v_mfma_f32_4x4x4_16b_bf16 v[232:235], v[232:233], v[220:221], 0\n\t"
"v_mfma_f32_4x4x4_16b_bf16 v[224:227], v[224:225], v[220:221], 0\n\t"
"v_mfma_f32_16x16x32_bf16 a[120:123], a[224:227], a[180:183], a[120:123]\n\t"
"ds_read_b128 a[172:175], v223 offset:6912\n\t"
"v_cvt_pk_bf16_f32 v236, v236, v237\n\t"
"v_mfma_f32_16x16x32_bf16 a[120:123], a[220:223], a[176:179], a[120:123]\n\t"
"ds_read_b128 a[176:179], v223 offset:13056\n\t"
"v_cvt_pk_bf16_f32 v237, v238, v239\n\t"
"v_mfma_f32_16x16x32_bf16 a[120:123], a[228:231], a[184:187], a[120:123]\n\t"
"v_cvt_pk_bf16_f32 v238, v228, v229\n\t"
"v_cvt_pk_bf16_f32 v239, v230, v231\n\t"
"v_mfma_f32_16x16x32_bf16 a[128:131], a[220:223], a[180:183], a[128:131]\n\t"
"ds_read_b128 a[180:183], v222 offset:20736\n\t"
"v_cvt_pk_bf16_f32 v232, v232, v233\n\t"
"v_mfma_f32_16x16x32_bf16 a[128:131], a[228:231], a[188:191], a[128:131]\n\t"
"v_cvt_pk_bf16_f32 v233, v234, v235\n\t"
"v_cvt_pk_bf16_f32 v234, v224, v225\n\t"
"v_mfma_f32_16x16x32_bf16 a[128:131], a[224:227], a[184:187], a[128:131]\n\t"
"ds_write_b128 v245, v[236:239] offset:27136\n\t"
"v_cvt_pk_bf16_f32 v235, v226, v227\n\t"
"v_mfma_f32_16x16x32_bf16 a[136:139], a[220:223], a[184:187], a[136:139]\n\t"
"v_mfma_f32_16x16x32_bf16 a[136:139], a[224:227], a[188:191], a[136:139]\n\t"
"ds_read_b128 a[184:187], v223 offset:26880\n\t"
"v_mfma_f32_16x16x32_bf16 a[136:139], a[228:231], a[192:195], a[136:139]\n\t"
"ds_write_b128 v245, v[232:235] offset:33280\n\t"
"v_mfma_f32_16x16x32_bf16 a[144:147], a[228:231], a[196:199], a[144:147]\n\t"
"v_mfma_f32_16x16x32_bf16 a[144:147], a[224:227], a[192:195], a[144:147]\n\t"
"v_mfma_f32_16x16x32_bf16 a[144:147], a[220:223], a[188:191], a[144:147]\n\t"
"ds_read_b128 a[188:191], v223 offset:33024\n\t"
"v_mfma_f32_16x16x32_bf16 a[152:155], a[220:223], a[192:195], a[152:155]\n\t"
"ds_read_b128 a[192:195], v222 offset:40704\n\t"
"v_mfma_f32_16x16x32_bf16 a[152:155], a[224:227], a[196:199], a[152:155]\n\t"
"v_mfma_f32_16x16x32_bf16 a[152:155], a[228:231], a[200:203], a[152:155]\n\t"
"s_barrier\n\t"
"v_mfma_f32_16x16x32_bf16 a[160:163], a[224:227], a[200:203], a[160:163]\n\t"
"s_waitcnt vmcnt(7) lgkmcnt(6)\n\t"
"ds_read_b64_tr_b16 v[228:229], v240 offset:40960\n\t"
"v_mfma_f32_16x16x32_bf16 a[160:163], a[220:223], a[196:199], a[160:163]\n\t"
"ds_read_b64_tr_b16 v[230:231], v240 offset:42496\n\t"
"v_mfma_f32_16x16x32_bf16 a[160:163], a[228:231], a[204:207], a[160:163]\n\t"
"ds_read_b64_tr_b16 v[224:225], v241 offset:40960\n\t"
"v_mfma_f32_16x16x32_bf16 v[200:203], a[232:235], a[168:171], v[200:203]\n\t"
"s_waitcnt lgkmcnt(8)\n\t"
"ds_read_b64_tr_b16 v[226:227], v241 offset:42496\n\t"
"v_subrev_u32 v240, 59904, v240\n\t"
"v_mfma_f32_16x16x32_bf16 v[200:203], v[248:251], a[176:179], v[200:203]\n\t"
"ds_read_b128 a[196:199], v223 offset:46848\n\t"
"v_subrev_u32 v241, 59904, v241\n\t"
"v_mfma_f32_16x16x32_bf16 v[200:203], v[96:99], a[172:175], v[200:203]\n\t"
"ds_read_b64_tr_b16 v[236:237], v240 offset:40960\n\t"
"v_mfma_f32_16x16x32_bf16 v[208:211], a[232:235], a[172:175], v[208:211]\n\t"
"s_waitcnt lgkmcnt(4)\n\t"
"ds_read_b64_tr_b16 v[238:239], v240 offset:42496\n\t"
"v_mfma_f32_16x16x32_bf16 v[208:211], v[96:99], a[176:179], v[208:211]\n\t"
"buffer_load_dwordx4 a[224:227], off, s[80:83], s20 offset:1024 sc1\n\t"
"v_mfma_f32_16x16x32_bf16 v[208:211], v[248:251], a[180:183], v[208:211]\n\t"
"s_waitcnt lgkmcnt(3)\n\t"
"ds_read_b64_tr_b16 v[232:233], v241 offset:40960\n\t"
"ds_read_b64_tr_b16 v[234:235], v241 offset:42496\n\t"
"v_mfma_f32_16x16x32_bf16 v[228:231], v[228:231], a[208:211], 0\n\t"
"buffer_load_dwordx4 a[220:223], off, s[80:83], s20 offset:2048 sc1\n\t"
"s_mov_b32 s20, 1302528\n\t"
"v_mfma_f32_16x16x32_bf16 v[224:227], v[224:227], a[208:211], 0\n\t"
"s_waitcnt lgkmcnt(2)\n\t"
"buffer_load_dwordx4 a[228:231], off, s[80:83], s20 offset:0 sc1\n\t"
"v_mfma_f32_16x16x32_bf16 v[236:239], v[236:239], a[208:211], 0\n\t"
"s_waitcnt lgkmcnt(0)\n\t"
"ds_read_b128 a[200:203], v223 offset:52992\n\t"
"v_mfma_f32_16x16x32_bf16 v[232:235], v[232:235], a[208:211], 0\n\t"
"ds_read_b128 a[204:207], v222 offset:60672\n\t"
"v_mfma_f32_16x16x32_bf16 v[216:219], v[96:99], a[180:183], v[216:219]\n\t"
"v_mfma_f32_16x16x32_bf16 v[216:219], a[232:235], a[176:179], v[216:219]\n\t"
"v_cvt_pk_bf16_f32 v228, v228, v229\n\t"
"v_cvt_pk_bf16_f32 v229, v230, v231\n\t"
"v_mfma_f32_16x16x32_bf16 v[216:219], v[248:251], a[184:187], v[216:219]\n\t"
"v_cvt_pk_bf16_f32 v230, v224, v225\n\t"
"v_cvt_pk_bf16_f32 v231, v226, v227\n\t"
"v_mfma_f32_16x16x32_bf16 a[4:7], a[232:235], a[180:183], a[4:7]\n\t"
"ds_read_b64_tr_b16 v[224:225], v246 offset:40960\n\t"
"v_cvt_pk_bf16_f32 v236, v236, v237\n\t"
"v_mfma_f32_16x16x32_bf16 a[4:7], v[248:251], a[188:191], a[4:7]\n\t"
"v_cvt_pk_bf16_f32 v237, v238, v239\n\t"
"v_cvt_pk_bf16_f32 v238, v232, v233\n\t"
"v_mfma_f32_16x16x32_bf16 a[4:7], v[96:99], a[184:187], a[4:7]\n\t"
"ds_write_b128 v243, v[228:231] offset:41984\n\t"
"v_mfma_f32_16x16x32_bf16 a[12:15], a[232:235], a[184:187], a[12:15]\n\t"
"v_cvt_pk_bf16_f32 v239, v234, v235\n\t"
"v_subrev_u32 v243, 59904, v243\n\t"
"v_mfma_f32_16x16x32_bf16 a[12:15], v[96:99], a[188:191], a[12:15]\n\t"
"ds_read_b64_tr_b16 v[232:233], v255 offset:40960\n\t"
"v_mfma_f32_16x16x32_bf16 a[12:15], v[248:251], a[192:195], a[12:15]\n\t"
"ds_write_b128 v243, v[236:239] offset:41984\n\t"
"v_mfma_f32_16x16x32_bf16 a[20:23], v[248:251], a[196:199], a[20:23]\n\t"
"s_waitcnt lgkmcnt(5)\n\t"
"v_mfma_f32_16x16x32_bf16 a[20:23], v[96:99], a[192:195], a[20:23]\n\t"
"ds_read_b64_tr_b16 v[228:229], v246 offset:47104\n\t"
"v_mfma_f32_16x16x32_bf16 a[20:23], a[232:235], a[188:191], a[20:23]\n\t"
"ds_read_b64_tr_b16 v[236:237], v255 offset:47104\n\t"
"v_mfma_f32_16x16x32_bf16 a[28:31], a[232:235], a[192:195], a[28:31]\n\t"
"s_waitcnt lgkmcnt(6)\n\t"
"v_mfma_f32_16x16x32_bf16 a[28:31], v[96:99], a[196:199], a[28:31]\n\t"
"v_mfma_f32_16x16x32_bf16 a[28:31], v[248:251], a[200:203], a[28:31]\n\t"
"s_barrier\n\t"
"v_mfma_f32_16x16x32_bf16 a[36:39], v[96:99], a[200:203], a[36:39]\n\t"
"s_waitcnt lgkmcnt(5)\n\t"
"v_mfma_f32_16x16x32_bf16 a[36:39], a[232:235], a[196:199], a[36:39]\n\t"
"buffer_load_dwordx4 v[96:99], off, s[80:83], s20 offset:1024 sc1\n\t"
"v_mfma_f32_16x16x32_bf16 a[36:39], v[248:251], a[204:207], a[36:39]\n\t"
"s_waitcnt lgkmcnt(1)\n\t"
"v_mfma_f32_4x4x4_16b_bf16 v[224:227], v[224:225], v[220:221], 0\n\t"
"s_waitcnt lgkmcnt(0)\n\t"
"v_mfma_f32_4x4x4_16b_bf16 v[232:235], v[232:233], v[220:221], 0\n\t"
"s_waitcnt vmcnt(8)\n\t"
"v_mfma_f32_4x4x4_16b_bf16 v[228:231], v[228:229], v[220:221], 0\n\t"
"v_mfma_f32_4x4x4_16b_bf16 v[236:239], v[236:237], v[220:221], 0\n\t"
"buffer_load_dwordx4 v[248:251], off, s[80:83], s20 offset:2048 sc1\n\t"
"s_mov_b32 s20, 1314816\n\t"
"v_mfma_f32_16x16x32_bf16 a[44:47], a[236:239], a[168:171], a[44:47]\n\t"
"buffer_load_dwordx4 a[232:235], off, s[80:83], s20 offset:0 sc1\n\t"
"v_mfma_f32_16x16x32_bf16 a[44:47], a[244:247], a[176:179], a[44:47]\n\t"
"v_cvt_pk_bf16_f32 v224, v224, v225\n\t"
"v_cvt_pk_bf16_f32 v225, v226, v227\n\t"
"v_mfma_f32_16x16x32_bf16 a[44:47], a[240:243], a[172:175], a[44:47]\n\t"
"v_cvt_pk_bf16_f32 v226, v232, v233\n\t"
"v_cvt_pk_bf16_f32 v227, v234, v235\n\t"
"v_mfma_f32_16x16x32_bf16 a[52:55], a[236:239], a[172:175], a[52:55]\n\t"
"ds_read_b64_tr_b16 v[232:233], v246 offset:53248\n\t"
"v_cvt_pk_bf16_f32 v228, v228, v229\n\t"
"v_mfma_f32_16x16x32_bf16 a[52:55], a[240:243], a[176:179], a[52:55]\n\t"
"v_cvt_pk_bf16_f32 v229, v230, v231\n\t"
"v_cvt_pk_bf16_f32 v230, v236, v237\n\t"
"v_mfma_f32_16x16x32_bf16 a[52:55], a[244:247], a[180:183], a[52:55]\n\t"
"ds_write_b128 v245, v[224:227] offset:40960\n\t"
"v_cvt_pk_bf16_f32 v231, v238, v239\n\t"
"v_mfma_f32_16x16x32_bf16 a[60:63], a[244:247], a[184:187], a[60:63]\n\t"
"v_mfma_f32_16x16x32_bf16 a[60:63], a[240:243], a[180:183], a[60:63]\n\t"
"ds_read_b64_tr_b16 v[236:237], v255 offset:53248\n\t"
"v_mfma_f32_16x16x32_bf16 a[60:63], a[236:239], a[176:179], a[60:63]\n\t"
"ds_write_b128 v245, v[228:231] offset:47104\n\t"
"v_mfma_f32_16x16x32_bf16 a[68:71], a[236:239], a[180:183], a[68:71]\n\t"
"v_mfma_f32_16x16x32_bf16 a[68:71], a[240:243], a[184:187], a[68:71]\n\t"
"v_mfma_f32_16x16x32_bf16 a[68:71], a[244:247], a[188:191], a[68:71]\n\t"
"v_mfma_f32_16x16x32_bf16 a[76:79], a[236:239], a[184:187], a[76:79]\n\t"
"s_waitcnt lgkmcnt(3)\n\t"
"v_mfma_f32_16x16x32_bf16 a[76:79], a[244:247], a[192:195], a[76:79]\n\t"
"s_waitcnt lgkmcnt(1)\n\t"
"v_mfma_f32_16x16x32_bf16 a[76:79], a[240:243], a[188:191], a[76:79]\n\t"
"v_mfma_f32_4x4x4_16b_bf16 v[232:235], v[232:233], v[220:221], 0\n\t"
"v_mfma_f32_4x4x4_16b_bf16 v[236:239], v[236:237], v[220:221], 0\n\t"
"v_mfma_f32_16x16x32_bf16 a[84:87], a[236:239], a[188:191], a[84:87]\n\t"
"v_mfma_f32_16x16x32_bf16 a[84:87], a[244:247], a[196:199], a[84:87]\n\t"
"v_cvt_pk_bf16_f32 v232, v232, v233\n\t"
"v_cvt_pk_bf16_f32 v233, v234, v235\n\t"
"v_mfma_f32_16x16x32_bf16 a[84:87], a[240:243], a[192:195], a[84:87]\n\t"
"v_cvt_pk_bf16_f32 v234, v236, v237\n\t"
"v_cvt_pk_bf16_f32 v235, v238, v239\n\t"
"v_mfma_f32_16x16x32_bf16 a[92:95], a[236:239], a[192:195], a[92:95]\n\t"
"v_mfma_f32_16x16x32_bf16 a[92:95], a[240:243], a[196:199], a[92:95]\n\t"
"v_mfma_f32_16x16x32_bf16 a[92:95], a[244:247], a[200:203], a[92:95]\n\t"
"ds_write_b128 v245, v[232:235] offset:53248\n\t"
"v_mfma_f32_16x16x32_bf16 a[100:103], a[240:243], a[200:203], a[100:103]\n\t"
"buffer_load_dwordx4 a[240:243], off, s[80:83], s20 offset:1024 sc1\n\t"
"v_mfma_f32_16x16x32_bf16 a[100:103], a[236:239], a[196:199], a[100:103]\n\t"
"s_waitcnt vmcnt(8)\n\t"
"v_mfma_f32_16x16x32_bf16 a[100:103], a[244:247], a[204:207], a[100:103]\n\t"
"v_mfma_f32_16x16x32_bf16 a[108:111], a[248:251], a[168:171], a[108:111]\n\t"
"buffer_load_dwordx4 a[236:239], off, s[80:83], s20 offset:2048 sc1\n\t"
"v_mfma_f32_16x16x32_bf16 a[108:111], a[216:219], a[176:179], a[108:111]\n\t"
"s_mov_b32 s20, 1327104\n\t"
"v_mfma_f32_16x16x32_bf16 a[108:111], a[212:215], a[172:175], a[108:111]\n\t"
"s_waitcnt lgkmcnt(0)\n\t"
"s_barrier\n\t"
"v_mfma_f32_16x16x32_bf16 a[116:119], a[248:251], a[172:175], a[116:119]\n\t"
"ds_read_b128 a[168:171], v222 offset:1024\n\t"
"v_mfma_f32_16x16x32_bf16 a[116:119], a[212:215], a[176:179], a[116:119]\n\t"
"buffer_load_dwordx4 a[244:247], off, s[80:83], s20 offset:0 sc1\n\t"
"v_mfma_f32_16x16x32_bf16 a[116:119], a[216:219], a[180:183], a[116:119]\n\t"
"ds_read_b128 a[172:175], v223 offset:7168\n\t"
"v_mfma_f32_16x16x32_bf16 a[124:127], a[216:219], a[184:187], a[124:127]\n\t"
"v_mfma_f32_16x16x32_bf16 a[124:127], a[212:215], a[180:183], a[124:127]\n\t"
"v_mfma_f32_16x16x32_bf16 a[124:127], a[248:251], a[176:179], a[124:127]\n\t"
"ds_read_b128 a[176:179], v223 offset:13312\n\t"
"v_mfma_f32_16x16x32_bf16 a[132:135], a[248:251], a[180:183], a[132:135]\n\t"
"ds_read_b64_tr_b16 v[224:225], v240 offset:1536\n\t"
"v_mfma_f32_16x16x32_bf16 a[132:135], a[212:215], a[184:187], a[132:135]\n\t"
"ds_read_b64_tr_b16 v[226:227], v240 offset:3072\n\t"
"v_mfma_f32_16x16x32_bf16 a[132:135], a[216:219], a[188:191], a[132:135]\n\t"
"ds_read_b64_tr_b16 v[228:229], v241 offset:1536\n\t"
"v_mfma_f32_16x16x32_bf16 a[140:143], a[216:219], a[192:195], a[140:143]\n\t"
"ds_read_b64_tr_b16 v[230:231], v241 offset:3072\n\t"
"v_mfma_f32_16x16x32_bf16 a[140:143], a[212:215], a[188:191], a[140:143]\n\t"
"ds_read_b128 a[180:183], v222 offset:20992\n\t"
"v_mfma_f32_16x16x32_bf16 a[140:143], a[248:251], a[184:187], a[140:143]\n\t"
"ds_read_b64_tr_b16 v[236:237], v240 offset:21504\n\t"
"v_mfma_f32_16x16x32_bf16 a[148:151], a[248:251], a[188:191], a[148:151]\n\t"
"s_waitcnt lgkmcnt(4)\n\t"
"ds_read_b64_tr_b16 v[238:239], v240 offset:23040\n\t"
"v_mfma_f32_16x16x32_bf16 a[148:151], a[212:215], a[192:195], a[148:151]\n\t"
"Lpg20_%=:\n\t"
"s_cbranch_vccnz Lvr2_%=\n\t"
"ds_read_b64_tr_b16 v[232:233], v241 offset:21504\n\t"
"v_mfma_f32_16x16x32_bf16 a[148:151], a[216:219], a[196:199], a[148:151]\n\t"
"ds_read_b64_tr_b16 v[234:235], v241 offset:23040\n\t"
"v_mfma_f32_16x16x32_bf16 a[156:159], a[216:219], a[200:203], a[156:159]\n\t"
"ds_read_b128 a[184:187], v223 offset:27136\n\t"
"v_mfma_f32_16x16x32_bf16 a[156:159], a[212:215], a[196:199], a[156:159]\n\t"
"ds_read_b128 a[188:191], v223 offset:33280\n\t"
"v_mfma_f32_16x16x32_bf16 a[156:159], a[248:251], a[192:195], a[156:159]\n\t"
"ds_read_b128 a[192:195], v222 offset:40960\n\t"
"v_mfma_f32_16x16x32_bf16 a[164:167], a[248:251], a[196:199], a[164:167]\n\t"
"buffer_load_dwordx4 a[248:251], off, s[80:83], s20 offset:1024 sc1\n\t"
"v_mfma_f32_16x16x32_bf16 a[164:167], a[212:215], a[200:203], a[164:167]\n\t"
"s_waitcnt lgkmcnt(8)\n\t"
"ds_read_b128 a[196:199], v223 offset:47104\n\t"
"v_mfma_f32_16x16x32_bf16 a[164:167], a[216:219], a[204:207], a[164:167]\n\t"
"s_waitcnt lgkmcnt(6)\n\t"
"ds_read_b128 a[200:203], v223 offset:53248\n\t"
"v_mfma_f32_16x16x32_bf16 v[224:227], v[224:227], a[208:211], 0\n\t"
"buffer_load_dwordx4 a[212:215], off, s[80:83], s20 offset:2048 sc1\n\t"
"v_mfma_f32_16x16x32_bf16 v[228:231], v[228:231], a[208:211], 0\n\t"
"s_waitcnt vmcnt(9) lgkmcnt(5)\n\t"
"ds_read_b128 a[204:207], v222 offset:60928\n\t"
"s_mov_b32 s20, 1339392\n\t"
"v_mfma_f32_16x16x32_bf16 v[236:239], v[236:239], a[208:211], 0\n\t"
"v_mfma_f32_16x16x32_bf16 v[232:235], v[232:235], a[208:211], 0\n\t"
"buffer_load_dwordx4 a[216:219], off, s[80:83], s20 offset:0 sc1\n\t"
"Lst28_%=:\n\t"
"v_mfma_f32_16x16x32_bf16 v[196:199], a[252:255], a[168:171], v[196:199]\n\t"
"v_mfma_f32_16x16x32_bf16 v[196:199], a[220:223], a[176:179], v[196:199]\n\t"
"v_cvt_pk_bf16_f32 v224, v224, v225\n\t"
"v_cvt_pk_bf16_f32 v225, v226, v227\n\t"
"v_mfma_f32_16x16x32_bf16 v[196:199], a[224:227], a[172:175], v[196:199]\n\t"
"v_cvt_pk_bf16_f32 v226, v228, v229\n\t"
"v_cvt_pk_bf16_f32 v227, v230, v231\n\t"
"v_mfma_f32_16x16x32_bf16 v[204:207], a[252:255], a[172:175], v[204:207]\n\t"
"s_waitcnt lgkmcnt(5)\n\t"
"v_cvt_pk_bf16_f32 v236, v236, v237\n\t"
"v_cvt_pk_bf16_f32 v237, v238, v239\n\t"
"v_mfma_f32_16x16x32_bf16 v[204:207], a[224:227], a[176:179], v[204:207]\n\t"
"v_cvt_pk_bf16_f32 v238, v232, v233\n\t"
"v_cvt_pk_bf16_f32 v239, v234, v235\n\t"
"v_mfma_f32_16x16x32_bf16 v[204:207], a[220:223], a[180:183], v[204:207]\n\t"
"ds_write_b128 v243, v[224:227] offset:512\n\t"
"v_mfma_f32_16x16x32_bf16 v[212:215], a[220:223], a[184:187], v[212:215]\n\t"
"s_waitcnt lgkmcnt(5)\n\t"
"v_mfma_f32_16x16x32_bf16 v[212:215], a[224:227], a[180:183], v[212:215]\n\t"
"v_mfma_f32_16x16x32_bf16 v[212:215], a[252:255], a[176:179], v[212:215]\n\t"
"ds_write_b128 v243, v[236:239] offset:20480\n\t"
"v_mfma_f32_16x16x32_bf16 a[0:3], a[252:255], a[180:183], a[0:3]\n\t"
"s_waitcnt lgkmcnt(5)\n\t"
"v_mfma_f32_16x16x32_bf16 a[0:3], a[224:227], a[184:187], a[0:3]\n\t"
"ds_read_b64_tr_b16 v[228:229], v240 offset:41472\n\t"
"v_mfma_f32_16x16x32_bf16 a[0:3], a[220:223], a[188:191], a[0:3]\n\t"
"ds_read_b64_tr_b16 v[230:231], v240 offset:43008\n\t"
"v_mfma_f32_16x16x32_bf16 a[8:11], a[220:223], a[192:195], a[8:11]\n\t"
"s_waitcnt lgkmcnt(6)\n\t"
"ds_read_b64_tr_b16 v[232:233], v241 offset:41472\n\t"
"v_mfma_f32_16x16x32_bf16 a[8:11], a[224:227], a[188:191], a[8:11]\n\t"
"ds_read_b64_tr_b16 v[234:235], v241 offset:43008\n\t"
"v_add_u32 v240, 59904, v240\n\t"
"v_mfma_f32_16x16x32_bf16 a[8:11], a[252:255], a[184:187], a[8:11]\n\t"
"v_add_u32 v241, 59904, v241\n\t"
"v_mfma_f32_16x16x32_bf16 a[16:19], a[252:255], a[188:191], a[16:19]\n\t"
"s_waitcnt lgkmcnt(7)\n\t"
"ds_read_b64_tr_b16 v[224:225], v240 offset:1536\n\t"
"v_mfma_f32_16x16x32_bf16 a[16:19], a[224:227], a[192:195], a[16:19]\n\t"
"ds_read_b64_tr_b16 v[226:227], v240 offset:3072\n\t"
"v_mfma_f32_16x16x32_bf16 a[16:19], a[220:223], a[196:199], a[16:19]\n\t"
"ds_read_b64_tr_b16 v[236:237], v241 offset:1536\n\t"
"v_mfma_f32_16x16x32_bf16 a[24:27], a[220:223], a[200:203], a[24:27]\n\t"
"s_waitcnt lgkmcnt(9)\n\t"
"ds_read_b64_tr_b16 v[238:239], v241 offset:3072\n\t"
"v_mfma_f32_16x16x32_bf16 a[24:27], a[224:227], a[196:199], a[24:27]\n\t"
"v_mfma_f32_16x16x32_bf16 a[24:27], a[252:255], a[192:195], a[24:27]\n\t"
"v_mfma_f32_16x16x32_bf16 a[32:35], a[252:255], a[196:199], a[32:35]\n\t"
"buffer_load_dwordx4 a[252:255], off, s[80:83], s20 offset:1024 sc1\n\t"
"v_mfma_f32_16x16x32_bf16 a[32:35], a[224:227], a[200:203], a[32:35]\n\t"
"s_waitcnt lgkmcnt(4)\n\t"
"v_mfma_f32_16x16x32_bf16 a[32:35], a[220:223], a[204:207], a[32:35]\n\t"
"s_waitcnt lgkmcnt(2)\n\t"
"v_mfma_f32_16x16x32_bf16 v[228:231], v[228:231], a[208:211], 0\n\t"
"buffer_load_dwordx4 a[220:223], off, s[80:83], s20 offset:2048 sc1\n\t"
"v_mfma_f32_16x16x32_bf16 v[232:235], v[232:235], a[208:211], 0\n\t"
"s_waitcnt vmcnt(9) lgkmcnt(0)\n\t"
"s_mov_b32 s20, 1351680\n\t"
"v_mfma_f32_16x16x32_bf16 v[224:227], v[224:227], a[208:211], 0\n\t"
"v_mfma_f32_16x16x32_bf16 v[236:239], v[236:239], a[208:211], 0\n\t"
"buffer_load_dwordx4 a[224:227], off, s[80:83], s20 offset:0 sc1\n\t"
"v_mfma_f32_16x16x32_bf16 a[40:43], a[228:231], a[168:171], a[40:43]\n\t"
"v_mfma_f32_16x16x32_bf16 a[40:43], v[248:251], a[176:179], a[40:43]\n\t"
"v_cvt_pk_bf16_f32 v228, v228, v229\n\t"
"v_cvt_pk_bf16_f32 v229, v230, v231\n\t"
"v_mfma_f32_16x16x32_bf16 a[40:43], v[96:99], a[172:175], a[40:43]\n\t"
"v_cvt_pk_bf16_f32 v230, v232, v233\n\t"
"v_cvt_pk_bf16_f32 v231, v234, v235\n\t"
"v_mfma_f32_16x16x32_bf16 a[48:51], a[228:231], a[172:175], a[48:51]\n\t"
"v_cvt_pk_bf16_f32 v224, v224, v225\n\t"
"v_cvt_pk_bf16_f32 v225, v226, v227\n\t"
"v_mfma_f32_16x16x32_bf16 a[48:51], v[96:99], a[176:179], a[48:51]\n\t"
"v_cvt_pk_bf16_f32 v226, v236, v237\n\t"
"v_cvt_pk_bf16_f32 v227, v238, v239\n\t"
"v_mfma_f32_16x16x32_bf16 a[48:51], v[248:251], a[180:183], a[48:51]\n\t"
"ds_write_b128 v243, v[228:231] offset:40448\n\t"
"v_mfma_f32_16x16x32_bf16 a[56:59], v[248:251], a[184:187], a[56:59]\n\t"
"v_add_u32 v243, 59904, v243\n\t"
"v_mfma_f32_16x16x32_bf16 a[56:59], v[96:99], a[180:183], a[56:59]\n\t"
"v_mfma_f32_16x16x32_bf16 a[56:59], a[228:231], a[176:179], a[56:59]\n\t"
"ds_write_b128 v243, v[224:227] offset:512\n\t"
"v_mfma_f32_16x16x32_bf16 a[64:67], a[228:231], a[180:183], a[64:67]\n\t"
"v_mfma_f32_16x16x32_bf16 a[64:67], v[96:99], a[184:187], a[64:67]\n\t"
"ds_read_b64_tr_b16 v[232:233], v240 offset:21504\n\t"
"v_mfma_f32_16x16x32_bf16 a[64:67], v[248:251], a[188:191], a[64:67]\n\t"
"ds_read_b64_tr_b16 v[234:235], v240 offset:23040\n\t"
"v_mfma_f32_16x16x32_bf16 a[72:75], v[248:251], a[192:195], a[72:75]\n\t"
"ds_read_b64_tr_b16 v[236:237], v241 offset:21504\n\t"
"v_mfma_f32_16x16x32_bf16 a[72:75], v[96:99], a[188:191], a[72:75]\n\t"
"ds_read_b64_tr_b16 v[238:239], v241 offset:23040\n\t"
"v_mfma_f32_16x16x32_bf16 a[72:75], a[228:231], a[184:187], a[72:75]\n\t"
"ds_read_b64_tr_b16 v[228:229], v240 offset:41472\n\t"
"v_mfma_f32_16x16x32_bf16 a[80:83], a[228:231], a[188:191], a[80:83]\n\t"
"ds_read_b64_tr_b16 v[230:231], v240 offset:43008\n\t"
"v_mfma_f32_16x16x32_bf16 a[80:83], v[96:99], a[192:195], a[80:83]\n\t"
"ds_read_b64_tr_b16 v[224:225], v241 offset:41472\n\t"
"v_mfma_f32_16x16x32_bf16 a[80:83], v[248:251], a[196:199], a[80:83]\n\t"
"ds_read_b64_tr_b16 v[226:227], v241 offset:43008\n\t"
"v_mfma_f32_16x16x32_bf16 a[88:91], v[248:251], a[200:203], a[88:91]\n\t"
"v_mfma_f32_16x16x32_bf16 a[88:91], v[96:99], a[196:199], a[88:91]\n\t"
"v_mfma_f32_16x16x32_bf16 a[88:91], a[228:231], a[192:195], a[88:91]\n\t"
"v_mfma_f32_16x16x32_bf16 a[96:99], a[228:231], a[196:199], a[96:99]\n\t"
"buffer_load_dwordx4 a[228:231], off, s[80:83], s20 offset:1024 sc1\n\t"
"v_mfma_f32_16x16x32_bf16 a[96:99], v[96:99], a[200:203], a[96:99]\n\t"
"s_waitcnt lgkmcnt(4)\n\t"
"v_mfma_f32_16x16x32_bf16 a[96:99], v[248:251], a[204:207], a[96:99]\n\t"
"s_waitcnt lgkmcnt(2)\n\t"
"v_mfma_f32_16x16x32_bf16 v[232:235], v[232:235], a[208:211], 0\n\t"
"buffer_load_dwordx4 v[96:99], off, s[80:83], s20 offset:2048 sc1\n\t"
"v_mfma_f32_16x16x32_bf16 v[236:239], v[236:239], a[208:211], 0\n\t"
"s_waitcnt vmcnt(9) lgkmcnt(0)\n\t"
"s_mov_b32 s20, 1216512\n\t"
"v_mfma_f32_16x16x32_bf16 v[228:231], v[228:231], a[208:211], 0\n\t"
"v_mfma_f32_16x16x32_bf16 v[224:227], v[224:227], a[208:211], 0\n\t"
"buffer_load_dwordx4 v[248:251], off, s[80:83], s20 offset:0 sc1\n\t"
"v_mfma_f32_16x16x32_bf16 a[104:107], a[232:235], a[168:171], a[104:107]\n\t"
"ds_read_b128 a[168:171], v222 offset:1280\n\t"
"v_mfma_f32_16x16x32_bf16 a[104:107], a[236:239], a[176:179], a[104:107]\n\t"
"v_cvt_pk_bf16_f32 v232, v232, v233\n\t"
"v_cvt_pk_bf16_f32 v233, v234, v235\n\t"
"v_mfma_f32_16x16x32_bf16 a[104:107], a[240:243], a[172:175], a[104:107]\n\t"
"v_cvt_pk_bf16_f32 v234, v236, v237\n\t"
"v_cvt_pk_bf16_f32 v235, v238, v239\n\t"
"v_mfma_f32_16x16x32_bf16 a[112:115], a[232:235], a[172:175], a[112:115]\n\t"
"ds_read_b128 a[172:175], v223 offset:7424\n\t"
"v_cvt_pk_bf16_f32 v228, v228, v229\n\t"
"v_mfma_f32_16x16x32_bf16 a[112:115], a[240:243], a[176:179], a[112:115]\n\t"
"v_cvt_pk_bf16_f32 v229, v230, v231\n\t"
"v_cvt_pk_bf16_f32 v230, v224, v225\n\t"
"v_mfma_f32_16x16x32_bf16 a[112:115], a[236:239], a[180:183], a[112:115]\n\t"
"buffer_load_dwordx4 a[208:211], off, s[80:83], s20 offset:1024 sc1\n\t"
"v_mfma_f32_16x16x32_bf16 a[120:123], a[236:239], a[184:187], a[120:123]\n\t"
"v_cvt_pk_bf16_f32 v231, v226, v227\n\t"
"v_mfma_f32_16x16x32_bf16 a[120:123], a[240:243], a[180:183], a[120:123]\n\t"
"v_mfma_f32_16x16x32_bf16 a[120:123], a[232:235], a[176:179], a[120:123]\n\t"
"ds_write_b128 v243, v[232:235] offset:20480\n\t"
"v_mfma_f32_16x16x32_bf16 a[128:131], a[232:235], a[180:183], a[128:131]\n\t"
"v_mfma_f32_16x16x32_bf16 a[128:131], a[240:243], a[184:187], a[128:131]\n\t"
"ds_read_b128 a[176:179], v223 offset:13568\n\t"
"v_mfma_f32_16x16x32_bf16 a[128:131], a[236:239], a[188:191], a[128:131]\n\t"
"ds_write_b128 v243, v[228:231] offset:40448\n\t"
"v_mfma_f32_16x16x32_bf16 a[136:139], a[236:239], a[192:195], a[136:139]\n\t"
"buffer_load_dwordx4 v[240:243], off, s[80:83], s20 offset:2048 sc1\n\t"
"v_mfma_f32_16x16x32_bf16 a[136:139], a[240:243], a[188:191], a[136:139]\n\t"
"ds_read_b128 a[180:183], v222 offset:21248\n\t"
"v_mfma_f32_16x16x32_bf16 a[136:139], a[232:235], a[184:187], a[136:139]\n\t"
"ds_read_b128 a[184:187], v223 offset:27392\n\t"
"v_mfma_f32_16x16x32_bf16 a[144:147], a[232:235], a[188:191], a[144:147]\n\t"
"s_mov_b32 s20, 1228800\n\t"
"ds_read_b128 a[188:191], v223 offset:33536\n\t"
"v_mfma_f32_16x16x32_bf16 a[144:147], a[240:243], a[192:195], a[144:147]\n\t"
"v_mfma_f32_16x16x32_bf16 a[144:147], a[236:239], a[196:199], a[144:147]\n\t"
"v_mfma_f32_16x16x32_bf16 a[152:155], a[236:239], a[200:203], a[152:155]\n\t"
"v_mfma_f32_16x16x32_bf16 a[152:155], a[240:243], a[196:199], a[152:155]\n\t"
"v_mfma_f32_16x16x32_bf16 a[152:155], a[232:235], a[192:195], a[152:155]\n\t"
"ds_read_b128 a[192:195], v222 offset:41216\n\t"
"v_mfma_f32_16x16x32_bf16 a[160:163], a[232:235], a[196:199], a[160:163]\n\t"
"buffer_load_dwordx4 a[232:235], off, s[80:83], s20 offset:0 sc1\n\t"
"v_mfma_f32_16x16x32_bf16 a[160:163], a[240:243], a[200:203], a[160:163]\n\t"
"s_waitcnt vmcnt(10) lgkmcnt(5)\n\t"
"ds_read_b128 a[196:199], v223 offset:47360\n\t"
"v_mfma_f32_16x16x32_bf16 a[160:163], a[236:239], a[204:207], a[160:163]\n\t"
"ds_read_b128 a[200:203], v223 offset:53504\n\t"
"v_mfma_f32_16x16x32_bf16 v[200:203], a[244:247], a[168:171], v[200:203]\n\t"
"s_waitcnt lgkmcnt(5)\n\t"
"ds_read_b128 a[204:207], v222 offset:61184\n\t"
"v_mfma_f32_16x16x32_bf16 v[200:203], a[212:215], a[176:179], v[200:203]\n\t"
"buffer_load_dwordx4 a[236:239], off, s[80:83], s20 offset:1024 sc1\n\t"
"v_mfma_f32_16x16x32_bf16 v[200:203], a[248:251], a[172:175], v[200:203]\n\t"
"v_mfma_f32_16x16x32_bf16 v[208:211], a[244:247], a[172:175], v[208:211]\n\t"
"buffer_load_dwordx4 a[240:243], off, s[80:83], s20 offset:2048 sc1\n\t"
"v_mfma_f32_16x16x32_bf16 v[208:211], a[248:251], a[176:179], v[208:211]\n\t"
"s_waitcnt lgkmcnt(5)\n\t"
"v_mfma_f32_16x16x32_bf16 v[208:211], a[212:215], a[180:183], v[208:211]\n\t"
"s_waitcnt lgkmcnt(0)\n\t"
"s_barrier\n\t"
"v_mfma_f32_16x16x32_bf16 v[216:219], a[212:215], a[184:187], v[216:219]\n\t"
"v_mfma_f32_16x16x32_bf16 v[216:219], a[248:251], a[180:183], v[216:219]\n\t"
"v_mfma_f32_16x16x32_bf16 v[216:219], a[244:247], a[176:179], v[216:219]\n\t"
"ds_read_b64_tr_b16 v[236:237], v246 offset:1536\n\t"
"v_mfma_f32_16x16x32_bf16 a[4:7], a[244:247], a[180:183], a[4:7]\n\t"
"ds_read_b64_tr_b16 v[224:225], v255 offset:1536\n\t"
"v_mfma_f32_16x16x32_bf16 a[4:7], a[248:251], a[184:187], a[4:7]\n\t"
"ds_read_b64_tr_b16 v[232:233], v246 offset:7680\n\t"
"v_mfma_f32_16x16x32_bf16 a[4:7], a[212:215], a[188:191], a[4:7]\n\t"
"ds_read_b64_tr_b16 v[228:229], v255 offset:7680\n\t"
"v_mfma_f32_16x16x32_bf16 a[12:15], a[212:215], a[192:195], a[12:15]\n\t"
"v_mfma_f32_16x16x32_bf16 a[12:15], a[248:251], a[188:191], a[12:15]\n\t"
"v_mfma_f32_16x16x32_bf16 a[12:15], a[244:247], a[184:187], a[12:15]\n\t"
"v_mfma_f32_16x16x32_bf16 a[20:23], a[244:247], a[188:191], a[20:23]\n\t"
"s_mov_b32 s20, 1241088\n\t"
"v_mfma_f32_16x16x32_bf16 a[20:23], a[248:251], a[192:195], a[20:23]\n\t"
"v_mfma_f32_16x16x32_bf16 a[20:23], a[212:215], a[196:199], a[20:23]\n\t"
"v_mfma_f32_16x16x32_bf16 a[28:31], a[212:215], a[200:203], a[28:31]\n\t"
"v_mfma_f32_16x16x32_bf16 a[28:31], a[248:251], a[196:199], a[28:31]\n\t"
"v_mfma_f32_16x16x32_bf16 a[28:31], a[244:247], a[192:195], a[28:31]\n\t"
"v_mfma_f32_16x16x32_bf16 a[36:39], a[244:247], a[196:199], a[36:39]\n\t"
"buffer_load_dwordx4 a[244:247], off, s[80:83], s20 offset:0 sc1\n\t"
"v_mfma_f32_16x16x32_bf16 a[36:39], a[248:251], a[200:203], a[36:39]\n\t"
"s_waitcnt lgkmcnt(2)\n\t"
"v_mfma_f32_16x16x32_bf16 a[36:39], a[212:215], a[204:207], a[36:39]\n\t"
"s_waitcnt lgkmcnt(1)\n\t"
"v_mfma_f32_4x4x4_16b_bf16 v[236:239], v[236:237], v[220:221], 0\n\t"
"s_waitcnt lgkmcnt(0)\n\t"
"v_mfma_f32_4x4x4_16b_bf16 v[224:227], v[224:225], v[220:221], 0\n\t"
"s_waitcnt vmcnt(10)\n\t"
"v_mfma_f32_4x4x4_16b_bf16 v[232:235], v[232:233], v[220:221], 0\n\t"
"v_mfma_f32_4x4x4_16b_bf16 v[228:231], v[228:229], v[220:221], 0\n\t"
"buffer_load_dwordx4 a[212:215], off, s[80:83], s20 offset:1024 sc1\n\t"
"v_mfma_f32_16x16x32_bf16 a[44:47], a[216:219], a[168:171], a[44:47]\n\t"
"buffer_load_dwordx4 a[248:251], off, s[80:83], s20 offset:2048 sc1\n\t"
"v_mfma_f32_16x16x32_bf16 a[44:47], a[220:223], a[176:179], a[44:47]\n\t"
"v_cvt_pk_bf16_f32 v236, v236, v237\n\t"
"v_cvt_pk_bf16_f32 v237, v238, v239\n\t"
"v_mfma_f32_16x16x32_bf16 a[44:47], a[252:255], a[172:175], a[44:47]\n\t"
"v_cvt_pk_bf16_f32 v238, v224, v225\n\t"
"v_cvt_pk_bf16_f32 v239, v226, v227\n\t"
"v_mfma_f32_16x16x32_bf16 a[52:55], a[216:219], a[172:175], a[52:55]\n\t"
"v_cvt_pk_bf16_f32 v232, v232, v233\n\t"
"v_cvt_pk_bf16_f32 v233, v234, v235\n\t"
"v_mfma_f32_16x16x32_bf16 a[52:55], a[252:255], a[176:179], a[52:55]\n\t"
"v_cvt_pk_bf16_f32 v234, v228, v229\n\t"
"v_cvt_pk_bf16_f32 v235, v230, v231\n\t"
"v_mfma_f32_16x16x32_bf16 a[52:55], a[220:223], a[180:183], a[52:55]\n\t"
"ds_write_b128 v245, v[236:239] offset:1536\n\t"
"v_mfma_f32_16x16x32_bf16 a[60:63], a[220:223], a[184:187], a[60:63]\n\t"
"v_mfma_f32_16x16x32_bf16 a[60:63], a[252:255], a[180:183], a[60:63]\n\t"
"v_mfma_f32_16x16x32_bf16 a[60:63], a[216:219], a[176:179], a[60:63]\n\t"
"ds_write_b128 v245, v[232:235] offset:7680\n\t"
"v_mfma_f32_16x16x32_bf16 a[68:71], a[216:219], a[180:183], a[68:71]\n\t"
"v_mfma_f32_16x16x32_bf16 a[68:71], a[252:255], a[184:187], a[68:71]\n\t"
"ds_read_b64_tr_b16 v[224:225], v246 offset:13824\n\t"
"v_mfma_f32_16x16x32_bf16 a[68:71], a[220:223], a[188:191], a[68:71]\n\t"
"ds_read_b64_tr_b16 v[228:229], v255 offset:13824\n\t"
"v_mfma_f32_16x16x32_bf16 a[76:79], a[220:223], a[192:195], a[76:79]\n\t"
"v_mfma_f32_16x16x32_bf16 a[76:79], a[252:255], a[188:191], a[76:79]\n\t"
"v_mfma_f32_16x16x32_bf16 a[76:79], a[216:219], a[184:187], a[76:79]\n\t"
"v_mfma_f32_16x16x32_bf16 a[84:87], a[216:219], a[188:191], a[84:87]\n\t"
"s_mov_b32 s20, 1253376\n\t"
"v_mfma_f32_16x16x32_bf16 a[84:87], a[252:255], a[192:195], a[84:87]\n\t"
"v_mfma_f32_16x16x32_bf16 a[84:87], a[220:223], a[196:199], a[84:87]\n\t"
"v_mfma_f32_16x16x32_bf16 a[92:95], a[220:223], a[200:203], a[92:95]\n\t"
"v_mfma_f32_16x16x32_bf16 a[92:95], a[252:255], a[196:199], a[92:95]\n\t"
"v_mfma_f32_16x16x32_bf16 a[92:95], a[216:219], a[192:195], a[92:95]\n\t"
"v_mfma_f32_16x16x32_bf16 a[100:103], a[216:219], a[196:199], a[100:103]\n\t"
"buffer_load_dwordx4 a[216:219], off, s[80:83], s20 offset:0 sc1\n\t"
"v_mfma_f32_16x16x32_bf16 a[100:103], a[252:255], a[200:203], a[100:103]\n\t"
"s_waitcnt lgkmcnt(0)\n\t"
"v_mfma_f32_16x16x32_bf16 a[100:103], a[220:223], a[204:207], a[100:103]\n\t"
"s_waitcnt vmcnt(10)\n\t"
"v_mfma_f32_4x4x4_16b_bf16 v[224:227], v[224:225], v[220:221], 0\n\t"
"v_mfma_f32_4x4x4_16b_bf16 v[228:231], v[228:229], v[220:221], 0\n\t"
"v_mfma_f32_16x16x32_bf16 a[108:111], a[224:227], a[168:171], a[108:111]\n\t"
"buffer_load_dwordx4 a[220:223], off, s[80:83], s20 offset:1024 sc1\n\t"
"v_mfma_f32_16x16x32_bf16 a[108:111], v[96:99], a[176:179], a[108:111]\n\t"
"buffer_load_dwordx4 a[252:255], off, s[80:83], s20 offset:2048 sc1\n\t"
"v_mfma_f32_16x16x32_bf16 a[108:111], a[228:231], a[172:175], a[108:111]\n\t"
"ds_read_b128 a[168:171], v222 offset:2560\n\t"
"v_cvt_pk_bf16_f32 v224, v224, v225\n\t"
"v_mfma_f32_16x16x32_bf16 a[116:119], a[224:227], a[172:175], a[116:119]\n\t"
"ds_read_b128 a[172:175], v223 offset:8704\n\t"
"v_cvt_pk_bf16_f32 v225, v226, v227\n\t"
"v_mfma_f32_16x16x32_bf16 a[116:119], a[228:231], a[176:179], a[116:119]\n\t"
"v_cvt_pk_bf16_f32 v226, v228, v229\n\t"
"v_cvt_pk_bf16_f32 v227, v230, v231\n\t"
"v_mfma_f32_16x16x32_bf16 a[116:119], v[96:99], a[180:183], a[116:119]\n\t"
"ds_write_b128 v245, v[224:227] offset:13824\n\t"
"v_mfma_f32_16x16x32_bf16 a[124:127], v[96:99], a[184:187], a[124:127]\n\t"
"v_mfma_f32_16x16x32_bf16 a[124:127], a[228:231], a[180:183], a[124:127]\n\t"
"v_mfma_f32_16x16x32_bf16 a[124:127], a[224:227], a[176:179], a[124:127]\n\t"
"ds_read_b128 a[176:179], v223 offset:14848\n\t"
"v_mfma_f32_16x16x32_bf16 a[132:135], a[224:227], a[180:183], a[132:135]\n\t"
"ds_read_b128 a[180:183], v222 offset:22528\n\t"
"v_mfma_f32_16x16x32_bf16 a[132:135], a[228:231], a[184:187], a[132:135]\n\t"
"ds_read_b64_tr_b16 v[236:237], v246 offset:21504\n\t"
"v_mfma_f32_16x16x32_bf16 a[132:135], v[96:99], a[188:191], a[132:135]\n\t"
"ds_read_b64_tr_b16 v[232:233], v255 offset:21504\n\t"
"v_mfma_f32_16x16x32_bf16 a[140:143], v[96:99], a[192:195], a[140:143]\n\t"
"v_mfma_f32_16x16x32_bf16 a[140:143], a[228:231], a[188:191], a[140:143]\n\t"
"v_mfma_f32_16x16x32_bf16 a[140:143], a[224:227], a[184:187], a[140:143]\n\t"
"ds_read_b128 a[184:187], v223 offset:28672\n\t"
"v_mfma_f32_16x16x32_bf16 a[148:151], a[224:227], a[188:191], a[148:151]\n\t"
"s_mov_b32 s20, 1265664\n\t"
"ds_read_b128 a[188:191], v223 offset:34816\n\t"
"v_mfma_f32_16x16x32_bf16 a[148:151], a[228:231], a[192:195], a[148:151]\n\t"
"v_mfma_f32_16x16x32_bf16 a[148:151], v[96:99], a[196:199], a[148:151]\n\t"
"v_mfma_f32_16x16x32_bf16 a[156:159], v[96:99], a[200:203], a[156:159]\n\t"
"v_mfma_f32_16x16x32_bf16 a[156:159], a[228:231], a[196:199], a[156:159]\n\t"
"v_mfma_f32_16x16x32_bf16 a[156:159], a[224:227], a[192:195], a[156:159]\n\t"
"ds_read_b128 a[192:195], v222 offset:42496\n\t"
"v_mfma_f32_16x16x32_bf16 a[164:167], a[224:227], a[196:199], a[164:167]\n\t"
"buffer_load_dwordx4 a[224:227], off, s[80:83], s20 offset:0 sc1\n\t"
"v_mfma_f32_16x16x32_bf16 a[164:167], a[228:231], a[200:203], a[164:167]\n\t"
"s_waitcnt lgkmcnt(3)\n\t"
"ds_read_b128 a[196:199], v223 offset:48640\n\t"
"v_mfma_f32_16x16x32_bf16 a[164:167], v[96:99], a[204:207], a[164:167]\n\t"
"s_waitcnt vmcnt(10)\n\t"
"ds_read_b128 a[200:203], v223 offset:54784\n\t"
"v_mfma_f32_4x4x4_16b_bf16 v[236:239], v[236:237], v[220:221], 0\n\t"
"v_mfma_f32_4x4x4_16b_bf16 v[232:235], v[232:233], v[220:221], 0\n\t"
"Lst29_%=:\n\t"
"v_mfma_f32_16x16x32_bf16 v[0:3], v[248:251], a[168:171], v[0:3]\n\t"
"buffer_load_dwordx4 v[96:99], off, s[80:83], s20 offset:1024 sc1\n\t"
"v_mfma_f32_16x16x32_bf16 v[0:3], v[240:243], a[176:179], v[0:3]\n\t"
"buffer_load_dwordx4 a[228:231], off, s[80:83], s20 offset:2048 sc1\n\t"
"v_mfma_f32_16x16x32_bf16 v[0:3], a[208:211], a[172:175], v[0:3]\n\t"
"ds_read_b128 a[204:207], v222 offset:62464\n\t"
"v_cvt_pk_bf16_f32 v236, v236, v237\n\t"
"v_mfma_f32_16x16x32_bf16 v[4:7], v[248:251], a[172:175], v[4:7]\n\t"
"s_waitcnt lgkmcnt(5)\n\t"
"v_cvt_pk_bf16_f32 v237, v238, v239\n\t"
"v_cvt_pk_bf16_f32 v238, v232, v233\n\t"
"v_mfma_f32_16x16x32_bf16 v[4:7], a[208:211], a[176:179], v[4:7]\n\t"
"v_cvt_pk_bf16_f32 v239, v234, v235\n\t"
"v_mfma_f32_16x16x32_bf16 v[4:7], v[240:243], a[180:183], v[4:7]\n\t"
"ds_write_b128 v245, v[236:239] offset:21504\n\t"
"v_mfma_f32_16x16x32_bf16 v[8:11], v[240:243], a[184:187], v[8:11]\n\t"
"s_waitcnt lgkmcnt(5)\n\t"
"v_mfma_f32_16x16x32_bf16 v[8:11], a[208:211], a[180:183], v[8:11]\n\t"
"v_mfma_f32_16x16x32_bf16 v[8:11], v[248:251], a[176:179], v[8:11]\n\t"
"ds_read_b64_tr_b16 v[228:229], v246 offset:27648\n\t"
"v_mfma_f32_16x16x32_bf16 v[12:15], v[248:251], a[180:183], v[12:15]\n\t"
"s_waitcnt lgkmcnt(5)\n\t"
"ds_read_b64_tr_b16 v[224:225], v255 offset:27648\n\t"
"v_mfma_f32_16x16x32_bf16 v[12:15], a[208:211], a[184:187], v[12:15]\n\t"
"v_mfma_f32_16x16x32_bf16 v[12:15], v[240:243], a[188:191], v[12:15]\n\t"
"v_mfma_f32_16x16x32_bf16 v[16:19], v[240:243], a[192:195], v[16:19]\n\t"
"s_waitcnt lgkmcnt(5)\n\t"
"v_mfma_f32_16x16x32_bf16 v[16:19], a[208:211], a[188:191], v[16:19]\n\t"
"v_mfma_f32_16x16x32_bf16 v[16:19], v[248:251], a[184:187], v[16:19]\n\t"
"v_mfma_f32_16x16x32_bf16 v[20:23], v[248:251], a[188:191], v[20:23]\n\t"
"s_waitcnt lgkmcnt(4)\n\t"
"s_mov_b32 s20, 1277952\n\t"
"v_mfma_f32_16x16x32_bf16 v[20:23], a[208:211], a[192:195], v[20:23]\n\t"
"v_mfma_f32_16x16x32_bf16 v[20:23], v[240:243], a[196:199], v[20:23]\n\t"
"v_mfma_f32_16x16x32_bf16 v[24:27], v[240:243], a[200:203], v[24:27]\n\t"
"s_waitcnt lgkmcnt(3)\n\t"
"v_mfma_f32_16x16x32_bf16 v[24:27], a[208:211], a[196:199], v[24:27]\n\t"
"v_mfma_f32_16x16x32_bf16 v[24:27], v[248:251], a[192:195], v[24:27]\n\t"
"v_mfma_f32_16x16x32_bf16 v[28:31], v[248:251], a[196:199], v[28:31]\n\t"
"buffer_load_dwordx4 v[248:251], off, s[80:83], s20 offset:0 sc1\n\t"
"v_mfma_f32_16x16x32_bf16 v[28:31], a[208:211], a[200:203], v[28:31]\n\t"
"s_waitcnt lgkmcnt(0)\n\t"
"v_mfma_f32_16x16x32_bf16 v[28:31], v[240:243], a[204:207], v[28:31]\n\t"
"s_waitcnt vmcnt(10)\n\t"
"v_mfma_f32_4x4x4_16b_bf16 v[228:231], v[228:229], v[220:221], 0\n\t"
"v_mfma_f32_4x4x4_16b_bf16 v[224:227], v[224:225], v[220:221], 0\n\t"
"v_mfma_f32_16x16x32_bf16 v[32:35], a[232:235], a[168:171], v[32:35]\n\t"
"buffer_load_dwordx4 a[208:211], off, s[80:83], s20 offset:1024 sc1\n\t"
"v_mfma_f32_16x16x32_bf16 v[32:35], a[240:243], a[176:179], v[32:35]\n\t"
"buffer_load_dwordx4 v[240:243], off, s[80:83], s20 offset:2048 sc1\n\t"
"v_mfma_f32_16x16x32_bf16 v[32:35], a[236:239], a[172:175], v[32:35]\n\t"
"v_cvt_pk_bf16_f32 v228, v228, v229\n\t"
"v_cvt_pk_bf16_f32 v229, v230, v231\n\t"
"v_mfma_f32_16x16x32_bf16 v[36:39], a[232:235], a[172:175], v[36:39]\n\t"
"v_cvt_pk_bf16_f32 v230, v224, v225\n\t"
"v_cvt_pk_bf16_f32 v231, v226, v227\n\t"
"v_mfma_f32_16x16x32_bf16 v[36:39], a[236:239], a[176:179], v[36:39]\n\t"
"v_mfma_f32_16x16x32_bf16 v[36:39], a[240:243], a[180:183], v[36:39]\n\t"
"ds_write_b128 v245, v[228:231] offset:27648\n\t"
"v_mfma_f32_16x16x32_bf16 v[40:43], a[240:243], a[184:187], v[40:43]\n\t"
"v_mfma_f32_16x16x32_bf16 v[40:43], a[236:239], a[180:183], v[40:43]\n\t"
"v_mfma_f32_16x16x32_bf16 v[40:43], a[232:235], a[176:179], v[40:43]\n\t"
"ds_read_b64_tr_b16 v[232:233], v246 offset:33792\n\t"
"v_mfma_f32_16x16x32_bf16 v[44:47], a[232:235], a[180:183], v[44:47]\n\t"
"ds_read_b64_tr_b16 v[236:237], v255 offset:33792\n\t"
"v_mfma_f32_16x16x32_bf16 v[44:47], a[236:239], a[184:187], v[44:47]\n\t"
"v_mfma_f32_16x16x32_bf16 v[44:47], a[240:243], a[188:191], v[44:47]\n\t"
"v_mfma_f32_16x16x32_bf16 v[48:51], a[240:243], a[192:195], v[48:51]\n\t"
"v_mfma_f32_16x16x32_bf16 v[48:51], a[236:239], a[188:191], v[48:51]\n\t"
"v_mfma_f32_16x16x32_bf16 v[48:51], a[232:235], a[184:187], v[48:51]\n\t"
"v_mfma_f32_16x16x32_bf16 v[52:55], a[232:235], a[188:191], v[52:55]\n\t"
"s_mov_b32 s20, 1511424\n\t"
"v_mfma_f32_16x16x32_bf16 v[52:55], a[236:239], a[192:195], v[52:55]\n\t"
"v_mfma_f32_16x16x32_bf16 v[52:55], a[240:243], a[196:199], v[52:55]\n\t"
"v_mfma_f32_16x16x32_bf16 v[56:59], a[240:243], a[200:203], v[56:59]\n\t"
"v_mfma_f32_16x16x32_bf16 v[56:59], a[236:239], a[196:199], v[56:59]\n\t"
"v_mfma_f32_16x16x32_bf16 v[56:59], a[232:235], a[192:195], v[56:59]\n\t"
"v_mfma_f32_16x16x32_bf16 v[60:63], a[232:235], a[196:199], v[60:63]\n\t"
"buffer_load_dwordx4 a[232:235], off, s[80:83], s20 offset:0 sc1\n\t"
"v_mfma_f32_16x16x32_bf16 v[60:63], a[236:239], a[200:203], v[60:63]\n\t"
"s_waitcnt lgkmcnt(0)\n\t"
"v_mfma_f32_16x16x32_bf16 v[60:63], a[240:243], a[204:207], v[60:63]\n\t"
"s_waitcnt vmcnt(10)\n\t"
"v_mfma_f32_4x4x4_16b_bf16 v[232:235], v[232:233], v[220:221], 0\n\t"
"v_mfma_f32_4x4x4_16b_bf16 v[236:239], v[236:237], v[220:221], 0\n\t"
"v_mfma_f32_16x16x32_bf16 v[64:67], a[244:247], a[168:171], v[64:67]\n\t"
"buffer_load_dwordx4 a[236:239], off, s[80:83], s20 offset:1024 sc1\n\t"
"v_mfma_f32_16x16x32_bf16 v[64:67], a[248:251], a[176:179], v[64:67]\n\t"
"buffer_load_dwordx4 a[240:243], off, s[80:83], s20 offset:2048 sc1\n\t"
"v_mfma_f32_16x16x32_bf16 v[64:67], a[212:215], a[172:175], v[64:67]\n\t"
"ds_read_b128 a[168:171], v222 offset:2816\n\t"
"v_cvt_pk_bf16_f32 v232, v232, v233\n\t"
"v_mfma_f32_16x16x32_bf16 v[68:71], a[244:247], a[172:175], v[68:71]\n\t"
"ds_read_b128 a[172:175], v223 offset:8960\n\t"
"v_cvt_pk_bf16_f32 v233, v234, v235\n\t"
"v_mfma_f32_16x16x32_bf16 v[68:71], a[212:215], a[176:179], v[68:71]\n\t"
"v_cvt_pk_bf16_f32 v234, v236, v237\n\t"
"v_cvt_pk_bf16_f32 v235, v238, v239\n\t"
"v_mfma_f32_16x16x32_bf16 v[68:71], a[248:251], a[180:183], v[68:71]\n\t"
"ds_write_b128 v245, v[232:235] offset:33792\n\t"
"v_mfma_f32_16x16x32_bf16 v[72:75], a[248:251], a[184:187], v[72:75]\n\t"
"v_mfma_f32_16x16x32_bf16 v[72:75], a[212:215], a[180:183], v[72:75]\n\t"
"v_mfma_f32_16x16x32_bf16 v[72:75], a[244:247], a[176:179], v[72:75]\n\t"
"ds_read_b128 a[176:179], v223 offset:15104\n\t"
"v_mfma_f32_16x16x32_bf16 v[76:79], a[244:247], a[180:183], v[76:79]\n\t"
"ds_read_b128 a[180:183], v222 offset:22784\n\t"
"v_mfma_f32_16x16x32_bf16 v[76:79], a[212:215], a[184:187], v[76:79]\n\t"
"ds_read_b64_tr_b16 v[224:225], v246 offset:41472\n\t"
"v_mfma_f32_16x16x32_bf16 v[76:79], a[248:251], a[188:191], v[76:79]\n\t"
"ds_read_b64_tr_b16 v[228:229], v255 offset:41472\n\t"
"v_mfma_f32_16x16x32_bf16 v[80:83], a[248:251], a[192:195], v[80:83]\n\t"
"v_mfma_f32_16x16x32_bf16 v[80:83], a[212:215], a[188:191], v[80:83]\n\t"
"v_mfma_f32_16x16x32_bf16 v[80:83], a[244:247], a[184:187], v[80:83]\n\t"
"ds_read_b128 a[184:187], v223 offset:28928\n\t"
"v_mfma_f32_16x16x32_bf16 v[84:87], a[244:247], a[188:191], v[84:87]\n\t"
"s_mov_b32 s20, 1523712\n\t"
"ds_read_b128 a[188:191], v223 offset:35072\n\t"
"v_mfma_f32_16x16x32_bf16 v[84:87], a[212:215], a[192:195], v[84:87]\n\t"
"v_mfma_f32_16x16x32_bf16 v[84:87], a[248:251], a[196:199], v[84:87]\n\t"
"v_mfma_f32_16x16x32_bf16 v[88:91], a[248:251], a[200:203], v[88:91]\n\t"
"v_mfma_f32_16x16x32_bf16 v[88:91], a[212:215], a[196:199], v[88:91]\n\t"
"v_mfma_f32_16x16x32_bf16 v[88:91], a[244:247], a[192:195], v[88:91]\n\t"
"ds_read_b128 a[192:195], v222 offset:42752\n\t"
"v_mfma_f32_16x16x32_bf16 v[92:95], a[244:247], a[196:199], v[92:95]\n\t"
"buffer_load_dwordx4 a[244:247], off, s[80:83], s20 offset:0 sc1\n\t"
"v_mfma_f32_16x16x32_bf16 v[92:95], a[212:215], a[200:203], v[92:95]\n\t"
"s_waitcnt lgkmcnt(3)\n\t"
"ds_read_b128 a[196:199], v223 offset:48896\n\t"
"v_mfma_f32_16x16x32_bf16 v[92:95], a[248:251], a[204:207], v[92:95]\n\t"
"s_waitcnt vmcnt(10)\n\t"
"ds_read_b128 a[200:203], v223 offset:55040\n\t"
"v_mfma_f32_4x4x4_16b_bf16 v[224:227], v[224:225], v[220:221], 0\n\t"
"v_mfma_f32_4x4x4_16b_bf16 v[228:231], v[228:229], v[220:221], 0\n\t"
"v_mfma_f32_16x16x32_bf16 v[100:103], a[216:219], a[168:171], v[100:103]\n\t"
"buffer_load_dwordx4 a[212:215], off, s[80:83], s20 offset:1024 sc1\n\t"
"v_mfma_f32_16x16x32_bf16 v[100:103], a[252:255], a[176:179], v[100:103]\n\t"
"buffer_load_dwordx4 a[248:251], off, s[80:83], s20 offset:2048 sc1\n\t"
"v_mfma_f32_16x16x32_bf16 v[100:103], a[220:223], a[172:175], v[100:103]\n\t"
"ds_read_b128 a[204:207], v222 offset:62720\n\t"
"v_cvt_pk_bf16_f32 v224, v224, v225\n\t"
"v_mfma_f32_16x16x32_bf16 v[104:107], a[216:219], a[172:175], v[104:107]\n\t"
"s_waitcnt lgkmcnt(5)\n\t"
"v_cvt_pk_bf16_f32 v225, v226, v227\n\t"
"v_cvt_pk_bf16_f32 v226, v228, v229\n\t"
"v_mfma_f32_16x16x32_bf16 v[104:107], a[220:223], a[176:179], v[104:107]\n\t"
"v_cvt_pk_bf16_f32 v227, v230, v231\n\t"
"v_mfma_f32_16x16x32_bf16 v[104:107], a[252:255], a[180:183], v[104:107]\n\t"
"ds_write_b128 v245, v[224:227] offset:41472\n\t"
"v_mfma_f32_16x16x32_bf16 v[108:111], a[252:255], a[184:187], v[108:111]\n\t"
"s_waitcnt lgkmcnt(5)\n\t"
"v_mfma_f32_16x16x32_bf16 v[108:111], a[220:223], a[180:183], v[108:111]\n\t"
"v_mfma_f32_16x16x32_bf16 v[108:111], a[216:219], a[176:179], v[108:111]\n\t"
"ds_read_b64_tr_b16 v[236:237], v246 offset:47616\n\t"
"v_mfma_f32_16x16x32_bf16 v[112:115], a[216:219], a[180:183], v[112:115]\n\t"
"s_waitcnt lgkmcnt(5)\n\t"
"ds_read_b64_tr_b16 v[232:233], v255 offset:47616\n\t"
"v_mfma_f32_16x16x32_bf16 v[112:115], a[220:223], a[184:187], v[112:115]\n\t"
"ds_read_b64_tr_b16 v[228:229], v246 offset:53760\n\t"
"v_mfma_f32_16x16x32_bf16 v[112:115], a[252:255], a[188:191], v[112:115]\n\t"
"ds_read_b64_tr_b16 v[224:225], v255 offset:53760\n\t"
"v_mfma_f32_16x16x32_bf16 v[116:119], a[252:255], a[192:195], v[116:119]\n\t"
"s_waitcnt lgkmcnt(7)\n\t"
"v_mfma_f32_16x16x32_bf16 v[116:119], a[220:223], a[188:191], v[116:119]\n\t"
"v_mfma_f32_16x16x32_bf16 v[116:119], a[216:219], a[184:187], v[116:119]\n\t"
"v_mfma_f32_16x16x32_bf16 v[120:123], a[216:219], a[188:191], v[120:123]\n\t"
"s_waitcnt lgkmcnt(6)\n\t"
"s_mov_b32 s20, 1536000\n\t"
"v_mfma_f32_16x16x32_bf16 v[120:123], a[220:223], a[192:195], v[120:123]\n\t"
"v_mfma_f32_16x16x32_bf16 v[120:123], a[252:255], a[196:199], v[120:123]\n\t"
"v_mfma_f32_16x16x32_bf16 v[124:127], a[252:255], a[200:203], v[124:127]\n\t"
"s_waitcnt lgkmcnt(5)\n\t"
"v_mfma_f32_16x16x32_bf16 v[124:127], a[220:223], a[196:199], v[124:127]\n\t"
"v_mfma_f32_16x16x32_bf16 v[124:127], a[216:219], a[192:195], v[124:127]\n\t"
"v_mfma_f32_16x16x32_bf16 v[128:131], a[216:219], a[196:199], v[128:131]\n\t"
"buffer_load_dwordx4 a[216:219], off, s[80:83], s20 offset:0 sc1\n\t"
"v_mfma_f32_16x16x32_bf16 v[128:131], a[220:223], a[200:203], v[128:131]\n\t"
"s_waitcnt lgkmcnt(2)\n\t"
"v_mfma_f32_16x16x32_bf16 v[128:131], a[252:255], a[204:207], v[128:131]\n\t"
"s_waitcnt lgkmcnt(1)\n\t"
"v_mfma_f32_4x4x4_16b_bf16 v[236:239], v[236:237], v[220:221], 0\n\t"
"s_waitcnt lgkmcnt(0)\n\t"
"v_mfma_f32_4x4x4_16b_bf16 v[232:235], v[232:233], v[220:221], 0\n\t"
"s_waitcnt vmcnt(10)\n\t"
"v_mfma_f32_4x4x4_16b_bf16 v[228:231], v[228:229], v[220:221], 0\n\t"
"v_mfma_f32_4x4x4_16b_bf16 v[224:227], v[224:225], v[220:221], 0\n\t"
"buffer_load_dwordx4 a[220:223], off, s[80:83], s20 offset:1024 sc1\n\t"
"v_mfma_f32_16x16x32_bf16 v[132:135], a[224:227], a[168:171], v[132:135]\n\t"
"buffer_load_dwordx4 a[252:255], off, s[80:83], s20 offset:2048 sc1\n\t"
"v_mfma_f32_16x16x32_bf16 v[132:135], a[228:231], a[176:179], v[132:135]\n\t"
"v_cvt_pk_bf16_f32 v236, v236, v237\n\t"
"v_cvt_pk_bf16_f32 v237, v238, v239\n\t"
"v_mfma_f32_16x16x32_bf16 v[132:135], v[96:99], a[172:175], v[132:135]\n\t"
"s_mov_b32 s20, 1548288\n\t"
"v_cvt_pk_bf16_f32 v238, v232, v233\n\t"
"v_cvt_pk_bf16_f32 v239, v234, v235\n\t"
"v_mfma_f32_16x16x32_bf16 v[136:139], a[224:227], a[172:175], v[136:139]\n\t"
"v_cvt_pk_bf16_f32 v228, v228, v229\n\t"
"v_cvt_pk_bf16_f32 v229, v230, v231\n\t"
"v_mfma_f32_16x16x32_bf16 v[136:139], v[96:99], a[176:179], v[136:139]\n\t"
"v_cvt_pk_bf16_f32 v230, v224, v225\n\t"
"v_cvt_pk_bf16_f32 v231, v226, v227\n\t"
"v_mfma_f32_16x16x32_bf16 v[136:139], a[228:231], a[180:183], v[136:139]\n\t"
"ds_write_b128 v245, v[236:239] offset:47616\n\t"
"v_mfma_f32_16x16x32_bf16 v[140:143], a[228:231], a[184:187], v[140:143]\n\t"
"v_mfma_f32_16x16x32_bf16 v[140:143], v[96:99], a[180:183], v[140:143]\n\t"
"v_mfma_f32_16x16x32_bf16 v[140:143], a[224:227], a[176:179], v[140:143]\n\t"
"ds_write_b128 v245, v[228:231] offset:53760\n\t"
"v_mfma_f32_16x16x32_bf16 v[144:147], a[224:227], a[180:183], v[144:147]\n\t"
"buffer_load_dwordx4 v[236:239], off, s[80:83], s20 offset:0 sc1\n\t"
"v_mfma_f32_16x16x32_bf16 v[144:147], v[96:99], a[184:187], v[144:147]\n\t"
"Lpg21_%=:\n\t"
"s_cbranch_vccnz Lvr3_%=\n\t"
"v_mfma_f32_16x16x32_bf16 v[144:147], a[228:231], a[188:191], v[144:147]\n\t"
"v_mfma_f32_16x16x32_bf16 v[148:151], a[228:231], a[192:195], v[148:151]\n\t"
"v_mfma_f32_16x16x32_bf16 v[148:151], v[96:99], a[188:191], v[148:151]\n\t"
"v_mfma_f32_16x16x32_bf16 v[148:151], a[224:227], a[184:187], v[148:151]\n\t"
"v_mfma_f32_16x16x32_bf16 v[152:155], a[224:227], a[188:191], v[152:155]\n\t"
"v_mfma_f32_16x16x32_bf16 v[152:155], v[96:99], a[192:195], v[152:155]\n\t"
"v_mfma_f32_16x16x32_bf16 v[152:155], a[228:231], a[196:199], v[152:155]\n\t"
"v_mfma_f32_16x16x32_bf16 v[156:159], a[228:231], a[200:203], v[156:159]\n\t"
"v_mfma_f32_16x16x32_bf16 v[156:159], v[96:99], a[196:199], v[156:159]\n\t"
"v_mfma_f32_16x16x32_bf16 v[156:159], a[224:227], a[192:195], v[156:159]\n\t"
"v_mfma_f32_16x16x32_bf16 v[160:163], a[224:227], a[196:199], v[160:163]\n\t"
"buffer_load_dwordx4 a[224:227], off, s[80:83], s20 offset:1024 sc1\n\t"
"v_mfma_f32_16x16x32_bf16 v[160:163], v[96:99], a[200:203], v[160:163]\n\t"
"s_waitcnt vmcnt(11)\n\t"
"v_mfma_f32_16x16x32_bf16 v[160:163], a[228:231], a[204:207], v[160:163]\n\t"
"v_mfma_f32_16x16x32_bf16 v[164:167], v[248:251], a[168:171], v[164:167]\n\t"
"buffer_load_dwordx4 v[96:99], off, s[80:83], s20 offset:2048 sc1\n\t"
"v_mfma_f32_16x16x32_bf16 v[164:167], v[240:243], a[176:179], v[164:167]\n\t"
"s_mov_b32 s20, 1560576\n\t"
"v_mfma_f32_16x16x32_bf16 v[164:167], a[208:211], a[172:175], v[164:167]\n\t"
"s_waitcnt lgkmcnt(0)\n\t"
"s_barrier\n\t"
"v_mfma_f32_16x16x32_bf16 v[168:171], v[248:251], a[172:175], v[168:171]\n\t"
"ds_read_b128 a[168:171], v222 offset:3072\n\t"
"v_mfma_f32_16x16x32_bf16 v[168:171], a[208:211], a[176:179], v[168:171]\n\t"
"buffer_load_dwordx4 a[228:231], off, s[80:83], s20 offset:0 sc1\n\t"
"v_mfma_f32_16x16x32_bf16 v[168:171], v[240:243], a[180:183], v[168:171]\n\t"
"ds_read_b128 a[172:175], v223 offset:9216\n\t"
"v_mfma_f32_16x16x32_bf16 v[172:175], v[240:243], a[184:187], v[172:175]\n\t"
"v_mfma_f32_16x16x32_bf16 v[172:175], a[208:211], a[180:183], v[172:175]\n\t"
"v_mfma_f32_16x16x32_bf16 v[172:175], v[248:251], a[176:179], v[172:175]\n\t"
"ds_read_b128 a[176:179], v223 offset:15360\n\t"
"v_mfma_f32_16x16x32_bf16 v[176:179], v[248:251], a[180:183], v[176:179]\n\t"
"ds_read_b128 a[180:183], v222 offset:23040\n\t"
"v_mfma_f32_16x16x32_bf16 v[176:179], a[208:211], a[184:187], v[176:179]\n\t"
"v_mfma_f32_16x16x32_bf16 v[176:179], v[240:243], a[188:191], v[176:179]\n\t"
"v_mfma_f32_16x16x32_bf16 v[180:183], v[240:243], a[192:195], v[180:183]\n\t"
"v_mfma_f32_16x16x32_bf16 v[180:183], a[208:211], a[188:191], v[180:183]\n\t"
"v_mfma_f32_16x16x32_bf16 v[180:183], v[248:251], a[184:187], v[180:183]\n\t"
"ds_read_b128 a[184:187], v223 offset:29184\n\t"
"v_mfma_f32_16x16x32_bf16 v[184:187], v[248:251], a[188:191], v[184:187]\n\t"
"ds_read_b128 a[188:191], v223 offset:35328\n\t"
"v_mfma_f32_16x16x32_bf16 v[184:187], a[208:211], a[192:195], v[184:187]\n\t"
"v_mfma_f32_16x16x32_bf16 v[184:187], v[240:243], a[196:199], v[184:187]\n\t"
"v_mfma_f32_16x16x32_bf16 v[188:191], v[240:243], a[200:203], v[188:191]\n\t"
"v_mfma_f32_16x16x32_bf16 v[188:191], a[208:211], a[196:199], v[188:191]\n\t"
"v_mfma_f32_16x16x32_bf16 v[188:191], v[248:251], a[192:195], v[188:191]\n\t"
"ds_read_b128 a[192:195], v222 offset:43008\n\t"
"v_mfma_f32_16x16x32_bf16 v[192:195], v[248:251], a[196:199], v[192:195]\n\t"
"buffer_load_dwordx4 v[248:251], off, s[80:83], s20 offset:1024 sc1\n\t"
"v_mfma_f32_16x16x32_bf16 v[192:195], a[208:211], a[200:203], v[192:195]\n\t"
"s_waitcnt vmcnt(11) lgkmcnt(4)\n\t"
"ds_read_b128 a[196:199], v223 offset:49152\n\t"
"v_mfma_f32_16x16x32_bf16 v[192:195], v[240:243], a[204:207], v[192:195]\n\t"
"ds_read_b128 a[200:203], v223 offset:55296\n\t"
"Lst30_%=:\n\t"
"v_mfma_f32_16x16x32_bf16 v[0:3], a[232:235], a[168:171], v[0:3]\n\t"
"buffer_load_dwordx4 a[208:211], off, s[80:83], s20 offset:2048 sc1\n\t"
"v_mfma_f32_16x16x32_bf16 v[0:3], a[240:243], a[176:179], v[0:3]\n\t"
"s_waitcnt lgkmcnt(5)\n\t"
"ds_read_b128 a[204:207], v222 offset:62976\n\t"
"s_mov_b32 s20, 1572864\n\t"
"v_mfma_f32_16x16x32_bf16 v[0:3], a[236:239], a[172:175], v[0:3]\n\t"
"v_mfma_f32_16x16x32_bf16 v[4:7], a[232:235], a[172:175], v[4:7]\n\t"
"buffer_load_dwordx4 v[240:243], off, s[80:83], s20 offset:0 sc1\n\t"
"v_mfma_f32_16x16x32_bf16 v[4:7], a[236:239], a[176:179], v[4:7]\n\t"
"s_waitcnt lgkmcnt(5)\n\t"
"v_mfma_f32_16x16x32_bf16 v[4:7], a[240:243], a[180:183], v[4:7]\n\t"
"v_mfma_f32_16x16x32_bf16 v[8:11], a[240:243], a[184:187], v[8:11]\n\t"
"s_waitcnt lgkmcnt(4)\n\t"
"v_mfma_f32_16x16x32_bf16 v[8:11], a[236:239], a[180:183], v[8:11]\n\t"
"v_mfma_f32_16x16x32_bf16 v[8:11], a[232:235], a[176:179], v[8:11]\n\t"
"v_mfma_f32_16x16x32_bf16 v[12:15], a[232:235], a[180:183], v[12:15]\n\t"
"s_waitcnt lgkmcnt(3)\n\t"
"v_mfma_f32_16x16x32_bf16 v[12:15], a[236:239], a[184:187], v[12:15]\n\t"
"v_mfma_f32_16x16x32_bf16 v[12:15], a[240:243], a[188:191], v[12:15]\n\t"
"v_mfma_f32_16x16x32_bf16 v[16:19], a[240:243], a[192:195], v[16:19]\n\t"
"s_waitcnt lgkmcnt(2)\n\t"
"v_mfma_f32_16x16x32_bf16 v[16:19], a[236:239], a[188:191], v[16:19]\n\t"
"v_mfma_f32_16x16x32_bf16 v[16:19], a[232:235], a[184:187], v[16:19]\n\t"
"v_mfma_f32_16x16x32_bf16 v[20:23], a[232:235], a[188:191], v[20:23]\n\t"
"s_waitcnt lgkmcnt(1)\n\t"
"v_mfma_f32_16x16x32_bf16 v[20:23], a[236:239], a[192:195], v[20:23]\n\t"
"v_mfma_f32_16x16x32_bf16 v[20:23], a[240:243], a[196:199], v[20:23]\n\t"
"v_mfma_f32_16x16x32_bf16 v[24:27], a[240:243], a[200:203], v[24:27]\n\t"
"s_waitcnt lgkmcnt(0)\n\t"
"v_mfma_f32_16x16x32_bf16 v[24:27], a[236:239], a[196:199], v[24:27]\n\t"
"v_mfma_f32_16x16x32_bf16 v[24:27], a[232:235], a[192:195], v[24:27]\n\t"
"v_mfma_f32_16x16x32_bf16 v[28:31], a[232:235], a[196:199], v[28:31]\n\t"
"buffer_load_dwordx4 a[232:235], off, s[80:83], s20 offset:1024 sc1\n\t"
"v_mfma_f32_16x16x32_bf16 v[28:31], a[236:239], a[200:203], v[28:31]\n\t"
"s_waitcnt vmcnt(11)\n\t"
"v_mfma_f32_16x16x32_bf16 v[28:31], a[240:243], a[204:207], v[28:31]\n\t"
"v_mfma_f32_16x16x32_bf16 v[32:35], a[244:247], a[168:171], v[32:35]\n\t"
"buffer_load_dwordx4 a[236:239], off, s[80:83], s20 offset:2048 sc1\n\t"
"v_mfma_f32_16x16x32_bf16 v[32:35], a[248:251], a[176:179], v[32:35]\n\t"
"s_mov_b32 s20, 1585152\n\t"
"v_mfma_f32_16x16x32_bf16 v[32:35], a[212:215], a[172:175], v[32:35]\n\t"
"v_mfma_f32_16x16x32_bf16 v[36:39], a[244:247], a[172:175], v[36:39]\n\t"
"buffer_load_dwordx4 a[240:243], off, s[80:83], s20 offset:0 sc1\n\t"
"v_mfma_f32_16x16x32_bf16 v[36:39], a[212:215], a[176:179], v[36:39]\n\t"
"v_mfma_f32_16x16x32_bf16 v[36:39], a[248:251], a[180:183], v[36:39]\n\t"
"v_mfma_f32_16x16x32_bf16 v[40:43], a[248:251], a[184:187], v[40:43]\n\t"
"v_mfma_f32_16x16x32_bf16 v[40:43], a[212:215], a[180:183], v[40:43]\n\t"
"v_mfma_f32_16x16x32_bf16 v[40:43], a[244:247], a[176:179], v[40:43]\n\t"
"v_mfma_f32_16x16x32_bf16 v[44:47], a[244:247], a[180:183], v[44:47]\n\t"
"v_mfma_f32_16x16x32_bf16 v[44:47], a[212:215], a[184:187], v[44:47]\n\t"
"v_mfma_f32_16x16x32_bf16 v[44:47], a[248:251], a[188:191], v[44:47]\n\t"
"v_mfma_f32_16x16x32_bf16 v[48:51], a[248:251], a[192:195], v[48:51]\n\t"
"v_mfma_f32_16x16x32_bf16 v[48:51], a[212:215], a[188:191], v[48:51]\n\t"
"v_mfma_f32_16x16x32_bf16 v[48:51], a[244:247], a[184:187], v[48:51]\n\t"
"v_mfma_f32_16x16x32_bf16 v[52:55], a[244:247], a[188:191], v[52:55]\n\t"
"v_mfma_f32_16x16x32_bf16 v[52:55], a[212:215], a[192:195], v[52:55]\n\t"
"v_mfma_f32_16x16x32_bf16 v[52:55], a[248:251], a[196:199], v[52:55]\n\t"
"v_mfma_f32_16x16x32_bf16 v[56:59], a[248:251], a[200:203], v[56:59]\n\t"
"v_mfma_f32_16x16x32_bf16 v[56:59], a[212:215], a[196:199], v[56:59]\n\t"
"v_mfma_f32_16x16x32_bf16 v[56:59], a[244:247], a[192:195], v[56:59]\n\t"
"v_mfma_f32_16x16x32_bf16 v[60:63], a[244:247], a[196:199], v[60:63]\n\t"
"buffer_load_dwordx4 a[244:247], off, s[80:83], s20 offset:1024 sc1\n\t"
"v_mfma_f32_16x16x32_bf16 v[60:63], a[212:215], a[200:203], v[60:63]\n\t"
"s_waitcnt vmcnt(11)\n\t"
"v_mfma_f32_16x16x32_bf16 v[60:63], a[248:251], a[204:207], v[60:63]\n\t"
"v_mfma_f32_16x16x32_bf16 v[64:67], a[216:219], a[168:171], v[64:67]\n\t"
"buffer_load_dwordx4 a[212:215], off, s[80:83], s20 offset:2048 sc1\n\t"
"v_mfma_f32_16x16x32_bf16 v[64:67], a[252:255], a[176:179], v[64:67]\n\t"
"s_mov_b32 s20, 1597440\n\t"
"ds_read_b128 a[168:171], v222 offset:3328\n\t"
"v_mfma_f32_16x16x32_bf16 v[64:67], a[220:223], a[172:175], v[64:67]\n\t"
"v_mfma_f32_16x16x32_bf16 v[68:71], a[216:219], a[172:175], v[68:71]\n\t"
"buffer_load_dwordx4 a[248:251], off, s[80:83], s20 offset:0 sc1\n\t"
"v_mfma_f32_16x16x32_bf16 v[68:71], a[220:223], a[176:179], v[68:71]\n\t"
"ds_read_b128 a[172:175], v223 offset:9472\n\t"
"v_mfma_f32_16x16x32_bf16 v[68:71], a[252:255], a[180:183], v[68:71]\n\t"
"v_mfma_f32_16x16x32_bf16 v[72:75], a[252:255], a[184:187], v[72:75]\n\t"
"v_mfma_f32_16x16x32_bf16 v[72:75], a[220:223], a[180:183], v[72:75]\n\t"
"v_mfma_f32_16x16x32_bf16 v[72:75], a[216:219], a[176:179], v[72:75]\n\t"
"ds_read_b128 a[176:179], v223 offset:15616\n\t"
"v_mfma_f32_16x16x32_bf16 v[76:79], a[216:219], a[180:183], v[76:79]\n\t"
"ds_read_b128 a[180:183], v222 offset:23296\n\t"
"v_mfma_f32_16x16x32_bf16 v[76:79], a[220:223], a[184:187], v[76:79]\n\t"
"v_mfma_f32_16x16x32_bf16 v[76:79], a[252:255], a[188:191], v[76:79]\n\t"
"v_mfma_f32_16x16x32_bf16 v[80:83], a[252:255], a[192:195], v[80:83]\n\t"
"v_mfma_f32_16x16x32_bf16 v[80:83], a[220:223], a[188:191], v[80:83]\n\t"
"v_mfma_f32_16x16x32_bf16 v[80:83], a[216:219], a[184:187], v[80:83]\n\t"
"ds_read_b128 a[184:187], v223 offset:29440\n\t"
"v_mfma_f32_16x16x32_bf16 v[84:87], a[216:219], a[188:191], v[84:87]\n\t"
"ds_read_b128 a[188:191], v223 offset:35584\n\t"
"v_mfma_f32_16x16x32_bf16 v[84:87], a[220:223], a[192:195], v[84:87]\n\t"
"v_mfma_f32_16x16x32_bf16 v[84:87], a[252:255], a[196:199], v[84:87]\n\t"
"v_mfma_f32_16x16x32_bf16 v[88:91], a[252:255], a[200:203], v[88:91]\n\t"
"v_mfma_f32_16x16x32_bf16 v[88:91], a[220:223], a[196:199], v[88:91]\n\t"
"v_mfma_f32_16x16x32_bf16 v[88:91], a[216:219], a[192:195], v[88:91]\n\t"
"ds_read_b128 a[192:195], v222 offset:43264\n\t"
"v_mfma_f32_16x16x32_bf16 v[92:95], a[216:219], a[196:199], v[92:95]\n\t"
"buffer_load_dwordx4 a[216:219], off, s[80:83], s20 offset:1024 sc1\n\t"
"v_mfma_f32_16x16x32_bf16 v[92:95], a[220:223], a[200:203], v[92:95]\n\t"
"s_waitcnt vmcnt(11) lgkmcnt(4)\n\t"
"ds_read_b128 a[196:199], v223 offset:49408\n\t"
"v_mfma_f32_16x16x32_bf16 v[92:95], a[252:255], a[204:207], v[92:95]\n\t"
"ds_read_b128 a[200:203], v223 offset:55552\n\t"
"v_mfma_f32_16x16x32_bf16 v[100:103], v[236:239], a[168:171], v[100:103]\n\t"
"buffer_load_dwordx4 a[220:223], off, s[80:83], s20 offset:2048 sc1\n\t"
"v_mfma_f32_16x16x32_bf16 v[100:103], v[96:99], a[176:179], v[100:103]\n\t"
"s_waitcnt lgkmcnt(5)\n\t"
"ds_read_b128 a[204:207], v222 offset:63232\n\t"
"s_mov_b32 s20, 1609728\n\t"
"v_mfma_f32_16x16x32_bf16 v[100:103], a[224:227], a[172:175], v[100:103]\n\t"
"v_mfma_f32_16x16x32_bf16 v[104:107], v[236:239], a[172:175], v[104:107]\n\t"
"buffer_load_dwordx4 a[252:255], off, s[80:83], s20 offset:0 sc1\n\t"
"v_mfma_f32_16x16x32_bf16 v[104:107], a[224:227], a[176:179], v[104:107]\n\t"
"s_waitcnt lgkmcnt(5)\n\t"
"v_mfma_f32_16x16x32_bf16 v[104:107], v[96:99], a[180:183], v[104:107]\n\t"
"v_mfma_f32_16x16x32_bf16 v[108:111], v[96:99], a[184:187], v[108:111]\n\t"
"s_waitcnt lgkmcnt(4)\n\t"
"v_fma_f32 v0, v100, s52, v0\n\t"
"v_fma_f32 v1, v101, s52, v1\n\t"
"v_mfma_f32_16x16x32_bf16 v[108:111], a[224:227], a[180:183], v[108:111]\n\t"
"v_fma_f32 v2, v102, s52, v2\n\t"
"v_fma_f32 v3, v103, s52, v3\n\t"
"v_mfma_f32_16x16x32_bf16 v[108:111], v[236:239], a[176:179], v[108:111]\n\t"
"v_fma_f32 v100, v0, s54, -v100\n\t"
"v_fma_f32 v101, v1, s54, -v101\n\t"
"v_mfma_f32_16x16x32_bf16 v[112:115], v[236:239], a[180:183], v[112:115]\n\t"
"s_waitcnt lgkmcnt(3)\n\t"
"v_fma_f32 v102, v2, s54, -v102\n\t"
"v_fma_f32 v103, v3, s54, -v103\n\t"
"v_mfma_f32_16x16x32_bf16 v[112:115], a[224:227], a[184:187], v[112:115]\n\t"
"v_fma_f32 v4, v104, s52, v4\n\t"
"v_fma_f32 v5, v105, s52, v5\n\t"
"v_mfma_f32_16x16x32_bf16 v[112:115], v[96:99], a[188:191], v[112:115]\n\t"
"v_fma_f32 v6, v106, s52, v6\n\t"
"v_fma_f32 v7, v107, s52, v7\n\t"
"v_mfma_f32_16x16x32_bf16 v[116:119], v[96:99], a[192:195], v[116:119]\n\t"
"s_waitcnt lgkmcnt(2)\n\t"
"v_fma_f32 v104, v4, s54, -v104\n\t"
"v_fma_f32 v105, v5, s54, -v105\n\t"
"v_mfma_f32_16x16x32_bf16 v[116:119], a[224:227], a[188:191], v[116:119]\n\t"
"v_fma_f32 v106, v6, s54, -v106\n\t"
"v_fma_f32 v107, v7, s54, -v107\n\t"
"v_mfma_f32_16x16x32_bf16 v[116:119], v[236:239], a[184:187], v[116:119]\n\t"
"v_fma_f32 v8, v108, s52, v8\n\t"
"v_fma_f32 v9, v109, s52, v9\n\t"
"v_mfma_f32_16x16x32_bf16 v[120:123], v[236:239], a[188:191], v[120:123]\n\t"
"s_waitcnt lgkmcnt(1)\n\t"
"v_fma_f32 v10, v110, s52, v10\n\t"
"v_fma_f32 v11, v111, s52, v11\n\t"
"v_mfma_f32_16x16x32_bf16 v[120:123], a[224:227], a[192:195], v[120:123]\n\t"
"v_fma_f32 v108, v8, s54, -v108\n\t"
"v_fma_f32 v109, v9, s54, -v109\n\t"
"v_mfma_f32_16x16x32_bf16 v[120:123], v[96:99], a[196:199], v[120:123]\n\t"
"v_fma_f32 v110, v10, s54, -v110\n\t"
"v_fma_f32 v111, v11, s54, -v111\n\t"
"v_mfma_f32_16x16x32_bf16 v[124:127], v[96:99], a[200:203], v[124:127]\n\t"
"s_waitcnt lgkmcnt(0)\n\t"
"v_fma_f32 v12, v112, s52, v12\n\t"
"v_fma_f32 v13, v113, s52, v13\n\t"
"v_mfma_f32_16x16x32_bf16 v[124:127], a[224:227], a[196:199], v[124:127]\n\t"
"v_fma_f32 v14, v114, s52, v14\n\t"
"v_fma_f32 v15, v115, s52, v15\n\t"
"v_mfma_f32_16x16x32_bf16 v[124:127], v[236:239], a[192:195], v[124:127]\n\t"
"v_fma_f32 v112, v12, s54, -v112\n\t"
"v_fma_f32 v113, v13, s54, -v113\n\t"
"v_mfma_f32_16x16x32_bf16 v[128:131], v[236:239], a[196:199], v[128:131]\n\t"
"s_waitcnt vmcnt(10)\n\t"
"v_fma_f32 v114, v14, s54, -v114\n\t"
"v_fma_f32 v115, v15, s54, -v115\n\t"
"v_mfma_f32_16x16x32_bf16 v[128:131], a[224:227], a[200:203], v[128:131]\n\t"
"v_fma_f32 v16, v116, s52, v16\n\t"
"v_fma_f32 v17, v117, s52, v17\n\t"
"v_mfma_f32_16x16x32_bf16 v[128:131], v[96:99], a[204:207], v[128:131]\n\t"
"v_fma_f32 v18, v118, s52, v18\n\t"
"v_fma_f32 v19, v119, s52, v19\n\t"
"v_mfma_f32_16x16x32_bf16 v[132:135], a[228:231], a[168:171], v[132:135]\n\t"
"v_fma_f32 v116, v16, s54, -v116\n\t"
"v_fma_f32 v117, v17, s54, -v117\n\t"
"v_mfma_f32_16x16x32_bf16 v[132:135], a[208:211], a[176:179], v[132:135]\n\t"
"v_fma_f32 v118, v18, s54, -v118\n\t"
"v_fma_f32 v119, v19, s54, -v119\n\t"
"v_mfma_f32_16x16x32_bf16 v[132:135], v[248:251], a[172:175], v[132:135]\n\t"
"buffer_load_dwordx4 v[236:239], off, s[80:83], s20 offset:1024 sc1\n\t"
"v_mfma_f32_16x16x32_bf16 v[136:139], a[228:231], a[172:175], v[136:139]\n\t"
"buffer_load_dwordx4 v[96:99], off, s[80:83], s20 offset:2048 sc1\n\t"
"v_mfma_f32_16x16x32_bf16 v[136:139], v[248:251], a[176:179], v[136:139]\n\t"
"s_mov_b32 s20, 1622016\n\t"
"v_fma_f32 v20, v120, s52, v20\n\t"
"v_fma_f32 v21, v121, s52, v21\n\t"
"v_mfma_f32_16x16x32_bf16 v[136:139], a[208:211], a[180:183], v[136:139]\n\t"
"v_fma_f32 v22, v122, s52, v22\n\t"
"v_fma_f32 v23, v123, s52, v23\n\t"
"v_mfma_f32_16x16x32_bf16 v[140:143], a[208:211], a[184:187], v[140:143]\n\t"
"v_fma_f32 v120, v20, s54, -v120\n\t"
"v_fma_f32 v121, v21, s54, -v121\n\t"
"v_mfma_f32_16x16x32_bf16 v[140:143], v[248:251], a[180:183], v[140:143]\n\t"
"v_fma_f32 v122, v22, s54, -v122\n\t"
"v_fma_f32 v123, v23, s54, -v123\n\t"
"v_mfma_f32_16x16x32_bf16 v[140:143], a[228:231], a[176:179], v[140:143]\n\t"
"buffer_load_dwordx4 a[224:227], off, s[80:83], s20 offset:0 sc1\n\t"
"v_mfma_f32_16x16x32_bf16 v[144:147], a[228:231], a[180:183], v[144:147]\n\t"
"v_fma_f32 v24, v124, s52, v24\n\t"
"v_fma_f32 v25, v125, s52, v25\n\t"
"v_mfma_f32_16x16x32_bf16 v[144:147], v[248:251], a[184:187], v[144:147]\n\t"
"v_fma_f32 v26, v126, s52, v26\n\t"
"v_fma_f32 v27, v127, s52, v27\n\t"
"v_mfma_f32_16x16x32_bf16 v[144:147], a[208:211], a[188:191], v[144:147]\n\t"
"v_fma_f32 v124, v24, s54, -v124\n\t"
"v_fma_f32 v125, v25, s54, -v125\n\t"
"v_mfma_f32_16x16x32_bf16 v[148:151], a[208:211], a[192:195], v[148:151]\n\t"
"v_fma_f32 v126, v26, s54, -v126\n\t"
"v_fma_f32 v127, v27, s54, -v127\n\t"
"v_mfma_f32_16x16x32_bf16 v[148:151], v[248:251], a[188:191], v[148:151]\n\t"
"v_fma_f32 v28, v128, s52, v28\n\t"
"v_fma_f32 v29, v129, s52, v29\n\t"
"v_mfma_f32_16x16x32_bf16 v[148:151], a[228:231], a[184:187], v[148:151]\n\t"
"v_fma_f32 v30, v130, s52, v30\n\t"
"v_fma_f32 v31, v131, s52, v31\n\t"
"v_mfma_f32_16x16x32_bf16 v[152:155], a[228:231], a[188:191], v[152:155]\n\t"
"v_fma_f32 v128, v28, s54, -v128\n\t"
"v_fma_f32 v129, v29, s54, -v129\n\t"
"v_mfma_f32_16x16x32_bf16 v[152:155], v[248:251], a[192:195], v[152:155]\n\t"
"v_fma_f32 v130, v30, s54, -v130\n\t"
"v_fma_f32 v131, v31, s54, -v131\n\t"
"v_mfma_f32_16x16x32_bf16 v[152:155], a[208:211], a[196:199], v[152:155]\n\t"
"v_fma_f32 v32, v132, s52, v32\n\t"
"v_fma_f32 v33, v133, s52, v33\n\t"
"v_mfma_f32_16x16x32_bf16 v[156:159], a[208:211], a[200:203], v[156:159]\n\t"
"v_fma_f32 v34, v134, s52, v34\n\t"
"v_fma_f32 v35, v135, s52, v35\n\t"
"v_mfma_f32_16x16x32_bf16 v[156:159], v[248:251], a[196:199], v[156:159]\n\t"
"v_fma_f32 v132, v32, s54, -v132\n\t"
"v_fma_f32 v133, v33, s54, -v133\n\t"
"v_mfma_f32_16x16x32_bf16 v[156:159], a[228:231], a[192:195], v[156:159]\n\t"
"v_fma_f32 v134, v34, s54, -v134\n\t"
"v_fma_f32 v135, v35, s54, -v135\n\t"
"v_mfma_f32_16x16x32_bf16 v[160:163], a[228:231], a[196:199], v[160:163]\n\t"
"s_waitcnt vmcnt(10)\n\t"
"v_fma_f32 v36, v136, s52, v36\n\t"
"v_fma_f32 v37, v137, s52, v37\n\t"
"v_mfma_f32_16x16x32_bf16 v[160:163], v[248:251], a[200:203], v[160:163]\n\t"
"v_fma_f32 v38, v138, s52, v38\n\t"
"v_fma_f32 v39, v139, s52, v39\n\t"
"v_mfma_f32_16x16x32_bf16 v[160:163], a[208:211], a[204:207], v[160:163]\n\t"
"v_fma_f32 v136, v36, s54, -v136\n\t"
"v_fma_f32 v137, v37, s54, -v137\n\t"
"v_mfma_f32_16x16x32_bf16 v[164:167], v[240:243], a[168:171], v[164:167]\n\t"
"ds_read_b128 a[168:171], v222 offset:1536\n\t"
"v_fma_f32 v138, v38, s54, -v138\n\t"
"v_mfma_f32_16x16x32_bf16 v[164:167], a[236:239], a[176:179], v[164:167]\n\t"
"v_fma_f32 v139, v39, s54, -v139\n\t"
"v_fma_f32 v40, v140, s52, v40\n\t"
"v_mfma_f32_16x16x32_bf16 v[164:167], a[232:235], a[172:175], v[164:167]\n\t"
"buffer_load_dwordx4 a[228:231], off, s[80:83], s20 offset:1024 sc1\n\t"
"v_mfma_f32_16x16x32_bf16 v[168:171], v[240:243], a[172:175], v[168:171]\n\t"
"buffer_load_dwordx4 v[248:251], off, s[80:83], s20 offset:2048 sc1\n\t"
"v_mfma_f32_16x16x32_bf16 v[168:171], a[232:235], a[176:179], v[168:171]\n\t"
"s_mov_b32 s20, 1634304\n\t"
"ds_read_b128 a[172:175], v223 offset:7680\n\t"
"v_fma_f32 v41, v141, s52, v41\n\t"
"v_mfma_f32_16x16x32_bf16 v[168:171], a[236:239], a[180:183], v[168:171]\n\t"
"v_fma_f32 v42, v142, s52, v42\n\t"
"v_fma_f32 v43, v143, s52, v43\n\t"
"v_mfma_f32_16x16x32_bf16 v[172:175], a[236:239], a[184:187], v[172:175]\n\t"
"v_fma_f32 v140, v40, s54, -v140\n\t"
"v_fma_f32 v141, v41, s54, -v141\n\t"
"v_mfma_f32_16x16x32_bf16 v[172:175], a[232:235], a[180:183], v[172:175]\n\t"
"v_fma_f32 v142, v42, s54, -v142\n\t"
"v_fma_f32 v143, v43, s54, -v143\n\t"
"v_mfma_f32_16x16x32_bf16 v[172:175], v[240:243], a[176:179], v[172:175]\n\t"
"buffer_load_dwordx4 a[208:211], off, s[80:83], s20 offset:0 sc1\n\t"
"v_mfma_f32_16x16x32_bf16 v[176:179], v[240:243], a[180:183], v[176:179]\n\t"
"ds_read_b128 a[176:179], v223 offset:13824\n\t"
"v_fma_f32 v44, v144, s52, v44\n\t"
"v_mfma_f32_16x16x32_bf16 v[176:179], a[232:235], a[184:187], v[176:179]\n\t"
"ds_read_b128 a[180:183], v222 offset:21504\n\t"
"v_fma_f32 v45, v145, s52, v45\n\t"
"v_mfma_f32_16x16x32_bf16 v[176:179], a[236:239], a[188:191], v[176:179]\n\t"
"v_fma_f32 v46, v146, s52, v46\n\t"
"v_fma_f32 v47, v147, s52, v47\n\t"
"v_mfma_f32_16x16x32_bf16 v[180:183], a[236:239], a[192:195], v[180:183]\n\t"
"v_fma_f32 v144, v44, s54, -v144\n\t"
"v_fma_f32 v145, v45, s54, -v145\n\t"
"v_mfma_f32_16x16x32_bf16 v[180:183], a[232:235], a[188:191], v[180:183]\n\t"
"v_fma_f32 v146, v46, s54, -v146\n\t"
"v_fma_f32 v147, v47, s54, -v147\n\t"
"v_mfma_f32_16x16x32_bf16 v[180:183], v[240:243], a[184:187], v[180:183]\n\t"
"ds_read_b128 a[184:187], v223 offset:27648\n\t"
"v_fma_f32 v48, v148, s52, v48\n\t"
"v_mfma_f32_16x16x32_bf16 v[184:187], v[240:243], a[188:191], v[184:187]\n\t"
"ds_read_b128 a[188:191], v223 offset:33792\n\t"
"v_fma_f32 v49, v149, s52, v49\n\t"
"v_mfma_f32_16x16x32_bf16 v[184:187], a[232:235], a[192:195], v[184:187]\n\t"
"v_fma_f32 v50, v150, s52, v50\n\t"
"v_fma_f32 v51, v151, s52, v51\n\t"
"v_mfma_f32_16x16x32_bf16 v[184:187], a[236:239], a[196:199], v[184:187]\n\t"
"v_fma_f32 v148, v48, s54, -v148\n\t"
"v_fma_f32 v149, v49, s54, -v149\n\t"
"v_mfma_f32_16x16x32_bf16 v[188:191], a[236:239], a[200:203], v[188:191]\n\t"
"v_fma_f32 v150, v50, s54, -v150\n\t"
"v_fma_f32 v151, v51, s54, -v151\n\t"
"v_mfma_f32_16x16x32_bf16 v[188:191], a[232:235], a[196:199], v[188:191]\n\t"
"v_fma_f32 v52, v152, s52, v52\n\t"
"v_fma_f32 v53, v153, s52, v53\n\t"
"v_mfma_f32_16x16x32_bf16 v[188:191], v[240:243], a[192:195], v[188:191]\n\t"
"ds_read_b128 a[192:195], v222 offset:41472\n\t"
"v_fma_f32 v54, v154, s52, v54\n\t"
"v_mfma_f32_16x16x32_bf16 v[192:195], v[240:243], a[196:199], v[192:195]\n\t"
"s_waitcnt vmcnt(10) lgkmcnt(4)\n\t"
"ds_read_b128 a[196:199], v223 offset:47616\n\t"
"v_fma_f32 v55, v155, s52, v55\n\t"
"v_mfma_f32_16x16x32_bf16 v[192:195], a[232:235], a[200:203], v[192:195]\n\t"
"ds_read_b128 a[200:203], v223 offset:53760\n\t"
"v_fma_f32 v152, v52, s54, -v152\n\t"
"v_mfma_f32_16x16x32_bf16 v[192:195], a[236:239], a[204:207], v[192:195]\n\t"
"ds_read_b128 a[204:207], v222 offset:61440\n\t"
"v_fma_f32 v153, v53, s54, -v153\n\t"
"Lst31_%=:\n\t"
"v_mfma_f32_16x16x32_bf16 v[196:199], a[240:243], a[168:171], v[196:199]\n\t"
"s_waitcnt lgkmcnt(6)\n\t"
"v_fma_f32 v154, v54, s54, -v154\n\t"
"v_fma_f32 v155, v55, s54, -v155\n\t"
"v_mfma_f32_16x16x32_bf16 v[196:199], a[212:215], a[176:179], v[196:199]\n\t"
"v_fma_f32 v56, v156, s52, v56\n\t"
"v_fma_f32 v57, v157, s52, v57\n\t"
"v_mfma_f32_16x16x32_bf16 v[196:199], a[244:247], a[172:175], v[196:199]\n\t"
"buffer_load_dwordx4 v[240:243], off, s[80:83], s20 offset:1024 sc1\n\t"
"v_mfma_f32_16x16x32_bf16 v[204:207], a[240:243], a[172:175], v[204:207]\n\t"
"buffer_load_dwordx4 a[232:235], off, s[80:83], s20 offset:2048 sc1\n\t"
"v_mfma_f32_16x16x32_bf16 v[204:207], a[244:247], a[176:179], v[204:207]\n\t"
"s_waitcnt lgkmcnt(5)\n\t"
"s_mov_b32 s20, 1646592\n\t"
"v_fma_f32 v58, v158, s52, v58\n\t"
"v_mfma_f32_16x16x32_bf16 v[204:207], a[212:215], a[180:183], v[204:207]\n\t"
"v_fma_f32 v59, v159, s52, v59\n\t"
"v_fma_f32 v156, v56, s54, -v156\n\t"
"v_mfma_f32_16x16x32_bf16 v[212:215], a[212:215], a[184:187], v[212:215]\n\t"
"s_waitcnt lgkmcnt(4)\n\t"
"v_fma_f32 v157, v57, s54, -v157\n\t"
"v_fma_f32 v158, v58, s54, -v158\n\t"
"v_mfma_f32_16x16x32_bf16 v[212:215], a[244:247], a[180:183], v[212:215]\n\t"
"v_fma_f32 v159, v59, s54, -v159\n\t"
"v_fma_f32 v60, v160, s52, v60\n\t"
"v_mfma_f32_16x16x32_bf16 v[212:215], a[240:243], a[176:179], v[212:215]\n\t"
"buffer_load_dwordx4 a[236:239], off, s[80:83], s20 offset:0 sc1\n\t"
"v_mfma_f32_16x16x32_bf16 a[0:3], a[240:243], a[180:183], a[0:3]\n\t"
"s_waitcnt lgkmcnt(3)\n\t"
"v_fma_f32 v61, v161, s52, v61\n\t"
"v_fma_f32 v62, v162, s52, v62\n\t"
"v_mfma_f32_16x16x32_bf16 a[0:3], a[244:247], a[184:187], a[0:3]\n\t"
"v_fma_f32 v63, v163, s52, v63\n\t"
"v_fma_f32 v160, v60, s54, -v160\n\t"
"v_mfma_f32_16x16x32_bf16 a[0:3], a[212:215], a[188:191], a[0:3]\n\t"
"v_fma_f32 v161, v61, s54, -v161\n\t"
"v_fma_f32 v162, v62, s54, -v162\n\t"
"v_mfma_f32_16x16x32_bf16 a[8:11], a[212:215], a[192:195], a[8:11]\n\t"
"s_waitcnt lgkmcnt(2)\n\t"
"v_fma_f32 v163, v63, s54, -v163\n\t"
"v_fma_f32 v64, v164, s52, v64\n\t"
"v_mfma_f32_16x16x32_bf16 a[8:11], a[244:247], a[188:191], a[8:11]\n\t"
"v_fma_f32 v65, v165, s52, v65\n\t"
"v_fma_f32 v66, v166, s52, v66\n\t"
"v_mfma_f32_16x16x32_bf16 a[8:11], a[240:243], a[184:187], a[8:11]\n\t"
"v_fma_f32 v67, v167, s52, v67\n\t"
"v_fma_f32 v164, v64, s54, -v164\n\t"
"v_mfma_f32_16x16x32_bf16 a[16:19], a[240:243], a[188:191], a[16:19]\n\t"
"s_waitcnt lgkmcnt(1)\n\t"
"v_fma_f32 v165, v65, s54, -v165\n\t"
"v_fma_f32 v166, v66, s54, -v166\n\t"
"v_mfma_f32_16x16x32_bf16 a[16:19], a[244:247], a[192:195], a[16:19]\n\t"
"v_fma_f32 v167, v67, s54, -v167\n\t"
"v_fma_f32 v68, v168, s52, v68\n\t"
"v_mfma_f32_16x16x32_bf16 a[16:19], a[212:215], a[196:199], a[16:19]\n\t"
"v_fma_f32 v69, v169, s52, v69\n\t"
"v_fma_f32 v70, v170, s52, v70\n\t"
"v_mfma_f32_16x16x32_bf16 a[24:27], a[212:215], a[200:203], a[24:27]\n\t"
"s_waitcnt lgkmcnt(0)\n\t"
"v_fma_f32 v71, v171, s52, v71\n\t"
"v_fma_f32 v168, v68, s54, -v168\n\t"
"v_mfma_f32_16x16x32_bf16 a[24:27], a[244:247], a[196:199], a[24:27]\n\t"
"v_fma_f32 v169, v69, s54, -v169\n\t"
"v_fma_f32 v170, v70, s54, -v170\n\t"
"v_mfma_f32_16x16x32_bf16 a[24:27], a[240:243], a[192:195], a[24:27]\n\t"
"v_fma_f32 v171, v71, s54, -v171\n\t"
"v_fma_f32 v72, v172, s52, v72\n\t"
"v_mfma_f32_16x16x32_bf16 a[32:35], a[240:243], a[196:199], a[32:35]\n\t"
"s_waitcnt vmcnt(10)\n\t"
"v_fma_f32 v73, v173, s52, v73\n\t"
"v_fma_f32 v74, v174, s52, v74\n\t"
"v_mfma_f32_16x16x32_bf16 a[32:35], a[244:247], a[200:203], a[32:35]\n\t"
"v_fma_f32 v75, v175, s52, v75\n\t"
"v_fma_f32 v172, v72, s54, -v172\n\t"
"v_mfma_f32_16x16x32_bf16 a[32:35], a[212:215], a[204:207], a[32:35]\n\t"
"v_fma_f32 v173, v73, s54, -v173\n\t"
"v_fma_f32 v174, v74, s54, -v174\n\t"
"v_mfma_f32_16x16x32_bf16 a[40:43], a[248:251], a[168:171], a[40:43]\n\t"
"v_fma_f32 v175, v75, s54, -v175\n\t"
"v_fma_f32 v76, v176, s52, v76\n\t"
"v_mfma_f32_16x16x32_bf16 a[40:43], a[220:223], a[176:179], a[40:43]\n\t"
"v_fma_f32 v77, v177, s52, v77\n\t"
"v_fma_f32 v78, v178, s52, v78\n\t"
"v_mfma_f32_16x16x32_bf16 a[40:43], a[216:219], a[172:175], a[40:43]\n\t"
"buffer_load_dwordx4 a[240:243], off, s[80:83], s20 offset:1024 sc1\n\t"
"v_mfma_f32_16x16x32_bf16 a[48:51], a[248:251], a[172:175], a[48:51]\n\t"
"buffer_load_dwordx4 a[212:215], off, s[80:83], s20 offset:2048 sc1\n\t"
"v_mfma_f32_16x16x32_bf16 a[48:51], a[216:219], a[176:179], a[48:51]\n\t"
"s_mov_b32 s20, 884736\n\t"
"v_fma_f32 v79, v179, s52, v79\n\t"
"v_fma_f32 v176, v76, s54, -v176\n\t"
"v_mfma_f32_16x16x32_bf16 a[48:51], a[220:223], a[180:183], a[48:51]\n\t"
"v_fma_f32 v177, v77, s54, -v177\n\t"
"v_fma_f32 v178, v78, s54, -v178\n\t"
"v_mfma_f32_16x16x32_bf16 a[56:59], a[220:223], a[184:187], a[56:59]\n\t"
"v_fma_f32 v179, v79, s54, -v179\n\t"
"v_fma_f32 v80, v180, s52, v80\n\t"
"v_mfma_f32_16x16x32_bf16 a[56:59], a[216:219], a[180:183], a[56:59]\n\t"
"v_fma_f32 v81, v181, s52, v81\n\t"
"v_fma_f32 v82, v182, s52, v82\n\t"
"v_mfma_f32_16x16x32_bf16 a[56:59], a[248:251], a[176:179], a[56:59]\n\t"
"buffer_load_dwordx4 a[244:247], off, s[80:83], s20 offset:0 sc1\n\t"
"v_mfma_f32_16x16x32_bf16 a[64:67], a[248:251], a[180:183], a[64:67]\n\t"
"v_fma_f32 v83, v183, s52, v83\n\t"
"v_fma_f32 v180, v80, s54, -v180\n\t"
"v_mfma_f32_16x16x32_bf16 a[64:67], a[216:219], a[184:187], a[64:67]\n\t"
"v_fma_f32 v181, v81, s54, -v181\n\t"
"v_fma_f32 v182, v82, s54, -v182\n\t"
"v_mfma_f32_16x16x32_bf16 a[64:67], a[220:223], a[188:191], a[64:67]\n\t"
"v_fma_f32 v183, v83, s54, -v183\n\t"
"v_fma_f32 v84, v184, s52, v84\n\t"
"v_mfma_f32_16x16x32_bf16 a[72:75], a[220:223], a[192:195], a[72:75]\n\t"
"v_fma_f32 v85, v185, s52, v85\n\t"
"v_fma_f32 v86, v186, s52, v86\n\t"
"v_mfma_f32_16x16x32_bf16 a[72:75], a[216:219], a[188:191], a[72:75]\n\t"
"v_fma_f32 v87, v187, s52, v87\n\t"
"v_fma_f32 v184, v84, s54, -v184\n\t"
"v_mfma_f32_16x16x32_bf16 a[72:75], a[248:251], a[184:187], a[72:75]\n\t"
"v_fma_f32 v185, v85, s54, -v185\n\t"
"v_fma_f32 v186, v86, s54, -v186\n\t"
"v_mfma_f32_16x16x32_bf16 a[80:83], a[248:251], a[188:191], a[80:83]\n\t"
"v_fma_f32 v187, v87, s54, -v187\n\t"
"v_fma_f32 v88, v188, s52, v88\n\t"
"v_mfma_f32_16x16x32_bf16 a[80:83], a[216:219], a[192:195], a[80:83]\n\t"
"v_fma_f32 v89, v189, s52, v89\n\t"
"v_fma_f32 v90, v190, s52, v90\n\t"
"v_mfma_f32_16x16x32_bf16 a[80:83], a[220:223], a[196:199], a[80:83]\n\t"
"v_fma_f32 v91, v191, s52, v91\n\t"
"v_fma_f32 v188, v88, s54, -v188\n\t"
"v_mfma_f32_16x16x32_bf16 a[88:91], a[220:223], a[200:203], a[88:91]\n\t"
"v_fma_f32 v189, v89, s54, -v189\n\t"
"v_fma_f32 v190, v90, s54, -v190\n\t"
"v_mfma_f32_16x16x32_bf16 a[88:91], a[216:219], a[196:199], a[88:91]\n\t"
"v_fma_f32 v191, v91, s54, -v191\n\t"
"v_fma_f32 v92, v192, s52, v92\n\t"
"v_mfma_f32_16x16x32_bf16 a[88:91], a[248:251], a[192:195], a[88:91]\n\t"
"v_fma_f32 v93, v193, s52, v93\n\t"
"v_fma_f32 v94, v194, s52, v94\n\t"
"v_mfma_f32_16x16x32_bf16 a[96:99], a[248:251], a[196:199], a[96:99]\n\t"
"s_waitcnt vmcnt(10)\n\t"
"v_fma_f32 v95, v195, s52, v95\n\t"
"v_fma_f32 v192, v92, s54, -v192\n\t"
"v_mfma_f32_16x16x32_bf16 a[96:99], a[216:219], a[200:203], a[96:99]\n\t"
"v_fma_f32 v193, v93, s54, -v193\n\t"
"v_fma_f32 v194, v94, s54, -v194\n\t"
"v_mfma_f32_16x16x32_bf16 a[96:99], a[220:223], a[204:207], a[96:99]\n\t"
"v_fma_f32 v195, v95, s54, -v195\n\t"
"v_mfma_f32_16x16x32_bf16 a[104:107], a[252:255], a[168:171], a[104:107]\n\t"
"buffer_load_dwordx4 a[248:251], off, s[80:83], s20 offset:1024 sc1\n\t"
"v_mfma_f32_16x16x32_bf16 a[104:107], v[96:99], a[176:179], a[104:107]\n\t"
"buffer_load_dwordx4 a[216:219], off, s[80:83], s20 offset:2048 sc1\n\t"
"v_mfma_f32_16x16x32_bf16 a[104:107], v[236:239], a[172:175], a[104:107]\n\t"
"s_mov_b32 s20, 897024\n\t"
"ds_read_b128 a[168:171], v222 offset:1792\n\t"
"v_mfma_f32_16x16x32_bf16 a[112:115], a[252:255], a[172:175], a[112:115]\n\t"
"Lpg22_%=:\n\t"
"s_cbranch_vccnz Lvr3_%=\n\t"
"ds_read_b128 a[172:175], v223 offset:7936\n\t"
"v_mfma_f32_16x16x32_bf16 a[112:115], v[236:239], a[176:179], a[112:115]\n\t"
"buffer_load_dwordx4 a[220:223], off, s[80:83], s20 offset:0 sc1\n\t"
"v_mfma_f32_16x16x32_bf16 a[112:115], v[96:99], a[180:183], a[112:115]\n\t"
"v_mfma_f32_16x16x32_bf16 a[120:123], v[96:99], a[184:187], a[120:123]\n\t"
"v_mfma_f32_16x16x32_bf16 a[120:123], v[236:239], a[180:183], a[120:123]\n\t"
"v_mfma_f32_16x16x32_bf16 a[120:123], a[252:255], a[176:179], a[120:123]\n\t"
"ds_read_b128 a[176:179], v223 offset:14080\n\t"
"v_mfma_f32_16x16x32_bf16 a[128:131], a[252:255], a[180:183], a[128:131]\n\t"
"ds_read_b128 a[180:183], v222 offset:21760\n\t"
"v_mfma_f32_16x16x32_bf16 a[128:131], v[236:239], a[184:187], a[128:131]\n\t"
"v_mfma_f32_16x16x32_bf16 a[128:131], v[96:99], a[188:191], a[128:131]\n\t"
"v_mfma_f32_16x16x32_bf16 a[136:139], v[96:99], a[192:195], a[136:139]\n\t"
"v_mfma_f32_16x16x32_bf16 a[136:139], v[236:239], a[188:191], a[136:139]\n\t"
"v_mfma_f32_16x16x32_bf16 a[136:139], a[252:255], a[184:187], a[136:139]\n\t"
"ds_read_b128 a[184:187], v223 offset:27904\n\t"
"v_mfma_f32_16x16x32_bf16 a[144:147], a[252:255], a[188:191], a[144:147]\n\t"
"ds_read_b128 a[188:191], v223 offset:34048\n\t"
"v_mfma_f32_16x16x32_bf16 a[144:147], v[236:239], a[192:195], a[144:147]\n\t"
"v_mfma_f32_16x16x32_bf16 a[144:147], v[96:99], a[196:199], a[144:147]\n\t"
"v_mfma_f32_16x16x32_bf16 a[152:155], v[96:99], a[200:203], a[152:155]\n\t"
"v_mfma_f32_16x16x32_bf16 a[152:155], v[236:239], a[196:199], a[152:155]\n\t"
"v_mfma_f32_16x16x32_bf16 a[152:155], a[252:255], a[192:195], a[152:155]\n\t"
"ds_read_b128 a[192:195], v222 offset:41728\n\t"
"v_mfma_f32_16x16x32_bf16 a[160:163], a[252:255], a[196:199], a[160:163]\n\t"
"buffer_load_dwordx4 a[252:255], off, s[80:83], s20 offset:1024 sc1\n\t"
"v_mfma_f32_16x16x32_bf16 a[160:163], v[236:239], a[200:203], a[160:163]\n\t"
"s_waitcnt vmcnt(11) lgkmcnt(4)\n\t"
"ds_read_b128 a[196:199], v223 offset:47872\n\t"
"v_mfma_f32_16x16x32_bf16 a[160:163], v[96:99], a[204:207], a[160:163]\n\t"
"ds_read_b128 a[200:203], v223 offset:54016\n\t"
"v_mfma_f32_16x16x32_bf16 v[200:203], a[224:227], a[168:171], v[200:203]\n\t"
"s_waitcnt lgkmcnt(5)\n\t"
"ds_read_b128 a[204:207], v222 offset:61696\n\t"
"v_mfma_f32_16x16x32_bf16 v[200:203], v[248:251], a[176:179], v[200:203]\n\t"
"v_mfma_f32_16x16x32_bf16 v[200:203], a[228:231], a[172:175], v[200:203]\n\t"
"v_mfma_f32_16x16x32_bf16 v[208:211], a[224:227], a[172:175], v[208:211]\n\t"
"s_waitcnt lgkmcnt(5)\n\t"
"v_mfma_f32_16x16x32_bf16 v[208:211], a[228:231], a[176:179], v[208:211]\n\t"
"v_mfma_f32_16x16x32_bf16 v[208:211], v[248:251], a[180:183], v[208:211]\n\t"
"s_waitcnt lgkmcnt(0)\n\t"
"s_barrier\n\t"
"v_mfma_f32_16x16x32_bf16 v[216:219], v[248:251], a[184:187], v[216:219]\n\t"
"v_mfma_f32_16x16x32_bf16 v[216:219], a[228:231], a[180:183], v[216:219]\n\t"
"v_mfma_f32_16x16x32_bf16 v[216:219], a[224:227], a[176:179], v[216:219]\n\t"
"v_pk_fma_f32 v[196:197], v[200:201], s[56:57], v[196:197]\n\t"
"v_pk_fma_f32 v[200:201], v[196:197], s[58:59], v[200:201] neg_lo:[0,0,1] neg_hi:[0,0,1]\n\t"
"v_pk_fma_f32 v[228:229], v[0:1], s[60:61], v[196:197]\n\t"
"v_pk_fma_f32 v[0:1], v[196:197], s[60:61], v[0:1]\n\t"
"v_pk_fma_f32 v[196:197], v[100:101], s[62:63], v[200:201]\n\t"
"v_pk_fma_f32 v[100:101], v[200:201], s[62:63], v[100:101]\n\t"
"v_pk_fma_f32 v[198:199], v[202:203], s[56:57], v[198:199]\n\t"
"v_pk_fma_f32 v[202:203], v[198:199], s[58:59], v[202:203] neg_lo:[0,0,1] neg_hi:[0,0,1]\n\t"
"v_pk_fma_f32 v[234:235], v[2:3], s[60:61], v[198:199]\n\t"
"v_pk_fma_f32 v[2:3], v[198:199], s[60:61], v[2:3]\n\t"
"v_pk_fma_f32 v[198:199], v[102:103], s[62:63], v[202:203]\n\t"
"v_pk_fma_f32 v[102:103], v[202:203], s[62:63], v[102:103]\n\t"
"v_mfma_f32_16x16x32_bf16 a[4:7], a[224:227], a[180:183], a[4:7]\n\t"
"v_cvt_pk_bf16_f32 v196, v196, v197\n\t"
"v_cvt_pk_bf16_f32 v200, v228, v229\n\t"
"v_mfma_f32_16x16x32_bf16 a[4:7], a[228:231], a[184:187], a[4:7]\n\t"
"v_cvt_pk_bf16_f32 v197, v198, v199\n\t"
"v_cvt_pk_bf16_f32 v201, v234, v235\n\t"
"v_mfma_f32_16x16x32_bf16 a[4:7], v[248:251], a[188:191], a[4:7]\n\t"
"v_pk_fma_f32 v[204:205], v[208:209], s[56:57], v[204:205]\n\t"
"v_pk_fma_f32 v[208:209], v[204:205], s[58:59], v[208:209] neg_lo:[0,0,1] neg_hi:[0,0,1]\n\t"
"v_pk_fma_f32 v[98:99], v[4:5], s[60:61], v[204:205]\n\t"
"v_pk_fma_f32 v[4:5], v[204:205], s[60:61], v[4:5]\n\t"
"v_pk_fma_f32 v[204:205], v[104:105], s[62:63], v[208:209]\n\t"
"v_pk_fma_f32 v[104:105], v[208:209], s[62:63], v[104:105]\n\t"
"v_mfma_f32_16x16x32_bf16 a[12:15], v[248:251], a[192:195], a[12:15]\n\t"
"v_cvt_pk_bf16_f32 v204, v204, v205\n\t"
"v_cvt_pk_bf16_f32 v208, v98, v99\n\t"
"v_mfma_f32_16x16x32_bf16 a[12:15], a[228:231], a[188:191], a[12:15]\n\t"
"v_mfma_f32_16x16x32_bf16 a[12:15], a[224:227], a[184:187], a[12:15]\n\t"
"v_pk_fma_f32 v[206:207], v[210:211], s[56:57], v[206:207]\n\t"
"v_pk_fma_f32 v[210:211], v[206:207], s[58:59], v[210:211] neg_lo:[0,0,1] neg_hi:[0,0,1]\n\t"
"v_pk_fma_f32 v[228:229], v[6:7], s[60:61], v[206:207]\n\t"
"v_pk_fma_f32 v[6:7], v[206:207], s[60:61], v[6:7]\n\t"
"v_pk_fma_f32 v[206:207], v[106:107], s[62:63], v[210:211]\n\t"
"v_pk_fma_f32 v[106:107], v[210:211], s[62:63], v[106:107]\n\t"
"v_mfma_f32_16x16x32_bf16 a[20:23], a[224:227], a[188:191], a[20:23]\n\t"
"v_cvt_pk_bf16_f32 v205, v206, v207\n\t"
"v_cvt_pk_bf16_f32 v209, v228, v229\n\t"
"v_mfma_f32_16x16x32_bf16 a[20:23], a[228:231], a[192:195], a[20:23]\n\t"
"v_accvgpr_read_b32 v224, a0\n\t"
"v_accvgpr_read_b32 v226, a4\n\t"
"v_mfma_f32_16x16x32_bf16 a[20:23], v[248:251], a[196:199], a[20:23]\n\t"
"v_accvgpr_read_b32 v225, a1\n\t"
"v_accvgpr_read_b32 v227, a5\n\t"
"v_pk_fma_f32 v[212:213], v[216:217], s[56:57], v[212:213]\n\t"
"v_pk_fma_f32 v[216:217], v[212:213], s[58:59], v[216:217] neg_lo:[0,0,1] neg_hi:[0,0,1]\n\t"
"v_pk_fma_f32 v[234:235], v[8:9], s[60:61], v[212:213]\n\t"
"v_pk_fma_f32 v[8:9], v[212:213], s[60:61], v[8:9]\n\t"
"v_pk_fma_f32 v[212:213], v[108:109], s[62:63], v[216:217]\n\t"
"v_pk_fma_f32 v[108:109], v[216:217], s[62:63], v[108:109]\n\t"
"v_mfma_f32_16x16x32_bf16 a[28:31], v[248:251], a[200:203], a[28:31]\n\t"
"v_cvt_pk_bf16_f32 v212, v212, v213\n\t"
"v_cvt_pk_bf16_f32 v216, v234, v235\n\t"
"v_mfma_f32_16x16x32_bf16 a[28:31], a[228:231], a[196:199], a[28:31]\n\t"
"v_accvgpr_read_b32 v230, a2\n\t"
"v_accvgpr_read_b32 v232, a6\n\t"
"v_mfma_f32_16x16x32_bf16 a[28:31], a[224:227], a[192:195], a[28:31]\n\t"
"v_accvgpr_read_b32 v231, a3\n\t"
"v_accvgpr_read_b32 v233, a7\n\t"
"v_pk_fma_f32 v[214:215], v[218:219], s[56:57], v[214:215]\n\t"
"v_pk_fma_f32 v[218:219], v[214:215], s[58:59], v[218:219] neg_lo:[0,0,1] neg_hi:[0,0,1]\n\t"
"v_pk_fma_f32 v[98:99], v[10:11], s[60:61], v[214:215]\n\t"
"v_pk_fma_f32 v[10:11], v[214:215], s[60:61], v[10:11]\n\t"
"v_pk_fma_f32 v[214:215], v[110:111], s[62:63], v[218:219]\n\t"
"v_pk_fma_f32 v[110:111], v[218:219], s[62:63], v[110:111]\n\t"
"v_mfma_f32_16x16x32_bf16 a[36:39], a[224:227], a[196:199], a[36:39]\n\t"
"s_waitcnt vmcnt(8)\n\t"
"v_cvt_pk_bf16_f32 v213, v214, v215\n\t"
"v_cvt_pk_bf16_f32 v217, v98, v99\n\t"
"v_mfma_f32_16x16x32_bf16 a[36:39], a[228:231], a[200:203], a[36:39]\n\t"
"v_accvgpr_read_b32 v236, a8\n\t"
"v_accvgpr_read_b32 v238, a12\n\t"
"v_mfma_f32_16x16x32_bf16 a[36:39], v[248:251], a[204:207], a[36:39]\n\t"
"buffer_load_dwordx4 a[224:227], off, s[80:83], s20 offset:2048 sc1\n\t"
"v_accvgpr_read_b32 v237, a9\n\t"
"v_accvgpr_read_b32 v239, a13\n\t"
"s_mov_b32 s20, 909312\n\t"
"v_pk_fma_f32 v[224:225], v[226:227], s[56:57], v[224:225]\n\t"
"v_pk_fma_f32 v[226:227], v[224:225], s[58:59], v[226:227] neg_lo:[0,0,1] neg_hi:[0,0,1]\n\t"
"v_pk_fma_f32 v[228:229], v[12:13], s[60:61], v[224:225]\n\t"
"v_pk_fma_f32 v[12:13], v[224:225], s[60:61], v[12:13]\n\t"
"v_pk_fma_f32 v[224:225], v[112:113], s[62:63], v[226:227]\n\t"
"v_pk_fma_f32 v[112:113], v[226:227], s[62:63], v[112:113]\n\t"
"v_mfma_f32_16x16x32_bf16 a[44:47], a[208:211], a[168:171], a[44:47]\n\t"
"v_cvt_pk_bf16_f32 v224, v224, v225\n\t"
"v_cvt_pk_bf16_f32 v228, v228, v229\n\t"
"v_mfma_f32_16x16x32_bf16 a[44:47], a[232:235], a[176:179], a[44:47]\n\t"
"v_accvgpr_write_b32 a0, v224\n\t"
"v_accvgpr_write_b32 a4, v228\n\t"
"v_mfma_f32_16x16x32_bf16 a[44:47], v[240:243], a[172:175], a[44:47]\n\t"
"buffer_load_dwordx4 a[228:231], off, s[80:83], s20 offset:0 sc1\n\t"
"v_accvgpr_read_b32 v224, a10\n\t"
"v_accvgpr_read_b32 v226, a14\n\t"
"v_pk_fma_f32 v[230:231], v[232:233], s[56:57], v[230:231]\n\t"
"v_pk_fma_f32 v[232:233], v[230:231], s[58:59], v[232:233] neg_lo:[0,0,1] neg_hi:[0,0,1]\n\t"
"v_pk_fma_f32 v[234:235], v[14:15], s[60:61], v[230:231]\n\t"
"v_pk_fma_f32 v[14:15], v[230:231], s[60:61], v[14:15]\n\t"
"v_pk_fma_f32 v[230:231], v[114:115], s[62:63], v[232:233]\n\t"
"v_pk_fma_f32 v[114:115], v[232:233], s[62:63], v[114:115]\n\t"
"v_mfma_f32_16x16x32_bf16 a[52:55], a[208:211], a[172:175], a[52:55]\n\t"
"v_cvt_pk_bf16_f32 v230, v230, v231\n\t"
"v_cvt_pk_bf16_f32 v234, v234, v235\n\t"
"v_mfma_f32_16x16x32_bf16 a[52:55], v[240:243], a[176:179], a[52:55]\n\t"
"v_accvgpr_write_b32 a1, v230\n\t"
"v_accvgpr_write_b32 a5, v234\n\t"
"v_mfma_f32_16x16x32_bf16 a[52:55], a[232:235], a[180:183], a[52:55]\n\t"
"v_accvgpr_read_b32 v225, a11\n\t"
"v_accvgpr_read_b32 v227, a15\n\t"
"v_accvgpr_read_b32 v230, a16\n\t"
"v_accvgpr_read_b32 v232, a20\n\t"
"v_accvgpr_read_b32 v231, a17\n\t"
"v_pk_fma_f32 v[236:237], v[238:239], s[56:57], v[236:237]\n\t"
"v_pk_fma_f32 v[238:239], v[236:237], s[58:59], v[238:239] neg_lo:[0,0,1] neg_hi:[0,0,1]\n\t"
"v_pk_fma_f32 v[98:99], v[16:17], s[60:61], v[236:237]\n\t"
"v_pk_fma_f32 v[16:17], v[236:237], s[60:61], v[16:17]\n\t"
"v_pk_fma_f32 v[236:237], v[116:117], s[62:63], v[238:239]\n\t"
"v_pk_fma_f32 v[116:117], v[238:239], s[62:63], v[116:117]\n\t"
"v_mfma_f32_16x16x32_bf16 a[60:63], a[232:235], a[184:187], a[60:63]\n\t"
"v_accvgpr_read_b32 v233, a21\n\t"
"v_cvt_pk_bf16_f32 v236, v236, v237\n\t"
"v_mfma_f32_16x16x32_bf16 a[60:63], v[240:243], a[180:183], a[60:63]\n\t"
"v_cvt_pk_bf16_f32 v98, v98, v99\n\t"
"v_accvgpr_write_b32 a8, v236\n\t"
"v_mfma_f32_16x16x32_bf16 a[60:63], a[208:211], a[176:179], a[60:63]\n\t"
"v_accvgpr_write_b32 a12, v98\n\t"
"v_accvgpr_read_b32 v236, a18\n\t"
"v_accvgpr_read_b32 v238, a22\n\t"
"v_accvgpr_read_b32 v237, a19\n\t"
"v_accvgpr_read_b32 v239, a23\n\t"
"v_pk_fma_f32 v[224:225], v[226:227], s[56:57], v[224:225]\n\t"
"v_pk_fma_f32 v[226:227], v[224:225], s[58:59], v[226:227] neg_lo:[0,0,1] neg_hi:[0,0,1]\n\t"
"v_pk_fma_f32 v[228:229], v[18:19], s[60:61], v[224:225]\n\t"
"v_pk_fma_f32 v[18:19], v[224:225], s[60:61], v[18:19]\n\t"
"v_pk_fma_f32 v[224:225], v[118:119], s[62:63], v[226:227]\n\t"
"v_pk_fma_f32 v[118:119], v[226:227], s[62:63], v[118:119]\n\t"
"v_pk_fma_f32 v[230:231], v[232:233], s[56:57], v[230:231]\n\t"
"v_pk_fma_f32 v[232:233], v[230:231], s[58:59], v[232:233] neg_lo:[0,0,1] neg_hi:[0,0,1]\n\t"
"v_pk_fma_f32 v[234:235], v[20:21], s[60:61], v[230:231]\n\t"
"v_pk_fma_f32 v[20:21], v[230:231], s[60:61], v[20:21]\n\t"
"v_pk_fma_f32 v[230:231], v[120:121], s[62:63], v[232:233]\n\t"
"v_pk_fma_f32 v[120:121], v[232:233], s[62:63], v[120:121]\n\t"
"v_mfma_f32_16x16x32_bf16 a[68:71], a[208:211], a[180:183], a[68:71]\n\t"
"v_cvt_pk_bf16_f32 v224, v224, v225\n\t"
"v_cvt_pk_bf16_f32 v228, v228, v229\n\t"
"v_mfma_f32_16x16x32_bf16 a[68:71], v[240:243], a[184:187], a[68:71]\n\t"
"v_cvt_pk_bf16_f32 v230, v230, v231\n\t"
"v_cvt_pk_bf16_f32 v234, v234, v235\n\t"
"v_mfma_f32_16x16x32_bf16 a[68:71], a[232:235], a[188:191], a[68:71]\n\t"
"v_accvgpr_write_b32 a9, v224\n\t"
"v_accvgpr_write_b32 a13, v228\n\t"
"v_accvgpr_write_b32 a16, v230\n\t"
"v_accvgpr_write_b32 a20, v234\n\t"
"v_accvgpr_read_b32 v224, a24\n\t"
"v_pk_fma_f32 v[236:237], v[238:239], s[56:57], v[236:237]\n\t"
"v_pk_fma_f32 v[238:239], v[236:237], s[58:59], v[238:239] neg_lo:[0,0,1] neg_hi:[0,0,1]\n\t"
"v_pk_fma_f32 v[98:99], v[22:23], s[60:61], v[236:237]\n\t"
"v_pk_fma_f32 v[22:23], v[236:237], s[60:61], v[22:23]\n\t"
"v_pk_fma_f32 v[236:237], v[122:123], s[62:63], v[238:239]\n\t"
"v_pk_fma_f32 v[122:123], v[238:239], s[62:63], v[122:123]\n\t"
"v_mfma_f32_16x16x32_bf16 a[76:79], a[232:235], a[192:195], a[76:79]\n\t"
"v_accvgpr_read_b32 v226, a28\n\t"
"v_accvgpr_read_b32 v225, a25\n\t"
"v_mfma_f32_16x16x32_bf16 a[76:79], v[240:243], a[188:191], a[76:79]\n\t"
"v_accvgpr_read_b32 v227, a29\n\t"
"v_cvt_pk_bf16_f32 v236, v236, v237\n\t"
"v_mfma_f32_16x16x32_bf16 a[76:79], a[208:211], a[184:187], a[76:79]\n\t"
"v_cvt_pk_bf16_f32 v98, v98, v99\n\t"
"v_accvgpr_write_b32 a17, v236\n\t"
"v_accvgpr_write_b32 a21, v98\n\t"
"v_accvgpr_read_b32 v230, a26\n\t"
"v_accvgpr_read_b32 v232, a30\n\t"
"v_pk_fma_f32 v[224:225], v[226:227], s[56:57], v[224:225]\n\t"
"v_pk_fma_f32 v[226:227], v[224:225], s[58:59], v[226:227] neg_lo:[0,0,1] neg_hi:[0,0,1]\n\t"
"v_pk_fma_f32 v[228:229], v[24:25], s[60:61], v[224:225]\n\t"
"v_pk_fma_f32 v[24:25], v[224:225], s[60:61], v[24:25]\n\t"
"v_pk_fma_f32 v[224:225], v[124:125], s[62:63], v[226:227]\n\t"
"v_pk_fma_f32 v[124:125], v[226:227], s[62:63], v[124:125]\n\t"
"v_mfma_f32_16x16x32_bf16 a[84:87], a[208:211], a[188:191], a[84:87]\n\t"
"v_accvgpr_read_b32 v231, a27\n\t"
"v_accvgpr_read_b32 v233, a31\n\t"
"v_mfma_f32_16x16x32_bf16 a[84:87], v[240:243], a[192:195], a[84:87]\n\t"
"v_cvt_pk_bf16_f32 v224, v224, v225\n\t"
"v_cvt_pk_bf16_f32 v228, v228, v229\n\t"
"v_mfma_f32_16x16x32_bf16 a[84:87], a[232:235], a[196:199], a[84:87]\n\t"
"v_accvgpr_write_b32 a24, v224\n\t"
"v_accvgpr_write_b32 a28, v228\n\t"
"v_accvgpr_read_b32 v236, a32\n\t"
"v_accvgpr_read_b32 v238, a36\n\t"
"v_accvgpr_read_b32 v237, a33\n\t"
"v_pk_fma_f32 v[230:231], v[232:233], s[56:57], v[230:231]\n\t"
"v_pk_fma_f32 v[232:233], v[230:231], s[58:59], v[232:233] neg_lo:[0,0,1] neg_hi:[0,0,1]\n\t"
"v_pk_fma_f32 v[234:235], v[26:27], s[60:61], v[230:231]\n\t"
"v_pk_fma_f32 v[26:27], v[230:231], s[60:61], v[26:27]\n\t"
"v_pk_fma_f32 v[230:231], v[126:127], s[62:63], v[232:233]\n\t"
"v_pk_fma_f32 v[126:127], v[232:233], s[62:63], v[126:127]\n\t"
"v_mfma_f32_16x16x32_bf16 a[92:95], a[232:235], a[200:203], a[92:95]\n\t"
"v_accvgpr_read_b32 v239, a37\n\t"
"v_cvt_pk_bf16_f32 v230, v230, v231\n\t"
"v_mfma_f32_16x16x32_bf16 a[92:95], v[240:243], a[196:199], a[92:95]\n\t"
"v_cvt_pk_bf16_f32 v234, v234, v235\n\t"
"v_accvgpr_write_b32 a25, v230\n\t"
"v_mfma_f32_16x16x32_bf16 a[92:95], a[208:211], a[192:195], a[92:95]\n\t"
"v_accvgpr_write_b32 a29, v234\n\t"
"v_accvgpr_read_b32 v224, a34\n\t"
"v_accvgpr_read_b32 v226, a38\n\t"
"v_accvgpr_read_b32 v225, a35\n\t"
"v_accvgpr_read_b32 v227, a39\n\t"
"v_pk_fma_f32 v[236:237], v[238:239], s[56:57], v[236:237]\n\t"
"v_pk_fma_f32 v[238:239], v[236:237], s[58:59], v[238:239] neg_lo:[0,0,1] neg_hi:[0,0,1]\n\t"
"v_pk_fma_f32 v[98:99], v[28:29], s[60:61], v[236:237]\n\t"
"v_pk_fma_f32 v[28:29], v[236:237], s[60:61], v[28:29]\n\t"
"v_pk_fma_f32 v[236:237], v[128:129], s[62:63], v[238:239]\n\t"
"v_pk_fma_f32 v[128:129], v[238:239], s[62:63], v[128:129]\n\t"
"v_mfma_f32_16x16x32_bf16 a[100:103], a[208:211], a[196:199], a[100:103]\n\t"
"s_waitcnt vmcnt(7)\n\t"
"v_cvt_pk_bf16_f32 v236, v236, v237\n\t"
"v_cvt_pk_bf16_f32 v98, v98, v99\n\t"
"v_mfma_f32_16x16x32_bf16 a[100:103], v[240:243], a[200:203], a[100:103]\n\t"
"v_accvgpr_write_b32 a32, v236\n\t"
"v_accvgpr_write_b32 a36, v98\n\t"
"v_mfma_f32_16x16x32_bf16 a[100:103], a[232:235], a[204:207], a[100:103]\n\t"
"buffer_load_dwordx4 a[208:211], off, s[80:83], s20 offset:1024 sc1\n\t"
"v_accvgpr_read_b32 v230, a40\n\t"
"v_accvgpr_read_b32 v232, a44\n\t"
"v_pk_fma_f32 v[224:225], v[226:227], s[56:57], v[224:225]\n\t"
"v_pk_fma_f32 v[226:227], v[224:225], s[58:59], v[226:227] neg_lo:[0,0,1] neg_hi:[0,0,1]\n\t"
"v_pk_fma_f32 v[228:229], v[30:31], s[60:61], v[224:225]\n\t"
"v_pk_fma_f32 v[30:31], v[224:225], s[60:61], v[30:31]\n\t"
"v_pk_fma_f32 v[224:225], v[130:131], s[62:63], v[226:227]\n\t"
"v_pk_fma_f32 v[130:131], v[226:227], s[62:63], v[130:131]\n\t"
"v_mfma_f32_16x16x32_bf16 a[108:111], a[236:239], a[168:171], a[108:111]\n\t"
"ds_read_b128 a[168:171], v244 offset:0\n\t"
"v_accvgpr_read_b32 v231, a41\n\t"
"v_mfma_f32_16x16x32_bf16 a[108:111], a[212:215], a[176:179], a[108:111]\n\t"
"v_accvgpr_read_b32 v233, a45\n\t"
"v_cvt_pk_bf16_f32 v224, v224, v225\n\t"
"v_mfma_f32_16x16x32_bf16 a[108:111], a[240:243], a[172:175], a[108:111]\n\t"
"buffer_load_dwordx4 a[232:235], off, s[80:83], s20 offset:2048 sc1\n\t"
"v_cvt_pk_bf16_f32 v228, v228, v229\n\t"
"v_accvgpr_write_b32 a33, v224\n\t"
"s_mov_b32 s20, 1069056\n\t"
"v_pk_fma_f32 v[230:231], v[232:233], s[56:57], v[230:231]\n\t"
"v_pk_fma_f32 v[232:233], v[230:231], s[58:59], v[232:233] neg_lo:[0,0,1] neg_hi:[0,0,1]\n\t"
"v_pk_fma_f32 v[234:235], v[32:33], s[60:61], v[230:231]\n\t"
"v_pk_fma_f32 v[32:33], v[230:231], s[60:61], v[32:33]\n\t"
"v_pk_fma_f32 v[230:231], v[132:133], s[62:63], v[232:233]\n\t"
"v_pk_fma_f32 v[132:133], v[232:233], s[62:63], v[132:133]\n\t"
"v_mfma_f32_16x16x32_bf16 a[116:119], a[236:239], a[172:175], a[116:119]\n\t"
"ds_read_b128 a[172:175], v244 offset:512\n\t"
"v_accvgpr_write_b32 a37, v228\n\t"
"v_mfma_f32_16x16x32_bf16 a[116:119], a[240:243], a[176:179], a[116:119]\n\t"
"v_accvgpr_read_b32 v236, a42\n\t"
"v_accvgpr_read_b32 v238, a46\n\t"
"v_mfma_f32_16x16x32_bf16 a[116:119], a[212:215], a[180:183], a[116:119]\n\t"
"buffer_load_dwordx4 v[240:243], off, s[80:83], s20 offset:0 sc1\n\t"
"v_accvgpr_read_b32 v237, a43\n\t"
"v_accvgpr_read_b32 v239, a47\n\t"
"v_pk_fma_f32 v[236:237], v[238:239], s[56:57], v[236:237]\n\t"
"v_pk_fma_f32 v[238:239], v[236:237], s[58:59], v[238:239] neg_lo:[0,0,1] neg_hi:[0,0,1]\n\t"
"v_pk_fma_f32 v[98:99], v[34:35], s[60:61], v[236:237]\n\t"
"v_pk_fma_f32 v[34:35], v[236:237], s[60:61], v[34:35]\n\t"
"v_pk_fma_f32 v[236:237], v[134:135], s[62:63], v[238:239]\n\t"
"v_pk_fma_f32 v[134:135], v[238:239], s[62:63], v[134:135]\n\t"
"v_mfma_f32_16x16x32_bf16 a[124:127], a[212:215], a[184:187], a[124:127]\n\t"
"v_cvt_pk_bf16_f32 v198, v230, v231\n\t"
"v_cvt_pk_bf16_f32 v202, v234, v235\n\t"
"v_mfma_f32_16x16x32_bf16 a[124:127], a[240:243], a[180:183], a[124:127]\n\t"
"v_accvgpr_read_b32 v224, a48\n\t"
"v_accvgpr_read_b32 v226, a52\n\t"
"v_mfma_f32_16x16x32_bf16 a[124:127], a[236:239], a[176:179], a[124:127]\n\t"
"global_load_dwordx4 v[248:251], v252, s[48:49] offset:0\n\t"
"ds_read_b128 a[176:179], v244 offset:1024\n\t"
"v_accvgpr_read_b32 v225, a49\n\t"
"v_accvgpr_read_b32 v227, a53\n\t"
"v_accvgpr_read_b32 v230, a50\n\t"
"v_accvgpr_read_b32 v232, a54\n\t"
"v_accvgpr_read_b32 v231, a51\n\t"
"v_accvgpr_read_b32 v233, a55\n\t"
"v_pk_fma_f32 v[224:225], v[226:227], s[56:57], v[224:225]\n\t"
"v_pk_fma_f32 v[226:227], v[224:225], s[58:59], v[226:227] neg_lo:[0,0,1] neg_hi:[0,0,1]\n\t"
"v_pk_fma_f32 v[228:229], v[36:37], s[60:61], v[224:225]\n\t"
"v_pk_fma_f32 v[36:37], v[224:225], s[60:61], v[36:37]\n\t"
"v_pk_fma_f32 v[224:225], v[136:137], s[62:63], v[226:227]\n\t"
"v_pk_fma_f32 v[136:137], v[226:227], s[62:63], v[136:137]\n\t"
"v_pk_fma_f32 v[230:231], v[232:233], s[56:57], v[230:231]\n\t"
"v_pk_fma_f32 v[232:233], v[230:231], s[58:59], v[232:233] neg_lo:[0,0,1] neg_hi:[0,0,1]\n\t"
"v_pk_fma_f32 v[234:235], v[38:39], s[60:61], v[230:231]\n\t"
"v_pk_fma_f32 v[38:39], v[230:231], s[60:61], v[38:39]\n\t"
"v_pk_fma_f32 v[230:231], v[138:139], s[62:63], v[232:233]\n\t"
"v_pk_fma_f32 v[138:139], v[232:233], s[62:63], v[138:139]\n\t"
"v_mfma_f32_16x16x32_bf16 a[132:135], a[236:239], a[180:183], a[132:135]\n\t"
"ds_read_b128 a[180:183], v244 offset:1536\n\t"
"v_cvt_pk_bf16_f32 v199, v236, v237\n\t"
"v_mfma_f32_16x16x32_bf16 a[132:135], a[240:243], a[184:187], a[132:135]\n\t"
"v_cvt_pk_bf16_f32 v203, v98, v99\n\t"
"v_accvgpr_read_b32 v236, a56\n\t"
"v_mfma_f32_16x16x32_bf16 a[132:135], a[212:215], a[188:191], a[132:135]\n\t"
"buffer_load_dwordx4 a[52:55], off, s[80:83], s20 offset:1024 sc1\n\t"
"v_accvgpr_read_b32 v238, a60\n\t"
"v_accvgpr_read_b32 v237, a57\n\t"
"v_accvgpr_read_b32 v239, a61\n\t"
"v_pk_fma_f32 v[236:237], v[238:239], s[56:57], v[236:237]\n\t"
"v_pk_fma_f32 v[238:239], v[236:237], s[58:59], v[238:239] neg_lo:[0,0,1] neg_hi:[0,0,1]\n\t"
"v_pk_fma_f32 v[98:99], v[40:41], s[60:61], v[236:237]\n\t"
"v_pk_fma_f32 v[40:41], v[236:237], s[60:61], v[40:41]\n\t"
"v_pk_fma_f32 v[236:237], v[140:141], s[62:63], v[238:239]\n\t"
"v_pk_fma_f32 v[140:141], v[238:239], s[62:63], v[140:141]\n\t"
"v_mfma_f32_16x16x32_bf16 a[140:143], a[212:215], a[192:195], a[140:143]\n\t"
"v_cvt_pk_bf16_f32 v206, v224, v225\n\t"
"v_cvt_pk_bf16_f32 v210, v228, v229\n\t"
"v_mfma_f32_16x16x32_bf16 a[140:143], a[240:243], a[188:191], a[140:143]\n\t"
"v_cvt_pk_bf16_f32 v207, v230, v231\n\t"
"v_cvt_pk_bf16_f32 v211, v234, v235\n\t"
"v_mfma_f32_16x16x32_bf16 a[140:143], a[236:239], a[184:187], a[140:143]\n\t"
"ds_read_b128 a[184:187], v244 offset:2048\n\t"
"v_accvgpr_read_b32 v224, a58\n\t"
"v_accvgpr_read_b32 v226, a62\n\t"
"v_accvgpr_read_b32 v225, a59\n\t"
"v_accvgpr_read_b32 v227, a63\n\t"
"v_pk_fma_f32 v[224:225], v[226:227], s[56:57], v[224:225]\n\t"
"v_pk_fma_f32 v[226:227], v[224:225], s[58:59], v[226:227] neg_lo:[0,0,1] neg_hi:[0,0,1]\n\t"
"v_pk_fma_f32 v[228:229], v[42:43], s[60:61], v[224:225]\n\t"
"v_pk_fma_f32 v[42:43], v[224:225], s[60:61], v[42:43]\n\t"
"v_pk_fma_f32 v[224:225], v[142:143], s[62:63], v[226:227]\n\t"
"v_pk_fma_f32 v[142:143], v[226:227], s[62:63], v[142:143]\n\t"
"v_mfma_f32_16x16x32_bf16 a[148:151], a[236:239], a[188:191], a[148:151]\n\t"
"ds_read_b128 a[188:191], v244 offset:2560\n\t"
"v_cvt_pk_bf16_f32 v214, v236, v237\n\t"
"v_mfma_f32_16x16x32_bf16 a[148:151], a[240:243], a[192:195], a[148:151]\n\t"
"v_cvt_pk_bf16_f32 v218, v98, v99\n\t"
"v_accvgpr_read_b32 v230, a64\n\t"
"v_mfma_f32_16x16x32_bf16 a[148:151], a[212:215], a[196:199], a[148:151]\n\t"
"buffer_load_dwordx4 a[56:59], off, s[80:83], s20 offset:2048 sc1\n\t"
"v_accvgpr_read_b32 v232, a68\n\t"
"v_accvgpr_read_b32 v231, a65\n\t"
"v_accvgpr_read_b32 v233, a69\n\t"
"v_pk_fma_f32 v[230:231], v[232:233], s[56:57], v[230:231]\n\t"
"v_pk_fma_f32 v[232:233], v[230:231], s[58:59], v[232:233] neg_lo:[0,0,1] neg_hi:[0,0,1]\n\t"
"v_pk_fma_f32 v[234:235], v[44:45], s[60:61], v[230:231]\n\t"
"v_pk_fma_f32 v[44:45], v[230:231], s[60:61], v[44:45]\n\t"
"v_pk_fma_f32 v[230:231], v[144:145], s[62:63], v[232:233]\n\t"
"v_pk_fma_f32 v[144:145], v[232:233], s[62:63], v[144:145]\n\t"
"v_mfma_f32_16x16x32_bf16 a[156:159], a[212:215], a[200:203], a[156:159]\n\t"
"s_mov_b32 s20, 1081344\n\t"
"v_cvt_pk_bf16_f32 v215, v224, v225\n\t"
"v_cvt_pk_bf16_f32 v219, v228, v229\n\t"
"v_mfma_f32_16x16x32_bf16 a[156:159], a[240:243], a[196:199], a[156:159]\n\t"
"v_accvgpr_read_b32 v236, a66\n\t"
"v_accvgpr_read_b32 v238, a70\n\t"
"v_mfma_f32_16x16x32_bf16 a[156:159], a[236:239], a[192:195], a[156:159]\n\t"
"ds_read_b128 a[192:195], v244 offset:3072\n\t"
"buffer_load_dwordx4 a[60:63], off, s[80:83], s20 offset:0 sc1\n\t"
"v_accvgpr_read_b32 v237, a67\n\t"
"v_accvgpr_read_b32 v239, a71\n\t"
"v_cvt_pk_bf16_f32 v230, v230, v231\n\t"
"v_cvt_pk_bf16_f32 v234, v234, v235\n\t"
"v_pk_fma_f32 v[236:237], v[238:239], s[56:57], v[236:237]\n\t"
"v_pk_fma_f32 v[238:239], v[236:237], s[58:59], v[238:239] neg_lo:[0,0,1] neg_hi:[0,0,1]\n\t"
"v_pk_fma_f32 v[98:99], v[46:47], s[60:61], v[236:237]\n\t"
"v_pk_fma_f32 v[46:47], v[236:237], s[60:61], v[46:47]\n\t"
"v_pk_fma_f32 v[236:237], v[146:147], s[62:63], v[238:239]\n\t"
"v_pk_fma_f32 v[146:147], v[238:239], s[62:63], v[146:147]\n\t"
"v_mfma_f32_16x16x32_bf16 a[164:167], a[236:239], a[196:199], a[164:167]\n\t"
"s_waitcnt vmcnt(11) lgkmcnt(4)\n\t"
"ds_read_b128 a[196:199], v244 offset:3584\n\t"
"v_accvgpr_write_b32 a2, v230\n\t"
"v_mfma_f32_16x16x32_bf16 a[164:167], a[240:243], a[200:203], a[164:167]\n\t"
"ds_read_b128 a[200:203], v244 offset:4096\n\t"
"v_accvgpr_write_b32 a6, v234\n\t"
"v_mfma_f32_16x16x32_bf16 a[164:167], a[212:215], a[204:207], a[164:167]\n\t"
"buffer_load_dwordx4 a[236:239], off, s[80:83], s20 offset:1024 sc1\n\t"
"ds_read_b128 a[204:207], v244 offset:4608\n\t"
"v_accvgpr_read_b32 v224, a72\n\t"
"v_accvgpr_read_b32 v226, a76\n\t"
"v_accvgpr_read_b32 v225, a73\n\t"
"v_accvgpr_read_b32 v227, a77\n\t"
"v_cvt_pk_bf16_f32 v236, v236, v237\n\t"
"v_cvt_pk_bf16_f32 v98, v98, v99\n\t"
"v_pk_fma_f32 v[224:225], v[226:227], s[56:57], v[224:225]\n\t"
"v_pk_fma_f32 v[226:227], v[224:225], s[58:59], v[226:227] neg_lo:[0,0,1] neg_hi:[0,0,1]\n\t"
"v_pk_fma_f32 v[228:229], v[48:49], s[60:61], v[224:225]\n\t"
"v_pk_fma_f32 v[48:49], v[224:225], s[60:61], v[48:49]\n\t"
"v_pk_fma_f32 v[224:225], v[148:149], s[62:63], v[226:227]\n\t"
"v_pk_fma_f32 v[148:149], v[226:227], s[62:63], v[148:149]\n\t"
"Lst32_%=:\n\t"
"v_mfma_f32_16x16x32_bf16 v[0:3], a[244:247], a[168:171], v[0:3]\n\t"
"s_waitcnt lgkmcnt(6)\n\t"
"v_accvgpr_write_b32 a3, v236\n\t"
"v_accvgpr_write_b32 a7, v98\n\t"
"v_mfma_f32_16x16x32_bf16 v[0:3], a[216:219], a[176:179], v[0:3]\n\t"
"v_accvgpr_read_b32 v230, a74\n\t"
"v_accvgpr_read_b32 v232, a78\n\t"
"v_mfma_f32_16x16x32_bf16 v[0:3], a[248:251], a[172:175], v[0:3]\n\t"
"buffer_load_dwordx4 a[212:215], off, s[80:83], s20 offset:2048 sc1\n\t"
"v_accvgpr_read_b32 v231, a75\n\t"
"v_accvgpr_read_b32 v233, a79\n\t"
"v_cvt_pk_bf16_f32 v224, v224, v225\n\t"
"v_cvt_pk_bf16_f32 v228, v228, v229\n\t"
"v_pk_fma_f32 v[230:231], v[232:233], s[56:57], v[230:231]\n\t"
"v_pk_fma_f32 v[232:233], v[230:231], s[58:59], v[232:233] neg_lo:[0,0,1] neg_hi:[0,0,1]\n\t"
"v_pk_fma_f32 v[234:235], v[50:51], s[60:61], v[230:231]\n\t"
"v_pk_fma_f32 v[50:51], v[230:231], s[60:61], v[50:51]\n\t"
"v_pk_fma_f32 v[230:231], v[150:151], s[62:63], v[232:233]\n\t"
"v_pk_fma_f32 v[150:151], v[232:233], s[62:63], v[150:151]\n\t"
"v_mfma_f32_16x16x32_bf16 v[4:7], a[244:247], a[172:175], v[4:7]\n\t"
"s_waitcnt lgkmcnt(5)\n\t"
"s_mov_b32 s20, 1093632\n\t"
"v_accvgpr_write_b32 a10, v224\n\t"
"v_mfma_f32_16x16x32_bf16 v[4:7], a[248:251], a[176:179], v[4:7]\n\t"
"v_accvgpr_write_b32 a14, v228\n\t"
"v_accvgpr_read_b32 v236, a80\n\t"
"v_mfma_f32_16x16x32_bf16 v[4:7], a[216:219], a[180:183], v[4:7]\n\t"
"buffer_load_dwordx4 a[240:243], off, s[80:83], s20 offset:0 sc1\n\t"
"v_accvgpr_read_b32 v238, a84\n\t"
"v_accvgpr_read_b32 v237, a81\n\t"
"v_accvgpr_read_b32 v239, a85\n\t"
"v_cvt_pk_bf16_f32 v230, v230, v231\n\t"
"v_cvt_pk_bf16_f32 v234, v234, v235\n\t"
"v_pk_fma_f32 v[236:237], v[238:239], s[56:57], v[236:237]\n\t"
"v_pk_fma_f32 v[238:239], v[236:237], s[58:59], v[238:239] neg_lo:[0,0,1] neg_hi:[0,0,1]\n\t"
"v_pk_fma_f32 v[98:99], v[52:53], s[60:61], v[236:237]\n\t"
"v_pk_fma_f32 v[52:53], v[236:237], s[60:61], v[52:53]\n\t"
"v_pk_fma_f32 v[236:237], v[152:153], s[62:63], v[238:239]\n\t"
"v_pk_fma_f32 v[152:153], v[238:239], s[62:63], v[152:153]\n\t"
"v_mfma_f32_16x16x32_bf16 v[8:11], a[216:219], a[184:187], v[8:11]\n\t"
"s_waitcnt lgkmcnt(4)\n\t"
"v_accvgpr_write_b32 a11, v230\n\t"
"v_accvgpr_write_b32 a15, v234\n\t"
"v_mfma_f32_16x16x32_bf16 v[8:11], a[248:251], a[180:183], v[8:11]\n\t"
"v_accvgpr_read_b32 v224, a82\n\t"
"v_accvgpr_read_b32 v226, a86\n\t"
"v_mfma_f32_16x16x32_bf16 v[8:11], a[244:247], a[176:179], v[8:11]\n\t"
"buffer_load_dwordx4 a[64:67], off, s[80:83], s20 offset:1024 sc1\n\t"
"v_accvgpr_read_b32 v225, a83\n\t"
"v_accvgpr_read_b32 v227, a87\n\t"
"v_cvt_pk_bf16_f32 v236, v236, v237\n\t"
"v_cvt_pk_bf16_f32 v98, v98, v99\n\t"
"v_pk_fma_f32 v[224:225], v[226:227], s[56:57], v[224:225]\n\t"
"v_pk_fma_f32 v[226:227], v[224:225], s[58:59], v[226:227] neg_lo:[0,0,1] neg_hi:[0,0,1]\n\t"
"v_pk_fma_f32 v[228:229], v[54:55], s[60:61], v[224:225]\n\t"
"v_pk_fma_f32 v[54:55], v[224:225], s[60:61], v[54:55]\n\t"
"v_pk_fma_f32 v[224:225], v[154:155], s[62:63], v[226:227]\n\t"
"v_pk_fma_f32 v[154:155], v[226:227], s[62:63], v[154:155]\n\t"
"v_mfma_f32_16x16x32_bf16 v[12:15], a[244:247], a[180:183], v[12:15]\n\t"
"s_waitcnt lgkmcnt(3)\n\t"
"v_accvgpr_write_b32 a18, v236\n\t"
"v_accvgpr_write_b32 a22, v98\n\t"
"v_mfma_f32_16x16x32_bf16 v[12:15], a[248:251], a[184:187], v[12:15]\n\t"
"v_accvgpr_read_b32 v230, a88\n\t"
"v_accvgpr_read_b32 v232, a92\n\t"
"v_mfma_f32_16x16x32_bf16 v[12:15], a[216:219], a[188:191], v[12:15]\n\t"
"buffer_load_dwordx4 a[68:71], off, s[80:83], s20 offset:2048 sc1\n\t"
"v_accvgpr_read_b32 v231, a89\n\t"
"v_accvgpr_read_b32 v233, a93\n\t"
"v_accvgpr_read_b32 v236, a90\n\t"
"v_accvgpr_read_b32 v238, a94\n\t"
"v_accvgpr_read_b32 v237, a91\n\t"
"v_accvgpr_read_b32 v239, a95\n\t"
"v_cvt_pk_bf16_f32 v224, v224, v225\n\t"
"v_cvt_pk_bf16_f32 v228, v228, v229\n\t"
"v_pk_fma_f32 v[230:231], v[232:233], s[56:57], v[230:231]\n\t"
"v_pk_fma_f32 v[232:233], v[230:231], s[58:59], v[232:233] neg_lo:[0,0,1] neg_hi:[0,0,1]\n\t"
"v_pk_fma_f32 v[234:235], v[56:57], s[60:61], v[230:231]\n\t"
"v_pk_fma_f32 v[56:57], v[230:231], s[60:61], v[56:57]\n\t"
"v_pk_fma_f32 v[230:231], v[156:157], s[62:63], v[232:233]\n\t"
"v_pk_fma_f32 v[156:157], v[232:233], s[62:63], v[156:157]\n\t"
"v_pk_fma_f32 v[236:237], v[238:239], s[56:57], v[236:237]\n\t"
"v_pk_fma_f32 v[238:239], v[236:237], s[58:59], v[238:239] neg_lo:[0,0,1] neg_hi:[0,0,1]\n\t"
"v_pk_fma_f32 v[98:99], v[58:59], s[60:61], v[236:237]\n\t"
"v_pk_fma_f32 v[58:59], v[236:237], s[60:61], v[58:59]\n\t"
"v_pk_fma_f32 v[236:237], v[158:159], s[62:63], v[238:239]\n\t"
"v_pk_fma_f32 v[158:159], v[238:239], s[62:63], v[158:159]\n\t"
"v_mfma_f32_16x16x32_bf16 v[16:19], a[216:219], a[192:195], v[16:19]\n\t"
"Lpg23_%=:\n\t"
"s_cbranch_vccnz Lvr3_%=\n\t"
"s_waitcnt lgkmcnt(2)\n\t"
"s_mov_b32 s20, 589824\n\t"
"v_mfma_f32_16x16x32_bf16 v[16:19], a[248:251], a[188:191], v[16:19]\n\t"
"buffer_load_dwordx4 a[72:75], off, s[80:83], s20 offset:0 sc1\n\t"
"v_mfma_f32_16x16x32_bf16 v[16:19], a[244:247], a[184:187], v[16:19]\n\t"
"v_accvgpr_write_b32 a19, v224\n\t"
"v_accvgpr_write_b32 a23, v228\n\t"
"v_accvgpr_read_b32 v224, a96\n\t"
"v_accvgpr_read_b32 v226, a100\n\t"
"v_accvgpr_read_b32 v225, a97\n\t"
"v_accvgpr_read_b32 v227, a101\n\t"
"v_cvt_pk_bf16_f32 v230, v230, v231\n\t"
"v_cvt_pk_bf16_f32 v234, v234, v235\n\t"
"v_pk_fma_f32 v[224:225], v[226:227], s[56:57], v[224:225]\n\t"
"v_pk_fma_f32 v[226:227], v[224:225], s[58:59], v[226:227] neg_lo:[0,0,1] neg_hi:[0,0,1]\n\t"
"v_pk_fma_f32 v[228:229], v[60:61], s[60:61], v[224:225]\n\t"
"v_pk_fma_f32 v[60:61], v[224:225], s[60:61], v[60:61]\n\t"
"v_pk_fma_f32 v[224:225], v[160:161], s[62:63], v[226:227]\n\t"
"v_pk_fma_f32 v[160:161], v[226:227], s[62:63], v[160:161]\n\t"
"v_mfma_f32_16x16x32_bf16 v[20:23], a[244:247], a[188:191], v[20:23]\n\t"
"s_waitcnt lgkmcnt(1)\n\t"
"v_accvgpr_write_b32 a26, v230\n\t"
"v_cvt_pk_bf16_f32 v236, v236, v237\n\t"
"v_mfma_f32_16x16x32_bf16 v[20:23], a[248:251], a[192:195], v[20:23]\n\t"
"v_accvgpr_write_b32 a30, v234\n\t"
"v_cvt_pk_bf16_f32 v98, v98, v99\n\t"
"v_mfma_f32_16x16x32_bf16 v[20:23], a[216:219], a[196:199], v[20:23]\n\t"
"buffer_load_dwordx4 a[76:79], off, s[80:83], s20 offset:1024 sc1\n\t"
"v_accvgpr_read_b32 v230, a98\n\t"
"v_accvgpr_read_b32 v232, a102\n\t"
"v_accvgpr_read_b32 v231, a99\n\t"
"v_accvgpr_read_b32 v233, a103\n\t"
"v_accvgpr_write_b32 a27, v236\n\t"
"v_accvgpr_write_b32 a31, v98\n\t"
"v_cvt_pk_bf16_f32 v224, v224, v225\n\t"
"v_cvt_pk_bf16_f32 v228, v228, v229\n\t"
"v_pk_fma_f32 v[230:231], v[232:233], s[56:57], v[230:231]\n\t"
"v_pk_fma_f32 v[232:233], v[230:231], s[58:59], v[232:233] neg_lo:[0,0,1] neg_hi:[0,0,1]\n\t"
"v_pk_fma_f32 v[234:235], v[62:63], s[60:61], v[230:231]\n\t"
"v_pk_fma_f32 v[62:63], v[230:231], s[60:61], v[62:63]\n\t"
"v_pk_fma_f32 v[230:231], v[162:163], s[62:63], v[232:233]\n\t"
"v_pk_fma_f32 v[162:163], v[232:233], s[62:63], v[162:163]\n\t"
"v_mfma_f32_16x16x32_bf16 v[24:27], a[216:219], a[200:203], v[24:27]\n\t"
"s_waitcnt lgkmcnt(0)\n\t"
"v_accvgpr_write_b32 a34, v224\n\t"
"v_accvgpr_write_b32 a38, v228\n\t"
"v_mfma_f32_16x16x32_bf16 v[24:27], a[248:251], a[196:199], v[24:27]\n\t"
"v_accvgpr_read_b32 v236, a104\n\t"
"v_accvgpr_read_b32 v238, a108\n\t"
"v_mfma_f32_16x16x32_bf16 v[24:27], a[244:247], a[192:195], v[24:27]\n\t"
"buffer_load_dwordx4 a[80:83], off, s[80:83], s20 offset:2048 sc1\n\t"
"v_accvgpr_read_b32 v237, a105\n\t"
"v_accvgpr_read_b32 v239, a109\n\t"
"v_cvt_pk_bf16_f32 v230, v230, v231\n\t"
"v_cvt_pk_bf16_f32 v234, v234, v235\n\t"
"v_pk_fma_f32 v[236:237], v[238:239], s[56:57], v[236:237]\n\t"
"v_pk_fma_f32 v[238:239], v[236:237], s[58:59], v[238:239] neg_lo:[0,0,1] neg_hi:[0,0,1]\n\t"
"v_pk_fma_f32 v[98:99], v[64:65], s[60:61], v[236:237]\n\t"
"v_pk_fma_f32 v[64:65], v[236:237], s[60:61], v[64:65]\n\t"
"v_pk_fma_f32 v[236:237], v[164:165], s[62:63], v[238:239]\n\t"
"v_pk_fma_f32 v[164:165], v[238:239], s[62:63], v[164:165]\n\t"
"v_mfma_f32_16x16x32_bf16 v[28:31], a[244:247], a[196:199], v[28:31]\n\t"
"s_waitcnt vmcnt(16)\n\t"
"s_mov_b32 s20, 602112\n\t"
"s_waitcnt vmcnt(11)\n\t"
"v_lshrrev_b32 v245, 10, v249\n\t"
"v_mfma_f32_16x16x32_bf16 v[28:31], a[248:251], a[200:203], v[28:31]\n\t"
"v_lshl_add_u32 v245, v245, 9, v249\n\t"
"v_lshrrev_b32 v246, 10, v250\n\t"
"v_mfma_f32_16x16x32_bf16 v[28:31], a[216:219], a[204:207], v[28:31]\n\t"
"buffer_load_dwordx4 a[244:247], off, s[80:83], s20 offset:0 sc1\n\t"
"v_accvgpr_read_b32 v224, a106\n\t"
"v_accvgpr_read_b32 v226, a110\n\t"
"v_accvgpr_read_b32 v225, a107\n\t"
"v_accvgpr_read_b32 v227, a111\n\t"
"v_lshl_add_u32 v246, v246, 9, v250\n\t"
"v_lshrrev_b32 v255, 10, v252\n\t"
"v_lshl_add_u32 v255, v255, 9, v252\n\t"
"v_accvgpr_write_b32 a35, v230\n\t"
"v_accvgpr_write_b32 a39, v234\n\t"
"v_cvt_pk_bf16_f32 v236, v236, v237\n\t"
"v_cvt_pk_bf16_f32 v98, v98, v99\n\t"
"v_add_u32 v245, 44032, v245\n\t"
"v_add_u32 v249, 55296, v245\n\t"
"v_add_u32 v246, 44032, v246\n\t"
"v_add_u32 v250, 55296, v246\n\t"
"v_add_u32 v255, 44032, v255\n\t"
"v_add_u32 v247, 55296, v255\n\t"
"v_pk_fma_f32 v[224:225], v[226:227], s[56:57], v[224:225]\n\t"
"v_pk_fma_f32 v[226:227], v[224:225], s[58:59], v[226:227] neg_lo:[0,0,1] neg_hi:[0,0,1]\n\t"
"v_pk_fma_f32 v[228:229], v[66:67], s[60:61], v[224:225]\n\t"
"v_pk_fma_f32 v[66:67], v[224:225], s[60:61], v[66:67]\n\t"
"v_pk_fma_f32 v[224:225], v[166:167], s[62:63], v[226:227]\n\t"
"v_pk_fma_f32 v[166:167], v[226:227], s[62:63], v[166:167]\n\t"
"v_mfma_f32_16x16x32_bf16 v[32:35], a[220:223], a[168:171], v[32:35]\n\t"
"v_accvgpr_write_b32 a104, v236\n\t"
"v_accvgpr_write_b32 a106, v98\n\t"
"v_mfma_f32_16x16x32_bf16 v[32:35], a[224:227], a[176:179], v[32:35]\n\t"
"v_accvgpr_read_b32 v230, a112\n\t"
"v_accvgpr_read_b32 v232, a116\n\t"
"v_mfma_f32_16x16x32_bf16 v[32:35], a[252:255], a[172:175], v[32:35]\n\t"
"buffer_load_dwordx4 a[216:219], off, s[80:83], s20 offset:1024 sc1\n\t"
"v_accvgpr_read_b32 v231, a113\n\t"
"v_accvgpr_read_b32 v233, a117\n\t"
"v_cvt_pk_bf16_f32 v224, v224, v225\n\t"
"v_cvt_pk_bf16_f32 v228, v228, v229\n\t"
"v_pk_fma_f32 v[230:231], v[232:233], s[56:57], v[230:231]\n\t"
"v_pk_fma_f32 v[232:233], v[230:231], s[58:59], v[232:233] neg_lo:[0,0,1] neg_hi:[0,0,1]\n\t"
"v_pk_fma_f32 v[234:235], v[68:69], s[60:61], v[230:231]\n\t"
"v_pk_fma_f32 v[68:69], v[230:231], s[60:61], v[68:69]\n\t"
"v_pk_fma_f32 v[230:231], v[168:169], s[62:63], v[232:233]\n\t"
"v_pk_fma_f32 v[168:169], v[232:233], s[62:63], v[168:169]\n\t"
"v_mfma_f32_16x16x32_bf16 v[36:39], a[220:223], a[172:175], v[36:39]\n\t"
"v_accvgpr_write_b32 a105, v224\n\t"
"v_accvgpr_write_b32 a107, v228\n\t"
"v_mfma_f32_16x16x32_bf16 v[36:39], a[252:255], a[176:179], v[36:39]\n\t"
"v_accvgpr_read_b32 v236, a114\n\t"
"v_accvgpr_read_b32 v238, a118\n\t"
"v_mfma_f32_16x16x32_bf16 v[36:39], a[224:227], a[180:183], v[36:39]\n\t"
"buffer_load_dwordx4 a[248:251], off, s[80:83], s20 offset:2048 sc1\n\t"
"v_accvgpr_read_b32 v237, a115\n\t"
"v_accvgpr_read_b32 v239, a119\n\t"
"v_cvt_pk_bf16_f32 v230, v230, v231\n\t"
"v_cvt_pk_bf16_f32 v234, v234, v235\n\t"
"v_pk_fma_f32 v[236:237], v[238:239], s[56:57], v[236:237]\n\t"
"v_pk_fma_f32 v[238:239], v[236:237], s[58:59], v[238:239] neg_lo:[0,0,1] neg_hi:[0,0,1]\n\t"
"v_pk_fma_f32 v[98:99], v[70:71], s[60:61], v[236:237]\n\t"
"v_pk_fma_f32 v[70:71], v[236:237], s[60:61], v[70:71]\n\t"
"v_pk_fma_f32 v[236:237], v[170:171], s[62:63], v[238:239]\n\t"
"v_pk_fma_f32 v[170:171], v[238:239], s[62:63], v[170:171]\n\t"
"v_mfma_f32_16x16x32_bf16 v[40:43], a[224:227], a[184:187], v[40:43]\n\t"
"s_mov_b32 s20, 614400\n\t"
"v_accvgpr_write_b32 a112, v230\n\t"
"v_accvgpr_write_b32 a114, v234\n\t"
"v_mfma_f32_16x16x32_bf16 v[40:43], a[252:255], a[180:183], v[40:43]\n\t"
"v_accvgpr_read_b32 v224, a120\n\t"
"v_accvgpr_read_b32 v226, a124\n\t"
"v_mfma_f32_16x16x32_bf16 v[40:43], a[220:223], a[176:179], v[40:43]\n\t"
"buffer_load_dwordx4 a[84:87], off, s[80:83], s20 offset:0 sc1\n\t"
"v_accvgpr_read_b32 v225, a121\n\t"
"v_accvgpr_read_b32 v227, a125\n\t"
"v_cvt_pk_bf16_f32 v236, v236, v237\n\t"
"v_cvt_pk_bf16_f32 v98, v98, v99\n\t"
"v_pk_fma_f32 v[224:225], v[226:227], s[56:57], v[224:225]\n\t"
"v_pk_fma_f32 v[226:227], v[224:225], s[58:59], v[226:227] neg_lo:[0,0,1] neg_hi:[0,0,1]\n\t"
"v_pk_fma_f32 v[228:229], v[72:73], s[60:61], v[224:225]\n\t"
"v_pk_fma_f32 v[72:73], v[224:225], s[60:61], v[72:73]\n\t"
"v_pk_fma_f32 v[224:225], v[172:173], s[62:63], v[226:227]\n\t"
"v_pk_fma_f32 v[172:173], v[226:227], s[62:63], v[172:173]\n\t"
"v_mfma_f32_16x16x32_bf16 v[44:47], a[220:223], a[180:183], v[44:47]\n\t"
"v_accvgpr_write_b32 a113, v236\n\t"
"v_accvgpr_write_b32 a115, v98\n\t"
"v_mfma_f32_16x16x32_bf16 v[44:47], a[252:255], a[184:187], v[44:47]\n\t"
"v_accvgpr_read_b32 v230, a122\n\t"
"v_accvgpr_read_b32 v232, a126\n\t"
"v_mfma_f32_16x16x32_bf16 v[44:47], a[224:227], a[188:191], v[44:47]\n\t"
"buffer_load_dwordx4 a[88:91], off, s[80:83], s20 offset:1024 sc1\n\t"
"v_accvgpr_read_b32 v231, a123\n\t"
"v_accvgpr_read_b32 v233, a127\n\t"
"v_accvgpr_read_b32 v236, a128\n\t"
"v_accvgpr_read_b32 v238, a132\n\t"
"v_accvgpr_read_b32 v237, a129\n\t"
"v_accvgpr_read_b32 v239, a133\n\t"
"v_cvt_pk_bf16_f32 v224, v224, v225\n\t"
"v_cvt_pk_bf16_f32 v228, v228, v229\n\t"
"v_pk_fma_f32 v[230:231], v[232:233], s[56:57], v[230:231]\n\t"
"v_pk_fma_f32 v[232:233], v[230:231], s[58:59], v[232:233] neg_lo:[0,0,1] neg_hi:[0,0,1]\n\t"
"v_pk_fma_f32 v[234:235], v[74:75], s[60:61], v[230:231]\n\t"
"v_pk_fma_f32 v[74:75], v[230:231], s[60:61], v[74:75]\n\t"
"v_pk_fma_f32 v[230:231], v[174:175], s[62:63], v[232:233]\n\t"
"v_pk_fma_f32 v[174:175], v[232:233], s[62:63], v[174:175]\n\t"
"v_pk_fma_f32 v[236:237], v[238:239], s[56:57], v[236:237]\n\t"
"v_pk_fma_f32 v[238:239], v[236:237], s[58:59], v[238:239] neg_lo:[0,0,1] neg_hi:[0,0,1]\n\t"
"v_pk_fma_f32 v[98:99], v[76:77], s[60:61], v[236:237]\n\t"
"v_pk_fma_f32 v[76:77], v[236:237], s[60:61], v[76:77]\n\t"
"v_pk_fma_f32 v[236:237], v[176:177], s[62:63], v[238:239]\n\t"
"v_pk_fma_f32 v[176:177], v[238:239], s[62:63], v[176:177]\n\t"
"v_mfma_f32_16x16x32_bf16 v[48:51], a[224:227], a[192:195], v[48:51]\n\t"
"v_accvgpr_write_b32 a120, v224\n\t"
"v_accvgpr_write_b32 a122, v228\n\t"
"v_mfma_f32_16x16x32_bf16 v[48:51], a[252:255], a[188:191], v[48:51]\n\t"
"v_accvgpr_read_b32 v224, a130\n\t"
"v_accvgpr_read_b32 v226, a134\n\t"
"v_mfma_f32_16x16x32_bf16 v[48:51], a[220:223], a[184:187], v[48:51]\n\t"
"buffer_load_dwordx4 a[92:95], off, s[80:83], s20 offset:2048 sc1\n\t"
"v_accvgpr_read_b32 v225, a131\n\t"
"v_accvgpr_read_b32 v227, a135\n\t"
"v_cvt_pk_bf16_f32 v230, v230, v231\n\t"
"v_cvt_pk_bf16_f32 v234, v234, v235\n\t"
"v_cvt_pk_bf16_f32 v236, v236, v237\n\t"
"v_cvt_pk_bf16_f32 v98, v98, v99\n\t"
"v_pk_fma_f32 v[224:225], v[226:227], s[56:57], v[224:225]\n\t"
"v_pk_fma_f32 v[226:227], v[224:225], s[58:59], v[226:227] neg_lo:[0,0,1] neg_hi:[0,0,1]\n\t"
"v_pk_fma_f32 v[228:229], v[78:79], s[60:61], v[224:225]\n\t"
"v_pk_fma_f32 v[78:79], v[224:225], s[60:61], v[78:79]\n\t"
"v_pk_fma_f32 v[224:225], v[178:179], s[62:63], v[226:227]\n\t"
"v_pk_fma_f32 v[178:179], v[226:227], s[62:63], v[178:179]\n\t"
"v_mfma_f32_16x16x32_bf16 v[52:55], a[220:223], a[188:191], v[52:55]\n\t"
"s_mov_b32 s20, 774144\n\t"
"v_accvgpr_write_b32 a121, v230\n\t"
"v_accvgpr_write_b32 a123, v234\n\t"
"v_mfma_f32_16x16x32_bf16 v[52:55], a[252:255], a[192:195], v[52:55]\n\t"
"v_accvgpr_write_b32 a128, v236\n\t"
"v_accvgpr_write_b32 a130, v98\n\t"
"v_mfma_f32_16x16x32_bf16 v[52:55], a[224:227], a[196:199], v[52:55]\n\t"
"ds_write_b128 v245, v[196:199] offset:0\n\t"
"v_accvgpr_read_b32 v230, a136\n\t"
"v_accvgpr_read_b32 v232, a140\n\t"
"v_accvgpr_read_b32 v231, a137\n\t"
"v_accvgpr_read_b32 v233, a141\n\t"
"v_cvt_pk_bf16_f32 v224, v224, v225\n\t"
"v_cvt_pk_bf16_f32 v228, v228, v229\n\t"
"v_pk_fma_f32 v[230:231], v[232:233], s[56:57], v[230:231]\n\t"
"v_pk_fma_f32 v[232:233], v[230:231], s[58:59], v[232:233] neg_lo:[0,0,1] neg_hi:[0,0,1]\n\t"
"v_pk_fma_f32 v[234:235], v[80:81], s[60:61], v[230:231]\n\t"
"v_pk_fma_f32 v[80:81], v[230:231], s[60:61], v[80:81]\n\t"
"v_pk_fma_f32 v[230:231], v[180:181], s[62:63], v[232:233]\n\t"
"v_pk_fma_f32 v[180:181], v[232:233], s[62:63], v[180:181]\n\t"
"v_mfma_f32_16x16x32_bf16 v[56:59], a[224:227], a[200:203], v[56:59]\n\t"
"v_accvgpr_write_b32 a129, v224\n\t"
"v_accvgpr_write_b32 a131, v228\n\t"
"v_mfma_f32_16x16x32_bf16 v[56:59], a[252:255], a[196:199], v[56:59]\n\t"
"v_accvgpr_read_b32 v236, a138\n\t"
"v_accvgpr_read_b32 v238, a142\n\t"
"v_mfma_f32_16x16x32_bf16 v[56:59], a[220:223], a[192:195], v[56:59]\n\t"
"buffer_load_dwordx4 a[96:99], off, s[80:83], s20 offset:0 sc1\n\t"
"v_accvgpr_read_b32 v237, a139\n\t"
"v_accvgpr_read_b32 v239, a143\n\t"
"v_cvt_pk_bf16_f32 v230, v230, v231\n\t"
"v_cvt_pk_bf16_f32 v234, v234, v235\n\t"
"v_pk_fma_f32 v[236:237], v[238:239], s[56:57], v[236:237]\n\t"
"v_pk_fma_f32 v[238:239], v[236:237], s[58:59], v[238:239] neg_lo:[0,0,1] neg_hi:[0,0,1]\n\t"
"v_pk_fma_f32 v[98:99], v[82:83], s[60:61], v[236:237]\n\t"
"v_pk_fma_f32 v[82:83], v[236:237], s[60:61], v[82:83]\n\t"
"v_pk_fma_f32 v[236:237], v[182:183], s[62:63], v[238:239]\n\t"
"v_pk_fma_f32 v[182:183], v[238:239], s[62:63], v[182:183]\n\t"
"v_mfma_f32_16x16x32_bf16 v[60:63], a[220:223], a[196:199], v[60:63]\n\t"
"v_accvgpr_write_b32 a136, v230\n\t"
"v_accvgpr_write_b32 a138, v234\n\t"
"v_mfma_f32_16x16x32_bf16 v[60:63], a[252:255], a[200:203], v[60:63]\n\t"
"v_accvgpr_read_b32 v224, a144\n\t"
"v_accvgpr_read_b32 v226, a148\n\t"
"v_mfma_f32_16x16x32_bf16 v[60:63], a[224:227], a[204:207], v[60:63]\n\t"
"ds_write_b128 v245, v[204:207] offset:18432\n\t"
"v_accvgpr_read_b32 v225, a145\n\t"
"v_accvgpr_read_b32 v227, a149\n\t"
"v_cvt_pk_bf16_f32 v236, v236, v237\n\t"
"v_cvt_pk_bf16_f32 v98, v98, v99\n\t"
"v_pk_fma_f32 v[224:225], v[226:227], s[56:57], v[224:225]\n\t"
"v_pk_fma_f32 v[226:227], v[224:225], s[58:59], v[226:227] neg_lo:[0,0,1] neg_hi:[0,0,1]\n\t"
"v_pk_fma_f32 v[228:229], v[84:85], s[60:61], v[224:225]\n\t"
"v_pk_fma_f32 v[84:85], v[224:225], s[60:61], v[84:85]\n\t"
"v_pk_fma_f32 v[224:225], v[184:185], s[62:63], v[226:227]\n\t"
"v_pk_fma_f32 v[184:185], v[226:227], s[62:63], v[184:185]\n\t"
"v_mfma_f32_16x16x32_bf16 v[64:67], a[228:231], a[168:171], v[64:67]\n\t"
"ds_read_b128 a[168:171], v244 offset:9216\n\t"
"v_accvgpr_write_b32 a137, v236\n\t"
"v_mfma_f32_16x16x32_bf16 v[64:67], a[232:235], a[176:179], v[64:67]\n\t"
"v_accvgpr_write_b32 a139, v98\n\t"
"v_accvgpr_read_b32 v230, a146\n\t"
"v_mfma_f32_16x16x32_bf16 v[64:67], a[208:211], a[172:175], v[64:67]\n\t"
"buffer_load_dwordx4 a[100:103], off, s[80:83], s20 offset:1024 sc1\n\t"
"v_accvgpr_read_b32 v232, a150\n\t"
"v_accvgpr_read_b32 v231, a147\n\t"
"v_accvgpr_read_b32 v233, a151\n\t"
"v_cvt_pk_bf16_f32 v224, v224, v225\n\t"
"v_cvt_pk_bf16_f32 v228, v228, v229\n\t"
"v_pk_fma_f32 v[230:231], v[232:233], s[56:57], v[230:231]\n\t"
"v_pk_fma_f32 v[232:233], v[230:231], s[58:59], v[232:233] neg_lo:[0,0,1] neg_hi:[0,0,1]\n\t"
"v_pk_fma_f32 v[234:235], v[86:87], s[60:61], v[230:231]\n\t"
"v_pk_fma_f32 v[86:87], v[230:231], s[60:61], v[86:87]\n\t"
"v_pk_fma_f32 v[230:231], v[186:187], s[62:63], v[232:233]\n\t"
"v_pk_fma_f32 v[186:187], v[232:233], s[62:63], v[186:187]\n\t"
"v_mfma_f32_16x16x32_bf16 v[68:71], a[228:231], a[172:175], v[68:71]\n\t"
"ds_read_b128 a[172:175], v244 offset:9728\n\t"
"v_accvgpr_write_b32 a144, v224\n\t"
"v_mfma_f32_16x16x32_bf16 v[68:71], a[208:211], a[176:179], v[68:71]\n\t"
"v_accvgpr_write_b32 a146, v228\n\t"
"v_accvgpr_read_b32 v236, a152\n\t"
"v_mfma_f32_16x16x32_bf16 v[68:71], a[232:235], a[180:183], v[68:71]\n\t"
"ds_write_b64 v246, a[104:105] offset:0\n\t"
"v_accvgpr_read_b32 v238, a156\n\t"
"v_accvgpr_read_b32 v237, a153\n\t"
"v_accvgpr_read_b32 v239, a157\n\t"
"v_cvt_pk_bf16_f32 v230, v230, v231\n\t"
"v_cvt_pk_bf16_f32 v234, v234, v235\n\t"
"v_pk_fma_f32 v[236:237], v[238:239], s[56:57], v[236:237]\n\t"
"v_pk_fma_f32 v[238:239], v[236:237], s[58:59], v[238:239] neg_lo:[0,0,1] neg_hi:[0,0,1]\n\t"
"v_pk_fma_f32 v[98:99], v[88:89], s[60:61], v[236:237]\n\t"
"v_pk_fma_f32 v[88:89], v[236:237], s[60:61], v[88:89]\n\t"
"v_pk_fma_f32 v[236:237], v[188:189], s[62:63], v[238:239]\n\t"
"v_pk_fma_f32 v[188:189], v[238:239], s[62:63], v[188:189]\n\t"
"v_mfma_f32_16x16x32_bf16 v[72:75], a[232:235], a[184:187], v[72:75]\n\t"
"v_accvgpr_write_b32 a145, v230\n\t"
"v_accvgpr_write_b32 a147, v234\n\t"
"v_mfma_f32_16x16x32_bf16 v[72:75], a[208:211], a[180:183], v[72:75]\n\t"
"v_accvgpr_read_b32 v224, a154\n\t"
"v_accvgpr_read_b32 v226, a158\n\t"
"v_mfma_f32_16x16x32_bf16 v[72:75], a[228:231], a[176:179], v[72:75]\n\t"
"buffer_load_dwordx4 a[220:223], off, s[80:83], s20 offset:2048 sc1\n\t"
"ds_read_b128 a[176:179], v244 offset:10240\n\t"
"v_accvgpr_read_b32 v225, a155\n\t"
"v_accvgpr_read_b32 v227, a159\n\t"
"v_cvt_pk_bf16_f32 v236, v236, v237\n\t"
"v_cvt_pk_bf16_f32 v98, v98, v99\n\t"
"s_mov_b32 s20, 786432\n\t"
"v_pk_fma_f32 v[224:225], v[226:227], s[56:57], v[224:225]\n\t"
"v_pk_fma_f32 v[226:227], v[224:225], s[58:59], v[226:227] neg_lo:[0,0,1] neg_hi:[0,0,1]\n\t"
"v_pk_fma_f32 v[228:229], v[90:91], s[60:61], v[224:225]\n\t"
"v_pk_fma_f32 v[90:91], v[224:225], s[60:61], v[90:91]\n\t"
"v_pk_fma_f32 v[224:225], v[190:191], s[62:63], v[226:227]\n\t"
"v_pk_fma_f32 v[190:191], v[226:227], s[62:63], v[190:191]\n\t"
"v_mfma_f32_16x16x32_bf16 v[76:79], a[228:231], a[180:183], v[76:79]\n\t"
"ds_read_b128 a[180:183], v244 offset:10752\n\t"
"v_accvgpr_write_b32 a152, v236\n\t"
"v_mfma_f32_16x16x32_bf16 v[76:79], a[208:211], a[184:187], v[76:79]\n\t"
"v_accvgpr_write_b32 a154, v98\n\t"
"v_accvgpr_read_b32 v230, a160\n\t"
"v_mfma_f32_16x16x32_bf16 v[76:79], a[232:235], a[188:191], v[76:79]\n\t"
"ds_write_b64 v246, a[112:113] offset:18432\n\t"
"v_accvgpr_read_b32 v232, a164\n\t"
"v_accvgpr_read_b32 v231, a161\n\t"
"v_accvgpr_read_b32 v233, a165\n\t"
"v_cvt_pk_bf16_f32 v224, v224, v225\n\t"
"v_cvt_pk_bf16_f32 v228, v228, v229\n\t"
"v_pk_fma_f32 v[230:231], v[232:233], s[56:57], v[230:231]\n\t"
"v_pk_fma_f32 v[232:233], v[230:231], s[58:59], v[232:233] neg_lo:[0,0,1] neg_hi:[0,0,1]\n\t"
"v_pk_fma_f32 v[234:235], v[92:93], s[60:61], v[230:231]\n\t"
"v_pk_fma_f32 v[92:93], v[230:231], s[60:61], v[92:93]\n\t"
"v_pk_fma_f32 v[230:231], v[192:193], s[62:63], v[232:233]\n\t"
"v_pk_fma_f32 v[192:193], v[232:233], s[62:63], v[192:193]\n\t"
"v_mfma_f32_16x16x32_bf16 v[80:83], a[232:235], a[192:195], v[80:83]\n\t"
"v_accvgpr_write_b32 a153, v224\n\t"
"v_accvgpr_write_b32 a155, v228\n\t"
"v_mfma_f32_16x16x32_bf16 v[80:83], a[208:211], a[188:191], v[80:83]\n\t"
"v_accvgpr_read_b32 v236, a162\n\t"
"v_accvgpr_read_b32 v238, a166\n\t"
"v_mfma_f32_16x16x32_bf16 v[80:83], a[228:231], a[184:187], v[80:83]\n\t"
"buffer_load_dwordx4 a[224:227], off, s[80:83], s20 offset:0 sc1\n\t"
"ds_read_b128 a[184:187], v244 offset:11264\n\t"
"v_accvgpr_read_b32 v237, a163\n\t"
"v_accvgpr_read_b32 v239, a167\n\t"
"v_cvt_pk_bf16_f32 v230, v230, v231\n\t"
"v_cvt_pk_bf16_f32 v234, v234, v235\n\t"
"v_pk_fma_f32 v[236:237], v[238:239], s[56:57], v[236:237]\n\t"
"v_pk_fma_f32 v[238:239], v[236:237], s[58:59], v[238:239] neg_lo:[0,0,1] neg_hi:[0,0,1]\n\t"
"v_pk_fma_f32 v[98:99], v[94:95], s[60:61], v[236:237]\n\t"
"v_pk_fma_f32 v[94:95], v[236:237], s[60:61], v[94:95]\n\t"
"v_pk_fma_f32 v[236:237], v[194:195], s[62:63], v[238:239]\n\t"
"v_pk_fma_f32 v[194:195], v[238:239], s[62:63], v[194:195]\n\t"
"v_mfma_f32_16x16x32_bf16 v[84:87], a[228:231], a[188:191], v[84:87]\n\t"
"ds_read_b128 a[188:191], v244 offset:11776\n\t"
"v_accvgpr_write_b32 a160, v230\n\t"
"v_mfma_f32_16x16x32_bf16 v[84:87], a[208:211], a[192:195], v[84:87]\n\t"
"v_accvgpr_write_b32 a162, v234\n\t"
"v_cvt_pk_bf16_f32 v236, v236, v237\n\t"
"v_mfma_f32_16x16x32_bf16 v[84:87], a[232:235], a[196:199], v[84:87]\n\t"
"buffer_load_dwordx4 a[252:255], off, s[80:83], s20 offset:1024 sc1\n\t"
"v_cvt_pk_bf16_f32 v98, v98, v99\n\t"
"v_mfma_f32_16x16x32_bf16 v[88:91], a[232:235], a[200:203], v[88:91]\n\t"
"v_accvgpr_write_b32 a161, v236\n\t"
"v_accvgpr_write_b32 a163, v98\n\t"
"v_mfma_f32_16x16x32_bf16 v[88:91], a[208:211], a[196:199], v[88:91]\n\t"
"v_mfma_f32_16x16x32_bf16 v[88:91], a[228:231], a[192:195], v[88:91]\n\t"
"buffer_load_dwordx4 a[108:111], off, s[80:83], s20 offset:2048 sc1\n\t"
"ds_write_b128 v245, v[200:203] offset:9216\n\t"
"ds_write_b128 v245, v[208:211] offset:27648\n\t"
"s_mov_b32 s20, 798720\n\t"
"v_mfma_f32_16x16x32_bf16 v[92:95], a[228:231], a[196:199], v[92:95]\n\t"
"buffer_load_dwordx4 a[116:119], off, s[80:83], s20 offset:0 sc1\n\t"
"v_mfma_f32_16x16x32_bf16 v[92:95], a[208:211], a[200:203], v[92:95]\n\t"
"buffer_load_dwordx4 a[124:127], off, s[80:83], s20 offset:1024 sc1\n\t"
"v_mfma_f32_16x16x32_bf16 v[92:95], a[232:235], a[204:207], v[92:95]\n\t"
"s_waitcnt vmcnt(23) lgkmcnt(6)\n\t"
"buffer_load_dwordx4 a[132:135], off, s[80:83], s20 offset:2048 sc1\n\t"
"s_mov_b32 s20, 1179648\n\t"
"v_mfma_f32_16x16x32_bf16 v[100:103], v[240:243], a[168:171], v[100:103]\n\t"
"s_waitcnt lgkmcnt(5)\n\t"
"ds_read_b128 a[192:195], v244 offset:12288\n\t"
"v_mfma_f32_16x16x32_bf16 v[100:103], a[56:59], a[176:179], v[100:103]\n\t"
"buffer_load_dwordx4 a[140:143], off, s[80:83], s20 offset:0 sc1\n\t"
"v_mfma_f32_16x16x32_bf16 v[100:103], a[52:55], a[172:175], v[100:103]\n\t"
"ds_write_b64 v246, a[106:107] offset:9216\n\t"
"ds_write_b64 v246, a[114:115] offset:27648\n\t"
"v_mfma_f32_16x16x32_bf16 v[104:107], v[240:243], a[172:175], v[104:107]\n\t"
"s_waitcnt lgkmcnt(6)\n\t"
"ds_read_b128 a[196:199], v244 offset:12800\n\t"
"v_mfma_f32_16x16x32_bf16 v[104:107], a[52:55], a[176:179], v[104:107]\n\t"
"ds_read_b128 a[200:203], v244 offset:13312\n\t"
"v_mfma_f32_16x16x32_bf16 v[104:107], a[56:59], a[180:183], v[104:107]\n\t"
"ds_read_b128 a[204:207], v244 offset:13824\n\t"
"s_waitcnt lgkmcnt(0)\n\t"
"s_barrier\n\t"
"v_mfma_f32_16x16x32_bf16 v[108:111], a[56:59], a[184:187], v[108:111]\n\t"
"ds_read_b128 a[40:43], v255 offset:0\n\t"
"v_mfma_f32_16x16x32_bf16 v[108:111], a[52:55], a[180:183], v[108:111]\n\t"
"buffer_load_dwordx4 a[208:211], off, s[80:83], s20 offset:1024 sc1\n\t"
"v_mfma_f32_16x16x32_bf16 v[108:111], v[240:243], a[176:179], v[108:111]\n\t"
"buffer_load_dwordx4 a[228:231], off, s[80:83], s20 offset:2048 sc1\n\t"
"s_mov_b32 s20, 1474560\n\t"
"v_mfma_f32_16x16x32_bf16 v[112:115], v[240:243], a[180:183], v[112:115]\n\t"
"buffer_load_dwordx4 a[232:235], off, s[80:83], s20 offset:0 sc1\n\t"
"v_mfma_f32_16x16x32_bf16 v[112:115], a[52:55], a[184:187], v[112:115]\n\t"
"s_add_u32 s29, s22, 384\n\t"
"ds_read_b128 a[44:47], v255 offset:1536\n\t"
"v_mfma_f32_16x16x32_bf16 v[112:115], a[56:59], a[188:191], v[112:115]\n\t"
"buffer_load_dwordx4 a[104:107], off, s[80:83], s20 offset:1024 sc1\n\t"
"v_mfma_f32_16x16x32_bf16 v[116:119], a[56:59], a[192:195], v[116:119]\n\t"
"s_waitcnt lgkmcnt(1)\n\t"
"ds_read_b128 a[48:51], v255 offset:3072\n\t"
"v_mfma_f32_16x16x32_bf16 v[116:119], a[52:55], a[188:191], v[116:119]\n\t"
"buffer_load_dwordx4 a[112:115], off, s[80:83], s20 offset:2048 sc1\n\t"
"v_mfma_f32_16x16x32_bf16 v[116:119], v[240:243], a[184:187], v[116:119]\n\t"
"s_mov_b32 s20, 1486848\n\t"
"ds_read_b128 v[196:199], v255 offset:18432\n\t"
"v_mfma_f32_16x16x32_bf16 v[120:123], v[240:243], a[188:191], v[120:123]\n\t"
"buffer_load_dwordx4 a[148:151], off, s[80:83], s20 offset:0 sc1\n\t"
"v_mfma_f32_16x16x32_bf16 v[120:123], a[52:55], a[192:195], v[120:123]\n\t"
"buffer_load_dwordx4 a[156:159], off, s[80:83], s20 offset:1024 sc1\n\t"
"v_mfma_f32_16x16x32_bf16 v[120:123], a[56:59], a[196:199], v[120:123]\n\t"
"s_waitcnt lgkmcnt(2)\n\t"
"ds_write_b128 v245, v[212:215] offset:36864\n\t"
"v_mfma_f32_16x16x32_bf16 v[124:127], a[56:59], a[200:203], v[124:127]\n\t"
"s_waitcnt lgkmcnt(2)\n\t"
"v_mfma_f32_16x16x32_bf16 v[124:127], a[52:55], a[196:199], v[124:127]\n\t"
"buffer_load_dwordx4 a[164:167], off, s[80:83], s20 offset:2048 sc1\n\t"
"v_mfma_f32_16x16x32_bf16 v[124:127], v[240:243], a[192:195], v[124:127]\n\t"
"s_mov_b32 s20, 1191936\n\t"
"ds_write_b128 v249, a[0:3] offset:0\n\t"
"v_mfma_f32_16x16x32_bf16 v[128:131], v[240:243], a[196:199], v[128:131]\n\t"
"s_waitcnt vmcnt(30)\n\t"
"v_mfma_f32_16x16x32_bf16 v[128:131], a[52:55], a[200:203], v[128:131]\n\t"
"buffer_load_dwordx4 a[52:55], off, s[80:83], s20 offset:0 sc1\n\t"
"v_mfma_f32_16x16x32_bf16 v[128:131], a[56:59], a[204:207], v[128:131]\n\t"
"ds_write_b128 v249, a[8:11] offset:18432\n\t"
"v_mfma_f32_16x16x32_bf16 v[132:135], a[60:63], a[168:171], v[132:135]\n\t"
"s_waitcnt lgkmcnt(3)\n\t"
"v_mfma_f32_16x16x32_bf16 v[132:135], a[212:215], a[176:179], v[132:135]\n\t"
"ds_read_b128 v[200:203], v255 offset:19968\n\t"
"v_mfma_f32_16x16x32_bf16 v[132:135], a[236:239], a[172:175], v[132:135]\n\t"
"ds_write_b128 v249, a[16:19] offset:36864\n\t"
"buffer_store_dwordx4 a[40:43], v248, s[16:19], s29 offen offset:0 nt\n\t"
"v_mfma_f32_16x16x32_bf16 v[136:139], a[60:63], a[172:175], v[136:139]\n\t"
"ds_read_b128 v[204:207], v255 offset:21504\n\t"
"v_mfma_f32_16x16x32_bf16 v[136:139], a[236:239], a[176:179], v[136:139]\n\t"
"buffer_load_dwordx4 a[40:43], off, s[80:83], s20 offset:1024 sc1\n\t"
"v_mfma_f32_16x16x32_bf16 v[136:139], a[212:215], a[180:183], v[136:139]\n\t"
"buffer_store_dwordx4 a[44:47], v248, s[16:19], s29 offen offset:128 nt\n\t"
"ds_write_b64 v246, a[120:121] offset:36864\n\t"
"v_mfma_f32_16x16x32_bf16 v[140:143], a[212:215], a[184:187], v[140:143]\n\t"
"v_mfma_f32_16x16x32_bf16 v[140:143], a[236:239], a[180:183], v[140:143]\n\t"
"buffer_load_dwordx4 a[44:47], off, s[80:83], s20 offset:2048 sc1\n\t"
"v_mfma_f32_16x16x32_bf16 v[140:143], a[60:63], a[176:179], v[140:143]\n\t"
"buffer_store_dwordx4 a[48:51], v248, s[16:19], s29 offen offset:256 nt\n\t"
"ds_write_b64 v250, a[128:129] offset:0\n\t"
"s_add_u32 s29, s22, 196992\n\t"
"s_mov_b32 s20, 1204224\n\t"
"v_mfma_f32_16x16x32_bf16 v[144:147], a[60:63], a[180:183], v[144:147]\n\t"
"s_waitcnt lgkmcnt(4)\n\t"
"v_mfma_f32_16x16x32_bf16 v[144:147], a[236:239], a[184:187], v[144:147]\n\t"
"buffer_load_dwordx4 a[48:51], off, s[80:83], s20 offset:0 sc1\n\t"
"v_mfma_f32_16x16x32_bf16 v[144:147], a[212:215], a[188:191], v[144:147]\n\t"
"ds_write_b64 v250, a[136:137] offset:18432\n\t"
"ds_write_b64 v250, a[144:145] offset:36864\n\t"
"v_mfma_f32_16x16x32_bf16 v[148:151], a[212:215], a[192:195], v[148:151]\n\t"
"buffer_load_dwordx4 a[56:59], off, s[80:83], s20 offset:1024 sc1\n\t"
"v_mfma_f32_16x16x32_bf16 v[148:151], a[236:239], a[188:191], v[148:151]\n\t"
"v_mfma_f32_16x16x32_bf16 v[148:151], a[60:63], a[184:187], v[148:151]\n\t"
"buffer_store_dwordx4 v[196:199], v248, s[16:19], s29 offen offset:0 nt\n\t"
"ds_write_b64 v246, a[122:123] offset:46080\n\t"
"v_mfma_f32_16x16x32_bf16 v[152:155], a[60:63], a[188:191], v[152:155]\n\t"
"v_mfma_f32_16x16x32_bf16 v[152:155], a[236:239], a[192:195], v[152:155]\n\t"
"v_mfma_f32_16x16x32_bf16 v[152:155], a[212:215], a[196:199], v[152:155]\n\t"
"buffer_store_dwordx4 v[200:203], v248, s[16:19], s29 offen offset:128 nt\n\t"
"ds_write_b64 v250, a[130:131] offset:9216\n\t"
"v_mfma_f32_16x16x32_bf16 v[156:159], a[212:215], a[200:203], v[156:159]\n\t"
"s_waitcnt lgkmcnt(6)\n\t"
"v_mfma_f32_16x16x32_bf16 v[156:159], a[236:239], a[196:199], v[156:159]\n\t"
"buffer_load_dwordx4 v[240:243], off, s[80:83], s20 offset:2048 sc1\n\t"
"v_mfma_f32_16x16x32_bf16 v[156:159], a[60:63], a[192:195], v[156:159]\n\t"
"s_mov_b32 s20, 1499136\n\t"
"ds_write_b128 v245, v[216:219] offset:46080\n\t"
"v_mfma_f32_16x16x32_bf16 v[160:163], a[60:63], a[196:199], v[160:163]\n\t"
"s_waitcnt vmcnt(38)\n\t"
"v_mfma_f32_16x16x32_bf16 v[160:163], a[236:239], a[200:203], v[160:163]\n\t"
"v_mfma_f32_16x16x32_bf16 v[160:163], a[212:215], a[204:207], v[160:163]\n\t"
"ds_write_b128 v249, a[4:7] offset:9216\n\t"
"ds_write_b128 v249, a[12:15] offset:27648\n\t"
"ds_write_b128 v249, a[20:23] offset:46080\n\t"
"v_mfma_f32_16x16x32_bf16 v[164:167], a[240:243], a[168:171], v[164:167]\n\t"
"v_mfma_f32_16x16x32_bf16 v[164:167], a[68:71], a[176:179], v[164:167]\n\t"
"ds_read_b128 a[168:171], v244 offset:24576\n\t"
"v_mfma_f32_16x16x32_bf16 v[164:167], a[64:67], a[172:175], v[164:167]\n\t"
"buffer_store_dwordx4 v[204:207], v248, s[16:19], s29 offen offset:256 nt\n\t"
"ds_write_b64 v250, a[138:139] offset:27648\n\t"
"v_mfma_f32_16x16x32_bf16 v[168:171], a[240:243], a[172:175], v[168:171]\n\t"
"ds_read_b128 a[172:175], v244 offset:25088\n\t"
"v_mfma_f32_16x16x32_bf16 v[168:171], a[64:67], a[176:179], v[168:171]\n\t"
"buffer_load_dwordx4 a[212:215], off, s[80:83], s20 offset:0 sc1\n\t"
"v_mfma_f32_16x16x32_bf16 v[168:171], a[68:71], a[180:183], v[168:171]\n\t"
"ds_write_b64 v250, a[146:147] offset:46080\n\t"
"v_mfma_f32_16x16x32_bf16 v[172:175], a[68:71], a[184:187], v[172:175]\n\t"
"buffer_load_dwordx4 a[236:239], off, s[80:83], s20 offset:1024 sc1\n\t"
"v_mfma_f32_16x16x32_bf16 v[172:175], a[64:67], a[180:183], v[172:175]\n\t"
"v_mfma_f32_16x16x32_bf16 v[172:175], a[240:243], a[176:179], v[172:175]\n\t"
"ds_read_b128 a[176:179], v244 offset:25600\n\t"
"v_mfma_f32_16x16x32_bf16 v[176:179], a[240:243], a[180:183], v[176:179]\n\t"
"ds_read_b128 a[180:183], v244 offset:26112\n\t"
"v_mfma_f32_16x16x32_bf16 v[176:179], a[64:67], a[184:187], v[176:179]\n\t"
"v_mfma_f32_16x16x32_bf16 v[176:179], a[68:71], a[188:191], v[176:179]\n\t"
"s_waitcnt lgkmcnt(2)\n\t"
"s_barrier\n\t"
"v_mfma_f32_16x16x32_bf16 v[180:183], a[68:71], a[192:195], v[180:183]\n\t"
"ds_read_b128 v[196:199], v255 offset:36864\n\t"
"v_mfma_f32_16x16x32_bf16 v[180:183], a[64:67], a[188:191], v[180:183]\n\t"
"buffer_load_dwordx4 a[60:63], off, s[80:83], s20 offset:2048 sc1\n\t"
"v_mfma_f32_16x16x32_bf16 v[180:183], a[240:243], a[184:187], v[180:183]\n\t"
"s_mov_b32 s20, 1363968\n\t"
"ds_read_b128 v[200:203], v255 offset:38400\n\t"
"v_mfma_f32_16x16x32_bf16 v[184:187], a[240:243], a[188:191], v[184:187]\n\t"
"s_waitcnt vmcnt(39) lgkmcnt(3)\n\t"
"ds_read_b128 a[184:187], v244 offset:26624\n\t"
"s_add_u32 s29, s22, 393600\n\t"
"v_mfma_f32_16x16x32_bf16 v[184:187], a[64:67], a[192:195], v[184:187]\n\t"
"Lpg24_%=:\n\t"
"s_cbranch_vccnz Lvr3_%=\n\t"
"ds_read_b128 v[204:207], v255 offset:39936\n\t"
"v_mfma_f32_16x16x32_bf16 v[184:187], a[68:71], a[196:199], v[184:187]\n\t"
"buffer_load_dwordx4 a[16:19], off, s[80:83], s20 offset:0 sc1\n\t"
"v_mfma_f32_16x16x32_bf16 v[188:191], a[68:71], a[200:203], v[188:191]\n\t"
"s_waitcnt lgkmcnt(3)\n\t"
"ds_read_b128 a[188:191], v244 offset:27136\n\t"
"v_mfma_f32_16x16x32_bf16 v[188:191], a[64:67], a[196:199], v[188:191]\n\t"
"buffer_load_dwordx4 a[20:23], off, s[80:83], s20 offset:1024 sc1\n\t"
"v_mfma_f32_16x16x32_bf16 v[188:191], a[240:243], a[192:195], v[188:191]\n\t"
"ds_read_b128 a[192:195], v244 offset:27648\n\t"
"v_mfma_f32_16x16x32_bf16 v[192:195], a[240:243], a[196:199], v[192:195]\n\t"
"buffer_load_dwordx4 a[120:123], off, s[80:83], s20 offset:2048 sc1\n\t"
"v_mfma_f32_16x16x32_bf16 v[192:195], a[64:67], a[200:203], v[192:195]\n\t"
"s_waitcnt lgkmcnt(4)\n\t"
"ds_read_b128 v[208:211], v247 offset:0\n\t"
"v_mfma_f32_16x16x32_bf16 v[192:195], a[68:71], a[204:207], v[192:195]\n\t"
"ds_write_b128 v245, a[24:27] offset:0\n\t"
"Lst33_%=:\n\t"
"v_mfma_f32_16x16x32_bf16 v[0:3], a[72:75], a[168:171], v[0:3]\n\t"
"s_waitcnt lgkmcnt(4)\n\t"
"v_mfma_f32_16x16x32_bf16 v[0:3], a[80:83], a[176:179], v[0:3]\n\t"
"ds_read_b128 a[196:199], v244 offset:28160\n\t"
"v_mfma_f32_16x16x32_bf16 v[0:3], a[76:79], a[172:175], v[0:3]\n\t"
"ds_write_b128 v245, a[32:35] offset:18432\n\t"
"v_mfma_f32_16x16x32_bf16 v[4:7], a[72:75], a[172:175], v[4:7]\n\t"
"v_mfma_f32_16x16x32_bf16 v[4:7], a[76:79], a[176:179], v[4:7]\n\t"
"ds_read_b128 a[200:203], v244 offset:28672\n\t"
"v_mfma_f32_16x16x32_bf16 v[4:7], a[80:83], a[180:183], v[4:7]\n\t"
"buffer_store_dwordx4 v[196:199], v248, s[16:19], s29 offen offset:0 nt\n\t"
"ds_read_b128 a[204:207], v244 offset:29184\n\t"
"ds_read_b128 v[212:215], v247 offset:1536\n\t"
"v_mfma_f32_16x16x32_bf16 v[8:11], a[80:83], a[184:187], v[8:11]\n\t"
"s_waitcnt lgkmcnt(8)\n\t"
"s_waitcnt lgkmcnt(6)\n\t"
"v_mfma_f32_16x16x32_bf16 v[8:11], a[76:79], a[180:183], v[8:11]\n\t"
"v_mfma_f32_16x16x32_bf16 v[8:11], a[72:75], a[176:179], v[8:11]\n\t"
"buffer_store_dwordx4 v[200:203], v248, s[16:19], s29 offen offset:128 nt\n\t"
"ds_write_b64 v246, a[152:153] offset:0\n\t"
"s_mov_b32 s20, 1658880\n\t"
"v_mfma_f32_16x16x32_bf16 v[12:15], a[72:75], a[180:183], v[12:15]\n\t"
"ds_read_b128 v[196:199], v247 offset:3072\n\t"
"v_mfma_f32_16x16x32_bf16 v[12:15], a[76:79], a[184:187], v[12:15]\n\t"
"v_mfma_f32_16x16x32_bf16 v[12:15], a[80:83], a[188:191], v[12:15]\n\t"
"ds_write_b64 v246, a[160:161] offset:18432\n\t"
"v_mfma_f32_16x16x32_bf16 v[16:19], a[80:83], a[192:195], v[16:19]\n\t"
"s_waitcnt lgkmcnt(7)\n\t"
"v_mfma_f32_16x16x32_bf16 v[16:19], a[76:79], a[188:191], v[16:19]\n\t"
"v_mfma_f32_16x16x32_bf16 v[16:19], a[72:75], a[184:187], v[16:19]\n\t"
"buffer_store_dwordx4 v[204:207], v248, s[16:19], s29 offen offset:256 nt\n\t"
"buffer_load_dwordx4 a[240:243], off, s[80:83], s20 offset:0 sc1\n\t"
"ds_read_b128 v[200:203], v247 offset:18432\n\t"
"s_add_u32 s29, s22, 590208\n\t"
"v_mfma_f32_16x16x32_bf16 v[20:23], a[72:75], a[188:191], v[20:23]\n\t"
"s_waitcnt lgkmcnt(6)\n\t"
"ds_read_b128 v[216:219], v247 offset:36864\n\t"
"s_waitcnt lgkmcnt(5)\n\t"
"v_mfma_f32_16x16x32_bf16 v[20:23], a[76:79], a[192:195], v[20:23]\n\t"
"ds_read_b128 v[204:207], v247 offset:19968\n\t"
"v_mfma_f32_16x16x32_bf16 v[20:23], a[80:83], a[196:199], v[20:23]\n\t"
"buffer_store_dwordx4 v[208:211], v248, s[16:19], s29 offen offset:0 nt\n\t"
"ds_read_b128 a[0:3], v247 offset:38400\n\t"
"v_mfma_f32_16x16x32_bf16 v[24:27], a[80:83], a[200:203], v[24:27]\n\t"
"ds_read_b128 v[208:211], v247 offset:21504\n\t"
"v_mfma_f32_16x16x32_bf16 v[24:27], a[76:79], a[196:199], v[24:27]\n\t"
"ds_read_b128 a[4:7], v247 offset:39936\n\t"
"v_mfma_f32_16x16x32_bf16 v[24:27], a[72:75], a[192:195], v[24:27]\n\t"
"ds_write_b128 v245, a[28:31] offset:9216\n\t"
"v_mfma_f32_16x16x32_bf16 v[28:31], a[72:75], a[196:199], v[28:31]\n\t"
"buffer_load_dwordx4 a[64:67], off, s[80:83], s20 offset:1024 sc1\n\t"
"v_mfma_f32_16x16x32_bf16 v[28:31], a[76:79], a[200:203], v[28:31]\n\t"
"s_waitcnt vmcnt(45)\n\t"
"v_mfma_f32_16x16x32_bf16 v[28:31], a[80:83], a[204:207], v[28:31]\n\t"
"ds_write_b128 v245, a[36:39] offset:27648\n\t"
"v_mfma_f32_16x16x32_bf16 v[32:35], a[244:247], a[168:171], v[32:35]\n\t"
"s_waitcnt lgkmcnt(9)\n\t"
"v_mfma_f32_16x16x32_bf16 v[32:35], a[248:251], a[176:179], v[32:35]\n\t"
"buffer_load_dwordx4 a[68:71], off, s[80:83], s20 offset:2048 sc1\n\t"
"v_mfma_f32_16x16x32_bf16 v[32:35], a[216:219], a[172:175], v[32:35]\n\t"
"buffer_store_dwordx4 v[212:215], v248, s[16:19], s29 offen offset:128 nt\n\t"
"ds_write_b64 v246, a[154:155] offset:9216\n\t"
"v_mfma_f32_16x16x32_bf16 v[36:39], a[244:247], a[172:175], v[36:39]\n\t"
"s_mov_b32 s20, 1671168\n\t"
"v_mfma_f32_16x16x32_bf16 v[36:39], a[216:219], a[176:179], v[36:39]\n\t"
"v_mfma_f32_16x16x32_bf16 v[36:39], a[248:251], a[180:183], v[36:39]\n\t"
"ds_write_b64 v246, a[162:163] offset:27648\n\t"
"v_mfma_f32_16x16x32_bf16 v[40:43], a[248:251], a[184:187], v[40:43]\n\t"
"v_mfma_f32_16x16x32_bf16 v[40:43], a[216:219], a[180:183], v[40:43]\n\t"
"v_mfma_f32_16x16x32_bf16 v[40:43], a[244:247], a[176:179], v[40:43]\n\t"
"buffer_store_dwordx4 v[196:199], v248, s[16:19], s29 offen offset:256 nt\n\t"
"s_add_u32 s29, s22, 786816\n\t"
"v_mfma_f32_16x16x32_bf16 v[44:47], a[244:247], a[180:183], v[44:47]\n\t"
"s_waitcnt lgkmcnt(9)\n\t"
"v_mfma_f32_16x16x32_bf16 v[44:47], a[216:219], a[184:187], v[44:47]\n\t"
"v_mfma_f32_16x16x32_bf16 v[44:47], a[248:251], a[188:191], v[44:47]\n\t"
"s_waitcnt lgkmcnt(0)\n\t"
"s_barrier\n\t"
"v_mfma_f32_16x16x32_bf16 v[48:51], a[248:251], a[192:195], v[48:51]\n\t"
"v_mfma_f32_16x16x32_bf16 v[48:51], a[216:219], a[188:191], v[48:51]\n\t"
"buffer_load_dwordx4 a[72:75], off, s[80:83], s20 offset:0 sc1\n\t"
"v_mfma_f32_16x16x32_bf16 v[48:51], a[244:247], a[184:187], v[48:51]\n\t"
"buffer_load_dwordx4 a[24:27], off, s[80:83], s20 offset:1024 sc1\n\t"
"v_mfma_f32_16x16x32_bf16 v[52:55], a[244:247], a[188:191], v[52:55]\n\t"
"v_mfma_f32_16x16x32_bf16 v[52:55], a[216:219], a[192:195], v[52:55]\n\t"
"v_mfma_f32_16x16x32_bf16 v[52:55], a[248:251], a[196:199], v[52:55]\n\t"
"buffer_store_dwordx4 v[200:203], v248, s[16:19], s29 offen offset:0 nt\n\t"
"v_mfma_f32_16x16x32_bf16 v[56:59], a[248:251], a[200:203], v[56:59]\n\t"
"v_mfma_f32_16x16x32_bf16 v[56:59], a[216:219], a[196:199], v[56:59]\n\t"
"buffer_load_dwordx4 a[28:31], off, s[80:83], s20 offset:2048 sc1\n\t"
"v_mfma_f32_16x16x32_bf16 v[56:59], a[244:247], a[192:195], v[56:59]\n\t"
"s_mov_b32 s20, 1376256\n\t"
"v_mfma_f32_16x16x32_bf16 v[60:63], a[244:247], a[196:199], v[60:63]\n\t"
"s_waitcnt vmcnt(49)\n\t"
"v_mfma_f32_16x16x32_bf16 v[60:63], a[216:219], a[200:203], v[60:63]\n\t"
"v_mfma_f32_16x16x32_bf16 v[60:63], a[248:251], a[204:207], v[60:63]\n\t"
"buffer_store_dwordx4 v[204:207], v248, s[16:19], s29 offen offset:128 nt\n\t"
"v_mfma_f32_16x16x32_bf16 v[64:67], a[84:87], a[168:171], v[64:67]\n\t"
"ds_read_b128 a[168:171], v244 offset:33792\n\t"
"v_mfma_f32_16x16x32_bf16 v[64:67], a[92:95], a[176:179], v[64:67]\n\t"
"v_mfma_f32_16x16x32_bf16 v[64:67], a[88:91], a[172:175], v[64:67]\n\t"
"buffer_load_dwordx4 a[216:219], off, s[80:83], s20 offset:0 sc1\n\t"
"v_mfma_f32_16x16x32_bf16 v[68:71], a[84:87], a[172:175], v[68:71]\n\t"
"ds_read_b128 a[172:175], v244 offset:34304\n\t"
"v_mfma_f32_16x16x32_bf16 v[68:71], a[88:91], a[176:179], v[68:71]\n\t"
"v_mfma_f32_16x16x32_bf16 v[68:71], a[92:95], a[180:183], v[68:71]\n\t"
"v_mfma_f32_16x16x32_bf16 v[72:75], a[92:95], a[184:187], v[72:75]\n\t"
"v_mfma_f32_16x16x32_bf16 v[72:75], a[88:91], a[180:183], v[72:75]\n\t"
"v_mfma_f32_16x16x32_bf16 v[72:75], a[84:87], a[176:179], v[72:75]\n\t"
"ds_read_b128 a[176:179], v244 offset:34816\n\t"
"v_mfma_f32_16x16x32_bf16 v[76:79], a[84:87], a[180:183], v[76:79]\n\t"
"buffer_load_dwordx4 a[244:247], off, s[80:83], s20 offset:1024 sc1\n\t"
"v_mfma_f32_16x16x32_bf16 v[76:79], a[88:91], a[184:187], v[76:79]\n\t"
"ds_read_b128 a[180:183], v244 offset:35328\n\t"
"v_mfma_f32_16x16x32_bf16 v[76:79], a[92:95], a[188:191], v[76:79]\n\t"
"buffer_store_dwordx4 v[208:211], v248, s[16:19], s29 offen offset:256 nt\n\t"
"ds_read_b128 v[196:199], v255 offset:0\n\t"
"ds_read_b128 v[212:215], v255 offset:18432\n\t"
"s_add_u32 s29, s22, 983424\n\t"
"v_mfma_f32_16x16x32_bf16 v[80:83], a[92:95], a[192:195], v[80:83]\n\t"
"ds_read_b128 v[200:203], v255 offset:1536\n\t"
"v_mfma_f32_16x16x32_bf16 v[80:83], a[88:91], a[188:191], v[80:83]\n\t"
"buffer_load_dwordx4 a[248:251], off, s[80:83], s20 offset:2048 sc1\n\t"
"v_mfma_f32_16x16x32_bf16 v[80:83], a[84:87], a[184:187], v[80:83]\n\t"
"ds_read_b128 a[184:187], v244 offset:35840\n\t"
"v_mfma_f32_16x16x32_bf16 v[84:87], a[84:87], a[188:191], v[84:87]\n\t"
"s_mov_b32 s20, 1388544\n\t"
"ds_read_b128 a[188:191], v244 offset:36352\n\t"
"v_mfma_f32_16x16x32_bf16 v[84:87], a[88:91], a[192:195], v[84:87]\n\t"
"ds_read_b128 a[8:11], v255 offset:19968\n\t"
"v_mfma_f32_16x16x32_bf16 v[84:87], a[92:95], a[196:199], v[84:87]\n\t"
"buffer_store_dwordx4 v[216:219], v248, s[16:19], s29 offen offset:0 nt\n\t"
"ds_read_b128 v[204:207], v255 offset:3072\n\t"
"ds_read_b128 a[12:15], v255 offset:21504\n\t"
"v_mfma_f32_16x16x32_bf16 v[88:91], a[92:95], a[200:203], v[88:91]\n\t"
"v_mfma_f32_16x16x32_bf16 v[88:91], a[88:91], a[196:199], v[88:91]\n\t"
"v_mfma_f32_16x16x32_bf16 v[88:91], a[84:87], a[192:195], v[88:91]\n\t"
"ds_read_b128 a[192:195], v244 offset:36864\n\t"
"v_mfma_f32_16x16x32_bf16 v[92:95], a[84:87], a[196:199], v[92:95]\n\t"
"buffer_load_dwordx4 a[32:35], off, s[80:83], s20 offset:0 sc1\n\t"
"v_mfma_f32_16x16x32_bf16 v[92:95], a[88:91], a[200:203], v[92:95]\n\t"
"s_waitcnt vmcnt(53) lgkmcnt(10)\n\t"
"ds_read_b128 a[196:199], v244 offset:37376\n\t"
"v_mfma_f32_16x16x32_bf16 v[92:95], a[92:95], a[204:207], v[92:95]\n\t"
"ds_read_b128 a[200:203], v244 offset:37888\n\t"
"v_mfma_f32_16x16x32_bf16 v[100:103], a[96:99], a[168:171], v[100:103]\n\t"
"s_waitcnt lgkmcnt(11)\n\t"
"ds_read_b128 a[204:207], v244 offset:38400\n\t"
"v_mfma_f32_16x16x32_bf16 v[100:103], a[220:223], a[176:179], v[100:103]\n\t"
"v_mfma_f32_16x16x32_bf16 v[100:103], a[100:103], a[172:175], v[100:103]\n\t"
"buffer_store_dwordx4 a[0:3], v248, s[16:19], s29 offen offset:128 nt\n\t"
"v_mfma_f32_16x16x32_bf16 v[104:107], a[96:99], a[172:175], v[104:107]\n\t"
"s_waitcnt lgkmcnt(8)\n\t"
"v_mfma_f32_16x16x32_bf16 v[104:107], a[100:103], a[176:179], v[104:107]\n\t"
"v_mfma_f32_16x16x32_bf16 v[104:107], a[220:223], a[180:183], v[104:107]\n\t"
"s_waitcnt lgkmcnt(0)\n\t"
"s_barrier\n\t"
"v_mfma_f32_16x16x32_bf16 v[108:111], a[220:223], a[184:187], v[108:111]\n\t"
"v_mfma_f32_16x16x32_bf16 v[108:111], a[100:103], a[180:183], v[108:111]\n\t"
"buffer_load_dwordx4 a[36:39], off, s[80:83], s20 offset:1024 sc1\n\t"
"v_mfma_f32_16x16x32_bf16 v[108:111], a[96:99], a[176:179], v[108:111]\n\t"
"buffer_load_dwordx4 a[76:79], off, s[80:83], s20 offset:2048 sc1\n\t"
"v_mfma_f32_16x16x32_bf16 v[112:115], a[96:99], a[180:183], v[112:115]\n\t"
"s_mov_b32 s20, 1683456\n\t"
"v_mfma_f32_16x16x32_bf16 v[112:115], a[100:103], a[184:187], v[112:115]\n\t"
"v_mfma_f32_16x16x32_bf16 v[112:115], a[220:223], a[188:191], v[112:115]\n\t"
"buffer_store_dwordx4 a[4:7], v248, s[16:19], s29 offen offset:256 nt\n\t"
"v_mfma_f32_16x16x32_bf16 v[116:119], a[220:223], a[192:195], v[116:119]\n\t"
"s_add_u32 s29, s22, 1180032\n\t"
"v_mfma_f32_16x16x32_bf16 v[116:119], a[100:103], a[188:191], v[116:119]\n\t"
"v_mfma_f32_16x16x32_bf16 v[116:119], a[96:99], a[184:187], v[116:119]\n\t"
"buffer_load_dwordx4 a[0:3], off, s[80:83], s20 offset:0 sc1\n\t"
"v_mfma_f32_16x16x32_bf16 v[120:123], a[96:99], a[188:191], v[120:123]\n\t"
"v_mfma_f32_16x16x32_bf16 v[120:123], a[100:103], a[192:195], v[120:123]\n\t"
"v_mfma_f32_16x16x32_bf16 v[120:123], a[220:223], a[196:199], v[120:123]\n\t"
"v_mfma_f32_16x16x32_bf16 v[124:127], a[220:223], a[200:203], v[124:127]\n\t"
"v_mfma_f32_16x16x32_bf16 v[124:127], a[100:103], a[196:199], v[124:127]\n\t"
"v_mfma_f32_16x16x32_bf16 v[124:127], a[96:99], a[192:195], v[124:127]\n\t"
"buffer_store_dwordx4 v[196:199], v248, s[16:19], s29 offen offset:0 nt\n\t"
"v_mfma_f32_16x16x32_bf16 v[128:131], a[96:99], a[196:199], v[128:131]\n\t"
"s_waitcnt vmcnt(56)\n\t"
"v_mfma_f32_16x16x32_bf16 v[128:131], a[100:103], a[200:203], v[128:131]\n\t"
"buffer_load_dwordx4 a[4:7], off, s[80:83], s20 offset:1024 sc1\n\t"
"v_mfma_f32_16x16x32_bf16 v[128:131], a[220:223], a[204:207], v[128:131]\n\t"
"v_mfma_f32_16x16x32_bf16 v[132:135], a[224:227], a[168:171], v[132:135]\n\t"
"v_mfma_f32_16x16x32_bf16 v[132:135], a[108:111], a[176:179], v[132:135]\n\t"
"buffer_load_dwordx4 a[220:223], off, s[80:83], s20 offset:2048 sc1\n\t"
"v_mfma_f32_16x16x32_bf16 v[132:135], a[252:255], a[172:175], v[132:135]\n\t"
"v_mfma_f32_16x16x32_bf16 v[136:139], a[224:227], a[172:175], v[136:139]\n\t"
"v_mfma_f32_16x16x32_bf16 v[136:139], a[252:255], a[176:179], v[136:139]\n\t"
"v_mfma_f32_16x16x32_bf16 v[136:139], a[108:111], a[180:183], v[136:139]\n\t"
"buffer_store_dwordx4 v[200:203], v248, s[16:19], s29 offen offset:128 nt\n\t"
"v_mfma_f32_16x16x32_bf16 v[140:143], a[108:111], a[184:187], v[140:143]\n\t"
"v_mfma_f32_16x16x32_bf16 v[140:143], a[252:255], a[180:183], v[140:143]\n\t"
"v_mfma_f32_16x16x32_bf16 v[140:143], a[224:227], a[176:179], v[140:143]\n\t"
"v_mfma_f32_16x16x32_bf16 v[144:147], a[224:227], a[180:183], v[144:147]\n\t"
"v_mfma_f32_16x16x32_bf16 v[144:147], a[252:255], a[184:187], v[144:147]\n\t"
"v_mfma_f32_16x16x32_bf16 v[144:147], a[108:111], a[188:191], v[144:147]\n\t"
"v_mfma_f32_16x16x32_bf16 v[148:151], a[108:111], a[192:195], v[148:151]\n\t"
"v_mfma_f32_16x16x32_bf16 v[148:151], a[252:255], a[188:191], v[148:151]\n\t"
"v_mfma_f32_16x16x32_bf16 v[148:151], a[224:227], a[184:187], v[148:151]\n\t"
"buffer_store_dwordx4 v[204:207], v248, s[16:19], s29 offen offset:256 nt\n\t"
"v_mfma_f32_16x16x32_bf16 v[152:155], a[224:227], a[188:191], v[152:155]\n\t"
"s_add_u32 s29, s22, 1376640\n\t"
"v_mfma_f32_16x16x32_bf16 v[152:155], a[252:255], a[192:195], v[152:155]\n\t"
"v_mfma_f32_16x16x32_bf16 v[152:155], a[108:111], a[196:199], v[152:155]\n\t"
"v_mfma_f32_16x16x32_bf16 v[156:159], a[108:111], a[200:203], v[156:159]\n\t"
"v_mfma_f32_16x16x32_bf16 v[156:159], a[252:255], a[196:199], v[156:159]\n\t"
"v_mfma_f32_16x16x32_bf16 v[156:159], a[224:227], a[192:195], v[156:159]\n\t"
"v_mfma_f32_16x16x32_bf16 v[160:163], a[224:227], a[196:199], v[160:163]\n\t"
"s_waitcnt vmcnt(57)\n\t"
"v_mfma_f32_16x16x32_bf16 v[160:163], a[252:255], a[200:203], v[160:163]\n\t"
"v_mfma_f32_16x16x32_bf16 v[160:163], a[108:111], a[204:207], v[160:163]\n\t"
"buffer_store_dwordx4 v[212:215], v248, s[16:19], s29 offen offset:0 nt\n\t"
"v_mfma_f32_16x16x32_bf16 v[164:167], a[116:119], a[168:171], v[164:167]\n\t"
"ds_read_b128 a[168:171], v222 offset:2048\n\t"
"v_mfma_f32_16x16x32_bf16 v[164:167], a[132:135], a[176:179], v[164:167]\n\t"
"ds_read_b128 v[196:199], v223 offset:8192\n\t"
"v_mfma_f32_16x16x32_bf16 v[164:167], a[124:127], a[172:175], v[164:167]\n\t"
"ds_read_b128 v[208:211], v223 offset:14336\n\t"
"v_mfma_f32_16x16x32_bf16 v[168:171], a[116:119], a[172:175], v[168:171]\n\t"
"ds_read_b128 a[172:175], v222 offset:512\n\t"
"v_mfma_f32_16x16x32_bf16 v[168:171], a[124:127], a[176:179], v[168:171]\n\t"
"ds_read_b128 v[200:203], v223 offset:6656\n\t"
"v_mfma_f32_16x16x32_bf16 v[168:171], a[132:135], a[180:183], v[168:171]\n\t"
"ds_read_b128 v[216:219], v223 offset:12800\n\t"
"v_mfma_f32_16x16x32_bf16 v[172:175], a[132:135], a[184:187], v[172:175]\n\t"
"v_mfma_f32_16x16x32_bf16 v[172:175], a[124:127], a[180:183], v[172:175]\n\t"
"v_mfma_f32_16x16x32_bf16 v[172:175], a[116:119], a[176:179], v[172:175]\n\t"
"buffer_store_dwordx4 a[8:11], v248, s[16:19], s29 offen offset:128 nt\n\t"
"v_mfma_f32_16x16x32_bf16 v[176:179], a[116:119], a[180:183], v[176:179]\n\t"
"v_mfma_f32_16x16x32_bf16 v[176:179], a[124:127], a[184:187], v[176:179]\n\t"
"v_mfma_f32_16x16x32_bf16 v[176:179], a[132:135], a[188:191], v[176:179]\n\t"
"s_barrier\n\t"
"v_mfma_f32_16x16x32_bf16 v[180:183], a[132:135], a[192:195], v[180:183]\n\t"
"v_mfma_f32_16x16x32_bf16 v[180:183], a[124:127], a[188:191], v[180:183]\n\t"
"v_mfma_f32_16x16x32_bf16 v[180:183], a[116:119], a[184:187], v[180:183]\n\t"
"v_mfma_f32_16x16x32_bf16 v[184:187], a[116:119], a[188:191], v[184:187]\n\t"
"v_mfma_f32_16x16x32_bf16 v[184:187], a[124:127], a[192:195], v[184:187]\n\t"
"v_mfma_f32_16x16x32_bf16 v[184:187], a[132:135], a[196:199], v[184:187]\n\t"
"buffer_store_dwordx4 a[12:15], v248, s[16:19], s29 offen offset:256 nt\n\t"
"v_mfma_f32_16x16x32_bf16 v[188:191], a[132:135], a[200:203], v[188:191]\n\t"
"ds_read_b128 a[176:179], v222 offset:2304\n\t"
"v_mfma_f32_16x16x32_bf16 v[188:191], a[124:127], a[196:199], v[188:191]\n\t"
"ds_read_b128 a[180:183], v223 offset:8448\n\t"
"v_mfma_f32_16x16x32_bf16 v[188:191], a[116:119], a[192:195], v[188:191]\n\t"
"ds_read_b128 a[184:187], v223 offset:14592\n\t"
"v_mfma_f32_16x16x32_bf16 v[192:195], a[116:119], a[196:199], v[192:195]\n\t"
"s_waitcnt vmcnt(57) lgkmcnt(6)\n\t"
"ds_read_b128 a[188:191], v222 offset:768\n\t"
"v_mfma_f32_16x16x32_bf16 v[192:195], a[124:127], a[200:203], v[192:195]\n\t"
"ds_read_b128 a[192:195], v223 offset:6912\n\t"
"v_mfma_f32_16x16x32_bf16 v[192:195], a[132:135], a[204:207], v[192:195]\n\t"
"ds_read_b128 a[196:199], v223 offset:13056\n\t"
"Lst34_%=:\n\t"
"v_mfma_f32_16x16x32_bf16 v[0:3], a[140:143], a[168:171], v[0:3]\n\t"
"s_waitcnt vmcnt(54) lgkmcnt(6)\n\t"
"v_mfma_f32_16x16x32_bf16 v[0:3], a[208:211], v[196:199], v[0:3]\n\t"
"v_mfma_f32_16x16x32_bf16 v[0:3], a[228:231], v[208:211], v[0:3]\n\t"
"v_mfma_f32_16x16x32_bf16 v[0:3], a[232:235], a[172:175], v[0:3]\n\t"
"s_waitcnt vmcnt(51)\n\t"
"v_mfma_f32_16x16x32_bf16 v[0:3], a[104:107], v[200:203], v[0:3]\n\t"
"v_mfma_f32_16x16x32_bf16 v[0:3], a[112:115], v[216:219], v[0:3]\n\t"
"v_mfma_f32_16x16x32_bf16 v[32:35], a[156:159], v[200:203], v[32:35]\n\t"
"s_waitcnt vmcnt(46)\n\t"
"v_mfma_f32_16x16x32_bf16 v[32:35], a[164:167], v[216:219], v[32:35]\n\t"
"v_mfma_f32_16x16x32_bf16 v[32:35], a[148:151], a[172:175], v[32:35]\n\t"
"v_mfma_f32_16x16x32_bf16 v[32:35], a[52:55], a[168:171], v[32:35]\n\t"
"s_waitcnt vmcnt(40)\n\t"
"v_cvt_pk_bf16_f32 v0, v0, v1\n\t"
"v_cvt_pk_bf16_f32 v1, v2, v3\n\t"
"v_mfma_f32_16x16x32_bf16 v[32:35], a[40:43], v[196:199], v[32:35]\n\t"
"v_mfma_f32_16x16x32_bf16 v[32:35], a[44:47], v[208:211], v[32:35]\n\t"
"v_mfma_f32_16x16x32_bf16 v[64:67], a[56:59], v[196:199], v[64:67]\n\t"
"s_waitcnt vmcnt(36)\n\t"
"ds_read_b128 a[200:203], v222 offset:22016\n\t"
"v_mfma_f32_16x16x32_bf16 v[64:67], v[240:243], v[208:211], v[64:67]\n\t"
"v_mfma_f32_16x16x32_bf16 v[64:67], a[48:51], a[168:171], v[64:67]\n\t"
"s_barrier\n\t"
"s_cmp_lt_u32 s69, 216\n\t"
"s_cbranch_scc0 Lna2_%=\n\t"
"s_add_u32 s71, s71, 1\n\t"
"v_mov_b32 v228, s71\n\t"
"s_mov_b64 exec, 1\n\t"
"buffer_store_dword v228, off, s[8:11], s70 sc1\n\t"
"s_mov_b64 exec, -1\n\t"
"Lna2_%=:\n\t"
"v_mfma_f32_16x16x32_bf16 v[64:67], a[212:215], a[172:175], v[64:67]\n\t"
"s_waitcnt vmcnt(33) lgkmcnt(4)\n\t"
"ds_read_b128 a[168:171], v222 offset:20480\n\t"
"v_mfma_f32_16x16x32_bf16 v[64:67], a[236:239], v[200:203], v[64:67]\n\t"
"v_cvt_pk_bf16_f32 v2, v32, v33\n\t"
"v_cvt_pk_bf16_f32 v3, v34, v35\n\t"
"v_mfma_f32_16x16x32_bf16 v[64:67], a[60:63], v[216:219], v[64:67]\n\t"
"ds_write_b128 v245, v[0:3] offset:0\n\t"
"v_mfma_f32_16x16x32_bf16 v[100:103], a[16:19], a[176:179], v[100:103]\n\t"
"s_waitcnt vmcnt(26) lgkmcnt(3)\n\t"
"v_mfma_f32_16x16x32_bf16 v[100:103], a[20:23], a[180:183], v[100:103]\n\t"
"v_mfma_f32_16x16x32_bf16 v[100:103], a[120:123], a[184:187], v[100:103]\n\t"
"v_mfma_f32_16x16x32_bf16 v[100:103], a[240:243], a[188:191], v[100:103]\n\t"
"s_waitcnt vmcnt(20)\n\t"
"v_cvt_pk_bf16_f32 v64, v64, v65\n\t"
"v_cvt_pk_bf16_f32 v65, v66, v67\n\t"
"v_mfma_f32_16x16x32_bf16 v[100:103], a[64:67], a[192:195], v[100:103]\n\t"
"v_mfma_f32_16x16x32_bf16 v[100:103], a[68:71], a[196:199], v[100:103]\n\t"
"ds_write_b64 v246, v[64:65] offset:0\n\t"
"v_mfma_f32_16x16x32_bf16 v[132:135], a[24:27], a[192:195], v[132:135]\n\t"
"s_waitcnt vmcnt(15)\n\t"
"v_mfma_f32_16x16x32_bf16 v[132:135], a[28:31], a[196:199], v[132:135]\n\t"
"v_mfma_f32_16x16x32_bf16 v[132:135], a[72:75], a[188:191], v[132:135]\n\t"
"v_mfma_f32_16x16x32_bf16 v[132:135], a[216:219], a[176:179], v[132:135]\n\t"
"s_waitcnt vmcnt(10)\n\t"
"v_cvt_pk_bf16_f32 v100, v100, v101\n\t"
"v_cvt_pk_bf16_f32 v101, v102, v103\n\t"
"v_mfma_f32_16x16x32_bf16 v[132:135], a[244:247], a[180:183], v[132:135]\n\t"
"v_mfma_f32_16x16x32_bf16 v[132:135], a[248:251], a[184:187], v[132:135]\n\t"
"v_mfma_f32_16x16x32_bf16 v[164:167], a[36:39], a[180:183], v[164:167]\n\t"
"s_waitcnt vmcnt(5)\n\t"
"ds_read_b128 a[172:175], v222 offset:22272\n\t"
"v_mfma_f32_16x16x32_bf16 v[164:167], a[76:79], a[184:187], v[164:167]\n\t"
"v_mfma_f32_16x16x32_bf16 v[164:167], a[32:35], a[176:179], v[164:167]\n\t"
"v_mfma_f32_16x16x32_bf16 v[164:167], a[0:3], a[188:191], v[164:167]\n\t"
"s_waitcnt lgkmcnt(4)\n\t"
"ds_read_b128 a[176:179], v222 offset:20736\n\t"
"v_cvt_pk_bf16_f32 v102, v132, v133\n\t"
"v_mfma_f32_16x16x32_bf16 v[164:167], a[4:7], a[192:195], v[164:167]\n\t"
"v_cvt_pk_bf16_f32 v103, v134, v135\n\t"
"v_mfma_f32_16x16x32_bf16 v[164:167], a[220:223], a[196:199], v[164:167]\n\t"
"ds_write_b128 v245, v[100:103] offset:9216\n\t"
"v_mfma_f32_16x16x32_bf16 v[4:7], a[140:143], v[196:199], v[4:7]\n\t"
"s_waitcnt lgkmcnt(5)\n\t"
"v_mfma_f32_16x16x32_bf16 v[4:7], a[208:211], v[208:211], v[4:7]\n\t"
"v_mfma_f32_16x16x32_bf16 v[4:7], a[228:231], a[200:203], v[4:7]\n\t"
"v_mfma_f32_16x16x32_bf16 v[4:7], a[232:235], v[200:203], v[4:7]\n\t"
"v_cvt_pk_bf16_f32 v164, v164, v165\n\t"
"v_cvt_pk_bf16_f32 v165, v166, v167\n\t"
"v_mfma_f32_16x16x32_bf16 v[4:7], a[104:107], v[216:219], v[4:7]\n\t"
"v_mfma_f32_16x16x32_bf16 v[4:7], a[112:115], a[168:171], v[4:7]\n\t"
"ds_write_b64 v246, v[164:165] offset:9216\n\t"
"v_mfma_f32_16x16x32_bf16 v[36:39], a[156:159], v[216:219], v[36:39]\n\t"
"v_mfma_f32_16x16x32_bf16 v[36:39], a[164:167], a[168:171], v[36:39]\n\t"
"v_mfma_f32_16x16x32_bf16 v[36:39], a[148:151], v[200:203], v[36:39]\n\t"
"v_mfma_f32_16x16x32_bf16 v[36:39], a[52:55], v[196:199], v[36:39]\n\t"
"v_cvt_pk_bf16_f32 v4, v4, v5\n\t"
"v_cvt_pk_bf16_f32 v5, v6, v7\n\t"
"v_mfma_f32_16x16x32_bf16 v[36:39], a[40:43], v[208:211], v[36:39]\n\t"
"v_mfma_f32_16x16x32_bf16 v[36:39], a[44:47], a[200:203], v[36:39]\n\t"
"v_mfma_f32_16x16x32_bf16 v[68:71], a[56:59], v[208:211], v[68:71]\n\t"
"ds_read_b128 a[188:191], v223 offset:28160\n\t"
"v_mfma_f32_16x16x32_bf16 v[68:71], v[240:243], a[200:203], v[68:71]\n\t"
"v_mfma_f32_16x16x32_bf16 v[68:71], a[48:51], v[196:199], v[68:71]\n\t"
"s_waitcnt lgkmcnt(1)\n\t"
"s_barrier\n\t"
"v_mfma_f32_16x16x32_bf16 v[68:71], a[212:215], v[200:203], v[68:71]\n\t"
"ds_read_b128 v[196:199], v255 offset:0\n\t"
"v_mfma_f32_16x16x32_bf16 v[68:71], a[236:239], v[216:219], v[68:71]\n\t"
"ds_read_b128 v[200:203], v255 offset:1536\n\t"
"v_cvt_pk_bf16_f32 v6, v36, v37\n\t"
"v_mfma_f32_16x16x32_bf16 v[68:71], a[60:63], a[168:171], v[68:71]\n\t"
"ds_read_b128 v[204:207], v255 offset:3072\n\t"
"v_cvt_pk_bf16_f32 v7, v38, v39\n\t"
"v_mfma_f32_16x16x32_bf16 v[104:107], a[16:19], a[180:183], v[104:107]\n\t"
"s_add_u32 s29, s23, 0\n\t"
"ds_read_b128 a[204:207], v223 offset:26624\n\t"
"v_mfma_f32_16x16x32_bf16 v[104:107], a[20:23], a[184:187], v[104:107]\n\t"
"v_mfma_f32_16x16x32_bf16 v[104:107], a[120:123], a[172:175], v[104:107]\n\t"
"ds_write_b128 v245, v[4:7] offset:18432\n\t"
"v_mfma_f32_16x16x32_bf16 v[104:107], a[240:243], a[192:195], v[104:107]\n\t"
"s_waitcnt lgkmcnt(4)\n\t"
"v_cvt_pk_bf16_f32 v68, v68, v69\n\t"
"v_cvt_pk_bf16_f32 v69, v70, v71\n\t"
"v_mfma_f32_16x16x32_bf16 v[104:107], a[64:67], a[196:199], v[104:107]\n\t"
"v_mfma_f32_16x16x32_bf16 v[104:107], a[68:71], a[176:179], v[104:107]\n\t"
"ds_write_b64 v246, v[68:69] offset:18432\n\t"
"v_mfma_f32_16x16x32_bf16 v[136:139], a[24:27], a[196:199], v[136:139]\n\t"
"s_waitcnt lgkmcnt(4)\n\t"
"v_mfma_f32_16x16x32_bf16 v[136:139], a[28:31], a[176:179], v[136:139]\n\t"
"v_mfma_f32_16x16x32_bf16 v[136:139], a[72:75], a[192:195], v[136:139]\n\t"
"buffer_store_dwordx4 v[196:199], v248, s[16:19], s29 offen offset:0 sc1\n\t"
"v_mfma_f32_16x16x32_bf16 v[136:139], a[216:219], a[180:183], v[136:139]\n\t"
"s_waitcnt lgkmcnt(3)\n\t"
"v_cvt_pk_bf16_f32 v104, v104, v105\n\t"
"v_cvt_pk_bf16_f32 v105, v106, v107\n\t"
"v_mfma_f32_16x16x32_bf16 v[136:139], a[244:247], a[184:187], v[136:139]\n\t"
"v_mfma_f32_16x16x32_bf16 v[136:139], a[248:251], a[172:175], v[136:139]\n\t"
"buffer_store_dwordx4 v[200:203], v248, s[16:19], s29 offen offset:128 sc1\n\t"
"v_mfma_f32_16x16x32_bf16 v[168:171], a[36:39], a[184:187], v[168:171]\n\t"
"ds_read_b128 v[212:215], v223 offset:28416\n\t"
"v_mfma_f32_16x16x32_bf16 v[168:171], a[76:79], a[172:175], v[168:171]\n\t"
"v_mfma_f32_16x16x32_bf16 v[168:171], a[32:35], a[180:183], v[168:171]\n\t"
"buffer_store_dwordx4 v[204:207], v248, s[16:19], s29 offen offset:256 sc1\n\t"
"v_mfma_f32_16x16x32_bf16 v[168:171], a[0:3], a[192:195], v[168:171]\n\t"
"ds_read_b128 a[180:183], v223 offset:26880\n\t"
"v_cvt_pk_bf16_f32 v106, v136, v137\n\t"
"v_mfma_f32_16x16x32_bf16 v[168:171], a[4:7], a[196:199], v[168:171]\n\t"
"v_cvt_pk_bf16_f32 v107, v138, v139\n\t"
"v_mfma_f32_16x16x32_bf16 v[168:171], a[220:223], a[176:179], v[168:171]\n\t"
"ds_write_b128 v245, v[104:107] offset:27648\n\t"
"v_mfma_f32_16x16x32_bf16 v[8:11], a[140:143], v[208:211], v[8:11]\n\t"
"s_waitcnt lgkmcnt(5)\n\t"
"v_mfma_f32_16x16x32_bf16 v[8:11], a[208:211], a[200:203], v[8:11]\n\t"
"v_mfma_f32_16x16x32_bf16 v[8:11], a[228:231], a[188:191], v[8:11]\n\t"
"v_mfma_f32_16x16x32_bf16 v[8:11], a[232:235], v[216:219], v[8:11]\n\t"
"v_cvt_pk_bf16_f32 v168, v168, v169\n\t"
"v_cvt_pk_bf16_f32 v169, v170, v171\n\t"
"v_mfma_f32_16x16x32_bf16 v[8:11], a[104:107], a[168:171], v[8:11]\n\t"
"v_mfma_f32_16x16x32_bf16 v[8:11], a[112:115], a[204:207], v[8:11]\n\t"
"ds_write_b64 v246, v[168:169] offset:27648\n\t"
"v_mfma_f32_16x16x32_bf16 v[40:43], a[156:159], a[168:171], v[40:43]\n\t"
"v_mfma_f32_16x16x32_bf16 v[40:43], a[164:167], a[204:207], v[40:43]\n\t"
"v_mfma_f32_16x16x32_bf16 v[40:43], a[148:151], v[216:219], v[40:43]\n\t"
"v_mfma_f32_16x16x32_bf16 v[40:43], a[52:55], v[208:211], v[40:43]\n\t"
"v_cvt_pk_bf16_f32 v8, v8, v9\n\t"
"v_cvt_pk_bf16_f32 v9, v10, v11\n\t"
"v_mfma_f32_16x16x32_bf16 v[40:43], a[40:43], a[200:203], v[40:43]\n\t"
"v_mfma_f32_16x16x32_bf16 v[40:43], a[44:47], a[188:191], v[40:43]\n\t"
"v_mfma_f32_16x16x32_bf16 v[72:75], a[56:59], a[200:203], v[72:75]\n\t"
"ds_read_b128 a[192:195], v223 offset:34304\n\t"
"v_mfma_f32_16x16x32_bf16 v[72:75], v[240:243], a[188:191], v[72:75]\n\t"
"v_mfma_f32_16x16x32_bf16 v[72:75], a[48:51], v[208:211], v[72:75]\n\t"
"s_waitcnt lgkmcnt(1)\n\t"
"s_barrier\n\t"
"v_mfma_f32_16x16x32_bf16 v[72:75], a[212:215], v[216:219], v[72:75]\n\t"
"ds_read_b128 v[196:199], v255 offset:18432\n\t"
"v_mfma_f32_16x16x32_bf16 v[72:75], a[236:239], a[168:171], v[72:75]\n\t"
"ds_read_b128 v[200:203], v255 offset:19968\n\t"
"v_cvt_pk_bf16_f32 v10, v40, v41\n\t"
"v_mfma_f32_16x16x32_bf16 v[72:75], a[60:63], a[204:207], v[72:75]\n\t"
"ds_read_b128 v[204:207], v255 offset:21504\n\t"
"v_cvt_pk_bf16_f32 v11, v42, v43\n\t"
"v_mfma_f32_16x16x32_bf16 v[108:111], a[16:19], a[184:187], v[108:111]\n\t"
"s_add_u32 s29, s23, 196608\n\t"
"ds_read_b128 v[208:211], v223 offset:32768\n\t"
"v_mfma_f32_16x16x32_bf16 v[108:111], a[20:23], a[172:175], v[108:111]\n\t"
"v_mfma_f32_16x16x32_bf16 v[108:111], a[120:123], v[212:215], v[108:111]\n\t"
"ds_write_b128 v245, v[8:11] offset:0\n\t"
"v_mfma_f32_16x16x32_bf16 v[108:111], a[240:243], a[196:199], v[108:111]\n\t"
"s_waitcnt lgkmcnt(4)\n\t"
"v_cvt_pk_bf16_f32 v72, v72, v73\n\t"
"v_cvt_pk_bf16_f32 v73, v74, v75\n\t"
"v_mfma_f32_16x16x32_bf16 v[108:111], a[64:67], a[176:179], v[108:111]\n\t"
"v_mfma_f32_16x16x32_bf16 v[108:111], a[68:71], a[180:183], v[108:111]\n\t"
"ds_write_b64 v246, v[72:73] offset:0\n\t"
"v_mfma_f32_16x16x32_bf16 v[140:143], a[24:27], a[176:179], v[140:143]\n\t"
"s_waitcnt lgkmcnt(4)\n\t"
"v_mfma_f32_16x16x32_bf16 v[140:143], a[28:31], a[180:183], v[140:143]\n\t"
"v_mfma_f32_16x16x32_bf16 v[140:143], a[72:75], a[196:199], v[140:143]\n\t"
"buffer_store_dwordx4 v[196:199], v248, s[16:19], s29 offen offset:0 sc1\n\t"
"v_mfma_f32_16x16x32_bf16 v[140:143], a[216:219], a[184:187], v[140:143]\n\t"
"s_waitcnt lgkmcnt(3)\n\t"
"v_cvt_pk_bf16_f32 v108, v108, v109\n\t"
"v_cvt_pk_bf16_f32 v109, v110, v111\n\t"
"v_mfma_f32_16x16x32_bf16 v[140:143], a[244:247], a[172:175], v[140:143]\n\t"
"v_mfma_f32_16x16x32_bf16 v[140:143], a[248:251], v[212:215], v[140:143]\n\t"
"buffer_store_dwordx4 v[200:203], v248, s[16:19], s29 offen offset:128 sc1\n\t"
"v_mfma_f32_16x16x32_bf16 v[172:175], a[36:39], a[172:175], v[172:175]\n\t"
"ds_read_b128 v[216:219], v223 offset:34560\n\t"
"v_mfma_f32_16x16x32_bf16 v[172:175], a[76:79], v[212:215], v[172:175]\n\t"
"v_mfma_f32_16x16x32_bf16 v[172:175], a[32:35], a[184:187], v[172:175]\n\t"
"buffer_store_dwordx4 v[204:207], v248, s[16:19], s29 offen offset:256 sc1\n\t"
"v_mfma_f32_16x16x32_bf16 v[172:175], a[0:3], a[196:199], v[172:175]\n\t"
"ds_read_b128 a[184:187], v223 offset:33024\n\t"
"v_cvt_pk_bf16_f32 v110, v140, v141\n\t"
"v_mfma_f32_16x16x32_bf16 v[172:175], a[4:7], a[176:179], v[172:175]\n\t"
"v_cvt_pk_bf16_f32 v111, v142, v143\n\t"
"v_mfma_f32_16x16x32_bf16 v[172:175], a[220:223], a[180:183], v[172:175]\n\t"
"ds_write_b128 v245, v[108:111] offset:9216\n\t"
"v_mfma_f32_16x16x32_bf16 v[12:15], a[140:143], a[200:203], v[12:15]\n\t"
"s_waitcnt lgkmcnt(5)\n\t"
"v_mfma_f32_16x16x32_bf16 v[12:15], a[208:211], a[188:191], v[12:15]\n\t"
"v_mfma_f32_16x16x32_bf16 v[12:15], a[228:231], a[192:195], v[12:15]\n\t"
"v_mfma_f32_16x16x32_bf16 v[12:15], a[232:235], a[168:171], v[12:15]\n\t"
"v_cvt_pk_bf16_f32 v172, v172, v173\n\t"
"v_cvt_pk_bf16_f32 v173, v174, v175\n\t"
"v_mfma_f32_16x16x32_bf16 v[12:15], a[104:107], a[204:207], v[12:15]\n\t"
"v_mfma_f32_16x16x32_bf16 v[12:15], a[112:115], v[208:211], v[12:15]\n\t"
"ds_write_b64 v246, v[172:173] offset:9216\n\t"
"v_mfma_f32_16x16x32_bf16 v[44:47], a[156:159], a[204:207], v[44:47]\n\t"
"v_mfma_f32_16x16x32_bf16 v[44:47], a[164:167], v[208:211], v[44:47]\n\t"
"v_mfma_f32_16x16x32_bf16 v[44:47], a[148:151], a[168:171], v[44:47]\n\t"
"v_mfma_f32_16x16x32_bf16 v[44:47], a[52:55], a[200:203], v[44:47]\n\t"
"v_cvt_pk_bf16_f32 v12, v12, v13\n\t"
"v_cvt_pk_bf16_f32 v13, v14, v15\n\t"
"v_mfma_f32_16x16x32_bf16 v[44:47], a[40:43], a[188:191], v[44:47]\n\t"
"v_mfma_f32_16x16x32_bf16 v[44:47], a[44:47], a[192:195], v[44:47]\n\t"
"v_mfma_f32_16x16x32_bf16 v[76:79], a[56:59], a[188:191], v[76:79]\n\t"
"ds_read_b128 a[196:199], v222 offset:41984\n\t"
"v_mfma_f32_16x16x32_bf16 v[76:79], v[240:243], a[192:195], v[76:79]\n\t"
"v_mfma_f32_16x16x32_bf16 v[76:79], a[48:51], a[200:203], v[76:79]\n\t"
"s_waitcnt lgkmcnt(1)\n\t"
"s_barrier\n\t"
"v_mfma_f32_16x16x32_bf16 v[76:79], a[212:215], a[168:171], v[76:79]\n\t"
"ds_read_b128 v[196:199], v255 offset:0\n\t"
"v_mfma_f32_16x16x32_bf16 v[76:79], a[236:239], a[204:207], v[76:79]\n\t"
"ds_read_b128 v[200:203], v255 offset:1536\n\t"
"v_cvt_pk_bf16_f32 v14, v44, v45\n\t"
"v_mfma_f32_16x16x32_bf16 v[76:79], a[60:63], v[208:211], v[76:79]\n\t"
"ds_read_b128 v[204:207], v255 offset:3072\n\t"
"v_cvt_pk_bf16_f32 v15, v46, v47\n\t"
"v_mfma_f32_16x16x32_bf16 v[112:115], a[16:19], a[172:175], v[112:115]\n\t"
"s_add_u32 s29, s23, 393216\n\t"
"ds_read_b128 a[168:171], v222 offset:40448\n\t"
"v_mfma_f32_16x16x32_bf16 v[112:115], a[20:23], v[212:215], v[112:115]\n\t"
"v_mfma_f32_16x16x32_bf16 v[112:115], a[120:123], v[216:219], v[112:115]\n\t"
"ds_write_b128 v245, v[12:15] offset:18432\n\t"
"v_mfma_f32_16x16x32_bf16 v[112:115], a[240:243], a[176:179], v[112:115]\n\t"
"s_waitcnt lgkmcnt(4)\n\t"
"v_cvt_pk_bf16_f32 v76, v76, v77\n\t"
"v_cvt_pk_bf16_f32 v77, v78, v79\n\t"
"v_mfma_f32_16x16x32_bf16 v[112:115], a[64:67], a[180:183], v[112:115]\n\t"
"v_mfma_f32_16x16x32_bf16 v[112:115], a[68:71], a[184:187], v[112:115]\n\t"
"ds_write_b64 v246, v[76:77] offset:18432\n\t"
"v_mfma_f32_16x16x32_bf16 v[144:147], a[24:27], a[180:183], v[144:147]\n\t"
"s_waitcnt lgkmcnt(4)\n\t"
"v_mfma_f32_16x16x32_bf16 v[144:147], a[28:31], a[184:187], v[144:147]\n\t"
"v_mfma_f32_16x16x32_bf16 v[144:147], a[72:75], a[176:179], v[144:147]\n\t"
"buffer_store_dwordx4 v[196:199], v248, s[16:19], s29 offen offset:0 sc1\n\t"
"v_mfma_f32_16x16x32_bf16 v[144:147], a[216:219], a[172:175], v[144:147]\n\t"
"s_waitcnt lgkmcnt(3)\n\t"
"v_cvt_pk_bf16_f32 v112, v112, v113\n\t"
"v_cvt_pk_bf16_f32 v113, v114, v115\n\t"
"v_mfma_f32_16x16x32_bf16 v[144:147], a[244:247], v[212:215], v[144:147]\n\t"
"v_mfma_f32_16x16x32_bf16 v[144:147], a[248:251], v[216:219], v[144:147]\n\t"
"buffer_store_dwordx4 v[200:203], v248, s[16:19], s29 offen offset:128 sc1\n\t"
"v_mfma_f32_16x16x32_bf16 v[176:179], a[36:39], v[212:215], v[176:179]\n\t"
"s_waitcnt lgkmcnt(2)\n\t"
"ds_read_b128 a[200:203], v222 offset:42240\n\t"
"v_mfma_f32_16x16x32_bf16 v[176:179], a[76:79], v[216:219], v[176:179]\n\t"
"Lpg25_%=:\n\t"
"s_cbranch_vccnz Lvr4_%=\n\t"
"v_mfma_f32_16x16x32_bf16 v[176:179], a[32:35], a[172:175], v[176:179]\n\t"
"buffer_store_dwordx4 v[204:207], v248, s[16:19], s29 offen offset:256 sc1\n\t"
"v_mfma_f32_16x16x32_bf16 v[176:179], a[0:3], a[176:179], v[176:179]\n\t"
"ds_read_b128 a[172:175], v222 offset:40704\n\t"
"v_cvt_pk_bf16_f32 v114, v144, v145\n\t"
"v_mfma_f32_16x16x32_bf16 v[176:179], a[4:7], a[180:183], v[176:179]\n\t"
"v_cvt_pk_bf16_f32 v115, v146, v147\n\t"
"v_mfma_f32_16x16x32_bf16 v[176:179], a[220:223], a[184:187], v[176:179]\n\t"
"ds_write_b128 v245, v[112:115] offset:27648\n\t"
"Lst35_%=:\n\t"
"v_mfma_f32_16x16x32_bf16 v[16:19], a[140:143], a[188:191], v[16:19]\n\t"
"v_mfma_f32_16x16x32_bf16 v[16:19], a[208:211], a[192:195], v[16:19]\n\t"
"v_mfma_f32_16x16x32_bf16 v[16:19], a[228:231], a[196:199], v[16:19]\n\t"
"v_mfma_f32_16x16x32_bf16 v[16:19], a[232:235], a[204:207], v[16:19]\n\t"
"v_cvt_pk_bf16_f32 v176, v176, v177\n\t"
"v_cvt_pk_bf16_f32 v177, v178, v179\n\t"
"v_mfma_f32_16x16x32_bf16 v[16:19], a[104:107], v[208:211], v[16:19]\n\t"
"v_mfma_f32_16x16x32_bf16 v[16:19], a[112:115], a[168:171], v[16:19]\n\t"
"ds_write_b64 v246, v[176:177] offset:27648\n\t"
"v_mfma_f32_16x16x32_bf16 v[48:51], a[156:159], v[208:211], v[48:51]\n\t"
"v_mfma_f32_16x16x32_bf16 v[48:51], a[164:167], a[168:171], v[48:51]\n\t"
"v_mfma_f32_16x16x32_bf16 v[48:51], a[148:151], a[204:207], v[48:51]\n\t"
"v_mfma_f32_16x16x32_bf16 v[48:51], a[52:55], a[188:191], v[48:51]\n\t"
"v_cvt_pk_bf16_f32 v16, v16, v17\n\t"
"v_cvt_pk_bf16_f32 v17, v18, v19\n\t"
"v_mfma_f32_16x16x32_bf16 v[48:51], a[40:43], a[192:195], v[48:51]\n\t"
"v_mfma_f32_16x16x32_bf16 v[48:51], a[44:47], a[196:199], v[48:51]\n\t"
"v_mfma_f32_16x16x32_bf16 v[80:83], a[56:59], a[192:195], v[80:83]\n\t"
"ds_read_b128 a[176:179], v223 offset:48128\n\t"
"v_mfma_f32_16x16x32_bf16 v[80:83], v[240:243], a[196:199], v[80:83]\n\t"
"v_mfma_f32_16x16x32_bf16 v[80:83], a[48:51], a[188:191], v[80:83]\n\t"
"s_waitcnt lgkmcnt(1)\n\t"
"s_barrier\n\t"
"v_mfma_f32_16x16x32_bf16 v[80:83], a[212:215], a[204:207], v[80:83]\n\t"
"ds_read_b128 v[196:199], v255 offset:18432\n\t"
"v_mfma_f32_16x16x32_bf16 v[80:83], a[236:239], v[208:211], v[80:83]\n\t"
"ds_read_b128 v[200:203], v255 offset:19968\n\t"
"v_cvt_pk_bf16_f32 v18, v48, v49\n\t"
"v_mfma_f32_16x16x32_bf16 v[80:83], a[60:63], a[168:171], v[80:83]\n\t"
"ds_read_b128 v[204:207], v255 offset:21504\n\t"
"v_cvt_pk_bf16_f32 v19, v50, v51\n\t"
"v_mfma_f32_16x16x32_bf16 v[116:119], a[16:19], v[212:215], v[116:119]\n\t"
"s_add_u32 s29, s23, 589824\n\t"
"ds_read_b128 a[188:191], v223 offset:46592\n\t"
"v_mfma_f32_16x16x32_bf16 v[116:119], a[20:23], v[216:219], v[116:119]\n\t"
"v_mfma_f32_16x16x32_bf16 v[116:119], a[120:123], a[200:203], v[116:119]\n\t"
"ds_write_b128 v245, v[16:19] offset:0\n\t"
"v_mfma_f32_16x16x32_bf16 v[116:119], a[240:243], a[180:183], v[116:119]\n\t"
"s_waitcnt lgkmcnt(4)\n\t"
"v_cvt_pk_bf16_f32 v80, v80, v81\n\t"
"v_cvt_pk_bf16_f32 v81, v82, v83\n\t"
"v_mfma_f32_16x16x32_bf16 v[116:119], a[64:67], a[184:187], v[116:119]\n\t"
"v_mfma_f32_16x16x32_bf16 v[116:119], a[68:71], a[172:175], v[116:119]\n\t"
"ds_write_b64 v246, v[80:81] offset:0\n\t"
"v_mfma_f32_16x16x32_bf16 v[148:151], a[24:27], a[184:187], v[148:151]\n\t"
"s_waitcnt lgkmcnt(4)\n\t"
"v_mfma_f32_16x16x32_bf16 v[148:151], a[28:31], a[172:175], v[148:151]\n\t"
"v_mfma_f32_16x16x32_bf16 v[148:151], a[72:75], a[180:183], v[148:151]\n\t"
"buffer_store_dwordx4 v[196:199], v248, s[16:19], s29 offen offset:0 sc1\n\t"
"v_mfma_f32_16x16x32_bf16 v[148:151], a[216:219], v[212:215], v[148:151]\n\t"
"s_waitcnt lgkmcnt(3)\n\t"
"v_cvt_pk_bf16_f32 v116, v116, v117\n\t"
"v_cvt_pk_bf16_f32 v117, v118, v119\n\t"
"v_mfma_f32_16x16x32_bf16 v[148:151], a[244:247], v[216:219], v[148:151]\n\t"
"v_mfma_f32_16x16x32_bf16 v[148:151], a[248:251], a[200:203], v[148:151]\n\t"
"buffer_store_dwordx4 v[200:203], v248, s[16:19], s29 offen offset:128 sc1\n\t"
"v_mfma_f32_16x16x32_bf16 v[180:183], a[36:39], v[216:219], v[180:183]\n\t"
"ds_read_b128 a[204:207], v223 offset:48384\n\t"
"v_mfma_f32_16x16x32_bf16 v[180:183], a[76:79], a[200:203], v[180:183]\n\t"
"v_mfma_f32_16x16x32_bf16 v[180:183], a[32:35], v[212:215], v[180:183]\n\t"
"buffer_store_dwordx4 v[204:207], v248, s[16:19], s29 offen offset:256 sc1\n\t"
"v_mfma_f32_16x16x32_bf16 v[180:183], a[0:3], a[180:183], v[180:183]\n\t"
"ds_read_b128 a[180:183], v223 offset:46848\n\t"
"v_cvt_pk_bf16_f32 v118, v148, v149\n\t"
"v_mfma_f32_16x16x32_bf16 v[180:183], a[4:7], a[184:187], v[180:183]\n\t"
"v_cvt_pk_bf16_f32 v119, v150, v151\n\t"
"v_mfma_f32_16x16x32_bf16 v[180:183], a[220:223], a[172:175], v[180:183]\n\t"
"ds_write_b128 v245, v[116:119] offset:9216\n\t"
"v_mfma_f32_16x16x32_bf16 v[20:23], a[140:143], a[192:195], v[20:23]\n\t"
"s_waitcnt lgkmcnt(5)\n\t"
"v_mfma_f32_16x16x32_bf16 v[20:23], a[208:211], a[196:199], v[20:23]\n\t"
"v_mfma_f32_16x16x32_bf16 v[20:23], a[228:231], a[176:179], v[20:23]\n\t"
"v_mfma_f32_16x16x32_bf16 v[20:23], a[232:235], v[208:211], v[20:23]\n\t"
"v_cvt_pk_bf16_f32 v180, v180, v181\n\t"
"v_cvt_pk_bf16_f32 v181, v182, v183\n\t"
"v_mfma_f32_16x16x32_bf16 v[20:23], a[104:107], a[168:171], v[20:23]\n\t"
"v_mfma_f32_16x16x32_bf16 v[20:23], a[112:115], a[188:191], v[20:23]\n\t"
"ds_write_b64 v246, v[180:181] offset:9216\n\t"
"v_mfma_f32_16x16x32_bf16 v[52:55], a[156:159], a[168:171], v[52:55]\n\t"
"v_mfma_f32_16x16x32_bf16 v[52:55], a[164:167], a[188:191], v[52:55]\n\t"
"v_mfma_f32_16x16x32_bf16 v[52:55], a[148:151], v[208:211], v[52:55]\n\t"
"v_mfma_f32_16x16x32_bf16 v[52:55], a[52:55], a[192:195], v[52:55]\n\t"
"v_cvt_pk_bf16_f32 v20, v20, v21\n\t"
"v_cvt_pk_bf16_f32 v21, v22, v23\n\t"
"v_mfma_f32_16x16x32_bf16 v[52:55], a[40:43], a[196:199], v[52:55]\n\t"
"v_mfma_f32_16x16x32_bf16 v[52:55], a[44:47], a[176:179], v[52:55]\n\t"
"v_mfma_f32_16x16x32_bf16 v[84:87], a[56:59], a[196:199], v[84:87]\n\t"
"ds_read_b128 v[212:215], v223 offset:54272\n\t"
"v_mfma_f32_16x16x32_bf16 v[84:87], v[240:243], a[176:179], v[84:87]\n\t"
"v_mfma_f32_16x16x32_bf16 v[84:87], a[48:51], a[192:195], v[84:87]\n\t"
"s_waitcnt lgkmcnt(1)\n\t"
"s_barrier\n\t"
"v_mfma_f32_16x16x32_bf16 v[84:87], a[212:215], v[208:211], v[84:87]\n\t"
"ds_read_b128 v[196:199], v255 offset:0\n\t"
"v_mfma_f32_16x16x32_bf16 v[84:87], a[236:239], a[168:171], v[84:87]\n\t"
"ds_read_b128 v[200:203], v255 offset:1536\n\t"
"v_cvt_pk_bf16_f32 v22, v52, v53\n\t"
"v_mfma_f32_16x16x32_bf16 v[84:87], a[60:63], a[188:191], v[84:87]\n\t"
"ds_read_b128 v[204:207], v255 offset:3072\n\t"
"v_cvt_pk_bf16_f32 v23, v54, v55\n\t"
"v_mfma_f32_16x16x32_bf16 v[120:123], a[16:19], v[216:219], v[120:123]\n\t"
"s_add_u32 s29, s23, 786432\n\t"
"ds_read_b128 a[192:195], v223 offset:52736\n\t"
"v_mfma_f32_16x16x32_bf16 v[120:123], a[20:23], a[200:203], v[120:123]\n\t"
"v_mfma_f32_16x16x32_bf16 v[120:123], a[120:123], a[204:207], v[120:123]\n\t"
"ds_write_b128 v245, v[20:23] offset:18432\n\t"
"v_mfma_f32_16x16x32_bf16 v[120:123], a[240:243], a[184:187], v[120:123]\n\t"
"s_waitcnt lgkmcnt(4)\n\t"
"v_cvt_pk_bf16_f32 v84, v84, v85\n\t"
"v_cvt_pk_bf16_f32 v85, v86, v87\n\t"
"v_mfma_f32_16x16x32_bf16 v[120:123], a[64:67], a[172:175], v[120:123]\n\t"
"v_mfma_f32_16x16x32_bf16 v[120:123], a[68:71], a[180:183], v[120:123]\n\t"
"ds_write_b64 v246, v[84:85] offset:18432\n\t"
"v_mfma_f32_16x16x32_bf16 v[152:155], a[24:27], a[172:175], v[152:155]\n\t"
"s_waitcnt lgkmcnt(4)\n\t"
"v_mfma_f32_16x16x32_bf16 v[152:155], a[28:31], a[180:183], v[152:155]\n\t"
"v_mfma_f32_16x16x32_bf16 v[152:155], a[72:75], a[184:187], v[152:155]\n\t"
"buffer_store_dwordx4 v[196:199], v248, s[16:19], s29 offen offset:0 sc1\n\t"
"v_mfma_f32_16x16x32_bf16 v[152:155], a[216:219], v[216:219], v[152:155]\n\t"
"s_waitcnt lgkmcnt(3)\n\t"
"v_cvt_pk_bf16_f32 v120, v120, v121\n\t"
"v_cvt_pk_bf16_f32 v121, v122, v123\n\t"
"v_mfma_f32_16x16x32_bf16 v[152:155], a[244:247], a[200:203], v[152:155]\n\t"
"v_mfma_f32_16x16x32_bf16 v[152:155], a[248:251], a[204:207], v[152:155]\n\t"
"buffer_store_dwordx4 v[200:203], v248, s[16:19], s29 offen offset:128 sc1\n\t"
"v_mfma_f32_16x16x32_bf16 v[184:187], a[36:39], a[200:203], v[184:187]\n\t"
"ds_read_b128 v[208:211], v223 offset:54528\n\t"
"v_mfma_f32_16x16x32_bf16 v[184:187], a[76:79], a[204:207], v[184:187]\n\t"
"v_mfma_f32_16x16x32_bf16 v[184:187], a[32:35], v[216:219], v[184:187]\n\t"
"buffer_store_dwordx4 v[204:207], v248, s[16:19], s29 offen offset:256 sc1\n\t"
"v_mfma_f32_16x16x32_bf16 v[184:187], a[0:3], a[184:187], v[184:187]\n\t"
"ds_read_b128 a[184:187], v223 offset:52992\n\t"
"v_cvt_pk_bf16_f32 v122, v152, v153\n\t"
"v_mfma_f32_16x16x32_bf16 v[184:187], a[4:7], a[172:175], v[184:187]\n\t"
"v_cvt_pk_bf16_f32 v123, v154, v155\n\t"
"v_mfma_f32_16x16x32_bf16 v[184:187], a[220:223], a[180:183], v[184:187]\n\t"
"ds_write_b128 v245, v[120:123] offset:27648\n\t"
"v_mfma_f32_16x16x32_bf16 v[24:27], a[140:143], a[196:199], v[24:27]\n\t"
"s_waitcnt lgkmcnt(5)\n\t"
"v_mfma_f32_16x16x32_bf16 v[24:27], a[208:211], a[176:179], v[24:27]\n\t"
"v_mfma_f32_16x16x32_bf16 v[24:27], a[228:231], v[212:215], v[24:27]\n\t"
"v_mfma_f32_16x16x32_bf16 v[24:27], a[232:235], a[168:171], v[24:27]\n\t"
"v_cvt_pk_bf16_f32 v184, v184, v185\n\t"
"v_cvt_pk_bf16_f32 v185, v186, v187\n\t"
"v_mfma_f32_16x16x32_bf16 v[24:27], a[104:107], a[188:191], v[24:27]\n\t"
"v_mfma_f32_16x16x32_bf16 v[24:27], a[112:115], a[192:195], v[24:27]\n\t"
"ds_write_b64 v246, v[184:185] offset:27648\n\t"
"v_mfma_f32_16x16x32_bf16 v[56:59], a[156:159], a[188:191], v[56:59]\n\t"
"v_mfma_f32_16x16x32_bf16 v[56:59], a[164:167], a[192:195], v[56:59]\n\t"
"v_mfma_f32_16x16x32_bf16 v[56:59], a[148:151], a[168:171], v[56:59]\n\t"
"v_mfma_f32_16x16x32_bf16 v[56:59], a[52:55], a[196:199], v[56:59]\n\t"
"v_cvt_pk_bf16_f32 v24, v24, v25\n\t"
"v_cvt_pk_bf16_f32 v25, v26, v27\n\t"
"v_mfma_f32_16x16x32_bf16 v[56:59], a[40:43], a[176:179], v[56:59]\n\t"
"v_mfma_f32_16x16x32_bf16 v[56:59], a[44:47], v[212:215], v[56:59]\n\t"
"v_mfma_f32_16x16x32_bf16 v[88:91], a[56:59], a[176:179], v[88:91]\n\t"
"ds_read_b128 v[216:219], v222 offset:61952\n\t"
"v_mfma_f32_16x16x32_bf16 v[88:91], v[240:243], v[212:215], v[88:91]\n\t"
"v_mfma_f32_16x16x32_bf16 v[88:91], a[48:51], a[196:199], v[88:91]\n\t"
"s_waitcnt lgkmcnt(1)\n\t"
"s_barrier\n\t"
"v_mfma_f32_16x16x32_bf16 v[88:91], a[212:215], a[168:171], v[88:91]\n\t"
"ds_read_b128 v[196:199], v255 offset:18432\n\t"
"v_mfma_f32_16x16x32_bf16 v[88:91], a[236:239], a[188:191], v[88:91]\n\t"
"ds_read_b128 v[200:203], v255 offset:19968\n\t"
"v_cvt_pk_bf16_f32 v26, v56, v57\n\t"
"v_mfma_f32_16x16x32_bf16 v[88:91], a[60:63], a[192:195], v[88:91]\n\t"
"ds_read_b128 v[204:207], v255 offset:21504\n\t"
"v_cvt_pk_bf16_f32 v27, v58, v59\n\t"
"v_mfma_f32_16x16x32_bf16 v[124:127], a[16:19], a[200:203], v[124:127]\n\t"
"s_add_u32 s29, s23, 983040\n\t"
"ds_read_b128 a[168:171], v222 offset:60416\n\t"
"v_mfma_f32_16x16x32_bf16 v[124:127], a[20:23], a[204:207], v[124:127]\n\t"
"v_mfma_f32_16x16x32_bf16 v[124:127], a[120:123], v[208:211], v[124:127]\n\t"
"ds_write_b128 v245, v[24:27] offset:0\n\t"
"v_mfma_f32_16x16x32_bf16 v[124:127], a[240:243], a[172:175], v[124:127]\n\t"
"s_waitcnt lgkmcnt(4)\n\t"
"v_cvt_pk_bf16_f32 v88, v88, v89\n\t"
"v_cvt_pk_bf16_f32 v89, v90, v91\n\t"
"v_mfma_f32_16x16x32_bf16 v[124:127], a[64:67], a[180:183], v[124:127]\n\t"
"v_mfma_f32_16x16x32_bf16 v[124:127], a[68:71], a[184:187], v[124:127]\n\t"
"ds_write_b64 v246, v[88:89] offset:0\n\t"
"v_mfma_f32_16x16x32_bf16 v[156:159], a[24:27], a[180:183], v[156:159]\n\t"
"s_waitcnt lgkmcnt(4)\n\t"
"v_mfma_f32_16x16x32_bf16 v[156:159], a[28:31], a[184:187], v[156:159]\n\t"
"v_mfma_f32_16x16x32_bf16 v[156:159], a[72:75], a[172:175], v[156:159]\n\t"
"buffer_store_dwordx4 v[196:199], v248, s[16:19], s29 offen offset:0 sc1\n\t"
"v_mfma_f32_16x16x32_bf16 v[156:159], a[216:219], a[200:203], v[156:159]\n\t"
"s_waitcnt lgkmcnt(3)\n\t"
"v_cvt_pk_bf16_f32 v124, v124, v125\n\t"
"v_cvt_pk_bf16_f32 v125, v126, v127\n\t"
"v_mfma_f32_16x16x32_bf16 v[156:159], a[244:247], a[204:207], v[156:159]\n\t"
"v_mfma_f32_16x16x32_bf16 v[156:159], a[248:251], v[208:211], v[156:159]\n\t"
"buffer_store_dwordx4 v[200:203], v248, s[16:19], s29 offen offset:128 sc1\n\t"
"v_mfma_f32_16x16x32_bf16 v[188:191], a[36:39], a[204:207], v[188:191]\n\t"
"ds_read_b128 a[196:199], v222 offset:62208\n\t"
"v_mfma_f32_16x16x32_bf16 v[188:191], a[76:79], v[208:211], v[188:191]\n\t"
"v_mfma_f32_16x16x32_bf16 v[188:191], a[32:35], a[200:203], v[188:191]\n\t"
"buffer_store_dwordx4 v[204:207], v248, s[16:19], s29 offen offset:256 sc1\n\t"
"v_mfma_f32_16x16x32_bf16 v[188:191], a[0:3], a[172:175], v[188:191]\n\t"
"ds_read_b128 a[172:175], v222 offset:60672\n\t"
"v_cvt_pk_bf16_f32 v126, v156, v157\n\t"
"v_mfma_f32_16x16x32_bf16 v[188:191], a[4:7], a[180:183], v[188:191]\n\t"
"v_cvt_pk_bf16_f32 v127, v158, v159\n\t"
"v_mfma_f32_16x16x32_bf16 v[188:191], a[220:223], a[184:187], v[188:191]\n\t"
"ds_write_b128 v245, v[124:127] offset:9216\n\t"
"v_mfma_f32_16x16x32_bf16 v[28:31], a[140:143], a[176:179], v[28:31]\n\t"
"s_waitcnt lgkmcnt(5)\n\t"
"v_mfma_f32_16x16x32_bf16 v[28:31], a[208:211], v[212:215], v[28:31]\n\t"
"v_mfma_f32_16x16x32_bf16 v[28:31], a[228:231], v[216:219], v[28:31]\n\t"
"v_mfma_f32_16x16x32_bf16 v[28:31], a[232:235], a[188:191], v[28:31]\n\t"
"v_cvt_pk_bf16_f32 v188, v188, v189\n\t"
"v_cvt_pk_bf16_f32 v189, v190, v191\n\t"
"v_mfma_f32_16x16x32_bf16 v[28:31], a[104:107], a[192:195], v[28:31]\n\t"
"v_mfma_f32_16x16x32_bf16 v[28:31], a[112:115], a[168:171], v[28:31]\n\t"
"ds_write_b64 v246, v[188:189] offset:9216\n\t"
"v_mfma_f32_16x16x32_bf16 v[60:63], a[156:159], a[192:195], v[60:63]\n\t"
"v_mfma_f32_16x16x32_bf16 v[60:63], a[164:167], a[168:171], v[60:63]\n\t"
"v_mfma_f32_16x16x32_bf16 v[60:63], a[148:151], a[188:191], v[60:63]\n\t"
"v_mfma_f32_16x16x32_bf16 v[60:63], a[52:55], a[176:179], v[60:63]\n\t"
"v_cvt_pk_bf16_f32 v28, v28, v29\n\t"
"v_cvt_pk_bf16_f32 v29, v30, v31\n\t"
"v_mfma_f32_16x16x32_bf16 v[60:63], a[40:43], v[212:215], v[60:63]\n\t"
"v_mfma_f32_16x16x32_bf16 v[60:63], a[44:47], v[216:219], v[60:63]\n\t"
"v_mfma_f32_16x16x32_bf16 v[92:95], a[56:59], v[212:215], v[92:95]\n\t"
"v_mfma_f32_16x16x32_bf16 v[92:95], v[240:243], v[216:219], v[92:95]\n\t"
"v_mfma_f32_16x16x32_bf16 v[92:95], a[48:51], a[176:179], v[92:95]\n\t"
"s_waitcnt lgkmcnt(0)\n\t"
"s_barrier\n\t"
"v_mfma_f32_16x16x32_bf16 v[92:95], a[212:215], a[188:191], v[92:95]\n\t"
"ds_read_b128 v[196:199], v255 offset:0\n\t"
"v_mfma_f32_16x16x32_bf16 v[92:95], a[236:239], a[192:195], v[92:95]\n\t"
"ds_read_b128 v[200:203], v255 offset:1536\n\t"
"v_cvt_pk_bf16_f32 v30, v60, v61\n\t"
"v_mfma_f32_16x16x32_bf16 v[92:95], a[60:63], a[168:171], v[92:95]\n\t"
"ds_read_b128 v[204:207], v255 offset:3072\n\t"
"v_cvt_pk_bf16_f32 v31, v62, v63\n\t"
"v_mfma_f32_16x16x32_bf16 v[128:131], a[16:19], a[204:207], v[128:131]\n\t"
"s_add_u32 s29, s23, 1179648\n\t"
"v_mfma_f32_16x16x32_bf16 v[128:131], a[20:23], v[208:211], v[128:131]\n\t"
"v_mfma_f32_16x16x32_bf16 v[128:131], a[120:123], a[196:199], v[128:131]\n\t"
"ds_write_b128 v245, v[28:31] offset:18432\n\t"
"v_mfma_f32_16x16x32_bf16 v[128:131], a[240:243], a[180:183], v[128:131]\n\t"
"s_waitcnt lgkmcnt(3)\n\t"
"v_cvt_pk_bf16_f32 v92, v92, v93\n\t"
"v_cvt_pk_bf16_f32 v93, v94, v95\n\t"
"v_mfma_f32_16x16x32_bf16 v[128:131], a[64:67], a[184:187], v[128:131]\n\t"
"v_mfma_f32_16x16x32_bf16 v[128:131], a[68:71], a[172:175], v[128:131]\n\t"
"ds_write_b64 v246, v[92:93] offset:18432\n\t"
"v_mfma_f32_16x16x32_bf16 v[160:163], a[24:27], a[184:187], v[160:163]\n\t"
"s_waitcnt lgkmcnt(3)\n\t"
"v_mfma_f32_16x16x32_bf16 v[160:163], a[28:31], a[172:175], v[160:163]\n\t"
"v_mfma_f32_16x16x32_bf16 v[160:163], a[72:75], a[180:183], v[160:163]\n\t"
"buffer_store_dwordx4 v[196:199], v248, s[16:19], s29 offen offset:0 sc1\n\t"
"v_mfma_f32_16x16x32_bf16 v[160:163], a[216:219], a[204:207], v[160:163]\n\t"
"s_waitcnt lgkmcnt(2)\n\t"
"v_cvt_pk_bf16_f32 v128, v128, v129\n\t"
"v_cvt_pk_bf16_f32 v129, v130, v131\n\t"
"v_mfma_f32_16x16x32_bf16 v[160:163], a[244:247], v[208:211], v[160:163]\n\t"
"v_mfma_f32_16x16x32_bf16 v[160:163], a[248:251], a[196:199], v[160:163]\n\t"
"buffer_store_dwordx4 v[200:203], v248, s[16:19], s29 offen offset:128 sc1\n\t"
"v_mfma_f32_16x16x32_bf16 v[192:195], a[36:39], v[208:211], v[192:195]\n\t"
"v_mfma_f32_16x16x32_bf16 v[192:195], a[76:79], a[196:199], v[192:195]\n\t"
"v_mfma_f32_16x16x32_bf16 v[192:195], a[32:35], a[204:207], v[192:195]\n\t"
"buffer_store_dwordx4 v[204:207], v248, s[16:19], s29 offen offset:256 sc1\n\t"
"v_mfma_f32_16x16x32_bf16 v[192:195], a[0:3], a[180:183], v[192:195]\n\t"
"v_cvt_pk_bf16_f32 v130, v160, v161\n\t"
"v_cvt_pk_bf16_f32 v131, v162, v163\n\t"
"v_mfma_f32_16x16x32_bf16 v[192:195], a[4:7], a[184:187], v[192:195]\n\t"
"v_mfma_f32_16x16x32_bf16 v[192:195], a[220:223], a[172:175], v[192:195]\n\t"
"ds_write_b128 v245, v[128:131] offset:27648\n\t"
"s_nop 6\n\t"
"v_cvt_pk_bf16_f32 v192, v192, v193\n\t"
"v_cvt_pk_bf16_f32 v193, v194, v195\n\t"
"ds_write_b64 v246, v[192:193] offset:27648\n\t"
"s_waitcnt lgkmcnt(0)\n\t"
"s_barrier\n\t"
"ds_read_b128 v[196:199], v255 offset:18432\n\t"
"ds_read_b128 v[200:203], v255 offset:19968\n\t"
"ds_read_b128 v[208:211], v255 offset:21504\n\t"
"s_waitcnt lgkmcnt(2)\n\t"
"s_add_u32 s29, s23, 1376256\n\t"
"buffer_store_dwordx4 v[196:199], v248, s[16:19], s29 offen offset:0 sc1\n\t"
"s_waitcnt lgkmcnt(1)\n\t"
"buffer_store_dwordx4 v[200:203], v248, s[16:19], s29 offen offset:128 sc1\n\t"
"s_waitcnt lgkmcnt(0)\n\t"
"buffer_store_dwordx4 v[208:211], v248, s[16:19], s29 offen offset:256 sc1\n\t"
"s_waitcnt vmcnt(0)\n\t"
"Lwino_end:\n\t"
  : "+{v205}"(tid)
  : "s"((unsigned long long)__builtin_amdgcn_kernarg_segment_ptr()), "s"(blockIdx.x), "s"((unsigned long long)lane_tab), "s"((unsigned)(size_t)(__attribute__((address_space(3))) char*)smem), "s"((unsigned)(size_t)__builtin_amdgcn_dispatch_ptr())
  : "v0", "v1", "v2", "v3", "v4", "v5", "v6", "v7", "v8", "v9", "v10", "v11", "v12", "v13", "v14", "v15", "v16", "v17", "v18", "v19", "v20", "v21", "v22", "v23", "v24", "v25", "v26", "v27", "v28", "v29", "v30", "v31", "v32", "v33", "v34", "v35", "v36", "v37", "v38", "v39", "v40", "v41", "v42", "v43", "v44", "v45", "v46", "v47", "v48", "v49", "v50", "v51", "v52", "v53", "v54", "v55", "v56", "v57", "v58", "v59", "v60", "v61", "v62", "v63", "v64", "v65", "v66", "v67", "v68", "v69", "v70", "v71", "v72", "v73", "v74", "v75", "v76", "v77", "v78", "v79", "v80", "v81", "v82", "v83", "v84", "v85", "v86", "v87", "v88", "v89", "v90", "v91", "v92", "v93", "v94", "v95", "v96", "v97", "v98", "v99", "v100", "v101", "v102", "v103", "v104", "v105", "v106", "v107", "v108", "v109", "v110", "v111", "v112", "v113", "v114", "v115", "v116", "v117", "v118", "v119", "v120", "v121", "v122", "v123", "v124", "v125", "v126", "v127", "v128", "v129", "v130", "v131", "v132", "v133", "v134", "v135", "v136", "v137", "v138", "v139", "v140", "v141", "v142", "v143", "v144", "v145", "v146", "v147", "v148", "v149", "v150", "v151", "v152", "v153", "v154", "v155", "v156", "v157", "v158", "v159", "v160", "v161", "v162", "v163", "v164", "v165", "v166", "v167", "v168", "v169", "v170", "v171", "v172", "v173", "v174", "v175", "v176", "v177", "v178", "v179", "v180", "v181", "v182", "v183", "v184", "v185", "v186", "v187", "v188", "v189", "v190", "v191", "v192", "v193", "v194", "v195", "v196", "v197", "v198", "v199", "v200", "v201", "v202", "v203", "v204", "v206", "v207", "v208", "v209", "v210", "v211", "v212", "v213", "v214", "v215", "v216", "v217", "v218", "v219", "v220", "v221", "v222", "v223", "v224", "v225", "v226", "v227", "v228", "v229", "v230", "v231", "v232", "v233", "v234", "v235", "v236", "v237", "v238", "v239", "v240", "v241", "v242", "v243", "v244", "v245", "v246", "v247", "v248", "v249", "v250", "v251", "v252", "v253", "v254", "v255", "a0", "a1", "a2", "a3", "a4", "a5", "a6", "a7", "a8", "a9", "a10", "a11", "a12", "a13", "a14", "a15", "a16", "a17", "a18", "a19", "a20", "a21", "a22", "a23", "a24", "a25", "a26", "a27", "a28", "a29", "a30", "a31", "a32", "a33", "a34", "a35", "a36", "a37", "a38", "a39", "a40", "a41", "a42", "a43", "a44", "a45", "a46", "a47", "a48", "a49", "a50", "a51", "a52", "a53", "a54", "a55", "a56", "a57", "a58", "a59", "a60", "a61", "a62", "a63", "a64", "a65", "a66", "a67", "a68", "a69", "a70", "a71", "a72", "a73", "a74", "a75", "a76", "a77", "a78", "a79", "a80", "a81", "a82", "a83", "a84", "a85", "a86", "a87", "a88", "a89", "a90", "a91", "a92", "a93", "a94", "a95", "a96", "a97", "a98", "a99", "a100", "a101", "a102", "a103", "a104", "a105", "a106", "a107", "a108", "a109", "a110", "a111", "a112", "a113", "a114", "a115", "a116", "a117", "a118", "a119", "a120", "a121", "a122", "a123", "a124", "a125", "a126", "a127", "a128", "a129", "a130", "a131", "a132", "a133", "a134", "a135", "a136", "a137", "a138", "a139", "a140", "a141", "a142", "a143", "a144", "a145", "a146", "a147", "a148", "a149", "a150", "a151", "a152", "a153", "a154", "a155", "a156", "a157", "a158", "a159", "a160", "a161", "a162", "a163", "a164", "a165", "a166", "a167", "a168", "a169", "a170", "a171", "a172", "a173", "a174", "a175", "a176", "a177", "a178", "a179", "a180", "a181", "a182", "a183", "a184", "a185", "a186", "a187", "a188", "a189", "a190", "a191", "a192", "a193", "a194", "a195", "a196", "a197", "a198", "a199", "a200", "a201", "a202", "a203", "a204", "a205", "a206", "a207", "a208", "a209", "a210", "a211", "a212", "a213", "a214", "a215", "a216", "a217", "a218", "a219", "a220", "a221", "a222", "a223", "a224", "a225", "a226", "a227", "a228", "a229", "a230", "a231", "a232", "a233", "a234", "a235", "a236", "a237", "a238", "a239", "a240", "a241", "a242", "a243", "a244", "a245", "a246", "a247", "a248", "a249", "a250", "a251", "a252", "a253", "a254", "a255", "s8", "s9", "s10", "s11", "s12", "s13", "s14", "s15", "s16", "s17", "s18", "s19", "s20", "s21", "s22", "s23", "s24", "s25", "s26", "s27", "s28", "s29", "s30", "s31", "s32", "s33", "s34", "s35", "s36", "s37", "s38", "s39", "s40", "s41", "s42", "s43", "s44", "s45", "s46", "s47", "s48", "s49", "s50", "s51", "s52", "s53", "s54", "s55", "s56", "s57", "s58", "s59", "s60", "s61", "s62", "s63", "s64", "s65", "s66", "s67", "s68", "s69", "s70", "s71", "s72", "s73", "s74", "s75", "s76", "s77", "s78", "s79", "s80", "s81", "s82", "s83", "scc", "vcc", "memory");
}

extern "C" void kernel_launch(void* const* d_in, const int* in_sizes, int n_in, void* d_out, int out_size, void* d_ws,
                              size_t ws_size, hipStream_t stream) {
  wino_main_kernel<<<256, 256, 0, stream>>>((const char*)d_in[0], (char*)d_ws, (char*)d_out, (const char*)d_in[1],
                                            ws_size >= (36ull << 20) ? ~0ull : 0ull);
}
```

```cpp
#include <hip/hip_runtime.h>
#include <hip/hip_bf16.h>
#include <stdint.h>

#define IMG_W 512
#define CH 192
#define PXB 384
#define ROWB (IMG_W * PXB)
#define U_BYTES 1769472

__device__ const __attribute__((aligned(1024))) unsigned lane_tab[6144] = {
  0x0,0x0,0x0,0x0,0x0,0x0,0x0,0x0,0x0,0x0,0x0,0x0,0x0,0x0,0x0,0x0,
  0x0,0x0,0x0,0x0,0x0,0x0,0x0,0x0,0x0,0x0,0x0,0x0,0x0,0x0,0x0,0x0,
  0x0,0x0,0x0,0x0,0x0,0x0,0x0,0x0,0x0,0x0,0x0,0x0,0x0,0x0,0x0,0x0,
  0x0,0x0,0x0,0x0,0x0,0x0,0x0,0x0,0x0,0x0,0x0,0x0,0x0,0x0,0x0,0x0,
  0x0,0x0,0x0,0x0,0x0,0x0,0x0,0x0,0x0,0x0,0x0,0x0,0x0,0x0,0x0,0x0,
  0x0,0x0,0x0,0x0,0x0,0x0,0x0,0x0,0x0,0x0,0x0,0x0,0x0,0x0,0x0,0x0,
  0x0,0x0,0x0,0x0,0x0,0x0,0x0,0x0,0x0,0x0,0x0,0x0,0x0,0x0,0x0,0x0,
  0x0,0x0,0x0,0x0,0x0,0x0,0x0,0x0,0x0,0x0,0x0,0x0,0x0,0x0,0x0,0x0,
  0x0,0x0,0x0,0x0,0x0,0x0,0x0,0x0,0x0,0x0,0x0,0x0,0x0,0x0,0x0,0x0,
  0x0,0x0,0x0,0x0,0x0,0x0,0x0,0x0,0x0,0x0,0x0,0x0,0x0,0x0,0x0,0x0,
  0x0,0x0,0x0,0x0,0x0,0x0,0x0,0x0,0x0,0x0,0x0,0x0,0x0,0x0,0x0,0x0,
  0x0,0x0,0x0,0x0,0x0,0x0,0x0,0x0,0x0,0x0,0x0,0x0,0x0,0x0,0x0,0x0,
  0x0,0x0,0x0,0x0,0x0,0x0,0x0,0x0,0x0,0x0,0x0,0x0,0x0,0x0,0x0,0x0,
  0x0,0x0,0x0,0x0,0x0,0x0,0x0,0x0,0x0,0x0,0x0,0x0,0x0,0x0,0x0,0x0,
  0x0,0x0,0x0,0x0,0x0,0x0,0x0,0x0,0x0,0x0,0x0,0x0,0x0,0x0,0x0,0x0,
  0x0,0x0,0x0,0x0,0x0,0x0,0x0,0x0,0x0,0x0,0x0,0x0,0x0,0x0,0x0,0x0,
  0x0,0xc042,0x0,0x0,0x0,0xc0420000,0x0,0x0,0x0,0x0,0x0,0xc042,0x0,0x0,0x0,0xc0420000,
  0x3f903f90,0x0,0x0,0x0,0x3f900000,0x0,0x3f90,0x0,0x0,0x0,0x3f903f90,0x0,0x0,0x0,0x3f900000,0x0,
  0x0,0x0,0x0,0x0,0x0,0x0,0x0,0x0,0x0,0x0,0x0,0x0,0x0,0x0,0x0,0x0,
  0x0,0x0,0x0,0x0,0x0,0x0,0x0,0x0,0x0,0x0,0x0,0x0,0x0,0x0,0x0,0x0,
  0x0,0x0,0x3f90,0x0,0x0,0x0,0x0,0x0,0x0,0x0,0x0,0x0,0x0,0x0,0x0,0x0,
  0x0,0x0,0x0,0x0,0xc0420000,0x0,0x0,0x0,0x0,0x0,0x0,0x0,0x0,0x0,0xc0420000,0x0,
  0x0,0x3f90,0x0,0x0,0x0,0x3f903f90,0x0,0x0,0x0,0x3f900000,0x0,0x3f90,0x0,0x0,0x0,0x3f903f90,
  0x0,0x3f90,0x0,0x0,0x0,0x3f903f90,0x0,0x0,0x0,0x3f900000,0x0,0x3f90,0x0,0x0,0x0,0x3f903f90,
  0x0,0x0,0x0,0x0,0x0,0x0,0x0,0x0,0x0,0x0,0x0,0x0,0x0,0x0,0x0,0x0,
  0x0,0x0,0x0,0x0,0x0,0x0,0x0,0x0,0x0,0x0,0x0,0x0,0x0,0x0,0x3f90,0x0,
  0x0,0xc042,0x3f900000,0x0,0x0,0xc0420000,0x0,0x0,0x0,0x0,0x0,0xc042,0x0,0x0,0x0,0xc0420000,
  0x0,0xc042,0x3f900000,0x0,0x0,0xc0420000,0x0,0x0,0x0,0x0,0x0,0xc042,0x0,0x0,0x0,0xc0420000,
  0x3f900000,0x0,0x0,0x0,0x3f900000,0x3f90,0x0,0x0,0x0,0x3f90,0x3f900000,0x0,0x0,0x0,0x3f900000,0x3f90,
  0xc042,0x0,0x0,0x0,0x0,0x0,0x0,0x0,0x0,0x0,0xc042,0x0,0x0,0x0,0x0,0x0,
  0x0,0x0,0x0,0x0,0x0,0x0,0x0,0x0,0x0,0x0,0x0,0x0,0x0,0x0,0x0,0x0,
  0x0,0x0,0x0,0x0,0x0,0x0,0x0,0x0,0x0,0x0,0x0,0x0,0x0,0x0,0x0,0x0,
  0xa8000000,0xae0,0xa800,0x60,0xa8080040,0x400af1,0xa920,0x600000,0xa8100090,0x900a82,0xa808,0x6000,0xa81800d0,0xd00a93,0xa928,0x60000000,
  0xa9200120,0x24000ae4,0xa840,0x40004,0xa9280160,0x24400af5,0xa960,0x40400,0xa93001b0,0x24900a86,0xa848,0x4000400,0xa93801f0,0x24d00a97,0xa968,0x4000000,
  0xa8401000,0x2000ae1,0xa880,0x0,0xa8481040,0x2400af0,0xa9a0,0x0,0xa8501090,0x2900a83,0xa888,0x0,0xa85810d0,0x2d00a92,0xa9a8,0x0,
  0xa9601120,0x2000ae5,0xa8c0,0x0,0xa9681160,0x2400af4,0xa9e0,0x0,0xa97011b0,0x2900a87,0xa8c8,0x0,0xa97811f0,0x2d00a96,0xa9e8,0x0,
  0xea800010,0x100ae2,0xa810,0x400,0xea880050,0x500af3,0xa930,0x0,0xea900080,0x800a80,0xa818,0x0,0xea9800c0,0xc00a91,0xa938,0x0,
  0xa9a00130,0x24100ae6,0xa850,0x0,0xa9a80170,0x24500af7,0xa970,0x60000,0xa9b001a0,0x24800a84,0xa858,0x0,0xa9b801e0,0x24c00a95,0xa978,0x6000000,
  0xc0c01010,0x2100ae3,0xa890,0x40,0xc0c81050,0x2500af2,0xa9b0,0x400040,0xc0d01080,0x2800a81,0xa898,0x404000,0xc0d810c0,0x2c00a90,0xa9b8,0x40004000,
  0xc1e01130,0x2100ae7,0xa8d0,0x40,0xc1e81170,0x2500af6,0xa9f0,0x400040,0xc1f011a0,0x2800a85,0xa8d8,0x404000,0xc1f811e0,0x2c00a94,0xa9f8,0x40004000,
  0xae000020,0x200ae9,0xae00,0x0,0xae080060,0x600af8,0xaf20,0x0,0xae1000b0,0xb00a8b,0xae08,0x0,0xae1800f0,0xf00a9a,0xaf28,0x0,
  0xeb200100,0x24200aed,0xae40,0x0,0xeb280140,0x24600afc,0xaf60,0x0,0xeb300190,0x24b00a8f,0xae48,0x0,0xeb3801d0,0x24f00a9e,0xaf68,0x400,
  0xc0401020,0x2200ae8,0xae80,0x4000060,0xc0481060,0x2600af9,0xafa0,0x600000,0xc05010b0,0x2b00a8a,0xae88,0x6000,0xc05810f0,0x2f00a9b,0xafa8,0x60000000,
  0xc1601100,0x2200aec,0xaec0,0x4000060,0xc1681140,0x2600afd,0xafe0,0x600000,0xc1701190,0x2b00a8e,0xaec8,0x6000,0xc17811d0,0x2f00a9f,0xafe8,0x60000000,
  0xa8800030,0x300aeb,0xae10,0x40000,0xa8880070,0x700afa,0xaf30,0x40040,0xa89000a0,0xa00a89,0xae18,0x4000040,0xa89800e0,0xe00a98,0xaf38,0x4004000,
  0xa8c00110,0x24300aef,0xae50,0x6,0xa8c80150,0x24700afe,0xaf70,0x0,0xa8d00180,0x24a00a8d,0xae58,0x600,0xa8d801c0,0x24e00a9c,0xaf78,0x0,
  0xa9e01030,0x2300aea,0xae90,0x0,0xa9e81070,0x2700afb,0xafb0,0x0,0xa9f010a0,0x2a00a88,0xae98,0x0,0xa9f810e0,0x2e00a99,0xafb8,0x0,
  0xae001110,0x2300aee,0xaed0,0x0,0xae081150,0x2700aff,0xaff0,0x0,0xae101180,0x2a00a8c,0xaed8,0x0,0xae1811c0,0x2e00a9d,0xaff8,0x0,
  0x0,0x0,0x0,0x0,0x0,0x0,0x0,0x0,0x0,0x0,0x0,0x0,0x0,0x0,0x0,0x0,
  0x0,0x0,0x0,0x0,0x0,0x0,0x0,0x0,0x0,0x0,0x0,0x0,0x0,0x0,0x0,0x0,
  0x0,0x0,0x0,0x0,0x0,0x0,0x0,0x0,0x0,0x0,0x0,0x0,0x0,0x0,0x0,0x0,
  0x0,0x0,0x0,0x0,0x0,0x0,0x0,0x0,0x0,0x0,0x0,0x0,0x0,0x0,0x0,0x0,
  0x0,0x0,0x0,0x0,0x0,0x0,0x0,0x0,0x0,0x0,0x0,0x0,0x0,0x0,0x0,0x0,
  0x0,0x0,0x0,0x0,0x0,0x0,0x0,0x0,0x0,0x0,0x0,0x0,0x0,0x0,0x0,0x0,
  0x0,0x0,0x0,0x0,0x0,0x0,0x0,0x0,0x0,0x0,0x0,0x0,0x0,0x0,0x0,0x0,
  0x0,0x0,0x0,0x0,0x0,0x0,0x0,0x0,0x0,0x0,0x0,0x0,0x0,0x0,0x0,0x0,
  0x0,0x0,0x0,0x0,0x0,0x0,0x0,0x0,0x0,0x0,0x0,0x0,0x0,0x0,0x0,0x0,
  0x0,0x0,0x0,0x0,0x0,0x0,0x0,0x0,0x0,0x0,0x0,0x0,0x0,0x0,0x0,0x0,
  0x0,0x0,0x0,0x0,0x0,0x0,0x0,0x0,0x0,0x0,0x0,0x0,0x0,0x0,0x0,0x0,
  0x0,0x0,0x0,0x0,0x0,0x0,0x0,0x0,0x0,0x0,0x0,0x0,0x0,0x0,0x0,0x0,
  0x0,0x0,0x0,0x0,0x0,0x0,0x0,0x0,0x0,0x0,0x0,0x0,0x0,0x0,0x0,0x0,
  0x0,0x0,0x0,0x0,0x0,0x0,0x0,0x0,0x0,0x0,0x0,0x0,0x0,0x0,0x0,0x0,
  0x0,0x0,0x0,0x0,0x0,0x0,0x0,0x0,0x0,0x0,0x0,0x0,0x0,0x0,0x0,0x0,
  0x0,0x0,0x0,0x0,0x0,0x0,0x0,0x0,0x0,0x0,0x0,0x0,0x0,0x0,0x0,0x0,
  0xc0420000,0x0,0x0,0x0,0x0,0x0,0x0,0x0,0x0,0x0,0xc0420000,0x0,0x0,0x0,0x0,0x0,
  0x3f90,0x0,0x0,0x0,0x0,0x0,0x3f90,0x0,0x0,0x0,0x3f90,0x0,0x0,0x0,0x0,0x0,
  0x0,0xc042,0x0,0x0,0x0,0xc0420000,0x0,0x0,0x0,0x0,0x0,0xc042,0x0,0x0,0x0,0xc0420000,
  0x0,0xc042,0x0,0x0,0x0,0xc0420000,0x0,0x0,0x0,0x0,0x0,0xc042,0x0,0x0,0x0,0xc0420000,
  0x0,0x0,0x0,0x3f90,0x0,0x0,0x0,0x0,0x0,0x0,0x0,0x0,0x0,0x0,0x0,0x0,
  0x0,0x0,0x0,0x0,0xc042,0x0,0x0,0x0,0x0,0x0,0x0,0x0,0x0,0x0,0xc042,0x0,
  0x3f900000,0x0,0x0,0x3f900000,0x3f900000,0x0,0x0,0x0,0x0,0x0,0x3f900000,0x0,0x0,0x0,0x3f900000,0x0,
  0x3f900000,0x0,0x0,0x3f900000,0x3f900000,0x0,0x0,0x0,0x0,0x0,0x3f900000,0x0,0x0,0x0,0x3f900000,0x0,
  0x0,0x0,0x0,0x0,0x0,0xc042,0x0,0x0,0x0,0x0,0x0,0x0,0x0,0x0,0x0,0xc042,
  0x3f90,0xc0420000,0x0,0x0,0x3f90,0x0,0x0,0x0,0x0,0x0,0x3f90,0xc0420000,0x0,0x0,0x3f903f90,0x0,
  0x0,0x0,0x0,0x0,0x0,0x0,0x0,0x0,0x0,0x0,0x0,0x0,0x0,0x0,0x0,0x0,
  0x0,0x0,0x0,0x0,0x0,0x0,0x0,0x0,0x0,0x0,0x0,0x0,0x0,0x0,0x0,0x0,
  0x3f90,0x0,0x0,0x0,0x3f903f90,0x0,0x0,0x0,0x3f900000,0x0,0x3f90,0x0,0x0,0x0,0x3f903f90,0x0,
  0x0,0x0,0x0,0x0,0x0,0x0,0x0,0x0,0x0,0x0,0x0,0x0,0x0,0x0,0x0,0x0,
  0x0,0x0,0x0,0x0,0x0,0x3f90,0x0,0x0,0x0,0x3f90,0x0,0x0,0x0,0x0,0x0,0x3f90,
  0x0,0x0,0x0,0x0,0x0,0x3f90,0x0,0x0,0x0,0x3f90,0x0,0x0,0x0,0x0,0x0,0x3f90,
  0xb4000000,0x1200ba2,0xb400,0x60000,0xb4080040,0x1600bb3,0xb520,0x0,0xb4100090,0x1b00b40,0xb408,0x6000000,0xb41800d0,0x1f00b51,0xb528,0x0,
  0xb4400120,0x25200ba6,0xb440,0x4,0xb4480160,0x25600bb7,0xb560,0x400,0xb45001b0,0x25b00b44,0xb448,0x400,0xb45801f0,0x25f00b55,0xb568,0x0,
  0xcc401000,0x3200ba3,0xb480,0x60,0xcc481040,0x3600bb2,0xb5a0,0x600000,0xcc501090,0x3b00b41,0xb488,0x6000,0xcc5810d0,0x3f00b50,0xb5a8,0x60000000,
  0xcd601120,0x3200ba7,0xb4c0,0x60,0xcd681160,0x3600bb6,0xb5e0,0x600000,0xcd7011b0,0x3b00b45,0xb4c8,0x6000,0xcd7811f0,0x3f00b54,0xb5e8,0x60000000,
  0xb5a00010,0x1300ba0,0xb410,0x4000,0xb5a80050,0x1700bb1,0xb530,0x0,0xb5b00080,0x1a00b42,0xb418,0x0,0xb5b800c0,0x1e00b53,0xb538,0x0,
  0xccc00130,0x25300ba4,0xb450,0x0,0xccc80170,0x25700bb5,0xb570,0x6,0xccd001a0,0x25a00b46,0xb458,0x0,0xccd801e0,0x25e00b57,0xb578,0x600,
  0xa9e01010,0x3300ba1,0xb490,0x40040000,0xa9e81050,0x3700bb0,0xb5b0,0x40000,0xa9f01080,0x3a00b43,0xb498,0x4000000,0xa9f810c0,0x3e00b52,0xb5b8,0x4000000,
  0xc1e01130,0x3300ba5,0xb4d0,0x40040000,0xc1e81170,0x3700bb4,0xb5f0,0x40000,0xc1f011a0,0x3a00b47,0xb4d8,0x4000000,0xc1f811e0,0x3e00b56,0xb5f8,0x4000000,
  0xb5200020,0x1000bab,0xba00,0x0,0xb5280060,0x1400bba,0xbb20,0x60,0xb53000b0,0x1900b49,0xba08,0x0,0xb53800f0,0x1d00b58,0xbb28,0x6000,
  0xea400100,0x25000baf,0xba40,0x600004,0xea480140,0x25400bbe,0xbb60,0x4,0xea500190,0x25900b4d,0xba48,0x60000400,0xea5801d0,0x25d00b5c,0xbb68,0x4000400,
  0xb5601020,0x3000baa,0xba80,0x0,0xb5681060,0x3400bbb,0xbba0,0x0,0xb57010b0,0x3900b48,0xba88,0x0,0xb57810f0,0x3d00b59,0xbba8,0x0,
  0xb4801100,0x3000bae,0xbac0,0x0,0xb4881140,0x3400bbf,0xbbe0,0x0,0xb4901190,0x3900b4c,0xbac8,0x0,0xb49811d0,0x3d00b5d,0xbbe8,0x0,
  0xb4c00030,0x1100ba9,0xba10,0x4,0xb4c80070,0x1500bb8,0xbb30,0x40004,0xb4d000a0,0x1800b4b,0xba18,0x40400,0xb4d800e0,0x1c00b5a,0xbb38,0x4000400,
  0xb5e00110,0x25100bad,0xba50,0x0,0xb5e80150,0x25500bbc,0xbb70,0x0,0xb5f00180,0x25800b4f,0xba58,0x0,0xb5f801c0,0x25c00b5e,0xbb78,0x0,
  0xcde01030,0x3100ba8,0xba90,0x0,0xcde81070,0x3500bb9,0xbbb0,0x40,0xcdf010a0,0x3800b4a,0xba98,0x40,0xcdf810e0,0x3c00b5b,0xbbb8,0x4000,
  0xb5201110,0x3100bac,0xbad0,0x0,0xb5281150,0x3500bbd,0xbbf0,0x40,0xb5301180,0x3800b4e,0xbad8,0x40,0xb53811c0,0x3c00b5f,0xbbf8,0x4000,
  0x0,0x0,0x0,0x0,0x0,0x0,0x0,0x0,0x0,0x0,0x0,0x0,0x0,0x0,0x0,0x0,
  0x0,0x0,0x0,0x0,0x0,0x0,0x0,0x0,0x0,0x0,0x0,0x0,0x0,0x0,0x0,0x0,
  0x0,0x0,0x0,0x0,0x0,0x0,0x0,0x0,0x0,0x0,0x0,0x0,0x0,0x0,0x0,0x0,
  0x0,0x0,0x0,0x0,0x0,0x0,0x0,0x0,0x0,0x0,0x0,0x0,0x0,0x0,0x0,0x0,
  0x0,0x0,0x0,0x0,0x0,0x0,0x0,0x0,0x0,0x0,0x0,0x0,0x0,0x0,0x0,0x0,
  0x0,0x0,0x0,0x0,0x0,0x0,0x0,0x0,0x0,0x0,0x0,0x0,0x0,0x0,0x0,0x0,
  0x0,0x0,0x0,0x0,0x0,0x0,0x0,0x0,0x0,0x0,0x0,0x0,0x0,0x0,0x0,0x0,
  0x0,0x0,0x0,0x0,0x0,0x0,0x0,0x0,0x0,0x0,0x0,0x0,0x0,0x0,0x0,0x0,
  0x0,0x0,0x0,0x0,0x0,0x0,0x0,0x0,0x0,0x0,0x0,0x0,0x0,0x0,0x0,0x0,
  0x0,0x0,0x0,0x0,0x0,0x0,0x0,0x0,0x0,0x0,0x0,0x0,0x0,0x0,0x0,0x0,
  0x0,0x0,0x0,0x0,0x0,0x0,0x0,0x0,0x0,0x0,0x0,0x0,0x0,0x0,0x0,0x0,
  0x0,0x0,0x0,0x0,0x0,0x0,0x0,0x0,0x0,0x0,0x0,0x0,0x0,0x0,0x0,0x0,
  0x0,0x0,0x0,0x0,0x0,0x0,0x0,0x0,0x0,0x0,0x0,0x0,0x0,0x0,0x0,0x0,
  0x0,0x0,0x0,0x0,0x0,0x0,0x0,0x0,0x0,0x0,0x0,0x0,0x0,0x0,0x0,0x0,
  0x0,0x0,0x0,0x0,0x0,0x0,0x0,0x0,0x0,0x0,0x0,0x0,0x0,0x0,0x0,0x0,
  0x0,0x0,0x0,0x0,0x0,0x0,0x0,0x0,0x0,0x0,0x0,0x0,0x0,0x0,0x0,0x0,
  0x3f90,0x3f90,0x0,0x0,0x0,0x3f90,0x3f90,0x0,0x0,0x0,0x3f90,0x3f90,0x0,0x0,0x0,0x3f90,
  0x0,0xc0420000,0x0,0x0,0x0,0x0,0x0,0x0,0x0,0x0,0x0,0xc0420000,0x0,0x0,0x0,0x0,
  0x3f900000,0x0,0x0,0x0,0x0,0x0,0x3f900000,0x0,0x0,0x0,0x3f900000,0x0,0x0,0x0,0x0,0x0,
  0x3f900000,0x0,0x0,0x0,0x0,0x0,0x3f900000,0x0,0x0,0x0,0x3f900000,0x0,0x0,0x0,0x0,0x0,
  0xc042,0x0,0x0,0x0,0xc0420000,0x0,0x0,0x0,0x0,0x0,0xc042,0x0,0x0,0x0,0xc0420000,0x0,
  0x0,0x0,0x0,0x0,0x0,0x3f900000,0x0,0x0,0x0,0x3f900000,0x0,0x0,0x0,0x0,0x0,0x3f900000,
  0x0,0x0,0x0,0x0,0x0,0xc042,0x0,0x0,0x0,0x0,0x0,0x0,0x0,0x0,0x0,0xc042,
  0x0,0x0,0x0,0x0,0x0,0xc042,0x0,0x0,0x0,0x0,0x0,0x0,0x0,0x0,0x0,0xc042,
  0x0,0x0,0x0,0x0,0x0,0x0,0x0,0x0,0x0,0x0,0x0,0x0,0x0,0x0,0x3f90,0x0,
  0x0,0x0,0x0,0x0,0x0,0xc0420000,0x0,0x0,0x0,0x0,0x0,0x0,0x0,0x0,0x0,0xc0420000,
  0x0,0x3f90,0x0,0x0,0x0,0x3f90,0x0,0x0,0x0,0x0,0x0,0x3f90,0x0,0x0,0x3f900000,0x3f90,
  0x0,0x3f90,0x0,0x0,0x0,0x3f90,0x0,0x0,0x0,0x0,0x0,0x3f90,0x0,0x0,0x3f900000,0x3f90,
  0x0,0x0,0x0,0x0,0x0,0x0,0x0,0x0,0x0,0x0,0x0,0x0,0x0,0x0,0x0,0x0,
  0x0,0x3f90,0x3f900000,0x0,0x0,0x3f90,0x0,0x0,0x0,0x0,0x0,0x3f90,0x0,0x0,0x0,0x3f90,
  0xc042,0x0,0x0,0x0,0x0,0x0,0x0,0x0,0x0,0x0,0xc042,0x0,0x0,0x0,0x0,0x0,
  0xc042,0x0,0x0,0x0,0x0,0x0,0x0,0x0,0x0,0x0,0xc042,0x0,0x0,0x0,0x0,0x0,
  0xc0000000,0x26001980,0x19200,0x44,0xc0080040,0x26401991,0x19320,0x440,0xc0100090,0x26901922,0x19208,0x4400,0xc01800d0,0x26d01933,0x19328,0x4000,
  0xd8000120,0x4001984,0x19240,0x600000,0xd8080160,0x4401995,0x19360,0x0,0xd81001b0,0x4901926,0x19248,0x60000000,0xd81801f0,0x4d01937,0x19368,0x0,
  0xc1201000,0x28001981,0x19280,0x40000,0xc1281040,0x28401990,0x193a0,0x4000000,0xc1301090,0x28901923,0x19288,0x4000000,0xc13810d0,0x28d01932,0x193a8,0x0,
  0xd8401120,0x28001985,0x192c0,0x40000,0xd8481160,0x28401994,0x193e0,0x4000000,0xd85011b0,0x28901927,0x192c8,0x4000000,0xd85811f0,0x28d01936,0x193e8,0x0,
  0xc0800010,0x26101982,0x19210,0x6,0xc0880050,0x26501993,0x19330,0x60000,0xc0900080,0x26801920,0x19218,0x600,0xc09800c0,0x26c01931,0x19338,0x6000000,
  0xc1a00130,0x4101986,0x19250,0x0,0xc1a80170,0x4501997,0x19370,0x400000,0xc1b001a0,0x4801924,0x19258,0x400000,0xc1b801e0,0x4c01935,0x19378,0x40000000,
  0xd9a01010,0x28101983,0x19290,0x0,0xd9a81050,0x28501992,0x193b0,0x60,0xd9b01080,0x28801921,0x19298,0x0,0xd9b810c0,0x28c01930,0x193b8,0x6000,
  0xd9e01130,0x28101987,0x192d0,0x0,0xd9e81170,0x28501996,0x193f0,0x60,0xd9f011a0,0x28801925,0x192d8,0x0,0xd9f811e0,0x28c01934,0x193f8,0x6000,
  0xc6000020,0x26201989,0x19800,0x0,0xc6080060,0x26601998,0x19920,0x0,0xc61000b0,0x26b0192b,0x19808,0x0,0xc61800f0,0x26f0193a,0x19928,0x400,
  0xde000100,0x420198d,0x19840,0x0,0xde080140,0x460199c,0x19960,0x600000,0xde100190,0x4b0192f,0x19848,0x0,0xde1801d0,0x4f0193e,0x19968,0x60000000,
  0xd9201020,0x28201988,0x19880,0x40,0xd9281060,0x28601999,0x199a0,0x40,0xd93010b0,0x28b0192a,0x19888,0x4000,0xd93810f0,0x28f0193b,0x199a8,0x4004000,
  0xd9601100,0x2820198c,0x198c0,0x40,0xd9681140,0x2860199d,0x199e0,0x40,0xd9701190,0x28b0192e,0x198c8,0x4000,0xd97811d0,0x28f0193f,0x199e8,0x4004000,
  0xd8800030,0x2630198b,0x19810,0x0,0xd8880070,0x2670199a,0x19930,0x0,0xd89000a0,0x26a01929,0x19818,0x0,0xd89800e0,0x26e01938,0x19938,0x0,
  0xeba00110,0x430198f,0x19850,0x4000040,0xeba80150,0x470199e,0x19970,0x40,0xebb00180,0x4a0192d,0x19858,0x4000,0xebb801c0,0x4e0193c,0x19978,0x4000,
  0xd8c01030,0x2830198a,0x19890,0x6,0xd8c81070,0x2870199b,0x199b0,0x0,0xd8d010a0,0x28a01928,0x19898,0x600,0xd8d810e0,0x28e01939,0x199b8,0x0,
  0xc6001110,0x2830198e,0x198d0,0x6,0xc6081150,0x2870199f,0x199f0,0x0,0xc6101180,0x28a0192c,0x198d8,0x600,0xc61811c0,0x28e0193d,0x199f8,0x0,
  0x0,0x0,0x0,0x0,0x0,0x0,0x0,0x0,0x0,0x0,0x0,0x0,0x0,0x0,0x0,0x0,
  0x0,0x0,0x0,0x0,0x0,0x0,0x0,0x0,0x0,0x0,0x0,0x0,0x0,0x0,0x0,0x0,
  0x0,0x0,0x0,0x0,0x0,0x0,0x0,0x0,0x0,0x0,0x0,0x0,0x0,0x0,0x0,0x0,
  0x0,0x0,0x0,0x0,0x0,0x0,0x0,0x0,0x0,0x0,0x0,0x0,0x0,0x0,0x0,0x0,
  0x0,0x0,0x0,0x0,0x0,0x0,0x0,0x0,0x0,0x0,0x0,0x0,0x0,0x0,0x0,0x0,
  0x0,0x0,0x0,0x0,0x0,0x0,0x0,0x0,0x0,0x0,0x0,0x0,0x0,0x0,0x0,0x0,
  0x0,0x0,0x0,0x0,0x0,0x0,0x0,0x0,0x0,0x0,0x0,0x0,0x0,0x0,0x0,0x0,
  0x0,0x0,0x0,0x0,0x0,0x0,0x0,0x0,0x0,0x0,0x0,0x0,0x0,0x0,0x0,0x0,
  0x0,0x0,0x0,0x0,0x0,0x0,0x0,0x0,0x0,0x0,0x0,0x0,0x0,0x0,0x0,0x0,
  0x0,0x0,0x0,0x0,0x0,0x0,0x0,0x0,0x0,0x0,0x0,0x0,0x0,0x0,0x0,0x0,
  0x0,0x0,0x0,0x0,0x0,0x0,0x0,0x0,0x0,0x0,0x0,0x0,0x0,0x0,0x0,0x0,
  0x0,0x0,0x0,0x0,0x0,0x0,0x0,0x0,0x0,0x0,0x0,0x0,0x0,0x0,0x0,0x0,
  0x0,0x0,0x0,0x0,0x0,0x0,0x0,0x0,0x0,0x0,0x0,0x0,0x0,0x0,0x0,0x0,
  0x0,0x0,0x0,0x0,0x0,0x0,0x0,0x0,0x0,0x0,0x0,0x0,0x0,0x0,0x0,0x0,
  0x0,0x0,0x0,0x0,0x0,0x0,0x0,0x0,0x0,0x0,0x0,0x0,0x0,0x0,0x0,0x0,
  0x0,0x0,0x0,0x0,0x0,0x0,0x0,0x0,0x0,0x0,0x0,0x0,0x0,0x0,0x0,0x0,
  0x3f903f90,0x0,0x0,0x0,0x3f900000,0x0,0x3f90,0x0,0x0,0x0,0x3f903f90,0x0,0x0,0x0,0x3f900000,0x0,
  0x0,0xc042,0x0,0x0,0x0,0x0,0x0,0x0,0x0,0x0,0x0,0xc042,0x0,0x0,0x0,0x0,
  0x0,0x0,0x0,0x0,0x0,0x0,0x0,0x0,0x0,0x0,0x0,0x0,0x0,0x0,0x0,0x3f900000,
  0x0,0x0,0x0,0x0,0x0,0x0,0x0,0x0,0x0,0x0,0x0,0x0,0x0,0x0,0x0,0x3f900000,
  0x0,0x0,0x0,0x0,0x0,0x0,0x0,0x0,0x0,0x0,0x0,0x0,0x0,0x0,0x0,0x0,
  0x0,0x3f90,0x0,0x3f900000,0x0,0x3f90,0x0,0x0,0x0,0x0,0x0,0x3f90,0x0,0x0,0x0,0x3f90,
  0xc042,0x0,0x0,0x0,0xc0420000,0x0,0x0,0x0,0x0,0x0,0xc042,0x0,0x0,0x0,0xc0420000,0x0,
  0xc042,0x0,0x0,0x0,0xc0420000,0x0,0x0,0x0,0x0,0x0,0xc042,0x0,0x0,0x0,0xc0420000,0x0,
  0x0,0xc0420000,0x0,0x0,0x0,0x0,0x0,0x0,0x0,0x0,0x0,0xc0420000,0x0,0x0,0x0,0x0,
  0x0,0x0,0x0,0x0,0x0,0xc042,0x0,0x0,0x0,0x0,0x0,0x0,0x0,0x0,0x0,0xc042,
  0x3f903f90,0x0,0x0,0x0,0x3f900000,0x0,0x3f90,0x0,0x0,0x0,0x3f903f90,0x0,0x0,0x0,0x3f900000,0x0,
  0x3f903f90,0x0,0x0,0x0,0x3f900000,0x0,0x3f90,0x0,0x0,0x0,0x3f903f90,0x0,0x0,0x0,0x3f900000,0x0,
  0x0,0x0,0x0,0x0,0xc042,0x0,0x0,0x0,0x0,0x0,0x0,0x0,0x0,0x0,0x3f90c042,0x0,
  0x0,0x0,0x0,0x0,0x0,0x3f90,0x0,0x0,0x0,0x3f90,0x0,0x0,0x0,0x0,0x0,0x3f90,
  0x0,0x0,0x0,0x0,0x0,0x0,0x0,0x0,0x0,0x0,0x0,0x0,0x0,0x0,0x0,0x0,
  0x0,0x0,0x0,0x0,0x0,0x0,0x0,0x0,0x0,0x0,0x0,0x0,0x0,0x0,0x0,0x0,
  0xcc000000,0x27201a42,0x19e00,0x40004,0xcc080040,0x27601a53,0x19f20,0x40400,0xcc100090,0x27b019e0,0x19e08,0x4000400,0xcc1800d0,0x27f019f1,0x19f28,0x4000000,
  0xcd200120,0x5201a46,0x19e40,0x60,0xcd280160,0x5601a57,0x19f60,0x0,0xcd3001b0,0x5b019e4,0x19e48,0x6000,0xcd3801f0,0x5f019f5,0x19f68,0x0,
  0xe4401000,0x29201a43,0x19e80,0x0,0xe4481040,0x29601a52,0x19fa0,0x0,0xe4501090,0x29b019e1,0x19e88,0x0,0xe45810d0,0x29f019f0,0x19fa8,0x40000000,
  0xeb601120,0x29201a47,0x19ec0,0x0,0xeb681160,0x29601a56,0x19fe0,0x0,0xeb7011b0,0x29b019e5,0x19ec8,0x0,0xeb7811f0,0x29f019f4,0x19fe8,0x40000000,
  0xe4800010,0x27301a40,0x19e10,0x0,0xe4880050,0x27701a51,0x19f30,0x0,0xe4900080,0x27a019e2,0x19e18,0x0,0xe49800c0,0x27e019f3,0x19f38,0x0,
  0xe5a00130,0x5301a44,0x19e50,0x40000040,0xe5a80170,0x5701a55,0x19f70,0x40,0xe5b001a0,0x5a019e6,0x19e58,0x4000,0xe5b801e0,0x5e019f7,0x19f78,0x4000,
  0xe4c01010,0x29301a41,0x19e90,0x6,0xe4c81050,0x29701a50,0x19fb0,0x60000,0xe4d01080,0x29a019e3,0x19e98,0x600,0xe4d810c0,0x29e019f2,0x19fb8,0x6000000,
  0xd9e01130,0x29301a45,0x19ed0,0x6,0xd9e81170,0x29701a54,0x19ff0,0x60000,0xd9f011a0,0x29a019e7,0x19ed8,0x600,0xd9f811e0,0x29e019f6,0x19ff8,0x6000000,
  0xe4000020,0x27001a4b,0x1a400,0x600000,0xe4080060,0x27401a5a,0x1a520,0x0,0xe41000b0,0x279019e9,0x1a408,0x60000000,0xe41800f0,0x27d019f8,0x1a528,0x0,
  0xe5200100,0x5001a4f,0x1a440,0x0,0xe5280140,0x5401a5e,0x1a560,0x60,0xe5300190,0x59019ed,0x1a448,0x0,0xe53801d0,0x5d019fc,0x1a568,0x6000,
  0xe5601020,0x29001a4a,0x1a480,0x40004,0xe5681060,0x29401a5b,0x1a5a0,0x40400,0xe57010b0,0x299019e8,0x1a488,0x4000400,0xe57810f0,0x29d019f9,0x1a5a8,0x4000000,
  0xcc801100,0x29001a4e,0x1a4c0,0x40004,0xcc881140,0x29401a5f,0x1a5e0,0x40400,0xcc901190,0x299019ec,0x1a4c8,0x4000400,0xcc9811d0,0x29d019fd,0x1a5e8,0x4000000,
  0xcda00030,0x27101a49,0x1a410,0x0,0xcda80070,0x27501a58,0x1a530,0x6,0xcdb000a0,0x278019eb,0x1a418,0x0,0xcdb800e0,0x27c019fa,0x1a538,0x4000600,
  0xeac00110,0x5101a4d,0x1a450,0x0,0xeac80150,0x5501a5c,0x1a570,0x40,0xead00180,0x58019ef,0x1a458,0x40,0xead801c0,0x5c019fe,0x1a578,0x4000,
  0xe5e01030,0x29101a48,0x1a490,0x0,0xe5e81070,0x29501a59,0x1a5b0,0x0,0xe5f010a0,0x298019ea,0x1a498,0x0,0xe5f810e0,0x29c019fb,0x1a5b8,0x0,
  0xe4001110,0x29101a4c,0x1a4d0,0x0,0xe4081150,0x29501a5d,0x1a5f0,0x0,0xe4101180,0x298019ee,0x1a4d8,0x0,0xe41811c0,0x29c019ff,0x1a5f8,0x0,
  0x0,0x0,0x800,0x10000000,0x10,0x90,0x890,0x10000000,0x20,0x120,0x920,0x10000000,0x30,0x1b0,0x9b0,0x10000000,
  0x40,0x240,0xa40,0x20003180,0x50,0x2d0,0xad0,0x20003190,0x60,0x360,0xb60,0x200031a0,0x70,0x3f0,0xbf0,0x200031b0,
  0x610,0xc00,0x1400,0x40000000,0x600,0xc90,0x1490,0x40000010,0x630,0xd20,0x1520,0x40000020,0x620,0xdb0,0x15b0,0x40000030,
  0x650,0xe40,0x1640,0x20033180,0x640,0xed0,0x16d0,0x20033190,0x670,0xf60,0x1760,0x200331a0,0x660,0xff0,0x17f0,0x200331b0,
  0xc20,0x10,0x808,0x40030020,0xc30,0x80,0x898,0x40030030,0xc00,0x130,0x928,0x40030000,0xc10,0x1a0,0x9b8,0x40030010,
  0xc60,0x250,0xa48,0x200631a0,0xc70,0x2c0,0xad8,0x200631b0,0xc40,0x370,0xb68,0x20063180,0xc50,0x3e0,0xbf8,0x20063190,
  0x1230,0xc10,0x1408,0x40060020,0x1220,0xc80,0x1498,0x40060030,0x1210,0xd30,0x1528,0x40060000,0x1200,0xda0,0x15b8,0x40060010,
  0x1270,0xe50,0x1648,0x10000000,0x1260,0xec0,0x16d8,0x10000000,0x1250,0xf70,0x1768,0x10000000,0x1240,0xfe0,0x17f8,0x10000000,
  0x1840,0x20,0x810,0x10000000,0x1850,0xb0,0x880,0x10000000,0x1860,0x100,0x930,0x10000000,0x1870,0x190,0x9a0,0x10000000,
  0x1800,0x260,0xa50,0x200031c0,0x1810,0x2f0,0xac0,0x200031d0,0x1820,0x340,0xb70,0x200031e0,0x1830,0x3d0,0xbe0,0x200031f0,
  0x1e50,0xc20,0x1410,0x40000040,0x1e40,0xcb0,0x1480,0x40000050,0x1e70,0xd00,0x1530,0x40000060,0x1e60,0xd90,0x15a0,0x40000070,
  0x1e10,0xe60,0x1650,0x200331c0,0x1e00,0xef0,0x16c0,0x200331d0,0x1e30,0xf40,0x1770,0x200331e0,0x1e20,0xfd0,0x17e0,0x200331f0,
  0x2460,0x30,0x818,0x40030060,0x2470,0xa0,0x888,0x40030070,0x2440,0x110,0x938,0x40030040,0x2450,0x180,0x9a8,0x40030050,
  0x2420,0x270,0xa58,0x200631e0,0x2430,0x2e0,0xac8,0x200631f0,0x2400,0x350,0xb78,0x200631c0,0x2410,0x3c0,0xbe8,0x200631d0,
  0x2a70,0xc30,0x1418,0x40060060,0x2a60,0xca0,0x1488,0x40060070,0x2a50,0xd10,0x1538,0x40060040,0x2a40,0xd80,0x15a8,0x40060050,
  0x2a30,0xe70,0x1658,0x10000000,0x2a20,0xee0,0x16c8,0x10000000,0x2a10,0xf50,0x1778,0x10000000,0x2a00,0xfc0,0x17e8,0x10000000,
  0xbfd8bf90,0x3f403f00,0xa800,0xa800,0xbfd83f90,0x3f40bf00,0xa840,0xa840,0x3f003f40,0xbf90bfd8,0xa890,0xa890,0xbf003f40,0x3f90bfd8,0xa8d0,0xa8d0,
  0xbfd8bf90,0x3f403f00,0xb420,0xb420,0xbfd83f90,0x3f40bf00,0xb460,0xb460,0x3f003f40,0xbf90bfd8,0xb4b0,0xb4b0,0xbf003f40,0x3f90bfd8,0xb4f0,0xb4f0,
  0xbfd8bf90,0x3f403f00,0x17400,0x17a00,0xbfd83f90,0x3f40bf00,0x17440,0x17a40,0x3f003f40,0xbf90bfd8,0x17490,0x17a90,0xbf003f40,0x3f90bfd8,0x174d0,0x17ad0,
  0xbfd8bf90,0x3f403f00,0x18020,0x18620,0xbfd83f90,0x3f40bf00,0x18060,0x18660,0x3f003f40,0xbf90bfd8,0x180b0,0x186b0,0xbf003f40,0x3f90bfd8,0x180f0,0x186f0,
  0xbfd8bf90,0x3f403f00,0xa810,0xa810,0xbfd83f90,0x3f40bf00,0xa850,0xa850,0x3f003f40,0xbf90bfd8,0xa880,0xa880,0xbf003f40,0x3f90bfd8,0xa8c0,0xa8c0,
  0xbfd8bf90,0x3f403f00,0xb430,0xb430,0xbfd83f90,0x3f40bf00,0xb470,0xb470,0x3f003f40,0xbf90bfd8,0xb4a0,0xb4a0,0xbf003f40,0x3f90bfd8,0xb4e0,0xb4e0,
  0xbfd8bf90,0x3f403f00,0x17410,0x17a10,0xbfd83f90,0x3f40bf00,0x17450,0x17a50,0x3f003f40,0xbf90bfd8,0x17480,0x17a80,0xbf003f40,0x3f90bfd8,0x174c0,0x17ac0,
  0xbfd8bf90,0x3f403f00,0x18030,0x18630,0xbfd83f90,0x3f40bf00,0x18070,0x18670,0x3f003f40,0xbf90bfd8,0x180a0,0x186a0,0xbf003f40,0x3f90bfd8,0x180e0,0x186e0,
  0xbfd8bf90,0x3f403f00,0xa820,0xa820,0xbfd83f90,0x3f40bf00,0xa860,0xa860,0x3f003f40,0xbf90bfd8,0xa8b0,0xa8b0,0xbf003f40,0x3f90bfd8,0xa8f0,0xa8f0,
  0xbfd8bf90,0x3f403f00,0xb400,0xb400,0xbfd83f90,0x3f40bf00,0xb440,0xb440,0x3f003f40,0xbf90bfd8,0xb490,0xb490,0xbf003f40,0x3f90bfd8,0xb4d0,0xb4d0,
  0xbfd8bf90,0x3f403f00,0x17420,0x17a20,0xbfd83f90,0x3f40bf00,0x17460,0x17a60,0x3f003f40,0xbf90bfd8,0x174b0,0x17ab0,0xbf003f40,0x3f90bfd8,0x174f0,0x17af0,
  0xbfd8bf90,0x3f403f00,0x18000,0x18600,0xbfd83f90,0x3f40bf00,0x18040,0x18640,0x3f003f40,0xbf90bfd8,0x18090,0x18690,0xbf003f40,0x3f90bfd8,0x180d0,0x186d0,
  0xbfd8bf90,0x3f403f00,0xa830,0xa830,0xbfd83f90,0x3f40bf00,0xa870,0xa870,0x3f003f40,0xbf90bfd8,0xa8a0,0xa8a0,0xbf003f40,0x3f90bfd8,0xa8e0,0xa8e0,
  0xbfd8bf90,0x3f403f00,0xb410,0xb410,0xbfd83f90,0x3f40bf00,0xb450,0xb450,0x3f003f40,0xbf90bfd8,0xb480,0xb480,0xbf003f40,0x3f90bfd8,0xb4c0,0xb4c0,
  0xbfd8bf90,0x3f403f00,0x17430,0x17a30,0xbfd83f90,0x3f40bf00,0x17470,0x17a70,0x3f003f40,0xbf90bfd8,0x174a0,0x17aa0,0xbf003f40,0x3f90bfd8,0x174e0,0x17ae0,
  0xbfd8bf90,0x3f403f00,0x18010,0x18610,0xbfd83f90,0x3f40bf00,0x18050,0x18650,0x3f003f40,0xbf90bfd8,0x18080,0x18680,0xbf003f40,0x3f90bfd8,0x180c0,0x186c0,
  0xa800,0xa820,0x0,0xa800,0xa808,0xa828,0x40,0xa840,0xa810,0xa830,0x90,0xa890,0xa818,0xa838,0xd0,0xa8d0,
  0xa920,0xa900,0x2400,0xa900,0xa928,0xa908,0x2440,0xa940,0xa930,0xa910,0x2490,0xa990,0xa938,0xa918,0x24d0,0xa9d0,
  0xa840,0xa860,0x200,0xc000,0xa848,0xa868,0x240,0xc040,0xa850,0xa870,0x290,0xc090,0xa858,0xa878,0x2d0,0xc0d0,
  0xa960,0xa940,0x200,0xc000,0xa968,0xa948,0x240,0xc040,0xa970,0xa950,0x290,0xc090,0xa978,0xa958,0x2d0,0xc0d0,
  0xea80,0xeaa0,0x10,0xa810,0xea88,0xeaa8,0x50,0xa850,0xea90,0xeab0,0x80,0xa880,0xea98,0xeab8,0xc0,0xa8c0,
  0xa9a0,0xa980,0x2410,0xa910,0xa9a8,0xa988,0x2450,0xa950,0xa9b0,0xa990,0x2480,0xa980,0xa9b8,0xa998,0x24c0,0xa9c0,
  0xc0c0,0xc0e0,0x210,0xc010,0xc0c8,0xc0e8,0x250,0xc050,0xc0d0,0xc0f0,0x280,0xc080,0xc0d8,0xc0f8,0x2c0,0xc0c0,
  0xc1e0,0xc1c0,0x210,0xc010,0xc1e8,0xc1c8,0x250,0xc050,0xc1f0,0xc1d0,0x280,0xc080,0xc1f8,0xc1d8,0x2c0,0xc0c0,
  0xae00,0xae20,0x20,0xa820,0xae08,0xae28,0x60,0xa860,0xae10,0xae30,0xb0,0xa8b0,0xae18,0xae38,0xf0,0xa8f0,
  0xeb20,0xeb00,0x2420,0xa920,0xeb28,0xeb08,0x2460,0xa960,0xeb30,0xeb10,0x24b0,0xa9b0,0xeb38,0xeb18,0x24f0,0xa9f0,
  0xc040,0xc060,0x220,0xc020,0xc048,0xc068,0x260,0xc060,0xc050,0xc070,0x2b0,0xc0b0,0xc058,0xc078,0x2f0,0xc0f0,
  0xc160,0xc140,0x220,0xc020,0xc168,0xc148,0x260,0xc060,0xc170,0xc150,0x2b0,0xc0b0,0xc178,0xc158,0x2f0,0xc0f0,
  0xa880,0xa8a0,0x30,0xa830,0xa888,0xa8a8,0x70,0xa870,0xa890,0xa8b0,0xa0,0xa8a0,0xa898,0xa8b8,0xe0,0xa8e0,
  0xa8c0,0xa8e0,0x2430,0xa930,0xa8c8,0xa8e8,0x2470,0xa970,0xa8d0,0xa8f0,0x24a0,0xa9a0,0xa8d8,0xa8f8,0x24e0,0xa9e0,
  0xa9e0,0xa9c0,0x230,0xc030,0xa9e8,0xa9c8,0x270,0xc070,0xa9f0,0xa9d0,0x2a0,0xc0a0,0xa9f8,0xa9d8,0x2e0,0xc0e0,
  0xae00,0xae20,0x230,0xc030,0xae08,0xae28,0x270,0xc070,0xae10,0xae30,0x2a0,0xc0a0,0xae18,0xae38,0x2e0,0xc0e0,
  0x180000,0x40,0x820,0x10000000,0x180010,0xd0,0x8b0,0x10000000,0x180020,0x160,0x900,0x10000000,0x180030,0x1f0,0x990,0x10000000,
  0x180040,0x200,0xa60,0x20093180,0x180050,0x290,0xaf0,0x20093190,0x180060,0x320,0xb40,0x200931a0,0x180070,0x3b0,0xbd0,0x200931b0,
  0x180610,0xc40,0x1420,0x40090000,0x180600,0xcd0,0x14b0,0x40090010,0x180630,0xd60,0x1500,0x40090020,0x180620,0xdf0,0x1590,0x40090030,
  0x180650,0xe00,0x1660,0x200c3180,0x180640,0xe90,0x16f0,0x200c3190,0x180670,0xf20,0x1740,0x200c31a0,0x180660,0xfb0,0x17d0,0x200c31b0,
  0x180c20,0x50,0x828,0x400c0020,0x180c30,0xc0,0x8b8,0x400c0030,0x180c00,0x170,0x908,0x400c0000,0x180c10,0x1e0,0x998,0x400c0010,
  0x180c60,0x210,0xa68,0x200f31a0,0x180c70,0x280,0xaf8,0x200f31b0,0x180c40,0x330,0xb48,0x200f3180,0x180c50,0x3a0,0xbd8,0x200f3190,
  0x181230,0xc50,0x1428,0x400f0020,0x181220,0xcc0,0x14b8,0x400f0030,0x181210,0xd70,0x1508,0x400f0000,0x181200,0xde0,0x1598,0x400f0010,
  0x181270,0xe10,0x1668,0x10000000,0x181260,0xe80,0x16f8,0x10000000,0x181250,0xf30,0x1748,0x10000000,0x181240,0xfa0,0x17d8,0x10000000,
  0x181840,0x60,0x830,0x10000000,0x181850,0xf0,0x8a0,0x10000000,0x181860,0x140,0x910,0x10000000,0x181870,0x1d0,0x980,0x10000000,
  0x181800,0x220,0xa70,0x200931c0,0x181810,0x2b0,0xae0,0x200931d0,0x181820,0x300,0xb50,0x200931e0,0x181830,0x390,0xbc0,0x200931f0,
  0x181e50,0xc60,0x1430,0x40090040,0x181e40,0xcf0,0x14a0,0x40090050,0x181e70,0xd40,0x1510,0x40090060,0x181e60,0xdd0,0x1580,0x40090070,
  0x181e10,0xe20,0x1670,0x200c31c0,0x181e00,0xeb0,0x16e0,0x200c31d0,0x181e30,0xf00,0x1750,0x200c31e0,0x181e20,0xf90,0x17c0,0x200c31f0,
  0x182460,0x70,0x838,0x400c0060,0x182470,0xe0,0x8a8,0x400c0070,0x182440,0x150,0x918,0x400c0040,0x182450,0x1c0,0x988,0x400c0050,
  0x182420,0x230,0xa78,0x200f31e0,0x182430,0x2a0,0xae8,0x200f31f0,0x182400,0x310,0xb58,0x200f31c0,0x182410,0x380,0xbc8,0x200f31d0,
  0x182a70,0xc70,0x1438,0x400f0060,0x182a60,0xce0,0x14a8,0x400f0070,0x182a50,0xd50,0x1518,0x400f0040,0x182a40,0xdc0,0x1588,0x400f0050,
  0x182a30,0xe30,0x1678,0x10000000,0x182a20,0xea0,0x16e8,0x10000000,0x182a10,0xf10,0x1758,0x10000000,0x182a00,0xf80,0x17c8,0x10000000,
  0xbfd8bf90,0x3f403f00,0xa800,0xa800,0xbfd83f90,0x3f40bf00,0xa840,0xa840,0x3f003f40,0xbf90bfd8,0xa890,0xa890,0xbf003f40,0x3f90bfd8,0xa8d0,0xa8d0,
  0xbfd8bf90,0x3f403f00,0xb420,0xb420,0xbfd83f90,0x3f40bf00,0xb460,0xb460,0x3f003f40,0xbf90bfd8,0xb4b0,0xb4b0,0xbf003f40,0x3f90bfd8,0xb4f0,0xb4f0,
  0xbfd8bf90,0x3f403f00,0x17400,0x17a00,0xbfd83f90,0x3f40bf00,0x17440,0x17a40,0x3f003f40,0xbf90bfd8,0x17490,0x17a90,0xbf003f40,0x3f90bfd8,0x174d0,0x17ad0,
  0xbfd8bf90,0x3f403f00,0x18020,0x18620,0xbfd83f90,0x3f40bf00,0x18060,0x18660,0x3f003f40,0xbf90bfd8,0x180b0,0x186b0,0xbf003f40,0x3f90bfd8,0x180f0,0x186f0,
  0xbfd8bf90,0x3f403f00,0xa810,0xa810,0xbfd83f90,0x3f40bf00,0xa850,0xa850,0x3f003f40,0xbf90bfd8,0xa880,0xa880,0xbf003f40,0x3f90bfd8,0xa8c0,0xa8c0,
  0xbfd8bf90,0x3f403f00,0xb430,0xb430,0xbfd83f90,0x3f40bf00,0xb470,0xb470,0x3f003f40,0xbf90bfd8,0xb4a0,0xb4a0,0xbf003f40,0x3f90bfd8,0xb4e0,0xb4e0,
  0xbfd8bf90,0x3f403f00,0x17410,0x17a10,0xbfd83f90,0x3f40bf00,0x17450,0x17a50,0x3f003f40,0xbf90bfd8,0x17480,0x17a80,0xbf003f40,0x3f90bfd8,0x174c0,0x17ac0,
  0xbfd8bf90,0x3f403f00,0x18030,0x18630,0xbfd83f90,0x3f40bf00,0x18070,0x18670,0x3f003f40,0xbf90bfd8,0x180a0,0x186a0,0xbf003f40,0x3f90bfd8,0x180e0,0x186e0,
  0xbfd8bf90,0x3f403f00,0xa820,0xa820,0xbfd83f90,0x3f40bf00,0xa860,0xa860,0x3f003f40,0xbf90bfd8,0xa8b0,0xa8b0,0xbf003f40,0x3f90bfd8,0xa8f0,0xa8f0,
  0xbfd8bf90,0x3f403f00,0xb400,0xb400,0xbfd83f90,0x3f40bf00,0xb440,0xb440,0x3f003f40,0xbf90bfd8,0xb490,0xb490,0xbf003f40,0x3f90bfd8,0xb4d0,0xb4d0,
  0xbfd8bf90,0x3f403f00,0x17420,0x17a20,0xbfd83f90,0x3f40bf00,0x17460,0x17a60,0x3f003f40,0xbf90bfd8,0x174b0,0x17ab0,0xbf003f40,0x3f90bfd8,0x174f0,0x17af0,
  0xbfd8bf90,0x3f403f00,0x18000,0x18600,0xbfd83f90,0x3f40bf00,0x18040,0x18640,0x3f003f40,0xbf90bfd8,0x18090,0x18690,0xbf003f40,0x3f90bfd8,0x180d0,0x186d0,
  0xbfd8bf90,0x3f403f00,0xa830,0xa830,0xbfd83f90,0x3f40bf00,0xa870,0xa870,0x3f003f40,0xbf90bfd8,0xa8a0,0xa8a0,0xbf003f40,0x3f90bfd8,0xa8e0,0xa8e0,
  0xbfd8bf90,0x3f403f00,0xb410,0xb410,0xbfd83f90,0x3f40bf00,0xb450,0xb450,0x3f003f40,0xbf90bfd8,0xb480,0xb480,0xbf003f40,0x3f90bfd8,0xb4c0,0xb4c0,
  0xbfd8bf90,0x3f403f00,0x17430,0x17a30,0xbfd83f90,0x3f40bf00,0x17470,0x17a70,0x3f003f40,0xbf90bfd8,0x174a0,0x17aa0,0xbf003f40,0x3f90bfd8,0x174e0,0x17ae0,
  0xbfd8bf90,0x3f403f00,0x18010,0x18610,0xbfd83f90,0x3f40bf00,0x18050,0x18650,0x3f003f40,0xbf90bfd8,0x18080,0x18680,0xbf003f40,0x3f90bfd8,0x180c0,0x186c0,
  0xb400,0xb420,0x120,0xb420,0xb408,0xb428,0x160,0xb460,0xb410,0xb430,0x1b0,0xb4b0,0xb418,0xb438,0x1f0,0xb4f0,
  0xb440,0xb460,0x2520,0xb520,0xb448,0xb468,0x2560,0xb560,0xb450,0xb470,0x25b0,0xb5b0,0xb458,0xb478,0x25f0,0xb5f0,
  0xcc40,0xcc60,0x320,0xcc20,0xcc48,0xcc68,0x360,0xcc60,0xcc50,0xcc70,0x3b0,0xccb0,0xcc58,0xcc78,0x3f0,0xccf0,
  0xcd60,0xcd40,0x320,0xcc20,0xcd68,0xcd48,0x360,0xcc60,0xcd70,0xcd50,0x3b0,0xccb0,0xcd78,0xcd58,0x3f0,0xccf0,
  0xb5a0,0xb580,0x130,0xb430,0xb5a8,0xb588,0x170,0xb470,0xb5b0,0xb590,0x1a0,0xb4a0,0xb5b8,0xb598,0x1e0,0xb4e0,
  0xccc0,0xcce0,0x2530,0xb530,0xccc8,0xcce8,0x2570,0xb570,0xccd0,0xccf0,0x25a0,0xb5a0,0xccd8,0xccf8,0x25e0,0xb5e0,
  0xa9e0,0xa9c0,0x330,0xcc30,0xa9e8,0xa9c8,0x370,0xcc70,0xa9f0,0xa9d0,0x3a0,0xcca0,0xa9f8,0xa9d8,0x3e0,0xcce0,
  0xc1e0,0xc1c0,0x330,0xcc30,0xc1e8,0xc1c8,0x370,0xcc70,0xc1f0,0xc1d0,0x3a0,0xcca0,0xc1f8,0xc1d8,0x3e0,0xcce0,
  0xb520,0xb500,0x100,0xb400,0xb528,0xb508,0x140,0xb440,0xb530,0xb510,0x190,0xb490,0xb538,0xb518,0x1d0,0xb4d0,
  0xea40,0xea60,0x2500,0xb500,0xea48,0xea68,0x2540,0xb540,0xea50,0xea70,0x2590,0xb590,0xea58,0xea78,0x25d0,0xb5d0,
  0xb560,0xb540,0x300,0xcc00,0xb568,0xb548,0x340,0xcc40,0xb570,0xb550,0x390,0xcc90,0xb578,0xb558,0x3d0,0xccd0,
  0xb480,0xb4a0,0x300,0xcc00,0xb488,0xb4a8,0x340,0xcc40,0xb490,0xb4b0,0x390,0xcc90,0xb498,0xb4b8,0x3d0,0xccd0,
  0xb4c0,0xb4e0,0x110,0xb410,0xb4c8,0xb4e8,0x150,0xb450,0xb4d0,0xb4f0,0x180,0xb480,0xb4d8,0xb4f8,0x1c0,0xb4c0,
  0xb5e0,0xb5c0,0x2510,0xb510,0xb5e8,0xb5c8,0x2550,0xb550,0xb5f0,0xb5d0,0x2580,0xb580,0xb5f8,0xb5d8,0x25c0,0xb5c0,
  0xcde0,0xcdc0,0x310,0xcc10,0xcde8,0xcdc8,0x350,0xcc50,0xcdf0,0xcdd0,0x380,0xcc80,0xcdf8,0xcdd8,0x3c0,0xccc0,
  0xb520,0xb500,0x310,0xcc10,0xb528,0xb508,0x350,0xcc50,0xb530,0xb510,0x380,0xcc80,0xb538,0xb518,0x3c0,0xccc0,
  0x0,0x400,0x840,0x10000000,0x10,0x490,0x8d0,0x10000000,0x20,0x520,0x960,0x10000000,0x30,0x5b0,0x9f0,0x10000000,
  0x40,0x640,0xa00,0x20123180,0x50,0x6d0,0xa90,0x20123190,0x60,0x760,0xb20,0x201231a0,0x70,0x7f0,0xbb0,0x201231b0,
  0x610,0x1000,0x1440,0x40120000,0x600,0x1090,0x14d0,0x40120010,0x630,0x1120,0x1560,0x40120020,0x620,0x11b0,0x15f0,0x40120030,
  0x650,0x1240,0x1600,0x20153180,0x640,0x12d0,0x1690,0x20153190,0x670,0x1360,0x1720,0x201531a0,0x660,0x13f0,0x17b0,0x201531b0,
  0xc20,0x410,0x848,0x40150020,0xc30,0x480,0x8d8,0x40150030,0xc00,0x530,0x968,0x40150000,0xc10,0x5a0,0x9f8,0x40150010,
  0xc60,0x650,0xa08,0x201831a0,0xc70,0x6c0,0xa98,0x201831b0,0xc40,0x770,0xb28,0x20183180,0xc50,0x7e0,0xbb8,0x20183190,
  0x1230,0x1010,0x1448,0x40180020,0x1220,0x1080,0x14d8,0x40180030,0x1210,0x1130,0x1568,0x40180000,0x1200,0x11a0,0x15f8,0x40180010,
  0x1270,0x1250,0x1608,0x10000000,0x1260,0x12c0,0x1698,0x10000000,0x1250,0x1370,0x1728,0x10000000,0x1240,0x13e0,0x17b8,0x10000000,
  0x1840,0x420,0x850,0x10000000,0x1850,0x4b0,0x8c0,0x10000000,0x1860,0x500,0x970,0x10000000,0x1870,0x590,0x9e0,0x10000000,
  0x1800,0x660,0xa10,0x201231c0,0x1810,0x6f0,0xa80,0x201231d0,0x1820,0x740,0xb30,0x201231e0,0x1830,0x7d0,0xba0,0x201231f0,
  0x1e50,0x1020,0x1450,0x40120040,0x1e40,0x10b0,0x14c0,0x40120050,0x1e70,0x1100,0x1570,0x40120060,0x1e60,0x1190,0x15e0,0x40120070,
  0x1e10,0x1260,0x1610,0x201531c0,0x1e00,0x12f0,0x1680,0x201531d0,0x1e30,0x1340,0x1730,0x201531e0,0x1e20,0x13d0,0x17a0,0x201531f0,
  0x2460,0x430,0x858,0x40150060,0x2470,0x4a0,0x8c8,0x40150070,0x2440,0x510,0x978,0x40150040,0x2450,0x580,0x9e8,0x40150050,
  0x2420,0x670,0xa18,0x201831e0,0x2430,0x6e0,0xa88,0x201831f0,0x2400,0x750,0xb38,0x201831c0,0x2410,0x7c0,0xba8,0x201831d0,
  0x2a70,0x1030,0x1458,0x40180060,0x2a60,0x10a0,0x14c8,0x40180070,0x2a50,0x1110,0x1578,0x40180040,0x2a40,0x1180,0x15e8,0x40180050,
  0x2a30,0x1270,0x1618,0x10000000,0x2a20,0x12e0,0x1688,0x10000000,0x2a10,0x1350,0x1738,0x10000000,0x2a00,0x13c0,0x17a8,0x10000000,
  0xbfd8bf90,0x3f403f00,0xa800,0xa800,0xbfd83f90,0x3f40bf00,0xa840,0xa840,0x3f003f40,0xbf90bfd8,0xa890,0xa890,0xbf003f40,0x3f90bfd8,0xa8d0,0xa8d0,
  0xbfd8bf90,0x3f403f00,0xb420,0xb420,0xbfd83f90,0x3f40bf00,0xb460,0xb460,0x3f003f40,0xbf90bfd8,0xb4b0,0xb4b0,0xbf003f40,0x3f90bfd8,0xb4f0,0xb4f0,
  0xbfd8bf90,0x3f403f00,0x17400,0x17a00,0xbfd83f90,0x3f40bf00,0x17440,0x17a40,0x3f003f40,0xbf90bfd8,0x17490,0x17a90,0xbf003f40,0x3f90bfd8,0x174d0,0x17ad0,
  0xbfd8bf90,0x3f403f00,0x18020,0x18620,0xbfd83f90,0x3f40bf00,0x18060,0x18660,0x3f003f40,0xbf90bfd8,0x180b0,0x186b0,0xbf003f40,0x3f90bfd8,0x180f0,0x186f0,
  0xbfd8bf90,0x3f403f00,0xa810,0xa810,0xbfd83f90,0x3f40bf00,0xa850,0xa850,0x3f003f40,0xbf90bfd8,0xa880,0xa880,0xbf003f40,0x3f90bfd8,0xa8c0,0xa8c0,
  0xbfd8bf90,0x3f403f00,0xb430,0xb430,0xbfd83f90,0x3f40bf00,0xb470,0xb470,0x3f003f40,0xbf90bfd8,0xb4a0,0xb4a0,0xbf003f40,0x3f90bfd8,0xb4e0,0xb4e0,
  0xbfd8bf90,0x3f403f00,0x17410,0x17a10,0xbfd83f90,0x3f40bf00,0x17450,0x17a50,0x3f003f40,0xbf90bfd8,0x17480,0x17a80,0xbf003f40,0x3f90bfd8,0x174c0,0x17ac0,
  0xbfd8bf90,0x3f403f00,0x18030,0x18630,0xbfd83f90,0x3f40bf00,0x18070,0x18670,0x3f003f40,0xbf90bfd8,0x180a0,0x186a0,0xbf003f40,0x3f90bfd8,0x180e0,0x186e0,
  0xbfd8bf90,0x3f403f00,0xa820,0xa820,0xbfd83f90,0x3f40bf00,0xa860,0xa860,0x3f003f40,0xbf90bfd8,0xa8b0,0xa8b0,0xbf003f40,0x3f90bfd8,0xa8f0,0xa8f0,
  0xbfd8bf90,0x3f403f00,0xb400,0xb400,0xbfd83f90,0x3f40bf00,0xb440,0xb440,0x3f003f40,0xbf90bfd8,0xb490,0xb490,0xbf003f40,0x3f90bfd8,0xb4d0,0xb4d0,
  0xbfd8bf90,0x3f403f00,0x17420,0x17a20,0xbfd83f90,0x3f40bf00,0x17460,0x17a60,0x3f003f40,0xbf90bfd8,0x174b0,0x17ab0,0xbf003f40,0x3f90bfd8,0x174f0,0x17af0,
  0xbfd8bf90,0x3f403f00,0x18000,0x18600,0xbfd83f90,0x3f40bf00,0x18040,0x18640,0x3f003f40,0xbf90bfd8,0x18090,0x18690,0xbf003f40,0x3f90bfd8,0x180d0,0x186d0,
  0xbfd8bf90,0x3f403f00,0xa830,0xa830,0xbfd83f90,0x3f40bf00,0xa870,0xa870,0x3f003f40,0xbf90bfd8,0xa8a0,0xa8a0,0xbf003f40,0x3f90bfd8,0xa8e0,0xa8e0,
  0xbfd8bf90,0x3f403f00,0xb410,0xb410,0xbfd83f90,0x3f40bf00,0xb450,0xb450,0x3f003f40,0xbf90bfd8,0xb480,0xb480,0xbf003f40,0x3f90bfd8,0xb4c0,0xb4c0,
  0xbfd8bf90,0x3f403f00,0x17430,0x17a30,0xbfd83f90,0x3f40bf00,0x17470,0x17a70,0x3f003f40,0xbf90bfd8,0x174a0,0x17aa0,0xbf003f40,0x3f90bfd8,0x174e0,0x17ae0,
  0xbfd8bf90,0x3f403f00,0x18010,0x18610,0xbfd83f90,0x3f40bf00,0x18050,0x18650,0x3f003f40,0xbf90bfd8,0x18080,0x18680,0xbf003f40,0x3f90bfd8,0x180c0,0x186c0,
  0xc000,0xc020,0x2600,0xc100,0xc008,0xc028,0x2640,0xc140,0xc010,0xc030,0x2690,0xc190,0xc018,0xc038,0x26d0,0xc1d0,
  0xd800,0xd820,0x400,0xd800,0xd808,0xd828,0x440,0xd840,0xd810,0xd830,0x490,0xd890,0xd818,0xd838,0x4d0,0xd8d0,
  0xc120,0xc100,0x2800,0xd900,0xc128,0xc108,0x2840,0xd940,0xc130,0xc110,0x2890,0xd990,0xc138,0xc118,0x28d0,0xd9d0,
  0xd840,0xd860,0x2800,0xd900,0xd848,0xd868,0x2840,0xd940,0xd850,0xd870,0x2890,0xd990,0xd858,0xd878,0x28d0,0xd9d0,
  0xc080,0xc0a0,0x2610,0xc110,0xc088,0xc0a8,0x2650,0xc150,0xc090,0xc0b0,0x2680,0xc180,0xc098,0xc0b8,0x26c0,0xc1c0,
  0xc1a0,0xc180,0x410,0xd810,0xc1a8,0xc188,0x450,0xd850,0xc1b0,0xc190,0x480,0xd880,0xc1b8,0xc198,0x4c0,0xd8c0,
  0xd9a0,0xd980,0x2810,0xd910,0xd9a8,0xd988,0x2850,0xd950,0xd9b0,0xd990,0x2880,0xd980,0xd9b8,0xd998,0x28c0,0xd9c0,
  0xd9e0,0xd9c0,0x2810,0xd910,0xd9e8,0xd9c8,0x2850,0xd950,0xd9f0,0xd9d0,0x2880,0xd980,0xd9f8,0xd9d8,0x28c0,0xd9c0,
  0xc600,0xc620,0x2620,0xc120,0xc608,0xc628,0x2660,0xc160,0xc610,0xc630,0x26b0,0xc1b0,0xc618,0xc638,0x26f0,0xc1f0,
  0xde00,0xde20,0x420,0xd820,0xde08,0xde28,0x460,0xd860,0xde10,0xde30,0x4b0,0xd8b0,0xde18,0xde38,0x4f0,0xd8f0,
  0xd920,0xd900,0x2820,0xd920,0xd928,0xd908,0x2860,0xd960,0xd930,0xd910,0x28b0,0xd9b0,0xd938,0xd918,0x28f0,0xd9f0,
  0xd960,0xd940,0x2820,0xd920,0xd968,0xd948,0x2860,0xd960,0xd970,0xd950,0x28b0,0xd9b0,0xd978,0xd958,0x28f0,0xd9f0,
  0xd880,0xd8a0,0x2630,0xc130,0xd888,0xd8a8,0x2670,0xc170,0xd890,0xd8b0,0x26a0,0xc1a0,0xd898,0xd8b8,0x26e0,0xc1e0,
  0xeba0,0xeb80,0x430,0xd830,0xeba8,0xeb88,0x470,0xd870,0xebb0,0xeb90,0x4a0,0xd8a0,0xebb8,0xeb98,0x4e0,0xd8e0,
  0xd8c0,0xd8e0,0x2830,0xd930,0xd8c8,0xd8e8,0x2870,0xd970,0xd8d0,0xd8f0,0x28a0,0xd9a0,0xd8d8,0xd8f8,0x28e0,0xd9e0,
  0xc600,0xc620,0x2830,0xd930,0xc608,0xc628,0x2870,0xd970,0xc610,0xc630,0x28a0,0xd9a0,0xc618,0xc638,0x28e0,0xd9e0,
  0x180000,0x440,0x860,0x10000000,0x180010,0x4d0,0x8f0,0x10000000,0x180020,0x560,0x940,0x10000000,0x180030,0x5f0,0x9d0,0x10000000,
  0x180040,0x600,0xa20,0x201b3180,0x180050,0x690,0xab0,0x201b3190,0x180060,0x720,0xb00,0x201b31a0,0x180070,0x7b0,0xb90,0x201b31b0,
  0x180610,0x1040,0x1460,0x401b0000,0x180600,0x10d0,0x14f0,0x401b0010,0x180630,0x1160,0x1540,0x401b0020,0x180620,0x11f0,0x15d0,0x401b0030,
  0x180650,0x1200,0x1620,0x201e3180,0x180640,0x1290,0x16b0,0x201e3190,0x180670,0x1320,0x1700,0x201e31a0,0x180660,0x13b0,0x1790,0x201e31b0,
  0x180c20,0x450,0x868,0x401e0020,0x180c30,0x4c0,0x8f8,0x401e0030,0x180c00,0x570,0x948,0x401e0000,0x180c10,0x5e0,0x9d8,0x401e0010,
  0x180c60,0x610,0xa28,0x202131a0,0x180c70,0x680,0xab8,0x202131b0,0x180c40,0x730,0xb08,0x20213180,0x180c50,0x7a0,0xb98,0x20213190,
  0x181230,0x1050,0x1468,0x40210020,0x181220,0x10c0,0x14f8,0x40210030,0x181210,0x1170,0x1548,0x40210000,0x181200,0x11e0,0x15d8,0x40210010,
  0x181270,0x1210,0x1628,0x10000000,0x181260,0x1280,0x16b8,0x10000000,0x181250,0x1330,0x1708,0x10000000,0x181240,0x13a0,0x1798,0x10000000,
  0x181840,0x460,0x870,0x10000000,0x181850,0x4f0,0x8e0,0x10000000,0x181860,0x540,0x950,0x10000000,0x181870,0x5d0,0x9c0,0x10000000,
  0x181800,0x620,0xa30,0x201b31c0,0x181810,0x6b0,0xaa0,0x201b31d0,0x181820,0x700,0xb10,0x201b31e0,0x181830,0x790,0xb80,0x201b31f0,
  0x181e50,0x1060,0x1470,0x401b0040,0x181e40,0x10f0,0x14e0,0x401b0050,0x181e70,0x1140,0x1550,0x401b0060,0x181e60,0x11d0,0x15c0,0x401b0070,
  0x181e10,0x1220,0x1630,0x201e31c0,0x181e00,0x12b0,0x16a0,0x201e31d0,0x181e30,0x1300,0x1710,0x201e31e0,0x181e20,0x1390,0x1780,0x201e31f0,
  0x182460,0x470,0x878,0x401e0060,0x182470,0x4e0,0x8e8,0x401e0070,0x182440,0x550,0x958,0x401e0040,0x182450,0x5c0,0x9c8,0x401e0050,
  0x182420,0x630,0xa38,0x202131e0,0x182430,0x6a0,0xaa8,0x202131f0,0x182400,0x710,0xb18,0x202131c0,0x182410,0x780,0xb88,0x202131d0,
  0x182a70,0x1070,0x1478,0x40210060,0x182a60,0x10e0,0x14e8,0x40210070,0x182a50,0x1150,0x1558,0x40210040,0x182a40,0x11c0,0x15c8,0x40210050,
  0x182a30,0x1230,0x1638,0x10000000,0x182a20,0x12a0,0x16a8,0x10000000,0x182a10,0x1310,0x1718,0x10000000,0x182a00,0x1380,0x1788,0x10000000,
  0xbfd8bf90,0x3f403f00,0xa800,0xa800,0xbfd83f90,0x3f40bf00,0xa840,0xa840,0x3f003f40,0xbf90bfd8,0xa890,0xa890,0xbf003f40,0x3f90bfd8,0xa8d0,0xa8d0,
  0xbfd8bf90,0x3f403f00,0xb420,0xb420,0xbfd83f90,0x3f40bf00,0xb460,0xb460,0x3f003f40,0xbf90bfd8,0xb4b0,0xb4b0,0xbf003f40,0x3f90bfd8,0xb4f0,0xb4f0,
  0xbfd8bf90,0x3f403f00,0x17400,0x17a00,0xbfd83f90,0x3f40bf00,0x17440,0x17a40,0x3f003f40,0xbf90bfd8,0x17490,0x17a90,0xbf003f40,0x3f90bfd8,0x174d0,0x17ad0,
  0xbfd8bf90,0x3f403f00,0x18020,0x18620,0xbfd83f90,0x3f40bf00,0x18060,0x18660,0x3f003f40,0xbf90bfd8,0x180b0,0x186b0,0xbf003f40,0x3f90bfd8,0x180f0,0x186f0,
  0xbfd8bf90,0x3f403f00,0xa810,0xa810,0xbfd83f90,0x3f40bf00,0xa850,0xa850,0x3f003f40,0xbf90bfd8,0xa880,0xa880,0xbf003f40,0x3f90bfd8,0xa8c0,0xa8c0,
  0xbfd8bf90,0x3f403f00,0xb430,0xb430,0xbfd83f90,0x3f40bf00,0xb470,0xb470,0x3f003f40,0xbf90bfd8,0xb4a0,0xb4a0,0xbf003f40,0x3f90bfd8,0xb4e0,0xb4e0,
  0xbfd8bf90,0x3f403f00,0x17410,0x17a10,0xbfd83f90,0x3f40bf00,0x17450,0x17a50,0x3f003f40,0xbf90bfd8,0x17480,0x17a80,0xbf003f40,0x3f90bfd8,0x174c0,0x17ac0,
  0xbfd8bf90,0x3f403f00,0x18030,0x18630,0xbfd83f90,0x3f40bf00,0x18070,0x18670,0x3f003f40,0xbf90bfd8,0x180a0,0x186a0,0xbf003f40,0x3f90bfd8,0x180e0,0x186e0,
  0xbfd8bf90,0x3f403f00,0xa820,0xa820,0xbfd83f90,0x3f40bf00,0xa860,0xa860,0x3f003f40,0xbf90bfd8,0xa8b0,0xa8b0,0xbf003f40,0x3f90bfd8,0xa8f0,0xa8f0,
  0xbfd8bf90,0x3f403f00,0xb400,0xb400,0xbfd83f90,0x3f40bf00,0xb440,0xb440,0x3f003f40,0xbf90bfd8,0xb490,0xb490,0xbf003f40,0x3f90bfd8,0xb4d0,0xb4d0,
  0xbfd8bf90,0x3f403f00,0x17420,0x17a20,0xbfd83f90,0x3f40bf00,0x17460,0x17a60,0x3f003f40,0xbf90bfd8,0x174b0,0x17ab0,0xbf003f40,0x3f90bfd8,0x174f0,0x17af0,
  0xbfd8bf90,0x3f403f00,0x18000,0x18600,0xbfd83f90,0x3f40bf00,0x18040,0x18640,0x3f003f40,0xbf90bfd8,0x18090,0x18690,0xbf003f40,0x3f90bfd8,0x180d0,0x186d0,
  0xbfd8bf90,0x3f403f00,0xa830,0xa830,0xbfd83f90,0x3f40bf00,0xa870,0xa870,0x3f003f40,0xbf90bfd8,0xa8a0,0xa8a0,0xbf003f40,0x3f90bfd8,0xa8e0,0xa8e0,
  0xbfd8bf90,0x3f403f00,0xb410,0xb410,0xbfd83f90,0x3f40bf00,0xb450,0xb450,0x3f003f40,0xbf90bfd8,0xb480,0xb480,0xbf003f40,0x3f90bfd8,0xb4c0,0xb4c0,
  0xbfd8bf90,0x3f403f00,0x17430,0x17a30,0xbfd83f90,0x3f40bf00,0x17470,0x17a70,0x3f003f40,0xbf90bfd8,0x174a0,0x17aa0,0xbf003f40,0x3f90bfd8,0x174e0,0x17ae0,
  0xbfd8bf90,0x3f403f00,0x18010,0x18610,0xbfd83f90,0x3f40bf00,0x18050,0x18650,0x3f003f40,0xbf90bfd8,0x18080,0x18680,0xbf003f40,0x3f90bfd8,0x180c0,0x186c0,
  0xcc00,0xcc20,0x2720,0xcd20,0xcc08,0xcc28,0x2760,0xcd60,0xcc10,0xcc30,0x27b0,0xcdb0,0xcc18,0xcc38,0x27f0,0xcdf0,
  0xcd20,0xcd00,0x520,0xe420,0xcd28,0xcd08,0x560,0xe460,0xcd30,0xcd10,0x5b0,0xe4b0,0xcd38,0xcd18,0x5f0,0xe4f0,
  0xe440,0xe460,0x2920,0xe520,0xe448,0xe468,0x2960,0xe560,0xe450,0xe470,0x29b0,0xe5b0,0xe458,0xe478,0x29f0,0xe5f0,
  0xeb60,0xeb40,0x2920,0xe520,0xeb68,0xeb48,0x2960,0xe560,0xeb70,0xeb50,0x29b0,0xe5b0,0xeb78,0xeb58,0x29f0,0xe5f0,
  0xe480,0xe4a0,0x2730,0xcd30,0xe488,0xe4a8,0x2770,0xcd70,0xe490,0xe4b0,0x27a0,0xcda0,0xe498,0xe4b8,0x27e0,0xcde0,
  0xe5a0,0xe580,0x530,0xe430,0xe5a8,0xe588,0x570,0xe470,0xe5b0,0xe590,0x5a0,0xe4a0,0xe5b8,0xe598,0x5e0,0xe4e0,
  0xe4c0,0xe4e0,0x2930,0xe530,0xe4c8,0xe4e8,0x2970,0xe570,0xe4d0,0xe4f0,0x29a0,0xe5a0,0xe4d8,0xe4f8,0x29e0,0xe5e0,
  0xd9e0,0xd9c0,0x2930,0xe530,0xd9e8,0xd9c8,0x2970,0xe570,0xd9f0,0xd9d0,0x29a0,0xe5a0,0xd9f8,0xd9d8,0x29e0,0xe5e0,
  0xe400,0xe420,0x2700,0xcd00,0xe408,0xe428,0x2740,0xcd40,0xe410,0xe430,0x2790,0xcd90,0xe418,0xe438,0x27d0,0xcdd0,
  0xe520,0xe500,0x500,0xe400,0xe528,0xe508,0x540,0xe440,0xe530,0xe510,0x590,0xe490,0xe538,0xe518,0x5d0,0xe4d0,
  0xe560,0xe540,0x2900,0xe500,0xe568,0xe548,0x2940,0xe540,0xe570,0xe550,0x2990,0xe590,0xe578,0xe558,0x29d0,0xe5d0,
  0xcc80,0xcca0,0x2900,0xe500,0xcc88,0xcca8,0x2940,0xe540,0xcc90,0xccb0,0x2990,0xe590,0xcc98,0xccb8,0x29d0,0xe5d0,
  0xcda0,0xcd80,0x2710,0xcd10,0xcda8,0xcd88,0x2750,0xcd50,0xcdb0,0xcd90,0x2780,0xcd80,0xcdb8,0xcd98,0x27c0,0xcdc0,
  0xeac0,0xeae0,0x510,0xe410,0xeac8,0xeae8,0x550,0xe450,0xead0,0xeaf0,0x580,0xe480,0xead8,0xeaf8,0x5c0,0xe4c0,
  0xe5e0,0xe5c0,0x2910,0xe510,0xe5e8,0xe5c8,0x2950,0xe550,0xe5f0,0xe5d0,0x2980,0xe580,0xe5f8,0xe5d8,0x29c0,0xe5c0,
  0xe400,0xe420,0x2910,0xe510,0xe408,0xe428,0x2950,0xe550,0xe410,0xe430,0x2980,0xe580,0xe418,0xe438,0x29c0,0xe5c0,
};

__global__ __launch_bounds__(256, 1) void wino_main_kernel(const char* __restrict__ x, char* __restrict__ Uq, char* __restrict__ out, const char* __restrict__ wt,
                                                           unsigned long long region_mask) {
  __shared__ __attribute__((aligned(1024))) char smem[163840];
  unsigned tid = threadIdx.x;
  asm volatile(
"s_load_dwordx2 s[12:13], %1, 0\n\t"
"s_load_dwordx2 s[52:53], %1, 24\n\t"
"s_load_dwordx2 s[8:9], %1, 8\n\t"
"s_load_dwordx2 s[16:17], %1, 16\n\t"
"s_load_dword s76, %1, 32\n\t"
"s_getpc_b64 s[40:41]\n\t"
"Lwino_pc:\n\t"
"s_bfe_u32 s42, %2, 0x50003\n\t"
"s_lshl_b32 s42, s42, 12\n\t"
"v_lshl_add_u32 v203, v205, 4, s42\n\t"
"v_min_u32 v203, (Lwino_end-Lwino_pc-16)&-16, v203\n\t"
"global_load_dwordx4 v[100:103], v203, s[40:41]\n\t"
"v_readfirstlane_b32 s73, v205\n\t"
"s_lshl2_add_u32 s69, s73, %2\n\t"
"s_and_b32 s60, %2, 7\n\t"
"s_lshr_b32 s61, %2, 7\n\t"
"s_lshl2_add_u32 s60, s60, s61\n\t"
"s_bfe_u32 s61, %2, 0x40003\n\t"
"s_lshl_b32 s60, s60, 8\n\t"
"s_add_u32 s60, s60, s61\n\t"
"s_sub_u32 s60, s60, 16\n\t"
"s_mul_i32 s21, s60, 12288\n\t"
"s_lshr_b32 s73, s73, 6\n\t"
"s_add_u32 s24, s73, 28\n\t"
"s_mulk_i32 s24, 1536\n\t"
"v_bfe_u32 v1, v205, 2, 3\n\t"
"v_lshrrev_b32 v2, 3, v205\n\t"
"v_and_or_b32 v1, v2, 24, v1\n\t"
"v_and_b32 v3, 6, v2\n\t"
"v_and_b32 v4, 3, v205\n\t"
"v_xor_b32 v4, v3, v4\n\t"
"v_mul_u32_u24 v1, 384, v1\n\t"
"v_lshl_add_u32 v28, v4, 4, v1\n\t"
"s_mov_b32 s14, 100663296\n\t"
"s_mov_b32 s15, 0x20000\n\t"
"s_mov_b32 s54, 663552\n\t"
"s_mov_b32 s55, s15\n\t"
"s_mov_b32 s10, 0x200000\n\t"
"s_mov_b32 s11, s15\n\t"
"s_lshl_b32 s60, s73, 11\n\t"
"v_lshl_add_u32 v252, v205, 4, s60\n\t"
"s_lshr_b32 s71, %5, 6\n\t"
"s_mul_i32 s71, s71, 0x9e3779b1\n\t"
"s_lshr_b32 s71, s71, 28\n\t"
"s_add_u32 s71, s71, 2\n\t"
"s_lshl_b32 s71, s71, 21\n\t"
"s_mov_b64 s[46:47], %3\n\t"
"s_add_u32 s48, s46, 12288\n\t"
"s_addc_u32 s49, s47, 0\n\t"
"global_load_dwordx4 v[244:247], v252, s[46:47] offset:2048\n\t"
"s_and_b32 s60, s69, 3\n\t"
"s_lshr_b32 s61, s69, 2\n\t"
"s_mul_i32 s62, s61, 171\n\t"
"s_lshr_b32 s62, s62, 9\n\t"
"s_mul_i32 s63, s62, 3\n\t"
"s_sub_u32 s61, s61, s63\n\t"
"s_mul_i32 s63, s62, 43\n\t"
"s_lshr_b32 s63, s63, 7\n\t"
"s_mul_i32 s64, s63, 3\n\t"
"s_sub_u32 s62, s62, s64\n\t"
"s_mul_i32 s74, s62, 18\n\t"
"s_add_u32 s74, s74, s63\n\t"
"s_lshl3_add_u32 s74, s74, s60\n\t"
"s_mul_i32 s74, s74, 12\n\t"
"s_add_u32 s74, s74, s61\n\t"
"s_lshl_b32 s74, s74, 7\n\t"
"v_bfe_u32 v96, v205, 3, 2\n\t"
"v_and_b32 v97, 32, v205\n\t"
"v_lshl_or_b32 v96, v96, 1, v97\n\t"
"v_mul_u32_u24 v96, 192, v96\n\t"
"v_and_b32 v97, 7, v205\n\t"
"v_lshl_add_u32 v96, v97, 4, v96\n\t"
"v_and_b32 v4, 7, v205\n\t"
"v_xor_b32 v4, v3, v4\n\t"
"v_and_b32 v2, 7, v2\n\t"
"v_mul_u32_u24 v2, 384, v2\n\t"
"v_lshl_add_u32 v29, v4, 4, v2\n\t"
"s_sub_u32 s58, s73, 1\n\t"
"s_lshl_b32 s59, s58, 10\n\t"
"s_lshl_b32 s58, s58, 7\n\t"
"s_waitcnt lgkmcnt(0)\n\t"
"s_and_b32 s13, s13, 0xffff\n\t"
"s_and_b32 s53, s53, 0xffff\n\t"
"s_and_b32 s71, s71, s76\n\t"
"s_add_u32 s8, s8, s71\n\t"
"s_addc_u32 s9, s9, 0\n\t"
"s_and_b32 s9, s9, 0xffff\n\t"
"s_cmp_lg_u32 s73, 0\n\t"
"s_cbranch_scc1 Lpb_%=\n\t"
"s_cmp_lt_u32 s69, 216\n\t"
"s_cbranch_scc0 Lnf1_%=\n\t"
"s_mov_b32 m0, 95232\n\t"
"s_add_u32 s64, s74, 0\n\t"
"buffer_load_dwordx4 v96, s[52:55], s64 offen lds\n\t"
"s_mov_b32 m0, 96256\n\t"
"s_add_u32 s64, s74, 73728\n\t"
"buffer_load_dwordx4 v96, s[52:55], s64 offen lds\n\t"
"s_mov_b32 m0, 97280\n\t"
"s_add_u32 s64, s74, 147456\n\t"
"buffer_load_dwordx4 v96, s[52:55], s64 offen lds\n\t"
"Lnf1_%=:\n\t"
"s_mov_b32 s72, 1900544\n\t"
"s_lshl2_add_u32 s70, %2, s72\n\t"
"s_mov_b32 s68, 3000\n\t"
"buffer_load_dwordx4 v[220:223], v252, s[8:11], s72 offen sc1\n\t"
"s_branch Lpa_%=\n\t"
"Lpb_%=:\n\t"
"s_lshr_b32 s56, %2, 3\n\t"
"s_bitcmp1_b32 0xaaaaaaaa, s56\n\t"
"s_cbranch_scc1 Lpc_%=\n\t"
"s_add_u32 m0, s59, 24576\n\t"
"s_add_u32 s57, s58, 0\n\t"
"buffer_load_dwordx4 v29, s[52:55], s57 offen offset:0 lds\n\t"
"buffer_load_dwordx4 v29, s[52:55], s57 offen offset:3072 lds\n\t"
"s_add_u32 m0, s59, 30720\n\t"
"s_add_u32 s57, s58, 6144\n\t"
"buffer_load_dwordx4 v29, s[52:55], s57 offen offset:0 lds\n\t"
"buffer_load_dwordx4 v29, s[52:55], s57 offen offset:3072 lds\n\t"
"s_add_u32 m0, s59, 36864\n\t"
"s_add_u32 s57, s58, 221184\n\t"
"buffer_load_dwordx4 v29, s[52:55], s57 offen offset:0 lds\n\t"
"buffer_load_dwordx4 v29, s[52:55], s57 offen offset:3072 lds\n\t"
"s_add_u32 m0, s59, 82944\n\t"
"s_add_u32 s57, s58, 227328\n\t"
"buffer_load_dwordx4 v29, s[52:55], s57 offen offset:0 lds\n\t"
"buffer_load_dwordx4 v29, s[52:55], s57 offen offset:3072 lds\n\t"
"s_add_u32 m0, s59, 89088\n\t"
"s_add_u32 s57, s58, 442368\n\t"
"buffer_load_dwordx4 v29, s[52:55], s57 offen offset:0 lds\n\t"
"buffer_load_dwordx4 v29, s[52:55], s57 offen offset:3072 lds\n\t"
"s_add_u32 m0, s59, 142848\n\t"
"s_add_u32 s57, s58, 448512\n\t"
"buffer_load_dwordx4 v29, s[52:55], s57 offen offset:0 lds\n\t"
"buffer_load_dwordx4 v29, s[52:55], s57 offen offset:3072 lds\n\t"
"s_branch Lpro_%=\n\t"
"Lpa_%=:\n\t"
"s_mul_i32 s75, s63, 288\n\t"
"s_add_u32 s75, s75, s62\n\t"
"s_lshl2_add_u32 s75, s75, s60\n\t"
"s_cmp_lt_u32 s61, 2\n\t"
"s_cselect_b64 vcc, -1, 0\n\t"
"s_cselect_b32 s61, s61, 4\n\t"
"s_mulk_i32 s61, 24\n\t"
"s_add_u32 s75, s75, s61\n\t"
"s_lshl_b32 s75, s75, 8\n\t"
"v_and_b32 v99, 12, v205\n\t"
"v_and_b32 v60, 35, v205\n\t"
"v_lshl_or_b32 v60, v99, 1, v60\n\t"
"v_bfe_u32 v99, v205, 4, 1\n\t"
"v_lshl_or_b32 v60, v99, 2, v60\n\t"
"v_cndmask_b32 v60, v205, v60, vcc\n\t"
"v_lshlrev_b32 v60, 1, v60\n\t"
"v_add_u32 v60, 95232, v60\n\t"
"v_lshrrev_b32 v97, 5, v205\n\t"
"v_lshl_or_b32 v97, v99, 2, v97\n\t"
"v_lshrrev_b32 v99, 4, v205\n\t"
"v_cndmask_b32 v97, v99, v97, vcc\n\t"
"v_mul_u32_u24 v97, 3072, v97\n\t"
"v_and_b32 v99, 15, v205\n\t"
"v_lshl_add_u32 v97, v99, 4, v97\n\t"
"s_cmp_lt_u32 s63, 2\n\t"
"s_mov_b32 s56, 0x3f2aaaab\n\t"
"s_mov_b32 s57, 0x3fc00000\n\t"
"s_mov_b32 s58, 0x3ee38e39\n\t"
"s_mov_b32 s59, 0x40100000\n\t"
"s_mov_b32 s62, 0xbf54ad4b\n\t"
"s_mov_b32 s63, 0xbefc0fc1\n\t"
"s_mov_b32 s64, 0xbe7c0fc1\n\t"
"s_mov_b32 s65, 0x3ea80a81\n\t"
"s_mov_b32 s38, 0x3f638e39\n\t"
"s_mov_b32 s39, 0x3f638e39\n\t"
"s_cselect_b32 s38, 0x3ea80a81, s38\n\t"
"s_cselect_b32 s39, 0xbebd0bd1, s39\n\t"
"v_mov_b64 v[32:33], 0\n\t"
"v_mov_b64 v[34:35], 0\n\t"
"v_mov_b64 v[36:37], 0\n\t"
"v_mov_b64 v[38:39], 0\n\t"
"v_mov_b64 v[40:41], 0\n\t"
"v_mov_b64 v[42:43], 0\n\t"
"v_mov_b64 v[44:45], 0\n\t"
"v_mov_b64 v[46:47], 0\n\t"
"v_mov_b64 v[48:49], 0\n\t"
"v_mov_b64 v[50:51], 0\n\t"
"v_mov_b64 v[52:53], 0\n\t"
"v_mov_b64 v[54:55], 0\n\t"
"s_mul_i32 s25, s73, 19968\n\t"
"s_add_u32 s25, s25, 61440\n\t"
"s_bfe_u32 s41, %2, 0x40003\n\t"
"s_cmp_eq_u32 s41, 0\n\t"
"s_cselect_b32 s40, -1, 0xbfffffff\n\t"
"s_cmp_eq_u32 s41, 15\n\t"
"s_cselect_b32 s41, -1, 0xdfffffff\n\t"
"s_and_b32 s40, s40, s41\n\t"
"s_sub_u32 s42, s21, 384\n\t"
"s_mul_i32 s41, s73, 589824\n\t"
"s_add_u32 s42, s42, s41\n\t"
"s_and_b32 s41, s40, 536883584\n\t"
"s_and_b32 s40, s40, 2.0\n\t"
"v_bfe_u32 v26, v205, 2, 3\n\t"
"v_add_u32 v27, -1, v26\n\t"
"v_cmp_gt_u32 vcc, 6, v27\n\t"
"v_lshrrev_b32 v27, 1, v27\n\t"
"v_mul_u32_u24 v27, 196608, v27\n\t"
"v_and_b32 v20, 0x70, v28\n\t"
"v_add3_u32 v27, v27, v20, s42\n\t"
"v_bfe_i32 v20, v205, 2, 1\n\t"
"v_mov_b32 v21, s40\n\t"
"v_bfi_b32 v20, v20, s41, v21\n\t"
"v_add_u32 v27, v27, v20\n\t"
"v_bfrev_b32 v20, 8\n\t"
"v_cndmask_b32 v254, v20, v27, vcc\n\t"
"v_bfe_u32 v26, v205, 2, 3\n\t"
"v_bfe_u32 v27, v205, 5, 1\n\t"
"v_mul_u32_u24 v27, 3072, v27\n\t"
"v_lshl_add_u32 v27, v26, 7, v27\n\t"
"v_and_b32 v26, 6, v26\n\t"
"v_lshrrev_b32 v20, 4, v205\n\t"
"v_and_b32 v20, 4, v20\n\t"
"v_and_or_b32 v20, v205, 3, v20\n\t"
"v_xor_b32 v20, v20, v26\n\t"
"v_lshl_add_u32 v20, v20, 4, v27\n\t"
"v_lshrrev_b32 v21, 7, v205\n\t"
"v_lshl_add_u32 v20, v21, 10, v20\n\t"
"v_lshrrev_b32 v21, 5, v205\n\t"
"v_and_b32 v21, 6, v21\n\t"
"v_bfe_u32 v22, v205, 1, 1\n\t"
"v_or_b32 v21, v21, v22\n\t"
"v_xor_b32 v21, v21, v26\n\t"
"v_lshl_add_u32 v21, v21, 4, v27\n\t"
"v_and_b32 v22, 1, v205\n\t"
"v_lshl_add_u32 v21, v22, 3, v21\n\t"
"v_add_u32 v22, 142848, v20\n\t"
"v_add_u32 v23, 142848, v21\n\t"
"v_add_u32 v24, 82944, v20\n\t"
"v_add_u32 v25, 82944, v21\n\t"
"s_and_b32 s17, s17, 0xffff\n\t"
"s_mov_b32 s18, 100663296\n\t"
"s_mov_b32 s19, 0x20000\n\t"
"s_lshr_b32 s41, s73, 1\n\t"
"s_add_u32 s42, s21, 196608\n\t"
"s_mul_i32 s43, s41, 384\n\t"
"s_add_u32 s22, s42, s43\n\t"
"s_lshl1_add_u32 s40, s43, s22\n\t"
"s_mul_i32 s43, s41, 196608\n\t"
"s_add_u32 s23, s42, s43\n\t"
"s_mov_b32 s44, 0x0fff0fff\n\t"
"s_mov_b32 s45, 0x0fff0fff\n\t"
"s_mul_i32 s26, s41, 19968\n\t"
"s_add_u32 s26, s26, 141312\n\t"
"s_sub_u32 s43, s41, s73\n\t"
"s_add_u32 s43, s43, 4\n\t"
"s_mul_i32 s27, s43, 589824\n\t"
"s_bitcmp1_b32 s73, 0\n\t"
"s_mov_b32 s36, 0x0fff0000\n\t"
"s_cselect_b32 s36, 0xfff0, s36\n\t"
"s_mov_b32 s37, s36\n\t"
"s_mul_i32 s80, s73, 3072\n\t"
"s_add_u32 s80, s80, s8\n\t"
"s_addc_u32 s81, s9, 0x100000\n\t"
"s_mov_b32 s82, 0x1c0000\n\t"
"s_mov_b32 s83, 0x800000\n\t"
"s_cmp_lt_u32 s69, 216\n\t"
"s_cbranch_scc0 Lnf3_%=\n\t"
"s_waitcnt vmcnt(1)\n\t"
"ds_read_u16_d16_hi v32, v60 offset:0\n\t"
"ds_read_u16_d16_hi v33, v60 offset:128\n\t"
"ds_read_u16_d16_hi v40, v60 offset:1024\n\t"
"ds_read_u16_d16_hi v41, v60 offset:1152\n\t"
"ds_read_u16_d16_hi v48, v60 offset:2048\n\t"
"ds_read_u16_d16_hi v49, v60 offset:2176\n\t"
"ds_read_u16_d16_hi v34, v60 offset:256\n\t"
"ds_read_u16_d16_hi v35, v60 offset:384\n\t"
"ds_read_u16_d16_hi v42, v60 offset:1280\n\t"
"ds_read_u16_d16_hi v43, v60 offset:1408\n\t"
"ds_read_u16_d16_hi v50, v60 offset:2304\n\t"
"ds_read_u16_d16_hi v51, v60 offset:2432\n\t"
"ds_read_u16_d16_hi v36, v60 offset:512\n\t"
"ds_read_u16_d16_hi v37, v60 offset:640\n\t"
"ds_read_u16_d16_hi v44, v60 offset:1536\n\t"
"ds_read_u16_d16_hi v45, v60 offset:1664\n\t"
"ds_read_u16_d16_hi v52, v60 offset:2560\n\t"
"ds_read_u16_d16_hi v53, v60 offset:2688\n\t"
"ds_read_u16_d16_hi v38, v60 offset:768\n\t"
"ds_read_u16_d16_hi v39, v60 offset:896\n\t"
"ds_read_u16_d16_hi v46, v60 offset:1792\n\t"
"ds_read_u16_d16_hi v47, v60 offset:1920\n\t"
"ds_read_u16_d16_hi v54, v60 offset:2816\n\t"
"ds_read_u16_d16_hi v55, v60 offset:2944\n\t"
"s_waitcnt lgkmcnt(15)\n\t"
"v_pk_fma_f32 v[56:57], v[48:49], s[58:59], v[32:33] op_sel:[0,0,0] op_sel_hi:[1,0,1]\n\t"
"v_pk_fma_f32 v[58:59], v[48:49], s[58:59], v[32:33] op_sel:[0,1,0] op_sel_hi:[1,1,1]\n\t"
"v_pk_mul_f32 v[104:105], v[32:33], s[38:39] op_sel:[0,0] op_sel_hi:[1,0]\n\t"
"v_pk_mul_f32 v[114:115], v[48:49], s[38:39] op_sel:[0,1] op_sel_hi:[1,1]\n\t"
"v_pk_fma_f32 v[106:107], v[40:41], s[56:57], v[56:57] op_sel:[0,0,0] op_sel_hi:[1,0,1]\n\t"
"v_pk_fma_f32 v[108:109], v[40:41], s[56:57], v[56:57] op_sel:[0,0,0] op_sel_hi:[1,0,1] neg_lo:[1,0,0] neg_hi:[1,0,0]\n\t"
"v_pk_fma_f32 v[110:111], v[40:41], s[56:57], v[58:59] op_sel:[0,1,0] op_sel_hi:[1,1,1]\n\t"
"v_pk_fma_f32 v[112:113], v[40:41], s[56:57], v[58:59] op_sel:[0,1,0] op_sel_hi:[1,1,1] neg_lo:[1,0,0] neg_hi:[1,0,0]\n\t"
"v_pk_mul_f32 v[106:107], v[106:107], s[62:63] op_sel:[0,0] op_sel_hi:[1,0]\n\t"
"v_pk_mul_f32 v[108:109], v[108:109], s[62:63] op_sel:[0,1] op_sel_hi:[1,1]\n\t"
"v_pk_mul_f32 v[110:111], v[110:111], s[64:65] op_sel:[0,0] op_sel_hi:[1,0]\n\t"
"v_pk_mul_f32 v[112:113], v[112:113], s[64:65] op_sel:[0,1] op_sel_hi:[1,1]\n\t"
"v_cvt_pk_bf16_f32 v64, v104, v105\n\t"
"v_cvt_pk_bf16_f32 v84, v114, v115\n\t"
"v_cvt_pk_bf16_f32 v68, v106, v107\n\t"
"v_cvt_pk_bf16_f32 v72, v108, v109\n\t"
"v_cvt_pk_bf16_f32 v76, v110, v111\n\t"
"v_cvt_pk_bf16_f32 v80, v112, v113\n\t"
"s_waitcnt lgkmcnt(12)\n\t"
"v_pk_fma_f32 v[56:57], v[50:51], s[58:59], v[34:35] op_sel:[0,0,0] op_sel_hi:[1,0,1]\n\t"
"v_pk_fma_f32 v[58:59], v[50:51], s[58:59], v[34:35] op_sel:[0,1,0] op_sel_hi:[1,1,1]\n\t"
"v_pk_mul_f32 v[104:105], v[34:35], s[38:39] op_sel:[0,0] op_sel_hi:[1,0]\n\t"
"v_pk_mul_f32 v[114:115], v[50:51], s[38:39] op_sel:[0,1] op_sel_hi:[1,1]\n\t"
"v_pk_fma_f32 v[106:107], v[42:43], s[56:57], v[56:57] op_sel:[0,0,0] op_sel_hi:[1,0,1]\n\t"
"v_pk_fma_f32 v[108:109], v[42:43], s[56:57], v[56:57] op_sel:[0,0,0] op_sel_hi:[1,0,1] neg_lo:[1,0,0] neg_hi:[1,0,0]\n\t"
"v_pk_fma_f32 v[110:111], v[42:43], s[56:57], v[58:59] op_sel:[0,1,0] op_sel_hi:[1,1,1]\n\t"
"v_pk_fma_f32 v[112:113], v[42:43], s[56:57], v[58:59] op_sel:[0,1,0] op_sel_hi:[1,1,1] neg_lo:[1,0,0] neg_hi:[1,0,0]\n\t"
"v_pk_mul_f32 v[106:107], v[106:107], s[62:63] op_sel:[0,0] op_sel_hi:[1,0]\n\t"
"v_pk_mul_f32 v[108:109], v[108:109], s[62:63] op_sel:[0,1] op_sel_hi:[1,1]\n\t"
"v_pk_mul_f32 v[110:111], v[110:111], s[64:65] op_sel:[0,0] op_sel_hi:[1,0]\n\t"
"v_pk_mul_f32 v[112:113], v[112:113], s[64:65] op_sel:[0,1] op_sel_hi:[1,1]\n\t"
"v_cvt_pk_bf16_f32 v65, v104, v105\n\t"
"v_cvt_pk_bf16_f32 v85, v114, v115\n\t"
"v_cvt_pk_bf16_f32 v69, v106, v107\n\t"
"v_cvt_pk_bf16_f32 v73, v108, v109\n\t"
"v_cvt_pk_bf16_f32 v77, v110, v111\n\t"
"v_cvt_pk_bf16_f32 v81, v112, v113\n\t"
"s_waitcnt lgkmcnt(6)\n\t"
"v_pk_fma_f32 v[56:57], v[52:53], s[58:59], v[36:37] op_sel:[0,0,0] op_sel_hi:[1,0,1]\n\t"
"v_pk_fma_f32 v[58:59], v[52:53], s[58:59], v[36:37] op_sel:[0,1,0] op_sel_hi:[1,1,1]\n\t"
"v_pk_mul_f32 v[104:105], v[36:37], s[38:39] op_sel:[0,0] op_sel_hi:[1,0]\n\t"
"v_pk_mul_f32 v[114:115], v[52:53], s[38:39] op_sel:[0,1] op_sel_hi:[1,1]\n\t"
"v_pk_fma_f32 v[106:107], v[44:45], s[56:57], v[56:57] op_sel:[0,0,0] op_sel_hi:[1,0,1]\n\t"
"v_pk_fma_f32 v[108:109], v[44:45], s[56:57], v[56:57] op_sel:[0,0,0] op_sel_hi:[1,0,1] neg_lo:[1,0,0] neg_hi:[1,0,0]\n\t"
"v_pk_fma_f32 v[110:111], v[44:45], s[56:57], v[58:59] op_sel:[0,1,0] op_sel_hi:[1,1,1]\n\t"
"v_pk_fma_f32 v[112:113], v[44:45], s[56:57], v[58:59] op_sel:[0,1,0] op_sel_hi:[1,1,1] neg_lo:[1,0,0] neg_hi:[1,0,0]\n\t"
"v_pk_mul_f32 v[106:107], v[106:107], s[62:63] op_sel:[0,0] op_sel_hi:[1,0]\n\t"
"v_pk_mul_f32 v[108:109], v[108:109], s[62:63] op_sel:[0,1] op_sel_hi:[1,1]\n\t"
"v_pk_mul_f32 v[110:111], v[110:111], s[64:65] op_sel:[0,0] op_sel_hi:[1,0]\n\t"
"v_pk_mul_f32 v[112:113], v[112:113], s[64:65] op_sel:[0,1] op_sel_hi:[1,1]\n\t"
"v_cvt_pk_bf16_f32 v66, v104, v105\n\t"
"v_cvt_pk_bf16_f32 v86, v114, v115\n\t"
"v_cvt_pk_bf16_f32 v70, v106, v107\n\t"
"v_cvt_pk_bf16_f32 v74, v108, v109\n\t"
"v_cvt_pk_bf16_f32 v78, v110, v111\n\t"
"v_cvt_pk_bf16_f32 v82, v112, v113\n\t"
"s_waitcnt lgkmcnt(0)\n\t"
"v_pk_fma_f32 v[56:57], v[54:55], s[58:59], v[38:39] op_sel:[0,0,0] op_sel_hi:[1,0,1]\n\t"
"v_pk_fma_f32 v[58:59], v[54:55], s[58:59], v[38:39] op_sel:[0,1,0] op_sel_hi:[1,1,1]\n\t"
"v_pk_mul_f32 v[104:105], v[38:39], s[38:39] op_sel:[0,0] op_sel_hi:[1,0]\n\t"
"v_pk_mul_f32 v[114:115], v[54:55], s[38:39] op_sel:[0,1] op_sel_hi:[1,1]\n\t"
"v_pk_fma_f32 v[106:107], v[46:47], s[56:57], v[56:57] op_sel:[0,0,0] op_sel_hi:[1,0,1]\n\t"
"v_pk_fma_f32 v[108:109], v[46:47], s[56:57], v[56:57] op_sel:[0,0,0] op_sel_hi:[1,0,1] neg_lo:[1,0,0] neg_hi:[1,0,0]\n\t"
"v_pk_fma_f32 v[110:111], v[46:47], s[56:57], v[58:59] op_sel:[0,1,0] op_sel_hi:[1,1,1]\n\t"
"v_pk_fma_f32 v[112:113], v[46:47], s[56:57], v[58:59] op_sel:[0,1,0] op_sel_hi:[1,1,1] neg_lo:[1,0,0] neg_hi:[1,0,0]\n\t"
"v_pk_mul_f32 v[106:107], v[106:107], s[62:63] op_sel:[0,0] op_sel_hi:[1,0]\n\t"
"v_pk_mul_f32 v[108:109], v[108:109], s[62:63] op_sel:[0,1] op_sel_hi:[1,1]\n\t"
"v_pk_mul_f32 v[110:111], v[110:111], s[64:65] op_sel:[0,0] op_sel_hi:[1,0]\n\t"
"v_pk_mul_f32 v[112:113], v[112:113], s[64:65] op_sel:[0,1] op_sel_hi:[1,1]\n\t"
"v_cvt_pk_bf16_f32 v67, v104, v105\n\t"
"v_cvt_pk_bf16_f32 v87, v114, v115\n\t"
"v_cvt_pk_bf16_f32 v71, v106, v107\n\t"
"v_cvt_pk_bf16_f32 v75, v108, v109\n\t"
"v_cvt_pk_bf16_f32 v79, v110, v111\n\t"
"v_cvt_pk_bf16_f32 v83, v112, v113\n\t"
"s_add_u32 s60, s75, 0\n\t"
"buffer_store_dwordx4 v[64:67], v97, s[8:11], s60 offen sc1\n\t"
"s_add_u32 s60, s75, 184320\n\t"
"buffer_store_dwordx4 v[84:87], v97, s[8:11], s60 offen sc1\n\t"
"s_add_u32 s60, s75, 36864\n\t"
"buffer_store_dwordx4 v[68:71], v97, s[8:11], s60 offen sc1\n\t"
"s_add_u32 s60, s75, 73728\n\t"
"s_xor_b32 s60, s60, 256\n\t"
"buffer_store_dwordx4 v[72:75], v97, s[8:11], s60 offen sc1\n\t"
"s_add_u32 s60, s75, 110592\n\t"
"s_xor_b32 s60, s60, 512\n\t"
"buffer_store_dwordx4 v[76:79], v97, s[8:11], s60 offen sc1\n\t"
"s_add_u32 s60, s75, 147456\n\t"
"s_xor_b32 s60, s60, 768\n\t"
"buffer_store_dwordx4 v[80:83], v97, s[8:11], s60 offen sc1\n\t"
"s_waitcnt vmcnt(6)\n\t"
"s_branch Lnf4_%=\n\t"
"Lnf3_%=:\n\t"
"s_waitcnt vmcnt(0)\n\t"
"Lnf4_%=:\n\t"
"s_lshr_b32 s60, %2, 2\n\t"
"v_readlane_b32 s61, v220, s60\n\t"
"v_readlane_b32 s62, v221, s60\n\t"
"v_readlane_b32 s63, v222, s60\n\t"
"v_readlane_b32 s64, v223, s60\n\t"
"s_bitcmp1_b32 %2, 0\n\t"
"s_cselect_b32 s61, s62, s61\n\t"
"s_cselect_b32 s63, s64, s63\n\t"
"s_bitcmp1_b32 %2, 1\n\t"
"s_cselect_b32 s71, s63, s61\n\t"
"s_branch Ljoin_%=\n\t"
"Lpc_%=:\n\t"
"s_add_u32 m0, s24, 59904\n\t"
"s_add_u32 s28, s21, 1769472\n\t"
"buffer_load_dwordx4 v28, s[12:15], s28 offen offset:0 nt lds\n\t"
"s_add_u32 m0, s24, 66048\n\t"
"s_add_u32 s28, s21, 1966080\n\t"
"buffer_load_dwordx4 v28, s[12:15], s28 offen offset:0 nt lds\n\t"
"s_add_u32 m0, s24, 72192\n\t"
"s_add_u32 s28, s21, 2162688\n\t"
"buffer_load_dwordx4 v28, s[12:15], s28 offen offset:0 nt lds\n\t"
"s_mul_i32 s60, s59, 6\n\t"
"s_mul_i32 s61, s59, 192\n\t"
"s_add_u32 s61, s61, s21\n\t"
"s_mul_i32 s62, s73, 3072\n\t"
"v_subrev_u32 v30, s62, v28\n\t"
"s_add_u32 m0, s60, 102912\n\t"
"s_add_u32 s28, s61, 1769472\n\t"
"buffer_load_dwordx4 v30, s[12:15], s28 offen nt lds\n\t"
"s_add_u32 m0, s24, 79872\n\t"
"s_add_u32 s28, s21, 2359296\n\t"
"buffer_load_dwordx4 v28, s[12:15], s28 offen offset:0 nt lds\n\t"
"s_add_u32 m0, s24, 86016\n\t"
"s_add_u32 s28, s21, 2555904\n\t"
"buffer_load_dwordx4 v28, s[12:15], s28 offen offset:0 nt lds\n\t"
"s_add_u32 m0, s24, 92160\n\t"
"s_add_u32 s28, s21, 2752512\n\t"
"buffer_load_dwordx4 v28, s[12:15], s28 offen offset:0 nt lds\n\t"
"s_add_u32 m0, s60, 122880\n\t"
"s_add_u32 s28, s61, 2359296\n\t"
"buffer_load_dwordx4 v30, s[12:15], s28 offen nt lds\n\t"
"s_mul_i32 s25, s73, 19968\n\t"
"s_add_u32 s25, s25, 61440\n\t"
"s_bfe_u32 s41, %2, 0x40003\n\t"
"s_cmp_eq_u32 s41, 0\n\t"
"s_cselect_b32 s40, -1, 0xbfffffff\n\t"
"s_cmp_eq_u32 s41, 15\n\t"
"s_cselect_b32 s41, -1, 0xdfffffff\n\t"
"s_and_b32 s40, s40, s41\n\t"
"s_sub_u32 s42, s21, 384\n\t"
"s_mul_i32 s41, s73, 589824\n\t"
"s_add_u32 s42, s42, s41\n\t"
"s_and_b32 s41, s40, 536883584\n\t"
"s_and_b32 s40, s40, 2.0\n\t"
"v_bfe_u32 v26, v205, 2, 3\n\t"
"v_add_u32 v27, -1, v26\n\t"
"v_cmp_gt_u32 vcc, 6, v27\n\t"
"v_lshrrev_b32 v27, 1, v27\n\t"
"v_mul_u32_u24 v27, 196608, v27\n\t"
"v_and_b32 v20, 0x70, v28\n\t"
"v_add3_u32 v27, v27, v20, s42\n\t"
"v_bfe_i32 v20, v205, 2, 1\n\t"
"v_mov_b32 v21, s40\n\t"
"v_bfi_b32 v20, v20, s41, v21\n\t"
"v_add_u32 v27, v27, v20\n\t"
"v_bfrev_b32 v20, 8\n\t"
"v_cndmask_b32 v254, v20, v27, vcc\n\t"
"s_add_u32 m0, s25, 0\n\t"
"s_nop 0\n\t"
"buffer_load_dwordx4 v254, s[12:15], 0 offen nt lds\n\t"
"s_cmp_eq_u32 s73, 3\n\t"
"s_cselect_b32 s62, -1, 1\n\t"
"s_mul_i32 s63, s62, 59904\n\t"
"s_mul_i32 s62, s62, 1769472\n\t"
"v_add_u32 v19, s62, v254\n\t"
"s_add_u32 m0, s25, s63\n\t"
"s_nop 0\n\t"
"buffer_load_dwordx4 v19, s[12:15], 0 offen nt lds\n\t"
"v_bfe_u32 v26, v205, 2, 3\n\t"
"v_bfe_u32 v27, v205, 5, 1\n\t"
"v_mul_u32_u24 v27, 3072, v27\n\t"
"v_lshl_add_u32 v27, v26, 7, v27\n\t"
"v_and_b32 v26, 6, v26\n\t"
"v_lshrrev_b32 v20, 4, v205\n\t"
"v_and_b32 v20, 4, v20\n\t"
"v_and_or_b32 v20, v205, 3, v20\n\t"
"v_xor_b32 v20, v20, v26\n\t"
"v_lshl_add_u32 v20, v20, 4, v27\n\t"
"v_lshrrev_b32 v21, 7, v205\n\t"
"v_lshl_add_u32 v20, v21, 10, v20\n\t"
"v_lshrrev_b32 v21, 5, v205\n\t"
"v_and_b32 v21, 6, v21\n\t"
"v_bfe_u32 v22, v205, 1, 1\n\t"
"v_or_b32 v21, v21, v22\n\t"
"v_xor_b32 v21, v21, v26\n\t"
"v_lshl_add_u32 v21, v21, 4, v27\n\t"
"v_and_b32 v22, 1, v205\n\t"
"v_lshl_add_u32 v21, v22, 3, v21\n\t"
"v_add_u32 v22, 142848, v20\n\t"
"v_add_u32 v23, 142848, v21\n\t"
"v_add_u32 v24, 82944, v20\n\t"
"v_add_u32 v25, 82944, v21\n\t"
"s_and_b32 s17, s17, 0xffff\n\t"
"s_mov_b32 s18, 100663296\n\t"
"s_mov_b32 s19, 0x20000\n\t"
"s_lshr_b32 s41, s73, 1\n\t"
"s_add_u32 s42, s21, 196608\n\t"
"s_mul_i32 s43, s41, 384\n\t"
"s_add_u32 s22, s42, s43\n\t"
"s_lshl1_add_u32 s40, s43, s22\n\t"
"s_mul_i32 s43, s41, 196608\n\t"
"s_add_u32 s23, s42, s43\n\t"
"s_mov_b32 s44, 0x0fff0fff\n\t"
"s_mov_b32 s45, 0x0fff0fff\n\t"
"s_mul_i32 s26, s41, 19968\n\t"
"s_add_u32 s26, s26, 141312\n\t"
"s_sub_u32 s43, s41, s73\n\t"
"s_add_u32 s43, s43, 4\n\t"
"s_mul_i32 s27, s43, 589824\n\t"
"s_bitcmp1_b32 s73, 0\n\t"
"s_mov_b32 s36, 0x0fff0000\n\t"
"s_cselect_b32 s36, 0xfff0, s36\n\t"
"s_mov_b32 s37, s36\n\t"
"s_mul_i32 s80, s73, 3072\n\t"
"s_add_u32 s80, s80, s8\n\t"
"s_addc_u32 s81, s9, 0x100000\n\t"
"s_mov_b32 s82, 0x1c0000\n\t"
"s_mov_b32 s83, 0x800000\n\t"
"s_add_u32 m0, s59, 24576\n\t"
"s_add_u32 s57, s58, 0\n\t"
"buffer_load_dwordx4 v29, s[52:55], s57 offen offset:0 lds\n\t"
"buffer_load_dwordx4 v29, s[52:55], s57 offen offset:3072 lds\n\t"
"s_add_u32 m0, s59, 30720\n\t"
"s_add_u32 s57, s58, 6144\n\t"
"buffer_load_dwordx4 v29, s[52:55], s57 offen offset:0 lds\n\t"
"buffer_load_dwordx4 v29, s[52:55], s57 offen offset:3072 lds\n\t"
"s_add_u32 m0, s59, 36864\n\t"
"s_add_u32 s57, s58, 221184\n\t"
"buffer_load_dwordx4 v29, s[52:55], s57 offen offset:0 lds\n\t"
"buffer_load_dwordx4 v29, s[52:55], s57 offen offset:3072 lds\n\t"
"s_add_u32 m0, s59, 82944\n\t"
"s_add_u32 s57, s58, 227328\n\t"
"buffer_load_dwordx4 v29, s[52:55], s57 offen offset:0 lds\n\t"
"buffer_load_dwordx4 v29, s[52:55], s57 offen offset:3072 lds\n\t"
"s_add_u32 m0, s59, 89088\n\t"
"s_add_u32 s57, s58, 442368\n\t"
"buffer_load_dwordx4 v29, s[52:55], s57 offen offset:0 lds\n\t"
"buffer_load_dwordx4 v29, s[52:55], s57 offen offset:3072 lds\n\t"
"s_add_u32 m0, s59, 142848\n\t"
"s_add_u32 s57, s58, 448512\n\t"
"buffer_load_dwordx4 v29, s[52:55], s57 offen offset:0 lds\n\t"
"buffer_load_dwordx4 v29, s[52:55], s57 offen offset:3072 lds\n\t"
"s_add_u32 m0, s59, 148992\n\t"
"s_add_u32 s57, s58, 147456\n\t"
"buffer_load_dwordx4 v29, s[52:55], s57 offen offset:0 lds\n\t"
"buffer_load_dwordx4 v29, s[52:55], s57 offen offset:3072 lds\n\t"
"s_add_u32 m0, s59, 155136\n\t"
"s_add_u32 s57, s58, 153600\n\t"
"buffer_load_dwordx4 v29, s[52:55], s57 offen offset:0 lds\n\t"
"buffer_load_dwordx4 v29, s[52:55], s57 offen offset:3072 lds\n\t"
"s_add_u32 m0, s59, 0\n\t"
"s_add_u32 s57, s58, 368640\n\t"
"buffer_load_dwordx4 v29, s[52:55], s57 offen offset:0 lds\n\t"
"buffer_load_dwordx4 v29, s[52:55], s57 offen offset:3072 lds\n\t"
"s_add_u32 m0, s59, 6144\n\t"
"s_add_u32 s57, s58, 374784\n\t"
"buffer_load_dwordx4 v29, s[52:55], s57 offen offset:0 lds\n\t"
"buffer_load_dwordx4 v29, s[52:55], s57 offen offset:3072 lds\n\t"
"s_add_u32 m0, s59, 12288\n\t"
"s_add_u32 s57, s58, 589824\n\t"
"buffer_load_dwordx4 v29, s[52:55], s57 offen offset:0 lds\n\t"
"buffer_load_dwordx4 v29, s[52:55], s57 offen offset:3072 lds\n\t"
"s_add_u32 m0, s59, 18432\n\t"
"s_add_u32 s57, s58, 595968\n\t"
"buffer_load_dwordx4 v29, s[52:55], s57 offen offset:0 lds\n\t"
"buffer_load_dwordx4 v29, s[52:55], s57 offen offset:3072 lds\n\t"
"s_add_u32 m0, s24, 19968\n\t"
"s_add_u32 s28, s21, 589824\n\t"
"buffer_load_dwordx4 v28, s[12:15], s28 offen offset:0 nt lds\n\t"
"s_add_u32 m0, s24, 26112\n\t"
"s_add_u32 s28, s21, 786432\n\t"
"buffer_load_dwordx4 v28, s[12:15], s28 offen offset:0 nt lds\n\t"
"s_add_u32 m0, s24, 32256\n\t"
"s_add_u32 s28, s21, 983040\n\t"
"buffer_load_dwordx4 v28, s[12:15], s28 offen offset:0 nt lds\n\t"
"s_add_u32 m0, s60, 62976\n\t"
"s_add_u32 s28, s61, 589824\n\t"
"buffer_load_dwordx4 v30, s[12:15], s28 offen nt lds\n\t"
"s_add_u32 m0, s24, 0\n\t"
"s_add_u32 s28, s21, 0\n\t"
"buffer_load_dwordx4 v28, s[12:15], s28 offen offset:0 nt lds\n\t"
"s_add_u32 m0, s24, 6144\n\t"
"s_add_u32 s28, s21, 196608\n\t"
"buffer_load_dwordx4 v28, s[12:15], s28 offen offset:0 nt lds\n\t"
"s_add_u32 m0, s24, 12288\n\t"
"s_add_u32 s28, s21, 393216\n\t"
"buffer_load_dwordx4 v28, s[12:15], s28 offen offset:0 nt lds\n\t"
"s_add_u32 m0, s60, 43008\n\t"
"s_add_u32 s28, s61, 0\n\t"
"buffer_load_dwordx4 v30, s[12:15], s28 offen nt lds\n\t"
"s_waitcnt vmcnt(42)\n\t"
"s_branch Ljoin_%=\n\t"
"Lfb_%=:\n\t"
"s_waitcnt vmcnt(0) lgkmcnt(0)\n\t"
"s_nop 15\n\t"
"v_lshrrev_b32 v255, 9, v252\n\t"
"v_lshl_add_u32 v255, v255, 10, v252\n\t"
"v_add_u32 v255, 44032, v255\n\t"
"ds_write_b128 v255, v[32:35] offset:0\n\t"
"ds_write_b128 v255, v[36:39] offset:3072\n\t"
"ds_write_b128 v255, v[40:43] offset:6144\n\t"
"ds_write_b128 v255, v[44:47] offset:9216\n\t"
"ds_write_b128 v255, v[48:51] offset:12288\n\t"
"ds_write_b128 v255, v[52:55] offset:15360\n\t"
"ds_write_b128 v255, v[56:59] offset:18432\n\t"
"ds_write_b128 v255, v[60:63] offset:21504\n\t"
"ds_write_b128 v255, v[64:67] offset:24576\n\t"
"ds_write_b128 v255, v[68:71] offset:27648\n\t"
"ds_write_b128 v255, v[72:75] offset:30720\n\t"
"ds_write_b128 v255, v[76:79] offset:33792\n\t"
"ds_write_b128 v255, v[80:83] offset:36864\n\t"
"ds_write_b128 v255, v[84:87] offset:39936\n\t"
"ds_write_b128 v255, v[88:91] offset:43008\n\t"
"ds_write_b128 v255, v[92:95] offset:46080\n\t"
"ds_write_b128 v255, v[96:99] offset:49152\n\t"
"ds_write_b128 v255, v[100:103] offset:52224\n\t"
"ds_write_b128 v255, v[104:107] offset:55296\n\t"
"ds_write_b128 v255, v[108:111] offset:58368\n\t"
"ds_write_b128 v255, v[112:115] offset:61440\n\t"
"ds_write_b128 v255, v[116:119] offset:64512\n\t"
"s_waitcnt lgkmcnt(0)\n\t"
"v_mov_b64 v[32:33], 0\n\t"
"v_mov_b64 v[34:35], 0\n\t"
"v_mov_b64 v[36:37], 0\n\t"
"v_mov_b64 v[38:39], 0\n\t"
"v_mov_b64 v[40:41], 0\n\t"
"v_mov_b64 v[42:43], 0\n\t"
"v_mov_b64 v[44:45], 0\n\t"
"v_mov_b64 v[46:47], 0\n\t"
"v_mov_b64 v[48:49], 0\n\t"
"v_mov_b64 v[50:51], 0\n\t"
"v_mov_b64 v[52:53], 0\n\t"
"v_mov_b64 v[54:55], 0\n\t"
"v_lshrrev_b32 v98, 4, v252\n\t"
"v_bfe_u32 v96, v98, 3, 2\n\t"
"v_and_b32 v97, 32, v98\n\t"
"v_lshl_or_b32 v96, v96, 1, v97\n\t"
"v_mul_u32_u24 v96, 192, v96\n\t"
"v_and_b32 v97, 7, v98\n\t"
"v_lshl_add_u32 v96, v97, 4, v96\n\t"
"s_mov_b32 s69, 0\n\t"
"Lfb1_%=:\n\t"
"s_and_b32 s60, s69, 3\n\t"
"s_lshr_b32 s61, s69, 2\n\t"
"s_mul_i32 s62, s61, 171\n\t"
"s_lshr_b32 s62, s62, 9\n\t"
"s_mul_i32 s63, s62, 3\n\t"
"s_sub_u32 s61, s61, s63\n\t"
"s_mul_i32 s63, s62, 43\n\t"
"s_lshr_b32 s63, s63, 7\n\t"
"s_mul_i32 s64, s63, 3\n\t"
"s_sub_u32 s62, s62, s64\n\t"
"s_mul_i32 s74, s62, 18\n\t"
"s_add_u32 s74, s74, s63\n\t"
"s_lshl3_add_u32 s74, s74, s60\n\t"
"s_mul_i32 s74, s74, 12\n\t"
"s_add_u32 s74, s74, s61\n\t"
"s_lshl_b32 s74, s74, 7\n\t"
"s_mov_b32 m0, 18432\n\t"
"s_add_u32 s64, s74, 0\n\t"
"buffer_load_dwordx4 v96, s[52:55], s64 offen lds\n\t"
"s_mov_b32 m0, 19456\n\t"
"s_add_u32 s64, s74, 73728\n\t"
"buffer_load_dwordx4 v96, s[52:55], s64 offen lds\n\t"
"s_mov_b32 m0, 20480\n\t"
"s_add_u32 s64, s74, 147456\n\t"
"buffer_load_dwordx4 v96, s[52:55], s64 offen lds\n\t"
"s_mul_i32 s75, s63, 288\n\t"
"s_add_u32 s75, s75, s62\n\t"
"s_lshl2_add_u32 s75, s75, s60\n\t"
"s_cmp_lt_u32 s61, 2\n\t"
"s_cselect_b64 vcc, -1, 0\n\t"
"s_cselect_b32 s61, s61, 4\n\t"
"s_mulk_i32 s61, 24\n\t"
"s_add_u32 s75, s75, s61\n\t"
"s_lshl_b32 s75, s75, 8\n\t"
"v_and_b32 v99, 12, v98\n\t"
"v_and_b32 v60, 35, v98\n\t"
"v_lshl_or_b32 v60, v99, 1, v60\n\t"
"v_bfe_u32 v99, v98, 4, 1\n\t"
"v_lshl_or_b32 v60, v99, 2, v60\n\t"
"v_cndmask_b32 v60, v98, v60, vcc\n\t"
"v_lshlrev_b32 v60, 1, v60\n\t"
"v_add_u32 v60, 18432, v60\n\t"
"v_lshrrev_b32 v97, 5, v98\n\t"
"v_lshl_or_b32 v97, v99, 2, v97\n\t"
"v_lshrrev_b32 v99, 4, v98\n\t"
"v_cndmask_b32 v97, v99, v97, vcc\n\t"
"v_mul_u32_u24 v97, 3072, v97\n\t"
"v_and_b32 v99, 15, v98\n\t"
"v_lshl_add_u32 v97, v99, 4, v97\n\t"
"s_cmp_lt_u32 s63, 2\n\t"
"s_mov_b32 s56, 0x3f2aaaab\n\t"
"s_mov_b32 s57, 0x3fc00000\n\t"
"s_mov_b32 s58, 0x3ee38e39\n\t"
"s_mov_b32 s59, 0x40100000\n\t"
"s_mov_b32 s62, 0xbf54ad4b\n\t"
"s_mov_b32 s63, 0xbefc0fc1\n\t"
"s_mov_b32 s64, 0xbe7c0fc1\n\t"
"s_mov_b32 s65, 0x3ea80a81\n\t"
"s_mov_b32 s38, 0x3f638e39\n\t"
"s_mov_b32 s39, 0x3f638e39\n\t"
"s_cselect_b32 s38, 0x3ea80a81, s38\n\t"
"s_cselect_b32 s39, 0xbebd0bd1, s39\n\t"
"s_waitcnt vmcnt(0)\n\t"
"ds_read_u16_d16_hi v32, v60 offset:0\n\t"
"ds_read_u16_d16_hi v33, v60 offset:128\n\t"
"ds_read_u16_d16_hi v40, v60 offset:1024\n\t"
"ds_read_u16_d16_hi v41, v60 offset:1152\n\t"
"ds_read_u16_d16_hi v48, v60 offset:2048\n\t"
"ds_read_u16_d16_hi v49, v60 offset:2176\n\t"
"ds_read_u16_d16_hi v34, v60 offset:256\n\t"
"ds_read_u16_d16_hi v35, v60 offset:384\n\t"
"ds_read_u16_d16_hi v42, v60 offset:1280\n\t"
"ds_read_u16_d16_hi v43, v60 offset:1408\n\t"
"ds_read_u16_d16_hi v50, v60 offset:2304\n\t"
"ds_read_u16_d16_hi v51, v60 offset:2432\n\t"
"ds_read_u16_d16_hi v36, v60 offset:512\n\t"
"ds_read_u16_d16_hi v37, v60 offset:640\n\t"
"ds_read_u16_d16_hi v44, v60 offset:1536\n\t"
"ds_read_u16_d16_hi v45, v60 offset:1664\n\t"
"ds_read_u16_d16_hi v52, v60 offset:2560\n\t"
"ds_read_u16_d16_hi v53, v60 offset:2688\n\t"
"ds_read_u16_d16_hi v38, v60 offset:768\n\t"
"ds_read_u16_d16_hi v39, v60 offset:896\n\t"
"ds_read_u16_d16_hi v46, v60 offset:1792\n\t"
"ds_read_u16_d16_hi v47, v60 offset:1920\n\t"
"ds_read_u16_d16_hi v54, v60 offset:2816\n\t"
"ds_read_u16_d16_hi v55, v60 offset:2944\n\t"
"s_waitcnt lgkmcnt(15)\n\t"
"v_pk_fma_f32 v[56:57], v[48:49], s[58:59], v[32:33] op_sel:[0,0,0] op_sel_hi:[1,0,1]\n\t"
"v_pk_fma_f32 v[58:59], v[48:49], s[58:59], v[32:33] op_sel:[0,1,0] op_sel_hi:[1,1,1]\n\t"
"v_pk_mul_f32 v[104:105], v[32:33], s[38:39] op_sel:[0,0] op_sel_hi:[1,0]\n\t"
"v_pk_mul_f32 v[114:115], v[48:49], s[38:39] op_sel:[0,1] op_sel_hi:[1,1]\n\t"
"v_pk_fma_f32 v[106:107], v[40:41], s[56:57], v[56:57] op_sel:[0,0,0] op_sel_hi:[1,0,1]\n\t"
"v_pk_fma_f32 v[108:109], v[40:41], s[56:57], v[56:57] op_sel:[0,0,0] op_sel_hi:[1,0,1] neg_lo:[1,0,0] neg_hi:[1,0,0]\n\t"
"v_pk_fma_f32 v[110:111], v[40:41], s[56:57], v[58:59] op_sel:[0,1,0] op_sel_hi:[1,1,1]\n\t"
"v_pk_fma_f32 v[112:113], v[40:41], s[56:57], v[58:59] op_sel:[0,1,0] op_sel_hi:[1,1,1] neg_lo:[1,0,0] neg_hi:[1,0,0]\n\t"
"v_pk_mul_f32 v[106:107], v[106:107], s[62:63] op_sel:[0,0] op_sel_hi:[1,0]\n\t"
"v_pk_mul_f32 v[108:109], v[108:109], s[62:63] op_sel:[0,1] op_sel_hi:[1,1]\n\t"
"v_pk_mul_f32 v[110:111], v[110:111], s[64:65] op_sel:[0,0] op_sel_hi:[1,0]\n\t"
"v_pk_mul_f32 v[112:113], v[112:113], s[64:65] op_sel:[0,1] op_sel_hi:[1,1]\n\t"
"v_cvt_pk_bf16_f32 v64, v104, v105\n\t"
"v_cvt_pk_bf16_f32 v84, v114, v115\n\t"
"v_cvt_pk_bf16_f32 v68, v106, v107\n\t"
"v_cvt_pk_bf16_f32 v72, v108, v109\n\t"
"v_cvt_pk_bf16_f32 v76, v110, v111\n\t"
"v_cvt_pk_bf16_f32 v80, v112, v113\n\t"
"s_waitcnt lgkmcnt(12)\n\t"
"v_pk_fma_f32 v[56:57], v[50:51], s[58:59], v[34:35] op_sel:[0,0,0] op_sel_hi:[1,0,1]\n\t"
"v_pk_fma_f32 v[58:59], v[50:51], s[58:59], v[34:35] op_sel:[0,1,0] op_sel_hi:[1,1,1]\n\t"
"v_pk_mul_f32 v[104:105], v[34:35], s[38:39] op_sel:[0,0] op_sel_hi:[1,0]\n\t"
"v_pk_mul_f32 v[114:115], v[50:51], s[38:39] op_sel:[0,1] op_sel_hi:[1,1]\n\t"
"v_pk_fma_f32 v[106:107], v[42:43], s[56:57], v[56:57] op_sel:[0,0,0] op_sel_hi:[1,0,1]\n\t"
"v_pk_fma_f32 v[108:109], v[42:43], s[56:57], v[56:57] op_sel:[0,0,0] op_sel_hi:[1,0,1] neg_lo:[1,0,0] neg_hi:[1,0,0]\n\t"
"v_pk_fma_f32 v[110:111], v[42:43], s[56:57], v[58:59] op_sel:[0,1,0] op_sel_hi:[1,1,1]\n\t"
"v_pk_fma_f32 v[112:113], v[42:43], s[56:57], v[58:59] op_sel:[0,1,0] op_sel_hi:[1,1,1] neg_lo:[1,0,0] neg_hi:[1,0,0]\n\t"
"v_pk_mul_f32 v[106:107], v[106:107], s[62:63] op_sel:[0,0] op_sel_hi:[1,0]\n\t"
"v_pk_mul_f32 v[108:109], v[108:109], s[62:63] op_sel:[0,1] op_sel_hi:[1,1]\n\t"
"v_pk_mul_f32 v[110:111], v[110:111], s[64:65] op_sel:[0,0] op_sel_hi:[1,0]\n\t"
"v_pk_mul_f32 v[112:113], v[112:113], s[64:65] op_sel:[0,1] op_sel_hi:[1,1]\n\t"
"v_cvt_pk_bf16_f32 v65, v104, v105\n\t"
"v_cvt_pk_bf16_f32 v85, v114, v115\n\t"
"v_cvt_pk_bf16_f32 v69, v106, v107\n\t"
"v_cvt_pk_bf16_f32 v73, v108, v109\n\t"
"v_cvt_pk_bf16_f32 v77, v110, v111\n\t"
"v_cvt_pk_bf16_f32 v81, v112, v113\n\t"
"s_waitcnt lgkmcnt(6)\n\t"
"v_pk_fma_f32 v[56:57], v[52:53], s[58:59], v[36:37] op_sel:[0,0,0] op_sel_hi:[1,0,1]\n\t"
"v_pk_fma_f32 v[58:59], v[52:53], s[58:59], v[36:37] op_sel:[0,1,0] op_sel_hi:[1,1,1]\n\t"
"v_pk_mul_f32 v[104:105], v[36:37], s[38:39] op_sel:[0,0] op_sel_hi:[1,0]\n\t"
"v_pk_mul_f32 v[114:115], v[52:53], s[38:39] op_sel:[0,1] op_sel_hi:[1,1]\n\t"
"v_pk_fma_f32 v[106:107], v[44:45], s[56:57], v[56:57] op_sel:[0,0,0] op_sel_hi:[1,0,1]\n\t"
"v_pk_fma_f32 v[108:109], v[44:45], s[56:57], v[56:57] op_sel:[0,0,0] op_sel_hi:[1,0,1] neg_lo:[1,0,0] neg_hi:[1,0,0]\n\t"
"v_pk_fma_f32 v[110:111], v[44:45], s[56:57], v[58:59] op_sel:[0,1,0] op_sel_hi:[1,1,1]\n\t"
"v_pk_fma_f32 v[112:113], v[44:45], s[56:57], v[58:59] op_sel:[0,1,0] op_sel_hi:[1,1,1] neg_lo:[1,0,0] neg_hi:[1,0,0]\n\t"
"v_pk_mul_f32 v[106:107], v[106:107], s[62:63] op_sel:[0,0] op_sel_hi:[1,0]\n\t"
"v_pk_mul_f32 v[108:109], v[108:109], s[62:63] op_sel:[0,1] op_sel_hi:[1,1]\n\t"
"v_pk_mul_f32 v[110:111], v[110:111], s[64:65] op_sel:[0,0] op_sel_hi:[1,0]\n\t"
"v_pk_mul_f32 v[112:113], v[112:113], s[64:65] op_sel:[0,1] op_sel_hi:[1,1]\n\t"
"v_cvt_pk_bf16_f32 v66, v104, v105\n\t"
"v_cvt_pk_bf16_f32 v86, v114, v115\n\t"
"v_cvt_pk_bf16_f32 v70, v106, v107\n\t"
"v_cvt_pk_bf16_f32 v74, v108, v109\n\t"
"v_cvt_pk_bf16_f32 v78, v110, v111\n\t"
"v_cvt_pk_bf16_f32 v82, v112, v113\n\t"
"s_waitcnt lgkmcnt(0)\n\t"
"v_pk_fma_f32 v[56:57], v[54:55], s[58:59], v[38:39] op_sel:[0,0,0] op_sel_hi:[1,0,1]\n\t"
"v_pk_fma_f32 v[58:59], v[54:55], s[58:59], v[38:39] op_sel:[0,1,0] op_sel_hi:[1,1,1]\n\t"
"v_pk_mul_f32 v[104:105], v[38:39], s[38:39] op_sel:[0,0] op_sel_hi:[1,0]\n\t"
"v_pk_mul_f32 v[114:115], v[54:55], s[38:39] op_sel:[0,1] op_sel_hi:[1,1]\n\t"
"v_pk_fma_f32 v[106:107], v[46:47], s[56:57], v[56:57] op_sel:[0,0,0] op_sel_hi:[1,0,1]\n\t"
"v_pk_fma_f32 v[108:109], v[46:47], s[56:57], v[56:57] op_sel:[0,0,0] op_sel_hi:[1,0,1] neg_lo:[1,0,0] neg_hi:[1,0,0]\n\t"
"v_pk_fma_f32 v[110:111], v[46:47], s[56:57], v[58:59] op_sel:[0,1,0] op_sel_hi:[1,1,1]\n\t"
"v_pk_fma_f32 v[112:113], v[46:47], s[56:57], v[58:59] op_sel:[0,1,0] op_sel_hi:[1,1,1] neg_lo:[1,0,0] neg_hi:[1,0,0]\n\t"
"v_pk_mul_f32 v[106:107], v[106:107], s[62:63] op_sel:[0,0] op_sel_hi:[1,0]\n\t"
"v_pk_mul_f32 v[108:109], v[108:109], s[62:63] op_sel:[0,1] op_sel_hi:[1,1]\n\t"
"v_pk_mul_f32 v[110:111], v[110:111], s[64:65] op_sel:[0,0] op_sel_hi:[1,0]\n\t"
"v_pk_mul_f32 v[112:113], v[112:113], s[64:65] op_sel:[0,1] op_sel_hi:[1,1]\n\t"
"v_cvt_pk_bf16_f32 v67, v104, v105\n\t"
"v_cvt_pk_bf16_f32 v87, v114, v115\n\t"
"v_cvt_pk_bf16_f32 v71, v106, v107\n\t"
"v_cvt_pk_bf16_f32 v75, v108, v109\n\t"
"v_cvt_pk_bf16_f32 v79, v110, v111\n\t"
"v_cvt_pk_bf16_f32 v83, v112, v113\n\t"
"s_add_u32 s60, s75, 0\n\t"
"buffer_store_dwordx4 v[64:67], v97, s[8:11], s60 offen sc1\n\t"
"s_add_u32 s60, s75, 184320\n\t"
"buffer_store_dwordx4 v[84:87], v97, s[8:11], s60 offen sc1\n\t"
"s_add_u32 s60, s75, 36864\n\t"
"buffer_store_dwordx4 v[68:71], v97, s[8:11], s60 offen sc1\n\t"
"s_add_u32 s60, s75, 73728\n\t"
"s_xor_b32 s60, s60, 256\n\t"
"buffer_store_dwordx4 v[72:75], v97, s[8:11], s60 offen sc1\n\t"
"s_add_u32 s60, s75, 110592\n\t"
"s_xor_b32 s60, s60, 512\n\t"
"buffer_store_dwordx4 v[76:79], v97, s[8:11], s60 offen sc1\n\t"
"s_add_u32 s60, s75, 147456\n\t"
"s_xor_b32 s60, s60, 768\n\t"
"buffer_store_dwordx4 v[80:83], v97, s[8:11], s60 offen sc1\n\t"
"s_waitcnt vmcnt(0)\n\t"
"s_add_u32 s69, s69, 1\n\t"
"s_cmp_lt_u32 s69, 216\n\t"
"s_cbranch_scc1 Lfb1_%=\n\t"
"s_mov_b32 s69, %2\n\t"
"s_mov_b64 vcc, 0\n\t"
"ds_read_b128 v[32:35], v255 offset:0\n\t"
"ds_read_b128 v[36:39], v255 offset:3072\n\t"
"ds_read_b128 v[40:43], v255 offset:6144\n\t"
"ds_read_b128 v[44:47], v255 offset:9216\n\t"
"ds_read_b128 v[48:51], v255 offset:12288\n\t"
"ds_read_b128 v[52:55], v255 offset:15360\n\t"
"ds_read_b128 v[56:59], v255 offset:18432\n\t"
"ds_read_b128 v[60:63], v255 offset:21504\n\t"
"ds_read_b128 v[64:67], v255 offset:24576\n\t"
"ds_read_b128 v[68:71], v255 offset:27648\n\t"
"ds_read_b128 v[72:75], v255 offset:30720\n\t"
"ds_read_b128 v[76:79], v255 offset:33792\n\t"
"ds_read_b128 v[80:83], v255 offset:36864\n\t"
"ds_read_b128 v[84:87], v255 offset:39936\n\t"
"ds_read_b128 v[88:91], v255 offset:43008\n\t"
"ds_read_b128 v[92:95], v255 offset:46080\n\t"
"ds_read_b128 v[96:99], v255 offset:49152\n\t"
"ds_read_b128 v[100:103], v255 offset:52224\n\t"
"ds_read_b128 v[104:107], v255 offset:55296\n\t"
"ds_read_b128 v[108:111], v255 offset:58368\n\t"
"ds_read_b128 v[112:115], v255 offset:61440\n\t"
"ds_read_b128 v[116:119], v255 offset:64512\n\t"
"s_waitcnt lgkmcnt(0)\n\t"
"s_branch Lgo_%=\n\t"
".p2align 12\n\t"
"Lpro_%=:\n\t"
"s_mul_i32 s25, s73, 19968\n\t"
"s_add_u32 s25, s25, 61440\n\t"
"s_bfe_u32 s41, %2, 0x40003\n\t"
"s_cmp_eq_u32 s41, 0\n\t"
"s_cselect_b32 s40, -1, 0xbfffffff\n\t"
"s_cmp_eq_u32 s41, 15\n\t"
"s_cselect_b32 s41, -1, 0xdfffffff\n\t"
"s_and_b32 s40, s40, s41\n\t"
"s_sub_u32 s42, s21, 384\n\t"
"s_mul_i32 s41, s73, 589824\n\t"
"s_add_u32 s42, s42, s41\n\t"
"s_and_b32 s41, s40, 536883584\n\t"
"s_and_b32 s40, s40, 2.0\n\t"
"v_bfe_u32 v26, v205, 2, 3\n\t"
"v_add_u32 v27, -1, v26\n\t"
"v_cmp_gt_u32 vcc, 6, v27\n\t"
"v_lshrrev_b32 v27, 1, v27\n\t"
"v_mul_u32_u24 v27, 196608, v27\n\t"
"v_and_b32 v20, 0x70, v28\n\t"
"v_add3_u32 v27, v27, v20, s42\n\t"
"v_bfe_i32 v20, v205, 2, 1\n\t"
"v_mov_b32 v21, s40\n\t"
"v_bfi_b32 v20, v20, s41, v21\n\t"
"v_add_u32 v27, v27, v20\n\t"
"v_bfrev_b32 v20, 8\n\t"
"v_cndmask_b32 v254, v20, v27, vcc\n\t"
"v_bfe_u32 v26, v205, 2, 3\n\t"
"v_bfe_u32 v27, v205, 5, 1\n\t"
"v_mul_u32_u24 v27, 3072, v27\n\t"
"v_lshl_add_u32 v27, v26, 7, v27\n\t"
"v_and_b32 v26, 6, v26\n\t"
"v_lshrrev_b32 v20, 4, v205\n\t"
"v_and_b32 v20, 4, v20\n\t"
"v_and_or_b32 v20, v205, 3, v20\n\t"
"v_xor_b32 v20, v20, v26\n\t"
"v_lshl_add_u32 v20, v20, 4, v27\n\t"
"v_lshrrev_b32 v21, 7, v205\n\t"
"v_lshl_add_u32 v20, v21, 10, v20\n\t"
"v_lshrrev_b32 v21, 5, v205\n\t"
"v_and_b32 v21, 6, v21\n\t"
"v_bfe_u32 v22, v205, 1, 1\n\t"
"v_or_b32 v21, v21, v22\n\t"
"v_xor_b32 v21, v21, v26\n\t"
"v_lshl_add_u32 v21, v21, 4, v27\n\t"
"v_and_b32 v22, 1, v205\n\t"
"v_lshl_add_u32 v21, v22, 3, v21\n\t"
"v_add_u32 v22, 142848, v20\n\t"
"v_add_u32 v23, 142848, v21\n\t"
"v_add_u32 v24, 82944, v20\n\t"
"v_add_u32 v25, 82944, v21\n\t"
"s_and_b32 s17, s17, 0xffff\n\t"
"s_mov_b32 s18, 100663296\n\t"
"s_mov_b32 s19, 0x20000\n\t"
"s_lshr_b32 s41, s73, 1\n\t"
"s_add_u32 s42, s21, 196608\n\t"
"s_mul_i32 s43, s41, 384\n\t"
"s_add_u32 s22, s42, s43\n\t"
"s_lshl1_add_u32 s40, s43, s22\n\t"
"s_mul_i32 s43, s41, 196608\n\t"
"s_add_u32 s23, s42, s43\n\t"
"s_mov_b32 s44, 0x0fff0fff\n\t"
"s_mov_b32 s45, 0x0fff0fff\n\t"
"s_mul_i32 s26, s41, 19968\n\t"
"s_add_u32 s26, s26, 141312\n\t"
"s_sub_u32 s43, s41, s73\n\t"
"s_add_u32 s43, s43, 4\n\t"
"s_mul_i32 s27, s43, 589824\n\t"
"s_bitcmp1_b32 s73, 0\n\t"
"s_mov_b32 s36, 0x0fff0000\n\t"
"s_cselect_b32 s36, 0xfff0, s36\n\t"
"s_mov_b32 s37, s36\n\t"
"s_mul_i32 s80, s73, 3072\n\t"
"s_add_u32 s80, s80, s8\n\t"
"s_addc_u32 s81, s9, 0x100000\n\t"
"s_mov_b32 s82, 0x1c0000\n\t"
"s_mov_b32 s83, 0x800000\n\t"
"s_add_u32 m0, s59, 148992\n\t"
"s_add_u32 s57, s58, 147456\n\t"
"buffer_load_dwordx4 v29, s[52:55], s57 offen offset:0 lds\n\t"
"buffer_load_dwordx4 v29, s[52:55], s57 offen offset:3072 lds\n\t"
"s_add_u32 m0, s59, 155136\n\t"
"s_add_u32 s57, s58, 153600\n\t"
"buffer_load_dwordx4 v29, s[52:55], s57 offen offset:0 lds\n\t"
"buffer_load_dwordx4 v29, s[52:55], s57 offen offset:3072 lds\n\t"
"s_add_u32 m0, s59, 0\n\t"
"s_add_u32 s57, s58, 368640\n\t"
"buffer_load_dwordx4 v29, s[52:55], s57 offen offset:0 lds\n\t"
"buffer_load_dwordx4 v29, s[52:55], s57 offen offset:3072 lds\n\t"
"s_add_u32 m0, s59, 6144\n\t"
"s_add_u32 s57, s58, 374784\n\t"
"buffer_load_dwordx4 v29, s[52:55], s57 offen offset:0 lds\n\t"
"buffer_load_dwordx4 v29, s[52:55], s57 offen offset:3072 lds\n\t"
"s_add_u32 m0, s59, 12288\n\t"
"s_add_u32 s57, s58, 589824\n\t"
"buffer_load_dwordx4 v29, s[52:55], s57 offen offset:0 lds\n\t"
"buffer_load_dwordx4 v29, s[52:55], s57 offen offset:3072 lds\n\t"
"s_add_u32 m0, s59, 18432\n\t"
"s_add_u32 s57, s58, 595968\n\t"
"buffer_load_dwordx4 v29, s[52:55], s57 offen offset:0 lds\n\t"
"buffer_load_dwordx4 v29, s[52:55], s57 offen offset:3072 lds\n\t"
"s_add_u32 m0, s25, 0\n\t"
"s_nop 0\n\t"
"buffer_load_dwordx4 v254, s[12:15], 0 offen nt lds\n\t"
"s_cmp_eq_u32 s73, 3\n\t"
"s_cselect_b32 s62, -1, 1\n\t"
"s_mul_i32 s63, s62, 59904\n\t"
"s_mul_i32 s62, s62, 1769472\n\t"
"v_add_u32 v19, s62, v254\n\t"
"s_add_u32 m0, s25, s63\n\t"
"s_nop 0\n\t"
"buffer_load_dwordx4 v19, s[12:15], 0 offen nt lds\n\t"
"s_add_u32 m0, s24, 59904\n\t"
"s_add_u32 s28, s21, 1769472\n\t"
"buffer_load_dwordx4 v28, s[12:15], s28 offen offset:0 nt lds\n\t"
"s_add_u32 m0, s24, 66048\n\t"
"s_add_u32 s28, s21, 1966080\n\t"
"buffer_load_dwordx4 v28, s[12:15], s28 offen offset:0 nt lds\n\t"
"s_add_u32 m0, s24, 72192\n\t"
"s_add_u32 s28, s21, 2162688\n\t"
"buffer_load_dwordx4 v28, s[12:15], s28 offen offset:0 nt lds\n\t"
"s_mul_i32 s60, s59, 6\n\t"
"s_mul_i32 s61, s59, 192\n\t"
"s_add_u32 s61, s61, s21\n\t"
"s_mul_i32 s62, s73, 3072\n\t"
"v_subrev_u32 v30, s62, v28\n\t"
"s_add_u32 m0, s60, 102912\n\t"
"s_add_u32 s28, s61, 1769472\n\t"
"buffer_load_dwordx4 v30, s[12:15], s28 offen nt lds\n\t"
"s_add_u32 m0, s24, 79872\n\t"
"s_add_u32 s28, s21, 2359296\n\t"
"buffer_load_dwordx4 v28, s[12:15], s28 offen offset:0 nt lds\n\t"
"s_add_u32 m0, s24, 86016\n\t"
"s_add_u32 s28, s21, 2555904\n\t"
"buffer_load_dwordx4 v28, s[12:15], s28 offen offset:0 nt lds\n\t"
"s_add_u32 m0, s24, 92160\n\t"
"s_add_u32 s28, s21, 2752512\n\t"
"buffer_load_dwordx4 v28, s[12:15], s28 offen offset:0 nt lds\n\t"
"s_add_u32 m0, s60, 122880\n\t"
"s_add_u32 s28, s61, 2359296\n\t"
"buffer_load_dwordx4 v30, s[12:15], s28 offen nt lds\n\t"
"s_add_u32 m0, s24, 19968\n\t"
"s_add_u32 s28, s21, 589824\n\t"
"buffer_load_dwordx4 v28, s[12:15], s28 offen offset:0 nt lds\n\t"
"s_add_u32 m0, s24, 26112\n\t"
"s_add_u32 s28, s21, 786432\n\t"
"buffer_load_dwordx4 v28, s[12:15], s28 offen offset:0 nt lds\n\t"
"s_add_u32 m0, s24, 32256\n\t"
"s_add_u32 s28, s21, 983040\n\t"
"buffer_load_dwordx4 v28, s[12:15], s28 offen offset:0 nt lds\n\t"
"s_add_u32 m0, s60, 62976\n\t"
"s_add_u32 s28, s61, 589824\n\t"
"buffer_load_dwordx4 v30, s[12:15], s28 offen nt lds\n\t"
"s_add_u32 m0, s24, 0\n\t"
"s_add_u32 s28, s21, 0\n\t"
"buffer_load_dwordx4 v28, s[12:15], s28 offen offset:0 nt lds\n\t"
"s_add_u32 m0, s24, 6144\n\t"
"s_add_u32 s28, s21, 196608\n\t"
"buffer_load_dwordx4 v28, s[12:15], s28 offen offset:0 nt lds\n\t"
"s_add_u32 m0, s24, 12288\n\t"
"s_add_u32 s28, s21, 393216\n\t"
"buffer_load_dwordx4 v28, s[12:15], s28 offen offset:0 nt lds\n\t"
"s_add_u32 m0, s60, 43008\n\t"
"s_add_u32 s28, s61, 0\n\t"
"buffer_load_dwordx4 v30, s[12:15], s28 offen nt lds\n\t"
"s_waitcnt vmcnt(42)\n\t"
"Ljoin_%=:\n\t"
"s_mov_b32 s41, 0xc0423f90\n\t"
"s_movk_i32 s43, 0x101\n\t"
"v_mov_b32 v6, 0x01000100\n\t"
"v_mov_b32 v7, 0xbebd0bd1\n\t"
"v_lshrrev_b32 v240, 16, v244\n\t"
"v_lshrrev_b32 v242, 16, v245\n\t"
"v_xor_b32 v241, 32, v240\n\t"
"v_and_b32 v244, 0xffff, v244\n\t"
"v_and_b32 v245, 0xffff, v245\n\t"
"v_cmp_le_u32 vcc, 9216, v242\n\t"
"v_mov_b32 v243, 0x3ea80a81\n\t"
"v_cndmask_b32 v243, v243, v7, vcc\n\t"
"v_and_b32 v8, 0xf000f, v247\n\t"
"v_mad_u32_u24 v8, v8, s43, v6\n\t"
"v_perm_b32 v196, s41, 0, v8\n\t"
"v_lshrrev_b32 v9, 4, v247\n\t"
"v_and_b32 v9, 0xf000f, v9\n\t"
"v_mad_u32_u24 v9, v9, s43, v6\n\t"
"v_perm_b32 v197, s41, 0, v9\n\t"
"v_lshrrev_b32 v10, 8, v247\n\t"
"v_and_b32 v10, 0xf000f, v10\n\t"
"v_mad_u32_u24 v10, v10, s43, v6\n\t"
"v_perm_b32 v198, s41, 0, v10\n\t"
"v_lshrrev_b32 v11, 12, v247\n\t"
"v_and_b32 v11, 0xf000f, v11\n\t"
"v_mad_u32_u24 v11, v11, s43, v6\n\t"
"v_perm_b32 v199, s41, 0, v11\n\t"
"v_mov_b32 v247, v28\n\t"
"s_mov_b64 vcc, -1\n\t"
"s_bitcmp1_b32 s73, 1\n\t"
"s_cbranch_scc1 Lvh0_%=\n\t"
"s_bitcmp1_b32 s73, 0\n\t"
"s_cbranch_scc1 Lpg2_%=\n\t"
"s_branch Lpg1_%=\n\t"
"Lvh0_%=:\n\t"
"s_bitcmp1_b32 s73, 0\n\t"
"s_cbranch_scc1 Lpg4_%=\n\t"
"s_branch Lpg3_%=\n\t"
"Lvr0_%=:\n\t"
"s_mov_b64 vcc, 0\n\t"
"v_add_u32 v240, 59904, v240\n\t"
"v_add_u32 v241, 59904, v241\n\t"
"s_cmp_lt_u32 s69, 216\n\t"
"s_cbranch_scc0 Lna1_%=\n\t"
"s_waitcnt vmcnt(0)\n\t"
"s_add_u32 s71, s71, 1\n\t"
"v_mov_b32 v31, s71\n\t"
"s_mov_b64 exec, 1\n\t"
"buffer_store_dword v31, off, s[8:11], s70 sc1\n\t"
"s_mov_b64 exec, -1\n\t"
"Lna1_%=:\n\t"
"s_cmp_eq_u32 s73, 0\n\t"
"s_cbranch_scc1 Lsk0_%=\n\t"
"s_lshr_b32 s56, %2, 3\n\t"
"s_bitcmp1_b32 0xaaaaaaaa, s56\n\t"
"s_cbranch_scc1 Lkc2_%=\n\t"
"s_waitcnt vmcnt(8)\n\t"
"s_branch Lke2_%=\n\t"
"Lkc2_%=:\n\t"
"s_waitcnt vmcnt(32)\n\t"
"Lke2_%=:\n\t"
"Lsk0_%=:\n\t"
"s_barrier\n\t"
"ds_read_b64_tr_b16 v[120:121], v240 offset:0\n\t"
"ds_read_b64_tr_b16 v[122:123], v240 offset:1536\n\t"
"ds_read_b64_tr_b16 v[124:125], v241 offset:0\n\t"
"ds_read_b64_tr_b16 v[126:127], v241 offset:1536\n\t"
"ds_read_b64_tr_b16 v[128:129], v240 offset:19968\n\t"
"ds_read_b64_tr_b16 v[130:131], v240 offset:21504\n\t"
"ds_read_b64_tr_b16 v[132:133], v241 offset:19968\n\t"
"ds_read_b64_tr_b16 v[134:135], v241 offset:21504\n\t"
"s_waitcnt lgkmcnt(6)\n\t"
"v_mfma_f32_16x16x32_bf16 v[120:123], v[120:123], v[196:199], 0\n\t"
"s_waitcnt lgkmcnt(4)\n\t"
"v_mfma_f32_16x16x32_bf16 v[124:127], v[124:127], v[196:199], 0\n\t"
"s_waitcnt lgkmcnt(2)\n\t"
"v_mfma_f32_16x16x32_bf16 v[128:131], v[128:131], v[196:199], 0\n\t"
"s_waitcnt lgkmcnt(0)\n\t"
"v_mfma_f32_16x16x32_bf16 v[132:135], v[132:135], v[196:199], 0\n\t"
"v_pk_mul_f32 v[120:121], v[120:121], v[242:243] op_sel:[0,1] op_sel_hi:[1,1]\n\t"
"v_pk_mul_f32 v[122:123], v[122:123], v[242:243] op_sel:[0,1] op_sel_hi:[1,1]\n\t"
"v_cvt_pk_bf16_f32 v120, v120, v121\n\t"
"v_cvt_pk_bf16_f32 v121, v122, v123\n\t"
"v_pk_mul_f32 v[124:125], v[124:125], v[242:243] op_sel:[0,1] op_sel_hi:[1,1]\n\t"
"v_pk_mul_f32 v[126:127], v[126:127], v[242:243] op_sel:[0,1] op_sel_hi:[1,1]\n\t"
"v_cvt_pk_bf16_f32 v122, v124, v125\n\t"
"v_cvt_pk_bf16_f32 v123, v126, v127\n\t"
"v_pk_mul_f32 v[128:129], v[128:129], v[242:243] op_sel:[0,1] op_sel_hi:[1,1]\n\t"
"v_pk_mul_f32 v[130:131], v[130:131], v[242:243] op_sel:[0,1] op_sel_hi:[1,1]\n\t"
"v_cvt_pk_bf16_f32 v128, v128, v129\n\t"
"v_cvt_pk_bf16_f32 v129, v130, v131\n\t"
"v_pk_mul_f32 v[132:133], v[132:133], v[242:243] op_sel:[0,1] op_sel_hi:[1,1]\n\t"
"v_pk_mul_f32 v[134:135], v[134:135], v[242:243] op_sel:[0,1] op_sel_hi:[1,1]\n\t"
"v_cvt_pk_bf16_f32 v130, v132, v133\n\t"
"v_cvt_pk_bf16_f32 v131, v134, v135\n\t"
"s_cmp_eq_u32 s73, 0\n\t"
"s_cbranch_scc1 Lsk1_%=\n\t"
"s_lshr_b32 s56, %2, 3\n\t"
"s_bitcmp1_b32 0xaaaaaaaa, s56\n\t"
"s_cbranch_scc1 Lkc3_%=\n\t"
"s_branch Lke3_%=\n\t"
"Lkc3_%=:\n\t"
"s_waitcnt vmcnt(8)\n\t"
"Lke3_%=:\n\t"
"Lsk1_%=:\n\t"
"s_barrier\n\t"
"ds_read_b64_tr_b16 a[0:1], v20 offset:24576\n\t"
"ds_read_b64_tr_b16 a[2:3], v20 offset:30720\n\t"
"ds_read_b64_tr_b16 a[24:25], v20 offset:24584\n\t"
"ds_read_b64_tr_b16 a[26:27], v20 offset:30728\n\t"
"ds_read_b64_tr_b16 a[48:49], v21 offset:26624\n\t"
"ds_read_b64_tr_b16 a[50:51], v21 offset:32768\n\t"
"ds_read_b64_tr_b16 a[8:9], v20 offset:36864\n\t"
"ds_read_b64_tr_b16 a[10:11], v24 offset:0\n\t"
"ds_read_b64_tr_b16 a[32:33], v20 offset:36872\n\t"
"ds_read_b64_tr_b16 a[34:35], v24 offset:8\n\t"
"ds_read_b64_tr_b16 a[56:57], v21 offset:38912\n\t"
"ds_read_b64_tr_b16 a[58:59], v25 offset:2048\n\t"
"ds_read_b64_tr_b16 a[16:17], v24 offset:6144\n\t"
"ds_read_b64_tr_b16 a[18:19], v22 offset:0\n\t"
"ds_read_b64_tr_b16 a[40:41], v24 offset:6152\n\t"
"ds_read_b64_tr_b16 a[42:43], v22 offset:8\n\t"
"ds_read_b64_tr_b16 a[64:65], v25 offset:8192\n\t"
"ds_read_b64_tr_b16 a[66:67], v23 offset:2048\n\t"
"ds_read_b64_tr_b16 a[72:73], v22 offset:6144\n\t"
"ds_read_b64_tr_b16 a[74:75], v22 offset:12288\n\t"
"ds_read_b64_tr_b16 a[96:97], v22 offset:6152\n\t"
"ds_read_b64_tr_b16 a[98:99], v22 offset:12296\n\t"
"ds_read_b64_tr_b16 a[120:121], v23 offset:8192\n\t"
"ds_read_b64_tr_b16 a[122:123], v23 offset:14336\n\t"
"s_waitcnt lgkmcnt(0)\n\t"
"s_barrier\n\t"
"s_cmp_eq_u32 s73, 0\n\t"
"s_cbranch_scc1 Lsk2_%=\n\t"
"s_add_u32 m0, s24, 39936\n\t"
"s_add_u32 s28, s21, 1179648\n\t"
"buffer_load_dwordx4 v28, s[12:15], s28 offen offset:0 nt lds\n\t"
"s_add_u32 m0, s24, 46080\n\t"
"s_add_u32 s28, s21, 1376256\n\t"
"buffer_load_dwordx4 v28, s[12:15], s28 offen offset:0 nt lds\n\t"
"s_add_u32 m0, s24, 52224\n\t"
"s_add_u32 s28, s21, 1572864\n\t"
"buffer_load_dwordx4 v28, s[12:15], s28 offen offset:0 nt lds\n\t"
"s_add_u32 m0, s60, 82944\n\t"
"s_add_u32 s28, s61, 1179648\n\t"
"buffer_load_dwordx4 v30, s[12:15], s28 offen nt lds\n\t"
"Lsk2_%=:\n\t"
"ds_read_b64_tr_b16 a[80:81], v20 offset:0\n\t"
"ds_read_b64_tr_b16 a[82:83], v20 offset:6144\n\t"
"ds_read_b64_tr_b16 a[104:105], v20 offset:8\n\t"
"ds_read_b64_tr_b16 a[106:107], v20 offset:6152\n\t"
"ds_read_b64_tr_b16 a[128:129], v21 offset:2048\n\t"
"ds_read_b64_tr_b16 a[130:131], v21 offset:8192\n\t"
"ds_read_b64_tr_b16 a[88:89], v20 offset:12288\n\t"
"ds_read_b64_tr_b16 a[90:91], v20 offset:18432\n\t"
"ds_read_b64_tr_b16 a[112:113], v20 offset:12296\n\t"
"ds_read_b64_tr_b16 a[114:115], v20 offset:18440\n\t"
"ds_read_b64_tr_b16 a[136:137], v21 offset:14336\n\t"
"ds_read_b64_tr_b16 a[138:139], v21 offset:20480\n\t"
"ds_write_b128 v242, v[120:123] offset:29184\n\t"
"ds_write_b128 v242, v[128:131] offset:30720\n\t"
"s_waitcnt lgkmcnt(0)\n\t"
"v_subrev_u32 v240, 59904, v240\n\t"
"v_subrev_u32 v241, 59904, v241\n\t"
"s_cmp_eq_u32 s73, 0\n\t"
"s_cbranch_scc1 Lsk3_%=\n\t"
"s_waitcnt vmcnt(4)\n\t"
"Lsk3_%=:\n\t"
"s_barrier\n\t"
"ds_read_b64_tr_b16 v[136:137], v240 offset:19968\n\t"
"ds_read_b64_tr_b16 v[138:139], v240 offset:21504\n\t"
"ds_read_b64_tr_b16 v[140:141], v241 offset:19968\n\t"
"ds_read_b64_tr_b16 v[142:143], v241 offset:21504\n\t"
"ds_read_b64_tr_b16 v[144:145], v240 offset:0\n\t"
"ds_read_b64_tr_b16 v[146:147], v240 offset:1536\n\t"
"ds_read_b64_tr_b16 v[148:149], v241 offset:0\n\t"
"ds_read_b64_tr_b16 v[150:151], v241 offset:1536\n\t"
"s_cmp_eq_u32 s73, 0\n\t"
"s_cbranch_scc1 Lsk4_%=\n\t"
"s_add_u32 m0, s24, 99840\n\t"
"s_add_u32 s28, s21, 2949120\n\t"
"buffer_load_dwordx4 v28, s[12:15], s28 offen offset:0 nt lds\n\t"
"s_add_u32 m0, s24, 105984\n\t"
"s_add_u32 s28, s21, 3145728\n\t"
"buffer_load_dwordx4 v28, s[12:15], s28 offen offset:0 nt lds\n\t"
"s_add_u32 m0, s24, 112128\n\t"
"s_add_u32 s28, s21, 3342336\n\t"
"buffer_load_dwordx4 v28, s[12:15], s28 offen offset:0 nt lds\n\t"
"s_add_u32 m0, s60, 142848\n\t"
"s_add_u32 s28, s61, 2949120\n\t"
"buffer_load_dwordx4 v30, s[12:15], s28 offen nt lds\n\t"
"Lsk4_%=:\n\t"
"s_waitcnt lgkmcnt(6)\n\t"
"v_mfma_f32_16x16x32_bf16 v[136:139], v[136:139], v[196:199], 0\n\t"
"s_waitcnt lgkmcnt(4)\n\t"
"v_mfma_f32_16x16x32_bf16 v[140:143], v[140:143], v[196:199], 0\n\t"
"s_waitcnt lgkmcnt(2)\n\t"
"v_mfma_f32_16x16x32_bf16 v[144:147], v[144:147], v[196:199], 0\n\t"
"s_waitcnt lgkmcnt(0)\n\t"
"v_mfma_f32_16x16x32_bf16 v[148:151], v[148:151], v[196:199], 0\n\t"
"v_pk_mul_f32 v[136:137], v[136:137], v[242:243] op_sel:[0,1] op_sel_hi:[1,1]\n\t"
"v_pk_mul_f32 v[138:139], v[138:139], v[242:243] op_sel:[0,1] op_sel_hi:[1,1]\n\t"
"v_cvt_pk_bf16_f32 v136, v136, v137\n\t"
"v_cvt_pk_bf16_f32 v137, v138, v139\n\t"
"v_pk_mul_f32 v[140:141], v[140:141], v[242:243] op_sel:[0,1] op_sel_hi:[1,1]\n\t"
"v_pk_mul_f32 v[142:143], v[142:143], v[242:243] op_sel:[0,1] op_sel_hi:[1,1]\n\t"
"v_cvt_pk_bf16_f32 v138, v140, v141\n\t"
"v_cvt_pk_bf16_f32 v139, v142, v143\n\t"
"ds_write_b128 v242, v[136:139] offset:26112\n\t"
"v_pk_mul_f32 v[144:145], v[144:145], v[242:243] op_sel:[0,1] op_sel_hi:[1,1]\n\t"
"v_pk_mul_f32 v[146:147], v[146:147], v[242:243] op_sel:[0,1] op_sel_hi:[1,1]\n\t"
"v_cvt_pk_bf16_f32 v144, v144, v145\n\t"
"v_cvt_pk_bf16_f32 v145, v146, v147\n\t"
"v_pk_mul_f32 v[148:149], v[148:149], v[242:243] op_sel:[0,1] op_sel_hi:[1,1]\n\t"
"v_pk_mul_f32 v[150:151], v[150:151], v[242:243] op_sel:[0,1] op_sel_hi:[1,1]\n\t"
"v_cvt_pk_bf16_f32 v146, v148, v149\n\t"
"v_cvt_pk_bf16_f32 v147, v150, v151\n\t"
"ds_write_b128 v242, v[144:147] offset:24576\n\t"
"s_mov_b64 vcc, 0\n\t"
"s_waitcnt lgkmcnt(0)\n\t"
"s_barrier\n\t"
"ds_read_b128 a[168:171], v244 offset:26112\n\t"
"ds_read_b128 a[172:175], v244 offset:26624\n\t"
"ds_read_b128 a[176:179], v244 offset:27136\n\t"
"ds_read_b128 a[180:183], v244 offset:35328\n\t"
"ds_read_b128 a[184:187], v244 offset:35840\n\t"
"ds_read_b128 a[188:191], v244 offset:36352\n\t"
"ds_read_b128 a[192:195], v244 offset:25600\n\t"
"s_waitcnt lgkmcnt(4)\n\t"
"v_mfma_f32_16x16x32_bf16 v[14:17], a[0:3], a[168:171], 0\n\t"
"ds_read_b64_tr_b16 v[224:225], v240 offset:512\n\t"
"v_mfma_f32_16x16x32_bf16 v[14:17], a[8:11], a[172:175], v[14:17]\n\t"
"ds_read_b64_tr_b16 v[226:227], v240 offset:2048\n\t"
"v_mfma_f32_16x16x32_bf16 v[14:17], a[16:19], a[176:179], v[14:17]\n\t"
"ds_read_b64_tr_b16 v[228:229], v241 offset:512\n\t"
"v_mfma_f32_16x16x32_bf16 v[46:49], a[32:35], a[172:175], 0\n\t"
"ds_read_b64_tr_b16 v[230:231], v241 offset:2048\n\t"
"v_mfma_f32_16x16x32_bf16 v[46:49], a[40:43], a[176:179], v[46:49]\n\t"
"v_mfma_f32_16x16x32_bf16 v[46:49], a[24:27], a[168:171], v[46:49]\n\t"
"v_mfma_f32_16x16x32_bf16 v[78:81], a[48:51], a[168:171], 0\n\t"
"s_waitcnt lgkmcnt(5)\n\t"
"v_mfma_f32_16x16x32_bf16 v[78:81], a[64:67], a[176:179], v[78:81]\n\t"
"ds_read_b64_tr_b16 v[232:233], v240 offset:20480\n\t"
"v_mfma_f32_16x16x32_bf16 v[78:81], a[56:59], a[172:175], v[78:81]\n\t"
"ds_read_b64_tr_b16 v[234:235], v240 offset:22016\n\t"
"v_mfma_f32_16x16x32_bf16 v[112:115], a[72:75], a[180:183], 0\n\t"
"s_waitcnt lgkmcnt(4)\n\t"
"ds_read_b64_tr_b16 v[236:237], v241 offset:20480\n\t"
"v_mfma_f32_16x16x32_bf16 v[112:115], a[80:83], a[184:187], v[112:115]\n\t"
"s_waitcnt lgkmcnt(3)\n\t"
"ds_read_b64_tr_b16 v[238:239], v241 offset:22016\n\t"
"v_mfma_f32_16x16x32_bf16 v[112:115], a[88:91], a[188:191], v[112:115]\n\t"
"ds_read_b128 a[176:179], v244 offset:34816\n\t"
"v_mfma_f32_16x16x32_bf16 v[224:227], v[224:227], v[196:199], 0\n\t"
"v_mfma_f32_16x16x32_bf16 v[228:231], v[228:231], v[196:199], 0\n\t"
"v_mfma_f32_16x16x32_bf16 v[144:147], a[112:115], a[188:191], 0\n\t"
"v_mfma_f32_16x16x32_bf16 v[144:147], a[96:99], a[180:183], v[144:147]\n\t"
"v_mfma_f32_16x16x32_bf16 v[144:147], a[104:107], a[184:187], v[144:147]\n\t"
"v_mfma_f32_16x16x32_bf16 v[176:179], a[120:123], a[180:183], 0\n\t"
"v_cvt_pk_bf16_f32 v224, v224, v225\n\t"
"v_cvt_pk_bf16_f32 v225, v226, v227\n\t"
"v_mfma_f32_16x16x32_bf16 v[176:179], a[128:131], a[184:187], v[176:179]\n\t"
"ds_read_b128 a[196:199], v244 offset:25088\n\t"
"v_cvt_pk_bf16_f32 v226, v228, v229\n\t"
"v_mfma_f32_16x16x32_bf16 v[176:179], a[136:139], a[188:191], v[176:179]\n\t"
"v_cvt_pk_bf16_f32 v227, v230, v231\n\t"
"v_mfma_f32_16x16x32_bf16 v[10:13], a[0:3], a[192:195], 0\n\t"
"s_waitcnt lgkmcnt(4)\n\t"
"v_mfma_f32_16x16x32_bf16 v[10:13], a[16:19], a[172:175], v[10:13]\n\t"
"s_waitcnt lgkmcnt(2)\n\t"
"v_mfma_f32_16x16x32_bf16 v[10:13], a[8:11], a[168:171], v[10:13]\n\t"
"v_mfma_f32_16x16x32_bf16 v[232:235], v[232:235], v[196:199], 0\n\t"
"v_mfma_f32_16x16x32_bf16 v[236:239], v[236:239], v[196:199], 0\n\t"
"s_waitcnt vmcnt(4)\n\t"
"s_barrier\n\t"
"v_mfma_f32_16x16x32_bf16 v[42:45], a[32:35], a[168:171], 0\n\t"
"ds_read_b64_tr_b16 v[228:229], v240 offset:39936\n\t"
"v_mfma_f32_16x16x32_bf16 v[42:45], a[24:27], a[192:195], v[42:45]\n\t"
"ds_read_b64_tr_b16 v[230:231], v240 offset:41472\n\t"
"v_mfma_f32_16x16x32_bf16 v[42:45], a[40:43], a[172:175], v[42:45]\n\t"
"ds_write_b128 v242, v[224:227] offset:0\n\t"
"v_mfma_f32_16x16x32_bf16 v[74:77], a[48:51], a[192:195], 0\n\t"
"s_waitcnt lgkmcnt(4)\n\t"
"v_cvt_pk_bf16_f32 v232, v232, v233\n\t"
"v_cvt_pk_bf16_f32 v233, v234, v235\n\t"
"v_mfma_f32_16x16x32_bf16 v[74:77], a[64:67], a[172:175], v[74:77]\n\t"
"ds_read_b64_tr_b16 v[224:225], v241 offset:39936\n\t"
"v_mfma_f32_16x16x32_bf16 v[74:77], a[56:59], a[168:171], v[74:77]\n\t"
"ds_read_b64_tr_b16 v[226:227], v241 offset:41472\n\t"
"v_cvt_pk_bf16_f32 v234, v236, v237\n\t"
"v_mfma_f32_16x16x32_bf16 v[108:111], a[72:75], a[176:179], 0\n\t"
"s_waitcnt lgkmcnt(3)\n\t"
"ds_read_b128 a[172:175], v244 offset:34304\n\t"
"v_cvt_pk_bf16_f32 v235, v238, v239\n\t"
"v_mfma_f32_16x16x32_bf16 v[108:111], a[88:91], a[184:187], v[108:111]\n\t"
"ds_read_b64_tr_b16 v[236:237], v240 offset:40448\n\t"
"v_mfma_f32_16x16x32_bf16 v[108:111], a[80:83], a[180:183], v[108:111]\n\t"
"ds_write_b128 v242, v[232:235] offset:1536\n\t"
"v_mfma_f32_16x16x32_bf16 v[228:231], v[228:231], v[196:199], 0\n\t"
"s_waitcnt lgkmcnt(3)\n\t"
"v_mfma_f32_16x16x32_bf16 v[224:227], v[224:227], v[196:199], 0\n\t"
"ds_read_b64_tr_b16 v[238:239], v240 offset:41984\n\t"
"v_mfma_f32_16x16x32_bf16 v[140:143], a[104:107], a[180:183], 0\n\t"
"ds_read_b64_tr_b16 v[232:233], v241 offset:40448\n\t"
"v_mfma_f32_16x16x32_bf16 v[140:143], a[96:99], a[176:179], v[140:143]\n\t"
"ds_read_b64_tr_b16 v[234:235], v241 offset:41984\n\t"
"v_mfma_f32_16x16x32_bf16 v[140:143], a[112:115], a[184:187], v[140:143]\n\t"
"v_pk_mul_f32 v[228:229], v[228:229], v[242:243] op_sel:[0,1] op_sel_hi:[1,1]\n\t"
"v_pk_mul_f32 v[230:231], v[230:231], v[242:243] op_sel:[0,1] op_sel_hi:[1,1]\n\t"
"v_mfma_f32_16x16x32_bf16 v[172:175], a[120:123], a[176:179], 0\n\t"
"v_cvt_pk_bf16_f32 v228, v228, v229\n\t"
"v_cvt_pk_bf16_f32 v229, v230, v231\n\t"
"v_mfma_f32_16x16x32_bf16 v[172:175], a[136:139], a[184:187], v[172:175]\n\t"
"v_mfma_f32_16x16x32_bf16 v[172:175], a[128:131], a[180:183], v[172:175]\n\t"
"v_pk_mul_f32 v[224:225], v[224:225], v[242:243] op_sel:[0,1] op_sel_hi:[1,1]\n\t"
"v_pk_mul_f32 v[226:227], v[226:227], v[242:243] op_sel:[0,1] op_sel_hi:[1,1]\n\t"
"v_mfma_f32_16x16x32_bf16 v[6:9], a[0:3], a[196:199], 0\n\t"
"v_cvt_pk_bf16_f32 v230, v224, v225\n\t"
"v_cvt_pk_bf16_f32 v231, v226, v227\n\t"
"v_mfma_f32_16x16x32_bf16 v[6:9], a[8:11], a[192:195], v[6:9]\n\t"
"v_add_u32 v240, 59904, v240\n\t"
"v_add_u32 v241, 59904, v241\n\t"
"v_mfma_f32_16x16x32_bf16 v[6:9], a[16:19], a[168:171], v[6:9]\n\t"
"ds_write_b128 v242, v[228:231] offset:27648\n\t"
"v_mfma_f32_16x16x32_bf16 v[38:41], a[32:35], a[192:195], 0\n\t"
"s_waitcnt lgkmcnt(3)\n\t"
"v_mfma_f32_16x16x32_bf16 v[38:41], a[40:43], a[168:171], v[38:41]\n\t"
"s_waitcnt lgkmcnt(1)\n\t"
"ds_read_b64_tr_b16 v[224:225], v240 offset:512\n\t"
"v_mfma_f32_16x16x32_bf16 v[38:41], a[24:27], a[196:199], v[38:41]\n\t"
"ds_read_b64_tr_b16 v[226:227], v240 offset:2048\n\t"
"v_mfma_f32_16x16x32_bf16 v[236:239], v[236:239], v[196:199], 0\n\t"
"ds_read_b64_tr_b16 v[228:229], v241 offset:512\n\t"
"v_mfma_f32_16x16x32_bf16 v[232:235], v[232:235], v[196:199], 0\n\t"
"ds_read_b64_tr_b16 v[230:231], v241 offset:2048\n\t"
"v_mfma_f32_16x16x32_bf16 v[70:73], a[48:51], a[196:199], 0\n\t"
"v_mfma_f32_16x16x32_bf16 v[70:73], a[56:59], a[192:195], v[70:73]\n\t"
"v_mfma_f32_16x16x32_bf16 v[70:73], a[64:67], a[168:171], v[70:73]\n\t"
"v_mfma_f32_16x16x32_bf16 v[104:107], a[72:75], a[172:175], 0\n\t"
"v_cvt_pk_bf16_f32 v236, v236, v237\n\t"
"v_cvt_pk_bf16_f32 v237, v238, v239\n\t"
"v_mfma_f32_16x16x32_bf16 v[104:107], a[80:83], a[176:179], v[104:107]\n\t"
"v_cvt_pk_bf16_f32 v238, v232, v233\n\t"
"v_mfma_f32_16x16x32_bf16 v[104:107], a[88:91], a[180:183], v[104:107]\n\t"
"s_waitcnt lgkmcnt(4)\n\t"
"s_barrier\n\t"
"v_mfma_f32_16x16x32_bf16 v[136:139], a[104:107], a[176:179], 0\n\t"
"ds_read_b128 a[168:171], v244 offset:24576\n\t"
"v_mfma_f32_16x16x32_bf16 v[136:139], a[112:115], a[180:183], v[136:139]\n\t"
"ds_read_b128 a[184:187], v244 offset:33792\n\t"
"v_cvt_pk_bf16_f32 v239, v234, v235\n\t"
"v_mfma_f32_16x16x32_bf16 v[136:139], a[96:99], a[172:175], v[136:139]\n\t"
"ds_write_b128 v242, v[236:239] offset:3072\n\t"
"v_mfma_f32_16x16x32_bf16 v[168:171], a[120:123], a[172:175], 0\n\t"
"s_waitcnt lgkmcnt(5)\n\t"
"v_mfma_f32_16x16x32_bf16 v[168:171], a[136:139], a[180:183], v[168:171]\n\t"
"s_waitcnt lgkmcnt(3)\n\t"
"ds_read_b128 a[180:183], v244 offset:26624\n\t"
"v_mfma_f32_16x16x32_bf16 v[168:171], a[128:131], a[176:179], v[168:171]\n\t"
"s_waitcnt lgkmcnt(3)\n\t"
"ds_read_b128 a[188:191], v244 offset:27136\n\t"
"v_mfma_f32_16x16x32_bf16 v[224:227], v[224:227], v[196:199], 0\n\t"
"ds_read_b128 a[200:203], v244 offset:27648\n\t"
"v_mfma_f32_16x16x32_bf16 v[228:231], v[228:231], v[196:199], 0\n\t"
"v_mfma_f32_16x16x32_bf16 v[2:5], a[0:3], a[168:171], 0\n\t"
"v_mfma_f32_16x16x32_bf16 v[2:5], a[8:11], a[196:199], v[2:5]\n\t"
"v_mfma_f32_16x16x32_bf16 v[2:5], a[16:19], a[192:195], v[2:5]\n\t"
"s_waitcnt vmcnt(0)\n\t"
"s_barrier\n\t"
"v_mfma_f32_16x16x32_bf16 v[34:37], a[32:35], a[196:199], 0\n\t"
"ds_read_b64_tr_b16 v[232:233], v240 offset:39936\n\t"
"v_mfma_f32_16x16x32_bf16 v[34:37], a[40:43], a[192:195], v[34:37]\n\t"
"ds_read_b64_tr_b16 v[234:235], v240 offset:41472\n\t"
"v_cvt_pk_bf16_f32 v224, v224, v225\n\t"
"v_mfma_f32_16x16x32_bf16 v[34:37], a[24:27], a[168:171], v[34:37]\n\t"
"ds_read_b64_tr_b16 v[236:237], v241 offset:39936\n\t"
"v_cvt_pk_bf16_f32 v225, v226, v227\n\t"
"v_mfma_f32_16x16x32_bf16 v[66:69], a[48:51], a[168:171], 0\n\t"
"s_waitcnt lgkmcnt(7)\n\t"
"ds_read_b64_tr_b16 v[238:239], v241 offset:41472\n\t"
"v_cvt_pk_bf16_f32 v226, v228, v229\n\t"
"v_mfma_f32_16x16x32_bf16 v[66:69], a[64:67], a[192:195], v[66:69]\n\t"
"ds_read_b128 a[168:171], v244 offset:35840\n\t"
"v_cvt_pk_bf16_f32 v227, v230, v231\n\t"
"v_mfma_f32_16x16x32_bf16 v[66:69], a[56:59], a[196:199], v[66:69]\n\t"
"s_cmp_lg_u32 s73, 0\n\t"
"s_cbranch_scc1 Lnp_%=\n\t"
"buffer_load_dwordx4 v[248:251], v252, s[8:11], s72 offen sc1\n\t"
"Lnp_%=:\n\t"
"ds_write_b128 v242, v[224:227] offset:4608\n\t"
"v_mfma_f32_16x16x32_bf16 v[100:103], a[72:75], a[184:187], 0\n\t"
"s_waitcnt lgkmcnt(4)\n\t"
"v_mfma_f32_16x16x32_bf16 v[100:103], a[80:83], a[172:175], v[100:103]\n\t"
"ds_read_b128 a[192:195], v244 offset:36352\n\t"
"v_mfma_f32_16x16x32_bf16 v[100:103], a[88:91], a[176:179], v[100:103]\n\t"
"s_waitcnt lgkmcnt(3)\n\t"
"ds_read_b128 a[204:207], v244 offset:36864\n\t"
"v_mfma_f32_16x16x32_bf16 v[232:235], v[232:235], v[196:199], 0\n\t"
"ds_read_b64_tr_b16 v[228:229], v240 offset:20480\n\t"
"v_mfma_f32_16x16x32_bf16 v[236:239], v[236:239], v[196:199], 0\n\t"
"ds_read_b64_tr_b16 v[230:231], v240 offset:22016\n\t"
"v_mfma_f32_16x16x32_bf16 v[132:135], a[112:115], a[176:179], 0\n\t"
"ds_read_b64_tr_b16 v[224:225], v241 offset:20480\n\t"
"v_mfma_f32_16x16x32_bf16 v[132:135], a[96:99], a[184:187], v[132:135]\n\t"
"ds_read_b64_tr_b16 v[226:227], v241 offset:22016\n\t"
"v_mfma_f32_16x16x32_bf16 v[132:135], a[104:107], a[172:175], v[132:135]\n\t"
"v_pk_mul_f32 v[232:233], v[232:233], v[242:243] op_sel:[0,1] op_sel_hi:[1,1]\n\t"
"v_pk_mul_f32 v[234:235], v[234:235], v[242:243] op_sel:[0,1] op_sel_hi:[1,1]\n\t"
"v_mfma_f32_16x16x32_bf16 v[164:167], a[120:123], a[184:187], 0\n\t"
"v_cvt_pk_bf16_f32 v232, v232, v233\n\t"
"v_cvt_pk_bf16_f32 v233, v234, v235\n\t"
"v_mfma_f32_16x16x32_bf16 v[164:167], a[128:131], a[172:175], v[164:167]\n\t"
"v_mfma_f32_16x16x32_bf16 v[164:167], a[136:139], a[176:179], v[164:167]\n\t"
"v_pk_mul_f32 v[236:237], v[236:237], v[242:243] op_sel:[0,1] op_sel_hi:[1,1]\n\t"
"v_pk_mul_f32 v[238:239], v[238:239], v[242:243] op_sel:[0,1] op_sel_hi:[1,1]\n\t"
"v_mfma_f32_16x16x32_bf16 v[18:21], a[0:3], a[180:183], 0\n\t"
"v_cvt_pk_bf16_f32 v234, v236, v237\n\t"
"v_cvt_pk_bf16_f32 v235, v238, v239\n\t"
"v_mfma_f32_16x16x32_bf16 v[18:21], a[8:11], a[188:191], v[18:21]\n\t"
"ds_read_b64_tr_b16 v[236:237], v240 offset:40448\n\t"
"v_mfma_f32_16x16x32_bf16 v[18:21], a[16:19], a[200:203], v[18:21]\n\t"
"ds_write_b128 v242, v[232:235] offset:32256\n\t"
"v_mfma_f32_16x16x32_bf16 v[50:53], a[32:35], a[188:191], 0\n\t"
"s_waitcnt lgkmcnt(2)\n\t"
"v_mfma_f32_16x16x32_bf16 v[50:53], a[40:43], a[200:203], v[50:53]\n\t"
"ds_read_b64_tr_b16 v[238:239], v240 offset:41984\n\t"
"v_mfma_f32_16x16x32_bf16 v[50:53], a[24:27], a[180:183], v[50:53]\n\t"
"ds_read_b64_tr_b16 v[232:233], v241 offset:40448\n\t"
"v_mfma_f32_16x16x32_bf16 v[228:231], v[228:231], v[196:199], 0\n\t"
"Lpg1_%=:\n\t"
"s_cbranch_vccnz Lpg5_%=\n\t"
"ds_read_b64_tr_b16 v[234:235], v241 offset:41984\n\t"
"v_mfma_f32_16x16x32_bf16 v[224:227], v[224:227], v[196:199], 0\n\t"
"v_mfma_f32_16x16x32_bf16 v[82:85], a[48:51], a[180:183], 0\n\t"
"v_mfma_f32_16x16x32_bf16 v[82:85], a[56:59], a[188:191], v[82:85]\n\t"
"v_mfma_f32_16x16x32_bf16 v[82:85], a[64:67], a[200:203], v[82:85]\n\t"
"v_mfma_f32_16x16x32_bf16 v[116:119], a[72:75], a[168:171], 0\n\t"
"v_cvt_pk_bf16_f32 v228, v228, v229\n\t"
"v_cvt_pk_bf16_f32 v229, v230, v231\n\t"
"v_mfma_f32_16x16x32_bf16 v[116:119], a[80:83], a[192:195], v[116:119]\n\t"
"v_cvt_pk_bf16_f32 v230, v224, v225\n\t"
"v_mfma_f32_16x16x32_bf16 v[116:119], a[88:91], a[204:207], v[116:119]\n\t"
"s_waitcnt lgkmcnt(3)\n\t"
"s_barrier\n\t"
"v_mfma_f32_16x16x32_bf16 v[148:151], a[104:107], a[192:195], 0\n\t"
"s_waitcnt lgkmcnt(2)\n\t"
"ds_read_b128 a[184:187], v244 offset:28160\n\t"
"v_mfma_f32_16x16x32_bf16 v[148:151], a[112:115], a[204:207], v[148:151]\n\t"
"s_waitcnt lgkmcnt(1)\n\t"
"ds_read_b128 a[196:199], v244 offset:37376\n\t"
"v_cvt_pk_bf16_f32 v231, v226, v227\n\t"
"v_mfma_f32_16x16x32_bf16 v[148:151], a[96:99], a[168:171], v[148:151]\n\t"
"ds_write_b128 v242, v[228:231] offset:6144\n\t"
"v_mfma_f32_16x16x32_bf16 v[236:239], v[236:239], v[196:199], 0\n\t"
"v_mfma_f32_16x16x32_bf16 v[232:235], v[232:235], v[196:199], 0\n\t"
"ds_read_b128 v[204:207], v244 offset:28672\n\t"
"v_mfma_f32_16x16x32_bf16 v[180:183], a[120:123], a[168:171], 0\n\t"
"s_waitcnt lgkmcnt(3)\n\t"
"v_mfma_f32_16x16x32_bf16 v[180:183], a[128:131], a[192:195], v[180:183]\n\t"
"v_mfma_f32_16x16x32_bf16 v[180:183], a[136:139], a[204:207], v[180:183]\n\t"
"v_mfma_f32_16x16x32_bf16 v[22:25], a[0:3], a[188:191], 0\n\t"
"v_cvt_pk_bf16_f32 v236, v236, v237\n\t"
"v_cvt_pk_bf16_f32 v237, v238, v239\n\t"
"v_mfma_f32_16x16x32_bf16 v[22:25], a[8:11], a[200:203], v[22:25]\n\t"
"v_cvt_pk_bf16_f32 v238, v232, v233\n\t"
"v_cvt_pk_bf16_f32 v239, v234, v235\n\t"
"v_mfma_f32_16x16x32_bf16 v[22:25], a[16:19], a[184:187], v[22:25]\n\t"
"ds_write_b128 v242, v[236:239] offset:7680\n\t"
"v_mfma_f32_16x16x32_bf16 v[54:57], a[32:35], a[200:203], 0\n\t"
"v_mfma_f32_16x16x32_bf16 v[54:57], a[40:43], a[184:187], v[54:57]\n\t"
"ds_read_b128 v[196:199], v244 offset:37888\n\t"
"v_mfma_f32_16x16x32_bf16 v[54:57], a[24:27], a[188:191], v[54:57]\n\t"
"v_mfma_f32_16x16x32_bf16 v[86:89], a[48:51], a[188:191], 0\n\t"
"s_waitcnt lgkmcnt(4)\n\t"
"v_mfma_f32_16x16x32_bf16 v[86:89], a[64:67], a[184:187], v[86:89]\n\t"
"v_mfma_f32_16x16x32_bf16 v[86:89], a[56:59], a[200:203], v[86:89]\n\t"
"v_mfma_f32_16x16x32_bf16 v[120:123], a[72:75], a[192:195], 0\n\t"
"s_mov_b32 s20, 294912\n\t"
"v_mfma_f32_16x16x32_bf16 v[120:123], a[80:83], a[204:207], v[120:123]\n\t"
"v_mfma_f32_16x16x32_bf16 v[120:123], a[88:91], a[196:199], v[120:123]\n\t"
"v_mfma_f32_16x16x32_bf16 v[152:155], a[104:107], a[204:207], 0\n\t"
"ds_read_b128 a[188:191], v244 offset:29184\n\t"
"v_mfma_f32_16x16x32_bf16 v[152:155], a[112:115], a[196:199], v[152:155]\n\t"
"v_mfma_f32_16x16x32_bf16 v[152:155], a[96:99], a[192:195], v[152:155]\n\t"
"s_waitcnt lgkmcnt(2)\n\t"
"s_cmp_lg_u32 s73, 0\n\t"
"s_cbranch_scc1 Lgo_%=\n\t"
"Lchk_%=:\n\t"
"s_waitcnt vmcnt(0)\n\t"
"v_cmpx_ne_u32_e64 s[60:61], v220, v248\n\t"
"v_cmpx_ne_u32_e64 s[60:61], v221, v249\n\t"
"v_cmpx_ne_u32_e64 s[60:61], v222, v250\n\t"
"v_cmpx_ne_u32_e64 s[60:61], v223, v251\n\t"
"s_not_b64 s[60:61], s[60:61]\n\t"
"s_mov_b64 exec, -1\n\t"
"s_lshl_b64 s[60:61], s[60:61], 10\n\t"
"s_cbranch_scc0 Lgo_%=\n\t"
"s_sub_u32 s68, s68, 1\n\t"
"s_cbranch_scc1 Lfb_%=\n\t"
"buffer_load_dwordx4 v[248:251], v252, s[8:11], s72 offen sc1\n\t"
"s_branch Lchk_%=\n\t"
"Lgo_%=:\n\t"
"s_barrier\n\t"
"v_mfma_f32_16x16x32_bf16 v[184:187], a[120:123], a[192:195], 0\n\t"
"s_lshr_b32 s28, s73, 1\n\t"
"s_mul_i32 s29, s28, 1757184\n\t"
"v_mfma_f32_16x16x32_bf16 v[184:187], a[136:139], a[196:199], v[184:187]\n\t"
"buffer_load_dwordx4 a[144:147], off, s[80:83], s20 offset:0 sc1\n\t"
"v_mfma_f32_16x16x32_bf16 v[184:187], a[128:131], a[204:207], v[184:187]\n\t"
"global_load_dwordx4 v[220:223], v252, s[48:49] offset:1024\n\t"
"v_mfma_f32_16x16x32_bf16 v[26:29], a[0:3], a[200:203], 0\n\t"
"buffer_load_dwordx4 a[152:155], off, s[80:83], s20 offset:1024 sc1\n\t"
"v_mfma_f32_16x16x32_bf16 v[26:29], a[8:11], a[184:187], v[26:29]\n\t"
"s_add_u32 s21, s21, s29\n\t"
"s_mul_i32 s29, s73, 3072\n\t"
"v_lshlrev_b32 v245, 4, v245\n\t"
"v_mfma_f32_16x16x32_bf16 v[26:29], a[16:19], v[204:207], v[26:29]\n\t"
"s_add_u32 s21, s21, s29\n\t"
"s_mul_i32 s28, s28, 53760\n\t"
"v_xor_b32 v255, 32, v246\n\t"
"v_mfma_f32_16x16x32_bf16 v[58:61], a[32:35], a[184:187], 0\n\t"
"s_lshl1_add_u32 s24, s24, s28\n\t"
"ds_read_b128 v[212:215], v244 offset:38400\n\t"
"s_sub_u32 s24, s24, 43008\n\t"
"v_mfma_f32_16x16x32_bf16 v[58:61], a[40:43], v[204:207], v[58:61]\n\t"
"buffer_load_dwordx4 a[160:163], off, s[80:83], s20 offset:2048 sc1\n\t"
"v_mfma_f32_16x16x32_bf16 v[58:61], a[24:27], a[200:203], v[58:61]\n\t"
"s_mov_b32 s20, 307200\n\t"
"s_mov_b32 s52, 0x3fd80000\n\t"
"s_mov_b32 s53, s52\n\t"
"v_mfma_f32_16x16x32_bf16 v[90:93], a[48:51], a[200:203], 0\n\t"
"buffer_load_dwordx4 v[200:203], off, s[80:83], s20 offset:0 sc1\n\t"
"v_mfma_f32_16x16x32_bf16 v[90:93], a[64:67], v[204:207], v[90:93]\n\t"
"s_waitcnt lgkmcnt(2)\n\t"
"s_mov_b32 s54, 0x3e97b426\n\t"
"s_mov_b32 s55, s54\n\t"
"v_mfma_f32_16x16x32_bf16 v[90:93], a[56:59], a[184:187], v[90:93]\n\t"
"s_mov_b32 s56, 0x3f400000\n\t"
"s_mov_b32 s57, s56\n\t"
"s_mov_b32 s58, 0x3f2aaaab\n\t"
"v_mfma_f32_16x16x32_bf16 v[124:127], a[72:75], a[204:207], 0\n\t"
"buffer_load_dwordx4 v[208:211], off, s[80:83], s20 offset:1024 sc1\n\t"
"v_mfma_f32_16x16x32_bf16 v[124:127], a[80:83], a[196:199], v[124:127]\n\t"
"s_mov_b32 s59, s58\n\t"
"s_mov_b32 s60, 0x3ee38e39\n\t"
"s_mov_b32 s61, s60\n\t"
"v_mfma_f32_16x16x32_bf16 v[124:127], a[88:91], v[196:199], v[124:127]\n\t"
"s_mov_b32 s62, 0x40100000\n\t"
"s_mov_b32 s63, s62\n\t"
"s_mov_b32 s64, 0x3faaaaab\n\t"
"v_mfma_f32_16x16x32_bf16 v[156:159], a[104:107], a[196:199], 0\n\t"
"buffer_load_dwordx4 v[216:219], off, s[80:83], s20 offset:2048 sc1\n\t"
"v_mfma_f32_16x16x32_bf16 v[156:159], a[112:115], v[196:199], v[156:159]\n\t"
"s_mov_b32 s20, 319488\n\t"
"s_mov_b32 s65, s64\n\t"
"s_mov_b32 s66, 0x3f17b426\n\t"
"v_mfma_f32_16x16x32_bf16 v[156:159], a[96:99], a[204:207], v[156:159]\n\t"
"s_mov_b32 s67, s66\n\t"
"v_mfma_f32_16x16x32_bf16 v[188:191], a[120:123], a[204:207], 0\n\t"
"buffer_load_dwordx4 a[4:7], off, s[80:83], s20 offset:0 sc1\n\t"
"v_mfma_f32_16x16x32_bf16 v[188:191], a[136:139], v[196:199], v[188:191]\n\t"
"s_waitcnt lgkmcnt(1)\n\t"
"v_mfma_f32_16x16x32_bf16 v[188:191], a[128:131], a[196:199], v[188:191]\n\t"
"v_mfma_f32_16x16x32_bf16 v[30:33], a[0:3], a[184:187], 0\n\t"
"buffer_load_dwordx4 a[0:3], off, s[80:83], s20 offset:1024 sc1\n\t"
"v_mfma_f32_16x16x32_bf16 v[30:33], a[8:11], v[204:207], v[30:33]\n\t"
"v_mfma_f32_16x16x32_bf16 v[30:33], a[16:19], a[188:191], v[30:33]\n\t"
"ds_read_b128 a[168:171], v244 offset:0\n\t"
"v_mfma_f32_16x16x32_bf16 v[62:65], a[32:35], v[204:207], 0\n\t"
"buffer_load_dwordx4 a[8:11], off, s[80:83], s20 offset:2048 sc1\n\t"
"v_mfma_f32_16x16x32_bf16 v[62:65], a[40:43], a[188:191], v[62:65]\n\t"
"s_mov_b32 s20, 479232\n\t"
"ds_read_b128 a[172:175], v244 offset:512\n\t"
"v_mfma_f32_16x16x32_bf16 v[62:65], a[24:27], a[184:187], v[62:65]\n\t"
"ds_read_b128 a[176:179], v244 offset:1024\n\t"
"v_mfma_f32_16x16x32_bf16 v[94:97], a[48:51], a[184:187], 0\n\t"
"buffer_load_dwordx4 a[16:19], off, s[80:83], s20 offset:0 sc1\n\t"
"v_mfma_f32_16x16x32_bf16 v[94:97], a[64:67], a[188:191], v[94:97]\n\t"
"s_waitcnt lgkmcnt(3)\n\t"
"ds_read_b64_tr_b16 v[224:225], v246 offset:0\n\t"
"v_mfma_f32_16x16x32_bf16 v[94:97], a[56:59], v[204:207], v[94:97]\n\t"
"ds_read_b64_tr_b16 v[228:229], v255 offset:0\n\t"
"v_mfma_f32_16x16x32_bf16 v[128:131], a[72:75], a[196:199], 0\n\t"
"ds_read_b128 a[180:183], v244 offset:1536\n\t"
"v_mfma_f32_16x16x32_bf16 v[128:131], a[80:83], v[196:199], v[128:131]\n\t"
"ds_read_b64_tr_b16 v[232:233], v246 offset:6144\n\t"
"v_mfma_f32_16x16x32_bf16 v[128:131], a[88:91], v[212:215], v[128:131]\n\t"
"buffer_load_dwordx4 v[204:207], off, s[80:83], s20 offset:1024 sc1\n\t"
"v_mfma_f32_16x16x32_bf16 v[160:163], a[104:107], v[196:199], 0\n\t"
"ds_read_b64_tr_b16 v[236:237], v255 offset:6144\n\t"
"v_mfma_f32_16x16x32_bf16 v[160:163], a[112:115], v[212:215], v[160:163]\n\t"
"ds_read_b128 a[184:187], v244 offset:2048\n\t"
"v_mfma_f32_16x16x32_bf16 v[160:163], a[96:99], a[196:199], v[160:163]\n\t"
"buffer_load_dwordx4 a[24:27], off, s[80:83], s20 offset:2048 sc1\n\t"
"s_mov_b32 s20, 491520\n\t"
"v_mfma_f32_16x16x32_bf16 v[192:195], a[120:123], a[196:199], 0\n\t"
"s_waitcnt vmcnt(11) lgkmcnt(5)\n\t"
"ds_read_b128 a[188:191], v244 offset:2560\n\t"
"v_mfma_f32_16x16x32_bf16 v[192:195], a[136:139], v[212:215], v[192:195]\n\t"
"buffer_load_dwordx4 a[32:35], off, s[80:83], s20 offset:0 sc1\n\t"
"v_mfma_f32_16x16x32_bf16 v[192:195], a[128:131], v[196:199], v[192:195]\n\t"
"s_waitcnt lgkmcnt(3)\n\t"
"ds_read_b128 a[192:195], v244 offset:3072\n\t"
"v_mfma_f32_4x4x4_16b_bf16 v[224:227], v[224:225], v[220:221], 0\n\t"
"s_waitcnt lgkmcnt(3)\n\t"
"v_mfma_f32_4x4x4_16b_bf16 v[228:231], v[228:229], v[220:221], 0\n\t"
"s_waitcnt vmcnt(10)\n\t"
"v_mfma_f32_4x4x4_16b_bf16 v[232:235], v[232:233], v[220:221], 0\n\t"
"v_mfma_f32_4x4x4_16b_bf16 v[236:239], v[236:237], v[220:221], 0\n\t"
"buffer_load_dwordx4 a[40:43], off, s[80:83], s20 offset:1024 sc1\n\t"
"Lst1_%=:\n\t"
"v_mfma_f32_16x16x32_bf16 v[2:5], a[144:147], a[168:171], v[2:5]\n\t"
"ds_read_b128 a[196:199], v244 offset:3584\n\t"
"v_cvt_pk_bf16_f32 v224, v224, v225\n\t"
"v_mfma_f32_16x16x32_bf16 v[2:5], a[160:163], a[176:179], v[2:5]\n\t"
"ds_read_b128 a[200:203], v244 offset:4096\n\t"
"v_cvt_pk_bf16_f32 v225, v226, v227\n\t"
"v_mfma_f32_16x16x32_bf16 v[2:5], a[152:155], a[172:175], v[2:5]\n\t"
"buffer_load_dwordx4 a[48:51], off, s[80:83], s20 offset:2048 sc1\n\t"
"s_mov_b32 s20, 503808\n\t"
"v_mfma_f32_16x16x32_bf16 v[6:9], a[144:147], a[172:175], v[6:9]\n\t"
"buffer_load_dwordx4 a[80:83], off, s[80:83], s20 offset:0 sc1\n\t"
"v_mfma_f32_16x16x32_bf16 v[6:9], a[152:155], a[176:179], v[6:9]\n\t"
"s_waitcnt lgkmcnt(4)\n\t"
"ds_read_b128 a[204:207], v244 offset:4608\n\t"
"v_cvt_pk_bf16_f32 v226, v228, v229\n\t"
"v_mfma_f32_16x16x32_bf16 v[6:9], a[160:163], a[180:183], v[6:9]\n\t"
"v_cvt_pk_bf16_f32 v227, v230, v231\n\t"
"v_cvt_pk_bf16_f32 v232, v232, v233\n\t"
"v_cvt_pk_bf16_f32 v233, v234, v235\n\t"
"v_cvt_pk_bf16_f32 v234, v236, v237\n\t"
"v_cvt_pk_bf16_f32 v235, v238, v239\n\t"
"v_mfma_f32_16x16x32_bf16 v[10:13], a[160:163], a[184:187], v[10:13]\n\t"
"s_waitcnt lgkmcnt(4)\n\t"
"v_mfma_f32_16x16x32_bf16 v[10:13], a[152:155], a[180:183], v[10:13]\n\t"
"buffer_load_dwordx4 a[88:91], off, s[80:83], s20 offset:1024 sc1\n\t"
"v_mfma_f32_16x16x32_bf16 v[10:13], a[144:147], a[176:179], v[10:13]\n\t"
"ds_write_b128 v245, v[224:227] offset:0\n\t"
"v_mfma_f32_16x16x32_bf16 v[14:17], a[144:147], a[180:183], v[14:17]\n\t"
"buffer_load_dwordx4 a[96:99], off, s[80:83], s20 offset:2048 sc1\n\t"
"v_mfma_f32_16x16x32_bf16 v[14:17], a[152:155], a[184:187], v[14:17]\n\t"
"s_waitcnt lgkmcnt(4)\n\t"
"ds_read_b64_tr_b16 v[228:229], v246 offset:12288\n\t"
"v_mfma_f32_16x16x32_bf16 v[14:17], a[160:163], a[188:191], v[14:17]\n\t"
"s_mov_b32 s20, 110592\n\t"
"ds_write_b128 v245, v[232:235] offset:6144\n\t"
"v_mfma_f32_16x16x32_bf16 v[18:21], a[160:163], a[192:195], v[18:21]\n\t"
"buffer_load_dwordx4 a[12:15], off, s[80:83], s20 offset:0 sc1\n\t"
"v_mfma_f32_16x16x32_bf16 v[18:21], a[152:155], a[188:191], v[18:21]\n\t"
"s_waitcnt lgkmcnt(5)\n\t"
"ds_read_b64_tr_b16 v[236:237], v255 offset:12288\n\t"
"v_mfma_f32_16x16x32_bf16 v[18:21], a[144:147], a[184:187], v[18:21]\n\t"
"buffer_load_dwordx4 a[20:23], off, s[80:83], s20 offset:1024 sc1\n\t"
"v_mfma_f32_16x16x32_bf16 v[22:25], a[144:147], a[188:191], v[22:25]\n\t"
"s_waitcnt lgkmcnt(5)\n\t"
"ds_read_b64_tr_b16 v[224:225], v246 offset:19968\n\t"
"v_mfma_f32_16x16x32_bf16 v[22:25], a[152:155], a[192:195], v[22:25]\n\t"
"ds_read_b64_tr_b16 v[232:233], v255 offset:19968\n\t"
"v_mfma_f32_16x16x32_bf16 v[22:25], a[160:163], a[196:199], v[22:25]\n\t"
"buffer_load_dwordx4 a[28:31], off, s[80:83], s20 offset:2048 sc1\n\t"
"v_mfma_f32_16x16x32_bf16 v[26:29], a[160:163], a[200:203], v[26:29]\n\t"
"s_waitcnt lgkmcnt(6)\n\t"
"s_mov_b32 s20, 122880\n\t"
"v_mfma_f32_16x16x32_bf16 v[26:29], a[152:155], a[196:199], v[26:29]\n\t"
"v_mfma_f32_16x16x32_bf16 v[26:29], a[144:147], a[192:195], v[26:29]\n\t"
"buffer_load_dwordx4 a[36:39], off, s[80:83], s20 offset:0 sc1\n\t"
"v_mfma_f32_16x16x32_bf16 v[30:33], a[144:147], a[196:199], v[30:33]\n\t"
"s_waitcnt lgkmcnt(4)\n\t"
"v_mfma_f32_16x16x32_bf16 v[30:33], a[152:155], a[200:203], v[30:33]\n\t"
"s_waitcnt lgkmcnt(2)\n\t"
"v_mfma_f32_16x16x32_bf16 v[30:33], a[160:163], a[204:207], v[30:33]\n\t"
"buffer_load_dwordx4 a[44:47], off, s[80:83], s20 offset:1024 sc1\n\t"
"v_mfma_f32_4x4x4_16b_bf16 v[228:231], v[228:229], v[220:221], 0\n\t"
"s_waitcnt lgkmcnt(0)\n\t"
"v_mfma_f32_4x4x4_16b_bf16 v[236:239], v[236:237], v[220:221], 0\n\t"
"s_waitcnt vmcnt(17)\n\t"
"v_mfma_f32_4x4x4_16b_bf16 v[224:227], v[224:225], v[220:221], 0\n\t"
"v_mfma_f32_4x4x4_16b_bf16 v[232:235], v[232:233], v[220:221], 0\n\t"
"v_mfma_f32_16x16x32_bf16 v[34:37], v[200:203], a[168:171], v[34:37]\n\t"
"buffer_load_dwordx4 a[52:55], off, s[80:83], s20 offset:2048 sc1\n\t"
"v_mfma_f32_16x16x32_bf16 v[34:37], v[216:219], a[176:179], v[34:37]\n\t"
"s_mov_b32 s20, 135168\n\t"
"v_cvt_pk_bf16_f32 v228, v228, v229\n\t"
"v_cvt_pk_bf16_f32 v229, v230, v231\n\t"
"v_mfma_f32_16x16x32_bf16 v[34:37], v[208:211], a[172:175], v[34:37]\n\t"
"v_cvt_pk_bf16_f32 v230, v236, v237\n\t"
"v_cvt_pk_bf16_f32 v231, v238, v239\n\t"
"v_mfma_f32_16x16x32_bf16 v[38:41], v[200:203], a[172:175], v[38:41]\n\t"
"v_cvt_pk_bf16_f32 v224, v224, v225\n\t"
"v_cvt_pk_bf16_f32 v225, v226, v227\n\t"
"v_mfma_f32_16x16x32_bf16 v[38:41], v[208:211], a[176:179], v[38:41]\n\t"
"v_cvt_pk_bf16_f32 v226, v232, v233\n\t"
"v_cvt_pk_bf16_f32 v227, v234, v235\n\t"
"v_mfma_f32_16x16x32_bf16 v[38:41], v[216:219], a[180:183], v[38:41]\n\t"
"buffer_load_dwordx4 a[84:87], off, s[80:83], s20 offset:0 sc1\n\t"
"v_mfma_f32_16x16x32_bf16 v[42:45], v[216:219], a[184:187], v[42:45]\n\t"
"buffer_load_dwordx4 a[92:95], off, s[80:83], s20 offset:1024 sc1\n\t"
"v_mfma_f32_16x16x32_bf16 v[42:45], v[208:211], a[180:183], v[42:45]\n\t"
"buffer_load_dwordx4 a[100:103], off, s[80:83], s20 offset:2048 sc1\n\t"
"v_mfma_f32_16x16x32_bf16 v[42:45], v[200:203], a[176:179], v[42:45]\n\t"
"s_mov_b32 s20, 147456\n\t"
"ds_write_b128 v245, v[228:231] offset:12288\n\t"
"v_mfma_f32_16x16x32_bf16 v[46:49], v[200:203], a[180:183], v[46:49]\n\t"
"v_mfma_f32_16x16x32_bf16 v[46:49], v[208:211], a[184:187], v[46:49]\n\t"
"buffer_load_dwordx4 a[140:143], off, s[80:83], s20 offset:0 sc1\n\t"
"v_mfma_f32_16x16x32_bf16 v[46:49], v[216:219], a[188:191], v[46:49]\n\t"
"ds_write_b128 v245, v[224:227] offset:19968\n\t"
"v_mfma_f32_16x16x32_bf16 v[50:53], v[216:219], a[192:195], v[50:53]\n\t"
"v_mfma_f32_16x16x32_bf16 v[50:53], v[208:211], a[188:191], v[50:53]\n\t"
"ds_read_b64_tr_b16 v[236:237], v246 offset:26112\n\t"
"v_mfma_f32_16x16x32_bf16 v[50:53], v[200:203], a[184:187], v[50:53]\n\t"
"ds_read_b64_tr_b16 v[232:233], v255 offset:26112\n\t"
"v_mfma_f32_16x16x32_bf16 v[54:57], v[200:203], a[188:191], v[54:57]\n\t"
"buffer_load_dwordx4 a[148:151], off, s[80:83], s20 offset:1024 sc1\n\t"
"v_mfma_f32_16x16x32_bf16 v[54:57], v[208:211], a[192:195], v[54:57]\n\t"
"ds_read_b64_tr_b16 v[228:229], v246 offset:32256\n\t"
"v_mfma_f32_16x16x32_bf16 v[54:57], v[216:219], a[196:199], v[54:57]\n\t"
"ds_read_b64_tr_b16 v[224:225], v255 offset:32256\n\t"
"v_mfma_f32_16x16x32_bf16 v[58:61], v[216:219], a[200:203], v[58:61]\n\t"
"buffer_load_dwordx4 a[156:159], off, s[80:83], s20 offset:2048 sc1\n\t"
"v_mfma_f32_16x16x32_bf16 v[58:61], v[208:211], a[196:199], v[58:61]\n\t"
"s_mov_b32 s20, 159744\n\t"
"v_mfma_f32_16x16x32_bf16 v[58:61], v[200:203], a[192:195], v[58:61]\n\t"
"v_mfma_f32_16x16x32_bf16 v[62:65], v[200:203], a[196:199], v[62:65]\n\t"
"s_waitcnt lgkmcnt(3)\n\t"
"v_mfma_f32_16x16x32_bf16 v[62:65], v[208:211], a[200:203], v[62:65]\n\t"
"buffer_load_dwordx4 v[240:243], off, s[80:83], s20 offset:0 sc1\n\t"
"v_mfma_f32_16x16x32_bf16 v[62:65], v[216:219], a[204:207], v[62:65]\n\t"
"s_waitcnt lgkmcnt(1)\n\t"
"v_mfma_f32_4x4x4_16b_bf16 v[236:239], v[236:237], v[220:221], 0\n\t"
"s_waitcnt lgkmcnt(0)\n\t"
"v_mfma_f32_4x4x4_16b_bf16 v[232:235], v[232:233], v[220:221], 0\n\t"
"s_waitcnt vmcnt(22)\n\t"
"v_mfma_f32_4x4x4_16b_bf16 v[228:231], v[228:229], v[220:221], 0\n\t"
"v_mfma_f32_4x4x4_16b_bf16 v[224:227], v[224:225], v[220:221], 0\n\t"
"v_mfma_f32_16x16x32_bf16 v[66:69], a[4:7], a[168:171], v[66:69]\n\t"
"buffer_load_dwordx4 a[208:211], off, s[80:83], s20 offset:1024 sc1\n\t"
"v_mfma_f32_16x16x32_bf16 v[66:69], a[8:11], a[176:179], v[66:69]\n\t"
"ds_read_b128 a[168:171], v244 offset:9216\n\t"
"v_cvt_pk_bf16_f32 v236, v236, v237\n\t"
"v_mfma_f32_16x16x32_bf16 v[66:69], a[0:3], a[172:175], v[66:69]\n\t"
"v_cvt_pk_bf16_f32 v237, v238, v239\n\t"
"v_cvt_pk_bf16_f32 v238, v232, v233\n\t"
"v_mfma_f32_16x16x32_bf16 v[70:73], a[4:7], a[172:175], v[70:73]\n\t"
"ds_read_b128 a[172:175], v244 offset:9728\n\t"
"v_cvt_pk_bf16_f32 v239, v234, v235\n\t"
"v_mfma_f32_16x16x32_bf16 v[70:73], a[0:3], a[176:179], v[70:73]\n\t"
"v_cvt_pk_bf16_f32 v228, v228, v229\n\t"
"v_cvt_pk_bf16_f32 v229, v230, v231\n\t"
"v_mfma_f32_16x16x32_bf16 v[70:73], a[8:11], a[180:183], v[70:73]\n\t"
"buffer_load_dwordx4 v[248:251], off, s[80:83], s20 offset:2048 sc1\n\t"
"v_cvt_pk_bf16_f32 v230, v224, v225\n\t"
"v_cvt_pk_bf16_f32 v231, v226, v227\n\t"
"s_mov_b32 s20, 172032\n\t"
"v_mfma_f32_16x16x32_bf16 v[74:77], a[8:11], a[184:187], v[74:77]\n\t"
"buffer_load_dwordx4 a[212:215], off, s[80:83], s20 offset:0 sc1\n\t"
"v_mfma_f32_16x16x32_bf16 v[74:77], a[0:3], a[180:183], v[74:77]\n\t"
"v_mfma_f32_16x16x32_bf16 v[74:77], a[4:7], a[176:179], v[74:77]\n\t"
"ds_write_b128 v245, v[236:239] offset:26112\n\t"
"v_mfma_f32_16x16x32_bf16 v[78:81], a[4:7], a[180:183], v[78:81]\n\t"
"v_mfma_f32_16x16x32_bf16 v[78:81], a[0:3], a[184:187], v[78:81]\n\t"
"buffer_load_dwordx4 a[216:219], off, s[80:83], s20 offset:1024 sc1\n\t"
"v_mfma_f32_16x16x32_bf16 v[78:81], a[8:11], a[188:191], v[78:81]\n\t"
"ds_write_b128 v245, v[228:231] offset:32256\n\t"
"v_mfma_f32_16x16x32_bf16 v[82:85], a[8:11], a[192:195], v[82:85]\n\t"
"v_mfma_f32_16x16x32_bf16 v[82:85], a[0:3], a[188:191], v[82:85]\n\t"
"ds_read_b128 a[176:179], v244 offset:10240\n\t"
"v_mfma_f32_16x16x32_bf16 v[82:85], a[4:7], a[184:187], v[82:85]\n\t"
"ds_read_b64_tr_b16 v[232:233], v246 offset:39936\n\t"
"v_mfma_f32_16x16x32_bf16 v[86:89], a[4:7], a[188:191], v[86:89]\n\t"
"ds_read_b64_tr_b16 v[224:225], v255 offset:39936\n\t"
"v_mfma_f32_16x16x32_bf16 v[86:89], a[0:3], a[192:195], v[86:89]\n\t"
"ds_read_b128 a[180:183], v244 offset:10752\n\t"
"v_mfma_f32_16x16x32_bf16 v[86:89], a[8:11], a[196:199], v[86:89]\n\t"
"buffer_load_dwordx4 a[220:223], off, s[80:83], s20 offset:2048 sc1\n\t"
"s_mov_b32 s20, 36864\n\t"
"v_mfma_f32_16x16x32_bf16 v[90:93], a[8:11], a[200:203], v[90:93]\n\t"
"ds_read_b64_tr_b16 v[236:237], v246 offset:46080\n\t"
"v_mfma_f32_16x16x32_bf16 v[90:93], a[0:3], a[196:199], v[90:93]\n\t"
"ds_read_b64_tr_b16 v[228:229], v255 offset:46080\n\t"
"v_mfma_f32_16x16x32_bf16 v[90:93], a[4:7], a[192:195], v[90:93]\n\t"
"ds_read_b128 a[184:187], v244 offset:11264\n\t"
"v_mfma_f32_16x16x32_bf16 v[94:97], a[4:7], a[196:199], v[94:97]\n\t"
"buffer_load_dwordx4 a[224:227], off, s[80:83], s20 offset:0 sc1\n\t"
"v_mfma_f32_16x16x32_bf16 v[94:97], a[0:3], a[200:203], v[94:97]\n\t"
"s_waitcnt lgkmcnt(4)\n\t"
"ds_read_b128 a[188:191], v244 offset:11776\n\t"
"v_mfma_f32_16x16x32_bf16 v[94:97], a[8:11], a[204:207], v[94:97]\n\t"
"buffer_load_dwordx4 a[228:231], off, s[80:83], s20 offset:1024 sc1\n\t"
"v_mfma_f32_4x4x4_16b_bf16 v[232:235], v[232:233], v[220:221], 0\n\t"
"s_waitcnt lgkmcnt(2)\n\t"
"v_mfma_f32_4x4x4_16b_bf16 v[224:227], v[224:225], v[220:221], 0\n\t"
"s_waitcnt vmcnt(26)\n\t"
"v_mfma_f32_4x4x4_16b_bf16 v[236:239], v[236:237], v[220:221], 0\n\t"
"ds_read_b128 a[192:195], v244 offset:12288\n\t"
"v_mfma_f32_4x4x4_16b_bf16 v[228:231], v[228:229], v[220:221], 0\n\t"
"v_mfma_f32_16x16x32_bf16 v[100:103], a[16:19], a[168:171], v[100:103]\n\t"
"ds_read_b128 a[196:199], v244 offset:12800\n\t"
"v_cvt_pk_bf16_f32 v232, v232, v233\n\t"
"v_mfma_f32_16x16x32_bf16 v[100:103], a[24:27], a[176:179], v[100:103]\n\t"
"ds_read_b128 a[200:203], v244 offset:13312\n\t"
"v_cvt_pk_bf16_f32 v233, v234, v235\n\t"
"v_mfma_f32_16x16x32_bf16 v[100:103], v[204:207], a[172:175], v[100:103]\n\t"
"buffer_load_dwordx4 a[232:235], off, s[80:83], s20 offset:2048 sc1\n\t"
"s_mov_b32 s20, 49152\n\t"
"v_mfma_f32_16x16x32_bf16 v[104:107], a[16:19], a[172:175], v[104:107]\n\t"
"buffer_load_dwordx4 a[236:239], off, s[80:83], s20 offset:0 sc1\n\t"
"v_mfma_f32_16x16x32_bf16 v[104:107], v[204:207], a[176:179], v[104:107]\n\t"
"s_waitcnt lgkmcnt(4)\n\t"
"ds_read_b128 a[204:207], v244 offset:13824\n\t"
"v_cvt_pk_bf16_f32 v234, v224, v225\n\t"
"v_mfma_f32_16x16x32_bf16 v[104:107], a[24:27], a[180:183], v[104:107]\n\t"
"v_cvt_pk_bf16_f32 v235, v226, v227\n\t"
"v_cvt_pk_bf16_f32 v236, v236, v237\n\t"
"v_cvt_pk_bf16_f32 v237, v238, v239\n\t"
"v_cvt_pk_bf16_f32 v238, v228, v229\n\t"
"v_cvt_pk_bf16_f32 v239, v230, v231\n\t"
"v_mfma_f32_16x16x32_bf16 v[108:111], a[24:27], a[184:187], v[108:111]\n\t"
"s_waitcnt lgkmcnt(4)\n\t"
"v_fma_f32 v196, -v2, s56, -v100\n\t"
"v_fma_f32 v197, -v3, s56, -v101\n\t"
"v_mfma_f32_16x16x32_bf16 v[108:111], v[204:207], a[180:183], v[108:111]\n\t"
"v_fma_f32 v198, -v4, s56, -v102\n\t"
"v_fma_f32 v199, -v5, s56, -v103\n\t"
"v_mfma_f32_16x16x32_bf16 v[108:111], a[16:19], a[176:179], v[108:111]\n\t"
"ds_write_b128 v245, v[232:235] offset:39936\n\t"
"v_mfma_f32_16x16x32_bf16 v[112:115], a[16:19], a[180:183], v[112:115]\n\t"
"buffer_load_dwordx4 a[240:243], off, s[80:83], s20 offset:1024 sc1\n\t"
"v_mfma_f32_16x16x32_bf16 v[112:115], v[204:207], a[184:187], v[112:115]\n\t"
"buffer_load_dwordx4 a[244:247], off, s[80:83], s20 offset:2048 sc1\n\t"
"v_mfma_f32_16x16x32_bf16 v[112:115], a[24:27], a[188:191], v[112:115]\n\t"
"s_waitcnt lgkmcnt(4)\n\t"
"ds_write_b128 v245, v[236:239] offset:46080\n\t"
"s_mov_b32 s20, 61440\n\t"
"v_mfma_f32_16x16x32_bf16 v[116:119], a[24:27], a[192:195], v[116:119]\n\t"
"buffer_load_dwordx4 a[248:251], off, s[80:83], s20 offset:0 sc1\n\t"
"v_mfma_f32_16x16x32_bf16 v[116:119], v[204:207], a[188:191], v[116:119]\n\t"
"s_waitcnt lgkmcnt(4)\n\t"
"ds_read_b64_tr_b16 v[224:225], v246 offset:52224\n\t"
"v_fma_f32 v212, -v10, s56, -v108\n\t"
"v_mfma_f32_16x16x32_bf16 v[116:119], a[16:19], a[184:187], v[116:119]\n\t"
"ds_read_b64_tr_b16 v[228:229], v255 offset:52224\n\t"
"v_fma_f32 v213, -v11, s56, -v109\n\t"
"v_mfma_f32_16x16x32_bf16 v[120:123], a[16:19], a[188:191], v[120:123]\n\t"
"s_waitcnt lgkmcnt(5)\n\t"
"v_fma_f32 v214, -v12, s56, -v110\n\t"
"v_fma_f32 v215, -v13, s56, -v111\n\t"
"v_mfma_f32_16x16x32_bf16 v[120:123], v[204:207], a[192:195], v[120:123]\n\t"
"v_fma_f32 v98, -v14, s56, -v112\n\t"
"v_fma_f32 v99, -v15, s56, -v113\n\t"
"v_mfma_f32_16x16x32_bf16 v[120:123], a[24:27], a[196:199], v[120:123]\n\t"
"buffer_load_dwordx4 a[252:255], off, s[80:83], s20 offset:1024 sc1\n\t"
"v_mfma_f32_16x16x32_bf16 v[124:127], a[24:27], a[200:203], v[124:127]\n\t"
"s_waitcnt lgkmcnt(4)\n\t"
"v_accvgpr_write_b32 a0, v98\n\t"
"v_fma_f32 v98, -v16, s56, -v114\n\t"
"v_mfma_f32_16x16x32_bf16 v[124:127], v[204:207], a[196:199], v[124:127]\n\t"
"v_accvgpr_write_b32 a1, v99\n\t"
"v_fma_f32 v99, -v17, s56, -v115\n\t"
"v_mfma_f32_16x16x32_bf16 v[124:127], a[16:19], a[192:195], v[124:127]\n\t"
"v_accvgpr_write_b32 a2, v98\n\t"
"v_accvgpr_write_b32 a3, v99\n\t"
"v_mfma_f32_16x16x32_bf16 v[128:131], a[16:19], a[196:199], v[128:131]\n\t"
"s_waitcnt lgkmcnt(1)\n\t"
"v_fma_f32 v98, -v18, s56, -v116\n\t"
"v_fma_f32 v99, -v19, s56, -v117\n\t"
"v_mfma_f32_16x16x32_bf16 v[128:131], v[204:207], a[200:203], v[128:131]\n\t"
"s_waitcnt lgkmcnt(0)\n\t"
"v_fma_f32 v204, -v6, s56, -v104\n\t"
"v_fma_f32 v205, -v7, s56, -v105\n\t"
"v_mfma_f32_16x16x32_bf16 v[128:131], a[24:27], a[204:207], v[128:131]\n\t"
"s_waitcnt vmcnt(29)\n\t"
"v_fma_f32 v206, -v8, s56, -v106\n\t"
"v_fma_f32 v207, -v9, s56, -v107\n\t"
"v_mfma_f32_4x4x4_16b_bf16 v[224:227], v[224:225], v[220:221], 0\n\t"
"v_mfma_f32_4x4x4_16b_bf16 v[228:231], v[228:229], v[220:221], 0\n\t"
"v_mfma_f32_16x16x32_bf16 v[132:135], a[32:35], a[168:171], v[132:135]\n\t"
"v_accvgpr_write_b32 a8, v98\n\t"
"v_fma_f32 v98, -v20, s56, -v118\n\t"
"v_mfma_f32_16x16x32_bf16 v[132:135], a[48:51], a[176:179], v[132:135]\n\t"
"v_cvt_pk_bf16_f32 v224, v224, v225\n\t"
"v_cvt_pk_bf16_f32 v225, v226, v227\n\t"
"v_mfma_f32_16x16x32_bf16 v[132:135], a[40:43], a[172:175], v[132:135]\n\t"
"v_cvt_pk_bf16_f32 v226, v228, v229\n\t"
"v_cvt_pk_bf16_f32 v227, v230, v231\n\t"
"v_mfma_f32_16x16x32_bf16 v[136:139], a[32:35], a[172:175], v[136:139]\n\t"
"v_accvgpr_write_b32 a9, v99\n\t"
"v_fma_f32 v99, -v21, s56, -v119\n\t"
"v_mfma_f32_16x16x32_bf16 v[136:139], a[40:43], a[176:179], v[136:139]\n\t"
"v_accvgpr_write_b32 a10, v98\n\t"
"v_accvgpr_write_b32 a11, v99\n\t"
"v_mfma_f32_16x16x32_bf16 v[136:139], a[48:51], a[180:183], v[136:139]\n\t"
"ds_write_b128 v245, v[224:227] offset:52224\n\t"
"v_mfma_f32_16x16x32_bf16 v[140:143], a[48:51], a[184:187], v[140:143]\n\t"
"v_fma_f32 v98, -v22, s56, -v120\n\t"
"v_fma_f32 v99, -v23, s56, -v121\n\t"
"v_mfma_f32_16x16x32_bf16 v[140:143], a[40:43], a[180:183], v[140:143]\n\t"
"v_accvgpr_write_b32 a16, v98\n\t"
"v_fma_f32 v98, -v24, s56, -v122\n\t"
"v_mfma_f32_16x16x32_bf16 v[140:143], a[32:35], a[176:179], v[140:143]\n\t"
"v_accvgpr_write_b32 a17, v99\n\t"
"v_fma_f32 v99, -v25, s56, -v123\n\t"
"v_mfma_f32_16x16x32_bf16 v[144:147], a[32:35], a[180:183], v[144:147]\n\t"
"v_accvgpr_write_b32 a18, v98\n\t"
"v_accvgpr_write_b32 a19, v99\n\t"
"v_mfma_f32_16x16x32_bf16 v[144:147], a[40:43], a[184:187], v[144:147]\n\t"
"v_fma_f32 v98, -v26, s56, -v124\n\t"
"v_fma_f32 v99, -v27, s56, -v125\n\t"
"v_mfma_f32_16x16x32_bf16 v[144:147], a[48:51], a[188:191], v[144:147]\n\t"
"v_accvgpr_write_b32 a24, v98\n\t"
"v_fma_f32 v98, -v28, s56, -v126\n\t"
"v_mfma_f32_16x16x32_bf16 v[148:151], a[48:51], a[192:195], v[148:151]\n\t"
"v_accvgpr_write_b32 a25, v99\n\t"
"v_fma_f32 v99, -v29, s56, -v127\n\t"
"v_mfma_f32_16x16x32_bf16 v[148:151], a[40:43], a[188:191], v[148:151]\n\t"
"v_accvgpr_write_b32 a26, v98\n\t"
"v_accvgpr_write_b32 a27, v99\n\t"
"v_mfma_f32_16x16x32_bf16 v[148:151], a[32:35], a[184:187], v[148:151]\n\t"
"v_fma_f32 v200, v100, s64, -v2\n\t"
"v_fma_f32 v201, v101, s64, -v3\n\t"
"v_mfma_f32_16x16x32_bf16 v[152:155], a[32:35], a[188:191], v[152:155]\n\t"
"v_fma_f32 v202, v102, s64, -v4\n\t"
"v_fma_f32 v203, v103, s64, -v5\n\t"
"v_mfma_f32_16x16x32_bf16 v[152:155], a[40:43], a[192:195], v[152:155]\n\t"
"v_fma_f32 v208, v104, s64, -v6\n\t"
"v_fma_f32 v209, v105, s64, -v7\n\t"
"v_mfma_f32_16x16x32_bf16 v[152:155], a[48:51], a[196:199], v[152:155]\n\t"
"v_fma_f32 v210, v106, s64, -v8\n\t"
"v_fma_f32 v211, v107, s64, -v9\n\t"
"v_mfma_f32_16x16x32_bf16 v[156:159], a[48:51], a[200:203], v[156:159]\n\t"
"v_fma_f32 v216, v108, s64, -v10\n\t"
"v_fma_f32 v217, v109, s64, -v11\n\t"
"v_mfma_f32_16x16x32_bf16 v[156:159], a[40:43], a[196:199], v[156:159]\n\t"
"v_fma_f32 v218, v110, s64, -v12\n\t"
"v_fma_f32 v219, v111, s64, -v13\n\t"
"v_mfma_f32_16x16x32_bf16 v[156:159], a[32:35], a[192:195], v[156:159]\n\t"
"v_fma_f32 v0, v2, s52, v100\n\t"
"v_fma_f32 v1, v3, s52, v101\n\t"
"v_mfma_f32_16x16x32_bf16 v[160:163], a[32:35], a[196:199], v[160:163]\n\t"
"s_waitcnt vmcnt(26)\n\t"
"v_fma_f32 v98, -v30, s56, -v128\n\t"
"v_fma_f32 v99, -v31, s56, -v129\n\t"
"v_mfma_f32_16x16x32_bf16 v[160:163], a[40:43], a[200:203], v[160:163]\n\t"
"v_accvgpr_write_b32 a32, v98\n\t"
"v_fma_f32 v98, -v32, s56, -v130\n\t"
"v_mfma_f32_16x16x32_bf16 v[160:163], a[48:51], a[204:207], v[160:163]\n\t"
"v_accvgpr_write_b32 a33, v99\n\t"
"v_fma_f32 v99, -v33, s56, -v131\n\t"
"v_mfma_f32_16x16x32_bf16 v[164:167], a[80:83], a[168:171], v[164:167]\n\t"
"v_accvgpr_write_b32 a34, v98\n\t"
"v_accvgpr_write_b32 a35, v99\n\t"
"v_mfma_f32_16x16x32_bf16 v[164:167], a[96:99], a[176:179], v[164:167]\n\t"
"v_fma_f32 v98, -v34, s56, -v132\n\t"
"v_mfma_f32_16x16x32_bf16 v[164:167], a[88:91], a[172:175], v[164:167]\n\t"
"s_waitcnt lgkmcnt(0)\n\t"
"s_barrier\n\t"
"v_mfma_f32_16x16x32_bf16 v[168:171], a[80:83], a[172:175], v[168:171]\n\t"
"ds_read_b128 a[168:171], v222 offset:0\n\t"
"v_mfma_f32_16x16x32_bf16 v[168:171], a[88:91], a[176:179], v[168:171]\n\t"
"ds_read_b128 a[172:175], v223 offset:6144\n\t"
"v_fma_f32 v99, -v35, s56, -v133\n\t"
"v_mfma_f32_16x16x32_bf16 v[168:171], a[96:99], a[180:183], v[168:171]\n\t"
"v_accvgpr_write_b32 a40, v98\n\t"
"v_fma_f32 v98, -v36, s56, -v134\n\t"
"v_mfma_f32_16x16x32_bf16 v[172:175], a[96:99], a[184:187], v[172:175]\n\t"
"v_accvgpr_write_b32 a41, v99\n\t"
"v_fma_f32 v99, -v37, s56, -v135\n\t"
"v_mfma_f32_16x16x32_bf16 v[172:175], a[88:91], a[180:183], v[172:175]\n\t"
"v_accvgpr_write_b32 a42, v98\n\t"
"v_accvgpr_write_b32 a43, v99\n\t"
"v_mfma_f32_16x16x32_bf16 v[172:175], a[80:83], a[176:179], v[172:175]\n\t"
"ds_read_b128 a[176:179], v223 offset:12288\n\t"
"v_fma_f32 v98, -v38, s56, -v136\n\t"
"v_mfma_f32_16x16x32_bf16 v[176:179], a[80:83], a[180:183], v[176:179]\n\t"
"ds_read_b128 a[180:183], v222 offset:19968\n\t"
"v_fma_f32 v99, -v39, s56, -v137\n\t"
"v_mfma_f32_16x16x32_bf16 v[176:179], a[88:91], a[184:187], v[176:179]\n\t"
"v_accvgpr_write_b32 a48, v98\n\t"
"v_fma_f32 v98, -v40, s56, -v138\n\t"
"v_mfma_f32_16x16x32_bf16 v[176:179], a[96:99], a[188:191], v[176:179]\n\t"
"Lpg2_%=:\n\t"
"s_cbranch_vccnz Lpg6_%=\n\t"
"v_accvgpr_write_b32 a49, v99\n\t"
"v_fma_f32 v99, -v41, s56, -v139\n\t"
"v_mfma_f32_16x16x32_bf16 v[180:183], a[96:99], a[192:195], v[180:183]\n\t"
"v_accvgpr_write_b32 a50, v98\n\t"
"v_accvgpr_write_b32 a51, v99\n\t"
"v_mfma_f32_16x16x32_bf16 v[180:183], a[88:91], a[188:191], v[180:183]\n\t"
"v_fma_f32 v98, -v42, s56, -v140\n\t"
"v_fma_f32 v99, -v43, s56, -v141\n\t"
"v_mfma_f32_16x16x32_bf16 v[180:183], a[80:83], a[184:187], v[180:183]\n\t"
"ds_read_b128 a[184:187], v223 offset:26112\n\t"
"v_accvgpr_write_b32 a56, v98\n\t"
"v_mfma_f32_16x16x32_bf16 v[184:187], a[80:83], a[188:191], v[184:187]\n\t"
"ds_read_b128 a[188:191], v223 offset:32256\n\t"
"v_fma_f32 v98, -v44, s56, -v142\n\t"
"v_mfma_f32_16x16x32_bf16 v[184:187], a[88:91], a[192:195], v[184:187]\n\t"
"v_accvgpr_write_b32 a57, v99\n\t"
"v_fma_f32 v99, -v45, s56, -v143\n\t"
"v_mfma_f32_16x16x32_bf16 v[184:187], a[96:99], a[196:199], v[184:187]\n\t"
"v_accvgpr_write_b32 a58, v98\n\t"
"v_accvgpr_write_b32 a59, v99\n\t"
"v_mfma_f32_16x16x32_bf16 v[188:191], a[96:99], a[200:203], v[188:191]\n\t"
"v_fma_f32 v98, -v46, s56, -v144\n\t"
"v_fma_f32 v99, -v47, s56, -v145\n\t"
"v_mfma_f32_16x16x32_bf16 v[188:191], a[88:91], a[196:199], v[188:191]\n\t"
"v_accvgpr_write_b32 a64, v98\n\t"
"v_fma_f32 v98, -v48, s56, -v146\n\t"
"v_mfma_f32_16x16x32_bf16 v[188:191], a[80:83], a[192:195], v[188:191]\n\t"
"ds_read_b128 a[192:195], v222 offset:39936\n\t"
"v_accvgpr_write_b32 a65, v99\n\t"
"v_mfma_f32_16x16x32_bf16 v[192:195], a[80:83], a[196:199], v[192:195]\n\t"
"s_waitcnt vmcnt(23) lgkmcnt(4)\n\t"
"ds_read_b128 a[196:199], v223 offset:46080\n\t"
"v_fma_f32 v99, -v49, s56, -v147\n\t"
"v_mfma_f32_16x16x32_bf16 v[192:195], a[88:91], a[200:203], v[192:195]\n\t"
"ds_read_b128 a[200:203], v223 offset:52224\n\t"
"v_accvgpr_write_b32 a66, v98\n\t"
"v_mfma_f32_16x16x32_bf16 v[192:195], a[96:99], a[204:207], v[192:195]\n\t"
"ds_read_b128 a[204:207], v222 offset:59904\n\t"
"v_accvgpr_write_b32 a67, v99\n\t"
"Lst2_%=:\n\t"
"v_mfma_f32_16x16x32_bf16 v[196:199], a[12:15], a[168:171], v[196:199]\n\t"
"s_waitcnt lgkmcnt(6)\n\t"
"v_fma_f32 v98, -v50, s56, -v148\n\t"
"v_fma_f32 v99, -v51, s56, -v149\n\t"
"v_mfma_f32_16x16x32_bf16 v[196:199], a[28:31], a[176:179], v[196:199]\n\t"
"v_accvgpr_write_b32 a72, v98\n\t"
"v_fma_f32 v98, -v52, s56, -v150\n\t"
"v_mfma_f32_16x16x32_bf16 v[196:199], a[20:23], a[172:175], v[196:199]\n\t"
"v_accvgpr_write_b32 a73, v99\n\t"
"v_fma_f32 v99, -v53, s56, -v151\n\t"
"v_mfma_f32_16x16x32_bf16 v[204:207], a[12:15], a[172:175], v[204:207]\n\t"
"s_waitcnt lgkmcnt(5)\n\t"
"v_accvgpr_write_b32 a74, v98\n\t"
"v_accvgpr_write_b32 a75, v99\n\t"
"v_mfma_f32_16x16x32_bf16 v[204:207], a[20:23], a[176:179], v[204:207]\n\t"
"v_fma_f32 v98, -v54, s56, -v152\n\t"
"v_fma_f32 v99, -v55, s56, -v153\n\t"
"v_mfma_f32_16x16x32_bf16 v[204:207], a[28:31], a[180:183], v[204:207]\n\t"
"v_accvgpr_write_b32 a80, v98\n\t"
"v_fma_f32 v98, -v56, s56, -v154\n\t"
"v_mfma_f32_16x16x32_bf16 v[212:215], a[28:31], a[184:187], v[212:215]\n\t"
"s_waitcnt lgkmcnt(4)\n\t"
"v_accvgpr_write_b32 a81, v99\n\t"
"v_fma_f32 v99, -v57, s56, -v155\n\t"
"v_mfma_f32_16x16x32_bf16 v[212:215], a[20:23], a[180:183], v[212:215]\n\t"
"v_accvgpr_write_b32 a82, v98\n\t"
"v_accvgpr_write_b32 a83, v99\n\t"
"v_mfma_f32_16x16x32_bf16 v[212:215], a[12:15], a[176:179], v[212:215]\n\t"
"v_fma_f32 v98, -v58, s56, -v156\n\t"
"v_fma_f32 v99, -v59, s56, -v157\n\t"
"v_mfma_f32_16x16x32_bf16 a[0:3], a[12:15], a[180:183], a[0:3]\n\t"
"s_waitcnt lgkmcnt(3)\n\t"
"v_accvgpr_write_b32 a88, v98\n\t"
"v_fma_f32 v98, -v60, s56, -v158\n\t"
"v_mfma_f32_16x16x32_bf16 a[0:3], a[20:23], a[184:187], a[0:3]\n\t"
"v_accvgpr_write_b32 a89, v99\n\t"
"v_fma_f32 v99, -v61, s56, -v159\n\t"
"v_mfma_f32_16x16x32_bf16 a[0:3], a[28:31], a[188:191], a[0:3]\n\t"
"v_accvgpr_write_b32 a90, v98\n\t"
"v_accvgpr_write_b32 a91, v99\n\t"
"v_mfma_f32_16x16x32_bf16 a[8:11], a[28:31], a[192:195], a[8:11]\n\t"
"s_waitcnt lgkmcnt(2)\n\t"
"v_fma_f32 v98, -v62, s56, -v160\n\t"
"v_fma_f32 v99, -v63, s56, -v161\n\t"
"v_mfma_f32_16x16x32_bf16 a[8:11], a[20:23], a[188:191], a[8:11]\n\t"
"v_accvgpr_write_b32 a96, v98\n\t"
"v_fma_f32 v98, -v64, s56, -v162\n\t"
"v_mfma_f32_16x16x32_bf16 a[8:11], a[12:15], a[184:187], a[8:11]\n\t"
"v_accvgpr_write_b32 a97, v99\n\t"
"v_fma_f32 v99, -v65, s56, -v163\n\t"
"v_mfma_f32_16x16x32_bf16 a[16:19], a[12:15], a[188:191], a[16:19]\n\t"
"s_waitcnt lgkmcnt(1)\n\t"
"v_accvgpr_write_b32 a98, v98\n\t"
"v_accvgpr_write_b32 a99, v99\n\t"
"v_mfma_f32_16x16x32_bf16 a[16:19], a[20:23], a[192:195], a[16:19]\n\t"
"v_fma_f32 v98, -v66, s56, -v164\n\t"
"v_fma_f32 v99, -v67, s56, -v165\n\t"
"v_mfma_f32_16x16x32_bf16 a[16:19], a[28:31], a[196:199], a[16:19]\n\t"
"v_accvgpr_write_b32 a104, v98\n\t"
"v_fma_f32 v98, -v68, s56, -v166\n\t"
"v_mfma_f32_16x16x32_bf16 a[24:27], a[28:31], a[200:203], a[24:27]\n\t"
"s_waitcnt lgkmcnt(0)\n\t"
"v_accvgpr_write_b32 a105, v99\n\t"
"v_fma_f32 v99, -v69, s56, -v167\n\t"
"v_mfma_f32_16x16x32_bf16 a[24:27], a[20:23], a[196:199], a[24:27]\n\t"
"v_accvgpr_write_b32 a106, v98\n\t"
"v_accvgpr_write_b32 a107, v99\n\t"
"v_mfma_f32_16x16x32_bf16 a[24:27], a[12:15], a[192:195], a[24:27]\n\t"
"v_fma_f32 v98, -v70, s56, -v168\n\t"
"v_fma_f32 v99, -v71, s56, -v169\n\t"
"v_mfma_f32_16x16x32_bf16 a[32:35], a[12:15], a[196:199], a[32:35]\n\t"
"s_waitcnt vmcnt(20)\n\t"
"v_accvgpr_write_b32 a112, v98\n\t"
"v_fma_f32 v98, -v72, s56, -v170\n\t"
"v_mfma_f32_16x16x32_bf16 a[32:35], a[20:23], a[200:203], a[32:35]\n\t"
"v_accvgpr_write_b32 a113, v99\n\t"
"v_fma_f32 v99, -v73, s56, -v171\n\t"
"v_mfma_f32_16x16x32_bf16 a[32:35], a[28:31], a[204:207], a[32:35]\n\t"
"v_accvgpr_write_b32 a114, v98\n\t"
"v_accvgpr_write_b32 a115, v99\n\t"
"v_mfma_f32_16x16x32_bf16 a[40:43], a[36:39], a[168:171], a[40:43]\n\t"
"v_fma_f32 v98, -v74, s56, -v172\n\t"
"v_fma_f32 v99, -v75, s56, -v173\n\t"
"v_mfma_f32_16x16x32_bf16 a[40:43], a[52:55], a[176:179], a[40:43]\n\t"
"v_accvgpr_write_b32 a120, v98\n\t"
"v_fma_f32 v98, -v76, s56, -v174\n\t"
"v_mfma_f32_16x16x32_bf16 a[40:43], a[44:47], a[172:175], a[40:43]\n\t"
"v_accvgpr_write_b32 a121, v99\n\t"
"v_fma_f32 v99, -v77, s56, -v175\n\t"
"v_mfma_f32_16x16x32_bf16 a[48:51], a[36:39], a[172:175], a[48:51]\n\t"
"v_accvgpr_write_b32 a122, v98\n\t"
"v_accvgpr_write_b32 a123, v99\n\t"
"v_mfma_f32_16x16x32_bf16 a[48:51], a[44:47], a[176:179], a[48:51]\n\t"
"v_fma_f32 v98, -v78, s56, -v176\n\t"
"v_fma_f32 v99, -v79, s56, -v177\n\t"
"v_mfma_f32_16x16x32_bf16 a[48:51], a[52:55], a[180:183], a[48:51]\n\t"
"v_accvgpr_write_b32 a128, v98\n\t"
"v_fma_f32 v98, -v80, s56, -v178\n\t"
"v_mfma_f32_16x16x32_bf16 a[56:59], a[52:55], a[184:187], a[56:59]\n\t"
"v_accvgpr_write_b32 a129, v99\n\t"
"v_fma_f32 v99, -v81, s56, -v179\n\t"
"v_mfma_f32_16x16x32_bf16 a[56:59], a[44:47], a[180:183], a[56:59]\n\t"
"v_accvgpr_write_b32 a130, v98\n\t"
"v_accvgpr_write_b32 a131, v99\n\t"
"v_mfma_f32_16x16x32_bf16 a[56:59], a[36:39], a[176:179], a[56:59]\n\t"
"v_fma_f32 v98, -v82, s56, -v180\n\t"
"v_fma_f32 v99, -v83, s56, -v181\n\t"
"v_mfma_f32_16x16x32_bf16 a[64:67], a[36:39], a[180:183], a[64:67]\n\t"
"v_accvgpr_write_b32 a136, v98\n\t"
"v_fma_f32 v98, -v84, s56, -v182\n\t"
"v_mfma_f32_16x16x32_bf16 a[64:67], a[44:47], a[184:187], a[64:67]\n\t"
"v_accvgpr_write_b32 a137, v99\n\t"
"v_fma_f32 v99, -v85, s56, -v183\n\t"
"v_mfma_f32_16x16x32_bf16 a[64:67], a[52:55], a[188:191], a[64:67]\n\t"
"v_accvgpr_write_b32 a138, v98\n\t"
"v_accvgpr_write_b32 a139, v99\n\t"
"v_mfma_f32_16x16x32_bf16 a[72:75], a[52:55], a[192:195], a[72:75]\n\t"
"v_fma_f32 v98, -v86, s56, -v184\n\t"
"v_fma_f32 v99, -v87, s56, -v185\n\t"
"v_mfma_f32_16x16x32_bf16 a[72:75], a[44:47], a[188:191], a[72:75]\n\t"
"v_accvgpr_write_b32 a144, v98\n\t"
"v_fma_f32 v98, -v88, s56, -v186\n\t"
"v_mfma_f32_16x16x32_bf16 a[72:75], a[36:39], a[184:187], a[72:75]\n\t"
"v_accvgpr_write_b32 a145, v99\n\t"
"v_fma_f32 v99, -v89, s56, -v187\n\t"
"v_mfma_f32_16x16x32_bf16 a[80:83], a[36:39], a[188:191], a[80:83]\n\t"
"v_accvgpr_write_b32 a146, v98\n\t"
"v_accvgpr_write_b32 a147, v99\n\t"
"v_mfma_f32_16x16x32_bf16 a[80:83], a[44:47], a[192:195], a[80:83]\n\t"
"v_fma_f32 v98, -v90, s56, -v188\n\t"
"v_fma_f32 v99, -v91, s56, -v189\n\t"
"v_mfma_f32_16x16x32_bf16 a[80:83], a[52:55], a[196:199], a[80:83]\n\t"
"v_accvgpr_write_b32 a152, v98\n\t"
"v_fma_f32 v98, -v92, s56, -v190\n\t"
"v_mfma_f32_16x16x32_bf16 a[88:91], a[52:55], a[200:203], a[88:91]\n\t"
"v_accvgpr_write_b32 a153, v99\n\t"
"v_fma_f32 v99, -v93, s56, -v191\n\t"
"v_mfma_f32_16x16x32_bf16 a[88:91], a[44:47], a[196:199], a[88:91]\n\t"
"v_accvgpr_write_b32 a154, v98\n\t"
"v_accvgpr_write_b32 a155, v99\n\t"
"v_mfma_f32_16x16x32_bf16 a[88:91], a[36:39], a[192:195], a[88:91]\n\t"
"v_fma_f32 v98, -v94, s56, -v192\n\t"
"v_fma_f32 v99, -v95, s56, -v193\n\t"
"v_mfma_f32_16x16x32_bf16 a[96:99], a[36:39], a[196:199], a[96:99]\n\t"
"s_waitcnt vmcnt(17)\n\t"
"v_accvgpr_write_b32 a160, v98\n\t"
"v_fma_f32 v98, -v96, s56, -v194\n\t"
"v_mfma_f32_16x16x32_bf16 a[96:99], a[44:47], a[200:203], a[96:99]\n\t"
"v_accvgpr_write_b32 a161, v99\n\t"
"v_fma_f32 v99, -v97, s56, -v195\n\t"
"v_mfma_f32_16x16x32_bf16 a[96:99], a[52:55], a[204:207], a[96:99]\n\t"
"v_accvgpr_write_b32 a162, v98\n\t"
"v_accvgpr_write_b32 a163, v99\n\t"
"v_mfma_f32_16x16x32_bf16 a[104:107], a[84:87], a[168:171], a[104:107]\n\t"
"ds_read_b128 a[168:171], v222 offset:256\n\t"
"v_fma_f32 v98, v112, s64, -v14\n\t"
"v_mfma_f32_16x16x32_bf16 a[104:107], a[100:103], a[176:179], a[104:107]\n\t"
"v_fma_f32 v99, v113, s64, -v15\n\t"
"v_accvgpr_write_b32 a4, v98\n\t"
"v_mfma_f32_16x16x32_bf16 a[104:107], a[92:95], a[172:175], a[104:107]\n\t"
"v_fma_f32 v98, v114, s64, -v16\n\t"
"v_accvgpr_write_b32 a5, v99\n\t"
"v_mfma_f32_16x16x32_bf16 a[112:115], a[84:87], a[172:175], a[112:115]\n\t"
"ds_read_b128 a[172:175], v223 offset:6400\n\t"
"v_fma_f32 v99, v115, s64, -v17\n\t"
"v_mfma_f32_16x16x32_bf16 a[112:115], a[92:95], a[176:179], a[112:115]\n\t"
"v_accvgpr_write_b32 a6, v98\n\t"
"v_accvgpr_write_b32 a7, v99\n\t"
"v_mfma_f32_16x16x32_bf16 a[112:115], a[100:103], a[180:183], a[112:115]\n\t"
"v_fma_f32 v98, v116, s64, -v18\n\t"
"v_fma_f32 v99, v117, s64, -v19\n\t"
"v_mfma_f32_16x16x32_bf16 a[120:123], a[100:103], a[184:187], a[120:123]\n\t"
"v_accvgpr_write_b32 a12, v98\n\t"
"v_fma_f32 v98, v118, s64, -v20\n\t"
"v_mfma_f32_16x16x32_bf16 a[120:123], a[92:95], a[180:183], a[120:123]\n\t"
"v_accvgpr_write_b32 a13, v99\n\t"
"v_fma_f32 v99, v119, s64, -v21\n\t"
"v_mfma_f32_16x16x32_bf16 a[120:123], a[84:87], a[176:179], a[120:123]\n\t"
"ds_read_b128 a[176:179], v223 offset:12544\n\t"
"v_accvgpr_write_b32 a14, v98\n\t"
"v_mfma_f32_16x16x32_bf16 a[128:131], a[84:87], a[180:183], a[128:131]\n\t"
"ds_read_b128 a[180:183], v222 offset:20224\n\t"
"v_accvgpr_write_b32 a15, v99\n\t"
"v_mfma_f32_16x16x32_bf16 a[128:131], a[92:95], a[184:187], a[128:131]\n\t"
"v_fma_f32 v98, v120, s64, -v22\n\t"
"v_fma_f32 v99, v121, s64, -v23\n\t"
"v_mfma_f32_16x16x32_bf16 a[128:131], a[100:103], a[188:191], a[128:131]\n\t"
"v_accvgpr_write_b32 a20, v98\n\t"
"v_fma_f32 v98, v122, s64, -v24\n\t"
"v_mfma_f32_16x16x32_bf16 a[136:139], a[100:103], a[192:195], a[136:139]\n\t"
"v_accvgpr_write_b32 a21, v99\n\t"
"v_fma_f32 v99, v123, s64, -v25\n\t"
"v_mfma_f32_16x16x32_bf16 a[136:139], a[92:95], a[188:191], a[136:139]\n\t"
"v_accvgpr_write_b32 a22, v98\n\t"
"v_accvgpr_write_b32 a23, v99\n\t"
"v_mfma_f32_16x16x32_bf16 a[136:139], a[84:87], a[184:187], a[136:139]\n\t"
"ds_read_b128 a[184:187], v223 offset:26368\n\t"
"v_fma_f32 v98, v124, s64, -v26\n\t"
"v_mfma_f32_16x16x32_bf16 a[144:147], a[84:87], a[188:191], a[144:147]\n\t"
"ds_read_b128 a[188:191], v223 offset:32512\n\t"
"v_fma_f32 v99, v125, s64, -v27\n\t"
"v_mfma_f32_16x16x32_bf16 a[144:147], a[92:95], a[192:195], a[144:147]\n\t"
"v_accvgpr_write_b32 a28, v98\n\t"
"v_fma_f32 v98, v126, s64, -v28\n\t"
"v_mfma_f32_16x16x32_bf16 a[144:147], a[100:103], a[196:199], a[144:147]\n\t"
"v_accvgpr_write_b32 a29, v99\n\t"
"v_fma_f32 v99, v127, s64, -v29\n\t"
"v_mfma_f32_16x16x32_bf16 a[152:155], a[100:103], a[200:203], a[152:155]\n\t"
"v_accvgpr_write_b32 a30, v98\n\t"
"v_accvgpr_write_b32 a31, v99\n\t"
"v_mfma_f32_16x16x32_bf16 a[152:155], a[92:95], a[196:199], a[152:155]\n\t"
"v_fma_f32 v98, v128, s64, -v30\n\t"
"v_fma_f32 v99, v129, s64, -v31\n\t"
"v_mfma_f32_16x16x32_bf16 a[152:155], a[84:87], a[192:195], a[152:155]\n\t"
"ds_read_b128 a[192:195], v222 offset:40192\n\t"
"v_accvgpr_write_b32 a36, v98\n\t"
"v_mfma_f32_16x16x32_bf16 a[160:163], a[84:87], a[196:199], a[160:163]\n\t"
"s_waitcnt vmcnt(14) lgkmcnt(4)\n\t"
"ds_read_b128 a[196:199], v223 offset:46336\n\t"
"v_fma_f32 v98, v130, s64, -v32\n\t"
"v_mfma_f32_16x16x32_bf16 a[160:163], a[92:95], a[200:203], a[160:163]\n\t"
"ds_read_b128 a[200:203], v223 offset:52480\n\t"
"v_accvgpr_write_b32 a37, v99\n\t"
"v_mfma_f32_16x16x32_bf16 a[160:163], a[100:103], a[204:207], a[160:163]\n\t"
"ds_read_b128 a[204:207], v222 offset:60160\n\t"
"v_fma_f32 v99, v131, s64, -v33\n\t"
"v_mfma_f32_16x16x32_bf16 v[200:203], a[140:143], a[168:171], v[200:203]\n\t"
"s_waitcnt lgkmcnt(6)\n\t"
"v_accvgpr_write_b32 a38, v98\n\t"
"v_accvgpr_write_b32 a39, v99\n\t"
"v_mfma_f32_16x16x32_bf16 v[200:203], a[156:159], a[176:179], v[200:203]\n\t"
"v_fma_f32 v98, v132, s64, -v34\n\t"
"v_fma_f32 v99, v133, s64, -v35\n\t"
"v_mfma_f32_16x16x32_bf16 v[200:203], a[148:151], a[172:175], v[200:203]\n\t"
"s_add_u32 m0, s25, 896\n\t"
"v_accvgpr_write_b32 a44, v98\n\t"
"v_fma_f32 v98, v134, s64, -v36\n\t"
"v_mfma_f32_16x16x32_bf16 v[208:211], a[140:143], a[172:175], v[208:211]\n\t"
"s_waitcnt lgkmcnt(5)\n\t"
"v_accvgpr_write_b32 a45, v99\n\t"
"v_fma_f32 v99, v135, s64, -v37\n\t"
"v_mfma_f32_16x16x32_bf16 v[208:211], a[148:151], a[176:179], v[208:211]\n\t"
"v_accvgpr_write_b32 a46, v98\n\t"
"v_mfma_f32_16x16x32_bf16 v[208:211], a[156:159], a[180:183], v[208:211]\n\t"
"s_waitcnt lgkmcnt(0)\n\t"
"s_barrier\n\t"
"v_mfma_f32_16x16x32_bf16 v[216:219], a[156:159], a[184:187], v[216:219]\n\t"
"v_accvgpr_write_b32 a47, v99\n\t"
"v_mfma_f32_16x16x32_bf16 v[216:219], a[148:151], a[180:183], v[216:219]\n\t"
"buffer_load_dwordx4 v254, s[12:15], 0 offen offset:128 nt lds\n\t"
"v_mfma_f32_16x16x32_bf16 v[216:219], a[140:143], a[176:179], v[216:219]\n\t"
"s_add_u32 m0, s26, 896\n\t"
"ds_read_b64_tr_b16 v[232:233], v246 offset:512\n\t"
"v_fma_f32 v98, v136, s64, -v38\n\t"
"v_mfma_f32_16x16x32_bf16 a[4:7], a[140:143], a[180:183], a[4:7]\n\t"
"ds_read_b64_tr_b16 v[236:237], v255 offset:512\n\t"
"v_fma_f32 v99, v137, s64, -v39\n\t"
"v_mfma_f32_16x16x32_bf16 a[4:7], a[148:151], a[184:187], a[4:7]\n\t"
"v_accvgpr_write_b32 a52, v98\n\t"
"v_fma_f32 v98, v138, s64, -v40\n\t"
"v_mfma_f32_16x16x32_bf16 a[4:7], a[156:159], a[188:191], a[4:7]\n\t"
"s_mov_b64 exec, s[36:37]\n\t"
"buffer_load_dwordx4 v254, s[12:15], s27 offen offset:128 nt lds\n\t"
"s_mov_b64 exec, -1\n\t"
"s_add_u32 m0, s24, 2432\n\t"
"s_add_u32 s28, s21, 3072\n\t"
"v_mfma_f32_16x16x32_bf16 a[12:15], a[156:159], a[192:195], a[12:15]\n\t"
"buffer_load_dwordx4 v247, s[12:15], s28 offen offset:128 nt lds\n\t"
"v_mfma_f32_16x16x32_bf16 a[12:15], a[148:151], a[188:191], a[12:15]\n\t"
"s_add_u32 m0, s24, 8576\n\t"
"s_add_u32 s28, s21, 199680\n\t"
"v_accvgpr_write_b32 a53, v99\n\t"
"v_mfma_f32_16x16x32_bf16 a[12:15], a[140:143], a[184:187], a[12:15]\n\t"
"buffer_load_dwordx4 v247, s[12:15], s28 offen offset:128 nt lds\n\t"
"s_add_u32 m0, s24, 14720\n\t"
"s_add_u32 s28, s21, 396288\n\t"
"v_mfma_f32_16x16x32_bf16 a[20:23], a[140:143], a[188:191], a[20:23]\n\t"
"buffer_load_dwordx4 v247, s[12:15], s28 offen offset:128 nt lds\n\t"
"v_mfma_f32_16x16x32_bf16 a[20:23], a[148:151], a[192:195], a[20:23]\n\t"
"s_add_u32 m0, s24, 22400\n\t"
"s_add_u32 s28, s21, 592896\n\t"
"v_fma_f32 v99, v139, s64, -v41\n\t"
"v_mfma_f32_16x16x32_bf16 a[20:23], a[156:159], a[196:199], a[20:23]\n\t"
"buffer_load_dwordx4 v247, s[12:15], s28 offen offset:128 nt lds\n\t"
"s_add_u32 m0, s24, 28544\n\t"
"s_add_u32 s28, s21, 789504\n\t"
"v_mfma_f32_16x16x32_bf16 a[28:31], a[156:159], a[200:203], a[28:31]\n\t"
"buffer_load_dwordx4 v247, s[12:15], s28 offen offset:128 nt lds\n\t"
"v_mfma_f32_16x16x32_bf16 a[28:31], a[148:151], a[196:199], a[28:31]\n\t"
"s_add_u32 m0, s24, 34688\n\t"
"s_add_u32 s28, s21, 986112\n\t"
"v_accvgpr_write_b32 a54, v98\n\t"
"v_mfma_f32_16x16x32_bf16 a[28:31], a[140:143], a[192:195], a[28:31]\n\t"
"v_accvgpr_write_b32 a55, v99\n\t"
"v_fma_f32 v98, v140, s64, -v42\n\t"
"v_mfma_f32_16x16x32_bf16 a[36:39], a[140:143], a[196:199], a[36:39]\n\t"
"s_waitcnt lgkmcnt(1)\n\t"
"v_fma_f32 v99, v141, s64, -v43\n\t"
"v_accvgpr_write_b32 a60, v98\n\t"
"v_mfma_f32_16x16x32_bf16 a[36:39], a[148:151], a[200:203], a[36:39]\n\t"
"s_waitcnt lgkmcnt(0)\n\t"
"v_fma_f32 v98, v142, s64, -v44\n\t"
"v_accvgpr_write_b32 a61, v99\n\t"
"v_mfma_f32_16x16x32_bf16 a[36:39], a[156:159], a[204:207], a[36:39]\n\t"
"buffer_load_dwordx4 v247, s[12:15], s28 offen offset:128 nt lds\n\t"
"v_mfma_f32_4x4x4_16b_bf16 v[232:235], v[232:233], v[220:221], 0\n\t"
"s_waitcnt vmcnt(19)\n\t"
"v_mfma_f32_4x4x4_16b_bf16 v[236:239], v[236:237], v[220:221], 0\n\t"
"v_mfma_f32_16x16x32_bf16 a[44:47], v[240:243], a[168:171], a[44:47]\n\t"
"v_fma_f32 v99, v143, s64, -v45\n\t"
"v_accvgpr_write_b32 a62, v98\n\t"
"v_mfma_f32_16x16x32_bf16 a[44:47], v[248:251], a[176:179], a[44:47]\n\t"
"v_accvgpr_write_b32 a63, v99\n\t"
"v_fma_f32 v98, v144, s64, -v46\n\t"
"v_mfma_f32_16x16x32_bf16 a[44:47], a[208:211], a[172:175], a[44:47]\n\t"
"v_fma_f32 v99, v145, s64, -v47\n\t"
"v_cvt_pk_bf16_f32 v232, v232, v233\n\t"
"v_mfma_f32_16x16x32_bf16 a[52:55], v[240:243], a[172:175], a[52:55]\n\t"
"v_cvt_pk_bf16_f32 v233, v234, v235\n\t"
"v_cvt_pk_bf16_f32 v234, v236, v237\n\t"
"v_mfma_f32_16x16x32_bf16 a[52:55], a[208:211], a[176:179], a[52:55]\n\t"
"v_cvt_pk_bf16_f32 v235, v238, v239\n\t"
"v_accvgpr_write_b32 a68, v98\n\t"
"v_mfma_f32_16x16x32_bf16 a[52:55], v[248:251], a[180:183], a[52:55]\n\t"
"ds_write_b128 v245, v[232:235] offset:512\n\t"
"v_fma_f32 v98, v146, s64, -v48\n\t"
"v_mfma_f32_16x16x32_bf16 a[60:63], v[248:251], a[184:187], a[60:63]\n\t"
"v_accvgpr_write_b32 a69, v99\n\t"
"v_fma_f32 v99, v147, s64, -v49\n\t"
"v_mfma_f32_16x16x32_bf16 a[60:63], a[208:211], a[180:183], a[60:63]\n\t"
"v_accvgpr_write_b32 a70, v98\n\t"
"v_accvgpr_write_b32 a71, v99\n\t"
"v_mfma_f32_16x16x32_bf16 a[60:63], v[240:243], a[176:179], a[60:63]\n\t"
"ds_read_b64_tr_b16 v[228:229], v246 offset:6656\n\t"
"v_fma_f32 v98, v148, s64, -v50\n\t"
"v_fma_f32 v99, v149, s64, -v51\n\t"
"v_mfma_f32_16x16x32_bf16 a[68:71], v[240:243], a[180:183], a[68:71]\n\t"
"ds_read_b64_tr_b16 v[224:225], v255 offset:6656\n\t"
"v_accvgpr_write_b32 a76, v98\n\t"
"v_mfma_f32_16x16x32_bf16 a[68:71], a[208:211], a[184:187], a[68:71]\n\t"
"v_fma_f32 v98, v150, s64, -v52\n\t"
"v_accvgpr_write_b32 a77, v99\n\t"
"v_mfma_f32_16x16x32_bf16 a[68:71], v[248:251], a[188:191], a[68:71]\n\t"
"v_fma_f32 v99, v151, s64, -v53\n\t"
"v_accvgpr_write_b32 a78, v98\n\t"
"v_accvgpr_write_b32 a79, v99\n\t"
"v_fma_f32 v98, v152, s64, -v54\n\t"
"v_fma_f32 v99, v153, s64, -v55\n\t"
"v_mfma_f32_16x16x32_bf16 a[76:79], v[248:251], a[192:195], a[76:79]\n\t"
"v_accvgpr_write_b32 a84, v98\n\t"
"v_fma_f32 v98, v154, s64, -v56\n\t"
"v_mfma_f32_16x16x32_bf16 a[76:79], a[208:211], a[188:191], a[76:79]\n\t"
"v_accvgpr_write_b32 a85, v99\n\t"
"v_fma_f32 v99, v155, s64, -v57\n\t"
"v_mfma_f32_16x16x32_bf16 a[76:79], v[240:243], a[184:187], a[76:79]\n\t"
"v_accvgpr_write_b32 a86, v98\n\t"
"v_accvgpr_write_b32 a87, v99\n\t"
"v_fma_f32 v98, v156, s64, -v58\n\t"
"v_fma_f32 v99, v157, s64, -v59\n\t"
"v_mfma_f32_16x16x32_bf16 a[84:87], v[240:243], a[188:191], a[84:87]\n\t"
"v_accvgpr_write_b32 a92, v98\n\t"
"v_fma_f32 v98, v158, s64, -v60\n\t"
"v_mfma_f32_16x16x32_bf16 a[84:87], a[208:211], a[192:195], a[84:87]\n\t"
"v_accvgpr_write_b32 a93, v99\n\t"
"v_fma_f32 v99, v159, s64, -v61\n\t"
"v_mfma_f32_16x16x32_bf16 a[84:87], v[248:251], a[196:199], a[84:87]\n\t"
"v_accvgpr_write_b32 a94, v98\n\t"
"v_accvgpr_write_b32 a95, v99\n\t"
"v_fma_f32 v98, v160, s64, -v62\n\t"
"v_fma_f32 v99, v161, s64, -v63\n\t"
"v_mfma_f32_16x16x32_bf16 a[92:95], v[248:251], a[200:203], a[92:95]\n\t"
"v_accvgpr_write_b32 a100, v98\n\t"
"v_fma_f32 v98, v162, s64, -v64\n\t"
"v_mfma_f32_16x16x32_bf16 a[92:95], a[208:211], a[196:199], a[92:95]\n\t"
"v_accvgpr_write_b32 a101, v99\n\t"
"v_fma_f32 v99, v163, s64, -v65\n\t"
"v_mfma_f32_16x16x32_bf16 a[92:95], v[240:243], a[192:195], a[92:95]\n\t"
"v_accvgpr_write_b32 a102, v98\n\t"
"v_accvgpr_write_b32 a103, v99\n\t"
"s_nop 1\n\t"
"v_mfma_f32_16x16x32_bf16 a[100:103], v[240:243], a[196:199], a[100:103]\n\t"
"buffer_load_dwordx4 v[240:243], off, s[80:83], s20 offset:2048 sc1\n\t"
"v_mfma_f32_16x16x32_bf16 a[100:103], a[208:211], a[200:203], a[100:103]\n\t"
"s_waitcnt lgkmcnt(0)\n\t"
"v_fma_f32 v98, v164, s64, -v66\n\t"
"v_fma_f32 v99, v165, s64, -v67\n\t"
"v_mfma_f32_16x16x32_bf16 a[100:103], v[248:251], a[204:207], a[100:103]\n\t"
"v_accvgpr_write_b32 a108, v98\n\t"
"v_fma_f32 v98, v166, s64, -v68\n\t"
"v_accvgpr_write_b32 a109, v99\n\t"
"v_fma_f32 v99, v167, s64, -v69\n\t"
"v_accvgpr_write_b32 a110, v98\n\t"
"v_accvgpr_write_b32 a111, v99\n\t"
"v_mfma_f32_4x4x4_16b_bf16 v[228:231], v[228:229], v[220:221], 0\n\t"
"s_waitcnt vmcnt(17)\n\t"
"v_fma_f32 v98, v168, s64, -v70\n\t"
"v_fma_f32 v99, v169, s64, -v71\n\t"
"v_mfma_f32_4x4x4_16b_bf16 v[224:227], v[224:225], v[220:221], 0\n\t"
"v_mfma_f32_16x16x32_bf16 a[108:111], a[212:215], a[168:171], a[108:111]\n\t"
"ds_read_b128 a[168:171], v222 offset:1536\n\t"
"v_accvgpr_write_b32 a116, v98\n\t"
"v_mfma_f32_16x16x32_bf16 a[108:111], a[220:223], a[176:179], a[108:111]\n\t"
"v_fma_f32 v98, v170, s64, -v72\n\t"
"v_accvgpr_write_b32 a117, v99\n\t"
"v_mfma_f32_16x16x32_bf16 a[108:111], a[216:219], a[172:175], a[108:111]\n\t"
"v_fma_f32 v99, v171, s64, -v73\n\t"
"v_accvgpr_write_b32 a118, v98\n\t"
"v_accvgpr_write_b32 a119, v99\n\t"
"v_fma_f32 v98, v172, s64, -v74\n\t"
"v_fma_f32 v99, v173, s64, -v75\n\t"
"v_mfma_f32_16x16x32_bf16 a[116:119], a[212:215], a[172:175], a[116:119]\n\t"
"ds_read_b128 a[172:175], v223 offset:7680\n\t"
"v_accvgpr_write_b32 a124, v98\n\t"
"v_mfma_f32_16x16x32_bf16 a[116:119], a[216:219], a[176:179], a[116:119]\n\t"
"v_fma_f32 v98, v174, s64, -v76\n\t"
"v_accvgpr_write_b32 a125, v99\n\t"
"v_mfma_f32_16x16x32_bf16 a[116:119], a[220:223], a[180:183], a[116:119]\n\t"
"v_fma_f32 v99, v175, s64, -v77\n\t"
"v_accvgpr_write_b32 a126, v98\n\t"
"v_accvgpr_write_b32 a127, v99\n\t"
"v_fma_f32 v98, v176, s64, -v78\n\t"
"v_fma_f32 v99, v177, s64, -v79\n\t"
"v_cvt_pk_bf16_f32 v228, v228, v229\n\t"
"v_cvt_pk_bf16_f32 v229, v230, v231\n\t"
"v_cvt_pk_bf16_f32 v230, v224, v225\n\t"
"v_cvt_pk_bf16_f32 v231, v226, v227\n\t"
"v_mfma_f32_16x16x32_bf16 a[124:127], a[220:223], a[184:187], a[124:127]\n\t"
"v_accvgpr_write_b32 a132, v98\n\t"
"v_fma_f32 v98, v178, s64, -v80\n\t"
"v_mfma_f32_16x16x32_bf16 a[124:127], a[216:219], a[180:183], a[124:127]\n\t"
"v_accvgpr_write_b32 a133, v99\n\t"
"v_fma_f32 v99, v179, s64, -v81\n\t"
"v_mfma_f32_16x16x32_bf16 a[124:127], a[212:215], a[176:179], a[124:127]\n\t"
"ds_write_b128 v245, v[228:231] offset:6656\n\t"
"v_accvgpr_write_b32 a134, v98\n\t"
"v_accvgpr_write_b32 a135, v99\n\t"
"v_fma_f32 v98, v180, s64, -v82\n\t"
"v_fma_f32 v99, v181, s64, -v83\n\t"
"v_mfma_f32_16x16x32_bf16 a[132:135], a[212:215], a[180:183], a[132:135]\n\t"
"ds_read_b128 a[176:179], v223 offset:13824\n\t"
"v_mfma_f32_16x16x32_bf16 a[132:135], a[216:219], a[184:187], a[132:135]\n\t"
"ds_read_b128 a[180:183], v222 offset:21504\n\t"
"v_accvgpr_write_b32 a140, v98\n\t"
"v_mfma_f32_16x16x32_bf16 a[132:135], a[220:223], a[188:191], a[132:135]\n\t"
"ds_read_b64_tr_b16 v[236:237], v246 offset:12800\n\t"
"v_fma_f32 v98, v182, s64, -v84\n\t"
"v_accvgpr_write_b32 a141, v99\n\t"
"v_fma_f32 v99, v183, s64, -v85\n\t"
"v_accvgpr_write_b32 a142, v98\n\t"
"v_accvgpr_write_b32 a143, v99\n\t"
"v_fma_f32 v98, v184, s64, -v86\n\t"
"v_fma_f32 v99, v185, s64, -v87\n\t"
"v_mfma_f32_16x16x32_bf16 a[140:143], a[220:223], a[192:195], a[140:143]\n\t"
"s_mov_b32 s20, 73728\n\t"
"ds_read_b64_tr_b16 v[232:233], v255 offset:12800\n\t"
"v_accvgpr_write_b32 a148, v98\n\t"
"v_mfma_f32_16x16x32_bf16 a[140:143], a[216:219], a[188:191], a[140:143]\n\t"
"v_fma_f32 v98, v186, s64, -v88\n\t"
"v_accvgpr_write_b32 a149, v99\n\t"
"v_mfma_f32_16x16x32_bf16 a[140:143], a[212:215], a[184:187], a[140:143]\n\t"
"ds_read_b128 a[184:187], v223 offset:27648\n\t"
"v_fma_f32 v99, v187, s64, -v89\n\t"
"v_accvgpr_write_b32 a150, v98\n\t"
"v_accvgpr_write_b32 a151, v99\n\t"
"v_fma_f32 v98, v188, s64, -v90\n\t"
"v_fma_f32 v99, v189, s64, -v91\n\t"
"v_mfma_f32_16x16x32_bf16 a[148:151], a[212:215], a[188:191], a[148:151]\n\t"
"ds_read_b128 a[188:191], v223 offset:33792\n\t"
"v_accvgpr_write_b32 a156, v98\n\t"
"v_mfma_f32_16x16x32_bf16 a[148:151], a[216:219], a[192:195], a[148:151]\n\t"
"v_fma_f32 v98, v190, s64, -v92\n\t"
"v_accvgpr_write_b32 a157, v99\n\t"
"v_mfma_f32_16x16x32_bf16 a[148:151], a[220:223], a[196:199], a[148:151]\n\t"
"v_fma_f32 v99, v191, s64, -v93\n\t"
"v_accvgpr_write_b32 a158, v98\n\t"
"v_accvgpr_write_b32 a159, v99\n\t"
"v_fma_f32 v98, v192, s64, -v94\n\t"
"v_fma_f32 v99, v193, s64, -v95\n\t"
"v_mfma_f32_16x16x32_bf16 a[156:159], a[220:223], a[200:203], a[156:159]\n\t"
"s_waitcnt lgkmcnt(2)\n\t"
"v_accvgpr_write_b32 a164, v98\n\t"
"v_fma_f32 v98, v194, s64, -v96\n\t"
"v_mfma_f32_16x16x32_bf16 a[156:159], a[216:219], a[196:199], a[156:159]\n\t"
"Lpg3_%=:\n\t"
"s_cbranch_vccnz Lpg7_%=\n\t"
"v_accvgpr_write_b32 a165, v99\n\t"
"v_fma_f32 v99, v195, s64, -v97\n\t"
"v_mfma_f32_16x16x32_bf16 a[156:159], a[212:215], a[192:195], a[156:159]\n\t"
"ds_read_b128 a[192:195], v222 offset:41472\n\t"
"v_accvgpr_write_b32 a166, v98\n\t"
"v_accvgpr_write_b32 a167, v99\n\t"
"s_nop 1\n\t"
"v_mfma_f32_16x16x32_bf16 a[164:167], a[212:215], a[196:199], a[164:167]\n\t"
"buffer_load_dwordx4 a[208:211], off, s[80:83], s20 offset:0 sc1\n\t"
"v_mfma_f32_16x16x32_bf16 a[164:167], a[216:219], a[200:203], a[164:167]\n\t"
"buffer_load_dwordx4 v[248:251], off, s[80:83], s20 offset:1024 sc1\n\t"
"v_mfma_f32_16x16x32_bf16 a[164:167], a[220:223], a[204:207], a[164:167]\n\t"
"ds_read_b128 a[196:199], v223 offset:47616\n\t"
"v_fma_f32 v100, -v100, s66, v2\n\t"
"v_fma_f32 v2, v4, s52, v102\n\t"
"v_fma_f32 v101, -v101, s66, v3\n\t"
"v_fma_f32 v3, v5, s52, v103\n\t"
"v_fma_f32 v102, -v102, s66, v4\n\t"
"v_fma_f32 v103, -v103, s66, v5\n\t"
"v_mfma_f32_4x4x4_16b_bf16 v[236:239], v[236:237], v[220:221], 0\n\t"
"s_waitcnt vmcnt(16)\n\t"
"v_mfma_f32_4x4x4_16b_bf16 v[232:235], v[232:233], v[220:221], 0\n\t"
"v_fma_f32 v4, v6, s52, v104\n\t"
"v_fma_f32 v5, v7, s52, v105\n\t"
"Lst3_%=:\n\t"
"v_mfma_f32_16x16x32_bf16 v[0:3], a[224:227], a[168:171], v[0:3]\n\t"
"ds_read_b128 a[200:203], v223 offset:53760\n\t"
"v_fma_f32 v104, -v104, s66, v6\n\t"
"v_mfma_f32_16x16x32_bf16 v[0:3], a[232:235], a[176:179], v[0:3]\n\t"
"ds_read_b128 a[204:207], v222 offset:61440\n\t"
"v_fma_f32 v6, v8, s52, v106\n\t"
"v_mfma_f32_16x16x32_bf16 v[0:3], a[228:231], a[172:175], v[0:3]\n\t"
"buffer_load_dwordx4 a[212:215], off, s[80:83], s20 offset:2048 sc1\n\t"
"v_fma_f32 v105, -v105, s66, v7\n\t"
"v_fma_f32 v7, v9, s52, v107\n\t"
"v_fma_f32 v106, -v106, s66, v8\n\t"
"v_fma_f32 v107, -v107, s66, v9\n\t"
"v_fma_f32 v8, v10, s52, v108\n\t"
"v_fma_f32 v9, v11, s52, v109\n\t"
"s_mov_b32 s20, 86016\n\t"
"v_mfma_f32_16x16x32_bf16 v[4:7], a[224:227], a[172:175], v[4:7]\n\t"
"buffer_load_dwordx4 a[216:219], off, s[80:83], s20 offset:0 sc1\n\t"
"v_mfma_f32_16x16x32_bf16 v[4:7], a[228:231], a[176:179], v[4:7]\n\t"
"buffer_load_dwordx4 a[220:223], off, s[80:83], s20 offset:1024 sc1\n\t"
"v_mfma_f32_16x16x32_bf16 v[4:7], a[232:235], a[180:183], v[4:7]\n\t"
"v_fma_f32 v108, -v108, s66, v10\n\t"
"v_fma_f32 v10, v12, s52, v110\n\t"
"v_fma_f32 v109, -v109, s66, v11\n\t"
"v_fma_f32 v11, v13, s52, v111\n\t"
"v_fma_f32 v110, -v110, s66, v12\n\t"
"v_fma_f32 v111, -v111, s66, v13\n\t"
"v_cvt_pk_bf16_f32 v236, v236, v237\n\t"
"v_cvt_pk_bf16_f32 v237, v238, v239\n\t"
"v_fma_f32 v12, v14, s52, v112\n\t"
"v_fma_f32 v13, v15, s52, v113\n\t"
"v_cvt_pk_bf16_f32 v238, v232, v233\n\t"
"v_cvt_pk_bf16_f32 v239, v234, v235\n\t"
"s_waitcnt lgkmcnt(5)\n\t"
"v_mfma_f32_16x16x32_bf16 v[8:11], a[232:235], a[184:187], v[8:11]\n\t"
"v_fma_f32 v112, -v112, s66, v14\n\t"
"v_fma_f32 v14, v16, s52, v114\n\t"
"v_mfma_f32_16x16x32_bf16 v[8:11], a[228:231], a[180:183], v[8:11]\n\t"
"v_fma_f32 v113, -v113, s66, v15\n\t"
"v_fma_f32 v15, v17, s52, v115\n\t"
"v_mfma_f32_16x16x32_bf16 v[8:11], a[224:227], a[176:179], v[8:11]\n\t"
"ds_write_b128 v245, v[236:239] offset:12800\n\t"
"v_fma_f32 v114, -v114, s66, v16\n\t"
"v_fma_f32 v115, -v115, s66, v17\n\t"
"v_fma_f32 v16, v18, s52, v116\n\t"
"v_fma_f32 v17, v19, s52, v117\n\t"
"s_waitcnt lgkmcnt(5)\n\t"
"v_mfma_f32_16x16x32_bf16 v[12:15], a[224:227], a[180:183], v[12:15]\n\t"
"ds_read_b64_tr_b16 v[224:225], v246 offset:20480\n\t"
"v_fma_f32 v116, -v116, s66, v18\n\t"
"v_mfma_f32_16x16x32_bf16 v[12:15], a[228:231], a[184:187], v[12:15]\n\t"
"ds_read_b64_tr_b16 v[228:229], v255 offset:20480\n\t"
"v_fma_f32 v18, v20, s52, v118\n\t"
"v_mfma_f32_16x16x32_bf16 v[12:15], a[232:235], a[188:191], v[12:15]\n\t"
"v_fma_f32 v117, -v117, s66, v19\n\t"
"v_fma_f32 v19, v21, s52, v119\n\t"
"v_fma_f32 v118, -v118, s66, v20\n\t"
"v_fma_f32 v119, -v119, s66, v21\n\t"
"v_fma_f32 v20, v22, s52, v120\n\t"
"v_fma_f32 v21, v23, s52, v121\n\t"
"s_waitcnt lgkmcnt(6)\n\t"
"v_mfma_f32_16x16x32_bf16 v[16:19], a[232:235], a[192:195], v[16:19]\n\t"
"v_fma_f32 v120, -v120, s66, v22\n\t"
"v_fma_f32 v22, v24, s52, v122\n\t"
"v_mfma_f32_16x16x32_bf16 v[16:19], a[228:231], a[188:191], v[16:19]\n\t"
"v_fma_f32 v121, -v121, s66, v23\n\t"
"v_fma_f32 v23, v25, s52, v123\n\t"
"v_mfma_f32_16x16x32_bf16 v[16:19], a[224:227], a[184:187], v[16:19]\n\t"
"v_fma_f32 v122, -v122, s66, v24\n\t"
"v_fma_f32 v123, -v123, s66, v25\n\t"
"v_fma_f32 v24, v26, s52, v124\n\t"
"v_fma_f32 v25, v27, s52, v125\n\t"
"s_waitcnt lgkmcnt(5)\n\t"
"v_mfma_f32_16x16x32_bf16 v[20:23], a[224:227], a[188:191], v[20:23]\n\t"
"v_fma_f32 v124, -v124, s66, v26\n\t"
"v_fma_f32 v26, v28, s52, v126\n\t"
"v_mfma_f32_16x16x32_bf16 v[20:23], a[228:231], a[192:195], v[20:23]\n\t"
"v_fma_f32 v125, -v125, s66, v27\n\t"
"v_fma_f32 v27, v29, s52, v127\n\t"
"v_mfma_f32_16x16x32_bf16 v[20:23], a[232:235], a[196:199], v[20:23]\n\t"
"v_fma_f32 v126, -v126, s66, v28\n\t"
"v_fma_f32 v127, -v127, s66, v29\n\t"
"v_fma_f32 v28, v30, s52, v128\n\t"
"v_fma_f32 v29, v31, s52, v129\n\t"
"s_waitcnt lgkmcnt(4)\n\t"
"v_mfma_f32_16x16x32_bf16 v[24:27], a[232:235], a[200:203], v[24:27]\n\t"
"v_fma_f32 v128, -v128, s66, v30\n\t"
"v_fma_f32 v30, v32, s52, v130\n\t"
"v_mfma_f32_16x16x32_bf16 v[24:27], a[228:231], a[196:199], v[24:27]\n\t"
"v_fma_f32 v129, -v129, s66, v31\n\t"
"v_fma_f32 v31, v33, s52, v131\n\t"
"v_mfma_f32_16x16x32_bf16 v[24:27], a[224:227], a[192:195], v[24:27]\n\t"
"v_fma_f32 v130, -v130, s66, v32\n\t"
"v_fma_f32 v131, -v131, s66, v33\n\t"
"s_waitcnt lgkmcnt(3)\n\t"
"v_mfma_f32_16x16x32_bf16 v[28:31], a[224:227], a[196:199], v[28:31]\n\t"
"buffer_load_dwordx4 a[224:227], off, s[80:83], s20 offset:2048 sc1\n\t"
"v_mfma_f32_16x16x32_bf16 v[28:31], a[228:231], a[200:203], v[28:31]\n\t"
"s_waitcnt lgkmcnt(0)\n\t"
"s_mov_b32 s20, 98304\n\t"
"v_fma_f32 v32, v34, s52, v132\n\t"
"v_mfma_f32_16x16x32_bf16 v[28:31], a[232:235], a[204:207], v[28:31]\n\t"
"v_fma_f32 v33, v35, s52, v133\n\t"
"v_fma_f32 v132, -v132, s66, v34\n\t"
"v_fma_f32 v34, v36, s52, v134\n\t"
"v_fma_f32 v133, -v133, s66, v35\n\t"
"v_fma_f32 v35, v37, s52, v135\n\t"
"v_fma_f32 v134, -v134, s66, v36\n\t"
"v_fma_f32 v135, -v135, s66, v37\n\t"
"v_mfma_f32_4x4x4_16b_bf16 v[224:227], v[224:225], v[220:221], 0\n\t"
"s_waitcnt vmcnt(17)\n\t"
"v_mfma_f32_4x4x4_16b_bf16 v[228:231], v[228:229], v[220:221], 0\n\t"
"v_fma_f32 v36, v38, s52, v136\n\t"
"v_fma_f32 v37, v39, s52, v137\n\t"
"v_mfma_f32_16x16x32_bf16 v[32:35], a[236:239], a[168:171], v[32:35]\n\t"
"v_fma_f32 v136, -v136, s66, v38\n\t"
"v_fma_f32 v38, v40, s52, v138\n\t"
"v_mfma_f32_16x16x32_bf16 v[32:35], a[244:247], a[176:179], v[32:35]\n\t"
"v_fma_f32 v137, -v137, s66, v39\n\t"
"v_fma_f32 v39, v41, s52, v139\n\t"
"v_mfma_f32_16x16x32_bf16 v[32:35], a[240:243], a[172:175], v[32:35]\n\t"
"buffer_load_dwordx4 a[228:231], off, s[80:83], s20 offset:0 sc1\n\t"
"v_fma_f32 v138, -v138, s66, v40\n\t"
"v_fma_f32 v139, -v139, s66, v41\n\t"
"v_fma_f32 v40, v42, s52, v140\n\t"
"v_fma_f32 v41, v43, s52, v141\n\t"
"v_mfma_f32_16x16x32_bf16 v[36:39], a[236:239], a[172:175], v[36:39]\n\t"
"v_fma_f32 v140, -v140, s66, v42\n\t"
"v_fma_f32 v42, v44, s52, v142\n\t"
"v_mfma_f32_16x16x32_bf16 v[36:39], a[240:243], a[176:179], v[36:39]\n\t"
"v_fma_f32 v141, -v141, s66, v43\n\t"
"v_fma_f32 v43, v45, s52, v143\n\t"
"v_mfma_f32_16x16x32_bf16 v[36:39], a[244:247], a[180:183], v[36:39]\n\t"
"buffer_load_dwordx4 a[232:235], off, s[80:83], s20 offset:1024 sc1\n\t"
"v_fma_f32 v142, -v142, s66, v44\n\t"
"v_fma_f32 v143, -v143, s66, v45\n\t"
"v_cvt_pk_bf16_f32 v224, v224, v225\n\t"
"v_cvt_pk_bf16_f32 v225, v226, v227\n\t"
"v_fma_f32 v44, v46, s52, v144\n\t"
"v_fma_f32 v45, v47, s52, v145\n\t"
"v_cvt_pk_bf16_f32 v226, v228, v229\n\t"
"v_cvt_pk_bf16_f32 v227, v230, v231\n\t"
"v_mfma_f32_16x16x32_bf16 v[40:43], a[244:247], a[184:187], v[40:43]\n\t"
"v_fma_f32 v144, -v144, s66, v46\n\t"
"v_fma_f32 v46, v48, s52, v146\n\t"
"v_mfma_f32_16x16x32_bf16 v[40:43], a[240:243], a[180:183], v[40:43]\n\t"
"v_fma_f32 v145, -v145, s66, v47\n\t"
"v_fma_f32 v47, v49, s52, v147\n\t"
"v_mfma_f32_16x16x32_bf16 v[40:43], a[236:239], a[176:179], v[40:43]\n\t"
"ds_write_b128 v245, v[224:227] offset:20480\n\t"
"v_fma_f32 v146, -v146, s66, v48\n\t"
"v_fma_f32 v147, -v147, s66, v49\n\t"
"v_fma_f32 v48, v50, s52, v148\n\t"
"v_fma_f32 v49, v51, s52, v149\n\t"
"v_mfma_f32_16x16x32_bf16 v[44:47], a[236:239], a[180:183], v[44:47]\n\t"
"ds_read_b64_tr_b16 v[232:233], v246 offset:26624\n\t"
"v_mfma_f32_16x16x32_bf16 v[44:47], a[240:243], a[184:187], v[44:47]\n\t"
"ds_read_b64_tr_b16 v[236:237], v255 offset:26624\n\t"
"v_fma_f32 v148, -v148, s66, v50\n\t"
"v_mfma_f32_16x16x32_bf16 v[44:47], a[244:247], a[188:191], v[44:47]\n\t"
"v_fma_f32 v50, v52, s52, v150\n\t"
"v_fma_f32 v149, -v149, s66, v51\n\t"
"v_fma_f32 v51, v53, s52, v151\n\t"
"v_fma_f32 v150, -v150, s66, v52\n\t"
"v_fma_f32 v151, -v151, s66, v53\n\t"
"v_fma_f32 v52, v54, s52, v152\n\t"
"v_fma_f32 v53, v55, s52, v153\n\t"
"v_mfma_f32_16x16x32_bf16 v[48:51], a[244:247], a[192:195], v[48:51]\n\t"
"v_fma_f32 v152, -v152, s66, v54\n\t"
"v_fma_f32 v54, v56, s52, v154\n\t"
"v_mfma_f32_16x16x32_bf16 v[48:51], a[240:243], a[188:191], v[48:51]\n\t"
"v_fma_f32 v153, -v153, s66, v55\n\t"
"v_fma_f32 v55, v57, s52, v155\n\t"
"v_mfma_f32_16x16x32_bf16 v[48:51], a[236:239], a[184:187], v[48:51]\n\t"
"v_fma_f32 v154, -v154, s66, v56\n\t"
"v_fma_f32 v155, -v155, s66, v57\n\t"
"v_fma_f32 v56, v58, s52, v156\n\t"
"v_fma_f32 v57, v59, s52, v157\n\t"
"v_mfma_f32_16x16x32_bf16 v[52:55], a[236:239], a[188:191], v[52:55]\n\t"
"v_fma_f32 v156, -v156, s66, v58\n\t"
"v_fma_f32 v58, v60, s52, v158\n\t"
"v_mfma_f32_16x16x32_bf16 v[52:55], a[240:243], a[192:195], v[52:55]\n\t"
"v_fma_f32 v157, -v157, s66, v59\n\t"
"v_fma_f32 v59, v61, s52, v159\n\t"
"v_mfma_f32_16x16x32_bf16 v[52:55], a[244:247], a[196:199], v[52:55]\n\t"
"v_fma_f32 v158, -v158, s66, v60\n\t"
"v_fma_f32 v159, -v159, s66, v61\n\t"
"v_fma_f32 v60, v62, s52, v160\n\t"
"v_fma_f32 v61, v63, s52, v161\n\t"
"v_mfma_f32_16x16x32_bf16 v[56:59], a[244:247], a[200:203], v[56:59]\n\t"
"v_fma_f32 v160, -v160, s66, v62\n\t"
"v_fma_f32 v62, v64, s52, v162\n\t"
"v_mfma_f32_16x16x32_bf16 v[56:59], a[240:243], a[196:199], v[56:59]\n\t"
"v_fma_f32 v161, -v161, s66, v63\n\t"
"v_fma_f32 v63, v65, s52, v163\n\t"
"v_mfma_f32_16x16x32_bf16 v[56:59], a[236:239], a[192:195], v[56:59]\n\t"
"v_fma_f32 v162, -v162, s66, v64\n\t"
"v_fma_f32 v163, -v163, s66, v65\n\t"
"v_mfma_f32_16x16x32_bf16 v[60:63], a[236:239], a[196:199], v[60:63]\n\t"
"s_waitcnt lgkmcnt(1)\n\t"
"v_fma_f32 v64, v66, s52, v164\n\t"
"v_fma_f32 v65, v67, s52, v165\n\t"
"v_mfma_f32_16x16x32_bf16 v[60:63], a[240:243], a[200:203], v[60:63]\n\t"
"s_waitcnt lgkmcnt(0)\n\t"
"v_fma_f32 v164, -v164, s66, v66\n\t"
"v_fma_f32 v66, v68, s52, v166\n\t"
"v_mfma_f32_16x16x32_bf16 v[60:63], a[244:247], a[204:207], v[60:63]\n\t"
"v_fma_f32 v165, -v165, s66, v67\n\t"
"v_fma_f32 v67, v69, s52, v167\n\t"
"v_fma_f32 v166, -v166, s66, v68\n\t"
"v_fma_f32 v167, -v167, s66, v69\n\t"
"v_mfma_f32_4x4x4_16b_bf16 v[232:235], v[232:233], v[220:221], 0\n\t"
"buffer_load_dwordx4 a[236:239], off, s[80:83], s20 offset:2048 sc1\n\t"
"s_mov_b32 s20, 331776\n\t"
"v_mfma_f32_4x4x4_16b_bf16 v[236:239], v[236:237], v[220:221], 0\n\t"
"s_waitcnt vmcnt(9)\n\t"
"v_fma_f32 v68, v70, s52, v168\n\t"
"v_fma_f32 v69, v71, s52, v169\n\t"
"v_mfma_f32_16x16x32_bf16 v[64:67], a[248:251], a[168:171], v[64:67]\n\t"
"ds_read_b128 a[168:171], v222 offset:1792\n\t"
"v_fma_f32 v168, -v168, s66, v70\n\t"
"v_mfma_f32_16x16x32_bf16 v[64:67], v[240:243], a[176:179], v[64:67]\n\t"
"v_fma_f32 v70, v72, s52, v170\n\t"
"v_fma_f32 v169, -v169, s66, v71\n\t"
"v_mfma_f32_16x16x32_bf16 v[64:67], a[252:255], a[172:175], v[64:67]\n\t"
"buffer_load_dwordx4 a[240:243], off, s[80:83], s20 offset:0 sc1\n\t"
"v_fma_f32 v71, v73, s52, v171\n\t"
"v_fma_f32 v170, -v170, s66, v72\n\t"
"v_fma_f32 v171, -v171, s66, v73\n\t"
"v_fma_f32 v72, v74, s52, v172\n\t"
"v_fma_f32 v73, v75, s52, v173\n\t"
"v_mfma_f32_16x16x32_bf16 v[68:71], a[248:251], a[172:175], v[68:71]\n\t"
"ds_read_b128 a[172:175], v223 offset:7936\n\t"
"v_fma_f32 v172, -v172, s66, v74\n\t"
"v_mfma_f32_16x16x32_bf16 v[68:71], a[252:255], a[176:179], v[68:71]\n\t"
"v_fma_f32 v74, v76, s52, v174\n\t"
"v_fma_f32 v173, -v173, s66, v75\n\t"
"v_mfma_f32_16x16x32_bf16 v[68:71], v[240:243], a[180:183], v[68:71]\n\t"
"buffer_load_dwordx4 a[244:247], off, s[80:83], s20 offset:1024 sc1\n\t"
"v_fma_f32 v75, v77, s52, v175\n\t"
"v_fma_f32 v174, -v174, s66, v76\n\t"
"v_fma_f32 v175, -v175, s66, v77\n\t"
"v_cvt_pk_bf16_f32 v232, v232, v233\n\t"
"v_cvt_pk_bf16_f32 v233, v234, v235\n\t"
"v_fma_f32 v76, v78, s52, v176\n\t"
"v_fma_f32 v77, v79, s52, v177\n\t"
"v_cvt_pk_bf16_f32 v234, v236, v237\n\t"
"v_cvt_pk_bf16_f32 v235, v238, v239\n\t"
"v_mfma_f32_16x16x32_bf16 v[72:75], v[240:243], a[184:187], v[72:75]\n\t"
"v_fma_f32 v176, -v176, s66, v78\n\t"
"v_fma_f32 v78, v80, s52, v178\n\t"
"v_mfma_f32_16x16x32_bf16 v[72:75], a[252:255], a[180:183], v[72:75]\n\t"
"v_fma_f32 v177, -v177, s66, v79\n\t"
"v_fma_f32 v79, v81, s52, v179\n\t"
"v_mfma_f32_16x16x32_bf16 v[72:75], a[248:251], a[176:179], v[72:75]\n\t"
"ds_write_b128 v245, v[232:235] offset:26624\n\t"
"v_fma_f32 v178, -v178, s66, v80\n\t"
"v_fma_f32 v179, -v179, s66, v81\n\t"
"v_fma_f32 v80, v82, s52, v180\n\t"
"v_fma_f32 v81, v83, s52, v181\n\t"
"v_mfma_f32_16x16x32_bf16 v[76:79], a[248:251], a[180:183], v[76:79]\n\t"
"ds_read_b128 a[176:179], v223 offset:14080\n\t"
"v_mfma_f32_16x16x32_bf16 v[76:79], a[252:255], a[184:187], v[76:79]\n\t"
"ds_read_b64_tr_b16 v[228:229], v246 offset:32768\n\t"
"v_fma_f32 v180, -v180, s66, v82\n\t"
"v_mfma_f32_16x16x32_bf16 v[76:79], v[240:243], a[188:191], v[76:79]\n\t"
"ds_read_b64_tr_b16 v[224:225], v255 offset:32768\n\t"
"v_fma_f32 v82, v84, s52, v182\n\t"
"v_fma_f32 v181, -v181, s66, v83\n\t"
"v_fma_f32 v83, v85, s52, v183\n\t"
"v_fma_f32 v182, -v182, s66, v84\n\t"
"v_fma_f32 v183, -v183, s66, v85\n\t"
"v_fma_f32 v84, v86, s52, v184\n\t"
"v_fma_f32 v85, v87, s52, v185\n\t"
"v_mfma_f32_16x16x32_bf16 v[80:83], v[240:243], a[192:195], v[80:83]\n\t"
"ds_read_b128 a[180:183], v222 offset:21760\n\t"
"v_fma_f32 v184, -v184, s66, v86\n\t"
"v_mfma_f32_16x16x32_bf16 v[80:83], a[252:255], a[188:191], v[80:83]\n\t"
"ds_read_b64_tr_b16 v[236:237], v246 offset:40448\n\t"
"v_fma_f32 v86, v88, s52, v186\n\t"
"v_mfma_f32_16x16x32_bf16 v[80:83], a[248:251], a[184:187], v[80:83]\n\t"
"ds_read_b64_tr_b16 v[232:233], v255 offset:40448\n\t"
"v_fma_f32 v185, -v185, s66, v87\n\t"
"v_fma_f32 v87, v89, s52, v187\n\t"
"v_fma_f32 v186, -v186, s66, v88\n\t"
"v_fma_f32 v187, -v187, s66, v89\n\t"
"v_fma_f32 v88, v90, s52, v188\n\t"
"v_fma_f32 v89, v91, s52, v189\n\t"
"v_mfma_f32_16x16x32_bf16 v[84:87], a[248:251], a[188:191], v[84:87]\n\t"
"ds_read_b128 a[184:187], v223 offset:27904\n\t"
"v_fma_f32 v188, -v188, s66, v90\n\t"
"v_mfma_f32_16x16x32_bf16 v[84:87], a[252:255], a[192:195], v[84:87]\n\t"
"ds_read_b128 a[188:191], v223 offset:34048\n\t"
"v_fma_f32 v90, v92, s52, v190\n\t"
"v_mfma_f32_16x16x32_bf16 v[84:87], v[240:243], a[196:199], v[84:87]\n\t"
"v_fma_f32 v189, -v189, s66, v91\n\t"
"v_fma_f32 v91, v93, s52, v191\n\t"
"v_fma_f32 v190, -v190, s66, v92\n\t"
"v_fma_f32 v191, -v191, s66, v93\n\t"
"v_fma_f32 v92, v94, s52, v192\n\t"
"v_fma_f32 v93, v95, s52, v193\n\t"
"v_mfma_f32_16x16x32_bf16 v[88:91], v[240:243], a[200:203], v[88:91]\n\t"
"v_fma_f32 v192, -v192, s66, v94\n\t"
"v_fma_f32 v94, v96, s52, v194\n\t"
"v_mfma_f32_16x16x32_bf16 v[88:91], a[252:255], a[196:199], v[88:91]\n\t"
"v_fma_f32 v193, -v193, s66, v95\n\t"
"v_fma_f32 v95, v97, s52, v195\n\t"
"v_mfma_f32_16x16x32_bf16 v[88:91], a[248:251], a[192:195], v[88:91]\n\t"
"ds_read_b128 a[192:195], v222 offset:41728\n\t"
"v_fma_f32 v194, -v194, s66, v96\n\t"
"v_fma_f32 v195, -v195, s66, v97\n\t"
"v_mfma_f32_16x16x32_bf16 v[92:95], a[248:251], a[196:199], v[92:95]\n\t"
"s_waitcnt lgkmcnt(7)\n\t"
"ds_read_b128 a[196:199], v223 offset:47872\n\t"
"v_mfma_f32_16x16x32_bf16 v[92:95], a[252:255], a[200:203], v[92:95]\n\t"
"buffer_load_dwordx4 v[96:99], off, s[80:83], s20 offset:2048 sc1\n\t"
"v_mfma_f32_16x16x32_bf16 v[92:95], v[240:243], a[204:207], v[92:95]\n\t"
"s_waitcnt lgkmcnt(5)\n\t"
"ds_read_b128 a[200:203], v223 offset:54016\n\t"
"s_mov_b32 s20, 344064\n\t"
"v_mfma_f32_4x4x4_16b_bf16 v[228:231], v[228:229], v[220:221], 0\n\t"
"s_waitcnt lgkmcnt(5)\n\t"
"v_mfma_f32_4x4x4_16b_bf16 v[224:227], v[224:225], v[220:221], 0\n\t"
"buffer_load_dwordx4 a[248:251], off, s[80:83], s20 offset:0 sc1\n\t"
"v_mfma_f32_4x4x4_16b_bf16 v[236:239], v[236:237], v[220:221], 0\n\t"
"s_waitcnt vmcnt(10)\n\t"
"v_mfma_f32_4x4x4_16b_bf16 v[232:235], v[232:233], v[220:221], 0\n\t"
"v_mfma_f32_16x16x32_bf16 v[100:103], a[208:211], a[168:171], v[100:103]\n\t"
"buffer_load_dwordx4 v[240:243], off, s[80:83], s20 offset:1024 sc1\n\t"
"v_mfma_f32_16x16x32_bf16 v[100:103], a[212:215], a[176:179], v[100:103]\n\t"
"ds_read_b128 a[204:207], v222 offset:61696\n\t"
"v_cvt_pk_bf16_f32 v228, v228, v229\n\t"
"v_mfma_f32_16x16x32_bf16 v[100:103], v[248:251], a[172:175], v[100:103]\n\t"
"s_add_u32 m0, s24, 896\n\t"
"s_add_u32 s28, s21, 0\n\t"
"v_cvt_pk_bf16_f32 v229, v230, v231\n\t"
"v_mfma_f32_16x16x32_bf16 v[104:107], a[208:211], a[172:175], v[104:107]\n\t"
"s_waitcnt lgkmcnt(5)\n\t"
"v_cvt_pk_bf16_f32 v230, v224, v225\n\t"
"v_cvt_pk_bf16_f32 v231, v226, v227\n\t"
"v_mfma_f32_16x16x32_bf16 v[104:107], v[248:251], a[176:179], v[104:107]\n\t"
"v_cvt_pk_bf16_f32 v236, v236, v237\n\t"
"v_mfma_f32_16x16x32_bf16 v[104:107], a[212:215], a[180:183], v[104:107]\n\t"
"s_waitcnt lgkmcnt(0)\n\t"
"s_barrier\n\t"
"v_mfma_f32_16x16x32_bf16 v[108:111], a[212:215], a[184:187], v[108:111]\n\t"
"v_cvt_pk_bf16_f32 v237, v238, v239\n\t"
"v_mfma_f32_16x16x32_bf16 v[108:111], v[248:251], a[180:183], v[108:111]\n\t"
"buffer_load_dwordx4 a[252:255], off, s[80:83], s20 offset:2048 sc1\n\t"
"v_mfma_f32_16x16x32_bf16 v[108:111], a[208:211], a[176:179], v[108:111]\n\t"
"ds_write_b128 v245, v[228:231] offset:32768\n\t"
"v_cvt_pk_bf16_f32 v238, v232, v233\n\t"
"v_cvt_pk_bf16_f32 v239, v234, v235\n\t"
"v_mfma_f32_16x16x32_bf16 v[112:115], a[208:211], a[180:183], v[112:115]\n\t"
"v_mfma_f32_16x16x32_bf16 v[112:115], v[248:251], a[184:187], v[112:115]\n\t"
"buffer_load_dwordx4 v247, s[12:15], s28 offen offset:128 nt lds\n\t"
"v_mfma_f32_16x16x32_bf16 v[112:115], a[212:215], a[188:191], v[112:115]\n\t"
"s_add_u32 m0, s24, 7040\n\t"
"s_add_u32 s28, s21, 196608\n\t"
"ds_write_b128 v245, v[236:239] offset:40448\n\t"
"v_mfma_f32_16x16x32_bf16 v[116:119], a[212:215], a[192:195], v[116:119]\n\t"
"buffer_load_dwordx4 v247, s[12:15], s28 offen offset:128 nt lds\n\t"
"v_mfma_f32_16x16x32_bf16 v[116:119], v[248:251], a[188:191], v[116:119]\n\t"
"s_add_u32 m0, s24, 13184\n\t"
"s_add_u32 s28, s21, 393216\n\t"
"ds_read_b64_tr_b16 v[224:225], v246 offset:46592\n\t"
"v_mfma_f32_16x16x32_bf16 v[116:119], a[208:211], a[184:187], v[116:119]\n\t"
"buffer_load_dwordx4 v247, s[12:15], s28 offen offset:128 nt lds\n\t"
"s_add_u32 m0, s24, 20864\n\t"
"s_add_u32 s28, s21, 589824\n\t"
"v_mfma_f32_16x16x32_bf16 v[120:123], a[208:211], a[188:191], v[120:123]\n\t"
"buffer_load_dwordx4 v247, s[12:15], s28 offen offset:128 nt lds\n\t"
"v_mfma_f32_16x16x32_bf16 v[120:123], v[248:251], a[192:195], v[120:123]\n\t"
"s_add_u32 m0, s24, 27008\n\t"
"s_add_u32 s28, s21, 786432\n\t"
"ds_read_b64_tr_b16 v[232:233], v255 offset:46592\n\t"
"v_mfma_f32_16x16x32_bf16 v[120:123], a[212:215], a[196:199], v[120:123]\n\t"
"buffer_load_dwordx4 v247, s[12:15], s28 offen offset:128 nt lds\n\t"
"s_add_u32 m0, s24, 33152\n\t"
"s_add_u32 s28, s21, 983040\n\t"
"v_mfma_f32_16x16x32_bf16 v[124:127], a[212:215], a[200:203], v[124:127]\n\t"
"buffer_load_dwordx4 v247, s[12:15], s28 offen offset:128 nt lds\n\t"
"v_mfma_f32_16x16x32_bf16 v[124:127], v[248:251], a[196:199], v[124:127]\n\t"
"ds_read_b64_tr_b16 v[228:229], v246 offset:52736\n\t"
"v_mfma_f32_16x16x32_bf16 v[124:127], a[208:211], a[192:195], v[124:127]\n\t"
"ds_read_b64_tr_b16 v[236:237], v255 offset:52736\n\t"
"v_mfma_f32_16x16x32_bf16 v[128:131], a[208:211], a[196:199], v[128:131]\n\t"
"s_waitcnt lgkmcnt(3)\n\t"
"v_mfma_f32_16x16x32_bf16 v[128:131], v[248:251], a[200:203], v[128:131]\n\t"
"s_waitcnt lgkmcnt(2)\n\t"
"v_mfma_f32_16x16x32_bf16 v[128:131], a[212:215], a[204:207], v[128:131]\n\t"
"v_mfma_f32_4x4x4_16b_bf16 v[224:227], v[224:225], v[220:221], 0\n\t"
"s_waitcnt lgkmcnt(1)\n\t"
"v_mfma_f32_4x4x4_16b_bf16 v[232:235], v[232:233], v[220:221], 0\n\t"
"s_waitcnt vmcnt(15) lgkmcnt(0)\n\t"
"v_mfma_f32_4x4x4_16b_bf16 v[228:231], v[228:229], v[220:221], 0\n\t"
"v_mfma_f32_4x4x4_16b_bf16 v[236:239], v[236:237], v[220:221], 0\n\t"
"v_mfma_f32_16x16x32_bf16 v[132:135], a[216:219], a[168:171], v[132:135]\n\t"
"v_cvt_pk_bf16_f32 v224, v224, v225\n\t"
"v_cvt_pk_bf16_f32 v225, v226, v227\n\t"
"v_mfma_f32_16x16x32_bf16 v[132:135], a[224:227], a[176:179], v[132:135]\n\t"
"v_cvt_pk_bf16_f32 v226, v232, v233\n\t"
"v_cvt_pk_bf16_f32 v227, v234, v235\n\t"
"v_mfma_f32_16x16x32_bf16 v[132:135], a[220:223], a[172:175], v[132:135]\n\t"
"ds_write_b128 v245, v[224:227] offset:46592\n\t"
"v_mfma_f32_16x16x32_bf16 v[136:139], a[216:219], a[172:175], v[136:139]\n\t"
"v_cvt_pk_bf16_f32 v228, v228, v229\n\t"
"v_cvt_pk_bf16_f32 v229, v230, v231\n\t"
"v_mfma_f32_16x16x32_bf16 v[136:139], a[220:223], a[176:179], v[136:139]\n\t"
"v_cvt_pk_bf16_f32 v230, v236, v237\n\t"
"v_cvt_pk_bf16_f32 v231, v238, v239\n\t"
"v_mfma_f32_16x16x32_bf16 v[136:139], a[224:227], a[180:183], v[136:139]\n\t"
"ds_write_b128 v245, v[228:231] offset:52736\n\t"
"v_mfma_f32_16x16x32_bf16 v[140:143], a[224:227], a[184:187], v[140:143]\n\t"
"s_mov_b32 s20, 356352\n\t"
"v_mfma_f32_16x16x32_bf16 v[140:143], a[220:223], a[180:183], v[140:143]\n\t"
"v_mfma_f32_16x16x32_bf16 v[140:143], a[216:219], a[176:179], v[140:143]\n\t"
"v_mfma_f32_16x16x32_bf16 v[144:147], a[216:219], a[180:183], v[144:147]\n\t"
"v_mfma_f32_16x16x32_bf16 v[144:147], a[220:223], a[184:187], v[144:147]\n\t"
"v_mfma_f32_16x16x32_bf16 v[144:147], a[224:227], a[188:191], v[144:147]\n\t"
"v_mfma_f32_16x16x32_bf16 v[148:151], a[224:227], a[192:195], v[148:151]\n\t"
"buffer_load_dwordx4 a[208:211], off, s[80:83], s20 offset:0 sc1\n\t"
"v_mfma_f32_16x16x32_bf16 v[148:151], a[220:223], a[188:191], v[148:151]\n\t"
"v_mfma_f32_16x16x32_bf16 v[148:151], a[216:219], a[184:187], v[148:151]\n\t"
"v_mfma_f32_16x16x32_bf16 v[152:155], a[216:219], a[188:191], v[152:155]\n\t"
"v_mfma_f32_16x16x32_bf16 v[152:155], a[220:223], a[192:195], v[152:155]\n\t"
"v_mfma_f32_16x16x32_bf16 v[152:155], a[224:227], a[196:199], v[152:155]\n\t"
"v_mfma_f32_16x16x32_bf16 v[156:159], a[224:227], a[200:203], v[156:159]\n\t"
"v_mfma_f32_16x16x32_bf16 v[156:159], a[220:223], a[196:199], v[156:159]\n\t"
"v_mfma_f32_16x16x32_bf16 v[156:159], a[216:219], a[192:195], v[156:159]\n\t"
"v_mfma_f32_16x16x32_bf16 v[160:163], a[216:219], a[196:199], v[160:163]\n\t"
"s_waitcnt vmcnt(13)\n\t"
"v_mfma_f32_16x16x32_bf16 v[160:163], a[220:223], a[200:203], v[160:163]\n\t"
"v_mfma_f32_16x16x32_bf16 v[160:163], a[224:227], a[204:207], v[160:163]\n\t"
"v_mfma_f32_16x16x32_bf16 v[164:167], a[228:231], a[168:171], v[164:167]\n\t"
"v_mfma_f32_16x16x32_bf16 v[164:167], a[236:239], a[176:179], v[164:167]\n\t"
"v_mfma_f32_16x16x32_bf16 v[164:167], a[232:235], a[172:175], v[164:167]\n\t"
"s_waitcnt lgkmcnt(0)\n\t"
"s_barrier\n\t"
"v_mfma_f32_16x16x32_bf16 v[168:171], a[228:231], a[172:175], v[168:171]\n\t"
"ds_read_b128 a[168:171], v222 offset:2048\n\t"
"v_mfma_f32_16x16x32_bf16 v[168:171], a[232:235], a[176:179], v[168:171]\n\t"
"ds_read_b128 a[172:175], v223 offset:8192\n\t"
"v_mfma_f32_16x16x32_bf16 v[168:171], a[236:239], a[180:183], v[168:171]\n\t"
"v_mfma_f32_16x16x32_bf16 v[172:175], a[236:239], a[184:187], v[172:175]\n\t"
"v_mfma_f32_16x16x32_bf16 v[172:175], a[232:235], a[180:183], v[172:175]\n\t"
"v_mfma_f32_16x16x32_bf16 v[172:175], a[228:231], a[176:179], v[172:175]\n\t"
"ds_read_b128 a[176:179], v223 offset:14336\n\t"
"v_mfma_f32_16x16x32_bf16 v[176:179], a[228:231], a[180:183], v[176:179]\n\t"
"ds_read_b128 a[180:183], v222 offset:22016\n\t"
"v_mfma_f32_16x16x32_bf16 v[176:179], a[232:235], a[184:187], v[176:179]\n\t"
"v_mfma_f32_16x16x32_bf16 v[176:179], a[236:239], a[188:191], v[176:179]\n\t"
"v_mfma_f32_16x16x32_bf16 v[180:183], a[236:239], a[192:195], v[180:183]\n\t"
"v_mfma_f32_16x16x32_bf16 v[180:183], a[232:235], a[188:191], v[180:183]\n\t"
"v_mfma_f32_16x16x32_bf16 v[180:183], a[228:231], a[184:187], v[180:183]\n\t"
"ds_read_b128 a[184:187], v223 offset:28160\n\t"
"v_mfma_f32_16x16x32_bf16 v[184:187], a[228:231], a[188:191], v[184:187]\n\t"
"ds_read_b128 a[188:191], v223 offset:34304\n\t"
"v_mfma_f32_16x16x32_bf16 v[184:187], a[232:235], a[192:195], v[184:187]\n\t"
"v_mfma_f32_16x16x32_bf16 v[184:187], a[236:239], a[196:199], v[184:187]\n\t"
"v_mfma_f32_16x16x32_bf16 v[188:191], a[236:239], a[200:203], v[188:191]\n\t"
"v_mfma_f32_16x16x32_bf16 v[188:191], a[232:235], a[196:199], v[188:191]\n\t"
"v_mfma_f32_16x16x32_bf16 v[188:191], a[228:231], a[192:195], v[188:191]\n\t"
"ds_read_b128 a[192:195], v222 offset:41984\n\t"
"v_mfma_f32_16x16x32_bf16 v[192:195], a[228:231], a[196:199], v[192:195]\n\t"
"buffer_load_dwordx4 v[236:239], off, s[80:83], s20 offset:1024 sc1\n\t"
"v_mfma_f32_16x16x32_bf16 v[192:195], a[232:235], a[200:203], v[192:195]\n\t"
"s_waitcnt vmcnt(11) lgkmcnt(4)\n\t"
"ds_read_b128 a[196:199], v223 offset:48128\n\t"
"v_mfma_f32_16x16x32_bf16 v[192:195], a[236:239], a[204:207], v[192:195]\n\t"
"ds_read_b128 a[200:203], v223 offset:54272\n\t"
"Lst4_%=:\n\t"
"v_mfma_f32_16x16x32_bf16 v[0:3], a[240:243], a[168:171], v[0:3]\n\t"
"buffer_load_dwordx4 v[248:251], off, s[80:83], s20 offset:2048 sc1\n\t"
"v_mfma_f32_16x16x32_bf16 v[0:3], v[96:99], a[176:179], v[0:3]\n\t"
"s_waitcnt lgkmcnt(5)\n\t"
"ds_read_b128 a[204:207], v222 offset:61952\n\t"
"s_mov_b32 s20, 368640\n\t"
"v_mfma_f32_16x16x32_bf16 v[0:3], a[244:247], a[172:175], v[0:3]\n\t"
"v_mfma_f32_16x16x32_bf16 v[4:7], a[240:243], a[172:175], v[4:7]\n\t"
"s_waitcnt lgkmcnt(5)\n\t"
"v_mfma_f32_16x16x32_bf16 v[4:7], a[244:247], a[176:179], v[4:7]\n\t"
"buffer_load_dwordx4 a[212:215], off, s[80:83], s20 offset:0 sc1\n\t"
"v_mfma_f32_16x16x32_bf16 v[4:7], v[96:99], a[180:183], v[4:7]\n\t"
"v_mfma_f32_16x16x32_bf16 v[8:11], v[96:99], a[184:187], v[8:11]\n\t"
"s_waitcnt lgkmcnt(4)\n\t"
"v_mfma_f32_16x16x32_bf16 v[8:11], a[244:247], a[180:183], v[8:11]\n\t"
"v_mfma_f32_16x16x32_bf16 v[8:11], a[240:243], a[176:179], v[8:11]\n\t"
"buffer_load_dwordx4 a[216:219], off, s[80:83], s20 offset:1024 sc1\n\t"
"v_mfma_f32_16x16x32_bf16 v[12:15], a[240:243], a[180:183], v[12:15]\n\t"
"s_waitcnt lgkmcnt(3)\n\t"
"v_mfma_f32_16x16x32_bf16 v[12:15], a[244:247], a[184:187], v[12:15]\n\t"
"v_mfma_f32_16x16x32_bf16 v[12:15], v[96:99], a[188:191], v[12:15]\n\t"
"v_mfma_f32_16x16x32_bf16 v[16:19], v[96:99], a[192:195], v[16:19]\n\t"
"buffer_load_dwordx4 a[220:223], off, s[80:83], s20 offset:2048 sc1\n\t"
"v_mfma_f32_16x16x32_bf16 v[16:19], a[244:247], a[188:191], v[16:19]\n\t"
"s_waitcnt lgkmcnt(2)\n\t"
"s_mov_b32 s20, 380928\n\t"
"v_mfma_f32_16x16x32_bf16 v[16:19], a[240:243], a[184:187], v[16:19]\n\t"
"v_mfma_f32_16x16x32_bf16 v[20:23], a[240:243], a[188:191], v[20:23]\n\t"
"s_waitcnt lgkmcnt(1)\n\t"
"v_mfma_f32_16x16x32_bf16 v[20:23], a[244:247], a[192:195], v[20:23]\n\t"
"buffer_load_dwordx4 a[224:227], off, s[80:83], s20 offset:0 sc1\n\t"
"v_mfma_f32_16x16x32_bf16 v[20:23], v[96:99], a[196:199], v[20:23]\n\t"
"s_waitcnt vmcnt(13) lgkmcnt(0)\n\t"
"v_mfma_f32_16x16x32_bf16 v[24:27], v[96:99], a[200:203], v[24:27]\n\t"
"v_mfma_f32_16x16x32_bf16 v[24:27], a[244:247], a[196:199], v[24:27]\n\t"
"v_mfma_f32_16x16x32_bf16 v[24:27], a[240:243], a[192:195], v[24:27]\n\t"
"Lpg4_%=:\n\t"
"s_cbranch_vccnz Lpg8_%=\n\t"
"v_mfma_f32_16x16x32_bf16 v[28:31], a[240:243], a[196:199], v[28:31]\n\t"
"buffer_load_dwordx4 a[228:231], off, s[80:83], s20 offset:1024 sc1\n\t"
"v_mfma_f32_16x16x32_bf16 v[28:31], a[244:247], a[200:203], v[28:31]\n\t"
"v_mfma_f32_16x16x32_bf16 v[28:31], v[96:99], a[204:207], v[28:31]\n\t"
"v_mfma_f32_16x16x32_bf16 v[32:35], a[248:251], a[168:171], v[32:35]\n\t"
"buffer_load_dwordx4 v[96:99], off, s[80:83], s20 offset:2048 sc1\n\t"
"v_mfma_f32_16x16x32_bf16 v[32:35], a[252:255], a[176:179], v[32:35]\n\t"
"s_mov_b32 s20, 393216\n\t"
"v_mfma_f32_16x16x32_bf16 v[32:35], v[240:243], a[172:175], v[32:35]\n\t"
"v_mfma_f32_16x16x32_bf16 v[36:39], a[248:251], a[172:175], v[36:39]\n\t"
"v_mfma_f32_16x16x32_bf16 v[36:39], v[240:243], a[176:179], v[36:39]\n\t"
"buffer_load_dwordx4 a[232:235], off, s[80:83], s20 offset:0 sc1\n\t"
"v_mfma_f32_16x16x32_bf16 v[36:39], a[252:255], a[180:183], v[36:39]\n\t"
"v_mfma_f32_16x16x32_bf16 v[40:43], a[252:255], a[184:187], v[40:43]\n\t"
"v_mfma_f32_16x16x32_bf16 v[40:43], v[240:243], a[180:183], v[40:43]\n\t"
"v_mfma_f32_16x16x32_bf16 v[40:43], a[248:251], a[176:179], v[40:43]\n\t"
"buffer_load_dwordx4 a[236:239], off, s[80:83], s20 offset:1024 sc1\n\t"
"v_mfma_f32_16x16x32_bf16 v[44:47], a[248:251], a[180:183], v[44:47]\n\t"
"v_mfma_f32_16x16x32_bf16 v[44:47], v[240:243], a[184:187], v[44:47]\n\t"
"v_mfma_f32_16x16x32_bf16 v[44:47], a[252:255], a[188:191], v[44:47]\n\t"
"v_mfma_f32_16x16x32_bf16 v[48:51], a[252:255], a[192:195], v[48:51]\n\t"
"buffer_load_dwordx4 a[240:243], off, s[80:83], s20 offset:2048 sc1\n\t"
"v_mfma_f32_16x16x32_bf16 v[48:51], v[240:243], a[188:191], v[48:51]\n\t"
"s_mov_b32 s20, 405504\n\t"
"v_mfma_f32_16x16x32_bf16 v[48:51], a[248:251], a[184:187], v[48:51]\n\t"
"v_mfma_f32_16x16x32_bf16 v[52:55], a[248:251], a[188:191], v[52:55]\n\t"
"v_mfma_f32_16x16x32_bf16 v[52:55], v[240:243], a[192:195], v[52:55]\n\t"
"buffer_load_dwordx4 a[244:247], off, s[80:83], s20 offset:0 sc1\n\t"
"v_mfma_f32_16x16x32_bf16 v[52:55], a[252:255], a[196:199], v[52:55]\n\t"
"v_mfma_f32_16x16x32_bf16 v[56:59], a[252:255], a[200:203], v[56:59]\n\t"
"v_mfma_f32_16x16x32_bf16 v[56:59], v[240:243], a[196:199], v[56:59]\n\t"
"v_mfma_f32_16x16x32_bf16 v[56:59], a[248:251], a[192:195], v[56:59]\n\t"
"v_mfma_f32_16x16x32_bf16 v[60:63], a[248:251], a[196:199], v[60:63]\n\t"
"buffer_load_dwordx4 a[248:251], off, s[80:83], s20 offset:1024 sc1\n\t"
"v_mfma_f32_16x16x32_bf16 v[60:63], v[240:243], a[200:203], v[60:63]\n\t"
"s_waitcnt vmcnt(11)\n\t"
"v_mfma_f32_16x16x32_bf16 v[60:63], a[252:255], a[204:207], v[60:63]\n\t"
"v_mfma_f32_16x16x32_bf16 v[64:67], a[208:211], a[168:171], v[64:67]\n\t"
"buffer_load_dwordx4 a[252:255], off, s[80:83], s20 offset:2048 sc1\n\t"
"v_mfma_f32_16x16x32_bf16 v[64:67], v[248:251], a[176:179], v[64:67]\n\t"
"ds_read_b128 a[168:171], v222 offset:2304\n\t"
"v_mfma_f32_16x16x32_bf16 v[64:67], v[236:239], a[172:175], v[64:67]\n\t"
"v_mfma_f32_16x16x32_bf16 v[68:71], a[208:211], a[172:175], v[68:71]\n\t"
"ds_read_b128 a[172:175], v223 offset:8448\n\t"
"v_mfma_f32_16x16x32_bf16 v[68:71], v[236:239], a[176:179], v[68:71]\n\t"
"v_mfma_f32_16x16x32_bf16 v[68:71], v[248:251], a[180:183], v[68:71]\n\t"
"v_mfma_f32_16x16x32_bf16 v[72:75], v[248:251], a[184:187], v[72:75]\n\t"
"v_mfma_f32_16x16x32_bf16 v[72:75], v[236:239], a[180:183], v[72:75]\n\t"
"v_mfma_f32_16x16x32_bf16 v[72:75], a[208:211], a[176:179], v[72:75]\n\t"
"ds_read_b128 a[176:179], v223 offset:14592\n\t"
"v_mfma_f32_16x16x32_bf16 v[76:79], a[208:211], a[180:183], v[76:79]\n\t"
"ds_read_b128 a[180:183], v222 offset:22272\n\t"
"v_mfma_f32_16x16x32_bf16 v[76:79], v[236:239], a[184:187], v[76:79]\n\t"
"v_mfma_f32_16x16x32_bf16 v[76:79], v[248:251], a[188:191], v[76:79]\n\t"
"v_mfma_f32_16x16x32_bf16 v[80:83], v[248:251], a[192:195], v[80:83]\n\t"
"v_mfma_f32_16x16x32_bf16 v[80:83], v[236:239], a[188:191], v[80:83]\n\t"
"v_mfma_f32_16x16x32_bf16 v[80:83], a[208:211], a[184:187], v[80:83]\n\t"
"ds_read_b128 a[184:187], v223 offset:28416\n\t"
"v_mfma_f32_16x16x32_bf16 v[84:87], a[208:211], a[188:191], v[84:87]\n\t"
"ds_read_b128 a[188:191], v223 offset:34560\n\t"
"v_mfma_f32_16x16x32_bf16 v[84:87], v[236:239], a[192:195], v[84:87]\n\t"
"v_mfma_f32_16x16x32_bf16 v[84:87], v[248:251], a[196:199], v[84:87]\n\t"
"s_mov_b32 s20, 417792\n\t"
"v_mfma_f32_16x16x32_bf16 v[88:91], v[248:251], a[200:203], v[88:91]\n\t"
"v_mfma_f32_16x16x32_bf16 v[88:91], v[236:239], a[196:199], v[88:91]\n\t"
"v_mfma_f32_16x16x32_bf16 v[88:91], a[208:211], a[192:195], v[88:91]\n\t"
"ds_read_b128 a[192:195], v222 offset:42240\n\t"
"v_mfma_f32_16x16x32_bf16 v[92:95], a[208:211], a[196:199], v[92:95]\n\t"
"s_waitcnt vmcnt(9) lgkmcnt(4)\n\t"
"ds_read_b128 a[196:199], v223 offset:48384\n\t"
"v_mfma_f32_16x16x32_bf16 v[92:95], v[236:239], a[200:203], v[92:95]\n\t"
"global_load_dwordx4 v[240:243], v252, s[48:49] offset:2048\n\t"
"v_mfma_f32_16x16x32_bf16 v[92:95], v[248:251], a[204:207], v[92:95]\n\t"
"buffer_load_dwordx4 v[248:251], off, s[80:83], s20 offset:0 sc1\n\t"
"v_mfma_f32_16x16x32_bf16 v[100:103], a[212:215], a[168:171], v[100:103]\n\t"
"s_waitcnt lgkmcnt(4)\n\t"
"ds_read_b128 a[200:203], v223 offset:54528\n\t"
"v_mfma_f32_16x16x32_bf16 v[100:103], a[220:223], a[176:179], v[100:103]\n\t"
"ds_read_b128 a[204:207], v222 offset:62208\n\t"
"v_mfma_f32_16x16x32_bf16 v[100:103], a[216:219], a[172:175], v[100:103]\n\t"
"v_mfma_f32_16x16x32_bf16 v[104:107], a[212:215], a[172:175], v[104:107]\n\t"
"s_waitcnt lgkmcnt(5)\n\t"
"v_mfma_f32_16x16x32_bf16 v[104:107], a[216:219], a[176:179], v[104:107]\n\t"
"global_load_dwordx4 a[208:211], v252, s[46:47] offset:1024\n\t"
"v_mfma_f32_16x16x32_bf16 v[104:107], a[220:223], a[180:183], v[104:107]\n\t"
"v_mfma_f32_16x16x32_bf16 v[108:111], a[220:223], a[184:187], v[108:111]\n\t"
"s_waitcnt lgkmcnt(4)\n\t"
"v_mfma_f32_16x16x32_bf16 v[108:111], a[216:219], a[180:183], v[108:111]\n\t"
"v_mfma_f32_16x16x32_bf16 v[108:111], a[212:215], a[176:179], v[108:111]\n\t"
"v_mfma_f32_16x16x32_bf16 v[112:115], a[212:215], a[180:183], v[112:115]\n\t"
"s_waitcnt lgkmcnt(3)\n\t"
"v_mfma_f32_16x16x32_bf16 v[112:115], a[216:219], a[184:187], v[112:115]\n\t"
"v_mfma_f32_16x16x32_bf16 v[112:115], a[220:223], a[188:191], v[112:115]\n\t"
"v_mfma_f32_16x16x32_bf16 v[116:119], a[220:223], a[192:195], v[116:119]\n\t"
"s_waitcnt lgkmcnt(2)\n\t"
"v_mfma_f32_16x16x32_bf16 v[116:119], a[216:219], a[188:191], v[116:119]\n\t"
"v_mfma_f32_16x16x32_bf16 v[116:119], a[212:215], a[184:187], v[116:119]\n\t"
"v_mfma_f32_16x16x32_bf16 v[120:123], a[212:215], a[188:191], v[120:123]\n\t"
"s_waitcnt lgkmcnt(1)\n\t"
"v_mfma_f32_16x16x32_bf16 v[120:123], a[216:219], a[192:195], v[120:123]\n\t"
"v_mfma_f32_16x16x32_bf16 v[120:123], a[220:223], a[196:199], v[120:123]\n\t"
"v_mfma_f32_16x16x32_bf16 v[124:127], a[220:223], a[200:203], v[124:127]\n\t"
"s_waitcnt lgkmcnt(0)\n\t"
"v_mfma_f32_16x16x32_bf16 v[124:127], a[216:219], a[196:199], v[124:127]\n\t"
"s_add_u32 m0, s24, 42368\n\t"
"s_add_u32 s28, s21, 1182720\n\t"
"v_mfma_f32_16x16x32_bf16 v[124:127], a[212:215], a[192:195], v[124:127]\n\t"
"v_mfma_f32_16x16x32_bf16 v[128:131], a[212:215], a[196:199], v[128:131]\n\t"
"buffer_load_dwordx4 a[212:215], off, s[80:83], s20 offset:1024 sc1\n\t"
"v_mfma_f32_16x16x32_bf16 v[128:131], a[216:219], a[200:203], v[128:131]\n\t"
"s_waitcnt vmcnt(10)\n\t"
"v_mfma_f32_16x16x32_bf16 v[128:131], a[220:223], a[204:207], v[128:131]\n\t"
"buffer_load_dwordx4 v247, s[12:15], s28 offen offset:128 nt lds\n\t"
"v_mfma_f32_16x16x32_bf16 v[132:135], a[224:227], a[168:171], v[132:135]\n\t"
"s_add_u32 m0, s24, 48512\n\t"
"s_add_u32 s28, s21, 1379328\n\t"
"v_mfma_f32_16x16x32_bf16 v[132:135], v[96:99], a[176:179], v[132:135]\n\t"
"buffer_load_dwordx4 v247, s[12:15], s28 offen offset:128 nt lds\n\t"
"v_mfma_f32_16x16x32_bf16 v[132:135], a[228:231], a[172:175], v[132:135]\n\t"
"s_add_u32 m0, s24, 54656\n\t"
"s_add_u32 s28, s21, 1575936\n\t"
"v_mfma_f32_16x16x32_bf16 v[136:139], a[224:227], a[172:175], v[136:139]\n\t"
"buffer_load_dwordx4 v247, s[12:15], s28 offen offset:128 nt lds\n\t"
"v_mfma_f32_16x16x32_bf16 v[136:139], a[228:231], a[176:179], v[136:139]\n\t"
"s_add_u32 m0, s24, 40832\n\t"
"s_add_u32 s28, s21, 1179648\n\t"
"v_mfma_f32_16x16x32_bf16 v[136:139], v[96:99], a[180:183], v[136:139]\n\t"
"s_barrier\n\t"
"v_mfma_f32_16x16x32_bf16 v[140:143], v[96:99], a[184:187], v[140:143]\n\t"
"v_mfma_f32_16x16x32_bf16 v[140:143], a[228:231], a[180:183], v[140:143]\n\t"
"buffer_load_dwordx4 v247, s[12:15], s28 offen offset:128 nt lds\n\t"
"v_mfma_f32_16x16x32_bf16 v[140:143], a[224:227], a[176:179], v[140:143]\n\t"
"s_add_u32 m0, s24, 46976\n\t"
"s_add_u32 s28, s21, 1376256\n\t"
"s_waitcnt vmcnt(7)\n\t"
"ds_read_b64_tr_b16 v[232:233], v240 offset:1024\n\t"
"v_mfma_f32_16x16x32_bf16 v[144:147], a[224:227], a[180:183], v[144:147]\n\t"
"buffer_load_dwordx4 v247, s[12:15], s28 offen offset:128 nt lds\n\t"
"v_mfma_f32_16x16x32_bf16 v[144:147], a[228:231], a[184:187], v[144:147]\n\t"
"s_add_u32 m0, s24, 53120\n\t"
"s_add_u32 s28, s21, 1572864\n\t"
"ds_read_b64_tr_b16 v[234:235], v240 offset:2560\n\t"
"v_mfma_f32_16x16x32_bf16 v[144:147], v[96:99], a[188:191], v[144:147]\n\t"
"buffer_load_dwordx4 v247, s[12:15], s28 offen offset:128 nt lds\n\t"
"v_mfma_f32_16x16x32_bf16 v[148:151], v[96:99], a[192:195], v[148:151]\n\t"
"ds_read_b64_tr_b16 v[236:237], v241 offset:1024\n\t"
"v_mfma_f32_16x16x32_bf16 v[148:151], a[228:231], a[188:191], v[148:151]\n\t"
"ds_read_b64_tr_b16 v[238:239], v241 offset:2560\n\t"
"v_mfma_f32_16x16x32_bf16 v[148:151], a[224:227], a[184:187], v[148:151]\n\t"
"ds_read_b64_tr_b16 v[224:225], v240 offset:20992\n\t"
"v_mfma_f32_16x16x32_bf16 v[152:155], a[224:227], a[188:191], v[152:155]\n\t"
"ds_read_b64_tr_b16 v[226:227], v240 offset:22528\n\t"
"v_mfma_f32_16x16x32_bf16 v[152:155], a[228:231], a[192:195], v[152:155]\n\t"
"ds_read_b64_tr_b16 v[228:229], v241 offset:20992\n\t"
"v_mfma_f32_16x16x32_bf16 v[152:155], v[96:99], a[196:199], v[152:155]\n\t"
"ds_read_b64_tr_b16 v[230:231], v241 offset:22528\n\t"
"v_mfma_f32_16x16x32_bf16 v[156:159], v[96:99], a[200:203], v[156:159]\n\t"
"v_mfma_f32_16x16x32_bf16 v[156:159], a[228:231], a[196:199], v[156:159]\n\t"
"v_mfma_f32_16x16x32_bf16 v[156:159], a[224:227], a[192:195], v[156:159]\n\t"
"v_mfma_f32_16x16x32_bf16 v[160:163], a[224:227], a[196:199], v[160:163]\n\t"
"s_waitcnt vmcnt(7) lgkmcnt(6)\n\t"
"v_mfma_f32_16x16x32_bf16 v[160:163], a[228:231], a[200:203], v[160:163]\n\t"
"s_waitcnt lgkmcnt(4)\n\t"
"v_mfma_f32_16x16x32_bf16 v[160:163], v[96:99], a[204:207], v[160:163]\n\t"
"s_waitcnt lgkmcnt(2)\n\t"
"v_mfma_f32_16x16x32_bf16 v[232:235], v[232:235], a[208:211], 0\n\t"
"s_waitcnt lgkmcnt(0)\n\t"
"v_mfma_f32_16x16x32_bf16 v[236:239], v[236:239], a[208:211], 0\n\t"
"v_mfma_f32_16x16x32_bf16 v[224:227], v[224:227], a[208:211], 0\n\t"
"v_mfma_f32_16x16x32_bf16 v[228:231], v[228:231], a[208:211], 0\n\t"
"v_mfma_f32_16x16x32_bf16 v[164:167], a[232:235], a[168:171], v[164:167]\n\t"
"ds_read_b128 a[168:171], v222 offset:512\n\t"
"v_mfma_f32_16x16x32_bf16 v[164:167], a[240:243], a[176:179], v[164:167]\n\t"
"v_cvt_pk_bf16_f32 v232, v232, v233\n\t"
"v_cvt_pk_bf16_f32 v233, v234, v235\n\t"
"v_mfma_f32_16x16x32_bf16 v[164:167], a[236:239], a[172:175], v[164:167]\n\t"
"v_cvt_pk_bf16_f32 v234, v236, v237\n\t"
"v_cvt_pk_bf16_f32 v235, v238, v239\n\t"
"v_mfma_f32_16x16x32_bf16 v[168:171], a[232:235], a[172:175], v[168:171]\n\t"
"ds_read_b128 a[172:175], v223 offset:6656\n\t"
"v_cvt_pk_bf16_f32 v224, v224, v225\n\t"
"v_mfma_f32_16x16x32_bf16 v[168:171], a[236:239], a[176:179], v[168:171]\n\t"
"v_cvt_pk_bf16_f32 v225, v226, v227\n\t"
"v_cvt_pk_bf16_f32 v226, v228, v229\n\t"
"v_mfma_f32_16x16x32_bf16 v[168:171], a[240:243], a[180:183], v[168:171]\n\t"
"ds_write_b128 v242, v[232:235] offset:24576\n\t"
"v_mfma_f32_16x16x32_bf16 v[172:175], a[240:243], a[184:187], v[172:175]\n\t"
"v_cvt_pk_bf16_f32 v227, v230, v231\n\t"
"v_add_u32 v240, 59904, v240\n\t"
"v_mfma_f32_16x16x32_bf16 v[172:175], a[236:239], a[180:183], v[172:175]\n\t"
"v_add_u32 v241, 59904, v241\n\t"
"v_mfma_f32_16x16x32_bf16 v[172:175], a[232:235], a[176:179], v[172:175]\n\t"
"ds_write_b128 v242, v[224:227] offset:26112\n\t"
"v_mfma_f32_16x16x32_bf16 v[176:179], a[232:235], a[180:183], v[176:179]\n\t"
"buffer_load_dwordx4 v[96:99], off, s[80:83], s20 offset:2048 sc1\n\t"
"v_mfma_f32_16x16x32_bf16 v[176:179], a[236:239], a[184:187], v[176:179]\n\t"
"ds_read_b128 a[176:179], v223 offset:12800\n\t"
"v_mfma_f32_16x16x32_bf16 v[176:179], a[240:243], a[188:191], v[176:179]\n\t"
"ds_read_b64_tr_b16 v[236:237], v240 offset:1024\n\t"
"v_mfma_f32_16x16x32_bf16 v[180:183], a[240:243], a[192:195], v[180:183]\n\t"
"ds_read_b64_tr_b16 v[238:239], v240 offset:2560\n\t"
"v_mfma_f32_16x16x32_bf16 v[180:183], a[236:239], a[188:191], v[180:183]\n\t"
"ds_read_b64_tr_b16 v[228:229], v241 offset:1024\n\t"
"v_mfma_f32_16x16x32_bf16 v[180:183], a[232:235], a[184:187], v[180:183]\n\t"
"ds_read_b64_tr_b16 v[230:231], v241 offset:2560\n\t"
"v_mfma_f32_16x16x32_bf16 v[184:187], a[232:235], a[188:191], v[184:187]\n\t"
"ds_read_b128 a[180:183], v222 offset:20480\n\t"
"v_mfma_f32_16x16x32_bf16 v[184:187], a[236:239], a[192:195], v[184:187]\n\t"
"ds_read_b64_tr_b16 v[232:233], v240 offset:20992\n\t"
"v_mfma_f32_16x16x32_bf16 v[184:187], a[240:243], a[196:199], v[184:187]\n\t"
"ds_read_b64_tr_b16 v[234:235], v240 offset:22528\n\t"
"v_mfma_f32_16x16x32_bf16 v[188:191], a[240:243], a[200:203], v[188:191]\n\t"
"ds_read_b64_tr_b16 v[224:225], v241 offset:20992\n\t"
"v_mfma_f32_16x16x32_bf16 v[188:191], a[236:239], a[196:199], v[188:191]\n\t"
"ds_read_b64_tr_b16 v[226:227], v241 offset:22528\n\t"
"v_mfma_f32_16x16x32_bf16 v[188:191], a[232:235], a[192:195], v[188:191]\n\t"
"ds_read_b128 a[184:187], v223 offset:26624\n\t"
"v_mfma_f32_16x16x32_bf16 v[192:195], a[232:235], a[196:199], v[192:195]\n\t"
"s_waitcnt lgkmcnt(8)\n\t"
"ds_read_b128 a[188:191], v223 offset:32768\n\t"
"v_mfma_f32_16x16x32_bf16 v[192:195], a[236:239], a[200:203], v[192:195]\n\t"
"s_waitcnt lgkmcnt(7)\n\t"
"ds_read_b128 a[192:195], v222 offset:40448\n\t"
"v_mfma_f32_16x16x32_bf16 v[192:195], a[240:243], a[204:207], v[192:195]\n\t"
"s_waitcnt lgkmcnt(5)\n\t"
"ds_read_b128 a[196:199], v223 offset:46592\n\t"
"v_mfma_f32_16x16x32_bf16 v[236:239], v[236:239], a[208:211], 0\n\t"
"s_waitcnt lgkmcnt(4)\n\t"
"ds_read_b128 a[200:203], v223 offset:52736\n\t"
"v_mfma_f32_16x16x32_bf16 v[228:231], v[228:231], a[208:211], 0\n\t"
"ds_read_b128 a[204:207], v222 offset:60416\n\t"
"v_mfma_f32_16x16x32_bf16 v[232:235], v[232:235], a[208:211], 0\n\t"
"v_mfma_f32_16x16x32_bf16 v[224:227], v[224:227], a[208:211], 0\n\t"
"Lst5_%=:\n\t"
"v_mfma_f32_16x16x32_bf16 v[196:199], a[244:247], a[168:171], v[196:199]\n\t"
"v_mfma_f32_16x16x32_bf16 v[196:199], a[252:255], a[176:179], v[196:199]\n\t"
"v_cvt_pk_bf16_f32 v236, v236, v237\n\t"
"v_cvt_pk_bf16_f32 v237, v238, v239\n\t"
"v_mfma_f32_16x16x32_bf16 v[196:199], a[248:251], a[172:175], v[196:199]\n\t"
"v_cvt_pk_bf16_f32 v238, v228, v229\n\t"
"v_cvt_pk_bf16_f32 v239, v230, v231\n\t"
"v_mfma_f32_16x16x32_bf16 v[204:207], a[244:247], a[172:175], v[204:207]\n\t"
"s_waitcnt lgkmcnt(5)\n\t"
"v_cvt_pk_bf16_f32 v232, v232, v233\n\t"
"v_cvt_pk_bf16_f32 v233, v234, v235\n\t"
"v_mfma_f32_16x16x32_bf16 v[204:207], a[248:251], a[176:179], v[204:207]\n\t"
"v_cvt_pk_bf16_f32 v234, v224, v225\n\t"
"v_cvt_pk_bf16_f32 v235, v226, v227\n\t"
"v_mfma_f32_16x16x32_bf16 v[204:207], a[252:255], a[180:183], v[204:207]\n\t"
"ds_write_b128 v242, v[236:239] offset:29184\n\t"
"v_mfma_f32_16x16x32_bf16 v[212:215], a[252:255], a[184:187], v[212:215]\n\t"
"s_waitcnt lgkmcnt(5)\n\t"
"s_mov_b32 s20, 430080\n\t"
"v_mfma_f32_16x16x32_bf16 v[212:215], a[248:251], a[180:183], v[212:215]\n\t"
"v_mfma_f32_16x16x32_bf16 v[212:215], a[244:247], a[176:179], v[212:215]\n\t"
"ds_write_b128 v242, v[232:235] offset:30720\n\t"
"v_mfma_f32_16x16x32_bf16 a[0:3], a[244:247], a[180:183], a[0:3]\n\t"
"s_waitcnt lgkmcnt(5)\n\t"
"v_mfma_f32_16x16x32_bf16 a[0:3], a[248:251], a[184:187], a[0:3]\n\t"
"v_mfma_f32_16x16x32_bf16 a[0:3], a[252:255], a[188:191], a[0:3]\n\t"
"s_barrier\n\t"
"v_mfma_f32_16x16x32_bf16 a[8:11], a[252:255], a[192:195], a[8:11]\n\t"
"s_waitcnt lgkmcnt(4)\n\t"
"ds_read_b64_tr_b16 v[228:229], v246 offset:1024\n\t"
"v_mfma_f32_16x16x32_bf16 a[8:11], a[248:251], a[188:191], a[8:11]\n\t"
"buffer_load_dwordx4 a[216:219], off, s[80:83], s20 offset:0 sc1\n\t"
"v_mfma_f32_16x16x32_bf16 a[8:11], a[244:247], a[184:187], a[8:11]\n\t"
"ds_read_b64_tr_b16 v[224:225], v255 offset:1024\n\t"
"v_mfma_f32_16x16x32_bf16 a[16:19], a[244:247], a[188:191], a[16:19]\n\t"
"s_waitcnt lgkmcnt(5)\n\t"
"ds_read_b64_tr_b16 v[236:237], v246 offset:7168\n\t"
"v_mfma_f32_16x16x32_bf16 a[16:19], a[248:251], a[192:195], a[16:19]\n\t"
"ds_read_b64_tr_b16 v[232:233], v255 offset:7168\n\t"
"v_mfma_f32_16x16x32_bf16 a[16:19], a[252:255], a[196:199], a[16:19]\n\t"
"buffer_load_dwordx4 a[220:223], off, s[80:83], s20 offset:1024 sc1\n\t"
"v_mfma_f32_16x16x32_bf16 a[24:27], a[252:255], a[200:203], a[24:27]\n\t"
"s_waitcnt lgkmcnt(6)\n\t"
"v_mfma_f32_16x16x32_bf16 a[24:27], a[248:251], a[196:199], a[24:27]\n\t"
"buffer_load_dwordx4 a[224:227], off, s[80:83], s20 offset:2048 sc1\n\t"
"v_mfma_f32_16x16x32_bf16 a[24:27], a[244:247], a[192:195], a[24:27]\n\t"
"s_mov_b32 s20, 442368\n\t"
"v_mfma_f32_16x16x32_bf16 a[32:35], a[244:247], a[196:199], a[32:35]\n\t"
"s_waitcnt lgkmcnt(3)\n\t"
"v_mfma_f32_16x16x32_bf16 a[32:35], a[248:251], a[200:203], a[32:35]\n\t"
"s_waitcnt lgkmcnt(2)\n\t"
"v_mfma_f32_16x16x32_bf16 a[32:35], a[252:255], a[204:207], a[32:35]\n\t"
"buffer_load_dwordx4 a[228:231], off, s[80:83], s20 offset:0 sc1\n\t"
"v_mfma_f32_4x4x4_16b_bf16 v[228:231], v[228:229], v[220:221], 0\n\t"
"s_waitcnt lgkmcnt(0)\n\t"
"v_mfma_f32_4x4x4_16b_bf16 v[224:227], v[224:225], v[220:221], 0\n\t"
"s_waitcnt vmcnt(4)\n\t"
"v_mfma_f32_4x4x4_16b_bf16 v[236:239], v[236:237], v[220:221], 0\n\t"
"v_mfma_f32_4x4x4_16b_bf16 v[232:235], v[232:233], v[220:221], 0\n\t"
"v_mfma_f32_16x16x32_bf16 a[40:43], v[248:251], a[168:171], a[40:43]\n\t"
"buffer_load_dwordx4 a[232:235], off, s[80:83], s20 offset:1024 sc1\n\t"
"v_mfma_f32_16x16x32_bf16 a[40:43], v[96:99], a[176:179], a[40:43]\n\t"
"v_cvt_pk_bf16_f32 v228, v228, v229\n\t"
"v_cvt_pk_bf16_f32 v229, v230, v231\n\t"
"v_mfma_f32_16x16x32_bf16 a[40:43], a[212:215], a[172:175], a[40:43]\n\t"
"v_cvt_pk_bf16_f32 v230, v224, v225\n\t"
"v_cvt_pk_bf16_f32 v231, v226, v227\n\t"
"v_mfma_f32_16x16x32_bf16 a[48:51], v[248:251], a[172:175], a[48:51]\n\t"
"ds_read_b64_tr_b16 v[224:225], v246 offset:13312\n\t"
"v_cvt_pk_bf16_f32 v236, v236, v237\n\t"
"v_mfma_f32_16x16x32_bf16 a[48:51], a[212:215], a[176:179], a[48:51]\n\t"
"v_cvt_pk_bf16_f32 v237, v238, v239\n\t"
"v_cvt_pk_bf16_f32 v238, v232, v233\n\t"
"v_mfma_f32_16x16x32_bf16 a[48:51], v[96:99], a[180:183], a[48:51]\n\t"
"buffer_load_dwordx4 a[236:239], off, s[80:83], s20 offset:2048 sc1\n\t"
"v_cvt_pk_bf16_f32 v239, v234, v235\n\t"
"s_mov_b32 s20, 454656\n\t"
"v_mfma_f32_16x16x32_bf16 a[56:59], v[96:99], a[184:187], a[56:59]\n\t"
"ds_read_b64_tr_b16 v[232:233], v255 offset:13312\n\t"
"v_mfma_f32_16x16x32_bf16 a[56:59], a[212:215], a[180:183], a[56:59]\n\t"
"buffer_load_dwordx4 a[240:243], off, s[80:83], s20 offset:0 sc1\n\t"
"v_mfma_f32_16x16x32_bf16 a[56:59], v[248:251], a[176:179], a[56:59]\n\t"
"ds_write_b128 v245, v[228:231] offset:1024\n\t"
"v_mfma_f32_16x16x32_bf16 a[64:67], v[248:251], a[180:183], a[64:67]\n\t"
"buffer_load_dwordx4 a[244:247], off, s[80:83], s20 offset:1024 sc1\n\t"
"v_mfma_f32_16x16x32_bf16 a[64:67], a[212:215], a[184:187], a[64:67]\n\t"
"ds_read_b64_tr_b16 v[228:229], v246 offset:20992\n\t"
"v_mfma_f32_16x16x32_bf16 a[64:67], v[96:99], a[188:191], a[64:67]\n\t"
"ds_write_b128 v245, v[236:239] offset:7168\n\t"
"v_mfma_f32_16x16x32_bf16 a[72:75], v[248:251], a[184:187], a[72:75]\n\t"
"buffer_load_dwordx4 a[248:251], off, s[80:83], s20 offset:2048 sc1\n\t"
"v_mfma_f32_16x16x32_bf16 a[72:75], v[96:99], a[192:195], a[72:75]\n\t"
"s_waitcnt lgkmcnt(3)\n\t"
"ds_read_b64_tr_b16 v[236:237], v255 offset:20992\n\t"
"v_mfma_f32_16x16x32_bf16 a[72:75], a[212:215], a[188:191], a[72:75]\n\t"
"s_mov_b32 s20, 466944\n\t"
"v_mfma_f32_4x4x4_16b_bf16 v[224:227], v[224:225], v[220:221], 0\n\t"
"s_waitcnt lgkmcnt(2)\n\t"
"v_mfma_f32_4x4x4_16b_bf16 v[232:235], v[232:233], v[220:221], 0\n\t"
"v_mfma_f32_4x4x4_16b_bf16 v[228:231], v[228:229], v[220:221], 0\n\t"
"s_waitcnt lgkmcnt(0)\n\t"
"buffer_load_dwordx4 a[252:255], off, s[80:83], s20 offset:0 sc1\n\t"
"v_mfma_f32_4x4x4_16b_bf16 v[236:239], v[236:237], v[220:221], 0\n\t"
"v_mfma_f32_16x16x32_bf16 a[80:83], v[248:251], a[188:191], a[80:83]\n\t"
"v_cvt_pk_bf16_f32 v224, v224, v225\n\t"
"v_cvt_pk_bf16_f32 v225, v226, v227\n\t"
"v_mfma_f32_16x16x32_bf16 a[80:83], v[96:99], a[196:199], a[80:83]\n\t"
"v_cvt_pk_bf16_f32 v226, v232, v233\n\t"
"v_cvt_pk_bf16_f32 v227, v234, v235\n\t"
"v_mfma_f32_16x16x32_bf16 a[80:83], a[212:215], a[192:195], a[80:83]\n\t"
"ds_write_b128 v245, v[224:227] offset:13312\n\t"
"v_mfma_f32_16x16x32_bf16 a[88:91], v[248:251], a[192:195], a[88:91]\n\t"
"v_cvt_pk_bf16_f32 v228, v228, v229\n\t"
"v_cvt_pk_bf16_f32 v229, v230, v231\n\t"
"v_mfma_f32_16x16x32_bf16 a[88:91], a[212:215], a[196:199], a[88:91]\n\t"
"ds_read_b64_tr_b16 v[232:233], v246 offset:27136\n\t"
"v_mfma_f32_16x16x32_bf16 a[88:91], v[96:99], a[200:203], a[88:91]\n\t"
"v_cvt_pk_bf16_f32 v230, v236, v237\n\t"
"v_cvt_pk_bf16_f32 v231, v238, v239\n\t"
"v_mfma_f32_16x16x32_bf16 a[96:99], a[212:215], a[200:203], a[96:99]\n\t"
"s_waitcnt vmcnt(7)\n\t"
"ds_read_b64_tr_b16 v[236:237], v255 offset:27136\n\t"
"v_mfma_f32_16x16x32_bf16 a[96:99], v[248:251], a[196:199], a[96:99]\n\t"
"ds_read_b64_tr_b16 v[224:225], v246 offset:33280\n\t"
"v_mfma_f32_16x16x32_bf16 a[96:99], v[96:99], a[204:207], a[96:99]\n\t"
"ds_write_b128 v245, v[228:231] offset:20992\n\t"
"v_mfma_f32_16x16x32_bf16 a[104:107], a[216:219], a[168:171], a[104:107]\n\t"
"buffer_load_dwordx4 a[212:215], off, s[80:83], s20 offset:1024 sc1\n\t"
"v_mfma_f32_16x16x32_bf16 a[104:107], a[224:227], a[176:179], a[104:107]\n\t"
"ds_read_b64_tr_b16 v[228:229], v255 offset:33280\n\t"
"v_mfma_f32_16x16x32_bf16 a[104:107], a[220:223], a[172:175], a[104:107]\n\t"
"buffer_load_dwordx4 v[96:99], off, s[80:83], s20 offset:2048 sc1\n\t"
"s_mov_b32 s20, 700416\n\t"
"v_mfma_f32_16x16x32_bf16 a[112:115], a[216:219], a[172:175], a[112:115]\n\t"
"buffer_load_dwordx4 v[248:251], off, s[80:83], s20 offset:0 sc1\n\t"
"v_mfma_f32_16x16x32_bf16 a[112:115], a[220:223], a[176:179], a[112:115]\n\t"
"s_waitcnt lgkmcnt(3)\n\t"
"ds_read_b128 a[168:171], v222 offset:768\n\t"
"v_mfma_f32_16x16x32_bf16 a[112:115], a[224:227], a[180:183], a[112:115]\n\t"
"s_waitcnt lgkmcnt(3)\n\t"
"ds_read_b128 a[172:175], v223 offset:6912\n\t"
"v_mfma_f32_4x4x4_16b_bf16 v[232:235], v[232:233], v[220:221], 0\n\t"
"s_waitcnt lgkmcnt(2)\n\t"
"v_mfma_f32_4x4x4_16b_bf16 v[236:239], v[236:237], v[220:221], 0\n\t"
"v_mfma_f32_4x4x4_16b_bf16 v[224:227], v[224:225], v[220:221], 0\n\t"
"v_mfma_f32_4x4x4_16b_bf16 v[228:231], v[228:229], v[220:221], 0\n\t"
"v_mfma_f32_16x16x32_bf16 a[120:123], a[220:223], a[180:183], a[120:123]\n\t"
"v_cvt_pk_bf16_f32 v232, v232, v233\n\t"
"v_cvt_pk_bf16_f32 v233, v234, v235\n\t"
"v_mfma_f32_16x16x32_bf16 a[120:123], a[216:219], a[176:179], a[120:123]\n\t"
"ds_read_b128 a[176:179], v223 offset:13056\n\t"
"v_cvt_pk_bf16_f32 v234, v236, v237\n\t"
"v_mfma_f32_16x16x32_bf16 a[120:123], a[224:227], a[184:187], a[120:123]\n\t"
"v_cvt_pk_bf16_f32 v235, v238, v239\n\t"
"v_cvt_pk_bf16_f32 v224, v224, v225\n\t"
"v_mfma_f32_16x16x32_bf16 a[128:131], a[216:219], a[180:183], a[128:131]\n\t"
"ds_read_b128 a[180:183], v222 offset:20736\n\t"
"v_cvt_pk_bf16_f32 v225, v226, v227\n\t"
"v_mfma_f32_16x16x32_bf16 a[128:131], a[224:227], a[188:191], a[128:131]\n\t"
"v_cvt_pk_bf16_f32 v226, v228, v229\n\t"
"v_cvt_pk_bf16_f32 v227, v230, v231\n\t"
"v_mfma_f32_16x16x32_bf16 a[128:131], a[220:223], a[184:187], a[128:131]\n\t"
"ds_write_b128 v245, v[232:235] offset:27136\n\t"
"v_mfma_f32_16x16x32_bf16 a[136:139], a[216:219], a[184:187], a[136:139]\n\t"
"v_mfma_f32_16x16x32_bf16 a[136:139], a[220:223], a[188:191], a[136:139]\n\t"
"ds_read_b128 a[184:187], v223 offset:26880\n\t"
"v_mfma_f32_16x16x32_bf16 a[136:139], a[224:227], a[192:195], a[136:139]\n\t"
"ds_write_b128 v245, v[224:227] offset:33280\n\t"
"v_mfma_f32_16x16x32_bf16 a[144:147], a[224:227], a[196:199], a[144:147]\n\t"
"v_mfma_f32_16x16x32_bf16 a[144:147], a[220:223], a[192:195], a[144:147]\n\t"
"v_mfma_f32_16x16x32_bf16 a[144:147], a[216:219], a[188:191], a[144:147]\n\t"
"ds_read_b128 a[188:191], v223 offset:33024\n\t"
"v_mfma_f32_16x16x32_bf16 a[152:155], a[216:219], a[192:195], a[152:155]\n\t"
"ds_read_b128 a[192:195], v222 offset:40704\n\t"
"v_mfma_f32_16x16x32_bf16 a[152:155], a[220:223], a[196:199], a[152:155]\n\t"
"v_mfma_f32_16x16x32_bf16 a[152:155], a[224:227], a[200:203], a[152:155]\n\t"
"s_barrier\n\t"
"v_mfma_f32_16x16x32_bf16 a[160:163], a[220:223], a[200:203], a[160:163]\n\t"
"s_waitcnt vmcnt(7) lgkmcnt(6)\n\t"
"ds_read_b64_tr_b16 v[236:237], v240 offset:40960\n\t"
"v_mfma_f32_16x16x32_bf16 a[160:163], a[216:219], a[196:199], a[160:163]\n\t"
"ds_read_b64_tr_b16 v[238:239], v240 offset:42496\n\t"
"v_mfma_f32_16x16x32_bf16 a[160:163], a[224:227], a[204:207], a[160:163]\n\t"
"ds_read_b64_tr_b16 v[228:229], v241 offset:40960\n\t"
"v_mfma_f32_16x16x32_bf16 v[200:203], a[228:231], a[168:171], v[200:203]\n\t"
"s_waitcnt lgkmcnt(8)\n\t"
"ds_read_b64_tr_b16 v[230:231], v241 offset:42496\n\t"
"v_subrev_u32 v240, 59904, v240\n\t"
"v_mfma_f32_16x16x32_bf16 v[200:203], a[236:239], a[176:179], v[200:203]\n\t"
"ds_read_b128 a[196:199], v223 offset:46848\n\t"
"v_subrev_u32 v241, 59904, v241\n\t"
"v_mfma_f32_16x16x32_bf16 v[200:203], a[232:235], a[172:175], v[200:203]\n\t"
"ds_read_b64_tr_b16 v[232:233], v240 offset:40960\n\t"
"v_mfma_f32_16x16x32_bf16 v[208:211], a[228:231], a[172:175], v[208:211]\n\t"
"s_waitcnt lgkmcnt(4)\n\t"
"ds_read_b64_tr_b16 v[234:235], v240 offset:42496\n\t"
"v_mfma_f32_16x16x32_bf16 v[208:211], a[232:235], a[176:179], v[208:211]\n\t"
"buffer_load_dwordx4 a[220:223], off, s[80:83], s20 offset:1024 sc1\n\t"
"v_mfma_f32_16x16x32_bf16 v[208:211], a[236:239], a[180:183], v[208:211]\n\t"
"s_waitcnt lgkmcnt(3)\n\t"
"ds_read_b64_tr_b16 v[224:225], v241 offset:40960\n\t"
"ds_read_b64_tr_b16 v[226:227], v241 offset:42496\n\t"
"v_mfma_f32_16x16x32_bf16 v[236:239], v[236:239], a[208:211], 0\n\t"
"buffer_load_dwordx4 a[216:219], off, s[80:83], s20 offset:2048 sc1\n\t"
"s_mov_b32 s20, 712704\n\t"
"v_mfma_f32_16x16x32_bf16 v[228:231], v[228:231], a[208:211], 0\n\t"
"s_waitcnt lgkmcnt(2)\n\t"
"buffer_load_dwordx4 a[224:227], off, s[80:83], s20 offset:0 sc1\n\t"
"v_mfma_f32_16x16x32_bf16 v[232:235], v[232:235], a[208:211], 0\n\t"
"s_waitcnt lgkmcnt(0)\n\t"
"ds_read_b128 a[200:203], v223 offset:52992\n\t"
"v_mfma_f32_16x16x32_bf16 v[224:227], v[224:227], a[208:211], 0\n\t"
"ds_read_b128 a[204:207], v222 offset:60672\n\t"
"v_mfma_f32_16x16x32_bf16 v[216:219], a[232:235], a[180:183], v[216:219]\n\t"
"v_mfma_f32_16x16x32_bf16 v[216:219], a[228:231], a[176:179], v[216:219]\n\t"
"v_cvt_pk_bf16_f32 v236, v236, v237\n\t"
"v_cvt_pk_bf16_f32 v237, v238, v239\n\t"
"v_mfma_f32_16x16x32_bf16 v[216:219], a[236:239], a[184:187], v[216:219]\n\t"
"v_cvt_pk_bf16_f32 v238, v228, v229\n\t"
"v_cvt_pk_bf16_f32 v239, v230, v231\n\t"
"v_mfma_f32_16x16x32_bf16 a[4:7], a[228:231], a[180:183], a[4:7]\n\t"
"ds_read_b64_tr_b16 v[228:229], v246 offset:40960\n\t"
"v_cvt_pk_bf16_f32 v232, v232, v233\n\t"
"v_mfma_f32_16x16x32_bf16 a[4:7], a[236:239], a[188:191], a[4:7]\n\t"
"v_cvt_pk_bf16_f32 v233, v234, v235\n\t"
"v_cvt_pk_bf16_f32 v234, v224, v225\n\t"
"v_mfma_f32_16x16x32_bf16 a[4:7], a[232:235], a[184:187], a[4:7]\n\t"
"ds_write_b128 v242, v[236:239] offset:32256\n\t"
"v_mfma_f32_16x16x32_bf16 a[12:15], a[228:231], a[184:187], a[12:15]\n\t"
"v_cvt_pk_bf16_f32 v235, v226, v227\n\t"
"v_mfma_f32_16x16x32_bf16 a[12:15], a[232:235], a[188:191], a[12:15]\n\t"
"ds_read_b64_tr_b16 v[224:225], v255 offset:40960\n\t"
"v_mfma_f32_16x16x32_bf16 a[12:15], a[236:239], a[192:195], a[12:15]\n\t"
"ds_write_b128 v242, v[232:235] offset:27648\n\t"
"v_mfma_f32_16x16x32_bf16 a[20:23], a[236:239], a[196:199], a[20:23]\n\t"
"s_waitcnt lgkmcnt(5)\n\t"
"v_mfma_f32_16x16x32_bf16 a[20:23], a[232:235], a[192:195], a[20:23]\n\t"
"ds_read_b64_tr_b16 v[236:237], v246 offset:47104\n\t"
"v_mfma_f32_16x16x32_bf16 a[20:23], a[228:231], a[188:191], a[20:23]\n\t"
"ds_read_b64_tr_b16 v[232:233], v255 offset:47104\n\t"
"v_mfma_f32_16x16x32_bf16 a[28:31], a[228:231], a[192:195], a[28:31]\n\t"
"s_waitcnt lgkmcnt(6)\n\t"
"v_mfma_f32_16x16x32_bf16 a[28:31], a[232:235], a[196:199], a[28:31]\n\t"
"v_mfma_f32_16x16x32_bf16 a[28:31], a[236:239], a[200:203], a[28:31]\n\t"
"s_barrier\n\t"
"v_mfma_f32_16x16x32_bf16 a[36:39], a[232:235], a[200:203], a[36:39]\n\t"
"s_waitcnt lgkmcnt(5)\n\t"
"v_mfma_f32_16x16x32_bf16 a[36:39], a[228:231], a[196:199], a[36:39]\n\t"
"buffer_load_dwordx4 a[232:235], off, s[80:83], s20 offset:1024 sc1\n\t"
"v_mfma_f32_16x16x32_bf16 a[36:39], a[236:239], a[204:207], a[36:39]\n\t"
"s_waitcnt lgkmcnt(1)\n\t"
"v_mfma_f32_4x4x4_16b_bf16 v[228:231], v[228:229], v[220:221], 0\n\t"
"s_waitcnt lgkmcnt(0)\n\t"
"v_mfma_f32_4x4x4_16b_bf16 v[224:227], v[224:225], v[220:221], 0\n\t"
"s_waitcnt vmcnt(8)\n\t"
"v_mfma_f32_4x4x4_16b_bf16 v[236:239], v[236:237], v[220:221], 0\n\t"
"v_mfma_f32_4x4x4_16b_bf16 v[232:235], v[232:233], v[220:221], 0\n\t"
"buffer_load_dwordx4 a[228:231], off, s[80:83], s20 offset:2048 sc1\n\t"
"s_mov_b32 s20, 724992\n\t"
"v_mfma_f32_16x16x32_bf16 a[44:47], a[240:243], a[168:171], a[44:47]\n\t"
"buffer_load_dwordx4 a[236:239], off, s[80:83], s20 offset:0 sc1\n\t"
"v_mfma_f32_16x16x32_bf16 a[44:47], a[248:251], a[176:179], a[44:47]\n\t"
"v_cvt_pk_bf16_f32 v228, v228, v229\n\t"
"v_cvt_pk_bf16_f32 v229, v230, v231\n\t"
"v_mfma_f32_16x16x32_bf16 a[44:47], a[244:247], a[172:175], a[44:47]\n\t"
"v_cvt_pk_bf16_f32 v230, v224, v225\n\t"
"v_cvt_pk_bf16_f32 v231, v226, v227\n\t"
"v_mfma_f32_16x16x32_bf16 a[52:55], a[240:243], a[172:175], a[52:55]\n\t"
"ds_read_b64_tr_b16 v[224:225], v246 offset:53248\n\t"
"v_cvt_pk_bf16_f32 v236, v236, v237\n\t"
"v_mfma_f32_16x16x32_bf16 a[52:55], a[244:247], a[176:179], a[52:55]\n\t"
"v_cvt_pk_bf16_f32 v237, v238, v239\n\t"
"v_cvt_pk_bf16_f32 v238, v232, v233\n\t"
"v_mfma_f32_16x16x32_bf16 a[52:55], a[248:251], a[180:183], a[52:55]\n\t"
"ds_write_b128 v245, v[228:231] offset:40960\n\t"
"v_cvt_pk_bf16_f32 v239, v234, v235\n\t"
"v_mfma_f32_16x16x32_bf16 a[60:63], a[248:251], a[184:187], a[60:63]\n\t"
"v_mfma_f32_16x16x32_bf16 a[60:63], a[244:247], a[180:183], a[60:63]\n\t"
"ds_read_b64_tr_b16 v[232:233], v255 offset:53248\n\t"
"v_mfma_f32_16x16x32_bf16 a[60:63], a[240:243], a[176:179], a[60:63]\n\t"
"ds_write_b128 v245, v[236:239] offset:47104\n\t"
"v_mfma_f32_16x16x32_bf16 a[68:71], a[240:243], a[180:183], a[68:71]\n\t"
"v_mfma_f32_16x16x32_bf16 a[68:71], a[244:247], a[184:187], a[68:71]\n\t"
"v_mfma_f32_16x16x32_bf16 a[68:71], a[248:251], a[188:191], a[68:71]\n\t"
"v_mfma_f32_16x16x32_bf16 a[76:79], a[240:243], a[184:187], a[76:79]\n\t"
"s_waitcnt lgkmcnt(3)\n\t"
"v_mfma_f32_16x16x32_bf16 a[76:79], a[248:251], a[192:195], a[76:79]\n\t"
"s_waitcnt lgkmcnt(1)\n\t"
"v_mfma_f32_16x16x32_bf16 a[76:79], a[244:247], a[188:191], a[76:79]\n\t"
"v_mfma_f32_4x4x4_16b_bf16 v[224:227], v[224:225], v[220:221], 0\n\t"
"v_mfma_f32_4x4x4_16b_bf16 v[232:235], v[232:233], v[220:221], 0\n\t"
"v_mfma_f32_16x16x32_bf16 a[84:87], a[240:243], a[188:191], a[84:87]\n\t"
"v_mfma_f32_16x16x32_bf16 a[84:87], a[248:251], a[196:199], a[84:87]\n\t"
"v_cvt_pk_bf16_f32 v224, v224, v225\n\t"
"v_cvt_pk_bf16_f32 v225, v226, v227\n\t"
"v_mfma_f32_16x16x32_bf16 a[84:87], a[244:247], a[192:195], a[84:87]\n\t"
"v_cvt_pk_bf16_f32 v226, v232, v233\n\t"
"v_cvt_pk_bf16_f32 v227, v234, v235\n\t"
"v_mfma_f32_16x16x32_bf16 a[92:95], a[240:243], a[192:195], a[92:95]\n\t"
"v_mfma_f32_16x16x32_bf16 a[92:95], a[244:247], a[196:199], a[92:95]\n\t"
"s_waitcnt vmcnt(7)\n\t"
"v_mfma_f32_16x16x32_bf16 a[92:95], a[248:251], a[200:203], a[92:95]\n\t"
"ds_write_b128 v245, v[224:227] offset:53248\n\t"
"v_mfma_f32_16x16x32_bf16 a[100:103], a[244:247], a[200:203], a[100:103]\n\t"
"buffer_load_dwordx4 a[244:247], off, s[80:83], s20 offset:1024 sc1\n\t"
"v_mfma_f32_16x16x32_bf16 a[100:103], a[240:243], a[196:199], a[100:103]\n\t"
"Lpg5_%=:\n\t"
"s_cbranch_vccnz Lpg9_%=\n\t"
"v_mfma_f32_16x16x32_bf16 a[100:103], a[248:251], a[204:207], a[100:103]\n\t"
"v_mfma_f32_16x16x32_bf16 a[108:111], a[252:255], a[168:171], a[108:111]\n\t"
"buffer_load_dwordx4 a[240:243], off, s[80:83], s20 offset:2048 sc1\n\t"
"v_mfma_f32_16x16x32_bf16 a[108:111], v[96:99], a[176:179], a[108:111]\n\t"
"s_mov_b32 s20, 737280\n\t"
"v_mfma_f32_16x16x32_bf16 a[108:111], a[212:215], a[172:175], a[108:111]\n\t"
"s_waitcnt lgkmcnt(0)\n\t"
"s_barrier\n\t"
"v_mfma_f32_16x16x32_bf16 a[116:119], a[252:255], a[172:175], a[116:119]\n\t"
"ds_read_b128 a[168:171], v222 offset:1024\n\t"
"v_mfma_f32_16x16x32_bf16 a[116:119], a[212:215], a[176:179], a[116:119]\n\t"
"buffer_load_dwordx4 a[248:251], off, s[80:83], s20 offset:0 sc1\n\t"
"v_mfma_f32_16x16x32_bf16 a[116:119], v[96:99], a[180:183], a[116:119]\n\t"
"ds_read_b128 a[172:175], v223 offset:7168\n\t"
"v_mfma_f32_16x16x32_bf16 a[124:127], v[96:99], a[184:187], a[124:127]\n\t"
"v_mfma_f32_16x16x32_bf16 a[124:127], a[212:215], a[180:183], a[124:127]\n\t"
"v_mfma_f32_16x16x32_bf16 a[124:127], a[252:255], a[176:179], a[124:127]\n\t"
"ds_read_b128 a[176:179], v223 offset:13312\n\t"
"v_mfma_f32_16x16x32_bf16 a[132:135], a[252:255], a[180:183], a[132:135]\n\t"
"ds_read_b64_tr_b16 v[228:229], v240 offset:1536\n\t"
"v_mfma_f32_16x16x32_bf16 a[132:135], a[212:215], a[184:187], a[132:135]\n\t"
"ds_read_b64_tr_b16 v[230:231], v240 offset:3072\n\t"
"v_mfma_f32_16x16x32_bf16 a[132:135], v[96:99], a[188:191], a[132:135]\n\t"
"ds_read_b64_tr_b16 v[236:237], v241 offset:1536\n\t"
"v_mfma_f32_16x16x32_bf16 a[140:143], v[96:99], a[192:195], a[140:143]\n\t"
"ds_read_b64_tr_b16 v[238:239], v241 offset:3072\n\t"
"v_mfma_f32_16x16x32_bf16 a[140:143], a[212:215], a[188:191], a[140:143]\n\t"
"ds_read_b128 a[180:183], v222 offset:20992\n\t"
"v_mfma_f32_16x16x32_bf16 a[140:143], a[252:255], a[184:187], a[140:143]\n\t"
"ds_read_b64_tr_b16 v[232:233], v240 offset:21504\n\t"
"v_mfma_f32_16x16x32_bf16 a[148:151], a[252:255], a[188:191], a[148:151]\n\t"
"ds_read_b64_tr_b16 v[234:235], v240 offset:23040\n\t"
"v_mfma_f32_16x16x32_bf16 a[148:151], a[212:215], a[192:195], a[148:151]\n\t"
"ds_read_b64_tr_b16 v[224:225], v241 offset:21504\n\t"
"v_mfma_f32_16x16x32_bf16 a[148:151], v[96:99], a[196:199], a[148:151]\n\t"
"ds_read_b64_tr_b16 v[226:227], v241 offset:23040\n\t"
"v_mfma_f32_16x16x32_bf16 a[156:159], v[96:99], a[200:203], a[156:159]\n\t"
"ds_read_b128 a[184:187], v223 offset:27136\n\t"
"v_mfma_f32_16x16x32_bf16 a[156:159], a[212:215], a[196:199], a[156:159]\n\t"
"ds_read_b128 a[188:191], v223 offset:33280\n\t"
"v_mfma_f32_16x16x32_bf16 a[156:159], a[252:255], a[192:195], a[156:159]\n\t"
"ds_read_b128 a[192:195], v222 offset:40960\n\t"
"v_mfma_f32_16x16x32_bf16 a[164:167], a[252:255], a[196:199], a[164:167]\n\t"
"buffer_load_dwordx4 a[252:255], off, s[80:83], s20 offset:1024 sc1\n\t"
"v_mfma_f32_16x16x32_bf16 a[164:167], a[212:215], a[200:203], a[164:167]\n\t"
"s_waitcnt lgkmcnt(8)\n\t"
"ds_read_b128 a[196:199], v223 offset:47104\n\t"
"v_mfma_f32_16x16x32_bf16 a[164:167], v[96:99], a[204:207], a[164:167]\n\t"
"s_waitcnt lgkmcnt(6)\n\t"
"ds_read_b128 a[200:203], v223 offset:53248\n\t"
"v_mfma_f32_16x16x32_bf16 v[228:231], v[228:231], a[208:211], 0\n\t"
"buffer_load_dwordx4 v[96:99], off, s[80:83], s20 offset:2048 sc1\n\t"
"v_mfma_f32_16x16x32_bf16 v[236:239], v[236:239], a[208:211], 0\n\t"
"s_waitcnt vmcnt(9) lgkmcnt(5)\n\t"
"ds_read_b128 a[204:207], v222 offset:60928\n\t"
"s_mov_b32 s20, 749568\n\t"
"v_mfma_f32_16x16x32_bf16 v[232:235], v[232:235], a[208:211], 0\n\t"
"v_mfma_f32_16x16x32_bf16 v[224:227], v[224:227], a[208:211], 0\n\t"
"buffer_load_dwordx4 a[212:215], off, s[80:83], s20 offset:0 sc1\n\t"
"Lst6_%=:\n\t"
"v_mfma_f32_16x16x32_bf16 v[196:199], v[248:251], a[168:171], v[196:199]\n\t"
"v_mfma_f32_16x16x32_bf16 v[196:199], a[216:219], a[176:179], v[196:199]\n\t"
"v_cvt_pk_bf16_f32 v228, v228, v229\n\t"
"v_cvt_pk_bf16_f32 v229, v230, v231\n\t"
"v_mfma_f32_16x16x32_bf16 v[196:199], a[220:223], a[172:175], v[196:199]\n\t"
"v_cvt_pk_bf16_f32 v230, v236, v237\n\t"
"v_cvt_pk_bf16_f32 v231, v238, v239\n\t"
"v_mfma_f32_16x16x32_bf16 v[204:207], v[248:251], a[172:175], v[204:207]\n\t"
"s_waitcnt lgkmcnt(5)\n\t"
"v_cvt_pk_bf16_f32 v232, v232, v233\n\t"
"v_cvt_pk_bf16_f32 v233, v234, v235\n\t"
"v_mfma_f32_16x16x32_bf16 v[204:207], a[220:223], a[176:179], v[204:207]\n\t"
"v_cvt_pk_bf16_f32 v234, v224, v225\n\t"
"v_cvt_pk_bf16_f32 v235, v226, v227\n\t"
"v_mfma_f32_16x16x32_bf16 v[204:207], a[216:219], a[180:183], v[204:207]\n\t"
"ds_write_b128 v242, v[228:231] offset:0\n\t"
"v_mfma_f32_16x16x32_bf16 v[212:215], a[216:219], a[184:187], v[212:215]\n\t"
"s_waitcnt lgkmcnt(5)\n\t"
"v_mfma_f32_16x16x32_bf16 v[212:215], a[220:223], a[180:183], v[212:215]\n\t"
"v_mfma_f32_16x16x32_bf16 v[212:215], v[248:251], a[176:179], v[212:215]\n\t"
"ds_write_b128 v242, v[232:235] offset:1536\n\t"
"v_mfma_f32_16x16x32_bf16 a[0:3], v[248:251], a[180:183], a[0:3]\n\t"
"s_waitcnt lgkmcnt(5)\n\t"
"v_mfma_f32_16x16x32_bf16 a[0:3], a[220:223], a[184:187], a[0:3]\n\t"
"ds_read_b64_tr_b16 v[236:237], v240 offset:41472\n\t"
"v_mfma_f32_16x16x32_bf16 a[0:3], a[216:219], a[188:191], a[0:3]\n\t"
"ds_read_b64_tr_b16 v[238:239], v240 offset:43008\n\t"
"v_mfma_f32_16x16x32_bf16 a[8:11], a[216:219], a[192:195], a[8:11]\n\t"
"s_waitcnt lgkmcnt(6)\n\t"
"ds_read_b64_tr_b16 v[224:225], v241 offset:41472\n\t"
"v_mfma_f32_16x16x32_bf16 a[8:11], a[220:223], a[188:191], a[8:11]\n\t"
"ds_read_b64_tr_b16 v[226:227], v241 offset:43008\n\t"
"v_add_u32 v240, 59904, v240\n\t"
"v_mfma_f32_16x16x32_bf16 a[8:11], v[248:251], a[184:187], a[8:11]\n\t"
"v_add_u32 v241, 59904, v241\n\t"
"v_mfma_f32_16x16x32_bf16 a[16:19], v[248:251], a[188:191], a[16:19]\n\t"
"s_waitcnt lgkmcnt(7)\n\t"
"ds_read_b64_tr_b16 v[228:229], v240 offset:1536\n\t"
"v_mfma_f32_16x16x32_bf16 a[16:19], a[220:223], a[192:195], a[16:19]\n\t"
"ds_read_b64_tr_b16 v[230:231], v240 offset:3072\n\t"
"v_mfma_f32_16x16x32_bf16 a[16:19], a[216:219], a[196:199], a[16:19]\n\t"
"ds_read_b64_tr_b16 v[232:233], v241 offset:1536\n\t"
"v_mfma_f32_16x16x32_bf16 a[24:27], a[216:219], a[200:203], a[24:27]\n\t"
"s_waitcnt lgkmcnt(9)\n\t"
"ds_read_b64_tr_b16 v[234:235], v241 offset:3072\n\t"
"v_mfma_f32_16x16x32_bf16 a[24:27], a[220:223], a[196:199], a[24:27]\n\t"
"v_mfma_f32_16x16x32_bf16 a[24:27], v[248:251], a[192:195], a[24:27]\n\t"
"v_mfma_f32_16x16x32_bf16 a[32:35], v[248:251], a[196:199], a[32:35]\n\t"
"buffer_load_dwordx4 v[248:251], off, s[80:83], s20 offset:1024 sc1\n\t"
"v_mfma_f32_16x16x32_bf16 a[32:35], a[220:223], a[200:203], a[32:35]\n\t"
"s_waitcnt lgkmcnt(4)\n\t"
"v_mfma_f32_16x16x32_bf16 a[32:35], a[216:219], a[204:207], a[32:35]\n\t"
"s_waitcnt lgkmcnt(2)\n\t"
"v_mfma_f32_16x16x32_bf16 v[236:239], v[236:239], a[208:211], 0\n\t"
"buffer_load_dwordx4 a[216:219], off, s[80:83], s20 offset:2048 sc1\n\t"
"v_mfma_f32_16x16x32_bf16 v[224:227], v[224:227], a[208:211], 0\n\t"
"s_waitcnt vmcnt(9) lgkmcnt(0)\n\t"
"s_mov_b32 s20, 761856\n\t"
"v_mfma_f32_16x16x32_bf16 v[228:231], v[228:231], a[208:211], 0\n\t"
"v_mfma_f32_16x16x32_bf16 v[232:235], v[232:235], a[208:211], 0\n\t"
"buffer_load_dwordx4 a[220:223], off, s[80:83], s20 offset:0 sc1\n\t"
"v_mfma_f32_16x16x32_bf16 a[40:43], a[224:227], a[168:171], a[40:43]\n\t"
"v_mfma_f32_16x16x32_bf16 a[40:43], a[228:231], a[176:179], a[40:43]\n\t"
"v_cvt_pk_bf16_f32 v236, v236, v237\n\t"
"v_cvt_pk_bf16_f32 v237, v238, v239\n\t"
"v_mfma_f32_16x16x32_bf16 a[40:43], a[232:235], a[172:175], a[40:43]\n\t"
"v_cvt_pk_bf16_f32 v238, v224, v225\n\t"
"v_cvt_pk_bf16_f32 v239, v226, v227\n\t"
"v_mfma_f32_16x16x32_bf16 a[48:51], a[224:227], a[172:175], a[48:51]\n\t"
"v_cvt_pk_bf16_f32 v228, v228, v229\n\t"
"v_cvt_pk_bf16_f32 v229, v230, v231\n\t"
"v_mfma_f32_16x16x32_bf16 a[48:51], a[232:235], a[176:179], a[48:51]\n\t"
"v_cvt_pk_bf16_f32 v230, v232, v233\n\t"
"v_cvt_pk_bf16_f32 v231, v234, v235\n\t"
"v_mfma_f32_16x16x32_bf16 a[48:51], a[228:231], a[180:183], a[48:51]\n\t"
"ds_write_b128 v242, v[236:239] offset:3072\n\t"
"v_mfma_f32_16x16x32_bf16 a[56:59], a[228:231], a[184:187], a[56:59]\n\t"
"v_mfma_f32_16x16x32_bf16 a[56:59], a[232:235], a[180:183], a[56:59]\n\t"
"v_mfma_f32_16x16x32_bf16 a[56:59], a[224:227], a[176:179], a[56:59]\n\t"
"ds_write_b128 v242, v[228:231] offset:4608\n\t"
"v_mfma_f32_16x16x32_bf16 a[64:67], a[224:227], a[180:183], a[64:67]\n\t"
"v_mfma_f32_16x16x32_bf16 a[64:67], a[232:235], a[184:187], a[64:67]\n\t"
"ds_read_b64_tr_b16 v[224:225], v240 offset:21504\n\t"
"v_mfma_f32_16x16x32_bf16 a[64:67], a[228:231], a[188:191], a[64:67]\n\t"
"ds_read_b64_tr_b16 v[226:227], v240 offset:23040\n\t"
"v_mfma_f32_16x16x32_bf16 a[72:75], a[228:231], a[192:195], a[72:75]\n\t"
"ds_read_b64_tr_b16 v[232:233], v241 offset:21504\n\t"
"v_mfma_f32_16x16x32_bf16 a[72:75], a[232:235], a[188:191], a[72:75]\n\t"
"ds_read_b64_tr_b16 v[234:235], v241 offset:23040\n\t"
"v_mfma_f32_16x16x32_bf16 a[72:75], a[224:227], a[184:187], a[72:75]\n\t"
"ds_read_b64_tr_b16 v[236:237], v240 offset:41472\n\t"
"v_mfma_f32_16x16x32_bf16 a[80:83], a[224:227], a[188:191], a[80:83]\n\t"
"ds_read_b64_tr_b16 v[238:239], v240 offset:43008\n\t"
"v_mfma_f32_16x16x32_bf16 a[80:83], a[232:235], a[192:195], a[80:83]\n\t"
"ds_read_b64_tr_b16 v[228:229], v241 offset:41472\n\t"
"v_mfma_f32_16x16x32_bf16 a[80:83], a[228:231], a[196:199], a[80:83]\n\t"
"ds_read_b64_tr_b16 v[230:231], v241 offset:43008\n\t"
"v_mfma_f32_16x16x32_bf16 a[88:91], a[228:231], a[200:203], a[88:91]\n\t"
"v_mfma_f32_16x16x32_bf16 a[88:91], a[232:235], a[196:199], a[88:91]\n\t"
"v_mfma_f32_16x16x32_bf16 a[88:91], a[224:227], a[192:195], a[88:91]\n\t"
"v_mfma_f32_16x16x32_bf16 a[96:99], a[224:227], a[196:199], a[96:99]\n\t"
"buffer_load_dwordx4 a[224:227], off, s[80:83], s20 offset:1024 sc1\n\t"
"v_mfma_f32_16x16x32_bf16 a[96:99], a[232:235], a[200:203], a[96:99]\n\t"
"s_waitcnt lgkmcnt(4)\n\t"
"v_mfma_f32_16x16x32_bf16 a[96:99], a[228:231], a[204:207], a[96:99]\n\t"
"s_waitcnt lgkmcnt(2)\n\t"
"v_mfma_f32_16x16x32_bf16 v[224:227], v[224:227], a[208:211], 0\n\t"
"buffer_load_dwordx4 a[228:231], off, s[80:83], s20 offset:2048 sc1\n\t"
"v_mfma_f32_16x16x32_bf16 v[232:235], v[232:235], a[208:211], 0\n\t"
"s_waitcnt vmcnt(9) lgkmcnt(0)\n\t"
"s_mov_b32 s20, 626688\n\t"
"v_mfma_f32_16x16x32_bf16 v[236:239], v[236:239], a[208:211], 0\n\t"
"v_mfma_f32_16x16x32_bf16 v[228:231], v[228:231], a[208:211], 0\n\t"
"buffer_load_dwordx4 a[232:235], off, s[80:83], s20 offset:0 sc1\n\t"
"v_mfma_f32_16x16x32_bf16 a[104:107], a[236:239], a[168:171], a[104:107]\n\t"
"ds_read_b128 a[168:171], v222 offset:1280\n\t"
"v_mfma_f32_16x16x32_bf16 a[104:107], a[240:243], a[176:179], a[104:107]\n\t"
"v_cvt_pk_bf16_f32 v224, v224, v225\n\t"
"v_cvt_pk_bf16_f32 v225, v226, v227\n\t"
"v_mfma_f32_16x16x32_bf16 a[104:107], a[244:247], a[172:175], a[104:107]\n\t"
"v_cvt_pk_bf16_f32 v226, v232, v233\n\t"
"v_cvt_pk_bf16_f32 v227, v234, v235\n\t"
"v_mfma_f32_16x16x32_bf16 a[112:115], a[236:239], a[172:175], a[112:115]\n\t"
"ds_read_b128 a[172:175], v223 offset:7424\n\t"
"v_cvt_pk_bf16_f32 v236, v236, v237\n\t"
"v_mfma_f32_16x16x32_bf16 a[112:115], a[244:247], a[176:179], a[112:115]\n\t"
"v_cvt_pk_bf16_f32 v237, v238, v239\n\t"
"v_cvt_pk_bf16_f32 v238, v228, v229\n\t"
"v_mfma_f32_16x16x32_bf16 a[112:115], a[240:243], a[180:183], a[112:115]\n\t"
"buffer_load_dwordx4 a[208:211], off, s[80:83], s20 offset:1024 sc1\n\t"
"v_mfma_f32_16x16x32_bf16 a[120:123], a[240:243], a[184:187], a[120:123]\n\t"
"v_cvt_pk_bf16_f32 v239, v230, v231\n\t"
"v_mfma_f32_16x16x32_bf16 a[120:123], a[244:247], a[180:183], a[120:123]\n\t"
"v_mfma_f32_16x16x32_bf16 a[120:123], a[236:239], a[176:179], a[120:123]\n\t"
"ds_write_b128 v242, v[224:227] offset:6144\n\t"
"v_mfma_f32_16x16x32_bf16 a[128:131], a[236:239], a[180:183], a[128:131]\n\t"
"v_mfma_f32_16x16x32_bf16 a[128:131], a[244:247], a[184:187], a[128:131]\n\t"
"ds_read_b128 a[176:179], v223 offset:13568\n\t"
"v_mfma_f32_16x16x32_bf16 a[128:131], a[240:243], a[188:191], a[128:131]\n\t"
"ds_write_b128 v242, v[236:239] offset:7680\n\t"
"v_mfma_f32_16x16x32_bf16 a[136:139], a[240:243], a[192:195], a[136:139]\n\t"
"buffer_load_dwordx4 v[240:243], off, s[80:83], s20 offset:2048 sc1\n\t"
"v_mfma_f32_16x16x32_bf16 a[136:139], a[244:247], a[188:191], a[136:139]\n\t"
"ds_read_b128 a[180:183], v222 offset:21248\n\t"
"v_mfma_f32_16x16x32_bf16 a[136:139], a[236:239], a[184:187], a[136:139]\n\t"
"ds_read_b128 a[184:187], v223 offset:27392\n\t"
"v_mfma_f32_16x16x32_bf16 a[144:147], a[236:239], a[188:191], a[144:147]\n\t"
"s_mov_b32 s20, 638976\n\t"
"ds_read_b128 a[188:191], v223 offset:33536\n\t"
"v_mfma_f32_16x16x32_bf16 a[144:147], a[244:247], a[192:195], a[144:147]\n\t"
"v_mfma_f32_16x16x32_bf16 a[144:147], a[240:243], a[196:199], a[144:147]\n\t"
"v_mfma_f32_16x16x32_bf16 a[152:155], a[240:243], a[200:203], a[152:155]\n\t"
"v_mfma_f32_16x16x32_bf16 a[152:155], a[244:247], a[196:199], a[152:155]\n\t"
"v_mfma_f32_16x16x32_bf16 a[152:155], a[236:239], a[192:195], a[152:155]\n\t"
"ds_read_b128 a[192:195], v222 offset:41216\n\t"
"v_mfma_f32_16x16x32_bf16 a[160:163], a[236:239], a[196:199], a[160:163]\n\t"
"buffer_load_dwordx4 a[236:239], off, s[80:83], s20 offset:0 sc1\n\t"
"v_mfma_f32_16x16x32_bf16 a[160:163], a[244:247], a[200:203], a[160:163]\n\t"
"s_waitcnt vmcnt(10) lgkmcnt(5)\n\t"
"ds_read_b128 a[196:199], v223 offset:47360\n\t"
"v_mfma_f32_16x16x32_bf16 a[160:163], a[240:243], a[204:207], a[160:163]\n\t"
"ds_read_b128 a[200:203], v223 offset:53504\n\t"
"v_mfma_f32_16x16x32_bf16 v[200:203], a[248:251], a[168:171], v[200:203]\n\t"
"s_waitcnt lgkmcnt(5)\n\t"
"ds_read_b128 a[204:207], v222 offset:61184\n\t"
"v_mfma_f32_16x16x32_bf16 v[200:203], v[96:99], a[176:179], v[200:203]\n\t"
"buffer_load_dwordx4 a[240:243], off, s[80:83], s20 offset:1024 sc1\n\t"
"v_mfma_f32_16x16x32_bf16 v[200:203], a[252:255], a[172:175], v[200:203]\n\t"
"s_add_u32 m0, s25, 256\n\t"
"v_mfma_f32_16x16x32_bf16 v[208:211], a[248:251], a[172:175], v[208:211]\n\t"
"buffer_load_dwordx4 a[244:247], off, s[80:83], s20 offset:2048 sc1\n\t"
"v_mfma_f32_16x16x32_bf16 v[208:211], a[252:255], a[176:179], v[208:211]\n\t"
"s_waitcnt lgkmcnt(5)\n\t"
"v_mfma_f32_16x16x32_bf16 v[208:211], v[96:99], a[180:183], v[208:211]\n\t"
"s_waitcnt lgkmcnt(0)\n\t"
"s_barrier\n\t"
"v_mfma_f32_16x16x32_bf16 v[216:219], v[96:99], a[184:187], v[216:219]\n\t"
"v_mfma_f32_16x16x32_bf16 v[216:219], a[252:255], a[180:183], v[216:219]\n\t"
"buffer_load_dwordx4 v254, s[12:15], 0 offen offset:256 nt lds\n\t"
"v_mfma_f32_16x16x32_bf16 v[216:219], a[248:251], a[176:179], v[216:219]\n\t"
"s_add_u32 m0, s26, 256\n\t"
"ds_read_b64_tr_b16 v[232:233], v246 offset:1536\n\t"
"v_mfma_f32_16x16x32_bf16 a[4:7], a[248:251], a[180:183], a[4:7]\n\t"
"ds_read_b64_tr_b16 v[228:229], v255 offset:1536\n\t"
"v_mfma_f32_16x16x32_bf16 a[4:7], a[252:255], a[184:187], a[4:7]\n\t"
"ds_read_b64_tr_b16 v[224:225], v246 offset:7680\n\t"
"v_mfma_f32_16x16x32_bf16 a[4:7], v[96:99], a[188:191], a[4:7]\n\t"
"s_mov_b64 exec, s[36:37]\n\t"
"buffer_load_dwordx4 v254, s[12:15], s27 offen offset:256 nt lds\n\t"
"s_mov_b64 exec, -1\n\t"
"s_add_u32 m0, s24, 256\n\t"
"s_add_u32 s28, s21, 0\n\t"
"v_mfma_f32_16x16x32_bf16 a[12:15], v[96:99], a[192:195], a[12:15]\n\t"
"buffer_load_dwordx4 v247, s[12:15], s28 offen offset:256 nt lds\n\t"
"v_mfma_f32_16x16x32_bf16 a[12:15], a[252:255], a[188:191], a[12:15]\n\t"
"s_add_u32 m0, s24, 6400\n\t"
"s_add_u32 s28, s21, 196608\n\t"
"ds_read_b64_tr_b16 v[236:237], v255 offset:7680\n\t"
"v_mfma_f32_16x16x32_bf16 a[12:15], a[248:251], a[184:187], a[12:15]\n\t"
"buffer_load_dwordx4 v247, s[12:15], s28 offen offset:256 nt lds\n\t"
"s_add_u32 m0, s24, 12544\n\t"
"s_add_u32 s28, s21, 393216\n\t"
"v_mfma_f32_16x16x32_bf16 a[20:23], a[248:251], a[188:191], a[20:23]\n\t"
"buffer_load_dwordx4 v247, s[12:15], s28 offen offset:256 nt lds\n\t"
"v_mfma_f32_16x16x32_bf16 a[20:23], a[252:255], a[192:195], a[20:23]\n\t"
"s_add_u32 m0, s24, 20224\n\t"
"s_add_u32 s28, s21, 589824\n\t"
"v_mfma_f32_16x16x32_bf16 a[20:23], v[96:99], a[196:199], a[20:23]\n\t"
"buffer_load_dwordx4 v247, s[12:15], s28 offen offset:256 nt lds\n\t"
"s_add_u32 m0, s24, 26368\n\t"
"s_add_u32 s28, s21, 786432\n\t"
"v_mfma_f32_16x16x32_bf16 a[28:31], v[96:99], a[200:203], a[28:31]\n\t"
"buffer_load_dwordx4 v247, s[12:15], s28 offen offset:256 nt lds\n\t"
"v_mfma_f32_16x16x32_bf16 a[28:31], a[252:255], a[196:199], a[28:31]\n\t"
"s_add_u32 m0, s24, 32512\n\t"
"s_add_u32 s28, s21, 983040\n\t"
"v_mfma_f32_16x16x32_bf16 a[28:31], a[248:251], a[192:195], a[28:31]\n\t"
"buffer_load_dwordx4 v247, s[12:15], s28 offen offset:256 nt lds\n\t"
"v_mfma_f32_16x16x32_bf16 a[36:39], a[248:251], a[196:199], a[36:39]\n\t"
"s_waitcnt lgkmcnt(3)\n\t"
"v_mfma_f32_16x16x32_bf16 a[36:39], a[252:255], a[200:203], a[36:39]\n\t"
"s_waitcnt lgkmcnt(2)\n\t"
"v_mfma_f32_16x16x32_bf16 a[36:39], v[96:99], a[204:207], a[36:39]\n\t"
"s_waitcnt lgkmcnt(1)\n\t"
"v_mfma_f32_4x4x4_16b_bf16 v[232:235], v[232:233], v[220:221], 0\n\t"
"s_waitcnt lgkmcnt(0)\n\t"
"v_mfma_f32_4x4x4_16b_bf16 v[228:231], v[228:229], v[220:221], 0\n\t"
"s_waitcnt vmcnt(17)\n\t"
"v_mfma_f32_4x4x4_16b_bf16 v[224:227], v[224:225], v[220:221], 0\n\t"
"v_mfma_f32_4x4x4_16b_bf16 v[236:239], v[236:237], v[220:221], 0\n\t"
"v_mfma_f32_16x16x32_bf16 a[44:47], a[212:215], a[168:171], a[44:47]\n\t"
"v_cvt_pk_bf16_f32 v232, v232, v233\n\t"
"v_cvt_pk_bf16_f32 v233, v234, v235\n\t"
"v_mfma_f32_16x16x32_bf16 a[44:47], a[216:219], a[176:179], a[44:47]\n\t"
"v_cvt_pk_bf16_f32 v234, v228, v229\n\t"
"v_cvt_pk_bf16_f32 v235, v230, v231\n\t"
"v_mfma_f32_16x16x32_bf16 a[44:47], v[248:251], a[172:175], a[44:47]\n\t"
"ds_write_b128 v245, v[232:235] offset:1536\n\t"
"v_mfma_f32_16x16x32_bf16 a[52:55], a[212:215], a[172:175], a[52:55]\n\t"
"v_cvt_pk_bf16_f32 v224, v224, v225\n\t"
"v_cvt_pk_bf16_f32 v225, v226, v227\n\t"
"v_mfma_f32_16x16x32_bf16 a[52:55], v[248:251], a[176:179], a[52:55]\n\t"
"v_cvt_pk_bf16_f32 v226, v236, v237\n\t"
"v_cvt_pk_bf16_f32 v227, v238, v239\n\t"
"v_mfma_f32_16x16x32_bf16 a[52:55], a[216:219], a[180:183], a[52:55]\n\t"
"ds_write_b128 v245, v[224:227] offset:7680\n\t"
"v_mfma_f32_16x16x32_bf16 a[60:63], a[216:219], a[184:187], a[60:63]\n\t"
"v_mfma_f32_16x16x32_bf16 a[60:63], v[248:251], a[180:183], a[60:63]\n\t"
"v_mfma_f32_16x16x32_bf16 a[60:63], a[212:215], a[176:179], a[60:63]\n\t"
"ds_read_b64_tr_b16 v[228:229], v246 offset:13824\n\t"
"v_mfma_f32_16x16x32_bf16 a[68:71], a[212:215], a[180:183], a[68:71]\n\t"
"ds_read_b64_tr_b16 v[236:237], v255 offset:13824\n\t"
"v_mfma_f32_16x16x32_bf16 a[68:71], v[248:251], a[184:187], a[68:71]\n\t"
"ds_read_b64_tr_b16 v[232:233], v246 offset:21504\n\t"
"v_mfma_f32_16x16x32_bf16 a[68:71], a[216:219], a[188:191], a[68:71]\n\t"
"ds_read_b64_tr_b16 v[224:225], v255 offset:21504\n\t"
"v_mfma_f32_16x16x32_bf16 a[76:79], a[216:219], a[192:195], a[76:79]\n\t"
"v_mfma_f32_16x16x32_bf16 a[76:79], v[248:251], a[188:191], a[76:79]\n\t"
"v_mfma_f32_16x16x32_bf16 a[76:79], a[212:215], a[184:187], a[76:79]\n\t"
"v_mfma_f32_16x16x32_bf16 a[84:87], a[212:215], a[188:191], a[84:87]\n\t"
"v_mfma_f32_16x16x32_bf16 a[84:87], v[248:251], a[192:195], a[84:87]\n\t"
"v_mfma_f32_16x16x32_bf16 a[84:87], a[216:219], a[196:199], a[84:87]\n\t"
"v_mfma_f32_16x16x32_bf16 a[92:95], a[216:219], a[200:203], a[92:95]\n\t"
"v_mfma_f32_16x16x32_bf16 a[92:95], v[248:251], a[196:199], a[92:95]\n\t"
"v_mfma_f32_16x16x32_bf16 a[92:95], a[212:215], a[192:195], a[92:95]\n\t"
"v_mfma_f32_16x16x32_bf16 a[100:103], a[212:215], a[196:199], a[100:103]\n\t"
"s_waitcnt lgkmcnt(3)\n\t"
"v_mfma_f32_16x16x32_bf16 a[100:103], v[248:251], a[200:203], a[100:103]\n\t"
"s_waitcnt lgkmcnt(2)\n\t"
"v_mfma_f32_16x16x32_bf16 a[100:103], a[216:219], a[204:207], a[100:103]\n\t"
"s_waitcnt lgkmcnt(1)\n\t"
"v_mfma_f32_4x4x4_16b_bf16 v[228:231], v[228:229], v[220:221], 0\n\t"
"s_waitcnt lgkmcnt(0)\n\t"
"v_mfma_f32_4x4x4_16b_bf16 v[236:239], v[236:237], v[220:221], 0\n\t"
"s_waitcnt vmcnt(14)\n\t"
"v_mfma_f32_4x4x4_16b_bf16 v[232:235], v[232:233], v[220:221], 0\n\t"
"s_mov_b32 s20, 651264\n\t"
"v_mfma_f32_4x4x4_16b_bf16 v[224:227], v[224:225], v[220:221], 0\n\t"
"v_mfma_f32_16x16x32_bf16 a[108:111], a[220:223], a[168:171], a[108:111]\n\t"
"ds_read_b128 a[168:171], v222 offset:2560\n\t"
"v_cvt_pk_bf16_f32 v228, v228, v229\n\t"
"v_mfma_f32_16x16x32_bf16 a[108:111], a[228:231], a[176:179], a[108:111]\n\t"
"v_cvt_pk_bf16_f32 v229, v230, v231\n\t"
"v_cvt_pk_bf16_f32 v230, v236, v237\n\t"
"v_mfma_f32_16x16x32_bf16 a[108:111], a[224:227], a[172:175], a[108:111]\n\t"
"v_cvt_pk_bf16_f32 v231, v238, v239\n\t"
"v_cvt_pk_bf16_f32 v232, v232, v233\n\t"
"v_mfma_f32_16x16x32_bf16 a[116:119], a[220:223], a[172:175], a[116:119]\n\t"
"ds_read_b128 a[172:175], v223 offset:8704\n\t"
"v_cvt_pk_bf16_f32 v233, v234, v235\n\t"
"v_mfma_f32_16x16x32_bf16 a[116:119], a[224:227], a[176:179], a[116:119]\n\t"
"v_cvt_pk_bf16_f32 v234, v224, v225\n\t"
"v_cvt_pk_bf16_f32 v235, v226, v227\n\t"
"v_mfma_f32_16x16x32_bf16 a[116:119], a[228:231], a[180:183], a[116:119]\n\t"
"ds_write_b128 v245, v[228:231] offset:13824\n\t"
"v_mfma_f32_16x16x32_bf16 a[124:127], a[228:231], a[184:187], a[124:127]\n\t"
"buffer_load_dwordx4 v[96:99], off, s[80:83], s20 offset:0 sc1\n\t"
"v_mfma_f32_16x16x32_bf16 a[124:127], a[224:227], a[180:183], a[124:127]\n\t"
"v_mfma_f32_16x16x32_bf16 a[124:127], a[220:223], a[176:179], a[124:127]\n\t"
"ds_write_b128 v245, v[232:235] offset:21504\n\t"
"v_mfma_f32_16x16x32_bf16 a[132:135], a[220:223], a[180:183], a[132:135]\n\t"
"v_mfma_f32_16x16x32_bf16 a[132:135], a[224:227], a[184:187], a[132:135]\n\t"
"ds_read_b128 a[176:179], v223 offset:14848\n\t"
"v_mfma_f32_16x16x32_bf16 a[132:135], a[228:231], a[188:191], a[132:135]\n\t"
"ds_read_b64_tr_b16 v[236:237], v246 offset:27648\n\t"
"v_mfma_f32_16x16x32_bf16 a[140:143], a[228:231], a[192:195], a[140:143]\n\t"
"ds_read_b64_tr_b16 v[224:225], v255 offset:27648\n\t"
"v_mfma_f32_16x16x32_bf16 a[140:143], a[224:227], a[188:191], a[140:143]\n\t"
"ds_read_b128 a[180:183], v222 offset:22528\n\t"
"v_mfma_f32_16x16x32_bf16 a[140:143], a[220:223], a[184:187], a[140:143]\n\t"
"ds_read_b64_tr_b16 v[228:229], v246 offset:33792\n\t"
"v_mfma_f32_16x16x32_bf16 a[148:151], a[220:223], a[188:191], a[148:151]\n\t"
"ds_read_b64_tr_b16 v[232:233], v255 offset:33792\n\t"
"v_mfma_f32_16x16x32_bf16 a[148:151], a[224:227], a[192:195], a[148:151]\n\t"
"ds_read_b128 a[184:187], v223 offset:28672\n\t"
"v_mfma_f32_16x16x32_bf16 a[148:151], a[228:231], a[196:199], a[148:151]\n\t"
"ds_read_b128 a[188:191], v223 offset:34816\n\t"
"v_mfma_f32_16x16x32_bf16 a[156:159], a[228:231], a[200:203], a[156:159]\n\t"
"v_mfma_f32_16x16x32_bf16 a[156:159], a[224:227], a[196:199], a[156:159]\n\t"
"v_mfma_f32_16x16x32_bf16 a[156:159], a[220:223], a[192:195], a[156:159]\n\t"
"ds_read_b128 a[192:195], v222 offset:42496\n\t"
"v_mfma_f32_16x16x32_bf16 a[164:167], a[220:223], a[196:199], a[164:167]\n\t"
"s_waitcnt lgkmcnt(7)\n\t"
"ds_read_b128 a[196:199], v223 offset:48640\n\t"
"v_mfma_f32_16x16x32_bf16 a[164:167], a[224:227], a[200:203], a[164:167]\n\t"
"s_waitcnt lgkmcnt(7)\n\t"
"ds_read_b128 a[200:203], v223 offset:54784\n\t"
"v_mfma_f32_16x16x32_bf16 a[164:167], a[228:231], a[204:207], a[164:167]\n\t"
"s_waitcnt lgkmcnt(6)\n\t"
"ds_read_b128 a[204:207], v222 offset:62464\n\t"
"v_mfma_f32_4x4x4_16b_bf16 v[236:239], v[236:237], v[220:221], 0\n\t"
"s_waitcnt lgkmcnt(6)\n\t"
"v_mfma_f32_4x4x4_16b_bf16 v[224:227], v[224:225], v[220:221], 0\n\t"
"s_waitcnt vmcnt(12)\n\t"
"v_mfma_f32_4x4x4_16b_bf16 v[228:231], v[228:229], v[220:221], 0\n\t"
"v_mfma_f32_4x4x4_16b_bf16 v[232:235], v[232:233], v[220:221], 0\n\t"
"Lst7_%=:\n\t"
"v_mfma_f32_16x16x32_bf16 v[0:3], a[232:235], a[168:171], v[0:3]\n\t"
"v_cvt_pk_bf16_f32 v236, v236, v237\n\t"
"v_cvt_pk_bf16_f32 v237, v238, v239\n\t"
"v_mfma_f32_16x16x32_bf16 v[0:3], v[240:243], a[176:179], v[0:3]\n\t"
"v_cvt_pk_bf16_f32 v238, v224, v225\n\t"
"v_cvt_pk_bf16_f32 v239, v226, v227\n\t"
"v_mfma_f32_16x16x32_bf16 v[0:3], a[208:211], a[172:175], v[0:3]\n\t"
"ds_write_b128 v245, v[236:239] offset:27648\n\t"
"v_mfma_f32_16x16x32_bf16 v[4:7], a[232:235], a[172:175], v[4:7]\n\t"
"s_waitcnt lgkmcnt(6)\n\t"
"v_cvt_pk_bf16_f32 v228, v228, v229\n\t"
"v_cvt_pk_bf16_f32 v229, v230, v231\n\t"
"v_mfma_f32_16x16x32_bf16 v[4:7], a[208:211], a[176:179], v[4:7]\n\t"
"v_cvt_pk_bf16_f32 v230, v232, v233\n\t"
"v_cvt_pk_bf16_f32 v231, v234, v235\n\t"
"v_mfma_f32_16x16x32_bf16 v[4:7], v[240:243], a[180:183], v[4:7]\n\t"
"ds_write_b128 v245, v[228:231] offset:33792\n\t"
"v_mfma_f32_16x16x32_bf16 v[8:11], v[240:243], a[184:187], v[8:11]\n\t"
"s_waitcnt lgkmcnt(6)\n\t"
"v_mfma_f32_16x16x32_bf16 v[8:11], a[208:211], a[180:183], v[8:11]\n\t"
"v_mfma_f32_16x16x32_bf16 v[8:11], a[232:235], a[176:179], v[8:11]\n\t"
"ds_read_b64_tr_b16 v[224:225], v246 offset:41472\n\t"
"v_mfma_f32_16x16x32_bf16 v[12:15], a[232:235], a[180:183], v[12:15]\n\t"
"s_waitcnt lgkmcnt(6)\n\t"
"ds_read_b64_tr_b16 v[232:233], v255 offset:41472\n\t"
"v_mfma_f32_16x16x32_bf16 v[12:15], a[208:211], a[184:187], v[12:15]\n\t"
"ds_read_b64_tr_b16 v[236:237], v246 offset:47616\n\t"
"v_mfma_f32_16x16x32_bf16 v[12:15], v[240:243], a[188:191], v[12:15]\n\t"
"buffer_load_dwordx4 v[248:251], off, s[80:83], s20 offset:1024 sc1\n\t"
"v_mfma_f32_16x16x32_bf16 v[16:19], v[240:243], a[192:195], v[16:19]\n\t"
"s_waitcnt lgkmcnt(7)\n\t"
"ds_read_b64_tr_b16 v[228:229], v255 offset:47616\n\t"
"v_mfma_f32_16x16x32_bf16 v[16:19], a[208:211], a[188:191], v[16:19]\n\t"
"v_mfma_f32_16x16x32_bf16 v[16:19], a[232:235], a[184:187], v[16:19]\n\t"
"buffer_load_dwordx4 a[212:215], off, s[80:83], s20 offset:2048 sc1\n\t"
"v_mfma_f32_16x16x32_bf16 v[20:23], a[232:235], a[188:191], v[20:23]\n\t"
"s_waitcnt lgkmcnt(7)\n\t"
"s_mov_b32 s20, 663552\n\t"
"v_mfma_f32_16x16x32_bf16 v[20:23], a[208:211], a[192:195], v[20:23]\n\t"
"v_mfma_f32_16x16x32_bf16 v[20:23], v[240:243], a[196:199], v[20:23]\n\t"
"v_mfma_f32_16x16x32_bf16 v[24:27], v[240:243], a[200:203], v[24:27]\n\t"
"buffer_load_dwordx4 a[216:219], off, s[80:83], s20 offset:0 sc1\n\t"
"v_mfma_f32_16x16x32_bf16 v[24:27], a[208:211], a[196:199], v[24:27]\n\t"
"s_waitcnt lgkmcnt(6)\n\t"
"v_mfma_f32_16x16x32_bf16 v[24:27], a[232:235], a[192:195], v[24:27]\n\t"
"v_mfma_f32_16x16x32_bf16 v[28:31], a[232:235], a[196:199], v[28:31]\n\t"
"s_waitcnt lgkmcnt(3)\n\t"
"v_mfma_f32_16x16x32_bf16 v[28:31], a[208:211], a[200:203], v[28:31]\n\t"
"buffer_load_dwordx4 a[208:211], off, s[80:83], s20 offset:1024 sc1\n\t"
"v_mfma_f32_16x16x32_bf16 v[28:31], v[240:243], a[204:207], v[28:31]\n\t"
"s_waitcnt lgkmcnt(1)\n\t"
"v_mfma_f32_4x4x4_16b_bf16 v[224:227], v[224:225], v[220:221], 0\n\t"
"s_waitcnt lgkmcnt(0)\n\t"
"v_mfma_f32_4x4x4_16b_bf16 v[232:235], v[232:233], v[220:221], 0\n\t"
"s_waitcnt vmcnt(13)\n\t"
"v_mfma_f32_4x4x4_16b_bf16 v[236:239], v[236:237], v[220:221], 0\n\t"
"v_mfma_f32_4x4x4_16b_bf16 v[228:231], v[228:229], v[220:221], 0\n\t"
"v_mfma_f32_16x16x32_bf16 v[32:35], a[236:239], a[168:171], v[32:35]\n\t"
"buffer_load_dwordx4 v[240:243], off, s[80:83], s20 offset:2048 sc1\n\t"
"v_mfma_f32_16x16x32_bf16 v[32:35], a[244:247], a[176:179], v[32:35]\n\t"
"s_mov_b32 s20, 675840\n\t"
"v_cvt_pk_bf16_f32 v224, v224, v225\n\t"
"v_cvt_pk_bf16_f32 v225, v226, v227\n\t"
"v_mfma_f32_16x16x32_bf16 v[32:35], a[240:243], a[172:175], v[32:35]\n\t"
"v_cvt_pk_bf16_f32 v226, v232, v233\n\t"
"v_cvt_pk_bf16_f32 v227, v234, v235\n\t"
"v_mfma_f32_16x16x32_bf16 v[36:39], a[236:239], a[172:175], v[36:39]\n\t"
"v_cvt_pk_bf16_f32 v236, v236, v237\n\t"
"v_cvt_pk_bf16_f32 v237, v238, v239\n\t"
"v_mfma_f32_16x16x32_bf16 v[36:39], a[240:243], a[176:179], v[36:39]\n\t"
"v_cvt_pk_bf16_f32 v238, v228, v229\n\t"
"v_cvt_pk_bf16_f32 v239, v230, v231\n\t"
"v_mfma_f32_16x16x32_bf16 v[36:39], a[244:247], a[180:183], v[36:39]\n\t"
"buffer_load_dwordx4 a[220:223], off, s[80:83], s20 offset:0 sc1\n\t"
"v_mfma_f32_16x16x32_bf16 v[40:43], a[244:247], a[184:187], v[40:43]\n\t"
"buffer_load_dwordx4 a[224:227], off, s[80:83], s20 offset:1024 sc1\n\t"
"v_mfma_f32_16x16x32_bf16 v[40:43], a[240:243], a[180:183], v[40:43]\n\t"
"v_mfma_f32_16x16x32_bf16 v[40:43], a[236:239], a[176:179], v[40:43]\n\t"
"ds_write_b128 v245, v[224:227] offset:41472\n\t"
"v_mfma_f32_16x16x32_bf16 v[44:47], a[236:239], a[180:183], v[44:47]\n\t"
"v_mfma_f32_16x16x32_bf16 v[44:47], a[240:243], a[184:187], v[44:47]\n\t"
"ds_read_b64_tr_b16 v[232:233], v246 offset:53760\n\t"
"v_mfma_f32_16x16x32_bf16 v[44:47], a[244:247], a[188:191], v[44:47]\n\t"
"ds_write_b128 v245, v[236:239] offset:47616\n\t"
"v_mfma_f32_16x16x32_bf16 v[48:51], a[244:247], a[192:195], v[48:51]\n\t"
"buffer_load_dwordx4 v[236:239], off, s[80:83], s20 offset:2048 sc1\n\t"
"v_mfma_f32_16x16x32_bf16 v[48:51], a[240:243], a[188:191], v[48:51]\n\t"
"s_mov_b32 s20, 688128\n\t"
"ds_read_b64_tr_b16 v[228:229], v255 offset:53760\n\t"
"v_mfma_f32_16x16x32_bf16 v[48:51], a[236:239], a[184:187], v[48:51]\n\t"
"buffer_load_dwordx4 a[228:231], off, s[80:83], s20 offset:0 sc1\n\t"
"v_mfma_f32_16x16x32_bf16 v[52:55], a[236:239], a[188:191], v[52:55]\n\t"
"v_mfma_f32_16x16x32_bf16 v[52:55], a[240:243], a[192:195], v[52:55]\n\t"
"v_mfma_f32_16x16x32_bf16 v[52:55], a[244:247], a[196:199], v[52:55]\n\t"
"v_mfma_f32_16x16x32_bf16 v[56:59], a[244:247], a[200:203], v[56:59]\n\t"
"buffer_load_dwordx4 a[232:235], off, s[80:83], s20 offset:1024 sc1\n\t"
"v_mfma_f32_16x16x32_bf16 v[56:59], a[240:243], a[196:199], v[56:59]\n\t"
"v_mfma_f32_16x16x32_bf16 v[56:59], a[236:239], a[192:195], v[56:59]\n\t"
"v_mfma_f32_16x16x32_bf16 v[60:63], a[236:239], a[196:199], v[60:63]\n\t"
"s_waitcnt lgkmcnt(2)\n\t"
"v_mfma_f32_16x16x32_bf16 v[60:63], a[240:243], a[200:203], v[60:63]\n\t"
"buffer_load_dwordx4 a[236:239], off, s[80:83], s20 offset:2048 sc1\n\t"
"v_mfma_f32_16x16x32_bf16 v[60:63], a[244:247], a[204:207], v[60:63]\n\t"
"s_waitcnt vmcnt(9) lgkmcnt(0)\n\t"
"s_mov_b32 s20, 921600\n\t"
"v_mfma_f32_4x4x4_16b_bf16 v[232:235], v[232:233], v[220:221], 0\n\t"
"v_mfma_f32_4x4x4_16b_bf16 v[228:231], v[228:229], v[220:221], 0\n\t"
"v_mfma_f32_16x16x32_bf16 v[64:67], v[96:99], a[168:171], v[64:67]\n\t"
"buffer_load_dwordx4 a[240:243], off, s[80:83], s20 offset:0 sc1\n\t"
"v_mfma_f32_16x16x32_bf16 v[64:67], a[212:215], a[176:179], v[64:67]\n\t"
"ds_read_b128 a[168:171], v222 offset:2816\n\t"
"v_cvt_pk_bf16_f32 v232, v232, v233\n\t"
"v_mfma_f32_16x16x32_bf16 v[64:67], v[248:251], a[172:175], v[64:67]\n\t"
"v_cvt_pk_bf16_f32 v233, v234, v235\n\t"
"v_cvt_pk_bf16_f32 v234, v228, v229\n\t"
"v_mfma_f32_16x16x32_bf16 v[68:71], v[96:99], a[172:175], v[68:71]\n\t"
"ds_read_b128 a[172:175], v223 offset:8960\n\t"
"v_cvt_pk_bf16_f32 v235, v230, v231\n\t"
"v_mfma_f32_16x16x32_bf16 v[68:71], v[248:251], a[176:179], v[68:71]\n\t"
"v_mfma_f32_16x16x32_bf16 v[68:71], a[212:215], a[180:183], v[68:71]\n\t"
"buffer_load_dwordx4 a[244:247], off, s[80:83], s20 offset:1024 sc1\n\t"
"v_mfma_f32_16x16x32_bf16 v[72:75], a[212:215], a[184:187], v[72:75]\n\t"
"buffer_load_dwordx4 a[248:251], off, s[80:83], s20 offset:2048 sc1\n\t"
"v_mfma_f32_16x16x32_bf16 v[72:75], v[248:251], a[180:183], v[72:75]\n\t"
"s_mov_b32 s20, 933888\n\t"
"v_mfma_f32_16x16x32_bf16 v[72:75], v[96:99], a[176:179], v[72:75]\n\t"
"ds_write_b128 v245, v[232:235] offset:53760\n\t"
"v_mfma_f32_16x16x32_bf16 v[76:79], v[96:99], a[180:183], v[76:79]\n\t"
"buffer_load_dwordx4 a[252:255], off, s[80:83], s20 offset:0 sc1\n\t"
"v_mfma_f32_16x16x32_bf16 v[76:79], v[248:251], a[184:187], v[76:79]\n\t"
"ds_read_b128 a[176:179], v223 offset:15104\n\t"
"v_mfma_f32_16x16x32_bf16 v[76:79], a[212:215], a[188:191], v[76:79]\n\t"
"ds_read_b128 a[180:183], v222 offset:22784\n\t"
"v_mfma_f32_16x16x32_bf16 v[80:83], a[212:215], a[192:195], v[80:83]\n\t"
"v_mfma_f32_16x16x32_bf16 v[80:83], v[248:251], a[188:191], v[80:83]\n\t"
"v_mfma_f32_16x16x32_bf16 v[80:83], v[96:99], a[184:187], v[80:83]\n\t"
"ds_read_b128 a[184:187], v223 offset:28928\n\t"
"v_mfma_f32_16x16x32_bf16 v[84:87], v[96:99], a[188:191], v[84:87]\n\t"
"s_waitcnt vmcnt(10) lgkmcnt(2)\n\t"
"ds_read_b128 a[188:191], v223 offset:35072\n\t"
"v_mfma_f32_16x16x32_bf16 v[84:87], v[248:251], a[192:195], v[84:87]\n\t"
"v_mfma_f32_16x16x32_bf16 v[84:87], a[212:215], a[196:199], v[84:87]\n\t"
"s_waitcnt lgkmcnt(2)\n\t"
"v_mfma_f32_16x16x32_bf16 v[88:91], a[212:215], a[200:203], v[88:91]\n\t"
"Lpg6_%=:\n\t"
"s_cbranch_vccnz Lpg10_%=\n\t"
"v_mfma_f32_16x16x32_bf16 v[88:91], v[248:251], a[196:199], v[88:91]\n\t"
"v_mfma_f32_16x16x32_bf16 v[88:91], v[96:99], a[192:195], v[88:91]\n\t"
"ds_read_b128 a[192:195], v222 offset:42752\n\t"
"v_mfma_f32_16x16x32_bf16 v[92:95], v[96:99], a[196:199], v[92:95]\n\t"
"buffer_load_dwordx4 v[96:99], off, s[80:83], s20 offset:1024 sc1\n\t"
"v_mfma_f32_16x16x32_bf16 v[92:95], v[248:251], a[200:203], v[92:95]\n\t"
"ds_read_b128 a[196:199], v223 offset:48896\n\t"
"v_mfma_f32_16x16x32_bf16 v[92:95], a[212:215], a[204:207], v[92:95]\n\t"
"ds_read_b128 a[200:203], v223 offset:55040\n\t"
"v_mfma_f32_16x16x32_bf16 v[100:103], a[216:219], a[168:171], v[100:103]\n\t"
"ds_read_b128 a[204:207], v222 offset:62720\n\t"
"v_mfma_f32_16x16x32_bf16 v[100:103], v[240:243], a[176:179], v[100:103]\n\t"
"buffer_load_dwordx4 v[248:251], off, s[80:83], s20 offset:2048 sc1\n\t"
"v_mfma_f32_16x16x32_bf16 v[100:103], a[208:211], a[172:175], v[100:103]\n\t"
"s_mov_b32 s20, 946176\n\t"
"s_add_u32 m0, s24, 1792\n\t"
"s_add_u32 s28, s21, 3072\n\t"
"v_mfma_f32_16x16x32_bf16 v[104:107], a[216:219], a[172:175], v[104:107]\n\t"
"buffer_load_dwordx4 a[212:215], off, s[80:83], s20 offset:0 sc1\n\t"
"v_mfma_f32_16x16x32_bf16 v[104:107], a[208:211], a[176:179], v[104:107]\n\t"
"s_waitcnt lgkmcnt(5)\n\t"
"v_mfma_f32_16x16x32_bf16 v[104:107], v[240:243], a[180:183], v[104:107]\n\t"
"s_waitcnt lgkmcnt(0)\n\t"
"s_barrier\n\t"
"v_mfma_f32_16x16x32_bf16 v[108:111], v[240:243], a[184:187], v[108:111]\n\t"
"v_mfma_f32_16x16x32_bf16 v[108:111], a[208:211], a[180:183], v[108:111]\n\t"
"buffer_load_dwordx4 v247, s[12:15], s28 offen offset:256 nt lds\n\t"
"v_mfma_f32_16x16x32_bf16 v[108:111], a[216:219], a[176:179], v[108:111]\n\t"
"s_add_u32 m0, s24, 7936\n\t"
"s_add_u32 s28, s21, 199680\n\t"
"v_mfma_f32_16x16x32_bf16 v[112:115], a[216:219], a[180:183], v[112:115]\n\t"
"buffer_load_dwordx4 v247, s[12:15], s28 offen offset:256 nt lds\n\t"
"v_mfma_f32_16x16x32_bf16 v[112:115], a[208:211], a[184:187], v[112:115]\n\t"
"s_add_u32 m0, s24, 14080\n\t"
"s_add_u32 s28, s21, 396288\n\t"
"v_mfma_f32_16x16x32_bf16 v[112:115], v[240:243], a[188:191], v[112:115]\n\t"
"buffer_load_dwordx4 v247, s[12:15], s28 offen offset:256 nt lds\n\t"
"s_add_u32 m0, s24, 21760\n\t"
"s_add_u32 s28, s21, 592896\n\t"
"v_mfma_f32_16x16x32_bf16 v[116:119], v[240:243], a[192:195], v[116:119]\n\t"
"buffer_load_dwordx4 v247, s[12:15], s28 offen offset:256 nt lds\n\t"
"v_mfma_f32_16x16x32_bf16 v[116:119], a[208:211], a[188:191], v[116:119]\n\t"
"s_add_u32 m0, s24, 27904\n\t"
"s_add_u32 s28, s21, 789504\n\t"
"v_mfma_f32_16x16x32_bf16 v[116:119], a[216:219], a[184:187], v[116:119]\n\t"
"buffer_load_dwordx4 v247, s[12:15], s28 offen offset:256 nt lds\n\t"
"v_mfma_f32_16x16x32_bf16 v[120:123], a[216:219], a[188:191], v[120:123]\n\t"
"s_add_u32 m0, s24, 34048\n\t"
"s_add_u32 s28, s21, 986112\n\t"
"v_mfma_f32_16x16x32_bf16 v[120:123], a[208:211], a[192:195], v[120:123]\n\t"
"buffer_load_dwordx4 v247, s[12:15], s28 offen offset:256 nt lds\n\t"
"v_mfma_f32_16x16x32_bf16 v[120:123], v[240:243], a[196:199], v[120:123]\n\t"
"v_mfma_f32_16x16x32_bf16 v[124:127], v[240:243], a[200:203], v[124:127]\n\t"
"v_mfma_f32_16x16x32_bf16 v[124:127], a[208:211], a[196:199], v[124:127]\n\t"
"v_mfma_f32_16x16x32_bf16 v[124:127], a[216:219], a[192:195], v[124:127]\n\t"
"v_mfma_f32_16x16x32_bf16 v[128:131], a[216:219], a[196:199], v[128:131]\n\t"
"s_waitcnt vmcnt(16)\n\t"
"v_mfma_f32_16x16x32_bf16 v[128:131], a[208:211], a[200:203], v[128:131]\n\t"
"v_mfma_f32_16x16x32_bf16 v[128:131], v[240:243], a[204:207], v[128:131]\n\t"
"v_mfma_f32_16x16x32_bf16 v[132:135], a[220:223], a[168:171], v[132:135]\n\t"
"v_mfma_f32_16x16x32_bf16 v[132:135], v[236:239], a[176:179], v[132:135]\n\t"
"v_mfma_f32_16x16x32_bf16 v[132:135], a[224:227], a[172:175], v[132:135]\n\t"
"v_mfma_f32_16x16x32_bf16 v[136:139], a[220:223], a[172:175], v[136:139]\n\t"
"v_mfma_f32_16x16x32_bf16 v[136:139], a[224:227], a[176:179], v[136:139]\n\t"
"v_mfma_f32_16x16x32_bf16 v[136:139], v[236:239], a[180:183], v[136:139]\n\t"
"v_mfma_f32_16x16x32_bf16 v[140:143], v[236:239], a[184:187], v[140:143]\n\t"
"v_mfma_f32_16x16x32_bf16 v[140:143], a[224:227], a[180:183], v[140:143]\n\t"
"v_mfma_f32_16x16x32_bf16 v[140:143], a[220:223], a[176:179], v[140:143]\n\t"
"v_mfma_f32_16x16x32_bf16 v[144:147], a[220:223], a[180:183], v[144:147]\n\t"
"v_mfma_f32_16x16x32_bf16 v[144:147], a[224:227], a[184:187], v[144:147]\n\t"
"v_mfma_f32_16x16x32_bf16 v[144:147], v[236:239], a[188:191], v[144:147]\n\t"
"v_mfma_f32_16x16x32_bf16 v[148:151], v[236:239], a[192:195], v[148:151]\n\t"
"v_mfma_f32_16x16x32_bf16 v[148:151], a[224:227], a[188:191], v[148:151]\n\t"
"v_mfma_f32_16x16x32_bf16 v[148:151], a[220:223], a[184:187], v[148:151]\n\t"
"v_mfma_f32_16x16x32_bf16 v[152:155], a[220:223], a[188:191], v[152:155]\n\t"
"v_mfma_f32_16x16x32_bf16 v[152:155], a[224:227], a[192:195], v[152:155]\n\t"
"v_mfma_f32_16x16x32_bf16 v[152:155], v[236:239], a[196:199], v[152:155]\n\t"
"v_mfma_f32_16x16x32_bf16 v[156:159], v[236:239], a[200:203], v[156:159]\n\t"
"v_mfma_f32_16x16x32_bf16 v[156:159], a[224:227], a[196:199], v[156:159]\n\t"
"v_mfma_f32_16x16x32_bf16 v[156:159], a[220:223], a[192:195], v[156:159]\n\t"
"v_mfma_f32_16x16x32_bf16 v[160:163], a[220:223], a[196:199], v[160:163]\n\t"
"s_waitcnt vmcnt(13)\n\t"
"v_mfma_f32_16x16x32_bf16 v[160:163], a[224:227], a[200:203], v[160:163]\n\t"
"v_mfma_f32_16x16x32_bf16 v[160:163], v[236:239], a[204:207], v[160:163]\n\t"
"v_mfma_f32_16x16x32_bf16 v[164:167], a[228:231], a[168:171], v[164:167]\n\t"
"ds_read_b128 a[168:171], v222 offset:3072\n\t"
"v_mfma_f32_16x16x32_bf16 v[164:167], a[236:239], a[176:179], v[164:167]\n\t"
"v_mfma_f32_16x16x32_bf16 v[164:167], a[232:235], a[172:175], v[164:167]\n\t"
"buffer_load_dwordx4 v[240:243], off, s[80:83], s20 offset:1024 sc1\n\t"
"v_mfma_f32_16x16x32_bf16 v[168:171], a[228:231], a[172:175], v[168:171]\n\t"
"ds_read_b128 a[172:175], v223 offset:9216\n\t"
"v_mfma_f32_16x16x32_bf16 v[168:171], a[232:235], a[176:179], v[168:171]\n\t"
"v_mfma_f32_16x16x32_bf16 v[168:171], a[236:239], a[180:183], v[168:171]\n\t"
"v_mfma_f32_16x16x32_bf16 v[172:175], a[236:239], a[184:187], v[172:175]\n\t"
"v_mfma_f32_16x16x32_bf16 v[172:175], a[232:235], a[180:183], v[172:175]\n\t"
"v_mfma_f32_16x16x32_bf16 v[172:175], a[228:231], a[176:179], v[172:175]\n\t"
"ds_read_b128 a[176:179], v223 offset:15360\n\t"
"v_mfma_f32_16x16x32_bf16 v[176:179], a[228:231], a[180:183], v[176:179]\n\t"
"ds_read_b128 a[180:183], v222 offset:23040\n\t"
"v_mfma_f32_16x16x32_bf16 v[176:179], a[232:235], a[184:187], v[176:179]\n\t"
"v_mfma_f32_16x16x32_bf16 v[176:179], a[236:239], a[188:191], v[176:179]\n\t"
"v_mfma_f32_16x16x32_bf16 v[180:183], a[236:239], a[192:195], v[180:183]\n\t"
"v_mfma_f32_16x16x32_bf16 v[180:183], a[232:235], a[188:191], v[180:183]\n\t"
"v_mfma_f32_16x16x32_bf16 v[180:183], a[228:231], a[184:187], v[180:183]\n\t"
"ds_read_b128 a[184:187], v223 offset:29184\n\t"
"v_mfma_f32_16x16x32_bf16 v[184:187], a[228:231], a[188:191], v[184:187]\n\t"
"ds_read_b128 a[188:191], v223 offset:35328\n\t"
"v_mfma_f32_16x16x32_bf16 v[184:187], a[232:235], a[192:195], v[184:187]\n\t"
"v_mfma_f32_16x16x32_bf16 v[184:187], a[236:239], a[196:199], v[184:187]\n\t"
"v_mfma_f32_16x16x32_bf16 v[188:191], a[236:239], a[200:203], v[188:191]\n\t"
"v_mfma_f32_16x16x32_bf16 v[188:191], a[232:235], a[196:199], v[188:191]\n\t"
"v_mfma_f32_16x16x32_bf16 v[188:191], a[228:231], a[192:195], v[188:191]\n\t"
"ds_read_b128 a[192:195], v222 offset:43008\n\t"
"v_mfma_f32_16x16x32_bf16 v[192:195], a[228:231], a[196:199], v[192:195]\n\t"
"s_waitcnt vmcnt(11) lgkmcnt(4)\n\t"
"ds_read_b128 a[196:199], v223 offset:49152\n\t"
"v_mfma_f32_16x16x32_bf16 v[192:195], a[232:235], a[200:203], v[192:195]\n\t"
"ds_read_b128 a[200:203], v223 offset:55296\n\t"
"v_mfma_f32_16x16x32_bf16 v[192:195], a[236:239], a[204:207], v[192:195]\n\t"
"ds_read_b128 a[204:207], v222 offset:62976\n\t"
"Lst8_%=:\n\t"
"v_mfma_f32_16x16x32_bf16 v[0:3], a[240:243], a[168:171], v[0:3]\n\t"
"s_mov_b64 vcc, -1\n\t"
"s_bitcmp1_b32 s73, 1\n\t"
"s_cbranch_scc1 Lvh1_%=\n\t"
"s_bitcmp1_b32 s73, 0\n\t"
"s_cbranch_scc1 Lpg14_%=\n\t"
"s_branch Lpg13_%=\n\t"
"Lvh1_%=:\n\t"
"s_bitcmp1_b32 s73, 0\n\t"
"s_cbranch_scc1 Lpg16_%=\n\t"
"s_branch Lpg15_%=\n\t"
"Lvr1_%=:\n\t"
"s_mov_b64 vcc, 0\n\t"
"s_waitcnt lgkmcnt(6)\n\t"
"v_mfma_f32_16x16x32_bf16 v[0:3], a[248:251], a[176:179], v[0:3]\n\t"
"v_mfma_f32_16x16x32_bf16 v[0:3], a[244:247], a[172:175], v[0:3]\n\t"
"v_mfma_f32_16x16x32_bf16 v[4:7], a[240:243], a[172:175], v[4:7]\n\t"
"s_waitcnt lgkmcnt(5)\n\t"
"v_mfma_f32_16x16x32_bf16 v[4:7], a[244:247], a[176:179], v[4:7]\n\t"
"v_mfma_f32_16x16x32_bf16 v[4:7], a[248:251], a[180:183], v[4:7]\n\t"
"v_mfma_f32_16x16x32_bf16 v[8:11], a[248:251], a[184:187], v[8:11]\n\t"
"s_waitcnt lgkmcnt(4)\n\t"
"v_mfma_f32_16x16x32_bf16 v[8:11], a[244:247], a[180:183], v[8:11]\n\t"
"v_mfma_f32_16x16x32_bf16 v[8:11], a[240:243], a[176:179], v[8:11]\n\t"
"buffer_load_dwordx4 a[208:211], off, s[80:83], s20 offset:2048 sc1\n\t"
"v_mfma_f32_16x16x32_bf16 v[12:15], a[240:243], a[180:183], v[12:15]\n\t"
"s_waitcnt lgkmcnt(3)\n\t"
"s_mov_b32 s20, 958464\n\t"
"v_mfma_f32_16x16x32_bf16 v[12:15], a[244:247], a[184:187], v[12:15]\n\t"
"v_mfma_f32_16x16x32_bf16 v[12:15], a[248:251], a[188:191], v[12:15]\n\t"
"v_mfma_f32_16x16x32_bf16 v[16:19], a[248:251], a[192:195], v[16:19]\n\t"
"buffer_load_dwordx4 v[236:239], off, s[80:83], s20 offset:0 sc1\n\t"
"v_mfma_f32_16x16x32_bf16 v[16:19], a[244:247], a[188:191], v[16:19]\n\t"
"s_waitcnt lgkmcnt(2)\n\t"
"v_mfma_f32_16x16x32_bf16 v[16:19], a[240:243], a[184:187], v[16:19]\n\t"
"v_mfma_f32_16x16x32_bf16 v[20:23], a[240:243], a[188:191], v[20:23]\n\t"
"s_waitcnt lgkmcnt(1)\n\t"
"v_mfma_f32_16x16x32_bf16 v[20:23], a[244:247], a[192:195], v[20:23]\n\t"
"buffer_load_dwordx4 a[216:219], off, s[80:83], s20 offset:1024 sc1\n\t"
"v_mfma_f32_16x16x32_bf16 v[20:23], a[248:251], a[196:199], v[20:23]\n\t"
"v_mfma_f32_16x16x32_bf16 v[24:27], a[248:251], a[200:203], v[24:27]\n\t"
"s_waitcnt lgkmcnt(0)\n\t"
"v_mfma_f32_16x16x32_bf16 v[24:27], a[244:247], a[196:199], v[24:27]\n\t"
"v_mfma_f32_16x16x32_bf16 v[24:27], a[240:243], a[192:195], v[24:27]\n\t"
"buffer_load_dwordx4 a[220:223], off, s[80:83], s20 offset:2048 sc1\n\t"
"v_mfma_f32_16x16x32_bf16 v[28:31], a[240:243], a[196:199], v[28:31]\n\t"
"s_waitcnt vmcnt(12)\n\t"
"s_mov_b32 s20, 970752\n\t"
"v_mfma_f32_16x16x32_bf16 v[28:31], a[244:247], a[200:203], v[28:31]\n\t"
"v_mfma_f32_16x16x32_bf16 v[28:31], a[248:251], a[204:207], v[28:31]\n\t"
"v_mfma_f32_16x16x32_bf16 v[32:35], a[252:255], a[168:171], v[32:35]\n\t"
"buffer_load_dwordx4 a[224:227], off, s[80:83], s20 offset:0 sc1\n\t"
"v_mfma_f32_16x16x32_bf16 v[32:35], v[248:251], a[176:179], v[32:35]\n\t"
"v_mfma_f32_16x16x32_bf16 v[32:35], v[96:99], a[172:175], v[32:35]\n\t"
"v_mfma_f32_16x16x32_bf16 v[36:39], a[252:255], a[172:175], v[36:39]\n\t"
"v_mfma_f32_16x16x32_bf16 v[36:39], v[96:99], a[176:179], v[36:39]\n\t"
"buffer_load_dwordx4 a[228:231], off, s[80:83], s20 offset:1024 sc1\n\t"
"v_mfma_f32_16x16x32_bf16 v[36:39], v[248:251], a[180:183], v[36:39]\n\t"
"v_mfma_f32_16x16x32_bf16 v[40:43], v[248:251], a[184:187], v[40:43]\n\t"
"v_mfma_f32_16x16x32_bf16 v[40:43], v[96:99], a[180:183], v[40:43]\n\t"
"v_mfma_f32_16x16x32_bf16 v[40:43], a[252:255], a[176:179], v[40:43]\n\t"
"buffer_load_dwordx4 a[232:235], off, s[80:83], s20 offset:2048 sc1\n\t"
"v_mfma_f32_16x16x32_bf16 v[44:47], a[252:255], a[180:183], v[44:47]\n\t"
"s_mov_b32 s20, 983040\n\t"
"v_mfma_f32_16x16x32_bf16 v[44:47], v[96:99], a[184:187], v[44:47]\n\t"
"v_mfma_f32_16x16x32_bf16 v[44:47], v[248:251], a[188:191], v[44:47]\n\t"
"v_mfma_f32_16x16x32_bf16 v[48:51], v[248:251], a[192:195], v[48:51]\n\t"
"buffer_load_dwordx4 a[236:239], off, s[80:83], s20 offset:0 sc1\n\t"
"v_mfma_f32_16x16x32_bf16 v[48:51], v[96:99], a[188:191], v[48:51]\n\t"
"v_mfma_f32_16x16x32_bf16 v[48:51], a[252:255], a[184:187], v[48:51]\n\t"
"v_mfma_f32_16x16x32_bf16 v[52:55], a[252:255], a[188:191], v[52:55]\n\t"
"v_mfma_f32_16x16x32_bf16 v[52:55], v[96:99], a[192:195], v[52:55]\n\t"
"buffer_load_dwordx4 a[240:243], off, s[80:83], s20 offset:1024 sc1\n\t"
"v_mfma_f32_16x16x32_bf16 v[52:55], v[248:251], a[196:199], v[52:55]\n\t"
"v_mfma_f32_16x16x32_bf16 v[56:59], v[248:251], a[200:203], v[56:59]\n\t"
"v_mfma_f32_16x16x32_bf16 v[56:59], v[96:99], a[196:199], v[56:59]\n\t"
"v_mfma_f32_16x16x32_bf16 v[56:59], a[252:255], a[192:195], v[56:59]\n\t"
"buffer_load_dwordx4 a[244:247], off, s[80:83], s20 offset:2048 sc1\n\t"
"v_mfma_f32_16x16x32_bf16 v[60:63], a[252:255], a[196:199], v[60:63]\n\t"
"s_waitcnt vmcnt(9)\n\t"
"s_mov_b32 s20, 995328\n\t"
"v_mfma_f32_16x16x32_bf16 v[60:63], v[96:99], a[200:203], v[60:63]\n\t"
"v_mfma_f32_16x16x32_bf16 v[60:63], v[248:251], a[204:207], v[60:63]\n\t"
"v_mfma_f32_16x16x32_bf16 v[64:67], a[212:215], a[168:171], v[64:67]\n\t"
"buffer_load_dwordx4 v[96:99], off, s[80:83], s20 offset:0 sc1\n\t"
"v_mfma_f32_16x16x32_bf16 v[64:67], a[208:211], a[176:179], v[64:67]\n\t"
"ds_read_b128 a[168:171], v222 offset:3328\n\t"
"v_mfma_f32_16x16x32_bf16 v[64:67], v[240:243], a[172:175], v[64:67]\n\t"
"v_mfma_f32_16x16x32_bf16 v[68:71], a[212:215], a[172:175], v[68:71]\n\t"
"buffer_load_dwordx4 v[248:251], off, s[80:83], s20 offset:1024 sc1\n\t"
"v_mfma_f32_16x16x32_bf16 v[68:71], v[240:243], a[176:179], v[68:71]\n\t"
"ds_read_b128 a[172:175], v223 offset:9472\n\t"
"v_mfma_f32_16x16x32_bf16 v[68:71], a[208:211], a[180:183], v[68:71]\n\t"
"v_mfma_f32_16x16x32_bf16 v[72:75], a[208:211], a[184:187], v[72:75]\n\t"
"buffer_load_dwordx4 a[248:251], off, s[80:83], s20 offset:2048 sc1\n\t"
"v_mfma_f32_16x16x32_bf16 v[72:75], v[240:243], a[180:183], v[72:75]\n\t"
"s_mov_b32 s20, 1007616\n\t"
"v_mfma_f32_16x16x32_bf16 v[72:75], a[212:215], a[176:179], v[72:75]\n\t"
"ds_read_b128 a[176:179], v223 offset:15616\n\t"
"v_mfma_f32_16x16x32_bf16 v[76:79], a[212:215], a[180:183], v[76:79]\n\t"
"buffer_load_dwordx4 a[252:255], off, s[80:83], s20 offset:0 sc1\n\t"
"v_mfma_f32_16x16x32_bf16 v[76:79], v[240:243], a[184:187], v[76:79]\n\t"
"ds_read_b128 a[180:183], v222 offset:23296\n\t"
"v_mfma_f32_16x16x32_bf16 v[76:79], a[208:211], a[188:191], v[76:79]\n\t"
"v_mfma_f32_16x16x32_bf16 v[80:83], a[208:211], a[192:195], v[80:83]\n\t"
"v_mfma_f32_16x16x32_bf16 v[80:83], v[240:243], a[188:191], v[80:83]\n\t"
"v_mfma_f32_16x16x32_bf16 v[80:83], a[212:215], a[184:187], v[80:83]\n\t"
"ds_read_b128 a[184:187], v223 offset:29440\n\t"
"v_mfma_f32_16x16x32_bf16 v[84:87], a[212:215], a[188:191], v[84:87]\n\t"
"ds_read_b128 a[188:191], v223 offset:35584\n\t"
"v_mfma_f32_16x16x32_bf16 v[84:87], v[240:243], a[192:195], v[84:87]\n\t"
"v_mfma_f32_16x16x32_bf16 v[84:87], a[208:211], a[196:199], v[84:87]\n\t"
"v_mfma_f32_16x16x32_bf16 v[88:91], a[208:211], a[200:203], v[88:91]\n\t"
"v_mfma_f32_16x16x32_bf16 v[88:91], v[240:243], a[196:199], v[88:91]\n\t"
"v_mfma_f32_16x16x32_bf16 v[88:91], a[212:215], a[192:195], v[88:91]\n\t"
"ds_read_b128 a[192:195], v222 offset:43264\n\t"
"v_mfma_f32_16x16x32_bf16 v[92:95], a[212:215], a[196:199], v[92:95]\n\t"
"buffer_load_dwordx4 a[212:215], off, s[80:83], s20 offset:1024 sc1\n\t"
"v_mfma_f32_16x16x32_bf16 v[92:95], v[240:243], a[200:203], v[92:95]\n\t"
"s_waitcnt vmcnt(11) lgkmcnt(4)\n\t"
"ds_read_b128 a[196:199], v223 offset:49408\n\t"
"v_mfma_f32_16x16x32_bf16 v[92:95], a[208:211], a[204:207], v[92:95]\n\t"
"global_load_dwordx4 v[240:243], v252, s[48:49] offset:2048\n\t"
"v_mfma_f32_16x16x32_bf16 v[100:103], v[236:239], a[168:171], v[100:103]\n\t"
"s_waitcnt lgkmcnt(4)\n\t"
"ds_read_b128 a[200:203], v223 offset:55552\n\t"
"v_mfma_f32_16x16x32_bf16 v[100:103], a[220:223], a[176:179], v[100:103]\n\t"
"ds_read_b128 a[204:207], v222 offset:63232\n\t"
"v_mfma_f32_16x16x32_bf16 v[100:103], a[216:219], a[172:175], v[100:103]\n\t"
"v_mfma_f32_16x16x32_bf16 v[104:107], v[236:239], a[172:175], v[104:107]\n\t"
"s_waitcnt lgkmcnt(5)\n\t"
"v_mfma_f32_16x16x32_bf16 v[104:107], a[216:219], a[176:179], v[104:107]\n\t"
"global_load_dwordx4 a[208:211], v252, s[46:47] offset:1024\n\t"
"v_mfma_f32_16x16x32_bf16 v[104:107], a[220:223], a[180:183], v[104:107]\n\t"
"v_mfma_f32_16x16x32_bf16 v[108:111], a[220:223], a[184:187], v[108:111]\n\t"
"s_waitcnt lgkmcnt(4)\n\t"
"v_mfma_f32_16x16x32_bf16 v[108:111], a[216:219], a[180:183], v[108:111]\n\t"
"v_mfma_f32_16x16x32_bf16 v[108:111], v[236:239], a[176:179], v[108:111]\n\t"
"v_mfma_f32_16x16x32_bf16 v[112:115], v[236:239], a[180:183], v[112:115]\n\t"
"s_waitcnt lgkmcnt(3)\n\t"
"v_mfma_f32_16x16x32_bf16 v[112:115], a[216:219], a[184:187], v[112:115]\n\t"
"v_mfma_f32_16x16x32_bf16 v[112:115], a[220:223], a[188:191], v[112:115]\n\t"
"v_mfma_f32_16x16x32_bf16 v[116:119], a[220:223], a[192:195], v[116:119]\n\t"
"s_waitcnt lgkmcnt(2)\n\t"
"v_mfma_f32_16x16x32_bf16 v[116:119], a[216:219], a[188:191], v[116:119]\n\t"
"v_mfma_f32_16x16x32_bf16 v[116:119], v[236:239], a[184:187], v[116:119]\n\t"
"v_mfma_f32_16x16x32_bf16 v[120:123], v[236:239], a[188:191], v[120:123]\n\t"
"s_waitcnt lgkmcnt(1)\n\t"
"v_mfma_f32_16x16x32_bf16 v[120:123], a[216:219], a[192:195], v[120:123]\n\t"
"v_mfma_f32_16x16x32_bf16 v[120:123], a[220:223], a[196:199], v[120:123]\n\t"
"v_mfma_f32_16x16x32_bf16 v[124:127], a[220:223], a[200:203], v[124:127]\n\t"
"s_waitcnt lgkmcnt(0)\n\t"
"v_mfma_f32_16x16x32_bf16 v[124:127], a[216:219], a[196:199], v[124:127]\n\t"
"v_mfma_f32_16x16x32_bf16 v[124:127], v[236:239], a[192:195], v[124:127]\n\t"
"v_mfma_f32_16x16x32_bf16 v[128:131], v[236:239], a[196:199], v[128:131]\n\t"
"s_waitcnt vmcnt(10)\n\t"
"v_mfma_f32_16x16x32_bf16 v[128:131], a[216:219], a[200:203], v[128:131]\n\t"
"buffer_load_dwordx4 a[216:219], off, s[80:83], s20 offset:2048 sc1\n\t"
"v_mfma_f32_16x16x32_bf16 v[128:131], a[220:223], a[204:207], v[128:131]\n\t"
"s_mov_b32 s20, 1019904\n\t"
"s_add_u32 m0, s24, 40192\n\t"
"s_add_u32 s28, s21, 1179648\n\t"
"v_mfma_f32_16x16x32_bf16 v[132:135], a[224:227], a[168:171], v[132:135]\n\t"
"v_mfma_f32_16x16x32_bf16 v[132:135], a[232:235], a[176:179], v[132:135]\n\t"
"buffer_load_dwordx4 a[220:223], off, s[80:83], s20 offset:0 sc1\n\t"
"v_mfma_f32_16x16x32_bf16 v[132:135], a[228:231], a[172:175], v[132:135]\n\t"
"v_mfma_f32_16x16x32_bf16 v[136:139], a[224:227], a[172:175], v[136:139]\n\t"
"buffer_load_dwordx4 v247, s[12:15], s28 offen offset:256 nt lds\n\t"
"v_mfma_f32_16x16x32_bf16 v[136:139], a[228:231], a[176:179], v[136:139]\n\t"
"s_add_u32 m0, s24, 46336\n\t"
"s_add_u32 s28, s21, 1376256\n\t"
"v_mfma_f32_16x16x32_bf16 v[136:139], a[232:235], a[180:183], v[136:139]\n\t"
"s_barrier\n\t"
"v_mfma_f32_16x16x32_bf16 v[140:143], a[232:235], a[184:187], v[140:143]\n\t"
"v_mfma_f32_16x16x32_bf16 v[140:143], a[228:231], a[180:183], v[140:143]\n\t"
"buffer_load_dwordx4 v247, s[12:15], s28 offen offset:256 nt lds\n\t"
"v_mfma_f32_16x16x32_bf16 v[140:143], a[224:227], a[176:179], v[140:143]\n\t"
"s_add_u32 m0, s24, 52480\n\t"
"s_add_u32 s28, s21, 1572864\n\t"
"s_waitcnt vmcnt(5)\n\t"
"ds_read_b64_tr_b16 v[224:225], v240 offset:512\n\t"
"v_mfma_f32_16x16x32_bf16 v[144:147], a[224:227], a[180:183], v[144:147]\n\t"
"buffer_load_dwordx4 v247, s[12:15], s28 offen offset:256 nt lds\n\t"
"v_mfma_f32_16x16x32_bf16 v[144:147], a[228:231], a[184:187], v[144:147]\n\t"
"s_add_u32 m0, s24, 41728\n\t"
"s_add_u32 s28, s21, 1182720\n\t"
"ds_read_b64_tr_b16 v[226:227], v240 offset:2048\n\t"
"v_mfma_f32_16x16x32_bf16 v[144:147], a[232:235], a[188:191], v[144:147]\n\t"
"buffer_load_dwordx4 v247, s[12:15], s28 offen offset:256 nt lds\n\t"
"s_add_u32 m0, s24, 47872\n\t"
"s_add_u32 s28, s21, 1379328\n\t"
"v_mfma_f32_16x16x32_bf16 v[148:151], a[232:235], a[192:195], v[148:151]\n\t"
"buffer_load_dwordx4 v247, s[12:15], s28 offen offset:256 nt lds\n\t"
"v_mfma_f32_16x16x32_bf16 v[148:151], a[228:231], a[188:191], v[148:151]\n\t"
"s_add_u32 m0, s24, 54016\n\t"
"s_add_u32 s28, s21, 1575936\n\t"
"ds_read_b64_tr_b16 v[236:237], v241 offset:512\n\t"
"v_mfma_f32_16x16x32_bf16 v[148:151], a[224:227], a[184:187], v[148:151]\n\t"
"buffer_load_dwordx4 v247, s[12:15], s28 offen offset:256 nt lds\n\t"
"v_mfma_f32_16x16x32_bf16 v[152:155], a[224:227], a[188:191], v[152:155]\n\t"
"ds_read_b64_tr_b16 v[238:239], v241 offset:2048\n\t"
"v_mfma_f32_16x16x32_bf16 v[152:155], a[228:231], a[192:195], v[152:155]\n\t"
"ds_read_b64_tr_b16 v[228:229], v240 offset:20480\n\t"
"v_mfma_f32_16x16x32_bf16 v[152:155], a[232:235], a[196:199], v[152:155]\n\t"
"ds_read_b64_tr_b16 v[230:231], v240 offset:22016\n\t"
"v_mfma_f32_16x16x32_bf16 v[156:159], a[232:235], a[200:203], v[156:159]\n\t"
"ds_read_b64_tr_b16 v[232:233], v241 offset:20480\n\t"
"v_mfma_f32_16x16x32_bf16 v[156:159], a[228:231], a[196:199], v[156:159]\n\t"
"ds_read_b64_tr_b16 v[234:235], v241 offset:22016\n\t"
"v_mfma_f32_16x16x32_bf16 v[156:159], a[224:227], a[192:195], v[156:159]\n\t"
"v_mfma_f32_16x16x32_bf16 v[160:163], a[224:227], a[196:199], v[160:163]\n\t"
"s_waitcnt vmcnt(8) lgkmcnt(6)\n\t"
"v_mfma_f32_16x16x32_bf16 v[160:163], a[228:231], a[200:203], v[160:163]\n\t"
"s_waitcnt lgkmcnt(4)\n\t"
"v_mfma_f32_16x16x32_bf16 v[160:163], a[232:235], a[204:207], v[160:163]\n\t"
"s_waitcnt lgkmcnt(2)\n\t"
"v_mfma_f32_16x16x32_bf16 v[224:227], v[224:227], a[208:211], 0\n\t"
"s_waitcnt lgkmcnt(0)\n\t"
"v_mfma_f32_16x16x32_bf16 v[236:239], v[236:239], a[208:211], 0\n\t"
"v_mfma_f32_16x16x32_bf16 v[228:231], v[228:231], a[208:211], 0\n\t"
"v_mfma_f32_16x16x32_bf16 v[232:235], v[232:235], a[208:211], 0\n\t"
"v_mfma_f32_16x16x32_bf16 v[164:167], a[236:239], a[168:171], v[164:167]\n\t"
"ds_read_b128 a[168:171], v222 offset:1536\n\t"
"v_mfma_f32_16x16x32_bf16 v[164:167], a[244:247], a[176:179], v[164:167]\n\t"
"v_cvt_pk_bf16_f32 v224, v224, v225\n\t"
"v_cvt_pk_bf16_f32 v225, v226, v227\n\t"
"v_mfma_f32_16x16x32_bf16 v[164:167], a[240:243], a[172:175], v[164:167]\n\t"
"v_cvt_pk_bf16_f32 v226, v236, v237\n\t"
"v_cvt_pk_bf16_f32 v227, v238, v239\n\t"
"v_mfma_f32_16x16x32_bf16 v[168:171], a[236:239], a[172:175], v[168:171]\n\t"
"ds_read_b128 a[172:175], v223 offset:7680\n\t"
"v_cvt_pk_bf16_f32 v228, v228, v229\n\t"
"v_mfma_f32_16x16x32_bf16 v[168:171], a[240:243], a[176:179], v[168:171]\n\t"
"v_cvt_pk_bf16_f32 v229, v230, v231\n\t"
"v_cvt_pk_bf16_f32 v230, v232, v233\n\t"
"v_mfma_f32_16x16x32_bf16 v[168:171], a[244:247], a[180:183], v[168:171]\n\t"
"ds_write_b128 v243, v[224:227] offset:3072\n\t"
"v_mfma_f32_16x16x32_bf16 v[172:175], a[244:247], a[184:187], v[172:175]\n\t"
"v_cvt_pk_bf16_f32 v231, v234, v235\n\t"
"v_add_u32 v240, 59904, v240\n\t"
"v_mfma_f32_16x16x32_bf16 v[172:175], a[240:243], a[180:183], v[172:175]\n\t"
"v_add_u32 v241, 59904, v241\n\t"
"v_mfma_f32_16x16x32_bf16 v[172:175], a[236:239], a[176:179], v[172:175]\n\t"
"ds_write_b128 v243, v[228:231] offset:23040\n\t"
"v_mfma_f32_16x16x32_bf16 v[176:179], a[236:239], a[180:183], v[176:179]\n\t"
"v_add_u32 v243, 59904, v243\n\t"
"v_mfma_f32_16x16x32_bf16 v[176:179], a[240:243], a[184:187], v[176:179]\n\t"
"ds_read_b128 a[176:179], v223 offset:13824\n\t"
"v_mfma_f32_16x16x32_bf16 v[176:179], a[244:247], a[188:191], v[176:179]\n\t"
"ds_read_b64_tr_b16 v[236:237], v240 offset:512\n\t"
"v_mfma_f32_16x16x32_bf16 v[180:183], a[244:247], a[192:195], v[180:183]\n\t"
"ds_read_b64_tr_b16 v[238:239], v240 offset:2048\n\t"
"v_mfma_f32_16x16x32_bf16 v[180:183], a[240:243], a[188:191], v[180:183]\n\t"
"buffer_load_dwordx4 a[224:227], off, s[80:83], s20 offset:1024 sc1\n\t"
"v_mfma_f32_16x16x32_bf16 v[180:183], a[236:239], a[184:187], v[180:183]\n\t"
"ds_read_b64_tr_b16 v[232:233], v241 offset:512\n\t"
"v_mfma_f32_16x16x32_bf16 v[184:187], a[236:239], a[188:191], v[184:187]\n\t"
"ds_read_b64_tr_b16 v[234:235], v241 offset:2048\n\t"
"v_mfma_f32_16x16x32_bf16 v[184:187], a[240:243], a[192:195], v[184:187]\n\t"
"ds_read_b128 a[180:183], v222 offset:21504\n\t"
"v_mfma_f32_16x16x32_bf16 v[184:187], a[244:247], a[196:199], v[184:187]\n\t"
"ds_read_b64_tr_b16 v[224:225], v240 offset:20480\n\t"
"v_mfma_f32_16x16x32_bf16 v[188:191], a[244:247], a[200:203], v[188:191]\n\t"
"ds_read_b64_tr_b16 v[226:227], v240 offset:22016\n\t"
"v_mfma_f32_16x16x32_bf16 v[188:191], a[240:243], a[196:199], v[188:191]\n\t"
"ds_read_b64_tr_b16 v[228:229], v241 offset:20480\n\t"
"v_mfma_f32_16x16x32_bf16 v[188:191], a[236:239], a[192:195], v[188:191]\n\t"
"ds_read_b64_tr_b16 v[230:231], v241 offset:22016\n\t"
"v_mfma_f32_16x16x32_bf16 v[192:195], a[236:239], a[196:199], v[192:195]\n\t"
"s_waitcnt lgkmcnt(7)\n\t"
"ds_read_b128 a[184:187], v223 offset:27648\n\t"
"v_mfma_f32_16x16x32_bf16 v[192:195], a[240:243], a[200:203], v[192:195]\n\t"
"s_waitcnt lgkmcnt(6)\n\t"
"ds_read_b128 a[188:191], v223 offset:33792\n\t"
"v_mfma_f32_16x16x32_bf16 v[192:195], a[244:247], a[204:207], v[192:195]\n\t"
"s_waitcnt lgkmcnt(4)\n\t"
"ds_read_b128 a[192:195], v222 offset:41472\n\t"
"v_mfma_f32_16x16x32_bf16 v[236:239], v[236:239], a[208:211], 0\n\t"
"ds_read_b128 a[196:199], v223 offset:47616\n\t"
"v_mfma_f32_16x16x32_bf16 v[232:235], v[232:235], a[208:211], 0\n\t"
"s_waitcnt lgkmcnt(4)\n\t"
"ds_read_b128 a[200:203], v223 offset:53760\n\t"
"v_mfma_f32_16x16x32_bf16 v[224:227], v[224:227], a[208:211], 0\n\t"
"ds_read_b128 a[204:207], v222 offset:61440\n\t"
"v_mfma_f32_16x16x32_bf16 v[228:231], v[228:231], a[208:211], 0\n\t"
"Lst9_%=:\n\t"
"v_mfma_f32_16x16x32_bf16 v[196:199], v[96:99], a[168:171], v[196:199]\n\t"
"s_mov_b64 vcc, -1\n\t"
"s_bitcmp1_b32 s73, 1\n\t"
"s_cbranch_scc1 Lvh2_%=\n\t"
"s_bitcmp1_b32 s73, 0\n\t"
"s_cbranch_scc1 Lpg18_%=\n\t"
"s_branch Lpg17_%=\n\t"
"Lvh2_%=:\n\t"
"s_bitcmp1_b32 s73, 0\n\t"
"s_cbranch_scc1 Lpg20_%=\n\t"
"s_branch Lpg19_%=\n\t"
"Lvr2_%=:\n\t"
"s_mov_b64 vcc, 0\n\t"
"v_mfma_f32_16x16x32_bf16 v[196:199], a[248:251], a[176:179], v[196:199]\n\t"
"v_cvt_pk_bf16_f32 v236, v236, v237\n\t"
"v_cvt_pk_bf16_f32 v237, v238, v239\n\t"
"v_mfma_f32_16x16x32_bf16 v[196:199], v[248:251], a[172:175], v[196:199]\n\t"
"v_cvt_pk_bf16_f32 v238, v232, v233\n\t"
"v_cvt_pk_bf16_f32 v239, v234, v235\n\t"
"v_mfma_f32_16x16x32_bf16 v[204:207], v[96:99], a[172:175], v[204:207]\n\t"
"s_waitcnt lgkmcnt(5)\n\t"
"v_cvt_pk_bf16_f32 v224, v224, v225\n\t"
"v_cvt_pk_bf16_f32 v225, v226, v227\n\t"
"v_mfma_f32_16x16x32_bf16 v[204:207], v[248:251], a[176:179], v[204:207]\n\t"
"v_cvt_pk_bf16_f32 v226, v228, v229\n\t"
"v_cvt_pk_bf16_f32 v227, v230, v231\n\t"
"v_mfma_f32_16x16x32_bf16 v[204:207], a[248:251], a[180:183], v[204:207]\n\t"
"ds_write_b128 v243, v[236:239] offset:3072\n\t"
"v_mfma_f32_16x16x32_bf16 v[212:215], a[248:251], a[184:187], v[212:215]\n\t"
"s_waitcnt lgkmcnt(5)\n\t"
"v_mfma_f32_16x16x32_bf16 v[212:215], v[248:251], a[180:183], v[212:215]\n\t"
"v_mfma_f32_16x16x32_bf16 v[212:215], v[96:99], a[176:179], v[212:215]\n\t"
"ds_write_b128 v243, v[224:227] offset:23040\n\t"
"v_mfma_f32_16x16x32_bf16 a[0:3], v[96:99], a[180:183], a[0:3]\n\t"
"s_waitcnt lgkmcnt(5)\n\t"
"v_mfma_f32_16x16x32_bf16 a[0:3], v[248:251], a[184:187], a[0:3]\n\t"
"v_mfma_f32_16x16x32_bf16 a[0:3], a[248:251], a[188:191], a[0:3]\n\t"
"s_barrier\n\t"
"v_mfma_f32_16x16x32_bf16 a[8:11], a[248:251], a[192:195], a[8:11]\n\t"
"s_waitcnt lgkmcnt(4)\n\t"
"ds_read_b64_tr_b16 v[232:233], v246 offset:512\n\t"
"v_mfma_f32_16x16x32_bf16 a[8:11], v[248:251], a[188:191], a[8:11]\n\t"
"ds_read_b64_tr_b16 v[228:229], v255 offset:512\n\t"
"v_mfma_f32_16x16x32_bf16 a[8:11], v[96:99], a[184:187], a[8:11]\n\t"
"ds_read_b64_tr_b16 v[236:237], v246 offset:6656\n\t"
"v_mfma_f32_16x16x32_bf16 a[16:19], v[96:99], a[188:191], a[16:19]\n\t"
"s_waitcnt lgkmcnt(6)\n\t"
"ds_read_b64_tr_b16 v[224:225], v255 offset:6656\n\t"
"v_mfma_f32_16x16x32_bf16 a[16:19], v[248:251], a[192:195], a[16:19]\n\t"
"buffer_load_dwordx4 a[228:231], off, s[80:83], s20 offset:2048 sc1\n\t"
"v_mfma_f32_16x16x32_bf16 a[16:19], a[248:251], a[196:199], a[16:19]\n\t"
"s_mov_b32 s20, 1032192\n\t"
"v_mfma_f32_16x16x32_bf16 a[24:27], a[248:251], a[200:203], a[24:27]\n\t"
"s_waitcnt lgkmcnt(6)\n\t"
"v_mfma_f32_16x16x32_bf16 a[24:27], v[248:251], a[196:199], a[24:27]\n\t"
"v_mfma_f32_16x16x32_bf16 a[24:27], v[96:99], a[192:195], a[24:27]\n\t"
"buffer_load_dwordx4 a[232:235], off, s[80:83], s20 offset:0 sc1\n\t"
"v_mfma_f32_16x16x32_bf16 a[32:35], v[96:99], a[196:199], a[32:35]\n\t"
"s_waitcnt lgkmcnt(3)\n\t"
"v_mfma_f32_16x16x32_bf16 a[32:35], v[248:251], a[200:203], a[32:35]\n\t"
"s_waitcnt lgkmcnt(2)\n\t"
"v_mfma_f32_16x16x32_bf16 a[32:35], a[248:251], a[204:207], a[32:35]\n\t"
"s_waitcnt lgkmcnt(1)\n\t"
"v_mfma_f32_4x4x4_16b_bf16 v[232:235], v[232:233], v[220:221], 0\n\t"
"s_waitcnt lgkmcnt(0)\n\t"
"v_mfma_f32_4x4x4_16b_bf16 v[228:231], v[228:229], v[220:221], 0\n\t"
"s_waitcnt vmcnt(10)\n\t"
"v_mfma_f32_4x4x4_16b_bf16 v[236:239], v[236:237], v[220:221], 0\n\t"
"v_mfma_f32_4x4x4_16b_bf16 v[224:227], v[224:225], v[220:221], 0\n\t"
"buffer_load_dwordx4 v[96:99], off, s[80:83], s20 offset:1024 sc1\n\t"
"v_mfma_f32_16x16x32_bf16 a[40:43], a[252:255], a[168:171], a[40:43]\n\t"
"buffer_load_dwordx4 v[248:251], off, s[80:83], s20 offset:2048 sc1\n\t"
"v_mfma_f32_16x16x32_bf16 a[40:43], a[216:219], a[176:179], a[40:43]\n\t"
"s_mov_b32 s20, 1044480\n\t"
"v_cvt_pk_bf16_f32 v232, v232, v233\n\t"
"v_cvt_pk_bf16_f32 v233, v234, v235\n\t"
"v_mfma_f32_16x16x32_bf16 a[40:43], a[212:215], a[172:175], a[40:43]\n\t"
"v_cvt_pk_bf16_f32 v234, v228, v229\n\t"
"v_cvt_pk_bf16_f32 v235, v230, v231\n\t"
"v_mfma_f32_16x16x32_bf16 a[48:51], a[252:255], a[172:175], a[48:51]\n\t"
"ds_read_b64_tr_b16 v[228:229], v246 offset:12800\n\t"
"v_cvt_pk_bf16_f32 v236, v236, v237\n\t"
"v_mfma_f32_16x16x32_bf16 a[48:51], a[212:215], a[176:179], a[48:51]\n\t"
"v_cvt_pk_bf16_f32 v237, v238, v239\n\t"
"v_cvt_pk_bf16_f32 v238, v224, v225\n\t"
"v_mfma_f32_16x16x32_bf16 a[48:51], a[216:219], a[180:183], a[48:51]\n\t"
"ds_write_b128 v245, v[232:235] offset:512\n\t"
"v_cvt_pk_bf16_f32 v239, v226, v227\n\t"
"v_mfma_f32_16x16x32_bf16 a[56:59], a[216:219], a[184:187], a[56:59]\n\t"
"v_mfma_f32_16x16x32_bf16 a[56:59], a[212:215], a[180:183], a[56:59]\n\t"
"buffer_load_dwordx4 a[236:239], off, s[80:83], s20 offset:0 sc1\n\t"
"v_mfma_f32_16x16x32_bf16 a[56:59], a[252:255], a[176:179], a[56:59]\n\t"
"ds_read_b64_tr_b16 v[224:225], v255 offset:12800\n\t"
"v_mfma_f32_16x16x32_bf16 a[64:67], a[252:255], a[180:183], a[64:67]\n\t"
"ds_read_b64_tr_b16 v[232:233], v246 offset:20480\n\t"
"v_mfma_f32_16x16x32_bf16 a[64:67], a[212:215], a[184:187], a[64:67]\n\t"
"buffer_load_dwordx4 a[240:243], off, s[80:83], s20 offset:1024 sc1\n\t"
"v_mfma_f32_16x16x32_bf16 a[64:67], a[216:219], a[188:191], a[64:67]\n\t"
"ds_write_b128 v245, v[236:239] offset:6656\n\t"
"v_mfma_f32_16x16x32_bf16 a[72:75], a[252:255], a[184:187], a[72:75]\n\t"
"buffer_load_dwordx4 a[244:247], off, s[80:83], s20 offset:2048 sc1\n\t"
"v_mfma_f32_16x16x32_bf16 a[72:75], a[216:219], a[192:195], a[72:75]\n\t"
"s_waitcnt lgkmcnt(2)\n\t"
"ds_read_b64_tr_b16 v[236:237], v255 offset:20480\n\t"
"v_mfma_f32_16x16x32_bf16 a[72:75], a[212:215], a[188:191], a[72:75]\n\t"
"s_waitcnt lgkmcnt(2)\n\t"
"s_mov_b32 s20, 1056768\n\t"
"v_mfma_f32_4x4x4_16b_bf16 v[228:231], v[228:229], v[220:221], 0\n\t"
"v_mfma_f32_4x4x4_16b_bf16 v[224:227], v[224:225], v[220:221], 0\n\t"
"v_mfma_f32_4x4x4_16b_bf16 v[232:235], v[232:233], v[220:221], 0\n\t"
"s_waitcnt lgkmcnt(0)\n\t"
"v_mfma_f32_4x4x4_16b_bf16 v[236:239], v[236:237], v[220:221], 0\n\t"
"v_mfma_f32_16x16x32_bf16 a[80:83], a[252:255], a[188:191], a[80:83]\n\t"
"buffer_load_dwordx4 a[248:251], off, s[80:83], s20 offset:0 sc1\n\t"
"v_mfma_f32_16x16x32_bf16 a[80:83], a[216:219], a[196:199], a[80:83]\n\t"
"v_cvt_pk_bf16_f32 v228, v228, v229\n\t"
"v_cvt_pk_bf16_f32 v229, v230, v231\n\t"
"v_mfma_f32_16x16x32_bf16 a[80:83], a[212:215], a[192:195], a[80:83]\n\t"
"v_cvt_pk_bf16_f32 v230, v224, v225\n\t"
"v_cvt_pk_bf16_f32 v231, v226, v227\n\t"
"v_mfma_f32_16x16x32_bf16 a[88:91], a[252:255], a[192:195], a[88:91]\n\t"
"ds_read_b64_tr_b16 v[224:225], v246 offset:26624\n\t"
"v_cvt_pk_bf16_f32 v232, v232, v233\n\t"
"v_mfma_f32_16x16x32_bf16 a[88:91], a[212:215], a[196:199], a[88:91]\n\t"
"v_cvt_pk_bf16_f32 v233, v234, v235\n\t"
"v_cvt_pk_bf16_f32 v234, v236, v237\n\t"
"v_mfma_f32_16x16x32_bf16 a[88:91], a[216:219], a[200:203], a[88:91]\n\t"
"ds_write_b128 v245, v[228:231] offset:12800\n\t"
"v_cvt_pk_bf16_f32 v235, v238, v239\n\t"
"v_mfma_f32_16x16x32_bf16 a[96:99], a[212:215], a[200:203], a[96:99]\n\t"
"s_waitcnt vmcnt(7)\n\t"
"v_mfma_f32_16x16x32_bf16 a[96:99], a[252:255], a[196:199], a[96:99]\n\t"
"ds_read_b64_tr_b16 v[236:237], v255 offset:26624\n\t"
"v_mfma_f32_16x16x32_bf16 a[96:99], a[216:219], a[204:207], a[96:99]\n\t"
"ds_write_b128 v245, v[232:235] offset:20480\n\t"
"v_mfma_f32_16x16x32_bf16 a[104:107], a[220:223], a[168:171], a[104:107]\n\t"
"buffer_load_dwordx4 a[212:215], off, s[80:83], s20 offset:1024 sc1\n\t"
"v_mfma_f32_16x16x32_bf16 a[104:107], a[228:231], a[176:179], a[104:107]\n\t"
"ds_read_b64_tr_b16 v[228:229], v246 offset:32768\n\t"
"v_mfma_f32_16x16x32_bf16 a[104:107], a[224:227], a[172:175], a[104:107]\n\t"
"s_waitcnt lgkmcnt(4)\n\t"
"ds_read_b64_tr_b16 v[232:233], v255 offset:32768\n\t"
"v_mfma_f32_16x16x32_bf16 a[112:115], a[220:223], a[172:175], a[112:115]\n\t"
"buffer_load_dwordx4 a[216:219], off, s[80:83], s20 offset:2048 sc1\n\t"
"v_mfma_f32_16x16x32_bf16 a[112:115], a[224:227], a[176:179], a[112:115]\n\t"
"Lpg7_%=:\n\t"
"s_cbranch_vccnz Lpg11_%=\n\t"
"s_waitcnt lgkmcnt(3)\n\t"
"ds_read_b128 a[168:171], v222 offset:1792\n\t"
"v_mfma_f32_16x16x32_bf16 a[112:115], a[228:231], a[180:183], a[112:115]\n\t"
"s_mov_b32 s20, 1216512\n\t"
"ds_read_b128 a[172:175], v223 offset:7936\n\t"
"v_mfma_f32_4x4x4_16b_bf16 v[224:227], v[224:225], v[220:221], 0\n\t"
"buffer_load_dwordx4 a[252:255], off, s[80:83], s20 offset:0 sc1\n\t"
"v_mfma_f32_4x4x4_16b_bf16 v[236:239], v[236:237], v[220:221], 0\n\t"
"s_waitcnt lgkmcnt(2)\n\t"
"v_mfma_f32_4x4x4_16b_bf16 v[228:231], v[228:229], v[220:221], 0\n\t"
"v_mfma_f32_4x4x4_16b_bf16 v[232:235], v[232:233], v[220:221], 0\n\t"
"v_mfma_f32_16x16x32_bf16 a[120:123], a[224:227], a[180:183], a[120:123]\n\t"
"v_cvt_pk_bf16_f32 v224, v224, v225\n\t"
"v_cvt_pk_bf16_f32 v225, v226, v227\n\t"
"v_mfma_f32_16x16x32_bf16 a[120:123], a[220:223], a[176:179], a[120:123]\n\t"
"ds_read_b128 a[176:179], v223 offset:14080\n\t"
"v_cvt_pk_bf16_f32 v226, v236, v237\n\t"
"v_mfma_f32_16x16x32_bf16 a[120:123], a[228:231], a[184:187], a[120:123]\n\t"
"v_cvt_pk_bf16_f32 v227, v238, v239\n\t"
"v_cvt_pk_bf16_f32 v228, v228, v229\n\t"
"v_mfma_f32_16x16x32_bf16 a[128:131], a[220:223], a[180:183], a[128:131]\n\t"
"ds_read_b128 a[180:183], v222 offset:21760\n\t"
"v_cvt_pk_bf16_f32 v229, v230, v231\n\t"
"v_mfma_f32_16x16x32_bf16 a[128:131], a[228:231], a[188:191], a[128:131]\n\t"
"v_cvt_pk_bf16_f32 v230, v232, v233\n\t"
"v_cvt_pk_bf16_f32 v231, v234, v235\n\t"
"v_mfma_f32_16x16x32_bf16 a[128:131], a[224:227], a[184:187], a[128:131]\n\t"
"ds_write_b128 v245, v[224:227] offset:26624\n\t"
"v_mfma_f32_16x16x32_bf16 a[136:139], a[220:223], a[184:187], a[136:139]\n\t"
"v_mfma_f32_16x16x32_bf16 a[136:139], a[224:227], a[188:191], a[136:139]\n\t"
"ds_read_b128 a[184:187], v223 offset:27904\n\t"
"v_mfma_f32_16x16x32_bf16 a[136:139], a[228:231], a[192:195], a[136:139]\n\t"
"ds_write_b128 v245, v[228:231] offset:32768\n\t"
"v_mfma_f32_16x16x32_bf16 a[144:147], a[228:231], a[196:199], a[144:147]\n\t"
"v_mfma_f32_16x16x32_bf16 a[144:147], a[224:227], a[192:195], a[144:147]\n\t"
"v_mfma_f32_16x16x32_bf16 a[144:147], a[220:223], a[188:191], a[144:147]\n\t"
"ds_read_b128 a[188:191], v223 offset:34048\n\t"
"v_mfma_f32_16x16x32_bf16 a[152:155], a[220:223], a[192:195], a[152:155]\n\t"
"ds_read_b128 a[192:195], v222 offset:41728\n\t"
"v_mfma_f32_16x16x32_bf16 a[152:155], a[224:227], a[196:199], a[152:155]\n\t"
"v_mfma_f32_16x16x32_bf16 a[152:155], a[228:231], a[200:203], a[152:155]\n\t"
"s_barrier\n\t"
"v_mfma_f32_16x16x32_bf16 a[160:163], a[224:227], a[200:203], a[160:163]\n\t"
"s_waitcnt vmcnt(7) lgkmcnt(6)\n\t"
"ds_read_b64_tr_b16 v[236:237], v240 offset:40448\n\t"
"v_mfma_f32_16x16x32_bf16 a[160:163], a[220:223], a[196:199], a[160:163]\n\t"
"ds_read_b64_tr_b16 v[238:239], v240 offset:41984\n\t"
"v_mfma_f32_16x16x32_bf16 a[160:163], a[228:231], a[204:207], a[160:163]\n\t"
"ds_read_b64_tr_b16 v[232:233], v241 offset:40448\n\t"
"v_mfma_f32_16x16x32_bf16 v[200:203], a[232:235], a[168:171], v[200:203]\n\t"
"s_waitcnt lgkmcnt(8)\n\t"
"ds_read_b64_tr_b16 v[234:235], v241 offset:41984\n\t"
"v_subrev_u32 v240, 59904, v240\n\t"
"v_mfma_f32_16x16x32_bf16 v[200:203], v[248:251], a[176:179], v[200:203]\n\t"
"ds_read_b128 a[196:199], v223 offset:47872\n\t"
"v_subrev_u32 v241, 59904, v241\n\t"
"v_mfma_f32_16x16x32_bf16 v[200:203], v[96:99], a[172:175], v[200:203]\n\t"
"ds_read_b64_tr_b16 v[224:225], v240 offset:40448\n\t"
"v_mfma_f32_16x16x32_bf16 v[208:211], a[232:235], a[172:175], v[208:211]\n\t"
"s_waitcnt lgkmcnt(4)\n\t"
"ds_read_b64_tr_b16 v[226:227], v240 offset:41984\n\t"
"v_mfma_f32_16x16x32_bf16 v[208:211], v[96:99], a[176:179], v[208:211]\n\t"
"buffer_load_dwordx4 a[224:227], off, s[80:83], s20 offset:1024 sc1\n\t"
"v_mfma_f32_16x16x32_bf16 v[208:211], v[248:251], a[180:183], v[208:211]\n\t"
"s_waitcnt lgkmcnt(3)\n\t"
"ds_read_b64_tr_b16 v[228:229], v241 offset:40448\n\t"
"ds_read_b64_tr_b16 v[230:231], v241 offset:41984\n\t"
"v_mfma_f32_16x16x32_bf16 v[236:239], v[236:239], a[208:211], 0\n\t"
"buffer_load_dwordx4 a[220:223], off, s[80:83], s20 offset:2048 sc1\n\t"
"s_mov_b32 s20, 1228800\n\t"
"v_mfma_f32_16x16x32_bf16 v[232:235], v[232:235], a[208:211], 0\n\t"
"s_waitcnt lgkmcnt(2)\n\t"
"buffer_load_dwordx4 a[228:231], off, s[80:83], s20 offset:0 sc1\n\t"
"v_mfma_f32_16x16x32_bf16 v[224:227], v[224:227], a[208:211], 0\n\t"
"s_waitcnt lgkmcnt(0)\n\t"
"ds_read_b128 a[200:203], v223 offset:54016\n\t"
"v_mfma_f32_16x16x32_bf16 v[228:231], v[228:231], a[208:211], 0\n\t"
"ds_read_b128 a[204:207], v222 offset:61696\n\t"
"v_mfma_f32_16x16x32_bf16 v[216:219], v[96:99], a[180:183], v[216:219]\n\t"
"v_mfma_f32_16x16x32_bf16 v[216:219], a[232:235], a[176:179], v[216:219]\n\t"
"v_cvt_pk_bf16_f32 v236, v236, v237\n\t"
"v_cvt_pk_bf16_f32 v237, v238, v239\n\t"
"v_mfma_f32_16x16x32_bf16 v[216:219], v[248:251], a[184:187], v[216:219]\n\t"
"v_cvt_pk_bf16_f32 v238, v232, v233\n\t"
"v_cvt_pk_bf16_f32 v239, v234, v235\n\t"
"v_mfma_f32_16x16x32_bf16 a[4:7], a[232:235], a[180:183], a[4:7]\n\t"
"ds_read_b64_tr_b16 v[232:233], v246 offset:40448\n\t"
"v_cvt_pk_bf16_f32 v224, v224, v225\n\t"
"v_mfma_f32_16x16x32_bf16 a[4:7], v[248:251], a[188:191], a[4:7]\n\t"
"v_cvt_pk_bf16_f32 v225, v226, v227\n\t"
"v_cvt_pk_bf16_f32 v226, v228, v229\n\t"
"v_mfma_f32_16x16x32_bf16 a[4:7], v[96:99], a[184:187], a[4:7]\n\t"
"ds_write_b128 v243, v[236:239] offset:43008\n\t"
"v_mfma_f32_16x16x32_bf16 a[12:15], a[232:235], a[184:187], a[12:15]\n\t"
"v_cvt_pk_bf16_f32 v227, v230, v231\n\t"
"v_subrev_u32 v243, 59904, v243\n\t"
"v_mfma_f32_16x16x32_bf16 a[12:15], v[96:99], a[188:191], a[12:15]\n\t"
"ds_read_b64_tr_b16 v[228:229], v255 offset:40448\n\t"
"v_mfma_f32_16x16x32_bf16 a[12:15], v[248:251], a[192:195], a[12:15]\n\t"
"ds_write_b128 v243, v[224:227] offset:43008\n\t"
"v_mfma_f32_16x16x32_bf16 a[20:23], v[248:251], a[196:199], a[20:23]\n\t"
"s_waitcnt lgkmcnt(5)\n\t"
"v_mfma_f32_16x16x32_bf16 a[20:23], v[96:99], a[192:195], a[20:23]\n\t"
"ds_read_b64_tr_b16 v[236:237], v246 offset:46592\n\t"
"v_mfma_f32_16x16x32_bf16 a[20:23], a[232:235], a[188:191], a[20:23]\n\t"
"ds_read_b64_tr_b16 v[224:225], v255 offset:46592\n\t"
"v_mfma_f32_16x16x32_bf16 a[28:31], a[232:235], a[192:195], a[28:31]\n\t"
"s_waitcnt lgkmcnt(6)\n\t"
"v_mfma_f32_16x16x32_bf16 a[28:31], v[96:99], a[196:199], a[28:31]\n\t"
"v_mfma_f32_16x16x32_bf16 a[28:31], v[248:251], a[200:203], a[28:31]\n\t"
"s_barrier\n\t"
"v_mfma_f32_16x16x32_bf16 a[36:39], v[96:99], a[200:203], a[36:39]\n\t"
"s_waitcnt lgkmcnt(5)\n\t"
"v_mfma_f32_16x16x32_bf16 a[36:39], a[232:235], a[196:199], a[36:39]\n\t"
"buffer_load_dwordx4 v[96:99], off, s[80:83], s20 offset:1024 sc1\n\t"
"v_mfma_f32_16x16x32_bf16 a[36:39], v[248:251], a[204:207], a[36:39]\n\t"
"s_waitcnt lgkmcnt(1)\n\t"
"v_mfma_f32_4x4x4_16b_bf16 v[232:235], v[232:233], v[220:221], 0\n\t"
"s_waitcnt lgkmcnt(0)\n\t"
"v_mfma_f32_4x4x4_16b_bf16 v[228:231], v[228:229], v[220:221], 0\n\t"
"s_waitcnt vmcnt(8)\n\t"
"v_mfma_f32_4x4x4_16b_bf16 v[236:239], v[236:237], v[220:221], 0\n\t"
"v_mfma_f32_4x4x4_16b_bf16 v[224:227], v[224:225], v[220:221], 0\n\t"
"buffer_load_dwordx4 v[248:251], off, s[80:83], s20 offset:2048 sc1\n\t"
"s_mov_b32 s20, 1241088\n\t"
"v_mfma_f32_16x16x32_bf16 a[44:47], a[236:239], a[168:171], a[44:47]\n\t"
"buffer_load_dwordx4 a[232:235], off, s[80:83], s20 offset:0 sc1\n\t"
"v_mfma_f32_16x16x32_bf16 a[44:47], a[244:247], a[176:179], a[44:47]\n\t"
"v_cvt_pk_bf16_f32 v232, v232, v233\n\t"
"v_cvt_pk_bf16_f32 v233, v234, v235\n\t"
"v_mfma_f32_16x16x32_bf16 a[44:47], a[240:243], a[172:175], a[44:47]\n\t"
"v_cvt_pk_bf16_f32 v234, v228, v229\n\t"
"v_cvt_pk_bf16_f32 v235, v230, v231\n\t"
"v_mfma_f32_16x16x32_bf16 a[52:55], a[236:239], a[172:175], a[52:55]\n\t"
"ds_read_b64_tr_b16 v[228:229], v246 offset:52736\n\t"
"v_cvt_pk_bf16_f32 v236, v236, v237\n\t"
"v_mfma_f32_16x16x32_bf16 a[52:55], a[240:243], a[176:179], a[52:55]\n\t"
"v_cvt_pk_bf16_f32 v237, v238, v239\n\t"
"v_cvt_pk_bf16_f32 v238, v224, v225\n\t"
"v_mfma_f32_16x16x32_bf16 a[52:55], a[244:247], a[180:183], a[52:55]\n\t"
"ds_write_b128 v245, v[232:235] offset:40448\n\t"
"v_cvt_pk_bf16_f32 v239, v226, v227\n\t"
"v_mfma_f32_16x16x32_bf16 a[60:63], a[244:247], a[184:187], a[60:63]\n\t"
"v_mfma_f32_16x16x32_bf16 a[60:63], a[240:243], a[180:183], a[60:63]\n\t"
"ds_read_b64_tr_b16 v[224:225], v255 offset:52736\n\t"
"v_mfma_f32_16x16x32_bf16 a[60:63], a[236:239], a[176:179], a[60:63]\n\t"
"ds_write_b128 v245, v[236:239] offset:46592\n\t"
"v_mfma_f32_16x16x32_bf16 a[68:71], a[236:239], a[180:183], a[68:71]\n\t"
"v_mfma_f32_16x16x32_bf16 a[68:71], a[240:243], a[184:187], a[68:71]\n\t"
"v_mfma_f32_16x16x32_bf16 a[68:71], a[244:247], a[188:191], a[68:71]\n\t"
"v_mfma_f32_16x16x32_bf16 a[76:79], a[236:239], a[184:187], a[76:79]\n\t"
"s_waitcnt lgkmcnt(3)\n\t"
"v_mfma_f32_16x16x32_bf16 a[76:79], a[244:247], a[192:195], a[76:79]\n\t"
"s_waitcnt lgkmcnt(1)\n\t"
"ds_read_b64_tr_b16 v[232:233], v240 offset:1024\n\t"
"v_mfma_f32_16x16x32_bf16 a[76:79], a[240:243], a[188:191], a[76:79]\n\t"
"ds_read_b64_tr_b16 v[234:235], v240 offset:2560\n\t"
"v_mfma_f32_4x4x4_16b_bf16 v[228:231], v[228:229], v[220:221], 0\n\t"
"v_mfma_f32_4x4x4_16b_bf16 v[224:227], v[224:225], v[220:221], 0\n\t"
"v_mfma_f32_16x16x32_bf16 a[84:87], a[236:239], a[188:191], a[84:87]\n\t"
"ds_read_b64_tr_b16 v[236:237], v241 offset:1024\n\t"
"v_mfma_f32_16x16x32_bf16 a[84:87], a[244:247], a[196:199], a[84:87]\n\t"
"ds_read_b64_tr_b16 v[238:239], v241 offset:2560\n\t"
"v_cvt_pk_bf16_f32 v228, v228, v229\n\t"
"v_mfma_f32_16x16x32_bf16 a[84:87], a[240:243], a[192:195], a[84:87]\n\t"
"v_cvt_pk_bf16_f32 v229, v230, v231\n\t"
"v_cvt_pk_bf16_f32 v230, v224, v225\n\t"
"v_mfma_f32_16x16x32_bf16 a[92:95], a[236:239], a[192:195], a[92:95]\n\t"
"v_cvt_pk_bf16_f32 v231, v226, v227\n\t"
"v_mfma_f32_16x16x32_bf16 a[92:95], a[240:243], a[196:199], a[92:95]\n\t"
"ds_read_b64_tr_b16 v[224:225], v240 offset:20992\n\t"
"v_mfma_f32_16x16x32_bf16 a[92:95], a[244:247], a[200:203], a[92:95]\n\t"
"ds_read_b64_tr_b16 v[226:227], v240 offset:22528\n\t"
"v_mfma_f32_16x16x32_bf16 a[100:103], a[240:243], a[200:203], a[100:103]\n\t"
"buffer_load_dwordx4 a[240:243], off, s[80:83], s20 offset:1024 sc1\n\t"
"v_mfma_f32_16x16x32_bf16 a[100:103], a[236:239], a[196:199], a[100:103]\n\t"
"s_waitcnt vmcnt(8)\n\t"
"v_mfma_f32_16x16x32_bf16 a[100:103], a[244:247], a[204:207], a[100:103]\n\t"
"ds_write_b128 v245, v[228:231] offset:52736\n\t"
"v_mfma_f32_16x16x32_bf16 a[108:111], a[248:251], a[168:171], a[108:111]\n\t"
"buffer_load_dwordx4 a[236:239], off, s[80:83], s20 offset:2048 sc1\n\t"
"v_mfma_f32_16x16x32_bf16 a[108:111], a[216:219], a[176:179], a[108:111]\n\t"
"s_waitcnt lgkmcnt(3)\n\t"
"ds_read_b64_tr_b16 v[228:229], v241 offset:20992\n\t"
"v_mfma_f32_16x16x32_bf16 a[108:111], a[212:215], a[172:175], a[108:111]\n\t"
"s_waitcnt lgkmcnt(2)\n\t"
"ds_read_b64_tr_b16 v[230:231], v241 offset:22528\n\t"
"s_mov_b32 s20, 1253376\n\t"
"v_mfma_f32_16x16x32_bf16 v[232:235], v[232:235], a[208:211], 0\n\t"
"buffer_load_dwordx4 a[244:247], off, s[80:83], s20 offset:0 sc1\n\t"
"v_mfma_f32_16x16x32_bf16 v[236:239], v[236:239], a[208:211], 0\n\t"
"v_mfma_f32_16x16x32_bf16 v[224:227], v[224:227], a[208:211], 0\n\t"
"s_waitcnt lgkmcnt(0)\n\t"
"v_mfma_f32_16x16x32_bf16 v[228:231], v[228:231], a[208:211], 0\n\t"
"s_barrier\n\t"
"v_mfma_f32_16x16x32_bf16 a[116:119], a[248:251], a[172:175], a[116:119]\n\t"
"ds_read_b128 a[168:171], v222 offset:2048\n\t"
"v_mfma_f32_16x16x32_bf16 a[116:119], a[216:219], a[180:183], a[116:119]\n\t"
"ds_read_b128 a[172:175], v223 offset:8192\n\t"
"v_cvt_pk_bf16_f32 v232, v232, v233\n\t"
"v_mfma_f32_16x16x32_bf16 a[116:119], a[212:215], a[176:179], a[116:119]\n\t"
"v_cvt_pk_bf16_f32 v233, v234, v235\n\t"
"v_cvt_pk_bf16_f32 v234, v236, v237\n\t"
"v_mfma_f32_16x16x32_bf16 a[124:127], a[248:251], a[176:179], a[124:127]\n\t"
"ds_read_b128 a[176:179], v223 offset:14336\n\t"
"v_cvt_pk_bf16_f32 v235, v238, v239\n\t"
"v_mfma_f32_16x16x32_bf16 a[124:127], a[212:215], a[180:183], a[124:127]\n\t"
"v_cvt_pk_bf16_f32 v224, v224, v225\n\t"
"v_cvt_pk_bf16_f32 v225, v226, v227\n\t"
"v_mfma_f32_16x16x32_bf16 a[124:127], a[216:219], a[184:187], a[124:127]\n\t"
"ds_write_b128 v243, v[232:235] offset:1536\n\t"
"v_mfma_f32_16x16x32_bf16 a[132:135], a[216:219], a[188:191], a[132:135]\n\t"
"v_cvt_pk_bf16_f32 v226, v228, v229\n\t"
"v_cvt_pk_bf16_f32 v227, v230, v231\n\t"
"v_mfma_f32_16x16x32_bf16 a[132:135], a[212:215], a[184:187], a[132:135]\n\t"
"ds_read_b64_tr_b16 v[236:237], v240 offset:40960\n\t"
"v_mfma_f32_16x16x32_bf16 a[132:135], a[248:251], a[180:183], a[132:135]\n\t"
"ds_write_b128 v243, v[224:227] offset:21504\n\t"
"v_mfma_f32_16x16x32_bf16 a[140:143], a[248:251], a[184:187], a[140:143]\n\t"
"v_mfma_f32_16x16x32_bf16 a[140:143], a[212:215], a[188:191], a[140:143]\n\t"
"ds_read_b64_tr_b16 v[238:239], v240 offset:42496\n\t"
"v_mfma_f32_16x16x32_bf16 a[140:143], a[216:219], a[192:195], a[140:143]\n\t"
"ds_read_b64_tr_b16 v[228:229], v241 offset:40960\n\t"
"v_mfma_f32_16x16x32_bf16 a[148:151], a[216:219], a[196:199], a[148:151]\n\t"
"ds_read_b64_tr_b16 v[230:231], v241 offset:42496\n\t"
"v_add_u32 v240, 59904, v240\n\t"
"v_mfma_f32_16x16x32_bf16 a[148:151], a[212:215], a[192:195], a[148:151]\n\t"
"ds_read_b128 a[180:183], v222 offset:22016\n\t"
"v_add_u32 v241, 59904, v241\n\t"
"v_mfma_f32_16x16x32_bf16 a[148:151], a[248:251], a[188:191], a[148:151]\n\t"
"ds_read_b64_tr_b16 v[232:233], v240 offset:1024\n\t"
"v_mfma_f32_16x16x32_bf16 a[156:159], a[248:251], a[192:195], a[156:159]\n\t"
"ds_read_b64_tr_b16 v[234:235], v240 offset:2560\n\t"
"v_mfma_f32_16x16x32_bf16 a[156:159], a[212:215], a[196:199], a[156:159]\n\t"
"ds_read_b64_tr_b16 v[224:225], v241 offset:1024\n\t"
"v_mfma_f32_16x16x32_bf16 a[156:159], a[216:219], a[200:203], a[156:159]\n\t"
"ds_read_b64_tr_b16 v[226:227], v241 offset:2560\n\t"
"v_mfma_f32_16x16x32_bf16 a[164:167], a[212:215], a[200:203], a[164:167]\n\t"
"s_waitcnt lgkmcnt(7)\n\t"
"ds_read_b128 a[184:187], v223 offset:28160\n\t"
"v_mfma_f32_16x16x32_bf16 a[164:167], a[248:251], a[196:199], a[164:167]\n\t"
"s_waitcnt lgkmcnt(6)\n\t"
"ds_read_b128 a[188:191], v223 offset:34304\n\t"
"v_mfma_f32_16x16x32_bf16 a[164:167], a[216:219], a[204:207], a[164:167]\n\t"
"buffer_load_dwordx4 a[212:215], off, s[80:83], s20 offset:1024 sc1\n\t"
"v_mfma_f32_16x16x32_bf16 v[236:239], v[236:239], a[208:211], 0\n\t"
"s_waitcnt lgkmcnt(4)\n\t"
"ds_read_b128 a[192:195], v222 offset:41984\n\t"
"v_mfma_f32_16x16x32_bf16 v[228:231], v[228:231], a[208:211], 0\n\t"
"buffer_load_dwordx4 a[216:219], off, s[80:83], s20 offset:2048 sc1\n\t"
"v_mfma_f32_16x16x32_bf16 v[232:235], v[232:235], a[208:211], 0\n\t"
"s_waitcnt vmcnt(9) lgkmcnt(3)\n\t"
"ds_read_b128 a[196:199], v223 offset:48128\n\t"
"s_mov_b32 s20, 1265664\n\t"
"v_mfma_f32_16x16x32_bf16 v[224:227], v[224:227], a[208:211], 0\n\t"
"buffer_load_dwordx4 a[248:251], off, s[80:83], s20 offset:0 sc1\n\t"
"Lst10_%=:\n\t"
"v_mfma_f32_16x16x32_bf16 v[0:3], a[252:255], a[168:171], v[0:3]\n\t"
"ds_read_b128 a[200:203], v223 offset:54272\n\t"
"v_mfma_f32_16x16x32_bf16 v[0:3], a[220:223], a[176:179], v[0:3]\n\t"
"ds_read_b128 a[204:207], v222 offset:61952\n\t"
"v_cvt_pk_bf16_f32 v236, v236, v237\n\t"
"v_mfma_f32_16x16x32_bf16 v[0:3], a[224:227], a[172:175], v[0:3]\n\t"
"v_cvt_pk_bf16_f32 v237, v238, v239\n\t"
"v_cvt_pk_bf16_f32 v238, v228, v229\n\t"
"v_mfma_f32_16x16x32_bf16 v[4:7], a[252:255], a[172:175], v[4:7]\n\t"
"s_waitcnt lgkmcnt(5)\n\t"
"v_cvt_pk_bf16_f32 v239, v230, v231\n\t"
"v_cvt_pk_bf16_f32 v232, v232, v233\n\t"
"v_mfma_f32_16x16x32_bf16 v[4:7], a[224:227], a[176:179], v[4:7]\n\t"
"v_cvt_pk_bf16_f32 v233, v234, v235\n\t"
"v_cvt_pk_bf16_f32 v234, v224, v225\n\t"
"v_mfma_f32_16x16x32_bf16 v[4:7], a[220:223], a[180:183], v[4:7]\n\t"
"ds_write_b128 v243, v[236:239] offset:41472\n\t"
"v_mfma_f32_16x16x32_bf16 v[8:11], a[220:223], a[184:187], v[8:11]\n\t"
"s_waitcnt lgkmcnt(5)\n\t"
"v_cvt_pk_bf16_f32 v235, v226, v227\n\t"
"v_add_u32 v243, 59904, v243\n\t"
"v_mfma_f32_16x16x32_bf16 v[8:11], a[224:227], a[180:183], v[8:11]\n\t"
"v_mfma_f32_16x16x32_bf16 v[8:11], a[252:255], a[176:179], v[8:11]\n\t"
"ds_write_b128 v243, v[232:235] offset:1536\n\t"
"v_mfma_f32_16x16x32_bf16 v[12:15], a[252:255], a[180:183], v[12:15]\n\t"
"s_waitcnt lgkmcnt(5)\n\t"
"v_mfma_f32_16x16x32_bf16 v[12:15], a[224:227], a[184:187], v[12:15]\n\t"
"ds_read_b64_tr_b16 v[228:229], v240 offset:20992\n\t"
"v_mfma_f32_16x16x32_bf16 v[12:15], a[220:223], a[188:191], v[12:15]\n\t"
"ds_read_b64_tr_b16 v[230:231], v240 offset:22528\n\t"
"v_mfma_f32_16x16x32_bf16 v[16:19], a[220:223], a[192:195], v[16:19]\n\t"
"s_waitcnt lgkmcnt(6)\n\t"
"ds_read_b64_tr_b16 v[224:225], v241 offset:20992\n\t"
"v_mfma_f32_16x16x32_bf16 v[16:19], a[224:227], a[188:191], v[16:19]\n\t"
"ds_read_b64_tr_b16 v[226:227], v241 offset:22528\n\t"
"v_mfma_f32_16x16x32_bf16 v[16:19], a[252:255], a[184:187], v[16:19]\n\t"
"ds_read_b64_tr_b16 v[236:237], v240 offset:40960\n\t"
"v_mfma_f32_16x16x32_bf16 v[20:23], a[252:255], a[188:191], v[20:23]\n\t"
"s_waitcnt lgkmcnt(8)\n\t"
"ds_read_b64_tr_b16 v[238:239], v240 offset:42496\n\t"
"v_mfma_f32_16x16x32_bf16 v[20:23], a[224:227], a[192:195], v[20:23]\n\t"
"ds_read_b64_tr_b16 v[232:233], v241 offset:40960\n\t"
"v_mfma_f32_16x16x32_bf16 v[20:23], a[220:223], a[196:199], v[20:23]\n\t"
"ds_read_b64_tr_b16 v[234:235], v241 offset:42496\n\t"
"v_mfma_f32_16x16x32_bf16 v[24:27], a[220:223], a[200:203], v[24:27]\n\t"
"s_waitcnt lgkmcnt(10)\n\t"
"v_mfma_f32_16x16x32_bf16 v[24:27], a[224:227], a[196:199], v[24:27]\n\t"
"v_mfma_f32_16x16x32_bf16 v[24:27], a[252:255], a[192:195], v[24:27]\n\t"
"v_mfma_f32_16x16x32_bf16 v[28:31], a[252:255], a[196:199], v[28:31]\n\t"
"buffer_load_dwordx4 a[252:255], off, s[80:83], s20 offset:1024 sc1\n\t"
"v_mfma_f32_16x16x32_bf16 v[28:31], a[224:227], a[200:203], v[28:31]\n\t"
"s_waitcnt lgkmcnt(4)\n\t"
"v_mfma_f32_16x16x32_bf16 v[28:31], a[220:223], a[204:207], v[28:31]\n\t"
"s_waitcnt lgkmcnt(2)\n\t"
"v_mfma_f32_16x16x32_bf16 v[228:231], v[228:231], a[208:211], 0\n\t"
"buffer_load_dwordx4 a[220:223], off, s[80:83], s20 offset:2048 sc1\n\t"
"v_mfma_f32_16x16x32_bf16 v[224:227], v[224:227], a[208:211], 0\n\t"
"s_waitcnt vmcnt(9) lgkmcnt(0)\n\t"
"s_mov_b32 s20, 1277952\n\t"
"v_mfma_f32_16x16x32_bf16 v[236:239], v[236:239], a[208:211], 0\n\t"
"v_mfma_f32_16x16x32_bf16 v[232:235], v[232:235], a[208:211], 0\n\t"
"buffer_load_dwordx4 a[224:227], off, s[80:83], s20 offset:0 sc1\n\t"
"v_mfma_f32_16x16x32_bf16 v[32:35], a[228:231], a[168:171], v[32:35]\n\t"
"v_mfma_f32_16x16x32_bf16 v[32:35], v[248:251], a[176:179], v[32:35]\n\t"
"v_cvt_pk_bf16_f32 v228, v228, v229\n\t"
"v_cvt_pk_bf16_f32 v229, v230, v231\n\t"
"v_mfma_f32_16x16x32_bf16 v[32:35], v[96:99], a[172:175], v[32:35]\n\t"
"v_cvt_pk_bf16_f32 v230, v224, v225\n\t"
"v_cvt_pk_bf16_f32 v231, v226, v227\n\t"
"v_mfma_f32_16x16x32_bf16 v[36:39], a[228:231], a[172:175], v[36:39]\n\t"
"ds_read_b64_tr_b16 v[224:225], v246 offset:1024\n\t"
"v_cvt_pk_bf16_f32 v236, v236, v237\n\t"
"v_mfma_f32_16x16x32_bf16 v[36:39], v[96:99], a[176:179], v[36:39]\n\t"
"v_cvt_pk_bf16_f32 v237, v238, v239\n\t"
"v_cvt_pk_bf16_f32 v238, v232, v233\n\t"
"v_mfma_f32_16x16x32_bf16 v[36:39], v[248:251], a[180:183], v[36:39]\n\t"
"buffer_load_dwordx4 a[208:211], off, s[80:83], s20 offset:1024 sc1\n\t"
"v_mfma_f32_16x16x32_bf16 v[40:43], v[248:251], a[184:187], v[40:43]\n\t"
"v_cvt_pk_bf16_f32 v239, v234, v235\n\t"
"v_mfma_f32_16x16x32_bf16 v[40:43], v[96:99], a[180:183], v[40:43]\n\t"
"ds_read_b64_tr_b16 v[232:233], v255 offset:1024\n\t"
"v_mfma_f32_16x16x32_bf16 v[40:43], a[228:231], a[176:179], v[40:43]\n\t"
"ds_write_b128 v243, v[228:231] offset:21504\n\t"
"v_mfma_f32_16x16x32_bf16 v[44:47], a[228:231], a[180:183], v[44:47]\n\t"
"v_mfma_f32_16x16x32_bf16 v[44:47], v[96:99], a[184:187], v[44:47]\n\t"
"v_mfma_f32_16x16x32_bf16 v[44:47], v[248:251], a[188:191], v[44:47]\n\t"
"s_barrier\n\t"
"v_mfma_f32_16x16x32_bf16 v[48:51], a[228:231], a[184:187], v[48:51]\n\t"
"s_waitcnt lgkmcnt(2)\n\t"
"ds_read_b64_tr_b16 v[228:229], v246 offset:7168\n\t"
"v_mfma_f32_16x16x32_bf16 v[48:51], v[248:251], a[192:195], v[48:51]\n\t"
"s_waitcnt lgkmcnt(2)\n\t"
"v_mfma_f32_16x16x32_bf16 v[48:51], v[96:99], a[188:191], v[48:51]\n\t"
"ds_write_b128 v243, v[236:239] offset:41472\n\t"
"ds_read_b64_tr_b16 v[236:237], v255 offset:7168\n\t"
"v_mfma_f32_4x4x4_16b_bf16 v[224:227], v[224:225], v[220:221], 0\n\t"
"buffer_load_dwordx4 v[240:243], off, s[80:83], s20 offset:2048 sc1\n\t"
"v_mfma_f32_4x4x4_16b_bf16 v[232:235], v[232:233], v[220:221], 0\n\t"
"s_waitcnt lgkmcnt(2)\n\t"
"v_mfma_f32_4x4x4_16b_bf16 v[228:231], v[228:229], v[220:221], 0\n\t"
"s_waitcnt lgkmcnt(0)\n\t"
"v_mfma_f32_4x4x4_16b_bf16 v[236:239], v[236:237], v[220:221], 0\n\t"
"v_mfma_f32_16x16x32_bf16 v[52:55], a[228:231], a[188:191], v[52:55]\n\t"
"s_mov_b32 s20, 1511424\n\t"
"v_cvt_pk_bf16_f32 v224, v224, v225\n\t"
"v_cvt_pk_bf16_f32 v225, v226, v227\n\t"
"v_mfma_f32_16x16x32_bf16 v[52:55], v[248:251], a[196:199], v[52:55]\n\t"
"v_cvt_pk_bf16_f32 v226, v232, v233\n\t"
"v_cvt_pk_bf16_f32 v227, v234, v235\n\t"
"v_mfma_f32_16x16x32_bf16 v[52:55], v[96:99], a[192:195], v[52:55]\n\t"
"ds_write_b128 v245, v[224:227] offset:1024\n\t"
"v_mfma_f32_16x16x32_bf16 v[56:59], a[228:231], a[192:195], v[56:59]\n\t"
"v_cvt_pk_bf16_f32 v228, v228, v229\n\t"
"v_cvt_pk_bf16_f32 v229, v230, v231\n\t"
"v_mfma_f32_16x16x32_bf16 v[56:59], v[96:99], a[196:199], v[56:59]\n\t"
"ds_read_b64_tr_b16 v[232:233], v246 offset:13312\n\t"
"v_mfma_f32_16x16x32_bf16 v[56:59], v[248:251], a[200:203], v[56:59]\n\t"
"v_cvt_pk_bf16_f32 v230, v236, v237\n\t"
"v_cvt_pk_bf16_f32 v231, v238, v239\n\t"
"v_mfma_f32_16x16x32_bf16 v[60:63], v[96:99], a[200:203], v[60:63]\n\t"
"s_waitcnt vmcnt(9)\n\t"
"ds_read_b64_tr_b16 v[236:237], v255 offset:13312\n\t"
"v_mfma_f32_16x16x32_bf16 v[60:63], a[228:231], a[196:199], v[60:63]\n\t"
"ds_read_b64_tr_b16 v[224:225], v246 offset:20992\n\t"
"v_mfma_f32_16x16x32_bf16 v[60:63], v[248:251], a[204:207], v[60:63]\n\t"
"ds_write_b128 v245, v[228:231] offset:7168\n\t"
"v_mfma_f32_16x16x32_bf16 v[64:67], a[232:235], a[168:171], v[64:67]\n\t"
"buffer_load_dwordx4 v[96:99], off, s[80:83], s20 offset:0 sc1\n\t"
"v_mfma_f32_16x16x32_bf16 v[64:67], a[236:239], a[176:179], v[64:67]\n\t"
"ds_read_b64_tr_b16 v[228:229], v255 offset:20992\n\t"
"v_mfma_f32_16x16x32_bf16 v[64:67], a[240:243], a[172:175], v[64:67]\n\t"
"buffer_load_dwordx4 v[248:251], off, s[80:83], s20 offset:1024 sc1\n\t"
"v_mfma_f32_16x16x32_bf16 v[68:71], a[232:235], a[172:175], v[68:71]\n\t"
"buffer_load_dwordx4 a[228:231], off, s[80:83], s20 offset:2048 sc1\n\t"
"v_mfma_f32_16x16x32_bf16 v[68:71], a[240:243], a[176:179], v[68:71]\n\t"
"s_waitcnt lgkmcnt(3)\n\t"
"ds_read_b128 a[168:171], v222 offset:2304\n\t"
"v_mfma_f32_16x16x32_bf16 v[68:71], a[236:239], a[180:183], v[68:71]\n\t"
"s_waitcnt lgkmcnt(3)\n\t"
"ds_read_b128 a[172:175], v223 offset:8448\n\t"
"v_mfma_f32_4x4x4_16b_bf16 v[232:235], v[232:233], v[220:221], 0\n\t"
"s_waitcnt lgkmcnt(2)\n\t"
"v_mfma_f32_4x4x4_16b_bf16 v[236:239], v[236:237], v[220:221], 0\n\t"
"v_mfma_f32_4x4x4_16b_bf16 v[224:227], v[224:225], v[220:221], 0\n\t"
"v_mfma_f32_4x4x4_16b_bf16 v[228:231], v[228:229], v[220:221], 0\n\t"
"v_mfma_f32_16x16x32_bf16 v[72:75], a[240:243], a[180:183], v[72:75]\n\t"
"v_cvt_pk_bf16_f32 v232, v232, v233\n\t"
"v_cvt_pk_bf16_f32 v233, v234, v235\n\t"
"v_mfma_f32_16x16x32_bf16 v[72:75], a[232:235], a[176:179], v[72:75]\n\t"
"ds_read_b128 a[176:179], v223 offset:14592\n\t"
"v_cvt_pk_bf16_f32 v234, v236, v237\n\t"
"v_mfma_f32_16x16x32_bf16 v[72:75], a[236:239], a[184:187], v[72:75]\n\t"
"v_cvt_pk_bf16_f32 v235, v238, v239\n\t"
"v_cvt_pk_bf16_f32 v224, v224, v225\n\t"
"v_mfma_f32_16x16x32_bf16 v[76:79], a[232:235], a[180:183], v[76:79]\n\t"
"ds_read_b64_tr_b16 v[236:237], v246 offset:27136\n\t"
"v_cvt_pk_bf16_f32 v225, v226, v227\n\t"
"v_mfma_f32_16x16x32_bf16 v[76:79], a[236:239], a[188:191], v[76:79]\n\t"
"ds_read_b128 a[180:183], v222 offset:22272\n\t"
"v_cvt_pk_bf16_f32 v226, v228, v229\n\t"
"v_mfma_f32_16x16x32_bf16 v[76:79], a[240:243], a[184:187], v[76:79]\n\t"
"ds_write_b128 v245, v[232:235] offset:13312\n\t"
"v_cvt_pk_bf16_f32 v227, v230, v231\n\t"
"v_mfma_f32_16x16x32_bf16 v[80:83], a[232:235], a[184:187], v[80:83]\n\t"
"v_mfma_f32_16x16x32_bf16 v[80:83], a[240:243], a[188:191], v[80:83]\n\t"
"ds_read_b64_tr_b16 v[228:229], v255 offset:27136\n\t"
"v_mfma_f32_16x16x32_bf16 v[80:83], a[236:239], a[192:195], v[80:83]\n\t"
"ds_write_b128 v245, v[224:227] offset:20992\n\t"
"v_mfma_f32_16x16x32_bf16 v[84:87], a[236:239], a[196:199], v[84:87]\n\t"
"s_mov_b32 s20, 1523712\n\t"
"v_mfma_f32_16x16x32_bf16 v[84:87], a[240:243], a[192:195], v[84:87]\n\t"
"ds_read_b64_tr_b16 v[232:233], v246 offset:33280\n\t"
"v_mfma_f32_16x16x32_bf16 v[84:87], a[232:235], a[188:191], v[84:87]\n\t"
"ds_read_b64_tr_b16 v[224:225], v255 offset:33280\n\t"
"v_mfma_f32_16x16x32_bf16 v[88:91], a[232:235], a[192:195], v[88:91]\n\t"
"ds_read_b128 a[184:187], v223 offset:28416\n\t"
"v_mfma_f32_16x16x32_bf16 v[88:91], a[240:243], a[196:199], v[88:91]\n\t"
"ds_read_b128 a[188:191], v223 offset:34560\n\t"
"v_mfma_f32_16x16x32_bf16 v[88:91], a[236:239], a[200:203], v[88:91]\n\t"
"ds_read_b128 a[192:195], v222 offset:42240\n\t"
"v_mfma_f32_16x16x32_bf16 v[92:95], a[240:243], a[200:203], v[92:95]\n\t"
"buffer_load_dwordx4 a[240:243], off, s[80:83], s20 offset:0 sc1\n\t"
"v_mfma_f32_16x16x32_bf16 v[92:95], a[232:235], a[196:199], v[92:95]\n\t"
"s_waitcnt lgkmcnt(6)\n\t"
"ds_read_b128 a[196:199], v223 offset:48384\n\t"
"v_mfma_f32_16x16x32_bf16 v[92:95], a[236:239], a[204:207], v[92:95]\n\t"
"s_waitcnt lgkmcnt(5)\n\t"
"ds_read_b128 a[200:203], v223 offset:54528\n\t"
"v_mfma_f32_4x4x4_16b_bf16 v[236:239], v[236:237], v[220:221], 0\n\t"
"s_waitcnt lgkmcnt(5)\n\t"
"v_mfma_f32_4x4x4_16b_bf16 v[228:231], v[228:229], v[220:221], 0\n\t"
"s_waitcnt vmcnt(10)\n\t"
"v_mfma_f32_4x4x4_16b_bf16 v[232:235], v[232:233], v[220:221], 0\n\t"
"v_mfma_f32_4x4x4_16b_bf16 v[224:227], v[224:225], v[220:221], 0\n\t"
"buffer_load_dwordx4 a[232:235], off, s[80:83], s20 offset:1024 sc1\n\t"
"v_mfma_f32_16x16x32_bf16 v[100:103], a[244:247], a[168:171], v[100:103]\n\t"
"buffer_load_dwordx4 a[236:239], off, s[80:83], s20 offset:2048 sc1\n\t"
"v_mfma_f32_16x16x32_bf16 v[100:103], a[216:219], a[176:179], v[100:103]\n\t"
"ds_read_b128 a[204:207], v222 offset:62208\n\t"
"v_cvt_pk_bf16_f32 v236, v236, v237\n\t"
"v_mfma_f32_16x16x32_bf16 v[100:103], a[212:215], a[172:175], v[100:103]\n\t"
"v_cvt_pk_bf16_f32 v237, v238, v239\n\t"
"v_cvt_pk_bf16_f32 v238, v228, v229\n\t"
"v_mfma_f32_16x16x32_bf16 v[104:107], a[244:247], a[172:175], v[104:107]\n\t"
"s_waitcnt lgkmcnt(5)\n\t"
"v_cvt_pk_bf16_f32 v239, v230, v231\n\t"
"v_cvt_pk_bf16_f32 v232, v232, v233\n\t"
"v_mfma_f32_16x16x32_bf16 v[104:107], a[212:215], a[176:179], v[104:107]\n\t"
"v_cvt_pk_bf16_f32 v233, v234, v235\n\t"
"v_cvt_pk_bf16_f32 v234, v224, v225\n\t"
"v_mfma_f32_16x16x32_bf16 v[104:107], a[216:219], a[180:183], v[104:107]\n\t"
"ds_write_b128 v245, v[236:239] offset:27136\n\t"
"v_cvt_pk_bf16_f32 v235, v226, v227\n\t"
"v_mfma_f32_16x16x32_bf16 v[108:111], a[216:219], a[184:187], v[108:111]\n\t"
"s_waitcnt lgkmcnt(5)\n\t"
"v_mfma_f32_16x16x32_bf16 v[108:111], a[212:215], a[180:183], v[108:111]\n\t"
"v_mfma_f32_16x16x32_bf16 v[108:111], a[244:247], a[176:179], v[108:111]\n\t"
"ds_write_b128 v245, v[232:235] offset:33280\n\t"
"v_mfma_f32_16x16x32_bf16 v[112:115], a[244:247], a[180:183], v[112:115]\n\t"
"s_waitcnt lgkmcnt(5)\n\t"
"v_mfma_f32_16x16x32_bf16 v[112:115], a[212:215], a[184:187], v[112:115]\n\t"
"ds_read_b64_tr_b16 v[228:229], v246 offset:40960\n\t"
"v_mfma_f32_16x16x32_bf16 v[112:115], a[216:219], a[188:191], v[112:115]\n\t"
"ds_read_b64_tr_b16 v[224:225], v255 offset:40960\n\t"
"v_mfma_f32_16x16x32_bf16 v[116:119], a[216:219], a[192:195], v[116:119]\n\t"
"s_waitcnt lgkmcnt(6)\n\t"
"ds_read_b64_tr_b16 v[236:237], v246 offset:47104\n\t"
"v_mfma_f32_16x16x32_bf16 v[116:119], a[212:215], a[188:191], v[116:119]\n\t"
"ds_read_b64_tr_b16 v[232:233], v255 offset:47104\n\t"
"v_mfma_f32_16x16x32_bf16 v[116:119], a[244:247], a[184:187], v[116:119]\n\t"
"v_mfma_f32_16x16x32_bf16 v[120:123], a[244:247], a[188:191], v[120:123]\n\t"
"s_waitcnt lgkmcnt(7)\n\t"
"s_mov_b32 s20, 1536000\n\t"
"v_mfma_f32_16x16x32_bf16 v[120:123], a[212:215], a[192:195], v[120:123]\n\t"
"v_mfma_f32_16x16x32_bf16 v[120:123], a[216:219], a[196:199], v[120:123]\n\t"
"v_mfma_f32_16x16x32_bf16 v[124:127], a[216:219], a[200:203], v[124:127]\n\t"
"s_waitcnt lgkmcnt(6)\n\t"
"v_mfma_f32_16x16x32_bf16 v[124:127], a[212:215], a[196:199], v[124:127]\n\t"
"v_mfma_f32_16x16x32_bf16 v[124:127], a[244:247], a[192:195], v[124:127]\n\t"
"v_mfma_f32_16x16x32_bf16 v[128:131], a[244:247], a[196:199], v[128:131]\n\t"
"buffer_load_dwordx4 a[244:247], off, s[80:83], s20 offset:0 sc1\n\t"
"v_mfma_f32_16x16x32_bf16 v[128:131], a[212:215], a[200:203], v[128:131]\n\t"
"s_waitcnt lgkmcnt(2)\n\t"
"v_mfma_f32_16x16x32_bf16 v[128:131], a[216:219], a[204:207], v[128:131]\n\t"
"s_waitcnt lgkmcnt(1)\n\t"
"v_mfma_f32_4x4x4_16b_bf16 v[228:231], v[228:229], v[220:221], 0\n\t"
"s_waitcnt lgkmcnt(0)\n\t"
"v_mfma_f32_4x4x4_16b_bf16 v[224:227], v[224:225], v[220:221], 0\n\t"
"s_waitcnt vmcnt(10)\n\t"
"v_mfma_f32_4x4x4_16b_bf16 v[236:239], v[236:237], v[220:221], 0\n\t"
"v_mfma_f32_4x4x4_16b_bf16 v[232:235], v[232:233], v[220:221], 0\n\t"
"buffer_load_dwordx4 a[212:215], off, s[80:83], s20 offset:1024 sc1\n\t"
"v_mfma_f32_16x16x32_bf16 v[132:135], a[248:251], a[168:171], v[132:135]\n\t"
"buffer_load_dwordx4 a[216:219], off, s[80:83], s20 offset:2048 sc1\n\t"
"v_mfma_f32_16x16x32_bf16 v[132:135], a[220:223], a[176:179], v[132:135]\n\t"
"v_cvt_pk_bf16_f32 v228, v228, v229\n\t"
"v_cvt_pk_bf16_f32 v229, v230, v231\n\t"
"v_mfma_f32_16x16x32_bf16 v[132:135], a[252:255], a[172:175], v[132:135]\n\t"
"v_cvt_pk_bf16_f32 v230, v224, v225\n\t"
"v_cvt_pk_bf16_f32 v231, v226, v227\n\t"
"v_mfma_f32_16x16x32_bf16 v[136:139], a[248:251], a[172:175], v[136:139]\n\t"
"ds_read_b64_tr_b16 v[224:225], v246 offset:53248\n\t"
"v_cvt_pk_bf16_f32 v236, v236, v237\n\t"
"v_mfma_f32_16x16x32_bf16 v[136:139], a[252:255], a[176:179], v[136:139]\n\t"
"s_mov_b32 s20, 1548288\n\t"
"v_cvt_pk_bf16_f32 v237, v238, v239\n\t"
"v_cvt_pk_bf16_f32 v238, v232, v233\n\t"
"v_mfma_f32_16x16x32_bf16 v[136:139], a[220:223], a[180:183], v[136:139]\n\t"
"ds_write_b128 v245, v[228:231] offset:40960\n\t"
"v_cvt_pk_bf16_f32 v239, v234, v235\n\t"
"v_mfma_f32_16x16x32_bf16 v[140:143], a[220:223], a[184:187], v[140:143]\n\t"
"v_mfma_f32_16x16x32_bf16 v[140:143], a[252:255], a[180:183], v[140:143]\n\t"
"ds_read_b64_tr_b16 v[232:233], v255 offset:53248\n\t"
"v_mfma_f32_16x16x32_bf16 v[140:143], a[248:251], a[176:179], v[140:143]\n\t"
"ds_write_b128 v245, v[236:239] offset:47104\n\t"
"v_mfma_f32_16x16x32_bf16 v[144:147], a[248:251], a[180:183], v[144:147]\n\t"
"v_mfma_f32_16x16x32_bf16 v[144:147], a[252:255], a[184:187], v[144:147]\n\t"
"buffer_load_dwordx4 v[236:239], off, s[80:83], s20 offset:0 sc1\n\t"
"v_mfma_f32_16x16x32_bf16 v[144:147], a[220:223], a[188:191], v[144:147]\n\t"
"v_mfma_f32_16x16x32_bf16 v[148:151], a[248:251], a[184:187], v[148:151]\n\t"
"s_waitcnt lgkmcnt(1)\n\t"
"v_mfma_f32_16x16x32_bf16 v[148:151], a[220:223], a[192:195], v[148:151]\n\t"
"v_mfma_f32_16x16x32_bf16 v[148:151], a[252:255], a[188:191], v[148:151]\n\t"
"v_mfma_f32_4x4x4_16b_bf16 v[224:227], v[224:225], v[220:221], 0\n\t"
"v_mfma_f32_4x4x4_16b_bf16 v[232:235], v[232:233], v[220:221], 0\n\t"
"s_waitcnt vmcnt(10)\n\t"
"v_mfma_f32_16x16x32_bf16 v[152:155], a[248:251], a[188:191], v[152:155]\n\t"
"Lpg8_%=:\n\t"
"s_cbranch_vccnz Lpg12_%=\n\t"
"v_mfma_f32_16x16x32_bf16 v[152:155], a[220:223], a[196:199], v[152:155]\n\t"
"v_cvt_pk_bf16_f32 v224, v224, v225\n\t"
"v_cvt_pk_bf16_f32 v225, v226, v227\n\t"
"v_mfma_f32_16x16x32_bf16 v[152:155], a[252:255], a[192:195], v[152:155]\n\t"
"v_cvt_pk_bf16_f32 v226, v232, v233\n\t"
"v_cvt_pk_bf16_f32 v227, v234, v235\n\t"
"v_mfma_f32_16x16x32_bf16 v[156:159], a[248:251], a[192:195], v[156:159]\n\t"
"v_mfma_f32_16x16x32_bf16 v[156:159], a[252:255], a[196:199], v[156:159]\n\t"
"v_mfma_f32_16x16x32_bf16 v[156:159], a[220:223], a[200:203], v[156:159]\n\t"
"ds_write_b128 v245, v[224:227] offset:53248\n\t"
"v_mfma_f32_16x16x32_bf16 v[160:163], a[252:255], a[200:203], v[160:163]\n\t"
"buffer_load_dwordx4 a[252:255], off, s[80:83], s20 offset:1024 sc1\n\t"
"v_mfma_f32_16x16x32_bf16 v[160:163], a[248:251], a[196:199], v[160:163]\n\t"
"v_mfma_f32_16x16x32_bf16 v[160:163], a[220:223], a[204:207], v[160:163]\n\t"
"v_mfma_f32_16x16x32_bf16 v[164:167], a[224:227], a[168:171], v[164:167]\n\t"
"buffer_load_dwordx4 a[220:223], off, s[80:83], s20 offset:2048 sc1\n\t"
"v_mfma_f32_16x16x32_bf16 v[164:167], v[240:243], a[176:179], v[164:167]\n\t"
"s_mov_b32 s20, 1560576\n\t"
"v_mfma_f32_16x16x32_bf16 v[164:167], a[208:211], a[172:175], v[164:167]\n\t"
"s_waitcnt lgkmcnt(0)\n\t"
"s_barrier\n\t"
"v_mfma_f32_16x16x32_bf16 v[168:171], a[224:227], a[172:175], v[168:171]\n\t"
"ds_read_b128 a[168:171], v222 offset:2560\n\t"
"v_mfma_f32_16x16x32_bf16 v[168:171], a[208:211], a[176:179], v[168:171]\n\t"
"buffer_load_dwordx4 a[248:251], off, s[80:83], s20 offset:0 sc1\n\t"
"v_mfma_f32_16x16x32_bf16 v[168:171], v[240:243], a[180:183], v[168:171]\n\t"
"ds_read_b128 a[172:175], v223 offset:8704\n\t"
"v_mfma_f32_16x16x32_bf16 v[172:175], v[240:243], a[184:187], v[172:175]\n\t"
"v_mfma_f32_16x16x32_bf16 v[172:175], a[208:211], a[180:183], v[172:175]\n\t"
"v_mfma_f32_16x16x32_bf16 v[172:175], a[224:227], a[176:179], v[172:175]\n\t"
"ds_read_b128 a[176:179], v223 offset:14848\n\t"
"v_mfma_f32_16x16x32_bf16 v[176:179], a[224:227], a[180:183], v[176:179]\n\t"
"ds_read_b128 a[180:183], v222 offset:22528\n\t"
"v_mfma_f32_16x16x32_bf16 v[176:179], a[208:211], a[184:187], v[176:179]\n\t"
"v_mfma_f32_16x16x32_bf16 v[176:179], v[240:243], a[188:191], v[176:179]\n\t"
"v_mfma_f32_16x16x32_bf16 v[180:183], v[240:243], a[192:195], v[180:183]\n\t"
"v_mfma_f32_16x16x32_bf16 v[180:183], a[208:211], a[188:191], v[180:183]\n\t"
"v_mfma_f32_16x16x32_bf16 v[180:183], a[224:227], a[184:187], v[180:183]\n\t"
"ds_read_b128 a[184:187], v223 offset:28672\n\t"
"v_mfma_f32_16x16x32_bf16 v[184:187], a[224:227], a[188:191], v[184:187]\n\t"
"ds_read_b128 a[188:191], v223 offset:34816\n\t"
"v_mfma_f32_16x16x32_bf16 v[184:187], a[208:211], a[192:195], v[184:187]\n\t"
"v_mfma_f32_16x16x32_bf16 v[184:187], v[240:243], a[196:199], v[184:187]\n\t"
"v_mfma_f32_16x16x32_bf16 v[188:191], v[240:243], a[200:203], v[188:191]\n\t"
"v_mfma_f32_16x16x32_bf16 v[188:191], a[208:211], a[196:199], v[188:191]\n\t"
"v_mfma_f32_16x16x32_bf16 v[188:191], a[224:227], a[192:195], v[188:191]\n\t"
"ds_read_b128 a[192:195], v222 offset:42496\n\t"
"v_mfma_f32_16x16x32_bf16 v[192:195], a[224:227], a[196:199], v[192:195]\n\t"
"buffer_load_dwordx4 a[224:227], off, s[80:83], s20 offset:1024 sc1\n\t"
"v_mfma_f32_16x16x32_bf16 v[192:195], a[208:211], a[200:203], v[192:195]\n\t"
"s_waitcnt vmcnt(11) lgkmcnt(4)\n\t"
"ds_read_b128 a[196:199], v223 offset:48640\n\t"
"v_mfma_f32_16x16x32_bf16 v[192:195], v[240:243], a[204:207], v[192:195]\n\t"
"ds_read_b128 a[200:203], v223 offset:54784\n\t"
"Lst11_%=:\n\t"
"v_mfma_f32_16x16x32_bf16 v[0:3], v[96:99], a[168:171], v[0:3]\n\t"
"buffer_load_dwordx4 v[240:243], off, s[80:83], s20 offset:2048 sc1\n\t"
"v_mfma_f32_16x16x32_bf16 v[0:3], a[228:231], a[176:179], v[0:3]\n\t"
"s_waitcnt lgkmcnt(5)\n\t"
"ds_read_b128 a[204:207], v222 offset:62464\n\t"
"s_mov_b32 s20, 1572864\n\t"
"v_mfma_f32_16x16x32_bf16 v[0:3], v[248:251], a[172:175], v[0:3]\n\t"
"v_mfma_f32_16x16x32_bf16 v[4:7], v[96:99], a[172:175], v[4:7]\n\t"
"buffer_load_dwordx4 a[208:211], off, s[80:83], s20 offset:0 sc1\n\t"
"v_mfma_f32_16x16x32_bf16 v[4:7], v[248:251], a[176:179], v[4:7]\n\t"
"s_waitcnt lgkmcnt(5)\n\t"
"v_mfma_f32_16x16x32_bf16 v[4:7], a[228:231], a[180:183], v[4:7]\n\t"
"v_mfma_f32_16x16x32_bf16 v[8:11], a[228:231], a[184:187], v[8:11]\n\t"
"s_waitcnt lgkmcnt(4)\n\t"
"v_mfma_f32_16x16x32_bf16 v[8:11], v[248:251], a[180:183], v[8:11]\n\t"
"v_mfma_f32_16x16x32_bf16 v[8:11], v[96:99], a[176:179], v[8:11]\n\t"
"v_mfma_f32_16x16x32_bf16 v[12:15], v[96:99], a[180:183], v[12:15]\n\t"
"s_waitcnt lgkmcnt(3)\n\t"
"v_mfma_f32_16x16x32_bf16 v[12:15], v[248:251], a[184:187], v[12:15]\n\t"
"v_mfma_f32_16x16x32_bf16 v[12:15], a[228:231], a[188:191], v[12:15]\n\t"
"v_mfma_f32_16x16x32_bf16 v[16:19], a[228:231], a[192:195], v[16:19]\n\t"
"s_waitcnt lgkmcnt(2)\n\t"
"v_mfma_f32_16x16x32_bf16 v[16:19], v[248:251], a[188:191], v[16:19]\n\t"
"v_mfma_f32_16x16x32_bf16 v[16:19], v[96:99], a[184:187], v[16:19]\n\t"
"v_mfma_f32_16x16x32_bf16 v[20:23], v[96:99], a[188:191], v[20:23]\n\t"
"s_waitcnt lgkmcnt(1)\n\t"
"v_mfma_f32_16x16x32_bf16 v[20:23], v[248:251], a[192:195], v[20:23]\n\t"
"v_mfma_f32_16x16x32_bf16 v[20:23], a[228:231], a[196:199], v[20:23]\n\t"
"v_mfma_f32_16x16x32_bf16 v[24:27], a[228:231], a[200:203], v[24:27]\n\t"
"s_waitcnt lgkmcnt(0)\n\t"
"s_add_u32 s21, s21, 6291456\n\t"
"v_add_u32 v254, 6291456, v254\n\t"
"v_mfma_f32_16x16x32_bf16 v[24:27], v[248:251], a[196:199], v[24:27]\n\t"
"v_mfma_f32_16x16x32_bf16 v[24:27], v[96:99], a[192:195], v[24:27]\n\t"
"v_mfma_f32_16x16x32_bf16 v[28:31], v[96:99], a[196:199], v[28:31]\n\t"
"buffer_load_dwordx4 v[96:99], off, s[80:83], s20 offset:1024 sc1\n\t"
"v_mfma_f32_16x16x32_bf16 v[28:31], v[248:251], a[200:203], v[28:31]\n\t"
"s_waitcnt vmcnt(11)\n\t"
"v_mfma_f32_16x16x32_bf16 v[28:31], a[228:231], a[204:207], v[28:31]\n\t"
"v_mfma_f32_16x16x32_bf16 v[32:35], a[240:243], a[168:171], v[32:35]\n\t"
"buffer_load_dwordx4 v[248:251], off, s[80:83], s20 offset:2048 sc1\n\t"
"v_mfma_f32_16x16x32_bf16 v[32:35], a[236:239], a[176:179], v[32:35]\n\t"
"s_mov_b32 s20, 1290240\n\t"
"v_mfma_f32_16x16x32_bf16 v[32:35], a[232:235], a[172:175], v[32:35]\n\t"
"v_mfma_f32_16x16x32_bf16 v[36:39], a[240:243], a[172:175], v[36:39]\n\t"
"buffer_load_dwordx4 a[228:231], off, s[80:83], s20 offset:0 sc1\n\t"
"v_mfma_f32_16x16x32_bf16 v[36:39], a[232:235], a[176:179], v[36:39]\n\t"
"v_mfma_f32_16x16x32_bf16 v[36:39], a[236:239], a[180:183], v[36:39]\n\t"
"v_mfma_f32_16x16x32_bf16 v[40:43], a[236:239], a[184:187], v[40:43]\n\t"
"v_mfma_f32_16x16x32_bf16 v[40:43], a[232:235], a[180:183], v[40:43]\n\t"
"v_mfma_f32_16x16x32_bf16 v[40:43], a[240:243], a[176:179], v[40:43]\n\t"
"v_mfma_f32_16x16x32_bf16 v[44:47], a[240:243], a[180:183], v[44:47]\n\t"
"v_mfma_f32_16x16x32_bf16 v[44:47], a[232:235], a[184:187], v[44:47]\n\t"
"v_mfma_f32_16x16x32_bf16 v[44:47], a[236:239], a[188:191], v[44:47]\n\t"
"v_mfma_f32_16x16x32_bf16 v[48:51], a[236:239], a[192:195], v[48:51]\n\t"
"v_mfma_f32_16x16x32_bf16 v[48:51], a[232:235], a[188:191], v[48:51]\n\t"
"v_mfma_f32_16x16x32_bf16 v[48:51], a[240:243], a[184:187], v[48:51]\n\t"
"v_mfma_f32_16x16x32_bf16 v[52:55], a[240:243], a[188:191], v[52:55]\n\t"
"v_mfma_f32_16x16x32_bf16 v[52:55], a[232:235], a[192:195], v[52:55]\n\t"
"v_mfma_f32_16x16x32_bf16 v[52:55], a[236:239], a[196:199], v[52:55]\n\t"
"v_mfma_f32_16x16x32_bf16 v[56:59], a[236:239], a[200:203], v[56:59]\n\t"
"v_mfma_f32_16x16x32_bf16 v[56:59], a[232:235], a[196:199], v[56:59]\n\t"
"v_mfma_f32_16x16x32_bf16 v[56:59], a[240:243], a[192:195], v[56:59]\n\t"
"v_mfma_f32_16x16x32_bf16 v[60:63], a[240:243], a[196:199], v[60:63]\n\t"
"buffer_load_dwordx4 a[240:243], off, s[80:83], s20 offset:1024 sc1\n\t"
"v_mfma_f32_16x16x32_bf16 v[60:63], a[232:235], a[200:203], v[60:63]\n\t"
"s_waitcnt vmcnt(11)\n\t"
"v_mfma_f32_16x16x32_bf16 v[60:63], a[236:239], a[204:207], v[60:63]\n\t"
"v_mfma_f32_16x16x32_bf16 v[64:67], a[244:247], a[168:171], v[64:67]\n\t"
"buffer_load_dwordx4 a[232:235], off, s[80:83], s20 offset:2048 sc1\n\t"
"v_mfma_f32_16x16x32_bf16 v[64:67], a[216:219], a[176:179], v[64:67]\n\t"
"s_mov_b32 s20, 1302528\n\t"
"ds_read_b128 a[168:171], v222 offset:2816\n\t"
"v_mfma_f32_16x16x32_bf16 v[64:67], a[212:215], a[172:175], v[64:67]\n\t"
"v_mfma_f32_16x16x32_bf16 v[68:71], a[244:247], a[172:175], v[68:71]\n\t"
"buffer_load_dwordx4 a[236:239], off, s[80:83], s20 offset:0 sc1\n\t"
"v_mfma_f32_16x16x32_bf16 v[68:71], a[212:215], a[176:179], v[68:71]\n\t"
"ds_read_b128 a[172:175], v223 offset:8960\n\t"
"v_mfma_f32_16x16x32_bf16 v[68:71], a[216:219], a[180:183], v[68:71]\n\t"
"v_mfma_f32_16x16x32_bf16 v[72:75], a[216:219], a[184:187], v[72:75]\n\t"
"v_mfma_f32_16x16x32_bf16 v[72:75], a[212:215], a[180:183], v[72:75]\n\t"
"v_mfma_f32_16x16x32_bf16 v[72:75], a[244:247], a[176:179], v[72:75]\n\t"
"ds_read_b128 a[176:179], v223 offset:15104\n\t"
"v_mfma_f32_16x16x32_bf16 v[76:79], a[244:247], a[180:183], v[76:79]\n\t"
"ds_read_b128 a[180:183], v222 offset:22784\n\t"
"v_mfma_f32_16x16x32_bf16 v[76:79], a[212:215], a[184:187], v[76:79]\n\t"
"v_mfma_f32_16x16x32_bf16 v[76:79], a[216:219], a[188:191], v[76:79]\n\t"
"v_mfma_f32_16x16x32_bf16 v[80:83], a[216:219], a[192:195], v[80:83]\n\t"
"v_mfma_f32_16x16x32_bf16 v[80:83], a[212:215], a[188:191], v[80:83]\n\t"
"v_mfma_f32_16x16x32_bf16 v[80:83], a[244:247], a[184:187], v[80:83]\n\t"
"ds_read_b128 a[184:187], v223 offset:28928\n\t"
"v_mfma_f32_16x16x32_bf16 v[84:87], a[244:247], a[188:191], v[84:87]\n\t"
"ds_read_b128 a[188:191], v223 offset:35072\n\t"
"v_mfma_f32_16x16x32_bf16 v[84:87], a[212:215], a[192:195], v[84:87]\n\t"
"v_mfma_f32_16x16x32_bf16 v[84:87], a[216:219], a[196:199], v[84:87]\n\t"
"v_mfma_f32_16x16x32_bf16 v[88:91], a[216:219], a[200:203], v[88:91]\n\t"
"v_mfma_f32_16x16x32_bf16 v[88:91], a[212:215], a[196:199], v[88:91]\n\t"
"v_mfma_f32_16x16x32_bf16 v[88:91], a[244:247], a[192:195], v[88:91]\n\t"
"ds_read_b128 a[192:195], v222 offset:42752\n\t"
"v_mfma_f32_16x16x32_bf16 v[92:95], a[244:247], a[196:199], v[92:95]\n\t"
"buffer_load_dwordx4 a[244:247], off, s[80:83], s20 offset:1024 sc1\n\t"
"v_mfma_f32_16x16x32_bf16 v[92:95], a[212:215], a[200:203], v[92:95]\n\t"
"s_waitcnt vmcnt(11) lgkmcnt(4)\n\t"
"ds_read_b128 a[196:199], v223 offset:48896\n\t"
"v_mfma_f32_16x16x32_bf16 v[92:95], a[216:219], a[204:207], v[92:95]\n\t"
"ds_read_b128 a[200:203], v223 offset:55040\n\t"
"v_mfma_f32_16x16x32_bf16 v[100:103], v[236:239], a[168:171], v[100:103]\n\t"
"s_waitcnt lgkmcnt(5)\n\t"
"ds_read_b128 a[204:207], v222 offset:62720\n\t"
"v_mfma_f32_16x16x32_bf16 v[100:103], a[220:223], a[176:179], v[100:103]\n\t"
"buffer_load_dwordx4 a[212:215], off, s[80:83], s20 offset:2048 sc1\n\t"
"v_mfma_f32_16x16x32_bf16 v[100:103], a[252:255], a[172:175], v[100:103]\n\t"
"s_mov_b32 s20, 1314816\n\t"
"s_add_u32 m0, s26, 512\n\t"
"v_mfma_f32_16x16x32_bf16 v[104:107], v[236:239], a[172:175], v[104:107]\n\t"
"buffer_load_dwordx4 a[216:219], off, s[80:83], s20 offset:0 sc1\n\t"
"v_mfma_f32_16x16x32_bf16 v[104:107], a[252:255], a[176:179], v[104:107]\n\t"
"s_waitcnt lgkmcnt(5)\n\t"
"v_mfma_f32_16x16x32_bf16 v[104:107], a[220:223], a[180:183], v[104:107]\n\t"
"s_waitcnt lgkmcnt(0)\n\t"
"s_barrier\n\t"
"v_mfma_f32_16x16x32_bf16 v[108:111], a[220:223], a[184:187], v[108:111]\n\t"
"v_fma_f32 v0, v100, s52, v0\n\t"
"v_mfma_f32_16x16x32_bf16 v[108:111], a[252:255], a[180:183], v[108:111]\n\t"
"v_fma_f32 v1, v101, s52, v1\n\t"
"v_fma_f32 v2, v102, s52, v2\n\t"
"v_mfma_f32_16x16x32_bf16 v[108:111], v[236:239], a[176:179], v[108:111]\n\t"
"s_mov_b64 exec, s[36:37]\n\t"
"buffer_load_dwordx4 v254, s[12:15], s27 offen offset:0 nt lds\n\t"
"s_mov_b64 exec, -1\n\t"
"s_add_u32 m0, s24, 22016\n\t"
"s_add_u32 s28, s21, 592896\n\t"
"v_mfma_f32_16x16x32_bf16 v[112:115], v[236:239], a[180:183], v[112:115]\n\t"
"buffer_load_dwordx4 v247, s[12:15], s28 offen offset:0 nt lds\n\t"
"v_mfma_f32_16x16x32_bf16 v[112:115], a[252:255], a[184:187], v[112:115]\n\t"
"s_add_u32 m0, s24, 28160\n\t"
"s_add_u32 s28, s21, 789504\n\t"
"v_fma_f32 v3, v103, s52, v3\n\t"
"v_mfma_f32_16x16x32_bf16 v[112:115], a[220:223], a[188:191], v[112:115]\n\t"
"buffer_load_dwordx4 v247, s[12:15], s28 offen offset:0 nt lds\n\t"
"s_add_u32 m0, s24, 34304\n\t"
"s_add_u32 s28, s21, 986112\n\t"
"v_mfma_f32_16x16x32_bf16 v[116:119], a[220:223], a[192:195], v[116:119]\n\t"
"buffer_load_dwordx4 v247, s[12:15], s28 offen offset:0 nt lds\n\t"
"v_mfma_f32_16x16x32_bf16 v[116:119], a[252:255], a[188:191], v[116:119]\n\t"
"s_add_u32 m0, s24, 41984\n\t"
"s_add_u32 s28, s21, 1182720\n\t"
"v_fma_f32 v100, v0, s54, -v100\n\t"
"v_mfma_f32_16x16x32_bf16 v[116:119], v[236:239], a[184:187], v[116:119]\n\t"
"v_fma_f32 v101, v1, s54, -v101\n\t"
"v_fma_f32 v102, v2, s54, -v102\n\t"
"v_mfma_f32_16x16x32_bf16 v[120:123], v[236:239], a[188:191], v[120:123]\n\t"
"v_fma_f32 v103, v3, s54, -v103\n\t"
"v_fma_f32 v4, v104, s52, v4\n\t"
"v_mfma_f32_16x16x32_bf16 v[120:123], a[252:255], a[192:195], v[120:123]\n\t"
"v_fma_f32 v5, v105, s52, v5\n\t"
"v_fma_f32 v6, v106, s52, v6\n\t"
"v_mfma_f32_16x16x32_bf16 v[120:123], a[220:223], a[196:199], v[120:123]\n\t"
"buffer_load_dwordx4 v247, s[12:15], s28 offen offset:0 nt lds\n\t"
"s_add_u32 m0, s24, 48128\n\t"
"s_add_u32 s28, s21, 1379328\n\t"
"v_mfma_f32_16x16x32_bf16 v[124:127], a[220:223], a[200:203], v[124:127]\n\t"
"buffer_load_dwordx4 v247, s[12:15], s28 offen offset:0 nt lds\n\t"
"v_mfma_f32_16x16x32_bf16 v[124:127], a[252:255], a[196:199], v[124:127]\n\t"
"v_fma_f32 v7, v107, s52, v7\n\t"
"v_fma_f32 v104, v4, s54, -v104\n\t"
"v_mfma_f32_16x16x32_bf16 v[124:127], v[236:239], a[192:195], v[124:127]\n\t"
"v_fma_f32 v105, v5, s54, -v105\n\t"
"v_fma_f32 v106, v6, s54, -v106\n\t"
"v_mfma_f32_16x16x32_bf16 v[128:131], v[236:239], a[196:199], v[128:131]\n\t"
"s_waitcnt vmcnt(16)\n\t"
"v_fma_f32 v107, v7, s54, -v107\n\t"
"v_fma_f32 v8, v108, s52, v8\n\t"
"v_mfma_f32_16x16x32_bf16 v[128:131], a[252:255], a[200:203], v[128:131]\n\t"
"v_fma_f32 v9, v109, s52, v9\n\t"
"v_fma_f32 v10, v110, s52, v10\n\t"
"v_mfma_f32_16x16x32_bf16 v[128:131], a[220:223], a[204:207], v[128:131]\n\t"
"v_fma_f32 v11, v111, s52, v11\n\t"
"v_fma_f32 v108, v8, s54, -v108\n\t"
"v_mfma_f32_16x16x32_bf16 v[132:135], a[248:251], a[168:171], v[132:135]\n\t"
"v_fma_f32 v109, v9, s54, -v109\n\t"
"v_fma_f32 v110, v10, s54, -v110\n\t"
"v_mfma_f32_16x16x32_bf16 v[132:135], v[240:243], a[176:179], v[132:135]\n\t"
"v_fma_f32 v111, v11, s54, -v111\n\t"
"v_fma_f32 v12, v112, s52, v12\n\t"
"v_mfma_f32_16x16x32_bf16 v[132:135], a[224:227], a[172:175], v[132:135]\n\t"
"v_fma_f32 v13, v113, s52, v13\n\t"
"v_fma_f32 v14, v114, s52, v14\n\t"
"v_mfma_f32_16x16x32_bf16 v[136:139], a[248:251], a[172:175], v[136:139]\n\t"
"v_fma_f32 v15, v115, s52, v15\n\t"
"v_fma_f32 v112, v12, s54, -v112\n\t"
"v_mfma_f32_16x16x32_bf16 v[136:139], a[224:227], a[176:179], v[136:139]\n\t"
"v_fma_f32 v113, v13, s54, -v113\n\t"
"v_fma_f32 v114, v14, s54, -v114\n\t"
"v_mfma_f32_16x16x32_bf16 v[136:139], v[240:243], a[180:183], v[136:139]\n\t"
"v_fma_f32 v115, v15, s54, -v115\n\t"
"v_fma_f32 v16, v116, s52, v16\n\t"
"v_mfma_f32_16x16x32_bf16 v[140:143], v[240:243], a[184:187], v[140:143]\n\t"
"v_fma_f32 v17, v117, s52, v17\n\t"
"v_fma_f32 v18, v118, s52, v18\n\t"
"v_mfma_f32_16x16x32_bf16 v[140:143], a[224:227], a[180:183], v[140:143]\n\t"
"v_fma_f32 v19, v119, s52, v19\n\t"
"v_fma_f32 v116, v16, s54, -v116\n\t"
"v_mfma_f32_16x16x32_bf16 v[140:143], a[248:251], a[176:179], v[140:143]\n\t"
"v_fma_f32 v117, v17, s54, -v117\n\t"
"v_fma_f32 v118, v18, s54, -v118\n\t"
"v_mfma_f32_16x16x32_bf16 v[144:147], a[248:251], a[180:183], v[144:147]\n\t"
"v_fma_f32 v119, v19, s54, -v119\n\t"
"v_fma_f32 v20, v120, s52, v20\n\t"
"v_mfma_f32_16x16x32_bf16 v[144:147], a[224:227], a[184:187], v[144:147]\n\t"
"v_fma_f32 v21, v121, s52, v21\n\t"
"v_fma_f32 v22, v122, s52, v22\n\t"
"v_mfma_f32_16x16x32_bf16 v[144:147], v[240:243], a[188:191], v[144:147]\n\t"
"v_fma_f32 v23, v123, s52, v23\n\t"
"v_fma_f32 v120, v20, s54, -v120\n\t"
"v_mfma_f32_16x16x32_bf16 v[148:151], v[240:243], a[192:195], v[148:151]\n\t"
"v_fma_f32 v121, v21, s54, -v121\n\t"
"v_fma_f32 v122, v22, s54, -v122\n\t"
"v_mfma_f32_16x16x32_bf16 v[148:151], a[224:227], a[188:191], v[148:151]\n\t"
"v_fma_f32 v123, v23, s54, -v123\n\t"
"v_fma_f32 v24, v124, s52, v24\n\t"
"v_mfma_f32_16x16x32_bf16 v[148:151], a[248:251], a[184:187], v[148:151]\n\t"
"v_fma_f32 v25, v125, s52, v25\n\t"
"v_fma_f32 v26, v126, s52, v26\n\t"
"v_mfma_f32_16x16x32_bf16 v[152:155], a[248:251], a[188:191], v[152:155]\n\t"
"v_fma_f32 v27, v127, s52, v27\n\t"
"v_fma_f32 v124, v24, s54, -v124\n\t"
"v_mfma_f32_16x16x32_bf16 v[152:155], a[224:227], a[192:195], v[152:155]\n\t"
"v_fma_f32 v125, v25, s54, -v125\n\t"
"v_fma_f32 v126, v26, s54, -v126\n\t"
"v_mfma_f32_16x16x32_bf16 v[152:155], v[240:243], a[196:199], v[152:155]\n\t"
"v_fma_f32 v127, v27, s54, -v127\n\t"
"v_fma_f32 v28, v128, s52, v28\n\t"
"v_mfma_f32_16x16x32_bf16 v[156:159], v[240:243], a[200:203], v[156:159]\n\t"
"v_fma_f32 v29, v129, s52, v29\n\t"
"v_fma_f32 v30, v130, s52, v30\n\t"
"v_mfma_f32_16x16x32_bf16 v[156:159], a[224:227], a[196:199], v[156:159]\n\t"
"v_fma_f32 v31, v131, s52, v31\n\t"
"v_fma_f32 v128, v28, s54, -v128\n\t"
"v_mfma_f32_16x16x32_bf16 v[156:159], a[248:251], a[192:195], v[156:159]\n\t"
"v_fma_f32 v129, v29, s54, -v129\n\t"
"v_fma_f32 v130, v30, s54, -v130\n\t"
"v_mfma_f32_16x16x32_bf16 v[160:163], a[248:251], a[196:199], v[160:163]\n\t"
"s_waitcnt vmcnt(13)\n\t"
"v_fma_f32 v131, v31, s54, -v131\n\t"
"v_fma_f32 v32, v132, s52, v32\n\t"
"v_mfma_f32_16x16x32_bf16 v[160:163], a[224:227], a[200:203], v[160:163]\n\t"
"v_fma_f32 v33, v133, s52, v33\n\t"
"v_fma_f32 v34, v134, s52, v34\n\t"
"v_mfma_f32_16x16x32_bf16 v[160:163], v[240:243], a[204:207], v[160:163]\n\t"
"buffer_load_dwordx4 v[236:239], off, s[80:83], s20 offset:1024 sc1\n\t"
"v_mfma_f32_16x16x32_bf16 v[164:167], a[208:211], a[168:171], v[164:167]\n\t"
"ds_read_b128 a[168:171], v222 offset:512\n\t"
"v_fma_f32 v35, v135, s52, v35\n\t"
"v_mfma_f32_16x16x32_bf16 v[164:167], v[248:251], a[176:179], v[164:167]\n\t"
"v_fma_f32 v132, v32, s54, -v132\n\t"
"v_fma_f32 v133, v33, s54, -v133\n\t"
"v_mfma_f32_16x16x32_bf16 v[164:167], v[96:99], a[172:175], v[164:167]\n\t"
"v_fma_f32 v134, v34, s54, -v134\n\t"
"v_fma_f32 v135, v35, s54, -v135\n\t"
"v_mfma_f32_16x16x32_bf16 v[168:171], a[208:211], a[172:175], v[168:171]\n\t"
"ds_read_b128 a[172:175], v223 offset:6656\n\t"
"v_fma_f32 v36, v136, s52, v36\n\t"
"v_mfma_f32_16x16x32_bf16 v[168:171], v[96:99], a[176:179], v[168:171]\n\t"
"v_fma_f32 v37, v137, s52, v37\n\t"
"v_fma_f32 v38, v138, s52, v38\n\t"
"v_mfma_f32_16x16x32_bf16 v[168:171], v[248:251], a[180:183], v[168:171]\n\t"
"v_fma_f32 v39, v139, s52, v39\n\t"
"v_fma_f32 v136, v36, s54, -v136\n\t"
"v_mfma_f32_16x16x32_bf16 v[172:175], v[248:251], a[184:187], v[172:175]\n\t"
"v_fma_f32 v137, v37, s54, -v137\n\t"
"v_fma_f32 v138, v38, s54, -v138\n\t"
"v_mfma_f32_16x16x32_bf16 v[172:175], v[96:99], a[180:183], v[172:175]\n\t"
"v_fma_f32 v139, v39, s54, -v139\n\t"
"v_fma_f32 v40, v140, s52, v40\n\t"
"v_mfma_f32_16x16x32_bf16 v[172:175], a[208:211], a[176:179], v[172:175]\n\t"
"ds_read_b128 a[176:179], v223 offset:12800\n\t"
"v_fma_f32 v41, v141, s52, v41\n\t"
"v_mfma_f32_16x16x32_bf16 v[176:179], a[208:211], a[180:183], v[176:179]\n\t"
"ds_read_b128 a[180:183], v222 offset:20480\n\t"
"v_fma_f32 v42, v142, s52, v42\n\t"
"v_mfma_f32_16x16x32_bf16 v[176:179], v[96:99], a[184:187], v[176:179]\n\t"
"v_fma_f32 v43, v143, s52, v43\n\t"
"v_fma_f32 v140, v40, s54, -v140\n\t"
"v_mfma_f32_16x16x32_bf16 v[176:179], v[248:251], a[188:191], v[176:179]\n\t"
"v_fma_f32 v141, v41, s54, -v141\n\t"
"v_fma_f32 v142, v42, s54, -v142\n\t"
"v_mfma_f32_16x16x32_bf16 v[180:183], v[248:251], a[192:195], v[180:183]\n\t"
"v_fma_f32 v143, v43, s54, -v143\n\t"
"v_fma_f32 v44, v144, s52, v44\n\t"
"v_mfma_f32_16x16x32_bf16 v[180:183], v[96:99], a[188:191], v[180:183]\n\t"
"v_fma_f32 v45, v145, s52, v45\n\t"
"v_fma_f32 v46, v146, s52, v46\n\t"
"v_mfma_f32_16x16x32_bf16 v[180:183], a[208:211], a[184:187], v[180:183]\n\t"
"ds_read_b128 a[184:187], v223 offset:26624\n\t"
"v_fma_f32 v47, v147, s52, v47\n\t"
"v_mfma_f32_16x16x32_bf16 v[184:187], a[208:211], a[188:191], v[184:187]\n\t"
"ds_read_b128 a[188:191], v223 offset:32768\n\t"
"v_fma_f32 v144, v44, s54, -v144\n\t"
"v_mfma_f32_16x16x32_bf16 v[184:187], v[96:99], a[192:195], v[184:187]\n\t"
"v_fma_f32 v145, v45, s54, -v145\n\t"
"v_fma_f32 v146, v46, s54, -v146\n\t"
"v_mfma_f32_16x16x32_bf16 v[184:187], v[248:251], a[196:199], v[184:187]\n\t"
"v_fma_f32 v147, v47, s54, -v147\n\t"
"v_fma_f32 v48, v148, s52, v48\n\t"
"v_mfma_f32_16x16x32_bf16 v[188:191], v[248:251], a[200:203], v[188:191]\n\t"
"v_fma_f32 v49, v149, s52, v49\n\t"
"v_fma_f32 v50, v150, s52, v50\n\t"
"v_mfma_f32_16x16x32_bf16 v[188:191], v[96:99], a[196:199], v[188:191]\n\t"
"v_fma_f32 v51, v151, s52, v51\n\t"
"v_fma_f32 v148, v48, s54, -v148\n\t"
"v_mfma_f32_16x16x32_bf16 v[188:191], a[208:211], a[192:195], v[188:191]\n\t"
"ds_read_b128 a[192:195], v222 offset:40448\n\t"
"v_fma_f32 v149, v49, s54, -v149\n\t"
"v_mfma_f32_16x16x32_bf16 v[192:195], a[208:211], a[196:199], v[192:195]\n\t"
"s_waitcnt vmcnt(11) lgkmcnt(4)\n\t"
"ds_read_b128 a[196:199], v223 offset:46592\n\t"
"v_fma_f32 v150, v50, s54, -v150\n\t"
"v_mfma_f32_16x16x32_bf16 v[192:195], v[96:99], a[200:203], v[192:195]\n\t"
"ds_read_b128 a[200:203], v223 offset:52736\n\t"
"v_fma_f32 v151, v51, s54, -v151\n\t"
"v_mfma_f32_16x16x32_bf16 v[192:195], v[248:251], a[204:207], v[192:195]\n\t"
"ds_read_b128 a[204:207], v222 offset:60416\n\t"
"v_fma_f32 v52, v152, s52, v52\n\t"
"Lst12_%=:\n\t"
"v_mfma_f32_16x16x32_bf16 v[196:199], a[228:231], a[168:171], v[196:199]\n\t"
"s_waitcnt lgkmcnt(6)\n\t"
"v_fma_f32 v53, v153, s52, v53\n\t"
"v_fma_f32 v54, v154, s52, v54\n\t"
"v_mfma_f32_16x16x32_bf16 v[196:199], a[232:235], a[176:179], v[196:199]\n\t"
"v_fma_f32 v55, v155, s52, v55\n\t"
"v_fma_f32 v152, v52, s54, -v152\n\t"
"v_mfma_f32_16x16x32_bf16 v[196:199], a[240:243], a[172:175], v[196:199]\n\t"
"v_fma_f32 v153, v53, s54, -v153\n\t"
"v_fma_f32 v154, v54, s54, -v154\n\t"
"v_mfma_f32_16x16x32_bf16 v[204:207], a[228:231], a[172:175], v[204:207]\n\t"
"s_waitcnt lgkmcnt(5)\n\t"
"v_fma_f32 v155, v55, s54, -v155\n\t"
"v_fma_f32 v56, v156, s52, v56\n\t"
"v_mfma_f32_16x16x32_bf16 v[204:207], a[240:243], a[176:179], v[204:207]\n\t"
"v_fma_f32 v57, v157, s52, v57\n\t"
"v_fma_f32 v58, v158, s52, v58\n\t"
"v_mfma_f32_16x16x32_bf16 v[204:207], a[232:235], a[180:183], v[204:207]\n\t"
"v_fma_f32 v59, v159, s52, v59\n\t"
"v_fma_f32 v156, v56, s54, -v156\n\t"
"v_mfma_f32_16x16x32_bf16 v[212:215], a[232:235], a[184:187], v[212:215]\n\t"
"s_waitcnt lgkmcnt(4)\n\t"
"v_fma_f32 v157, v57, s54, -v157\n\t"
"v_fma_f32 v158, v58, s54, -v158\n\t"
"v_mfma_f32_16x16x32_bf16 v[212:215], a[240:243], a[180:183], v[212:215]\n\t"
"v_fma_f32 v159, v59, s54, -v159\n\t"
"v_fma_f32 v60, v160, s52, v60\n\t"
"v_mfma_f32_16x16x32_bf16 v[212:215], a[228:231], a[176:179], v[212:215]\n\t"
"v_fma_f32 v61, v161, s52, v61\n\t"
"v_fma_f32 v62, v162, s52, v62\n\t"
"v_mfma_f32_16x16x32_bf16 a[0:3], a[228:231], a[180:183], a[0:3]\n\t"
"s_waitcnt lgkmcnt(3)\n\t"
"v_fma_f32 v63, v163, s52, v63\n\t"
"v_fma_f32 v160, v60, s54, -v160\n\t"
"v_mfma_f32_16x16x32_bf16 a[0:3], a[240:243], a[184:187], a[0:3]\n\t"
"v_fma_f32 v161, v61, s54, -v161\n\t"
"v_fma_f32 v162, v62, s54, -v162\n\t"
"v_mfma_f32_16x16x32_bf16 a[0:3], a[232:235], a[188:191], a[0:3]\n\t"
"buffer_load_dwordx4 v[96:99], off, s[80:83], s20 offset:2048 sc1\n\t"
"s_mov_b32 s20, 1327104\n\t"
"v_mfma_f32_16x16x32_bf16 a[8:11], a[232:235], a[192:195], a[8:11]\n\t"
"buffer_load_dwordx4 v[248:251], off, s[80:83], s20 offset:0 sc1\n\t"
"v_mfma_f32_16x16x32_bf16 a[8:11], a[240:243], a[188:191], a[8:11]\n\t"
"s_waitcnt lgkmcnt(2)\n\t"
"v_fma_f32 v163, v63, s54, -v163\n\t"
"v_fma_f32 v64, v164, s52, v64\n\t"
"v_mfma_f32_16x16x32_bf16 a[8:11], a[228:231], a[184:187], a[8:11]\n\t"
"v_fma_f32 v65, v165, s52, v65\n\t"
"v_fma_f32 v66, v166, s52, v66\n\t"
"v_mfma_f32_16x16x32_bf16 a[16:19], a[228:231], a[188:191], a[16:19]\n\t"
"s_waitcnt lgkmcnt(1)\n\t"
"v_fma_f32 v67, v167, s52, v67\n\t"
"v_fma_f32 v164, v64, s54, -v164\n\t"
"v_mfma_f32_16x16x32_bf16 a[16:19], a[240:243], a[192:195], a[16:19]\n\t"
"v_fma_f32 v165, v65, s54, -v165\n\t"
"v_fma_f32 v166, v66, s54, -v166\n\t"
"v_mfma_f32_16x16x32_bf16 a[16:19], a[232:235], a[196:199], a[16:19]\n\t"
"buffer_load_dwordx4 v[240:243], off, s[80:83], s20 offset:1024 sc1\n\t"
"v_mfma_f32_16x16x32_bf16 a[24:27], a[232:235], a[200:203], a[24:27]\n\t"
"buffer_load_dwordx4 a[208:211], off, s[80:83], s20 offset:2048 sc1\n\t"
"v_mfma_f32_16x16x32_bf16 a[24:27], a[240:243], a[196:199], a[24:27]\n\t"
"s_waitcnt lgkmcnt(0)\n\t"
"v_fma_f32 v167, v67, s54, -v167\n\t"
"v_fma_f32 v68, v168, s52, v68\n\t"
"v_mfma_f32_16x16x32_bf16 a[24:27], a[228:231], a[192:195], a[24:27]\n\t"
"s_mov_b32 s20, 1339392\n\t"
"v_fma_f32 v69, v169, s52, v69\n\t"
"v_fma_f32 v70, v170, s52, v70\n\t"
"v_mfma_f32_16x16x32_bf16 a[32:35], a[228:231], a[196:199], a[32:35]\n\t"
"s_waitcnt vmcnt(12)\n\t"
"v_fma_f32 v71, v171, s52, v71\n\t"
"v_fma_f32 v168, v68, s54, -v168\n\t"
"v_mfma_f32_16x16x32_bf16 a[32:35], a[240:243], a[200:203], a[32:35]\n\t"
"v_fma_f32 v169, v69, s54, -v169\n\t"
"v_fma_f32 v170, v70, s54, -v170\n\t"
"v_mfma_f32_16x16x32_bf16 a[32:35], a[232:235], a[204:207], a[32:35]\n\t"
"v_fma_f32 v171, v71, s54, -v171\n\t"
"v_fma_f32 v72, v172, s52, v72\n\t"
"v_mfma_f32_16x16x32_bf16 a[40:43], a[236:239], a[168:171], a[40:43]\n\t"
"v_fma_f32 v73, v173, s52, v73\n\t"
"v_fma_f32 v74, v174, s52, v74\n\t"
"v_mfma_f32_16x16x32_bf16 a[40:43], a[212:215], a[176:179], a[40:43]\n\t"
"v_fma_f32 v75, v175, s52, v75\n\t"
"v_fma_f32 v172, v72, s54, -v172\n\t"
"v_mfma_f32_16x16x32_bf16 a[40:43], a[244:247], a[172:175], a[40:43]\n\t"
"buffer_load_dwordx4 a[220:223], off, s[80:83], s20 offset:0 sc1\n\t"
"v_mfma_f32_16x16x32_bf16 a[48:51], a[236:239], a[172:175], a[48:51]\n\t"
"buffer_load_dwordx4 a[224:227], off, s[80:83], s20 offset:1024 sc1\n\t"
"v_mfma_f32_16x16x32_bf16 a[48:51], a[244:247], a[176:179], a[48:51]\n\t"
"v_fma_f32 v173, v73, s54, -v173\n\t"
"v_fma_f32 v174, v74, s54, -v174\n\t"
"v_mfma_f32_16x16x32_bf16 a[48:51], a[212:215], a[180:183], a[48:51]\n\t"
"v_fma_f32 v175, v75, s54, -v175\n\t"
"v_fma_f32 v76, v176, s52, v76\n\t"
"v_mfma_f32_16x16x32_bf16 a[56:59], a[212:215], a[184:187], a[56:59]\n\t"
"v_fma_f32 v77, v177, s52, v77\n\t"
"v_fma_f32 v78, v178, s52, v78\n\t"
"v_mfma_f32_16x16x32_bf16 a[56:59], a[244:247], a[180:183], a[56:59]\n\t"
"v_fma_f32 v79, v179, s52, v79\n\t"
"v_fma_f32 v176, v76, s54, -v176\n\t"
"v_mfma_f32_16x16x32_bf16 a[56:59], a[236:239], a[176:179], a[56:59]\n\t"
"v_fma_f32 v177, v77, s54, -v177\n\t"
"v_fma_f32 v178, v78, s54, -v178\n\t"
"v_mfma_f32_16x16x32_bf16 a[64:67], a[236:239], a[180:183], a[64:67]\n\t"
"v_fma_f32 v179, v79, s54, -v179\n\t"
"v_fma_f32 v80, v180, s52, v80\n\t"
"v_mfma_f32_16x16x32_bf16 a[64:67], a[244:247], a[184:187], a[64:67]\n\t"
"v_fma_f32 v81, v181, s52, v81\n\t"
"v_fma_f32 v82, v182, s52, v82\n\t"
"v_mfma_f32_16x16x32_bf16 a[64:67], a[212:215], a[188:191], a[64:67]\n\t"
"buffer_load_dwordx4 a[228:231], off, s[80:83], s20 offset:2048 sc1\n\t"
"s_mov_b32 s20, 1351680\n\t"
"v_mfma_f32_16x16x32_bf16 a[72:75], a[212:215], a[192:195], a[72:75]\n\t"
"buffer_load_dwordx4 a[232:235], off, s[80:83], s20 offset:0 sc1\n\t"
"v_mfma_f32_16x16x32_bf16 a[72:75], a[244:247], a[188:191], a[72:75]\n\t"
"v_fma_f32 v83, v183, s52, v83\n\t"
"v_fma_f32 v180, v80, s54, -v180\n\t"
"v_mfma_f32_16x16x32_bf16 a[72:75], a[236:239], a[184:187], a[72:75]\n\t"
"v_fma_f32 v181, v81, s54, -v181\n\t"
"v_fma_f32 v182, v82, s54, -v182\n\t"
"v_mfma_f32_16x16x32_bf16 a[80:83], a[236:239], a[188:191], a[80:83]\n\t"
"v_fma_f32 v183, v83, s54, -v183\n\t"
"v_fma_f32 v84, v184, s52, v84\n\t"
"v_mfma_f32_16x16x32_bf16 a[80:83], a[244:247], a[192:195], a[80:83]\n\t"
"v_fma_f32 v85, v185, s52, v85\n\t"
"v_fma_f32 v86, v186, s52, v86\n\t"
"v_mfma_f32_16x16x32_bf16 a[80:83], a[212:215], a[196:199], a[80:83]\n\t"
"buffer_load_dwordx4 a[240:243], off, s[80:83], s20 offset:1024 sc1\n\t"
"v_mfma_f32_16x16x32_bf16 a[88:91], a[212:215], a[200:203], a[88:91]\n\t"
"buffer_load_dwordx4 a[248:251], off, s[80:83], s20 offset:2048 sc1\n\t"
"v_mfma_f32_16x16x32_bf16 a[88:91], a[244:247], a[196:199], a[88:91]\n\t"
"s_mov_b32 s20, 1585152\n\t"
"v_fma_f32 v87, v187, s52, v87\n\t"
"v_fma_f32 v184, v84, s54, -v184\n\t"
"v_mfma_f32_16x16x32_bf16 a[88:91], a[236:239], a[192:195], a[88:91]\n\t"
"v_fma_f32 v185, v85, s54, -v185\n\t"
"v_fma_f32 v186, v86, s54, -v186\n\t"
"v_mfma_f32_16x16x32_bf16 a[96:99], a[236:239], a[196:199], a[96:99]\n\t"
"s_waitcnt vmcnt(9)\n\t"
"v_fma_f32 v187, v87, s54, -v187\n\t"
"v_fma_f32 v88, v188, s52, v88\n\t"
"v_mfma_f32_16x16x32_bf16 a[96:99], a[244:247], a[200:203], a[96:99]\n\t"
"v_fma_f32 v89, v189, s52, v89\n\t"
"v_fma_f32 v90, v190, s52, v90\n\t"
"v_mfma_f32_16x16x32_bf16 a[96:99], a[212:215], a[204:207], a[96:99]\n\t"
"v_fma_f32 v91, v191, s52, v91\n\t"
"v_fma_f32 v188, v88, s54, -v188\n\t"
"v_mfma_f32_16x16x32_bf16 a[104:107], a[216:219], a[168:171], a[104:107]\n\t"
"Lpg9_%=:\n\t"
"s_cbranch_vccnz Lvr0_%=\n\t"
"ds_read_b128 a[168:171], v222 offset:768\n\t"
"v_fma_f32 v189, v89, s54, -v189\n\t"
"v_mfma_f32_16x16x32_bf16 a[104:107], v[96:99], a[176:179], a[104:107]\n\t"
"v_fma_f32 v190, v90, s54, -v190\n\t"
"v_fma_f32 v191, v91, s54, -v191\n\t"
"v_mfma_f32_16x16x32_bf16 a[104:107], v[236:239], a[172:175], a[104:107]\n\t"
"buffer_load_dwordx4 a[236:239], off, s[80:83], s20 offset:0 sc1\n\t"
"v_mfma_f32_16x16x32_bf16 a[112:115], a[216:219], a[172:175], a[112:115]\n\t"
"buffer_load_dwordx4 a[212:215], off, s[80:83], s20 offset:1024 sc1\n\t"
"v_mfma_f32_16x16x32_bf16 a[112:115], v[236:239], a[176:179], a[112:115]\n\t"
"buffer_load_dwordx4 a[244:247], off, s[80:83], s20 offset:2048 sc1\n\t"
"v_mfma_f32_16x16x32_bf16 a[112:115], v[96:99], a[180:183], a[112:115]\n\t"
"s_mov_b32 s20, 1597440\n\t"
"ds_read_b128 a[172:175], v223 offset:6912\n\t"
"v_fma_f32 v92, v192, s52, v92\n\t"
"v_mfma_f32_16x16x32_bf16 a[120:123], v[96:99], a[184:187], a[120:123]\n\t"
"v_fma_f32 v93, v193, s52, v93\n\t"
"v_fma_f32 v94, v194, s52, v94\n\t"
"v_mfma_f32_16x16x32_bf16 a[120:123], v[236:239], a[180:183], a[120:123]\n\t"
"v_fma_f32 v95, v195, s52, v95\n\t"
"v_fma_f32 v192, v92, s54, -v192\n\t"
"v_mfma_f32_16x16x32_bf16 a[120:123], a[216:219], a[176:179], a[120:123]\n\t"
"ds_read_b128 a[176:179], v223 offset:13056\n\t"
"v_fma_f32 v193, v93, s54, -v193\n\t"
"v_mfma_f32_16x16x32_bf16 a[128:131], a[216:219], a[180:183], a[128:131]\n\t"
"ds_read_b128 a[180:183], v222 offset:20736\n\t"
"v_fma_f32 v194, v94, s54, -v194\n\t"
"v_mfma_f32_16x16x32_bf16 a[128:131], v[236:239], a[184:187], a[128:131]\n\t"
"v_fma_f32 v195, v95, s54, -v195\n\t"
"v_mfma_f32_16x16x32_bf16 a[128:131], v[96:99], a[188:191], a[128:131]\n\t"
"buffer_load_dwordx4 a[252:255], off, s[80:83], s20 offset:0 sc1\n\t"
"v_mfma_f32_16x16x32_bf16 a[136:139], v[96:99], a[192:195], a[136:139]\n\t"
"v_mfma_f32_16x16x32_bf16 a[136:139], v[236:239], a[188:191], a[136:139]\n\t"
"v_mfma_f32_16x16x32_bf16 a[136:139], a[216:219], a[184:187], a[136:139]\n\t"
"ds_read_b128 a[184:187], v223 offset:26880\n\t"
"v_mfma_f32_16x16x32_bf16 a[144:147], a[216:219], a[188:191], a[144:147]\n\t"
"ds_read_b128 a[188:191], v223 offset:33024\n\t"
"v_mfma_f32_16x16x32_bf16 a[144:147], v[236:239], a[192:195], a[144:147]\n\t"
"v_mfma_f32_16x16x32_bf16 a[144:147], v[96:99], a[196:199], a[144:147]\n\t"
"v_mfma_f32_16x16x32_bf16 a[152:155], v[96:99], a[200:203], a[152:155]\n\t"
"v_mfma_f32_16x16x32_bf16 a[152:155], v[236:239], a[196:199], a[152:155]\n\t"
"v_mfma_f32_16x16x32_bf16 a[152:155], a[216:219], a[192:195], a[152:155]\n\t"
"ds_read_b128 a[192:195], v222 offset:40704\n\t"
"v_mfma_f32_16x16x32_bf16 a[160:163], a[216:219], a[196:199], a[160:163]\n\t"
"buffer_load_dwordx4 a[216:219], off, s[80:83], s20 offset:1024 sc1\n\t"
"v_mfma_f32_16x16x32_bf16 a[160:163], v[236:239], a[200:203], a[160:163]\n\t"
"s_waitcnt vmcnt(11) lgkmcnt(4)\n\t"
"ds_read_b128 a[196:199], v223 offset:46848\n\t"
"v_mfma_f32_16x16x32_bf16 a[160:163], v[96:99], a[204:207], a[160:163]\n\t"
"ds_read_b128 a[200:203], v223 offset:52992\n\t"
"v_mfma_f32_16x16x32_bf16 v[200:203], v[248:251], a[168:171], v[200:203]\n\t"
"buffer_load_dwordx4 v[96:99], off, s[80:83], s20 offset:2048 sc1\n\t"
"v_mfma_f32_16x16x32_bf16 v[200:203], a[208:211], a[176:179], v[200:203]\n\t"
"s_waitcnt lgkmcnt(5)\n\t"
"ds_read_b128 a[204:207], v222 offset:60672\n\t"
"s_mov_b32 s20, 1609728\n\t"
"v_mfma_f32_16x16x32_bf16 v[200:203], v[240:243], a[172:175], v[200:203]\n\t"
"s_add_u32 m0, s24, 54272\n\t"
"s_add_u32 s28, s21, 1575936\n\t"
"v_mfma_f32_16x16x32_bf16 v[208:211], v[248:251], a[172:175], v[208:211]\n\t"
"buffer_load_dwordx4 v[236:239], off, s[80:83], s20 offset:0 sc1\n\t"
"v_mfma_f32_16x16x32_bf16 v[208:211], v[240:243], a[176:179], v[208:211]\n\t"
"s_waitcnt lgkmcnt(5)\n\t"
"v_mfma_f32_16x16x32_bf16 v[208:211], a[208:211], a[180:183], v[208:211]\n\t"
"v_mfma_f32_16x16x32_bf16 v[216:219], a[208:211], a[184:187], v[216:219]\n\t"
"buffer_load_dwordx4 v247, s[12:15], s28 offen offset:0 nt lds\n\t"
"v_mfma_f32_16x16x32_bf16 v[216:219], v[240:243], a[180:183], v[216:219]\n\t"
"s_waitcnt lgkmcnt(4)\n\t"
"s_add_u32 m0, s25, 512\n\t"
"v_mfma_f32_16x16x32_bf16 v[216:219], v[248:251], a[176:179], v[216:219]\n\t"
"buffer_load_dwordx4 v254, s[12:15], 0 offen offset:0 nt lds\n\t"
"v_mfma_f32_16x16x32_bf16 a[4:7], v[248:251], a[180:183], a[4:7]\n\t"
"s_waitcnt lgkmcnt(3)\n\t"
"s_add_u32 m0, s24, 2048\n\t"
"s_add_u32 s28, s21, 3072\n\t"
"v_mfma_f32_16x16x32_bf16 a[4:7], v[240:243], a[184:187], a[4:7]\n\t"
"buffer_load_dwordx4 v247, s[12:15], s28 offen offset:0 nt lds\n\t"
"v_mfma_f32_16x16x32_bf16 a[4:7], a[208:211], a[188:191], a[4:7]\n\t"
"s_add_u32 m0, s24, 8192\n\t"
"s_add_u32 s28, s21, 199680\n\t"
"v_mfma_f32_16x16x32_bf16 a[12:15], a[208:211], a[192:195], a[12:15]\n\t"
"buffer_load_dwordx4 v247, s[12:15], s28 offen offset:0 nt lds\n\t"
"v_mfma_f32_16x16x32_bf16 a[12:15], v[240:243], a[188:191], a[12:15]\n\t"
"s_waitcnt lgkmcnt(2)\n\t"
"s_add_u32 m0, s24, 14336\n\t"
"s_add_u32 s28, s21, 396288\n\t"
"v_mfma_f32_16x16x32_bf16 a[12:15], v[248:251], a[184:187], a[12:15]\n\t"
"buffer_load_dwordx4 v247, s[12:15], s28 offen offset:0 nt lds\n\t"
"v_mfma_f32_16x16x32_bf16 a[20:23], v[248:251], a[188:191], a[20:23]\n\t"
"s_waitcnt lgkmcnt(1)\n\t"
"v_mfma_f32_16x16x32_bf16 a[20:23], v[240:243], a[192:195], a[20:23]\n\t"
"v_mfma_f32_16x16x32_bf16 a[20:23], a[208:211], a[196:199], a[20:23]\n\t"
"v_mfma_f32_16x16x32_bf16 a[28:31], a[208:211], a[200:203], a[28:31]\n\t"
"s_waitcnt lgkmcnt(0)\n\t"
"v_mfma_f32_16x16x32_bf16 a[28:31], v[240:243], a[196:199], a[28:31]\n\t"
"v_mfma_f32_16x16x32_bf16 a[28:31], v[248:251], a[192:195], a[28:31]\n\t"
"v_mfma_f32_16x16x32_bf16 a[36:39], v[248:251], a[196:199], a[36:39]\n\t"
"s_waitcnt vmcnt(15)\n\t"
"v_mfma_f32_16x16x32_bf16 a[36:39], v[240:243], a[200:203], a[36:39]\n\t"
"v_mfma_f32_16x16x32_bf16 a[36:39], a[208:211], a[204:207], a[36:39]\n\t"
"v_mfma_f32_16x16x32_bf16 a[44:47], a[220:223], a[168:171], a[44:47]\n\t"
"v_mfma_f32_16x16x32_bf16 a[44:47], a[228:231], a[176:179], a[44:47]\n\t"
"v_mfma_f32_16x16x32_bf16 a[44:47], a[224:227], a[172:175], a[44:47]\n\t"
"v_mfma_f32_16x16x32_bf16 a[52:55], a[220:223], a[172:175], a[52:55]\n\t"
"v_mfma_f32_16x16x32_bf16 a[52:55], a[224:227], a[176:179], a[52:55]\n\t"
"v_mfma_f32_16x16x32_bf16 a[52:55], a[228:231], a[180:183], a[52:55]\n\t"
"v_mfma_f32_16x16x32_bf16 a[60:63], a[228:231], a[184:187], a[60:63]\n\t"
"v_mfma_f32_16x16x32_bf16 a[60:63], a[224:227], a[180:183], a[60:63]\n\t"
"v_mfma_f32_16x16x32_bf16 a[60:63], a[220:223], a[176:179], a[60:63]\n\t"
"v_mfma_f32_16x16x32_bf16 a[68:71], a[220:223], a[180:183], a[68:71]\n\t"
"v_mfma_f32_16x16x32_bf16 a[68:71], a[224:227], a[184:187], a[68:71]\n\t"
"v_mfma_f32_16x16x32_bf16 a[68:71], a[228:231], a[188:191], a[68:71]\n\t"
"v_mfma_f32_16x16x32_bf16 a[76:79], a[228:231], a[192:195], a[76:79]\n\t"
"v_mfma_f32_16x16x32_bf16 a[76:79], a[224:227], a[188:191], a[76:79]\n\t"
"v_mfma_f32_16x16x32_bf16 a[76:79], a[220:223], a[184:187], a[76:79]\n\t"
"v_mfma_f32_16x16x32_bf16 a[84:87], a[220:223], a[188:191], a[84:87]\n\t"
"v_mfma_f32_16x16x32_bf16 a[84:87], a[224:227], a[192:195], a[84:87]\n\t"
"v_mfma_f32_16x16x32_bf16 a[84:87], a[228:231], a[196:199], a[84:87]\n\t"
"v_mfma_f32_16x16x32_bf16 a[92:95], a[228:231], a[200:203], a[92:95]\n\t"
"v_mfma_f32_16x16x32_bf16 a[92:95], a[224:227], a[196:199], a[92:95]\n\t"
"v_mfma_f32_16x16x32_bf16 a[92:95], a[220:223], a[192:195], a[92:95]\n\t"
"buffer_load_dwordx4 v[248:251], off, s[80:83], s20 offset:1024 sc1\n\t"
"v_mfma_f32_16x16x32_bf16 a[100:103], a[220:223], a[196:199], a[100:103]\n\t"
"s_waitcnt vmcnt(13)\n\t"
"v_mfma_f32_16x16x32_bf16 a[100:103], a[224:227], a[200:203], a[100:103]\n\t"
"v_mfma_f32_16x16x32_bf16 a[100:103], a[228:231], a[204:207], a[100:103]\n\t"
"v_mfma_f32_16x16x32_bf16 a[108:111], a[232:235], a[168:171], a[108:111]\n\t"
"ds_read_b128 a[168:171], v222 offset:1024\n\t"
"v_mfma_f32_16x16x32_bf16 a[108:111], a[248:251], a[176:179], a[108:111]\n\t"
"v_mfma_f32_16x16x32_bf16 a[108:111], a[240:243], a[172:175], a[108:111]\n\t"
"v_mfma_f32_16x16x32_bf16 a[116:119], a[232:235], a[172:175], a[116:119]\n\t"
"ds_read_b128 a[172:175], v223 offset:7168\n\t"
"v_mfma_f32_16x16x32_bf16 a[116:119], a[240:243], a[176:179], a[116:119]\n\t"
"v_mfma_f32_16x16x32_bf16 a[116:119], a[248:251], a[180:183], a[116:119]\n\t"
"v_mfma_f32_16x16x32_bf16 a[124:127], a[248:251], a[184:187], a[124:127]\n\t"
"v_mfma_f32_16x16x32_bf16 a[124:127], a[240:243], a[180:183], a[124:127]\n\t"
"v_mfma_f32_16x16x32_bf16 a[124:127], a[232:235], a[176:179], a[124:127]\n\t"
"ds_read_b128 a[176:179], v223 offset:13312\n\t"
"v_mfma_f32_16x16x32_bf16 a[132:135], a[232:235], a[180:183], a[132:135]\n\t"
"ds_read_b128 a[180:183], v222 offset:20992\n\t"
"v_mfma_f32_16x16x32_bf16 a[132:135], a[240:243], a[184:187], a[132:135]\n\t"
"v_mfma_f32_16x16x32_bf16 a[132:135], a[248:251], a[188:191], a[132:135]\n\t"
"v_mfma_f32_16x16x32_bf16 a[140:143], a[248:251], a[192:195], a[140:143]\n\t"
"v_mfma_f32_16x16x32_bf16 a[140:143], a[240:243], a[188:191], a[140:143]\n\t"
"v_mfma_f32_16x16x32_bf16 a[140:143], a[232:235], a[184:187], a[140:143]\n\t"
"ds_read_b128 a[184:187], v223 offset:27136\n\t"
"v_mfma_f32_16x16x32_bf16 a[148:151], a[232:235], a[188:191], a[148:151]\n\t"
"ds_read_b128 a[188:191], v223 offset:33280\n\t"
"v_mfma_f32_16x16x32_bf16 a[148:151], a[240:243], a[192:195], a[148:151]\n\t"
"v_mfma_f32_16x16x32_bf16 a[148:151], a[248:251], a[196:199], a[148:151]\n\t"
"v_mfma_f32_16x16x32_bf16 a[156:159], a[248:251], a[200:203], a[156:159]\n\t"
"v_mfma_f32_16x16x32_bf16 a[156:159], a[240:243], a[196:199], a[156:159]\n\t"
"v_mfma_f32_16x16x32_bf16 a[156:159], a[232:235], a[192:195], a[156:159]\n\t"
"ds_read_b128 a[192:195], v222 offset:40960\n\t"
"v_mfma_f32_16x16x32_bf16 a[164:167], a[232:235], a[196:199], a[164:167]\n\t"
"s_waitcnt vmcnt(10) lgkmcnt(4)\n\t"
"ds_read_b128 a[196:199], v223 offset:47104\n\t"
"v_mfma_f32_16x16x32_bf16 a[164:167], a[240:243], a[200:203], a[164:167]\n\t"
"ds_read_b128 a[200:203], v223 offset:53248\n\t"
"v_mfma_f32_16x16x32_bf16 a[164:167], a[248:251], a[204:207], a[164:167]\n\t"
"ds_read_b128 a[204:207], v222 offset:60928\n\t"
"Lst13_%=:\n\t"
"v_mfma_f32_16x16x32_bf16 v[196:199], a[236:239], a[168:171], v[196:199]\n\t"
"s_waitcnt lgkmcnt(6)\n\t"
"v_mfma_f32_16x16x32_bf16 v[196:199], a[244:247], a[176:179], v[196:199]\n\t"
"v_mfma_f32_16x16x32_bf16 v[196:199], a[212:215], a[172:175], v[196:199]\n\t"
"v_mfma_f32_16x16x32_bf16 v[204:207], a[236:239], a[172:175], v[204:207]\n\t"
"s_waitcnt lgkmcnt(5)\n\t"
"v_mfma_f32_16x16x32_bf16 v[204:207], a[212:215], a[176:179], v[204:207]\n\t"
"v_mfma_f32_16x16x32_bf16 v[204:207], a[244:247], a[180:183], v[204:207]\n\t"
"v_mfma_f32_16x16x32_bf16 v[212:215], a[244:247], a[184:187], v[212:215]\n\t"
"s_waitcnt lgkmcnt(4)\n\t"
"v_mfma_f32_16x16x32_bf16 v[212:215], a[212:215], a[180:183], v[212:215]\n\t"
"buffer_load_dwordx4 v[240:243], off, s[80:83], s20 offset:2048 sc1\n\t"
"v_mfma_f32_16x16x32_bf16 v[212:215], a[236:239], a[176:179], v[212:215]\n\t"
"s_mov_b32 s20, 1622016\n\t"
"v_mfma_f32_16x16x32_bf16 a[0:3], a[236:239], a[180:183], a[0:3]\n\t"
"s_waitcnt lgkmcnt(3)\n\t"
"v_mfma_f32_16x16x32_bf16 a[0:3], a[212:215], a[184:187], a[0:3]\n\t"
"v_mfma_f32_16x16x32_bf16 a[0:3], a[244:247], a[188:191], a[0:3]\n\t"
"buffer_load_dwordx4 a[208:211], off, s[80:83], s20 offset:0 sc1\n\t"
"v_mfma_f32_16x16x32_bf16 a[8:11], a[244:247], a[192:195], a[8:11]\n\t"
"s_waitcnt lgkmcnt(2)\n\t"
"v_mfma_f32_16x16x32_bf16 a[8:11], a[212:215], a[188:191], a[8:11]\n\t"
"v_mfma_f32_16x16x32_bf16 a[8:11], a[236:239], a[184:187], a[8:11]\n\t"
"v_mfma_f32_16x16x32_bf16 a[16:19], a[236:239], a[188:191], a[16:19]\n\t"
"buffer_load_dwordx4 a[220:223], off, s[80:83], s20 offset:1024 sc1\n\t"
"v_mfma_f32_16x16x32_bf16 a[16:19], a[212:215], a[192:195], a[16:19]\n\t"
"s_waitcnt lgkmcnt(1)\n\t"
"v_mfma_f32_16x16x32_bf16 a[16:19], a[244:247], a[196:199], a[16:19]\n\t"
"v_mfma_f32_16x16x32_bf16 a[24:27], a[244:247], a[200:203], a[24:27]\n\t"
"s_waitcnt lgkmcnt(0)\n\t"
"v_mfma_f32_16x16x32_bf16 a[24:27], a[212:215], a[196:199], a[24:27]\n\t"
"buffer_load_dwordx4 a[224:227], off, s[80:83], s20 offset:2048 sc1\n\t"
"v_mfma_f32_16x16x32_bf16 a[24:27], a[236:239], a[192:195], a[24:27]\n\t"
"s_mov_b32 s20, 1634304\n\t"
"v_mfma_f32_16x16x32_bf16 a[32:35], a[236:239], a[196:199], a[32:35]\n\t"
"s_waitcnt vmcnt(11)\n\t"
"v_mfma_f32_16x16x32_bf16 a[32:35], a[212:215], a[200:203], a[32:35]\n\t"
"v_mfma_f32_16x16x32_bf16 a[32:35], a[244:247], a[204:207], a[32:35]\n\t"
"buffer_load_dwordx4 a[212:215], off, s[80:83], s20 offset:0 sc1\n\t"
"v_mfma_f32_16x16x32_bf16 a[40:43], a[252:255], a[168:171], a[40:43]\n\t"
"v_mfma_f32_16x16x32_bf16 a[40:43], v[96:99], a[176:179], a[40:43]\n\t"
"v_mfma_f32_16x16x32_bf16 a[40:43], a[216:219], a[172:175], a[40:43]\n\t"
"v_mfma_f32_16x16x32_bf16 a[48:51], a[252:255], a[172:175], a[48:51]\n\t"
"buffer_load_dwordx4 a[228:231], off, s[80:83], s20 offset:1024 sc1\n\t"
"v_mfma_f32_16x16x32_bf16 a[48:51], a[216:219], a[176:179], a[48:51]\n\t"
"v_mfma_f32_16x16x32_bf16 a[48:51], v[96:99], a[180:183], a[48:51]\n\t"
"v_mfma_f32_16x16x32_bf16 a[56:59], v[96:99], a[184:187], a[56:59]\n\t"
"buffer_load_dwordx4 a[232:235], off, s[80:83], s20 offset:2048 sc1\n\t"
"v_mfma_f32_16x16x32_bf16 a[56:59], a[216:219], a[180:183], a[56:59]\n\t"
"s_mov_b32 s20, 1646592\n\t"
"v_mfma_f32_16x16x32_bf16 a[56:59], a[252:255], a[176:179], a[56:59]\n\t"
"v_mfma_f32_16x16x32_bf16 a[64:67], a[252:255], a[180:183], a[64:67]\n\t"
"v_mfma_f32_16x16x32_bf16 a[64:67], a[216:219], a[184:187], a[64:67]\n\t"
"buffer_load_dwordx4 a[236:239], off, s[80:83], s20 offset:0 sc1\n\t"
"v_mfma_f32_16x16x32_bf16 a[64:67], v[96:99], a[188:191], a[64:67]\n\t"
"v_mfma_f32_16x16x32_bf16 a[72:75], v[96:99], a[192:195], a[72:75]\n\t"
"v_mfma_f32_16x16x32_bf16 a[72:75], a[216:219], a[188:191], a[72:75]\n\t"
"v_mfma_f32_16x16x32_bf16 a[72:75], a[252:255], a[184:187], a[72:75]\n\t"
"buffer_load_dwordx4 a[240:243], off, s[80:83], s20 offset:1024 sc1\n\t"
"v_mfma_f32_16x16x32_bf16 a[80:83], a[252:255], a[188:191], a[80:83]\n\t"
"v_mfma_f32_16x16x32_bf16 a[80:83], a[216:219], a[192:195], a[80:83]\n\t"
"v_mfma_f32_16x16x32_bf16 a[80:83], v[96:99], a[196:199], a[80:83]\n\t"
"v_mfma_f32_16x16x32_bf16 a[88:91], v[96:99], a[200:203], a[88:91]\n\t"
"buffer_load_dwordx4 a[244:247], off, s[80:83], s20 offset:2048 sc1\n\t"
"v_mfma_f32_16x16x32_bf16 a[88:91], a[216:219], a[196:199], a[88:91]\n\t"
"s_mov_b32 s20, 884736\n\t"
"v_mfma_f32_16x16x32_bf16 a[88:91], a[252:255], a[192:195], a[88:91]\n\t"
"v_mfma_f32_16x16x32_bf16 a[96:99], a[252:255], a[196:199], a[96:99]\n\t"
"s_waitcnt vmcnt(9)\n\t"
"v_mfma_f32_16x16x32_bf16 a[96:99], a[216:219], a[200:203], a[96:99]\n\t"
"buffer_load_dwordx4 a[216:219], off, s[80:83], s20 offset:0 sc1\n\t"
"v_mfma_f32_16x16x32_bf16 a[96:99], v[96:99], a[204:207], a[96:99]\n\t"
"v_mfma_f32_16x16x32_bf16 a[104:107], v[236:239], a[168:171], a[104:107]\n\t"
"ds_read_b128 a[168:171], v222 offset:1280\n\t"
"v_mfma_f32_16x16x32_bf16 a[104:107], v[240:243], a[176:179], a[104:107]\n\t"
"buffer_load_dwordx4 a[248:251], off, s[80:83], s20 offset:1024 sc1\n\t"
"v_mfma_f32_16x16x32_bf16 a[104:107], v[248:251], a[172:175], a[104:107]\n\t"
"v_mfma_f32_16x16x32_bf16 a[112:115], v[236:239], a[172:175], a[112:115]\n\t"
"ds_read_b128 a[172:175], v223 offset:7424\n\t"
"v_mfma_f32_16x16x32_bf16 a[112:115], v[248:251], a[176:179], a[112:115]\n\t"
"buffer_load_dwordx4 a[252:255], off, s[80:83], s20 offset:2048 sc1\n\t"
"v_mfma_f32_16x16x32_bf16 a[112:115], v[240:243], a[180:183], a[112:115]\n\t"
"v_mfma_f32_16x16x32_bf16 a[120:123], v[240:243], a[184:187], a[120:123]\n\t"
"v_mfma_f32_16x16x32_bf16 a[120:123], v[248:251], a[180:183], a[120:123]\n\t"
"v_mfma_f32_16x16x32_bf16 a[120:123], v[236:239], a[176:179], a[120:123]\n\t"
"ds_read_b128 a[176:179], v223 offset:13568\n\t"
"v_mfma_f32_16x16x32_bf16 a[128:131], v[236:239], a[180:183], a[128:131]\n\t"
"ds_read_b128 a[180:183], v222 offset:21248\n\t"
"v_mfma_f32_16x16x32_bf16 a[128:131], v[248:251], a[184:187], a[128:131]\n\t"
"v_mfma_f32_16x16x32_bf16 a[128:131], v[240:243], a[188:191], a[128:131]\n\t"
"v_mfma_f32_16x16x32_bf16 a[136:139], v[240:243], a[192:195], a[136:139]\n\t"
"v_mfma_f32_16x16x32_bf16 a[136:139], v[248:251], a[188:191], a[136:139]\n\t"
"v_mfma_f32_16x16x32_bf16 a[136:139], v[236:239], a[184:187], a[136:139]\n\t"
"ds_read_b128 a[184:187], v223 offset:27392\n\t"
"v_mfma_f32_16x16x32_bf16 a[144:147], v[236:239], a[188:191], a[144:147]\n\t"
"ds_read_b128 a[188:191], v223 offset:33536\n\t"
"v_mfma_f32_16x16x32_bf16 a[144:147], v[248:251], a[192:195], a[144:147]\n\t"
"v_mfma_f32_16x16x32_bf16 a[144:147], v[240:243], a[196:199], a[144:147]\n\t"
"s_mov_b32 s20, 897024\n\t"
"v_mfma_f32_16x16x32_bf16 a[152:155], v[240:243], a[200:203], a[152:155]\n\t"
"v_mfma_f32_16x16x32_bf16 a[152:155], v[248:251], a[196:199], a[152:155]\n\t"
"v_mfma_f32_16x16x32_bf16 a[152:155], v[236:239], a[192:195], a[152:155]\n\t"
"ds_read_b128 a[192:195], v222 offset:41216\n\t"
"v_mfma_f32_16x16x32_bf16 a[160:163], v[236:239], a[196:199], a[160:163]\n\t"
"s_waitcnt vmcnt(9) lgkmcnt(4)\n\t"
"ds_read_b128 a[196:199], v223 offset:47360\n\t"
"v_mfma_f32_16x16x32_bf16 a[160:163], v[248:251], a[200:203], a[160:163]\n\t"
"ds_read_b128 a[200:203], v223 offset:53504\n\t"
"v_mfma_f32_16x16x32_bf16 a[160:163], v[240:243], a[204:207], a[160:163]\n\t"
"ds_read_b128 a[204:207], v222 offset:61184\n\t"
"v_mfma_f32_16x16x32_bf16 v[200:203], a[208:211], a[168:171], v[200:203]\n\t"
"buffer_load_dwordx4 v[240:243], off, s[80:83], s20 offset:0 sc1\n\t"
"v_mfma_f32_16x16x32_bf16 v[200:203], a[224:227], a[176:179], v[200:203]\n\t"
"s_waitcnt lgkmcnt(6)\n\t"
"v_mfma_f32_16x16x32_bf16 v[200:203], a[220:223], a[172:175], v[200:203]\n\t"
"s_add_u32 m0, s24, 20480\n\t"
"s_add_u32 s28, s21, 589824\n\t"
"v_mfma_f32_16x16x32_bf16 v[208:211], a[208:211], a[172:175], v[208:211]\n\t"
"s_waitcnt lgkmcnt(5)\n\t"
"v_mfma_f32_16x16x32_bf16 v[208:211], a[220:223], a[176:179], v[208:211]\n\t"
"v_mfma_f32_16x16x32_bf16 v[208:211], a[224:227], a[180:183], v[208:211]\n\t"
"s_waitcnt lgkmcnt(0)\n\t"
"s_barrier\n\t"
"v_mfma_f32_16x16x32_bf16 v[216:219], a[224:227], a[184:187], v[216:219]\n\t"
"v_mfma_f32_16x16x32_bf16 v[216:219], a[220:223], a[180:183], v[216:219]\n\t"
"buffer_load_dwordx4 v247, s[12:15], s28 offen offset:0 nt lds\n\t"
"v_mfma_f32_16x16x32_bf16 v[216:219], a[208:211], a[176:179], v[216:219]\n\t"
"s_add_u32 m0, s24, 26624\n\t"
"s_add_u32 s28, s21, 786432\n\t"
"v_pk_fma_f32 v[196:197], v[200:201], s[56:57], v[196:197]\n\t"
"v_pk_fma_f32 v[200:201], v[196:197], s[58:59], v[200:201] neg_lo:[0,0,1] neg_hi:[0,0,1]\n\t"
"v_pk_fma_f32 v[228:229], v[0:1], s[60:61], v[196:197]\n\t"
"v_pk_fma_f32 v[0:1], v[196:197], s[60:61], v[0:1]\n\t"
"v_pk_fma_f32 v[196:197], v[100:101], s[62:63], v[200:201]\n\t"
"v_pk_fma_f32 v[100:101], v[200:201], s[62:63], v[100:101]\n\t"
"v_pk_fma_f32 v[198:199], v[202:203], s[56:57], v[198:199]\n\t"
"v_pk_fma_f32 v[202:203], v[198:199], s[58:59], v[202:203] neg_lo:[0,0,1] neg_hi:[0,0,1]\n\t"
"v_pk_fma_f32 v[234:235], v[2:3], s[60:61], v[198:199]\n\t"
"v_pk_fma_f32 v[2:3], v[198:199], s[60:61], v[2:3]\n\t"
"v_pk_fma_f32 v[198:199], v[102:103], s[62:63], v[202:203]\n\t"
"v_pk_fma_f32 v[102:103], v[202:203], s[62:63], v[102:103]\n\t"
"v_mfma_f32_16x16x32_bf16 a[4:7], a[208:211], a[180:183], a[4:7]\n\t"
"v_cvt_pk_bf16_f32 v196, v196, v197\n\t"
"v_cvt_pk_bf16_f32 v200, v228, v229\n\t"
"v_mfma_f32_16x16x32_bf16 a[4:7], a[220:223], a[184:187], a[4:7]\n\t"
"v_cvt_pk_bf16_f32 v197, v198, v199\n\t"
"v_cvt_pk_bf16_f32 v201, v234, v235\n\t"
"v_mfma_f32_16x16x32_bf16 a[4:7], a[224:227], a[188:191], a[4:7]\n\t"
"buffer_load_dwordx4 v247, s[12:15], s28 offen offset:0 nt lds\n\t"
"s_add_u32 m0, s24, 32768\n\t"
"s_add_u32 s28, s21, 983040\n\t"
"v_pk_fma_f32 v[204:205], v[208:209], s[56:57], v[204:205]\n\t"
"v_pk_fma_f32 v[208:209], v[204:205], s[58:59], v[208:209] neg_lo:[0,0,1] neg_hi:[0,0,1]\n\t"
"v_pk_fma_f32 v[98:99], v[4:5], s[60:61], v[204:205]\n\t"
"v_pk_fma_f32 v[4:5], v[204:205], s[60:61], v[4:5]\n\t"
"v_pk_fma_f32 v[204:205], v[104:105], s[62:63], v[208:209]\n\t"
"v_pk_fma_f32 v[104:105], v[208:209], s[62:63], v[104:105]\n\t"
"v_mfma_f32_16x16x32_bf16 a[12:15], a[224:227], a[192:195], a[12:15]\n\t"
"v_cvt_pk_bf16_f32 v204, v204, v205\n\t"
"v_cvt_pk_bf16_f32 v208, v98, v99\n\t"
"v_mfma_f32_16x16x32_bf16 a[12:15], a[220:223], a[188:191], a[12:15]\n\t"
"v_mfma_f32_16x16x32_bf16 a[12:15], a[208:211], a[184:187], a[12:15]\n\t"
"buffer_load_dwordx4 v247, s[12:15], s28 offen offset:0 nt lds\n\t"
"s_add_u32 m0, s24, 40448\n\t"
"s_add_u32 s28, s21, 1179648\n\t"
"v_pk_fma_f32 v[206:207], v[210:211], s[56:57], v[206:207]\n\t"
"v_pk_fma_f32 v[210:211], v[206:207], s[58:59], v[210:211] neg_lo:[0,0,1] neg_hi:[0,0,1]\n\t"
"v_pk_fma_f32 v[228:229], v[6:7], s[60:61], v[206:207]\n\t"
"v_pk_fma_f32 v[6:7], v[206:207], s[60:61], v[6:7]\n\t"
"v_pk_fma_f32 v[206:207], v[106:107], s[62:63], v[210:211]\n\t"
"v_pk_fma_f32 v[106:107], v[210:211], s[62:63], v[106:107]\n\t"
"v_mfma_f32_16x16x32_bf16 a[20:23], a[208:211], a[188:191], a[20:23]\n\t"
"v_cvt_pk_bf16_f32 v205, v206, v207\n\t"
"v_cvt_pk_bf16_f32 v209, v228, v229\n\t"
"v_mfma_f32_16x16x32_bf16 a[20:23], a[220:223], a[192:195], a[20:23]\n\t"
"v_accvgpr_read_b32 v224, a0\n\t"
"v_accvgpr_read_b32 v226, a4\n\t"
"v_mfma_f32_16x16x32_bf16 a[20:23], a[224:227], a[196:199], a[20:23]\n\t"
"buffer_load_dwordx4 v247, s[12:15], s28 offen offset:0 nt lds\n\t"
"v_accvgpr_read_b32 v225, a1\n\t"
"v_accvgpr_read_b32 v227, a5\n\t"
"s_add_u32 m0, s24, 46592\n\t"
"s_add_u32 s28, s21, 1376256\n\t"
"v_pk_fma_f32 v[212:213], v[216:217], s[56:57], v[212:213]\n\t"
"v_pk_fma_f32 v[216:217], v[212:213], s[58:59], v[216:217] neg_lo:[0,0,1] neg_hi:[0,0,1]\n\t"
"v_pk_fma_f32 v[234:235], v[8:9], s[60:61], v[212:213]\n\t"
"v_pk_fma_f32 v[8:9], v[212:213], s[60:61], v[8:9]\n\t"
"v_pk_fma_f32 v[212:213], v[108:109], s[62:63], v[216:217]\n\t"
"v_pk_fma_f32 v[108:109], v[216:217], s[62:63], v[108:109]\n\t"
"v_mfma_f32_16x16x32_bf16 a[28:31], a[224:227], a[200:203], a[28:31]\n\t"
"v_cvt_pk_bf16_f32 v212, v212, v213\n\t"
"v_cvt_pk_bf16_f32 v216, v234, v235\n\t"
"v_mfma_f32_16x16x32_bf16 a[28:31], a[220:223], a[196:199], a[28:31]\n\t"
"v_accvgpr_read_b32 v230, a2\n\t"
"v_accvgpr_read_b32 v232, a6\n\t"
"v_mfma_f32_16x16x32_bf16 a[28:31], a[208:211], a[192:195], a[28:31]\n\t"
"buffer_load_dwordx4 v247, s[12:15], s28 offen offset:0 nt lds\n\t"
"v_accvgpr_read_b32 v231, a3\n\t"
"v_accvgpr_read_b32 v233, a7\n\t"
"v_pk_fma_f32 v[214:215], v[218:219], s[56:57], v[214:215]\n\t"
"v_pk_fma_f32 v[218:219], v[214:215], s[58:59], v[218:219] neg_lo:[0,0,1] neg_hi:[0,0,1]\n\t"
"v_pk_fma_f32 v[98:99], v[10:11], s[60:61], v[214:215]\n\t"
"v_pk_fma_f32 v[10:11], v[214:215], s[60:61], v[10:11]\n\t"
"v_pk_fma_f32 v[214:215], v[110:111], s[62:63], v[218:219]\n\t"
"v_pk_fma_f32 v[110:111], v[218:219], s[62:63], v[110:111]\n\t"
"v_mfma_f32_16x16x32_bf16 a[36:39], a[208:211], a[196:199], a[36:39]\n\t"
"s_waitcnt vmcnt(12)\n\t"
"v_cvt_pk_bf16_f32 v213, v214, v215\n\t"
"v_cvt_pk_bf16_f32 v217, v98, v99\n\t"
"v_mfma_f32_16x16x32_bf16 a[36:39], a[220:223], a[200:203], a[36:39]\n\t"
"v_accvgpr_read_b32 v236, a8\n\t"
"v_accvgpr_read_b32 v238, a12\n\t"
"v_mfma_f32_16x16x32_bf16 a[36:39], a[224:227], a[204:207], a[36:39]\n\t"
"buffer_load_dwordx4 a[208:211], off, s[80:83], s20 offset:1024 sc1\n\t"
"v_accvgpr_read_b32 v237, a9\n\t"
"v_accvgpr_read_b32 v239, a13\n\t"
"v_pk_fma_f32 v[224:225], v[226:227], s[56:57], v[224:225]\n\t"
"v_pk_fma_f32 v[226:227], v[224:225], s[58:59], v[226:227] neg_lo:[0,0,1] neg_hi:[0,0,1]\n\t"
"v_pk_fma_f32 v[228:229], v[12:13], s[60:61], v[224:225]\n\t"
"v_pk_fma_f32 v[12:13], v[224:225], s[60:61], v[12:13]\n\t"
"v_pk_fma_f32 v[224:225], v[112:113], s[62:63], v[226:227]\n\t"
"v_pk_fma_f32 v[112:113], v[226:227], s[62:63], v[112:113]\n\t"
"v_mfma_f32_16x16x32_bf16 a[44:47], a[212:215], a[168:171], a[44:47]\n\t"
"v_cvt_pk_bf16_f32 v224, v224, v225\n\t"
"v_cvt_pk_bf16_f32 v228, v228, v229\n\t"
"v_mfma_f32_16x16x32_bf16 a[44:47], a[232:235], a[176:179], a[44:47]\n\t"
"v_accvgpr_write_b32 a0, v224\n\t"
"v_accvgpr_write_b32 a4, v228\n\t"
"v_mfma_f32_16x16x32_bf16 a[44:47], a[228:231], a[172:175], a[44:47]\n\t"
"v_accvgpr_read_b32 v224, a10\n\t"
"v_accvgpr_read_b32 v226, a14\n\t"
"v_accvgpr_read_b32 v225, a11\n\t"
"v_accvgpr_read_b32 v227, a15\n\t"
"v_pk_fma_f32 v[230:231], v[232:233], s[56:57], v[230:231]\n\t"
"v_pk_fma_f32 v[232:233], v[230:231], s[58:59], v[232:233] neg_lo:[0,0,1] neg_hi:[0,0,1]\n\t"
"v_pk_fma_f32 v[234:235], v[14:15], s[60:61], v[230:231]\n\t"
"v_pk_fma_f32 v[14:15], v[230:231], s[60:61], v[14:15]\n\t"
"v_pk_fma_f32 v[230:231], v[114:115], s[62:63], v[232:233]\n\t"
"v_pk_fma_f32 v[114:115], v[232:233], s[62:63], v[114:115]\n\t"
"v_mfma_f32_16x16x32_bf16 a[52:55], a[212:215], a[172:175], a[52:55]\n\t"
"v_cvt_pk_bf16_f32 v230, v230, v231\n\t"
"v_cvt_pk_bf16_f32 v234, v234, v235\n\t"
"v_mfma_f32_16x16x32_bf16 a[52:55], a[228:231], a[176:179], a[52:55]\n\t"
"v_accvgpr_write_b32 a1, v230\n\t"
"v_accvgpr_write_b32 a5, v234\n\t"
"v_mfma_f32_16x16x32_bf16 a[52:55], a[232:235], a[180:183], a[52:55]\n\t"
"v_accvgpr_read_b32 v230, a16\n\t"
"v_accvgpr_read_b32 v232, a20\n\t"
"v_accvgpr_read_b32 v231, a17\n\t"
"v_accvgpr_read_b32 v233, a21\n\t"
"v_pk_fma_f32 v[236:237], v[238:239], s[56:57], v[236:237]\n\t"
"v_pk_fma_f32 v[238:239], v[236:237], s[58:59], v[238:239] neg_lo:[0,0,1] neg_hi:[0,0,1]\n\t"
"v_pk_fma_f32 v[98:99], v[16:17], s[60:61], v[236:237]\n\t"
"v_pk_fma_f32 v[16:17], v[236:237], s[60:61], v[16:17]\n\t"
"v_pk_fma_f32 v[236:237], v[116:117], s[62:63], v[238:239]\n\t"
"v_pk_fma_f32 v[116:117], v[238:239], s[62:63], v[116:117]\n\t"
"v_mfma_f32_16x16x32_bf16 a[60:63], a[232:235], a[184:187], a[60:63]\n\t"
"v_cvt_pk_bf16_f32 v236, v236, v237\n\t"
"v_cvt_pk_bf16_f32 v98, v98, v99\n\t"
"v_mfma_f32_16x16x32_bf16 a[60:63], a[228:231], a[180:183], a[60:63]\n\t"
"v_accvgpr_write_b32 a8, v236\n\t"
"v_accvgpr_write_b32 a12, v98\n\t"
"v_mfma_f32_16x16x32_bf16 a[60:63], a[212:215], a[176:179], a[60:63]\n\t"
"buffer_load_dwordx4 a[220:223], off, s[80:83], s20 offset:2048 sc1\n\t"
"v_accvgpr_read_b32 v236, a18\n\t"
"v_accvgpr_read_b32 v238, a22\n\t"
"v_pk_fma_f32 v[224:225], v[226:227], s[56:57], v[224:225]\n\t"
"v_pk_fma_f32 v[226:227], v[224:225], s[58:59], v[226:227] neg_lo:[0,0,1] neg_hi:[0,0,1]\n\t"
"v_pk_fma_f32 v[228:229], v[18:19], s[60:61], v[224:225]\n\t"
"v_pk_fma_f32 v[18:19], v[224:225], s[60:61], v[18:19]\n\t"
"v_pk_fma_f32 v[224:225], v[118:119], s[62:63], v[226:227]\n\t"
"v_pk_fma_f32 v[118:119], v[226:227], s[62:63], v[118:119]\n\t"
"v_pk_fma_f32 v[230:231], v[232:233], s[56:57], v[230:231]\n\t"
"v_pk_fma_f32 v[232:233], v[230:231], s[58:59], v[232:233] neg_lo:[0,0,1] neg_hi:[0,0,1]\n\t"
"v_pk_fma_f32 v[234:235], v[20:21], s[60:61], v[230:231]\n\t"
"v_pk_fma_f32 v[20:21], v[230:231], s[60:61], v[20:21]\n\t"
"v_pk_fma_f32 v[230:231], v[120:121], s[62:63], v[232:233]\n\t"
"v_pk_fma_f32 v[120:121], v[232:233], s[62:63], v[120:121]\n\t"
"v_mfma_f32_16x16x32_bf16 a[68:71], a[212:215], a[180:183], a[68:71]\n\t"
"s_mov_b32 s20, 909312\n\t"
"v_accvgpr_read_b32 v237, a19\n\t"
"v_accvgpr_read_b32 v239, a23\n\t"
"v_mfma_f32_16x16x32_bf16 a[68:71], a[228:231], a[184:187], a[68:71]\n\t"
"v_cvt_pk_bf16_f32 v224, v224, v225\n\t"
"v_cvt_pk_bf16_f32 v228, v228, v229\n\t"
"v_mfma_f32_16x16x32_bf16 a[68:71], a[232:235], a[188:191], a[68:71]\n\t"
"buffer_load_dwordx4 a[224:227], off, s[80:83], s20 offset:0 sc1\n\t"
"v_cvt_pk_bf16_f32 v230, v230, v231\n\t"
"v_cvt_pk_bf16_f32 v234, v234, v235\n\t"
"v_pk_fma_f32 v[236:237], v[238:239], s[56:57], v[236:237]\n\t"
"v_pk_fma_f32 v[238:239], v[236:237], s[58:59], v[238:239] neg_lo:[0,0,1] neg_hi:[0,0,1]\n\t"
"v_pk_fma_f32 v[98:99], v[22:23], s[60:61], v[236:237]\n\t"
"v_pk_fma_f32 v[22:23], v[236:237], s[60:61], v[22:23]\n\t"
"v_pk_fma_f32 v[236:237], v[122:123], s[62:63], v[238:239]\n\t"
"v_pk_fma_f32 v[122:123], v[238:239], s[62:63], v[122:123]\n\t"
"v_mfma_f32_16x16x32_bf16 a[76:79], a[232:235], a[192:195], a[76:79]\n\t"
"v_accvgpr_write_b32 a9, v224\n\t"
"v_accvgpr_write_b32 a13, v228\n\t"
"v_mfma_f32_16x16x32_bf16 a[76:79], a[228:231], a[188:191], a[76:79]\n\t"
"v_accvgpr_write_b32 a16, v230\n\t"
"v_accvgpr_write_b32 a20, v234\n\t"
"v_mfma_f32_16x16x32_bf16 a[76:79], a[212:215], a[184:187], a[76:79]\n\t"
"v_accvgpr_read_b32 v224, a24\n\t"
"v_accvgpr_read_b32 v226, a28\n\t"
"v_accvgpr_read_b32 v225, a25\n\t"
"v_accvgpr_read_b32 v227, a29\n\t"
"v_cvt_pk_bf16_f32 v236, v236, v237\n\t"
"v_cvt_pk_bf16_f32 v98, v98, v99\n\t"
"v_pk_fma_f32 v[224:225], v[226:227], s[56:57], v[224:225]\n\t"
"v_pk_fma_f32 v[226:227], v[224:225], s[58:59], v[226:227] neg_lo:[0,0,1] neg_hi:[0,0,1]\n\t"
"v_pk_fma_f32 v[228:229], v[24:25], s[60:61], v[224:225]\n\t"
"v_pk_fma_f32 v[24:25], v[224:225], s[60:61], v[24:25]\n\t"
"v_pk_fma_f32 v[224:225], v[124:125], s[62:63], v[226:227]\n\t"
"v_pk_fma_f32 v[124:125], v[226:227], s[62:63], v[124:125]\n\t"
"v_mfma_f32_16x16x32_bf16 a[84:87], a[212:215], a[188:191], a[84:87]\n\t"
"v_accvgpr_write_b32 a17, v236\n\t"
"v_accvgpr_write_b32 a21, v98\n\t"
"v_mfma_f32_16x16x32_bf16 a[84:87], a[228:231], a[192:195], a[84:87]\n\t"
"v_accvgpr_read_b32 v230, a26\n\t"
"v_accvgpr_read_b32 v232, a30\n\t"
"v_mfma_f32_16x16x32_bf16 a[84:87], a[232:235], a[196:199], a[84:87]\n\t"
"v_accvgpr_read_b32 v231, a27\n\t"
"v_accvgpr_read_b32 v233, a31\n\t"
"v_cvt_pk_bf16_f32 v224, v224, v225\n\t"
"v_cvt_pk_bf16_f32 v228, v228, v229\n\t"
"v_accvgpr_write_b32 a24, v224\n\t"
"v_pk_fma_f32 v[230:231], v[232:233], s[56:57], v[230:231]\n\t"
"v_pk_fma_f32 v[232:233], v[230:231], s[58:59], v[232:233] neg_lo:[0,0,1] neg_hi:[0,0,1]\n\t"
"v_pk_fma_f32 v[234:235], v[26:27], s[60:61], v[230:231]\n\t"
"v_pk_fma_f32 v[26:27], v[230:231], s[60:61], v[26:27]\n\t"
"v_pk_fma_f32 v[230:231], v[126:127], s[62:63], v[232:233]\n\t"
"v_pk_fma_f32 v[126:127], v[232:233], s[62:63], v[126:127]\n\t"
"v_mfma_f32_16x16x32_bf16 a[92:95], a[232:235], a[200:203], a[92:95]\n\t"
"v_accvgpr_write_b32 a28, v228\n\t"
"v_accvgpr_read_b32 v236, a32\n\t"
"v_mfma_f32_16x16x32_bf16 a[92:95], a[228:231], a[196:199], a[92:95]\n\t"
"v_accvgpr_read_b32 v238, a36\n\t"
"v_accvgpr_read_b32 v237, a33\n\t"
"v_mfma_f32_16x16x32_bf16 a[92:95], a[212:215], a[192:195], a[92:95]\n\t"
"v_accvgpr_read_b32 v239, a37\n\t"
"v_cvt_pk_bf16_f32 v230, v230, v231\n\t"
"v_cvt_pk_bf16_f32 v234, v234, v235\n\t"
"v_accvgpr_write_b32 a25, v230\n\t"
"v_accvgpr_write_b32 a29, v234\n\t"
"v_pk_fma_f32 v[236:237], v[238:239], s[56:57], v[236:237]\n\t"
"v_pk_fma_f32 v[238:239], v[236:237], s[58:59], v[238:239] neg_lo:[0,0,1] neg_hi:[0,0,1]\n\t"
"v_pk_fma_f32 v[98:99], v[28:29], s[60:61], v[236:237]\n\t"
"v_pk_fma_f32 v[28:29], v[236:237], s[60:61], v[28:29]\n\t"
"v_pk_fma_f32 v[236:237], v[128:129], s[62:63], v[238:239]\n\t"
"v_pk_fma_f32 v[128:129], v[238:239], s[62:63], v[128:129]\n\t"
"v_mfma_f32_16x16x32_bf16 a[100:103], a[212:215], a[196:199], a[100:103]\n\t"
"s_waitcnt vmcnt(12)\n\t"
"v_accvgpr_read_b32 v224, a34\n\t"
"v_accvgpr_read_b32 v226, a38\n\t"
"v_mfma_f32_16x16x32_bf16 a[100:103], a[228:231], a[200:203], a[100:103]\n\t"
"v_accvgpr_read_b32 v225, a35\n\t"
"v_accvgpr_read_b32 v227, a39\n\t"
"v_mfma_f32_16x16x32_bf16 a[100:103], a[232:235], a[204:207], a[100:103]\n\t"
"buffer_load_dwordx4 a[212:215], off, s[80:83], s20 offset:1024 sc1\n\t"
"v_cvt_pk_bf16_f32 v236, v236, v237\n\t"
"v_cvt_pk_bf16_f32 v98, v98, v99\n\t"
"v_pk_fma_f32 v[224:225], v[226:227], s[56:57], v[224:225]\n\t"
"v_pk_fma_f32 v[226:227], v[224:225], s[58:59], v[226:227] neg_lo:[0,0,1] neg_hi:[0,0,1]\n\t"
"v_pk_fma_f32 v[228:229], v[30:31], s[60:61], v[224:225]\n\t"
"v_pk_fma_f32 v[30:31], v[224:225], s[60:61], v[30:31]\n\t"
"v_pk_fma_f32 v[224:225], v[130:131], s[62:63], v[226:227]\n\t"
"v_pk_fma_f32 v[130:131], v[226:227], s[62:63], v[130:131]\n\t"
"v_mfma_f32_16x16x32_bf16 a[108:111], a[236:239], a[168:171], a[108:111]\n\t"
"Lpg10_%=:\n\t"
"s_cbranch_vccnz Lvr0_%=\n\t"
"ds_read_b128 a[168:171], v244 offset:0\n\t"
"v_accvgpr_write_b32 a32, v236\n\t"
"v_mfma_f32_16x16x32_bf16 a[108:111], a[244:247], a[176:179], a[108:111]\n\t"
"v_accvgpr_write_b32 a36, v98\n\t"
"v_accvgpr_read_b32 v230, a40\n\t"
"v_mfma_f32_16x16x32_bf16 a[108:111], a[240:243], a[172:175], a[108:111]\n\t"
"buffer_load_dwordx4 a[228:231], off, s[80:83], s20 offset:2048 sc1\n\t"
"v_accvgpr_read_b32 v232, a44\n\t"
"v_accvgpr_read_b32 v231, a41\n\t"
"v_accvgpr_read_b32 v233, a45\n\t"
"v_cvt_pk_bf16_f32 v224, v224, v225\n\t"
"v_cvt_pk_bf16_f32 v228, v228, v229\n\t"
"s_mov_b32 s20, 1069056\n\t"
"v_pk_fma_f32 v[230:231], v[232:233], s[56:57], v[230:231]\n\t"
"v_pk_fma_f32 v[232:233], v[230:231], s[58:59], v[232:233] neg_lo:[0,0,1] neg_hi:[0,0,1]\n\t"
"v_pk_fma_f32 v[234:235], v[32:33], s[60:61], v[230:231]\n\t"
"v_pk_fma_f32 v[32:33], v[230:231], s[60:61], v[32:33]\n\t"
"v_pk_fma_f32 v[230:231], v[132:133], s[62:63], v[232:233]\n\t"
"v_pk_fma_f32 v[132:133], v[232:233], s[62:63], v[132:133]\n\t"
"v_mfma_f32_16x16x32_bf16 a[116:119], a[236:239], a[172:175], a[116:119]\n\t"
"ds_read_b128 a[172:175], v244 offset:512\n\t"
"v_accvgpr_write_b32 a33, v224\n\t"
"v_mfma_f32_16x16x32_bf16 a[116:119], a[240:243], a[176:179], a[116:119]\n\t"
"s_add_u32 m0, s24, 52736\n\t"
"s_add_u32 s28, s21, 1572864\n\t"
"v_accvgpr_write_b32 a37, v228\n\t"
"v_mfma_f32_16x16x32_bf16 a[116:119], a[244:247], a[180:183], a[116:119]\n\t"
"buffer_load_dwordx4 a[232:235], off, s[80:83], s20 offset:0 sc1\n\t"
"v_accvgpr_read_b32 v236, a42\n\t"
"v_accvgpr_read_b32 v238, a46\n\t"
"v_accvgpr_read_b32 v237, a43\n\t"
"v_accvgpr_read_b32 v239, a47\n\t"
"v_pk_fma_f32 v[236:237], v[238:239], s[56:57], v[236:237]\n\t"
"v_pk_fma_f32 v[238:239], v[236:237], s[58:59], v[238:239] neg_lo:[0,0,1] neg_hi:[0,0,1]\n\t"
"v_pk_fma_f32 v[98:99], v[34:35], s[60:61], v[236:237]\n\t"
"v_pk_fma_f32 v[34:35], v[236:237], s[60:61], v[34:35]\n\t"
"v_pk_fma_f32 v[236:237], v[134:135], s[62:63], v[238:239]\n\t"
"v_pk_fma_f32 v[134:135], v[238:239], s[62:63], v[134:135]\n\t"
"v_mfma_f32_16x16x32_bf16 a[124:127], a[244:247], a[184:187], a[124:127]\n\t"
"v_cvt_pk_bf16_f32 v198, v230, v231\n\t"
"v_cvt_pk_bf16_f32 v202, v234, v235\n\t"
"v_mfma_f32_16x16x32_bf16 a[124:127], a[240:243], a[180:183], a[124:127]\n\t"
"v_accvgpr_read_b32 v224, a48\n\t"
"v_accvgpr_read_b32 v226, a52\n\t"
"v_mfma_f32_16x16x32_bf16 a[124:127], a[236:239], a[176:179], a[124:127]\n\t"
"buffer_load_dwordx4 v247, s[12:15], s28 offen offset:0 nt lds\n\t"
"ds_read_b128 a[176:179], v244 offset:1024\n\t"
"v_accvgpr_read_b32 v225, a49\n\t"
"v_accvgpr_read_b32 v227, a53\n\t"
"v_accvgpr_read_b32 v230, a50\n\t"
"v_accvgpr_read_b32 v232, a54\n\t"
"v_accvgpr_read_b32 v231, a51\n\t"
"v_accvgpr_read_b32 v233, a55\n\t"
"v_pk_fma_f32 v[224:225], v[226:227], s[56:57], v[224:225]\n\t"
"v_pk_fma_f32 v[226:227], v[224:225], s[58:59], v[226:227] neg_lo:[0,0,1] neg_hi:[0,0,1]\n\t"
"v_pk_fma_f32 v[228:229], v[36:37], s[60:61], v[224:225]\n\t"
"v_pk_fma_f32 v[36:37], v[224:225], s[60:61], v[36:37]\n\t"
"v_pk_fma_f32 v[224:225], v[136:137], s[62:63], v[226:227]\n\t"
"v_pk_fma_f32 v[136:137], v[226:227], s[62:63], v[136:137]\n\t"
"v_pk_fma_f32 v[230:231], v[232:233], s[56:57], v[230:231]\n\t"
"v_pk_fma_f32 v[232:233], v[230:231], s[58:59], v[232:233] neg_lo:[0,0,1] neg_hi:[0,0,1]\n\t"
"v_pk_fma_f32 v[234:235], v[38:39], s[60:61], v[230:231]\n\t"
"v_pk_fma_f32 v[38:39], v[230:231], s[60:61], v[38:39]\n\t"
"v_pk_fma_f32 v[230:231], v[138:139], s[62:63], v[232:233]\n\t"
"v_pk_fma_f32 v[138:139], v[232:233], s[62:63], v[138:139]\n\t"
"v_mfma_f32_16x16x32_bf16 a[132:135], a[236:239], a[180:183], a[132:135]\n\t"
"s_add_u32 m0, s24, 512\n\t"
"s_add_u32 s28, s21, 0\n\t"
"ds_read_b128 a[180:183], v244 offset:1536\n\t"
"v_mfma_f32_16x16x32_bf16 a[132:135], a[240:243], a[184:187], a[132:135]\n\t"
"buffer_load_dwordx4 a[40:43], off, s[80:83], s20 offset:1024 sc1\n\t"
"v_mfma_f32_16x16x32_bf16 a[132:135], a[244:247], a[188:191], a[132:135]\n\t"
"buffer_load_dwordx4 v247, s[12:15], s28 offen offset:0 nt lds\n\t"
"v_cvt_pk_bf16_f32 v199, v236, v237\n\t"
"v_cvt_pk_bf16_f32 v203, v98, v99\n\t"
"v_accvgpr_read_b32 v236, a56\n\t"
"v_accvgpr_read_b32 v238, a60\n\t"
"v_accvgpr_read_b32 v237, a57\n\t"
"v_accvgpr_read_b32 v239, a61\n\t"
"s_add_u32 m0, s24, 6656\n\t"
"s_add_u32 s28, s21, 196608\n\t"
"v_pk_fma_f32 v[236:237], v[238:239], s[56:57], v[236:237]\n\t"
"v_pk_fma_f32 v[238:239], v[236:237], s[58:59], v[238:239] neg_lo:[0,0,1] neg_hi:[0,0,1]\n\t"
"v_pk_fma_f32 v[98:99], v[40:41], s[60:61], v[236:237]\n\t"
"v_pk_fma_f32 v[40:41], v[236:237], s[60:61], v[40:41]\n\t"
"v_pk_fma_f32 v[236:237], v[140:141], s[62:63], v[238:239]\n\t"
"v_pk_fma_f32 v[140:141], v[238:239], s[62:63], v[140:141]\n\t"
"v_mfma_f32_16x16x32_bf16 a[140:143], a[244:247], a[192:195], a[140:143]\n\t"
"v_cvt_pk_bf16_f32 v206, v224, v225\n\t"
"v_cvt_pk_bf16_f32 v210, v228, v229\n\t"
"v_mfma_f32_16x16x32_bf16 a[140:143], a[240:243], a[188:191], a[140:143]\n\t"
"v_cvt_pk_bf16_f32 v207, v230, v231\n\t"
"v_cvt_pk_bf16_f32 v211, v234, v235\n\t"
"v_mfma_f32_16x16x32_bf16 a[140:143], a[236:239], a[184:187], a[140:143]\n\t"
"buffer_load_dwordx4 v247, s[12:15], s28 offen offset:0 nt lds\n\t"
"ds_read_b128 a[184:187], v244 offset:2048\n\t"
"v_accvgpr_read_b32 v224, a58\n\t"
"v_accvgpr_read_b32 v226, a62\n\t"
"v_accvgpr_read_b32 v225, a59\n\t"
"v_accvgpr_read_b32 v227, a63\n\t"
"s_add_u32 m0, s24, 12800\n\t"
"s_add_u32 s28, s21, 393216\n\t"
"v_pk_fma_f32 v[224:225], v[226:227], s[56:57], v[224:225]\n\t"
"v_pk_fma_f32 v[226:227], v[224:225], s[58:59], v[226:227] neg_lo:[0,0,1] neg_hi:[0,0,1]\n\t"
"v_pk_fma_f32 v[228:229], v[42:43], s[60:61], v[224:225]\n\t"
"v_pk_fma_f32 v[42:43], v[224:225], s[60:61], v[42:43]\n\t"
"v_pk_fma_f32 v[224:225], v[142:143], s[62:63], v[226:227]\n\t"
"v_pk_fma_f32 v[142:143], v[226:227], s[62:63], v[142:143]\n\t"
"v_mfma_f32_16x16x32_bf16 a[148:151], a[236:239], a[188:191], a[148:151]\n\t"
"ds_read_b128 a[188:191], v244 offset:2560\n\t"
"v_cvt_pk_bf16_f32 v214, v236, v237\n\t"
"v_mfma_f32_16x16x32_bf16 a[148:151], a[240:243], a[192:195], a[148:151]\n\t"
"v_cvt_pk_bf16_f32 v218, v98, v99\n\t"
"v_accvgpr_read_b32 v230, a64\n\t"
"v_mfma_f32_16x16x32_bf16 a[148:151], a[244:247], a[196:199], a[148:151]\n\t"
"buffer_load_dwordx4 v247, s[12:15], s28 offen offset:0 nt lds\n\t"
"v_accvgpr_read_b32 v232, a68\n\t"
"v_accvgpr_read_b32 v231, a65\n\t"
"v_accvgpr_read_b32 v233, a69\n\t"
"v_pk_fma_f32 v[230:231], v[232:233], s[56:57], v[230:231]\n\t"
"v_pk_fma_f32 v[232:233], v[230:231], s[58:59], v[232:233] neg_lo:[0,0,1] neg_hi:[0,0,1]\n\t"
"v_pk_fma_f32 v[234:235], v[44:45], s[60:61], v[230:231]\n\t"
"v_pk_fma_f32 v[44:45], v[230:231], s[60:61], v[44:45]\n\t"
"v_pk_fma_f32 v[230:231], v[144:145], s[62:63], v[232:233]\n\t"
"v_pk_fma_f32 v[144:145], v[232:233], s[62:63], v[144:145]\n\t"
"v_mfma_f32_16x16x32_bf16 a[156:159], a[244:247], a[200:203], a[156:159]\n\t"
"v_cvt_pk_bf16_f32 v215, v224, v225\n\t"
"v_cvt_pk_bf16_f32 v219, v228, v229\n\t"
"v_mfma_f32_16x16x32_bf16 a[156:159], a[240:243], a[196:199], a[156:159]\n\t"
"v_accvgpr_read_b32 v236, a66\n\t"
"v_accvgpr_read_b32 v238, a70\n\t"
"v_mfma_f32_16x16x32_bf16 a[156:159], a[236:239], a[192:195], a[156:159]\n\t"
"buffer_load_dwordx4 a[48:51], off, s[80:83], s20 offset:2048 sc1\n\t"
"ds_read_b128 a[192:195], v244 offset:3072\n\t"
"v_accvgpr_read_b32 v237, a67\n\t"
"v_accvgpr_read_b32 v239, a71\n\t"
"v_cvt_pk_bf16_f32 v230, v230, v231\n\t"
"v_cvt_pk_bf16_f32 v234, v234, v235\n\t"
"v_pk_fma_f32 v[236:237], v[238:239], s[56:57], v[236:237]\n\t"
"v_pk_fma_f32 v[238:239], v[236:237], s[58:59], v[238:239] neg_lo:[0,0,1] neg_hi:[0,0,1]\n\t"
"v_pk_fma_f32 v[98:99], v[46:47], s[60:61], v[236:237]\n\t"
"v_pk_fma_f32 v[46:47], v[236:237], s[60:61], v[46:47]\n\t"
"v_pk_fma_f32 v[236:237], v[146:147], s[62:63], v[238:239]\n\t"
"v_pk_fma_f32 v[146:147], v[238:239], s[62:63], v[146:147]\n\t"
"v_mfma_f32_16x16x32_bf16 a[164:167], a[236:239], a[196:199], a[164:167]\n\t"
"s_waitcnt vmcnt(18) lgkmcnt(4)\n\t"
"ds_read_b128 a[196:199], v244 offset:3584\n\t"
"v_accvgpr_write_b32 a2, v230\n\t"
"v_mfma_f32_16x16x32_bf16 a[164:167], a[240:243], a[200:203], a[164:167]\n\t"
"ds_read_b128 a[200:203], v244 offset:4096\n\t"
"v_accvgpr_write_b32 a6, v234\n\t"
"v_mfma_f32_16x16x32_bf16 a[164:167], a[244:247], a[204:207], a[164:167]\n\t"
"ds_read_b128 a[204:207], v244 offset:4608\n\t"
"v_accvgpr_read_b32 v224, a72\n\t"
"v_accvgpr_read_b32 v226, a76\n\t"
"v_accvgpr_read_b32 v225, a73\n\t"
"v_accvgpr_read_b32 v227, a77\n\t"
"v_cvt_pk_bf16_f32 v236, v236, v237\n\t"
"v_cvt_pk_bf16_f32 v98, v98, v99\n\t"
"v_pk_fma_f32 v[224:225], v[226:227], s[56:57], v[224:225]\n\t"
"v_pk_fma_f32 v[226:227], v[224:225], s[58:59], v[226:227] neg_lo:[0,0,1] neg_hi:[0,0,1]\n\t"
"v_pk_fma_f32 v[228:229], v[48:49], s[60:61], v[224:225]\n\t"
"v_pk_fma_f32 v[48:49], v[224:225], s[60:61], v[48:49]\n\t"
"v_pk_fma_f32 v[224:225], v[148:149], s[62:63], v[226:227]\n\t"
"v_pk_fma_f32 v[148:149], v[226:227], s[62:63], v[148:149]\n\t"
"Lst14_%=:\n\t"
"v_mfma_f32_16x16x32_bf16 v[0:3], a[216:219], a[168:171], v[0:3]\n\t"
"s_waitcnt lgkmcnt(6)\n\t"
"v_accvgpr_write_b32 a3, v236\n\t"
"v_accvgpr_write_b32 a7, v98\n\t"
"v_mfma_f32_16x16x32_bf16 v[0:3], a[252:255], a[176:179], v[0:3]\n\t"
"v_accvgpr_read_b32 v230, a74\n\t"
"v_accvgpr_read_b32 v232, a78\n\t"
"v_mfma_f32_16x16x32_bf16 v[0:3], a[248:251], a[172:175], v[0:3]\n\t"
"s_mov_b32 s20, 1081344\n\t"
"v_accvgpr_read_b32 v231, a75\n\t"
"v_accvgpr_read_b32 v233, a79\n\t"
"v_cvt_pk_bf16_f32 v224, v224, v225\n\t"
"v_cvt_pk_bf16_f32 v228, v228, v229\n\t"
"v_accvgpr_write_b32 a10, v224\n\t"
"v_pk_fma_f32 v[230:231], v[232:233], s[56:57], v[230:231]\n\t"
"v_pk_fma_f32 v[232:233], v[230:231], s[58:59], v[232:233] neg_lo:[0,0,1] neg_hi:[0,0,1]\n\t"
"v_pk_fma_f32 v[234:235], v[50:51], s[60:61], v[230:231]\n\t"
"v_pk_fma_f32 v[50:51], v[230:231], s[60:61], v[50:51]\n\t"
"v_pk_fma_f32 v[230:231], v[150:151], s[62:63], v[232:233]\n\t"
"v_pk_fma_f32 v[150:151], v[232:233], s[62:63], v[150:151]\n\t"
"v_mfma_f32_16x16x32_bf16 v[4:7], a[216:219], a[172:175], v[4:7]\n\t"
"s_waitcnt lgkmcnt(5)\n\t"
"v_accvgpr_write_b32 a14, v228\n\t"
"v_accvgpr_read_b32 v236, a80\n\t"
"v_mfma_f32_16x16x32_bf16 v[4:7], a[248:251], a[176:179], v[4:7]\n\t"
"v_accvgpr_read_b32 v238, a84\n\t"
"v_accvgpr_read_b32 v237, a81\n\t"
"v_mfma_f32_16x16x32_bf16 v[4:7], a[252:255], a[180:183], v[4:7]\n\t"
"v_accvgpr_read_b32 v239, a85\n\t"
"v_cvt_pk_bf16_f32 v230, v230, v231\n\t"
"v_cvt_pk_bf16_f32 v234, v234, v235\n\t"
"v_accvgpr_write_b32 a11, v230\n\t"
"v_accvgpr_write_b32 a15, v234\n\t"
"v_pk_fma_f32 v[236:237], v[238:239], s[56:57], v[236:237]\n\t"
"v_pk_fma_f32 v[238:239], v[236:237], s[58:59], v[238:239] neg_lo:[0,0,1] neg_hi:[0,0,1]\n\t"
"v_pk_fma_f32 v[98:99], v[52:53], s[60:61], v[236:237]\n\t"
"v_pk_fma_f32 v[52:53], v[236:237], s[60:61], v[52:53]\n\t"
"v_pk_fma_f32 v[236:237], v[152:153], s[62:63], v[238:239]\n\t"
"v_pk_fma_f32 v[152:153], v[238:239], s[62:63], v[152:153]\n\t"
"v_mfma_f32_16x16x32_bf16 v[8:11], a[252:255], a[184:187], v[8:11]\n\t"
"s_waitcnt lgkmcnt(4)\n\t"
"v_accvgpr_read_b32 v224, a82\n\t"
"v_accvgpr_read_b32 v226, a86\n\t"
"v_mfma_f32_16x16x32_bf16 v[8:11], a[248:251], a[180:183], v[8:11]\n\t"
"v_accvgpr_read_b32 v225, a83\n\t"
"v_accvgpr_read_b32 v227, a87\n\t"
"v_mfma_f32_16x16x32_bf16 v[8:11], a[216:219], a[176:179], v[8:11]\n\t"
"buffer_load_dwordx4 a[56:59], off, s[80:83], s20 offset:0 sc1\n\t"
"v_cvt_pk_bf16_f32 v236, v236, v237\n\t"
"v_cvt_pk_bf16_f32 v98, v98, v99\n\t"
"v_pk_fma_f32 v[224:225], v[226:227], s[56:57], v[224:225]\n\t"
"v_pk_fma_f32 v[226:227], v[224:225], s[58:59], v[226:227] neg_lo:[0,0,1] neg_hi:[0,0,1]\n\t"
"v_pk_fma_f32 v[228:229], v[54:55], s[60:61], v[224:225]\n\t"
"v_pk_fma_f32 v[54:55], v[224:225], s[60:61], v[54:55]\n\t"
"v_pk_fma_f32 v[224:225], v[154:155], s[62:63], v[226:227]\n\t"
"v_pk_fma_f32 v[154:155], v[226:227], s[62:63], v[154:155]\n\t"
"v_mfma_f32_16x16x32_bf16 v[12:15], a[216:219], a[180:183], v[12:15]\n\t"
"s_waitcnt lgkmcnt(3)\n\t"
"v_accvgpr_write_b32 a18, v236\n\t"
"v_accvgpr_write_b32 a22, v98\n\t"
"v_mfma_f32_16x16x32_bf16 v[12:15], a[248:251], a[184:187], v[12:15]\n\t"
"v_accvgpr_read_b32 v230, a88\n\t"
"v_accvgpr_read_b32 v232, a92\n\t"
"v_mfma_f32_16x16x32_bf16 v[12:15], a[252:255], a[188:191], v[12:15]\n\t"
"buffer_load_dwordx4 a[64:67], off, s[80:83], s20 offset:1024 sc1\n\t"
"v_accvgpr_read_b32 v231, a89\n\t"
"v_accvgpr_read_b32 v233, a93\n\t"
"v_accvgpr_read_b32 v236, a90\n\t"
"v_accvgpr_read_b32 v238, a94\n\t"
"v_accvgpr_read_b32 v237, a91\n\t"
"v_accvgpr_read_b32 v239, a95\n\t"
"v_cvt_pk_bf16_f32 v224, v224, v225\n\t"
"v_cvt_pk_bf16_f32 v228, v228, v229\n\t"
"v_pk_fma_f32 v[230:231], v[232:233], s[56:57], v[230:231]\n\t"
"v_pk_fma_f32 v[232:233], v[230:231], s[58:59], v[232:233] neg_lo:[0,0,1] neg_hi:[0,0,1]\n\t"
"v_pk_fma_f32 v[234:235], v[56:57], s[60:61], v[230:231]\n\t"
"v_pk_fma_f32 v[56:57], v[230:231], s[60:61], v[56:57]\n\t"
"v_pk_fma_f32 v[230:231], v[156:157], s[62:63], v[232:233]\n\t"
"v_pk_fma_f32 v[156:157], v[232:233], s[62:63], v[156:157]\n\t"
"v_pk_fma_f32 v[236:237], v[238:239], s[56:57], v[236:237]\n\t"
"v_pk_fma_f32 v[238:239], v[236:237], s[58:59], v[238:239] neg_lo:[0,0,1] neg_hi:[0,0,1]\n\t"
"v_pk_fma_f32 v[98:99], v[58:59], s[60:61], v[236:237]\n\t"
"v_pk_fma_f32 v[58:59], v[236:237], s[60:61], v[58:59]\n\t"
"v_pk_fma_f32 v[236:237], v[158:159], s[62:63], v[238:239]\n\t"
"v_pk_fma_f32 v[158:159], v[238:239], s[62:63], v[158:159]\n\t"
"v_mfma_f32_16x16x32_bf16 v[16:19], a[252:255], a[192:195], v[16:19]\n\t"
"s_waitcnt lgkmcnt(2)\n\t"
"v_accvgpr_write_b32 a19, v224\n\t"
"v_accvgpr_write_b32 a23, v228\n\t"
"v_mfma_f32_16x16x32_bf16 v[16:19], a[248:251], a[188:191], v[16:19]\n\t"
"v_accvgpr_read_b32 v224, a96\n\t"
"v_accvgpr_read_b32 v226, a100\n\t"
"v_mfma_f32_16x16x32_bf16 v[16:19], a[216:219], a[184:187], v[16:19]\n\t"
"buffer_load_dwordx4 a[72:75], off, s[80:83], s20 offset:2048 sc1\n\t"
"v_accvgpr_read_b32 v225, a97\n\t"
"v_accvgpr_read_b32 v227, a101\n\t"
"v_cvt_pk_bf16_f32 v230, v230, v231\n\t"
"v_cvt_pk_bf16_f32 v234, v234, v235\n\t"
"v_pk_fma_f32 v[224:225], v[226:227], s[56:57], v[224:225]\n\t"
"v_pk_fma_f32 v[226:227], v[224:225], s[58:59], v[226:227] neg_lo:[0,0,1] neg_hi:[0,0,1]\n\t"
"v_pk_fma_f32 v[228:229], v[60:61], s[60:61], v[224:225]\n\t"
"v_pk_fma_f32 v[60:61], v[224:225], s[60:61], v[60:61]\n\t"
"v_pk_fma_f32 v[224:225], v[160:161], s[62:63], v[226:227]\n\t"
"v_pk_fma_f32 v[160:161], v[226:227], s[62:63], v[160:161]\n\t"
"v_mfma_f32_16x16x32_bf16 v[20:23], a[216:219], a[188:191], v[20:23]\n\t"
"s_waitcnt lgkmcnt(1)\n\t"
"s_mov_b32 s20, 1093632\n\t"
"v_accvgpr_write_b32 a26, v230\n\t"
"v_mfma_f32_16x16x32_bf16 v[20:23], a[248:251], a[192:195], v[20:23]\n\t"
"v_cvt_pk_bf16_f32 v236, v236, v237\n\t"
"v_accvgpr_write_b32 a30, v234\n\t"
"v_mfma_f32_16x16x32_bf16 v[20:23], a[252:255], a[196:199], v[20:23]\n\t"
"buffer_load_dwordx4 a[80:83], off, s[80:83], s20 offset:0 sc1\n\t"
"v_accvgpr_read_b32 v230, a98\n\t"
"v_accvgpr_read_b32 v232, a102\n\t"
"v_accvgpr_read_b32 v231, a99\n\t"
"v_accvgpr_read_b32 v233, a103\n\t"
"v_cvt_pk_bf16_f32 v98, v98, v99\n\t"
"v_accvgpr_write_b32 a27, v236\n\t"
"v_accvgpr_write_b32 a31, v98\n\t"
"v_cvt_pk_bf16_f32 v224, v224, v225\n\t"
"v_cvt_pk_bf16_f32 v228, v228, v229\n\t"
"v_pk_fma_f32 v[230:231], v[232:233], s[56:57], v[230:231]\n\t"
"v_pk_fma_f32 v[232:233], v[230:231], s[58:59], v[232:233] neg_lo:[0,0,1] neg_hi:[0,0,1]\n\t"
"v_pk_fma_f32 v[234:235], v[62:63], s[60:61], v[230:231]\n\t"
"v_pk_fma_f32 v[62:63], v[230:231], s[60:61], v[62:63]\n\t"
"v_pk_fma_f32 v[230:231], v[162:163], s[62:63], v[232:233]\n\t"
"v_pk_fma_f32 v[162:163], v[232:233], s[62:63], v[162:163]\n\t"
"v_mfma_f32_16x16x32_bf16 v[24:27], a[252:255], a[200:203], v[24:27]\n\t"
"s_waitcnt lgkmcnt(0)\n\t"
"v_accvgpr_write_b32 a34, v224\n\t"
"v_accvgpr_write_b32 a38, v228\n\t"
"v_mfma_f32_16x16x32_bf16 v[24:27], a[248:251], a[196:199], v[24:27]\n\t"
"v_accvgpr_read_b32 v236, a104\n\t"
"v_accvgpr_read_b32 v238, a108\n\t"
"v_mfma_f32_16x16x32_bf16 v[24:27], a[216:219], a[192:195], v[24:27]\n\t"
"buffer_load_dwordx4 a[88:91], off, s[80:83], s20 offset:1024 sc1\n\t"
"v_accvgpr_read_b32 v237, a105\n\t"
"v_accvgpr_read_b32 v239, a109\n\t"
"v_cvt_pk_bf16_f32 v230, v230, v231\n\t"
"v_cvt_pk_bf16_f32 v234, v234, v235\n\t"
"v_pk_fma_f32 v[236:237], v[238:239], s[56:57], v[236:237]\n\t"
"v_pk_fma_f32 v[238:239], v[236:237], s[58:59], v[238:239] neg_lo:[0,0,1] neg_hi:[0,0,1]\n\t"
"v_pk_fma_f32 v[98:99], v[64:65], s[60:61], v[236:237]\n\t"
"v_pk_fma_f32 v[64:65], v[236:237], s[60:61], v[64:65]\n\t"
"v_pk_fma_f32 v[236:237], v[164:165], s[62:63], v[238:239]\n\t"
"v_pk_fma_f32 v[164:165], v[238:239], s[62:63], v[164:165]\n\t"
"v_mfma_f32_16x16x32_bf16 v[28:31], a[216:219], a[196:199], v[28:31]\n\t"
"s_waitcnt vmcnt(15)\n\t"
"v_accvgpr_write_b32 a35, v230\n\t"
"v_accvgpr_write_b32 a39, v234\n\t"
"v_mfma_f32_16x16x32_bf16 v[28:31], a[248:251], a[200:203], v[28:31]\n\t"
"v_accvgpr_read_b32 v224, a106\n\t"
"v_accvgpr_read_b32 v226, a110\n\t"
"v_mfma_f32_16x16x32_bf16 v[28:31], a[252:255], a[204:207], v[28:31]\n\t"
"buffer_load_dwordx4 a[96:99], off, s[80:83], s20 offset:2048 sc1\n\t"
"v_accvgpr_read_b32 v225, a107\n\t"
"v_accvgpr_read_b32 v227, a111\n\t"
"v_cvt_pk_bf16_f32 v236, v236, v237\n\t"
"v_cvt_pk_bf16_f32 v98, v98, v99\n\t"
"v_pk_fma_f32 v[224:225], v[226:227], s[56:57], v[224:225]\n\t"
"v_pk_fma_f32 v[226:227], v[224:225], s[58:59], v[226:227] neg_lo:[0,0,1] neg_hi:[0,0,1]\n\t"
"v_pk_fma_f32 v[228:229], v[66:67], s[60:61], v[224:225]\n\t"
"v_pk_fma_f32 v[66:67], v[224:225], s[60:61], v[66:67]\n\t"
"v_pk_fma_f32 v[224:225], v[166:167], s[62:63], v[226:227]\n\t"
"v_pk_fma_f32 v[166:167], v[226:227], s[62:63], v[166:167]\n\t"
"v_mfma_f32_16x16x32_bf16 v[32:35], v[240:243], a[168:171], v[32:35]\n\t"
"s_mov_b32 s20, 589824\n\t"
"v_accvgpr_write_b32 a104, v236\n\t"
"v_accvgpr_write_b32 a106, v98\n\t"
"v_mfma_f32_16x16x32_bf16 v[32:35], a[220:223], a[176:179], v[32:35]\n\t"
"v_accvgpr_read_b32 v230, a112\n\t"
"v_accvgpr_read_b32 v232, a116\n\t"
"v_mfma_f32_16x16x32_bf16 v[32:35], a[208:211], a[172:175], v[32:35]\n\t"
"buffer_load_dwordx4 a[108:111], off, s[80:83], s20 offset:0 sc1\n\t"
"v_accvgpr_read_b32 v231, a113\n\t"
"v_accvgpr_read_b32 v233, a117\n\t"
"v_cvt_pk_bf16_f32 v224, v224, v225\n\t"
"v_cvt_pk_bf16_f32 v228, v228, v229\n\t"
"v_pk_fma_f32 v[230:231], v[232:233], s[56:57], v[230:231]\n\t"
"v_pk_fma_f32 v[232:233], v[230:231], s[58:59], v[232:233] neg_lo:[0,0,1] neg_hi:[0,0,1]\n\t"
"v_pk_fma_f32 v[234:235], v[68:69], s[60:61], v[230:231]\n\t"
"v_pk_fma_f32 v[68:69], v[230:231], s[60:61], v[68:69]\n\t"
"v_pk_fma_f32 v[230:231], v[168:169], s[62:63], v[232:233]\n\t"
"v_pk_fma_f32 v[168:169], v[232:233], s[62:63], v[168:169]\n\t"
"v_mfma_f32_16x16x32_bf16 v[36:39], v[240:243], a[172:175], v[36:39]\n\t"
"v_accvgpr_write_b32 a105, v224\n\t"
"v_accvgpr_write_b32 a107, v228\n\t"
"v_mfma_f32_16x16x32_bf16 v[36:39], a[208:211], a[176:179], v[36:39]\n\t"
"v_accvgpr_read_b32 v236, a114\n\t"
"v_accvgpr_read_b32 v238, a118\n\t"
"v_mfma_f32_16x16x32_bf16 v[36:39], a[220:223], a[180:183], v[36:39]\n\t"
"buffer_load_dwordx4 a[60:63], off, s[80:83], s20 offset:1024 sc1\n\t"
"v_accvgpr_read_b32 v237, a115\n\t"
"v_accvgpr_read_b32 v239, a119\n\t"
"v_cvt_pk_bf16_f32 v230, v230, v231\n\t"
"v_cvt_pk_bf16_f32 v234, v234, v235\n\t"
"v_pk_fma_f32 v[236:237], v[238:239], s[56:57], v[236:237]\n\t"
"v_pk_fma_f32 v[238:239], v[236:237], s[58:59], v[238:239] neg_lo:[0,0,1] neg_hi:[0,0,1]\n\t"
"v_pk_fma_f32 v[98:99], v[70:71], s[60:61], v[236:237]\n\t"
"v_pk_fma_f32 v[70:71], v[236:237], s[60:61], v[70:71]\n\t"
"v_pk_fma_f32 v[236:237], v[170:171], s[62:63], v[238:239]\n\t"
"v_pk_fma_f32 v[170:171], v[238:239], s[62:63], v[170:171]\n\t"
"v_mfma_f32_16x16x32_bf16 v[40:43], a[220:223], a[184:187], v[40:43]\n\t"
"v_accvgpr_write_b32 a112, v230\n\t"
"v_accvgpr_write_b32 a114, v234\n\t"
"v_mfma_f32_16x16x32_bf16 v[40:43], a[208:211], a[180:183], v[40:43]\n\t"
"v_accvgpr_read_b32 v224, a120\n\t"
"v_accvgpr_read_b32 v226, a124\n\t"
"v_mfma_f32_16x16x32_bf16 v[40:43], v[240:243], a[176:179], v[40:43]\n\t"
"global_load_dwordx4 v[248:251], v252, s[48:49] offset:0\n\t"
"v_accvgpr_read_b32 v225, a121\n\t"
"v_accvgpr_read_b32 v227, a125\n\t"
"v_cvt_pk_bf16_f32 v236, v236, v237\n\t"
"v_cvt_pk_bf16_f32 v98, v98, v99\n\t"
"v_pk_fma_f32 v[224:225], v[226:227], s[56:57], v[224:225]\n\t"
"v_pk_fma_f32 v[226:227], v[224:225], s[58:59], v[226:227] neg_lo:[0,0,1] neg_hi:[0,0,1]\n\t"
"v_pk_fma_f32 v[228:229], v[72:73], s[60:61], v[224:225]\n\t"
"v_pk_fma_f32 v[72:73], v[224:225], s[60:61], v[72:73]\n\t"
"v_pk_fma_f32 v[224:225], v[172:173], s[62:63], v[226:227]\n\t"
"v_pk_fma_f32 v[172:173], v[226:227], s[62:63], v[172:173]\n\t"
"v_mfma_f32_16x16x32_bf16 v[44:47], v[240:243], a[180:183], v[44:47]\n\t"
"v_accvgpr_write_b32 a113, v236\n\t"
"v_accvgpr_write_b32 a115, v98\n\t"
"v_mfma_f32_16x16x32_bf16 v[44:47], a[208:211], a[184:187], v[44:47]\n\t"
"v_accvgpr_read_b32 v230, a122\n\t"
"v_accvgpr_read_b32 v232, a126\n\t"
"v_mfma_f32_16x16x32_bf16 v[44:47], a[220:223], a[188:191], v[44:47]\n\t"
"buffer_load_dwordx4 a[116:119], off, s[80:83], s20 offset:2048 sc1\n\t"
"v_accvgpr_read_b32 v231, a123\n\t"
"v_accvgpr_read_b32 v233, a127\n\t"
"v_accvgpr_read_b32 v236, a128\n\t"
"v_accvgpr_read_b32 v238, a132\n\t"
"v_accvgpr_read_b32 v237, a129\n\t"
"v_accvgpr_read_b32 v239, a133\n\t"
"v_cvt_pk_bf16_f32 v224, v224, v225\n\t"
"v_cvt_pk_bf16_f32 v228, v228, v229\n\t"
"s_mov_b32 s20, 602112\n\t"
"v_pk_fma_f32 v[230:231], v[232:233], s[56:57], v[230:231]\n\t"
"v_pk_fma_f32 v[232:233], v[230:231], s[58:59], v[232:233] neg_lo:[0,0,1] neg_hi:[0,0,1]\n\t"
"v_pk_fma_f32 v[234:235], v[74:75], s[60:61], v[230:231]\n\t"
"v_pk_fma_f32 v[74:75], v[230:231], s[60:61], v[74:75]\n\t"
"v_pk_fma_f32 v[230:231], v[174:175], s[62:63], v[232:233]\n\t"
"v_pk_fma_f32 v[174:175], v[232:233], s[62:63], v[174:175]\n\t"
"v_pk_fma_f32 v[236:237], v[238:239], s[56:57], v[236:237]\n\t"
"v_pk_fma_f32 v[238:239], v[236:237], s[58:59], v[238:239] neg_lo:[0,0,1] neg_hi:[0,0,1]\n\t"
"v_pk_fma_f32 v[98:99], v[76:77], s[60:61], v[236:237]\n\t"
"v_pk_fma_f32 v[76:77], v[236:237], s[60:61], v[76:77]\n\t"
"v_pk_fma_f32 v[236:237], v[176:177], s[62:63], v[238:239]\n\t"
"v_pk_fma_f32 v[176:177], v[238:239], s[62:63], v[176:177]\n\t"
"v_mfma_f32_16x16x32_bf16 v[48:51], a[220:223], a[192:195], v[48:51]\n\t"
"v_accvgpr_write_b32 a120, v224\n\t"
"v_accvgpr_write_b32 a122, v228\n\t"
"v_mfma_f32_16x16x32_bf16 v[48:51], a[208:211], a[188:191], v[48:51]\n\t"
"v_accvgpr_read_b32 v224, a130\n\t"
"v_accvgpr_read_b32 v226, a134\n\t"
"v_mfma_f32_16x16x32_bf16 v[48:51], v[240:243], a[184:187], v[48:51]\n\t"
"buffer_load_dwordx4 a[100:103], off, s[80:83], s20 offset:0 sc1\n\t"
"v_accvgpr_read_b32 v225, a131\n\t"
"v_accvgpr_read_b32 v227, a135\n\t"
"v_cvt_pk_bf16_f32 v230, v230, v231\n\t"
"v_cvt_pk_bf16_f32 v234, v234, v235\n\t"
"v_cvt_pk_bf16_f32 v236, v236, v237\n\t"
"v_cvt_pk_bf16_f32 v98, v98, v99\n\t"
"v_pk_fma_f32 v[224:225], v[226:227], s[56:57], v[224:225]\n\t"
"v_pk_fma_f32 v[226:227], v[224:225], s[58:59], v[226:227] neg_lo:[0,0,1] neg_hi:[0,0,1]\n\t"
"v_pk_fma_f32 v[228:229], v[78:79], s[60:61], v[224:225]\n\t"
"v_pk_fma_f32 v[78:79], v[224:225], s[60:61], v[78:79]\n\t"
"v_pk_fma_f32 v[224:225], v[178:179], s[62:63], v[226:227]\n\t"
"v_pk_fma_f32 v[178:179], v[226:227], s[62:63], v[178:179]\n\t"
"v_mfma_f32_16x16x32_bf16 v[52:55], v[240:243], a[188:191], v[52:55]\n\t"
"v_accvgpr_write_b32 a121, v230\n\t"
"v_accvgpr_write_b32 a123, v234\n\t"
"v_mfma_f32_16x16x32_bf16 v[52:55], a[208:211], a[192:195], v[52:55]\n\t"
"v_accvgpr_write_b32 a128, v236\n\t"
"v_accvgpr_write_b32 a130, v98\n\t"
"v_mfma_f32_16x16x32_bf16 v[52:55], a[220:223], a[196:199], v[52:55]\n\t"
"buffer_load_dwordx4 a[124:127], off, s[80:83], s20 offset:1024 sc1\n\t"
"v_accvgpr_read_b32 v230, a136\n\t"
"v_accvgpr_read_b32 v232, a140\n\t"
"v_accvgpr_read_b32 v231, a137\n\t"
"v_accvgpr_read_b32 v233, a141\n\t"
"v_cvt_pk_bf16_f32 v224, v224, v225\n\t"
"v_cvt_pk_bf16_f32 v228, v228, v229\n\t"
"v_pk_fma_f32 v[230:231], v[232:233], s[56:57], v[230:231]\n\t"
"v_pk_fma_f32 v[232:233], v[230:231], s[58:59], v[232:233] neg_lo:[0,0,1] neg_hi:[0,0,1]\n\t"
"v_pk_fma_f32 v[234:235], v[80:81], s[60:61], v[230:231]\n\t"
"v_pk_fma_f32 v[80:81], v[230:231], s[60:61], v[80:81]\n\t"
"v_pk_fma_f32 v[230:231], v[180:181], s[62:63], v[232:233]\n\t"
"v_pk_fma_f32 v[180:181], v[232:233], s[62:63], v[180:181]\n\t"
"v_mfma_f32_16x16x32_bf16 v[56:59], a[220:223], a[200:203], v[56:59]\n\t"
"v_accvgpr_write_b32 a129, v224\n\t"
"v_accvgpr_write_b32 a131, v228\n\t"
"v_mfma_f32_16x16x32_bf16 v[56:59], a[208:211], a[196:199], v[56:59]\n\t"
"v_accvgpr_read_b32 v236, a138\n\t"
"v_accvgpr_read_b32 v238, a142\n\t"
"v_mfma_f32_16x16x32_bf16 v[56:59], v[240:243], a[192:195], v[56:59]\n\t"
"buffer_load_dwordx4 a[132:135], off, s[80:83], s20 offset:2048 sc1\n\t"
"v_accvgpr_read_b32 v237, a139\n\t"
"v_accvgpr_read_b32 v239, a143\n\t"
"v_cvt_pk_bf16_f32 v230, v230, v231\n\t"
"v_cvt_pk_bf16_f32 v234, v234, v235\n\t"
"s_mov_b32 s20, 614400\n\t"
"v_pk_fma_f32 v[236:237], v[238:239], s[56:57], v[236:237]\n\t"
"v_pk_fma_f32 v[238:239], v[236:237], s[58:59], v[238:239] neg_lo:[0,0,1] neg_hi:[0,0,1]\n\t"
"v_pk_fma_f32 v[98:99], v[82:83], s[60:61], v[236:237]\n\t"
"v_pk_fma_f32 v[82:83], v[236:237], s[60:61], v[82:83]\n\t"
"v_pk_fma_f32 v[236:237], v[182:183], s[62:63], v[238:239]\n\t"
"v_pk_fma_f32 v[182:183], v[238:239], s[62:63], v[182:183]\n\t"
"v_mfma_f32_16x16x32_bf16 v[60:63], v[240:243], a[196:199], v[60:63]\n\t"
"s_waitcnt vmcnt(20)\n\t"
"v_accvgpr_write_b32 a136, v230\n\t"
"v_accvgpr_write_b32 a138, v234\n\t"
"v_mfma_f32_16x16x32_bf16 v[60:63], a[208:211], a[200:203], v[60:63]\n\t"
"v_accvgpr_read_b32 v224, a144\n\t"
"v_accvgpr_read_b32 v226, a148\n\t"
"v_mfma_f32_16x16x32_bf16 v[60:63], a[220:223], a[204:207], v[60:63]\n\t"
"buffer_load_dwordx4 a[44:47], off, s[80:83], s20 offset:0 sc1\n\t"
"v_accvgpr_read_b32 v225, a145\n\t"
"v_accvgpr_read_b32 v227, a149\n\t"
"v_cvt_pk_bf16_f32 v236, v236, v237\n\t"
"v_cvt_pk_bf16_f32 v98, v98, v99\n\t"
"v_pk_fma_f32 v[224:225], v[226:227], s[56:57], v[224:225]\n\t"
"v_pk_fma_f32 v[226:227], v[224:225], s[58:59], v[226:227] neg_lo:[0,0,1] neg_hi:[0,0,1]\n\t"
"v_pk_fma_f32 v[228:229], v[84:85], s[60:61], v[224:225]\n\t"
"v_pk_fma_f32 v[84:85], v[224:225], s[60:61], v[84:85]\n\t"
"v_pk_fma_f32 v[224:225], v[184:185], s[62:63], v[226:227]\n\t"
"v_pk_fma_f32 v[184:185], v[226:227], s[62:63], v[184:185]\n\t"
"v_mfma_f32_16x16x32_bf16 v[64:67], a[224:227], a[168:171], v[64:67]\n\t"
"ds_read_b128 a[168:171], v244 offset:9216\n\t"
"v_accvgpr_write_b32 a137, v236\n\t"
"v_mfma_f32_16x16x32_bf16 v[64:67], a[228:231], a[176:179], v[64:67]\n\t"
"v_accvgpr_write_b32 a139, v98\n\t"
"v_accvgpr_read_b32 v230, a146\n\t"
"v_mfma_f32_16x16x32_bf16 v[64:67], a[212:215], a[172:175], v[64:67]\n\t"
"buffer_load_dwordx4 v[240:243], off, s[80:83], s20 offset:1024 sc1\n\t"
"v_accvgpr_read_b32 v232, a150\n\t"
"v_accvgpr_read_b32 v231, a147\n\t"
"v_accvgpr_read_b32 v233, a151\n\t"
"v_cvt_pk_bf16_f32 v224, v224, v225\n\t"
"v_cvt_pk_bf16_f32 v228, v228, v229\n\t"
"v_pk_fma_f32 v[230:231], v[232:233], s[56:57], v[230:231]\n\t"
"v_pk_fma_f32 v[232:233], v[230:231], s[58:59], v[232:233] neg_lo:[0,0,1] neg_hi:[0,0,1]\n\t"
"v_pk_fma_f32 v[234:235], v[86:87], s[60:61], v[230:231]\n\t"
"v_pk_fma_f32 v[86:87], v[230:231], s[60:61], v[86:87]\n\t"
"v_pk_fma_f32 v[230:231], v[186:187], s[62:63], v[232:233]\n\t"
"v_pk_fma_f32 v[186:187], v[232:233], s[62:63], v[186:187]\n\t"
"v_mfma_f32_16x16x32_bf16 v[68:71], a[224:227], a[172:175], v[68:71]\n\t"
"Lpg11_%=:\n\t"
"s_cbranch_vccnz Lvr0_%=\n\t"
"ds_read_b128 a[172:175], v244 offset:9728\n\t"
"v_accvgpr_write_b32 a144, v224\n\t"
"v_mfma_f32_16x16x32_bf16 v[68:71], a[212:215], a[176:179], v[68:71]\n\t"
"v_accvgpr_write_b32 a146, v228\n\t"
"v_accvgpr_read_b32 v236, a152\n\t"
"v_mfma_f32_16x16x32_bf16 v[68:71], a[228:231], a[180:183], v[68:71]\n\t"
"buffer_load_dwordx4 a[140:143], off, s[80:83], s20 offset:2048 sc1\n\t"
"v_accvgpr_read_b32 v238, a156\n\t"
"v_accvgpr_read_b32 v237, a153\n\t"
"v_accvgpr_read_b32 v239, a157\n\t"
"v_cvt_pk_bf16_f32 v230, v230, v231\n\t"
"v_cvt_pk_bf16_f32 v234, v234, v235\n\t"
"s_mov_b32 s20, 774144\n\t"
"v_pk_fma_f32 v[236:237], v[238:239], s[56:57], v[236:237]\n\t"
"v_pk_fma_f32 v[238:239], v[236:237], s[58:59], v[238:239] neg_lo:[0,0,1] neg_hi:[0,0,1]\n\t"
"v_pk_fma_f32 v[98:99], v[88:89], s[60:61], v[236:237]\n\t"
"v_pk_fma_f32 v[88:89], v[236:237], s[60:61], v[88:89]\n\t"
"v_pk_fma_f32 v[236:237], v[188:189], s[62:63], v[238:239]\n\t"
"v_pk_fma_f32 v[188:189], v[238:239], s[62:63], v[188:189]\n\t"
"v_mfma_f32_16x16x32_bf16 v[72:75], a[228:231], a[184:187], v[72:75]\n\t"
"v_accvgpr_write_b32 a145, v230\n\t"
"v_accvgpr_write_b32 a147, v234\n\t"
"v_mfma_f32_16x16x32_bf16 v[72:75], a[212:215], a[180:183], v[72:75]\n\t"
"v_accvgpr_read_b32 v224, a154\n\t"
"v_accvgpr_read_b32 v226, a158\n\t"
"v_mfma_f32_16x16x32_bf16 v[72:75], a[224:227], a[176:179], v[72:75]\n\t"
"buffer_load_dwordx4 a[148:151], off, s[80:83], s20 offset:0 sc1\n\t"
"ds_read_b128 a[176:179], v244 offset:10240\n\t"
"v_accvgpr_read_b32 v225, a155\n\t"
"v_accvgpr_read_b32 v227, a159\n\t"
"v_cvt_pk_bf16_f32 v236, v236, v237\n\t"
"v_cvt_pk_bf16_f32 v98, v98, v99\n\t"
"v_pk_fma_f32 v[224:225], v[226:227], s[56:57], v[224:225]\n\t"
"v_pk_fma_f32 v[226:227], v[224:225], s[58:59], v[226:227] neg_lo:[0,0,1] neg_hi:[0,0,1]\n\t"
"v_pk_fma_f32 v[228:229], v[90:91], s[60:61], v[224:225]\n\t"
"v_pk_fma_f32 v[90:91], v[224:225], s[60:61], v[90:91]\n\t"
"v_pk_fma_f32 v[224:225], v[190:191], s[62:63], v[226:227]\n\t"
"v_pk_fma_f32 v[190:191], v[226:227], s[62:63], v[190:191]\n\t"
"v_mfma_f32_16x16x32_bf16 v[76:79], a[224:227], a[180:183], v[76:79]\n\t"
"ds_read_b128 a[180:183], v244 offset:10752\n\t"
"v_accvgpr_write_b32 a152, v236\n\t"
"v_mfma_f32_16x16x32_bf16 v[76:79], a[212:215], a[184:187], v[76:79]\n\t"
"v_accvgpr_write_b32 a154, v98\n\t"
"v_accvgpr_read_b32 v230, a160\n\t"
"v_mfma_f32_16x16x32_bf16 v[76:79], a[228:231], a[188:191], v[76:79]\n\t"
"buffer_load_dwordx4 a[52:55], off, s[80:83], s20 offset:1024 sc1\n\t"
"v_accvgpr_read_b32 v232, a164\n\t"
"v_accvgpr_read_b32 v231, a161\n\t"
"v_accvgpr_read_b32 v233, a165\n\t"
"v_cvt_pk_bf16_f32 v224, v224, v225\n\t"
"v_cvt_pk_bf16_f32 v228, v228, v229\n\t"
"v_pk_fma_f32 v[230:231], v[232:233], s[56:57], v[230:231]\n\t"
"v_pk_fma_f32 v[232:233], v[230:231], s[58:59], v[232:233] neg_lo:[0,0,1] neg_hi:[0,0,1]\n\t"
"v_pk_fma_f32 v[234:235], v[92:93], s[60:61], v[230:231]\n\t"
"v_pk_fma_f32 v[92:93], v[230:231], s[60:61], v[92:93]\n\t"
"v_pk_fma_f32 v[230:231], v[192:193], s[62:63], v[232:233]\n\t"
"v_pk_fma_f32 v[192:193], v[232:233], s[62:63], v[192:193]\n\t"
"v_mfma_f32_16x16x32_bf16 v[80:83], a[228:231], a[192:195], v[80:83]\n\t"
"v_accvgpr_write_b32 a153, v224\n\t"
"v_accvgpr_write_b32 a155, v228\n\t"
"v_mfma_f32_16x16x32_bf16 v[80:83], a[212:215], a[188:191], v[80:83]\n\t"
"v_accvgpr_read_b32 v236, a162\n\t"
"v_accvgpr_read_b32 v238, a166\n\t"
"v_mfma_f32_16x16x32_bf16 v[80:83], a[224:227], a[184:187], v[80:83]\n\t"
"buffer_load_dwordx4 a[156:159], off, s[80:83], s20 offset:2048 sc1\n\t"
"ds_read_b128 a[184:187], v244 offset:11264\n\t"
"v_accvgpr_read_b32 v237, a163\n\t"
"v_accvgpr_read_b32 v239, a167\n\t"
"v_cvt_pk_bf16_f32 v230, v230, v231\n\t"
"v_cvt_pk_bf16_f32 v234, v234, v235\n\t"
"s_mov_b32 s20, 786432\n\t"
"v_pk_fma_f32 v[236:237], v[238:239], s[56:57], v[236:237]\n\t"
"v_pk_fma_f32 v[238:239], v[236:237], s[58:59], v[238:239] neg_lo:[0,0,1] neg_hi:[0,0,1]\n\t"
"v_pk_fma_f32 v[98:99], v[94:95], s[60:61], v[236:237]\n\t"
"v_pk_fma_f32 v[94:95], v[236:237], s[60:61], v[94:95]\n\t"
"v_pk_fma_f32 v[236:237], v[194:195], s[62:63], v[238:239]\n\t"
"v_pk_fma_f32 v[194:195], v[238:239], s[62:63], v[194:195]\n\t"
"v_mfma_f32_16x16x32_bf16 v[84:87], a[224:227], a[188:191], v[84:87]\n\t"
"ds_read_b128 a[188:191], v244 offset:11776\n\t"
"v_accvgpr_write_b32 a160, v230\n\t"
"v_mfma_f32_16x16x32_bf16 v[84:87], a[212:215], a[192:195], v[84:87]\n\t"
"v_accvgpr_write_b32 a162, v234\n\t"
"v_cvt_pk_bf16_f32 v236, v236, v237\n\t"
"v_mfma_f32_16x16x32_bf16 v[84:87], a[228:231], a[196:199], v[84:87]\n\t"
"buffer_load_dwordx4 a[68:71], off, s[80:83], s20 offset:0 sc1\n\t"
"v_cvt_pk_bf16_f32 v98, v98, v99\n\t"
"v_mfma_f32_16x16x32_bf16 v[88:91], a[228:231], a[200:203], v[88:91]\n\t"
"v_accvgpr_write_b32 a161, v236\n\t"
"v_accvgpr_write_b32 a163, v98\n\t"
"v_mfma_f32_16x16x32_bf16 v[88:91], a[212:215], a[196:199], v[88:91]\n\t"
"v_mfma_f32_16x16x32_bf16 v[88:91], a[224:227], a[192:195], v[88:91]\n\t"
"buffer_load_dwordx4 a[164:167], off, s[80:83], s20 offset:1024 sc1\n\t"
"v_mfma_f32_16x16x32_bf16 v[92:95], a[224:227], a[196:199], v[92:95]\n\t"
"s_waitcnt vmcnt(21) lgkmcnt(3)\n\t"
"ds_read_b128 a[192:195], v244 offset:12288\n\t"
"v_mfma_f32_16x16x32_bf16 v[92:95], a[212:215], a[200:203], v[92:95]\n\t"
"buffer_load_dwordx4 v[236:239], off, s[80:83], s20 offset:2048 sc1\n\t"
"v_mfma_f32_16x16x32_bf16 v[92:95], a[228:231], a[204:207], v[92:95]\n\t"
"s_mov_b32 s20, 798720\n\t"
"ds_read_b128 a[196:199], v244 offset:12800\n\t"
"v_mfma_f32_16x16x32_bf16 v[100:103], a[232:235], a[168:171], v[100:103]\n\t"
"s_waitcnt lgkmcnt(4)\n\t"
"ds_read_b128 a[200:203], v244 offset:13312\n\t"
"v_mfma_f32_16x16x32_bf16 v[100:103], a[48:51], a[176:179], v[100:103]\n\t"
"ds_read_b128 a[204:207], v244 offset:13824\n\t"
"v_mfma_f32_16x16x32_bf16 v[100:103], a[40:43], a[172:175], v[100:103]\n\t"
"buffer_load_dwordx4 a[76:79], off, s[80:83], s20 offset:0 sc1\n\t"
"v_mfma_f32_16x16x32_bf16 v[104:107], a[232:235], a[172:175], v[104:107]\n\t"
"buffer_load_dwordx4 a[84:87], off, s[80:83], s20 offset:1024 sc1\n\t"
"v_mfma_f32_16x16x32_bf16 v[104:107], a[40:43], a[176:179], v[104:107]\n\t"
"s_waitcnt lgkmcnt(5)\n\t"
"v_mfma_f32_16x16x32_bf16 v[104:107], a[48:51], a[180:183], v[104:107]\n\t"
"s_waitcnt lgkmcnt(0)\n\t"
"s_barrier\n\t"
"v_mfma_f32_16x16x32_bf16 v[108:111], a[48:51], a[184:187], v[108:111]\n\t"
"v_mfma_f32_16x16x32_bf16 v[108:111], a[40:43], a[180:183], v[108:111]\n\t"
"buffer_load_dwordx4 a[92:95], off, s[80:83], s20 offset:2048 sc1\n\t"
"v_mfma_f32_16x16x32_bf16 v[108:111], a[232:235], a[176:179], v[108:111]\n\t"
"s_mov_b32 s20, 1179648\n\t"
"s_waitcnt vmcnt(16)\n\t"
"ds_write_b128 v249, v[196:199] offset:0\n\t"
"v_mfma_f32_16x16x32_bf16 v[112:115], a[232:235], a[180:183], v[112:115]\n\t"
"buffer_load_dwordx4 v[96:99], off, s[80:83], s20 offset:0 sc1\n\t"
"v_mfma_f32_16x16x32_bf16 v[112:115], a[40:43], a[184:187], v[112:115]\n\t"
"v_mfma_f32_16x16x32_bf16 v[112:115], a[48:51], a[188:191], v[112:115]\n\t"
"buffer_load_dwordx4 a[208:211], off, s[80:83], s20 offset:1024 sc1\n\t"
"v_mfma_f32_16x16x32_bf16 v[116:119], a[48:51], a[192:195], v[116:119]\n\t"
"v_mfma_f32_16x16x32_bf16 v[116:119], a[40:43], a[188:191], v[116:119]\n\t"
"v_mfma_f32_16x16x32_bf16 v[116:119], a[232:235], a[184:187], v[116:119]\n\t"
"buffer_load_dwordx4 a[212:215], off, s[80:83], s20 offset:2048 sc1\n\t"
"v_mfma_f32_16x16x32_bf16 v[120:123], a[232:235], a[188:191], v[120:123]\n\t"
"s_mov_b32 s20, 1191936\n\t"
"v_mfma_f32_16x16x32_bf16 v[120:123], a[40:43], a[192:195], v[120:123]\n\t"
"v_mfma_f32_16x16x32_bf16 v[120:123], a[48:51], a[196:199], v[120:123]\n\t"
"buffer_load_dwordx4 a[216:219], off, s[80:83], s20 offset:0 sc1\n\t"
"v_mfma_f32_16x16x32_bf16 v[124:127], a[48:51], a[200:203], v[124:127]\n\t"
"v_mfma_f32_16x16x32_bf16 v[124:127], a[40:43], a[196:199], v[124:127]\n\t"
"v_mfma_f32_16x16x32_bf16 v[124:127], a[232:235], a[192:195], v[124:127]\n\t"
"buffer_load_dwordx4 a[220:223], off, s[80:83], s20 offset:1024 sc1\n\t"
"v_mfma_f32_16x16x32_bf16 v[128:131], a[232:235], a[196:199], v[128:131]\n\t"
"v_mfma_f32_16x16x32_bf16 v[128:131], a[40:43], a[200:203], v[128:131]\n\t"
"v_mfma_f32_16x16x32_bf16 v[128:131], a[48:51], a[204:207], v[128:131]\n\t"
"buffer_load_dwordx4 a[224:227], off, s[80:83], s20 offset:2048 sc1\n\t"
"v_mfma_f32_16x16x32_bf16 v[132:135], a[56:59], a[168:171], v[132:135]\n\t"
"s_mov_b32 s20, 1204224\n\t"
"v_mfma_f32_16x16x32_bf16 v[132:135], a[72:75], a[176:179], v[132:135]\n\t"
"v_mfma_f32_16x16x32_bf16 v[132:135], a[64:67], a[172:175], v[132:135]\n\t"
"ds_write_b64 v250, a[104:105] offset:0\n\t"
"v_mfma_f32_16x16x32_bf16 v[136:139], a[56:59], a[172:175], v[136:139]\n\t"
"buffer_load_dwordx4 a[228:231], off, s[80:83], s20 offset:0 sc1\n\t"
"v_mfma_f32_16x16x32_bf16 v[136:139], a[64:67], a[176:179], v[136:139]\n\t"
"v_mfma_f32_16x16x32_bf16 v[136:139], a[72:75], a[180:183], v[136:139]\n\t"
"buffer_load_dwordx4 a[232:235], off, s[80:83], s20 offset:1024 sc1\n\t"
"v_mfma_f32_16x16x32_bf16 v[140:143], a[72:75], a[184:187], v[140:143]\n\t"
"v_mfma_f32_16x16x32_bf16 v[140:143], a[64:67], a[180:183], v[140:143]\n\t"
"v_mfma_f32_16x16x32_bf16 v[140:143], a[56:59], a[176:179], v[140:143]\n\t"
"buffer_load_dwordx4 a[236:239], off, s[80:83], s20 offset:2048 sc1\n\t"
"v_mfma_f32_16x16x32_bf16 v[144:147], a[56:59], a[180:183], v[144:147]\n\t"
"s_mov_b32 s20, 1363968\n\t"
"v_mfma_f32_16x16x32_bf16 v[144:147], a[64:67], a[184:187], v[144:147]\n\t"
"v_mfma_f32_16x16x32_bf16 v[144:147], a[72:75], a[188:191], v[144:147]\n\t"
"buffer_load_dwordx4 a[240:243], off, s[80:83], s20 offset:0 sc1\n\t"
"v_mfma_f32_16x16x32_bf16 v[148:151], a[72:75], a[192:195], v[148:151]\n\t"
"v_mfma_f32_16x16x32_bf16 v[148:151], a[64:67], a[188:191], v[148:151]\n\t"
"v_mfma_f32_16x16x32_bf16 v[148:151], a[56:59], a[184:187], v[148:151]\n\t"
"buffer_load_dwordx4 a[244:247], off, s[80:83], s20 offset:1024 sc1\n\t"
"v_mfma_f32_16x16x32_bf16 v[152:155], a[56:59], a[188:191], v[152:155]\n\t"
"v_mfma_f32_16x16x32_bf16 v[152:155], a[64:67], a[192:195], v[152:155]\n\t"
"v_mfma_f32_16x16x32_bf16 v[152:155], a[72:75], a[196:199], v[152:155]\n\t"
"ds_write_b128 v249, v[200:203] offset:6144\n\t"
"v_mfma_f32_16x16x32_bf16 v[156:159], a[72:75], a[200:203], v[156:159]\n\t"
"buffer_load_dwordx4 a[248:251], off, s[80:83], s20 offset:2048 sc1\n\t"
"v_mfma_f32_16x16x32_bf16 v[156:159], a[64:67], a[196:199], v[156:159]\n\t"
"s_mov_b32 s20, 1376256\n\t"
"v_mfma_f32_16x16x32_bf16 v[156:159], a[56:59], a[192:195], v[156:159]\n\t"
"buffer_load_dwordx4 a[252:255], off, s[80:83], s20 offset:0 sc1\n\t"
"v_mfma_f32_16x16x32_bf16 v[160:163], a[56:59], a[196:199], v[160:163]\n\t"
"v_mfma_f32_16x16x32_bf16 v[160:163], a[64:67], a[200:203], v[160:163]\n\t"
"v_mfma_f32_16x16x32_bf16 v[160:163], a[72:75], a[204:207], v[160:163]\n\t"
"buffer_load_dwordx4 a[72:75], off, s[80:83], s20 offset:1024 sc1\n\t"
"v_mfma_f32_16x16x32_bf16 v[164:167], a[80:83], a[168:171], v[164:167]\n\t"
"ds_read_b128 a[168:171], v244 offset:24576\n\t"
"v_mfma_f32_16x16x32_bf16 v[164:167], a[96:99], a[176:179], v[164:167]\n\t"
"v_mfma_f32_16x16x32_bf16 v[164:167], a[88:91], a[172:175], v[164:167]\n\t"
"ds_write_b64 v250, a[106:107] offset:6144\n\t"
"v_mfma_f32_16x16x32_bf16 v[168:171], a[80:83], a[172:175], v[168:171]\n\t"
"ds_read_b128 a[172:175], v244 offset:25088\n\t"
"v_mfma_f32_16x16x32_bf16 v[168:171], a[88:91], a[176:179], v[168:171]\n\t"
"v_mfma_f32_16x16x32_bf16 v[168:171], a[96:99], a[180:183], v[168:171]\n\t"
"v_mfma_f32_16x16x32_bf16 v[172:175], a[96:99], a[184:187], v[172:175]\n\t"
"v_mfma_f32_16x16x32_bf16 v[172:175], a[88:91], a[180:183], v[172:175]\n\t"
"v_mfma_f32_16x16x32_bf16 v[172:175], a[80:83], a[176:179], v[172:175]\n\t"
"ds_read_b128 a[176:179], v244 offset:25600\n\t"
"v_mfma_f32_16x16x32_bf16 v[176:179], a[80:83], a[180:183], v[176:179]\n\t"
"ds_read_b128 a[180:183], v244 offset:26112\n\t"
"v_mfma_f32_16x16x32_bf16 v[176:179], a[88:91], a[184:187], v[176:179]\n\t"
"v_mfma_f32_16x16x32_bf16 v[176:179], a[96:99], a[188:191], v[176:179]\n\t"
"s_waitcnt lgkmcnt(3)\n\t"
"s_barrier\n\t"
"v_mfma_f32_16x16x32_bf16 v[180:183], a[96:99], a[192:195], v[180:183]\n\t"
"v_mfma_f32_16x16x32_bf16 v[180:183], a[88:91], a[188:191], v[180:183]\n\t"
"buffer_load_dwordx4 v[196:199], off, s[80:83], s20 offset:2048 sc1\n\t"
"v_mfma_f32_16x16x32_bf16 v[180:183], a[80:83], a[184:187], v[180:183]\n\t"
"s_mov_b32 s20, 1388544\n\t"
"ds_read_b128 a[184:187], v244 offset:26624\n\t"
"v_mfma_f32_16x16x32_bf16 v[184:187], a[80:83], a[188:191], v[184:187]\n\t"
"ds_read_b128 a[188:191], v244 offset:27136\n\t"
"v_mfma_f32_16x16x32_bf16 v[184:187], a[88:91], a[192:195], v[184:187]\n\t"
"buffer_load_dwordx4 a[104:107], off, s[80:83], s20 offset:0 sc1\n\t"
"v_mfma_f32_16x16x32_bf16 v[184:187], a[96:99], a[196:199], v[184:187]\n\t"
"ds_write_b128 v249, v[204:207] offset:12288\n\t"
"v_mfma_f32_16x16x32_bf16 v[188:191], a[96:99], a[200:203], v[188:191]\n\t"
"v_mfma_f32_16x16x32_bf16 v[188:191], a[88:91], a[196:199], v[188:191]\n\t"
"buffer_load_dwordx4 v[200:203], off, s[80:83], s20 offset:1024 sc1\n\t"
"v_mfma_f32_16x16x32_bf16 v[188:191], a[80:83], a[192:195], v[188:191]\n\t"
"ds_read_b128 a[192:195], v244 offset:27648\n\t"
"v_mfma_f32_16x16x32_bf16 v[192:195], a[80:83], a[196:199], v[192:195]\n\t"
"s_waitcnt vmcnt(32) lgkmcnt(5)\n\t"
"ds_read_b128 a[196:199], v244 offset:28160\n\t"
"v_mfma_f32_16x16x32_bf16 v[192:195], a[88:91], a[200:203], v[192:195]\n\t"
"buffer_load_dwordx4 a[80:83], off, s[80:83], s20 offset:2048 sc1\n\t"
"v_mfma_f32_16x16x32_bf16 v[192:195], a[96:99], a[204:207], v[192:195]\n\t"
"s_mov_b32 s20, 1474560\n\t"
"ds_read_b128 a[200:203], v244 offset:28672\n\t"
"Lst15_%=:\n\t"
"v_mfma_f32_16x16x32_bf16 v[0:3], a[108:111], a[168:171], v[0:3]\n\t"
"s_waitcnt lgkmcnt(6)\n\t"
"ds_read_b128 a[204:207], v244 offset:29184\n\t"
"v_mfma_f32_16x16x32_bf16 v[0:3], a[116:119], a[176:179], v[0:3]\n\t"
"buffer_load_dwordx4 a[88:91], off, s[80:83], s20 offset:0 sc1\n\t"
"v_mfma_f32_16x16x32_bf16 v[0:3], a[60:63], a[172:175], v[0:3]\n\t"
"ds_write_b64 v250, a[112:113] offset:12288\n\t"
"v_mfma_f32_16x16x32_bf16 v[4:7], a[108:111], a[172:175], v[4:7]\n\t"
"s_waitcnt lgkmcnt(7)\n\t"
"v_mfma_f32_16x16x32_bf16 v[4:7], a[60:63], a[176:179], v[4:7]\n\t"
"buffer_load_dwordx4 a[96:99], off, s[80:83], s20 offset:1024 sc1\n\t"
"v_mfma_f32_16x16x32_bf16 v[4:7], a[116:119], a[180:183], v[4:7]\n\t"
"ds_write_b128 v249, v[208:211] offset:18432\n\t"
"v_mfma_f32_16x16x32_bf16 v[8:11], a[116:119], a[184:187], v[8:11]\n\t"
"s_waitcnt lgkmcnt(7)\n\t"
"v_mfma_f32_16x16x32_bf16 v[8:11], a[60:63], a[180:183], v[8:11]\n\t"
"v_mfma_f32_16x16x32_bf16 v[8:11], a[108:111], a[176:179], v[8:11]\n\t"
"ds_write_b64 v250, a[114:115] offset:18432\n\t"
"v_mfma_f32_16x16x32_bf16 v[12:15], a[108:111], a[180:183], v[12:15]\n\t"
"s_waitcnt lgkmcnt(6)\n\t"
"v_mfma_f32_16x16x32_bf16 v[12:15], a[60:63], a[184:187], v[12:15]\n\t"
"ds_read_b128 a[40:43], v252 offset:0\n\t"
"v_mfma_f32_16x16x32_bf16 v[12:15], a[116:119], a[188:191], v[12:15]\n\t"
"v_mfma_f32_16x16x32_bf16 v[16:19], a[116:119], a[192:195], v[16:19]\n\t"
"s_waitcnt lgkmcnt(6)\n\t"
"v_mfma_f32_16x16x32_bf16 v[16:19], a[60:63], a[188:191], v[16:19]\n\t"
"v_mfma_f32_16x16x32_bf16 v[16:19], a[108:111], a[184:187], v[16:19]\n\t"
"v_mfma_f32_16x16x32_bf16 v[20:23], a[108:111], a[188:191], v[20:23]\n\t"
"s_waitcnt lgkmcnt(5)\n\t"
"v_mfma_f32_16x16x32_bf16 v[20:23], a[60:63], a[192:195], v[20:23]\n\t"
"ds_read_b128 a[48:51], v252 offset:1024\n\t"
"v_mfma_f32_16x16x32_bf16 v[20:23], a[116:119], a[196:199], v[20:23]\n\t"
"v_mfma_f32_16x16x32_bf16 v[24:27], a[116:119], a[200:203], v[24:27]\n\t"
"s_waitcnt lgkmcnt(5)\n\t"
"ds_read_b128 a[56:59], v252 offset:2048\n\t"
"v_mfma_f32_16x16x32_bf16 v[24:27], a[60:63], a[196:199], v[24:27]\n\t"
"v_mfma_f32_16x16x32_bf16 v[24:27], a[108:111], a[192:195], v[24:27]\n\t"
"v_mfma_f32_16x16x32_bf16 v[28:31], a[108:111], a[196:199], v[28:31]\n\t"
"s_waitcnt vmcnt(32)\n\t"
"v_mfma_f32_16x16x32_bf16 v[28:31], a[60:63], a[200:203], v[28:31]\n\t"
"buffer_load_dwordx4 a[60:63], off, s[80:83], s20 offset:2048 sc1\n\t"
"v_mfma_f32_16x16x32_bf16 v[28:31], a[116:119], a[204:207], v[28:31]\n\t"
"s_add_u32 s29, s22, 384\n\t"
"v_mfma_f32_16x16x32_bf16 v[32:35], a[100:103], a[168:171], v[32:35]\n\t"
"v_mfma_f32_16x16x32_bf16 v[32:35], a[132:135], a[176:179], v[32:35]\n\t"
"s_waitcnt lgkmcnt(2)\n\t"
"v_mfma_f32_16x16x32_bf16 v[32:35], a[124:127], a[172:175], v[32:35]\n\t"
"v_mfma_f32_16x16x32_bf16 v[36:39], a[100:103], a[172:175], v[36:39]\n\t"
"v_mfma_f32_16x16x32_bf16 v[36:39], a[124:127], a[176:179], v[36:39]\n\t"
"v_mfma_f32_16x16x32_bf16 v[36:39], a[132:135], a[180:183], v[36:39]\n\t"
"buffer_store_dwordx4 a[40:43], v248, s[16:19], s29 offen offset:0 nt\n\t"
"v_mfma_f32_16x16x32_bf16 v[40:43], a[132:135], a[184:187], v[40:43]\n\t"
"v_mfma_f32_16x16x32_bf16 v[40:43], a[124:127], a[180:183], v[40:43]\n\t"
"s_mov_b32 s20, 1486848\n\t"
"v_mfma_f32_16x16x32_bf16 v[40:43], a[100:103], a[176:179], v[40:43]\n\t"
"v_mfma_f32_16x16x32_bf16 v[44:47], a[100:103], a[180:183], v[44:47]\n\t"
"v_mfma_f32_16x16x32_bf16 v[44:47], a[124:127], a[184:187], v[44:47]\n\t"
"v_mfma_f32_16x16x32_bf16 v[44:47], a[132:135], a[188:191], v[44:47]\n\t"
"s_waitcnt lgkmcnt(0)\n\t"
"s_barrier\n\t"
"v_mfma_f32_16x16x32_bf16 v[48:51], a[132:135], a[192:195], v[48:51]\n\t"
"v_mfma_f32_16x16x32_bf16 v[48:51], a[124:127], a[188:191], v[48:51]\n\t"
"buffer_load_dwordx4 v[204:207], off, s[80:83], s20 offset:0 sc1\n\t"
"v_mfma_f32_16x16x32_bf16 v[48:51], a[100:103], a[184:187], v[48:51]\n\t"
"ds_write_b128 v249, v[212:215] offset:0\n\t"
"v_mfma_f32_16x16x32_bf16 v[52:55], a[100:103], a[188:191], v[52:55]\n\t"
"v_mfma_f32_16x16x32_bf16 v[52:55], a[124:127], a[192:195], v[52:55]\n\t"
"buffer_load_dwordx4 a[112:115], off, s[80:83], s20 offset:1024 sc1\n\t"
"v_mfma_f32_16x16x32_bf16 v[52:55], a[132:135], a[196:199], v[52:55]\n\t"
"ds_write_b64 v250, a[120:121] offset:0\n\t"
"v_mfma_f32_16x16x32_bf16 v[56:59], a[132:135], a[200:203], v[56:59]\n\t"
"v_mfma_f32_16x16x32_bf16 v[56:59], a[124:127], a[196:199], v[56:59]\n\t"
"buffer_load_dwordx4 v[208:211], off, s[80:83], s20 offset:2048 sc1\n\t"
"v_mfma_f32_16x16x32_bf16 v[56:59], a[100:103], a[192:195], v[56:59]\n\t"
"buffer_store_dwordx4 a[48:51], v248, s[16:19], s29 offen offset:128 nt\n\t"
"ds_write_b64 v250, a[122:123] offset:6144\n\t"
"v_mfma_f32_16x16x32_bf16 v[60:63], a[100:103], a[196:199], v[60:63]\n\t"
"s_waitcnt vmcnt(35)\n\t"
"v_mfma_f32_16x16x32_bf16 v[60:63], a[124:127], a[200:203], v[60:63]\n\t"
"v_mfma_f32_16x16x32_bf16 v[60:63], a[132:135], a[204:207], v[60:63]\n\t"
"ds_write_b128 v249, v[216:219] offset:6144\n\t"
"v_mfma_f32_16x16x32_bf16 v[64:67], a[44:47], a[168:171], v[64:67]\n\t"
"v_mfma_f32_16x16x32_bf16 v[64:67], a[140:143], a[176:179], v[64:67]\n\t"
"ds_read_b128 a[168:171], v244 offset:33792\n\t"
"v_mfma_f32_16x16x32_bf16 v[64:67], v[240:243], a[172:175], v[64:67]\n\t"
"v_mfma_f32_16x16x32_bf16 v[68:71], a[44:47], a[172:175], v[68:71]\n\t"
"ds_read_b128 a[172:175], v244 offset:34304\n\t"
"v_mfma_f32_16x16x32_bf16 v[68:71], v[240:243], a[176:179], v[68:71]\n\t"
"v_mfma_f32_16x16x32_bf16 v[68:71], a[140:143], a[180:183], v[68:71]\n\t"
"v_mfma_f32_16x16x32_bf16 v[72:75], a[140:143], a[184:187], v[72:75]\n\t"
"v_mfma_f32_16x16x32_bf16 v[72:75], v[240:243], a[180:183], v[72:75]\n\t"
"v_mfma_f32_16x16x32_bf16 v[72:75], a[44:47], a[176:179], v[72:75]\n\t"
"ds_read_b128 a[176:179], v244 offset:34816\n\t"
"v_mfma_f32_16x16x32_bf16 v[76:79], a[44:47], a[180:183], v[76:79]\n\t"
"ds_read_b128 a[180:183], v244 offset:35328\n\t"
"v_mfma_f32_16x16x32_bf16 v[76:79], v[240:243], a[184:187], v[76:79]\n\t"
"ds_read_b128 a[40:43], v252 offset:12288\n\t"
"v_mfma_f32_16x16x32_bf16 v[76:79], a[140:143], a[188:191], v[76:79]\n\t"
"buffer_store_dwordx4 a[56:59], v248, s[16:19], s29 offen offset:256 nt\n\t"
"ds_read_b128 a[48:51], v252 offset:13312\n\t"
"v_mfma_f32_16x16x32_bf16 v[80:83], a[140:143], a[192:195], v[80:83]\n\t"
"ds_read_b128 a[64:67], v252 offset:14336\n\t"
"v_mfma_f32_16x16x32_bf16 v[80:83], v[240:243], a[188:191], v[80:83]\n\t"
"v_mfma_f32_16x16x32_bf16 v[80:83], a[44:47], a[184:187], v[80:83]\n\t"
"ds_read_b128 a[184:187], v244 offset:35840\n\t"
"v_mfma_f32_16x16x32_bf16 v[84:87], a[44:47], a[188:191], v[84:87]\n\t"
"s_mov_b32 s20, 1499136\n\t"
"ds_read_b128 a[188:191], v244 offset:36352\n\t"
"v_mfma_f32_16x16x32_bf16 v[84:87], v[240:243], a[192:195], v[84:87]\n\t"
"v_mfma_f32_16x16x32_bf16 v[84:87], a[140:143], a[196:199], v[84:87]\n\t"
"v_mfma_f32_16x16x32_bf16 v[88:91], a[140:143], a[200:203], v[88:91]\n\t"
"v_mfma_f32_16x16x32_bf16 v[88:91], v[240:243], a[196:199], v[88:91]\n\t"
"v_mfma_f32_16x16x32_bf16 v[88:91], a[44:47], a[192:195], v[88:91]\n\t"
"s_add_u32 s29, s22, 196992\n\t"
"ds_read_b128 a[192:195], v244 offset:36864\n\t"
"v_mfma_f32_16x16x32_bf16 v[92:95], a[44:47], a[196:199], v[92:95]\n\t"
"buffer_load_dwordx4 a[44:47], off, s[80:83], s20 offset:0 sc1\n\t"
"v_mfma_f32_16x16x32_bf16 v[92:95], v[240:243], a[200:203], v[92:95]\n\t"
"s_waitcnt vmcnt(34) lgkmcnt(7)\n\t"
"ds_read_b128 a[196:199], v244 offset:37376\n\t"
"v_mfma_f32_16x16x32_bf16 v[92:95], a[140:143], a[204:207], v[92:95]\n\t"
"s_waitcnt lgkmcnt(6)\n\t"
"ds_read_b128 a[200:203], v244 offset:37888\n\t"
"v_mfma_f32_16x16x32_bf16 v[100:103], a[148:151], a[168:171], v[100:103]\n\t"
"ds_read_b128 a[204:207], v244 offset:38400\n\t"
"v_mfma_f32_16x16x32_bf16 v[100:103], a[156:159], a[176:179], v[100:103]\n\t"
"v_mfma_f32_16x16x32_bf16 v[100:103], a[52:55], a[172:175], v[100:103]\n\t"
"buffer_store_dwordx4 a[40:43], v248, s[16:19], s29 offen offset:0 nt\n\t"
"v_mfma_f32_16x16x32_bf16 v[104:107], a[148:151], a[172:175], v[104:107]\n\t"
"s_waitcnt lgkmcnt(5)\n\t"
"v_mfma_f32_16x16x32_bf16 v[104:107], a[52:55], a[176:179], v[104:107]\n\t"
"v_mfma_f32_16x16x32_bf16 v[104:107], a[156:159], a[180:183], v[104:107]\n\t"
"s_waitcnt lgkmcnt(0)\n\t"
"s_barrier\n\t"
"v_mfma_f32_16x16x32_bf16 v[108:111], a[156:159], a[184:187], v[108:111]\n\t"
"v_mfma_f32_16x16x32_bf16 v[108:111], a[52:55], a[180:183], v[108:111]\n\t"
"buffer_load_dwordx4 v[212:215], off, s[80:83], s20 offset:1024 sc1\n\t"
"v_mfma_f32_16x16x32_bf16 v[108:111], a[148:151], a[176:179], v[108:111]\n\t"
"ds_write_b128 v249, a[0:3] offset:12288\n\t"
"v_mfma_f32_16x16x32_bf16 v[112:115], a[148:151], a[180:183], v[112:115]\n\t"
"v_mfma_f32_16x16x32_bf16 v[112:115], a[52:55], a[184:187], v[112:115]\n\t"
"buffer_load_dwordx4 a[120:123], off, s[80:83], s20 offset:2048 sc1\n\t"
"v_mfma_f32_16x16x32_bf16 v[112:115], a[156:159], a[188:191], v[112:115]\n\t"
"s_mov_b32 s20, 1658880\n\t"
"ds_write_b64 v250, a[128:129] offset:12288\n\t"
"v_mfma_f32_16x16x32_bf16 v[116:119], a[156:159], a[192:195], v[116:119]\n\t"
"v_mfma_f32_16x16x32_bf16 v[116:119], a[52:55], a[188:191], v[116:119]\n\t"
"buffer_load_dwordx4 v[216:219], off, s[80:83], s20 offset:0 sc1\n\t"
"v_mfma_f32_16x16x32_bf16 v[116:119], a[148:151], a[184:187], v[116:119]\n\t"
"ds_write_b128 v249, a[4:7] offset:18432\n\t"
"v_mfma_f32_16x16x32_bf16 v[120:123], a[148:151], a[188:191], v[120:123]\n\t"
"v_mfma_f32_16x16x32_bf16 v[120:123], a[52:55], a[192:195], v[120:123]\n\t"
"v_mfma_f32_16x16x32_bf16 v[120:123], a[156:159], a[196:199], v[120:123]\n\t"
"ds_write_b64 v250, a[130:131] offset:18432\n\t"
"v_mfma_f32_16x16x32_bf16 v[124:127], a[156:159], a[200:203], v[124:127]\n\t"
"v_mfma_f32_16x16x32_bf16 v[124:127], a[52:55], a[196:199], v[124:127]\n\t"
"v_mfma_f32_16x16x32_bf16 v[124:127], a[148:151], a[192:195], v[124:127]\n\t"
"buffer_store_dwordx4 a[48:51], v248, s[16:19], s29 offen offset:128 nt\n\t"
"v_mfma_f32_16x16x32_bf16 v[128:131], a[148:151], a[196:199], v[128:131]\n\t"
"s_waitcnt vmcnt(36)\n\t"
"v_mfma_f32_16x16x32_bf16 v[128:131], a[52:55], a[200:203], v[128:131]\n\t"
"v_mfma_f32_16x16x32_bf16 v[128:131], a[156:159], a[204:207], v[128:131]\n\t"
"v_mfma_f32_16x16x32_bf16 v[132:135], a[68:71], a[168:171], v[132:135]\n\t"
"v_mfma_f32_16x16x32_bf16 v[132:135], v[236:239], a[176:179], v[132:135]\n\t"
"v_mfma_f32_16x16x32_bf16 v[132:135], a[164:167], a[172:175], v[132:135]\n\t"
"v_mfma_f32_16x16x32_bf16 v[136:139], a[68:71], a[172:175], v[136:139]\n\t"
"v_mfma_f32_16x16x32_bf16 v[136:139], a[164:167], a[176:179], v[136:139]\n\t"
"v_mfma_f32_16x16x32_bf16 v[136:139], v[236:239], a[180:183], v[136:139]\n\t"
"v_mfma_f32_16x16x32_bf16 v[140:143], v[236:239], a[184:187], v[140:143]\n\t"
"v_mfma_f32_16x16x32_bf16 v[140:143], a[164:167], a[180:183], v[140:143]\n\t"
"ds_read_b128 a[40:43], v252 offset:0\n\t"
"v_mfma_f32_16x16x32_bf16 v[140:143], a[68:71], a[176:179], v[140:143]\n\t"
"v_mfma_f32_16x16x32_bf16 v[144:147], a[68:71], a[180:183], v[144:147]\n\t"
"v_mfma_f32_16x16x32_bf16 v[144:147], a[164:167], a[184:187], v[144:147]\n\t"
"v_mfma_f32_16x16x32_bf16 v[144:147], v[236:239], a[188:191], v[144:147]\n\t"
"buffer_store_dwordx4 a[64:67], v248, s[16:19], s29 offen offset:256 nt\n\t"
"v_mfma_f32_16x16x32_bf16 v[148:151], v[236:239], a[192:195], v[148:151]\n\t"
"v_mfma_f32_16x16x32_bf16 v[148:151], a[164:167], a[188:191], v[148:151]\n\t"
"v_mfma_f32_16x16x32_bf16 v[148:151], a[68:71], a[184:187], v[148:151]\n\t"
"v_mfma_f32_16x16x32_bf16 v[152:155], a[68:71], a[188:191], v[152:155]\n\t"
"v_mfma_f32_16x16x32_bf16 v[152:155], a[164:167], a[192:195], v[152:155]\n\t"
"ds_read_b128 a[48:51], v252 offset:1024\n\t"
"v_mfma_f32_16x16x32_bf16 v[152:155], v[236:239], a[196:199], v[152:155]\n\t"
"v_mfma_f32_16x16x32_bf16 v[156:159], v[236:239], a[200:203], v[156:159]\n\t"
"ds_read_b128 a[56:59], v252 offset:2048\n\t"
"v_mfma_f32_16x16x32_bf16 v[156:159], a[164:167], a[196:199], v[156:159]\n\t"
"s_add_u32 s29, s22, 393600\n\t"
"v_mfma_f32_16x16x32_bf16 v[156:159], a[68:71], a[192:195], v[156:159]\n\t"
"v_mfma_f32_16x16x32_bf16 v[160:163], a[68:71], a[196:199], v[160:163]\n\t"
"s_waitcnt vmcnt(34)\n\t"
"v_mfma_f32_16x16x32_bf16 v[160:163], a[164:167], a[200:203], v[160:163]\n\t"
"s_waitcnt lgkmcnt(2)\n\t"
"v_mfma_f32_16x16x32_bf16 v[160:163], v[236:239], a[204:207], v[160:163]\n\t"
"v_mfma_f32_16x16x32_bf16 v[164:167], a[76:79], a[168:171], v[164:167]\n\t"
"ds_read_b128 a[168:171], v222 offset:3072\n\t"
"v_mfma_f32_16x16x32_bf16 v[164:167], a[92:95], a[176:179], v[164:167]\n\t"
"v_mfma_f32_16x16x32_bf16 v[164:167], a[84:87], a[172:175], v[164:167]\n\t"
"buffer_store_dwordx4 a[40:43], v248, s[16:19], s29 offen offset:0 nt\n\t"
"v_mfma_f32_16x16x32_bf16 v[168:171], a[76:79], a[172:175], v[168:171]\n\t"
"ds_read_b128 a[172:175], v223 offset:9216\n\t"
"v_mfma_f32_16x16x32_bf16 v[168:171], a[84:87], a[176:179], v[168:171]\n\t"
"v_mfma_f32_16x16x32_bf16 v[168:171], a[92:95], a[180:183], v[168:171]\n\t"
"v_mfma_f32_16x16x32_bf16 v[172:175], a[92:95], a[184:187], v[172:175]\n\t"
"v_mfma_f32_16x16x32_bf16 v[172:175], a[84:87], a[180:183], v[172:175]\n\t"
"v_mfma_f32_16x16x32_bf16 v[172:175], a[76:79], a[176:179], v[172:175]\n\t"
"ds_read_b128 a[176:179], v223 offset:15360\n\t"
"v_mfma_f32_16x16x32_bf16 v[176:179], a[76:79], a[180:183], v[176:179]\n\t"
"ds_read_b128 a[180:183], v222 offset:23040\n\t"
"v_mfma_f32_16x16x32_bf16 v[176:179], a[84:87], a[184:187], v[176:179]\n\t"
"v_mfma_f32_16x16x32_bf16 v[176:179], a[92:95], a[188:191], v[176:179]\n\t"
"s_waitcnt lgkmcnt(4)\n\t"
"s_barrier\n\t"
"v_mfma_f32_16x16x32_bf16 v[180:183], a[92:95], a[192:195], v[180:183]\n\t"
"v_mfma_f32_16x16x32_bf16 v[180:183], a[84:87], a[188:191], v[180:183]\n\t"
"buffer_load_dwordx4 a[0:3], off, s[80:83], s20 offset:1024 sc1\n\t"
"v_mfma_f32_16x16x32_bf16 v[180:183], a[76:79], a[184:187], v[180:183]\n\t"
"ds_read_b128 a[184:187], v223 offset:29184\n\t"
"v_mfma_f32_16x16x32_bf16 v[184:187], a[76:79], a[188:191], v[184:187]\n\t"
"ds_read_b128 a[188:191], v223 offset:35328\n\t"
"v_mfma_f32_16x16x32_bf16 v[184:187], a[84:87], a[192:195], v[184:187]\n\t"
"buffer_load_dwordx4 a[128:131], off, s[80:83], s20 offset:2048 sc1\n\t"
"v_mfma_f32_16x16x32_bf16 v[184:187], a[92:95], a[196:199], v[184:187]\n\t"
"buffer_store_dwordx4 a[48:51], v248, s[16:19], s29 offen offset:128 nt\n\t"
"ds_write_b64 v250, a[136:137] offset:0\n\t"
"s_mov_b32 s20, 1671168\n\t"
"v_mfma_f32_16x16x32_bf16 v[188:191], a[92:95], a[200:203], v[188:191]\n\t"
"v_mfma_f32_16x16x32_bf16 v[188:191], a[84:87], a[196:199], v[188:191]\n\t"
"buffer_load_dwordx4 a[4:7], off, s[80:83], s20 offset:0 sc1\n\t"
"v_mfma_f32_16x16x32_bf16 v[188:191], a[76:79], a[192:195], v[188:191]\n\t"
"ds_read_b128 a[192:195], v222 offset:43008\n\t"
"v_mfma_f32_16x16x32_bf16 v[192:195], a[76:79], a[196:199], v[192:195]\n\t"
"s_waitcnt vmcnt(36) lgkmcnt(5)\n\t"
"ds_read_b128 a[196:199], v223 offset:49152\n\t"
"v_mfma_f32_16x16x32_bf16 v[192:195], a[84:87], a[200:203], v[192:195]\n\t"
"ds_read_b128 a[200:203], v223 offset:55296\n\t"
"v_mfma_f32_16x16x32_bf16 v[192:195], a[92:95], a[204:207], v[192:195]\n\t"
"ds_write_b128 v249, a[8:11] offset:0\n\t"
"Lst16_%=:\n\t"
"v_mfma_f32_16x16x32_bf16 v[0:3], v[96:99], a[168:171], v[0:3]\n\t"
"s_waitcnt lgkmcnt(7)\n\t"
"v_mfma_f32_16x16x32_bf16 v[0:3], a[212:215], a[176:179], v[0:3]\n\t"
"ds_read_b128 a[204:207], v222 offset:62976\n\t"
"v_mfma_f32_16x16x32_bf16 v[0:3], a[208:211], a[172:175], v[0:3]\n\t"
"ds_write_b128 v249, a[12:15] offset:6144\n\t"
"v_mfma_f32_16x16x32_bf16 v[4:7], v[96:99], a[172:175], v[4:7]\n\t"
"s_waitcnt lgkmcnt(8)\n\t"
"v_mfma_f32_16x16x32_bf16 v[4:7], a[208:211], a[176:179], v[4:7]\n\t"
"v_mfma_f32_16x16x32_bf16 v[4:7], a[212:215], a[180:183], v[4:7]\n\t"
"ds_read_b128 a[40:43], v252 offset:12288\n\t"
"v_mfma_f32_16x16x32_bf16 v[8:11], a[212:215], a[184:187], v[8:11]\n\t"
"s_waitcnt lgkmcnt(8)\n\t"
"v_mfma_f32_16x16x32_bf16 v[8:11], a[208:211], a[180:183], v[8:11]\n\t"
"v_mfma_f32_16x16x32_bf16 v[8:11], v[96:99], a[176:179], v[8:11]\n\t"
"buffer_store_dwordx4 a[56:59], v248, s[16:19], s29 offen offset:256 nt\n\t"
"ds_write_b64 v250, a[138:139] offset:6144\n\t"
"v_mfma_f32_16x16x32_bf16 v[12:15], v[96:99], a[180:183], v[12:15]\n\t"
"s_waitcnt lgkmcnt(7)\n\t"
"v_mfma_f32_16x16x32_bf16 v[12:15], a[208:211], a[184:187], v[12:15]\n\t"
"v_mfma_f32_16x16x32_bf16 v[12:15], a[212:215], a[188:191], v[12:15]\n\t"
"v_mfma_f32_16x16x32_bf16 v[16:19], a[212:215], a[192:195], v[16:19]\n\t"
"s_waitcnt lgkmcnt(6)\n\t"
"v_mfma_f32_16x16x32_bf16 v[16:19], a[208:211], a[188:191], v[16:19]\n\t"
"v_mfma_f32_16x16x32_bf16 v[16:19], v[96:99], a[184:187], v[16:19]\n\t"
"v_mfma_f32_16x16x32_bf16 v[20:23], v[96:99], a[188:191], v[20:23]\n\t"
"s_waitcnt lgkmcnt(5)\n\t"
"v_mfma_f32_16x16x32_bf16 v[20:23], a[208:211], a[192:195], v[20:23]\n\t"
"ds_read_b128 a[48:51], v252 offset:13312\n\t"
"v_mfma_f32_16x16x32_bf16 v[20:23], a[212:215], a[196:199], v[20:23]\n\t"
"s_add_u32 s29, s22, 590208\n\t"
"v_mfma_f32_16x16x32_bf16 v[24:27], a[212:215], a[200:203], v[24:27]\n\t"
"s_waitcnt lgkmcnt(4)\n\t"
"ds_read_b128 a[56:59], v252 offset:14336\n\t"
"v_mfma_f32_16x16x32_bf16 v[24:27], a[208:211], a[196:199], v[24:27]\n\t"
"v_mfma_f32_16x16x32_bf16 v[24:27], v[96:99], a[192:195], v[24:27]\n\t"
"s_waitcnt lgkmcnt(3)\n\t"
"v_mfma_f32_16x16x32_bf16 v[28:31], v[96:99], a[196:199], v[28:31]\n\t"
"buffer_load_dwordx4 v[96:99], off, s[80:83], s20 offset:1024 sc1\n\t"
"v_mfma_f32_16x16x32_bf16 v[28:31], a[208:211], a[200:203], v[28:31]\n\t"
"s_waitcnt vmcnt(35)\n\t"
"v_mfma_f32_16x16x32_bf16 v[28:31], a[212:215], a[204:207], v[28:31]\n\t"
"buffer_store_dwordx4 a[40:43], v248, s[16:19], s29 offen offset:0 nt\n\t"
"v_mfma_f32_16x16x32_bf16 v[32:35], a[216:219], a[168:171], v[32:35]\n\t"
"v_mfma_f32_16x16x32_bf16 v[32:35], a[224:227], a[176:179], v[32:35]\n\t"
"buffer_load_dwordx4 a[208:211], off, s[80:83], s20 offset:2048 sc1\n\t"
"v_mfma_f32_16x16x32_bf16 v[32:35], a[220:223], a[172:175], v[32:35]\n\t"
"s_mov_b32 s20, 1683456\n\t"
"v_mfma_f32_16x16x32_bf16 v[36:39], a[216:219], a[172:175], v[36:39]\n\t"
"buffer_load_dwordx4 a[212:215], off, s[80:83], s20 offset:0 sc1\n\t"
"v_mfma_f32_16x16x32_bf16 v[36:39], a[220:223], a[176:179], v[36:39]\n\t"
"v_mfma_f32_16x16x32_bf16 v[36:39], a[224:227], a[180:183], v[36:39]\n\t"
"v_mfma_f32_16x16x32_bf16 v[40:43], a[224:227], a[184:187], v[40:43]\n\t"
"v_mfma_f32_16x16x32_bf16 v[40:43], a[220:223], a[180:183], v[40:43]\n\t"
"v_mfma_f32_16x16x32_bf16 v[40:43], a[216:219], a[176:179], v[40:43]\n\t"
"v_mfma_f32_16x16x32_bf16 v[44:47], a[216:219], a[180:183], v[44:47]\n\t"
"v_mfma_f32_16x16x32_bf16 v[44:47], a[220:223], a[184:187], v[44:47]\n\t"
"v_mfma_f32_16x16x32_bf16 v[44:47], a[224:227], a[188:191], v[44:47]\n\t"
"s_waitcnt lgkmcnt(0)\n\t"
"s_barrier\n\t"
"v_mfma_f32_16x16x32_bf16 v[48:51], a[224:227], a[192:195], v[48:51]\n\t"
"v_mfma_f32_16x16x32_bf16 v[48:51], a[220:223], a[188:191], v[48:51]\n\t"
"buffer_load_dwordx4 a[136:139], off, s[80:83], s20 offset:1024 sc1\n\t"
"v_mfma_f32_16x16x32_bf16 v[48:51], a[216:219], a[184:187], v[48:51]\n\t"
"ds_write_b128 v249, a[16:19] offset:12288\n\t"
"v_mfma_f32_16x16x32_bf16 v[52:55], a[216:219], a[188:191], v[52:55]\n\t"
"v_mfma_f32_16x16x32_bf16 v[52:55], a[220:223], a[192:195], v[52:55]\n\t"
"buffer_load_dwordx4 a[12:15], off, s[80:83], s20 offset:2048 sc1\n\t"
"v_mfma_f32_16x16x32_bf16 v[52:55], a[224:227], a[196:199], v[52:55]\n\t"
"buffer_store_dwordx4 a[48:51], v248, s[16:19], s29 offen offset:128 nt\n\t"
"ds_write_b64 v250, a[144:145] offset:12288\n\t"
"s_mov_b32 s20, 0\n\t"
"v_mfma_f32_16x16x32_bf16 v[56:59], a[224:227], a[200:203], v[56:59]\n\t"
"Lpg12_%=:\n\t"
"s_cbranch_vccnz Lvr0_%=\n\t"
"v_mfma_f32_16x16x32_bf16 v[56:59], a[220:223], a[196:199], v[56:59]\n\t"
"v_mfma_f32_16x16x32_bf16 v[56:59], a[216:219], a[192:195], v[56:59]\n\t"
"ds_write_b128 v249, a[20:23] offset:18432\n\t"
"v_mfma_f32_16x16x32_bf16 v[60:63], a[216:219], a[196:199], v[60:63]\n\t"
"buffer_load_dwordx4 a[216:219], off, s[80:83], s20 offset:0 sc1\n\t"
"v_mfma_f32_16x16x32_bf16 v[60:63], a[220:223], a[200:203], v[60:63]\n\t"
"s_waitcnt vmcnt(39)\n\t"
"v_mfma_f32_16x16x32_bf16 v[60:63], a[224:227], a[204:207], v[60:63]\n\t"
"ds_write_b64 v250, a[146:147] offset:18432\n\t"
"v_mfma_f32_16x16x32_bf16 v[64:67], a[228:231], a[168:171], v[64:67]\n\t"
"buffer_load_dwordx4 a[220:223], off, s[80:83], s20 offset:1024 sc1\n\t"
"v_mfma_f32_16x16x32_bf16 v[64:67], a[236:239], a[176:179], v[64:67]\n\t"
"ds_read_b128 a[168:171], v222 offset:3328\n\t"
"v_mfma_f32_16x16x32_bf16 v[64:67], a[232:235], a[172:175], v[64:67]\n\t"
"v_mfma_f32_16x16x32_bf16 v[68:71], a[228:231], a[172:175], v[68:71]\n\t"
"buffer_load_dwordx4 a[224:227], off, s[80:83], s20 offset:2048 sc1\n\t"
"v_mfma_f32_16x16x32_bf16 v[68:71], a[232:235], a[176:179], v[68:71]\n\t"
"ds_read_b128 a[172:175], v223 offset:9472\n\t"
"v_mfma_f32_16x16x32_bf16 v[68:71], a[236:239], a[180:183], v[68:71]\n\t"
"ds_read_b128 a[40:43], v252 offset:0\n\t"
"v_mfma_f32_16x16x32_bf16 v[72:75], a[236:239], a[184:187], v[72:75]\n\t"
"v_mfma_f32_16x16x32_bf16 v[72:75], a[232:235], a[180:183], v[72:75]\n\t"
"v_mfma_f32_16x16x32_bf16 v[72:75], a[228:231], a[176:179], v[72:75]\n\t"
"ds_read_b128 a[176:179], v223 offset:15616\n\t"
"v_mfma_f32_16x16x32_bf16 v[76:79], a[228:231], a[180:183], v[76:79]\n\t"
"ds_read_b128 a[180:183], v222 offset:23296\n\t"
"v_mfma_f32_16x16x32_bf16 v[76:79], a[232:235], a[184:187], v[76:79]\n\t"
"ds_read_b128 a[48:51], v252 offset:1024\n\t"
"v_mfma_f32_16x16x32_bf16 v[76:79], a[236:239], a[188:191], v[76:79]\n\t"
"buffer_store_dwordx4 a[56:59], v248, s[16:19], s29 offen offset:256 nt\n\t"
"v_mfma_f32_16x16x32_bf16 v[80:83], a[236:239], a[192:195], v[80:83]\n\t"
"ds_read_b128 a[64:67], v252 offset:2048\n\t"
"v_mfma_f32_16x16x32_bf16 v[80:83], a[232:235], a[188:191], v[80:83]\n\t"
"v_mfma_f32_16x16x32_bf16 v[80:83], a[228:231], a[184:187], v[80:83]\n\t"
"ds_read_b128 a[184:187], v223 offset:29440\n\t"
"v_mfma_f32_16x16x32_bf16 v[84:87], a[228:231], a[188:191], v[84:87]\n\t"
"s_mov_b32 s20, 12288\n\t"
"ds_read_b128 a[188:191], v223 offset:35584\n\t"
"v_mfma_f32_16x16x32_bf16 v[84:87], a[232:235], a[192:195], v[84:87]\n\t"
"s_add_u32 s29, s22, 786816\n\t"
"v_mfma_f32_16x16x32_bf16 v[84:87], a[236:239], a[196:199], v[84:87]\n\t"
"v_mfma_f32_16x16x32_bf16 v[88:91], a[236:239], a[200:203], v[88:91]\n\t"
"v_mfma_f32_16x16x32_bf16 v[88:91], a[232:235], a[196:199], v[88:91]\n\t"
"s_waitcnt lgkmcnt(6)\n\t"
"v_mfma_f32_16x16x32_bf16 v[88:91], a[228:231], a[192:195], v[88:91]\n\t"
"ds_read_b128 a[192:195], v222 offset:43264\n\t"
"v_mfma_f32_16x16x32_bf16 v[92:95], a[228:231], a[196:199], v[92:95]\n\t"
"buffer_load_dwordx4 a[228:231], off, s[80:83], s20 offset:0 sc1\n\t"
"v_mfma_f32_16x16x32_bf16 v[92:95], a[232:235], a[200:203], v[92:95]\n\t"
"s_waitcnt vmcnt(40) lgkmcnt(6)\n\t"
"ds_read_b128 a[196:199], v223 offset:49408\n\t"
"v_mfma_f32_16x16x32_bf16 v[92:95], a[236:239], a[204:207], v[92:95]\n\t"
"ds_read_b128 a[200:203], v223 offset:55552\n\t"
"v_mfma_f32_16x16x32_bf16 v[100:103], a[240:243], a[168:171], v[100:103]\n\t"
"s_waitcnt lgkmcnt(7)\n\t"
"ds_read_b128 a[204:207], v222 offset:63232\n\t"
"v_mfma_f32_16x16x32_bf16 v[100:103], a[248:251], a[176:179], v[100:103]\n\t"
"buffer_load_dwordx4 a[232:235], off, s[80:83], s20 offset:1024 sc1\n\t"
"v_mfma_f32_16x16x32_bf16 v[100:103], a[244:247], a[172:175], v[100:103]\n\t"
"buffer_store_dwordx4 a[40:43], v248, s[16:19], s29 offen offset:0 nt\n\t"
"v_mfma_f32_16x16x32_bf16 v[104:107], a[240:243], a[172:175], v[104:107]\n\t"
"s_waitcnt lgkmcnt(5)\n\t"
"v_mfma_f32_16x16x32_bf16 v[104:107], a[244:247], a[176:179], v[104:107]\n\t"
"v_mfma_f32_16x16x32_bf16 v[104:107], a[248:251], a[180:183], v[104:107]\n\t"
"s_waitcnt lgkmcnt(0)\n\t"
"s_barrier\n\t"
"v_mfma_f32_16x16x32_bf16 v[108:111], a[248:251], a[184:187], v[108:111]\n\t"
"v_mfma_f32_16x16x32_bf16 v[108:111], a[244:247], a[180:183], v[108:111]\n\t"
"buffer_load_dwordx4 a[236:239], off, s[80:83], s20 offset:2048 sc1\n\t"
"v_mfma_f32_16x16x32_bf16 v[108:111], a[240:243], a[176:179], v[108:111]\n\t"
"s_mov_b32 s20, 24576\n\t"
"ds_write_b128 v249, a[24:27] offset:0\n\t"
"v_mfma_f32_16x16x32_bf16 v[112:115], a[240:243], a[180:183], v[112:115]\n\t"
"buffer_load_dwordx4 a[16:19], off, s[80:83], s20 offset:0 sc1\n\t"
"v_mfma_f32_16x16x32_bf16 v[112:115], a[244:247], a[184:187], v[112:115]\n\t"
"buffer_load_dwordx4 a[144:147], off, s[80:83], s20 offset:1024 sc1\n\t"
"v_mfma_f32_16x16x32_bf16 v[112:115], a[248:251], a[188:191], v[112:115]\n\t"
"ds_write_b64 v250, a[152:153] offset:0\n\t"
"v_mfma_f32_16x16x32_bf16 v[116:119], a[248:251], a[192:195], v[116:119]\n\t"
"v_mfma_f32_16x16x32_bf16 v[116:119], a[244:247], a[188:191], v[116:119]\n\t"
"buffer_load_dwordx4 a[20:23], off, s[80:83], s20 offset:2048 sc1\n\t"
"v_mfma_f32_16x16x32_bf16 v[116:119], a[240:243], a[184:187], v[116:119]\n\t"
"buffer_store_dwordx4 a[48:51], v248, s[16:19], s29 offen offset:128 nt\n\t"
"ds_write_b64 v250, a[154:155] offset:6144\n\t"
"v_mfma_f32_16x16x32_bf16 v[120:123], a[240:243], a[188:191], v[120:123]\n\t"
"s_mov_b32 s20, 184320\n\t"
"v_mfma_f32_16x16x32_bf16 v[120:123], a[244:247], a[192:195], v[120:123]\n\t"
"v_mfma_f32_16x16x32_bf16 v[120:123], a[248:251], a[196:199], v[120:123]\n\t"
"ds_write_b128 v249, a[28:31] offset:6144\n\t"
"v_mfma_f32_16x16x32_bf16 v[124:127], a[248:251], a[200:203], v[124:127]\n\t"
"v_mfma_f32_16x16x32_bf16 v[124:127], a[244:247], a[196:199], v[124:127]\n\t"
"v_mfma_f32_16x16x32_bf16 v[124:127], a[240:243], a[192:195], v[124:127]\n\t"
"v_mfma_f32_16x16x32_bf16 v[128:131], a[240:243], a[196:199], v[128:131]\n\t"
"buffer_load_dwordx4 a[240:243], off, s[80:83], s20 offset:0 sc1\n\t"
"v_mfma_f32_16x16x32_bf16 v[128:131], a[244:247], a[200:203], v[128:131]\n\t"
"s_waitcnt vmcnt(45)\n\t"
"v_mfma_f32_16x16x32_bf16 v[128:131], a[248:251], a[204:207], v[128:131]\n\t"
"v_mfma_f32_16x16x32_bf16 v[132:135], a[252:255], a[168:171], v[132:135]\n\t"
"buffer_load_dwordx4 a[244:247], off, s[80:83], s20 offset:1024 sc1\n\t"
"v_mfma_f32_16x16x32_bf16 v[132:135], v[196:199], a[176:179], v[132:135]\n\t"
"v_mfma_f32_16x16x32_bf16 v[132:135], a[72:75], a[172:175], v[132:135]\n\t"
"ds_read_b128 a[40:43], v252 offset:12288\n\t"
"v_mfma_f32_16x16x32_bf16 v[136:139], a[252:255], a[172:175], v[136:139]\n\t"
"buffer_load_dwordx4 a[248:251], off, s[80:83], s20 offset:2048 sc1\n\t"
"v_mfma_f32_16x16x32_bf16 v[136:139], a[72:75], a[176:179], v[136:139]\n\t"
"v_mfma_f32_16x16x32_bf16 v[136:139], v[196:199], a[180:183], v[136:139]\n\t"
"buffer_store_dwordx4 a[64:67], v248, s[16:19], s29 offen offset:256 nt\n\t"
"v_mfma_f32_16x16x32_bf16 v[140:143], v[196:199], a[184:187], v[140:143]\n\t"
"v_mfma_f32_16x16x32_bf16 v[140:143], a[72:75], a[180:183], v[140:143]\n\t"
"v_mfma_f32_16x16x32_bf16 v[140:143], a[252:255], a[176:179], v[140:143]\n\t"
"v_mfma_f32_16x16x32_bf16 v[144:147], a[252:255], a[180:183], v[144:147]\n\t"
"v_mfma_f32_16x16x32_bf16 v[144:147], a[72:75], a[184:187], v[144:147]\n\t"
"v_mfma_f32_16x16x32_bf16 v[144:147], v[196:199], a[188:191], v[144:147]\n\t"
"v_mfma_f32_16x16x32_bf16 v[148:151], v[196:199], a[192:195], v[148:151]\n\t"
"v_mfma_f32_16x16x32_bf16 v[148:151], a[72:75], a[188:191], v[148:151]\n\t"
"v_mfma_f32_16x16x32_bf16 v[148:151], a[252:255], a[184:187], v[148:151]\n\t"
"s_add_u32 s29, s22, 983424\n\t"
"v_mfma_f32_16x16x32_bf16 v[152:155], a[252:255], a[188:191], v[152:155]\n\t"
"s_mov_b32 s20, 196608\n\t"
"v_mfma_f32_16x16x32_bf16 v[152:155], a[72:75], a[192:195], v[152:155]\n\t"
"ds_read_b128 a[48:51], v252 offset:13312\n\t"
"v_mfma_f32_16x16x32_bf16 v[152:155], v[196:199], a[196:199], v[152:155]\n\t"
"s_waitcnt lgkmcnt(1)\n\t"
"v_mfma_f32_16x16x32_bf16 v[156:159], v[196:199], a[200:203], v[156:159]\n\t"
"ds_read_b128 a[56:59], v252 offset:14336\n\t"
"v_mfma_f32_16x16x32_bf16 v[156:159], a[72:75], a[196:199], v[156:159]\n\t"
"v_mfma_f32_16x16x32_bf16 v[156:159], a[252:255], a[192:195], v[156:159]\n\t"
"buffer_store_dwordx4 a[40:43], v248, s[16:19], s29 offen offset:0 nt\n\t"
"v_mfma_f32_16x16x32_bf16 v[160:163], a[252:255], a[196:199], v[160:163]\n\t"
"s_waitcnt vmcnt(46)\n\t"
"v_mfma_f32_16x16x32_bf16 v[160:163], a[72:75], a[200:203], v[160:163]\n\t"
"buffer_load_dwordx4 a[252:255], off, s[80:83], s20 offset:0 sc1\n\t"
"v_mfma_f32_16x16x32_bf16 v[160:163], v[196:199], a[204:207], v[160:163]\n\t"
"global_load_dwordx4 v[240:243], v252, s[48:49] offset:2048\n\t"
"v_mfma_f32_16x16x32_bf16 v[164:167], a[104:107], a[168:171], v[164:167]\n\t"
"ds_read_b128 a[168:171], v222 offset:1536\n\t"
"v_mfma_f32_16x16x32_bf16 v[164:167], a[80:83], a[176:179], v[164:167]\n\t"
"v_mfma_f32_16x16x32_bf16 v[164:167], v[200:203], a[172:175], v[164:167]\n\t"
"v_mfma_f32_16x16x32_bf16 v[168:171], a[104:107], a[172:175], v[168:171]\n\t"
"ds_read_b128 a[172:175], v223 offset:7680\n\t"
"v_mfma_f32_16x16x32_bf16 v[168:171], v[200:203], a[176:179], v[168:171]\n\t"
"global_load_dwordx4 a[8:11], v252, s[46:47] offset:1024\n\t"
"v_mfma_f32_16x16x32_bf16 v[168:171], a[80:83], a[180:183], v[168:171]\n\t"
"v_mfma_f32_16x16x32_bf16 v[172:175], a[80:83], a[184:187], v[172:175]\n\t"
"v_mfma_f32_16x16x32_bf16 v[172:175], v[200:203], a[180:183], v[172:175]\n\t"
"v_mfma_f32_16x16x32_bf16 v[172:175], a[104:107], a[176:179], v[172:175]\n\t"
"ds_read_b128 a[176:179], v223 offset:13824\n\t"
"v_mfma_f32_16x16x32_bf16 v[176:179], a[104:107], a[180:183], v[176:179]\n\t"
"s_waitcnt lgkmcnt(4)\n\t"
"ds_read_b128 a[180:183], v222 offset:21504\n\t"
"v_mfma_f32_16x16x32_bf16 v[176:179], v[200:203], a[184:187], v[176:179]\n\t"
"v_mfma_f32_16x16x32_bf16 v[176:179], a[80:83], a[188:191], v[176:179]\n\t"
"s_waitcnt lgkmcnt(4)\n\t"
"s_barrier\n\t"
"v_mfma_f32_16x16x32_bf16 v[180:183], a[80:83], a[192:195], v[180:183]\n\t"
"v_mfma_f32_16x16x32_bf16 v[180:183], v[200:203], a[188:191], v[180:183]\n\t"
"buffer_load_dwordx4 a[24:27], off, s[80:83], s20 offset:1024 sc1\n\t"
"v_mfma_f32_16x16x32_bf16 v[180:183], a[104:107], a[184:187], v[180:183]\n\t"
"ds_read_b128 a[184:187], v223 offset:27648\n\t"
"v_mfma_f32_16x16x32_bf16 v[184:187], a[104:107], a[188:191], v[184:187]\n\t"
"ds_read_b128 a[188:191], v223 offset:33792\n\t"
"v_mfma_f32_16x16x32_bf16 v[184:187], v[200:203], a[192:195], v[184:187]\n\t"
"buffer_load_dwordx4 a[152:155], off, s[80:83], s20 offset:2048 sc1\n\t"
"v_mfma_f32_16x16x32_bf16 v[184:187], a[80:83], a[196:199], v[184:187]\n\t"
"buffer_store_dwordx4 a[48:51], v248, s[16:19], s29 offen offset:128 nt\n\t"
"ds_write_b64 v250, a[160:161] offset:12288\n\t"
"s_mov_b32 s20, 208896\n\t"
"v_mfma_f32_16x16x32_bf16 v[188:191], a[80:83], a[200:203], v[188:191]\n\t"
"v_mfma_f32_16x16x32_bf16 v[188:191], v[200:203], a[196:199], v[188:191]\n\t"
"buffer_load_dwordx4 a[28:31], off, s[80:83], s20 offset:0 sc1\n\t"
"v_mfma_f32_16x16x32_bf16 v[188:191], a[104:107], a[192:195], v[188:191]\n\t"
"ds_read_b128 a[192:195], v222 offset:41472\n\t"
"v_mfma_f32_16x16x32_bf16 v[192:195], a[104:107], a[196:199], v[192:195]\n\t"
"s_waitcnt vmcnt(50) lgkmcnt(5)\n\t"
"ds_read_b128 a[196:199], v223 offset:47616\n\t"
"v_mfma_f32_16x16x32_bf16 v[192:195], v[200:203], a[200:203], v[192:195]\n\t"
"buffer_load_dwordx4 a[104:107], off, s[80:83], s20 offset:1024 sc1\n\t"
"v_mfma_f32_16x16x32_bf16 v[192:195], a[80:83], a[204:207], v[192:195]\n\t"
"ds_write_b128 v249, a[32:35] offset:12288\n\t"
"Lst17_%=:\n\t"
"v_mfma_f32_16x16x32_bf16 v[0:3], a[88:91], a[168:171], v[0:3]\n\t"
"s_waitcnt lgkmcnt(6)\n\t"
"v_mfma_f32_16x16x32_bf16 v[0:3], a[60:63], a[176:179], v[0:3]\n\t"
"ds_read_b128 a[200:203], v223 offset:53760\n\t"
"v_mfma_f32_16x16x32_bf16 v[0:3], a[96:99], a[172:175], v[0:3]\n\t"
"buffer_load_dwordx4 v[200:203], off, s[80:83], s20 offset:2048 sc1\n\t"
"v_mfma_f32_16x16x32_bf16 v[4:7], a[88:91], a[172:175], v[4:7]\n\t"
"s_waitcnt lgkmcnt(6)\n\t"
"ds_read_b128 a[204:207], v222 offset:61440\n\t"
"v_mfma_f32_16x16x32_bf16 v[4:7], a[96:99], a[176:179], v[4:7]\n\t"
"ds_read_b128 a[108:111], v252 offset:0\n\t"
"s_waitcnt vmcnt(7)\n\t"
"v_add_u32 v240, 59904, v240\n\t"
"v_mfma_f32_16x16x32_bf16 v[4:7], a[60:63], a[180:183], v[4:7]\n\t"
"ds_write_b128 v249, a[36:39] offset:18432\n\t"
"v_mfma_f32_16x16x32_bf16 v[8:11], a[60:63], a[184:187], v[8:11]\n\t"
"s_waitcnt lgkmcnt(8)\n\t"
"v_add_u32 v241, 59904, v241\n\t"
"v_cvt_pk_bf16_f32 v0, v0, v1\n\t"
"v_mfma_f32_16x16x32_bf16 v[8:11], a[96:99], a[180:183], v[8:11]\n\t"
"v_cvt_pk_bf16_f32 v2, v2, v3\n\t"
"v_accvgpr_write_b32 a40, v0\n\t"
"v_mfma_f32_16x16x32_bf16 v[8:11], a[88:91], a[176:179], v[8:11]\n\t"
"ds_read_b64_tr_b16 v[228:229], v240 offset:20480\n\t"
"v_accvgpr_write_b32 a41, v2\n\t"
"v_mfma_f32_16x16x32_bf16 v[12:15], a[88:91], a[180:183], v[12:15]\n\t"
"s_waitcnt lgkmcnt(7)\n\t"
"ds_read_b64_tr_b16 v[230:231], v240 offset:22016\n\t"
"v_cvt_pk_bf16_f32 v4, v4, v5\n\t"
"v_mfma_f32_16x16x32_bf16 v[12:15], a[96:99], a[184:187], v[12:15]\n\t"
"ds_read_b64_tr_b16 v[236:237], v241 offset:20480\n\t"
"v_cvt_pk_bf16_f32 v6, v6, v7\n\t"
"v_mfma_f32_16x16x32_bf16 v[12:15], a[60:63], a[188:191], v[12:15]\n\t"
"buffer_store_dwordx4 a[56:59], v248, s[16:19], s29 offen offset:256 nt\n\t"
"ds_read_b64_tr_b16 v[238:239], v241 offset:22016\n\t"
"ds_read_b64_tr_b16 v[232:233], v240 offset:40448\n\t"
"v_accvgpr_write_b32 a48, v4\n\t"
"v_mfma_f32_16x16x32_bf16 v[16:19], a[60:63], a[192:195], v[16:19]\n\t"
"s_waitcnt lgkmcnt(10)\n\t"
"ds_read_b64_tr_b16 v[234:235], v240 offset:41984\n\t"
"v_cvt_pk_bf16_f32 v8, v8, v9\n\t"
"v_mfma_f32_16x16x32_bf16 v[16:19], a[96:99], a[188:191], v[16:19]\n\t"
"s_add_u32 s29, s22, 1180032\n\t"
"ds_read_b64_tr_b16 v[224:225], v241 offset:40448\n\t"
"v_cvt_pk_bf16_f32 v10, v10, v11\n\t"
"v_mfma_f32_16x16x32_bf16 v[16:19], a[88:91], a[184:187], v[16:19]\n\t"
"ds_read_b64_tr_b16 v[226:227], v241 offset:41984\n\t"
"v_accvgpr_write_b32 a49, v6\n\t"
"v_mfma_f32_16x16x32_bf16 v[20:23], a[88:91], a[188:191], v[20:23]\n\t"
"s_waitcnt lgkmcnt(11)\n\t"
"v_cvt_pk_bf16_f32 v12, v12, v13\n\t"
"v_cvt_pk_bf16_f32 v14, v14, v15\n\t"
"v_mfma_f32_16x16x32_bf16 v[20:23], a[96:99], a[192:195], v[20:23]\n\t"
"s_waitcnt lgkmcnt(9)\n\t"
"ds_read_b128 a[116:119], v252 offset:1024\n\t"
"v_accvgpr_write_b32 a56, v8\n\t"
"v_mfma_f32_16x16x32_bf16 v[20:23], a[60:63], a[196:199], v[20:23]\n\t"
"ds_write_b64 v250, a[162:163] offset:18432\n\t"
"v_mfma_f32_16x16x32_bf16 v[24:27], a[60:63], a[200:203], v[24:27]\n\t"
"v_accvgpr_write_b32 a57, v10\n\t"
"v_cvt_pk_bf16_f32 v16, v16, v17\n\t"
"v_mfma_f32_16x16x32_bf16 v[24:27], a[96:99], a[196:199], v[24:27]\n\t"
"v_cvt_pk_bf16_f32 v18, v18, v19\n\t"
"v_accvgpr_write_b32 a64, v12\n\t"
"v_mfma_f32_16x16x32_bf16 v[24:27], a[88:91], a[192:195], v[24:27]\n\t"
"v_accvgpr_write_b32 a65, v14\n\t"
"v_accvgpr_write_b32 a72, v16\n\t"
"v_mfma_f32_16x16x32_bf16 v[28:31], a[88:91], a[196:199], v[28:31]\n\t"
"s_waitcnt vmcnt(7) lgkmcnt(8)\n\t"
"v_cvt_pk_bf16_f32 v20, v20, v21\n\t"
"v_cvt_pk_bf16_f32 v22, v22, v23\n\t"
"v_mfma_f32_16x16x32_bf16 v[28:31], a[96:99], a[200:203], v[28:31]\n\t"
"s_waitcnt lgkmcnt(6)\n\t"
"ds_read_b128 a[124:127], v252 offset:2048\n\t"
"v_accvgpr_write_b32 a73, v18\n\t"
"v_mfma_f32_16x16x32_bf16 v[28:31], a[60:63], a[204:207], v[28:31]\n\t"
"s_waitcnt lgkmcnt(5)\n\t"
"v_accvgpr_write_b32 a80, v20\n\t"
"v_accvgpr_write_b32 a81, v22\n\t"
"v_mfma_f32_16x16x32_bf16 v[228:231], v[228:231], a[8:11], 0\n\t"
"s_waitcnt lgkmcnt(3)\n\t"
"v_cvt_pk_bf16_f32 v24, v24, v25\n\t"
"v_cvt_pk_bf16_f32 v26, v26, v27\n\t"
"v_mfma_f32_16x16x32_bf16 v[236:239], v[236:239], a[8:11], 0\n\t"
"buffer_store_dwordx4 a[108:111], v248, s[16:19], s29 offen offset:0 nt\n\t"
"v_accvgpr_write_b32 a88, v24\n\t"
"v_accvgpr_write_b32 a89, v26\n\t"
"v_mfma_f32_16x16x32_bf16 v[232:235], v[232:235], a[8:11], 0\n\t"
"v_mfma_f32_16x16x32_bf16 v[224:227], v[224:227], a[8:11], 0\n\t"
"v_cvt_pk_bf16_f32 v28, v28, v29\n\t"
"v_cvt_pk_bf16_f32 v30, v30, v31\n\t"
"v_mfma_f32_16x16x32_bf16 v[32:35], v[204:207], a[168:171], v[32:35]\n\t"
"v_accvgpr_write_b32 a96, v28\n\t"
"v_accvgpr_write_b32 a97, v30\n\t"
"v_mfma_f32_16x16x32_bf16 v[32:35], v[208:211], a[176:179], v[32:35]\n\t"
"v_cvt_pk_bf16_f32 v228, v228, v229\n\t"
"v_cvt_pk_bf16_f32 v229, v230, v231\n\t"
"v_mfma_f32_16x16x32_bf16 v[32:35], a[112:115], a[172:175], v[32:35]\n\t"
"v_cvt_pk_bf16_f32 v230, v236, v237\n\t"
"v_cvt_pk_bf16_f32 v231, v238, v239\n\t"
"v_mfma_f32_16x16x32_bf16 v[36:39], v[204:207], a[172:175], v[36:39]\n\t"
"s_waitcnt lgkmcnt(2)\n\t"
"v_cvt_pk_bf16_f32 v232, v232, v233\n\t"
"v_cvt_pk_bf16_f32 v233, v234, v235\n\t"
"v_mfma_f32_16x16x32_bf16 v[36:39], a[112:115], a[176:179], v[36:39]\n\t"
"v_cvt_pk_bf16_f32 v234, v224, v225\n\t"
"v_cvt_pk_bf16_f32 v235, v226, v227\n\t"
"v_mfma_f32_16x16x32_bf16 v[36:39], v[208:211], a[180:183], v[36:39]\n\t"
"ds_write_b128 v242, v[228:231] offset:30720\n\t"
"v_mfma_f32_16x16x32_bf16 v[40:43], v[208:211], a[184:187], v[40:43]\n\t"
"v_cvt_pk_bf16_f32 v32, v32, v33\n\t"
"v_cvt_pk_bf16_f32 v34, v34, v35\n\t"
"v_mfma_f32_16x16x32_bf16 v[40:43], a[112:115], a[180:183], v[40:43]\n\t"
"s_mov_b32 s20, 294912\n\t"
"v_accvgpr_write_b32 a42, v32\n\t"
"v_accvgpr_write_b32 a43, v34\n\t"
"v_mfma_f32_16x16x32_bf16 v[40:43], v[204:207], a[176:179], v[40:43]\n\t"
"ds_write_b128 v242, v[232:235] offset:32256\n\t"
"v_mfma_f32_16x16x32_bf16 v[44:47], v[204:207], a[180:183], v[44:47]\n\t"
"v_cvt_pk_bf16_f32 v36, v36, v37\n\t"
"v_cvt_pk_bf16_f32 v38, v38, v39\n\t"
"v_mfma_f32_16x16x32_bf16 v[44:47], a[112:115], a[184:187], v[44:47]\n\t"
"v_accvgpr_write_b32 a50, v36\n\t"
"v_mfma_f32_16x16x32_bf16 v[44:47], v[208:211], a[188:191], v[44:47]\n\t"
"s_waitcnt lgkmcnt(2)\n\t"
"s_barrier\n\t"
"v_mfma_f32_16x16x32_bf16 v[48:51], v[208:211], a[192:195], v[48:51]\n\t"
"ds_read_b64_tr_b16 v[236:237], v240 offset:512\n\t"
"v_mfma_f32_16x16x32_bf16 v[48:51], a[112:115], a[188:191], v[48:51]\n\t"
"ds_read_b64_tr_b16 v[238:239], v240 offset:2048\n\t"
"v_cvt_pk_bf16_f32 v40, v40, v41\n\t"
"v_mfma_f32_16x16x32_bf16 v[48:51], v[204:207], a[184:187], v[48:51]\n\t"
"buffer_store_dwordx4 a[116:119], v248, s[16:19], s29 offen offset:128 nt\n\t"
"buffer_load_dwordx4 a[32:35], off, s[80:83], s20 offset:0 sc1\n\t"
"ds_read_b64_tr_b16 v[224:225], v241 offset:512\n\t"
"v_mfma_f32_16x16x32_bf16 v[52:55], v[204:207], a[188:191], v[52:55]\n\t"
"ds_read_b64_tr_b16 v[226:227], v241 offset:2048\n\t"
"v_cvt_pk_bf16_f32 v42, v42, v43\n\t"
"v_mfma_f32_16x16x32_bf16 v[52:55], a[112:115], a[192:195], v[52:55]\n\t"
"v_accvgpr_write_b32 a51, v38\n\t"
"v_cvt_pk_bf16_f32 v44, v44, v45\n\t"
"v_mfma_f32_16x16x32_bf16 v[52:55], v[208:211], a[196:199], v[52:55]\n\t"
"v_cvt_pk_bf16_f32 v46, v46, v47\n\t"
"v_accvgpr_write_b32 a58, v40\n\t"
"v_mfma_f32_16x16x32_bf16 v[56:59], v[208:211], a[200:203], v[56:59]\n\t"
"v_accvgpr_write_b32 a59, v42\n\t"
"v_cvt_pk_bf16_f32 v48, v48, v49\n\t"
"v_mfma_f32_16x16x32_bf16 v[56:59], a[112:115], a[196:199], v[56:59]\n\t"
"ds_read_b128 a[148:151], v252 offset:12288\n\t"
"v_cvt_pk_bf16_f32 v50, v50, v51\n\t"
"v_mfma_f32_16x16x32_bf16 v[56:59], v[204:207], a[192:195], v[56:59]\n\t"
"buffer_load_dwordx4 a[160:163], off, s[80:83], s20 offset:1024 sc1\n\t"
"v_mfma_f32_16x16x32_bf16 v[60:63], v[204:207], a[196:199], v[60:63]\n\t"
"buffer_load_dwordx4 a[36:39], off, s[80:83], s20 offset:2048 sc1\n\t"
"v_mfma_f32_16x16x32_bf16 v[60:63], a[112:115], a[200:203], v[60:63]\n\t"
"s_waitcnt lgkmcnt(1)\n\t"
"s_mov_b32 s20, 307200\n\t"
"v_accvgpr_write_b32 a66, v44\n\t"
"v_mfma_f32_16x16x32_bf16 v[60:63], v[208:211], a[204:207], v[60:63]\n\t"
"v_accvgpr_write_b32 a67, v46\n\t"
"v_cvt_pk_bf16_f32 v52, v52, v53\n\t"
"v_mfma_f32_16x16x32_bf16 v[236:239], v[236:239], a[8:11], 0\n\t"
"v_cvt_pk_bf16_f32 v54, v54, v55\n\t"
"v_accvgpr_write_b32 a74, v48\n\t"
"v_mfma_f32_16x16x32_bf16 v[224:227], v[224:227], a[8:11], 0\n\t"
"buffer_load_dwordx4 a[112:115], off, s[80:83], s20 offset:0 sc1\n\t"
"v_mfma_f32_16x16x32_bf16 v[64:67], a[44:47], a[168:171], v[64:67]\n\t"
"buffer_load_dwordx4 v[208:211], off, s[80:83], s20 offset:1024 sc1\n\t"
"v_mfma_f32_16x16x32_bf16 v[64:67], a[120:123], a[176:179], v[64:67]\n\t"
"ds_read_b128 a[168:171], v222 offset:1792\n\t"
"v_accvgpr_write_b32 a75, v50\n\t"
"v_mfma_f32_16x16x32_bf16 v[64:67], v[212:215], a[172:175], v[64:67]\n\t"
"buffer_store_dwordx4 a[124:127], v248, s[16:19], s29 offen offset:256 nt\n\t"
"v_cvt_pk_bf16_f32 v56, v56, v57\n\t"
"v_cvt_pk_bf16_f32 v58, v58, v59\n\t"
"v_accvgpr_write_b32 a82, v52\n\t"
"v_accvgpr_write_b32 a83, v54\n\t"
"v_mfma_f32_16x16x32_bf16 v[68:71], a[44:47], a[172:175], v[68:71]\n\t"
"ds_read_b128 a[172:175], v223 offset:7936\n\t"
"v_cvt_pk_bf16_f32 v236, v236, v237\n\t"
"v_mfma_f32_16x16x32_bf16 v[68:71], v[212:215], a[176:179], v[68:71]\n\t"
"v_cvt_pk_bf16_f32 v237, v238, v239\n\t"
"v_cvt_pk_bf16_f32 v238, v224, v225\n\t"
"v_mfma_f32_16x16x32_bf16 v[68:71], a[120:123], a[180:183], v[68:71]\n\t"
"s_add_u32 s29, s22, 1376640\n\t"
"v_cvt_pk_bf16_f32 v239, v226, v227\n\t"
"v_subrev_u32 v240, 59904, v240\n\t"
"v_mfma_f32_16x16x32_bf16 v[72:75], a[120:123], a[184:187], v[72:75]\n\t"
"v_subrev_u32 v241, 59904, v241\n\t"
"v_cvt_pk_bf16_f32 v60, v60, v61\n\t"
"v_mfma_f32_16x16x32_bf16 v[72:75], v[212:215], a[180:183], v[72:75]\n\t"
"v_cvt_pk_bf16_f32 v62, v62, v63\n\t"
"v_accvgpr_write_b32 a90, v56\n\t"
"v_mfma_f32_16x16x32_bf16 v[72:75], a[44:47], a[176:179], v[72:75]\n\t"
"s_waitcnt lgkmcnt(2)\n\t"
"ds_write_b128 v242, v[236:239] offset:29184\n\t"
"v_mfma_f32_16x16x32_bf16 v[76:79], a[44:47], a[180:183], v[76:79]\n\t"
"v_accvgpr_write_b32 a91, v58\n\t"
"v_accvgpr_write_b32 a98, v60\n\t"
"v_mfma_f32_16x16x32_bf16 v[76:79], v[212:215], a[184:187], v[76:79]\n\t"
"ds_read_b128 a[176:179], v223 offset:14080\n\t"
"v_mfma_f32_16x16x32_bf16 v[76:79], a[120:123], a[188:191], v[76:79]\n\t"
"ds_read_b128 a[180:183], v222 offset:21760\n\t"
"v_accvgpr_write_b32 a99, v62\n\t"
"v_mfma_f32_16x16x32_bf16 v[80:83], a[120:123], a[192:195], v[80:83]\n\t"
"ds_read_b64_tr_b16 v[228:229], v240 offset:512\n\t"
"v_cvt_pk_bf16_f32 v64, v64, v65\n\t"
"v_mfma_f32_16x16x32_bf16 v[80:83], v[212:215], a[188:191], v[80:83]\n\t"
"ds_read_b64_tr_b16 v[230:231], v240 offset:2048\n\t"
"v_cvt_pk_bf16_f32 v66, v66, v67\n\t"
"v_mfma_f32_16x16x32_bf16 v[80:83], a[44:47], a[184:187], v[80:83]\n\t"
"ds_read_b64_tr_b16 v[232:233], v241 offset:512\n\t"
"v_cvt_pk_bf16_f32 v68, v68, v69\n\t"
"v_mfma_f32_16x16x32_bf16 v[84:87], a[44:47], a[188:191], v[84:87]\n\t"
"ds_read_b64_tr_b16 v[234:235], v241 offset:2048\n\t"
"v_cvt_pk_bf16_f32 v70, v70, v71\n\t"
"v_mfma_f32_16x16x32_bf16 v[84:87], v[212:215], a[192:195], v[84:87]\n\t"
"ds_read_b128 a[184:187], v223 offset:27904\n\t"
"v_accvgpr_write_b32 a108, v64\n\t"
"v_mfma_f32_16x16x32_bf16 v[84:87], a[120:123], a[196:199], v[84:87]\n\t"
"buffer_store_dwordx4 a[148:151], v248, s[16:19], s29 offen offset:0 nt\n\t"
"ds_read_b128 a[60:63], v252 offset:13312\n\t"
"ds_read_b128 a[188:191], v223 offset:34048\n\t"
"v_accvgpr_write_b32 a109, v66\n\t"
"v_mfma_f32_16x16x32_bf16 v[88:91], a[120:123], a[200:203], v[88:91]\n\t"
"ds_read_b128 a[100:103], v252 offset:14336\n\t"
"v_cvt_pk_bf16_f32 v72, v72, v73\n\t"
"v_mfma_f32_16x16x32_bf16 v[88:91], v[212:215], a[196:199], v[88:91]\n\t"
"v_cvt_pk_bf16_f32 v74, v74, v75\n\t"
"v_accvgpr_write_b32 a116, v68\n\t"
"v_mfma_f32_16x16x32_bf16 v[88:91], a[44:47], a[192:195], v[88:91]\n\t"
"ds_read_b128 a[192:195], v222 offset:41728\n\t"
"v_accvgpr_write_b32 a117, v70\n\t"
"v_mfma_f32_16x16x32_bf16 v[92:95], a[44:47], a[196:199], v[92:95]\n\t"
"s_waitcnt lgkmcnt(7)\n\t"
"ds_read_b128 a[196:199], v223 offset:47872\n\t"
"v_cvt_pk_bf16_f32 v76, v76, v77\n\t"
"v_mfma_f32_16x16x32_bf16 v[92:95], v[212:215], a[200:203], v[92:95]\n\t"
"s_waitcnt lgkmcnt(6)\n\t"
"ds_read_b128 a[200:203], v223 offset:54016\n\t"
"v_cvt_pk_bf16_f32 v78, v78, v79\n\t"
"v_mfma_f32_16x16x32_bf16 v[92:95], a[120:123], a[204:207], v[92:95]\n\t"
"ds_read_b128 a[204:207], v222 offset:61696\n\t"
"v_accvgpr_write_b32 a124, v72\n\t"
"v_mfma_f32_16x16x32_bf16 v[228:231], v[228:231], a[8:11], 0\n\t"
"s_waitcnt lgkmcnt(6)\n\t"
"v_accvgpr_write_b32 a125, v74\n\t"
"v_cvt_pk_bf16_f32 v80, v80, v81\n\t"
"v_mfma_f32_16x16x32_bf16 v[232:235], v[232:235], a[8:11], 0\n\t"
"v_cvt_pk_bf16_f32 v82, v82, v83\n\t"
"v_accvgpr_write_b32 a132, v76\n\t"
"v_mfma_f32_16x16x32_bf16 v[100:103], v[216:219], a[168:171], v[100:103]\n\t"
"v_accvgpr_write_b32 a133, v78\n\t"
"v_cvt_pk_bf16_f32 v84, v84, v85\n\t"
"v_mfma_f32_16x16x32_bf16 v[100:103], a[128:131], a[176:179], v[100:103]\n\t"
"v_cvt_pk_bf16_f32 v86, v86, v87\n\t"
"v_accvgpr_write_b32 a140, v80\n\t"
"v_mfma_f32_16x16x32_bf16 v[100:103], a[0:3], a[172:175], v[100:103]\n\t"
"buffer_load_dwordx4 a[120:123], off, s[80:83], s20 offset:2048 sc1\n\t"
"v_mfma_f32_16x16x32_bf16 v[104:107], v[216:219], a[172:175], v[104:107]\n\t"
"v_cvt_pk_bf16_f32 v228, v228, v229\n\t"
"v_cvt_pk_bf16_f32 v229, v230, v231\n\t"
"v_mfma_f32_16x16x32_bf16 v[104:107], a[0:3], a[176:179], v[104:107]\n\t"
"v_cvt_pk_bf16_f32 v230, v232, v233\n\t"
"v_mfma_f32_16x16x32_bf16 v[104:107], a[128:131], a[180:183], v[104:107]\n\t"
"s_waitcnt lgkmcnt(0)\n\t"
"s_barrier\n\t"
"v_mfma_f32_16x16x32_bf16 v[108:111], a[128:131], a[184:187], v[108:111]\n\t"
"v_cvt_pk_bf16_f32 v231, v234, v235\n\t"
"v_mfma_f32_16x16x32_bf16 v[108:111], a[0:3], a[180:183], v[108:111]\n\t"
"v_accvgpr_write_b32 a141, v82\n\t"
"v_cvt_pk_bf16_f32 v88, v88, v89\n\t"
"v_mfma_f32_16x16x32_bf16 v[108:111], v[216:219], a[176:179], v[108:111]\n\t"
"buffer_store_dwordx4 a[60:63], v248, s[16:19], s29 offen offset:128 nt\n\t"
"ds_read_b64_tr_b16 v[224:225], v240 offset:20480\n\t"
"ds_read_b64_tr_b16 v[226:227], v240 offset:22016\n\t"
"v_cvt_pk_bf16_f32 v90, v90, v91\n\t"
"v_mfma_f32_16x16x32_bf16 v[112:115], v[216:219], a[180:183], v[112:115]\n\t"
"ds_read_b64_tr_b16 v[236:237], v241 offset:20480\n\t"
"v_accvgpr_write_b32 a148, v84\n\t"
"v_mfma_f32_16x16x32_bf16 v[112:115], a[0:3], a[184:187], v[112:115]\n\t"
"ds_read_b64_tr_b16 v[238:239], v241 offset:22016\n\t"
"v_accvgpr_write_b32 a149, v86\n\t"
"v_mfma_f32_16x16x32_bf16 v[112:115], a[128:131], a[188:191], v[112:115]\n\t"
"ds_write_b128 v242, v[228:231] offset:24576\n\t"
"v_mfma_f32_16x16x32_bf16 v[116:119], a[128:131], a[192:195], v[116:119]\n\t"
"v_cvt_pk_bf16_f32 v92, v92, v93\n\t"
"v_cvt_pk_bf16_f32 v94, v94, v95\n\t"
"v_mfma_f32_16x16x32_bf16 v[116:119], a[0:3], a[188:191], v[116:119]\n\t"
"ds_read_b64_tr_b16 v[232:233], v240 offset:40448\n\t"
"v_mfma_f32_16x16x32_bf16 v[116:119], v[216:219], a[184:187], v[116:119]\n\t"
"ds_read_b64_tr_b16 v[234:235], v240 offset:41984\n\t"
"v_accvgpr_write_b32 a156, v88\n\t"
"v_mfma_f32_16x16x32_bf16 v[120:123], v[216:219], a[188:191], v[120:123]\n\t"
"s_mov_b32 s20, 319488\n\t"
"ds_read_b64_tr_b16 v[228:229], v241 offset:40448\n\t"
"v_accvgpr_write_b32 a157, v90\n\t"
"v_mfma_f32_16x16x32_bf16 v[120:123], a[0:3], a[192:195], v[120:123]\n\t"
"ds_read_b64_tr_b16 v[230:231], v241 offset:41984\n\t"
"v_accvgpr_write_b32 a164, v92\n\t"
"v_mfma_f32_16x16x32_bf16 v[120:123], a[128:131], a[196:199], v[120:123]\n\t"
"ds_write_b128 v249, a[40:43] offset:0\n\t"
"v_mfma_f32_16x16x32_bf16 v[124:127], a[128:131], a[200:203], v[124:127]\n\t"
"v_accvgpr_write_b32 a165, v94\n\t"
"v_cvt_pk_bf16_f32 v100, v100, v101\n\t"
"v_mfma_f32_16x16x32_bf16 v[124:127], a[0:3], a[196:199], v[124:127]\n\t"
"v_cvt_pk_bf16_f32 v102, v102, v103\n\t"
"v_cvt_pk_bf16_f32 v104, v104, v105\n\t"
"v_mfma_f32_16x16x32_bf16 v[124:127], v[216:219], a[192:195], v[124:127]\n\t"
"ds_write_b128 v249, a[56:59] offset:12288\n\t"
"v_mfma_f32_16x16x32_bf16 v[128:131], v[216:219], a[196:199], v[128:131]\n\t"
"buffer_load_dwordx4 v[216:219], off, s[80:83], s20 offset:0 sc1\n\t"
"v_mfma_f32_16x16x32_bf16 v[128:131], a[0:3], a[200:203], v[128:131]\n\t"
"s_waitcnt lgkmcnt(7)\n\t"
"v_cvt_pk_bf16_f32 v106, v106, v107\n\t"
"v_accvgpr_write_b32 a44, v100\n\t"
"v_mfma_f32_16x16x32_bf16 v[128:131], a[128:131], a[204:207], v[128:131]\n\t"
"buffer_store_dwordx4 a[100:103], v248, s[16:19], s29 offen offset:256 nt\n\t"
"ds_write_b64 v250, a[108:109] offset:0\n\t"
"v_accvgpr_write_b32 a45, v102\n\t"
"v_mfma_f32_16x16x32_bf16 v[224:227], v[224:227], a[8:11], 0\n\t"
"s_waitcnt lgkmcnt(3)\n\t"
"ds_write_b64 v250, a[124:125] offset:12288\n\t"
"v_mfma_f32_16x16x32_bf16 v[236:239], v[236:239], a[8:11], 0\n\t"
"buffer_load_dwordx4 a[128:131], off, s[80:83], s20 offset:1024 sc1\n\t"
"v_mfma_f32_16x16x32_bf16 v[232:235], v[232:235], a[8:11], 0\n\t"
"ds_write_b128 v249, a[48:51] offset:6144\n\t"
"v_mfma_f32_16x16x32_bf16 v[228:231], v[228:231], a[8:11], 0\n\t"
"v_cvt_pk_bf16_f32 v108, v108, v109\n\t"
"v_cvt_pk_bf16_f32 v110, v110, v111\n\t"
"v_mfma_f32_16x16x32_bf16 v[132:135], a[4:7], a[168:171], v[132:135]\n\t"
"v_accvgpr_write_b32 a52, v104\n\t"
"v_accvgpr_write_b32 a53, v106\n\t"
"v_mfma_f32_16x16x32_bf16 v[132:135], a[208:211], a[176:179], v[132:135]\n\t"
"v_cvt_pk_bf16_f32 v112, v112, v113\n\t"
"v_cvt_pk_bf16_f32 v114, v114, v115\n\t"
"v_mfma_f32_16x16x32_bf16 v[132:135], v[96:99], a[172:175], v[132:135]\n\t"
"ds_write_b128 v249, a[64:67] offset:18432\n\t"
"v_mfma_f32_16x16x32_bf16 v[136:139], a[4:7], a[172:175], v[136:139]\n\t"
"v_cvt_pk_bf16_f32 v224, v224, v225\n\t"
"v_cvt_pk_bf16_f32 v225, v226, v227\n\t"
"v_mfma_f32_16x16x32_bf16 v[136:139], v[96:99], a[176:179], v[136:139]\n\t"
"v_accvgpr_write_b32 a60, v108\n\t"
"v_accvgpr_write_b32 a61, v110\n\t"
"v_mfma_f32_16x16x32_bf16 v[136:139], a[208:211], a[180:183], v[136:139]\n\t"
"ds_write_b64 v250, a[116:117] offset:6144\n\t"
"v_cvt_pk_bf16_f32 v226, v236, v237\n\t"
"v_cvt_pk_bf16_f32 v227, v238, v239\n\t"
"v_mfma_f32_16x16x32_bf16 v[140:143], a[208:211], a[184:187], v[140:143]\n\t"
"v_cvt_pk_bf16_f32 v232, v232, v233\n\t"
"v_cvt_pk_bf16_f32 v233, v234, v235\n\t"
"v_mfma_f32_16x16x32_bf16 v[140:143], v[96:99], a[180:183], v[140:143]\n\t"
"Lpg13_%=:\n\t"
"s_cbranch_vccnz Lvr1_%=\n\t"
"v_cvt_pk_bf16_f32 v116, v116, v117\n\t"
"v_cvt_pk_bf16_f32 v118, v118, v119\n\t"
"v_mfma_f32_16x16x32_bf16 v[140:143], a[4:7], a[176:179], v[140:143]\n\t"
"ds_write_b128 v242, v[224:227] offset:26112\n\t"
"v_cvt_pk_bf16_f32 v234, v228, v229\n\t"
"v_cvt_pk_bf16_f32 v235, v230, v231\n\t"
"v_accvgpr_write_b32 a68, v112\n\t"
"v_accvgpr_write_b32 a69, v114\n\t"
"v_mfma_f32_16x16x32_bf16 v[144:147], a[4:7], a[180:183], v[144:147]\n\t"
"v_cvt_pk_bf16_f32 v120, v120, v121\n\t"
"v_cvt_pk_bf16_f32 v122, v122, v123\n\t"
"v_mfma_f32_16x16x32_bf16 v[144:147], v[96:99], a[184:187], v[144:147]\n\t"
"v_accvgpr_write_b32 a76, v116\n\t"
"v_accvgpr_write_b32 a77, v118\n\t"
"v_mfma_f32_16x16x32_bf16 v[144:147], a[208:211], a[188:191], v[144:147]\n\t"
"ds_write_b128 v242, v[232:235] offset:27648\n\t"
"v_mfma_f32_16x16x32_bf16 v[148:151], a[208:211], a[192:195], v[148:151]\n\t"
"v_cvt_pk_bf16_f32 v124, v124, v125\n\t"
"v_cvt_pk_bf16_f32 v126, v126, v127\n\t"
"v_mfma_f32_16x16x32_bf16 v[148:151], v[96:99], a[188:191], v[148:151]\n\t"
"v_accvgpr_write_b32 a84, v120\n\t"
"v_accvgpr_write_b32 a85, v122\n\t"
"v_mfma_f32_16x16x32_bf16 v[148:151], a[4:7], a[184:187], v[148:151]\n\t"
"ds_write_b64 v250, a[132:133] offset:18432\n\t"
"v_mfma_f32_16x16x32_bf16 v[152:155], a[4:7], a[188:191], v[152:155]\n\t"
"v_cvt_pk_bf16_f32 v128, v128, v129\n\t"
"v_cvt_pk_bf16_f32 v130, v130, v131\n\t"
"v_mfma_f32_16x16x32_bf16 v[152:155], v[96:99], a[192:195], v[152:155]\n\t"
"v_accvgpr_write_b32 a92, v124\n\t"
"v_accvgpr_write_b32 a93, v126\n\t"
"v_mfma_f32_16x16x32_bf16 v[152:155], a[208:211], a[196:199], v[152:155]\n\t"
"v_accvgpr_write_b32 a100, v128\n\t"
"v_accvgpr_write_b32 a101, v130\n\t"
"v_mfma_f32_16x16x32_bf16 v[156:159], a[208:211], a[200:203], v[156:159]\n\t"
"v_cvt_pk_bf16_f32 v132, v132, v133\n\t"
"v_cvt_pk_bf16_f32 v134, v134, v135\n\t"
"v_mfma_f32_16x16x32_bf16 v[156:159], v[96:99], a[196:199], v[156:159]\n\t"
"v_cvt_pk_bf16_f32 v136, v136, v137\n\t"
"v_cvt_pk_bf16_f32 v138, v138, v139\n\t"
"v_mfma_f32_16x16x32_bf16 v[156:159], a[4:7], a[192:195], v[156:159]\n\t"
"v_accvgpr_write_b32 a46, v132\n\t"
"v_accvgpr_write_b32 a47, v134\n\t"
"v_mfma_f32_16x16x32_bf16 v[160:163], a[4:7], a[196:199], v[160:163]\n\t"
"v_cvt_pk_bf16_f32 v140, v140, v141\n\t"
"v_cvt_pk_bf16_f32 v142, v142, v143\n\t"
"v_mfma_f32_16x16x32_bf16 v[160:163], v[96:99], a[200:203], v[160:163]\n\t"
"v_accvgpr_write_b32 a54, v136\n\t"
"v_accvgpr_write_b32 a55, v138\n\t"
"v_mfma_f32_16x16x32_bf16 v[160:163], a[208:211], a[204:207], v[160:163]\n\t"
"v_cvt_pk_bf16_f32 v144, v144, v145\n\t"
"v_cvt_pk_bf16_f32 v146, v146, v147\n\t"
"v_mfma_f32_16x16x32_bf16 v[164:167], a[212:215], a[168:171], v[164:167]\n\t"
"v_accvgpr_write_b32 a62, v140\n\t"
"v_accvgpr_write_b32 a63, v142\n\t"
"v_mfma_f32_16x16x32_bf16 v[164:167], a[12:15], a[176:179], v[164:167]\n\t"
"v_cvt_pk_bf16_f32 v148, v148, v149\n\t"
"v_cvt_pk_bf16_f32 v150, v150, v151\n\t"
"v_mfma_f32_16x16x32_bf16 v[164:167], a[136:139], a[172:175], v[164:167]\n\t"
"buffer_load_dwordx4 a[4:7], off, s[80:83], s20 offset:2048 sc1\n\t"
"s_mov_b32 s20, 479232\n\t"
"v_mfma_f32_16x16x32_bf16 v[168:171], a[212:215], a[172:175], v[168:171]\n\t"
"buffer_load_dwordx4 a[208:211], off, s[80:83], s20 offset:0 sc1\n\t"
"v_mfma_f32_16x16x32_bf16 v[168:171], a[136:139], a[176:179], v[168:171]\n\t"
"v_accvgpr_write_b32 a70, v144\n\t"
"v_accvgpr_write_b32 a71, v146\n\t"
"v_mfma_f32_16x16x32_bf16 v[168:171], a[12:15], a[180:183], v[168:171]\n\t"
"v_cvt_pk_bf16_f32 v152, v152, v153\n\t"
"v_cvt_pk_bf16_f32 v154, v154, v155\n\t"
"v_mfma_f32_16x16x32_bf16 v[172:175], a[12:15], a[184:187], v[172:175]\n\t"
"v_accvgpr_write_b32 a78, v148\n\t"
"v_accvgpr_write_b32 a79, v150\n\t"
"v_mfma_f32_16x16x32_bf16 v[172:175], a[136:139], a[180:183], v[172:175]\n\t"
"v_cvt_pk_bf16_f32 v156, v156, v157\n\t"
"v_cvt_pk_bf16_f32 v158, v158, v159\n\t"
"v_mfma_f32_16x16x32_bf16 v[172:175], a[212:215], a[176:179], v[172:175]\n\t"
"v_accvgpr_write_b32 a86, v152\n\t"
"v_accvgpr_write_b32 a87, v154\n\t"
"v_mfma_f32_16x16x32_bf16 v[176:179], a[212:215], a[180:183], v[176:179]\n\t"
"v_cvt_pk_bf16_f32 v160, v160, v161\n\t"
"v_cvt_pk_bf16_f32 v162, v162, v163\n\t"
"v_mfma_f32_16x16x32_bf16 v[176:179], a[136:139], a[184:187], v[176:179]\n\t"
"v_accvgpr_write_b32 a94, v156\n\t"
"v_mfma_f32_16x16x32_bf16 v[176:179], a[12:15], a[188:191], v[176:179]\n\t"
"s_waitcnt lgkmcnt(0)\n\t"
"s_barrier\n\t"
"v_mfma_f32_16x16x32_bf16 v[180:183], a[12:15], a[192:195], v[180:183]\n\t"
"ds_read_b128 a[168:171], v244 offset:24576\n\t"
"v_mfma_f32_16x16x32_bf16 v[180:183], a[136:139], a[188:191], v[180:183]\n\t"
"ds_read_b128 a[172:175], v244 offset:25088\n\t"
"v_accvgpr_write_b32 a95, v158\n\t"
"v_mfma_f32_16x16x32_bf16 v[180:183], a[212:215], a[184:187], v[180:183]\n\t"
"ds_read_b128 a[176:179], v244 offset:25600\n\t"
"v_cvt_pk_bf16_f32 v164, v164, v165\n\t"
"v_mfma_f32_16x16x32_bf16 v[184:187], a[212:215], a[188:191], v[184:187]\n\t"
"s_add_u32 s29, s23, 0\n\t"
"ds_read_b128 v[196:199], v252 offset:0\n\t"
"v_cvt_pk_bf16_f32 v166, v166, v167\n\t"
"v_mfma_f32_16x16x32_bf16 v[184:187], a[136:139], a[192:195], v[184:187]\n\t"
"ds_read_b128 a[180:183], v244 offset:26112\n\t"
"v_accvgpr_write_b32 a102, v160\n\t"
"v_mfma_f32_16x16x32_bf16 v[184:187], a[12:15], a[196:199], v[184:187]\n\t"
"ds_read_b128 v[204:207], v252 offset:1024\n\t"
"v_accvgpr_write_b32 a103, v162\n\t"
"v_mfma_f32_16x16x32_bf16 v[188:191], a[12:15], a[200:203], v[188:191]\n\t"
"ds_read_b128 a[184:187], v244 offset:26624\n\t"
"v_cvt_pk_bf16_f32 v168, v168, v169\n\t"
"v_mfma_f32_16x16x32_bf16 v[188:191], a[136:139], a[196:199], v[188:191]\n\t"
"ds_read_b128 a[188:191], v244 offset:27136\n\t"
"v_cvt_pk_bf16_f32 v170, v170, v171\n\t"
"v_mfma_f32_16x16x32_bf16 v[188:191], a[212:215], a[192:195], v[188:191]\n\t"
"buffer_load_dwordx4 a[40:43], off, s[80:83], s20 offset:1024 sc1\n\t"
"s_waitcnt lgkmcnt(4)\n\t"
"v_mfma_f32_16x16x32_bf16 v[192:195], a[212:215], a[196:199], v[192:195]\n\t"
"buffer_load_dwordx4 a[48:51], off, s[80:83], s20 offset:2048 sc1\n\t"
"v_mfma_f32_16x16x32_bf16 v[192:195], a[136:139], a[200:203], v[192:195]\n\t"
"s_mov_b32 s20, 491520\n\t"
"ds_read_b128 a[192:195], v244 offset:27648\n\t"
"v_accvgpr_write_b32 a110, v164\n\t"
"v_mfma_f32_16x16x32_bf16 v[192:195], a[12:15], a[204:207], v[192:195]\n\t"
"buffer_load_dwordx4 a[56:59], off, s[80:83], s20 offset:0 sc1\n\t"
"s_waitcnt lgkmcnt(3)\n\t"
"Lst18_%=:\n\t"
"v_mfma_f32_16x16x32_bf16 v[2:5], a[216:219], a[168:171], 0\n\t"
"ds_read_b128 a[196:199], v244 offset:28160\n\t"
"v_accvgpr_write_b32 a111, v166\n\t"
"v_mfma_f32_16x16x32_bf16 v[2:5], a[224:227], a[176:179], v[2:5]\n\t"
"ds_read_b128 v[212:215], v252 offset:2048\n\t"
"v_cvt_pk_bf16_f32 v172, v172, v173\n\t"
"v_mfma_f32_16x16x32_bf16 v[2:5], a[220:223], a[172:175], v[2:5]\n\t"
"buffer_load_dwordx4 a[64:67], off, s[80:83], s20 offset:1024 sc1\n\t"
"v_mfma_f32_16x16x32_bf16 v[6:9], a[216:219], a[172:175], 0\n\t"
"s_waitcnt lgkmcnt(4)\n\t"
"ds_read_b128 a[200:203], v244 offset:28672\n\t"
"v_cvt_pk_bf16_f32 v174, v174, v175\n\t"
"v_mfma_f32_16x16x32_bf16 v[6:9], a[220:223], a[176:179], v[6:9]\n\t"
"ds_read_b128 a[204:207], v244 offset:29184\n\t"
"v_accvgpr_write_b32 a118, v168\n\t"
"v_mfma_f32_16x16x32_bf16 v[6:9], a[224:227], a[180:183], v[6:9]\n\t"
"buffer_store_dwordx4 v[196:199], v248, s[16:19], s29 offen offset:0 nt\n\t"
"buffer_load_dwordx4 a[136:139], off, s[80:83], s20 offset:2048 sc1\n\t"
"v_accvgpr_write_b32 a119, v170\n\t"
"s_mov_b32 s20, 503808\n\t"
"v_mfma_f32_16x16x32_bf16 v[10:13], a[224:227], a[184:187], 0\n\t"
"s_waitcnt lgkmcnt(5)\n\t"
"v_cvt_pk_bf16_f32 v176, v176, v177\n\t"
"v_cvt_pk_bf16_f32 v178, v178, v179\n\t"
"v_mfma_f32_16x16x32_bf16 v[10:13], a[220:223], a[180:183], v[10:13]\n\t"
"v_accvgpr_write_b32 a126, v172\n\t"
"v_accvgpr_write_b32 a127, v174\n\t"
"v_mfma_f32_16x16x32_bf16 v[10:13], a[216:219], a[176:179], v[10:13]\n\t"
"buffer_store_dwordx4 v[204:207], v248, s[16:19], s29 offen offset:128 nt\n\t"
"buffer_load_dwordx4 a[12:15], off, s[80:83], s20 offset:0 sc1\n\t"
"ds_read_b128 v[196:199], v252 offset:12288\n\t"
"v_mfma_f32_16x16x32_bf16 v[14:17], a[216:219], a[180:183], 0\n\t"
"s_waitcnt lgkmcnt(5)\n\t"
"v_cvt_pk_bf16_f32 v180, v180, v181\n\t"
"v_cvt_pk_bf16_f32 v182, v182, v183\n\t"
"v_mfma_f32_16x16x32_bf16 v[14:17], a[220:223], a[184:187], v[14:17]\n\t"
"s_waitcnt lgkmcnt(3)\n\t"
"v_accvgpr_write_b32 a134, v176\n\t"
"v_accvgpr_write_b32 a135, v178\n\t"
"v_mfma_f32_16x16x32_bf16 v[14:17], a[224:227], a[188:191], v[14:17]\n\t"
"buffer_load_dwordx4 a[212:215], off, s[80:83], s20 offset:1024 sc1\n\t"
"v_mfma_f32_16x16x32_bf16 v[18:21], a[224:227], a[192:195], 0\n\t"
"v_cvt_pk_bf16_f32 v184, v184, v185\n\t"
"v_cvt_pk_bf16_f32 v186, v186, v187\n\t"
"v_mfma_f32_16x16x32_bf16 v[18:21], a[220:223], a[188:191], v[18:21]\n\t"
"v_accvgpr_write_b32 a142, v180\n\t"
"v_accvgpr_write_b32 a143, v182\n\t"
"v_mfma_f32_16x16x32_bf16 v[18:21], a[216:219], a[184:187], v[18:21]\n\t"
"v_cvt_pk_bf16_f32 v188, v188, v189\n\t"
"v_cvt_pk_bf16_f32 v190, v190, v191\n\t"
"v_mfma_f32_16x16x32_bf16 v[22:25], a[216:219], a[188:191], 0\n\t"
"s_waitcnt lgkmcnt(2)\n\t"
"v_accvgpr_write_b32 a150, v184\n\t"
"v_accvgpr_write_b32 a151, v186\n\t"
"v_mfma_f32_16x16x32_bf16 v[22:25], a[220:223], a[192:195], v[22:25]\n\t"
"ds_read_b128 v[204:207], v252 offset:13312\n\t"
"v_cvt_pk_bf16_f32 v192, v192, v193\n\t"
"v_mfma_f32_16x16x32_bf16 v[22:25], a[224:227], a[196:199], v[22:25]\n\t"
"v_cvt_pk_bf16_f32 v194, v194, v195\n\t"
"v_accvgpr_write_b32 a158, v188\n\t"
"v_mfma_f32_16x16x32_bf16 v[26:29], a[224:227], a[200:203], 0\n\t"
"s_waitcnt lgkmcnt(2)\n\t"
"ds_read_b128 a[0:3], v252 offset:14336\n\t"
"v_accvgpr_write_b32 a159, v190\n\t"
"v_mfma_f32_16x16x32_bf16 v[26:29], a[220:223], a[196:199], v[26:29]\n\t"
"v_accvgpr_write_b32 a166, v192\n\t"
"v_accvgpr_write_b32 a167, v194\n\t"
"v_mfma_f32_16x16x32_bf16 v[26:29], a[216:219], a[192:195], v[26:29]\n\t"
"buffer_store_dwordx4 v[212:215], v248, s[16:19], s29 offen offset:256 nt\n\t"
"s_add_u32 s29, s23, 393216\n\t"
"v_mfma_f32_16x16x32_bf16 v[30:33], a[216:219], a[196:199], 0\n\t"
"v_mfma_f32_16x16x32_bf16 v[30:33], a[220:223], a[200:203], v[30:33]\n\t"
"buffer_load_dwordx4 a[216:219], off, s[80:83], s20 offset:2048 sc1\n\t"
"v_mfma_f32_16x16x32_bf16 v[30:33], a[224:227], a[204:207], v[30:33]\n\t"
"s_mov_b32 s20, 110592\n\t"
"s_waitcnt lgkmcnt(2)\n\t"
"v_mfma_f32_16x16x32_bf16 v[34:37], a[228:231], a[168:171], 0\n\t"
"buffer_load_dwordx4 a[220:223], off, s[80:83], s20 offset:0 sc1\n\t"
"v_mfma_f32_16x16x32_bf16 v[34:37], a[236:239], a[176:179], v[34:37]\n\t"
"v_mfma_f32_16x16x32_bf16 v[34:37], a[232:235], a[172:175], v[34:37]\n\t"
"buffer_store_dwordx4 v[196:199], v248, s[16:19], s29 offen offset:0 nt\n\t"
"v_mfma_f32_16x16x32_bf16 v[38:41], a[228:231], a[172:175], 0\n\t"
"v_mfma_f32_16x16x32_bf16 v[38:41], a[232:235], a[176:179], v[38:41]\n\t"
"buffer_load_dwordx4 a[224:227], off, s[80:83], s20 offset:1024 sc1\n\t"
"v_mfma_f32_16x16x32_bf16 v[38:41], a[236:239], a[180:183], v[38:41]\n\t"
"v_mfma_f32_16x16x32_bf16 v[42:45], a[236:239], a[184:187], 0\n\t"
"v_mfma_f32_16x16x32_bf16 v[42:45], a[232:235], a[180:183], v[42:45]\n\t"
"v_mfma_f32_16x16x32_bf16 v[42:45], a[228:231], a[176:179], v[42:45]\n\t"
"ds_read_b64_tr_b16 v[236:237], v240 offset:1024\n\t"
"v_mfma_f32_16x16x32_bf16 v[46:49], a[228:231], a[180:183], 0\n\t"
"ds_read_b64_tr_b16 v[238:239], v240 offset:2560\n\t"
"v_mfma_f32_16x16x32_bf16 v[46:49], a[232:235], a[184:187], v[46:49]\n\t"
"ds_read_b64_tr_b16 v[228:229], v241 offset:1024\n\t"
"v_mfma_f32_16x16x32_bf16 v[46:49], a[236:239], a[188:191], v[46:49]\n\t"
"s_waitcnt lgkmcnt(3)\n\t"
"s_barrier\n\t"
"v_mfma_f32_16x16x32_bf16 v[50:53], a[236:239], a[192:195], 0\n\t"
"ds_read_b64_tr_b16 v[230:231], v241 offset:2560\n\t"
"v_mfma_f32_16x16x32_bf16 v[50:53], a[232:235], a[188:191], v[50:53]\n\t"
"ds_read_b64_tr_b16 v[224:225], v240 offset:20992\n\t"
"v_mfma_f32_16x16x32_bf16 v[50:53], a[228:231], a[184:187], v[50:53]\n\t"
"ds_read_b64_tr_b16 v[226:227], v240 offset:22528\n\t"
"v_mfma_f32_16x16x32_bf16 v[54:57], a[228:231], a[188:191], 0\n\t"
"ds_read_b64_tr_b16 v[232:233], v241 offset:20992\n\t"
"v_mfma_f32_16x16x32_bf16 v[54:57], a[232:235], a[192:195], v[54:57]\n\t"
"ds_read_b64_tr_b16 v[234:235], v241 offset:22528\n\t"
"v_mfma_f32_16x16x32_bf16 v[54:57], a[236:239], a[196:199], v[54:57]\n\t"
"buffer_store_dwordx4 v[204:207], v248, s[16:19], s29 offen offset:128 nt\n\t"
"ds_write_b64 v250, a[140:141] offset:0\n\t"
"v_mfma_f32_16x16x32_bf16 v[58:61], a[236:239], a[200:203], 0\n\t"
"v_mfma_f32_16x16x32_bf16 v[58:61], a[232:235], a[196:199], v[58:61]\n\t"
"v_mfma_f32_16x16x32_bf16 v[58:61], a[228:231], a[192:195], v[58:61]\n\t"
"ds_write_b128 v249, a[72:75] offset:0\n\t"
"v_mfma_f32_16x16x32_bf16 v[62:65], a[228:231], a[196:199], 0\n\t"
"buffer_load_dwordx4 a[228:231], off, s[80:83], s20 offset:2048 sc1\n\t"
"v_mfma_f32_16x16x32_bf16 v[62:65], a[232:235], a[200:203], v[62:65]\n\t"
"s_waitcnt lgkmcnt(6)\n\t"
"s_mov_b32 s20, 122880\n\t"
"v_mfma_f32_16x16x32_bf16 v[62:65], a[236:239], a[204:207], v[62:65]\n\t"
"s_waitcnt lgkmcnt(4)\n\t"
"ds_write_b128 v249, a[88:91] offset:12288\n\t"
"v_mfma_f32_16x16x32_bf16 v[236:239], v[236:239], a[8:11], 0\n\t"
"buffer_load_dwordx4 a[232:235], off, s[80:83], s20 offset:0 sc1\n\t"
"v_mfma_f32_16x16x32_bf16 v[228:231], v[228:231], a[8:11], 0\n\t"
"s_waitcnt lgkmcnt(3)\n\t"
"v_mfma_f32_16x16x32_bf16 v[224:227], v[224:227], a[8:11], 0\n\t"
"ds_write_b64 v250, a[156:157] offset:12288\n\t"
"v_mfma_f32_16x16x32_bf16 v[232:235], v[232:235], a[8:11], 0\n\t"
"buffer_load_dwordx4 a[236:239], off, s[80:83], s20 offset:1024 sc1\n\t"
"v_mfma_f32_16x16x32_bf16 v[66:69], a[16:19], a[168:171], 0\n\t"
"ds_read_b128 a[168:171], v244 offset:33792\n\t"
"v_mfma_f32_16x16x32_bf16 v[66:69], a[20:23], a[176:179], v[66:69]\n\t"
"v_cvt_pk_bf16_f32 v236, v236, v237\n\t"
"v_cvt_pk_bf16_f32 v237, v238, v239\n\t"
"v_mfma_f32_16x16x32_bf16 v[66:69], a[144:147], a[172:175], v[66:69]\n\t"
"ds_write_b128 v249, a[80:83] offset:6144\n\t"
"v_mfma_f32_16x16x32_bf16 v[70:73], a[16:19], a[172:175], 0\n\t"
"v_cvt_pk_bf16_f32 v238, v228, v229\n\t"
"v_cvt_pk_bf16_f32 v239, v230, v231\n\t"
"v_mfma_f32_16x16x32_bf16 v[70:73], a[144:147], a[176:179], v[70:73]\n\t"
"ds_read_b128 a[172:175], v244 offset:34304\n\t"
"v_mfma_f32_16x16x32_bf16 v[70:73], a[20:23], a[180:183], v[70:73]\n\t"
"buffer_store_dwordx4 a[0:3], v248, s[16:19], s29 offen offset:256 nt\n\t"
"ds_write_b64 v250, a[148:149] offset:6144\n\t"
"v_cvt_pk_bf16_f32 v224, v224, v225\n\t"
"v_mfma_f32_16x16x32_bf16 v[74:77], a[20:23], a[184:187], 0\n\t"
"v_cvt_pk_bf16_f32 v225, v226, v227\n\t"
"v_cvt_pk_bf16_f32 v226, v232, v233\n\t"
"v_mfma_f32_16x16x32_bf16 v[74:77], a[144:147], a[180:183], v[74:77]\n\t"
"v_cvt_pk_bf16_f32 v227, v234, v235\n\t"
"v_mfma_f32_16x16x32_bf16 v[74:77], a[16:19], a[176:179], v[74:77]\n\t"
"ds_read_b128 a[176:179], v244 offset:34816\n\t"
"v_mfma_f32_16x16x32_bf16 v[78:81], a[16:19], a[180:183], 0\n\t"
"ds_read_b128 a[180:183], v244 offset:35328\n\t"
"v_mfma_f32_16x16x32_bf16 v[78:81], a[144:147], a[184:187], v[78:81]\n\t"
"v_mfma_f32_16x16x32_bf16 v[78:81], a[20:23], a[188:191], v[78:81]\n\t"
"ds_write_b128 v249, a[96:99] offset:18432\n\t"
"v_mfma_f32_16x16x32_bf16 v[82:85], a[20:23], a[192:195], 0\n\t"
"v_mfma_f32_16x16x32_bf16 v[82:85], a[144:147], a[188:191], v[82:85]\n\t"
"v_mfma_f32_16x16x32_bf16 v[82:85], a[16:19], a[184:187], v[82:85]\n\t"
"ds_read_b128 a[184:187], v244 offset:35840\n\t"
"v_mfma_f32_16x16x32_bf16 v[86:89], a[16:19], a[188:191], 0\n\t"
"ds_read_b128 a[188:191], v244 offset:36352\n\t"
"v_mfma_f32_16x16x32_bf16 v[86:89], a[144:147], a[192:195], v[86:89]\n\t"
"v_mfma_f32_16x16x32_bf16 v[86:89], a[20:23], a[196:199], v[86:89]\n\t"
"ds_write_b64 v250, a[164:165] offset:18432\n\t"
"v_mfma_f32_16x16x32_bf16 v[90:93], a[20:23], a[200:203], 0\n\t"
"v_mfma_f32_16x16x32_bf16 v[90:93], a[144:147], a[196:199], v[90:93]\n\t"
"v_mfma_f32_16x16x32_bf16 v[90:93], a[16:19], a[192:195], v[90:93]\n\t"
"ds_read_b128 a[192:195], v244 offset:36864\n\t"
"v_mfma_f32_16x16x32_bf16 v[94:97], a[16:19], a[196:199], 0\n\t"
"s_waitcnt lgkmcnt(6)\n\t"
"ds_read_b128 a[196:199], v244 offset:37376\n\t"
"v_mfma_f32_16x16x32_bf16 v[94:97], a[144:147], a[200:203], v[94:97]\n\t"
"ds_read_b128 a[200:203], v244 offset:37888\n\t"
"v_mfma_f32_16x16x32_bf16 v[94:97], a[20:23], a[204:207], v[94:97]\n\t"
"ds_read_b128 a[204:207], v244 offset:38400\n\t"
"v_mfma_f32_16x16x32_bf16 v[100:103], a[240:243], a[168:171], 0\n\t"
"s_waitcnt lgkmcnt(8)\n\t"
"v_mfma_f32_16x16x32_bf16 v[100:103], a[248:251], a[176:179], v[100:103]\n\t"
"v_mfma_f32_16x16x32_bf16 v[100:103], a[244:247], a[172:175], v[100:103]\n\t"
"v_mfma_f32_16x16x32_bf16 v[104:107], a[240:243], a[172:175], 0\n\t"
"s_waitcnt lgkmcnt(6)\n\t"
"v_mfma_f32_16x16x32_bf16 v[104:107], a[244:247], a[176:179], v[104:107]\n\t"
"v_mfma_f32_16x16x32_bf16 v[104:107], a[248:251], a[180:183], v[104:107]\n\t"
"s_waitcnt lgkmcnt(0)\n\t"
"s_barrier\n\t"
"v_mfma_f32_16x16x32_bf16 v[108:111], a[248:251], a[184:187], 0\n\t"
"ds_read_b128 v[196:199], v252 offset:0\n\t"
"v_mfma_f32_16x16x32_bf16 v[108:111], a[244:247], a[180:183], v[108:111]\n\t"
"ds_read_b128 v[204:207], v252 offset:1024\n\t"
"v_mfma_f32_16x16x32_bf16 v[108:111], a[240:243], a[176:179], v[108:111]\n\t"
"ds_write_b128 v242, v[236:239] offset:24576\n\t"
"v_mfma_f32_16x16x32_bf16 v[112:115], a[240:243], a[180:183], 0\n\t"
"s_add_u32 s29, s23, 786432\n\t"
"v_mfma_f32_16x16x32_bf16 v[112:115], a[244:247], a[184:187], v[112:115]\n\t"
"ds_read_b64_tr_b16 v[228:229], v240 offset:40960\n\t"
"v_mfma_f32_16x16x32_bf16 v[112:115], a[248:251], a[188:191], v[112:115]\n\t"
"ds_write_b128 v242, v[224:227] offset:26112\n\t"
"v_mfma_f32_16x16x32_bf16 v[116:119], a[248:251], a[192:195], 0\n\t"
"s_waitcnt lgkmcnt(4)\n\t"
"v_mfma_f32_16x16x32_bf16 v[116:119], a[244:247], a[188:191], v[116:119]\n\t"
"ds_read_b64_tr_b16 v[230:231], v240 offset:42496\n\t"
"v_mfma_f32_16x16x32_bf16 v[116:119], a[240:243], a[184:187], v[116:119]\n\t"
"ds_read_b128 v[212:215], v252 offset:2048\n\t"
"v_mfma_f32_16x16x32_bf16 v[120:123], a[240:243], a[188:191], 0\n\t"
"s_waitcnt lgkmcnt(5)\n\t"
"ds_read_b64_tr_b16 v[232:233], v241 offset:40960\n\t"
"v_mfma_f32_16x16x32_bf16 v[120:123], a[244:247], a[192:195], v[120:123]\n\t"
"ds_read_b64_tr_b16 v[234:235], v241 offset:42496\n\t"
"v_add_u32 v240, 59904, v240\n\t"
"v_mfma_f32_16x16x32_bf16 v[120:123], a[248:251], a[196:199], v[120:123]\n\t"
"ds_read_b128 a[0:3], v252 offset:12288\n\t"
"v_add_u32 v241, 59904, v241\n\t"
"v_mfma_f32_16x16x32_bf16 v[124:127], a[244:247], a[196:199], 0\n\t"
"s_waitcnt lgkmcnt(4)\n\t"
"ds_read_b64_tr_b16 v[236:237], v240 offset:1024\n\t"
"v_mfma_f32_16x16x32_bf16 v[124:127], a[240:243], a[192:195], v[124:127]\n\t"
"s_waitcnt lgkmcnt(4)\n\t"
"ds_read_b64_tr_b16 v[238:239], v240 offset:2560\n\t"
"v_mfma_f32_16x16x32_bf16 v[124:127], a[248:251], a[200:203], v[124:127]\n\t"
"ds_read_b64_tr_b16 v[224:225], v241 offset:1024\n\t"
"ds_read_b64_tr_b16 v[226:227], v241 offset:2560\n\t"
"v_mfma_f32_16x16x32_bf16 v[228:231], v[228:231], a[8:11], 0\n\t"
"s_waitcnt lgkmcnt(5)\n\t"
"buffer_store_dwordx4 v[196:199], v248, s[16:19], s29 offen offset:0 nt\n\t"
"v_mfma_f32_16x16x32_bf16 v[232:235], v[232:235], a[8:11], 0\n\t"
"s_waitcnt lgkmcnt(2)\n\t"
"v_mfma_f32_16x16x32_bf16 v[236:239], v[236:239], a[8:11], 0\n\t"
"s_waitcnt lgkmcnt(0)\n\t"
"v_mfma_f32_16x16x32_bf16 v[224:227], v[224:227], a[8:11], 0\n\t"
"buffer_store_dwordx4 v[204:207], v248, s[16:19], s29 offen offset:128 nt\n\t"
"v_mfma_f32_16x16x32_bf16 v[128:131], a[244:247], a[200:203], 0\n\t"
"s_waitcnt vmcnt(42)\n\t"
"v_mfma_f32_16x16x32_bf16 v[128:131], a[240:243], a[196:199], v[128:131]\n\t"
"buffer_load_dwordx4 a[244:247], off, s[80:83], s20 offset:2048 sc1\n\t"
"v_mfma_f32_16x16x32_bf16 v[128:131], a[248:251], a[204:207], v[128:131]\n\t"
"buffer_store_dwordx4 v[212:215], v248, s[16:19], s29 offen offset:256 nt\n\t"
"v_cvt_pk_bf16_f32 v228, v228, v229\n\t"
"v_cvt_pk_bf16_f32 v229, v230, v231\n\t"
"v_cvt_pk_bf16_f32 v230, v232, v233\n\t"
"v_cvt_pk_bf16_f32 v231, v234, v235\n\t"
"s_mov_b32 s20, 135168\n\t"
"v_mfma_f32_16x16x32_bf16 v[132:135], a[252:255], a[168:171], 0\n\t"
"s_add_u32 s29, s23, 1179648\n\t"
"ds_read_b64_tr_b16 v[232:233], v240 offset:20992\n\t"
"v_cvt_pk_bf16_f32 v236, v236, v237\n\t"
"v_mfma_f32_16x16x32_bf16 v[132:135], a[152:155], a[176:179], v[132:135]\n\t"
"ds_read_b64_tr_b16 v[234:235], v240 offset:22528\n\t"
"v_cvt_pk_bf16_f32 v237, v238, v239\n\t"
"v_mfma_f32_16x16x32_bf16 v[132:135], a[24:27], a[172:175], v[132:135]\n\t"
"ds_write_b128 v242, v[228:231] offset:27648\n\t"
"v_mfma_f32_16x16x32_bf16 v[136:139], a[252:255], a[172:175], 0\n\t"
"buffer_load_dwordx4 a[240:243], off, s[80:83], s20 offset:0 sc1\n\t"
"v_mfma_f32_16x16x32_bf16 v[136:139], a[24:27], a[176:179], v[136:139]\n\t"
"buffer_load_dwordx4 a[248:251], off, s[80:83], s20 offset:1024 sc1\n\t"
"v_mfma_f32_16x16x32_bf16 v[136:139], a[152:155], a[180:183], v[136:139]\n\t"
"buffer_store_dwordx4 a[0:3], v248, s[16:19], s29 offen offset:0 nt\n\t"
"ds_read_b128 v[196:199], v252 offset:13312\n\t"
"v_cvt_pk_bf16_f32 v238, v224, v225\n\t"
"v_cvt_pk_bf16_f32 v239, v226, v227\n\t"
"v_mfma_f32_16x16x32_bf16 v[140:143], a[152:155], a[184:187], 0\n\t"
"ds_read_b64_tr_b16 v[224:225], v241 offset:20992\n\t"
"v_mfma_f32_16x16x32_bf16 v[140:143], a[24:27], a[180:183], v[140:143]\n\t"
"ds_read_b64_tr_b16 v[226:227], v241 offset:22528\n\t"
"v_mfma_f32_16x16x32_bf16 v[140:143], a[252:255], a[176:179], v[140:143]\n\t"
"ds_write_b128 v242, v[236:239] offset:29184\n\t"
"v_mfma_f32_16x16x32_bf16 v[144:147], a[252:255], a[180:183], 0\n\t"
"v_mfma_f32_16x16x32_bf16 v[144:147], a[24:27], a[184:187], v[144:147]\n\t"
"ds_read_b64_tr_b16 v[228:229], v240 offset:40960\n\t"
"v_mfma_f32_16x16x32_bf16 v[144:147], a[152:155], a[188:191], v[144:147]\n\t"
"ds_read_b64_tr_b16 v[230:231], v240 offset:42496\n\t"
"v_mfma_f32_16x16x32_bf16 v[148:151], a[252:255], a[184:187], 0\n\t"
"s_waitcnt lgkmcnt(7)\n\t"
"ds_read_b64_tr_b16 v[236:237], v241 offset:40960\n\t"
"v_mfma_f32_16x16x32_bf16 v[148:151], a[152:155], a[192:195], v[148:151]\n\t"
"s_waitcnt lgkmcnt(4)\n\t"
"ds_read_b64_tr_b16 v[238:239], v241 offset:42496\n\t"
"v_mfma_f32_16x16x32_bf16 v[148:151], a[24:27], a[188:191], v[148:151]\n\t"
"v_mfma_f32_16x16x32_bf16 v[232:235], v[232:235], a[8:11], 0\n\t"
"ds_read_b128 v[204:207], v252 offset:14336\n\t"
"v_mfma_f32_16x16x32_bf16 v[224:227], v[224:227], a[8:11], 0\n\t"
"s_waitcnt lgkmcnt(3)\n\t"
"v_mfma_f32_16x16x32_bf16 v[228:231], v[228:231], a[8:11], 0\n\t"
"s_waitcnt lgkmcnt(1)\n\t"
"v_mfma_f32_16x16x32_bf16 v[236:239], v[236:239], a[8:11], 0\n\t"
"v_mfma_f32_16x16x32_bf16 v[152:155], a[252:255], a[188:191], 0\n\t"
"v_mfma_f32_16x16x32_bf16 v[152:155], a[152:155], a[196:199], v[152:155]\n\t"
"v_cvt_pk_bf16_f32 v232, v232, v233\n\t"
"v_cvt_pk_bf16_f32 v233, v234, v235\n\t"
"v_mfma_f32_16x16x32_bf16 v[152:155], a[24:27], a[192:195], v[152:155]\n\t"
"v_cvt_pk_bf16_f32 v234, v224, v225\n\t"
"v_cvt_pk_bf16_f32 v235, v226, v227\n\t"
"v_mfma_f32_16x16x32_bf16 v[156:159], a[252:255], a[192:195], 0\n\t"
"v_cvt_pk_bf16_f32 v228, v228, v229\n\t"
"v_cvt_pk_bf16_f32 v229, v230, v231\n\t"
"v_mfma_f32_16x16x32_bf16 v[156:159], a[24:27], a[196:199], v[156:159]\n\t"
"v_cvt_pk_bf16_f32 v230, v236, v237\n\t"
"v_cvt_pk_bf16_f32 v231, v238, v239\n\t"
"v_mfma_f32_16x16x32_bf16 v[156:159], a[152:155], a[200:203], v[156:159]\n\t"
"ds_write_b128 v242, v[232:235] offset:30720\n\t"
"v_mfma_f32_16x16x32_bf16 v[160:163], a[24:27], a[200:203], 0\n\t"
"s_waitcnt vmcnt(43)\n\t"
"v_mfma_f32_16x16x32_bf16 v[160:163], a[252:255], a[196:199], v[160:163]\n\t"
"buffer_load_dwordx4 a[252:255], off, s[80:83], s20 offset:2048 sc1\n\t"
"v_mfma_f32_16x16x32_bf16 v[160:163], a[152:155], a[204:207], v[160:163]\n\t"
"buffer_store_dwordx4 v[196:199], v248, s[16:19], s29 offen offset:128 nt\n\t"
"s_mov_b32 s20, 147456\n\t"
"v_mfma_f32_16x16x32_bf16 v[164:167], a[28:31], a[168:171], 0\n\t"
"v_mfma_f32_16x16x32_bf16 v[164:167], v[200:203], a[176:179], v[164:167]\n\t"
"v_mfma_f32_16x16x32_bf16 v[164:167], a[104:107], a[172:175], v[164:167]\n\t"
"s_waitcnt lgkmcnt(1)\n\t"
"ds_write_b128 v242, v[228:231] offset:32256\n\t"
"v_mfma_f32_16x16x32_bf16 v[168:171], a[28:31], a[172:175], 0\n\t"
"buffer_load_dwordx4 v[240:243], off, s[80:83], s20 offset:0 sc1\n\t"
"v_mfma_f32_16x16x32_bf16 v[168:171], a[104:107], a[176:179], v[168:171]\n\t"
"v_mfma_f32_16x16x32_bf16 v[168:171], v[200:203], a[180:183], v[168:171]\n\t"
"buffer_store_dwordx4 v[204:207], v248, s[16:19], s29 offen offset:256 nt\n\t"
"v_mfma_f32_16x16x32_bf16 v[172:175], v[200:203], a[184:187], 0\n\t"
"v_mfma_f32_16x16x32_bf16 v[172:175], a[104:107], a[180:183], v[172:175]\n\t"
"v_mfma_f32_16x16x32_bf16 v[172:175], a[28:31], a[176:179], v[172:175]\n\t"
"ds_read_b64_tr_b16 v[224:225], v246 offset:512\n\t"
"v_mfma_f32_16x16x32_bf16 v[176:179], a[28:31], a[180:183], 0\n\t"
"ds_read_b64_tr_b16 v[236:237], v255 offset:512\n\t"
"v_mfma_f32_16x16x32_bf16 v[176:179], a[104:107], a[184:187], v[176:179]\n\t"
"ds_read_b64_tr_b16 v[232:233], v246 offset:6656\n\t"
"v_mfma_f32_16x16x32_bf16 v[176:179], v[200:203], a[188:191], v[176:179]\n\t"
"s_waitcnt lgkmcnt(3)\n\t"
"s_barrier\n\t"
"v_mfma_f32_16x16x32_bf16 v[180:183], v[200:203], a[192:195], 0\n\t"
"ds_read_b128 a[168:171], v244 offset:24576\n\t"
"v_mfma_f32_16x16x32_bf16 v[180:183], a[104:107], a[188:191], v[180:183]\n\t"
"ds_read_b128 a[172:175], v244 offset:25088\n\t"
"v_mfma_f32_16x16x32_bf16 v[180:183], a[28:31], a[184:187], v[180:183]\n\t"
"ds_read_b128 a[176:179], v244 offset:25600\n\t"
"v_mfma_f32_16x16x32_bf16 v[184:187], a[28:31], a[188:191], 0\n\t"
"ds_read_b128 a[180:183], v244 offset:26112\n\t"
"v_mfma_f32_16x16x32_bf16 v[184:187], a[104:107], a[192:195], v[184:187]\n\t"
"ds_read_b64_tr_b16 v[228:229], v255 offset:6656\n\t"
"v_mfma_f32_16x16x32_bf16 v[184:187], v[200:203], a[196:199], v[184:187]\n\t"
"ds_read_b128 a[184:187], v244 offset:26624\n\t"
"v_mfma_f32_16x16x32_bf16 v[188:191], v[200:203], a[200:203], 0\n\t"
"ds_read_b128 a[188:191], v244 offset:27136\n\t"
"v_mfma_f32_16x16x32_bf16 v[188:191], a[104:107], a[196:199], v[188:191]\n\t"
"v_mfma_f32_16x16x32_bf16 v[188:191], a[28:31], a[192:195], v[188:191]\n\t"
"ds_read_b128 a[192:195], v244 offset:27648\n\t"
"v_mfma_f32_16x16x32_bf16 v[192:195], a[28:31], a[196:199], 0\n\t"
"s_waitcnt lgkmcnt(10)\n\t"
"ds_read_b128 a[196:199], v244 offset:28160\n\t"
"v_mfma_f32_16x16x32_bf16 v[192:195], a[104:107], a[200:203], v[192:195]\n\t"
"s_waitcnt lgkmcnt(10)\n\t"
"ds_read_b128 a[200:203], v244 offset:28672\n\t"
"v_mfma_f32_16x16x32_bf16 v[192:195], v[200:203], a[204:207], v[192:195]\n\t"
"s_waitcnt lgkmcnt(10)\n\t"
"ds_write_b128 v249, a[44:47] offset:0\n\t"
"v_mfma_f32_4x4x4_16b_bf16 v[224:227], v[224:225], v[220:221], 0\n\t"
"s_waitcnt lgkmcnt(6)\n\t"
"v_mfma_f32_4x4x4_16b_bf16 v[236:239], v[236:237], v[220:221], 0\n\t"
"s_waitcnt vmcnt(41)\n\t"
"v_mfma_f32_4x4x4_16b_bf16 v[232:235], v[232:233], v[220:221], 0\n\t"
"v_mfma_f32_4x4x4_16b_bf16 v[228:231], v[228:229], v[220:221], 0\n\t"
"ds_write_b128 v249, a[60:63] offset:12288\n\t"
"Lst19_%=:\n\t"
"v_mfma_f32_16x16x32_bf16 v[2:5], a[32:35], a[168:171], v[2:5]\n\t"
"v_cvt_pk_bf16_f32 v224, v224, v225\n\t"
"v_cvt_pk_bf16_f32 v225, v226, v227\n\t"
"v_mfma_f32_16x16x32_bf16 v[2:5], a[36:39], a[176:179], v[2:5]\n\t"
"ds_read_b128 a[204:207], v244 offset:29184\n\t"
"v_mfma_f32_16x16x32_bf16 v[2:5], a[160:163], a[172:175], v[2:5]\n\t"
"ds_write_b64 v250, a[110:111] offset:0\n\t"
"v_mfma_f32_16x16x32_bf16 v[6:9], a[32:35], a[172:175], v[6:9]\n\t"
"s_waitcnt lgkmcnt(8)\n\t"
"v_cvt_pk_bf16_f32 v226, v236, v237\n\t"
"v_cvt_pk_bf16_f32 v227, v238, v239\n\t"
"v_mfma_f32_16x16x32_bf16 v[6:9], a[160:163], a[176:179], v[6:9]\n\t"
"v_cvt_pk_bf16_f32 v232, v232, v233\n\t"
"v_cvt_pk_bf16_f32 v233, v234, v235\n\t"
"v_mfma_f32_16x16x32_bf16 v[6:9], a[36:39], a[180:183], v[6:9]\n\t"
"ds_write_b128 v245, v[224:227] offset:512\n\t"
"v_cvt_pk_bf16_f32 v234, v228, v229\n\t"
"v_cvt_pk_bf16_f32 v235, v230, v231\n\t"
"v_mfma_f32_16x16x32_bf16 v[10:13], a[36:39], a[184:187], v[10:13]\n\t"
"s_waitcnt lgkmcnt(8)\n\t"
"v_mfma_f32_16x16x32_bf16 v[10:13], a[160:163], a[180:183], v[10:13]\n\t"
"v_mfma_f32_16x16x32_bf16 v[10:13], a[32:35], a[176:179], v[10:13]\n\t"
"ds_write_b64 v250, a[126:127] offset:12288\n\t"
"v_mfma_f32_16x16x32_bf16 v[14:17], a[32:35], a[180:183], v[14:17]\n\t"
"s_waitcnt lgkmcnt(8)\n\t"
"ds_read_b64_tr_b16 v[236:237], v246 offset:12800\n\t"
"v_mfma_f32_16x16x32_bf16 v[14:17], a[160:163], a[184:187], v[14:17]\n\t"
"ds_read_b64_tr_b16 v[228:229], v255 offset:12800\n\t"
"v_mfma_f32_16x16x32_bf16 v[14:17], a[36:39], a[188:191], v[14:17]\n\t"
"ds_write_b128 v245, v[232:235] offset:6656\n\t"
"v_mfma_f32_16x16x32_bf16 v[18:21], a[36:39], a[192:195], v[18:21]\n\t"
"s_waitcnt lgkmcnt(10)\n\t"
"v_mfma_f32_16x16x32_bf16 v[18:21], a[160:163], a[188:191], v[18:21]\n\t"
"ds_read_b64_tr_b16 v[224:225], v246 offset:20480\n\t"
"v_mfma_f32_16x16x32_bf16 v[18:21], a[32:35], a[184:187], v[18:21]\n\t"
"ds_read_b64_tr_b16 v[232:233], v255 offset:20480\n\t"
"v_mfma_f32_16x16x32_bf16 v[22:25], a[32:35], a[188:191], v[22:25]\n\t"
"s_waitcnt lgkmcnt(11)\n\t"
"v_mfma_f32_16x16x32_bf16 v[22:25], a[160:163], a[192:195], v[22:25]\n\t"
"v_mfma_f32_16x16x32_bf16 v[22:25], a[36:39], a[196:199], v[22:25]\n\t"
"ds_write_b128 v249, a[52:55] offset:6144\n\t"
"v_mfma_f32_16x16x32_bf16 v[26:29], a[36:39], a[200:203], v[26:29]\n\t"
"s_waitcnt lgkmcnt(9)\n\t"
"v_mfma_f32_16x16x32_bf16 v[26:29], a[160:163], a[196:199], v[26:29]\n\t"
"v_mfma_f32_16x16x32_bf16 v[26:29], a[32:35], a[192:195], v[26:29]\n\t"
"ds_write_b128 v249, a[68:71] offset:18432\n\t"
"v_mfma_f32_16x16x32_bf16 v[30:33], a[32:35], a[196:199], v[30:33]\n\t"
"s_waitcnt vmcnt(36) lgkmcnt(2)\n\t"
"v_mfma_f32_16x16x32_bf16 v[30:33], a[160:163], a[200:203], v[30:33]\n\t"
"v_mfma_f32_16x16x32_bf16 v[30:33], a[36:39], a[204:207], v[30:33]\n\t"
"ds_write_b64 v250, a[118:119] offset:6144\n\t"
"v_mfma_f32_4x4x4_16b_bf16 v[236:239], v[236:237], v[220:221], 0\n\t"
"v_mfma_f32_4x4x4_16b_bf16 v[228:231], v[228:229], v[220:221], 0\n\t"
"Lpg14_%=:\n\t"
"s_cbranch_vccnz Lvr1_%=\n\t"
"ds_write_b64 v250, a[134:135] offset:18432\n\t"
"v_mfma_f32_4x4x4_16b_bf16 v[224:227], v[224:225], v[220:221], 0\n\t"
"v_mfma_f32_4x4x4_16b_bf16 v[232:235], v[232:233], v[220:221], 0\n\t"
"v_mfma_f32_16x16x32_bf16 v[34:37], a[112:115], a[168:171], v[34:37]\n\t"
"v_cvt_pk_bf16_f32 v236, v236, v237\n\t"
"v_cvt_pk_bf16_f32 v237, v238, v239\n\t"
"v_mfma_f32_16x16x32_bf16 v[34:37], a[120:123], a[176:179], v[34:37]\n\t"
"v_cvt_pk_bf16_f32 v238, v228, v229\n\t"
"v_cvt_pk_bf16_f32 v239, v230, v231\n\t"
"v_mfma_f32_16x16x32_bf16 v[34:37], v[208:211], a[172:175], v[34:37]\n\t"
"ds_write_b128 v245, v[236:239] offset:12800\n\t"
"v_mfma_f32_16x16x32_bf16 v[38:41], a[112:115], a[172:175], v[38:41]\n\t"
"v_cvt_pk_bf16_f32 v224, v224, v225\n\t"
"v_cvt_pk_bf16_f32 v225, v226, v227\n\t"
"v_mfma_f32_16x16x32_bf16 v[38:41], v[208:211], a[176:179], v[38:41]\n\t"
"v_cvt_pk_bf16_f32 v226, v232, v233\n\t"
"v_cvt_pk_bf16_f32 v227, v234, v235\n\t"
"v_mfma_f32_16x16x32_bf16 v[38:41], a[120:123], a[180:183], v[38:41]\n\t"
"ds_write_b128 v245, v[224:227] offset:20480\n\t"
"v_mfma_f32_16x16x32_bf16 v[42:45], a[120:123], a[184:187], v[42:45]\n\t"
"v_mfma_f32_16x16x32_bf16 v[42:45], v[208:211], a[180:183], v[42:45]\n\t"
"v_mfma_f32_16x16x32_bf16 v[42:45], a[112:115], a[176:179], v[42:45]\n\t"
"ds_read_b64_tr_b16 v[228:229], v246 offset:26624\n\t"
"v_mfma_f32_16x16x32_bf16 v[46:49], a[112:115], a[180:183], v[46:49]\n\t"
"ds_read_b64_tr_b16 v[232:233], v255 offset:26624\n\t"
"v_mfma_f32_16x16x32_bf16 v[46:49], v[208:211], a[184:187], v[46:49]\n\t"
"ds_read_b64_tr_b16 v[236:237], v246 offset:32768\n\t"
"v_mfma_f32_16x16x32_bf16 v[46:49], a[120:123], a[188:191], v[46:49]\n\t"
"s_waitcnt lgkmcnt(5)\n\t"
"s_barrier\n\t"
"v_mfma_f32_16x16x32_bf16 v[50:53], a[120:123], a[192:195], v[50:53]\n\t"
"ds_read_b128 v[196:199], v252 offset:0\n\t"
"v_mfma_f32_16x16x32_bf16 v[50:53], v[208:211], a[188:191], v[50:53]\n\t"
"ds_read_b128 v[204:207], v252 offset:1024\n\t"
"v_mfma_f32_16x16x32_bf16 v[50:53], a[112:115], a[184:187], v[50:53]\n\t"
"ds_read_b64_tr_b16 v[224:225], v255 offset:32768\n\t"
"v_mfma_f32_16x16x32_bf16 v[54:57], a[112:115], a[188:191], v[54:57]\n\t"
"s_add_u32 s29, s23, 1152\n\t"
"ds_read_b128 v[212:215], v252 offset:2048\n\t"
"v_mfma_f32_16x16x32_bf16 v[54:57], v[208:211], a[192:195], v[54:57]\n\t"
"ds_read_b128 a[0:3], v252 offset:12288\n\t"
"v_mfma_f32_16x16x32_bf16 v[54:57], a[120:123], a[196:199], v[54:57]\n\t"
"ds_read_b128 a[8:11], v252 offset:13312\n\t"
"v_mfma_f32_16x16x32_bf16 v[58:61], a[120:123], a[200:203], v[58:61]\n\t"
"s_waitcnt lgkmcnt(5)\n\t"
"ds_read_b128 a[16:19], v252 offset:14336\n\t"
"v_mfma_f32_16x16x32_bf16 v[58:61], v[208:211], a[196:199], v[58:61]\n\t"
"v_mfma_f32_16x16x32_bf16 v[58:61], a[112:115], a[192:195], v[58:61]\n\t"
"v_mfma_f32_16x16x32_bf16 v[62:65], a[112:115], a[196:199], v[62:65]\n\t"
"s_waitcnt lgkmcnt(5)\n\t"
"v_mfma_f32_16x16x32_bf16 v[62:65], v[208:211], a[200:203], v[62:65]\n\t"
"v_mfma_f32_16x16x32_bf16 v[62:65], a[120:123], a[204:207], v[62:65]\n\t"
"buffer_store_dwordx4 v[196:199], v248, s[16:19], s29 offen offset:0 nt\n\t"
"v_mfma_f32_4x4x4_16b_bf16 v[228:231], v[228:229], v[220:221], 0\n\t"
"s_waitcnt lgkmcnt(4)\n\t"
"v_mfma_f32_4x4x4_16b_bf16 v[232:235], v[232:233], v[220:221], 0\n\t"
"s_waitcnt vmcnt(32)\n\t"
"v_mfma_f32_4x4x4_16b_bf16 v[236:239], v[236:237], v[220:221], 0\n\t"
"buffer_store_dwordx4 v[204:207], v248, s[16:19], s29 offen offset:128 nt\n\t"
"s_waitcnt lgkmcnt(3)\n\t"
"v_mfma_f32_4x4x4_16b_bf16 v[224:227], v[224:225], v[220:221], 0\n\t"
"s_waitcnt lgkmcnt(2)\n\t"
"v_mfma_f32_16x16x32_bf16 v[66:69], v[216:219], a[168:171], v[66:69]\n\t"
"ds_read_b128 a[168:171], v244 offset:33792\n\t"
"v_cvt_pk_bf16_f32 v228, v228, v229\n\t"
"v_mfma_f32_16x16x32_bf16 v[66:69], a[4:7], a[176:179], v[66:69]\n\t"
"v_cvt_pk_bf16_f32 v229, v230, v231\n\t"
"v_cvt_pk_bf16_f32 v230, v232, v233\n\t"
"v_mfma_f32_16x16x32_bf16 v[66:69], a[128:131], a[172:175], v[66:69]\n\t"
"s_waitcnt lgkmcnt(2)\n\t"
"v_cvt_pk_bf16_f32 v231, v234, v235\n\t"
"v_cvt_pk_bf16_f32 v236, v236, v237\n\t"
"v_mfma_f32_16x16x32_bf16 v[70:73], v[216:219], a[172:175], v[70:73]\n\t"
"ds_read_b128 a[172:175], v244 offset:34304\n\t"
"v_cvt_pk_bf16_f32 v237, v238, v239\n\t"
"v_mfma_f32_16x16x32_bf16 v[70:73], a[128:131], a[176:179], v[70:73]\n\t"
"v_cvt_pk_bf16_f32 v238, v224, v225\n\t"
"v_cvt_pk_bf16_f32 v239, v226, v227\n\t"
"v_mfma_f32_16x16x32_bf16 v[70:73], a[4:7], a[180:183], v[70:73]\n\t"
"buffer_store_dwordx4 v[212:215], v248, s[16:19], s29 offen offset:256 nt\n\t"
"s_add_u32 s29, s23, 394368\n\t"
"s_waitcnt lgkmcnt(2)\n\t"
"v_mfma_f32_16x16x32_bf16 v[74:77], a[4:7], a[184:187], v[74:77]\n\t"
"v_mfma_f32_16x16x32_bf16 v[74:77], a[128:131], a[180:183], v[74:77]\n\t"
"v_mfma_f32_16x16x32_bf16 v[74:77], v[216:219], a[176:179], v[74:77]\n\t"
"ds_write_b128 v245, v[228:231] offset:26624\n\t"
"v_mfma_f32_16x16x32_bf16 v[78:81], v[216:219], a[180:183], v[78:81]\n\t"
"v_mfma_f32_16x16x32_bf16 v[78:81], a[128:131], a[184:187], v[78:81]\n\t"
"ds_read_b128 a[176:179], v244 offset:34816\n\t"
"v_mfma_f32_16x16x32_bf16 v[78:81], a[4:7], a[188:191], v[78:81]\n\t"
"buffer_store_dwordx4 a[0:3], v248, s[16:19], s29 offen offset:0 nt\n\t"
"ds_read_b64_tr_b16 v[232:233], v246 offset:40448\n\t"
"ds_read_b64_tr_b16 v[224:225], v255 offset:40448\n\t"
"v_mfma_f32_16x16x32_bf16 v[82:85], a[4:7], a[192:195], v[82:85]\n\t"
"ds_read_b128 a[180:183], v244 offset:35328\n\t"
"v_mfma_f32_16x16x32_bf16 v[82:85], a[128:131], a[188:191], v[82:85]\n\t"
"ds_read_b64_tr_b16 v[228:229], v246 offset:46592\n\t"
"v_mfma_f32_16x16x32_bf16 v[82:85], v[216:219], a[184:187], v[82:85]\n\t"
"ds_write_b128 v245, v[236:239] offset:32768\n\t"
"buffer_store_dwordx4 a[8:11], v248, s[16:19], s29 offen offset:128 nt\n\t"
"v_mfma_f32_16x16x32_bf16 v[86:89], v[216:219], a[188:191], v[86:89]\n\t"
"ds_read_b64_tr_b16 v[236:237], v255 offset:46592\n\t"
"v_mfma_f32_16x16x32_bf16 v[86:89], a[128:131], a[192:195], v[86:89]\n\t"
"ds_read_b128 a[184:187], v244 offset:35840\n\t"
"v_mfma_f32_16x16x32_bf16 v[86:89], a[4:7], a[196:199], v[86:89]\n\t"
"buffer_store_dwordx4 a[16:19], v248, s[16:19], s29 offen offset:256 nt\n\t"
"ds_read_b128 a[188:191], v244 offset:36352\n\t"
"v_mfma_f32_16x16x32_bf16 v[90:93], a[4:7], a[200:203], v[90:93]\n\t"
"v_mfma_f32_16x16x32_bf16 v[90:93], a[128:131], a[196:199], v[90:93]\n\t"
"v_mfma_f32_16x16x32_bf16 v[90:93], v[216:219], a[192:195], v[90:93]\n\t"
"ds_read_b128 a[192:195], v244 offset:36864\n\t"
"v_mfma_f32_16x16x32_bf16 v[94:97], v[216:219], a[196:199], v[94:97]\n\t"
"s_waitcnt lgkmcnt(8)\n\t"
"ds_read_b128 a[196:199], v244 offset:37376\n\t"
"v_mfma_f32_16x16x32_bf16 v[94:97], a[128:131], a[200:203], v[94:97]\n\t"
"s_waitcnt lgkmcnt(8)\n\t"
"ds_read_b128 a[200:203], v244 offset:37888\n\t"
"v_mfma_f32_16x16x32_bf16 v[94:97], a[4:7], a[204:207], v[94:97]\n\t"
"s_waitcnt lgkmcnt(7)\n\t"
"ds_read_b128 a[204:207], v244 offset:38400\n\t"
"v_mfma_f32_4x4x4_16b_bf16 v[232:235], v[232:233], v[220:221], 0\n\t"
"s_waitcnt lgkmcnt(6)\n\t"
"v_mfma_f32_4x4x4_16b_bf16 v[224:227], v[224:225], v[220:221], 0\n\t"
"s_waitcnt vmcnt(34)\n\t"
"v_mfma_f32_4x4x4_16b_bf16 v[228:231], v[228:229], v[220:221], 0\n\t"
"v_mfma_f32_4x4x4_16b_bf16 v[236:239], v[236:237], v[220:221], 0\n\t"
"v_mfma_f32_16x16x32_bf16 v[100:103], a[208:211], a[168:171], v[100:103]\n\t"
"v_cvt_pk_bf16_f32 v232, v232, v233\n\t"
"v_cvt_pk_bf16_f32 v233, v234, v235\n\t"
"v_mfma_f32_16x16x32_bf16 v[100:103], a[48:51], a[176:179], v[100:103]\n\t"
"v_cvt_pk_bf16_f32 v234, v224, v225\n\t"
"v_cvt_pk_bf16_f32 v235, v226, v227\n\t"
"v_mfma_f32_16x16x32_bf16 v[100:103], a[40:43], a[172:175], v[100:103]\n\t"
"ds_write_b128 v245, v[232:235] offset:40448\n\t"
"v_mfma_f32_16x16x32_bf16 v[104:107], a[208:211], a[172:175], v[104:107]\n\t"
"s_waitcnt lgkmcnt(6)\n\t"
"v_cvt_pk_bf16_f32 v228, v228, v229\n\t"
"v_cvt_pk_bf16_f32 v229, v230, v231\n\t"
"v_mfma_f32_16x16x32_bf16 v[104:107], a[40:43], a[176:179], v[104:107]\n\t"
"v_cvt_pk_bf16_f32 v230, v236, v237\n\t"
"v_mfma_f32_16x16x32_bf16 v[104:107], a[48:51], a[180:183], v[104:107]\n\t"
"s_waitcnt lgkmcnt(1)\n\t"
"s_barrier\n\t"
"v_mfma_f32_16x16x32_bf16 v[108:111], a[48:51], a[184:187], v[108:111]\n\t"
"v_cvt_pk_bf16_f32 v231, v238, v239\n\t"
"v_mfma_f32_16x16x32_bf16 v[108:111], a[40:43], a[180:183], v[108:111]\n\t"
"v_fma_f32 v196, -v2, s56, -v100\n\t"
"v_fma_f32 v197, -v3, s56, -v101\n\t"
"v_mfma_f32_16x16x32_bf16 v[108:111], a[208:211], a[176:179], v[108:111]\n\t"
"ds_write_b128 v245, v[228:231] offset:46592\n\t"
"v_mfma_f32_16x16x32_bf16 v[112:115], a[208:211], a[180:183], v[112:115]\n\t"
"v_fma_f32 v198, -v4, s56, -v102\n\t"
"v_fma_f32 v199, -v5, s56, -v103\n\t"
"v_mfma_f32_16x16x32_bf16 v[112:115], a[40:43], a[184:187], v[112:115]\n\t"
"ds_read_b64_tr_b16 v[224:225], v246 offset:52736\n\t"
"v_mfma_f32_16x16x32_bf16 v[112:115], a[48:51], a[188:191], v[112:115]\n\t"
"ds_read_b64_tr_b16 v[236:237], v255 offset:52736\n\t"
"v_fma_f32 v204, -v6, s56, -v104\n\t"
"v_mfma_f32_16x16x32_bf16 v[116:119], a[48:51], a[192:195], v[116:119]\n\t"
"v_fma_f32 v205, -v7, s56, -v105\n\t"
"v_fma_f32 v206, -v8, s56, -v106\n\t"
"v_mfma_f32_16x16x32_bf16 v[116:119], a[40:43], a[188:191], v[116:119]\n\t"
"v_fma_f32 v207, -v9, s56, -v107\n\t"
"v_fma_f32 v212, -v10, s56, -v108\n\t"
"v_mfma_f32_16x16x32_bf16 v[116:119], a[208:211], a[184:187], v[116:119]\n\t"
"ds_write_b128 v249, a[76:79] offset:0\n\t"
"v_mfma_f32_16x16x32_bf16 v[120:123], a[208:211], a[188:191], v[120:123]\n\t"
"v_fma_f32 v213, -v11, s56, -v109\n\t"
"v_fma_f32 v214, -v12, s56, -v110\n\t"
"v_mfma_f32_16x16x32_bf16 v[120:123], a[40:43], a[192:195], v[120:123]\n\t"
"v_fma_f32 v215, -v13, s56, -v111\n\t"
"v_fma_f32 v98, -v14, s56, -v112\n\t"
"v_mfma_f32_16x16x32_bf16 v[120:123], a[48:51], a[196:199], v[120:123]\n\t"
"ds_write_b128 v249, a[92:95] offset:12288\n\t"
"v_mfma_f32_16x16x32_bf16 v[124:127], a[48:51], a[200:203], v[124:127]\n\t"
"v_fma_f32 v99, -v15, s56, -v113\n\t"
"v_accvgpr_write_b32 a0, v98\n\t"
"v_mfma_f32_16x16x32_bf16 v[124:127], a[40:43], a[196:199], v[124:127]\n\t"
"v_fma_f32 v98, -v16, s56, -v114\n\t"
"v_accvgpr_write_b32 a1, v99\n\t"
"v_mfma_f32_16x16x32_bf16 v[124:127], a[208:211], a[192:195], v[124:127]\n\t"
"ds_write_b64 v250, a[142:143] offset:0\n\t"
"v_mfma_f32_16x16x32_bf16 v[128:131], a[208:211], a[196:199], v[128:131]\n\t"
"buffer_load_dwordx4 a[208:211], off, s[80:83], s20 offset:1024 sc1\n\t"
"v_mfma_f32_16x16x32_bf16 v[128:131], a[40:43], a[200:203], v[128:131]\n\t"
"s_waitcnt lgkmcnt(3)\n\t"
"v_fma_f32 v99, -v17, s56, -v115\n\t"
"v_accvgpr_write_b32 a2, v98\n\t"
"v_mfma_f32_16x16x32_bf16 v[128:131], a[48:51], a[204:207], v[128:131]\n\t"
"s_waitcnt vmcnt(31)\n\t"
"ds_write_b64 v250, a[158:159] offset:12288\n\t"
"v_mfma_f32_4x4x4_16b_bf16 v[224:227], v[224:225], v[220:221], 0\n\t"
"v_mfma_f32_4x4x4_16b_bf16 v[236:239], v[236:237], v[220:221], 0\n\t"
"ds_write_b128 v249, a[84:87] offset:6144\n\t"
"v_mfma_f32_16x16x32_bf16 v[132:135], a[56:59], a[168:171], v[132:135]\n\t"
"v_accvgpr_write_b32 a3, v99\n\t"
"v_fma_f32 v98, -v18, s56, -v116\n\t"
"v_mfma_f32_16x16x32_bf16 v[132:135], a[136:139], a[176:179], v[132:135]\n\t"
"v_cvt_pk_bf16_f32 v224, v224, v225\n\t"
"v_cvt_pk_bf16_f32 v225, v226, v227\n\t"
"v_mfma_f32_16x16x32_bf16 v[132:135], a[64:67], a[172:175], v[132:135]\n\t"
"ds_write_b128 v249, a[100:103] offset:18432\n\t"
"v_mfma_f32_16x16x32_bf16 v[136:139], a[56:59], a[172:175], v[136:139]\n\t"
"v_cvt_pk_bf16_f32 v226, v236, v237\n\t"
"v_cvt_pk_bf16_f32 v227, v238, v239\n\t"
"v_mfma_f32_16x16x32_bf16 v[136:139], a[64:67], a[176:179], v[136:139]\n\t"
"v_fma_f32 v99, -v19, s56, -v117\n\t"
"v_accvgpr_write_b32 a8, v98\n\t"
"v_mfma_f32_16x16x32_bf16 v[136:139], a[136:139], a[180:183], v[136:139]\n\t"
"ds_write_b128 v245, v[224:227] offset:52736\n\t"
"v_mfma_f32_16x16x32_bf16 v[140:143], a[136:139], a[184:187], v[140:143]\n\t"
"buffer_load_dwordx4 v[236:239], off, s[80:83], s20 offset:2048 sc1\n\t"
"v_mfma_f32_16x16x32_bf16 v[140:143], a[64:67], a[180:183], v[140:143]\n\t"
"v_fma_f32 v98, -v20, s56, -v118\n\t"
"v_accvgpr_write_b32 a9, v99\n\t"
"v_mfma_f32_16x16x32_bf16 v[140:143], a[56:59], a[176:179], v[140:143]\n\t"
"ds_write_b64 v250, a[150:151] offset:6144\n\t"
"v_mfma_f32_16x16x32_bf16 v[144:147], a[56:59], a[180:183], v[144:147]\n\t"
"v_fma_f32 v99, -v21, s56, -v119\n\t"
"v_accvgpr_write_b32 a10, v98\n\t"
"v_mfma_f32_16x16x32_bf16 v[144:147], a[64:67], a[184:187], v[144:147]\n\t"
"v_accvgpr_write_b32 a11, v99\n\t"
"v_fma_f32 v98, -v22, s56, -v120\n\t"
"v_mfma_f32_16x16x32_bf16 v[144:147], a[136:139], a[188:191], v[144:147]\n\t"
"ds_write_b64 v250, a[166:167] offset:18432\n\t"
"v_mfma_f32_16x16x32_bf16 v[148:151], a[136:139], a[192:195], v[148:151]\n\t"
"v_fma_f32 v99, -v23, s56, -v121\n\t"
"v_accvgpr_write_b32 a16, v98\n\t"
"v_mfma_f32_16x16x32_bf16 v[148:151], a[64:67], a[188:191], v[148:151]\n\t"
"v_fma_f32 v98, -v24, s56, -v122\n\t"
"v_accvgpr_write_b32 a17, v99\n\t"
"v_mfma_f32_16x16x32_bf16 v[148:151], a[56:59], a[184:187], v[148:151]\n\t"
"v_fma_f32 v99, -v25, s56, -v123\n\t"
"v_accvgpr_write_b32 a18, v98\n\t"
"v_mfma_f32_16x16x32_bf16 v[152:155], a[56:59], a[188:191], v[152:155]\n\t"
"v_accvgpr_write_b32 a19, v99\n\t"
"v_fma_f32 v98, -v26, s56, -v124\n\t"
"v_mfma_f32_16x16x32_bf16 v[152:155], a[64:67], a[192:195], v[152:155]\n\t"
"v_fma_f32 v99, -v27, s56, -v125\n\t"
"v_accvgpr_write_b32 a24, v98\n\t"
"v_mfma_f32_16x16x32_bf16 v[152:155], a[136:139], a[196:199], v[152:155]\n\t"
"v_fma_f32 v98, -v28, s56, -v126\n\t"
"v_accvgpr_write_b32 a25, v99\n\t"
"v_mfma_f32_16x16x32_bf16 v[156:159], a[136:139], a[200:203], v[156:159]\n\t"
"v_fma_f32 v99, -v29, s56, -v127\n\t"
"v_accvgpr_write_b32 a26, v98\n\t"
"v_mfma_f32_16x16x32_bf16 v[156:159], a[64:67], a[196:199], v[156:159]\n\t"
"v_accvgpr_write_b32 a27, v99\n\t"
"v_fma_f32 v98, -v30, s56, -v128\n\t"
"v_mfma_f32_16x16x32_bf16 v[156:159], a[56:59], a[192:195], v[156:159]\n\t"
"v_fma_f32 v99, -v31, s56, -v129\n\t"
"v_accvgpr_write_b32 a32, v98\n\t"
"v_mfma_f32_16x16x32_bf16 v[160:163], a[56:59], a[196:199], v[160:163]\n\t"
"s_waitcnt vmcnt(27)\n\t"
"v_fma_f32 v98, -v32, s56, -v130\n\t"
"v_accvgpr_write_b32 a33, v99\n\t"
"v_mfma_f32_16x16x32_bf16 v[160:163], a[64:67], a[200:203], v[160:163]\n\t"
"v_fma_f32 v99, -v33, s56, -v131\n\t"
"v_accvgpr_write_b32 a34, v98\n\t"
"v_mfma_f32_16x16x32_bf16 v[160:163], a[136:139], a[204:207], v[160:163]\n\t"
"v_accvgpr_write_b32 a35, v99\n\t"
"v_fma_f32 v98, -v34, s56, -v132\n\t"
"v_mfma_f32_16x16x32_bf16 v[164:167], a[12:15], a[168:171], v[164:167]\n\t"
"v_fma_f32 v99, -v35, s56, -v133\n\t"
"v_accvgpr_write_b32 a40, v98\n\t"
"v_mfma_f32_16x16x32_bf16 v[164:167], a[216:219], a[176:179], v[164:167]\n\t"
"v_fma_f32 v98, -v36, s56, -v134\n\t"
"v_mfma_f32_16x16x32_bf16 v[164:167], a[212:215], a[172:175], v[164:167]\n\t"
"s_waitcnt lgkmcnt(0)\n\t"
"s_barrier\n\t"
"v_mfma_f32_16x16x32_bf16 v[168:171], a[12:15], a[172:175], v[168:171]\n\t"
"ds_read_b128 a[80:83], v252 offset:0\n\t"
"v_mfma_f32_16x16x32_bf16 v[168:171], a[212:215], a[176:179], v[168:171]\n\t"
"ds_read_b128 a[88:91], v252 offset:1024\n\t"
"v_accvgpr_write_b32 a41, v99\n\t"
"v_mfma_f32_16x16x32_bf16 v[168:171], a[216:219], a[180:183], v[168:171]\n\t"
"ds_read_b128 a[96:99], v252 offset:2048\n\t"
"v_fma_f32 v99, -v37, s56, -v135\n\t"
"v_mfma_f32_16x16x32_bf16 v[172:175], a[216:219], a[184:187], v[172:175]\n\t"
"s_add_u32 s29, s23, 787584\n\t"
"ds_read_b128 a[168:171], v222 offset:512\n\t"
"v_accvgpr_write_b32 a42, v98\n\t"
"v_mfma_f32_16x16x32_bf16 v[172:175], a[212:215], a[180:183], v[172:175]\n\t"
"ds_read_b128 a[172:175], v223 offset:6656\n\t"
"v_accvgpr_write_b32 a43, v99\n\t"
"v_mfma_f32_16x16x32_bf16 v[172:175], a[12:15], a[176:179], v[172:175]\n\t"
"ds_read_b128 a[176:179], v223 offset:12800\n\t"
"v_fma_f32 v98, -v38, s56, -v136\n\t"
"v_mfma_f32_16x16x32_bf16 v[176:179], a[12:15], a[180:183], v[176:179]\n\t"
"s_waitcnt lgkmcnt(5)\n\t"
"ds_read_b128 a[104:107], v252 offset:12288\n\t"
"v_fma_f32 v99, -v39, s56, -v137\n\t"
"v_mfma_f32_16x16x32_bf16 v[176:179], a[212:215], a[184:187], v[176:179]\n\t"
"ds_read_b128 a[180:183], v222 offset:20480\n\t"
"v_accvgpr_write_b32 a48, v98\n\t"
"v_mfma_f32_16x16x32_bf16 v[176:179], a[216:219], a[188:191], v[176:179]\n\t"
"ds_read_b128 a[112:115], v252 offset:13312\n\t"
"v_fma_f32 v98, -v40, s56, -v138\n\t"
"v_mfma_f32_16x16x32_bf16 v[180:183], a[216:219], a[192:195], v[180:183]\n\t"
"s_waitcnt lgkmcnt(7)\n\t"
"ds_read_b128 a[120:123], v252 offset:14336\n\t"
"v_accvgpr_write_b32 a49, v99\n\t"
"v_mfma_f32_16x16x32_bf16 v[180:183], a[212:215], a[188:191], v[180:183]\n\t"
"v_fma_f32 v99, -v41, s56, -v139\n\t"
"v_accvgpr_write_b32 a50, v98\n\t"
"v_mfma_f32_16x16x32_bf16 v[180:183], a[12:15], a[184:187], v[180:183]\n\t"
"ds_read_b128 a[184:187], v223 offset:26624\n\t"
"v_accvgpr_write_b32 a51, v99\n\t"
"v_mfma_f32_16x16x32_bf16 v[184:187], a[12:15], a[188:191], v[184:187]\n\t"
"s_waitcnt lgkmcnt(8)\n\t"
"ds_read_b128 a[188:191], v223 offset:32768\n\t"
"v_fma_f32 v98, -v42, s56, -v140\n\t"
"v_mfma_f32_16x16x32_bf16 v[184:187], a[212:215], a[192:195], v[184:187]\n\t"
"s_mov_b32 s20, 159744\n\t"
"v_fma_f32 v99, -v43, s56, -v141\n\t"
"v_accvgpr_write_b32 a56, v98\n\t"
"v_mfma_f32_16x16x32_bf16 v[184:187], a[216:219], a[196:199], v[184:187]\n\t"
"v_fma_f32 v98, -v44, s56, -v142\n\t"
"v_accvgpr_write_b32 a57, v99\n\t"
"v_mfma_f32_16x16x32_bf16 v[188:191], a[216:219], a[200:203], v[188:191]\n\t"
"s_waitcnt lgkmcnt(5)\n\t"
"v_fma_f32 v99, -v45, s56, -v143\n\t"
"v_accvgpr_write_b32 a58, v98\n\t"
"v_mfma_f32_16x16x32_bf16 v[188:191], a[212:215], a[196:199], v[188:191]\n\t"
"v_accvgpr_write_b32 a59, v99\n\t"
"v_fma_f32 v98, -v46, s56, -v144\n\t"
"v_mfma_f32_16x16x32_bf16 v[188:191], a[12:15], a[192:195], v[188:191]\n\t"
"buffer_store_dwordx4 a[80:83], v248, s[16:19], s29 offen offset:0 nt\n\t"
"ds_read_b128 a[192:195], v222 offset:40448\n\t"
"v_fma_f32 v99, -v47, s56, -v145\n\t"
"v_accvgpr_write_b32 a64, v98\n\t"
"v_fma_f32 v98, -v48, s56, -v146\n\t"
"v_mfma_f32_16x16x32_bf16 v[192:195], a[12:15], a[196:199], v[192:195]\n\t"
"s_waitcnt vmcnt(23)\n\t"
"ds_read_b128 a[196:199], v223 offset:46592\n\t"
"v_accvgpr_write_b32 a65, v99\n\t"
"v_mfma_f32_16x16x32_bf16 v[192:195], a[212:215], a[200:203], v[192:195]\n\t"
"s_waitcnt lgkmcnt(5)\n\t"
"ds_read_b128 a[200:203], v223 offset:52736\n\t"
"v_fma_f32 v99, -v49, s56, -v147\n\t"
"v_mfma_f32_16x16x32_bf16 v[192:195], a[216:219], a[204:207], v[192:195]\n\t"
"buffer_store_dwordx4 a[88:91], v248, s[16:19], s29 offen offset:128 nt\n\t"
"buffer_load_dwordx4 a[212:215], off, s[80:83], s20 offset:0 sc1\n\t"
"ds_read_b128 a[204:207], v222 offset:60416\n\t"
"Lst20_%=:\n\t"
"v_mfma_f32_16x16x32_bf16 v[196:199], a[220:223], a[168:171], v[196:199]\n\t"
"s_waitcnt lgkmcnt(6)\n\t"
"v_accvgpr_write_b32 a66, v98\n\t"
"v_accvgpr_write_b32 a67, v99\n\t"
"v_mfma_f32_16x16x32_bf16 v[196:199], a[228:231], a[176:179], v[196:199]\n\t"
"v_fma_f32 v98, -v50, s56, -v148\n\t"
"v_fma_f32 v99, -v51, s56, -v149\n\t"
"v_mfma_f32_16x16x32_bf16 v[196:199], a[224:227], a[172:175], v[196:199]\n\t"
"buffer_store_dwordx4 a[96:99], v248, s[16:19], s29 offen offset:256 nt\n\t"
"buffer_load_dwordx4 a[216:219], off, s[80:83], s20 offset:1024 sc1\n\t"
"v_accvgpr_write_b32 a72, v98\n\t"
"s_add_u32 s29, s23, 1180800\n\t"
"v_mfma_f32_16x16x32_bf16 v[204:207], a[220:223], a[172:175], v[204:207]\n\t"
"s_waitcnt lgkmcnt(5)\n\t"
"v_fma_f32 v98, -v52, s56, -v150\n\t"
"v_accvgpr_write_b32 a73, v99\n\t"
"v_mfma_f32_16x16x32_bf16 v[204:207], a[224:227], a[176:179], v[204:207]\n\t"
"v_fma_f32 v99, -v53, s56, -v151\n\t"
"v_accvgpr_write_b32 a74, v98\n\t"
"v_mfma_f32_16x16x32_bf16 v[204:207], a[228:231], a[180:183], v[204:207]\n\t"
"buffer_store_dwordx4 a[104:107], v248, s[16:19], s29 offen offset:0 nt\n\t"
"v_accvgpr_write_b32 a75, v99\n\t"
"v_fma_f32 v98, -v54, s56, -v152\n\t"
"v_fma_f32 v99, -v55, s56, -v153\n\t"
"v_accvgpr_write_b32 a80, v98\n\t"
"v_mfma_f32_16x16x32_bf16 v[212:215], a[228:231], a[184:187], v[212:215]\n\t"
"s_waitcnt lgkmcnt(4)\n\t"
"v_fma_f32 v98, -v56, s56, -v154\n\t"
"v_accvgpr_write_b32 a81, v99\n\t"
"v_mfma_f32_16x16x32_bf16 v[212:215], a[224:227], a[180:183], v[212:215]\n\t"
"v_fma_f32 v99, -v57, s56, -v155\n\t"
"v_accvgpr_write_b32 a82, v98\n\t"
"v_mfma_f32_16x16x32_bf16 v[212:215], a[220:223], a[176:179], v[212:215]\n\t"
"buffer_store_dwordx4 a[112:115], v248, s[16:19], s29 offen offset:128 nt\n\t"
"v_accvgpr_write_b32 a83, v99\n\t"
"v_fma_f32 v98, -v58, s56, -v156\n\t"
"v_fma_f32 v99, -v59, s56, -v157\n\t"
"v_accvgpr_write_b32 a88, v98\n\t"
"v_mfma_f32_16x16x32_bf16 a[0:3], a[220:223], a[180:183], a[0:3]\n\t"
"s_waitcnt lgkmcnt(3)\n\t"
"v_fma_f32 v98, -v60, s56, -v158\n\t"
"v_accvgpr_write_b32 a89, v99\n\t"
"v_mfma_f32_16x16x32_bf16 a[0:3], a[224:227], a[184:187], a[0:3]\n\t"
"v_fma_f32 v99, -v61, s56, -v159\n\t"
"v_accvgpr_write_b32 a90, v98\n\t"
"v_mfma_f32_16x16x32_bf16 a[0:3], a[228:231], a[188:191], a[0:3]\n\t"
"buffer_store_dwordx4 a[120:123], v248, s[16:19], s29 offen offset:256 nt\n\t"
"v_accvgpr_write_b32 a91, v99\n\t"
"v_fma_f32 v98, -v62, s56, -v160\n\t"
"v_fma_f32 v99, -v63, s56, -v161\n\t"
"v_accvgpr_write_b32 a96, v98\n\t"
"v_mfma_f32_16x16x32_bf16 a[8:11], a[228:231], a[192:195], a[8:11]\n\t"
"s_waitcnt lgkmcnt(2)\n\t"
"v_fma_f32 v98, -v64, s56, -v162\n\t"
"v_accvgpr_write_b32 a97, v99\n\t"
"v_mfma_f32_16x16x32_bf16 a[8:11], a[224:227], a[188:191], a[8:11]\n\t"
"v_fma_f32 v99, -v65, s56, -v163\n\t"
"v_accvgpr_write_b32 a98, v98\n\t"
"v_mfma_f32_16x16x32_bf16 a[8:11], a[220:223], a[184:187], a[8:11]\n\t"
"buffer_load_dwordx4 v[248:251], off, s[80:83], s20 offset:2048 sc1\n\t"
"v_mfma_f32_16x16x32_bf16 a[16:19], a[220:223], a[188:191], a[16:19]\n\t"
"s_waitcnt lgkmcnt(1)\n\t"
"v_accvgpr_write_b32 a99, v99\n\t"
"v_fma_f32 v98, -v66, s56, -v164\n\t"
"v_mfma_f32_16x16x32_bf16 a[16:19], a[224:227], a[192:195], a[16:19]\n\t"
"s_mov_b32 s20, 172032\n\t"
"v_fma_f32 v99, -v67, s56, -v165\n\t"
"v_accvgpr_write_b32 a104, v98\n\t"
"v_mfma_f32_16x16x32_bf16 a[16:19], a[228:231], a[196:199], a[16:19]\n\t"
"v_fma_f32 v98, -v68, s56, -v166\n\t"
"v_accvgpr_write_b32 a105, v99\n\t"
"v_mfma_f32_16x16x32_bf16 a[24:27], a[228:231], a[200:203], a[24:27]\n\t"
"s_waitcnt lgkmcnt(0)\n\t"
"v_fma_f32 v99, -v69, s56, -v167\n\t"
"v_accvgpr_write_b32 a106, v98\n\t"
"v_mfma_f32_16x16x32_bf16 a[24:27], a[224:227], a[196:199], a[24:27]\n\t"
"v_accvgpr_write_b32 a107, v99\n\t"
"v_fma_f32 v98, -v70, s56, -v168\n\t"
"v_mfma_f32_16x16x32_bf16 a[24:27], a[220:223], a[192:195], a[24:27]\n\t"
"v_fma_f32 v99, -v71, s56, -v169\n\t"
"v_accvgpr_write_b32 a112, v98\n\t"
"v_mfma_f32_16x16x32_bf16 a[32:35], a[220:223], a[196:199], a[32:35]\n\t"
"s_waitcnt vmcnt(25)\n\t"
"v_fma_f32 v98, -v72, s56, -v170\n\t"
"v_accvgpr_write_b32 a113, v99\n\t"
"v_mfma_f32_16x16x32_bf16 a[32:35], a[224:227], a[200:203], a[32:35]\n\t"
"v_fma_f32 v99, -v73, s56, -v171\n\t"
"v_accvgpr_write_b32 a114, v98\n\t"
"v_mfma_f32_16x16x32_bf16 a[32:35], a[228:231], a[204:207], a[32:35]\n\t"
"v_accvgpr_write_b32 a115, v99\n\t"
"v_fma_f32 v98, -v74, s56, -v172\n\t"
"v_mfma_f32_16x16x32_bf16 a[40:43], a[232:235], a[168:171], a[40:43]\n\t"
"v_fma_f32 v99, -v75, s56, -v173\n\t"
"v_accvgpr_write_b32 a120, v98\n\t"
"v_mfma_f32_16x16x32_bf16 a[40:43], a[244:247], a[176:179], a[40:43]\n\t"
"v_fma_f32 v98, -v76, s56, -v174\n\t"
"v_accvgpr_write_b32 a121, v99\n\t"
"v_mfma_f32_16x16x32_bf16 a[40:43], a[236:239], a[172:175], a[40:43]\n\t"
"buffer_load_dwordx4 a[220:223], off, s[80:83], s20 offset:0 sc1\n\t"
"v_mfma_f32_16x16x32_bf16 a[48:51], a[232:235], a[172:175], a[48:51]\n\t"
"buffer_load_dwordx4 a[224:227], off, s[80:83], s20 offset:1024 sc1\n\t"
"v_mfma_f32_16x16x32_bf16 a[48:51], a[236:239], a[176:179], a[48:51]\n\t"
"buffer_load_dwordx4 a[228:231], off, s[80:83], s20 offset:2048 sc1\n\t"
"v_mfma_f32_16x16x32_bf16 a[48:51], a[244:247], a[180:183], a[48:51]\n\t"
"v_fma_f32 v99, -v77, s56, -v175\n\t"
"v_accvgpr_write_b32 a122, v98\n\t"
"v_mfma_f32_16x16x32_bf16 a[56:59], a[244:247], a[184:187], a[56:59]\n\t"
"v_accvgpr_write_b32 a123, v99\n\t"
"v_fma_f32 v98, -v78, s56, -v176\n\t"
"v_mfma_f32_16x16x32_bf16 a[56:59], a[236:239], a[180:183], a[56:59]\n\t"
"v_fma_f32 v99, -v79, s56, -v177\n\t"
"v_accvgpr_write_b32 a128, v98\n\t"
"v_mfma_f32_16x16x32_bf16 a[56:59], a[232:235], a[176:179], a[56:59]\n\t"
"v_fma_f32 v98, -v80, s56, -v178\n\t"
"v_accvgpr_write_b32 a129, v99\n\t"
"v_mfma_f32_16x16x32_bf16 a[64:67], a[232:235], a[180:183], a[64:67]\n\t"
"v_fma_f32 v99, -v81, s56, -v179\n\t"
"v_accvgpr_write_b32 a130, v98\n\t"
"v_mfma_f32_16x16x32_bf16 a[64:67], a[236:239], a[184:187], a[64:67]\n\t"
"v_accvgpr_write_b32 a131, v99\n\t"
"v_mfma_f32_16x16x32_bf16 a[64:67], a[244:247], a[188:191], a[64:67]\n\t"
"s_barrier\n\t"
"v_mfma_f32_16x16x32_bf16 a[72:75], a[244:247], a[192:195], a[72:75]\n\t"
"v_fma_f32 v98, -v82, s56, -v180\n\t"
"v_mfma_f32_16x16x32_bf16 a[72:75], a[236:239], a[188:191], a[72:75]\n\t"
"v_fma_f32 v99, -v83, s56, -v181\n\t"
"v_accvgpr_write_b32 a136, v98\n\t"
"v_mfma_f32_16x16x32_bf16 a[72:75], a[232:235], a[184:187], a[72:75]\n\t"
"v_fma_f32 v98, -v84, s56, -v182\n\t"
"v_accvgpr_write_b32 a137, v99\n\t"
"v_mfma_f32_16x16x32_bf16 a[80:83], a[232:235], a[188:191], a[80:83]\n\t"
"s_mov_b32 s20, 36864\n\t"
"v_fma_f32 v99, -v85, s56, -v183\n\t"
"v_accvgpr_write_b32 a138, v98\n\t"
"v_mfma_f32_16x16x32_bf16 a[80:83], a[236:239], a[192:195], a[80:83]\n\t"
"v_accvgpr_write_b32 a139, v99\n\t"
"v_fma_f32 v98, -v86, s56, -v184\n\t"
"v_mfma_f32_16x16x32_bf16 a[80:83], a[244:247], a[196:199], a[80:83]\n\t"
"v_fma_f32 v99, -v87, s56, -v185\n\t"
"v_accvgpr_write_b32 a144, v98\n\t"
"v_mfma_f32_16x16x32_bf16 a[88:91], a[244:247], a[200:203], a[88:91]\n\t"
"v_fma_f32 v98, -v88, s56, -v186\n\t"
"v_accvgpr_write_b32 a145, v99\n\t"
"v_mfma_f32_16x16x32_bf16 a[88:91], a[236:239], a[196:199], a[88:91]\n\t"
"v_fma_f32 v99, -v89, s56, -v187\n\t"
"v_accvgpr_write_b32 a146, v98\n\t"
"v_mfma_f32_16x16x32_bf16 a[88:91], a[232:235], a[192:195], a[88:91]\n\t"
"v_accvgpr_write_b32 a147, v99\n\t"
"v_fma_f32 v98, -v90, s56, -v188\n\t"
"v_mfma_f32_16x16x32_bf16 a[96:99], a[232:235], a[196:199], a[96:99]\n\t"
"s_waitcnt vmcnt(23)\n\t"
"v_fma_f32 v99, -v91, s56, -v189\n\t"
"v_accvgpr_write_b32 a152, v98\n\t"
"v_mfma_f32_16x16x32_bf16 a[96:99], a[236:239], a[200:203], a[96:99]\n\t"
"v_fma_f32 v98, -v92, s56, -v190\n\t"
"v_accvgpr_write_b32 a153, v99\n\t"
"v_mfma_f32_16x16x32_bf16 a[96:99], a[244:247], a[204:207], a[96:99]\n\t"
"v_fma_f32 v99, -v93, s56, -v191\n\t"
"v_accvgpr_write_b32 a154, v98\n\t"
"v_mfma_f32_16x16x32_bf16 a[104:107], a[240:243], a[168:171], a[104:107]\n\t"
"ds_read_b128 a[168:171], v222 offset:768\n\t"
"v_accvgpr_write_b32 a155, v99\n\t"
"v_mfma_f32_16x16x32_bf16 a[104:107], a[252:255], a[176:179], a[104:107]\n\t"
"v_fma_f32 v98, -v94, s56, -v192\n\t"
"v_fma_f32 v99, -v95, s56, -v193\n\t"
"v_mfma_f32_16x16x32_bf16 a[104:107], a[248:251], a[172:175], a[104:107]\n\t"
"buffer_load_dwordx4 a[232:235], off, s[80:83], s20 offset:0 sc1\n\t"
"v_mfma_f32_16x16x32_bf16 a[112:115], a[240:243], a[172:175], a[112:115]\n\t"
"buffer_load_dwordx4 a[236:239], off, s[80:83], s20 offset:1024 sc1\n\t"
"v_mfma_f32_16x16x32_bf16 a[112:115], a[248:251], a[176:179], a[112:115]\n\t"
"buffer_load_dwordx4 a[244:247], off, s[80:83], s20 offset:2048 sc1\n\t"
"v_mfma_f32_16x16x32_bf16 a[112:115], a[252:255], a[180:183], a[112:115]\n\t"
"ds_read_b128 a[172:175], v223 offset:6912\n\t"
"v_accvgpr_write_b32 a160, v98\n\t"
"v_mfma_f32_16x16x32_bf16 a[120:123], a[252:255], a[184:187], a[120:123]\n\t"
"v_fma_f32 v98, -v96, s56, -v194\n\t"
"v_accvgpr_write_b32 a161, v99\n\t"
"v_mfma_f32_16x16x32_bf16 a[120:123], a[248:251], a[180:183], a[120:123]\n\t"
"v_fma_f32 v99, -v97, s56, -v195\n\t"
"v_accvgpr_write_b32 a162, v98\n\t"
"v_mfma_f32_16x16x32_bf16 a[120:123], a[240:243], a[176:179], a[120:123]\n\t"
"ds_read_b128 a[176:179], v223 offset:13056\n\t"
"v_accvgpr_write_b32 a163, v99\n\t"
"v_mfma_f32_16x16x32_bf16 a[128:131], a[240:243], a[180:183], a[128:131]\n\t"
"ds_read_b128 a[180:183], v222 offset:20736\n\t"
"v_fma_f32 v200, v100, s64, -v2\n\t"
"v_mfma_f32_16x16x32_bf16 a[128:131], a[248:251], a[184:187], a[128:131]\n\t"
"Lpg15_%=:\n\t"
"s_cbranch_vccnz Lvr1_%=\n\t"
"v_fma_f32 v201, v101, s64, -v3\n\t"
"v_fma_f32 v202, v102, s64, -v4\n\t"
"v_mfma_f32_16x16x32_bf16 a[128:131], a[252:255], a[188:191], a[128:131]\n\t"
"v_fma_f32 v203, v103, s64, -v5\n\t"
"v_fma_f32 v208, v104, s64, -v6\n\t"
"v_mfma_f32_16x16x32_bf16 a[136:139], a[252:255], a[192:195], a[136:139]\n\t"
"v_fma_f32 v209, v105, s64, -v7\n\t"
"v_fma_f32 v210, v106, s64, -v8\n\t"
"v_mfma_f32_16x16x32_bf16 a[136:139], a[248:251], a[188:191], a[136:139]\n\t"
"v_fma_f32 v211, v107, s64, -v9\n\t"
"v_fma_f32 v216, v108, s64, -v10\n\t"
"v_mfma_f32_16x16x32_bf16 a[136:139], a[240:243], a[184:187], a[136:139]\n\t"
"ds_read_b128 a[184:187], v223 offset:26880\n\t"
"v_fma_f32 v217, v109, s64, -v11\n\t"
"v_mfma_f32_16x16x32_bf16 a[144:147], a[240:243], a[188:191], a[144:147]\n\t"
"s_mov_b32 s20, 49152\n\t"
"ds_read_b128 a[188:191], v223 offset:33024\n\t"
"v_fma_f32 v218, v110, s64, -v12\n\t"
"v_mfma_f32_16x16x32_bf16 a[144:147], a[248:251], a[192:195], a[144:147]\n\t"
"v_fma_f32 v219, v111, s64, -v13\n\t"
"v_fma_f32 v98, v112, s64, -v14\n\t"
"v_mfma_f32_16x16x32_bf16 a[144:147], a[252:255], a[196:199], a[144:147]\n\t"
"v_fma_f32 v99, v113, s64, -v15\n\t"
"v_accvgpr_write_b32 a4, v98\n\t"
"v_mfma_f32_16x16x32_bf16 a[152:155], a[252:255], a[200:203], a[152:155]\n\t"
"v_fma_f32 v98, v114, s64, -v16\n\t"
"v_accvgpr_write_b32 a5, v99\n\t"
"v_mfma_f32_16x16x32_bf16 a[152:155], a[248:251], a[196:199], a[152:155]\n\t"
"v_fma_f32 v99, v115, s64, -v17\n\t"
"v_accvgpr_write_b32 a6, v98\n\t"
"v_mfma_f32_16x16x32_bf16 a[152:155], a[240:243], a[192:195], a[152:155]\n\t"
"ds_read_b128 a[192:195], v222 offset:40704\n\t"
"v_accvgpr_write_b32 a7, v99\n\t"
"v_mfma_f32_16x16x32_bf16 a[160:163], a[240:243], a[196:199], a[160:163]\n\t"
"s_waitcnt vmcnt(15) lgkmcnt(4)\n\t"
"ds_read_b128 a[196:199], v223 offset:46848\n\t"
"v_fma_f32 v98, v116, s64, -v18\n\t"
"v_mfma_f32_16x16x32_bf16 a[160:163], a[248:251], a[200:203], a[160:163]\n\t"
"ds_read_b128 a[200:203], v223 offset:52992\n\t"
"v_fma_f32 v99, v117, s64, -v19\n\t"
"v_mfma_f32_16x16x32_bf16 a[160:163], a[252:255], a[204:207], a[160:163]\n\t"
"ds_read_b128 a[204:207], v222 offset:60672\n\t"
"v_accvgpr_write_b32 a12, v98\n\t"
"v_mfma_f32_16x16x32_bf16 v[200:203], v[240:243], a[168:171], v[200:203]\n\t"
"s_waitcnt lgkmcnt(6)\n\t"
"v_fma_f32 v98, v118, s64, -v20\n\t"
"v_accvgpr_write_b32 a13, v99\n\t"
"v_mfma_f32_16x16x32_bf16 v[200:203], v[236:239], a[176:179], v[200:203]\n\t"
"v_fma_f32 v99, v119, s64, -v21\n\t"
"v_accvgpr_write_b32 a14, v98\n\t"
"v_mfma_f32_16x16x32_bf16 v[200:203], a[208:211], a[172:175], v[200:203]\n\t"
"buffer_load_dwordx4 a[240:243], off, s[80:83], s20 offset:0 sc1\n\t"
"v_mfma_f32_16x16x32_bf16 v[208:211], v[240:243], a[172:175], v[208:211]\n\t"
"buffer_load_dwordx4 a[248:251], off, s[80:83], s20 offset:1024 sc1\n\t"
"v_mfma_f32_16x16x32_bf16 v[208:211], a[208:211], a[176:179], v[208:211]\n\t"
"s_waitcnt lgkmcnt(5)\n\t"
"v_accvgpr_write_b32 a15, v99\n\t"
"v_mfma_f32_16x16x32_bf16 v[208:211], v[236:239], a[180:183], v[208:211]\n\t"
"s_waitcnt lgkmcnt(0)\n\t"
"s_barrier\n\t"
"v_mfma_f32_16x16x32_bf16 v[216:219], v[236:239], a[184:187], v[216:219]\n\t"
"v_fma_f32 v98, v120, s64, -v22\n\t"
"v_mfma_f32_16x16x32_bf16 v[216:219], a[208:211], a[180:183], v[216:219]\n\t"
"buffer_load_dwordx4 a[252:255], off, s[80:83], s20 offset:2048 sc1\n\t"
"v_mfma_f32_16x16x32_bf16 v[216:219], v[240:243], a[176:179], v[216:219]\n\t"
"ds_read_b64_tr_b16 v[232:233], v246 offset:1024\n\t"
"v_fma_f32 v99, v121, s64, -v23\n\t"
"v_mfma_f32_16x16x32_bf16 a[4:7], v[240:243], a[180:183], a[4:7]\n\t"
"ds_read_b64_tr_b16 v[228:229], v255 offset:1024\n\t"
"v_accvgpr_write_b32 a20, v98\n\t"
"v_mfma_f32_16x16x32_bf16 a[4:7], a[208:211], a[184:187], a[4:7]\n\t"
"v_fma_f32 v98, v122, s64, -v24\n\t"
"v_accvgpr_write_b32 a21, v99\n\t"
"v_mfma_f32_16x16x32_bf16 a[4:7], v[236:239], a[188:191], a[4:7]\n\t"
"v_fma_f32 v99, v123, s64, -v25\n\t"
"v_accvgpr_write_b32 a22, v98\n\t"
"v_mfma_f32_16x16x32_bf16 a[12:15], v[236:239], a[192:195], a[12:15]\n\t"
"v_accvgpr_write_b32 a23, v99\n\t"
"v_fma_f32 v98, v124, s64, -v26\n\t"
"v_mfma_f32_16x16x32_bf16 a[12:15], a[208:211], a[188:191], a[12:15]\n\t"
"v_fma_f32 v99, v125, s64, -v27\n\t"
"v_accvgpr_write_b32 a28, v98\n\t"
"v_mfma_f32_16x16x32_bf16 a[12:15], v[240:243], a[184:187], a[12:15]\n\t"
"v_fma_f32 v98, v126, s64, -v28\n\t"
"v_accvgpr_write_b32 a29, v99\n\t"
"v_fma_f32 v99, v127, s64, -v29\n\t"
"v_mfma_f32_16x16x32_bf16 a[20:23], v[240:243], a[188:191], a[20:23]\n\t"
"s_mov_b32 s20, 61440\n\t"
"v_accvgpr_write_b32 a30, v98\n\t"
"v_accvgpr_write_b32 a31, v99\n\t"
"v_mfma_f32_16x16x32_bf16 a[20:23], a[208:211], a[192:195], a[20:23]\n\t"
"v_fma_f32 v98, v128, s64, -v30\n\t"
"v_fma_f32 v99, v129, s64, -v31\n\t"
"v_mfma_f32_16x16x32_bf16 a[20:23], v[236:239], a[196:199], a[20:23]\n\t"
"v_accvgpr_write_b32 a36, v98\n\t"
"v_fma_f32 v98, v130, s64, -v32\n\t"
"v_mfma_f32_16x16x32_bf16 a[28:31], v[236:239], a[200:203], a[28:31]\n\t"
"v_accvgpr_write_b32 a37, v99\n\t"
"v_fma_f32 v99, v131, s64, -v33\n\t"
"v_mfma_f32_16x16x32_bf16 a[28:31], a[208:211], a[196:199], a[28:31]\n\t"
"v_accvgpr_write_b32 a38, v98\n\t"
"v_accvgpr_write_b32 a39, v99\n\t"
"v_mfma_f32_16x16x32_bf16 a[28:31], v[240:243], a[192:195], a[28:31]\n\t"
"v_fma_f32 v98, v132, s64, -v34\n\t"
"v_fma_f32 v99, v133, s64, -v35\n\t"
"v_mfma_f32_16x16x32_bf16 a[36:39], v[240:243], a[196:199], a[36:39]\n\t"
"s_waitcnt lgkmcnt(1)\n\t"
"v_accvgpr_write_b32 a44, v98\n\t"
"v_fma_f32 v98, v134, s64, -v36\n\t"
"v_mfma_f32_16x16x32_bf16 a[36:39], a[208:211], a[200:203], a[36:39]\n\t"
"s_waitcnt lgkmcnt(0)\n\t"
"v_accvgpr_write_b32 a45, v99\n\t"
"v_fma_f32 v99, v135, s64, -v37\n\t"
"v_mfma_f32_16x16x32_bf16 a[36:39], v[236:239], a[204:207], a[36:39]\n\t"
"v_accvgpr_write_b32 a46, v98\n\t"
"v_accvgpr_write_b32 a47, v99\n\t"
"v_mfma_f32_4x4x4_16b_bf16 v[232:235], v[232:233], v[220:221], 0\n\t"
"buffer_load_dwordx4 v[240:243], off, s[80:83], s20 offset:0 sc1\n\t"
"v_fma_f32 v98, v136, s64, -v38\n\t"
"v_fma_f32 v99, v137, s64, -v39\n\t"
"v_mfma_f32_4x4x4_16b_bf16 v[228:231], v[228:229], v[220:221], 0\n\t"
"s_waitcnt vmcnt(10)\n\t"
"v_mfma_f32_16x16x32_bf16 a[44:47], a[212:215], a[168:171], a[44:47]\n\t"
"buffer_load_dwordx4 a[208:211], off, s[80:83], s20 offset:1024 sc1\n\t"
"v_mfma_f32_16x16x32_bf16 a[44:47], v[248:251], a[176:179], a[44:47]\n\t"
"v_accvgpr_write_b32 a52, v98\n\t"
"v_fma_f32 v98, v138, s64, -v40\n\t"
"v_mfma_f32_16x16x32_bf16 a[44:47], a[216:219], a[172:175], a[44:47]\n\t"
"v_accvgpr_write_b32 a53, v99\n\t"
"v_fma_f32 v99, v139, s64, -v41\n\t"
"v_accvgpr_write_b32 a54, v98\n\t"
"v_accvgpr_write_b32 a55, v99\n\t"
"v_fma_f32 v98, v140, s64, -v42\n\t"
"v_fma_f32 v99, v141, s64, -v43\n\t"
"v_mfma_f32_16x16x32_bf16 a[52:55], a[212:215], a[172:175], a[52:55]\n\t"
"v_accvgpr_write_b32 a60, v98\n\t"
"v_fma_f32 v98, v142, s64, -v44\n\t"
"v_mfma_f32_16x16x32_bf16 a[52:55], a[216:219], a[176:179], a[52:55]\n\t"
"v_accvgpr_write_b32 a61, v99\n\t"
"v_fma_f32 v99, v143, s64, -v45\n\t"
"v_mfma_f32_16x16x32_bf16 a[52:55], v[248:251], a[180:183], a[52:55]\n\t"
"v_accvgpr_write_b32 a62, v98\n\t"
"v_accvgpr_write_b32 a63, v99\n\t"
"v_fma_f32 v98, v144, s64, -v46\n\t"
"v_fma_f32 v99, v145, s64, -v47\n\t"
"v_cvt_pk_bf16_f32 v232, v232, v233\n\t"
"v_cvt_pk_bf16_f32 v233, v234, v235\n\t"
"v_cvt_pk_bf16_f32 v234, v228, v229\n\t"
"v_cvt_pk_bf16_f32 v235, v230, v231\n\t"
"v_mfma_f32_16x16x32_bf16 a[60:63], v[248:251], a[184:187], a[60:63]\n\t"
"v_accvgpr_write_b32 a68, v98\n\t"
"v_fma_f32 v98, v146, s64, -v48\n\t"
"v_mfma_f32_16x16x32_bf16 a[60:63], a[216:219], a[180:183], a[60:63]\n\t"
"v_accvgpr_write_b32 a69, v99\n\t"
"v_fma_f32 v99, v147, s64, -v49\n\t"
"v_mfma_f32_16x16x32_bf16 a[60:63], a[212:215], a[176:179], a[60:63]\n\t"
"ds_write_b128 v245, v[232:235] offset:1024\n\t"
"v_accvgpr_write_b32 a70, v98\n\t"
"v_accvgpr_write_b32 a71, v99\n\t"
"v_fma_f32 v98, v148, s64, -v50\n\t"
"v_fma_f32 v99, v149, s64, -v51\n\t"
"v_mfma_f32_16x16x32_bf16 a[68:71], a[212:215], a[180:183], a[68:71]\n\t"
"ds_read_b64_tr_b16 v[236:237], v246 offset:7168\n\t"
"v_mfma_f32_16x16x32_bf16 a[68:71], a[216:219], a[184:187], a[68:71]\n\t"
"ds_read_b64_tr_b16 v[224:225], v255 offset:7168\n\t"
"v_accvgpr_write_b32 a76, v98\n\t"
"v_mfma_f32_16x16x32_bf16 a[68:71], v[248:251], a[188:191], a[68:71]\n\t"
"v_fma_f32 v98, v150, s64, -v52\n\t"
"v_accvgpr_write_b32 a77, v99\n\t"
"v_fma_f32 v99, v151, s64, -v53\n\t"
"v_accvgpr_write_b32 a78, v98\n\t"
"v_accvgpr_write_b32 a79, v99\n\t"
"v_fma_f32 v98, v152, s64, -v54\n\t"
"v_fma_f32 v99, v153, s64, -v55\n\t"
"v_mfma_f32_16x16x32_bf16 a[76:79], v[248:251], a[192:195], a[76:79]\n\t"
"v_accvgpr_write_b32 a84, v98\n\t"
"v_fma_f32 v98, v154, s64, -v56\n\t"
"v_mfma_f32_16x16x32_bf16 a[76:79], a[216:219], a[188:191], a[76:79]\n\t"
"v_accvgpr_write_b32 a85, v99\n\t"
"v_fma_f32 v99, v155, s64, -v57\n\t"
"v_mfma_f32_16x16x32_bf16 a[76:79], a[212:215], a[184:187], a[76:79]\n\t"
"v_accvgpr_write_b32 a86, v98\n\t"
"v_accvgpr_write_b32 a87, v99\n\t"
"v_fma_f32 v98, v156, s64, -v58\n\t"
"v_fma_f32 v99, v157, s64, -v59\n\t"
"v_mfma_f32_16x16x32_bf16 a[84:87], a[212:215], a[188:191], a[84:87]\n\t"
"v_accvgpr_write_b32 a92, v98\n\t"
"v_fma_f32 v98, v158, s64, -v60\n\t"
"v_mfma_f32_16x16x32_bf16 a[84:87], a[216:219], a[192:195], a[84:87]\n\t"
"v_accvgpr_write_b32 a93, v99\n\t"
"v_fma_f32 v99, v159, s64, -v61\n\t"
"v_mfma_f32_16x16x32_bf16 a[84:87], v[248:251], a[196:199], a[84:87]\n\t"
"v_accvgpr_write_b32 a94, v98\n\t"
"v_accvgpr_write_b32 a95, v99\n\t"
"v_fma_f32 v98, v160, s64, -v62\n\t"
"v_fma_f32 v99, v161, s64, -v63\n\t"
"v_mfma_f32_16x16x32_bf16 a[92:95], v[248:251], a[200:203], a[92:95]\n\t"
"v_accvgpr_write_b32 a100, v98\n\t"
"v_fma_f32 v98, v162, s64, -v64\n\t"
"v_mfma_f32_16x16x32_bf16 a[92:95], a[216:219], a[196:199], a[92:95]\n\t"
"v_accvgpr_write_b32 a101, v99\n\t"
"v_fma_f32 v99, v163, s64, -v65\n\t"
"v_mfma_f32_16x16x32_bf16 a[92:95], a[212:215], a[192:195], a[92:95]\n\t"
"v_accvgpr_write_b32 a102, v98\n\t"
"v_accvgpr_write_b32 a103, v99\n\t"
"s_nop 1\n\t"
"v_mfma_f32_16x16x32_bf16 a[100:103], a[212:215], a[196:199], a[100:103]\n\t"
"buffer_load_dwordx4 a[212:215], off, s[80:83], s20 offset:2048 sc1\n\t"
"v_mfma_f32_16x16x32_bf16 a[100:103], a[216:219], a[200:203], a[100:103]\n\t"
"s_waitcnt lgkmcnt(0)\n\t"
"s_mov_b32 s20, 73728\n\t"
"v_fma_f32 v98, v164, s64, -v66\n\t"
"v_mfma_f32_16x16x32_bf16 a[100:103], v[248:251], a[204:207], a[100:103]\n\t"
"v_fma_f32 v99, v165, s64, -v67\n\t"
"v_accvgpr_write_b32 a108, v98\n\t"
"v_fma_f32 v98, v166, s64, -v68\n\t"
"v_accvgpr_write_b32 a109, v99\n\t"
"v_fma_f32 v99, v167, s64, -v69\n\t"
"v_accvgpr_write_b32 a110, v98\n\t"
"v_accvgpr_write_b32 a111, v99\n\t"
"v_mfma_f32_4x4x4_16b_bf16 v[236:239], v[236:237], v[220:221], 0\n\t"
"s_waitcnt vmcnt(9)\n\t"
"v_fma_f32 v98, v168, s64, -v70\n\t"
"v_fma_f32 v99, v169, s64, -v71\n\t"
"v_mfma_f32_4x4x4_16b_bf16 v[224:227], v[224:225], v[220:221], 0\n\t"
"v_mfma_f32_16x16x32_bf16 a[108:111], a[220:223], a[168:171], a[108:111]\n\t"
"buffer_load_dwordx4 v[248:251], off, s[80:83], s20 offset:0 sc1\n\t"
"v_mfma_f32_16x16x32_bf16 a[108:111], a[228:231], a[176:179], a[108:111]\n\t"
"buffer_load_dwordx4 a[216:219], off, s[80:83], s20 offset:1024 sc1\n\t"
"v_mfma_f32_16x16x32_bf16 a[108:111], a[224:227], a[172:175], a[108:111]\n\t"
"ds_read_b128 a[168:171], v222 offset:2048\n\t"
"v_accvgpr_write_b32 a116, v98\n\t"
"v_fma_f32 v98, v170, s64, -v72\n\t"
"v_accvgpr_write_b32 a117, v99\n\t"
"v_fma_f32 v99, v171, s64, -v73\n\t"
"v_accvgpr_write_b32 a118, v98\n\t"
"v_accvgpr_write_b32 a119, v99\n\t"
"v_fma_f32 v98, v172, s64, -v74\n\t"
"v_fma_f32 v99, v173, s64, -v75\n\t"
"v_mfma_f32_16x16x32_bf16 a[116:119], a[220:223], a[172:175], a[116:119]\n\t"
"ds_read_b128 a[172:175], v223 offset:8192\n\t"
"v_accvgpr_write_b32 a124, v98\n\t"
"v_mfma_f32_16x16x32_bf16 a[116:119], a[224:227], a[176:179], a[116:119]\n\t"
"v_fma_f32 v98, v174, s64, -v76\n\t"
"v_accvgpr_write_b32 a125, v99\n\t"
"v_mfma_f32_16x16x32_bf16 a[116:119], a[228:231], a[180:183], a[116:119]\n\t"
"v_fma_f32 v99, v175, s64, -v77\n\t"
"v_accvgpr_write_b32 a126, v98\n\t"
"v_accvgpr_write_b32 a127, v99\n\t"
"v_fma_f32 v98, v176, s64, -v78\n\t"
"v_fma_f32 v99, v177, s64, -v79\n\t"
"v_cvt_pk_bf16_f32 v236, v236, v237\n\t"
"v_cvt_pk_bf16_f32 v237, v238, v239\n\t"
"v_cvt_pk_bf16_f32 v238, v224, v225\n\t"
"v_cvt_pk_bf16_f32 v239, v226, v227\n\t"
"v_mfma_f32_16x16x32_bf16 a[124:127], a[228:231], a[184:187], a[124:127]\n\t"
"v_accvgpr_write_b32 a132, v98\n\t"
"v_fma_f32 v98, v178, s64, -v80\n\t"
"v_mfma_f32_16x16x32_bf16 a[124:127], a[224:227], a[180:183], a[124:127]\n\t"
"v_accvgpr_write_b32 a133, v99\n\t"
"v_fma_f32 v99, v179, s64, -v81\n\t"
"v_mfma_f32_16x16x32_bf16 a[124:127], a[220:223], a[176:179], a[124:127]\n\t"
"ds_write_b128 v245, v[236:239] offset:7168\n\t"
"v_accvgpr_write_b32 a134, v98\n\t"
"v_accvgpr_write_b32 a135, v99\n\t"
"v_fma_f32 v98, v180, s64, -v82\n\t"
"v_fma_f32 v99, v181, s64, -v83\n\t"
"v_mfma_f32_16x16x32_bf16 a[132:135], a[220:223], a[180:183], a[132:135]\n\t"
"ds_read_b128 a[176:179], v223 offset:14336\n\t"
"v_mfma_f32_16x16x32_bf16 a[132:135], a[224:227], a[184:187], a[132:135]\n\t"
"ds_read_b128 a[180:183], v222 offset:22016\n\t"
"v_accvgpr_write_b32 a140, v98\n\t"
"v_mfma_f32_16x16x32_bf16 a[132:135], a[228:231], a[188:191], a[132:135]\n\t"
"ds_read_b64_tr_b16 v[228:229], v246 offset:13312\n\t"
"v_fma_f32 v98, v182, s64, -v84\n\t"
"v_accvgpr_write_b32 a141, v99\n\t"
"v_fma_f32 v99, v183, s64, -v85\n\t"
"v_accvgpr_write_b32 a142, v98\n\t"
"v_accvgpr_write_b32 a143, v99\n\t"
"v_fma_f32 v98, v184, s64, -v86\n\t"
"v_fma_f32 v99, v185, s64, -v87\n\t"
"v_mfma_f32_16x16x32_bf16 a[140:143], a[228:231], a[192:195], a[140:143]\n\t"
"ds_read_b64_tr_b16 v[232:233], v255 offset:13312\n\t"
"v_accvgpr_write_b32 a148, v98\n\t"
"v_mfma_f32_16x16x32_bf16 a[140:143], a[224:227], a[188:191], a[140:143]\n\t"
"v_fma_f32 v98, v186, s64, -v88\n\t"
"v_accvgpr_write_b32 a149, v99\n\t"
"v_mfma_f32_16x16x32_bf16 a[140:143], a[220:223], a[184:187], a[140:143]\n\t"
"ds_read_b128 a[184:187], v223 offset:28160\n\t"
"v_fma_f32 v99, v187, s64, -v89\n\t"
"v_accvgpr_write_b32 a150, v98\n\t"
"v_accvgpr_write_b32 a151, v99\n\t"
"v_fma_f32 v98, v188, s64, -v90\n\t"
"v_fma_f32 v99, v189, s64, -v91\n\t"
"v_mfma_f32_16x16x32_bf16 a[148:151], a[220:223], a[188:191], a[148:151]\n\t"
"ds_read_b128 a[188:191], v223 offset:34304\n\t"
"v_accvgpr_write_b32 a156, v98\n\t"
"v_mfma_f32_16x16x32_bf16 a[148:151], a[224:227], a[192:195], a[148:151]\n\t"
"v_fma_f32 v98, v190, s64, -v92\n\t"
"v_accvgpr_write_b32 a157, v99\n\t"
"v_mfma_f32_16x16x32_bf16 a[148:151], a[228:231], a[196:199], a[148:151]\n\t"
"v_fma_f32 v99, v191, s64, -v93\n\t"
"v_accvgpr_write_b32 a158, v98\n\t"
"v_accvgpr_write_b32 a159, v99\n\t"
"v_fma_f32 v98, v192, s64, -v94\n\t"
"v_fma_f32 v99, v193, s64, -v95\n\t"
"v_mfma_f32_16x16x32_bf16 a[156:159], a[228:231], a[200:203], a[156:159]\n\t"
"v_accvgpr_write_b32 a164, v98\n\t"
"v_fma_f32 v98, v194, s64, -v96\n\t"
"v_mfma_f32_16x16x32_bf16 a[156:159], a[224:227], a[196:199], a[156:159]\n\t"
"v_accvgpr_write_b32 a165, v99\n\t"
"v_fma_f32 v99, v195, s64, -v97\n\t"
"v_mfma_f32_16x16x32_bf16 a[156:159], a[220:223], a[192:195], a[156:159]\n\t"
"s_add_u32 m0, s25, -128\n\t"
"ds_read_b128 a[192:195], v222 offset:41984\n\t"
"v_accvgpr_write_b32 a166, v98\n\t"
"v_accvgpr_write_b32 a167, v99\n\t"
"s_nop 1\n\t"
"v_mfma_f32_16x16x32_bf16 a[164:167], a[220:223], a[196:199], a[164:167]\n\t"
"buffer_load_dwordx4 a[220:223], off, s[80:83], s20 offset:2048 sc1\n\t"
"v_mfma_f32_16x16x32_bf16 a[164:167], a[224:227], a[200:203], a[164:167]\n\t"
"s_waitcnt lgkmcnt(3)\n\t"
"ds_read_b128 a[196:199], v223 offset:48128\n\t"
"v_fma_f32 v0, v2, s52, v100\n\t"
"v_mfma_f32_16x16x32_bf16 a[164:167], a[228:231], a[204:207], a[164:167]\n\t"
"ds_read_b128 a[200:203], v223 offset:54272\n\t"
"v_fma_f32 v1, v3, s52, v101\n\t"
"v_fma_f32 v100, -v100, s66, v2\n\t"
"v_fma_f32 v2, v4, s52, v102\n\t"
"v_fma_f32 v101, -v101, s66, v3\n\t"
"v_fma_f32 v3, v5, s52, v103\n\t"
"v_fma_f32 v102, -v102, s66, v4\n\t"
"v_fma_f32 v103, -v103, s66, v5\n\t"
"v_mfma_f32_4x4x4_16b_bf16 v[228:231], v[228:229], v[220:221], 0\n\t"
"s_waitcnt vmcnt(9)\n\t"
"v_mfma_f32_4x4x4_16b_bf16 v[232:235], v[232:233], v[220:221], 0\n\t"
"buffer_load_dwordx4 v254, s[12:15], 0 offen offset:128 nt lds\n\t"
"v_fma_f32 v4, v6, s52, v104\n\t"
"v_fma_f32 v5, v7, s52, v105\n\t"
"s_add_u32 m0, s26, -128\n\t"
"Lst21_%=:\n\t"
"v_mfma_f32_16x16x32_bf16 v[0:3], a[232:235], a[168:171], v[0:3]\n\t"
"ds_read_b128 a[204:207], v222 offset:61952\n\t"
"v_fma_f32 v104, -v104, s66, v6\n\t"
"v_mfma_f32_16x16x32_bf16 v[0:3], a[244:247], a[176:179], v[0:3]\n\t"
"v_fma_f32 v6, v8, s52, v106\n\t"
"v_fma_f32 v105, -v105, s66, v7\n\t"
"v_mfma_f32_16x16x32_bf16 v[0:3], a[236:239], a[172:175], v[0:3]\n\t"
"s_mov_b64 exec, s[36:37]\n\t"
"buffer_load_dwordx4 v254, s[12:15], s27 offen offset:128 nt lds\n\t"
"s_mov_b64 exec, -1\n\t"
"v_fma_f32 v7, v9, s52, v107\n\t"
"v_fma_f32 v106, -v106, s66, v8\n\t"
"v_fma_f32 v107, -v107, s66, v9\n\t"
"v_fma_f32 v8, v10, s52, v108\n\t"
"v_fma_f32 v9, v11, s52, v109\n\t"
"s_add_u32 m0, s24, -128\n\t"
"s_add_u32 s28, s21, 0\n\t"
"v_mfma_f32_16x16x32_bf16 v[4:7], a[232:235], a[172:175], v[4:7]\n\t"
"buffer_load_dwordx4 v247, s[12:15], s28 offen offset:128 nt lds\n\t"
"v_mfma_f32_16x16x32_bf16 v[4:7], a[236:239], a[176:179], v[4:7]\n\t"
"s_add_u32 m0, s24, 6016\n\t"
"s_add_u32 s28, s21, 196608\n\t"
"v_fma_f32 v108, -v108, s66, v10\n\t"
"v_mfma_f32_16x16x32_bf16 v[4:7], a[244:247], a[180:183], v[4:7]\n\t"
"buffer_load_dwordx4 v247, s[12:15], s28 offen offset:128 nt lds\n\t"
"v_fma_f32 v10, v12, s52, v110\n\t"
"v_fma_f32 v109, -v109, s66, v11\n\t"
"v_fma_f32 v11, v13, s52, v111\n\t"
"v_fma_f32 v110, -v110, s66, v12\n\t"
"v_fma_f32 v111, -v111, s66, v13\n\t"
"v_cvt_pk_bf16_f32 v228, v228, v229\n\t"
"v_cvt_pk_bf16_f32 v229, v230, v231\n\t"
"v_fma_f32 v12, v14, s52, v112\n\t"
"v_fma_f32 v13, v15, s52, v113\n\t"
"v_cvt_pk_bf16_f32 v230, v232, v233\n\t"
"v_cvt_pk_bf16_f32 v231, v234, v235\n\t"
"s_add_u32 m0, s24, 12160\n\t"
"s_add_u32 s28, s21, 393216\n\t"
"s_waitcnt lgkmcnt(5)\n\t"
"v_mfma_f32_16x16x32_bf16 v[8:11], a[244:247], a[184:187], v[8:11]\n\t"
"buffer_load_dwordx4 v247, s[12:15], s28 offen offset:128 nt lds\n\t"
"v_mfma_f32_16x16x32_bf16 v[8:11], a[236:239], a[180:183], v[8:11]\n\t"
"s_add_u32 m0, s24, 19840\n\t"
"s_add_u32 s28, s21, 589824\n\t"
"v_fma_f32 v112, -v112, s66, v14\n\t"
"v_mfma_f32_16x16x32_bf16 v[8:11], a[232:235], a[176:179], v[8:11]\n\t"
"ds_write_b128 v245, v[228:231] offset:13312\n\t"
"v_fma_f32 v14, v16, s52, v114\n\t"
"v_fma_f32 v113, -v113, s66, v15\n\t"
"v_fma_f32 v15, v17, s52, v115\n\t"
"v_fma_f32 v114, -v114, s66, v16\n\t"
"v_fma_f32 v115, -v115, s66, v17\n\t"
"v_fma_f32 v16, v18, s52, v116\n\t"
"v_fma_f32 v17, v19, s52, v117\n\t"
"s_waitcnt lgkmcnt(5)\n\t"
"v_mfma_f32_16x16x32_bf16 v[12:15], a[232:235], a[180:183], v[12:15]\n\t"
"buffer_load_dwordx4 v247, s[12:15], s28 offen offset:128 nt lds\n\t"
"v_mfma_f32_16x16x32_bf16 v[12:15], a[236:239], a[184:187], v[12:15]\n\t"
"s_add_u32 m0, s24, 25984\n\t"
"s_add_u32 s28, s21, 786432\n\t"
"ds_read_b64_tr_b16 v[224:225], v246 offset:20992\n\t"
"v_mfma_f32_16x16x32_bf16 v[12:15], a[244:247], a[188:191], v[12:15]\n\t"
"buffer_load_dwordx4 v247, s[12:15], s28 offen offset:128 nt lds\n\t"
"v_fma_f32 v116, -v116, s66, v18\n\t"
"v_fma_f32 v18, v20, s52, v118\n\t"
"v_fma_f32 v117, -v117, s66, v19\n\t"
"v_fma_f32 v19, v21, s52, v119\n\t"
"v_fma_f32 v118, -v118, s66, v20\n\t"
"v_fma_f32 v119, -v119, s66, v21\n\t"
"v_fma_f32 v20, v22, s52, v120\n\t"
"v_fma_f32 v21, v23, s52, v121\n\t"
"s_add_u32 m0, s24, 32128\n\t"
"s_add_u32 s28, s21, 983040\n\t"
"s_waitcnt lgkmcnt(5)\n\t"
"v_mfma_f32_16x16x32_bf16 v[16:19], a[244:247], a[192:195], v[16:19]\n\t"
"buffer_load_dwordx4 v247, s[12:15], s28 offen offset:128 nt lds\n\t"
"v_mfma_f32_16x16x32_bf16 v[16:19], a[236:239], a[188:191], v[16:19]\n\t"
"ds_read_b64_tr_b16 v[236:237], v255 offset:20992\n\t"
"v_fma_f32 v120, -v120, s66, v22\n\t"
"v_mfma_f32_16x16x32_bf16 v[16:19], a[232:235], a[184:187], v[16:19]\n\t"
"v_fma_f32 v22, v24, s52, v122\n\t"
"v_fma_f32 v121, -v121, s66, v23\n\t"
"v_fma_f32 v23, v25, s52, v123\n\t"
"v_fma_f32 v122, -v122, s66, v24\n\t"
"v_fma_f32 v123, -v123, s66, v25\n\t"
"v_fma_f32 v24, v26, s52, v124\n\t"
"v_fma_f32 v25, v27, s52, v125\n\t"
"s_waitcnt lgkmcnt(5)\n\t"
"v_mfma_f32_16x16x32_bf16 v[20:23], a[232:235], a[188:191], v[20:23]\n\t"
"v_fma_f32 v124, -v124, s66, v26\n\t"
"v_fma_f32 v26, v28, s52, v126\n\t"
"v_mfma_f32_16x16x32_bf16 v[20:23], a[236:239], a[192:195], v[20:23]\n\t"
"v_fma_f32 v125, -v125, s66, v27\n\t"
"v_fma_f32 v27, v29, s52, v127\n\t"
"v_mfma_f32_16x16x32_bf16 v[20:23], a[244:247], a[196:199], v[20:23]\n\t"
"v_fma_f32 v126, -v126, s66, v28\n\t"
"v_fma_f32 v127, -v127, s66, v29\n\t"
"v_fma_f32 v28, v30, s52, v128\n\t"
"v_fma_f32 v29, v31, s52, v129\n\t"
"s_waitcnt lgkmcnt(4)\n\t"
"v_mfma_f32_16x16x32_bf16 v[24:27], a[244:247], a[200:203], v[24:27]\n\t"
"v_fma_f32 v128, -v128, s66, v30\n\t"
"v_fma_f32 v30, v32, s52, v130\n\t"
"v_mfma_f32_16x16x32_bf16 v[24:27], a[236:239], a[196:199], v[24:27]\n\t"
"v_fma_f32 v129, -v129, s66, v31\n\t"
"v_fma_f32 v31, v33, s52, v131\n\t"
"v_mfma_f32_16x16x32_bf16 v[24:27], a[232:235], a[192:195], v[24:27]\n\t"
"v_fma_f32 v130, -v130, s66, v32\n\t"
"v_fma_f32 v131, -v131, s66, v33\n\t"
"s_waitcnt lgkmcnt(3)\n\t"
"v_mfma_f32_16x16x32_bf16 v[28:31], a[232:235], a[196:199], v[28:31]\n\t"
"s_waitcnt lgkmcnt(1)\n\t"
"v_fma_f32 v32, v34, s52, v132\n\t"
"v_fma_f32 v33, v35, s52, v133\n\t"
"v_mfma_f32_16x16x32_bf16 v[28:31], a[236:239], a[200:203], v[28:31]\n\t"
"s_waitcnt lgkmcnt(0)\n\t"
"v_fma_f32 v132, -v132, s66, v34\n\t"
"v_fma_f32 v34, v36, s52, v134\n\t"
"v_mfma_f32_16x16x32_bf16 v[28:31], a[244:247], a[204:207], v[28:31]\n\t"
"v_fma_f32 v133, -v133, s66, v35\n\t"
"v_fma_f32 v35, v37, s52, v135\n\t"
"v_fma_f32 v134, -v134, s66, v36\n\t"
"v_fma_f32 v135, -v135, s66, v37\n\t"
"v_mfma_f32_4x4x4_16b_bf16 v[224:227], v[224:225], v[220:221], 0\n\t"
"s_waitcnt vmcnt(14)\n\t"
"v_mfma_f32_4x4x4_16b_bf16 v[236:239], v[236:237], v[220:221], 0\n\t"
"v_fma_f32 v36, v38, s52, v136\n\t"
"v_fma_f32 v37, v39, s52, v137\n\t"
"v_mfma_f32_16x16x32_bf16 v[32:35], a[240:243], a[168:171], v[32:35]\n\t"
"v_fma_f32 v136, -v136, s66, v38\n\t"
"v_fma_f32 v38, v40, s52, v138\n\t"
"v_mfma_f32_16x16x32_bf16 v[32:35], a[252:255], a[176:179], v[32:35]\n\t"
"v_fma_f32 v137, -v137, s66, v39\n\t"
"v_fma_f32 v39, v41, s52, v139\n\t"
"v_mfma_f32_16x16x32_bf16 v[32:35], a[248:251], a[172:175], v[32:35]\n\t"
"v_fma_f32 v138, -v138, s66, v40\n\t"
"v_fma_f32 v139, -v139, s66, v41\n\t"
"v_fma_f32 v40, v42, s52, v140\n\t"
"v_fma_f32 v41, v43, s52, v141\n\t"
"v_mfma_f32_16x16x32_bf16 v[36:39], a[240:243], a[172:175], v[36:39]\n\t"
"v_fma_f32 v140, -v140, s66, v42\n\t"
"v_fma_f32 v42, v44, s52, v142\n\t"
"v_mfma_f32_16x16x32_bf16 v[36:39], a[248:251], a[176:179], v[36:39]\n\t"
"v_fma_f32 v141, -v141, s66, v43\n\t"
"v_fma_f32 v43, v45, s52, v143\n\t"
"v_mfma_f32_16x16x32_bf16 v[36:39], a[252:255], a[180:183], v[36:39]\n\t"
"v_fma_f32 v142, -v142, s66, v44\n\t"
"v_fma_f32 v143, -v143, s66, v45\n\t"
"v_cvt_pk_bf16_f32 v224, v224, v225\n\t"
"v_cvt_pk_bf16_f32 v225, v226, v227\n\t"
"v_fma_f32 v44, v46, s52, v144\n\t"
"v_fma_f32 v45, v47, s52, v145\n\t"
"v_cvt_pk_bf16_f32 v226, v236, v237\n\t"
"v_cvt_pk_bf16_f32 v227, v238, v239\n\t"
"v_mfma_f32_16x16x32_bf16 v[40:43], a[252:255], a[184:187], v[40:43]\n\t"
"s_mov_b32 s20, 86016\n\t"
"v_fma_f32 v144, -v144, s66, v46\n\t"
"v_fma_f32 v46, v48, s52, v146\n\t"
"v_mfma_f32_16x16x32_bf16 v[40:43], a[248:251], a[180:183], v[40:43]\n\t"
"v_fma_f32 v145, -v145, s66, v47\n\t"
"v_fma_f32 v47, v49, s52, v147\n\t"
"v_mfma_f32_16x16x32_bf16 v[40:43], a[240:243], a[176:179], v[40:43]\n\t"
"ds_write_b128 v245, v[224:227] offset:20992\n\t"
"v_fma_f32 v146, -v146, s66, v48\n\t"
"v_fma_f32 v147, -v147, s66, v49\n\t"
"v_fma_f32 v48, v50, s52, v148\n\t"
"v_fma_f32 v49, v51, s52, v149\n\t"
"v_mfma_f32_16x16x32_bf16 v[44:47], a[240:243], a[180:183], v[44:47]\n\t"
"ds_read_b64_tr_b16 v[232:233], v246 offset:27136\n\t"
"v_mfma_f32_16x16x32_bf16 v[44:47], a[248:251], a[184:187], v[44:47]\n\t"
"ds_read_b64_tr_b16 v[228:229], v255 offset:27136\n\t"
"v_fma_f32 v148, -v148, s66, v50\n\t"
"v_mfma_f32_16x16x32_bf16 v[44:47], a[252:255], a[188:191], v[44:47]\n\t"
"v_fma_f32 v50, v52, s52, v150\n\t"
"v_fma_f32 v149, -v149, s66, v51\n\t"
"v_fma_f32 v51, v53, s52, v151\n\t"
"v_fma_f32 v150, -v150, s66, v52\n\t"
"v_fma_f32 v151, -v151, s66, v53\n\t"
"v_fma_f32 v52, v54, s52, v152\n\t"
"v_fma_f32 v53, v55, s52, v153\n\t"
"v_mfma_f32_16x16x32_bf16 v[48:51], a[252:255], a[192:195], v[48:51]\n\t"
"v_fma_f32 v152, -v152, s66, v54\n\t"
"v_fma_f32 v54, v56, s52, v154\n\t"
"v_mfma_f32_16x16x32_bf16 v[48:51], a[248:251], a[188:191], v[48:51]\n\t"
"Lpg16_%=:\n\t"
"s_cbranch_vccnz Lvr1_%=\n\t"
"v_fma_f32 v153, -v153, s66, v55\n\t"
"v_fma_f32 v55, v57, s52, v155\n\t"
"v_mfma_f32_16x16x32_bf16 v[48:51], a[240:243], a[184:187], v[48:51]\n\t"
"v_fma_f32 v154, -v154, s66, v56\n\t"
"v_fma_f32 v155, -v155, s66, v57\n\t"
"v_fma_f32 v56, v58, s52, v156\n\t"
"v_fma_f32 v57, v59, s52, v157\n\t"
"v_mfma_f32_16x16x32_bf16 v[52:55], a[240:243], a[188:191], v[52:55]\n\t"
"v_fma_f32 v156, -v156, s66, v58\n\t"
"v_fma_f32 v58, v60, s52, v158\n\t"
"v_mfma_f32_16x16x32_bf16 v[52:55], a[248:251], a[192:195], v[52:55]\n\t"
"v_fma_f32 v157, -v157, s66, v59\n\t"
"v_fma_f32 v59, v61, s52, v159\n\t"
"v_mfma_f32_16x16x32_bf16 v[52:55], a[252:255], a[196:199], v[52:55]\n\t"
"buffer_load_dwordx4 a[224:227], off, s[80:83], s20 offset:0 sc1\n\t"
"v_fma_f32 v158, -v158, s66, v60\n\t"
"v_fma_f32 v159, -v159, s66, v61\n\t"
"v_fma_f32 v60, v62, s52, v160\n\t"
"v_fma_f32 v61, v63, s52, v161\n\t"
"v_mfma_f32_16x16x32_bf16 v[56:59], a[252:255], a[200:203], v[56:59]\n\t"
"v_fma_f32 v160, -v160, s66, v62\n\t"
"v_fma_f32 v62, v64, s52, v162\n\t"
"v_mfma_f32_16x16x32_bf16 v[56:59], a[248:251], a[196:199], v[56:59]\n\t"
"v_fma_f32 v161, -v161, s66, v63\n\t"
"v_fma_f32 v63, v65, s52, v163\n\t"
"v_mfma_f32_16x16x32_bf16 v[56:59], a[240:243], a[192:195], v[56:59]\n\t"
"v_fma_f32 v162, -v162, s66, v64\n\t"
"v_fma_f32 v163, -v163, s66, v65\n\t"
"v_mfma_f32_16x16x32_bf16 v[60:63], a[240:243], a[196:199], v[60:63]\n\t"
"s_waitcnt lgkmcnt(1)\n\t"
"v_fma_f32 v64, v66, s52, v164\n\t"
"v_fma_f32 v65, v67, s52, v165\n\t"
"v_mfma_f32_16x16x32_bf16 v[60:63], a[248:251], a[200:203], v[60:63]\n\t"
"s_waitcnt lgkmcnt(0)\n\t"
"v_fma_f32 v164, -v164, s66, v66\n\t"
"v_fma_f32 v66, v68, s52, v166\n\t"
"v_mfma_f32_16x16x32_bf16 v[60:63], a[252:255], a[204:207], v[60:63]\n\t"
"v_fma_f32 v165, -v165, s66, v67\n\t"
"v_fma_f32 v67, v69, s52, v167\n\t"
"v_fma_f32 v166, -v166, s66, v68\n\t"
"v_fma_f32 v167, -v167, s66, v69\n\t"
"v_mfma_f32_4x4x4_16b_bf16 v[232:235], v[232:233], v[220:221], 0\n\t"
"s_waitcnt vmcnt(12)\n\t"
"v_mfma_f32_4x4x4_16b_bf16 v[228:231], v[228:229], v[220:221], 0\n\t"
"v_fma_f32 v68, v70, s52, v168\n\t"
"v_fma_f32 v69, v71, s52, v169\n\t"
"v_mfma_f32_16x16x32_bf16 v[64:67], v[240:243], a[168:171], v[64:67]\n\t"
"ds_read_b128 a[168:171], v222 offset:2304\n\t"
"v_fma_f32 v168, -v168, s66, v70\n\t"
"v_mfma_f32_16x16x32_bf16 v[64:67], a[212:215], a[176:179], v[64:67]\n\t"
"v_fma_f32 v70, v72, s52, v170\n\t"
"v_fma_f32 v169, -v169, s66, v71\n\t"
"v_mfma_f32_16x16x32_bf16 v[64:67], a[208:211], a[172:175], v[64:67]\n\t"
"v_fma_f32 v71, v73, s52, v171\n\t"
"v_fma_f32 v170, -v170, s66, v72\n\t"
"v_fma_f32 v171, -v171, s66, v73\n\t"
"v_fma_f32 v72, v74, s52, v172\n\t"
"v_fma_f32 v73, v75, s52, v173\n\t"
"v_mfma_f32_16x16x32_bf16 v[68:71], v[240:243], a[172:175], v[68:71]\n\t"
"ds_read_b128 a[172:175], v223 offset:8448\n\t"
"v_fma_f32 v172, -v172, s66, v74\n\t"
"v_mfma_f32_16x16x32_bf16 v[68:71], a[208:211], a[176:179], v[68:71]\n\t"
"v_fma_f32 v74, v76, s52, v174\n\t"
"v_fma_f32 v173, -v173, s66, v75\n\t"
"v_mfma_f32_16x16x32_bf16 v[68:71], a[212:215], a[180:183], v[68:71]\n\t"
"v_fma_f32 v75, v77, s52, v175\n\t"
"v_fma_f32 v174, -v174, s66, v76\n\t"
"v_fma_f32 v175, -v175, s66, v77\n\t"
"v_cvt_pk_bf16_f32 v232, v232, v233\n\t"
"v_cvt_pk_bf16_f32 v233, v234, v235\n\t"
"v_fma_f32 v76, v78, s52, v176\n\t"
"v_fma_f32 v77, v79, s52, v177\n\t"
"v_cvt_pk_bf16_f32 v234, v228, v229\n\t"
"v_cvt_pk_bf16_f32 v235, v230, v231\n\t"
"v_mfma_f32_16x16x32_bf16 v[72:75], a[212:215], a[184:187], v[72:75]\n\t"
"v_fma_f32 v176, -v176, s66, v78\n\t"
"v_fma_f32 v78, v80, s52, v178\n\t"
"v_mfma_f32_16x16x32_bf16 v[72:75], a[208:211], a[180:183], v[72:75]\n\t"
"v_fma_f32 v177, -v177, s66, v79\n\t"
"v_fma_f32 v79, v81, s52, v179\n\t"
"v_mfma_f32_16x16x32_bf16 v[72:75], v[240:243], a[176:179], v[72:75]\n\t"
"ds_write_b128 v245, v[232:235] offset:27136\n\t"
"v_fma_f32 v178, -v178, s66, v80\n\t"
"v_fma_f32 v179, -v179, s66, v81\n\t"
"v_fma_f32 v80, v82, s52, v180\n\t"
"v_fma_f32 v81, v83, s52, v181\n\t"
"v_mfma_f32_16x16x32_bf16 v[76:79], v[240:243], a[180:183], v[76:79]\n\t"
"ds_read_b128 a[176:179], v223 offset:14592\n\t"
"v_mfma_f32_16x16x32_bf16 v[76:79], a[208:211], a[184:187], v[76:79]\n\t"
"ds_read_b64_tr_b16 v[236:237], v246 offset:33280\n\t"
"v_fma_f32 v180, -v180, s66, v82\n\t"
"v_mfma_f32_16x16x32_bf16 v[76:79], a[212:215], a[188:191], v[76:79]\n\t"
"ds_read_b64_tr_b16 v[224:225], v255 offset:33280\n\t"
"v_fma_f32 v82, v84, s52, v182\n\t"
"v_fma_f32 v181, -v181, s66, v83\n\t"
"v_fma_f32 v83, v85, s52, v183\n\t"
"v_fma_f32 v182, -v182, s66, v84\n\t"
"v_fma_f32 v183, -v183, s66, v85\n\t"
"v_fma_f32 v84, v86, s52, v184\n\t"
"v_fma_f32 v85, v87, s52, v185\n\t"
"v_mfma_f32_16x16x32_bf16 v[80:83], a[212:215], a[192:195], v[80:83]\n\t"
"ds_read_b128 a[180:183], v222 offset:22272\n\t"
"v_fma_f32 v184, -v184, s66, v86\n\t"
"v_mfma_f32_16x16x32_bf16 v[80:83], a[208:211], a[188:191], v[80:83]\n\t"
"ds_read_b64_tr_b16 v[228:229], v246 offset:40960\n\t"
"v_fma_f32 v86, v88, s52, v186\n\t"
"v_mfma_f32_16x16x32_bf16 v[80:83], v[240:243], a[184:187], v[80:83]\n\t"
"ds_read_b64_tr_b16 v[232:233], v255 offset:40960\n\t"
"v_fma_f32 v185, -v185, s66, v87\n\t"
"v_fma_f32 v87, v89, s52, v187\n\t"
"v_fma_f32 v186, -v186, s66, v88\n\t"
"v_fma_f32 v187, -v187, s66, v89\n\t"
"v_fma_f32 v88, v90, s52, v188\n\t"
"v_fma_f32 v89, v91, s52, v189\n\t"
"v_mfma_f32_16x16x32_bf16 v[84:87], v[240:243], a[188:191], v[84:87]\n\t"
"ds_read_b128 a[184:187], v223 offset:28416\n\t"
"v_fma_f32 v188, -v188, s66, v90\n\t"
"v_mfma_f32_16x16x32_bf16 v[84:87], a[208:211], a[192:195], v[84:87]\n\t"
"ds_read_b128 a[188:191], v223 offset:34560\n\t"
"v_fma_f32 v90, v92, s52, v190\n\t"
"v_mfma_f32_16x16x32_bf16 v[84:87], a[212:215], a[196:199], v[84:87]\n\t"
"buffer_load_dwordx4 a[228:231], off, s[80:83], s20 offset:1024 sc1\n\t"
"v_fma_f32 v189, -v189, s66, v91\n\t"
"v_fma_f32 v91, v93, s52, v191\n\t"
"v_fma_f32 v190, -v190, s66, v92\n\t"
"v_fma_f32 v191, -v191, s66, v93\n\t"
"v_fma_f32 v92, v94, s52, v192\n\t"
"v_fma_f32 v93, v95, s52, v193\n\t"
"v_mfma_f32_16x16x32_bf16 v[88:91], a[212:215], a[200:203], v[88:91]\n\t"
"v_fma_f32 v192, -v192, s66, v94\n\t"
"v_fma_f32 v94, v96, s52, v194\n\t"
"v_mfma_f32_16x16x32_bf16 v[88:91], a[208:211], a[196:199], v[88:91]\n\t"
"v_fma_f32 v193, -v193, s66, v95\n\t"
"v_fma_f32 v95, v97, s52, v195\n\t"
"v_mfma_f32_16x16x32_bf16 v[88:91], v[240:243], a[192:195], v[88:91]\n\t"
"buffer_load_dwordx4 a[232:235], off, s[80:83], s20 offset:2048 sc1\n\t"
"v_fma_f32 v194, -v194, s66, v96\n\t"
"v_fma_f32 v195, -v195, s66, v97\n\t"
"s_mov_b32 s20, 98304\n\t"
"v_mfma_f32_16x16x32_bf16 v[92:95], v[240:243], a[196:199], v[92:95]\n\t"
"buffer_load_dwordx4 a[236:239], off, s[80:83], s20 offset:0 sc1\n\t"
"v_mfma_f32_16x16x32_bf16 v[92:95], a[208:211], a[200:203], v[92:95]\n\t"
"buffer_load_dwordx4 v[240:243], off, s[80:83], s20 offset:1024 sc1\n\t"
"v_mfma_f32_16x16x32_bf16 v[92:95], a[212:215], a[204:207], v[92:95]\n\t"
"s_waitcnt lgkmcnt(3)\n\t"
"ds_read_b128 a[192:195], v222 offset:42240\n\t"
"v_mfma_f32_4x4x4_16b_bf16 v[236:239], v[236:237], v[220:221], 0\n\t"
"s_waitcnt lgkmcnt(3)\n\t"
"v_mfma_f32_4x4x4_16b_bf16 v[224:227], v[224:225], v[220:221], 0\n\t"
"s_waitcnt vmcnt(13)\n\t"
"v_mfma_f32_4x4x4_16b_bf16 v[228:231], v[228:229], v[220:221], 0\n\t"
"v_mfma_f32_4x4x4_16b_bf16 v[232:235], v[232:233], v[220:221], 0\n\t"
"buffer_load_dwordx4 a[208:211], off, s[80:83], s20 offset:2048 sc1\n\t"
"s_mov_b32 s20, 331776\n\t"
"v_mfma_f32_16x16x32_bf16 v[100:103], v[248:251], a[168:171], v[100:103]\n\t"
"ds_read_b128 a[196:199], v223 offset:48384\n\t"
"v_cvt_pk_bf16_f32 v236, v236, v237\n\t"
"v_mfma_f32_16x16x32_bf16 v[100:103], a[220:223], a[176:179], v[100:103]\n\t"
"ds_read_b128 a[200:203], v223 offset:54528\n\t"
"v_cvt_pk_bf16_f32 v237, v238, v239\n\t"
"v_mfma_f32_16x16x32_bf16 v[100:103], a[216:219], a[172:175], v[100:103]\n\t"
"ds_read_b128 a[204:207], v222 offset:62208\n\t"
"v_cvt_pk_bf16_f32 v238, v224, v225\n\t"
"v_mfma_f32_16x16x32_bf16 v[104:107], v[248:251], a[172:175], v[104:107]\n\t"
"s_waitcnt lgkmcnt(5)\n\t"
"v_cvt_pk_bf16_f32 v239, v226, v227\n\t"
"v_cvt_pk_bf16_f32 v228, v228, v229\n\t"
"v_mfma_f32_16x16x32_bf16 v[104:107], a[216:219], a[176:179], v[104:107]\n\t"
"v_cvt_pk_bf16_f32 v229, v230, v231\n\t"
"v_mfma_f32_16x16x32_bf16 v[104:107], a[220:223], a[180:183], v[104:107]\n\t"
"s_waitcnt lgkmcnt(0)\n\t"
"s_barrier\n\t"
"v_mfma_f32_16x16x32_bf16 v[108:111], a[220:223], a[184:187], v[108:111]\n\t"
"v_cvt_pk_bf16_f32 v230, v232, v233\n\t"
"v_mfma_f32_16x16x32_bf16 v[108:111], a[216:219], a[180:183], v[108:111]\n\t"
"buffer_load_dwordx4 v[96:99], off, s[80:83], s20 offset:0 sc1\n\t"
"v_mfma_f32_16x16x32_bf16 v[108:111], v[248:251], a[176:179], v[108:111]\n\t"
"buffer_load_dwordx4 a[212:215], off, s[80:83], s20 offset:1024 sc1\n\t"
"v_cvt_pk_bf16_f32 v231, v234, v235\n\t"
"v_mfma_f32_16x16x32_bf16 v[112:115], v[248:251], a[180:183], v[112:115]\n\t"
"ds_read_b64_tr_b16 v[224:225], v246 offset:47104\n\t"
"v_mfma_f32_16x16x32_bf16 v[112:115], a[216:219], a[184:187], v[112:115]\n\t"
"buffer_load_dwordx4 a[240:243], off, s[80:83], s20 offset:2048 sc1\n\t"
"v_mfma_f32_16x16x32_bf16 v[112:115], a[220:223], a[188:191], v[112:115]\n\t"
"s_mov_b32 s20, 344064\n\t"
"ds_write_b128 v245, v[236:239] offset:33280\n\t"
"v_mfma_f32_16x16x32_bf16 v[116:119], a[220:223], a[192:195], v[116:119]\n\t"
"buffer_load_dwordx4 a[244:247], off, s[80:83], s20 offset:0 sc1\n\t"
"v_mfma_f32_16x16x32_bf16 v[116:119], a[216:219], a[188:191], v[116:119]\n\t"
"buffer_load_dwordx4 a[248:251], off, s[80:83], s20 offset:1024 sc1\n\t"
"v_mfma_f32_16x16x32_bf16 v[116:119], v[248:251], a[184:187], v[116:119]\n\t"
"ds_write_b128 v245, v[228:231] offset:40960\n\t"
"v_mfma_f32_16x16x32_bf16 v[120:123], v[248:251], a[188:191], v[120:123]\n\t"
"v_mfma_f32_16x16x32_bf16 v[120:123], a[216:219], a[192:195], v[120:123]\n\t"
"ds_read_b64_tr_b16 v[232:233], v255 offset:47104\n\t"
"v_mfma_f32_16x16x32_bf16 v[120:123], a[220:223], a[196:199], v[120:123]\n\t"
"ds_read_b64_tr_b16 v[236:237], v246 offset:53248\n\t"
"v_mfma_f32_16x16x32_bf16 v[124:127], a[220:223], a[200:203], v[124:127]\n\t"
"ds_read_b64_tr_b16 v[228:229], v255 offset:53248\n\t"
"v_mfma_f32_16x16x32_bf16 v[124:127], a[216:219], a[196:199], v[124:127]\n\t"
"buffer_load_dwordx4 a[252:255], off, s[80:83], s20 offset:2048 sc1\n\t"
"v_mfma_f32_16x16x32_bf16 v[124:127], v[248:251], a[192:195], v[124:127]\n\t"
"s_mov_b32 s20, 356352\n\t"
"v_mfma_f32_16x16x32_bf16 v[128:131], v[248:251], a[196:199], v[128:131]\n\t"
"buffer_load_dwordx4 v[248:251], off, s[80:83], s20 offset:0 sc1\n\t"
"v_mfma_f32_16x16x32_bf16 v[128:131], a[216:219], a[200:203], v[128:131]\n\t"
"s_waitcnt lgkmcnt(2)\n\t"
"v_mfma_f32_16x16x32_bf16 v[128:131], a[220:223], a[204:207], v[128:131]\n\t"
"s_waitcnt lgkmcnt(1)\n\t"
"v_mfma_f32_4x4x4_16b_bf16 v[224:227], v[224:225], v[220:221], 0\n\t"
"s_waitcnt lgkmcnt(0)\n\t"
"v_mfma_f32_4x4x4_16b_bf16 v[232:235], v[232:233], v[220:221], 0\n\t"
"s_waitcnt vmcnt(10)\n\t"
"v_mfma_f32_4x4x4_16b_bf16 v[236:239], v[236:237], v[220:221], 0\n\t"
"v_mfma_f32_4x4x4_16b_bf16 v[228:231], v[228:229], v[220:221], 0\n\t"
"buffer_load_dwordx4 a[216:219], off, s[80:83], s20 offset:1024 sc1\n\t"
"v_mfma_f32_16x16x32_bf16 v[132:135], a[224:227], a[168:171], v[132:135]\n\t"
"buffer_load_dwordx4 a[220:223], off, s[80:83], s20 offset:2048 sc1\n\t"
"v_mfma_f32_16x16x32_bf16 v[132:135], a[232:235], a[176:179], v[132:135]\n\t"
"v_cvt_pk_bf16_f32 v224, v224, v225\n\t"
"v_cvt_pk_bf16_f32 v225, v226, v227\n\t"
"v_mfma_f32_16x16x32_bf16 v[132:135], a[228:231], a[172:175], v[132:135]\n\t"
"v_cvt_pk_bf16_f32 v226, v232, v233\n\t"
"v_cvt_pk_bf16_f32 v227, v234, v235\n\t"
"v_mfma_f32_16x16x32_bf16 v[136:139], a[224:227], a[172:175], v[136:139]\n\t"
"v_cvt_pk_bf16_f32 v236, v236, v237\n\t"
"v_cvt_pk_bf16_f32 v237, v238, v239\n\t"
"v_mfma_f32_16x16x32_bf16 v[136:139], a[228:231], a[176:179], v[136:139]\n\t"
"s_mov_b32 s20, 368640\n\t"
"v_cvt_pk_bf16_f32 v238, v228, v229\n\t"
"v_cvt_pk_bf16_f32 v239, v230, v231\n\t"
"v_mfma_f32_16x16x32_bf16 v[136:139], a[232:235], a[180:183], v[136:139]\n\t"
"ds_write_b128 v245, v[224:227] offset:47104\n\t"
"v_mfma_f32_16x16x32_bf16 v[140:143], a[232:235], a[184:187], v[140:143]\n\t"
"v_mfma_f32_16x16x32_bf16 v[140:143], a[228:231], a[180:183], v[140:143]\n\t"
"v_mfma_f32_16x16x32_bf16 v[140:143], a[224:227], a[176:179], v[140:143]\n\t"
"ds_write_b128 v245, v[236:239] offset:53248\n\t"
"v_mfma_f32_16x16x32_bf16 v[144:147], a[224:227], a[180:183], v[144:147]\n\t"
"v_mfma_f32_16x16x32_bf16 v[144:147], a[228:231], a[184:187], v[144:147]\n\t"
"buffer_load_dwordx4 v[236:239], off, s[80:83], s20 offset:0 sc1\n\t"
"v_mfma_f32_16x16x32_bf16 v[144:147], a[232:235], a[188:191], v[144:147]\n\t"
"v_mfma_f32_16x16x32_bf16 v[148:151], a[232:235], a[192:195], v[148:151]\n\t"
"v_mfma_f32_16x16x32_bf16 v[148:151], a[228:231], a[188:191], v[148:151]\n\t"
"v_mfma_f32_16x16x32_bf16 v[148:151], a[224:227], a[184:187], v[148:151]\n\t"
"s_add_u32 m0, s24, 1408\n\t"
"s_add_u32 s28, s21, 3072\n\t"
"v_mfma_f32_16x16x32_bf16 v[152:155], a[224:227], a[188:191], v[152:155]\n\t"
"v_mfma_f32_16x16x32_bf16 v[152:155], a[228:231], a[192:195], v[152:155]\n\t"
"v_mfma_f32_16x16x32_bf16 v[152:155], a[232:235], a[196:199], v[152:155]\n\t"
"v_mfma_f32_16x16x32_bf16 v[156:159], a[232:235], a[200:203], v[156:159]\n\t"
"buffer_load_dwordx4 v247, s[12:15], s28 offen offset:128 nt lds\n\t"
"v_mfma_f32_16x16x32_bf16 v[156:159], a[228:231], a[196:199], v[156:159]\n\t"
"s_add_u32 m0, s24, 7552\n\t"
"s_add_u32 s28, s21, 199680\n\t"
"v_mfma_f32_16x16x32_bf16 v[156:159], a[224:227], a[192:195], v[156:159]\n\t"
"buffer_load_dwordx4 v247, s[12:15], s28 offen offset:128 nt lds\n\t"
"v_mfma_f32_16x16x32_bf16 v[160:163], a[224:227], a[196:199], v[160:163]\n\t"
"s_waitcnt vmcnt(12)\n\t"
"s_add_u32 m0, s24, 13696\n\t"
"s_add_u32 s28, s21, 396288\n\t"
"v_mfma_f32_16x16x32_bf16 v[160:163], a[228:231], a[200:203], v[160:163]\n\t"
"buffer_load_dwordx4 v247, s[12:15], s28 offen offset:128 nt lds\n\t"
"v_mfma_f32_16x16x32_bf16 v[160:163], a[232:235], a[204:207], v[160:163]\n\t"
"s_add_u32 m0, s24, 21376\n\t"
"s_add_u32 s28, s21, 592896\n\t"
"v_mfma_f32_16x16x32_bf16 v[164:167], a[236:239], a[168:171], v[164:167]\n\t"
"buffer_load_dwordx4 v247, s[12:15], s28 offen offset:128 nt lds\n\t"
"v_mfma_f32_16x16x32_bf16 v[164:167], a[208:211], a[176:179], v[164:167]\n\t"
"s_add_u32 m0, s24, 27520\n\t"
"s_add_u32 s28, s21, 789504\n\t"
"v_mfma_f32_16x16x32_bf16 v[164:167], v[240:243], a[172:175], v[164:167]\n\t"
"s_waitcnt lgkmcnt(0)\n\t"
"s_barrier\n\t"
"v_mfma_f32_16x16x32_bf16 v[168:171], a[236:239], a[172:175], v[168:171]\n\t"
"ds_read_b128 a[168:171], v222 offset:2560\n\t"
"v_mfma_f32_16x16x32_bf16 v[168:171], v[240:243], a[176:179], v[168:171]\n\t"
"buffer_load_dwordx4 v247, s[12:15], s28 offen offset:128 nt lds\n\t"
"v_mfma_f32_16x16x32_bf16 v[168:171], a[208:211], a[180:183], v[168:171]\n\t"
"s_add_u32 m0, s24, 33664\n\t"
"s_add_u32 s28, s21, 986112\n\t"
"ds_read_b128 a[172:175], v223 offset:8704\n\t"
"v_mfma_f32_16x16x32_bf16 v[172:175], a[208:211], a[184:187], v[172:175]\n\t"
"buffer_load_dwordx4 v247, s[12:15], s28 offen offset:128 nt lds\n\t"
"v_mfma_f32_16x16x32_bf16 v[172:175], v[240:243], a[180:183], v[172:175]\n\t"
"v_mfma_f32_16x16x32_bf16 v[172:175], a[236:239], a[176:179], v[172:175]\n\t"
"ds_read_b128 a[176:179], v223 offset:14848\n\t"
"v_mfma_f32_16x16x32_bf16 v[176:179], a[236:239], a[180:183], v[176:179]\n\t"
"ds_read_b128 a[180:183], v222 offset:22528\n\t"
"v_mfma_f32_16x16x32_bf16 v[176:179], v[240:243], a[184:187], v[176:179]\n\t"
"v_mfma_f32_16x16x32_bf16 v[176:179], a[208:211], a[188:191], v[176:179]\n\t"
"v_mfma_f32_16x16x32_bf16 v[180:183], a[208:211], a[192:195], v[180:183]\n\t"
"v_mfma_f32_16x16x32_bf16 v[180:183], v[240:243], a[188:191], v[180:183]\n\t"
"v_mfma_f32_16x16x32_bf16 v[180:183], a[236:239], a[184:187], v[180:183]\n\t"
"ds_read_b128 a[184:187], v223 offset:28672\n\t"
"v_mfma_f32_16x16x32_bf16 v[184:187], a[236:239], a[188:191], v[184:187]\n\t"
"ds_read_b128 a[188:191], v223 offset:34816\n\t"
"v_mfma_f32_16x16x32_bf16 v[184:187], v[240:243], a[192:195], v[184:187]\n\t"
"v_mfma_f32_16x16x32_bf16 v[184:187], a[208:211], a[196:199], v[184:187]\n\t"
"v_mfma_f32_16x16x32_bf16 v[188:191], a[208:211], a[200:203], v[188:191]\n\t"
"v_mfma_f32_16x16x32_bf16 v[188:191], v[240:243], a[196:199], v[188:191]\n\t"
"v_mfma_f32_16x16x32_bf16 v[188:191], a[236:239], a[192:195], v[188:191]\n\t"
"ds_read_b128 a[192:195], v222 offset:42496\n\t"
"v_mfma_f32_16x16x32_bf16 v[192:195], a[236:239], a[196:199], v[192:195]\n\t"
"s_waitcnt vmcnt(13) lgkmcnt(4)\n\t"
"ds_read_b128 a[196:199], v223 offset:48640\n\t"
"v_mfma_f32_16x16x32_bf16 v[192:195], v[240:243], a[200:203], v[192:195]\n\t"
"ds_read_b128 a[200:203], v223 offset:54784\n\t"
"v_mfma_f32_16x16x32_bf16 v[192:195], a[208:211], a[204:207], v[192:195]\n\t"
"ds_read_b128 a[204:207], v222 offset:62464\n\t"
"Lst22_%=:\n\t"
"v_mfma_f32_16x16x32_bf16 v[0:3], v[96:99], a[168:171], v[0:3]\n\t"
"s_waitcnt lgkmcnt(6)\n\t"
"v_mfma_f32_16x16x32_bf16 v[0:3], a[240:243], a[176:179], v[0:3]\n\t"
"v_mfma_f32_16x16x32_bf16 v[0:3], a[212:215], a[172:175], v[0:3]\n\t"
"v_mfma_f32_16x16x32_bf16 v[4:7], v[96:99], a[172:175], v[4:7]\n\t"
"s_waitcnt lgkmcnt(5)\n\t"
"v_mfma_f32_16x16x32_bf16 v[4:7], a[212:215], a[176:179], v[4:7]\n\t"
"v_mfma_f32_16x16x32_bf16 v[4:7], a[240:243], a[180:183], v[4:7]\n\t"
"v_mfma_f32_16x16x32_bf16 v[8:11], a[240:243], a[184:187], v[8:11]\n\t"
"s_waitcnt lgkmcnt(4)\n\t"
"v_mfma_f32_16x16x32_bf16 v[8:11], a[212:215], a[180:183], v[8:11]\n\t"
"v_mfma_f32_16x16x32_bf16 v[8:11], v[96:99], a[176:179], v[8:11]\n\t"
"v_mfma_f32_16x16x32_bf16 v[12:15], v[96:99], a[180:183], v[12:15]\n\t"
"s_waitcnt lgkmcnt(3)\n\t"
"v_mfma_f32_16x16x32_bf16 v[12:15], a[212:215], a[184:187], v[12:15]\n\t"
"v_mfma_f32_16x16x32_bf16 v[12:15], a[240:243], a[188:191], v[12:15]\n\t"
"v_mfma_f32_16x16x32_bf16 v[16:19], a[240:243], a[192:195], v[16:19]\n\t"
"s_waitcnt lgkmcnt(2)\n\t"
"v_mfma_f32_16x16x32_bf16 v[16:19], a[212:215], a[188:191], v[16:19]\n\t"
"v_mfma_f32_16x16x32_bf16 v[16:19], v[96:99], a[184:187], v[16:19]\n\t"
"buffer_load_dwordx4 a[224:227], off, s[80:83], s20 offset:1024 sc1\n\t"
"v_mfma_f32_16x16x32_bf16 v[20:23], v[96:99], a[188:191], v[20:23]\n\t"
"s_waitcnt lgkmcnt(1)\n\t"
"v_mfma_f32_16x16x32_bf16 v[20:23], a[212:215], a[192:195], v[20:23]\n\t"
"v_mfma_f32_16x16x32_bf16 v[20:23], a[240:243], a[196:199], v[20:23]\n\t"
"v_mfma_f32_16x16x32_bf16 v[24:27], a[240:243], a[200:203], v[24:27]\n\t"
"s_waitcnt lgkmcnt(0)\n\t"
"v_mfma_f32_16x16x32_bf16 v[24:27], a[212:215], a[196:199], v[24:27]\n\t"
"v_mfma_f32_16x16x32_bf16 v[24:27], v[96:99], a[192:195], v[24:27]\n\t"
"v_mfma_f32_16x16x32_bf16 v[28:31], v[96:99], a[196:199], v[28:31]\n\t"
"s_waitcnt vmcnt(11)\n\t"
"v_mfma_f32_16x16x32_bf16 v[28:31], a[212:215], a[200:203], v[28:31]\n\t"
"v_mfma_f32_16x16x32_bf16 v[28:31], a[240:243], a[204:207], v[28:31]\n\t"
"v_mfma_f32_16x16x32_bf16 v[32:35], a[244:247], a[168:171], v[32:35]\n\t"
"v_mfma_f32_16x16x32_bf16 v[32:35], a[252:255], a[176:179], v[32:35]\n\t"
"v_mfma_f32_16x16x32_bf16 v[32:35], a[248:251], a[172:175], v[32:35]\n\t"
"v_mfma_f32_16x16x32_bf16 v[36:39], a[244:247], a[172:175], v[36:39]\n\t"
"v_mfma_f32_16x16x32_bf16 v[36:39], a[248:251], a[176:179], v[36:39]\n\t"
"v_mfma_f32_16x16x32_bf16 v[36:39], a[252:255], a[180:183], v[36:39]\n\t"
"v_mfma_f32_16x16x32_bf16 v[40:43], a[252:255], a[184:187], v[40:43]\n\t"
"v_mfma_f32_16x16x32_bf16 v[40:43], a[248:251], a[180:183], v[40:43]\n\t"
"v_mfma_f32_16x16x32_bf16 v[40:43], a[244:247], a[176:179], v[40:43]\n\t"
"buffer_load_dwordx4 v[96:99], off, s[80:83], s20 offset:2048 sc1\n\t"
"v_mfma_f32_16x16x32_bf16 v[44:47], a[244:247], a[180:183], v[44:47]\n\t"
"s_mov_b32 s20, 380928\n\t"
"v_mfma_f32_16x16x32_bf16 v[44:47], a[248:251], a[184:187], v[44:47]\n\t"
"v_mfma_f32_16x16x32_bf16 v[44:47], a[252:255], a[188:191], v[44:47]\n\t"
"v_mfma_f32_16x16x32_bf16 v[48:51], a[252:255], a[192:195], v[48:51]\n\t"
"buffer_load_dwordx4 a[212:215], off, s[80:83], s20 offset:0 sc1\n\t"
"v_mfma_f32_16x16x32_bf16 v[48:51], a[248:251], a[188:191], v[48:51]\n\t"
"v_mfma_f32_16x16x32_bf16 v[48:51], a[244:247], a[184:187], v[48:51]\n\t"
"v_mfma_f32_16x16x32_bf16 v[52:55], a[244:247], a[188:191], v[52:55]\n\t"
"v_mfma_f32_16x16x32_bf16 v[52:55], a[248:251], a[192:195], v[52:55]\n\t"
"buffer_load_dwordx4 a[228:231], off, s[80:83], s20 offset:1024 sc1\n\t"
"v_mfma_f32_16x16x32_bf16 v[52:55], a[252:255], a[196:199], v[52:55]\n\t"
"v_mfma_f32_16x16x32_bf16 v[56:59], a[252:255], a[200:203], v[56:59]\n\t"
"v_mfma_f32_16x16x32_bf16 v[56:59], a[248:251], a[196:199], v[56:59]\n\t"
"v_mfma_f32_16x16x32_bf16 v[56:59], a[244:247], a[192:195], v[56:59]\n\t"
"buffer_load_dwordx4 a[232:235], off, s[80:83], s20 offset:2048 sc1\n\t"
"v_mfma_f32_16x16x32_bf16 v[60:63], a[244:247], a[196:199], v[60:63]\n\t"
"s_waitcnt vmcnt(12)\n\t"
"s_mov_b32 s20, 393216\n\t"
"v_mfma_f32_16x16x32_bf16 v[60:63], a[248:251], a[200:203], v[60:63]\n\t"
"v_mfma_f32_16x16x32_bf16 v[60:63], a[252:255], a[204:207], v[60:63]\n\t"
"v_mfma_f32_16x16x32_bf16 v[64:67], v[248:251], a[168:171], v[64:67]\n\t"
"buffer_load_dwordx4 a[236:239], off, s[80:83], s20 offset:0 sc1\n\t"
"v_mfma_f32_16x16x32_bf16 v[64:67], a[220:223], a[176:179], v[64:67]\n\t"
"ds_read_b128 a[168:171], v222 offset:2816\n\t"
"v_mfma_f32_16x16x32_bf16 v[64:67], a[216:219], a[172:175], v[64:67]\n\t"
"v_mfma_f32_16x16x32_bf16 v[68:71], v[248:251], a[172:175], v[68:71]\n\t"
"ds_read_b128 a[172:175], v223 offset:8960\n\t"
"v_mfma_f32_16x16x32_bf16 v[68:71], a[216:219], a[176:179], v[68:71]\n\t"
"buffer_load_dwordx4 a[240:243], off, s[80:83], s20 offset:1024 sc1\n\t"
"v_mfma_f32_16x16x32_bf16 v[68:71], a[220:223], a[180:183], v[68:71]\n\t"
"v_mfma_f32_16x16x32_bf16 v[72:75], a[220:223], a[184:187], v[72:75]\n\t"
"v_mfma_f32_16x16x32_bf16 v[72:75], a[216:219], a[180:183], v[72:75]\n\t"
"v_mfma_f32_16x16x32_bf16 v[72:75], v[248:251], a[176:179], v[72:75]\n\t"
"ds_read_b128 a[176:179], v223 offset:15104\n\t"
"v_mfma_f32_16x16x32_bf16 v[76:79], v[248:251], a[180:183], v[76:79]\n\t"
"buffer_load_dwordx4 a[244:247], off, s[80:83], s20 offset:2048 sc1\n\t"
"v_mfma_f32_16x16x32_bf16 v[76:79], a[216:219], a[184:187], v[76:79]\n\t"
"s_mov_b32 s20, 405504\n\t"
"ds_read_b128 a[180:183], v222 offset:22784\n\t"
"v_mfma_f32_16x16x32_bf16 v[76:79], a[220:223], a[188:191], v[76:79]\n\t"
"v_mfma_f32_16x16x32_bf16 v[80:83], a[220:223], a[192:195], v[80:83]\n\t"
"buffer_load_dwordx4 a[248:251], off, s[80:83], s20 offset:0 sc1\n\t"
"v_mfma_f32_16x16x32_bf16 v[80:83], a[216:219], a[188:191], v[80:83]\n\t"
"v_mfma_f32_16x16x32_bf16 v[80:83], v[248:251], a[184:187], v[80:83]\n\t"
"ds_read_b128 a[184:187], v223 offset:28928\n\t"
"v_mfma_f32_16x16x32_bf16 v[84:87], v[248:251], a[188:191], v[84:87]\n\t"
"ds_read_b128 a[188:191], v223 offset:35072\n\t"
"v_mfma_f32_16x16x32_bf16 v[84:87], a[216:219], a[192:195], v[84:87]\n\t"
"buffer_load_dwordx4 a[252:255], off, s[80:83], s20 offset:1024 sc1\n\t"
"v_mfma_f32_16x16x32_bf16 v[84:87], a[220:223], a[196:199], v[84:87]\n\t"
"v_mfma_f32_16x16x32_bf16 v[88:91], a[220:223], a[200:203], v[88:91]\n\t"
"v_mfma_f32_16x16x32_bf16 v[88:91], a[216:219], a[196:199], v[88:91]\n\t"
"v_mfma_f32_16x16x32_bf16 v[88:91], v[248:251], a[192:195], v[88:91]\n\t"
"ds_read_b128 a[192:195], v222 offset:42752\n\t"
"v_mfma_f32_16x16x32_bf16 v[92:95], v[248:251], a[196:199], v[92:95]\n\t"
"buffer_load_dwordx4 v[248:251], off, s[80:83], s20 offset:2048 sc1\n\t"
"v_mfma_f32_16x16x32_bf16 v[92:95], a[216:219], a[200:203], v[92:95]\n\t"
"s_waitcnt vmcnt(9) lgkmcnt(4)\n\t"
"ds_read_b128 a[196:199], v223 offset:48896\n\t"
"s_mov_b32 s20, 417792\n\t"
"v_mfma_f32_16x16x32_bf16 v[92:95], a[220:223], a[204:207], v[92:95]\n\t"
"global_load_dwordx4 v[240:243], v252, s[48:49] offset:2048\n\t"
"v_mfma_f32_16x16x32_bf16 v[100:103], v[236:239], a[168:171], v[100:103]\n\t"
"buffer_load_dwordx4 a[216:219], off, s[80:83], s20 offset:0 sc1\n\t"
"v_mfma_f32_16x16x32_bf16 v[100:103], v[96:99], a[176:179], v[100:103]\n\t"
"s_waitcnt lgkmcnt(4)\n\t"
"ds_read_b128 a[200:203], v223 offset:55040\n\t"
"v_mfma_f32_16x16x32_bf16 v[100:103], a[224:227], a[172:175], v[100:103]\n\t"
"ds_read_b128 a[204:207], v222 offset:62720\n\t"
"v_mfma_f32_16x16x32_bf16 v[104:107], v[236:239], a[172:175], v[104:107]\n\t"
"buffer_load_dwordx4 a[220:223], off, s[80:83], s20 offset:1024 sc1\n\t"
"v_mfma_f32_16x16x32_bf16 v[104:107], a[224:227], a[176:179], v[104:107]\n\t"
"global_load_dwordx4 a[208:211], v252, s[46:47] offset:1024\n\t"
"v_mfma_f32_16x16x32_bf16 v[104:107], v[96:99], a[180:183], v[104:107]\n\t"
"s_waitcnt lgkmcnt(5)\n\t"
"v_mfma_f32_16x16x32_bf16 v[108:111], v[96:99], a[184:187], v[108:111]\n\t"
"s_waitcnt lgkmcnt(4)\n\t"
"v_mfma_f32_16x16x32_bf16 v[108:111], a[224:227], a[180:183], v[108:111]\n\t"
"v_mfma_f32_16x16x32_bf16 v[108:111], v[236:239], a[176:179], v[108:111]\n\t"
"v_mfma_f32_16x16x32_bf16 v[112:115], v[236:239], a[180:183], v[112:115]\n\t"
"s_waitcnt lgkmcnt(3)\n\t"
"v_mfma_f32_16x16x32_bf16 v[112:115], a[224:227], a[184:187], v[112:115]\n\t"
"v_mfma_f32_16x16x32_bf16 v[112:115], v[96:99], a[188:191], v[112:115]\n\t"
"v_mfma_f32_16x16x32_bf16 v[116:119], v[96:99], a[192:195], v[116:119]\n\t"
"s_waitcnt lgkmcnt(2)\n\t"
"v_mfma_f32_16x16x32_bf16 v[116:119], a[224:227], a[188:191], v[116:119]\n\t"
"v_mfma_f32_16x16x32_bf16 v[116:119], v[236:239], a[184:187], v[116:119]\n\t"
"v_mfma_f32_16x16x32_bf16 v[120:123], v[236:239], a[188:191], v[120:123]\n\t"
"s_waitcnt lgkmcnt(1)\n\t"
"v_mfma_f32_16x16x32_bf16 v[120:123], a[224:227], a[192:195], v[120:123]\n\t"
"v_mfma_f32_16x16x32_bf16 v[120:123], v[96:99], a[196:199], v[120:123]\n\t"
"v_mfma_f32_16x16x32_bf16 v[124:127], v[96:99], a[200:203], v[124:127]\n\t"
"s_waitcnt lgkmcnt(0)\n\t"
"v_mfma_f32_16x16x32_bf16 v[124:127], a[224:227], a[196:199], v[124:127]\n\t"
"v_mfma_f32_16x16x32_bf16 v[124:127], v[236:239], a[192:195], v[124:127]\n\t"
"v_mfma_f32_16x16x32_bf16 v[128:131], v[236:239], a[196:199], v[128:131]\n\t"
"s_waitcnt vmcnt(10)\n\t"
"v_mfma_f32_16x16x32_bf16 v[128:131], a[224:227], a[200:203], v[128:131]\n\t"
"buffer_load_dwordx4 a[224:227], off, s[80:83], s20 offset:2048 sc1\n\t"
"v_mfma_f32_16x16x32_bf16 v[128:131], v[96:99], a[204:207], v[128:131]\n\t"
"s_mov_b32 s20, 430080\n\t"
"v_mfma_f32_16x16x32_bf16 v[132:135], a[212:215], a[168:171], v[132:135]\n\t"
"v_mfma_f32_16x16x32_bf16 v[132:135], a[232:235], a[176:179], v[132:135]\n\t"
"buffer_load_dwordx4 v[96:99], off, s[80:83], s20 offset:0 sc1\n\t"
"v_mfma_f32_16x16x32_bf16 v[132:135], a[228:231], a[172:175], v[132:135]\n\t"
"v_mfma_f32_16x16x32_bf16 v[136:139], a[212:215], a[172:175], v[136:139]\n\t"
"v_mfma_f32_16x16x32_bf16 v[136:139], a[228:231], a[176:179], v[136:139]\n\t"
"v_mfma_f32_16x16x32_bf16 v[136:139], a[232:235], a[180:183], v[136:139]\n\t"
"s_barrier\n\t"
"v_mfma_f32_16x16x32_bf16 v[140:143], a[232:235], a[184:187], v[140:143]\n\t"
"s_add_u32 m0, s24, 39808\n\t"
"s_add_u32 s28, s21, 1179648\n\t"
"v_mfma_f32_16x16x32_bf16 v[140:143], a[228:231], a[180:183], v[140:143]\n\t"
"v_mfma_f32_16x16x32_bf16 v[140:143], a[212:215], a[176:179], v[140:143]\n\t"
"s_waitcnt vmcnt(5)\n\t"
"ds_read_b64_tr_b16 v[232:233], v240 offset:0\n\t"
"v_mfma_f32_16x16x32_bf16 v[144:147], a[212:215], a[180:183], v[144:147]\n\t"
"buffer_load_dwordx4 v247, s[12:15], s28 offen offset:128 nt lds\n\t"
"v_mfma_f32_16x16x32_bf16 v[144:147], a[228:231], a[184:187], v[144:147]\n\t"
"s_add_u32 m0, s24, 45952\n\t"
"s_add_u32 s28, s21, 1376256\n\t"
"ds_read_b64_tr_b16 v[234:235], v240 offset:1536\n\t"
"v_mfma_f32_16x16x32_bf16 v[144:147], a[232:235], a[188:191], v[144:147]\n\t"
"buffer_load_dwordx4 v247, s[12:15], s28 offen offset:128 nt lds\n\t"
"v_mfma_f32_16x16x32_bf16 v[148:151], a[232:235], a[192:195], v[148:151]\n\t"
"s_add_u32 m0, s24, 52096\n\t"
"s_add_u32 s28, s21, 1572864\n\t"
"ds_read_b64_tr_b16 v[228:229], v241 offset:0\n\t"
"v_mfma_f32_16x16x32_bf16 v[148:151], a[228:231], a[188:191], v[148:151]\n\t"
"buffer_load_dwordx4 v247, s[12:15], s28 offen offset:128 nt lds\n\t"
"v_mfma_f32_16x16x32_bf16 v[148:151], a[212:215], a[184:187], v[148:151]\n\t"
"s_add_u32 m0, s24, 41344\n\t"
"s_add_u32 s28, s21, 1182720\n\t"
"ds_read_b64_tr_b16 v[230:231], v241 offset:1536\n\t"
"v_mfma_f32_16x16x32_bf16 v[152:155], a[212:215], a[188:191], v[152:155]\n\t"
"buffer_load_dwordx4 v247, s[12:15], s28 offen offset:128 nt lds\n\t"
"v_mfma_f32_16x16x32_bf16 v[152:155], a[228:231], a[192:195], v[152:155]\n\t"
"s_add_u32 m0, s24, 47488\n\t"
"s_add_u32 s28, s21, 1379328\n\t"
"ds_read_b64_tr_b16 v[224:225], v240 offset:19968\n\t"
"v_mfma_f32_16x16x32_bf16 v[152:155], a[232:235], a[196:199], v[152:155]\n\t"
"ds_read_b64_tr_b16 v[226:227], v240 offset:21504\n\t"
"v_mfma_f32_16x16x32_bf16 v[156:159], a[232:235], a[200:203], v[156:159]\n\t"
"buffer_load_dwordx4 v247, s[12:15], s28 offen offset:128 nt lds\n\t"
"v_mfma_f32_16x16x32_bf16 v[156:159], a[228:231], a[196:199], v[156:159]\n\t"
"s_add_u32 m0, s24, 53632\n\t"
"s_add_u32 s28, s21, 1575936\n\t"
"ds_read_b64_tr_b16 v[236:237], v241 offset:19968\n\t"
"v_mfma_f32_16x16x32_bf16 v[156:159], a[212:215], a[192:195], v[156:159]\n\t"
"ds_read_b64_tr_b16 v[238:239], v241 offset:21504\n\t"
"v_mfma_f32_16x16x32_bf16 v[160:163], a[212:215], a[196:199], v[160:163]\n\t"
"buffer_load_dwordx4 v247, s[12:15], s28 offen offset:128 nt lds\n\t"
"v_mfma_f32_16x16x32_bf16 v[160:163], a[228:231], a[200:203], v[160:163]\n\t"
"s_waitcnt vmcnt(8) lgkmcnt(4)\n\t"
"v_mfma_f32_16x16x32_bf16 v[160:163], a[232:235], a[204:207], v[160:163]\n\t"
"s_waitcnt lgkmcnt(2)\n\t"
"v_mfma_f32_16x16x32_bf16 v[232:235], v[232:235], a[208:211], 0\n\t"
"v_mfma_f32_16x16x32_bf16 v[228:231], v[228:231], a[208:211], 0\n\t"
"s_waitcnt lgkmcnt(0)\n\t"
"v_mfma_f32_16x16x32_bf16 v[224:227], v[224:227], a[208:211], 0\n\t"
"v_mfma_f32_16x16x32_bf16 v[236:239], v[236:239], a[208:211], 0\n\t"
"v_mfma_f32_16x16x32_bf16 v[164:167], a[236:239], a[168:171], v[164:167]\n\t"
"ds_read_b128 a[168:171], v222 offset:1024\n\t"
"v_mfma_f32_16x16x32_bf16 v[164:167], a[244:247], a[176:179], v[164:167]\n\t"
"v_cvt_pk_bf16_f32 v232, v232, v233\n\t"
"v_cvt_pk_bf16_f32 v233, v234, v235\n\t"
"v_mfma_f32_16x16x32_bf16 v[164:167], a[240:243], a[172:175], v[164:167]\n\t"
"v_cvt_pk_bf16_f32 v234, v228, v229\n\t"
"v_cvt_pk_bf16_f32 v235, v230, v231\n\t"
"v_mfma_f32_16x16x32_bf16 v[168:171], a[236:239], a[172:175], v[168:171]\n\t"
"ds_read_b128 a[172:175], v223 offset:7168\n\t"
"v_cvt_pk_bf16_f32 v224, v224, v225\n\t"
"v_mfma_f32_16x16x32_bf16 v[168:171], a[240:243], a[176:179], v[168:171]\n\t"
"v_cvt_pk_bf16_f32 v225, v226, v227\n\t"
"v_cvt_pk_bf16_f32 v226, v236, v237\n\t"
"v_mfma_f32_16x16x32_bf16 v[168:171], a[244:247], a[180:183], v[168:171]\n\t"
"ds_write_b128 v242, v[232:235] offset:24576\n\t"
"v_mfma_f32_16x16x32_bf16 v[172:175], a[244:247], a[184:187], v[172:175]\n\t"
"v_cvt_pk_bf16_f32 v227, v238, v239\n\t"
"v_add_u32 v240, 59904, v240\n\t"
"v_mfma_f32_16x16x32_bf16 v[172:175], a[240:243], a[180:183], v[172:175]\n\t"
"Lpg17_%=:\n\t"
"s_cbranch_vccnz Lvr2_%=\n\t"
"v_add_u32 v241, 59904, v241\n\t"
"v_mfma_f32_16x16x32_bf16 v[172:175], a[236:239], a[176:179], v[172:175]\n\t"
"ds_write_b128 v242, v[224:227] offset:26112\n\t"
"v_mfma_f32_16x16x32_bf16 v[176:179], a[236:239], a[180:183], v[176:179]\n\t"
"v_mfma_f32_16x16x32_bf16 v[176:179], a[240:243], a[184:187], v[176:179]\n\t"
"ds_read_b128 a[176:179], v223 offset:13312\n\t"
"v_mfma_f32_16x16x32_bf16 v[176:179], a[244:247], a[188:191], v[176:179]\n\t"
"ds_read_b64_tr_b16 v[228:229], v240 offset:0\n\t"
"v_mfma_f32_16x16x32_bf16 v[180:183], a[244:247], a[192:195], v[180:183]\n\t"
"ds_read_b64_tr_b16 v[230:231], v240 offset:1536\n\t"
"v_mfma_f32_16x16x32_bf16 v[180:183], a[240:243], a[188:191], v[180:183]\n\t"
"ds_read_b64_tr_b16 v[236:237], v241 offset:0\n\t"
"v_mfma_f32_16x16x32_bf16 v[180:183], a[236:239], a[184:187], v[180:183]\n\t"
"ds_read_b64_tr_b16 v[238:239], v241 offset:1536\n\t"
"v_mfma_f32_16x16x32_bf16 v[184:187], a[236:239], a[188:191], v[184:187]\n\t"
"ds_read_b128 a[180:183], v222 offset:20992\n\t"
"v_mfma_f32_16x16x32_bf16 v[184:187], a[240:243], a[192:195], v[184:187]\n\t"
"ds_read_b64_tr_b16 v[232:233], v240 offset:19968\n\t"
"v_mfma_f32_16x16x32_bf16 v[184:187], a[244:247], a[196:199], v[184:187]\n\t"
"ds_read_b64_tr_b16 v[234:235], v240 offset:21504\n\t"
"v_mfma_f32_16x16x32_bf16 v[188:191], a[244:247], a[200:203], v[188:191]\n\t"
"ds_read_b64_tr_b16 v[224:225], v241 offset:19968\n\t"
"v_mfma_f32_16x16x32_bf16 v[188:191], a[240:243], a[196:199], v[188:191]\n\t"
"ds_read_b64_tr_b16 v[226:227], v241 offset:21504\n\t"
"v_mfma_f32_16x16x32_bf16 v[188:191], a[236:239], a[192:195], v[188:191]\n\t"
"ds_read_b128 a[184:187], v223 offset:27136\n\t"
"v_mfma_f32_16x16x32_bf16 v[192:195], a[236:239], a[196:199], v[192:195]\n\t"
"buffer_load_dwordx4 a[212:215], off, s[80:83], s20 offset:1024 sc1\n\t"
"v_mfma_f32_16x16x32_bf16 v[192:195], a[240:243], a[200:203], v[192:195]\n\t"
"s_waitcnt lgkmcnt(6)\n\t"
"ds_read_b128 a[188:191], v223 offset:33280\n\t"
"v_mfma_f32_16x16x32_bf16 v[192:195], a[244:247], a[204:207], v[192:195]\n\t"
"s_waitcnt lgkmcnt(4)\n\t"
"ds_read_b128 a[192:195], v222 offset:40960\n\t"
"v_mfma_f32_16x16x32_bf16 v[228:231], v[228:231], a[208:211], 0\n\t"
"s_waitcnt lgkmcnt(3)\n\t"
"ds_read_b128 a[196:199], v223 offset:47104\n\t"
"v_mfma_f32_16x16x32_bf16 v[236:239], v[236:239], a[208:211], 0\n\t"
"ds_read_b128 a[200:203], v223 offset:53248\n\t"
"v_mfma_f32_16x16x32_bf16 v[232:235], v[232:235], a[208:211], 0\n\t"
"ds_read_b128 a[204:207], v222 offset:60928\n\t"
"v_mfma_f32_16x16x32_bf16 v[224:227], v[224:227], a[208:211], 0\n\t"
"Lst23_%=:\n\t"
"v_mfma_f32_16x16x32_bf16 v[196:199], a[248:251], a[168:171], v[196:199]\n\t"
"s_mov_b64 vcc, -1\n\t"
"s_bitcmp1_b32 s73, 1\n\t"
"s_cbranch_scc1 Lvh3_%=\n\t"
"s_bitcmp1_b32 s73, 0\n\t"
"s_cbranch_scc1 Lpg22_%=\n\t"
"s_branch Lpg21_%=\n\t"
"Lvh3_%=:\n\t"
"s_bitcmp1_b32 s73, 0\n\t"
"s_cbranch_scc1 Lpg24_%=\n\t"
"s_branch Lpg23_%=\n\t"
"Lvr3_%=:\n\t"
"s_mov_b64 vcc, 0\n\t"
"v_mfma_f32_16x16x32_bf16 v[196:199], v[248:251], a[176:179], v[196:199]\n\t"
"v_cvt_pk_bf16_f32 v228, v228, v229\n\t"
"v_cvt_pk_bf16_f32 v229, v230, v231\n\t"
"v_mfma_f32_16x16x32_bf16 v[196:199], a[252:255], a[172:175], v[196:199]\n\t"
"v_cvt_pk_bf16_f32 v230, v236, v237\n\t"
"v_cvt_pk_bf16_f32 v231, v238, v239\n\t"
"v_mfma_f32_16x16x32_bf16 v[204:207], a[248:251], a[172:175], v[204:207]\n\t"
"s_waitcnt lgkmcnt(5)\n\t"
"v_cvt_pk_bf16_f32 v232, v232, v233\n\t"
"v_cvt_pk_bf16_f32 v233, v234, v235\n\t"
"v_mfma_f32_16x16x32_bf16 v[204:207], a[252:255], a[176:179], v[204:207]\n\t"
"v_cvt_pk_bf16_f32 v234, v224, v225\n\t"
"v_cvt_pk_bf16_f32 v235, v226, v227\n\t"
"v_mfma_f32_16x16x32_bf16 v[204:207], v[248:251], a[180:183], v[204:207]\n\t"
"ds_write_b128 v242, v[228:231] offset:29184\n\t"
"v_mfma_f32_16x16x32_bf16 v[212:215], v[248:251], a[184:187], v[212:215]\n\t"
"s_waitcnt lgkmcnt(5)\n\t"
"v_mfma_f32_16x16x32_bf16 v[212:215], a[252:255], a[180:183], v[212:215]\n\t"
"v_mfma_f32_16x16x32_bf16 v[212:215], a[248:251], a[176:179], v[212:215]\n\t"
"ds_write_b128 v242, v[232:235] offset:30720\n\t"
"v_mfma_f32_16x16x32_bf16 a[0:3], a[248:251], a[180:183], a[0:3]\n\t"
"s_waitcnt lgkmcnt(5)\n\t"
"v_mfma_f32_16x16x32_bf16 a[0:3], a[252:255], a[184:187], a[0:3]\n\t"
"v_mfma_f32_16x16x32_bf16 a[0:3], v[248:251], a[188:191], a[0:3]\n\t"
"s_barrier\n\t"
"v_mfma_f32_16x16x32_bf16 a[8:11], v[248:251], a[192:195], a[8:11]\n\t"
"s_waitcnt lgkmcnt(4)\n\t"
"ds_read_b64_tr_b16 v[236:237], v246 offset:0\n\t"
"v_mfma_f32_16x16x32_bf16 a[8:11], a[252:255], a[188:191], a[8:11]\n\t"
"ds_read_b64_tr_b16 v[224:225], v255 offset:0\n\t"
"v_mfma_f32_16x16x32_bf16 a[8:11], a[248:251], a[184:187], a[8:11]\n\t"
"ds_read_b64_tr_b16 v[228:229], v246 offset:6144\n\t"
"v_mfma_f32_16x16x32_bf16 a[16:19], a[248:251], a[188:191], a[16:19]\n\t"
"s_waitcnt lgkmcnt(6)\n\t"
"ds_read_b64_tr_b16 v[232:233], v255 offset:6144\n\t"
"v_mfma_f32_16x16x32_bf16 a[16:19], a[252:255], a[192:195], a[16:19]\n\t"
"buffer_load_dwordx4 a[228:231], off, s[80:83], s20 offset:2048 sc1\n\t"
"v_mfma_f32_16x16x32_bf16 a[16:19], v[248:251], a[196:199], a[16:19]\n\t"
"s_mov_b32 s20, 442368\n\t"
"v_mfma_f32_16x16x32_bf16 a[24:27], v[248:251], a[200:203], a[24:27]\n\t"
"s_waitcnt lgkmcnt(6)\n\t"
"v_mfma_f32_16x16x32_bf16 a[24:27], a[252:255], a[196:199], a[24:27]\n\t"
"v_mfma_f32_16x16x32_bf16 a[24:27], a[248:251], a[192:195], a[24:27]\n\t"
"buffer_load_dwordx4 a[232:235], off, s[80:83], s20 offset:0 sc1\n\t"
"v_mfma_f32_16x16x32_bf16 a[32:35], a[248:251], a[196:199], a[32:35]\n\t"
"s_waitcnt lgkmcnt(3)\n\t"
"v_mfma_f32_16x16x32_bf16 a[32:35], a[252:255], a[200:203], a[32:35]\n\t"
"s_waitcnt lgkmcnt(2)\n\t"
"v_mfma_f32_16x16x32_bf16 a[32:35], v[248:251], a[204:207], a[32:35]\n\t"
"s_waitcnt lgkmcnt(1)\n\t"
"v_mfma_f32_4x4x4_16b_bf16 v[236:239], v[236:237], v[220:221], 0\n\t"
"s_waitcnt lgkmcnt(0)\n\t"
"v_mfma_f32_4x4x4_16b_bf16 v[224:227], v[224:225], v[220:221], 0\n\t"
"s_waitcnt vmcnt(10)\n\t"
"v_mfma_f32_4x4x4_16b_bf16 v[228:231], v[228:229], v[220:221], 0\n\t"
"v_mfma_f32_4x4x4_16b_bf16 v[232:235], v[232:233], v[220:221], 0\n\t"
"buffer_load_dwordx4 v[248:251], off, s[80:83], s20 offset:1024 sc1\n\t"
"v_mfma_f32_16x16x32_bf16 a[40:43], a[216:219], a[168:171], a[40:43]\n\t"
"buffer_load_dwordx4 a[236:239], off, s[80:83], s20 offset:2048 sc1\n\t"
"v_mfma_f32_16x16x32_bf16 a[40:43], a[224:227], a[176:179], a[40:43]\n\t"
"s_mov_b32 s20, 454656\n\t"
"v_cvt_pk_bf16_f32 v236, v236, v237\n\t"
"v_cvt_pk_bf16_f32 v237, v238, v239\n\t"
"v_mfma_f32_16x16x32_bf16 a[40:43], a[220:223], a[172:175], a[40:43]\n\t"
"v_cvt_pk_bf16_f32 v238, v224, v225\n\t"
"v_cvt_pk_bf16_f32 v239, v226, v227\n\t"
"v_mfma_f32_16x16x32_bf16 a[48:51], a[216:219], a[172:175], a[48:51]\n\t"
"ds_read_b64_tr_b16 v[224:225], v246 offset:12288\n\t"
"v_cvt_pk_bf16_f32 v228, v228, v229\n\t"
"v_mfma_f32_16x16x32_bf16 a[48:51], a[220:223], a[176:179], a[48:51]\n\t"
"v_cvt_pk_bf16_f32 v229, v230, v231\n\t"
"v_cvt_pk_bf16_f32 v230, v232, v233\n\t"
"v_mfma_f32_16x16x32_bf16 a[48:51], a[224:227], a[180:183], a[48:51]\n\t"
"ds_write_b128 v245, v[236:239] offset:0\n\t"
"v_cvt_pk_bf16_f32 v231, v234, v235\n\t"
"v_mfma_f32_16x16x32_bf16 a[56:59], a[224:227], a[184:187], a[56:59]\n\t"
"v_mfma_f32_16x16x32_bf16 a[56:59], a[220:223], a[180:183], a[56:59]\n\t"
"buffer_load_dwordx4 a[240:243], off, s[80:83], s20 offset:0 sc1\n\t"
"v_mfma_f32_16x16x32_bf16 a[56:59], a[216:219], a[176:179], a[56:59]\n\t"
"ds_read_b64_tr_b16 v[232:233], v255 offset:12288\n\t"
"v_mfma_f32_16x16x32_bf16 a[64:67], a[216:219], a[180:183], a[64:67]\n\t"
"ds_read_b64_tr_b16 v[236:237], v246 offset:19968\n\t"
"v_mfma_f32_16x16x32_bf16 a[64:67], a[220:223], a[184:187], a[64:67]\n\t"
"buffer_load_dwordx4 a[244:247], off, s[80:83], s20 offset:1024 sc1\n\t"
"v_mfma_f32_16x16x32_bf16 a[64:67], a[224:227], a[188:191], a[64:67]\n\t"
"ds_write_b128 v245, v[228:231] offset:6144\n\t"
"v_mfma_f32_16x16x32_bf16 a[72:75], a[216:219], a[184:187], a[72:75]\n\t"
"buffer_load_dwordx4 a[248:251], off, s[80:83], s20 offset:2048 sc1\n\t"
"v_mfma_f32_16x16x32_bf16 a[72:75], a[224:227], a[192:195], a[72:75]\n\t"
"s_waitcnt lgkmcnt(2)\n\t"
"ds_read_b64_tr_b16 v[228:229], v255 offset:19968\n\t"
"v_mfma_f32_16x16x32_bf16 a[72:75], a[220:223], a[188:191], a[72:75]\n\t"
"s_waitcnt lgkmcnt(2)\n\t"
"s_mov_b32 s20, 466944\n\t"
"v_mfma_f32_4x4x4_16b_bf16 v[224:227], v[224:225], v[220:221], 0\n\t"
"v_mfma_f32_4x4x4_16b_bf16 v[232:235], v[232:233], v[220:221], 0\n\t"
"v_mfma_f32_4x4x4_16b_bf16 v[236:239], v[236:237], v[220:221], 0\n\t"
"s_waitcnt lgkmcnt(0)\n\t"
"v_mfma_f32_4x4x4_16b_bf16 v[228:231], v[228:229], v[220:221], 0\n\t"
"v_mfma_f32_16x16x32_bf16 a[80:83], a[216:219], a[188:191], a[80:83]\n\t"
"buffer_load_dwordx4 a[252:255], off, s[80:83], s20 offset:0 sc1\n\t"
"v_mfma_f32_16x16x32_bf16 a[80:83], a[224:227], a[196:199], a[80:83]\n\t"
"v_cvt_pk_bf16_f32 v224, v224, v225\n\t"
"v_cvt_pk_bf16_f32 v225, v226, v227\n\t"
"v_mfma_f32_16x16x32_bf16 a[80:83], a[220:223], a[192:195], a[80:83]\n\t"
"v_cvt_pk_bf16_f32 v226, v232, v233\n\t"
"v_cvt_pk_bf16_f32 v227, v234, v235\n\t"
"v_mfma_f32_16x16x32_bf16 a[88:91], a[216:219], a[192:195], a[88:91]\n\t"
"ds_read_b64_tr_b16 v[232:233], v246 offset:26112\n\t"
"v_cvt_pk_bf16_f32 v236, v236, v237\n\t"
"v_mfma_f32_16x16x32_bf16 a[88:91], a[220:223], a[196:199], a[88:91]\n\t"
"v_cvt_pk_bf16_f32 v237, v238, v239\n\t"
"v_cvt_pk_bf16_f32 v238, v228, v229\n\t"
"v_mfma_f32_16x16x32_bf16 a[88:91], a[224:227], a[200:203], a[88:91]\n\t"
"ds_write_b128 v245, v[224:227] offset:12288\n\t"
"v_cvt_pk_bf16_f32 v239, v230, v231\n\t"
"v_mfma_f32_16x16x32_bf16 a[96:99], a[220:223], a[200:203], a[96:99]\n\t"
"s_waitcnt vmcnt(7)\n\t"
"v_mfma_f32_16x16x32_bf16 a[96:99], a[216:219], a[196:199], a[96:99]\n\t"
"ds_read_b64_tr_b16 v[228:229], v255 offset:26112\n\t"
"v_mfma_f32_16x16x32_bf16 a[96:99], a[224:227], a[204:207], a[96:99]\n\t"
"ds_write_b128 v245, v[236:239] offset:19968\n\t"
"v_mfma_f32_16x16x32_bf16 a[104:107], v[96:99], a[168:171], a[104:107]\n\t"
"buffer_load_dwordx4 a[220:223], off, s[80:83], s20 offset:1024 sc1\n\t"
"v_mfma_f32_16x16x32_bf16 a[104:107], a[228:231], a[176:179], a[104:107]\n\t"
"ds_read_b64_tr_b16 v[224:225], v246 offset:32256\n\t"
"v_mfma_f32_16x16x32_bf16 a[104:107], a[212:215], a[172:175], a[104:107]\n\t"
"ds_read_b64_tr_b16 v[236:237], v255 offset:32256\n\t"
"v_mfma_f32_16x16x32_bf16 a[112:115], v[96:99], a[172:175], a[112:115]\n\t"
"buffer_load_dwordx4 a[216:219], off, s[80:83], s20 offset:2048 sc1\n\t"
"v_mfma_f32_16x16x32_bf16 a[112:115], a[212:215], a[176:179], a[112:115]\n\t"
"s_waitcnt lgkmcnt(3)\n\t"
"ds_read_b128 a[168:171], v222 offset:1280\n\t"
"s_mov_b32 s20, 700416\n\t"
"v_mfma_f32_16x16x32_bf16 a[112:115], a[228:231], a[180:183], a[112:115]\n\t"
"buffer_load_dwordx4 a[224:227], off, s[80:83], s20 offset:0 sc1\n\t"
"v_mfma_f32_4x4x4_16b_bf16 v[232:235], v[232:233], v[220:221], 0\n\t"
"s_waitcnt lgkmcnt(2)\n\t"
"v_mfma_f32_4x4x4_16b_bf16 v[228:231], v[228:229], v[220:221], 0\n\t"
"s_waitcnt lgkmcnt(1)\n\t"
"v_mfma_f32_4x4x4_16b_bf16 v[224:227], v[224:225], v[220:221], 0\n\t"
"v_mfma_f32_4x4x4_16b_bf16 v[236:239], v[236:237], v[220:221], 0\n\t"
"v_mfma_f32_16x16x32_bf16 a[120:123], a[212:215], a[180:183], a[120:123]\n\t"
"ds_read_b128 a[172:175], v223 offset:7424\n\t"
"v_cvt_pk_bf16_f32 v232, v232, v233\n\t"
"v_mfma_f32_16x16x32_bf16 a[120:123], v[96:99], a[176:179], a[120:123]\n\t"
"ds_read_b128 a[176:179], v223 offset:13568\n\t"
"v_cvt_pk_bf16_f32 v233, v234, v235\n\t"
"v_mfma_f32_16x16x32_bf16 a[120:123], a[228:231], a[184:187], a[120:123]\n\t"
"v_cvt_pk_bf16_f32 v234, v228, v229\n\t"
"v_cvt_pk_bf16_f32 v235, v230, v231\n\t"
"v_mfma_f32_16x16x32_bf16 a[128:131], v[96:99], a[180:183], a[128:131]\n\t"
"ds_read_b128 a[180:183], v222 offset:21248\n\t"
"v_cvt_pk_bf16_f32 v224, v224, v225\n\t"
"v_mfma_f32_16x16x32_bf16 a[128:131], a[228:231], a[188:191], a[128:131]\n\t"
"v_cvt_pk_bf16_f32 v225, v226, v227\n\t"
"v_cvt_pk_bf16_f32 v226, v236, v237\n\t"
"v_mfma_f32_16x16x32_bf16 a[128:131], a[212:215], a[184:187], a[128:131]\n\t"
"ds_write_b128 v245, v[232:235] offset:26112\n\t"
"v_cvt_pk_bf16_f32 v227, v238, v239\n\t"
"v_mfma_f32_16x16x32_bf16 a[136:139], v[96:99], a[184:187], a[136:139]\n\t"
"v_mfma_f32_16x16x32_bf16 a[136:139], a[212:215], a[188:191], a[136:139]\n\t"
"ds_read_b128 a[184:187], v223 offset:27392\n\t"
"v_mfma_f32_16x16x32_bf16 a[136:139], a[228:231], a[192:195], a[136:139]\n\t"
"ds_write_b128 v245, v[224:227] offset:32256\n\t"
"v_mfma_f32_16x16x32_bf16 a[144:147], a[228:231], a[196:199], a[144:147]\n\t"
"v_mfma_f32_16x16x32_bf16 a[144:147], a[212:215], a[192:195], a[144:147]\n\t"
"v_mfma_f32_16x16x32_bf16 a[144:147], v[96:99], a[188:191], a[144:147]\n\t"
"ds_read_b128 a[188:191], v223 offset:33536\n\t"
"v_mfma_f32_16x16x32_bf16 a[152:155], v[96:99], a[192:195], a[152:155]\n\t"
"ds_read_b128 a[192:195], v222 offset:41216\n\t"
"v_mfma_f32_16x16x32_bf16 a[152:155], a[212:215], a[196:199], a[152:155]\n\t"
"v_mfma_f32_16x16x32_bf16 a[152:155], a[228:231], a[200:203], a[152:155]\n\t"
"s_barrier\n\t"
"v_mfma_f32_16x16x32_bf16 a[160:163], a[212:215], a[200:203], a[160:163]\n\t"
"s_waitcnt vmcnt(7) lgkmcnt(6)\n\t"
"ds_read_b64_tr_b16 v[228:229], v240 offset:39936\n\t"
"v_mfma_f32_16x16x32_bf16 a[160:163], v[96:99], a[196:199], a[160:163]\n\t"
"ds_read_b64_tr_b16 v[230:231], v240 offset:41472\n\t"
"v_mfma_f32_16x16x32_bf16 a[160:163], a[228:231], a[204:207], a[160:163]\n\t"
"ds_read_b64_tr_b16 v[236:237], v241 offset:39936\n\t"
"v_mfma_f32_16x16x32_bf16 v[200:203], a[232:235], a[168:171], v[200:203]\n\t"
"s_waitcnt lgkmcnt(8)\n\t"
"ds_read_b64_tr_b16 v[238:239], v241 offset:41472\n\t"
"v_subrev_u32 v240, 59904, v240\n\t"
"v_mfma_f32_16x16x32_bf16 v[200:203], a[236:239], a[176:179], v[200:203]\n\t"
"ds_read_b128 a[196:199], v223 offset:47360\n\t"
"v_subrev_u32 v241, 59904, v241\n\t"
"v_mfma_f32_16x16x32_bf16 v[200:203], v[248:251], a[172:175], v[200:203]\n\t"
"ds_read_b64_tr_b16 v[232:233], v240 offset:39936\n\t"
"v_mfma_f32_16x16x32_bf16 v[208:211], a[232:235], a[172:175], v[208:211]\n\t"
"s_waitcnt lgkmcnt(4)\n\t"
"ds_read_b64_tr_b16 v[234:235], v240 offset:41472\n\t"
"v_mfma_f32_16x16x32_bf16 v[208:211], v[248:251], a[176:179], v[208:211]\n\t"
"buffer_load_dwordx4 a[212:215], off, s[80:83], s20 offset:1024 sc1\n\t"
"v_mfma_f32_16x16x32_bf16 v[208:211], a[236:239], a[180:183], v[208:211]\n\t"
"s_waitcnt lgkmcnt(3)\n\t"
"ds_read_b64_tr_b16 v[224:225], v241 offset:39936\n\t"
"ds_read_b64_tr_b16 v[226:227], v241 offset:41472\n\t"
"v_mfma_f32_16x16x32_bf16 v[228:231], v[228:231], a[208:211], 0\n\t"
"buffer_load_dwordx4 v[96:99], off, s[80:83], s20 offset:2048 sc1\n\t"
"s_mov_b32 s20, 712704\n\t"
"v_mfma_f32_16x16x32_bf16 v[236:239], v[236:239], a[208:211], 0\n\t"
"s_waitcnt lgkmcnt(2)\n\t"
"buffer_load_dwordx4 a[228:231], off, s[80:83], s20 offset:0 sc1\n\t"
"v_mfma_f32_16x16x32_bf16 v[232:235], v[232:235], a[208:211], 0\n\t"
"s_waitcnt lgkmcnt(0)\n\t"
"ds_read_b128 a[200:203], v223 offset:53504\n\t"
"v_mfma_f32_16x16x32_bf16 v[224:227], v[224:227], a[208:211], 0\n\t"
"ds_read_b128 a[204:207], v222 offset:61184\n\t"
"v_mfma_f32_16x16x32_bf16 v[216:219], v[248:251], a[180:183], v[216:219]\n\t"
"v_mfma_f32_16x16x32_bf16 v[216:219], a[232:235], a[176:179], v[216:219]\n\t"
"v_cvt_pk_bf16_f32 v228, v228, v229\n\t"
"v_cvt_pk_bf16_f32 v229, v230, v231\n\t"
"v_mfma_f32_16x16x32_bf16 v[216:219], a[236:239], a[184:187], v[216:219]\n\t"
"v_cvt_pk_bf16_f32 v230, v236, v237\n\t"
"v_cvt_pk_bf16_f32 v231, v238, v239\n\t"
"v_mfma_f32_16x16x32_bf16 a[4:7], a[232:235], a[180:183], a[4:7]\n\t"
"ds_read_b64_tr_b16 v[236:237], v246 offset:39936\n\t"
"v_cvt_pk_bf16_f32 v232, v232, v233\n\t"
"v_mfma_f32_16x16x32_bf16 a[4:7], a[236:239], a[188:191], a[4:7]\n\t"
"v_cvt_pk_bf16_f32 v233, v234, v235\n\t"
"v_cvt_pk_bf16_f32 v234, v224, v225\n\t"
"v_mfma_f32_16x16x32_bf16 a[4:7], v[248:251], a[184:187], a[4:7]\n\t"
"ds_write_b128 v242, v[228:231] offset:32256\n\t"
"v_mfma_f32_16x16x32_bf16 a[12:15], a[232:235], a[184:187], a[12:15]\n\t"
"v_cvt_pk_bf16_f32 v235, v226, v227\n\t"
"v_mfma_f32_16x16x32_bf16 a[12:15], v[248:251], a[188:191], a[12:15]\n\t"
"ds_read_b64_tr_b16 v[224:225], v255 offset:39936\n\t"
"v_mfma_f32_16x16x32_bf16 a[12:15], a[236:239], a[192:195], a[12:15]\n\t"
"ds_write_b128 v242, v[232:235] offset:27648\n\t"
"v_mfma_f32_16x16x32_bf16 a[20:23], a[236:239], a[196:199], a[20:23]\n\t"
"s_waitcnt lgkmcnt(5)\n\t"
"v_mfma_f32_16x16x32_bf16 a[20:23], v[248:251], a[192:195], a[20:23]\n\t"
"ds_read_b64_tr_b16 v[228:229], v246 offset:46080\n\t"
"v_mfma_f32_16x16x32_bf16 a[20:23], a[232:235], a[188:191], a[20:23]\n\t"
"ds_read_b64_tr_b16 v[232:233], v255 offset:46080\n\t"
"v_mfma_f32_16x16x32_bf16 a[28:31], a[232:235], a[192:195], a[28:31]\n\t"
"s_waitcnt lgkmcnt(6)\n\t"
"v_mfma_f32_16x16x32_bf16 a[28:31], v[248:251], a[196:199], a[28:31]\n\t"
"v_mfma_f32_16x16x32_bf16 a[28:31], a[236:239], a[200:203], a[28:31]\n\t"
"s_barrier\n\t"
"v_mfma_f32_16x16x32_bf16 a[36:39], v[248:251], a[200:203], a[36:39]\n\t"
"s_waitcnt lgkmcnt(5)\n\t"
"v_mfma_f32_16x16x32_bf16 a[36:39], a[232:235], a[196:199], a[36:39]\n\t"
"buffer_load_dwordx4 v[248:251], off, s[80:83], s20 offset:1024 sc1\n\t"
"v_mfma_f32_16x16x32_bf16 a[36:39], a[236:239], a[204:207], a[36:39]\n\t"
"s_waitcnt lgkmcnt(1)\n\t"
"v_mfma_f32_4x4x4_16b_bf16 v[236:239], v[236:237], v[220:221], 0\n\t"
"s_waitcnt lgkmcnt(0)\n\t"
"v_mfma_f32_4x4x4_16b_bf16 v[224:227], v[224:225], v[220:221], 0\n\t"
"s_waitcnt vmcnt(8)\n\t"
"v_mfma_f32_4x4x4_16b_bf16 v[228:231], v[228:229], v[220:221], 0\n\t"
"v_mfma_f32_4x4x4_16b_bf16 v[232:235], v[232:233], v[220:221], 0\n\t"
"buffer_load_dwordx4 a[232:235], off, s[80:83], s20 offset:2048 sc1\n\t"
"s_mov_b32 s20, 724992\n\t"
"v_mfma_f32_16x16x32_bf16 a[44:47], a[240:243], a[168:171], a[44:47]\n\t"
"buffer_load_dwordx4 a[236:239], off, s[80:83], s20 offset:0 sc1\n\t"
"v_mfma_f32_16x16x32_bf16 a[44:47], a[248:251], a[176:179], a[44:47]\n\t"
"v_cvt_pk_bf16_f32 v236, v236, v237\n\t"
"v_cvt_pk_bf16_f32 v237, v238, v239\n\t"
"v_mfma_f32_16x16x32_bf16 a[44:47], a[244:247], a[172:175], a[44:47]\n\t"
"v_cvt_pk_bf16_f32 v238, v224, v225\n\t"
"v_cvt_pk_bf16_f32 v239, v226, v227\n\t"
"v_mfma_f32_16x16x32_bf16 a[52:55], a[240:243], a[172:175], a[52:55]\n\t"
"ds_read_b64_tr_b16 v[224:225], v246 offset:52224\n\t"
"v_cvt_pk_bf16_f32 v228, v228, v229\n\t"
"v_mfma_f32_16x16x32_bf16 a[52:55], a[244:247], a[176:179], a[52:55]\n\t"
"v_cvt_pk_bf16_f32 v229, v230, v231\n\t"
"v_cvt_pk_bf16_f32 v230, v232, v233\n\t"
"v_mfma_f32_16x16x32_bf16 a[52:55], a[248:251], a[180:183], a[52:55]\n\t"
"ds_write_b128 v245, v[236:239] offset:39936\n\t"
"v_cvt_pk_bf16_f32 v231, v234, v235\n\t"
"v_mfma_f32_16x16x32_bf16 a[60:63], a[248:251], a[184:187], a[60:63]\n\t"
"v_mfma_f32_16x16x32_bf16 a[60:63], a[244:247], a[180:183], a[60:63]\n\t"
"ds_read_b64_tr_b16 v[232:233], v255 offset:52224\n\t"
"v_mfma_f32_16x16x32_bf16 a[60:63], a[240:243], a[176:179], a[60:63]\n\t"
"ds_write_b128 v245, v[228:231] offset:46080\n\t"
"v_mfma_f32_16x16x32_bf16 a[68:71], a[240:243], a[180:183], a[68:71]\n\t"
"v_mfma_f32_16x16x32_bf16 a[68:71], a[244:247], a[184:187], a[68:71]\n\t"
"v_mfma_f32_16x16x32_bf16 a[68:71], a[248:251], a[188:191], a[68:71]\n\t"
"v_mfma_f32_16x16x32_bf16 a[76:79], a[240:243], a[184:187], a[76:79]\n\t"
"s_waitcnt lgkmcnt(3)\n\t"
"v_mfma_f32_16x16x32_bf16 a[76:79], a[248:251], a[192:195], a[76:79]\n\t"
"s_waitcnt lgkmcnt(1)\n\t"
"v_mfma_f32_16x16x32_bf16 a[76:79], a[244:247], a[188:191], a[76:79]\n\t"
"v_mfma_f32_4x4x4_16b_bf16 v[224:227], v[224:225], v[220:221], 0\n\t"
"v_mfma_f32_4x4x4_16b_bf16 v[232:235], v[232:233], v[220:221], 0\n\t"
"v_mfma_f32_16x16x32_bf16 a[84:87], a[240:243], a[188:191], a[84:87]\n\t"
"v_mfma_f32_16x16x32_bf16 a[84:87], a[248:251], a[196:199], a[84:87]\n\t"
"v_cvt_pk_bf16_f32 v224, v224, v225\n\t"
"v_cvt_pk_bf16_f32 v225, v226, v227\n\t"
"v_mfma_f32_16x16x32_bf16 a[84:87], a[244:247], a[192:195], a[84:87]\n\t"
"v_cvt_pk_bf16_f32 v226, v232, v233\n\t"
"v_cvt_pk_bf16_f32 v227, v234, v235\n\t"
"v_mfma_f32_16x16x32_bf16 a[92:95], a[240:243], a[192:195], a[92:95]\n\t"
"v_mfma_f32_16x16x32_bf16 a[92:95], a[244:247], a[196:199], a[92:95]\n\t"
"v_mfma_f32_16x16x32_bf16 a[92:95], a[248:251], a[200:203], a[92:95]\n\t"
"ds_write_b128 v245, v[224:227] offset:52224\n\t"
"v_mfma_f32_16x16x32_bf16 a[100:103], a[244:247], a[200:203], a[100:103]\n\t"
"buffer_load_dwordx4 a[244:247], off, s[80:83], s20 offset:1024 sc1\n\t"
"v_mfma_f32_16x16x32_bf16 a[100:103], a[240:243], a[196:199], a[100:103]\n\t"
"s_waitcnt vmcnt(8)\n\t"
"v_mfma_f32_16x16x32_bf16 a[100:103], a[248:251], a[204:207], a[100:103]\n\t"
"v_mfma_f32_16x16x32_bf16 a[108:111], a[252:255], a[168:171], a[108:111]\n\t"
"buffer_load_dwordx4 a[240:243], off, s[80:83], s20 offset:2048 sc1\n\t"
"v_mfma_f32_16x16x32_bf16 a[108:111], a[216:219], a[176:179], a[108:111]\n\t"
"s_mov_b32 s20, 737280\n\t"
"v_mfma_f32_16x16x32_bf16 a[108:111], a[220:223], a[172:175], a[108:111]\n\t"
"s_waitcnt lgkmcnt(0)\n\t"
"s_barrier\n\t"
"v_mfma_f32_16x16x32_bf16 a[116:119], a[252:255], a[172:175], a[116:119]\n\t"
"ds_read_b128 a[168:171], v222 offset:0\n\t"
"v_mfma_f32_16x16x32_bf16 a[116:119], a[220:223], a[176:179], a[116:119]\n\t"
"buffer_load_dwordx4 a[248:251], off, s[80:83], s20 offset:0 sc1\n\t"
"v_mfma_f32_16x16x32_bf16 a[116:119], a[216:219], a[180:183], a[116:119]\n\t"
"ds_read_b128 a[172:175], v223 offset:6144\n\t"
"v_mfma_f32_16x16x32_bf16 a[124:127], a[216:219], a[184:187], a[124:127]\n\t"
"v_mfma_f32_16x16x32_bf16 a[124:127], a[220:223], a[180:183], a[124:127]\n\t"
"v_mfma_f32_16x16x32_bf16 a[124:127], a[252:255], a[176:179], a[124:127]\n\t"
"ds_read_b128 a[176:179], v223 offset:12288\n\t"
"v_mfma_f32_16x16x32_bf16 a[132:135], a[252:255], a[180:183], a[132:135]\n\t"
"ds_read_b64_tr_b16 v[236:237], v240 offset:512\n\t"
"v_mfma_f32_16x16x32_bf16 a[132:135], a[220:223], a[184:187], a[132:135]\n\t"
"ds_read_b64_tr_b16 v[238:239], v240 offset:2048\n\t"
"v_mfma_f32_16x16x32_bf16 a[132:135], a[216:219], a[188:191], a[132:135]\n\t"
"ds_read_b64_tr_b16 v[228:229], v241 offset:512\n\t"
"v_mfma_f32_16x16x32_bf16 a[140:143], a[216:219], a[192:195], a[140:143]\n\t"
"ds_read_b64_tr_b16 v[230:231], v241 offset:2048\n\t"
"v_mfma_f32_16x16x32_bf16 a[140:143], a[220:223], a[188:191], a[140:143]\n\t"
"ds_read_b128 a[180:183], v222 offset:19968\n\t"
"v_mfma_f32_16x16x32_bf16 a[140:143], a[252:255], a[184:187], a[140:143]\n\t"
"ds_read_b64_tr_b16 v[232:233], v240 offset:20480\n\t"
"v_mfma_f32_16x16x32_bf16 a[148:151], a[252:255], a[188:191], a[148:151]\n\t"
"ds_read_b64_tr_b16 v[234:235], v240 offset:22016\n\t"
"v_mfma_f32_16x16x32_bf16 a[148:151], a[220:223], a[192:195], a[148:151]\n\t"
"ds_read_b64_tr_b16 v[224:225], v241 offset:20480\n\t"
"v_mfma_f32_16x16x32_bf16 a[148:151], a[216:219], a[196:199], a[148:151]\n\t"
"ds_read_b64_tr_b16 v[226:227], v241 offset:22016\n\t"
"v_mfma_f32_16x16x32_bf16 a[156:159], a[216:219], a[200:203], a[156:159]\n\t"
"ds_read_b128 a[184:187], v223 offset:26112\n\t"
"v_mfma_f32_16x16x32_bf16 a[156:159], a[220:223], a[196:199], a[156:159]\n\t"
"ds_read_b128 a[188:191], v223 offset:32256\n\t"
"v_mfma_f32_16x16x32_bf16 a[156:159], a[252:255], a[192:195], a[156:159]\n\t"
"ds_read_b128 a[192:195], v222 offset:39936\n\t"
"v_mfma_f32_16x16x32_bf16 a[164:167], a[252:255], a[196:199], a[164:167]\n\t"
"buffer_load_dwordx4 a[252:255], off, s[80:83], s20 offset:1024 sc1\n\t"
"v_mfma_f32_16x16x32_bf16 a[164:167], a[220:223], a[200:203], a[164:167]\n\t"
"s_waitcnt lgkmcnt(8)\n\t"
"ds_read_b128 a[196:199], v223 offset:46080\n\t"
"v_mfma_f32_16x16x32_bf16 a[164:167], a[216:219], a[204:207], a[164:167]\n\t"
"s_waitcnt lgkmcnt(6)\n\t"
"ds_read_b128 a[200:203], v223 offset:52224\n\t"
"v_mfma_f32_16x16x32_bf16 v[236:239], v[236:239], a[208:211], 0\n\t"
"buffer_load_dwordx4 a[216:219], off, s[80:83], s20 offset:2048 sc1\n\t"
"v_mfma_f32_16x16x32_bf16 v[228:231], v[228:231], a[208:211], 0\n\t"
"s_waitcnt vmcnt(9) lgkmcnt(5)\n\t"
"ds_read_b128 a[204:207], v222 offset:59904\n\t"
"s_mov_b32 s20, 749568\n\t"
"v_mfma_f32_16x16x32_bf16 v[232:235], v[232:235], a[208:211], 0\n\t"
"v_mfma_f32_16x16x32_bf16 v[224:227], v[224:227], a[208:211], 0\n\t"
"buffer_load_dwordx4 a[220:223], off, s[80:83], s20 offset:0 sc1\n\t"
"Lst24_%=:\n\t"
"v_mfma_f32_16x16x32_bf16 v[196:199], a[224:227], a[168:171], v[196:199]\n\t"
"v_mfma_f32_16x16x32_bf16 v[196:199], v[96:99], a[176:179], v[196:199]\n\t"
"v_cvt_pk_bf16_f32 v236, v236, v237\n\t"
"v_cvt_pk_bf16_f32 v237, v238, v239\n\t"
"v_mfma_f32_16x16x32_bf16 v[196:199], a[212:215], a[172:175], v[196:199]\n\t"
"v_cvt_pk_bf16_f32 v238, v228, v229\n\t"
"v_cvt_pk_bf16_f32 v239, v230, v231\n\t"
"v_mfma_f32_16x16x32_bf16 v[204:207], a[224:227], a[172:175], v[204:207]\n\t"
"s_waitcnt lgkmcnt(5)\n\t"
"v_cvt_pk_bf16_f32 v232, v232, v233\n\t"
"v_cvt_pk_bf16_f32 v233, v234, v235\n\t"
"v_mfma_f32_16x16x32_bf16 v[204:207], a[212:215], a[176:179], v[204:207]\n\t"
"v_cvt_pk_bf16_f32 v234, v224, v225\n\t"
"v_cvt_pk_bf16_f32 v235, v226, v227\n\t"
"v_mfma_f32_16x16x32_bf16 v[204:207], v[96:99], a[180:183], v[204:207]\n\t"
"ds_write_b128 v242, v[236:239] offset:0\n\t"
"v_mfma_f32_16x16x32_bf16 v[212:215], v[96:99], a[184:187], v[212:215]\n\t"
"s_waitcnt lgkmcnt(5)\n\t"
"v_mfma_f32_16x16x32_bf16 v[212:215], a[212:215], a[180:183], v[212:215]\n\t"
"v_mfma_f32_16x16x32_bf16 v[212:215], a[224:227], a[176:179], v[212:215]\n\t"
"ds_write_b128 v242, v[232:235] offset:1536\n\t"
"v_mfma_f32_16x16x32_bf16 a[0:3], a[224:227], a[180:183], a[0:3]\n\t"
"s_waitcnt lgkmcnt(5)\n\t"
"v_mfma_f32_16x16x32_bf16 a[0:3], a[212:215], a[184:187], a[0:3]\n\t"
"ds_read_b64_tr_b16 v[228:229], v240 offset:40448\n\t"
"v_mfma_f32_16x16x32_bf16 a[0:3], v[96:99], a[188:191], a[0:3]\n\t"
"ds_read_b64_tr_b16 v[230:231], v240 offset:41984\n\t"
"v_mfma_f32_16x16x32_bf16 a[8:11], v[96:99], a[192:195], a[8:11]\n\t"
"s_waitcnt lgkmcnt(6)\n\t"
"ds_read_b64_tr_b16 v[224:225], v241 offset:40448\n\t"
"v_mfma_f32_16x16x32_bf16 a[8:11], a[212:215], a[188:191], a[8:11]\n\t"
"ds_read_b64_tr_b16 v[226:227], v241 offset:41984\n\t"
"v_add_u32 v240, 59904, v240\n\t"
"v_mfma_f32_16x16x32_bf16 a[8:11], a[224:227], a[184:187], a[8:11]\n\t"
"v_add_u32 v241, 59904, v241\n\t"
"v_mfma_f32_16x16x32_bf16 a[16:19], a[224:227], a[188:191], a[16:19]\n\t"
"s_waitcnt lgkmcnt(7)\n\t"
"ds_read_b64_tr_b16 v[236:237], v240 offset:512\n\t"
"v_mfma_f32_16x16x32_bf16 a[16:19], a[212:215], a[192:195], a[16:19]\n\t"
"ds_read_b64_tr_b16 v[238:239], v240 offset:2048\n\t"
"v_mfma_f32_16x16x32_bf16 a[16:19], v[96:99], a[196:199], a[16:19]\n\t"
"ds_read_b64_tr_b16 v[232:233], v241 offset:512\n\t"
"v_mfma_f32_16x16x32_bf16 a[24:27], v[96:99], a[200:203], a[24:27]\n\t"
"s_waitcnt lgkmcnt(9)\n\t"
"ds_read_b64_tr_b16 v[234:235], v241 offset:2048\n\t"
"v_mfma_f32_16x16x32_bf16 a[24:27], a[212:215], a[196:199], a[24:27]\n\t"
"v_mfma_f32_16x16x32_bf16 a[24:27], a[224:227], a[192:195], a[24:27]\n\t"
"v_mfma_f32_16x16x32_bf16 a[32:35], a[224:227], a[196:199], a[32:35]\n\t"
"buffer_load_dwordx4 a[224:227], off, s[80:83], s20 offset:1024 sc1\n\t"
"v_mfma_f32_16x16x32_bf16 a[32:35], a[212:215], a[200:203], a[32:35]\n\t"
"s_waitcnt lgkmcnt(4)\n\t"
"v_mfma_f32_16x16x32_bf16 a[32:35], v[96:99], a[204:207], a[32:35]\n\t"
"s_waitcnt lgkmcnt(2)\n\t"
"v_mfma_f32_16x16x32_bf16 v[228:231], v[228:231], a[208:211], 0\n\t"
"buffer_load_dwordx4 v[96:99], off, s[80:83], s20 offset:2048 sc1\n\t"
"v_mfma_f32_16x16x32_bf16 v[224:227], v[224:227], a[208:211], 0\n\t"
"s_waitcnt vmcnt(9) lgkmcnt(0)\n\t"
"s_mov_b32 s20, 761856\n\t"
"v_mfma_f32_16x16x32_bf16 v[236:239], v[236:239], a[208:211], 0\n\t"
"v_mfma_f32_16x16x32_bf16 v[232:235], v[232:235], a[208:211], 0\n\t"
"buffer_load_dwordx4 a[212:215], off, s[80:83], s20 offset:0 sc1\n\t"
"v_mfma_f32_16x16x32_bf16 a[40:43], a[228:231], a[168:171], a[40:43]\n\t"
"v_mfma_f32_16x16x32_bf16 a[40:43], a[232:235], a[176:179], a[40:43]\n\t"
"v_cvt_pk_bf16_f32 v228, v228, v229\n\t"
"v_cvt_pk_bf16_f32 v229, v230, v231\n\t"
"v_mfma_f32_16x16x32_bf16 a[40:43], v[248:251], a[172:175], a[40:43]\n\t"
"v_cvt_pk_bf16_f32 v230, v224, v225\n\t"
"v_cvt_pk_bf16_f32 v231, v226, v227\n\t"
"v_mfma_f32_16x16x32_bf16 a[48:51], a[228:231], a[172:175], a[48:51]\n\t"
"v_cvt_pk_bf16_f32 v236, v236, v237\n\t"
"v_cvt_pk_bf16_f32 v237, v238, v239\n\t"
"v_mfma_f32_16x16x32_bf16 a[48:51], v[248:251], a[176:179], a[48:51]\n\t"
"v_cvt_pk_bf16_f32 v238, v232, v233\n\t"
"v_cvt_pk_bf16_f32 v239, v234, v235\n\t"
"v_mfma_f32_16x16x32_bf16 a[48:51], a[232:235], a[180:183], a[48:51]\n\t"
"ds_write_b128 v242, v[228:231] offset:3072\n\t"
"v_mfma_f32_16x16x32_bf16 a[56:59], a[232:235], a[184:187], a[56:59]\n\t"
"v_mfma_f32_16x16x32_bf16 a[56:59], v[248:251], a[180:183], a[56:59]\n\t"
"v_mfma_f32_16x16x32_bf16 a[56:59], a[228:231], a[176:179], a[56:59]\n\t"
"ds_write_b128 v242, v[236:239] offset:4608\n\t"
"v_mfma_f32_16x16x32_bf16 a[64:67], a[228:231], a[180:183], a[64:67]\n\t"
"v_mfma_f32_16x16x32_bf16 a[64:67], v[248:251], a[184:187], a[64:67]\n\t"
"ds_read_b64_tr_b16 v[224:225], v240 offset:20480\n\t"
"v_mfma_f32_16x16x32_bf16 a[64:67], a[232:235], a[188:191], a[64:67]\n\t"
"ds_read_b64_tr_b16 v[226:227], v240 offset:22016\n\t"
"v_mfma_f32_16x16x32_bf16 a[72:75], a[232:235], a[192:195], a[72:75]\n\t"
"ds_read_b64_tr_b16 v[232:233], v241 offset:20480\n\t"
"v_mfma_f32_16x16x32_bf16 a[72:75], v[248:251], a[188:191], a[72:75]\n\t"
"ds_read_b64_tr_b16 v[234:235], v241 offset:22016\n\t"
"v_mfma_f32_16x16x32_bf16 a[72:75], a[228:231], a[184:187], a[72:75]\n\t"
"ds_read_b64_tr_b16 v[228:229], v240 offset:40448\n\t"
"v_mfma_f32_16x16x32_bf16 a[80:83], a[228:231], a[188:191], a[80:83]\n\t"
"ds_read_b64_tr_b16 v[230:231], v240 offset:41984\n\t"
"v_mfma_f32_16x16x32_bf16 a[80:83], v[248:251], a[192:195], a[80:83]\n\t"
"ds_read_b64_tr_b16 v[236:237], v241 offset:40448\n\t"
"v_mfma_f32_16x16x32_bf16 a[80:83], a[232:235], a[196:199], a[80:83]\n\t"
"ds_read_b64_tr_b16 v[238:239], v241 offset:41984\n\t"
"v_mfma_f32_16x16x32_bf16 a[88:91], a[232:235], a[200:203], a[88:91]\n\t"
"v_mfma_f32_16x16x32_bf16 a[88:91], v[248:251], a[196:199], a[88:91]\n\t"
"v_mfma_f32_16x16x32_bf16 a[88:91], a[228:231], a[192:195], a[88:91]\n\t"
"v_mfma_f32_16x16x32_bf16 a[96:99], a[228:231], a[196:199], a[96:99]\n\t"
"buffer_load_dwordx4 a[228:231], off, s[80:83], s20 offset:1024 sc1\n\t"
"v_mfma_f32_16x16x32_bf16 a[96:99], v[248:251], a[200:203], a[96:99]\n\t"
"s_waitcnt lgkmcnt(4)\n\t"
"v_mfma_f32_16x16x32_bf16 a[96:99], a[232:235], a[204:207], a[96:99]\n\t"
"s_waitcnt lgkmcnt(2)\n\t"
"v_mfma_f32_16x16x32_bf16 v[224:227], v[224:227], a[208:211], 0\n\t"
"buffer_load_dwordx4 v[248:251], off, s[80:83], s20 offset:2048 sc1\n\t"
"v_mfma_f32_16x16x32_bf16 v[232:235], v[232:235], a[208:211], 0\n\t"
"s_waitcnt vmcnt(9) lgkmcnt(0)\n\t"
"s_mov_b32 s20, 626688\n\t"
"v_mfma_f32_16x16x32_bf16 v[228:231], v[228:231], a[208:211], 0\n\t"
"v_mfma_f32_16x16x32_bf16 v[236:239], v[236:239], a[208:211], 0\n\t"
"buffer_load_dwordx4 a[232:235], off, s[80:83], s20 offset:0 sc1\n\t"
"v_mfma_f32_16x16x32_bf16 a[104:107], a[236:239], a[168:171], a[104:107]\n\t"
"ds_read_b128 a[168:171], v222 offset:256\n\t"
"v_mfma_f32_16x16x32_bf16 a[104:107], a[240:243], a[176:179], a[104:107]\n\t"
"v_cvt_pk_bf16_f32 v224, v224, v225\n\t"
"v_cvt_pk_bf16_f32 v225, v226, v227\n\t"
"v_mfma_f32_16x16x32_bf16 a[104:107], a[244:247], a[172:175], a[104:107]\n\t"
"v_cvt_pk_bf16_f32 v226, v232, v233\n\t"
"v_cvt_pk_bf16_f32 v227, v234, v235\n\t"
"v_mfma_f32_16x16x32_bf16 a[112:115], a[236:239], a[172:175], a[112:115]\n\t"
"ds_read_b128 a[172:175], v223 offset:6400\n\t"
"v_cvt_pk_bf16_f32 v228, v228, v229\n\t"
"v_mfma_f32_16x16x32_bf16 a[112:115], a[244:247], a[176:179], a[112:115]\n\t"
"v_cvt_pk_bf16_f32 v229, v230, v231\n\t"
"v_cvt_pk_bf16_f32 v230, v236, v237\n\t"
"v_mfma_f32_16x16x32_bf16 a[112:115], a[240:243], a[180:183], a[112:115]\n\t"
"Lpg18_%=:\n\t"
"s_cbranch_vccnz Lvr2_%=\n\t"
"buffer_load_dwordx4 a[208:211], off, s[80:83], s20 offset:1024 sc1\n\t"
"v_mfma_f32_16x16x32_bf16 a[120:123], a[240:243], a[184:187], a[120:123]\n\t"
"v_cvt_pk_bf16_f32 v231, v238, v239\n\t"
"v_mfma_f32_16x16x32_bf16 a[120:123], a[244:247], a[180:183], a[120:123]\n\t"
"v_mfma_f32_16x16x32_bf16 a[120:123], a[236:239], a[176:179], a[120:123]\n\t"
"ds_write_b128 v242, v[224:227] offset:6144\n\t"
"v_mfma_f32_16x16x32_bf16 a[128:131], a[236:239], a[180:183], a[128:131]\n\t"
"v_mfma_f32_16x16x32_bf16 a[128:131], a[244:247], a[184:187], a[128:131]\n\t"
"ds_read_b128 a[176:179], v223 offset:12544\n\t"
"v_mfma_f32_16x16x32_bf16 a[128:131], a[240:243], a[188:191], a[128:131]\n\t"
"ds_write_b128 v242, v[228:231] offset:7680\n\t"
"v_mfma_f32_16x16x32_bf16 a[136:139], a[240:243], a[192:195], a[136:139]\n\t"
"buffer_load_dwordx4 v[240:243], off, s[80:83], s20 offset:2048 sc1\n\t"
"v_mfma_f32_16x16x32_bf16 a[136:139], a[244:247], a[188:191], a[136:139]\n\t"
"ds_read_b128 a[180:183], v222 offset:20224\n\t"
"v_mfma_f32_16x16x32_bf16 a[136:139], a[236:239], a[184:187], a[136:139]\n\t"
"ds_read_b128 a[184:187], v223 offset:26368\n\t"
"v_mfma_f32_16x16x32_bf16 a[144:147], a[236:239], a[188:191], a[144:147]\n\t"
"s_mov_b32 s20, 638976\n\t"
"ds_read_b128 a[188:191], v223 offset:32512\n\t"
"v_mfma_f32_16x16x32_bf16 a[144:147], a[244:247], a[192:195], a[144:147]\n\t"
"v_mfma_f32_16x16x32_bf16 a[144:147], a[240:243], a[196:199], a[144:147]\n\t"
"v_mfma_f32_16x16x32_bf16 a[152:155], a[240:243], a[200:203], a[152:155]\n\t"
"v_mfma_f32_16x16x32_bf16 a[152:155], a[244:247], a[196:199], a[152:155]\n\t"
"v_mfma_f32_16x16x32_bf16 a[152:155], a[236:239], a[192:195], a[152:155]\n\t"
"ds_read_b128 a[192:195], v222 offset:40192\n\t"
"v_mfma_f32_16x16x32_bf16 a[160:163], a[236:239], a[196:199], a[160:163]\n\t"
"buffer_load_dwordx4 a[236:239], off, s[80:83], s20 offset:0 sc1\n\t"
"v_mfma_f32_16x16x32_bf16 a[160:163], a[244:247], a[200:203], a[160:163]\n\t"
"s_waitcnt vmcnt(10) lgkmcnt(5)\n\t"
"ds_read_b128 a[196:199], v223 offset:46336\n\t"
"v_mfma_f32_16x16x32_bf16 a[160:163], a[240:243], a[204:207], a[160:163]\n\t"
"ds_read_b128 a[200:203], v223 offset:52480\n\t"
"v_mfma_f32_16x16x32_bf16 v[200:203], a[248:251], a[168:171], v[200:203]\n\t"
"s_waitcnt lgkmcnt(5)\n\t"
"ds_read_b128 a[204:207], v222 offset:60160\n\t"
"v_mfma_f32_16x16x32_bf16 v[200:203], a[216:219], a[176:179], v[200:203]\n\t"
"buffer_load_dwordx4 a[240:243], off, s[80:83], s20 offset:1024 sc1\n\t"
"v_mfma_f32_16x16x32_bf16 v[200:203], a[252:255], a[172:175], v[200:203]\n\t"
"s_add_u32 m0, s25, 768\n\t"
"v_mfma_f32_16x16x32_bf16 v[208:211], a[248:251], a[172:175], v[208:211]\n\t"
"buffer_load_dwordx4 a[244:247], off, s[80:83], s20 offset:2048 sc1\n\t"
"v_mfma_f32_16x16x32_bf16 v[208:211], a[252:255], a[176:179], v[208:211]\n\t"
"s_waitcnt lgkmcnt(5)\n\t"
"v_mfma_f32_16x16x32_bf16 v[208:211], a[216:219], a[180:183], v[208:211]\n\t"
"s_waitcnt lgkmcnt(0)\n\t"
"s_barrier\n\t"
"v_mfma_f32_16x16x32_bf16 v[216:219], a[216:219], a[184:187], v[216:219]\n\t"
"v_mfma_f32_16x16x32_bf16 v[216:219], a[252:255], a[180:183], v[216:219]\n\t"
"buffer_load_dwordx4 v254, s[12:15], 0 offen offset:256 nt lds\n\t"
"v_mfma_f32_16x16x32_bf16 v[216:219], a[248:251], a[176:179], v[216:219]\n\t"
"s_add_u32 m0, s26, 768\n\t"
"ds_read_b64_tr_b16 v[232:233], v246 offset:512\n\t"
"v_mfma_f32_16x16x32_bf16 a[4:7], a[248:251], a[180:183], a[4:7]\n\t"
"ds_read_b64_tr_b16 v[236:237], v255 offset:512\n\t"
"v_mfma_f32_16x16x32_bf16 a[4:7], a[252:255], a[184:187], a[4:7]\n\t"
"ds_read_b64_tr_b16 v[224:225], v246 offset:6656\n\t"
"v_mfma_f32_16x16x32_bf16 a[4:7], a[216:219], a[188:191], a[4:7]\n\t"
"s_mov_b64 exec, s[36:37]\n\t"
"buffer_load_dwordx4 v254, s[12:15], s27 offen offset:256 nt lds\n\t"
"s_mov_b64 exec, -1\n\t"
"s_add_u32 m0, s24, 2304\n\t"
"s_add_u32 s28, s21, 3072\n\t"
"v_mfma_f32_16x16x32_bf16 a[12:15], a[216:219], a[192:195], a[12:15]\n\t"
"buffer_load_dwordx4 v247, s[12:15], s28 offen offset:256 nt lds\n\t"
"v_mfma_f32_16x16x32_bf16 a[12:15], a[252:255], a[188:191], a[12:15]\n\t"
"s_add_u32 m0, s24, 8448\n\t"
"s_add_u32 s28, s21, 199680\n\t"
"ds_read_b64_tr_b16 v[228:229], v255 offset:6656\n\t"
"v_mfma_f32_16x16x32_bf16 a[12:15], a[248:251], a[184:187], a[12:15]\n\t"
"buffer_load_dwordx4 v247, s[12:15], s28 offen offset:256 nt lds\n\t"
"s_add_u32 m0, s24, 14592\n\t"
"s_add_u32 s28, s21, 396288\n\t"
"v_mfma_f32_16x16x32_bf16 a[20:23], a[248:251], a[188:191], a[20:23]\n\t"
"buffer_load_dwordx4 v247, s[12:15], s28 offen offset:256 nt lds\n\t"
"v_mfma_f32_16x16x32_bf16 a[20:23], a[252:255], a[192:195], a[20:23]\n\t"
"s_add_u32 m0, s24, 22272\n\t"
"s_add_u32 s28, s21, 592896\n\t"
"v_mfma_f32_16x16x32_bf16 a[20:23], a[216:219], a[196:199], a[20:23]\n\t"
"buffer_load_dwordx4 v247, s[12:15], s28 offen offset:256 nt lds\n\t"
"s_add_u32 m0, s24, 28416\n\t"
"s_add_u32 s28, s21, 789504\n\t"
"v_mfma_f32_16x16x32_bf16 a[28:31], a[216:219], a[200:203], a[28:31]\n\t"
"buffer_load_dwordx4 v247, s[12:15], s28 offen offset:256 nt lds\n\t"
"v_mfma_f32_16x16x32_bf16 a[28:31], a[252:255], a[196:199], a[28:31]\n\t"
"s_add_u32 m0, s24, 34560\n\t"
"s_add_u32 s28, s21, 986112\n\t"
"v_mfma_f32_16x16x32_bf16 a[28:31], a[248:251], a[192:195], a[28:31]\n\t"
"buffer_load_dwordx4 v247, s[12:15], s28 offen offset:256 nt lds\n\t"
"v_mfma_f32_16x16x32_bf16 a[36:39], a[248:251], a[196:199], a[36:39]\n\t"
"s_waitcnt lgkmcnt(3)\n\t"
"v_mfma_f32_16x16x32_bf16 a[36:39], a[252:255], a[200:203], a[36:39]\n\t"
"s_waitcnt lgkmcnt(2)\n\t"
"v_mfma_f32_16x16x32_bf16 a[36:39], a[216:219], a[204:207], a[36:39]\n\t"
"s_waitcnt lgkmcnt(1)\n\t"
"v_mfma_f32_4x4x4_16b_bf16 v[232:235], v[232:233], v[220:221], 0\n\t"
"s_waitcnt lgkmcnt(0)\n\t"
"v_mfma_f32_4x4x4_16b_bf16 v[236:239], v[236:237], v[220:221], 0\n\t"
"s_waitcnt vmcnt(17)\n\t"
"v_mfma_f32_4x4x4_16b_bf16 v[224:227], v[224:225], v[220:221], 0\n\t"
"v_mfma_f32_4x4x4_16b_bf16 v[228:231], v[228:229], v[220:221], 0\n\t"
"v_mfma_f32_16x16x32_bf16 a[44:47], a[220:223], a[168:171], a[44:47]\n\t"
"v_cvt_pk_bf16_f32 v232, v232, v233\n\t"
"v_cvt_pk_bf16_f32 v233, v234, v235\n\t"
"v_mfma_f32_16x16x32_bf16 a[44:47], v[96:99], a[176:179], a[44:47]\n\t"
"v_cvt_pk_bf16_f32 v234, v236, v237\n\t"
"v_cvt_pk_bf16_f32 v235, v238, v239\n\t"
"v_mfma_f32_16x16x32_bf16 a[44:47], a[224:227], a[172:175], a[44:47]\n\t"
"ds_write_b128 v245, v[232:235] offset:512\n\t"
"v_mfma_f32_16x16x32_bf16 a[52:55], a[220:223], a[172:175], a[52:55]\n\t"
"v_cvt_pk_bf16_f32 v224, v224, v225\n\t"
"v_cvt_pk_bf16_f32 v225, v226, v227\n\t"
"v_mfma_f32_16x16x32_bf16 a[52:55], a[224:227], a[176:179], a[52:55]\n\t"
"v_cvt_pk_bf16_f32 v226, v228, v229\n\t"
"v_cvt_pk_bf16_f32 v227, v230, v231\n\t"
"v_mfma_f32_16x16x32_bf16 a[52:55], v[96:99], a[180:183], a[52:55]\n\t"
"ds_write_b128 v245, v[224:227] offset:6656\n\t"
"v_mfma_f32_16x16x32_bf16 a[60:63], v[96:99], a[184:187], a[60:63]\n\t"
"v_mfma_f32_16x16x32_bf16 a[60:63], a[224:227], a[180:183], a[60:63]\n\t"
"v_mfma_f32_16x16x32_bf16 a[60:63], a[220:223], a[176:179], a[60:63]\n\t"
"ds_read_b64_tr_b16 v[236:237], v246 offset:12800\n\t"
"v_mfma_f32_16x16x32_bf16 a[68:71], a[220:223], a[180:183], a[68:71]\n\t"
"ds_read_b64_tr_b16 v[228:229], v255 offset:12800\n\t"
"v_mfma_f32_16x16x32_bf16 a[68:71], a[224:227], a[184:187], a[68:71]\n\t"
"ds_read_b64_tr_b16 v[232:233], v246 offset:20480\n\t"
"v_mfma_f32_16x16x32_bf16 a[68:71], v[96:99], a[188:191], a[68:71]\n\t"
"ds_read_b64_tr_b16 v[224:225], v255 offset:20480\n\t"
"v_mfma_f32_16x16x32_bf16 a[76:79], v[96:99], a[192:195], a[76:79]\n\t"
"v_mfma_f32_16x16x32_bf16 a[76:79], a[224:227], a[188:191], a[76:79]\n\t"
"v_mfma_f32_16x16x32_bf16 a[76:79], a[220:223], a[184:187], a[76:79]\n\t"
"v_mfma_f32_16x16x32_bf16 a[84:87], a[220:223], a[188:191], a[84:87]\n\t"
"v_mfma_f32_16x16x32_bf16 a[84:87], a[224:227], a[192:195], a[84:87]\n\t"
"v_mfma_f32_16x16x32_bf16 a[84:87], v[96:99], a[196:199], a[84:87]\n\t"
"v_mfma_f32_16x16x32_bf16 a[92:95], v[96:99], a[200:203], a[92:95]\n\t"
"v_mfma_f32_16x16x32_bf16 a[92:95], a[224:227], a[196:199], a[92:95]\n\t"
"v_mfma_f32_16x16x32_bf16 a[92:95], a[220:223], a[192:195], a[92:95]\n\t"
"v_mfma_f32_16x16x32_bf16 a[100:103], a[220:223], a[196:199], a[100:103]\n\t"
"s_waitcnt lgkmcnt(3)\n\t"
"v_mfma_f32_16x16x32_bf16 a[100:103], a[224:227], a[200:203], a[100:103]\n\t"
"s_waitcnt lgkmcnt(2)\n\t"
"v_mfma_f32_16x16x32_bf16 a[100:103], v[96:99], a[204:207], a[100:103]\n\t"
"s_waitcnt lgkmcnt(1)\n\t"
"v_mfma_f32_4x4x4_16b_bf16 v[236:239], v[236:237], v[220:221], 0\n\t"
"s_waitcnt lgkmcnt(0)\n\t"
"v_mfma_f32_4x4x4_16b_bf16 v[228:231], v[228:229], v[220:221], 0\n\t"
"s_waitcnt vmcnt(14)\n\t"
"v_mfma_f32_4x4x4_16b_bf16 v[232:235], v[232:233], v[220:221], 0\n\t"
"s_mov_b32 s20, 651264\n\t"
"v_mfma_f32_4x4x4_16b_bf16 v[224:227], v[224:225], v[220:221], 0\n\t"
"v_mfma_f32_16x16x32_bf16 a[108:111], a[212:215], a[168:171], a[108:111]\n\t"
"ds_read_b128 a[168:171], v222 offset:1536\n\t"
"v_cvt_pk_bf16_f32 v236, v236, v237\n\t"
"v_mfma_f32_16x16x32_bf16 a[108:111], v[248:251], a[176:179], a[108:111]\n\t"
"v_cvt_pk_bf16_f32 v237, v238, v239\n\t"
"v_cvt_pk_bf16_f32 v238, v228, v229\n\t"
"v_mfma_f32_16x16x32_bf16 a[108:111], a[228:231], a[172:175], a[108:111]\n\t"
"v_cvt_pk_bf16_f32 v239, v230, v231\n\t"
"v_cvt_pk_bf16_f32 v232, v232, v233\n\t"
"v_mfma_f32_16x16x32_bf16 a[116:119], a[212:215], a[172:175], a[116:119]\n\t"
"ds_read_b128 a[172:175], v223 offset:7680\n\t"
"v_cvt_pk_bf16_f32 v233, v234, v235\n\t"
"v_mfma_f32_16x16x32_bf16 a[116:119], a[228:231], a[176:179], a[116:119]\n\t"
"v_cvt_pk_bf16_f32 v234, v224, v225\n\t"
"v_cvt_pk_bf16_f32 v235, v226, v227\n\t"
"v_mfma_f32_16x16x32_bf16 a[116:119], v[248:251], a[180:183], a[116:119]\n\t"
"ds_write_b128 v245, v[236:239] offset:12800\n\t"
"v_mfma_f32_16x16x32_bf16 a[124:127], v[248:251], a[184:187], a[124:127]\n\t"
"buffer_load_dwordx4 v[96:99], off, s[80:83], s20 offset:0 sc1\n\t"
"v_mfma_f32_16x16x32_bf16 a[124:127], a[228:231], a[180:183], a[124:127]\n\t"
"v_mfma_f32_16x16x32_bf16 a[124:127], a[212:215], a[176:179], a[124:127]\n\t"
"ds_write_b128 v245, v[232:235] offset:20480\n\t"
"v_mfma_f32_16x16x32_bf16 a[132:135], a[212:215], a[180:183], a[132:135]\n\t"
"v_mfma_f32_16x16x32_bf16 a[132:135], a[228:231], a[184:187], a[132:135]\n\t"
"ds_read_b128 a[176:179], v223 offset:13824\n\t"
"v_mfma_f32_16x16x32_bf16 a[132:135], v[248:251], a[188:191], a[132:135]\n\t"
"ds_read_b64_tr_b16 v[228:229], v246 offset:26624\n\t"
"v_mfma_f32_16x16x32_bf16 a[140:143], v[248:251], a[192:195], a[140:143]\n\t"
"ds_read_b64_tr_b16 v[224:225], v255 offset:26624\n\t"
"v_mfma_f32_16x16x32_bf16 a[140:143], a[228:231], a[188:191], a[140:143]\n\t"
"ds_read_b128 a[180:183], v222 offset:21504\n\t"
"v_mfma_f32_16x16x32_bf16 a[140:143], a[212:215], a[184:187], a[140:143]\n\t"
"ds_read_b64_tr_b16 v[236:237], v246 offset:32768\n\t"
"v_mfma_f32_16x16x32_bf16 a[148:151], a[212:215], a[188:191], a[148:151]\n\t"
"ds_read_b64_tr_b16 v[232:233], v255 offset:32768\n\t"
"v_mfma_f32_16x16x32_bf16 a[148:151], a[228:231], a[192:195], a[148:151]\n\t"
"ds_read_b128 a[184:187], v223 offset:27648\n\t"
"v_mfma_f32_16x16x32_bf16 a[148:151], v[248:251], a[196:199], a[148:151]\n\t"
"ds_read_b128 a[188:191], v223 offset:33792\n\t"
"v_mfma_f32_16x16x32_bf16 a[156:159], v[248:251], a[200:203], a[156:159]\n\t"
"v_mfma_f32_16x16x32_bf16 a[156:159], a[228:231], a[196:199], a[156:159]\n\t"
"v_mfma_f32_16x16x32_bf16 a[156:159], a[212:215], a[192:195], a[156:159]\n\t"
"ds_read_b128 a[192:195], v222 offset:41472\n\t"
"v_mfma_f32_16x16x32_bf16 a[164:167], a[212:215], a[196:199], a[164:167]\n\t"
"s_waitcnt lgkmcnt(7)\n\t"
"ds_read_b128 a[196:199], v223 offset:47616\n\t"
"v_mfma_f32_16x16x32_bf16 a[164:167], a[228:231], a[200:203], a[164:167]\n\t"
"s_waitcnt lgkmcnt(7)\n\t"
"ds_read_b128 a[200:203], v223 offset:53760\n\t"
"v_mfma_f32_16x16x32_bf16 a[164:167], v[248:251], a[204:207], a[164:167]\n\t"
"s_waitcnt lgkmcnt(6)\n\t"
"ds_read_b128 a[204:207], v222 offset:61440\n\t"
"v_mfma_f32_4x4x4_16b_bf16 v[228:231], v[228:229], v[220:221], 0\n\t"
"s_waitcnt lgkmcnt(6)\n\t"
"v_mfma_f32_4x4x4_16b_bf16 v[224:227], v[224:225], v[220:221], 0\n\t"
"s_waitcnt vmcnt(12)\n\t"
"v_mfma_f32_4x4x4_16b_bf16 v[236:239], v[236:237], v[220:221], 0\n\t"
"v_mfma_f32_4x4x4_16b_bf16 v[232:235], v[232:233], v[220:221], 0\n\t"
"Lst25_%=:\n\t"
"v_mfma_f32_16x16x32_bf16 v[0:3], a[232:235], a[168:171], v[0:3]\n\t"
"v_cvt_pk_bf16_f32 v228, v228, v229\n\t"
"v_cvt_pk_bf16_f32 v229, v230, v231\n\t"
"v_mfma_f32_16x16x32_bf16 v[0:3], v[240:243], a[176:179], v[0:3]\n\t"
"v_cvt_pk_bf16_f32 v230, v224, v225\n\t"
"v_cvt_pk_bf16_f32 v231, v226, v227\n\t"
"v_mfma_f32_16x16x32_bf16 v[0:3], a[208:211], a[172:175], v[0:3]\n\t"
"ds_write_b128 v245, v[228:231] offset:26624\n\t"
"v_mfma_f32_16x16x32_bf16 v[4:7], a[232:235], a[172:175], v[4:7]\n\t"
"s_waitcnt lgkmcnt(6)\n\t"
"v_cvt_pk_bf16_f32 v236, v236, v237\n\t"
"v_cvt_pk_bf16_f32 v237, v238, v239\n\t"
"v_mfma_f32_16x16x32_bf16 v[4:7], a[208:211], a[176:179], v[4:7]\n\t"
"v_cvt_pk_bf16_f32 v238, v232, v233\n\t"
"v_cvt_pk_bf16_f32 v239, v234, v235\n\t"
"v_mfma_f32_16x16x32_bf16 v[4:7], v[240:243], a[180:183], v[4:7]\n\t"
"ds_write_b128 v245, v[236:239] offset:32768\n\t"
"v_mfma_f32_16x16x32_bf16 v[8:11], v[240:243], a[184:187], v[8:11]\n\t"
"s_waitcnt lgkmcnt(6)\n\t"
"v_mfma_f32_16x16x32_bf16 v[8:11], a[208:211], a[180:183], v[8:11]\n\t"
"v_mfma_f32_16x16x32_bf16 v[8:11], a[232:235], a[176:179], v[8:11]\n\t"
"ds_read_b64_tr_b16 v[224:225], v246 offset:40448\n\t"
"v_mfma_f32_16x16x32_bf16 v[12:15], a[232:235], a[180:183], v[12:15]\n\t"
"s_waitcnt lgkmcnt(6)\n\t"
"ds_read_b64_tr_b16 v[232:233], v255 offset:40448\n\t"
"v_mfma_f32_16x16x32_bf16 v[12:15], a[208:211], a[184:187], v[12:15]\n\t"
"ds_read_b64_tr_b16 v[228:229], v246 offset:46592\n\t"
"v_mfma_f32_16x16x32_bf16 v[12:15], v[240:243], a[188:191], v[12:15]\n\t"
"buffer_load_dwordx4 v[248:251], off, s[80:83], s20 offset:1024 sc1\n\t"
"v_mfma_f32_16x16x32_bf16 v[16:19], v[240:243], a[192:195], v[16:19]\n\t"
"s_waitcnt lgkmcnt(7)\n\t"
"ds_read_b64_tr_b16 v[236:237], v255 offset:46592\n\t"
"v_mfma_f32_16x16x32_bf16 v[16:19], a[208:211], a[188:191], v[16:19]\n\t"
"v_mfma_f32_16x16x32_bf16 v[16:19], a[232:235], a[184:187], v[16:19]\n\t"
"buffer_load_dwordx4 a[212:215], off, s[80:83], s20 offset:2048 sc1\n\t"
"v_mfma_f32_16x16x32_bf16 v[20:23], a[232:235], a[188:191], v[20:23]\n\t"
"s_waitcnt lgkmcnt(7)\n\t"
"s_mov_b32 s20, 663552\n\t"
"v_mfma_f32_16x16x32_bf16 v[20:23], a[208:211], a[192:195], v[20:23]\n\t"
"v_mfma_f32_16x16x32_bf16 v[20:23], v[240:243], a[196:199], v[20:23]\n\t"
"v_mfma_f32_16x16x32_bf16 v[24:27], v[240:243], a[200:203], v[24:27]\n\t"
"buffer_load_dwordx4 a[216:219], off, s[80:83], s20 offset:0 sc1\n\t"
"v_mfma_f32_16x16x32_bf16 v[24:27], a[208:211], a[196:199], v[24:27]\n\t"
"s_waitcnt lgkmcnt(6)\n\t"
"v_mfma_f32_16x16x32_bf16 v[24:27], a[232:235], a[192:195], v[24:27]\n\t"
"v_mfma_f32_16x16x32_bf16 v[28:31], a[232:235], a[196:199], v[28:31]\n\t"
"s_waitcnt lgkmcnt(3)\n\t"
"v_mfma_f32_16x16x32_bf16 v[28:31], a[208:211], a[200:203], v[28:31]\n\t"
"buffer_load_dwordx4 a[208:211], off, s[80:83], s20 offset:1024 sc1\n\t"
"v_mfma_f32_16x16x32_bf16 v[28:31], v[240:243], a[204:207], v[28:31]\n\t"
"s_waitcnt lgkmcnt(1)\n\t"
"v_mfma_f32_4x4x4_16b_bf16 v[224:227], v[224:225], v[220:221], 0\n\t"
"s_waitcnt lgkmcnt(0)\n\t"
"v_mfma_f32_4x4x4_16b_bf16 v[232:235], v[232:233], v[220:221], 0\n\t"
"s_waitcnt vmcnt(13)\n\t"
"v_mfma_f32_4x4x4_16b_bf16 v[228:231], v[228:229], v[220:221], 0\n\t"
"v_mfma_f32_4x4x4_16b_bf16 v[236:239], v[236:237], v[220:221], 0\n\t"
"v_mfma_f32_16x16x32_bf16 v[32:35], a[236:239], a[168:171], v[32:35]\n\t"
"buffer_load_dwordx4 v[240:243], off, s[80:83], s20 offset:2048 sc1\n\t"
"v_mfma_f32_16x16x32_bf16 v[32:35], a[244:247], a[176:179], v[32:35]\n\t"
"s_mov_b32 s20, 675840\n\t"
"v_cvt_pk_bf16_f32 v224, v224, v225\n\t"
"v_cvt_pk_bf16_f32 v225, v226, v227\n\t"
"v_mfma_f32_16x16x32_bf16 v[32:35], a[240:243], a[172:175], v[32:35]\n\t"
"v_cvt_pk_bf16_f32 v226, v232, v233\n\t"
"v_cvt_pk_bf16_f32 v227, v234, v235\n\t"
"v_mfma_f32_16x16x32_bf16 v[36:39], a[236:239], a[172:175], v[36:39]\n\t"
"v_cvt_pk_bf16_f32 v228, v228, v229\n\t"
"v_cvt_pk_bf16_f32 v229, v230, v231\n\t"
"v_mfma_f32_16x16x32_bf16 v[36:39], a[240:243], a[176:179], v[36:39]\n\t"
"v_cvt_pk_bf16_f32 v230, v236, v237\n\t"
"v_cvt_pk_bf16_f32 v231, v238, v239\n\t"
"v_mfma_f32_16x16x32_bf16 v[36:39], a[244:247], a[180:183], v[36:39]\n\t"
"buffer_load_dwordx4 a[220:223], off, s[80:83], s20 offset:0 sc1\n\t"
"v_mfma_f32_16x16x32_bf16 v[40:43], a[244:247], a[184:187], v[40:43]\n\t"
"buffer_load_dwordx4 a[224:227], off, s[80:83], s20 offset:1024 sc1\n\t"
"v_mfma_f32_16x16x32_bf16 v[40:43], a[240:243], a[180:183], v[40:43]\n\t"
"v_mfma_f32_16x16x32_bf16 v[40:43], a[236:239], a[176:179], v[40:43]\n\t"
"ds_write_b128 v245, v[224:227] offset:40448\n\t"
"v_mfma_f32_16x16x32_bf16 v[44:47], a[236:239], a[180:183], v[44:47]\n\t"
"v_mfma_f32_16x16x32_bf16 v[44:47], a[240:243], a[184:187], v[44:47]\n\t"
"buffer_load_dwordx4 a[228:231], off, s[80:83], s20 offset:2048 sc1\n\t"
"v_mfma_f32_16x16x32_bf16 v[44:47], a[244:247], a[188:191], v[44:47]\n\t"
"s_mov_b32 s20, 688128\n\t"
"ds_write_b128 v245, v[228:231] offset:46592\n\t"
"v_mfma_f32_16x16x32_bf16 v[48:51], a[244:247], a[192:195], v[48:51]\n\t"
"v_mfma_f32_16x16x32_bf16 v[48:51], a[240:243], a[188:191], v[48:51]\n\t"
"ds_read_b64_tr_b16 v[232:233], v246 offset:52736\n\t"
"v_mfma_f32_16x16x32_bf16 v[48:51], a[236:239], a[184:187], v[48:51]\n\t"
"ds_read_b64_tr_b16 v[236:237], v255 offset:52736\n\t"
"v_mfma_f32_16x16x32_bf16 v[52:55], a[236:239], a[188:191], v[52:55]\n\t"
"buffer_load_dwordx4 a[232:235], off, s[80:83], s20 offset:0 sc1\n\t"
"v_mfma_f32_16x16x32_bf16 v[52:55], a[240:243], a[192:195], v[52:55]\n\t"
"v_mfma_f32_16x16x32_bf16 v[52:55], a[244:247], a[196:199], v[52:55]\n\t"
"v_mfma_f32_16x16x32_bf16 v[56:59], a[244:247], a[200:203], v[56:59]\n\t"
"buffer_load_dwordx4 a[248:251], off, s[80:83], s20 offset:1024 sc1\n\t"
"v_mfma_f32_16x16x32_bf16 v[56:59], a[240:243], a[196:199], v[56:59]\n\t"
"v_mfma_f32_16x16x32_bf16 v[56:59], a[236:239], a[192:195], v[56:59]\n\t"
"v_mfma_f32_16x16x32_bf16 v[60:63], a[236:239], a[196:199], v[60:63]\n\t"
"s_waitcnt lgkmcnt(1)\n\t"
"v_mfma_f32_16x16x32_bf16 v[60:63], a[240:243], a[200:203], v[60:63]\n\t"
"buffer_load_dwordx4 a[236:239], off, s[80:83], s20 offset:2048 sc1\n\t"
"v_mfma_f32_16x16x32_bf16 v[60:63], a[244:247], a[204:207], v[60:63]\n\t"
"s_waitcnt vmcnt(9) lgkmcnt(0)\n\t"
"s_mov_b32 s20, 921600\n\t"
"v_mfma_f32_4x4x4_16b_bf16 v[232:235], v[232:233], v[220:221], 0\n\t"
"v_mfma_f32_4x4x4_16b_bf16 v[236:239], v[236:237], v[220:221], 0\n\t"
"v_mfma_f32_16x16x32_bf16 v[64:67], v[96:99], a[168:171], v[64:67]\n\t"
"buffer_load_dwordx4 a[240:243], off, s[80:83], s20 offset:0 sc1\n\t"
"v_mfma_f32_16x16x32_bf16 v[64:67], a[212:215], a[176:179], v[64:67]\n\t"
"ds_read_b128 a[168:171], v222 offset:1792\n\t"
"v_cvt_pk_bf16_f32 v232, v232, v233\n\t"
"v_mfma_f32_16x16x32_bf16 v[64:67], v[248:251], a[172:175], v[64:67]\n\t"
"v_cvt_pk_bf16_f32 v233, v234, v235\n\t"
"v_cvt_pk_bf16_f32 v234, v236, v237\n\t"
"v_mfma_f32_16x16x32_bf16 v[68:71], v[96:99], a[172:175], v[68:71]\n\t"
"ds_read_b128 a[172:175], v223 offset:7936\n\t"
"v_cvt_pk_bf16_f32 v235, v238, v239\n\t"
"v_mfma_f32_16x16x32_bf16 v[68:71], v[248:251], a[176:179], v[68:71]\n\t"
"v_mfma_f32_16x16x32_bf16 v[68:71], a[212:215], a[180:183], v[68:71]\n\t"
"buffer_load_dwordx4 a[244:247], off, s[80:83], s20 offset:1024 sc1\n\t"
"v_mfma_f32_16x16x32_bf16 v[72:75], a[212:215], a[184:187], v[72:75]\n\t"
"buffer_load_dwordx4 v[236:239], off, s[80:83], s20 offset:2048 sc1\n\t"
"v_mfma_f32_16x16x32_bf16 v[72:75], v[248:251], a[180:183], v[72:75]\n\t"
"s_mov_b32 s20, 933888\n\t"
"v_mfma_f32_16x16x32_bf16 v[72:75], v[96:99], a[176:179], v[72:75]\n\t"
"ds_write_b128 v245, v[232:235] offset:52736\n\t"
"v_mfma_f32_16x16x32_bf16 v[76:79], v[96:99], a[180:183], v[76:79]\n\t"
"buffer_load_dwordx4 a[252:255], off, s[80:83], s20 offset:0 sc1\n\t"
"v_mfma_f32_16x16x32_bf16 v[76:79], v[248:251], a[184:187], v[76:79]\n\t"
"ds_read_b128 a[176:179], v223 offset:14080\n\t"
"v_mfma_f32_16x16x32_bf16 v[76:79], a[212:215], a[188:191], v[76:79]\n\t"
"ds_read_b128 a[180:183], v222 offset:21760\n\t"
"v_mfma_f32_16x16x32_bf16 v[80:83], a[212:215], a[192:195], v[80:83]\n\t"
"v_mfma_f32_16x16x32_bf16 v[80:83], v[248:251], a[188:191], v[80:83]\n\t"
"v_mfma_f32_16x16x32_bf16 v[80:83], v[96:99], a[184:187], v[80:83]\n\t"
"ds_read_b128 a[184:187], v223 offset:27904\n\t"
"v_mfma_f32_16x16x32_bf16 v[84:87], v[96:99], a[188:191], v[84:87]\n\t"
"ds_read_b128 a[188:191], v223 offset:34048\n\t"
"v_mfma_f32_16x16x32_bf16 v[84:87], v[248:251], a[192:195], v[84:87]\n\t"
"v_mfma_f32_16x16x32_bf16 v[84:87], a[212:215], a[196:199], v[84:87]\n\t"
"v_mfma_f32_16x16x32_bf16 v[88:91], a[212:215], a[200:203], v[88:91]\n\t"
"v_mfma_f32_16x16x32_bf16 v[88:91], v[248:251], a[196:199], v[88:91]\n\t"
"v_mfma_f32_16x16x32_bf16 v[88:91], v[96:99], a[192:195], v[88:91]\n\t"
"ds_read_b128 a[192:195], v222 offset:41728\n\t"
"v_mfma_f32_16x16x32_bf16 v[92:95], v[96:99], a[196:199], v[92:95]\n\t"
"buffer_load_dwordx4 v[96:99], off, s[80:83], s20 offset:1024 sc1\n\t"
"v_mfma_f32_16x16x32_bf16 v[92:95], v[248:251], a[200:203], v[92:95]\n\t"
"s_waitcnt vmcnt(11) lgkmcnt(4)\n\t"
"ds_read_b128 a[196:199], v223 offset:47872\n\t"
"v_mfma_f32_16x16x32_bf16 v[92:95], a[212:215], a[204:207], v[92:95]\n\t"
"ds_read_b128 a[200:203], v223 offset:54016\n\t"
"v_mfma_f32_16x16x32_bf16 v[100:103], a[216:219], a[168:171], v[100:103]\n\t"
"s_waitcnt lgkmcnt(5)\n\t"
"ds_read_b128 a[204:207], v222 offset:61696\n\t"
"v_mfma_f32_16x16x32_bf16 v[100:103], v[240:243], a[176:179], v[100:103]\n\t"
"buffer_load_dwordx4 v[248:251], off, s[80:83], s20 offset:2048 sc1\n\t"
"v_mfma_f32_16x16x32_bf16 v[100:103], a[208:211], a[172:175], v[100:103]\n\t"
"s_mov_b32 s20, 946176\n\t"
"s_add_u32 m0, s24, 768\n\t"
"s_add_u32 s28, s21, 0\n\t"
"v_mfma_f32_16x16x32_bf16 v[104:107], a[216:219], a[172:175], v[104:107]\n\t"
"buffer_load_dwordx4 a[212:215], off, s[80:83], s20 offset:0 sc1\n\t"
"v_mfma_f32_16x16x32_bf16 v[104:107], a[208:211], a[176:179], v[104:107]\n\t"
"s_waitcnt lgkmcnt(5)\n\t"
"v_mfma_f32_16x16x32_bf16 v[104:107], v[240:243], a[180:183], v[104:107]\n\t"
"s_waitcnt lgkmcnt(0)\n\t"
"s_barrier\n\t"
"v_mfma_f32_16x16x32_bf16 v[108:111], v[240:243], a[184:187], v[108:111]\n\t"
"v_mfma_f32_16x16x32_bf16 v[108:111], a[208:211], a[180:183], v[108:111]\n\t"
"buffer_load_dwordx4 v247, s[12:15], s28 offen offset:256 nt lds\n\t"
"v_mfma_f32_16x16x32_bf16 v[108:111], a[216:219], a[176:179], v[108:111]\n\t"
"s_add_u32 m0, s24, 6912\n\t"
"s_add_u32 s28, s21, 196608\n\t"
"v_mfma_f32_16x16x32_bf16 v[112:115], a[216:219], a[180:183], v[112:115]\n\t"
"buffer_load_dwordx4 v247, s[12:15], s28 offen offset:256 nt lds\n\t"
"v_mfma_f32_16x16x32_bf16 v[112:115], a[208:211], a[184:187], v[112:115]\n\t"
"s_add_u32 m0, s24, 13056\n\t"
"s_add_u32 s28, s21, 393216\n\t"
"v_mfma_f32_16x16x32_bf16 v[112:115], v[240:243], a[188:191], v[112:115]\n\t"
"buffer_load_dwordx4 v247, s[12:15], s28 offen offset:256 nt lds\n\t"
"s_add_u32 m0, s24, 20736\n\t"
"s_add_u32 s28, s21, 589824\n\t"
"v_mfma_f32_16x16x32_bf16 v[116:119], v[240:243], a[192:195], v[116:119]\n\t"
"buffer_load_dwordx4 v247, s[12:15], s28 offen offset:256 nt lds\n\t"
"v_mfma_f32_16x16x32_bf16 v[116:119], a[208:211], a[188:191], v[116:119]\n\t"
"s_add_u32 m0, s24, 26880\n\t"
"s_add_u32 s28, s21, 786432\n\t"
"v_mfma_f32_16x16x32_bf16 v[116:119], a[216:219], a[184:187], v[116:119]\n\t"
"buffer_load_dwordx4 v247, s[12:15], s28 offen offset:256 nt lds\n\t"
"v_mfma_f32_16x16x32_bf16 v[120:123], a[216:219], a[188:191], v[120:123]\n\t"
"s_add_u32 m0, s24, 33024\n\t"
"s_add_u32 s28, s21, 983040\n\t"
"v_mfma_f32_16x16x32_bf16 v[120:123], a[208:211], a[192:195], v[120:123]\n\t"
"buffer_load_dwordx4 v247, s[12:15], s28 offen offset:256 nt lds\n\t"
"v_mfma_f32_16x16x32_bf16 v[120:123], v[240:243], a[196:199], v[120:123]\n\t"
"v_mfma_f32_16x16x32_bf16 v[124:127], v[240:243], a[200:203], v[124:127]\n\t"
"v_mfma_f32_16x16x32_bf16 v[124:127], a[208:211], a[196:199], v[124:127]\n\t"
"v_mfma_f32_16x16x32_bf16 v[124:127], a[216:219], a[192:195], v[124:127]\n\t"
"v_mfma_f32_16x16x32_bf16 v[128:131], a[216:219], a[196:199], v[128:131]\n\t"
"s_waitcnt vmcnt(16)\n\t"
"v_mfma_f32_16x16x32_bf16 v[128:131], a[208:211], a[200:203], v[128:131]\n\t"
"v_mfma_f32_16x16x32_bf16 v[128:131], v[240:243], a[204:207], v[128:131]\n\t"
"v_mfma_f32_16x16x32_bf16 v[132:135], a[220:223], a[168:171], v[132:135]\n\t"
"v_mfma_f32_16x16x32_bf16 v[132:135], a[228:231], a[176:179], v[132:135]\n\t"
"v_mfma_f32_16x16x32_bf16 v[132:135], a[224:227], a[172:175], v[132:135]\n\t"
"v_mfma_f32_16x16x32_bf16 v[136:139], a[220:223], a[172:175], v[136:139]\n\t"
"v_mfma_f32_16x16x32_bf16 v[136:139], a[224:227], a[176:179], v[136:139]\n\t"
"v_mfma_f32_16x16x32_bf16 v[136:139], a[228:231], a[180:183], v[136:139]\n\t"
"v_mfma_f32_16x16x32_bf16 v[140:143], a[228:231], a[184:187], v[140:143]\n\t"
"v_mfma_f32_16x16x32_bf16 v[140:143], a[224:227], a[180:183], v[140:143]\n\t"
"v_mfma_f32_16x16x32_bf16 v[140:143], a[220:223], a[176:179], v[140:143]\n\t"
"v_mfma_f32_16x16x32_bf16 v[144:147], a[220:223], a[180:183], v[144:147]\n\t"
"v_mfma_f32_16x16x32_bf16 v[144:147], a[224:227], a[184:187], v[144:147]\n\t"
"v_mfma_f32_16x16x32_bf16 v[144:147], a[228:231], a[188:191], v[144:147]\n\t"
"v_mfma_f32_16x16x32_bf16 v[148:151], a[228:231], a[192:195], v[148:151]\n\t"
"v_mfma_f32_16x16x32_bf16 v[148:151], a[224:227], a[188:191], v[148:151]\n\t"
"v_mfma_f32_16x16x32_bf16 v[148:151], a[220:223], a[184:187], v[148:151]\n\t"
"v_mfma_f32_16x16x32_bf16 v[152:155], a[220:223], a[188:191], v[152:155]\n\t"
"v_mfma_f32_16x16x32_bf16 v[152:155], a[224:227], a[192:195], v[152:155]\n\t"
"v_mfma_f32_16x16x32_bf16 v[152:155], a[228:231], a[196:199], v[152:155]\n\t"
"v_mfma_f32_16x16x32_bf16 v[156:159], a[228:231], a[200:203], v[156:159]\n\t"
"v_mfma_f32_16x16x32_bf16 v[156:159], a[224:227], a[196:199], v[156:159]\n\t"
"v_mfma_f32_16x16x32_bf16 v[156:159], a[220:223], a[192:195], v[156:159]\n\t"
"v_mfma_f32_16x16x32_bf16 v[160:163], a[220:223], a[196:199], v[160:163]\n\t"
"s_waitcnt vmcnt(13)\n\t"
"v_mfma_f32_16x16x32_bf16 v[160:163], a[224:227], a[200:203], v[160:163]\n\t"
"v_mfma_f32_16x16x32_bf16 v[160:163], a[228:231], a[204:207], v[160:163]\n\t"
"v_mfma_f32_16x16x32_bf16 v[164:167], a[232:235], a[168:171], v[164:167]\n\t"
"ds_read_b128 a[168:171], v222 offset:2048\n\t"
"v_mfma_f32_16x16x32_bf16 v[164:167], a[236:239], a[176:179], v[164:167]\n\t"
"v_mfma_f32_16x16x32_bf16 v[164:167], a[248:251], a[172:175], v[164:167]\n\t"
"buffer_load_dwordx4 v[240:243], off, s[80:83], s20 offset:1024 sc1\n\t"
"v_mfma_f32_16x16x32_bf16 v[168:171], a[232:235], a[172:175], v[168:171]\n\t"
"ds_read_b128 a[172:175], v223 offset:8192\n\t"
"v_mfma_f32_16x16x32_bf16 v[168:171], a[248:251], a[176:179], v[168:171]\n\t"
"v_mfma_f32_16x16x32_bf16 v[168:171], a[236:239], a[180:183], v[168:171]\n\t"
"v_mfma_f32_16x16x32_bf16 v[172:175], a[236:239], a[184:187], v[172:175]\n\t"
"v_mfma_f32_16x16x32_bf16 v[172:175], a[248:251], a[180:183], v[172:175]\n\t"
"v_mfma_f32_16x16x32_bf16 v[172:175], a[232:235], a[176:179], v[172:175]\n\t"
"ds_read_b128 a[176:179], v223 offset:14336\n\t"
"v_mfma_f32_16x16x32_bf16 v[176:179], a[232:235], a[180:183], v[176:179]\n\t"
"ds_read_b128 a[180:183], v222 offset:22016\n\t"
"v_mfma_f32_16x16x32_bf16 v[176:179], a[248:251], a[184:187], v[176:179]\n\t"
"v_mfma_f32_16x16x32_bf16 v[176:179], a[236:239], a[188:191], v[176:179]\n\t"
"v_mfma_f32_16x16x32_bf16 v[180:183], a[236:239], a[192:195], v[180:183]\n\t"
"v_mfma_f32_16x16x32_bf16 v[180:183], a[248:251], a[188:191], v[180:183]\n\t"
"v_mfma_f32_16x16x32_bf16 v[180:183], a[232:235], a[184:187], v[180:183]\n\t"
"ds_read_b128 a[184:187], v223 offset:28160\n\t"
"v_mfma_f32_16x16x32_bf16 v[184:187], a[232:235], a[188:191], v[184:187]\n\t"
"ds_read_b128 a[188:191], v223 offset:34304\n\t"
"v_mfma_f32_16x16x32_bf16 v[184:187], a[248:251], a[192:195], v[184:187]\n\t"
"v_mfma_f32_16x16x32_bf16 v[184:187], a[236:239], a[196:199], v[184:187]\n\t"
"v_mfma_f32_16x16x32_bf16 v[188:191], a[236:239], a[200:203], v[188:191]\n\t"
"v_mfma_f32_16x16x32_bf16 v[188:191], a[248:251], a[196:199], v[188:191]\n\t"
"v_mfma_f32_16x16x32_bf16 v[188:191], a[232:235], a[192:195], v[188:191]\n\t"
"ds_read_b128 a[192:195], v222 offset:41984\n\t"
"v_mfma_f32_16x16x32_bf16 v[192:195], a[232:235], a[196:199], v[192:195]\n\t"
"s_waitcnt vmcnt(11) lgkmcnt(4)\n\t"
"ds_read_b128 a[196:199], v223 offset:48128\n\t"
"v_mfma_f32_16x16x32_bf16 v[192:195], a[248:251], a[200:203], v[192:195]\n\t"
"ds_read_b128 a[200:203], v223 offset:54272\n\t"
"v_mfma_f32_16x16x32_bf16 v[192:195], a[236:239], a[204:207], v[192:195]\n\t"
"ds_read_b128 a[204:207], v222 offset:61952\n\t"
"Lst26_%=:\n\t"
"v_mfma_f32_16x16x32_bf16 v[0:3], a[240:243], a[168:171], v[0:3]\n\t"
"s_waitcnt lgkmcnt(6)\n\t"
"s_add_u32 s23, s40, 6291456\n\t"
"s_add_u32 s22, s22, 6291456\n\t"
"v_mfma_f32_16x16x32_bf16 v[0:3], v[236:239], a[176:179], v[0:3]\n\t"
"v_mfma_f32_16x16x32_bf16 v[0:3], a[244:247], a[172:175], v[0:3]\n\t"
"v_mfma_f32_16x16x32_bf16 v[4:7], a[240:243], a[172:175], v[4:7]\n\t"
"s_waitcnt lgkmcnt(5)\n\t"
"v_mfma_f32_16x16x32_bf16 v[4:7], a[244:247], a[176:179], v[4:7]\n\t"
"v_mfma_f32_16x16x32_bf16 v[4:7], v[236:239], a[180:183], v[4:7]\n\t"
"v_mfma_f32_16x16x32_bf16 v[8:11], v[236:239], a[184:187], v[8:11]\n\t"
"s_waitcnt lgkmcnt(4)\n\t"
"v_mfma_f32_16x16x32_bf16 v[8:11], a[244:247], a[180:183], v[8:11]\n\t"
"v_mfma_f32_16x16x32_bf16 v[8:11], a[240:243], a[176:179], v[8:11]\n\t"
"buffer_load_dwordx4 a[208:211], off, s[80:83], s20 offset:2048 sc1\n\t"
"v_mfma_f32_16x16x32_bf16 v[12:15], a[240:243], a[180:183], v[12:15]\n\t"
"s_waitcnt lgkmcnt(3)\n\t"
"s_mov_b32 s20, 958464\n\t"
"v_mfma_f32_16x16x32_bf16 v[12:15], a[244:247], a[184:187], v[12:15]\n\t"
"v_mfma_f32_16x16x32_bf16 v[12:15], v[236:239], a[188:191], v[12:15]\n\t"
"v_mfma_f32_16x16x32_bf16 v[16:19], v[236:239], a[192:195], v[16:19]\n\t"
"buffer_load_dwordx4 a[216:219], off, s[80:83], s20 offset:0 sc1\n\t"
"v_mfma_f32_16x16x32_bf16 v[16:19], a[244:247], a[188:191], v[16:19]\n\t"
"s_waitcnt lgkmcnt(2)\n\t"
"v_mfma_f32_16x16x32_bf16 v[16:19], a[240:243], a[184:187], v[16:19]\n\t"
"v_mfma_f32_16x16x32_bf16 v[20:23], a[240:243], a[188:191], v[20:23]\n\t"
"s_waitcnt lgkmcnt(1)\n\t"
"v_mfma_f32_16x16x32_bf16 v[20:23], a[244:247], a[192:195], v[20:23]\n\t"
"buffer_load_dwordx4 a[220:223], off, s[80:83], s20 offset:1024 sc1\n\t"
"v_mfma_f32_16x16x32_bf16 v[20:23], v[236:239], a[196:199], v[20:23]\n\t"
"v_mfma_f32_16x16x32_bf16 v[24:27], v[236:239], a[200:203], v[24:27]\n\t"
"s_waitcnt lgkmcnt(0)\n\t"
"v_mfma_f32_16x16x32_bf16 v[24:27], a[244:247], a[196:199], v[24:27]\n\t"
"v_mfma_f32_16x16x32_bf16 v[24:27], a[240:243], a[192:195], v[24:27]\n\t"
"buffer_load_dwordx4 a[224:227], off, s[80:83], s20 offset:2048 sc1\n\t"
"v_mfma_f32_16x16x32_bf16 v[28:31], a[240:243], a[196:199], v[28:31]\n\t"
"s_waitcnt vmcnt(12)\n\t"
"s_mov_b32 s20, 970752\n\t"
"v_mfma_f32_16x16x32_bf16 v[28:31], a[244:247], a[200:203], v[28:31]\n\t"
"v_mfma_f32_16x16x32_bf16 v[28:31], v[236:239], a[204:207], v[28:31]\n\t"
"v_mfma_f32_16x16x32_bf16 v[32:35], a[252:255], a[168:171], v[32:35]\n\t"
"buffer_load_dwordx4 a[228:231], off, s[80:83], s20 offset:0 sc1\n\t"
"v_mfma_f32_16x16x32_bf16 v[32:35], v[248:251], a[176:179], v[32:35]\n\t"
"v_mfma_f32_16x16x32_bf16 v[32:35], v[96:99], a[172:175], v[32:35]\n\t"
"v_mfma_f32_16x16x32_bf16 v[36:39], a[252:255], a[172:175], v[36:39]\n\t"
"v_mfma_f32_16x16x32_bf16 v[36:39], v[96:99], a[176:179], v[36:39]\n\t"
"buffer_load_dwordx4 a[232:235], off, s[80:83], s20 offset:1024 sc1\n\t"
"v_mfma_f32_16x16x32_bf16 v[36:39], v[248:251], a[180:183], v[36:39]\n\t"
"v_mfma_f32_16x16x32_bf16 v[40:43], v[248:251], a[184:187], v[40:43]\n\t"
"v_mfma_f32_16x16x32_bf16 v[40:43], v[96:99], a[180:183], v[40:43]\n\t"
"v_mfma_f32_16x16x32_bf16 v[40:43], a[252:255], a[176:179], v[40:43]\n\t"
"buffer_load_dwordx4 a[236:239], off, s[80:83], s20 offset:2048 sc1\n\t"
"v_mfma_f32_16x16x32_bf16 v[44:47], a[252:255], a[180:183], v[44:47]\n\t"
"s_mov_b32 s20, 983040\n\t"
"v_mfma_f32_16x16x32_bf16 v[44:47], v[96:99], a[184:187], v[44:47]\n\t"
"v_mfma_f32_16x16x32_bf16 v[44:47], v[248:251], a[188:191], v[44:47]\n\t"
"v_mfma_f32_16x16x32_bf16 v[48:51], v[248:251], a[192:195], v[48:51]\n\t"
"buffer_load_dwordx4 a[240:243], off, s[80:83], s20 offset:0 sc1\n\t"
"v_mfma_f32_16x16x32_bf16 v[48:51], v[96:99], a[188:191], v[48:51]\n\t"
"v_mfma_f32_16x16x32_bf16 v[48:51], a[252:255], a[184:187], v[48:51]\n\t"
"s_waitcnt vmcnt(7)\n\t"
"v_mfma_f32_16x16x32_bf16 v[52:55], a[252:255], a[188:191], v[52:55]\n\t"
"v_mfma_f32_16x16x32_bf16 v[52:55], v[96:99], a[192:195], v[52:55]\n\t"
"buffer_load_dwordx4 a[244:247], off, s[80:83], s20 offset:1024 sc1\n\t"
"v_mfma_f32_16x16x32_bf16 v[52:55], v[248:251], a[196:199], v[52:55]\n\t"
"Lpg19_%=:\n\t"
"s_cbranch_vccnz Lvr2_%=\n\t"
"v_mfma_f32_16x16x32_bf16 v[56:59], v[248:251], a[200:203], v[56:59]\n\t"
"v_mfma_f32_16x16x32_bf16 v[56:59], v[96:99], a[196:199], v[56:59]\n\t"
"v_mfma_f32_16x16x32_bf16 v[56:59], a[252:255], a[192:195], v[56:59]\n\t"
"buffer_load_dwordx4 a[248:251], off, s[80:83], s20 offset:2048 sc1\n\t"
"v_mfma_f32_16x16x32_bf16 v[60:63], a[252:255], a[196:199], v[60:63]\n\t"
"s_mov_b32 s20, 995328\n\t"
"v_mfma_f32_16x16x32_bf16 v[60:63], v[96:99], a[200:203], v[60:63]\n\t"
"v_mfma_f32_16x16x32_bf16 v[60:63], v[248:251], a[204:207], v[60:63]\n\t"
"v_mfma_f32_16x16x32_bf16 v[64:67], a[212:215], a[168:171], v[64:67]\n\t"
"buffer_load_dwordx4 v[96:99], off, s[80:83], s20 offset:0 sc1\n\t"
"v_mfma_f32_16x16x32_bf16 v[64:67], a[208:211], a[176:179], v[64:67]\n\t"
"ds_read_b128 a[168:171], v222 offset:2304\n\t"
"v_mfma_f32_16x16x32_bf16 v[64:67], v[240:243], a[172:175], v[64:67]\n\t"
"v_mfma_f32_16x16x32_bf16 v[68:71], a[212:215], a[172:175], v[68:71]\n\t"
"buffer_load_dwordx4 v[248:251], off, s[80:83], s20 offset:1024 sc1\n\t"
"v_mfma_f32_16x16x32_bf16 v[68:71], v[240:243], a[176:179], v[68:71]\n\t"
"ds_read_b128 a[172:175], v223 offset:8448\n\t"
"v_mfma_f32_16x16x32_bf16 v[68:71], a[208:211], a[180:183], v[68:71]\n\t"
"v_mfma_f32_16x16x32_bf16 v[72:75], a[208:211], a[184:187], v[72:75]\n\t"
"buffer_load_dwordx4 a[252:255], off, s[80:83], s20 offset:2048 sc1\n\t"
"v_mfma_f32_16x16x32_bf16 v[72:75], v[240:243], a[180:183], v[72:75]\n\t"
"v_mfma_f32_16x16x32_bf16 v[72:75], a[212:215], a[176:179], v[72:75]\n\t"
"ds_read_b128 a[176:179], v223 offset:14592\n\t"
"v_mfma_f32_16x16x32_bf16 v[76:79], a[212:215], a[180:183], v[76:79]\n\t"
"ds_read_b128 a[180:183], v222 offset:22272\n\t"
"v_mfma_f32_16x16x32_bf16 v[76:79], v[240:243], a[184:187], v[76:79]\n\t"
"v_mfma_f32_16x16x32_bf16 v[76:79], a[208:211], a[188:191], v[76:79]\n\t"
"v_mfma_f32_16x16x32_bf16 v[80:83], a[208:211], a[192:195], v[80:83]\n\t"
"v_mfma_f32_16x16x32_bf16 v[80:83], v[240:243], a[188:191], v[80:83]\n\t"
"v_mfma_f32_16x16x32_bf16 v[80:83], a[212:215], a[184:187], v[80:83]\n\t"
"ds_read_b128 a[184:187], v223 offset:28416\n\t"
"v_mfma_f32_16x16x32_bf16 v[84:87], a[212:215], a[188:191], v[84:87]\n\t"
"s_mov_b32 s20, 1007616\n\t"
"ds_read_b128 a[188:191], v223 offset:34560\n\t"
"v_mfma_f32_16x16x32_bf16 v[84:87], v[240:243], a[192:195], v[84:87]\n\t"
"v_mfma_f32_16x16x32_bf16 v[84:87], a[208:211], a[196:199], v[84:87]\n\t"
"v_mfma_f32_16x16x32_bf16 v[88:91], a[208:211], a[200:203], v[88:91]\n\t"
"v_mfma_f32_16x16x32_bf16 v[88:91], v[240:243], a[196:199], v[88:91]\n\t"
"v_mfma_f32_16x16x32_bf16 v[88:91], a[212:215], a[192:195], v[88:91]\n\t"
"ds_read_b128 a[192:195], v222 offset:42240\n\t"
"v_mfma_f32_16x16x32_bf16 v[92:95], a[212:215], a[196:199], v[92:95]\n\t"
"buffer_load_dwordx4 a[212:215], off, s[80:83], s20 offset:0 sc1\n\t"
"v_mfma_f32_16x16x32_bf16 v[92:95], v[240:243], a[200:203], v[92:95]\n\t"
"s_waitcnt vmcnt(10) lgkmcnt(4)\n\t"
"ds_read_b128 a[196:199], v223 offset:48384\n\t"
"v_mfma_f32_16x16x32_bf16 v[92:95], a[208:211], a[204:207], v[92:95]\n\t"
"global_load_dwordx4 v[240:243], v252, s[48:49] offset:2048\n\t"
"v_mfma_f32_16x16x32_bf16 v[100:103], a[216:219], a[168:171], v[100:103]\n\t"
"s_waitcnt lgkmcnt(4)\n\t"
"ds_read_b128 a[200:203], v223 offset:54528\n\t"
"v_mfma_f32_16x16x32_bf16 v[100:103], a[224:227], a[176:179], v[100:103]\n\t"
"ds_read_b128 a[204:207], v222 offset:62208\n\t"
"v_mfma_f32_16x16x32_bf16 v[100:103], a[220:223], a[172:175], v[100:103]\n\t"
"v_mfma_f32_16x16x32_bf16 v[104:107], a[216:219], a[172:175], v[104:107]\n\t"
"s_waitcnt lgkmcnt(5)\n\t"
"v_mfma_f32_16x16x32_bf16 v[104:107], a[220:223], a[176:179], v[104:107]\n\t"
"global_load_dwordx4 a[208:211], v252, s[46:47] offset:1024\n\t"
"v_mfma_f32_16x16x32_bf16 v[104:107], a[224:227], a[180:183], v[104:107]\n\t"
"v_mfma_f32_16x16x32_bf16 v[108:111], a[224:227], a[184:187], v[108:111]\n\t"
"s_waitcnt lgkmcnt(4)\n\t"
"v_mfma_f32_16x16x32_bf16 v[108:111], a[220:223], a[180:183], v[108:111]\n\t"
"v_mfma_f32_16x16x32_bf16 v[108:111], a[216:219], a[176:179], v[108:111]\n\t"
"v_mfma_f32_16x16x32_bf16 v[112:115], a[216:219], a[180:183], v[112:115]\n\t"
"s_waitcnt lgkmcnt(3)\n\t"
"v_mfma_f32_16x16x32_bf16 v[112:115], a[220:223], a[184:187], v[112:115]\n\t"
"v_mfma_f32_16x16x32_bf16 v[112:115], a[224:227], a[188:191], v[112:115]\n\t"
"v_mfma_f32_16x16x32_bf16 v[116:119], a[224:227], a[192:195], v[116:119]\n\t"
"s_waitcnt lgkmcnt(2)\n\t"
"v_mfma_f32_16x16x32_bf16 v[116:119], a[220:223], a[188:191], v[116:119]\n\t"
"v_mfma_f32_16x16x32_bf16 v[116:119], a[216:219], a[184:187], v[116:119]\n\t"
"v_mfma_f32_16x16x32_bf16 v[120:123], a[216:219], a[188:191], v[120:123]\n\t"
"s_waitcnt lgkmcnt(1)\n\t"
"v_mfma_f32_16x16x32_bf16 v[120:123], a[220:223], a[192:195], v[120:123]\n\t"
"v_mfma_f32_16x16x32_bf16 v[120:123], a[224:227], a[196:199], v[120:123]\n\t"
"v_mfma_f32_16x16x32_bf16 v[124:127], a[224:227], a[200:203], v[124:127]\n\t"
"s_waitcnt lgkmcnt(0)\n\t"
"v_mfma_f32_16x16x32_bf16 v[124:127], a[220:223], a[196:199], v[124:127]\n\t"
"v_mfma_f32_16x16x32_bf16 v[124:127], a[216:219], a[192:195], v[124:127]\n\t"
"v_mfma_f32_16x16x32_bf16 v[128:131], a[216:219], a[196:199], v[128:131]\n\t"
"buffer_load_dwordx4 a[216:219], off, s[80:83], s20 offset:1024 sc1\n\t"
"v_mfma_f32_16x16x32_bf16 v[128:131], a[220:223], a[200:203], v[128:131]\n\t"
"s_waitcnt vmcnt(10)\n\t"
"v_mfma_f32_16x16x32_bf16 v[128:131], a[224:227], a[204:207], v[128:131]\n\t"
"s_add_u32 m0, s24, 42240\n\t"
"s_add_u32 s28, s21, 1182720\n\t"
"v_mfma_f32_16x16x32_bf16 v[132:135], a[228:231], a[168:171], v[132:135]\n\t"
"buffer_load_dwordx4 a[220:223], off, s[80:83], s20 offset:2048 sc1\n\t"
"v_mfma_f32_16x16x32_bf16 v[132:135], a[236:239], a[176:179], v[132:135]\n\t"
"v_mfma_f32_16x16x32_bf16 v[132:135], a[232:235], a[172:175], v[132:135]\n\t"
"v_mfma_f32_16x16x32_bf16 v[136:139], a[228:231], a[172:175], v[136:139]\n\t"
"buffer_load_dwordx4 v247, s[12:15], s28 offen offset:256 nt lds\n\t"
"v_mfma_f32_16x16x32_bf16 v[136:139], a[232:235], a[176:179], v[136:139]\n\t"
"s_add_u32 m0, s24, 48384\n\t"
"s_add_u32 s28, s21, 1379328\n\t"
"v_mfma_f32_16x16x32_bf16 v[136:139], a[236:239], a[180:183], v[136:139]\n\t"
"s_barrier\n\t"
"v_mfma_f32_16x16x32_bf16 v[140:143], a[236:239], a[184:187], v[140:143]\n\t"
"v_mfma_f32_16x16x32_bf16 v[140:143], a[232:235], a[180:183], v[140:143]\n\t"
"buffer_load_dwordx4 v247, s[12:15], s28 offen offset:256 nt lds\n\t"
"v_mfma_f32_16x16x32_bf16 v[140:143], a[228:231], a[176:179], v[140:143]\n\t"
"s_add_u32 m0, s24, 54528\n\t"
"s_add_u32 s28, s21, 1575936\n\t"
"s_waitcnt vmcnt(5)\n\t"
"ds_read_b64_tr_b16 v[224:225], v240 offset:1024\n\t"
"v_mfma_f32_16x16x32_bf16 v[144:147], a[228:231], a[180:183], v[144:147]\n\t"
"buffer_load_dwordx4 v247, s[12:15], s28 offen offset:256 nt lds\n\t"
"v_mfma_f32_16x16x32_bf16 v[144:147], a[232:235], a[184:187], v[144:147]\n\t"
"s_add_u32 m0, s24, 40704\n\t"
"s_add_u32 s28, s21, 1179648\n\t"
"ds_read_b64_tr_b16 v[226:227], v240 offset:2560\n\t"
"v_mfma_f32_16x16x32_bf16 v[144:147], a[236:239], a[188:191], v[144:147]\n\t"
"buffer_load_dwordx4 v247, s[12:15], s28 offen offset:256 nt lds\n\t"
"s_add_u32 m0, s24, 46848\n\t"
"s_add_u32 s28, s21, 1376256\n\t"
"v_mfma_f32_16x16x32_bf16 v[148:151], a[236:239], a[192:195], v[148:151]\n\t"
"buffer_load_dwordx4 v247, s[12:15], s28 offen offset:256 nt lds\n\t"
"v_mfma_f32_16x16x32_bf16 v[148:151], a[232:235], a[188:191], v[148:151]\n\t"
"s_add_u32 m0, s24, 52992\n\t"
"s_add_u32 s28, s21, 1572864\n\t"
"ds_read_b64_tr_b16 v[228:229], v241 offset:1024\n\t"
"v_mfma_f32_16x16x32_bf16 v[148:151], a[228:231], a[184:187], v[148:151]\n\t"
"buffer_load_dwordx4 v247, s[12:15], s28 offen offset:256 nt lds\n\t"
"v_mfma_f32_16x16x32_bf16 v[152:155], a[228:231], a[188:191], v[152:155]\n\t"
"ds_read_b64_tr_b16 v[230:231], v241 offset:2560\n\t"
"v_mfma_f32_16x16x32_bf16 v[152:155], a[232:235], a[192:195], v[152:155]\n\t"
"ds_read_b64_tr_b16 v[236:237], v240 offset:20992\n\t"
"v_mfma_f32_16x16x32_bf16 v[152:155], a[236:239], a[196:199], v[152:155]\n\t"
"ds_read_b64_tr_b16 v[238:239], v240 offset:22528\n\t"
"v_mfma_f32_16x16x32_bf16 v[156:159], a[236:239], a[200:203], v[156:159]\n\t"
"ds_read_b64_tr_b16 v[232:233], v241 offset:20992\n\t"
"v_mfma_f32_16x16x32_bf16 v[156:159], a[232:235], a[196:199], v[156:159]\n\t"
"ds_read_b64_tr_b16 v[234:235], v241 offset:22528\n\t"
"v_mfma_f32_16x16x32_bf16 v[156:159], a[228:231], a[192:195], v[156:159]\n\t"
"v_mfma_f32_16x16x32_bf16 v[160:163], a[228:231], a[196:199], v[160:163]\n\t"
"s_waitcnt vmcnt(8) lgkmcnt(6)\n\t"
"v_mfma_f32_16x16x32_bf16 v[160:163], a[232:235], a[200:203], v[160:163]\n\t"
"s_waitcnt lgkmcnt(4)\n\t"
"v_mfma_f32_16x16x32_bf16 v[160:163], a[236:239], a[204:207], v[160:163]\n\t"
"s_waitcnt lgkmcnt(2)\n\t"
"v_mfma_f32_16x16x32_bf16 v[224:227], v[224:227], a[208:211], 0\n\t"
"s_waitcnt lgkmcnt(0)\n\t"
"v_mfma_f32_16x16x32_bf16 v[228:231], v[228:231], a[208:211], 0\n\t"
"v_mfma_f32_16x16x32_bf16 v[236:239], v[236:239], a[208:211], 0\n\t"
"v_mfma_f32_16x16x32_bf16 v[232:235], v[232:235], a[208:211], 0\n\t"
"v_mfma_f32_16x16x32_bf16 v[164:167], a[240:243], a[168:171], v[164:167]\n\t"
"ds_read_b128 a[168:171], v222 offset:512\n\t"
"v_mfma_f32_16x16x32_bf16 v[164:167], a[248:251], a[176:179], v[164:167]\n\t"
"v_cvt_pk_bf16_f32 v224, v224, v225\n\t"
"v_cvt_pk_bf16_f32 v225, v226, v227\n\t"
"v_mfma_f32_16x16x32_bf16 v[164:167], a[244:247], a[172:175], v[164:167]\n\t"
"v_cvt_pk_bf16_f32 v226, v228, v229\n\t"
"v_cvt_pk_bf16_f32 v227, v230, v231\n\t"
"v_mfma_f32_16x16x32_bf16 v[168:171], a[240:243], a[172:175], v[168:171]\n\t"
"ds_read_b128 a[172:175], v223 offset:6656\n\t"
"v_cvt_pk_bf16_f32 v236, v236, v237\n\t"
"v_mfma_f32_16x16x32_bf16 v[168:171], a[244:247], a[176:179], v[168:171]\n\t"
"v_cvt_pk_bf16_f32 v237, v238, v239\n\t"
"v_cvt_pk_bf16_f32 v238, v232, v233\n\t"
"v_mfma_f32_16x16x32_bf16 v[168:171], a[248:251], a[180:183], v[168:171]\n\t"
"ds_write_b128 v243, v[224:227] offset:2048\n\t"
"v_mfma_f32_16x16x32_bf16 v[172:175], a[248:251], a[184:187], v[172:175]\n\t"
"s_mov_b32 s20, 1019904\n\t"
"v_cvt_pk_bf16_f32 v239, v234, v235\n\t"
"v_add_u32 v240, 59904, v240\n\t"
"v_mfma_f32_16x16x32_bf16 v[172:175], a[244:247], a[180:183], v[172:175]\n\t"
"v_add_u32 v241, 59904, v241\n\t"
"v_mfma_f32_16x16x32_bf16 v[172:175], a[240:243], a[176:179], v[172:175]\n\t"
"ds_write_b128 v243, v[236:239] offset:22016\n\t"
"v_mfma_f32_16x16x32_bf16 v[176:179], a[240:243], a[180:183], v[176:179]\n\t"
"v_add_u32 v243, 59904, v243\n\t"
"v_mfma_f32_16x16x32_bf16 v[176:179], a[244:247], a[184:187], v[176:179]\n\t"
"ds_read_b128 a[176:179], v223 offset:12800\n\t"
"v_mfma_f32_16x16x32_bf16 v[176:179], a[248:251], a[188:191], v[176:179]\n\t"
"ds_read_b64_tr_b16 v[228:229], v240 offset:1024\n\t"
"v_mfma_f32_16x16x32_bf16 v[180:183], a[248:251], a[192:195], v[180:183]\n\t"
"buffer_load_dwordx4 a[224:227], off, s[80:83], s20 offset:0 sc1\n\t"
"v_mfma_f32_16x16x32_bf16 v[180:183], a[244:247], a[188:191], v[180:183]\n\t"
"ds_read_b64_tr_b16 v[230:231], v240 offset:2560\n\t"
"v_mfma_f32_16x16x32_bf16 v[180:183], a[240:243], a[184:187], v[180:183]\n\t"
"ds_read_b64_tr_b16 v[232:233], v241 offset:1024\n\t"
"v_mfma_f32_16x16x32_bf16 v[184:187], a[240:243], a[188:191], v[184:187]\n\t"
"ds_read_b64_tr_b16 v[234:235], v241 offset:2560\n\t"
"v_mfma_f32_16x16x32_bf16 v[184:187], a[244:247], a[192:195], v[184:187]\n\t"
"ds_read_b128 a[180:183], v222 offset:20480\n\t"
"v_mfma_f32_16x16x32_bf16 v[184:187], a[248:251], a[196:199], v[184:187]\n\t"
"ds_read_b64_tr_b16 v[224:225], v240 offset:20992\n\t"
"v_mfma_f32_16x16x32_bf16 v[188:191], a[248:251], a[200:203], v[188:191]\n\t"
"ds_read_b64_tr_b16 v[226:227], v240 offset:22528\n\t"
"v_mfma_f32_16x16x32_bf16 v[188:191], a[244:247], a[196:199], v[188:191]\n\t"
"ds_read_b64_tr_b16 v[236:237], v241 offset:20992\n\t"
"v_mfma_f32_16x16x32_bf16 v[188:191], a[240:243], a[192:195], v[188:191]\n\t"
"ds_read_b64_tr_b16 v[238:239], v241 offset:22528\n\t"
"v_mfma_f32_16x16x32_bf16 v[192:195], a[240:243], a[196:199], v[192:195]\n\t"
"s_waitcnt lgkmcnt(7)\n\t"
"ds_read_b128 a[184:187], v223 offset:26624\n\t"
"v_mfma_f32_16x16x32_bf16 v[192:195], a[244:247], a[200:203], v[192:195]\n\t"
"s_waitcnt lgkmcnt(6)\n\t"
"ds_read_b128 a[188:191], v223 offset:32768\n\t"
"v_mfma_f32_16x16x32_bf16 v[192:195], a[248:251], a[204:207], v[192:195]\n\t"
"s_waitcnt lgkmcnt(4)\n\t"
"ds_read_b128 a[192:195], v222 offset:40448\n\t"
"v_mfma_f32_16x16x32_bf16 v[228:231], v[228:231], a[208:211], 0\n\t"
"ds_read_b128 a[196:199], v223 offset:46592\n\t"
"v_mfma_f32_16x16x32_bf16 v[232:235], v[232:235], a[208:211], 0\n\t"
"s_waitcnt lgkmcnt(4)\n\t"
"ds_read_b128 a[200:203], v223 offset:52736\n\t"
"v_mfma_f32_16x16x32_bf16 v[224:227], v[224:227], a[208:211], 0\n\t"
"ds_read_b128 a[204:207], v222 offset:60416\n\t"
"v_mfma_f32_16x16x32_bf16 v[236:239], v[236:239], a[208:211], 0\n\t"
"Lst27_%=:\n\t"
"v_mfma_f32_16x16x32_bf16 v[196:199], v[96:99], a[168:171], v[196:199]\n\t"
"s_mov_b64 vcc, -1\n\t"
"s_bitcmp1_b32 s73, 1\n\t"
"s_cbranch_scc1 Lvh4_%=\n\t"
"s_bitcmp1_b32 s73, 0\n\t"
"s_cbranch_scc1 Lvr4_%=\n\t"
"s_branch Lpg25_%=\n\t"
"Lvh4_%=:\n\t"
"s_bitcmp1_b32 s73, 0\n\t"
"s_cbranch_scc1 Lvr4_%=\n\t"
"s_branch Lvr4_%=\n\t"
"Lvr4_%=:\n\t"
"s_mov_b64 vcc, 0\n\t"
"v_mfma_f32_16x16x32_bf16 v[196:199], a[252:255], a[176:179], v[196:199]\n\t"
"v_cvt_pk_bf16_f32 v228, v228, v229\n\t"
"v_cvt_pk_bf16_f32 v229, v230, v231\n\t"
"v_mfma_f32_16x16x32_bf16 v[196:199], v[248:251], a[172:175], v[196:199]\n\t"
"v_cvt_pk_bf16_f32 v230, v232, v233\n\t"
"v_cvt_pk_bf16_f32 v231, v234, v235\n\t"
"v_mfma_f32_16x16x32_bf16 v[204:207], v[96:99], a[172:175], v[204:207]\n\t"
"s_waitcnt lgkmcnt(5)\n\t"
"v_cvt_pk_bf16_f32 v224, v224, v225\n\t"
"v_cvt_pk_bf16_f32 v225, v226, v227\n\t"
"v_mfma_f32_16x16x32_bf16 v[204:207], v[248:251], a[176:179], v[204:207]\n\t"
"v_cvt_pk_bf16_f32 v226, v236, v237\n\t"
"v_cvt_pk_bf16_f32 v227, v238, v239\n\t"
"v_mfma_f32_16x16x32_bf16 v[204:207], a[252:255], a[180:183], v[204:207]\n\t"
"ds_write_b128 v243, v[228:231] offset:2048\n\t"
"v_mfma_f32_16x16x32_bf16 v[212:215], a[252:255], a[184:187], v[212:215]\n\t"
"s_waitcnt lgkmcnt(5)\n\t"
"v_mfma_f32_16x16x32_bf16 v[212:215], v[248:251], a[180:183], v[212:215]\n\t"
"v_mfma_f32_16x16x32_bf16 v[212:215], v[96:99], a[176:179], v[212:215]\n\t"
"ds_write_b128 v243, v[224:227] offset:22016\n\t"
"v_mfma_f32_16x16x32_bf16 a[0:3], v[96:99], a[180:183], a[0:3]\n\t"
"s_waitcnt lgkmcnt(5)\n\t"
"v_mfma_f32_16x16x32_bf16 a[0:3], v[248:251], a[184:187], a[0:3]\n\t"
"v_mfma_f32_16x16x32_bf16 a[0:3], a[252:255], a[188:191], a[0:3]\n\t"
"s_barrier\n\t"
"v_mfma_f32_16x16x32_bf16 a[8:11], a[252:255], a[192:195], a[8:11]\n\t"
"s_waitcnt lgkmcnt(4)\n\t"
"ds_read_b64_tr_b16 v[232:233], v246 offset:1024\n\t"
"v_mfma_f32_16x16x32_bf16 a[8:11], v[248:251], a[188:191], a[8:11]\n\t"
"ds_read_b64_tr_b16 v[236:237], v255 offset:1024\n\t"
"v_mfma_f32_16x16x32_bf16 a[8:11], v[96:99], a[184:187], a[8:11]\n\t"
"ds_read_b64_tr_b16 v[228:229], v246 offset:7168\n\t"
"v_mfma_f32_16x16x32_bf16 a[16:19], v[96:99], a[188:191], a[16:19]\n\t"
"s_waitcnt lgkmcnt(6)\n\t"
"ds_read_b64_tr_b16 v[224:225], v255 offset:7168\n\t"
"v_mfma_f32_16x16x32_bf16 a[16:19], v[248:251], a[192:195], a[16:19]\n\t"
"buffer_load_dwordx4 a[228:231], off, s[80:83], s20 offset:1024 sc1\n\t"
"v_mfma_f32_16x16x32_bf16 a[16:19], a[252:255], a[196:199], a[16:19]\n\t"
"v_mfma_f32_16x16x32_bf16 a[24:27], a[252:255], a[200:203], a[24:27]\n\t"
"s_waitcnt lgkmcnt(6)\n\t"
"v_mfma_f32_16x16x32_bf16 a[24:27], v[248:251], a[196:199], a[24:27]\n\t"
"buffer_load_dwordx4 a[232:235], off, s[80:83], s20 offset:2048 sc1\n\t"
"v_mfma_f32_16x16x32_bf16 a[24:27], v[96:99], a[192:195], a[24:27]\n\t"
"s_mov_b32 s20, 1032192\n\t"
"v_mfma_f32_16x16x32_bf16 a[32:35], v[96:99], a[196:199], a[32:35]\n\t"
"s_waitcnt lgkmcnt(3)\n\t"
"v_mfma_f32_16x16x32_bf16 a[32:35], v[248:251], a[200:203], a[32:35]\n\t"
"s_waitcnt lgkmcnt(2)\n\t"
"v_mfma_f32_16x16x32_bf16 a[32:35], a[252:255], a[204:207], a[32:35]\n\t"
"buffer_load_dwordx4 v[96:99], off, s[80:83], s20 offset:0 sc1\n\t"
"v_mfma_f32_4x4x4_16b_bf16 v[232:235], v[232:233], v[220:221], 0\n\t"
"s_waitcnt lgkmcnt(0)\n\t"
"v_mfma_f32_4x4x4_16b_bf16 v[236:239], v[236:237], v[220:221], 0\n\t"
"s_waitcnt vmcnt(10)\n\t"
"v_mfma_f32_4x4x4_16b_bf16 v[228:231], v[228:229], v[220:221], 0\n\t"
"v_mfma_f32_4x4x4_16b_bf16 v[224:227], v[224:225], v[220:221], 0\n\t"
"v_mfma_f32_16x16x32_bf16 a[40:43], a[212:215], a[168:171], a[40:43]\n\t"
"buffer_load_dwordx4 v[248:251], off, s[80:83], s20 offset:1024 sc1\n\t"
"v_mfma_f32_16x16x32_bf16 a[40:43], a[220:223], a[176:179], a[40:43]\n\t"
"v_cvt_pk_bf16_f32 v232, v232, v233\n\t"
"v_cvt_pk_bf16_f32 v233, v234, v235\n\t"
"v_mfma_f32_16x16x32_bf16 a[40:43], a[216:219], a[172:175], a[40:43]\n\t"
"v_cvt_pk_bf16_f32 v234, v236, v237\n\t"
"v_cvt_pk_bf16_f32 v235, v238, v239\n\t"
"v_mfma_f32_16x16x32_bf16 a[48:51], a[212:215], a[172:175], a[48:51]\n\t"
"ds_read_b64_tr_b16 v[236:237], v246 offset:13312\n\t"
"v_cvt_pk_bf16_f32 v228, v228, v229\n\t"
"v_mfma_f32_16x16x32_bf16 a[48:51], a[216:219], a[176:179], a[48:51]\n\t"
"v_cvt_pk_bf16_f32 v229, v230, v231\n\t"
"v_cvt_pk_bf16_f32 v230, v224, v225\n\t"
"v_mfma_f32_16x16x32_bf16 a[48:51], a[220:223], a[180:183], a[48:51]\n\t"
"ds_write_b128 v245, v[232:235] offset:1024\n\t"
"v_cvt_pk_bf16_f32 v231, v226, v227\n\t"
"v_mfma_f32_16x16x32_bf16 a[56:59], a[220:223], a[184:187], a[56:59]\n\t"
"v_mfma_f32_16x16x32_bf16 a[56:59], a[216:219], a[180:183], a[56:59]\n\t"
"buffer_load_dwordx4 a[236:239], off, s[80:83], s20 offset:2048 sc1\n\t"
"v_mfma_f32_16x16x32_bf16 a[56:59], a[212:215], a[176:179], a[56:59]\n\t"
"s_mov_b32 s20, 1044480\n\t"
"ds_read_b64_tr_b16 v[224:225], v255 offset:13312\n\t"
"v_mfma_f32_16x16x32_bf16 a[64:67], a[212:215], a[180:183], a[64:67]\n\t"
"ds_read_b64_tr_b16 v[232:233], v246 offset:20992\n\t"
"v_mfma_f32_16x16x32_bf16 a[64:67], a[216:219], a[184:187], a[64:67]\n\t"
"buffer_load_dwordx4 a[240:243], off, s[80:83], s20 offset:0 sc1\n\t"
"v_mfma_f32_16x16x32_bf16 a[64:67], a[220:223], a[188:191], a[64:67]\n\t"
"ds_write_b128 v245, v[228:231] offset:7168\n\t"
"v_mfma_f32_16x16x32_bf16 a[72:75], a[212:215], a[184:187], a[72:75]\n\t"
"buffer_load_dwordx4 a[244:247], off, s[80:83], s20 offset:1024 sc1\n\t"
"v_mfma_f32_16x16x32_bf16 a[72:75], a[220:223], a[192:195], a[72:75]\n\t"
"s_waitcnt lgkmcnt(2)\n\t"
"ds_read_b64_tr_b16 v[228:229], v255 offset:20992\n\t"
"v_mfma_f32_16x16x32_bf16 a[72:75], a[216:219], a[188:191], a[72:75]\n\t"
"s_waitcnt lgkmcnt(2)\n\t"
"v_mfma_f32_4x4x4_16b_bf16 v[236:239], v[236:237], v[220:221], 0\n\t"
"v_mfma_f32_4x4x4_16b_bf16 v[224:227], v[224:225], v[220:221], 0\n\t"
"v_mfma_f32_4x4x4_16b_bf16 v[232:235], v[232:233], v[220:221], 0\n\t"
"s_waitcnt lgkmcnt(0)\n\t"
"v_mfma_f32_4x4x4_16b_bf16 v[228:231], v[228:229], v[220:221], 0\n\t"
"buffer_load_dwordx4 a[248:251], off, s[80:83], s20 offset:2048 sc1\n\t"
"s_mov_b32 s20, 1056768\n\t"
"v_mfma_f32_16x16x32_bf16 a[80:83], a[212:215], a[188:191], a[80:83]\n\t"
"buffer_load_dwordx4 a[252:255], off, s[80:83], s20 offset:0 sc1\n\t"
"v_mfma_f32_16x16x32_bf16 a[80:83], a[220:223], a[196:199], a[80:83]\n\t"
"v_cvt_pk_bf16_f32 v236, v236, v237\n\t"
"v_cvt_pk_bf16_f32 v237, v238, v239\n\t"
"v_mfma_f32_16x16x32_bf16 a[80:83], a[216:219], a[192:195], a[80:83]\n\t"
"v_cvt_pk_bf16_f32 v238, v224, v225\n\t"
"v_cvt_pk_bf16_f32 v239, v226, v227\n\t"
"v_mfma_f32_16x16x32_bf16 a[88:91], a[212:215], a[192:195], a[88:91]\n\t"
"ds_read_b64_tr_b16 v[224:225], v246 offset:27136\n\t"
"v_cvt_pk_bf16_f32 v232, v232, v233\n\t"
"v_mfma_f32_16x16x32_bf16 a[88:91], a[216:219], a[196:199], a[88:91]\n\t"
"v_cvt_pk_bf16_f32 v233, v234, v235\n\t"
"v_cvt_pk_bf16_f32 v234, v228, v229\n\t"
"v_mfma_f32_16x16x32_bf16 a[88:91], a[220:223], a[200:203], a[88:91]\n\t"
"ds_write_b128 v245, v[236:239] offset:13312\n\t"
"v_cvt_pk_bf16_f32 v235, v230, v231\n\t"
"v_mfma_f32_16x16x32_bf16 a[96:99], a[216:219], a[200:203], a[96:99]\n\t"
"s_waitcnt vmcnt(7)\n\t"
"v_mfma_f32_16x16x32_bf16 a[96:99], a[212:215], a[196:199], a[96:99]\n\t"
"ds_read_b64_tr_b16 v[228:229], v255 offset:27136\n\t"
"v_mfma_f32_16x16x32_bf16 a[96:99], a[220:223], a[204:207], a[96:99]\n\t"
"ds_write_b128 v245, v[232:235] offset:20992\n\t"
"v_mfma_f32_16x16x32_bf16 a[104:107], a[224:227], a[168:171], a[104:107]\n\t"
"buffer_load_dwordx4 a[216:219], off, s[80:83], s20 offset:1024 sc1\n\t"
"v_mfma_f32_16x16x32_bf16 a[104:107], a[232:235], a[176:179], a[104:107]\n\t"
"ds_read_b64_tr_b16 v[236:237], v246 offset:33280\n\t"
"v_mfma_f32_16x16x32_bf16 a[104:107], a[228:231], a[172:175], a[104:107]\n\t"
"ds_read_b64_tr_b16 v[232:233], v255 offset:33280\n\t"
"v_mfma_f32_16x16x32_bf16 a[112:115], a[224:227], a[172:175], a[112:115]\n\t"
"buffer_load_dwordx4 a[212:215], off, s[80:83], s20 offset:2048 sc1\n\t"
"v_mfma_f32_16x16x32_bf16 a[112:115], a[228:231], a[176:179], a[112:115]\n\t"
"s_waitcnt lgkmcnt(3)\n\t"
"ds_read_b128 a[168:171], v222 offset:768\n\t"
"s_mov_b32 s20, 1216512\n\t"
"v_mfma_f32_16x16x32_bf16 a[112:115], a[232:235], a[180:183], a[112:115]\n\t"
"buffer_load_dwordx4 a[220:223], off, s[80:83], s20 offset:0 sc1\n\t"
"v_mfma_f32_4x4x4_16b_bf16 v[224:227], v[224:225], v[220:221], 0\n\t"
"s_waitcnt lgkmcnt(2)\n\t"
"v_mfma_f32_4x4x4_16b_bf16 v[228:231], v[228:229], v[220:221], 0\n\t"
"s_waitcnt lgkmcnt(1)\n\t"
"v_mfma_f32_4x4x4_16b_bf16 v[236:239], v[236:237], v[220:221], 0\n\t"
"v_mfma_f32_4x4x4_16b_bf16 v[232:235], v[232:233], v[220:221], 0\n\t"
"v_mfma_f32_16x16x32_bf16 a[120:123], a[228:231], a[180:183], a[120:123]\n\t"
"ds_read_b128 a[172:175], v223 offset:6912\n\t"
"v_cvt_pk_bf16_f32 v224, v224, v225\n\t"
"v_mfma_f32_16x16x32_bf16 a[120:123], a[224:227], a[176:179], a[120:123]\n\t"
"ds_read_b128 a[176:179], v223 offset:13056\n\t"
"v_cvt_pk_bf16_f32 v225, v226, v227\n\t"
"v_mfma_f32_16x16x32_bf16 a[120:123], a[232:235], a[184:187], a[120:123]\n\t"
"v_cvt_pk_bf16_f32 v226, v228, v229\n\t"
"v_cvt_pk_bf16_f32 v227, v230, v231\n\t"
"v_mfma_f32_16x16x32_bf16 a[128:131], a[224:227], a[180:183], a[128:131]\n\t"
"ds_read_b128 a[180:183], v222 offset:20736\n\t"
"v_cvt_pk_bf16_f32 v236, v236, v237\n\t"
"v_mfma_f32_16x16x32_bf16 a[128:131], a[232:235], a[188:191], a[128:131]\n\t"
"v_cvt_pk_bf16_f32 v237, v238, v239\n\t"
"v_cvt_pk_bf16_f32 v238, v232, v233\n\t"
"v_mfma_f32_16x16x32_bf16 a[128:131], a[228:231], a[184:187], a[128:131]\n\t"
"ds_write_b128 v245, v[224:227] offset:27136\n\t"
"v_cvt_pk_bf16_f32 v239, v234, v235\n\t"
"v_mfma_f32_16x16x32_bf16 a[136:139], a[224:227], a[184:187], a[136:139]\n\t"
"v_mfma_f32_16x16x32_bf16 a[136:139], a[228:231], a[188:191], a[136:139]\n\t"
"ds_read_b128 a[184:187], v223 offset:26880\n\t"
"v_mfma_f32_16x16x32_bf16 a[136:139], a[232:235], a[192:195], a[136:139]\n\t"
"ds_write_b128 v245, v[236:239] offset:33280\n\t"
"v_mfma_f32_16x16x32_bf16 a[144:147], a[232:235], a[196:199], a[144:147]\n\t"
"v_mfma_f32_16x16x32_bf16 a[144:147], a[228:231], a[192:195], a[144:147]\n\t"
"v_mfma_f32_16x16x32_bf16 a[144:147], a[224:227], a[188:191], a[144:147]\n\t"
"ds_read_b128 a[188:191], v223 offset:33024\n\t"
"v_mfma_f32_16x16x32_bf16 a[152:155], a[224:227], a[192:195], a[152:155]\n\t"
"ds_read_b128 a[192:195], v222 offset:40704\n\t"
"v_mfma_f32_16x16x32_bf16 a[152:155], a[228:231], a[196:199], a[152:155]\n\t"
"v_mfma_f32_16x16x32_bf16 a[152:155], a[232:235], a[200:203], a[152:155]\n\t"
"s_barrier\n\t"
"v_mfma_f32_16x16x32_bf16 a[160:163], a[228:231], a[200:203], a[160:163]\n\t"
"s_waitcnt vmcnt(7) lgkmcnt(6)\n\t"
"ds_read_b64_tr_b16 v[228:229], v240 offset:40960\n\t"
"v_mfma_f32_16x16x32_bf16 a[160:163], a[224:227], a[196:199], a[160:163]\n\t"
"ds_read_b64_tr_b16 v[230:231], v240 offset:42496\n\t"
"v_mfma_f32_16x16x32_bf16 a[160:163], a[232:235], a[204:207], a[160:163]\n\t"
"ds_read_b64_tr_b16 v[232:233], v241 offset:40960\n\t"
"v_mfma_f32_16x16x32_bf16 v[200:203], v[96:99], a[168:171], v[200:203]\n\t"
"s_waitcnt lgkmcnt(8)\n\t"
"ds_read_b64_tr_b16 v[234:235], v241 offset:42496\n\t"
"v_subrev_u32 v240, 59904, v240\n\t"
"v_mfma_f32_16x16x32_bf16 v[200:203], a[236:239], a[176:179], v[200:203]\n\t"
"ds_read_b128 a[196:199], v223 offset:46848\n\t"
"v_subrev_u32 v241, 59904, v241\n\t"
"v_mfma_f32_16x16x32_bf16 v[200:203], v[248:251], a[172:175], v[200:203]\n\t"
"ds_read_b64_tr_b16 v[224:225], v240 offset:40960\n\t"
"v_mfma_f32_16x16x32_bf16 v[208:211], v[96:99], a[172:175], v[208:211]\n\t"
"s_waitcnt lgkmcnt(4)\n\t"
"ds_read_b64_tr_b16 v[226:227], v240 offset:42496\n\t"
"v_mfma_f32_16x16x32_bf16 v[208:211], v[248:251], a[176:179], v[208:211]\n\t"
"buffer_load_dwordx4 a[228:231], off, s[80:83], s20 offset:1024 sc1\n\t"
"v_mfma_f32_16x16x32_bf16 v[208:211], a[236:239], a[180:183], v[208:211]\n\t"
"s_waitcnt lgkmcnt(3)\n\t"
"ds_read_b64_tr_b16 v[236:237], v241 offset:40960\n\t"
"ds_read_b64_tr_b16 v[238:239], v241 offset:42496\n\t"
"v_mfma_f32_16x16x32_bf16 v[228:231], v[228:231], a[208:211], 0\n\t"
"buffer_load_dwordx4 a[224:227], off, s[80:83], s20 offset:2048 sc1\n\t"
"s_mov_b32 s20, 1228800\n\t"
"v_mfma_f32_16x16x32_bf16 v[232:235], v[232:235], a[208:211], 0\n\t"
"s_waitcnt lgkmcnt(2)\n\t"
"buffer_load_dwordx4 a[232:235], off, s[80:83], s20 offset:0 sc1\n\t"
"v_mfma_f32_16x16x32_bf16 v[224:227], v[224:227], a[208:211], 0\n\t"
"s_waitcnt lgkmcnt(0)\n\t"
"ds_read_b128 a[200:203], v223 offset:52992\n\t"
"v_mfma_f32_16x16x32_bf16 v[236:239], v[236:239], a[208:211], 0\n\t"
"ds_read_b128 a[204:207], v222 offset:60672\n\t"
"v_mfma_f32_16x16x32_bf16 v[216:219], v[248:251], a[180:183], v[216:219]\n\t"
"v_mfma_f32_16x16x32_bf16 v[216:219], v[96:99], a[176:179], v[216:219]\n\t"
"v_cvt_pk_bf16_f32 v228, v228, v229\n\t"
"v_cvt_pk_bf16_f32 v229, v230, v231\n\t"
"v_mfma_f32_16x16x32_bf16 v[216:219], a[236:239], a[184:187], v[216:219]\n\t"
"v_cvt_pk_bf16_f32 v230, v232, v233\n\t"
"v_cvt_pk_bf16_f32 v231, v234, v235\n\t"
"v_mfma_f32_16x16x32_bf16 a[4:7], v[96:99], a[180:183], a[4:7]\n\t"
"ds_read_b64_tr_b16 v[232:233], v246 offset:40960\n\t"
"v_cvt_pk_bf16_f32 v224, v224, v225\n\t"
"v_mfma_f32_16x16x32_bf16 a[4:7], a[236:239], a[188:191], a[4:7]\n\t"
"v_cvt_pk_bf16_f32 v225, v226, v227\n\t"
"v_cvt_pk_bf16_f32 v226, v236, v237\n\t"
"v_mfma_f32_16x16x32_bf16 a[4:7], v[248:251], a[184:187], a[4:7]\n\t"
"ds_write_b128 v243, v[228:231] offset:41984\n\t"
"v_mfma_f32_16x16x32_bf16 a[12:15], v[96:99], a[184:187], a[12:15]\n\t"
"v_cvt_pk_bf16_f32 v227, v238, v239\n\t"
"v_subrev_u32 v243, 59904, v243\n\t"
"v_mfma_f32_16x16x32_bf16 a[12:15], v[248:251], a[188:191], a[12:15]\n\t"
"ds_read_b64_tr_b16 v[236:237], v255 offset:40960\n\t"
"v_mfma_f32_16x16x32_bf16 a[12:15], a[236:239], a[192:195], a[12:15]\n\t"
"ds_write_b128 v243, v[224:227] offset:41984\n\t"
"v_mfma_f32_16x16x32_bf16 a[20:23], a[236:239], a[196:199], a[20:23]\n\t"
"s_waitcnt lgkmcnt(5)\n\t"
"v_mfma_f32_16x16x32_bf16 a[20:23], v[248:251], a[192:195], a[20:23]\n\t"
"ds_read_b64_tr_b16 v[228:229], v246 offset:47104\n\t"
"v_mfma_f32_16x16x32_bf16 a[20:23], v[96:99], a[188:191], a[20:23]\n\t"
"ds_read_b64_tr_b16 v[224:225], v255 offset:47104\n\t"
"v_mfma_f32_16x16x32_bf16 a[28:31], v[96:99], a[192:195], a[28:31]\n\t"
"s_waitcnt lgkmcnt(6)\n\t"
"v_mfma_f32_16x16x32_bf16 a[28:31], v[248:251], a[196:199], a[28:31]\n\t"
"v_mfma_f32_16x16x32_bf16 a[28:31], a[236:239], a[200:203], a[28:31]\n\t"
"s_barrier\n\t"
"v_mfma_f32_16x16x32_bf16 a[36:39], v[248:251], a[200:203], a[36:39]\n\t"
"s_waitcnt lgkmcnt(5)\n\t"
"v_mfma_f32_16x16x32_bf16 a[36:39], v[96:99], a[196:199], a[36:39]\n\t"
"buffer_load_dwordx4 v[248:251], off, s[80:83], s20 offset:1024 sc1\n\t"
"v_mfma_f32_16x16x32_bf16 a[36:39], a[236:239], a[204:207], a[36:39]\n\t"
"s_waitcnt lgkmcnt(1)\n\t"
"v_mfma_f32_4x4x4_16b_bf16 v[232:235], v[232:233], v[220:221], 0\n\t"
"s_waitcnt lgkmcnt(0)\n\t"
"v_mfma_f32_4x4x4_16b_bf16 v[236:239], v[236:237], v[220:221], 0\n\t"
"s_waitcnt vmcnt(8)\n\t"
"v_mfma_f32_4x4x4_16b_bf16 v[228:231], v[228:229], v[220:221], 0\n\t"
"v_mfma_f32_4x4x4_16b_bf16 v[224:227], v[224:225], v[220:221], 0\n\t"
"buffer_load_dwordx4 v[96:99], off, s[80:83], s20 offset:2048 sc1\n\t"
"s_mov_b32 s20, 1241088\n\t"
"v_mfma_f32_16x16x32_bf16 a[44:47], a[240:243], a[168:171], a[44:47]\n\t"
"buffer_load_dwordx4 a[236:239], off, s[80:83], s20 offset:0 sc1\n\t"
"v_mfma_f32_16x16x32_bf16 a[44:47], a[248:251], a[176:179], a[44:47]\n\t"
"v_cvt_pk_bf16_f32 v232, v232, v233\n\t"
"v_cvt_pk_bf16_f32 v233, v234, v235\n\t"
"v_mfma_f32_16x16x32_bf16 a[44:47], a[244:247], a[172:175], a[44:47]\n\t"
"v_cvt_pk_bf16_f32 v234, v236, v237\n\t"
"v_cvt_pk_bf16_f32 v235, v238, v239\n\t"
"v_mfma_f32_16x16x32_bf16 a[52:55], a[240:243], a[172:175], a[52:55]\n\t"
"ds_read_b64_tr_b16 v[236:237], v246 offset:53248\n\t"
"v_cvt_pk_bf16_f32 v228, v228, v229\n\t"
"v_mfma_f32_16x16x32_bf16 a[52:55], a[244:247], a[176:179], a[52:55]\n\t"
"v_cvt_pk_bf16_f32 v229, v230, v231\n\t"
"v_cvt_pk_bf16_f32 v230, v224, v225\n\t"
"v_mfma_f32_16x16x32_bf16 a[52:55], a[248:251], a[180:183], a[52:55]\n\t"
"ds_write_b128 v245, v[232:235] offset:40960\n\t"
"v_cvt_pk_bf16_f32 v231, v226, v227\n\t"
"v_mfma_f32_16x16x32_bf16 a[60:63], a[248:251], a[184:187], a[60:63]\n\t"
"v_mfma_f32_16x16x32_bf16 a[60:63], a[244:247], a[180:183], a[60:63]\n\t"
"ds_read_b64_tr_b16 v[224:225], v255 offset:53248\n\t"
"v_mfma_f32_16x16x32_bf16 a[60:63], a[240:243], a[176:179], a[60:63]\n\t"
"ds_write_b128 v245, v[228:231] offset:47104\n\t"
"v_mfma_f32_16x16x32_bf16 a[68:71], a[240:243], a[180:183], a[68:71]\n\t"
"v_mfma_f32_16x16x32_bf16 a[68:71], a[244:247], a[184:187], a[68:71]\n\t"
"v_mfma_f32_16x16x32_bf16 a[68:71], a[248:251], a[188:191], a[68:71]\n\t"
"v_mfma_f32_16x16x32_bf16 a[76:79], a[240:243], a[184:187], a[76:79]\n\t"
"s_waitcnt lgkmcnt(3)\n\t"
"v_mfma_f32_16x16x32_bf16 a[76:79], a[248:251], a[192:195], a[76:79]\n\t"
"s_waitcnt lgkmcnt(1)\n\t"
"ds_read_b64_tr_b16 v[232:233], v240 offset:1536\n\t"
"v_mfma_f32_16x16x32_bf16 a[76:79], a[244:247], a[188:191], a[76:79]\n\t"
"ds_read_b64_tr_b16 v[234:235], v240 offset:3072\n\t"
"v_mfma_f32_4x4x4_16b_bf16 v[236:239], v[236:237], v[220:221], 0\n\t"
"v_mfma_f32_4x4x4_16b_bf16 v[224:227], v[224:225], v[220:221], 0\n\t"
"v_mfma_f32_16x16x32_bf16 a[84:87], a[240:243], a[188:191], a[84:87]\n\t"
"ds_read_b64_tr_b16 v[228:229], v241 offset:1536\n\t"
"v_mfma_f32_16x16x32_bf16 a[84:87], a[248:251], a[196:199], a[84:87]\n\t"
"ds_read_b64_tr_b16 v[230:231], v241 offset:3072\n\t"
"v_cvt_pk_bf16_f32 v236, v236, v237\n\t"
"v_mfma_f32_16x16x32_bf16 a[84:87], a[244:247], a[192:195], a[84:87]\n\t"
"v_cvt_pk_bf16_f32 v237, v238, v239\n\t"
"v_cvt_pk_bf16_f32 v238, v224, v225\n\t"
"v_mfma_f32_16x16x32_bf16 a[92:95], a[240:243], a[192:195], a[92:95]\n\t"
"v_cvt_pk_bf16_f32 v239, v226, v227\n\t"
"v_mfma_f32_16x16x32_bf16 a[92:95], a[244:247], a[196:199], a[92:95]\n\t"
"ds_read_b64_tr_b16 v[224:225], v240 offset:21504\n\t"
"v_mfma_f32_16x16x32_bf16 a[92:95], a[248:251], a[200:203], a[92:95]\n\t"
"ds_read_b64_tr_b16 v[226:227], v240 offset:23040\n\t"
"v_mfma_f32_16x16x32_bf16 a[100:103], a[244:247], a[200:203], a[100:103]\n\t"
"buffer_load_dwordx4 a[244:247], off, s[80:83], s20 offset:1024 sc1\n\t"
"v_mfma_f32_16x16x32_bf16 a[100:103], a[240:243], a[196:199], a[100:103]\n\t"
"s_waitcnt vmcnt(8)\n\t"
"v_mfma_f32_16x16x32_bf16 a[100:103], a[248:251], a[204:207], a[100:103]\n\t"
"ds_write_b128 v245, v[236:239] offset:53248\n\t"
"v_mfma_f32_16x16x32_bf16 a[108:111], a[252:255], a[168:171], a[108:111]\n\t"
"buffer_load_dwordx4 a[240:243], off, s[80:83], s20 offset:2048 sc1\n\t"
"v_mfma_f32_16x16x32_bf16 a[108:111], a[212:215], a[176:179], a[108:111]\n\t"
"s_waitcnt lgkmcnt(3)\n\t"
"ds_read_b64_tr_b16 v[236:237], v241 offset:21504\n\t"
"v_mfma_f32_16x16x32_bf16 a[108:111], a[216:219], a[172:175], a[108:111]\n\t"
"s_waitcnt lgkmcnt(2)\n\t"
"ds_read_b64_tr_b16 v[238:239], v241 offset:23040\n\t"
"s_mov_b32 s20, 1253376\n\t"
"v_mfma_f32_16x16x32_bf16 v[232:235], v[232:235], a[208:211], 0\n\t"
"buffer_load_dwordx4 a[248:251], off, s[80:83], s20 offset:0 sc1\n\t"
"v_mfma_f32_16x16x32_bf16 v[228:231], v[228:231], a[208:211], 0\n\t"
"v_mfma_f32_16x16x32_bf16 v[224:227], v[224:227], a[208:211], 0\n\t"
"Lpg20_%=:\n\t"
"s_cbranch_vccnz Lvr2_%=\n\t"
"s_waitcnt lgkmcnt(0)\n\t"
"v_mfma_f32_16x16x32_bf16 v[236:239], v[236:239], a[208:211], 0\n\t"
"s_barrier\n\t"
"v_mfma_f32_16x16x32_bf16 a[116:119], a[252:255], a[172:175], a[116:119]\n\t"
"ds_read_b128 a[168:171], v222 offset:2560\n\t"
"v_mfma_f32_16x16x32_bf16 a[116:119], a[212:215], a[180:183], a[116:119]\n\t"
"ds_read_b128 a[172:175], v223 offset:8704\n\t"
"v_cvt_pk_bf16_f32 v232, v232, v233\n\t"
"v_mfma_f32_16x16x32_bf16 a[116:119], a[216:219], a[176:179], a[116:119]\n\t"
"v_cvt_pk_bf16_f32 v233, v234, v235\n\t"
"v_cvt_pk_bf16_f32 v234, v228, v229\n\t"
"v_mfma_f32_16x16x32_bf16 a[124:127], a[252:255], a[176:179], a[124:127]\n\t"
"ds_read_b128 a[176:179], v223 offset:14848\n\t"
"v_cvt_pk_bf16_f32 v235, v230, v231\n\t"
"v_mfma_f32_16x16x32_bf16 a[124:127], a[216:219], a[180:183], a[124:127]\n\t"
"v_cvt_pk_bf16_f32 v224, v224, v225\n\t"
"v_cvt_pk_bf16_f32 v225, v226, v227\n\t"
"v_mfma_f32_16x16x32_bf16 a[124:127], a[212:215], a[184:187], a[124:127]\n\t"
"ds_write_b128 v243, v[232:235] offset:512\n\t"
"v_mfma_f32_16x16x32_bf16 a[132:135], a[212:215], a[188:191], a[132:135]\n\t"
"v_cvt_pk_bf16_f32 v226, v236, v237\n\t"
"v_cvt_pk_bf16_f32 v227, v238, v239\n\t"
"v_mfma_f32_16x16x32_bf16 a[132:135], a[216:219], a[184:187], a[132:135]\n\t"
"ds_read_b64_tr_b16 v[228:229], v240 offset:41472\n\t"
"v_mfma_f32_16x16x32_bf16 a[132:135], a[252:255], a[180:183], a[132:135]\n\t"
"ds_write_b128 v243, v[224:227] offset:20480\n\t"
"v_mfma_f32_16x16x32_bf16 a[140:143], a[252:255], a[184:187], a[140:143]\n\t"
"v_mfma_f32_16x16x32_bf16 a[140:143], a[216:219], a[188:191], a[140:143]\n\t"
"ds_read_b64_tr_b16 v[230:231], v240 offset:43008\n\t"
"v_mfma_f32_16x16x32_bf16 a[140:143], a[212:215], a[192:195], a[140:143]\n\t"
"ds_read_b64_tr_b16 v[236:237], v241 offset:41472\n\t"
"v_mfma_f32_16x16x32_bf16 a[148:151], a[212:215], a[196:199], a[148:151]\n\t"
"ds_read_b64_tr_b16 v[238:239], v241 offset:43008\n\t"
"v_add_u32 v240, 59904, v240\n\t"
"v_mfma_f32_16x16x32_bf16 a[148:151], a[216:219], a[192:195], a[148:151]\n\t"
"ds_read_b128 a[180:183], v222 offset:22528\n\t"
"v_add_u32 v241, 59904, v241\n\t"
"v_mfma_f32_16x16x32_bf16 a[148:151], a[252:255], a[188:191], a[148:151]\n\t"
"ds_read_b64_tr_b16 v[232:233], v240 offset:1536\n\t"
"v_mfma_f32_16x16x32_bf16 a[156:159], a[252:255], a[192:195], a[156:159]\n\t"
"ds_read_b64_tr_b16 v[234:235], v240 offset:3072\n\t"
"v_mfma_f32_16x16x32_bf16 a[156:159], a[216:219], a[196:199], a[156:159]\n\t"
"ds_read_b64_tr_b16 v[224:225], v241 offset:1536\n\t"
"v_mfma_f32_16x16x32_bf16 a[156:159], a[212:215], a[200:203], a[156:159]\n\t"
"ds_read_b64_tr_b16 v[226:227], v241 offset:3072\n\t"
"v_mfma_f32_16x16x32_bf16 a[164:167], a[216:219], a[200:203], a[164:167]\n\t"
"s_waitcnt lgkmcnt(7)\n\t"
"ds_read_b128 a[184:187], v223 offset:28672\n\t"
"v_mfma_f32_16x16x32_bf16 a[164:167], a[252:255], a[196:199], a[164:167]\n\t"
"s_waitcnt lgkmcnt(6)\n\t"
"ds_read_b128 a[188:191], v223 offset:34816\n\t"
"v_mfma_f32_16x16x32_bf16 a[164:167], a[212:215], a[204:207], a[164:167]\n\t"
"buffer_load_dwordx4 a[216:219], off, s[80:83], s20 offset:1024 sc1\n\t"
"v_mfma_f32_16x16x32_bf16 v[228:231], v[228:231], a[208:211], 0\n\t"
"s_waitcnt lgkmcnt(4)\n\t"
"ds_read_b128 a[192:195], v222 offset:42496\n\t"
"v_mfma_f32_16x16x32_bf16 v[236:239], v[236:239], a[208:211], 0\n\t"
"buffer_load_dwordx4 a[212:215], off, s[80:83], s20 offset:2048 sc1\n\t"
"v_mfma_f32_16x16x32_bf16 v[232:235], v[232:235], a[208:211], 0\n\t"
"s_waitcnt vmcnt(9) lgkmcnt(3)\n\t"
"ds_read_b128 a[196:199], v223 offset:48640\n\t"
"s_mov_b32 s20, 1265664\n\t"
"v_mfma_f32_16x16x32_bf16 v[224:227], v[224:227], a[208:211], 0\n\t"
"buffer_load_dwordx4 a[252:255], off, s[80:83], s20 offset:0 sc1\n\t"
"Lst28_%=:\n\t"
"v_mfma_f32_16x16x32_bf16 v[0:3], a[220:223], a[168:171], v[0:3]\n\t"
"ds_read_b128 a[200:203], v223 offset:54784\n\t"
"v_mfma_f32_16x16x32_bf16 v[0:3], a[224:227], a[176:179], v[0:3]\n\t"
"ds_read_b128 a[204:207], v222 offset:62464\n\t"
"v_cvt_pk_bf16_f32 v228, v228, v229\n\t"
"v_mfma_f32_16x16x32_bf16 v[0:3], a[228:231], a[172:175], v[0:3]\n\t"
"v_cvt_pk_bf16_f32 v229, v230, v231\n\t"
"v_cvt_pk_bf16_f32 v230, v236, v237\n\t"
"v_mfma_f32_16x16x32_bf16 v[4:7], a[220:223], a[172:175], v[4:7]\n\t"
"s_waitcnt lgkmcnt(5)\n\t"
"v_cvt_pk_bf16_f32 v231, v238, v239\n\t"
"v_cvt_pk_bf16_f32 v232, v232, v233\n\t"
"v_mfma_f32_16x16x32_bf16 v[4:7], a[228:231], a[176:179], v[4:7]\n\t"
"v_cvt_pk_bf16_f32 v233, v234, v235\n\t"
"v_cvt_pk_bf16_f32 v234, v224, v225\n\t"
"v_mfma_f32_16x16x32_bf16 v[4:7], a[224:227], a[180:183], v[4:7]\n\t"
"ds_write_b128 v243, v[228:231] offset:40448\n\t"
"v_mfma_f32_16x16x32_bf16 v[8:11], a[224:227], a[184:187], v[8:11]\n\t"
"s_waitcnt lgkmcnt(5)\n\t"
"v_cvt_pk_bf16_f32 v235, v226, v227\n\t"
"v_add_u32 v243, 59904, v243\n\t"
"v_mfma_f32_16x16x32_bf16 v[8:11], a[228:231], a[180:183], v[8:11]\n\t"
"v_mfma_f32_16x16x32_bf16 v[8:11], a[220:223], a[176:179], v[8:11]\n\t"
"ds_write_b128 v243, v[232:235] offset:512\n\t"
"v_mfma_f32_16x16x32_bf16 v[12:15], a[220:223], a[180:183], v[12:15]\n\t"
"s_waitcnt lgkmcnt(5)\n\t"
"v_mfma_f32_16x16x32_bf16 v[12:15], a[228:231], a[184:187], v[12:15]\n\t"
"ds_read_b64_tr_b16 v[236:237], v240 offset:21504\n\t"
"v_mfma_f32_16x16x32_bf16 v[12:15], a[224:227], a[188:191], v[12:15]\n\t"
"ds_read_b64_tr_b16 v[238:239], v240 offset:23040\n\t"
"v_mfma_f32_16x16x32_bf16 v[16:19], a[224:227], a[192:195], v[16:19]\n\t"
"s_waitcnt lgkmcnt(6)\n\t"
"ds_read_b64_tr_b16 v[224:225], v241 offset:21504\n\t"
"v_mfma_f32_16x16x32_bf16 v[16:19], a[228:231], a[188:191], v[16:19]\n\t"
"ds_read_b64_tr_b16 v[226:227], v241 offset:23040\n\t"
"v_mfma_f32_16x16x32_bf16 v[16:19], a[220:223], a[184:187], v[16:19]\n\t"
"ds_read_b64_tr_b16 v[228:229], v240 offset:41472\n\t"
"v_mfma_f32_16x16x32_bf16 v[20:23], a[220:223], a[188:191], v[20:23]\n\t"
"s_waitcnt lgkmcnt(8)\n\t"
"ds_read_b64_tr_b16 v[230:231], v240 offset:43008\n\t"
"v_mfma_f32_16x16x32_bf16 v[20:23], a[228:231], a[192:195], v[20:23]\n\t"
"ds_read_b64_tr_b16 v[232:233], v241 offset:41472\n\t"
"v_mfma_f32_16x16x32_bf16 v[20:23], a[224:227], a[196:199], v[20:23]\n\t"
"ds_read_b64_tr_b16 v[234:235], v241 offset:43008\n\t"
"v_mfma_f32_16x16x32_bf16 v[24:27], a[224:227], a[200:203], v[24:27]\n\t"
"s_waitcnt lgkmcnt(10)\n\t"
"v_mfma_f32_16x16x32_bf16 v[24:27], a[228:231], a[196:199], v[24:27]\n\t"
"v_mfma_f32_16x16x32_bf16 v[24:27], a[220:223], a[192:195], v[24:27]\n\t"
"v_mfma_f32_16x16x32_bf16 v[28:31], a[220:223], a[196:199], v[28:31]\n\t"
"buffer_load_dwordx4 a[220:223], off, s[80:83], s20 offset:1024 sc1\n\t"
"v_mfma_f32_16x16x32_bf16 v[28:31], a[228:231], a[200:203], v[28:31]\n\t"
"s_waitcnt lgkmcnt(4)\n\t"
"v_mfma_f32_16x16x32_bf16 v[28:31], a[224:227], a[204:207], v[28:31]\n\t"
"s_waitcnt lgkmcnt(2)\n\t"
"v_mfma_f32_16x16x32_bf16 v[236:239], v[236:239], a[208:211], 0\n\t"
"buffer_load_dwordx4 a[224:227], off, s[80:83], s20 offset:2048 sc1\n\t"
"v_mfma_f32_16x16x32_bf16 v[224:227], v[224:227], a[208:211], 0\n\t"
"s_waitcnt vmcnt(9) lgkmcnt(0)\n\t"
"s_mov_b32 s20, 1277952\n\t"
"v_mfma_f32_16x16x32_bf16 v[228:231], v[228:231], a[208:211], 0\n\t"
"v_mfma_f32_16x16x32_bf16 v[232:235], v[232:235], a[208:211], 0\n\t"
"buffer_load_dwordx4 a[228:231], off, s[80:83], s20 offset:0 sc1\n\t"
"v_mfma_f32_16x16x32_bf16 v[32:35], a[232:235], a[168:171], v[32:35]\n\t"
"v_mfma_f32_16x16x32_bf16 v[32:35], v[96:99], a[176:179], v[32:35]\n\t"
"v_cvt_pk_bf16_f32 v236, v236, v237\n\t"
"v_cvt_pk_bf16_f32 v237, v238, v239\n\t"
"v_mfma_f32_16x16x32_bf16 v[32:35], v[248:251], a[172:175], v[32:35]\n\t"
"v_cvt_pk_bf16_f32 v238, v224, v225\n\t"
"v_cvt_pk_bf16_f32 v239, v226, v227\n\t"
"v_mfma_f32_16x16x32_bf16 v[36:39], a[232:235], a[172:175], v[36:39]\n\t"
"ds_read_b64_tr_b16 v[224:225], v246 offset:1536\n\t"
"v_cvt_pk_bf16_f32 v228, v228, v229\n\t"
"v_mfma_f32_16x16x32_bf16 v[36:39], v[248:251], a[176:179], v[36:39]\n\t"
"v_cvt_pk_bf16_f32 v229, v230, v231\n\t"
"v_cvt_pk_bf16_f32 v230, v232, v233\n\t"
"v_mfma_f32_16x16x32_bf16 v[36:39], v[96:99], a[180:183], v[36:39]\n\t"
"buffer_load_dwordx4 a[208:211], off, s[80:83], s20 offset:1024 sc1\n\t"
"v_mfma_f32_16x16x32_bf16 v[40:43], v[96:99], a[184:187], v[40:43]\n\t"
"v_cvt_pk_bf16_f32 v231, v234, v235\n\t"
"v_mfma_f32_16x16x32_bf16 v[40:43], v[248:251], a[180:183], v[40:43]\n\t"
"ds_read_b64_tr_b16 v[232:233], v255 offset:1536\n\t"
"v_mfma_f32_16x16x32_bf16 v[40:43], a[232:235], a[176:179], v[40:43]\n\t"
"ds_write_b128 v243, v[236:239] offset:20480\n\t"
"v_mfma_f32_16x16x32_bf16 v[44:47], a[232:235], a[180:183], v[44:47]\n\t"
"v_mfma_f32_16x16x32_bf16 v[44:47], v[248:251], a[184:187], v[44:47]\n\t"
"v_mfma_f32_16x16x32_bf16 v[44:47], v[96:99], a[188:191], v[44:47]\n\t"
"s_barrier\n\t"
"v_mfma_f32_16x16x32_bf16 v[48:51], a[232:235], a[184:187], v[48:51]\n\t"
"s_waitcnt lgkmcnt(2)\n\t"
"ds_read_b64_tr_b16 v[236:237], v246 offset:7680\n\t"
"v_mfma_f32_16x16x32_bf16 v[48:51], v[96:99], a[192:195], v[48:51]\n\t"
"s_waitcnt lgkmcnt(2)\n\t"
"v_mfma_f32_16x16x32_bf16 v[48:51], v[248:251], a[188:191], v[48:51]\n\t"
"ds_write_b128 v243, v[228:231] offset:40448\n\t"
"ds_read_b64_tr_b16 v[228:229], v255 offset:7680\n\t"
"v_mfma_f32_4x4x4_16b_bf16 v[224:227], v[224:225], v[220:221], 0\n\t"
"buffer_load_dwordx4 v[240:243], off, s[80:83], s20 offset:2048 sc1\n\t"
"v_mfma_f32_4x4x4_16b_bf16 v[232:235], v[232:233], v[220:221], 0\n\t"
"s_waitcnt lgkmcnt(2)\n\t"
"v_mfma_f32_4x4x4_16b_bf16 v[236:239], v[236:237], v[220:221], 0\n\t"
"s_waitcnt lgkmcnt(0)\n\t"
"v_mfma_f32_4x4x4_16b_bf16 v[228:231], v[228:229], v[220:221], 0\n\t"
"v_mfma_f32_16x16x32_bf16 v[52:55], a[232:235], a[188:191], v[52:55]\n\t"
"s_mov_b32 s20, 1511424\n\t"
"v_cvt_pk_bf16_f32 v224, v224, v225\n\t"
"v_cvt_pk_bf16_f32 v225, v226, v227\n\t"
"v_mfma_f32_16x16x32_bf16 v[52:55], v[96:99], a[196:199], v[52:55]\n\t"
"v_cvt_pk_bf16_f32 v226, v232, v233\n\t"
"v_cvt_pk_bf16_f32 v227, v234, v235\n\t"
"v_mfma_f32_16x16x32_bf16 v[52:55], v[248:251], a[192:195], v[52:55]\n\t"
"ds_write_b128 v245, v[224:227] offset:1536\n\t"
"v_mfma_f32_16x16x32_bf16 v[56:59], a[232:235], a[192:195], v[56:59]\n\t"
"v_cvt_pk_bf16_f32 v236, v236, v237\n\t"
"v_cvt_pk_bf16_f32 v237, v238, v239\n\t"
"v_mfma_f32_16x16x32_bf16 v[56:59], v[248:251], a[196:199], v[56:59]\n\t"
"ds_read_b64_tr_b16 v[232:233], v246 offset:13824\n\t"
"v_mfma_f32_16x16x32_bf16 v[56:59], v[96:99], a[200:203], v[56:59]\n\t"
"v_cvt_pk_bf16_f32 v238, v228, v229\n\t"
"v_cvt_pk_bf16_f32 v239, v230, v231\n\t"
"v_mfma_f32_16x16x32_bf16 v[60:63], v[248:251], a[200:203], v[60:63]\n\t"
"s_waitcnt vmcnt(9)\n\t"
"ds_read_b64_tr_b16 v[228:229], v255 offset:13824\n\t"
"v_mfma_f32_16x16x32_bf16 v[60:63], a[232:235], a[196:199], v[60:63]\n\t"
"ds_read_b64_tr_b16 v[224:225], v246 offset:21504\n\t"
"v_mfma_f32_16x16x32_bf16 v[60:63], v[96:99], a[204:207], v[60:63]\n\t"
"ds_write_b128 v245, v[236:239] offset:7680\n\t"
"v_mfma_f32_16x16x32_bf16 v[64:67], a[236:239], a[168:171], v[64:67]\n\t"
"buffer_load_dwordx4 v[248:251], off, s[80:83], s20 offset:0 sc1\n\t"
"v_mfma_f32_16x16x32_bf16 v[64:67], a[240:243], a[176:179], v[64:67]\n\t"
"ds_read_b64_tr_b16 v[236:237], v255 offset:21504\n\t"
"v_mfma_f32_16x16x32_bf16 v[64:67], a[244:247], a[172:175], v[64:67]\n\t"
"buffer_load_dwordx4 v[96:99], off, s[80:83], s20 offset:1024 sc1\n\t"
"v_mfma_f32_16x16x32_bf16 v[68:71], a[236:239], a[172:175], v[68:71]\n\t"
"buffer_load_dwordx4 a[232:235], off, s[80:83], s20 offset:2048 sc1\n\t"
"v_mfma_f32_16x16x32_bf16 v[68:71], a[244:247], a[176:179], v[68:71]\n\t"
"s_waitcnt lgkmcnt(3)\n\t"
"ds_read_b128 a[168:171], v222 offset:2816\n\t"
"v_mfma_f32_16x16x32_bf16 v[68:71], a[240:243], a[180:183], v[68:71]\n\t"
"s_waitcnt lgkmcnt(3)\n\t"
"ds_read_b128 a[172:175], v223 offset:8960\n\t"
"v_mfma_f32_4x4x4_16b_bf16 v[232:235], v[232:233], v[220:221], 0\n\t"
"s_waitcnt lgkmcnt(2)\n\t"
"v_mfma_f32_4x4x4_16b_bf16 v[228:231], v[228:229], v[220:221], 0\n\t"
"v_mfma_f32_4x4x4_16b_bf16 v[224:227], v[224:225], v[220:221], 0\n\t"
"v_mfma_f32_4x4x4_16b_bf16 v[236:239], v[236:237], v[220:221], 0\n\t"
"v_mfma_f32_16x16x32_bf16 v[72:75], a[244:247], a[180:183], v[72:75]\n\t"
"v_cvt_pk_bf16_f32 v232, v232, v233\n\t"
"v_cvt_pk_bf16_f32 v233, v234, v235\n\t"
"v_mfma_f32_16x16x32_bf16 v[72:75], a[236:239], a[176:179], v[72:75]\n\t"
"ds_read_b128 a[176:179], v223 offset:15104\n\t"
"v_cvt_pk_bf16_f32 v234, v228, v229\n\t"
"v_mfma_f32_16x16x32_bf16 v[72:75], a[240:243], a[184:187], v[72:75]\n\t"
"v_cvt_pk_bf16_f32 v235, v230, v231\n\t"
"v_cvt_pk_bf16_f32 v224, v224, v225\n\t"
"v_mfma_f32_16x16x32_bf16 v[76:79], a[236:239], a[180:183], v[76:79]\n\t"
"ds_read_b64_tr_b16 v[228:229], v246 offset:27648\n\t"
"v_cvt_pk_bf16_f32 v225, v226, v227\n\t"
"v_mfma_f32_16x16x32_bf16 v[76:79], a[240:243], a[188:191], v[76:79]\n\t"
"ds_read_b128 a[180:183], v222 offset:22784\n\t"
"v_cvt_pk_bf16_f32 v226, v236, v237\n\t"
"v_mfma_f32_16x16x32_bf16 v[76:79], a[244:247], a[184:187], v[76:79]\n\t"
"ds_write_b128 v245, v[232:235] offset:13824\n\t"
"v_cvt_pk_bf16_f32 v227, v238, v239\n\t"
"v_mfma_f32_16x16x32_bf16 v[80:83], a[236:239], a[184:187], v[80:83]\n\t"
"v_mfma_f32_16x16x32_bf16 v[80:83], a[244:247], a[188:191], v[80:83]\n\t"
"ds_read_b64_tr_b16 v[236:237], v255 offset:27648\n\t"
"v_mfma_f32_16x16x32_bf16 v[80:83], a[240:243], a[192:195], v[80:83]\n\t"
"ds_write_b128 v245, v[224:227] offset:21504\n\t"
"v_mfma_f32_16x16x32_bf16 v[84:87], a[240:243], a[196:199], v[84:87]\n\t"
"s_mov_b32 s20, 1523712\n\t"
"v_mfma_f32_16x16x32_bf16 v[84:87], a[244:247], a[192:195], v[84:87]\n\t"
"ds_read_b64_tr_b16 v[232:233], v246 offset:33792\n\t"
"v_mfma_f32_16x16x32_bf16 v[84:87], a[236:239], a[188:191], v[84:87]\n\t"
"ds_read_b64_tr_b16 v[224:225], v255 offset:33792\n\t"
"v_mfma_f32_16x16x32_bf16 v[88:91], a[236:239], a[192:195], v[88:91]\n\t"
"ds_read_b128 a[184:187], v223 offset:28928\n\t"
"v_mfma_f32_16x16x32_bf16 v[88:91], a[244:247], a[196:199], v[88:91]\n\t"
"ds_read_b128 a[188:191], v223 offset:35072\n\t"
"v_mfma_f32_16x16x32_bf16 v[88:91], a[240:243], a[200:203], v[88:91]\n\t"
"ds_read_b128 a[192:195], v222 offset:42752\n\t"
"v_mfma_f32_16x16x32_bf16 v[92:95], a[244:247], a[200:203], v[92:95]\n\t"
"buffer_load_dwordx4 a[244:247], off, s[80:83], s20 offset:0 sc1\n\t"
"v_mfma_f32_16x16x32_bf16 v[92:95], a[236:239], a[196:199], v[92:95]\n\t"
"s_waitcnt lgkmcnt(6)\n\t"
"ds_read_b128 a[196:199], v223 offset:48896\n\t"
"v_mfma_f32_16x16x32_bf16 v[92:95], a[240:243], a[204:207], v[92:95]\n\t"
"s_waitcnt lgkmcnt(5)\n\t"
"ds_read_b128 a[200:203], v223 offset:55040\n\t"
"v_mfma_f32_4x4x4_16b_bf16 v[228:231], v[228:229], v[220:221], 0\n\t"
"s_waitcnt lgkmcnt(5)\n\t"
"v_mfma_f32_4x4x4_16b_bf16 v[236:239], v[236:237], v[220:221], 0\n\t"
"s_waitcnt vmcnt(10)\n\t"
"v_mfma_f32_4x4x4_16b_bf16 v[232:235], v[232:233], v[220:221], 0\n\t"
"v_mfma_f32_4x4x4_16b_bf16 v[224:227], v[224:225], v[220:221], 0\n\t"
"buffer_load_dwordx4 a[236:239], off, s[80:83], s20 offset:1024 sc1\n\t"
"v_mfma_f32_16x16x32_bf16 v[100:103], a[248:251], a[168:171], v[100:103]\n\t"
"buffer_load_dwordx4 a[240:243], off, s[80:83], s20 offset:2048 sc1\n\t"
"v_mfma_f32_16x16x32_bf16 v[100:103], a[212:215], a[176:179], v[100:103]\n\t"
"ds_read_b128 a[204:207], v222 offset:62720\n\t"
"v_cvt_pk_bf16_f32 v228, v228, v229\n\t"
"v_mfma_f32_16x16x32_bf16 v[100:103], a[216:219], a[172:175], v[100:103]\n\t"
"v_cvt_pk_bf16_f32 v229, v230, v231\n\t"
"v_cvt_pk_bf16_f32 v230, v236, v237\n\t"
"v_mfma_f32_16x16x32_bf16 v[104:107], a[248:251], a[172:175], v[104:107]\n\t"
"s_waitcnt lgkmcnt(5)\n\t"
"v_cvt_pk_bf16_f32 v231, v238, v239\n\t"
"v_cvt_pk_bf16_f32 v232, v232, v233\n\t"
"v_mfma_f32_16x16x32_bf16 v[104:107], a[216:219], a[176:179], v[104:107]\n\t"
"v_cvt_pk_bf16_f32 v233, v234, v235\n\t"
"v_cvt_pk_bf16_f32 v234, v224, v225\n\t"
"v_mfma_f32_16x16x32_bf16 v[104:107], a[212:215], a[180:183], v[104:107]\n\t"
"ds_write_b128 v245, v[228:231] offset:27648\n\t"
"v_cvt_pk_bf16_f32 v235, v226, v227\n\t"
"v_mfma_f32_16x16x32_bf16 v[108:111], a[212:215], a[184:187], v[108:111]\n\t"
"s_waitcnt lgkmcnt(5)\n\t"
"v_mfma_f32_16x16x32_bf16 v[108:111], a[216:219], a[180:183], v[108:111]\n\t"
"v_mfma_f32_16x16x32_bf16 v[108:111], a[248:251], a[176:179], v[108:111]\n\t"
"ds_write_b128 v245, v[232:235] offset:33792\n\t"
"v_mfma_f32_16x16x32_bf16 v[112:115], a[248:251], a[180:183], v[112:115]\n\t"
"s_waitcnt lgkmcnt(5)\n\t"
"v_mfma_f32_16x16x32_bf16 v[112:115], a[216:219], a[184:187], v[112:115]\n\t"
"ds_read_b64_tr_b16 v[236:237], v246 offset:41472\n\t"
"v_mfma_f32_16x16x32_bf16 v[112:115], a[212:215], a[188:191], v[112:115]\n\t"
"ds_read_b64_tr_b16 v[224:225], v255 offset:41472\n\t"
"v_mfma_f32_16x16x32_bf16 v[116:119], a[212:215], a[192:195], v[116:119]\n\t"
"s_waitcnt lgkmcnt(6)\n\t"
"ds_read_b64_tr_b16 v[228:229], v246 offset:47616\n\t"
"v_mfma_f32_16x16x32_bf16 v[116:119], a[216:219], a[188:191], v[116:119]\n\t"
"ds_read_b64_tr_b16 v[232:233], v255 offset:47616\n\t"
"v_mfma_f32_16x16x32_bf16 v[116:119], a[248:251], a[184:187], v[116:119]\n\t"
"v_mfma_f32_16x16x32_bf16 v[120:123], a[248:251], a[188:191], v[120:123]\n\t"
"s_waitcnt lgkmcnt(7)\n\t"
"s_mov_b32 s20, 1536000\n\t"
"v_mfma_f32_16x16x32_bf16 v[120:123], a[216:219], a[192:195], v[120:123]\n\t"
"v_mfma_f32_16x16x32_bf16 v[120:123], a[212:215], a[196:199], v[120:123]\n\t"
"v_mfma_f32_16x16x32_bf16 v[124:127], a[212:215], a[200:203], v[124:127]\n\t"
"s_waitcnt lgkmcnt(6)\n\t"
"v_mfma_f32_16x16x32_bf16 v[124:127], a[216:219], a[196:199], v[124:127]\n\t"
"v_mfma_f32_16x16x32_bf16 v[124:127], a[248:251], a[192:195], v[124:127]\n\t"
"v_mfma_f32_16x16x32_bf16 v[128:131], a[248:251], a[196:199], v[128:131]\n\t"
"buffer_load_dwordx4 a[248:251], off, s[80:83], s20 offset:0 sc1\n\t"
"v_mfma_f32_16x16x32_bf16 v[128:131], a[216:219], a[200:203], v[128:131]\n\t"
"s_waitcnt lgkmcnt(2)\n\t"
"v_mfma_f32_16x16x32_bf16 v[128:131], a[212:215], a[204:207], v[128:131]\n\t"
"s_waitcnt lgkmcnt(1)\n\t"
"v_mfma_f32_4x4x4_16b_bf16 v[236:239], v[236:237], v[220:221], 0\n\t"
"s_waitcnt lgkmcnt(0)\n\t"
"v_mfma_f32_4x4x4_16b_bf16 v[224:227], v[224:225], v[220:221], 0\n\t"
"s_waitcnt vmcnt(10)\n\t"
"v_mfma_f32_4x4x4_16b_bf16 v[228:231], v[228:229], v[220:221], 0\n\t"
"v_mfma_f32_4x4x4_16b_bf16 v[232:235], v[232:233], v[220:221], 0\n\t"
"buffer_load_dwordx4 a[212:215], off, s[80:83], s20 offset:1024 sc1\n\t"
"v_mfma_f32_16x16x32_bf16 v[132:135], a[252:255], a[168:171], v[132:135]\n\t"
"buffer_load_dwordx4 a[216:219], off, s[80:83], s20 offset:2048 sc1\n\t"
"v_mfma_f32_16x16x32_bf16 v[132:135], a[224:227], a[176:179], v[132:135]\n\t"
"v_cvt_pk_bf16_f32 v236, v236, v237\n\t"
"v_cvt_pk_bf16_f32 v237, v238, v239\n\t"
"v_mfma_f32_16x16x32_bf16 v[132:135], a[220:223], a[172:175], v[132:135]\n\t"
"s_mov_b32 s20, 1548288\n\t"
"v_cvt_pk_bf16_f32 v238, v224, v225\n\t"
"v_cvt_pk_bf16_f32 v239, v226, v227\n\t"
"v_mfma_f32_16x16x32_bf16 v[136:139], a[252:255], a[172:175], v[136:139]\n\t"
"ds_read_b64_tr_b16 v[224:225], v246 offset:53760\n\t"
"v_cvt_pk_bf16_f32 v228, v228, v229\n\t"
"v_mfma_f32_16x16x32_bf16 v[136:139], a[220:223], a[176:179], v[136:139]\n\t"
"v_cvt_pk_bf16_f32 v229, v230, v231\n\t"
"v_cvt_pk_bf16_f32 v230, v232, v233\n\t"
"v_mfma_f32_16x16x32_bf16 v[136:139], a[224:227], a[180:183], v[136:139]\n\t"
"ds_write_b128 v245, v[236:239] offset:41472\n\t"
"v_cvt_pk_bf16_f32 v231, v234, v235\n\t"
"v_mfma_f32_16x16x32_bf16 v[140:143], a[224:227], a[184:187], v[140:143]\n\t"
"v_mfma_f32_16x16x32_bf16 v[140:143], a[220:223], a[180:183], v[140:143]\n\t"
"ds_read_b64_tr_b16 v[232:233], v255 offset:53760\n\t"
"v_mfma_f32_16x16x32_bf16 v[140:143], a[252:255], a[176:179], v[140:143]\n\t"
"ds_write_b128 v245, v[228:231] offset:47616\n\t"
"v_mfma_f32_16x16x32_bf16 v[144:147], a[252:255], a[180:183], v[144:147]\n\t"
"buffer_load_dwordx4 v[236:239], off, s[80:83], s20 offset:0 sc1\n\t"
"v_mfma_f32_16x16x32_bf16 v[144:147], a[220:223], a[184:187], v[144:147]\n\t"
"v_mfma_f32_16x16x32_bf16 v[144:147], a[224:227], a[188:191], v[144:147]\n\t"
"v_mfma_f32_16x16x32_bf16 v[148:151], a[252:255], a[184:187], v[148:151]\n\t"
"s_waitcnt lgkmcnt(3)\n\t"
"v_mfma_f32_16x16x32_bf16 v[148:151], a[224:227], a[192:195], v[148:151]\n\t"
"s_waitcnt lgkmcnt(1)\n\t"
"v_mfma_f32_16x16x32_bf16 v[148:151], a[220:223], a[188:191], v[148:151]\n\t"
"v_mfma_f32_4x4x4_16b_bf16 v[224:227], v[224:225], v[220:221], 0\n\t"
"v_mfma_f32_4x4x4_16b_bf16 v[232:235], v[232:233], v[220:221], 0\n\t"
"v_mfma_f32_16x16x32_bf16 v[152:155], a[252:255], a[188:191], v[152:155]\n\t"
"v_mfma_f32_16x16x32_bf16 v[152:155], a[224:227], a[196:199], v[152:155]\n\t"
"v_cvt_pk_bf16_f32 v224, v224, v225\n\t"
"v_cvt_pk_bf16_f32 v225, v226, v227\n\t"
"v_mfma_f32_16x16x32_bf16 v[152:155], a[220:223], a[192:195], v[152:155]\n\t"
"v_cvt_pk_bf16_f32 v226, v232, v233\n\t"
"v_cvt_pk_bf16_f32 v227, v234, v235\n\t"
"v_mfma_f32_16x16x32_bf16 v[156:159], a[252:255], a[192:195], v[156:159]\n\t"
"v_mfma_f32_16x16x32_bf16 v[156:159], a[220:223], a[196:199], v[156:159]\n\t"
"v_mfma_f32_16x16x32_bf16 v[156:159], a[224:227], a[200:203], v[156:159]\n\t"
"ds_write_b128 v245, v[224:227] offset:53760\n\t"
"v_mfma_f32_16x16x32_bf16 v[160:163], a[220:223], a[200:203], v[160:163]\n\t"
"buffer_load_dwordx4 a[220:223], off, s[80:83], s20 offset:1024 sc1\n\t"
"v_mfma_f32_16x16x32_bf16 v[160:163], a[252:255], a[196:199], v[160:163]\n\t"
"s_waitcnt vmcnt(11)\n\t"
"v_mfma_f32_16x16x32_bf16 v[160:163], a[224:227], a[204:207], v[160:163]\n\t"
"v_mfma_f32_16x16x32_bf16 v[164:167], a[228:231], a[168:171], v[164:167]\n\t"
"buffer_load_dwordx4 a[224:227], off, s[80:83], s20 offset:2048 sc1\n\t"
"v_mfma_f32_16x16x32_bf16 v[164:167], v[240:243], a[176:179], v[164:167]\n\t"
"s_mov_b32 s20, 1560576\n\t"
"v_mfma_f32_16x16x32_bf16 v[164:167], a[208:211], a[172:175], v[164:167]\n\t"
"s_waitcnt lgkmcnt(0)\n\t"
"s_barrier\n\t"
"v_mfma_f32_16x16x32_bf16 v[168:171], a[228:231], a[172:175], v[168:171]\n\t"
"ds_read_b128 a[168:171], v222 offset:3072\n\t"
"v_mfma_f32_16x16x32_bf16 v[168:171], a[208:211], a[176:179], v[168:171]\n\t"
"buffer_load_dwordx4 a[252:255], off, s[80:83], s20 offset:0 sc1\n\t"
"v_mfma_f32_16x16x32_bf16 v[168:171], v[240:243], a[180:183], v[168:171]\n\t"
"ds_read_b128 a[172:175], v223 offset:9216\n\t"
"v_mfma_f32_16x16x32_bf16 v[172:175], v[240:243], a[184:187], v[172:175]\n\t"
"v_mfma_f32_16x16x32_bf16 v[172:175], a[208:211], a[180:183], v[172:175]\n\t"
"v_mfma_f32_16x16x32_bf16 v[172:175], a[228:231], a[176:179], v[172:175]\n\t"
"ds_read_b128 a[176:179], v223 offset:15360\n\t"
"v_mfma_f32_16x16x32_bf16 v[176:179], a[228:231], a[180:183], v[176:179]\n\t"
"ds_read_b128 a[180:183], v222 offset:23040\n\t"
"v_mfma_f32_16x16x32_bf16 v[176:179], a[208:211], a[184:187], v[176:179]\n\t"
"v_mfma_f32_16x16x32_bf16 v[176:179], v[240:243], a[188:191], v[176:179]\n\t"
"v_mfma_f32_16x16x32_bf16 v[180:183], v[240:243], a[192:195], v[180:183]\n\t"
"v_mfma_f32_16x16x32_bf16 v[180:183], a[208:211], a[188:191], v[180:183]\n\t"
"v_mfma_f32_16x16x32_bf16 v[180:183], a[228:231], a[184:187], v[180:183]\n\t"
"ds_read_b128 a[184:187], v223 offset:29184\n\t"
"v_mfma_f32_16x16x32_bf16 v[184:187], a[228:231], a[188:191], v[184:187]\n\t"
"ds_read_b128 a[188:191], v223 offset:35328\n\t"
"v_mfma_f32_16x16x32_bf16 v[184:187], a[208:211], a[192:195], v[184:187]\n\t"
"v_mfma_f32_16x16x32_bf16 v[184:187], v[240:243], a[196:199], v[184:187]\n\t"
"v_mfma_f32_16x16x32_bf16 v[188:191], v[240:243], a[200:203], v[188:191]\n\t"
"v_mfma_f32_16x16x32_bf16 v[188:191], a[208:211], a[196:199], v[188:191]\n\t"
"v_mfma_f32_16x16x32_bf16 v[188:191], a[228:231], a[192:195], v[188:191]\n\t"
"ds_read_b128 a[192:195], v222 offset:43008\n\t"
"v_mfma_f32_16x16x32_bf16 v[192:195], a[228:231], a[196:199], v[192:195]\n\t"
"buffer_load_dwordx4 a[228:231], off, s[80:83], s20 offset:1024 sc1\n\t"
"v_mfma_f32_16x16x32_bf16 v[192:195], a[208:211], a[200:203], v[192:195]\n\t"
"s_waitcnt vmcnt(11) lgkmcnt(4)\n\t"
"ds_read_b128 a[196:199], v223 offset:49152\n\t"
"v_mfma_f32_16x16x32_bf16 v[192:195], v[240:243], a[204:207], v[192:195]\n\t"
"ds_read_b128 a[200:203], v223 offset:55296\n\t"
"Lst29_%=:\n\t"
"v_mfma_f32_16x16x32_bf16 v[0:3], v[248:251], a[168:171], v[0:3]\n\t"
"buffer_load_dwordx4 a[208:211], off, s[80:83], s20 offset:2048 sc1\n\t"
"v_mfma_f32_16x16x32_bf16 v[0:3], a[232:235], a[176:179], v[0:3]\n\t"
"s_waitcnt lgkmcnt(5)\n\t"
"ds_read_b128 a[204:207], v222 offset:62976\n\t"
"s_mov_b32 s20, 1572864\n\t"
"v_mfma_f32_16x16x32_bf16 v[0:3], v[96:99], a[172:175], v[0:3]\n\t"
"v_mfma_f32_16x16x32_bf16 v[4:7], v[248:251], a[172:175], v[4:7]\n\t"
"buffer_load_dwordx4 v[240:243], off, s[80:83], s20 offset:0 sc1\n\t"
"v_mfma_f32_16x16x32_bf16 v[4:7], v[96:99], a[176:179], v[4:7]\n\t"
"s_waitcnt lgkmcnt(5)\n\t"
"v_mfma_f32_16x16x32_bf16 v[4:7], a[232:235], a[180:183], v[4:7]\n\t"
"v_mfma_f32_16x16x32_bf16 v[8:11], a[232:235], a[184:187], v[8:11]\n\t"
"s_waitcnt lgkmcnt(4)\n\t"
"v_mfma_f32_16x16x32_bf16 v[8:11], v[96:99], a[180:183], v[8:11]\n\t"
"v_mfma_f32_16x16x32_bf16 v[8:11], v[248:251], a[176:179], v[8:11]\n\t"
"v_mfma_f32_16x16x32_bf16 v[12:15], v[248:251], a[180:183], v[12:15]\n\t"
"s_waitcnt lgkmcnt(3)\n\t"
"v_mfma_f32_16x16x32_bf16 v[12:15], v[96:99], a[184:187], v[12:15]\n\t"
"v_mfma_f32_16x16x32_bf16 v[12:15], a[232:235], a[188:191], v[12:15]\n\t"
"v_mfma_f32_16x16x32_bf16 v[16:19], a[232:235], a[192:195], v[16:19]\n\t"
"s_waitcnt lgkmcnt(2)\n\t"
"v_mfma_f32_16x16x32_bf16 v[16:19], v[96:99], a[188:191], v[16:19]\n\t"
"v_mfma_f32_16x16x32_bf16 v[16:19], v[248:251], a[184:187], v[16:19]\n\t"
"v_mfma_f32_16x16x32_bf16 v[20:23], v[248:251], a[188:191], v[20:23]\n\t"
"s_waitcnt lgkmcnt(1)\n\t"
"v_mfma_f32_16x16x32_bf16 v[20:23], v[96:99], a[192:195], v[20:23]\n\t"
"v_mfma_f32_16x16x32_bf16 v[20:23], a[232:235], a[196:199], v[20:23]\n\t"
"v_mfma_f32_16x16x32_bf16 v[24:27], a[232:235], a[200:203], v[24:27]\n\t"
"s_waitcnt lgkmcnt(0)\n\t"
"v_mfma_f32_16x16x32_bf16 v[24:27], v[96:99], a[196:199], v[24:27]\n\t"
"v_mfma_f32_16x16x32_bf16 v[24:27], v[248:251], a[192:195], v[24:27]\n\t"
"v_mfma_f32_16x16x32_bf16 v[28:31], v[248:251], a[196:199], v[28:31]\n\t"
"buffer_load_dwordx4 v[248:251], off, s[80:83], s20 offset:1024 sc1\n\t"
"v_mfma_f32_16x16x32_bf16 v[28:31], v[96:99], a[200:203], v[28:31]\n\t"
"s_waitcnt vmcnt(11)\n\t"
"v_mfma_f32_16x16x32_bf16 v[28:31], a[232:235], a[204:207], v[28:31]\n\t"
"v_mfma_f32_16x16x32_bf16 v[32:35], a[244:247], a[168:171], v[32:35]\n\t"
"buffer_load_dwordx4 v[96:99], off, s[80:83], s20 offset:2048 sc1\n\t"
"v_mfma_f32_16x16x32_bf16 v[32:35], a[240:243], a[176:179], v[32:35]\n\t"
"s_mov_b32 s20, 1290240\n\t"
"v_mfma_f32_16x16x32_bf16 v[32:35], a[236:239], a[172:175], v[32:35]\n\t"
"v_mfma_f32_16x16x32_bf16 v[36:39], a[244:247], a[172:175], v[36:39]\n\t"
"buffer_load_dwordx4 a[232:235], off, s[80:83], s20 offset:0 sc1\n\t"
"v_mfma_f32_16x16x32_bf16 v[36:39], a[236:239], a[176:179], v[36:39]\n\t"
"v_mfma_f32_16x16x32_bf16 v[36:39], a[240:243], a[180:183], v[36:39]\n\t"
"v_mfma_f32_16x16x32_bf16 v[40:43], a[240:243], a[184:187], v[40:43]\n\t"
"v_mfma_f32_16x16x32_bf16 v[40:43], a[236:239], a[180:183], v[40:43]\n\t"
"v_mfma_f32_16x16x32_bf16 v[40:43], a[244:247], a[176:179], v[40:43]\n\t"
"v_mfma_f32_16x16x32_bf16 v[44:47], a[244:247], a[180:183], v[44:47]\n\t"
"v_mfma_f32_16x16x32_bf16 v[44:47], a[236:239], a[184:187], v[44:47]\n\t"
"v_mfma_f32_16x16x32_bf16 v[44:47], a[240:243], a[188:191], v[44:47]\n\t"
"v_mfma_f32_16x16x32_bf16 v[48:51], a[240:243], a[192:195], v[48:51]\n\t"
"v_mfma_f32_16x16x32_bf16 v[48:51], a[236:239], a[188:191], v[48:51]\n\t"
"v_mfma_f32_16x16x32_bf16 v[48:51], a[244:247], a[184:187], v[48:51]\n\t"
"v_mfma_f32_16x16x32_bf16 v[52:55], a[244:247], a[188:191], v[52:55]\n\t"
"v_mfma_f32_16x16x32_bf16 v[52:55], a[236:239], a[192:195], v[52:55]\n\t"
"v_mfma_f32_16x16x32_bf16 v[52:55], a[240:243], a[196:199], v[52:55]\n\t"
"v_mfma_f32_16x16x32_bf16 v[56:59], a[240:243], a[200:203], v[56:59]\n\t"
"v_mfma_f32_16x16x32_bf16 v[56:59], a[236:239], a[196:199], v[56:59]\n\t"
"v_mfma_f32_16x16x32_bf16 v[56:59], a[244:247], a[192:195], v[56:59]\n\t"
"v_mfma_f32_16x16x32_bf16 v[60:63], a[244:247], a[196:199], v[60:63]\n\t"
"buffer_load_dwordx4 a[244:247], off, s[80:83], s20 offset:1024 sc1\n\t"
"v_mfma_f32_16x16x32_bf16 v[60:63], a[236:239], a[200:203], v[60:63]\n\t"
"s_waitcnt vmcnt(11)\n\t"
"v_mfma_f32_16x16x32_bf16 v[60:63], a[240:243], a[204:207], v[60:63]\n\t"
"v_mfma_f32_16x16x32_bf16 v[64:67], a[248:251], a[168:171], v[64:67]\n\t"
"buffer_load_dwordx4 a[236:239], off, s[80:83], s20 offset:2048 sc1\n\t"
"v_mfma_f32_16x16x32_bf16 v[64:67], a[216:219], a[176:179], v[64:67]\n\t"
"s_mov_b32 s20, 1302528\n\t"
"ds_read_b128 a[168:171], v222 offset:3328\n\t"
"v_mfma_f32_16x16x32_bf16 v[64:67], a[212:215], a[172:175], v[64:67]\n\t"
"v_mfma_f32_16x16x32_bf16 v[68:71], a[248:251], a[172:175], v[68:71]\n\t"
"buffer_load_dwordx4 a[240:243], off, s[80:83], s20 offset:0 sc1\n\t"
"v_mfma_f32_16x16x32_bf16 v[68:71], a[212:215], a[176:179], v[68:71]\n\t"
"ds_read_b128 a[172:175], v223 offset:9472\n\t"
"v_mfma_f32_16x16x32_bf16 v[68:71], a[216:219], a[180:183], v[68:71]\n\t"
"v_mfma_f32_16x16x32_bf16 v[72:75], a[216:219], a[184:187], v[72:75]\n\t"
"v_mfma_f32_16x16x32_bf16 v[72:75], a[212:215], a[180:183], v[72:75]\n\t"
"v_mfma_f32_16x16x32_bf16 v[72:75], a[248:251], a[176:179], v[72:75]\n\t"
"ds_read_b128 a[176:179], v223 offset:15616\n\t"
"v_mfma_f32_16x16x32_bf16 v[76:79], a[248:251], a[180:183], v[76:79]\n\t"
"ds_read_b128 a[180:183], v222 offset:23296\n\t"
"v_mfma_f32_16x16x32_bf16 v[76:79], a[212:215], a[184:187], v[76:79]\n\t"
"v_mfma_f32_16x16x32_bf16 v[76:79], a[216:219], a[188:191], v[76:79]\n\t"
"v_mfma_f32_16x16x32_bf16 v[80:83], a[216:219], a[192:195], v[80:83]\n\t"
"v_mfma_f32_16x16x32_bf16 v[80:83], a[212:215], a[188:191], v[80:83]\n\t"
"v_mfma_f32_16x16x32_bf16 v[80:83], a[248:251], a[184:187], v[80:83]\n\t"
"ds_read_b128 a[184:187], v223 offset:29440\n\t"
"v_mfma_f32_16x16x32_bf16 v[84:87], a[248:251], a[188:191], v[84:87]\n\t"
"ds_read_b128 a[188:191], v223 offset:35584\n\t"
"v_mfma_f32_16x16x32_bf16 v[84:87], a[212:215], a[192:195], v[84:87]\n\t"
"v_mfma_f32_16x16x32_bf16 v[84:87], a[216:219], a[196:199], v[84:87]\n\t"
"v_mfma_f32_16x16x32_bf16 v[88:91], a[216:219], a[200:203], v[88:91]\n\t"
"v_mfma_f32_16x16x32_bf16 v[88:91], a[212:215], a[196:199], v[88:91]\n\t"
"v_mfma_f32_16x16x32_bf16 v[88:91], a[248:251], a[192:195], v[88:91]\n\t"
"ds_read_b128 a[192:195], v222 offset:43264\n\t"
"v_mfma_f32_16x16x32_bf16 v[92:95], a[248:251], a[196:199], v[92:95]\n\t"
"buffer_load_dwordx4 a[248:251], off, s[80:83], s20 offset:1024 sc1\n\t"
"v_mfma_f32_16x16x32_bf16 v[92:95], a[212:215], a[200:203], v[92:95]\n\t"
"s_waitcnt vmcnt(11) lgkmcnt(4)\n\t"
"ds_read_b128 a[196:199], v223 offset:49408\n\t"
"v_mfma_f32_16x16x32_bf16 v[92:95], a[216:219], a[204:207], v[92:95]\n\t"
"ds_read_b128 a[200:203], v223 offset:55552\n\t"
"v_mfma_f32_16x16x32_bf16 v[100:103], v[236:239], a[168:171], v[100:103]\n\t"
"buffer_load_dwordx4 a[212:215], off, s[80:83], s20 offset:2048 sc1\n\t"
"v_mfma_f32_16x16x32_bf16 v[100:103], a[224:227], a[176:179], v[100:103]\n\t"
"s_waitcnt lgkmcnt(5)\n\t"
"ds_read_b128 a[204:207], v222 offset:63232\n\t"
"s_mov_b32 s20, 1314816\n\t"
"v_mfma_f32_16x16x32_bf16 v[100:103], a[220:223], a[172:175], v[100:103]\n\t"
"v_mfma_f32_16x16x32_bf16 v[104:107], v[236:239], a[172:175], v[104:107]\n\t"
"buffer_load_dwordx4 a[216:219], off, s[80:83], s20 offset:0 sc1\n\t"
"v_mfma_f32_16x16x32_bf16 v[104:107], a[220:223], a[176:179], v[104:107]\n\t"
"s_waitcnt lgkmcnt(5)\n\t"
"v_mfma_f32_16x16x32_bf16 v[104:107], a[224:227], a[180:183], v[104:107]\n\t"
"v_mfma_f32_16x16x32_bf16 v[108:111], a[224:227], a[184:187], v[108:111]\n\t"
"s_waitcnt lgkmcnt(4)\n\t"
"v_fma_f32 v0, v100, s52, v0\n\t"
"v_fma_f32 v1, v101, s52, v1\n\t"
"v_mfma_f32_16x16x32_bf16 v[108:111], a[220:223], a[180:183], v[108:111]\n\t"
"v_fma_f32 v2, v102, s52, v2\n\t"
"v_fma_f32 v3, v103, s52, v3\n\t"
"v_mfma_f32_16x16x32_bf16 v[108:111], v[236:239], a[176:179], v[108:111]\n\t"
"v_fma_f32 v100, v0, s54, -v100\n\t"
"v_fma_f32 v101, v1, s54, -v101\n\t"
"v_mfma_f32_16x16x32_bf16 v[112:115], v[236:239], a[180:183], v[112:115]\n\t"
"s_waitcnt lgkmcnt(3)\n\t"
"v_fma_f32 v102, v2, s54, -v102\n\t"
"v_fma_f32 v103, v3, s54, -v103\n\t"
"v_mfma_f32_16x16x32_bf16 v[112:115], a[220:223], a[184:187], v[112:115]\n\t"
"s_waitcnt lgkmcnt(1)\n\t"
"v_fma_f32 v4, v104, s52, v4\n\t"
"v_fma_f32 v5, v105, s52, v5\n\t"
"v_mfma_f32_16x16x32_bf16 v[112:115], a[224:227], a[188:191], v[112:115]\n\t"
"Lpg21_%=:\n\t"
"s_cbranch_vccnz Lvr3_%=\n\t"
"v_fma_f32 v6, v106, s52, v6\n\t"
"v_fma_f32 v7, v107, s52, v7\n\t"
"v_mfma_f32_16x16x32_bf16 v[116:119], a[224:227], a[192:195], v[116:119]\n\t"
"v_fma_f32 v104, v4, s54, -v104\n\t"
"v_fma_f32 v105, v5, s54, -v105\n\t"
"v_mfma_f32_16x16x32_bf16 v[116:119], a[220:223], a[188:191], v[116:119]\n\t"
"v_fma_f32 v106, v6, s54, -v106\n\t"
"v_fma_f32 v107, v7, s54, -v107\n\t"
"v_mfma_f32_16x16x32_bf16 v[116:119], v[236:239], a[184:187], v[116:119]\n\t"
"v_fma_f32 v8, v108, s52, v8\n\t"
"v_fma_f32 v9, v109, s52, v9\n\t"
"v_mfma_f32_16x16x32_bf16 v[120:123], v[236:239], a[188:191], v[120:123]\n\t"
"v_fma_f32 v10, v110, s52, v10\n\t"
"v_fma_f32 v11, v111, s52, v11\n\t"
"v_mfma_f32_16x16x32_bf16 v[120:123], a[220:223], a[192:195], v[120:123]\n\t"
"v_fma_f32 v108, v8, s54, -v108\n\t"
"v_fma_f32 v109, v9, s54, -v109\n\t"
"v_mfma_f32_16x16x32_bf16 v[120:123], a[224:227], a[196:199], v[120:123]\n\t"
"v_fma_f32 v110, v10, s54, -v110\n\t"
"v_fma_f32 v111, v11, s54, -v111\n\t"
"v_mfma_f32_16x16x32_bf16 v[124:127], a[224:227], a[200:203], v[124:127]\n\t"
"s_waitcnt lgkmcnt(0)\n\t"
"v_fma_f32 v12, v112, s52, v12\n\t"
"v_fma_f32 v13, v113, s52, v13\n\t"
"v_mfma_f32_16x16x32_bf16 v[124:127], a[220:223], a[196:199], v[124:127]\n\t"
"v_fma_f32 v14, v114, s52, v14\n\t"
"v_fma_f32 v15, v115, s52, v15\n\t"
"v_mfma_f32_16x16x32_bf16 v[124:127], v[236:239], a[192:195], v[124:127]\n\t"
"v_fma_f32 v112, v12, s54, -v112\n\t"
"v_fma_f32 v113, v13, s54, -v113\n\t"
"v_mfma_f32_16x16x32_bf16 v[128:131], v[236:239], a[196:199], v[128:131]\n\t"
"s_waitcnt vmcnt(10)\n\t"
"v_fma_f32 v114, v14, s54, -v114\n\t"
"v_fma_f32 v115, v15, s54, -v115\n\t"
"v_mfma_f32_16x16x32_bf16 v[128:131], a[220:223], a[200:203], v[128:131]\n\t"
"v_fma_f32 v16, v116, s52, v16\n\t"
"v_fma_f32 v17, v117, s52, v17\n\t"
"v_mfma_f32_16x16x32_bf16 v[128:131], a[224:227], a[204:207], v[128:131]\n\t"
"v_fma_f32 v18, v118, s52, v18\n\t"
"v_fma_f32 v19, v119, s52, v19\n\t"
"v_mfma_f32_16x16x32_bf16 v[132:135], a[252:255], a[168:171], v[132:135]\n\t"
"v_fma_f32 v116, v16, s54, -v116\n\t"
"v_fma_f32 v117, v17, s54, -v117\n\t"
"v_mfma_f32_16x16x32_bf16 v[132:135], a[208:211], a[176:179], v[132:135]\n\t"
"v_fma_f32 v118, v18, s54, -v118\n\t"
"v_fma_f32 v119, v19, s54, -v119\n\t"
"v_mfma_f32_16x16x32_bf16 v[132:135], a[228:231], a[172:175], v[132:135]\n\t"
"buffer_load_dwordx4 v[236:239], off, s[80:83], s20 offset:1024 sc1\n\t"
"v_mfma_f32_16x16x32_bf16 v[136:139], a[252:255], a[172:175], v[136:139]\n\t"
"buffer_load_dwordx4 a[220:223], off, s[80:83], s20 offset:2048 sc1\n\t"
"v_mfma_f32_16x16x32_bf16 v[136:139], a[228:231], a[176:179], v[136:139]\n\t"
"s_mov_b32 s20, 1327104\n\t"
"v_fma_f32 v20, v120, s52, v20\n\t"
"v_fma_f32 v21, v121, s52, v21\n\t"
"v_mfma_f32_16x16x32_bf16 v[136:139], a[208:211], a[180:183], v[136:139]\n\t"
"v_fma_f32 v22, v122, s52, v22\n\t"
"v_fma_f32 v23, v123, s52, v23\n\t"
"v_mfma_f32_16x16x32_bf16 v[140:143], a[208:211], a[184:187], v[140:143]\n\t"
"v_fma_f32 v120, v20, s54, -v120\n\t"
"v_fma_f32 v121, v21, s54, -v121\n\t"
"v_mfma_f32_16x16x32_bf16 v[140:143], a[228:231], a[180:183], v[140:143]\n\t"
"v_fma_f32 v122, v22, s54, -v122\n\t"
"v_fma_f32 v123, v23, s54, -v123\n\t"
"v_mfma_f32_16x16x32_bf16 v[140:143], a[252:255], a[176:179], v[140:143]\n\t"
"buffer_load_dwordx4 a[224:227], off, s[80:83], s20 offset:0 sc1\n\t"
"v_mfma_f32_16x16x32_bf16 v[144:147], a[252:255], a[180:183], v[144:147]\n\t"
"v_fma_f32 v24, v124, s52, v24\n\t"
"v_fma_f32 v25, v125, s52, v25\n\t"
"v_mfma_f32_16x16x32_bf16 v[144:147], a[228:231], a[184:187], v[144:147]\n\t"
"v_fma_f32 v26, v126, s52, v26\n\t"
"v_fma_f32 v27, v127, s52, v27\n\t"
"v_mfma_f32_16x16x32_bf16 v[144:147], a[208:211], a[188:191], v[144:147]\n\t"
"v_fma_f32 v124, v24, s54, -v124\n\t"
"v_fma_f32 v125, v25, s54, -v125\n\t"
"v_mfma_f32_16x16x32_bf16 v[148:151], a[208:211], a[192:195], v[148:151]\n\t"
"v_fma_f32 v126, v26, s54, -v126\n\t"
"v_fma_f32 v127, v27, s54, -v127\n\t"
"v_mfma_f32_16x16x32_bf16 v[148:151], a[228:231], a[188:191], v[148:151]\n\t"
"v_fma_f32 v28, v128, s52, v28\n\t"
"v_fma_f32 v29, v129, s52, v29\n\t"
"v_mfma_f32_16x16x32_bf16 v[148:151], a[252:255], a[184:187], v[148:151]\n\t"
"v_fma_f32 v30, v130, s52, v30\n\t"
"v_fma_f32 v31, v131, s52, v31\n\t"
"v_mfma_f32_16x16x32_bf16 v[152:155], a[252:255], a[188:191], v[152:155]\n\t"
"v_fma_f32 v128, v28, s54, -v128\n\t"
"v_fma_f32 v129, v29, s54, -v129\n\t"
"v_mfma_f32_16x16x32_bf16 v[152:155], a[228:231], a[192:195], v[152:155]\n\t"
"v_fma_f32 v130, v30, s54, -v130\n\t"
"v_fma_f32 v131, v31, s54, -v131\n\t"
"v_mfma_f32_16x16x32_bf16 v[152:155], a[208:211], a[196:199], v[152:155]\n\t"
"v_fma_f32 v32, v132, s52, v32\n\t"
"v_fma_f32 v33, v133, s52, v33\n\t"
"v_mfma_f32_16x16x32_bf16 v[156:159], a[208:211], a[200:203], v[156:159]\n\t"
"v_fma_f32 v34, v134, s52, v34\n\t"
"v_fma_f32 v35, v135, s52, v35\n\t"
"v_mfma_f32_16x16x32_bf16 v[156:159], a[228:231], a[196:199], v[156:159]\n\t"
"v_fma_f32 v132, v32, s54, -v132\n\t"
"v_fma_f32 v133, v33, s54, -v133\n\t"
"v_mfma_f32_16x16x32_bf16 v[156:159], a[252:255], a[192:195], v[156:159]\n\t"
"v_fma_f32 v134, v34, s54, -v134\n\t"
"v_fma_f32 v135, v35, s54, -v135\n\t"
"v_mfma_f32_16x16x32_bf16 v[160:163], a[252:255], a[196:199], v[160:163]\n\t"
"s_waitcnt vmcnt(10)\n\t"
"v_fma_f32 v36, v136, s52, v36\n\t"
"v_fma_f32 v37, v137, s52, v37\n\t"
"v_mfma_f32_16x16x32_bf16 v[160:163], a[228:231], a[200:203], v[160:163]\n\t"
"v_fma_f32 v38, v138, s52, v38\n\t"
"v_fma_f32 v39, v139, s52, v39\n\t"
"v_mfma_f32_16x16x32_bf16 v[160:163], a[208:211], a[204:207], v[160:163]\n\t"
"v_fma_f32 v136, v36, s54, -v136\n\t"
"v_fma_f32 v137, v37, s54, -v137\n\t"
"v_mfma_f32_16x16x32_bf16 v[164:167], v[240:243], a[168:171], v[164:167]\n\t"
"ds_read_b128 a[168:171], v222 offset:1024\n\t"
"v_fma_f32 v138, v38, s54, -v138\n\t"
"v_mfma_f32_16x16x32_bf16 v[164:167], v[96:99], a[176:179], v[164:167]\n\t"
"v_fma_f32 v139, v39, s54, -v139\n\t"
"v_fma_f32 v40, v140, s52, v40\n\t"
"v_mfma_f32_16x16x32_bf16 v[164:167], v[248:251], a[172:175], v[164:167]\n\t"
"buffer_load_dwordx4 a[252:255], off, s[80:83], s20 offset:1024 sc1\n\t"
"v_mfma_f32_16x16x32_bf16 v[168:171], v[240:243], a[172:175], v[168:171]\n\t"
"buffer_load_dwordx4 a[208:211], off, s[80:83], s20 offset:2048 sc1\n\t"
"v_mfma_f32_16x16x32_bf16 v[168:171], v[248:251], a[176:179], v[168:171]\n\t"
"s_mov_b32 s20, 1339392\n\t"
"ds_read_b128 a[172:175], v223 offset:7168\n\t"
"v_fma_f32 v41, v141, s52, v41\n\t"
"v_mfma_f32_16x16x32_bf16 v[168:171], v[96:99], a[180:183], v[168:171]\n\t"
"v_fma_f32 v42, v142, s52, v42\n\t"
"v_fma_f32 v43, v143, s52, v43\n\t"
"v_mfma_f32_16x16x32_bf16 v[172:175], v[96:99], a[184:187], v[172:175]\n\t"
"v_fma_f32 v140, v40, s54, -v140\n\t"
"v_fma_f32 v141, v41, s54, -v141\n\t"
"v_mfma_f32_16x16x32_bf16 v[172:175], v[248:251], a[180:183], v[172:175]\n\t"
"v_fma_f32 v142, v42, s54, -v142\n\t"
"v_fma_f32 v143, v43, s54, -v143\n\t"
"v_mfma_f32_16x16x32_bf16 v[172:175], v[240:243], a[176:179], v[172:175]\n\t"
"buffer_load_dwordx4 a[228:231], off, s[80:83], s20 offset:0 sc1\n\t"
"v_mfma_f32_16x16x32_bf16 v[176:179], v[240:243], a[180:183], v[176:179]\n\t"
"ds_read_b128 a[176:179], v223 offset:13312\n\t"
"v_fma_f32 v44, v144, s52, v44\n\t"
"v_mfma_f32_16x16x32_bf16 v[176:179], v[248:251], a[184:187], v[176:179]\n\t"
"ds_read_b128 a[180:183], v222 offset:20992\n\t"
"v_fma_f32 v45, v145, s52, v45\n\t"
"v_mfma_f32_16x16x32_bf16 v[176:179], v[96:99], a[188:191], v[176:179]\n\t"
"v_fma_f32 v46, v146, s52, v46\n\t"
"v_fma_f32 v47, v147, s52, v47\n\t"
"v_mfma_f32_16x16x32_bf16 v[180:183], v[96:99], a[192:195], v[180:183]\n\t"
"v_fma_f32 v144, v44, s54, -v144\n\t"
"v_fma_f32 v145, v45, s54, -v145\n\t"
"v_mfma_f32_16x16x32_bf16 v[180:183], v[248:251], a[188:191], v[180:183]\n\t"
"v_fma_f32 v146, v46, s54, -v146\n\t"
"v_fma_f32 v147, v47, s54, -v147\n\t"
"v_mfma_f32_16x16x32_bf16 v[180:183], v[240:243], a[184:187], v[180:183]\n\t"
"ds_read_b128 a[184:187], v223 offset:27136\n\t"
"v_fma_f32 v48, v148, s52, v48\n\t"
"v_mfma_f32_16x16x32_bf16 v[184:187], v[240:243], a[188:191], v[184:187]\n\t"
"ds_read_b128 a[188:191], v223 offset:33280\n\t"
"v_fma_f32 v49, v149, s52, v49\n\t"
"v_mfma_f32_16x16x32_bf16 v[184:187], v[248:251], a[192:195], v[184:187]\n\t"
"v_fma_f32 v50, v150, s52, v50\n\t"
"v_fma_f32 v51, v151, s52, v51\n\t"
"v_mfma_f32_16x16x32_bf16 v[184:187], v[96:99], a[196:199], v[184:187]\n\t"
"v_fma_f32 v148, v48, s54, -v148\n\t"
"v_fma_f32 v149, v49, s54, -v149\n\t"
"v_mfma_f32_16x16x32_bf16 v[188:191], v[96:99], a[200:203], v[188:191]\n\t"
"v_fma_f32 v150, v50, s54, -v150\n\t"
"v_fma_f32 v151, v51, s54, -v151\n\t"
"v_mfma_f32_16x16x32_bf16 v[188:191], v[248:251], a[196:199], v[188:191]\n\t"
"v_fma_f32 v52, v152, s52, v52\n\t"
"v_fma_f32 v53, v153, s52, v53\n\t"
"v_mfma_f32_16x16x32_bf16 v[188:191], v[240:243], a[192:195], v[188:191]\n\t"
"ds_read_b128 a[192:195], v222 offset:40960\n\t"
"v_fma_f32 v54, v154, s52, v54\n\t"
"v_mfma_f32_16x16x32_bf16 v[192:195], v[240:243], a[196:199], v[192:195]\n\t"
"s_waitcnt vmcnt(10) lgkmcnt(4)\n\t"
"ds_read_b128 a[196:199], v223 offset:47104\n\t"
"v_fma_f32 v55, v155, s52, v55\n\t"
"v_mfma_f32_16x16x32_bf16 v[192:195], v[248:251], a[200:203], v[192:195]\n\t"
"ds_read_b128 a[200:203], v223 offset:53248\n\t"
"v_fma_f32 v152, v52, s54, -v152\n\t"
"v_mfma_f32_16x16x32_bf16 v[192:195], v[96:99], a[204:207], v[192:195]\n\t"
"ds_read_b128 a[204:207], v222 offset:60928\n\t"
"v_fma_f32 v153, v53, s54, -v153\n\t"
"Lst30_%=:\n\t"
"v_mfma_f32_16x16x32_bf16 v[196:199], a[232:235], a[168:171], v[196:199]\n\t"
"s_waitcnt lgkmcnt(6)\n\t"
"v_fma_f32 v154, v54, s54, -v154\n\t"
"v_fma_f32 v155, v55, s54, -v155\n\t"
"v_mfma_f32_16x16x32_bf16 v[196:199], a[236:239], a[176:179], v[196:199]\n\t"
"v_fma_f32 v56, v156, s52, v56\n\t"
"v_fma_f32 v57, v157, s52, v57\n\t"
"v_mfma_f32_16x16x32_bf16 v[196:199], a[244:247], a[172:175], v[196:199]\n\t"
"buffer_load_dwordx4 v[240:243], off, s[80:83], s20 offset:1024 sc1\n\t"
"v_mfma_f32_16x16x32_bf16 v[204:207], a[232:235], a[172:175], v[204:207]\n\t"
"buffer_load_dwordx4 v[96:99], off, s[80:83], s20 offset:2048 sc1\n\t"
"v_mfma_f32_16x16x32_bf16 v[204:207], a[244:247], a[176:179], v[204:207]\n\t"
"s_waitcnt lgkmcnt(5)\n\t"
"s_mov_b32 s20, 1351680\n\t"
"v_fma_f32 v58, v158, s52, v58\n\t"
"v_mfma_f32_16x16x32_bf16 v[204:207], a[236:239], a[180:183], v[204:207]\n\t"
"v_fma_f32 v59, v159, s52, v59\n\t"
"v_fma_f32 v156, v56, s54, -v156\n\t"
"v_mfma_f32_16x16x32_bf16 v[212:215], a[236:239], a[184:187], v[212:215]\n\t"
"s_waitcnt lgkmcnt(4)\n\t"
"v_fma_f32 v157, v57, s54, -v157\n\t"
"v_fma_f32 v158, v58, s54, -v158\n\t"
"v_mfma_f32_16x16x32_bf16 v[212:215], a[244:247], a[180:183], v[212:215]\n\t"
"v_fma_f32 v159, v59, s54, -v159\n\t"
"v_fma_f32 v60, v160, s52, v60\n\t"
"v_mfma_f32_16x16x32_bf16 v[212:215], a[232:235], a[176:179], v[212:215]\n\t"
"buffer_load_dwordx4 v[248:251], off, s[80:83], s20 offset:0 sc1\n\t"
"v_mfma_f32_16x16x32_bf16 a[0:3], a[232:235], a[180:183], a[0:3]\n\t"
"s_waitcnt lgkmcnt(3)\n\t"
"v_fma_f32 v61, v161, s52, v61\n\t"
"v_fma_f32 v62, v162, s52, v62\n\t"
"v_mfma_f32_16x16x32_bf16 a[0:3], a[244:247], a[184:187], a[0:3]\n\t"
"v_fma_f32 v63, v163, s52, v63\n\t"
"v_fma_f32 v160, v60, s54, -v160\n\t"
"v_mfma_f32_16x16x32_bf16 a[0:3], a[236:239], a[188:191], a[0:3]\n\t"
"v_fma_f32 v161, v61, s54, -v161\n\t"
"v_fma_f32 v162, v62, s54, -v162\n\t"
"v_mfma_f32_16x16x32_bf16 a[8:11], a[236:239], a[192:195], a[8:11]\n\t"
"s_waitcnt lgkmcnt(2)\n\t"
"v_fma_f32 v163, v63, s54, -v163\n\t"
"v_fma_f32 v64, v164, s52, v64\n\t"
"v_mfma_f32_16x16x32_bf16 a[8:11], a[244:247], a[188:191], a[8:11]\n\t"
"v_fma_f32 v65, v165, s52, v65\n\t"
"v_fma_f32 v66, v166, s52, v66\n\t"
"v_mfma_f32_16x16x32_bf16 a[8:11], a[232:235], a[184:187], a[8:11]\n\t"
"v_fma_f32 v67, v167, s52, v67\n\t"
"v_fma_f32 v164, v64, s54, -v164\n\t"
"v_mfma_f32_16x16x32_bf16 a[16:19], a[232:235], a[188:191], a[16:19]\n\t"
"s_waitcnt lgkmcnt(1)\n\t"
"v_fma_f32 v165, v65, s54, -v165\n\t"
"v_fma_f32 v166, v66, s54, -v166\n\t"
"v_mfma_f32_16x16x32_bf16 a[16:19], a[244:247], a[192:195], a[16:19]\n\t"
"v_fma_f32 v167, v67, s54, -v167\n\t"
"v_fma_f32 v68, v168, s52, v68\n\t"
"v_mfma_f32_16x16x32_bf16 a[16:19], a[236:239], a[196:199], a[16:19]\n\t"
"v_fma_f32 v69, v169, s52, v69\n\t"
"v_fma_f32 v70, v170, s52, v70\n\t"
"v_mfma_f32_16x16x32_bf16 a[24:27], a[236:239], a[200:203], a[24:27]\n\t"
"s_waitcnt lgkmcnt(0)\n\t"
"v_fma_f32 v71, v171, s52, v71\n\t"
"v_fma_f32 v168, v68, s54, -v168\n\t"
"v_mfma_f32_16x16x32_bf16 a[24:27], a[244:247], a[196:199], a[24:27]\n\t"
"v_fma_f32 v169, v69, s54, -v169\n\t"
"v_fma_f32 v170, v70, s54, -v170\n\t"
"v_mfma_f32_16x16x32_bf16 a[24:27], a[232:235], a[192:195], a[24:27]\n\t"
"v_fma_f32 v171, v71, s54, -v171\n\t"
"v_fma_f32 v72, v172, s52, v72\n\t"
"v_mfma_f32_16x16x32_bf16 a[32:35], a[232:235], a[196:199], a[32:35]\n\t"
"s_waitcnt vmcnt(10)\n\t"
"v_fma_f32 v73, v173, s52, v73\n\t"
"v_fma_f32 v74, v174, s52, v74\n\t"
"v_mfma_f32_16x16x32_bf16 a[32:35], a[244:247], a[200:203], a[32:35]\n\t"
"v_fma_f32 v75, v175, s52, v75\n\t"
"v_fma_f32 v172, v72, s54, -v172\n\t"
"v_mfma_f32_16x16x32_bf16 a[32:35], a[236:239], a[204:207], a[32:35]\n\t"
"v_fma_f32 v173, v73, s54, -v173\n\t"
"v_fma_f32 v174, v74, s54, -v174\n\t"
"v_mfma_f32_16x16x32_bf16 a[40:43], a[240:243], a[168:171], a[40:43]\n\t"
"v_fma_f32 v175, v75, s54, -v175\n\t"
"v_fma_f32 v76, v176, s52, v76\n\t"
"v_mfma_f32_16x16x32_bf16 a[40:43], a[212:215], a[176:179], a[40:43]\n\t"
"v_fma_f32 v77, v177, s52, v77\n\t"
"v_fma_f32 v78, v178, s52, v78\n\t"
"v_mfma_f32_16x16x32_bf16 a[40:43], a[248:251], a[172:175], a[40:43]\n\t"
"buffer_load_dwordx4 a[232:235], off, s[80:83], s20 offset:1024 sc1\n\t"
"v_mfma_f32_16x16x32_bf16 a[48:51], a[240:243], a[172:175], a[48:51]\n\t"
"buffer_load_dwordx4 a[236:239], off, s[80:83], s20 offset:2048 sc1\n\t"
"v_mfma_f32_16x16x32_bf16 a[48:51], a[248:251], a[176:179], a[48:51]\n\t"
"s_mov_b32 s20, 1585152\n\t"
"v_fma_f32 v79, v179, s52, v79\n\t"
"v_fma_f32 v176, v76, s54, -v176\n\t"
"v_mfma_f32_16x16x32_bf16 a[48:51], a[212:215], a[180:183], a[48:51]\n\t"
"v_fma_f32 v177, v77, s54, -v177\n\t"
"v_fma_f32 v178, v78, s54, -v178\n\t"
"v_mfma_f32_16x16x32_bf16 a[56:59], a[212:215], a[184:187], a[56:59]\n\t"
"v_fma_f32 v179, v79, s54, -v179\n\t"
"v_fma_f32 v80, v180, s52, v80\n\t"
"v_mfma_f32_16x16x32_bf16 a[56:59], a[248:251], a[180:183], a[56:59]\n\t"
"v_fma_f32 v81, v181, s52, v81\n\t"
"v_fma_f32 v82, v182, s52, v82\n\t"
"v_mfma_f32_16x16x32_bf16 a[56:59], a[240:243], a[176:179], a[56:59]\n\t"
"buffer_load_dwordx4 a[244:247], off, s[80:83], s20 offset:0 sc1\n\t"
"v_mfma_f32_16x16x32_bf16 a[64:67], a[240:243], a[180:183], a[64:67]\n\t"
"v_fma_f32 v83, v183, s52, v83\n\t"
"v_fma_f32 v180, v80, s54, -v180\n\t"
"v_mfma_f32_16x16x32_bf16 a[64:67], a[248:251], a[184:187], a[64:67]\n\t"
"v_fma_f32 v181, v81, s54, -v181\n\t"
"v_fma_f32 v182, v82, s54, -v182\n\t"
"v_mfma_f32_16x16x32_bf16 a[64:67], a[212:215], a[188:191], a[64:67]\n\t"
"v_fma_f32 v183, v83, s54, -v183\n\t"
"v_fma_f32 v84, v184, s52, v84\n\t"
"v_mfma_f32_16x16x32_bf16 a[72:75], a[212:215], a[192:195], a[72:75]\n\t"
"v_fma_f32 v85, v185, s52, v85\n\t"
"v_fma_f32 v86, v186, s52, v86\n\t"
"v_mfma_f32_16x16x32_bf16 a[72:75], a[248:251], a[188:191], a[72:75]\n\t"
"v_fma_f32 v87, v187, s52, v87\n\t"
"v_fma_f32 v184, v84, s54, -v184\n\t"
"v_mfma_f32_16x16x32_bf16 a[72:75], a[240:243], a[184:187], a[72:75]\n\t"
"v_fma_f32 v185, v85, s54, -v185\n\t"
"v_fma_f32 v186, v86, s54, -v186\n\t"
"v_mfma_f32_16x16x32_bf16 a[80:83], a[240:243], a[188:191], a[80:83]\n\t"
"v_fma_f32 v187, v87, s54, -v187\n\t"
"v_fma_f32 v88, v188, s52, v88\n\t"
"v_mfma_f32_16x16x32_bf16 a[80:83], a[248:251], a[192:195], a[80:83]\n\t"
"v_fma_f32 v89, v189, s52, v89\n\t"
"v_fma_f32 v90, v190, s52, v90\n\t"
"v_mfma_f32_16x16x32_bf16 a[80:83], a[212:215], a[196:199], a[80:83]\n\t"
"v_fma_f32 v91, v191, s52, v91\n\t"
"v_fma_f32 v188, v88, s54, -v188\n\t"
"v_mfma_f32_16x16x32_bf16 a[88:91], a[212:215], a[200:203], a[88:91]\n\t"
"v_fma_f32 v189, v89, s54, -v189\n\t"
"v_fma_f32 v190, v90, s54, -v190\n\t"
"v_mfma_f32_16x16x32_bf16 a[88:91], a[248:251], a[196:199], a[88:91]\n\t"
"v_fma_f32 v191, v91, s54, -v191\n\t"
"v_fma_f32 v92, v192, s52, v92\n\t"
"v_mfma_f32_16x16x32_bf16 a[88:91], a[240:243], a[192:195], a[88:91]\n\t"
"v_fma_f32 v93, v193, s52, v93\n\t"
"v_fma_f32 v94, v194, s52, v94\n\t"
"v_mfma_f32_16x16x32_bf16 a[96:99], a[240:243], a[196:199], a[96:99]\n\t"
"s_waitcnt vmcnt(10)\n\t"
"v_fma_f32 v95, v195, s52, v95\n\t"
"v_fma_f32 v192, v92, s54, -v192\n\t"
"v_mfma_f32_16x16x32_bf16 a[96:99], a[248:251], a[200:203], a[96:99]\n\t"
"v_fma_f32 v193, v93, s54, -v193\n\t"
"v_fma_f32 v194, v94, s54, -v194\n\t"
"v_mfma_f32_16x16x32_bf16 a[96:99], a[212:215], a[204:207], a[96:99]\n\t"
"v_fma_f32 v195, v95, s54, -v195\n\t"
"v_mfma_f32_16x16x32_bf16 a[104:107], a[216:219], a[168:171], a[104:107]\n\t"
"buffer_load_dwordx4 a[240:243], off, s[80:83], s20 offset:1024 sc1\n\t"
"v_mfma_f32_16x16x32_bf16 a[104:107], a[220:223], a[176:179], a[104:107]\n\t"
"buffer_load_dwordx4 a[212:215], off, s[80:83], s20 offset:2048 sc1\n\t"
"v_mfma_f32_16x16x32_bf16 a[104:107], v[236:239], a[172:175], a[104:107]\n\t"
"s_mov_b32 s20, 1597440\n\t"
"ds_read_b128 a[168:171], v222 offset:1280\n\t"
"v_mfma_f32_16x16x32_bf16 a[112:115], a[216:219], a[172:175], a[112:115]\n\t"
"buffer_load_dwordx4 a[248:251], off, s[80:83], s20 offset:0 sc1\n\t"
"v_mfma_f32_16x16x32_bf16 a[112:115], v[236:239], a[176:179], a[112:115]\n\t"
"ds_read_b128 a[172:175], v223 offset:7424\n\t"
"v_mfma_f32_16x16x32_bf16 a[112:115], a[220:223], a[180:183], a[112:115]\n\t"
"v_mfma_f32_16x16x32_bf16 a[120:123], a[220:223], a[184:187], a[120:123]\n\t"
"v_mfma_f32_16x16x32_bf16 a[120:123], v[236:239], a[180:183], a[120:123]\n\t"
"v_mfma_f32_16x16x32_bf16 a[120:123], a[216:219], a[176:179], a[120:123]\n\t"
"ds_read_b128 a[176:179], v223 offset:13568\n\t"
"v_mfma_f32_16x16x32_bf16 a[128:131], a[216:219], a[180:183], a[128:131]\n\t"
"ds_read_b128 a[180:183], v222 offset:21248\n\t"
"v_mfma_f32_16x16x32_bf16 a[128:131], v[236:239], a[184:187], a[128:131]\n\t"
"v_mfma_f32_16x16x32_bf16 a[128:131], a[220:223], a[188:191], a[128:131]\n\t"
"v_mfma_f32_16x16x32_bf16 a[136:139], a[220:223], a[192:195], a[136:139]\n\t"
"v_mfma_f32_16x16x32_bf16 a[136:139], v[236:239], a[188:191], a[136:139]\n\t"
"v_mfma_f32_16x16x32_bf16 a[136:139], a[216:219], a[184:187], a[136:139]\n\t"
"ds_read_b128 a[184:187], v223 offset:27392\n\t"
"v_mfma_f32_16x16x32_bf16 a[144:147], a[216:219], a[188:191], a[144:147]\n\t"
"ds_read_b128 a[188:191], v223 offset:33536\n\t"
"v_mfma_f32_16x16x32_bf16 a[144:147], v[236:239], a[192:195], a[144:147]\n\t"
"v_mfma_f32_16x16x32_bf16 a[144:147], a[220:223], a[196:199], a[144:147]\n\t"
"v_mfma_f32_16x16x32_bf16 a[152:155], a[220:223], a[200:203], a[152:155]\n\t"
"v_mfma_f32_16x16x32_bf16 a[152:155], v[236:239], a[196:199], a[152:155]\n\t"
"v_mfma_f32_16x16x32_bf16 a[152:155], a[216:219], a[192:195], a[152:155]\n\t"
"ds_read_b128 a[192:195], v222 offset:41216\n\t"
"v_mfma_f32_16x16x32_bf16 a[160:163], a[216:219], a[196:199], a[160:163]\n\t"
"buffer_load_dwordx4 a[216:219], off, s[80:83], s20 offset:1024 sc1\n\t"
"v_mfma_f32_16x16x32_bf16 a[160:163], v[236:239], a[200:203], a[160:163]\n\t"
"s_waitcnt vmcnt(11) lgkmcnt(4)\n\t"
"ds_read_b128 a[196:199], v223 offset:47360\n\t"
"v_mfma_f32_16x16x32_bf16 a[160:163], a[220:223], a[204:207], a[160:163]\n\t"
"ds_read_b128 a[200:203], v223 offset:53504\n\t"
"v_mfma_f32_16x16x32_bf16 v[200:203], a[224:227], a[168:171], v[200:203]\n\t"
"buffer_load_dwordx4 v[236:239], off, s[80:83], s20 offset:2048 sc1\n\t"
"v_mfma_f32_16x16x32_bf16 v[200:203], a[208:211], a[176:179], v[200:203]\n\t"
"s_waitcnt lgkmcnt(5)\n\t"
"ds_read_b128 a[204:207], v222 offset:61184\n\t"
"s_mov_b32 s20, 1609728\n\t"
"v_mfma_f32_16x16x32_bf16 v[200:203], a[252:255], a[172:175], v[200:203]\n\t"
"v_mfma_f32_16x16x32_bf16 v[208:211], a[224:227], a[172:175], v[208:211]\n\t"
"buffer_load_dwordx4 a[220:223], off, s[80:83], s20 offset:0 sc1\n\t"
"v_mfma_f32_16x16x32_bf16 v[208:211], a[252:255], a[176:179], v[208:211]\n\t"
"s_waitcnt lgkmcnt(5)\n\t"
"v_mfma_f32_16x16x32_bf16 v[208:211], a[208:211], a[180:183], v[208:211]\n\t"
"v_mfma_f32_16x16x32_bf16 v[216:219], a[208:211], a[184:187], v[216:219]\n\t"
"s_waitcnt lgkmcnt(4)\n\t"
"v_mfma_f32_16x16x32_bf16 v[216:219], a[252:255], a[180:183], v[216:219]\n\t"
"v_mfma_f32_16x16x32_bf16 v[216:219], a[224:227], a[176:179], v[216:219]\n\t"
"v_mfma_f32_16x16x32_bf16 a[4:7], a[224:227], a[180:183], a[4:7]\n\t"
"s_waitcnt lgkmcnt(3)\n\t"
"v_mfma_f32_16x16x32_bf16 a[4:7], a[252:255], a[184:187], a[4:7]\n\t"
"v_mfma_f32_16x16x32_bf16 a[4:7], a[208:211], a[188:191], a[4:7]\n\t"
"v_mfma_f32_16x16x32_bf16 a[12:15], a[208:211], a[192:195], a[12:15]\n\t"
"s_waitcnt lgkmcnt(2)\n\t"
"v_mfma_f32_16x16x32_bf16 a[12:15], a[252:255], a[188:191], a[12:15]\n\t"
"v_mfma_f32_16x16x32_bf16 a[12:15], a[224:227], a[184:187], a[12:15]\n\t"
"v_mfma_f32_16x16x32_bf16 a[20:23], a[224:227], a[188:191], a[20:23]\n\t"
"s_waitcnt lgkmcnt(1)\n\t"
"v_mfma_f32_16x16x32_bf16 a[20:23], a[252:255], a[192:195], a[20:23]\n\t"
"v_mfma_f32_16x16x32_bf16 a[20:23], a[208:211], a[196:199], a[20:23]\n\t"
"v_mfma_f32_16x16x32_bf16 a[28:31], a[208:211], a[200:203], a[28:31]\n\t"
"s_waitcnt lgkmcnt(0)\n\t"
"v_mfma_f32_16x16x32_bf16 a[28:31], a[252:255], a[196:199], a[28:31]\n\t"
"v_mfma_f32_16x16x32_bf16 a[28:31], a[224:227], a[192:195], a[28:31]\n\t"
"v_mfma_f32_16x16x32_bf16 a[36:39], a[224:227], a[196:199], a[36:39]\n\t"
"buffer_load_dwordx4 a[224:227], off, s[80:83], s20 offset:1024 sc1\n\t"
"v_mfma_f32_16x16x32_bf16 a[36:39], a[252:255], a[200:203], a[36:39]\n\t"
"s_waitcnt vmcnt(11)\n\t"
"v_mfma_f32_16x16x32_bf16 a[36:39], a[208:211], a[204:207], a[36:39]\n\t"
"v_mfma_f32_16x16x32_bf16 a[44:47], a[228:231], a[168:171], a[44:47]\n\t"
"buffer_load_dwordx4 a[208:211], off, s[80:83], s20 offset:2048 sc1\n\t"
"v_mfma_f32_16x16x32_bf16 a[44:47], v[96:99], a[176:179], a[44:47]\n\t"
"s_mov_b32 s20, 1622016\n\t"
"v_mfma_f32_16x16x32_bf16 a[44:47], v[240:243], a[172:175], a[44:47]\n\t"
"v_mfma_f32_16x16x32_bf16 a[52:55], a[228:231], a[172:175], a[52:55]\n\t"
"buffer_load_dwordx4 a[252:255], off, s[80:83], s20 offset:0 sc1\n\t"
"v_mfma_f32_16x16x32_bf16 a[52:55], v[240:243], a[176:179], a[52:55]\n\t"
"v_mfma_f32_16x16x32_bf16 a[52:55], v[96:99], a[180:183], a[52:55]\n\t"
"v_mfma_f32_16x16x32_bf16 a[60:63], v[96:99], a[184:187], a[60:63]\n\t"
"v_mfma_f32_16x16x32_bf16 a[60:63], v[240:243], a[180:183], a[60:63]\n\t"
"v_mfma_f32_16x16x32_bf16 a[60:63], a[228:231], a[176:179], a[60:63]\n\t"
"v_mfma_f32_16x16x32_bf16 a[68:71], a[228:231], a[180:183], a[68:71]\n\t"
"v_mfma_f32_16x16x32_bf16 a[68:71], v[240:243], a[184:187], a[68:71]\n\t"
"v_mfma_f32_16x16x32_bf16 a[68:71], v[96:99], a[188:191], a[68:71]\n\t"
"v_mfma_f32_16x16x32_bf16 a[76:79], v[96:99], a[192:195], a[76:79]\n\t"
"v_mfma_f32_16x16x32_bf16 a[76:79], v[240:243], a[188:191], a[76:79]\n\t"
"v_mfma_f32_16x16x32_bf16 a[76:79], a[228:231], a[184:187], a[76:79]\n\t"
"v_mfma_f32_16x16x32_bf16 a[84:87], a[228:231], a[188:191], a[84:87]\n\t"
"v_mfma_f32_16x16x32_bf16 a[84:87], v[240:243], a[192:195], a[84:87]\n\t"
"v_mfma_f32_16x16x32_bf16 a[84:87], v[96:99], a[196:199], a[84:87]\n\t"
"v_mfma_f32_16x16x32_bf16 a[92:95], v[96:99], a[200:203], a[92:95]\n\t"
"v_mfma_f32_16x16x32_bf16 a[92:95], v[240:243], a[196:199], a[92:95]\n\t"
"v_mfma_f32_16x16x32_bf16 a[92:95], a[228:231], a[192:195], a[92:95]\n\t"
"v_mfma_f32_16x16x32_bf16 a[100:103], a[228:231], a[196:199], a[100:103]\n\t"
"buffer_load_dwordx4 a[228:231], off, s[80:83], s20 offset:1024 sc1\n\t"
"v_mfma_f32_16x16x32_bf16 a[100:103], v[240:243], a[200:203], a[100:103]\n\t"
"s_waitcnt vmcnt(11)\n\t"
"v_mfma_f32_16x16x32_bf16 a[100:103], v[96:99], a[204:207], a[100:103]\n\t"
"v_mfma_f32_16x16x32_bf16 a[108:111], v[248:251], a[168:171], a[108:111]\n\t"
"buffer_load_dwordx4 v[240:243], off, s[80:83], s20 offset:2048 sc1\n\t"
"v_mfma_f32_16x16x32_bf16 a[108:111], a[236:239], a[176:179], a[108:111]\n\t"
"ds_read_b128 a[168:171], v222 offset:1536\n\t"
"v_mfma_f32_16x16x32_bf16 a[108:111], a[232:235], a[172:175], a[108:111]\n\t"
"v_mfma_f32_16x16x32_bf16 a[116:119], v[248:251], a[172:175], a[116:119]\n\t"
"ds_read_b128 a[172:175], v223 offset:7680\n\t"
"v_mfma_f32_16x16x32_bf16 a[116:119], a[232:235], a[176:179], a[116:119]\n\t"
"v_mfma_f32_16x16x32_bf16 a[116:119], a[236:239], a[180:183], a[116:119]\n\t"
"v_mfma_f32_16x16x32_bf16 a[124:127], a[236:239], a[184:187], a[124:127]\n\t"
"v_mfma_f32_16x16x32_bf16 a[124:127], a[232:235], a[180:183], a[124:127]\n\t"
"v_mfma_f32_16x16x32_bf16 a[124:127], v[248:251], a[176:179], a[124:127]\n\t"
"ds_read_b128 a[176:179], v223 offset:13824\n\t"
"v_mfma_f32_16x16x32_bf16 a[132:135], v[248:251], a[180:183], a[132:135]\n\t"
"ds_read_b128 a[180:183], v222 offset:21504\n\t"
"v_mfma_f32_16x16x32_bf16 a[132:135], a[232:235], a[184:187], a[132:135]\n\t"
"v_mfma_f32_16x16x32_bf16 a[132:135], a[236:239], a[188:191], a[132:135]\n\t"
"v_mfma_f32_16x16x32_bf16 a[140:143], a[236:239], a[192:195], a[140:143]\n\t"
"v_mfma_f32_16x16x32_bf16 a[140:143], a[232:235], a[188:191], a[140:143]\n\t"
"v_mfma_f32_16x16x32_bf16 a[140:143], v[248:251], a[184:187], a[140:143]\n\t"
"ds_read_b128 a[184:187], v223 offset:27648\n\t"
"v_mfma_f32_16x16x32_bf16 a[148:151], v[248:251], a[188:191], a[148:151]\n\t"
"s_mov_b32 s20, 1634304\n\t"
"ds_read_b128 a[188:191], v223 offset:33792\n\t"
"v_mfma_f32_16x16x32_bf16 a[148:151], a[232:235], a[192:195], a[148:151]\n\t"
"v_mfma_f32_16x16x32_bf16 a[148:151], a[236:239], a[196:199], a[148:151]\n\t"
"v_mfma_f32_16x16x32_bf16 a[156:159], a[236:239], a[200:203], a[156:159]\n\t"
"v_mfma_f32_16x16x32_bf16 a[156:159], a[232:235], a[196:199], a[156:159]\n\t"
"v_mfma_f32_16x16x32_bf16 a[156:159], v[248:251], a[192:195], a[156:159]\n\t"
"ds_read_b128 a[192:195], v222 offset:41472\n\t"
"v_mfma_f32_16x16x32_bf16 a[164:167], v[248:251], a[196:199], a[164:167]\n\t"
"buffer_load_dwordx4 v[248:251], off, s[80:83], s20 offset:0 sc1\n\t"
"v_mfma_f32_16x16x32_bf16 a[164:167], a[232:235], a[200:203], a[164:167]\n\t"
"s_waitcnt vmcnt(10) lgkmcnt(4)\n\t"
"ds_read_b128 a[196:199], v223 offset:47616\n\t"
"v_mfma_f32_16x16x32_bf16 a[164:167], a[236:239], a[204:207], a[164:167]\n\t"
"ds_read_b128 a[200:203], v223 offset:53760\n\t"
"Lst31_%=:\n\t"
"v_mfma_f32_16x16x32_bf16 v[196:199], a[244:247], a[168:171], v[196:199]\n\t"
"buffer_load_dwordx4 a[232:235], off, s[80:83], s20 offset:1024 sc1\n\t"
"v_mfma_f32_16x16x32_bf16 v[196:199], a[212:215], a[176:179], v[196:199]\n\t"
"s_waitcnt lgkmcnt(5)\n\t"
"ds_read_b128 a[204:207], v222 offset:61440\n\t"
"v_mfma_f32_16x16x32_bf16 v[196:199], a[240:243], a[172:175], v[196:199]\n\t"
"v_mfma_f32_16x16x32_bf16 v[204:207], a[244:247], a[172:175], v[204:207]\n\t"
"buffer_load_dwordx4 a[236:239], off, s[80:83], s20 offset:2048 sc1\n\t"
"v_mfma_f32_16x16x32_bf16 v[204:207], a[240:243], a[176:179], v[204:207]\n\t"
"s_waitcnt lgkmcnt(5)\n\t"
"v_mfma_f32_16x16x32_bf16 v[204:207], a[212:215], a[180:183], v[204:207]\n\t"
"v_mfma_f32_16x16x32_bf16 v[212:215], a[212:215], a[184:187], v[212:215]\n\t"
"s_waitcnt lgkmcnt(4)\n\t"
"v_mfma_f32_16x16x32_bf16 v[212:215], a[240:243], a[180:183], v[212:215]\n\t"
"v_mfma_f32_16x16x32_bf16 v[212:215], a[244:247], a[176:179], v[212:215]\n\t"
"v_mfma_f32_16x16x32_bf16 a[0:3], a[244:247], a[180:183], a[0:3]\n\t"
"s_waitcnt lgkmcnt(3)\n\t"
"v_mfma_f32_16x16x32_bf16 a[0:3], a[240:243], a[184:187], a[0:3]\n\t"
"v_mfma_f32_16x16x32_bf16 a[0:3], a[212:215], a[188:191], a[0:3]\n\t"
"v_mfma_f32_16x16x32_bf16 a[8:11], a[212:215], a[192:195], a[8:11]\n\t"
"s_waitcnt lgkmcnt(2)\n\t"
"v_mfma_f32_16x16x32_bf16 a[8:11], a[240:243], a[188:191], a[8:11]\n\t"
"v_mfma_f32_16x16x32_bf16 a[8:11], a[244:247], a[184:187], a[8:11]\n\t"
"v_mfma_f32_16x16x32_bf16 a[16:19], a[244:247], a[188:191], a[16:19]\n\t"
"s_waitcnt lgkmcnt(1)\n\t"
"s_mov_b32 s20, 1646592\n\t"
"v_mfma_f32_16x16x32_bf16 a[16:19], a[240:243], a[192:195], a[16:19]\n\t"
"v_mfma_f32_16x16x32_bf16 a[16:19], a[212:215], a[196:199], a[16:19]\n\t"
"v_mfma_f32_16x16x32_bf16 a[24:27], a[212:215], a[200:203], a[24:27]\n\t"
"s_waitcnt lgkmcnt(0)\n\t"
"v_mfma_f32_16x16x32_bf16 a[24:27], a[240:243], a[196:199], a[24:27]\n\t"
"v_mfma_f32_16x16x32_bf16 a[24:27], a[244:247], a[192:195], a[24:27]\n\t"
"v_mfma_f32_16x16x32_bf16 a[32:35], a[244:247], a[196:199], a[32:35]\n\t"
"buffer_load_dwordx4 a[244:247], off, s[80:83], s20 offset:0 sc1\n\t"
"v_mfma_f32_16x16x32_bf16 a[32:35], a[240:243], a[200:203], a[32:35]\n\t"
"s_waitcnt vmcnt(10)\n\t"
"v_mfma_f32_16x16x32_bf16 a[32:35], a[212:215], a[204:207], a[32:35]\n\t"
"v_mfma_f32_16x16x32_bf16 a[40:43], a[248:251], a[168:171], a[40:43]\n\t"
"buffer_load_dwordx4 a[212:215], off, s[80:83], s20 offset:1024 sc1\n\t"
"v_mfma_f32_16x16x32_bf16 a[40:43], v[236:239], a[176:179], a[40:43]\n\t"
"v_mfma_f32_16x16x32_bf16 a[40:43], a[216:219], a[172:175], a[40:43]\n\t"
"v_mfma_f32_16x16x32_bf16 a[48:51], a[248:251], a[172:175], a[48:51]\n\t"
"buffer_load_dwordx4 a[240:243], off, s[80:83], s20 offset:2048 sc1\n\t"
"v_mfma_f32_16x16x32_bf16 a[48:51], a[216:219], a[176:179], a[48:51]\n\t"
"v_mfma_f32_16x16x32_bf16 a[48:51], v[236:239], a[180:183], a[48:51]\n\t"
"v_mfma_f32_16x16x32_bf16 a[56:59], v[236:239], a[184:187], a[56:59]\n\t"
"v_mfma_f32_16x16x32_bf16 a[56:59], a[216:219], a[180:183], a[56:59]\n\t"
"v_mfma_f32_16x16x32_bf16 a[56:59], a[248:251], a[176:179], a[56:59]\n\t"
"v_mfma_f32_16x16x32_bf16 a[64:67], a[248:251], a[180:183], a[64:67]\n\t"
"v_mfma_f32_16x16x32_bf16 a[64:67], a[216:219], a[184:187], a[64:67]\n\t"
"v_mfma_f32_16x16x32_bf16 a[64:67], v[236:239], a[188:191], a[64:67]\n\t"
"v_mfma_f32_16x16x32_bf16 a[72:75], v[236:239], a[192:195], a[72:75]\n\t"
"v_mfma_f32_16x16x32_bf16 a[72:75], a[216:219], a[188:191], a[72:75]\n\t"
"v_mfma_f32_16x16x32_bf16 a[72:75], a[248:251], a[184:187], a[72:75]\n\t"
"s_waitcnt vmcnt(9)\n\t"
"v_mfma_f32_16x16x32_bf16 a[80:83], a[248:251], a[188:191], a[80:83]\n\t"
"s_mov_b32 s20, 884736\n\t"
"v_mfma_f32_16x16x32_bf16 a[80:83], a[216:219], a[192:195], a[80:83]\n\t"
"Lpg22_%=:\n\t"
"s_cbranch_vccnz Lvr3_%=\n\t"
"v_mfma_f32_16x16x32_bf16 a[80:83], v[236:239], a[196:199], a[80:83]\n\t"
"v_mfma_f32_16x16x32_bf16 a[88:91], v[236:239], a[200:203], a[88:91]\n\t"
"v_mfma_f32_16x16x32_bf16 a[88:91], a[216:219], a[196:199], a[88:91]\n\t"
"v_mfma_f32_16x16x32_bf16 a[88:91], a[248:251], a[192:195], a[88:91]\n\t"
"v_mfma_f32_16x16x32_bf16 a[96:99], a[248:251], a[196:199], a[96:99]\n\t"
"buffer_load_dwordx4 a[248:251], off, s[80:83], s20 offset:0 sc1\n\t"
"v_mfma_f32_16x16x32_bf16 a[96:99], a[216:219], a[200:203], a[96:99]\n\t"
"v_mfma_f32_16x16x32_bf16 a[96:99], v[236:239], a[204:207], a[96:99]\n\t"
"v_mfma_f32_16x16x32_bf16 a[104:107], a[220:223], a[168:171], a[104:107]\n\t"
"buffer_load_dwordx4 a[216:219], off, s[80:83], s20 offset:1024 sc1\n\t"
"v_mfma_f32_16x16x32_bf16 a[104:107], a[208:211], a[176:179], a[104:107]\n\t"
"ds_read_b128 a[168:171], v222 offset:1792\n\t"
"v_mfma_f32_16x16x32_bf16 a[104:107], a[224:227], a[172:175], a[104:107]\n\t"
"v_mfma_f32_16x16x32_bf16 a[112:115], a[220:223], a[172:175], a[112:115]\n\t"
"ds_read_b128 a[172:175], v223 offset:7936\n\t"
"v_mfma_f32_16x16x32_bf16 a[112:115], a[224:227], a[176:179], a[112:115]\n\t"
"v_mfma_f32_16x16x32_bf16 a[112:115], a[208:211], a[180:183], a[112:115]\n\t"
"v_mfma_f32_16x16x32_bf16 a[120:123], a[208:211], a[184:187], a[120:123]\n\t"
"v_mfma_f32_16x16x32_bf16 a[120:123], a[224:227], a[180:183], a[120:123]\n\t"
"v_mfma_f32_16x16x32_bf16 a[120:123], a[220:223], a[176:179], a[120:123]\n\t"
"ds_read_b128 a[176:179], v223 offset:14080\n\t"
"v_mfma_f32_16x16x32_bf16 a[128:131], a[220:223], a[180:183], a[128:131]\n\t"
"ds_read_b128 a[180:183], v222 offset:21760\n\t"
"v_mfma_f32_16x16x32_bf16 a[128:131], a[224:227], a[184:187], a[128:131]\n\t"
"v_mfma_f32_16x16x32_bf16 a[128:131], a[208:211], a[188:191], a[128:131]\n\t"
"v_mfma_f32_16x16x32_bf16 a[136:139], a[208:211], a[192:195], a[136:139]\n\t"
"v_mfma_f32_16x16x32_bf16 a[136:139], a[224:227], a[188:191], a[136:139]\n\t"
"v_mfma_f32_16x16x32_bf16 a[136:139], a[220:223], a[184:187], a[136:139]\n\t"
"ds_read_b128 a[184:187], v223 offset:27904\n\t"
"v_mfma_f32_16x16x32_bf16 a[144:147], a[220:223], a[188:191], a[144:147]\n\t"
"ds_read_b128 a[188:191], v223 offset:34048\n\t"
"v_mfma_f32_16x16x32_bf16 a[144:147], a[224:227], a[192:195], a[144:147]\n\t"
"v_mfma_f32_16x16x32_bf16 a[144:147], a[208:211], a[196:199], a[144:147]\n\t"
"v_mfma_f32_16x16x32_bf16 a[152:155], a[208:211], a[200:203], a[152:155]\n\t"
"v_mfma_f32_16x16x32_bf16 a[152:155], a[224:227], a[196:199], a[152:155]\n\t"
"v_mfma_f32_16x16x32_bf16 a[152:155], a[220:223], a[192:195], a[152:155]\n\t"
"ds_read_b128 a[192:195], v222 offset:41728\n\t"
"v_mfma_f32_16x16x32_bf16 a[160:163], a[220:223], a[196:199], a[160:163]\n\t"
"buffer_load_dwordx4 a[220:223], off, s[80:83], s20 offset:2048 sc1\n\t"
"v_mfma_f32_16x16x32_bf16 a[160:163], a[224:227], a[200:203], a[160:163]\n\t"
"s_waitcnt vmcnt(9) lgkmcnt(4)\n\t"
"ds_read_b128 a[196:199], v223 offset:47872\n\t"
"s_mov_b32 s20, 897024\n\t"
"v_mfma_f32_16x16x32_bf16 a[160:163], a[208:211], a[204:207], a[160:163]\n\t"
"ds_read_b128 a[200:203], v223 offset:54016\n\t"
"v_mfma_f32_16x16x32_bf16 v[200:203], a[252:255], a[168:171], v[200:203]\n\t"
"s_waitcnt lgkmcnt(5)\n\t"
"ds_read_b128 a[204:207], v222 offset:61696\n\t"
"v_mfma_f32_16x16x32_bf16 v[200:203], v[240:243], a[176:179], v[200:203]\n\t"
"buffer_load_dwordx4 a[208:211], off, s[80:83], s20 offset:0 sc1\n\t"
"v_mfma_f32_16x16x32_bf16 v[200:203], a[228:231], a[172:175], v[200:203]\n\t"
"v_mfma_f32_16x16x32_bf16 v[208:211], a[252:255], a[172:175], v[208:211]\n\t"
"buffer_load_dwordx4 a[224:227], off, s[80:83], s20 offset:1024 sc1\n\t"
"v_mfma_f32_16x16x32_bf16 v[208:211], a[228:231], a[176:179], v[208:211]\n\t"
"s_waitcnt lgkmcnt(5)\n\t"
"v_mfma_f32_16x16x32_bf16 v[208:211], v[240:243], a[180:183], v[208:211]\n\t"
"s_waitcnt lgkmcnt(0)\n\t"
"s_barrier\n\t"
"v_mfma_f32_16x16x32_bf16 v[216:219], v[240:243], a[184:187], v[216:219]\n\t"
"v_mfma_f32_16x16x32_bf16 v[216:219], a[228:231], a[180:183], v[216:219]\n\t"
"v_mfma_f32_16x16x32_bf16 v[216:219], a[252:255], a[176:179], v[216:219]\n\t"
"v_pk_fma_f32 v[196:197], v[200:201], s[56:57], v[196:197]\n\t"
"v_pk_fma_f32 v[200:201], v[196:197], s[58:59], v[200:201] neg_lo:[0,0,1] neg_hi:[0,0,1]\n\t"
"v_pk_fma_f32 v[228:229], v[0:1], s[60:61], v[196:197]\n\t"
"v_pk_fma_f32 v[0:1], v[196:197], s[60:61], v[0:1]\n\t"
"v_pk_fma_f32 v[196:197], v[100:101], s[62:63], v[200:201]\n\t"
"v_pk_fma_f32 v[100:101], v[200:201], s[62:63], v[100:101]\n\t"
"v_pk_fma_f32 v[198:199], v[202:203], s[56:57], v[198:199]\n\t"
"v_pk_fma_f32 v[202:203], v[198:199], s[58:59], v[202:203] neg_lo:[0,0,1] neg_hi:[0,0,1]\n\t"
"v_pk_fma_f32 v[234:235], v[2:3], s[60:61], v[198:199]\n\t"
"v_pk_fma_f32 v[2:3], v[198:199], s[60:61], v[2:3]\n\t"
"v_pk_fma_f32 v[198:199], v[102:103], s[62:63], v[202:203]\n\t"
"v_pk_fma_f32 v[102:103], v[202:203], s[62:63], v[102:103]\n\t"
"v_mfma_f32_16x16x32_bf16 a[4:7], a[252:255], a[180:183], a[4:7]\n\t"
"v_cvt_pk_bf16_f32 v196, v196, v197\n\t"
"v_cvt_pk_bf16_f32 v200, v228, v229\n\t"
"v_mfma_f32_16x16x32_bf16 a[4:7], a[228:231], a[184:187], a[4:7]\n\t"
"v_cvt_pk_bf16_f32 v197, v198, v199\n\t"
"v_cvt_pk_bf16_f32 v201, v234, v235\n\t"
"v_mfma_f32_16x16x32_bf16 a[4:7], v[240:243], a[188:191], a[4:7]\n\t"
"v_pk_fma_f32 v[204:205], v[208:209], s[56:57], v[204:205]\n\t"
"v_pk_fma_f32 v[208:209], v[204:205], s[58:59], v[208:209] neg_lo:[0,0,1] neg_hi:[0,0,1]\n\t"
"v_pk_fma_f32 v[98:99], v[4:5], s[60:61], v[204:205]\n\t"
"v_pk_fma_f32 v[4:5], v[204:205], s[60:61], v[4:5]\n\t"
"v_pk_fma_f32 v[204:205], v[104:105], s[62:63], v[208:209]\n\t"
"v_pk_fma_f32 v[104:105], v[208:209], s[62:63], v[104:105]\n\t"
"v_mfma_f32_16x16x32_bf16 a[12:15], v[240:243], a[192:195], a[12:15]\n\t"
"v_cvt_pk_bf16_f32 v204, v204, v205\n\t"
"v_cvt_pk_bf16_f32 v208, v98, v99\n\t"
"v_mfma_f32_16x16x32_bf16 a[12:15], a[228:231], a[188:191], a[12:15]\n\t"
"v_mfma_f32_16x16x32_bf16 a[12:15], a[252:255], a[184:187], a[12:15]\n\t"
"v_pk_fma_f32 v[206:207], v[210:211], s[56:57], v[206:207]\n\t"
"v_pk_fma_f32 v[210:211], v[206:207], s[58:59], v[210:211] neg_lo:[0,0,1] neg_hi:[0,0,1]\n\t"
"v_pk_fma_f32 v[228:229], v[6:7], s[60:61], v[206:207]\n\t"
"v_pk_fma_f32 v[6:7], v[206:207], s[60:61], v[6:7]\n\t"
"v_pk_fma_f32 v[206:207], v[106:107], s[62:63], v[210:211]\n\t"
"v_pk_fma_f32 v[106:107], v[210:211], s[62:63], v[106:107]\n\t"
"v_mfma_f32_16x16x32_bf16 a[20:23], a[252:255], a[188:191], a[20:23]\n\t"
"v_cvt_pk_bf16_f32 v205, v206, v207\n\t"
"v_cvt_pk_bf16_f32 v209, v228, v229\n\t"
"v_mfma_f32_16x16x32_bf16 a[20:23], a[228:231], a[192:195], a[20:23]\n\t"
"v_accvgpr_read_b32 v224, a0\n\t"
"v_accvgpr_read_b32 v226, a4\n\t"
"v_mfma_f32_16x16x32_bf16 a[20:23], v[240:243], a[196:199], a[20:23]\n\t"
"v_accvgpr_read_b32 v225, a1\n\t"
"v_accvgpr_read_b32 v227, a5\n\t"
"v_pk_fma_f32 v[212:213], v[216:217], s[56:57], v[212:213]\n\t"
"v_pk_fma_f32 v[216:217], v[212:213], s[58:59], v[216:217] neg_lo:[0,0,1] neg_hi:[0,0,1]\n\t"
"v_pk_fma_f32 v[234:235], v[8:9], s[60:61], v[212:213]\n\t"
"v_pk_fma_f32 v[8:9], v[212:213], s[60:61], v[8:9]\n\t"
"v_pk_fma_f32 v[212:213], v[108:109], s[62:63], v[216:217]\n\t"
"v_pk_fma_f32 v[108:109], v[216:217], s[62:63], v[108:109]\n\t"
"v_mfma_f32_16x16x32_bf16 a[28:31], v[240:243], a[200:203], a[28:31]\n\t"
"v_cvt_pk_bf16_f32 v212, v212, v213\n\t"
"v_cvt_pk_bf16_f32 v216, v234, v235\n\t"
"v_mfma_f32_16x16x32_bf16 a[28:31], a[228:231], a[196:199], a[28:31]\n\t"
"v_accvgpr_read_b32 v230, a2\n\t"
"v_accvgpr_read_b32 v232, a6\n\t"
"v_mfma_f32_16x16x32_bf16 a[28:31], a[252:255], a[192:195], a[28:31]\n\t"
"v_accvgpr_read_b32 v231, a3\n\t"
"v_accvgpr_read_b32 v233, a7\n\t"
"v_pk_fma_f32 v[214:215], v[218:219], s[56:57], v[214:215]\n\t"
"v_pk_fma_f32 v[218:219], v[214:215], s[58:59], v[218:219] neg_lo:[0,0,1] neg_hi:[0,0,1]\n\t"
"v_pk_fma_f32 v[98:99], v[10:11], s[60:61], v[214:215]\n\t"
"v_pk_fma_f32 v[10:11], v[214:215], s[60:61], v[10:11]\n\t"
"v_pk_fma_f32 v[214:215], v[110:111], s[62:63], v[218:219]\n\t"
"v_pk_fma_f32 v[110:111], v[218:219], s[62:63], v[110:111]\n\t"
"v_mfma_f32_16x16x32_bf16 a[36:39], a[252:255], a[196:199], a[36:39]\n\t"
"s_waitcnt vmcnt(8)\n\t"
"v_cvt_pk_bf16_f32 v213, v214, v215\n\t"
"v_cvt_pk_bf16_f32 v217, v98, v99\n\t"
"v_mfma_f32_16x16x32_bf16 a[36:39], a[228:231], a[200:203], a[36:39]\n\t"
"v_accvgpr_read_b32 v236, a8\n\t"
"v_accvgpr_read_b32 v238, a12\n\t"
"v_mfma_f32_16x16x32_bf16 a[36:39], v[240:243], a[204:207], a[36:39]\n\t"
"buffer_load_dwordx4 a[252:255], off, s[80:83], s20 offset:2048 sc1\n\t"
"v_accvgpr_read_b32 v237, a9\n\t"
"v_accvgpr_read_b32 v239, a13\n\t"
"s_mov_b32 s20, 909312\n\t"
"v_pk_fma_f32 v[224:225], v[226:227], s[56:57], v[224:225]\n\t"
"v_pk_fma_f32 v[226:227], v[224:225], s[58:59], v[226:227] neg_lo:[0,0,1] neg_hi:[0,0,1]\n\t"
"v_pk_fma_f32 v[228:229], v[12:13], s[60:61], v[224:225]\n\t"
"v_pk_fma_f32 v[12:13], v[224:225], s[60:61], v[12:13]\n\t"
"v_pk_fma_f32 v[224:225], v[112:113], s[62:63], v[226:227]\n\t"
"v_pk_fma_f32 v[112:113], v[226:227], s[62:63], v[112:113]\n\t"
"v_mfma_f32_16x16x32_bf16 a[44:47], v[248:251], a[168:171], a[44:47]\n\t"
"v_cvt_pk_bf16_f32 v224, v224, v225\n\t"
"v_cvt_pk_bf16_f32 v228, v228, v229\n\t"
"v_mfma_f32_16x16x32_bf16 a[44:47], a[236:239], a[176:179], a[44:47]\n\t"
"v_accvgpr_write_b32 a0, v224\n\t"
"v_accvgpr_write_b32 a4, v228\n\t"
"v_mfma_f32_16x16x32_bf16 a[44:47], a[232:235], a[172:175], a[44:47]\n\t"
"buffer_load_dwordx4 a[228:231], off, s[80:83], s20 offset:0 sc1\n\t"
"v_accvgpr_read_b32 v224, a10\n\t"
"v_accvgpr_read_b32 v226, a14\n\t"
"v_pk_fma_f32 v[230:231], v[232:233], s[56:57], v[230:231]\n\t"
"v_pk_fma_f32 v[232:233], v[230:231], s[58:59], v[232:233] neg_lo:[0,0,1] neg_hi:[0,0,1]\n\t"
"v_pk_fma_f32 v[234:235], v[14:15], s[60:61], v[230:231]\n\t"
"v_pk_fma_f32 v[14:15], v[230:231], s[60:61], v[14:15]\n\t"
"v_pk_fma_f32 v[230:231], v[114:115], s[62:63], v[232:233]\n\t"
"v_pk_fma_f32 v[114:115], v[232:233], s[62:63], v[114:115]\n\t"
"v_mfma_f32_16x16x32_bf16 a[52:55], v[248:251], a[172:175], a[52:55]\n\t"
"v_cvt_pk_bf16_f32 v230, v230, v231\n\t"
"v_cvt_pk_bf16_f32 v234, v234, v235\n\t"
"v_mfma_f32_16x16x32_bf16 a[52:55], a[232:235], a[176:179], a[52:55]\n\t"
"v_accvgpr_write_b32 a1, v230\n\t"
"v_accvgpr_write_b32 a5, v234\n\t"
"v_mfma_f32_16x16x32_bf16 a[52:55], a[236:239], a[180:183], a[52:55]\n\t"
"buffer_load_dwordx4 v[240:243], off, s[80:83], s20 offset:1024 sc1\n\t"
"v_accvgpr_read_b32 v225, a11\n\t"
"v_accvgpr_read_b32 v227, a15\n\t"
"v_pk_fma_f32 v[236:237], v[238:239], s[56:57], v[236:237]\n\t"
"v_pk_fma_f32 v[238:239], v[236:237], s[58:59], v[238:239] neg_lo:[0,0,1] neg_hi:[0,0,1]\n\t"
"v_pk_fma_f32 v[98:99], v[16:17], s[60:61], v[236:237]\n\t"
"v_pk_fma_f32 v[16:17], v[236:237], s[60:61], v[16:17]\n\t"
"v_pk_fma_f32 v[236:237], v[116:117], s[62:63], v[238:239]\n\t"
"v_pk_fma_f32 v[116:117], v[238:239], s[62:63], v[116:117]\n\t"
"v_mfma_f32_16x16x32_bf16 a[60:63], a[236:239], a[184:187], a[60:63]\n\t"
"v_accvgpr_read_b32 v230, a16\n\t"
"v_accvgpr_read_b32 v232, a20\n\t"
"v_mfma_f32_16x16x32_bf16 a[60:63], a[232:235], a[180:183], a[60:63]\n\t"
"v_accvgpr_read_b32 v231, a17\n\t"
"v_accvgpr_read_b32 v233, a21\n\t"
"v_mfma_f32_16x16x32_bf16 a[60:63], v[248:251], a[176:179], a[60:63]\n\t"
"v_cvt_pk_bf16_f32 v236, v236, v237\n\t"
"v_cvt_pk_bf16_f32 v98, v98, v99\n\t"
"v_accvgpr_write_b32 a8, v236\n\t"
"v_accvgpr_write_b32 a12, v98\n\t"
"v_accvgpr_read_b32 v236, a18\n\t"
"v_pk_fma_f32 v[224:225], v[226:227], s[56:57], v[224:225]\n\t"
"v_pk_fma_f32 v[226:227], v[224:225], s[58:59], v[226:227] neg_lo:[0,0,1] neg_hi:[0,0,1]\n\t"
"v_pk_fma_f32 v[228:229], v[18:19], s[60:61], v[224:225]\n\t"
"v_pk_fma_f32 v[18:19], v[224:225], s[60:61], v[18:19]\n\t"
"v_pk_fma_f32 v[224:225], v[118:119], s[62:63], v[226:227]\n\t"
"v_pk_fma_f32 v[118:119], v[226:227], s[62:63], v[118:119]\n\t"
"v_pk_fma_f32 v[230:231], v[232:233], s[56:57], v[230:231]\n\t"
"v_pk_fma_f32 v[232:233], v[230:231], s[58:59], v[232:233] neg_lo:[0,0,1] neg_hi:[0,0,1]\n\t"
"v_pk_fma_f32 v[234:235], v[20:21], s[60:61], v[230:231]\n\t"
"v_pk_fma_f32 v[20:21], v[230:231], s[60:61], v[20:21]\n\t"
"v_pk_fma_f32 v[230:231], v[120:121], s[62:63], v[232:233]\n\t"
"v_pk_fma_f32 v[120:121], v[232:233], s[62:63], v[120:121]\n\t"
"v_mfma_f32_16x16x32_bf16 a[68:71], v[248:251], a[180:183], a[68:71]\n\t"
"v_accvgpr_read_b32 v238, a22\n\t"
"v_accvgpr_read_b32 v237, a19\n\t"
"v_mfma_f32_16x16x32_bf16 a[68:71], a[232:235], a[184:187], a[68:71]\n\t"
"v_accvgpr_read_b32 v239, a23\n\t"
"v_cvt_pk_bf16_f32 v224, v224, v225\n\t"
"v_mfma_f32_16x16x32_bf16 a[68:71], a[236:239], a[188:191], a[68:71]\n\t"
"v_cvt_pk_bf16_f32 v228, v228, v229\n\t"
"v_cvt_pk_bf16_f32 v230, v230, v231\n\t"
"v_cvt_pk_bf16_f32 v234, v234, v235\n\t"
"v_accvgpr_write_b32 a9, v224\n\t"
"v_accvgpr_write_b32 a13, v228\n\t"
"v_pk_fma_f32 v[236:237], v[238:239], s[56:57], v[236:237]\n\t"
"v_pk_fma_f32 v[238:239], v[236:237], s[58:59], v[238:239] neg_lo:[0,0,1] neg_hi:[0,0,1]\n\t"
"v_pk_fma_f32 v[98:99], v[22:23], s[60:61], v[236:237]\n\t"
"v_pk_fma_f32 v[22:23], v[236:237], s[60:61], v[22:23]\n\t"
"v_pk_fma_f32 v[236:237], v[122:123], s[62:63], v[238:239]\n\t"
"v_pk_fma_f32 v[122:123], v[238:239], s[62:63], v[122:123]\n\t"
"v_mfma_f32_16x16x32_bf16 a[76:79], a[236:239], a[192:195], a[76:79]\n\t"
"v_accvgpr_write_b32 a16, v230\n\t"
"v_accvgpr_write_b32 a20, v234\n\t"
"v_mfma_f32_16x16x32_bf16 a[76:79], a[232:235], a[188:191], a[76:79]\n\t"
"v_accvgpr_read_b32 v224, a24\n\t"
"v_accvgpr_read_b32 v226, a28\n\t"
"v_mfma_f32_16x16x32_bf16 a[76:79], v[248:251], a[184:187], a[76:79]\n\t"
"v_accvgpr_read_b32 v225, a25\n\t"
"v_accvgpr_read_b32 v227, a29\n\t"
"v_cvt_pk_bf16_f32 v236, v236, v237\n\t"
"v_cvt_pk_bf16_f32 v98, v98, v99\n\t"
"v_accvgpr_write_b32 a17, v236\n\t"
"v_pk_fma_f32 v[224:225], v[226:227], s[56:57], v[224:225]\n\t"
"v_pk_fma_f32 v[226:227], v[224:225], s[58:59], v[226:227] neg_lo:[0,0,1] neg_hi:[0,0,1]\n\t"
"v_pk_fma_f32 v[228:229], v[24:25], s[60:61], v[224:225]\n\t"
"v_pk_fma_f32 v[24:25], v[224:225], s[60:61], v[24:25]\n\t"
"v_pk_fma_f32 v[224:225], v[124:125], s[62:63], v[226:227]\n\t"
"v_pk_fma_f32 v[124:125], v[226:227], s[62:63], v[124:125]\n\t"
"v_mfma_f32_16x16x32_bf16 a[84:87], v[248:251], a[188:191], a[84:87]\n\t"
"v_accvgpr_write_b32 a21, v98\n\t"
"v_accvgpr_read_b32 v230, a26\n\t"
"v_mfma_f32_16x16x32_bf16 a[84:87], a[232:235], a[192:195], a[84:87]\n\t"
"v_accvgpr_read_b32 v232, a30\n\t"
"v_accvgpr_read_b32 v231, a27\n\t"
"v_mfma_f32_16x16x32_bf16 a[84:87], a[236:239], a[196:199], a[84:87]\n\t"
"v_accvgpr_read_b32 v233, a31\n\t"
"v_cvt_pk_bf16_f32 v224, v224, v225\n\t"
"v_cvt_pk_bf16_f32 v228, v228, v229\n\t"
"v_accvgpr_write_b32 a24, v224\n\t"
"v_accvgpr_write_b32 a28, v228\n\t"
"v_pk_fma_f32 v[230:231], v[232:233], s[56:57], v[230:231]\n\t"
"v_pk_fma_f32 v[232:233], v[230:231], s[58:59], v[232:233] neg_lo:[0,0,1] neg_hi:[0,0,1]\n\t"
"v_pk_fma_f32 v[234:235], v[26:27], s[60:61], v[230:231]\n\t"
"v_pk_fma_f32 v[26:27], v[230:231], s[60:61], v[26:27]\n\t"
"v_pk_fma_f32 v[230:231], v[126:127], s[62:63], v[232:233]\n\t"
"v_pk_fma_f32 v[126:127], v[232:233], s[62:63], v[126:127]\n\t"
"v_mfma_f32_16x16x32_bf16 a[92:95], a[236:239], a[200:203], a[92:95]\n\t"
"v_accvgpr_read_b32 v236, a32\n\t"
"v_accvgpr_read_b32 v238, a36\n\t"
"v_mfma_f32_16x16x32_bf16 a[92:95], a[232:235], a[196:199], a[92:95]\n\t"
"v_accvgpr_read_b32 v237, a33\n\t"
"v_accvgpr_read_b32 v239, a37\n\t"
"v_mfma_f32_16x16x32_bf16 a[92:95], v[248:251], a[192:195], a[92:95]\n\t"
"v_cvt_pk_bf16_f32 v230, v230, v231\n\t"
"v_cvt_pk_bf16_f32 v234, v234, v235\n\t"
"v_accvgpr_write_b32 a25, v230\n\t"
"v_accvgpr_write_b32 a29, v234\n\t"
"v_accvgpr_read_b32 v224, a34\n\t"
"v_pk_fma_f32 v[236:237], v[238:239], s[56:57], v[236:237]\n\t"
"v_pk_fma_f32 v[238:239], v[236:237], s[58:59], v[238:239] neg_lo:[0,0,1] neg_hi:[0,0,1]\n\t"
"v_pk_fma_f32 v[98:99], v[28:29], s[60:61], v[236:237]\n\t"
"v_pk_fma_f32 v[28:29], v[236:237], s[60:61], v[28:29]\n\t"
"v_pk_fma_f32 v[236:237], v[128:129], s[62:63], v[238:239]\n\t"
"v_pk_fma_f32 v[128:129], v[238:239], s[62:63], v[128:129]\n\t"
"v_mfma_f32_16x16x32_bf16 a[100:103], v[248:251], a[196:199], a[100:103]\n\t"
"s_waitcnt vmcnt(8)\n\t"
"v_accvgpr_read_b32 v226, a38\n\t"
"v_accvgpr_read_b32 v225, a35\n\t"
"v_mfma_f32_16x16x32_bf16 a[100:103], a[232:235], a[200:203], a[100:103]\n\t"
"v_accvgpr_read_b32 v227, a39\n\t"
"v_cvt_pk_bf16_f32 v236, v236, v237\n\t"
"v_mfma_f32_16x16x32_bf16 a[100:103], a[236:239], a[204:207], a[100:103]\n\t"
"buffer_load_dwordx4 a[232:235], off, s[80:83], s20 offset:2048 sc1\n\t"
"v_cvt_pk_bf16_f32 v98, v98, v99\n\t"
"v_accvgpr_write_b32 a32, v236\n\t"
"v_pk_fma_f32 v[224:225], v[226:227], s[56:57], v[224:225]\n\t"
"v_pk_fma_f32 v[226:227], v[224:225], s[58:59], v[226:227] neg_lo:[0,0,1] neg_hi:[0,0,1]\n\t"
"v_pk_fma_f32 v[228:229], v[30:31], s[60:61], v[224:225]\n\t"
"v_pk_fma_f32 v[30:31], v[224:225], s[60:61], v[30:31]\n\t"
"v_pk_fma_f32 v[224:225], v[130:131], s[62:63], v[226:227]\n\t"
"v_pk_fma_f32 v[130:131], v[226:227], s[62:63], v[130:131]\n\t"
"v_mfma_f32_16x16x32_bf16 a[108:111], a[244:247], a[168:171], a[108:111]\n\t"
"s_mov_b32 s20, 1069056\n\t"
"ds_read_b128 a[168:171], v244 offset:0\n\t"
"v_accvgpr_write_b32 a36, v98\n\t"
"v_mfma_f32_16x16x32_bf16 a[108:111], a[240:243], a[176:179], a[108:111]\n\t"
"v_accvgpr_read_b32 v230, a40\n\t"
"v_accvgpr_read_b32 v232, a44\n\t"
"v_mfma_f32_16x16x32_bf16 a[108:111], a[212:215], a[172:175], a[108:111]\n\t"
"buffer_load_dwordx4 a[236:239], off, s[80:83], s20 offset:0 sc1\n\t"
"v_accvgpr_read_b32 v231, a41\n\t"
"v_accvgpr_read_b32 v233, a45\n\t"
"v_cvt_pk_bf16_f32 v224, v224, v225\n\t"
"v_cvt_pk_bf16_f32 v228, v228, v229\n\t"
"v_pk_fma_f32 v[230:231], v[232:233], s[56:57], v[230:231]\n\t"
"v_pk_fma_f32 v[232:233], v[230:231], s[58:59], v[232:233] neg_lo:[0,0,1] neg_hi:[0,0,1]\n\t"
"v_pk_fma_f32 v[234:235], v[32:33], s[60:61], v[230:231]\n\t"
"v_pk_fma_f32 v[32:33], v[230:231], s[60:61], v[32:33]\n\t"
"v_pk_fma_f32 v[230:231], v[132:133], s[62:63], v[232:233]\n\t"
"v_pk_fma_f32 v[132:133], v[232:233], s[62:63], v[132:133]\n\t"
"v_mfma_f32_16x16x32_bf16 a[116:119], a[244:247], a[172:175], a[116:119]\n\t"
"ds_read_b128 a[172:175], v244 offset:512\n\t"
"v_accvgpr_write_b32 a33, v224\n\t"
"v_mfma_f32_16x16x32_bf16 a[116:119], a[212:215], a[176:179], a[116:119]\n\t"
"v_accvgpr_write_b32 a37, v228\n\t"
"v_accvgpr_read_b32 v236, a42\n\t"
"v_mfma_f32_16x16x32_bf16 a[116:119], a[240:243], a[180:183], a[116:119]\n\t"
"global_load_dwordx4 v[248:251], v252, s[48:49] offset:0\n\t"
"v_accvgpr_read_b32 v238, a46\n\t"
"v_accvgpr_read_b32 v237, a43\n\t"
"v_accvgpr_read_b32 v239, a47\n\t"
"v_pk_fma_f32 v[236:237], v[238:239], s[56:57], v[236:237]\n\t"
"v_pk_fma_f32 v[238:239], v[236:237], s[58:59], v[238:239] neg_lo:[0,0,1] neg_hi:[0,0,1]\n\t"
"v_pk_fma_f32 v[98:99], v[34:35], s[60:61], v[236:237]\n\t"
"v_pk_fma_f32 v[34:35], v[236:237], s[60:61], v[34:35]\n\t"
"v_pk_fma_f32 v[236:237], v[134:135], s[62:63], v[238:239]\n\t"
"v_pk_fma_f32 v[134:135], v[238:239], s[62:63], v[134:135]\n\t"
"v_mfma_f32_16x16x32_bf16 a[124:127], a[240:243], a[184:187], a[124:127]\n\t"
"v_cvt_pk_bf16_f32 v198, v230, v231\n\t"
"v_cvt_pk_bf16_f32 v202, v234, v235\n\t"
"v_mfma_f32_16x16x32_bf16 a[124:127], a[212:215], a[180:183], a[124:127]\n\t"
"v_accvgpr_read_b32 v224, a48\n\t"
"v_accvgpr_read_b32 v226, a52\n\t"
"v_mfma_f32_16x16x32_bf16 a[124:127], a[244:247], a[176:179], a[124:127]\n\t"
"ds_read_b128 a[176:179], v244 offset:1024\n\t"
"v_accvgpr_read_b32 v225, a49\n\t"
"v_accvgpr_read_b32 v227, a53\n\t"
"v_accvgpr_read_b32 v230, a50\n\t"
"v_accvgpr_read_b32 v232, a54\n\t"
"v_accvgpr_read_b32 v231, a51\n\t"
"v_accvgpr_read_b32 v233, a55\n\t"
"v_pk_fma_f32 v[224:225], v[226:227], s[56:57], v[224:225]\n\t"
"v_pk_fma_f32 v[226:227], v[224:225], s[58:59], v[226:227] neg_lo:[0,0,1] neg_hi:[0,0,1]\n\t"
"v_pk_fma_f32 v[228:229], v[36:37], s[60:61], v[224:225]\n\t"
"v_pk_fma_f32 v[36:37], v[224:225], s[60:61], v[36:37]\n\t"
"v_pk_fma_f32 v[224:225], v[136:137], s[62:63], v[226:227]\n\t"
"v_pk_fma_f32 v[136:137], v[226:227], s[62:63], v[136:137]\n\t"
"v_pk_fma_f32 v[230:231], v[232:233], s[56:57], v[230:231]\n\t"
"v_pk_fma_f32 v[232:233], v[230:231], s[58:59], v[232:233] neg_lo:[0,0,1] neg_hi:[0,0,1]\n\t"
"v_pk_fma_f32 v[234:235], v[38:39], s[60:61], v[230:231]\n\t"
"v_pk_fma_f32 v[38:39], v[230:231], s[60:61], v[38:39]\n\t"
"v_pk_fma_f32 v[230:231], v[138:139], s[62:63], v[232:233]\n\t"
"v_pk_fma_f32 v[138:139], v[232:233], s[62:63], v[138:139]\n\t"
"v_mfma_f32_16x16x32_bf16 a[132:135], a[244:247], a[180:183], a[132:135]\n\t"
"ds_read_b128 a[180:183], v244 offset:1536\n\t"
"v_cvt_pk_bf16_f32 v199, v236, v237\n\t"
"v_mfma_f32_16x16x32_bf16 a[132:135], a[212:215], a[184:187], a[132:135]\n\t"
"v_cvt_pk_bf16_f32 v203, v98, v99\n\t"
"v_accvgpr_read_b32 v236, a56\n\t"
"v_mfma_f32_16x16x32_bf16 a[132:135], a[240:243], a[188:191], a[132:135]\n\t"
"buffer_load_dwordx4 a[52:55], off, s[80:83], s20 offset:1024 sc1\n\t"
"v_accvgpr_read_b32 v238, a60\n\t"
"v_accvgpr_read_b32 v237, a57\n\t"
"v_accvgpr_read_b32 v239, a61\n\t"
"v_pk_fma_f32 v[236:237], v[238:239], s[56:57], v[236:237]\n\t"
"v_pk_fma_f32 v[238:239], v[236:237], s[58:59], v[238:239] neg_lo:[0,0,1] neg_hi:[0,0,1]\n\t"
"v_pk_fma_f32 v[98:99], v[40:41], s[60:61], v[236:237]\n\t"
"v_pk_fma_f32 v[40:41], v[236:237], s[60:61], v[40:41]\n\t"
"v_pk_fma_f32 v[236:237], v[140:141], s[62:63], v[238:239]\n\t"
"v_pk_fma_f32 v[140:141], v[238:239], s[62:63], v[140:141]\n\t"
"v_mfma_f32_16x16x32_bf16 a[140:143], a[240:243], a[192:195], a[140:143]\n\t"
"v_cvt_pk_bf16_f32 v206, v224, v225\n\t"
"v_cvt_pk_bf16_f32 v210, v228, v229\n\t"
"v_mfma_f32_16x16x32_bf16 a[140:143], a[212:215], a[188:191], a[140:143]\n\t"
"v_cvt_pk_bf16_f32 v207, v230, v231\n\t"
"v_cvt_pk_bf16_f32 v211, v234, v235\n\t"
"v_mfma_f32_16x16x32_bf16 a[140:143], a[244:247], a[184:187], a[140:143]\n\t"
"ds_read_b128 a[184:187], v244 offset:2048\n\t"
"v_accvgpr_read_b32 v224, a58\n\t"
"v_accvgpr_read_b32 v226, a62\n\t"
"v_accvgpr_read_b32 v225, a59\n\t"
"v_accvgpr_read_b32 v227, a63\n\t"
"v_pk_fma_f32 v[224:225], v[226:227], s[56:57], v[224:225]\n\t"
"v_pk_fma_f32 v[226:227], v[224:225], s[58:59], v[226:227] neg_lo:[0,0,1] neg_hi:[0,0,1]\n\t"
"v_pk_fma_f32 v[228:229], v[42:43], s[60:61], v[224:225]\n\t"
"v_pk_fma_f32 v[42:43], v[224:225], s[60:61], v[42:43]\n\t"
"v_pk_fma_f32 v[224:225], v[142:143], s[62:63], v[226:227]\n\t"
"v_pk_fma_f32 v[142:143], v[226:227], s[62:63], v[142:143]\n\t"
"v_mfma_f32_16x16x32_bf16 a[148:151], a[244:247], a[188:191], a[148:151]\n\t"
"ds_read_b128 a[188:191], v244 offset:2560\n\t"
"v_cvt_pk_bf16_f32 v214, v236, v237\n\t"
"v_mfma_f32_16x16x32_bf16 a[148:151], a[212:215], a[192:195], a[148:151]\n\t"
"v_cvt_pk_bf16_f32 v218, v98, v99\n\t"
"v_accvgpr_read_b32 v230, a64\n\t"
"v_mfma_f32_16x16x32_bf16 a[148:151], a[240:243], a[196:199], a[148:151]\n\t"
"buffer_load_dwordx4 a[56:59], off, s[80:83], s20 offset:2048 sc1\n\t"
"v_accvgpr_read_b32 v232, a68\n\t"
"v_accvgpr_read_b32 v231, a65\n\t"
"v_accvgpr_read_b32 v233, a69\n\t"
"v_pk_fma_f32 v[230:231], v[232:233], s[56:57], v[230:231]\n\t"
"v_pk_fma_f32 v[232:233], v[230:231], s[58:59], v[232:233] neg_lo:[0,0,1] neg_hi:[0,0,1]\n\t"
"v_pk_fma_f32 v[234:235], v[44:45], s[60:61], v[230:231]\n\t"
"v_pk_fma_f32 v[44:45], v[230:231], s[60:61], v[44:45]\n\t"
"v_pk_fma_f32 v[230:231], v[144:145], s[62:63], v[232:233]\n\t"
"v_pk_fma_f32 v[144:145], v[232:233], s[62:63], v[144:145]\n\t"
"v_mfma_f32_16x16x32_bf16 a[156:159], a[240:243], a[200:203], a[156:159]\n\t"
"s_mov_b32 s20, 1081344\n\t"
"v_cvt_pk_bf16_f32 v215, v224, v225\n\t"
"v_cvt_pk_bf16_f32 v219, v228, v229\n\t"
"v_mfma_f32_16x16x32_bf16 a[156:159], a[212:215], a[196:199], a[156:159]\n\t"
"v_accvgpr_read_b32 v236, a66\n\t"
"v_accvgpr_read_b32 v238, a70\n\t"
"v_mfma_f32_16x16x32_bf16 a[156:159], a[244:247], a[192:195], a[156:159]\n\t"
"ds_read_b128 a[192:195], v244 offset:3072\n\t"
"buffer_load_dwordx4 a[60:63], off, s[80:83], s20 offset:0 sc1\n\t"
"v_accvgpr_read_b32 v237, a67\n\t"
"v_accvgpr_read_b32 v239, a71\n\t"
"v_cvt_pk_bf16_f32 v230, v230, v231\n\t"
"v_cvt_pk_bf16_f32 v234, v234, v235\n\t"
"v_pk_fma_f32 v[236:237], v[238:239], s[56:57], v[236:237]\n\t"
"v_pk_fma_f32 v[238:239], v[236:237], s[58:59], v[238:239] neg_lo:[0,0,1] neg_hi:[0,0,1]\n\t"
"v_pk_fma_f32 v[98:99], v[46:47], s[60:61], v[236:237]\n\t"
"v_pk_fma_f32 v[46:47], v[236:237], s[60:61], v[46:47]\n\t"
"v_pk_fma_f32 v[236:237], v[146:147], s[62:63], v[238:239]\n\t"
"v_pk_fma_f32 v[146:147], v[238:239], s[62:63], v[146:147]\n\t"
"v_mfma_f32_16x16x32_bf16 a[164:167], a[244:247], a[196:199], a[164:167]\n\t"
"s_waitcnt vmcnt(11) lgkmcnt(4)\n\t"
"ds_read_b128 a[196:199], v244 offset:3584\n\t"
"v_accvgpr_write_b32 a2, v230\n\t"
"v_mfma_f32_16x16x32_bf16 a[164:167], a[212:215], a[200:203], a[164:167]\n\t"
"ds_read_b128 a[200:203], v244 offset:4096\n\t"
"v_accvgpr_write_b32 a6, v234\n\t"
"v_mfma_f32_16x16x32_bf16 a[164:167], a[240:243], a[204:207], a[164:167]\n\t"
"buffer_load_dwordx4 a[212:215], off, s[80:83], s20 offset:1024 sc1\n\t"
"ds_read_b128 a[204:207], v244 offset:4608\n\t"
"v_accvgpr_read_b32 v224, a72\n\t"
"v_accvgpr_read_b32 v226, a76\n\t"
"v_accvgpr_read_b32 v225, a73\n\t"
"v_accvgpr_read_b32 v227, a77\n\t"
"v_cvt_pk_bf16_f32 v236, v236, v237\n\t"
"v_cvt_pk_bf16_f32 v98, v98, v99\n\t"
"v_pk_fma_f32 v[224:225], v[226:227], s[56:57], v[224:225]\n\t"
"v_pk_fma_f32 v[226:227], v[224:225], s[58:59], v[226:227] neg_lo:[0,0,1] neg_hi:[0,0,1]\n\t"
"v_pk_fma_f32 v[228:229], v[48:49], s[60:61], v[224:225]\n\t"
"v_pk_fma_f32 v[48:49], v[224:225], s[60:61], v[48:49]\n\t"
"v_pk_fma_f32 v[224:225], v[148:149], s[62:63], v[226:227]\n\t"
"v_pk_fma_f32 v[148:149], v[226:227], s[62:63], v[148:149]\n\t"
"Lst32_%=:\n\t"
"v_mfma_f32_16x16x32_bf16 v[0:3], a[248:251], a[168:171], v[0:3]\n\t"
"s_waitcnt lgkmcnt(6)\n\t"
"v_accvgpr_write_b32 a3, v236\n\t"
"v_accvgpr_write_b32 a7, v98\n\t"
"v_mfma_f32_16x16x32_bf16 v[0:3], a[220:223], a[176:179], v[0:3]\n\t"
"v_accvgpr_read_b32 v230, a74\n\t"
"v_accvgpr_read_b32 v232, a78\n\t"
"v_mfma_f32_16x16x32_bf16 v[0:3], a[216:219], a[172:175], v[0:3]\n\t"
"buffer_load_dwordx4 a[240:243], off, s[80:83], s20 offset:2048 sc1\n\t"
"v_accvgpr_read_b32 v231, a75\n\t"
"v_accvgpr_read_b32 v233, a79\n\t"
"v_cvt_pk_bf16_f32 v224, v224, v225\n\t"
"v_cvt_pk_bf16_f32 v228, v228, v229\n\t"
"v_pk_fma_f32 v[230:231], v[232:233], s[56:57], v[230:231]\n\t"
"v_pk_fma_f32 v[232:233], v[230:231], s[58:59], v[232:233] neg_lo:[0,0,1] neg_hi:[0,0,1]\n\t"
"v_pk_fma_f32 v[234:235], v[50:51], s[60:61], v[230:231]\n\t"
"v_pk_fma_f32 v[50:51], v[230:231], s[60:61], v[50:51]\n\t"
"v_pk_fma_f32 v[230:231], v[150:151], s[62:63], v[232:233]\n\t"
"v_pk_fma_f32 v[150:151], v[232:233], s[62:63], v[150:151]\n\t"
"v_mfma_f32_16x16x32_bf16 v[4:7], a[248:251], a[172:175], v[4:7]\n\t"
"s_waitcnt lgkmcnt(5)\n\t"
"s_mov_b32 s20, 1093632\n\t"
"v_accvgpr_write_b32 a10, v224\n\t"
"v_mfma_f32_16x16x32_bf16 v[4:7], a[216:219], a[176:179], v[4:7]\n\t"
"v_accvgpr_write_b32 a14, v228\n\t"
"v_accvgpr_read_b32 v236, a80\n\t"
"v_mfma_f32_16x16x32_bf16 v[4:7], a[220:223], a[180:183], v[4:7]\n\t"
"buffer_load_dwordx4 a[244:247], off, s[80:83], s20 offset:0 sc1\n\t"
"v_accvgpr_read_b32 v238, a84\n\t"
"v_accvgpr_read_b32 v237, a81\n\t"
"v_accvgpr_read_b32 v239, a85\n\t"
"v_cvt_pk_bf16_f32 v230, v230, v231\n\t"
"v_cvt_pk_bf16_f32 v234, v234, v235\n\t"
"v_pk_fma_f32 v[236:237], v[238:239], s[56:57], v[236:237]\n\t"
"v_pk_fma_f32 v[238:239], v[236:237], s[58:59], v[238:239] neg_lo:[0,0,1] neg_hi:[0,0,1]\n\t"
"v_pk_fma_f32 v[98:99], v[52:53], s[60:61], v[236:237]\n\t"
"v_pk_fma_f32 v[52:53], v[236:237], s[60:61], v[52:53]\n\t"
"v_pk_fma_f32 v[236:237], v[152:153], s[62:63], v[238:239]\n\t"
"v_pk_fma_f32 v[152:153], v[238:239], s[62:63], v[152:153]\n\t"
"v_mfma_f32_16x16x32_bf16 v[8:11], a[220:223], a[184:187], v[8:11]\n\t"
"s_waitcnt lgkmcnt(4)\n\t"
"v_accvgpr_write_b32 a11, v230\n\t"
"v_accvgpr_write_b32 a15, v234\n\t"
"v_mfma_f32_16x16x32_bf16 v[8:11], a[216:219], a[180:183], v[8:11]\n\t"
"v_accvgpr_read_b32 v224, a82\n\t"
"v_accvgpr_read_b32 v226, a86\n\t"
"v_mfma_f32_16x16x32_bf16 v[8:11], a[248:251], a[176:179], v[8:11]\n\t"
"buffer_load_dwordx4 a[64:67], off, s[80:83], s20 offset:1024 sc1\n\t"
"v_accvgpr_read_b32 v225, a83\n\t"
"v_accvgpr_read_b32 v227, a87\n\t"
"v_cvt_pk_bf16_f32 v236, v236, v237\n\t"
"v_cvt_pk_bf16_f32 v98, v98, v99\n\t"
"v_pk_fma_f32 v[224:225], v[226:227], s[56:57], v[224:225]\n\t"
"v_pk_fma_f32 v[226:227], v[224:225], s[58:59], v[226:227] neg_lo:[0,0,1] neg_hi:[0,0,1]\n\t"
"v_pk_fma_f32 v[228:229], v[54:55], s[60:61], v[224:225]\n\t"
"v_pk_fma_f32 v[54:55], v[224:225], s[60:61], v[54:55]\n\t"
"v_pk_fma_f32 v[224:225], v[154:155], s[62:63], v[226:227]\n\t"
"v_pk_fma_f32 v[154:155], v[226:227], s[62:63], v[154:155]\n\t"
"v_mfma_f32_16x16x32_bf16 v[12:15], a[248:251], a[180:183], v[12:15]\n\t"
"s_waitcnt lgkmcnt(3)\n\t"
"v_accvgpr_write_b32 a18, v236\n\t"
"v_accvgpr_write_b32 a22, v98\n\t"
"v_mfma_f32_16x16x32_bf16 v[12:15], a[216:219], a[184:187], v[12:15]\n\t"
"v_accvgpr_read_b32 v230, a88\n\t"
"v_accvgpr_read_b32 v232, a92\n\t"
"v_mfma_f32_16x16x32_bf16 v[12:15], a[220:223], a[188:191], v[12:15]\n\t"
"buffer_load_dwordx4 a[68:71], off, s[80:83], s20 offset:2048 sc1\n\t"
"v_accvgpr_read_b32 v231, a89\n\t"
"v_accvgpr_read_b32 v233, a93\n\t"
"v_accvgpr_read_b32 v236, a90\n\t"
"v_accvgpr_read_b32 v238, a94\n\t"
"v_accvgpr_read_b32 v237, a91\n\t"
"v_accvgpr_read_b32 v239, a95\n\t"
"v_cvt_pk_bf16_f32 v224, v224, v225\n\t"
"v_cvt_pk_bf16_f32 v228, v228, v229\n\t"
"Lpg23_%=:\n\t"
"s_cbranch_vccnz Lvr3_%=\n\t"
"v_pk_fma_f32 v[230:231], v[232:233], s[56:57], v[230:231]\n\t"
"v_pk_fma_f32 v[232:233], v[230:231], s[58:59], v[232:233] neg_lo:[0,0,1] neg_hi:[0,0,1]\n\t"
"v_pk_fma_f32 v[234:235], v[56:57], s[60:61], v[230:231]\n\t"
"v_pk_fma_f32 v[56:57], v[230:231], s[60:61], v[56:57]\n\t"
"v_pk_fma_f32 v[230:231], v[156:157], s[62:63], v[232:233]\n\t"
"v_pk_fma_f32 v[156:157], v[232:233], s[62:63], v[156:157]\n\t"
"v_pk_fma_f32 v[236:237], v[238:239], s[56:57], v[236:237]\n\t"
"v_pk_fma_f32 v[238:239], v[236:237], s[58:59], v[238:239] neg_lo:[0,0,1] neg_hi:[0,0,1]\n\t"
"v_pk_fma_f32 v[98:99], v[58:59], s[60:61], v[236:237]\n\t"
"v_pk_fma_f32 v[58:59], v[236:237], s[60:61], v[58:59]\n\t"
"v_pk_fma_f32 v[236:237], v[158:159], s[62:63], v[238:239]\n\t"
"v_pk_fma_f32 v[158:159], v[238:239], s[62:63], v[158:159]\n\t"
"v_mfma_f32_16x16x32_bf16 v[16:19], a[220:223], a[192:195], v[16:19]\n\t"
"s_waitcnt lgkmcnt(2)\n\t"
"s_mov_b32 s20, 589824\n\t"
"v_accvgpr_write_b32 a19, v224\n\t"
"v_mfma_f32_16x16x32_bf16 v[16:19], a[216:219], a[188:191], v[16:19]\n\t"
"v_accvgpr_write_b32 a23, v228\n\t"
"v_accvgpr_read_b32 v224, a96\n\t"
"v_mfma_f32_16x16x32_bf16 v[16:19], a[248:251], a[184:187], v[16:19]\n\t"
"buffer_load_dwordx4 a[72:75], off, s[80:83], s20 offset:0 sc1\n\t"
"v_accvgpr_read_b32 v226, a100\n\t"
"v_accvgpr_read_b32 v225, a97\n\t"
"v_accvgpr_read_b32 v227, a101\n\t"
"v_cvt_pk_bf16_f32 v230, v230, v231\n\t"
"v_cvt_pk_bf16_f32 v234, v234, v235\n\t"
"v_pk_fma_f32 v[224:225], v[226:227], s[56:57], v[224:225]\n\t"
"v_pk_fma_f32 v[226:227], v[224:225], s[58:59], v[226:227] neg_lo:[0,0,1] neg_hi:[0,0,1]\n\t"
"v_pk_fma_f32 v[228:229], v[60:61], s[60:61], v[224:225]\n\t"
"v_pk_fma_f32 v[60:61], v[224:225], s[60:61], v[60:61]\n\t"
"v_pk_fma_f32 v[224:225], v[160:161], s[62:63], v[226:227]\n\t"
"v_pk_fma_f32 v[160:161], v[226:227], s[62:63], v[160:161]\n\t"
"v_mfma_f32_16x16x32_bf16 v[20:23], a[248:251], a[188:191], v[20:23]\n\t"
"s_waitcnt lgkmcnt(1)\n\t"
"v_accvgpr_write_b32 a26, v230\n\t"
"v_cvt_pk_bf16_f32 v236, v236, v237\n\t"
"v_mfma_f32_16x16x32_bf16 v[20:23], a[216:219], a[192:195], v[20:23]\n\t"
"v_accvgpr_write_b32 a30, v234\n\t"
"v_cvt_pk_bf16_f32 v98, v98, v99\n\t"
"v_mfma_f32_16x16x32_bf16 v[20:23], a[220:223], a[196:199], v[20:23]\n\t"
"buffer_load_dwordx4 a[76:79], off, s[80:83], s20 offset:1024 sc1\n\t"
"v_accvgpr_read_b32 v230, a98\n\t"
"v_accvgpr_read_b32 v232, a102\n\t"
"v_accvgpr_read_b32 v231, a99\n\t"
"v_accvgpr_read_b32 v233, a103\n\t"
"v_accvgpr_write_b32 a27, v236\n\t"
"v_accvgpr_write_b32 a31, v98\n\t"
"v_cvt_pk_bf16_f32 v224, v224, v225\n\t"
"v_cvt_pk_bf16_f32 v228, v228, v229\n\t"
"v_pk_fma_f32 v[230:231], v[232:233], s[56:57], v[230:231]\n\t"
"v_pk_fma_f32 v[232:233], v[230:231], s[58:59], v[232:233] neg_lo:[0,0,1] neg_hi:[0,0,1]\n\t"
"v_pk_fma_f32 v[234:235], v[62:63], s[60:61], v[230:231]\n\t"
"v_pk_fma_f32 v[62:63], v[230:231], s[60:61], v[62:63]\n\t"
"v_pk_fma_f32 v[230:231], v[162:163], s[62:63], v[232:233]\n\t"
"v_pk_fma_f32 v[162:163], v[232:233], s[62:63], v[162:163]\n\t"
"v_mfma_f32_16x16x32_bf16 v[24:27], a[220:223], a[200:203], v[24:27]\n\t"
"s_waitcnt lgkmcnt(0)\n\t"
"v_accvgpr_write_b32 a34, v224\n\t"
"v_accvgpr_write_b32 a38, v228\n\t"
"v_mfma_f32_16x16x32_bf16 v[24:27], a[216:219], a[196:199], v[24:27]\n\t"
"v_accvgpr_read_b32 v236, a104\n\t"
"v_accvgpr_read_b32 v238, a108\n\t"
"v_mfma_f32_16x16x32_bf16 v[24:27], a[248:251], a[192:195], v[24:27]\n\t"
"buffer_load_dwordx4 a[80:83], off, s[80:83], s20 offset:2048 sc1\n\t"
"v_accvgpr_read_b32 v237, a105\n\t"
"v_accvgpr_read_b32 v239, a109\n\t"
"v_cvt_pk_bf16_f32 v230, v230, v231\n\t"
"v_cvt_pk_bf16_f32 v234, v234, v235\n\t"
"v_pk_fma_f32 v[236:237], v[238:239], s[56:57], v[236:237]\n\t"
"v_pk_fma_f32 v[238:239], v[236:237], s[58:59], v[238:239] neg_lo:[0,0,1] neg_hi:[0,0,1]\n\t"
"v_pk_fma_f32 v[98:99], v[64:65], s[60:61], v[236:237]\n\t"
"v_pk_fma_f32 v[64:65], v[236:237], s[60:61], v[64:65]\n\t"
"v_pk_fma_f32 v[236:237], v[164:165], s[62:63], v[238:239]\n\t"
"v_pk_fma_f32 v[164:165], v[238:239], s[62:63], v[164:165]\n\t"
"v_mfma_f32_16x16x32_bf16 v[28:31], a[248:251], a[196:199], v[28:31]\n\t"
"s_waitcnt vmcnt(16)\n\t"
"s_mov_b32 s20, 602112\n\t"
"s_waitcnt vmcnt(11)\n\t"
"v_lshrrev_b32 v245, 10, v249\n\t"
"v_mfma_f32_16x16x32_bf16 v[28:31], a[216:219], a[200:203], v[28:31]\n\t"
"v_lshl_add_u32 v245, v245, 9, v249\n\t"
"v_lshrrev_b32 v246, 10, v250\n\t"
"v_mfma_f32_16x16x32_bf16 v[28:31], a[220:223], a[204:207], v[28:31]\n\t"
"buffer_load_dwordx4 a[216:219], off, s[80:83], s20 offset:0 sc1\n\t"
"v_accvgpr_read_b32 v224, a106\n\t"
"v_accvgpr_read_b32 v226, a110\n\t"
"v_accvgpr_read_b32 v225, a107\n\t"
"v_accvgpr_read_b32 v227, a111\n\t"
"v_lshl_add_u32 v246, v246, 9, v250\n\t"
"v_lshrrev_b32 v255, 10, v252\n\t"
"v_lshl_add_u32 v255, v255, 9, v252\n\t"
"v_accvgpr_write_b32 a35, v230\n\t"
"v_accvgpr_write_b32 a39, v234\n\t"
"v_cvt_pk_bf16_f32 v236, v236, v237\n\t"
"v_cvt_pk_bf16_f32 v98, v98, v99\n\t"
"v_add_u32 v245, 44032, v245\n\t"
"v_add_u32 v249, 55296, v245\n\t"
"v_add_u32 v246, 44032, v246\n\t"
"v_add_u32 v250, 55296, v246\n\t"
"v_add_u32 v255, 44032, v255\n\t"
"v_add_u32 v247, 55296, v255\n\t"
"v_pk_fma_f32 v[224:225], v[226:227], s[56:57], v[224:225]\n\t"
"v_pk_fma_f32 v[226:227], v[224:225], s[58:59], v[226:227] neg_lo:[0,0,1] neg_hi:[0,0,1]\n\t"
"v_pk_fma_f32 v[228:229], v[66:67], s[60:61], v[224:225]\n\t"
"v_pk_fma_f32 v[66:67], v[224:225], s[60:61], v[66:67]\n\t"
"v_pk_fma_f32 v[224:225], v[166:167], s[62:63], v[226:227]\n\t"
"v_pk_fma_f32 v[166:167], v[226:227], s[62:63], v[166:167]\n\t"
"v_mfma_f32_16x16x32_bf16 v[32:35], a[208:211], a[168:171], v[32:35]\n\t"
"v_accvgpr_write_b32 a104, v236\n\t"
"v_accvgpr_write_b32 a106, v98\n\t"
"v_mfma_f32_16x16x32_bf16 v[32:35], a[252:255], a[176:179], v[32:35]\n\t"
"v_accvgpr_read_b32 v230, a112\n\t"
"v_accvgpr_read_b32 v232, a116\n\t"
"v_mfma_f32_16x16x32_bf16 v[32:35], a[224:227], a[172:175], v[32:35]\n\t"
"buffer_load_dwordx4 a[220:223], off, s[80:83], s20 offset:1024 sc1\n\t"
"v_accvgpr_read_b32 v231, a113\n\t"
"v_accvgpr_read_b32 v233, a117\n\t"
"v_cvt_pk_bf16_f32 v224, v224, v225\n\t"
"v_cvt_pk_bf16_f32 v228, v228, v229\n\t"
"v_pk_fma_f32 v[230:231], v[232:233], s[56:57], v[230:231]\n\t"
"v_pk_fma_f32 v[232:233], v[230:231], s[58:59], v[232:233] neg_lo:[0,0,1] neg_hi:[0,0,1]\n\t"
"v_pk_fma_f32 v[234:235], v[68:69], s[60:61], v[230:231]\n\t"
"v_pk_fma_f32 v[68:69], v[230:231], s[60:61], v[68:69]\n\t"
"v_pk_fma_f32 v[230:231], v[168:169], s[62:63], v[232:233]\n\t"
"v_pk_fma_f32 v[168:169], v[232:233], s[62:63], v[168:169]\n\t"
"v_mfma_f32_16x16x32_bf16 v[36:39], a[208:211], a[172:175], v[36:39]\n\t"
"v_accvgpr_write_b32 a105, v224\n\t"
"v_accvgpr_write_b32 a107, v228\n\t"
"v_mfma_f32_16x16x32_bf16 v[36:39], a[224:227], a[176:179], v[36:39]\n\t"
"v_accvgpr_read_b32 v236, a114\n\t"
"v_accvgpr_read_b32 v238, a118\n\t"
"v_mfma_f32_16x16x32_bf16 v[36:39], a[252:255], a[180:183], v[36:39]\n\t"
"buffer_load_dwordx4 a[248:251], off, s[80:83], s20 offset:2048 sc1\n\t"
"v_accvgpr_read_b32 v237, a115\n\t"
"v_accvgpr_read_b32 v239, a119\n\t"
"v_cvt_pk_bf16_f32 v230, v230, v231\n\t"
"v_cvt_pk_bf16_f32 v234, v234, v235\n\t"
"v_pk_fma_f32 v[236:237], v[238:239], s[56:57], v[236:237]\n\t"
"v_pk_fma_f32 v[238:239], v[236:237], s[58:59], v[238:239] neg_lo:[0,0,1] neg_hi:[0,0,1]\n\t"
"v_pk_fma_f32 v[98:99], v[70:71], s[60:61], v[236:237]\n\t"
"v_pk_fma_f32 v[70:71], v[236:237], s[60:61], v[70:71]\n\t"
"v_pk_fma_f32 v[236:237], v[170:171], s[62:63], v[238:239]\n\t"
"v_pk_fma_f32 v[170:171], v[238:239], s[62:63], v[170:171]\n\t"
"v_mfma_f32_16x16x32_bf16 v[40:43], a[252:255], a[184:187], v[40:43]\n\t"
"s_mov_b32 s20, 614400\n\t"
"v_accvgpr_write_b32 a112, v230\n\t"
"v_accvgpr_write_b32 a114, v234\n\t"
"v_mfma_f32_16x16x32_bf16 v[40:43], a[224:227], a[180:183], v[40:43]\n\t"
"v_accvgpr_read_b32 v224, a120\n\t"
"v_accvgpr_read_b32 v226, a124\n\t"
"v_mfma_f32_16x16x32_bf16 v[40:43], a[208:211], a[176:179], v[40:43]\n\t"
"buffer_load_dwordx4 a[84:87], off, s[80:83], s20 offset:0 sc1\n\t"
"v_accvgpr_read_b32 v225, a121\n\t"
"v_accvgpr_read_b32 v227, a125\n\t"
"v_cvt_pk_bf16_f32 v236, v236, v237\n\t"
"v_cvt_pk_bf16_f32 v98, v98, v99\n\t"
"v_pk_fma_f32 v[224:225], v[226:227], s[56:57], v[224:225]\n\t"
"v_pk_fma_f32 v[226:227], v[224:225], s[58:59], v[226:227] neg_lo:[0,0,1] neg_hi:[0,0,1]\n\t"
"v_pk_fma_f32 v[228:229], v[72:73], s[60:61], v[224:225]\n\t"
"v_pk_fma_f32 v[72:73], v[224:225], s[60:61], v[72:73]\n\t"
"v_pk_fma_f32 v[224:225], v[172:173], s[62:63], v[226:227]\n\t"
"v_pk_fma_f32 v[172:173], v[226:227], s[62:63], v[172:173]\n\t"
"v_mfma_f32_16x16x32_bf16 v[44:47], a[208:211], a[180:183], v[44:47]\n\t"
"v_accvgpr_write_b32 a113, v236\n\t"
"v_accvgpr_write_b32 a115, v98\n\t"
"v_mfma_f32_16x16x32_bf16 v[44:47], a[224:227], a[184:187], v[44:47]\n\t"
"v_accvgpr_read_b32 v230, a122\n\t"
"v_accvgpr_read_b32 v232, a126\n\t"
"v_mfma_f32_16x16x32_bf16 v[44:47], a[252:255], a[188:191], v[44:47]\n\t"
"buffer_load_dwordx4 a[88:91], off, s[80:83], s20 offset:1024 sc1\n\t"
"v_accvgpr_read_b32 v231, a123\n\t"
"v_accvgpr_read_b32 v233, a127\n\t"
"v_accvgpr_read_b32 v236, a128\n\t"
"v_accvgpr_read_b32 v238, a132\n\t"
"v_accvgpr_read_b32 v237, a129\n\t"
"v_accvgpr_read_b32 v239, a133\n\t"
"v_cvt_pk_bf16_f32 v224, v224, v225\n\t"
"v_cvt_pk_bf16_f32 v228, v228, v229\n\t"
"v_pk_fma_f32 v[230:231], v[232:233], s[56:57], v[230:231]\n\t"
"v_pk_fma_f32 v[232:233], v[230:231], s[58:59], v[232:233] neg_lo:[0,0,1] neg_hi:[0,0,1]\n\t"
"v_pk_fma_f32 v[234:235], v[74:75], s[60:61], v[230:231]\n\t"
"v_pk_fma_f32 v[74:75], v[230:231], s[60:61], v[74:75]\n\t"
"v_pk_fma_f32 v[230:231], v[174:175], s[62:63], v[232:233]\n\t"
"v_pk_fma_f32 v[174:175], v[232:233], s[62:63], v[174:175]\n\t"
"v_pk_fma_f32 v[236:237], v[238:239], s[56:57], v[236:237]\n\t"
"v_pk_fma_f32 v[238:239], v[236:237], s[58:59], v[238:239] neg_lo:[0,0,1] neg_hi:[0,0,1]\n\t"
"v_pk_fma_f32 v[98:99], v[76:77], s[60:61], v[236:237]\n\t"
"v_pk_fma_f32 v[76:77], v[236:237], s[60:61], v[76:77]\n\t"
"v_pk_fma_f32 v[236:237], v[176:177], s[62:63], v[238:239]\n\t"
"v_pk_fma_f32 v[176:177], v[238:239], s[62:63], v[176:177]\n\t"
"v_mfma_f32_16x16x32_bf16 v[48:51], a[252:255], a[192:195], v[48:51]\n\t"
"v_accvgpr_write_b32 a120, v224\n\t"
"v_accvgpr_write_b32 a122, v228\n\t"
"v_mfma_f32_16x16x32_bf16 v[48:51], a[224:227], a[188:191], v[48:51]\n\t"
"v_accvgpr_read_b32 v224, a130\n\t"
"v_accvgpr_read_b32 v226, a134\n\t"
"v_mfma_f32_16x16x32_bf16 v[48:51], a[208:211], a[184:187], v[48:51]\n\t"
"buffer_load_dwordx4 a[92:95], off, s[80:83], s20 offset:2048 sc1\n\t"
"v_accvgpr_read_b32 v225, a131\n\t"
"v_accvgpr_read_b32 v227, a135\n\t"
"v_cvt_pk_bf16_f32 v230, v230, v231\n\t"
"v_cvt_pk_bf16_f32 v234, v234, v235\n\t"
"v_cvt_pk_bf16_f32 v236, v236, v237\n\t"
"v_cvt_pk_bf16_f32 v98, v98, v99\n\t"
"v_pk_fma_f32 v[224:225], v[226:227], s[56:57], v[224:225]\n\t"
"v_pk_fma_f32 v[226:227], v[224:225], s[58:59], v[226:227] neg_lo:[0,0,1] neg_hi:[0,0,1]\n\t"
"v_pk_fma_f32 v[228:229], v[78:79], s[60:61], v[224:225]\n\t"
"v_pk_fma_f32 v[78:79], v[224:225], s[60:61], v[78:79]\n\t"
"v_pk_fma_f32 v[224:225], v[178:179], s[62:63], v[226:227]\n\t"
"v_pk_fma_f32 v[178:179], v[226:227], s[62:63], v[178:179]\n\t"
"v_mfma_f32_16x16x32_bf16 v[52:55], a[208:211], a[188:191], v[52:55]\n\t"
"s_mov_b32 s20, 774144\n\t"
"v_accvgpr_write_b32 a121, v230\n\t"
"v_accvgpr_write_b32 a123, v234\n\t"
"v_mfma_f32_16x16x32_bf16 v[52:55], a[224:227], a[192:195], v[52:55]\n\t"
"v_accvgpr_write_b32 a128, v236\n\t"
"v_accvgpr_write_b32 a130, v98\n\t"
"v_mfma_f32_16x16x32_bf16 v[52:55], a[252:255], a[196:199], v[52:55]\n\t"
"ds_write_b128 v245, v[196:199] offset:0\n\t"
"v_accvgpr_read_b32 v230, a136\n\t"
"v_accvgpr_read_b32 v232, a140\n\t"
"v_accvgpr_read_b32 v231, a137\n\t"
"v_accvgpr_read_b32 v233, a141\n\t"
"v_cvt_pk_bf16_f32 v224, v224, v225\n\t"
"v_cvt_pk_bf16_f32 v228, v228, v229\n\t"
"v_pk_fma_f32 v[230:231], v[232:233], s[56:57], v[230:231]\n\t"
"v_pk_fma_f32 v[232:233], v[230:231], s[58:59], v[232:233] neg_lo:[0,0,1] neg_hi:[0,0,1]\n\t"
"v_pk_fma_f32 v[234:235], v[80:81], s[60:61], v[230:231]\n\t"
"v_pk_fma_f32 v[80:81], v[230:231], s[60:61], v[80:81]\n\t"
"v_pk_fma_f32 v[230:231], v[180:181], s[62:63], v[232:233]\n\t"
"v_pk_fma_f32 v[180:181], v[232:233], s[62:63], v[180:181]\n\t"
"v_mfma_f32_16x16x32_bf16 v[56:59], a[252:255], a[200:203], v[56:59]\n\t"
"v_accvgpr_write_b32 a129, v224\n\t"
"v_accvgpr_write_b32 a131, v228\n\t"
"v_mfma_f32_16x16x32_bf16 v[56:59], a[224:227], a[196:199], v[56:59]\n\t"
"v_accvgpr_read_b32 v236, a138\n\t"
"v_accvgpr_read_b32 v238, a142\n\t"
"v_mfma_f32_16x16x32_bf16 v[56:59], a[208:211], a[192:195], v[56:59]\n\t"
"buffer_load_dwordx4 a[96:99], off, s[80:83], s20 offset:0 sc1\n\t"
"v_accvgpr_read_b32 v237, a139\n\t"
"v_accvgpr_read_b32 v239, a143\n\t"
"v_cvt_pk_bf16_f32 v230, v230, v231\n\t"
"v_cvt_pk_bf16_f32 v234, v234, v235\n\t"
"v_pk_fma_f32 v[236:237], v[238:239], s[56:57], v[236:237]\n\t"
"v_pk_fma_f32 v[238:239], v[236:237], s[58:59], v[238:239] neg_lo:[0,0,1] neg_hi:[0,0,1]\n\t"
"v_pk_fma_f32 v[98:99], v[82:83], s[60:61], v[236:237]\n\t"
"v_pk_fma_f32 v[82:83], v[236:237], s[60:61], v[82:83]\n\t"
"v_pk_fma_f32 v[236:237], v[182:183], s[62:63], v[238:239]\n\t"
"v_pk_fma_f32 v[182:183], v[238:239], s[62:63], v[182:183]\n\t"
"v_mfma_f32_16x16x32_bf16 v[60:63], a[208:211], a[196:199], v[60:63]\n\t"
"v_accvgpr_write_b32 a136, v230\n\t"
"v_accvgpr_write_b32 a138, v234\n\t"
"v_mfma_f32_16x16x32_bf16 v[60:63], a[224:227], a[200:203], v[60:63]\n\t"
"v_accvgpr_read_b32 v224, a144\n\t"
"v_accvgpr_read_b32 v226, a148\n\t"
"v_mfma_f32_16x16x32_bf16 v[60:63], a[252:255], a[204:207], v[60:63]\n\t"
"ds_write_b128 v245, v[204:207] offset:18432\n\t"
"v_accvgpr_read_b32 v225, a145\n\t"
"v_accvgpr_read_b32 v227, a149\n\t"
"v_cvt_pk_bf16_f32 v236, v236, v237\n\t"
"v_cvt_pk_bf16_f32 v98, v98, v99\n\t"
"v_pk_fma_f32 v[224:225], v[226:227], s[56:57], v[224:225]\n\t"
"v_pk_fma_f32 v[226:227], v[224:225], s[58:59], v[226:227] neg_lo:[0,0,1] neg_hi:[0,0,1]\n\t"
"v_pk_fma_f32 v[228:229], v[84:85], s[60:61], v[224:225]\n\t"
"v_pk_fma_f32 v[84:85], v[224:225], s[60:61], v[84:85]\n\t"
"v_pk_fma_f32 v[224:225], v[184:185], s[62:63], v[226:227]\n\t"
"v_pk_fma_f32 v[184:185], v[226:227], s[62:63], v[184:185]\n\t"
"v_mfma_f32_16x16x32_bf16 v[64:67], a[228:231], a[168:171], v[64:67]\n\t"
"ds_read_b128 a[168:171], v244 offset:9216\n\t"
"v_accvgpr_write_b32 a137, v236\n\t"
"v_mfma_f32_16x16x32_bf16 v[64:67], a[232:235], a[176:179], v[64:67]\n\t"
"v_accvgpr_write_b32 a139, v98\n\t"
"v_accvgpr_read_b32 v230, a146\n\t"
"v_mfma_f32_16x16x32_bf16 v[64:67], v[240:243], a[172:175], v[64:67]\n\t"
"buffer_load_dwordx4 a[100:103], off, s[80:83], s20 offset:1024 sc1\n\t"
"v_accvgpr_read_b32 v232, a150\n\t"
"v_accvgpr_read_b32 v231, a147\n\t"
"v_accvgpr_read_b32 v233, a151\n\t"
"v_cvt_pk_bf16_f32 v224, v224, v225\n\t"
"v_cvt_pk_bf16_f32 v228, v228, v229\n\t"
"v_pk_fma_f32 v[230:231], v[232:233], s[56:57], v[230:231]\n\t"
"v_pk_fma_f32 v[232:233], v[230:231], s[58:59], v[232:233] neg_lo:[0,0,1] neg_hi:[0,0,1]\n\t"
"v_pk_fma_f32 v[234:235], v[86:87], s[60:61], v[230:231]\n\t"
"v_pk_fma_f32 v[86:87], v[230:231], s[60:61], v[86:87]\n\t"
"v_pk_fma_f32 v[230:231], v[186:187], s[62:63], v[232:233]\n\t"
"v_pk_fma_f32 v[186:187], v[232:233], s[62:63], v[186:187]\n\t"
"v_mfma_f32_16x16x32_bf16 v[68:71], a[228:231], a[172:175], v[68:71]\n\t"
"ds_read_b128 a[172:175], v244 offset:9728\n\t"
"v_accvgpr_write_b32 a144, v224\n\t"
"v_mfma_f32_16x16x32_bf16 v[68:71], v[240:243], a[176:179], v[68:71]\n\t"
"v_accvgpr_write_b32 a146, v228\n\t"
"v_accvgpr_read_b32 v236, a152\n\t"
"v_mfma_f32_16x16x32_bf16 v[68:71], a[232:235], a[180:183], v[68:71]\n\t"
"ds_write_b64 v246, a[104:105] offset:0\n\t"
"v_accvgpr_read_b32 v238, a156\n\t"
"v_accvgpr_read_b32 v237, a153\n\t"
"v_accvgpr_read_b32 v239, a157\n\t"
"v_cvt_pk_bf16_f32 v230, v230, v231\n\t"
"v_cvt_pk_bf16_f32 v234, v234, v235\n\t"
"v_pk_fma_f32 v[236:237], v[238:239], s[56:57], v[236:237]\n\t"
"v_pk_fma_f32 v[238:239], v[236:237], s[58:59], v[238:239] neg_lo:[0,0,1] neg_hi:[0,0,1]\n\t"
"v_pk_fma_f32 v[98:99], v[88:89], s[60:61], v[236:237]\n\t"
"v_pk_fma_f32 v[88:89], v[236:237], s[60:61], v[88:89]\n\t"
"v_pk_fma_f32 v[236:237], v[188:189], s[62:63], v[238:239]\n\t"
"v_pk_fma_f32 v[188:189], v[238:239], s[62:63], v[188:189]\n\t"
"v_mfma_f32_16x16x32_bf16 v[72:75], a[232:235], a[184:187], v[72:75]\n\t"
"v_accvgpr_write_b32 a145, v230\n\t"
"v_accvgpr_write_b32 a147, v234\n\t"
"v_mfma_f32_16x16x32_bf16 v[72:75], v[240:243], a[180:183], v[72:75]\n\t"
"v_accvgpr_read_b32 v224, a154\n\t"
"v_accvgpr_read_b32 v226, a158\n\t"
"v_mfma_f32_16x16x32_bf16 v[72:75], a[228:231], a[176:179], v[72:75]\n\t"
"buffer_load_dwordx4 a[208:211], off, s[80:83], s20 offset:2048 sc1\n\t"
"ds_read_b128 a[176:179], v244 offset:10240\n\t"
"v_accvgpr_read_b32 v225, a155\n\t"
"v_accvgpr_read_b32 v227, a159\n\t"
"v_cvt_pk_bf16_f32 v236, v236, v237\n\t"
"v_cvt_pk_bf16_f32 v98, v98, v99\n\t"
"s_mov_b32 s20, 786432\n\t"
"v_pk_fma_f32 v[224:225], v[226:227], s[56:57], v[224:225]\n\t"
"v_pk_fma_f32 v[226:227], v[224:225], s[58:59], v[226:227] neg_lo:[0,0,1] neg_hi:[0,0,1]\n\t"
"v_pk_fma_f32 v[228:229], v[90:91], s[60:61], v[224:225]\n\t"
"v_pk_fma_f32 v[90:91], v[224:225], s[60:61], v[90:91]\n\t"
"v_pk_fma_f32 v[224:225], v[190:191], s[62:63], v[226:227]\n\t"
"v_pk_fma_f32 v[190:191], v[226:227], s[62:63], v[190:191]\n\t"
"v_mfma_f32_16x16x32_bf16 v[76:79], a[228:231], a[180:183], v[76:79]\n\t"
"ds_read_b128 a[180:183], v244 offset:10752\n\t"
"v_accvgpr_write_b32 a152, v236\n\t"
"v_mfma_f32_16x16x32_bf16 v[76:79], v[240:243], a[184:187], v[76:79]\n\t"
"v_accvgpr_write_b32 a154, v98\n\t"
"v_accvgpr_read_b32 v230, a160\n\t"
"v_mfma_f32_16x16x32_bf16 v[76:79], a[232:235], a[188:191], v[76:79]\n\t"
"ds_write_b64 v246, a[112:113] offset:18432\n\t"
"v_accvgpr_read_b32 v232, a164\n\t"
"v_accvgpr_read_b32 v231, a161\n\t"
"v_accvgpr_read_b32 v233, a165\n\t"
"v_cvt_pk_bf16_f32 v224, v224, v225\n\t"
"v_cvt_pk_bf16_f32 v228, v228, v229\n\t"
"v_pk_fma_f32 v[230:231], v[232:233], s[56:57], v[230:231]\n\t"
"v_pk_fma_f32 v[232:233], v[230:231], s[58:59], v[232:233] neg_lo:[0,0,1] neg_hi:[0,0,1]\n\t"
"v_pk_fma_f32 v[234:235], v[92:93], s[60:61], v[230:231]\n\t"
"v_pk_fma_f32 v[92:93], v[230:231], s[60:61], v[92:93]\n\t"
"v_pk_fma_f32 v[230:231], v[192:193], s[62:63], v[232:233]\n\t"
"v_pk_fma_f32 v[192:193], v[232:233], s[62:63], v[192:193]\n\t"
"v_mfma_f32_16x16x32_bf16 v[80:83], a[232:235], a[192:195], v[80:83]\n\t"
"v_accvgpr_write_b32 a153, v224\n\t"
"v_accvgpr_write_b32 a155, v228\n\t"
"v_mfma_f32_16x16x32_bf16 v[80:83], v[240:243], a[188:191], v[80:83]\n\t"
"v_accvgpr_read_b32 v236, a162\n\t"
"v_accvgpr_read_b32 v238, a166\n\t"
"v_mfma_f32_16x16x32_bf16 v[80:83], a[228:231], a[184:187], v[80:83]\n\t"
"buffer_load_dwordx4 a[224:227], off, s[80:83], s20 offset:0 sc1\n\t"
"ds_read_b128 a[184:187], v244 offset:11264\n\t"
"v_accvgpr_read_b32 v237, a163\n\t"
"v_accvgpr_read_b32 v239, a167\n\t"
"v_cvt_pk_bf16_f32 v230, v230, v231\n\t"
"v_cvt_pk_bf16_f32 v234, v234, v235\n\t"
"v_pk_fma_f32 v[236:237], v[238:239], s[56:57], v[236:237]\n\t"
"v_pk_fma_f32 v[238:239], v[236:237], s[58:59], v[238:239] neg_lo:[0,0,1] neg_hi:[0,0,1]\n\t"
"v_pk_fma_f32 v[98:99], v[94:95], s[60:61], v[236:237]\n\t"
"v_pk_fma_f32 v[94:95], v[236:237], s[60:61], v[94:95]\n\t"
"v_pk_fma_f32 v[236:237], v[194:195], s[62:63], v[238:239]\n\t"
"v_pk_fma_f32 v[194:195], v[238:239], s[62:63], v[194:195]\n\t"
"v_mfma_f32_16x16x32_bf16 v[84:87], a[228:231], a[188:191], v[84:87]\n\t"
"ds_read_b128 a[188:191], v244 offset:11776\n\t"
"v_accvgpr_write_b32 a160, v230\n\t"
"v_mfma_f32_16x16x32_bf16 v[84:87], v[240:243], a[192:195], v[84:87]\n\t"
"v_accvgpr_write_b32 a162, v234\n\t"
"v_cvt_pk_bf16_f32 v236, v236, v237\n\t"
"v_mfma_f32_16x16x32_bf16 v[84:87], a[232:235], a[196:199], v[84:87]\n\t"
"buffer_load_dwordx4 a[252:255], off, s[80:83], s20 offset:1024 sc1\n\t"
"v_cvt_pk_bf16_f32 v98, v98, v99\n\t"
"v_mfma_f32_16x16x32_bf16 v[88:91], a[232:235], a[200:203], v[88:91]\n\t"
"v_accvgpr_write_b32 a161, v236\n\t"
"v_accvgpr_write_b32 a163, v98\n\t"
"v_mfma_f32_16x16x32_bf16 v[88:91], v[240:243], a[196:199], v[88:91]\n\t"
"v_mfma_f32_16x16x32_bf16 v[88:91], a[228:231], a[192:195], v[88:91]\n\t"
"buffer_load_dwordx4 a[108:111], off, s[80:83], s20 offset:2048 sc1\n\t"
"ds_write_b128 v245, v[200:203] offset:9216\n\t"
"ds_write_b128 v245, v[208:211] offset:27648\n\t"
"s_mov_b32 s20, 798720\n\t"
"v_mfma_f32_16x16x32_bf16 v[92:95], a[228:231], a[196:199], v[92:95]\n\t"
"buffer_load_dwordx4 a[116:119], off, s[80:83], s20 offset:0 sc1\n\t"
"v_mfma_f32_16x16x32_bf16 v[92:95], v[240:243], a[200:203], v[92:95]\n\t"
"buffer_load_dwordx4 a[124:127], off, s[80:83], s20 offset:1024 sc1\n\t"
"v_mfma_f32_16x16x32_bf16 v[92:95], a[232:235], a[204:207], v[92:95]\n\t"
"s_waitcnt vmcnt(23) lgkmcnt(6)\n\t"
"buffer_load_dwordx4 a[132:135], off, s[80:83], s20 offset:2048 sc1\n\t"
"s_mov_b32 s20, 1179648\n\t"
"v_mfma_f32_16x16x32_bf16 v[100:103], a[236:239], a[168:171], v[100:103]\n\t"
"s_waitcnt lgkmcnt(5)\n\t"
"ds_read_b128 a[192:195], v244 offset:12288\n\t"
"v_mfma_f32_16x16x32_bf16 v[100:103], a[56:59], a[176:179], v[100:103]\n\t"
"buffer_load_dwordx4 a[140:143], off, s[80:83], s20 offset:0 sc1\n\t"
"v_mfma_f32_16x16x32_bf16 v[100:103], a[52:55], a[172:175], v[100:103]\n\t"
"ds_write_b64 v246, a[106:107] offset:9216\n\t"
"ds_write_b64 v246, a[114:115] offset:27648\n\t"
"v_mfma_f32_16x16x32_bf16 v[104:107], a[236:239], a[172:175], v[104:107]\n\t"
"s_waitcnt lgkmcnt(6)\n\t"
"ds_read_b128 a[196:199], v244 offset:12800\n\t"
"v_mfma_f32_16x16x32_bf16 v[104:107], a[52:55], a[176:179], v[104:107]\n\t"
"ds_read_b128 a[200:203], v244 offset:13312\n\t"
"v_mfma_f32_16x16x32_bf16 v[104:107], a[56:59], a[180:183], v[104:107]\n\t"
"ds_read_b128 a[204:207], v244 offset:13824\n\t"
"s_waitcnt lgkmcnt(0)\n\t"
"s_barrier\n\t"
"v_mfma_f32_16x16x32_bf16 v[108:111], a[56:59], a[184:187], v[108:111]\n\t"
"ds_read_b128 a[40:43], v255 offset:0\n\t"
"v_mfma_f32_16x16x32_bf16 v[108:111], a[52:55], a[180:183], v[108:111]\n\t"
"buffer_load_dwordx4 a[228:231], off, s[80:83], s20 offset:1024 sc1\n\t"
"v_mfma_f32_16x16x32_bf16 v[108:111], a[236:239], a[176:179], v[108:111]\n\t"
"buffer_load_dwordx4 a[232:235], off, s[80:83], s20 offset:2048 sc1\n\t"
"s_mov_b32 s20, 1474560\n\t"
"v_mfma_f32_16x16x32_bf16 v[112:115], a[236:239], a[180:183], v[112:115]\n\t"
"buffer_load_dwordx4 a[148:151], off, s[80:83], s20 offset:0 sc1\n\t"
"v_mfma_f32_16x16x32_bf16 v[112:115], a[52:55], a[184:187], v[112:115]\n\t"
"s_add_u32 s29, s22, 384\n\t"
"ds_read_b128 a[44:47], v255 offset:1536\n\t"
"v_mfma_f32_16x16x32_bf16 v[112:115], a[56:59], a[188:191], v[112:115]\n\t"
"buffer_load_dwordx4 a[104:107], off, s[80:83], s20 offset:1024 sc1\n\t"
"v_mfma_f32_16x16x32_bf16 v[116:119], a[56:59], a[192:195], v[116:119]\n\t"
"s_waitcnt lgkmcnt(1)\n\t"
"ds_read_b128 a[48:51], v255 offset:3072\n\t"
"v_mfma_f32_16x16x32_bf16 v[116:119], a[52:55], a[188:191], v[116:119]\n\t"
"buffer_load_dwordx4 a[112:115], off, s[80:83], s20 offset:2048 sc1\n\t"
"v_mfma_f32_16x16x32_bf16 v[116:119], a[236:239], a[184:187], v[116:119]\n\t"
"s_mov_b32 s20, 1486848\n\t"
"ds_read_b128 v[196:199], v255 offset:18432\n\t"
"v_mfma_f32_16x16x32_bf16 v[120:123], a[236:239], a[188:191], v[120:123]\n\t"
"buffer_load_dwordx4 a[156:159], off, s[80:83], s20 offset:0 sc1\n\t"
"v_mfma_f32_16x16x32_bf16 v[120:123], a[52:55], a[192:195], v[120:123]\n\t"
"buffer_load_dwordx4 a[164:167], off, s[80:83], s20 offset:1024 sc1\n\t"
"v_mfma_f32_16x16x32_bf16 v[120:123], a[56:59], a[196:199], v[120:123]\n\t"
"s_waitcnt lgkmcnt(2)\n\t"
"ds_write_b128 v245, v[212:215] offset:36864\n\t"
"v_mfma_f32_16x16x32_bf16 v[124:127], a[56:59], a[200:203], v[124:127]\n\t"
"s_waitcnt lgkmcnt(2)\n\t"
"v_mfma_f32_16x16x32_bf16 v[124:127], a[52:55], a[196:199], v[124:127]\n\t"
"buffer_load_dwordx4 v[240:243], off, s[80:83], s20 offset:2048 sc1\n\t"
"v_mfma_f32_16x16x32_bf16 v[124:127], a[236:239], a[192:195], v[124:127]\n\t"
"s_mov_b32 s20, 1191936\n\t"
"ds_write_b128 v249, a[0:3] offset:0\n\t"
"v_mfma_f32_16x16x32_bf16 v[128:131], a[236:239], a[196:199], v[128:131]\n\t"
"s_waitcnt vmcnt(30)\n\t"
"v_mfma_f32_16x16x32_bf16 v[128:131], a[52:55], a[200:203], v[128:131]\n\t"
"buffer_load_dwordx4 a[236:239], off, s[80:83], s20 offset:0 sc1\n\t"
"v_mfma_f32_16x16x32_bf16 v[128:131], a[56:59], a[204:207], v[128:131]\n\t"
"ds_write_b128 v249, a[8:11] offset:18432\n\t"
"v_mfma_f32_16x16x32_bf16 v[132:135], a[60:63], a[168:171], v[132:135]\n\t"
"s_waitcnt lgkmcnt(3)\n\t"
"v_mfma_f32_16x16x32_bf16 v[132:135], a[240:243], a[176:179], v[132:135]\n\t"
"ds_read_b128 v[200:203], v255 offset:19968\n\t"
"v_mfma_f32_16x16x32_bf16 v[132:135], a[212:215], a[172:175], v[132:135]\n\t"
"ds_write_b128 v249, a[16:19] offset:36864\n\t"
"buffer_store_dwordx4 a[40:43], v248, s[16:19], s29 offen offset:0 nt\n\t"
"v_mfma_f32_16x16x32_bf16 v[136:139], a[60:63], a[172:175], v[136:139]\n\t"
"ds_read_b128 v[204:207], v255 offset:21504\n\t"
"v_mfma_f32_16x16x32_bf16 v[136:139], a[212:215], a[176:179], v[136:139]\n\t"
"buffer_load_dwordx4 a[40:43], off, s[80:83], s20 offset:1024 sc1\n\t"
"v_mfma_f32_16x16x32_bf16 v[136:139], a[240:243], a[180:183], v[136:139]\n\t"
"buffer_store_dwordx4 a[44:47], v248, s[16:19], s29 offen offset:128 nt\n\t"
"ds_write_b64 v246, a[120:121] offset:36864\n\t"
"v_mfma_f32_16x16x32_bf16 v[140:143], a[240:243], a[184:187], v[140:143]\n\t"
"v_mfma_f32_16x16x32_bf16 v[140:143], a[212:215], a[180:183], v[140:143]\n\t"
"buffer_load_dwordx4 a[44:47], off, s[80:83], s20 offset:2048 sc1\n\t"
"v_mfma_f32_16x16x32_bf16 v[140:143], a[60:63], a[176:179], v[140:143]\n\t"
"buffer_store_dwordx4 a[48:51], v248, s[16:19], s29 offen offset:256 nt\n\t"
"ds_write_b64 v250, a[128:129] offset:0\n\t"
"s_add_u32 s29, s22, 196992\n\t"
"s_mov_b32 s20, 1204224\n\t"
"v_mfma_f32_16x16x32_bf16 v[144:147], a[60:63], a[180:183], v[144:147]\n\t"
"s_waitcnt lgkmcnt(4)\n\t"
"v_mfma_f32_16x16x32_bf16 v[144:147], a[212:215], a[184:187], v[144:147]\n\t"
"buffer_load_dwordx4 a[48:51], off, s[80:83], s20 offset:0 sc1\n\t"
"v_mfma_f32_16x16x32_bf16 v[144:147], a[240:243], a[188:191], v[144:147]\n\t"
"ds_write_b64 v250, a[136:137] offset:18432\n\t"
"ds_write_b64 v250, a[144:145] offset:36864\n\t"
"v_mfma_f32_16x16x32_bf16 v[148:151], a[240:243], a[192:195], v[148:151]\n\t"
"buffer_load_dwordx4 a[52:55], off, s[80:83], s20 offset:1024 sc1\n\t"
"v_mfma_f32_16x16x32_bf16 v[148:151], a[212:215], a[188:191], v[148:151]\n\t"
"v_mfma_f32_16x16x32_bf16 v[148:151], a[60:63], a[184:187], v[148:151]\n\t"
"buffer_store_dwordx4 v[196:199], v248, s[16:19], s29 offen offset:0 nt\n\t"
"ds_write_b64 v246, a[122:123] offset:46080\n\t"
"v_mfma_f32_16x16x32_bf16 v[152:155], a[60:63], a[188:191], v[152:155]\n\t"
"v_mfma_f32_16x16x32_bf16 v[152:155], a[212:215], a[192:195], v[152:155]\n\t"
"v_mfma_f32_16x16x32_bf16 v[152:155], a[240:243], a[196:199], v[152:155]\n\t"
"buffer_store_dwordx4 v[200:203], v248, s[16:19], s29 offen offset:128 nt\n\t"
"ds_write_b64 v250, a[130:131] offset:9216\n\t"
"v_mfma_f32_16x16x32_bf16 v[156:159], a[240:243], a[200:203], v[156:159]\n\t"
"s_waitcnt lgkmcnt(6)\n\t"
"v_mfma_f32_16x16x32_bf16 v[156:159], a[212:215], a[196:199], v[156:159]\n\t"
"buffer_load_dwordx4 a[56:59], off, s[80:83], s20 offset:2048 sc1\n\t"
"v_mfma_f32_16x16x32_bf16 v[156:159], a[60:63], a[192:195], v[156:159]\n\t"
"s_mov_b32 s20, 1499136\n\t"
"ds_write_b128 v245, v[216:219] offset:46080\n\t"
"v_mfma_f32_16x16x32_bf16 v[160:163], a[60:63], a[196:199], v[160:163]\n\t"
"s_waitcnt vmcnt(38)\n\t"
"v_mfma_f32_16x16x32_bf16 v[160:163], a[212:215], a[200:203], v[160:163]\n\t"
"v_mfma_f32_16x16x32_bf16 v[160:163], a[240:243], a[204:207], v[160:163]\n\t"
"ds_write_b128 v249, a[4:7] offset:9216\n\t"
"ds_write_b128 v249, a[12:15] offset:27648\n\t"
"ds_write_b128 v249, a[20:23] offset:46080\n\t"
"v_mfma_f32_16x16x32_bf16 v[164:167], a[244:247], a[168:171], v[164:167]\n\t"
"v_mfma_f32_16x16x32_bf16 v[164:167], a[68:71], a[176:179], v[164:167]\n\t"
"ds_read_b128 a[168:171], v244 offset:24576\n\t"
"v_mfma_f32_16x16x32_bf16 v[164:167], a[64:67], a[172:175], v[164:167]\n\t"
"buffer_store_dwordx4 v[204:207], v248, s[16:19], s29 offen offset:256 nt\n\t"
"ds_write_b64 v250, a[138:139] offset:27648\n\t"
"v_mfma_f32_16x16x32_bf16 v[168:171], a[244:247], a[172:175], v[168:171]\n\t"
"ds_read_b128 a[172:175], v244 offset:25088\n\t"
"v_mfma_f32_16x16x32_bf16 v[168:171], a[64:67], a[176:179], v[168:171]\n\t"
"buffer_load_dwordx4 a[212:215], off, s[80:83], s20 offset:0 sc1\n\t"
"v_mfma_f32_16x16x32_bf16 v[168:171], a[68:71], a[180:183], v[168:171]\n\t"
"ds_write_b64 v250, a[146:147] offset:46080\n\t"
"v_mfma_f32_16x16x32_bf16 v[172:175], a[68:71], a[184:187], v[172:175]\n\t"
"buffer_load_dwordx4 a[240:243], off, s[80:83], s20 offset:1024 sc1\n\t"
"v_mfma_f32_16x16x32_bf16 v[172:175], a[64:67], a[180:183], v[172:175]\n\t"
"v_mfma_f32_16x16x32_bf16 v[172:175], a[244:247], a[176:179], v[172:175]\n\t"
"Lpg24_%=:\n\t"
"s_cbranch_vccnz Lvr3_%=\n\t"
"ds_read_b128 a[176:179], v244 offset:25600\n\t"
"v_mfma_f32_16x16x32_bf16 v[176:179], a[244:247], a[180:183], v[176:179]\n\t"
"ds_read_b128 a[180:183], v244 offset:26112\n\t"
"v_mfma_f32_16x16x32_bf16 v[176:179], a[64:67], a[184:187], v[176:179]\n\t"
"v_mfma_f32_16x16x32_bf16 v[176:179], a[68:71], a[188:191], v[176:179]\n\t"
"s_waitcnt lgkmcnt(2)\n\t"
"s_barrier\n\t"
"v_mfma_f32_16x16x32_bf16 v[180:183], a[68:71], a[192:195], v[180:183]\n\t"
"ds_read_b128 v[196:199], v255 offset:36864\n\t"
"v_mfma_f32_16x16x32_bf16 v[180:183], a[64:67], a[188:191], v[180:183]\n\t"
"buffer_load_dwordx4 a[60:63], off, s[80:83], s20 offset:2048 sc1\n\t"
"v_mfma_f32_16x16x32_bf16 v[180:183], a[244:247], a[184:187], v[180:183]\n\t"
"s_mov_b32 s20, 1363968\n\t"
"ds_read_b128 v[200:203], v255 offset:38400\n\t"
"v_mfma_f32_16x16x32_bf16 v[184:187], a[244:247], a[188:191], v[184:187]\n\t"
"s_add_u32 s29, s22, 393600\n\t"
"ds_read_b128 a[184:187], v244 offset:26624\n\t"
"v_mfma_f32_16x16x32_bf16 v[184:187], a[64:67], a[192:195], v[184:187]\n\t"
"buffer_load_dwordx4 a[16:19], off, s[80:83], s20 offset:0 sc1\n\t"
"v_mfma_f32_16x16x32_bf16 v[184:187], a[68:71], a[196:199], v[184:187]\n\t"
"ds_read_b128 v[204:207], v255 offset:39936\n\t"
"v_mfma_f32_16x16x32_bf16 v[188:191], a[68:71], a[200:203], v[188:191]\n\t"
"s_waitcnt lgkmcnt(3)\n\t"
"ds_read_b128 a[188:191], v244 offset:27136\n\t"
"v_mfma_f32_16x16x32_bf16 v[188:191], a[64:67], a[196:199], v[188:191]\n\t"
"buffer_load_dwordx4 a[20:23], off, s[80:83], s20 offset:1024 sc1\n\t"
"v_mfma_f32_16x16x32_bf16 v[188:191], a[244:247], a[192:195], v[188:191]\n\t"
"ds_read_b128 a[192:195], v244 offset:27648\n\t"
"v_mfma_f32_16x16x32_bf16 v[192:195], a[244:247], a[196:199], v[192:195]\n\t"
"buffer_load_dwordx4 a[120:123], off, s[80:83], s20 offset:2048 sc1\n\t"
"v_mfma_f32_16x16x32_bf16 v[192:195], a[64:67], a[200:203], v[192:195]\n\t"
"s_waitcnt vmcnt(42)\n\t"
"ds_read_b128 v[208:211], v247 offset:0\n\t"
"s_waitcnt lgkmcnt(5)\n\t"
"v_mfma_f32_16x16x32_bf16 v[192:195], a[68:71], a[204:207], v[192:195]\n\t"
"ds_write_b128 v245, a[24:27] offset:0\n\t"
"Lst33_%=:\n\t"
"v_mfma_f32_16x16x32_bf16 v[0:3], a[72:75], a[168:171], v[0:3]\n\t"
"s_waitcnt lgkmcnt(4)\n\t"
"v_mfma_f32_16x16x32_bf16 v[0:3], a[80:83], a[176:179], v[0:3]\n\t"
"ds_read_b128 a[196:199], v244 offset:28160\n\t"
"v_mfma_f32_16x16x32_bf16 v[0:3], a[76:79], a[172:175], v[0:3]\n\t"
"ds_write_b128 v245, a[32:35] offset:18432\n\t"
"v_mfma_f32_16x16x32_bf16 v[4:7], a[72:75], a[172:175], v[4:7]\n\t"
"v_mfma_f32_16x16x32_bf16 v[4:7], a[76:79], a[176:179], v[4:7]\n\t"
"ds_read_b128 a[200:203], v244 offset:28672\n\t"
"v_mfma_f32_16x16x32_bf16 v[4:7], a[80:83], a[180:183], v[4:7]\n\t"
"buffer_store_dwordx4 v[196:199], v248, s[16:19], s29 offen offset:0 nt\n\t"
"ds_read_b128 a[204:207], v244 offset:29184\n\t"
"ds_read_b128 v[212:215], v247 offset:1536\n\t"
"v_mfma_f32_16x16x32_bf16 v[8:11], a[80:83], a[184:187], v[8:11]\n\t"
"s_waitcnt lgkmcnt(8)\n\t"
"s_waitcnt lgkmcnt(6)\n\t"
"v_mfma_f32_16x16x32_bf16 v[8:11], a[76:79], a[180:183], v[8:11]\n\t"
"v_mfma_f32_16x16x32_bf16 v[8:11], a[72:75], a[176:179], v[8:11]\n\t"
"buffer_store_dwordx4 v[200:203], v248, s[16:19], s29 offen offset:128 nt\n\t"
"ds_write_b64 v246, a[152:153] offset:0\n\t"
"s_mov_b32 s20, 1658880\n\t"
"v_mfma_f32_16x16x32_bf16 v[12:15], a[72:75], a[180:183], v[12:15]\n\t"
"ds_read_b128 v[196:199], v247 offset:3072\n\t"
"v_mfma_f32_16x16x32_bf16 v[12:15], a[76:79], a[184:187], v[12:15]\n\t"
"v_mfma_f32_16x16x32_bf16 v[12:15], a[80:83], a[188:191], v[12:15]\n\t"
"ds_write_b64 v246, a[160:161] offset:18432\n\t"
"v_mfma_f32_16x16x32_bf16 v[16:19], a[80:83], a[192:195], v[16:19]\n\t"
"s_waitcnt lgkmcnt(7)\n\t"
"v_mfma_f32_16x16x32_bf16 v[16:19], a[76:79], a[188:191], v[16:19]\n\t"
"v_mfma_f32_16x16x32_bf16 v[16:19], a[72:75], a[184:187], v[16:19]\n\t"
"buffer_store_dwordx4 v[204:207], v248, s[16:19], s29 offen offset:256 nt\n\t"
"buffer_load_dwordx4 a[244:247], off, s[80:83], s20 offset:0 sc1\n\t"
"ds_read_b128 v[200:203], v247 offset:18432\n\t"
"s_add_u32 s29, s22, 590208\n\t"
"v_mfma_f32_16x16x32_bf16 v[20:23], a[72:75], a[188:191], v[20:23]\n\t"
"s_waitcnt lgkmcnt(6)\n\t"
"ds_read_b128 v[216:219], v247 offset:36864\n\t"
"s_waitcnt lgkmcnt(5)\n\t"
"v_mfma_f32_16x16x32_bf16 v[20:23], a[76:79], a[192:195], v[20:23]\n\t"
"ds_read_b128 v[204:207], v247 offset:19968\n\t"
"v_mfma_f32_16x16x32_bf16 v[20:23], a[80:83], a[196:199], v[20:23]\n\t"
"buffer_store_dwordx4 v[208:211], v248, s[16:19], s29 offen offset:0 nt\n\t"
"ds_read_b128 a[0:3], v247 offset:38400\n\t"
"v_mfma_f32_16x16x32_bf16 v[24:27], a[80:83], a[200:203], v[24:27]\n\t"
"ds_read_b128 v[208:211], v247 offset:21504\n\t"
"v_mfma_f32_16x16x32_bf16 v[24:27], a[76:79], a[196:199], v[24:27]\n\t"
"ds_read_b128 a[4:7], v247 offset:39936\n\t"
"v_mfma_f32_16x16x32_bf16 v[24:27], a[72:75], a[192:195], v[24:27]\n\t"
"ds_write_b128 v245, a[28:31] offset:9216\n\t"
"v_mfma_f32_16x16x32_bf16 v[28:31], a[72:75], a[196:199], v[28:31]\n\t"
"buffer_load_dwordx4 a[64:67], off, s[80:83], s20 offset:1024 sc1\n\t"
"v_mfma_f32_16x16x32_bf16 v[28:31], a[76:79], a[200:203], v[28:31]\n\t"
"s_waitcnt vmcnt(45)\n\t"
"v_mfma_f32_16x16x32_bf16 v[28:31], a[80:83], a[204:207], v[28:31]\n\t"
"ds_write_b128 v245, a[36:39] offset:27648\n\t"
"v_mfma_f32_16x16x32_bf16 v[32:35], a[216:219], a[168:171], v[32:35]\n\t"
"s_waitcnt lgkmcnt(9)\n\t"
"v_mfma_f32_16x16x32_bf16 v[32:35], a[248:251], a[176:179], v[32:35]\n\t"
"buffer_load_dwordx4 a[68:71], off, s[80:83], s20 offset:2048 sc1\n\t"
"v_mfma_f32_16x16x32_bf16 v[32:35], a[220:223], a[172:175], v[32:35]\n\t"
"buffer_store_dwordx4 v[212:215], v248, s[16:19], s29 offen offset:128 nt\n\t"
"ds_write_b64 v246, a[154:155] offset:9216\n\t"
"v_mfma_f32_16x16x32_bf16 v[36:39], a[216:219], a[172:175], v[36:39]\n\t"
"s_mov_b32 s20, 1671168\n\t"
"v_mfma_f32_16x16x32_bf16 v[36:39], a[220:223], a[176:179], v[36:39]\n\t"
"v_mfma_f32_16x16x32_bf16 v[36:39], a[248:251], a[180:183], v[36:39]\n\t"
"ds_write_b64 v246, a[162:163] offset:27648\n\t"
"v_mfma_f32_16x16x32_bf16 v[40:43], a[248:251], a[184:187], v[40:43]\n\t"
"v_mfma_f32_16x16x32_bf16 v[40:43], a[220:223], a[180:183], v[40:43]\n\t"
"v_mfma_f32_16x16x32_bf16 v[40:43], a[216:219], a[176:179], v[40:43]\n\t"
"buffer_store_dwordx4 v[196:199], v248, s[16:19], s29 offen offset:256 nt\n\t"
"s_add_u32 s29, s22, 786816\n\t"
"v_mfma_f32_16x16x32_bf16 v[44:47], a[216:219], a[180:183], v[44:47]\n\t"
"s_waitcnt lgkmcnt(9)\n\t"
"v_mfma_f32_16x16x32_bf16 v[44:47], a[220:223], a[184:187], v[44:47]\n\t"
"v_mfma_f32_16x16x32_bf16 v[44:47], a[248:251], a[188:191], v[44:47]\n\t"
"s_waitcnt lgkmcnt(0)\n\t"
"s_barrier\n\t"
"v_mfma_f32_16x16x32_bf16 v[48:51], a[248:251], a[192:195], v[48:51]\n\t"
"v_mfma_f32_16x16x32_bf16 v[48:51], a[220:223], a[188:191], v[48:51]\n\t"
"buffer_load_dwordx4 a[72:75], off, s[80:83], s20 offset:0 sc1\n\t"
"v_mfma_f32_16x16x32_bf16 v[48:51], a[216:219], a[184:187], v[48:51]\n\t"
"buffer_load_dwordx4 a[24:27], off, s[80:83], s20 offset:1024 sc1\n\t"
"v_mfma_f32_16x16x32_bf16 v[52:55], a[216:219], a[188:191], v[52:55]\n\t"
"v_mfma_f32_16x16x32_bf16 v[52:55], a[220:223], a[192:195], v[52:55]\n\t"
"v_mfma_f32_16x16x32_bf16 v[52:55], a[248:251], a[196:199], v[52:55]\n\t"
"buffer_store_dwordx4 v[200:203], v248, s[16:19], s29 offen offset:0 nt\n\t"
"v_mfma_f32_16x16x32_bf16 v[56:59], a[248:251], a[200:203], v[56:59]\n\t"
"v_mfma_f32_16x16x32_bf16 v[56:59], a[220:223], a[196:199], v[56:59]\n\t"
"buffer_load_dwordx4 a[28:31], off, s[80:83], s20 offset:2048 sc1\n\t"
"v_mfma_f32_16x16x32_bf16 v[56:59], a[216:219], a[192:195], v[56:59]\n\t"
"s_mov_b32 s20, 1376256\n\t"
"v_mfma_f32_16x16x32_bf16 v[60:63], a[216:219], a[196:199], v[60:63]\n\t"
"s_waitcnt vmcnt(49)\n\t"
"v_mfma_f32_16x16x32_bf16 v[60:63], a[220:223], a[200:203], v[60:63]\n\t"
"v_mfma_f32_16x16x32_bf16 v[60:63], a[248:251], a[204:207], v[60:63]\n\t"
"buffer_store_dwordx4 v[204:207], v248, s[16:19], s29 offen offset:128 nt\n\t"
"v_mfma_f32_16x16x32_bf16 v[64:67], a[84:87], a[168:171], v[64:67]\n\t"
"ds_read_b128 a[168:171], v244 offset:33792\n\t"
"v_mfma_f32_16x16x32_bf16 v[64:67], a[92:95], a[176:179], v[64:67]\n\t"
"v_mfma_f32_16x16x32_bf16 v[64:67], a[88:91], a[172:175], v[64:67]\n\t"
"buffer_load_dwordx4 a[216:219], off, s[80:83], s20 offset:0 sc1\n\t"
"v_mfma_f32_16x16x32_bf16 v[68:71], a[84:87], a[172:175], v[68:71]\n\t"
"ds_read_b128 a[172:175], v244 offset:34304\n\t"
"v_mfma_f32_16x16x32_bf16 v[68:71], a[88:91], a[176:179], v[68:71]\n\t"
"v_mfma_f32_16x16x32_bf16 v[68:71], a[92:95], a[180:183], v[68:71]\n\t"
"v_mfma_f32_16x16x32_bf16 v[72:75], a[92:95], a[184:187], v[72:75]\n\t"
"v_mfma_f32_16x16x32_bf16 v[72:75], a[88:91], a[180:183], v[72:75]\n\t"
"v_mfma_f32_16x16x32_bf16 v[72:75], a[84:87], a[176:179], v[72:75]\n\t"
"ds_read_b128 a[176:179], v244 offset:34816\n\t"
"v_mfma_f32_16x16x32_bf16 v[76:79], a[84:87], a[180:183], v[76:79]\n\t"
"buffer_load_dwordx4 a[220:223], off, s[80:83], s20 offset:1024 sc1\n\t"
"v_mfma_f32_16x16x32_bf16 v[76:79], a[88:91], a[184:187], v[76:79]\n\t"
"ds_read_b128 a[180:183], v244 offset:35328\n\t"
"v_mfma_f32_16x16x32_bf16 v[76:79], a[92:95], a[188:191], v[76:79]\n\t"
"buffer_store_dwordx4 v[208:211], v248, s[16:19], s29 offen offset:256 nt\n\t"
"ds_read_b128 v[196:199], v255 offset:0\n\t"
"ds_read_b128 v[212:215], v255 offset:18432\n\t"
"s_add_u32 s29, s22, 983424\n\t"
"v_mfma_f32_16x16x32_bf16 v[80:83], a[92:95], a[192:195], v[80:83]\n\t"
"ds_read_b128 v[200:203], v255 offset:1536\n\t"
"v_mfma_f32_16x16x32_bf16 v[80:83], a[88:91], a[188:191], v[80:83]\n\t"
"buffer_load_dwordx4 a[248:251], off, s[80:83], s20 offset:2048 sc1\n\t"
"v_mfma_f32_16x16x32_bf16 v[80:83], a[84:87], a[184:187], v[80:83]\n\t"
"ds_read_b128 a[184:187], v244 offset:35840\n\t"
"v_mfma_f32_16x16x32_bf16 v[84:87], a[84:87], a[188:191], v[84:87]\n\t"
"s_mov_b32 s20, 1388544\n\t"
"ds_read_b128 a[188:191], v244 offset:36352\n\t"
"v_mfma_f32_16x16x32_bf16 v[84:87], a[88:91], a[192:195], v[84:87]\n\t"
"ds_read_b128 a[8:11], v255 offset:19968\n\t"
"v_mfma_f32_16x16x32_bf16 v[84:87], a[92:95], a[196:199], v[84:87]\n\t"
"buffer_store_dwordx4 v[216:219], v248, s[16:19], s29 offen offset:0 nt\n\t"
"ds_read_b128 v[204:207], v255 offset:3072\n\t"
"ds_read_b128 a[12:15], v255 offset:21504\n\t"
"v_mfma_f32_16x16x32_bf16 v[88:91], a[92:95], a[200:203], v[88:91]\n\t"
"v_mfma_f32_16x16x32_bf16 v[88:91], a[88:91], a[196:199], v[88:91]\n\t"
"v_mfma_f32_16x16x32_bf16 v[88:91], a[84:87], a[192:195], v[88:91]\n\t"
"ds_read_b128 a[192:195], v244 offset:36864\n\t"
"v_mfma_f32_16x16x32_bf16 v[92:95], a[84:87], a[196:199], v[92:95]\n\t"
"buffer_load_dwordx4 a[32:35], off, s[80:83], s20 offset:0 sc1\n\t"
"v_mfma_f32_16x16x32_bf16 v[92:95], a[88:91], a[200:203], v[92:95]\n\t"
"s_waitcnt vmcnt(53) lgkmcnt(10)\n\t"
"ds_read_b128 a[196:199], v244 offset:37376\n\t"
"v_mfma_f32_16x16x32_bf16 v[92:95], a[92:95], a[204:207], v[92:95]\n\t"
"ds_read_b128 a[200:203], v244 offset:37888\n\t"
"v_mfma_f32_16x16x32_bf16 v[100:103], a[96:99], a[168:171], v[100:103]\n\t"
"s_waitcnt lgkmcnt(11)\n\t"
"ds_read_b128 a[204:207], v244 offset:38400\n\t"
"v_mfma_f32_16x16x32_bf16 v[100:103], a[208:211], a[176:179], v[100:103]\n\t"
"v_mfma_f32_16x16x32_bf16 v[100:103], a[100:103], a[172:175], v[100:103]\n\t"
"buffer_store_dwordx4 a[0:3], v248, s[16:19], s29 offen offset:128 nt\n\t"
"v_mfma_f32_16x16x32_bf16 v[104:107], a[96:99], a[172:175], v[104:107]\n\t"
"s_waitcnt lgkmcnt(8)\n\t"
"v_mfma_f32_16x16x32_bf16 v[104:107], a[100:103], a[176:179], v[104:107]\n\t"
"v_mfma_f32_16x16x32_bf16 v[104:107], a[208:211], a[180:183], v[104:107]\n\t"
"s_waitcnt lgkmcnt(0)\n\t"
"s_barrier\n\t"
"v_mfma_f32_16x16x32_bf16 v[108:111], a[208:211], a[184:187], v[108:111]\n\t"
"v_mfma_f32_16x16x32_bf16 v[108:111], a[100:103], a[180:183], v[108:111]\n\t"
"buffer_load_dwordx4 a[36:39], off, s[80:83], s20 offset:1024 sc1\n\t"
"v_mfma_f32_16x16x32_bf16 v[108:111], a[96:99], a[176:179], v[108:111]\n\t"
"buffer_load_dwordx4 a[76:79], off, s[80:83], s20 offset:2048 sc1\n\t"
"v_mfma_f32_16x16x32_bf16 v[112:115], a[96:99], a[180:183], v[112:115]\n\t"
"s_mov_b32 s20, 1683456\n\t"
"v_mfma_f32_16x16x32_bf16 v[112:115], a[100:103], a[184:187], v[112:115]\n\t"
"v_mfma_f32_16x16x32_bf16 v[112:115], a[208:211], a[188:191], v[112:115]\n\t"
"buffer_store_dwordx4 a[4:7], v248, s[16:19], s29 offen offset:256 nt\n\t"
"v_mfma_f32_16x16x32_bf16 v[116:119], a[208:211], a[192:195], v[116:119]\n\t"
"s_add_u32 s29, s22, 1180032\n\t"
"v_mfma_f32_16x16x32_bf16 v[116:119], a[100:103], a[188:191], v[116:119]\n\t"
"v_mfma_f32_16x16x32_bf16 v[116:119], a[96:99], a[184:187], v[116:119]\n\t"
"buffer_load_dwordx4 a[0:3], off, s[80:83], s20 offset:0 sc1\n\t"
"v_mfma_f32_16x16x32_bf16 v[120:123], a[96:99], a[188:191], v[120:123]\n\t"
"v_mfma_f32_16x16x32_bf16 v[120:123], a[100:103], a[192:195], v[120:123]\n\t"
"v_mfma_f32_16x16x32_bf16 v[120:123], a[208:211], a[196:199], v[120:123]\n\t"
"v_mfma_f32_16x16x32_bf16 v[124:127], a[208:211], a[200:203], v[124:127]\n\t"
"v_mfma_f32_16x16x32_bf16 v[124:127], a[100:103], a[196:199], v[124:127]\n\t"
"v_mfma_f32_16x16x32_bf16 v[124:127], a[96:99], a[192:195], v[124:127]\n\t"
"buffer_store_dwordx4 v[196:199], v248, s[16:19], s29 offen offset:0 nt\n\t"
"v_mfma_f32_16x16x32_bf16 v[128:131], a[96:99], a[196:199], v[128:131]\n\t"
"s_waitcnt vmcnt(56)\n\t"
"v_mfma_f32_16x16x32_bf16 v[128:131], a[100:103], a[200:203], v[128:131]\n\t"
"buffer_load_dwordx4 a[4:7], off, s[80:83], s20 offset:1024 sc1\n\t"
"v_mfma_f32_16x16x32_bf16 v[128:131], a[208:211], a[204:207], v[128:131]\n\t"
"v_mfma_f32_16x16x32_bf16 v[132:135], a[224:227], a[168:171], v[132:135]\n\t"
"v_mfma_f32_16x16x32_bf16 v[132:135], a[108:111], a[176:179], v[132:135]\n\t"
"buffer_load_dwordx4 a[208:211], off, s[80:83], s20 offset:2048 sc1\n\t"
"v_mfma_f32_16x16x32_bf16 v[132:135], a[252:255], a[172:175], v[132:135]\n\t"
"v_mfma_f32_16x16x32_bf16 v[136:139], a[224:227], a[172:175], v[136:139]\n\t"
"v_mfma_f32_16x16x32_bf16 v[136:139], a[252:255], a[176:179], v[136:139]\n\t"
"v_mfma_f32_16x16x32_bf16 v[136:139], a[108:111], a[180:183], v[136:139]\n\t"
"buffer_store_dwordx4 v[200:203], v248, s[16:19], s29 offen offset:128 nt\n\t"
"v_mfma_f32_16x16x32_bf16 v[140:143], a[108:111], a[184:187], v[140:143]\n\t"
"v_mfma_f32_16x16x32_bf16 v[140:143], a[252:255], a[180:183], v[140:143]\n\t"
"v_mfma_f32_16x16x32_bf16 v[140:143], a[224:227], a[176:179], v[140:143]\n\t"
"v_mfma_f32_16x16x32_bf16 v[144:147], a[224:227], a[180:183], v[144:147]\n\t"
"v_mfma_f32_16x16x32_bf16 v[144:147], a[252:255], a[184:187], v[144:147]\n\t"
"v_mfma_f32_16x16x32_bf16 v[144:147], a[108:111], a[188:191], v[144:147]\n\t"
"v_mfma_f32_16x16x32_bf16 v[148:151], a[108:111], a[192:195], v[148:151]\n\t"
"v_mfma_f32_16x16x32_bf16 v[148:151], a[252:255], a[188:191], v[148:151]\n\t"
"v_mfma_f32_16x16x32_bf16 v[148:151], a[224:227], a[184:187], v[148:151]\n\t"
"buffer_store_dwordx4 v[204:207], v248, s[16:19], s29 offen offset:256 nt\n\t"
"v_mfma_f32_16x16x32_bf16 v[152:155], a[224:227], a[188:191], v[152:155]\n\t"
"s_add_u32 s29, s22, 1376640\n\t"
"v_mfma_f32_16x16x32_bf16 v[152:155], a[252:255], a[192:195], v[152:155]\n\t"
"v_mfma_f32_16x16x32_bf16 v[152:155], a[108:111], a[196:199], v[152:155]\n\t"
"v_mfma_f32_16x16x32_bf16 v[156:159], a[108:111], a[200:203], v[156:159]\n\t"
"v_mfma_f32_16x16x32_bf16 v[156:159], a[252:255], a[196:199], v[156:159]\n\t"
"v_mfma_f32_16x16x32_bf16 v[156:159], a[224:227], a[192:195], v[156:159]\n\t"
"v_mfma_f32_16x16x32_bf16 v[160:163], a[224:227], a[196:199], v[160:163]\n\t"
"s_waitcnt vmcnt(57)\n\t"
"v_mfma_f32_16x16x32_bf16 v[160:163], a[252:255], a[200:203], v[160:163]\n\t"
"v_mfma_f32_16x16x32_bf16 v[160:163], a[108:111], a[204:207], v[160:163]\n\t"
"buffer_store_dwordx4 v[212:215], v248, s[16:19], s29 offen offset:0 nt\n\t"
"v_mfma_f32_16x16x32_bf16 v[164:167], a[116:119], a[168:171], v[164:167]\n\t"
"ds_read_b128 a[168:171], v222 offset:2048\n\t"
"v_mfma_f32_16x16x32_bf16 v[164:167], a[132:135], a[176:179], v[164:167]\n\t"
"ds_read_b128 v[196:199], v223 offset:8192\n\t"
"v_mfma_f32_16x16x32_bf16 v[164:167], a[124:127], a[172:175], v[164:167]\n\t"
"ds_read_b128 v[208:211], v223 offset:14336\n\t"
"v_mfma_f32_16x16x32_bf16 v[168:171], a[116:119], a[172:175], v[168:171]\n\t"
"ds_read_b128 a[172:175], v222 offset:512\n\t"
"v_mfma_f32_16x16x32_bf16 v[168:171], a[124:127], a[176:179], v[168:171]\n\t"
"ds_read_b128 v[200:203], v223 offset:6656\n\t"
"v_mfma_f32_16x16x32_bf16 v[168:171], a[132:135], a[180:183], v[168:171]\n\t"
"ds_read_b128 v[216:219], v223 offset:12800\n\t"
"v_mfma_f32_16x16x32_bf16 v[172:175], a[132:135], a[184:187], v[172:175]\n\t"
"v_mfma_f32_16x16x32_bf16 v[172:175], a[124:127], a[180:183], v[172:175]\n\t"
"v_mfma_f32_16x16x32_bf16 v[172:175], a[116:119], a[176:179], v[172:175]\n\t"
"buffer_store_dwordx4 a[8:11], v248, s[16:19], s29 offen offset:128 nt\n\t"
"v_mfma_f32_16x16x32_bf16 v[176:179], a[116:119], a[180:183], v[176:179]\n\t"
"v_mfma_f32_16x16x32_bf16 v[176:179], a[124:127], a[184:187], v[176:179]\n\t"
"v_mfma_f32_16x16x32_bf16 v[176:179], a[132:135], a[188:191], v[176:179]\n\t"
"s_barrier\n\t"
"v_mfma_f32_16x16x32_bf16 v[180:183], a[132:135], a[192:195], v[180:183]\n\t"
"v_mfma_f32_16x16x32_bf16 v[180:183], a[124:127], a[188:191], v[180:183]\n\t"
"v_mfma_f32_16x16x32_bf16 v[180:183], a[116:119], a[184:187], v[180:183]\n\t"
"v_mfma_f32_16x16x32_bf16 v[184:187], a[116:119], a[188:191], v[184:187]\n\t"
"v_mfma_f32_16x16x32_bf16 v[184:187], a[124:127], a[192:195], v[184:187]\n\t"
"v_mfma_f32_16x16x32_bf16 v[184:187], a[132:135], a[196:199], v[184:187]\n\t"
"buffer_store_dwordx4 a[12:15], v248, s[16:19], s29 offen offset:256 nt\n\t"
"v_mfma_f32_16x16x32_bf16 v[188:191], a[132:135], a[200:203], v[188:191]\n\t"
"ds_read_b128 a[176:179], v222 offset:2304\n\t"
"v_mfma_f32_16x16x32_bf16 v[188:191], a[124:127], a[196:199], v[188:191]\n\t"
"ds_read_b128 a[180:183], v223 offset:8448\n\t"
"v_mfma_f32_16x16x32_bf16 v[188:191], a[116:119], a[192:195], v[188:191]\n\t"
"ds_read_b128 a[184:187], v223 offset:14592\n\t"
"v_mfma_f32_16x16x32_bf16 v[192:195], a[116:119], a[196:199], v[192:195]\n\t"
"s_waitcnt vmcnt(57) lgkmcnt(6)\n\t"
"ds_read_b128 a[188:191], v222 offset:768\n\t"
"v_mfma_f32_16x16x32_bf16 v[192:195], a[124:127], a[200:203], v[192:195]\n\t"
"ds_read_b128 a[192:195], v223 offset:6912\n\t"
"v_mfma_f32_16x16x32_bf16 v[192:195], a[132:135], a[204:207], v[192:195]\n\t"
"ds_read_b128 a[196:199], v223 offset:13056\n\t"
"Lst34_%=:\n\t"
"v_mfma_f32_16x16x32_bf16 v[0:3], a[140:143], a[168:171], v[0:3]\n\t"
"s_waitcnt vmcnt(54) lgkmcnt(6)\n\t"
"v_mfma_f32_16x16x32_bf16 v[0:3], a[228:231], v[196:199], v[0:3]\n\t"
"v_mfma_f32_16x16x32_bf16 v[0:3], a[232:235], v[208:211], v[0:3]\n\t"
"v_mfma_f32_16x16x32_bf16 v[0:3], a[148:151], a[172:175], v[0:3]\n\t"
"s_waitcnt vmcnt(51)\n\t"
"v_mfma_f32_16x16x32_bf16 v[0:3], a[104:107], v[200:203], v[0:3]\n\t"
"v_mfma_f32_16x16x32_bf16 v[0:3], a[112:115], v[216:219], v[0:3]\n\t"
"v_mfma_f32_16x16x32_bf16 v[32:35], a[164:167], v[200:203], v[32:35]\n\t"
"s_waitcnt vmcnt(46)\n\t"
"v_mfma_f32_16x16x32_bf16 v[32:35], v[240:243], v[216:219], v[32:35]\n\t"
"v_mfma_f32_16x16x32_bf16 v[32:35], a[156:159], a[172:175], v[32:35]\n\t"
"v_mfma_f32_16x16x32_bf16 v[32:35], a[236:239], a[168:171], v[32:35]\n\t"
"s_waitcnt vmcnt(40)\n\t"
"v_cvt_pk_bf16_f32 v0, v0, v1\n\t"
"v_cvt_pk_bf16_f32 v1, v2, v3\n\t"
"v_mfma_f32_16x16x32_bf16 v[32:35], a[40:43], v[196:199], v[32:35]\n\t"
"v_mfma_f32_16x16x32_bf16 v[32:35], a[44:47], v[208:211], v[32:35]\n\t"
"v_mfma_f32_16x16x32_bf16 v[64:67], a[52:55], v[196:199], v[64:67]\n\t"
"s_waitcnt vmcnt(36)\n\t"
"ds_read_b128 a[200:203], v222 offset:22016\n\t"
"v_mfma_f32_16x16x32_bf16 v[64:67], a[56:59], v[208:211], v[64:67]\n\t"
"v_mfma_f32_16x16x32_bf16 v[64:67], a[48:51], a[168:171], v[64:67]\n\t"
"s_barrier\n\t"
"s_cmp_lt_u32 s69, 216\n\t"
"s_cbranch_scc0 Lna2_%=\n\t"
"s_add_u32 s71, s71, 1\n\t"
"v_mov_b32 v228, s71\n\t"
"s_mov_b64 exec, 1\n\t"
"buffer_store_dword v228, off, s[8:11], s70 sc1\n\t"
"s_mov_b64 exec, -1\n\t"
"Lna2_%=:\n\t"
"v_mfma_f32_16x16x32_bf16 v[64:67], a[212:215], a[172:175], v[64:67]\n\t"
"s_waitcnt vmcnt(33) lgkmcnt(4)\n\t"
"ds_read_b128 a[168:171], v222 offset:20480\n\t"
"v_mfma_f32_16x16x32_bf16 v[64:67], a[240:243], v[200:203], v[64:67]\n\t"
"v_cvt_pk_bf16_f32 v2, v32, v33\n\t"
"v_cvt_pk_bf16_f32 v3, v34, v35\n\t"
"v_mfma_f32_16x16x32_bf16 v[64:67], a[60:63], v[216:219], v[64:67]\n\t"
"ds_write_b128 v245, v[0:3] offset:0\n\t"
"v_mfma_f32_16x16x32_bf16 v[100:103], a[16:19], a[176:179], v[100:103]\n\t"
"s_waitcnt vmcnt(26) lgkmcnt(3)\n\t"
"v_mfma_f32_16x16x32_bf16 v[100:103], a[20:23], a[180:183], v[100:103]\n\t"
"v_mfma_f32_16x16x32_bf16 v[100:103], a[120:123], a[184:187], v[100:103]\n\t"
"v_mfma_f32_16x16x32_bf16 v[100:103], a[244:247], a[188:191], v[100:103]\n\t"
"s_waitcnt vmcnt(20)\n\t"
"v_cvt_pk_bf16_f32 v64, v64, v65\n\t"
"v_cvt_pk_bf16_f32 v65, v66, v67\n\t"
"v_mfma_f32_16x16x32_bf16 v[100:103], a[64:67], a[192:195], v[100:103]\n\t"
"v_mfma_f32_16x16x32_bf16 v[100:103], a[68:71], a[196:199], v[100:103]\n\t"
"ds_write_b64 v246, v[64:65] offset:0\n\t"
"v_mfma_f32_16x16x32_bf16 v[132:135], a[24:27], a[192:195], v[132:135]\n\t"
"s_waitcnt vmcnt(15)\n\t"
"v_mfma_f32_16x16x32_bf16 v[132:135], a[28:31], a[196:199], v[132:135]\n\t"
"v_mfma_f32_16x16x32_bf16 v[132:135], a[72:75], a[188:191], v[132:135]\n\t"
"v_mfma_f32_16x16x32_bf16 v[132:135], a[216:219], a[176:179], v[132:135]\n\t"
"s_waitcnt vmcnt(10)\n\t"
"v_cvt_pk_bf16_f32 v100, v100, v101\n\t"
"v_cvt_pk_bf16_f32 v101, v102, v103\n\t"
"v_mfma_f32_16x16x32_bf16 v[132:135], a[220:223], a[180:183], v[132:135]\n\t"
"v_mfma_f32_16x16x32_bf16 v[132:135], a[248:251], a[184:187], v[132:135]\n\t"
"v_mfma_f32_16x16x32_bf16 v[164:167], a[36:39], a[180:183], v[164:167]\n\t"
"s_waitcnt vmcnt(5)\n\t"
"ds_read_b128 a[172:175], v222 offset:22272\n\t"
"v_mfma_f32_16x16x32_bf16 v[164:167], a[76:79], a[184:187], v[164:167]\n\t"
"v_mfma_f32_16x16x32_bf16 v[164:167], a[32:35], a[176:179], v[164:167]\n\t"
"v_mfma_f32_16x16x32_bf16 v[164:167], a[0:3], a[188:191], v[164:167]\n\t"
"s_waitcnt lgkmcnt(4)\n\t"
"ds_read_b128 a[176:179], v222 offset:20736\n\t"
"v_cvt_pk_bf16_f32 v102, v132, v133\n\t"
"v_mfma_f32_16x16x32_bf16 v[164:167], a[4:7], a[192:195], v[164:167]\n\t"
"v_cvt_pk_bf16_f32 v103, v134, v135\n\t"
"v_mfma_f32_16x16x32_bf16 v[164:167], a[208:211], a[196:199], v[164:167]\n\t"
"ds_write_b128 v245, v[100:103] offset:9216\n\t"
"v_mfma_f32_16x16x32_bf16 v[4:7], a[140:143], v[196:199], v[4:7]\n\t"
"s_waitcnt lgkmcnt(5)\n\t"
"v_mfma_f32_16x16x32_bf16 v[4:7], a[228:231], v[208:211], v[4:7]\n\t"
"v_mfma_f32_16x16x32_bf16 v[4:7], a[232:235], a[200:203], v[4:7]\n\t"
"v_mfma_f32_16x16x32_bf16 v[4:7], a[148:151], v[200:203], v[4:7]\n\t"
"v_cvt_pk_bf16_f32 v164, v164, v165\n\t"
"v_cvt_pk_bf16_f32 v165, v166, v167\n\t"
"v_mfma_f32_16x16x32_bf16 v[4:7], a[104:107], v[216:219], v[4:7]\n\t"
"v_mfma_f32_16x16x32_bf16 v[4:7], a[112:115], a[168:171], v[4:7]\n\t"
"ds_write_b64 v246, v[164:165] offset:9216\n\t"
"v_mfma_f32_16x16x32_bf16 v[36:39], a[164:167], v[216:219], v[36:39]\n\t"
"v_mfma_f32_16x16x32_bf16 v[36:39], v[240:243], a[168:171], v[36:39]\n\t"
"v_mfma_f32_16x16x32_bf16 v[36:39], a[156:159], v[200:203], v[36:39]\n\t"
"v_mfma_f32_16x16x32_bf16 v[36:39], a[236:239], v[196:199], v[36:39]\n\t"
"v_cvt_pk_bf16_f32 v4, v4, v5\n\t"
"v_cvt_pk_bf16_f32 v5, v6, v7\n\t"
"v_mfma_f32_16x16x32_bf16 v[36:39], a[40:43], v[208:211], v[36:39]\n\t"
"v_mfma_f32_16x16x32_bf16 v[36:39], a[44:47], a[200:203], v[36:39]\n\t"
"v_mfma_f32_16x16x32_bf16 v[68:71], a[52:55], v[208:211], v[68:71]\n\t"
"ds_read_b128 a[188:191], v223 offset:28160\n\t"
"v_mfma_f32_16x16x32_bf16 v[68:71], a[56:59], a[200:203], v[68:71]\n\t"
"v_mfma_f32_16x16x32_bf16 v[68:71], a[48:51], v[196:199], v[68:71]\n\t"
"s_waitcnt lgkmcnt(1)\n\t"
"s_barrier\n\t"
"v_mfma_f32_16x16x32_bf16 v[68:71], a[212:215], v[200:203], v[68:71]\n\t"
"ds_read_b128 v[196:199], v255 offset:0\n\t"
"v_mfma_f32_16x16x32_bf16 v[68:71], a[240:243], v[216:219], v[68:71]\n\t"
"ds_read_b128 v[200:203], v255 offset:1536\n\t"
"v_cvt_pk_bf16_f32 v6, v36, v37\n\t"
"v_mfma_f32_16x16x32_bf16 v[68:71], a[60:63], a[168:171], v[68:71]\n\t"
"ds_read_b128 v[204:207], v255 offset:3072\n\t"
"v_cvt_pk_bf16_f32 v7, v38, v39\n\t"
"v_mfma_f32_16x16x32_bf16 v[104:107], a[16:19], a[180:183], v[104:107]\n\t"
"s_add_u32 s29, s23, 0\n\t"
"ds_read_b128 a[204:207], v223 offset:26624\n\t"
"v_mfma_f32_16x16x32_bf16 v[104:107], a[20:23], a[184:187], v[104:107]\n\t"
"v_mfma_f32_16x16x32_bf16 v[104:107], a[120:123], a[172:175], v[104:107]\n\t"
"ds_write_b128 v245, v[4:7] offset:18432\n\t"
"v_mfma_f32_16x16x32_bf16 v[104:107], a[244:247], a[192:195], v[104:107]\n\t"
"s_waitcnt lgkmcnt(4)\n\t"
"v_cvt_pk_bf16_f32 v68, v68, v69\n\t"
"v_cvt_pk_bf16_f32 v69, v70, v71\n\t"
"v_mfma_f32_16x16x32_bf16 v[104:107], a[64:67], a[196:199], v[104:107]\n\t"
"v_mfma_f32_16x16x32_bf16 v[104:107], a[68:71], a[176:179], v[104:107]\n\t"
"ds_write_b64 v246, v[68:69] offset:18432\n\t"
"v_mfma_f32_16x16x32_bf16 v[136:139], a[24:27], a[196:199], v[136:139]\n\t"
"s_waitcnt lgkmcnt(4)\n\t"
"v_mfma_f32_16x16x32_bf16 v[136:139], a[28:31], a[176:179], v[136:139]\n\t"
"v_mfma_f32_16x16x32_bf16 v[136:139], a[72:75], a[192:195], v[136:139]\n\t"
"buffer_store_dwordx4 v[196:199], v248, s[16:19], s29 offen offset:0 sc1\n\t"
"v_mfma_f32_16x16x32_bf16 v[136:139], a[216:219], a[180:183], v[136:139]\n\t"
"s_waitcnt lgkmcnt(3)\n\t"
"v_cvt_pk_bf16_f32 v104, v104, v105\n\t"
"v_cvt_pk_bf16_f32 v105, v106, v107\n\t"
"v_mfma_f32_16x16x32_bf16 v[136:139], a[220:223], a[184:187], v[136:139]\n\t"
"v_mfma_f32_16x16x32_bf16 v[136:139], a[248:251], a[172:175], v[136:139]\n\t"
"buffer_store_dwordx4 v[200:203], v248, s[16:19], s29 offen offset:128 sc1\n\t"
"v_mfma_f32_16x16x32_bf16 v[168:171], a[36:39], a[184:187], v[168:171]\n\t"
"ds_read_b128 v[212:215], v223 offset:28416\n\t"
"v_mfma_f32_16x16x32_bf16 v[168:171], a[76:79], a[172:175], v[168:171]\n\t"
"v_mfma_f32_16x16x32_bf16 v[168:171], a[32:35], a[180:183], v[168:171]\n\t"
"buffer_store_dwordx4 v[204:207], v248, s[16:19], s29 offen offset:256 sc1\n\t"
"v_mfma_f32_16x16x32_bf16 v[168:171], a[0:3], a[192:195], v[168:171]\n\t"
"ds_read_b128 a[180:183], v223 offset:26880\n\t"
"v_cvt_pk_bf16_f32 v106, v136, v137\n\t"
"v_mfma_f32_16x16x32_bf16 v[168:171], a[4:7], a[196:199], v[168:171]\n\t"
"v_cvt_pk_bf16_f32 v107, v138, v139\n\t"
"v_mfma_f32_16x16x32_bf16 v[168:171], a[208:211], a[176:179], v[168:171]\n\t"
"ds_write_b128 v245, v[104:107] offset:27648\n\t"
"v_mfma_f32_16x16x32_bf16 v[8:11], a[140:143], v[208:211], v[8:11]\n\t"
"s_waitcnt lgkmcnt(5)\n\t"
"v_mfma_f32_16x16x32_bf16 v[8:11], a[228:231], a[200:203], v[8:11]\n\t"
"v_mfma_f32_16x16x32_bf16 v[8:11], a[232:235], a[188:191], v[8:11]\n\t"
"v_mfma_f32_16x16x32_bf16 v[8:11], a[148:151], v[216:219], v[8:11]\n\t"
"v_cvt_pk_bf16_f32 v168, v168, v169\n\t"
"v_cvt_pk_bf16_f32 v169, v170, v171\n\t"
"v_mfma_f32_16x16x32_bf16 v[8:11], a[104:107], a[168:171], v[8:11]\n\t"
"v_mfma_f32_16x16x32_bf16 v[8:11], a[112:115], a[204:207], v[8:11]\n\t"
"ds_write_b64 v246, v[168:169] offset:27648\n\t"
"v_mfma_f32_16x16x32_bf16 v[40:43], a[164:167], a[168:171], v[40:43]\n\t"
"v_mfma_f32_16x16x32_bf16 v[40:43], v[240:243], a[204:207], v[40:43]\n\t"
"v_mfma_f32_16x16x32_bf16 v[40:43], a[156:159], v[216:219], v[40:43]\n\t"
"v_mfma_f32_16x16x32_bf16 v[40:43], a[236:239], v[208:211], v[40:43]\n\t"
"v_cvt_pk_bf16_f32 v8, v8, v9\n\t"
"v_cvt_pk_bf16_f32 v9, v10, v11\n\t"
"v_mfma_f32_16x16x32_bf16 v[40:43], a[40:43], a[200:203], v[40:43]\n\t"
"v_mfma_f32_16x16x32_bf16 v[40:43], a[44:47], a[188:191], v[40:43]\n\t"
"v_mfma_f32_16x16x32_bf16 v[72:75], a[52:55], a[200:203], v[72:75]\n\t"
"ds_read_b128 a[192:195], v223 offset:34304\n\t"
"v_mfma_f32_16x16x32_bf16 v[72:75], a[56:59], a[188:191], v[72:75]\n\t"
"v_mfma_f32_16x16x32_bf16 v[72:75], a[48:51], v[208:211], v[72:75]\n\t"
"s_waitcnt lgkmcnt(1)\n\t"
"s_barrier\n\t"
"v_mfma_f32_16x16x32_bf16 v[72:75], a[212:215], v[216:219], v[72:75]\n\t"
"ds_read_b128 v[196:199], v255 offset:18432\n\t"
"v_mfma_f32_16x16x32_bf16 v[72:75], a[240:243], a[168:171], v[72:75]\n\t"
"ds_read_b128 v[200:203], v255 offset:19968\n\t"
"v_cvt_pk_bf16_f32 v10, v40, v41\n\t"
"v_mfma_f32_16x16x32_bf16 v[72:75], a[60:63], a[204:207], v[72:75]\n\t"
"ds_read_b128 v[204:207], v255 offset:21504\n\t"
"v_cvt_pk_bf16_f32 v11, v42, v43\n\t"
"v_mfma_f32_16x16x32_bf16 v[108:111], a[16:19], a[184:187], v[108:111]\n\t"
"s_add_u32 s29, s23, 196608\n\t"
"ds_read_b128 v[208:211], v223 offset:32768\n\t"
"v_mfma_f32_16x16x32_bf16 v[108:111], a[20:23], a[172:175], v[108:111]\n\t"
"v_mfma_f32_16x16x32_bf16 v[108:111], a[120:123], v[212:215], v[108:111]\n\t"
"ds_write_b128 v245, v[8:11] offset:0\n\t"
"v_mfma_f32_16x16x32_bf16 v[108:111], a[244:247], a[196:199], v[108:111]\n\t"
"s_waitcnt lgkmcnt(4)\n\t"
"v_cvt_pk_bf16_f32 v72, v72, v73\n\t"
"v_cvt_pk_bf16_f32 v73, v74, v75\n\t"
"v_mfma_f32_16x16x32_bf16 v[108:111], a[64:67], a[176:179], v[108:111]\n\t"
"v_mfma_f32_16x16x32_bf16 v[108:111], a[68:71], a[180:183], v[108:111]\n\t"
"ds_write_b64 v246, v[72:73] offset:0\n\t"
"v_mfma_f32_16x16x32_bf16 v[140:143], a[24:27], a[176:179], v[140:143]\n\t"
"s_waitcnt lgkmcnt(4)\n\t"
"v_mfma_f32_16x16x32_bf16 v[140:143], a[28:31], a[180:183], v[140:143]\n\t"
"v_mfma_f32_16x16x32_bf16 v[140:143], a[72:75], a[196:199], v[140:143]\n\t"
"buffer_store_dwordx4 v[196:199], v248, s[16:19], s29 offen offset:0 sc1\n\t"
"v_mfma_f32_16x16x32_bf16 v[140:143], a[216:219], a[184:187], v[140:143]\n\t"
"s_waitcnt lgkmcnt(3)\n\t"
"v_cvt_pk_bf16_f32 v108, v108, v109\n\t"
"v_cvt_pk_bf16_f32 v109, v110, v111\n\t"
"v_mfma_f32_16x16x32_bf16 v[140:143], a[220:223], a[172:175], v[140:143]\n\t"
"v_mfma_f32_16x16x32_bf16 v[140:143], a[248:251], v[212:215], v[140:143]\n\t"
"buffer_store_dwordx4 v[200:203], v248, s[16:19], s29 offen offset:128 sc1\n\t"
"v_mfma_f32_16x16x32_bf16 v[172:175], a[36:39], a[172:175], v[172:175]\n\t"
"ds_read_b128 v[216:219], v223 offset:34560\n\t"
"v_mfma_f32_16x16x32_bf16 v[172:175], a[76:79], v[212:215], v[172:175]\n\t"
"v_mfma_f32_16x16x32_bf16 v[172:175], a[32:35], a[184:187], v[172:175]\n\t"
"buffer_store_dwordx4 v[204:207], v248, s[16:19], s29 offen offset:256 sc1\n\t"
"v_mfma_f32_16x16x32_bf16 v[172:175], a[0:3], a[196:199], v[172:175]\n\t"
"ds_read_b128 a[184:187], v223 offset:33024\n\t"
"v_cvt_pk_bf16_f32 v110, v140, v141\n\t"
"v_mfma_f32_16x16x32_bf16 v[172:175], a[4:7], a[176:179], v[172:175]\n\t"
"v_cvt_pk_bf16_f32 v111, v142, v143\n\t"
"v_mfma_f32_16x16x32_bf16 v[172:175], a[208:211], a[180:183], v[172:175]\n\t"
"ds_write_b128 v245, v[108:111] offset:9216\n\t"
"v_mfma_f32_16x16x32_bf16 v[12:15], a[140:143], a[200:203], v[12:15]\n\t"
"s_waitcnt lgkmcnt(5)\n\t"
"v_mfma_f32_16x16x32_bf16 v[12:15], a[228:231], a[188:191], v[12:15]\n\t"
"v_mfma_f32_16x16x32_bf16 v[12:15], a[232:235], a[192:195], v[12:15]\n\t"
"v_mfma_f32_16x16x32_bf16 v[12:15], a[148:151], a[168:171], v[12:15]\n\t"
"v_cvt_pk_bf16_f32 v172, v172, v173\n\t"
"v_cvt_pk_bf16_f32 v173, v174, v175\n\t"
"v_mfma_f32_16x16x32_bf16 v[12:15], a[104:107], a[204:207], v[12:15]\n\t"
"v_mfma_f32_16x16x32_bf16 v[12:15], a[112:115], v[208:211], v[12:15]\n\t"
"ds_write_b64 v246, v[172:173] offset:9216\n\t"
"v_mfma_f32_16x16x32_bf16 v[44:47], a[164:167], a[204:207], v[44:47]\n\t"
"v_mfma_f32_16x16x32_bf16 v[44:47], v[240:243], v[208:211], v[44:47]\n\t"
"v_mfma_f32_16x16x32_bf16 v[44:47], a[156:159], a[168:171], v[44:47]\n\t"
"v_mfma_f32_16x16x32_bf16 v[44:47], a[236:239], a[200:203], v[44:47]\n\t"
"v_cvt_pk_bf16_f32 v12, v12, v13\n\t"
"v_cvt_pk_bf16_f32 v13, v14, v15\n\t"
"v_mfma_f32_16x16x32_bf16 v[44:47], a[40:43], a[188:191], v[44:47]\n\t"
"v_mfma_f32_16x16x32_bf16 v[44:47], a[44:47], a[192:195], v[44:47]\n\t"
"v_mfma_f32_16x16x32_bf16 v[76:79], a[52:55], a[188:191], v[76:79]\n\t"
"ds_read_b128 a[196:199], v222 offset:41984\n\t"
"v_mfma_f32_16x16x32_bf16 v[76:79], a[56:59], a[192:195], v[76:79]\n\t"
"v_mfma_f32_16x16x32_bf16 v[76:79], a[48:51], a[200:203], v[76:79]\n\t"
"s_waitcnt lgkmcnt(1)\n\t"
"s_barrier\n\t"
"v_mfma_f32_16x16x32_bf16 v[76:79], a[212:215], a[168:171], v[76:79]\n\t"
"ds_read_b128 v[196:199], v255 offset:0\n\t"
"v_mfma_f32_16x16x32_bf16 v[76:79], a[240:243], a[204:207], v[76:79]\n\t"
"ds_read_b128 v[200:203], v255 offset:1536\n\t"
"v_cvt_pk_bf16_f32 v14, v44, v45\n\t"
"v_mfma_f32_16x16x32_bf16 v[76:79], a[60:63], v[208:211], v[76:79]\n\t"
"ds_read_b128 v[204:207], v255 offset:3072\n\t"
"v_cvt_pk_bf16_f32 v15, v46, v47\n\t"
"v_mfma_f32_16x16x32_bf16 v[112:115], a[16:19], a[172:175], v[112:115]\n\t"
"s_add_u32 s29, s23, 393216\n\t"
"ds_read_b128 a[168:171], v222 offset:40448\n\t"
"v_mfma_f32_16x16x32_bf16 v[112:115], a[20:23], v[212:215], v[112:115]\n\t"
"v_mfma_f32_16x16x32_bf16 v[112:115], a[120:123], v[216:219], v[112:115]\n\t"
"ds_write_b128 v245, v[12:15] offset:18432\n\t"
"v_mfma_f32_16x16x32_bf16 v[112:115], a[244:247], a[176:179], v[112:115]\n\t"
"s_waitcnt lgkmcnt(4)\n\t"
"v_cvt_pk_bf16_f32 v76, v76, v77\n\t"
"v_cvt_pk_bf16_f32 v77, v78, v79\n\t"
"v_mfma_f32_16x16x32_bf16 v[112:115], a[64:67], a[180:183], v[112:115]\n\t"
"Lpg25_%=:\n\t"
"s_cbranch_vccnz Lvr4_%=\n\t"
"v_mfma_f32_16x16x32_bf16 v[112:115], a[68:71], a[184:187], v[112:115]\n\t"
"ds_write_b64 v246, v[76:77] offset:18432\n\t"
"v_mfma_f32_16x16x32_bf16 v[144:147], a[24:27], a[180:183], v[144:147]\n\t"
"s_waitcnt lgkmcnt(4)\n\t"
"v_mfma_f32_16x16x32_bf16 v[144:147], a[28:31], a[184:187], v[144:147]\n\t"
"v_mfma_f32_16x16x32_bf16 v[144:147], a[72:75], a[176:179], v[144:147]\n\t"
"buffer_store_dwordx4 v[196:199], v248, s[16:19], s29 offen offset:0 sc1\n\t"
"v_mfma_f32_16x16x32_bf16 v[144:147], a[216:219], a[172:175], v[144:147]\n\t"
"s_waitcnt lgkmcnt(3)\n\t"
"v_cvt_pk_bf16_f32 v112, v112, v113\n\t"
"v_cvt_pk_bf16_f32 v113, v114, v115\n\t"
"v_mfma_f32_16x16x32_bf16 v[144:147], a[220:223], v[212:215], v[144:147]\n\t"
"v_mfma_f32_16x16x32_bf16 v[144:147], a[248:251], v[216:219], v[144:147]\n\t"
"buffer_store_dwordx4 v[200:203], v248, s[16:19], s29 offen offset:128 sc1\n\t"
"v_mfma_f32_16x16x32_bf16 v[176:179], a[36:39], v[212:215], v[176:179]\n\t"
"ds_read_b128 a[200:203], v222 offset:42240\n\t"
"v_mfma_f32_16x16x32_bf16 v[176:179], a[76:79], v[216:219], v[176:179]\n\t"
"v_mfma_f32_16x16x32_bf16 v[176:179], a[32:35], a[172:175], v[176:179]\n\t"
"buffer_store_dwordx4 v[204:207], v248, s[16:19], s29 offen offset:256 sc1\n\t"
"v_mfma_f32_16x16x32_bf16 v[176:179], a[0:3], a[176:179], v[176:179]\n\t"
"ds_read_b128 a[172:175], v222 offset:40704\n\t"
"v_cvt_pk_bf16_f32 v114, v144, v145\n\t"
"v_mfma_f32_16x16x32_bf16 v[176:179], a[4:7], a[180:183], v[176:179]\n\t"
"v_cvt_pk_bf16_f32 v115, v146, v147\n\t"
"v_mfma_f32_16x16x32_bf16 v[176:179], a[208:211], a[184:187], v[176:179]\n\t"
"ds_write_b128 v245, v[112:115] offset:27648\n\t"
"Lst35_%=:\n\t"
"v_mfma_f32_16x16x32_bf16 v[16:19], a[140:143], a[188:191], v[16:19]\n\t"
"s_waitcnt lgkmcnt(5)\n\t"
"v_mfma_f32_16x16x32_bf16 v[16:19], a[228:231], a[192:195], v[16:19]\n\t"
"v_mfma_f32_16x16x32_bf16 v[16:19], a[232:235], a[196:199], v[16:19]\n\t"
"v_mfma_f32_16x16x32_bf16 v[16:19], a[148:151], a[204:207], v[16:19]\n\t"
"v_cvt_pk_bf16_f32 v176, v176, v177\n\t"
"v_cvt_pk_bf16_f32 v177, v178, v179\n\t"
"v_mfma_f32_16x16x32_bf16 v[16:19], a[104:107], v[208:211], v[16:19]\n\t"
"v_mfma_f32_16x16x32_bf16 v[16:19], a[112:115], a[168:171], v[16:19]\n\t"
"ds_write_b64 v246, v[176:177] offset:27648\n\t"
"v_mfma_f32_16x16x32_bf16 v[48:51], a[164:167], v[208:211], v[48:51]\n\t"
"v_mfma_f32_16x16x32_bf16 v[48:51], v[240:243], a[168:171], v[48:51]\n\t"
"v_mfma_f32_16x16x32_bf16 v[48:51], a[156:159], a[204:207], v[48:51]\n\t"
"v_mfma_f32_16x16x32_bf16 v[48:51], a[236:239], a[188:191], v[48:51]\n\t"
"v_cvt_pk_bf16_f32 v16, v16, v17\n\t"
"v_cvt_pk_bf16_f32 v17, v18, v19\n\t"
"v_mfma_f32_16x16x32_bf16 v[48:51], a[40:43], a[192:195], v[48:51]\n\t"
"v_mfma_f32_16x16x32_bf16 v[48:51], a[44:47], a[196:199], v[48:51]\n\t"
"v_mfma_f32_16x16x32_bf16 v[80:83], a[52:55], a[192:195], v[80:83]\n\t"
"ds_read_b128 a[176:179], v223 offset:48128\n\t"
"v_mfma_f32_16x16x32_bf16 v[80:83], a[56:59], a[196:199], v[80:83]\n\t"
"v_mfma_f32_16x16x32_bf16 v[80:83], a[48:51], a[188:191], v[80:83]\n\t"
"s_waitcnt lgkmcnt(1)\n\t"
"s_barrier\n\t"
"v_mfma_f32_16x16x32_bf16 v[80:83], a[212:215], a[204:207], v[80:83]\n\t"
"ds_read_b128 v[196:199], v255 offset:18432\n\t"
"v_mfma_f32_16x16x32_bf16 v[80:83], a[240:243], v[208:211], v[80:83]\n\t"
"ds_read_b128 v[200:203], v255 offset:19968\n\t"
"v_cvt_pk_bf16_f32 v18, v48, v49\n\t"
"v_mfma_f32_16x16x32_bf16 v[80:83], a[60:63], a[168:171], v[80:83]\n\t"
"ds_read_b128 v[204:207], v255 offset:21504\n\t"
"v_cvt_pk_bf16_f32 v19, v50, v51\n\t"
"v_mfma_f32_16x16x32_bf16 v[116:119], a[16:19], v[212:215], v[116:119]\n\t"
"s_add_u32 s29, s23, 589824\n\t"
"ds_read_b128 a[188:191], v223 offset:46592\n\t"
"v_mfma_f32_16x16x32_bf16 v[116:119], a[20:23], v[216:219], v[116:119]\n\t"
"v_mfma_f32_16x16x32_bf16 v[116:119], a[120:123], a[200:203], v[116:119]\n\t"
"ds_write_b128 v245, v[16:19] offset:0\n\t"
"v_mfma_f32_16x16x32_bf16 v[116:119], a[244:247], a[180:183], v[116:119]\n\t"
"s_waitcnt lgkmcnt(4)\n\t"
"v_cvt_pk_bf16_f32 v80, v80, v81\n\t"
"v_cvt_pk_bf16_f32 v81, v82, v83\n\t"
"v_mfma_f32_16x16x32_bf16 v[116:119], a[64:67], a[184:187], v[116:119]\n\t"
"v_mfma_f32_16x16x32_bf16 v[116:119], a[68:71], a[172:175], v[116:119]\n\t"
"ds_write_b64 v246, v[80:81] offset:0\n\t"
"v_mfma_f32_16x16x32_bf16 v[148:151], a[24:27], a[184:187], v[148:151]\n\t"
"s_waitcnt lgkmcnt(4)\n\t"
"v_mfma_f32_16x16x32_bf16 v[148:151], a[28:31], a[172:175], v[148:151]\n\t"
"v_mfma_f32_16x16x32_bf16 v[148:151], a[72:75], a[180:183], v[148:151]\n\t"
"buffer_store_dwordx4 v[196:199], v248, s[16:19], s29 offen offset:0 sc1\n\t"
"v_mfma_f32_16x16x32_bf16 v[148:151], a[216:219], v[212:215], v[148:151]\n\t"
"s_waitcnt lgkmcnt(3)\n\t"
"v_cvt_pk_bf16_f32 v116, v116, v117\n\t"
"v_cvt_pk_bf16_f32 v117, v118, v119\n\t"
"v_mfma_f32_16x16x32_bf16 v[148:151], a[220:223], v[216:219], v[148:151]\n\t"
"v_mfma_f32_16x16x32_bf16 v[148:151], a[248:251], a[200:203], v[148:151]\n\t"
"buffer_store_dwordx4 v[200:203], v248, s[16:19], s29 offen offset:128 sc1\n\t"
"v_mfma_f32_16x16x32_bf16 v[180:183], a[36:39], v[216:219], v[180:183]\n\t"
"ds_read_b128 a[204:207], v223 offset:48384\n\t"
"v_mfma_f32_16x16x32_bf16 v[180:183], a[76:79], a[200:203], v[180:183]\n\t"
"v_mfma_f32_16x16x32_bf16 v[180:183], a[32:35], v[212:215], v[180:183]\n\t"
"buffer_store_dwordx4 v[204:207], v248, s[16:19], s29 offen offset:256 sc1\n\t"
"v_mfma_f32_16x16x32_bf16 v[180:183], a[0:3], a[180:183], v[180:183]\n\t"
"ds_read_b128 a[180:183], v223 offset:46848\n\t"
"v_cvt_pk_bf16_f32 v118, v148, v149\n\t"
"v_mfma_f32_16x16x32_bf16 v[180:183], a[4:7], a[184:187], v[180:183]\n\t"
"v_cvt_pk_bf16_f32 v119, v150, v151\n\t"
"v_mfma_f32_16x16x32_bf16 v[180:183], a[208:211], a[172:175], v[180:183]\n\t"
"ds_write_b128 v245, v[116:119] offset:9216\n\t"
"v_mfma_f32_16x16x32_bf16 v[20:23], a[140:143], a[192:195], v[20:23]\n\t"
"s_waitcnt lgkmcnt(5)\n\t"
"v_mfma_f32_16x16x32_bf16 v[20:23], a[228:231], a[196:199], v[20:23]\n\t"
"v_mfma_f32_16x16x32_bf16 v[20:23], a[232:235], a[176:179], v[20:23]\n\t"
"v_mfma_f32_16x16x32_bf16 v[20:23], a[148:151], v[208:211], v[20:23]\n\t"
"v_cvt_pk_bf16_f32 v180, v180, v181\n\t"
"v_cvt_pk_bf16_f32 v181, v182, v183\n\t"
"v_mfma_f32_16x16x32_bf16 v[20:23], a[104:107], a[168:171], v[20:23]\n\t"
"v_mfma_f32_16x16x32_bf16 v[20:23], a[112:115], a[188:191], v[20:23]\n\t"
"ds_write_b64 v246, v[180:181] offset:9216\n\t"
"v_mfma_f32_16x16x32_bf16 v[52:55], a[164:167], a[168:171], v[52:55]\n\t"
"v_mfma_f32_16x16x32_bf16 v[52:55], v[240:243], a[188:191], v[52:55]\n\t"
"v_mfma_f32_16x16x32_bf16 v[52:55], a[156:159], v[208:211], v[52:55]\n\t"
"v_mfma_f32_16x16x32_bf16 v[52:55], a[236:239], a[192:195], v[52:55]\n\t"
"v_cvt_pk_bf16_f32 v20, v20, v21\n\t"
"v_cvt_pk_bf16_f32 v21, v22, v23\n\t"
"v_mfma_f32_16x16x32_bf16 v[52:55], a[40:43], a[196:199], v[52:55]\n\t"
"v_mfma_f32_16x16x32_bf16 v[52:55], a[44:47], a[176:179], v[52:55]\n\t"
"v_mfma_f32_16x16x32_bf16 v[84:87], a[52:55], a[196:199], v[84:87]\n\t"
"ds_read_b128 v[212:215], v223 offset:54272\n\t"
"v_mfma_f32_16x16x32_bf16 v[84:87], a[56:59], a[176:179], v[84:87]\n\t"
"v_mfma_f32_16x16x32_bf16 v[84:87], a[48:51], a[192:195], v[84:87]\n\t"
"s_waitcnt lgkmcnt(1)\n\t"
"s_barrier\n\t"
"v_mfma_f32_16x16x32_bf16 v[84:87], a[212:215], v[208:211], v[84:87]\n\t"
"ds_read_b128 v[196:199], v255 offset:0\n\t"
"v_mfma_f32_16x16x32_bf16 v[84:87], a[240:243], a[168:171], v[84:87]\n\t"
"ds_read_b128 v[200:203], v255 offset:1536\n\t"
"v_cvt_pk_bf16_f32 v22, v52, v53\n\t"
"v_mfma_f32_16x16x32_bf16 v[84:87], a[60:63], a[188:191], v[84:87]\n\t"
"ds_read_b128 v[204:207], v255 offset:3072\n\t"
"v_cvt_pk_bf16_f32 v23, v54, v55\n\t"
"v_mfma_f32_16x16x32_bf16 v[120:123], a[16:19], v[216:219], v[120:123]\n\t"
"s_add_u32 s29, s23, 786432\n\t"
"ds_read_b128 a[192:195], v223 offset:52736\n\t"
"v_mfma_f32_16x16x32_bf16 v[120:123], a[20:23], a[200:203], v[120:123]\n\t"
"v_mfma_f32_16x16x32_bf16 v[120:123], a[120:123], a[204:207], v[120:123]\n\t"
"ds_write_b128 v245, v[20:23] offset:18432\n\t"
"v_mfma_f32_16x16x32_bf16 v[120:123], a[244:247], a[184:187], v[120:123]\n\t"
"s_waitcnt lgkmcnt(4)\n\t"
"v_cvt_pk_bf16_f32 v84, v84, v85\n\t"
"v_cvt_pk_bf16_f32 v85, v86, v87\n\t"
"v_mfma_f32_16x16x32_bf16 v[120:123], a[64:67], a[172:175], v[120:123]\n\t"
"v_mfma_f32_16x16x32_bf16 v[120:123], a[68:71], a[180:183], v[120:123]\n\t"
"ds_write_b64 v246, v[84:85] offset:18432\n\t"
"v_mfma_f32_16x16x32_bf16 v[152:155], a[24:27], a[172:175], v[152:155]\n\t"
"s_waitcnt lgkmcnt(4)\n\t"
"v_mfma_f32_16x16x32_bf16 v[152:155], a[28:31], a[180:183], v[152:155]\n\t"
"v_mfma_f32_16x16x32_bf16 v[152:155], a[72:75], a[184:187], v[152:155]\n\t"
"buffer_store_dwordx4 v[196:199], v248, s[16:19], s29 offen offset:0 sc1\n\t"
"v_mfma_f32_16x16x32_bf16 v[152:155], a[216:219], v[216:219], v[152:155]\n\t"
"s_waitcnt lgkmcnt(3)\n\t"
"v_cvt_pk_bf16_f32 v120, v120, v121\n\t"
"v_cvt_pk_bf16_f32 v121, v122, v123\n\t"
"v_mfma_f32_16x16x32_bf16 v[152:155], a[220:223], a[200:203], v[152:155]\n\t"
"v_mfma_f32_16x16x32_bf16 v[152:155], a[248:251], a[204:207], v[152:155]\n\t"
"buffer_store_dwordx4 v[200:203], v248, s[16:19], s29 offen offset:128 sc1\n\t"
"v_mfma_f32_16x16x32_bf16 v[184:187], a[36:39], a[200:203], v[184:187]\n\t"
"ds_read_b128 v[208:211], v223 offset:54528\n\t"
"v_mfma_f32_16x16x32_bf16 v[184:187], a[76:79], a[204:207], v[184:187]\n\t"
"v_mfma_f32_16x16x32_bf16 v[184:187], a[32:35], v[216:219], v[184:187]\n\t"
"buffer_store_dwordx4 v[204:207], v248, s[16:19], s29 offen offset:256 sc1\n\t"
"v_mfma_f32_16x16x32_bf16 v[184:187], a[0:3], a[184:187], v[184:187]\n\t"
"ds_read_b128 a[184:187], v223 offset:52992\n\t"
"v_cvt_pk_bf16_f32 v122, v152, v153\n\t"
"v_mfma_f32_16x16x32_bf16 v[184:187], a[4:7], a[172:175], v[184:187]\n\t"
"v_cvt_pk_bf16_f32 v123, v154, v155\n\t"
"v_mfma_f32_16x16x32_bf16 v[184:187], a[208:211], a[180:183], v[184:187]\n\t"
"ds_write_b128 v245, v[120:123] offset:27648\n\t"
"v_mfma_f32_16x16x32_bf16 v[24:27], a[140:143], a[196:199], v[24:27]\n\t"
"s_waitcnt lgkmcnt(5)\n\t"
"v_mfma_f32_16x16x32_bf16 v[24:27], a[228:231], a[176:179], v[24:27]\n\t"
"v_mfma_f32_16x16x32_bf16 v[24:27], a[232:235], v[212:215], v[24:27]\n\t"
"v_mfma_f32_16x16x32_bf16 v[24:27], a[148:151], a[168:171], v[24:27]\n\t"
"v_cvt_pk_bf16_f32 v184, v184, v185\n\t"
"v_cvt_pk_bf16_f32 v185, v186, v187\n\t"
"v_mfma_f32_16x16x32_bf16 v[24:27], a[104:107], a[188:191], v[24:27]\n\t"
"v_mfma_f32_16x16x32_bf16 v[24:27], a[112:115], a[192:195], v[24:27]\n\t"
"ds_write_b64 v246, v[184:185] offset:27648\n\t"
"v_mfma_f32_16x16x32_bf16 v[56:59], a[164:167], a[188:191], v[56:59]\n\t"
"v_mfma_f32_16x16x32_bf16 v[56:59], v[240:243], a[192:195], v[56:59]\n\t"
"v_mfma_f32_16x16x32_bf16 v[56:59], a[156:159], a[168:171], v[56:59]\n\t"
"v_mfma_f32_16x16x32_bf16 v[56:59], a[236:239], a[196:199], v[56:59]\n\t"
"v_cvt_pk_bf16_f32 v24, v24, v25\n\t"
"v_cvt_pk_bf16_f32 v25, v26, v27\n\t"
"v_mfma_f32_16x16x32_bf16 v[56:59], a[40:43], a[176:179], v[56:59]\n\t"
"v_mfma_f32_16x16x32_bf16 v[56:59], a[44:47], v[212:215], v[56:59]\n\t"
"v_mfma_f32_16x16x32_bf16 v[88:91], a[52:55], a[176:179], v[88:91]\n\t"
"ds_read_b128 v[216:219], v222 offset:61952\n\t"
"v_mfma_f32_16x16x32_bf16 v[88:91], a[56:59], v[212:215], v[88:91]\n\t"
"v_mfma_f32_16x16x32_bf16 v[88:91], a[48:51], a[196:199], v[88:91]\n\t"
"s_waitcnt lgkmcnt(1)\n\t"
"s_barrier\n\t"
"v_mfma_f32_16x16x32_bf16 v[88:91], a[212:215], a[168:171], v[88:91]\n\t"
"ds_read_b128 v[196:199], v255 offset:18432\n\t"
"v_mfma_f32_16x16x32_bf16 v[88:91], a[240:243], a[188:191], v[88:91]\n\t"
"ds_read_b128 v[200:203], v255 offset:19968\n\t"
"v_cvt_pk_bf16_f32 v26, v56, v57\n\t"
"v_mfma_f32_16x16x32_bf16 v[88:91], a[60:63], a[192:195], v[88:91]\n\t"
"ds_read_b128 v[204:207], v255 offset:21504\n\t"
"v_cvt_pk_bf16_f32 v27, v58, v59\n\t"
"v_mfma_f32_16x16x32_bf16 v[124:127], a[16:19], a[200:203], v[124:127]\n\t"
"s_add_u32 s29, s23, 983040\n\t"
"ds_read_b128 a[168:171], v222 offset:60416\n\t"
"v_mfma_f32_16x16x32_bf16 v[124:127], a[20:23], a[204:207], v[124:127]\n\t"
"v_mfma_f32_16x16x32_bf16 v[124:127], a[120:123], v[208:211], v[124:127]\n\t"
"ds_write_b128 v245, v[24:27] offset:0\n\t"
"v_mfma_f32_16x16x32_bf16 v[124:127], a[244:247], a[172:175], v[124:127]\n\t"
"s_waitcnt lgkmcnt(4)\n\t"
"v_cvt_pk_bf16_f32 v88, v88, v89\n\t"
"v_cvt_pk_bf16_f32 v89, v90, v91\n\t"
"v_mfma_f32_16x16x32_bf16 v[124:127], a[64:67], a[180:183], v[124:127]\n\t"
"v_mfma_f32_16x16x32_bf16 v[124:127], a[68:71], a[184:187], v[124:127]\n\t"
"ds_write_b64 v246, v[88:89] offset:0\n\t"
"v_mfma_f32_16x16x32_bf16 v[156:159], a[24:27], a[180:183], v[156:159]\n\t"
"s_waitcnt lgkmcnt(4)\n\t"
"v_mfma_f32_16x16x32_bf16 v[156:159], a[28:31], a[184:187], v[156:159]\n\t"
"v_mfma_f32_16x16x32_bf16 v[156:159], a[72:75], a[172:175], v[156:159]\n\t"
"buffer_store_dwordx4 v[196:199], v248, s[16:19], s29 offen offset:0 sc1\n\t"
"v_mfma_f32_16x16x32_bf16 v[156:159], a[216:219], a[200:203], v[156:159]\n\t"
"s_waitcnt lgkmcnt(3)\n\t"
"v_cvt_pk_bf16_f32 v124, v124, v125\n\t"
"v_cvt_pk_bf16_f32 v125, v126, v127\n\t"
"v_mfma_f32_16x16x32_bf16 v[156:159], a[220:223], a[204:207], v[156:159]\n\t"
"v_mfma_f32_16x16x32_bf16 v[156:159], a[248:251], v[208:211], v[156:159]\n\t"
"buffer_store_dwordx4 v[200:203], v248, s[16:19], s29 offen offset:128 sc1\n\t"
"v_mfma_f32_16x16x32_bf16 v[188:191], a[36:39], a[204:207], v[188:191]\n\t"
"ds_read_b128 a[196:199], v222 offset:62208\n\t"
"v_mfma_f32_16x16x32_bf16 v[188:191], a[76:79], v[208:211], v[188:191]\n\t"
"v_mfma_f32_16x16x32_bf16 v[188:191], a[32:35], a[200:203], v[188:191]\n\t"
"buffer_store_dwordx4 v[204:207], v248, s[16:19], s29 offen offset:256 sc1\n\t"
"v_mfma_f32_16x16x32_bf16 v[188:191], a[0:3], a[172:175], v[188:191]\n\t"
"ds_read_b128 a[172:175], v222 offset:60672\n\t"
"v_cvt_pk_bf16_f32 v126, v156, v157\n\t"
"v_mfma_f32_16x16x32_bf16 v[188:191], a[4:7], a[180:183], v[188:191]\n\t"
"v_cvt_pk_bf16_f32 v127, v158, v159\n\t"
"v_mfma_f32_16x16x32_bf16 v[188:191], a[208:211], a[184:187], v[188:191]\n\t"
"ds_write_b128 v245, v[124:127] offset:9216\n\t"
"v_mfma_f32_16x16x32_bf16 v[28:31], a[140:143], a[176:179], v[28:31]\n\t"
"s_waitcnt lgkmcnt(5)\n\t"
"v_mfma_f32_16x16x32_bf16 v[28:31], a[228:231], v[212:215], v[28:31]\n\t"
"v_mfma_f32_16x16x32_bf16 v[28:31], a[232:235], v[216:219], v[28:31]\n\t"
"v_mfma_f32_16x16x32_bf16 v[28:31], a[148:151], a[188:191], v[28:31]\n\t"
"v_cvt_pk_bf16_f32 v188, v188, v189\n\t"
"v_cvt_pk_bf16_f32 v189, v190, v191\n\t"
"v_mfma_f32_16x16x32_bf16 v[28:31], a[104:107], a[192:195], v[28:31]\n\t"
"v_mfma_f32_16x16x32_bf16 v[28:31], a[112:115], a[168:171], v[28:31]\n\t"
"ds_write_b64 v246, v[188:189] offset:9216\n\t"
"v_mfma_f32_16x16x32_bf16 v[60:63], a[164:167], a[192:195], v[60:63]\n\t"
"v_mfma_f32_16x16x32_bf16 v[60:63], v[240:243], a[168:171], v[60:63]\n\t"
"v_mfma_f32_16x16x32_bf16 v[60:63], a[156:159], a[188:191], v[60:63]\n\t"
"v_mfma_f32_16x16x32_bf16 v[60:63], a[236:239], a[176:179], v[60:63]\n\t"
"v_cvt_pk_bf16_f32 v28, v28, v29\n\t"
"v_cvt_pk_bf16_f32 v29, v30, v31\n\t"
"v_mfma_f32_16x16x32_bf16 v[60:63], a[40:43], v[212:215], v[60:63]\n\t"
"v_mfma_f32_16x16x32_bf16 v[60:63], a[44:47], v[216:219], v[60:63]\n\t"
"v_mfma_f32_16x16x32_bf16 v[92:95], a[52:55], v[212:215], v[92:95]\n\t"
"v_mfma_f32_16x16x32_bf16 v[92:95], a[56:59], v[216:219], v[92:95]\n\t"
"v_mfma_f32_16x16x32_bf16 v[92:95], a[48:51], a[176:179], v[92:95]\n\t"
"s_waitcnt lgkmcnt(0)\n\t"
"s_barrier\n\t"
"v_mfma_f32_16x16x32_bf16 v[92:95], a[212:215], a[188:191], v[92:95]\n\t"
"ds_read_b128 v[196:199], v255 offset:0\n\t"
"v_mfma_f32_16x16x32_bf16 v[92:95], a[240:243], a[192:195], v[92:95]\n\t"
"ds_read_b128 v[200:203], v255 offset:1536\n\t"
"v_cvt_pk_bf16_f32 v30, v60, v61\n\t"
"v_mfma_f32_16x16x32_bf16 v[92:95], a[60:63], a[168:171], v[92:95]\n\t"
"ds_read_b128 v[204:207], v255 offset:3072\n\t"
"v_cvt_pk_bf16_f32 v31, v62, v63\n\t"
"v_mfma_f32_16x16x32_bf16 v[128:131], a[16:19], a[204:207], v[128:131]\n\t"
"s_add_u32 s29, s23, 1179648\n\t"
"v_mfma_f32_16x16x32_bf16 v[128:131], a[20:23], v[208:211], v[128:131]\n\t"
"v_mfma_f32_16x16x32_bf16 v[128:131], a[120:123], a[196:199], v[128:131]\n\t"
"ds_write_b128 v245, v[28:31] offset:18432\n\t"
"v_mfma_f32_16x16x32_bf16 v[128:131], a[244:247], a[180:183], v[128:131]\n\t"
"s_waitcnt lgkmcnt(3)\n\t"
"v_cvt_pk_bf16_f32 v92, v92, v93\n\t"
"v_cvt_pk_bf16_f32 v93, v94, v95\n\t"
"v_mfma_f32_16x16x32_bf16 v[128:131], a[64:67], a[184:187], v[128:131]\n\t"
"v_mfma_f32_16x16x32_bf16 v[128:131], a[68:71], a[172:175], v[128:131]\n\t"
"ds_write_b64 v246, v[92:93] offset:18432\n\t"
"v_mfma_f32_16x16x32_bf16 v[160:163], a[24:27], a[184:187], v[160:163]\n\t"
"s_waitcnt lgkmcnt(3)\n\t"
"v_mfma_f32_16x16x32_bf16 v[160:163], a[28:31], a[172:175], v[160:163]\n\t"
"v_mfma_f32_16x16x32_bf16 v[160:163], a[72:75], a[180:183], v[160:163]\n\t"
"buffer_store_dwordx4 v[196:199], v248, s[16:19], s29 offen offset:0 sc1\n\t"
"v_mfma_f32_16x16x32_bf16 v[160:163], a[216:219], a[204:207], v[160:163]\n\t"
"s_waitcnt lgkmcnt(2)\n\t"
"v_cvt_pk_bf16_f32 v128, v128, v129\n\t"
"v_cvt_pk_bf16_f32 v129, v130, v131\n\t"
"v_mfma_f32_16x16x32_bf16 v[160:163], a[220:223], v[208:211], v[160:163]\n\t"
"v_mfma_f32_16x16x32_bf16 v[160:163], a[248:251], a[196:199], v[160:163]\n\t"
"buffer_store_dwordx4 v[200:203], v248, s[16:19], s29 offen offset:128 sc1\n\t"
"v_mfma_f32_16x16x32_bf16 v[192:195], a[36:39], v[208:211], v[192:195]\n\t"
"v_mfma_f32_16x16x32_bf16 v[192:195], a[76:79], a[196:199], v[192:195]\n\t"
"v_mfma_f32_16x16x32_bf16 v[192:195], a[32:35], a[204:207], v[192:195]\n\t"
"buffer_store_dwordx4 v[204:207], v248, s[16:19], s29 offen offset:256 sc1\n\t"
"v_mfma_f32_16x16x32_bf16 v[192:195], a[0:3], a[180:183], v[192:195]\n\t"
"v_cvt_pk_bf16_f32 v130, v160, v161\n\t"
"v_cvt_pk_bf16_f32 v131, v162, v163\n\t"
"v_mfma_f32_16x16x32_bf16 v[192:195], a[4:7], a[184:187], v[192:195]\n\t"
"v_mfma_f32_16x16x32_bf16 v[192:195], a[208:211], a[172:175], v[192:195]\n\t"
"ds_write_b128 v245, v[128:131] offset:27648\n\t"
"s_nop 6\n\t"
"v_cvt_pk_bf16_f32 v192, v192, v193\n\t"
"v_cvt_pk_bf16_f32 v193, v194, v195\n\t"
"ds_write_b64 v246, v[192:193] offset:27648\n\t"
"s_waitcnt lgkmcnt(0)\n\t"
"s_barrier\n\t"
"ds_read_b128 v[196:199], v255 offset:18432\n\t"
"ds_read_b128 v[200:203], v255 offset:19968\n\t"
"ds_read_b128 v[208:211], v255 offset:21504\n\t"
"s_waitcnt lgkmcnt(2)\n\t"
"s_add_u32 s29, s23, 1376256\n\t"
"buffer_store_dwordx4 v[196:199], v248, s[16:19], s29 offen offset:0 sc1\n\t"
"s_waitcnt lgkmcnt(1)\n\t"
"buffer_store_dwordx4 v[200:203], v248, s[16:19], s29 offen offset:128 sc1\n\t"
"s_waitcnt lgkmcnt(0)\n\t"
"buffer_store_dwordx4 v[208:211], v248, s[16:19], s29 offen offset:256 sc1\n\t"
"s_waitcnt vmcnt(0)\n\t"
"Lwino_end:\n\t"
  : "+{v205}"(tid)
  : "s"((unsigned long long)__builtin_amdgcn_kernarg_segment_ptr()), "s"(blockIdx.x), "s"((unsigned long long)lane_tab), "s"((unsigned)(size_t)(__attribute__((address_space(3))) char*)smem), "s"((unsigned)(size_t)__builtin_amdgcn_dispatch_ptr())
  : "v0", "v1", "v2", "v3", "v4", "v5", "v6", "v7", "v8", "v9", "v10", "v11", "v12", "v13", "v14", "v15", "v16", "v17", "v18", "v19", "v20", "v21", "v22", "v23", "v24", "v25", "v26", "v27", "v28", "v29", "v30", "v31", "v32", "v33", "v34", "v35", "v36", "v37", "v38", "v39", "v40", "v41", "v42", "v43", "v44", "v45", "v46", "v47", "v48", "v49", "v50", "v51", "v52", "v53", "v54", "v55", "v56", "v57", "v58", "v59", "v60", "v61", "v62", "v63", "v64", "v65", "v66", "v67", "v68", "v69", "v70", "v71", "v72", "v73", "v74", "v75", "v76", "v77", "v78", "v79", "v80", "v81", "v82", "v83", "v84", "v85", "v86", "v87", "v88", "v89", "v90", "v91", "v92", "v93", "v94", "v95", "v96", "v97", "v98", "v99", "v100", "v101", "v102", "v103", "v104", "v105", "v106", "v107", "v108", "v109", "v110", "v111", "v112", "v113", "v114", "v115", "v116", "v117", "v118", "v119", "v120", "v121", "v122", "v123", "v124", "v125", "v126", "v127", "v128", "v129", "v130", "v131", "v132", "v133", "v134", "v135", "v136", "v137", "v138", "v139", "v140", "v141", "v142", "v143", "v144", "v145", "v146", "v147", "v148", "v149", "v150", "v151", "v152", "v153", "v154", "v155", "v156", "v157", "v158", "v159", "v160", "v161", "v162", "v163", "v164", "v165", "v166", "v167", "v168", "v169", "v170", "v171", "v172", "v173", "v174", "v175", "v176", "v177", "v178", "v179", "v180", "v181", "v182", "v183", "v184", "v185", "v186", "v187", "v188", "v189", "v190", "v191", "v192", "v193", "v194", "v195", "v196", "v197", "v198", "v199", "v200", "v201", "v202", "v203", "v204", "v206", "v207", "v208", "v209", "v210", "v211", "v212", "v213", "v214", "v215", "v216", "v217", "v218", "v219", "v220", "v221", "v222", "v223", "v224", "v225", "v226", "v227", "v228", "v229", "v230", "v231", "v232", "v233", "v234", "v235", "v236", "v237", "v238", "v239", "v240", "v241", "v242", "v243", "v244", "v245", "v246", "v247", "v248", "v249", "v250", "v251", "v252", "v253", "v254", "v255", "a0", "a1", "a2", "a3", "a4", "a5", "a6", "a7", "a8", "a9", "a10", "a11", "a12", "a13", "a14", "a15", "a16", "a17", "a18", "a19", "a20", "a21", "a22", "a23", "a24", "a25", "a26", "a27", "a28", "a29", "a30", "a31", "a32", "a33", "a34", "a35", "a36", "a37", "a38", "a39", "a40", "a41", "a42", "a43", "a44", "a45", "a46", "a47", "a48", "a49", "a50", "a51", "a52", "a53", "a54", "a55", "a56", "a57", "a58", "a59", "a60", "a61", "a62", "a63", "a64", "a65", "a66", "a67", "a68", "a69", "a70", "a71", "a72", "a73", "a74", "a75", "a76", "a77", "a78", "a79", "a80", "a81", "a82", "a83", "a84", "a85", "a86", "a87", "a88", "a89", "a90", "a91", "a92", "a93", "a94", "a95", "a96", "a97", "a98", "a99", "a100", "a101", "a102", "a103", "a104", "a105", "a106", "a107", "a108", "a109", "a110", "a111", "a112", "a113", "a114", "a115", "a116", "a117", "a118", "a119", "a120", "a121", "a122", "a123", "a124", "a125", "a126", "a127", "a128", "a129", "a130", "a131", "a132", "a133", "a134", "a135", "a136", "a137", "a138", "a139", "a140", "a141", "a142", "a143", "a144", "a145", "a146", "a147", "a148", "a149", "a150", "a151", "a152", "a153", "a154", "a155", "a156", "a157", "a158", "a159", "a160", "a161", "a162", "a163", "a164", "a165", "a166", "a167", "a168", "a169", "a170", "a171", "a172", "a173", "a174", "a175", "a176", "a177", "a178", "a179", "a180", "a181", "a182", "a183", "a184", "a185", "a186", "a187", "a188", "a189", "a190", "a191", "a192", "a193", "a194", "a195", "a196", "a197", "a198", "a199", "a200", "a201", "a202", "a203", "a204", "a205", "a206", "a207", "a208", "a209", "a210", "a211", "a212", "a213", "a214", "a215", "a216", "a217", "a218", "a219", "a220", "a221", "a222", "a223", "a224", "a225", "a226", "a227", "a228", "a229", "a230", "a231", "a232", "a233", "a234", "a235", "a236", "a237", "a238", "a239", "a240", "a241", "a242", "a243", "a244", "a245", "a246", "a247", "a248", "a249", "a250", "a251", "a252", "a253", "a254", "a255", "s8", "s9", "s10", "s11", "s12", "s13", "s14", "s15", "s16", "s17", "s18", "s19", "s20", "s21", "s22", "s23", "s24", "s25", "s26", "s27", "s28", "s29", "s30", "s31", "s32", "s33", "s34", "s35", "s36", "s37", "s38", "s39", "s40", "s41", "s42", "s43", "s44", "s45", "s46", "s47", "s48", "s49", "s50", "s51", "s52", "s53", "s54", "s55", "s56", "s57", "s58", "s59", "s60", "s61", "s62", "s63", "s64", "s65", "s66", "s67", "s68", "s69", "s70", "s71", "s72", "s73", "s74", "s75", "s76", "s77", "s78", "s79", "s80", "s81", "s82", "s83", "scc", "vcc", "memory");
}

extern "C" void kernel_launch(void* const* d_in, const int* in_sizes, int n_in, void* d_out, int out_size, void* d_ws,
                              size_t ws_size, hipStream_t stream) {
  wino_main_kernel<<<256, 256, 0, stream>>>((const char*)d_in[0], (char*)d_ws, (char*)d_out, (const char*)d_in[1],
                                            ws_size >= (36ull << 20) ? ~0ull : 0ull);
}
```

```cpp
#include <hip/hip_runtime.h>
#include <hip/hip_bf16.h>
#include <stdint.h>

#define IMG_W 512
#define CH 192
#define PXB 384
#define ROWB (IMG_W * PXB)
#define U_BYTES 1769472

__device__ const __attribute__((aligned(1024))) unsigned lane_tab[6144] = {
  0x0,0x0,0x0,0x0,0x0,0x0,0x0,0x0,0x0,0x0,0x0,0x0,0x0,0x0,0x0,0x0,
  0x0,0x0,0x0,0x0,0x0,0x0,0x0,0x0,0x0,0x0,0x0,0x0,0x0,0x0,0x0,0x0,
  0x0,0x0,0x0,0x0,0x0,0x0,0x0,0x0,0x0,0x0,0x0,0x0,0x0,0x0,0x0,0x0,
  0x0,0x0,0x0,0x0,0x0,0x0,0x0,0x0,0x0,0x0,0x0,0x0,0x0,0x0,0x0,0x0,
  0x0,0x0,0x0,0x0,0x0,0x0,0x0,0x0,0x0,0x0,0x0,0x0,0x0,0x0,0x0,0x0,
  0x0,0x0,0x0,0x0,0x0,0x0,0x0,0x0,0x0,0x0,0x0,0x0,0x0,0x0,0x0,0x0,
  0x0,0x0,0x0,0x0,0x0,0x0,0x0,0x0,0x0,0x0,0x0,0x0,0x0,0x0,0x0,0x0,
  0x0,0x0,0x0,0x0,0x0,0x0,0x0,0x0,0x0,0x0,0x0,0x0,0x0,0x0,0x0,0x0,
  0x0,0x0,0x0,0x0,0x0,0x0,0x0,0x0,0x0,0x0,0x0,0x0,0x0,0x0,0x0,0x0,
  0x0,0x0,0x0,0x0,0x0,0x0,0x0,0x0,0x0,0x0,0x0,0x0,0x0,0x0,0x0,0x0,
  0x0,0x0,0x0,0x0,0x0,0x0,0x0,0x0,0x0,0x0,0x0,0x0,0x0,0x0,0x0,0x0,
  0x0,0x0,0x0,0x0,0x0,0x0,0x0,0x0,0x0,0x0,0x0,0x0,0x0,0x0,0x0,0x0,
  0x0,0x0,0x0,0x0,0x0,0x0,0x0,0x0,0x0,0x0,0x0,0x0,0x0,0x0,0x0,0x0,
  0x0,0x0,0x0,0x0,0x0,0x0,0x0,0x0,0x0,0x0,0x0,0x0,0x0,0x0,0x0,0x0,
  0x0,0x0,0x0,0x0,0x0,0x0,0x0,0x0,0x0,0x0,0x0,0x0,0x0,0x0,0x0,0x0,
  0x0,0x0,0x0,0x0,0x0,0x0,0x0,0x0,0x0,0x0,0x0,0x0,0x0,0x0,0x0,0x0,
  0x0,0xc042,0x0,0x0,0x0,0xc0420000,0x0,0x0,0x0,0x0,0x0,0xc042,0x0,0x0,0x0,0xc0420000,
  0x3f903f90,0x0,0x0,0x0,0x3f900000,0x0,0x3f90,0x0,0x0,0x0,0x3f903f90,0x0,0x0,0x0,0x3f900000,0x0,
  0x0,0x0,0x0,0x0,0x0,0x0,0x0,0x0,0x0,0x0,0x0,0x0,0x0,0x0,0x0,0x0,
  0x0,0x0,0x0,0x0,0x0,0x0,0x0,0x0,0x0,0x0,0x0,0x0,0x0,0x0,0x0,0x0,
  0x0,0x0,0x3f90,0x0,0x0,0x0,0x0,0x0,0x0,0x0,0x0,0x0,0x0,0x0,0x0,0x0,
  0x0,0x0,0x0,0x0,0xc0420000,0x0,0x0,0x0,0x0,0x0,0x0,0x0,0x0,0x0,0xc0420000,0x0,
  0x0,0x3f90,0x0,0x0,0x0,0x3f903f90,0x0,0x0,0x0,0x3f900000,0x0,0x3f90,0x0,0x0,0x0,0x3f903f90,
  0x0,0x3f90,0x0,0x0,0x0,0x3f903f90,0x0,0x0,0x0,0x3f900000,0x0,0x3f90,0x0,0x0,0x0,0x3f903f90,
  0x0,0x0,0x0,0x0,0x0,0x0,0x0,0x0,0x0,0x0,0x0,0x0,0x0,0x0,0x0,0x0,
  0x0,0x0,0x0,0x0,0x0,0x0,0x0,0x0,0x0,0x0,0x0,0x0,0x0,0x0,0x3f90,0x0,
  0x0,0xc042,0x3f900000,0x0,0x0,0xc0420000,0x0,0x0,0x0,0x0,0x0,0xc042,0x0,0x0,0x0,0xc0420000,
  0x0,0xc042,0x3f900000,0x0,0x0,0xc0420000,0x0,0x0,0x0,0x0,0x0,0xc042,0x0,0x0,0x0,0xc0420000,
  0x3f900000,0x0,0x0,0x0,0x3f900000,0x3f90,0x0,0x0,0x0,0x3f90,0x3f900000,0x0,0x0,0x0,0x3f900000,0x3f90,
  0xc042,0x0,0x0,0x0,0x0,0x0,0x0,0x0,0x0,0x0,0xc042,0x0,0x0,0x0,0x0,0x0,
  0x0,0x0,0x0,0x0,0x0,0x0,0x0,0x0,0x0,0x0,0x0,0x0,0x0,0x0,0x0,0x0,
  0x0,0x0,0x0,0x0,0x0,0x0,0x0,0x0,0x0,0x0,0x0,0x0,0x0,0x0,0x0,0x0,
  0xa8000000,0xae0,0xa800,0x60,0xa8080040,0x400af1,0xa920,0x600000,0xa8100090,0x900a82,0xa808,0x6000,0xa81800d0,0xd00a93,0xa928,0x60000000,
  0xa9200120,0x24000ae4,0xa840,0x40004,0xa9280160,0x24400af5,0xa960,0x40400,0xa93001b0,0x24900a86,0xa848,0x4000400,0xa93801f0,0x24d00a97,0xa968,0x4000000,
  0xa8401000,0x2000ae1,0xa880,0x0,0xa8481040,0x2400af0,0xa9a0,0x0,0xa8501090,0x2900a83,0xa888,0x0,0xa85810d0,0x2d00a92,0xa9a8,0x0,
  0xa9601120,0x2000ae5,0xa8c0,0x0,0xa9681160,0x2400af4,0xa9e0,0x0,0xa97011b0,0x2900a87,0xa8c8,0x0,0xa97811f0,0x2d00a96,0xa9e8,0x0,
  0xea800010,0x100ae2,0xa810,0x400,0xea880050,0x500af3,0xa930,0x0,0xea900080,0x800a80,0xa818,0x0,0xea9800c0,0xc00a91,0xa938,0x0,
  0xa9a00130,0x24100ae6,0xa850,0x0,0xa9a80170,0x24500af7,0xa970,0x60000,0xa9b001a0,0x24800a84,0xa858,0x0,0xa9b801e0,0x24c00a95,0xa978,0x6000000,
  0xc0c01010,0x2100ae3,0xa890,0x40,0xc0c81050,0x2500af2,0xa9b0,0x400040,0xc0d01080,0x2800a81,0xa898,0x404000,0xc0d810c0,0x2c00a90,0xa9b8,0x40004000,
  0xc1e01130,0x2100ae7,0xa8d0,0x40,0xc1e81170,0x2500af6,0xa9f0,0x400040,0xc1f011a0,0x2800a85,0xa8d8,0x404000,0xc1f811e0,0x2c00a94,0xa9f8,0x40004000,
  0xae000020,0x200ae9,0xae00,0x0,0xae080060,0x600af8,0xaf20,0x0,0xae1000b0,0xb00a8b,0xae08,0x0,0xae1800f0,0xf00a9a,0xaf28,0x0,
  0xeb200100,0x24200aed,0xae40,0x0,0xeb280140,0x24600afc,0xaf60,0x0,0xeb300190,0x24b00a8f,0xae48,0x0,0xeb3801d0,0x24f00a9e,0xaf68,0x400,
  0xc0401020,0x2200ae8,0xae80,0x4000060,0xc0481060,0x2600af9,0xafa0,0x600000,0xc05010b0,0x2b00a8a,0xae88,0x6000,0xc05810f0,0x2f00a9b,0xafa8,0x60000000,
  0xc1601100,0x2200aec,0xaec0,0x4000060,0xc1681140,0x2600afd,0xafe0,0x600000,0xc1701190,0x2b00a8e,0xaec8,0x6000,0xc17811d0,0x2f00a9f,0xafe8,0x60000000,
  0xa8800030,0x300aeb,0xae10,0x40000,0xa8880070,0x700afa,0xaf30,0x40040,0xa89000a0,0xa00a89,0xae18,0x4000040,0xa89800e0,0xe00a98,0xaf38,0x4004000,
  0xa8c00110,0x24300aef,0xae50,0x6,0xa8c80150,0x24700afe,0xaf70,0x0,0xa8d00180,0x24a00a8d,0xae58,0x600,0xa8d801c0,0x24e00a9c,0xaf78,0x0,
  0xa9e01030,0x2300aea,0xae90,0x0,0xa9e81070,0x2700afb,0xafb0,0x0,0xa9f010a0,0x2a00a88,0xae98,0x0,0xa9f810e0,0x2e00a99,0xafb8,0x0,
  0xae001110,0x2300aee,0xaed0,0x0,0xae081150,0x2700aff,0xaff0,0x0,0xae101180,0x2a00a8c,0xaed8,0x0,0xae1811c0,0x2e00a9d,0xaff8,0x0,
  0x0,0x0,0x0,0x0,0x0,0x0,0x0,0x0,0x0,0x0,0x0,0x0,0x0,0x0,0x0,0x0,
  0x0,0x0,0x0,0x0,0x0,0x0,0x0,0x0,0x0,0x0,0x0,0x0,0x0,0x0,0x0,0x0,
  0x0,0x0,0x0,0x0,0x0,0x0,0x0,0x0,0x0,0x0,0x0,0x0,0x0,0x0,0x0,0x0,
  0x0,0x0,0x0,0x0,0x0,0x0,0x0,0x0,0x0,0x0,0x0,0x0,0x0,0x0,0x0,0x0,
  0x0,0x0,0x0,0x0,0x0,0x0,0x0,0x0,0x0,0x0,0x0,0x0,0x0,0x0,0x0,0x0,
  0x0,0x0,0x0,0x0,0x0,0x0,0x0,0x0,0x0,0x0,0x0,0x0,0x0,0x0,0x0,0x0,
  0x0,0x0,0x0,0x0,0x0,0x0,0x0,0x0,0x0,0x0,0x0,0x0,0x0,0x0,0x0,0x0,
  0x0,0x0,0x0,0x0,0x0,0x0,0x0,0x0,0x0,0x0,0x0,0x0,0x0,0x0,0x0,0x0,
  0x0,0x0,0x0,0x0,0x0,0x0,0x0,0x0,0x0,0x0,0x0,0x0,0x0,0x0,0x0,0x0,
  0x0,0x0,0x0,0x0,0x0,0x0,0x0,0x0,0x0,0x0,0x0,0x0,0x0,0x0,0x0,0x0,
  0x0,0x0,0x0,0x0,0x0,0x0,0x0,0x0,0x0,0x0,0x0,0x0,0x0,0x0,0x0,0x0,
  0x0,0x0,0x0,0x0,0x0,0x0,0x0,0x0,0x0,0x0,0x0,0x0,0x0,0x0,0x0,0x0,
  0x0,0x0,0x0,0x0,0x0,0x0,0x0,0x0,0x0,0x0,0x0,0x0,0x0,0x0,0x0,0x0,
  0x0,0x0,0x0,0x0,0x0,0x0,0x0,0x0,0x0,0x0,0x0,0x0,0x0,0x0,0x0,0x0,
  0x0,0x0,0x0,0x0,0x0,0x0,0x0,0x0,0x0,0x0,0x0,0x0,0x0,0x0,0x0,0x0,
  0x0,0x0,0x0,0x0,0x0,0x0,0x0,0x0,0x0,0x0,0x0,0x0,0x0,0x0,0x0,0x0,
  0xc0420000,0x0,0x0,0x0,0x0,0x0,0x0,0x0,0x0,0x0,0xc0420000,0x0,0x0,0x0,0x0,0x0,
  0x3f90,0x0,0x0,0x0,0x0,0x0,0x3f90,0x0,0x0,0x0,0x3f90,0x0,0x0,0x0,0x0,0x0,
  0x0,0xc042,0x0,0x0,0x0,0xc0420000,0x0,0x0,0x0,0x0,0x0,0xc042,0x0,0x0,0x0,0xc0420000,
  0x0,0xc042,0x0,0x0,0x0,0xc0420000,0x0,0x0,0x0,0x0,0x0,0xc042,0x0,0x0,0x0,0xc0420000,
  0x0,0x0,0x0,0x3f90,0x0,0x0,0x0,0x0,0x0,0x0,0x0,0x0,0x0,0x0,0x0,0x0,
  0x0,0x0,0x0,0x0,0xc042,0x0,0x0,0x0,0x0,0x0,0x0,0x0,0x0,0x0,0xc042,0x0,
  0x3f900000,0x0,0x0,0x3f900000,0x3f900000,0x0,0x0,0x0,0x0,0x0,0x3f900000,0x0,0x0,0x0,0x3f900000,0x0,
  0x3f900000,0x0,0x0,0x3f900000,0x3f900000,0x0,0x0,0x0,0x0,0x0,0x3f900000,0x0,0x0,0x0,0x3f900000,0x0,
  0x0,0x0,0x0,0x0,0x0,0xc042,0x0,0x0,0x0,0x0,0x0,0x0,0x0,0x0,0x0,0xc042,
  0x3f90,0xc0420000,0x0,0x0,0x3f90,0x0,0x0,0x0,0x0,0x0,0x3f90,0xc0420000,0x0,0x0,0x3f903f90,0x0,
  0x0,0x0,0x0,0x0,0x0,0x0,0x0,0x0,0x0,0x0,0x0,0x0,0x0,0x0,0x0,0x0,
  0x0,0x0,0x0,0x0,0x0,0x0,0x0,0x0,0x0,0x0,0x0,0x0,0x0,0x0,0x0,0x0,
  0x3f90,0x0,0x0,0x0,0x3f903f90,0x0,0x0,0x0,0x3f900000,0x0,0x3f90,0x0,0x0,0x0,0x3f903f90,0x0,
  0x0,0x0,0x0,0x0,0x0,0x0,0x0,0x0,0x0,0x0,0x0,0x0,0x0,0x0,0x0,0x0,
  0x0,0x0,0x0,0x0,0x0,0x3f90,0x0,0x0,0x0,0x3f90,0x0,0x0,0x0,0x0,0x0,0x3f90,
  0x0,0x0,0x0,0x0,0x0,0x3f90,0x0,0x0,0x0,0x3f90,0x0,0x0,0x0,0x0,0x0,0x3f90,
  0xb4000000,0x1200ba2,0xb400,0x60000,0xb4080040,0x1600bb3,0xb520,0x0,0xb4100090,0x1b00b40,0xb408,0x6000000,0xb41800d0,0x1f00b51,0xb528,0x0,
  0xb4400120,0x25200ba6,0xb440,0x4,0xb4480160,0x25600bb7,0xb560,0x400,0xb45001b0,0x25b00b44,0xb448,0x400,0xb45801f0,0x25f00b55,0xb568,0x0,
  0xcc401000,0x3200ba3,0xb480,0x60,0xcc481040,0x3600bb2,0xb5a0,0x600000,0xcc501090,0x3b00b41,0xb488,0x6000,0xcc5810d0,0x3f00b50,0xb5a8,0x60000000,
  0xcd601120,0x3200ba7,0xb4c0,0x60,0xcd681160,0x3600bb6,0xb5e0,0x600000,0xcd7011b0,0x3b00b45,0xb4c8,0x6000,0xcd7811f0,0x3f00b54,0xb5e8,0x60000000,
  0xb5a00010,0x1300ba0,0xb410,0x4000,0xb5a80050,0x1700bb1,0xb530,0x0,0xb5b00080,0x1a00b42,0xb418,0x0,0xb5b800c0,0x1e00b53,0xb538,0x0,
  0xccc00130,0x25300ba4,0xb450,0x0,0xccc80170,0x25700bb5,0xb570,0x6,0xccd001a0,0x25a00b46,0xb458,0x0,0xccd801e0,0x25e00b57,0xb578,0x600,
  0xa9e01010,0x3300ba1,0xb490,0x40040000,0xa9e81050,0x3700bb0,0xb5b0,0x40000,0xa9f01080,0x3a00b43,0xb498,0x4000000,0xa9f810c0,0x3e00b52,0xb5b8,0x4000000,
  0xc1e01130,0x3300ba5,0xb4d0,0x40040000,0xc1e81170,0x3700bb4,0xb5f0,0x40000,0xc1f011a0,0x3a00b47,0xb4d8,0x4000000,0xc1f811e0,0x3e00b56,0xb5f8,0x4000000,
  0xb5200020,0x1000bab,0xba00,0x0,0xb5280060,0x1400bba,0xbb20,0x60,0xb53000b0,0x1900b49,0xba08,0x0,0xb53800f0,0x1d00b58,0xbb28,0x6000,
  0xea400100,0x25000baf,0xba40,0x600004,0xea480140,0x25400bbe,0xbb60,0x4,0xea500190,0x25900b4d,0xba48,0x60000400,0xea5801d0,0x25d00b5c,0xbb68,0x4000400,
  0xb5601020,0x3000baa,0xba80,0x0,0xb5681060,0x3400bbb,0xbba0,0x0,0xb57010b0,0x3900b48,0xba88,0x0,0xb57810f0,0x3d00b59,0xbba8,0x0,
  0xb4801100,0x3000bae,0xbac0,0x0,0xb4881140,0x3400bbf,0xbbe0,0x0,0xb4901190,0x3900b4c,0xbac8,0x0,0xb49811d0,0x3d00b5d,0xbbe8,0x0,
  0xb4c00030,0x1100ba9,0xba10,0x4,0xb4c80070,0x1500bb8,0xbb30,0x40004,0xb4d000a0,0x1800b4b,0xba18,0x40400,0xb4d800e0,0x1c00b5a,0xbb38,0x4000400,
  0xb5e00110,0x25100bad,0xba50,0x0,0xb5e80150,0x25500bbc,0xbb70,0x0,0xb5f00180,0x25800b4f,0xba58,0x0,0xb5f801c0,0x25c00b5e,0xbb78,0x0,
  0xcde01030,0x3100ba8,0xba90,0x0,0xcde81070,0x3500bb9,0xbbb0,0x40,0xcdf010a0,0x3800b4a,0xba98,0x40,0xcdf810e0,0x3c00b5b,0xbbb8,0x4000,
  0xb5201110,0x3100bac,0xbad0,0x0,0xb5281150,0x3500bbd,0xbbf0,0x40,0xb5301180,0x3800b4e,0xbad8,0x40,0xb53811c0,0x3c00b5f,0xbbf8,0x4000,
  0x0,0x0,0x0,0x0,0x0,0x0,0x0,0x0,0x0,0x0,0x0,0x0,0x0,0x0,0x0,0x0,
  0x0,0x0,0x0,0x0,0x0,0x0,0x0,0x0,0x0,0x0,0x0,0x0,0x0,0x0,0x0,0x0,
  0x0,0x0,0x0,0x0,0x0,0x0,0x0,0x0,0x0,0x0,0x0,0x0,0x0,0x0,0x0,0x0,
  0x0,0x0,0x0,0x0,0x0,0x0,0x0,0x0,0x0,0x0,0x0,0x0,0x0,0x0,0x0,0x0,
  0x0,0x0,0x0,0x0,0x0,0x0,0x0,0x0,0x0,0x0,0x0,0x0,0x0,0x0,0x0,0x0,
  0x0,0x0,0x0,0x0,0x0,0x0,0x0,0x0,0x0,0x0,0x0,0x0,0x0,0x0,0x0,0x0,
  0x0,0x0,0x0,0x0,0x0,0x0,0x0,0x0,0x0,0x0,0x0,0x0,0x0,0x0,0x0,0x0,
  0x0,0x0,0x0,0x0,0x0,0x0,0x0,0x0,0x0,0x0,0x0,0x0,0x0,0x0,0x0,0x0,
  0x0,0x0,0x0,0x0,0x0,0x0,0x0,0x0,0x0,0x0,0x0,0x0,0x0,0x0,0x0,0x0,
  0x0,0x0,0x0,0x0,0x0,0x0,0x0,0x0,0x0,0x0,0x0,0x0,0x0,0x0,0x0,0x0,
  0x0,0x0,0x0,0x0,0x0,0x0,0x0,0x0,0x0,0x0,0x0,0x0,0x0,0x0,0x0,0x0,
  0x0,0x0,0x0,0x0,0x0,0x0,0x0,0x0,0x0,0x0,0x0,0x0,0x0,0x0,0x0,0x0,
  0x0,0x0,0x0,0x0,0x0,0x0,0x0,0x0,0x0,0x0,0x0,0x0,0x0,0x0,0x0,0x0,
  0x0,0x0,0x0,0x0,0x0,0x0,0x0,0x0,0x0,0x0,0x0,0x0,0x0,0x0,0x0,0x0,
  0x0,0x0,0x0,0x0,0x0,0x0,0x0,0x0,0x0,0x0,0x0,0x0,0x0,0x0,0x0,0x0,
  0x0,0x0,0x0,0x0,0x0,0x0,0x0,0x0,0x0,0x0,0x0,0x0,0x0,0x0,0x0,0x0,
  0x3f90,0x3f90,0x0,0x0,0x0,0x3f90,0x3f90,0x0,0x0,0x0,0x3f90,0x3f90,0x0,0x0,0x0,0x3f90,
  0x0,0xc0420000,0x0,0x0,0x0,0x0,0x0,0x0,0x0,0x0,0x0,0xc0420000,0x0,0x0,0x0,0x0,
  0x3f900000,0x0,0x0,0x0,0x0,0x0,0x3f900000,0x0,0x0,0x0,0x3f900000,0x0,0x0,0x0,0x0,0x0,
  0x3f900000,0x0,0x0,0x0,0x0,0x0,0x3f900000,0x0,0x0,0x0,0x3f900000,0x0,0x0,0x0,0x0,0x0,
  0xc042,0x0,0x0,0x0,0xc0420000,0x0,0x0,0x0,0x0,0x0,0xc042,0x0,0x0,0x0,0xc0420000,0x0,
  0x0,0x0,0x0,0x0,0x0,0x3f900000,0x0,0x0,0x0,0x3f900000,0x0,0x0,0x0,0x0,0x0,0x3f900000,
  0x0,0x0,0x0,0x0,0x0,0xc042,0x0,0x0,0x0,0x0,0x0,0x0,0x0,0x0,0x0,0xc042,
  0x0,0x0,0x0,0x0,0x0,0xc042,0x0,0x0,0x0,0x0,0x0,0x0,0x0,0x0,0x0,0xc042,
  0x0,0x0,0x0,0x0,0x0,0x0,0x0,0x0,0x0,0x0,0x0,0x0,0x0,0x0,0x3f90,0x0,
  0x0,0x0,0x0,0x0,0x0,0xc0420000,0x0,0x0,0x0,0x0,0x0,0x0,0x0,0x0,0x0,0xc0420000,
  0x0,0x3f90,0x0,0x0,0x0,0x3f90,0x0,0x0,0x0,0x0,0x0,0x3f90,0x0,0x0,0x3f900000,0x3f90,
  0x0,0x3f90,0x0,0x0,0x0,0x3f90,0x0,0x0,0x0,0x0,0x0,0x3f90,0x0,0x0,0x3f900000,0x3f90,
  0x0,0x0,0x0,0x0,0x0,0x0,0x0,0x0,0x0,0x0,0x0,0x0,0x0,0x0,0x0,0x0,
  0x0,0x3f90,0x3f900000,0x0,0x0,0x3f90,0x0,0x0,0x0,0x0,0x0,0x3f90,0x0,0x0,0x0,0x3f90,
  0xc042,0x0,0x0,0x0,0x0,0x0,0x0,0x0,0x0,0x0,0xc042,0x0,0x0,0x0,0x0,0x0,
  0xc042,0x0,0x0,0x0,0x0,0x0,0x0,0x0,0x0,0x0,0xc042,0x0,0x0,0x0,0x0,0x0,
  0xc0000000,0x26001980,0x19200,0x44,0xc0080040,0x26401991,0x19320,0x440,0xc0100090,0x26901922,0x19208,0x4400,0xc01800d0,0x26d01933,0x19328,0x4000,
  0xd8000120,0x4001984,0x19240,0x600000,0xd8080160,0x4401995,0x19360,0x0,0xd81001b0,0x4901926,0x19248,0x60000000,0xd81801f0,0x4d01937,0x19368,0x0,
  0xc1201000,0x28001981,0x19280,0x40000,0xc1281040,0x28401990,0x193a0,0x4000000,0xc1301090,0x28901923,0x19288,0x4000000,0xc13810d0,0x28d01932,0x193a8,0x0,
  0xd8401120,0x28001985,0x192c0,0x40000,0xd8481160,0x28401994,0x193e0,0x4000000,0xd85011b0,0x28901927,0x192c8,0x4000000,0xd85811f0,0x28d01936,0x193e8,0x0,
  0xc0800010,0x26101982,0x19210,0x6,0xc0880050,0x26501993,0x19330,0x60000,0xc0900080,0x26801920,0x19218,0x600,0xc09800c0,0x26c01931,0x19338,0x6000000,
  0xc1a00130,0x4101986,0x19250,0x0,0xc1a80170,0x4501997,0x19370,0x400000,0xc1b001a0,0x4801924,0x19258,0x400000,0xc1b801e0,0x4c01935,0x19378,0x40000000,
  0xd9a01010,0x28101983,0x19290,0x0,0xd9a81050,0x28501992,0x193b0,0x60,0xd9b01080,0x28801921,0x19298,0x0,0xd9b810c0,0x28c01930,0x193b8,0x6000,
  0xd9e01130,0x28101987,0x192d0,0x0,0xd9e81170,0x28501996,0x193f0,0x60,0xd9f011a0,0x28801925,0x192d8,0x0,0xd9f811e0,0x28c01934,0x193f8,0x6000,
  0xc6000020,0x26201989,0x19800,0x0,0xc6080060,0x26601998,0x19920,0x0,0xc61000b0,0x26b0192b,0x19808,0x0,0xc61800f0,0x26f0193a,0x19928,0x400,
  0xde000100,0x420198d,0x19840,0x0,0xde080140,0x460199c,0x19960,0x600000,0xde100190,0x4b0192f,0x19848,0x0,0xde1801d0,0x4f0193e,0x19968,0x60000000,
  0xd9201020,0x28201988,0x19880,0x40,0xd9281060,0x28601999,0x199a0,0x40,0xd93010b0,0x28b0192a,0x19888,0x4000,0xd93810f0,0x28f0193b,0x199a8,0x4004000,
  0xd9601100,0x2820198c,0x198c0,0x40,0xd9681140,0x2860199d,0x199e0,0x40,0xd9701190,0x28b0192e,0x198c8,0x4000,0xd97811d0,0x28f0193f,0x199e8,0x4004000,
  0xd8800030,0x2630198b,0x19810,0x0,0xd8880070,0x2670199a,0x19930,0x0,0xd89000a0,0x26a01929,0x19818,0x0,0xd89800e0,0x26e01938,0x19938,0x0,
  0xeba00110,0x430198f,0x19850,0x4000040,0xeba80150,0x470199e,0x19970,0x40,0xebb00180,0x4a0192d,0x19858,0x4000,0xebb801c0,0x4e0193c,0x19978,0x4000,
  0xd8c01030,0x2830198a,0x19890,0x6,0xd8c81070,0x2870199b,0x199b0,0x0,0xd8d010a0,0x28a01928,0x19898,0x600,0xd8d810e0,0x28e01939,0x199b8,0x0,
  0xc6001110,0x2830198e,0x198d0,0x6,0xc6081150,0x2870199f,0x199f0,0x0,0xc6101180,0x28a0192c,0x198d8,0x600,0xc61811c0,0x28e0193d,0x199f8,0x0,
  0x0,0x0,0x0,0x0,0x0,0x0,0x0,0x0,0x0,0x0,0x0,0x0,0x0,0x0,0x0,0x0,
  0x0,0x0,0x0,0x0,0x0,0x0,0x0,0x0,0x0,0x0,0x0,0x0,0x0,0x0,0x0,0x0,
  0x0,0x0,0x0,0x0,0x0,0x0,0x0,0x0,0x0,0x0,0x0,0x0,0x0,0x0,0x0,0x0,
  0x0,0x0,0x0,0x0,0x0,0x0,0x0,0x0,0x0,0x0,0x0,0x0,0x0,0x0,0x0,0x0,
  0x0,0x0,0x0,0x0,0x0,0x0,0x0,0x0,0x0,0x0,0x0,0x0,0x0,0x0,0x0,0x0,
  0x0,0x0,0x0,0x0,0x0,0x0,0x0,0x0,0x0,0x0,0x0,0x0,0x0,0x0,0x0,0x0,
  0x0,0x0,0x0,0x0,0x0,0x0,0x0,0x0,0x0,0x0,0x0,0x0,0x0,0x0,0x0,0x0,
  0x0,0x0,0x0,0x0,0x0,0x0,0x0,0x0,0x0,0x0,0x0,0x0,0x0,0x0,0x0,0x0,
  0x0,0x0,0x0,0x0,0x0,0x0,0x0,0x0,0x0,0x0,0x0,0x0,0x0,0x0,0x0,0x0,
  0x0,0x0,0x0,0x0,0x0,0x0,0x0,0x0,0x0,0x0,0x0,0x0,0x0,0x0,0x0,0x0,
  0x0,0x0,0x0,0x0,0x0,0x0,0x0,0x0,0x0,0x0,0x0,0x0,0x0,0x0,0x0,0x0,
  0x0,0x0,0x0,0x0,0x0,0x0,0x0,0x0,0x0,0x0,0x0,0x0,0x0,0x0,0x0,0x0,
  0x0,0x0,0x0,0x0,0x0,0x0,0x0,0x0,0x0,0x0,0x0,0x0,0x0,0x0,0x0,0x0,
  0x0,0x0,0x0,0x0,0x0,0x0,0x0,0x0,0x0,0x0,0x0,0x0,0x0,0x0,0x0,0x0,
  0x0,0x0,0x0,0x0,0x0,0x0,0x0,0x0,0x0,0x0,0x0,0x0,0x0,0x0,0x0,0x0,
  0x0,0x0,0x0,0x0,0x0,0x0,0x0,0x0,0x0,0x0,0x0,0x0,0x0,0x0,0x0,0x0,
  0x3f903f90,0x0,0x0,0x0,0x3f900000,0x0,0x3f90,0x0,0x0,0x0,0x3f903f90,0x0,0x0,0x0,0x3f900000,0x0,
  0x0,0xc042,0x0,0x0,0x0,0x0,0x0,0x0,0x0,0x0,0x0,0xc042,0x0,0x0,0x0,0x0,
  0x0,0x0,0x0,0x0,0x0,0x0,0x0,0x0,0x0,0x0,0x0,0x0,0x0,0x0,0x0,0x3f900000,
  0x0,0x0,0x0,0x0,0x0,0x0,0x0,0x0,0x0,0x0,0x0,0x0,0x0,0x0,0x0,0x3f900000,
  0x0,0x0,0x0,0x0,0x0,0x0,0x0,0x0,0x0,0x0,0x0,0x0,0x0,0x0,0x0,0x0,
  0x0,0x3f90,0x0,0x3f900000,0x0,0x3f90,0x0,0x0,0x0,0x0,0x0,0x3f90,0x0,0x0,0x0,0x3f90,
  0xc042,0x0,0x0,0x0,0xc0420000,0x0,0x0,0x0,0x0,0x0,0xc042,0x0,0x0,0x0,0xc0420000,0x0,
  0xc042,0x0,0x0,0x0,0xc0420000,0x0,0x0,0x0,0x0,0x0,0xc042,0x0,0x0,0x0,0xc0420000,0x0,
  0x0,0xc0420000,0x0,0x0,0x0,0x0,0x0,0x0,0x0,0x0,0x0,0xc0420000,0x0,0x0,0x0,0x0,
  0x0,0x0,0x0,0x0,0x0,0xc042,0x0,0x0,0x0,0x0,0x0,0x0,0x0,0x0,0x0,0xc042,
  0x3f903f90,0x0,0x0,0x0,0x3f900000,0x0,0x3f90,0x0,0x0,0x0,0x3f903f90,0x0,0x0,0x0,0x3f900000,0x0,
  0x3f903f90,0x0,0x0,0x0,0x3f900000,0x0,0x3f90,0x0,0x0,0x0,0x3f903f90,0x0,0x0,0x0,0x3f900000,0x0,
  0x0,0x0,0x0,0x0,0xc042,0x0,0x0,0x0,0x0,0x0,0x0,0x0,0x0,0x0,0x3f90c042,0x0,
  0x0,0x0,0x0,0x0,0x0,0x3f90,0x0,0x0,0x0,0x3f90,0x0,0x0,0x0,0x0,0x0,0x3f90,
  0x0,0x0,0x0,0x0,0x0,0x0,0x0,0x0,0x0,0x0,0x0,0x0,0x0,0x0,0x0,0x0,
  0x0,0x0,0x0,0x0,0x0,0x0,0x0,0x0,0x0,0x0,0x0,0x0,0x0,0x0,0x0,0x0,
  0xcc000000,0x27201a42,0x19e00,0x40004,0xcc080040,0x27601a53,0x19f20,0x40400,0xcc100090,0x27b019e0,0x19e08,0x4000400,0xcc1800d0,0x27f019f1,0x19f28,0x4000000,
  0xcd200120,0x5201a46,0x19e40,0x60,0xcd280160,0x5601a57,0x19f60,0x0,0xcd3001b0,0x5b019e4,0x19e48,0x6000,0xcd3801f0,0x5f019f5,0x19f68,0x0,
  0xe4401000,0x29201a43,0x19e80,0x0,0xe4481040,0x29601a52,0x19fa0,0x0,0xe4501090,0x29b019e1,0x19e88,0x0,0xe45810d0,0x29f019f0,0x19fa8,0x40000000,
  0xeb601120,0x29201a47,0x19ec0,0x0,0xeb681160,0x29601a56,0x19fe0,0x0,0xeb7011b0,0x29b019e5,0x19ec8,0x0,0xeb7811f0,0x29f019f4,0x19fe8,0x40000000,
  0xe4800010,0x27301a40,0x19e10,0x0,0xe4880050,0x27701a51,0x19f30,0x0,0xe4900080,0x27a019e2,0x19e18,0x0,0xe49800c0,0x27e019f3,0x19f38,0x0,
  0xe5a00130,0x5301a44,0x19e50,0x40000040,0xe5a80170,0x5701a55,0x19f70,0x40,0xe5b001a0,0x5a019e6,0x19e58,0x4000,0xe5b801e0,0x5e019f7,0x19f78,0x4000,
  0xe4c01010,0x29301a41,0x19e90,0x6,0xe4c81050,0x29701a50,0x19fb0,0x60000,0xe4d01080,0x29a019e3,0x19e98,0x600,0xe4d810c0,0x29e019f2,0x19fb8,0x6000000,
  0xd9e01130,0x29301a45,0x19ed0,0x6,0xd9e81170,0x29701a54,0x19ff0,0x60000,0xd9f011a0,0x29a019e7,0x19ed8,0x600,0xd9f811e0,0x29e019f6,0x19ff8,0x6000000,
  0xe4000020,0x27001a4b,0x1a400,0x600000,0xe4080060,0x27401a5a,0x1a520,0x0,0xe41000b0,0x279019e9,0x1a408,0x60000000,0xe41800f0,0x27d019f8,0x1a528,0x0,
  0xe5200100,0x5001a4f,0x1a440,0x0,0xe5280140,0x5401a5e,0x1a560,0x60,0xe5300190,0x59019ed,0x1a448,0x0,0xe53801d0,0x5d019fc,0x1a568,0x6000,
  0xe5601020,0x29001a4a,0x1a480,0x40004,0xe5681060,0x29401a5b,0x1a5a0,0x40400,0xe57010b0,0x299019e8,0x1a488,0x4000400,0xe57810f0,0x29d019f9,0x1a5a8,0x4000000,
  0xcc801100,0x29001a4e,0x1a4c0,0x40004,0xcc881140,0x29401a5f,0x1a5e0,0x40400,0xcc901190,0x299019ec,0x1a4c8,0x4000400,0xcc9811d0,0x29d019fd,0x1a5e8,0x4000000,
  0xcda00030,0x27101a49,0x1a410,0x0,0xcda80070,0x27501a58,0x1a530,0x6,0xcdb000a0,0x278019eb,0x1a418,0x0,0xcdb800e0,0x27c019fa,0x1a538,0x4000600,
  0xeac00110,0x5101a4d,0x1a450,0x0,0xeac80150,0x5501a5c,0x1a570,0x40,0xead00180,0x58019ef,0x1a458,0x40,0xead801c0,0x5c019fe,0x1a578,0x4000,
  0xe5e01030,0x29101a48,0x1a490,0x0,0xe5e81070,0x29501a59,0x1a5b0,0x0,0xe5f010a0,0x298019ea,0x1a498,0x0,0xe5f810e0,0x29c019fb,0x1a5b8,0x0,
  0xe4001110,0x29101a4c,0x1a4d0,0x0,0xe4081150,0x29501a5d,0x1a5f0,0x0,0xe4101180,0x298019ee,0x1a4d8,0x0,0xe41811c0,0x29c019ff,0x1a5f8,0x0,
  0x0,0x0,0x800,0x10000000,0x10,0x90,0x890,0x10000000,0x20,0x120,0x920,0x10000000,0x30,0x1b0,0x9b0,0x10000000,
  0x40,0x240,0xa40,0x20003180,0x50,0x2d0,0xad0,0x20003190,0x60,0x360,0xb60,0x200031a0,0x70,0x3f0,0xbf0,0x200031b0,
  0x610,0xc00,0x1400,0x40000000,0x600,0xc90,0x1490,0x40000010,0x630,0xd20,0x1520,0x40000020,0x620,0xdb0,0x15b0,0x40000030,
  0x650,0xe40,0x1640,0x20033180,0x640,0xed0,0x16d0,0x20033190,0x670,0xf60,0x1760,0x200331a0,0x660,0xff0,0x17f0,0x200331b0,
  0xc20,0x10,0x808,0x40030020,0xc30,0x80,0x898,0x40030030,0xc00,0x130,0x928,0x40030000,0xc10,0x1a0,0x9b8,0x40030010,
  0xc60,0x250,0xa48,0x200631a0,0xc70,0x2c0,0xad8,0x200631b0,0xc40,0x370,0xb68,0x20063180,0xc50,0x3e0,0xbf8,0x20063190,
  0x1230,0xc10,0x1408,0x40060020,0x1220,0xc80,0x1498,0x40060030,0x1210,0xd30,0x1528,0x40060000,0x1200,0xda0,0x15b8,0x40060010,
  0x1270,0xe50,0x1648,0x10000000,0x1260,0xec0,0x16d8,0x10000000,0x1250,0xf70,0x1768,0x10000000,0x1240,0xfe0,0x17f8,0x10000000,
  0x1840,0x20,0x810,0x10000000,0x1850,0xb0,0x880,0x10000000,0x1860,0x100,0x930,0x10000000,0x1870,0x190,0x9a0,0x10000000,
  0x1800,0x260,0xa50,0x200031c0,0x1810,0x2f0,0xac0,0x200031d0,0x1820,0x340,0xb70,0x200031e0,0x1830,0x3d0,0xbe0,0x200031f0,
  0x1e50,0xc20,0x1410,0x40000040,0x1e40,0xcb0,0x1480,0x40000050,0x1e70,0xd00,0x1530,0x40000060,0x1e60,0xd90,0x15a0,0x40000070,
  0x1e10,0xe60,0x1650,0x200331c0,0x1e00,0xef0,0x16c0,0x200331d0,0x1e30,0xf40,0x1770,0x200331e0,0x1e20,0xfd0,0x17e0,0x200331f0,
  0x2460,0x30,0x818,0x40030060,0x2470,0xa0,0x888,0x40030070,0x2440,0x110,0x938,0x40030040,0x2450,0x180,0x9a8,0x40030050,
  0x2420,0x270,0xa58,0x200631e0,0x2430,0x2e0,0xac8,0x200631f0,0x2400,0x350,0xb78,0x200631c0,0x2410,0x3c0,0xbe8,0x200631d0,
  0x2a70,0xc30,0x1418,0x40060060,0x2a60,0xca0,0x1488,0x40060070,0x2a50,0xd10,0x1538,0x40060040,0x2a40,0xd80,0x15a8,0x40060050,
  0x2a30,0xe70,0x1658,0x10000000,0x2a20,0xee0,0x16c8,0x10000000,0x2a10,0xf50,0x1778,0x10000000,0x2a00,0xfc0,0x17e8,0x10000000,
  0xbfd8bf90,0x3f403f00,0xa800,0xa800,0xbfd83f90,0x3f40bf00,0xa840,0xa840,0x3f003f40,0xbf90bfd8,0xa890,0xa890,0xbf003f40,0x3f90bfd8,0xa8d0,0xa8d0,
  0xbfd8bf90,0x3f403f00,0xb420,0xb420,0xbfd83f90,0x3f40bf00,0xb460,0xb460,0x3f003f40,0xbf90bfd8,0xb4b0,0xb4b0,0xbf003f40,0x3f90bfd8,0xb4f0,0xb4f0,
  0xbfd8bf90,0x3f403f00,0x17400,0x17a00,0xbfd83f90,0x3f40bf00,0x17440,0x17a40,0x3f003f40,0xbf90bfd8,0x17490,0x17a90,0xbf003f40,0x3f90bfd8,0x174d0,0x17ad0,
  0xbfd8bf90,0x3f403f00,0x18020,0x18620,0xbfd83f90,0x3f40bf00,0x18060,0x18660,0x3f003f40,0xbf90bfd8,0x180b0,0x186b0,0xbf003f40,0x3f90bfd8,0x180f0,0x186f0,
  0xbfd8bf90,0x3f403f00,0xa810,0xa810,0xbfd83f90,0x3f40bf00,0xa850,0xa850,0x3f003f40,0xbf90bfd8,0xa880,0xa880,0xbf003f40,0x3f90bfd8,0xa8c0,0xa8c0,
  0xbfd8bf90,0x3f403f00,0xb430,0xb430,0xbfd83f90,0x3f40bf00,0xb470,0xb470,0x3f003f40,0xbf90bfd8,0xb4a0,0xb4a0,0xbf003f40,0x3f90bfd8,0xb4e0,0xb4e0,
  0xbfd8bf90,0x3f403f00,0x17410,0x17a10,0xbfd83f90,0x3f40bf00,0x17450,0x17a50,0x3f003f40,0xbf90bfd8,0x17480,0x17a80,0xbf003f40,0x3f90bfd8,0x174c0,0x17ac0,
  0xbfd8bf90,0x3f403f00,0x18030,0x18630,0xbfd83f90,0x3f40bf00,0x18070,0x18670,0x3f003f40,0xbf90bfd8,0x180a0,0x186a0,0xbf003f40,0x3f90bfd8,0x180e0,0x186e0,
  0xbfd8bf90,0x3f403f00,0xa820,0xa820,0xbfd83f90,0x3f40bf00,0xa860,0xa860,0x3f003f40,0xbf90bfd8,0xa8b0,0xa8b0,0xbf003f40,0x3f90bfd8,0xa8f0,0xa8f0,
  0xbfd8bf90,0x3f403f00,0xb400,0xb400,0xbfd83f90,0x3f40bf00,0xb440,0xb440,0x3f003f40,0xbf90bfd8,0xb490,0xb490,0xbf003f40,0x3f90bfd8,0xb4d0,0xb4d0,
  0xbfd8bf90,0x3f403f00,0x17420,0x17a20,0xbfd83f90,0x3f40bf00,0x17460,0x17a60,0x3f003f40,0xbf90bfd8,0x174b0,0x17ab0,0xbf003f40,0x3f90bfd8,0x174f0,0x17af0,
  0xbfd8bf90,0x3f403f00,0x18000,0x18600,0xbfd83f90,0x3f40bf00,0x18040,0x18640,0x3f003f40,0xbf90bfd8,0x18090,0x18690,0xbf003f40,0x3f90bfd8,0x180d0,0x186d0,
  0xbfd8bf90,0x3f403f00,0xa830,0xa830,0xbfd83f90,0x3f40bf00,0xa870,0xa870,0x3f003f40,0xbf90bfd8,0xa8a0,0xa8a0,0xbf003f40,0x3f90bfd8,0xa8e0,0xa8e0,
  0xbfd8bf90,0x3f403f00,0xb410,0xb410,0xbfd83f90,0x3f40bf00,0xb450,0xb450,0x3f003f40,0xbf90bfd8,0xb480,0xb480,0xbf003f40,0x3f90bfd8,0xb4c0,0xb4c0,
  0xbfd8bf90,0x3f403f00,0x17430,0x17a30,0xbfd83f90,0x3f40bf00,0x17470,0x17a70,0x3f003f40,0xbf90bfd8,0x174a0,0x17aa0,0xbf003f40,0x3f90bfd8,0x174e0,0x17ae0,
  0xbfd8bf90,0x3f403f00,0x18010,0x18610,0xbfd83f90,0x3f40bf00,0x18050,0x18650,0x3f003f40,0xbf90bfd8,0x18080,0x18680,0xbf003f40,0x3f90bfd8,0x180c0,0x186c0,
  0xa800,0xa820,0x0,0xa800,0xa808,0xa828,0x40,0xa840,0xa810,0xa830,0x90,0xa890,0xa818,0xa838,0xd0,0xa8d0,
  0xa920,0xa900,0x2400,0xa900,0xa928,0xa908,0x2440,0xa940,0xa930,0xa910,0x2490,0xa990,0xa938,0xa918,0x24d0,0xa9d0,
  0xa840,0xa860,0x200,0xc000,0xa848,0xa868,0x240,0xc040,0xa850,0xa870,0x290,0xc090,0xa858,0xa878,0x2d0,0xc0d0,
  0xa960,0xa940,0x200,0xc000,0xa968,0xa948,0x240,0xc040,0xa970,0xa950,0x290,0xc090,0xa978,0xa958,0x2d0,0xc0d0,
  0xea80,0xeaa0,0x10,0xa810,0xea88,0xeaa8,0x50,0xa850,0xea90,0xeab0,0x80,0xa880,0xea98,0xeab8,0xc0,0xa8c0,
  0xa9a0,0xa980,0x2410,0xa910,0xa9a8,0xa988,0x2450,0xa950,0xa9b0,0xa990,0x2480,0xa980,0xa9b8,0xa998,0x24c0,0xa9c0,
  0xc0c0,0xc0e0,0x210,0xc010,0xc0c8,0xc0e8,0x250,0xc050,0xc0d0,0xc0f0,0x280,0xc080,0xc0d8,0xc0f8,0x2c0,0xc0c0,
  0xc1e0,0xc1c0,0x210,0xc010,0xc1e8,0xc1c8,0x250,0xc050,0xc1f0,0xc1d0,0x280,0xc080,0xc1f8,0xc1d8,0x2c0,0xc0c0,
  0xae00,0xae20,0x20,0xa820,0xae08,0xae28,0x60,0xa860,0xae10,0xae30,0xb0,0xa8b0,0xae18,0xae38,0xf0,0xa8f0,
  0xeb20,0xeb00,0x2420,0xa920,0xeb28,0xeb08,0x2460,0xa960,0xeb30,0xeb10,0x24b0,0xa9b0,0xeb38,0xeb18,0x24f0,0xa9f0,
  0xc040,0xc060,0x220,0xc020,0xc048,0xc068,0x260,0xc060,0xc050,0xc070,0x2b0,0xc0b0,0xc058,0xc078,0x2f0,0xc0f0,
  0xc160,0xc140,0x220,0xc020,0xc168,0xc148,0x260,0xc060,0xc170,0xc150,0x2b0,0xc0b0,0xc178,0xc158,0x2f0,0xc0f0,
  0xa880,0xa8a0,0x30,0xa830,0xa888,0xa8a8,0x70,0xa870,0xa890,0xa8b0,0xa0,0xa8a0,0xa898,0xa8b8,0xe0,0xa8e0,
  0xa8c0,0xa8e0,0x2430,0xa930,0xa8c8,0xa8e8,0x2470,0xa970,0xa8d0,0xa8f0,0x24a0,0xa9a0,0xa8d8,0xa8f8,0x24e0,0xa9e0,
  0xa9e0,0xa9c0,0x230,0xc030,0xa9e8,0xa9c8,0x270,0xc070,0xa9f0,0xa9d0,0x2a0,0xc0a0,0xa9f8,0xa9d8,0x2e0,0xc0e0,
  0xae00,0xae20,0x230,0xc030,0xae08,0xae28,0x270,0xc070,0xae10,0xae30,0x2a0,0xc0a0,0xae18,0xae38,0x2e0,0xc0e0,
  0x180000,0x40,0x820,0x10000000,0x180010,0xd0,0x8b0,0x10000000,0x180020,0x160,0x900,0x10000000,0x180030,0x1f0,0x990,0x10000000,
  0x180040,0x200,0xa60,0x20093180,0x180050,0x290,0xaf0,0x20093190,0x180060,0x320,0xb40,0x200931a0,0x180070,0x3b0,0xbd0,0x200931b0,
  0x180610,0xc40,0x1420,0x40090000,0x180600,0xcd0,0x14b0,0x40090010,0x180630,0xd60,0x1500,0x40090020,0x180620,0xdf0,0x1590,0x40090030,
  0x180650,0xe00,0x1660,0x200c3180,0x180640,0xe90,0x16f0,0x200c3190,0x180670,0xf20,0x1740,0x200c31a0,0x180660,0xfb0,0x17d0,0x200c31b0,
  0x180c20,0x50,0x828,0x400c0020,0x180c30,0xc0,0x8b8,0x400c0030,0x180c00,0x170,0x908,0x400c0000,0x180c10,0x1e0,0x998,0x400c0010,
  0x180c60,0x210,0xa68,0x200f31a0,0x180c70,0x280,0xaf8,0x200f31b0,0x180c40,0x330,0xb48,0x200f3180,0x180c50,0x3a0,0xbd8,0x200f3190,
  0x181230,0xc50,0x1428,0x400f0020,0x181220,0xcc0,0x14b8,0x400f0030,0x181210,0xd70,0x1508,0x400f0000,0x181200,0xde0,0x1598,0x400f0010,
  0x181270,0xe10,0x1668,0x10000000,0x181260,0xe80,0x16f8,0x10000000,0x181250,0xf30,0x1748,0x10000000,0x181240,0xfa0,0x17d8,0x10000000,
  0x181840,0x60,0x830,0x10000000,0x181850,0xf0,0x8a0,0x10000000,0x181860,0x140,0x910,0x10000000,0x181870,0x1d0,0x980,0x10000000,
  0x181800,0x220,0xa70,0x200931c0,0x181810,0x2b0,0xae0,0x200931d0,0x181820,0x300,0xb50,0x200931e0,0x181830,0x390,0xbc0,0x200931f0,
  0x181e50,0xc60,0x1430,0x40090040,0x181e40,0xcf0,0x14a0,0x40090050,0x181e70,0xd40,0x1510,0x40090060,0x181e60,0xdd0,0x1580,0x40090070,
  0x181e10,0xe20,0x1670,0x200c31c0,0x181e00,0xeb0,0x16e0,0x200c31d0,0x181e30,0xf00,0x1750,0x200c31e0,0x181e20,0xf90,0x17c0,0x200c31f0,
  0x182460,0x70,0x838,0x400c0060,0x182470,0xe0,0x8a8,0x400c0070,0x182440,0x150,0x918,0x400c0040,0x182450,0x1c0,0x988,0x400c0050,
  0x182420,0x230,0xa78,0x200f31e0,0x182430,0x2a0,0xae8,0x200f31f0,0x182400,0x310,0xb58,0x200f31c0,0x182410,0x380,0xbc8,0x200f31d0,
  0x182a70,0xc70,0x1438,0x400f0060,0x182a60,0xce0,0x14a8,0x400f0070,0x182a50,0xd50,0x1518,0x400f0040,0x182a40,0xdc0,0x1588,0x400f0050,
  0x182a30,0xe30,0x1678,0x10000000,0x182a20,0xea0,0x16e8,0x10000000,0x182a10,0xf10,0x1758,0x10000000,0x182a00,0xf80,0x17c8,0x10000000,
  0xbfd8bf90,0x3f403f00,0xa800,0xa800,0xbfd83f90,0x3f40bf00,0xa840,0xa840,0x3f003f40,0xbf90bfd8,0xa890,0xa890,0xbf003f40,0x3f90bfd8,0xa8d0,0xa8d0,
  0xbfd8bf90,0x3f403f00,0xb420,0xb420,0xbfd83f90,0x3f40bf00,0xb460,0xb460,0x3f003f40,0xbf90bfd8,0xb4b0,0xb4b0,0xbf003f40,0x3f90bfd8,0xb4f0,0xb4f0,
  0xbfd8bf90,0x3f403f00,0x17400,0x17a00,0xbfd83f90,0x3f40bf00,0x17440,0x17a40,0x3f003f40,0xbf90bfd8,0x17490,0x17a90,0xbf003f40,0x3f90bfd8,0x174d0,0x17ad0,
  0xbfd8bf90,0x3f403f00,0x18020,0x18620,0xbfd83f90,0x3f40bf00,0x18060,0x18660,0x3f003f40,0xbf90bfd8,0x180b0,0x186b0,0xbf003f40,0x3f90bfd8,0x180f0,0x186f0,
  0xbfd8bf90,0x3f403f00,0xa810,0xa810,0xbfd83f90,0x3f40bf00,0xa850,0xa850,0x3f003f40,0xbf90bfd8,0xa880,0xa880,0xbf003f40,0x3f90bfd8,0xa8c0,0xa8c0,
  0xbfd8bf90,0x3f403f00,0xb430,0xb430,0xbfd83f90,0x3f40bf00,0xb470,0xb470,0x3f003f40,0xbf90bfd8,0xb4a0,0xb4a0,0xbf003f40,0x3f90bfd8,0xb4e0,0xb4e0,
  0xbfd8bf90,0x3f403f00,0x17410,0x17a10,0xbfd83f90,0x3f40bf00,0x17450,0x17a50,0x3f003f40,0xbf90bfd8,0x17480,0x17a80,0xbf003f40,0x3f90bfd8,0x174c0,0x17ac0,
  0xbfd8bf90,0x3f403f00,0x18030,0x18630,0xbfd83f90,0x3f40bf00,0x18070,0x18670,0x3f003f40,0xbf90bfd8,0x180a0,0x186a0,0xbf003f40,0x3f90bfd8,0x180e0,0x186e0,
  0xbfd8bf90,0x3f403f00,0xa820,0xa820,0xbfd83f90,0x3f40bf00,0xa860,0xa860,0x3f003f40,0xbf90bfd8,0xa8b0,0xa8b0,0xbf003f40,0x3f90bfd8,0xa8f0,0xa8f0,
  0xbfd8bf90,0x3f403f00,0xb400,0xb400,0xbfd83f90,0x3f40bf00,0xb440,0xb440,0x3f003f40,0xbf90bfd8,0xb490,0xb490,0xbf003f40,0x3f90bfd8,0xb4d0,0xb4d0,
  0xbfd8bf90,0x3f403f00,0x17420,0x17a20,0xbfd83f90,0x3f40bf00,0x17460,0x17a60,0x3f003f40,0xbf90bfd8,0x174b0,0x17ab0,0xbf003f40,0x3f90bfd8,0x174f0,0x17af0,
  0xbfd8bf90,0x3f403f00,0x18000,0x18600,0xbfd83f90,0x3f40bf00,0x18040,0x18640,0x3f003f40,0xbf90bfd8,0x18090,0x18690,0xbf003f40,0x3f90bfd8,0x180d0,0x186d0,
  0xbfd8bf90,0x3f403f00,0xa830,0xa830,0xbfd83f90,0x3f40bf00,0xa870,0xa870,0x3f003f40,0xbf90bfd8,0xa8a0,0xa8a0,0xbf003f40,0x3f90bfd8,0xa8e0,0xa8e0,
  0xbfd8bf90,0x3f403f00,0xb410,0xb410,0xbfd83f90,0x3f40bf00,0xb450,0xb450,0x3f003f40,0xbf90bfd8,0xb480,0xb480,0xbf003f40,0x3f90bfd8,0xb4c0,0xb4c0,
  0xbfd8bf90,0x3f403f00,0x17430,0x17a30,0xbfd83f90,0x3f40bf00,0x17470,0x17a70,0x3f003f40,0xbf90bfd8,0x174a0,0x17aa0,0xbf003f40,0x3f90bfd8,0x174e0,0x17ae0,
  0xbfd8bf90,0x3f403f00,0x18010,0x18610,0xbfd83f90,0x3f40bf00,0x18050,0x18650,0x3f003f40,0xbf90bfd8,0x18080,0x18680,0xbf003f40,0x3f90bfd8,0x180c0,0x186c0,
  0xb400,0xb420,0x120,0xb420,0xb408,0xb428,0x160,0xb460,0xb410,0xb430,0x1b0,0xb4b0,0xb418,0xb438,0x1f0,0xb4f0,
  0xb440,0xb460,0x2520,0xb520,0xb448,0xb468,0x2560,0xb560,0xb450,0xb470,0x25b0,0xb5b0,0xb458,0xb478,0x25f0,0xb5f0,
  0xcc40,0xcc60,0x320,0xcc20,0xcc48,0xcc68,0x360,0xcc60,0xcc50,0xcc70,0x3b0,0xccb0,0xcc58,0xcc78,0x3f0,0xccf0,
  0xcd60,0xcd40,0x320,0xcc20,0xcd68,0xcd48,0x360,0xcc60,0xcd70,0xcd50,0x3b0,0xccb0,0xcd78,0xcd58,0x3f0,0xccf0,
  0xb5a0,0xb580,0x130,0xb430,0xb5a8,0xb588,0x170,0xb470,0xb5b0,0xb590,0x1a0,0xb4a0,0xb5b8,0xb598,0x1e0,0xb4e0,
  0xccc0,0xcce0,0x2530,0xb530,0xccc8,0xcce8,0x2570,0xb570,0xccd0,0xccf0,0x25a0,0xb5a0,0xccd8,0xccf8,0x25e0,0xb5e0,
  0xa9e0,0xa9c0,0x330,0xcc30,0xa9e8,0xa9c8,0x370,0xcc70,0xa9f0,0xa9d0,0x3a0,0xcca0,0xa9f8,0xa9d8,0x3e0,0xcce0,
  0xc1e0,0xc1c0,0x330,0xcc30,0xc1e8,0xc1c8,0x370,0xcc70,0xc1f0,0xc1d0,0x3a0,0xcca0,0xc1f8,0xc1d8,0x3e0,0xcce0,
  0xb520,0xb500,0x100,0xb400,0xb528,0xb508,0x140,0xb440,0xb530,0xb510,0x190,0xb490,0xb538,0xb518,0x1d0,0xb4d0,
  0xea40,0xea60,0x2500,0xb500,0xea48,0xea68,0x2540,0xb540,0xea50,0xea70,0x2590,0xb590,0xea58,0xea78,0x25d0,0xb5d0,
  0xb560,0xb540,0x300,0xcc00,0xb568,0xb548,0x340,0xcc40,0xb570,0xb550,0x390,0xcc90,0xb578,0xb558,0x3d0,0xccd0,
  0xb480,0xb4a0,0x300,0xcc00,0xb488,0xb4a8,0x340,0xcc40,0xb490,0xb4b0,0x390,0xcc90,0xb498,0xb4b8,0x3d0,0xccd0,
  0xb4c0,0xb4e0,0x110,0xb410,0xb4c8,0xb4e8,0x150,0xb450,0xb4d0,0xb4f0,0x180,0xb480,0xb4d8,0xb4f8,0x1c0,0xb4c0,
  0xb5e0,0xb5c0,0x2510,0xb510,0xb5e8,0xb5c8,0x2550,0xb550,0xb5f0,0xb5d0,0x2580,0xb580,0xb5f8,0xb5d8,0x25c0,0xb5c0,
  0xcde0,0xcdc0,0x310,0xcc10,0xcde8,0xcdc8,0x350,0xcc50,0xcdf0,0xcdd0,0x380,0xcc80,0xcdf8,0xcdd8,0x3c0,0xccc0,
  0xb520,0xb500,0x310,0xcc10,0xb528,0xb508,0x350,0xcc50,0xb530,0xb510,0x380,0xcc80,0xb538,0xb518,0x3c0,0xccc0,
  0x0,0x400,0x840,0x10000000,0x10,0x490,0x8d0,0x10000000,0x20,0x520,0x960,0x10000000,0x30,0x5b0,0x9f0,0x10000000,
  0x40,0x640,0xa00,0x20123180,0x50,0x6d0,0xa90,0x20123190,0x60,0x760,0xb20,0x201231a0,0x70,0x7f0,0xbb0,0x201231b0,
  0x610,0x1000,0x1440,0x40120000,0x600,0x1090,0x14d0,0x40120010,0x630,0x1120,0x1560,0x40120020,0x620,0x11b0,0x15f0,0x40120030,
  0x650,0x1240,0x1600,0x20153180,0x640,0x12d0,0x1690,0x20153190,0x670,0x1360,0x1720,0x201531a0,0x660,0x13f0,0x17b0,0x201531b0,
  0xc20,0x410,0x848,0x40150020,0xc30,0x480,0x8d8,0x40150030,0xc00,0x530,0x968,0x40150000,0xc10,0x5a0,0x9f8,0x40150010,
  0xc60,0x650,0xa08,0x201831a0,0xc70,0x6c0,0xa98,0x201831b0,0xc40,0x770,0xb28,0x20183180,0xc50,0x7e0,0xbb8,0x20183190,
  0x1230,0x1010,0x1448,0x40180020,0x1220,0x1080,0x14d8,0x40180030,0x1210,0x1130,0x1568,0x40180000,0x1200,0x11a0,0x15f8,0x40180010,
  0x1270,0x1250,0x1608,0x10000000,0x1260,0x12c0,0x1698,0x10000000,0x1250,0x1370,0x1728,0x10000000,0x1240,0x13e0,0x17b8,0x10000000,
  0x1840,0x420,0x850,0x10000000,0x1850,0x4b0,0x8c0,0x10000000,0x1860,0x500,0x970,0x10000000,0x1870,0x590,0x9e0,0x10000000,
  0x1800,0x660,0xa10,0x201231c0,0x1810,0x6f0,0xa80,0x201231d0,0x1820,0x740,0xb30,0x201231e0,0x1830,0x7d0,0xba0,0x201231f0,
  0x1e50,0x1020,0x1450,0x40120040,0x1e40,0x10b0,0x14c0,0x40120050,0x1e70,0x1100,0x1570,0x40120060,0x1e60,0x1190,0x15e0,0x40120070,
  0x1e10,0x1260,0x1610,0x201531c0,0x1e00,0x12f0,0x1680,0x201531d0,0x1e30,0x1340,0x1730,0x201531e0,0x1e20,0x13d0,0x17a0,0x201531f0,
  0x2460,0x430,0x858,0x40150060,0x2470,0x4a0,0x8c8,0x40150070,0x2440,0x510,0x978,0x40150040,0x2450,0x580,0x9e8,0x40150050,
  0x2420,0x670,0xa18,0x201831e0,0x2430,0x6e0,0xa88,0x201831f0,0x2400,0x750,0xb38,0x201831c0,0x2410,0x7c0,0xba8,0x201831d0,
  0x2a70,0x1030,0x1458,0x40180060,0x2a60,0x10a0,0x14c8,0x40180070,0x2a50,0x1110,0x1578,0x40180040,0x2a40,0x1180,0x15e8,0x40180050,
  0x2a30,0x1270,0x1618,0x10000000,0x2a20,0x12e0,0x1688,0x10000000,0x2a10,0x1350,0x1738,0x10000000,0x2a00,0x13c0,0x17a8,0x10000000,
  0xbfd8bf90,0x3f403f00,0xa800,0xa800,0xbfd83f90,0x3f40bf00,0xa840,0xa840,0x3f003f40,0xbf90bfd8,0xa890,0xa890,0xbf003f40,0x3f90bfd8,0xa8d0,0xa8d0,
  0xbfd8bf90,0x3f403f00,0xb420,0xb420,0xbfd83f90,0x3f40bf00,0xb460,0xb460,0x3f003f40,0xbf90bfd8,0xb4b0,0xb4b0,0xbf003f40,0x3f90bfd8,0xb4f0,0xb4f0,
  0xbfd8bf90,0x3f403f00,0x17400,0x17a00,0xbfd83f90,0x3f40bf00,0x17440,0x17a40,0x3f003f40,0xbf90bfd8,0x17490,0x17a90,0xbf003f40,0x3f90bfd8,0x174d0,0x17ad0,
  0xbfd8bf90,0x3f403f00,0x18020,0x18620,0xbfd83f90,0x3f40bf00,0x18060,0x18660,0x3f003f40,0xbf90bfd8,0x180b0,0x186b0,0xbf003f40,0x3f90bfd8,0x180f0,0x186f0,
  0xbfd8bf90,0x3f403f00,0xa810,0xa810,0xbfd83f90,0x3f40bf00,0xa850,0xa850,0x3f003f40,0xbf90bfd8,0xa880,0xa880,0xbf003f40,0x3f90bfd8,0xa8c0,0xa8c0,
  0xbfd8bf90,0x3f403f00,0xb430,0xb430,0xbfd83f90,0x3f40bf00,0xb470,0xb470,0x3f003f40,0xbf90bfd8,0xb4a0,0xb4a0,0xbf003f40,0x3f90bfd8,0xb4e0,0xb4e0,
  0xbfd8bf90,0x3f403f00,0x17410,0x17a10,0xbfd83f90,0x3f40bf00,0x17450,0x17a50,0x3f003f40,0xbf90bfd8,0x17480,0x17a80,0xbf003f40,0x3f90bfd8,0x174c0,0x17ac0,
  0xbfd8bf90,0x3f403f00,0x18030,0x18630,0xbfd83f90,0x3f40bf00,0x18070,0x18670,0x3f003f40,0xbf90bfd8,0x180a0,0x186a0,0xbf003f40,0x3f90bfd8,0x180e0,0x186e0,
  0xbfd8bf90,0x3f403f00,0xa820,0xa820,0xbfd83f90,0x3f40bf00,0xa860,0xa860,0x3f003f40,0xbf90bfd8,0xa8b0,0xa8b0,0xbf003f40,0x3f90bfd8,0xa8f0,0xa8f0,
  0xbfd8bf90,0x3f403f00,0xb400,0xb400,0xbfd83f90,0x3f40bf00,0xb440,0xb440,0x3f003f40,0xbf90bfd8,0xb490,0xb490,0xbf003f40,0x3f90bfd8,0xb4d0,0xb4d0,
  0xbfd8bf90,0x3f403f00,0x17420,0x17a20,0xbfd83f90,0x3f40bf00,0x17460,0x17a60,0x3f003f40,0xbf90bfd8,0x174b0,0x17ab0,0xbf003f40,0x3f90bfd8,0x174f0,0x17af0,
  0xbfd8bf90,0x3f403f00,0x18000,0x18600,0xbfd83f90,0x3f40bf00,0x18040,0x18640,0x3f003f40,0xbf90bfd8,0x18090,0x18690,0xbf003f40,0x3f90bfd8,0x180d0,0x186d0,
  0xbfd8bf90,0x3f403f00,0xa830,0xa830,0xbfd83f90,0x3f40bf00,0xa870,0xa870,0x3f003f40,0xbf90bfd8,0xa8a0,0xa8a0,0xbf003f40,0x3f90bfd8,0xa8e0,0xa8e0,
  0xbfd8bf90,0x3f403f00,0xb410,0xb410,0xbfd83f90,0x3f40bf00,0xb450,0xb450,0x3f003f40,0xbf90bfd8,0xb480,0xb480,0xbf003f40,0x3f90bfd8,0xb4c0,0xb4c0,
  0xbfd8bf90,0x3f403f00,0x17430,0x17a30,0xbfd83f90,0x3f40bf00,0x17470,0x17a70,0x3f003f40,0xbf90bfd8,0x174a0,0x17aa0,0xbf003f40,0x3f90bfd8,0x174e0,0x17ae0,
  0xbfd8bf90,0x3f403f00,0x18010,0x18610,0xbfd83f90,0x3f40bf00,0x18050,0x18650,0x3f003f40,0xbf90bfd8,0x18080,0x18680,0xbf003f40,0x3f90bfd8,0x180c0,0x186c0,
  0xc000,0xc020,0x2600,0xc100,0xc008,0xc028,0x2640,0xc140,0xc010,0xc030,0x2690,0xc190,0xc018,0xc038,0x26d0,0xc1d0,
  0xd800,0xd820,0x400,0xd800,0xd808,0xd828,0x440,0xd840,0xd810,0xd830,0x490,0xd890,0xd818,0xd838,0x4d0,0xd8d0,
  0xc120,0xc100,0x2800,0xd900,0xc128,0xc108,0x2840,0xd940,0xc130,0xc110,0x2890,0xd990,0xc138,0xc118,0x28d0,0xd9d0,
  0xd840,0xd860,0x2800,0xd900,0xd848,0xd868,0x2840,0xd940,0xd850,0xd870,0x2890,0xd990,0xd858,0xd878,0x28d0,0xd9d0,
  0xc080,0xc0a0,0x2610,0xc110,0xc088,0xc0a8,0x2650,0xc150,0xc090,0xc0b0,0x2680,0xc180,0xc098,0xc0b8,0x26c0,0xc1c0,
  0xc1a0,0xc180,0x410,0xd810,0xc1a8,0xc188,0x450,0xd850,0xc1b0,0xc190,0x480,0xd880,0xc1b8,0xc198,0x4c0,0xd8c0,
  0xd9a0,0xd980,0x2810,0xd910,0xd9a8,0xd988,0x2850,0xd950,0xd9b0,0xd990,0x2880,0xd980,0xd9b8,0xd998,0x28c0,0xd9c0,
  0xd9e0,0xd9c0,0x2810,0xd910,0xd9e8,0xd9c8,0x2850,0xd950,0xd9f0,0xd9d0,0x2880,0xd980,0xd9f8,0xd9d8,0x28c0,0xd9c0,
  0xc600,0xc620,0x2620,0xc120,0xc608,0xc628,0x2660,0xc160,0xc610,0xc630,0x26b0,0xc1b0,0xc618,0xc638,0x26f0,0xc1f0,
  0xde00,0xde20,0x420,0xd820,0xde08,0xde28,0x460,0xd860,0xde10,0xde30,0x4b0,0xd8b0,0xde18,0xde38,0x4f0,0xd8f0,
  0xd920,0xd900,0x2820,0xd920,0xd928,0xd908,0x2860,0xd960,0xd930,0xd910,0x28b0,0xd9b0,0xd938,0xd918,0x28f0,0xd9f0,
  0xd960,0xd940,0x2820,0xd920,0xd968,0xd948,0x2860,0xd960,0xd970,0xd950,0x28b0,0xd9b0,0xd978,0xd958,0x28f0,0xd9f0,
  0xd880,0xd8a0,0x2630,0xc130,0xd888,0xd8a8,0x2670,0xc170,0xd890,0xd8b0,0x26a0,0xc1a0,0xd898,0xd8b8,0x26e0,0xc1e0,
  0xeba0,0xeb80,0x430,0xd830,0xeba8,0xeb88,0x470,0xd870,0xebb0,0xeb90,0x4a0,0xd8a0,0xebb8,0xeb98,0x4e0,0xd8e0,
  0xd8c0,0xd8e0,0x2830,0xd930,0xd8c8,0xd8e8,0x2870,0xd970,0xd8d0,0xd8f0,0x28a0,0xd9a0,0xd8d8,0xd8f8,0x28e0,0xd9e0,
  0xc600,0xc620,0x2830,0xd930,0xc608,0xc628,0x2870,0xd970,0xc610,0xc630,0x28a0,0xd9a0,0xc618,0xc638,0x28e0,0xd9e0,
  0x180000,0x440,0x860,0x10000000,0x180010,0x4d0,0x8f0,0x10000000,0x180020,0x560,0x940,0x10000000,0x180030,0x5f0,0x9d0,0x10000000,
  0x180040,0x600,0xa20,0x201b3180,0x180050,0x690,0xab0,0x201b3190,0x180060,0x720,0xb00,0x201b31a0,0x180070,0x7b0,0xb90,0x201b31b0,
  0x180610,0x1040,0x1460,0x401b0000,0x180600,0x10d0,0x14f0,0x401b0010,0x180630,0x1160,0x1540,0x401b0020,0x180620,0x11f0,0x15d0,0x401b0030,
  0x180650,0x1200,0x1620,0x201e3180,0x180640,0x1290,0x16b0,0x201e3190,0x180670,0x1320,0x1700,0x201e31a0,0x180660,0x13b0,0x1790,0x201e31b0,
  0x180c20,0x450,0x868,0x401e0020,0x180c30,0x4c0,0x8f8,0x401e0030,0x180c00,0x570,0x948,0x401e0000,0x180c10,0x5e0,0x9d8,0x401e0010,
  0x180c60,0x610,0xa28,0x202131a0,0x180c70,0x680,0xab8,0x202131b0,0x180c40,0x730,0xb08,0x20213180,0x180c50,0x7a0,0xb98,0x20213190,
  0x181230,0x1050,0x1468,0x40210020,0x181220,0x10c0,0x14f8,0x40210030,0x181210,0x1170,0x1548,0x40210000,0x181200,0x11e0,0x15d8,0x40210010,
  0x181270,0x1210,0x1628,0x10000000,0x181260,0x1280,0x16b8,0x10000000,0x181250,0x1330,0x1708,0x10000000,0x181240,0x13a0,0x1798,0x10000000,
  0x181840,0x460,0x870,0x10000000,0x181850,0x4f0,0x8e0,0x10000000,0x181860,0x540,0x950,0x10000000,0x181870,0x5d0,0x9c0,0x10000000,
  0x181800,0x620,0xa30,0x201b31c0,0x181810,0x6b0,0xaa0,0x201b31d0,0x181820,0x700,0xb10,0x201b31e0,0x181830,0x790,0xb80,0x201b31f0,
  0x181e50,0x1060,0x1470,0x401b0040,0x181e40,0x10f0,0x14e0,0x401b0050,0x181e70,0x1140,0x1550,0x401b0060,0x181e60,0x11d0,0x15c0,0x401b0070,
  0x181e10,0x1220,0x1630,0x201e31c0,0x181e00,0x12b0,0x16a0,0x201e31d0,0x181e30,0x1300,0x1710,0x201e31e0,0x181e20,0x1390,0x1780,0x201e31f0,
  0x182460,0x470,0x878,0x401e0060,0x182470,0x4e0,0x8e8,0x401e0070,0x182440,0x550,0x958,0x401e0040,0x182450,0x5c0,0x9c8,0x401e0050,
  0x182420,0x630,0xa38,0x202131e0,0x182430,0x6a0,0xaa8,0x202131f0,0x182400,0x710,0xb18,0x202131c0,0x182410,0x780,0xb88,0x202131d0,
  0x182a70,0x1070,0x1478,0x40210060,0x182a60,0x10e0,0x14e8,0x40210070,0x182a50,0x1150,0x1558,0x40210040,0x182a40,0x11c0,0x15c8,0x40210050,
  0x182a30,0x1230,0x1638,0x10000000,0x182a20,0x12a0,0x16a8,0x10000000,0x182a10,0x1310,0x1718,0x10000000,0x182a00,0x1380,0x1788,0x10000000,
  0xbfd8bf90,0x3f403f00,0xa800,0xa800,0xbfd83f90,0x3f40bf00,0xa840,0xa840,0x3f003f40,0xbf90bfd8,0xa890,0xa890,0xbf003f40,0x3f90bfd8,0xa8d0,0xa8d0,
  0xbfd8bf90,0x3f403f00,0xb420,0xb420,0xbfd83f90,0x3f40bf00,0xb460,0xb460,0x3f003f40,0xbf90bfd8,0xb4b0,0xb4b0,0xbf003f40,0x3f90bfd8,0xb4f0,0xb4f0,
  0xbfd8bf90,0x3f403f00,0x17400,0x17a00,0xbfd83f90,0x3f40bf00,0x17440,0x17a40,0x3f003f40,0xbf90bfd8,0x17490,0x17a90,0xbf003f40,0x3f90bfd8,0x174d0,0x17ad0,
  0xbfd8bf90,0x3f403f00,0x18020,0x18620,0xbfd83f90,0x3f40bf00,0x18060,0x18660,0x3f003f40,0xbf90bfd8,0x180b0,0x186b0,0xbf003f40,0x3f90bfd8,0x180f0,0x186f0,
  0xbfd8bf90,0x3f403f00,0xa810,0xa810,0xbfd83f90,0x3f40bf00,0xa850,0xa850,0x3f003f40,0xbf90bfd8,0xa880,0xa880,0xbf003f40,0x3f90bfd8,0xa8c0,0xa8c0,
  0xbfd8bf90,0x3f403f00,0xb430,0xb430,0xbfd83f90,0x3f40bf00,0xb470,0xb470,0x3f003f40,0xbf90bfd8,0xb4a0,0xb4a0,0xbf003f40,0x3f90bfd8,0xb4e0,0xb4e0,
  0xbfd8bf90,0x3f403f00,0x17410,0x17a10,0xbfd83f90,0x3f40bf00,0x17450,0x17a50,0x3f003f40,0xbf90bfd8,0x17480,0x17a80,0xbf003f40,0x3f90bfd8,0x174c0,0x17ac0,
  0xbfd8bf90,0x3f403f00,0x18030,0x18630,0xbfd83f90,0x3f40bf00,0x18070,0x18670,0x3f003f40,0xbf90bfd8,0x180a0,0x186a0,0xbf003f40,0x3f90bfd8,0x180e0,0x186e0,
  0xbfd8bf90,0x3f403f00,0xa820,0xa820,0xbfd83f90,0x3f40bf00,0xa860,0xa860,0x3f003f40,0xbf90bfd8,0xa8b0,0xa8b0,0xbf003f40,0x3f90bfd8,0xa8f0,0xa8f0,
  0xbfd8bf90,0x3f403f00,0xb400,0xb400,0xbfd83f90,0x3f40bf00,0xb440,0xb440,0x3f003f40,0xbf90bfd8,0xb490,0xb490,0xbf003f40,0x3f90bfd8,0xb4d0,0xb4d0,
  0xbfd8bf90,0x3f403f00,0x17420,0x17a20,0xbfd83f90,0x3f40bf00,0x17460,0x17a60,0x3f003f40,0xbf90bfd8,0x174b0,0x17ab0,0xbf003f40,0x3f90bfd8,0x174f0,0x17af0,
  0xbfd8bf90,0x3f403f00,0x18000,0x18600,0xbfd83f90,0x3f40bf00,0x18040,0x18640,0x3f003f40,0xbf90bfd8,0x18090,0x18690,0xbf003f40,0x3f90bfd8,0x180d0,0x186d0,
  0xbfd8bf90,0x3f403f00,0xa830,0xa830,0xbfd83f90,0x3f40bf00,0xa870,0xa870,0x3f003f40,0xbf90bfd8,0xa8a0,0xa8a0,0xbf003f40,0x3f90bfd8,0xa8e0,0xa8e0,
  0xbfd8bf90,0x3f403f00,0xb410,0xb410,0xbfd83f90,0x3f40bf00,0xb450,0xb450,0x3f003f40,0xbf90bfd8,0xb480,0xb480,0xbf003f40,0x3f90bfd8,0xb4c0,0xb4c0,
  0xbfd8bf90,0x3f403f00,0x17430,0x17a30,0xbfd83f90,0x3f40bf00,0x17470,0x17a70,0x3f003f40,0xbf90bfd8,0x174a0,0x17aa0,0xbf003f40,0x3f90bfd8,0x174e0,0x17ae0,
  0xbfd8bf90,0x3f403f00,0x18010,0x18610,0xbfd83f90,0x3f40bf00,0x18050,0x18650,0x3f003f40,0xbf90bfd8,0x18080,0x18680,0xbf003f40,0x3f90bfd8,0x180c0,0x186c0,
  0xcc00,0xcc20,0x2720,0xcd20,0xcc08,0xcc28,0x2760,0xcd60,0xcc10,0xcc30,0x27b0,0xcdb0,0xcc18,0xcc38,0x27f0,0xcdf0,
  0xcd20,0xcd00,0x520,0xe420,0xcd28,0xcd08,0x560,0xe460,0xcd30,0xcd10,0x5b0,0xe4b0,0xcd38,0xcd18,0x5f0,0xe4f0,
  0xe440,0xe460,0x2920,0xe520,0xe448,0xe468,0x2960,0xe560,0xe450,0xe470,0x29b0,0xe5b0,0xe458,0xe478,0x29f0,0xe5f0,
  0xeb60,0xeb40,0x2920,0xe520,0xeb68,0xeb48,0x2960,0xe560,0xeb70,0xeb50,0x29b0,0xe5b0,0xeb78,0xeb58,0x29f0,0xe5f0,
  0xe480,0xe4a0,0x2730,0xcd30,0xe488,0xe4a8,0x2770,0xcd70,0xe490,0xe4b0,0x27a0,0xcda0,0xe498,0xe4b8,0x27e0,0xcde0,
  0xe5a0,0xe580,0x530,0xe430,0xe5a8,0xe588,0x570,0xe470,0xe5b0,0xe590,0x5a0,0xe4a0,0xe5b8,0xe598,0x5e0,0xe4e0,
  0xe4c0,0xe4e0,0x2930,0xe530,0xe4c8,0xe4e8,0x2970,0xe570,0xe4d0,0xe4f0,0x29a0,0xe5a0,0xe4d8,0xe4f8,0x29e0,0xe5e0,
  0xd9e0,0xd9c0,0x2930,0xe530,0xd9e8,0xd9c8,0x2970,0xe570,0xd9f0,0xd9d0,0x29a0,0xe5a0,0xd9f8,0xd9d8,0x29e0,0xe5e0,
  0xe400,0xe420,0x2700,0xcd00,0xe408,0xe428,0x2740,0xcd40,0xe410,0xe430,0x2790,0xcd90,0xe418,0xe438,0x27d0,0xcdd0,
  0xe520,0xe500,0x500,0xe400,0xe528,0xe508,0x540,0xe440,0xe530,0xe510,0x590,0xe490,0xe538,0xe518,0x5d0,0xe4d0,
  0xe560,0xe540,0x2900,0xe500,0xe568,0xe548,0x2940,0xe540,0xe570,0xe550,0x2990,0xe590,0xe578,0xe558,0x29d0,0xe5d0,
  0xcc80,0xcca0,0x2900,0xe500,0xcc88,0xcca8,0x2940,0xe540,0xcc90,0xccb0,0x2990,0xe590,0xcc98,0xccb8,0x29d0,0xe5d0,
  0xcda0,0xcd80,0x2710,0xcd10,0xcda8,0xcd88,0x2750,0xcd50,0xcdb0,0xcd90,0x2780,0xcd80,0xcdb8,0xcd98,0x27c0,0xcdc0,
  0xeac0,0xeae0,0x510,0xe410,0xeac8,0xeae8,0x550,0xe450,0xead0,0xeaf0,0x580,0xe480,0xead8,0xeaf8,0x5c0,0xe4c0,
  0xe5e0,0xe5c0,0x2910,0xe510,0xe5e8,0xe5c8,0x2950,0xe550,0xe5f0,0xe5d0,0x2980,0xe580,0xe5f8,0xe5d8,0x29c0,0xe5c0,
  0xe400,0xe420,0x2910,0xe510,0xe408,0xe428,0x2950,0xe550,0xe410,0xe430,0x2980,0xe580,0xe418,0xe438,0x29c0,0xe5c0,
};

__global__ __launch_bounds__(256, 1) void wino_main_kernel(const char* __restrict__ x, char* __restrict__ Uq, char* __restrict__ out, const char* __restrict__ wt,
                                                           unsigned long long region_mask) {
  __shared__ __attribute__((aligned(1024))) char smem[163840];
  unsigned tid = threadIdx.x;
  asm volatile(
"s_load_dwordx2 s[12:13], %1, 0\n\t"
"s_load_dwordx2 s[52:53], %1, 24\n\t"
"s_load_dwordx2 s[8:9], %1, 8\n\t"
"s_load_dwordx2 s[16:17], %1, 16\n\t"
"s_load_dword s76, %1, 32\n\t"
"s_getpc_b64 s[40:41]\n\t"
"Lwino_pc:\n\t"
"s_bfe_u32 s42, %2, 0x50003\n\t"
"s_lshl_b32 s42, s42, 12\n\t"
"v_lshl_add_u32 v203, v205, 4, s42\n\t"
"v_min_u32 v203, (Lwino_end-Lwino_pc-16)&-16, v203\n\t"
"global_load_dwordx4 v[100:103], v203, s[40:41]\n\t"
"v_readfirstlane_b32 s73, v205\n\t"
"s_lshl2_add_u32 s69, s73, %2\n\t"
"s_and_b32 s60, %2, 7\n\t"
"s_lshr_b32 s61, %2, 7\n\t"
"s_lshl2_add_u32 s60, s60, s61\n\t"
"s_bfe_u32 s61, %2, 0x40003\n\t"
"s_lshl_b32 s60, s60, 8\n\t"
"s_add_u32 s60, s60, s61\n\t"
"s_sub_u32 s60, s60, 16\n\t"
"s_mul_i32 s21, s60, 12288\n\t"
"s_lshr_b32 s73, s73, 6\n\t"
"s_add_u32 s24, s73, 28\n\t"
"s_mulk_i32 s24, 1536\n\t"
"v_bfe_u32 v1, v205, 2, 3\n\t"
"v_lshrrev_b32 v2, 3, v205\n\t"
"v_and_or_b32 v1, v2, 24, v1\n\t"
"v_and_b32 v3, 6, v2\n\t"
"v_and_b32 v4, 3, v205\n\t"
"v_xor_b32 v4, v3, v4\n\t"
"v_mul_u32_u24 v1, 384, v1\n\t"
"v_lshl_add_u32 v28, v4, 4, v1\n\t"
"s_mov_b32 s14, 100663296\n\t"
"s_mov_b32 s15, 0x20000\n\t"
"s_mov_b32 s54, 663552\n\t"
"s_mov_b32 s55, s15\n\t"
"s_mov_b32 s10, 0x200000\n\t"
"s_mov_b32 s11, s15\n\t"
"s_lshl_b32 s60, s73, 11\n\t"
"v_lshl_add_u32 v252, v205, 4, s60\n\t"
"s_lshr_b32 s71, %5, 6\n\t"
"s_mul_i32 s71, s71, 0x9e3779b1\n\t"
"s_lshr_b32 s71, s71, 28\n\t"
"s_add_u32 s71, s71, 2\n\t"
"s_lshl_b32 s71, s71, 21\n\t"
"s_mov_b64 s[46:47], %3\n\t"
"s_add_u32 s48, s46, 12288\n\t"
"s_addc_u32 s49, s47, 0\n\t"
"global_load_dwordx4 v[244:247], v252, s[46:47] offset:2048\n\t"
"s_and_b32 s60, s69, 3\n\t"
"s_lshr_b32 s61, s69, 2\n\t"
"s_mul_i32 s62, s61, 171\n\t"
"s_lshr_b32 s62, s62, 9\n\t"
"s_mul_i32 s63, s62, 3\n\t"
"s_sub_u32 s61, s61, s63\n\t"
"s_mul_i32 s63, s62, 43\n\t"
"s_lshr_b32 s63, s63, 7\n\t"
"s_mul_i32 s64, s63, 3\n\t"
"s_sub_u32 s62, s62, s64\n\t"
"s_mul_i32 s74, s62, 18\n\t"
"s_add_u32 s74, s74, s63\n\t"
"s_lshl3_add_u32 s74, s74, s60\n\t"
"s_mul_i32 s74, s74, 12\n\t"
"s_add_u32 s74, s74, s61\n\t"
"s_lshl_b32 s74, s74, 7\n\t"
"v_bfe_u32 v96, v205, 3, 2\n\t"
"v_and_b32 v97, 32, v205\n\t"
"v_lshl_or_b32 v96, v96, 1, v97\n\t"
"v_mul_u32_u24 v96, 192, v96\n\t"
"v_and_b32 v97, 7, v205\n\t"
"v_lshl_add_u32 v96, v97, 4, v96\n\t"
"v_and_b32 v4, 7, v205\n\t"
"v_xor_b32 v4, v3, v4\n\t"
"v_and_b32 v2, 7, v2\n\t"
"v_mul_u32_u24 v2, 384, v2\n\t"
"v_lshl_add_u32 v29, v4, 4, v2\n\t"
"s_sub_u32 s58, s73, 1\n\t"
"s_lshl_b32 s59, s58, 10\n\t"
"s_lshl_b32 s58, s58, 7\n\t"
"s_waitcnt lgkmcnt(0)\n\t"
"s_and_b32 s13, s13, 0xffff\n\t"
"s_and_b32 s53, s53, 0xffff\n\t"
"s_and_b32 s71, s71, s76\n\t"
"s_add_u32 s8, s8, s71\n\t"
"s_addc_u32 s9, s9, 0\n\t"
"s_and_b32 s9, s9, 0xffff\n\t"
"s_cmp_lg_u32 s73, 0\n\t"
"s_cbranch_scc1 Lpb_%=\n\t"
"s_cmp_lt_u32 s69, 216\n\t"
"s_cbranch_scc0 Lnf1_%=\n\t"
"s_mov_b32 m0, 95232\n\t"
"s_add_u32 s64, s74, 0\n\t"
"buffer_load_dwordx4 v96, s[52:55], s64 offen lds\n\t"
"s_mov_b32 m0, 96256\n\t"
"s_add_u32 s64, s74, 73728\n\t"
"buffer_load_dwordx4 v96, s[52:55], s64 offen lds\n\t"
"s_mov_b32 m0, 97280\n\t"
"s_add_u32 s64, s74, 147456\n\t"
"buffer_load_dwordx4 v96, s[52:55], s64 offen lds\n\t"
"Lnf1_%=:\n\t"
"s_mov_b32 s72, 1900544\n\t"
"s_lshl2_add_u32 s70, %2, s72\n\t"
"s_mov_b32 s68, 3000\n\t"
"buffer_load_dwordx4 v[220:223], v252, s[8:11], s72 offen sc1\n\t"
"s_branch Lpa_%=\n\t"
"Lpb_%=:\n\t"
"s_lshr_b32 s56, %2, 3\n\t"
"s_bitcmp1_b32 0xaaaaaaaa, s56\n\t"
"s_cbranch_scc1 Lpc_%=\n\t"
"s_add_u32 m0, s59, 24576\n\t"
"s_add_u32 s57, s58, 0\n\t"
"buffer_load_dwordx4 v29, s[52:55], s57 offen offset:0 lds\n\t"
"buffer_load_dwordx4 v29, s[52:55], s57 offen offset:3072 lds\n\t"
"s_add_u32 m0, s59, 30720\n\t"
"s_add_u32 s57, s58, 6144\n\t"
"buffer_load_dwordx4 v29, s[52:55], s57 offen offset:0 lds\n\t"
"buffer_load_dwordx4 v29, s[52:55], s57 offen offset:3072 lds\n\t"
"s_add_u32 m0, s59, 36864\n\t"
"s_add_u32 s57, s58, 221184\n\t"
"buffer_load_dwordx4 v29, s[52:55], s57 offen offset:0 lds\n\t"
"buffer_load_dwordx4 v29, s[52:55], s57 offen offset:3072 lds\n\t"
"s_add_u32 m0, s59, 82944\n\t"
"s_add_u32 s57, s58, 227328\n\t"
"buffer_load_dwordx4 v29, s[52:55], s57 offen offset:0 lds\n\t"
"buffer_load_dwordx4 v29, s[52:55], s57 offen offset:3072 lds\n\t"
"s_add_u32 m0, s59, 89088\n\t"
"s_add_u32 s57, s58, 442368\n\t"
"buffer_load_dwordx4 v29, s[52:55], s57 offen offset:0 lds\n\t"
"buffer_load_dwordx4 v29, s[52:55], s57 offen offset:3072 lds\n\t"
"s_add_u32 m0, s59, 142848\n\t"
"s_add_u32 s57, s58, 448512\n\t"
"buffer_load_dwordx4 v29, s[52:55], s57 offen offset:0 lds\n\t"
"buffer_load_dwordx4 v29, s[52:55], s57 offen offset:3072 lds\n\t"
"s_branch Lpro_%=\n\t"
"Lpa_%=:\n\t"
"s_mul_i32 s75, s63, 288\n\t"
"s_add_u32 s75, s75, s62\n\t"
"s_lshl2_add_u32 s75, s75, s60\n\t"
"s_cmp_lt_u32 s61, 2\n\t"
"s_cselect_b64 vcc, -1, 0\n\t"
"s_cselect_b32 s61, s61, 4\n\t"
"s_mulk_i32 s61, 24\n\t"
"s_add_u32 s75, s75, s61\n\t"
"s_lshl_b32 s75, s75, 8\n\t"
"v_and_b32 v99, 12, v205\n\t"
"v_and_b32 v60, 35, v205\n\t"
"v_lshl_or_b32 v60, v99, 1, v60\n\t"
"v_bfe_u32 v99, v205, 4, 1\n\t"
"v_lshl_or_b32 v60, v99, 2, v60\n\t"
"v_cndmask_b32 v60, v205, v60, vcc\n\t"
"v_lshlrev_b32 v60, 1, v60\n\t"
"v_add_u32 v60, 95232, v60\n\t"
"v_lshrrev_b32 v97, 5, v205\n\t"
"v_lshl_or_b32 v97, v99, 2, v97\n\t"
"v_lshrrev_b32 v99, 4, v205\n\t"
"v_cndmask_b32 v97, v99, v97, vcc\n\t"
"v_mul_u32_u24 v97, 3072, v97\n\t"
"v_and_b32 v99, 15, v205\n\t"
"v_lshl_add_u32 v97, v99, 4, v97\n\t"
"s_cmp_lt_u32 s63, 2\n\t"
"s_mov_b32 s56, 0x3f2aaaab\n\t"
"s_mov_b32 s57, 0x3fc00000\n\t"
"s_mov_b32 s58, 0x3ee38e39\n\t"
"s_mov_b32 s59, 0x40100000\n\t"
"s_mov_b32 s62, 0xbf54ad4b\n\t"
"s_mov_b32 s63, 0xbefc0fc1\n\t"
"s_mov_b32 s64, 0xbe7c0fc1\n\t"
"s_mov_b32 s65, 0x3ea80a81\n\t"
"s_mov_b32 s38, 0x3f638e39\n\t"
"s_mov_b32 s39, 0x3f638e39\n\t"
"s_cselect_b32 s38, 0x3ea80a81, s38\n\t"
"s_cselect_b32 s39, 0xbebd0bd1, s39\n\t"
"v_mov_b64 v[32:33], 0\n\t"
"v_mov_b64 v[34:35], 0\n\t"
"v_mov_b64 v[36:37], 0\n\t"
"v_mov_b64 v[38:39], 0\n\t"
"v_mov_b64 v[40:41], 0\n\t"
"v_mov_b64 v[42:43], 0\n\t"
"v_mov_b64 v[44:45], 0\n\t"
"v_mov_b64 v[46:47], 0\n\t"
"v_mov_b64 v[48:49], 0\n\t"
"v_mov_b64 v[50:51], 0\n\t"
"v_mov_b64 v[52:53], 0\n\t"
"v_mov_b64 v[54:55], 0\n\t"
"s_mul_i32 s25, s73, 19968\n\t"
"s_add_u32 s25, s25, 61440\n\t"
"s_bfe_u32 s41, %2, 0x40003\n\t"
"s_cmp_eq_u32 s41, 0\n\t"
"s_cselect_b32 s40, -1, 0xbfffffff\n\t"
"s_cmp_eq_u32 s41, 15\n\t"
"s_cselect_b32 s41, -1, 0xdfffffff\n\t"
"s_and_b32 s40, s40, s41\n\t"
"s_sub_u32 s42, s21, 384\n\t"
"s_mul_i32 s41, s73, 589824\n\t"
"s_add_u32 s42, s42, s41\n\t"
"s_and_b32 s41, s40, 536883584\n\t"
"s_and_b32 s40, s40, 2.0\n\t"
"v_bfe_u32 v26, v205, 2, 3\n\t"
"v_add_u32 v27, -1, v26\n\t"
"v_cmp_gt_u32 vcc, 6, v27\n\t"
"v_lshrrev_b32 v27, 1, v27\n\t"
"v_mul_u32_u24 v27, 196608, v27\n\t"
"v_and_b32 v20, 0x70, v28\n\t"
"v_add3_u32 v27, v27, v20, s42\n\t"
"v_bfe_i32 v20, v205, 2, 1\n\t"
"v_mov_b32 v21, s40\n\t"
"v_bfi_b32 v20, v20, s41, v21\n\t"
"v_add_u32 v27, v27, v20\n\t"
"v_bfrev_b32 v20, 8\n\t"
"v_cndmask_b32 v254, v20, v27, vcc\n\t"
"v_bfe_u32 v26, v205, 2, 3\n\t"
"v_bfe_u32 v27, v205, 5, 1\n\t"
"v_mul_u32_u24 v27, 3072, v27\n\t"
"v_lshl_add_u32 v27, v26, 7, v27\n\t"
"v_and_b32 v26, 6, v26\n\t"
"v_lshrrev_b32 v20, 4, v205\n\t"
"v_and_b32 v20, 4, v20\n\t"
"v_and_or_b32 v20, v205, 3, v20\n\t"
"v_xor_b32 v20, v20, v26\n\t"
"v_lshl_add_u32 v20, v20, 4, v27\n\t"
"v_lshrrev_b32 v21, 7, v205\n\t"
"v_lshl_add_u32 v20, v21, 10, v20\n\t"
"v_lshrrev_b32 v21, 5, v205\n\t"
"v_and_b32 v21, 6, v21\n\t"
"v_bfe_u32 v22, v205, 1, 1\n\t"
"v_or_b32 v21, v21, v22\n\t"
"v_xor_b32 v21, v21, v26\n\t"
"v_lshl_add_u32 v21, v21, 4, v27\n\t"
"v_and_b32 v22, 1, v205\n\t"
"v_lshl_add_u32 v21, v22, 3, v21\n\t"
"v_add_u32 v22, 142848, v20\n\t"
"v_add_u32 v23, 142848, v21\n\t"
"v_add_u32 v24, 82944, v20\n\t"
"v_add_u32 v25, 82944, v21\n\t"
"s_and_b32 s17, s17, 0xffff\n\t"
"s_mov_b32 s18, 100663296\n\t"
"s_mov_b32 s19, 0x20000\n\t"
"s_lshr_b32 s41, s73, 1\n\t"
"s_add_u32 s42, s21, 196608\n\t"
"s_mul_i32 s43, s41, 384\n\t"
"s_add_u32 s22, s42, s43\n\t"
"s_lshl1_add_u32 s40, s43, s22\n\t"
"s_mul_i32 s43, s41, 196608\n\t"
"s_add_u32 s23, s42, s43\n\t"
"s_mov_b32 s44, 0x0fff0fff\n\t"
"s_mov_b32 s45, 0x0fff0fff\n\t"
"s_mul_i32 s26, s41, 19968\n\t"
"s_add_u32 s26, s26, 141312\n\t"
"s_sub_u32 s43, s41, s73\n\t"
"s_add_u32 s43, s43, 4\n\t"
"s_mul_i32 s27, s43, 589824\n\t"
"s_bitcmp1_b32 s73, 0\n\t"
"s_mov_b32 s36, 0x0fff0000\n\t"
"s_cselect_b32 s36, 0xfff0, s36\n\t"
"s_mov_b32 s37, s36\n\t"
"s_mul_i32 s80, s73, 3072\n\t"
"s_add_u32 s80, s80, s8\n\t"
"s_addc_u32 s81, s9, 0x100000\n\t"
"s_mov_b32 s82, 0x1c0000\n\t"
"s_mov_b32 s83, 0x800000\n\t"
"s_cmp_lt_u32 s69, 216\n\t"
"s_cbranch_scc0 Lnf3_%=\n\t"
"s_waitcnt vmcnt(1)\n\t"
"ds_read_u16_d16_hi v32, v60 offset:0\n\t"
"ds_read_u16_d16_hi v33, v60 offset:128\n\t"
"ds_read_u16_d16_hi v40, v60 offset:1024\n\t"
"ds_read_u16_d16_hi v41, v60 offset:1152\n\t"
"ds_read_u16_d16_hi v48, v60 offset:2048\n\t"
"ds_read_u16_d16_hi v49, v60 offset:2176\n\t"
"ds_read_u16_d16_hi v34, v60 offset:256\n\t"
"ds_read_u16_d16_hi v35, v60 offset:384\n\t"
"ds_read_u16_d16_hi v42, v60 offset:1280\n\t"
"ds_read_u16_d16_hi v43, v60 offset:1408\n\t"
"ds_read_u16_d16_hi v50, v60 offset:2304\n\t"
"ds_read_u16_d16_hi v51, v60 offset:2432\n\t"
"ds_read_u16_d16_hi v36, v60 offset:512\n\t"
"ds_read_u16_d16_hi v37, v60 offset:640\n\t"
"ds_read_u16_d16_hi v44, v60 offset:1536\n\t"
"ds_read_u16_d16_hi v45, v60 offset:1664\n\t"
"ds_read_u16_d16_hi v52, v60 offset:2560\n\t"
"ds_read_u16_d16_hi v53, v60 offset:2688\n\t"
"ds_read_u16_d16_hi v38, v60 offset:768\n\t"
"ds_read_u16_d16_hi v39, v60 offset:896\n\t"
"ds_read_u16_d16_hi v46, v60 offset:1792\n\t"
"ds_read_u16_d16_hi v47, v60 offset:1920\n\t"
"ds_read_u16_d16_hi v54, v60 offset:2816\n\t"
"ds_read_u16_d16_hi v55, v60 offset:2944\n\t"
"s_waitcnt lgkmcnt(15)\n\t"
"v_pk_fma_f32 v[56:57], v[48:49], s[58:59], v[32:33] op_sel:[0,0,0] op_sel_hi:[1,0,1]\n\t"
"v_pk_fma_f32 v[58:59], v[48:49], s[58:59], v[32:33] op_sel:[0,1,0] op_sel_hi:[1,1,1]\n\t"
"v_pk_mul_f32 v[104:105], v[32:33], s[38:39] op_sel:[0,0] op_sel_hi:[1,0]\n\t"
"v_pk_mul_f32 v[114:115], v[48:49], s[38:39] op_sel:[0,1] op_sel_hi:[1,1]\n\t"
"v_pk_fma_f32 v[106:107], v[40:41], s[56:57], v[56:57] op_sel:[0,0,0] op_sel_hi:[1,0,1]\n\t"
"v_pk_fma_f32 v[108:109], v[40:41], s[56:57], v[56:57] op_sel:[0,0,0] op_sel_hi:[1,0,1] neg_lo:[1,0,0] neg_hi:[1,0,0]\n\t"
"v_pk_fma_f32 v[110:111], v[40:41], s[56:57], v[58:59] op_sel:[0,1,0] op_sel_hi:[1,1,1]\n\t"
"v_pk_fma_f32 v[112:113], v[40:41], s[56:57], v[58:59] op_sel:[0,1,0] op_sel_hi:[1,1,1] neg_lo:[1,0,0] neg_hi:[1,0,0]\n\t"
"v_pk_mul_f32 v[106:107], v[106:107], s[62:63] op_sel:[0,0] op_sel_hi:[1,0]\n\t"
"v_pk_mul_f32 v[108:109], v[108:109], s[62:63] op_sel:[0,1] op_sel_hi:[1,1]\n\t"
"v_pk_mul_f32 v[110:111], v[110:111], s[64:65] op_sel:[0,0] op_sel_hi:[1,0]\n\t"
"v_pk_mul_f32 v[112:113], v[112:113], s[64:65] op_sel:[0,1] op_sel_hi:[1,1]\n\t"
"v_cvt_pk_bf16_f32 v64, v104, v105\n\t"
"v_cvt_pk_bf16_f32 v84, v114, v115\n\t"
"v_cvt_pk_bf16_f32 v68, v106, v107\n\t"
"v_cvt_pk_bf16_f32 v72, v108, v109\n\t"
"v_cvt_pk_bf16_f32 v76, v110, v111\n\t"
"v_cvt_pk_bf16_f32 v80, v112, v113\n\t"
"s_waitcnt lgkmcnt(12)\n\t"
"v_pk_fma_f32 v[56:57], v[50:51], s[58:59], v[34:35] op_sel:[0,0,0] op_sel_hi:[1,0,1]\n\t"
"v_pk_fma_f32 v[58:59], v[50:51], s[58:59], v[34:35] op_sel:[0,1,0] op_sel_hi:[1,1,1]\n\t"
"v_pk_mul_f32 v[104:105], v[34:35], s[38:39] op_sel:[0,0] op_sel_hi:[1,0]\n\t"
"v_pk_mul_f32 v[114:115], v[50:51], s[38:39] op_sel:[0,1] op_sel_hi:[1,1]\n\t"
"v_pk_fma_f32 v[106:107], v[42:43], s[56:57], v[56:57] op_sel:[0,0,0] op_sel_hi:[1,0,1]\n\t"
"v_pk_fma_f32 v[108:109], v[42:43], s[56:57], v[56:57] op_sel:[0,0,0] op_sel_hi:[1,0,1] neg_lo:[1,0,0] neg_hi:[1,0,0]\n\t"
"v_pk_fma_f32 v[110:111], v[42:43], s[56:57], v[58:59] op_sel:[0,1,0] op_sel_hi:[1,1,1]\n\t"
"v_pk_fma_f32 v[112:113], v[42:43], s[56:57], v[58:59] op_sel:[0,1,0] op_sel_hi:[1,1,1] neg_lo:[1,0,0] neg_hi:[1,0,0]\n\t"
"v_pk_mul_f32 v[106:107], v[106:107], s[62:63] op_sel:[0,0] op_sel_hi:[1,0]\n\t"
"v_pk_mul_f32 v[108:109], v[108:109], s[62:63] op_sel:[0,1] op_sel_hi:[1,1]\n\t"
"v_pk_mul_f32 v[110:111], v[110:111], s[64:65] op_sel:[0,0] op_sel_hi:[1,0]\n\t"
"v_pk_mul_f32 v[112:113], v[112:113], s[64:65] op_sel:[0,1] op_sel_hi:[1,1]\n\t"
"v_cvt_pk_bf16_f32 v65, v104, v105\n\t"
"v_cvt_pk_bf16_f32 v85, v114, v115\n\t"
"v_cvt_pk_bf16_f32 v69, v106, v107\n\t"
"v_cvt_pk_bf16_f32 v73, v108, v109\n\t"
"v_cvt_pk_bf16_f32 v77, v110, v111\n\t"
"v_cvt_pk_bf16_f32 v81, v112, v113\n\t"
"s_waitcnt lgkmcnt(6)\n\t"
"v_pk_fma_f32 v[56:57], v[52:53], s[58:59], v[36:37] op_sel:[0,0,0] op_sel_hi:[1,0,1]\n\t"
"v_pk_fma_f32 v[58:59], v[52:53], s[58:59], v[36:37] op_sel:[0,1,0] op_sel_hi:[1,1,1]\n\t"
"v_pk_mul_f32 v[104:105], v[36:37], s[38:39] op_sel:[0,0] op_sel_hi:[1,0]\n\t"
"v_pk_mul_f32 v[114:115], v[52:53], s[38:39] op_sel:[0,1] op_sel_hi:[1,1]\n\t"
"v_pk_fma_f32 v[106:107], v[44:45], s[56:57], v[56:57] op_sel:[0,0,0] op_sel_hi:[1,0,1]\n\t"
"v_pk_fma_f32 v[108:109], v[44:45], s[56:57], v[56:57] op_sel:[0,0,0] op_sel_hi:[1,0,1] neg_lo:[1,0,0] neg_hi:[1,0,0]\n\t"
"v_pk_fma_f32 v[110:111], v[44:45], s[56:57], v[58:59] op_sel:[0,1,0] op_sel_hi:[1,1,1]\n\t"
"v_pk_fma_f32 v[112:113], v[44:45], s[56:57], v[58:59] op_sel:[0,1,0] op_sel_hi:[1,1,1] neg_lo:[1,0,0] neg_hi:[1,0,0]\n\t"
"v_pk_mul_f32 v[106:107], v[106:107], s[62:63] op_sel:[0,0] op_sel_hi:[1,0]\n\t"
"v_pk_mul_f32 v[108:109], v[108:109], s[62:63] op_sel:[0,1] op_sel_hi:[1,1]\n\t"
"v_pk_mul_f32 v[110:111], v[110:111], s[64:65] op_sel:[0,0] op_sel_hi:[1,0]\n\t"
"v_pk_mul_f32 v[112:113], v[112:113], s[64:65] op_sel:[0,1] op_sel_hi:[1,1]\n\t"
"v_cvt_pk_bf16_f32 v66, v104, v105\n\t"
"v_cvt_pk_bf16_f32 v86, v114, v115\n\t"
"v_cvt_pk_bf16_f32 v70, v106, v107\n\t"
"v_cvt_pk_bf16_f32 v74, v108, v109\n\t"
"v_cvt_pk_bf16_f32 v78, v110, v111\n\t"
"v_cvt_pk_bf16_f32 v82, v112, v113\n\t"
"s_waitcnt lgkmcnt(0)\n\t"
"v_pk_fma_f32 v[56:57], v[54:55], s[58:59], v[38:39] op_sel:[0,0,0] op_sel_hi:[1,0,1]\n\t"
"v_pk_fma_f32 v[58:59], v[54:55], s[58:59], v[38:39] op_sel:[0,1,0] op_sel_hi:[1,1,1]\n\t"
"v_pk_mul_f32 v[104:105], v[38:39], s[38:39] op_sel:[0,0] op_sel_hi:[1,0]\n\t"
"v_pk_mul_f32 v[114:115], v[54:55], s[38:39] op_sel:[0,1] op_sel_hi:[1,1]\n\t"
"v_pk_fma_f32 v[106:107], v[46:47], s[56:57], v[56:57] op_sel:[0,0,0] op_sel_hi:[1,0,1]\n\t"
"v_pk_fma_f32 v[108:109], v[46:47], s[56:57], v[56:57] op_sel:[0,0,0] op_sel_hi:[1,0,1] neg_lo:[1,0,0] neg_hi:[1,0,0]\n\t"
"v_pk_fma_f32 v[110:111], v[46:47], s[56:57], v[58:59] op_sel:[0,1,0] op_sel_hi:[1,1,1]\n\t"
"v_pk_fma_f32 v[112:113], v[46:47], s[56:57], v[58:59] op_sel:[0,1,0] op_sel_hi:[1,1,1] neg_lo:[1,0,0] neg_hi:[1,0,0]\n\t"
"v_pk_mul_f32 v[106:107], v[106:107], s[62:63] op_sel:[0,0] op_sel_hi:[1,0]\n\t"
"v_pk_mul_f32 v[108:109], v[108:109], s[62:63] op_sel:[0,1] op_sel_hi:[1,1]\n\t"
"v_pk_mul_f32 v[110:111], v[110:111], s[64:65] op_sel:[0,0] op_sel_hi:[1,0]\n\t"
"v_pk_mul_f32 v[112:113], v[112:113], s[64:65] op_sel:[0,1] op_sel_hi:[1,1]\n\t"
"v_cvt_pk_bf16_f32 v67, v104, v105\n\t"
"v_cvt_pk_bf16_f32 v87, v114, v115\n\t"
"v_cvt_pk_bf16_f32 v71, v106, v107\n\t"
"v_cvt_pk_bf16_f32 v75, v108, v109\n\t"
"v_cvt_pk_bf16_f32 v79, v110, v111\n\t"
"v_cvt_pk_bf16_f32 v83, v112, v113\n\t"
"s_add_u32 s60, s75, 0\n\t"
"buffer_store_dwordx4 v[64:67], v97, s[8:11], s60 offen sc1\n\t"
"s_add_u32 s60, s75, 184320\n\t"
"buffer_store_dwordx4 v[84:87], v97, s[8:11], s60 offen sc1\n\t"
"s_add_u32 s60, s75, 36864\n\t"
"buffer_store_dwordx4 v[68:71], v97, s[8:11], s60 offen sc1\n\t"
"s_add_u32 s60, s75, 73728\n\t"
"s_xor_b32 s60, s60, 256\n\t"
"buffer_store_dwordx4 v[72:75], v97, s[8:11], s60 offen sc1\n\t"
"s_add_u32 s60, s75, 110592\n\t"
"s_xor_b32 s60, s60, 512\n\t"
"buffer_store_dwordx4 v[76:79], v97, s[8:11], s60 offen sc1\n\t"
"s_add_u32 s60, s75, 147456\n\t"
"s_xor_b32 s60, s60, 768\n\t"
"buffer_store_dwordx4 v[80:83], v97, s[8:11], s60 offen sc1\n\t"
"s_waitcnt vmcnt(6)\n\t"
"s_branch Lnf4_%=\n\t"
"Lnf3_%=:\n\t"
"s_waitcnt vmcnt(0)\n\t"
"Lnf4_%=:\n\t"
"s_lshr_b32 s60, %2, 2\n\t"
"v_readlane_b32 s61, v220, s60\n\t"
"v_readlane_b32 s62, v221, s60\n\t"
"v_readlane_b32 s63, v222, s60\n\t"
"v_readlane_b32 s64, v223, s60\n\t"
"s_bitcmp1_b32 %2, 0\n\t"
"s_cselect_b32 s61, s62, s61\n\t"
"s_cselect_b32 s63, s64, s63\n\t"
"s_bitcmp1_b32 %2, 1\n\t"
"s_cselect_b32 s71, s63, s61\n\t"
"s_branch Ljoin_%=\n\t"
"Lpc_%=:\n\t"
"s_add_u32 m0, s24, 59904\n\t"
"s_add_u32 s28, s21, 1769472\n\t"
"buffer_load_dwordx4 v28, s[12:15], s28 offen offset:0 nt lds\n\t"
"s_add_u32 m0, s24, 66048\n\t"
"s_add_u32 s28, s21, 1966080\n\t"
"buffer_load_dwordx4 v28, s[12:15], s28 offen offset:0 nt lds\n\t"
"s_add_u32 m0, s24, 72192\n\t"
"s_add_u32 s28, s21, 2162688\n\t"
"buffer_load_dwordx4 v28, s[12:15], s28 offen offset:0 nt lds\n\t"
"s_mul_i32 s60, s59, 6\n\t"
"s_mul_i32 s61, s59, 192\n\t"
"s_add_u32 s61, s61, s21\n\t"
"s_mul_i32 s62, s73, 3072\n\t"
"v_subrev_u32 v30, s62, v28\n\t"
"s_add_u32 m0, s60, 102912\n\t"
"s_add_u32 s28, s61, 1769472\n\t"
"buffer_load_dwordx4 v30, s[12:15], s28 offen nt lds\n\t"
"s_add_u32 m0, s24, 79872\n\t"
"s_add_u32 s28, s21, 2359296\n\t"
"buffer_load_dwordx4 v28, s[12:15], s28 offen offset:0 nt lds\n\t"
"s_add_u32 m0, s24, 86016\n\t"
"s_add_u32 s28, s21, 2555904\n\t"
"buffer_load_dwordx4 v28, s[12:15], s28 offen offset:0 nt lds\n\t"
"s_add_u32 m0, s24, 92160\n\t"
"s_add_u32 s28, s21, 2752512\n\t"
"buffer_load_dwordx4 v28, s[12:15], s28 offen offset:0 nt lds\n\t"
"s_add_u32 m0, s60, 122880\n\t"
"s_add_u32 s28, s61, 2359296\n\t"
"buffer_load_dwordx4 v30, s[12:15], s28 offen nt lds\n\t"
"s_mul_i32 s25, s73, 19968\n\t"
"s_add_u32 s25, s25, 61440\n\t"
"s_bfe_u32 s41, %2, 0x40003\n\t"
"s_cmp_eq_u32 s41, 0\n\t"
"s_cselect_b32 s40, -1, 0xbfffffff\n\t"
"s_cmp_eq_u32 s41, 15\n\t"
"s_cselect_b32 s41, -1, 0xdfffffff\n\t"
"s_and_b32 s40, s40, s41\n\t"
"s_sub_u32 s42, s21, 384\n\t"
"s_mul_i32 s41, s73, 589824\n\t"
"s_add_u32 s42, s42, s41\n\t"
"s_and_b32 s41, s40, 536883584\n\t"
"s_and_b32 s40, s40, 2.0\n\t"
"v_bfe_u32 v26, v205, 2, 3\n\t"
"v_add_u32 v27, -1, v26\n\t"
"v_cmp_gt_u32 vcc, 6, v27\n\t"
"v_lshrrev_b32 v27, 1, v27\n\t"
"v_mul_u32_u24 v27, 196608, v27\n\t"
"v_and_b32 v20, 0x70, v28\n\t"
"v_add3_u32 v27, v27, v20, s42\n\t"
"v_bfe_i32 v20, v205, 2, 1\n\t"
"v_mov_b32 v21, s40\n\t"
"v_bfi_b32 v20, v20, s41, v21\n\t"
"v_add_u32 v27, v27, v20\n\t"
"v_bfrev_b32 v20, 8\n\t"
"v_cndmask_b32 v254, v20, v27, vcc\n\t"
"s_add_u32 m0, s25, 0\n\t"
"s_nop 0\n\t"
"buffer_load_dwordx4 v254, s[12:15], 0 offen nt lds\n\t"
"s_cmp_eq_u32 s73, 3\n\t"
"s_cselect_b32 s62, -1, 1\n\t"
"s_mul_i32 s63, s62, 59904\n\t"
"s_mul_i32 s62, s62, 1769472\n\t"
"v_add_u32 v19, s62, v254\n\t"
"s_add_u32 m0, s25, s63\n\t"
"s_nop 0\n\t"
"buffer_load_dwordx4 v19, s[12:15], 0 offen nt lds\n\t"
"v_bfe_u32 v26, v205, 2, 3\n\t"
"v_bfe_u32 v27, v205, 5, 1\n\t"
"v_mul_u32_u24 v27, 3072, v27\n\t"
"v_lshl_add_u32 v27, v26, 7, v27\n\t"
"v_and_b32 v26, 6, v26\n\t"
"v_lshrrev_b32 v20, 4, v205\n\t"
"v_and_b32 v20, 4, v20\n\t"
"v_and_or_b32 v20, v205, 3, v20\n\t"
"v_xor_b32 v20, v20, v26\n\t"
"v_lshl_add_u32 v20, v20, 4, v27\n\t"
"v_lshrrev_b32 v21, 7, v205\n\t"
"v_lshl_add_u32 v20, v21, 10, v20\n\t"
"v_lshrrev_b32 v21, 5, v205\n\t"
"v_and_b32 v21, 6, v21\n\t"
"v_bfe_u32 v22, v205, 1, 1\n\t"
"v_or_b32 v21, v21, v22\n\t"
"v_xor_b32 v21, v21, v26\n\t"
"v_lshl_add_u32 v21, v21, 4, v27\n\t"
"v_and_b32 v22, 1, v205\n\t"
"v_lshl_add_u32 v21, v22, 3, v21\n\t"
"v_add_u32 v22, 142848, v20\n\t"
"v_add_u32 v23, 142848, v21\n\t"
"v_add_u32 v24, 82944, v20\n\t"
"v_add_u32 v25, 82944, v21\n\t"
"s_and_b32 s17, s17, 0xffff\n\t"
"s_mov_b32 s18, 100663296\n\t"
"s_mov_b32 s19, 0x20000\n\t"
"s_lshr_b32 s41, s73, 1\n\t"
"s_add_u32 s42, s21, 196608\n\t"
"s_mul_i32 s43, s41, 384\n\t"
"s_add_u32 s22, s42, s43\n\t"
"s_lshl1_add_u32 s40, s43, s22\n\t"
"s_mul_i32 s43, s41, 196608\n\t"
"s_add_u32 s23, s42, s43\n\t"
"s_mov_b32 s44, 0x0fff0fff\n\t"
"s_mov_b32 s45, 0x0fff0fff\n\t"
"s_mul_i32 s26, s41, 19968\n\t"
"s_add_u32 s26, s26, 141312\n\t"
"s_sub_u32 s43, s41, s73\n\t"
"s_add_u32 s43, s43, 4\n\t"
"s_mul_i32 s27, s43, 589824\n\t"
"s_bitcmp1_b32 s73, 0\n\t"
"s_mov_b32 s36, 0x0fff0000\n\t"
"s_cselect_b32 s36, 0xfff0, s36\n\t"
"s_mov_b32 s37, s36\n\t"
"s_mul_i32 s80, s73, 3072\n\t"
"s_add_u32 s80, s80, s8\n\t"
"s_addc_u32 s81, s9, 0x100000\n\t"
"s_mov_b32 s82, 0x1c0000\n\t"
"s_mov_b32 s83, 0x800000\n\t"
"s_add_u32 m0, s59, 24576\n\t"
"s_add_u32 s57, s58, 0\n\t"
"buffer_load_dwordx4 v29, s[52:55], s57 offen offset:0 lds\n\t"
"buffer_load_dwordx4 v29, s[52:55], s57 offen offset:3072 lds\n\t"
"s_add_u32 m0, s59, 30720\n\t"
"s_add_u32 s57, s58, 6144\n\t"
"buffer_load_dwordx4 v29, s[52:55], s57 offen offset:0 lds\n\t"
"buffer_load_dwordx4 v29, s[52:55], s57 offen offset:3072 lds\n\t"
"s_add_u32 m0, s59, 36864\n\t"
"s_add_u32 s57, s58, 221184\n\t"
"buffer_load_dwordx4 v29, s[52:55], s57 offen offset:0 lds\n\t"
"buffer_load_dwordx4 v29, s[52:55], s57 offen offset:3072 lds\n\t"
"s_add_u32 m0, s59, 82944\n\t"
"s_add_u32 s57, s58, 227328\n\t"
"buffer_load_dwordx4 v29, s[52:55], s57 offen offset:0 lds\n\t"
"buffer_load_dwordx4 v29, s[52:55], s57 offen offset:3072 lds\n\t"
"s_add_u32 m0, s59, 89088\n\t"
"s_add_u32 s57, s58, 442368\n\t"
"buffer_load_dwordx4 v29, s[52:55], s57 offen offset:0 lds\n\t"
"buffer_load_dwordx4 v29, s[52:55], s57 offen offset:3072 lds\n\t"
"s_add_u32 m0, s59, 142848\n\t"
"s_add_u32 s57, s58, 448512\n\t"
"buffer_load_dwordx4 v29, s[52:55], s57 offen offset:0 lds\n\t"
"buffer_load_dwordx4 v29, s[52:55], s57 offen offset:3072 lds\n\t"
"s_add_u32 m0, s59, 148992\n\t"
"s_add_u32 s57, s58, 147456\n\t"
"buffer_load_dwordx4 v29, s[52:55], s57 offen offset:0 lds\n\t"
"buffer_load_dwordx4 v29, s[52:55], s57 offen offset:3072 lds\n\t"
"s_add_u32 m0, s59, 155136\n\t"
"s_add_u32 s57, s58, 153600\n\t"
"buffer_load_dwordx4 v29, s[52:55], s57 offen offset:0 lds\n\t"
"buffer_load_dwordx4 v29, s[52:55], s57 offen offset:3072 lds\n\t"
"s_add_u32 m0, s59, 0\n\t"
"s_add_u32 s57, s58, 368640\n\t"
"buffer_load_dwordx4 v29, s[52:55], s57 offen offset:0 lds\n\t"
"buffer_load_dwordx4 v29, s[52:55], s57 offen offset:3072 lds\n\t"
"s_add_u32 m0, s59, 6144\n\t"
"s_add_u32 s57, s58, 374784\n\t"
"buffer_load_dwordx4 v29, s[52:55], s57 offen offset:0 lds\n\t"
"buffer_load_dwordx4 v29, s[52:55], s57 offen offset:3072 lds\n\t"
"s_add_u32 m0, s59, 12288\n\t"
"s_add_u32 s57, s58, 589824\n\t"
"buffer_load_dwordx4 v29, s[52:55], s57 offen offset:0 lds\n\t"
"buffer_load_dwordx4 v29, s[52:55], s57 offen offset:3072 lds\n\t"
"s_add_u32 m0, s59, 18432\n\t"
"s_add_u32 s57, s58, 595968\n\t"
"buffer_load_dwordx4 v29, s[52:55], s57 offen offset:0 lds\n\t"
"buffer_load_dwordx4 v29, s[52:55], s57 offen offset:3072 lds\n\t"
"s_add_u32 m0, s24, 19968\n\t"
"s_add_u32 s28, s21, 589824\n\t"
"buffer_load_dwordx4 v28, s[12:15], s28 offen offset:0 nt lds\n\t"
"s_add_u32 m0, s24, 26112\n\t"
"s_add_u32 s28, s21, 786432\n\t"
"buffer_load_dwordx4 v28, s[12:15], s28 offen offset:0 nt lds\n\t"
"s_add_u32 m0, s24, 32256\n\t"
"s_add_u32 s28, s21, 983040\n\t"
"buffer_load_dwordx4 v28, s[12:15], s28 offen offset:0 nt lds\n\t"
"s_add_u32 m0, s60, 62976\n\t"
"s_add_u32 s28, s61, 589824\n\t"
"buffer_load_dwordx4 v30, s[12:15], s28 offen nt lds\n\t"
"s_add_u32 m0, s24, 0\n\t"
"s_add_u32 s28, s21, 0\n\t"
"buffer_load_dwordx4 v28, s[12:15], s28 offen offset:0 nt lds\n\t"
"s_add_u32 m0, s24, 6144\n\t"
"s_add_u32 s28, s21, 196608\n\t"
"buffer_load_dwordx4 v28, s[12:15], s28 offen offset:0 nt lds\n\t"
"s_add_u32 m0, s24, 12288\n\t"
"s_add_u32 s28, s21, 393216\n\t"
"buffer_load_dwordx4 v28, s[12:15], s28 offen offset:0 nt lds\n\t"
"s_add_u32 m0, s60, 43008\n\t"
"s_add_u32 s28, s61, 0\n\t"
"buffer_load_dwordx4 v30, s[12:15], s28 offen nt lds\n\t"
"s_waitcnt vmcnt(42)\n\t"
"s_branch Ljoin_%=\n\t"
"Lfb_%=:\n\t"
"s_waitcnt vmcnt(0) lgkmcnt(0)\n\t"
"s_nop 15\n\t"
"v_lshrrev_b32 v255, 9, v252\n\t"
"v_lshl_add_u32 v255, v255, 10, v252\n\t"
"v_add_u32 v255, 44032, v255\n\t"
"ds_write_b128 v255, v[32:35] offset:0\n\t"
"ds_write_b128 v255, v[36:39] offset:3072\n\t"
"ds_write_b128 v255, v[40:43] offset:6144\n\t"
"ds_write_b128 v255, v[44:47] offset:9216\n\t"
"ds_write_b128 v255, v[48:51] offset:12288\n\t"
"ds_write_b128 v255, v[52:55] offset:15360\n\t"
"ds_write_b128 v255, v[56:59] offset:18432\n\t"
"ds_write_b128 v255, v[60:63] offset:21504\n\t"
"ds_write_b128 v255, v[64:67] offset:24576\n\t"
"ds_write_b128 v255, v[68:71] offset:27648\n\t"
"ds_write_b128 v255, v[72:75] offset:30720\n\t"
"ds_write_b128 v255, v[76:79] offset:33792\n\t"
"ds_write_b128 v255, v[80:83] offset:36864\n\t"
"ds_write_b128 v255, v[84:87] offset:39936\n\t"
"ds_write_b128 v255, v[88:91] offset:43008\n\t"
"ds_write_b128 v255, v[92:95] offset:46080\n\t"
"ds_write_b128 v255, v[96:99] offset:49152\n\t"
"ds_write_b128 v255, v[100:103] offset:52224\n\t"
"ds_write_b128 v255, v[104:107] offset:55296\n\t"
"ds_write_b128 v255, v[108:111] offset:58368\n\t"
"ds_write_b128 v255, v[112:115] offset:61440\n\t"
"ds_write_b128 v255, v[116:119] offset:64512\n\t"
"s_waitcnt lgkmcnt(0)\n\t"
"v_mov_b64 v[32:33], 0\n\t"
"v_mov_b64 v[34:35], 0\n\t"
"v_mov_b64 v[36:37], 0\n\t"
"v_mov_b64 v[38:39], 0\n\t"
"v_mov_b64 v[40:41], 0\n\t"
"v_mov_b64 v[42:43], 0\n\t"
"v_mov_b64 v[44:45], 0\n\t"
"v_mov_b64 v[46:47], 0\n\t"
"v_mov_b64 v[48:49], 0\n\t"
"v_mov_b64 v[50:51], 0\n\t"
"v_mov_b64 v[52:53], 0\n\t"
"v_mov_b64 v[54:55], 0\n\t"
"v_lshrrev_b32 v98, 4, v252\n\t"
"v_bfe_u32 v96, v98, 3, 2\n\t"
"v_and_b32 v97, 32, v98\n\t"
"v_lshl_or_b32 v96, v96, 1, v97\n\t"
"v_mul_u32_u24 v96, 192, v96\n\t"
"v_and_b32 v97, 7, v98\n\t"
"v_lshl_add_u32 v96, v97, 4, v96\n\t"
"s_mov_b32 s69, 0\n\t"
"Lfb1_%=:\n\t"
"s_and_b32 s60, s69, 3\n\t"
"s_lshr_b32 s61, s69, 2\n\t"
"s_mul_i32 s62, s61, 171\n\t"
"s_lshr_b32 s62, s62, 9\n\t"
"s_mul_i32 s63, s62, 3\n\t"
"s_sub_u32 s61, s61, s63\n\t"
"s_mul_i32 s63, s62, 43\n\t"
"s_lshr_b32 s63, s63, 7\n\t"
"s_mul_i32 s64, s63, 3\n\t"
"s_sub_u32 s62, s62, s64\n\t"
"s_mul_i32 s74, s62, 18\n\t"
"s_add_u32 s74, s74, s63\n\t"
"s_lshl3_add_u32 s74, s74, s60\n\t"
"s_mul_i32 s74, s74, 12\n\t"
"s_add_u32 s74, s74, s61\n\t"
"s_lshl_b32 s74, s74, 7\n\t"
"s_mov_b32 m0, 18432\n\t"
"s_add_u32 s64, s74, 0\n\t"
"buffer_load_dwordx4 v96, s[52:55], s64 offen lds\n\t"
"s_mov_b32 m0, 19456\n\t"
"s_add_u32 s64, s74, 73728\n\t"
"buffer_load_dwordx4 v96, s[52:55], s64 offen lds\n\t"
"s_mov_b32 m0, 20480\n\t"
"s_add_u32 s64, s74, 147456\n\t"
"buffer_load_dwordx4 v96, s[52:55], s64 offen lds\n\t"
"s_mul_i32 s75, s63, 288\n\t"
"s_add_u32 s75, s75, s62\n\t"
"s_lshl2_add_u32 s75, s75, s60\n\t"
"s_cmp_lt_u32 s61, 2\n\t"
"s_cselect_b64 vcc, -1, 0\n\t"
"s_cselect_b32 s61, s61, 4\n\t"
"s_mulk_i32 s61, 24\n\t"
"s_add_u32 s75, s75, s61\n\t"
"s_lshl_b32 s75, s75, 8\n\t"
"v_and_b32 v99, 12, v98\n\t"
"v_and_b32 v60, 35, v98\n\t"
"v_lshl_or_b32 v60, v99, 1, v60\n\t"
"v_bfe_u32 v99, v98, 4, 1\n\t"
"v_lshl_or_b32 v60, v99, 2, v60\n\t"
"v_cndmask_b32 v60, v98, v60, vcc\n\t"
"v_lshlrev_b32 v60, 1, v60\n\t"
"v_add_u32 v60, 18432, v60\n\t"
"v_lshrrev_b32 v97, 5, v98\n\t"
"v_lshl_or_b32 v97, v99, 2, v97\n\t"
"v_lshrrev_b32 v99, 4, v98\n\t"
"v_cndmask_b32 v97, v99, v97, vcc\n\t"
"v_mul_u32_u24 v97, 3072, v97\n\t"
"v_and_b32 v99, 15, v98\n\t"
"v_lshl_add_u32 v97, v99, 4, v97\n\t"
"s_cmp_lt_u32 s63, 2\n\t"
"s_mov_b32 s56, 0x3f2aaaab\n\t"
"s_mov_b32 s57, 0x3fc00000\n\t"
"s_mov_b32 s58, 0x3ee38e39\n\t"
"s_mov_b32 s59, 0x40100000\n\t"
"s_mov_b32 s62, 0xbf54ad4b\n\t"
"s_mov_b32 s63, 0xbefc0fc1\n\t"
"s_mov_b32 s64, 0xbe7c0fc1\n\t"
"s_mov_b32 s65, 0x3ea80a81\n\t"
"s_mov_b32 s38, 0x3f638e39\n\t"
"s_mov_b32 s39, 0x3f638e39\n\t"
"s_cselect_b32 s38, 0x3ea80a81, s38\n\t"
"s_cselect_b32 s39, 0xbebd0bd1, s39\n\t"
"s_waitcnt vmcnt(0)\n\t"
"ds_read_u16_d16_hi v32, v60 offset:0\n\t"
"ds_read_u16_d16_hi v33, v60 offset:128\n\t"
"ds_read_u16_d16_hi v40, v60 offset:1024\n\t"
"ds_read_u16_d16_hi v41, v60 offset:1152\n\t"
"ds_read_u16_d16_hi v48, v60 offset:2048\n\t"
"ds_read_u16_d16_hi v49, v60 offset:2176\n\t"
"ds_read_u16_d16_hi v34, v60 offset:256\n\t"
"ds_read_u16_d16_hi v35, v60 offset:384\n\t"
"ds_read_u16_d16_hi v42, v60 offset:1280\n\t"
"ds_read_u16_d16_hi v43, v60 offset:1408\n\t"
"ds_read_u16_d16_hi v50, v60 offset:2304\n\t"
"ds_read_u16_d16_hi v51, v60 offset:2432\n\t"
"ds_read_u16_d16_hi v36, v60 offset:512\n\t"
"ds_read_u16_d16_hi v37, v60 offset:640\n\t"
"ds_read_u16_d16_hi v44, v60 offset:1536\n\t"
"ds_read_u16_d16_hi v45, v60 offset:1664\n\t"
"ds_read_u16_d16_hi v52, v60 offset:2560\n\t"
"ds_read_u16_d16_hi v53, v60 offset:2688\n\t"
"ds_read_u16_d16_hi v38, v60 offset:768\n\t"
"ds_read_u16_d16_hi v39, v60 offset:896\n\t"
"ds_read_u16_d16_hi v46, v60 offset:1792\n\t"
"ds_read_u16_d16_hi v47, v60 offset:1920\n\t"
"ds_read_u16_d16_hi v54, v60 offset:2816\n\t"
"ds_read_u16_d16_hi v55, v60 offset:2944\n\t"
"s_waitcnt lgkmcnt(15)\n\t"
"v_pk_fma_f32 v[56:57], v[48:49], s[58:59], v[32:33] op_sel:[0,0,0] op_sel_hi:[1,0,1]\n\t"
"v_pk_fma_f32 v[58:59], v[48:49], s[58:59], v[32:33] op_sel:[0,1,0] op_sel_hi:[1,1,1]\n\t"
"v_pk_mul_f32 v[104:105], v[32:33], s[38:39] op_sel:[0,0] op_sel_hi:[1,0]\n\t"
"v_pk_mul_f32 v[114:115], v[48:49], s[38:39] op_sel:[0,1] op_sel_hi:[1,1]\n\t"
"v_pk_fma_f32 v[106:107], v[40:41], s[56:57], v[56:57] op_sel:[0,0,0] op_sel_hi:[1,0,1]\n\t"
"v_pk_fma_f32 v[108:109], v[40:41], s[56:57], v[56:57] op_sel:[0,0,0] op_sel_hi:[1,0,1] neg_lo:[1,0,0] neg_hi:[1,0,0]\n\t"
"v_pk_fma_f32 v[110:111], v[40:41], s[56:57], v[58:59] op_sel:[0,1,0] op_sel_hi:[1,1,1]\n\t"
"v_pk_fma_f32 v[112:113], v[40:41], s[56:57], v[58:59] op_sel:[0,1,0] op_sel_hi:[1,1,1] neg_lo:[1,0,0] neg_hi:[1,0,0]\n\t"
"v_pk_mul_f32 v[106:107], v[106:107], s[62:63] op_sel:[0,0] op_sel_hi:[1,0]\n\t"
"v_pk_mul_f32 v[108:109], v[108:109], s[62:63] op_sel:[0,1] op_sel_hi:[1,1]\n\t"
"v_pk_mul_f32 v[110:111], v[110:111], s[64:65] op_sel:[0,0] op_sel_hi:[1,0]\n\t"
"v_pk_mul_f32 v[112:113], v[112:113], s[64:65] op_sel:[0,1] op_sel_hi:[1,1]\n\t"
"v_cvt_pk_bf16_f32 v64, v104, v105\n\t"
"v_cvt_pk_bf16_f32 v84, v114, v115\n\t"
"v_cvt_pk_bf16_f32 v68, v106, v107\n\t"
"v_cvt_pk_bf16_f32 v72, v108, v109\n\t"
"v_cvt_pk_bf16_f32 v76, v110, v111\n\t"
"v_cvt_pk_bf16_f32 v80, v112, v113\n\t"
"s_waitcnt lgkmcnt(12)\n\t"
"v_pk_fma_f32 v[56:57], v[50:51], s[58:59], v[34:35] op_sel:[0,0,0] op_sel_hi:[1,0,1]\n\t"
"v_pk_fma_f32 v[58:59], v[50:51], s[58:59], v[34:35] op_sel:[0,1,0] op_sel_hi:[1,1,1]\n\t"
"v_pk_mul_f32 v[104:105], v[34:35], s[38:39] op_sel:[0,0] op_sel_hi:[1,0]\n\t"
"v_pk_mul_f32 v[114:115], v[50:51], s[38:39] op_sel:[0,1] op_sel_hi:[1,1]\n\t"
"v_pk_fma_f32 v[106:107], v[42:43], s[56:57], v[56:57] op_sel:[0,0,0] op_sel_hi:[1,0,1]\n\t"
"v_pk_fma_f32 v[108:109], v[42:43], s[56:57], v[56:57] op_sel:[0,0,0] op_sel_hi:[1,0,1] neg_lo:[1,0,0] neg_hi:[1,0,0]\n\t"
"v_pk_fma_f32 v[110:111], v[42:43], s[56:57], v[58:59] op_sel:[0,1,0] op_sel_hi:[1,1,1]\n\t"
"v_pk_fma_f32 v[112:113], v[42:43], s[56:57], v[58:59] op_sel:[0,1,0] op_sel_hi:[1,1,1] neg_lo:[1,0,0] neg_hi:[1,0,0]\n\t"
"v_pk_mul_f32 v[106:107], v[106:107], s[62:63] op_sel:[0,0] op_sel_hi:[1,0]\n\t"
"v_pk_mul_f32 v[108:109], v[108:109], s[62:63] op_sel:[0,1] op_sel_hi:[1,1]\n\t"
"v_pk_mul_f32 v[110:111], v[110:111], s[64:65] op_sel:[0,0] op_sel_hi:[1,0]\n\t"
"v_pk_mul_f32 v[112:113], v[112:113], s[64:65] op_sel:[0,1] op_sel_hi:[1,1]\n\t"
"v_cvt_pk_bf16_f32 v65, v104, v105\n\t"
"v_cvt_pk_bf16_f32 v85, v114, v115\n\t"
"v_cvt_pk_bf16_f32 v69, v106, v107\n\t"
"v_cvt_pk_bf16_f32 v73, v108, v109\n\t"
"v_cvt_pk_bf16_f32 v77, v110, v111\n\t"
"v_cvt_pk_bf16_f32 v81, v112, v113\n\t"
"s_waitcnt lgkmcnt(6)\n\t"
"v_pk_fma_f32 v[56:57], v[52:53], s[58:59], v[36:37] op_sel:[0,0,0] op_sel_hi:[1,0,1]\n\t"
"v_pk_fma_f32 v[58:59], v[52:53], s[58:59], v[36:37] op_sel:[0,1,0] op_sel_hi:[1,1,1]\n\t"
"v_pk_mul_f32 v[104:105], v[36:37], s[38:39] op_sel:[0,0] op_sel_hi:[1,0]\n\t"
"v_pk_mul_f32 v[114:115], v[52:53], s[38:39] op_sel:[0,1] op_sel_hi:[1,1]\n\t"
"v_pk_fma_f32 v[106:107], v[44:45], s[56:57], v[56:57] op_sel:[0,0,0] op_sel_hi:[1,0,1]\n\t"
"v_pk_fma_f32 v[108:109], v[44:45], s[56:57], v[56:57] op_sel:[0,0,0] op_sel_hi:[1,0,1] neg_lo:[1,0,0] neg_hi:[1,0,0]\n\t"
"v_pk_fma_f32 v[110:111], v[44:45], s[56:57], v[58:59] op_sel:[0,1,0] op_sel_hi:[1,1,1]\n\t"
"v_pk_fma_f32 v[112:113], v[44:45], s[56:57], v[58:59] op_sel:[0,1,0] op_sel_hi:[1,1,1] neg_lo:[1,0,0] neg_hi:[1,0,0]\n\t"
"v_pk_mul_f32 v[106:107], v[106:107], s[62:63] op_sel:[0,0] op_sel_hi:[1,0]\n\t"
"v_pk_mul_f32 v[108:109], v[108:109], s[62:63] op_sel:[0,1] op_sel_hi:[1,1]\n\t"
"v_pk_mul_f32 v[110:111], v[110:111], s[64:65] op_sel:[0,0] op_sel_hi:[1,0]\n\t"
"v_pk_mul_f32 v[112:113], v[112:113], s[64:65] op_sel:[0,1] op_sel_hi:[1,1]\n\t"
"v_cvt_pk_bf16_f32 v66, v104, v105\n\t"
"v_cvt_pk_bf16_f32 v86, v114, v115\n\t"
"v_cvt_pk_bf16_f32 v70, v106, v107\n\t"
"v_cvt_pk_bf16_f32 v74, v108, v109\n\t"
"v_cvt_pk_bf16_f32 v78, v110, v111\n\t"
"v_cvt_pk_bf16_f32 v82, v112, v113\n\t"
"s_waitcnt lgkmcnt(0)\n\t"
"v_pk_fma_f32 v[56:57], v[54:55], s[58:59], v[38:39] op_sel:[0,0,0] op_sel_hi:[1,0,1]\n\t"
"v_pk_fma_f32 v[58:59], v[54:55], s[58:59], v[38:39] op_sel:[0,1,0] op_sel_hi:[1,1,1]\n\t"
"v_pk_mul_f32 v[104:105], v[38:39], s[38:39] op_sel:[0,0] op_sel_hi:[1,0]\n\t"
"v_pk_mul_f32 v[114:115], v[54:55], s[38:39] op_sel:[0,1] op_sel_hi:[1,1]\n\t"
"v_pk_fma_f32 v[106:107], v[46:47], s[56:57], v[56:57] op_sel:[0,0,0] op_sel_hi:[1,0,1]\n\t"
"v_pk_fma_f32 v[108:109], v[46:47], s[56:57], v[56:57] op_sel:[0,0,0] op_sel_hi:[1,0,1] neg_lo:[1,0,0] neg_hi:[1,0,0]\n\t"
"v_pk_fma_f32 v[110:111], v[46:47], s[56:57], v[58:59] op_sel:[0,1,0] op_sel_hi:[1,1,1]\n\t"
"v_pk_fma_f32 v[112:113], v[46:47], s[56:57], v[58:59] op_sel:[0,1,0] op_sel_hi:[1,1,1] neg_lo:[1,0,0] neg_hi:[1,0,0]\n\t"
"v_pk_mul_f32 v[106:107], v[106:107], s[62:63] op_sel:[0,0] op_sel_hi:[1,0]\n\t"
"v_pk_mul_f32 v[108:109], v[108:109], s[62:63] op_sel:[0,1] op_sel_hi:[1,1]\n\t"
"v_pk_mul_f32 v[110:111], v[110:111], s[64:65] op_sel:[0,0] op_sel_hi:[1,0]\n\t"
"v_pk_mul_f32 v[112:113], v[112:113], s[64:65] op_sel:[0,1] op_sel_hi:[1,1]\n\t"
"v_cvt_pk_bf16_f32 v67, v104, v105\n\t"
"v_cvt_pk_bf16_f32 v87, v114, v115\n\t"
"v_cvt_pk_bf16_f32 v71, v106, v107\n\t"
"v_cvt_pk_bf16_f32 v75, v108, v109\n\t"
"v_cvt_pk_bf16_f32 v79, v110, v111\n\t"
"v_cvt_pk_bf16_f32 v83, v112, v113\n\t"
"s_add_u32 s60, s75, 0\n\t"
"buffer_store_dwordx4 v[64:67], v97, s[8:11], s60 offen sc1\n\t"
"s_add_u32 s60, s75, 184320\n\t"
"buffer_store_dwordx4 v[84:87], v97, s[8:11], s60 offen sc1\n\t"
"s_add_u32 s60, s75, 36864\n\t"
"buffer_store_dwordx4 v[68:71], v97, s[8:11], s60 offen sc1\n\t"
"s_add_u32 s60, s75, 73728\n\t"
"s_xor_b32 s60, s60, 256\n\t"
"buffer_store_dwordx4 v[72:75], v97, s[8:11], s60 offen sc1\n\t"
"s_add_u32 s60, s75, 110592\n\t"
"s_xor_b32 s60, s60, 512\n\t"
"buffer_store_dwordx4 v[76:79], v97, s[8:11], s60 offen sc1\n\t"
"s_add_u32 s60, s75, 147456\n\t"
"s_xor_b32 s60, s60, 768\n\t"
"buffer_store_dwordx4 v[80:83], v97, s[8:11], s60 offen sc1\n\t"
"s_waitcnt vmcnt(0)\n\t"
"s_add_u32 s69, s69, 1\n\t"
"s_cmp_lt_u32 s69, 216\n\t"
"s_cbranch_scc1 Lfb1_%=\n\t"
"s_mov_b32 s69, %2\n\t"
"s_mov_b64 vcc, 0\n\t"
"ds_read_b128 v[32:35], v255 offset:0\n\t"
"ds_read_b128 v[36:39], v255 offset:3072\n\t"
"ds_read_b128 v[40:43], v255 offset:6144\n\t"
"ds_read_b128 v[44:47], v255 offset:9216\n\t"
"ds_read_b128 v[48:51], v255 offset:12288\n\t"
"ds_read_b128 v[52:55], v255 offset:15360\n\t"
"ds_read_b128 v[56:59], v255 offset:18432\n\t"
"ds_read_b128 v[60:63], v255 offset:21504\n\t"
"ds_read_b128 v[64:67], v255 offset:24576\n\t"
"ds_read_b128 v[68:71], v255 offset:27648\n\t"
"ds_read_b128 v[72:75], v255 offset:30720\n\t"
"ds_read_b128 v[76:79], v255 offset:33792\n\t"
"ds_read_b128 v[80:83], v255 offset:36864\n\t"
"ds_read_b128 v[84:87], v255 offset:39936\n\t"
"ds_read_b128 v[88:91], v255 offset:43008\n\t"
"ds_read_b128 v[92:95], v255 offset:46080\n\t"
"ds_read_b128 v[96:99], v255 offset:49152\n\t"
"ds_read_b128 v[100:103], v255 offset:52224\n\t"
"ds_read_b128 v[104:107], v255 offset:55296\n\t"
"ds_read_b128 v[108:111], v255 offset:58368\n\t"
"ds_read_b128 v[112:115], v255 offset:61440\n\t"
"ds_read_b128 v[116:119], v255 offset:64512\n\t"
"s_waitcnt lgkmcnt(0)\n\t"
"s_branch Lgo_%=\n\t"
".p2align 12\n\t"
"Lpro_%=:\n\t"
"s_mul_i32 s25, s73, 19968\n\t"
"s_add_u32 s25, s25, 61440\n\t"
"s_bfe_u32 s41, %2, 0x40003\n\t"
"s_cmp_eq_u32 s41, 0\n\t"
"s_cselect_b32 s40, -1, 0xbfffffff\n\t"
"s_cmp_eq_u32 s41, 15\n\t"
"s_cselect_b32 s41, -1, 0xdfffffff\n\t"
"s_and_b32 s40, s40, s41\n\t"
"s_sub_u32 s42, s21, 384\n\t"
"s_mul_i32 s41, s73, 589824\n\t"
"s_add_u32 s42, s42, s41\n\t"
"s_and_b32 s41, s40, 536883584\n\t"
"s_and_b32 s40, s40, 2.0\n\t"
"v_bfe_u32 v26, v205, 2, 3\n\t"
"v_add_u32 v27, -1, v26\n\t"
"v_cmp_gt_u32 vcc, 6, v27\n\t"
"v_lshrrev_b32 v27, 1, v27\n\t"
"v_mul_u32_u24 v27, 196608, v27\n\t"
"v_and_b32 v20, 0x70, v28\n\t"
"v_add3_u32 v27, v27, v20, s42\n\t"
"v_bfe_i32 v20, v205, 2, 1\n\t"
"v_mov_b32 v21, s40\n\t"
"v_bfi_b32 v20, v20, s41, v21\n\t"
"v_add_u32 v27, v27, v20\n\t"
"v_bfrev_b32 v20, 8\n\t"
"v_cndmask_b32 v254, v20, v27, vcc\n\t"
"v_bfe_u32 v26, v205, 2, 3\n\t"
"v_bfe_u32 v27, v205, 5, 1\n\t"
"v_mul_u32_u24 v27, 3072, v27\n\t"
"v_lshl_add_u32 v27, v26, 7, v27\n\t"
"v_and_b32 v26, 6, v26\n\t"
"v_lshrrev_b32 v20, 4, v205\n\t"
"v_and_b32 v20, 4, v20\n\t"
"v_and_or_b32 v20, v205, 3, v20\n\t"
"v_xor_b32 v20, v20, v26\n\t"
"v_lshl_add_u32 v20, v20, 4, v27\n\t"
"v_lshrrev_b32 v21, 7, v205\n\t"
"v_lshl_add_u32 v20, v21, 10, v20\n\t"
"v_lshrrev_b32 v21, 5, v205\n\t"
"v_and_b32 v21, 6, v21\n\t"
"v_bfe_u32 v22, v205, 1, 1\n\t"
"v_or_b32 v21, v21, v22\n\t"
"v_xor_b32 v21, v21, v26\n\t"
"v_lshl_add_u32 v21, v21, 4, v27\n\t"
"v_and_b32 v22, 1, v205\n\t"
"v_lshl_add_u32 v21, v22, 3, v21\n\t"
"v_add_u32 v22, 142848, v20\n\t"
"v_add_u32 v23, 142848, v21\n\t"
"v_add_u32 v24, 82944, v20\n\t"
"v_add_u32 v25, 82944, v21\n\t"
"s_and_b32 s17, s17, 0xffff\n\t"
"s_mov_b32 s18, 100663296\n\t"
"s_mov_b32 s19, 0x20000\n\t"
"s_lshr_b32 s41, s73, 1\n\t"
"s_add_u32 s42, s21, 196608\n\t"
"s_mul_i32 s43, s41, 384\n\t"
"s_add_u32 s22, s42, s43\n\t"
"s_lshl1_add_u32 s40, s43, s22\n\t"
"s_mul_i32 s43, s41, 196608\n\t"
"s_add_u32 s23, s42, s43\n\t"
"s_mov_b32 s44, 0x0fff0fff\n\t"
"s_mov_b32 s45, 0x0fff0fff\n\t"
"s_mul_i32 s26, s41, 19968\n\t"
"s_add_u32 s26, s26, 141312\n\t"
"s_sub_u32 s43, s41, s73\n\t"
"s_add_u32 s43, s43, 4\n\t"
"s_mul_i32 s27, s43, 589824\n\t"
"s_bitcmp1_b32 s73, 0\n\t"
"s_mov_b32 s36, 0x0fff0000\n\t"
"s_cselect_b32 s36, 0xfff0, s36\n\t"
"s_mov_b32 s37, s36\n\t"
"s_mul_i32 s80, s73, 3072\n\t"
"s_add_u32 s80, s80, s8\n\t"
"s_addc_u32 s81, s9, 0x100000\n\t"
"s_mov_b32 s82, 0x1c0000\n\t"
"s_mov_b32 s83, 0x800000\n\t"
"s_add_u32 m0, s59, 148992\n\t"
"s_add_u32 s57, s58, 147456\n\t"
"buffer_load_dwordx4 v29, s[52:55], s57 offen offset:0 lds\n\t"
"buffer_load_dwordx4 v29, s[52:55], s57 offen offset:3072 lds\n\t"
"s_add_u32 m0, s59, 155136\n\t"
"s_add_u32 s57, s58, 153600\n\t"
"buffer_load_dwordx4 v29, s[52:55], s57 offen offset:0 lds\n\t"
"buffer_load_dwordx4 v29, s[52:55], s57 offen offset:3072 lds\n\t"
"s_add_u32 m0, s59, 0\n\t"
"s_add_u32 s57, s58, 368640\n\t"
"buffer_load_dwordx4 v29, s[52:55], s57 offen offset:0 lds\n\t"
"buffer_load_dwordx4 v29, s[52:55], s57 offen offset:3072 lds\n\t"
"s_add_u32 m0, s59, 6144\n\t"
"s_add_u32 s57, s58, 374784\n\t"
"buffer_load_dwordx4 v29, s[52:55], s57 offen offset:0 lds\n\t"
"buffer_load_dwordx4 v29, s[52:55], s57 offen offset:3072 lds\n\t"
"s_add_u32 m0, s59, 12288\n\t"
"s_add_u32 s57, s58, 589824\n\t"
"buffer_load_dwordx4 v29, s[52:55], s57 offen offset:0 lds\n\t"
"buffer_load_dwordx4 v29, s[52:55], s57 offen offset:3072 lds\n\t"
"s_add_u32 m0, s59, 18432\n\t"
"s_add_u32 s57, s58, 595968\n\t"
"buffer_load_dwordx4 v29, s[52:55], s57 offen offset:0 lds\n\t"
"buffer_load_dwordx4 v29, s[52:55], s57 offen offset:3072 lds\n\t"
"s_add_u32 m0, s25, 0\n\t"
"s_nop 0\n\t"
"buffer_load_dwordx4 v254, s[12:15], 0 offen nt lds\n\t"
"s_cmp_eq_u32 s73, 3\n\t"
"s_cselect_b32 s62, -1, 1\n\t"
"s_mul_i32 s63, s62, 59904\n\t"
"s_mul_i32 s62, s62, 1769472\n\t"
"v_add_u32 v19, s62, v254\n\t"
"s_add_u32 m0, s25, s63\n\t"
"s_nop 0\n\t"
"buffer_load_dwordx4 v19, s[12:15], 0 offen nt lds\n\t"
"s_add_u32 m0, s24, 59904\n\t"
"s_add_u32 s28, s21, 1769472\n\t"
"buffer_load_dwordx4 v28, s[12:15], s28 offen offset:0 nt lds\n\t"
"s_add_u32 m0, s24, 66048\n\t"
"s_add_u32 s28, s21, 1966080\n\t"
"buffer_load_dwordx4 v28, s[12:15], s28 offen offset:0 nt lds\n\t"
"s_add_u32 m0, s24, 72192\n\t"
"s_add_u32 s28, s21, 2162688\n\t"
"buffer_load_dwordx4 v28, s[12:15], s28 offen offset:0 nt lds\n\t"
"s_mul_i32 s60, s59, 6\n\t"
"s_mul_i32 s61, s59, 192\n\t"
"s_add_u32 s61, s61, s21\n\t"
"s_mul_i32 s62, s73, 3072\n\t"
"v_subrev_u32 v30, s62, v28\n\t"
"s_add_u32 m0, s60, 102912\n\t"
"s_add_u32 s28, s61, 1769472\n\t"
"buffer_load_dwordx4 v30, s[12:15], s28 offen nt lds\n\t"
"s_add_u32 m0, s24, 79872\n\t"
"s_add_u32 s28, s21, 2359296\n\t"
"buffer_load_dwordx4 v28, s[12:15], s28 offen offset:0 nt lds\n\t"
"s_add_u32 m0, s24, 86016\n\t"
"s_add_u32 s28, s21, 2555904\n\t"
"buffer_load_dwordx4 v28, s[12:15], s28 offen offset:0 nt lds\n\t"
"s_add_u32 m0, s24, 92160\n\t"
"s_add_u32 s28, s21, 2752512\n\t"
"buffer_load_dwordx4 v28, s[12:15], s28 offen offset:0 nt lds\n\t"
"s_add_u32 m0, s60, 122880\n\t"
"s_add_u32 s28, s61, 2359296\n\t"
"buffer_load_dwordx4 v30, s[12:15], s28 offen nt lds\n\t"
"s_add_u32 m0, s24, 19968\n\t"
"s_add_u32 s28, s21, 589824\n\t"
"buffer_load_dwordx4 v28, s[12:15], s28 offen offset:0 nt lds\n\t"
"s_add_u32 m0, s24, 26112\n\t"
"s_add_u32 s28, s21, 786432\n\t"
"buffer_load_dwordx4 v28, s[12:15], s28 offen offset:0 nt lds\n\t"
"s_add_u32 m0, s24, 32256\n\t"
"s_add_u32 s28, s21, 983040\n\t"
"buffer_load_dwordx4 v28, s[12:15], s28 offen offset:0 nt lds\n\t"
"s_add_u32 m0, s60, 62976\n\t"
"s_add_u32 s28, s61, 589824\n\t"
"buffer_load_dwordx4 v30, s[12:15], s28 offen nt lds\n\t"
"s_add_u32 m0, s24, 0\n\t"
"s_add_u32 s28, s21, 0\n\t"
"buffer_load_dwordx4 v28, s[12:15], s28 offen offset:0 nt lds\n\t"
"s_add_u32 m0, s24, 6144\n\t"
"s_add_u32 s28, s21, 196608\n\t"
"buffer_load_dwordx4 v28, s[12:15], s28 offen offset:0 nt lds\n\t"
"s_add_u32 m0, s24, 12288\n\t"
"s_add_u32 s28, s21, 393216\n\t"
"buffer_load_dwordx4 v28, s[12:15], s28 offen offset:0 nt lds\n\t"
"s_add_u32 m0, s60, 43008\n\t"
"s_add_u32 s28, s61, 0\n\t"
"buffer_load_dwordx4 v30, s[12:15], s28 offen nt lds\n\t"
"s_waitcnt vmcnt(42)\n\t"
"Ljoin_%=:\n\t"
"s_mov_b32 s41, 0xc0423f90\n\t"
"s_movk_i32 s43, 0x101\n\t"
"v_mov_b32 v6, 0x01000100\n\t"
"v_mov_b32 v7, 0xbebd0bd1\n\t"
"v_lshrrev_b32 v240, 16, v244\n\t"
"v_lshrrev_b32 v242, 16, v245\n\t"
"v_xor_b32 v241, 32, v240\n\t"
"v_and_b32 v244, 0xffff, v244\n\t"
"v_and_b32 v245, 0xffff, v245\n\t"
"v_cmp_le_u32 vcc, 9216, v242\n\t"
"v_mov_b32 v243, 0x3ea80a81\n\t"
"v_cndmask_b32 v243, v243, v7, vcc\n\t"
"v_and_b32 v8, 0xf000f, v247\n\t"
"v_mad_u32_u24 v8, v8, s43, v6\n\t"
"v_perm_b32 v196, s41, 0, v8\n\t"
"v_lshrrev_b32 v9, 4, v247\n\t"
"v_and_b32 v9, 0xf000f, v9\n\t"
"v_mad_u32_u24 v9, v9, s43, v6\n\t"
"v_perm_b32 v197, s41, 0, v9\n\t"
"v_lshrrev_b32 v10, 8, v247\n\t"
"v_and_b32 v10, 0xf000f, v10\n\t"
"v_mad_u32_u24 v10, v10, s43, v6\n\t"
"v_perm_b32 v198, s41, 0, v10\n\t"
"v_lshrrev_b32 v11, 12, v247\n\t"
"v_and_b32 v11, 0xf000f, v11\n\t"
"v_mad_u32_u24 v11, v11, s43, v6\n\t"
"v_perm_b32 v199, s41, 0, v11\n\t"
"v_mov_b32 v247, v28\n\t"
"s_mov_b64 vcc, -1\n\t"
"s_bitcmp1_b32 s73, 1\n\t"
"s_cbranch_scc1 Lvh0_%=\n\t"
"s_bitcmp1_b32 s73, 0\n\t"
"s_cbranch_scc1 Lpg2_%=\n\t"
"s_branch Lpg1_%=\n\t"
"Lvh0_%=:\n\t"
"s_bitcmp1_b32 s73, 0\n\t"
"s_cbranch_scc1 Lpg4_%=\n\t"
"s_branch Lpg3_%=\n\t"
"Lvr0_%=:\n\t"
"s_mov_b64 vcc, 0\n\t"
"v_add_u32 v240, 59904, v240\n\t"
"v_add_u32 v241, 59904, v241\n\t"
"s_cmp_lt_u32 s69, 216\n\t"
"s_cbranch_scc0 Lna1_%=\n\t"
"s_waitcnt vmcnt(0)\n\t"
"s_add_u32 s71, s71, 1\n\t"
"v_mov_b32 v31, s71\n\t"
"s_mov_b64 exec, 1\n\t"
"buffer_store_dword v31, off, s[8:11], s70 sc1\n\t"
"s_mov_b64 exec, -1\n\t"
"Lna1_%=:\n\t"
"s_cmp_eq_u32 s73, 0\n\t"
"s_cbranch_scc1 Lsk0_%=\n\t"
"s_lshr_b32 s56, %2, 3\n\t"
"s_bitcmp1_b32 0xaaaaaaaa, s56\n\t"
"s_cbranch_scc1 Lkc2_%=\n\t"
"s_waitcnt vmcnt(8)\n\t"
"s_branch Lke2_%=\n\t"
"Lkc2_%=:\n\t"
"s_waitcnt vmcnt(32)\n\t"
"Lke2_%=:\n\t"
"Lsk0_%=:\n\t"
"s_barrier\n\t"
"ds_read_b64_tr_b16 v[120:121], v240 offset:0\n\t"
"ds_read_b64_tr_b16 v[122:123], v240 offset:1536\n\t"
"ds_read_b64_tr_b16 v[124:125], v241 offset:0\n\t"
"ds_read_b64_tr_b16 v[126:127], v241 offset:1536\n\t"
"ds_read_b64_tr_b16 v[128:129], v240 offset:19968\n\t"
"ds_read_b64_tr_b16 v[130:131], v240 offset:21504\n\t"
"ds_read_b64_tr_b16 v[132:133], v241 offset:19968\n\t"
"ds_read_b64_tr_b16 v[134:135], v241 offset:21504\n\t"
"s_waitcnt lgkmcnt(6)\n\t"
"v_mfma_f32_16x16x32_bf16 v[120:123], v[120:123], v[196:199], 0\n\t"
"s_waitcnt lgkmcnt(4)\n\t"
"v_mfma_f32_16x16x32_bf16 v[124:127], v[124:127], v[196:199], 0\n\t"
"s_waitcnt lgkmcnt(2)\n\t"
"v_mfma_f32_16x16x32_bf16 v[128:131], v[128:131], v[196:199], 0\n\t"
"s_waitcnt lgkmcnt(0)\n\t"
"v_mfma_f32_16x16x32_bf16 v[132:135], v[132:135], v[196:199], 0\n\t"
"v_pk_mul_f32 v[120:121], v[120:121], v[242:243] op_sel:[0,1] op_sel_hi:[1,1]\n\t"
"v_pk_mul_f32 v[122:123], v[122:123], v[242:243] op_sel:[0,1] op_sel_hi:[1,1]\n\t"
"v_cvt_pk_bf16_f32 v120, v120, v121\n\t"
"v_cvt_pk_bf16_f32 v121, v122, v123\n\t"
"v_pk_mul_f32 v[124:125], v[124:125], v[242:243] op_sel:[0,1] op_sel_hi:[1,1]\n\t"
"v_pk_mul_f32 v[126:127], v[126:127], v[242:243] op_sel:[0,1] op_sel_hi:[1,1]\n\t"
"v_cvt_pk_bf16_f32 v122, v124, v125\n\t"
"v_cvt_pk_bf16_f32 v123, v126, v127\n\t"
"v_pk_mul_f32 v[128:129], v[128:129], v[242:243] op_sel:[0,1] op_sel_hi:[1,1]\n\t"
"v_pk_mul_f32 v[130:131], v[130:131], v[242:243] op_sel:[0,1] op_sel_hi:[1,1]\n\t"
"v_cvt_pk_bf16_f32 v128, v128, v129\n\t"
"v_cvt_pk_bf16_f32 v129, v130, v131\n\t"
"v_pk_mul_f32 v[132:133], v[132:133], v[242:243] op_sel:[0,1] op_sel_hi:[1,1]\n\t"
"v_pk_mul_f32 v[134:135], v[134:135], v[242:243] op_sel:[0,1] op_sel_hi:[1,1]\n\t"
"v_cvt_pk_bf16_f32 v130, v132, v133\n\t"
"v_cvt_pk_bf16_f32 v131, v134, v135\n\t"
"s_cmp_eq_u32 s73, 0\n\t"
"s_cbranch_scc1 Lsk1_%=\n\t"
"s_lshr_b32 s56, %2, 3\n\t"
"s_bitcmp1_b32 0xaaaaaaaa, s56\n\t"
"s_cbranch_scc1 Lkc3_%=\n\t"
"s_branch Lke3_%=\n\t"
"Lkc3_%=:\n\t"
"s_waitcnt vmcnt(8)\n\t"
"Lke3_%=:\n\t"
"Lsk1_%=:\n\t"
"s_barrier\n\t"
"ds_read_b64_tr_b16 a[0:1], v20 offset:24576\n\t"
"ds_read_b64_tr_b16 a[2:3], v20 offset:30720\n\t"
"ds_read_b64_tr_b16 a[24:25], v20 offset:24584\n\t"
"ds_read_b64_tr_b16 a[26:27], v20 offset:30728\n\t"
"ds_read_b64_tr_b16 a[48:49], v21 offset:26624\n\t"
"ds_read_b64_tr_b16 a[50:51], v21 offset:32768\n\t"
"ds_read_b64_tr_b16 a[8:9], v20 offset:36864\n\t"
"ds_read_b64_tr_b16 a[10:11], v24 offset:0\n\t"
"ds_read_b64_tr_b16 a[32:33], v20 offset:36872\n\t"
"ds_read_b64_tr_b16 a[34:35], v24 offset:8\n\t"
"ds_read_b64_tr_b16 a[56:57], v21 offset:38912\n\t"
"ds_read_b64_tr_b16 a[58:59], v25 offset:2048\n\t"
"ds_read_b64_tr_b16 a[16:17], v24 offset:6144\n\t"
"ds_read_b64_tr_b16 a[18:19], v22 offset:0\n\t"
"ds_read_b64_tr_b16 a[40:41], v24 offset:6152\n\t"
"ds_read_b64_tr_b16 a[42:43], v22 offset:8\n\t"
"ds_read_b64_tr_b16 a[64:65], v25 offset:8192\n\t"
"ds_read_b64_tr_b16 a[66:67], v23 offset:2048\n\t"
"ds_read_b64_tr_b16 a[72:73], v22 offset:6144\n\t"
"ds_read_b64_tr_b16 a[74:75], v22 offset:12288\n\t"
"ds_read_b64_tr_b16 a[96:97], v22 offset:6152\n\t"
"ds_read_b64_tr_b16 a[98:99], v22 offset:12296\n\t"
"ds_read_b64_tr_b16 a[120:121], v23 offset:8192\n\t"
"ds_read_b64_tr_b16 a[122:123], v23 offset:14336\n\t"
"s_waitcnt lgkmcnt(0)\n\t"
"s_barrier\n\t"
"s_cmp_eq_u32 s73, 0\n\t"
"s_cbranch_scc1 Lsk2_%=\n\t"
"s_add_u32 m0, s24, 39936\n\t"
"s_add_u32 s28, s21, 1179648\n\t"
"buffer_load_dwordx4 v28, s[12:15], s28 offen offset:0 nt lds\n\t"
"s_add_u32 m0, s24, 46080\n\t"
"s_add_u32 s28, s21, 1376256\n\t"
"buffer_load_dwordx4 v28, s[12:15], s28 offen offset:0 nt lds\n\t"
"s_add_u32 m0, s24, 52224\n\t"
"s_add_u32 s28, s21, 1572864\n\t"
"buffer_load_dwordx4 v28, s[12:15], s28 offen offset:0 nt lds\n\t"
"s_add_u32 m0, s60, 82944\n\t"
"s_add_u32 s28, s61, 1179648\n\t"
"buffer_load_dwordx4 v30, s[12:15], s28 offen nt lds\n\t"
"Lsk2_%=:\n\t"
"ds_read_b64_tr_b16 a[80:81], v20 offset:0\n\t"
"ds_read_b64_tr_b16 a[82:83], v20 offset:6144\n\t"
"ds_read_b64_tr_b16 a[104:105], v20 offset:8\n\t"
"ds_read_b64_tr_b16 a[106:107], v20 offset:6152\n\t"
"ds_read_b64_tr_b16 a[128:129], v21 offset:2048\n\t"
"ds_read_b64_tr_b16 a[130:131], v21 offset:8192\n\t"
"ds_read_b64_tr_b16 a[88:89], v20 offset:12288\n\t"
"ds_read_b64_tr_b16 a[90:91], v20 offset:18432\n\t"
"ds_read_b64_tr_b16 a[112:113], v20 offset:12296\n\t"
"ds_read_b64_tr_b16 a[114:115], v20 offset:18440\n\t"
"ds_read_b64_tr_b16 a[136:137], v21 offset:14336\n\t"
"ds_read_b64_tr_b16 a[138:139], v21 offset:20480\n\t"
"ds_write_b128 v242, v[120:123] offset:29184\n\t"
"ds_write_b128 v242, v[128:131] offset:30720\n\t"
"s_waitcnt lgkmcnt(0)\n\t"
"v_subrev_u32 v240, 59904, v240\n\t"
"v_subrev_u32 v241, 59904, v241\n\t"
"s_cmp_eq_u32 s73, 0\n\t"
"s_cbranch_scc1 Lsk3_%=\n\t"
"s_waitcnt vmcnt(4)\n\t"
"Lsk3_%=:\n\t"
"s_barrier\n\t"
"ds_read_b64_tr_b16 v[136:137], v240 offset:19968\n\t"
"ds_read_b64_tr_b16 v[138:139], v240 offset:21504\n\t"
"ds_read_b64_tr_b16 v[140:141], v241 offset:19968\n\t"
"ds_read_b64_tr_b16 v[142:143], v241 offset:21504\n\t"
"ds_read_b64_tr_b16 v[144:145], v240 offset:0\n\t"
"ds_read_b64_tr_b16 v[146:147], v240 offset:1536\n\t"
"ds_read_b64_tr_b16 v[148:149], v241 offset:0\n\t"
"ds_read_b64_tr_b16 v[150:151], v241 offset:1536\n\t"
"s_cmp_eq_u32 s73, 0\n\t"
"s_cbranch_scc1 Lsk4_%=\n\t"
"s_add_u32 m0, s24, 99840\n\t"
"s_add_u32 s28, s21, 2949120\n\t"
"buffer_load_dwordx4 v28, s[12:15], s28 offen offset:0 nt lds\n\t"
"s_add_u32 m0, s24, 105984\n\t"
"s_add_u32 s28, s21, 3145728\n\t"
"buffer_load_dwordx4 v28, s[12:15], s28 offen offset:0 nt lds\n\t"
"s_add_u32 m0, s24, 112128\n\t"
"s_add_u32 s28, s21, 3342336\n\t"
"buffer_load_dwordx4 v28, s[12:15], s28 offen offset:0 nt lds\n\t"
"s_add_u32 m0, s60, 142848\n\t"
"s_add_u32 s28, s61, 2949120\n\t"
"buffer_load_dwordx4 v30, s[12:15], s28 offen nt lds\n\t"
"Lsk4_%=:\n\t"
"s_waitcnt lgkmcnt(6)\n\t"
"v_mfma_f32_16x16x32_bf16 v[136:139], v[136:139], v[196:199], 0\n\t"
"s_waitcnt lgkmcnt(4)\n\t"
"v_mfma_f32_16x16x32_bf16 v[140:143], v[140:143], v[196:199], 0\n\t"
"s_waitcnt lgkmcnt(2)\n\t"
"v_mfma_f32_16x16x32_bf16 v[144:147], v[144:147], v[196:199], 0\n\t"
"s_waitcnt lgkmcnt(0)\n\t"
"v_mfma_f32_16x16x32_bf16 v[148:151], v[148:151], v[196:199], 0\n\t"
"v_pk_mul_f32 v[136:137], v[136:137], v[242:243] op_sel:[0,1] op_sel_hi:[1,1]\n\t"
"v_pk_mul_f32 v[138:139], v[138:139], v[242:243] op_sel:[0,1] op_sel_hi:[1,1]\n\t"
"v_cvt_pk_bf16_f32 v136, v136, v137\n\t"
"v_cvt_pk_bf16_f32 v137, v138, v139\n\t"
"v_pk_mul_f32 v[140:141], v[140:141], v[242:243] op_sel:[0,1] op_sel_hi:[1,1]\n\t"
"v_pk_mul_f32 v[142:143], v[142:143], v[242:243] op_sel:[0,1] op_sel_hi:[1,1]\n\t"
"v_cvt_pk_bf16_f32 v138, v140, v141\n\t"
"v_cvt_pk_bf16_f32 v139, v142, v143\n\t"
"ds_write_b128 v242, v[136:139] offset:26112\n\t"
"v_pk_mul_f32 v[144:145], v[144:145], v[242:243] op_sel:[0,1] op_sel_hi:[1,1]\n\t"
"v_pk_mul_f32 v[146:147], v[146:147], v[242:243] op_sel:[0,1] op_sel_hi:[1,1]\n\t"
"v_cvt_pk_bf16_f32 v144, v144, v145\n\t"
"v_cvt_pk_bf16_f32 v145, v146, v147\n\t"
"v_pk_mul_f32 v[148:149], v[148:149], v[242:243] op_sel:[0,1] op_sel_hi:[1,1]\n\t"
"v_pk_mul_f32 v[150:151], v[150:151], v[242:243] op_sel:[0,1] op_sel_hi:[1,1]\n\t"
"v_cvt_pk_bf16_f32 v146, v148, v149\n\t"
"v_cvt_pk_bf16_f32 v147, v150, v151\n\t"
"ds_write_b128 v242, v[144:147] offset:24576\n\t"
"s_mov_b64 vcc, 0\n\t"
"s_waitcnt lgkmcnt(0)\n\t"
"s_barrier\n\t"
"ds_read_b128 a[168:171], v244 offset:26112\n\t"
"ds_read_b128 a[172:175], v244 offset:26624\n\t"
"ds_read_b128 a[176:179], v244 offset:27136\n\t"
"ds_read_b128 a[180:183], v244 offset:35328\n\t"
"ds_read_b128 a[184:187], v244 offset:35840\n\t"
"ds_read_b128 a[188:191], v244 offset:36352\n\t"
"ds_read_b128 a[192:195], v244 offset:25600\n\t"
"s_waitcnt lgkmcnt(4)\n\t"
"v_mfma_f32_16x16x32_bf16 v[14:17], a[0:3], a[168:171], 0\n\t"
"ds_read_b64_tr_b16 v[224:225], v240 offset:512\n\t"
"v_mfma_f32_16x16x32_bf16 v[14:17], a[8:11], a[172:175], v[14:17]\n\t"
"ds_read_b64_tr_b16 v[226:227], v240 offset:2048\n\t"
"v_mfma_f32_16x16x32_bf16 v[14:17], a[16:19], a[176:179], v[14:17]\n\t"
"ds_read_b64_tr_b16 v[228:229], v241 offset:512\n\t"
"v_mfma_f32_16x16x32_bf16 v[46:49], a[32:35], a[172:175], 0\n\t"
"ds_read_b64_tr_b16 v[230:231], v241 offset:2048\n\t"
"v_mfma_f32_16x16x32_bf16 v[46:49], a[40:43], a[176:179], v[46:49]\n\t"
"v_mfma_f32_16x16x32_bf16 v[46:49], a[24:27], a[168:171], v[46:49]\n\t"
"v_mfma_f32_16x16x32_bf16 v[78:81], a[48:51], a[168:171], 0\n\t"
"s_waitcnt lgkmcnt(5)\n\t"
"v_mfma_f32_16x16x32_bf16 v[78:81], a[64:67], a[176:179], v[78:81]\n\t"
"ds_read_b64_tr_b16 v[232:233], v240 offset:20480\n\t"
"v_mfma_f32_16x16x32_bf16 v[78:81], a[56:59], a[172:175], v[78:81]\n\t"
"ds_read_b64_tr_b16 v[234:235], v240 offset:22016\n\t"
"v_mfma_f32_16x16x32_bf16 v[112:115], a[72:75], a[180:183], 0\n\t"
"s_waitcnt lgkmcnt(4)\n\t"
"ds_read_b64_tr_b16 v[236:237], v241 offset:20480\n\t"
"v_mfma_f32_16x16x32_bf16 v[112:115], a[80:83], a[184:187], v[112:115]\n\t"
"s_waitcnt lgkmcnt(3)\n\t"
"ds_read_b64_tr_b16 v[238:239], v241 offset:22016\n\t"
"v_mfma_f32_16x16x32_bf16 v[112:115], a[88:91], a[188:191], v[112:115]\n\t"
"ds_read_b128 a[176:179], v244 offset:34816\n\t"
"v_mfma_f32_16x16x32_bf16 v[224:227], v[224:227], v[196:199], 0\n\t"
"v_mfma_f32_16x16x32_bf16 v[228:231], v[228:231], v[196:199], 0\n\t"
"v_mfma_f32_16x16x32_bf16 v[144:147], a[112:115], a[188:191], 0\n\t"
"v_mfma_f32_16x16x32_bf16 v[144:147], a[96:99], a[180:183], v[144:147]\n\t"
"v_mfma_f32_16x16x32_bf16 v[144:147], a[104:107], a[184:187], v[144:147]\n\t"
"v_mfma_f32_16x16x32_bf16 v[176:179], a[120:123], a[180:183], 0\n\t"
"v_cvt_pk_bf16_f32 v224, v224, v225\n\t"
"v_cvt_pk_bf16_f32 v225, v226, v227\n\t"
"v_mfma_f32_16x16x32_bf16 v[176:179], a[128:131], a[184:187], v[176:179]\n\t"
"ds_read_b128 a[196:199], v244 offset:25088\n\t"
"v_cvt_pk_bf16_f32 v226, v228, v229\n\t"
"v_mfma_f32_16x16x32_bf16 v[176:179], a[136:139], a[188:191], v[176:179]\n\t"
"v_cvt_pk_bf16_f32 v227, v230, v231\n\t"
"v_mfma_f32_16x16x32_bf16 v[10:13], a[0:3], a[192:195], 0\n\t"
"s_waitcnt lgkmcnt(4)\n\t"
"v_mfma_f32_16x16x32_bf16 v[10:13], a[16:19], a[172:175], v[10:13]\n\t"
"s_waitcnt lgkmcnt(2)\n\t"
"v_mfma_f32_16x16x32_bf16 v[10:13], a[8:11], a[168:171], v[10:13]\n\t"
"v_mfma_f32_16x16x32_bf16 v[232:235], v[232:235], v[196:199], 0\n\t"
"v_mfma_f32_16x16x32_bf16 v[236:239], v[236:239], v[196:199], 0\n\t"
"s_waitcnt vmcnt(4)\n\t"
"s_barrier\n\t"
"v_mfma_f32_16x16x32_bf16 v[42:45], a[32:35], a[168:171], 0\n\t"
"ds_read_b64_tr_b16 v[228:229], v240 offset:39936\n\t"
"v_mfma_f32_16x16x32_bf16 v[42:45], a[24:27], a[192:195], v[42:45]\n\t"
"ds_read_b64_tr_b16 v[230:231], v240 offset:41472\n\t"
"v_mfma_f32_16x16x32_bf16 v[42:45], a[40:43], a[172:175], v[42:45]\n\t"
"ds_write_b128 v242, v[224:227] offset:0\n\t"
"v_mfma_f32_16x16x32_bf16 v[74:77], a[48:51], a[192:195], 0\n\t"
"s_waitcnt lgkmcnt(4)\n\t"
"v_cvt_pk_bf16_f32 v232, v232, v233\n\t"
"v_cvt_pk_bf16_f32 v233, v234, v235\n\t"
"v_mfma_f32_16x16x32_bf16 v[74:77], a[64:67], a[172:175], v[74:77]\n\t"
"ds_read_b64_tr_b16 v[224:225], v241 offset:39936\n\t"
"v_mfma_f32_16x16x32_bf16 v[74:77], a[56:59], a[168:171], v[74:77]\n\t"
"ds_read_b64_tr_b16 v[226:227], v241 offset:41472\n\t"
"v_cvt_pk_bf16_f32 v234, v236, v237\n\t"
"v_mfma_f32_16x16x32_bf16 v[108:111], a[72:75], a[176:179], 0\n\t"
"s_waitcnt lgkmcnt(3)\n\t"
"ds_read_b128 a[172:175], v244 offset:34304\n\t"
"v_cvt_pk_bf16_f32 v235, v238, v239\n\t"
"v_mfma_f32_16x16x32_bf16 v[108:111], a[88:91], a[184:187], v[108:111]\n\t"
"ds_read_b64_tr_b16 v[236:237], v240 offset:40448\n\t"
"v_mfma_f32_16x16x32_bf16 v[108:111], a[80:83], a[180:183], v[108:111]\n\t"
"ds_write_b128 v242, v[232:235] offset:1536\n\t"
"v_mfma_f32_16x16x32_bf16 v[228:231], v[228:231], v[196:199], 0\n\t"
"s_waitcnt lgkmcnt(3)\n\t"
"v_mfma_f32_16x16x32_bf16 v[224:227], v[224:227], v[196:199], 0\n\t"
"ds_read_b64_tr_b16 v[238:239], v240 offset:41984\n\t"
"v_mfma_f32_16x16x32_bf16 v[140:143], a[104:107], a[180:183], 0\n\t"
"ds_read_b64_tr_b16 v[232:233], v241 offset:40448\n\t"
"v_mfma_f32_16x16x32_bf16 v[140:143], a[96:99], a[176:179], v[140:143]\n\t"
"ds_read_b64_tr_b16 v[234:235], v241 offset:41984\n\t"
"v_mfma_f32_16x16x32_bf16 v[140:143], a[112:115], a[184:187], v[140:143]\n\t"
"v_pk_mul_f32 v[228:229], v[228:229], v[242:243] op_sel:[0,1] op_sel_hi:[1,1]\n\t"
"v_pk_mul_f32 v[230:231], v[230:231], v[242:243] op_sel:[0,1] op_sel_hi:[1,1]\n\t"
"v_mfma_f32_16x16x32_bf16 v[172:175], a[120:123], a[176:179], 0\n\t"
"v_cvt_pk_bf16_f32 v228, v228, v229\n\t"
"v_cvt_pk_bf16_f32 v229, v230, v231\n\t"
"v_mfma_f32_16x16x32_bf16 v[172:175], a[136:139], a[184:187], v[172:175]\n\t"
"v_mfma_f32_16x16x32_bf16 v[172:175], a[128:131], a[180:183], v[172:175]\n\t"
"v_pk_mul_f32 v[224:225], v[224:225], v[242:243] op_sel:[0,1] op_sel_hi:[1,1]\n\t"
"v_pk_mul_f32 v[226:227], v[226:227], v[242:243] op_sel:[0,1] op_sel_hi:[1,1]\n\t"
"v_mfma_f32_16x16x32_bf16 v[6:9], a[0:3], a[196:199], 0\n\t"
"v_cvt_pk_bf16_f32 v230, v224, v225\n\t"
"v_cvt_pk_bf16_f32 v231, v226, v227\n\t"
"v_mfma_f32_16x16x32_bf16 v[6:9], a[8:11], a[192:195], v[6:9]\n\t"
"v_add_u32 v240, 59904, v240\n\t"
"v_add_u32 v241, 59904, v241\n\t"
"v_mfma_f32_16x16x32_bf16 v[6:9], a[16:19], a[168:171], v[6:9]\n\t"
"ds_write_b128 v242, v[228:231] offset:27648\n\t"
"v_mfma_f32_16x16x32_bf16 v[38:41], a[32:35], a[192:195], 0\n\t"
"s_waitcnt lgkmcnt(3)\n\t"
"v_mfma_f32_16x16x32_bf16 v[38:41], a[40:43], a[168:171], v[38:41]\n\t"
"s_waitcnt lgkmcnt(1)\n\t"
"ds_read_b64_tr_b16 v[224:225], v240 offset:512\n\t"
"v_mfma_f32_16x16x32_bf16 v[38:41], a[24:27], a[196:199], v[38:41]\n\t"
"ds_read_b64_tr_b16 v[226:227], v240 offset:2048\n\t"
"v_mfma_f32_16x16x32_bf16 v[236:239], v[236:239], v[196:199], 0\n\t"
"ds_read_b64_tr_b16 v[228:229], v241 offset:512\n\t"
"v_mfma_f32_16x16x32_bf16 v[232:235], v[232:235], v[196:199], 0\n\t"
"ds_read_b64_tr_b16 v[230:231], v241 offset:2048\n\t"
"v_mfma_f32_16x16x32_bf16 v[70:73], a[48:51], a[196:199], 0\n\t"
"v_mfma_f32_16x16x32_bf16 v[70:73], a[56:59], a[192:195], v[70:73]\n\t"
"v_mfma_f32_16x16x32_bf16 v[70:73], a[64:67], a[168:171], v[70:73]\n\t"
"v_mfma_f32_16x16x32_bf16 v[104:107], a[72:75], a[172:175], 0\n\t"
"v_cvt_pk_bf16_f32 v236, v236, v237\n\t"
"v_cvt_pk_bf16_f32 v237, v238, v239\n\t"
"v_mfma_f32_16x16x32_bf16 v[104:107], a[80:83], a[176:179], v[104:107]\n\t"
"v_cvt_pk_bf16_f32 v238, v232, v233\n\t"
"v_mfma_f32_16x16x32_bf16 v[104:107], a[88:91], a[180:183], v[104:107]\n\t"
"s_waitcnt lgkmcnt(4)\n\t"
"s_barrier\n\t"
"v_mfma_f32_16x16x32_bf16 v[136:139], a[104:107], a[176:179], 0\n\t"
"ds_read_b128 a[168:171], v244 offset:24576\n\t"
"v_mfma_f32_16x16x32_bf16 v[136:139], a[112:115], a[180:183], v[136:139]\n\t"
"ds_read_b128 a[184:187], v244 offset:33792\n\t"
"v_cvt_pk_bf16_f32 v239, v234, v235\n\t"
"v_mfma_f32_16x16x32_bf16 v[136:139], a[96:99], a[172:175], v[136:139]\n\t"
"ds_write_b128 v242, v[236:239] offset:3072\n\t"
"v_mfma_f32_16x16x32_bf16 v[168:171], a[120:123], a[172:175], 0\n\t"
"s_waitcnt lgkmcnt(5)\n\t"
"v_mfma_f32_16x16x32_bf16 v[168:171], a[136:139], a[180:183], v[168:171]\n\t"
"s_waitcnt lgkmcnt(3)\n\t"
"ds_read_b128 a[180:183], v244 offset:26624\n\t"
"v_mfma_f32_16x16x32_bf16 v[168:171], a[128:131], a[176:179], v[168:171]\n\t"
"s_waitcnt lgkmcnt(3)\n\t"
"ds_read_b128 a[188:191], v244 offset:27136\n\t"
"v_mfma_f32_16x16x32_bf16 v[224:227], v[224:227], v[196:199], 0\n\t"
"ds_read_b128 a[200:203], v244 offset:27648\n\t"
"v_mfma_f32_16x16x32_bf16 v[228:231], v[228:231], v[196:199], 0\n\t"
"v_mfma_f32_16x16x32_bf16 v[2:5], a[0:3], a[168:171], 0\n\t"
"v_mfma_f32_16x16x32_bf16 v[2:5], a[8:11], a[196:199], v[2:5]\n\t"
"v_mfma_f32_16x16x32_bf16 v[2:5], a[16:19], a[192:195], v[2:5]\n\t"
"s_waitcnt vmcnt(0)\n\t"
"s_barrier\n\t"
"v_mfma_f32_16x16x32_bf16 v[34:37], a[32:35], a[196:199], 0\n\t"
"ds_read_b64_tr_b16 v[232:233], v240 offset:39936\n\t"
"v_mfma_f32_16x16x32_bf16 v[34:37], a[40:43], a[192:195], v[34:37]\n\t"
"ds_read_b64_tr_b16 v[234:235], v240 offset:41472\n\t"
"v_cvt_pk_bf16_f32 v224, v224, v225\n\t"
"v_mfma_f32_16x16x32_bf16 v[34:37], a[24:27], a[168:171], v[34:37]\n\t"
"ds_read_b64_tr_b16 v[236:237], v241 offset:39936\n\t"
"v_cvt_pk_bf16_f32 v225, v226, v227\n\t"
"v_mfma_f32_16x16x32_bf16 v[66:69], a[48:51], a[168:171], 0\n\t"
"s_waitcnt lgkmcnt(7)\n\t"
"ds_read_b64_tr_b16 v[238:239], v241 offset:41472\n\t"
"v_cvt_pk_bf16_f32 v226, v228, v229\n\t"
"v_mfma_f32_16x16x32_bf16 v[66:69], a[64:67], a[192:195], v[66:69]\n\t"
"ds_read_b128 a[168:171], v244 offset:35840\n\t"
"v_cvt_pk_bf16_f32 v227, v230, v231\n\t"
"v_mfma_f32_16x16x32_bf16 v[66:69], a[56:59], a[196:199], v[66:69]\n\t"
"s_cmp_lg_u32 s73, 0\n\t"
"s_cbranch_scc1 Lnp_%=\n\t"
"buffer_load_dwordx4 v[248:251], v252, s[8:11], s72 offen sc1\n\t"
"Lnp_%=:\n\t"
"ds_write_b128 v242, v[224:227] offset:4608\n\t"
"v_mfma_f32_16x16x32_bf16 v[100:103], a[72:75], a[184:187], 0\n\t"
"s_waitcnt lgkmcnt(4)\n\t"
"v_mfma_f32_16x16x32_bf16 v[100:103], a[80:83], a[172:175], v[100:103]\n\t"
"ds_read_b128 a[192:195], v244 offset:36352\n\t"
"v_mfma_f32_16x16x32_bf16 v[100:103], a[88:91], a[176:179], v[100:103]\n\t"
"s_waitcnt lgkmcnt(3)\n\t"
"ds_read_b128 a[204:207], v244 offset:36864\n\t"
"v_mfma_f32_16x16x32_bf16 v[232:235], v[232:235], v[196:199], 0\n\t"
"ds_read_b64_tr_b16 v[228:229], v240 offset:20480\n\t"
"v_mfma_f32_16x16x32_bf16 v[236:239], v[236:239], v[196:199], 0\n\t"
"ds_read_b64_tr_b16 v[230:231], v240 offset:22016\n\t"
"v_mfma_f32_16x16x32_bf16 v[132:135], a[112:115], a[176:179], 0\n\t"
"ds_read_b64_tr_b16 v[224:225], v241 offset:20480\n\t"
"v_mfma_f32_16x16x32_bf16 v[132:135], a[96:99], a[184:187], v[132:135]\n\t"
"ds_read_b64_tr_b16 v[226:227], v241 offset:22016\n\t"
"v_mfma_f32_16x16x32_bf16 v[132:135], a[104:107], a[172:175], v[132:135]\n\t"
"v_pk_mul_f32 v[232:233], v[232:233], v[242:243] op_sel:[0,1] op_sel_hi:[1,1]\n\t"
"v_pk_mul_f32 v[234:235], v[234:235], v[242:243] op_sel:[0,1] op_sel_hi:[1,1]\n\t"
"v_mfma_f32_16x16x32_bf16 v[164:167], a[120:123], a[184:187], 0\n\t"
"v_cvt_pk_bf16_f32 v232, v232, v233\n\t"
"v_cvt_pk_bf16_f32 v233, v234, v235\n\t"
"v_mfma_f32_16x16x32_bf16 v[164:167], a[128:131], a[172:175], v[164:167]\n\t"
"v_mfma_f32_16x16x32_bf16 v[164:167], a[136:139], a[176:179], v[164:167]\n\t"
"v_pk_mul_f32 v[236:237], v[236:237], v[242:243] op_sel:[0,1] op_sel_hi:[1,1]\n\t"
"v_pk_mul_f32 v[238:239], v[238:239], v[242:243] op_sel:[0,1] op_sel_hi:[1,1]\n\t"
"v_mfma_f32_16x16x32_bf16 v[18:21], a[0:3], a[180:183], 0\n\t"
"v_cvt_pk_bf16_f32 v234, v236, v237\n\t"
"v_cvt_pk_bf16_f32 v235, v238, v239\n\t"
"v_mfma_f32_16x16x32_bf16 v[18:21], a[8:11], a[188:191], v[18:21]\n\t"
"ds_read_b64_tr_b16 v[236:237], v240 offset:40448\n\t"
"v_mfma_f32_16x16x32_bf16 v[18:21], a[16:19], a[200:203], v[18:21]\n\t"
"ds_write_b128 v242, v[232:235] offset:32256\n\t"
"v_mfma_f32_16x16x32_bf16 v[50:53], a[32:35], a[188:191], 0\n\t"
"s_waitcnt lgkmcnt(2)\n\t"
"v_mfma_f32_16x16x32_bf16 v[50:53], a[40:43], a[200:203], v[50:53]\n\t"
"ds_read_b64_tr_b16 v[238:239], v240 offset:41984\n\t"
"v_mfma_f32_16x16x32_bf16 v[50:53], a[24:27], a[180:183], v[50:53]\n\t"
"ds_read_b64_tr_b16 v[232:233], v241 offset:40448\n\t"
"v_mfma_f32_16x16x32_bf16 v[228:231], v[228:231], v[196:199], 0\n\t"
"Lpg1_%=:\n\t"
"s_cbranch_vccnz Lpg5_%=\n\t"
"ds_read_b64_tr_b16 v[234:235], v241 offset:41984\n\t"
"v_mfma_f32_16x16x32_bf16 v[224:227], v[224:227], v[196:199], 0\n\t"
"v_mfma_f32_16x16x32_bf16 v[82:85], a[48:51], a[180:183], 0\n\t"
"v_mfma_f32_16x16x32_bf16 v[82:85], a[56:59], a[188:191], v[82:85]\n\t"
"v_mfma_f32_16x16x32_bf16 v[82:85], a[64:67], a[200:203], v[82:85]\n\t"
"v_mfma_f32_16x16x32_bf16 v[116:119], a[72:75], a[168:171], 0\n\t"
"v_cvt_pk_bf16_f32 v228, v228, v229\n\t"
"v_cvt_pk_bf16_f32 v229, v230, v231\n\t"
"v_mfma_f32_16x16x32_bf16 v[116:119], a[80:83], a[192:195], v[116:119]\n\t"
"v_cvt_pk_bf16_f32 v230, v224, v225\n\t"
"v_mfma_f32_16x16x32_bf16 v[116:119], a[88:91], a[204:207], v[116:119]\n\t"
"s_waitcnt lgkmcnt(3)\n\t"
"s_barrier\n\t"
"v_mfma_f32_16x16x32_bf16 v[148:151], a[104:107], a[192:195], 0\n\t"
"s_waitcnt lgkmcnt(2)\n\t"
"ds_read_b128 a[184:187], v244 offset:28160\n\t"
"v_mfma_f32_16x16x32_bf16 v[148:151], a[112:115], a[204:207], v[148:151]\n\t"
"s_waitcnt lgkmcnt(1)\n\t"
"ds_read_b128 a[196:199], v244 offset:37376\n\t"
"v_cvt_pk_bf16_f32 v231, v226, v227\n\t"
"v_mfma_f32_16x16x32_bf16 v[148:151], a[96:99], a[168:171], v[148:151]\n\t"
"ds_write_b128 v242, v[228:231] offset:6144\n\t"
"v_mfma_f32_16x16x32_bf16 v[236:239], v[236:239], v[196:199], 0\n\t"
"v_mfma_f32_16x16x32_bf16 v[232:235], v[232:235], v[196:199], 0\n\t"
"ds_read_b128 v[204:207], v244 offset:28672\n\t"
"v_mfma_f32_16x16x32_bf16 v[180:183], a[120:123], a[168:171], 0\n\t"
"s_waitcnt lgkmcnt(3)\n\t"
"v_mfma_f32_16x16x32_bf16 v[180:183], a[128:131], a[192:195], v[180:183]\n\t"
"v_mfma_f32_16x16x32_bf16 v[180:183], a[136:139], a[204:207], v[180:183]\n\t"
"v_mfma_f32_16x16x32_bf16 v[22:25], a[0:3], a[188:191], 0\n\t"
"v_cvt_pk_bf16_f32 v236, v236, v237\n\t"
"v_cvt_pk_bf16_f32 v237, v238, v239\n\t"
"v_mfma_f32_16x16x32_bf16 v[22:25], a[8:11], a[200:203], v[22:25]\n\t"
"v_cvt_pk_bf16_f32 v238, v232, v233\n\t"
"v_cvt_pk_bf16_f32 v239, v234, v235\n\t"
"v_mfma_f32_16x16x32_bf16 v[22:25], a[16:19], a[184:187], v[22:25]\n\t"
"ds_write_b128 v242, v[236:239] offset:7680\n\t"
"v_mfma_f32_16x16x32_bf16 v[54:57], a[32:35], a[200:203], 0\n\t"
"v_mfma_f32_16x16x32_bf16 v[54:57], a[40:43], a[184:187], v[54:57]\n\t"
"ds_read_b128 v[196:199], v244 offset:37888\n\t"
"v_mfma_f32_16x16x32_bf16 v[54:57], a[24:27], a[188:191], v[54:57]\n\t"
"v_mfma_f32_16x16x32_bf16 v[86:89], a[48:51], a[188:191], 0\n\t"
"s_waitcnt lgkmcnt(4)\n\t"
"v_mfma_f32_16x16x32_bf16 v[86:89], a[64:67], a[184:187], v[86:89]\n\t"
"v_mfma_f32_16x16x32_bf16 v[86:89], a[56:59], a[200:203], v[86:89]\n\t"
"v_mfma_f32_16x16x32_bf16 v[120:123], a[72:75], a[192:195], 0\n\t"
"s_mov_b32 s20, 294912\n\t"
"v_mfma_f32_16x16x32_bf16 v[120:123], a[80:83], a[204:207], v[120:123]\n\t"
"v_mfma_f32_16x16x32_bf16 v[120:123], a[88:91], a[196:199], v[120:123]\n\t"
"v_mfma_f32_16x16x32_bf16 v[152:155], a[104:107], a[204:207], 0\n\t"
"ds_read_b128 a[188:191], v244 offset:29184\n\t"
"v_mfma_f32_16x16x32_bf16 v[152:155], a[112:115], a[196:199], v[152:155]\n\t"
"v_mfma_f32_16x16x32_bf16 v[152:155], a[96:99], a[192:195], v[152:155]\n\t"
"s_waitcnt lgkmcnt(2)\n\t"
"s_cmp_lg_u32 s73, 0\n\t"
"s_cbranch_scc1 Lgo_%=\n\t"
"Lchk_%=:\n\t"
"s_waitcnt vmcnt(0)\n\t"
"v_cmpx_ne_u32_e64 s[60:61], v220, v248\n\t"
"v_cmpx_ne_u32_e64 s[60:61], v221, v249\n\t"
"v_cmpx_ne_u32_e64 s[60:61], v222, v250\n\t"
"v_cmpx_ne_u32_e64 s[60:61], v223, v251\n\t"
"s_not_b64 s[60:61], s[60:61]\n\t"
"s_mov_b64 exec, -1\n\t"
"s_lshl_b64 s[60:61], s[60:61], 10\n\t"
"s_cbranch_scc0 Lgo_%=\n\t"
"s_sub_u32 s68, s68, 1\n\t"
"s_cbranch_scc1 Lfb_%=\n\t"
"buffer_load_dwordx4 v[248:251], v252, s[8:11], s72 offen sc1\n\t"
"s_branch Lchk_%=\n\t"
"Lgo_%=:\n\t"
"s_barrier\n\t"
"v_mfma_f32_16x16x32_bf16 v[184:187], a[120:123], a[192:195], 0\n\t"
"s_lshr_b32 s28, s73, 1\n\t"
"s_mul_i32 s29, s28, 1757184\n\t"
"v_mfma_f32_16x16x32_bf16 v[184:187], a[136:139], a[196:199], v[184:187]\n\t"
"buffer_load_dwordx4 a[144:147], off, s[80:83], s20 offset:0 sc1\n\t"
"v_mfma_f32_16x16x32_bf16 v[184:187], a[128:131], a[204:207], v[184:187]\n\t"
"global_load_dwordx4 v[220:223], v252, s[48:49] offset:1024\n\t"
"v_mfma_f32_16x16x32_bf16 v[26:29], a[0:3], a[200:203], 0\n\t"
"buffer_load_dwordx4 a[152:155], off, s[80:83], s20 offset:1024 sc1\n\t"
"v_mfma_f32_16x16x32_bf16 v[26:29], a[8:11], a[184:187], v[26:29]\n\t"
"s_add_u32 s21, s21, s29\n\t"
"s_mul_i32 s29, s73, 3072\n\t"
"v_lshlrev_b32 v245, 4, v245\n\t"
"v_mfma_f32_16x16x32_bf16 v[26:29], a[16:19], v[204:207], v[26:29]\n\t"
"s_add_u32 s21, s21, s29\n\t"
"s_mul_i32 s28, s28, 53760\n\t"
"v_xor_b32 v255, 32, v246\n\t"
"v_mfma_f32_16x16x32_bf16 v[58:61], a[32:35], a[184:187], 0\n\t"
"s_lshl1_add_u32 s24, s24, s28\n\t"
"ds_read_b128 v[212:215], v244 offset:38400\n\t"
"s_sub_u32 s24, s24, 43008\n\t"
"v_mfma_f32_16x16x32_bf16 v[58:61], a[40:43], v[204:207], v[58:61]\n\t"
"buffer_load_dwordx4 a[160:163], off, s[80:83], s20 offset:2048 sc1\n\t"
"v_mfma_f32_16x16x32_bf16 v[58:61], a[24:27], a[200:203], v[58:61]\n\t"
"s_mov_b32 s20, 307200\n\t"
"s_mov_b32 s52, 0x3fd80000\n\t"
"s_mov_b32 s53, s52\n\t"
"v_mfma_f32_16x16x32_bf16 v[90:93], a[48:51], a[200:203], 0\n\t"
"buffer_load_dwordx4 v[200:203], off, s[80:83], s20 offset:0 sc1\n\t"
"v_mfma_f32_16x16x32_bf16 v[90:93], a[64:67], v[204:207], v[90:93]\n\t"
"s_waitcnt lgkmcnt(2)\n\t"
"s_mov_b32 s54, 0x3e97b426\n\t"
"s_mov_b32 s55, s54\n\t"
"v_mfma_f32_16x16x32_bf16 v[90:93], a[56:59], a[184:187], v[90:93]\n\t"
"s_mov_b32 s56, 0x3f400000\n\t"
"s_mov_b32 s57, s56\n\t"
"s_mov_b32 s58, 0x3f2aaaab\n\t"
"v_mfma_f32_16x16x32_bf16 v[124:127], a[72:75], a[204:207], 0\n\t"
"buffer_load_dwordx4 v[208:211], off, s[80:83], s20 offset:1024 sc1\n\t"
"v_mfma_f32_16x16x32_bf16 v[124:127], a[80:83], a[196:199], v[124:127]\n\t"
"s_mov_b32 s59, s58\n\t"
"s_mov_b32 s60, 0x3ee38e39\n\t"
"s_mov_b32 s61, s60\n\t"
"v_mfma_f32_16x16x32_bf16 v[124:127], a[88:91], v[196:199], v[124:127]\n\t"
"s_mov_b32 s62, 0x40100000\n\t"
"s_mov_b32 s63, s62\n\t"
"s_mov_b32 s64, 0x3faaaaab\n\t"
"v_mfma_f32_16x16x32_bf16 v[156:159], a[104:107], a[196:199], 0\n\t"
"buffer_load_dwordx4 v[216:219], off, s[80:83], s20 offset:2048 sc1\n\t"
"v_mfma_f32_16x16x32_bf16 v[156:159], a[112:115], v[196:199], v[156:159]\n\t"
"s_mov_b32 s20, 319488\n\t"
"s_mov_b32 s65, s64\n\t"
"s_mov_b32 s66, 0x3f17b426\n\t"
"v_mfma_f32_16x16x32_bf16 v[156:159], a[96:99], a[204:207], v[156:159]\n\t"
"s_mov_b32 s67, s66\n\t"
"v_mfma_f32_16x16x32_bf16 v[188:191], a[120:123], a[204:207], 0\n\t"
"buffer_load_dwordx4 a[4:7], off, s[80:83], s20 offset:0 sc1\n\t"
"v_mfma_f32_16x16x32_bf16 v[188:191], a[136:139], v[196:199], v[188:191]\n\t"
"s_waitcnt lgkmcnt(1)\n\t"
"v_mfma_f32_16x16x32_bf16 v[188:191], a[128:131], a[196:199], v[188:191]\n\t"
"v_mfma_f32_16x16x32_bf16 v[30:33], a[0:3], a[184:187], 0\n\t"
"buffer_load_dwordx4 a[0:3], off, s[80:83], s20 offset:1024 sc1\n\t"
"v_mfma_f32_16x16x32_bf16 v[30:33], a[8:11], v[204:207], v[30:33]\n\t"
"v_mfma_f32_16x16x32_bf16 v[30:33], a[16:19], a[188:191], v[30:33]\n\t"
"ds_read_b128 a[168:171], v244 offset:0\n\t"
"v_mfma_f32_16x16x32_bf16 v[62:65], a[32:35], v[204:207], 0\n\t"
"buffer_load_dwordx4 a[8:11], off, s[80:83], s20 offset:2048 sc1\n\t"
"v_mfma_f32_16x16x32_bf16 v[62:65], a[40:43], a[188:191], v[62:65]\n\t"
"s_mov_b32 s20, 479232\n\t"
"ds_read_b128 a[172:175], v244 offset:512\n\t"
"v_mfma_f32_16x16x32_bf16 v[62:65], a[24:27], a[184:187], v[62:65]\n\t"
"ds_read_b128 a[176:179], v244 offset:1024\n\t"
"v_mfma_f32_16x16x32_bf16 v[94:97], a[48:51], a[184:187], 0\n\t"
"buffer_load_dwordx4 a[16:19], off, s[80:83], s20 offset:0 sc1\n\t"
"v_mfma_f32_16x16x32_bf16 v[94:97], a[64:67], a[188:191], v[94:97]\n\t"
"s_waitcnt lgkmcnt(3)\n\t"
"ds_read_b64_tr_b16 v[224:225], v246 offset:0\n\t"
"v_mfma_f32_16x16x32_bf16 v[94:97], a[56:59], v[204:207], v[94:97]\n\t"
"ds_read_b64_tr_b16 v[228:229], v255 offset:0\n\t"
"v_mfma_f32_16x16x32_bf16 v[128:131], a[72:75], a[196:199], 0\n\t"
"ds_read_b128 a[180:183], v244 offset:1536\n\t"
"v_mfma_f32_16x16x32_bf16 v[128:131], a[80:83], v[196:199], v[128:131]\n\t"
"ds_read_b64_tr_b16 v[232:233], v246 offset:6144\n\t"
"v_mfma_f32_16x16x32_bf16 v[128:131], a[88:91], v[212:215], v[128:131]\n\t"
"buffer_load_dwordx4 v[204:207], off, s[80:83], s20 offset:1024 sc1\n\t"
"v_mfma_f32_16x16x32_bf16 v[160:163], a[104:107], v[196:199], 0\n\t"
"ds_read_b64_tr_b16 v[236:237], v255 offset:6144\n\t"
"v_mfma_f32_16x16x32_bf16 v[160:163], a[112:115], v[212:215], v[160:163]\n\t"
"ds_read_b128 a[184:187], v244 offset:2048\n\t"
"v_mfma_f32_16x16x32_bf16 v[160:163], a[96:99], a[196:199], v[160:163]\n\t"
"buffer_load_dwordx4 a[24:27], off, s[80:83], s20 offset:2048 sc1\n\t"
"s_mov_b32 s20, 491520\n\t"
"v_mfma_f32_16x16x32_bf16 v[192:195], a[120:123], a[196:199], 0\n\t"
"s_waitcnt vmcnt(11) lgkmcnt(5)\n\t"
"ds_read_b128 a[188:191], v244 offset:2560\n\t"
"v_mfma_f32_16x16x32_bf16 v[192:195], a[136:139], v[212:215], v[192:195]\n\t"
"buffer_load_dwordx4 a[32:35], off, s[80:83], s20 offset:0 sc1\n\t"
"v_mfma_f32_16x16x32_bf16 v[192:195], a[128:131], v[196:199], v[192:195]\n\t"
"s_waitcnt lgkmcnt(3)\n\t"
"ds_read_b128 a[192:195], v244 offset:3072\n\t"
"v_mfma_f32_4x4x4_16b_bf16 v[224:227], v[224:225], v[220:221], 0\n\t"
"s_waitcnt lgkmcnt(3)\n\t"
"v_mfma_f32_4x4x4_16b_bf16 v[228:231], v[228:229], v[220:221], 0\n\t"
"s_waitcnt vmcnt(10)\n\t"
"v_mfma_f32_4x4x4_16b_bf16 v[232:235], v[232:233], v[220:221], 0\n\t"
"v_mfma_f32_4x4x4_16b_bf16 v[236:239], v[236:237], v[220:221], 0\n\t"
"buffer_load_dwordx4 a[40:43], off, s[80:83], s20 offset:1024 sc1\n\t"
"Lst1_%=:\n\t"
"v_mfma_f32_16x16x32_bf16 v[2:5], a[144:147], a[168:171], v[2:5]\n\t"
"ds_read_b128 a[196:199], v244 offset:3584\n\t"
"v_cvt_pk_bf16_f32 v224, v224, v225\n\t"
"v_mfma_f32_16x16x32_bf16 v[2:5], a[160:163], a[176:179], v[2:5]\n\t"
"ds_read_b128 a[200:203], v244 offset:4096\n\t"
"v_cvt_pk_bf16_f32 v225, v226, v227\n\t"
"v_mfma_f32_16x16x32_bf16 v[2:5], a[152:155], a[172:175], v[2:5]\n\t"
"buffer_load_dwordx4 a[48:51], off, s[80:83], s20 offset:2048 sc1\n\t"
"s_mov_b32 s20, 503808\n\t"
"v_mfma_f32_16x16x32_bf16 v[6:9], a[144:147], a[172:175], v[6:9]\n\t"
"buffer_load_dwordx4 a[80:83], off, s[80:83], s20 offset:0 sc1\n\t"
"v_mfma_f32_16x16x32_bf16 v[6:9], a[152:155], a[176:179], v[6:9]\n\t"
"s_waitcnt lgkmcnt(4)\n\t"
"ds_read_b128 a[204:207], v244 offset:4608\n\t"
"v_cvt_pk_bf16_f32 v226, v228, v229\n\t"
"v_mfma_f32_16x16x32_bf16 v[6:9], a[160:163], a[180:183], v[6:9]\n\t"
"v_cvt_pk_bf16_f32 v227, v230, v231\n\t"
"v_cvt_pk_bf16_f32 v232, v232, v233\n\t"
"v_cvt_pk_bf16_f32 v233, v234, v235\n\t"
"v_cvt_pk_bf16_f32 v234, v236, v237\n\t"
"v_cvt_pk_bf16_f32 v235, v238, v239\n\t"
"v_mfma_f32_16x16x32_bf16 v[10:13], a[160:163], a[184:187], v[10:13]\n\t"
"s_waitcnt lgkmcnt(4)\n\t"
"v_mfma_f32_16x16x32_bf16 v[10:13], a[152:155], a[180:183], v[10:13]\n\t"
"buffer_load_dwordx4 a[88:91], off, s[80:83], s20 offset:1024 sc1\n\t"
"v_mfma_f32_16x16x32_bf16 v[10:13], a[144:147], a[176:179], v[10:13]\n\t"
"ds_write_b128 v245, v[224:227] offset:0\n\t"
"v_mfma_f32_16x16x32_bf16 v[14:17], a[144:147], a[180:183], v[14:17]\n\t"
"buffer_load_dwordx4 a[96:99], off, s[80:83], s20 offset:2048 sc1\n\t"
"v_mfma_f32_16x16x32_bf16 v[14:17], a[152:155], a[184:187], v[14:17]\n\t"
"s_waitcnt lgkmcnt(4)\n\t"
"ds_read_b64_tr_b16 v[228:229], v246 offset:12288\n\t"
"v_mfma_f32_16x16x32_bf16 v[14:17], a[160:163], a[188:191], v[14:17]\n\t"
"s_mov_b32 s20, 110592\n\t"
"ds_write_b128 v245, v[232:235] offset:6144\n\t"
"v_mfma_f32_16x16x32_bf16 v[18:21], a[160:163], a[192:195], v[18:21]\n\t"
"buffer_load_dwordx4 a[12:15], off, s[80:83], s20 offset:0 sc1\n\t"
"v_mfma_f32_16x16x32_bf16 v[18:21], a[152:155], a[188:191], v[18:21]\n\t"
"s_waitcnt lgkmcnt(5)\n\t"
"ds_read_b64_tr_b16 v[236:237], v255 offset:12288\n\t"
"v_mfma_f32_16x16x32_bf16 v[18:21], a[144:147], a[184:187], v[18:21]\n\t"
"buffer_load_dwordx4 a[20:23], off, s[80:83], s20 offset:1024 sc1\n\t"
"v_mfma_f32_16x16x32_bf16 v[22:25], a[144:147], a[188:191], v[22:25]\n\t"
"s_waitcnt lgkmcnt(5)\n\t"
"ds_read_b64_tr_b16 v[224:225], v246 offset:19968\n\t"
"v_mfma_f32_16x16x32_bf16 v[22:25], a[152:155], a[192:195], v[22:25]\n\t"
"ds_read_b64_tr_b16 v[232:233], v255 offset:19968\n\t"
"v_mfma_f32_16x16x32_bf16 v[22:25], a[160:163], a[196:199], v[22:25]\n\t"
"buffer_load_dwordx4 a[28:31], off, s[80:83], s20 offset:2048 sc1\n\t"
"v_mfma_f32_16x16x32_bf16 v[26:29], a[160:163], a[200:203], v[26:29]\n\t"
"s_waitcnt lgkmcnt(6)\n\t"
"s_mov_b32 s20, 122880\n\t"
"v_mfma_f32_16x16x32_bf16 v[26:29], a[152:155], a[196:199], v[26:29]\n\t"
"v_mfma_f32_16x16x32_bf16 v[26:29], a[144:147], a[192:195], v[26:29]\n\t"
"buffer_load_dwordx4 a[36:39], off, s[80:83], s20 offset:0 sc1\n\t"
"v_mfma_f32_16x16x32_bf16 v[30:33], a[144:147], a[196:199], v[30:33]\n\t"
"s_waitcnt lgkmcnt(4)\n\t"
"v_mfma_f32_16x16x32_bf16 v[30:33], a[152:155], a[200:203], v[30:33]\n\t"
"s_waitcnt lgkmcnt(2)\n\t"
"v_mfma_f32_16x16x32_bf16 v[30:33], a[160:163], a[204:207], v[30:33]\n\t"
"buffer_load_dwordx4 a[44:47], off, s[80:83], s20 offset:1024 sc1\n\t"
"v_mfma_f32_4x4x4_16b_bf16 v[228:231], v[228:229], v[220:221], 0\n\t"
"s_waitcnt lgkmcnt(0)\n\t"
"v_mfma_f32_4x4x4_16b_bf16 v[236:239], v[236:237], v[220:221], 0\n\t"
"s_waitcnt vmcnt(17)\n\t"
"v_mfma_f32_4x4x4_16b_bf16 v[224:227], v[224:225], v[220:221], 0\n\t"
"v_mfma_f32_4x4x4_16b_bf16 v[232:235], v[232:233], v[220:221], 0\n\t"
"v_mfma_f32_16x16x32_bf16 v[34:37], v[200:203], a[168:171], v[34:37]\n\t"
"buffer_load_dwordx4 a[52:55], off, s[80:83], s20 offset:2048 sc1\n\t"
"v_mfma_f32_16x16x32_bf16 v[34:37], v[216:219], a[176:179], v[34:37]\n\t"
"s_mov_b32 s20, 135168\n\t"
"v_cvt_pk_bf16_f32 v228, v228, v229\n\t"
"v_cvt_pk_bf16_f32 v229, v230, v231\n\t"
"v_mfma_f32_16x16x32_bf16 v[34:37], v[208:211], a[172:175], v[34:37]\n\t"
"v_cvt_pk_bf16_f32 v230, v236, v237\n\t"
"v_cvt_pk_bf16_f32 v231, v238, v239\n\t"
"v_mfma_f32_16x16x32_bf16 v[38:41], v[200:203], a[172:175], v[38:41]\n\t"
"v_cvt_pk_bf16_f32 v224, v224, v225\n\t"
"v_cvt_pk_bf16_f32 v225, v226, v227\n\t"
"v_mfma_f32_16x16x32_bf16 v[38:41], v[208:211], a[176:179], v[38:41]\n\t"
"v_cvt_pk_bf16_f32 v226, v232, v233\n\t"
"v_cvt_pk_bf16_f32 v227, v234, v235\n\t"
"v_mfma_f32_16x16x32_bf16 v[38:41], v[216:219], a[180:183], v[38:41]\n\t"
"buffer_load_dwordx4 a[84:87], off, s[80:83], s20 offset:0 sc1\n\t"
"v_mfma_f32_16x16x32_bf16 v[42:45], v[216:219], a[184:187], v[42:45]\n\t"
"buffer_load_dwordx4 a[92:95], off, s[80:83], s20 offset:1024 sc1\n\t"
"v_mfma_f32_16x16x32_bf16 v[42:45], v[208:211], a[180:183], v[42:45]\n\t"
"buffer_load_dwordx4 a[100:103], off, s[80:83], s20 offset:2048 sc1\n\t"
"v_mfma_f32_16x16x32_bf16 v[42:45], v[200:203], a[176:179], v[42:45]\n\t"
"s_mov_b32 s20, 147456\n\t"
"ds_write_b128 v245, v[228:231] offset:12288\n\t"
"v_mfma_f32_16x16x32_bf16 v[46:49], v[200:203], a[180:183], v[46:49]\n\t"
"v_mfma_f32_16x16x32_bf16 v[46:49], v[208:211], a[184:187], v[46:49]\n\t"
"buffer_load_dwordx4 a[140:143], off, s[80:83], s20 offset:0 sc1\n\t"
"v_mfma_f32_16x16x32_bf16 v[46:49], v[216:219], a[188:191], v[46:49]\n\t"
"ds_write_b128 v245, v[224:227] offset:19968\n\t"
"v_mfma_f32_16x16x32_bf16 v[50:53], v[216:219], a[192:195], v[50:53]\n\t"
"v_mfma_f32_16x16x32_bf16 v[50:53], v[208:211], a[188:191], v[50:53]\n\t"
"ds_read_b64_tr_b16 v[236:237], v246 offset:26112\n\t"
"v_mfma_f32_16x16x32_bf16 v[50:53], v[200:203], a[184:187], v[50:53]\n\t"
"ds_read_b64_tr_b16 v[232:233], v255 offset:26112\n\t"
"v_mfma_f32_16x16x32_bf16 v[54:57], v[200:203], a[188:191], v[54:57]\n\t"
"buffer_load_dwordx4 a[148:151], off, s[80:83], s20 offset:1024 sc1\n\t"
"v_mfma_f32_16x16x32_bf16 v[54:57], v[208:211], a[192:195], v[54:57]\n\t"
"ds_read_b64_tr_b16 v[228:229], v246 offset:32256\n\t"
"v_mfma_f32_16x16x32_bf16 v[54:57], v[216:219], a[196:199], v[54:57]\n\t"
"ds_read_b64_tr_b16 v[224:225], v255 offset:32256\n\t"
"v_mfma_f32_16x16x32_bf16 v[58:61], v[216:219], a[200:203], v[58:61]\n\t"
"buffer_load_dwordx4 a[156:159], off, s[80:83], s20 offset:2048 sc1\n\t"
"v_mfma_f32_16x16x32_bf16 v[58:61], v[208:211], a[196:199], v[58:61]\n\t"
"s_mov_b32 s20, 159744\n\t"
"v_mfma_f32_16x16x32_bf16 v[58:61], v[200:203], a[192:195], v[58:61]\n\t"
"v_mfma_f32_16x16x32_bf16 v[62:65], v[200:203], a[196:199], v[62:65]\n\t"
"s_waitcnt lgkmcnt(3)\n\t"
"v_mfma_f32_16x16x32_bf16 v[62:65], v[208:211], a[200:203], v[62:65]\n\t"
"buffer_load_dwordx4 v[240:243], off, s[80:83], s20 offset:0 sc1\n\t"
"v_mfma_f32_16x16x32_bf16 v[62:65], v[216:219], a[204:207], v[62:65]\n\t"
"s_waitcnt lgkmcnt(1)\n\t"
"v_mfma_f32_4x4x4_16b_bf16 v[236:239], v[236:237], v[220:221], 0\n\t"
"s_waitcnt lgkmcnt(0)\n\t"
"v_mfma_f32_4x4x4_16b_bf16 v[232:235], v[232:233], v[220:221], 0\n\t"
"s_waitcnt vmcnt(22)\n\t"
"v_mfma_f32_4x4x4_16b_bf16 v[228:231], v[228:229], v[220:221], 0\n\t"
"v_mfma_f32_4x4x4_16b_bf16 v[224:227], v[224:225], v[220:221], 0\n\t"
"v_mfma_f32_16x16x32_bf16 v[66:69], a[4:7], a[168:171], v[66:69]\n\t"
"buffer_load_dwordx4 a[208:211], off, s[80:83], s20 offset:1024 sc1\n\t"
"v_mfma_f32_16x16x32_bf16 v[66:69], a[8:11], a[176:179], v[66:69]\n\t"
"ds_read_b128 a[168:171], v244 offset:9216\n\t"
"v_cvt_pk_bf16_f32 v236, v236, v237\n\t"
"v_mfma_f32_16x16x32_bf16 v[66:69], a[0:3], a[172:175], v[66:69]\n\t"
"v_cvt_pk_bf16_f32 v237, v238, v239\n\t"
"v_cvt_pk_bf16_f32 v238, v232, v233\n\t"
"v_mfma_f32_16x16x32_bf16 v[70:73], a[4:7], a[172:175], v[70:73]\n\t"
"ds_read_b128 a[172:175], v244 offset:9728\n\t"
"v_cvt_pk_bf16_f32 v239, v234, v235\n\t"
"v_mfma_f32_16x16x32_bf16 v[70:73], a[0:3], a[176:179], v[70:73]\n\t"
"v_cvt_pk_bf16_f32 v228, v228, v229\n\t"
"v_cvt_pk_bf16_f32 v229, v230, v231\n\t"
"v_mfma_f32_16x16x32_bf16 v[70:73], a[8:11], a[180:183], v[70:73]\n\t"
"buffer_load_dwordx4 v[248:251], off, s[80:83], s20 offset:2048 sc1\n\t"
"v_cvt_pk_bf16_f32 v230, v224, v225\n\t"
"v_cvt_pk_bf16_f32 v231, v226, v227\n\t"
"s_mov_b32 s20, 172032\n\t"
"v_mfma_f32_16x16x32_bf16 v[74:77], a[8:11], a[184:187], v[74:77]\n\t"
"buffer_load_dwordx4 a[212:215], off, s[80:83], s20 offset:0 sc1\n\t"
"v_mfma_f32_16x16x32_bf16 v[74:77], a[0:3], a[180:183], v[74:77]\n\t"
"v_mfma_f32_16x16x32_bf16 v[74:77], a[4:7], a[176:179], v[74:77]\n\t"
"ds_write_b128 v245, v[236:239] offset:26112\n\t"
"v_mfma_f32_16x16x32_bf16 v[78:81], a[4:7], a[180:183], v[78:81]\n\t"
"v_mfma_f32_16x16x32_bf16 v[78:81], a[0:3], a[184:187], v[78:81]\n\t"
"buffer_load_dwordx4 a[216:219], off, s[80:83], s20 offset:1024 sc1\n\t"
"v_mfma_f32_16x16x32_bf16 v[78:81], a[8:11], a[188:191], v[78:81]\n\t"
"ds_write_b128 v245, v[228:231] offset:32256\n\t"
"v_mfma_f32_16x16x32_bf16 v[82:85], a[8:11], a[192:195], v[82:85]\n\t"
"v_mfma_f32_16x16x32_bf16 v[82:85], a[0:3], a[188:191], v[82:85]\n\t"
"ds_read_b128 a[176:179], v244 offset:10240\n\t"
"v_mfma_f32_16x16x32_bf16 v[82:85], a[4:7], a[184:187], v[82:85]\n\t"
"ds_read_b64_tr_b16 v[232:233], v246 offset:39936\n\t"
"v_mfma_f32_16x16x32_bf16 v[86:89], a[4:7], a[188:191], v[86:89]\n\t"
"ds_read_b64_tr_b16 v[224:225], v255 offset:39936\n\t"
"v_mfma_f32_16x16x32_bf16 v[86:89], a[0:3], a[192:195], v[86:89]\n\t"
"ds_read_b128 a[180:183], v244 offset:10752\n\t"
"v_mfma_f32_16x16x32_bf16 v[86:89], a[8:11], a[196:199], v[86:89]\n\t"
"buffer_load_dwordx4 a[220:223], off, s[80:83], s20 offset:2048 sc1\n\t"
"s_mov_b32 s20, 36864\n\t"
"v_mfma_f32_16x16x32_bf16 v[90:93], a[8:11], a[200:203], v[90:93]\n\t"
"ds_read_b64_tr_b16 v[236:237], v246 offset:46080\n\t"
"v_mfma_f32_16x16x32_bf16 v[90:93], a[0:3], a[196:199], v[90:93]\n\t"
"ds_read_b64_tr_b16 v[228:229], v255 offset:46080\n\t"
"v_mfma_f32_16x16x32_bf16 v[90:93], a[4:7], a[192:195], v[90:93]\n\t"
"ds_read_b128 a[184:187], v244 offset:11264\n\t"
"v_mfma_f32_16x16x32_bf16 v[94:97], a[4:7], a[196:199], v[94:97]\n\t"
"buffer_load_dwordx4 a[224:227], off, s[80:83], s20 offset:0 sc1\n\t"
"v_mfma_f32_16x16x32_bf16 v[94:97], a[0:3], a[200:203], v[94:97]\n\t"
"s_waitcnt lgkmcnt(4)\n\t"
"ds_read_b128 a[188:191], v244 offset:11776\n\t"
"v_mfma_f32_16x16x32_bf16 v[94:97], a[8:11], a[204:207], v[94:97]\n\t"
"buffer_load_dwordx4 a[228:231], off, s[80:83], s20 offset:1024 sc1\n\t"
"v_mfma_f32_4x4x4_16b_bf16 v[232:235], v[232:233], v[220:221], 0\n\t"
"s_waitcnt lgkmcnt(2)\n\t"
"v_mfma_f32_4x4x4_16b_bf16 v[224:227], v[224:225], v[220:221], 0\n\t"
"s_waitcnt vmcnt(26)\n\t"
"v_mfma_f32_4x4x4_16b_bf16 v[236:239], v[236:237], v[220:221], 0\n\t"
"ds_read_b128 a[192:195], v244 offset:12288\n\t"
"v_mfma_f32_4x4x4_16b_bf16 v[228:231], v[228:229], v[220:221], 0\n\t"
"v_mfma_f32_16x16x32_bf16 v[100:103], a[16:19], a[168:171], v[100:103]\n\t"
"ds_read_b128 a[196:199], v244 offset:12800\n\t"
"v_cvt_pk_bf16_f32 v232, v232, v233\n\t"
"v_mfma_f32_16x16x32_bf16 v[100:103], a[24:27], a[176:179], v[100:103]\n\t"
"ds_read_b128 a[200:203], v244 offset:13312\n\t"
"v_cvt_pk_bf16_f32 v233, v234, v235\n\t"
"v_mfma_f32_16x16x32_bf16 v[100:103], v[204:207], a[172:175], v[100:103]\n\t"
"buffer_load_dwordx4 a[232:235], off, s[80:83], s20 offset:2048 sc1\n\t"
"s_mov_b32 s20, 49152\n\t"
"v_mfma_f32_16x16x32_bf16 v[104:107], a[16:19], a[172:175], v[104:107]\n\t"
"buffer_load_dwordx4 a[236:239], off, s[80:83], s20 offset:0 sc1\n\t"
"v_mfma_f32_16x16x32_bf16 v[104:107], v[204:207], a[176:179], v[104:107]\n\t"
"s_waitcnt lgkmcnt(4)\n\t"
"ds_read_b128 a[204:207], v244 offset:13824\n\t"
"v_cvt_pk_bf16_f32 v234, v224, v225\n\t"
"v_mfma_f32_16x16x32_bf16 v[104:107], a[24:27], a[180:183], v[104:107]\n\t"
"v_cvt_pk_bf16_f32 v235, v226, v227\n\t"
"v_cvt_pk_bf16_f32 v236, v236, v237\n\t"
"v_cvt_pk_bf16_f32 v237, v238, v239\n\t"
"v_cvt_pk_bf16_f32 v238, v228, v229\n\t"
"v_cvt_pk_bf16_f32 v239, v230, v231\n\t"
"v_mfma_f32_16x16x32_bf16 v[108:111], a[24:27], a[184:187], v[108:111]\n\t"
"s_waitcnt lgkmcnt(4)\n\t"
"v_fma_f32 v196, -v2, s56, -v100\n\t"
"v_fma_f32 v197, -v3, s56, -v101\n\t"
"v_mfma_f32_16x16x32_bf16 v[108:111], v[204:207], a[180:183], v[108:111]\n\t"
"v_fma_f32 v198, -v4, s56, -v102\n\t"
"v_fma_f32 v199, -v5, s56, -v103\n\t"
"v_mfma_f32_16x16x32_bf16 v[108:111], a[16:19], a[176:179], v[108:111]\n\t"
"ds_write_b128 v245, v[232:235] offset:39936\n\t"
"v_mfma_f32_16x16x32_bf16 v[112:115], a[16:19], a[180:183], v[112:115]\n\t"
"buffer_load_dwordx4 a[240:243], off, s[80:83], s20 offset:1024 sc1\n\t"
"v_mfma_f32_16x16x32_bf16 v[112:115], v[204:207], a[184:187], v[112:115]\n\t"
"buffer_load_dwordx4 a[244:247], off, s[80:83], s20 offset:2048 sc1\n\t"
"v_mfma_f32_16x16x32_bf16 v[112:115], a[24:27], a[188:191], v[112:115]\n\t"
"s_waitcnt lgkmcnt(4)\n\t"
"ds_write_b128 v245, v[236:239] offset:46080\n\t"
"s_mov_b32 s20, 61440\n\t"
"v_mfma_f32_16x16x32_bf16 v[116:119], a[24:27], a[192:195], v[116:119]\n\t"
"buffer_load_dwordx4 a[248:251], off, s[80:83], s20 offset:0 sc1\n\t"
"v_mfma_f32_16x16x32_bf16 v[116:119], v[204:207], a[188:191], v[116:119]\n\t"
"s_waitcnt lgkmcnt(4)\n\t"
"ds_read_b64_tr_b16 v[224:225], v246 offset:52224\n\t"
"v_fma_f32 v212, -v10, s56, -v108\n\t"
"v_mfma_f32_16x16x32_bf16 v[116:119], a[16:19], a[184:187], v[116:119]\n\t"
"ds_read_b64_tr_b16 v[228:229], v255 offset:52224\n\t"
"v_fma_f32 v213, -v11, s56, -v109\n\t"
"v_mfma_f32_16x16x32_bf16 v[120:123], a[16:19], a[188:191], v[120:123]\n\t"
"s_waitcnt lgkmcnt(5)\n\t"
"v_fma_f32 v214, -v12, s56, -v110\n\t"
"v_fma_f32 v215, -v13, s56, -v111\n\t"
"v_mfma_f32_16x16x32_bf16 v[120:123], v[204:207], a[192:195], v[120:123]\n\t"
"v_fma_f32 v98, -v14, s56, -v112\n\t"
"v_fma_f32 v99, -v15, s56, -v113\n\t"
"v_mfma_f32_16x16x32_bf16 v[120:123], a[24:27], a[196:199], v[120:123]\n\t"
"buffer_load_dwordx4 a[252:255], off, s[80:83], s20 offset:1024 sc1\n\t"
"v_mfma_f32_16x16x32_bf16 v[124:127], a[24:27], a[200:203], v[124:127]\n\t"
"s_waitcnt lgkmcnt(4)\n\t"
"v_accvgpr_write_b32 a0, v98\n\t"
"v_fma_f32 v98, -v16, s56, -v114\n\t"
"v_mfma_f32_16x16x32_bf16 v[124:127], v[204:207], a[196:199], v[124:127]\n\t"
"v_accvgpr_write_b32 a1, v99\n\t"
"v_fma_f32 v99, -v17, s56, -v115\n\t"
"v_mfma_f32_16x16x32_bf16 v[124:127], a[16:19], a[192:195], v[124:127]\n\t"
"v_accvgpr_write_b32 a2, v98\n\t"
"v_accvgpr_write_b32 a3, v99\n\t"
"v_mfma_f32_16x16x32_bf16 v[128:131], a[16:19], a[196:199], v[128:131]\n\t"
"s_waitcnt lgkmcnt(1)\n\t"
"v_fma_f32 v98, -v18, s56, -v116\n\t"
"v_fma_f32 v99, -v19, s56, -v117\n\t"
"v_mfma_f32_16x16x32_bf16 v[128:131], v[204:207], a[200:203], v[128:131]\n\t"
"s_waitcnt lgkmcnt(0)\n\t"
"v_fma_f32 v204, -v6, s56, -v104\n\t"
"v_fma_f32 v205, -v7, s56, -v105\n\t"
"v_mfma_f32_16x16x32_bf16 v[128:131], a[24:27], a[204:207], v[128:131]\n\t"
"s_waitcnt vmcnt(29)\n\t"
"v_fma_f32 v206, -v8, s56, -v106\n\t"
"v_fma_f32 v207, -v9, s56, -v107\n\t"
"v_mfma_f32_4x4x4_16b_bf16 v[224:227], v[224:225], v[220:221], 0\n\t"
"v_mfma_f32_4x4x4_16b_bf16 v[228:231], v[228:229], v[220:221], 0\n\t"
"v_mfma_f32_16x16x32_bf16 v[132:135], a[32:35], a[168:171], v[132:135]\n\t"
"v_accvgpr_write_b32 a8, v98\n\t"
"v_fma_f32 v98, -v20, s56, -v118\n\t"
"v_mfma_f32_16x16x32_bf16 v[132:135], a[48:51], a[176:179], v[132:135]\n\t"
"v_cvt_pk_bf16_f32 v224, v224, v225\n\t"
"v_cvt_pk_bf16_f32 v225, v226, v227\n\t"
"v_mfma_f32_16x16x32_bf16 v[132:135], a[40:43], a[172:175], v[132:135]\n\t"
"v_cvt_pk_bf16_f32 v226, v228, v229\n\t"
"v_cvt_pk_bf16_f32 v227, v230, v231\n\t"
"v_mfma_f32_16x16x32_bf16 v[136:139], a[32:35], a[172:175], v[136:139]\n\t"
"v_accvgpr_write_b32 a9, v99\n\t"
"v_fma_f32 v99, -v21, s56, -v119\n\t"
"v_mfma_f32_16x16x32_bf16 v[136:139], a[40:43], a[176:179], v[136:139]\n\t"
"v_accvgpr_write_b32 a10, v98\n\t"
"v_accvgpr_write_b32 a11, v99\n\t"
"v_mfma_f32_16x16x32_bf16 v[136:139], a[48:51], a[180:183], v[136:139]\n\t"
"ds_write_b128 v245, v[224:227] offset:52224\n\t"
"v_mfma_f32_16x16x32_bf16 v[140:143], a[48:51], a[184:187], v[140:143]\n\t"
"v_fma_f32 v98, -v22, s56, -v120\n\t"
"v_fma_f32 v99, -v23, s56, -v121\n\t"
"v_mfma_f32_16x16x32_bf16 v[140:143], a[40:43], a[180:183], v[140:143]\n\t"
"v_accvgpr_write_b32 a16, v98\n\t"
"v_fma_f32 v98, -v24, s56, -v122\n\t"
"v_mfma_f32_16x16x32_bf16 v[140:143], a[32:35], a[176:179], v[140:143]\n\t"
"v_accvgpr_write_b32 a17, v99\n\t"
"v_fma_f32 v99, -v25, s56, -v123\n\t"
"v_mfma_f32_16x16x32_bf16 v[144:147], a[32:35], a[180:183], v[144:147]\n\t"
"v_accvgpr_write_b32 a18, v98\n\t"
"v_accvgpr_write_b32 a19, v99\n\t"
"v_mfma_f32_16x16x32_bf16 v[144:147], a[40:43], a[184:187], v[144:147]\n\t"
"v_fma_f32 v98, -v26, s56, -v124\n\t"
"v_fma_f32 v99, -v27, s56, -v125\n\t"
"v_mfma_f32_16x16x32_bf16 v[144:147], a[48:51], a[188:191], v[144:147]\n\t"
"v_accvgpr_write_b32 a24, v98\n\t"
"v_fma_f32 v98, -v28, s56, -v126\n\t"
"v_mfma_f32_16x16x32_bf16 v[148:151], a[48:51], a[192:195], v[148:151]\n\t"
"v_accvgpr_write_b32 a25, v99\n\t"
"v_fma_f32 v99, -v29, s56, -v127\n\t"
"v_mfma_f32_16x16x32_bf16 v[148:151], a[40:43], a[188:191], v[148:151]\n\t"
"v_accvgpr_write_b32 a26, v98\n\t"
"v_accvgpr_write_b32 a27, v99\n\t"
"v_mfma_f32_16x16x32_bf16 v[148:151], a[32:35], a[184:187], v[148:151]\n\t"
"v_fma_f32 v200, v100, s64, -v2\n\t"
"v_fma_f32 v201, v101, s64, -v3\n\t"
"v_mfma_f32_16x16x32_bf16 v[152:155], a[32:35], a[188:191], v[152:155]\n\t"
"v_fma_f32 v202, v102, s64, -v4\n\t"
"v_fma_f32 v203, v103, s64, -v5\n\t"
"v_mfma_f32_16x16x32_bf16 v[152:155], a[40:43], a[192:195], v[152:155]\n\t"
"v_fma_f32 v208, v104, s64, -v6\n\t"
"v_fma_f32 v209, v105, s64, -v7\n\t"
"v_mfma_f32_16x16x32_bf16 v[152:155], a[48:51], a[196:199], v[152:155]\n\t"
"v_fma_f32 v210, v106, s64, -v8\n\t"
"v_fma_f32 v211, v107, s64, -v9\n\t"
"v_mfma_f32_16x16x32_bf16 v[156:159], a[48:51], a[200:203], v[156:159]\n\t"
"v_fma_f32 v216, v108, s64, -v10\n\t"
"v_fma_f32 v217, v109, s64, -v11\n\t"
"v_mfma_f32_16x16x32_bf16 v[156:159], a[40:43], a[196:199], v[156:159]\n\t"
"v_fma_f32 v218, v110, s64, -v12\n\t"
"v_fma_f32 v219, v111, s64, -v13\n\t"
"v_mfma_f32_16x16x32_bf16 v[156:159], a[32:35], a[192:195], v[156:159]\n\t"
"v_fma_f32 v0, v2, s52, v100\n\t"
"v_fma_f32 v1, v3, s52, v101\n\t"
"v_mfma_f32_16x16x32_bf16 v[160:163], a[32:35], a[196:199], v[160:163]\n\t"
"s_waitcnt vmcnt(26)\n\t"
"v_fma_f32 v98, -v30, s56, -v128\n\t"
"v_fma_f32 v99, -v31, s56, -v129\n\t"
"v_mfma_f32_16x16x32_bf16 v[160:163], a[40:43], a[200:203], v[160:163]\n\t"
"v_accvgpr_write_b32 a32, v98\n\t"
"v_fma_f32 v98, -v32, s56, -v130\n\t"
"v_mfma_f32_16x16x32_bf16 v[160:163], a[48:51], a[204:207], v[160:163]\n\t"
"v_accvgpr_write_b32 a33, v99\n\t"
"v_fma_f32 v99, -v33, s56, -v131\n\t"
"v_mfma_f32_16x16x32_bf16 v[164:167], a[80:83], a[168:171], v[164:167]\n\t"
"v_accvgpr_write_b32 a34, v98\n\t"
"v_accvgpr_write_b32 a35, v99\n\t"
"v_mfma_f32_16x16x32_bf16 v[164:167], a[96:99], a[176:179], v[164:167]\n\t"
"v_fma_f32 v98, -v34, s56, -v132\n\t"
"v_mfma_f32_16x16x32_bf16 v[164:167], a[88:91], a[172:175], v[164:167]\n\t"
"s_waitcnt lgkmcnt(0)\n\t"
"s_barrier\n\t"
"v_mfma_f32_16x16x32_bf16 v[168:171], a[80:83], a[172:175], v[168:171]\n\t"
"ds_read_b128 a[168:171], v222 offset:0\n\t"
"v_mfma_f32_16x16x32_bf16 v[168:171], a[88:91], a[176:179], v[168:171]\n\t"
"ds_read_b128 a[172:175], v223 offset:6144\n\t"
"v_fma_f32 v99, -v35, s56, -v133\n\t"
"v_mfma_f32_16x16x32_bf16 v[168:171], a[96:99], a[180:183], v[168:171]\n\t"
"v_accvgpr_write_b32 a40, v98\n\t"
"v_fma_f32 v98, -v36, s56, -v134\n\t"
"v_mfma_f32_16x16x32_bf16 v[172:175], a[96:99], a[184:187], v[172:175]\n\t"
"v_accvgpr_write_b32 a41, v99\n\t"
"v_fma_f32 v99, -v37, s56, -v135\n\t"
"v_mfma_f32_16x16x32_bf16 v[172:175], a[88:91], a[180:183], v[172:175]\n\t"
"v_accvgpr_write_b32 a42, v98\n\t"
"v_accvgpr_write_b32 a43, v99\n\t"
"v_mfma_f32_16x16x32_bf16 v[172:175], a[80:83], a[176:179], v[172:175]\n\t"
"ds_read_b128 a[176:179], v223 offset:12288\n\t"
"v_fma_f32 v98, -v38, s56, -v136\n\t"
"v_mfma_f32_16x16x32_bf16 v[176:179], a[80:83], a[180:183], v[176:179]\n\t"
"ds_read_b128 a[180:183], v222 offset:19968\n\t"
"v_fma_f32 v99, -v39, s56, -v137\n\t"
"v_mfma_f32_16x16x32_bf16 v[176:179], a[88:91], a[184:187], v[176:179]\n\t"
"v_accvgpr_write_b32 a48, v98\n\t"
"v_fma_f32 v98, -v40, s56, -v138\n\t"
"v_mfma_f32_16x16x32_bf16 v[176:179], a[96:99], a[188:191], v[176:179]\n\t"
"Lpg2_%=:\n\t"
"s_cbranch_vccnz Lpg6_%=\n\t"
"v_accvgpr_write_b32 a49, v99\n\t"
"v_fma_f32 v99, -v41, s56, -v139\n\t"
"v_mfma_f32_16x16x32_bf16 v[180:183], a[96:99], a[192:195], v[180:183]\n\t"
"v_accvgpr_write_b32 a50, v98\n\t"
"v_accvgpr_write_b32 a51, v99\n\t"
"v_mfma_f32_16x16x32_bf16 v[180:183], a[88:91], a[188:191], v[180:183]\n\t"
"v_fma_f32 v98, -v42, s56, -v140\n\t"
"v_fma_f32 v99, -v43, s56, -v141\n\t"
"v_mfma_f32_16x16x32_bf16 v[180:183], a[80:83], a[184:187], v[180:183]\n\t"
"ds_read_b128 a[184:187], v223 offset:26112\n\t"
"v_accvgpr_write_b32 a56, v98\n\t"
"v_mfma_f32_16x16x32_bf16 v[184:187], a[80:83], a[188:191], v[184:187]\n\t"
"ds_read_b128 a[188:191], v223 offset:32256\n\t"
"v_fma_f32 v98, -v44, s56, -v142\n\t"
"v_mfma_f32_16x16x32_bf16 v[184:187], a[88:91], a[192:195], v[184:187]\n\t"
"v_accvgpr_write_b32 a57, v99\n\t"
"v_fma_f32 v99, -v45, s56, -v143\n\t"
"v_mfma_f32_16x16x32_bf16 v[184:187], a[96:99], a[196:199], v[184:187]\n\t"
"v_accvgpr_write_b32 a58, v98\n\t"
"v_accvgpr_write_b32 a59, v99\n\t"
"v_mfma_f32_16x16x32_bf16 v[188:191], a[96:99], a[200:203], v[188:191]\n\t"
"v_fma_f32 v98, -v46, s56, -v144\n\t"
"v_fma_f32 v99, -v47, s56, -v145\n\t"
"v_mfma_f32_16x16x32_bf16 v[188:191], a[88:91], a[196:199], v[188:191]\n\t"
"v_accvgpr_write_b32 a64, v98\n\t"
"v_fma_f32 v98, -v48, s56, -v146\n\t"
"v_mfma_f32_16x16x32_bf16 v[188:191], a[80:83], a[192:195], v[188:191]\n\t"
"ds_read_b128 a[192:195], v222 offset:39936\n\t"
"v_accvgpr_write_b32 a65, v99\n\t"
"v_mfma_f32_16x16x32_bf16 v[192:195], a[80:83], a[196:199], v[192:195]\n\t"
"s_waitcnt vmcnt(23) lgkmcnt(4)\n\t"
"ds_read_b128 a[196:199], v223 offset:46080\n\t"
"v_fma_f32 v99, -v49, s56, -v147\n\t"
"v_mfma_f32_16x16x32_bf16 v[192:195], a[88:91], a[200:203], v[192:195]\n\t"
"ds_read_b128 a[200:203], v223 offset:52224\n\t"
"v_accvgpr_write_b32 a66, v98\n\t"
"v_mfma_f32_16x16x32_bf16 v[192:195], a[96:99], a[204:207], v[192:195]\n\t"
"ds_read_b128 a[204:207], v222 offset:59904\n\t"
"v_accvgpr_write_b32 a67, v99\n\t"
"Lst2_%=:\n\t"
"v_mfma_f32_16x16x32_bf16 v[196:199], a[12:15], a[168:171], v[196:199]\n\t"
"s_waitcnt lgkmcnt(6)\n\t"
"v_fma_f32 v98, -v50, s56, -v148\n\t"
"v_fma_f32 v99, -v51, s56, -v149\n\t"
"v_mfma_f32_16x16x32_bf16 v[196:199], a[28:31], a[176:179], v[196:199]\n\t"
"v_accvgpr_write_b32 a72, v98\n\t"
"v_fma_f32 v98, -v52, s56, -v150\n\t"
"v_mfma_f32_16x16x32_bf16 v[196:199], a[20:23], a[172:175], v[196:199]\n\t"
"v_accvgpr_write_b32 a73, v99\n\t"
"v_fma_f32 v99, -v53, s56, -v151\n\t"
"v_mfma_f32_16x16x32_bf16 v[204:207], a[12:15], a[172:175], v[204:207]\n\t"
"s_waitcnt lgkmcnt(5)\n\t"
"v_accvgpr_write_b32 a74, v98\n\t"
"v_accvgpr_write_b32 a75, v99\n\t"
"v_mfma_f32_16x16x32_bf16 v[204:207], a[20:23], a[176:179], v[204:207]\n\t"
"v_fma_f32 v98, -v54, s56, -v152\n\t"
"v_fma_f32 v99, -v55, s56, -v153\n\t"
"v_mfma_f32_16x16x32_bf16 v[204:207], a[28:31], a[180:183], v[204:207]\n\t"
"v_accvgpr_write_b32 a80, v98\n\t"
"v_fma_f32 v98, -v56, s56, -v154\n\t"
"v_mfma_f32_16x16x32_bf16 v[212:215], a[28:31], a[184:187], v[212:215]\n\t"
"s_waitcnt lgkmcnt(4)\n\t"
"v_accvgpr_write_b32 a81, v99\n\t"
"v_fma_f32 v99, -v57, s56, -v155\n\t"
"v_mfma_f32_16x16x32_bf16 v[212:215], a[20:23], a[180:183], v[212:215]\n\t"
"v_accvgpr_write_b32 a82, v98\n\t"
"v_accvgpr_write_b32 a83, v99\n\t"
"v_mfma_f32_16x16x32_bf16 v[212:215], a[12:15], a[176:179], v[212:215]\n\t"
"v_fma_f32 v98, -v58, s56, -v156\n\t"
"v_fma_f32 v99, -v59, s56, -v157\n\t"
"v_mfma_f32_16x16x32_bf16 a[0:3], a[12:15], a[180:183], a[0:3]\n\t"
"s_waitcnt lgkmcnt(3)\n\t"
"v_accvgpr_write_b32 a88, v98\n\t"
"v_fma_f32 v98, -v60, s56, -v158\n\t"
"v_mfma_f32_16x16x32_bf16 a[0:3], a[20:23], a[184:187], a[0:3]\n\t"
"v_accvgpr_write_b32 a89, v99\n\t"
"v_fma_f32 v99, -v61, s56, -v159\n\t"
"v_mfma_f32_16x16x32_bf16 a[0:3], a[28:31], a[188:191], a[0:3]\n\t"
"v_accvgpr_write_b32 a90, v98\n\t"
"v_accvgpr_write_b32 a91, v99\n\t"
"v_mfma_f32_16x16x32_bf16 a[8:11], a[28:31], a[192:195], a[8:11]\n\t"
"s_waitcnt lgkmcnt(2)\n\t"
"v_fma_f32 v98, -v62, s56, -v160\n\t"
"v_fma_f32 v99, -v63, s56, -v161\n\t"
"v_mfma_f32_16x16x32_bf16 a[8:11], a[20:23], a[188:191], a[8:11]\n\t"
"v_accvgpr_write_b32 a96, v98\n\t"
"v_fma_f32 v98, -v64, s56, -v162\n\t"
"v_mfma_f32_16x16x32_bf16 a[8:11], a[12:15], a[184:187], a[8:11]\n\t"
"v_accvgpr_write_b32 a97, v99\n\t"
"v_fma_f32 v99, -v65, s56, -v163\n\t"
"v_mfma_f32_16x16x32_bf16 a[16:19], a[12:15], a[188:191], a[16:19]\n\t"
"s_waitcnt lgkmcnt(1)\n\t"
"v_accvgpr_write_b32 a98, v98\n\t"
"v_accvgpr_write_b32 a99, v99\n\t"
"v_mfma_f32_16x16x32_bf16 a[16:19], a[20:23], a[192:195], a[16:19]\n\t"
"v_fma_f32 v98, -v66, s56, -v164\n\t"
"v_fma_f32 v99, -v67, s56, -v165\n\t"
"v_mfma_f32_16x16x32_bf16 a[16:19], a[28:31], a[196:199], a[16:19]\n\t"
"v_accvgpr_write_b32 a104, v98\n\t"
"v_fma_f32 v98, -v68, s56, -v166\n\t"
"v_mfma_f32_16x16x32_bf16 a[24:27], a[28:31], a[200:203], a[24:27]\n\t"
"s_waitcnt lgkmcnt(0)\n\t"
"v_accvgpr_write_b32 a105, v99\n\t"
"v_fma_f32 v99, -v69, s56, -v167\n\t"
"v_mfma_f32_16x16x32_bf16 a[24:27], a[20:23], a[196:199], a[24:27]\n\t"
"v_accvgpr_write_b32 a106, v98\n\t"
"v_accvgpr_write_b32 a107, v99\n\t"
"v_mfma_f32_16x16x32_bf16 a[24:27], a[12:15], a[192:195], a[24:27]\n\t"
"v_fma_f32 v98, -v70, s56, -v168\n\t"
"v_fma_f32 v99, -v71, s56, -v169\n\t"
"v_mfma_f32_16x16x32_bf16 a[32:35], a[12:15], a[196:199], a[32:35]\n\t"
"s_waitcnt vmcnt(20)\n\t"
"v_accvgpr_write_b32 a112, v98\n\t"
"v_fma_f32 v98, -v72, s56, -v170\n\t"
"v_mfma_f32_16x16x32_bf16 a[32:35], a[20:23], a[200:203], a[32:35]\n\t"
"v_accvgpr_write_b32 a113, v99\n\t"
"v_fma_f32 v99, -v73, s56, -v171\n\t"
"v_mfma_f32_16x16x32_bf16 a[32:35], a[28:31], a[204:207], a[32:35]\n\t"
"v_accvgpr_write_b32 a114, v98\n\t"
"v_accvgpr_write_b32 a115, v99\n\t"
"v_mfma_f32_16x16x32_bf16 a[40:43], a[36:39], a[168:171], a[40:43]\n\t"
"v_fma_f32 v98, -v74, s56, -v172\n\t"
"v_fma_f32 v99, -v75, s56, -v173\n\t"
"v_mfma_f32_16x16x32_bf16 a[40:43], a[52:55], a[176:179], a[40:43]\n\t"
"v_accvgpr_write_b32 a120, v98\n\t"
"v_fma_f32 v98, -v76, s56, -v174\n\t"
"v_mfma_f32_16x16x32_bf16 a[40:43], a[44:47], a[172:175], a[40:43]\n\t"
"v_accvgpr_write_b32 a121, v99\n\t"
"v_fma_f32 v99, -v77, s56, -v175\n\t"
"v_mfma_f32_16x16x32_bf16 a[48:51], a[36:39], a[172:175], a[48:51]\n\t"
"v_accvgpr_write_b32 a122, v98\n\t"
"v_accvgpr_write_b32 a123, v99\n\t"
"v_mfma_f32_16x16x32_bf16 a[48:51], a[44:47], a[176:179], a[48:51]\n\t"
"v_fma_f32 v98, -v78, s56, -v176\n\t"
"v_fma_f32 v99, -v79, s56, -v177\n\t"
"v_mfma_f32_16x16x32_bf16 a[48:51], a[52:55], a[180:183], a[48:51]\n\t"
"v_accvgpr_write_b32 a128, v98\n\t"
"v_fma_f32 v98, -v80, s56, -v178\n\t"
"v_mfma_f32_16x16x32_bf16 a[56:59], a[52:55], a[184:187], a[56:59]\n\t"
"v_accvgpr_write_b32 a129, v99\n\t"
"v_fma_f32 v99, -v81, s56, -v179\n\t"
"v_mfma_f32_16x16x32_bf16 a[56:59], a[44:47], a[180:183], a[56:59]\n\t"
"v_accvgpr_write_b32 a130, v98\n\t"
"v_accvgpr_write_b32 a131, v99\n\t"
"v_mfma_f32_16x16x32_bf16 a[56:59], a[36:39], a[176:179], a[56:59]\n\t"
"v_fma_f32 v98, -v82, s56, -v180\n\t"
"v_fma_f32 v99, -v83, s56, -v181\n\t"
"v_mfma_f32_16x16x32_bf16 a[64:67], a[36:39], a[180:183], a[64:67]\n\t"
"v_accvgpr_write_b32 a136, v98\n\t"
"v_fma_f32 v98, -v84, s56, -v182\n\t"
"v_mfma_f32_16x16x32_bf16 a[64:67], a[44:47], a[184:187], a[64:67]\n\t"
"v_accvgpr_write_b32 a137, v99\n\t"
"v_fma_f32 v99, -v85, s56, -v183\n\t"
"v_mfma_f32_16x16x32_bf16 a[64:67], a[52:55], a[188:191], a[64:67]\n\t"
"v_accvgpr_write_b32 a138, v98\n\t"
"v_accvgpr_write_b32 a139, v99\n\t"
"v_mfma_f32_16x16x32_bf16 a[72:75], a[52:55], a[192:195], a[72:75]\n\t"
"v_fma_f32 v98, -v86, s56, -v184\n\t"
"v_fma_f32 v99, -v87, s56, -v185\n\t"
"v_mfma_f32_16x16x32_bf16 a[72:75], a[44:47], a[188:191], a[72:75]\n\t"
"v_accvgpr_write_b32 a144, v98\n\t"
"v_fma_f32 v98, -v88, s56, -v186\n\t"
"v_mfma_f32_16x16x32_bf16 a[72:75], a[36:39], a[184:187], a[72:75]\n\t"
"v_accvgpr_write_b32 a145, v99\n\t"
"v_fma_f32 v99, -v89, s56, -v187\n\t"
"v_mfma_f32_16x16x32_bf16 a[80:83], a[36:39], a[188:191], a[80:83]\n\t"
"v_accvgpr_write_b32 a146, v98\n\t"
"v_accvgpr_write_b32 a147, v99\n\t"
"v_mfma_f32_16x16x32_bf16 a[80:83], a[44:47], a[192:195], a[80:83]\n\t"
"v_fma_f32 v98, -v90, s56, -v188\n\t"
"v_fma_f32 v99, -v91, s56, -v189\n\t"
"v_mfma_f32_16x16x32_bf16 a[80:83], a[52:55], a[196:199], a[80:83]\n\t"
"v_accvgpr_write_b32 a152, v98\n\t"
"v_fma_f32 v98, -v92, s56, -v190\n\t"
"v_mfma_f32_16x16x32_bf16 a[88:91], a[52:55], a[200:203], a[88:91]\n\t"
"v_accvgpr_write_b32 a153, v99\n\t"
"v_fma_f32 v99, -v93, s56, -v191\n\t"
"v_mfma_f32_16x16x32_bf16 a[88:91], a[44:47], a[196:199], a[88:91]\n\t"
"v_accvgpr_write_b32 a154, v98\n\t"
"v_accvgpr_write_b32 a155, v99\n\t"
"v_mfma_f32_16x16x32_bf16 a[88:91], a[36:39], a[192:195], a[88:91]\n\t"
"v_fma_f32 v98, -v94, s56, -v192\n\t"
"v_fma_f32 v99, -v95, s56, -v193\n\t"
"v_mfma_f32_16x16x32_bf16 a[96:99], a[36:39], a[196:199], a[96:99]\n\t"
"s_waitcnt vmcnt(17)\n\t"
"v_accvgpr_write_b32 a160, v98\n\t"
"v_fma_f32 v98, -v96, s56, -v194\n\t"
"v_mfma_f32_16x16x32_bf16 a[96:99], a[44:47], a[200:203], a[96:99]\n\t"
"v_accvgpr_write_b32 a161, v99\n\t"
"v_fma_f32 v99, -v97, s56, -v195\n\t"
"v_mfma_f32_16x16x32_bf16 a[96:99], a[52:55], a[204:207], a[96:99]\n\t"
"v_accvgpr_write_b32 a162, v98\n\t"
"v_accvgpr_write_b32 a163, v99\n\t"
"v_mfma_f32_16x16x32_bf16 a[104:107], a[84:87], a[168:171], a[104:107]\n\t"
"ds_read_b128 a[168:171], v222 offset:256\n\t"
"v_fma_f32 v98, v112, s64, -v14\n\t"
"v_mfma_f32_16x16x32_bf16 a[104:107], a[100:103], a[176:179], a[104:107]\n\t"
"v_fma_f32 v99, v113, s64, -v15\n\t"
"v_accvgpr_write_b32 a4, v98\n\t"
"v_mfma_f32_16x16x32_bf16 a[104:107], a[92:95], a[172:175], a[104:107]\n\t"
"v_fma_f32 v98, v114, s64, -v16\n\t"
"v_accvgpr_write_b32 a5, v99\n\t"
"v_mfma_f32_16x16x32_bf16 a[112:115], a[84:87], a[172:175], a[112:115]\n\t"
"ds_read_b128 a[172:175], v223 offset:6400\n\t"
"v_fma_f32 v99, v115, s64, -v17\n\t"
"v_mfma_f32_16x16x32_bf16 a[112:115], a[92:95], a[176:179], a[112:115]\n\t"
"v_accvgpr_write_b32 a6, v98\n\t"
"v_accvgpr_write_b32 a7, v99\n\t"
"v_mfma_f32_16x16x32_bf16 a[112:115], a[100:103], a[180:183], a[112:115]\n\t"
"v_fma_f32 v98, v116, s64, -v18\n\t"
"v_fma_f32 v99, v117, s64, -v19\n\t"
"v_mfma_f32_16x16x32_bf16 a[120:123], a[100:103], a[184:187], a[120:123]\n\t"
"v_accvgpr_write_b32 a12, v98\n\t"
"v_fma_f32 v98, v118, s64, -v20\n\t"
"v_mfma_f32_16x16x32_bf16 a[120:123], a[92:95], a[180:183], a[120:123]\n\t"
"v_accvgpr_write_b32 a13, v99\n\t"
"v_fma_f32 v99, v119, s64, -v21\n\t"
"v_mfma_f32_16x16x32_bf16 a[120:123], a[84:87], a[176:179], a[120:123]\n\t"
"ds_read_b128 a[176:179], v223 offset:12544\n\t"
"v_accvgpr_write_b32 a14, v98\n\t"
"v_mfma_f32_16x16x32_bf16 a[128:131], a[84:87], a[180:183], a[128:131]\n\t"
"ds_read_b128 a[180:183], v222 offset:20224\n\t"
"v_accvgpr_write_b32 a15, v99\n\t"
"v_mfma_f32_16x16x32_bf16 a[128:131], a[92:95], a[184:187], a[128:131]\n\t"
"v_fma_f32 v98, v120, s64, -v22\n\t"
"v_fma_f32 v99, v121, s64, -v23\n\t"
"v_mfma_f32_16x16x32_bf16 a[128:131], a[100:103], a[188:191], a[128:131]\n\t"
"v_accvgpr_write_b32 a20, v98\n\t"
"v_fma_f32 v98, v122, s64, -v24\n\t"
"v_mfma_f32_16x16x32_bf16 a[136:139], a[100:103], a[192:195], a[136:139]\n\t"
"v_accvgpr_write_b32 a21, v99\n\t"
"v_fma_f32 v99, v123, s64, -v25\n\t"
"v_mfma_f32_16x16x32_bf16 a[136:139], a[92:95], a[188:191], a[136:139]\n\t"
"v_accvgpr_write_b32 a22, v98\n\t"
"v_accvgpr_write_b32 a23, v99\n\t"
"v_mfma_f32_16x16x32_bf16 a[136:139], a[84:87], a[184:187], a[136:139]\n\t"
"ds_read_b128 a[184:187], v223 offset:26368\n\t"
"v_fma_f32 v98, v124, s64, -v26\n\t"
"v_mfma_f32_16x16x32_bf16 a[144:147], a[84:87], a[188:191], a[144:147]\n\t"
"ds_read_b128 a[188:191], v223 offset:32512\n\t"
"v_fma_f32 v99, v125, s64, -v27\n\t"
"v_mfma_f32_16x16x32_bf16 a[144:147], a[92:95], a[192:195], a[144:147]\n\t"
"v_accvgpr_write_b32 a28, v98\n\t"
"v_fma_f32 v98, v126, s64, -v28\n\t"
"v_mfma_f32_16x16x32_bf16 a[144:147], a[100:103], a[196:199], a[144:147]\n\t"
"v_accvgpr_write_b32 a29, v99\n\t"
"v_fma_f32 v99, v127, s64, -v29\n\t"
"v_mfma_f32_16x16x32_bf16 a[152:155], a[100:103], a[200:203], a[152:155]\n\t"
"v_accvgpr_write_b32 a30, v98\n\t"
"v_accvgpr_write_b32 a31, v99\n\t"
"v_mfma_f32_16x16x32_bf16 a[152:155], a[92:95], a[196:199], a[152:155]\n\t"
"v_fma_f32 v98, v128, s64, -v30\n\t"
"v_fma_f32 v99, v129, s64, -v31\n\t"
"v_mfma_f32_16x16x32_bf16 a[152:155], a[84:87], a[192:195], a[152:155]\n\t"
"ds_read_b128 a[192:195], v222 offset:40192\n\t"
"v_accvgpr_write_b32 a36, v98\n\t"
"v_mfma_f32_16x16x32_bf16 a[160:163], a[84:87], a[196:199], a[160:163]\n\t"
"s_waitcnt vmcnt(14) lgkmcnt(4)\n\t"
"ds_read_b128 a[196:199], v223 offset:46336\n\t"
"v_fma_f32 v98, v130, s64, -v32\n\t"
"v_mfma_f32_16x16x32_bf16 a[160:163], a[92:95], a[200:203], a[160:163]\n\t"
"ds_read_b128 a[200:203], v223 offset:52480\n\t"
"v_accvgpr_write_b32 a37, v99\n\t"
"v_mfma_f32_16x16x32_bf16 a[160:163], a[100:103], a[204:207], a[160:163]\n\t"
"ds_read_b128 a[204:207], v222 offset:60160\n\t"
"v_fma_f32 v99, v131, s64, -v33\n\t"
"v_mfma_f32_16x16x32_bf16 v[200:203], a[140:143], a[168:171], v[200:203]\n\t"
"s_waitcnt lgkmcnt(6)\n\t"
"v_accvgpr_write_b32 a38, v98\n\t"
"v_accvgpr_write_b32 a39, v99\n\t"
"v_mfma_f32_16x16x32_bf16 v[200:203], a[156:159], a[176:179], v[200:203]\n\t"
"v_fma_f32 v98, v132, s64, -v34\n\t"
"v_fma_f32 v99, v133, s64, -v35\n\t"
"v_mfma_f32_16x16x32_bf16 v[200:203], a[148:151], a[172:175], v[200:203]\n\t"
"s_add_u32 m0, s25, 896\n\t"
"v_accvgpr_write_b32 a44, v98\n\t"
"v_fma_f32 v98, v134, s64, -v36\n\t"
"v_mfma_f32_16x16x32_bf16 v[208:211], a[140:143], a[172:175], v[208:211]\n\t"
"s_waitcnt lgkmcnt(5)\n\t"
"v_accvgpr_write_b32 a45, v99\n\t"
"v_fma_f32 v99, v135, s64, -v37\n\t"
"v_mfma_f32_16x16x32_bf16 v[208:211], a[148:151], a[176:179], v[208:211]\n\t"
"v_accvgpr_write_b32 a46, v98\n\t"
"v_mfma_f32_16x16x32_bf16 v[208:211], a[156:159], a[180:183], v[208:211]\n\t"
"s_waitcnt lgkmcnt(0)\n\t"
"s_barrier\n\t"
"v_mfma_f32_16x16x32_bf16 v[216:219], a[156:159], a[184:187], v[216:219]\n\t"
"v_accvgpr_write_b32 a47, v99\n\t"
"v_mfma_f32_16x16x32_bf16 v[216:219], a[148:151], a[180:183], v[216:219]\n\t"
"buffer_load_dwordx4 v254, s[12:15], 0 offen offset:128 nt lds\n\t"
"v_mfma_f32_16x16x32_bf16 v[216:219], a[140:143], a[176:179], v[216:219]\n\t"
"s_add_u32 m0, s26, 896\n\t"
"ds_read_b64_tr_b16 v[232:233], v246 offset:512\n\t"
"v_fma_f32 v98, v136, s64, -v38\n\t"
"v_mfma_f32_16x16x32_bf16 a[4:7], a[140:143], a[180:183], a[4:7]\n\t"
"ds_read_b64_tr_b16 v[236:237], v255 offset:512\n\t"
"v_fma_f32 v99, v137, s64, -v39\n\t"
"v_mfma_f32_16x16x32_bf16 a[4:7], a[148:151], a[184:187], a[4:7]\n\t"
"v_accvgpr_write_b32 a52, v98\n\t"
"v_fma_f32 v98, v138, s64, -v40\n\t"
"v_mfma_f32_16x16x32_bf16 a[4:7], a[156:159], a[188:191], a[4:7]\n\t"
"s_mov_b64 exec, s[36:37]\n\t"
"buffer_load_dwordx4 v254, s[12:15], s27 offen offset:128 nt lds\n\t"
"s_mov_b64 exec, -1\n\t"
"s_add_u32 m0, s24, 2432\n\t"
"s_add_u32 s28, s21, 3072\n\t"
"v_mfma_f32_16x16x32_bf16 a[12:15], a[156:159], a[192:195], a[12:15]\n\t"
"buffer_load_dwordx4 v247, s[12:15], s28 offen offset:128 nt lds\n\t"
"v_mfma_f32_16x16x32_bf16 a[12:15], a[148:151], a[188:191], a[12:15]\n\t"
"s_add_u32 m0, s24, 8576\n\t"
"s_add_u32 s28, s21, 199680\n\t"
"v_accvgpr_write_b32 a53, v99\n\t"
"v_mfma_f32_16x16x32_bf16 a[12:15], a[140:143], a[184:187], a[12:15]\n\t"
"buffer_load_dwordx4 v247, s[12:15], s28 offen offset:128 nt lds\n\t"
"s_add_u32 m0, s24, 14720\n\t"
"s_add_u32 s28, s21, 396288\n\t"
"v_mfma_f32_16x16x32_bf16 a[20:23], a[140:143], a[188:191], a[20:23]\n\t"
"buffer_load_dwordx4 v247, s[12:15], s28 offen offset:128 nt lds\n\t"
"v_mfma_f32_16x16x32_bf16 a[20:23], a[148:151], a[192:195], a[20:23]\n\t"
"s_add_u32 m0, s24, 22400\n\t"
"s_add_u32 s28, s21, 592896\n\t"
"v_fma_f32 v99, v139, s64, -v41\n\t"
"v_mfma_f32_16x16x32_bf16 a[20:23], a[156:159], a[196:199], a[20:23]\n\t"
"buffer_load_dwordx4 v247, s[12:15], s28 offen offset:128 nt lds\n\t"
"s_add_u32 m0, s24, 28544\n\t"
"s_add_u32 s28, s21, 789504\n\t"
"v_mfma_f32_16x16x32_bf16 a[28:31], a[156:159], a[200:203], a[28:31]\n\t"
"buffer_load_dwordx4 v247, s[12:15], s28 offen offset:128 nt lds\n\t"
"v_mfma_f32_16x16x32_bf16 a[28:31], a[148:151], a[196:199], a[28:31]\n\t"
"s_add_u32 m0, s24, 34688\n\t"
"s_add_u32 s28, s21, 986112\n\t"
"v_accvgpr_write_b32 a54, v98\n\t"
"v_mfma_f32_16x16x32_bf16 a[28:31], a[140:143], a[192:195], a[28:31]\n\t"
"v_accvgpr_write_b32 a55, v99\n\t"
"v_fma_f32 v98, v140, s64, -v42\n\t"
"v_mfma_f32_16x16x32_bf16 a[36:39], a[140:143], a[196:199], a[36:39]\n\t"
"s_waitcnt lgkmcnt(1)\n\t"
"v_fma_f32 v99, v141, s64, -v43\n\t"
"v_accvgpr_write_b32 a60, v98\n\t"
"v_mfma_f32_16x16x32_bf16 a[36:39], a[148:151], a[200:203], a[36:39]\n\t"
"s_waitcnt lgkmcnt(0)\n\t"
"v_fma_f32 v98, v142, s64, -v44\n\t"
"v_accvgpr_write_b32 a61, v99\n\t"
"v_mfma_f32_16x16x32_bf16 a[36:39], a[156:159], a[204:207], a[36:39]\n\t"
"buffer_load_dwordx4 v247, s[12:15], s28 offen offset:128 nt lds\n\t"
"v_mfma_f32_4x4x4_16b_bf16 v[232:235], v[232:233], v[220:221], 0\n\t"
"s_waitcnt vmcnt(19)\n\t"
"v_mfma_f32_4x4x4_16b_bf16 v[236:239], v[236:237], v[220:221], 0\n\t"
"v_mfma_f32_16x16x32_bf16 a[44:47], v[240:243], a[168:171], a[44:47]\n\t"
"v_fma_f32 v99, v143, s64, -v45\n\t"
"v_accvgpr_write_b32 a62, v98\n\t"
"v_mfma_f32_16x16x32_bf16 a[44:47], v[248:251], a[176:179], a[44:47]\n\t"
"v_accvgpr_write_b32 a63, v99\n\t"
"v_fma_f32 v98, v144, s64, -v46\n\t"
"v_mfma_f32_16x16x32_bf16 a[44:47], a[208:211], a[172:175], a[44:47]\n\t"
"v_fma_f32 v99, v145, s64, -v47\n\t"
"v_cvt_pk_bf16_f32 v232, v232, v233\n\t"
"v_mfma_f32_16x16x32_bf16 a[52:55], v[240:243], a[172:175], a[52:55]\n\t"
"v_cvt_pk_bf16_f32 v233, v234, v235\n\t"
"v_cvt_pk_bf16_f32 v234, v236, v237\n\t"
"v_mfma_f32_16x16x32_bf16 a[52:55], a[208:211], a[176:179], a[52:55]\n\t"
"v_cvt_pk_bf16_f32 v235, v238, v239\n\t"
"v_accvgpr_write_b32 a68, v98\n\t"
"v_mfma_f32_16x16x32_bf16 a[52:55], v[248:251], a[180:183], a[52:55]\n\t"
"ds_write_b128 v245, v[232:235] offset:512\n\t"
"v_fma_f32 v98, v146, s64, -v48\n\t"
"v_mfma_f32_16x16x32_bf16 a[60:63], v[248:251], a[184:187], a[60:63]\n\t"
"v_accvgpr_write_b32 a69, v99\n\t"
"v_fma_f32 v99, v147, s64, -v49\n\t"
"v_mfma_f32_16x16x32_bf16 a[60:63], a[208:211], a[180:183], a[60:63]\n\t"
"v_accvgpr_write_b32 a70, v98\n\t"
"v_accvgpr_write_b32 a71, v99\n\t"
"v_mfma_f32_16x16x32_bf16 a[60:63], v[240:243], a[176:179], a[60:63]\n\t"
"ds_read_b64_tr_b16 v[228:229], v246 offset:6656\n\t"
"v_fma_f32 v98, v148, s64, -v50\n\t"
"v_fma_f32 v99, v149, s64, -v51\n\t"
"v_mfma_f32_16x16x32_bf16 a[68:71], v[240:243], a[180:183], a[68:71]\n\t"
"ds_read_b64_tr_b16 v[224:225], v255 offset:6656\n\t"
"v_accvgpr_write_b32 a76, v98\n\t"
"v_mfma_f32_16x16x32_bf16 a[68:71], a[208:211], a[184:187], a[68:71]\n\t"
"v_fma_f32 v98, v150, s64, -v52\n\t"
"v_accvgpr_write_b32 a77, v99\n\t"
"v_mfma_f32_16x16x32_bf16 a[68:71], v[248:251], a[188:191], a[68:71]\n\t"
"v_fma_f32 v99, v151, s64, -v53\n\t"
"v_accvgpr_write_b32 a78, v98\n\t"
"v_accvgpr_write_b32 a79, v99\n\t"
"v_fma_f32 v98, v152, s64, -v54\n\t"
"v_fma_f32 v99, v153, s64, -v55\n\t"
"v_mfma_f32_16x16x32_bf16 a[76:79], v[248:251], a[192:195], a[76:79]\n\t"
"v_accvgpr_write_b32 a84, v98\n\t"
"v_fma_f32 v98, v154, s64, -v56\n\t"
"v_mfma_f32_16x16x32_bf16 a[76:79], a[208:211], a[188:191], a[76:79]\n\t"
"v_accvgpr_write_b32 a85, v99\n\t"
"v_fma_f32 v99, v155, s64, -v57\n\t"
"v_mfma_f32_16x16x32_bf16 a[76:79], v[240:243], a[184:187], a[76:79]\n\t"
"v_accvgpr_write_b32 a86, v98\n\t"
"v_accvgpr_write_b32 a87, v99\n\t"
"v_fma_f32 v98, v156, s64, -v58\n\t"
"v_fma_f32 v99, v157, s64, -v59\n\t"
"v_mfma_f32_16x16x32_bf16 a[84:87], v[240:243], a[188:191], a[84:87]\n\t"
"v_accvgpr_write_b32 a92, v98\n\t"
"v_fma_f32 v98, v158, s64, -v60\n\t"
"v_mfma_f32_16x16x32_bf16 a[84:87], a[208:211], a[192:195], a[84:87]\n\t"
"v_accvgpr_write_b32 a93, v99\n\t"
"v_fma_f32 v99, v159, s64, -v61\n\t"
"v_mfma_f32_16x16x32_bf16 a[84:87], v[248:251], a[196:199], a[84:87]\n\t"
"v_accvgpr_write_b32 a94, v98\n\t"
"v_accvgpr_write_b32 a95, v99\n\t"
"v_fma_f32 v98, v160, s64, -v62\n\t"
"v_fma_f32 v99, v161, s64, -v63\n\t"
"v_mfma_f32_16x16x32_bf16 a[92:95], v[248:251], a[200:203], a[92:95]\n\t"
"v_accvgpr_write_b32 a100, v98\n\t"
"v_fma_f32 v98, v162, s64, -v64\n\t"
"v_mfma_f32_16x16x32_bf16 a[92:95], a[208:211], a[196:199], a[92:95]\n\t"
"v_accvgpr_write_b32 a101, v99\n\t"
"v_fma_f32 v99, v163, s64, -v65\n\t"
"v_mfma_f32_16x16x32_bf16 a[92:95], v[240:243], a[192:195], a[92:95]\n\t"
"v_accvgpr_write_b32 a102, v98\n\t"
"v_accvgpr_write_b32 a103, v99\n\t"
"s_nop 1\n\t"
"v_mfma_f32_16x16x32_bf16 a[100:103], v[240:243], a[196:199], a[100:103]\n\t"
"buffer_load_dwordx4 v[240:243], off, s[80:83], s20 offset:2048 sc1\n\t"
"v_mfma_f32_16x16x32_bf16 a[100:103], a[208:211], a[200:203], a[100:103]\n\t"
"s_waitcnt lgkmcnt(0)\n\t"
"v_fma_f32 v98, v164, s64, -v66\n\t"
"v_fma_f32 v99, v165, s64, -v67\n\t"
"v_mfma_f32_16x16x32_bf16 a[100:103], v[248:251], a[204:207], a[100:103]\n\t"
"v_accvgpr_write_b32 a108, v98\n\t"
"v_fma_f32 v98, v166, s64, -v68\n\t"
"v_accvgpr_write_b32 a109, v99\n\t"
"v_fma_f32 v99, v167, s64, -v69\n\t"
"v_accvgpr_write_b32 a110, v98\n\t"
"v_accvgpr_write_b32 a111, v99\n\t"
"v_mfma_f32_4x4x4_16b_bf16 v[228:231], v[228:229], v[220:221], 0\n\t"
"s_waitcnt vmcnt(17)\n\t"
"v_fma_f32 v98, v168, s64, -v70\n\t"
"v_fma_f32 v99, v169, s64, -v71\n\t"
"v_mfma_f32_4x4x4_16b_bf16 v[224:227], v[224:225], v[220:221], 0\n\t"
"v_mfma_f32_16x16x32_bf16 a[108:111], a[212:215], a[168:171], a[108:111]\n\t"
"ds_read_b128 a[168:171], v222 offset:1536\n\t"
"v_accvgpr_write_b32 a116, v98\n\t"
"v_mfma_f32_16x16x32_bf16 a[108:111], a[220:223], a[176:179], a[108:111]\n\t"
"v_fma_f32 v98, v170, s64, -v72\n\t"
"v_accvgpr_write_b32 a117, v99\n\t"
"v_mfma_f32_16x16x32_bf16 a[108:111], a[216:219], a[172:175], a[108:111]\n\t"
"v_fma_f32 v99, v171, s64, -v73\n\t"
"v_accvgpr_write_b32 a118, v98\n\t"
"v_accvgpr_write_b32 a119, v99\n\t"
"v_fma_f32 v98, v172, s64, -v74\n\t"
"v_fma_f32 v99, v173, s64, -v75\n\t"
"v_mfma_f32_16x16x32_bf16 a[116:119], a[212:215], a[172:175], a[116:119]\n\t"
"ds_read_b128 a[172:175], v223 offset:7680\n\t"
"v_accvgpr_write_b32 a124, v98\n\t"
"v_mfma_f32_16x16x32_bf16 a[116:119], a[216:219], a[176:179], a[116:119]\n\t"
"v_fma_f32 v98, v174, s64, -v76\n\t"
"v_accvgpr_write_b32 a125, v99\n\t"
"v_mfma_f32_16x16x32_bf16 a[116:119], a[220:223], a[180:183], a[116:119]\n\t"
"v_fma_f32 v99, v175, s64, -v77\n\t"
"v_accvgpr_write_b32 a126, v98\n\t"
"v_accvgpr_write_b32 a127, v99\n\t"
"v_fma_f32 v98, v176, s64, -v78\n\t"
"v_fma_f32 v99, v177, s64, -v79\n\t"
"v_cvt_pk_bf16_f32 v228, v228, v229\n\t"
"v_cvt_pk_bf16_f32 v229, v230, v231\n\t"
"v_cvt_pk_bf16_f32 v230, v224, v225\n\t"
"v_cvt_pk_bf16_f32 v231, v226, v227\n\t"
"v_mfma_f32_16x16x32_bf16 a[124:127], a[220:223], a[184:187], a[124:127]\n\t"
"v_accvgpr_write_b32 a132, v98\n\t"
"v_fma_f32 v98, v178, s64, -v80\n\t"
"v_mfma_f32_16x16x32_bf16 a[124:127], a[216:219], a[180:183], a[124:127]\n\t"
"v_accvgpr_write_b32 a133, v99\n\t"
"v_fma_f32 v99, v179, s64, -v81\n\t"
"v_mfma_f32_16x16x32_bf16 a[124:127], a[212:215], a[176:179], a[124:127]\n\t"
"ds_write_b128 v245, v[228:231] offset:6656\n\t"
"v_accvgpr_write_b32 a134, v98\n\t"
"v_accvgpr_write_b32 a135, v99\n\t"
"v_fma_f32 v98, v180, s64, -v82\n\t"
"v_fma_f32 v99, v181, s64, -v83\n\t"
"v_mfma_f32_16x16x32_bf16 a[132:135], a[212:215], a[180:183], a[132:135]\n\t"
"ds_read_b128 a[176:179], v223 offset:13824\n\t"
"v_mfma_f32_16x16x32_bf16 a[132:135], a[216:219], a[184:187], a[132:135]\n\t"
"ds_read_b128 a[180:183], v222 offset:21504\n\t"
"v_accvgpr_write_b32 a140, v98\n\t"
"v_mfma_f32_16x16x32_bf16 a[132:135], a[220:223], a[188:191], a[132:135]\n\t"
"ds_read_b64_tr_b16 v[236:237], v246 offset:12800\n\t"
"v_fma_f32 v98, v182, s64, -v84\n\t"
"v_accvgpr_write_b32 a141, v99\n\t"
"v_fma_f32 v99, v183, s64, -v85\n\t"
"v_accvgpr_write_b32 a142, v98\n\t"
"v_accvgpr_write_b32 a143, v99\n\t"
"v_fma_f32 v98, v184, s64, -v86\n\t"
"v_fma_f32 v99, v185, s64, -v87\n\t"
"v_mfma_f32_16x16x32_bf16 a[140:143], a[220:223], a[192:195], a[140:143]\n\t"
"s_mov_b32 s20, 73728\n\t"
"ds_read_b64_tr_b16 v[232:233], v255 offset:12800\n\t"
"v_accvgpr_write_b32 a148, v98\n\t"
"v_mfma_f32_16x16x32_bf16 a[140:143], a[216:219], a[188:191], a[140:143]\n\t"
"v_fma_f32 v98, v186, s64, -v88\n\t"
"v_accvgpr_write_b32 a149, v99\n\t"
"v_mfma_f32_16x16x32_bf16 a[140:143], a[212:215], a[184:187], a[140:143]\n\t"
"ds_read_b128 a[184:187], v223 offset:27648\n\t"
"v_fma_f32 v99, v187, s64, -v89\n\t"
"v_accvgpr_write_b32 a150, v98\n\t"
"v_accvgpr_write_b32 a151, v99\n\t"
"v_fma_f32 v98, v188, s64, -v90\n\t"
"v_fma_f32 v99, v189, s64, -v91\n\t"
"v_mfma_f32_16x16x32_bf16 a[148:151], a[212:215], a[188:191], a[148:151]\n\t"
"ds_read_b128 a[188:191], v223 offset:33792\n\t"
"v_accvgpr_write_b32 a156, v98\n\t"
"v_mfma_f32_16x16x32_bf16 a[148:151], a[216:219], a[192:195], a[148:151]\n\t"
"v_fma_f32 v98, v190, s64, -v92\n\t"
"v_accvgpr_write_b32 a157, v99\n\t"
"v_mfma_f32_16x16x32_bf16 a[148:151], a[220:223], a[196:199], a[148:151]\n\t"
"v_fma_f32 v99, v191, s64, -v93\n\t"
"v_accvgpr_write_b32 a158, v98\n\t"
"v_accvgpr_write_b32 a159, v99\n\t"
"v_fma_f32 v98, v192, s64, -v94\n\t"
"v_fma_f32 v99, v193, s64, -v95\n\t"
"v_mfma_f32_16x16x32_bf16 a[156:159], a[220:223], a[200:203], a[156:159]\n\t"
"s_waitcnt lgkmcnt(2)\n\t"
"v_accvgpr_write_b32 a164, v98\n\t"
"v_fma_f32 v98, v194, s64, -v96\n\t"
"v_mfma_f32_16x16x32_bf16 a[156:159], a[216:219], a[196:199], a[156:159]\n\t"
"Lpg3_%=:\n\t"
"s_cbranch_vccnz Lpg7_%=\n\t"
"v_accvgpr_write_b32 a165, v99\n\t"
"v_fma_f32 v99, v195, s64, -v97\n\t"
"v_mfma_f32_16x16x32_bf16 a[156:159], a[212:215], a[192:195], a[156:159]\n\t"
"ds_read_b128 a[192:195], v222 offset:41472\n\t"
"v_accvgpr_write_b32 a166, v98\n\t"
"v_accvgpr_write_b32 a167, v99\n\t"
"s_nop 1\n\t"
"v_mfma_f32_16x16x32_bf16 a[164:167], a[212:215], a[196:199], a[164:167]\n\t"
"buffer_load_dwordx4 a[208:211], off, s[80:83], s20 offset:0 sc1\n\t"
"v_mfma_f32_16x16x32_bf16 a[164:167], a[216:219], a[200:203], a[164:167]\n\t"
"buffer_load_dwordx4 v[248:251], off, s[80:83], s20 offset:1024 sc1\n\t"
"v_mfma_f32_16x16x32_bf16 a[164:167], a[220:223], a[204:207], a[164:167]\n\t"
"ds_read_b128 a[196:199], v223 offset:47616\n\t"
"v_fma_f32 v100, -v100, s66, v2\n\t"
"v_fma_f32 v2, v4, s52, v102\n\t"
"v_fma_f32 v101, -v101, s66, v3\n\t"
"v_fma_f32 v3, v5, s52, v103\n\t"
"v_fma_f32 v102, -v102, s66, v4\n\t"
"v_fma_f32 v103, -v103, s66, v5\n\t"
"v_mfma_f32_4x4x4_16b_bf16 v[236:239], v[236:237], v[220:221], 0\n\t"
"s_waitcnt vmcnt(16)\n\t"
"v_mfma_f32_4x4x4_16b_bf16 v[232:235], v[232:233], v[220:221], 0\n\t"
"v_fma_f32 v4, v6, s52, v104\n\t"
"v_fma_f32 v5, v7, s52, v105\n\t"
"Lst3_%=:\n\t"
"v_mfma_f32_16x16x32_bf16 v[0:3], a[224:227], a[168:171], v[0:3]\n\t"
"ds_read_b128 a[200:203], v223 offset:53760\n\t"
"v_fma_f32 v104, -v104, s66, v6\n\t"
"v_mfma_f32_16x16x32_bf16 v[0:3], a[232:235], a[176:179], v[0:3]\n\t"
"ds_read_b128 a[204:207], v222 offset:61440\n\t"
"v_fma_f32 v6, v8, s52, v106\n\t"
"v_mfma_f32_16x16x32_bf16 v[0:3], a[228:231], a[172:175], v[0:3]\n\t"
"buffer_load_dwordx4 a[212:215], off, s[80:83], s20 offset:2048 sc1\n\t"
"v_fma_f32 v105, -v105, s66, v7\n\t"
"v_fma_f32 v7, v9, s52, v107\n\t"
"v_fma_f32 v106, -v106, s66, v8\n\t"
"v_fma_f32 v107, -v107, s66, v9\n\t"
"v_fma_f32 v8, v10, s52, v108\n\t"
"v_fma_f32 v9, v11, s52, v109\n\t"
"s_mov_b32 s20, 86016\n\t"
"v_mfma_f32_16x16x32_bf16 v[4:7], a[224:227], a[172:175], v[4:7]\n\t"
"buffer_load_dwordx4 a[216:219], off, s[80:83], s20 offset:0 sc1\n\t"
"v_mfma_f32_16x16x32_bf16 v[4:7], a[228:231], a[176:179], v[4:7]\n\t"
"buffer_load_dwordx4 a[220:223], off, s[80:83], s20 offset:1024 sc1\n\t"
"v_mfma_f32_16x16x32_bf16 v[4:7], a[232:235], a[180:183], v[4:7]\n\t"
"v_fma_f32 v108, -v108, s66, v10\n\t"
"v_fma_f32 v10, v12, s52, v110\n\t"
"v_fma_f32 v109, -v109, s66, v11\n\t"
"v_fma_f32 v11, v13, s52, v111\n\t"
"v_fma_f32 v110, -v110, s66, v12\n\t"
"v_fma_f32 v111, -v111, s66, v13\n\t"
"v_cvt_pk_bf16_f32 v236, v236, v237\n\t"
"v_cvt_pk_bf16_f32 v237, v238, v239\n\t"
"v_fma_f32 v12, v14, s52, v112\n\t"
"v_fma_f32 v13, v15, s52, v113\n\t"
"v_cvt_pk_bf16_f32 v238, v232, v233\n\t"
"v_cvt_pk_bf16_f32 v239, v234, v235\n\t"
"s_waitcnt lgkmcnt(5)\n\t"
"v_mfma_f32_16x16x32_bf16 v[8:11], a[232:235], a[184:187], v[8:11]\n\t"
"v_fma_f32 v112, -v112, s66, v14\n\t"
"v_fma_f32 v14, v16, s52, v114\n\t"
"v_mfma_f32_16x16x32_bf16 v[8:11], a[228:231], a[180:183], v[8:11]\n\t"
"v_fma_f32 v113, -v113, s66, v15\n\t"
"v_fma_f32 v15, v17, s52, v115\n\t"
"v_mfma_f32_16x16x32_bf16 v[8:11], a[224:227], a[176:179], v[8:11]\n\t"
"ds_write_b128 v245, v[236:239] offset:12800\n\t"
"v_fma_f32 v114, -v114, s66, v16\n\t"
"v_fma_f32 v115, -v115, s66, v17\n\t"
"v_fma_f32 v16, v18, s52, v116\n\t"
"v_fma_f32 v17, v19, s52, v117\n\t"
"s_waitcnt lgkmcnt(5)\n\t"
"v_mfma_f32_16x16x32_bf16 v[12:15], a[224:227], a[180:183], v[12:15]\n\t"
"ds_read_b64_tr_b16 v[224:225], v246 offset:20480\n\t"
"v_fma_f32 v116, -v116, s66, v18\n\t"
"v_mfma_f32_16x16x32_bf16 v[12:15], a[228:231], a[184:187], v[12:15]\n\t"
"ds_read_b64_tr_b16 v[228:229], v255 offset:20480\n\t"
"v_fma_f32 v18, v20, s52, v118\n\t"
"v_mfma_f32_16x16x32_bf16 v[12:15], a[232:235], a[188:191], v[12:15]\n\t"
"v_fma_f32 v117, -v117, s66, v19\n\t"
"v_fma_f32 v19, v21, s52, v119\n\t"
"v_fma_f32 v118, -v118, s66, v20\n\t"
"v_fma_f32 v119, -v119, s66, v21\n\t"
"v_fma_f32 v20, v22, s52, v120\n\t"
"v_fma_f32 v21, v23, s52, v121\n\t"
"s_waitcnt lgkmcnt(6)\n\t"
"v_mfma_f32_16x16x32_bf16 v[16:19], a[232:235], a[192:195], v[16:19]\n\t"
"v_fma_f32 v120, -v120, s66, v22\n\t"
"v_fma_f32 v22, v24, s52, v122\n\t"
"v_mfma_f32_16x16x32_bf16 v[16:19], a[228:231], a[188:191], v[16:19]\n\t"
"v_fma_f32 v121, -v121, s66, v23\n\t"
"v_fma_f32 v23, v25, s52, v123\n\t"
"v_mfma_f32_16x16x32_bf16 v[16:19], a[224:227], a[184:187], v[16:19]\n\t"
"v_fma_f32 v122, -v122, s66, v24\n\t"
"v_fma_f32 v123, -v123, s66, v25\n\t"
"v_fma_f32 v24, v26, s52, v124\n\t"
"v_fma_f32 v25, v27, s52, v125\n\t"
"s_waitcnt lgkmcnt(5)\n\t"
"v_mfma_f32_16x16x32_bf16 v[20:23], a[224:227], a[188:191], v[20:23]\n\t"
"v_fma_f32 v124, -v124, s66, v26\n\t"
"v_fma_f32 v26, v28, s52, v126\n\t"
"v_mfma_f32_16x16x32_bf16 v[20:23], a[228:231], a[192:195], v[20:23]\n\t"
"v_fma_f32 v125, -v125, s66, v27\n\t"
"v_fma_f32 v27, v29, s52, v127\n\t"
"v_mfma_f32_16x16x32_bf16 v[20:23], a[232:235], a[196:199], v[20:23]\n\t"
"v_fma_f32 v126, -v126, s66, v28\n\t"
"v_fma_f32 v127, -v127, s66, v29\n\t"
"v_fma_f32 v28, v30, s52, v128\n\t"
"v_fma_f32 v29, v31, s52, v129\n\t"
"s_waitcnt lgkmcnt(4)\n\t"
"v_mfma_f32_16x16x32_bf16 v[24:27], a[232:235], a[200:203], v[24:27]\n\t"
"v_fma_f32 v128, -v128, s66, v30\n\t"
"v_fma_f32 v30, v32, s52, v130\n\t"
"v_mfma_f32_16x16x32_bf16 v[24:27], a[228:231], a[196:199], v[24:27]\n\t"
"v_fma_f32 v129, -v129, s66, v31\n\t"
"v_fma_f32 v31, v33, s52, v131\n\t"
"v_mfma_f32_16x16x32_bf16 v[24:27], a[224:227], a[192:195], v[24:27]\n\t"
"v_fma_f32 v130, -v130, s66, v32\n\t"
"v_fma_f32 v131, -v131, s66, v33\n\t"
"s_waitcnt lgkmcnt(3)\n\t"
"v_mfma_f32_16x16x32_bf16 v[28:31], a[224:227], a[196:199], v[28:31]\n\t"
"buffer_load_dwordx4 a[224:227], off, s[80:83], s20 offset:2048 sc1\n\t"
"v_mfma_f32_16x16x32_bf16 v[28:31], a[228:231], a[200:203], v[28:31]\n\t"
"s_waitcnt lgkmcnt(0)\n\t"
"s_mov_b32 s20, 98304\n\t"
"v_fma_f32 v32, v34, s52, v132\n\t"
"v_mfma_f32_16x16x32_bf16 v[28:31], a[232:235], a[204:207], v[28:31]\n\t"
"v_fma_f32 v33, v35, s52, v133\n\t"
"v_fma_f32 v132, -v132, s66, v34\n\t"
"v_fma_f32 v34, v36, s52, v134\n\t"
"v_fma_f32 v133, -v133, s66, v35\n\t"
"v_fma_f32 v35, v37, s52, v135\n\t"
"v_fma_f32 v134, -v134, s66, v36\n\t"
"v_fma_f32 v135, -v135, s66, v37\n\t"
"v_mfma_f32_4x4x4_16b_bf16 v[224:227], v[224:225], v[220:221], 0\n\t"
"s_waitcnt vmcnt(17)\n\t"
"v_mfma_f32_4x4x4_16b_bf16 v[228:231], v[228:229], v[220:221], 0\n\t"
"v_fma_f32 v36, v38, s52, v136\n\t"
"v_fma_f32 v37, v39, s52, v137\n\t"
"v_mfma_f32_16x16x32_bf16 v[32:35], a[236:239], a[168:171], v[32:35]\n\t"
"v_fma_f32 v136, -v136, s66, v38\n\t"
"v_fma_f32 v38, v40, s52, v138\n\t"
"v_mfma_f32_16x16x32_bf16 v[32:35], a[244:247], a[176:179], v[32:35]\n\t"
"v_fma_f32 v137, -v137, s66, v39\n\t"
"v_fma_f32 v39, v41, s52, v139\n\t"
"v_mfma_f32_16x16x32_bf16 v[32:35], a[240:243], a[172:175], v[32:35]\n\t"
"buffer_load_dwordx4 a[228:231], off, s[80:83], s20 offset:0 sc1\n\t"
"v_fma_f32 v138, -v138, s66, v40\n\t"
"v_fma_f32 v139, -v139, s66, v41\n\t"
"v_fma_f32 v40, v42, s52, v140\n\t"
"v_fma_f32 v41, v43, s52, v141\n\t"
"v_mfma_f32_16x16x32_bf16 v[36:39], a[236:239], a[172:175], v[36:39]\n\t"
"v_fma_f32 v140, -v140, s66, v42\n\t"
"v_fma_f32 v42, v44, s52, v142\n\t"
"v_mfma_f32_16x16x32_bf16 v[36:39], a[240:243], a[176:179], v[36:39]\n\t"
"v_fma_f32 v141, -v141, s66, v43\n\t"
"v_fma_f32 v43, v45, s52, v143\n\t"
"v_mfma_f32_16x16x32_bf16 v[36:39], a[244:247], a[180:183], v[36:39]\n\t"
"buffer_load_dwordx4 a[232:235], off, s[80:83], s20 offset:1024 sc1\n\t"
"v_fma_f32 v142, -v142, s66, v44\n\t"
"v_fma_f32 v143, -v143, s66, v45\n\t"
"v_cvt_pk_bf16_f32 v224, v224, v225\n\t"
"v_cvt_pk_bf16_f32 v225, v226, v227\n\t"
"v_fma_f32 v44, v46, s52, v144\n\t"
"v_fma_f32 v45, v47, s52, v145\n\t"
"v_cvt_pk_bf16_f32 v226, v228, v229\n\t"
"v_cvt_pk_bf16_f32 v227, v230, v231\n\t"
"v_mfma_f32_16x16x32_bf16 v[40:43], a[244:247], a[184:187], v[40:43]\n\t"
"v_fma_f32 v144, -v144, s66, v46\n\t"
"v_fma_f32 v46, v48, s52, v146\n\t"
"v_mfma_f32_16x16x32_bf16 v[40:43], a[240:243], a[180:183], v[40:43]\n\t"
"v_fma_f32 v145, -v145, s66, v47\n\t"
"v_fma_f32 v47, v49, s52, v147\n\t"
"v_mfma_f32_16x16x32_bf16 v[40:43], a[236:239], a[176:179], v[40:43]\n\t"
"ds_write_b128 v245, v[224:227] offset:20480\n\t"
"v_fma_f32 v146, -v146, s66, v48\n\t"
"v_fma_f32 v147, -v147, s66, v49\n\t"
"v_fma_f32 v48, v50, s52, v148\n\t"
"v_fma_f32 v49, v51, s52, v149\n\t"
"v_mfma_f32_16x16x32_bf16 v[44:47], a[236:239], a[180:183], v[44:47]\n\t"
"ds_read_b64_tr_b16 v[232:233], v246 offset:26624\n\t"
"v_mfma_f32_16x16x32_bf16 v[44:47], a[240:243], a[184:187], v[44:47]\n\t"
"ds_read_b64_tr_b16 v[236:237], v255 offset:26624\n\t"
"v_fma_f32 v148, -v148, s66, v50\n\t"
"v_mfma_f32_16x16x32_bf16 v[44:47], a[244:247], a[188:191], v[44:47]\n\t"
"v_fma_f32 v50, v52, s52, v150\n\t"
"v_fma_f32 v149, -v149, s66, v51\n\t"
"v_fma_f32 v51, v53, s52, v151\n\t"
"v_fma_f32 v150, -v150, s66, v52\n\t"
"v_fma_f32 v151, -v151, s66, v53\n\t"
"v_fma_f32 v52, v54, s52, v152\n\t"
"v_fma_f32 v53, v55, s52, v153\n\t"
"v_mfma_f32_16x16x32_bf16 v[48:51], a[244:247], a[192:195], v[48:51]\n\t"
"v_fma_f32 v152, -v152, s66, v54\n\t"
"v_fma_f32 v54, v56, s52, v154\n\t"
"v_mfma_f32_16x16x32_bf16 v[48:51], a[240:243], a[188:191], v[48:51]\n\t"
"v_fma_f32 v153, -v153, s66, v55\n\t"
"v_fma_f32 v55, v57, s52, v155\n\t"
"v_mfma_f32_16x16x32_bf16 v[48:51], a[236:239], a[184:187], v[48:51]\n\t"
"v_fma_f32 v154, -v154, s66, v56\n\t"
"v_fma_f32 v155, -v155, s66, v57\n\t"
"v_fma_f32 v56, v58, s52, v156\n\t"
"v_fma_f32 v57, v59, s52, v157\n\t"
"v_mfma_f32_16x16x32_bf16 v[52:55], a[236:239], a[188:191], v[52:55]\n\t"
"v_fma_f32 v156, -v156, s66, v58\n\t"
"v_fma_f32 v58, v60, s52, v158\n\t"
"v_mfma_f32_16x16x32_bf16 v[52:55], a[240:243], a[192:195], v[52:55]\n\t"
"v_fma_f32 v157, -v157, s66, v59\n\t"
"v_fma_f32 v59, v61, s52, v159\n\t"
"v_mfma_f32_16x16x32_bf16 v[52:55], a[244:247], a[196:199], v[52:55]\n\t"
"v_fma_f32 v158, -v158, s66, v60\n\t"
"v_fma_f32 v159, -v159, s66, v61\n\t"
"v_fma_f32 v60, v62, s52, v160\n\t"
"v_fma_f32 v61, v63, s52, v161\n\t"
"v_mfma_f32_16x16x32_bf16 v[56:59], a[244:247], a[200:203], v[56:59]\n\t"
"v_fma_f32 v160, -v160, s66, v62\n\t"
"v_fma_f32 v62, v64, s52, v162\n\t"
"v_mfma_f32_16x16x32_bf16 v[56:59], a[240:243], a[196:199], v[56:59]\n\t"
"v_fma_f32 v161, -v161, s66, v63\n\t"
"v_fma_f32 v63, v65, s52, v163\n\t"
"v_mfma_f32_16x16x32_bf16 v[56:59], a[236:239], a[192:195], v[56:59]\n\t"
"v_fma_f32 v162, -v162, s66, v64\n\t"
"v_fma_f32 v163, -v163, s66, v65\n\t"
"v_mfma_f32_16x16x32_bf16 v[60:63], a[236:239], a[196:199], v[60:63]\n\t"
"s_waitcnt lgkmcnt(1)\n\t"
"v_fma_f32 v64, v66, s52, v164\n\t"
"v_fma_f32 v65, v67, s52, v165\n\t"
"v_mfma_f32_16x16x32_bf16 v[60:63], a[240:243], a[200:203], v[60:63]\n\t"
"s_waitcnt lgkmcnt(0)\n\t"
"v_fma_f32 v164, -v164, s66, v66\n\t"
"v_fma_f32 v66, v68, s52, v166\n\t"
"v_mfma_f32_16x16x32_bf16 v[60:63], a[244:247], a[204:207], v[60:63]\n\t"
"v_fma_f32 v165, -v165, s66, v67\n\t"
"v_fma_f32 v67, v69, s52, v167\n\t"
"v_fma_f32 v166, -v166, s66, v68\n\t"
"v_fma_f32 v167, -v167, s66, v69\n\t"
"v_mfma_f32_4x4x4_16b_bf16 v[232:235], v[232:233], v[220:221], 0\n\t"
"buffer_load_dwordx4 a[236:239], off, s[80:83], s20 offset:2048 sc1\n\t"
"s_mov_b32 s20, 331776\n\t"
"v_mfma_f32_4x4x4_16b_bf16 v[236:239], v[236:237], v[220:221], 0\n\t"
"s_waitcnt vmcnt(9)\n\t"
"v_fma_f32 v68, v70, s52, v168\n\t"
"v_fma_f32 v69, v71, s52, v169\n\t"
"v_mfma_f32_16x16x32_bf16 v[64:67], a[248:251], a[168:171], v[64:67]\n\t"
"ds_read_b128 a[168:171], v222 offset:1792\n\t"
"v_fma_f32 v168, -v168, s66, v70\n\t"
"v_mfma_f32_16x16x32_bf16 v[64:67], v[240:243], a[176:179], v[64:67]\n\t"
"v_fma_f32 v70, v72, s52, v170\n\t"
"v_fma_f32 v169, -v169, s66, v71\n\t"
"v_mfma_f32_16x16x32_bf16 v[64:67], a[252:255], a[172:175], v[64:67]\n\t"
"buffer_load_dwordx4 a[240:243], off, s[80:83], s20 offset:0 sc1\n\t"
"v_fma_f32 v71, v73, s52, v171\n\t"
"v_fma_f32 v170, -v170, s66, v72\n\t"
"v_fma_f32 v171, -v171, s66, v73\n\t"
"v_fma_f32 v72, v74, s52, v172\n\t"
"v_fma_f32 v73, v75, s52, v173\n\t"
"v_mfma_f32_16x16x32_bf16 v[68:71], a[248:251], a[172:175], v[68:71]\n\t"
"ds_read_b128 a[172:175], v223 offset:7936\n\t"
"v_fma_f32 v172, -v172, s66, v74\n\t"
"v_mfma_f32_16x16x32_bf16 v[68:71], a[252:255], a[176:179], v[68:71]\n\t"
"v_fma_f32 v74, v76, s52, v174\n\t"
"v_fma_f32 v173, -v173, s66, v75\n\t"
"v_mfma_f32_16x16x32_bf16 v[68:71], v[240:243], a[180:183], v[68:71]\n\t"
"buffer_load_dwordx4 a[244:247], off, s[80:83], s20 offset:1024 sc1\n\t"
"v_fma_f32 v75, v77, s52, v175\n\t"
"v_fma_f32 v174, -v174, s66, v76\n\t"
"v_fma_f32 v175, -v175, s66, v77\n\t"
"v_cvt_pk_bf16_f32 v232, v232, v233\n\t"
"v_cvt_pk_bf16_f32 v233, v234, v235\n\t"
"v_fma_f32 v76, v78, s52, v176\n\t"
"v_fma_f32 v77, v79, s52, v177\n\t"
"v_cvt_pk_bf16_f32 v234, v236, v237\n\t"
"v_cvt_pk_bf16_f32 v235, v238, v239\n\t"
"v_mfma_f32_16x16x32_bf16 v[72:75], v[240:243], a[184:187], v[72:75]\n\t"
"v_fma_f32 v176, -v176, s66, v78\n\t"
"v_fma_f32 v78, v80, s52, v178\n\t"
"v_mfma_f32_16x16x32_bf16 v[72:75], a[252:255], a[180:183], v[72:75]\n\t"
"v_fma_f32 v177, -v177, s66, v79\n\t"
"v_fma_f32 v79, v81, s52, v179\n\t"
"v_mfma_f32_16x16x32_bf16 v[72:75], a[248:251], a[176:179], v[72:75]\n\t"
"ds_write_b128 v245, v[232:235] offset:26624\n\t"
"v_fma_f32 v178, -v178, s66, v80\n\t"
"v_fma_f32 v179, -v179, s66, v81\n\t"
"v_fma_f32 v80, v82, s52, v180\n\t"
"v_fma_f32 v81, v83, s52, v181\n\t"
"v_mfma_f32_16x16x32_bf16 v[76:79], a[248:251], a[180:183], v[76:79]\n\t"
"ds_read_b128 a[176:179], v223 offset:14080\n\t"
"v_mfma_f32_16x16x32_bf16 v[76:79], a[252:255], a[184:187], v[76:79]\n\t"
"ds_read_b64_tr_b16 v[228:229], v246 offset:32768\n\t"
"v_fma_f32 v180, -v180, s66, v82\n\t"
"v_mfma_f32_16x16x32_bf16 v[76:79], v[240:243], a[188:191], v[76:79]\n\t"
"ds_read_b64_tr_b16 v[224:225], v255 offset:32768\n\t"
"v_fma_f32 v82, v84, s52, v182\n\t"
"v_fma_f32 v181, -v181, s66, v83\n\t"
"v_fma_f32 v83, v85, s52, v183\n\t"
"v_fma_f32 v182, -v182, s66, v84\n\t"
"v_fma_f32 v183, -v183, s66, v85\n\t"
"v_fma_f32 v84, v86, s52, v184\n\t"
"v_fma_f32 v85, v87, s52, v185\n\t"
"v_mfma_f32_16x16x32_bf16 v[80:83], v[240:243], a[192:195], v[80:83]\n\t"
"ds_read_b128 a[180:183], v222 offset:21760\n\t"
"v_fma_f32 v184, -v184, s66, v86\n\t"
"v_mfma_f32_16x16x32_bf16 v[80:83], a[252:255], a[188:191], v[80:83]\n\t"
"ds_read_b64_tr_b16 v[236:237], v246 offset:40448\n\t"
"v_fma_f32 v86, v88, s52, v186\n\t"
"v_mfma_f32_16x16x32_bf16 v[80:83], a[248:251], a[184:187], v[80:83]\n\t"
"ds_read_b64_tr_b16 v[232:233], v255 offset:40448\n\t"
"v_fma_f32 v185, -v185, s66, v87\n\t"
"v_fma_f32 v87, v89, s52, v187\n\t"
"v_fma_f32 v186, -v186, s66, v88\n\t"
"v_fma_f32 v187, -v187, s66, v89\n\t"
"v_fma_f32 v88, v90, s52, v188\n\t"
"v_fma_f32 v89, v91, s52, v189\n\t"
"v_mfma_f32_16x16x32_bf16 v[84:87], a[248:251], a[188:191], v[84:87]\n\t"
"ds_read_b128 a[184:187], v223 offset:27904\n\t"
"v_fma_f32 v188, -v188, s66, v90\n\t"
"v_mfma_f32_16x16x32_bf16 v[84:87], a[252:255], a[192:195], v[84:87]\n\t"
"ds_read_b128 a[188:191], v223 offset:34048\n\t"
"v_fma_f32 v90, v92, s52, v190\n\t"
"v_mfma_f32_16x16x32_bf16 v[84:87], v[240:243], a[196:199], v[84:87]\n\t"
"v_fma_f32 v189, -v189, s66, v91\n\t"
"v_fma_f32 v91, v93, s52, v191\n\t"
"v_fma_f32 v190, -v190, s66, v92\n\t"
"v_fma_f32 v191, -v191, s66, v93\n\t"
"v_fma_f32 v92, v94, s52, v192\n\t"
"v_fma_f32 v93, v95, s52, v193\n\t"
"v_mfma_f32_16x16x32_bf16 v[88:91], v[240:243], a[200:203], v[88:91]\n\t"
"v_fma_f32 v192, -v192, s66, v94\n\t"
"v_fma_f32 v94, v96, s52, v194\n\t"
"v_mfma_f32_16x16x32_bf16 v[88:91], a[252:255], a[196:199], v[88:91]\n\t"
"v_fma_f32 v193, -v193, s66, v95\n\t"
"v_fma_f32 v95, v97, s52, v195\n\t"
"v_mfma_f32_16x16x32_bf16 v[88:91], a[248:251], a[192:195], v[88:91]\n\t"
"ds_read_b128 a[192:195], v222 offset:41728\n\t"
"v_fma_f32 v194, -v194, s66, v96\n\t"
"v_fma_f32 v195, -v195, s66, v97\n\t"
"v_mfma_f32_16x16x32_bf16 v[92:95], a[248:251], a[196:199], v[92:95]\n\t"
"s_waitcnt lgkmcnt(7)\n\t"
"ds_read_b128 a[196:199], v223 offset:47872\n\t"
"v_mfma_f32_16x16x32_bf16 v[92:95], a[252:255], a[200:203], v[92:95]\n\t"
"buffer_load_dwordx4 v[96:99], off, s[80:83], s20 offset:2048 sc1\n\t"
"v_mfma_f32_16x16x32_bf16 v[92:95], v[240:243], a[204:207], v[92:95]\n\t"
"s_waitcnt lgkmcnt(5)\n\t"
"ds_read_b128 a[200:203], v223 offset:54016\n\t"
"s_mov_b32 s20, 344064\n\t"
"v_mfma_f32_4x4x4_16b_bf16 v[228:231], v[228:229], v[220:221], 0\n\t"
"s_waitcnt lgkmcnt(5)\n\t"
"v_mfma_f32_4x4x4_16b_bf16 v[224:227], v[224:225], v[220:221], 0\n\t"
"buffer_load_dwordx4 a[248:251], off, s[80:83], s20 offset:0 sc1\n\t"
"v_mfma_f32_4x4x4_16b_bf16 v[236:239], v[236:237], v[220:221], 0\n\t"
"s_waitcnt vmcnt(10)\n\t"
"v_mfma_f32_4x4x4_16b_bf16 v[232:235], v[232:233], v[220:221], 0\n\t"
"v_mfma_f32_16x16x32_bf16 v[100:103], a[208:211], a[168:171], v[100:103]\n\t"
"buffer_load_dwordx4 v[240:243], off, s[80:83], s20 offset:1024 sc1\n\t"
"v_mfma_f32_16x16x32_bf16 v[100:103], a[212:215], a[176:179], v[100:103]\n\t"
"ds_read_b128 a[204:207], v222 offset:61696\n\t"
"v_cvt_pk_bf16_f32 v228, v228, v229\n\t"
"v_mfma_f32_16x16x32_bf16 v[100:103], v[248:251], a[172:175], v[100:103]\n\t"
"s_add_u32 m0, s24, 896\n\t"
"s_add_u32 s28, s21, 0\n\t"
"v_cvt_pk_bf16_f32 v229, v230, v231\n\t"
"v_mfma_f32_16x16x32_bf16 v[104:107], a[208:211], a[172:175], v[104:107]\n\t"
"s_waitcnt lgkmcnt(5)\n\t"
"v_cvt_pk_bf16_f32 v230, v224, v225\n\t"
"v_cvt_pk_bf16_f32 v231, v226, v227\n\t"
"v_mfma_f32_16x16x32_bf16 v[104:107], v[248:251], a[176:179], v[104:107]\n\t"
"v_cvt_pk_bf16_f32 v236, v236, v237\n\t"
"v_mfma_f32_16x16x32_bf16 v[104:107], a[212:215], a[180:183], v[104:107]\n\t"
"s_waitcnt lgkmcnt(0)\n\t"
"s_barrier\n\t"
"v_mfma_f32_16x16x32_bf16 v[108:111], a[212:215], a[184:187], v[108:111]\n\t"
"v_cvt_pk_bf16_f32 v237, v238, v239\n\t"
"v_mfma_f32_16x16x32_bf16 v[108:111], v[248:251], a[180:183], v[108:111]\n\t"
"buffer_load_dwordx4 a[252:255], off, s[80:83], s20 offset:2048 sc1\n\t"
"v_mfma_f32_16x16x32_bf16 v[108:111], a[208:211], a[176:179], v[108:111]\n\t"
"ds_write_b128 v245, v[228:231] offset:32768\n\t"
"v_cvt_pk_bf16_f32 v238, v232, v233\n\t"
"v_cvt_pk_bf16_f32 v239, v234, v235\n\t"
"v_mfma_f32_16x16x32_bf16 v[112:115], a[208:211], a[180:183], v[112:115]\n\t"
"v_mfma_f32_16x16x32_bf16 v[112:115], v[248:251], a[184:187], v[112:115]\n\t"
"buffer_load_dwordx4 v247, s[12:15], s28 offen offset:128 nt lds\n\t"
"v_mfma_f32_16x16x32_bf16 v[112:115], a[212:215], a[188:191], v[112:115]\n\t"
"s_add_u32 m0, s24, 7040\n\t"
"s_add_u32 s28, s21, 196608\n\t"
"ds_write_b128 v245, v[236:239] offset:40448\n\t"
"v_mfma_f32_16x16x32_bf16 v[116:119], a[212:215], a[192:195], v[116:119]\n\t"
"buffer_load_dwordx4 v247, s[12:15], s28 offen offset:128 nt lds\n\t"
"v_mfma_f32_16x16x32_bf16 v[116:119], v[248:251], a[188:191], v[116:119]\n\t"
"s_add_u32 m0, s24, 13184\n\t"
"s_add_u32 s28, s21, 393216\n\t"
"ds_read_b64_tr_b16 v[224:225], v246 offset:46592\n\t"
"v_mfma_f32_16x16x32_bf16 v[116:119], a[208:211], a[184:187], v[116:119]\n\t"
"buffer_load_dwordx4 v247, s[12:15], s28 offen offset:128 nt lds\n\t"
"s_add_u32 m0, s24, 20864\n\t"
"s_add_u32 s28, s21, 589824\n\t"
"v_mfma_f32_16x16x32_bf16 v[120:123], a[208:211], a[188:191], v[120:123]\n\t"
"buffer_load_dwordx4 v247, s[12:15], s28 offen offset:128 nt lds\n\t"
"v_mfma_f32_16x16x32_bf16 v[120:123], v[248:251], a[192:195], v[120:123]\n\t"
"s_add_u32 m0, s24, 27008\n\t"
"s_add_u32 s28, s21, 786432\n\t"
"ds_read_b64_tr_b16 v[232:233], v255 offset:46592\n\t"
"v_mfma_f32_16x16x32_bf16 v[120:123], a[212:215], a[196:199], v[120:123]\n\t"
"buffer_load_dwordx4 v247, s[12:15], s28 offen offset:128 nt lds\n\t"
"s_add_u32 m0, s24, 33152\n\t"
"s_add_u32 s28, s21, 983040\n\t"
"v_mfma_f32_16x16x32_bf16 v[124:127], a[212:215], a[200:203], v[124:127]\n\t"
"buffer_load_dwordx4 v247, s[12:15], s28 offen offset:128 nt lds\n\t"
"v_mfma_f32_16x16x32_bf16 v[124:127], v[248:251], a[196:199], v[124:127]\n\t"
"ds_read_b64_tr_b16 v[228:229], v246 offset:52736\n\t"
"v_mfma_f32_16x16x32_bf16 v[124:127], a[208:211], a[192:195], v[124:127]\n\t"
"ds_read_b64_tr_b16 v[236:237], v255 offset:52736\n\t"
"v_mfma_f32_16x16x32_bf16 v[128:131], a[208:211], a[196:199], v[128:131]\n\t"
"s_waitcnt lgkmcnt(3)\n\t"
"v_mfma_f32_16x16x32_bf16 v[128:131], v[248:251], a[200:203], v[128:131]\n\t"
"s_waitcnt lgkmcnt(2)\n\t"
"v_mfma_f32_16x16x32_bf16 v[128:131], a[212:215], a[204:207], v[128:131]\n\t"
"v_mfma_f32_4x4x4_16b_bf16 v[224:227], v[224:225], v[220:221], 0\n\t"
"s_waitcnt lgkmcnt(1)\n\t"
"v_mfma_f32_4x4x4_16b_bf16 v[232:235], v[232:233], v[220:221], 0\n\t"
"s_waitcnt vmcnt(15) lgkmcnt(0)\n\t"
"v_mfma_f32_4x4x4_16b_bf16 v[228:231], v[228:229], v[220:221], 0\n\t"
"v_mfma_f32_4x4x4_16b_bf16 v[236:239], v[236:237], v[220:221], 0\n\t"
"v_mfma_f32_16x16x32_bf16 v[132:135], a[216:219], a[168:171], v[132:135]\n\t"
"v_cvt_pk_bf16_f32 v224, v224, v225\n\t"
"v_cvt_pk_bf16_f32 v225, v226, v227\n\t"
"v_mfma_f32_16x16x32_bf16 v[132:135], a[224:227], a[176:179], v[132:135]\n\t"
"v_cvt_pk_bf16_f32 v226, v232, v233\n\t"
"v_cvt_pk_bf16_f32 v227, v234, v235\n\t"
"v_mfma_f32_16x16x32_bf16 v[132:135], a[220:223], a[172:175], v[132:135]\n\t"
"ds_write_b128 v245, v[224:227] offset:46592\n\t"
"v_mfma_f32_16x16x32_bf16 v[136:139], a[216:219], a[172:175], v[136:139]\n\t"
"v_cvt_pk_bf16_f32 v228, v228, v229\n\t"
"v_cvt_pk_bf16_f32 v229, v230, v231\n\t"
"v_mfma_f32_16x16x32_bf16 v[136:139], a[220:223], a[176:179], v[136:139]\n\t"
"v_cvt_pk_bf16_f32 v230, v236, v237\n\t"
"v_cvt_pk_bf16_f32 v231, v238, v239\n\t"
"v_mfma_f32_16x16x32_bf16 v[136:139], a[224:227], a[180:183], v[136:139]\n\t"
"ds_write_b128 v245, v[228:231] offset:52736\n\t"
"v_mfma_f32_16x16x32_bf16 v[140:143], a[224:227], a[184:187], v[140:143]\n\t"
"s_mov_b32 s20, 356352\n\t"
"v_mfma_f32_16x16x32_bf16 v[140:143], a[220:223], a[180:183], v[140:143]\n\t"
"v_mfma_f32_16x16x32_bf16 v[140:143], a[216:219], a[176:179], v[140:143]\n\t"
"v_mfma_f32_16x16x32_bf16 v[144:147], a[216:219], a[180:183], v[144:147]\n\t"
"v_mfma_f32_16x16x32_bf16 v[144:147], a[220:223], a[184:187], v[144:147]\n\t"
"v_mfma_f32_16x16x32_bf16 v[144:147], a[224:227], a[188:191], v[144:147]\n\t"
"v_mfma_f32_16x16x32_bf16 v[148:151], a[224:227], a[192:195], v[148:151]\n\t"
"buffer_load_dwordx4 a[208:211], off, s[80:83], s20 offset:0 sc1\n\t"
"v_mfma_f32_16x16x32_bf16 v[148:151], a[220:223], a[188:191], v[148:151]\n\t"
"v_mfma_f32_16x16x32_bf16 v[148:151], a[216:219], a[184:187], v[148:151]\n\t"
"v_mfma_f32_16x16x32_bf16 v[152:155], a[216:219], a[188:191], v[152:155]\n\t"
"v_mfma_f32_16x16x32_bf16 v[152:155], a[220:223], a[192:195], v[152:155]\n\t"
"v_mfma_f32_16x16x32_bf16 v[152:155], a[224:227], a[196:199], v[152:155]\n\t"
"v_mfma_f32_16x16x32_bf16 v[156:159], a[224:227], a[200:203], v[156:159]\n\t"
"v_mfma_f32_16x16x32_bf16 v[156:159], a[220:223], a[196:199], v[156:159]\n\t"
"v_mfma_f32_16x16x32_bf16 v[156:159], a[216:219], a[192:195], v[156:159]\n\t"
"v_mfma_f32_16x16x32_bf16 v[160:163], a[216:219], a[196:199], v[160:163]\n\t"
"s_waitcnt vmcnt(13)\n\t"
"v_mfma_f32_16x16x32_bf16 v[160:163], a[220:223], a[200:203], v[160:163]\n\t"
"v_mfma_f32_16x16x32_bf16 v[160:163], a[224:227], a[204:207], v[160:163]\n\t"
"v_mfma_f32_16x16x32_bf16 v[164:167], a[228:231], a[168:171], v[164:167]\n\t"
"v_mfma_f32_16x16x32_bf16 v[164:167], a[236:239], a[176:179], v[164:167]\n\t"
"v_mfma_f32_16x16x32_bf16 v[164:167], a[232:235], a[172:175], v[164:167]\n\t"
"s_waitcnt lgkmcnt(0)\n\t"
"s_barrier\n\t"
"v_mfma_f32_16x16x32_bf16 v[168:171], a[228:231], a[172:175], v[168:171]\n\t"
"ds_read_b128 a[168:171], v222 offset:2048\n\t"
"v_mfma_f32_16x16x32_bf16 v[168:171], a[232:235], a[176:179], v[168:171]\n\t"
"ds_read_b128 a[172:175], v223 offset:8192\n\t"
"v_mfma_f32_16x16x32_bf16 v[168:171], a[236:239], a[180:183], v[168:171]\n\t"
"v_mfma_f32_16x16x32_bf16 v[172:175], a[236:239], a[184:187], v[172:175]\n\t"
"v_mfma_f32_16x16x32_bf16 v[172:175], a[232:235], a[180:183], v[172:175]\n\t"
"v_mfma_f32_16x16x32_bf16 v[172:175], a[228:231], a[176:179], v[172:175]\n\t"
"ds_read_b128 a[176:179], v223 offset:14336\n\t"
"v_mfma_f32_16x16x32_bf16 v[176:179], a[228:231], a[180:183], v[176:179]\n\t"
"ds_read_b128 a[180:183], v222 offset:22016\n\t"
"v_mfma_f32_16x16x32_bf16 v[176:179], a[232:235], a[184:187], v[176:179]\n\t"
"v_mfma_f32_16x16x32_bf16 v[176:179], a[236:239], a[188:191], v[176:179]\n\t"
"v_mfma_f32_16x16x32_bf16 v[180:183], a[236:239], a[192:195], v[180:183]\n\t"
"v_mfma_f32_16x16x32_bf16 v[180:183], a[232:235], a[188:191], v[180:183]\n\t"
"v_mfma_f32_16x16x32_bf16 v[180:183], a[228:231], a[184:187], v[180:183]\n\t"
"ds_read_b128 a[184:187], v223 offset:28160\n\t"
"v_mfma_f32_16x16x32_bf16 v[184:187], a[228:231], a[188:191], v[184:187]\n\t"
"ds_read_b128 a[188:191], v223 offset:34304\n\t"
"v_mfma_f32_16x16x32_bf16 v[184:187], a[232:235], a[192:195], v[184:187]\n\t"
"v_mfma_f32_16x16x32_bf16 v[184:187], a[236:239], a[196:199], v[184:187]\n\t"
"v_mfma_f32_16x16x32_bf16 v[188:191], a[236:239], a[200:203], v[188:191]\n\t"
"v_mfma_f32_16x16x32_bf16 v[188:191], a[232:235], a[196:199], v[188:191]\n\t"
"v_mfma_f32_16x16x32_bf16 v[188:191], a[228:231], a[192:195], v[188:191]\n\t"
"ds_read_b128 a[192:195], v222 offset:41984\n\t"
"v_mfma_f32_16x16x32_bf16 v[192:195], a[228:231], a[196:199], v[192:195]\n\t"
"buffer_load_dwordx4 v[236:239], off, s[80:83], s20 offset:1024 sc1\n\t"
"v_mfma_f32_16x16x32_bf16 v[192:195], a[232:235], a[200:203], v[192:195]\n\t"
"s_waitcnt vmcnt(11) lgkmcnt(4)\n\t"
"ds_read_b128 a[196:199], v223 offset:48128\n\t"
"v_mfma_f32_16x16x32_bf16 v[192:195], a[236:239], a[204:207], v[192:195]\n\t"
"ds_read_b128 a[200:203], v223 offset:54272\n\t"
"Lst4_%=:\n\t"
"v_mfma_f32_16x16x32_bf16 v[0:3], a[240:243], a[168:171], v[0:3]\n\t"
"buffer_load_dwordx4 v[248:251], off, s[80:83], s20 offset:2048 sc1\n\t"
"v_mfma_f32_16x16x32_bf16 v[0:3], v[96:99], a[176:179], v[0:3]\n\t"
"s_waitcnt lgkmcnt(5)\n\t"
"ds_read_b128 a[204:207], v222 offset:61952\n\t"
"s_mov_b32 s20, 368640\n\t"
"v_mfma_f32_16x16x32_bf16 v[0:3], a[244:247], a[172:175], v[0:3]\n\t"
"v_mfma_f32_16x16x32_bf16 v[4:7], a[240:243], a[172:175], v[4:7]\n\t"
"s_waitcnt lgkmcnt(5)\n\t"
"v_mfma_f32_16x16x32_bf16 v[4:7], a[244:247], a[176:179], v[4:7]\n\t"
"buffer_load_dwordx4 a[212:215], off, s[80:83], s20 offset:0 sc1\n\t"
"v_mfma_f32_16x16x32_bf16 v[4:7], v[96:99], a[180:183], v[4:7]\n\t"
"v_mfma_f32_16x16x32_bf16 v[8:11], v[96:99], a[184:187], v[8:11]\n\t"
"s_waitcnt lgkmcnt(4)\n\t"
"v_mfma_f32_16x16x32_bf16 v[8:11], a[244:247], a[180:183], v[8:11]\n\t"
"v_mfma_f32_16x16x32_bf16 v[8:11], a[240:243], a[176:179], v[8:11]\n\t"
"buffer_load_dwordx4 a[216:219], off, s[80:83], s20 offset:1024 sc1\n\t"
"v_mfma_f32_16x16x32_bf16 v[12:15], a[240:243], a[180:183], v[12:15]\n\t"
"s_waitcnt lgkmcnt(3)\n\t"
"v_mfma_f32_16x16x32_bf16 v[12:15], a[244:247], a[184:187], v[12:15]\n\t"
"v_mfma_f32_16x16x32_bf16 v[12:15], v[96:99], a[188:191], v[12:15]\n\t"
"v_mfma_f32_16x16x32_bf16 v[16:19], v[96:99], a[192:195], v[16:19]\n\t"
"buffer_load_dwordx4 a[220:223], off, s[80:83], s20 offset:2048 sc1\n\t"
"v_mfma_f32_16x16x32_bf16 v[16:19], a[244:247], a[188:191], v[16:19]\n\t"
"s_waitcnt lgkmcnt(2)\n\t"
"s_mov_b32 s20, 380928\n\t"
"v_mfma_f32_16x16x32_bf16 v[16:19], a[240:243], a[184:187], v[16:19]\n\t"
"v_mfma_f32_16x16x32_bf16 v[20:23], a[240:243], a[188:191], v[20:23]\n\t"
"s_waitcnt lgkmcnt(1)\n\t"
"v_mfma_f32_16x16x32_bf16 v[20:23], a[244:247], a[192:195], v[20:23]\n\t"
"buffer_load_dwordx4 a[224:227], off, s[80:83], s20 offset:0 sc1\n\t"
"v_mfma_f32_16x16x32_bf16 v[20:23], v[96:99], a[196:199], v[20:23]\n\t"
"s_waitcnt vmcnt(13) lgkmcnt(0)\n\t"
"v_mfma_f32_16x16x32_bf16 v[24:27], v[96:99], a[200:203], v[24:27]\n\t"
"v_mfma_f32_16x16x32_bf16 v[24:27], a[244:247], a[196:199], v[24:27]\n\t"
"v_mfma_f32_16x16x32_bf16 v[24:27], a[240:243], a[192:195], v[24:27]\n\t"
"Lpg4_%=:\n\t"
"s_cbranch_vccnz Lpg8_%=\n\t"
"v_mfma_f32_16x16x32_bf16 v[28:31], a[240:243], a[196:199], v[28:31]\n\t"
"buffer_load_dwordx4 a[228:231], off, s[80:83], s20 offset:1024 sc1\n\t"
"v_mfma_f32_16x16x32_bf16 v[28:31], a[244:247], a[200:203], v[28:31]\n\t"
"v_mfma_f32_16x16x32_bf16 v[28:31], v[96:99], a[204:207], v[28:31]\n\t"
"v_mfma_f32_16x16x32_bf16 v[32:35], a[248:251], a[168:171], v[32:35]\n\t"
"buffer_load_dwordx4 v[96:99], off, s[80:83], s20 offset:2048 sc1\n\t"
"v_mfma_f32_16x16x32_bf16 v[32:35], a[252:255], a[176:179], v[32:35]\n\t"
"s_mov_b32 s20, 393216\n\t"
"v_mfma_f32_16x16x32_bf16 v[32:35], v[240:243], a[172:175], v[32:35]\n\t"
"v_mfma_f32_16x16x32_bf16 v[36:39], a[248:251], a[172:175], v[36:39]\n\t"
"v_mfma_f32_16x16x32_bf16 v[36:39], v[240:243], a[176:179], v[36:39]\n\t"
"buffer_load_dwordx4 a[232:235], off, s[80:83], s20 offset:0 sc1\n\t"
"v_mfma_f32_16x16x32_bf16 v[36:39], a[252:255], a[180:183], v[36:39]\n\t"
"v_mfma_f32_16x16x32_bf16 v[40:43], a[252:255], a[184:187], v[40:43]\n\t"
"v_mfma_f32_16x16x32_bf16 v[40:43], v[240:243], a[180:183], v[40:43]\n\t"
"v_mfma_f32_16x16x32_bf16 v[40:43], a[248:251], a[176:179], v[40:43]\n\t"
"buffer_load_dwordx4 a[236:239], off, s[80:83], s20 offset:1024 sc1\n\t"
"v_mfma_f32_16x16x32_bf16 v[44:47], a[248:251], a[180:183], v[44:47]\n\t"
"v_mfma_f32_16x16x32_bf16 v[44:47], v[240:243], a[184:187], v[44:47]\n\t"
"v_mfma_f32_16x16x32_bf16 v[44:47], a[252:255], a[188:191], v[44:47]\n\t"
"v_mfma_f32_16x16x32_bf16 v[48:51], a[252:255], a[192:195], v[48:51]\n\t"
"buffer_load_dwordx4 a[240:243], off, s[80:83], s20 offset:2048 sc1\n\t"
"v_mfma_f32_16x16x32_bf16 v[48:51], v[240:243], a[188:191], v[48:51]\n\t"
"s_mov_b32 s20, 405504\n\t"
"v_mfma_f32_16x16x32_bf16 v[48:51], a[248:251], a[184:187], v[48:51]\n\t"
"v_mfma_f32_16x16x32_bf16 v[52:55], a[248:251], a[188:191], v[52:55]\n\t"
"v_mfma_f32_16x16x32_bf16 v[52:55], v[240:243], a[192:195], v[52:55]\n\t"
"buffer_load_dwordx4 a[244:247], off, s[80:83], s20 offset:0 sc1\n\t"
"v_mfma_f32_16x16x32_bf16 v[52:55], a[252:255], a[196:199], v[52:55]\n\t"
"v_mfma_f32_16x16x32_bf16 v[56:59], a[252:255], a[200:203], v[56:59]\n\t"
"v_mfma_f32_16x16x32_bf16 v[56:59], v[240:243], a[196:199], v[56:59]\n\t"
"v_mfma_f32_16x16x32_bf16 v[56:59], a[248:251], a[192:195], v[56:59]\n\t"
"v_mfma_f32_16x16x32_bf16 v[60:63], a[248:251], a[196:199], v[60:63]\n\t"
"buffer_load_dwordx4 a[248:251], off, s[80:83], s20 offset:1024 sc1\n\t"
"v_mfma_f32_16x16x32_bf16 v[60:63], v[240:243], a[200:203], v[60:63]\n\t"
"s_waitcnt vmcnt(11)\n\t"
"v_mfma_f32_16x16x32_bf16 v[60:63], a[252:255], a[204:207], v[60:63]\n\t"
"v_mfma_f32_16x16x32_bf16 v[64:67], a[208:211], a[168:171], v[64:67]\n\t"
"buffer_load_dwordx4 a[252:255], off, s[80:83], s20 offset:2048 sc1\n\t"
"v_mfma_f32_16x16x32_bf16 v[64:67], v[248:251], a[176:179], v[64:67]\n\t"
"ds_read_b128 a[168:171], v222 offset:2304\n\t"
"v_mfma_f32_16x16x32_bf16 v[64:67], v[236:239], a[172:175], v[64:67]\n\t"
"v_mfma_f32_16x16x32_bf16 v[68:71], a[208:211], a[172:175], v[68:71]\n\t"
"ds_read_b128 a[172:175], v223 offset:8448\n\t"
"v_mfma_f32_16x16x32_bf16 v[68:71], v[236:239], a[176:179], v[68:71]\n\t"
"v_mfma_f32_16x16x32_bf16 v[68:71], v[248:251], a[180:183], v[68:71]\n\t"
"v_mfma_f32_16x16x32_bf16 v[72:75], v[248:251], a[184:187], v[72:75]\n\t"
"v_mfma_f32_16x16x32_bf16 v[72:75], v[236:239], a[180:183], v[72:75]\n\t"
"v_mfma_f32_16x16x32_bf16 v[72:75], a[208:211], a[176:179], v[72:75]\n\t"
"ds_read_b128 a[176:179], v223 offset:14592\n\t"
"v_mfma_f32_16x16x32_bf16 v[76:79], a[208:211], a[180:183], v[76:79]\n\t"
"ds_read_b128 a[180:183], v222 offset:22272\n\t"
"v_mfma_f32_16x16x32_bf16 v[76:79], v[236:239], a[184:187], v[76:79]\n\t"
"v_mfma_f32_16x16x32_bf16 v[76:79], v[248:251], a[188:191], v[76:79]\n\t"
"v_mfma_f32_16x16x32_bf16 v[80:83], v[248:251], a[192:195], v[80:83]\n\t"
"v_mfma_f32_16x16x32_bf16 v[80:83], v[236:239], a[188:191], v[80:83]\n\t"
"v_mfma_f32_16x16x32_bf16 v[80:83], a[208:211], a[184:187], v[80:83]\n\t"
"ds_read_b128 a[184:187], v223 offset:28416\n\t"
"v_mfma_f32_16x16x32_bf16 v[84:87], a[208:211], a[188:191], v[84:87]\n\t"
"ds_read_b128 a[188:191], v223 offset:34560\n\t"
"v_mfma_f32_16x16x32_bf16 v[84:87], v[236:239], a[192:195], v[84:87]\n\t"
"v_mfma_f32_16x16x32_bf16 v[84:87], v[248:251], a[196:199], v[84:87]\n\t"
"s_mov_b32 s20, 417792\n\t"
"v_mfma_f32_16x16x32_bf16 v[88:91], v[248:251], a[200:203], v[88:91]\n\t"
"v_mfma_f32_16x16x32_bf16 v[88:91], v[236:239], a[196:199], v[88:91]\n\t"
"v_mfma_f32_16x16x32_bf16 v[88:91], a[208:211], a[192:195], v[88:91]\n\t"
"ds_read_b128 a[192:195], v222 offset:42240\n\t"
"v_mfma_f32_16x16x32_bf16 v[92:95], a[208:211], a[196:199], v[92:95]\n\t"
"s_waitcnt vmcnt(9) lgkmcnt(4)\n\t"
"ds_read_b128 a[196:199], v223 offset:48384\n\t"
"v_mfma_f32_16x16x32_bf16 v[92:95], v[236:239], a[200:203], v[92:95]\n\t"
"global_load_dwordx4 v[240:243], v252, s[48:49] offset:2048\n\t"
"v_mfma_f32_16x16x32_bf16 v[92:95], v[248:251], a[204:207], v[92:95]\n\t"
"buffer_load_dwordx4 v[248:251], off, s[80:83], s20 offset:0 sc1\n\t"
"v_mfma_f32_16x16x32_bf16 v[100:103], a[212:215], a[168:171], v[100:103]\n\t"
"s_waitcnt lgkmcnt(4)\n\t"
"ds_read_b128 a[200:203], v223 offset:54528\n\t"
"v_mfma_f32_16x16x32_bf16 v[100:103], a[220:223], a[176:179], v[100:103]\n\t"
"ds_read_b128 a[204:207], v222 offset:62208\n\t"
"v_mfma_f32_16x16x32_bf16 v[100:103], a[216:219], a[172:175], v[100:103]\n\t"
"v_mfma_f32_16x16x32_bf16 v[104:107], a[212:215], a[172:175], v[104:107]\n\t"
"s_waitcnt lgkmcnt(5)\n\t"
"v_mfma_f32_16x16x32_bf16 v[104:107], a[216:219], a[176:179], v[104:107]\n\t"
"global_load_dwordx4 a[208:211], v252, s[46:47] offset:1024\n\t"
"v_mfma_f32_16x16x32_bf16 v[104:107], a[220:223], a[180:183], v[104:107]\n\t"
"v_mfma_f32_16x16x32_bf16 v[108:111], a[220:223], a[184:187], v[108:111]\n\t"
"s_waitcnt lgkmcnt(4)\n\t"
"v_mfma_f32_16x16x32_bf16 v[108:111], a[216:219], a[180:183], v[108:111]\n\t"
"v_mfma_f32_16x16x32_bf16 v[108:111], a[212:215], a[176:179], v[108:111]\n\t"
"v_mfma_f32_16x16x32_bf16 v[112:115], a[212:215], a[180:183], v[112:115]\n\t"
"s_waitcnt lgkmcnt(3)\n\t"
"v_mfma_f32_16x16x32_bf16 v[112:115], a[216:219], a[184:187], v[112:115]\n\t"
"v_mfma_f32_16x16x32_bf16 v[112:115], a[220:223], a[188:191], v[112:115]\n\t"
"v_mfma_f32_16x16x32_bf16 v[116:119], a[220:223], a[192:195], v[116:119]\n\t"
"s_waitcnt lgkmcnt(2)\n\t"
"v_mfma_f32_16x16x32_bf16 v[116:119], a[216:219], a[188:191], v[116:119]\n\t"
"v_mfma_f32_16x16x32_bf16 v[116:119], a[212:215], a[184:187], v[116:119]\n\t"
"v_mfma_f32_16x16x32_bf16 v[120:123], a[212:215], a[188:191], v[120:123]\n\t"
"s_waitcnt lgkmcnt(1)\n\t"
"v_mfma_f32_16x16x32_bf16 v[120:123], a[216:219], a[192:195], v[120:123]\n\t"
"v_mfma_f32_16x16x32_bf16 v[120:123], a[220:223], a[196:199], v[120:123]\n\t"
"v_mfma_f32_16x16x32_bf16 v[124:127], a[220:223], a[200:203], v[124:127]\n\t"
"s_waitcnt lgkmcnt(0)\n\t"
"v_mfma_f32_16x16x32_bf16 v[124:127], a[216:219], a[196:199], v[124:127]\n\t"
"s_add_u32 m0, s24, 42368\n\t"
"s_add_u32 s28, s21, 1182720\n\t"
"v_mfma_f32_16x16x32_bf16 v[124:127], a[212:215], a[192:195], v[124:127]\n\t"
"v_mfma_f32_16x16x32_bf16 v[128:131], a[212:215], a[196:199], v[128:131]\n\t"
"buffer_load_dwordx4 a[212:215], off, s[80:83], s20 offset:1024 sc1\n\t"
"v_mfma_f32_16x16x32_bf16 v[128:131], a[216:219], a[200:203], v[128:131]\n\t"
"s_waitcnt vmcnt(10)\n\t"
"v_mfma_f32_16x16x32_bf16 v[128:131], a[220:223], a[204:207], v[128:131]\n\t"
"buffer_load_dwordx4 v247, s[12:15], s28 offen offset:128 nt lds\n\t"
"v_mfma_f32_16x16x32_bf16 v[132:135], a[224:227], a[168:171], v[132:135]\n\t"
"s_add_u32 m0, s24, 48512\n\t"
"s_add_u32 s28, s21, 1379328\n\t"
"v_mfma_f32_16x16x32_bf16 v[132:135], v[96:99], a[176:179], v[132:135]\n\t"
"buffer_load_dwordx4 v247, s[12:15], s28 offen offset:128 nt lds\n\t"
"v_mfma_f32_16x16x32_bf16 v[132:135], a[228:231], a[172:175], v[132:135]\n\t"
"s_add_u32 m0, s24, 54656\n\t"
"s_add_u32 s28, s21, 1575936\n\t"
"v_mfma_f32_16x16x32_bf16 v[136:139], a[224:227], a[172:175], v[136:139]\n\t"
"buffer_load_dwordx4 v247, s[12:15], s28 offen offset:128 nt lds\n\t"
"v_mfma_f32_16x16x32_bf16 v[136:139], a[228:231], a[176:179], v[136:139]\n\t"
"s_add_u32 m0, s24, 40832\n\t"
"s_add_u32 s28, s21, 1179648\n\t"
"v_mfma_f32_16x16x32_bf16 v[136:139], v[96:99], a[180:183], v[136:139]\n\t"
"s_barrier\n\t"
"v_mfma_f32_16x16x32_bf16 v[140:143], v[96:99], a[184:187], v[140:143]\n\t"
"v_mfma_f32_16x16x32_bf16 v[140:143], a[228:231], a[180:183], v[140:143]\n\t"
"buffer_load_dwordx4 v247, s[12:15], s28 offen offset:128 nt lds\n\t"
"v_mfma_f32_16x16x32_bf16 v[140:143], a[224:227], a[176:179], v[140:143]\n\t"
"s_add_u32 m0, s24, 46976\n\t"
"s_add_u32 s28, s21, 1376256\n\t"
"s_waitcnt vmcnt(7)\n\t"
"ds_read_b64_tr_b16 v[232:233], v240 offset:1024\n\t"
"v_mfma_f32_16x16x32_bf16 v[144:147], a[224:227], a[180:183], v[144:147]\n\t"
"buffer_load_dwordx4 v247, s[12:15], s28 offen offset:128 nt lds\n\t"
"v_mfma_f32_16x16x32_bf16 v[144:147], a[228:231], a[184:187], v[144:147]\n\t"
"s_add_u32 m0, s24, 53120\n\t"
"s_add_u32 s28, s21, 1572864\n\t"
"ds_read_b64_tr_b16 v[234:235], v240 offset:2560\n\t"
"v_mfma_f32_16x16x32_bf16 v[144:147], v[96:99], a[188:191], v[144:147]\n\t"
"buffer_load_dwordx4 v247, s[12:15], s28 offen offset:128 nt lds\n\t"
"v_mfma_f32_16x16x32_bf16 v[148:151], v[96:99], a[192:195], v[148:151]\n\t"
"ds_read_b64_tr_b16 v[236:237], v241 offset:1024\n\t"
"v_mfma_f32_16x16x32_bf16 v[148:151], a[228:231], a[188:191], v[148:151]\n\t"
"ds_read_b64_tr_b16 v[238:239], v241 offset:2560\n\t"
"v_mfma_f32_16x16x32_bf16 v[148:151], a[224:227], a[184:187], v[148:151]\n\t"
"ds_read_b64_tr_b16 v[224:225], v240 offset:20992\n\t"
"v_mfma_f32_16x16x32_bf16 v[152:155], a[224:227], a[188:191], v[152:155]\n\t"
"ds_read_b64_tr_b16 v[226:227], v240 offset:22528\n\t"
"v_mfma_f32_16x16x32_bf16 v[152:155], a[228:231], a[192:195], v[152:155]\n\t"
"ds_read_b64_tr_b16 v[228:229], v241 offset:20992\n\t"
"v_mfma_f32_16x16x32_bf16 v[152:155], v[96:99], a[196:199], v[152:155]\n\t"
"ds_read_b64_tr_b16 v[230:231], v241 offset:22528\n\t"
"v_mfma_f32_16x16x32_bf16 v[156:159], v[96:99], a[200:203], v[156:159]\n\t"
"v_mfma_f32_16x16x32_bf16 v[156:159], a[228:231], a[196:199], v[156:159]\n\t"
"v_mfma_f32_16x16x32_bf16 v[156:159], a[224:227], a[192:195], v[156:159]\n\t"
"v_mfma_f32_16x16x32_bf16 v[160:163], a[224:227], a[196:199], v[160:163]\n\t"
"s_waitcnt vmcnt(7) lgkmcnt(6)\n\t"
"v_mfma_f32_16x16x32_bf16 v[160:163], a[228:231], a[200:203], v[160:163]\n\t"
"s_waitcnt lgkmcnt(4)\n\t"
"v_mfma_f32_16x16x32_bf16 v[160:163], v[96:99], a[204:207], v[160:163]\n\t"
"s_waitcnt lgkmcnt(2)\n\t"
"v_mfma_f32_16x16x32_bf16 v[232:235], v[232:235], a[208:211], 0\n\t"
"s_waitcnt lgkmcnt(0)\n\t"
"v_mfma_f32_16x16x32_bf16 v[236:239], v[236:239], a[208:211], 0\n\t"
"v_mfma_f32_16x16x32_bf16 v[224:227], v[224:227], a[208:211], 0\n\t"
"v_mfma_f32_16x16x32_bf16 v[228:231], v[228:231], a[208:211], 0\n\t"
"v_mfma_f32_16x16x32_bf16 v[164:167], a[232:235], a[168:171], v[164:167]\n\t"
"ds_read_b128 a[168:171], v222 offset:512\n\t"
"v_mfma_f32_16x16x32_bf16 v[164:167], a[240:243], a[176:179], v[164:167]\n\t"
"v_cvt_pk_bf16_f32 v232, v232, v233\n\t"
"v_cvt_pk_bf16_f32 v233, v234, v235\n\t"
"v_mfma_f32_16x16x32_bf16 v[164:167], a[236:239], a[172:175], v[164:167]\n\t"
"v_cvt_pk_bf16_f32 v234, v236, v237\n\t"
"v_cvt_pk_bf16_f32 v235, v238, v239\n\t"
"v_mfma_f32_16x16x32_bf16 v[168:171], a[232:235], a[172:175], v[168:171]\n\t"
"ds_read_b128 a[172:175], v223 offset:6656\n\t"
"v_cvt_pk_bf16_f32 v224, v224, v225\n\t"
"v_mfma_f32_16x16x32_bf16 v[168:171], a[236:239], a[176:179], v[168:171]\n\t"
"v_cvt_pk_bf16_f32 v225, v226, v227\n\t"
"v_cvt_pk_bf16_f32 v226, v228, v229\n\t"
"v_mfma_f32_16x16x32_bf16 v[168:171], a[240:243], a[180:183], v[168:171]\n\t"
"ds_write_b128 v242, v[232:235] offset:24576\n\t"
"v_mfma_f32_16x16x32_bf16 v[172:175], a[240:243], a[184:187], v[172:175]\n\t"
"v_cvt_pk_bf16_f32 v227, v230, v231\n\t"
"v_add_u32 v240, 59904, v240\n\t"
"v_mfma_f32_16x16x32_bf16 v[172:175], a[236:239], a[180:183], v[172:175]\n\t"
"v_add_u32 v241, 59904, v241\n\t"
"v_mfma_f32_16x16x32_bf16 v[172:175], a[232:235], a[176:179], v[172:175]\n\t"
"ds_write_b128 v242, v[224:227] offset:26112\n\t"
"v_mfma_f32_16x16x32_bf16 v[176:179], a[232:235], a[180:183], v[176:179]\n\t"
"buffer_load_dwordx4 v[96:99], off, s[80:83], s20 offset:2048 sc1\n\t"
"v_mfma_f32_16x16x32_bf16 v[176:179], a[236:239], a[184:187], v[176:179]\n\t"
"ds_read_b128 a[176:179], v223 offset:12800\n\t"
"v_mfma_f32_16x16x32_bf16 v[176:179], a[240:243], a[188:191], v[176:179]\n\t"
"ds_read_b64_tr_b16 v[236:237], v240 offset:1024\n\t"
"v_mfma_f32_16x16x32_bf16 v[180:183], a[240:243], a[192:195], v[180:183]\n\t"
"ds_read_b64_tr_b16 v[238:239], v240 offset:2560\n\t"
"v_mfma_f32_16x16x32_bf16 v[180:183], a[236:239], a[188:191], v[180:183]\n\t"
"ds_read_b64_tr_b16 v[228:229], v241 offset:1024\n\t"
"v_mfma_f32_16x16x32_bf16 v[180:183], a[232:235], a[184:187], v[180:183]\n\t"
"ds_read_b64_tr_b16 v[230:231], v241 offset:2560\n\t"
"v_mfma_f32_16x16x32_bf16 v[184:187], a[232:235], a[188:191], v[184:187]\n\t"
"ds_read_b128 a[180:183], v222 offset:20480\n\t"
"v_mfma_f32_16x16x32_bf16 v[184:187], a[236:239], a[192:195], v[184:187]\n\t"
"ds_read_b64_tr_b16 v[232:233], v240 offset:20992\n\t"
"v_mfma_f32_16x16x32_bf16 v[184:187], a[240:243], a[196:199], v[184:187]\n\t"
"ds_read_b64_tr_b16 v[234:235], v240 offset:22528\n\t"
"v_mfma_f32_16x16x32_bf16 v[188:191], a[240:243], a[200:203], v[188:191]\n\t"
"ds_read_b64_tr_b16 v[224:225], v241 offset:20992\n\t"
"v_mfma_f32_16x16x32_bf16 v[188:191], a[236:239], a[196:199], v[188:191]\n\t"
"ds_read_b64_tr_b16 v[226:227], v241 offset:22528\n\t"
"v_mfma_f32_16x16x32_bf16 v[188:191], a[232:235], a[192:195], v[188:191]\n\t"
"ds_read_b128 a[184:187], v223 offset:26624\n\t"
"v_mfma_f32_16x16x32_bf16 v[192:195], a[232:235], a[196:199], v[192:195]\n\t"
"s_waitcnt lgkmcnt(8)\n\t"
"ds_read_b128 a[188:191], v223 offset:32768\n\t"
"v_mfma_f32_16x16x32_bf16 v[192:195], a[236:239], a[200:203], v[192:195]\n\t"
"s_waitcnt lgkmcnt(7)\n\t"
"ds_read_b128 a[192:195], v222 offset:40448\n\t"
"v_mfma_f32_16x16x32_bf16 v[192:195], a[240:243], a[204:207], v[192:195]\n\t"
"s_waitcnt lgkmcnt(5)\n\t"
"ds_read_b128 a[196:199], v223 offset:46592\n\t"
"v_mfma_f32_16x16x32_bf16 v[236:239], v[236:239], a[208:211], 0\n\t"
"s_waitcnt lgkmcnt(4)\n\t"
"ds_read_b128 a[200:203], v223 offset:52736\n\t"
"v_mfma_f32_16x16x32_bf16 v[228:231], v[228:231], a[208:211], 0\n\t"
"ds_read_b128 a[204:207], v222 offset:60416\n\t"
"v_mfma_f32_16x16x32_bf16 v[232:235], v[232:235], a[208:211], 0\n\t"
"v_mfma_f32_16x16x32_bf16 v[224:227], v[224:227], a[208:211], 0\n\t"
"Lst5_%=:\n\t"
"v_mfma_f32_16x16x32_bf16 v[196:199], a[244:247], a[168:171], v[196:199]\n\t"
"v_mfma_f32_16x16x32_bf16 v[196:199], a[252:255], a[176:179], v[196:199]\n\t"
"v_cvt_pk_bf16_f32 v236, v236, v237\n\t"
"v_cvt_pk_bf16_f32 v237, v238, v239\n\t"
"v_mfma_f32_16x16x32_bf16 v[196:199], a[248:251], a[172:175], v[196:199]\n\t"
"v_cvt_pk_bf16_f32 v238, v228, v229\n\t"
"v_cvt_pk_bf16_f32 v239, v230, v231\n\t"
"v_mfma_f32_16x16x32_bf16 v[204:207], a[244:247], a[172:175], v[204:207]\n\t"
"s_waitcnt lgkmcnt(5)\n\t"
"v_cvt_pk_bf16_f32 v232, v232, v233\n\t"
"v_cvt_pk_bf16_f32 v233, v234, v235\n\t"
"v_mfma_f32_16x16x32_bf16 v[204:207], a[248:251], a[176:179], v[204:207]\n\t"
"v_cvt_pk_bf16_f32 v234, v224, v225\n\t"
"v_cvt_pk_bf16_f32 v235, v226, v227\n\t"
"v_mfma_f32_16x16x32_bf16 v[204:207], a[252:255], a[180:183], v[204:207]\n\t"
"ds_write_b128 v242, v[236:239] offset:29184\n\t"
"v_mfma_f32_16x16x32_bf16 v[212:215], a[252:255], a[184:187], v[212:215]\n\t"
"s_waitcnt lgkmcnt(5)\n\t"
"s_mov_b32 s20, 430080\n\t"
"v_mfma_f32_16x16x32_bf16 v[212:215], a[248:251], a[180:183], v[212:215]\n\t"
"v_mfma_f32_16x16x32_bf16 v[212:215], a[244:247], a[176:179], v[212:215]\n\t"
"ds_write_b128 v242, v[232:235] offset:30720\n\t"
"v_mfma_f32_16x16x32_bf16 a[0:3], a[244:247], a[180:183], a[0:3]\n\t"
"s_waitcnt lgkmcnt(5)\n\t"
"v_mfma_f32_16x16x32_bf16 a[0:3], a[248:251], a[184:187], a[0:3]\n\t"
"v_mfma_f32_16x16x32_bf16 a[0:3], a[252:255], a[188:191], a[0:3]\n\t"
"s_barrier\n\t"
"v_mfma_f32_16x16x32_bf16 a[8:11], a[252:255], a[192:195], a[8:11]\n\t"
"s_waitcnt lgkmcnt(4)\n\t"
"ds_read_b64_tr_b16 v[228:229], v246 offset:1024\n\t"
"v_mfma_f32_16x16x32_bf16 a[8:11], a[248:251], a[188:191], a[8:11]\n\t"
"buffer_load_dwordx4 a[216:219], off, s[80:83], s20 offset:0 sc1\n\t"
"v_mfma_f32_16x16x32_bf16 a[8:11], a[244:247], a[184:187], a[8:11]\n\t"
"ds_read_b64_tr_b16 v[224:225], v255 offset:1024\n\t"
"v_mfma_f32_16x16x32_bf16 a[16:19], a[244:247], a[188:191], a[16:19]\n\t"
"s_waitcnt lgkmcnt(5)\n\t"
"ds_read_b64_tr_b16 v[236:237], v246 offset:7168\n\t"
"v_mfma_f32_16x16x32_bf16 a[16:19], a[248:251], a[192:195], a[16:19]\n\t"
"ds_read_b64_tr_b16 v[232:233], v255 offset:7168\n\t"
"v_mfma_f32_16x16x32_bf16 a[16:19], a[252:255], a[196:199], a[16:19]\n\t"
"buffer_load_dwordx4 a[220:223], off, s[80:83], s20 offset:1024 sc1\n\t"
"v_mfma_f32_16x16x32_bf16 a[24:27], a[252:255], a[200:203], a[24:27]\n\t"
"s_waitcnt lgkmcnt(6)\n\t"
"v_mfma_f32_16x16x32_bf16 a[24:27], a[248:251], a[196:199], a[24:27]\n\t"
"buffer_load_dwordx4 a[224:227], off, s[80:83], s20 offset:2048 sc1\n\t"
"v_mfma_f32_16x16x32_bf16 a[24:27], a[244:247], a[192:195], a[24:27]\n\t"
"s_mov_b32 s20, 442368\n\t"
"v_mfma_f32_16x16x32_bf16 a[32:35], a[244:247], a[196:199], a[32:35]\n\t"
"s_waitcnt lgkmcnt(3)\n\t"
"v_mfma_f32_16x16x32_bf16 a[32:35], a[248:251], a[200:203], a[32:35]\n\t"
"s_waitcnt lgkmcnt(2)\n\t"
"v_mfma_f32_16x16x32_bf16 a[32:35], a[252:255], a[204:207], a[32:35]\n\t"
"buffer_load_dwordx4 a[228:231], off, s[80:83], s20 offset:0 sc1\n\t"
"v_mfma_f32_4x4x4_16b_bf16 v[228:231], v[228:229], v[220:221], 0\n\t"
"s_waitcnt lgkmcnt(0)\n\t"
"v_mfma_f32_4x4x4_16b_bf16 v[224:227], v[224:225], v[220:221], 0\n\t"
"s_waitcnt vmcnt(4)\n\t"
"v_mfma_f32_4x4x4_16b_bf16 v[236:239], v[236:237], v[220:221], 0\n\t"
"v_mfma_f32_4x4x4_16b_bf16 v[232:235], v[232:233], v[220:221], 0\n\t"
"v_mfma_f32_16x16x32_bf16 a[40:43], v[248:251], a[168:171], a[40:43]\n\t"
"buffer_load_dwordx4 a[232:235], off, s[80:83], s20 offset:1024 sc1\n\t"
"v_mfma_f32_16x16x32_bf16 a[40:43], v[96:99], a[176:179], a[40:43]\n\t"
"v_cvt_pk_bf16_f32 v228, v228, v229\n\t"
"v_cvt_pk_bf16_f32 v229, v230, v231\n\t"
"v_mfma_f32_16x16x32_bf16 a[40:43], a[212:215], a[172:175], a[40:43]\n\t"
"v_cvt_pk_bf16_f32 v230, v224, v225\n\t"
"v_cvt_pk_bf16_f32 v231, v226, v227\n\t"
"v_mfma_f32_16x16x32_bf16 a[48:51], v[248:251], a[172:175], a[48:51]\n\t"
"ds_read_b64_tr_b16 v[224:225], v246 offset:13312\n\t"
"v_cvt_pk_bf16_f32 v236, v236, v237\n\t"
"v_mfma_f32_16x16x32_bf16 a[48:51], a[212:215], a[176:179], a[48:51]\n\t"
"v_cvt_pk_bf16_f32 v237, v238, v239\n\t"
"v_cvt_pk_bf16_f32 v238, v232, v233\n\t"
"v_mfma_f32_16x16x32_bf16 a[48:51], v[96:99], a[180:183], a[48:51]\n\t"
"buffer_load_dwordx4 a[236:239], off, s[80:83], s20 offset:2048 sc1\n\t"
"v_cvt_pk_bf16_f32 v239, v234, v235\n\t"
"s_mov_b32 s20, 454656\n\t"
"v_mfma_f32_16x16x32_bf16 a[56:59], v[96:99], a[184:187], a[56:59]\n\t"
"ds_read_b64_tr_b16 v[232:233], v255 offset:13312\n\t"
"v_mfma_f32_16x16x32_bf16 a[56:59], a[212:215], a[180:183], a[56:59]\n\t"
"buffer_load_dwordx4 a[240:243], off, s[80:83], s20 offset:0 sc1\n\t"
"v_mfma_f32_16x16x32_bf16 a[56:59], v[248:251], a[176:179], a[56:59]\n\t"
"ds_write_b128 v245, v[228:231] offset:1024\n\t"
"v_mfma_f32_16x16x32_bf16 a[64:67], v[248:251], a[180:183], a[64:67]\n\t"
"buffer_load_dwordx4 a[244:247], off, s[80:83], s20 offset:1024 sc1\n\t"
"v_mfma_f32_16x16x32_bf16 a[64:67], a[212:215], a[184:187], a[64:67]\n\t"
"ds_read_b64_tr_b16 v[228:229], v246 offset:20992\n\t"
"v_mfma_f32_16x16x32_bf16 a[64:67], v[96:99], a[188:191], a[64:67]\n\t"
"ds_write_b128 v245, v[236:239] offset:7168\n\t"
"v_mfma_f32_16x16x32_bf16 a[72:75], v[248:251], a[184:187], a[72:75]\n\t"
"buffer_load_dwordx4 a[248:251], off, s[80:83], s20 offset:2048 sc1\n\t"
"v_mfma_f32_16x16x32_bf16 a[72:75], v[96:99], a[192:195], a[72:75]\n\t"
"s_waitcnt lgkmcnt(3)\n\t"
"ds_read_b64_tr_b16 v[236:237], v255 offset:20992\n\t"
"v_mfma_f32_16x16x32_bf16 a[72:75], a[212:215], a[188:191], a[72:75]\n\t"
"s_mov_b32 s20, 466944\n\t"
"v_mfma_f32_4x4x4_16b_bf16 v[224:227], v[224:225], v[220:221], 0\n\t"
"s_waitcnt lgkmcnt(2)\n\t"
"v_mfma_f32_4x4x4_16b_bf16 v[232:235], v[232:233], v[220:221], 0\n\t"
"v_mfma_f32_4x4x4_16b_bf16 v[228:231], v[228:229], v[220:221], 0\n\t"
"s_waitcnt lgkmcnt(0)\n\t"
"buffer_load_dwordx4 a[252:255], off, s[80:83], s20 offset:0 sc1\n\t"
"v_mfma_f32_4x4x4_16b_bf16 v[236:239], v[236:237], v[220:221], 0\n\t"
"v_mfma_f32_16x16x32_bf16 a[80:83], v[248:251], a[188:191], a[80:83]\n\t"
"v_cvt_pk_bf16_f32 v224, v224, v225\n\t"
"v_cvt_pk_bf16_f32 v225, v226, v227\n\t"
"v_mfma_f32_16x16x32_bf16 a[80:83], v[96:99], a[196:199], a[80:83]\n\t"
"v_cvt_pk_bf16_f32 v226, v232, v233\n\t"
"v_cvt_pk_bf16_f32 v227, v234, v235\n\t"
"v_mfma_f32_16x16x32_bf16 a[80:83], a[212:215], a[192:195], a[80:83]\n\t"
"ds_write_b128 v245, v[224:227] offset:13312\n\t"
"v_mfma_f32_16x16x32_bf16 a[88:91], v[248:251], a[192:195], a[88:91]\n\t"
"v_cvt_pk_bf16_f32 v228, v228, v229\n\t"
"v_cvt_pk_bf16_f32 v229, v230, v231\n\t"
"v_mfma_f32_16x16x32_bf16 a[88:91], a[212:215], a[196:199], a[88:91]\n\t"
"ds_read_b64_tr_b16 v[232:233], v246 offset:27136\n\t"
"v_mfma_f32_16x16x32_bf16 a[88:91], v[96:99], a[200:203], a[88:91]\n\t"
"v_cvt_pk_bf16_f32 v230, v236, v237\n\t"
"v_cvt_pk_bf16_f32 v231, v238, v239\n\t"
"v_mfma_f32_16x16x32_bf16 a[96:99], a[212:215], a[200:203], a[96:99]\n\t"
"s_waitcnt vmcnt(7)\n\t"
"ds_read_b64_tr_b16 v[236:237], v255 offset:27136\n\t"
"v_mfma_f32_16x16x32_bf16 a[96:99], v[248:251], a[196:199], a[96:99]\n\t"
"ds_read_b64_tr_b16 v[224:225], v246 offset:33280\n\t"
"v_mfma_f32_16x16x32_bf16 a[96:99], v[96:99], a[204:207], a[96:99]\n\t"
"ds_write_b128 v245, v[228:231] offset:20992\n\t"
"v_mfma_f32_16x16x32_bf16 a[104:107], a[216:219], a[168:171], a[104:107]\n\t"
"buffer_load_dwordx4 a[212:215], off, s[80:83], s20 offset:1024 sc1\n\t"
"v_mfma_f32_16x16x32_bf16 a[104:107], a[224:227], a[176:179], a[104:107]\n\t"
"ds_read_b64_tr_b16 v[228:229], v255 offset:33280\n\t"
"v_mfma_f32_16x16x32_bf16 a[104:107], a[220:223], a[172:175], a[104:107]\n\t"
"buffer_load_dwordx4 v[96:99], off, s[80:83], s20 offset:2048 sc1\n\t"
"s_mov_b32 s20, 700416\n\t"
"v_mfma_f32_16x16x32_bf16 a[112:115], a[216:219], a[172:175], a[112:115]\n\t"
"buffer_load_dwordx4 v[248:251], off, s[80:83], s20 offset:0 sc1\n\t"
"v_mfma_f32_16x16x32_bf16 a[112:115], a[220:223], a[176:179], a[112:115]\n\t"
"s_waitcnt lgkmcnt(3)\n\t"
"ds_read_b128 a[168:171], v222 offset:768\n\t"
"v_mfma_f32_16x16x32_bf16 a[112:115], a[224:227], a[180:183], a[112:115]\n\t"
"s_waitcnt lgkmcnt(3)\n\t"
"ds_read_b128 a[172:175], v223 offset:6912\n\t"
"v_mfma_f32_4x4x4_16b_bf16 v[232:235], v[232:233], v[220:221], 0\n\t"
"s_waitcnt lgkmcnt(2)\n\t"
"v_mfma_f32_4x4x4_16b_bf16 v[236:239], v[236:237], v[220:221], 0\n\t"
"v_mfma_f32_4x4x4_16b_bf16 v[224:227], v[224:225], v[220:221], 0\n\t"
"v_mfma_f32_4x4x4_16b_bf16 v[228:231], v[228:229], v[220:221], 0\n\t"
"v_mfma_f32_16x16x32_bf16 a[120:123], a[220:223], a[180:183], a[120:123]\n\t"
"v_cvt_pk_bf16_f32 v232, v232, v233\n\t"
"v_cvt_pk_bf16_f32 v233, v234, v235\n\t"
"v_mfma_f32_16x16x32_bf16 a[120:123], a[216:219], a[176:179], a[120:123]\n\t"
"ds_read_b128 a[176:179], v223 offset:13056\n\t"
"v_cvt_pk_bf16_f32 v234, v236, v237\n\t"
"v_mfma_f32_16x16x32_bf16 a[120:123], a[224:227], a[184:187], a[120:123]\n\t"
"v_cvt_pk_bf16_f32 v235, v238, v239\n\t"
"v_cvt_pk_bf16_f32 v224, v224, v225\n\t"
"v_mfma_f32_16x16x32_bf16 a[128:131], a[216:219], a[180:183], a[128:131]\n\t"
"ds_read_b128 a[180:183], v222 offset:20736\n\t"
"v_cvt_pk_bf16_f32 v225, v226, v227\n\t"
"v_mfma_f32_16x16x32_bf16 a[128:131], a[224:227], a[188:191], a[128:131]\n\t"
"v_cvt_pk_bf16_f32 v226, v228, v229\n\t"
"v_cvt_pk_bf16_f32 v227, v230, v231\n\t"
"v_mfma_f32_16x16x32_bf16 a[128:131], a[220:223], a[184:187], a[128:131]\n\t"
"ds_write_b128 v245, v[232:235] offset:27136\n\t"
"v_mfma_f32_16x16x32_bf16 a[136:139], a[216:219], a[184:187], a[136:139]\n\t"
"v_mfma_f32_16x16x32_bf16 a[136:139], a[220:223], a[188:191], a[136:139]\n\t"
"ds_read_b128 a[184:187], v223 offset:26880\n\t"
"v_mfma_f32_16x16x32_bf16 a[136:139], a[224:227], a[192:195], a[136:139]\n\t"
"ds_write_b128 v245, v[224:227] offset:33280\n\t"
"v_mfma_f32_16x16x32_bf16 a[144:147], a[224:227], a[196:199], a[144:147]\n\t"
"v_mfma_f32_16x16x32_bf16 a[144:147], a[220:223], a[192:195], a[144:147]\n\t"
"v_mfma_f32_16x16x32_bf16 a[144:147], a[216:219], a[188:191], a[144:147]\n\t"
"ds_read_b128 a[188:191], v223 offset:33024\n\t"
"v_mfma_f32_16x16x32_bf16 a[152:155], a[216:219], a[192:195], a[152:155]\n\t"
"ds_read_b128 a[192:195], v222 offset:40704\n\t"
"v_mfma_f32_16x16x32_bf16 a[152:155], a[220:223], a[196:199], a[152:155]\n\t"
"v_mfma_f32_16x16x32_bf16 a[152:155], a[224:227], a[200:203], a[152:155]\n\t"
"s_barrier\n\t"
"v_mfma_f32_16x16x32_bf16 a[160:163], a[220:223], a[200:203], a[160:163]\n\t"
"s_waitcnt vmcnt(7) lgkmcnt(6)\n\t"
"ds_read_b64_tr_b16 v[236:237], v240 offset:40960\n\t"
"v_mfma_f32_16x16x32_bf16 a[160:163], a[216:219], a[196:199], a[160:163]\n\t"
"ds_read_b64_tr_b16 v[238:239], v240 offset:42496\n\t"
"v_mfma_f32_16x16x32_bf16 a[160:163], a[224:227], a[204:207], a[160:163]\n\t"
"ds_read_b64_tr_b16 v[228:229], v241 offset:40960\n\t"
"v_mfma_f32_16x16x32_bf16 v[200:203], a[228:231], a[168:171], v[200:203]\n\t"
"s_waitcnt lgkmcnt(8)\n\t"
"ds_read_b64_tr_b16 v[230:231], v241 offset:42496\n\t"
"v_subrev_u32 v240, 59904, v240\n\t"
"v_mfma_f32_16x16x32_bf16 v[200:203], a[236:239], a[176:179], v[200:203]\n\t"
"ds_read_b128 a[196:199], v223 offset:46848\n\t"
"v_subrev_u32 v241, 59904, v241\n\t"
"v_mfma_f32_16x16x32_bf16 v[200:203], a[232:235], a[172:175], v[200:203]\n\t"
"ds_read_b64_tr_b16 v[232:233], v240 offset:40960\n\t"
"v_mfma_f32_16x16x32_bf16 v[208:211], a[228:231], a[172:175], v[208:211]\n\t"
"s_waitcnt lgkmcnt(4)\n\t"
"ds_read_b64_tr_b16 v[234:235], v240 offset:42496\n\t"
"v_mfma_f32_16x16x32_bf16 v[208:211], a[232:235], a[176:179], v[208:211]\n\t"
"buffer_load_dwordx4 a[220:223], off, s[80:83], s20 offset:1024 sc1\n\t"
"v_mfma_f32_16x16x32_bf16 v[208:211], a[236:239], a[180:183], v[208:211]\n\t"
"s_waitcnt lgkmcnt(3)\n\t"
"ds_read_b64_tr_b16 v[224:225], v241 offset:40960\n\t"
"ds_read_b64_tr_b16 v[226:227], v241 offset:42496\n\t"
"v_mfma_f32_16x16x32_bf16 v[236:239], v[236:239], a[208:211], 0\n\t"
"buffer_load_dwordx4 a[216:219], off, s[80:83], s20 offset:2048 sc1\n\t"
"s_mov_b32 s20, 712704\n\t"
"v_mfma_f32_16x16x32_bf16 v[228:231], v[228:231], a[208:211], 0\n\t"
"s_waitcnt lgkmcnt(2)\n\t"
"buffer_load_dwordx4 a[224:227], off, s[80:83], s20 offset:0 sc1\n\t"
"v_mfma_f32_16x16x32_bf16 v[232:235], v[232:235], a[208:211], 0\n\t"
"s_waitcnt lgkmcnt(0)\n\t"
"ds_read_b128 a[200:203], v223 offset:52992\n\t"
"v_mfma_f32_16x16x32_bf16 v[224:227], v[224:227], a[208:211], 0\n\t"
"ds_read_b128 a[204:207], v222 offset:60672\n\t"
"v_mfma_f32_16x16x32_bf16 v[216:219], a[232:235], a[180:183], v[216:219]\n\t"
"v_mfma_f32_16x16x32_bf16 v[216:219], a[228:231], a[176:179], v[216:219]\n\t"
"v_cvt_pk_bf16_f32 v236, v236, v237\n\t"
"v_cvt_pk_bf16_f32 v237, v238, v239\n\t"
"v_mfma_f32_16x16x32_bf16 v[216:219], a[236:239], a[184:187], v[216:219]\n\t"
"v_cvt_pk_bf16_f32 v238, v228, v229\n\t"
"v_cvt_pk_bf16_f32 v239, v230, v231\n\t"
"v_mfma_f32_16x16x32_bf16 a[4:7], a[228:231], a[180:183], a[4:7]\n\t"
"ds_read_b64_tr_b16 v[228:229], v246 offset:40960\n\t"
"v_cvt_pk_bf16_f32 v232, v232, v233\n\t"
"v_mfma_f32_16x16x32_bf16 a[4:7], a[236:239], a[188:191], a[4:7]\n\t"
"v_cvt_pk_bf16_f32 v233, v234, v235\n\t"
"v_cvt_pk_bf16_f32 v234, v224, v225\n\t"
"v_mfma_f32_16x16x32_bf16 a[4:7], a[232:235], a[184:187], a[4:7]\n\t"
"ds_write_b128 v242, v[236:239] offset:32256\n\t"
"v_mfma_f32_16x16x32_bf16 a[12:15], a[228:231], a[184:187], a[12:15]\n\t"
"v_cvt_pk_bf16_f32 v235, v226, v227\n\t"
"v_mfma_f32_16x16x32_bf16 a[12:15], a[232:235], a[188:191], a[12:15]\n\t"
"ds_read_b64_tr_b16 v[224:225], v255 offset:40960\n\t"
"v_mfma_f32_16x16x32_bf16 a[12:15], a[236:239], a[192:195], a[12:15]\n\t"
"ds_write_b128 v242, v[232:235] offset:27648\n\t"
"v_mfma_f32_16x16x32_bf16 a[20:23], a[236:239], a[196:199], a[20:23]\n\t"
"s_waitcnt lgkmcnt(5)\n\t"
"v_mfma_f32_16x16x32_bf16 a[20:23], a[232:235], a[192:195], a[20:23]\n\t"
"ds_read_b64_tr_b16 v[236:237], v246 offset:47104\n\t"
"v_mfma_f32_16x16x32_bf16 a[20:23], a[228:231], a[188:191], a[20:23]\n\t"
"ds_read_b64_tr_b16 v[232:233], v255 offset:47104\n\t"
"v_mfma_f32_16x16x32_bf16 a[28:31], a[228:231], a[192:195], a[28:31]\n\t"
"s_waitcnt lgkmcnt(6)\n\t"
"v_mfma_f32_16x16x32_bf16 a[28:31], a[232:235], a[196:199], a[28:31]\n\t"
"v_mfma_f32_16x16x32_bf16 a[28:31], a[236:239], a[200:203], a[28:31]\n\t"
"s_barrier\n\t"
"v_mfma_f32_16x16x32_bf16 a[36:39], a[232:235], a[200:203], a[36:39]\n\t"
"s_waitcnt lgkmcnt(5)\n\t"
"v_mfma_f32_16x16x32_bf16 a[36:39], a[228:231], a[196:199], a[36:39]\n\t"
"buffer_load_dwordx4 a[232:235], off, s[80:83], s20 offset:1024 sc1\n\t"
"v_mfma_f32_16x16x32_bf16 a[36:39], a[236:239], a[204:207], a[36:39]\n\t"
"s_waitcnt lgkmcnt(1)\n\t"
"v_mfma_f32_4x4x4_16b_bf16 v[228:231], v[228:229], v[220:221], 0\n\t"
"s_waitcnt lgkmcnt(0)\n\t"
"v_mfma_f32_4x4x4_16b_bf16 v[224:227], v[224:225], v[220:221], 0\n\t"
"s_waitcnt vmcnt(8)\n\t"
"v_mfma_f32_4x4x4_16b_bf16 v[236:239], v[236:237], v[220:221], 0\n\t"
"v_mfma_f32_4x4x4_16b_bf16 v[232:235], v[232:233], v[220:221], 0\n\t"
"buffer_load_dwordx4 a[228:231], off, s[80:83], s20 offset:2048 sc1\n\t"
"s_mov_b32 s20, 724992\n\t"
"v_mfma_f32_16x16x32_bf16 a[44:47], a[240:243], a[168:171], a[44:47]\n\t"
"buffer_load_dwordx4 a[236:239], off, s[80:83], s20 offset:0 sc1\n\t"
"v_mfma_f32_16x16x32_bf16 a[44:47], a[248:251], a[176:179], a[44:47]\n\t"
"v_cvt_pk_bf16_f32 v228, v228, v229\n\t"
"v_cvt_pk_bf16_f32 v229, v230, v231\n\t"
"v_mfma_f32_16x16x32_bf16 a[44:47], a[244:247], a[172:175], a[44:47]\n\t"
"v_cvt_pk_bf16_f32 v230, v224, v225\n\t"
"v_cvt_pk_bf16_f32 v231, v226, v227\n\t"
"v_mfma_f32_16x16x32_bf16 a[52:55], a[240:243], a[172:175], a[52:55]\n\t"
"ds_read_b64_tr_b16 v[224:225], v246 offset:53248\n\t"
"v_cvt_pk_bf16_f32 v236, v236, v237\n\t"
"v_mfma_f32_16x16x32_bf16 a[52:55], a[244:247], a[176:179], a[52:55]\n\t"
"v_cvt_pk_bf16_f32 v237, v238, v239\n\t"
"v_cvt_pk_bf16_f32 v238, v232, v233\n\t"
"v_mfma_f32_16x16x32_bf16 a[52:55], a[248:251], a[180:183], a[52:55]\n\t"
"ds_write_b128 v245, v[228:231] offset:40960\n\t"
"v_cvt_pk_bf16_f32 v239, v234, v235\n\t"
"v_mfma_f32_16x16x32_bf16 a[60:63], a[248:251], a[184:187], a[60:63]\n\t"
"v_mfma_f32_16x16x32_bf16 a[60:63], a[244:247], a[180:183], a[60:63]\n\t"
"ds_read_b64_tr_b16 v[232:233], v255 offset:53248\n\t"
"v_mfma_f32_16x16x32_bf16 a[60:63], a[240:243], a[176:179], a[60:63]\n\t"
"ds_write_b128 v245, v[236:239] offset:47104\n\t"
"v_mfma_f32_16x16x32_bf16 a[68:71], a[240:243], a[180:183], a[68:71]\n\t"
"v_mfma_f32_16x16x32_bf16 a[68:71], a[244:247], a[184:187], a[68:71]\n\t"
"v_mfma_f32_16x16x32_bf16 a[68:71], a[248:251], a[188:191], a[68:71]\n\t"
"v_mfma_f32_16x16x32_bf16 a[76:79], a[240:243], a[184:187], a[76:79]\n\t"
"s_waitcnt lgkmcnt(3)\n\t"
"v_mfma_f32_16x16x32_bf16 a[76:79], a[248:251], a[192:195], a[76:79]\n\t"
"s_waitcnt lgkmcnt(1)\n\t"
"v_mfma_f32_16x16x32_bf16 a[76:79], a[244:247], a[188:191], a[76:79]\n\t"
"v_mfma_f32_4x4x4_16b_bf16 v[224:227], v[224:225], v[220:221], 0\n\t"
"v_mfma_f32_4x4x4_16b_bf16 v[232:235], v[232:233], v[220:221], 0\n\t"
"v_mfma_f32_16x16x32_bf16 a[84:87], a[240:243], a[188:191], a[84:87]\n\t"
"v_mfma_f32_16x16x32_bf16 a[84:87], a[248:251], a[196:199], a[84:87]\n\t"
"v_cvt_pk_bf16_f32 v224, v224, v225\n\t"
"v_cvt_pk_bf16_f32 v225, v226, v227\n\t"
"v_mfma_f32_16x16x32_bf16 a[84:87], a[244:247], a[192:195], a[84:87]\n\t"
"v_cvt_pk_bf16_f32 v226, v232, v233\n\t"
"v_cvt_pk_bf16_f32 v227, v234, v235\n\t"
"v_mfma_f32_16x16x32_bf16 a[92:95], a[240:243], a[192:195], a[92:95]\n\t"
"v_mfma_f32_16x16x32_bf16 a[92:95], a[244:247], a[196:199], a[92:95]\n\t"
"s_waitcnt vmcnt(7)\n\t"
"v_mfma_f32_16x16x32_bf16 a[92:95], a[248:251], a[200:203], a[92:95]\n\t"
"ds_write_b128 v245, v[224:227] offset:53248\n\t"
"v_mfma_f32_16x16x32_bf16 a[100:103], a[244:247], a[200:203], a[100:103]\n\t"
"buffer_load_dwordx4 a[244:247], off, s[80:83], s20 offset:1024 sc1\n\t"
"v_mfma_f32_16x16x32_bf16 a[100:103], a[240:243], a[196:199], a[100:103]\n\t"
"Lpg5_%=:\n\t"
"s_cbranch_vccnz Lpg9_%=\n\t"
"v_mfma_f32_16x16x32_bf16 a[100:103], a[248:251], a[204:207], a[100:103]\n\t"
"v_mfma_f32_16x16x32_bf16 a[108:111], a[252:255], a[168:171], a[108:111]\n\t"
"buffer_load_dwordx4 a[240:243], off, s[80:83], s20 offset:2048 sc1\n\t"
"v_mfma_f32_16x16x32_bf16 a[108:111], v[96:99], a[176:179], a[108:111]\n\t"
"s_mov_b32 s20, 737280\n\t"
"v_mfma_f32_16x16x32_bf16 a[108:111], a[212:215], a[172:175], a[108:111]\n\t"
"s_waitcnt lgkmcnt(0)\n\t"
"s_barrier\n\t"
"v_mfma_f32_16x16x32_bf16 a[116:119], a[252:255], a[172:175], a[116:119]\n\t"
"ds_read_b128 a[168:171], v222 offset:1024\n\t"
"v_mfma_f32_16x16x32_bf16 a[116:119], a[212:215], a[176:179], a[116:119]\n\t"
"buffer_load_dwordx4 a[248:251], off, s[80:83], s20 offset:0 sc1\n\t"
"v_mfma_f32_16x16x32_bf16 a[116:119], v[96:99], a[180:183], a[116:119]\n\t"
"ds_read_b128 a[172:175], v223 offset:7168\n\t"
"v_mfma_f32_16x16x32_bf16 a[124:127], v[96:99], a[184:187], a[124:127]\n\t"
"v_mfma_f32_16x16x32_bf16 a[124:127], a[212:215], a[180:183], a[124:127]\n\t"
"v_mfma_f32_16x16x32_bf16 a[124:127], a[252:255], a[176:179], a[124:127]\n\t"
"ds_read_b128 a[176:179], v223 offset:13312\n\t"
"v_mfma_f32_16x16x32_bf16 a[132:135], a[252:255], a[180:183], a[132:135]\n\t"
"ds_read_b64_tr_b16 v[228:229], v240 offset:1536\n\t"
"v_mfma_f32_16x16x32_bf16 a[132:135], a[212:215], a[184:187], a[132:135]\n\t"
"ds_read_b64_tr_b16 v[230:231], v240 offset:3072\n\t"
"v_mfma_f32_16x16x32_bf16 a[132:135], v[96:99], a[188:191], a[132:135]\n\t"
"ds_read_b64_tr_b16 v[236:237], v241 offset:1536\n\t"
"v_mfma_f32_16x16x32_bf16 a[140:143], v[96:99], a[192:195], a[140:143]\n\t"
"ds_read_b64_tr_b16 v[238:239], v241 offset:3072\n\t"
"v_mfma_f32_16x16x32_bf16 a[140:143], a[212:215], a[188:191], a[140:143]\n\t"
"ds_read_b128 a[180:183], v222 offset:20992\n\t"
"v_mfma_f32_16x16x32_bf16 a[140:143], a[252:255], a[184:187], a[140:143]\n\t"
"ds_read_b64_tr_b16 v[232:233], v240 offset:21504\n\t"
"v_mfma_f32_16x16x32_bf16 a[148:151], a[252:255], a[188:191], a[148:151]\n\t"
"ds_read_b64_tr_b16 v[234:235], v240 offset:23040\n\t"
"v_mfma_f32_16x16x32_bf16 a[148:151], a[212:215], a[192:195], a[148:151]\n\t"
"ds_read_b64_tr_b16 v[224:225], v241 offset:21504\n\t"
"v_mfma_f32_16x16x32_bf16 a[148:151], v[96:99], a[196:199], a[148:151]\n\t"
"ds_read_b64_tr_b16 v[226:227], v241 offset:23040\n\t"
"v_mfma_f32_16x16x32_bf16 a[156:159], v[96:99], a[200:203], a[156:159]\n\t"
"ds_read_b128 a[184:187], v223 offset:27136\n\t"
"v_mfma_f32_16x16x32_bf16 a[156:159], a[212:215], a[196:199], a[156:159]\n\t"
"ds_read_b128 a[188:191], v223 offset:33280\n\t"
"v_mfma_f32_16x16x32_bf16 a[156:159], a[252:255], a[192:195], a[156:159]\n\t"
"ds_read_b128 a[192:195], v222 offset:40960\n\t"
"v_mfma_f32_16x16x32_bf16 a[164:167], a[252:255], a[196:199], a[164:167]\n\t"
"buffer_load_dwordx4 a[252:255], off, s[80:83], s20 offset:1024 sc1\n\t"
"v_mfma_f32_16x16x32_bf16 a[164:167], a[212:215], a[200:203], a[164:167]\n\t"
"s_waitcnt lgkmcnt(8)\n\t"
"ds_read_b128 a[196:199], v223 offset:47104\n\t"
"v_mfma_f32_16x16x32_bf16 a[164:167], v[96:99], a[204:207], a[164:167]\n\t"
"s_waitcnt lgkmcnt(6)\n\t"
"ds_read_b128 a[200:203], v223 offset:53248\n\t"
"v_mfma_f32_16x16x32_bf16 v[228:231], v[228:231], a[208:211], 0\n\t"
"buffer_load_dwordx4 v[96:99], off, s[80:83], s20 offset:2048 sc1\n\t"
"v_mfma_f32_16x16x32_bf16 v[236:239], v[236:239], a[208:211], 0\n\t"
"s_waitcnt vmcnt(9) lgkmcnt(5)\n\t"
"ds_read_b128 a[204:207], v222 offset:60928\n\t"
"s_mov_b32 s20, 749568\n\t"
"v_mfma_f32_16x16x32_bf16 v[232:235], v[232:235], a[208:211], 0\n\t"
"v_mfma_f32_16x16x32_bf16 v[224:227], v[224:227], a[208:211], 0\n\t"
"buffer_load_dwordx4 a[212:215], off, s[80:83], s20 offset:0 sc1\n\t"
"Lst6_%=:\n\t"
"v_mfma_f32_16x16x32_bf16 v[196:199], v[248:251], a[168:171], v[196:199]\n\t"
"v_mfma_f32_16x16x32_bf16 v[196:199], a[216:219], a[176:179], v[196:199]\n\t"
"v_cvt_pk_bf16_f32 v228, v228, v229\n\t"
"v_cvt_pk_bf16_f32 v229, v230, v231\n\t"
"v_mfma_f32_16x16x32_bf16 v[196:199], a[220:223], a[172:175], v[196:199]\n\t"
"v_cvt_pk_bf16_f32 v230, v236, v237\n\t"
"v_cvt_pk_bf16_f32 v231, v238, v239\n\t"
"v_mfma_f32_16x16x32_bf16 v[204:207], v[248:251], a[172:175], v[204:207]\n\t"
"s_waitcnt lgkmcnt(5)\n\t"
"v_cvt_pk_bf16_f32 v232, v232, v233\n\t"
"v_cvt_pk_bf16_f32 v233, v234, v235\n\t"
"v_mfma_f32_16x16x32_bf16 v[204:207], a[220:223], a[176:179], v[204:207]\n\t"
"v_cvt_pk_bf16_f32 v234, v224, v225\n\t"
"v_cvt_pk_bf16_f32 v235, v226, v227\n\t"
"v_mfma_f32_16x16x32_bf16 v[204:207], a[216:219], a[180:183], v[204:207]\n\t"
"ds_write_b128 v242, v[228:231] offset:0\n\t"
"v_mfma_f32_16x16x32_bf16 v[212:215], a[216:219], a[184:187], v[212:215]\n\t"
"s_waitcnt lgkmcnt(5)\n\t"
"v_mfma_f32_16x16x32_bf16 v[212:215], a[220:223], a[180:183], v[212:215]\n\t"
"v_mfma_f32_16x16x32_bf16 v[212:215], v[248:251], a[176:179], v[212:215]\n\t"
"ds_write_b128 v242, v[232:235] offset:1536\n\t"
"v_mfma_f32_16x16x32_bf16 a[0:3], v[248:251], a[180:183], a[0:3]\n\t"
"s_waitcnt lgkmcnt(5)\n\t"
"v_mfma_f32_16x16x32_bf16 a[0:3], a[220:223], a[184:187], a[0:3]\n\t"
"ds_read_b64_tr_b16 v[236:237], v240 offset:41472\n\t"
"v_mfma_f32_16x16x32_bf16 a[0:3], a[216:219], a[188:191], a[0:3]\n\t"
"ds_read_b64_tr_b16 v[238:239], v240 offset:43008\n\t"
"v_mfma_f32_16x16x32_bf16 a[8:11], a[216:219], a[192:195], a[8:11]\n\t"
"s_waitcnt lgkmcnt(6)\n\t"
"ds_read_b64_tr_b16 v[224:225], v241 offset:41472\n\t"
"v_mfma_f32_16x16x32_bf16 a[8:11], a[220:223], a[188:191], a[8:11]\n\t"
"ds_read_b64_tr_b16 v[226:227], v241 offset:43008\n\t"
"v_add_u32 v240, 59904, v240\n\t"
"v_mfma_f32_16x16x32_bf16 a[8:11], v[248:251], a[184:187], a[8:11]\n\t"
"v_add_u32 v241, 59904, v241\n\t"
"v_mfma_f32_16x16x32_bf16 a[16:19], v[248:251], a[188:191], a[16:19]\n\t"
"s_waitcnt lgkmcnt(7)\n\t"
"ds_read_b64_tr_b16 v[228:229], v240 offset:1536\n\t"
"v_mfma_f32_16x16x32_bf16 a[16:19], a[220:223], a[192:195], a[16:19]\n\t"
"ds_read_b64_tr_b16 v[230:231], v240 offset:3072\n\t"
"v_mfma_f32_16x16x32_bf16 a[16:19], a[216:219], a[196:199], a[16:19]\n\t"
"ds_read_b64_tr_b16 v[232:233], v241 offset:1536\n\t"
"v_mfma_f32_16x16x32_bf16 a[24:27], a[216:219], a[200:203], a[24:27]\n\t"
"s_waitcnt lgkmcnt(9)\n\t"
"ds_read_b64_tr_b16 v[234:235], v241 offset:3072\n\t"
"v_mfma_f32_16x16x32_bf16 a[24:27], a[220:223], a[196:199], a[24:27]\n\t"
"v_mfma_f32_16x16x32_bf16 a[24:27], v[248:251], a[192:195], a[24:27]\n\t"
"v_mfma_f32_16x16x32_bf16 a[32:35], v[248:251], a[196:199], a[32:35]\n\t"
"buffer_load_dwordx4 v[248:251], off, s[80:83], s20 offset:1024 sc1\n\t"
"v_mfma_f32_16x16x32_bf16 a[32:35], a[220:223], a[200:203], a[32:35]\n\t"
"s_waitcnt lgkmcnt(4)\n\t"
"v_mfma_f32_16x16x32_bf16 a[32:35], a[216:219], a[204:207], a[32:35]\n\t"
"s_waitcnt lgkmcnt(2)\n\t"
"v_mfma_f32_16x16x32_bf16 v[236:239], v[236:239], a[208:211], 0\n\t"
"buffer_load_dwordx4 a[216:219], off, s[80:83], s20 offset:2048 sc1\n\t"
"v_mfma_f32_16x16x32_bf16 v[224:227], v[224:227], a[208:211], 0\n\t"
"s_waitcnt vmcnt(9) lgkmcnt(0)\n\t"
"s_mov_b32 s20, 761856\n\t"
"v_mfma_f32_16x16x32_bf16 v[228:231], v[228:231], a[208:211], 0\n\t"
"v_mfma_f32_16x16x32_bf16 v[232:235], v[232:235], a[208:211], 0\n\t"
"buffer_load_dwordx4 a[220:223], off, s[80:83], s20 offset:0 sc1\n\t"
"v_mfma_f32_16x16x32_bf16 a[40:43], a[224:227], a[168:171], a[40:43]\n\t"
"v_mfma_f32_16x16x32_bf16 a[40:43], a[228:231], a[176:179], a[40:43]\n\t"
"v_cvt_pk_bf16_f32 v236, v236, v237\n\t"
"v_cvt_pk_bf16_f32 v237, v238, v239\n\t"
"v_mfma_f32_16x16x32_bf16 a[40:43], a[232:235], a[172:175], a[40:43]\n\t"
"v_cvt_pk_bf16_f32 v238, v224, v225\n\t"
"v_cvt_pk_bf16_f32 v239, v226, v227\n\t"
"v_mfma_f32_16x16x32_bf16 a[48:51], a[224:227], a[172:175], a[48:51]\n\t"
"v_cvt_pk_bf16_f32 v228, v228, v229\n\t"
"v_cvt_pk_bf16_f32 v229, v230, v231\n\t"
"v_mfma_f32_16x16x32_bf16 a[48:51], a[232:235], a[176:179], a[48:51]\n\t"
"v_cvt_pk_bf16_f32 v230, v232, v233\n\t"
"v_cvt_pk_bf16_f32 v231, v234, v235\n\t"
"v_mfma_f32_16x16x32_bf16 a[48:51], a[228:231], a[180:183], a[48:51]\n\t"
"ds_write_b128 v242, v[236:239] offset:3072\n\t"
"v_mfma_f32_16x16x32_bf16 a[56:59], a[228:231], a[184:187], a[56:59]\n\t"
"v_mfma_f32_16x16x32_bf16 a[56:59], a[232:235], a[180:183], a[56:59]\n\t"
"v_mfma_f32_16x16x32_bf16 a[56:59], a[224:227], a[176:179], a[56:59]\n\t"
"ds_write_b128 v242, v[228:231] offset:4608\n\t"
"v_mfma_f32_16x16x32_bf16 a[64:67], a[224:227], a[180:183], a[64:67]\n\t"
"v_mfma_f32_16x16x32_bf16 a[64:67], a[232:235], a[184:187], a[64:67]\n\t"
"ds_read_b64_tr_b16 v[224:225], v240 offset:21504\n\t"
"v_mfma_f32_16x16x32_bf16 a[64:67], a[228:231], a[188:191], a[64:67]\n\t"
"ds_read_b64_tr_b16 v[226:227], v240 offset:23040\n\t"
"v_mfma_f32_16x16x32_bf16 a[72:75], a[228:231], a[192:195], a[72:75]\n\t"
"ds_read_b64_tr_b16 v[232:233], v241 offset:21504\n\t"
"v_mfma_f32_16x16x32_bf16 a[72:75], a[232:235], a[188:191], a[72:75]\n\t"
"ds_read_b64_tr_b16 v[234:235], v241 offset:23040\n\t"
"v_mfma_f32_16x16x32_bf16 a[72:75], a[224:227], a[184:187], a[72:75]\n\t"
"ds_read_b64_tr_b16 v[236:237], v240 offset:41472\n\t"
"v_mfma_f32_16x16x32_bf16 a[80:83], a[224:227], a[188:191], a[80:83]\n\t"
"ds_read_b64_tr_b16 v[238:239], v240 offset:43008\n\t"
"v_mfma_f32_16x16x32_bf16 a[80:83], a[232:235], a[192:195], a[80:83]\n\t"
"ds_read_b64_tr_b16 v[228:229], v241 offset:41472\n\t"
"v_mfma_f32_16x16x32_bf16 a[80:83], a[228:231], a[196:199], a[80:83]\n\t"
"ds_read_b64_tr_b16 v[230:231], v241 offset:43008\n\t"
"v_mfma_f32_16x16x32_bf16 a[88:91], a[228:231], a[200:203], a[88:91]\n\t"
"v_mfma_f32_16x16x32_bf16 a[88:91], a[232:235], a[196:199], a[88:91]\n\t"
"v_mfma_f32_16x16x32_bf16 a[88:91], a[224:227], a[192:195], a[88:91]\n\t"
"v_mfma_f32_16x16x32_bf16 a[96:99], a[224:227], a[196:199], a[96:99]\n\t"
"buffer_load_dwordx4 a[224:227], off, s[80:83], s20 offset:1024 sc1\n\t"
"v_mfma_f32_16x16x32_bf16 a[96:99], a[232:235], a[200:203], a[96:99]\n\t"
"s_waitcnt lgkmcnt(4)\n\t"
"v_mfma_f32_16x16x32_bf16 a[96:99], a[228:231], a[204:207], a[96:99]\n\t"
"s_waitcnt lgkmcnt(2)\n\t"
"v_mfma_f32_16x16x32_bf16 v[224:227], v[224:227], a[208:211], 0\n\t"
"buffer_load_dwordx4 a[228:231], off, s[80:83], s20 offset:2048 sc1\n\t"
"v_mfma_f32_16x16x32_bf16 v[232:235], v[232:235], a[208:211], 0\n\t"
"s_waitcnt vmcnt(9) lgkmcnt(0)\n\t"
"s_mov_b32 s20, 626688\n\t"
"v_mfma_f32_16x16x32_bf16 v[236:239], v[236:239], a[208:211], 0\n\t"
"v_mfma_f32_16x16x32_bf16 v[228:231], v[228:231], a[208:211], 0\n\t"
"buffer_load_dwordx4 a[232:235], off, s[80:83], s20 offset:0 sc1\n\t"
"v_mfma_f32_16x16x32_bf16 a[104:107], a[236:239], a[168:171], a[104:107]\n\t"
"ds_read_b128 a[168:171], v222 offset:1280\n\t"
"v_mfma_f32_16x16x32_bf16 a[104:107], a[240:243], a[176:179], a[104:107]\n\t"
"v_cvt_pk_bf16_f32 v224, v224, v225\n\t"
"v_cvt_pk_bf16_f32 v225, v226, v227\n\t"
"v_mfma_f32_16x16x32_bf16 a[104:107], a[244:247], a[172:175], a[104:107]\n\t"
"v_cvt_pk_bf16_f32 v226, v232, v233\n\t"
"v_cvt_pk_bf16_f32 v227, v234, v235\n\t"
"v_mfma_f32_16x16x32_bf16 a[112:115], a[236:239], a[172:175], a[112:115]\n\t"
"ds_read_b128 a[172:175], v223 offset:7424\n\t"
"v_cvt_pk_bf16_f32 v236, v236, v237\n\t"
"v_mfma_f32_16x16x32_bf16 a[112:115], a[244:247], a[176:179], a[112:115]\n\t"
"v_cvt_pk_bf16_f32 v237, v238, v239\n\t"
"v_cvt_pk_bf16_f32 v238, v228, v229\n\t"
"v_mfma_f32_16x16x32_bf16 a[112:115], a[240:243], a[180:183], a[112:115]\n\t"
"buffer_load_dwordx4 a[208:211], off, s[80:83], s20 offset:1024 sc1\n\t"
"v_mfma_f32_16x16x32_bf16 a[120:123], a[240:243], a[184:187], a[120:123]\n\t"
"v_cvt_pk_bf16_f32 v239, v230, v231\n\t"
"v_mfma_f32_16x16x32_bf16 a[120:123], a[244:247], a[180:183], a[120:123]\n\t"
"v_mfma_f32_16x16x32_bf16 a[120:123], a[236:239], a[176:179], a[120:123]\n\t"
"ds_write_b128 v242, v[224:227] offset:6144\n\t"
"v_mfma_f32_16x16x32_bf16 a[128:131], a[236:239], a[180:183], a[128:131]\n\t"
"v_mfma_f32_16x16x32_bf16 a[128:131], a[244:247], a[184:187], a[128:131]\n\t"
"ds_read_b128 a[176:179], v223 offset:13568\n\t"
"v_mfma_f32_16x16x32_bf16 a[128:131], a[240:243], a[188:191], a[128:131]\n\t"
"ds_write_b128 v242, v[236:239] offset:7680\n\t"
"v_mfma_f32_16x16x32_bf16 a[136:139], a[240:243], a[192:195], a[136:139]\n\t"
"buffer_load_dwordx4 v[240:243], off, s[80:83], s20 offset:2048 sc1\n\t"
"v_mfma_f32_16x16x32_bf16 a[136:139], a[244:247], a[188:191], a[136:139]\n\t"
"ds_read_b128 a[180:183], v222 offset:21248\n\t"
"v_mfma_f32_16x16x32_bf16 a[136:139], a[236:239], a[184:187], a[136:139]\n\t"
"ds_read_b128 a[184:187], v223 offset:27392\n\t"
"v_mfma_f32_16x16x32_bf16 a[144:147], a[236:239], a[188:191], a[144:147]\n\t"
"s_mov_b32 s20, 638976\n\t"
"ds_read_b128 a[188:191], v223 offset:33536\n\t"
"v_mfma_f32_16x16x32_bf16 a[144:147], a[244:247], a[192:195], a[144:147]\n\t"
"v_mfma_f32_16x16x32_bf16 a[144:147], a[240:243], a[196:199], a[144:147]\n\t"
"v_mfma_f32_16x16x32_bf16 a[152:155], a[240:243], a[200:203], a[152:155]\n\t"
"v_mfma_f32_16x16x32_bf16 a[152:155], a[244:247], a[196:199], a[152:155]\n\t"
"v_mfma_f32_16x16x32_bf16 a[152:155], a[236:239], a[192:195], a[152:155]\n\t"
"ds_read_b128 a[192:195], v222 offset:41216\n\t"
"v_mfma_f32_16x16x32_bf16 a[160:163], a[236:239], a[196:199], a[160:163]\n\t"
"buffer_load_dwordx4 a[236:239], off, s[80:83], s20 offset:0 sc1\n\t"
"v_mfma_f32_16x16x32_bf16 a[160:163], a[244:247], a[200:203], a[160:163]\n\t"
"s_waitcnt vmcnt(10) lgkmcnt(5)\n\t"
"ds_read_b128 a[196:199], v223 offset:47360\n\t"
"v_mfma_f32_16x16x32_bf16 a[160:163], a[240:243], a[204:207], a[160:163]\n\t"
"ds_read_b128 a[200:203], v223 offset:53504\n\t"
"v_mfma_f32_16x16x32_bf16 v[200:203], a[248:251], a[168:171], v[200:203]\n\t"
"s_waitcnt lgkmcnt(5)\n\t"
"ds_read_b128 a[204:207], v222 offset:61184\n\t"
"v_mfma_f32_16x16x32_bf16 v[200:203], v[96:99], a[176:179], v[200:203]\n\t"
"buffer_load_dwordx4 a[240:243], off, s[80:83], s20 offset:1024 sc1\n\t"
"v_mfma_f32_16x16x32_bf16 v[200:203], a[252:255], a[172:175], v[200:203]\n\t"
"s_add_u32 m0, s25, 256\n\t"
"v_mfma_f32_16x16x32_bf16 v[208:211], a[248:251], a[172:175], v[208:211]\n\t"
"buffer_load_dwordx4 a[244:247], off, s[80:83], s20 offset:2048 sc1\n\t"
"v_mfma_f32_16x16x32_bf16 v[208:211], a[252:255], a[176:179], v[208:211]\n\t"
"s_waitcnt lgkmcnt(5)\n\t"
"v_mfma_f32_16x16x32_bf16 v[208:211], v[96:99], a[180:183], v[208:211]\n\t"
"s_waitcnt lgkmcnt(0)\n\t"
"s_barrier\n\t"
"v_mfma_f32_16x16x32_bf16 v[216:219], v[96:99], a[184:187], v[216:219]\n\t"
"v_mfma_f32_16x16x32_bf16 v[216:219], a[252:255], a[180:183], v[216:219]\n\t"
"buffer_load_dwordx4 v254, s[12:15], 0 offen offset:256 nt lds\n\t"
"v_mfma_f32_16x16x32_bf16 v[216:219], a[248:251], a[176:179], v[216:219]\n\t"
"s_add_u32 m0, s26, 256\n\t"
"ds_read_b64_tr_b16 v[232:233], v246 offset:1536\n\t"
"v_mfma_f32_16x16x32_bf16 a[4:7], a[248:251], a[180:183], a[4:7]\n\t"
"ds_read_b64_tr_b16 v[228:229], v255 offset:1536\n\t"
"v_mfma_f32_16x16x32_bf16 a[4:7], a[252:255], a[184:187], a[4:7]\n\t"
"ds_read_b64_tr_b16 v[224:225], v246 offset:7680\n\t"
"v_mfma_f32_16x16x32_bf16 a[4:7], v[96:99], a[188:191], a[4:7]\n\t"
"s_mov_b64 exec, s[36:37]\n\t"
"buffer_load_dwordx4 v254, s[12:15], s27 offen offset:256 nt lds\n\t"
"s_mov_b64 exec, -1\n\t"
"s_add_u32 m0, s24, 256\n\t"
"s_add_u32 s28, s21, 0\n\t"
"v_mfma_f32_16x16x32_bf16 a[12:15], v[96:99], a[192:195], a[12:15]\n\t"
"buffer_load_dwordx4 v247, s[12:15], s28 offen offset:256 nt lds\n\t"
"v_mfma_f32_16x16x32_bf16 a[12:15], a[252:255], a[188:191], a[12:15]\n\t"
"s_add_u32 m0, s24, 6400\n\t"
"s_add_u32 s28, s21, 196608\n\t"
"ds_read_b64_tr_b16 v[236:237], v255 offset:7680\n\t"
"v_mfma_f32_16x16x32_bf16 a[12:15], a[248:251], a[184:187], a[12:15]\n\t"
"buffer_load_dwordx4 v247, s[12:15], s28 offen offset:256 nt lds\n\t"
"s_add_u32 m0, s24, 12544\n\t"
"s_add_u32 s28, s21, 393216\n\t"
"v_mfma_f32_16x16x32_bf16 a[20:23], a[248:251], a[188:191], a[20:23]\n\t"
"buffer_load_dwordx4 v247, s[12:15], s28 offen offset:256 nt lds\n\t"
"v_mfma_f32_16x16x32_bf16 a[20:23], a[252:255], a[192:195], a[20:23]\n\t"
"s_add_u32 m0, s24, 20224\n\t"
"s_add_u32 s28, s21, 589824\n\t"
"v_mfma_f32_16x16x32_bf16 a[20:23], v[96:99], a[196:199], a[20:23]\n\t"
"buffer_load_dwordx4 v247, s[12:15], s28 offen offset:256 nt lds\n\t"
"s_add_u32 m0, s24, 26368\n\t"
"s_add_u32 s28, s21, 786432\n\t"
"v_mfma_f32_16x16x32_bf16 a[28:31], v[96:99], a[200:203], a[28:31]\n\t"
"buffer_load_dwordx4 v247, s[12:15], s28 offen offset:256 nt lds\n\t"
"v_mfma_f32_16x16x32_bf16 a[28:31], a[252:255], a[196:199], a[28:31]\n\t"
"s_add_u32 m0, s24, 32512\n\t"
"s_add_u32 s28, s21, 983040\n\t"
"v_mfma_f32_16x16x32_bf16 a[28:31], a[248:251], a[192:195], a[28:31]\n\t"
"buffer_load_dwordx4 v247, s[12:15], s28 offen offset:256 nt lds\n\t"
"v_mfma_f32_16x16x32_bf16 a[36:39], a[248:251], a[196:199], a[36:39]\n\t"
"s_waitcnt lgkmcnt(3)\n\t"
"v_mfma_f32_16x16x32_bf16 a[36:39], a[252:255], a[200:203], a[36:39]\n\t"
"s_waitcnt lgkmcnt(2)\n\t"
"v_mfma_f32_16x16x32_bf16 a[36:39], v[96:99], a[204:207], a[36:39]\n\t"
"s_waitcnt lgkmcnt(1)\n\t"
"v_mfma_f32_4x4x4_16b_bf16 v[232:235], v[232:233], v[220:221], 0\n\t"
"s_waitcnt lgkmcnt(0)\n\t"
"v_mfma_f32_4x4x4_16b_bf16 v[228:231], v[228:229], v[220:221], 0\n\t"
"s_waitcnt vmcnt(17)\n\t"
"v_mfma_f32_4x4x4_16b_bf16 v[224:227], v[224:225], v[220:221], 0\n\t"
"v_mfma_f32_4x4x4_16b_bf16 v[236:239], v[236:237], v[220:221], 0\n\t"
"v_mfma_f32_16x16x32_bf16 a[44:47], a[212:215], a[168:171], a[44:47]\n\t"
"v_cvt_pk_bf16_f32 v232, v232, v233\n\t"
"v_cvt_pk_bf16_f32 v233, v234, v235\n\t"
"v_mfma_f32_16x16x32_bf16 a[44:47], a[216:219], a[176:179], a[44:47]\n\t"
"v_cvt_pk_bf16_f32 v234, v228, v229\n\t"
"v_cvt_pk_bf16_f32 v235, v230, v231\n\t"
"v_mfma_f32_16x16x32_bf16 a[44:47], v[248:251], a[172:175], a[44:47]\n\t"
"ds_write_b128 v245, v[232:235] offset:1536\n\t"
"v_mfma_f32_16x16x32_bf16 a[52:55], a[212:215], a[172:175], a[52:55]\n\t"
"v_cvt_pk_bf16_f32 v224, v224, v225\n\t"
"v_cvt_pk_bf16_f32 v225, v226, v227\n\t"
"v_mfma_f32_16x16x32_bf16 a[52:55], v[248:251], a[176:179], a[52:55]\n\t"
"v_cvt_pk_bf16_f32 v226, v236, v237\n\t"
"v_cvt_pk_bf16_f32 v227, v238, v239\n\t"
"v_mfma_f32_16x16x32_bf16 a[52:55], a[216:219], a[180:183], a[52:55]\n\t"
"ds_write_b128 v245, v[224:227] offset:7680\n\t"
"v_mfma_f32_16x16x32_bf16 a[60:63], a[216:219], a[184:187], a[60:63]\n\t"
"v_mfma_f32_16x16x32_bf16 a[60:63], v[248:251], a[180:183], a[60:63]\n\t"
"v_mfma_f32_16x16x32_bf16 a[60:63], a[212:215], a[176:179], a[60:63]\n\t"
"ds_read_b64_tr_b16 v[228:229], v246 offset:13824\n\t"
"v_mfma_f32_16x16x32_bf16 a[68:71], a[212:215], a[180:183], a[68:71]\n\t"
"ds_read_b64_tr_b16 v[236:237], v255 offset:13824\n\t"
"v_mfma_f32_16x16x32_bf16 a[68:71], v[248:251], a[184:187], a[68:71]\n\t"
"ds_read_b64_tr_b16 v[232:233], v246 offset:21504\n\t"
"v_mfma_f32_16x16x32_bf16 a[68:71], a[216:219], a[188:191], a[68:71]\n\t"
"ds_read_b64_tr_b16 v[224:225], v255 offset:21504\n\t"
"v_mfma_f32_16x16x32_bf16 a[76:79], a[216:219], a[192:195], a[76:79]\n\t"
"v_mfma_f32_16x16x32_bf16 a[76:79], v[248:251], a[188:191], a[76:79]\n\t"
"v_mfma_f32_16x16x32_bf16 a[76:79], a[212:215], a[184:187], a[76:79]\n\t"
"v_mfma_f32_16x16x32_bf16 a[84:87], a[212:215], a[188:191], a[84:87]\n\t"
"v_mfma_f32_16x16x32_bf16 a[84:87], v[248:251], a[192:195], a[84:87]\n\t"
"v_mfma_f32_16x16x32_bf16 a[84:87], a[216:219], a[196:199], a[84:87]\n\t"
"v_mfma_f32_16x16x32_bf16 a[92:95], a[216:219], a[200:203], a[92:95]\n\t"
"v_mfma_f32_16x16x32_bf16 a[92:95], v[248:251], a[196:199], a[92:95]\n\t"
"v_mfma_f32_16x16x32_bf16 a[92:95], a[212:215], a[192:195], a[92:95]\n\t"
"v_mfma_f32_16x16x32_bf16 a[100:103], a[212:215], a[196:199], a[100:103]\n\t"
"s_waitcnt lgkmcnt(3)\n\t"
"v_mfma_f32_16x16x32_bf16 a[100:103], v[248:251], a[200:203], a[100:103]\n\t"
"s_waitcnt lgkmcnt(2)\n\t"
"v_mfma_f32_16x16x32_bf16 a[100:103], a[216:219], a[204:207], a[100:103]\n\t"
"s_waitcnt lgkmcnt(1)\n\t"
"v_mfma_f32_4x4x4_16b_bf16 v[228:231], v[228:229], v[220:221], 0\n\t"
"s_waitcnt lgkmcnt(0)\n\t"
"v_mfma_f32_4x4x4_16b_bf16 v[236:239], v[236:237], v[220:221], 0\n\t"
"s_waitcnt vmcnt(14)\n\t"
"v_mfma_f32_4x4x4_16b_bf16 v[232:235], v[232:233], v[220:221], 0\n\t"
"s_mov_b32 s20, 651264\n\t"
"v_mfma_f32_4x4x4_16b_bf16 v[224:227], v[224:225], v[220:221], 0\n\t"
"v_mfma_f32_16x16x32_bf16 a[108:111], a[220:223], a[168:171], a[108:111]\n\t"
"ds_read_b128 a[168:171], v222 offset:2560\n\t"
"v_cvt_pk_bf16_f32 v228, v228, v229\n\t"
"v_mfma_f32_16x16x32_bf16 a[108:111], a[228:231], a[176:179], a[108:111]\n\t"
"v_cvt_pk_bf16_f32 v229, v230, v231\n\t"
"v_cvt_pk_bf16_f32 v230, v236, v237\n\t"
"v_mfma_f32_16x16x32_bf16 a[108:111], a[224:227], a[172:175], a[108:111]\n\t"
"v_cvt_pk_bf16_f32 v231, v238, v239\n\t"
"v_cvt_pk_bf16_f32 v232, v232, v233\n\t"
"v_mfma_f32_16x16x32_bf16 a[116:119], a[220:223], a[172:175], a[116:119]\n\t"
"ds_read_b128 a[172:175], v223 offset:8704\n\t"
"v_cvt_pk_bf16_f32 v233, v234, v235\n\t"
"v_mfma_f32_16x16x32_bf16 a[116:119], a[224:227], a[176:179], a[116:119]\n\t"
"v_cvt_pk_bf16_f32 v234, v224, v225\n\t"
"v_cvt_pk_bf16_f32 v235, v226, v227\n\t"
"v_mfma_f32_16x16x32_bf16 a[116:119], a[228:231], a[180:183], a[116:119]\n\t"
"ds_write_b128 v245, v[228:231] offset:13824\n\t"
"v_mfma_f32_16x16x32_bf16 a[124:127], a[228:231], a[184:187], a[124:127]\n\t"
"buffer_load_dwordx4 v[96:99], off, s[80:83], s20 offset:0 sc1\n\t"
"v_mfma_f32_16x16x32_bf16 a[124:127], a[224:227], a[180:183], a[124:127]\n\t"
"v_mfma_f32_16x16x32_bf16 a[124:127], a[220:223], a[176:179], a[124:127]\n\t"
"ds_write_b128 v245, v[232:235] offset:21504\n\t"
"v_mfma_f32_16x16x32_bf16 a[132:135], a[220:223], a[180:183], a[132:135]\n\t"
"v_mfma_f32_16x16x32_bf16 a[132:135], a[224:227], a[184:187], a[132:135]\n\t"
"ds_read_b128 a[176:179], v223 offset:14848\n\t"
"v_mfma_f32_16x16x32_bf16 a[132:135], a[228:231], a[188:191], a[132:135]\n\t"
"ds_read_b64_tr_b16 v[236:237], v246 offset:27648\n\t"
"v_mfma_f32_16x16x32_bf16 a[140:143], a[228:231], a[192:195], a[140:143]\n\t"
"ds_read_b64_tr_b16 v[224:225], v255 offset:27648\n\t"
"v_mfma_f32_16x16x32_bf16 a[140:143], a[224:227], a[188:191], a[140:143]\n\t"
"ds_read_b128 a[180:183], v222 offset:22528\n\t"
"v_mfma_f32_16x16x32_bf16 a[140:143], a[220:223], a[184:187], a[140:143]\n\t"
"ds_read_b64_tr_b16 v[228:229], v246 offset:33792\n\t"
"v_mfma_f32_16x16x32_bf16 a[148:151], a[220:223], a[188:191], a[148:151]\n\t"
"ds_read_b64_tr_b16 v[232:233], v255 offset:33792\n\t"
"v_mfma_f32_16x16x32_bf16 a[148:151], a[224:227], a[192:195], a[148:151]\n\t"
"ds_read_b128 a[184:187], v223 offset:28672\n\t"
"v_mfma_f32_16x16x32_bf16 a[148:151], a[228:231], a[196:199], a[148:151]\n\t"
"ds_read_b128 a[188:191], v223 offset:34816\n\t"
"v_mfma_f32_16x16x32_bf16 a[156:159], a[228:231], a[200:203], a[156:159]\n\t"
"v_mfma_f32_16x16x32_bf16 a[156:159], a[224:227], a[196:199], a[156:159]\n\t"
"v_mfma_f32_16x16x32_bf16 a[156:159], a[220:223], a[192:195], a[156:159]\n\t"
"ds_read_b128 a[192:195], v222 offset:42496\n\t"
"v_mfma_f32_16x16x32_bf16 a[164:167], a[220:223], a[196:199], a[164:167]\n\t"
"s_waitcnt lgkmcnt(7)\n\t"
"ds_read_b128 a[196:199], v223 offset:48640\n\t"
"v_mfma_f32_16x16x32_bf16 a[164:167], a[224:227], a[200:203], a[164:167]\n\t"
"s_waitcnt lgkmcnt(7)\n\t"
"ds_read_b128 a[200:203], v223 offset:54784\n\t"
"v_mfma_f32_16x16x32_bf16 a[164:167], a[228:231], a[204:207], a[164:167]\n\t"
"s_waitcnt lgkmcnt(6)\n\t"
"ds_read_b128 a[204:207], v222 offset:62464\n\t"
"v_mfma_f32_4x4x4_16b_bf16 v[236:239], v[236:237], v[220:221], 0\n\t"
"s_waitcnt lgkmcnt(6)\n\t"
"v_mfma_f32_4x4x4_16b_bf16 v[224:227], v[224:225], v[220:221], 0\n\t"
"s_waitcnt vmcnt(12)\n\t"
"v_mfma_f32_4x4x4_16b_bf16 v[228:231], v[228:229], v[220:221], 0\n\t"
"v_mfma_f32_4x4x4_16b_bf16 v[232:235], v[232:233], v[220:221], 0\n\t"
"Lst7_%=:\n\t"
"v_mfma_f32_16x16x32_bf16 v[0:3], a[232:235], a[168:171], v[0:3]\n\t"
"v_cvt_pk_bf16_f32 v236, v236, v237\n\t"
"v_cvt_pk_bf16_f32 v237, v238, v239\n\t"
"v_mfma_f32_16x16x32_bf16 v[0:3], v[240:243], a[176:179], v[0:3]\n\t"
"v_cvt_pk_bf16_f32 v238, v224, v225\n\t"
"v_cvt_pk_bf16_f32 v239, v226, v227\n\t"
"v_mfma_f32_16x16x32_bf16 v[0:3], a[208:211], a[172:175], v[0:3]\n\t"
"ds_write_b128 v245, v[236:239] offset:27648\n\t"
"v_mfma_f32_16x16x32_bf16 v[4:7], a[232:235], a[172:175], v[4:7]\n\t"
"s_waitcnt lgkmcnt(6)\n\t"
"v_cvt_pk_bf16_f32 v228, v228, v229\n\t"
"v_cvt_pk_bf16_f32 v229, v230, v231\n\t"
"v_mfma_f32_16x16x32_bf16 v[4:7], a[208:211], a[176:179], v[4:7]\n\t"
"v_cvt_pk_bf16_f32 v230, v232, v233\n\t"
"v_cvt_pk_bf16_f32 v231, v234, v235\n\t"
"v_mfma_f32_16x16x32_bf16 v[4:7], v[240:243], a[180:183], v[4:7]\n\t"
"ds_write_b128 v245, v[228:231] offset:33792\n\t"
"v_mfma_f32_16x16x32_bf16 v[8:11], v[240:243], a[184:187], v[8:11]\n\t"
"s_waitcnt lgkmcnt(6)\n\t"
"v_mfma_f32_16x16x32_bf16 v[8:11], a[208:211], a[180:183], v[8:11]\n\t"
"v_mfma_f32_16x16x32_bf16 v[8:11], a[232:235], a[176:179], v[8:11]\n\t"
"ds_read_b64_tr_b16 v[224:225], v246 offset:41472\n\t"
"v_mfma_f32_16x16x32_bf16 v[12:15], a[232:235], a[180:183], v[12:15]\n\t"
"s_waitcnt lgkmcnt(6)\n\t"
"ds_read_b64_tr_b16 v[232:233], v255 offset:41472\n\t"
"v_mfma_f32_16x16x32_bf16 v[12:15], a[208:211], a[184:187], v[12:15]\n\t"
"ds_read_b64_tr_b16 v[236:237], v246 offset:47616\n\t"
"v_mfma_f32_16x16x32_bf16 v[12:15], v[240:243], a[188:191], v[12:15]\n\t"
"buffer_load_dwordx4 v[248:251], off, s[80:83], s20 offset:1024 sc1\n\t"
"v_mfma_f32_16x16x32_bf16 v[16:19], v[240:243], a[192:195], v[16:19]\n\t"
"s_waitcnt lgkmcnt(7)\n\t"
"ds_read_b64_tr_b16 v[228:229], v255 offset:47616\n\t"
"v_mfma_f32_16x16x32_bf16 v[16:19], a[208:211], a[188:191], v[16:19]\n\t"
"v_mfma_f32_16x16x32_bf16 v[16:19], a[232:235], a[184:187], v[16:19]\n\t"
"buffer_load_dwordx4 a[212:215], off, s[80:83], s20 offset:2048 sc1\n\t"
"v_mfma_f32_16x16x32_bf16 v[20:23], a[232:235], a[188:191], v[20:23]\n\t"
"s_waitcnt lgkmcnt(7)\n\t"
"s_mov_b32 s20, 663552\n\t"
"v_mfma_f32_16x16x32_bf16 v[20:23], a[208:211], a[192:195], v[20:23]\n\t"
"v_mfma_f32_16x16x32_bf16 v[20:23], v[240:243], a[196:199], v[20:23]\n\t"
"v_mfma_f32_16x16x32_bf16 v[24:27], v[240:243], a[200:203], v[24:27]\n\t"
"buffer_load_dwordx4 a[216:219], off, s[80:83], s20 offset:0 sc1\n\t"
"v_mfma_f32_16x16x32_bf16 v[24:27], a[208:211], a[196:199], v[24:27]\n\t"
"s_waitcnt lgkmcnt(6)\n\t"
"v_mfma_f32_16x16x32_bf16 v[24:27], a[232:235], a[192:195], v[24:27]\n\t"
"v_mfma_f32_16x16x32_bf16 v[28:31], a[232:235], a[196:199], v[28:31]\n\t"
"s_waitcnt lgkmcnt(3)\n\t"
"v_mfma_f32_16x16x32_bf16 v[28:31], a[208:211], a[200:203], v[28:31]\n\t"
"buffer_load_dwordx4 a[208:211], off, s[80:83], s20 offset:1024 sc1\n\t"
"v_mfma_f32_16x16x32_bf16 v[28:31], v[240:243], a[204:207], v[28:31]\n\t"
"s_waitcnt lgkmcnt(1)\n\t"
"v_mfma_f32_4x4x4_16b_bf16 v[224:227], v[224:225], v[220:221], 0\n\t"
"s_waitcnt lgkmcnt(0)\n\t"
"v_mfma_f32_4x4x4_16b_bf16 v[232:235], v[232:233], v[220:221], 0\n\t"
"s_waitcnt vmcnt(13)\n\t"
"v_mfma_f32_4x4x4_16b_bf16 v[236:239], v[236:237], v[220:221], 0\n\t"
"v_mfma_f32_4x4x4_16b_bf16 v[228:231], v[228:229], v[220:221], 0\n\t"
"v_mfma_f32_16x16x32_bf16 v[32:35], a[236:239], a[168:171], v[32:35]\n\t"
"buffer_load_dwordx4 v[240:243], off, s[80:83], s20 offset:2048 sc1\n\t"
"v_mfma_f32_16x16x32_bf16 v[32:35], a[244:247], a[176:179], v[32:35]\n\t"
"s_mov_b32 s20, 675840\n\t"
"v_cvt_pk_bf16_f32 v224, v224, v225\n\t"
"v_cvt_pk_bf16_f32 v225, v226, v227\n\t"
"v_mfma_f32_16x16x32_bf16 v[32:35], a[240:243], a[172:175], v[32:35]\n\t"
"v_cvt_pk_bf16_f32 v226, v232, v233\n\t"
"v_cvt_pk_bf16_f32 v227, v234, v235\n\t"
"v_mfma_f32_16x16x32_bf16 v[36:39], a[236:239], a[172:175], v[36:39]\n\t"
"v_cvt_pk_bf16_f32 v236, v236, v237\n\t"
"v_cvt_pk_bf16_f32 v237, v238, v239\n\t"
"v_mfma_f32_16x16x32_bf16 v[36:39], a[240:243], a[176:179], v[36:39]\n\t"
"v_cvt_pk_bf16_f32 v238, v228, v229\n\t"
"v_cvt_pk_bf16_f32 v239, v230, v231\n\t"
"v_mfma_f32_16x16x32_bf16 v[36:39], a[244:247], a[180:183], v[36:39]\n\t"
"buffer_load_dwordx4 a[220:223], off, s[80:83], s20 offset:0 sc1\n\t"
"v_mfma_f32_16x16x32_bf16 v[40:43], a[244:247], a[184:187], v[40:43]\n\t"
"buffer_load_dwordx4 a[224:227], off, s[80:83], s20 offset:1024 sc1\n\t"
"v_mfma_f32_16x16x32_bf16 v[40:43], a[240:243], a[180:183], v[40:43]\n\t"
"v_mfma_f32_16x16x32_bf16 v[40:43], a[236:239], a[176:179], v[40:43]\n\t"
"ds_write_b128 v245, v[224:227] offset:41472\n\t"
"v_mfma_f32_16x16x32_bf16 v[44:47], a[236:239], a[180:183], v[44:47]\n\t"
"v_mfma_f32_16x16x32_bf16 v[44:47], a[240:243], a[184:187], v[44:47]\n\t"
"ds_read_b64_tr_b16 v[232:233], v246 offset:53760\n\t"
"v_mfma_f32_16x16x32_bf16 v[44:47], a[244:247], a[188:191], v[44:47]\n\t"
"ds_write_b128 v245, v[236:239] offset:47616\n\t"
"v_mfma_f32_16x16x32_bf16 v[48:51], a[244:247], a[192:195], v[48:51]\n\t"
"buffer_load_dwordx4 v[236:239], off, s[80:83], s20 offset:2048 sc1\n\t"
"v_mfma_f32_16x16x32_bf16 v[48:51], a[240:243], a[188:191], v[48:51]\n\t"
"s_mov_b32 s20, 688128\n\t"
"ds_read_b64_tr_b16 v[228:229], v255 offset:53760\n\t"
"v_mfma_f32_16x16x32_bf16 v[48:51], a[236:239], a[184:187], v[48:51]\n\t"
"buffer_load_dwordx4 a[228:231], off, s[80:83], s20 offset:0 sc1\n\t"
"v_mfma_f32_16x16x32_bf16 v[52:55], a[236:239], a[188:191], v[52:55]\n\t"
"v_mfma_f32_16x16x32_bf16 v[52:55], a[240:243], a[192:195], v[52:55]\n\t"
"v_mfma_f32_16x16x32_bf16 v[52:55], a[244:247], a[196:199], v[52:55]\n\t"
"v_mfma_f32_16x16x32_bf16 v[56:59], a[244:247], a[200:203], v[56:59]\n\t"
"buffer_load_dwordx4 a[232:235], off, s[80:83], s20 offset:1024 sc1\n\t"
"v_mfma_f32_16x16x32_bf16 v[56:59], a[240:243], a[196:199], v[56:59]\n\t"
"v_mfma_f32_16x16x32_bf16 v[56:59], a[236:239], a[192:195], v[56:59]\n\t"
"v_mfma_f32_16x16x32_bf16 v[60:63], a[236:239], a[196:199], v[60:63]\n\t"
"s_waitcnt lgkmcnt(2)\n\t"
"v_mfma_f32_16x16x32_bf16 v[60:63], a[240:243], a[200:203], v[60:63]\n\t"
"buffer_load_dwordx4 a[236:239], off, s[80:83], s20 offset:2048 sc1\n\t"
"v_mfma_f32_16x16x32_bf16 v[60:63], a[244:247], a[204:207], v[60:63]\n\t"
"s_waitcnt vmcnt(9) lgkmcnt(0)\n\t"
"s_mov_b32 s20, 921600\n\t"
"v_mfma_f32_4x4x4_16b_bf16 v[232:235], v[232:233], v[220:221], 0\n\t"
"v_mfma_f32_4x4x4_16b_bf16 v[228:231], v[228:229], v[220:221], 0\n\t"
"v_mfma_f32_16x16x32_bf16 v[64:67], v[96:99], a[168:171], v[64:67]\n\t"
"buffer_load_dwordx4 a[240:243], off, s[80:83], s20 offset:0 sc1\n\t"
"v_mfma_f32_16x16x32_bf16 v[64:67], a[212:215], a[176:179], v[64:67]\n\t"
"ds_read_b128 a[168:171], v222 offset:2816\n\t"
"v_cvt_pk_bf16_f32 v232, v232, v233\n\t"
"v_mfma_f32_16x16x32_bf16 v[64:67], v[248:251], a[172:175], v[64:67]\n\t"
"v_cvt_pk_bf16_f32 v233, v234, v235\n\t"
"v_cvt_pk_bf16_f32 v234, v228, v229\n\t"
"v_mfma_f32_16x16x32_bf16 v[68:71], v[96:99], a[172:175], v[68:71]\n\t"
"ds_read_b128 a[172:175], v223 offset:8960\n\t"
"v_cvt_pk_bf16_f32 v235, v230, v231\n\t"
"v_mfma_f32_16x16x32_bf16 v[68:71], v[248:251], a[176:179], v[68:71]\n\t"
"v_mfma_f32_16x16x32_bf16 v[68:71], a[212:215], a[180:183], v[68:71]\n\t"
"buffer_load_dwordx4 a[244:247], off, s[80:83], s20 offset:1024 sc1\n\t"
"v_mfma_f32_16x16x32_bf16 v[72:75], a[212:215], a[184:187], v[72:75]\n\t"
"buffer_load_dwordx4 a[248:251], off, s[80:83], s20 offset:2048 sc1\n\t"
"v_mfma_f32_16x16x32_bf16 v[72:75], v[248:251], a[180:183], v[72:75]\n\t"
"s_mov_b32 s20, 933888\n\t"
"v_mfma_f32_16x16x32_bf16 v[72:75], v[96:99], a[176:179], v[72:75]\n\t"
"ds_write_b128 v245, v[232:235] offset:53760\n\t"
"v_mfma_f32_16x16x32_bf16 v[76:79], v[96:99], a[180:183], v[76:79]\n\t"
"buffer_load_dwordx4 a[252:255], off, s[80:83], s20 offset:0 sc1\n\t"
"v_mfma_f32_16x16x32_bf16 v[76:79], v[248:251], a[184:187], v[76:79]\n\t"
"ds_read_b128 a[176:179], v223 offset:15104\n\t"
"v_mfma_f32_16x16x32_bf16 v[76:79], a[212:215], a[188:191], v[76:79]\n\t"
"ds_read_b128 a[180:183], v222 offset:22784\n\t"
"v_mfma_f32_16x16x32_bf16 v[80:83], a[212:215], a[192:195], v[80:83]\n\t"
"v_mfma_f32_16x16x32_bf16 v[80:83], v[248:251], a[188:191], v[80:83]\n\t"
"v_mfma_f32_16x16x32_bf16 v[80:83], v[96:99], a[184:187], v[80:83]\n\t"
"ds_read_b128 a[184:187], v223 offset:28928\n\t"
"v_mfma_f32_16x16x32_bf16 v[84:87], v[96:99], a[188:191], v[84:87]\n\t"
"s_waitcnt vmcnt(10) lgkmcnt(2)\n\t"
"ds_read_b128 a[188:191], v223 offset:35072\n\t"
"v_mfma_f32_16x16x32_bf16 v[84:87], v[248:251], a[192:195], v[84:87]\n\t"
"v_mfma_f32_16x16x32_bf16 v[84:87], a[212:215], a[196:199], v[84:87]\n\t"
"s_waitcnt lgkmcnt(2)\n\t"
"v_mfma_f32_16x16x32_bf16 v[88:91], a[212:215], a[200:203], v[88:91]\n\t"
"Lpg6_%=:\n\t"
"s_cbranch_vccnz Lpg10_%=\n\t"
"v_mfma_f32_16x16x32_bf16 v[88:91], v[248:251], a[196:199], v[88:91]\n\t"
"v_mfma_f32_16x16x32_bf16 v[88:91], v[96:99], a[192:195], v[88:91]\n\t"
"ds_read_b128 a[192:195], v222 offset:42752\n\t"
"v_mfma_f32_16x16x32_bf16 v[92:95], v[96:99], a[196:199], v[92:95]\n\t"
"buffer_load_dwordx4 v[96:99], off, s[80:83], s20 offset:1024 sc1\n\t"
"v_mfma_f32_16x16x32_bf16 v[92:95], v[248:251], a[200:203], v[92:95]\n\t"
"ds_read_b128 a[196:199], v223 offset:48896\n\t"
"v_mfma_f32_16x16x32_bf16 v[92:95], a[212:215], a[204:207], v[92:95]\n\t"
"ds_read_b128 a[200:203], v223 offset:55040\n\t"
"v_mfma_f32_16x16x32_bf16 v[100:103], a[216:219], a[168:171], v[100:103]\n\t"
"ds_read_b128 a[204:207], v222 offset:62720\n\t"
"v_mfma_f32_16x16x32_bf16 v[100:103], v[240:243], a[176:179], v[100:103]\n\t"
"buffer_load_dwordx4 v[248:251], off, s[80:83], s20 offset:2048 sc1\n\t"
"v_mfma_f32_16x16x32_bf16 v[100:103], a[208:211], a[172:175], v[100:103]\n\t"
"s_mov_b32 s20, 946176\n\t"
"s_add_u32 m0, s24, 1792\n\t"
"s_add_u32 s28, s21, 3072\n\t"
"v_mfma_f32_16x16x32_bf16 v[104:107], a[216:219], a[172:175], v[104:107]\n\t"
"buffer_load_dwordx4 a[212:215], off, s[80:83], s20 offset:0 sc1\n\t"
"v_mfma_f32_16x16x32_bf16 v[104:107], a[208:211], a[176:179], v[104:107]\n\t"
"s_waitcnt lgkmcnt(5)\n\t"
"v_mfma_f32_16x16x32_bf16 v[104:107], v[240:243], a[180:183], v[104:107]\n\t"
"s_waitcnt lgkmcnt(0)\n\t"
"s_barrier\n\t"
"v_mfma_f32_16x16x32_bf16 v[108:111], v[240:243], a[184:187], v[108:111]\n\t"
"v_mfma_f32_16x16x32_bf16 v[108:111], a[208:211], a[180:183], v[108:111]\n\t"
"buffer_load_dwordx4 v247, s[12:15], s28 offen offset:256 nt lds\n\t"
"v_mfma_f32_16x16x32_bf16 v[108:111], a[216:219], a[176:179], v[108:111]\n\t"
"s_add_u32 m0, s24, 7936\n\t"
"s_add_u32 s28, s21, 199680\n\t"
"v_mfma_f32_16x16x32_bf16 v[112:115], a[216:219], a[180:183], v[112:115]\n\t"
"buffer_load_dwordx4 v247, s[12:15], s28 offen offset:256 nt lds\n\t"
"v_mfma_f32_16x16x32_bf16 v[112:115], a[208:211], a[184:187], v[112:115]\n\t"
"s_add_u32 m0, s24, 14080\n\t"
"s_add_u32 s28, s21, 396288\n\t"
"v_mfma_f32_16x16x32_bf16 v[112:115], v[240:243], a[188:191], v[112:115]\n\t"
"buffer_load_dwordx4 v247, s[12:15], s28 offen offset:256 nt lds\n\t"
"s_add_u32 m0, s24, 21760\n\t"
"s_add_u32 s28, s21, 592896\n\t"
"v_mfma_f32_16x16x32_bf16 v[116:119], v[240:243], a[192:195], v[116:119]\n\t"
"buffer_load_dwordx4 v247, s[12:15], s28 offen offset:256 nt lds\n\t"
"v_mfma_f32_16x16x32_bf16 v[116:119], a[208:211], a[188:191], v[116:119]\n\t"
"s_add_u32 m0, s24, 27904\n\t"
"s_add_u32 s28, s21, 789504\n\t"
"v_mfma_f32_16x16x32_bf16 v[116:119], a[216:219], a[184:187], v[116:119]\n\t"
"buffer_load_dwordx4 v247, s[12:15], s28 offen offset:256 nt lds\n\t"
"v_mfma_f32_16x16x32_bf16 v[120:123], a[216:219], a[188:191], v[120:123]\n\t"
"s_add_u32 m0, s24, 34048\n\t"
"s_add_u32 s28, s21, 986112\n\t"
"v_mfma_f32_16x16x32_bf16 v[120:123], a[208:211], a[192:195], v[120:123]\n\t"
"buffer_load_dwordx4 v247, s[12:15], s28 offen offset:256 nt lds\n\t"
"v_mfma_f32_16x16x32_bf16 v[120:123], v[240:243], a[196:199], v[120:123]\n\t"
"v_mfma_f32_16x16x32_bf16 v[124:127], v[240:243], a[200:203], v[124:127]\n\t"
"v_mfma_f32_16x16x32_bf16 v[124:127], a[208:211], a[196:199], v[124:127]\n\t"
"v_mfma_f32_16x16x32_bf16 v[124:127], a[216:219], a[192:195], v[124:127]\n\t"
"v_mfma_f32_16x16x32_bf16 v[128:131], a[216:219], a[196:199], v[128:131]\n\t"
"s_waitcnt vmcnt(16)\n\t"
"v_mfma_f32_16x16x32_bf16 v[128:131], a[208:211], a[200:203], v[128:131]\n\t"
"v_mfma_f32_16x16x32_bf16 v[128:131], v[240:243], a[204:207], v[128:131]\n\t"
"v_mfma_f32_16x16x32_bf16 v[132:135], a[220:223], a[168:171], v[132:135]\n\t"
"v_mfma_f32_16x16x32_bf16 v[132:135], v[236:239], a[176:179], v[132:135]\n\t"
"v_mfma_f32_16x16x32_bf16 v[132:135], a[224:227], a[172:175], v[132:135]\n\t"
"v_mfma_f32_16x16x32_bf16 v[136:139], a[220:223], a[172:175], v[136:139]\n\t"
"v_mfma_f32_16x16x32_bf16 v[136:139], a[224:227], a[176:179], v[136:139]\n\t"
"v_mfma_f32_16x16x32_bf16 v[136:139], v[236:239], a[180:183], v[136:139]\n\t"
"v_mfma_f32_16x16x32_bf16 v[140:143], v[236:239], a[184:187], v[140:143]\n\t"
"v_mfma_f32_16x16x32_bf16 v[140:143], a[224:227], a[180:183], v[140:143]\n\t"
"v_mfma_f32_16x16x32_bf16 v[140:143], a[220:223], a[176:179], v[140:143]\n\t"
"v_mfma_f32_16x16x32_bf16 v[144:147], a[220:223], a[180:183], v[144:147]\n\t"
"v_mfma_f32_16x16x32_bf16 v[144:147], a[224:227], a[184:187], v[144:147]\n\t"
"v_mfma_f32_16x16x32_bf16 v[144:147], v[236:239], a[188:191], v[144:147]\n\t"
"v_mfma_f32_16x16x32_bf16 v[148:151], v[236:239], a[192:195], v[148:151]\n\t"
"v_mfma_f32_16x16x32_bf16 v[148:151], a[224:227], a[188:191], v[148:151]\n\t"
"v_mfma_f32_16x16x32_bf16 v[148:151], a[220:223], a[184:187], v[148:151]\n\t"
"v_mfma_f32_16x16x32_bf16 v[152:155], a[220:223], a[188:191], v[152:155]\n\t"
"v_mfma_f32_16x16x32_bf16 v[152:155], a[224:227], a[192:195], v[152:155]\n\t"
"v_mfma_f32_16x16x32_bf16 v[152:155], v[236:239], a[196:199], v[152:155]\n\t"
"v_mfma_f32_16x16x32_bf16 v[156:159], v[236:239], a[200:203], v[156:159]\n\t"
"v_mfma_f32_16x16x32_bf16 v[156:159], a[224:227], a[196:199], v[156:159]\n\t"
"v_mfma_f32_16x16x32_bf16 v[156:159], a[220:223], a[192:195], v[156:159]\n\t"
"v_mfma_f32_16x16x32_bf16 v[160:163], a[220:223], a[196:199], v[160:163]\n\t"
"s_waitcnt vmcnt(13)\n\t"
"v_mfma_f32_16x16x32_bf16 v[160:163], a[224:227], a[200:203], v[160:163]\n\t"
"v_mfma_f32_16x16x32_bf16 v[160:163], v[236:239], a[204:207], v[160:163]\n\t"
"v_mfma_f32_16x16x32_bf16 v[164:167], a[228:231], a[168:171], v[164:167]\n\t"
"ds_read_b128 a[168:171], v222 offset:3072\n\t"
"v_mfma_f32_16x16x32_bf16 v[164:167], a[236:239], a[176:179], v[164:167]\n\t"
"v_mfma_f32_16x16x32_bf16 v[164:167], a[232:235], a[172:175], v[164:167]\n\t"
"buffer_load_dwordx4 v[240:243], off, s[80:83], s20 offset:1024 sc1\n\t"
"v_mfma_f32_16x16x32_bf16 v[168:171], a[228:231], a[172:175], v[168:171]\n\t"
"ds_read_b128 a[172:175], v223 offset:9216\n\t"
"v_mfma_f32_16x16x32_bf16 v[168:171], a[232:235], a[176:179], v[168:171]\n\t"
"v_mfma_f32_16x16x32_bf16 v[168:171], a[236:239], a[180:183], v[168:171]\n\t"
"v_mfma_f32_16x16x32_bf16 v[172:175], a[236:239], a[184:187], v[172:175]\n\t"
"v_mfma_f32_16x16x32_bf16 v[172:175], a[232:235], a[180:183], v[172:175]\n\t"
"v_mfma_f32_16x16x32_bf16 v[172:175], a[228:231], a[176:179], v[172:175]\n\t"
"ds_read_b128 a[176:179], v223 offset:15360\n\t"
"v_mfma_f32_16x16x32_bf16 v[176:179], a[228:231], a[180:183], v[176:179]\n\t"
"ds_read_b128 a[180:183], v222 offset:23040\n\t"
"v_mfma_f32_16x16x32_bf16 v[176:179], a[232:235], a[184:187], v[176:179]\n\t"
"v_mfma_f32_16x16x32_bf16 v[176:179], a[236:239], a[188:191], v[176:179]\n\t"
"v_mfma_f32_16x16x32_bf16 v[180:183], a[236:239], a[192:195], v[180:183]\n\t"
"v_mfma_f32_16x16x32_bf16 v[180:183], a[232:235], a[188:191], v[180:183]\n\t"
"v_mfma_f32_16x16x32_bf16 v[180:183], a[228:231], a[184:187], v[180:183]\n\t"
"ds_read_b128 a[184:187], v223 offset:29184\n\t"
"v_mfma_f32_16x16x32_bf16 v[184:187], a[228:231], a[188:191], v[184:187]\n\t"
"ds_read_b128 a[188:191], v223 offset:35328\n\t"
"v_mfma_f32_16x16x32_bf16 v[184:187], a[232:235], a[192:195], v[184:187]\n\t"
"v_mfma_f32_16x16x32_bf16 v[184:187], a[236:239], a[196:199], v[184:187]\n\t"
"v_mfma_f32_16x16x32_bf16 v[188:191], a[236:239], a[200:203], v[188:191]\n\t"
"v_mfma_f32_16x16x32_bf16 v[188:191], a[232:235], a[196:199], v[188:191]\n\t"
"v_mfma_f32_16x16x32_bf16 v[188:191], a[228:231], a[192:195], v[188:191]\n\t"
"ds_read_b128 a[192:195], v222 offset:43008\n\t"
"v_mfma_f32_16x16x32_bf16 v[192:195], a[228:231], a[196:199], v[192:195]\n\t"
"s_waitcnt vmcnt(11) lgkmcnt(4)\n\t"
"ds_read_b128 a[196:199], v223 offset:49152\n\t"
"v_mfma_f32_16x16x32_bf16 v[192:195], a[232:235], a[200:203], v[192:195]\n\t"
"ds_read_b128 a[200:203], v223 offset:55296\n\t"
"v_mfma_f32_16x16x32_bf16 v[192:195], a[236:239], a[204:207], v[192:195]\n\t"
"ds_read_b128 a[204:207], v222 offset:62976\n\t"
"Lst8_%=:\n\t"
"v_mfma_f32_16x16x32_bf16 v[0:3], a[240:243], a[168:171], v[0:3]\n\t"
"s_mov_b64 vcc, -1\n\t"
"s_bitcmp1_b32 s73, 1\n\t"
"s_cbranch_scc1 Lvh1_%=\n\t"
"s_bitcmp1_b32 s73, 0\n\t"
"s_cbranch_scc1 Lpg14_%=\n\t"
"s_branch Lpg13_%=\n\t"
"Lvh1_%=:\n\t"
"s_bitcmp1_b32 s73, 0\n\t"
"s_cbranch_scc1 Lpg16_%=\n\t"
"s_branch Lpg15_%=\n\t"
"Lvr1_%=:\n\t"
"s_mov_b64 vcc, 0\n\t"
"s_waitcnt lgkmcnt(6)\n\t"
"v_mfma_f32_16x16x32_bf16 v[0:3], a[248:251], a[176:179], v[0:3]\n\t"
"v_mfma_f32_16x16x32_bf16 v[0:3], a[244:247], a[172:175], v[0:3]\n\t"
"v_mfma_f32_16x16x32_bf16 v[4:7], a[240:243], a[172:175], v[4:7]\n\t"
"s_waitcnt lgkmcnt(5)\n\t"
"v_mfma_f32_16x16x32_bf16 v[4:7], a[244:247], a[176:179], v[4:7]\n\t"
"v_mfma_f32_16x16x32_bf16 v[4:7], a[248:251], a[180:183], v[4:7]\n\t"
"v_mfma_f32_16x16x32_bf16 v[8:11], a[248:251], a[184:187], v[8:11]\n\t"
"s_waitcnt lgkmcnt(4)\n\t"
"v_mfma_f32_16x16x32_bf16 v[8:11], a[244:247], a[180:183], v[8:11]\n\t"
"v_mfma_f32_16x16x32_bf16 v[8:11], a[240:243], a[176:179], v[8:11]\n\t"
"buffer_load_dwordx4 a[208:211], off, s[80:83], s20 offset:2048 sc1\n\t"
"v_mfma_f32_16x16x32_bf16 v[12:15], a[240:243], a[180:183], v[12:15]\n\t"
"s_waitcnt lgkmcnt(3)\n\t"
"s_mov_b32 s20, 958464\n\t"
"v_mfma_f32_16x16x32_bf16 v[12:15], a[244:247], a[184:187], v[12:15]\n\t"
"v_mfma_f32_16x16x32_bf16 v[12:15], a[248:251], a[188:191], v[12:15]\n\t"
"v_mfma_f32_16x16x32_bf16 v[16:19], a[248:251], a[192:195], v[16:19]\n\t"
"buffer_load_dwordx4 v[236:239], off, s[80:83], s20 offset:0 sc1\n\t"
"v_mfma_f32_16x16x32_bf16 v[16:19], a[244:247], a[188:191], v[16:19]\n\t"
"s_waitcnt lgkmcnt(2)\n\t"
"v_mfma_f32_16x16x32_bf16 v[16:19], a[240:243], a[184:187], v[16:19]\n\t"
"v_mfma_f32_16x16x32_bf16 v[20:23], a[240:243], a[188:191], v[20:23]\n\t"
"s_waitcnt lgkmcnt(1)\n\t"
"v_mfma_f32_16x16x32_bf16 v[20:23], a[244:247], a[192:195], v[20:23]\n\t"
"buffer_load_dwordx4 a[216:219], off, s[80:83], s20 offset:1024 sc1\n\t"
"v_mfma_f32_16x16x32_bf16 v[20:23], a[248:251], a[196:199], v[20:23]\n\t"
"v_mfma_f32_16x16x32_bf16 v[24:27], a[248:251], a[200:203], v[24:27]\n\t"
"s_waitcnt lgkmcnt(0)\n\t"
"v_mfma_f32_16x16x32_bf16 v[24:27], a[244:247], a[196:199], v[24:27]\n\t"
"v_mfma_f32_16x16x32_bf16 v[24:27], a[240:243], a[192:195], v[24:27]\n\t"
"buffer_load_dwordx4 a[220:223], off, s[80:83], s20 offset:2048 sc1\n\t"
"v_mfma_f32_16x16x32_bf16 v[28:31], a[240:243], a[196:199], v[28:31]\n\t"
"s_waitcnt vmcnt(12)\n\t"
"s_mov_b32 s20, 970752\n\t"
"v_mfma_f32_16x16x32_bf16 v[28:31], a[244:247], a[200:203], v[28:31]\n\t"
"v_mfma_f32_16x16x32_bf16 v[28:31], a[248:251], a[204:207], v[28:31]\n\t"
"v_mfma_f32_16x16x32_bf16 v[32:35], a[252:255], a[168:171], v[32:35]\n\t"
"buffer_load_dwordx4 a[224:227], off, s[80:83], s20 offset:0 sc1\n\t"
"v_mfma_f32_16x16x32_bf16 v[32:35], v[248:251], a[176:179], v[32:35]\n\t"
"v_mfma_f32_16x16x32_bf16 v[32:35], v[96:99], a[172:175], v[32:35]\n\t"
"v_mfma_f32_16x16x32_bf16 v[36:39], a[252:255], a[172:175], v[36:39]\n\t"
"v_mfma_f32_16x16x32_bf16 v[36:39], v[96:99], a[176:179], v[36:39]\n\t"
"buffer_load_dwordx4 a[228:231], off, s[80:83], s20 offset:1024 sc1\n\t"
"v_mfma_f32_16x16x32_bf16 v[36:39], v[248:251], a[180:183], v[36:39]\n\t"
"v_mfma_f32_16x16x32_bf16 v[40:43], v[248:251], a[184:187], v[40:43]\n\t"
"v_mfma_f32_16x16x32_bf16 v[40:43], v[96:99], a[180:183], v[40:43]\n\t"
"v_mfma_f32_16x16x32_bf16 v[40:43], a[252:255], a[176:179], v[40:43]\n\t"
"buffer_load_dwordx4 a[232:235], off, s[80:83], s20 offset:2048 sc1\n\t"
"v_mfma_f32_16x16x32_bf16 v[44:47], a[252:255], a[180:183], v[44:47]\n\t"
"s_mov_b32 s20, 983040\n\t"
"v_mfma_f32_16x16x32_bf16 v[44:47], v[96:99], a[184:187], v[44:47]\n\t"
"v_mfma_f32_16x16x32_bf16 v[44:47], v[248:251], a[188:191], v[44:47]\n\t"
"v_mfma_f32_16x16x32_bf16 v[48:51], v[248:251], a[192:195], v[48:51]\n\t"
"buffer_load_dwordx4 a[236:239], off, s[80:83], s20 offset:0 sc1\n\t"
"v_mfma_f32_16x16x32_bf16 v[48:51], v[96:99], a[188:191], v[48:51]\n\t"
"v_mfma_f32_16x16x32_bf16 v[48:51], a[252:255], a[184:187], v[48:51]\n\t"
"v_mfma_f32_16x16x32_bf16 v[52:55], a[252:255], a[188:191], v[52:55]\n\t"
"v_mfma_f32_16x16x32_bf16 v[52:55], v[96:99], a[192:195], v[52:55]\n\t"
"buffer_load_dwordx4 a[240:243], off, s[80:83], s20 offset:1024 sc1\n\t"
"v_mfma_f32_16x16x32_bf16 v[52:55], v[248:251], a[196:199], v[52:55]\n\t"
"v_mfma_f32_16x16x32_bf16 v[56:59], v[248:251], a[200:203], v[56:59]\n\t"
"v_mfma_f32_16x16x32_bf16 v[56:59], v[96:99], a[196:199], v[56:59]\n\t"
"v_mfma_f32_16x16x32_bf16 v[56:59], a[252:255], a[192:195], v[56:59]\n\t"
"buffer_load_dwordx4 a[244:247], off, s[80:83], s20 offset:2048 sc1\n\t"
"v_mfma_f32_16x16x32_bf16 v[60:63], a[252:255], a[196:199], v[60:63]\n\t"
"s_waitcnt vmcnt(9)\n\t"
"s_mov_b32 s20, 995328\n\t"
"v_mfma_f32_16x16x32_bf16 v[60:63], v[96:99], a[200:203], v[60:63]\n\t"
"v_mfma_f32_16x16x32_bf16 v[60:63], v[248:251], a[204:207], v[60:63]\n\t"
"v_mfma_f32_16x16x32_bf16 v[64:67], a[212:215], a[168:171], v[64:67]\n\t"
"buffer_load_dwordx4 v[96:99], off, s[80:83], s20 offset:0 sc1\n\t"
"v_mfma_f32_16x16x32_bf16 v[64:67], a[208:211], a[176:179], v[64:67]\n\t"
"ds_read_b128 a[168:171], v222 offset:3328\n\t"
"v_mfma_f32_16x16x32_bf16 v[64:67], v[240:243], a[172:175], v[64:67]\n\t"
"v_mfma_f32_16x16x32_bf16 v[68:71], a[212:215], a[172:175], v[68:71]\n\t"
"buffer_load_dwordx4 v[248:251], off, s[80:83], s20 offset:1024 sc1\n\t"
"v_mfma_f32_16x16x32_bf16 v[68:71], v[240:243], a[176:179], v[68:71]\n\t"
"ds_read_b128 a[172:175], v223 offset:9472\n\t"
"v_mfma_f32_16x16x32_bf16 v[68:71], a[208:211], a[180:183], v[68:71]\n\t"
"v_mfma_f32_16x16x32_bf16 v[72:75], a[208:211], a[184:187], v[72:75]\n\t"
"buffer_load_dwordx4 a[248:251], off, s[80:83], s20 offset:2048 sc1\n\t"
"v_mfma_f32_16x16x32_bf16 v[72:75], v[240:243], a[180:183], v[72:75]\n\t"
"s_mov_b32 s20, 1007616\n\t"
"v_mfma_f32_16x16x32_bf16 v[72:75], a[212:215], a[176:179], v[72:75]\n\t"
"ds_read_b128 a[176:179], v223 offset:15616\n\t"
"v_mfma_f32_16x16x32_bf16 v[76:79], a[212:215], a[180:183], v[76:79]\n\t"
"buffer_load_dwordx4 a[252:255], off, s[80:83], s20 offset:0 sc1\n\t"
"v_mfma_f32_16x16x32_bf16 v[76:79], v[240:243], a[184:187], v[76:79]\n\t"
"ds_read_b128 a[180:183], v222 offset:23296\n\t"
"v_mfma_f32_16x16x32_bf16 v[76:79], a[208:211], a[188:191], v[76:79]\n\t"
"v_mfma_f32_16x16x32_bf16 v[80:83], a[208:211], a[192:195], v[80:83]\n\t"
"v_mfma_f32_16x16x32_bf16 v[80:83], v[240:243], a[188:191], v[80:83]\n\t"
"v_mfma_f32_16x16x32_bf16 v[80:83], a[212:215], a[184:187], v[80:83]\n\t"
"ds_read_b128 a[184:187], v223 offset:29440\n\t"
"v_mfma_f32_16x16x32_bf16 v[84:87], a[212:215], a[188:191], v[84:87]\n\t"
"ds_read_b128 a[188:191], v223 offset:35584\n\t"
"v_mfma_f32_16x16x32_bf16 v[84:87], v[240:243], a[192:195], v[84:87]\n\t"
"v_mfma_f32_16x16x32_bf16 v[84:87], a[208:211], a[196:199], v[84:87]\n\t"
"v_mfma_f32_16x16x32_bf16 v[88:91], a[208:211], a[200:203], v[88:91]\n\t"
"v_mfma_f32_16x16x32_bf16 v[88:91], v[240:243], a[196:199], v[88:91]\n\t"
"v_mfma_f32_16x16x32_bf16 v[88:91], a[212:215], a[192:195], v[88:91]\n\t"
"ds_read_b128 a[192:195], v222 offset:43264\n\t"
"v_mfma_f32_16x16x32_bf16 v[92:95], a[212:215], a[196:199], v[92:95]\n\t"
"buffer_load_dwordx4 a[212:215], off, s[80:83], s20 offset:1024 sc1\n\t"
"v_mfma_f32_16x16x32_bf16 v[92:95], v[240:243], a[200:203], v[92:95]\n\t"
"s_waitcnt vmcnt(11) lgkmcnt(4)\n\t"
"ds_read_b128 a[196:199], v223 offset:49408\n\t"
"v_mfma_f32_16x16x32_bf16 v[92:95], a[208:211], a[204:207], v[92:95]\n\t"
"global_load_dwordx4 v[240:243], v252, s[48:49] offset:2048\n\t"
"v_mfma_f32_16x16x32_bf16 v[100:103], v[236:239], a[168:171], v[100:103]\n\t"
"s_waitcnt lgkmcnt(4)\n\t"
"ds_read_b128 a[200:203], v223 offset:55552\n\t"
"v_mfma_f32_16x16x32_bf16 v[100:103], a[220:223], a[176:179], v[100:103]\n\t"
"ds_read_b128 a[204:207], v222 offset:63232\n\t"
"v_mfma_f32_16x16x32_bf16 v[100:103], a[216:219], a[172:175], v[100:103]\n\t"
"v_mfma_f32_16x16x32_bf16 v[104:107], v[236:239], a[172:175], v[104:107]\n\t"
"s_waitcnt lgkmcnt(5)\n\t"
"v_mfma_f32_16x16x32_bf16 v[104:107], a[216:219], a[176:179], v[104:107]\n\t"
"global_load_dwordx4 a[208:211], v252, s[46:47] offset:1024\n\t"
"v_mfma_f32_16x16x32_bf16 v[104:107], a[220:223], a[180:183], v[104:107]\n\t"
"v_mfma_f32_16x16x32_bf16 v[108:111], a[220:223], a[184:187], v[108:111]\n\t"
"s_waitcnt lgkmcnt(4)\n\t"
"v_mfma_f32_16x16x32_bf16 v[108:111], a[216:219], a[180:183], v[108:111]\n\t"
"v_mfma_f32_16x16x32_bf16 v[108:111], v[236:239], a[176:179], v[108:111]\n\t"
"v_mfma_f32_16x16x32_bf16 v[112:115], v[236:239], a[180:183], v[112:115]\n\t"
"s_waitcnt lgkmcnt(3)\n\t"
"v_mfma_f32_16x16x32_bf16 v[112:115], a[216:219], a[184:187], v[112:115]\n\t"
"v_mfma_f32_16x16x32_bf16 v[112:115], a[220:223], a[188:191], v[112:115]\n\t"
"v_mfma_f32_16x16x32_bf16 v[116:119], a[220:223], a[192:195], v[116:119]\n\t"
"s_waitcnt lgkmcnt(2)\n\t"
"v_mfma_f32_16x16x32_bf16 v[116:119], a[216:219], a[188:191], v[116:119]\n\t"
"v_mfma_f32_16x16x32_bf16 v[116:119], v[236:239], a[184:187], v[116:119]\n\t"
"v_mfma_f32_16x16x32_bf16 v[120:123], v[236:239], a[188:191], v[120:123]\n\t"
"s_waitcnt lgkmcnt(1)\n\t"
"v_mfma_f32_16x16x32_bf16 v[120:123], a[216:219], a[192:195], v[120:123]\n\t"
"v_mfma_f32_16x16x32_bf16 v[120:123], a[220:223], a[196:199], v[120:123]\n\t"
"v_mfma_f32_16x16x32_bf16 v[124:127], a[220:223], a[200:203], v[124:127]\n\t"
"s_waitcnt lgkmcnt(0)\n\t"
"v_mfma_f32_16x16x32_bf16 v[124:127], a[216:219], a[196:199], v[124:127]\n\t"
"v_mfma_f32_16x16x32_bf16 v[124:127], v[236:239], a[192:195], v[124:127]\n\t"
"v_mfma_f32_16x16x32_bf16 v[128:131], v[236:239], a[196:199], v[128:131]\n\t"
"s_waitcnt vmcnt(10)\n\t"
"v_mfma_f32_16x16x32_bf16 v[128:131], a[216:219], a[200:203], v[128:131]\n\t"
"buffer_load_dwordx4 a[216:219], off, s[80:83], s20 offset:2048 sc1\n\t"
"v_mfma_f32_16x16x32_bf16 v[128:131], a[220:223], a[204:207], v[128:131]\n\t"
"s_mov_b32 s20, 1019904\n\t"
"s_add_u32 m0, s24, 40192\n\t"
"s_add_u32 s28, s21, 1179648\n\t"
"v_mfma_f32_16x16x32_bf16 v[132:135], a[224:227], a[168:171], v[132:135]\n\t"
"v_mfma_f32_16x16x32_bf16 v[132:135], a[232:235], a[176:179], v[132:135]\n\t"
"buffer_load_dwordx4 a[220:223], off, s[80:83], s20 offset:0 sc1\n\t"
"v_mfma_f32_16x16x32_bf16 v[132:135], a[228:231], a[172:175], v[132:135]\n\t"
"v_mfma_f32_16x16x32_bf16 v[136:139], a[224:227], a[172:175], v[136:139]\n\t"
"buffer_load_dwordx4 v247, s[12:15], s28 offen offset:256 nt lds\n\t"
"v_mfma_f32_16x16x32_bf16 v[136:139], a[228:231], a[176:179], v[136:139]\n\t"
"s_add_u32 m0, s24, 46336\n\t"
"s_add_u32 s28, s21, 1376256\n\t"
"v_mfma_f32_16x16x32_bf16 v[136:139], a[232:235], a[180:183], v[136:139]\n\t"
"s_barrier\n\t"
"v_mfma_f32_16x16x32_bf16 v[140:143], a[232:235], a[184:187], v[140:143]\n\t"
"v_mfma_f32_16x16x32_bf16 v[140:143], a[228:231], a[180:183], v[140:143]\n\t"
"buffer_load_dwordx4 v247, s[12:15], s28 offen offset:256 nt lds\n\t"
"v_mfma_f32_16x16x32_bf16 v[140:143], a[224:227], a[176:179], v[140:143]\n\t"
"s_add_u32 m0, s24, 52480\n\t"
"s_add_u32 s28, s21, 1572864\n\t"
"s_waitcnt vmcnt(5)\n\t"
"ds_read_b64_tr_b16 v[224:225], v240 offset:512\n\t"
"v_mfma_f32_16x16x32_bf16 v[144:147], a[224:227], a[180:183], v[144:147]\n\t"
"buffer_load_dwordx4 v247, s[12:15], s28 offen offset:256 nt lds\n\t"
"v_mfma_f32_16x16x32_bf16 v[144:147], a[228:231], a[184:187], v[144:147]\n\t"
"s_add_u32 m0, s24, 41728\n\t"
"s_add_u32 s28, s21, 1182720\n\t"
"ds_read_b64_tr_b16 v[226:227], v240 offset:2048\n\t"
"v_mfma_f32_16x16x32_bf16 v[144:147], a[232:235], a[188:191], v[144:147]\n\t"
"buffer_load_dwordx4 v247, s[12:15], s28 offen offset:256 nt lds\n\t"
"s_add_u32 m0, s24, 47872\n\t"
"s_add_u32 s28, s21, 1379328\n\t"
"v_mfma_f32_16x16x32_bf16 v[148:151], a[232:235], a[192:195], v[148:151]\n\t"
"buffer_load_dwordx4 v247, s[12:15], s28 offen offset:256 nt lds\n\t"
"v_mfma_f32_16x16x32_bf16 v[148:151], a[228:231], a[188:191], v[148:151]\n\t"
"s_add_u32 m0, s24, 54016\n\t"
"s_add_u32 s28, s21, 1575936\n\t"
"ds_read_b64_tr_b16 v[236:237], v241 offset:512\n\t"
"v_mfma_f32_16x16x32_bf16 v[148:151], a[224:227], a[184:187], v[148:151]\n\t"
"buffer_load_dwordx4 v247, s[12:15], s28 offen offset:256 nt lds\n\t"
"v_mfma_f32_16x16x32_bf16 v[152:155], a[224:227], a[188:191], v[152:155]\n\t"
"ds_read_b64_tr_b16 v[238:239], v241 offset:2048\n\t"
"v_mfma_f32_16x16x32_bf16 v[152:155], a[228:231], a[192:195], v[152:155]\n\t"
"ds_read_b64_tr_b16 v[228:229], v240 offset:20480\n\t"
"v_mfma_f32_16x16x32_bf16 v[152:155], a[232:235], a[196:199], v[152:155]\n\t"
"ds_read_b64_tr_b16 v[230:231], v240 offset:22016\n\t"
"v_mfma_f32_16x16x32_bf16 v[156:159], a[232:235], a[200:203], v[156:159]\n\t"
"ds_read_b64_tr_b16 v[232:233], v241 offset:20480\n\t"
"v_mfma_f32_16x16x32_bf16 v[156:159], a[228:231], a[196:199], v[156:159]\n\t"
"ds_read_b64_tr_b16 v[234:235], v241 offset:22016\n\t"
"v_mfma_f32_16x16x32_bf16 v[156:159], a[224:227], a[192:195], v[156:159]\n\t"
"v_mfma_f32_16x16x32_bf16 v[160:163], a[224:227], a[196:199], v[160:163]\n\t"
"s_waitcnt vmcnt(8) lgkmcnt(6)\n\t"
"v_mfma_f32_16x16x32_bf16 v[160:163], a[228:231], a[200:203], v[160:163]\n\t"
"s_waitcnt lgkmcnt(4)\n\t"
"v_mfma_f32_16x16x32_bf16 v[160:163], a[232:235], a[204:207], v[160:163]\n\t"
"s_waitcnt lgkmcnt(2)\n\t"
"v_mfma_f32_16x16x32_bf16 v[224:227], v[224:227], a[208:211], 0\n\t"
"s_waitcnt lgkmcnt(0)\n\t"
"v_mfma_f32_16x16x32_bf16 v[236:239], v[236:239], a[208:211], 0\n\t"
"v_mfma_f32_16x16x32_bf16 v[228:231], v[228:231], a[208:211], 0\n\t"
"v_mfma_f32_16x16x32_bf16 v[232:235], v[232:235], a[208:211], 0\n\t"
"v_mfma_f32_16x16x32_bf16 v[164:167], a[236:239], a[168:171], v[164:167]\n\t"
"ds_read_b128 a[168:171], v222 offset:1536\n\t"
"v_mfma_f32_16x16x32_bf16 v[164:167], a[244:247], a[176:179], v[164:167]\n\t"
"v_cvt_pk_bf16_f32 v224, v224, v225\n\t"
"v_cvt_pk_bf16_f32 v225, v226, v227\n\t"
"v_mfma_f32_16x16x32_bf16 v[164:167], a[240:243], a[172:175], v[164:167]\n\t"
"v_cvt_pk_bf16_f32 v226, v236, v237\n\t"
"v_cvt_pk_bf16_f32 v227, v238, v239\n\t"
"v_mfma_f32_16x16x32_bf16 v[168:171], a[236:239], a[172:175], v[168:171]\n\t"
"ds_read_b128 a[172:175], v223 offset:7680\n\t"
"v_cvt_pk_bf16_f32 v228, v228, v229\n\t"
"v_mfma_f32_16x16x32_bf16 v[168:171], a[240:243], a[176:179], v[168:171]\n\t"
"v_cvt_pk_bf16_f32 v229, v230, v231\n\t"
"v_cvt_pk_bf16_f32 v230, v232, v233\n\t"
"v_mfma_f32_16x16x32_bf16 v[168:171], a[244:247], a[180:183], v[168:171]\n\t"
"ds_write_b128 v243, v[224:227] offset:3072\n\t"
"v_mfma_f32_16x16x32_bf16 v[172:175], a[244:247], a[184:187], v[172:175]\n\t"
"v_cvt_pk_bf16_f32 v231, v234, v235\n\t"
"v_add_u32 v240, 59904, v240\n\t"
"v_mfma_f32_16x16x32_bf16 v[172:175], a[240:243], a[180:183], v[172:175]\n\t"
"v_add_u32 v241, 59904, v241\n\t"
"v_mfma_f32_16x16x32_bf16 v[172:175], a[236:239], a[176:179], v[172:175]\n\t"
"ds_write_b128 v243, v[228:231] offset:23040\n\t"
"v_mfma_f32_16x16x32_bf16 v[176:179], a[236:239], a[180:183], v[176:179]\n\t"
"v_add_u32 v243, 59904, v243\n\t"
"v_mfma_f32_16x16x32_bf16 v[176:179], a[240:243], a[184:187], v[176:179]\n\t"
"ds_read_b128 a[176:179], v223 offset:13824\n\t"
"v_mfma_f32_16x16x32_bf16 v[176:179], a[244:247], a[188:191], v[176:179]\n\t"
"ds_read_b64_tr_b16 v[236:237], v240 offset:512\n\t"
"v_mfma_f32_16x16x32_bf16 v[180:183], a[244:247], a[192:195], v[180:183]\n\t"
"ds_read_b64_tr_b16 v[238:239], v240 offset:2048\n\t"
"v_mfma_f32_16x16x32_bf16 v[180:183], a[240:243], a[188:191], v[180:183]\n\t"
"buffer_load_dwordx4 a[224:227], off, s[80:83], s20 offset:1024 sc1\n\t"
"v_mfma_f32_16x16x32_bf16 v[180:183], a[236:239], a[184:187], v[180:183]\n\t"
"ds_read_b64_tr_b16 v[232:233], v241 offset:512\n\t"
"v_mfma_f32_16x16x32_bf16 v[184:187], a[236:239], a[188:191], v[184:187]\n\t"
"ds_read_b64_tr_b16 v[234:235], v241 offset:2048\n\t"
"v_mfma_f32_16x16x32_bf16 v[184:187], a[240:243], a[192:195], v[184:187]\n\t"
"ds_read_b128 a[180:183], v222 offset:21504\n\t"
"v_mfma_f32_16x16x32_bf16 v[184:187], a[244:247], a[196:199], v[184:187]\n\t"
"ds_read_b64_tr_b16 v[224:225], v240 offset:20480\n\t"
"v_mfma_f32_16x16x32_bf16 v[188:191], a[244:247], a[200:203], v[188:191]\n\t"
"ds_read_b64_tr_b16 v[226:227], v240 offset:22016\n\t"
"v_mfma_f32_16x16x32_bf16 v[188:191], a[240:243], a[196:199], v[188:191]\n\t"
"ds_read_b64_tr_b16 v[228:229], v241 offset:20480\n\t"
"v_mfma_f32_16x16x32_bf16 v[188:191], a[236:239], a[192:195], v[188:191]\n\t"
"ds_read_b64_tr_b16 v[230:231], v241 offset:22016\n\t"
"v_mfma_f32_16x16x32_bf16 v[192:195], a[236:239], a[196:199], v[192:195]\n\t"
"s_waitcnt lgkmcnt(7)\n\t"
"ds_read_b128 a[184:187], v223 offset:27648\n\t"
"v_mfma_f32_16x16x32_bf16 v[192:195], a[240:243], a[200:203], v[192:195]\n\t"
"s_waitcnt lgkmcnt(6)\n\t"
"ds_read_b128 a[188:191], v223 offset:33792\n\t"
"v_mfma_f32_16x16x32_bf16 v[192:195], a[244:247], a[204:207], v[192:195]\n\t"
"s_waitcnt lgkmcnt(4)\n\t"
"ds_read_b128 a[192:195], v222 offset:41472\n\t"
"v_mfma_f32_16x16x32_bf16 v[236:239], v[236:239], a[208:211], 0\n\t"
"ds_read_b128 a[196:199], v223 offset:47616\n\t"
"v_mfma_f32_16x16x32_bf16 v[232:235], v[232:235], a[208:211], 0\n\t"
"s_waitcnt lgkmcnt(4)\n\t"
"ds_read_b128 a[200:203], v223 offset:53760\n\t"
"v_mfma_f32_16x16x32_bf16 v[224:227], v[224:227], a[208:211], 0\n\t"
"ds_read_b128 a[204:207], v222 offset:61440\n\t"
"v_mfma_f32_16x16x32_bf16 v[228:231], v[228:231], a[208:211], 0\n\t"
"Lst9_%=:\n\t"
"v_mfma_f32_16x16x32_bf16 v[196:199], v[96:99], a[168:171], v[196:199]\n\t"
"s_mov_b64 vcc, -1\n\t"
"s_bitcmp1_b32 s73, 1\n\t"
"s_cbranch_scc1 Lvh2_%=\n\t"
"s_bitcmp1_b32 s73, 0\n\t"
"s_cbranch_scc1 Lpg18_%=\n\t"
"s_branch Lpg17_%=\n\t"
"Lvh2_%=:\n\t"
"s_bitcmp1_b32 s73, 0\n\t"
"s_cbranch_scc1 Lpg20_%=\n\t"
"s_branch Lpg19_%=\n\t"
"Lvr2_%=:\n\t"
"s_mov_b64 vcc, 0\n\t"
"v_mfma_f32_16x16x32_bf16 v[196:199], a[248:251], a[176:179], v[196:199]\n\t"
"v_cvt_pk_bf16_f32 v236, v236, v237\n\t"
"v_cvt_pk_bf16_f32 v237, v238, v239\n\t"
"v_mfma_f32_16x16x32_bf16 v[196:199], v[248:251], a[172:175], v[196:199]\n\t"
"v_cvt_pk_bf16_f32 v238, v232, v233\n\t"
"v_cvt_pk_bf16_f32 v239, v234, v235\n\t"
"v_mfma_f32_16x16x32_bf16 v[204:207], v[96:99], a[172:175], v[204:207]\n\t"
"s_waitcnt lgkmcnt(5)\n\t"
"v_cvt_pk_bf16_f32 v224, v224, v225\n\t"
"v_cvt_pk_bf16_f32 v225, v226, v227\n\t"
"v_mfma_f32_16x16x32_bf16 v[204:207], v[248:251], a[176:179], v[204:207]\n\t"
"v_cvt_pk_bf16_f32 v226, v228, v229\n\t"
"v_cvt_pk_bf16_f32 v227, v230, v231\n\t"
"v_mfma_f32_16x16x32_bf16 v[204:207], a[248:251], a[180:183], v[204:207]\n\t"
"ds_write_b128 v243, v[236:239] offset:3072\n\t"
"v_mfma_f32_16x16x32_bf16 v[212:215], a[248:251], a[184:187], v[212:215]\n\t"
"s_waitcnt lgkmcnt(5)\n\t"
"v_mfma_f32_16x16x32_bf16 v[212:215], v[248:251], a[180:183], v[212:215]\n\t"
"v_mfma_f32_16x16x32_bf16 v[212:215], v[96:99], a[176:179], v[212:215]\n\t"
"ds_write_b128 v243, v[224:227] offset:23040\n\t"
"v_mfma_f32_16x16x32_bf16 a[0:3], v[96:99], a[180:183], a[0:3]\n\t"
"s_waitcnt lgkmcnt(5)\n\t"
"v_mfma_f32_16x16x32_bf16 a[0:3], v[248:251], a[184:187], a[0:3]\n\t"
"v_mfma_f32_16x16x32_bf16 a[0:3], a[248:251], a[188:191], a[0:3]\n\t"
"s_barrier\n\t"
"v_mfma_f32_16x16x32_bf16 a[8:11], a[248:251], a[192:195], a[8:11]\n\t"
"s_waitcnt lgkmcnt(4)\n\t"
"ds_read_b64_tr_b16 v[232:233], v246 offset:512\n\t"
"v_mfma_f32_16x16x32_bf16 a[8:11], v[248:251], a[188:191], a[8:11]\n\t"
"ds_read_b64_tr_b16 v[228:229], v255 offset:512\n\t"
"v_mfma_f32_16x16x32_bf16 a[8:11], v[96:99], a[184:187], a[8:11]\n\t"
"ds_read_b64_tr_b16 v[236:237], v246 offset:6656\n\t"
"v_mfma_f32_16x16x32_bf16 a[16:19], v[96:99], a[188:191], a[16:19]\n\t"
"s_waitcnt lgkmcnt(6)\n\t"
"ds_read_b64_tr_b16 v[224:225], v255 offset:6656\n\t"
"v_mfma_f32_16x16x32_bf16 a[16:19], v[248:251], a[192:195], a[16:19]\n\t"
"buffer_load_dwordx4 a[228:231], off, s[80:83], s20 offset:2048 sc1\n\t"
"v_mfma_f32_16x16x32_bf16 a[16:19], a[248:251], a[196:199], a[16:19]\n\t"
"s_mov_b32 s20, 1032192\n\t"
"v_mfma_f32_16x16x32_bf16 a[24:27], a[248:251], a[200:203], a[24:27]\n\t"
"s_waitcnt lgkmcnt(6)\n\t"
"v_mfma_f32_16x16x32_bf16 a[24:27], v[248:251], a[196:199], a[24:27]\n\t"
"v_mfma_f32_16x16x32_bf16 a[24:27], v[96:99], a[192:195], a[24:27]\n\t"
"buffer_load_dwordx4 a[232:235], off, s[80:83], s20 offset:0 sc1\n\t"
"v_mfma_f32_16x16x32_bf16 a[32:35], v[96:99], a[196:199], a[32:35]\n\t"
"s_waitcnt lgkmcnt(3)\n\t"
"v_mfma_f32_16x16x32_bf16 a[32:35], v[248:251], a[200:203], a[32:35]\n\t"
"s_waitcnt lgkmcnt(2)\n\t"
"v_mfma_f32_16x16x32_bf16 a[32:35], a[248:251], a[204:207], a[32:35]\n\t"
"s_waitcnt lgkmcnt(1)\n\t"
"v_mfma_f32_4x4x4_16b_bf16 v[232:235], v[232:233], v[220:221], 0\n\t"
"s_waitcnt lgkmcnt(0)\n\t"
"v_mfma_f32_4x4x4_16b_bf16 v[228:231], v[228:229], v[220:221], 0\n\t"
"s_waitcnt vmcnt(10)\n\t"
"v_mfma_f32_4x4x4_16b_bf16 v[236:239], v[236:237], v[220:221], 0\n\t"
"v_mfma_f32_4x4x4_16b_bf16 v[224:227], v[224:225], v[220:221], 0\n\t"
"buffer_load_dwordx4 v[96:99], off, s[80:83], s20 offset:1024 sc1\n\t"
"v_mfma_f32_16x16x32_bf16 a[40:43], a[252:255], a[168:171], a[40:43]\n\t"
"buffer_load_dwordx4 v[248:251], off, s[80:83], s20 offset:2048 sc1\n\t"
"v_mfma_f32_16x16x32_bf16 a[40:43], a[216:219], a[176:179], a[40:43]\n\t"
"s_mov_b32 s20, 1044480\n\t"
"v_cvt_pk_bf16_f32 v232, v232, v233\n\t"
"v_cvt_pk_bf16_f32 v233, v234, v235\n\t"
"v_mfma_f32_16x16x32_bf16 a[40:43], a[212:215], a[172:175], a[40:43]\n\t"
"v_cvt_pk_bf16_f32 v234, v228, v229\n\t"
"v_cvt_pk_bf16_f32 v235, v230, v231\n\t"
"v_mfma_f32_16x16x32_bf16 a[48:51], a[252:255], a[172:175], a[48:51]\n\t"
"ds_read_b64_tr_b16 v[228:229], v246 offset:12800\n\t"
"v_cvt_pk_bf16_f32 v236, v236, v237\n\t"
"v_mfma_f32_16x16x32_bf16 a[48:51], a[212:215], a[176:179], a[48:51]\n\t"
"v_cvt_pk_bf16_f32 v237, v238, v239\n\t"
"v_cvt_pk_bf16_f32 v238, v224, v225\n\t"
"v_mfma_f32_16x16x32_bf16 a[48:51], a[216:219], a[180:183], a[48:51]\n\t"
"ds_write_b128 v245, v[232:235] offset:512\n\t"
"v_cvt_pk_bf16_f32 v239, v226, v227\n\t"
"v_mfma_f32_16x16x32_bf16 a[56:59], a[216:219], a[184:187], a[56:59]\n\t"
"v_mfma_f32_16x16x32_bf16 a[56:59], a[212:215], a[180:183], a[56:59]\n\t"
"buffer_load_dwordx4 a[236:239], off, s[80:83], s20 offset:0 sc1\n\t"
"v_mfma_f32_16x16x32_bf16 a[56:59], a[252:255], a[176:179], a[56:59]\n\t"
"ds_read_b64_tr_b16 v[224:225], v255 offset:12800\n\t"
"v_mfma_f32_16x16x32_bf16 a[64:67], a[252:255], a[180:183], a[64:67]\n\t"
"ds_read_b64_tr_b16 v[232:233], v246 offset:20480\n\t"
"v_mfma_f32_16x16x32_bf16 a[64:67], a[212:215], a[184:187], a[64:67]\n\t"
"buffer_load_dwordx4 a[240:243], off, s[80:83], s20 offset:1024 sc1\n\t"
"v_mfma_f32_16x16x32_bf16 a[64:67], a[216:219], a[188:191], a[64:67]\n\t"
"ds_write_b128 v245, v[236:239] offset:6656\n\t"
"v_mfma_f32_16x16x32_bf16 a[72:75], a[252:255], a[184:187], a[72:75]\n\t"
"buffer_load_dwordx4 a[244:247], off, s[80:83], s20 offset:2048 sc1\n\t"
"v_mfma_f32_16x16x32_bf16 a[72:75], a[216:219], a[192:195], a[72:75]\n\t"
"s_waitcnt lgkmcnt(2)\n\t"
"ds_read_b64_tr_b16 v[236:237], v255 offset:20480\n\t"
"v_mfma_f32_16x16x32_bf16 a[72:75], a[212:215], a[188:191], a[72:75]\n\t"
"s_waitcnt lgkmcnt(2)\n\t"
"s_mov_b32 s20, 1056768\n\t"
"v_mfma_f32_4x4x4_16b_bf16 v[228:231], v[228:229], v[220:221], 0\n\t"
"v_mfma_f32_4x4x4_16b_bf16 v[224:227], v[224:225], v[220:221], 0\n\t"
"v_mfma_f32_4x4x4_16b_bf16 v[232:235], v[232:233], v[220:221], 0\n\t"
"s_waitcnt lgkmcnt(0)\n\t"
"v_mfma_f32_4x4x4_16b_bf16 v[236:239], v[236:237], v[220:221], 0\n\t"
"v_mfma_f32_16x16x32_bf16 a[80:83], a[252:255], a[188:191], a[80:83]\n\t"
"buffer_load_dwordx4 a[248:251], off, s[80:83], s20 offset:0 sc1\n\t"
"v_mfma_f32_16x16x32_bf16 a[80:83], a[216:219], a[196:199], a[80:83]\n\t"
"v_cvt_pk_bf16_f32 v228, v228, v229\n\t"
"v_cvt_pk_bf16_f32 v229, v230, v231\n\t"
"v_mfma_f32_16x16x32_bf16 a[80:83], a[212:215], a[192:195], a[80:83]\n\t"
"v_cvt_pk_bf16_f32 v230, v224, v225\n\t"
"v_cvt_pk_bf16_f32 v231, v226, v227\n\t"
"v_mfma_f32_16x16x32_bf16 a[88:91], a[252:255], a[192:195], a[88:91]\n\t"
"ds_read_b64_tr_b16 v[224:225], v246 offset:26624\n\t"
"v_cvt_pk_bf16_f32 v232, v232, v233\n\t"
"v_mfma_f32_16x16x32_bf16 a[88:91], a[212:215], a[196:199], a[88:91]\n\t"
"v_cvt_pk_bf16_f32 v233, v234, v235\n\t"
"v_cvt_pk_bf16_f32 v234, v236, v237\n\t"
"v_mfma_f32_16x16x32_bf16 a[88:91], a[216:219], a[200:203], a[88:91]\n\t"
"ds_write_b128 v245, v[228:231] offset:12800\n\t"
"v_cvt_pk_bf16_f32 v235, v238, v239\n\t"
"v_mfma_f32_16x16x32_bf16 a[96:99], a[212:215], a[200:203], a[96:99]\n\t"
"s_waitcnt vmcnt(7)\n\t"
"v_mfma_f32_16x16x32_bf16 a[96:99], a[252:255], a[196:199], a[96:99]\n\t"
"ds_read_b64_tr_b16 v[236:237], v255 offset:26624\n\t"
"v_mfma_f32_16x16x32_bf16 a[96:99], a[216:219], a[204:207], a[96:99]\n\t"
"ds_write_b128 v245, v[232:235] offset:20480\n\t"
"v_mfma_f32_16x16x32_bf16 a[104:107], a[220:223], a[168:171], a[104:107]\n\t"
"buffer_load_dwordx4 a[212:215], off, s[80:83], s20 offset:1024 sc1\n\t"
"v_mfma_f32_16x16x32_bf16 a[104:107], a[228:231], a[176:179], a[104:107]\n\t"
"ds_read_b64_tr_b16 v[228:229], v246 offset:32768\n\t"
"v_mfma_f32_16x16x32_bf16 a[104:107], a[224:227], a[172:175], a[104:107]\n\t"
"s_waitcnt lgkmcnt(4)\n\t"
"ds_read_b64_tr_b16 v[232:233], v255 offset:32768\n\t"
"v_mfma_f32_16x16x32_bf16 a[112:115], a[220:223], a[172:175], a[112:115]\n\t"
"buffer_load_dwordx4 a[216:219], off, s[80:83], s20 offset:2048 sc1\n\t"
"v_mfma_f32_16x16x32_bf16 a[112:115], a[224:227], a[176:179], a[112:115]\n\t"
"Lpg7_%=:\n\t"
"s_cbranch_vccnz Lpg11_%=\n\t"
"s_waitcnt lgkmcnt(3)\n\t"
"ds_read_b128 a[168:171], v222 offset:1792\n\t"
"v_mfma_f32_16x16x32_bf16 a[112:115], a[228:231], a[180:183], a[112:115]\n\t"
"s_mov_b32 s20, 1216512\n\t"
"ds_read_b128 a[172:175], v223 offset:7936\n\t"
"v_mfma_f32_4x4x4_16b_bf16 v[224:227], v[224:225], v[220:221], 0\n\t"
"buffer_load_dwordx4 a[252:255], off, s[80:83], s20 offset:0 sc1\n\t"
"v_mfma_f32_4x4x4_16b_bf16 v[236:239], v[236:237], v[220:221], 0\n\t"
"s_waitcnt lgkmcnt(2)\n\t"
"v_mfma_f32_4x4x4_16b_bf16 v[228:231], v[228:229], v[220:221], 0\n\t"
"v_mfma_f32_4x4x4_16b_bf16 v[232:235], v[232:233], v[220:221], 0\n\t"
"v_mfma_f32_16x16x32_bf16 a[120:123], a[224:227], a[180:183], a[120:123]\n\t"
"v_cvt_pk_bf16_f32 v224, v224, v225\n\t"
"v_cvt_pk_bf16_f32 v225, v226, v227\n\t"
"v_mfma_f32_16x16x32_bf16 a[120:123], a[220:223], a[176:179], a[120:123]\n\t"
"ds_read_b128 a[176:179], v223 offset:14080\n\t"
"v_cvt_pk_bf16_f32 v226, v236, v237\n\t"
"v_mfma_f32_16x16x32_bf16 a[120:123], a[228:231], a[184:187], a[120:123]\n\t"
"v_cvt_pk_bf16_f32 v227, v238, v239\n\t"
"v_cvt_pk_bf16_f32 v228, v228, v229\n\t"
"v_mfma_f32_16x16x32_bf16 a[128:131], a[220:223], a[180:183], a[128:131]\n\t"
"ds_read_b128 a[180:183], v222 offset:21760\n\t"
"v_cvt_pk_bf16_f32 v229, v230, v231\n\t"
"v_mfma_f32_16x16x32_bf16 a[128:131], a[228:231], a[188:191], a[128:131]\n\t"
"v_cvt_pk_bf16_f32 v230, v232, v233\n\t"
"v_cvt_pk_bf16_f32 v231, v234, v235\n\t"
"v_mfma_f32_16x16x32_bf16 a[128:131], a[224:227], a[184:187], a[128:131]\n\t"
"ds_write_b128 v245, v[224:227] offset:26624\n\t"
"v_mfma_f32_16x16x32_bf16 a[136:139], a[220:223], a[184:187], a[136:139]\n\t"
"v_mfma_f32_16x16x32_bf16 a[136:139], a[224:227], a[188:191], a[136:139]\n\t"
"ds_read_b128 a[184:187], v223 offset:27904\n\t"
"v_mfma_f32_16x16x32_bf16 a[136:139], a[228:231], a[192:195], a[136:139]\n\t"
"ds_write_b128 v245, v[228:231] offset:32768\n\t"
"v_mfma_f32_16x16x32_bf16 a[144:147], a[228:231], a[196:199], a[144:147]\n\t"
"v_mfma_f32_16x16x32_bf16 a[144:147], a[224:227], a[192:195], a[144:147]\n\t"
"v_mfma_f32_16x16x32_bf16 a[144:147], a[220:223], a[188:191], a[144:147]\n\t"
"ds_read_b128 a[188:191], v223 offset:34048\n\t"
"v_mfma_f32_16x16x32_bf16 a[152:155], a[220:223], a[192:195], a[152:155]\n\t"
"ds_read_b128 a[192:195], v222 offset:41728\n\t"
"v_mfma_f32_16x16x32_bf16 a[152:155], a[224:227], a[196:199], a[152:155]\n\t"
"v_mfma_f32_16x16x32_bf16 a[152:155], a[228:231], a[200:203], a[152:155]\n\t"
"s_barrier\n\t"
"v_mfma_f32_16x16x32_bf16 a[160:163], a[224:227], a[200:203], a[160:163]\n\t"
"s_waitcnt vmcnt(7) lgkmcnt(6)\n\t"
"ds_read_b64_tr_b16 v[236:237], v240 offset:40448\n\t"
"v_mfma_f32_16x16x32_bf16 a[160:163], a[220:223], a[196:199], a[160:163]\n\t"
"ds_read_b64_tr_b16 v[238:239], v240 offset:41984\n\t"
"v_mfma_f32_16x16x32_bf16 a[160:163], a[228:231], a[204:207], a[160:163]\n\t"
"ds_read_b64_tr_b16 v[232:233], v241 offset:40448\n\t"
"v_mfma_f32_16x16x32_bf16 v[200:203], a[232:235], a[168:171], v[200:203]\n\t"
"s_waitcnt lgkmcnt(8)\n\t"
"ds_read_b64_tr_b16 v[234:235], v241 offset:41984\n\t"
"v_subrev_u32 v240, 59904, v240\n\t"
"v_mfma_f32_16x16x32_bf16 v[200:203], v[248:251], a[176:179], v[200:203]\n\t"
"ds_read_b128 a[196:199], v223 offset:47872\n\t"
"v_subrev_u32 v241, 59904, v241\n\t"
"v_mfma_f32_16x16x32_bf16 v[200:203], v[96:99], a[172:175], v[200:203]\n\t"
"ds_read_b64_tr_b16 v[224:225], v240 offset:40448\n\t"
"v_mfma_f32_16x16x32_bf16 v[208:211], a[232:235], a[172:175], v[208:211]\n\t"
"s_waitcnt lgkmcnt(4)\n\t"
"ds_read_b64_tr_b16 v[226:227], v240 offset:41984\n\t"
"v_mfma_f32_16x16x32_bf16 v[208:211], v[96:99], a[176:179], v[208:211]\n\t"
"buffer_load_dwordx4 a[224:227], off, s[80:83], s20 offset:1024 sc1\n\t"
"v_mfma_f32_16x16x32_bf16 v[208:211], v[248:251], a[180:183], v[208:211]\n\t"
"s_waitcnt lgkmcnt(3)\n\t"
"ds_read_b64_tr_b16 v[228:229], v241 offset:40448\n\t"
"ds_read_b64_tr_b16 v[230:231], v241 offset:41984\n\t"
"v_mfma_f32_16x16x32_bf16 v[236:239], v[236:239], a[208:211], 0\n\t"
"buffer_load_dwordx4 a[220:223], off, s[80:83], s20 offset:2048 sc1\n\t"
"s_mov_b32 s20, 1228800\n\t"
"v_mfma_f32_16x16x32_bf16 v[232:235], v[232:235], a[208:211], 0\n\t"
"s_waitcnt lgkmcnt(2)\n\t"
"buffer_load_dwordx4 a[228:231], off, s[80:83], s20 offset:0 sc1\n\t"
"v_mfma_f32_16x16x32_bf16 v[224:227], v[224:227], a[208:211], 0\n\t"
"s_waitcnt lgkmcnt(0)\n\t"
"ds_read_b128 a[200:203], v223 offset:54016\n\t"
"v_mfma_f32_16x16x32_bf16 v[228:231], v[228:231], a[208:211], 0\n\t"
"ds_read_b128 a[204:207], v222 offset:61696\n\t"
"v_mfma_f32_16x16x32_bf16 v[216:219], v[96:99], a[180:183], v[216:219]\n\t"
"v_mfma_f32_16x16x32_bf16 v[216:219], a[232:235], a[176:179], v[216:219]\n\t"
"v_cvt_pk_bf16_f32 v236, v236, v237\n\t"
"v_cvt_pk_bf16_f32 v237, v238, v239\n\t"
"v_mfma_f32_16x16x32_bf16 v[216:219], v[248:251], a[184:187], v[216:219]\n\t"
"v_cvt_pk_bf16_f32 v238, v232, v233\n\t"
"v_cvt_pk_bf16_f32 v239, v234, v235\n\t"
"v_mfma_f32_16x16x32_bf16 a[4:7], a[232:235], a[180:183], a[4:7]\n\t"
"ds_read_b64_tr_b16 v[232:233], v246 offset:40448\n\t"
"v_cvt_pk_bf16_f32 v224, v224, v225\n\t"
"v_mfma_f32_16x16x32_bf16 a[4:7], v[248:251], a[188:191], a[4:7]\n\t"
"v_cvt_pk_bf16_f32 v225, v226, v227\n\t"
"v_cvt_pk_bf16_f32 v226, v228, v229\n\t"
"v_mfma_f32_16x16x32_bf16 a[4:7], v[96:99], a[184:187], a[4:7]\n\t"
"ds_write_b128 v243, v[236:239] offset:43008\n\t"
"v_mfma_f32_16x16x32_bf16 a[12:15], a[232:235], a[184:187], a[12:15]\n\t"
"v_cvt_pk_bf16_f32 v227, v230, v231\n\t"
"v_subrev_u32 v243, 59904, v243\n\t"
"v_mfma_f32_16x16x32_bf16 a[12:15], v[96:99], a[188:191], a[12:15]\n\t"
"ds_read_b64_tr_b16 v[228:229], v255 offset:40448\n\t"
"v_mfma_f32_16x16x32_bf16 a[12:15], v[248:251], a[192:195], a[12:15]\n\t"
"ds_write_b128 v243, v[224:227] offset:43008\n\t"
"v_mfma_f32_16x16x32_bf16 a[20:23], v[248:251], a[196:199], a[20:23]\n\t"
"s_waitcnt lgkmcnt(5)\n\t"
"v_mfma_f32_16x16x32_bf16 a[20:23], v[96:99], a[192:195], a[20:23]\n\t"
"ds_read_b64_tr_b16 v[236:237], v246 offset:46592\n\t"
"v_mfma_f32_16x16x32_bf16 a[20:23], a[232:235], a[188:191], a[20:23]\n\t"
"ds_read_b64_tr_b16 v[224:225], v255 offset:46592\n\t"
"v_mfma_f32_16x16x32_bf16 a[28:31], a[232:235], a[192:195], a[28:31]\n\t"
"s_waitcnt lgkmcnt(6)\n\t"
"v_mfma_f32_16x16x32_bf16 a[28:31], v[96:99], a[196:199], a[28:31]\n\t"
"v_mfma_f32_16x16x32_bf16 a[28:31], v[248:251], a[200:203], a[28:31]\n\t"
"s_barrier\n\t"
"v_mfma_f32_16x16x32_bf16 a[36:39], v[96:99], a[200:203], a[36:39]\n\t"
"s_waitcnt lgkmcnt(5)\n\t"
"v_mfma_f32_16x16x32_bf16 a[36:39], a[232:235], a[196:199], a[36:39]\n\t"
"buffer_load_dwordx4 v[96:99], off, s[80:83], s20 offset:1024 sc1\n\t"
"v_mfma_f32_16x16x32_bf16 a[36:39], v[248:251], a[204:207], a[36:39]\n\t"
"s_waitcnt lgkmcnt(1)\n\t"
"v_mfma_f32_4x4x4_16b_bf16 v[232:235], v[232:233], v[220:221], 0\n\t"
"s_waitcnt lgkmcnt(0)\n\t"
"v_mfma_f32_4x4x4_16b_bf16 v[228:231], v[228:229], v[220:221], 0\n\t"
"s_waitcnt vmcnt(8)\n\t"
"v_mfma_f32_4x4x4_16b_bf16 v[236:239], v[236:237], v[220:221], 0\n\t"
"v_mfma_f32_4x4x4_16b_bf16 v[224:227], v[224:225], v[220:221], 0\n\t"
"buffer_load_dwordx4 v[248:251], off, s[80:83], s20 offset:2048 sc1\n\t"
"s_mov_b32 s20, 1241088\n\t"
"v_mfma_f32_16x16x32_bf16 a[44:47], a[236:239], a[168:171], a[44:47]\n\t"
"buffer_load_dwordx4 a[232:235], off, s[80:83], s20 offset:0 sc1\n\t"
"v_mfma_f32_16x16x32_bf16 a[44:47], a[244:247], a[176:179], a[44:47]\n\t"
"v_cvt_pk_bf16_f32 v232, v232, v233\n\t"
"v_cvt_pk_bf16_f32 v233, v234, v235\n\t"
"v_mfma_f32_16x16x32_bf16 a[44:47], a[240:243], a[172:175], a[44:47]\n\t"
"v_cvt_pk_bf16_f32 v234, v228, v229\n\t"
"v_cvt_pk_bf16_f32 v235, v230, v231\n\t"
"v_mfma_f32_16x16x32_bf16 a[52:55], a[236:239], a[172:175], a[52:55]\n\t"
"ds_read_b64_tr_b16 v[228:229], v246 offset:52736\n\t"
"v_cvt_pk_bf16_f32 v236, v236, v237\n\t"
"v_mfma_f32_16x16x32_bf16 a[52:55], a[240:243], a[176:179], a[52:55]\n\t"
"v_cvt_pk_bf16_f32 v237, v238, v239\n\t"
"v_cvt_pk_bf16_f32 v238, v224, v225\n\t"
"v_mfma_f32_16x16x32_bf16 a[52:55], a[244:247], a[180:183], a[52:55]\n\t"
"ds_write_b128 v245, v[232:235] offset:40448\n\t"
"v_cvt_pk_bf16_f32 v239, v226, v227\n\t"
"v_mfma_f32_16x16x32_bf16 a[60:63], a[244:247], a[184:187], a[60:63]\n\t"
"v_mfma_f32_16x16x32_bf16 a[60:63], a[240:243], a[180:183], a[60:63]\n\t"
"ds_read_b64_tr_b16 v[224:225], v255 offset:52736\n\t"
"v_mfma_f32_16x16x32_bf16 a[60:63], a[236:239], a[176:179], a[60:63]\n\t"
"ds_write_b128 v245, v[236:239] offset:46592\n\t"
"v_mfma_f32_16x16x32_bf16 a[68:71], a[236:239], a[180:183], a[68:71]\n\t"
"v_mfma_f32_16x16x32_bf16 a[68:71], a[240:243], a[184:187], a[68:71]\n\t"
"v_mfma_f32_16x16x32_bf16 a[68:71], a[244:247], a[188:191], a[68:71]\n\t"
"v_mfma_f32_16x16x32_bf16 a[76:79], a[236:239], a[184:187], a[76:79]\n\t"
"s_waitcnt lgkmcnt(3)\n\t"
"v_mfma_f32_16x16x32_bf16 a[76:79], a[244:247], a[192:195], a[76:79]\n\t"
"s_waitcnt lgkmcnt(1)\n\t"
"ds_read_b64_tr_b16 v[232:233], v240 offset:1024\n\t"
"v_mfma_f32_16x16x32_bf16 a[76:79], a[240:243], a[188:191], a[76:79]\n\t"
"ds_read_b64_tr_b16 v[234:235], v240 offset:2560\n\t"
"v_mfma_f32_4x4x4_16b_bf16 v[228:231], v[228:229], v[220:221], 0\n\t"
"v_mfma_f32_4x4x4_16b_bf16 v[224:227], v[224:225], v[220:221], 0\n\t"
"v_mfma_f32_16x16x32_bf16 a[84:87], a[236:239], a[188:191], a[84:87]\n\t"
"ds_read_b64_tr_b16 v[236:237], v241 offset:1024\n\t"
"v_mfma_f32_16x16x32_bf16 a[84:87], a[244:247], a[196:199], a[84:87]\n\t"
"ds_read_b64_tr_b16 v[238:239], v241 offset:2560\n\t"
"v_cvt_pk_bf16_f32 v228, v228, v229\n\t"
"v_mfma_f32_16x16x32_bf16 a[84:87], a[240:243], a[192:195], a[84:87]\n\t"
"v_cvt_pk_bf16_f32 v229, v230, v231\n\t"
"v_cvt_pk_bf16_f32 v230, v224, v225\n\t"
"v_mfma_f32_16x16x32_bf16 a[92:95], a[236:239], a[192:195], a[92:95]\n\t"
"v_cvt_pk_bf16_f32 v231, v226, v227\n\t"
"v_mfma_f32_16x16x32_bf16 a[92:95], a[240:243], a[196:199], a[92:95]\n\t"
"ds_read_b64_tr_b16 v[224:225], v240 offset:20992\n\t"
"v_mfma_f32_16x16x32_bf16 a[92:95], a[244:247], a[200:203], a[92:95]\n\t"
"ds_read_b64_tr_b16 v[226:227], v240 offset:22528\n\t"
"v_mfma_f32_16x16x32_bf16 a[100:103], a[240:243], a[200:203], a[100:103]\n\t"
"buffer_load_dwordx4 a[240:243], off, s[80:83], s20 offset:1024 sc1\n\t"
"v_mfma_f32_16x16x32_bf16 a[100:103], a[236:239], a[196:199], a[100:103]\n\t"
"s_waitcnt vmcnt(8)\n\t"
"v_mfma_f32_16x16x32_bf16 a[100:103], a[244:247], a[204:207], a[100:103]\n\t"
"ds_write_b128 v245, v[228:231] offset:52736\n\t"
"v_mfma_f32_16x16x32_bf16 a[108:111], a[248:251], a[168:171], a[108:111]\n\t"
"buffer_load_dwordx4 a[236:239], off, s[80:83], s20 offset:2048 sc1\n\t"
"v_mfma_f32_16x16x32_bf16 a[108:111], a[216:219], a[176:179], a[108:111]\n\t"
"s_waitcnt lgkmcnt(3)\n\t"
"ds_read_b64_tr_b16 v[228:229], v241 offset:20992\n\t"
"v_mfma_f32_16x16x32_bf16 a[108:111], a[212:215], a[172:175], a[108:111]\n\t"
"s_waitcnt lgkmcnt(2)\n\t"
"ds_read_b64_tr_b16 v[230:231], v241 offset:22528\n\t"
"s_mov_b32 s20, 1253376\n\t"
"v_mfma_f32_16x16x32_bf16 v[232:235], v[232:235], a[208:211], 0\n\t"
"buffer_load_dwordx4 a[244:247], off, s[80:83], s20 offset:0 sc1\n\t"
"v_mfma_f32_16x16x32_bf16 v[236:239], v[236:239], a[208:211], 0\n\t"
"v_mfma_f32_16x16x32_bf16 v[224:227], v[224:227], a[208:211], 0\n\t"
"s_waitcnt lgkmcnt(0)\n\t"
"v_mfma_f32_16x16x32_bf16 v[228:231], v[228:231], a[208:211], 0\n\t"
"s_barrier\n\t"
"v_mfma_f32_16x16x32_bf16 a[116:119], a[248:251], a[172:175], a[116:119]\n\t"
"ds_read_b128 a[168:171], v222 offset:2048\n\t"
"v_mfma_f32_16x16x32_bf16 a[116:119], a[216:219], a[180:183], a[116:119]\n\t"
"ds_read_b128 a[172:175], v223 offset:8192\n\t"
"v_cvt_pk_bf16_f32 v232, v232, v233\n\t"
"v_mfma_f32_16x16x32_bf16 a[116:119], a[212:215], a[176:179], a[116:119]\n\t"
"v_cvt_pk_bf16_f32 v233, v234, v235\n\t"
"v_cvt_pk_bf16_f32 v234, v236, v237\n\t"
"v_mfma_f32_16x16x32_bf16 a[124:127], a[248:251], a[176:179], a[124:127]\n\t"
"ds_read_b128 a[176:179], v223 offset:14336\n\t"
"v_cvt_pk_bf16_f32 v235, v238, v239\n\t"
"v_mfma_f32_16x16x32_bf16 a[124:127], a[212:215], a[180:183], a[124:127]\n\t"
"v_cvt_pk_bf16_f32 v224, v224, v225\n\t"
"v_cvt_pk_bf16_f32 v225, v226, v227\n\t"
"v_mfma_f32_16x16x32_bf16 a[124:127], a[216:219], a[184:187], a[124:127]\n\t"
"ds_write_b128 v243, v[232:235] offset:1536\n\t"
"v_mfma_f32_16x16x32_bf16 a[132:135], a[216:219], a[188:191], a[132:135]\n\t"
"v_cvt_pk_bf16_f32 v226, v228, v229\n\t"
"v_cvt_pk_bf16_f32 v227, v230, v231\n\t"
"v_mfma_f32_16x16x32_bf16 a[132:135], a[212:215], a[184:187], a[132:135]\n\t"
"ds_read_b64_tr_b16 v[236:237], v240 offset:40960\n\t"
"v_mfma_f32_16x16x32_bf16 a[132:135], a[248:251], a[180:183], a[132:135]\n\t"
"ds_write_b128 v243, v[224:227] offset:21504\n\t"
"v_mfma_f32_16x16x32_bf16 a[140:143], a[248:251], a[184:187], a[140:143]\n\t"
"v_mfma_f32_16x16x32_bf16 a[140:143], a[212:215], a[188:191], a[140:143]\n\t"
"ds_read_b64_tr_b16 v[238:239], v240 offset:42496\n\t"
"v_mfma_f32_16x16x32_bf16 a[140:143], a[216:219], a[192:195], a[140:143]\n\t"
"ds_read_b64_tr_b16 v[228:229], v241 offset:40960\n\t"
"v_mfma_f32_16x16x32_bf16 a[148:151], a[216:219], a[196:199], a[148:151]\n\t"
"ds_read_b64_tr_b16 v[230:231], v241 offset:42496\n\t"
"v_add_u32 v240, 59904, v240\n\t"
"v_mfma_f32_16x16x32_bf16 a[148:151], a[212:215], a[192:195], a[148:151]\n\t"
"ds_read_b128 a[180:183], v222 offset:22016\n\t"
"v_add_u32 v241, 59904, v241\n\t"
"v_mfma_f32_16x16x32_bf16 a[148:151], a[248:251], a[188:191], a[148:151]\n\t"
"ds_read_b64_tr_b16 v[232:233], v240 offset:1024\n\t"
"v_mfma_f32_16x16x32_bf16 a[156:159], a[248:251], a[192:195], a[156:159]\n\t"
"ds_read_b64_tr_b16 v[234:235], v240 offset:2560\n\t"
"v_mfma_f32_16x16x32_bf16 a[156:159], a[212:215], a[196:199], a[156:159]\n\t"
"ds_read_b64_tr_b16 v[224:225], v241 offset:1024\n\t"
"v_mfma_f32_16x16x32_bf16 a[156:159], a[216:219], a[200:203], a[156:159]\n\t"
"ds_read_b64_tr_b16 v[226:227], v241 offset:2560\n\t"
"v_mfma_f32_16x16x32_bf16 a[164:167], a[212:215], a[200:203], a[164:167]\n\t"
"s_waitcnt lgkmcnt(7)\n\t"
"ds_read_b128 a[184:187], v223 offset:28160\n\t"
"v_mfma_f32_16x16x32_bf16 a[164:167], a[248:251], a[196:199], a[164:167]\n\t"
"s_waitcnt lgkmcnt(6)\n\t"
"ds_read_b128 a[188:191], v223 offset:34304\n\t"
"v_mfma_f32_16x16x32_bf16 a[164:167], a[216:219], a[204:207], a[164:167]\n\t"
"buffer_load_dwordx4 a[212:215], off, s[80:83], s20 offset:1024 sc1\n\t"
"v_mfma_f32_16x16x32_bf16 v[236:239], v[236:239], a[208:211], 0\n\t"
"s_waitcnt lgkmcnt(4)\n\t"
"ds_read_b128 a[192:195], v222 offset:41984\n\t"
"v_mfma_f32_16x16x32_bf16 v[228:231], v[228:231], a[208:211], 0\n\t"
"buffer_load_dwordx4 a[216:219], off, s[80:83], s20 offset:2048 sc1\n\t"
"v_mfma_f32_16x16x32_bf16 v[232:235], v[232:235], a[208:211], 0\n\t"
"s_waitcnt vmcnt(9) lgkmcnt(3)\n\t"
"ds_read_b128 a[196:199], v223 offset:48128\n\t"
"s_mov_b32 s20, 1265664\n\t"
"v_mfma_f32_16x16x32_bf16 v[224:227], v[224:227], a[208:211], 0\n\t"
"buffer_load_dwordx4 a[248:251], off, s[80:83], s20 offset:0 sc1\n\t"
"Lst10_%=:\n\t"
"v_mfma_f32_16x16x32_bf16 v[0:3], a[252:255], a[168:171], v[0:3]\n\t"
"ds_read_b128 a[200:203], v223 offset:54272\n\t"
"v_mfma_f32_16x16x32_bf16 v[0:3], a[220:223], a[176:179], v[0:3]\n\t"
"ds_read_b128 a[204:207], v222 offset:61952\n\t"
"v_cvt_pk_bf16_f32 v236, v236, v237\n\t"
"v_mfma_f32_16x16x32_bf16 v[0:3], a[224:227], a[172:175], v[0:3]\n\t"
"v_cvt_pk_bf16_f32 v237, v238, v239\n\t"
"v_cvt_pk_bf16_f32 v238, v228, v229\n\t"
"v_mfma_f32_16x16x32_bf16 v[4:7], a[252:255], a[172:175], v[4:7]\n\t"
"s_waitcnt lgkmcnt(5)\n\t"
"v_cvt_pk_bf16_f32 v239, v230, v231\n\t"
"v_cvt_pk_bf16_f32 v232, v232, v233\n\t"
"v_mfma_f32_16x16x32_bf16 v[4:7], a[224:227], a[176:179], v[4:7]\n\t"
"v_cvt_pk_bf16_f32 v233, v234, v235\n\t"
"v_cvt_pk_bf16_f32 v234, v224, v225\n\t"
"v_mfma_f32_16x16x32_bf16 v[4:7], a[220:223], a[180:183], v[4:7]\n\t"
"ds_write_b128 v243, v[236:239] offset:41472\n\t"
"v_mfma_f32_16x16x32_bf16 v[8:11], a[220:223], a[184:187], v[8:11]\n\t"
"s_waitcnt lgkmcnt(5)\n\t"
"v_cvt_pk_bf16_f32 v235, v226, v227\n\t"
"v_add_u32 v243, 59904, v243\n\t"
"v_mfma_f32_16x16x32_bf16 v[8:11], a[224:227], a[180:183], v[8:11]\n\t"
"v_mfma_f32_16x16x32_bf16 v[8:11], a[252:255], a[176:179], v[8:11]\n\t"
"ds_write_b128 v243, v[232:235] offset:1536\n\t"
"v_mfma_f32_16x16x32_bf16 v[12:15], a[252:255], a[180:183], v[12:15]\n\t"
"s_waitcnt lgkmcnt(5)\n\t"
"v_mfma_f32_16x16x32_bf16 v[12:15], a[224:227], a[184:187], v[12:15]\n\t"
"ds_read_b64_tr_b16 v[228:229], v240 offset:20992\n\t"
"v_mfma_f32_16x16x32_bf16 v[12:15], a[220:223], a[188:191], v[12:15]\n\t"
"ds_read_b64_tr_b16 v[230:231], v240 offset:22528\n\t"
"v_mfma_f32_16x16x32_bf16 v[16:19], a[220:223], a[192:195], v[16:19]\n\t"
"s_waitcnt lgkmcnt(6)\n\t"
"ds_read_b64_tr_b16 v[224:225], v241 offset:20992\n\t"
"v_mfma_f32_16x16x32_bf16 v[16:19], a[224:227], a[188:191], v[16:19]\n\t"
"ds_read_b64_tr_b16 v[226:227], v241 offset:22528\n\t"
"v_mfma_f32_16x16x32_bf16 v[16:19], a[252:255], a[184:187], v[16:19]\n\t"
"ds_read_b64_tr_b16 v[236:237], v240 offset:40960\n\t"
"v_mfma_f32_16x16x32_bf16 v[20:23], a[252:255], a[188:191], v[20:23]\n\t"
"s_waitcnt lgkmcnt(8)\n\t"
"ds_read_b64_tr_b16 v[238:239], v240 offset:42496\n\t"
"v_mfma_f32_16x16x32_bf16 v[20:23], a[224:227], a[192:195], v[20:23]\n\t"
"ds_read_b64_tr_b16 v[232:233], v241 offset:40960\n\t"
"v_mfma_f32_16x16x32_bf16 v[20:23], a[220:223], a[196:199], v[20:23]\n\t"
"ds_read_b64_tr_b16 v[234:235], v241 offset:42496\n\t"
"v_mfma_f32_16x16x32_bf16 v[24:27], a[220:223], a[200:203], v[24:27]\n\t"
"s_waitcnt lgkmcnt(10)\n\t"
"v_mfma_f32_16x16x32_bf16 v[24:27], a[224:227], a[196:199], v[24:27]\n\t"
"v_mfma_f32_16x16x32_bf16 v[24:27], a[252:255], a[192:195], v[24:27]\n\t"
"v_mfma_f32_16x16x32_bf16 v[28:31], a[252:255], a[196:199], v[28:31]\n\t"
"buffer_load_dwordx4 a[252:255], off, s[80:83], s20 offset:1024 sc1\n\t"
"v_mfma_f32_16x16x32_bf16 v[28:31], a[224:227], a[200:203], v[28:31]\n\t"
"s_waitcnt lgkmcnt(4)\n\t"
"v_mfma_f32_16x16x32_bf16 v[28:31], a[220:223], a[204:207], v[28:31]\n\t"
"s_waitcnt lgkmcnt(2)\n\t"
"v_mfma_f32_16x16x32_bf16 v[228:231], v[228:231], a[208:211], 0\n\t"
"buffer_load_dwordx4 a[220:223], off, s[80:83], s20 offset:2048 sc1\n\t"
"v_mfma_f32_16x16x32_bf16 v[224:227], v[224:227], a[208:211], 0\n\t"
"s_waitcnt vmcnt(9) lgkmcnt(0)\n\t"
"s_mov_b32 s20, 1277952\n\t"
"v_mfma_f32_16x16x32_bf16 v[236:239], v[236:239], a[208:211], 0\n\t"
"v_mfma_f32_16x16x32_bf16 v[232:235], v[232:235], a[208:211], 0\n\t"
"buffer_load_dwordx4 a[224:227], off, s[80:83], s20 offset:0 sc1\n\t"
"v_mfma_f32_16x16x32_bf16 v[32:35], a[228:231], a[168:171], v[32:35]\n\t"
"v_mfma_f32_16x16x32_bf16 v[32:35], v[248:251], a[176:179], v[32:35]\n\t"
"v_cvt_pk_bf16_f32 v228, v228, v229\n\t"
"v_cvt_pk_bf16_f32 v229, v230, v231\n\t"
"v_mfma_f32_16x16x32_bf16 v[32:35], v[96:99], a[172:175], v[32:35]\n\t"
"v_cvt_pk_bf16_f32 v230, v224, v225\n\t"
"v_cvt_pk_bf16_f32 v231, v226, v227\n\t"
"v_mfma_f32_16x16x32_bf16 v[36:39], a[228:231], a[172:175], v[36:39]\n\t"
"ds_read_b64_tr_b16 v[224:225], v246 offset:1024\n\t"
"v_cvt_pk_bf16_f32 v236, v236, v237\n\t"
"v_mfma_f32_16x16x32_bf16 v[36:39], v[96:99], a[176:179], v[36:39]\n\t"
"v_cvt_pk_bf16_f32 v237, v238, v239\n\t"
"v_cvt_pk_bf16_f32 v238, v232, v233\n\t"
"v_mfma_f32_16x16x32_bf16 v[36:39], v[248:251], a[180:183], v[36:39]\n\t"
"buffer_load_dwordx4 a[208:211], off, s[80:83], s20 offset:1024 sc1\n\t"
"v_mfma_f32_16x16x32_bf16 v[40:43], v[248:251], a[184:187], v[40:43]\n\t"
"v_cvt_pk_bf16_f32 v239, v234, v235\n\t"
"v_mfma_f32_16x16x32_bf16 v[40:43], v[96:99], a[180:183], v[40:43]\n\t"
"ds_read_b64_tr_b16 v[232:233], v255 offset:1024\n\t"
"v_mfma_f32_16x16x32_bf16 v[40:43], a[228:231], a[176:179], v[40:43]\n\t"
"ds_write_b128 v243, v[228:231] offset:21504\n\t"
"v_mfma_f32_16x16x32_bf16 v[44:47], a[228:231], a[180:183], v[44:47]\n\t"
"v_mfma_f32_16x16x32_bf16 v[44:47], v[96:99], a[184:187], v[44:47]\n\t"
"v_mfma_f32_16x16x32_bf16 v[44:47], v[248:251], a[188:191], v[44:47]\n\t"
"s_barrier\n\t"
"v_mfma_f32_16x16x32_bf16 v[48:51], a[228:231], a[184:187], v[48:51]\n\t"
"s_waitcnt lgkmcnt(2)\n\t"
"ds_read_b64_tr_b16 v[228:229], v246 offset:7168\n\t"
"v_mfma_f32_16x16x32_bf16 v[48:51], v[248:251], a[192:195], v[48:51]\n\t"
"s_waitcnt lgkmcnt(2)\n\t"
"v_mfma_f32_16x16x32_bf16 v[48:51], v[96:99], a[188:191], v[48:51]\n\t"
"ds_write_b128 v243, v[236:239] offset:41472\n\t"
"ds_read_b64_tr_b16 v[236:237], v255 offset:7168\n\t"
"v_mfma_f32_4x4x4_16b_bf16 v[224:227], v[224:225], v[220:221], 0\n\t"
"buffer_load_dwordx4 v[240:243], off, s[80:83], s20 offset:2048 sc1\n\t"
"v_mfma_f32_4x4x4_16b_bf16 v[232:235], v[232:233], v[220:221], 0\n\t"
"s_waitcnt lgkmcnt(2)\n\t"
"v_mfma_f32_4x4x4_16b_bf16 v[228:231], v[228:229], v[220:221], 0\n\t"
"s_waitcnt lgkmcnt(0)\n\t"
"v_mfma_f32_4x4x4_16b_bf16 v[236:239], v[236:237], v[220:221], 0\n\t"
"v_mfma_f32_16x16x32_bf16 v[52:55], a[228:231], a[188:191], v[52:55]\n\t"
"s_mov_b32 s20, 1511424\n\t"
"v_cvt_pk_bf16_f32 v224, v224, v225\n\t"
"v_cvt_pk_bf16_f32 v225, v226, v227\n\t"
"v_mfma_f32_16x16x32_bf16 v[52:55], v[248:251], a[196:199], v[52:55]\n\t"
"v_cvt_pk_bf16_f32 v226, v232, v233\n\t"
"v_cvt_pk_bf16_f32 v227, v234, v235\n\t"
"v_mfma_f32_16x16x32_bf16 v[52:55], v[96:99], a[192:195], v[52:55]\n\t"
"ds_write_b128 v245, v[224:227] offset:1024\n\t"
"v_mfma_f32_16x16x32_bf16 v[56:59], a[228:231], a[192:195], v[56:59]\n\t"
"v_cvt_pk_bf16_f32 v228, v228, v229\n\t"
"v_cvt_pk_bf16_f32 v229, v230, v231\n\t"
"v_mfma_f32_16x16x32_bf16 v[56:59], v[96:99], a[196:199], v[56:59]\n\t"
"ds_read_b64_tr_b16 v[232:233], v246 offset:13312\n\t"
"v_mfma_f32_16x16x32_bf16 v[56:59], v[248:251], a[200:203], v[56:59]\n\t"
"v_cvt_pk_bf16_f32 v230, v236, v237\n\t"
"v_cvt_pk_bf16_f32 v231, v238, v239\n\t"
"v_mfma_f32_16x16x32_bf16 v[60:63], v[96:99], a[200:203], v[60:63]\n\t"
"s_waitcnt vmcnt(9)\n\t"
"ds_read_b64_tr_b16 v[236:237], v255 offset:13312\n\t"
"v_mfma_f32_16x16x32_bf16 v[60:63], a[228:231], a[196:199], v[60:63]\n\t"
"ds_read_b64_tr_b16 v[224:225], v246 offset:20992\n\t"
"v_mfma_f32_16x16x32_bf16 v[60:63], v[248:251], a[204:207], v[60:63]\n\t"
"ds_write_b128 v245, v[228:231] offset:7168\n\t"
"v_mfma_f32_16x16x32_bf16 v[64:67], a[232:235], a[168:171], v[64:67]\n\t"
"buffer_load_dwordx4 v[96:99], off, s[80:83], s20 offset:0 sc1\n\t"
"v_mfma_f32_16x16x32_bf16 v[64:67], a[236:239], a[176:179], v[64:67]\n\t"
"ds_read_b64_tr_b16 v[228:229], v255 offset:20992\n\t"
"v_mfma_f32_16x16x32_bf16 v[64:67], a[240:243], a[172:175], v[64:67]\n\t"
"buffer_load_dwordx4 v[248:251], off, s[80:83], s20 offset:1024 sc1\n\t"
"v_mfma_f32_16x16x32_bf16 v[68:71], a[232:235], a[172:175], v[68:71]\n\t"
"buffer_load_dwordx4 a[228:231], off, s[80:83], s20 offset:2048 sc1\n\t"
"v_mfma_f32_16x16x32_bf16 v[68:71], a[240:243], a[176:179], v[68:71]\n\t"
"s_waitcnt lgkmcnt(3)\n\t"
"ds_read_b128 a[168:171], v222 offset:2304\n\t"
"v_mfma_f32_16x16x32_bf16 v[68:71], a[236:239], a[180:183], v[68:71]\n\t"
"s_waitcnt lgkmcnt(3)\n\t"
"ds_read_b128 a[172:175], v223 offset:8448\n\t"
"v_mfma_f32_4x4x4_16b_bf16 v[232:235], v[232:233], v[220:221], 0\n\t"
"s_waitcnt lgkmcnt(2)\n\t"
"v_mfma_f32_4x4x4_16b_bf16 v[236:239], v[236:237], v[220:221], 0\n\t"
"v_mfma_f32_4x4x4_16b_bf16 v[224:227], v[224:225], v[220:221], 0\n\t"
"v_mfma_f32_4x4x4_16b_bf16 v[228:231], v[228:229], v[220:221], 0\n\t"
"v_mfma_f32_16x16x32_bf16 v[72:75], a[240:243], a[180:183], v[72:75]\n\t"
"v_cvt_pk_bf16_f32 v232, v232, v233\n\t"
"v_cvt_pk_bf16_f32 v233, v234, v235\n\t"
"v_mfma_f32_16x16x32_bf16 v[72:75], a[232:235], a[176:179], v[72:75]\n\t"
"ds_read_b128 a[176:179], v223 offset:14592\n\t"
"v_cvt_pk_bf16_f32 v234, v236, v237\n\t"
"v_mfma_f32_16x16x32_bf16 v[72:75], a[236:239], a[184:187], v[72:75]\n\t"
"v_cvt_pk_bf16_f32 v235, v238, v239\n\t"
"v_cvt_pk_bf16_f32 v224, v224, v225\n\t"
"v_mfma_f32_16x16x32_bf16 v[76:79], a[232:235], a[180:183], v[76:79]\n\t"
"ds_read_b64_tr_b16 v[236:237], v246 offset:27136\n\t"
"v_cvt_pk_bf16_f32 v225, v226, v227\n\t"
"v_mfma_f32_16x16x32_bf16 v[76:79], a[236:239], a[188:191], v[76:79]\n\t"
"ds_read_b128 a[180:183], v222 offset:22272\n\t"
"v_cvt_pk_bf16_f32 v226, v228, v229\n\t"
"v_mfma_f32_16x16x32_bf16 v[76:79], a[240:243], a[184:187], v[76:79]\n\t"
"ds_write_b128 v245, v[232:235] offset:13312\n\t"
"v_cvt_pk_bf16_f32 v227, v230, v231\n\t"
"v_mfma_f32_16x16x32_bf16 v[80:83], a[232:235], a[184:187], v[80:83]\n\t"
"v_mfma_f32_16x16x32_bf16 v[80:83], a[240:243], a[188:191], v[80:83]\n\t"
"ds_read_b64_tr_b16 v[228:229], v255 offset:27136\n\t"
"v_mfma_f32_16x16x32_bf16 v[80:83], a[236:239], a[192:195], v[80:83]\n\t"
"ds_write_b128 v245, v[224:227] offset:20992\n\t"
"v_mfma_f32_16x16x32_bf16 v[84:87], a[236:239], a[196:199], v[84:87]\n\t"
"s_mov_b32 s20, 1523712\n\t"
"v_mfma_f32_16x16x32_bf16 v[84:87], a[240:243], a[192:195], v[84:87]\n\t"
"ds_read_b64_tr_b16 v[232:233], v246 offset:33280\n\t"
"v_mfma_f32_16x16x32_bf16 v[84:87], a[232:235], a[188:191], v[84:87]\n\t"
"ds_read_b64_tr_b16 v[224:225], v255 offset:33280\n\t"
"v_mfma_f32_16x16x32_bf16 v[88:91], a[232:235], a[192:195], v[88:91]\n\t"
"ds_read_b128 a[184:187], v223 offset:28416\n\t"
"v_mfma_f32_16x16x32_bf16 v[88:91], a[240:243], a[196:199], v[88:91]\n\t"
"ds_read_b128 a[188:191], v223 offset:34560\n\t"
"v_mfma_f32_16x16x32_bf16 v[88:91], a[236:239], a[200:203], v[88:91]\n\t"
"ds_read_b128 a[192:195], v222 offset:42240\n\t"
"v_mfma_f32_16x16x32_bf16 v[92:95], a[240:243], a[200:203], v[92:95]\n\t"
"buffer_load_dwordx4 a[240:243], off, s[80:83], s20 offset:0 sc1\n\t"
"v_mfma_f32_16x16x32_bf16 v[92:95], a[232:235], a[196:199], v[92:95]\n\t"
"s_waitcnt lgkmcnt(6)\n\t"
"ds_read_b128 a[196:199], v223 offset:48384\n\t"
"v_mfma_f32_16x16x32_bf16 v[92:95], a[236:239], a[204:207], v[92:95]\n\t"
"s_waitcnt lgkmcnt(5)\n\t"
"ds_read_b128 a[200:203], v223 offset:54528\n\t"
"v_mfma_f32_4x4x4_16b_bf16 v[236:239], v[236:237], v[220:221], 0\n\t"
"s_waitcnt lgkmcnt(5)\n\t"
"v_mfma_f32_4x4x4_16b_bf16 v[228:231], v[228:229], v[220:221], 0\n\t"
"s_waitcnt vmcnt(10)\n\t"
"v_mfma_f32_4x4x4_16b_bf16 v[232:235], v[232:233], v[220:221], 0\n\t"
"v_mfma_f32_4x4x4_16b_bf16 v[224:227], v[224:225], v[220:221], 0\n\t"
"buffer_load_dwordx4 a[232:235], off, s[80:83], s20 offset:1024 sc1\n\t"
"v_mfma_f32_16x16x32_bf16 v[100:103], a[244:247], a[168:171], v[100:103]\n\t"
"buffer_load_dwordx4 a[236:239], off, s[80:83], s20 offset:2048 sc1\n\t"
"v_mfma_f32_16x16x32_bf16 v[100:103], a[216:219], a[176:179], v[100:103]\n\t"
"ds_read_b128 a[204:207], v222 offset:62208\n\t"
"v_cvt_pk_bf16_f32 v236, v236, v237\n\t"
"v_mfma_f32_16x16x32_bf16 v[100:103], a[212:215], a[172:175], v[100:103]\n\t"
"v_cvt_pk_bf16_f32 v237, v238, v239\n\t"
"v_cvt_pk_bf16_f32 v238, v228, v229\n\t"
"v_mfma_f32_16x16x32_bf16 v[104:107], a[244:247], a[172:175], v[104:107]\n\t"
"s_waitcnt lgkmcnt(5)\n\t"
"v_cvt_pk_bf16_f32 v239, v230, v231\n\t"
"v_cvt_pk_bf16_f32 v232, v232, v233\n\t"
"v_mfma_f32_16x16x32_bf16 v[104:107], a[212:215], a[176:179], v[104:107]\n\t"
"v_cvt_pk_bf16_f32 v233, v234, v235\n\t"
"v_cvt_pk_bf16_f32 v234, v224, v225\n\t"
"v_mfma_f32_16x16x32_bf16 v[104:107], a[216:219], a[180:183], v[104:107]\n\t"
"ds_write_b128 v245, v[236:239] offset:27136\n\t"
"v_cvt_pk_bf16_f32 v235, v226, v227\n\t"
"v_mfma_f32_16x16x32_bf16 v[108:111], a[216:219], a[184:187], v[108:111]\n\t"
"s_waitcnt lgkmcnt(5)\n\t"
"v_mfma_f32_16x16x32_bf16 v[108:111], a[212:215], a[180:183], v[108:111]\n\t"
"v_mfma_f32_16x16x32_bf16 v[108:111], a[244:247], a[176:179], v[108:111]\n\t"
"ds_write_b128 v245, v[232:235] offset:33280\n\t"
"v_mfma_f32_16x16x32_bf16 v[112:115], a[244:247], a[180:183], v[112:115]\n\t"
"s_waitcnt lgkmcnt(5)\n\t"
"v_mfma_f32_16x16x32_bf16 v[112:115], a[212:215], a[184:187], v[112:115]\n\t"
"ds_read_b64_tr_b16 v[228:229], v246 offset:40960\n\t"
"v_mfma_f32_16x16x32_bf16 v[112:115], a[216:219], a[188:191], v[112:115]\n\t"
"ds_read_b64_tr_b16 v[224:225], v255 offset:40960\n\t"
"v_mfma_f32_16x16x32_bf16 v[116:119], a[216:219], a[192:195], v[116:119]\n\t"
"s_waitcnt lgkmcnt(6)\n\t"
"ds_read_b64_tr_b16 v[236:237], v246 offset:47104\n\t"
"v_mfma_f32_16x16x32_bf16 v[116:119], a[212:215], a[188:191], v[116:119]\n\t"
"ds_read_b64_tr_b16 v[232:233], v255 offset:47104\n\t"
"v_mfma_f32_16x16x32_bf16 v[116:119], a[244:247], a[184:187], v[116:119]\n\t"
"v_mfma_f32_16x16x32_bf16 v[120:123], a[244:247], a[188:191], v[120:123]\n\t"
"s_waitcnt lgkmcnt(7)\n\t"
"s_mov_b32 s20, 1536000\n\t"
"v_mfma_f32_16x16x32_bf16 v[120:123], a[212:215], a[192:195], v[120:123]\n\t"
"v_mfma_f32_16x16x32_bf16 v[120:123], a[216:219], a[196:199], v[120:123]\n\t"
"v_mfma_f32_16x16x32_bf16 v[124:127], a[216:219], a[200:203], v[124:127]\n\t"
"s_waitcnt lgkmcnt(6)\n\t"
"v_mfma_f32_16x16x32_bf16 v[124:127], a[212:215], a[196:199], v[124:127]\n\t"
"v_mfma_f32_16x16x32_bf16 v[124:127], a[244:247], a[192:195], v[124:127]\n\t"
"v_mfma_f32_16x16x32_bf16 v[128:131], a[244:247], a[196:199], v[128:131]\n\t"
"buffer_load_dwordx4 a[244:247], off, s[80:83], s20 offset:0 sc1\n\t"
"v_mfma_f32_16x16x32_bf16 v[128:131], a[212:215], a[200:203], v[128:131]\n\t"
"s_waitcnt lgkmcnt(2)\n\t"
"v_mfma_f32_16x16x32_bf16 v[128:131], a[216:219], a[204:207], v[128:131]\n\t"
"s_waitcnt lgkmcnt(1)\n\t"
"v_mfma_f32_4x4x4_16b_bf16 v[228:231], v[228:229], v[220:221], 0\n\t"
"s_waitcnt lgkmcnt(0)\n\t"
"v_mfma_f32_4x4x4_16b_bf16 v[224:227], v[224:225], v[220:221], 0\n\t"
"s_waitcnt vmcnt(10)\n\t"
"v_mfma_f32_4x4x4_16b_bf16 v[236:239], v[236:237], v[220:221], 0\n\t"
"v_mfma_f32_4x4x4_16b_bf16 v[232:235], v[232:233], v[220:221], 0\n\t"
"buffer_load_dwordx4 a[212:215], off, s[80:83], s20 offset:1024 sc1\n\t"
"v_mfma_f32_16x16x32_bf16 v[132:135], a[248:251], a[168:171], v[132:135]\n\t"
"buffer_load_dwordx4 a[216:219], off, s[80:83], s20 offset:2048 sc1\n\t"
"v_mfma_f32_16x16x32_bf16 v[132:135], a[220:223], a[176:179], v[132:135]\n\t"
"v_cvt_pk_bf16_f32 v228, v228, v229\n\t"
"v_cvt_pk_bf16_f32 v229, v230, v231\n\t"
"v_mfma_f32_16x16x32_bf16 v[132:135], a[252:255], a[172:175], v[132:135]\n\t"
"v_cvt_pk_bf16_f32 v230, v224, v225\n\t"
"v_cvt_pk_bf16_f32 v231, v226, v227\n\t"
"v_mfma_f32_16x16x32_bf16 v[136:139], a[248:251], a[172:175], v[136:139]\n\t"
"ds_read_b64_tr_b16 v[224:225], v246 offset:53248\n\t"
"v_cvt_pk_bf16_f32 v236, v236, v237\n\t"
"v_mfma_f32_16x16x32_bf16 v[136:139], a[252:255], a[176:179], v[136:139]\n\t"
"s_mov_b32 s20, 1548288\n\t"
"v_cvt_pk_bf16_f32 v237, v238, v239\n\t"
"v_cvt_pk_bf16_f32 v238, v232, v233\n\t"
"v_mfma_f32_16x16x32_bf16 v[136:139], a[220:223], a[180:183], v[136:139]\n\t"
"ds_write_b128 v245, v[228:231] offset:40960\n\t"
"v_cvt_pk_bf16_f32 v239, v234, v235\n\t"
"v_mfma_f32_16x16x32_bf16 v[140:143], a[220:223], a[184:187], v[140:143]\n\t"
"v_mfma_f32_16x16x32_bf16 v[140:143], a[252:255], a[180:183], v[140:143]\n\t"
"ds_read_b64_tr_b16 v[232:233], v255 offset:53248\n\t"
"v_mfma_f32_16x16x32_bf16 v[140:143], a[248:251], a[176:179], v[140:143]\n\t"
"ds_write_b128 v245, v[236:239] offset:47104\n\t"
"v_mfma_f32_16x16x32_bf16 v[144:147], a[248:251], a[180:183], v[144:147]\n\t"
"v_mfma_f32_16x16x32_bf16 v[144:147], a[252:255], a[184:187], v[144:147]\n\t"
"buffer_load_dwordx4 v[236:239], off, s[80:83], s20 offset:0 sc1\n\t"
"v_mfma_f32_16x16x32_bf16 v[144:147], a[220:223], a[188:191], v[144:147]\n\t"
"v_mfma_f32_16x16x32_bf16 v[148:151], a[248:251], a[184:187], v[148:151]\n\t"
"s_waitcnt lgkmcnt(1)\n\t"
"v_mfma_f32_16x16x32_bf16 v[148:151], a[220:223], a[192:195], v[148:151]\n\t"
"v_mfma_f32_16x16x32_bf16 v[148:151], a[252:255], a[188:191], v[148:151]\n\t"
"v_mfma_f32_4x4x4_16b_bf16 v[224:227], v[224:225], v[220:221], 0\n\t"
"v_mfma_f32_4x4x4_16b_bf16 v[232:235], v[232:233], v[220:221], 0\n\t"
"s_waitcnt vmcnt(10)\n\t"
"v_mfma_f32_16x16x32_bf16 v[152:155], a[248:251], a[188:191], v[152:155]\n\t"
"Lpg8_%=:\n\t"
"s_cbranch_vccnz Lpg12_%=\n\t"
"v_mfma_f32_16x16x32_bf16 v[152:155], a[220:223], a[196:199], v[152:155]\n\t"
"v_cvt_pk_bf16_f32 v224, v224, v225\n\t"
"v_cvt_pk_bf16_f32 v225, v226, v227\n\t"
"v_mfma_f32_16x16x32_bf16 v[152:155], a[252:255], a[192:195], v[152:155]\n\t"
"v_cvt_pk_bf16_f32 v226, v232, v233\n\t"
"v_cvt_pk_bf16_f32 v227, v234, v235\n\t"
"v_mfma_f32_16x16x32_bf16 v[156:159], a[248:251], a[192:195], v[156:159]\n\t"
"v_mfma_f32_16x16x32_bf16 v[156:159], a[252:255], a[196:199], v[156:159]\n\t"
"v_mfma_f32_16x16x32_bf16 v[156:159], a[220:223], a[200:203], v[156:159]\n\t"
"ds_write_b128 v245, v[224:227] offset:53248\n\t"
"v_mfma_f32_16x16x32_bf16 v[160:163], a[252:255], a[200:203], v[160:163]\n\t"
"buffer_load_dwordx4 a[252:255], off, s[80:83], s20 offset:1024 sc1\n\t"
"v_mfma_f32_16x16x32_bf16 v[160:163], a[248:251], a[196:199], v[160:163]\n\t"
"v_mfma_f32_16x16x32_bf16 v[160:163], a[220:223], a[204:207], v[160:163]\n\t"
"v_mfma_f32_16x16x32_bf16 v[164:167], a[224:227], a[168:171], v[164:167]\n\t"
"buffer_load_dwordx4 a[220:223], off, s[80:83], s20 offset:2048 sc1\n\t"
"v_mfma_f32_16x16x32_bf16 v[164:167], v[240:243], a[176:179], v[164:167]\n\t"
"s_mov_b32 s20, 1560576\n\t"
"v_mfma_f32_16x16x32_bf16 v[164:167], a[208:211], a[172:175], v[164:167]\n\t"
"s_waitcnt lgkmcnt(0)\n\t"
"s_barrier\n\t"
"v_mfma_f32_16x16x32_bf16 v[168:171], a[224:227], a[172:175], v[168:171]\n\t"
"ds_read_b128 a[168:171], v222 offset:2560\n\t"
"v_mfma_f32_16x16x32_bf16 v[168:171], a[208:211], a[176:179], v[168:171]\n\t"
"buffer_load_dwordx4 a[248:251], off, s[80:83], s20 offset:0 sc1\n\t"
"v_mfma_f32_16x16x32_bf16 v[168:171], v[240:243], a[180:183], v[168:171]\n\t"
"ds_read_b128 a[172:175], v223 offset:8704\n\t"
"v_mfma_f32_16x16x32_bf16 v[172:175], v[240:243], a[184:187], v[172:175]\n\t"
"v_mfma_f32_16x16x32_bf16 v[172:175], a[208:211], a[180:183], v[172:175]\n\t"
"v_mfma_f32_16x16x32_bf16 v[172:175], a[224:227], a[176:179], v[172:175]\n\t"
"ds_read_b128 a[176:179], v223 offset:14848\n\t"
"v_mfma_f32_16x16x32_bf16 v[176:179], a[224:227], a[180:183], v[176:179]\n\t"
"ds_read_b128 a[180:183], v222 offset:22528\n\t"
"v_mfma_f32_16x16x32_bf16 v[176:179], a[208:211], a[184:187], v[176:179]\n\t"
"v_mfma_f32_16x16x32_bf16 v[176:179], v[240:243], a[188:191], v[176:179]\n\t"
"v_mfma_f32_16x16x32_bf16 v[180:183], v[240:243], a[192:195], v[180:183]\n\t"
"v_mfma_f32_16x16x32_bf16 v[180:183], a[208:211], a[188:191], v[180:183]\n\t"
"v_mfma_f32_16x16x32_bf16 v[180:183], a[224:227], a[184:187], v[180:183]\n\t"
"ds_read_b128 a[184:187], v223 offset:28672\n\t"
"v_mfma_f32_16x16x32_bf16 v[184:187], a[224:227], a[188:191], v[184:187]\n\t"
"ds_read_b128 a[188:191], v223 offset:34816\n\t"
"v_mfma_f32_16x16x32_bf16 v[184:187], a[208:211], a[192:195], v[184:187]\n\t"
"v_mfma_f32_16x16x32_bf16 v[184:187], v[240:243], a[196:199], v[184:187]\n\t"
"v_mfma_f32_16x16x32_bf16 v[188:191], v[240:243], a[200:203], v[188:191]\n\t"
"v_mfma_f32_16x16x32_bf16 v[188:191], a[208:211], a[196:199], v[188:191]\n\t"
"v_mfma_f32_16x16x32_bf16 v[188:191], a[224:227], a[192:195], v[188:191]\n\t"
"ds_read_b128 a[192:195], v222 offset:42496\n\t"
"v_mfma_f32_16x16x32_bf16 v[192:195], a[224:227], a[196:199], v[192:195]\n\t"
"buffer_load_dwordx4 a[224:227], off, s[80:83], s20 offset:1024 sc1\n\t"
"v_mfma_f32_16x16x32_bf16 v[192:195], a[208:211], a[200:203], v[192:195]\n\t"
"s_waitcnt vmcnt(11) lgkmcnt(4)\n\t"
"ds_read_b128 a[196:199], v223 offset:48640\n\t"
"v_mfma_f32_16x16x32_bf16 v[192:195], v[240:243], a[204:207], v[192:195]\n\t"
"ds_read_b128 a[200:203], v223 offset:54784\n\t"
"Lst11_%=:\n\t"
"v_mfma_f32_16x16x32_bf16 v[0:3], v[96:99], a[168:171], v[0:3]\n\t"
"buffer_load_dwordx4 v[240:243], off, s[80:83], s20 offset:2048 sc1\n\t"
"v_mfma_f32_16x16x32_bf16 v[0:3], a[228:231], a[176:179], v[0:3]\n\t"
"s_waitcnt lgkmcnt(5)\n\t"
"ds_read_b128 a[204:207], v222 offset:62464\n\t"
"s_mov_b32 s20, 1572864\n\t"
"v_mfma_f32_16x16x32_bf16 v[0:3], v[248:251], a[172:175], v[0:3]\n\t"
"v_mfma_f32_16x16x32_bf16 v[4:7], v[96:99], a[172:175], v[4:7]\n\t"
"buffer_load_dwordx4 a[208:211], off, s[80:83], s20 offset:0 sc1\n\t"
"v_mfma_f32_16x16x32_bf16 v[4:7], v[248:251], a[176:179], v[4:7]\n\t"
"s_waitcnt lgkmcnt(5)\n\t"
"v_mfma_f32_16x16x32_bf16 v[4:7], a[228:231], a[180:183], v[4:7]\n\t"
"v_mfma_f32_16x16x32_bf16 v[8:11], a[228:231], a[184:187], v[8:11]\n\t"
"s_waitcnt lgkmcnt(4)\n\t"
"v_mfma_f32_16x16x32_bf16 v[8:11], v[248:251], a[180:183], v[8:11]\n\t"
"v_mfma_f32_16x16x32_bf16 v[8:11], v[96:99], a[176:179], v[8:11]\n\t"
"v_mfma_f32_16x16x32_bf16 v[12:15], v[96:99], a[180:183], v[12:15]\n\t"
"s_waitcnt lgkmcnt(3)\n\t"
"v_mfma_f32_16x16x32_bf16 v[12:15], v[248:251], a[184:187], v[12:15]\n\t"
"v_mfma_f32_16x16x32_bf16 v[12:15], a[228:231], a[188:191], v[12:15]\n\t"
"v_mfma_f32_16x16x32_bf16 v[16:19], a[228:231], a[192:195], v[16:19]\n\t"
"s_waitcnt lgkmcnt(2)\n\t"
"v_mfma_f32_16x16x32_bf16 v[16:19], v[248:251], a[188:191], v[16:19]\n\t"
"v_mfma_f32_16x16x32_bf16 v[16:19], v[96:99], a[184:187], v[16:19]\n\t"
"v_mfma_f32_16x16x32_bf16 v[20:23], v[96:99], a[188:191], v[20:23]\n\t"
"s_waitcnt lgkmcnt(1)\n\t"
"v_mfma_f32_16x16x32_bf16 v[20:23], v[248:251], a[192:195], v[20:23]\n\t"
"v_mfma_f32_16x16x32_bf16 v[20:23], a[228:231], a[196:199], v[20:23]\n\t"
"v_mfma_f32_16x16x32_bf16 v[24:27], a[228:231], a[200:203], v[24:27]\n\t"
"s_waitcnt lgkmcnt(0)\n\t"
"s_add_u32 s21, s21, 6291456\n\t"
"v_add_u32 v254, 6291456, v254\n\t"
"v_mfma_f32_16x16x32_bf16 v[24:27], v[248:251], a[196:199], v[24:27]\n\t"
"v_mfma_f32_16x16x32_bf16 v[24:27], v[96:99], a[192:195], v[24:27]\n\t"
"v_mfma_f32_16x16x32_bf16 v[28:31], v[96:99], a[196:199], v[28:31]\n\t"
"buffer_load_dwordx4 v[96:99], off, s[80:83], s20 offset:1024 sc1\n\t"
"v_mfma_f32_16x16x32_bf16 v[28:31], v[248:251], a[200:203], v[28:31]\n\t"
"s_waitcnt vmcnt(11)\n\t"
"v_mfma_f32_16x16x32_bf16 v[28:31], a[228:231], a[204:207], v[28:31]\n\t"
"v_mfma_f32_16x16x32_bf16 v[32:35], a[240:243], a[168:171], v[32:35]\n\t"
"buffer_load_dwordx4 v[248:251], off, s[80:83], s20 offset:2048 sc1\n\t"
"v_mfma_f32_16x16x32_bf16 v[32:35], a[236:239], a[176:179], v[32:35]\n\t"
"s_mov_b32 s20, 1290240\n\t"
"v_mfma_f32_16x16x32_bf16 v[32:35], a[232:235], a[172:175], v[32:35]\n\t"
"v_mfma_f32_16x16x32_bf16 v[36:39], a[240:243], a[172:175], v[36:39]\n\t"
"buffer_load_dwordx4 a[228:231], off, s[80:83], s20 offset:0 sc1\n\t"
"v_mfma_f32_16x16x32_bf16 v[36:39], a[232:235], a[176:179], v[36:39]\n\t"
"v_mfma_f32_16x16x32_bf16 v[36:39], a[236:239], a[180:183], v[36:39]\n\t"
"v_mfma_f32_16x16x32_bf16 v[40:43], a[236:239], a[184:187], v[40:43]\n\t"
"v_mfma_f32_16x16x32_bf16 v[40:43], a[232:235], a[180:183], v[40:43]\n\t"
"v_mfma_f32_16x16x32_bf16 v[40:43], a[240:243], a[176:179], v[40:43]\n\t"
"v_mfma_f32_16x16x32_bf16 v[44:47], a[240:243], a[180:183], v[44:47]\n\t"
"v_mfma_f32_16x16x32_bf16 v[44:47], a[232:235], a[184:187], v[44:47]\n\t"
"v_mfma_f32_16x16x32_bf16 v[44:47], a[236:239], a[188:191], v[44:47]\n\t"
"v_mfma_f32_16x16x32_bf16 v[48:51], a[236:239], a[192:195], v[48:51]\n\t"
"v_mfma_f32_16x16x32_bf16 v[48:51], a[232:235], a[188:191], v[48:51]\n\t"
"v_mfma_f32_16x16x32_bf16 v[48:51], a[240:243], a[184:187], v[48:51]\n\t"
"v_mfma_f32_16x16x32_bf16 v[52:55], a[240:243], a[188:191], v[52:55]\n\t"
"v_mfma_f32_16x16x32_bf16 v[52:55], a[232:235], a[192:195], v[52:55]\n\t"
"v_mfma_f32_16x16x32_bf16 v[52:55], a[236:239], a[196:199], v[52:55]\n\t"
"v_mfma_f32_16x16x32_bf16 v[56:59], a[236:239], a[200:203], v[56:59]\n\t"
"v_mfma_f32_16x16x32_bf16 v[56:59], a[232:235], a[196:199], v[56:59]\n\t"
"v_mfma_f32_16x16x32_bf16 v[56:59], a[240:243], a[192:195], v[56:59]\n\t"
"v_mfma_f32_16x16x32_bf16 v[60:63], a[240:243], a[196:199], v[60:63]\n\t"
"buffer_load_dwordx4 a[240:243], off, s[80:83], s20 offset:1024 sc1\n\t"
"v_mfma_f32_16x16x32_bf16 v[60:63], a[232:235], a[200:203], v[60:63]\n\t"
"s_waitcnt vmcnt(11)\n\t"
"v_mfma_f32_16x16x32_bf16 v[60:63], a[236:239], a[204:207], v[60:63]\n\t"
"v_mfma_f32_16x16x32_bf16 v[64:67], a[244:247], a[168:171], v[64:67]\n\t"
"buffer_load_dwordx4 a[232:235], off, s[80:83], s20 offset:2048 sc1\n\t"
"v_mfma_f32_16x16x32_bf16 v[64:67], a[216:219], a[176:179], v[64:67]\n\t"
"s_mov_b32 s20, 1302528\n\t"
"ds_read_b128 a[168:171], v222 offset:2816\n\t"
"v_mfma_f32_16x16x32_bf16 v[64:67], a[212:215], a[172:175], v[64:67]\n\t"
"v_mfma_f32_16x16x32_bf16 v[68:71], a[244:247], a[172:175], v[68:71]\n\t"
"buffer_load_dwordx4 a[236:239], off, s[80:83], s20 offset:0 sc1\n\t"
"v_mfma_f32_16x16x32_bf16 v[68:71], a[212:215], a[176:179], v[68:71]\n\t"
"ds_read_b128 a[172:175], v223 offset:8960\n\t"
"v_mfma_f32_16x16x32_bf16 v[68:71], a[216:219], a[180:183], v[68:71]\n\t"
"v_mfma_f32_16x16x32_bf16 v[72:75], a[216:219], a[184:187], v[72:75]\n\t"
"v_mfma_f32_16x16x32_bf16 v[72:75], a[212:215], a[180:183], v[72:75]\n\t"
"v_mfma_f32_16x16x32_bf16 v[72:75], a[244:247], a[176:179], v[72:75]\n\t"
"ds_read_b128 a[176:179], v223 offset:15104\n\t"
"v_mfma_f32_16x16x32_bf16 v[76:79], a[244:247], a[180:183], v[76:79]\n\t"
"ds_read_b128 a[180:183], v222 offset:22784\n\t"
"v_mfma_f32_16x16x32_bf16 v[76:79], a[212:215], a[184:187], v[76:79]\n\t"
"v_mfma_f32_16x16x32_bf16 v[76:79], a[216:219], a[188:191], v[76:79]\n\t"
"v_mfma_f32_16x16x32_bf16 v[80:83], a[216:219], a[192:195], v[80:83]\n\t"
"v_mfma_f32_16x16x32_bf16 v[80:83], a[212:215], a[188:191], v[80:83]\n\t"
"v_mfma_f32_16x16x32_bf16 v[80:83], a[244:247], a[184:187], v[80:83]\n\t"
"ds_read_b128 a[184:187], v223 offset:28928\n\t"
"v_mfma_f32_16x16x32_bf16 v[84:87], a[244:247], a[188:191], v[84:87]\n\t"
"ds_read_b128 a[188:191], v223 offset:35072\n\t"
"v_mfma_f32_16x16x32_bf16 v[84:87], a[212:215], a[192:195], v[84:87]\n\t"
"v_mfma_f32_16x16x32_bf16 v[84:87], a[216:219], a[196:199], v[84:87]\n\t"
"v_mfma_f32_16x16x32_bf16 v[88:91], a[216:219], a[200:203], v[88:91]\n\t"
"v_mfma_f32_16x16x32_bf16 v[88:91], a[212:215], a[196:199], v[88:91]\n\t"
"v_mfma_f32_16x16x32_bf16 v[88:91], a[244:247], a[192:195], v[88:91]\n\t"
"ds_read_b128 a[192:195], v222 offset:42752\n\t"
"v_mfma_f32_16x16x32_bf16 v[92:95], a[244:247], a[196:199], v[92:95]\n\t"
"buffer_load_dwordx4 a[244:247], off, s[80:83], s20 offset:1024 sc1\n\t"
"v_mfma_f32_16x16x32_bf16 v[92:95], a[212:215], a[200:203], v[92:95]\n\t"
"s_waitcnt vmcnt(11) lgkmcnt(4)\n\t"
"ds_read_b128 a[196:199], v223 offset:48896\n\t"
"v_mfma_f32_16x16x32_bf16 v[92:95], a[216:219], a[204:207], v[92:95]\n\t"
"ds_read_b128 a[200:203], v223 offset:55040\n\t"
"v_mfma_f32_16x16x32_bf16 v[100:103], v[236:239], a[168:171], v[100:103]\n\t"
"s_waitcnt lgkmcnt(5)\n\t"
"ds_read_b128 a[204:207], v222 offset:62720\n\t"
"v_mfma_f32_16x16x32_bf16 v[100:103], a[220:223], a[176:179], v[100:103]\n\t"
"buffer_load_dwordx4 a[212:215], off, s[80:83], s20 offset:2048 sc1\n\t"
"v_mfma_f32_16x16x32_bf16 v[100:103], a[252:255], a[172:175], v[100:103]\n\t"
"s_mov_b32 s20, 1314816\n\t"
"s_add_u32 m0, s26, 512\n\t"
"v_mfma_f32_16x16x32_bf16 v[104:107], v[236:239], a[172:175], v[104:107]\n\t"
"buffer_load_dwordx4 a[216:219], off, s[80:83], s20 offset:0 sc1\n\t"
"v_mfma_f32_16x16x32_bf16 v[104:107], a[252:255], a[176:179], v[104:107]\n\t"
"s_waitcnt lgkmcnt(5)\n\t"
"v_mfma_f32_16x16x32_bf16 v[104:107], a[220:223], a[180:183], v[104:107]\n\t"
"s_waitcnt lgkmcnt(0)\n\t"
"s_barrier\n\t"
"v_mfma_f32_16x16x32_bf16 v[108:111], a[220:223], a[184:187], v[108:111]\n\t"
"v_fma_f32 v0, v100, s52, v0\n\t"
"v_mfma_f32_16x16x32_bf16 v[108:111], a[252:255], a[180:183], v[108:111]\n\t"
"v_fma_f32 v1, v101, s52, v1\n\t"
"v_fma_f32 v2, v102, s52, v2\n\t"
"v_mfma_f32_16x16x32_bf16 v[108:111], v[236:239], a[176:179], v[108:111]\n\t"
"s_mov_b64 exec, s[36:37]\n\t"
"buffer_load_dwordx4 v254, s[12:15], s27 offen offset:0 nt lds\n\t"
"s_mov_b64 exec, -1\n\t"
"s_add_u32 m0, s24, 22016\n\t"
"s_add_u32 s28, s21, 592896\n\t"
"v_mfma_f32_16x16x32_bf16 v[112:115], v[236:239], a[180:183], v[112:115]\n\t"
"buffer_load_dwordx4 v247, s[12:15], s28 offen offset:0 nt lds\n\t"
"v_mfma_f32_16x16x32_bf16 v[112:115], a[252:255], a[184:187], v[112:115]\n\t"
"s_add_u32 m0, s24, 28160\n\t"
"s_add_u32 s28, s21, 789504\n\t"
"v_fma_f32 v3, v103, s52, v3\n\t"
"v_mfma_f32_16x16x32_bf16 v[112:115], a[220:223], a[188:191], v[112:115]\n\t"
"buffer_load_dwordx4 v247, s[12:15], s28 offen offset:0 nt lds\n\t"
"s_add_u32 m0, s24, 34304\n\t"
"s_add_u32 s28, s21, 986112\n\t"
"v_mfma_f32_16x16x32_bf16 v[116:119], a[220:223], a[192:195], v[116:119]\n\t"
"buffer_load_dwordx4 v247, s[12:15], s28 offen offset:0 nt lds\n\t"
"v_mfma_f32_16x16x32_bf16 v[116:119], a[252:255], a[188:191], v[116:119]\n\t"
"s_add_u32 m0, s24, 41984\n\t"
"s_add_u32 s28, s21, 1182720\n\t"
"v_fma_f32 v100, v0, s54, -v100\n\t"
"v_mfma_f32_16x16x32_bf16 v[116:119], v[236:239], a[184:187], v[116:119]\n\t"
"v_fma_f32 v101, v1, s54, -v101\n\t"
"v_fma_f32 v102, v2, s54, -v102\n\t"
"v_mfma_f32_16x16x32_bf16 v[120:123], v[236:239], a[188:191], v[120:123]\n\t"
"v_fma_f32 v103, v3, s54, -v103\n\t"
"v_fma_f32 v4, v104, s52, v4\n\t"
"v_mfma_f32_16x16x32_bf16 v[120:123], a[252:255], a[192:195], v[120:123]\n\t"
"v_fma_f32 v5, v105, s52, v5\n\t"
"v_fma_f32 v6, v106, s52, v6\n\t"
"v_mfma_f32_16x16x32_bf16 v[120:123], a[220:223], a[196:199], v[120:123]\n\t"
"buffer_load_dwordx4 v247, s[12:15], s28 offen offset:0 nt lds\n\t"
"s_add_u32 m0, s24, 48128\n\t"
"s_add_u32 s28, s21, 1379328\n\t"
"v_mfma_f32_16x16x32_bf16 v[124:127], a[220:223], a[200:203], v[124:127]\n\t"
"buffer_load_dwordx4 v247, s[12:15], s28 offen offset:0 nt lds\n\t"
"v_mfma_f32_16x16x32_bf16 v[124:127], a[252:255], a[196:199], v[124:127]\n\t"
"v_fma_f32 v7, v107, s52, v7\n\t"
"v_fma_f32 v104, v4, s54, -v104\n\t"
"v_mfma_f32_16x16x32_bf16 v[124:127], v[236:239], a[192:195], v[124:127]\n\t"
"v_fma_f32 v105, v5, s54, -v105\n\t"
"v_fma_f32 v106, v6, s54, -v106\n\t"
"v_mfma_f32_16x16x32_bf16 v[128:131], v[236:239], a[196:199], v[128:131]\n\t"
"s_waitcnt vmcnt(16)\n\t"
"v_fma_f32 v107, v7, s54, -v107\n\t"
"v_fma_f32 v8, v108, s52, v8\n\t"
"v_mfma_f32_16x16x32_bf16 v[128:131], a[252:255], a[200:203], v[128:131]\n\t"
"v_fma_f32 v9, v109, s52, v9\n\t"
"v_fma_f32 v10, v110, s52, v10\n\t"
"v_mfma_f32_16x16x32_bf16 v[128:131], a[220:223], a[204:207], v[128:131]\n\t"
"v_fma_f32 v11, v111, s52, v11\n\t"
"v_fma_f32 v108, v8, s54, -v108\n\t"
"v_mfma_f32_16x16x32_bf16 v[132:135], a[248:251], a[168:171], v[132:135]\n\t"
"v_fma_f32 v109, v9, s54, -v109\n\t"
"v_fma_f32 v110, v10, s54, -v110\n\t"
"v_mfma_f32_16x16x32_bf16 v[132:135], v[240:243], a[176:179], v[132:135]\n\t"
"v_fma_f32 v111, v11, s54, -v111\n\t"
"v_fma_f32 v12, v112, s52, v12\n\t"
"v_mfma_f32_16x16x32_bf16 v[132:135], a[224:227], a[172:175], v[132:135]\n\t"
"v_fma_f32 v13, v113, s52, v13\n\t"
"v_fma_f32 v14, v114, s52, v14\n\t"
"v_mfma_f32_16x16x32_bf16 v[136:139], a[248:251], a[172:175], v[136:139]\n\t"
"v_fma_f32 v15, v115, s52, v15\n\t"
"v_fma_f32 v112, v12, s54, -v112\n\t"
"v_mfma_f32_16x16x32_bf16 v[136:139], a[224:227], a[176:179], v[136:139]\n\t"
"v_fma_f32 v113, v13, s54, -v113\n\t"
"v_fma_f32 v114, v14, s54, -v114\n\t"
"v_mfma_f32_16x16x32_bf16 v[136:139], v[240:243], a[180:183], v[136:139]\n\t"
"v_fma_f32 v115, v15, s54, -v115\n\t"
"v_fma_f32 v16, v116, s52, v16\n\t"
"v_mfma_f32_16x16x32_bf16 v[140:143], v[240:243], a[184:187], v[140:143]\n\t"
"v_fma_f32 v17, v117, s52, v17\n\t"
"v_fma_f32 v18, v118, s52, v18\n\t"
"v_mfma_f32_16x16x32_bf16 v[140:143], a[224:227], a[180:183], v[140:143]\n\t"
"v_fma_f32 v19, v119, s52, v19\n\t"
"v_fma_f32 v116, v16, s54, -v116\n\t"
"v_mfma_f32_16x16x32_bf16 v[140:143], a[248:251], a[176:179], v[140:143]\n\t"
"v_fma_f32 v117, v17, s54, -v117\n\t"
"v_fma_f32 v118, v18, s54, -v118\n\t"
"v_mfma_f32_16x16x32_bf16 v[144:147], a[248:251], a[180:183], v[144:147]\n\t"
"v_fma_f32 v119, v19, s54, -v119\n\t"
"v_fma_f32 v20, v120, s52, v20\n\t"
"v_mfma_f32_16x16x32_bf16 v[144:147], a[224:227], a[184:187], v[144:147]\n\t"
"v_fma_f32 v21, v121, s52, v21\n\t"
"v_fma_f32 v22, v122, s52, v22\n\t"
"v_mfma_f32_16x16x32_bf16 v[144:147], v[240:243], a[188:191], v[144:147]\n\t"
"v_fma_f32 v23, v123, s52, v23\n\t"
"v_fma_f32 v120, v20, s54, -v120\n\t"
"v_mfma_f32_16x16x32_bf16 v[148:151], v[240:243], a[192:195], v[148:151]\n\t"
"v_fma_f32 v121, v21, s54, -v121\n\t"
"v_fma_f32 v122, v22, s54, -v122\n\t"
"v_mfma_f32_16x16x32_bf16 v[148:151], a[224:227], a[188:191], v[148:151]\n\t"
"v_fma_f32 v123, v23, s54, -v123\n\t"
"v_fma_f32 v24, v124, s52, v24\n\t"
"v_mfma_f32_16x16x32_bf16 v[148:151], a[248:251], a[184:187], v[148:151]\n\t"
"v_fma_f32 v25, v125, s52, v25\n\t"
"v_fma_f32 v26, v126, s52, v26\n\t"
"v_mfma_f32_16x16x32_bf16 v[152:155], a[248:251], a[188:191], v[152:155]\n\t"
"v_fma_f32 v27, v127, s52, v27\n\t"
"v_fma_f32 v124, v24, s54, -v124\n\t"
"v_mfma_f32_16x16x32_bf16 v[152:155], a[224:227], a[192:195], v[152:155]\n\t"
"v_fma_f32 v125, v25, s54, -v125\n\t"
"v_fma_f32 v126, v26, s54, -v126\n\t"
"v_mfma_f32_16x16x32_bf16 v[152:155], v[240:243], a[196:199], v[152:155]\n\t"
"v_fma_f32 v127, v27, s54, -v127\n\t"
"v_fma_f32 v28, v128, s52, v28\n\t"
"v_mfma_f32_16x16x32_bf16 v[156:159], v[240:243], a[200:203], v[156:159]\n\t"
"v_fma_f32 v29, v129, s52, v29\n\t"
"v_fma_f32 v30, v130, s52, v30\n\t"
"v_mfma_f32_16x16x32_bf16 v[156:159], a[224:227], a[196:199], v[156:159]\n\t"
"v_fma_f32 v31, v131, s52, v31\n\t"
"v_fma_f32 v128, v28, s54, -v128\n\t"
"v_mfma_f32_16x16x32_bf16 v[156:159], a[248:251], a[192:195], v[156:159]\n\t"
"v_fma_f32 v129, v29, s54, -v129\n\t"
"v_fma_f32 v130, v30, s54, -v130\n\t"
"v_mfma_f32_16x16x32_bf16 v[160:163], a[248:251], a[196:199], v[160:163]\n\t"
"s_waitcnt vmcnt(13)\n\t"
"v_fma_f32 v131, v31, s54, -v131\n\t"
"v_fma_f32 v32, v132, s52, v32\n\t"
"v_mfma_f32_16x16x32_bf16 v[160:163], a[224:227], a[200:203], v[160:163]\n\t"
"v_fma_f32 v33, v133, s52, v33\n\t"
"v_fma_f32 v34, v134, s52, v34\n\t"
"v_mfma_f32_16x16x32_bf16 v[160:163], v[240:243], a[204:207], v[160:163]\n\t"
"buffer_load_dwordx4 v[236:239], off, s[80:83], s20 offset:1024 sc1\n\t"
"v_mfma_f32_16x16x32_bf16 v[164:167], a[208:211], a[168:171], v[164:167]\n\t"
"ds_read_b128 a[168:171], v222 offset:512\n\t"
"v_fma_f32 v35, v135, s52, v35\n\t"
"v_mfma_f32_16x16x32_bf16 v[164:167], v[248:251], a[176:179], v[164:167]\n\t"
"v_fma_f32 v132, v32, s54, -v132\n\t"
"v_fma_f32 v133, v33, s54, -v133\n\t"
"v_mfma_f32_16x16x32_bf16 v[164:167], v[96:99], a[172:175], v[164:167]\n\t"
"v_fma_f32 v134, v34, s54, -v134\n\t"
"v_fma_f32 v135, v35, s54, -v135\n\t"
"v_mfma_f32_16x16x32_bf16 v[168:171], a[208:211], a[172:175], v[168:171]\n\t"
"ds_read_b128 a[172:175], v223 offset:6656\n\t"
"v_fma_f32 v36, v136, s52, v36\n\t"
"v_mfma_f32_16x16x32_bf16 v[168:171], v[96:99], a[176:179], v[168:171]\n\t"
"v_fma_f32 v37, v137, s52, v37\n\t"
"v_fma_f32 v38, v138, s52, v38\n\t"
"v_mfma_f32_16x16x32_bf16 v[168:171], v[248:251], a[180:183], v[168:171]\n\t"
"v_fma_f32 v39, v139, s52, v39\n\t"
"v_fma_f32 v136, v36, s54, -v136\n\t"
"v_mfma_f32_16x16x32_bf16 v[172:175], v[248:251], a[184:187], v[172:175]\n\t"
"v_fma_f32 v137, v37, s54, -v137\n\t"
"v_fma_f32 v138, v38, s54, -v138\n\t"
"v_mfma_f32_16x16x32_bf16 v[172:175], v[96:99], a[180:183], v[172:175]\n\t"
"v_fma_f32 v139, v39, s54, -v139\n\t"
"v_fma_f32 v40, v140, s52, v40\n\t"
"v_mfma_f32_16x16x32_bf16 v[172:175], a[208:211], a[176:179], v[172:175]\n\t"
"ds_read_b128 a[176:179], v223 offset:12800\n\t"
"v_fma_f32 v41, v141, s52, v41\n\t"
"v_mfma_f32_16x16x32_bf16 v[176:179], a[208:211], a[180:183], v[176:179]\n\t"
"ds_read_b128 a[180:183], v222 offset:20480\n\t"
"v_fma_f32 v42, v142, s52, v42\n\t"
"v_mfma_f32_16x16x32_bf16 v[176:179], v[96:99], a[184:187], v[176:179]\n\t"
"v_fma_f32 v43, v143, s52, v43\n\t"
"v_fma_f32 v140, v40, s54, -v140\n\t"
"v_mfma_f32_16x16x32_bf16 v[176:179], v[248:251], a[188:191], v[176:179]\n\t"
"v_fma_f32 v141, v41, s54, -v141\n\t"
"v_fma_f32 v142, v42, s54, -v142\n\t"
"v_mfma_f32_16x16x32_bf16 v[180:183], v[248:251], a[192:195], v[180:183]\n\t"
"v_fma_f32 v143, v43, s54, -v143\n\t"
"v_fma_f32 v44, v144, s52, v44\n\t"
"v_mfma_f32_16x16x32_bf16 v[180:183], v[96:99], a[188:191], v[180:183]\n\t"
"v_fma_f32 v45, v145, s52, v45\n\t"
"v_fma_f32 v46, v146, s52, v46\n\t"
"v_mfma_f32_16x16x32_bf16 v[180:183], a[208:211], a[184:187], v[180:183]\n\t"
"ds_read_b128 a[184:187], v223 offset:26624\n\t"
"v_fma_f32 v47, v147, s52, v47\n\t"
"v_mfma_f32_16x16x32_bf16 v[184:187], a[208:211], a[188:191], v[184:187]\n\t"
"ds_read_b128 a[188:191], v223 offset:32768\n\t"
"v_fma_f32 v144, v44, s54, -v144\n\t"
"v_mfma_f32_16x16x32_bf16 v[184:187], v[96:99], a[192:195], v[184:187]\n\t"
"v_fma_f32 v145, v45, s54, -v145\n\t"
"v_fma_f32 v146, v46, s54, -v146\n\t"
"v_mfma_f32_16x16x32_bf16 v[184:187], v[248:251], a[196:199], v[184:187]\n\t"
"v_fma_f32 v147, v47, s54, -v147\n\t"
"v_fma_f32 v48, v148, s52, v48\n\t"
"v_mfma_f32_16x16x32_bf16 v[188:191], v[248:251], a[200:203], v[188:191]\n\t"
"v_fma_f32 v49, v149, s52, v49\n\t"
"v_fma_f32 v50, v150, s52, v50\n\t"
"v_mfma_f32_16x16x32_bf16 v[188:191], v[96:99], a[196:199], v[188:191]\n\t"
"v_fma_f32 v51, v151, s52, v51\n\t"
"v_fma_f32 v148, v48, s54, -v148\n\t"
"v_mfma_f32_16x16x32_bf16 v[188:191], a[208:211], a[192:195], v[188:191]\n\t"
"ds_read_b128 a[192:195], v222 offset:40448\n\t"
"v_fma_f32 v149, v49, s54, -v149\n\t"
"v_mfma_f32_16x16x32_bf16 v[192:195], a[208:211], a[196:199], v[192:195]\n\t"
"s_waitcnt vmcnt(11) lgkmcnt(4)\n\t"
"ds_read_b128 a[196:199], v223 offset:46592\n\t"
"v_fma_f32 v150, v50, s54, -v150\n\t"
"v_mfma_f32_16x16x32_bf16 v[192:195], v[96:99], a[200:203], v[192:195]\n\t"
"ds_read_b128 a[200:203], v223 offset:52736\n\t"
"v_fma_f32 v151, v51, s54, -v151\n\t"
"v_mfma_f32_16x16x32_bf16 v[192:195], v[248:251], a[204:207], v[192:195]\n\t"
"ds_read_b128 a[204:207], v222 offset:60416\n\t"
"v_fma_f32 v52, v152, s52, v52\n\t"
"Lst12_%=:\n\t"
"v_mfma_f32_16x16x32_bf16 v[196:199], a[228:231], a[168:171], v[196:199]\n\t"
"s_waitcnt lgkmcnt(6)\n\t"
"v_fma_f32 v53, v153, s52, v53\n\t"
"v_fma_f32 v54, v154, s52, v54\n\t"
"v_mfma_f32_16x16x32_bf16 v[196:199], a[232:235], a[176:179], v[196:199]\n\t"
"v_fma_f32 v55, v155, s52, v55\n\t"
"v_fma_f32 v152, v52, s54, -v152\n\t"
"v_mfma_f32_16x16x32_bf16 v[196:199], a[240:243], a[172:175], v[196:199]\n\t"
"v_fma_f32 v153, v53, s54, -v153\n\t"
"v_fma_f32 v154, v54, s54, -v154\n\t"
"v_mfma_f32_16x16x32_bf16 v[204:207], a[228:231], a[172:175], v[204:207]\n\t"
"s_waitcnt lgkmcnt(5)\n\t"
"v_fma_f32 v155, v55, s54, -v155\n\t"
"v_fma_f32 v56, v156, s52, v56\n\t"
"v_mfma_f32_16x16x32_bf16 v[204:207], a[240:243], a[176:179], v[204:207]\n\t"
"v_fma_f32 v57, v157, s52, v57\n\t"
"v_fma_f32 v58, v158, s52, v58\n\t"
"v_mfma_f32_16x16x32_bf16 v[204:207], a[232:235], a[180:183], v[204:207]\n\t"
"v_fma_f32 v59, v159, s52, v59\n\t"
"v_fma_f32 v156, v56, s54, -v156\n\t"
"v_mfma_f32_16x16x32_bf16 v[212:215], a[232:235], a[184:187], v[212:215]\n\t"
"s_waitcnt lgkmcnt(4)\n\t"
"v_fma_f32 v157, v57, s54, -v157\n\t"
"v_fma_f32 v158, v58, s54, -v158\n\t"
"v_mfma_f32_16x16x32_bf16 v[212:215], a[240:243], a[180:183], v[212:215]\n\t"
"v_fma_f32 v159, v59, s54, -v159\n\t"
"v_fma_f32 v60, v160, s52, v60\n\t"
"v_mfma_f32_16x16x32_bf16 v[212:215], a[228:231], a[176:179], v[212:215]\n\t"
"v_fma_f32 v61, v161, s52, v61\n\t"
"v_fma_f32 v62, v162, s52, v62\n\t"
"v_mfma_f32_16x16x32_bf16 a[0:3], a[228:231], a[180:183], a[0:3]\n\t"
"s_waitcnt lgkmcnt(3)\n\t"
"v_fma_f32 v63, v163, s52, v63\n\t"
"v_fma_f32 v160, v60, s54, -v160\n\t"
"v_mfma_f32_16x16x32_bf16 a[0:3], a[240:243], a[184:187], a[0:3]\n\t"
"v_fma_f32 v161, v61, s54, -v161\n\t"
"v_fma_f32 v162, v62, s54, -v162\n\t"
"v_mfma_f32_16x16x32_bf16 a[0:3], a[232:235], a[188:191], a[0:3]\n\t"
"buffer_load_dwordx4 v[96:99], off, s[80:83], s20 offset:2048 sc1\n\t"
"s_mov_b32 s20, 1327104\n\t"
"v_mfma_f32_16x16x32_bf16 a[8:11], a[232:235], a[192:195], a[8:11]\n\t"
"buffer_load_dwordx4 v[248:251], off, s[80:83], s20 offset:0 sc1\n\t"
"v_mfma_f32_16x16x32_bf16 a[8:11], a[240:243], a[188:191], a[8:11]\n\t"
"s_waitcnt lgkmcnt(2)\n\t"
"v_fma_f32 v163, v63, s54, -v163\n\t"
"v_fma_f32 v64, v164, s52, v64\n\t"
"v_mfma_f32_16x16x32_bf16 a[8:11], a[228:231], a[184:187], a[8:11]\n\t"
"v_fma_f32 v65, v165, s52, v65\n\t"
"v_fma_f32 v66, v166, s52, v66\n\t"
"v_mfma_f32_16x16x32_bf16 a[16:19], a[228:231], a[188:191], a[16:19]\n\t"
"s_waitcnt lgkmcnt(1)\n\t"
"v_fma_f32 v67, v167, s52, v67\n\t"
"v_fma_f32 v164, v64, s54, -v164\n\t"
"v_mfma_f32_16x16x32_bf16 a[16:19], a[240:243], a[192:195], a[16:19]\n\t"
"v_fma_f32 v165, v65, s54, -v165\n\t"
"v_fma_f32 v166, v66, s54, -v166\n\t"
"v_mfma_f32_16x16x32_bf16 a[16:19], a[232:235], a[196:199], a[16:19]\n\t"
"buffer_load_dwordx4 v[240:243], off, s[80:83], s20 offset:1024 sc1\n\t"
"v_mfma_f32_16x16x32_bf16 a[24:27], a[232:235], a[200:203], a[24:27]\n\t"
"buffer_load_dwordx4 a[208:211], off, s[80:83], s20 offset:2048 sc1\n\t"
"v_mfma_f32_16x16x32_bf16 a[24:27], a[240:243], a[196:199], a[24:27]\n\t"
"s_waitcnt lgkmcnt(0)\n\t"
"v_fma_f32 v167, v67, s54, -v167\n\t"
"v_fma_f32 v68, v168, s52, v68\n\t"
"v_mfma_f32_16x16x32_bf16 a[24:27], a[228:231], a[192:195], a[24:27]\n\t"
"s_mov_b32 s20, 1339392\n\t"
"v_fma_f32 v69, v169, s52, v69\n\t"
"v_fma_f32 v70, v170, s52, v70\n\t"
"v_mfma_f32_16x16x32_bf16 a[32:35], a[228:231], a[196:199], a[32:35]\n\t"
"s_waitcnt vmcnt(12)\n\t"
"v_fma_f32 v71, v171, s52, v71\n\t"
"v_fma_f32 v168, v68, s54, -v168\n\t"
"v_mfma_f32_16x16x32_bf16 a[32:35], a[240:243], a[200:203], a[32:35]\n\t"
"v_fma_f32 v169, v69, s54, -v169\n\t"
"v_fma_f32 v170, v70, s54, -v170\n\t"
"v_mfma_f32_16x16x32_bf16 a[32:35], a[232:235], a[204:207], a[32:35]\n\t"
"v_fma_f32 v171, v71, s54, -v171\n\t"
"v_fma_f32 v72, v172, s52, v72\n\t"
"v_mfma_f32_16x16x32_bf16 a[40:43], a[236:239], a[168:171], a[40:43]\n\t"
"v_fma_f32 v73, v173, s52, v73\n\t"
"v_fma_f32 v74, v174, s52, v74\n\t"
"v_mfma_f32_16x16x32_bf16 a[40:43], a[212:215], a[176:179], a[40:43]\n\t"
"v_fma_f32 v75, v175, s52, v75\n\t"
"v_fma_f32 v172, v72, s54, -v172\n\t"
"v_mfma_f32_16x16x32_bf16 a[40:43], a[244:247], a[172:175], a[40:43]\n\t"
"buffer_load_dwordx4 a[220:223], off, s[80:83], s20 offset:0 sc1\n\t"
"v_mfma_f32_16x16x32_bf16 a[48:51], a[236:239], a[172:175], a[48:51]\n\t"
"buffer_load_dwordx4 a[224:227], off, s[80:83], s20 offset:1024 sc1\n\t"
"v_mfma_f32_16x16x32_bf16 a[48:51], a[244:247], a[176:179], a[48:51]\n\t"
"v_fma_f32 v173, v73, s54, -v173\n\t"
"v_fma_f32 v174, v74, s54, -v174\n\t"
"v_mfma_f32_16x16x32_bf16 a[48:51], a[212:215], a[180:183], a[48:51]\n\t"
"v_fma_f32 v175, v75, s54, -v175\n\t"
"v_fma_f32 v76, v176, s52, v76\n\t"
"v_mfma_f32_16x16x32_bf16 a[56:59], a[212:215], a[184:187], a[56:59]\n\t"
"v_fma_f32 v77, v177, s52, v77\n\t"
"v_fma_f32 v78, v178, s52, v78\n\t"
"v_mfma_f32_16x16x32_bf16 a[56:59], a[244:247], a[180:183], a[56:59]\n\t"
"v_fma_f32 v79, v179, s52, v79\n\t"
"v_fma_f32 v176, v76, s54, -v176\n\t"
"v_mfma_f32_16x16x32_bf16 a[56:59], a[236:239], a[176:179], a[56:59]\n\t"
"v_fma_f32 v177, v77, s54, -v177\n\t"
"v_fma_f32 v178, v78, s54, -v178\n\t"
"v_mfma_f32_16x16x32_bf16 a[64:67], a[236:239], a[180:183], a[64:67]\n\t"
"v_fma_f32 v179, v79, s54, -v179\n\t"
"v_fma_f32 v80, v180, s52, v80\n\t"
"v_mfma_f32_16x16x32_bf16 a[64:67], a[244:247], a[184:187], a[64:67]\n\t"
"v_fma_f32 v81, v181, s52, v81\n\t"
"v_fma_f32 v82, v182, s52, v82\n\t"
"v_mfma_f32_16x16x32_bf16 a[64:67], a[212:215], a[188:191], a[64:67]\n\t"
"buffer_load_dwordx4 a[228:231], off, s[80:83], s20 offset:2048 sc1\n\t"
"s_mov_b32 s20, 1351680\n\t"
"v_mfma_f32_16x16x32_bf16 a[72:75], a[212:215], a[192:195], a[72:75]\n\t"
"buffer_load_dwordx4 a[232:235], off, s[80:83], s20 offset:0 sc1\n\t"
"v_mfma_f32_16x16x32_bf16 a[72:75], a[244:247], a[188:191], a[72:75]\n\t"
"v_fma_f32 v83, v183, s52, v83\n\t"
"v_fma_f32 v180, v80, s54, -v180\n\t"
"v_mfma_f32_16x16x32_bf16 a[72:75], a[236:239], a[184:187], a[72:75]\n\t"
"v_fma_f32 v181, v81, s54, -v181\n\t"
"v_fma_f32 v182, v82, s54, -v182\n\t"
"v_mfma_f32_16x16x32_bf16 a[80:83], a[236:239], a[188:191], a[80:83]\n\t"
"v_fma_f32 v183, v83, s54, -v183\n\t"
"v_fma_f32 v84, v184, s52, v84\n\t"
"v_mfma_f32_16x16x32_bf16 a[80:83], a[244:247], a[192:195], a[80:83]\n\t"
"v_fma_f32 v85, v185, s52, v85\n\t"
"v_fma_f32 v86, v186, s52, v86\n\t"
"v_mfma_f32_16x16x32_bf16 a[80:83], a[212:215], a[196:199], a[80:83]\n\t"
"buffer_load_dwordx4 a[240:243], off, s[80:83], s20 offset:1024 sc1\n\t"
"v_mfma_f32_16x16x32_bf16 a[88:91], a[212:215], a[200:203], a[88:91]\n\t"
"buffer_load_dwordx4 a[248:251], off, s[80:83], s20 offset:2048 sc1\n\t"
"v_mfma_f32_16x16x32_bf16 a[88:91], a[244:247], a[196:199], a[88:91]\n\t"
"s_mov_b32 s20, 1585152\n\t"
"v_fma_f32 v87, v187, s52, v87\n\t"
"v_fma_f32 v184, v84, s54, -v184\n\t"
"v_mfma_f32_16x16x32_bf16 a[88:91], a[236:239], a[192:195], a[88:91]\n\t"
"v_fma_f32 v185, v85, s54, -v185\n\t"
"v_fma_f32 v186, v86, s54, -v186\n\t"
"v_mfma_f32_16x16x32_bf16 a[96:99], a[236:239], a[196:199], a[96:99]\n\t"
"s_waitcnt vmcnt(9)\n\t"
"v_fma_f32 v187, v87, s54, -v187\n\t"
"v_fma_f32 v88, v188, s52, v88\n\t"
"v_mfma_f32_16x16x32_bf16 a[96:99], a[244:247], a[200:203], a[96:99]\n\t"
"v_fma_f32 v89, v189, s52, v89\n\t"
"v_fma_f32 v90, v190, s52, v90\n\t"
"v_mfma_f32_16x16x32_bf16 a[96:99], a[212:215], a[204:207], a[96:99]\n\t"
"v_fma_f32 v91, v191, s52, v91\n\t"
"v_fma_f32 v188, v88, s54, -v188\n\t"
"v_mfma_f32_16x16x32_bf16 a[104:107], a[216:219], a[168:171], a[104:107]\n\t"
"Lpg9_%=:\n\t"
"s_cbranch_vccnz Lvr0_%=\n\t"
"ds_read_b128 a[168:171], v222 offset:768\n\t"
"v_fma_f32 v189, v89, s54, -v189\n\t"
"v_mfma_f32_16x16x32_bf16 a[104:107], v[96:99], a[176:179], a[104:107]\n\t"
"v_fma_f32 v190, v90, s54, -v190\n\t"
"v_fma_f32 v191, v91, s54, -v191\n\t"
"v_mfma_f32_16x16x32_bf16 a[104:107], v[236:239], a[172:175], a[104:107]\n\t"
"buffer_load_dwordx4 a[236:239], off, s[80:83], s20 offset:0 sc1\n\t"
"v_mfma_f32_16x16x32_bf16 a[112:115], a[216:219], a[172:175], a[112:115]\n\t"
"buffer_load_dwordx4 a[212:215], off, s[80:83], s20 offset:1024 sc1\n\t"
"v_mfma_f32_16x16x32_bf16 a[112:115], v[236:239], a[176:179], a[112:115]\n\t"
"buffer_load_dwordx4 a[244:247], off, s[80:83], s20 offset:2048 sc1\n\t"
"v_mfma_f32_16x16x32_bf16 a[112:115], v[96:99], a[180:183], a[112:115]\n\t"
"s_mov_b32 s20, 1597440\n\t"
"ds_read_b128 a[172:175], v223 offset:6912\n\t"
"v_fma_f32 v92, v192, s52, v92\n\t"
"v_mfma_f32_16x16x32_bf16 a[120:123], v[96:99], a[184:187], a[120:123]\n\t"
"v_fma_f32 v93, v193, s52, v93\n\t"
"v_fma_f32 v94, v194, s52, v94\n\t"
"v_mfma_f32_16x16x32_bf16 a[120:123], v[236:239], a[180:183], a[120:123]\n\t"
"v_fma_f32 v95, v195, s52, v95\n\t"
"v_fma_f32 v192, v92, s54, -v192\n\t"
"v_mfma_f32_16x16x32_bf16 a[120:123], a[216:219], a[176:179], a[120:123]\n\t"
"ds_read_b128 a[176:179], v223 offset:13056\n\t"
"v_fma_f32 v193, v93, s54, -v193\n\t"
"v_mfma_f32_16x16x32_bf16 a[128:131], a[216:219], a[180:183], a[128:131]\n\t"
"ds_read_b128 a[180:183], v222 offset:20736\n\t"
"v_fma_f32 v194, v94, s54, -v194\n\t"
"v_mfma_f32_16x16x32_bf16 a[128:131], v[236:239], a[184:187], a[128:131]\n\t"
"v_fma_f32 v195, v95, s54, -v195\n\t"
"v_mfma_f32_16x16x32_bf16 a[128:131], v[96:99], a[188:191], a[128:131]\n\t"
"buffer_load_dwordx4 a[252:255], off, s[80:83], s20 offset:0 sc1\n\t"
"v_mfma_f32_16x16x32_bf16 a[136:139], v[96:99], a[192:195], a[136:139]\n\t"
"v_mfma_f32_16x16x32_bf16 a[136:139], v[236:239], a[188:191], a[136:139]\n\t"
"v_mfma_f32_16x16x32_bf16 a[136:139], a[216:219], a[184:187], a[136:139]\n\t"
"ds_read_b128 a[184:187], v223 offset:26880\n\t"
"v_mfma_f32_16x16x32_bf16 a[144:147], a[216:219], a[188:191], a[144:147]\n\t"
"ds_read_b128 a[188:191], v223 offset:33024\n\t"
"v_mfma_f32_16x16x32_bf16 a[144:147], v[236:239], a[192:195], a[144:147]\n\t"
"v_mfma_f32_16x16x32_bf16 a[144:147], v[96:99], a[196:199], a[144:147]\n\t"
"v_mfma_f32_16x16x32_bf16 a[152:155], v[96:99], a[200:203], a[152:155]\n\t"
"v_mfma_f32_16x16x32_bf16 a[152:155], v[236:239], a[196:199], a[152:155]\n\t"
"v_mfma_f32_16x16x32_bf16 a[152:155], a[216:219], a[192:195], a[152:155]\n\t"
"ds_read_b128 a[192:195], v222 offset:40704\n\t"
"v_mfma_f32_16x16x32_bf16 a[160:163], a[216:219], a[196:199], a[160:163]\n\t"
"buffer_load_dwordx4 a[216:219], off, s[80:83], s20 offset:1024 sc1\n\t"
"v_mfma_f32_16x16x32_bf16 a[160:163], v[236:239], a[200:203], a[160:163]\n\t"
"s_waitcnt vmcnt(11) lgkmcnt(4)\n\t"
"ds_read_b128 a[196:199], v223 offset:46848\n\t"
"v_mfma_f32_16x16x32_bf16 a[160:163], v[96:99], a[204:207], a[160:163]\n\t"
"ds_read_b128 a[200:203], v223 offset:52992\n\t"
"v_mfma_f32_16x16x32_bf16 v[200:203], v[248:251], a[168:171], v[200:203]\n\t"
"buffer_load_dwordx4 v[96:99], off, s[80:83], s20 offset:2048 sc1\n\t"
"v_mfma_f32_16x16x32_bf16 v[200:203], a[208:211], a[176:179], v[200:203]\n\t"
"s_waitcnt lgkmcnt(5)\n\t"
"ds_read_b128 a[204:207], v222 offset:60672\n\t"
"s_mov_b32 s20, 1609728\n\t"
"v_mfma_f32_16x16x32_bf16 v[200:203], v[240:243], a[172:175], v[200:203]\n\t"
"s_add_u32 m0, s24, 54272\n\t"
"s_add_u32 s28, s21, 1575936\n\t"
"v_mfma_f32_16x16x32_bf16 v[208:211], v[248:251], a[172:175], v[208:211]\n\t"
"buffer_load_dwordx4 v[236:239], off, s[80:83], s20 offset:0 sc1\n\t"
"v_mfma_f32_16x16x32_bf16 v[208:211], v[240:243], a[176:179], v[208:211]\n\t"
"s_waitcnt lgkmcnt(5)\n\t"
"v_mfma_f32_16x16x32_bf16 v[208:211], a[208:211], a[180:183], v[208:211]\n\t"
"v_mfma_f32_16x16x32_bf16 v[216:219], a[208:211], a[184:187], v[216:219]\n\t"
"buffer_load_dwordx4 v247, s[12:15], s28 offen offset:0 nt lds\n\t"
"v_mfma_f32_16x16x32_bf16 v[216:219], v[240:243], a[180:183], v[216:219]\n\t"
"s_waitcnt lgkmcnt(4)\n\t"
"s_add_u32 m0, s25, 512\n\t"
"v_mfma_f32_16x16x32_bf16 v[216:219], v[248:251], a[176:179], v[216:219]\n\t"
"buffer_load_dwordx4 v254, s[12:15], 0 offen offset:0 nt lds\n\t"
"v_mfma_f32_16x16x32_bf16 a[4:7], v[248:251], a[180:183], a[4:7]\n\t"
"s_waitcnt lgkmcnt(3)\n\t"
"s_add_u32 m0, s24, 2048\n\t"
"s_add_u32 s28, s21, 3072\n\t"
"v_mfma_f32_16x16x32_bf16 a[4:7], v[240:243], a[184:187], a[4:7]\n\t"
"buffer_load_dwordx4 v247, s[12:15], s28 offen offset:0 nt lds\n\t"
"v_mfma_f32_16x16x32_bf16 a[4:7], a[208:211], a[188:191], a[4:7]\n\t"
"s_add_u32 m0, s24, 8192\n\t"
"s_add_u32 s28, s21, 199680\n\t"
"v_mfma_f32_16x16x32_bf16 a[12:15], a[208:211], a[192:195], a[12:15]\n\t"
"buffer_load_dwordx4 v247, s[12:15], s28 offen offset:0 nt lds\n\t"
"v_mfma_f32_16x16x32_bf16 a[12:15], v[240:243], a[188:191], a[12:15]\n\t"
"s_waitcnt lgkmcnt(2)\n\t"
"s_add_u32 m0, s24, 14336\n\t"
"s_add_u32 s28, s21, 396288\n\t"
"v_mfma_f32_16x16x32_bf16 a[12:15], v[248:251], a[184:187], a[12:15]\n\t"
"buffer_load_dwordx4 v247, s[12:15], s28 offen offset:0 nt lds\n\t"
"v_mfma_f32_16x16x32_bf16 a[20:23], v[248:251], a[188:191], a[20:23]\n\t"
"s_waitcnt lgkmcnt(1)\n\t"
"v_mfma_f32_16x16x32_bf16 a[20:23], v[240:243], a[192:195], a[20:23]\n\t"
"v_mfma_f32_16x16x32_bf16 a[20:23], a[208:211], a[196:199], a[20:23]\n\t"
"v_mfma_f32_16x16x32_bf16 a[28:31], a[208:211], a[200:203], a[28:31]\n\t"
"s_waitcnt lgkmcnt(0)\n\t"
"v_mfma_f32_16x16x32_bf16 a[28:31], v[240:243], a[196:199], a[28:31]\n\t"
"v_mfma_f32_16x16x32_bf16 a[28:31], v[248:251], a[192:195], a[28:31]\n\t"
"v_mfma_f32_16x16x32_bf16 a[36:39], v[248:251], a[196:199], a[36:39]\n\t"
"s_waitcnt vmcnt(15)\n\t"
"v_mfma_f32_16x16x32_bf16 a[36:39], v[240:243], a[200:203], a[36:39]\n\t"
"v_mfma_f32_16x16x32_bf16 a[36:39], a[208:211], a[204:207], a[36:39]\n\t"
"v_mfma_f32_16x16x32_bf16 a[44:47], a[220:223], a[168:171], a[44:47]\n\t"
"v_mfma_f32_16x16x32_bf16 a[44:47], a[228:231], a[176:179], a[44:47]\n\t"
"v_mfma_f32_16x16x32_bf16 a[44:47], a[224:227], a[172:175], a[44:47]\n\t"
"v_mfma_f32_16x16x32_bf16 a[52:55], a[220:223], a[172:175], a[52:55]\n\t"
"v_mfma_f32_16x16x32_bf16 a[52:55], a[224:227], a[176:179], a[52:55]\n\t"
"v_mfma_f32_16x16x32_bf16 a[52:55], a[228:231], a[180:183], a[52:55]\n\t"
"v_mfma_f32_16x16x32_bf16 a[60:63], a[228:231], a[184:187], a[60:63]\n\t"
"v_mfma_f32_16x16x32_bf16 a[60:63], a[224:227], a[180:183], a[60:63]\n\t"
"v_mfma_f32_16x16x32_bf16 a[60:63], a[220:223], a[176:179], a[60:63]\n\t"
"v_mfma_f32_16x16x32_bf16 a[68:71], a[220:223], a[180:183], a[68:71]\n\t"
"v_mfma_f32_16x16x32_bf16 a[68:71], a[224:227], a[184:187], a[68:71]\n\t"
"v_mfma_f32_16x16x32_bf16 a[68:71], a[228:231], a[188:191], a[68:71]\n\t"
"v_mfma_f32_16x16x32_bf16 a[76:79], a[228:231], a[192:195], a[76:79]\n\t"
"v_mfma_f32_16x16x32_bf16 a[76:79], a[224:227], a[188:191], a[76:79]\n\t"
"v_mfma_f32_16x16x32_bf16 a[76:79], a[220:223], a[184:187], a[76:79]\n\t"
"v_mfma_f32_16x16x32_bf16 a[84:87], a[220:223], a[188:191], a[84:87]\n\t"
"v_mfma_f32_16x16x32_bf16 a[84:87], a[224:227], a[192:195], a[84:87]\n\t"
"v_mfma_f32_16x16x32_bf16 a[84:87], a[228:231], a[196:199], a[84:87]\n\t"
"v_mfma_f32_16x16x32_bf16 a[92:95], a[228:231], a[200:203], a[92:95]\n\t"
"v_mfma_f32_16x16x32_bf16 a[92:95], a[224:227], a[196:199], a[92:95]\n\t"
"v_mfma_f32_16x16x32_bf16 a[92:95], a[220:223], a[192:195], a[92:95]\n\t"
"buffer_load_dwordx4 v[248:251], off, s[80:83], s20 offset:1024 sc1\n\t"
"v_mfma_f32_16x16x32_bf16 a[100:103], a[220:223], a[196:199], a[100:103]\n\t"
"s_waitcnt vmcnt(13)\n\t"
"v_mfma_f32_16x16x32_bf16 a[100:103], a[224:227], a[200:203], a[100:103]\n\t"
"v_mfma_f32_16x16x32_bf16 a[100:103], a[228:231], a[204:207], a[100:103]\n\t"
"v_mfma_f32_16x16x32_bf16 a[108:111], a[232:235], a[168:171], a[108:111]\n\t"
"ds_read_b128 a[168:171], v222 offset:1024\n\t"
"v_mfma_f32_16x16x32_bf16 a[108:111], a[248:251], a[176:179], a[108:111]\n\t"
"v_mfma_f32_16x16x32_bf16 a[108:111], a[240:243], a[172:175], a[108:111]\n\t"
"v_mfma_f32_16x16x32_bf16 a[116:119], a[232:235], a[172:175], a[116:119]\n\t"
"ds_read_b128 a[172:175], v223 offset:7168\n\t"
"v_mfma_f32_16x16x32_bf16 a[116:119], a[240:243], a[176:179], a[116:119]\n\t"
"v_mfma_f32_16x16x32_bf16 a[116:119], a[248:251], a[180:183], a[116:119]\n\t"
"v_mfma_f32_16x16x32_bf16 a[124:127], a[248:251], a[184:187], a[124:127]\n\t"
"v_mfma_f32_16x16x32_bf16 a[124:127], a[240:243], a[180:183], a[124:127]\n\t"
"v_mfma_f32_16x16x32_bf16 a[124:127], a[232:235], a[176:179], a[124:127]\n\t"
"ds_read_b128 a[176:179], v223 offset:13312\n\t"
"v_mfma_f32_16x16x32_bf16 a[132:135], a[232:235], a[180:183], a[132:135]\n\t"
"ds_read_b128 a[180:183], v222 offset:20992\n\t"
"v_mfma_f32_16x16x32_bf16 a[132:135], a[240:243], a[184:187], a[132:135]\n\t"
"v_mfma_f32_16x16x32_bf16 a[132:135], a[248:251], a[188:191], a[132:135]\n\t"
"v_mfma_f32_16x16x32_bf16 a[140:143], a[248:251], a[192:195], a[140:143]\n\t"
"v_mfma_f32_16x16x32_bf16 a[140:143], a[240:243], a[188:191], a[140:143]\n\t"
"v_mfma_f32_16x16x32_bf16 a[140:143], a[232:235], a[184:187], a[140:143]\n\t"
"ds_read_b128 a[184:187], v223 offset:27136\n\t"
"v_mfma_f32_16x16x32_bf16 a[148:151], a[232:235], a[188:191], a[148:151]\n\t"
"ds_read_b128 a[188:191], v223 offset:33280\n\t"
"v_mfma_f32_16x16x32_bf16 a[148:151], a[240:243], a[192:195], a[148:151]\n\t"
"v_mfma_f32_16x16x32_bf16 a[148:151], a[248:251], a[196:199], a[148:151]\n\t"
"v_mfma_f32_16x16x32_bf16 a[156:159], a[248:251], a[200:203], a[156:159]\n\t"
"v_mfma_f32_16x16x32_bf16 a[156:159], a[240:243], a[196:199], a[156:159]\n\t"
"v_mfma_f32_16x16x32_bf16 a[156:159], a[232:235], a[192:195], a[156:159]\n\t"
"ds_read_b128 a[192:195], v222 offset:40960\n\t"
"v_mfma_f32_16x16x32_bf16 a[164:167], a[232:235], a[196:199], a[164:167]\n\t"
"s_waitcnt vmcnt(10) lgkmcnt(4)\n\t"
"ds_read_b128 a[196:199], v223 offset:47104\n\t"
"v_mfma_f32_16x16x32_bf16 a[164:167], a[240:243], a[200:203], a[164:167]\n\t"
"ds_read_b128 a[200:203], v223 offset:53248\n\t"
"v_mfma_f32_16x16x32_bf16 a[164:167], a[248:251], a[204:207], a[164:167]\n\t"
"ds_read_b128 a[204:207], v222 offset:60928\n\t"
"Lst13_%=:\n\t"
"v_mfma_f32_16x16x32_bf16 v[196:199], a[236:239], a[168:171], v[196:199]\n\t"
"s_waitcnt lgkmcnt(6)\n\t"
"v_mfma_f32_16x16x32_bf16 v[196:199], a[244:247], a[176:179], v[196:199]\n\t"
"v_mfma_f32_16x16x32_bf16 v[196:199], a[212:215], a[172:175], v[196:199]\n\t"
"v_mfma_f32_16x16x32_bf16 v[204:207], a[236:239], a[172:175], v[204:207]\n\t"
"s_waitcnt lgkmcnt(5)\n\t"
"v_mfma_f32_16x16x32_bf16 v[204:207], a[212:215], a[176:179], v[204:207]\n\t"
"v_mfma_f32_16x16x32_bf16 v[204:207], a[244:247], a[180:183], v[204:207]\n\t"
"v_mfma_f32_16x16x32_bf16 v[212:215], a[244:247], a[184:187], v[212:215]\n\t"
"s_waitcnt lgkmcnt(4)\n\t"
"v_mfma_f32_16x16x32_bf16 v[212:215], a[212:215], a[180:183], v[212:215]\n\t"
"buffer_load_dwordx4 v[240:243], off, s[80:83], s20 offset:2048 sc1\n\t"
"v_mfma_f32_16x16x32_bf16 v[212:215], a[236:239], a[176:179], v[212:215]\n\t"
"s_mov_b32 s20, 1622016\n\t"
"v_mfma_f32_16x16x32_bf16 a[0:3], a[236:239], a[180:183], a[0:3]\n\t"
"s_waitcnt lgkmcnt(3)\n\t"
"v_mfma_f32_16x16x32_bf16 a[0:3], a[212:215], a[184:187], a[0:3]\n\t"
"v_mfma_f32_16x16x32_bf16 a[0:3], a[244:247], a[188:191], a[0:3]\n\t"
"buffer_load_dwordx4 a[208:211], off, s[80:83], s20 offset:0 sc1\n\t"
"v_mfma_f32_16x16x32_bf16 a[8:11], a[244:247], a[192:195], a[8:11]\n\t"
"s_waitcnt lgkmcnt(2)\n\t"
"v_mfma_f32_16x16x32_bf16 a[8:11], a[212:215], a[188:191], a[8:11]\n\t"
"v_mfma_f32_16x16x32_bf16 a[8:11], a[236:239], a[184:187], a[8:11]\n\t"
"v_mfma_f32_16x16x32_bf16 a[16:19], a[236:239], a[188:191], a[16:19]\n\t"
"buffer_load_dwordx4 a[220:223], off, s[80:83], s20 offset:1024 sc1\n\t"
"v_mfma_f32_16x16x32_bf16 a[16:19], a[212:215], a[192:195], a[16:19]\n\t"
"s_waitcnt lgkmcnt(1)\n\t"
"v_mfma_f32_16x16x32_bf16 a[16:19], a[244:247], a[196:199], a[16:19]\n\t"
"v_mfma_f32_16x16x32_bf16 a[24:27], a[244:247], a[200:203], a[24:27]\n\t"
"s_waitcnt lgkmcnt(0)\n\t"
"v_mfma_f32_16x16x32_bf16 a[24:27], a[212:215], a[196:199], a[24:27]\n\t"
"buffer_load_dwordx4 a[224:227], off, s[80:83], s20 offset:2048 sc1\n\t"
"v_mfma_f32_16x16x32_bf16 a[24:27], a[236:239], a[192:195], a[24:27]\n\t"
"s_mov_b32 s20, 1634304\n\t"
"v_mfma_f32_16x16x32_bf16 a[32:35], a[236:239], a[196:199], a[32:35]\n\t"
"s_waitcnt vmcnt(11)\n\t"
"v_mfma_f32_16x16x32_bf16 a[32:35], a[212:215], a[200:203], a[32:35]\n\t"
"v_mfma_f32_16x16x32_bf16 a[32:35], a[244:247], a[204:207], a[32:35]\n\t"
"buffer_load_dwordx4 a[212:215], off, s[80:83], s20 offset:0 sc1\n\t"
"v_mfma_f32_16x16x32_bf16 a[40:43], a[252:255], a[168:171], a[40:43]\n\t"
"v_mfma_f32_16x16x32_bf16 a[40:43], v[96:99], a[176:179], a[40:43]\n\t"
"v_mfma_f32_16x16x32_bf16 a[40:43], a[216:219], a[172:175], a[40:43]\n\t"
"v_mfma_f32_16x16x32_bf16 a[48:51], a[252:255], a[172:175], a[48:51]\n\t"
"buffer_load_dwordx4 a[228:231], off, s[80:83], s20 offset:1024 sc1\n\t"
"v_mfma_f32_16x16x32_bf16 a[48:51], a[216:219], a[176:179], a[48:51]\n\t"
"v_mfma_f32_16x16x32_bf16 a[48:51], v[96:99], a[180:183], a[48:51]\n\t"
"v_mfma_f32_16x16x32_bf16 a[56:59], v[96:99], a[184:187], a[56:59]\n\t"
"buffer_load_dwordx4 a[232:235], off, s[80:83], s20 offset:2048 sc1\n\t"
"v_mfma_f32_16x16x32_bf16 a[56:59], a[216:219], a[180:183], a[56:59]\n\t"
"s_mov_b32 s20, 1646592\n\t"
"v_mfma_f32_16x16x32_bf16 a[56:59], a[252:255], a[176:179], a[56:59]\n\t"
"v_mfma_f32_16x16x32_bf16 a[64:67], a[252:255], a[180:183], a[64:67]\n\t"
"v_mfma_f32_16x16x32_bf16 a[64:67], a[216:219], a[184:187], a[64:67]\n\t"
"buffer_load_dwordx4 a[236:239], off, s[80:83], s20 offset:0 sc1\n\t"
"v_mfma_f32_16x16x32_bf16 a[64:67], v[96:99], a[188:191], a[64:67]\n\t"
"v_mfma_f32_16x16x32_bf16 a[72:75], v[96:99], a[192:195], a[72:75]\n\t"
"v_mfma_f32_16x16x32_bf16 a[72:75], a[216:219], a[188:191], a[72:75]\n\t"
"v_mfma_f32_16x16x32_bf16 a[72:75], a[252:255], a[184:187], a[72:75]\n\t"
"buffer_load_dwordx4 a[240:243], off, s[80:83], s20 offset:1024 sc1\n\t"
"v_mfma_f32_16x16x32_bf16 a[80:83], a[252:255], a[188:191], a[80:83]\n\t"
"v_mfma_f32_16x16x32_bf16 a[80:83], a[216:219], a[192:195], a[80:83]\n\t"
"v_mfma_f32_16x16x32_bf16 a[80:83], v[96:99], a[196:199], a[80:83]\n\t"
"v_mfma_f32_16x16x32_bf16 a[88:91], v[96:99], a[200:203], a[88:91]\n\t"
"buffer_load_dwordx4 a[244:247], off, s[80:83], s20 offset:2048 sc1\n\t"
"v_mfma_f32_16x16x32_bf16 a[88:91], a[216:219], a[196:199], a[88:91]\n\t"
"s_mov_b32 s20, 884736\n\t"
"v_mfma_f32_16x16x32_bf16 a[88:91], a[252:255], a[192:195], a[88:91]\n\t"
"v_mfma_f32_16x16x32_bf16 a[96:99], a[252:255], a[196:199], a[96:99]\n\t"
"s_waitcnt vmcnt(9)\n\t"
"v_mfma_f32_16x16x32_bf16 a[96:99], a[216:219], a[200:203], a[96:99]\n\t"
"buffer_load_dwordx4 a[216:219], off, s[80:83], s20 offset:0 sc1\n\t"
"v_mfma_f32_16x16x32_bf16 a[96:99], v[96:99], a[204:207], a[96:99]\n\t"
"v_mfma_f32_16x16x32_bf16 a[104:107], v[236:239], a[168:171], a[104:107]\n\t"
"ds_read_b128 a[168:171], v222 offset:1280\n\t"
"v_mfma_f32_16x16x32_bf16 a[104:107], v[240:243], a[176:179], a[104:107]\n\t"
"buffer_load_dwordx4 a[248:251], off, s[80:83], s20 offset:1024 sc1\n\t"
"v_mfma_f32_16x16x32_bf16 a[104:107], v[248:251], a[172:175], a[104:107]\n\t"
"v_mfma_f32_16x16x32_bf16 a[112:115], v[236:239], a[172:175], a[112:115]\n\t"
"ds_read_b128 a[172:175], v223 offset:7424\n\t"
"v_mfma_f32_16x16x32_bf16 a[112:115], v[248:251], a[176:179], a[112:115]\n\t"
"buffer_load_dwordx4 a[252:255], off, s[80:83], s20 offset:2048 sc1\n\t"
"v_mfma_f32_16x16x32_bf16 a[112:115], v[240:243], a[180:183], a[112:115]\n\t"
"v_mfma_f32_16x16x32_bf16 a[120:123], v[240:243], a[184:187], a[120:123]\n\t"
"v_mfma_f32_16x16x32_bf16 a[120:123], v[248:251], a[180:183], a[120:123]\n\t"
"v_mfma_f32_16x16x32_bf16 a[120:123], v[236:239], a[176:179], a[120:123]\n\t"
"ds_read_b128 a[176:179], v223 offset:13568\n\t"
"v_mfma_f32_16x16x32_bf16 a[128:131], v[236:239], a[180:183], a[128:131]\n\t"
"ds_read_b128 a[180:183], v222 offset:21248\n\t"
"v_mfma_f32_16x16x32_bf16 a[128:131], v[248:251], a[184:187], a[128:131]\n\t"
"v_mfma_f32_16x16x32_bf16 a[128:131], v[240:243], a[188:191], a[128:131]\n\t"
"v_mfma_f32_16x16x32_bf16 a[136:139], v[240:243], a[192:195], a[136:139]\n\t"
"v_mfma_f32_16x16x32_bf16 a[136:139], v[248:251], a[188:191], a[136:139]\n\t"
"v_mfma_f32_16x16x32_bf16 a[136:139], v[236:239], a[184:187], a[136:139]\n\t"
"ds_read_b128 a[184:187], v223 offset:27392\n\t"
"v_mfma_f32_16x16x32_bf16 a[144:147], v[236:239], a[188:191], a[144:147]\n\t"
"ds_read_b128 a[188:191], v223 offset:33536\n\t"
"v_mfma_f32_16x16x32_bf16 a[144:147], v[248:251], a[192:195], a[144:147]\n\t"
"v_mfma_f32_16x16x32_bf16 a[144:147], v[240:243], a[196:199], a[144:147]\n\t"
"s_mov_b32 s20, 897024\n\t"
"v_mfma_f32_16x16x32_bf16 a[152:155], v[240:243], a[200:203], a[152:155]\n\t"
"v_mfma_f32_16x16x32_bf16 a[152:155], v[248:251], a[196:199], a[152:155]\n\t"
"v_mfma_f32_16x16x32_bf16 a[152:155], v[236:239], a[192:195], a[152:155]\n\t"
"ds_read_b128 a[192:195], v222 offset:41216\n\t"
"v_mfma_f32_16x16x32_bf16 a[160:163], v[236:239], a[196:199], a[160:163]\n\t"
"s_waitcnt vmcnt(9) lgkmcnt(4)\n\t"
"ds_read_b128 a[196:199], v223 offset:47360\n\t"
"v_mfma_f32_16x16x32_bf16 a[160:163], v[248:251], a[200:203], a[160:163]\n\t"
"ds_read_b128 a[200:203], v223 offset:53504\n\t"
"v_mfma_f32_16x16x32_bf16 a[160:163], v[240:243], a[204:207], a[160:163]\n\t"
"ds_read_b128 a[204:207], v222 offset:61184\n\t"
"v_mfma_f32_16x16x32_bf16 v[200:203], a[208:211], a[168:171], v[200:203]\n\t"
"buffer_load_dwordx4 v[240:243], off, s[80:83], s20 offset:0 sc1\n\t"
"v_mfma_f32_16x16x32_bf16 v[200:203], a[224:227], a[176:179], v[200:203]\n\t"
"s_waitcnt lgkmcnt(6)\n\t"
"v_mfma_f32_16x16x32_bf16 v[200:203], a[220:223], a[172:175], v[200:203]\n\t"
"s_add_u32 m0, s24, 20480\n\t"
"s_add_u32 s28, s21, 589824\n\t"
"v_mfma_f32_16x16x32_bf16 v[208:211], a[208:211], a[172:175], v[208:211]\n\t"
"s_waitcnt lgkmcnt(5)\n\t"
"v_mfma_f32_16x16x32_bf16 v[208:211], a[220:223], a[176:179], v[208:211]\n\t"
"v_mfma_f32_16x16x32_bf16 v[208:211], a[224:227], a[180:183], v[208:211]\n\t"
"s_waitcnt lgkmcnt(0)\n\t"
"s_barrier\n\t"
"v_mfma_f32_16x16x32_bf16 v[216:219], a[224:227], a[184:187], v[216:219]\n\t"
"v_mfma_f32_16x16x32_bf16 v[216:219], a[220:223], a[180:183], v[216:219]\n\t"
"buffer_load_dwordx4 v247, s[12:15], s28 offen offset:0 nt lds\n\t"
"v_mfma_f32_16x16x32_bf16 v[216:219], a[208:211], a[176:179], v[216:219]\n\t"
"s_add_u32 m0, s24, 26624\n\t"
"s_add_u32 s28, s21, 786432\n\t"
"v_pk_fma_f32 v[196:197], v[200:201], s[56:57], v[196:197]\n\t"
"v_pk_fma_f32 v[200:201], v[196:197], s[58:59], v[200:201] neg_lo:[0,0,1] neg_hi:[0,0,1]\n\t"
"v_pk_fma_f32 v[228:229], v[0:1], s[60:61], v[196:197]\n\t"
"v_pk_fma_f32 v[0:1], v[196:197], s[60:61], v[0:1]\n\t"
"v_pk_fma_f32 v[196:197], v[100:101], s[62:63], v[200:201]\n\t"
"v_pk_fma_f32 v[100:101], v[200:201], s[62:63], v[100:101]\n\t"
"v_pk_fma_f32 v[198:199], v[202:203], s[56:57], v[198:199]\n\t"
"v_pk_fma_f32 v[202:203], v[198:199], s[58:59], v[202:203] neg_lo:[0,0,1] neg_hi:[0,0,1]\n\t"
"v_pk_fma_f32 v[234:235], v[2:3], s[60:61], v[198:199]\n\t"
"v_pk_fma_f32 v[2:3], v[198:199], s[60:61], v[2:3]\n\t"
"v_pk_fma_f32 v[198:199], v[102:103], s[62:63], v[202:203]\n\t"
"v_pk_fma_f32 v[102:103], v[202:203], s[62:63], v[102:103]\n\t"
"v_mfma_f32_16x16x32_bf16 a[4:7], a[208:211], a[180:183], a[4:7]\n\t"
"v_cvt_pk_bf16_f32 v196, v196, v197\n\t"
"v_cvt_pk_bf16_f32 v200, v228, v229\n\t"
"v_mfma_f32_16x16x32_bf16 a[4:7], a[220:223], a[184:187], a[4:7]\n\t"
"v_cvt_pk_bf16_f32 v197, v198, v199\n\t"
"v_cvt_pk_bf16_f32 v201, v234, v235\n\t"
"v_mfma_f32_16x16x32_bf16 a[4:7], a[224:227], a[188:191], a[4:7]\n\t"
"buffer_load_dwordx4 v247, s[12:15], s28 offen offset:0 nt lds\n\t"
"s_add_u32 m0, s24, 32768\n\t"
"s_add_u32 s28, s21, 983040\n\t"
"v_pk_fma_f32 v[204:205], v[208:209], s[56:57], v[204:205]\n\t"
"v_pk_fma_f32 v[208:209], v[204:205], s[58:59], v[208:209] neg_lo:[0,0,1] neg_hi:[0,0,1]\n\t"
"v_pk_fma_f32 v[98:99], v[4:5], s[60:61], v[204:205]\n\t"
"v_pk_fma_f32 v[4:5], v[204:205], s[60:61], v[4:5]\n\t"
"v_pk_fma_f32 v[204:205], v[104:105], s[62:63], v[208:209]\n\t"
"v_pk_fma_f32 v[104:105], v[208:209], s[62:63], v[104:105]\n\t"
"v_mfma_f32_16x16x32_bf16 a[12:15], a[224:227], a[192:195], a[12:15]\n\t"
"v_cvt_pk_bf16_f32 v204, v204, v205\n\t"
"v_cvt_pk_bf16_f32 v208, v98, v99\n\t"
"v_mfma_f32_16x16x32_bf16 a[12:15], a[220:223], a[188:191], a[12:15]\n\t"
"v_mfma_f32_16x16x32_bf16 a[12:15], a[208:211], a[184:187], a[12:15]\n\t"
"buffer_load_dwordx4 v247, s[12:15], s28 offen offset:0 nt lds\n\t"
"s_add_u32 m0, s24, 40448\n\t"
"s_add_u32 s28, s21, 1179648\n\t"
"v_pk_fma_f32 v[206:207], v[210:211], s[56:57], v[206:207]\n\t"
"v_pk_fma_f32 v[210:211], v[206:207], s[58:59], v[210:211] neg_lo:[0,0,1] neg_hi:[0,0,1]\n\t"
"v_pk_fma_f32 v[228:229], v[6:7], s[60:61], v[206:207]\n\t"
"v_pk_fma_f32 v[6:7], v[206:207], s[60:61], v[6:7]\n\t"
"v_pk_fma_f32 v[206:207], v[106:107], s[62:63], v[210:211]\n\t"
"v_pk_fma_f32 v[106:107], v[210:211], s[62:63], v[106:107]\n\t"
"v_mfma_f32_16x16x32_bf16 a[20:23], a[208:211], a[188:191], a[20:23]\n\t"
"v_cvt_pk_bf16_f32 v205, v206, v207\n\t"
"v_cvt_pk_bf16_f32 v209, v228, v229\n\t"
"v_mfma_f32_16x16x32_bf16 a[20:23], a[220:223], a[192:195], a[20:23]\n\t"
"v_accvgpr_read_b32 v224, a0\n\t"
"v_accvgpr_read_b32 v226, a4\n\t"
"v_mfma_f32_16x16x32_bf16 a[20:23], a[224:227], a[196:199], a[20:23]\n\t"
"buffer_load_dwordx4 v247, s[12:15], s28 offen offset:0 nt lds\n\t"
"v_accvgpr_read_b32 v225, a1\n\t"
"v_accvgpr_read_b32 v227, a5\n\t"
"s_add_u32 m0, s24, 46592\n\t"
"s_add_u32 s28, s21, 1376256\n\t"
"v_pk_fma_f32 v[212:213], v[216:217], s[56:57], v[212:213]\n\t"
"v_pk_fma_f32 v[216:217], v[212:213], s[58:59], v[216:217] neg_lo:[0,0,1] neg_hi:[0,0,1]\n\t"
"v_pk_fma_f32 v[234:235], v[8:9], s[60:61], v[212:213]\n\t"
"v_pk_fma_f32 v[8:9], v[212:213], s[60:61], v[8:9]\n\t"
"v_pk_fma_f32 v[212:213], v[108:109], s[62:63], v[216:217]\n\t"
"v_pk_fma_f32 v[108:109], v[216:217], s[62:63], v[108:109]\n\t"
"v_mfma_f32_16x16x32_bf16 a[28:31], a[224:227], a[200:203], a[28:31]\n\t"
"v_cvt_pk_bf16_f32 v212, v212, v213\n\t"
"v_cvt_pk_bf16_f32 v216, v234, v235\n\t"
"v_mfma_f32_16x16x32_bf16 a[28:31], a[220:223], a[196:199], a[28:31]\n\t"
"v_accvgpr_read_b32 v230, a2\n\t"
"v_accvgpr_read_b32 v232, a6\n\t"
"v_mfma_f32_16x16x32_bf16 a[28:31], a[208:211], a[192:195], a[28:31]\n\t"
"buffer_load_dwordx4 v247, s[12:15], s28 offen offset:0 nt lds\n\t"
"v_accvgpr_read_b32 v231, a3\n\t"
"v_accvgpr_read_b32 v233, a7\n\t"
"v_pk_fma_f32 v[214:215], v[218:219], s[56:57], v[214:215]\n\t"
"v_pk_fma_f32 v[218:219], v[214:215], s[58:59], v[218:219] neg_lo:[0,0,1] neg_hi:[0,0,1]\n\t"
"v_pk_fma_f32 v[98:99], v[10:11], s[60:61], v[214:215]\n\t"
"v_pk_fma_f32 v[10:11], v[214:215], s[60:61], v[10:11]\n\t"
"v_pk_fma_f32 v[214:215], v[110:111], s[62:63], v[218:219]\n\t"
"v_pk_fma_f32 v[110:111], v[218:219], s[62:63], v[110:111]\n\t"
"v_mfma_f32_16x16x32_bf16 a[36:39], a[208:211], a[196:199], a[36:39]\n\t"
"s_waitcnt vmcnt(12)\n\t"
"v_cvt_pk_bf16_f32 v213, v214, v215\n\t"
"v_cvt_pk_bf16_f32 v217, v98, v99\n\t"
"v_mfma_f32_16x16x32_bf16 a[36:39], a[220:223], a[200:203], a[36:39]\n\t"
"v_accvgpr_read_b32 v236, a8\n\t"
"v_accvgpr_read_b32 v238, a12\n\t"
"v_mfma_f32_16x16x32_bf16 a[36:39], a[224:227], a[204:207], a[36:39]\n\t"
"buffer_load_dwordx4 a[208:211], off, s[80:83], s20 offset:1024 sc1\n\t"
"v_accvgpr_read_b32 v237, a9\n\t"
"v_accvgpr_read_b32 v239, a13\n\t"
"v_pk_fma_f32 v[224:225], v[226:227], s[56:57], v[224:225]\n\t"
"v_pk_fma_f32 v[226:227], v[224:225], s[58:59], v[226:227] neg_lo:[0,0,1] neg_hi:[0,0,1]\n\t"
"v_pk_fma_f32 v[228:229], v[12:13], s[60:61], v[224:225]\n\t"
"v_pk_fma_f32 v[12:13], v[224:225], s[60:61], v[12:13]\n\t"
"v_pk_fma_f32 v[224:225], v[112:113], s[62:63], v[226:227]\n\t"
"v_pk_fma_f32 v[112:113], v[226:227], s[62:63], v[112:113]\n\t"
"v_mfma_f32_16x16x32_bf16 a[44:47], a[212:215], a[168:171], a[44:47]\n\t"
"v_cvt_pk_bf16_f32 v224, v224, v225\n\t"
"v_cvt_pk_bf16_f32 v228, v228, v229\n\t"
"v_mfma_f32_16x16x32_bf16 a[44:47], a[232:235], a[176:179], a[44:47]\n\t"
"v_accvgpr_write_b32 a0, v224\n\t"
"v_accvgpr_write_b32 a4, v228\n\t"
"v_mfma_f32_16x16x32_bf16 a[44:47], a[228:231], a[172:175], a[44:47]\n\t"
"v_accvgpr_read_b32 v224, a10\n\t"
"v_accvgpr_read_b32 v226, a14\n\t"
"v_accvgpr_read_b32 v225, a11\n\t"
"v_accvgpr_read_b32 v227, a15\n\t"
"v_pk_fma_f32 v[230:231], v[232:233], s[56:57], v[230:231]\n\t"
"v_pk_fma_f32 v[232:233], v[230:231], s[58:59], v[232:233] neg_lo:[0,0,1] neg_hi:[0,0,1]\n\t"
"v_pk_fma_f32 v[234:235], v[14:15], s[60:61], v[230:231]\n\t"
"v_pk_fma_f32 v[14:15], v[230:231], s[60:61], v[14:15]\n\t"
"v_pk_fma_f32 v[230:231], v[114:115], s[62:63], v[232:233]\n\t"
"v_pk_fma_f32 v[114:115], v[232:233], s[62:63], v[114:115]\n\t"
"v_mfma_f32_16x16x32_bf16 a[52:55], a[212:215], a[172:175], a[52:55]\n\t"
"v_cvt_pk_bf16_f32 v230, v230, v231\n\t"
"v_cvt_pk_bf16_f32 v234, v234, v235\n\t"
"v_mfma_f32_16x16x32_bf16 a[52:55], a[228:231], a[176:179], a[52:55]\n\t"
"v_accvgpr_write_b32 a1, v230\n\t"
"v_accvgpr_write_b32 a5, v234\n\t"
"v_mfma_f32_16x16x32_bf16 a[52:55], a[232:235], a[180:183], a[52:55]\n\t"
"v_accvgpr_read_b32 v230, a16\n\t"
"v_accvgpr_read_b32 v232, a20\n\t"
"v_accvgpr_read_b32 v231, a17\n\t"
"v_accvgpr_read_b32 v233, a21\n\t"
"v_pk_fma_f32 v[236:237], v[238:239], s[56:57], v[236:237]\n\t"
"v_pk_fma_f32 v[238:239], v[236:237], s[58:59], v[238:239] neg_lo:[0,0,1] neg_hi:[0,0,1]\n\t"
"v_pk_fma_f32 v[98:99], v[16:17], s[60:61], v[236:237]\n\t"
"v_pk_fma_f32 v[16:17], v[236:237], s[60:61], v[16:17]\n\t"
"v_pk_fma_f32 v[236:237], v[116:117], s[62:63], v[238:239]\n\t"
"v_pk_fma_f32 v[116:117], v[238:239], s[62:63], v[116:117]\n\t"
"v_mfma_f32_16x16x32_bf16 a[60:63], a[232:235], a[184:187], a[60:63]\n\t"
"v_cvt_pk_bf16_f32 v236, v236, v237\n\t"
"v_cvt_pk_bf16_f32 v98, v98, v99\n\t"
"v_mfma_f32_16x16x32_bf16 a[60:63], a[228:231], a[180:183], a[60:63]\n\t"
"v_accvgpr_write_b32 a8, v236\n\t"
"v_accvgpr_write_b32 a12, v98\n\t"
"v_mfma_f32_16x16x32_bf16 a[60:63], a[212:215], a[176:179], a[60:63]\n\t"
"buffer_load_dwordx4 a[220:223], off, s[80:83], s20 offset:2048 sc1\n\t"
"v_accvgpr_read_b32 v236, a18\n\t"
"v_accvgpr_read_b32 v238, a22\n\t"
"v_pk_fma_f32 v[224:225], v[226:227], s[56:57], v[224:225]\n\t"
"v_pk_fma_f32 v[226:227], v[224:225], s[58:59], v[226:227] neg_lo:[0,0,1] neg_hi:[0,0,1]\n\t"
"v_pk_fma_f32 v[228:229], v[18:19], s[60:61], v[224:225]\n\t"
"v_pk_fma_f32 v[18:19], v[224:225], s[60:61], v[18:19]\n\t"
"v_pk_fma_f32 v[224:225], v[118:119], s[62:63], v[226:227]\n\t"
"v_pk_fma_f32 v[118:119], v[226:227], s[62:63], v[118:119]\n\t"
"v_pk_fma_f32 v[230:231], v[232:233], s[56:57], v[230:231]\n\t"
"v_pk_fma_f32 v[232:233], v[230:231], s[58:59], v[232:233] neg_lo:[0,0,1] neg_hi:[0,0,1]\n\t"
"v_pk_fma_f32 v[234:235], v[20:21], s[60:61], v[230:231]\n\t"
"v_pk_fma_f32 v[20:21], v[230:231], s[60:61], v[20:21]\n\t"
"v_pk_fma_f32 v[230:231], v[120:121], s[62:63], v[232:233]\n\t"
"v_pk_fma_f32 v[120:121], v[232:233], s[62:63], v[120:121]\n\t"
"v_mfma_f32_16x16x32_bf16 a[68:71], a[212:215], a[180:183], a[68:71]\n\t"
"s_mov_b32 s20, 909312\n\t"
"v_accvgpr_read_b32 v237, a19\n\t"
"v_accvgpr_read_b32 v239, a23\n\t"
"v_mfma_f32_16x16x32_bf16 a[68:71], a[228:231], a[184:187], a[68:71]\n\t"
"v_cvt_pk_bf16_f32 v224, v224, v225\n\t"
"v_cvt_pk_bf16_f32 v228, v228, v229\n\t"
"v_mfma_f32_16x16x32_bf16 a[68:71], a[232:235], a[188:191], a[68:71]\n\t"
"buffer_load_dwordx4 a[224:227], off, s[80:83], s20 offset:0 sc1\n\t"
"v_cvt_pk_bf16_f32 v230, v230, v231\n\t"
"v_cvt_pk_bf16_f32 v234, v234, v235\n\t"
"v_pk_fma_f32 v[236:237], v[238:239], s[56:57], v[236:237]\n\t"
"v_pk_fma_f32 v[238:239], v[236:237], s[58:59], v[238:239] neg_lo:[0,0,1] neg_hi:[0,0,1]\n\t"
"v_pk_fma_f32 v[98:99], v[22:23], s[60:61], v[236:237]\n\t"
"v_pk_fma_f32 v[22:23], v[236:237], s[60:61], v[22:23]\n\t"
"v_pk_fma_f32 v[236:237], v[122:123], s[62:63], v[238:239]\n\t"
"v_pk_fma_f32 v[122:123], v[238:239], s[62:63], v[122:123]\n\t"
"v_mfma_f32_16x16x32_bf16 a[76:79], a[232:235], a[192:195], a[76:79]\n\t"
"v_accvgpr_write_b32 a9, v224\n\t"
"v_accvgpr_write_b32 a13, v228\n\t"
"v_mfma_f32_16x16x32_bf16 a[76:79], a[228:231], a[188:191], a[76:79]\n\t"
"v_accvgpr_write_b32 a16, v230\n\t"
"v_accvgpr_write_b32 a20, v234\n\t"
"v_mfma_f32_16x16x32_bf16 a[76:79], a[212:215], a[184:187], a[76:79]\n\t"
"v_accvgpr_read_b32 v224, a24\n\t"
"v_accvgpr_read_b32 v226, a28\n\t"
"v_accvgpr_read_b32 v225, a25\n\t"
"v_accvgpr_read_b32 v227, a29\n\t"
"v_cvt_pk_bf16_f32 v236, v236, v237\n\t"
"v_cvt_pk_bf16_f32 v98, v98, v99\n\t"
"v_pk_fma_f32 v[224:225], v[226:227], s[56:57], v[224:225]\n\t"
"v_pk_fma_f32 v[226:227], v[224:225], s[58:59], v[226:227] neg_lo:[0,0,1] neg_hi:[0,0,1]\n\t"
"v_pk_fma_f32 v[228:229], v[24:25], s[60:61], v[224:225]\n\t"
"v_pk_fma_f32 v[24:25], v[224:225], s[60:61], v[24:25]\n\t"
"v_pk_fma_f32 v[224:225], v[124:125], s[62:63], v[226:227]\n\t"
"v_pk_fma_f32 v[124:125], v[226:227], s[62:63], v[124:125]\n\t"
"v_mfma_f32_16x16x32_bf16 a[84:87], a[212:215], a[188:191], a[84:87]\n\t"
"v_accvgpr_write_b32 a17, v236\n\t"
"v_accvgpr_write_b32 a21, v98\n\t"
"v_mfma_f32_16x16x32_bf16 a[84:87], a[228:231], a[192:195], a[84:87]\n\t"
"v_accvgpr_read_b32 v230, a26\n\t"
"v_accvgpr_read_b32 v232, a30\n\t"
"v_mfma_f32_16x16x32_bf16 a[84:87], a[232:235], a[196:199], a[84:87]\n\t"
"v_accvgpr_read_b32 v231, a27\n\t"
"v_accvgpr_read_b32 v233, a31\n\t"
"v_cvt_pk_bf16_f32 v224, v224, v225\n\t"
"v_cvt_pk_bf16_f32 v228, v228, v229\n\t"
"v_accvgpr_write_b32 a24, v224\n\t"
"v_pk_fma_f32 v[230:231], v[232:233], s[56:57], v[230:231]\n\t"
"v_pk_fma_f32 v[232:233], v[230:231], s[58:59], v[232:233] neg_lo:[0,0,1] neg_hi:[0,0,1]\n\t"
"v_pk_fma_f32 v[234:235], v[26:27], s[60:61], v[230:231]\n\t"
"v_pk_fma_f32 v[26:27], v[230:231], s[60:61], v[26:27]\n\t"
"v_pk_fma_f32 v[230:231], v[126:127], s[62:63], v[232:233]\n\t"
"v_pk_fma_f32 v[126:127], v[232:233], s[62:63], v[126:127]\n\t"
"v_mfma_f32_16x16x32_bf16 a[92:95], a[232:235], a[200:203], a[92:95]\n\t"
"v_accvgpr_write_b32 a28, v228\n\t"
"v_accvgpr_read_b32 v236, a32\n\t"
"v_mfma_f32_16x16x32_bf16 a[92:95], a[228:231], a[196:199], a[92:95]\n\t"
"v_accvgpr_read_b32 v238, a36\n\t"
"v_accvgpr_read_b32 v237, a33\n\t"
"v_mfma_f32_16x16x32_bf16 a[92:95], a[212:215], a[192:195], a[92:95]\n\t"
"v_accvgpr_read_b32 v239, a37\n\t"
"v_cvt_pk_bf16_f32 v230, v230, v231\n\t"
"v_cvt_pk_bf16_f32 v234, v234, v235\n\t"
"v_accvgpr_write_b32 a25, v230\n\t"
"v_accvgpr_write_b32 a29, v234\n\t"
"v_pk_fma_f32 v[236:237], v[238:239], s[56:57], v[236:237]\n\t"
"v_pk_fma_f32 v[238:239], v[236:237], s[58:59], v[238:239] neg_lo:[0,0,1] neg_hi:[0,0,1]\n\t"
"v_pk_fma_f32 v[98:99], v[28:29], s[60:61], v[236:237]\n\t"
"v_pk_fma_f32 v[28:29], v[236:237], s[60:61], v[28:29]\n\t"
"v_pk_fma_f32 v[236:237], v[128:129], s[62:63], v[238:239]\n\t"
"v_pk_fma_f32 v[128:129], v[238:239], s[62:63], v[128:129]\n\t"
"v_mfma_f32_16x16x32_bf16 a[100:103], a[212:215], a[196:199], a[100:103]\n\t"
"s_waitcnt vmcnt(12)\n\t"
"v_accvgpr_read_b32 v224, a34\n\t"
"v_accvgpr_read_b32 v226, a38\n\t"
"v_mfma_f32_16x16x32_bf16 a[100:103], a[228:231], a[200:203], a[100:103]\n\t"
"v_accvgpr_read_b32 v225, a35\n\t"
"v_accvgpr_read_b32 v227, a39\n\t"
"v_mfma_f32_16x16x32_bf16 a[100:103], a[232:235], a[204:207], a[100:103]\n\t"
"buffer_load_dwordx4 a[212:215], off, s[80:83], s20 offset:1024 sc1\n\t"
"v_cvt_pk_bf16_f32 v236, v236, v237\n\t"
"v_cvt_pk_bf16_f32 v98, v98, v99\n\t"
"v_pk_fma_f32 v[224:225], v[226:227], s[56:57], v[224:225]\n\t"
"v_pk_fma_f32 v[226:227], v[224:225], s[58:59], v[226:227] neg_lo:[0,0,1] neg_hi:[0,0,1]\n\t"
"v_pk_fma_f32 v[228:229], v[30:31], s[60:61], v[224:225]\n\t"
"v_pk_fma_f32 v[30:31], v[224:225], s[60:61], v[30:31]\n\t"
"v_pk_fma_f32 v[224:225], v[130:131], s[62:63], v[226:227]\n\t"
"v_pk_fma_f32 v[130:131], v[226:227], s[62:63], v[130:131]\n\t"
"v_mfma_f32_16x16x32_bf16 a[108:111], a[236:239], a[168:171], a[108:111]\n\t"
"Lpg10_%=:\n\t"
"s_cbranch_vccnz Lvr0_%=\n\t"
"ds_read_b128 a[168:171], v244 offset:0\n\t"
"v_accvgpr_write_b32 a32, v236\n\t"
"v_mfma_f32_16x16x32_bf16 a[108:111], a[244:247], a[176:179], a[108:111]\n\t"
"v_accvgpr_write_b32 a36, v98\n\t"
"v_accvgpr_read_b32 v230, a40\n\t"
"v_mfma_f32_16x16x32_bf16 a[108:111], a[240:243], a[172:175], a[108:111]\n\t"
"buffer_load_dwordx4 a[228:231], off, s[80:83], s20 offset:2048 sc1\n\t"
"v_accvgpr_read_b32 v232, a44\n\t"
"v_accvgpr_read_b32 v231, a41\n\t"
"v_accvgpr_read_b32 v233, a45\n\t"
"v_cvt_pk_bf16_f32 v224, v224, v225\n\t"
"v_cvt_pk_bf16_f32 v228, v228, v229\n\t"
"s_mov_b32 s20, 1069056\n\t"
"v_pk_fma_f32 v[230:231], v[232:233], s[56:57], v[230:231]\n\t"
"v_pk_fma_f32 v[232:233], v[230:231], s[58:59], v[232:233] neg_lo:[0,0,1] neg_hi:[0,0,1]\n\t"
"v_pk_fma_f32 v[234:235], v[32:33], s[60:61], v[230:231]\n\t"
"v_pk_fma_f32 v[32:33], v[230:231], s[60:61], v[32:33]\n\t"
"v_pk_fma_f32 v[230:231], v[132:133], s[62:63], v[232:233]\n\t"
"v_pk_fma_f32 v[132:133], v[232:233], s[62:63], v[132:133]\n\t"
"v_mfma_f32_16x16x32_bf16 a[116:119], a[236:239], a[172:175], a[116:119]\n\t"
"ds_read_b128 a[172:175], v244 offset:512\n\t"
"v_accvgpr_write_b32 a33, v224\n\t"
"v_mfma_f32_16x16x32_bf16 a[116:119], a[240:243], a[176:179], a[116:119]\n\t"
"s_add_u32 m0, s24, 52736\n\t"
"s_add_u32 s28, s21, 1572864\n\t"
"v_accvgpr_write_b32 a37, v228\n\t"
"v_mfma_f32_16x16x32_bf16 a[116:119], a[244:247], a[180:183], a[116:119]\n\t"
"buffer_load_dwordx4 a[232:235], off, s[80:83], s20 offset:0 sc1\n\t"
"v_accvgpr_read_b32 v236, a42\n\t"
"v_accvgpr_read_b32 v238, a46\n\t"
"v_accvgpr_read_b32 v237, a43\n\t"
"v_accvgpr_read_b32 v239, a47\n\t"
"v_pk_fma_f32 v[236:237], v[238:239], s[56:57], v[236:237]\n\t"
"v_pk_fma_f32 v[238:239], v[236:237], s[58:59], v[238:239] neg_lo:[0,0,1] neg_hi:[0,0,1]\n\t"
"v_pk_fma_f32 v[98:99], v[34:35], s[60:61], v[236:237]\n\t"
"v_pk_fma_f32 v[34:35], v[236:237], s[60:61], v[34:35]\n\t"
"v_pk_fma_f32 v[236:237], v[134:135], s[62:63], v[238:239]\n\t"
"v_pk_fma_f32 v[134:135], v[238:239], s[62:63], v[134:135]\n\t"
"v_mfma_f32_16x16x32_bf16 a[124:127], a[244:247], a[184:187], a[124:127]\n\t"
"v_cvt_pk_bf16_f32 v198, v230, v231\n\t"
"v_cvt_pk_bf16_f32 v202, v234, v235\n\t"
"v_mfma_f32_16x16x32_bf16 a[124:127], a[240:243], a[180:183], a[124:127]\n\t"
"v_accvgpr_read_b32 v224, a48\n\t"
"v_accvgpr_read_b32 v226, a52\n\t"
"v_mfma_f32_16x16x32_bf16 a[124:127], a[236:239], a[176:179], a[124:127]\n\t"
"buffer_load_dwordx4 v247, s[12:15], s28 offen offset:0 nt lds\n\t"
"ds_read_b128 a[176:179], v244 offset:1024\n\t"
"v_accvgpr_read_b32 v225, a49\n\t"
"v_accvgpr_read_b32 v227, a53\n\t"
"v_accvgpr_read_b32 v230, a50\n\t"
"v_accvgpr_read_b32 v232, a54\n\t"
"v_accvgpr_read_b32 v231, a51\n\t"
"v_accvgpr_read_b32 v233, a55\n\t"
"v_pk_fma_f32 v[224:225], v[226:227], s[56:57], v[224:225]\n\t"
"v_pk_fma_f32 v[226:227], v[224:225], s[58:59], v[226:227] neg_lo:[0,0,1] neg_hi:[0,0,1]\n\t"
"v_pk_fma_f32 v[228:229], v[36:37], s[60:61], v[224:225]\n\t"
"v_pk_fma_f32 v[36:37], v[224:225], s[60:61], v[36:37]\n\t"
"v_pk_fma_f32 v[224:225], v[136:137], s[62:63], v[226:227]\n\t"
"v_pk_fma_f32 v[136:137], v[226:227], s[62:63], v[136:137]\n\t"
"v_pk_fma_f32 v[230:231], v[232:233], s[56:57], v[230:231]\n\t"
"v_pk_fma_f32 v[232:233], v[230:231], s[58:59], v[232:233] neg_lo:[0,0,1] neg_hi:[0,0,1]\n\t"
"v_pk_fma_f32 v[234:235], v[38:39], s[60:61], v[230:231]\n\t"
"v_pk_fma_f32 v[38:39], v[230:231], s[60:61], v[38:39]\n\t"
"v_pk_fma_f32 v[230:231], v[138:139], s[62:63], v[232:233]\n\t"
"v_pk_fma_f32 v[138:139], v[232:233], s[62:63], v[138:139]\n\t"
"v_mfma_f32_16x16x32_bf16 a[132:135], a[236:239], a[180:183], a[132:135]\n\t"
"s_add_u32 m0, s24, 512\n\t"
"s_add_u32 s28, s21, 0\n\t"
"ds_read_b128 a[180:183], v244 offset:1536\n\t"
"v_mfma_f32_16x16x32_bf16 a[132:135], a[240:243], a[184:187], a[132:135]\n\t"
"buffer_load_dwordx4 a[40:43], off, s[80:83], s20 offset:1024 sc1\n\t"
"v_mfma_f32_16x16x32_bf16 a[132:135], a[244:247], a[188:191], a[132:135]\n\t"
"buffer_load_dwordx4 v247, s[12:15], s28 offen offset:0 nt lds\n\t"
"v_cvt_pk_bf16_f32 v199, v236, v237\n\t"
"v_cvt_pk_bf16_f32 v203, v98, v99\n\t"
"v_accvgpr_read_b32 v236, a56\n\t"
"v_accvgpr_read_b32 v238, a60\n\t"
"v_accvgpr_read_b32 v237, a57\n\t"
"v_accvgpr_read_b32 v239, a61\n\t"
"s_add_u32 m0, s24, 6656\n\t"
"s_add_u32 s28, s21, 196608\n\t"
"v_pk_fma_f32 v[236:237], v[238:239], s[56:57], v[236:237]\n\t"
"v_pk_fma_f32 v[238:239], v[236:237], s[58:59], v[238:239] neg_lo:[0,0,1] neg_hi:[0,0,1]\n\t"
"v_pk_fma_f32 v[98:99], v[40:41], s[60:61], v[236:237]\n\t"
"v_pk_fma_f32 v[40:41], v[236:237], s[60:61], v[40:41]\n\t"
"v_pk_fma_f32 v[236:237], v[140:141], s[62:63], v[238:239]\n\t"
"v_pk_fma_f32 v[140:141], v[238:239], s[62:63], v[140:141]\n\t"
"v_mfma_f32_16x16x32_bf16 a[140:143], a[244:247], a[192:195], a[140:143]\n\t"
"v_cvt_pk_bf16_f32 v206, v224, v225\n\t"
"v_cvt_pk_bf16_f32 v210, v228, v229\n\t"
"v_mfma_f32_16x16x32_bf16 a[140:143], a[240:243], a[188:191], a[140:143]\n\t"
"v_cvt_pk_bf16_f32 v207, v230, v231\n\t"
"v_cvt_pk_bf16_f32 v211, v234, v235\n\t"
"v_mfma_f32_16x16x32_bf16 a[140:143], a[236:239], a[184:187], a[140:143]\n\t"
"buffer_load_dwordx4 v247, s[12:15], s28 offen offset:0 nt lds\n\t"
"ds_read_b128 a[184:187], v244 offset:2048\n\t"
"v_accvgpr_read_b32 v224, a58\n\t"
"v_accvgpr_read_b32 v226, a62\n\t"
"v_accvgpr_read_b32 v225, a59\n\t"
"v_accvgpr_read_b32 v227, a63\n\t"
"s_add_u32 m0, s24, 12800\n\t"
"s_add_u32 s28, s21, 393216\n\t"
"v_pk_fma_f32 v[224:225], v[226:227], s[56:57], v[224:225]\n\t"
"v_pk_fma_f32 v[226:227], v[224:225], s[58:59], v[226:227] neg_lo:[0,0,1] neg_hi:[0,0,1]\n\t"
"v_pk_fma_f32 v[228:229], v[42:43], s[60:61], v[224:225]\n\t"
"v_pk_fma_f32 v[42:43], v[224:225], s[60:61], v[42:43]\n\t"
"v_pk_fma_f32 v[224:225], v[142:143], s[62:63], v[226:227]\n\t"
"v_pk_fma_f32 v[142:143], v[226:227], s[62:63], v[142:143]\n\t"
"v_mfma_f32_16x16x32_bf16 a[148:151], a[236:239], a[188:191], a[148:151]\n\t"
"ds_read_b128 a[188:191], v244 offset:2560\n\t"
"v_cvt_pk_bf16_f32 v214, v236, v237\n\t"
"v_mfma_f32_16x16x32_bf16 a[148:151], a[240:243], a[192:195], a[148:151]\n\t"
"v_cvt_pk_bf16_f32 v218, v98, v99\n\t"
"v_accvgpr_read_b32 v230, a64\n\t"
"v_mfma_f32_16x16x32_bf16 a[148:151], a[244:247], a[196:199], a[148:151]\n\t"
"buffer_load_dwordx4 v247, s[12:15], s28 offen offset:0 nt lds\n\t"
"v_accvgpr_read_b32 v232, a68\n\t"
"v_accvgpr_read_b32 v231, a65\n\t"
"v_accvgpr_read_b32 v233, a69\n\t"
"v_pk_fma_f32 v[230:231], v[232:233], s[56:57], v[230:231]\n\t"
"v_pk_fma_f32 v[232:233], v[230:231], s[58:59], v[232:233] neg_lo:[0,0,1] neg_hi:[0,0,1]\n\t"
"v_pk_fma_f32 v[234:235], v[44:45], s[60:61], v[230:231]\n\t"
"v_pk_fma_f32 v[44:45], v[230:231], s[60:61], v[44:45]\n\t"
"v_pk_fma_f32 v[230:231], v[144:145], s[62:63], v[232:233]\n\t"
"v_pk_fma_f32 v[144:145], v[232:233], s[62:63], v[144:145]\n\t"
"v_mfma_f32_16x16x32_bf16 a[156:159], a[244:247], a[200:203], a[156:159]\n\t"
"v_cvt_pk_bf16_f32 v215, v224, v225\n\t"
"v_cvt_pk_bf16_f32 v219, v228, v229\n\t"
"v_mfma_f32_16x16x32_bf16 a[156:159], a[240:243], a[196:199], a[156:159]\n\t"
"v_accvgpr_read_b32 v236, a66\n\t"
"v_accvgpr_read_b32 v238, a70\n\t"
"v_mfma_f32_16x16x32_bf16 a[156:159], a[236:239], a[192:195], a[156:159]\n\t"
"buffer_load_dwordx4 a[48:51], off, s[80:83], s20 offset:2048 sc1\n\t"
"ds_read_b128 a[192:195], v244 offset:3072\n\t"
"v_accvgpr_read_b32 v237, a67\n\t"
"v_accvgpr_read_b32 v239, a71\n\t"
"v_cvt_pk_bf16_f32 v230, v230, v231\n\t"
"v_cvt_pk_bf16_f32 v234, v234, v235\n\t"
"v_pk_fma_f32 v[236:237], v[238:239], s[56:57], v[236:237]\n\t"
"v_pk_fma_f32 v[238:239], v[236:237], s[58:59], v[238:239] neg_lo:[0,0,1] neg_hi:[0,0,1]\n\t"
"v_pk_fma_f32 v[98:99], v[46:47], s[60:61], v[236:237]\n\t"
"v_pk_fma_f32 v[46:47], v[236:237], s[60:61], v[46:47]\n\t"
"v_pk_fma_f32 v[236:237], v[146:147], s[62:63], v[238:239]\n\t"
"v_pk_fma_f32 v[146:147], v[238:239], s[62:63], v[146:147]\n\t"
"v_mfma_f32_16x16x32_bf16 a[164:167], a[236:239], a[196:199], a[164:167]\n\t"
"s_waitcnt vmcnt(18) lgkmcnt(4)\n\t"
"ds_read_b128 a[196:199], v244 offset:3584\n\t"
"v_accvgpr_write_b32 a2, v230\n\t"
"v_mfma_f32_16x16x32_bf16 a[164:167], a[240:243], a[200:203], a[164:167]\n\t"
"ds_read_b128 a[200:203], v244 offset:4096\n\t"
"v_accvgpr_write_b32 a6, v234\n\t"
"v_mfma_f32_16x16x32_bf16 a[164:167], a[244:247], a[204:207], a[164:167]\n\t"
"ds_read_b128 a[204:207], v244 offset:4608\n\t"
"v_accvgpr_read_b32 v224, a72\n\t"
"v_accvgpr_read_b32 v226, a76\n\t"
"v_accvgpr_read_b32 v225, a73\n\t"
"v_accvgpr_read_b32 v227, a77\n\t"
"v_cvt_pk_bf16_f32 v236, v236, v237\n\t"
"v_cvt_pk_bf16_f32 v98, v98, v99\n\t"
"v_pk_fma_f32 v[224:225], v[226:227], s[56:57], v[224:225]\n\t"
"v_pk_fma_f32 v[226:227], v[224:225], s[58:59], v[226:227] neg_lo:[0,0,1] neg_hi:[0,0,1]\n\t"
"v_pk_fma_f32 v[228:229], v[48:49], s[60:61], v[224:225]\n\t"
"v_pk_fma_f32 v[48:49], v[224:225], s[60:61], v[48:49]\n\t"
"v_pk_fma_f32 v[224:225], v[148:149], s[62:63], v[226:227]\n\t"
"v_pk_fma_f32 v[148:149], v[226:227], s[62:63], v[148:149]\n\t"
"Lst14_%=:\n\t"
"v_mfma_f32_16x16x32_bf16 v[0:3], a[216:219], a[168:171], v[0:3]\n\t"
"s_waitcnt lgkmcnt(6)\n\t"
"v_accvgpr_write_b32 a3, v236\n\t"
"v_accvgpr_write_b32 a7, v98\n\t"
"v_mfma_f32_16x16x32_bf16 v[0:3], a[252:255], a[176:179], v[0:3]\n\t"
"v_accvgpr_read_b32 v230, a74\n\t"
"v_accvgpr_read_b32 v232, a78\n\t"
"v_mfma_f32_16x16x32_bf16 v[0:3], a[248:251], a[172:175], v[0:3]\n\t"
"s_mov_b32 s20, 1081344\n\t"
"v_accvgpr_read_b32 v231, a75\n\t"
"v_accvgpr_read_b32 v233, a79\n\t"
"v_cvt_pk_bf16_f32 v224, v224, v225\n\t"
"v_cvt_pk_bf16_f32 v228, v228, v229\n\t"
"v_accvgpr_write_b32 a10, v224\n\t"
"v_pk_fma_f32 v[230:231], v[232:233], s[56:57], v[230:231]\n\t"
"v_pk_fma_f32 v[232:233], v[230:231], s[58:59], v[232:233] neg_lo:[0,0,1] neg_hi:[0,0,1]\n\t"
"v_pk_fma_f32 v[234:235], v[50:51], s[60:61], v[230:231]\n\t"
"v_pk_fma_f32 v[50:51], v[230:231], s[60:61], v[50:51]\n\t"
"v_pk_fma_f32 v[230:231], v[150:151], s[62:63], v[232:233]\n\t"
"v_pk_fma_f32 v[150:151], v[232:233], s[62:63], v[150:151]\n\t"
"v_mfma_f32_16x16x32_bf16 v[4:7], a[216:219], a[172:175], v[4:7]\n\t"
"s_waitcnt lgkmcnt(5)\n\t"
"v_accvgpr_write_b32 a14, v228\n\t"
"v_accvgpr_read_b32 v236, a80\n\t"
"v_mfma_f32_16x16x32_bf16 v[4:7], a[248:251], a[176:179], v[4:7]\n\t"
"v_accvgpr_read_b32 v238, a84\n\t"
"v_accvgpr_read_b32 v237, a81\n\t"
"v_mfma_f32_16x16x32_bf16 v[4:7], a[252:255], a[180:183], v[4:7]\n\t"
"v_accvgpr_read_b32 v239, a85\n\t"
"v_cvt_pk_bf16_f32 v230, v230, v231\n\t"
"v_cvt_pk_bf16_f32 v234, v234, v235\n\t"
"v_accvgpr_write_b32 a11, v230\n\t"
"v_accvgpr_write_b32 a15, v234\n\t"
"v_pk_fma_f32 v[236:237], v[238:239], s[56:57], v[236:237]\n\t"
"v_pk_fma_f32 v[238:239], v[236:237], s[58:59], v[238:239] neg_lo:[0,0,1] neg_hi:[0,0,1]\n\t"
"v_pk_fma_f32 v[98:99], v[52:53], s[60:61], v[236:237]\n\t"
"v_pk_fma_f32 v[52:53], v[236:237], s[60:61], v[52:53]\n\t"
"v_pk_fma_f32 v[236:237], v[152:153], s[62:63], v[238:239]\n\t"
"v_pk_fma_f32 v[152:153], v[238:239], s[62:63], v[152:153]\n\t"
"v_mfma_f32_16x16x32_bf16 v[8:11], a[252:255], a[184:187], v[8:11]\n\t"
"s_waitcnt lgkmcnt(4)\n\t"
"v_accvgpr_read_b32 v224, a82\n\t"
"v_accvgpr_read_b32 v226, a86\n\t"
"v_mfma_f32_16x16x32_bf16 v[8:11], a[248:251], a[180:183], v[8:11]\n\t"
"v_accvgpr_read_b32 v225, a83\n\t"
"v_accvgpr_read_b32 v227, a87\n\t"
"v_mfma_f32_16x16x32_bf16 v[8:11], a[216:219], a[176:179], v[8:11]\n\t"
"buffer_load_dwordx4 a[56:59], off, s[80:83], s20 offset:0 sc1\n\t"
"v_cvt_pk_bf16_f32 v236, v236, v237\n\t"
"v_cvt_pk_bf16_f32 v98, v98, v99\n\t"
"v_pk_fma_f32 v[224:225], v[226:227], s[56:57], v[224:225]\n\t"
"v_pk_fma_f32 v[226:227], v[224:225], s[58:59], v[226:227] neg_lo:[0,0,1] neg_hi:[0,0,1]\n\t"
"v_pk_fma_f32 v[228:229], v[54:55], s[60:61], v[224:225]\n\t"
"v_pk_fma_f32 v[54:55], v[224:225], s[60:61], v[54:55]\n\t"
"v_pk_fma_f32 v[224:225], v[154:155], s[62:63], v[226:227]\n\t"
"v_pk_fma_f32 v[154:155], v[226:227], s[62:63], v[154:155]\n\t"
"v_mfma_f32_16x16x32_bf16 v[12:15], a[216:219], a[180:183], v[12:15]\n\t"
"s_waitcnt lgkmcnt(3)\n\t"
"v_accvgpr_write_b32 a18, v236\n\t"
"v_accvgpr_write_b32 a22, v98\n\t"
"v_mfma_f32_16x16x32_bf16 v[12:15], a[248:251], a[184:187], v[12:15]\n\t"
"v_accvgpr_read_b32 v230, a88\n\t"
"v_accvgpr_read_b32 v232, a92\n\t"
"v_mfma_f32_16x16x32_bf16 v[12:15], a[252:255], a[188:191], v[12:15]\n\t"
"buffer_load_dwordx4 a[64:67], off, s[80:83], s20 offset:1024 sc1\n\t"
"v_accvgpr_read_b32 v231, a89\n\t"
"v_accvgpr_read_b32 v233, a93\n\t"
"v_accvgpr_read_b32 v236, a90\n\t"
"v_accvgpr_read_b32 v238, a94\n\t"
"v_accvgpr_read_b32 v237, a91\n\t"
"v_accvgpr_read_b32 v239, a95\n\t"
"v_cvt_pk_bf16_f32 v224, v224, v225\n\t"
"v_cvt_pk_bf16_f32 v228, v228, v229\n\t"
"v_pk_fma_f32 v[230:231], v[232:233], s[56:57], v[230:231]\n\t"
"v_pk_fma_f32 v[232:233], v[230:231], s[58:59], v[232:233] neg_lo:[0,0,1] neg_hi:[0,0,1]\n\t"
"v_pk_fma_f32 v[234:235], v[56:57], s[60:61], v[230:231]\n\t"
"v_pk_fma_f32 v[56:57], v[230:231], s[60:61], v[56:57]\n\t"
"v_pk_fma_f32 v[230:231], v[156:157], s[62:63], v[232:233]\n\t"
"v_pk_fma_f32 v[156:157], v[232:233], s[62:63], v[156:157]\n\t"
"v_pk_fma_f32 v[236:237], v[238:239], s[56:57], v[236:237]\n\t"
"v_pk_fma_f32 v[238:239], v[236:237], s[58:59], v[238:239] neg_lo:[0,0,1] neg_hi:[0,0,1]\n\t"
"v_pk_fma_f32 v[98:99], v[58:59], s[60:61], v[236:237]\n\t"
"v_pk_fma_f32 v[58:59], v[236:237], s[60:61], v[58:59]\n\t"
"v_pk_fma_f32 v[236:237], v[158:159], s[62:63], v[238:239]\n\t"
"v_pk_fma_f32 v[158:159], v[238:239], s[62:63], v[158:159]\n\t"
"v_mfma_f32_16x16x32_bf16 v[16:19], a[252:255], a[192:195], v[16:19]\n\t"
"s_waitcnt lgkmcnt(2)\n\t"
"v_accvgpr_write_b32 a19, v224\n\t"
"v_accvgpr_write_b32 a23, v228\n\t"
"v_mfma_f32_16x16x32_bf16 v[16:19], a[248:251], a[188:191], v[16:19]\n\t"
"v_accvgpr_read_b32 v224, a96\n\t"
"v_accvgpr_read_b32 v226, a100\n\t"
"v_mfma_f32_16x16x32_bf16 v[16:19], a[216:219], a[184:187], v[16:19]\n\t"
"buffer_load_dwordx4 a[72:75], off, s[80:83], s20 offset:2048 sc1\n\t"
"v_accvgpr_read_b32 v225, a97\n\t"
"v_accvgpr_read_b32 v227, a101\n\t"
"v_cvt_pk_bf16_f32 v230, v230, v231\n\t"
"v_cvt_pk_bf16_f32 v234, v234, v235\n\t"
"v_pk_fma_f32 v[224:225], v[226:227], s[56:57], v[224:225]\n\t"
"v_pk_fma_f32 v[226:227], v[224:225], s[58:59], v[226:227] neg_lo:[0,0,1] neg_hi:[0,0,1]\n\t"
"v_pk_fma_f32 v[228:229], v[60:61], s[60:61], v[224:225]\n\t"
"v_pk_fma_f32 v[60:61], v[224:225], s[60:61], v[60:61]\n\t"
"v_pk_fma_f32 v[224:225], v[160:161], s[62:63], v[226:227]\n\t"
"v_pk_fma_f32 v[160:161], v[226:227], s[62:63], v[160:161]\n\t"
"v_mfma_f32_16x16x32_bf16 v[20:23], a[216:219], a[188:191], v[20:23]\n\t"
"s_waitcnt lgkmcnt(1)\n\t"
"s_mov_b32 s20, 1093632\n\t"
"v_accvgpr_write_b32 a26, v230\n\t"
"v_mfma_f32_16x16x32_bf16 v[20:23], a[248:251], a[192:195], v[20:23]\n\t"
"v_cvt_pk_bf16_f32 v236, v236, v237\n\t"
"v_accvgpr_write_b32 a30, v234\n\t"
"v_mfma_f32_16x16x32_bf16 v[20:23], a[252:255], a[196:199], v[20:23]\n\t"
"buffer_load_dwordx4 a[80:83], off, s[80:83], s20 offset:0 sc1\n\t"
"v_accvgpr_read_b32 v230, a98\n\t"
"v_accvgpr_read_b32 v232, a102\n\t"
"v_accvgpr_read_b32 v231, a99\n\t"
"v_accvgpr_read_b32 v233, a103\n\t"
"v_cvt_pk_bf16_f32 v98, v98, v99\n\t"
"v_accvgpr_write_b32 a27, v236\n\t"
"v_accvgpr_write_b32 a31, v98\n\t"
"v_cvt_pk_bf16_f32 v224, v224, v225\n\t"
"v_cvt_pk_bf16_f32 v228, v228, v229\n\t"
"v_pk_fma_f32 v[230:231], v[232:233], s[56:57], v[230:231]\n\t"
"v_pk_fma_f32 v[232:233], v[230:231], s[58:59], v[232:233] neg_lo:[0,0,1] neg_hi:[0,0,1]\n\t"
"v_pk_fma_f32 v[234:235], v[62:63], s[60:61], v[230:231]\n\t"
"v_pk_fma_f32 v[62:63], v[230:231], s[60:61], v[62:63]\n\t"
"v_pk_fma_f32 v[230:231], v[162:163], s[62:63], v[232:233]\n\t"
"v_pk_fma_f32 v[162:163], v[232:233], s[62:63], v[162:163]\n\t"
"v_mfma_f32_16x16x32_bf16 v[24:27], a[252:255], a[200:203], v[24:27]\n\t"
"s_waitcnt lgkmcnt(0)\n\t"
"v_accvgpr_write_b32 a34, v224\n\t"
"v_accvgpr_write_b32 a38, v228\n\t"
"v_mfma_f32_16x16x32_bf16 v[24:27], a[248:251], a[196:199], v[24:27]\n\t"
"v_accvgpr_read_b32 v236, a104\n\t"
"v_accvgpr_read_b32 v238, a108\n\t"
"v_mfma_f32_16x16x32_bf16 v[24:27], a[216:219], a[192:195], v[24:27]\n\t"
"buffer_load_dwordx4 a[88:91], off, s[80:83], s20 offset:1024 sc1\n\t"
"v_accvgpr_read_b32 v237, a105\n\t"
"v_accvgpr_read_b32 v239, a109\n\t"
"v_cvt_pk_bf16_f32 v230, v230, v231\n\t"
"v_cvt_pk_bf16_f32 v234, v234, v235\n\t"
"v_pk_fma_f32 v[236:237], v[238:239], s[56:57], v[236:237]\n\t"
"v_pk_fma_f32 v[238:239], v[236:237], s[58:59], v[238:239] neg_lo:[0,0,1] neg_hi:[0,0,1]\n\t"
"v_pk_fma_f32 v[98:99], v[64:65], s[60:61], v[236:237]\n\t"
"v_pk_fma_f32 v[64:65], v[236:237], s[60:61], v[64:65]\n\t"
"v_pk_fma_f32 v[236:237], v[164:165], s[62:63], v[238:239]\n\t"
"v_pk_fma_f32 v[164:165], v[238:239], s[62:63], v[164:165]\n\t"
"v_mfma_f32_16x16x32_bf16 v[28:31], a[216:219], a[196:199], v[28:31]\n\t"
"s_waitcnt vmcnt(15)\n\t"
"v_accvgpr_write_b32 a35, v230\n\t"
"v_accvgpr_write_b32 a39, v234\n\t"
"v_mfma_f32_16x16x32_bf16 v[28:31], a[248:251], a[200:203], v[28:31]\n\t"
"v_accvgpr_read_b32 v224, a106\n\t"
"v_accvgpr_read_b32 v226, a110\n\t"
"v_mfma_f32_16x16x32_bf16 v[28:31], a[252:255], a[204:207], v[28:31]\n\t"
"buffer_load_dwordx4 a[96:99], off, s[80:83], s20 offset:2048 sc1\n\t"
"v_accvgpr_read_b32 v225, a107\n\t"
"v_accvgpr_read_b32 v227, a111\n\t"
"v_cvt_pk_bf16_f32 v236, v236, v237\n\t"
"v_cvt_pk_bf16_f32 v98, v98, v99\n\t"
"v_pk_fma_f32 v[224:225], v[226:227], s[56:57], v[224:225]\n\t"
"v_pk_fma_f32 v[226:227], v[224:225], s[58:59], v[226:227] neg_lo:[0,0,1] neg_hi:[0,0,1]\n\t"
"v_pk_fma_f32 v[228:229], v[66:67], s[60:61], v[224:225]\n\t"
"v_pk_fma_f32 v[66:67], v[224:225], s[60:61], v[66:67]\n\t"
"v_pk_fma_f32 v[224:225], v[166:167], s[62:63], v[226:227]\n\t"
"v_pk_fma_f32 v[166:167], v[226:227], s[62:63], v[166:167]\n\t"
"v_mfma_f32_16x16x32_bf16 v[32:35], v[240:243], a[168:171], v[32:35]\n\t"
"s_mov_b32 s20, 589824\n\t"
"v_accvgpr_write_b32 a104, v236\n\t"
"v_accvgpr_write_b32 a106, v98\n\t"
"v_mfma_f32_16x16x32_bf16 v[32:35], a[220:223], a[176:179], v[32:35]\n\t"
"v_accvgpr_read_b32 v230, a112\n\t"
"v_accvgpr_read_b32 v232, a116\n\t"
"v_mfma_f32_16x16x32_bf16 v[32:35], a[208:211], a[172:175], v[32:35]\n\t"
"buffer_load_dwordx4 a[108:111], off, s[80:83], s20 offset:0 sc1\n\t"
"v_accvgpr_read_b32 v231, a113\n\t"
"v_accvgpr_read_b32 v233, a117\n\t"
"v_cvt_pk_bf16_f32 v224, v224, v225\n\t"
"v_cvt_pk_bf16_f32 v228, v228, v229\n\t"
"v_pk_fma_f32 v[230:231], v[232:233], s[56:57], v[230:231]\n\t"
"v_pk_fma_f32 v[232:233], v[230:231], s[58:59], v[232:233] neg_lo:[0,0,1] neg_hi:[0,0,1]\n\t"
"v_pk_fma_f32 v[234:235], v[68:69], s[60:61], v[230:231]\n\t"
"v_pk_fma_f32 v[68:69], v[230:231], s[60:61], v[68:69]\n\t"
"v_pk_fma_f32 v[230:231], v[168:169], s[62:63], v[232:233]\n\t"
"v_pk_fma_f32 v[168:169], v[232:233], s[62:63], v[168:169]\n\t"
"v_mfma_f32_16x16x32_bf16 v[36:39], v[240:243], a[172:175], v[36:39]\n\t"
"v_accvgpr_write_b32 a105, v224\n\t"
"v_accvgpr_write_b32 a107, v228\n\t"
"v_mfma_f32_16x16x32_bf16 v[36:39], a[208:211], a[176:179], v[36:39]\n\t"
"v_accvgpr_read_b32 v236, a114\n\t"
"v_accvgpr_read_b32 v238, a118\n\t"
"v_mfma_f32_16x16x32_bf16 v[36:39], a[220:223], a[180:183], v[36:39]\n\t"
"buffer_load_dwordx4 a[60:63], off, s[80:83], s20 offset:1024 sc1\n\t"
"v_accvgpr_read_b32 v237, a115\n\t"
"v_accvgpr_read_b32 v239, a119\n\t"
"v_cvt_pk_bf16_f32 v230, v230, v231\n\t"
"v_cvt_pk_bf16_f32 v234, v234, v235\n\t"
"v_pk_fma_f32 v[236:237], v[238:239], s[56:57], v[236:237]\n\t"
"v_pk_fma_f32 v[238:239], v[236:237], s[58:59], v[238:239] neg_lo:[0,0,1] neg_hi:[0,0,1]\n\t"
"v_pk_fma_f32 v[98:99], v[70:71], s[60:61], v[236:237]\n\t"
"v_pk_fma_f32 v[70:71], v[236:237], s[60:61], v[70:71]\n\t"
"v_pk_fma_f32 v[236:237], v[170:171], s[62:63], v[238:239]\n\t"
"v_pk_fma_f32 v[170:171], v[238:239], s[62:63], v[170:171]\n\t"
"v_mfma_f32_16x16x32_bf16 v[40:43], a[220:223], a[184:187], v[40:43]\n\t"
"v_accvgpr_write_b32 a112, v230\n\t"
"v_accvgpr_write_b32 a114, v234\n\t"
"v_mfma_f32_16x16x32_bf16 v[40:43], a[208:211], a[180:183], v[40:43]\n\t"
"v_accvgpr_read_b32 v224, a120\n\t"
"v_accvgpr_read_b32 v226, a124\n\t"
"v_mfma_f32_16x16x32_bf16 v[40:43], v[240:243], a[176:179], v[40:43]\n\t"
"global_load_dwordx4 v[248:251], v252, s[48:49] offset:0\n\t"
"v_accvgpr_read_b32 v225, a121\n\t"
"v_accvgpr_read_b32 v227, a125\n\t"
"v_cvt_pk_bf16_f32 v236, v236, v237\n\t"
"v_cvt_pk_bf16_f32 v98, v98, v99\n\t"
"v_pk_fma_f32 v[224:225], v[226:227], s[56:57], v[224:225]\n\t"
"v_pk_fma_f32 v[226:227], v[224:225], s[58:59], v[226:227] neg_lo:[0,0,1] neg_hi:[0,0,1]\n\t"
"v_pk_fma_f32 v[228:229], v[72:73], s[60:61], v[224:225]\n\t"
"v_pk_fma_f32 v[72:73], v[224:225], s[60:61], v[72:73]\n\t"
"v_pk_fma_f32 v[224:225], v[172:173], s[62:63], v[226:227]\n\t"
"v_pk_fma_f32 v[172:173], v[226:227], s[62:63], v[172:173]\n\t"
"v_mfma_f32_16x16x32_bf16 v[44:47], v[240:243], a[180:183], v[44:47]\n\t"
"v_accvgpr_write_b32 a113, v236\n\t"
"v_accvgpr_write_b32 a115, v98\n\t"
"v_mfma_f32_16x16x32_bf16 v[44:47], a[208:211], a[184:187], v[44:47]\n\t"
"v_accvgpr_read_b32 v230, a122\n\t"
"v_accvgpr_read_b32 v232, a126\n\t"
"v_mfma_f32_16x16x32_bf16 v[44:47], a[220:223], a[188:191], v[44:47]\n\t"
"buffer_load_dwordx4 a[116:119], off, s[80:83], s20 offset:2048 sc1\n\t"
"v_accvgpr_read_b32 v231, a123\n\t"
"v_accvgpr_read_b32 v233, a127\n\t"
"v_accvgpr_read_b32 v236, a128\n\t"
"v_accvgpr_read_b32 v238, a132\n\t"
"v_accvgpr_read_b32 v237, a129\n\t"
"v_accvgpr_read_b32 v239, a133\n\t"
"v_cvt_pk_bf16_f32 v224, v224, v225\n\t"
"v_cvt_pk_bf16_f32 v228, v228, v229\n\t"
"s_mov_b32 s20, 602112\n\t"
"v_pk_fma_f32 v[230:231], v[232:233], s[56:57], v[230:231]\n\t"
"v_pk_fma_f32 v[232:233], v[230:231], s[58:59], v[232:233] neg_lo:[0,0,1] neg_hi:[0,0,1]\n\t"
"v_pk_fma_f32 v[234:235], v[74:75], s[60:61], v[230:231]\n\t"
"v_pk_fma_f32 v[74:75], v[230:231], s[60:61], v[74:75]\n\t"
"v_pk_fma_f32 v[230:231], v[174:175], s[62:63], v[232:233]\n\t"
"v_pk_fma_f32 v[174:175], v[232:233], s[62:63], v[174:175]\n\t"
"v_pk_fma_f32 v[236:237], v[238:239], s[56:57], v[236:237]\n\t"
"v_pk_fma_f32 v[238:239], v[236:237], s[58:59], v[238:239] neg_lo:[0,0,1] neg_hi:[0,0,1]\n\t"
"v_pk_fma_f32 v[98:99], v[76:77], s[60:61], v[236:237]\n\t"
"v_pk_fma_f32 v[76:77], v[236:237], s[60:61], v[76:77]\n\t"
"v_pk_fma_f32 v[236:237], v[176:177], s[62:63], v[238:239]\n\t"
"v_pk_fma_f32 v[176:177], v[238:239], s[62:63], v[176:177]\n\t"
"v_mfma_f32_16x16x32_bf16 v[48:51], a[220:223], a[192:195], v[48:51]\n\t"
"v_accvgpr_write_b32 a120, v224\n\t"
"v_accvgpr_write_b32 a122, v228\n\t"
"v_mfma_f32_16x16x32_bf16 v[48:51], a[208:211], a[188:191], v[48:51]\n\t"
"v_accvgpr_read_b32 v224, a130\n\t"
"v_accvgpr_read_b32 v226, a134\n\t"
"v_mfma_f32_16x16x32_bf16 v[48:51], v[240:243], a[184:187], v[48:51]\n\t"
"buffer_load_dwordx4 a[100:103], off, s[80:83], s20 offset:0 sc1\n\t"
"v_accvgpr_read_b32 v225, a131\n\t"
"v_accvgpr_read_b32 v227, a135\n\t"
"v_cvt_pk_bf16_f32 v230, v230, v231\n\t"
"v_cvt_pk_bf16_f32 v234, v234, v235\n\t"
"v_cvt_pk_bf16_f32 v236, v236, v237\n\t"
"v_cvt_pk_bf16_f32 v98, v98, v99\n\t"
"v_pk_fma_f32 v[224:225], v[226:227], s[56:57], v[224:225]\n\t"
"v_pk_fma_f32 v[226:227], v[224:225], s[58:59], v[226:227] neg_lo:[0,0,1] neg_hi:[0,0,1]\n\t"
"v_pk_fma_f32 v[228:229], v[78:79], s[60:61], v[224:225]\n\t"
"v_pk_fma_f32 v[78:79], v[224:225], s[60:61], v[78:79]\n\t"
"v_pk_fma_f32 v[224:225], v[178:179], s[62:63], v[226:227]\n\t"
"v_pk_fma_f32 v[178:179], v[226:227], s[62:63], v[178:179]\n\t"
"v_mfma_f32_16x16x32_bf16 v[52:55], v[240:243], a[188:191], v[52:55]\n\t"
"v_accvgpr_write_b32 a121, v230\n\t"
"v_accvgpr_write_b32 a123, v234\n\t"
"v_mfma_f32_16x16x32_bf16 v[52:55], a[208:211], a[192:195], v[52:55]\n\t"
"v_accvgpr_write_b32 a128, v236\n\t"
"v_accvgpr_write_b32 a130, v98\n\t"
"v_mfma_f32_16x16x32_bf16 v[52:55], a[220:223], a[196:199], v[52:55]\n\t"
"buffer_load_dwordx4 a[124:127], off, s[80:83], s20 offset:1024 sc1\n\t"
"v_accvgpr_read_b32 v230, a136\n\t"
"v_accvgpr_read_b32 v232, a140\n\t"
"v_accvgpr_read_b32 v231, a137\n\t"
"v_accvgpr_read_b32 v233, a141\n\t"
"v_cvt_pk_bf16_f32 v224, v224, v225\n\t"
"v_cvt_pk_bf16_f32 v228, v228, v229\n\t"
"v_pk_fma_f32 v[230:231], v[232:233], s[56:57], v[230:231]\n\t"
"v_pk_fma_f32 v[232:233], v[230:231], s[58:59], v[232:233] neg_lo:[0,0,1] neg_hi:[0,0,1]\n\t"
"v_pk_fma_f32 v[234:235], v[80:81], s[60:61], v[230:231]\n\t"
"v_pk_fma_f32 v[80:81], v[230:231], s[60:61], v[80:81]\n\t"
"v_pk_fma_f32 v[230:231], v[180:181], s[62:63], v[232:233]\n\t"
"v_pk_fma_f32 v[180:181], v[232:233], s[62:63], v[180:181]\n\t"
"v_mfma_f32_16x16x32_bf16 v[56:59], a[220:223], a[200:203], v[56:59]\n\t"
"v_accvgpr_write_b32 a129, v224\n\t"
"v_accvgpr_write_b32 a131, v228\n\t"
"v_mfma_f32_16x16x32_bf16 v[56:59], a[208:211], a[196:199], v[56:59]\n\t"
"v_accvgpr_read_b32 v236, a138\n\t"
"v_accvgpr_read_b32 v238, a142\n\t"
"v_mfma_f32_16x16x32_bf16 v[56:59], v[240:243], a[192:195], v[56:59]\n\t"
"buffer_load_dwordx4 a[132:135], off, s[80:83], s20 offset:2048 sc1\n\t"
"v_accvgpr_read_b32 v237, a139\n\t"
"v_accvgpr_read_b32 v239, a143\n\t"
"v_cvt_pk_bf16_f32 v230, v230, v231\n\t"
"v_cvt_pk_bf16_f32 v234, v234, v235\n\t"
"s_mov_b32 s20, 614400\n\t"
"v_pk_fma_f32 v[236:237], v[238:239], s[56:57], v[236:237]\n\t"
"v_pk_fma_f32 v[238:239], v[236:237], s[58:59], v[238:239] neg_lo:[0,0,1] neg_hi:[0,0,1]\n\t"
"v_pk_fma_f32 v[98:99], v[82:83], s[60:61], v[236:237]\n\t"
"v_pk_fma_f32 v[82:83], v[236:237], s[60:61], v[82:83]\n\t"
"v_pk_fma_f32 v[236:237], v[182:183], s[62:63], v[238:239]\n\t"
"v_pk_fma_f32 v[182:183], v[238:239], s[62:63], v[182:183]\n\t"
"v_mfma_f32_16x16x32_bf16 v[60:63], v[240:243], a[196:199], v[60:63]\n\t"
"s_waitcnt vmcnt(20)\n\t"
"v_accvgpr_write_b32 a136, v230\n\t"
"v_accvgpr_write_b32 a138, v234\n\t"
"v_mfma_f32_16x16x32_bf16 v[60:63], a[208:211], a[200:203], v[60:63]\n\t"
"v_accvgpr_read_b32 v224, a144\n\t"
"v_accvgpr_read_b32 v226, a148\n\t"
"v_mfma_f32_16x16x32_bf16 v[60:63], a[220:223], a[204:207], v[60:63]\n\t"
"buffer_load_dwordx4 a[44:47], off, s[80:83], s20 offset:0 sc1\n\t"
"v_accvgpr_read_b32 v225, a145\n\t"
"v_accvgpr_read_b32 v227, a149\n\t"
"v_cvt_pk_bf16_f32 v236, v236, v237\n\t"
"v_cvt_pk_bf16_f32 v98, v98, v99\n\t"
"v_pk_fma_f32 v[224:225], v[226:227], s[56:57], v[224:225]\n\t"
"v_pk_fma_f32 v[226:227], v[224:225], s[58:59], v[226:227] neg_lo:[0,0,1] neg_hi:[0,0,1]\n\t"
"v_pk_fma_f32 v[228:229], v[84:85], s[60:61], v[224:225]\n\t"
"v_pk_fma_f32 v[84:85], v[224:225], s[60:61], v[84:85]\n\t"
"v_pk_fma_f32 v[224:225], v[184:185], s[62:63], v[226:227]\n\t"
"v_pk_fma_f32 v[184:185], v[226:227], s[62:63], v[184:185]\n\t"
"v_mfma_f32_16x16x32_bf16 v[64:67], a[224:227], a[168:171], v[64:67]\n\t"
"ds_read_b128 a[168:171], v244 offset:9216\n\t"
"v_accvgpr_write_b32 a137, v236\n\t"
"v_mfma_f32_16x16x32_bf16 v[64:67], a[228:231], a[176:179], v[64:67]\n\t"
"v_accvgpr_write_b32 a139, v98\n\t"
"v_accvgpr_read_b32 v230, a146\n\t"
"v_mfma_f32_16x16x32_bf16 v[64:67], a[212:215], a[172:175], v[64:67]\n\t"
"buffer_load_dwordx4 v[240:243], off, s[80:83], s20 offset:1024 sc1\n\t"
"v_accvgpr_read_b32 v232, a150\n\t"
"v_accvgpr_read_b32 v231, a147\n\t"
"v_accvgpr_read_b32 v233, a151\n\t"
"v_cvt_pk_bf16_f32 v224, v224, v225\n\t"
"v_cvt_pk_bf16_f32 v228, v228, v229\n\t"
"v_pk_fma_f32 v[230:231], v[232:233], s[56:57], v[230:231]\n\t"
"v_pk_fma_f32 v[232:233], v[230:231], s[58:59], v[232:233] neg_lo:[0,0,1] neg_hi:[0,0,1]\n\t"
"v_pk_fma_f32 v[234:235], v[86:87], s[60:61], v[230:231]\n\t"
"v_pk_fma_f32 v[86:87], v[230:231], s[60:61], v[86:87]\n\t"
"v_pk_fma_f32 v[230:231], v[186:187], s[62:63], v[232:233]\n\t"
"v_pk_fma_f32 v[186:187], v[232:233], s[62:63], v[186:187]\n\t"
"v_mfma_f32_16x16x32_bf16 v[68:71], a[224:227], a[172:175], v[68:71]\n\t"
"Lpg11_%=:\n\t"
"s_cbranch_vccnz Lvr0_%=\n\t"
"ds_read_b128 a[172:175], v244 offset:9728\n\t"
"v_accvgpr_write_b32 a144, v224\n\t"
"v_mfma_f32_16x16x32_bf16 v[68:71], a[212:215], a[176:179], v[68:71]\n\t"
"v_accvgpr_write_b32 a146, v228\n\t"
"v_accvgpr_read_b32 v236, a152\n\t"
"v_mfma_f32_16x16x32_bf16 v[68:71], a[228:231], a[180:183], v[68:71]\n\t"
"buffer_load_dwordx4 a[140:143], off, s[80:83], s20 offset:2048 sc1\n\t"
"v_accvgpr_read_b32 v238, a156\n\t"
"v_accvgpr_read_b32 v237, a153\n\t"
"v_accvgpr_read_b32 v239, a157\n\t"
"v_cvt_pk_bf16_f32 v230, v230, v231\n\t"
"v_cvt_pk_bf16_f32 v234, v234, v235\n\t"
"s_mov_b32 s20, 774144\n\t"
"v_pk_fma_f32 v[236:237], v[238:239], s[56:57], v[236:237]\n\t"
"v_pk_fma_f32 v[238:239], v[236:237], s[58:59], v[238:239] neg_lo:[0,0,1] neg_hi:[0,0,1]\n\t"
"v_pk_fma_f32 v[98:99], v[88:89], s[60:61], v[236:237]\n\t"
"v_pk_fma_f32 v[88:89], v[236:237], s[60:61], v[88:89]\n\t"
"v_pk_fma_f32 v[236:237], v[188:189], s[62:63], v[238:239]\n\t"
"v_pk_fma_f32 v[188:189], v[238:239], s[62:63], v[188:189]\n\t"
"v_mfma_f32_16x16x32_bf16 v[72:75], a[228:231], a[184:187], v[72:75]\n\t"
"v_accvgpr_write_b32 a145, v230\n\t"
"v_accvgpr_write_b32 a147, v234\n\t"
"v_mfma_f32_16x16x32_bf16 v[72:75], a[212:215], a[180:183], v[72:75]\n\t"
"v_accvgpr_read_b32 v224, a154\n\t"
"v_accvgpr_read_b32 v226, a158\n\t"
"v_mfma_f32_16x16x32_bf16 v[72:75], a[224:227], a[176:179], v[72:75]\n\t"
"buffer_load_dwordx4 a[148:151], off, s[80:83], s20 offset:0 sc1\n\t"
"ds_read_b128 a[176:179], v244 offset:10240\n\t"
"v_accvgpr_read_b32 v225, a155\n\t"
"v_accvgpr_read_b32 v227, a159\n\t"
"v_cvt_pk_bf16_f32 v236, v236, v237\n\t"
"v_cvt_pk_bf16_f32 v98, v98, v99\n\t"
"v_pk_fma_f32 v[224:225], v[226:227], s[56:57], v[224:225]\n\t"
"v_pk_fma_f32 v[226:227], v[224:225], s[58:59], v[226:227] neg_lo:[0,0,1] neg_hi:[0,0,1]\n\t"
"v_pk_fma_f32 v[228:229], v[90:91], s[60:61], v[224:225]\n\t"
"v_pk_fma_f32 v[90:91], v[224:225], s[60:61], v[90:91]\n\t"
"v_pk_fma_f32 v[224:225], v[190:191], s[62:63], v[226:227]\n\t"
"v_pk_fma_f32 v[190:191], v[226:227], s[62:63], v[190:191]\n\t"
"v_mfma_f32_16x16x32_bf16 v[76:79], a[224:227], a[180:183], v[76:79]\n\t"
"ds_read_b128 a[180:183], v244 offset:10752\n\t"
"v_accvgpr_write_b32 a152, v236\n\t"
"v_mfma_f32_16x16x32_bf16 v[76:79], a[212:215], a[184:187], v[76:79]\n\t"
"v_accvgpr_write_b32 a154, v98\n\t"
"v_accvgpr_read_b32 v230, a160\n\t"
"v_mfma_f32_16x16x32_bf16 v[76:79], a[228:231], a[188:191], v[76:79]\n\t"
"buffer_load_dwordx4 a[52:55], off, s[80:83], s20 offset:1024 sc1\n\t"
"v_accvgpr_read_b32 v232, a164\n\t"
"v_accvgpr_read_b32 v231, a161\n\t"
"v_accvgpr_read_b32 v233, a165\n\t"
"v_cvt_pk_bf16_f32 v224, v224, v225\n\t"
"v_cvt_pk_bf16_f32 v228, v228, v229\n\t"
"v_pk_fma_f32 v[230:231], v[232:233], s[56:57], v[230:231]\n\t"
"v_pk_fma_f32 v[232:233], v[230:231], s[58:59], v[232:233] neg_lo:[0,0,1] neg_hi:[0,0,1]\n\t"
"v_pk_fma_f32 v[234:235], v[92:93], s[60:61], v[230:231]\n\t"
"v_pk_fma_f32 v[92:93], v[230:231], s[60:61], v[92:93]\n\t"
"v_pk_fma_f32 v[230:231], v[192:193], s[62:63], v[232:233]\n\t"
"v_pk_fma_f32 v[192:193], v[232:233], s[62:63], v[192:193]\n\t"
"v_mfma_f32_16x16x32_bf16 v[80:83], a[228:231], a[192:195], v[80:83]\n\t"
"v_accvgpr_write_b32 a153, v224\n\t"
"v_accvgpr_write_b32 a155, v228\n\t"
"v_mfma_f32_16x16x32_bf16 v[80:83], a[212:215], a[188:191], v[80:83]\n\t"
"v_accvgpr_read_b32 v236, a162\n\t"
"v_accvgpr_read_b32 v238, a166\n\t"
"v_mfma_f32_16x16x32_bf16 v[80:83], a[224:227], a[184:187], v[80:83]\n\t"
"buffer_load_dwordx4 a[156:159], off, s[80:83], s20 offset:2048 sc1\n\t"
"ds_read_b128 a[184:187], v244 offset:11264\n\t"
"v_accvgpr_read_b32 v237, a163\n\t"
"v_accvgpr_read_b32 v239, a167\n\t"
"v_cvt_pk_bf16_f32 v230, v230, v231\n\t"
"v_cvt_pk_bf16_f32 v234, v234, v235\n\t"
"s_mov_b32 s20, 786432\n\t"
"v_pk_fma_f32 v[236:237], v[238:239], s[56:57], v[236:237]\n\t"
"v_pk_fma_f32 v[238:239], v[236:237], s[58:59], v[238:239] neg_lo:[0,0,1] neg_hi:[0,0,1]\n\t"
"v_pk_fma_f32 v[98:99], v[94:95], s[60:61], v[236:237]\n\t"
"v_pk_fma_f32 v[94:95], v[236:237], s[60:61], v[94:95]\n\t"
"v_pk_fma_f32 v[236:237], v[194:195], s[62:63], v[238:239]\n\t"
"v_pk_fma_f32 v[194:195], v[238:239], s[62:63], v[194:195]\n\t"
"v_mfma_f32_16x16x32_bf16 v[84:87], a[224:227], a[188:191], v[84:87]\n\t"
"ds_read_b128 a[188:191], v244 offset:11776\n\t"
"v_accvgpr_write_b32 a160, v230\n\t"
"v_mfma_f32_16x16x32_bf16 v[84:87], a[212:215], a[192:195], v[84:87]\n\t"
"v_accvgpr_write_b32 a162, v234\n\t"
"v_cvt_pk_bf16_f32 v236, v236, v237\n\t"
"v_mfma_f32_16x16x32_bf16 v[84:87], a[228:231], a[196:199], v[84:87]\n\t"
"buffer_load_dwordx4 a[68:71], off, s[80:83], s20 offset:0 sc1\n\t"
"v_cvt_pk_bf16_f32 v98, v98, v99\n\t"
"v_mfma_f32_16x16x32_bf16 v[88:91], a[228:231], a[200:203], v[88:91]\n\t"
"v_accvgpr_write_b32 a161, v236\n\t"
"v_accvgpr_write_b32 a163, v98\n\t"
"v_mfma_f32_16x16x32_bf16 v[88:91], a[212:215], a[196:199], v[88:91]\n\t"
"v_mfma_f32_16x16x32_bf16 v[88:91], a[224:227], a[192:195], v[88:91]\n\t"
"buffer_load_dwordx4 a[164:167], off, s[80:83], s20 offset:1024 sc1\n\t"
"v_mfma_f32_16x16x32_bf16 v[92:95], a[224:227], a[196:199], v[92:95]\n\t"
"s_waitcnt vmcnt(21) lgkmcnt(3)\n\t"
"ds_read_b128 a[192:195], v244 offset:12288\n\t"
"v_mfma_f32_16x16x32_bf16 v[92:95], a[212:215], a[200:203], v[92:95]\n\t"
"buffer_load_dwordx4 v[236:239], off, s[80:83], s20 offset:2048 sc1\n\t"
"v_mfma_f32_16x16x32_bf16 v[92:95], a[228:231], a[204:207], v[92:95]\n\t"
"s_mov_b32 s20, 798720\n\t"
"ds_read_b128 a[196:199], v244 offset:12800\n\t"
"v_mfma_f32_16x16x32_bf16 v[100:103], a[232:235], a[168:171], v[100:103]\n\t"
"s_waitcnt lgkmcnt(4)\n\t"
"ds_read_b128 a[200:203], v244 offset:13312\n\t"
"v_mfma_f32_16x16x32_bf16 v[100:103], a[48:51], a[176:179], v[100:103]\n\t"
"ds_read_b128 a[204:207], v244 offset:13824\n\t"
"v_mfma_f32_16x16x32_bf16 v[100:103], a[40:43], a[172:175], v[100:103]\n\t"
"buffer_load_dwordx4 a[76:79], off, s[80:83], s20 offset:0 sc1\n\t"
"v_mfma_f32_16x16x32_bf16 v[104:107], a[232:235], a[172:175], v[104:107]\n\t"
"buffer_load_dwordx4 a[84:87], off, s[80:83], s20 offset:1024 sc1\n\t"
"v_mfma_f32_16x16x32_bf16 v[104:107], a[40:43], a[176:179], v[104:107]\n\t"
"s_waitcnt lgkmcnt(5)\n\t"
"v_mfma_f32_16x16x32_bf16 v[104:107], a[48:51], a[180:183], v[104:107]\n\t"
"s_waitcnt lgkmcnt(0)\n\t"
"s_barrier\n\t"
"v_mfma_f32_16x16x32_bf16 v[108:111], a[48:51], a[184:187], v[108:111]\n\t"
"v_mfma_f32_16x16x32_bf16 v[108:111], a[40:43], a[180:183], v[108:111]\n\t"
"buffer_load_dwordx4 a[92:95], off, s[80:83], s20 offset:2048 sc1\n\t"
"v_mfma_f32_16x16x32_bf16 v[108:111], a[232:235], a[176:179], v[108:111]\n\t"
"s_mov_b32 s20, 1179648\n\t"
"s_waitcnt vmcnt(16)\n\t"
"ds_write_b128 v249, v[196:199] offset:0\n\t"
"v_mfma_f32_16x16x32_bf16 v[112:115], a[232:235], a[180:183], v[112:115]\n\t"
"buffer_load_dwordx4 v[96:99], off, s[80:83], s20 offset:0 sc1\n\t"
"v_mfma_f32_16x16x32_bf16 v[112:115], a[40:43], a[184:187], v[112:115]\n\t"
"v_mfma_f32_16x16x32_bf16 v[112:115], a[48:51], a[188:191], v[112:115]\n\t"
"buffer_load_dwordx4 a[208:211], off, s[80:83], s20 offset:1024 sc1\n\t"
"v_mfma_f32_16x16x32_bf16 v[116:119], a[48:51], a[192:195], v[116:119]\n\t"
"v_mfma_f32_16x16x32_bf16 v[116:119], a[40:43], a[188:191], v[116:119]\n\t"
"v_mfma_f32_16x16x32_bf16 v[116:119], a[232:235], a[184:187], v[116:119]\n\t"
"buffer_load_dwordx4 a[212:215], off, s[80:83], s20 offset:2048 sc1\n\t"
"v_mfma_f32_16x16x32_bf16 v[120:123], a[232:235], a[188:191], v[120:123]\n\t"
"s_mov_b32 s20, 1191936\n\t"
"v_mfma_f32_16x16x32_bf16 v[120:123], a[40:43], a[192:195], v[120:123]\n\t"
"v_mfma_f32_16x16x32_bf16 v[120:123], a[48:51], a[196:199], v[120:123]\n\t"
"buffer_load_dwordx4 a[216:219], off, s[80:83], s20 offset:0 sc1\n\t"
"v_mfma_f32_16x16x32_bf16 v[124:127], a[48:51], a[200:203], v[124:127]\n\t"
"v_mfma_f32_16x16x32_bf16 v[124:127], a[40:43], a[196:199], v[124:127]\n\t"
"v_mfma_f32_16x16x32_bf16 v[124:127], a[232:235], a[192:195], v[124:127]\n\t"
"buffer_load_dwordx4 a[220:223], off, s[80:83], s20 offset:1024 sc1\n\t"
"v_mfma_f32_16x16x32_bf16 v[128:131], a[232:235], a[196:199], v[128:131]\n\t"
"v_mfma_f32_16x16x32_bf16 v[128:131], a[40:43], a[200:203], v[128:131]\n\t"
"v_mfma_f32_16x16x32_bf16 v[128:131], a[48:51], a[204:207], v[128:131]\n\t"
"buffer_load_dwordx4 a[224:227], off, s[80:83], s20 offset:2048 sc1\n\t"
"v_mfma_f32_16x16x32_bf16 v[132:135], a[56:59], a[168:171], v[132:135]\n\t"
"s_mov_b32 s20, 1204224\n\t"
"v_mfma_f32_16x16x32_bf16 v[132:135], a[72:75], a[176:179], v[132:135]\n\t"
"v_mfma_f32_16x16x32_bf16 v[132:135], a[64:67], a[172:175], v[132:135]\n\t"
"ds_write_b64 v250, a[104:105] offset:0\n\t"
"v_mfma_f32_16x16x32_bf16 v[136:139], a[56:59], a[172:175], v[136:139]\n\t"
"buffer_load_dwordx4 a[228:231], off, s[80:83], s20 offset:0 sc1\n\t"
"v_mfma_f32_16x16x32_bf16 v[136:139], a[64:67], a[176:179], v[136:139]\n\t"
"v_mfma_f32_16x16x32_bf16 v[136:139], a[72:75], a[180:183], v[136:139]\n\t"
"buffer_load_dwordx4 a[232:235], off, s[80:83], s20 offset:1024 sc1\n\t"
"v_mfma_f32_16x16x32_bf16 v[140:143], a[72:75], a[184:187], v[140:143]\n\t"
"v_mfma_f32_16x16x32_bf16 v[140:143], a[64:67], a[180:183], v[140:143]\n\t"
"v_mfma_f32_16x16x32_bf16 v[140:143], a[56:59], a[176:179], v[140:143]\n\t"
"buffer_load_dwordx4 a[236:239], off, s[80:83], s20 offset:2048 sc1\n\t"
"v_mfma_f32_16x16x32_bf16 v[144:147], a[56:59], a[180:183], v[144:147]\n\t"
"s_mov_b32 s20, 1363968\n\t"
"v_mfma_f32_16x16x32_bf16 v[144:147], a[64:67], a[184:187], v[144:147]\n\t"
"v_mfma_f32_16x16x32_bf16 v[144:147], a[72:75], a[188:191], v[144:147]\n\t"
"buffer_load_dwordx4 a[240:243], off, s[80:83], s20 offset:0 sc1\n\t"
"v_mfma_f32_16x16x32_bf16 v[148:151], a[72:75], a[192:195], v[148:151]\n\t"
"v_mfma_f32_16x16x32_bf16 v[148:151], a[64:67], a[188:191], v[148:151]\n\t"
"v_mfma_f32_16x16x32_bf16 v[148:151], a[56:59], a[184:187], v[148:151]\n\t"
"buffer_load_dwordx4 a[244:247], off, s[80:83], s20 offset:1024 sc1\n\t"
"v_mfma_f32_16x16x32_bf16 v[152:155], a[56:59], a[188:191], v[152:155]\n\t"
"v_mfma_f32_16x16x32_bf16 v[152:155], a[64:67], a[192:195], v[152:155]\n\t"
"v_mfma_f32_16x16x32_bf16 v[152:155], a[72:75], a[196:199], v[152:155]\n\t"
"ds_write_b128 v249, v[200:203] offset:6144\n\t"
"v_mfma_f32_16x16x32_bf16 v[156:159], a[72:75], a[200:203], v[156:159]\n\t"
"buffer_load_dwordx4 a[248:251], off, s[80:83], s20 offset:2048 sc1\n\t"
"v_mfma_f32_16x16x32_bf16 v[156:159], a[64:67], a[196:199], v[156:159]\n\t"
"s_mov_b32 s20, 1376256\n\t"
"v_mfma_f32_16x16x32_bf16 v[156:159], a[56:59], a[192:195], v[156:159]\n\t"
"buffer_load_dwordx4 a[252:255], off, s[80:83], s20 offset:0 sc1\n\t"
"v_mfma_f32_16x16x32_bf16 v[160:163], a[56:59], a[196:199], v[160:163]\n\t"
"v_mfma_f32_16x16x32_bf16 v[160:163], a[64:67], a[200:203], v[160:163]\n\t"
"v_mfma_f32_16x16x32_bf16 v[160:163], a[72:75], a[204:207], v[160:163]\n\t"
"buffer_load_dwordx4 a[72:75], off, s[80:83], s20 offset:1024 sc1\n\t"
"v_mfma_f32_16x16x32_bf16 v[164:167], a[80:83], a[168:171], v[164:167]\n\t"
"ds_read_b128 a[168:171], v244 offset:24576\n\t"
"v_mfma_f32_16x16x32_bf16 v[164:167], a[96:99], a[176:179], v[164:167]\n\t"
"v_mfma_f32_16x16x32_bf16 v[164:167], a[88:91], a[172:175], v[164:167]\n\t"
"ds_write_b64 v250, a[106:107] offset:6144\n\t"
"v_mfma_f32_16x16x32_bf16 v[168:171], a[80:83], a[172:175], v[168:171]\n\t"
"ds_read_b128 a[172:175], v244 offset:25088\n\t"
"v_mfma_f32_16x16x32_bf16 v[168:171], a[88:91], a[176:179], v[168:171]\n\t"
"v_mfma_f32_16x16x32_bf16 v[168:171], a[96:99], a[180:183], v[168:171]\n\t"
"v_mfma_f32_16x16x32_bf16 v[172:175], a[96:99], a[184:187], v[172:175]\n\t"
"v_mfma_f32_16x16x32_bf16 v[172:175], a[88:91], a[180:183], v[172:175]\n\t"
"v_mfma_f32_16x16x32_bf16 v[172:175], a[80:83], a[176:179], v[172:175]\n\t"
"ds_read_b128 a[176:179], v244 offset:25600\n\t"
"v_mfma_f32_16x16x32_bf16 v[176:179], a[80:83], a[180:183], v[176:179]\n\t"
"ds_read_b128 a[180:183], v244 offset:26112\n\t"
"v_mfma_f32_16x16x32_bf16 v[176:179], a[88:91], a[184:187], v[176:179]\n\t"
"v_mfma_f32_16x16x32_bf16 v[176:179], a[96:99], a[188:191], v[176:179]\n\t"
"s_waitcnt lgkmcnt(3)\n\t"
"s_barrier\n\t"
"v_mfma_f32_16x16x32_bf16 v[180:183], a[96:99], a[192:195], v[180:183]\n\t"
"v_mfma_f32_16x16x32_bf16 v[180:183], a[88:91], a[188:191], v[180:183]\n\t"
"buffer_load_dwordx4 v[196:199], off, s[80:83], s20 offset:2048 sc1\n\t"
"v_mfma_f32_16x16x32_bf16 v[180:183], a[80:83], a[184:187], v[180:183]\n\t"
"s_mov_b32 s20, 1388544\n\t"
"ds_read_b128 a[184:187], v244 offset:26624\n\t"
"v_mfma_f32_16x16x32_bf16 v[184:187], a[80:83], a[188:191], v[184:187]\n\t"
"ds_read_b128 a[188:191], v244 offset:27136\n\t"
"v_mfma_f32_16x16x32_bf16 v[184:187], a[88:91], a[192:195], v[184:187]\n\t"
"buffer_load_dwordx4 a[104:107], off, s[80:83], s20 offset:0 sc1\n\t"
"v_mfma_f32_16x16x32_bf16 v[184:187], a[96:99], a[196:199], v[184:187]\n\t"
"ds_write_b128 v249, v[204:207] offset:12288\n\t"
"v_mfma_f32_16x16x32_bf16 v[188:191], a[96:99], a[200:203], v[188:191]\n\t"
"v_mfma_f32_16x16x32_bf16 v[188:191], a[88:91], a[196:199], v[188:191]\n\t"
"buffer_load_dwordx4 v[200:203], off, s[80:83], s20 offset:1024 sc1\n\t"
"v_mfma_f32_16x16x32_bf16 v[188:191], a[80:83], a[192:195], v[188:191]\n\t"
"ds_read_b128 a[192:195], v244 offset:27648\n\t"
"v_mfma_f32_16x16x32_bf16 v[192:195], a[80:83], a[196:199], v[192:195]\n\t"
"s_waitcnt vmcnt(32) lgkmcnt(5)\n\t"
"ds_read_b128 a[196:199], v244 offset:28160\n\t"
"v_mfma_f32_16x16x32_bf16 v[192:195], a[88:91], a[200:203], v[192:195]\n\t"
"buffer_load_dwordx4 a[80:83], off, s[80:83], s20 offset:2048 sc1\n\t"
"v_mfma_f32_16x16x32_bf16 v[192:195], a[96:99], a[204:207], v[192:195]\n\t"
"s_mov_b32 s20, 1474560\n\t"
"ds_read_b128 a[200:203], v244 offset:28672\n\t"
"Lst15_%=:\n\t"
"v_mfma_f32_16x16x32_bf16 v[0:3], a[108:111], a[168:171], v[0:3]\n\t"
"s_waitcnt lgkmcnt(6)\n\t"
"ds_read_b128 a[204:207], v244 offset:29184\n\t"
"v_mfma_f32_16x16x32_bf16 v[0:3], a[116:119], a[176:179], v[0:3]\n\t"
"buffer_load_dwordx4 a[88:91], off, s[80:83], s20 offset:0 sc1\n\t"
"v_mfma_f32_16x16x32_bf16 v[0:3], a[60:63], a[172:175], v[0:3]\n\t"
"ds_write_b64 v250, a[112:113] offset:12288\n\t"
"v_mfma_f32_16x16x32_bf16 v[4:7], a[108:111], a[172:175], v[4:7]\n\t"
"s_waitcnt lgkmcnt(7)\n\t"
"v_mfma_f32_16x16x32_bf16 v[4:7], a[60:63], a[176:179], v[4:7]\n\t"
"buffer_load_dwordx4 a[96:99], off, s[80:83], s20 offset:1024 sc1\n\t"
"v_mfma_f32_16x16x32_bf16 v[4:7], a[116:119], a[180:183], v[4:7]\n\t"
"ds_write_b128 v249, v[208:211] offset:18432\n\t"
"v_mfma_f32_16x16x32_bf16 v[8:11], a[116:119], a[184:187], v[8:11]\n\t"
"s_waitcnt lgkmcnt(7)\n\t"
"v_mfma_f32_16x16x32_bf16 v[8:11], a[60:63], a[180:183], v[8:11]\n\t"
"v_mfma_f32_16x16x32_bf16 v[8:11], a[108:111], a[176:179], v[8:11]\n\t"
"ds_write_b64 v250, a[114:115] offset:18432\n\t"
"v_mfma_f32_16x16x32_bf16 v[12:15], a[108:111], a[180:183], v[12:15]\n\t"
"s_waitcnt lgkmcnt(6)\n\t"
"v_mfma_f32_16x16x32_bf16 v[12:15], a[60:63], a[184:187], v[12:15]\n\t"
"ds_read_b128 a[40:43], v252 offset:0\n\t"
"v_mfma_f32_16x16x32_bf16 v[12:15], a[116:119], a[188:191], v[12:15]\n\t"
"v_mfma_f32_16x16x32_bf16 v[16:19], a[116:119], a[192:195], v[16:19]\n\t"
"s_waitcnt lgkmcnt(6)\n\t"
"v_mfma_f32_16x16x32_bf16 v[16:19], a[60:63], a[188:191], v[16:19]\n\t"
"v_mfma_f32_16x16x32_bf16 v[16:19], a[108:111], a[184:187], v[16:19]\n\t"
"v_mfma_f32_16x16x32_bf16 v[20:23], a[108:111], a[188:191], v[20:23]\n\t"
"s_waitcnt lgkmcnt(5)\n\t"
"v_mfma_f32_16x16x32_bf16 v[20:23], a[60:63], a[192:195], v[20:23]\n\t"
"ds_read_b128 a[48:51], v252 offset:1024\n\t"
"v_mfma_f32_16x16x32_bf16 v[20:23], a[116:119], a[196:199], v[20:23]\n\t"
"v_mfma_f32_16x16x32_bf16 v[24:27], a[116:119], a[200:203], v[24:27]\n\t"
"s_waitcnt lgkmcnt(5)\n\t"
"ds_read_b128 a[56:59], v252 offset:2048\n\t"
"v_mfma_f32_16x16x32_bf16 v[24:27], a[60:63], a[196:199], v[24:27]\n\t"
"v_mfma_f32_16x16x32_bf16 v[24:27], a[108:111], a[192:195], v[24:27]\n\t"
"v_mfma_f32_16x16x32_bf16 v[28:31], a[108:111], a[196:199], v[28:31]\n\t"
"s_waitcnt vmcnt(32)\n\t"
"v_mfma_f32_16x16x32_bf16 v[28:31], a[60:63], a[200:203], v[28:31]\n\t"
"buffer_load_dwordx4 a[60:63], off, s[80:83], s20 offset:2048 sc1\n\t"
"v_mfma_f32_16x16x32_bf16 v[28:31], a[116:119], a[204:207], v[28:31]\n\t"
"s_add_u32 s29, s22, 384\n\t"
"v_mfma_f32_16x16x32_bf16 v[32:35], a[100:103], a[168:171], v[32:35]\n\t"
"v_mfma_f32_16x16x32_bf16 v[32:35], a[132:135], a[176:179], v[32:35]\n\t"
"s_waitcnt lgkmcnt(2)\n\t"
"v_mfma_f32_16x16x32_bf16 v[32:35], a[124:127], a[172:175], v[32:35]\n\t"
"v_mfma_f32_16x16x32_bf16 v[36:39], a[100:103], a[172:175], v[36:39]\n\t"
"v_mfma_f32_16x16x32_bf16 v[36:39], a[124:127], a[176:179], v[36:39]\n\t"
"v_mfma_f32_16x16x32_bf16 v[36:39], a[132:135], a[180:183], v[36:39]\n\t"
"buffer_store_dwordx4 a[40:43], v248, s[16:19], s29 offen offset:0 nt\n\t"
"v_mfma_f32_16x16x32_bf16 v[40:43], a[132:135], a[184:187], v[40:43]\n\t"
"v_mfma_f32_16x16x32_bf16 v[40:43], a[124:127], a[180:183], v[40:43]\n\t"
"s_mov_b32 s20, 1486848\n\t"
"v_mfma_f32_16x16x32_bf16 v[40:43], a[100:103], a[176:179], v[40:43]\n\t"
"v_mfma_f32_16x16x32_bf16 v[44:47], a[100:103], a[180:183], v[44:47]\n\t"
"v_mfma_f32_16x16x32_bf16 v[44:47], a[124:127], a[184:187], v[44:47]\n\t"
"v_mfma_f32_16x16x32_bf16 v[44:47], a[132:135], a[188:191], v[44:47]\n\t"
"s_waitcnt lgkmcnt(0)\n\t"
"s_barrier\n\t"
"v_mfma_f32_16x16x32_bf16 v[48:51], a[132:135], a[192:195], v[48:51]\n\t"
"v_mfma_f32_16x16x32_bf16 v[48:51], a[124:127], a[188:191], v[48:51]\n\t"
"buffer_load_dwordx4 v[204:207], off, s[80:83], s20 offset:0 sc1\n\t"
"v_mfma_f32_16x16x32_bf16 v[48:51], a[100:103], a[184:187], v[48:51]\n\t"
"ds_write_b128 v249, v[212:215] offset:0\n\t"
"v_mfma_f32_16x16x32_bf16 v[52:55], a[100:103], a[188:191], v[52:55]\n\t"
"v_mfma_f32_16x16x32_bf16 v[52:55], a[124:127], a[192:195], v[52:55]\n\t"
"buffer_load_dwordx4 a[112:115], off, s[80:83], s20 offset:1024 sc1\n\t"
"v_mfma_f32_16x16x32_bf16 v[52:55], a[132:135], a[196:199], v[52:55]\n\t"
"ds_write_b64 v250, a[120:121] offset:0\n\t"
"v_mfma_f32_16x16x32_bf16 v[56:59], a[132:135], a[200:203], v[56:59]\n\t"
"v_mfma_f32_16x16x32_bf16 v[56:59], a[124:127], a[196:199], v[56:59]\n\t"
"buffer_load_dwordx4 v[208:211], off, s[80:83], s20 offset:2048 sc1\n\t"
"v_mfma_f32_16x16x32_bf16 v[56:59], a[100:103], a[192:195], v[56:59]\n\t"
"buffer_store_dwordx4 a[48:51], v248, s[16:19], s29 offen offset:128 nt\n\t"
"ds_write_b64 v250, a[122:123] offset:6144\n\t"
"v_mfma_f32_16x16x32_bf16 v[60:63], a[100:103], a[196:199], v[60:63]\n\t"
"s_waitcnt vmcnt(35)\n\t"
"v_mfma_f32_16x16x32_bf16 v[60:63], a[124:127], a[200:203], v[60:63]\n\t"
"v_mfma_f32_16x16x32_bf16 v[60:63], a[132:135], a[204:207], v[60:63]\n\t"
"ds_write_b128 v249, v[216:219] offset:6144\n\t"
"v_mfma_f32_16x16x32_bf16 v[64:67], a[44:47], a[168:171], v[64:67]\n\t"
"v_mfma_f32_16x16x32_bf16 v[64:67], a[140:143], a[176:179], v[64:67]\n\t"
"ds_read_b128 a[168:171], v244 offset:33792\n\t"
"v_mfma_f32_16x16x32_bf16 v[64:67], v[240:243], a[172:175], v[64:67]\n\t"
"v_mfma_f32_16x16x32_bf16 v[68:71], a[44:47], a[172:175], v[68:71]\n\t"
"ds_read_b128 a[172:175], v244 offset:34304\n\t"
"v_mfma_f32_16x16x32_bf16 v[68:71], v[240:243], a[176:179], v[68:71]\n\t"
"v_mfma_f32_16x16x32_bf16 v[68:71], a[140:143], a[180:183], v[68:71]\n\t"
"v_mfma_f32_16x16x32_bf16 v[72:75], a[140:143], a[184:187], v[72:75]\n\t"
"v_mfma_f32_16x16x32_bf16 v[72:75], v[240:243], a[180:183], v[72:75]\n\t"
"v_mfma_f32_16x16x32_bf16 v[72:75], a[44:47], a[176:179], v[72:75]\n\t"
"ds_read_b128 a[176:179], v244 offset:34816\n\t"
"v_mfma_f32_16x16x32_bf16 v[76:79], a[44:47], a[180:183], v[76:79]\n\t"
"ds_read_b128 a[180:183], v244 offset:35328\n\t"
"v_mfma_f32_16x16x32_bf16 v[76:79], v[240:243], a[184:187], v[76:79]\n\t"
"ds_read_b128 a[40:43], v252 offset:12288\n\t"
"v_mfma_f32_16x16x32_bf16 v[76:79], a[140:143], a[188:191], v[76:79]\n\t"
"buffer_store_dwordx4 a[56:59], v248, s[16:19], s29 offen offset:256 nt\n\t"
"ds_read_b128 a[48:51], v252 offset:13312\n\t"
"v_mfma_f32_16x16x32_bf16 v[80:83], a[140:143], a[192:195], v[80:83]\n\t"
"ds_read_b128 a[64:67], v252 offset:14336\n\t"
"v_mfma_f32_16x16x32_bf16 v[80:83], v[240:243], a[188:191], v[80:83]\n\t"
"v_mfma_f32_16x16x32_bf16 v[80:83], a[44:47], a[184:187], v[80:83]\n\t"
"ds_read_b128 a[184:187], v244 offset:35840\n\t"
"v_mfma_f32_16x16x32_bf16 v[84:87], a[44:47], a[188:191], v[84:87]\n\t"
"s_mov_b32 s20, 1499136\n\t"
"ds_read_b128 a[188:191], v244 offset:36352\n\t"
"v_mfma_f32_16x16x32_bf16 v[84:87], v[240:243], a[192:195], v[84:87]\n\t"
"v_mfma_f32_16x16x32_bf16 v[84:87], a[140:143], a[196:199], v[84:87]\n\t"
"v_mfma_f32_16x16x32_bf16 v[88:91], a[140:143], a[200:203], v[88:91]\n\t"
"v_mfma_f32_16x16x32_bf16 v[88:91], v[240:243], a[196:199], v[88:91]\n\t"
"v_mfma_f32_16x16x32_bf16 v[88:91], a[44:47], a[192:195], v[88:91]\n\t"
"s_add_u32 s29, s22, 196992\n\t"
"ds_read_b128 a[192:195], v244 offset:36864\n\t"
"v_mfma_f32_16x16x32_bf16 v[92:95], a[44:47], a[196:199], v[92:95]\n\t"
"buffer_load_dwordx4 a[44:47], off, s[80:83], s20 offset:0 sc1\n\t"
"v_mfma_f32_16x16x32_bf16 v[92:95], v[240:243], a[200:203], v[92:95]\n\t"
"s_waitcnt vmcnt(34) lgkmcnt(7)\n\t"
"ds_read_b128 a[196:199], v244 offset:37376\n\t"
"v_mfma_f32_16x16x32_bf16 v[92:95], a[140:143], a[204:207], v[92:95]\n\t"
"s_waitcnt lgkmcnt(6)\n\t"
"ds_read_b128 a[200:203], v244 offset:37888\n\t"
"v_mfma_f32_16x16x32_bf16 v[100:103], a[148:151], a[168:171], v[100:103]\n\t"
"ds_read_b128 a[204:207], v244 offset:38400\n\t"
"v_mfma_f32_16x16x32_bf16 v[100:103], a[156:159], a[176:179], v[100:103]\n\t"
"v_mfma_f32_16x16x32_bf16 v[100:103], a[52:55], a[172:175], v[100:103]\n\t"
"buffer_store_dwordx4 a[40:43], v248, s[16:19], s29 offen offset:0 nt\n\t"
"v_mfma_f32_16x16x32_bf16 v[104:107], a[148:151], a[172:175], v[104:107]\n\t"
"s_waitcnt lgkmcnt(5)\n\t"
"v_mfma_f32_16x16x32_bf16 v[104:107], a[52:55], a[176:179], v[104:107]\n\t"
"v_mfma_f32_16x16x32_bf16 v[104:107], a[156:159], a[180:183], v[104:107]\n\t"
"s_waitcnt lgkmcnt(0)\n\t"
"s_barrier\n\t"
"v_mfma_f32_16x16x32_bf16 v[108:111], a[156:159], a[184:187], v[108:111]\n\t"
"v_mfma_f32_16x16x32_bf16 v[108:111], a[52:55], a[180:183], v[108:111]\n\t"
"buffer_load_dwordx4 v[212:215], off, s[80:83], s20 offset:1024 sc1\n\t"
"v_mfma_f32_16x16x32_bf16 v[108:111], a[148:151], a[176:179], v[108:111]\n\t"
"ds_write_b128 v249, a[0:3] offset:12288\n\t"
"v_mfma_f32_16x16x32_bf16 v[112:115], a[148:151], a[180:183], v[112:115]\n\t"
"v_mfma_f32_16x16x32_bf16 v[112:115], a[52:55], a[184:187], v[112:115]\n\t"
"buffer_load_dwordx4 a[120:123], off, s[80:83], s20 offset:2048 sc1\n\t"
"v_mfma_f32_16x16x32_bf16 v[112:115], a[156:159], a[188:191], v[112:115]\n\t"
"s_mov_b32 s20, 1658880\n\t"
"ds_write_b64 v250, a[128:129] offset:12288\n\t"
"v_mfma_f32_16x16x32_bf16 v[116:119], a[156:159], a[192:195], v[116:119]\n\t"
"v_mfma_f32_16x16x32_bf16 v[116:119], a[52:55], a[188:191], v[116:119]\n\t"
"buffer_load_dwordx4 v[216:219], off, s[80:83], s20 offset:0 sc1\n\t"
"v_mfma_f32_16x16x32_bf16 v[116:119], a[148:151], a[184:187], v[116:119]\n\t"
"ds_write_b128 v249, a[4:7] offset:18432\n\t"
"v_mfma_f32_16x16x32_bf16 v[120:123], a[148:151], a[188:191], v[120:123]\n\t"
"v_mfma_f32_16x16x32_bf16 v[120:123], a[52:55], a[192:195], v[120:123]\n\t"
"v_mfma_f32_16x16x32_bf16 v[120:123], a[156:159], a[196:199], v[120:123]\n\t"
"ds_write_b64 v250, a[130:131] offset:18432\n\t"
"v_mfma_f32_16x16x32_bf16 v[124:127], a[156:159], a[200:203], v[124:127]\n\t"
"v_mfma_f32_16x16x32_bf16 v[124:127], a[52:55], a[196:199], v[124:127]\n\t"
"v_mfma_f32_16x16x32_bf16 v[124:127], a[148:151], a[192:195], v[124:127]\n\t"
"buffer_store_dwordx4 a[48:51], v248, s[16:19], s29 offen offset:128 nt\n\t"
"v_mfma_f32_16x16x32_bf16 v[128:131], a[148:151], a[196:199], v[128:131]\n\t"
"s_waitcnt vmcnt(36)\n\t"
"v_mfma_f32_16x16x32_bf16 v[128:131], a[52:55], a[200:203], v[128:131]\n\t"
"v_mfma_f32_16x16x32_bf16 v[128:131], a[156:159], a[204:207], v[128:131]\n\t"
"v_mfma_f32_16x16x32_bf16 v[132:135], a[68:71], a[168:171], v[132:135]\n\t"
"v_mfma_f32_16x16x32_bf16 v[132:135], v[236:239], a[176:179], v[132:135]\n\t"
"v_mfma_f32_16x16x32_bf16 v[132:135], a[164:167], a[172:175], v[132:135]\n\t"
"v_mfma_f32_16x16x32_bf16 v[136:139], a[68:71], a[172:175], v[136:139]\n\t"
"v_mfma_f32_16x16x32_bf16 v[136:139], a[164:167], a[176:179], v[136:139]\n\t"
"v_mfma_f32_16x16x32_bf16 v[136:139], v[236:239], a[180:183], v[136:139]\n\t"
"v_mfma_f32_16x16x32_bf16 v[140:143], v[236:239], a[184:187], v[140:143]\n\t"
"v_mfma_f32_16x16x32_bf16 v[140:143], a[164:167], a[180:183], v[140:143]\n\t"
"ds_read_b128 a[40:43], v252 offset:0\n\t"
"v_mfma_f32_16x16x32_bf16 v[140:143], a[68:71], a[176:179], v[140:143]\n\t"
"v_mfma_f32_16x16x32_bf16 v[144:147], a[68:71], a[180:183], v[144:147]\n\t"
"v_mfma_f32_16x16x32_bf16 v[144:147], a[164:167], a[184:187], v[144:147]\n\t"
"v_mfma_f32_16x16x32_bf16 v[144:147], v[236:239], a[188:191], v[144:147]\n\t"
"buffer_store_dwordx4 a[64:67], v248, s[16:19], s29 offen offset:256 nt\n\t"
"v_mfma_f32_16x16x32_bf16 v[148:151], v[236:239], a[192:195], v[148:151]\n\t"
"v_mfma_f32_16x16x32_bf16 v[148:151], a[164:167], a[188:191], v[148:151]\n\t"
"v_mfma_f32_16x16x32_bf16 v[148:151], a[68:71], a[184:187], v[148:151]\n\t"
"v_mfma_f32_16x16x32_bf16 v[152:155], a[68:71], a[188:191], v[152:155]\n\t"
"v_mfma_f32_16x16x32_bf16 v[152:155], a[164:167], a[192:195], v[152:155]\n\t"
"ds_read_b128 a[48:51], v252 offset:1024\n\t"
"v_mfma_f32_16x16x32_bf16 v[152:155], v[236:239], a[196:199], v[152:155]\n\t"
"v_mfma_f32_16x16x32_bf16 v[156:159], v[236:239], a[200:203], v[156:159]\n\t"
"ds_read_b128 a[56:59], v252 offset:2048\n\t"
"v_mfma_f32_16x16x32_bf16 v[156:159], a[164:167], a[196:199], v[156:159]\n\t"
"s_add_u32 s29, s22, 393600\n\t"
"v_mfma_f32_16x16x32_bf16 v[156:159], a[68:71], a[192:195], v[156:159]\n\t"
"v_mfma_f32_16x16x32_bf16 v[160:163], a[68:71], a[196:199], v[160:163]\n\t"
"s_waitcnt vmcnt(34)\n\t"
"v_mfma_f32_16x16x32_bf16 v[160:163], a[164:167], a[200:203], v[160:163]\n\t"
"s_waitcnt lgkmcnt(2)\n\t"
"v_mfma_f32_16x16x32_bf16 v[160:163], v[236:239], a[204:207], v[160:163]\n\t"
"v_mfma_f32_16x16x32_bf16 v[164:167], a[76:79], a[168:171], v[164:167]\n\t"
"ds_read_b128 a[168:171], v222 offset:3072\n\t"
"v_mfma_f32_16x16x32_bf16 v[164:167], a[92:95], a[176:179], v[164:167]\n\t"
"v_mfma_f32_16x16x32_bf16 v[164:167], a[84:87], a[172:175], v[164:167]\n\t"
"buffer_store_dwordx4 a[40:43], v248, s[16:19], s29 offen offset:0 nt\n\t"
"v_mfma_f32_16x16x32_bf16 v[168:171], a[76:79], a[172:175], v[168:171]\n\t"
"ds_read_b128 a[172:175], v223 offset:9216\n\t"
"v_mfma_f32_16x16x32_bf16 v[168:171], a[84:87], a[176:179], v[168:171]\n\t"
"v_mfma_f32_16x16x32_bf16 v[168:171], a[92:95], a[180:183], v[168:171]\n\t"
"v_mfma_f32_16x16x32_bf16 v[172:175], a[92:95], a[184:187], v[172:175]\n\t"
"v_mfma_f32_16x16x32_bf16 v[172:175], a[84:87], a[180:183], v[172:175]\n\t"
"v_mfma_f32_16x16x32_bf16 v[172:175], a[76:79], a[176:179], v[172:175]\n\t"
"ds_read_b128 a[176:179], v223 offset:15360\n\t"
"v_mfma_f32_16x16x32_bf16 v[176:179], a[76:79], a[180:183], v[176:179]\n\t"
"ds_read_b128 a[180:183], v222 offset:23040\n\t"
"v_mfma_f32_16x16x32_bf16 v[176:179], a[84:87], a[184:187], v[176:179]\n\t"
"v_mfma_f32_16x16x32_bf16 v[176:179], a[92:95], a[188:191], v[176:179]\n\t"
"s_waitcnt lgkmcnt(4)\n\t"
"s_barrier\n\t"
"v_mfma_f32_16x16x32_bf16 v[180:183], a[92:95], a[192:195], v[180:183]\n\t"
"v_mfma_f32_16x16x32_bf16 v[180:183], a[84:87], a[188:191], v[180:183]\n\t"
"buffer_load_dwordx4 a[0:3], off, s[80:83], s20 offset:1024 sc1\n\t"
"v_mfma_f32_16x16x32_bf16 v[180:183], a[76:79], a[184:187], v[180:183]\n\t"
"ds_read_b128 a[184:187], v223 offset:29184\n\t"
"v_mfma_f32_16x16x32_bf16 v[184:187], a[76:79], a[188:191], v[184:187]\n\t"
"ds_read_b128 a[188:191], v223 offset:35328\n\t"
"v_mfma_f32_16x16x32_bf16 v[184:187], a[84:87], a[192:195], v[184:187]\n\t"
"buffer_load_dwordx4 a[128:131], off, s[80:83], s20 offset:2048 sc1\n\t"
"v_mfma_f32_16x16x32_bf16 v[184:187], a[92:95], a[196:199], v[184:187]\n\t"
"buffer_store_dwordx4 a[48:51], v248, s[16:19], s29 offen offset:128 nt\n\t"
"ds_write_b64 v250, a[136:137] offset:0\n\t"
"s_mov_b32 s20, 1671168\n\t"
"v_mfma_f32_16x16x32_bf16 v[188:191], a[92:95], a[200:203], v[188:191]\n\t"
"v_mfma_f32_16x16x32_bf16 v[188:191], a[84:87], a[196:199], v[188:191]\n\t"
"buffer_load_dwordx4 a[4:7], off, s[80:83], s20 offset:0 sc1\n\t"
"v_mfma_f32_16x16x32_bf16 v[188:191], a[76:79], a[192:195], v[188:191]\n\t"
"ds_read_b128 a[192:195], v222 offset:43008\n\t"
"v_mfma_f32_16x16x32_bf16 v[192:195], a[76:79], a[196:199], v[192:195]\n\t"
"s_waitcnt vmcnt(36) lgkmcnt(5)\n\t"
"ds_read_b128 a[196:199], v223 offset:49152\n\t"
"v_mfma_f32_16x16x32_bf16 v[192:195], a[84:87], a[200:203], v[192:195]\n\t"
"ds_read_b128 a[200:203], v223 offset:55296\n\t"
"v_mfma_f32_16x16x32_bf16 v[192:195], a[92:95], a[204:207], v[192:195]\n\t"
"ds_write_b128 v249, a[8:11] offset:0\n\t"
"Lst16_%=:\n\t"
"v_mfma_f32_16x16x32_bf16 v[0:3], v[96:99], a[168:171], v[0:3]\n\t"
"s_waitcnt lgkmcnt(7)\n\t"
"v_mfma_f32_16x16x32_bf16 v[0:3], a[212:215], a[176:179], v[0:3]\n\t"
"ds_read_b128 a[204:207], v222 offset:62976\n\t"
"v_mfma_f32_16x16x32_bf16 v[0:3], a[208:211], a[172:175], v[0:3]\n\t"
"ds_write_b128 v249, a[12:15] offset:6144\n\t"
"v_mfma_f32_16x16x32_bf16 v[4:7], v[96:99], a[172:175], v[4:7]\n\t"
"s_waitcnt lgkmcnt(8)\n\t"
"v_mfma_f32_16x16x32_bf16 v[4:7], a[208:211], a[176:179], v[4:7]\n\t"
"v_mfma_f32_16x16x32_bf16 v[4:7], a[212:215], a[180:183], v[4:7]\n\t"
"ds_read_b128 a[40:43], v252 offset:12288\n\t"
"v_mfma_f32_16x16x32_bf16 v[8:11], a[212:215], a[184:187], v[8:11]\n\t"
"s_waitcnt lgkmcnt(8)\n\t"
"v_mfma_f32_16x16x32_bf16 v[8:11], a[208:211], a[180:183], v[8:11]\n\t"
"v_mfma_f32_16x16x32_bf16 v[8:11], v[96:99], a[176:179], v[8:11]\n\t"
"buffer_store_dwordx4 a[56:59], v248, s[16:19], s29 offen offset:256 nt\n\t"
"ds_write_b64 v250, a[138:139] offset:6144\n\t"
"v_mfma_f32_16x16x32_bf16 v[12:15], v[96:99], a[180:183], v[12:15]\n\t"
"s_waitcnt lgkmcnt(7)\n\t"
"v_mfma_f32_16x16x32_bf16 v[12:15], a[208:211], a[184:187], v[12:15]\n\t"
"v_mfma_f32_16x16x32_bf16 v[12:15], a[212:215], a[188:191], v[12:15]\n\t"
"v_mfma_f32_16x16x32_bf16 v[16:19], a[212:215], a[192:195], v[16:19]\n\t"
"s_waitcnt lgkmcnt(6)\n\t"
"v_mfma_f32_16x16x32_bf16 v[16:19], a[208:211], a[188:191], v[16:19]\n\t"
"v_mfma_f32_16x16x32_bf16 v[16:19], v[96:99], a[184:187], v[16:19]\n\t"
"v_mfma_f32_16x16x32_bf16 v[20:23], v[96:99], a[188:191], v[20:23]\n\t"
"s_waitcnt lgkmcnt(5)\n\t"
"v_mfma_f32_16x16x32_bf16 v[20:23], a[208:211], a[192:195], v[20:23]\n\t"
"ds_read_b128 a[48:51], v252 offset:13312\n\t"
"v_mfma_f32_16x16x32_bf16 v[20:23], a[212:215], a[196:199], v[20:23]\n\t"
"s_add_u32 s29, s22, 590208\n\t"
"v_mfma_f32_16x16x32_bf16 v[24:27], a[212:215], a[200:203], v[24:27]\n\t"
"s_waitcnt lgkmcnt(4)\n\t"
"ds_read_b128 a[56:59], v252 offset:14336\n\t"
"v_mfma_f32_16x16x32_bf16 v[24:27], a[208:211], a[196:199], v[24:27]\n\t"
"v_mfma_f32_16x16x32_bf16 v[24:27], v[96:99], a[192:195], v[24:27]\n\t"
"s_waitcnt lgkmcnt(3)\n\t"
"v_mfma_f32_16x16x32_bf16 v[28:31], v[96:99], a[196:199], v[28:31]\n\t"
"buffer_load_dwordx4 v[96:99], off, s[80:83], s20 offset:1024 sc1\n\t"
"v_mfma_f32_16x16x32_bf16 v[28:31], a[208:211], a[200:203], v[28:31]\n\t"
"s_waitcnt vmcnt(35)\n\t"
"v_mfma_f32_16x16x32_bf16 v[28:31], a[212:215], a[204:207], v[28:31]\n\t"
"buffer_store_dwordx4 a[40:43], v248, s[16:19], s29 offen offset:0 nt\n\t"
"v_mfma_f32_16x16x32_bf16 v[32:35], a[216:219], a[168:171], v[32:35]\n\t"
"v_mfma_f32_16x16x32_bf16 v[32:35], a[224:227], a[176:179], v[32:35]\n\t"
"buffer_load_dwordx4 a[208:211], off, s[80:83], s20 offset:2048 sc1\n\t"
"v_mfma_f32_16x16x32_bf16 v[32:35], a[220:223], a[172:175], v[32:35]\n\t"
"s_mov_b32 s20, 1683456\n\t"
"v_mfma_f32_16x16x32_bf16 v[36:39], a[216:219], a[172:175], v[36:39]\n\t"
"buffer_load_dwordx4 a[212:215], off, s[80:83], s20 offset:0 sc1\n\t"
"v_mfma_f32_16x16x32_bf16 v[36:39], a[220:223], a[176:179], v[36:39]\n\t"
"v_mfma_f32_16x16x32_bf16 v[36:39], a[224:227], a[180:183], v[36:39]\n\t"
"v_mfma_f32_16x16x32_bf16 v[40:43], a[224:227], a[184:187], v[40:43]\n\t"
"v_mfma_f32_16x16x32_bf16 v[40:43], a[220:223], a[180:183], v[40:43]\n\t"
"v_mfma_f32_16x16x32_bf16 v[40:43], a[216:219], a[176:179], v[40:43]\n\t"
"v_mfma_f32_16x16x32_bf16 v[44:47], a[216:219], a[180:183], v[44:47]\n\t"
"v_mfma_f32_16x16x32_bf16 v[44:47], a[220:223], a[184:187], v[44:47]\n\t"
"v_mfma_f32_16x16x32_bf16 v[44:47], a[224:227], a[188:191], v[44:47]\n\t"
"s_waitcnt lgkmcnt(0)\n\t"
"s_barrier\n\t"
"v_mfma_f32_16x16x32_bf16 v[48:51], a[224:227], a[192:195], v[48:51]\n\t"
"v_mfma_f32_16x16x32_bf16 v[48:51], a[220:223], a[188:191], v[48:51]\n\t"
"buffer_load_dwordx4 a[136:139], off, s[80:83], s20 offset:1024 sc1\n\t"
"v_mfma_f32_16x16x32_bf16 v[48:51], a[216:219], a[184:187], v[48:51]\n\t"
"ds_write_b128 v249, a[16:19] offset:12288\n\t"
"v_mfma_f32_16x16x32_bf16 v[52:55], a[216:219], a[188:191], v[52:55]\n\t"
"v_mfma_f32_16x16x32_bf16 v[52:55], a[220:223], a[192:195], v[52:55]\n\t"
"buffer_load_dwordx4 a[12:15], off, s[80:83], s20 offset:2048 sc1\n\t"
"v_mfma_f32_16x16x32_bf16 v[52:55], a[224:227], a[196:199], v[52:55]\n\t"
"buffer_store_dwordx4 a[48:51], v248, s[16:19], s29 offen offset:128 nt\n\t"
"ds_write_b64 v250, a[144:145] offset:12288\n\t"
"s_mov_b32 s20, 0\n\t"
"v_mfma_f32_16x16x32_bf16 v[56:59], a[224:227], a[200:203], v[56:59]\n\t"
"Lpg12_%=:\n\t"
"s_cbranch_vccnz Lvr0_%=\n\t"
"v_mfma_f32_16x16x32_bf16 v[56:59], a[220:223], a[196:199], v[56:59]\n\t"
"v_mfma_f32_16x16x32_bf16 v[56:59], a[216:219], a[192:195], v[56:59]\n\t"
"ds_write_b128 v249, a[20:23] offset:18432\n\t"
"v_mfma_f32_16x16x32_bf16 v[60:63], a[216:219], a[196:199], v[60:63]\n\t"
"buffer_load_dwordx4 a[216:219], off, s[80:83], s20 offset:0 sc1\n\t"
"v_mfma_f32_16x16x32_bf16 v[60:63], a[220:223], a[200:203], v[60:63]\n\t"
"s_waitcnt vmcnt(39)\n\t"
"v_mfma_f32_16x16x32_bf16 v[60:63], a[224:227], a[204:207], v[60:63]\n\t"
"ds_write_b64 v250, a[146:147] offset:18432\n\t"
"v_mfma_f32_16x16x32_bf16 v[64:67], a[228:231], a[168:171], v[64:67]\n\t"
"buffer_load_dwordx4 a[220:223], off, s[80:83], s20 offset:1024 sc1\n\t"
"v_mfma_f32_16x16x32_bf16 v[64:67], a[236:239], a[176:179], v[64:67]\n\t"
"ds_read_b128 a[168:171], v222 offset:3328\n\t"
"v_mfma_f32_16x16x32_bf16 v[64:67], a[232:235], a[172:175], v[64:67]\n\t"
"v_mfma_f32_16x16x32_bf16 v[68:71], a[228:231], a[172:175], v[68:71]\n\t"
"buffer_load_dwordx4 a[224:227], off, s[80:83], s20 offset:2048 sc1\n\t"
"v_mfma_f32_16x16x32_bf16 v[68:71], a[232:235], a[176:179], v[68:71]\n\t"
"ds_read_b128 a[172:175], v223 offset:9472\n\t"
"v_mfma_f32_16x16x32_bf16 v[68:71], a[236:239], a[180:183], v[68:71]\n\t"
"ds_read_b128 a[40:43], v252 offset:0\n\t"
"v_mfma_f32_16x16x32_bf16 v[72:75], a[236:239], a[184:187], v[72:75]\n\t"
"v_mfma_f32_16x16x32_bf16 v[72:75], a[232:235], a[180:183], v[72:75]\n\t"
"v_mfma_f32_16x16x32_bf16 v[72:75], a[228:231], a[176:179], v[72:75]\n\t"
"ds_read_b128 a[176:179], v223 offset:15616\n\t"
"v_mfma_f32_16x16x32_bf16 v[76:79], a[228:231], a[180:183], v[76:79]\n\t"
"ds_read_b128 a[180:183], v222 offset:23296\n\t"
"v_mfma_f32_16x16x32_bf16 v[76:79], a[232:235], a[184:187], v[76:79]\n\t"
"ds_read_b128 a[48:51], v252 offset:1024\n\t"
"v_mfma_f32_16x16x32_bf16 v[76:79], a[236:239], a[188:191], v[76:79]\n\t"
"buffer_store_dwordx4 a[56:59], v248, s[16:19], s29 offen offset:256 nt\n\t"
"v_mfma_f32_16x16x32_bf16 v[80:83], a[236:239], a[192:195], v[80:83]\n\t"
"ds_read_b128 a[64:67], v252 offset:2048\n\t"
"v_mfma_f32_16x16x32_bf16 v[80:83], a[232:235], a[188:191], v[80:83]\n\t"
"v_mfma_f32_16x16x32_bf16 v[80:83], a[228:231], a[184:187], v[80:83]\n\t"
"ds_read_b128 a[184:187], v223 offset:29440\n\t"
"v_mfma_f32_16x16x32_bf16 v[84:87], a[228:231], a[188:191], v[84:87]\n\t"
"s_mov_b32 s20, 12288\n\t"
"ds_read_b128 a[188:191], v223 offset:35584\n\t"
"v_mfma_f32_16x16x32_bf16 v[84:87], a[232:235], a[192:195], v[84:87]\n\t"
"s_add_u32 s29, s22, 786816\n\t"
"v_mfma_f32_16x16x32_bf16 v[84:87], a[236:239], a[196:199], v[84:87]\n\t"
"v_mfma_f32_16x16x32_bf16 v[88:91], a[236:239], a[200:203], v[88:91]\n\t"
"v_mfma_f32_16x16x32_bf16 v[88:91], a[232:235], a[196:199], v[88:91]\n\t"
"s_waitcnt lgkmcnt(6)\n\t"
"v_mfma_f32_16x16x32_bf16 v[88:91], a[228:231], a[192:195], v[88:91]\n\t"
"ds_read_b128 a[192:195], v222 offset:43264\n\t"
"v_mfma_f32_16x16x32_bf16 v[92:95], a[228:231], a[196:199], v[92:95]\n\t"
"buffer_load_dwordx4 a[228:231], off, s[80:83], s20 offset:0 sc1\n\t"
"v_mfma_f32_16x16x32_bf16 v[92:95], a[232:235], a[200:203], v[92:95]\n\t"
"s_waitcnt vmcnt(40) lgkmcnt(6)\n\t"
"ds_read_b128 a[196:199], v223 offset:49408\n\t"
"v_mfma_f32_16x16x32_bf16 v[92:95], a[236:239], a[204:207], v[92:95]\n\t"
"ds_read_b128 a[200:203], v223 offset:55552\n\t"
"v_mfma_f32_16x16x32_bf16 v[100:103], a[240:243], a[168:171], v[100:103]\n\t"
"s_waitcnt lgkmcnt(7)\n\t"
"ds_read_b128 a[204:207], v222 offset:63232\n\t"
"v_mfma_f32_16x16x32_bf16 v[100:103], a[248:251], a[176:179], v[100:103]\n\t"
"buffer_load_dwordx4 a[232:235], off, s[80:83], s20 offset:1024 sc1\n\t"
"v_mfma_f32_16x16x32_bf16 v[100:103], a[244:247], a[172:175], v[100:103]\n\t"
"buffer_store_dwordx4 a[40:43], v248, s[16:19], s29 offen offset:0 nt\n\t"
"v_mfma_f32_16x16x32_bf16 v[104:107], a[240:243], a[172:175], v[104:107]\n\t"
"s_waitcnt lgkmcnt(5)\n\t"
"v_mfma_f32_16x16x32_bf16 v[104:107], a[244:247], a[176:179], v[104:107]\n\t"
"v_mfma_f32_16x16x32_bf16 v[104:107], a[248:251], a[180:183], v[104:107]\n\t"
"s_waitcnt lgkmcnt(0)\n\t"
"s_barrier\n\t"
"v_mfma_f32_16x16x32_bf16 v[108:111], a[248:251], a[184:187], v[108:111]\n\t"
"v_mfma_f32_16x16x32_bf16 v[108:111], a[244:247], a[180:183], v[108:111]\n\t"
"buffer_load_dwordx4 a[236:239], off, s[80:83], s20 offset:2048 sc1\n\t"
"v_mfma_f32_16x16x32_bf16 v[108:111], a[240:243], a[176:179], v[108:111]\n\t"
"s_mov_b32 s20, 24576\n\t"
"ds_write_b128 v249, a[24:27] offset:0\n\t"
"v_mfma_f32_16x16x32_bf16 v[112:115], a[240:243], a[180:183], v[112:115]\n\t"
"buffer_load_dwordx4 a[16:19], off, s[80:83], s20 offset:0 sc1\n\t"
"v_mfma_f32_16x16x32_bf16 v[112:115], a[244:247], a[184:187], v[112:115]\n\t"
"buffer_load_dwordx4 a[144:147], off, s[80:83], s20 offset:1024 sc1\n\t"
"v_mfma_f32_16x16x32_bf16 v[112:115], a[248:251], a[188:191], v[112:115]\n\t"
"ds_write_b64 v250, a[152:153] offset:0\n\t"
"v_mfma_f32_16x16x32_bf16 v[116:119], a[248:251], a[192:195], v[116:119]\n\t"
"v_mfma_f32_16x16x32_bf16 v[116:119], a[244:247], a[188:191], v[116:119]\n\t"
"buffer_load_dwordx4 a[20:23], off, s[80:83], s20 offset:2048 sc1\n\t"
"v_mfma_f32_16x16x32_bf16 v[116:119], a[240:243], a[184:187], v[116:119]\n\t"
"buffer_store_dwordx4 a[48:51], v248, s[16:19], s29 offen offset:128 nt\n\t"
"ds_write_b64 v250, a[154:155] offset:6144\n\t"
"v_mfma_f32_16x16x32_bf16 v[120:123], a[240:243], a[188:191], v[120:123]\n\t"
"s_mov_b32 s20, 184320\n\t"
"v_mfma_f32_16x16x32_bf16 v[120:123], a[244:247], a[192:195], v[120:123]\n\t"
"v_mfma_f32_16x16x32_bf16 v[120:123], a[248:251], a[196:199], v[120:123]\n\t"
"ds_write_b128 v249, a[28:31] offset:6144\n\t"
"v_mfma_f32_16x16x32_bf16 v[124:127], a[248:251], a[200:203], v[124:127]\n\t"
"v_mfma_f32_16x16x32_bf16 v[124:127], a[244:247], a[196:199], v[124:127]\n\t"
"v_mfma_f32_16x16x32_bf16 v[124:127], a[240:243], a[192:195], v[124:127]\n\t"
"v_mfma_f32_16x16x32_bf16 v[128:131], a[240:243], a[196:199], v[128:131]\n\t"
"buffer_load_dwordx4 a[240:243], off, s[80:83], s20 offset:0 sc1\n\t"
"v_mfma_f32_16x16x32_bf16 v[128:131], a[244:247], a[200:203], v[128:131]\n\t"
"s_waitcnt vmcnt(45)\n\t"
"v_mfma_f32_16x16x32_bf16 v[128:131], a[248:251], a[204:207], v[128:131]\n\t"
"v_mfma_f32_16x16x32_bf16 v[132:135], a[252:255], a[168:171], v[132:135]\n\t"
"buffer_load_dwordx4 a[244:247], off, s[80:83], s20 offset:1024 sc1\n\t"
"v_mfma_f32_16x16x32_bf16 v[132:135], v[196:199], a[176:179], v[132:135]\n\t"
"v_mfma_f32_16x16x32_bf16 v[132:135], a[72:75], a[172:175], v[132:135]\n\t"
"ds_read_b128 a[40:43], v252 offset:12288\n\t"
"v_mfma_f32_16x16x32_bf16 v[136:139], a[252:255], a[172:175], v[136:139]\n\t"
"buffer_load_dwordx4 a[248:251], off, s[80:83], s20 offset:2048 sc1\n\t"
"v_mfma_f32_16x16x32_bf16 v[136:139], a[72:75], a[176:179], v[136:139]\n\t"
"v_mfma_f32_16x16x32_bf16 v[136:139], v[196:199], a[180:183], v[136:139]\n\t"
"buffer_store_dwordx4 a[64:67], v248, s[16:19], s29 offen offset:256 nt\n\t"
"v_mfma_f32_16x16x32_bf16 v[140:143], v[196:199], a[184:187], v[140:143]\n\t"
"v_mfma_f32_16x16x32_bf16 v[140:143], a[72:75], a[180:183], v[140:143]\n\t"
"v_mfma_f32_16x16x32_bf16 v[140:143], a[252:255], a[176:179], v[140:143]\n\t"
"v_mfma_f32_16x16x32_bf16 v[144:147], a[252:255], a[180:183], v[144:147]\n\t"
"v_mfma_f32_16x16x32_bf16 v[144:147], a[72:75], a[184:187], v[144:147]\n\t"
"v_mfma_f32_16x16x32_bf16 v[144:147], v[196:199], a[188:191], v[144:147]\n\t"
"v_mfma_f32_16x16x32_bf16 v[148:151], v[196:199], a[192:195], v[148:151]\n\t"
"v_mfma_f32_16x16x32_bf16 v[148:151], a[72:75], a[188:191], v[148:151]\n\t"
"v_mfma_f32_16x16x32_bf16 v[148:151], a[252:255], a[184:187], v[148:151]\n\t"
"s_add_u32 s29, s22, 983424\n\t"
"v_mfma_f32_16x16x32_bf16 v[152:155], a[252:255], a[188:191], v[152:155]\n\t"
"s_mov_b32 s20, 196608\n\t"
"v_mfma_f32_16x16x32_bf16 v[152:155], a[72:75], a[192:195], v[152:155]\n\t"
"ds_read_b128 a[48:51], v252 offset:13312\n\t"
"v_mfma_f32_16x16x32_bf16 v[152:155], v[196:199], a[196:199], v[152:155]\n\t"
"s_waitcnt lgkmcnt(1)\n\t"
"v_mfma_f32_16x16x32_bf16 v[156:159], v[196:199], a[200:203], v[156:159]\n\t"
"ds_read_b128 a[56:59], v252 offset:14336\n\t"
"v_mfma_f32_16x16x32_bf16 v[156:159], a[72:75], a[196:199], v[156:159]\n\t"
"v_mfma_f32_16x16x32_bf16 v[156:159], a[252:255], a[192:195], v[156:159]\n\t"
"buffer_store_dwordx4 a[40:43], v248, s[16:19], s29 offen offset:0 nt\n\t"
"v_mfma_f32_16x16x32_bf16 v[160:163], a[252:255], a[196:199], v[160:163]\n\t"
"s_waitcnt vmcnt(46)\n\t"
"v_mfma_f32_16x16x32_bf16 v[160:163], a[72:75], a[200:203], v[160:163]\n\t"
"buffer_load_dwordx4 a[252:255], off, s[80:83], s20 offset:0 sc1\n\t"
"v_mfma_f32_16x16x32_bf16 v[160:163], v[196:199], a[204:207], v[160:163]\n\t"
"global_load_dwordx4 v[240:243], v252, s[48:49] offset:2048\n\t"
"v_mfma_f32_16x16x32_bf16 v[164:167], a[104:107], a[168:171], v[164:167]\n\t"
"ds_read_b128 a[168:171], v222 offset:1536\n\t"
"v_mfma_f32_16x16x32_bf16 v[164:167], a[80:83], a[176:179], v[164:167]\n\t"
"v_mfma_f32_16x16x32_bf16 v[164:167], v[200:203], a[172:175], v[164:167]\n\t"
"v_mfma_f32_16x16x32_bf16 v[168:171], a[104:107], a[172:175], v[168:171]\n\t"
"ds_read_b128 a[172:175], v223 offset:7680\n\t"
"v_mfma_f32_16x16x32_bf16 v[168:171], v[200:203], a[176:179], v[168:171]\n\t"
"global_load_dwordx4 a[8:11], v252, s[46:47] offset:1024\n\t"
"v_mfma_f32_16x16x32_bf16 v[168:171], a[80:83], a[180:183], v[168:171]\n\t"
"v_mfma_f32_16x16x32_bf16 v[172:175], a[80:83], a[184:187], v[172:175]\n\t"
"v_mfma_f32_16x16x32_bf16 v[172:175], v[200:203], a[180:183], v[172:175]\n\t"
"v_mfma_f32_16x16x32_bf16 v[172:175], a[104:107], a[176:179], v[172:175]\n\t"
"ds_read_b128 a[176:179], v223 offset:13824\n\t"
"v_mfma_f32_16x16x32_bf16 v[176:179], a[104:107], a[180:183], v[176:179]\n\t"
"s_waitcnt lgkmcnt(4)\n\t"
"ds_read_b128 a[180:183], v222 offset:21504\n\t"
"v_mfma_f32_16x16x32_bf16 v[176:179], v[200:203], a[184:187], v[176:179]\n\t"
"v_mfma_f32_16x16x32_bf16 v[176:179], a[80:83], a[188:191], v[176:179]\n\t"
"s_waitcnt lgkmcnt(4)\n\t"
"s_barrier\n\t"
"v_mfma_f32_16x16x32_bf16 v[180:183], a[80:83], a[192:195], v[180:183]\n\t"
"v_mfma_f32_16x16x32_bf16 v[180:183], v[200:203], a[188:191], v[180:183]\n\t"
"buffer_load_dwordx4 a[24:27], off, s[80:83], s20 offset:1024 sc1\n\t"
"v_mfma_f32_16x16x32_bf16 v[180:183], a[104:107], a[184:187], v[180:183]\n\t"
"ds_read_b128 a[184:187], v223 offset:27648\n\t"
"v_mfma_f32_16x16x32_bf16 v[184:187], a[104:107], a[188:191], v[184:187]\n\t"
"ds_read_b128 a[188:191], v223 offset:33792\n\t"
"v_mfma_f32_16x16x32_bf16 v[184:187], v[200:203], a[192:195], v[184:187]\n\t"
"buffer_load_dwordx4 a[152:155], off, s[80:83], s20 offset:2048 sc1\n\t"
"v_mfma_f32_16x16x32_bf16 v[184:187], a[80:83], a[196:199], v[184:187]\n\t"
"buffer_store_dwordx4 a[48:51], v248, s[16:19], s29 offen offset:128 nt\n\t"
"ds_write_b64 v250, a[160:161] offset:12288\n\t"
"s_mov_b32 s20, 208896\n\t"
"v_mfma_f32_16x16x32_bf16 v[188:191], a[80:83], a[200:203], v[188:191]\n\t"
"v_mfma_f32_16x16x32_bf16 v[188:191], v[200:203], a[196:199], v[188:191]\n\t"
"buffer_load_dwordx4 a[28:31], off, s[80:83], s20 offset:0 sc1\n\t"
"v_mfma_f32_16x16x32_bf16 v[188:191], a[104:107], a[192:195], v[188:191]\n\t"
"ds_read_b128 a[192:195], v222 offset:41472\n\t"
"v_mfma_f32_16x16x32_bf16 v[192:195], a[104:107], a[196:199], v[192:195]\n\t"
"s_waitcnt vmcnt(50) lgkmcnt(5)\n\t"
"ds_read_b128 a[196:199], v223 offset:47616\n\t"
"v_mfma_f32_16x16x32_bf16 v[192:195], v[200:203], a[200:203], v[192:195]\n\t"
"buffer_load_dwordx4 a[104:107], off, s[80:83], s20 offset:1024 sc1\n\t"
"v_mfma_f32_16x16x32_bf16 v[192:195], a[80:83], a[204:207], v[192:195]\n\t"
"ds_write_b128 v249, a[32:35] offset:12288\n\t"
"Lst17_%=:\n\t"
"v_mfma_f32_16x16x32_bf16 v[0:3], a[88:91], a[168:171], v[0:3]\n\t"
"s_waitcnt lgkmcnt(6)\n\t"
"v_mfma_f32_16x16x32_bf16 v[0:3], a[60:63], a[176:179], v[0:3]\n\t"
"ds_read_b128 a[200:203], v223 offset:53760\n\t"
"v_mfma_f32_16x16x32_bf16 v[0:3], a[96:99], a[172:175], v[0:3]\n\t"
"buffer_load_dwordx4 v[200:203], off, s[80:83], s20 offset:2048 sc1\n\t"
"v_mfma_f32_16x16x32_bf16 v[4:7], a[88:91], a[172:175], v[4:7]\n\t"
"s_waitcnt lgkmcnt(6)\n\t"
"ds_read_b128 a[204:207], v222 offset:61440\n\t"
"v_mfma_f32_16x16x32_bf16 v[4:7], a[96:99], a[176:179], v[4:7]\n\t"
"ds_read_b128 a[108:111], v252 offset:0\n\t"
"s_waitcnt vmcnt(7)\n\t"
"v_add_u32 v240, 59904, v240\n\t"
"v_mfma_f32_16x16x32_bf16 v[4:7], a[60:63], a[180:183], v[4:7]\n\t"
"ds_write_b128 v249, a[36:39] offset:18432\n\t"
"v_mfma_f32_16x16x32_bf16 v[8:11], a[60:63], a[184:187], v[8:11]\n\t"
"s_waitcnt lgkmcnt(8)\n\t"
"v_add_u32 v241, 59904, v241\n\t"
"v_cvt_pk_bf16_f32 v0, v0, v1\n\t"
"v_mfma_f32_16x16x32_bf16 v[8:11], a[96:99], a[180:183], v[8:11]\n\t"
"v_cvt_pk_bf16_f32 v2, v2, v3\n\t"
"v_accvgpr_write_b32 a40, v0\n\t"
"v_mfma_f32_16x16x32_bf16 v[8:11], a[88:91], a[176:179], v[8:11]\n\t"
"ds_read_b64_tr_b16 v[228:229], v240 offset:20480\n\t"
"v_accvgpr_write_b32 a41, v2\n\t"
"v_mfma_f32_16x16x32_bf16 v[12:15], a[88:91], a[180:183], v[12:15]\n\t"
"s_waitcnt lgkmcnt(7)\n\t"
"ds_read_b64_tr_b16 v[230:231], v240 offset:22016\n\t"
"v_cvt_pk_bf16_f32 v4, v4, v5\n\t"
"v_mfma_f32_16x16x32_bf16 v[12:15], a[96:99], a[184:187], v[12:15]\n\t"
"ds_read_b64_tr_b16 v[236:237], v241 offset:20480\n\t"
"v_cvt_pk_bf16_f32 v6, v6, v7\n\t"
"v_mfma_f32_16x16x32_bf16 v[12:15], a[60:63], a[188:191], v[12:15]\n\t"
"buffer_store_dwordx4 a[56:59], v248, s[16:19], s29 offen offset:256 nt\n\t"
"ds_read_b64_tr_b16 v[238:239], v241 offset:22016\n\t"
"ds_read_b64_tr_b16 v[232:233], v240 offset:40448\n\t"
"v_accvgpr_write_b32 a48, v4\n\t"
"v_mfma_f32_16x16x32_bf16 v[16:19], a[60:63], a[192:195], v[16:19]\n\t"
"s_waitcnt lgkmcnt(10)\n\t"
"ds_read_b64_tr_b16 v[234:235], v240 offset:41984\n\t"
"v_cvt_pk_bf16_f32 v8, v8, v9\n\t"
"v_mfma_f32_16x16x32_bf16 v[16:19], a[96:99], a[188:191], v[16:19]\n\t"
"s_add_u32 s29, s22, 1180032\n\t"
"ds_read_b64_tr_b16 v[224:225], v241 offset:40448\n\t"
"v_cvt_pk_bf16_f32 v10, v10, v11\n\t"
"v_mfma_f32_16x16x32_bf16 v[16:19], a[88:91], a[184:187], v[16:19]\n\t"
"ds_read_b64_tr_b16 v[226:227], v241 offset:41984\n\t"
"v_accvgpr_write_b32 a49, v6\n\t"
"v_mfma_f32_16x16x32_bf16 v[20:23], a[88:91], a[188:191], v[20:23]\n\t"
"s_waitcnt lgkmcnt(11)\n\t"
"v_cvt_pk_bf16_f32 v12, v12, v13\n\t"
"v_cvt_pk_bf16_f32 v14, v14, v15\n\t"
"v_mfma_f32_16x16x32_bf16 v[20:23], a[96:99], a[192:195], v[20:23]\n\t"
"s_waitcnt lgkmcnt(9)\n\t"
"ds_read_b128 a[116:119], v252 offset:1024\n\t"
"v_accvgpr_write_b32 a56, v8\n\t"
"v_mfma_f32_16x16x32_bf16 v[20:23], a[60:63], a[196:199], v[20:23]\n\t"
"ds_write_b64 v250, a[162:163] offset:18432\n\t"
"v_mfma_f32_16x16x32_bf16 v[24:27], a[60:63], a[200:203], v[24:27]\n\t"
"v_accvgpr_write_b32 a57, v10\n\t"
"v_cvt_pk_bf16_f32 v16, v16, v17\n\t"
"v_mfma_f32_16x16x32_bf16 v[24:27], a[96:99], a[196:199], v[24:27]\n\t"
"v_cvt_pk_bf16_f32 v18, v18, v19\n\t"
"v_accvgpr_write_b32 a64, v12\n\t"
"v_mfma_f32_16x16x32_bf16 v[24:27], a[88:91], a[192:195], v[24:27]\n\t"
"v_accvgpr_write_b32 a65, v14\n\t"
"v_accvgpr_write_b32 a72, v16\n\t"
"v_mfma_f32_16x16x32_bf16 v[28:31], a[88:91], a[196:199], v[28:31]\n\t"
"s_waitcnt vmcnt(7) lgkmcnt(8)\n\t"
"v_cvt_pk_bf16_f32 v20, v20, v21\n\t"
"v_cvt_pk_bf16_f32 v22, v22, v23\n\t"
"v_mfma_f32_16x16x32_bf16 v[28:31], a[96:99], a[200:203], v[28:31]\n\t"
"s_waitcnt lgkmcnt(6)\n\t"
"ds_read_b128 a[124:127], v252 offset:2048\n\t"
"v_accvgpr_write_b32 a73, v18\n\t"
"v_mfma_f32_16x16x32_bf16 v[28:31], a[60:63], a[204:207], v[28:31]\n\t"
"s_waitcnt lgkmcnt(5)\n\t"
"v_accvgpr_write_b32 a80, v20\n\t"
"v_accvgpr_write_b32 a81, v22\n\t"
"v_mfma_f32_16x16x32_bf16 v[228:231], v[228:231], a[8:11], 0\n\t"
"s_waitcnt lgkmcnt(3)\n\t"
"v_cvt_pk_bf16_f32 v24, v24, v25\n\t"
"v_cvt_pk_bf16_f32 v26, v26, v27\n\t"
"v_mfma_f32_16x16x32_bf16 v[236:239], v[236:239], a[8:11], 0\n\t"
"buffer_store_dwordx4 a[108:111], v248, s[16:19], s29 offen offset:0 nt\n\t"
"v_accvgpr_write_b32 a88, v24\n\t"
"v_accvgpr_write_b32 a89, v26\n\t"
"v_mfma_f32_16x16x32_bf16 v[232:235], v[232:235], a[8:11], 0\n\t"
"v_mfma_f32_16x16x32_bf16 v[224:227], v[224:227], a[8:11], 0\n\t"
"v_cvt_pk_bf16_f32 v28, v28, v29\n\t"
"v_cvt_pk_bf16_f32 v30, v30, v31\n\t"
"v_mfma_f32_16x16x32_bf16 v[32:35], v[204:207], a[168:171], v[32:35]\n\t"
"v_accvgpr_write_b32 a96, v28\n\t"
"v_accvgpr_write_b32 a97, v30\n\t"
"v_mfma_f32_16x16x32_bf16 v[32:35], v[208:211], a[176:179], v[32:35]\n\t"
"v_cvt_pk_bf16_f32 v228, v228, v229\n\t"
"v_cvt_pk_bf16_f32 v229, v230, v231\n\t"
"v_mfma_f32_16x16x32_bf16 v[32:35], a[112:115], a[172:175], v[32:35]\n\t"
"v_cvt_pk_bf16_f32 v230, v236, v237\n\t"
"v_cvt_pk_bf16_f32 v231, v238, v239\n\t"
"v_mfma_f32_16x16x32_bf16 v[36:39], v[204:207], a[172:175], v[36:39]\n\t"
"s_waitcnt lgkmcnt(2)\n\t"
"v_cvt_pk_bf16_f32 v232, v232, v233\n\t"
"v_cvt_pk_bf16_f32 v233, v234, v235\n\t"
"v_mfma_f32_16x16x32_bf16 v[36:39], a[112:115], a[176:179], v[36:39]\n\t"
"v_cvt_pk_bf16_f32 v234, v224, v225\n\t"
"v_cvt_pk_bf16_f32 v235, v226, v227\n\t"
"v_mfma_f32_16x16x32_bf16 v[36:39], v[208:211], a[180:183], v[36:39]\n\t"
"ds_write_b128 v242, v[228:231] offset:30720\n\t"
"v_mfma_f32_16x16x32_bf16 v[40:43], v[208:211], a[184:187], v[40:43]\n\t"
"v_cvt_pk_bf16_f32 v32, v32, v33\n\t"
"v_cvt_pk_bf16_f32 v34, v34, v35\n\t"
"v_mfma_f32_16x16x32_bf16 v[40:43], a[112:115], a[180:183], v[40:43]\n\t"
"s_mov_b32 s20, 294912\n\t"
"v_accvgpr_write_b32 a42, v32\n\t"
"v_accvgpr_write_b32 a43, v34\n\t"
"v_mfma_f32_16x16x32_bf16 v[40:43], v[204:207], a[176:179], v[40:43]\n\t"
"ds_write_b128 v242, v[232:235] offset:32256\n\t"
"v_mfma_f32_16x16x32_bf16 v[44:47], v[204:207], a[180:183], v[44:47]\n\t"
"v_cvt_pk_bf16_f32 v36, v36, v37\n\t"
"v_cvt_pk_bf16_f32 v38, v38, v39\n\t"
"v_mfma_f32_16x16x32_bf16 v[44:47], a[112:115], a[184:187], v[44:47]\n\t"
"v_accvgpr_write_b32 a50, v36\n\t"
"v_mfma_f32_16x16x32_bf16 v[44:47], v[208:211], a[188:191], v[44:47]\n\t"
"s_waitcnt lgkmcnt(2)\n\t"
"s_barrier\n\t"
"v_mfma_f32_16x16x32_bf16 v[48:51], v[208:211], a[192:195], v[48:51]\n\t"
"ds_read_b64_tr_b16 v[236:237], v240 offset:512\n\t"
"v_mfma_f32_16x16x32_bf16 v[48:51], a[112:115], a[188:191], v[48:51]\n\t"
"ds_read_b64_tr_b16 v[238:239], v240 offset:2048\n\t"
"v_cvt_pk_bf16_f32 v40, v40, v41\n\t"
"v_mfma_f32_16x16x32_bf16 v[48:51], v[204:207], a[184:187], v[48:51]\n\t"
"buffer_store_dwordx4 a[116:119], v248, s[16:19], s29 offen offset:128 nt\n\t"
"buffer_load_dwordx4 a[32:35], off, s[80:83], s20 offset:0 sc1\n\t"
"ds_read_b64_tr_b16 v[224:225], v241 offset:512\n\t"
"v_mfma_f32_16x16x32_bf16 v[52:55], v[204:207], a[188:191], v[52:55]\n\t"
"ds_read_b64_tr_b16 v[226:227], v241 offset:2048\n\t"
"v_cvt_pk_bf16_f32 v42, v42, v43\n\t"
"v_mfma_f32_16x16x32_bf16 v[52:55], a[112:115], a[192:195], v[52:55]\n\t"
"v_accvgpr_write_b32 a51, v38\n\t"
"v_cvt_pk_bf16_f32 v44, v44, v45\n\t"
"v_mfma_f32_16x16x32_bf16 v[52:55], v[208:211], a[196:199], v[52:55]\n\t"
"v_cvt_pk_bf16_f32 v46, v46, v47\n\t"
"v_accvgpr_write_b32 a58, v40\n\t"
"v_mfma_f32_16x16x32_bf16 v[56:59], v[208:211], a[200:203], v[56:59]\n\t"
"v_accvgpr_write_b32 a59, v42\n\t"
"v_cvt_pk_bf16_f32 v48, v48, v49\n\t"
"v_mfma_f32_16x16x32_bf16 v[56:59], a[112:115], a[196:199], v[56:59]\n\t"
"ds_read_b128 a[148:151], v252 offset:12288\n\t"
"v_cvt_pk_bf16_f32 v50, v50, v51\n\t"
"v_mfma_f32_16x16x32_bf16 v[56:59], v[204:207], a[192:195], v[56:59]\n\t"
"buffer_load_dwordx4 a[160:163], off, s[80:83], s20 offset:1024 sc1\n\t"
"v_mfma_f32_16x16x32_bf16 v[60:63], v[204:207], a[196:199], v[60:63]\n\t"
"buffer_load_dwordx4 a[36:39], off, s[80:83], s20 offset:2048 sc1\n\t"
"v_mfma_f32_16x16x32_bf16 v[60:63], a[112:115], a[200:203], v[60:63]\n\t"
"s_waitcnt lgkmcnt(1)\n\t"
"s_mov_b32 s20, 307200\n\t"
"v_accvgpr_write_b32 a66, v44\n\t"
"v_mfma_f32_16x16x32_bf16 v[60:63], v[208:211], a[204:207], v[60:63]\n\t"
"v_accvgpr_write_b32 a67, v46\n\t"
"v_cvt_pk_bf16_f32 v52, v52, v53\n\t"
"v_mfma_f32_16x16x32_bf16 v[236:239], v[236:239], a[8:11], 0\n\t"
"v_cvt_pk_bf16_f32 v54, v54, v55\n\t"
"v_accvgpr_write_b32 a74, v48\n\t"
"v_mfma_f32_16x16x32_bf16 v[224:227], v[224:227], a[8:11], 0\n\t"
"buffer_load_dwordx4 a[112:115], off, s[80:83], s20 offset:0 sc1\n\t"
"v_mfma_f32_16x16x32_bf16 v[64:67], a[44:47], a[168:171], v[64:67]\n\t"
"buffer_load_dwordx4 v[208:211], off, s[80:83], s20 offset:1024 sc1\n\t"
"v_mfma_f32_16x16x32_bf16 v[64:67], a[120:123], a[176:179], v[64:67]\n\t"
"ds_read_b128 a[168:171], v222 offset:1792\n\t"
"v_accvgpr_write_b32 a75, v50\n\t"
"v_mfma_f32_16x16x32_bf16 v[64:67], v[212:215], a[172:175], v[64:67]\n\t"
"buffer_store_dwordx4 a[124:127], v248, s[16:19], s29 offen offset:256 nt\n\t"
"v_cvt_pk_bf16_f32 v56, v56, v57\n\t"
"v_cvt_pk_bf16_f32 v58, v58, v59\n\t"
"v_accvgpr_write_b32 a82, v52\n\t"
"v_accvgpr_write_b32 a83, v54\n\t"
"v_mfma_f32_16x16x32_bf16 v[68:71], a[44:47], a[172:175], v[68:71]\n\t"
"ds_read_b128 a[172:175], v223 offset:7936\n\t"
"v_cvt_pk_bf16_f32 v236, v236, v237\n\t"
"v_mfma_f32_16x16x32_bf16 v[68:71], v[212:215], a[176:179], v[68:71]\n\t"
"v_cvt_pk_bf16_f32 v237, v238, v239\n\t"
"v_cvt_pk_bf16_f32 v238, v224, v225\n\t"
"v_mfma_f32_16x16x32_bf16 v[68:71], a[120:123], a[180:183], v[68:71]\n\t"
"s_add_u32 s29, s22, 1376640\n\t"
"v_cvt_pk_bf16_f32 v239, v226, v227\n\t"
"v_subrev_u32 v240, 59904, v240\n\t"
"v_mfma_f32_16x16x32_bf16 v[72:75], a[120:123], a[184:187], v[72:75]\n\t"
"v_subrev_u32 v241, 59904, v241\n\t"
"v_cvt_pk_bf16_f32 v60, v60, v61\n\t"
"v_mfma_f32_16x16x32_bf16 v[72:75], v[212:215], a[180:183], v[72:75]\n\t"
"v_cvt_pk_bf16_f32 v62, v62, v63\n\t"
"v_accvgpr_write_b32 a90, v56\n\t"
"v_mfma_f32_16x16x32_bf16 v[72:75], a[44:47], a[176:179], v[72:75]\n\t"
"s_waitcnt lgkmcnt(2)\n\t"
"ds_write_b128 v242, v[236:239] offset:29184\n\t"
"v_mfma_f32_16x16x32_bf16 v[76:79], a[44:47], a[180:183], v[76:79]\n\t"
"v_accvgpr_write_b32 a91, v58\n\t"
"v_accvgpr_write_b32 a98, v60\n\t"
"v_mfma_f32_16x16x32_bf16 v[76:79], v[212:215], a[184:187], v[76:79]\n\t"
"ds_read_b128 a[176:179], v223 offset:14080\n\t"
"v_mfma_f32_16x16x32_bf16 v[76:79], a[120:123], a[188:191], v[76:79]\n\t"
"ds_read_b128 a[180:183], v222 offset:21760\n\t"
"v_accvgpr_write_b32 a99, v62\n\t"
"v_mfma_f32_16x16x32_bf16 v[80:83], a[120:123], a[192:195], v[80:83]\n\t"
"ds_read_b64_tr_b16 v[228:229], v240 offset:512\n\t"
"v_cvt_pk_bf16_f32 v64, v64, v65\n\t"
"v_mfma_f32_16x16x32_bf16 v[80:83], v[212:215], a[188:191], v[80:83]\n\t"
"ds_read_b64_tr_b16 v[230:231], v240 offset:2048\n\t"
"v_cvt_pk_bf16_f32 v66, v66, v67\n\t"
"v_mfma_f32_16x16x32_bf16 v[80:83], a[44:47], a[184:187], v[80:83]\n\t"
"ds_read_b64_tr_b16 v[232:233], v241 offset:512\n\t"
"v_cvt_pk_bf16_f32 v68, v68, v69\n\t"
"v_mfma_f32_16x16x32_bf16 v[84:87], a[44:47], a[188:191], v[84:87]\n\t"
"ds_read_b64_tr_b16 v[234:235], v241 offset:2048\n\t"
"v_cvt_pk_bf16_f32 v70, v70, v71\n\t"
"v_mfma_f32_16x16x32_bf16 v[84:87], v[212:215], a[192:195], v[84:87]\n\t"
"ds_read_b128 a[184:187], v223 offset:27904\n\t"
"v_accvgpr_write_b32 a108, v64\n\t"
"v_mfma_f32_16x16x32_bf16 v[84:87], a[120:123], a[196:199], v[84:87]\n\t"
"buffer_store_dwordx4 a[148:151], v248, s[16:19], s29 offen offset:0 nt\n\t"
"ds_read_b128 a[60:63], v252 offset:13312\n\t"
"ds_read_b128 a[188:191], v223 offset:34048\n\t"
"v_accvgpr_write_b32 a109, v66\n\t"
"v_mfma_f32_16x16x32_bf16 v[88:91], a[120:123], a[200:203], v[88:91]\n\t"
"ds_read_b128 a[100:103], v252 offset:14336\n\t"
"v_cvt_pk_bf16_f32 v72, v72, v73\n\t"
"v_mfma_f32_16x16x32_bf16 v[88:91], v[212:215], a[196:199], v[88:91]\n\t"
"v_cvt_pk_bf16_f32 v74, v74, v75\n\t"
"v_accvgpr_write_b32 a116, v68\n\t"
"v_mfma_f32_16x16x32_bf16 v[88:91], a[44:47], a[192:195], v[88:91]\n\t"
"ds_read_b128 a[192:195], v222 offset:41728\n\t"
"v_accvgpr_write_b32 a117, v70\n\t"
"v_mfma_f32_16x16x32_bf16 v[92:95], a[44:47], a[196:199], v[92:95]\n\t"
"s_waitcnt lgkmcnt(7)\n\t"
"ds_read_b128 a[196:199], v223 offset:47872\n\t"
"v_cvt_pk_bf16_f32 v76, v76, v77\n\t"
"v_mfma_f32_16x16x32_bf16 v[92:95], v[212:215], a[200:203], v[92:95]\n\t"
"s_waitcnt lgkmcnt(6)\n\t"
"ds_read_b128 a[200:203], v223 offset:54016\n\t"
"v_cvt_pk_bf16_f32 v78, v78, v79\n\t"
"v_mfma_f32_16x16x32_bf16 v[92:95], a[120:123], a[204:207], v[92:95]\n\t"
"ds_read_b128 a[204:207], v222 offset:61696\n\t"
"v_accvgpr_write_b32 a124, v72\n\t"
"v_mfma_f32_16x16x32_bf16 v[228:231], v[228:231], a[8:11], 0\n\t"
"s_waitcnt lgkmcnt(6)\n\t"
"v_accvgpr_write_b32 a125, v74\n\t"
"v_cvt_pk_bf16_f32 v80, v80, v81\n\t"
"v_mfma_f32_16x16x32_bf16 v[232:235], v[232:235], a[8:11], 0\n\t"
"v_cvt_pk_bf16_f32 v82, v82, v83\n\t"
"v_accvgpr_write_b32 a132, v76\n\t"
"v_mfma_f32_16x16x32_bf16 v[100:103], v[216:219], a[168:171], v[100:103]\n\t"
"v_accvgpr_write_b32 a133, v78\n\t"
"v_cvt_pk_bf16_f32 v84, v84, v85\n\t"
"v_mfma_f32_16x16x32_bf16 v[100:103], a[128:131], a[176:179], v[100:103]\n\t"
"v_cvt_pk_bf16_f32 v86, v86, v87\n\t"
"v_accvgpr_write_b32 a140, v80\n\t"
"v_mfma_f32_16x16x32_bf16 v[100:103], a[0:3], a[172:175], v[100:103]\n\t"
"buffer_load_dwordx4 a[120:123], off, s[80:83], s20 offset:2048 sc1\n\t"
"v_mfma_f32_16x16x32_bf16 v[104:107], v[216:219], a[172:175], v[104:107]\n\t"
"v_cvt_pk_bf16_f32 v228, v228, v229\n\t"
"v_cvt_pk_bf16_f32 v229, v230, v231\n\t"
"v_mfma_f32_16x16x32_bf16 v[104:107], a[0:3], a[176:179], v[104:107]\n\t"
"v_cvt_pk_bf16_f32 v230, v232, v233\n\t"
"v_mfma_f32_16x16x32_bf16 v[104:107], a[128:131], a[180:183], v[104:107]\n\t"
"s_waitcnt lgkmcnt(0)\n\t"
"s_barrier\n\t"
"v_mfma_f32_16x16x32_bf16 v[108:111], a[128:131], a[184:187], v[108:111]\n\t"
"v_cvt_pk_bf16_f32 v231, v234, v235\n\t"
"v_mfma_f32_16x16x32_bf16 v[108:111], a[0:3], a[180:183], v[108:111]\n\t"
"v_accvgpr_write_b32 a141, v82\n\t"
"v_cvt_pk_bf16_f32 v88, v88, v89\n\t"
"v_mfma_f32_16x16x32_bf16 v[108:111], v[216:219], a[176:179], v[108:111]\n\t"
"buffer_store_dwordx4 a[60:63], v248, s[16:19], s29 offen offset:128 nt\n\t"
"ds_read_b64_tr_b16 v[224:225], v240 offset:20480\n\t"
"ds_read_b64_tr_b16 v[226:227], v240 offset:22016\n\t"
"v_cvt_pk_bf16_f32 v90, v90, v91\n\t"
"v_mfma_f32_16x16x32_bf16 v[112:115], v[216:219], a[180:183], v[112:115]\n\t"
"ds_read_b64_tr_b16 v[236:237], v241 offset:20480\n\t"
"v_accvgpr_write_b32 a148, v84\n\t"
"v_mfma_f32_16x16x32_bf16 v[112:115], a[0:3], a[184:187], v[112:115]\n\t"
"ds_read_b64_tr_b16 v[238:239], v241 offset:22016\n\t"
"v_accvgpr_write_b32 a149, v86\n\t"
"v_mfma_f32_16x16x32_bf16 v[112:115], a[128:131], a[188:191], v[112:115]\n\t"
"ds_write_b128 v242, v[228:231] offset:24576\n\t"
"v_mfma_f32_16x16x32_bf16 v[116:119], a[128:131], a[192:195], v[116:119]\n\t"
"v_cvt_pk_bf16_f32 v92, v92, v93\n\t"
"v_cvt_pk_bf16_f32 v94, v94, v95\n\t"
"v_mfma_f32_16x16x32_bf16 v[116:119], a[0:3], a[188:191], v[116:119]\n\t"
"ds_read_b64_tr_b16 v[232:233], v240 offset:40448\n\t"
"v_mfma_f32_16x16x32_bf16 v[116:119], v[216:219], a[184:187], v[116:119]\n\t"
"ds_read_b64_tr_b16 v[234:235], v240 offset:41984\n\t"
"v_accvgpr_write_b32 a156, v88\n\t"
"v_mfma_f32_16x16x32_bf16 v[120:123], v[216:219], a[188:191], v[120:123]\n\t"
"s_mov_b32 s20, 319488\n\t"
"ds_read_b64_tr_b16 v[228:229], v241 offset:40448\n\t"
"v_accvgpr_write_b32 a157, v90\n\t"
"v_mfma_f32_16x16x32_bf16 v[120:123], a[0:3], a[192:195], v[120:123]\n\t"
"ds_read_b64_tr_b16 v[230:231], v241 offset:41984\n\t"
"v_accvgpr_write_b32 a164, v92\n\t"
"v_mfma_f32_16x16x32_bf16 v[120:123], a[128:131], a[196:199], v[120:123]\n\t"
"ds_write_b128 v249, a[40:43] offset:0\n\t"
"v_mfma_f32_16x16x32_bf16 v[124:127], a[128:131], a[200:203], v[124:127]\n\t"
"v_accvgpr_write_b32 a165, v94\n\t"
"v_cvt_pk_bf16_f32 v100, v100, v101\n\t"
"v_mfma_f32_16x16x32_bf16 v[124:127], a[0:3], a[196:199], v[124:127]\n\t"
"v_cvt_pk_bf16_f32 v102, v102, v103\n\t"
"v_cvt_pk_bf16_f32 v104, v104, v105\n\t"
"v_mfma_f32_16x16x32_bf16 v[124:127], v[216:219], a[192:195], v[124:127]\n\t"
"ds_write_b128 v249, a[56:59] offset:12288\n\t"
"v_mfma_f32_16x16x32_bf16 v[128:131], v[216:219], a[196:199], v[128:131]\n\t"
"buffer_load_dwordx4 v[216:219], off, s[80:83], s20 offset:0 sc1\n\t"
"v_mfma_f32_16x16x32_bf16 v[128:131], a[0:3], a[200:203], v[128:131]\n\t"
"s_waitcnt lgkmcnt(7)\n\t"
"v_cvt_pk_bf16_f32 v106, v106, v107\n\t"
"v_accvgpr_write_b32 a44, v100\n\t"
"v_mfma_f32_16x16x32_bf16 v[128:131], a[128:131], a[204:207], v[128:131]\n\t"
"buffer_store_dwordx4 a[100:103], v248, s[16:19], s29 offen offset:256 nt\n\t"
"ds_write_b64 v250, a[108:109] offset:0\n\t"
"v_accvgpr_write_b32 a45, v102\n\t"
"v_mfma_f32_16x16x32_bf16 v[224:227], v[224:227], a[8:11], 0\n\t"
"s_waitcnt lgkmcnt(3)\n\t"
"ds_write_b64 v250, a[124:125] offset:12288\n\t"
"v_mfma_f32_16x16x32_bf16 v[236:239], v[236:239], a[8:11], 0\n\t"
"buffer_load_dwordx4 a[128:131], off, s[80:83], s20 offset:1024 sc1\n\t"
"v_mfma_f32_16x16x32_bf16 v[232:235], v[232:235], a[8:11], 0\n\t"
"ds_write_b128 v249, a[48:51] offset:6144\n\t"
"v_mfma_f32_16x16x32_bf16 v[228:231], v[228:231], a[8:11], 0\n\t"
"v_cvt_pk_bf16_f32 v108, v108, v109\n\t"
"v_cvt_pk_bf16_f32 v110, v110, v111\n\t"
"v_mfma_f32_16x16x32_bf16 v[132:135], a[4:7], a[168:171], v[132:135]\n\t"
"v_accvgpr_write_b32 a52, v104\n\t"
"v_accvgpr_write_b32 a53, v106\n\t"
"v_mfma_f32_16x16x32_bf16 v[132:135], a[208:211], a[176:179], v[132:135]\n\t"
"v_cvt_pk_bf16_f32 v112, v112, v113\n\t"
"v_cvt_pk_bf16_f32 v114, v114, v115\n\t"
"v_mfma_f32_16x16x32_bf16 v[132:135], v[96:99], a[172:175], v[132:135]\n\t"
"ds_write_b128 v249, a[64:67] offset:18432\n\t"
"v_mfma_f32_16x16x32_bf16 v[136:139], a[4:7], a[172:175], v[136:139]\n\t"
"v_cvt_pk_bf16_f32 v224, v224, v225\n\t"
"v_cvt_pk_bf16_f32 v225, v226, v227\n\t"
"v_mfma_f32_16x16x32_bf16 v[136:139], v[96:99], a[176:179], v[136:139]\n\t"
"v_accvgpr_write_b32 a60, v108\n\t"
"v_accvgpr_write_b32 a61, v110\n\t"
"v_mfma_f32_16x16x32_bf16 v[136:139], a[208:211], a[180:183], v[136:139]\n\t"
"ds_write_b64 v250, a[116:117] offset:6144\n\t"
"v_cvt_pk_bf16_f32 v226, v236, v237\n\t"
"v_cvt_pk_bf16_f32 v227, v238, v239\n\t"
"v_mfma_f32_16x16x32_bf16 v[140:143], a[208:211], a[184:187], v[140:143]\n\t"
"v_cvt_pk_bf16_f32 v232, v232, v233\n\t"
"v_cvt_pk_bf16_f32 v233, v234, v235\n\t"
"v_mfma_f32_16x16x32_bf16 v[140:143], v[96:99], a[180:183], v[140:143]\n\t"
"Lpg13_%=:\n\t"
"s_cbranch_vccnz Lvr1_%=\n\t"
"v_cvt_pk_bf16_f32 v116, v116, v117\n\t"
"v_cvt_pk_bf16_f32 v118, v118, v119\n\t"
"v_mfma_f32_16x16x32_bf16 v[140:143], a[4:7], a[176:179], v[140:143]\n\t"
"ds_write_b128 v242, v[224:227] offset:26112\n\t"
"v_cvt_pk_bf16_f32 v234, v228, v229\n\t"
"v_cvt_pk_bf16_f32 v235, v230, v231\n\t"
"v_accvgpr_write_b32 a68, v112\n\t"
"v_accvgpr_write_b32 a69, v114\n\t"
"v_mfma_f32_16x16x32_bf16 v[144:147], a[4:7], a[180:183], v[144:147]\n\t"
"v_cvt_pk_bf16_f32 v120, v120, v121\n\t"
"v_cvt_pk_bf16_f32 v122, v122, v123\n\t"
"v_mfma_f32_16x16x32_bf16 v[144:147], v[96:99], a[184:187], v[144:147]\n\t"
"v_accvgpr_write_b32 a76, v116\n\t"
"v_accvgpr_write_b32 a77, v118\n\t"
"v_mfma_f32_16x16x32_bf16 v[144:147], a[208:211], a[188:191], v[144:147]\n\t"
"ds_write_b128 v242, v[232:235] offset:27648\n\t"
"v_mfma_f32_16x16x32_bf16 v[148:151], a[208:211], a[192:195], v[148:151]\n\t"
"v_cvt_pk_bf16_f32 v124, v124, v125\n\t"
"v_cvt_pk_bf16_f32 v126, v126, v127\n\t"
"v_mfma_f32_16x16x32_bf16 v[148:151], v[96:99], a[188:191], v[148:151]\n\t"
"v_accvgpr_write_b32 a84, v120\n\t"
"v_accvgpr_write_b32 a85, v122\n\t"
"v_mfma_f32_16x16x32_bf16 v[148:151], a[4:7], a[184:187], v[148:151]\n\t"
"ds_write_b64 v250, a[132:133] offset:18432\n\t"
"v_mfma_f32_16x16x32_bf16 v[152:155], a[4:7], a[188:191], v[152:155]\n\t"
"v_cvt_pk_bf16_f32 v128, v128, v129\n\t"
"v_cvt_pk_bf16_f32 v130, v130, v131\n\t"
"v_mfma_f32_16x16x32_bf16 v[152:155], v[96:99], a[192:195], v[152:155]\n\t"
"v_accvgpr_write_b32 a92, v124\n\t"
"v_accvgpr_write_b32 a93, v126\n\t"
"v_mfma_f32_16x16x32_bf16 v[152:155], a[208:211], a[196:199], v[152:155]\n\t"
"v_accvgpr_write_b32 a100, v128\n\t"
"v_accvgpr_write_b32 a101, v130\n\t"
"v_mfma_f32_16x16x32_bf16 v[156:159], a[208:211], a[200:203], v[156:159]\n\t"
"v_cvt_pk_bf16_f32 v132, v132, v133\n\t"
"v_cvt_pk_bf16_f32 v134, v134, v135\n\t"
"v_mfma_f32_16x16x32_bf16 v[156:159], v[96:99], a[196:199], v[156:159]\n\t"
"v_cvt_pk_bf16_f32 v136, v136, v137\n\t"
"v_cvt_pk_bf16_f32 v138, v138, v139\n\t"
"v_mfma_f32_16x16x32_bf16 v[156:159], a[4:7], a[192:195], v[156:159]\n\t"
"v_accvgpr_write_b32 a46, v132\n\t"
"v_accvgpr_write_b32 a47, v134\n\t"
"v_mfma_f32_16x16x32_bf16 v[160:163], a[4:7], a[196:199], v[160:163]\n\t"
"v_cvt_pk_bf16_f32 v140, v140, v141\n\t"
"v_cvt_pk_bf16_f32 v142, v142, v143\n\t"
"v_mfma_f32_16x16x32_bf16 v[160:163], v[96:99], a[200:203], v[160:163]\n\t"
"v_accvgpr_write_b32 a54, v136\n\t"
"v_accvgpr_write_b32 a55, v138\n\t"
"v_mfma_f32_16x16x32_bf16 v[160:163], a[208:211], a[204:207], v[160:163]\n\t"
"v_cvt_pk_bf16_f32 v144, v144, v145\n\t"
"v_cvt_pk_bf16_f32 v146, v146, v147\n\t"
"v_mfma_f32_16x16x32_bf16 v[164:167], a[212:215], a[168:171], v[164:167]\n\t"
"v_accvgpr_write_b32 a62, v140\n\t"
"v_accvgpr_write_b32 a63, v142\n\t"
"v_mfma_f32_16x16x32_bf16 v[164:167], a[12:15], a[176:179], v[164:167]\n\t"
"v_cvt_pk_bf16_f32 v148, v148, v149\n\t"
"v_cvt_pk_bf16_f32 v150, v150, v151\n\t"
"v_mfma_f32_16x16x32_bf16 v[164:167], a[136:139], a[172:175], v[164:167]\n\t"
"buffer_load_dwordx4 a[4:7], off, s[80:83], s20 offset:2048 sc1\n\t"
"s_mov_b32 s20, 479232\n\t"
"v_mfma_f32_16x16x32_bf16 v[168:171], a[212:215], a[172:175], v[168:171]\n\t"
"buffer_load_dwordx4 a[208:211], off, s[80:83], s20 offset:0 sc1\n\t"
"v_mfma_f32_16x16x32_bf16 v[168:171], a[136:139], a[176:179], v[168:171]\n\t"
"v_accvgpr_write_b32 a70, v144\n\t"
"v_accvgpr_write_b32 a71, v146\n\t"
"v_mfma_f32_16x16x32_bf16 v[168:171], a[12:15], a[180:183], v[168:171]\n\t"
"v_cvt_pk_bf16_f32 v152, v152, v153\n\t"
"v_cvt_pk_bf16_f32 v154, v154, v155\n\t"
"v_mfma_f32_16x16x32_bf16 v[172:175], a[12:15], a[184:187], v[172:175]\n\t"
"v_accvgpr_write_b32 a78, v148\n\t"
"v_accvgpr_write_b32 a79, v150\n\t"
"v_mfma_f32_16x16x32_bf16 v[172:175], a[136:139], a[180:183], v[172:175]\n\t"
"v_cvt_pk_bf16_f32 v156, v156, v157\n\t"
"v_cvt_pk_bf16_f32 v158, v158, v159\n\t"
"v_mfma_f32_16x16x32_bf16 v[172:175], a[212:215], a[176:179], v[172:175]\n\t"
"v_accvgpr_write_b32 a86, v152\n\t"
"v_accvgpr_write_b32 a87, v154\n\t"
"v_mfma_f32_16x16x32_bf16 v[176:179], a[212:215], a[180:183], v[176:179]\n\t"
"v_cvt_pk_bf16_f32 v160, v160, v161\n\t"
"v_cvt_pk_bf16_f32 v162, v162, v163\n\t"
"v_mfma_f32_16x16x32_bf16 v[176:179], a[136:139], a[184:187], v[176:179]\n\t"
"v_accvgpr_write_b32 a94, v156\n\t"
"v_mfma_f32_16x16x32_bf16 v[176:179], a[12:15], a[188:191], v[176:179]\n\t"
"s_waitcnt lgkmcnt(0)\n\t"
"s_barrier\n\t"
"v_mfma_f32_16x16x32_bf16 v[180:183], a[12:15], a[192:195], v[180:183]\n\t"
"ds_read_b128 a[168:171], v244 offset:24576\n\t"
"v_mfma_f32_16x16x32_bf16 v[180:183], a[136:139], a[188:191], v[180:183]\n\t"
"ds_read_b128 a[172:175], v244 offset:25088\n\t"
"v_accvgpr_write_b32 a95, v158\n\t"
"v_mfma_f32_16x16x32_bf16 v[180:183], a[212:215], a[184:187], v[180:183]\n\t"
"ds_read_b128 a[176:179], v244 offset:25600\n\t"
"v_cvt_pk_bf16_f32 v164, v164, v165\n\t"
"v_mfma_f32_16x16x32_bf16 v[184:187], a[212:215], a[188:191], v[184:187]\n\t"
"s_add_u32 s29, s23, 0\n\t"
"ds_read_b128 v[196:199], v252 offset:0\n\t"
"v_cvt_pk_bf16_f32 v166, v166, v167\n\t"
"v_mfma_f32_16x16x32_bf16 v[184:187], a[136:139], a[192:195], v[184:187]\n\t"
"ds_read_b128 a[180:183], v244 offset:26112\n\t"
"v_accvgpr_write_b32 a102, v160\n\t"
"v_mfma_f32_16x16x32_bf16 v[184:187], a[12:15], a[196:199], v[184:187]\n\t"
"ds_read_b128 v[204:207], v252 offset:1024\n\t"
"v_accvgpr_write_b32 a103, v162\n\t"
"v_mfma_f32_16x16x32_bf16 v[188:191], a[12:15], a[200:203], v[188:191]\n\t"
"ds_read_b128 a[184:187], v244 offset:26624\n\t"
"v_cvt_pk_bf16_f32 v168, v168, v169\n\t"
"v_mfma_f32_16x16x32_bf16 v[188:191], a[136:139], a[196:199], v[188:191]\n\t"
"ds_read_b128 a[188:191], v244 offset:27136\n\t"
"v_cvt_pk_bf16_f32 v170, v170, v171\n\t"
"v_mfma_f32_16x16x32_bf16 v[188:191], a[212:215], a[192:195], v[188:191]\n\t"
"buffer_load_dwordx4 a[40:43], off, s[80:83], s20 offset:1024 sc1\n\t"
"s_waitcnt lgkmcnt(4)\n\t"
"v_mfma_f32_16x16x32_bf16 v[192:195], a[212:215], a[196:199], v[192:195]\n\t"
"buffer_load_dwordx4 a[48:51], off, s[80:83], s20 offset:2048 sc1\n\t"
"v_mfma_f32_16x16x32_bf16 v[192:195], a[136:139], a[200:203], v[192:195]\n\t"
"s_mov_b32 s20, 491520\n\t"
"ds_read_b128 a[192:195], v244 offset:27648\n\t"
"v_accvgpr_write_b32 a110, v164\n\t"
"v_mfma_f32_16x16x32_bf16 v[192:195], a[12:15], a[204:207], v[192:195]\n\t"
"buffer_load_dwordx4 a[56:59], off, s[80:83], s20 offset:0 sc1\n\t"
"s_waitcnt lgkmcnt(3)\n\t"
"Lst18_%=:\n\t"
"v_mfma_f32_16x16x32_bf16 v[2:5], a[216:219], a[168:171], 0\n\t"
"ds_read_b128 a[196:199], v244 offset:28160\n\t"
"v_accvgpr_write_b32 a111, v166\n\t"
"v_mfma_f32_16x16x32_bf16 v[2:5], a[224:227], a[176:179], v[2:5]\n\t"
"ds_read_b128 v[212:215], v252 offset:2048\n\t"
"v_cvt_pk_bf16_f32 v172, v172, v173\n\t"
"v_mfma_f32_16x16x32_bf16 v[2:5], a[220:223], a[172:175], v[2:5]\n\t"
"buffer_load_dwordx4 a[64:67], off, s[80:83], s20 offset:1024 sc1\n\t"
"v_mfma_f32_16x16x32_bf16 v[6:9], a[216:219], a[172:175], 0\n\t"
"s_waitcnt lgkmcnt(4)\n\t"
"ds_read_b128 a[200:203], v244 offset:28672\n\t"
"v_cvt_pk_bf16_f32 v174, v174, v175\n\t"
"v_mfma_f32_16x16x32_bf16 v[6:9], a[220:223], a[176:179], v[6:9]\n\t"
"ds_read_b128 a[204:207], v244 offset:29184\n\t"
"v_accvgpr_write_b32 a118, v168\n\t"
"v_mfma_f32_16x16x32_bf16 v[6:9], a[224:227], a[180:183], v[6:9]\n\t"
"buffer_store_dwordx4 v[196:199], v248, s[16:19], s29 offen offset:0 nt\n\t"
"buffer_load_dwordx4 a[136:139], off, s[80:83], s20 offset:2048 sc1\n\t"
"v_accvgpr_write_b32 a119, v170\n\t"
"s_mov_b32 s20, 503808\n\t"
"v_mfma_f32_16x16x32_bf16 v[10:13], a[224:227], a[184:187], 0\n\t"
"s_waitcnt lgkmcnt(5)\n\t"
"v_cvt_pk_bf16_f32 v176, v176, v177\n\t"
"v_cvt_pk_bf16_f32 v178, v178, v179\n\t"
"v_mfma_f32_16x16x32_bf16 v[10:13], a[220:223], a[180:183], v[10:13]\n\t"
"v_accvgpr_write_b32 a126, v172\n\t"
"v_accvgpr_write_b32 a127, v174\n\t"
"v_mfma_f32_16x16x32_bf16 v[10:13], a[216:219], a[176:179], v[10:13]\n\t"
"buffer_store_dwordx4 v[204:207], v248, s[16:19], s29 offen offset:128 nt\n\t"
"buffer_load_dwordx4 a[12:15], off, s[80:83], s20 offset:0 sc1\n\t"
"ds_read_b128 v[196:199], v252 offset:12288\n\t"
"v_mfma_f32_16x16x32_bf16 v[14:17], a[216:219], a[180:183], 0\n\t"
"s_waitcnt lgkmcnt(5)\n\t"
"v_cvt_pk_bf16_f32 v180, v180, v181\n\t"
"v_cvt_pk_bf16_f32 v182, v182, v183\n\t"
"v_mfma_f32_16x16x32_bf16 v[14:17], a[220:223], a[184:187], v[14:17]\n\t"
"s_waitcnt lgkmcnt(3)\n\t"
"v_accvgpr_write_b32 a134, v176\n\t"
"v_accvgpr_write_b32 a135, v178\n\t"
"v_mfma_f32_16x16x32_bf16 v[14:17], a[224:227], a[188:191], v[14:17]\n\t"
"buffer_load_dwordx4 a[212:215], off, s[80:83], s20 offset:1024 sc1\n\t"
"v_mfma_f32_16x16x32_bf16 v[18:21], a[224:227], a[192:195], 0\n\t"
"v_cvt_pk_bf16_f32 v184, v184, v185\n\t"
"v_cvt_pk_bf16_f32 v186, v186, v187\n\t"
"v_mfma_f32_16x16x32_bf16 v[18:21], a[220:223], a[188:191], v[18:21]\n\t"
"v_accvgpr_write_b32 a142, v180\n\t"
"v_accvgpr_write_b32 a143, v182\n\t"
"v_mfma_f32_16x16x32_bf16 v[18:21], a[216:219], a[184:187], v[18:21]\n\t"
"v_cvt_pk_bf16_f32 v188, v188, v189\n\t"
"v_cvt_pk_bf16_f32 v190, v190, v191\n\t"
"v_mfma_f32_16x16x32_bf16 v[22:25], a[216:219], a[188:191], 0\n\t"
"s_waitcnt lgkmcnt(2)\n\t"
"v_accvgpr_write_b32 a150, v184\n\t"
"v_accvgpr_write_b32 a151, v186\n\t"
"v_mfma_f32_16x16x32_bf16 v[22:25], a[220:223], a[192:195], v[22:25]\n\t"
"ds_read_b128 v[204:207], v252 offset:13312\n\t"
"v_cvt_pk_bf16_f32 v192, v192, v193\n\t"
"v_mfma_f32_16x16x32_bf16 v[22:25], a[224:227], a[196:199], v[22:25]\n\t"
"v_cvt_pk_bf16_f32 v194, v194, v195\n\t"
"v_accvgpr_write_b32 a158, v188\n\t"
"v_mfma_f32_16x16x32_bf16 v[26:29], a[224:227], a[200:203], 0\n\t"
"s_waitcnt lgkmcnt(2)\n\t"
"ds_read_b128 a[0:3], v252 offset:14336\n\t"
"v_accvgpr_write_b32 a159, v190\n\t"
"v_mfma_f32_16x16x32_bf16 v[26:29], a[220:223], a[196:199], v[26:29]\n\t"
"v_accvgpr_write_b32 a166, v192\n\t"
"v_accvgpr_write_b32 a167, v194\n\t"
"v_mfma_f32_16x16x32_bf16 v[26:29], a[216:219], a[192:195], v[26:29]\n\t"
"buffer_store_dwordx4 v[212:215], v248, s[16:19], s29 offen offset:256 nt\n\t"
"s_add_u32 s29, s23, 393216\n\t"
"v_mfma_f32_16x16x32_bf16 v[30:33], a[216:219], a[196:199], 0\n\t"
"v_mfma_f32_16x16x32_bf16 v[30:33], a[220:223], a[200:203], v[30:33]\n\t"
"buffer_load_dwordx4 a[216:219], off, s[80:83], s20 offset:2048 sc1\n\t"
"v_mfma_f32_16x16x32_bf16 v[30:33], a[224:227], a[204:207], v[30:33]\n\t"
"s_mov_b32 s20, 110592\n\t"
"s_waitcnt lgkmcnt(2)\n\t"
"v_mfma_f32_16x16x32_bf16 v[34:37], a[228:231], a[168:171], 0\n\t"
"buffer_load_dwordx4 a[220:223], off, s[80:83], s20 offset:0 sc1\n\t"
"v_mfma_f32_16x16x32_bf16 v[34:37], a[236:239], a[176:179], v[34:37]\n\t"
"v_mfma_f32_16x16x32_bf16 v[34:37], a[232:235], a[172:175], v[34:37]\n\t"
"buffer_store_dwordx4 v[196:199], v248, s[16:19], s29 offen offset:0 nt\n\t"
"v_mfma_f32_16x16x32_bf16 v[38:41], a[228:231], a[172:175], 0\n\t"
"v_mfma_f32_16x16x32_bf16 v[38:41], a[232:235], a[176:179], v[38:41]\n\t"
"buffer_load_dwordx4 a[224:227], off, s[80:83], s20 offset:1024 sc1\n\t"
"v_mfma_f32_16x16x32_bf16 v[38:41], a[236:239], a[180:183], v[38:41]\n\t"
"v_mfma_f32_16x16x32_bf16 v[42:45], a[236:239], a[184:187], 0\n\t"
"v_mfma_f32_16x16x32_bf16 v[42:45], a[232:235], a[180:183], v[42:45]\n\t"
"v_mfma_f32_16x16x32_bf16 v[42:45], a[228:231], a[176:179], v[42:45]\n\t"
"ds_read_b64_tr_b16 v[236:237], v240 offset:1024\n\t"
"v_mfma_f32_16x16x32_bf16 v[46:49], a[228:231], a[180:183], 0\n\t"
"ds_read_b64_tr_b16 v[238:239], v240 offset:2560\n\t"
"v_mfma_f32_16x16x32_bf16 v[46:49], a[232:235], a[184:187], v[46:49]\n\t"
"ds_read_b64_tr_b16 v[228:229], v241 offset:1024\n\t"
"v_mfma_f32_16x16x32_bf16 v[46:49], a[236:239], a[188:191], v[46:49]\n\t"
"s_waitcnt lgkmcnt(3)\n\t"
"s_barrier\n\t"
"v_mfma_f32_16x16x32_bf16 v[50:53], a[236:239], a[192:195], 0\n\t"
"ds_read_b64_tr_b16 v[230:231], v241 offset:2560\n\t"
"v_mfma_f32_16x16x32_bf16 v[50:53], a[232:235], a[188:191], v[50:53]\n\t"
"ds_read_b64_tr_b16 v[224:225], v240 offset:20992\n\t"
"v_mfma_f32_16x16x32_bf16 v[50:53], a[228:231], a[184:187], v[50:53]\n\t"
"ds_read_b64_tr_b16 v[226:227], v240 offset:22528\n\t"
"v_mfma_f32_16x16x32_bf16 v[54:57], a[228:231], a[188:191], 0\n\t"
"ds_read_b64_tr_b16 v[232:233], v241 offset:20992\n\t"
"v_mfma_f32_16x16x32_bf16 v[54:57], a[232:235], a[192:195], v[54:57]\n\t"
"ds_read_b64_tr_b16 v[234:235], v241 offset:22528\n\t"
"v_mfma_f32_16x16x32_bf16 v[54:57], a[236:239], a[196:199], v[54:57]\n\t"
"buffer_store_dwordx4 v[204:207], v248, s[16:19], s29 offen offset:128 nt\n\t"
"ds_write_b64 v250, a[140:141] offset:0\n\t"
"v_mfma_f32_16x16x32_bf16 v[58:61], a[236:239], a[200:203], 0\n\t"
"v_mfma_f32_16x16x32_bf16 v[58:61], a[232:235], a[196:199], v[58:61]\n\t"
"v_mfma_f32_16x16x32_bf16 v[58:61], a[228:231], a[192:195], v[58:61]\n\t"
"ds_write_b128 v249, a[72:75] offset:0\n\t"
"v_mfma_f32_16x16x32_bf16 v[62:65], a[228:231], a[196:199], 0\n\t"
"buffer_load_dwordx4 a[228:231], off, s[80:83], s20 offset:2048 sc1\n\t"
"v_mfma_f32_16x16x32_bf16 v[62:65], a[232:235], a[200:203], v[62:65]\n\t"
"s_waitcnt lgkmcnt(6)\n\t"
"s_mov_b32 s20, 122880\n\t"
"v_mfma_f32_16x16x32_bf16 v[62:65], a[236:239], a[204:207], v[62:65]\n\t"
"s_waitcnt lgkmcnt(4)\n\t"
"ds_write_b128 v249, a[88:91] offset:12288\n\t"
"v_mfma_f32_16x16x32_bf16 v[236:239], v[236:239], a[8:11], 0\n\t"
"buffer_load_dwordx4 a[232:235], off, s[80:83], s20 offset:0 sc1\n\t"
"v_mfma_f32_16x16x32_bf16 v[228:231], v[228:231], a[8:11], 0\n\t"
"s_waitcnt lgkmcnt(3)\n\t"
"v_mfma_f32_16x16x32_bf16 v[224:227], v[224:227], a[8:11], 0\n\t"
"ds_write_b64 v250, a[156:157] offset:12288\n\t"
"v_mfma_f32_16x16x32_bf16 v[232:235], v[232:235], a[8:11], 0\n\t"
"buffer_load_dwordx4 a[236:239], off, s[80:83], s20 offset:1024 sc1\n\t"
"v_mfma_f32_16x16x32_bf16 v[66:69], a[16:19], a[168:171], 0\n\t"
"ds_read_b128 a[168:171], v244 offset:33792\n\t"
"v_mfma_f32_16x16x32_bf16 v[66:69], a[20:23], a[176:179], v[66:69]\n\t"
"v_cvt_pk_bf16_f32 v236, v236, v237\n\t"
"v_cvt_pk_bf16_f32 v237, v238, v239\n\t"
"v_mfma_f32_16x16x32_bf16 v[66:69], a[144:147], a[172:175], v[66:69]\n\t"
"ds_write_b128 v249, a[80:83] offset:6144\n\t"
"v_mfma_f32_16x16x32_bf16 v[70:73], a[16:19], a[172:175], 0\n\t"
"v_cvt_pk_bf16_f32 v238, v228, v229\n\t"
"v_cvt_pk_bf16_f32 v239, v230, v231\n\t"
"v_mfma_f32_16x16x32_bf16 v[70:73], a[144:147], a[176:179], v[70:73]\n\t"
"ds_read_b128 a[172:175], v244 offset:34304\n\t"
"v_mfma_f32_16x16x32_bf16 v[70:73], a[20:23], a[180:183], v[70:73]\n\t"
"buffer_store_dwordx4 a[0:3], v248, s[16:19], s29 offen offset:256 nt\n\t"
"ds_write_b64 v250, a[148:149] offset:6144\n\t"
"v_cvt_pk_bf16_f32 v224, v224, v225\n\t"
"v_mfma_f32_16x16x32_bf16 v[74:77], a[20:23], a[184:187], 0\n\t"
"v_cvt_pk_bf16_f32 v225, v226, v227\n\t"
"v_cvt_pk_bf16_f32 v226, v232, v233\n\t"
"v_mfma_f32_16x16x32_bf16 v[74:77], a[144:147], a[180:183], v[74:77]\n\t"
"v_cvt_pk_bf16_f32 v227, v234, v235\n\t"
"v_mfma_f32_16x16x32_bf16 v[74:77], a[16:19], a[176:179], v[74:77]\n\t"
"ds_read_b128 a[176:179], v244 offset:34816\n\t"
"v_mfma_f32_16x16x32_bf16 v[78:81], a[16:19], a[180:183], 0\n\t"
"ds_read_b128 a[180:183], v244 offset:35328\n\t"
"v_mfma_f32_16x16x32_bf16 v[78:81], a[144:147], a[184:187], v[78:81]\n\t"
"v_mfma_f32_16x16x32_bf16 v[78:81], a[20:23], a[188:191], v[78:81]\n\t"
"ds_write_b128 v249, a[96:99] offset:18432\n\t"
"v_mfma_f32_16x16x32_bf16 v[82:85], a[20:23], a[192:195], 0\n\t"
"v_mfma_f32_16x16x32_bf16 v[82:85], a[144:147], a[188:191], v[82:85]\n\t"
"v_mfma_f32_16x16x32_bf16 v[82:85], a[16:19], a[184:187], v[82:85]\n\t"
"ds_read_b128 a[184:187], v244 offset:35840\n\t"
"v_mfma_f32_16x16x32_bf16 v[86:89], a[16:19], a[188:191], 0\n\t"
"ds_read_b128 a[188:191], v244 offset:36352\n\t"
"v_mfma_f32_16x16x32_bf16 v[86:89], a[144:147], a[192:195], v[86:89]\n\t"
"v_mfma_f32_16x16x32_bf16 v[86:89], a[20:23], a[196:199], v[86:89]\n\t"
"ds_write_b64 v250, a[164:165] offset:18432\n\t"
"v_mfma_f32_16x16x32_bf16 v[90:93], a[20:23], a[200:203], 0\n\t"
"v_mfma_f32_16x16x32_bf16 v[90:93], a[144:147], a[196:199], v[90:93]\n\t"
"v_mfma_f32_16x16x32_bf16 v[90:93], a[16:19], a[192:195], v[90:93]\n\t"
"ds_read_b128 a[192:195], v244 offset:36864\n\t"
"v_mfma_f32_16x16x32_bf16 v[94:97], a[16:19], a[196:199], 0\n\t"
"s_waitcnt lgkmcnt(6)\n\t"
"ds_read_b128 a[196:199], v244 offset:37376\n\t"
"v_mfma_f32_16x16x32_bf16 v[94:97], a[144:147], a[200:203], v[94:97]\n\t"
"ds_read_b128 a[200:203], v244 offset:37888\n\t"
"v_mfma_f32_16x16x32_bf16 v[94:97], a[20:23], a[204:207], v[94:97]\n\t"
"ds_read_b128 a[204:207], v244 offset:38400\n\t"
"v_mfma_f32_16x16x32_bf16 v[100:103], a[240:243], a[168:171], 0\n\t"
"s_waitcnt lgkmcnt(8)\n\t"
"v_mfma_f32_16x16x32_bf16 v[100:103], a[248:251], a[176:179], v[100:103]\n\t"
"v_mfma_f32_16x16x32_bf16 v[100:103], a[244:247], a[172:175], v[100:103]\n\t"
"v_mfma_f32_16x16x32_bf16 v[104:107], a[240:243], a[172:175], 0\n\t"
"s_waitcnt lgkmcnt(6)\n\t"
"v_mfma_f32_16x16x32_bf16 v[104:107], a[244:247], a[176:179], v[104:107]\n\t"
"v_mfma_f32_16x16x32_bf16 v[104:107], a[248:251], a[180:183], v[104:107]\n\t"
"s_waitcnt lgkmcnt(0)\n\t"
"s_barrier\n\t"
"v_mfma_f32_16x16x32_bf16 v[108:111], a[248:251], a[184:187], 0\n\t"
"ds_read_b128 v[196:199], v252 offset:0\n\t"
"v_mfma_f32_16x16x32_bf16 v[108:111], a[244:247], a[180:183], v[108:111]\n\t"
"ds_read_b128 v[204:207], v252 offset:1024\n\t"
"v_mfma_f32_16x16x32_bf16 v[108:111], a[240:243], a[176:179], v[108:111]\n\t"
"ds_write_b128 v242, v[236:239] offset:24576\n\t"
"v_mfma_f32_16x16x32_bf16 v[112:115], a[240:243], a[180:183], 0\n\t"
"s_add_u32 s29, s23, 786432\n\t"
"v_mfma_f32_16x16x32_bf16 v[112:115], a[244:247], a[184:187], v[112:115]\n\t"
"ds_read_b64_tr_b16 v[228:229], v240 offset:40960\n\t"
"v_mfma_f32_16x16x32_bf16 v[112:115], a[248:251], a[188:191], v[112:115]\n\t"
"ds_write_b128 v242, v[224:227] offset:26112\n\t"
"v_mfma_f32_16x16x32_bf16 v[116:119], a[248:251], a[192:195], 0\n\t"
"s_waitcnt lgkmcnt(4)\n\t"
"v_mfma_f32_16x16x32_bf16 v[116:119], a[244:247], a[188:191], v[116:119]\n\t"
"ds_read_b64_tr_b16 v[230:231], v240 offset:42496\n\t"
"v_mfma_f32_16x16x32_bf16 v[116:119], a[240:243], a[184:187], v[116:119]\n\t"
"ds_read_b128 v[212:215], v252 offset:2048\n\t"
"v_mfma_f32_16x16x32_bf16 v[120:123], a[240:243], a[188:191], 0\n\t"
"s_waitcnt lgkmcnt(5)\n\t"
"ds_read_b64_tr_b16 v[232:233], v241 offset:40960\n\t"
"v_mfma_f32_16x16x32_bf16 v[120:123], a[244:247], a[192:195], v[120:123]\n\t"
"ds_read_b64_tr_b16 v[234:235], v241 offset:42496\n\t"
"v_add_u32 v240, 59904, v240\n\t"
"v_mfma_f32_16x16x32_bf16 v[120:123], a[248:251], a[196:199], v[120:123]\n\t"
"ds_read_b128 a[0:3], v252 offset:12288\n\t"
"v_add_u32 v241, 59904, v241\n\t"
"v_mfma_f32_16x16x32_bf16 v[124:127], a[244:247], a[196:199], 0\n\t"
"s_waitcnt lgkmcnt(4)\n\t"
"ds_read_b64_tr_b16 v[236:237], v240 offset:1024\n\t"
"v_mfma_f32_16x16x32_bf16 v[124:127], a[240:243], a[192:195], v[124:127]\n\t"
"s_waitcnt lgkmcnt(4)\n\t"
"ds_read_b64_tr_b16 v[238:239], v240 offset:2560\n\t"
"v_mfma_f32_16x16x32_bf16 v[124:127], a[248:251], a[200:203], v[124:127]\n\t"
"ds_read_b64_tr_b16 v[224:225], v241 offset:1024\n\t"
"ds_read_b64_tr_b16 v[226:227], v241 offset:2560\n\t"
"v_mfma_f32_16x16x32_bf16 v[228:231], v[228:231], a[8:11], 0\n\t"
"s_waitcnt lgkmcnt(5)\n\t"
"buffer_store_dwordx4 v[196:199], v248, s[16:19], s29 offen offset:0 nt\n\t"
"v_mfma_f32_16x16x32_bf16 v[232:235], v[232:235], a[8:11], 0\n\t"
"s_waitcnt lgkmcnt(2)\n\t"
"v_mfma_f32_16x16x32_bf16 v[236:239], v[236:239], a[8:11], 0\n\t"
"s_waitcnt lgkmcnt(0)\n\t"
"v_mfma_f32_16x16x32_bf16 v[224:227], v[224:227], a[8:11], 0\n\t"
"buffer_store_dwordx4 v[204:207], v248, s[16:19], s29 offen offset:128 nt\n\t"
"v_mfma_f32_16x16x32_bf16 v[128:131], a[244:247], a[200:203], 0\n\t"
"s_waitcnt vmcnt(42)\n\t"
"v_mfma_f32_16x16x32_bf16 v[128:131], a[240:243], a[196:199], v[128:131]\n\t"
"buffer_load_dwordx4 a[244:247], off, s[80:83], s20 offset:2048 sc1\n\t"
"v_mfma_f32_16x16x32_bf16 v[128:131], a[248:251], a[204:207], v[128:131]\n\t"
"buffer_store_dwordx4 v[212:215], v248, s[16:19], s29 offen offset:256 nt\n\t"
"v_cvt_pk_bf16_f32 v228, v228, v229\n\t"
"v_cvt_pk_bf16_f32 v229, v230, v231\n\t"
"v_cvt_pk_bf16_f32 v230, v232, v233\n\t"
"v_cvt_pk_bf16_f32 v231, v234, v235\n\t"
"s_mov_b32 s20, 135168\n\t"
"v_mfma_f32_16x16x32_bf16 v[132:135], a[252:255], a[168:171], 0\n\t"
"s_add_u32 s29, s23, 1179648\n\t"
"ds_read_b64_tr_b16 v[232:233], v240 offset:20992\n\t"
"v_cvt_pk_bf16_f32 v236, v236, v237\n\t"
"v_mfma_f32_16x16x32_bf16 v[132:135], a[152:155], a[176:179], v[132:135]\n\t"
"ds_read_b64_tr_b16 v[234:235], v240 offset:22528\n\t"
"v_cvt_pk_bf16_f32 v237, v238, v239\n\t"
"v_mfma_f32_16x16x32_bf16 v[132:135], a[24:27], a[172:175], v[132:135]\n\t"
"ds_write_b128 v242, v[228:231] offset:27648\n\t"
"v_mfma_f32_16x16x32_bf16 v[136:139], a[252:255], a[172:175], 0\n\t"
"buffer_load_dwordx4 a[240:243], off, s[80:83], s20 offset:0 sc1\n\t"
"v_mfma_f32_16x16x32_bf16 v[136:139], a[24:27], a[176:179], v[136:139]\n\t"
"buffer_load_dwordx4 a[248:251], off, s[80:83], s20 offset:1024 sc1\n\t"
"v_mfma_f32_16x16x32_bf16 v[136:139], a[152:155], a[180:183], v[136:139]\n\t"
"buffer_store_dwordx4 a[0:3], v248, s[16:19], s29 offen offset:0 nt\n\t"
"ds_read_b128 v[196:199], v252 offset:13312\n\t"
"v_cvt_pk_bf16_f32 v238, v224, v225\n\t"
"v_cvt_pk_bf16_f32 v239, v226, v227\n\t"
"v_mfma_f32_16x16x32_bf16 v[140:143], a[152:155], a[184:187], 0\n\t"
"ds_read_b64_tr_b16 v[224:225], v241 offset:20992\n\t"
"v_mfma_f32_16x16x32_bf16 v[140:143], a[24:27], a[180:183], v[140:143]\n\t"
"ds_read_b64_tr_b16 v[226:227], v241 offset:22528\n\t"
"v_mfma_f32_16x16x32_bf16 v[140:143], a[252:255], a[176:179], v[140:143]\n\t"
"ds_write_b128 v242, v[236:239] offset:29184\n\t"
"v_mfma_f32_16x16x32_bf16 v[144:147], a[252:255], a[180:183], 0\n\t"
"v_mfma_f32_16x16x32_bf16 v[144:147], a[24:27], a[184:187], v[144:147]\n\t"
"ds_read_b64_tr_b16 v[228:229], v240 offset:40960\n\t"
"v_mfma_f32_16x16x32_bf16 v[144:147], a[152:155], a[188:191], v[144:147]\n\t"
"ds_read_b64_tr_b16 v[230:231], v240 offset:42496\n\t"
"v_mfma_f32_16x16x32_bf16 v[148:151], a[252:255], a[184:187], 0\n\t"
"s_waitcnt lgkmcnt(7)\n\t"
"ds_read_b64_tr_b16 v[236:237], v241 offset:40960\n\t"
"v_mfma_f32_16x16x32_bf16 v[148:151], a[152:155], a[192:195], v[148:151]\n\t"
"s_waitcnt lgkmcnt(4)\n\t"
"ds_read_b64_tr_b16 v[238:239], v241 offset:42496\n\t"
"v_mfma_f32_16x16x32_bf16 v[148:151], a[24:27], a[188:191], v[148:151]\n\t"
"v_mfma_f32_16x16x32_bf16 v[232:235], v[232:235], a[8:11], 0\n\t"
"ds_read_b128 v[204:207], v252 offset:14336\n\t"
"v_mfma_f32_16x16x32_bf16 v[224:227], v[224:227], a[8:11], 0\n\t"
"s_waitcnt lgkmcnt(3)\n\t"
"v_mfma_f32_16x16x32_bf16 v[228:231], v[228:231], a[8:11], 0\n\t"
"s_waitcnt lgkmcnt(1)\n\t"
"v_mfma_f32_16x16x32_bf16 v[236:239], v[236:239], a[8:11], 0\n\t"
"v_mfma_f32_16x16x32_bf16 v[152:155], a[252:255], a[188:191], 0\n\t"
"v_mfma_f32_16x16x32_bf16 v[152:155], a[152:155], a[196:199], v[152:155]\n\t"
"v_cvt_pk_bf16_f32 v232, v232, v233\n\t"
"v_cvt_pk_bf16_f32 v233, v234, v235\n\t"
"v_mfma_f32_16x16x32_bf16 v[152:155], a[24:27], a[192:195], v[152:155]\n\t"
"v_cvt_pk_bf16_f32 v234, v224, v225\n\t"
"v_cvt_pk_bf16_f32 v235, v226, v227\n\t"
"v_mfma_f32_16x16x32_bf16 v[156:159], a[252:255], a[192:195], 0\n\t"
"v_cvt_pk_bf16_f32 v228, v228, v229\n\t"
"v_cvt_pk_bf16_f32 v229, v230, v231\n\t"
"v_mfma_f32_16x16x32_bf16 v[156:159], a[24:27], a[196:199], v[156:159]\n\t"
"v_cvt_pk_bf16_f32 v230, v236, v237\n\t"
"v_cvt_pk_bf16_f32 v231, v238, v239\n\t"
"v_mfma_f32_16x16x32_bf16 v[156:159], a[152:155], a[200:203], v[156:159]\n\t"
"ds_write_b128 v242, v[232:235] offset:30720\n\t"
"v_mfma_f32_16x16x32_bf16 v[160:163], a[24:27], a[200:203], 0\n\t"
"s_waitcnt vmcnt(43)\n\t"
"v_mfma_f32_16x16x32_bf16 v[160:163], a[252:255], a[196:199], v[160:163]\n\t"
"buffer_load_dwordx4 a[252:255], off, s[80:83], s20 offset:2048 sc1\n\t"
"v_mfma_f32_16x16x32_bf16 v[160:163], a[152:155], a[204:207], v[160:163]\n\t"
"buffer_store_dwordx4 v[196:199], v248, s[16:19], s29 offen offset:128 nt\n\t"
"s_mov_b32 s20, 147456\n\t"
"v_mfma_f32_16x16x32_bf16 v[164:167], a[28:31], a[168:171], 0\n\t"
"v_mfma_f32_16x16x32_bf16 v[164:167], v[200:203], a[176:179], v[164:167]\n\t"
"v_mfma_f32_16x16x32_bf16 v[164:167], a[104:107], a[172:175], v[164:167]\n\t"
"s_waitcnt lgkmcnt(1)\n\t"
"ds_write_b128 v242, v[228:231] offset:32256\n\t"
"v_mfma_f32_16x16x32_bf16 v[168:171], a[28:31], a[172:175], 0\n\t"
"buffer_load_dwordx4 v[240:243], off, s[80:83], s20 offset:0 sc1\n\t"
"v_mfma_f32_16x16x32_bf16 v[168:171], a[104:107], a[176:179], v[168:171]\n\t"
"v_mfma_f32_16x16x32_bf16 v[168:171], v[200:203], a[180:183], v[168:171]\n\t"
"buffer_store_dwordx4 v[204:207], v248, s[16:19], s29 offen offset:256 nt\n\t"
"v_mfma_f32_16x16x32_bf16 v[172:175], v[200:203], a[184:187], 0\n\t"
"v_mfma_f32_16x16x32_bf16 v[172:175], a[104:107], a[180:183], v[172:175]\n\t"
"v_mfma_f32_16x16x32_bf16 v[172:175], a[28:31], a[176:179], v[172:175]\n\t"
"ds_read_b64_tr_b16 v[224:225], v246 offset:512\n\t"
"v_mfma_f32_16x16x32_bf16 v[176:179], a[28:31], a[180:183], 0\n\t"
"ds_read_b64_tr_b16 v[236:237], v255 offset:512\n\t"
"v_mfma_f32_16x16x32_bf16 v[176:179], a[104:107], a[184:187], v[176:179]\n\t"
"ds_read_b64_tr_b16 v[232:233], v246 offset:6656\n\t"
"v_mfma_f32_16x16x32_bf16 v[176:179], v[200:203], a[188:191], v[176:179]\n\t"
"s_waitcnt lgkmcnt(3)\n\t"
"s_barrier\n\t"
"v_mfma_f32_16x16x32_bf16 v[180:183], v[200:203], a[192:195], 0\n\t"
"ds_read_b128 a[168:171], v244 offset:24576\n\t"
"v_mfma_f32_16x16x32_bf16 v[180:183], a[104:107], a[188:191], v[180:183]\n\t"
"ds_read_b128 a[172:175], v244 offset:25088\n\t"
"v_mfma_f32_16x16x32_bf16 v[180:183], a[28:31], a[184:187], v[180:183]\n\t"
"ds_read_b128 a[176:179], v244 offset:25600\n\t"
"v_mfma_f32_16x16x32_bf16 v[184:187], a[28:31], a[188:191], 0\n\t"
"ds_read_b128 a[180:183], v244 offset:26112\n\t"
"v_mfma_f32_16x16x32_bf16 v[184:187], a[104:107], a[192:195], v[184:187]\n\t"
"ds_read_b64_tr_b16 v[228:229], v255 offset:6656\n\t"
"v_mfma_f32_16x16x32_bf16 v[184:187], v[200:203], a[196:199], v[184:187]\n\t"
"ds_read_b128 a[184:187], v244 offset:26624\n\t"
"v_mfma_f32_16x16x32_bf16 v[188:191], v[200:203], a[200:203], 0\n\t"
"ds_read_b128 a[188:191], v244 offset:27136\n\t"
"v_mfma_f32_16x16x32_bf16 v[188:191], a[104:107], a[196:199], v[188:191]\n\t"
"v_mfma_f32_16x16x32_bf16 v[188:191], a[28:31], a[192:195], v[188:191]\n\t"
"ds_read_b128 a[192:195], v244 offset:27648\n\t"
"v_mfma_f32_16x16x32_bf16 v[192:195], a[28:31], a[196:199], 0\n\t"
"s_waitcnt lgkmcnt(10)\n\t"
"ds_read_b128 a[196:199], v244 offset:28160\n\t"
"v_mfma_f32_16x16x32_bf16 v[192:195], a[104:107], a[200:203], v[192:195]\n\t"
"s_waitcnt lgkmcnt(10)\n\t"
"ds_read_b128 a[200:203], v244 offset:28672\n\t"
"v_mfma_f32_16x16x32_bf16 v[192:195], v[200:203], a[204:207], v[192:195]\n\t"
"s_waitcnt lgkmcnt(10)\n\t"
"ds_write_b128 v249, a[44:47] offset:0\n\t"
"v_mfma_f32_4x4x4_16b_bf16 v[224:227], v[224:225], v[220:221], 0\n\t"
"s_waitcnt lgkmcnt(6)\n\t"
"v_mfma_f32_4x4x4_16b_bf16 v[236:239], v[236:237], v[220:221], 0\n\t"
"s_waitcnt vmcnt(41)\n\t"
"v_mfma_f32_4x4x4_16b_bf16 v[232:235], v[232:233], v[220:221], 0\n\t"
"v_mfma_f32_4x4x4_16b_bf16 v[228:231], v[228:229], v[220:221], 0\n\t"
"ds_write_b128 v249, a[60:63] offset:12288\n\t"
"Lst19_%=:\n\t"
"v_mfma_f32_16x16x32_bf16 v[2:5], a[32:35], a[168:171], v[2:5]\n\t"
"v_cvt_pk_bf16_f32 v224, v224, v225\n\t"
"v_cvt_pk_bf16_f32 v225, v226, v227\n\t"
"v_mfma_f32_16x16x32_bf16 v[2:5], a[36:39], a[176:179], v[2:5]\n\t"
"ds_read_b128 a[204:207], v244 offset:29184\n\t"
"v_mfma_f32_16x16x32_bf16 v[2:5], a[160:163], a[172:175], v[2:5]\n\t"
"ds_write_b64 v250, a[110:111] offset:0\n\t"
"v_mfma_f32_16x16x32_bf16 v[6:9], a[32:35], a[172:175], v[6:9]\n\t"
"s_waitcnt lgkmcnt(8)\n\t"
"v_cvt_pk_bf16_f32 v226, v236, v237\n\t"
"v_cvt_pk_bf16_f32 v227, v238, v239\n\t"
"v_mfma_f32_16x16x32_bf16 v[6:9], a[160:163], a[176:179], v[6:9]\n\t"
"v_cvt_pk_bf16_f32 v232, v232, v233\n\t"
"v_cvt_pk_bf16_f32 v233, v234, v235\n\t"
"v_mfma_f32_16x16x32_bf16 v[6:9], a[36:39], a[180:183], v[6:9]\n\t"
"ds_write_b128 v245, v[224:227] offset:512\n\t"
"v_cvt_pk_bf16_f32 v234, v228, v229\n\t"
"v_cvt_pk_bf16_f32 v235, v230, v231\n\t"
"v_mfma_f32_16x16x32_bf16 v[10:13], a[36:39], a[184:187], v[10:13]\n\t"
"s_waitcnt lgkmcnt(8)\n\t"
"v_mfma_f32_16x16x32_bf16 v[10:13], a[160:163], a[180:183], v[10:13]\n\t"
"v_mfma_f32_16x16x32_bf16 v[10:13], a[32:35], a[176:179], v[10:13]\n\t"
"ds_write_b64 v250, a[126:127] offset:12288\n\t"
"v_mfma_f32_16x16x32_bf16 v[14:17], a[32:35], a[180:183], v[14:17]\n\t"
"s_waitcnt lgkmcnt(8)\n\t"
"ds_read_b64_tr_b16 v[236:237], v246 offset:12800\n\t"
"v_mfma_f32_16x16x32_bf16 v[14:17], a[160:163], a[184:187], v[14:17]\n\t"
"ds_read_b64_tr_b16 v[228:229], v255 offset:12800\n\t"
"v_mfma_f32_16x16x32_bf16 v[14:17], a[36:39], a[188:191], v[14:17]\n\t"
"ds_write_b128 v245, v[232:235] offset:6656\n\t"
"v_mfma_f32_16x16x32_bf16 v[18:21], a[36:39], a[192:195], v[18:21]\n\t"
"s_waitcnt lgkmcnt(10)\n\t"
"v_mfma_f32_16x16x32_bf16 v[18:21], a[160:163], a[188:191], v[18:21]\n\t"
"ds_read_b64_tr_b16 v[224:225], v246 offset:20480\n\t"
"v_mfma_f32_16x16x32_bf16 v[18:21], a[32:35], a[184:187], v[18:21]\n\t"
"ds_read_b64_tr_b16 v[232:233], v255 offset:20480\n\t"
"v_mfma_f32_16x16x32_bf16 v[22:25], a[32:35], a[188:191], v[22:25]\n\t"
"s_waitcnt lgkmcnt(11)\n\t"
"v_mfma_f32_16x16x32_bf16 v[22:25], a[160:163], a[192:195], v[22:25]\n\t"
"v_mfma_f32_16x16x32_bf16 v[22:25], a[36:39], a[196:199], v[22:25]\n\t"
"ds_write_b128 v249, a[52:55] offset:6144\n\t"
"v_mfma_f32_16x16x32_bf16 v[26:29], a[36:39], a[200:203], v[26:29]\n\t"
"s_waitcnt lgkmcnt(9)\n\t"
"v_mfma_f32_16x16x32_bf16 v[26:29], a[160:163], a[196:199], v[26:29]\n\t"
"v_mfma_f32_16x16x32_bf16 v[26:29], a[32:35], a[192:195], v[26:29]\n\t"
"ds_write_b128 v249, a[68:71] offset:18432\n\t"
"v_mfma_f32_16x16x32_bf16 v[30:33], a[32:35], a[196:199], v[30:33]\n\t"
"s_waitcnt vmcnt(36) lgkmcnt(2)\n\t"
"v_mfma_f32_16x16x32_bf16 v[30:33], a[160:163], a[200:203], v[30:33]\n\t"
"v_mfma_f32_16x16x32_bf16 v[30:33], a[36:39], a[204:207], v[30:33]\n\t"
"ds_write_b64 v250, a[118:119] offset:6144\n\t"
"v_mfma_f32_4x4x4_16b_bf16 v[236:239], v[236:237], v[220:221], 0\n\t"
"v_mfma_f32_4x4x4_16b_bf16 v[228:231], v[228:229], v[220:221], 0\n\t"
"Lpg14_%=:\n\t"
"s_cbranch_vccnz Lvr1_%=\n\t"
"ds_write_b64 v250, a[134:135] offset:18432\n\t"
"v_mfma_f32_4x4x4_16b_bf16 v[224:227], v[224:225], v[220:221], 0\n\t"
"v_mfma_f32_4x4x4_16b_bf16 v[232:235], v[232:233], v[220:221], 0\n\t"
"v_mfma_f32_16x16x32_bf16 v[34:37], a[112:115], a[168:171], v[34:37]\n\t"
"v_cvt_pk_bf16_f32 v236, v236, v237\n\t"
"v_cvt_pk_bf16_f32 v237, v238, v239\n\t"
"v_mfma_f32_16x16x32_bf16 v[34:37], a[120:123], a[176:179], v[34:37]\n\t"
"v_cvt_pk_bf16_f32 v238, v228, v229\n\t"
"v_cvt_pk_bf16_f32 v239, v230, v231\n\t"
"v_mfma_f32_16x16x32_bf16 v[34:37], v[208:211], a[172:175], v[34:37]\n\t"
"ds_write_b128 v245, v[236:239] offset:12800\n\t"
"v_mfma_f32_16x16x32_bf16 v[38:41], a[112:115], a[172:175], v[38:41]\n\t"
"v_cvt_pk_bf16_f32 v224, v224, v225\n\t"
"v_cvt_pk_bf16_f32 v225, v226, v227\n\t"
"v_mfma_f32_16x16x32_bf16 v[38:41], v[208:211], a[176:179], v[38:41]\n\t"
"v_cvt_pk_bf16_f32 v226, v232, v233\n\t"
"v_cvt_pk_bf16_f32 v227, v234, v235\n\t"
"v_mfma_f32_16x16x32_bf16 v[38:41], a[120:123], a[180:183], v[38:41]\n\t"
"ds_write_b128 v245, v[224:227] offset:20480\n\t"
"v_mfma_f32_16x16x32_bf16 v[42:45], a[120:123], a[184:187], v[42:45]\n\t"
"v_mfma_f32_16x16x32_bf16 v[42:45], v[208:211], a[180:183], v[42:45]\n\t"
"v_mfma_f32_16x16x32_bf16 v[42:45], a[112:115], a[176:179], v[42:45]\n\t"
"ds_read_b64_tr_b16 v[228:229], v246 offset:26624\n\t"
"v_mfma_f32_16x16x32_bf16 v[46:49], a[112:115], a[180:183], v[46:49]\n\t"
"ds_read_b64_tr_b16 v[232:233], v255 offset:26624\n\t"
"v_mfma_f32_16x16x32_bf16 v[46:49], v[208:211], a[184:187], v[46:49]\n\t"
"ds_read_b64_tr_b16 v[236:237], v246 offset:32768\n\t"
"v_mfma_f32_16x16x32_bf16 v[46:49], a[120:123], a[188:191], v[46:49]\n\t"
"s_waitcnt lgkmcnt(5)\n\t"
"s_barrier\n\t"
"v_mfma_f32_16x16x32_bf16 v[50:53], a[120:123], a[192:195], v[50:53]\n\t"
"ds_read_b128 v[196:199], v252 offset:0\n\t"
"v_mfma_f32_16x16x32_bf16 v[50:53], v[208:211], a[188:191], v[50:53]\n\t"
"ds_read_b128 v[204:207], v252 offset:1024\n\t"
"v_mfma_f32_16x16x32_bf16 v[50:53], a[112:115], a[184:187], v[50:53]\n\t"
"ds_read_b64_tr_b16 v[224:225], v255 offset:32768\n\t"
"v_mfma_f32_16x16x32_bf16 v[54:57], a[112:115], a[188:191], v[54:57]\n\t"
"s_add_u32 s29, s23, 1152\n\t"
"ds_read_b128 v[212:215], v252 offset:2048\n\t"
"v_mfma_f32_16x16x32_bf16 v[54:57], v[208:211], a[192:195], v[54:57]\n\t"
"ds_read_b128 a[0:3], v252 offset:12288\n\t"
"v_mfma_f32_16x16x32_bf16 v[54:57], a[120:123], a[196:199], v[54:57]\n\t"
"ds_read_b128 a[8:11], v252 offset:13312\n\t"
"v_mfma_f32_16x16x32_bf16 v[58:61], a[120:123], a[200:203], v[58:61]\n\t"
"s_waitcnt lgkmcnt(5)\n\t"
"ds_read_b128 a[16:19], v252 offset:14336\n\t"
"v_mfma_f32_16x16x32_bf16 v[58:61], v[208:211], a[196:199], v[58:61]\n\t"
"v_mfma_f32_16x16x32_bf16 v[58:61], a[112:115], a[192:195], v[58:61]\n\t"
"v_mfma_f32_16x16x32_bf16 v[62:65], a[112:115], a[196:199], v[62:65]\n\t"
"s_waitcnt lgkmcnt(5)\n\t"
"v_mfma_f32_16x16x32_bf16 v[62:65], v[208:211], a[200:203], v[62:65]\n\t"
"v_mfma_f32_16x16x32_bf16 v[62:65], a[120:123], a[204:207], v[62:65]\n\t"
"buffer_store_dwordx4 v[196:199], v248, s[16:19], s29 offen offset:0 nt\n\t"
"v_mfma_f32_4x4x4_16b_bf16 v[228:231], v[228:229], v[220:221], 0\n\t"
"s_waitcnt lgkmcnt(4)\n\t"
"v_mfma_f32_4x4x4_16b_bf16 v[232:235], v[232:233], v[220:221], 0\n\t"
"s_waitcnt vmcnt(32)\n\t"
"v_mfma_f32_4x4x4_16b_bf16 v[236:239], v[236:237], v[220:221], 0\n\t"
"buffer_store_dwordx4 v[204:207], v248, s[16:19], s29 offen offset:128 nt\n\t"
"s_waitcnt lgkmcnt(3)\n\t"
"v_mfma_f32_4x4x4_16b_bf16 v[224:227], v[224:225], v[220:221], 0\n\t"
"s_waitcnt lgkmcnt(2)\n\t"
"v_mfma_f32_16x16x32_bf16 v[66:69], v[216:219], a[168:171], v[66:69]\n\t"
"ds_read_b128 a[168:171], v244 offset:33792\n\t"
"v_cvt_pk_bf16_f32 v228, v228, v229\n\t"
"v_mfma_f32_16x16x32_bf16 v[66:69], a[4:7], a[176:179], v[66:69]\n\t"
"v_cvt_pk_bf16_f32 v229, v230, v231\n\t"
"v_cvt_pk_bf16_f32 v230, v232, v233\n\t"
"v_mfma_f32_16x16x32_bf16 v[66:69], a[128:131], a[172:175], v[66:69]\n\t"
"s_waitcnt lgkmcnt(2)\n\t"
"v_cvt_pk_bf16_f32 v231, v234, v235\n\t"
"v_cvt_pk_bf16_f32 v236, v236, v237\n\t"
"v_mfma_f32_16x16x32_bf16 v[70:73], v[216:219], a[172:175], v[70:73]\n\t"
"ds_read_b128 a[172:175], v244 offset:34304\n\t"
"v_cvt_pk_bf16_f32 v237, v238, v239\n\t"
"v_mfma_f32_16x16x32_bf16 v[70:73], a[128:131], a[176:179], v[70:73]\n\t"
"v_cvt_pk_bf16_f32 v238, v224, v225\n\t"
"v_cvt_pk_bf16_f32 v239, v226, v227\n\t"
"v_mfma_f32_16x16x32_bf16 v[70:73], a[4:7], a[180:183], v[70:73]\n\t"
"buffer_store_dwordx4 v[212:215], v248, s[16:19], s29 offen offset:256 nt\n\t"
"s_add_u32 s29, s23, 394368\n\t"
"s_waitcnt lgkmcnt(2)\n\t"
"v_mfma_f32_16x16x32_bf16 v[74:77], a[4:7], a[184:187], v[74:77]\n\t"
"v_mfma_f32_16x16x32_bf16 v[74:77], a[128:131], a[180:183], v[74:77]\n\t"
"v_mfma_f32_16x16x32_bf16 v[74:77], v[216:219], a[176:179], v[74:77]\n\t"
"ds_write_b128 v245, v[228:231] offset:26624\n\t"
"v_mfma_f32_16x16x32_bf16 v[78:81], v[216:219], a[180:183], v[78:81]\n\t"
"v_mfma_f32_16x16x32_bf16 v[78:81], a[128:131], a[184:187], v[78:81]\n\t"
"ds_read_b128 a[176:179], v244 offset:34816\n\t"
"v_mfma_f32_16x16x32_bf16 v[78:81], a[4:7], a[188:191], v[78:81]\n\t"
"buffer_store_dwordx4 a[0:3], v248, s[16:19], s29 offen offset:0 nt\n\t"
"ds_read_b64_tr_b16 v[232:233], v246 offset:40448\n\t"
"ds_read_b64_tr_b16 v[224:225], v255 offset:40448\n\t"
"v_mfma_f32_16x16x32_bf16 v[82:85], a[4:7], a[192:195], v[82:85]\n\t"
"ds_read_b128 a[180:183], v244 offset:35328\n\t"
"v_mfma_f32_16x16x32_bf16 v[82:85], a[128:131], a[188:191], v[82:85]\n\t"
"ds_read_b64_tr_b16 v[228:229], v246 offset:46592\n\t"
"v_mfma_f32_16x16x32_bf16 v[82:85], v[216:219], a[184:187], v[82:85]\n\t"
"ds_write_b128 v245, v[236:239] offset:32768\n\t"
"buffer_store_dwordx4 a[8:11], v248, s[16:19], s29 offen offset:128 nt\n\t"
"v_mfma_f32_16x16x32_bf16 v[86:89], v[216:219], a[188:191], v[86:89]\n\t"
"ds_read_b64_tr_b16 v[236:237], v255 offset:46592\n\t"
"v_mfma_f32_16x16x32_bf16 v[86:89], a[128:131], a[192:195], v[86:89]\n\t"
"ds_read_b128 a[184:187], v244 offset:35840\n\t"
"v_mfma_f32_16x16x32_bf16 v[86:89], a[4:7], a[196:199], v[86:89]\n\t"
"buffer_store_dwordx4 a[16:19], v248, s[16:19], s29 offen offset:256 nt\n\t"
"ds_read_b128 a[188:191], v244 offset:36352\n\t"
"v_mfma_f32_16x16x32_bf16 v[90:93], a[4:7], a[200:203], v[90:93]\n\t"
"v_mfma_f32_16x16x32_bf16 v[90:93], a[128:131], a[196:199], v[90:93]\n\t"
"v_mfma_f32_16x16x32_bf16 v[90:93], v[216:219], a[192:195], v[90:93]\n\t"
"ds_read_b128 a[192:195], v244 offset:36864\n\t"
"v_mfma_f32_16x16x32_bf16 v[94:97], v[216:219], a[196:199], v[94:97]\n\t"
"s_waitcnt lgkmcnt(8)\n\t"
"ds_read_b128 a[196:199], v244 offset:37376\n\t"
"v_mfma_f32_16x16x32_bf16 v[94:97], a[128:131], a[200:203], v[94:97]\n\t"
"s_waitcnt lgkmcnt(8)\n\t"
"ds_read_b128 a[200:203], v244 offset:37888\n\t"
"v_mfma_f32_16x16x32_bf16 v[94:97], a[4:7], a[204:207], v[94:97]\n\t"
"s_waitcnt lgkmcnt(7)\n\t"
"ds_read_b128 a[204:207], v244 offset:38400\n\t"
"v_mfma_f32_4x4x4_16b_bf16 v[232:235], v[232:233], v[220:221], 0\n\t"
"s_waitcnt lgkmcnt(6)\n\t"
"v_mfma_f32_4x4x4_16b_bf16 v[224:227], v[224:225], v[220:221], 0\n\t"
"s_waitcnt vmcnt(34)\n\t"
"v_mfma_f32_4x4x4_16b_bf16 v[228:231], v[228:229], v[220:221], 0\n\t"
"v_mfma_f32_4x4x4_16b_bf16 v[236:239], v[236:237], v[220:221], 0\n\t"
"v_mfma_f32_16x16x32_bf16 v[100:103], a[208:211], a[168:171], v[100:103]\n\t"
"v_cvt_pk_bf16_f32 v232, v232, v233\n\t"
"v_cvt_pk_bf16_f32 v233, v234, v235\n\t"
"v_mfma_f32_16x16x32_bf16 v[100:103], a[48:51], a[176:179], v[100:103]\n\t"
"v_cvt_pk_bf16_f32 v234, v224, v225\n\t"
"v_cvt_pk_bf16_f32 v235, v226, v227\n\t"
"v_mfma_f32_16x16x32_bf16 v[100:103], a[40:43], a[172:175], v[100:103]\n\t"
"ds_write_b128 v245, v[232:235] offset:40448\n\t"
"v_mfma_f32_16x16x32_bf16 v[104:107], a[208:211], a[172:175], v[104:107]\n\t"
"s_waitcnt lgkmcnt(6)\n\t"
"v_cvt_pk_bf16_f32 v228, v228, v229\n\t"
"v_cvt_pk_bf16_f32 v229, v230, v231\n\t"
"v_mfma_f32_16x16x32_bf16 v[104:107], a[40:43], a[176:179], v[104:107]\n\t"
"v_cvt_pk_bf16_f32 v230, v236, v237\n\t"
"v_mfma_f32_16x16x32_bf16 v[104:107], a[48:51], a[180:183], v[104:107]\n\t"
"s_waitcnt lgkmcnt(1)\n\t"
"s_barrier\n\t"
"v_mfma_f32_16x16x32_bf16 v[108:111], a[48:51], a[184:187], v[108:111]\n\t"
"v_cvt_pk_bf16_f32 v231, v238, v239\n\t"
"v_mfma_f32_16x16x32_bf16 v[108:111], a[40:43], a[180:183], v[108:111]\n\t"
"v_fma_f32 v196, -v2, s56, -v100\n\t"
"v_fma_f32 v197, -v3, s56, -v101\n\t"
"v_mfma_f32_16x16x32_bf16 v[108:111], a[208:211], a[176:179], v[108:111]\n\t"
"ds_write_b128 v245, v[228:231] offset:46592\n\t"
"v_mfma_f32_16x16x32_bf16 v[112:115], a[208:211], a[180:183], v[112:115]\n\t"
"v_fma_f32 v198, -v4, s56, -v102\n\t"
"v_fma_f32 v199, -v5, s56, -v103\n\t"
"v_mfma_f32_16x16x32_bf16 v[112:115], a[40:43], a[184:187], v[112:115]\n\t"
"ds_read_b64_tr_b16 v[224:225], v246 offset:52736\n\t"
"v_mfma_f32_16x16x32_bf16 v[112:115], a[48:51], a[188:191], v[112:115]\n\t"
"ds_read_b64_tr_b16 v[236:237], v255 offset:52736\n\t"
"v_fma_f32 v204, -v6, s56, -v104\n\t"
"v_mfma_f32_16x16x32_bf16 v[116:119], a[48:51], a[192:195], v[116:119]\n\t"
"v_fma_f32 v205, -v7, s56, -v105\n\t"
"v_fma_f32 v206, -v8, s56, -v106\n\t"
"v_mfma_f32_16x16x32_bf16 v[116:119], a[40:43], a[188:191], v[116:119]\n\t"
"v_fma_f32 v207, -v9, s56, -v107\n\t"
"v_fma_f32 v212, -v10, s56, -v108\n\t"
"v_mfma_f32_16x16x32_bf16 v[116:119], a[208:211], a[184:187], v[116:119]\n\t"
"ds_write_b128 v249, a[76:79] offset:0\n\t"
"v_mfma_f32_16x16x32_bf16 v[120:123], a[208:211], a[188:191], v[120:123]\n\t"
"v_fma_f32 v213, -v11, s56, -v109\n\t"
"v_fma_f32 v214, -v12, s56, -v110\n\t"
"v_mfma_f32_16x16x32_bf16 v[120:123], a[40:43], a[192:195], v[120:123]\n\t"
"v_fma_f32 v215, -v13, s56, -v111\n\t"
"v_fma_f32 v98, -v14, s56, -v112\n\t"
"v_mfma_f32_16x16x32_bf16 v[120:123], a[48:51], a[196:199], v[120:123]\n\t"
"ds_write_b128 v249, a[92:95] offset:12288\n\t"
"v_mfma_f32_16x16x32_bf16 v[124:127], a[48:51], a[200:203], v[124:127]\n\t"
"v_fma_f32 v99, -v15, s56, -v113\n\t"
"v_accvgpr_write_b32 a0, v98\n\t"
"v_mfma_f32_16x16x32_bf16 v[124:127], a[40:43], a[196:199], v[124:127]\n\t"
"v_fma_f32 v98, -v16, s56, -v114\n\t"
"v_accvgpr_write_b32 a1, v99\n\t"
"v_mfma_f32_16x16x32_bf16 v[124:127], a[208:211], a[192:195], v[124:127]\n\t"
"ds_write_b64 v250, a[142:143] offset:0\n\t"
"v_mfma_f32_16x16x32_bf16 v[128:131], a[208:211], a[196:199], v[128:131]\n\t"
"buffer_load_dwordx4 a[208:211], off, s[80:83], s20 offset:1024 sc1\n\t"
"v_mfma_f32_16x16x32_bf16 v[128:131], a[40:43], a[200:203], v[128:131]\n\t"
"s_waitcnt lgkmcnt(3)\n\t"
"v_fma_f32 v99, -v17, s56, -v115\n\t"
"v_accvgpr_write_b32 a2, v98\n\t"
"v_mfma_f32_16x16x32_bf16 v[128:131], a[48:51], a[204:207], v[128:131]\n\t"
"s_waitcnt vmcnt(31)\n\t"
"ds_write_b64 v250, a[158:159] offset:12288\n\t"
"v_mfma_f32_4x4x4_16b_bf16 v[224:227], v[224:225], v[220:221], 0\n\t"
"v_mfma_f32_4x4x4_16b_bf16 v[236:239], v[236:237], v[220:221], 0\n\t"
"ds_write_b128 v249, a[84:87] offset:6144\n\t"
"v_mfma_f32_16x16x32_bf16 v[132:135], a[56:59], a[168:171], v[132:135]\n\t"
"v_accvgpr_write_b32 a3, v99\n\t"
"v_fma_f32 v98, -v18, s56, -v116\n\t"
"v_mfma_f32_16x16x32_bf16 v[132:135], a[136:139], a[176:179], v[132:135]\n\t"
"v_cvt_pk_bf16_f32 v224, v224, v225\n\t"
"v_cvt_pk_bf16_f32 v225, v226, v227\n\t"
"v_mfma_f32_16x16x32_bf16 v[132:135], a[64:67], a[172:175], v[132:135]\n\t"
"ds_write_b128 v249, a[100:103] offset:18432\n\t"
"v_mfma_f32_16x16x32_bf16 v[136:139], a[56:59], a[172:175], v[136:139]\n\t"
"v_cvt_pk_bf16_f32 v226, v236, v237\n\t"
"v_cvt_pk_bf16_f32 v227, v238, v239\n\t"
"v_mfma_f32_16x16x32_bf16 v[136:139], a[64:67], a[176:179], v[136:139]\n\t"
"v_fma_f32 v99, -v19, s56, -v117\n\t"
"v_accvgpr_write_b32 a8, v98\n\t"
"v_mfma_f32_16x16x32_bf16 v[136:139], a[136:139], a[180:183], v[136:139]\n\t"
"ds_write_b128 v245, v[224:227] offset:52736\n\t"
"v_mfma_f32_16x16x32_bf16 v[140:143], a[136:139], a[184:187], v[140:143]\n\t"
"buffer_load_dwordx4 v[236:239], off, s[80:83], s20 offset:2048 sc1\n\t"
"v_mfma_f32_16x16x32_bf16 v[140:143], a[64:67], a[180:183], v[140:143]\n\t"
"v_fma_f32 v98, -v20, s56, -v118\n\t"
"v_accvgpr_write_b32 a9, v99\n\t"
"v_mfma_f32_16x16x32_bf16 v[140:143], a[56:59], a[176:179], v[140:143]\n\t"
"ds_write_b64 v250, a[150:151] offset:6144\n\t"
"v_mfma_f32_16x16x32_bf16 v[144:147], a[56:59], a[180:183], v[144:147]\n\t"
"v_fma_f32 v99, -v21, s56, -v119\n\t"
"v_accvgpr_write_b32 a10, v98\n\t"
"v_mfma_f32_16x16x32_bf16 v[144:147], a[64:67], a[184:187], v[144:147]\n\t"
"v_accvgpr_write_b32 a11, v99\n\t"
"v_fma_f32 v98, -v22, s56, -v120\n\t"
"v_mfma_f32_16x16x32_bf16 v[144:147], a[136:139], a[188:191], v[144:147]\n\t"
"ds_write_b64 v250, a[166:167] offset:18432\n\t"
"v_mfma_f32_16x16x32_bf16 v[148:151], a[136:139], a[192:195], v[148:151]\n\t"
"v_fma_f32 v99, -v23, s56, -v121\n\t"
"v_accvgpr_write_b32 a16, v98\n\t"
"v_mfma_f32_16x16x32_bf16 v[148:151], a[64:67], a[188:191], v[148:151]\n\t"
"v_fma_f32 v98, -v24, s56, -v122\n\t"
"v_accvgpr_write_b32 a17, v99\n\t"
"v_mfma_f32_16x16x32_bf16 v[148:151], a[56:59], a[184:187], v[148:151]\n\t"
"v_fma_f32 v99, -v25, s56, -v123\n\t"
"v_accvgpr_write_b32 a18, v98\n\t"
"v_mfma_f32_16x16x32_bf16 v[152:155], a[56:59], a[188:191], v[152:155]\n\t"
"v_accvgpr_write_b32 a19, v99\n\t"
"v_fma_f32 v98, -v26, s56, -v124\n\t"
"v_mfma_f32_16x16x32_bf16 v[152:155], a[64:67], a[192:195], v[152:155]\n\t"
"v_fma_f32 v99, -v27, s56, -v125\n\t"
"v_accvgpr_write_b32 a24, v98\n\t"
"v_mfma_f32_16x16x32_bf16 v[152:155], a[136:139], a[196:199], v[152:155]\n\t"
"v_fma_f32 v98, -v28, s56, -v126\n\t"
"v_accvgpr_write_b32 a25, v99\n\t"
"v_mfma_f32_16x16x32_bf16 v[156:159], a[136:139], a[200:203], v[156:159]\n\t"
"v_fma_f32 v99, -v29, s56, -v127\n\t"
"v_accvgpr_write_b32 a26, v98\n\t"
"v_mfma_f32_16x16x32_bf16 v[156:159], a[64:67], a[196:199], v[156:159]\n\t"
"v_accvgpr_write_b32 a27, v99\n\t"
"v_fma_f32 v98, -v30, s56, -v128\n\t"
"v_mfma_f32_16x16x32_bf16 v[156:159], a[56:59], a[192:195], v[156:159]\n\t"
"v_fma_f32 v99, -v31, s56, -v129\n\t"
"v_accvgpr_write_b32 a32, v98\n\t"
"v_mfma_f32_16x16x32_bf16 v[160:163], a[56:59], a[196:199], v[160:163]\n\t"
"s_waitcnt vmcnt(27)\n\t"
"v_fma_f32 v98, -v32, s56, -v130\n\t"
"v_accvgpr_write_b32 a33, v99\n\t"
"v_mfma_f32_16x16x32_bf16 v[160:163], a[64:67], a[200:203], v[160:163]\n\t"
"v_fma_f32 v99, -v33, s56, -v131\n\t"
"v_accvgpr_write_b32 a34, v98\n\t"
"v_mfma_f32_16x16x32_bf16 v[160:163], a[136:139], a[204:207], v[160:163]\n\t"
"v_accvgpr_write_b32 a35, v99\n\t"
"v_fma_f32 v98, -v34, s56, -v132\n\t"
"v_mfma_f32_16x16x32_bf16 v[164:167], a[12:15], a[168:171], v[164:167]\n\t"
"v_fma_f32 v99, -v35, s56, -v133\n\t"
"v_accvgpr_write_b32 a40, v98\n\t"
"v_mfma_f32_16x16x32_bf16 v[164:167], a[216:219], a[176:179], v[164:167]\n\t"
"v_fma_f32 v98, -v36, s56, -v134\n\t"
"v_mfma_f32_16x16x32_bf16 v[164:167], a[212:215], a[172:175], v[164:167]\n\t"
"s_waitcnt lgkmcnt(0)\n\t"
"s_barrier\n\t"
"v_mfma_f32_16x16x32_bf16 v[168:171], a[12:15], a[172:175], v[168:171]\n\t"
"ds_read_b128 a[80:83], v252 offset:0\n\t"
"v_mfma_f32_16x16x32_bf16 v[168:171], a[212:215], a[176:179], v[168:171]\n\t"
"ds_read_b128 a[88:91], v252 offset:1024\n\t"
"v_accvgpr_write_b32 a41, v99\n\t"
"v_mfma_f32_16x16x32_bf16 v[168:171], a[216:219], a[180:183], v[168:171]\n\t"
"ds_read_b128 a[96:99], v252 offset:2048\n\t"
"v_fma_f32 v99, -v37, s56, -v135\n\t"
"v_mfma_f32_16x16x32_bf16 v[172:175], a[216:219], a[184:187], v[172:175]\n\t"
"s_add_u32 s29, s23, 787584\n\t"
"ds_read_b128 a[168:171], v222 offset:512\n\t"
"v_accvgpr_write_b32 a42, v98\n\t"
"v_mfma_f32_16x16x32_bf16 v[172:175], a[212:215], a[180:183], v[172:175]\n\t"
"ds_read_b128 a[172:175], v223 offset:6656\n\t"
"v_accvgpr_write_b32 a43, v99\n\t"
"v_mfma_f32_16x16x32_bf16 v[172:175], a[12:15], a[176:179], v[172:175]\n\t"
"ds_read_b128 a[176:179], v223 offset:12800\n\t"
"v_fma_f32 v98, -v38, s56, -v136\n\t"
"v_mfma_f32_16x16x32_bf16 v[176:179], a[12:15], a[180:183], v[176:179]\n\t"
"s_waitcnt lgkmcnt(5)\n\t"
"ds_read_b128 a[104:107], v252 offset:12288\n\t"
"v_fma_f32 v99, -v39, s56, -v137\n\t"
"v_mfma_f32_16x16x32_bf16 v[176:179], a[212:215], a[184:187], v[176:179]\n\t"
"ds_read_b128 a[180:183], v222 offset:20480\n\t"
"v_accvgpr_write_b32 a48, v98\n\t"
"v_mfma_f32_16x16x32_bf16 v[176:179], a[216:219], a[188:191], v[176:179]\n\t"
"ds_read_b128 a[112:115], v252 offset:13312\n\t"
"v_fma_f32 v98, -v40, s56, -v138\n\t"
"v_mfma_f32_16x16x32_bf16 v[180:183], a[216:219], a[192:195], v[180:183]\n\t"
"s_waitcnt lgkmcnt(7)\n\t"
"ds_read_b128 a[120:123], v252 offset:14336\n\t"
"v_accvgpr_write_b32 a49, v99\n\t"
"v_mfma_f32_16x16x32_bf16 v[180:183], a[212:215], a[188:191], v[180:183]\n\t"
"v_fma_f32 v99, -v41, s56, -v139\n\t"
"v_accvgpr_write_b32 a50, v98\n\t"
"v_mfma_f32_16x16x32_bf16 v[180:183], a[12:15], a[184:187], v[180:183]\n\t"
"ds_read_b128 a[184:187], v223 offset:26624\n\t"
"v_accvgpr_write_b32 a51, v99\n\t"
"v_mfma_f32_16x16x32_bf16 v[184:187], a[12:15], a[188:191], v[184:187]\n\t"
"s_waitcnt lgkmcnt(8)\n\t"
"ds_read_b128 a[188:191], v223 offset:32768\n\t"
"v_fma_f32 v98, -v42, s56, -v140\n\t"
"v_mfma_f32_16x16x32_bf16 v[184:187], a[212:215], a[192:195], v[184:187]\n\t"
"s_mov_b32 s20, 159744\n\t"
"v_fma_f32 v99, -v43, s56, -v141\n\t"
"v_accvgpr_write_b32 a56, v98\n\t"
"v_mfma_f32_16x16x32_bf16 v[184:187], a[216:219], a[196:199], v[184:187]\n\t"
"v_fma_f32 v98, -v44, s56, -v142\n\t"
"v_accvgpr_write_b32 a57, v99\n\t"
"v_mfma_f32_16x16x32_bf16 v[188:191], a[216:219], a[200:203], v[188:191]\n\t"
"s_waitcnt lgkmcnt(5)\n\t"
"v_fma_f32 v99, -v45, s56, -v143\n\t"
"v_accvgpr_write_b32 a58, v98\n\t"
"v_mfma_f32_16x16x32_bf16 v[188:191], a[212:215], a[196:199], v[188:191]\n\t"
"v_accvgpr_write_b32 a59, v99\n\t"
"v_fma_f32 v98, -v46, s56, -v144\n\t"
"v_mfma_f32_16x16x32_bf16 v[188:191], a[12:15], a[192:195], v[188:191]\n\t"
"buffer_store_dwordx4 a[80:83], v248, s[16:19], s29 offen offset:0 nt\n\t"
"ds_read_b128 a[192:195], v222 offset:40448\n\t"
"v_fma_f32 v99, -v47, s56, -v145\n\t"
"v_accvgpr_write_b32 a64, v98\n\t"
"v_fma_f32 v98, -v48, s56, -v146\n\t"
"v_mfma_f32_16x16x32_bf16 v[192:195], a[12:15], a[196:199], v[192:195]\n\t"
"s_waitcnt vmcnt(23)\n\t"
"ds_read_b128 a[196:199], v223 offset:46592\n\t"
"v_accvgpr_write_b32 a65, v99\n\t"
"v_mfma_f32_16x16x32_bf16 v[192:195], a[212:215], a[200:203], v[192:195]\n\t"
"s_waitcnt lgkmcnt(5)\n\t"
"ds_read_b128 a[200:203], v223 offset:52736\n\t"
"v_fma_f32 v99, -v49, s56, -v147\n\t"
"v_mfma_f32_16x16x32_bf16 v[192:195], a[216:219], a[204:207], v[192:195]\n\t"
"buffer_store_dwordx4 a[88:91], v248, s[16:19], s29 offen offset:128 nt\n\t"
"buffer_load_dwordx4 a[212:215], off, s[80:83], s20 offset:0 sc1\n\t"
"ds_read_b128 a[204:207], v222 offset:60416\n\t"
"Lst20_%=:\n\t"
"v_mfma_f32_16x16x32_bf16 v[196:199], a[220:223], a[168:171], v[196:199]\n\t"
"s_waitcnt lgkmcnt(6)\n\t"
"v_accvgpr_write_b32 a66, v98\n\t"
"v_accvgpr_write_b32 a67, v99\n\t"
"v_mfma_f32_16x16x32_bf16 v[196:199], a[228:231], a[176:179], v[196:199]\n\t"
"v_fma_f32 v98, -v50, s56, -v148\n\t"
"v_fma_f32 v99, -v51, s56, -v149\n\t"
"v_mfma_f32_16x16x32_bf16 v[196:199], a[224:227], a[172:175], v[196:199]\n\t"
"buffer_store_dwordx4 a[96:99], v248, s[16:19], s29 offen offset:256 nt\n\t"
"buffer_load_dwordx4 a[216:219], off, s[80:83], s20 offset:1024 sc1\n\t"
"v_accvgpr_write_b32 a72, v98\n\t"
"s_add_u32 s29, s23, 1180800\n\t"
"v_mfma_f32_16x16x32_bf16 v[204:207], a[220:223], a[172:175], v[204:207]\n\t"
"s_waitcnt lgkmcnt(5)\n\t"
"v_fma_f32 v98, -v52, s56, -v150\n\t"
"v_accvgpr_write_b32 a73, v99\n\t"
"v_mfma_f32_16x16x32_bf16 v[204:207], a[224:227], a[176:179], v[204:207]\n\t"
"v_fma_f32 v99, -v53, s56, -v151\n\t"
"v_accvgpr_write_b32 a74, v98\n\t"
"v_mfma_f32_16x16x32_bf16 v[204:207], a[228:231], a[180:183], v[204:207]\n\t"
"buffer_store_dwordx4 a[104:107], v248, s[16:19], s29 offen offset:0 nt\n\t"
"v_accvgpr_write_b32 a75, v99\n\t"
"v_fma_f32 v98, -v54, s56, -v152\n\t"
"v_fma_f32 v99, -v55, s56, -v153\n\t"
"v_accvgpr_write_b32 a80, v98\n\t"
"v_mfma_f32_16x16x32_bf16 v[212:215], a[228:231], a[184:187], v[212:215]\n\t"
"s_waitcnt lgkmcnt(4)\n\t"
"v_fma_f32 v98, -v56, s56, -v154\n\t"
"v_accvgpr_write_b32 a81, v99\n\t"
"v_mfma_f32_16x16x32_bf16 v[212:215], a[224:227], a[180:183], v[212:215]\n\t"
"v_fma_f32 v99, -v57, s56, -v155\n\t"
"v_accvgpr_write_b32 a82, v98\n\t"
"v_mfma_f32_16x16x32_bf16 v[212:215], a[220:223], a[176:179], v[212:215]\n\t"
"buffer_store_dwordx4 a[112:115], v248, s[16:19], s29 offen offset:128 nt\n\t"
"v_accvgpr_write_b32 a83, v99\n\t"
"v_fma_f32 v98, -v58, s56, -v156\n\t"
"v_fma_f32 v99, -v59, s56, -v157\n\t"
"v_accvgpr_write_b32 a88, v98\n\t"
"v_mfma_f32_16x16x32_bf16 a[0:3], a[220:223], a[180:183], a[0:3]\n\t"
"s_waitcnt lgkmcnt(3)\n\t"
"v_fma_f32 v98, -v60, s56, -v158\n\t"
"v_accvgpr_write_b32 a89, v99\n\t"
"v_mfma_f32_16x16x32_bf16 a[0:3], a[224:227], a[184:187], a[0:3]\n\t"
"v_fma_f32 v99, -v61, s56, -v159\n\t"
"v_accvgpr_write_b32 a90, v98\n\t"
"v_mfma_f32_16x16x32_bf16 a[0:3], a[228:231], a[188:191], a[0:3]\n\t"
"buffer_store_dwordx4 a[120:123], v248, s[16:19], s29 offen offset:256 nt\n\t"
"v_accvgpr_write_b32 a91, v99\n\t"
"v_fma_f32 v98, -v62, s56, -v160\n\t"
"v_fma_f32 v99, -v63, s56, -v161\n\t"
"v_accvgpr_write_b32 a96, v98\n\t"
"v_mfma_f32_16x16x32_bf16 a[8:11], a[228:231], a[192:195], a[8:11]\n\t"
"s_waitcnt lgkmcnt(2)\n\t"
"v_fma_f32 v98, -v64, s56, -v162\n\t"
"v_accvgpr_write_b32 a97, v99\n\t"
"v_mfma_f32_16x16x32_bf16 a[8:11], a[224:227], a[188:191], a[8:11]\n\t"
"v_fma_f32 v99, -v65, s56, -v163\n\t"
"v_accvgpr_write_b32 a98, v98\n\t"
"v_mfma_f32_16x16x32_bf16 a[8:11], a[220:223], a[184:187], a[8:11]\n\t"
"buffer_load_dwordx4 v[248:251], off, s[80:83], s20 offset:2048 sc1\n\t"
"v_mfma_f32_16x16x32_bf16 a[16:19], a[220:223], a[188:191], a[16:19]\n\t"
"s_waitcnt lgkmcnt(1)\n\t"
"v_accvgpr_write_b32 a99, v99\n\t"
"v_fma_f32 v98, -v66, s56, -v164\n\t"
"v_mfma_f32_16x16x32_bf16 a[16:19], a[224:227], a[192:195], a[16:19]\n\t"
"s_mov_b32 s20, 172032\n\t"
"v_fma_f32 v99, -v67, s56, -v165\n\t"
"v_accvgpr_write_b32 a104, v98\n\t"
"v_mfma_f32_16x16x32_bf16 a[16:19], a[228:231], a[196:199], a[16:19]\n\t"
"v_fma_f32 v98, -v68, s56, -v166\n\t"
"v_accvgpr_write_b32 a105, v99\n\t"
"v_mfma_f32_16x16x32_bf16 a[24:27], a[228:231], a[200:203], a[24:27]\n\t"
"s_waitcnt lgkmcnt(0)\n\t"
"v_fma_f32 v99, -v69, s56, -v167\n\t"
"v_accvgpr_write_b32 a106, v98\n\t"
"v_mfma_f32_16x16x32_bf16 a[24:27], a[224:227], a[196:199], a[24:27]\n\t"
"v_accvgpr_write_b32 a107, v99\n\t"
"v_fma_f32 v98, -v70, s56, -v168\n\t"
"v_mfma_f32_16x16x32_bf16 a[24:27], a[220:223], a[192:195], a[24:27]\n\t"
"v_fma_f32 v99, -v71, s56, -v169\n\t"
"v_accvgpr_write_b32 a112, v98\n\t"
"v_mfma_f32_16x16x32_bf16 a[32:35], a[220:223], a[196:199], a[32:35]\n\t"
"s_waitcnt vmcnt(25)\n\t"
"v_fma_f32 v98, -v72, s56, -v170\n\t"
"v_accvgpr_write_b32 a113, v99\n\t"
"v_mfma_f32_16x16x32_bf16 a[32:35], a[224:227], a[200:203], a[32:35]\n\t"
"v_fma_f32 v99, -v73, s56, -v171\n\t"
"v_accvgpr_write_b32 a114, v98\n\t"
"v_mfma_f32_16x16x32_bf16 a[32:35], a[228:231], a[204:207], a[32:35]\n\t"
"v_accvgpr_write_b32 a115, v99\n\t"
"v_fma_f32 v98, -v74, s56, -v172\n\t"
"v_mfma_f32_16x16x32_bf16 a[40:43], a[232:235], a[168:171], a[40:43]\n\t"
"v_fma_f32 v99, -v75, s56, -v173\n\t"
"v_accvgpr_write_b32 a120, v98\n\t"
"v_mfma_f32_16x16x32_bf16 a[40:43], a[244:247], a[176:179], a[40:43]\n\t"
"v_fma_f32 v98, -v76, s56, -v174\n\t"
"v_accvgpr_write_b32 a121, v99\n\t"
"v_mfma_f32_16x16x32_bf16 a[40:43], a[236:239], a[172:175], a[40:43]\n\t"
"buffer_load_dwordx4 a[220:223], off, s[80:83], s20 offset:0 sc1\n\t"
"v_mfma_f32_16x16x32_bf16 a[48:51], a[232:235], a[172:175], a[48:51]\n\t"
"buffer_load_dwordx4 a[224:227], off, s[80:83], s20 offset:1024 sc1\n\t"
"v_mfma_f32_16x16x32_bf16 a[48:51], a[236:239], a[176:179], a[48:51]\n\t"
"buffer_load_dwordx4 a[228:231], off, s[80:83], s20 offset:2048 sc1\n\t"
"v_mfma_f32_16x16x32_bf16 a[48:51], a[244:247], a[180:183], a[48:51]\n\t"
"v_fma_f32 v99, -v77, s56, -v175\n\t"
"v_accvgpr_write_b32 a122, v98\n\t"
"v_mfma_f32_16x16x32_bf16 a[56:59], a[244:247], a[184:187], a[56:59]\n\t"
"v_accvgpr_write_b32 a123, v99\n\t"
"v_fma_f32 v98, -v78, s56, -v176\n\t"
"v_mfma_f32_16x16x32_bf16 a[56:59], a[236:239], a[180:183], a[56:59]\n\t"
"v_fma_f32 v99, -v79, s56, -v177\n\t"
"v_accvgpr_write_b32 a128, v98\n\t"
"v_mfma_f32_16x16x32_bf16 a[56:59], a[232:235], a[176:179], a[56:59]\n\t"
"v_fma_f32 v98, -v80, s56, -v178\n\t"
"v_accvgpr_write_b32 a129, v99\n\t"
"v_mfma_f32_16x16x32_bf16 a[64:67], a[232:235], a[180:183], a[64:67]\n\t"
"v_fma_f32 v99, -v81, s56, -v179\n\t"
"v_accvgpr_write_b32 a130, v98\n\t"
"v_mfma_f32_16x16x32_bf16 a[64:67], a[236:239], a[184:187], a[64:67]\n\t"
"v_accvgpr_write_b32 a131, v99\n\t"
"v_mfma_f32_16x16x32_bf16 a[64:67], a[244:247], a[188:191], a[64:67]\n\t"
"s_barrier\n\t"
"v_mfma_f32_16x16x32_bf16 a[72:75], a[244:247], a[192:195], a[72:75]\n\t"
"v_fma_f32 v98, -v82, s56, -v180\n\t"
"v_mfma_f32_16x16x32_bf16 a[72:75], a[236:239], a[188:191], a[72:75]\n\t"
"v_fma_f32 v99, -v83, s56, -v181\n\t"
"v_accvgpr_write_b32 a136, v98\n\t"
"v_mfma_f32_16x16x32_bf16 a[72:75], a[232:235], a[184:187], a[72:75]\n\t"
"v_fma_f32 v98, -v84, s56, -v182\n\t"
"v_accvgpr_write_b32 a137, v99\n\t"
"v_mfma_f32_16x16x32_bf16 a[80:83], a[232:235], a[188:191], a[80:83]\n\t"
"s_mov_b32 s20, 36864\n\t"
"v_fma_f32 v99, -v85, s56, -v183\n\t"
"v_accvgpr_write_b32 a138, v98\n\t"
"v_mfma_f32_16x16x32_bf16 a[80:83], a[236:239], a[192:195], a[80:83]\n\t"
"v_accvgpr_write_b32 a139, v99\n\t"
"v_fma_f32 v98, -v86, s56, -v184\n\t"
"v_mfma_f32_16x16x32_bf16 a[80:83], a[244:247], a[196:199], a[80:83]\n\t"
"v_fma_f32 v99, -v87, s56, -v185\n\t"
"v_accvgpr_write_b32 a144, v98\n\t"
"v_mfma_f32_16x16x32_bf16 a[88:91], a[244:247], a[200:203], a[88:91]\n\t"
"v_fma_f32 v98, -v88, s56, -v186\n\t"
"v_accvgpr_write_b32 a145, v99\n\t"
"v_mfma_f32_16x16x32_bf16 a[88:91], a[236:239], a[196:199], a[88:91]\n\t"
"v_fma_f32 v99, -v89, s56, -v187\n\t"
"v_accvgpr_write_b32 a146, v98\n\t"
"v_mfma_f32_16x16x32_bf16 a[88:91], a[232:235], a[192:195], a[88:91]\n\t"
"v_accvgpr_write_b32 a147, v99\n\t"
"v_fma_f32 v98, -v90, s56, -v188\n\t"
"v_mfma_f32_16x16x32_bf16 a[96:99], a[232:235], a[196:199], a[96:99]\n\t"
"s_waitcnt vmcnt(23)\n\t"
"v_fma_f32 v99, -v91, s56, -v189\n\t"
"v_accvgpr_write_b32 a152, v98\n\t"
"v_mfma_f32_16x16x32_bf16 a[96:99], a[236:239], a[200:203], a[96:99]\n\t"
"v_fma_f32 v98, -v92, s56, -v190\n\t"
"v_accvgpr_write_b32 a153, v99\n\t"
"v_mfma_f32_16x16x32_bf16 a[96:99], a[244:247], a[204:207], a[96:99]\n\t"
"v_fma_f32 v99, -v93, s56, -v191\n\t"
"v_accvgpr_write_b32 a154, v98\n\t"
"v_mfma_f32_16x16x32_bf16 a[104:107], a[240:243], a[168:171], a[104:107]\n\t"
"ds_read_b128 a[168:171], v222 offset:768\n\t"
"v_accvgpr_write_b32 a155, v99\n\t"
"v_mfma_f32_16x16x32_bf16 a[104:107], a[252:255], a[176:179], a[104:107]\n\t"
"v_fma_f32 v98, -v94, s56, -v192\n\t"
"v_fma_f32 v99, -v95, s56, -v193\n\t"
"v_mfma_f32_16x16x32_bf16 a[104:107], a[248:251], a[172:175], a[104:107]\n\t"
"buffer_load_dwordx4 a[232:235], off, s[80:83], s20 offset:0 sc1\n\t"
"v_mfma_f32_16x16x32_bf16 a[112:115], a[240:243], a[172:175], a[112:115]\n\t"
"buffer_load_dwordx4 a[236:239], off, s[80:83], s20 offset:1024 sc1\n\t"
"v_mfma_f32_16x16x32_bf16 a[112:115], a[248:251], a[176:179], a[112:115]\n\t"
"buffer_load_dwordx4 a[244:247], off, s[80:83], s20 offset:2048 sc1\n\t"
"v_mfma_f32_16x16x32_bf16 a[112:115], a[252:255], a[180:183], a[112:115]\n\t"
"ds_read_b128 a[172:175], v223 offset:6912\n\t"
"v_accvgpr_write_b32 a160, v98\n\t"
"v_mfma_f32_16x16x32_bf16 a[120:123], a[252:255], a[184:187], a[120:123]\n\t"
"v_fma_f32 v98, -v96, s56, -v194\n\t"
"v_accvgpr_write_b32 a161, v99\n\t"
"v_mfma_f32_16x16x32_bf16 a[120:123], a[248:251], a[180:183], a[120:123]\n\t"
"v_fma_f32 v99, -v97, s56, -v195\n\t"
"v_accvgpr_write_b32 a162, v98\n\t"
"v_mfma_f32_16x16x32_bf16 a[120:123], a[240:243], a[176:179], a[120:123]\n\t"
"ds_read_b128 a[176:179], v223 offset:13056\n\t"
"v_accvgpr_write_b32 a163, v99\n\t"
"v_mfma_f32_16x16x32_bf16 a[128:131], a[240:243], a[180:183], a[128:131]\n\t"
"ds_read_b128 a[180:183], v222 offset:20736\n\t"
"v_fma_f32 v200, v100, s64, -v2\n\t"
"v_mfma_f32_16x16x32_bf16 a[128:131], a[248:251], a[184:187], a[128:131]\n\t"
"Lpg15_%=:\n\t"
"s_cbranch_vccnz Lvr1_%=\n\t"
"v_fma_f32 v201, v101, s64, -v3\n\t"
"v_fma_f32 v202, v102, s64, -v4\n\t"
"v_mfma_f32_16x16x32_bf16 a[128:131], a[252:255], a[188:191], a[128:131]\n\t"
"v_fma_f32 v203, v103, s64, -v5\n\t"
"v_fma_f32 v208, v104, s64, -v6\n\t"
"v_mfma_f32_16x16x32_bf16 a[136:139], a[252:255], a[192:195], a[136:139]\n\t"
"v_fma_f32 v209, v105, s64, -v7\n\t"
"v_fma_f32 v210, v106, s64, -v8\n\t"
"v_mfma_f32_16x16x32_bf16 a[136:139], a[248:251], a[188:191], a[136:139]\n\t"
"v_fma_f32 v211, v107, s64, -v9\n\t"
"v_fma_f32 v216, v108, s64, -v10\n\t"
"v_mfma_f32_16x16x32_bf16 a[136:139], a[240:243], a[184:187], a[136:139]\n\t"
"ds_read_b128 a[184:187], v223 offset:26880\n\t"
"v_fma_f32 v217, v109, s64, -v11\n\t"
"v_mfma_f32_16x16x32_bf16 a[144:147], a[240:243], a[188:191], a[144:147]\n\t"
"s_mov_b32 s20, 49152\n\t"
"ds_read_b128 a[188:191], v223 offset:33024\n\t"
"v_fma_f32 v218, v110, s64, -v12\n\t"
"v_mfma_f32_16x16x32_bf16 a[144:147], a[248:251], a[192:195], a[144:147]\n\t"
"v_fma_f32 v219, v111, s64, -v13\n\t"
"v_fma_f32 v98, v112, s64, -v14\n\t"
"v_mfma_f32_16x16x32_bf16 a[144:147], a[252:255], a[196:199], a[144:147]\n\t"
"v_fma_f32 v99, v113, s64, -v15\n\t"
"v_accvgpr_write_b32 a4, v98\n\t"
"v_mfma_f32_16x16x32_bf16 a[152:155], a[252:255], a[200:203], a[152:155]\n\t"
"v_fma_f32 v98, v114, s64, -v16\n\t"
"v_accvgpr_write_b32 a5, v99\n\t"
"v_mfma_f32_16x16x32_bf16 a[152:155], a[248:251], a[196:199], a[152:155]\n\t"
"v_fma_f32 v99, v115, s64, -v17\n\t"
"v_accvgpr_write_b32 a6, v98\n\t"
"v_mfma_f32_16x16x32_bf16 a[152:155], a[240:243], a[192:195], a[152:155]\n\t"
"ds_read_b128 a[192:195], v222 offset:40704\n\t"
"v_accvgpr_write_b32 a7, v99\n\t"
"v_mfma_f32_16x16x32_bf16 a[160:163], a[240:243], a[196:199], a[160:163]\n\t"
"s_waitcnt vmcnt(15) lgkmcnt(4)\n\t"
"ds_read_b128 a[196:199], v223 offset:46848\n\t"
"v_fma_f32 v98, v116, s64, -v18\n\t"
"v_mfma_f32_16x16x32_bf16 a[160:163], a[248:251], a[200:203], a[160:163]\n\t"
"ds_read_b128 a[200:203], v223 offset:52992\n\t"
"v_fma_f32 v99, v117, s64, -v19\n\t"
"v_mfma_f32_16x16x32_bf16 a[160:163], a[252:255], a[204:207], a[160:163]\n\t"
"ds_read_b128 a[204:207], v222 offset:60672\n\t"
"v_accvgpr_write_b32 a12, v98\n\t"
"v_mfma_f32_16x16x32_bf16 v[200:203], v[240:243], a[168:171], v[200:203]\n\t"
"s_waitcnt lgkmcnt(6)\n\t"
"v_fma_f32 v98, v118, s64, -v20\n\t"
"v_accvgpr_write_b32 a13, v99\n\t"
"v_mfma_f32_16x16x32_bf16 v[200:203], v[236:239], a[176:179], v[200:203]\n\t"
"v_fma_f32 v99, v119, s64, -v21\n\t"
"v_accvgpr_write_b32 a14, v98\n\t"
"v_mfma_f32_16x16x32_bf16 v[200:203], a[208:211], a[172:175], v[200:203]\n\t"
"buffer_load_dwordx4 a[240:243], off, s[80:83], s20 offset:0 sc1\n\t"
"v_mfma_f32_16x16x32_bf16 v[208:211], v[240:243], a[172:175], v[208:211]\n\t"
"buffer_load_dwordx4 a[248:251], off, s[80:83], s20 offset:1024 sc1\n\t"
"v_mfma_f32_16x16x32_bf16 v[208:211], a[208:211], a[176:179], v[208:211]\n\t"
"s_waitcnt lgkmcnt(5)\n\t"
"v_accvgpr_write_b32 a15, v99\n\t"
"v_mfma_f32_16x16x32_bf16 v[208:211], v[236:239], a[180:183], v[208:211]\n\t"
"s_waitcnt lgkmcnt(0)\n\t"
"s_barrier\n\t"
"v_mfma_f32_16x16x32_bf16 v[216:219], v[236:239], a[184:187], v[216:219]\n\t"
"v_fma_f32 v98, v120, s64, -v22\n\t"
"v_mfma_f32_16x16x32_bf16 v[216:219], a[208:211], a[180:183], v[216:219]\n\t"
"buffer_load_dwordx4 a[252:255], off, s[80:83], s20 offset:2048 sc1\n\t"
"v_mfma_f32_16x16x32_bf16 v[216:219], v[240:243], a[176:179], v[216:219]\n\t"
"ds_read_b64_tr_b16 v[232:233], v246 offset:1024\n\t"
"v_fma_f32 v99, v121, s64, -v23\n\t"
"v_mfma_f32_16x16x32_bf16 a[4:7], v[240:243], a[180:183], a[4:7]\n\t"
"ds_read_b64_tr_b16 v[228:229], v255 offset:1024\n\t"
"v_accvgpr_write_b32 a20, v98\n\t"
"v_mfma_f32_16x16x32_bf16 a[4:7], a[208:211], a[184:187], a[4:7]\n\t"
"v_fma_f32 v98, v122, s64, -v24\n\t"
"v_accvgpr_write_b32 a21, v99\n\t"
"v_mfma_f32_16x16x32_bf16 a[4:7], v[236:239], a[188:191], a[4:7]\n\t"
"v_fma_f32 v99, v123, s64, -v25\n\t"
"v_accvgpr_write_b32 a22, v98\n\t"
"v_mfma_f32_16x16x32_bf16 a[12:15], v[236:239], a[192:195], a[12:15]\n\t"
"v_accvgpr_write_b32 a23, v99\n\t"
"v_fma_f32 v98, v124, s64, -v26\n\t"
"v_mfma_f32_16x16x32_bf16 a[12:15], a[208:211], a[188:191], a[12:15]\n\t"
"v_fma_f32 v99, v125, s64, -v27\n\t"
"v_accvgpr_write_b32 a28, v98\n\t"
"v_mfma_f32_16x16x32_bf16 a[12:15], v[240:243], a[184:187], a[12:15]\n\t"
"v_fma_f32 v98, v126, s64, -v28\n\t"
"v_accvgpr_write_b32 a29, v99\n\t"
"v_fma_f32 v99, v127, s64, -v29\n\t"
"v_mfma_f32_16x16x32_bf16 a[20:23], v[240:243], a[188:191], a[20:23]\n\t"
"s_mov_b32 s20, 61440\n\t"
"v_accvgpr_write_b32 a30, v98\n\t"
"v_accvgpr_write_b32 a31, v99\n\t"
"v_mfma_f32_16x16x32_bf16 a[20:23], a[208:211], a[192:195], a[20:23]\n\t"
"v_fma_f32 v98, v128, s64, -v30\n\t"
"v_fma_f32 v99, v129, s64, -v31\n\t"
"v_mfma_f32_16x16x32_bf16 a[20:23], v[236:239], a[196:199], a[20:23]\n\t"
"v_accvgpr_write_b32 a36, v98\n\t"
"v_fma_f32 v98, v130, s64, -v32\n\t"
"v_mfma_f32_16x16x32_bf16 a[28:31], v[236:239], a[200:203], a[28:31]\n\t"
"v_accvgpr_write_b32 a37, v99\n\t"
"v_fma_f32 v99, v131, s64, -v33\n\t"
"v_mfma_f32_16x16x32_bf16 a[28:31], a[208:211], a[196:199], a[28:31]\n\t"
"v_accvgpr_write_b32 a38, v98\n\t"
"v_accvgpr_write_b32 a39, v99\n\t"
"v_mfma_f32_16x16x32_bf16 a[28:31], v[240:243], a[192:195], a[28:31]\n\t"
"v_fma_f32 v98, v132, s64, -v34\n\t"
"v_fma_f32 v99, v133, s64, -v35\n\t"
"v_mfma_f32_16x16x32_bf16 a[36:39], v[240:243], a[196:199], a[36:39]\n\t"
"s_waitcnt lgkmcnt(1)\n\t"
"v_accvgpr_write_b32 a44, v98\n\t"
"v_fma_f32 v98, v134, s64, -v36\n\t"
"v_mfma_f32_16x16x32_bf16 a[36:39], a[208:211], a[200:203], a[36:39]\n\t"
"s_waitcnt lgkmcnt(0)\n\t"
"v_accvgpr_write_b32 a45, v99\n\t"
"v_fma_f32 v99, v135, s64, -v37\n\t"
"v_mfma_f32_16x16x32_bf16 a[36:39], v[236:239], a[204:207], a[36:39]\n\t"
"v_accvgpr_write_b32 a46, v98\n\t"
"v_accvgpr_write_b32 a47, v99\n\t"
"v_mfma_f32_4x4x4_16b_bf16 v[232:235], v[232:233], v[220:221], 0\n\t"
"buffer_load_dwordx4 v[240:243], off, s[80:83], s20 offset:0 sc1\n\t"
"v_fma_f32 v98, v136, s64, -v38\n\t"
"v_fma_f32 v99, v137, s64, -v39\n\t"
"v_mfma_f32_4x4x4_16b_bf16 v[228:231], v[228:229], v[220:221], 0\n\t"
"s_waitcnt vmcnt(10)\n\t"
"v_mfma_f32_16x16x32_bf16 a[44:47], a[212:215], a[168:171], a[44:47]\n\t"
"buffer_load_dwordx4 a[208:211], off, s[80:83], s20 offset:1024 sc1\n\t"
"v_mfma_f32_16x16x32_bf16 a[44:47], v[248:251], a[176:179], a[44:47]\n\t"
"v_accvgpr_write_b32 a52, v98\n\t"
"v_fma_f32 v98, v138, s64, -v40\n\t"
"v_mfma_f32_16x16x32_bf16 a[44:47], a[216:219], a[172:175], a[44:47]\n\t"
"v_accvgpr_write_b32 a53, v99\n\t"
"v_fma_f32 v99, v139, s64, -v41\n\t"
"v_accvgpr_write_b32 a54, v98\n\t"
"v_accvgpr_write_b32 a55, v99\n\t"
"v_fma_f32 v98, v140, s64, -v42\n\t"
"v_fma_f32 v99, v141, s64, -v43\n\t"
"v_mfma_f32_16x16x32_bf16 a[52:55], a[212:215], a[172:175], a[52:55]\n\t"
"v_accvgpr_write_b32 a60, v98\n\t"
"v_fma_f32 v98, v142, s64, -v44\n\t"
"v_mfma_f32_16x16x32_bf16 a[52:55], a[216:219], a[176:179], a[52:55]\n\t"
"v_accvgpr_write_b32 a61, v99\n\t"
"v_fma_f32 v99, v143, s64, -v45\n\t"
"v_mfma_f32_16x16x32_bf16 a[52:55], v[248:251], a[180:183], a[52:55]\n\t"
"v_accvgpr_write_b32 a62, v98\n\t"
"v_accvgpr_write_b32 a63, v99\n\t"
"v_fma_f32 v98, v144, s64, -v46\n\t"
"v_fma_f32 v99, v145, s64, -v47\n\t"
"v_cvt_pk_bf16_f32 v232, v232, v233\n\t"
"v_cvt_pk_bf16_f32 v233, v234, v235\n\t"
"v_cvt_pk_bf16_f32 v234, v228, v229\n\t"
"v_cvt_pk_bf16_f32 v235, v230, v231\n\t"
"v_mfma_f32_16x16x32_bf16 a[60:63], v[248:251], a[184:187], a[60:63]\n\t"
"v_accvgpr_write_b32 a68, v98\n\t"
"v_fma_f32 v98, v146, s64, -v48\n\t"
"v_mfma_f32_16x16x32_bf16 a[60:63], a[216:219], a[180:183], a[60:63]\n\t"
"v_accvgpr_write_b32 a69, v99\n\t"
"v_fma_f32 v99, v147, s64, -v49\n\t"
"v_mfma_f32_16x16x32_bf16 a[60:63], a[212:215], a[176:179], a[60:63]\n\t"
"ds_write_b128 v245, v[232:235] offset:1024\n\t"
"v_accvgpr_write_b32 a70, v98\n\t"
"v_accvgpr_write_b32 a71, v99\n\t"
"v_fma_f32 v98, v148, s64, -v50\n\t"
"v_fma_f32 v99, v149, s64, -v51\n\t"
"v_mfma_f32_16x16x32_bf16 a[68:71], a[212:215], a[180:183], a[68:71]\n\t"
"ds_read_b64_tr_b16 v[236:237], v246 offset:7168\n\t"
"v_mfma_f32_16x16x32_bf16 a[68:71], a[216:219], a[184:187], a[68:71]\n\t"
"ds_read_b64_tr_b16 v[224:225], v255 offset:7168\n\t"
"v_accvgpr_write_b32 a76, v98\n\t"
"v_mfma_f32_16x16x32_bf16 a[68:71], v[248:251], a[188:191], a[68:71]\n\t"
"v_fma_f32 v98, v150, s64, -v52\n\t"
"v_accvgpr_write_b32 a77, v99\n\t"
"v_fma_f32 v99, v151, s64, -v53\n\t"
"v_accvgpr_write_b32 a78, v98\n\t"
"v_accvgpr_write_b32 a79, v99\n\t"
"v_fma_f32 v98, v152, s64, -v54\n\t"
"v_fma_f32 v99, v153, s64, -v55\n\t"
"v_mfma_f32_16x16x32_bf16 a[76:79], v[248:251], a[192:195], a[76:79]\n\t"
"v_accvgpr_write_b32 a84, v98\n\t"
"v_fma_f32 v98, v154, s64, -v56\n\t"
"v_mfma_f32_16x16x32_bf16 a[76:79], a[216:219], a[188:191], a[76:79]\n\t"
"v_accvgpr_write_b32 a85, v99\n\t"
"v_fma_f32 v99, v155, s64, -v57\n\t"
"v_mfma_f32_16x16x32_bf16 a[76:79], a[212:215], a[184:187], a[76:79]\n\t"
"v_accvgpr_write_b32 a86, v98\n\t"
"v_accvgpr_write_b32 a87, v99\n\t"
"v_fma_f32 v98, v156, s64, -v58\n\t"
"v_fma_f32 v99, v157, s64, -v59\n\t"
"v_mfma_f32_16x16x32_bf16 a[84:87], a[212:215], a[188:191], a[84:87]\n\t"
"v_accvgpr_write_b32 a92, v98\n\t"
"v_fma_f32 v98, v158, s64, -v60\n\t"
"v_mfma_f32_16x16x32_bf16 a[84:87], a[216:219], a[192:195], a[84:87]\n\t"
"v_accvgpr_write_b32 a93, v99\n\t"
"v_fma_f32 v99, v159, s64, -v61\n\t"
"v_mfma_f32_16x16x32_bf16 a[84:87], v[248:251], a[196:199], a[84:87]\n\t"
"v_accvgpr_write_b32 a94, v98\n\t"
"v_accvgpr_write_b32 a95, v99\n\t"
"v_fma_f32 v98, v160, s64, -v62\n\t"
"v_fma_f32 v99, v161, s64, -v63\n\t"
"v_mfma_f32_16x16x32_bf16 a[92:95], v[248:251], a[200:203], a[92:95]\n\t"
"v_accvgpr_write_b32 a100, v98\n\t"
"v_fma_f32 v98, v162, s64, -v64\n\t"
"v_mfma_f32_16x16x32_bf16 a[92:95], a[216:219], a[196:199], a[92:95]\n\t"
"v_accvgpr_write_b32 a101, v99\n\t"
"v_fma_f32 v99, v163, s64, -v65\n\t"
"v_mfma_f32_16x16x32_bf16 a[92:95], a[212:215], a[192:195], a[92:95]\n\t"
"v_accvgpr_write_b32 a102, v98\n\t"
"v_accvgpr_write_b32 a103, v99\n\t"
"s_nop 1\n\t"
"v_mfma_f32_16x16x32_bf16 a[100:103], a[212:215], a[196:199], a[100:103]\n\t"
"buffer_load_dwordx4 a[212:215], off, s[80:83], s20 offset:2048 sc1\n\t"
"v_mfma_f32_16x16x32_bf16 a[100:103], a[216:219], a[200:203], a[100:103]\n\t"
"s_waitcnt lgkmcnt(0)\n\t"
"s_mov_b32 s20, 73728\n\t"
"v_fma_f32 v98, v164, s64, -v66\n\t"
"v_mfma_f32_16x16x32_bf16 a[100:103], v[248:251], a[204:207], a[100:103]\n\t"
"v_fma_f32 v99, v165, s64, -v67\n\t"
"v_accvgpr_write_b32 a108, v98\n\t"
"v_fma_f32 v98, v166, s64, -v68\n\t"
"v_accvgpr_write_b32 a109, v99\n\t"
"v_fma_f32 v99, v167, s64, -v69\n\t"
"v_accvgpr_write_b32 a110, v98\n\t"
"v_accvgpr_write_b32 a111, v99\n\t"
"v_mfma_f32_4x4x4_16b_bf16 v[236:239], v[236:237], v[220:221], 0\n\t"
"s_waitcnt vmcnt(9)\n\t"
"v_fma_f32 v98, v168, s64, -v70\n\t"
"v_fma_f32 v99, v169, s64, -v71\n\t"
"v_mfma_f32_4x4x4_16b_bf16 v[224:227], v[224:225], v[220:221], 0\n\t"
"v_mfma_f32_16x16x32_bf16 a[108:111], a[220:223], a[168:171], a[108:111]\n\t"
"buffer_load_dwordx4 v[248:251], off, s[80:83], s20 offset:0 sc1\n\t"
"v_mfma_f32_16x16x32_bf16 a[108:111], a[228:231], a[176:179], a[108:111]\n\t"
"buffer_load_dwordx4 a[216:219], off, s[80:83], s20 offset:1024 sc1\n\t"
"v_mfma_f32_16x16x32_bf16 a[108:111], a[224:227], a[172:175], a[108:111]\n\t"
"ds_read_b128 a[168:171], v222 offset:2048\n\t"
"v_accvgpr_write_b32 a116, v98\n\t"
"v_fma_f32 v98, v170, s64, -v72\n\t"
"v_accvgpr_write_b32 a117, v99\n\t"
"v_fma_f32 v99, v171, s64, -v73\n\t"
"v_accvgpr_write_b32 a118, v98\n\t"
"v_accvgpr_write_b32 a119, v99\n\t"
"v_fma_f32 v98, v172, s64, -v74\n\t"
"v_fma_f32 v99, v173, s64, -v75\n\t"
"v_mfma_f32_16x16x32_bf16 a[116:119], a[220:223], a[172:175], a[116:119]\n\t"
"ds_read_b128 a[172:175], v223 offset:8192\n\t"
"v_accvgpr_write_b32 a124, v98\n\t"
"v_mfma_f32_16x16x32_bf16 a[116:119], a[224:227], a[176:179], a[116:119]\n\t"
"v_fma_f32 v98, v174, s64, -v76\n\t"
"v_accvgpr_write_b32 a125, v99\n\t"
"v_mfma_f32_16x16x32_bf16 a[116:119], a[228:231], a[180:183], a[116:119]\n\t"
"v_fma_f32 v99, v175, s64, -v77\n\t"
"v_accvgpr_write_b32 a126, v98\n\t"
"v_accvgpr_write_b32 a127, v99\n\t"
"v_fma_f32 v98, v176, s64, -v78\n\t"
"v_fma_f32 v99, v177, s64, -v79\n\t"
"v_cvt_pk_bf16_f32 v236, v236, v237\n\t"
"v_cvt_pk_bf16_f32 v237, v238, v239\n\t"
"v_cvt_pk_bf16_f32 v238, v224, v225\n\t"
"v_cvt_pk_bf16_f32 v239, v226, v227\n\t"
"v_mfma_f32_16x16x32_bf16 a[124:127], a[228:231], a[184:187], a[124:127]\n\t"
"v_accvgpr_write_b32 a132, v98\n\t"
"v_fma_f32 v98, v178, s64, -v80\n\t"
"v_mfma_f32_16x16x32_bf16 a[124:127], a[224:227], a[180:183], a[124:127]\n\t"
"v_accvgpr_write_b32 a133, v99\n\t"
"v_fma_f32 v99, v179, s64, -v81\n\t"
"v_mfma_f32_16x16x32_bf16 a[124:127], a[220:223], a[176:179], a[124:127]\n\t"
"ds_write_b128 v245, v[236:239] offset:7168\n\t"
"v_accvgpr_write_b32 a134, v98\n\t"
"v_accvgpr_write_b32 a135, v99\n\t"
"v_fma_f32 v98, v180, s64, -v82\n\t"
"v_fma_f32 v99, v181, s64, -v83\n\t"
"v_mfma_f32_16x16x32_bf16 a[132:135], a[220:223], a[180:183], a[132:135]\n\t"
"ds_read_b128 a[176:179], v223 offset:14336\n\t"
"v_mfma_f32_16x16x32_bf16 a[132:135], a[224:227], a[184:187], a[132:135]\n\t"
"ds_read_b128 a[180:183], v222 offset:22016\n\t"
"v_accvgpr_write_b32 a140, v98\n\t"
"v_mfma_f32_16x16x32_bf16 a[132:135], a[228:231], a[188:191], a[132:135]\n\t"
"ds_read_b64_tr_b16 v[228:229], v246 offset:13312\n\t"
"v_fma_f32 v98, v182, s64, -v84\n\t"
"v_accvgpr_write_b32 a141, v99\n\t"
"v_fma_f32 v99, v183, s64, -v85\n\t"
"v_accvgpr_write_b32 a142, v98\n\t"
"v_accvgpr_write_b32 a143, v99\n\t"
"v_fma_f32 v98, v184, s64, -v86\n\t"
"v_fma_f32 v99, v185, s64, -v87\n\t"
"v_mfma_f32_16x16x32_bf16 a[140:143], a[228:231], a[192:195], a[140:143]\n\t"
"ds_read_b64_tr_b16 v[232:233], v255 offset:13312\n\t"
"v_accvgpr_write_b32 a148, v98\n\t"
"v_mfma_f32_16x16x32_bf16 a[140:143], a[224:227], a[188:191], a[140:143]\n\t"
"v_fma_f32 v98, v186, s64, -v88\n\t"
"v_accvgpr_write_b32 a149, v99\n\t"
"v_mfma_f32_16x16x32_bf16 a[140:143], a[220:223], a[184:187], a[140:143]\n\t"
"ds_read_b128 a[184:187], v223 offset:28160\n\t"
"v_fma_f32 v99, v187, s64, -v89\n\t"
"v_accvgpr_write_b32 a150, v98\n\t"
"v_accvgpr_write_b32 a151, v99\n\t"
"v_fma_f32 v98, v188, s64, -v90\n\t"
"v_fma_f32 v99, v189, s64, -v91\n\t"
"v_mfma_f32_16x16x32_bf16 a[148:151], a[220:223], a[188:191], a[148:151]\n\t"
"ds_read_b128 a[188:191], v223 offset:34304\n\t"
"v_accvgpr_write_b32 a156, v98\n\t"
"v_mfma_f32_16x16x32_bf16 a[148:151], a[224:227], a[192:195], a[148:151]\n\t"
"v_fma_f32 v98, v190, s64, -v92\n\t"
"v_accvgpr_write_b32 a157, v99\n\t"
"v_mfma_f32_16x16x32_bf16 a[148:151], a[228:231], a[196:199], a[148:151]\n\t"
"v_fma_f32 v99, v191, s64, -v93\n\t"
"v_accvgpr_write_b32 a158, v98\n\t"
"v_accvgpr_write_b32 a159, v99\n\t"
"v_fma_f32 v98, v192, s64, -v94\n\t"
"v_fma_f32 v99, v193, s64, -v95\n\t"
"v_mfma_f32_16x16x32_bf16 a[156:159], a[228:231], a[200:203], a[156:159]\n\t"
"v_accvgpr_write_b32 a164, v98\n\t"
"v_fma_f32 v98, v194, s64, -v96\n\t"
"v_mfma_f32_16x16x32_bf16 a[156:159], a[224:227], a[196:199], a[156:159]\n\t"
"v_accvgpr_write_b32 a165, v99\n\t"
"v_fma_f32 v99, v195, s64, -v97\n\t"
"v_mfma_f32_16x16x32_bf16 a[156:159], a[220:223], a[192:195], a[156:159]\n\t"
"s_add_u32 m0, s25, -128\n\t"
"ds_read_b128 a[192:195], v222 offset:41984\n\t"
"v_accvgpr_write_b32 a166, v98\n\t"
"v_accvgpr_write_b32 a167, v99\n\t"
"s_nop 1\n\t"
"v_mfma_f32_16x16x32_bf16 a[164:167], a[220:223], a[196:199], a[164:167]\n\t"
"buffer_load_dwordx4 a[220:223], off, s[80:83], s20 offset:2048 sc1\n\t"
"v_mfma_f32_16x16x32_bf16 a[164:167], a[224:227], a[200:203], a[164:167]\n\t"
"s_waitcnt lgkmcnt(3)\n\t"
"ds_read_b128 a[196:199], v223 offset:48128\n\t"
"v_fma_f32 v0, v2, s52, v100\n\t"
"v_mfma_f32_16x16x32_bf16 a[164:167], a[228:231], a[204:207], a[164:167]\n\t"
"ds_read_b128 a[200:203], v223 offset:54272\n\t"
"v_fma_f32 v1, v3, s52, v101\n\t"
"v_fma_f32 v100, -v100, s66, v2\n\t"
"v_fma_f32 v2, v4, s52, v102\n\t"
"v_fma_f32 v101, -v101, s66, v3\n\t"
"v_fma_f32 v3, v5, s52, v103\n\t"
"v_fma_f32 v102, -v102, s66, v4\n\t"
"v_fma_f32 v103, -v103, s66, v5\n\t"
"v_mfma_f32_4x4x4_16b_bf16 v[228:231], v[228:229], v[220:221], 0\n\t"
"s_waitcnt vmcnt(9)\n\t"
"v_mfma_f32_4x4x4_16b_bf16 v[232:235], v[232:233], v[220:221], 0\n\t"
"buffer_load_dwordx4 v254, s[12:15], 0 offen offset:128 nt lds\n\t"
"v_fma_f32 v4, v6, s52, v104\n\t"
"v_fma_f32 v5, v7, s52, v105\n\t"
"s_add_u32 m0, s26, -128\n\t"
"Lst21_%=:\n\t"
"v_mfma_f32_16x16x32_bf16 v[0:3], a[232:235], a[168:171], v[0:3]\n\t"
"ds_read_b128 a[204:207], v222 offset:61952\n\t"
"v_fma_f32 v104, -v104, s66, v6\n\t"
"v_mfma_f32_16x16x32_bf16 v[0:3], a[244:247], a[176:179], v[0:3]\n\t"
"v_fma_f32 v6, v8, s52, v106\n\t"
"v_fma_f32 v105, -v105, s66, v7\n\t"
"v_mfma_f32_16x16x32_bf16 v[0:3], a[236:239], a[172:175], v[0:3]\n\t"
"s_mov_b64 exec, s[36:37]\n\t"
"buffer_load_dwordx4 v254, s[12:15], s27 offen offset:128 nt lds\n\t"
"s_mov_b64 exec, -1\n\t"
"v_fma_f32 v7, v9, s52, v107\n\t"
"v_fma_f32 v106, -v106, s66, v8\n\t"
"v_fma_f32 v107, -v107, s66, v9\n\t"
"v_fma_f32 v8, v10, s52, v108\n\t"
"v_fma_f32 v9, v11, s52, v109\n\t"
"s_add_u32 m0, s24, -128\n\t"
"s_add_u32 s28, s21, 0\n\t"
"v_mfma_f32_16x16x32_bf16 v[4:7], a[232:235], a[172:175], v[4:7]\n\t"
"buffer_load_dwordx4 v247, s[12:15], s28 offen offset:128 nt lds\n\t"
"v_mfma_f32_16x16x32_bf16 v[4:7], a[236:239], a[176:179], v[4:7]\n\t"
"s_add_u32 m0, s24, 6016\n\t"
"s_add_u32 s28, s21, 196608\n\t"
"v_fma_f32 v108, -v108, s66, v10\n\t"
"v_mfma_f32_16x16x32_bf16 v[4:7], a[244:247], a[180:183], v[4:7]\n\t"
"buffer_load_dwordx4 v247, s[12:15], s28 offen offset:128 nt lds\n\t"
"v_fma_f32 v10, v12, s52, v110\n\t"
"v_fma_f32 v109, -v109, s66, v11\n\t"
"v_fma_f32 v11, v13, s52, v111\n\t"
"v_fma_f32 v110, -v110, s66, v12\n\t"
"v_fma_f32 v111, -v111, s66, v13\n\t"
"v_cvt_pk_bf16_f32 v228, v228, v229\n\t"
"v_cvt_pk_bf16_f32 v229, v230, v231\n\t"
"v_fma_f32 v12, v14, s52, v112\n\t"
"v_fma_f32 v13, v15, s52, v113\n\t"
"v_cvt_pk_bf16_f32 v230, v232, v233\n\t"
"v_cvt_pk_bf16_f32 v231, v234, v235\n\t"
"s_add_u32 m0, s24, 12160\n\t"
"s_add_u32 s28, s21, 393216\n\t"
"s_waitcnt lgkmcnt(5)\n\t"
"v_mfma_f32_16x16x32_bf16 v[8:11], a[244:247], a[184:187], v[8:11]\n\t"
"buffer_load_dwordx4 v247, s[12:15], s28 offen offset:128 nt lds\n\t"
"v_mfma_f32_16x16x32_bf16 v[8:11], a[236:239], a[180:183], v[8:11]\n\t"
"s_add_u32 m0, s24, 19840\n\t"
"s_add_u32 s28, s21, 589824\n\t"
"v_fma_f32 v112, -v112, s66, v14\n\t"
"v_mfma_f32_16x16x32_bf16 v[8:11], a[232:235], a[176:179], v[8:11]\n\t"
"ds_write_b128 v245, v[228:231] offset:13312\n\t"
"v_fma_f32 v14, v16, s52, v114\n\t"
"v_fma_f32 v113, -v113, s66, v15\n\t"
"v_fma_f32 v15, v17, s52, v115\n\t"
"v_fma_f32 v114, -v114, s66, v16\n\t"
"v_fma_f32 v115, -v115, s66, v17\n\t"
"v_fma_f32 v16, v18, s52, v116\n\t"
"v_fma_f32 v17, v19, s52, v117\n\t"
"s_waitcnt lgkmcnt(5)\n\t"
"v_mfma_f32_16x16x32_bf16 v[12:15], a[232:235], a[180:183], v[12:15]\n\t"
"buffer_load_dwordx4 v247, s[12:15], s28 offen offset:128 nt lds\n\t"
"v_mfma_f32_16x16x32_bf16 v[12:15], a[236:239], a[184:187], v[12:15]\n\t"
"s_add_u32 m0, s24, 25984\n\t"
"s_add_u32 s28, s21, 786432\n\t"
"ds_read_b64_tr_b16 v[224:225], v246 offset:20992\n\t"
"v_mfma_f32_16x16x32_bf16 v[12:15], a[244:247], a[188:191], v[12:15]\n\t"
"buffer_load_dwordx4 v247, s[12:15], s28 offen offset:128 nt lds\n\t"
"v_fma_f32 v116, -v116, s66, v18\n\t"
"v_fma_f32 v18, v20, s52, v118\n\t"
"v_fma_f32 v117, -v117, s66, v19\n\t"
"v_fma_f32 v19, v21, s52, v119\n\t"
"v_fma_f32 v118, -v118, s66, v20\n\t"
"v_fma_f32 v119, -v119, s66, v21\n\t"
"v_fma_f32 v20, v22, s52, v120\n\t"
"v_fma_f32 v21, v23, s52, v121\n\t"
"s_add_u32 m0, s24, 32128\n\t"
"s_add_u32 s28, s21, 983040\n\t"
"s_waitcnt lgkmcnt(5)\n\t"
"v_mfma_f32_16x16x32_bf16 v[16:19], a[244:247], a[192:195], v[16:19]\n\t"
"buffer_load_dwordx4 v247, s[12:15], s28 offen offset:128 nt lds\n\t"
"v_mfma_f32_16x16x32_bf16 v[16:19], a[236:239], a[188:191], v[16:19]\n\t"
"ds_read_b64_tr_b16 v[236:237], v255 offset:20992\n\t"
"v_fma_f32 v120, -v120, s66, v22\n\t"
"v_mfma_f32_16x16x32_bf16 v[16:19], a[232:235], a[184:187], v[16:19]\n\t"
"v_fma_f32 v22, v24, s52, v122\n\t"
"v_fma_f32 v121, -v121, s66, v23\n\t"
"v_fma_f32 v23, v25, s52, v123\n\t"
"v_fma_f32 v122, -v122, s66, v24\n\t"
"v_fma_f32 v123, -v123, s66, v25\n\t"
"v_fma_f32 v24, v26, s52, v124\n\t"
"v_fma_f32 v25, v27, s52, v125\n\t"
"s_waitcnt lgkmcnt(5)\n\t"
"v_mfma_f32_16x16x32_bf16 v[20:23], a[232:235], a[188:191], v[20:23]\n\t"
"v_fma_f32 v124, -v124, s66, v26\n\t"
"v_fma_f32 v26, v28, s52, v126\n\t"
"v_mfma_f32_16x16x32_bf16 v[20:23], a[236:239], a[192:195], v[20:23]\n\t"
"v_fma_f32 v125, -v125, s66, v27\n\t"
"v_fma_f32 v27, v29, s52, v127\n\t"
"v_mfma_f32_16x16x32_bf16 v[20:23], a[244:247], a[196:199], v[20:23]\n\t"
"v_fma_f32 v126, -v126, s66, v28\n\t"
"v_fma_f32 v127, -v127, s66, v29\n\t"
"v_fma_f32 v28, v30, s52, v128\n\t"
"v_fma_f32 v29, v31, s52, v129\n\t"
"s_waitcnt lgkmcnt(4)\n\t"
"v_mfma_f32_16x16x32_bf16 v[24:27], a[244:247], a[200:203], v[24:27]\n\t"
"v_fma_f32 v128, -v128, s66, v30\n\t"
"v_fma_f32 v30, v32, s52, v130\n\t"
"v_mfma_f32_16x16x32_bf16 v[24:27], a[236:239], a[196:199], v[24:27]\n\t"
"v_fma_f32 v129, -v129, s66, v31\n\t"
"v_fma_f32 v31, v33, s52, v131\n\t"
"v_mfma_f32_16x16x32_bf16 v[24:27], a[232:235], a[192:195], v[24:27]\n\t"
"v_fma_f32 v130, -v130, s66, v32\n\t"
"v_fma_f32 v131, -v131, s66, v33\n\t"
"s_waitcnt lgkmcnt(3)\n\t"
"v_mfma_f32_16x16x32_bf16 v[28:31], a[232:235], a[196:199], v[28:31]\n\t"
"s_waitcnt lgkmcnt(1)\n\t"
"v_fma_f32 v32, v34, s52, v132\n\t"
"v_fma_f32 v33, v35, s52, v133\n\t"
"v_mfma_f32_16x16x32_bf16 v[28:31], a[236:239], a[200:203], v[28:31]\n\t"
"s_waitcnt lgkmcnt(0)\n\t"
"v_fma_f32 v132, -v132, s66, v34\n\t"
"v_fma_f32 v34, v36, s52, v134\n\t"
"v_mfma_f32_16x16x32_bf16 v[28:31], a[244:247], a[204:207], v[28:31]\n\t"
"v_fma_f32 v133, -v133, s66, v35\n\t"
"v_fma_f32 v35, v37, s52, v135\n\t"
"v_fma_f32 v134, -v134, s66, v36\n\t"
"v_fma_f32 v135, -v135, s66, v37\n\t"
"v_mfma_f32_4x4x4_16b_bf16 v[224:227], v[224:225], v[220:221], 0\n\t"
"s_waitcnt vmcnt(14)\n\t"
"v_mfma_f32_4x4x4_16b_bf16 v[236:239], v[236:237], v[220:221], 0\n\t"
"v_fma_f32 v36, v38, s52, v136\n\t"
"v_fma_f32 v37, v39, s52, v137\n\t"
"v_mfma_f32_16x16x32_bf16 v[32:35], a[240:243], a[168:171], v[32:35]\n\t"
"v_fma_f32 v136, -v136, s66, v38\n\t"
"v_fma_f32 v38, v40, s52, v138\n\t"
"v_mfma_f32_16x16x32_bf16 v[32:35], a[252:255], a[176:179], v[32:35]\n\t"
"v_fma_f32 v137, -v137, s66, v39\n\t"
"v_fma_f32 v39, v41, s52, v139\n\t"
"v_mfma_f32_16x16x32_bf16 v[32:35], a[248:251], a[172:175], v[32:35]\n\t"
"v_fma_f32 v138, -v138, s66, v40\n\t"
"v_fma_f32 v139, -v139, s66, v41\n\t"
"v_fma_f32 v40, v42, s52, v140\n\t"
"v_fma_f32 v41, v43, s52, v141\n\t"
"v_mfma_f32_16x16x32_bf16 v[36:39], a[240:243], a[172:175], v[36:39]\n\t"
"v_fma_f32 v140, -v140, s66, v42\n\t"
"v_fma_f32 v42, v44, s52, v142\n\t"
"v_mfma_f32_16x16x32_bf16 v[36:39], a[248:251], a[176:179], v[36:39]\n\t"
"v_fma_f32 v141, -v141, s66, v43\n\t"
"v_fma_f32 v43, v45, s52, v143\n\t"
"v_mfma_f32_16x16x32_bf16 v[36:39], a[252:255], a[180:183], v[36:39]\n\t"
"v_fma_f32 v142, -v142, s66, v44\n\t"
"v_fma_f32 v143, -v143, s66, v45\n\t"
"v_cvt_pk_bf16_f32 v224, v224, v225\n\t"
"v_cvt_pk_bf16_f32 v225, v226, v227\n\t"
"v_fma_f32 v44, v46, s52, v144\n\t"
"v_fma_f32 v45, v47, s52, v145\n\t"
"v_cvt_pk_bf16_f32 v226, v236, v237\n\t"
"v_cvt_pk_bf16_f32 v227, v238, v239\n\t"
"v_mfma_f32_16x16x32_bf16 v[40:43], a[252:255], a[184:187], v[40:43]\n\t"
"s_mov_b32 s20, 86016\n\t"
"v_fma_f32 v144, -v144, s66, v46\n\t"
"v_fma_f32 v46, v48, s52, v146\n\t"
"v_mfma_f32_16x16x32_bf16 v[40:43], a[248:251], a[180:183], v[40:43]\n\t"
"v_fma_f32 v145, -v145, s66, v47\n\t"
"v_fma_f32 v47, v49, s52, v147\n\t"
"v_mfma_f32_16x16x32_bf16 v[40:43], a[240:243], a[176:179], v[40:43]\n\t"
"ds_write_b128 v245, v[224:227] offset:20992\n\t"
"v_fma_f32 v146, -v146, s66, v48\n\t"
"v_fma_f32 v147, -v147, s66, v49\n\t"
"v_fma_f32 v48, v50, s52, v148\n\t"
"v_fma_f32 v49, v51, s52, v149\n\t"
"v_mfma_f32_16x16x32_bf16 v[44:47], a[240:243], a[180:183], v[44:47]\n\t"
"ds_read_b64_tr_b16 v[232:233], v246 offset:27136\n\t"
"v_mfma_f32_16x16x32_bf16 v[44:47], a[248:251], a[184:187], v[44:47]\n\t"
"ds_read_b64_tr_b16 v[228:229], v255 offset:27136\n\t"
"v_fma_f32 v148, -v148, s66, v50\n\t"
"v_mfma_f32_16x16x32_bf16 v[44:47], a[252:255], a[188:191], v[44:47]\n\t"
"v_fma_f32 v50, v52, s52, v150\n\t"
"v_fma_f32 v149, -v149, s66, v51\n\t"
"v_fma_f32 v51, v53, s52, v151\n\t"
"v_fma_f32 v150, -v150, s66, v52\n\t"
"v_fma_f32 v151, -v151, s66, v53\n\t"
"v_fma_f32 v52, v54, s52, v152\n\t"
"v_fma_f32 v53, v55, s52, v153\n\t"
"v_mfma_f32_16x16x32_bf16 v[48:51], a[252:255], a[192:195], v[48:51]\n\t"
"v_fma_f32 v152, -v152, s66, v54\n\t"
"v_fma_f32 v54, v56, s52, v154\n\t"
"v_mfma_f32_16x16x32_bf16 v[48:51], a[248:251], a[188:191], v[48:51]\n\t"
"Lpg16_%=:\n\t"
"s_cbranch_vccnz Lvr1_%=\n\t"
"v_fma_f32 v153, -v153, s66, v55\n\t"
"v_fma_f32 v55, v57, s52, v155\n\t"
"v_mfma_f32_16x16x32_bf16 v[48:51], a[240:243], a[184:187], v[48:51]\n\t"
"v_fma_f32 v154, -v154, s66, v56\n\t"
"v_fma_f32 v155, -v155, s66, v57\n\t"
"v_fma_f32 v56, v58, s52, v156\n\t"
"v_fma_f32 v57, v59, s52, v157\n\t"
"v_mfma_f32_16x16x32_bf16 v[52:55], a[240:243], a[188:191], v[52:55]\n\t"
"v_fma_f32 v156, -v156, s66, v58\n\t"
"v_fma_f32 v58, v60, s52, v158\n\t"
"v_mfma_f32_16x16x32_bf16 v[52:55], a[248:251], a[192:195], v[52:55]\n\t"
"v_fma_f32 v157, -v157, s66, v59\n\t"
"v_fma_f32 v59, v61, s52, v159\n\t"
"v_mfma_f32_16x16x32_bf16 v[52:55], a[252:255], a[196:199], v[52:55]\n\t"
"buffer_load_dwordx4 a[224:227], off, s[80:83], s20 offset:0 sc1\n\t"
"v_fma_f32 v158, -v158, s66, v60\n\t"
"v_fma_f32 v159, -v159, s66, v61\n\t"
"v_fma_f32 v60, v62, s52, v160\n\t"
"v_fma_f32 v61, v63, s52, v161\n\t"
"v_mfma_f32_16x16x32_bf16 v[56:59], a[252:255], a[200:203], v[56:59]\n\t"
"v_fma_f32 v160, -v160, s66, v62\n\t"
"v_fma_f32 v62, v64, s52, v162\n\t"
"v_mfma_f32_16x16x32_bf16 v[56:59], a[248:251], a[196:199], v[56:59]\n\t"
"v_fma_f32 v161, -v161, s66, v63\n\t"
"v_fma_f32 v63, v65, s52, v163\n\t"
"v_mfma_f32_16x16x32_bf16 v[56:59], a[240:243], a[192:195], v[56:59]\n\t"
"v_fma_f32 v162, -v162, s66, v64\n\t"
"v_fma_f32 v163, -v163, s66, v65\n\t"
"v_mfma_f32_16x16x32_bf16 v[60:63], a[240:243], a[196:199], v[60:63]\n\t"
"s_waitcnt lgkmcnt(1)\n\t"
"v_fma_f32 v64, v66, s52, v164\n\t"
"v_fma_f32 v65, v67, s52, v165\n\t"
"v_mfma_f32_16x16x32_bf16 v[60:63], a[248:251], a[200:203], v[60:63]\n\t"
"s_waitcnt lgkmcnt(0)\n\t"
"v_fma_f32 v164, -v164, s66, v66\n\t"
"v_fma_f32 v66, v68, s52, v166\n\t"
"v_mfma_f32_16x16x32_bf16 v[60:63], a[252:255], a[204:207], v[60:63]\n\t"
"v_fma_f32 v165, -v165, s66, v67\n\t"
"v_fma_f32 v67, v69, s52, v167\n\t"
"v_fma_f32 v166, -v166, s66, v68\n\t"
"v_fma_f32 v167, -v167, s66, v69\n\t"
"v_mfma_f32_4x4x4_16b_bf16 v[232:235], v[232:233], v[220:221], 0\n\t"
"s_waitcnt vmcnt(12)\n\t"
"v_mfma_f32_4x4x4_16b_bf16 v[228:231], v[228:229], v[220:221], 0\n\t"
"v_fma_f32 v68, v70, s52, v168\n\t"
"v_fma_f32 v69, v71, s52, v169\n\t"
"v_mfma_f32_16x16x32_bf16 v[64:67], v[240:243], a[168:171], v[64:67]\n\t"
"ds_read_b128 a[168:171], v222 offset:2304\n\t"
"v_fma_f32 v168, -v168, s66, v70\n\t"
"v_mfma_f32_16x16x32_bf16 v[64:67], a[212:215], a[176:179], v[64:67]\n\t"
"v_fma_f32 v70, v72, s52, v170\n\t"
"v_fma_f32 v169, -v169, s66, v71\n\t"
"v_mfma_f32_16x16x32_bf16 v[64:67], a[208:211], a[172:175], v[64:67]\n\t"
"v_fma_f32 v71, v73, s52, v171\n\t"
"v_fma_f32 v170, -v170, s66, v72\n\t"
"v_fma_f32 v171, -v171, s66, v73\n\t"
"v_fma_f32 v72, v74, s52, v172\n\t"
"v_fma_f32 v73, v75, s52, v173\n\t"
"v_mfma_f32_16x16x32_bf16 v[68:71], v[240:243], a[172:175], v[68:71]\n\t"
"ds_read_b128 a[172:175], v223 offset:8448\n\t"
"v_fma_f32 v172, -v172, s66, v74\n\t"
"v_mfma_f32_16x16x32_bf16 v[68:71], a[208:211], a[176:179], v[68:71]\n\t"
"v_fma_f32 v74, v76, s52, v174\n\t"
"v_fma_f32 v173, -v173, s66, v75\n\t"
"v_mfma_f32_16x16x32_bf16 v[68:71], a[212:215], a[180:183], v[68:71]\n\t"
"v_fma_f32 v75, v77, s52, v175\n\t"
"v_fma_f32 v174, -v174, s66, v76\n\t"
"v_fma_f32 v175, -v175, s66, v77\n\t"
"v_cvt_pk_bf16_f32 v232, v232, v233\n\t"
"v_cvt_pk_bf16_f32 v233, v234, v235\n\t"
"v_fma_f32 v76, v78, s52, v176\n\t"
"v_fma_f32 v77, v79, s52, v177\n\t"
"v_cvt_pk_bf16_f32 v234, v228, v229\n\t"
"v_cvt_pk_bf16_f32 v235, v230, v231\n\t"
"v_mfma_f32_16x16x32_bf16 v[72:75], a[212:215], a[184:187], v[72:75]\n\t"
"v_fma_f32 v176, -v176, s66, v78\n\t"
"v_fma_f32 v78, v80, s52, v178\n\t"
"v_mfma_f32_16x16x32_bf16 v[72:75], a[208:211], a[180:183], v[72:75]\n\t"
"v_fma_f32 v177, -v177, s66, v79\n\t"
"v_fma_f32 v79, v81, s52, v179\n\t"
"v_mfma_f32_16x16x32_bf16 v[72:75], v[240:243], a[176:179], v[72:75]\n\t"
"ds_write_b128 v245, v[232:235] offset:27136\n\t"
"v_fma_f32 v178, -v178, s66, v80\n\t"
"v_fma_f32 v179, -v179, s66, v81\n\t"
"v_fma_f32 v80, v82, s52, v180\n\t"
"v_fma_f32 v81, v83, s52, v181\n\t"
"v_mfma_f32_16x16x32_bf16 v[76:79], v[240:243], a[180:183], v[76:79]\n\t"
"ds_read_b128 a[176:179], v223 offset:14592\n\t"
"v_mfma_f32_16x16x32_bf16 v[76:79], a[208:211], a[184:187], v[76:79]\n\t"
"ds_read_b64_tr_b16 v[236:237], v246 offset:33280\n\t"
"v_fma_f32 v180, -v180, s66, v82\n\t"
"v_mfma_f32_16x16x32_bf16 v[76:79], a[212:215], a[188:191], v[76:79]\n\t"
"ds_read_b64_tr_b16 v[224:225], v255 offset:33280\n\t"
"v_fma_f32 v82, v84, s52, v182\n\t"
"v_fma_f32 v181, -v181, s66, v83\n\t"
"v_fma_f32 v83, v85, s52, v183\n\t"
"v_fma_f32 v182, -v182, s66, v84\n\t"
"v_fma_f32 v183, -v183, s66, v85\n\t"
"v_fma_f32 v84, v86, s52, v184\n\t"
"v_fma_f32 v85, v87, s52, v185\n\t"
"v_mfma_f32_16x16x32_bf16 v[80:83], a[212:215], a[192:195], v[80:83]\n\t"
"ds_read_b128 a[180:183], v222 offset:22272\n\t"
"v_fma_f32 v184, -v184, s66, v86\n\t"
"v_mfma_f32_16x16x32_bf16 v[80:83], a[208:211], a[188:191], v[80:83]\n\t"
"ds_read_b64_tr_b16 v[228:229], v246 offset:40960\n\t"
"v_fma_f32 v86, v88, s52, v186\n\t"
"v_mfma_f32_16x16x32_bf16 v[80:83], v[240:243], a[184:187], v[80:83]\n\t"
"ds_read_b64_tr_b16 v[232:233], v255 offset:40960\n\t"
"v_fma_f32 v185, -v185, s66, v87\n\t"
"v_fma_f32 v87, v89, s52, v187\n\t"
"v_fma_f32 v186, -v186, s66, v88\n\t"
"v_fma_f32 v187, -v187, s66, v89\n\t"
"v_fma_f32 v88, v90, s52, v188\n\t"
"v_fma_f32 v89, v91, s52, v189\n\t"
"v_mfma_f32_16x16x32_bf16 v[84:87], v[240:243], a[188:191], v[84:87]\n\t"
"ds_read_b128 a[184:187], v223 offset:28416\n\t"
"v_fma_f32 v188, -v188, s66, v90\n\t"
"v_mfma_f32_16x16x32_bf16 v[84:87], a[208:211], a[192:195], v[84:87]\n\t"
"ds_read_b128 a[188:191], v223 offset:34560\n\t"
"v_fma_f32 v90, v92, s52, v190\n\t"
"v_mfma_f32_16x16x32_bf16 v[84:87], a[212:215], a[196:199], v[84:87]\n\t"
"buffer_load_dwordx4 a[228:231], off, s[80:83], s20 offset:1024 sc1\n\t"
"v_fma_f32 v189, -v189, s66, v91\n\t"
"v_fma_f32 v91, v93, s52, v191\n\t"
"v_fma_f32 v190, -v190, s66, v92\n\t"
"v_fma_f32 v191, -v191, s66, v93\n\t"
"v_fma_f32 v92, v94, s52, v192\n\t"
"v_fma_f32 v93, v95, s52, v193\n\t"
"v_mfma_f32_16x16x32_bf16 v[88:91], a[212:215], a[200:203], v[88:91]\n\t"
"v_fma_f32 v192, -v192, s66, v94\n\t"
"v_fma_f32 v94, v96, s52, v194\n\t"
"v_mfma_f32_16x16x32_bf16 v[88:91], a[208:211], a[196:199], v[88:91]\n\t"
"v_fma_f32 v193, -v193, s66, v95\n\t"
"v_fma_f32 v95, v97, s52, v195\n\t"
"v_mfma_f32_16x16x32_bf16 v[88:91], v[240:243], a[192:195], v[88:91]\n\t"
"buffer_load_dwordx4 a[232:235], off, s[80:83], s20 offset:2048 sc1\n\t"
"v_fma_f32 v194, -v194, s66, v96\n\t"
"v_fma_f32 v195, -v195, s66, v97\n\t"
"s_mov_b32 s20, 98304\n\t"
"v_mfma_f32_16x16x32_bf16 v[92:95], v[240:243], a[196:199], v[92:95]\n\t"
"buffer_load_dwordx4 a[236:239], off, s[80:83], s20 offset:0 sc1\n\t"
"v_mfma_f32_16x16x32_bf16 v[92:95], a[208:211], a[200:203], v[92:95]\n\t"
"buffer_load_dwordx4 v[240:243], off, s[80:83], s20 offset:1024 sc1\n\t"
"v_mfma_f32_16x16x32_bf16 v[92:95], a[212:215], a[204:207], v[92:95]\n\t"
"s_waitcnt lgkmcnt(3)\n\t"
"ds_read_b128 a[192:195], v222 offset:42240\n\t"
"v_mfma_f32_4x4x4_16b_bf16 v[236:239], v[236:237], v[220:221], 0\n\t"
"s_waitcnt lgkmcnt(3)\n\t"
"v_mfma_f32_4x4x4_16b_bf16 v[224:227], v[224:225], v[220:221], 0\n\t"
"s_waitcnt vmcnt(13)\n\t"
"v_mfma_f32_4x4x4_16b_bf16 v[228:231], v[228:229], v[220:221], 0\n\t"
"v_mfma_f32_4x4x4_16b_bf16 v[232:235], v[232:233], v[220:221], 0\n\t"
"buffer_load_dwordx4 a[208:211], off, s[80:83], s20 offset:2048 sc1\n\t"
"s_mov_b32 s20, 331776\n\t"
"v_mfma_f32_16x16x32_bf16 v[100:103], v[248:251], a[168:171], v[100:103]\n\t"
"ds_read_b128 a[196:199], v223 offset:48384\n\t"
"v_cvt_pk_bf16_f32 v236, v236, v237\n\t"
"v_mfma_f32_16x16x32_bf16 v[100:103], a[220:223], a[176:179], v[100:103]\n\t"
"ds_read_b128 a[200:203], v223 offset:54528\n\t"
"v_cvt_pk_bf16_f32 v237, v238, v239\n\t"
"v_mfma_f32_16x16x32_bf16 v[100:103], a[216:219], a[172:175], v[100:103]\n\t"
"ds_read_b128 a[204:207], v222 offset:62208\n\t"
"v_cvt_pk_bf16_f32 v238, v224, v225\n\t"
"v_mfma_f32_16x16x32_bf16 v[104:107], v[248:251], a[172:175], v[104:107]\n\t"
"s_waitcnt lgkmcnt(5)\n\t"
"v_cvt_pk_bf16_f32 v239, v226, v227\n\t"
"v_cvt_pk_bf16_f32 v228, v228, v229\n\t"
"v_mfma_f32_16x16x32_bf16 v[104:107], a[216:219], a[176:179], v[104:107]\n\t"
"v_cvt_pk_bf16_f32 v229, v230, v231\n\t"
"v_mfma_f32_16x16x32_bf16 v[104:107], a[220:223], a[180:183], v[104:107]\n\t"
"s_waitcnt lgkmcnt(0)\n\t"
"s_barrier\n\t"
"v_mfma_f32_16x16x32_bf16 v[108:111], a[220:223], a[184:187], v[108:111]\n\t"
"v_cvt_pk_bf16_f32 v230, v232, v233\n\t"
"v_mfma_f32_16x16x32_bf16 v[108:111], a[216:219], a[180:183], v[108:111]\n\t"
"buffer_load_dwordx4 v[96:99], off, s[80:83], s20 offset:0 sc1\n\t"
"v_mfma_f32_16x16x32_bf16 v[108:111], v[248:251], a[176:179], v[108:111]\n\t"
"buffer_load_dwordx4 a[212:215], off, s[80:83], s20 offset:1024 sc1\n\t"
"v_cvt_pk_bf16_f32 v231, v234, v235\n\t"
"v_mfma_f32_16x16x32_bf16 v[112:115], v[248:251], a[180:183], v[112:115]\n\t"
"ds_read_b64_tr_b16 v[224:225], v246 offset:47104\n\t"
"v_mfma_f32_16x16x32_bf16 v[112:115], a[216:219], a[184:187], v[112:115]\n\t"
"buffer_load_dwordx4 a[240:243], off, s[80:83], s20 offset:2048 sc1\n\t"
"v_mfma_f32_16x16x32_bf16 v[112:115], a[220:223], a[188:191], v[112:115]\n\t"
"s_mov_b32 s20, 344064\n\t"
"ds_write_b128 v245, v[236:239] offset:33280\n\t"
"v_mfma_f32_16x16x32_bf16 v[116:119], a[220:223], a[192:195], v[116:119]\n\t"
"buffer_load_dwordx4 a[244:247], off, s[80:83], s20 offset:0 sc1\n\t"
"v_mfma_f32_16x16x32_bf16 v[116:119], a[216:219], a[188:191], v[116:119]\n\t"
"buffer_load_dwordx4 a[248:251], off, s[80:83], s20 offset:1024 sc1\n\t"
"v_mfma_f32_16x16x32_bf16 v[116:119], v[248:251], a[184:187], v[116:119]\n\t"
"ds_write_b128 v245, v[228:231] offset:40960\n\t"
"v_mfma_f32_16x16x32_bf16 v[120:123], v[248:251], a[188:191], v[120:123]\n\t"
"v_mfma_f32_16x16x32_bf16 v[120:123], a[216:219], a[192:195], v[120:123]\n\t"
"ds_read_b64_tr_b16 v[232:233], v255 offset:47104\n\t"
"v_mfma_f32_16x16x32_bf16 v[120:123], a[220:223], a[196:199], v[120:123]\n\t"
"ds_read_b64_tr_b16 v[236:237], v246 offset:53248\n\t"
"v_mfma_f32_16x16x32_bf16 v[124:127], a[220:223], a[200:203], v[124:127]\n\t"
"ds_read_b64_tr_b16 v[228:229], v255 offset:53248\n\t"
"v_mfma_f32_16x16x32_bf16 v[124:127], a[216:219], a[196:199], v[124:127]\n\t"
"buffer_load_dwordx4 a[252:255], off, s[80:83], s20 offset:2048 sc1\n\t"
"v_mfma_f32_16x16x32_bf16 v[124:127], v[248:251], a[192:195], v[124:127]\n\t"
"s_mov_b32 s20, 356352\n\t"
"v_mfma_f32_16x16x32_bf16 v[128:131], v[248:251], a[196:199], v[128:131]\n\t"
"buffer_load_dwordx4 v[248:251], off, s[80:83], s20 offset:0 sc1\n\t"
"v_mfma_f32_16x16x32_bf16 v[128:131], a[216:219], a[200:203], v[128:131]\n\t"
"s_waitcnt lgkmcnt(2)\n\t"
"v_mfma_f32_16x16x32_bf16 v[128:131], a[220:223], a[204:207], v[128:131]\n\t"
"s_waitcnt lgkmcnt(1)\n\t"
"v_mfma_f32_4x4x4_16b_bf16 v[224:227], v[224:225], v[220:221], 0\n\t"
"s_waitcnt lgkmcnt(0)\n\t"
"v_mfma_f32_4x4x4_16b_bf16 v[232:235], v[232:233], v[220:221], 0\n\t"
"s_waitcnt vmcnt(10)\n\t"
"v_mfma_f32_4x4x4_16b_bf16 v[236:239], v[236:237], v[220:221], 0\n\t"
"v_mfma_f32_4x4x4_16b_bf16 v[228:231], v[228:229], v[220:221], 0\n\t"
"buffer_load_dwordx4 a[216:219], off, s[80:83], s20 offset:1024 sc1\n\t"
"v_mfma_f32_16x16x32_bf16 v[132:135], a[224:227], a[168:171], v[132:135]\n\t"
"buffer_load_dwordx4 a[220:223], off, s[80:83], s20 offset:2048 sc1\n\t"
"v_mfma_f32_16x16x32_bf16 v[132:135], a[232:235], a[176:179], v[132:135]\n\t"
"v_cvt_pk_bf16_f32 v224, v224, v225\n\t"
"v_cvt_pk_bf16_f32 v225, v226, v227\n\t"
"v_mfma_f32_16x16x32_bf16 v[132:135], a[228:231], a[172:175], v[132:135]\n\t"
"v_cvt_pk_bf16_f32 v226, v232, v233\n\t"
"v_cvt_pk_bf16_f32 v227, v234, v235\n\t"
"v_mfma_f32_16x16x32_bf16 v[136:139], a[224:227], a[172:175], v[136:139]\n\t"
"v_cvt_pk_bf16_f32 v236, v236, v237\n\t"
"v_cvt_pk_bf16_f32 v237, v238, v239\n\t"
"v_mfma_f32_16x16x32_bf16 v[136:139], a[228:231], a[176:179], v[136:139]\n\t"
"s_mov_b32 s20, 368640\n\t"
"v_cvt_pk_bf16_f32 v238, v228, v229\n\t"
"v_cvt_pk_bf16_f32 v239, v230, v231\n\t"
"v_mfma_f32_16x16x32_bf16 v[136:139], a[232:235], a[180:183], v[136:139]\n\t"
"ds_write_b128 v245, v[224:227] offset:47104\n\t"
"v_mfma_f32_16x16x32_bf16 v[140:143], a[232:235], a[184:187], v[140:143]\n\t"
"v_mfma_f32_16x16x32_bf16 v[140:143], a[228:231], a[180:183], v[140:143]\n\t"
"v_mfma_f32_16x16x32_bf16 v[140:143], a[224:227], a[176:179], v[140:143]\n\t"
"ds_write_b128 v245, v[236:239] offset:53248\n\t"
"v_mfma_f32_16x16x32_bf16 v[144:147], a[224:227], a[180:183], v[144:147]\n\t"
"v_mfma_f32_16x16x32_bf16 v[144:147], a[228:231], a[184:187], v[144:147]\n\t"
"buffer_load_dwordx4 v[236:239], off, s[80:83], s20 offset:0 sc1\n\t"
"v_mfma_f32_16x16x32_bf16 v[144:147], a[232:235], a[188:191], v[144:147]\n\t"
"v_mfma_f32_16x16x32_bf16 v[148:151], a[232:235], a[192:195], v[148:151]\n\t"
"v_mfma_f32_16x16x32_bf16 v[148:151], a[228:231], a[188:191], v[148:151]\n\t"
"v_mfma_f32_16x16x32_bf16 v[148:151], a[224:227], a[184:187], v[148:151]\n\t"
"s_add_u32 m0, s24, 1408\n\t"
"s_add_u32 s28, s21, 3072\n\t"
"v_mfma_f32_16x16x32_bf16 v[152:155], a[224:227], a[188:191], v[152:155]\n\t"
"v_mfma_f32_16x16x32_bf16 v[152:155], a[228:231], a[192:195], v[152:155]\n\t"
"v_mfma_f32_16x16x32_bf16 v[152:155], a[232:235], a[196:199], v[152:155]\n\t"
"v_mfma_f32_16x16x32_bf16 v[156:159], a[232:235], a[200:203], v[156:159]\n\t"
"buffer_load_dwordx4 v247, s[12:15], s28 offen offset:128 nt lds\n\t"
"v_mfma_f32_16x16x32_bf16 v[156:159], a[228:231], a[196:199], v[156:159]\n\t"
"s_add_u32 m0, s24, 7552\n\t"
"s_add_u32 s28, s21, 199680\n\t"
"v_mfma_f32_16x16x32_bf16 v[156:159], a[224:227], a[192:195], v[156:159]\n\t"
"buffer_load_dwordx4 v247, s[12:15], s28 offen offset:128 nt lds\n\t"
"v_mfma_f32_16x16x32_bf16 v[160:163], a[224:227], a[196:199], v[160:163]\n\t"
"s_waitcnt vmcnt(12)\n\t"
"s_add_u32 m0, s24, 13696\n\t"
"s_add_u32 s28, s21, 396288\n\t"
"v_mfma_f32_16x16x32_bf16 v[160:163], a[228:231], a[200:203], v[160:163]\n\t"
"buffer_load_dwordx4 v247, s[12:15], s28 offen offset:128 nt lds\n\t"
"v_mfma_f32_16x16x32_bf16 v[160:163], a[232:235], a[204:207], v[160:163]\n\t"
"s_add_u32 m0, s24, 21376\n\t"
"s_add_u32 s28, s21, 592896\n\t"
"v_mfma_f32_16x16x32_bf16 v[164:167], a[236:239], a[168:171], v[164:167]\n\t"
"buffer_load_dwordx4 v247, s[12:15], s28 offen offset:128 nt lds\n\t"
"v_mfma_f32_16x16x32_bf16 v[164:167], a[208:211], a[176:179], v[164:167]\n\t"
"s_add_u32 m0, s24, 27520\n\t"
"s_add_u32 s28, s21, 789504\n\t"
"v_mfma_f32_16x16x32_bf16 v[164:167], v[240:243], a[172:175], v[164:167]\n\t"
"s_waitcnt lgkmcnt(0)\n\t"
"s_barrier\n\t"
"v_mfma_f32_16x16x32_bf16 v[168:171], a[236:239], a[172:175], v[168:171]\n\t"
"ds_read_b128 a[168:171], v222 offset:2560\n\t"
"v_mfma_f32_16x16x32_bf16 v[168:171], v[240:243], a[176:179], v[168:171]\n\t"
"buffer_load_dwordx4 v247, s[12:15], s28 offen offset:128 nt lds\n\t"
"v_mfma_f32_16x16x32_bf16 v[168:171], a[208:211], a[180:183], v[168:171]\n\t"
"s_add_u32 m0, s24, 33664\n\t"
"s_add_u32 s28, s21, 986112\n\t"
"ds_read_b128 a[172:175], v223 offset:8704\n\t"
"v_mfma_f32_16x16x32_bf16 v[172:175], a[208:211], a[184:187], v[172:175]\n\t"
"buffer_load_dwordx4 v247, s[12:15], s28 offen offset:128 nt lds\n\t"
"v_mfma_f32_16x16x32_bf16 v[172:175], v[240:243], a[180:183], v[172:175]\n\t"
"v_mfma_f32_16x16x32_bf16 v[172:175], a[236:239], a[176:179], v[172:175]\n\t"
"ds_read_b128 a[176:179], v223 offset:14848\n\t"
"v_mfma_f32_16x16x32_bf16 v[176:179], a[236:239], a[180:183], v[176:179]\n\t"
"ds_read_b128 a[180:183], v222 offset:22528\n\t"
"v_mfma_f32_16x16x32_bf16 v[176:179], v[240:243], a[184:187], v[176:179]\n\t"
"v_mfma_f32_16x16x32_bf16 v[176:179], a[208:211], a[188:191], v[176:179]\n\t"
"v_mfma_f32_16x16x32_bf16 v[180:183], a[208:211], a[192:195], v[180:183]\n\t"
"v_mfma_f32_16x16x32_bf16 v[180:183], v[240:243], a[188:191], v[180:183]\n\t"
"v_mfma_f32_16x16x32_bf16 v[180:183], a[236:239], a[184:187], v[180:183]\n\t"
"ds_read_b128 a[184:187], v223 offset:28672\n\t"
"v_mfma_f32_16x16x32_bf16 v[184:187], a[236:239], a[188:191], v[184:187]\n\t"
"ds_read_b128 a[188:191], v223 offset:34816\n\t"
"v_mfma_f32_16x16x32_bf16 v[184:187], v[240:243], a[192:195], v[184:187]\n\t"
"v_mfma_f32_16x16x32_bf16 v[184:187], a[208:211], a[196:199], v[184:187]\n\t"
"v_mfma_f32_16x16x32_bf16 v[188:191], a[208:211], a[200:203], v[188:191]\n\t"
"v_mfma_f32_16x16x32_bf16 v[188:191], v[240:243], a[196:199], v[188:191]\n\t"
"v_mfma_f32_16x16x32_bf16 v[188:191], a[236:239], a[192:195], v[188:191]\n\t"
"ds_read_b128 a[192:195], v222 offset:42496\n\t"
"v_mfma_f32_16x16x32_bf16 v[192:195], a[236:239], a[196:199], v[192:195]\n\t"
"s_waitcnt vmcnt(13) lgkmcnt(4)\n\t"
"ds_read_b128 a[196:199], v223 offset:48640\n\t"
"v_mfma_f32_16x16x32_bf16 v[192:195], v[240:243], a[200:203], v[192:195]\n\t"
"ds_read_b128 a[200:203], v223 offset:54784\n\t"
"v_mfma_f32_16x16x32_bf16 v[192:195], a[208:211], a[204:207], v[192:195]\n\t"
"ds_read_b128 a[204:207], v222 offset:62464\n\t"
"Lst22_%=:\n\t"
"v_mfma_f32_16x16x32_bf16 v[0:3], v[96:99], a[168:171], v[0:3]\n\t"
"s_waitcnt lgkmcnt(6)\n\t"
"v_mfma_f32_16x16x32_bf16 v[0:3], a[240:243], a[176:179], v[0:3]\n\t"
"v_mfma_f32_16x16x32_bf16 v[0:3], a[212:215], a[172:175], v[0:3]\n\t"
"v_mfma_f32_16x16x32_bf16 v[4:7], v[96:99], a[172:175], v[4:7]\n\t"
"s_waitcnt lgkmcnt(5)\n\t"
"v_mfma_f32_16x16x32_bf16 v[4:7], a[212:215], a[176:179], v[4:7]\n\t"
"v_mfma_f32_16x16x32_bf16 v[4:7], a[240:243], a[180:183], v[4:7]\n\t"
"v_mfma_f32_16x16x32_bf16 v[8:11], a[240:243], a[184:187], v[8:11]\n\t"
"s_waitcnt lgkmcnt(4)\n\t"
"v_mfma_f32_16x16x32_bf16 v[8:11], a[212:215], a[180:183], v[8:11]\n\t"
"v_mfma_f32_16x16x32_bf16 v[8:11], v[96:99], a[176:179], v[8:11]\n\t"
"v_mfma_f32_16x16x32_bf16 v[12:15], v[96:99], a[180:183], v[12:15]\n\t"
"s_waitcnt lgkmcnt(3)\n\t"
"v_mfma_f32_16x16x32_bf16 v[12:15], a[212:215], a[184:187], v[12:15]\n\t"
"v_mfma_f32_16x16x32_bf16 v[12:15], a[240:243], a[188:191], v[12:15]\n\t"
"v_mfma_f32_16x16x32_bf16 v[16:19], a[240:243], a[192:195], v[16:19]\n\t"
"s_waitcnt lgkmcnt(2)\n\t"
"v_mfma_f32_16x16x32_bf16 v[16:19], a[212:215], a[188:191], v[16:19]\n\t"
"v_mfma_f32_16x16x32_bf16 v[16:19], v[96:99], a[184:187], v[16:19]\n\t"
"buffer_load_dwordx4 a[224:227], off, s[80:83], s20 offset:1024 sc1\n\t"
"v_mfma_f32_16x16x32_bf16 v[20:23], v[96:99], a[188:191], v[20:23]\n\t"
"s_waitcnt lgkmcnt(1)\n\t"
"v_mfma_f32_16x16x32_bf16 v[20:23], a[212:215], a[192:195], v[20:23]\n\t"
"v_mfma_f32_16x16x32_bf16 v[20:23], a[240:243], a[196:199], v[20:23]\n\t"
"v_mfma_f32_16x16x32_bf16 v[24:27], a[240:243], a[200:203], v[24:27]\n\t"
"s_waitcnt lgkmcnt(0)\n\t"
"v_mfma_f32_16x16x32_bf16 v[24:27], a[212:215], a[196:199], v[24:27]\n\t"
"v_mfma_f32_16x16x32_bf16 v[24:27], v[96:99], a[192:195], v[24:27]\n\t"
"v_mfma_f32_16x16x32_bf16 v[28:31], v[96:99], a[196:199], v[28:31]\n\t"
"s_waitcnt vmcnt(11)\n\t"
"v_mfma_f32_16x16x32_bf16 v[28:31], a[212:215], a[200:203], v[28:31]\n\t"
"v_mfma_f32_16x16x32_bf16 v[28:31], a[240:243], a[204:207], v[28:31]\n\t"
"v_mfma_f32_16x16x32_bf16 v[32:35], a[244:247], a[168:171], v[32:35]\n\t"
"v_mfma_f32_16x16x32_bf16 v[32:35], a[252:255], a[176:179], v[32:35]\n\t"
"v_mfma_f32_16x16x32_bf16 v[32:35], a[248:251], a[172:175], v[32:35]\n\t"
"v_mfma_f32_16x16x32_bf16 v[36:39], a[244:247], a[172:175], v[36:39]\n\t"
"v_mfma_f32_16x16x32_bf16 v[36:39], a[248:251], a[176:179], v[36:39]\n\t"
"v_mfma_f32_16x16x32_bf16 v[36:39], a[252:255], a[180:183], v[36:39]\n\t"
"v_mfma_f32_16x16x32_bf16 v[40:43], a[252:255], a[184:187], v[40:43]\n\t"
"v_mfma_f32_16x16x32_bf16 v[40:43], a[248:251], a[180:183], v[40:43]\n\t"
"v_mfma_f32_16x16x32_bf16 v[40:43], a[244:247], a[176:179], v[40:43]\n\t"
"buffer_load_dwordx4 v[96:99], off, s[80:83], s20 offset:2048 sc1\n\t"
"v_mfma_f32_16x16x32_bf16 v[44:47], a[244:247], a[180:183], v[44:47]\n\t"
"s_mov_b32 s20, 380928\n\t"
"v_mfma_f32_16x16x32_bf16 v[44:47], a[248:251], a[184:187], v[44:47]\n\t"
"v_mfma_f32_16x16x32_bf16 v[44:47], a[252:255], a[188:191], v[44:47]\n\t"
"v_mfma_f32_16x16x32_bf16 v[48:51], a[252:255], a[192:195], v[48:51]\n\t"
"buffer_load_dwordx4 a[212:215], off, s[80:83], s20 offset:0 sc1\n\t"
"v_mfma_f32_16x16x32_bf16 v[48:51], a[248:251], a[188:191], v[48:51]\n\t"
"v_mfma_f32_16x16x32_bf16 v[48:51], a[244:247], a[184:187], v[48:51]\n\t"
"v_mfma_f32_16x16x32_bf16 v[52:55], a[244:247], a[188:191], v[52:55]\n\t"
"v_mfma_f32_16x16x32_bf16 v[52:55], a[248:251], a[192:195], v[52:55]\n\t"
"buffer_load_dwordx4 a[228:231], off, s[80:83], s20 offset:1024 sc1\n\t"
"v_mfma_f32_16x16x32_bf16 v[52:55], a[252:255], a[196:199], v[52:55]\n\t"
"v_mfma_f32_16x16x32_bf16 v[56:59], a[252:255], a[200:203], v[56:59]\n\t"
"v_mfma_f32_16x16x32_bf16 v[56:59], a[248:251], a[196:199], v[56:59]\n\t"
"v_mfma_f32_16x16x32_bf16 v[56:59], a[244:247], a[192:195], v[56:59]\n\t"
"buffer_load_dwordx4 a[232:235], off, s[80:83], s20 offset:2048 sc1\n\t"
"v_mfma_f32_16x16x32_bf16 v[60:63], a[244:247], a[196:199], v[60:63]\n\t"
"s_waitcnt vmcnt(12)\n\t"
"s_mov_b32 s20, 393216\n\t"
"v_mfma_f32_16x16x32_bf16 v[60:63], a[248:251], a[200:203], v[60:63]\n\t"
"v_mfma_f32_16x16x32_bf16 v[60:63], a[252:255], a[204:207], v[60:63]\n\t"
"v_mfma_f32_16x16x32_bf16 v[64:67], v[248:251], a[168:171], v[64:67]\n\t"
"buffer_load_dwordx4 a[236:239], off, s[80:83], s20 offset:0 sc1\n\t"
"v_mfma_f32_16x16x32_bf16 v[64:67], a[220:223], a[176:179], v[64:67]\n\t"
"ds_read_b128 a[168:171], v222 offset:2816\n\t"
"v_mfma_f32_16x16x32_bf16 v[64:67], a[216:219], a[172:175], v[64:67]\n\t"
"v_mfma_f32_16x16x32_bf16 v[68:71], v[248:251], a[172:175], v[68:71]\n\t"
"ds_read_b128 a[172:175], v223 offset:8960\n\t"
"v_mfma_f32_16x16x32_bf16 v[68:71], a[216:219], a[176:179], v[68:71]\n\t"
"buffer_load_dwordx4 a[240:243], off, s[80:83], s20 offset:1024 sc1\n\t"
"v_mfma_f32_16x16x32_bf16 v[68:71], a[220:223], a[180:183], v[68:71]\n\t"
"v_mfma_f32_16x16x32_bf16 v[72:75], a[220:223], a[184:187], v[72:75]\n\t"
"v_mfma_f32_16x16x32_bf16 v[72:75], a[216:219], a[180:183], v[72:75]\n\t"
"v_mfma_f32_16x16x32_bf16 v[72:75], v[248:251], a[176:179], v[72:75]\n\t"
"ds_read_b128 a[176:179], v223 offset:15104\n\t"
"v_mfma_f32_16x16x32_bf16 v[76:79], v[248:251], a[180:183], v[76:79]\n\t"
"buffer_load_dwordx4 a[244:247], off, s[80:83], s20 offset:2048 sc1\n\t"
"v_mfma_f32_16x16x32_bf16 v[76:79], a[216:219], a[184:187], v[76:79]\n\t"
"s_mov_b32 s20, 405504\n\t"
"ds_read_b128 a[180:183], v222 offset:22784\n\t"
"v_mfma_f32_16x16x32_bf16 v[76:79], a[220:223], a[188:191], v[76:79]\n\t"
"v_mfma_f32_16x16x32_bf16 v[80:83], a[220:223], a[192:195], v[80:83]\n\t"
"buffer_load_dwordx4 a[248:251], off, s[80:83], s20 offset:0 sc1\n\t"
"v_mfma_f32_16x16x32_bf16 v[80:83], a[216:219], a[188:191], v[80:83]\n\t"
"v_mfma_f32_16x16x32_bf16 v[80:83], v[248:251], a[184:187], v[80:83]\n\t"
"ds_read_b128 a[184:187], v223 offset:28928\n\t"
"v_mfma_f32_16x16x32_bf16 v[84:87], v[248:251], a[188:191], v[84:87]\n\t"
"ds_read_b128 a[188:191], v223 offset:35072\n\t"
"v_mfma_f32_16x16x32_bf16 v[84:87], a[216:219], a[192:195], v[84:87]\n\t"
"buffer_load_dwordx4 a[252:255], off, s[80:83], s20 offset:1024 sc1\n\t"
"v_mfma_f32_16x16x32_bf16 v[84:87], a[220:223], a[196:199], v[84:87]\n\t"
"v_mfma_f32_16x16x32_bf16 v[88:91], a[220:223], a[200:203], v[88:91]\n\t"
"v_mfma_f32_16x16x32_bf16 v[88:91], a[216:219], a[196:199], v[88:91]\n\t"
"v_mfma_f32_16x16x32_bf16 v[88:91], v[248:251], a[192:195], v[88:91]\n\t"
"ds_read_b128 a[192:195], v222 offset:42752\n\t"
"v_mfma_f32_16x16x32_bf16 v[92:95], v[248:251], a[196:199], v[92:95]\n\t"
"buffer_load_dwordx4 v[248:251], off, s[80:83], s20 offset:2048 sc1\n\t"
"v_mfma_f32_16x16x32_bf16 v[92:95], a[216:219], a[200:203], v[92:95]\n\t"
"s_waitcnt vmcnt(9) lgkmcnt(4)\n\t"
"ds_read_b128 a[196:199], v223 offset:48896\n\t"
"s_mov_b32 s20, 417792\n\t"
"v_mfma_f32_16x16x32_bf16 v[92:95], a[220:223], a[204:207], v[92:95]\n\t"
"global_load_dwordx4 v[240:243], v252, s[48:49] offset:2048\n\t"
"v_mfma_f32_16x16x32_bf16 v[100:103], v[236:239], a[168:171], v[100:103]\n\t"
"buffer_load_dwordx4 a[216:219], off, s[80:83], s20 offset:0 sc1\n\t"
"v_mfma_f32_16x16x32_bf16 v[100:103], v[96:99], a[176:179], v[100:103]\n\t"
"s_waitcnt lgkmcnt(4)\n\t"
"ds_read_b128 a[200:203], v223 offset:55040\n\t"
"v_mfma_f32_16x16x32_bf16 v[100:103], a[224:227], a[172:175], v[100:103]\n\t"
"ds_read_b128 a[204:207], v222 offset:62720\n\t"
"v_mfma_f32_16x16x32_bf16 v[104:107], v[236:239], a[172:175], v[104:107]\n\t"
"buffer_load_dwordx4 a[220:223], off, s[80:83], s20 offset:1024 sc1\n\t"
"v_mfma_f32_16x16x32_bf16 v[104:107], a[224:227], a[176:179], v[104:107]\n\t"
"global_load_dwordx4 a[208:211], v252, s[46:47] offset:1024\n\t"
"v_mfma_f32_16x16x32_bf16 v[104:107], v[96:99], a[180:183], v[104:107]\n\t"
"s_waitcnt lgkmcnt(5)\n\t"
"v_mfma_f32_16x16x32_bf16 v[108:111], v[96:99], a[184:187], v[108:111]\n\t"
"s_waitcnt lgkmcnt(4)\n\t"
"v_mfma_f32_16x16x32_bf16 v[108:111], a[224:227], a[180:183], v[108:111]\n\t"
"v_mfma_f32_16x16x32_bf16 v[108:111], v[236:239], a[176:179], v[108:111]\n\t"
"v_mfma_f32_16x16x32_bf16 v[112:115], v[236:239], a[180:183], v[112:115]\n\t"
"s_waitcnt lgkmcnt(3)\n\t"
"v_mfma_f32_16x16x32_bf16 v[112:115], a[224:227], a[184:187], v[112:115]\n\t"
"v_mfma_f32_16x16x32_bf16 v[112:115], v[96:99], a[188:191], v[112:115]\n\t"
"v_mfma_f32_16x16x32_bf16 v[116:119], v[96:99], a[192:195], v[116:119]\n\t"
"s_waitcnt lgkmcnt(2)\n\t"
"v_mfma_f32_16x16x32_bf16 v[116:119], a[224:227], a[188:191], v[116:119]\n\t"
"v_mfma_f32_16x16x32_bf16 v[116:119], v[236:239], a[184:187], v[116:119]\n\t"
"v_mfma_f32_16x16x32_bf16 v[120:123], v[236:239], a[188:191], v[120:123]\n\t"
"s_waitcnt lgkmcnt(1)\n\t"
"v_mfma_f32_16x16x32_bf16 v[120:123], a[224:227], a[192:195], v[120:123]\n\t"
"v_mfma_f32_16x16x32_bf16 v[120:123], v[96:99], a[196:199], v[120:123]\n\t"
"v_mfma_f32_16x16x32_bf16 v[124:127], v[96:99], a[200:203], v[124:127]\n\t"
"s_waitcnt lgkmcnt(0)\n\t"
"v_mfma_f32_16x16x32_bf16 v[124:127], a[224:227], a[196:199], v[124:127]\n\t"
"v_mfma_f32_16x16x32_bf16 v[124:127], v[236:239], a[192:195], v[124:127]\n\t"
"v_mfma_f32_16x16x32_bf16 v[128:131], v[236:239], a[196:199], v[128:131]\n\t"
"s_waitcnt vmcnt(10)\n\t"
"v_mfma_f32_16x16x32_bf16 v[128:131], a[224:227], a[200:203], v[128:131]\n\t"
"buffer_load_dwordx4 a[224:227], off, s[80:83], s20 offset:2048 sc1\n\t"
"v_mfma_f32_16x16x32_bf16 v[128:131], v[96:99], a[204:207], v[128:131]\n\t"
"s_mov_b32 s20, 430080\n\t"
"v_mfma_f32_16x16x32_bf16 v[132:135], a[212:215], a[168:171], v[132:135]\n\t"
"v_mfma_f32_16x16x32_bf16 v[132:135], a[232:235], a[176:179], v[132:135]\n\t"
"buffer_load_dwordx4 v[96:99], off, s[80:83], s20 offset:0 sc1\n\t"
"v_mfma_f32_16x16x32_bf16 v[132:135], a[228:231], a[172:175], v[132:135]\n\t"
"v_mfma_f32_16x16x32_bf16 v[136:139], a[212:215], a[172:175], v[136:139]\n\t"
"v_mfma_f32_16x16x32_bf16 v[136:139], a[228:231], a[176:179], v[136:139]\n\t"
"v_mfma_f32_16x16x32_bf16 v[136:139], a[232:235], a[180:183], v[136:139]\n\t"
"s_barrier\n\t"
"v_mfma_f32_16x16x32_bf16 v[140:143], a[232:235], a[184:187], v[140:143]\n\t"
"s_add_u32 m0, s24, 39808\n\t"
"s_add_u32 s28, s21, 1179648\n\t"
"v_mfma_f32_16x16x32_bf16 v[140:143], a[228:231], a[180:183], v[140:143]\n\t"
"v_mfma_f32_16x16x32_bf16 v[140:143], a[212:215], a[176:179], v[140:143]\n\t"
"s_waitcnt vmcnt(5)\n\t"
"ds_read_b64_tr_b16 v[232:233], v240 offset:0\n\t"
"v_mfma_f32_16x16x32_bf16 v[144:147], a[212:215], a[180:183], v[144:147]\n\t"
"buffer_load_dwordx4 v247, s[12:15], s28 offen offset:128 nt lds\n\t"
"v_mfma_f32_16x16x32_bf16 v[144:147], a[228:231], a[184:187], v[144:147]\n\t"
"s_add_u32 m0, s24, 45952\n\t"
"s_add_u32 s28, s21, 1376256\n\t"
"ds_read_b64_tr_b16 v[234:235], v240 offset:1536\n\t"
"v_mfma_f32_16x16x32_bf16 v[144:147], a[232:235], a[188:191], v[144:147]\n\t"
"buffer_load_dwordx4 v247, s[12:15], s28 offen offset:128 nt lds\n\t"
"v_mfma_f32_16x16x32_bf16 v[148:151], a[232:235], a[192:195], v[148:151]\n\t"
"s_add_u32 m0, s24, 52096\n\t"
"s_add_u32 s28, s21, 1572864\n\t"
"ds_read_b64_tr_b16 v[228:229], v241 offset:0\n\t"
"v_mfma_f32_16x16x32_bf16 v[148:151], a[228:231], a[188:191], v[148:151]\n\t"
"buffer_load_dwordx4 v247, s[12:15], s28 offen offset:128 nt lds\n\t"
"v_mfma_f32_16x16x32_bf16 v[148:151], a[212:215], a[184:187], v[148:151]\n\t"
"s_add_u32 m0, s24, 41344\n\t"
"s_add_u32 s28, s21, 1182720\n\t"
"ds_read_b64_tr_b16 v[230:231], v241 offset:1536\n\t"
"v_mfma_f32_16x16x32_bf16 v[152:155], a[212:215], a[188:191], v[152:155]\n\t"
"buffer_load_dwordx4 v247, s[12:15], s28 offen offset:128 nt lds\n\t"
"v_mfma_f32_16x16x32_bf16 v[152:155], a[228:231], a[192:195], v[152:155]\n\t"
"s_add_u32 m0, s24, 47488\n\t"
"s_add_u32 s28, s21, 1379328\n\t"
"ds_read_b64_tr_b16 v[224:225], v240 offset:19968\n\t"
"v_mfma_f32_16x16x32_bf16 v[152:155], a[232:235], a[196:199], v[152:155]\n\t"
"ds_read_b64_tr_b16 v[226:227], v240 offset:21504\n\t"
"v_mfma_f32_16x16x32_bf16 v[156:159], a[232:235], a[200:203], v[156:159]\n\t"
"buffer_load_dwordx4 v247, s[12:15], s28 offen offset:128 nt lds\n\t"
"v_mfma_f32_16x16x32_bf16 v[156:159], a[228:231], a[196:199], v[156:159]\n\t"
"s_add_u32 m0, s24, 53632\n\t"
"s_add_u32 s28, s21, 1575936\n\t"
"ds_read_b64_tr_b16 v[236:237], v241 offset:19968\n\t"
"v_mfma_f32_16x16x32_bf16 v[156:159], a[212:215], a[192:195], v[156:159]\n\t"
"ds_read_b64_tr_b16 v[238:239], v241 offset:21504\n\t"
"v_mfma_f32_16x16x32_bf16 v[160:163], a[212:215], a[196:199], v[160:163]\n\t"
"buffer_load_dwordx4 v247, s[12:15], s28 offen offset:128 nt lds\n\t"
"v_mfma_f32_16x16x32_bf16 v[160:163], a[228:231], a[200:203], v[160:163]\n\t"
"s_waitcnt vmcnt(8) lgkmcnt(4)\n\t"
"v_mfma_f32_16x16x32_bf16 v[160:163], a[232:235], a[204:207], v[160:163]\n\t"
"s_waitcnt lgkmcnt(2)\n\t"
"v_mfma_f32_16x16x32_bf16 v[232:235], v[232:235], a[208:211], 0\n\t"
"v_mfma_f32_16x16x32_bf16 v[228:231], v[228:231], a[208:211], 0\n\t"
"s_waitcnt lgkmcnt(0)\n\t"
"v_mfma_f32_16x16x32_bf16 v[224:227], v[224:227], a[208:211], 0\n\t"
"v_mfma_f32_16x16x32_bf16 v[236:239], v[236:239], a[208:211], 0\n\t"
"v_mfma_f32_16x16x32_bf16 v[164:167], a[236:239], a[168:171], v[164:167]\n\t"
"ds_read_b128 a[168:171], v222 offset:1024\n\t"
"v_mfma_f32_16x16x32_bf16 v[164:167], a[244:247], a[176:179], v[164:167]\n\t"
"v_cvt_pk_bf16_f32 v232, v232, v233\n\t"
"v_cvt_pk_bf16_f32 v233, v234, v235\n\t"
"v_mfma_f32_16x16x32_bf16 v[164:167], a[240:243], a[172:175], v[164:167]\n\t"
"v_cvt_pk_bf16_f32 v234, v228, v229\n\t"
"v_cvt_pk_bf16_f32 v235, v230, v231\n\t"
"v_mfma_f32_16x16x32_bf16 v[168:171], a[236:239], a[172:175], v[168:171]\n\t"
"ds_read_b128 a[172:175], v223 offset:7168\n\t"
"v_cvt_pk_bf16_f32 v224, v224, v225\n\t"
"v_mfma_f32_16x16x32_bf16 v[168:171], a[240:243], a[176:179], v[168:171]\n\t"
"v_cvt_pk_bf16_f32 v225, v226, v227\n\t"
"v_cvt_pk_bf16_f32 v226, v236, v237\n\t"
"v_mfma_f32_16x16x32_bf16 v[168:171], a[244:247], a[180:183], v[168:171]\n\t"
"ds_write_b128 v242, v[232:235] offset:24576\n\t"
"v_mfma_f32_16x16x32_bf16 v[172:175], a[244:247], a[184:187], v[172:175]\n\t"
"v_cvt_pk_bf16_f32 v227, v238, v239\n\t"
"v_add_u32 v240, 59904, v240\n\t"
"v_mfma_f32_16x16x32_bf16 v[172:175], a[240:243], a[180:183], v[172:175]\n\t"
"Lpg17_%=:\n\t"
"s_cbranch_vccnz Lvr2_%=\n\t"
"v_add_u32 v241, 59904, v241\n\t"
"v_mfma_f32_16x16x32_bf16 v[172:175], a[236:239], a[176:179], v[172:175]\n\t"
"ds_write_b128 v242, v[224:227] offset:26112\n\t"
"v_mfma_f32_16x16x32_bf16 v[176:179], a[236:239], a[180:183], v[176:179]\n\t"
"v_mfma_f32_16x16x32_bf16 v[176:179], a[240:243], a[184:187], v[176:179]\n\t"
"ds_read_b128 a[176:179], v223 offset:13312\n\t"
"v_mfma_f32_16x16x32_bf16 v[176:179], a[244:247], a[188:191], v[176:179]\n\t"
"ds_read_b64_tr_b16 v[228:229], v240 offset:0\n\t"
"v_mfma_f32_16x16x32_bf16 v[180:183], a[244:247], a[192:195], v[180:183]\n\t"
"ds_read_b64_tr_b16 v[230:231], v240 offset:1536\n\t"
"v_mfma_f32_16x16x32_bf16 v[180:183], a[240:243], a[188:191], v[180:183]\n\t"
"ds_read_b64_tr_b16 v[236:237], v241 offset:0\n\t"
"v_mfma_f32_16x16x32_bf16 v[180:183], a[236:239], a[184:187], v[180:183]\n\t"
"ds_read_b64_tr_b16 v[238:239], v241 offset:1536\n\t"
"v_mfma_f32_16x16x32_bf16 v[184:187], a[236:239], a[188:191], v[184:187]\n\t"
"ds_read_b128 a[180:183], v222 offset:20992\n\t"
"v_mfma_f32_16x16x32_bf16 v[184:187], a[240:243], a[192:195], v[184:187]\n\t"
"ds_read_b64_tr_b16 v[232:233], v240 offset:19968\n\t"
"v_mfma_f32_16x16x32_bf16 v[184:187], a[244:247], a[196:199], v[184:187]\n\t"
"ds_read_b64_tr_b16 v[234:235], v240 offset:21504\n\t"
"v_mfma_f32_16x16x32_bf16 v[188:191], a[244:247], a[200:203], v[188:191]\n\t"
"ds_read_b64_tr_b16 v[224:225], v241 offset:19968\n\t"
"v_mfma_f32_16x16x32_bf16 v[188:191], a[240:243], a[196:199], v[188:191]\n\t"
"ds_read_b64_tr_b16 v[226:227], v241 offset:21504\n\t"
"v_mfma_f32_16x16x32_bf16 v[188:191], a[236:239], a[192:195], v[188:191]\n\t"
"ds_read_b128 a[184:187], v223 offset:27136\n\t"
"v_mfma_f32_16x16x32_bf16 v[192:195], a[236:239], a[196:199], v[192:195]\n\t"
"buffer_load_dwordx4 a[212:215], off, s[80:83], s20 offset:1024 sc1\n\t"
"v_mfma_f32_16x16x32_bf16 v[192:195], a[240:243], a[200:203], v[192:195]\n\t"
"s_waitcnt lgkmcnt(6)\n\t"
"ds_read_b128 a[188:191], v223 offset:33280\n\t"
"v_mfma_f32_16x16x32_bf16 v[192:195], a[244:247], a[204:207], v[192:195]\n\t"
"s_waitcnt lgkmcnt(4)\n\t"
"ds_read_b128 a[192:195], v222 offset:40960\n\t"
"v_mfma_f32_16x16x32_bf16 v[228:231], v[228:231], a[208:211], 0\n\t"
"s_waitcnt lgkmcnt(3)\n\t"
"ds_read_b128 a[196:199], v223 offset:47104\n\t"
"v_mfma_f32_16x16x32_bf16 v[236:239], v[236:239], a[208:211], 0\n\t"
"ds_read_b128 a[200:203], v223 offset:53248\n\t"
"v_mfma_f32_16x16x32_bf16 v[232:235], v[232:235], a[208:211], 0\n\t"
"ds_read_b128 a[204:207], v222 offset:60928\n\t"
"v_mfma_f32_16x16x32_bf16 v[224:227], v[224:227], a[208:211], 0\n\t"
"Lst23_%=:\n\t"
"v_mfma_f32_16x16x32_bf16 v[196:199], a[248:251], a[168:171], v[196:199]\n\t"
"s_mov_b64 vcc, -1\n\t"
"s_bitcmp1_b32 s73, 1\n\t"
"s_cbranch_scc1 Lvh3_%=\n\t"
"s_bitcmp1_b32 s73, 0\n\t"
"s_cbranch_scc1 Lpg22_%=\n\t"
"s_branch Lpg21_%=\n\t"
"Lvh3_%=:\n\t"
"s_bitcmp1_b32 s73, 0\n\t"
"s_cbranch_scc1 Lpg24_%=\n\t"
"s_branch Lpg23_%=\n\t"
"Lvr3_%=:\n\t"
"s_mov_b64 vcc, 0\n\t"
"v_mfma_f32_16x16x32_bf16 v[196:199], v[248:251], a[176:179], v[196:199]\n\t"
"v_cvt_pk_bf16_f32 v228, v228, v229\n\t"
"v_cvt_pk_bf16_f32 v229, v230, v231\n\t"
"v_mfma_f32_16x16x32_bf16 v[196:199], a[252:255], a[172:175], v[196:199]\n\t"
"v_cvt_pk_bf16_f32 v230, v236, v237\n\t"
"v_cvt_pk_bf16_f32 v231, v238, v239\n\t"
"v_mfma_f32_16x16x32_bf16 v[204:207], a[248:251], a[172:175], v[204:207]\n\t"
"s_waitcnt lgkmcnt(5)\n\t"
"v_cvt_pk_bf16_f32 v232, v232, v233\n\t"
"v_cvt_pk_bf16_f32 v233, v234, v235\n\t"
"v_mfma_f32_16x16x32_bf16 v[204:207], a[252:255], a[176:179], v[204:207]\n\t"
"v_cvt_pk_bf16_f32 v234, v224, v225\n\t"
"v_cvt_pk_bf16_f32 v235, v226, v227\n\t"
"v_mfma_f32_16x16x32_bf16 v[204:207], v[248:251], a[180:183], v[204:207]\n\t"
"ds_write_b128 v242, v[228:231] offset:29184\n\t"
"v_mfma_f32_16x16x32_bf16 v[212:215], v[248:251], a[184:187], v[212:215]\n\t"
"s_waitcnt lgkmcnt(5)\n\t"
"v_mfma_f32_16x16x32_bf16 v[212:215], a[252:255], a[180:183], v[212:215]\n\t"
"v_mfma_f32_16x16x32_bf16 v[212:215], a[248:251], a[176:179], v[212:215]\n\t"
"ds_write_b128 v242, v[232:235] offset:30720\n\t"
"v_mfma_f32_16x16x32_bf16 a[0:3], a[248:251], a[180:183], a[0:3]\n\t"
"s_waitcnt lgkmcnt(5)\n\t"
"v_mfma_f32_16x16x32_bf16 a[0:3], a[252:255], a[184:187], a[0:3]\n\t"
"v_mfma_f32_16x16x32_bf16 a[0:3], v[248:251], a[188:191], a[0:3]\n\t"
"s_barrier\n\t"
"v_mfma_f32_16x16x32_bf16 a[8:11], v[248:251], a[192:195], a[8:11]\n\t"
"s_waitcnt lgkmcnt(4)\n\t"
"ds_read_b64_tr_b16 v[236:237], v246 offset:0\n\t"
"v_mfma_f32_16x16x32_bf16 a[8:11], a[252:255], a[188:191], a[8:11]\n\t"
"ds_read_b64_tr_b16 v[224:225], v255 offset:0\n\t"
"v_mfma_f32_16x16x32_bf16 a[8:11], a[248:251], a[184:187], a[8:11]\n\t"
"ds_read_b64_tr_b16 v[228:229], v246 offset:6144\n\t"
"v_mfma_f32_16x16x32_bf16 a[16:19], a[248:251], a[188:191], a[16:19]\n\t"
"s_waitcnt lgkmcnt(6)\n\t"
"ds_read_b64_tr_b16 v[232:233], v255 offset:6144\n\t"
"v_mfma_f32_16x16x32_bf16 a[16:19], a[252:255], a[192:195], a[16:19]\n\t"
"buffer_load_dwordx4 a[228:231], off, s[80:83], s20 offset:2048 sc1\n\t"
"v_mfma_f32_16x16x32_bf16 a[16:19], v[248:251], a[196:199], a[16:19]\n\t"
"s_mov_b32 s20, 442368\n\t"
"v_mfma_f32_16x16x32_bf16 a[24:27], v[248:251], a[200:203], a[24:27]\n\t"
"s_waitcnt lgkmcnt(6)\n\t"
"v_mfma_f32_16x16x32_bf16 a[24:27], a[252:255], a[196:199], a[24:27]\n\t"
"v_mfma_f32_16x16x32_bf16 a[24:27], a[248:251], a[192:195], a[24:27]\n\t"
"buffer_load_dwordx4 a[232:235], off, s[80:83], s20 offset:0 sc1\n\t"
"v_mfma_f32_16x16x32_bf16 a[32:35], a[248:251], a[196:199], a[32:35]\n\t"
"s_waitcnt lgkmcnt(3)\n\t"
"v_mfma_f32_16x16x32_bf16 a[32:35], a[252:255], a[200:203], a[32:35]\n\t"
"s_waitcnt lgkmcnt(2)\n\t"
"v_mfma_f32_16x16x32_bf16 a[32:35], v[248:251], a[204:207], a[32:35]\n\t"
"s_waitcnt lgkmcnt(1)\n\t"
"v_mfma_f32_4x4x4_16b_bf16 v[236:239], v[236:237], v[220:221], 0\n\t"
"s_waitcnt lgkmcnt(0)\n\t"
"v_mfma_f32_4x4x4_16b_bf16 v[224:227], v[224:225], v[220:221], 0\n\t"
"s_waitcnt vmcnt(10)\n\t"
"v_mfma_f32_4x4x4_16b_bf16 v[228:231], v[228:229], v[220:221], 0\n\t"
"v_mfma_f32_4x4x4_16b_bf16 v[232:235], v[232:233], v[220:221], 0\n\t"
"buffer_load_dwordx4 v[248:251], off, s[80:83], s20 offset:1024 sc1\n\t"
"v_mfma_f32_16x16x32_bf16 a[40:43], a[216:219], a[168:171], a[40:43]\n\t"
"buffer_load_dwordx4 a[236:239], off, s[80:83], s20 offset:2048 sc1\n\t"
"v_mfma_f32_16x16x32_bf16 a[40:43], a[224:227], a[176:179], a[40:43]\n\t"
"s_mov_b32 s20, 454656\n\t"
"v_cvt_pk_bf16_f32 v236, v236, v237\n\t"
"v_cvt_pk_bf16_f32 v237, v238, v239\n\t"
"v_mfma_f32_16x16x32_bf16 a[40:43], a[220:223], a[172:175], a[40:43]\n\t"
"v_cvt_pk_bf16_f32 v238, v224, v225\n\t"
"v_cvt_pk_bf16_f32 v239, v226, v227\n\t"
"v_mfma_f32_16x16x32_bf16 a[48:51], a[216:219], a[172:175], a[48:51]\n\t"
"ds_read_b64_tr_b16 v[224:225], v246 offset:12288\n\t"
"v_cvt_pk_bf16_f32 v228, v228, v229\n\t"
"v_mfma_f32_16x16x32_bf16 a[48:51], a[220:223], a[176:179], a[48:51]\n\t"
"v_cvt_pk_bf16_f32 v229, v230, v231\n\t"
"v_cvt_pk_bf16_f32 v230, v232, v233\n\t"
"v_mfma_f32_16x16x32_bf16 a[48:51], a[224:227], a[180:183], a[48:51]\n\t"
"ds_write_b128 v245, v[236:239] offset:0\n\t"
"v_cvt_pk_bf16_f32 v231, v234, v235\n\t"
"v_mfma_f32_16x16x32_bf16 a[56:59], a[224:227], a[184:187], a[56:59]\n\t"
"v_mfma_f32_16x16x32_bf16 a[56:59], a[220:223], a[180:183], a[56:59]\n\t"
"buffer_load_dwordx4 a[240:243], off, s[80:83], s20 offset:0 sc1\n\t"
"v_mfma_f32_16x16x32_bf16 a[56:59], a[216:219], a[176:179], a[56:59]\n\t"
"ds_read_b64_tr_b16 v[232:233], v255 offset:12288\n\t"
"v_mfma_f32_16x16x32_bf16 a[64:67], a[216:219], a[180:183], a[64:67]\n\t"
"ds_read_b64_tr_b16 v[236:237], v246 offset:19968\n\t"
"v_mfma_f32_16x16x32_bf16 a[64:67], a[220:223], a[184:187], a[64:67]\n\t"
"buffer_load_dwordx4 a[244:247], off, s[80:83], s20 offset:1024 sc1\n\t"
"v_mfma_f32_16x16x32_bf16 a[64:67], a[224:227], a[188:191], a[64:67]\n\t"
"ds_write_b128 v245, v[228:231] offset:6144\n\t"
"v_mfma_f32_16x16x32_bf16 a[72:75], a[216:219], a[184:187], a[72:75]\n\t"
"buffer_load_dwordx4 a[248:251], off, s[80:83], s20 offset:2048 sc1\n\t"
"v_mfma_f32_16x16x32_bf16 a[72:75], a[224:227], a[192:195], a[72:75]\n\t"
"s_waitcnt lgkmcnt(2)\n\t"
"ds_read_b64_tr_b16 v[228:229], v255 offset:19968\n\t"
"v_mfma_f32_16x16x32_bf16 a[72:75], a[220:223], a[188:191], a[72:75]\n\t"
"s_waitcnt lgkmcnt(2)\n\t"
"s_mov_b32 s20, 466944\n\t"
"v_mfma_f32_4x4x4_16b_bf16 v[224:227], v[224:225], v[220:221], 0\n\t"
"v_mfma_f32_4x4x4_16b_bf16 v[232:235], v[232:233], v[220:221], 0\n\t"
"v_mfma_f32_4x4x4_16b_bf16 v[236:239], v[236:237], v[220:221], 0\n\t"
"s_waitcnt lgkmcnt(0)\n\t"
"v_mfma_f32_4x4x4_16b_bf16 v[228:231], v[228:229], v[220:221], 0\n\t"
"v_mfma_f32_16x16x32_bf16 a[80:83], a[216:219], a[188:191], a[80:83]\n\t"
"buffer_load_dwordx4 a[252:255], off, s[80:83], s20 offset:0 sc1\n\t"
"v_mfma_f32_16x16x32_bf16 a[80:83], a[224:227], a[196:199], a[80:83]\n\t"
"v_cvt_pk_bf16_f32 v224, v224, v225\n\t"
"v_cvt_pk_bf16_f32 v225, v226, v227\n\t"
"v_mfma_f32_16x16x32_bf16 a[80:83], a[220:223], a[192:195], a[80:83]\n\t"
"v_cvt_pk_bf16_f32 v226, v232, v233\n\t"
"v_cvt_pk_bf16_f32 v227, v234, v235\n\t"
"v_mfma_f32_16x16x32_bf16 a[88:91], a[216:219], a[192:195], a[88:91]\n\t"
"ds_read_b64_tr_b16 v[232:233], v246 offset:26112\n\t"
"v_cvt_pk_bf16_f32 v236, v236, v237\n\t"
"v_mfma_f32_16x16x32_bf16 a[88:91], a[220:223], a[196:199], a[88:91]\n\t"
"v_cvt_pk_bf16_f32 v237, v238, v239\n\t"
"v_cvt_pk_bf16_f32 v238, v228, v229\n\t"
"v_mfma_f32_16x16x32_bf16 a[88:91], a[224:227], a[200:203], a[88:91]\n\t"
"ds_write_b128 v245, v[224:227] offset:12288\n\t"
"v_cvt_pk_bf16_f32 v239, v230, v231\n\t"
"v_mfma_f32_16x16x32_bf16 a[96:99], a[220:223], a[200:203], a[96:99]\n\t"
"s_waitcnt vmcnt(7)\n\t"
"v_mfma_f32_16x16x32_bf16 a[96:99], a[216:219], a[196:199], a[96:99]\n\t"
"ds_read_b64_tr_b16 v[228:229], v255 offset:26112\n\t"
"v_mfma_f32_16x16x32_bf16 a[96:99], a[224:227], a[204:207], a[96:99]\n\t"
"ds_write_b128 v245, v[236:239] offset:19968\n\t"
"v_mfma_f32_16x16x32_bf16 a[104:107], v[96:99], a[168:171], a[104:107]\n\t"
"buffer_load_dwordx4 a[220:223], off, s[80:83], s20 offset:1024 sc1\n\t"
"v_mfma_f32_16x16x32_bf16 a[104:107], a[228:231], a[176:179], a[104:107]\n\t"
"ds_read_b64_tr_b16 v[224:225], v246 offset:32256\n\t"
"v_mfma_f32_16x16x32_bf16 a[104:107], a[212:215], a[172:175], a[104:107]\n\t"
"ds_read_b64_tr_b16 v[236:237], v255 offset:32256\n\t"
"v_mfma_f32_16x16x32_bf16 a[112:115], v[96:99], a[172:175], a[112:115]\n\t"
"buffer_load_dwordx4 a[216:219], off, s[80:83], s20 offset:2048 sc1\n\t"
"v_mfma_f32_16x16x32_bf16 a[112:115], a[212:215], a[176:179], a[112:115]\n\t"
"s_waitcnt lgkmcnt(3)\n\t"
"ds_read_b128 a[168:171], v222 offset:1280\n\t"
"s_mov_b32 s20, 700416\n\t"
"v_mfma_f32_16x16x32_bf16 a[112:115], a[228:231], a[180:183], a[112:115]\n\t"
"buffer_load_dwordx4 a[224:227], off, s[80:83], s20 offset:0 sc1\n\t"
"v_mfma_f32_4x4x4_16b_bf16 v[232:235], v[232:233], v[220:221], 0\n\t"
"s_waitcnt lgkmcnt(2)\n\t"
"v_mfma_f32_4x4x4_16b_bf16 v[228:231], v[228:229], v[220:221], 0\n\t"
"s_waitcnt lgkmcnt(1)\n\t"
"v_mfma_f32_4x4x4_16b_bf16 v[224:227], v[224:225], v[220:221], 0\n\t"
"v_mfma_f32_4x4x4_16b_bf16 v[236:239], v[236:237], v[220:221], 0\n\t"
"v_mfma_f32_16x16x32_bf16 a[120:123], a[212:215], a[180:183], a[120:123]\n\t"
"ds_read_b128 a[172:175], v223 offset:7424\n\t"
"v_cvt_pk_bf16_f32 v232, v232, v233\n\t"
"v_mfma_f32_16x16x32_bf16 a[120:123], v[96:99], a[176:179], a[120:123]\n\t"
"ds_read_b128 a[176:179], v223 offset:13568\n\t"
"v_cvt_pk_bf16_f32 v233, v234, v235\n\t"
"v_mfma_f32_16x16x32_bf16 a[120:123], a[228:231], a[184:187], a[120:123]\n\t"
"v_cvt_pk_bf16_f32 v234, v228, v229\n\t"
"v_cvt_pk_bf16_f32 v235, v230, v231\n\t"
"v_mfma_f32_16x16x32_bf16 a[128:131], v[96:99], a[180:183], a[128:131]\n\t"
"ds_read_b128 a[180:183], v222 offset:21248\n\t"
"v_cvt_pk_bf16_f32 v224, v224, v225\n\t"
"v_mfma_f32_16x16x32_bf16 a[128:131], a[228:231], a[188:191], a[128:131]\n\t"
"v_cvt_pk_bf16_f32 v225, v226, v227\n\t"
"v_cvt_pk_bf16_f32 v226, v236, v237\n\t"
"v_mfma_f32_16x16x32_bf16 a[128:131], a[212:215], a[184:187], a[128:131]\n\t"
"ds_write_b128 v245, v[232:235] offset:26112\n\t"
"v_cvt_pk_bf16_f32 v227, v238, v239\n\t"
"v_mfma_f32_16x16x32_bf16 a[136:139], v[96:99], a[184:187], a[136:139]\n\t"
"v_mfma_f32_16x16x32_bf16 a[136:139], a[212:215], a[188:191], a[136:139]\n\t"
"ds_read_b128 a[184:187], v223 offset:27392\n\t"
"v_mfma_f32_16x16x32_bf16 a[136:139], a[228:231], a[192:195], a[136:139]\n\t"
"ds_write_b128 v245, v[224:227] offset:32256\n\t"
"v_mfma_f32_16x16x32_bf16 a[144:147], a[228:231], a[196:199], a[144:147]\n\t"
"v_mfma_f32_16x16x32_bf16 a[144:147], a[212:215], a[192:195], a[144:147]\n\t"
"v_mfma_f32_16x16x32_bf16 a[144:147], v[96:99], a[188:191], a[144:147]\n\t"
"ds_read_b128 a[188:191], v223 offset:33536\n\t"
"v_mfma_f32_16x16x32_bf16 a[152:155], v[96:99], a[192:195], a[152:155]\n\t"
"ds_read_b128 a[192:195], v222 offset:41216\n\t"
"v_mfma_f32_16x16x32_bf16 a[152:155], a[212:215], a[196:199], a[152:155]\n\t"
"v_mfma_f32_16x16x32_bf16 a[152:155], a[228:231], a[200:203], a[152:155]\n\t"
"s_barrier\n\t"
"v_mfma_f32_16x16x32_bf16 a[160:163], a[212:215], a[200:203], a[160:163]\n\t"
"s_waitcnt vmcnt(7) lgkmcnt(6)\n\t"
"ds_read_b64_tr_b16 v[228:229], v240 offset:39936\n\t"
"v_mfma_f32_16x16x32_bf16 a[160:163], v[96:99], a[196:199], a[160:163]\n\t"
"ds_read_b64_tr_b16 v[230:231], v240 offset:41472\n\t"
"v_mfma_f32_16x16x32_bf16 a[160:163], a[228:231], a[204:207], a[160:163]\n\t"
"ds_read_b64_tr_b16 v[236:237], v241 offset:39936\n\t"
"v_mfma_f32_16x16x32_bf16 v[200:203], a[232:235], a[168:171], v[200:203]\n\t"
"s_waitcnt lgkmcnt(8)\n\t"
"ds_read_b64_tr_b16 v[238:239], v241 offset:41472\n\t"
"v_subrev_u32 v240, 59904, v240\n\t"
"v_mfma_f32_16x16x32_bf16 v[200:203], a[236:239], a[176:179], v[200:203]\n\t"
"ds_read_b128 a[196:199], v223 offset:47360\n\t"
"v_subrev_u32 v241, 59904, v241\n\t"
"v_mfma_f32_16x16x32_bf16 v[200:203], v[248:251], a[172:175], v[200:203]\n\t"
"ds_read_b64_tr_b16 v[232:233], v240 offset:39936\n\t"
"v_mfma_f32_16x16x32_bf16 v[208:211], a[232:235], a[172:175], v[208:211]\n\t"
"s_waitcnt lgkmcnt(4)\n\t"
"ds_read_b64_tr_b16 v[234:235], v240 offset:41472\n\t"
"v_mfma_f32_16x16x32_bf16 v[208:211], v[248:251], a[176:179], v[208:211]\n\t"
"buffer_load_dwordx4 a[212:215], off, s[80:83], s20 offset:1024 sc1\n\t"
"v_mfma_f32_16x16x32_bf16 v[208:211], a[236:239], a[180:183], v[208:211]\n\t"
"s_waitcnt lgkmcnt(3)\n\t"
"ds_read_b64_tr_b16 v[224:225], v241 offset:39936\n\t"
"ds_read_b64_tr_b16 v[226:227], v241 offset:41472\n\t"
"v_mfma_f32_16x16x32_bf16 v[228:231], v[228:231], a[208:211], 0\n\t"
"buffer_load_dwordx4 v[96:99], off, s[80:83], s20 offset:2048 sc1\n\t"
"s_mov_b32 s20, 712704\n\t"
"v_mfma_f32_16x16x32_bf16 v[236:239], v[236:239], a[208:211], 0\n\t"
"s_waitcnt lgkmcnt(2)\n\t"
"buffer_load_dwordx4 a[228:231], off, s[80:83], s20 offset:0 sc1\n\t"
"v_mfma_f32_16x16x32_bf16 v[232:235], v[232:235], a[208:211], 0\n\t"
"s_waitcnt lgkmcnt(0)\n\t"
"ds_read_b128 a[200:203], v223 offset:53504\n\t"
"v_mfma_f32_16x16x32_bf16 v[224:227], v[224:227], a[208:211], 0\n\t"
"ds_read_b128 a[204:207], v222 offset:61184\n\t"
"v_mfma_f32_16x16x32_bf16 v[216:219], v[248:251], a[180:183], v[216:219]\n\t"
"v_mfma_f32_16x16x32_bf16 v[216:219], a[232:235], a[176:179], v[216:219]\n\t"
"v_cvt_pk_bf16_f32 v228, v228, v229\n\t"
"v_cvt_pk_bf16_f32 v229, v230, v231\n\t"
"v_mfma_f32_16x16x32_bf16 v[216:219], a[236:239], a[184:187], v[216:219]\n\t"
"v_cvt_pk_bf16_f32 v230, v236, v237\n\t"
"v_cvt_pk_bf16_f32 v231, v238, v239\n\t"
"v_mfma_f32_16x16x32_bf16 a[4:7], a[232:235], a[180:183], a[4:7]\n\t"
"ds_read_b64_tr_b16 v[236:237], v246 offset:39936\n\t"
"v_cvt_pk_bf16_f32 v232, v232, v233\n\t"
"v_mfma_f32_16x16x32_bf16 a[4:7], a[236:239], a[188:191], a[4:7]\n\t"
"v_cvt_pk_bf16_f32 v233, v234, v235\n\t"
"v_cvt_pk_bf16_f32 v234, v224, v225\n\t"
"v_mfma_f32_16x16x32_bf16 a[4:7], v[248:251], a[184:187], a[4:7]\n\t"
"ds_write_b128 v242, v[228:231] offset:32256\n\t"
"v_mfma_f32_16x16x32_bf16 a[12:15], a[232:235], a[184:187], a[12:15]\n\t"
"v_cvt_pk_bf16_f32 v235, v226, v227\n\t"
"v_mfma_f32_16x16x32_bf16 a[12:15], v[248:251], a[188:191], a[12:15]\n\t"
"ds_read_b64_tr_b16 v[224:225], v255 offset:39936\n\t"
"v_mfma_f32_16x16x32_bf16 a[12:15], a[236:239], a[192:195], a[12:15]\n\t"
"ds_write_b128 v242, v[232:235] offset:27648\n\t"
"v_mfma_f32_16x16x32_bf16 a[20:23], a[236:239], a[196:199], a[20:23]\n\t"
"s_waitcnt lgkmcnt(5)\n\t"
"v_mfma_f32_16x16x32_bf16 a[20:23], v[248:251], a[192:195], a[20:23]\n\t"
"ds_read_b64_tr_b16 v[228:229], v246 offset:46080\n\t"
"v_mfma_f32_16x16x32_bf16 a[20:23], a[232:235], a[188:191], a[20:23]\n\t"
"ds_read_b64_tr_b16 v[232:233], v255 offset:46080\n\t"
"v_mfma_f32_16x16x32_bf16 a[28:31], a[232:235], a[192:195], a[28:31]\n\t"
"s_waitcnt lgkmcnt(6)\n\t"
"v_mfma_f32_16x16x32_bf16 a[28:31], v[248:251], a[196:199], a[28:31]\n\t"
"v_mfma_f32_16x16x32_bf16 a[28:31], a[236:239], a[200:203], a[28:31]\n\t"
"s_barrier\n\t"
"v_mfma_f32_16x16x32_bf16 a[36:39], v[248:251], a[200:203], a[36:39]\n\t"
"s_waitcnt lgkmcnt(5)\n\t"
"v_mfma_f32_16x16x32_bf16 a[36:39], a[232:235], a[196:199], a[36:39]\n\t"
"buffer_load_dwordx4 v[248:251], off, s[80:83], s20 offset:1024 sc1\n\t"
"v_mfma_f32_16x16x32_bf16 a[36:39], a[236:239], a[204:207], a[36:39]\n\t"
"s_waitcnt lgkmcnt(1)\n\t"
"v_mfma_f32_4x4x4_16b_bf16 v[236:239], v[236:237], v[220:221], 0\n\t"
"s_waitcnt lgkmcnt(0)\n\t"
"v_mfma_f32_4x4x4_16b_bf16 v[224:227], v[224:225], v[220:221], 0\n\t"
"s_waitcnt vmcnt(8)\n\t"
"v_mfma_f32_4x4x4_16b_bf16 v[228:231], v[228:229], v[220:221], 0\n\t"
"v_mfma_f32_4x4x4_16b_bf16 v[232:235], v[232:233], v[220:221], 0\n\t"
"buffer_load_dwordx4 a[232:235], off, s[80:83], s20 offset:2048 sc1\n\t"
"s_mov_b32 s20, 724992\n\t"
"v_mfma_f32_16x16x32_bf16 a[44:47], a[240:243], a[168:171], a[44:47]\n\t"
"buffer_load_dwordx4 a[236:239], off, s[80:83], s20 offset:0 sc1\n\t"
"v_mfma_f32_16x16x32_bf16 a[44:47], a[248:251], a[176:179], a[44:47]\n\t"
"v_cvt_pk_bf16_f32 v236, v236, v237\n\t"
"v_cvt_pk_bf16_f32 v237, v238, v239\n\t"
"v_mfma_f32_16x16x32_bf16 a[44:47], a[244:247], a[172:175], a[44:47]\n\t"
"v_cvt_pk_bf16_f32 v238, v224, v225\n\t"
"v_cvt_pk_bf16_f32 v239, v226, v227\n\t"
"v_mfma_f32_16x16x32_bf16 a[52:55], a[240:243], a[172:175], a[52:55]\n\t"
"ds_read_b64_tr_b16 v[224:225], v246 offset:52224\n\t"
"v_cvt_pk_bf16_f32 v228, v228, v229\n\t"
"v_mfma_f32_16x16x32_bf16 a[52:55], a[244:247], a[176:179], a[52:55]\n\t"
"v_cvt_pk_bf16_f32 v229, v230, v231\n\t"
"v_cvt_pk_bf16_f32 v230, v232, v233\n\t"
"v_mfma_f32_16x16x32_bf16 a[52:55], a[248:251], a[180:183], a[52:55]\n\t"
"ds_write_b128 v245, v[236:239] offset:39936\n\t"
"v_cvt_pk_bf16_f32 v231, v234, v235\n\t"
"v_mfma_f32_16x16x32_bf16 a[60:63], a[248:251], a[184:187], a[60:63]\n\t"
"v_mfma_f32_16x16x32_bf16 a[60:63], a[244:247], a[180:183], a[60:63]\n\t"
"ds_read_b64_tr_b16 v[232:233], v255 offset:52224\n\t"
"v_mfma_f32_16x16x32_bf16 a[60:63], a[240:243], a[176:179], a[60:63]\n\t"
"ds_write_b128 v245, v[228:231] offset:46080\n\t"
"v_mfma_f32_16x16x32_bf16 a[68:71], a[240:243], a[180:183], a[68:71]\n\t"
"v_mfma_f32_16x16x32_bf16 a[68:71], a[244:247], a[184:187], a[68:71]\n\t"
"v_mfma_f32_16x16x32_bf16 a[68:71], a[248:251], a[188:191], a[68:71]\n\t"
"v_mfma_f32_16x16x32_bf16 a[76:79], a[240:243], a[184:187], a[76:79]\n\t"
"s_waitcnt lgkmcnt(3)\n\t"
"v_mfma_f32_16x16x32_bf16 a[76:79], a[248:251], a[192:195], a[76:79]\n\t"
"s_waitcnt lgkmcnt(1)\n\t"
"v_mfma_f32_16x16x32_bf16 a[76:79], a[244:247], a[188:191], a[76:79]\n\t"
"v_mfma_f32_4x4x4_16b_bf16 v[224:227], v[224:225], v[220:221], 0\n\t"
"v_mfma_f32_4x4x4_16b_bf16 v[232:235], v[232:233], v[220:221], 0\n\t"
"v_mfma_f32_16x16x32_bf16 a[84:87], a[240:243], a[188:191], a[84:87]\n\t"
"v_mfma_f32_16x16x32_bf16 a[84:87], a[248:251], a[196:199], a[84:87]\n\t"
"v_cvt_pk_bf16_f32 v224, v224, v225\n\t"
"v_cvt_pk_bf16_f32 v225, v226, v227\n\t"
"v_mfma_f32_16x16x32_bf16 a[84:87], a[244:247], a[192:195], a[84:87]\n\t"
"v_cvt_pk_bf16_f32 v226, v232, v233\n\t"
"v_cvt_pk_bf16_f32 v227, v234, v235\n\t"
"v_mfma_f32_16x16x32_bf16 a[92:95], a[240:243], a[192:195], a[92:95]\n\t"
"v_mfma_f32_16x16x32_bf16 a[92:95], a[244:247], a[196:199], a[92:95]\n\t"
"v_mfma_f32_16x16x32_bf16 a[92:95], a[248:251], a[200:203], a[92:95]\n\t"
"ds_write_b128 v245, v[224:227] offset:52224\n\t"
"v_mfma_f32_16x16x32_bf16 a[100:103], a[244:247], a[200:203], a[100:103]\n\t"
"buffer_load_dwordx4 a[244:247], off, s[80:83], s20 offset:1024 sc1\n\t"
"v_mfma_f32_16x16x32_bf16 a[100:103], a[240:243], a[196:199], a[100:103]\n\t"
"s_waitcnt vmcnt(8)\n\t"
"v_mfma_f32_16x16x32_bf16 a[100:103], a[248:251], a[204:207], a[100:103]\n\t"
"v_mfma_f32_16x16x32_bf16 a[108:111], a[252:255], a[168:171], a[108:111]\n\t"
"buffer_load_dwordx4 a[240:243], off, s[80:83], s20 offset:2048 sc1\n\t"
"v_mfma_f32_16x16x32_bf16 a[108:111], a[216:219], a[176:179], a[108:111]\n\t"
"s_mov_b32 s20, 737280\n\t"
"v_mfma_f32_16x16x32_bf16 a[108:111], a[220:223], a[172:175], a[108:111]\n\t"
"s_waitcnt lgkmcnt(0)\n\t"
"s_barrier\n\t"
"v_mfma_f32_16x16x32_bf16 a[116:119], a[252:255], a[172:175], a[116:119]\n\t"
"ds_read_b128 a[168:171], v222 offset:0\n\t"
"v_mfma_f32_16x16x32_bf16 a[116:119], a[220:223], a[176:179], a[116:119]\n\t"
"buffer_load_dwordx4 a[248:251], off, s[80:83], s20 offset:0 sc1\n\t"
"v_mfma_f32_16x16x32_bf16 a[116:119], a[216:219], a[180:183], a[116:119]\n\t"
"ds_read_b128 a[172:175], v223 offset:6144\n\t"
"v_mfma_f32_16x16x32_bf16 a[124:127], a[216:219], a[184:187], a[124:127]\n\t"
"v_mfma_f32_16x16x32_bf16 a[124:127], a[220:223], a[180:183], a[124:127]\n\t"
"v_mfma_f32_16x16x32_bf16 a[124:127], a[252:255], a[176:179], a[124:127]\n\t"
"ds_read_b128 a[176:179], v223 offset:12288\n\t"
"v_mfma_f32_16x16x32_bf16 a[132:135], a[252:255], a[180:183], a[132:135]\n\t"
"ds_read_b64_tr_b16 v[236:237], v240 offset:512\n\t"
"v_mfma_f32_16x16x32_bf16 a[132:135], a[220:223], a[184:187], a[132:135]\n\t"
"ds_read_b64_tr_b16 v[238:239], v240 offset:2048\n\t"
"v_mfma_f32_16x16x32_bf16 a[132:135], a[216:219], a[188:191], a[132:135]\n\t"
"ds_read_b64_tr_b16 v[228:229], v241 offset:512\n\t"
"v_mfma_f32_16x16x32_bf16 a[140:143], a[216:219], a[192:195], a[140:143]\n\t"
"ds_read_b64_tr_b16 v[230:231], v241 offset:2048\n\t"
"v_mfma_f32_16x16x32_bf16 a[140:143], a[220:223], a[188:191], a[140:143]\n\t"
"ds_read_b128 a[180:183], v222 offset:19968\n\t"
"v_mfma_f32_16x16x32_bf16 a[140:143], a[252:255], a[184:187], a[140:143]\n\t"
"ds_read_b64_tr_b16 v[232:233], v240 offset:20480\n\t"
"v_mfma_f32_16x16x32_bf16 a[148:151], a[252:255], a[188:191], a[148:151]\n\t"
"ds_read_b64_tr_b16 v[234:235], v240 offset:22016\n\t"
"v_mfma_f32_16x16x32_bf16 a[148:151], a[220:223], a[192:195], a[148:151]\n\t"
"ds_read_b64_tr_b16 v[224:225], v241 offset:20480\n\t"
"v_mfma_f32_16x16x32_bf16 a[148:151], a[216:219], a[196:199], a[148:151]\n\t"
"ds_read_b64_tr_b16 v[226:227], v241 offset:22016\n\t"
"v_mfma_f32_16x16x32_bf16 a[156:159], a[216:219], a[200:203], a[156:159]\n\t"
"ds_read_b128 a[184:187], v223 offset:26112\n\t"
"v_mfma_f32_16x16x32_bf16 a[156:159], a[220:223], a[196:199], a[156:159]\n\t"
"ds_read_b128 a[188:191], v223 offset:32256\n\t"
"v_mfma_f32_16x16x32_bf16 a[156:159], a[252:255], a[192:195], a[156:159]\n\t"
"ds_read_b128 a[192:195], v222 offset:39936\n\t"
"v_mfma_f32_16x16x32_bf16 a[164:167], a[252:255], a[196:199], a[164:167]\n\t"
"buffer_load_dwordx4 a[252:255], off, s[80:83], s20 offset:1024 sc1\n\t"
"v_mfma_f32_16x16x32_bf16 a[164:167], a[220:223], a[200:203], a[164:167]\n\t"
"s_waitcnt lgkmcnt(8)\n\t"
"ds_read_b128 a[196:199], v223 offset:46080\n\t"
"v_mfma_f32_16x16x32_bf16 a[164:167], a[216:219], a[204:207], a[164:167]\n\t"
"s_waitcnt lgkmcnt(6)\n\t"
"ds_read_b128 a[200:203], v223 offset:52224\n\t"
"v_mfma_f32_16x16x32_bf16 v[236:239], v[236:239], a[208:211], 0\n\t"
"buffer_load_dwordx4 a[216:219], off, s[80:83], s20 offset:2048 sc1\n\t"
"v_mfma_f32_16x16x32_bf16 v[228:231], v[228:231], a[208:211], 0\n\t"
"s_waitcnt vmcnt(9) lgkmcnt(5)\n\t"
"ds_read_b128 a[204:207], v222 offset:59904\n\t"
"s_mov_b32 s20, 749568\n\t"
"v_mfma_f32_16x16x32_bf16 v[232:235], v[232:235], a[208:211], 0\n\t"
"v_mfma_f32_16x16x32_bf16 v[224:227], v[224:227], a[208:211], 0\n\t"
"buffer_load_dwordx4 a[220:223], off, s[80:83], s20 offset:0 sc1\n\t"
"Lst24_%=:\n\t"
"v_mfma_f32_16x16x32_bf16 v[196:199], a[224:227], a[168:171], v[196:199]\n\t"
"v_mfma_f32_16x16x32_bf16 v[196:199], v[96:99], a[176:179], v[196:199]\n\t"
"v_cvt_pk_bf16_f32 v236, v236, v237\n\t"
"v_cvt_pk_bf16_f32 v237, v238, v239\n\t"
"v_mfma_f32_16x16x32_bf16 v[196:199], a[212:215], a[172:175], v[196:199]\n\t"
"v_cvt_pk_bf16_f32 v238, v228, v229\n\t"
"v_cvt_pk_bf16_f32 v239, v230, v231\n\t"
"v_mfma_f32_16x16x32_bf16 v[204:207], a[224:227], a[172:175], v[204:207]\n\t"
"s_waitcnt lgkmcnt(5)\n\t"
"v_cvt_pk_bf16_f32 v232, v232, v233\n\t"
"v_cvt_pk_bf16_f32 v233, v234, v235\n\t"
"v_mfma_f32_16x16x32_bf16 v[204:207], a[212:215], a[176:179], v[204:207]\n\t"
"v_cvt_pk_bf16_f32 v234, v224, v225\n\t"
"v_cvt_pk_bf16_f32 v235, v226, v227\n\t"
"v_mfma_f32_16x16x32_bf16 v[204:207], v[96:99], a[180:183], v[204:207]\n\t"
"ds_write_b128 v242, v[236:239] offset:0\n\t"
"v_mfma_f32_16x16x32_bf16 v[212:215], v[96:99], a[184:187], v[212:215]\n\t"
"s_waitcnt lgkmcnt(5)\n\t"
"v_mfma_f32_16x16x32_bf16 v[212:215], a[212:215], a[180:183], v[212:215]\n\t"
"v_mfma_f32_16x16x32_bf16 v[212:215], a[224:227], a[176:179], v[212:215]\n\t"
"ds_write_b128 v242, v[232:235] offset:1536\n\t"
"v_mfma_f32_16x16x32_bf16 a[0:3], a[224:227], a[180:183], a[0:3]\n\t"
"s_waitcnt lgkmcnt(5)\n\t"
"v_mfma_f32_16x16x32_bf16 a[0:3], a[212:215], a[184:187], a[0:3]\n\t"
"ds_read_b64_tr_b16 v[228:229], v240 offset:40448\n\t"
"v_mfma_f32_16x16x32_bf16 a[0:3], v[96:99], a[188:191], a[0:3]\n\t"
"ds_read_b64_tr_b16 v[230:231], v240 offset:41984\n\t"
"v_mfma_f32_16x16x32_bf16 a[8:11], v[96:99], a[192:195], a[8:11]\n\t"
"s_waitcnt lgkmcnt(6)\n\t"
"ds_read_b64_tr_b16 v[224:225], v241 offset:40448\n\t"
"v_mfma_f32_16x16x32_bf16 a[8:11], a[212:215], a[188:191], a[8:11]\n\t"
"ds_read_b64_tr_b16 v[226:227], v241 offset:41984\n\t"
"v_add_u32 v240, 59904, v240\n\t"
"v_mfma_f32_16x16x32_bf16 a[8:11], a[224:227], a[184:187], a[8:11]\n\t"
"v_add_u32 v241, 59904, v241\n\t"
"v_mfma_f32_16x16x32_bf16 a[16:19], a[224:227], a[188:191], a[16:19]\n\t"
"s_waitcnt lgkmcnt(7)\n\t"
"ds_read_b64_tr_b16 v[236:237], v240 offset:512\n\t"
"v_mfma_f32_16x16x32_bf16 a[16:19], a[212:215], a[192:195], a[16:19]\n\t"
"ds_read_b64_tr_b16 v[238:239], v240 offset:2048\n\t"
"v_mfma_f32_16x16x32_bf16 a[16:19], v[96:99], a[196:199], a[16:19]\n\t"
"ds_read_b64_tr_b16 v[232:233], v241 offset:512\n\t"
"v_mfma_f32_16x16x32_bf16 a[24:27], v[96:99], a[200:203], a[24:27]\n\t"
"s_waitcnt lgkmcnt(9)\n\t"
"ds_read_b64_tr_b16 v[234:235], v241 offset:2048\n\t"
"v_mfma_f32_16x16x32_bf16 a[24:27], a[212:215], a[196:199], a[24:27]\n\t"
"v_mfma_f32_16x16x32_bf16 a[24:27], a[224:227], a[192:195], a[24:27]\n\t"
"v_mfma_f32_16x16x32_bf16 a[32:35], a[224:227], a[196:199], a[32:35]\n\t"
"buffer_load_dwordx4 a[224:227], off, s[80:83], s20 offset:1024 sc1\n\t"
"v_mfma_f32_16x16x32_bf16 a[32:35], a[212:215], a[200:203], a[32:35]\n\t"
"s_waitcnt lgkmcnt(4)\n\t"
"v_mfma_f32_16x16x32_bf16 a[32:35], v[96:99], a[204:207], a[32:35]\n\t"
"s_waitcnt lgkmcnt(2)\n\t"
"v_mfma_f32_16x16x32_bf16 v[228:231], v[228:231], a[208:211], 0\n\t"
"buffer_load_dwordx4 v[96:99], off, s[80:83], s20 offset:2048 sc1\n\t"
"v_mfma_f32_16x16x32_bf16 v[224:227], v[224:227], a[208:211], 0\n\t"
"s_waitcnt vmcnt(9) lgkmcnt(0)\n\t"
"s_mov_b32 s20, 761856\n\t"
"v_mfma_f32_16x16x32_bf16 v[236:239], v[236:239], a[208:211], 0\n\t"
"v_mfma_f32_16x16x32_bf16 v[232:235], v[232:235], a[208:211], 0\n\t"
"buffer_load_dwordx4 a[212:215], off, s[80:83], s20 offset:0 sc1\n\t"
"v_mfma_f32_16x16x32_bf16 a[40:43], a[228:231], a[168:171], a[40:43]\n\t"
"v_mfma_f32_16x16x32_bf16 a[40:43], a[232:235], a[176:179], a[40:43]\n\t"
"v_cvt_pk_bf16_f32 v228, v228, v229\n\t"
"v_cvt_pk_bf16_f32 v229, v230, v231\n\t"
"v_mfma_f32_16x16x32_bf16 a[40:43], v[248:251], a[172:175], a[40:43]\n\t"
"v_cvt_pk_bf16_f32 v230, v224, v225\n\t"
"v_cvt_pk_bf16_f32 v231, v226, v227\n\t"
"v_mfma_f32_16x16x32_bf16 a[48:51], a[228:231], a[172:175], a[48:51]\n\t"
"v_cvt_pk_bf16_f32 v236, v236, v237\n\t"
"v_cvt_pk_bf16_f32 v237, v238, v239\n\t"
"v_mfma_f32_16x16x32_bf16 a[48:51], v[248:251], a[176:179], a[48:51]\n\t"
"v_cvt_pk_bf16_f32 v238, v232, v233\n\t"
"v_cvt_pk_bf16_f32 v239, v234, v235\n\t"
"v_mfma_f32_16x16x32_bf16 a[48:51], a[232:235], a[180:183], a[48:51]\n\t"
"ds_write_b128 v242, v[228:231] offset:3072\n\t"
"v_mfma_f32_16x16x32_bf16 a[56:59], a[232:235], a[184:187], a[56:59]\n\t"
"v_mfma_f32_16x16x32_bf16 a[56:59], v[248:251], a[180:183], a[56:59]\n\t"
"v_mfma_f32_16x16x32_bf16 a[56:59], a[228:231], a[176:179], a[56:59]\n\t"
"ds_write_b128 v242, v[236:239] offset:4608\n\t"
"v_mfma_f32_16x16x32_bf16 a[64:67], a[228:231], a[180:183], a[64:67]\n\t"
"v_mfma_f32_16x16x32_bf16 a[64:67], v[248:251], a[184:187], a[64:67]\n\t"
"ds_read_b64_tr_b16 v[224:225], v240 offset:20480\n\t"
"v_mfma_f32_16x16x32_bf16 a[64:67], a[232:235], a[188:191], a[64:67]\n\t"
"ds_read_b64_tr_b16 v[226:227], v240 offset:22016\n\t"
"v_mfma_f32_16x16x32_bf16 a[72:75], a[232:235], a[192:195], a[72:75]\n\t"
"ds_read_b64_tr_b16 v[232:233], v241 offset:20480\n\t"
"v_mfma_f32_16x16x32_bf16 a[72:75], v[248:251], a[188:191], a[72:75]\n\t"
"ds_read_b64_tr_b16 v[234:235], v241 offset:22016\n\t"
"v_mfma_f32_16x16x32_bf16 a[72:75], a[228:231], a[184:187], a[72:75]\n\t"
"ds_read_b64_tr_b16 v[228:229], v240 offset:40448\n\t"
"v_mfma_f32_16x16x32_bf16 a[80:83], a[228:231], a[188:191], a[80:83]\n\t"
"ds_read_b64_tr_b16 v[230:231], v240 offset:41984\n\t"
"v_mfma_f32_16x16x32_bf16 a[80:83], v[248:251], a[192:195], a[80:83]\n\t"
"ds_read_b64_tr_b16 v[236:237], v241 offset:40448\n\t"
"v_mfma_f32_16x16x32_bf16 a[80:83], a[232:235], a[196:199], a[80:83]\n\t"
"ds_read_b64_tr_b16 v[238:239], v241 offset:41984\n\t"
"v_mfma_f32_16x16x32_bf16 a[88:91], a[232:235], a[200:203], a[88:91]\n\t"
"v_mfma_f32_16x16x32_bf16 a[88:91], v[248:251], a[196:199], a[88:91]\n\t"
"v_mfma_f32_16x16x32_bf16 a[88:91], a[228:231], a[192:195], a[88:91]\n\t"
"v_mfma_f32_16x16x32_bf16 a[96:99], a[228:231], a[196:199], a[96:99]\n\t"
"buffer_load_dwordx4 a[228:231], off, s[80:83], s20 offset:1024 sc1\n\t"
"v_mfma_f32_16x16x32_bf16 a[96:99], v[248:251], a[200:203], a[96:99]\n\t"
"s_waitcnt lgkmcnt(4)\n\t"
"v_mfma_f32_16x16x32_bf16 a[96:99], a[232:235], a[204:207], a[96:99]\n\t"
"s_waitcnt lgkmcnt(2)\n\t"
"v_mfma_f32_16x16x32_bf16 v[224:227], v[224:227], a[208:211], 0\n\t"
"buffer_load_dwordx4 v[248:251], off, s[80:83], s20 offset:2048 sc1\n\t"
"v_mfma_f32_16x16x32_bf16 v[232:235], v[232:235], a[208:211], 0\n\t"
"s_waitcnt vmcnt(9) lgkmcnt(0)\n\t"
"s_mov_b32 s20, 626688\n\t"
"v_mfma_f32_16x16x32_bf16 v[228:231], v[228:231], a[208:211], 0\n\t"
"v_mfma_f32_16x16x32_bf16 v[236:239], v[236:239], a[208:211], 0\n\t"
"buffer_load_dwordx4 a[232:235], off, s[80:83], s20 offset:0 sc1\n\t"
"v_mfma_f32_16x16x32_bf16 a[104:107], a[236:239], a[168:171], a[104:107]\n\t"
"ds_read_b128 a[168:171], v222 offset:256\n\t"
"v_mfma_f32_16x16x32_bf16 a[104:107], a[240:243], a[176:179], a[104:107]\n\t"
"v_cvt_pk_bf16_f32 v224, v224, v225\n\t"
"v_cvt_pk_bf16_f32 v225, v226, v227\n\t"
"v_mfma_f32_16x16x32_bf16 a[104:107], a[244:247], a[172:175], a[104:107]\n\t"
"v_cvt_pk_bf16_f32 v226, v232, v233\n\t"
"v_cvt_pk_bf16_f32 v227, v234, v235\n\t"
"v_mfma_f32_16x16x32_bf16 a[112:115], a[236:239], a[172:175], a[112:115]\n\t"
"ds_read_b128 a[172:175], v223 offset:6400\n\t"
"v_cvt_pk_bf16_f32 v228, v228, v229\n\t"
"v_mfma_f32_16x16x32_bf16 a[112:115], a[244:247], a[176:179], a[112:115]\n\t"
"v_cvt_pk_bf16_f32 v229, v230, v231\n\t"
"v_cvt_pk_bf16_f32 v230, v236, v237\n\t"
"v_mfma_f32_16x16x32_bf16 a[112:115], a[240:243], a[180:183], a[112:115]\n\t"
"Lpg18_%=:\n\t"
"s_cbranch_vccnz Lvr2_%=\n\t"
"buffer_load_dwordx4 a[208:211], off, s[80:83], s20 offset:1024 sc1\n\t"
"v_mfma_f32_16x16x32_bf16 a[120:123], a[240:243], a[184:187], a[120:123]\n\t"
"v_cvt_pk_bf16_f32 v231, v238, v239\n\t"
"v_mfma_f32_16x16x32_bf16 a[120:123], a[244:247], a[180:183], a[120:123]\n\t"
"v_mfma_f32_16x16x32_bf16 a[120:123], a[236:239], a[176:179], a[120:123]\n\t"
"ds_write_b128 v242, v[224:227] offset:6144\n\t"
"v_mfma_f32_16x16x32_bf16 a[128:131], a[236:239], a[180:183], a[128:131]\n\t"
"v_mfma_f32_16x16x32_bf16 a[128:131], a[244:247], a[184:187], a[128:131]\n\t"
"ds_read_b128 a[176:179], v223 offset:12544\n\t"
"v_mfma_f32_16x16x32_bf16 a[128:131], a[240:243], a[188:191], a[128:131]\n\t"
"ds_write_b128 v242, v[228:231] offset:7680\n\t"
"v_mfma_f32_16x16x32_bf16 a[136:139], a[240:243], a[192:195], a[136:139]\n\t"
"buffer_load_dwordx4 v[240:243], off, s[80:83], s20 offset:2048 sc1\n\t"
"v_mfma_f32_16x16x32_bf16 a[136:139], a[244:247], a[188:191], a[136:139]\n\t"
"ds_read_b128 a[180:183], v222 offset:20224\n\t"
"v_mfma_f32_16x16x32_bf16 a[136:139], a[236:239], a[184:187], a[136:139]\n\t"
"ds_read_b128 a[184:187], v223 offset:26368\n\t"
"v_mfma_f32_16x16x32_bf16 a[144:147], a[236:239], a[188:191], a[144:147]\n\t"
"s_mov_b32 s20, 638976\n\t"
"ds_read_b128 a[188:191], v223 offset:32512\n\t"
"v_mfma_f32_16x16x32_bf16 a[144:147], a[244:247], a[192:195], a[144:147]\n\t"
"v_mfma_f32_16x16x32_bf16 a[144:147], a[240:243], a[196:199], a[144:147]\n\t"
"v_mfma_f32_16x16x32_bf16 a[152:155], a[240:243], a[200:203], a[152:155]\n\t"
"v_mfma_f32_16x16x32_bf16 a[152:155], a[244:247], a[196:199], a[152:155]\n\t"
"v_mfma_f32_16x16x32_bf16 a[152:155], a[236:239], a[192:195], a[152:155]\n\t"
"ds_read_b128 a[192:195], v222 offset:40192\n\t"
"v_mfma_f32_16x16x32_bf16 a[160:163], a[236:239], a[196:199], a[160:163]\n\t"
"buffer_load_dwordx4 a[236:239], off, s[80:83], s20 offset:0 sc1\n\t"
"v_mfma_f32_16x16x32_bf16 a[160:163], a[244:247], a[200:203], a[160:163]\n\t"
"s_waitcnt vmcnt(10) lgkmcnt(5)\n\t"
"ds_read_b128 a[196:199], v223 offset:46336\n\t"
"v_mfma_f32_16x16x32_bf16 a[160:163], a[240:243], a[204:207], a[160:163]\n\t"
"ds_read_b128 a[200:203], v223 offset:52480\n\t"
"v_mfma_f32_16x16x32_bf16 v[200:203], a[248:251], a[168:171], v[200:203]\n\t"
"s_waitcnt lgkmcnt(5)\n\t"
"ds_read_b128 a[204:207], v222 offset:60160\n\t"
"v_mfma_f32_16x16x32_bf16 v[200:203], a[216:219], a[176:179], v[200:203]\n\t"
"buffer_load_dwordx4 a[240:243], off, s[80:83], s20 offset:1024 sc1\n\t"
"v_mfma_f32_16x16x32_bf16 v[200:203], a[252:255], a[172:175], v[200:203]\n\t"
"s_add_u32 m0, s25, 768\n\t"
"v_mfma_f32_16x16x32_bf16 v[208:211], a[248:251], a[172:175], v[208:211]\n\t"
"buffer_load_dwordx4 a[244:247], off, s[80:83], s20 offset:2048 sc1\n\t"
"v_mfma_f32_16x16x32_bf16 v[208:211], a[252:255], a[176:179], v[208:211]\n\t"
"s_waitcnt lgkmcnt(5)\n\t"
"v_mfma_f32_16x16x32_bf16 v[208:211], a[216:219], a[180:183], v[208:211]\n\t"
"s_waitcnt lgkmcnt(0)\n\t"
"s_barrier\n\t"
"v_mfma_f32_16x16x32_bf16 v[216:219], a[216:219], a[184:187], v[216:219]\n\t"
"v_mfma_f32_16x16x32_bf16 v[216:219], a[252:255], a[180:183], v[216:219]\n\t"
"buffer_load_dwordx4 v254, s[12:15], 0 offen offset:256 nt lds\n\t"
"v_mfma_f32_16x16x32_bf16 v[216:219], a[248:251], a[176:179], v[216:219]\n\t"
"s_add_u32 m0, s26, 768\n\t"
"ds_read_b64_tr_b16 v[232:233], v246 offset:512\n\t"
"v_mfma_f32_16x16x32_bf16 a[4:7], a[248:251], a[180:183], a[4:7]\n\t"
"ds_read_b64_tr_b16 v[236:237], v255 offset:512\n\t"
"v_mfma_f32_16x16x32_bf16 a[4:7], a[252:255], a[184:187], a[4:7]\n\t"
"ds_read_b64_tr_b16 v[224:225], v246 offset:6656\n\t"
"v_mfma_f32_16x16x32_bf16 a[4:7], a[216:219], a[188:191], a[4:7]\n\t"
"s_mov_b64 exec, s[36:37]\n\t"
"buffer_load_dwordx4 v254, s[12:15], s27 offen offset:256 nt lds\n\t"
"s_mov_b64 exec, -1\n\t"
"s_add_u32 m0, s24, 2304\n\t"
"s_add_u32 s28, s21, 3072\n\t"
"v_mfma_f32_16x16x32_bf16 a[12:15], a[216:219], a[192:195], a[12:15]\n\t"
"buffer_load_dwordx4 v247, s[12:15], s28 offen offset:256 nt lds\n\t"
"v_mfma_f32_16x16x32_bf16 a[12:15], a[252:255], a[188:191], a[12:15]\n\t"
"s_add_u32 m0, s24, 8448\n\t"
"s_add_u32 s28, s21, 199680\n\t"
"ds_read_b64_tr_b16 v[228:229], v255 offset:6656\n\t"
"v_mfma_f32_16x16x32_bf16 a[12:15], a[248:251], a[184:187], a[12:15]\n\t"
"buffer_load_dwordx4 v247, s[12:15], s28 offen offset:256 nt lds\n\t"
"s_add_u32 m0, s24, 14592\n\t"
"s_add_u32 s28, s21, 396288\n\t"
"v_mfma_f32_16x16x32_bf16 a[20:23], a[248:251], a[188:191], a[20:23]\n\t"
"buffer_load_dwordx4 v247, s[12:15], s28 offen offset:256 nt lds\n\t"
"v_mfma_f32_16x16x32_bf16 a[20:23], a[252:255], a[192:195], a[20:23]\n\t"
"s_add_u32 m0, s24, 22272\n\t"
"s_add_u32 s28, s21, 592896\n\t"
"v_mfma_f32_16x16x32_bf16 a[20:23], a[216:219], a[196:199], a[20:23]\n\t"
"buffer_load_dwordx4 v247, s[12:15], s28 offen offset:256 nt lds\n\t"
"s_add_u32 m0, s24, 28416\n\t"
"s_add_u32 s28, s21, 789504\n\t"
"v_mfma_f32_16x16x32_bf16 a[28:31], a[216:219], a[200:203], a[28:31]\n\t"
"buffer_load_dwordx4 v247, s[12:15], s28 offen offset:256 nt lds\n\t"
"v_mfma_f32_16x16x32_bf16 a[28:31], a[252:255], a[196:199], a[28:31]\n\t"
"s_add_u32 m0, s24, 34560\n\t"
"s_add_u32 s28, s21, 986112\n\t"
"v_mfma_f32_16x16x32_bf16 a[28:31], a[248:251], a[192:195], a[28:31]\n\t"
"buffer_load_dwordx4 v247, s[12:15], s28 offen offset:256 nt lds\n\t"
"v_mfma_f32_16x16x32_bf16 a[36:39], a[248:251], a[196:199], a[36:39]\n\t"
"s_waitcnt lgkmcnt(3)\n\t"
"v_mfma_f32_16x16x32_bf16 a[36:39], a[252:255], a[200:203], a[36:39]\n\t"
"s_waitcnt lgkmcnt(2)\n\t"
"v_mfma_f32_16x16x32_bf16 a[36:39], a[216:219], a[204:207], a[36:39]\n\t"
"s_waitcnt lgkmcnt(1)\n\t"
"v_mfma_f32_4x4x4_16b_bf16 v[232:235], v[232:233], v[220:221], 0\n\t"
"s_waitcnt lgkmcnt(0)\n\t"
"v_mfma_f32_4x4x4_16b_bf16 v[236:239], v[236:237], v[220:221], 0\n\t"
"s_waitcnt vmcnt(17)\n\t"
"v_mfma_f32_4x4x4_16b_bf16 v[224:227], v[224:225], v[220:221], 0\n\t"
"v_mfma_f32_4x4x4_16b_bf16 v[228:231], v[228:229], v[220:221], 0\n\t"
"v_mfma_f32_16x16x32_bf16 a[44:47], a[220:223], a[168:171], a[44:47]\n\t"
"v_cvt_pk_bf16_f32 v232, v232, v233\n\t"
"v_cvt_pk_bf16_f32 v233, v234, v235\n\t"
"v_mfma_f32_16x16x32_bf16 a[44:47], v[96:99], a[176:179], a[44:47]\n\t"
"v_cvt_pk_bf16_f32 v234, v236, v237\n\t"
"v_cvt_pk_bf16_f32 v235, v238, v239\n\t"
"v_mfma_f32_16x16x32_bf16 a[44:47], a[224:227], a[172:175], a[44:47]\n\t"
"ds_write_b128 v245, v[232:235] offset:512\n\t"
"v_mfma_f32_16x16x32_bf16 a[52:55], a[220:223], a[172:175], a[52:55]\n\t"
"v_cvt_pk_bf16_f32 v224, v224, v225\n\t"
"v_cvt_pk_bf16_f32 v225, v226, v227\n\t"
"v_mfma_f32_16x16x32_bf16 a[52:55], a[224:227], a[176:179], a[52:55]\n\t"
"v_cvt_pk_bf16_f32 v226, v228, v229\n\t"
"v_cvt_pk_bf16_f32 v227, v230, v231\n\t"
"v_mfma_f32_16x16x32_bf16 a[52:55], v[96:99], a[180:183], a[52:55]\n\t"
"ds_write_b128 v245, v[224:227] offset:6656\n\t"
"v_mfma_f32_16x16x32_bf16 a[60:63], v[96:99], a[184:187], a[60:63]\n\t"
"v_mfma_f32_16x16x32_bf16 a[60:63], a[224:227], a[180:183], a[60:63]\n\t"
"v_mfma_f32_16x16x32_bf16 a[60:63], a[220:223], a[176:179], a[60:63]\n\t"
"ds_read_b64_tr_b16 v[236:237], v246 offset:12800\n\t"
"v_mfma_f32_16x16x32_bf16 a[68:71], a[220:223], a[180:183], a[68:71]\n\t"
"ds_read_b64_tr_b16 v[228:229], v255 offset:12800\n\t"
"v_mfma_f32_16x16x32_bf16 a[68:71], a[224:227], a[184:187], a[68:71]\n\t"
"ds_read_b64_tr_b16 v[232:233], v246 offset:20480\n\t"
"v_mfma_f32_16x16x32_bf16 a[68:71], v[96:99], a[188:191], a[68:71]\n\t"
"ds_read_b64_tr_b16 v[224:225], v255 offset:20480\n\t"
"v_mfma_f32_16x16x32_bf16 a[76:79], v[96:99], a[192:195], a[76:79]\n\t"
"v_mfma_f32_16x16x32_bf16 a[76:79], a[224:227], a[188:191], a[76:79]\n\t"
"v_mfma_f32_16x16x32_bf16 a[76:79], a[220:223], a[184:187], a[76:79]\n\t"
"v_mfma_f32_16x16x32_bf16 a[84:87], a[220:223], a[188:191], a[84:87]\n\t"
"v_mfma_f32_16x16x32_bf16 a[84:87], a[224:227], a[192:195], a[84:87]\n\t"
"v_mfma_f32_16x16x32_bf16 a[84:87], v[96:99], a[196:199], a[84:87]\n\t"
"v_mfma_f32_16x16x32_bf16 a[92:95], v[96:99], a[200:203], a[92:95]\n\t"
"v_mfma_f32_16x16x32_bf16 a[92:95], a[224:227], a[196:199], a[92:95]\n\t"
"v_mfma_f32_16x16x32_bf16 a[92:95], a[220:223], a[192:195], a[92:95]\n\t"
"v_mfma_f32_16x16x32_bf16 a[100:103], a[220:223], a[196:199], a[100:103]\n\t"
"s_waitcnt lgkmcnt(3)\n\t"
"v_mfma_f32_16x16x32_bf16 a[100:103], a[224:227], a[200:203], a[100:103]\n\t"
"s_waitcnt lgkmcnt(2)\n\t"
"v_mfma_f32_16x16x32_bf16 a[100:103], v[96:99], a[204:207], a[100:103]\n\t"
"s_waitcnt lgkmcnt(1)\n\t"
"v_mfma_f32_4x4x4_16b_bf16 v[236:239], v[236:237], v[220:221], 0\n\t"
"s_waitcnt lgkmcnt(0)\n\t"
"v_mfma_f32_4x4x4_16b_bf16 v[228:231], v[228:229], v[220:221], 0\n\t"
"s_waitcnt vmcnt(14)\n\t"
"v_mfma_f32_4x4x4_16b_bf16 v[232:235], v[232:233], v[220:221], 0\n\t"
"s_mov_b32 s20, 651264\n\t"
"v_mfma_f32_4x4x4_16b_bf16 v[224:227], v[224:225], v[220:221], 0\n\t"
"v_mfma_f32_16x16x32_bf16 a[108:111], a[212:215], a[168:171], a[108:111]\n\t"
"ds_read_b128 a[168:171], v222 offset:1536\n\t"
"v_cvt_pk_bf16_f32 v236, v236, v237\n\t"
"v_mfma_f32_16x16x32_bf16 a[108:111], v[248:251], a[176:179], a[108:111]\n\t"
"v_cvt_pk_bf16_f32 v237, v238, v239\n\t"
"v_cvt_pk_bf16_f32 v238, v228, v229\n\t"
"v_mfma_f32_16x16x32_bf16 a[108:111], a[228:231], a[172:175], a[108:111]\n\t"
"v_cvt_pk_bf16_f32 v239, v230, v231\n\t"
"v_cvt_pk_bf16_f32 v232, v232, v233\n\t"
"v_mfma_f32_16x16x32_bf16 a[116:119], a[212:215], a[172:175], a[116:119]\n\t"
"ds_read_b128 a[172:175], v223 offset:7680\n\t"
"v_cvt_pk_bf16_f32 v233, v234, v235\n\t"
"v_mfma_f32_16x16x32_bf16 a[116:119], a[228:231], a[176:179], a[116:119]\n\t"
"v_cvt_pk_bf16_f32 v234, v224, v225\n\t"
"v_cvt_pk_bf16_f32 v235, v226, v227\n\t"
"v_mfma_f32_16x16x32_bf16 a[116:119], v[248:251], a[180:183], a[116:119]\n\t"
"ds_write_b128 v245, v[236:239] offset:12800\n\t"
"v_mfma_f32_16x16x32_bf16 a[124:127], v[248:251], a[184:187], a[124:127]\n\t"
"buffer_load_dwordx4 v[96:99], off, s[80:83], s20 offset:0 sc1\n\t"
"v_mfma_f32_16x16x32_bf16 a[124:127], a[228:231], a[180:183], a[124:127]\n\t"
"v_mfma_f32_16x16x32_bf16 a[124:127], a[212:215], a[176:179], a[124:127]\n\t"
"ds_write_b128 v245, v[232:235] offset:20480\n\t"
"v_mfma_f32_16x16x32_bf16 a[132:135], a[212:215], a[180:183], a[132:135]\n\t"
"v_mfma_f32_16x16x32_bf16 a[132:135], a[228:231], a[184:187], a[132:135]\n\t"
"ds_read_b128 a[176:179], v223 offset:13824\n\t"
"v_mfma_f32_16x16x32_bf16 a[132:135], v[248:251], a[188:191], a[132:135]\n\t"
"ds_read_b64_tr_b16 v[228:229], v246 offset:26624\n\t"
"v_mfma_f32_16x16x32_bf16 a[140:143], v[248:251], a[192:195], a[140:143]\n\t"
"ds_read_b64_tr_b16 v[224:225], v255 offset:26624\n\t"
"v_mfma_f32_16x16x32_bf16 a[140:143], a[228:231], a[188:191], a[140:143]\n\t"
"ds_read_b128 a[180:183], v222 offset:21504\n\t"
"v_mfma_f32_16x16x32_bf16 a[140:143], a[212:215], a[184:187], a[140:143]\n\t"
"ds_read_b64_tr_b16 v[236:237], v246 offset:32768\n\t"
"v_mfma_f32_16x16x32_bf16 a[148:151], a[212:215], a[188:191], a[148:151]\n\t"
"ds_read_b64_tr_b16 v[232:233], v255 offset:32768\n\t"
"v_mfma_f32_16x16x32_bf16 a[148:151], a[228:231], a[192:195], a[148:151]\n\t"
"ds_read_b128 a[184:187], v223 offset:27648\n\t"
"v_mfma_f32_16x16x32_bf16 a[148:151], v[248:251], a[196:199], a[148:151]\n\t"
"ds_read_b128 a[188:191], v223 offset:33792\n\t"
"v_mfma_f32_16x16x32_bf16 a[156:159], v[248:251], a[200:203], a[156:159]\n\t"
"v_mfma_f32_16x16x32_bf16 a[156:159], a[228:231], a[196:199], a[156:159]\n\t"
"v_mfma_f32_16x16x32_bf16 a[156:159], a[212:215], a[192:195], a[156:159]\n\t"
"ds_read_b128 a[192:195], v222 offset:41472\n\t"
"v_mfma_f32_16x16x32_bf16 a[164:167], a[212:215], a[196:199], a[164:167]\n\t"
"s_waitcnt lgkmcnt(7)\n\t"
"ds_read_b128 a[196:199], v223 offset:47616\n\t"
"v_mfma_f32_16x16x32_bf16 a[164:167], a[228:231], a[200:203], a[164:167]\n\t"
"s_waitcnt lgkmcnt(7)\n\t"
"ds_read_b128 a[200:203], v223 offset:53760\n\t"
"v_mfma_f32_16x16x32_bf16 a[164:167], v[248:251], a[204:207], a[164:167]\n\t"
"s_waitcnt lgkmcnt(6)\n\t"
"ds_read_b128 a[204:207], v222 offset:61440\n\t"
"v_mfma_f32_4x4x4_16b_bf16 v[228:231], v[228:229], v[220:221], 0\n\t"
"s_waitcnt lgkmcnt(6)\n\t"
"v_mfma_f32_4x4x4_16b_bf16 v[224:227], v[224:225], v[220:221], 0\n\t"
"s_waitcnt vmcnt(12)\n\t"
"v_mfma_f32_4x4x4_16b_bf16 v[236:239], v[236:237], v[220:221], 0\n\t"
"v_mfma_f32_4x4x4_16b_bf16 v[232:235], v[232:233], v[220:221], 0\n\t"
"Lst25_%=:\n\t"
"v_mfma_f32_16x16x32_bf16 v[0:3], a[232:235], a[168:171], v[0:3]\n\t"
"v_cvt_pk_bf16_f32 v228, v228, v229\n\t"
"v_cvt_pk_bf16_f32 v229, v230, v231\n\t"
"v_mfma_f32_16x16x32_bf16 v[0:3], v[240:243], a[176:179], v[0:3]\n\t"
"v_cvt_pk_bf16_f32 v230, v224, v225\n\t"
"v_cvt_pk_bf16_f32 v231, v226, v227\n\t"
"v_mfma_f32_16x16x32_bf16 v[0:3], a[208:211], a[172:175], v[0:3]\n\t"
"ds_write_b128 v245, v[228:231] offset:26624\n\t"
"v_mfma_f32_16x16x32_bf16 v[4:7], a[232:235], a[172:175], v[4:7]\n\t"
"s_waitcnt lgkmcnt(6)\n\t"
"v_cvt_pk_bf16_f32 v236, v236, v237\n\t"
"v_cvt_pk_bf16_f32 v237, v238, v239\n\t"
"v_mfma_f32_16x16x32_bf16 v[4:7], a[208:211], a[176:179], v[4:7]\n\t"
"v_cvt_pk_bf16_f32 v238, v232, v233\n\t"
"v_cvt_pk_bf16_f32 v239, v234, v235\n\t"
"v_mfma_f32_16x16x32_bf16 v[4:7], v[240:243], a[180:183], v[4:7]\n\t"
"ds_write_b128 v245, v[236:239] offset:32768\n\t"
"v_mfma_f32_16x16x32_bf16 v[8:11], v[240:243], a[184:187], v[8:11]\n\t"
"s_waitcnt lgkmcnt(6)\n\t"
"v_mfma_f32_16x16x32_bf16 v[8:11], a[208:211], a[180:183], v[8:11]\n\t"
"v_mfma_f32_16x16x32_bf16 v[8:11], a[232:235], a[176:179], v[8:11]\n\t"
"ds_read_b64_tr_b16 v[224:225], v246 offset:40448\n\t"
"v_mfma_f32_16x16x32_bf16 v[12:15], a[232:235], a[180:183], v[12:15]\n\t"
"s_waitcnt lgkmcnt(6)\n\t"
"ds_read_b64_tr_b16 v[232:233], v255 offset:40448\n\t"
"v_mfma_f32_16x16x32_bf16 v[12:15], a[208:211], a[184:187], v[12:15]\n\t"
"ds_read_b64_tr_b16 v[228:229], v246 offset:46592\n\t"
"v_mfma_f32_16x16x32_bf16 v[12:15], v[240:243], a[188:191], v[12:15]\n\t"
"buffer_load_dwordx4 v[248:251], off, s[80:83], s20 offset:1024 sc1\n\t"
"v_mfma_f32_16x16x32_bf16 v[16:19], v[240:243], a[192:195], v[16:19]\n\t"
"s_waitcnt lgkmcnt(7)\n\t"
"ds_read_b64_tr_b16 v[236:237], v255 offset:46592\n\t"
"v_mfma_f32_16x16x32_bf16 v[16:19], a[208:211], a[188:191], v[16:19]\n\t"
"v_mfma_f32_16x16x32_bf16 v[16:19], a[232:235], a[184:187], v[16:19]\n\t"
"buffer_load_dwordx4 a[212:215], off, s[80:83], s20 offset:2048 sc1\n\t"
"v_mfma_f32_16x16x32_bf16 v[20:23], a[232:235], a[188:191], v[20:23]\n\t"
"s_waitcnt lgkmcnt(7)\n\t"
"s_mov_b32 s20, 663552\n\t"
"v_mfma_f32_16x16x32_bf16 v[20:23], a[208:211], a[192:195], v[20:23]\n\t"
"v_mfma_f32_16x16x32_bf16 v[20:23], v[240:243], a[196:199], v[20:23]\n\t"
"v_mfma_f32_16x16x32_bf16 v[24:27], v[240:243], a[200:203], v[24:27]\n\t"
"buffer_load_dwordx4 a[216:219], off, s[80:83], s20 offset:0 sc1\n\t"
"v_mfma_f32_16x16x32_bf16 v[24:27], a[208:211], a[196:199], v[24:27]\n\t"
"s_waitcnt lgkmcnt(6)\n\t"
"v_mfma_f32_16x16x32_bf16 v[24:27], a[232:235], a[192:195], v[24:27]\n\t"
"v_mfma_f32_16x16x32_bf16 v[28:31], a[232:235], a[196:199], v[28:31]\n\t"
"s_waitcnt lgkmcnt(3)\n\t"
"v_mfma_f32_16x16x32_bf16 v[28:31], a[208:211], a[200:203], v[28:31]\n\t"
"buffer_load_dwordx4 a[208:211], off, s[80:83], s20 offset:1024 sc1\n\t"
"v_mfma_f32_16x16x32_bf16 v[28:31], v[240:243], a[204:207], v[28:31]\n\t"
"s_waitcnt lgkmcnt(1)\n\t"
"v_mfma_f32_4x4x4_16b_bf16 v[224:227], v[224:225], v[220:221], 0\n\t"
"s_waitcnt lgkmcnt(0)\n\t"
"v_mfma_f32_4x4x4_16b_bf16 v[232:235], v[232:233], v[220:221], 0\n\t"
"s_waitcnt vmcnt(13)\n\t"
"v_mfma_f32_4x4x4_16b_bf16 v[228:231], v[228:229], v[220:221], 0\n\t"
"v_mfma_f32_4x4x4_16b_bf16 v[236:239], v[236:237], v[220:221], 0\n\t"
"v_mfma_f32_16x16x32_bf16 v[32:35], a[236:239], a[168:171], v[32:35]\n\t"
"buffer_load_dwordx4 v[240:243], off, s[80:83], s20 offset:2048 sc1\n\t"
"v_mfma_f32_16x16x32_bf16 v[32:35], a[244:247], a[176:179], v[32:35]\n\t"
"s_mov_b32 s20, 675840\n\t"
"v_cvt_pk_bf16_f32 v224, v224, v225\n\t"
"v_cvt_pk_bf16_f32 v225, v226, v227\n\t"
"v_mfma_f32_16x16x32_bf16 v[32:35], a[240:243], a[172:175], v[32:35]\n\t"
"v_cvt_pk_bf16_f32 v226, v232, v233\n\t"
"v_cvt_pk_bf16_f32 v227, v234, v235\n\t"
"v_mfma_f32_16x16x32_bf16 v[36:39], a[236:239], a[172:175], v[36:39]\n\t"
"v_cvt_pk_bf16_f32 v228, v228, v229\n\t"
"v_cvt_pk_bf16_f32 v229, v230, v231\n\t"
"v_mfma_f32_16x16x32_bf16 v[36:39], a[240:243], a[176:179], v[36:39]\n\t"
"v_cvt_pk_bf16_f32 v230, v236, v237\n\t"
"v_cvt_pk_bf16_f32 v231, v238, v239\n\t"
"v_mfma_f32_16x16x32_bf16 v[36:39], a[244:247], a[180:183], v[36:39]\n\t"
"buffer_load_dwordx4 a[220:223], off, s[80:83], s20 offset:0 sc1\n\t"
"v_mfma_f32_16x16x32_bf16 v[40:43], a[244:247], a[184:187], v[40:43]\n\t"
"buffer_load_dwordx4 a[224:227], off, s[80:83], s20 offset:1024 sc1\n\t"
"v_mfma_f32_16x16x32_bf16 v[40:43], a[240:243], a[180:183], v[40:43]\n\t"
"v_mfma_f32_16x16x32_bf16 v[40:43], a[236:239], a[176:179], v[40:43]\n\t"
"ds_write_b128 v245, v[224:227] offset:40448\n\t"
"v_mfma_f32_16x16x32_bf16 v[44:47], a[236:239], a[180:183], v[44:47]\n\t"
"v_mfma_f32_16x16x32_bf16 v[44:47], a[240:243], a[184:187], v[44:47]\n\t"
"buffer_load_dwordx4 a[228:231], off, s[80:83], s20 offset:2048 sc1\n\t"
"v_mfma_f32_16x16x32_bf16 v[44:47], a[244:247], a[188:191], v[44:47]\n\t"
"s_mov_b32 s20, 688128\n\t"
"ds_write_b128 v245, v[228:231] offset:46592\n\t"
"v_mfma_f32_16x16x32_bf16 v[48:51], a[244:247], a[192:195], v[48:51]\n\t"
"v_mfma_f32_16x16x32_bf16 v[48:51], a[240:243], a[188:191], v[48:51]\n\t"
"ds_read_b64_tr_b16 v[232:233], v246 offset:52736\n\t"
"v_mfma_f32_16x16x32_bf16 v[48:51], a[236:239], a[184:187], v[48:51]\n\t"
"ds_read_b64_tr_b16 v[236:237], v255 offset:52736\n\t"
"v_mfma_f32_16x16x32_bf16 v[52:55], a[236:239], a[188:191], v[52:55]\n\t"
"buffer_load_dwordx4 a[232:235], off, s[80:83], s20 offset:0 sc1\n\t"
"v_mfma_f32_16x16x32_bf16 v[52:55], a[240:243], a[192:195], v[52:55]\n\t"
"v_mfma_f32_16x16x32_bf16 v[52:55], a[244:247], a[196:199], v[52:55]\n\t"
"v_mfma_f32_16x16x32_bf16 v[56:59], a[244:247], a[200:203], v[56:59]\n\t"
"buffer_load_dwordx4 a[248:251], off, s[80:83], s20 offset:1024 sc1\n\t"
"v_mfma_f32_16x16x32_bf16 v[56:59], a[240:243], a[196:199], v[56:59]\n\t"
"v_mfma_f32_16x16x32_bf16 v[56:59], a[236:239], a[192:195], v[56:59]\n\t"
"v_mfma_f32_16x16x32_bf16 v[60:63], a[236:239], a[196:199], v[60:63]\n\t"
"s_waitcnt lgkmcnt(1)\n\t"
"v_mfma_f32_16x16x32_bf16 v[60:63], a[240:243], a[200:203], v[60:63]\n\t"
"buffer_load_dwordx4 a[236:239], off, s[80:83], s20 offset:2048 sc1\n\t"
"v_mfma_f32_16x16x32_bf16 v[60:63], a[244:247], a[204:207], v[60:63]\n\t"
"s_waitcnt vmcnt(9) lgkmcnt(0)\n\t"
"s_mov_b32 s20, 921600\n\t"
"v_mfma_f32_4x4x4_16b_bf16 v[232:235], v[232:233], v[220:221], 0\n\t"
"v_mfma_f32_4x4x4_16b_bf16 v[236:239], v[236:237], v[220:221], 0\n\t"
"v_mfma_f32_16x16x32_bf16 v[64:67], v[96:99], a[168:171], v[64:67]\n\t"
"buffer_load_dwordx4 a[240:243], off, s[80:83], s20 offset:0 sc1\n\t"
"v_mfma_f32_16x16x32_bf16 v[64:67], a[212:215], a[176:179], v[64:67]\n\t"
"ds_read_b128 a[168:171], v222 offset:1792\n\t"
"v_cvt_pk_bf16_f32 v232, v232, v233\n\t"
"v_mfma_f32_16x16x32_bf16 v[64:67], v[248:251], a[172:175], v[64:67]\n\t"
"v_cvt_pk_bf16_f32 v233, v234, v235\n\t"
"v_cvt_pk_bf16_f32 v234, v236, v237\n\t"
"v_mfma_f32_16x16x32_bf16 v[68:71], v[96:99], a[172:175], v[68:71]\n\t"
"ds_read_b128 a[172:175], v223 offset:7936\n\t"
"v_cvt_pk_bf16_f32 v235, v238, v239\n\t"
"v_mfma_f32_16x16x32_bf16 v[68:71], v[248:251], a[176:179], v[68:71]\n\t"
"v_mfma_f32_16x16x32_bf16 v[68:71], a[212:215], a[180:183], v[68:71]\n\t"
"buffer_load_dwordx4 a[244:247], off, s[80:83], s20 offset:1024 sc1\n\t"
"v_mfma_f32_16x16x32_bf16 v[72:75], a[212:215], a[184:187], v[72:75]\n\t"
"buffer_load_dwordx4 v[236:239], off, s[80:83], s20 offset:2048 sc1\n\t"
"v_mfma_f32_16x16x32_bf16 v[72:75], v[248:251], a[180:183], v[72:75]\n\t"
"s_mov_b32 s20, 933888\n\t"
"v_mfma_f32_16x16x32_bf16 v[72:75], v[96:99], a[176:179], v[72:75]\n\t"
"ds_write_b128 v245, v[232:235] offset:52736\n\t"
"v_mfma_f32_16x16x32_bf16 v[76:79], v[96:99], a[180:183], v[76:79]\n\t"
"buffer_load_dwordx4 a[252:255], off, s[80:83], s20 offset:0 sc1\n\t"
"v_mfma_f32_16x16x32_bf16 v[76:79], v[248:251], a[184:187], v[76:79]\n\t"
"ds_read_b128 a[176:179], v223 offset:14080\n\t"
"v_mfma_f32_16x16x32_bf16 v[76:79], a[212:215], a[188:191], v[76:79]\n\t"
"ds_read_b128 a[180:183], v222 offset:21760\n\t"
"v_mfma_f32_16x16x32_bf16 v[80:83], a[212:215], a[192:195], v[80:83]\n\t"
"v_mfma_f32_16x16x32_bf16 v[80:83], v[248:251], a[188:191], v[80:83]\n\t"
"v_mfma_f32_16x16x32_bf16 v[80:83], v[96:99], a[184:187], v[80:83]\n\t"
"ds_read_b128 a[184:187], v223 offset:27904\n\t"
"v_mfma_f32_16x16x32_bf16 v[84:87], v[96:99], a[188:191], v[84:87]\n\t"
"ds_read_b128 a[188:191], v223 offset:34048\n\t"
"v_mfma_f32_16x16x32_bf16 v[84:87], v[248:251], a[192:195], v[84:87]\n\t"
"v_mfma_f32_16x16x32_bf16 v[84:87], a[212:215], a[196:199], v[84:87]\n\t"
"v_mfma_f32_16x16x32_bf16 v[88:91], a[212:215], a[200:203], v[88:91]\n\t"
"v_mfma_f32_16x16x32_bf16 v[88:91], v[248:251], a[196:199], v[88:91]\n\t"
"v_mfma_f32_16x16x32_bf16 v[88:91], v[96:99], a[192:195], v[88:91]\n\t"
"ds_read_b128 a[192:195], v222 offset:41728\n\t"
"v_mfma_f32_16x16x32_bf16 v[92:95], v[96:99], a[196:199], v[92:95]\n\t"
"buffer_load_dwordx4 v[96:99], off, s[80:83], s20 offset:1024 sc1\n\t"
"v_mfma_f32_16x16x32_bf16 v[92:95], v[248:251], a[200:203], v[92:95]\n\t"
"s_waitcnt vmcnt(11) lgkmcnt(4)\n\t"
"ds_read_b128 a[196:199], v223 offset:47872\n\t"
"v_mfma_f32_16x16x32_bf16 v[92:95], a[212:215], a[204:207], v[92:95]\n\t"
"ds_read_b128 a[200:203], v223 offset:54016\n\t"
"v_mfma_f32_16x16x32_bf16 v[100:103], a[216:219], a[168:171], v[100:103]\n\t"
"s_waitcnt lgkmcnt(5)\n\t"
"ds_read_b128 a[204:207], v222 offset:61696\n\t"
"v_mfma_f32_16x16x32_bf16 v[100:103], v[240:243], a[176:179], v[100:103]\n\t"
"buffer_load_dwordx4 v[248:251], off, s[80:83], s20 offset:2048 sc1\n\t"
"v_mfma_f32_16x16x32_bf16 v[100:103], a[208:211], a[172:175], v[100:103]\n\t"
"s_mov_b32 s20, 946176\n\t"
"s_add_u32 m0, s24, 768\n\t"
"s_add_u32 s28, s21, 0\n\t"
"v_mfma_f32_16x16x32_bf16 v[104:107], a[216:219], a[172:175], v[104:107]\n\t"
"buffer_load_dwordx4 a[212:215], off, s[80:83], s20 offset:0 sc1\n\t"
"v_mfma_f32_16x16x32_bf16 v[104:107], a[208:211], a[176:179], v[104:107]\n\t"
"s_waitcnt lgkmcnt(5)\n\t"
"v_mfma_f32_16x16x32_bf16 v[104:107], v[240:243], a[180:183], v[104:107]\n\t"
"s_waitcnt lgkmcnt(0)\n\t"
"s_barrier\n\t"
"v_mfma_f32_16x16x32_bf16 v[108:111], v[240:243], a[184:187], v[108:111]\n\t"
"v_mfma_f32_16x16x32_bf16 v[108:111], a[208:211], a[180:183], v[108:111]\n\t"
"buffer_load_dwordx4 v247, s[12:15], s28 offen offset:256 nt lds\n\t"
"v_mfma_f32_16x16x32_bf16 v[108:111], a[216:219], a[176:179], v[108:111]\n\t"
"s_add_u32 m0, s24, 6912\n\t"
"s_add_u32 s28, s21, 196608\n\t"
"v_mfma_f32_16x16x32_bf16 v[112:115], a[216:219], a[180:183], v[112:115]\n\t"
"buffer_load_dwordx4 v247, s[12:15], s28 offen offset:256 nt lds\n\t"
"v_mfma_f32_16x16x32_bf16 v[112:115], a[208:211], a[184:187], v[112:115]\n\t"
"s_add_u32 m0, s24, 13056\n\t"
"s_add_u32 s28, s21, 393216\n\t"
"v_mfma_f32_16x16x32_bf16 v[112:115], v[240:243], a[188:191], v[112:115]\n\t"
"buffer_load_dwordx4 v247, s[12:15], s28 offen offset:256 nt lds\n\t"
"s_add_u32 m0, s24, 20736\n\t"
"s_add_u32 s28, s21, 589824\n\t"
"v_mfma_f32_16x16x32_bf16 v[116:119], v[240:243], a[192:195], v[116:119]\n\t"
"buffer_load_dwordx4 v247, s[12:15], s28 offen offset:256 nt lds\n\t"
"v_mfma_f32_16x16x32_bf16 v[116:119], a[208:211], a[188:191], v[116:119]\n\t"
"s_add_u32 m0, s24, 26880\n\t"
"s_add_u32 s28, s21, 786432\n\t"
"v_mfma_f32_16x16x32_bf16 v[116:119], a[216:219], a[184:187], v[116:119]\n\t"
"buffer_load_dwordx4 v247, s[12:15], s28 offen offset:256 nt lds\n\t"
"v_mfma_f32_16x16x32_bf16 v[120:123], a[216:219], a[188:191], v[120:123]\n\t"
"s_add_u32 m0, s24, 33024\n\t"
"s_add_u32 s28, s21, 983040\n\t"
"v_mfma_f32_16x16x32_bf16 v[120:123], a[208:211], a[192:195], v[120:123]\n\t"
"buffer_load_dwordx4 v247, s[12:15], s28 offen offset:256 nt lds\n\t"
"v_mfma_f32_16x16x32_bf16 v[120:123], v[240:243], a[196:199], v[120:123]\n\t"
"v_mfma_f32_16x16x32_bf16 v[124:127], v[240:243], a[200:203], v[124:127]\n\t"
"v_mfma_f32_16x16x32_bf16 v[124:127], a[208:211], a[196:199], v[124:127]\n\t"
"v_mfma_f32_16x16x32_bf16 v[124:127], a[216:219], a[192:195], v[124:127]\n\t"
"v_mfma_f32_16x16x32_bf16 v[128:131], a[216:219], a[196:199], v[128:131]\n\t"
"s_waitcnt vmcnt(16)\n\t"
"v_mfma_f32_16x16x32_bf16 v[128:131], a[208:211], a[200:203], v[128:131]\n\t"
"v_mfma_f32_16x16x32_bf16 v[128:131], v[240:243], a[204:207], v[128:131]\n\t"
"v_mfma_f32_16x16x32_bf16 v[132:135], a[220:223], a[168:171], v[132:135]\n\t"
"v_mfma_f32_16x16x32_bf16 v[132:135], a[228:231], a[176:179], v[132:135]\n\t"
"v_mfma_f32_16x16x32_bf16 v[132:135], a[224:227], a[172:175], v[132:135]\n\t"
"v_mfma_f32_16x16x32_bf16 v[136:139], a[220:223], a[172:175], v[136:139]\n\t"
"v_mfma_f32_16x16x32_bf16 v[136:139], a[224:227], a[176:179], v[136:139]\n\t"
"v_mfma_f32_16x16x32_bf16 v[136:139], a[228:231], a[180:183], v[136:139]\n\t"
"v_mfma_f32_16x16x32_bf16 v[140:143], a[228:231], a[184:187], v[140:143]\n\t"
"v_mfma_f32_16x16x32_bf16 v[140:143], a[224:227], a[180:183], v[140:143]\n\t"
"v_mfma_f32_16x16x32_bf16 v[140:143], a[220:223], a[176:179], v[140:143]\n\t"
"v_mfma_f32_16x16x32_bf16 v[144:147], a[220:223], a[180:183], v[144:147]\n\t"
"v_mfma_f32_16x16x32_bf16 v[144:147], a[224:227], a[184:187], v[144:147]\n\t"
"v_mfma_f32_16x16x32_bf16 v[144:147], a[228:231], a[188:191], v[144:147]\n\t"
"v_mfma_f32_16x16x32_bf16 v[148:151], a[228:231], a[192:195], v[148:151]\n\t"
"v_mfma_f32_16x16x32_bf16 v[148:151], a[224:227], a[188:191], v[148:151]\n\t"
"v_mfma_f32_16x16x32_bf16 v[148:151], a[220:223], a[184:187], v[148:151]\n\t"
"v_mfma_f32_16x16x32_bf16 v[152:155], a[220:223], a[188:191], v[152:155]\n\t"
"v_mfma_f32_16x16x32_bf16 v[152:155], a[224:227], a[192:195], v[152:155]\n\t"
"v_mfma_f32_16x16x32_bf16 v[152:155], a[228:231], a[196:199], v[152:155]\n\t"
"v_mfma_f32_16x16x32_bf16 v[156:159], a[228:231], a[200:203], v[156:159]\n\t"
"v_mfma_f32_16x16x32_bf16 v[156:159], a[224:227], a[196:199], v[156:159]\n\t"
"v_mfma_f32_16x16x32_bf16 v[156:159], a[220:223], a[192:195], v[156:159]\n\t"
"v_mfma_f32_16x16x32_bf16 v[160:163], a[220:223], a[196:199], v[160:163]\n\t"
"s_waitcnt vmcnt(13)\n\t"
"v_mfma_f32_16x16x32_bf16 v[160:163], a[224:227], a[200:203], v[160:163]\n\t"
"v_mfma_f32_16x16x32_bf16 v[160:163], a[228:231], a[204:207], v[160:163]\n\t"
"v_mfma_f32_16x16x32_bf16 v[164:167], a[232:235], a[168:171], v[164:167]\n\t"
"ds_read_b128 a[168:171], v222 offset:2048\n\t"
"v_mfma_f32_16x16x32_bf16 v[164:167], a[236:239], a[176:179], v[164:167]\n\t"
"v_mfma_f32_16x16x32_bf16 v[164:167], a[248:251], a[172:175], v[164:167]\n\t"
"buffer_load_dwordx4 v[240:243], off, s[80:83], s20 offset:1024 sc1\n\t"
"v_mfma_f32_16x16x32_bf16 v[168:171], a[232:235], a[172:175], v[168:171]\n\t"
"ds_read_b128 a[172:175], v223 offset:8192\n\t"
"v_mfma_f32_16x16x32_bf16 v[168:171], a[248:251], a[176:179], v[168:171]\n\t"
"v_mfma_f32_16x16x32_bf16 v[168:171], a[236:239], a[180:183], v[168:171]\n\t"
"v_mfma_f32_16x16x32_bf16 v[172:175], a[236:239], a[184:187], v[172:175]\n\t"
"v_mfma_f32_16x16x32_bf16 v[172:175], a[248:251], a[180:183], v[172:175]\n\t"
"v_mfma_f32_16x16x32_bf16 v[172:175], a[232:235], a[176:179], v[172:175]\n\t"
"ds_read_b128 a[176:179], v223 offset:14336\n\t"
"v_mfma_f32_16x16x32_bf16 v[176:179], a[232:235], a[180:183], v[176:179]\n\t"
"ds_read_b128 a[180:183], v222 offset:22016\n\t"
"v_mfma_f32_16x16x32_bf16 v[176:179], a[248:251], a[184:187], v[176:179]\n\t"
"v_mfma_f32_16x16x32_bf16 v[176:179], a[236:239], a[188:191], v[176:179]\n\t"
"v_mfma_f32_16x16x32_bf16 v[180:183], a[236:239], a[192:195], v[180:183]\n\t"
"v_mfma_f32_16x16x32_bf16 v[180:183], a[248:251], a[188:191], v[180:183]\n\t"
"v_mfma_f32_16x16x32_bf16 v[180:183], a[232:235], a[184:187], v[180:183]\n\t"
"ds_read_b128 a[184:187], v223 offset:28160\n\t"
"v_mfma_f32_16x16x32_bf16 v[184:187], a[232:235], a[188:191], v[184:187]\n\t"
"ds_read_b128 a[188:191], v223 offset:34304\n\t"
"v_mfma_f32_16x16x32_bf16 v[184:187], a[248:251], a[192:195], v[184:187]\n\t"
"v_mfma_f32_16x16x32_bf16 v[184:187], a[236:239], a[196:199], v[184:187]\n\t"
"v_mfma_f32_16x16x32_bf16 v[188:191], a[236:239], a[200:203], v[188:191]\n\t"
"v_mfma_f32_16x16x32_bf16 v[188:191], a[248:251], a[196:199], v[188:191]\n\t"
"v_mfma_f32_16x16x32_bf16 v[188:191], a[232:235], a[192:195], v[188:191]\n\t"
"ds_read_b128 a[192:195], v222 offset:41984\n\t"
"v_mfma_f32_16x16x32_bf16 v[192:195], a[232:235], a[196:199], v[192:195]\n\t"
"s_waitcnt vmcnt(11) lgkmcnt(4)\n\t"
"ds_read_b128 a[196:199], v223 offset:48128\n\t"
"v_mfma_f32_16x16x32_bf16 v[192:195], a[248:251], a[200:203], v[192:195]\n\t"
"ds_read_b128 a[200:203], v223 offset:54272\n\t"
"v_mfma_f32_16x16x32_bf16 v[192:195], a[236:239], a[204:207], v[192:195]\n\t"
"ds_read_b128 a[204:207], v222 offset:61952\n\t"
"Lst26_%=:\n\t"
"v_mfma_f32_16x16x32_bf16 v[0:3], a[240:243], a[168:171], v[0:3]\n\t"
"s_waitcnt lgkmcnt(6)\n\t"
"s_add_u32 s23, s40, 6291456\n\t"
"s_add_u32 s22, s22, 6291456\n\t"
"v_mfma_f32_16x16x32_bf16 v[0:3], v[236:239], a[176:179], v[0:3]\n\t"
"v_mfma_f32_16x16x32_bf16 v[0:3], a[244:247], a[172:175], v[0:3]\n\t"
"v_mfma_f32_16x16x32_bf16 v[4:7], a[240:243], a[172:175], v[4:7]\n\t"
"s_waitcnt lgkmcnt(5)\n\t"
"v_mfma_f32_16x16x32_bf16 v[4:7], a[244:247], a[176:179], v[4:7]\n\t"
"v_mfma_f32_16x16x32_bf16 v[4:7], v[236:239], a[180:183], v[4:7]\n\t"
"v_mfma_f32_16x16x32_bf16 v[8:11], v[236:239], a[184:187], v[8:11]\n\t"
"s_waitcnt lgkmcnt(4)\n\t"
"v_mfma_f32_16x16x32_bf16 v[8:11], a[244:247], a[180:183], v[8:11]\n\t"
"v_mfma_f32_16x16x32_bf16 v[8:11], a[240:243], a[176:179], v[8:11]\n\t"
"buffer_load_dwordx4 a[208:211], off, s[80:83], s20 offset:2048 sc1\n\t"
"v_mfma_f32_16x16x32_bf16 v[12:15], a[240:243], a[180:183], v[12:15]\n\t"
"s_waitcnt lgkmcnt(3)\n\t"
"s_mov_b32 s20, 958464\n\t"
"v_mfma_f32_16x16x32_bf16 v[12:15], a[244:247], a[184:187], v[12:15]\n\t"
"v_mfma_f32_16x16x32_bf16 v[12:15], v[236:239], a[188:191], v[12:15]\n\t"
"v_mfma_f32_16x16x32_bf16 v[16:19], v[236:239], a[192:195], v[16:19]\n\t"
"buffer_load_dwordx4 a[216:219], off, s[80:83], s20 offset:0 sc1\n\t"
"v_mfma_f32_16x16x32_bf16 v[16:19], a[244:247], a[188:191], v[16:19]\n\t"
"s_waitcnt lgkmcnt(2)\n\t"
"v_mfma_f32_16x16x32_bf16 v[16:19], a[240:243], a[184:187], v[16:19]\n\t"
"v_mfma_f32_16x16x32_bf16 v[20:23], a[240:243], a[188:191], v[20:23]\n\t"
"s_waitcnt lgkmcnt(1)\n\t"
"v_mfma_f32_16x16x32_bf16 v[20:23], a[244:247], a[192:195], v[20:23]\n\t"
"buffer_load_dwordx4 a[220:223], off, s[80:83], s20 offset:1024 sc1\n\t"
"v_mfma_f32_16x16x32_bf16 v[20:23], v[236:239], a[196:199], v[20:23]\n\t"
"v_mfma_f32_16x16x32_bf16 v[24:27], v[236:239], a[200:203], v[24:27]\n\t"
"s_waitcnt lgkmcnt(0)\n\t"
"v_mfma_f32_16x16x32_bf16 v[24:27], a[244:247], a[196:199], v[24:27]\n\t"
"v_mfma_f32_16x16x32_bf16 v[24:27], a[240:243], a[192:195], v[24:27]\n\t"
"buffer_load_dwordx4 a[224:227], off, s[80:83], s20 offset:2048 sc1\n\t"
"v_mfma_f32_16x16x32_bf16 v[28:31], a[240:243], a[196:199], v[28:31]\n\t"
"s_waitcnt vmcnt(12)\n\t"
"s_mov_b32 s20, 970752\n\t"
"v_mfma_f32_16x16x32_bf16 v[28:31], a[244:247], a[200:203], v[28:31]\n\t"
"v_mfma_f32_16x16x32_bf16 v[28:31], v[236:239], a[204:207], v[28:31]\n\t"
"v_mfma_f32_16x16x32_bf16 v[32:35], a[252:255], a[168:171], v[32:35]\n\t"
"buffer_load_dwordx4 a[228:231], off, s[80:83], s20 offset:0 sc1\n\t"
"v_mfma_f32_16x16x32_bf16 v[32:35], v[248:251], a[176:179], v[32:35]\n\t"
"v_mfma_f32_16x16x32_bf16 v[32:35], v[96:99], a[172:175], v[32:35]\n\t"
"v_mfma_f32_16x16x32_bf16 v[36:39], a[252:255], a[172:175], v[36:39]\n\t"
"v_mfma_f32_16x16x32_bf16 v[36:39], v[96:99], a[176:179], v[36:39]\n\t"
"buffer_load_dwordx4 a[232:235], off, s[80:83], s20 offset:1024 sc1\n\t"
"v_mfma_f32_16x16x32_bf16 v[36:39], v[248:251], a[180:183], v[36:39]\n\t"
"v_mfma_f32_16x16x32_bf16 v[40:43], v[248:251], a[184:187], v[40:43]\n\t"
"v_mfma_f32_16x16x32_bf16 v[40:43], v[96:99], a[180:183], v[40:43]\n\t"
"v_mfma_f32_16x16x32_bf16 v[40:43], a[252:255], a[176:179], v[40:43]\n\t"
"buffer_load_dwordx4 a[236:239], off, s[80:83], s20 offset:2048 sc1\n\t"
"v_mfma_f32_16x16x32_bf16 v[44:47], a[252:255], a[180:183], v[44:47]\n\t"
"s_mov_b32 s20, 983040\n\t"
"v_mfma_f32_16x16x32_bf16 v[44:47], v[96:99], a[184:187], v[44:47]\n\t"
"v_mfma_f32_16x16x32_bf16 v[44:47], v[248:251], a[188:191], v[44:47]\n\t"
"v_mfma_f32_16x16x32_bf16 v[48:51], v[248:251], a[192:195], v[48:51]\n\t"
"buffer_load_dwordx4 a[240:243], off, s[80:83], s20 offset:0 sc1\n\t"
"v_mfma_f32_16x16x32_bf16 v[48:51], v[96:99], a[188:191], v[48:51]\n\t"
"v_mfma_f32_16x16x32_bf16 v[48:51], a[252:255], a[184:187], v[48:51]\n\t"
"s_waitcnt vmcnt(7)\n\t"
"v_mfma_f32_16x16x32_bf16 v[52:55], a[252:255], a[188:191], v[52:55]\n\t"
"v_mfma_f32_16x16x32_bf16 v[52:55], v[96:99], a[192:195], v[52:55]\n\t"
"buffer_load_dwordx4 a[244:247], off, s[80:83], s20 offset:1024 sc1\n\t"
"v_mfma_f32_16x16x32_bf16 v[52:55], v[248:251], a[196:199], v[52:55]\n\t"
"Lpg19_%=:\n\t"
"s_cbranch_vccnz Lvr2_%=\n\t"
"v_mfma_f32_16x16x32_bf16 v[56:59], v[248:251], a[200:203], v[56:59]\n\t"
"v_mfma_f32_16x16x32_bf16 v[56:59], v[96:99], a[196:199], v[56:59]\n\t"
"v_mfma_f32_16x16x32_bf16 v[56:59], a[252:255], a[192:195], v[56:59]\n\t"
"buffer_load_dwordx4 a[248:251], off, s[80:83], s20 offset:2048 sc1\n\t"
"v_mfma_f32_16x16x32_bf16 v[60:63], a[252:255], a[196:199], v[60:63]\n\t"
"s_mov_b32 s20, 995328\n\t"
"v_mfma_f32_16x16x32_bf16 v[60:63], v[96:99], a[200:203], v[60:63]\n\t"
"v_mfma_f32_16x16x32_bf16 v[60:63], v[248:251], a[204:207], v[60:63]\n\t"
"v_mfma_f32_16x16x32_bf16 v[64:67], a[212:215], a[168:171], v[64:67]\n\t"
"buffer_load_dwordx4 v[96:99], off, s[80:83], s20 offset:0 sc1\n\t"
"v_mfma_f32_16x16x32_bf16 v[64:67], a[208:211], a[176:179], v[64:67]\n\t"
"ds_read_b128 a[168:171], v222 offset:2304\n\t"
"v_mfma_f32_16x16x32_bf16 v[64:67], v[240:243], a[172:175], v[64:67]\n\t"
"v_mfma_f32_16x16x32_bf16 v[68:71], a[212:215], a[172:175], v[68:71]\n\t"
"buffer_load_dwordx4 v[248:251], off, s[80:83], s20 offset:1024 sc1\n\t"
"v_mfma_f32_16x16x32_bf16 v[68:71], v[240:243], a[176:179], v[68:71]\n\t"
"ds_read_b128 a[172:175], v223 offset:8448\n\t"
"v_mfma_f32_16x16x32_bf16 v[68:71], a[208:211], a[180:183], v[68:71]\n\t"
"v_mfma_f32_16x16x32_bf16 v[72:75], a[208:211], a[184:187], v[72:75]\n\t"
"buffer_load_dwordx4 a[252:255], off, s[80:83], s20 offset:2048 sc1\n\t"
"v_mfma_f32_16x16x32_bf16 v[72:75], v[240:243], a[180:183], v[72:75]\n\t"
"v_mfma_f32_16x16x32_bf16 v[72:75], a[212:215], a[176:179], v[72:75]\n\t"
"ds_read_b128 a[176:179], v223 offset:14592\n\t"
"v_mfma_f32_16x16x32_bf16 v[76:79], a[212:215], a[180:183], v[76:79]\n\t"
"ds_read_b128 a[180:183], v222 offset:22272\n\t"
"v_mfma_f32_16x16x32_bf16 v[76:79], v[240:243], a[184:187], v[76:79]\n\t"
"v_mfma_f32_16x16x32_bf16 v[76:79], a[208:211], a[188:191], v[76:79]\n\t"
"v_mfma_f32_16x16x32_bf16 v[80:83], a[208:211], a[192:195], v[80:83]\n\t"
"v_mfma_f32_16x16x32_bf16 v[80:83], v[240:243], a[188:191], v[80:83]\n\t"
"v_mfma_f32_16x16x32_bf16 v[80:83], a[212:215], a[184:187], v[80:83]\n\t"
"ds_read_b128 a[184:187], v223 offset:28416\n\t"
"v_mfma_f32_16x16x32_bf16 v[84:87], a[212:215], a[188:191], v[84:87]\n\t"
"s_mov_b32 s20, 1007616\n\t"
"ds_read_b128 a[188:191], v223 offset:34560\n\t"
"v_mfma_f32_16x16x32_bf16 v[84:87], v[240:243], a[192:195], v[84:87]\n\t"
"v_mfma_f32_16x16x32_bf16 v[84:87], a[208:211], a[196:199], v[84:87]\n\t"
"v_mfma_f32_16x16x32_bf16 v[88:91], a[208:211], a[200:203], v[88:91]\n\t"
"v_mfma_f32_16x16x32_bf16 v[88:91], v[240:243], a[196:199], v[88:91]\n\t"
"v_mfma_f32_16x16x32_bf16 v[88:91], a[212:215], a[192:195], v[88:91]\n\t"
"ds_read_b128 a[192:195], v222 offset:42240\n\t"
"v_mfma_f32_16x16x32_bf16 v[92:95], a[212:215], a[196:199], v[92:95]\n\t"
"buffer_load_dwordx4 a[212:215], off, s[80:83], s20 offset:0 sc1\n\t"
"v_mfma_f32_16x16x32_bf16 v[92:95], v[240:243], a[200:203], v[92:95]\n\t"
"s_waitcnt vmcnt(10) lgkmcnt(4)\n\t"
"ds_read_b128 a[196:199], v223 offset:48384\n\t"
"v_mfma_f32_16x16x32_bf16 v[92:95], a[208:211], a[204:207], v[92:95]\n\t"
"global_load_dwordx4 v[240:243], v252, s[48:49] offset:2048\n\t"
"v_mfma_f32_16x16x32_bf16 v[100:103], a[216:219], a[168:171], v[100:103]\n\t"
"s_waitcnt lgkmcnt(4)\n\t"
"ds_read_b128 a[200:203], v223 offset:54528\n\t"
"v_mfma_f32_16x16x32_bf16 v[100:103], a[224:227], a[176:179], v[100:103]\n\t"
"ds_read_b128 a[204:207], v222 offset:62208\n\t"
"v_mfma_f32_16x16x32_bf16 v[100:103], a[220:223], a[172:175], v[100:103]\n\t"
"v_mfma_f32_16x16x32_bf16 v[104:107], a[216:219], a[172:175], v[104:107]\n\t"
"s_waitcnt lgkmcnt(5)\n\t"
"v_mfma_f32_16x16x32_bf16 v[104:107], a[220:223], a[176:179], v[104:107]\n\t"
"global_load_dwordx4 a[208:211], v252, s[46:47] offset:1024\n\t"
"v_mfma_f32_16x16x32_bf16 v[104:107], a[224:227], a[180:183], v[104:107]\n\t"
"v_mfma_f32_16x16x32_bf16 v[108:111], a[224:227], a[184:187], v[108:111]\n\t"
"s_waitcnt lgkmcnt(4)\n\t"
"v_mfma_f32_16x16x32_bf16 v[108:111], a[220:223], a[180:183], v[108:111]\n\t"
"v_mfma_f32_16x16x32_bf16 v[108:111], a[216:219], a[176:179], v[108:111]\n\t"
"v_mfma_f32_16x16x32_bf16 v[112:115], a[216:219], a[180:183], v[112:115]\n\t"
"s_waitcnt lgkmcnt(3)\n\t"
"v_mfma_f32_16x16x32_bf16 v[112:115], a[220:223], a[184:187], v[112:115]\n\t"
"v_mfma_f32_16x16x32_bf16 v[112:115], a[224:227], a[188:191], v[112:115]\n\t"
"v_mfma_f32_16x16x32_bf16 v[116:119], a[224:227], a[192:195], v[116:119]\n\t"
"s_waitcnt lgkmcnt(2)\n\t"
"v_mfma_f32_16x16x32_bf16 v[116:119], a[220:223], a[188:191], v[116:119]\n\t"
"v_mfma_f32_16x16x32_bf16 v[116:119], a[216:219], a[184:187], v[116:119]\n\t"
"v_mfma_f32_16x16x32_bf16 v[120:123], a[216:219], a[188:191], v[120:123]\n\t"
"s_waitcnt lgkmcnt(1)\n\t"
"v_mfma_f32_16x16x32_bf16 v[120:123], a[220:223], a[192:195], v[120:123]\n\t"
"v_mfma_f32_16x16x32_bf16 v[120:123], a[224:227], a[196:199], v[120:123]\n\t"
"v_mfma_f32_16x16x32_bf16 v[124:127], a[224:227], a[200:203], v[124:127]\n\t"
"s_waitcnt lgkmcnt(0)\n\t"
"v_mfma_f32_16x16x32_bf16 v[124:127], a[220:223], a[196:199], v[124:127]\n\t"
"v_mfma_f32_16x16x32_bf16 v[124:127], a[216:219], a[192:195], v[124:127]\n\t"
"v_mfma_f32_16x16x32_bf16 v[128:131], a[216:219], a[196:199], v[128:131]\n\t"
"buffer_load_dwordx4 a[216:219], off, s[80:83], s20 offset:1024 sc1\n\t"
"v_mfma_f32_16x16x32_bf16 v[128:131], a[220:223], a[200:203], v[128:131]\n\t"
"s_waitcnt vmcnt(10)\n\t"
"v_mfma_f32_16x16x32_bf16 v[128:131], a[224:227], a[204:207], v[128:131]\n\t"
"s_add_u32 m0, s24, 42240\n\t"
"s_add_u32 s28, s21, 1182720\n\t"
"v_mfma_f32_16x16x32_bf16 v[132:135], a[228:231], a[168:171], v[132:135]\n\t"
"buffer_load_dwordx4 a[220:223], off, s[80:83], s20 offset:2048 sc1\n\t"
"v_mfma_f32_16x16x32_bf16 v[132:135], a[236:239], a[176:179], v[132:135]\n\t"
"v_mfma_f32_16x16x32_bf16 v[132:135], a[232:235], a[172:175], v[132:135]\n\t"
"v_mfma_f32_16x16x32_bf16 v[136:139], a[228:231], a[172:175], v[136:139]\n\t"
"buffer_load_dwordx4 v247, s[12:15], s28 offen offset:256 nt lds\n\t"
"v_mfma_f32_16x16x32_bf16 v[136:139], a[232:235], a[176:179], v[136:139]\n\t"
"s_add_u32 m0, s24, 48384\n\t"
"s_add_u32 s28, s21, 1379328\n\t"
"v_mfma_f32_16x16x32_bf16 v[136:139], a[236:239], a[180:183], v[136:139]\n\t"
"s_barrier\n\t"
"v_mfma_f32_16x16x32_bf16 v[140:143], a[236:239], a[184:187], v[140:143]\n\t"
"v_mfma_f32_16x16x32_bf16 v[140:143], a[232:235], a[180:183], v[140:143]\n\t"
"buffer_load_dwordx4 v247, s[12:15], s28 offen offset:256 nt lds\n\t"
"v_mfma_f32_16x16x32_bf16 v[140:143], a[228:231], a[176:179], v[140:143]\n\t"
"s_add_u32 m0, s24, 54528\n\t"
"s_add_u32 s28, s21, 1575936\n\t"
"s_waitcnt vmcnt(5)\n\t"
"ds_read_b64_tr_b16 v[224:225], v240 offset:1024\n\t"
"v_mfma_f32_16x16x32_bf16 v[144:147], a[228:231], a[180:183], v[144:147]\n\t"
"buffer_load_dwordx4 v247, s[12:15], s28 offen offset:256 nt lds\n\t"
"v_mfma_f32_16x16x32_bf16 v[144:147], a[232:235], a[184:187], v[144:147]\n\t"
"s_add_u32 m0, s24, 40704\n\t"
"s_add_u32 s28, s21, 1179648\n\t"
"ds_read_b64_tr_b16 v[226:227], v240 offset:2560\n\t"
"v_mfma_f32_16x16x32_bf16 v[144:147], a[236:239], a[188:191], v[144:147]\n\t"
"buffer_load_dwordx4 v247, s[12:15], s28 offen offset:256 nt lds\n\t"
"s_add_u32 m0, s24, 46848\n\t"
"s_add_u32 s28, s21, 1376256\n\t"
"v_mfma_f32_16x16x32_bf16 v[148:151], a[236:239], a[192:195], v[148:151]\n\t"
"buffer_load_dwordx4 v247, s[12:15], s28 offen offset:256 nt lds\n\t"
"v_mfma_f32_16x16x32_bf16 v[148:151], a[232:235], a[188:191], v[148:151]\n\t"
"s_add_u32 m0, s24, 52992\n\t"
"s_add_u32 s28, s21, 1572864\n\t"
"ds_read_b64_tr_b16 v[228:229], v241 offset:1024\n\t"
"v_mfma_f32_16x16x32_bf16 v[148:151], a[228:231], a[184:187], v[148:151]\n\t"
"buffer_load_dwordx4 v247, s[12:15], s28 offen offset:256 nt lds\n\t"
"v_mfma_f32_16x16x32_bf16 v[152:155], a[228:231], a[188:191], v[152:155]\n\t"
"ds_read_b64_tr_b16 v[230:231], v241 offset:2560\n\t"
"v_mfma_f32_16x16x32_bf16 v[152:155], a[232:235], a[192:195], v[152:155]\n\t"
"ds_read_b64_tr_b16 v[236:237], v240 offset:20992\n\t"
"v_mfma_f32_16x16x32_bf16 v[152:155], a[236:239], a[196:199], v[152:155]\n\t"
"ds_read_b64_tr_b16 v[238:239], v240 offset:22528\n\t"
"v_mfma_f32_16x16x32_bf16 v[156:159], a[236:239], a[200:203], v[156:159]\n\t"
"ds_read_b64_tr_b16 v[232:233], v241 offset:20992\n\t"
"v_mfma_f32_16x16x32_bf16 v[156:159], a[232:235], a[196:199], v[156:159]\n\t"
"ds_read_b64_tr_b16 v[234:235], v241 offset:22528\n\t"
"v_mfma_f32_16x16x32_bf16 v[156:159], a[228:231], a[192:195], v[156:159]\n\t"
"v_mfma_f32_16x16x32_bf16 v[160:163], a[228:231], a[196:199], v[160:163]\n\t"
"s_waitcnt vmcnt(8) lgkmcnt(6)\n\t"
"v_mfma_f32_16x16x32_bf16 v[160:163], a[232:235], a[200:203], v[160:163]\n\t"
"s_waitcnt lgkmcnt(4)\n\t"
"v_mfma_f32_16x16x32_bf16 v[160:163], a[236:239], a[204:207], v[160:163]\n\t"
"s_waitcnt lgkmcnt(2)\n\t"
"v_mfma_f32_16x16x32_bf16 v[224:227], v[224:227], a[208:211], 0\n\t"
"s_waitcnt lgkmcnt(0)\n\t"
"v_mfma_f32_16x16x32_bf16 v[228:231], v[228:231], a[208:211], 0\n\t"
"v_mfma_f32_16x16x32_bf16 v[236:239], v[236:239], a[208:211], 0\n\t"
"v_mfma_f32_16x16x32_bf16 v[232:235], v[232:235], a[208:211], 0\n\t"
"v_mfma_f32_16x16x32_bf16 v[164:167], a[240:243], a[168:171], v[164:167]\n\t"
"ds_read_b128 a[168:171], v222 offset:512\n\t"
"v_mfma_f32_16x16x32_bf16 v[164:167], a[248:251], a[176:179], v[164:167]\n\t"
"v_cvt_pk_bf16_f32 v224, v224, v225\n\t"
"v_cvt_pk_bf16_f32 v225, v226, v227\n\t"
"v_mfma_f32_16x16x32_bf16 v[164:167], a[244:247], a[172:175], v[164:167]\n\t"
"v_cvt_pk_bf16_f32 v226, v228, v229\n\t"
"v_cvt_pk_bf16_f32 v227, v230, v231\n\t"
"v_mfma_f32_16x16x32_bf16 v[168:171], a[240:243], a[172:175], v[168:171]\n\t"
"ds_read_b128 a[172:175], v223 offset:6656\n\t"
"v_cvt_pk_bf16_f32 v236, v236, v237\n\t"
"v_mfma_f32_16x16x32_bf16 v[168:171], a[244:247], a[176:179], v[168:171]\n\t"
"v_cvt_pk_bf16_f32 v237, v238, v239\n\t"
"v_cvt_pk_bf16_f32 v238, v232, v233\n\t"
"v_mfma_f32_16x16x32_bf16 v[168:171], a[248:251], a[180:183], v[168:171]\n\t"
"ds_write_b128 v243, v[224:227] offset:2048\n\t"
"v_mfma_f32_16x16x32_bf16 v[172:175], a[248:251], a[184:187], v[172:175]\n\t"
"s_mov_b32 s20, 1019904\n\t"
"v_cvt_pk_bf16_f32 v239, v234, v235\n\t"
"v_add_u32 v240, 59904, v240\n\t"
"v_mfma_f32_16x16x32_bf16 v[172:175], a[244:247], a[180:183], v[172:175]\n\t"
"v_add_u32 v241, 59904, v241\n\t"
"v_mfma_f32_16x16x32_bf16 v[172:175], a[240:243], a[176:179], v[172:175]\n\t"
"ds_write_b128 v243, v[236:239] offset:22016\n\t"
"v_mfma_f32_16x16x32_bf16 v[176:179], a[240:243], a[180:183], v[176:179]\n\t"
"v_add_u32 v243, 59904, v243\n\t"
"v_mfma_f32_16x16x32_bf16 v[176:179], a[244:247], a[184:187], v[176:179]\n\t"
"ds_read_b128 a[176:179], v223 offset:12800\n\t"
"v_mfma_f32_16x16x32_bf16 v[176:179], a[248:251], a[188:191], v[176:179]\n\t"
"ds_read_b64_tr_b16 v[228:229], v240 offset:1024\n\t"
"v_mfma_f32_16x16x32_bf16 v[180:183], a[248:251], a[192:195], v[180:183]\n\t"
"buffer_load_dwordx4 a[224:227], off, s[80:83], s20 offset:0 sc1\n\t"
"v_mfma_f32_16x16x32_bf16 v[180:183], a[244:247], a[188:191], v[180:183]\n\t"
"ds_read_b64_tr_b16 v[230:231], v240 offset:2560\n\t"
"v_mfma_f32_16x16x32_bf16 v[180:183], a[240:243], a[184:187], v[180:183]\n\t"
"ds_read_b64_tr_b16 v[232:233], v241 offset:1024\n\t"
"v_mfma_f32_16x16x32_bf16 v[184:187], a[240:243], a[188:191], v[184:187]\n\t"
"ds_read_b64_tr_b16 v[234:235], v241 offset:2560\n\t"
"v_mfma_f32_16x16x32_bf16 v[184:187], a[244:247], a[192:195], v[184:187]\n\t"
"ds_read_b128 a[180:183], v222 offset:20480\n\t"
"v_mfma_f32_16x16x32_bf16 v[184:187], a[248:251], a[196:199], v[184:187]\n\t"
"ds_read_b64_tr_b16 v[224:225], v240 offset:20992\n\t"
"v_mfma_f32_16x16x32_bf16 v[188:191], a[248:251], a[200:203], v[188:191]\n\t"
"ds_read_b64_tr_b16 v[226:227], v240 offset:22528\n\t"
"v_mfma_f32_16x16x32_bf16 v[188:191], a[244:247], a[196:199], v[188:191]\n\t"
"ds_read_b64_tr_b16 v[236:237], v241 offset:20992\n\t"
"v_mfma_f32_16x16x32_bf16 v[188:191], a[240:243], a[192:195], v[188:191]\n\t"
"ds_read_b64_tr_b16 v[238:239], v241 offset:22528\n\t"
"v_mfma_f32_16x16x32_bf16 v[192:195], a[240:243], a[196:199], v[192:195]\n\t"
"s_waitcnt lgkmcnt(7)\n\t"
"ds_read_b128 a[184:187], v223 offset:26624\n\t"
"v_mfma_f32_16x16x32_bf16 v[192:195], a[244:247], a[200:203], v[192:195]\n\t"
"s_waitcnt lgkmcnt(6)\n\t"
"ds_read_b128 a[188:191], v223 offset:32768\n\t"
"v_mfma_f32_16x16x32_bf16 v[192:195], a[248:251], a[204:207], v[192:195]\n\t"
"s_waitcnt lgkmcnt(4)\n\t"
"ds_read_b128 a[192:195], v222 offset:40448\n\t"
"v_mfma_f32_16x16x32_bf16 v[228:231], v[228:231], a[208:211], 0\n\t"
"ds_read_b128 a[196:199], v223 offset:46592\n\t"
"v_mfma_f32_16x16x32_bf16 v[232:235], v[232:235], a[208:211], 0\n\t"
"s_waitcnt lgkmcnt(4)\n\t"
"ds_read_b128 a[200:203], v223 offset:52736\n\t"
"v_mfma_f32_16x16x32_bf16 v[224:227], v[224:227], a[208:211], 0\n\t"
"ds_read_b128 a[204:207], v222 offset:60416\n\t"
"v_mfma_f32_16x16x32_bf16 v[236:239], v[236:239], a[208:211], 0\n\t"
"Lst27_%=:\n\t"
"v_mfma_f32_16x16x32_bf16 v[196:199], v[96:99], a[168:171], v[196:199]\n\t"
"s_mov_b64 vcc, -1\n\t"
"s_bitcmp1_b32 s73, 1\n\t"
"s_cbranch_scc1 Lvh4_%=\n\t"
"s_bitcmp1_b32 s73, 0\n\t"
"s_cbranch_scc1 Lvr4_%=\n\t"
"s_branch Lpg25_%=\n\t"
"Lvh4_%=:\n\t"
"s_bitcmp1_b32 s73, 0\n\t"
"s_cbranch_scc1 Lvr4_%=\n\t"
"s_branch Lvr4_%=\n\t"
"Lvr4_%=:\n\t"
"s_mov_b64 vcc, 0\n\t"
"v_mfma_f32_16x16x32_bf16 v[196:199], a[252:255], a[176:179], v[196:199]\n\t"
"v_cvt_pk_bf16_f32 v228, v228, v229\n\t"
"v_cvt_pk_bf16_f32 v229, v230, v231\n\t"
"v_mfma_f32_16x16x32_bf16 v[196:199], v[248:251], a[172:175], v[196:199]\n\t"
"v_cvt_pk_bf16_f32 v230, v232, v233\n\t"
"v_cvt_pk_bf16_f32 v231, v234, v235\n\t"
"v_mfma_f32_16x16x32_bf16 v[204:207], v[96:99], a[172:175], v[204:207]\n\t"
"s_waitcnt lgkmcnt(5)\n\t"
"v_cvt_pk_bf16_f32 v224, v224, v225\n\t"
"v_cvt_pk_bf16_f32 v225, v226, v227\n\t"
"v_mfma_f32_16x16x32_bf16 v[204:207], v[248:251], a[176:179], v[204:207]\n\t"
"v_cvt_pk_bf16_f32 v226, v236, v237\n\t"
"v_cvt_pk_bf16_f32 v227, v238, v239\n\t"
"v_mfma_f32_16x16x32_bf16 v[204:207], a[252:255], a[180:183], v[204:207]\n\t"
"ds_write_b128 v243, v[228:231] offset:2048\n\t"
"v_mfma_f32_16x16x32_bf16 v[212:215], a[252:255], a[184:187], v[212:215]\n\t"
"s_waitcnt lgkmcnt(5)\n\t"
"v_mfma_f32_16x16x32_bf16 v[212:215], v[248:251], a[180:183], v[212:215]\n\t"
"v_mfma_f32_16x16x32_bf16 v[212:215], v[96:99], a[176:179], v[212:215]\n\t"
"ds_write_b128 v243, v[224:227] offset:22016\n\t"
"v_mfma_f32_16x16x32_bf16 a[0:3], v[96:99], a[180:183], a[0:3]\n\t"
"s_waitcnt lgkmcnt(5)\n\t"
"v_mfma_f32_16x16x32_bf16 a[0:3], v[248:251], a[184:187], a[0:3]\n\t"
"v_mfma_f32_16x16x32_bf16 a[0:3], a[252:255], a[188:191], a[0:3]\n\t"
"s_barrier\n\t"
"v_mfma_f32_16x16x32_bf16 a[8:11], a[252:255], a[192:195], a[8:11]\n\t"
"s_waitcnt lgkmcnt(4)\n\t"
"ds_read_b64_tr_b16 v[232:233], v246 offset:1024\n\t"
"v_mfma_f32_16x16x32_bf16 a[8:11], v[248:251], a[188:191], a[8:11]\n\t"
"ds_read_b64_tr_b16 v[236:237], v255 offset:1024\n\t"
"v_mfma_f32_16x16x32_bf16 a[8:11], v[96:99], a[184:187], a[8:11]\n\t"
"ds_read_b64_tr_b16 v[228:229], v246 offset:7168\n\t"
"v_mfma_f32_16x16x32_bf16 a[16:19], v[96:99], a[188:191], a[16:19]\n\t"
"s_waitcnt lgkmcnt(6)\n\t"
"ds_read_b64_tr_b16 v[224:225], v255 offset:7168\n\t"
"v_mfma_f32_16x16x32_bf16 a[16:19], v[248:251], a[192:195], a[16:19]\n\t"
"buffer_load_dwordx4 a[228:231], off, s[80:83], s20 offset:1024 sc1\n\t"
"v_mfma_f32_16x16x32_bf16 a[16:19], a[252:255], a[196:199], a[16:19]\n\t"
"v_mfma_f32_16x16x32_bf16 a[24:27], a[252:255], a[200:203], a[24:27]\n\t"
"s_waitcnt lgkmcnt(6)\n\t"
"v_mfma_f32_16x16x32_bf16 a[24:27], v[248:251], a[196:199], a[24:27]\n\t"
"buffer_load_dwordx4 a[232:235], off, s[80:83], s20 offset:2048 sc1\n\t"
"v_mfma_f32_16x16x32_bf16 a[24:27], v[96:99], a[192:195], a[24:27]\n\t"
"s_mov_b32 s20, 1032192\n\t"
"v_mfma_f32_16x16x32_bf16 a[32:35], v[96:99], a[196:199], a[32:35]\n\t"
"s_waitcnt lgkmcnt(3)\n\t"
"v_mfma_f32_16x16x32_bf16 a[32:35], v[248:251], a[200:203], a[32:35]\n\t"
"s_waitcnt lgkmcnt(2)\n\t"
"v_mfma_f32_16x16x32_bf16 a[32:35], a[252:255], a[204:207], a[32:35]\n\t"
"buffer_load_dwordx4 v[96:99], off, s[80:83], s20 offset:0 sc1\n\t"
"v_mfma_f32_4x4x4_16b_bf16 v[232:235], v[232:233], v[220:221], 0\n\t"
"s_waitcnt lgkmcnt(0)\n\t"
"v_mfma_f32_4x4x4_16b_bf16 v[236:239], v[236:237], v[220:221], 0\n\t"
"s_waitcnt vmcnt(10)\n\t"
"v_mfma_f32_4x4x4_16b_bf16 v[228:231], v[228:229], v[220:221], 0\n\t"
"v_mfma_f32_4x4x4_16b_bf16 v[224:227], v[224:225], v[220:221], 0\n\t"
"v_mfma_f32_16x16x32_bf16 a[40:43], a[212:215], a[168:171], a[40:43]\n\t"
"buffer_load_dwordx4 v[248:251], off, s[80:83], s20 offset:1024 sc1\n\t"
"v_mfma_f32_16x16x32_bf16 a[40:43], a[220:223], a[176:179], a[40:43]\n\t"
"v_cvt_pk_bf16_f32 v232, v232, v233\n\t"
"v_cvt_pk_bf16_f32 v233, v234, v235\n\t"
"v_mfma_f32_16x16x32_bf16 a[40:43], a[216:219], a[172:175], a[40:43]\n\t"
"v_cvt_pk_bf16_f32 v234, v236, v237\n\t"
"v_cvt_pk_bf16_f32 v235, v238, v239\n\t"
"v_mfma_f32_16x16x32_bf16 a[48:51], a[212:215], a[172:175], a[48:51]\n\t"
"ds_read_b64_tr_b16 v[236:237], v246 offset:13312\n\t"
"v_cvt_pk_bf16_f32 v228, v228, v229\n\t"
"v_mfma_f32_16x16x32_bf16 a[48:51], a[216:219], a[176:179], a[48:51]\n\t"
"v_cvt_pk_bf16_f32 v229, v230, v231\n\t"
"v_cvt_pk_bf16_f32 v230, v224, v225\n\t"
"v_mfma_f32_16x16x32_bf16 a[48:51], a[220:223], a[180:183], a[48:51]\n\t"
"ds_write_b128 v245, v[232:235] offset:1024\n\t"
"v_cvt_pk_bf16_f32 v231, v226, v227\n\t"
"v_mfma_f32_16x16x32_bf16 a[56:59], a[220:223], a[184:187], a[56:59]\n\t"
"v_mfma_f32_16x16x32_bf16 a[56:59], a[216:219], a[180:183], a[56:59]\n\t"
"buffer_load_dwordx4 a[236:239], off, s[80:83], s20 offset:2048 sc1\n\t"
"v_mfma_f32_16x16x32_bf16 a[56:59], a[212:215], a[176:179], a[56:59]\n\t"
"s_mov_b32 s20, 1044480\n\t"
"ds_read_b64_tr_b16 v[224:225], v255 offset:13312\n\t"
"v_mfma_f32_16x16x32_bf16 a[64:67], a[212:215], a[180:183], a[64:67]\n\t"
"ds_read_b64_tr_b16 v[232:233], v246 offset:20992\n\t"
"v_mfma_f32_16x16x32_bf16 a[64:67], a[216:219], a[184:187], a[64:67]\n\t"
"buffer_load_dwordx4 a[240:243], off, s[80:83], s20 offset:0 sc1\n\t"
"v_mfma_f32_16x16x32_bf16 a[64:67], a[220:223], a[188:191], a[64:67]\n\t"
"ds_write_b128 v245, v[228:231] offset:7168\n\t"
"v_mfma_f32_16x16x32_bf16 a[72:75], a[212:215], a[184:187], a[72:75]\n\t"
"buffer_load_dwordx4 a[244:247], off, s[80:83], s20 offset:1024 sc1\n\t"
"v_mfma_f32_16x16x32_bf16 a[72:75], a[220:223], a[192:195], a[72:75]\n\t"
"s_waitcnt lgkmcnt(2)\n\t"
"ds_read_b64_tr_b16 v[228:229], v255 offset:20992\n\t"
"v_mfma_f32_16x16x32_bf16 a[72:75], a[216:219], a[188:191], a[72:75]\n\t"
"s_waitcnt lgkmcnt(2)\n\t"
"v_mfma_f32_4x4x4_16b_bf16 v[236:239], v[236:237], v[220:221], 0\n\t"
"v_mfma_f32_4x4x4_16b_bf16 v[224:227], v[224:225], v[220:221], 0\n\t"
"v_mfma_f32_4x4x4_16b_bf16 v[232:235], v[232:233], v[220:221], 0\n\t"
"s_waitcnt lgkmcnt(0)\n\t"
"v_mfma_f32_4x4x4_16b_bf16 v[228:231], v[228:229], v[220:221], 0\n\t"
"buffer_load_dwordx4 a[248:251], off, s[80:83], s20 offset:2048 sc1\n\t"
"s_mov_b32 s20, 1056768\n\t"
"v_mfma_f32_16x16x32_bf16 a[80:83], a[212:215], a[188:191], a[80:83]\n\t"
"buffer_load_dwordx4 a[252:255], off, s[80:83], s20 offset:0 sc1\n\t"
"v_mfma_f32_16x16x32_bf16 a[80:83], a[220:223], a[196:199], a[80:83]\n\t"
"v_cvt_pk_bf16_f32 v236, v236, v237\n\t"
"v_cvt_pk_bf16_f32 v237, v238, v239\n\t"
"v_mfma_f32_16x16x32_bf16 a[80:83], a[216:219], a[192:195], a[80:83]\n\t"
"v_cvt_pk_bf16_f32 v238, v224, v225\n\t"
"v_cvt_pk_bf16_f32 v239, v226, v227\n\t"
"v_mfma_f32_16x16x32_bf16 a[88:91], a[212:215], a[192:195], a[88:91]\n\t"
"ds_read_b64_tr_b16 v[224:225], v246 offset:27136\n\t"
"v_cvt_pk_bf16_f32 v232, v232, v233\n\t"
"v_mfma_f32_16x16x32_bf16 a[88:91], a[216:219], a[196:199], a[88:91]\n\t"
"v_cvt_pk_bf16_f32 v233, v234, v235\n\t"
"v_cvt_pk_bf16_f32 v234, v228, v229\n\t"
"v_mfma_f32_16x16x32_bf16 a[88:91], a[220:223], a[200:203], a[88:91]\n\t"
"ds_write_b128 v245, v[236:239] offset:13312\n\t"
"v_cvt_pk_bf16_f32 v235, v230, v231\n\t"
"v_mfma_f32_16x16x32_bf16 a[96:99], a[216:219], a[200:203], a[96:99]\n\t"
"s_waitcnt vmcnt(7)\n\t"
"v_mfma_f32_16x16x32_bf16 a[96:99], a[212:215], a[196:199], a[96:99]\n\t"
"ds_read_b64_tr_b16 v[228:229], v255 offset:27136\n\t"
"v_mfma_f32_16x16x32_bf16 a[96:99], a[220:223], a[204:207], a[96:99]\n\t"
"ds_write_b128 v245, v[232:235] offset:20992\n\t"
"v_mfma_f32_16x16x32_bf16 a[104:107], a[224:227], a[168:171], a[104:107]\n\t"
"buffer_load_dwordx4 a[216:219], off, s[80:83], s20 offset:1024 sc1\n\t"
"v_mfma_f32_16x16x32_bf16 a[104:107], a[232:235], a[176:179], a[104:107]\n\t"
"ds_read_b64_tr_b16 v[236:237], v246 offset:33280\n\t"
"v_mfma_f32_16x16x32_bf16 a[104:107], a[228:231], a[172:175], a[104:107]\n\t"
"ds_read_b64_tr_b16 v[232:233], v255 offset:33280\n\t"
"v_mfma_f32_16x16x32_bf16 a[112:115], a[224:227], a[172:175], a[112:115]\n\t"
"buffer_load_dwordx4 a[212:215], off, s[80:83], s20 offset:2048 sc1\n\t"
"v_mfma_f32_16x16x32_bf16 a[112:115], a[228:231], a[176:179], a[112:115]\n\t"
"s_waitcnt lgkmcnt(3)\n\t"
"ds_read_b128 a[168:171], v222 offset:768\n\t"
"s_mov_b32 s20, 1216512\n\t"
"v_mfma_f32_16x16x32_bf16 a[112:115], a[232:235], a[180:183], a[112:115]\n\t"
"buffer_load_dwordx4 a[220:223], off, s[80:83], s20 offset:0 sc1\n\t"
"v_mfma_f32_4x4x4_16b_bf16 v[224:227], v[224:225], v[220:221], 0\n\t"
"s_waitcnt lgkmcnt(2)\n\t"
"v_mfma_f32_4x4x4_16b_bf16 v[228:231], v[228:229], v[220:221], 0\n\t"
"s_waitcnt lgkmcnt(1)\n\t"
"v_mfma_f32_4x4x4_16b_bf16 v[236:239], v[236:237], v[220:221], 0\n\t"
"v_mfma_f32_4x4x4_16b_bf16 v[232:235], v[232:233], v[220:221], 0\n\t"
"v_mfma_f32_16x16x32_bf16 a[120:123], a[228:231], a[180:183], a[120:123]\n\t"
"ds_read_b128 a[172:175], v223 offset:6912\n\t"
"v_cvt_pk_bf16_f32 v224, v224, v225\n\t"
"v_mfma_f32_16x16x32_bf16 a[120:123], a[224:227], a[176:179], a[120:123]\n\t"
"ds_read_b128 a[176:179], v223 offset:13056\n\t"
"v_cvt_pk_bf16_f32 v225, v226, v227\n\t"
"v_mfma_f32_16x16x32_bf16 a[120:123], a[232:235], a[184:187], a[120:123]\n\t"
"v_cvt_pk_bf16_f32 v226, v228, v229\n\t"
"v_cvt_pk_bf16_f32 v227, v230, v231\n\t"
"v_mfma_f32_16x16x32_bf16 a[128:131], a[224:227], a[180:183], a[128:131]\n\t"
"ds_read_b128 a[180:183], v222 offset:20736\n\t"
"v_cvt_pk_bf16_f32 v236, v236, v237\n\t"
"v_mfma_f32_16x16x32_bf16 a[128:131], a[232:235], a[188:191], a[128:131]\n\t"
"v_cvt_pk_bf16_f32 v237, v238, v239\n\t"
"v_cvt_pk_bf16_f32 v238, v232, v233\n\t"
"v_mfma_f32_16x16x32_bf16 a[128:131], a[228:231], a[184:187], a[128:131]\n\t"
"ds_write_b128 v245, v[224:227] offset:27136\n\t"
"v_cvt_pk_bf16_f32 v239, v234, v235\n\t"
"v_mfma_f32_16x16x32_bf16 a[136:139], a[224:227], a[184:187], a[136:139]\n\t"
"v_mfma_f32_16x16x32_bf16 a[136:139], a[228:231], a[188:191], a[136:139]\n\t"
"ds_read_b128 a[184:187], v223 offset:26880\n\t"
"v_mfma_f32_16x16x32_bf16 a[136:139], a[232:235], a[192:195], a[136:139]\n\t"
"ds_write_b128 v245, v[236:239] offset:33280\n\t"
"v_mfma_f32_16x16x32_bf16 a[144:147], a[232:235], a[196:199], a[144:147]\n\t"
"v_mfma_f32_16x16x32_bf16 a[144:147], a[228:231], a[192:195], a[144:147]\n\t"
"v_mfma_f32_16x16x32_bf16 a[144:147], a[224:227], a[188:191], a[144:147]\n\t"
"ds_read_b128 a[188:191], v223 offset:33024\n\t"
"v_mfma_f32_16x16x32_bf16 a[152:155], a[224:227], a[192:195], a[152:155]\n\t"
"ds_read_b128 a[192:195], v222 offset:40704\n\t"
"v_mfma_f32_16x16x32_bf16 a[152:155], a[228:231], a[196:199], a[152:155]\n\t"
"v_mfma_f32_16x16x32_bf16 a[152:155], a[232:235], a[200:203], a[152:155]\n\t"
"s_barrier\n\t"
"v_mfma_f32_16x16x32_bf16 a[160:163], a[228:231], a[200:203], a[160:163]\n\t"
"s_waitcnt vmcnt(7) lgkmcnt(6)\n\t"
"ds_read_b64_tr_b16 v[228:229], v240 offset:40960\n\t"
"v_mfma_f32_16x16x32_bf16 a[160:163], a[224:227], a[196:199], a[160:163]\n\t"
"ds_read_b64_tr_b16 v[230:231], v240 offset:42496\n\t"
"v_mfma_f32_16x16x32_bf16 a[160:163], a[232:235], a[204:207], a[160:163]\n\t"
"ds_read_b64_tr_b16 v[232:233], v241 offset:40960\n\t"
"v_mfma_f32_16x16x32_bf16 v[200:203], v[96:99], a[168:171], v[200:203]\n\t"
"s_waitcnt lgkmcnt(8)\n\t"
"ds_read_b64_tr_b16 v[234:235], v241 offset:42496\n\t"
"v_subrev_u32 v240, 59904, v240\n\t"
"v_mfma_f32_16x16x32_bf16 v[200:203], a[236:239], a[176:179], v[200:203]\n\t"
"ds_read_b128 a[196:199], v223 offset:46848\n\t"
"v_subrev_u32 v241, 59904, v241\n\t"
"v_mfma_f32_16x16x32_bf16 v[200:203], v[248:251], a[172:175], v[200:203]\n\t"
"ds_read_b64_tr_b16 v[224:225], v240 offset:40960\n\t"
"v_mfma_f32_16x16x32_bf16 v[208:211], v[96:99], a[172:175], v[208:211]\n\t"
"s_waitcnt lgkmcnt(4)\n\t"
"ds_read_b64_tr_b16 v[226:227], v240 offset:42496\n\t"
"v_mfma_f32_16x16x32_bf16 v[208:211], v[248:251], a[176:179], v[208:211]\n\t"
"buffer_load_dwordx4 a[228:231], off, s[80:83], s20 offset:1024 sc1\n\t"
"v_mfma_f32_16x16x32_bf16 v[208:211], a[236:239], a[180:183], v[208:211]\n\t"
"s_waitcnt lgkmcnt(3)\n\t"
"ds_read_b64_tr_b16 v[236:237], v241 offset:40960\n\t"
"ds_read_b64_tr_b16 v[238:239], v241 offset:42496\n\t"
"v_mfma_f32_16x16x32_bf16 v[228:231], v[228:231], a[208:211], 0\n\t"
"buffer_load_dwordx4 a[224:227], off, s[80:83], s20 offset:2048 sc1\n\t"
"s_mov_b32 s20, 1228800\n\t"
"v_mfma_f32_16x16x32_bf16 v[232:235], v[232:235], a[208:211], 0\n\t"
"s_waitcnt lgkmcnt(2)\n\t"
"buffer_load_dwordx4 a[232:235], off, s[80:83], s20 offset:0 sc1\n\t"
"v_mfma_f32_16x16x32_bf16 v[224:227], v[224:227], a[208:211], 0\n\t"
"s_waitcnt lgkmcnt(0)\n\t"
"ds_read_b128 a[200:203], v223 offset:52992\n\t"
"v_mfma_f32_16x16x32_bf16 v[236:239], v[236:239], a[208:211], 0\n\t"
"ds_read_b128 a[204:207], v222 offset:60672\n\t"
"v_mfma_f32_16x16x32_bf16 v[216:219], v[248:251], a[180:183], v[216:219]\n\t"
"v_mfma_f32_16x16x32_bf16 v[216:219], v[96:99], a[176:179], v[216:219]\n\t"
"v_cvt_pk_bf16_f32 v228, v228, v229\n\t"
"v_cvt_pk_bf16_f32 v229, v230, v231\n\t"
"v_mfma_f32_16x16x32_bf16 v[216:219], a[236:239], a[184:187], v[216:219]\n\t"
"v_cvt_pk_bf16_f32 v230, v232, v233\n\t"
"v_cvt_pk_bf16_f32 v231, v234, v235\n\t"
"v_mfma_f32_16x16x32_bf16 a[4:7], v[96:99], a[180:183], a[4:7]\n\t"
"ds_read_b64_tr_b16 v[232:233], v246 offset:40960\n\t"
"v_cvt_pk_bf16_f32 v224, v224, v225\n\t"
"v_mfma_f32_16x16x32_bf16 a[4:7], a[236:239], a[188:191], a[4:7]\n\t"
"v_cvt_pk_bf16_f32 v225, v226, v227\n\t"
"v_cvt_pk_bf16_f32 v226, v236, v237\n\t"
"v_mfma_f32_16x16x32_bf16 a[4:7], v[248:251], a[184:187], a[4:7]\n\t"
"ds_write_b128 v243, v[228:231] offset:41984\n\t"
"v_mfma_f32_16x16x32_bf16 a[12:15], v[96:99], a[184:187], a[12:15]\n\t"
"v_cvt_pk_bf16_f32 v227, v238, v239\n\t"
"v_subrev_u32 v243, 59904, v243\n\t"
"v_mfma_f32_16x16x32_bf16 a[12:15], v[248:251], a[188:191], a[12:15]\n\t"
"ds_read_b64_tr_b16 v[236:237], v255 offset:40960\n\t"
"v_mfma_f32_16x16x32_bf16 a[12:15], a[236:239], a[192:195], a[12:15]\n\t"
"ds_write_b128 v243, v[224:227] offset:41984\n\t"
"v_mfma_f32_16x16x32_bf16 a[20:23], a[236:239], a[196:199], a[20:23]\n\t"
"s_waitcnt lgkmcnt(5)\n\t"
"v_mfma_f32_16x16x32_bf16 a[20:23], v[248:251], a[192:195], a[20:23]\n\t"
"ds_read_b64_tr_b16 v[228:229], v246 offset:47104\n\t"
"v_mfma_f32_16x16x32_bf16 a[20:23], v[96:99], a[188:191], a[20:23]\n\t"
"ds_read_b64_tr_b16 v[224:225], v255 offset:47104\n\t"
"v_mfma_f32_16x16x32_bf16 a[28:31], v[96:99], a[192:195], a[28:31]\n\t"
"s_waitcnt lgkmcnt(6)\n\t"
"v_mfma_f32_16x16x32_bf16 a[28:31], v[248:251], a[196:199], a[28:31]\n\t"
"v_mfma_f32_16x16x32_bf16 a[28:31], a[236:239], a[200:203], a[28:31]\n\t"
"s_barrier\n\t"
"v_mfma_f32_16x16x32_bf16 a[36:39], v[248:251], a[200:203], a[36:39]\n\t"
"s_waitcnt lgkmcnt(5)\n\t"
"v_mfma_f32_16x16x32_bf16 a[36:39], v[96:99], a[196:199], a[36:39]\n\t"
"buffer_load_dwordx4 v[248:251], off, s[80:83], s20 offset:1024 sc1\n\t"
"v_mfma_f32_16x16x32_bf16 a[36:39], a[236:239], a[204:207], a[36:39]\n\t"
"s_waitcnt lgkmcnt(1)\n\t"
"v_mfma_f32_4x4x4_16b_bf16 v[232:235], v[232:233], v[220:221], 0\n\t"
"s_waitcnt lgkmcnt(0)\n\t"
"v_mfma_f32_4x4x4_16b_bf16 v[236:239], v[236:237], v[220:221], 0\n\t"
"s_waitcnt vmcnt(8)\n\t"
"v_mfma_f32_4x4x4_16b_bf16 v[228:231], v[228:229], v[220:221], 0\n\t"
"v_mfma_f32_4x4x4_16b_bf16 v[224:227], v[224:225], v[220:221], 0\n\t"
"buffer_load_dwordx4 v[96:99], off, s[80:83], s20 offset:2048 sc1\n\t"
"s_mov_b32 s20, 1241088\n\t"
"v_mfma_f32_16x16x32_bf16 a[44:47], a[240:243], a[168:171], a[44:47]\n\t"
"buffer_load_dwordx4 a[236:239], off, s[80:83], s20 offset:0 sc1\n\t"
"v_mfma_f32_16x16x32_bf16 a[44:47], a[248:251], a[176:179], a[44:47]\n\t"
"v_cvt_pk_bf16_f32 v232, v232, v233\n\t"
"v_cvt_pk_bf16_f32 v233, v234, v235\n\t"
"v_mfma_f32_16x16x32_bf16 a[44:47], a[244:247], a[172:175], a[44:47]\n\t"
"v_cvt_pk_bf16_f32 v234, v236, v237\n\t"
"v_cvt_pk_bf16_f32 v235, v238, v239\n\t"
"v_mfma_f32_16x16x32_bf16 a[52:55], a[240:243], a[172:175], a[52:55]\n\t"
"ds_read_b64_tr_b16 v[236:237], v246 offset:53248\n\t"
"v_cvt_pk_bf16_f32 v228, v228, v229\n\t"
"v_mfma_f32_16x16x32_bf16 a[52:55], a[244:247], a[176:179], a[52:55]\n\t"
"v_cvt_pk_bf16_f32 v229, v230, v231\n\t"
"v_cvt_pk_bf16_f32 v230, v224, v225\n\t"
"v_mfma_f32_16x16x32_bf16 a[52:55], a[248:251], a[180:183], a[52:55]\n\t"
"ds_write_b128 v245, v[232:235] offset:40960\n\t"
"v_cvt_pk_bf16_f32 v231, v226, v227\n\t"
"v_mfma_f32_16x16x32_bf16 a[60:63], a[248:251], a[184:187], a[60:63]\n\t"
"v_mfma_f32_16x16x32_bf16 a[60:63], a[244:247], a[180:183], a[60:63]\n\t"
"ds_read_b64_tr_b16 v[224:225], v255 offset:53248\n\t"
"v_mfma_f32_16x16x32_bf16 a[60:63], a[240:243], a[176:179], a[60:63]\n\t"
"ds_write_b128 v245, v[228:231] offset:47104\n\t"
"v_mfma_f32_16x16x32_bf16 a[68:71], a[240:243], a[180:183], a[68:71]\n\t"
"v_mfma_f32_16x16x32_bf16 a[68:71], a[244:247], a[184:187], a[68:71]\n\t"
"v_mfma_f32_16x16x32_bf16 a[68:71], a[248:251], a[188:191], a[68:71]\n\t"
"v_mfma_f32_16x16x32_bf16 a[76:79], a[240:243], a[184:187], a[76:79]\n\t"
"s_waitcnt lgkmcnt(3)\n\t"
"v_mfma_f32_16x16x32_bf16 a[76:79], a[248:251], a[192:195], a[76:79]\n\t"
"s_waitcnt lgkmcnt(1)\n\t"
"ds_read_b64_tr_b16 v[232:233], v240 offset:1536\n\t"
"v_mfma_f32_16x16x32_bf16 a[76:79], a[244:247], a[188:191], a[76:79]\n\t"
"ds_read_b64_tr_b16 v[234:235], v240 offset:3072\n\t"
"v_mfma_f32_4x4x4_16b_bf16 v[236:239], v[236:237], v[220:221], 0\n\t"
"v_mfma_f32_4x4x4_16b_bf16 v[224:227], v[224:225], v[220:221], 0\n\t"
"v_mfma_f32_16x16x32_bf16 a[84:87], a[240:243], a[188:191], a[84:87]\n\t"
"ds_read_b64_tr_b16 v[228:229], v241 offset:1536\n\t"
"v_mfma_f32_16x16x32_bf16 a[84:87], a[248:251], a[196:199], a[84:87]\n\t"
"ds_read_b64_tr_b16 v[230:231], v241 offset:3072\n\t"
"v_cvt_pk_bf16_f32 v236, v236, v237\n\t"
"v_mfma_f32_16x16x32_bf16 a[84:87], a[244:247], a[192:195], a[84:87]\n\t"
"v_cvt_pk_bf16_f32 v237, v238, v239\n\t"
"v_cvt_pk_bf16_f32 v238, v224, v225\n\t"
"v_mfma_f32_16x16x32_bf16 a[92:95], a[240:243], a[192:195], a[92:95]\n\t"
"v_cvt_pk_bf16_f32 v239, v226, v227\n\t"
"v_mfma_f32_16x16x32_bf16 a[92:95], a[244:247], a[196:199], a[92:95]\n\t"
"ds_read_b64_tr_b16 v[224:225], v240 offset:21504\n\t"
"v_mfma_f32_16x16x32_bf16 a[92:95], a[248:251], a[200:203], a[92:95]\n\t"
"ds_read_b64_tr_b16 v[226:227], v240 offset:23040\n\t"
"v_mfma_f32_16x16x32_bf16 a[100:103], a[244:247], a[200:203], a[100:103]\n\t"
"buffer_load_dwordx4 a[244:247], off, s[80:83], s20 offset:1024 sc1\n\t"
"v_mfma_f32_16x16x32_bf16 a[100:103], a[240:243], a[196:199], a[100:103]\n\t"
"s_waitcnt vmcnt(8)\n\t"
"v_mfma_f32_16x16x32_bf16 a[100:103], a[248:251], a[204:207], a[100:103]\n\t"
"ds_write_b128 v245, v[236:239] offset:53248\n\t"
"v_mfma_f32_16x16x32_bf16 a[108:111], a[252:255], a[168:171], a[108:111]\n\t"
"buffer_load_dwordx4 a[240:243], off, s[80:83], s20 offset:2048 sc1\n\t"
"v_mfma_f32_16x16x32_bf16 a[108:111], a[212:215], a[176:179], a[108:111]\n\t"
"s_waitcnt lgkmcnt(3)\n\t"
"ds_read_b64_tr_b16 v[236:237], v241 offset:21504\n\t"
"v_mfma_f32_16x16x32_bf16 a[108:111], a[216:219], a[172:175], a[108:111]\n\t"
"s_waitcnt lgkmcnt(2)\n\t"
"ds_read_b64_tr_b16 v[238:239], v241 offset:23040\n\t"
"s_mov_b32 s20, 1253376\n\t"
"v_mfma_f32_16x16x32_bf16 v[232:235], v[232:235], a[208:211], 0\n\t"
"buffer_load_dwordx4 a[248:251], off, s[80:83], s20 offset:0 sc1\n\t"
"v_mfma_f32_16x16x32_bf16 v[228:231], v[228:231], a[208:211], 0\n\t"
"v_mfma_f32_16x16x32_bf16 v[224:227], v[224:227], a[208:211], 0\n\t"
"Lpg20_%=:\n\t"
"s_cbranch_vccnz Lvr2_%=\n\t"
"s_waitcnt lgkmcnt(0)\n\t"
"v_mfma_f32_16x16x32_bf16 v[236:239], v[236:239], a[208:211], 0\n\t"
"s_barrier\n\t"
"v_mfma_f32_16x16x32_bf16 a[116:119], a[252:255], a[172:175], a[116:119]\n\t"
"ds_read_b128 a[168:171], v222 offset:2560\n\t"
"v_mfma_f32_16x16x32_bf16 a[116:119], a[212:215], a[180:183], a[116:119]\n\t"
"ds_read_b128 a[172:175], v223 offset:8704\n\t"
"v_cvt_pk_bf16_f32 v232, v232, v233\n\t"
"v_mfma_f32_16x16x32_bf16 a[116:119], a[216:219], a[176:179], a[116:119]\n\t"
"v_cvt_pk_bf16_f32 v233, v234, v235\n\t"
"v_cvt_pk_bf16_f32 v234, v228, v229\n\t"
"v_mfma_f32_16x16x32_bf16 a[124:127], a[252:255], a[176:179], a[124:127]\n\t"
"ds_read_b128 a[176:179], v223 offset:14848\n\t"
"v_cvt_pk_bf16_f32 v235, v230, v231\n\t"
"v_mfma_f32_16x16x32_bf16 a[124:127], a[216:219], a[180:183], a[124:127]\n\t"
"v_cvt_pk_bf16_f32 v224, v224, v225\n\t"
"v_cvt_pk_bf16_f32 v225, v226, v227\n\t"
"v_mfma_f32_16x16x32_bf16 a[124:127], a[212:215], a[184:187], a[124:127]\n\t"
"ds_write_b128 v243, v[232:235] offset:512\n\t"
"v_mfma_f32_16x16x32_bf16 a[132:135], a[212:215], a[188:191], a[132:135]\n\t"
"v_cvt_pk_bf16_f32 v226, v236, v237\n\t"
"v_cvt_pk_bf16_f32 v227, v238, v239\n\t"
"v_mfma_f32_16x16x32_bf16 a[132:135], a[216:219], a[184:187], a[132:135]\n\t"
"ds_read_b64_tr_b16 v[228:229], v240 offset:41472\n\t"
"v_mfma_f32_16x16x32_bf16 a[132:135], a[252:255], a[180:183], a[132:135]\n\t"
"ds_write_b128 v243, v[224:227] offset:20480\n\t"
"v_mfma_f32_16x16x32_bf16 a[140:143], a[252:255], a[184:187], a[140:143]\n\t"
"v_mfma_f32_16x16x32_bf16 a[140:143], a[216:219], a[188:191], a[140:143]\n\t"
"ds_read_b64_tr_b16 v[230:231], v240 offset:43008\n\t"
"v_mfma_f32_16x16x32_bf16 a[140:143], a[212:215], a[192:195], a[140:143]\n\t"
"ds_read_b64_tr_b16 v[236:237], v241 offset:41472\n\t"
"v_mfma_f32_16x16x32_bf16 a[148:151], a[212:215], a[196:199], a[148:151]\n\t"
"ds_read_b64_tr_b16 v[238:239], v241 offset:43008\n\t"
"v_add_u32 v240, 59904, v240\n\t"
"v_mfma_f32_16x16x32_bf16 a[148:151], a[216:219], a[192:195], a[148:151]\n\t"
"ds_read_b128 a[180:183], v222 offset:22528\n\t"
"v_add_u32 v241, 59904, v241\n\t"
"v_mfma_f32_16x16x32_bf16 a[148:151], a[252:255], a[188:191], a[148:151]\n\t"
"ds_read_b64_tr_b16 v[232:233], v240 offset:1536\n\t"
"v_mfma_f32_16x16x32_bf16 a[156:159], a[252:255], a[192:195], a[156:159]\n\t"
"ds_read_b64_tr_b16 v[234:235], v240 offset:3072\n\t"
"v_mfma_f32_16x16x32_bf16 a[156:159], a[216:219], a[196:199], a[156:159]\n\t"
"ds_read_b64_tr_b16 v[224:225], v241 offset:1536\n\t"
"v_mfma_f32_16x16x32_bf16 a[156:159], a[212:215], a[200:203], a[156:159]\n\t"
"ds_read_b64_tr_b16 v[226:227], v241 offset:3072\n\t"
"v_mfma_f32_16x16x32_bf16 a[164:167], a[216:219], a[200:203], a[164:167]\n\t"
"s_waitcnt lgkmcnt(7)\n\t"
"ds_read_b128 a[184:187], v223 offset:28672\n\t"
"v_mfma_f32_16x16x32_bf16 a[164:167], a[252:255], a[196:199], a[164:167]\n\t"
"s_waitcnt lgkmcnt(6)\n\t"
"ds_read_b128 a[188:191], v223 offset:34816\n\t"
"v_mfma_f32_16x16x32_bf16 a[164:167], a[212:215], a[204:207], a[164:167]\n\t"
"buffer_load_dwordx4 a[216:219], off, s[80:83], s20 offset:1024 sc1\n\t"
"v_mfma_f32_16x16x32_bf16 v[228:231], v[228:231], a[208:211], 0\n\t"
"s_waitcnt lgkmcnt(4)\n\t"
"ds_read_b128 a[192:195], v222 offset:42496\n\t"
"v_mfma_f32_16x16x32_bf16 v[236:239], v[236:239], a[208:211], 0\n\t"
"buffer_load_dwordx4 a[212:215], off, s[80:83], s20 offset:2048 sc1\n\t"
"v_mfma_f32_16x16x32_bf16 v[232:235], v[232:235], a[208:211], 0\n\t"
"s_waitcnt vmcnt(9) lgkmcnt(3)\n\t"
"ds_read_b128 a[196:199], v223 offset:48640\n\t"
"s_mov_b32 s20, 1265664\n\t"
"v_mfma_f32_16x16x32_bf16 v[224:227], v[224:227], a[208:211], 0\n\t"
"buffer_load_dwordx4 a[252:255], off, s[80:83], s20 offset:0 sc1\n\t"
"Lst28_%=:\n\t"
"v_mfma_f32_16x16x32_bf16 v[0:3], a[220:223], a[168:171], v[0:3]\n\t"
"ds_read_b128 a[200:203], v223 offset:54784\n\t"
"v_mfma_f32_16x16x32_bf16 v[0:3], a[224:227], a[176:179], v[0:3]\n\t"
"ds_read_b128 a[204:207], v222 offset:62464\n\t"
"v_cvt_pk_bf16_f32 v228, v228, v229\n\t"
"v_mfma_f32_16x16x32_bf16 v[0:3], a[228:231], a[172:175], v[0:3]\n\t"
"v_cvt_pk_bf16_f32 v229, v230, v231\n\t"
"v_cvt_pk_bf16_f32 v230, v236, v237\n\t"
"v_mfma_f32_16x16x32_bf16 v[4:7], a[220:223], a[172:175], v[4:7]\n\t"
"s_waitcnt lgkmcnt(5)\n\t"
"v_cvt_pk_bf16_f32 v231, v238, v239\n\t"
"v_cvt_pk_bf16_f32 v232, v232, v233\n\t"
"v_mfma_f32_16x16x32_bf16 v[4:7], a[228:231], a[176:179], v[4:7]\n\t"
"v_cvt_pk_bf16_f32 v233, v234, v235\n\t"
"v_cvt_pk_bf16_f32 v234, v224, v225\n\t"
"v_mfma_f32_16x16x32_bf16 v[4:7], a[224:227], a[180:183], v[4:7]\n\t"
"ds_write_b128 v243, v[228:231] offset:40448\n\t"
"v_mfma_f32_16x16x32_bf16 v[8:11], a[224:227], a[184:187], v[8:11]\n\t"
"s_waitcnt lgkmcnt(5)\n\t"
"v_cvt_pk_bf16_f32 v235, v226, v227\n\t"
"v_add_u32 v243, 59904, v243\n\t"
"v_mfma_f32_16x16x32_bf16 v[8:11], a[228:231], a[180:183], v[8:11]\n\t"
"v_mfma_f32_16x16x32_bf16 v[8:11], a[220:223], a[176:179], v[8:11]\n\t"
"ds_write_b128 v243, v[232:235] offset:512\n\t"
"v_mfma_f32_16x16x32_bf16 v[12:15], a[220:223], a[180:183], v[12:15]\n\t"
"s_waitcnt lgkmcnt(5)\n\t"
"v_mfma_f32_16x16x32_bf16 v[12:15], a[228:231], a[184:187], v[12:15]\n\t"
"ds_read_b64_tr_b16 v[236:237], v240 offset:21504\n\t"
"v_mfma_f32_16x16x32_bf16 v[12:15], a[224:227], a[188:191], v[12:15]\n\t"
"ds_read_b64_tr_b16 v[238:239], v240 offset:23040\n\t"
"v_mfma_f32_16x16x32_bf16 v[16:19], a[224:227], a[192:195], v[16:19]\n\t"
"s_waitcnt lgkmcnt(6)\n\t"
"ds_read_b64_tr_b16 v[224:225], v241 offset:21504\n\t"
"v_mfma_f32_16x16x32_bf16 v[16:19], a[228:231], a[188:191], v[16:19]\n\t"
"ds_read_b64_tr_b16 v[226:227], v241 offset:23040\n\t"
"v_mfma_f32_16x16x32_bf16 v[16:19], a[220:223], a[184:187], v[16:19]\n\t"
"ds_read_b64_tr_b16 v[228:229], v240 offset:41472\n\t"
"v_mfma_f32_16x16x32_bf16 v[20:23], a[220:223], a[188:191], v[20:23]\n\t"
"s_waitcnt lgkmcnt(8)\n\t"
"ds_read_b64_tr_b16 v[230:231], v240 offset:43008\n\t"
"v_mfma_f32_16x16x32_bf16 v[20:23], a[228:231], a[192:195], v[20:23]\n\t"
"ds_read_b64_tr_b16 v[232:233], v241 offset:41472\n\t"
"v_mfma_f32_16x16x32_bf16 v[20:23], a[224:227], a[196:199], v[20:23]\n\t"
"ds_read_b64_tr_b16 v[234:235], v241 offset:43008\n\t"
"v_mfma_f32_16x16x32_bf16 v[24:27], a[224:227], a[200:203], v[24:27]\n\t"
"s_waitcnt lgkmcnt(10)\n\t"
"v_mfma_f32_16x16x32_bf16 v[24:27], a[228:231], a[196:199], v[24:27]\n\t"
"v_mfma_f32_16x16x32_bf16 v[24:27], a[220:223], a[192:195], v[24:27]\n\t"
"v_mfma_f32_16x16x32_bf16 v[28:31], a[220:223], a[196:199], v[28:31]\n\t"
"buffer_load_dwordx4 a[220:223], off, s[80:83], s20 offset:1024 sc1\n\t"
"v_mfma_f32_16x16x32_bf16 v[28:31], a[228:231], a[200:203], v[28:31]\n\t"
"s_waitcnt lgkmcnt(4)\n\t"
"v_mfma_f32_16x16x32_bf16 v[28:31], a[224:227], a[204:207], v[28:31]\n\t"
"s_waitcnt lgkmcnt(2)\n\t"
"v_mfma_f32_16x16x32_bf16 v[236:239], v[236:239], a[208:211], 0\n\t"
"buffer_load_dwordx4 a[224:227], off, s[80:83], s20 offset:2048 sc1\n\t"
"v_mfma_f32_16x16x32_bf16 v[224:227], v[224:227], a[208:211], 0\n\t"
"s_waitcnt vmcnt(9) lgkmcnt(0)\n\t"
"s_mov_b32 s20, 1277952\n\t"
"v_mfma_f32_16x16x32_bf16 v[228:231], v[228:231], a[208:211], 0\n\t"
"v_mfma_f32_16x16x32_bf16 v[232:235], v[232:235], a[208:211], 0\n\t"
"buffer_load_dwordx4 a[228:231], off, s[80:83], s20 offset:0 sc1\n\t"
"v_mfma_f32_16x16x32_bf16 v[32:35], a[232:235], a[168:171], v[32:35]\n\t"
"v_mfma_f32_16x16x32_bf16 v[32:35], v[96:99], a[176:179], v[32:35]\n\t"
"v_cvt_pk_bf16_f32 v236, v236, v237\n\t"
"v_cvt_pk_bf16_f32 v237, v238, v239\n\t"
"v_mfma_f32_16x16x32_bf16 v[32:35], v[248:251], a[172:175], v[32:35]\n\t"
"v_cvt_pk_bf16_f32 v238, v224, v225\n\t"
"v_cvt_pk_bf16_f32 v239, v226, v227\n\t"
"v_mfma_f32_16x16x32_bf16 v[36:39], a[232:235], a[172:175], v[36:39]\n\t"
"ds_read_b64_tr_b16 v[224:225], v246 offset:1536\n\t"
"v_cvt_pk_bf16_f32 v228, v228, v229\n\t"
"v_mfma_f32_16x16x32_bf16 v[36:39], v[248:251], a[176:179], v[36:39]\n\t"
"v_cvt_pk_bf16_f32 v229, v230, v231\n\t"
"v_cvt_pk_bf16_f32 v230, v232, v233\n\t"
"v_mfma_f32_16x16x32_bf16 v[36:39], v[96:99], a[180:183], v[36:39]\n\t"
"buffer_load_dwordx4 a[208:211], off, s[80:83], s20 offset:1024 sc1\n\t"
"v_mfma_f32_16x16x32_bf16 v[40:43], v[96:99], a[184:187], v[40:43]\n\t"
"v_cvt_pk_bf16_f32 v231, v234, v235\n\t"
"v_mfma_f32_16x16x32_bf16 v[40:43], v[248:251], a[180:183], v[40:43]\n\t"
"ds_read_b64_tr_b16 v[232:233], v255 offset:1536\n\t"
"v_mfma_f32_16x16x32_bf16 v[40:43], a[232:235], a[176:179], v[40:43]\n\t"
"ds_write_b128 v243, v[236:239] offset:20480\n\t"
"v_mfma_f32_16x16x32_bf16 v[44:47], a[232:235], a[180:183], v[44:47]\n\t"
"v_mfma_f32_16x16x32_bf16 v[44:47], v[248:251], a[184:187], v[44:47]\n\t"
"v_mfma_f32_16x16x32_bf16 v[44:47], v[96:99], a[188:191], v[44:47]\n\t"
"s_barrier\n\t"
"v_mfma_f32_16x16x32_bf16 v[48:51], a[232:235], a[184:187], v[48:51]\n\t"
"s_waitcnt lgkmcnt(2)\n\t"
"ds_read_b64_tr_b16 v[236:237], v246 offset:7680\n\t"
"v_mfma_f32_16x16x32_bf16 v[48:51], v[96:99], a[192:195], v[48:51]\n\t"
"s_waitcnt lgkmcnt(2)\n\t"
"v_mfma_f32_16x16x32_bf16 v[48:51], v[248:251], a[188:191], v[48:51]\n\t"
"ds_write_b128 v243, v[228:231] offset:40448\n\t"
"ds_read_b64_tr_b16 v[228:229], v255 offset:7680\n\t"
"v_mfma_f32_4x4x4_16b_bf16 v[224:227], v[224:225], v[220:221], 0\n\t"
"buffer_load_dwordx4 v[240:243], off, s[80:83], s20 offset:2048 sc1\n\t"
"v_mfma_f32_4x4x4_16b_bf16 v[232:235], v[232:233], v[220:221], 0\n\t"
"s_waitcnt lgkmcnt(2)\n\t"
"v_mfma_f32_4x4x4_16b_bf16 v[236:239], v[236:237], v[220:221], 0\n\t"
"s_waitcnt lgkmcnt(0)\n\t"
"v_mfma_f32_4x4x4_16b_bf16 v[228:231], v[228:229], v[220:221], 0\n\t"
"v_mfma_f32_16x16x32_bf16 v[52:55], a[232:235], a[188:191], v[52:55]\n\t"
"s_mov_b32 s20, 1511424\n\t"
"v_cvt_pk_bf16_f32 v224, v224, v225\n\t"
"v_cvt_pk_bf16_f32 v225, v226, v227\n\t"
"v_mfma_f32_16x16x32_bf16 v[52:55], v[96:99], a[196:199], v[52:55]\n\t"
"v_cvt_pk_bf16_f32 v226, v232, v233\n\t"
"v_cvt_pk_bf16_f32 v227, v234, v235\n\t"
"v_mfma_f32_16x16x32_bf16 v[52:55], v[248:251], a[192:195], v[52:55]\n\t"
"ds_write_b128 v245, v[224:227] offset:1536\n\t"
"v_mfma_f32_16x16x32_bf16 v[56:59], a[232:235], a[192:195], v[56:59]\n\t"
"v_cvt_pk_bf16_f32 v236, v236, v237\n\t"
"v_cvt_pk_bf16_f32 v237, v238, v239\n\t"
"v_mfma_f32_16x16x32_bf16 v[56:59], v[248:251], a[196:199], v[56:59]\n\t"
"ds_read_b64_tr_b16 v[232:233], v246 offset:13824\n\t"
"v_mfma_f32_16x16x32_bf16 v[56:59], v[96:99], a[200:203], v[56:59]\n\t"
"v_cvt_pk_bf16_f32 v238, v228, v229\n\t"
"v_cvt_pk_bf16_f32 v239, v230, v231\n\t"
"v_mfma_f32_16x16x32_bf16 v[60:63], v[248:251], a[200:203], v[60:63]\n\t"
"s_waitcnt vmcnt(9)\n\t"
"ds_read_b64_tr_b16 v[228:229], v255 offset:13824\n\t"
"v_mfma_f32_16x16x32_bf16 v[60:63], a[232:235], a[196:199], v[60:63]\n\t"
"ds_read_b64_tr_b16 v[224:225], v246 offset:21504\n\t"
"v_mfma_f32_16x16x32_bf16 v[60:63], v[96:99], a[204:207], v[60:63]\n\t"
"ds_write_b128 v245, v[236:239] offset:7680\n\t"
"v_mfma_f32_16x16x32_bf16 v[64:67], a[236:239], a[168:171], v[64:67]\n\t"
"buffer_load_dwordx4 v[248:251], off, s[80:83], s20 offset:0 sc1\n\t"
"v_mfma_f32_16x16x32_bf16 v[64:67], a[240:243], a[176:179], v[64:67]\n\t"
"ds_read_b64_tr_b16 v[236:237], v255 offset:21504\n\t"
"v_mfma_f32_16x16x32_bf16 v[64:67], a[244:247], a[172:175], v[64:67]\n\t"
"buffer_load_dwordx4 v[96:99], off, s[80:83], s20 offset:1024 sc1\n\t"
"v_mfma_f32_16x16x32_bf16 v[68:71], a[236:239], a[172:175], v[68:71]\n\t"
"buffer_load_dwordx4 a[232:235], off, s[80:83], s20 offset:2048 sc1\n\t"
"v_mfma_f32_16x16x32_bf16 v[68:71], a[244:247], a[176:179], v[68:71]\n\t"
"s_waitcnt lgkmcnt(3)\n\t"
"ds_read_b128 a[168:171], v222 offset:2816\n\t"
"v_mfma_f32_16x16x32_bf16 v[68:71], a[240:243], a[180:183], v[68:71]\n\t"
"s_waitcnt lgkmcnt(3)\n\t"
"ds_read_b128 a[172:175], v223 offset:8960\n\t"
"v_mfma_f32_4x4x4_16b_bf16 v[232:235], v[232:233], v[220:221], 0\n\t"
"s_waitcnt lgkmcnt(2)\n\t"
"v_mfma_f32_4x4x4_16b_bf16 v[228:231], v[228:229], v[220:221], 0\n\t"
"v_mfma_f32_4x4x4_16b_bf16 v[224:227], v[224:225], v[220:221], 0\n\t"
"v_mfma_f32_4x4x4_16b_bf16 v[236:239], v[236:237], v[220:221], 0\n\t"
"v_mfma_f32_16x16x32_bf16 v[72:75], a[244:247], a[180:183], v[72:75]\n\t"
"v_cvt_pk_bf16_f32 v232, v232, v233\n\t"
"v_cvt_pk_bf16_f32 v233, v234, v235\n\t"
"v_mfma_f32_16x16x32_bf16 v[72:75], a[236:239], a[176:179], v[72:75]\n\t"
"ds_read_b128 a[176:179], v223 offset:15104\n\t"
"v_cvt_pk_bf16_f32 v234, v228, v229\n\t"
"v_mfma_f32_16x16x32_bf16 v[72:75], a[240:243], a[184:187], v[72:75]\n\t"
"v_cvt_pk_bf16_f32 v235, v230, v231\n\t"
"v_cvt_pk_bf16_f32 v224, v224, v225\n\t"
"v_mfma_f32_16x16x32_bf16 v[76:79], a[236:239], a[180:183], v[76:79]\n\t"
"ds_read_b64_tr_b16 v[228:229], v246 offset:27648\n\t"
"v_cvt_pk_bf16_f32 v225, v226, v227\n\t"
"v_mfma_f32_16x16x32_bf16 v[76:79], a[240:243], a[188:191], v[76:79]\n\t"
"ds_read_b128 a[180:183], v222 offset:22784\n\t"
"v_cvt_pk_bf16_f32 v226, v236, v237\n\t"
"v_mfma_f32_16x16x32_bf16 v[76:79], a[244:247], a[184:187], v[76:79]\n\t"
"ds_write_b128 v245, v[232:235] offset:13824\n\t"
"v_cvt_pk_bf16_f32 v227, v238, v239\n\t"
"v_mfma_f32_16x16x32_bf16 v[80:83], a[236:239], a[184:187], v[80:83]\n\t"
"v_mfma_f32_16x16x32_bf16 v[80:83], a[244:247], a[188:191], v[80:83]\n\t"
"ds_read_b64_tr_b16 v[236:237], v255 offset:27648\n\t"
"v_mfma_f32_16x16x32_bf16 v[80:83], a[240:243], a[192:195], v[80:83]\n\t"
"ds_write_b128 v245, v[224:227] offset:21504\n\t"
"v_mfma_f32_16x16x32_bf16 v[84:87], a[240:243], a[196:199], v[84:87]\n\t"
"s_mov_b32 s20, 1523712\n\t"
"v_mfma_f32_16x16x32_bf16 v[84:87], a[244:247], a[192:195], v[84:87]\n\t"
"ds_read_b64_tr_b16 v[232:233], v246 offset:33792\n\t"
"v_mfma_f32_16x16x32_bf16 v[84:87], a[236:239], a[188:191], v[84:87]\n\t"
"ds_read_b64_tr_b16 v[224:225], v255 offset:33792\n\t"
"v_mfma_f32_16x16x32_bf16 v[88:91], a[236:239], a[192:195], v[88:91]\n\t"
"ds_read_b128 a[184:187], v223 offset:28928\n\t"
"v_mfma_f32_16x16x32_bf16 v[88:91], a[244:247], a[196:199], v[88:91]\n\t"
"ds_read_b128 a[188:191], v223 offset:35072\n\t"
"v_mfma_f32_16x16x32_bf16 v[88:91], a[240:243], a[200:203], v[88:91]\n\t"
"ds_read_b128 a[192:195], v222 offset:42752\n\t"
"v_mfma_f32_16x16x32_bf16 v[92:95], a[244:247], a[200:203], v[92:95]\n\t"
"buffer_load_dwordx4 a[244:247], off, s[80:83], s20 offset:0 sc1\n\t"
"v_mfma_f32_16x16x32_bf16 v[92:95], a[236:239], a[196:199], v[92:95]\n\t"
"s_waitcnt lgkmcnt(6)\n\t"
"ds_read_b128 a[196:199], v223 offset:48896\n\t"
"v_mfma_f32_16x16x32_bf16 v[92:95], a[240:243], a[204:207], v[92:95]\n\t"
"s_waitcnt lgkmcnt(5)\n\t"
"ds_read_b128 a[200:203], v223 offset:55040\n\t"
"v_mfma_f32_4x4x4_16b_bf16 v[228:231], v[228:229], v[220:221], 0\n\t"
"s_waitcnt lgkmcnt(5)\n\t"
"v_mfma_f32_4x4x4_16b_bf16 v[236:239], v[236:237], v[220:221], 0\n\t"
"s_waitcnt vmcnt(10)\n\t"
"v_mfma_f32_4x4x4_16b_bf16 v[232:235], v[232:233], v[220:221], 0\n\t"
"v_mfma_f32_4x4x4_16b_bf16 v[224:227], v[224:225], v[220:221], 0\n\t"
"buffer_load_dwordx4 a[236:239], off, s[80:83], s20 offset:1024 sc1\n\t"
"v_mfma_f32_16x16x32_bf16 v[100:103], a[248:251], a[168:171], v[100:103]\n\t"
"buffer_load_dwordx4 a[240:243], off, s[80:83], s20 offset:2048 sc1\n\t"
"v_mfma_f32_16x16x32_bf16 v[100:103], a[212:215], a[176:179], v[100:103]\n\t"
"ds_read_b128 a[204:207], v222 offset:62720\n\t"
"v_cvt_pk_bf16_f32 v228, v228, v229\n\t"
"v_mfma_f32_16x16x32_bf16 v[100:103], a[216:219], a[172:175], v[100:103]\n\t"
"v_cvt_pk_bf16_f32 v229, v230, v231\n\t"
"v_cvt_pk_bf16_f32 v230, v236, v237\n\t"
"v_mfma_f32_16x16x32_bf16 v[104:107], a[248:251], a[172:175], v[104:107]\n\t"
"s_waitcnt lgkmcnt(5)\n\t"
"v_cvt_pk_bf16_f32 v231, v238, v239\n\t"
"v_cvt_pk_bf16_f32 v232, v232, v233\n\t"
"v_mfma_f32_16x16x32_bf16 v[104:107], a[216:219], a[176:179], v[104:107]\n\t"
"v_cvt_pk_bf16_f32 v233, v234, v235\n\t"
"v_cvt_pk_bf16_f32 v234, v224, v225\n\t"
"v_mfma_f32_16x16x32_bf16 v[104:107], a[212:215], a[180:183], v[104:107]\n\t"
"ds_write_b128 v245, v[228:231] offset:27648\n\t"
"v_cvt_pk_bf16_f32 v235, v226, v227\n\t"
"v_mfma_f32_16x16x32_bf16 v[108:111], a[212:215], a[184:187], v[108:111]\n\t"
"s_waitcnt lgkmcnt(5)\n\t"
"v_mfma_f32_16x16x32_bf16 v[108:111], a[216:219], a[180:183], v[108:111]\n\t"
"v_mfma_f32_16x16x32_bf16 v[108:111], a[248:251], a[176:179], v[108:111]\n\t"
"ds_write_b128 v245, v[232:235] offset:33792\n\t"
"v_mfma_f32_16x16x32_bf16 v[112:115], a[248:251], a[180:183], v[112:115]\n\t"
"s_waitcnt lgkmcnt(5)\n\t"
"v_mfma_f32_16x16x32_bf16 v[112:115], a[216:219], a[184:187], v[112:115]\n\t"
"ds_read_b64_tr_b16 v[236:237], v246 offset:41472\n\t"
"v_mfma_f32_16x16x32_bf16 v[112:115], a[212:215], a[188:191], v[112:115]\n\t"
"ds_read_b64_tr_b16 v[224:225], v255 offset:41472\n\t"
"v_mfma_f32_16x16x32_bf16 v[116:119], a[212:215], a[192:195], v[116:119]\n\t"
"s_waitcnt lgkmcnt(6)\n\t"
"ds_read_b64_tr_b16 v[228:229], v246 offset:47616\n\t"
"v_mfma_f32_16x16x32_bf16 v[116:119], a[216:219], a[188:191], v[116:119]\n\t"
"ds_read_b64_tr_b16 v[232:233], v255 offset:47616\n\t"
"v_mfma_f32_16x16x32_bf16 v[116:119], a[248:251], a[184:187], v[116:119]\n\t"
"v_mfma_f32_16x16x32_bf16 v[120:123], a[248:251], a[188:191], v[120:123]\n\t"
"s_waitcnt lgkmcnt(7)\n\t"
"s_mov_b32 s20, 1536000\n\t"
"v_mfma_f32_16x16x32_bf16 v[120:123], a[216:219], a[192:195], v[120:123]\n\t"
"v_mfma_f32_16x16x32_bf16 v[120:123], a[212:215], a[196:199], v[120:123]\n\t"
"v_mfma_f32_16x16x32_bf16 v[124:127], a[212:215], a[200:203], v[124:127]\n\t"
"s_waitcnt lgkmcnt(6)\n\t"
"v_mfma_f32_16x16x32_bf16 v[124:127], a[216:219], a[196:199], v[124:127]\n\t"
"v_mfma_f32_16x16x32_bf16 v[124:127], a[248:251], a[192:195], v[124:127]\n\t"
"v_mfma_f32_16x16x32_bf16 v[128:131], a[248:251], a[196:199], v[128:131]\n\t"
"buffer_load_dwordx4 a[248:251], off, s[80:83], s20 offset:0 sc1\n\t"
"v_mfma_f32_16x16x32_bf16 v[128:131], a[216:219], a[200:203], v[128:131]\n\t"
"s_waitcnt lgkmcnt(2)\n\t"
"v_mfma_f32_16x16x32_bf16 v[128:131], a[212:215], a[204:207], v[128:131]\n\t"
"s_waitcnt lgkmcnt(1)\n\t"
"v_mfma_f32_4x4x4_16b_bf16 v[236:239], v[236:237], v[220:221], 0\n\t"
"s_waitcnt lgkmcnt(0)\n\t"
"v_mfma_f32_4x4x4_16b_bf16 v[224:227], v[224:225], v[220:221], 0\n\t"
"s_waitcnt vmcnt(10)\n\t"
"v_mfma_f32_4x4x4_16b_bf16 v[228:231], v[228:229], v[220:221], 0\n\t"
"v_mfma_f32_4x4x4_16b_bf16 v[232:235], v[232:233], v[220:221], 0\n\t"
"buffer_load_dwordx4 a[212:215], off, s[80:83], s20 offset:1024 sc1\n\t"
"v_mfma_f32_16x16x32_bf16 v[132:135], a[252:255], a[168:171], v[132:135]\n\t"
"buffer_load_dwordx4 a[216:219], off, s[80:83], s20 offset:2048 sc1\n\t"
"v_mfma_f32_16x16x32_bf16 v[132:135], a[224:227], a[176:179], v[132:135]\n\t"
"v_cvt_pk_bf16_f32 v236, v236, v237\n\t"
"v_cvt_pk_bf16_f32 v237, v238, v239\n\t"
"v_mfma_f32_16x16x32_bf16 v[132:135], a[220:223], a[172:175], v[132:135]\n\t"
"s_mov_b32 s20, 1548288\n\t"
"v_cvt_pk_bf16_f32 v238, v224, v225\n\t"
"v_cvt_pk_bf16_f32 v239, v226, v227\n\t"
"v_mfma_f32_16x16x32_bf16 v[136:139], a[252:255], a[172:175], v[136:139]\n\t"
"ds_read_b64_tr_b16 v[224:225], v246 offset:53760\n\t"
"v_cvt_pk_bf16_f32 v228, v228, v229\n\t"
"v_mfma_f32_16x16x32_bf16 v[136:139], a[220:223], a[176:179], v[136:139]\n\t"
"v_cvt_pk_bf16_f32 v229, v230, v231\n\t"
"v_cvt_pk_bf16_f32 v230, v232, v233\n\t"
"v_mfma_f32_16x16x32_bf16 v[136:139], a[224:227], a[180:183], v[136:139]\n\t"
"ds_write_b128 v245, v[236:239] offset:41472\n\t"
"v_cvt_pk_bf16_f32 v231, v234, v235\n\t"
"v_mfma_f32_16x16x32_bf16 v[140:143], a[224:227], a[184:187], v[140:143]\n\t"
"v_mfma_f32_16x16x32_bf16 v[140:143], a[220:223], a[180:183], v[140:143]\n\t"
"ds_read_b64_tr_b16 v[232:233], v255 offset:53760\n\t"
"v_mfma_f32_16x16x32_bf16 v[140:143], a[252:255], a[176:179], v[140:143]\n\t"
"ds_write_b128 v245, v[228:231] offset:47616\n\t"
"v_mfma_f32_16x16x32_bf16 v[144:147], a[252:255], a[180:183], v[144:147]\n\t"
"buffer_load_dwordx4 v[236:239], off, s[80:83], s20 offset:0 sc1\n\t"
"v_mfma_f32_16x16x32_bf16 v[144:147], a[220:223], a[184:187], v[144:147]\n\t"
"v_mfma_f32_16x16x32_bf16 v[144:147], a[224:227], a[188:191], v[144:147]\n\t"
"v_mfma_f32_16x16x32_bf16 v[148:151], a[252:255], a[184:187], v[148:151]\n\t"
"s_waitcnt lgkmcnt(3)\n\t"
"v_mfma_f32_16x16x32_bf16 v[148:151], a[224:227], a[192:195], v[148:151]\n\t"
"s_waitcnt lgkmcnt(1)\n\t"
"v_mfma_f32_16x16x32_bf16 v[148:151], a[220:223], a[188:191], v[148:151]\n\t"
"v_mfma_f32_4x4x4_16b_bf16 v[224:227], v[224:225], v[220:221], 0\n\t"
"v_mfma_f32_4x4x4_16b_bf16 v[232:235], v[232:233], v[220:221], 0\n\t"
"v_mfma_f32_16x16x32_bf16 v[152:155], a[252:255], a[188:191], v[152:155]\n\t"
"v_mfma_f32_16x16x32_bf16 v[152:155], a[224:227], a[196:199], v[152:155]\n\t"
"v_cvt_pk_bf16_f32 v224, v224, v225\n\t"
"v_cvt_pk_bf16_f32 v225, v226, v227\n\t"
"v_mfma_f32_16x16x32_bf16 v[152:155], a[220:223], a[192:195], v[152:155]\n\t"
"v_cvt_pk_bf16_f32 v226, v232, v233\n\t"
"v_cvt_pk_bf16_f32 v227, v234, v235\n\t"
"v_mfma_f32_16x16x32_bf16 v[156:159], a[252:255], a[192:195], v[156:159]\n\t"
"v_mfma_f32_16x16x32_bf16 v[156:159], a[220:223], a[196:199], v[156:159]\n\t"
"v_mfma_f32_16x16x32_bf16 v[156:159], a[224:227], a[200:203], v[156:159]\n\t"
"ds_write_b128 v245, v[224:227] offset:53760\n\t"
"v_mfma_f32_16x16x32_bf16 v[160:163], a[220:223], a[200:203], v[160:163]\n\t"
"buffer_load_dwordx4 a[220:223], off, s[80:83], s20 offset:1024 sc1\n\t"
"v_mfma_f32_16x16x32_bf16 v[160:163], a[252:255], a[196:199], v[160:163]\n\t"
"s_waitcnt vmcnt(11)\n\t"
"v_mfma_f32_16x16x32_bf16 v[160:163], a[224:227], a[204:207], v[160:163]\n\t"
"v_mfma_f32_16x16x32_bf16 v[164:167], a[228:231], a[168:171], v[164:167]\n\t"
"buffer_load_dwordx4 a[224:227], off, s[80:83], s20 offset:2048 sc1\n\t"
"v_mfma_f32_16x16x32_bf16 v[164:167], v[240:243], a[176:179], v[164:167]\n\t"
"s_mov_b32 s20, 1560576\n\t"
"v_mfma_f32_16x16x32_bf16 v[164:167], a[208:211], a[172:175], v[164:167]\n\t"
"s_waitcnt lgkmcnt(0)\n\t"
"s_barrier\n\t"
"v_mfma_f32_16x16x32_bf16 v[168:171], a[228:231], a[172:175], v[168:171]\n\t"
"ds_read_b128 a[168:171], v222 offset:3072\n\t"
"v_mfma_f32_16x16x32_bf16 v[168:171], a[208:211], a[176:179], v[168:171]\n\t"
"buffer_load_dwordx4 a[252:255], off, s[80:83], s20 offset:0 sc1\n\t"
"v_mfma_f32_16x16x32_bf16 v[168:171], v[240:243], a[180:183], v[168:171]\n\t"
"ds_read_b128 a[172:175], v223 offset:9216\n\t"
"v_mfma_f32_16x16x32_bf16 v[172:175], v[240:243], a[184:187], v[172:175]\n\t"
"v_mfma_f32_16x16x32_bf16 v[172:175], a[208:211], a[180:183], v[172:175]\n\t"
"v_mfma_f32_16x16x32_bf16 v[172:175], a[228:231], a[176:179], v[172:175]\n\t"
"ds_read_b128 a[176:179], v223 offset:15360\n\t"
"v_mfma_f32_16x16x32_bf16 v[176:179], a[228:231], a[180:183], v[176:179]\n\t"
"ds_read_b128 a[180:183], v222 offset:23040\n\t"
"v_mfma_f32_16x16x32_bf16 v[176:179], a[208:211], a[184:187], v[176:179]\n\t"
"v_mfma_f32_16x16x32_bf16 v[176:179], v[240:243], a[188:191], v[176:179]\n\t"
"v_mfma_f32_16x16x32_bf16 v[180:183], v[240:243], a[192:195], v[180:183]\n\t"
"v_mfma_f32_16x16x32_bf16 v[180:183], a[208:211], a[188:191], v[180:183]\n\t"
"v_mfma_f32_16x16x32_bf16 v[180:183], a[228:231], a[184:187], v[180:183]\n\t"
"ds_read_b128 a[184:187], v223 offset:29184\n\t"
"v_mfma_f32_16x16x32_bf16 v[184:187], a[228:231], a[188:191], v[184:187]\n\t"
"ds_read_b128 a[188:191], v223 offset:35328\n\t"
"v_mfma_f32_16x16x32_bf16 v[184:187], a[208:211], a[192:195], v[184:187]\n\t"
"v_mfma_f32_16x16x32_bf16 v[184:187], v[240:243], a[196:199], v[184:187]\n\t"
"v_mfma_f32_16x16x32_bf16 v[188:191], v[240:243], a[200:203], v[188:191]\n\t"
"v_mfma_f32_16x16x32_bf16 v[188:191], a[208:211], a[196:199], v[188:191]\n\t"
"v_mfma_f32_16x16x32_bf16 v[188:191], a[228:231], a[192:195], v[188:191]\n\t"
"ds_read_b128 a[192:195], v222 offset:43008\n\t"
"v_mfma_f32_16x16x32_bf16 v[192:195], a[228:231], a[196:199], v[192:195]\n\t"
"buffer_load_dwordx4 a[228:231], off, s[80:83], s20 offset:1024 sc1\n\t"
"v_mfma_f32_16x16x32_bf16 v[192:195], a[208:211], a[200:203], v[192:195]\n\t"
"s_waitcnt vmcnt(11) lgkmcnt(4)\n\t"
"ds_read_b128 a[196:199], v223 offset:49152\n\t"
"v_mfma_f32_16x16x32_bf16 v[192:195], v[240:243], a[204:207], v[192:195]\n\t"
"ds_read_b128 a[200:203], v223 offset:55296\n\t"
"Lst29_%=:\n\t"
"v_mfma_f32_16x16x32_bf16 v[0:3], v[248:251], a[168:171], v[0:3]\n\t"
"buffer_load_dwordx4 a[208:211], off, s[80:83], s20 offset:2048 sc1\n\t"
"v_mfma_f32_16x16x32_bf16 v[0:3], a[232:235], a[176:179], v[0:3]\n\t"
"s_waitcnt lgkmcnt(5)\n\t"
"ds_read_b128 a[204:207], v222 offset:62976\n\t"
"s_mov_b32 s20, 1572864\n\t"
"v_mfma_f32_16x16x32_bf16 v[0:3], v[96:99], a[172:175], v[0:3]\n\t"
"v_mfma_f32_16x16x32_bf16 v[4:7], v[248:251], a[172:175], v[4:7]\n\t"
"buffer_load_dwordx4 v[240:243], off, s[80:83], s20 offset:0 sc1\n\t"
"v_mfma_f32_16x16x32_bf16 v[4:7], v[96:99], a[176:179], v[4:7]\n\t"
"s_waitcnt lgkmcnt(5)\n\t"
"v_mfma_f32_16x16x32_bf16 v[4:7], a[232:235], a[180:183], v[4:7]\n\t"
"v_mfma_f32_16x16x32_bf16 v[8:11], a[232:235], a[184:187], v[8:11]\n\t"
"s_waitcnt lgkmcnt(4)\n\t"
"v_mfma_f32_16x16x32_bf16 v[8:11], v[96:99], a[180:183], v[8:11]\n\t"
"v_mfma_f32_16x16x32_bf16 v[8:11], v[248:251], a[176:179], v[8:11]\n\t"
"v_mfma_f32_16x16x32_bf16 v[12:15], v[248:251], a[180:183], v[12:15]\n\t"
"s_waitcnt lgkmcnt(3)\n\t"
"v_mfma_f32_16x16x32_bf16 v[12:15], v[96:99], a[184:187], v[12:15]\n\t"
"v_mfma_f32_16x16x32_bf16 v[12:15], a[232:235], a[188:191], v[12:15]\n\t"
"v_mfma_f32_16x16x32_bf16 v[16:19], a[232:235], a[192:195], v[16:19]\n\t"
"s_waitcnt lgkmcnt(2)\n\t"
"v_mfma_f32_16x16x32_bf16 v[16:19], v[96:99], a[188:191], v[16:19]\n\t"
"v_mfma_f32_16x16x32_bf16 v[16:19], v[248:251], a[184:187], v[16:19]\n\t"
"v_mfma_f32_16x16x32_bf16 v[20:23], v[248:251], a[188:191], v[20:23]\n\t"
"s_waitcnt lgkmcnt(1)\n\t"
"v_mfma_f32_16x16x32_bf16 v[20:23], v[96:99], a[192:195], v[20:23]\n\t"
"v_mfma_f32_16x16x32_bf16 v[20:23], a[232:235], a[196:199], v[20:23]\n\t"
"v_mfma_f32_16x16x32_bf16 v[24:27], a[232:235], a[200:203], v[24:27]\n\t"
"s_waitcnt lgkmcnt(0)\n\t"
"v_mfma_f32_16x16x32_bf16 v[24:27], v[96:99], a[196:199], v[24:27]\n\t"
"v_mfma_f32_16x16x32_bf16 v[24:27], v[248:251], a[192:195], v[24:27]\n\t"
"v_mfma_f32_16x16x32_bf16 v[28:31], v[248:251], a[196:199], v[28:31]\n\t"
"buffer_load_dwordx4 v[248:251], off, s[80:83], s20 offset:1024 sc1\n\t"
"v_mfma_f32_16x16x32_bf16 v[28:31], v[96:99], a[200:203], v[28:31]\n\t"
"s_waitcnt vmcnt(11)\n\t"
"v_mfma_f32_16x16x32_bf16 v[28:31], a[232:235], a[204:207], v[28:31]\n\t"
"v_mfma_f32_16x16x32_bf16 v[32:35], a[244:247], a[168:171], v[32:35]\n\t"
"buffer_load_dwordx4 v[96:99], off, s[80:83], s20 offset:2048 sc1\n\t"
"v_mfma_f32_16x16x32_bf16 v[32:35], a[240:243], a[176:179], v[32:35]\n\t"
"s_mov_b32 s20, 1290240\n\t"
"v_mfma_f32_16x16x32_bf16 v[32:35], a[236:239], a[172:175], v[32:35]\n\t"
"v_mfma_f32_16x16x32_bf16 v[36:39], a[244:247], a[172:175], v[36:39]\n\t"
"buffer_load_dwordx4 a[232:235], off, s[80:83], s20 offset:0 sc1\n\t"
"v_mfma_f32_16x16x32_bf16 v[36:39], a[236:239], a[176:179], v[36:39]\n\t"
"v_mfma_f32_16x16x32_bf16 v[36:39], a[240:243], a[180:183], v[36:39]\n\t"
"v_mfma_f32_16x16x32_bf16 v[40:43], a[240:243], a[184:187], v[40:43]\n\t"
"v_mfma_f32_16x16x32_bf16 v[40:43], a[236:239], a[180:183], v[40:43]\n\t"
"v_mfma_f32_16x16x32_bf16 v[40:43], a[244:247], a[176:179], v[40:43]\n\t"
"v_mfma_f32_16x16x32_bf16 v[44:47], a[244:247], a[180:183], v[44:47]\n\t"
"v_mfma_f32_16x16x32_bf16 v[44:47], a[236:239], a[184:187], v[44:47]\n\t"
"v_mfma_f32_16x16x32_bf16 v[44:47], a[240:243], a[188:191], v[44:47]\n\t"
"v_mfma_f32_16x16x32_bf16 v[48:51], a[240:243], a[192:195], v[48:51]\n\t"
"v_mfma_f32_16x16x32_bf16 v[48:51], a[236:239], a[188:191], v[48:51]\n\t"
"v_mfma_f32_16x16x32_bf16 v[48:51], a[244:247], a[184:187], v[48:51]\n\t"
"v_mfma_f32_16x16x32_bf16 v[52:55], a[244:247], a[188:191], v[52:55]\n\t"
"v_mfma_f32_16x16x32_bf16 v[52:55], a[236:239], a[192:195], v[52:55]\n\t"
"v_mfma_f32_16x16x32_bf16 v[52:55], a[240:243], a[196:199], v[52:55]\n\t"
"v_mfma_f32_16x16x32_bf16 v[56:59], a[240:243], a[200:203], v[56:59]\n\t"
"v_mfma_f32_16x16x32_bf16 v[56:59], a[236:239], a[196:199], v[56:59]\n\t"
"v_mfma_f32_16x16x32_bf16 v[56:59], a[244:247], a[192:195], v[56:59]\n\t"
"v_mfma_f32_16x16x32_bf16 v[60:63], a[244:247], a[196:199], v[60:63]\n\t"
"buffer_load_dwordx4 a[244:247], off, s[80:83], s20 offset:1024 sc1\n\t"
"v_mfma_f32_16x16x32_bf16 v[60:63], a[236:239], a[200:203], v[60:63]\n\t"
"s_waitcnt vmcnt(11)\n\t"
"v_mfma_f32_16x16x32_bf16 v[60:63], a[240:243], a[204:207], v[60:63]\n\t"
"v_mfma_f32_16x16x32_bf16 v[64:67], a[248:251], a[168:171], v[64:67]\n\t"
"buffer_load_dwordx4 a[236:239], off, s[80:83], s20 offset:2048 sc1\n\t"
"v_mfma_f32_16x16x32_bf16 v[64:67], a[216:219], a[176:179], v[64:67]\n\t"
"s_mov_b32 s20, 1302528\n\t"
"ds_read_b128 a[168:171], v222 offset:3328\n\t"
"v_mfma_f32_16x16x32_bf16 v[64:67], a[212:215], a[172:175], v[64:67]\n\t"
"v_mfma_f32_16x16x32_bf16 v[68:71], a[248:251], a[172:175], v[68:71]\n\t"
"buffer_load_dwordx4 a[240:243], off, s[80:83], s20 offset:0 sc1\n\t"
"v_mfma_f32_16x16x32_bf16 v[68:71], a[212:215], a[176:179], v[68:71]\n\t"
"ds_read_b128 a[172:175], v223 offset:9472\n\t"
"v_mfma_f32_16x16x32_bf16 v[68:71], a[216:219], a[180:183], v[68:71]\n\t"
"v_mfma_f32_16x16x32_bf16 v[72:75], a[216:219], a[184:187], v[72:75]\n\t"
"v_mfma_f32_16x16x32_bf16 v[72:75], a[212:215], a[180:183], v[72:75]\n\t"
"v_mfma_f32_16x16x32_bf16 v[72:75], a[248:251], a[176:179], v[72:75]\n\t"
"ds_read_b128 a[176:179], v223 offset:15616\n\t"
"v_mfma_f32_16x16x32_bf16 v[76:79], a[248:251], a[180:183], v[76:79]\n\t"
"ds_read_b128 a[180:183], v222 offset:23296\n\t"
"v_mfma_f32_16x16x32_bf16 v[76:79], a[212:215], a[184:187], v[76:79]\n\t"
"v_mfma_f32_16x16x32_bf16 v[76:79], a[216:219], a[188:191], v[76:79]\n\t"
"v_mfma_f32_16x16x32_bf16 v[80:83], a[216:219], a[192:195], v[80:83]\n\t"
"v_mfma_f32_16x16x32_bf16 v[80:83], a[212:215], a[188:191], v[80:83]\n\t"
"v_mfma_f32_16x16x32_bf16 v[80:83], a[248:251], a[184:187], v[80:83]\n\t"
"ds_read_b128 a[184:187], v223 offset:29440\n\t"
"v_mfma_f32_16x16x32_bf16 v[84:87], a[248:251], a[188:191], v[84:87]\n\t"
"ds_read_b128 a[188:191], v223 offset:35584\n\t"
"v_mfma_f32_16x16x32_bf16 v[84:87], a[212:215], a[192:195], v[84:87]\n\t"
"v_mfma_f32_16x16x32_bf16 v[84:87], a[216:219], a[196:199], v[84:87]\n\t"
"v_mfma_f32_16x16x32_bf16 v[88:91], a[216:219], a[200:203], v[88:91]\n\t"
"v_mfma_f32_16x16x32_bf16 v[88:91], a[212:215], a[196:199], v[88:91]\n\t"
"v_mfma_f32_16x16x32_bf16 v[88:91], a[248:251], a[192:195], v[88:91]\n\t"
"ds_read_b128 a[192:195], v222 offset:43264\n\t"
"v_mfma_f32_16x16x32_bf16 v[92:95], a[248:251], a[196:199], v[92:95]\n\t"
"buffer_load_dwordx4 a[248:251], off, s[80:83], s20 offset:1024 sc1\n\t"
"v_mfma_f32_16x16x32_bf16 v[92:95], a[212:215], a[200:203], v[92:95]\n\t"
"s_waitcnt vmcnt(11) lgkmcnt(4)\n\t"
"ds_read_b128 a[196:199], v223 offset:49408\n\t"
"v_mfma_f32_16x16x32_bf16 v[92:95], a[216:219], a[204:207], v[92:95]\n\t"
"ds_read_b128 a[200:203], v223 offset:55552\n\t"
"v_mfma_f32_16x16x32_bf16 v[100:103], v[236:239], a[168:171], v[100:103]\n\t"
"buffer_load_dwordx4 a[212:215], off, s[80:83], s20 offset:2048 sc1\n\t"
"v_mfma_f32_16x16x32_bf16 v[100:103], a[224:227], a[176:179], v[100:103]\n\t"
"s_waitcnt lgkmcnt(5)\n\t"
"ds_read_b128 a[204:207], v222 offset:63232\n\t"
"s_mov_b32 s20, 1314816\n\t"
"v_mfma_f32_16x16x32_bf16 v[100:103], a[220:223], a[172:175], v[100:103]\n\t"
"v_mfma_f32_16x16x32_bf16 v[104:107], v[236:239], a[172:175], v[104:107]\n\t"
"buffer_load_dwordx4 a[216:219], off, s[80:83], s20 offset:0 sc1\n\t"
"v_mfma_f32_16x16x32_bf16 v[104:107], a[220:223], a[176:179], v[104:107]\n\t"
"s_waitcnt lgkmcnt(5)\n\t"
"v_mfma_f32_16x16x32_bf16 v[104:107], a[224:227], a[180:183], v[104:107]\n\t"
"v_mfma_f32_16x16x32_bf16 v[108:111], a[224:227], a[184:187], v[108:111]\n\t"
"s_waitcnt lgkmcnt(4)\n\t"
"v_fma_f32 v0, v100, s52, v0\n\t"
"v_fma_f32 v1, v101, s52, v1\n\t"
"v_mfma_f32_16x16x32_bf16 v[108:111], a[220:223], a[180:183], v[108:111]\n\t"
"v_fma_f32 v2, v102, s52, v2\n\t"
"v_fma_f32 v3, v103, s52, v3\n\t"
"v_mfma_f32_16x16x32_bf16 v[108:111], v[236:239], a[176:179], v[108:111]\n\t"
"v_fma_f32 v100, v0, s54, -v100\n\t"
"v_fma_f32 v101, v1, s54, -v101\n\t"
"v_mfma_f32_16x16x32_bf16 v[112:115], v[236:239], a[180:183], v[112:115]\n\t"
"s_waitcnt lgkmcnt(3)\n\t"
"v_fma_f32 v102, v2, s54, -v102\n\t"
"v_fma_f32 v103, v3, s54, -v103\n\t"
"v_mfma_f32_16x16x32_bf16 v[112:115], a[220:223], a[184:187], v[112:115]\n\t"
"s_waitcnt lgkmcnt(1)\n\t"
"v_fma_f32 v4, v104, s52, v4\n\t"
"v_fma_f32 v5, v105, s52, v5\n\t"
"v_mfma_f32_16x16x32_bf16 v[112:115], a[224:227], a[188:191], v[112:115]\n\t"
"Lpg21_%=:\n\t"
"s_cbranch_vccnz Lvr3_%=\n\t"
"v_fma_f32 v6, v106, s52, v6\n\t"
"v_fma_f32 v7, v107, s52, v7\n\t"
"v_mfma_f32_16x16x32_bf16 v[116:119], a[224:227], a[192:195], v[116:119]\n\t"
"v_fma_f32 v104, v4, s54, -v104\n\t"
"v_fma_f32 v105, v5, s54, -v105\n\t"
"v_mfma_f32_16x16x32_bf16 v[116:119], a[220:223], a[188:191], v[116:119]\n\t"
"v_fma_f32 v106, v6, s54, -v106\n\t"
"v_fma_f32 v107, v7, s54, -v107\n\t"
"v_mfma_f32_16x16x32_bf16 v[116:119], v[236:239], a[184:187], v[116:119]\n\t"
"v_fma_f32 v8, v108, s52, v8\n\t"
"v_fma_f32 v9, v109, s52, v9\n\t"
"v_mfma_f32_16x16x32_bf16 v[120:123], v[236:239], a[188:191], v[120:123]\n\t"
"v_fma_f32 v10, v110, s52, v10\n\t"
"v_fma_f32 v11, v111, s52, v11\n\t"
"v_mfma_f32_16x16x32_bf16 v[120:123], a[220:223], a[192:195], v[120:123]\n\t"
"v_fma_f32 v108, v8, s54, -v108\n\t"
"v_fma_f32 v109, v9, s54, -v109\n\t"
"v_mfma_f32_16x16x32_bf16 v[120:123], a[224:227], a[196:199], v[120:123]\n\t"
"v_fma_f32 v110, v10, s54, -v110\n\t"
"v_fma_f32 v111, v11, s54, -v111\n\t"
"v_mfma_f32_16x16x32_bf16 v[124:127], a[224:227], a[200:203], v[124:127]\n\t"
"s_waitcnt lgkmcnt(0)\n\t"
"v_fma_f32 v12, v112, s52, v12\n\t"
"v_fma_f32 v13, v113, s52, v13\n\t"
"v_mfma_f32_16x16x32_bf16 v[124:127], a[220:223], a[196:199], v[124:127]\n\t"
"v_fma_f32 v14, v114, s52, v14\n\t"
"v_fma_f32 v15, v115, s52, v15\n\t"
"v_mfma_f32_16x16x32_bf16 v[124:127], v[236:239], a[192:195], v[124:127]\n\t"
"v_fma_f32 v112, v12, s54, -v112\n\t"
"v_fma_f32 v113, v13, s54, -v113\n\t"
"v_mfma_f32_16x16x32_bf16 v[128:131], v[236:239], a[196:199], v[128:131]\n\t"
"s_waitcnt vmcnt(10)\n\t"
"v_fma_f32 v114, v14, s54, -v114\n\t"
"v_fma_f32 v115, v15, s54, -v115\n\t"
"v_mfma_f32_16x16x32_bf16 v[128:131], a[220:223], a[200:203], v[128:131]\n\t"
"v_fma_f32 v16, v116, s52, v16\n\t"
"v_fma_f32 v17, v117, s52, v17\n\t"
"v_mfma_f32_16x16x32_bf16 v[128:131], a[224:227], a[204:207], v[128:131]\n\t"
"v_fma_f32 v18, v118, s52, v18\n\t"
"v_fma_f32 v19, v119, s52, v19\n\t"
"v_mfma_f32_16x16x32_bf16 v[132:135], a[252:255], a[168:171], v[132:135]\n\t"
"v_fma_f32 v116, v16, s54, -v116\n\t"
"v_fma_f32 v117, v17, s54, -v117\n\t"
"v_mfma_f32_16x16x32_bf16 v[132:135], a[208:211], a[176:179], v[132:135]\n\t"
"v_fma_f32 v118, v18, s54, -v118\n\t"
"v_fma_f32 v119, v19, s54, -v119\n\t"
"v_mfma_f32_16x16x32_bf16 v[132:135], a[228:231], a[172:175], v[132:135]\n\t"
"buffer_load_dwordx4 v[236:239], off, s[80:83], s20 offset:1024 sc1\n\t"
"v_mfma_f32_16x16x32_bf16 v[136:139], a[252:255], a[172:175], v[136:139]\n\t"
"buffer_load_dwordx4 a[220:223], off, s[80:83], s20 offset:2048 sc1\n\t"
"v_mfma_f32_16x16x32_bf16 v[136:139], a[228:231], a[176:179], v[136:139]\n\t"
"s_mov_b32 s20, 1327104\n\t"
"v_fma_f32 v20, v120, s52, v20\n\t"
"v_fma_f32 v21, v121, s52, v21\n\t"
"v_mfma_f32_16x16x32_bf16 v[136:139], a[208:211], a[180:183], v[136:139]\n\t"
"v_fma_f32 v22, v122, s52, v22\n\t"
"v_fma_f32 v23, v123, s52, v23\n\t"
"v_mfma_f32_16x16x32_bf16 v[140:143], a[208:211], a[184:187], v[140:143]\n\t"
"v_fma_f32 v120, v20, s54, -v120\n\t"
"v_fma_f32 v121, v21, s54, -v121\n\t"
"v_mfma_f32_16x16x32_bf16 v[140:143], a[228:231], a[180:183], v[140:143]\n\t"
"v_fma_f32 v122, v22, s54, -v122\n\t"
"v_fma_f32 v123, v23, s54, -v123\n\t"
"v_mfma_f32_16x16x32_bf16 v[140:143], a[252:255], a[176:179], v[140:143]\n\t"
"buffer_load_dwordx4 a[224:227], off, s[80:83], s20 offset:0 sc1\n\t"
"v_mfma_f32_16x16x32_bf16 v[144:147], a[252:255], a[180:183], v[144:147]\n\t"
"v_fma_f32 v24, v124, s52, v24\n\t"
"v_fma_f32 v25, v125, s52, v25\n\t"
"v_mfma_f32_16x16x32_bf16 v[144:147], a[228:231], a[184:187], v[144:147]\n\t"
"v_fma_f32 v26, v126, s52, v26\n\t"
"v_fma_f32 v27, v127, s52, v27\n\t"
"v_mfma_f32_16x16x32_bf16 v[144:147], a[208:211], a[188:191], v[144:147]\n\t"
"v_fma_f32 v124, v24, s54, -v124\n\t"
"v_fma_f32 v125, v25, s54, -v125\n\t"
"v_mfma_f32_16x16x32_bf16 v[148:151], a[208:211], a[192:195], v[148:151]\n\t"
"v_fma_f32 v126, v26, s54, -v126\n\t"
"v_fma_f32 v127, v27, s54, -v127\n\t"
"v_mfma_f32_16x16x32_bf16 v[148:151], a[228:231], a[188:191], v[148:151]\n\t"
"v_fma_f32 v28, v128, s52, v28\n\t"
"v_fma_f32 v29, v129, s52, v29\n\t"
"v_mfma_f32_16x16x32_bf16 v[148:151], a[252:255], a[184:187], v[148:151]\n\t"
"v_fma_f32 v30, v130, s52, v30\n\t"
"v_fma_f32 v31, v131, s52, v31\n\t"
"v_mfma_f32_16x16x32_bf16 v[152:155], a[252:255], a[188:191], v[152:155]\n\t"
"v_fma_f32 v128, v28, s54, -v128\n\t"
"v_fma_f32 v129, v29, s54, -v129\n\t"
"v_mfma_f32_16x16x32_bf16 v[152:155], a[228:231], a[192:195], v[152:155]\n\t"
"v_fma_f32 v130, v30, s54, -v130\n\t"
"v_fma_f32 v131, v31, s54, -v131\n\t"
"v_mfma_f32_16x16x32_bf16 v[152:155], a[208:211], a[196:199], v[152:155]\n\t"
"v_fma_f32 v32, v132, s52, v32\n\t"
"v_fma_f32 v33, v133, s52, v33\n\t"
"v_mfma_f32_16x16x32_bf16 v[156:159], a[208:211], a[200:203], v[156:159]\n\t"
"v_fma_f32 v34, v134, s52, v34\n\t"
"v_fma_f32 v35, v135, s52, v35\n\t"
"v_mfma_f32_16x16x32_bf16 v[156:159], a[228:231], a[196:199], v[156:159]\n\t"
"v_fma_f32 v132, v32, s54, -v132\n\t"
"v_fma_f32 v133, v33, s54, -v133\n\t"
"v_mfma_f32_16x16x32_bf16 v[156:159], a[252:255], a[192:195], v[156:159]\n\t"
"v_fma_f32 v134, v34, s54, -v134\n\t"
"v_fma_f32 v135, v35, s54, -v135\n\t"
"v_mfma_f32_16x16x32_bf16 v[160:163], a[252:255], a[196:199], v[160:163]\n\t"
"s_waitcnt vmcnt(10)\n\t"
"v_fma_f32 v36, v136, s52, v36\n\t"
"v_fma_f32 v37, v137, s52, v37\n\t"
"v_mfma_f32_16x16x32_bf16 v[160:163], a[228:231], a[200:203], v[160:163]\n\t"
"v_fma_f32 v38, v138, s52, v38\n\t"
"v_fma_f32 v39, v139, s52, v39\n\t"
"v_mfma_f32_16x16x32_bf16 v[160:163], a[208:211], a[204:207], v[160:163]\n\t"
"v_fma_f32 v136, v36, s54, -v136\n\t"
"v_fma_f32 v137, v37, s54, -v137\n\t"
"v_mfma_f32_16x16x32_bf16 v[164:167], v[240:243], a[168:171], v[164:167]\n\t"
"ds_read_b128 a[168:171], v222 offset:1024\n\t"
"v_fma_f32 v138, v38, s54, -v138\n\t"
"v_mfma_f32_16x16x32_bf16 v[164:167], v[96:99], a[176:179], v[164:167]\n\t"
"v_fma_f32 v139, v39, s54, -v139\n\t"
"v_fma_f32 v40, v140, s52, v40\n\t"
"v_mfma_f32_16x16x32_bf16 v[164:167], v[248:251], a[172:175], v[164:167]\n\t"
"buffer_load_dwordx4 a[252:255], off, s[80:83], s20 offset:1024 sc1\n\t"
"v_mfma_f32_16x16x32_bf16 v[168:171], v[240:243], a[172:175], v[168:171]\n\t"
"buffer_load_dwordx4 a[208:211], off, s[80:83], s20 offset:2048 sc1\n\t"
"v_mfma_f32_16x16x32_bf16 v[168:171], v[248:251], a[176:179], v[168:171]\n\t"
"s_mov_b32 s20, 1339392\n\t"
"ds_read_b128 a[172:175], v223 offset:7168\n\t"
"v_fma_f32 v41, v141, s52, v41\n\t"
"v_mfma_f32_16x16x32_bf16 v[168:171], v[96:99], a[180:183], v[168:171]\n\t"
"v_fma_f32 v42, v142, s52, v42\n\t"
"v_fma_f32 v43, v143, s52, v43\n\t"
"v_mfma_f32_16x16x32_bf16 v[172:175], v[96:99], a[184:187], v[172:175]\n\t"
"v_fma_f32 v140, v40, s54, -v140\n\t"
"v_fma_f32 v141, v41, s54, -v141\n\t"
"v_mfma_f32_16x16x32_bf16 v[172:175], v[248:251], a[180:183], v[172:175]\n\t"
"v_fma_f32 v142, v42, s54, -v142\n\t"
"v_fma_f32 v143, v43, s54, -v143\n\t"
"v_mfma_f32_16x16x32_bf16 v[172:175], v[240:243], a[176:179], v[172:175]\n\t"
"buffer_load_dwordx4 a[228:231], off, s[80:83], s20 offset:0 sc1\n\t"
"v_mfma_f32_16x16x32_bf16 v[176:179], v[240:243], a[180:183], v[176:179]\n\t"
"ds_read_b128 a[176:179], v223 offset:13312\n\t"
"v_fma_f32 v44, v144, s52, v44\n\t"
"v_mfma_f32_16x16x32_bf16 v[176:179], v[248:251], a[184:187], v[176:179]\n\t"
"ds_read_b128 a[180:183], v222 offset:20992\n\t"
"v_fma_f32 v45, v145, s52, v45\n\t"
"v_mfma_f32_16x16x32_bf16 v[176:179], v[96:99], a[188:191], v[176:179]\n\t"
"v_fma_f32 v46, v146, s52, v46\n\t"
"v_fma_f32 v47, v147, s52, v47\n\t"
"v_mfma_f32_16x16x32_bf16 v[180:183], v[96:99], a[192:195], v[180:183]\n\t"
"v_fma_f32 v144, v44, s54, -v144\n\t"
"v_fma_f32 v145, v45, s54, -v145\n\t"
"v_mfma_f32_16x16x32_bf16 v[180:183], v[248:251], a[188:191], v[180:183]\n\t"
"v_fma_f32 v146, v46, s54, -v146\n\t"
"v_fma_f32 v147, v47, s54, -v147\n\t"
"v_mfma_f32_16x16x32_bf16 v[180:183], v[240:243], a[184:187], v[180:183]\n\t"
"ds_read_b128 a[184:187], v223 offset:27136\n\t"
"v_fma_f32 v48, v148, s52, v48\n\t"
"v_mfma_f32_16x16x32_bf16 v[184:187], v[240:243], a[188:191], v[184:187]\n\t"
"ds_read_b128 a[188:191], v223 offset:33280\n\t"
"v_fma_f32 v49, v149, s52, v49\n\t"
"v_mfma_f32_16x16x32_bf16 v[184:187], v[248:251], a[192:195], v[184:187]\n\t"
"v_fma_f32 v50, v150, s52, v50\n\t"
"v_fma_f32 v51, v151, s52, v51\n\t"
"v_mfma_f32_16x16x32_bf16 v[184:187], v[96:99], a[196:199], v[184:187]\n\t"
"v_fma_f32 v148, v48, s54, -v148\n\t"
"v_fma_f32 v149, v49, s54, -v149\n\t"
"v_mfma_f32_16x16x32_bf16 v[188:191], v[96:99], a[200:203], v[188:191]\n\t"
"v_fma_f32 v150, v50, s54, -v150\n\t"
"v_fma_f32 v151, v51, s54, -v151\n\t"
"v_mfma_f32_16x16x32_bf16 v[188:191], v[248:251], a[196:199], v[188:191]\n\t"
"v_fma_f32 v52, v152, s52, v52\n\t"
"v_fma_f32 v53, v153, s52, v53\n\t"
"v_mfma_f32_16x16x32_bf16 v[188:191], v[240:243], a[192:195], v[188:191]\n\t"
"ds_read_b128 a[192:195], v222 offset:40960\n\t"
"v_fma_f32 v54, v154, s52, v54\n\t"
"v_mfma_f32_16x16x32_bf16 v[192:195], v[240:243], a[196:199], v[192:195]\n\t"
"s_waitcnt vmcnt(10) lgkmcnt(4)\n\t"
"ds_read_b128 a[196:199], v223 offset:47104\n\t"
"v_fma_f32 v55, v155, s52, v55\n\t"
"v_mfma_f32_16x16x32_bf16 v[192:195], v[248:251], a[200:203], v[192:195]\n\t"
"ds_read_b128 a[200:203], v223 offset:53248\n\t"
"v_fma_f32 v152, v52, s54, -v152\n\t"
"v_mfma_f32_16x16x32_bf16 v[192:195], v[96:99], a[204:207], v[192:195]\n\t"
"ds_read_b128 a[204:207], v222 offset:60928\n\t"
"v_fma_f32 v153, v53, s54, -v153\n\t"
"Lst30_%=:\n\t"
"v_mfma_f32_16x16x32_bf16 v[196:199], a[232:235], a[168:171], v[196:199]\n\t"
"s_waitcnt lgkmcnt(6)\n\t"
"v_fma_f32 v154, v54, s54, -v154\n\t"
"v_fma_f32 v155, v55, s54, -v155\n\t"
"v_mfma_f32_16x16x32_bf16 v[196:199], a[236:239], a[176:179], v[196:199]\n\t"
"v_fma_f32 v56, v156, s52, v56\n\t"
"v_fma_f32 v57, v157, s52, v57\n\t"
"v_mfma_f32_16x16x32_bf16 v[196:199], a[244:247], a[172:175], v[196:199]\n\t"
"buffer_load_dwordx4 v[240:243], off, s[80:83], s20 offset:1024 sc1\n\t"
"v_mfma_f32_16x16x32_bf16 v[204:207], a[232:235], a[172:175], v[204:207]\n\t"
"buffer_load_dwordx4 v[96:99], off, s[80:83], s20 offset:2048 sc1\n\t"
"v_mfma_f32_16x16x32_bf16 v[204:207], a[244:247], a[176:179], v[204:207]\n\t"
"s_waitcnt lgkmcnt(5)\n\t"
"s_mov_b32 s20, 1351680\n\t"
"v_fma_f32 v58, v158, s52, v58\n\t"
"v_mfma_f32_16x16x32_bf16 v[204:207], a[236:239], a[180:183], v[204:207]\n\t"
"v_fma_f32 v59, v159, s52, v59\n\t"
"v_fma_f32 v156, v56, s54, -v156\n\t"
"v_mfma_f32_16x16x32_bf16 v[212:215], a[236:239], a[184:187], v[212:215]\n\t"
"s_waitcnt lgkmcnt(4)\n\t"
"v_fma_f32 v157, v57, s54, -v157\n\t"
"v_fma_f32 v158, v58, s54, -v158\n\t"
"v_mfma_f32_16x16x32_bf16 v[212:215], a[244:247], a[180:183], v[212:215]\n\t"
"v_fma_f32 v159, v59, s54, -v159\n\t"
"v_fma_f32 v60, v160, s52, v60\n\t"
"v_mfma_f32_16x16x32_bf16 v[212:215], a[232:235], a[176:179], v[212:215]\n\t"
"buffer_load_dwordx4 v[248:251], off, s[80:83], s20 offset:0 sc1\n\t"
"v_mfma_f32_16x16x32_bf16 a[0:3], a[232:235], a[180:183], a[0:3]\n\t"
"s_waitcnt lgkmcnt(3)\n\t"
"v_fma_f32 v61, v161, s52, v61\n\t"
"v_fma_f32 v62, v162, s52, v62\n\t"
"v_mfma_f32_16x16x32_bf16 a[0:3], a[244:247], a[184:187], a[0:3]\n\t"
"v_fma_f32 v63, v163, s52, v63\n\t"
"v_fma_f32 v160, v60, s54, -v160\n\t"
"v_mfma_f32_16x16x32_bf16 a[0:3], a[236:239], a[188:191], a[0:3]\n\t"
"v_fma_f32 v161, v61, s54, -v161\n\t"
"v_fma_f32 v162, v62, s54, -v162\n\t"
"v_mfma_f32_16x16x32_bf16 a[8:11], a[236:239], a[192:195], a[8:11]\n\t"
"s_waitcnt lgkmcnt(2)\n\t"
"v_fma_f32 v163, v63, s54, -v163\n\t"
"v_fma_f32 v64, v164, s52, v64\n\t"
"v_mfma_f32_16x16x32_bf16 a[8:11], a[244:247], a[188:191], a[8:11]\n\t"
"v_fma_f32 v65, v165, s52, v65\n\t"
"v_fma_f32 v66, v166, s52, v66\n\t"
"v_mfma_f32_16x16x32_bf16 a[8:11], a[232:235], a[184:187], a[8:11]\n\t"
"v_fma_f32 v67, v167, s52, v67\n\t"
"v_fma_f32 v164, v64, s54, -v164\n\t"
"v_mfma_f32_16x16x32_bf16 a[16:19], a[232:235], a[188:191], a[16:19]\n\t"
"s_waitcnt lgkmcnt(1)\n\t"
"v_fma_f32 v165, v65, s54, -v165\n\t"
"v_fma_f32 v166, v66, s54, -v166\n\t"
"v_mfma_f32_16x16x32_bf16 a[16:19], a[244:247], a[192:195], a[16:19]\n\t"
"v_fma_f32 v167, v67, s54, -v167\n\t"
"v_fma_f32 v68, v168, s52, v68\n\t"
"v_mfma_f32_16x16x32_bf16 a[16:19], a[236:239], a[196:199], a[16:19]\n\t"
"v_fma_f32 v69, v169, s52, v69\n\t"
"v_fma_f32 v70, v170, s52, v70\n\t"
"v_mfma_f32_16x16x32_bf16 a[24:27], a[236:239], a[200:203], a[24:27]\n\t"
"s_waitcnt lgkmcnt(0)\n\t"
"v_fma_f32 v71, v171, s52, v71\n\t"
"v_fma_f32 v168, v68, s54, -v168\n\t"
"v_mfma_f32_16x16x32_bf16 a[24:27], a[244:247], a[196:199], a[24:27]\n\t"
"v_fma_f32 v169, v69, s54, -v169\n\t"
"v_fma_f32 v170, v70, s54, -v170\n\t"
"v_mfma_f32_16x16x32_bf16 a[24:27], a[232:235], a[192:195], a[24:27]\n\t"
"v_fma_f32 v171, v71, s54, -v171\n\t"
"v_fma_f32 v72, v172, s52, v72\n\t"
"v_mfma_f32_16x16x32_bf16 a[32:35], a[232:235], a[196:199], a[32:35]\n\t"
"s_waitcnt vmcnt(10)\n\t"
"v_fma_f32 v73, v173, s52, v73\n\t"
"v_fma_f32 v74, v174, s52, v74\n\t"
"v_mfma_f32_16x16x32_bf16 a[32:35], a[244:247], a[200:203], a[32:35]\n\t"
"v_fma_f32 v75, v175, s52, v75\n\t"
"v_fma_f32 v172, v72, s54, -v172\n\t"
"v_mfma_f32_16x16x32_bf16 a[32:35], a[236:239], a[204:207], a[32:35]\n\t"
"v_fma_f32 v173, v73, s54, -v173\n\t"
"v_fma_f32 v174, v74, s54, -v174\n\t"
"v_mfma_f32_16x16x32_bf16 a[40:43], a[240:243], a[168:171], a[40:43]\n\t"
"v_fma_f32 v175, v75, s54, -v175\n\t"
"v_fma_f32 v76, v176, s52, v76\n\t"
"v_mfma_f32_16x16x32_bf16 a[40:43], a[212:215], a[176:179], a[40:43]\n\t"
"v_fma_f32 v77, v177, s52, v77\n\t"
"v_fma_f32 v78, v178, s52, v78\n\t"
"v_mfma_f32_16x16x32_bf16 a[40:43], a[248:251], a[172:175], a[40:43]\n\t"
"buffer_load_dwordx4 a[232:235], off, s[80:83], s20 offset:1024 sc1\n\t"
"v_mfma_f32_16x16x32_bf16 a[48:51], a[240:243], a[172:175], a[48:51]\n\t"
"buffer_load_dwordx4 a[236:239], off, s[80:83], s20 offset:2048 sc1\n\t"
"v_mfma_f32_16x16x32_bf16 a[48:51], a[248:251], a[176:179], a[48:51]\n\t"
"s_mov_b32 s20, 1585152\n\t"
"v_fma_f32 v79, v179, s52, v79\n\t"
"v_fma_f32 v176, v76, s54, -v176\n\t"
"v_mfma_f32_16x16x32_bf16 a[48:51], a[212:215], a[180:183], a[48:51]\n\t"
"v_fma_f32 v177, v77, s54, -v177\n\t"
"v_fma_f32 v178, v78, s54, -v178\n\t"
"v_mfma_f32_16x16x32_bf16 a[56:59], a[212:215], a[184:187], a[56:59]\n\t"
"v_fma_f32 v179, v79, s54, -v179\n\t"
"v_fma_f32 v80, v180, s52, v80\n\t"
"v_mfma_f32_16x16x32_bf16 a[56:59], a[248:251], a[180:183], a[56:59]\n\t"
"v_fma_f32 v81, v181, s52, v81\n\t"
"v_fma_f32 v82, v182, s52, v82\n\t"
"v_mfma_f32_16x16x32_bf16 a[56:59], a[240:243], a[176:179], a[56:59]\n\t"
"buffer_load_dwordx4 a[244:247], off, s[80:83], s20 offset:0 sc1\n\t"
"v_mfma_f32_16x16x32_bf16 a[64:67], a[240:243], a[180:183], a[64:67]\n\t"
"v_fma_f32 v83, v183, s52, v83\n\t"
"v_fma_f32 v180, v80, s54, -v180\n\t"
"v_mfma_f32_16x16x32_bf16 a[64:67], a[248:251], a[184:187], a[64:67]\n\t"
"v_fma_f32 v181, v81, s54, -v181\n\t"
"v_fma_f32 v182, v82, s54, -v182\n\t"
"v_mfma_f32_16x16x32_bf16 a[64:67], a[212:215], a[188:191], a[64:67]\n\t"
"v_fma_f32 v183, v83, s54, -v183\n\t"
"v_fma_f32 v84, v184, s52, v84\n\t"
"v_mfma_f32_16x16x32_bf16 a[72:75], a[212:215], a[192:195], a[72:75]\n\t"
"v_fma_f32 v85, v185, s52, v85\n\t"
"v_fma_f32 v86, v186, s52, v86\n\t"
"v_mfma_f32_16x16x32_bf16 a[72:75], a[248:251], a[188:191], a[72:75]\n\t"
"v_fma_f32 v87, v187, s52, v87\n\t"
"v_fma_f32 v184, v84, s54, -v184\n\t"
"v_mfma_f32_16x16x32_bf16 a[72:75], a[240:243], a[184:187], a[72:75]\n\t"
"v_fma_f32 v185, v85, s54, -v185\n\t"
"v_fma_f32 v186, v86, s54, -v186\n\t"
"v_mfma_f32_16x16x32_bf16 a[80:83], a[240:243], a[188:191], a[80:83]\n\t"
"v_fma_f32 v187, v87, s54, -v187\n\t"
"v_fma_f32 v88, v188, s52, v88\n\t"
"v_mfma_f32_16x16x32_bf16 a[80:83], a[248:251], a[192:195], a[80:83]\n\t"
"v_fma_f32 v89, v189, s52, v89\n\t"
"v_fma_f32 v90, v190, s52, v90\n\t"
"v_mfma_f32_16x16x32_bf16 a[80:83], a[212:215], a[196:199], a[80:83]\n\t"
"v_fma_f32 v91, v191, s52, v91\n\t"
"v_fma_f32 v188, v88, s54, -v188\n\t"
"v_mfma_f32_16x16x32_bf16 a[88:91], a[212:215], a[200:203], a[88:91]\n\t"
"v_fma_f32 v189, v89, s54, -v189\n\t"
"v_fma_f32 v190, v90, s54, -v190\n\t"
"v_mfma_f32_16x16x32_bf16 a[88:91], a[248:251], a[196:199], a[88:91]\n\t"
"v_fma_f32 v191, v91, s54, -v191\n\t"
"v_fma_f32 v92, v192, s52, v92\n\t"
"v_mfma_f32_16x16x32_bf16 a[88:91], a[240:243], a[192:195], a[88:91]\n\t"
"v_fma_f32 v93, v193, s52, v93\n\t"
"v_fma_f32 v94, v194, s52, v94\n\t"
"v_mfma_f32_16x16x32_bf16 a[96:99], a[240:243], a[196:199], a[96:99]\n\t"
"s_waitcnt vmcnt(10)\n\t"
"v_fma_f32 v95, v195, s52, v95\n\t"
"v_fma_f32 v192, v92, s54, -v192\n\t"
"v_mfma_f32_16x16x32_bf16 a[96:99], a[248:251], a[200:203], a[96:99]\n\t"
"v_fma_f32 v193, v93, s54, -v193\n\t"
"v_fma_f32 v194, v94, s54, -v194\n\t"
"v_mfma_f32_16x16x32_bf16 a[96:99], a[212:215], a[204:207], a[96:99]\n\t"
"v_fma_f32 v195, v95, s54, -v195\n\t"
"v_mfma_f32_16x16x32_bf16 a[104:107], a[216:219], a[168:171], a[104:107]\n\t"
"buffer_load_dwordx4 a[240:243], off, s[80:83], s20 offset:1024 sc1\n\t"
"v_mfma_f32_16x16x32_bf16 a[104:107], a[220:223], a[176:179], a[104:107]\n\t"
"buffer_load_dwordx4 a[212:215], off, s[80:83], s20 offset:2048 sc1\n\t"
"v_mfma_f32_16x16x32_bf16 a[104:107], v[236:239], a[172:175], a[104:107]\n\t"
"s_mov_b32 s20, 1597440\n\t"
"ds_read_b128 a[168:171], v222 offset:1280\n\t"
"v_mfma_f32_16x16x32_bf16 a[112:115], a[216:219], a[172:175], a[112:115]\n\t"
"buffer_load_dwordx4 a[248:251], off, s[80:83], s20 offset:0 sc1\n\t"
"v_mfma_f32_16x16x32_bf16 a[112:115], v[236:239], a[176:179], a[112:115]\n\t"
"ds_read_b128 a[172:175], v223 offset:7424\n\t"
"v_mfma_f32_16x16x32_bf16 a[112:115], a[220:223], a[180:183], a[112:115]\n\t"
"v_mfma_f32_16x16x32_bf16 a[120:123], a[220:223], a[184:187], a[120:123]\n\t"
"v_mfma_f32_16x16x32_bf16 a[120:123], v[236:239], a[180:183], a[120:123]\n\t"
"v_mfma_f32_16x16x32_bf16 a[120:123], a[216:219], a[176:179], a[120:123]\n\t"
"ds_read_b128 a[176:179], v223 offset:13568\n\t"
"v_mfma_f32_16x16x32_bf16 a[128:131], a[216:219], a[180:183], a[128:131]\n\t"
"ds_read_b128 a[180:183], v222 offset:21248\n\t"
"v_mfma_f32_16x16x32_bf16 a[128:131], v[236:239], a[184:187], a[128:131]\n\t"
"v_mfma_f32_16x16x32_bf16 a[128:131], a[220:223], a[188:191], a[128:131]\n\t"
"v_mfma_f32_16x16x32_bf16 a[136:139], a[220:223], a[192:195], a[136:139]\n\t"
"v_mfma_f32_16x16x32_bf16 a[136:139], v[236:239], a[188:191], a[136:139]\n\t"
"v_mfma_f32_16x16x32_bf16 a[136:139], a[216:219], a[184:187], a[136:139]\n\t"
"ds_read_b128 a[184:187], v223 offset:27392\n\t"
"v_mfma_f32_16x16x32_bf16 a[144:147], a[216:219], a[188:191], a[144:147]\n\t"
"ds_read_b128 a[188:191], v223 offset:33536\n\t"
"v_mfma_f32_16x16x32_bf16 a[144:147], v[236:239], a[192:195], a[144:147]\n\t"
"v_mfma_f32_16x16x32_bf16 a[144:147], a[220:223], a[196:199], a[144:147]\n\t"
"v_mfma_f32_16x16x32_bf16 a[152:155], a[220:223], a[200:203], a[152:155]\n\t"
"v_mfma_f32_16x16x32_bf16 a[152:155], v[236:239], a[196:199], a[152:155]\n\t"
"v_mfma_f32_16x16x32_bf16 a[152:155], a[216:219], a[192:195], a[152:155]\n\t"
"ds_read_b128 a[192:195], v222 offset:41216\n\t"
"v_mfma_f32_16x16x32_bf16 a[160:163], a[216:219], a[196:199], a[160:163]\n\t"
"buffer_load_dwordx4 a[216:219], off, s[80:83], s20 offset:1024 sc1\n\t"
"v_mfma_f32_16x16x32_bf16 a[160:163], v[236:239], a[200:203], a[160:163]\n\t"
"s_waitcnt vmcnt(11) lgkmcnt(4)\n\t"
"ds_read_b128 a[196:199], v223 offset:47360\n\t"
"v_mfma_f32_16x16x32_bf16 a[160:163], a[220:223], a[204:207], a[160:163]\n\t"
"ds_read_b128 a[200:203], v223 offset:53504\n\t"
"v_mfma_f32_16x16x32_bf16 v[200:203], a[224:227], a[168:171], v[200:203]\n\t"
"buffer_load_dwordx4 v[236:239], off, s[80:83], s20 offset:2048 sc1\n\t"
"v_mfma_f32_16x16x32_bf16 v[200:203], a[208:211], a[176:179], v[200:203]\n\t"
"s_waitcnt lgkmcnt(5)\n\t"
"ds_read_b128 a[204:207], v222 offset:61184\n\t"
"s_mov_b32 s20, 1609728\n\t"
"v_mfma_f32_16x16x32_bf16 v[200:203], a[252:255], a[172:175], v[200:203]\n\t"
"v_mfma_f32_16x16x32_bf16 v[208:211], a[224:227], a[172:175], v[208:211]\n\t"
"buffer_load_dwordx4 a[220:223], off, s[80:83], s20 offset:0 sc1\n\t"
"v_mfma_f32_16x16x32_bf16 v[208:211], a[252:255], a[176:179], v[208:211]\n\t"
"s_waitcnt lgkmcnt(5)\n\t"
"v_mfma_f32_16x16x32_bf16 v[208:211], a[208:211], a[180:183], v[208:211]\n\t"
"v_mfma_f32_16x16x32_bf16 v[216:219], a[208:211], a[184:187], v[216:219]\n\t"
"s_waitcnt lgkmcnt(4)\n\t"
"v_mfma_f32_16x16x32_bf16 v[216:219], a[252:255], a[180:183], v[216:219]\n\t"
"v_mfma_f32_16x16x32_bf16 v[216:219], a[224:227], a[176:179], v[216:219]\n\t"
"v_mfma_f32_16x16x32_bf16 a[4:7], a[224:227], a[180:183], a[4:7]\n\t"
"s_waitcnt lgkmcnt(3)\n\t"
"v_mfma_f32_16x16x32_bf16 a[4:7], a[252:255], a[184:187], a[4:7]\n\t"
"v_mfma_f32_16x16x32_bf16 a[4:7], a[208:211], a[188:191], a[4:7]\n\t"
"v_mfma_f32_16x16x32_bf16 a[12:15], a[208:211], a[192:195], a[12:15]\n\t"
"s_waitcnt lgkmcnt(2)\n\t"
"v_mfma_f32_16x16x32_bf16 a[12:15], a[252:255], a[188:191], a[12:15]\n\t"
"v_mfma_f32_16x16x32_bf16 a[12:15], a[224:227], a[184:187], a[12:15]\n\t"
"v_mfma_f32_16x16x32_bf16 a[20:23], a[224:227], a[188:191], a[20:23]\n\t"
"s_waitcnt lgkmcnt(1)\n\t"
"v_mfma_f32_16x16x32_bf16 a[20:23], a[252:255], a[192:195], a[20:23]\n\t"
"v_mfma_f32_16x16x32_bf16 a[20:23], a[208:211], a[196:199], a[20:23]\n\t"
"v_mfma_f32_16x16x32_bf16 a[28:31], a[208:211], a[200:203], a[28:31]\n\t"
"s_waitcnt lgkmcnt(0)\n\t"
"v_mfma_f32_16x16x32_bf16 a[28:31], a[252:255], a[196:199], a[28:31]\n\t"
"v_mfma_f32_16x16x32_bf16 a[28:31], a[224:227], a[192:195], a[28:31]\n\t"
"v_mfma_f32_16x16x32_bf16 a[36:39], a[224:227], a[196:199], a[36:39]\n\t"
"buffer_load_dwordx4 a[224:227], off, s[80:83], s20 offset:1024 sc1\n\t"
"v_mfma_f32_16x16x32_bf16 a[36:39], a[252:255], a[200:203], a[36:39]\n\t"
"s_waitcnt vmcnt(11)\n\t"
"v_mfma_f32_16x16x32_bf16 a[36:39], a[208:211], a[204:207], a[36:39]\n\t"
"v_mfma_f32_16x16x32_bf16 a[44:47], a[228:231], a[168:171], a[44:47]\n\t"
"buffer_load_dwordx4 a[208:211], off, s[80:83], s20 offset:2048 sc1\n\t"
"v_mfma_f32_16x16x32_bf16 a[44:47], v[96:99], a[176:179], a[44:47]\n\t"
"s_mov_b32 s20, 1622016\n\t"
"v_mfma_f32_16x16x32_bf16 a[44:47], v[240:243], a[172:175], a[44:47]\n\t"
"v_mfma_f32_16x16x32_bf16 a[52:55], a[228:231], a[172:175], a[52:55]\n\t"
"buffer_load_dwordx4 a[252:255], off, s[80:83], s20 offset:0 sc1\n\t"
"v_mfma_f32_16x16x32_bf16 a[52:55], v[240:243], a[176:179], a[52:55]\n\t"
"v_mfma_f32_16x16x32_bf16 a[52:55], v[96:99], a[180:183], a[52:55]\n\t"
"v_mfma_f32_16x16x32_bf16 a[60:63], v[96:99], a[184:187], a[60:63]\n\t"
"v_mfma_f32_16x16x32_bf16 a[60:63], v[240:243], a[180:183], a[60:63]\n\t"
"v_mfma_f32_16x16x32_bf16 a[60:63], a[228:231], a[176:179], a[60:63]\n\t"
"v_mfma_f32_16x16x32_bf16 a[68:71], a[228:231], a[180:183], a[68:71]\n\t"
"v_mfma_f32_16x16x32_bf16 a[68:71], v[240:243], a[184:187], a[68:71]\n\t"
"v_mfma_f32_16x16x32_bf16 a[68:71], v[96:99], a[188:191], a[68:71]\n\t"
"v_mfma_f32_16x16x32_bf16 a[76:79], v[96:99], a[192:195], a[76:79]\n\t"
"v_mfma_f32_16x16x32_bf16 a[76:79], v[240:243], a[188:191], a[76:79]\n\t"
"v_mfma_f32_16x16x32_bf16 a[76:79], a[228:231], a[184:187], a[76:79]\n\t"
"v_mfma_f32_16x16x32_bf16 a[84:87], a[228:231], a[188:191], a[84:87]\n\t"
"v_mfma_f32_16x16x32_bf16 a[84:87], v[240:243], a[192:195], a[84:87]\n\t"
"v_mfma_f32_16x16x32_bf16 a[84:87], v[96:99], a[196:199], a[84:87]\n\t"
"v_mfma_f32_16x16x32_bf16 a[92:95], v[96:99], a[200:203], a[92:95]\n\t"
"v_mfma_f32_16x16x32_bf16 a[92:95], v[240:243], a[196:199], a[92:95]\n\t"
"v_mfma_f32_16x16x32_bf16 a[92:95], a[228:231], a[192:195], a[92:95]\n\t"
"v_mfma_f32_16x16x32_bf16 a[100:103], a[228:231], a[196:199], a[100:103]\n\t"
"buffer_load_dwordx4 a[228:231], off, s[80:83], s20 offset:1024 sc1\n\t"
"v_mfma_f32_16x16x32_bf16 a[100:103], v[240:243], a[200:203], a[100:103]\n\t"
"s_waitcnt vmcnt(11)\n\t"
"v_mfma_f32_16x16x32_bf16 a[100:103], v[96:99], a[204:207], a[100:103]\n\t"
"v_mfma_f32_16x16x32_bf16 a[108:111], v[248:251], a[168:171], a[108:111]\n\t"
"buffer_load_dwordx4 v[240:243], off, s[80:83], s20 offset:2048 sc1\n\t"
"v_mfma_f32_16x16x32_bf16 a[108:111], a[236:239], a[176:179], a[108:111]\n\t"
"ds_read_b128 a[168:171], v222 offset:1536\n\t"
"v_mfma_f32_16x16x32_bf16 a[108:111], a[232:235], a[172:175], a[108:111]\n\t"
"v_mfma_f32_16x16x32_bf16 a[116:119], v[248:251], a[172:175], a[116:119]\n\t"
"ds_read_b128 a[172:175], v223 offset:7680\n\t"
"v_mfma_f32_16x16x32_bf16 a[116:119], a[232:235], a[176:179], a[116:119]\n\t"
"v_mfma_f32_16x16x32_bf16 a[116:119], a[236:239], a[180:183], a[116:119]\n\t"
"v_mfma_f32_16x16x32_bf16 a[124:127], a[236:239], a[184:187], a[124:127]\n\t"
"v_mfma_f32_16x16x32_bf16 a[124:127], a[232:235], a[180:183], a[124:127]\n\t"
"v_mfma_f32_16x16x32_bf16 a[124:127], v[248:251], a[176:179], a[124:127]\n\t"
"ds_read_b128 a[176:179], v223 offset:13824\n\t"
"v_mfma_f32_16x16x32_bf16 a[132:135], v[248:251], a[180:183], a[132:135]\n\t"
"ds_read_b128 a[180:183], v222 offset:21504\n\t"
"v_mfma_f32_16x16x32_bf16 a[132:135], a[232:235], a[184:187], a[132:135]\n\t"
"v_mfma_f32_16x16x32_bf16 a[132:135], a[236:239], a[188:191], a[132:135]\n\t"
"v_mfma_f32_16x16x32_bf16 a[140:143], a[236:239], a[192:195], a[140:143]\n\t"
"v_mfma_f32_16x16x32_bf16 a[140:143], a[232:235], a[188:191], a[140:143]\n\t"
"v_mfma_f32_16x16x32_bf16 a[140:143], v[248:251], a[184:187], a[140:143]\n\t"
"ds_read_b128 a[184:187], v223 offset:27648\n\t"
"v_mfma_f32_16x16x32_bf16 a[148:151], v[248:251], a[188:191], a[148:151]\n\t"
"s_mov_b32 s20, 1634304\n\t"
"ds_read_b128 a[188:191], v223 offset:33792\n\t"
"v_mfma_f32_16x16x32_bf16 a[148:151], a[232:235], a[192:195], a[148:151]\n\t"
"v_mfma_f32_16x16x32_bf16 a[148:151], a[236:239], a[196:199], a[148:151]\n\t"
"v_mfma_f32_16x16x32_bf16 a[156:159], a[236:239], a[200:203], a[156:159]\n\t"
"v_mfma_f32_16x16x32_bf16 a[156:159], a[232:235], a[196:199], a[156:159]\n\t"
"v_mfma_f32_16x16x32_bf16 a[156:159], v[248:251], a[192:195], a[156:159]\n\t"
"ds_read_b128 a[192:195], v222 offset:41472\n\t"
"v_mfma_f32_16x16x32_bf16 a[164:167], v[248:251], a[196:199], a[164:167]\n\t"
"buffer_load_dwordx4 v[248:251], off, s[80:83], s20 offset:0 sc1\n\t"
"v_mfma_f32_16x16x32_bf16 a[164:167], a[232:235], a[200:203], a[164:167]\n\t"
"s_waitcnt vmcnt(10) lgkmcnt(4)\n\t"
"ds_read_b128 a[196:199], v223 offset:47616\n\t"
"v_mfma_f32_16x16x32_bf16 a[164:167], a[236:239], a[204:207], a[164:167]\n\t"
"ds_read_b128 a[200:203], v223 offset:53760\n\t"
"Lst31_%=:\n\t"
"v_mfma_f32_16x16x32_bf16 v[196:199], a[244:247], a[168:171], v[196:199]\n\t"
"buffer_load_dwordx4 a[232:235], off, s[80:83], s20 offset:1024 sc1\n\t"
"v_mfma_f32_16x16x32_bf16 v[196:199], a[212:215], a[176:179], v[196:199]\n\t"
"s_waitcnt lgkmcnt(5)\n\t"
"ds_read_b128 a[204:207], v222 offset:61440\n\t"
"v_mfma_f32_16x16x32_bf16 v[196:199], a[240:243], a[172:175], v[196:199]\n\t"
"v_mfma_f32_16x16x32_bf16 v[204:207], a[244:247], a[172:175], v[204:207]\n\t"
"buffer_load_dwordx4 a[236:239], off, s[80:83], s20 offset:2048 sc1\n\t"
"v_mfma_f32_16x16x32_bf16 v[204:207], a[240:243], a[176:179], v[204:207]\n\t"
"s_waitcnt lgkmcnt(5)\n\t"
"v_mfma_f32_16x16x32_bf16 v[204:207], a[212:215], a[180:183], v[204:207]\n\t"
"v_mfma_f32_16x16x32_bf16 v[212:215], a[212:215], a[184:187], v[212:215]\n\t"
"s_waitcnt lgkmcnt(4)\n\t"
"v_mfma_f32_16x16x32_bf16 v[212:215], a[240:243], a[180:183], v[212:215]\n\t"
"v_mfma_f32_16x16x32_bf16 v[212:215], a[244:247], a[176:179], v[212:215]\n\t"
"v_mfma_f32_16x16x32_bf16 a[0:3], a[244:247], a[180:183], a[0:3]\n\t"
"s_waitcnt lgkmcnt(3)\n\t"
"v_mfma_f32_16x16x32_bf16 a[0:3], a[240:243], a[184:187], a[0:3]\n\t"
"v_mfma_f32_16x16x32_bf16 a[0:3], a[212:215], a[188:191], a[0:3]\n\t"
"v_mfma_f32_16x16x32_bf16 a[8:11], a[212:215], a[192:195], a[8:11]\n\t"
"s_waitcnt lgkmcnt(2)\n\t"
"v_mfma_f32_16x16x32_bf16 a[8:11], a[240:243], a[188:191], a[8:11]\n\t"
"v_mfma_f32_16x16x32_bf16 a[8:11], a[244:247], a[184:187], a[8:11]\n\t"
"v_mfma_f32_16x16x32_bf16 a[16:19], a[244:247], a[188:191], a[16:19]\n\t"
"s_waitcnt lgkmcnt(1)\n\t"
"s_mov_b32 s20, 1646592\n\t"
"v_mfma_f32_16x16x32_bf16 a[16:19], a[240:243], a[192:195], a[16:19]\n\t"
"v_mfma_f32_16x16x32_bf16 a[16:19], a[212:215], a[196:199], a[16:19]\n\t"
"v_mfma_f32_16x16x32_bf16 a[24:27], a[212:215], a[200:203], a[24:27]\n\t"
"s_waitcnt lgkmcnt(0)\n\t"
"v_mfma_f32_16x16x32_bf16 a[24:27], a[240:243], a[196:199], a[24:27]\n\t"
"v_mfma_f32_16x16x32_bf16 a[24:27], a[244:247], a[192:195], a[24:27]\n\t"
"v_mfma_f32_16x16x32_bf16 a[32:35], a[244:247], a[196:199], a[32:35]\n\t"
"buffer_load_dwordx4 a[244:247], off, s[80:83], s20 offset:0 sc1\n\t"
"v_mfma_f32_16x16x32_bf16 a[32:35], a[240:243], a[200:203], a[32:35]\n\t"
"s_waitcnt vmcnt(10)\n\t"
"v_mfma_f32_16x16x32_bf16 a[32:35], a[212:215], a[204:207], a[32:35]\n\t"
"v_mfma_f32_16x16x32_bf16 a[40:43], a[248:251], a[168:171], a[40:43]\n\t"
"buffer_load_dwordx4 a[212:215], off, s[80:83], s20 offset:1024 sc1\n\t"
"v_mfma_f32_16x16x32_bf16 a[40:43], v[236:239], a[176:179], a[40:43]\n\t"
"v_mfma_f32_16x16x32_bf16 a[40:43], a[216:219], a[172:175], a[40:43]\n\t"
"v_mfma_f32_16x16x32_bf16 a[48:51], a[248:251], a[172:175], a[48:51]\n\t"
"buffer_load_dwordx4 a[240:243], off, s[80:83], s20 offset:2048 sc1\n\t"
"v_mfma_f32_16x16x32_bf16 a[48:51], a[216:219], a[176:179], a[48:51]\n\t"
"v_mfma_f32_16x16x32_bf16 a[48:51], v[236:239], a[180:183], a[48:51]\n\t"
"v_mfma_f32_16x16x32_bf16 a[56:59], v[236:239], a[184:187], a[56:59]\n\t"
"v_mfma_f32_16x16x32_bf16 a[56:59], a[216:219], a[180:183], a[56:59]\n\t"
"v_mfma_f32_16x16x32_bf16 a[56:59], a[248:251], a[176:179], a[56:59]\n\t"
"v_mfma_f32_16x16x32_bf16 a[64:67], a[248:251], a[180:183], a[64:67]\n\t"
"v_mfma_f32_16x16x32_bf16 a[64:67], a[216:219], a[184:187], a[64:67]\n\t"
"v_mfma_f32_16x16x32_bf16 a[64:67], v[236:239], a[188:191], a[64:67]\n\t"
"v_mfma_f32_16x16x32_bf16 a[72:75], v[236:239], a[192:195], a[72:75]\n\t"
"v_mfma_f32_16x16x32_bf16 a[72:75], a[216:219], a[188:191], a[72:75]\n\t"
"v_mfma_f32_16x16x32_bf16 a[72:75], a[248:251], a[184:187], a[72:75]\n\t"
"s_waitcnt vmcnt(9)\n\t"
"v_mfma_f32_16x16x32_bf16 a[80:83], a[248:251], a[188:191], a[80:83]\n\t"
"s_mov_b32 s20, 884736\n\t"
"v_mfma_f32_16x16x32_bf16 a[80:83], a[216:219], a[192:195], a[80:83]\n\t"
"Lpg22_%=:\n\t"
"s_cbranch_vccnz Lvr3_%=\n\t"
"v_mfma_f32_16x16x32_bf16 a[80:83], v[236:239], a[196:199], a[80:83]\n\t"
"v_mfma_f32_16x16x32_bf16 a[88:91], v[236:239], a[200:203], a[88:91]\n\t"
"v_mfma_f32_16x16x32_bf16 a[88:91], a[216:219], a[196:199], a[88:91]\n\t"
"v_mfma_f32_16x16x32_bf16 a[88:91], a[248:251], a[192:195], a[88:91]\n\t"
"v_mfma_f32_16x16x32_bf16 a[96:99], a[248:251], a[196:199], a[96:99]\n\t"
"buffer_load_dwordx4 a[248:251], off, s[80:83], s20 offset:0 sc1\n\t"
"v_mfma_f32_16x16x32_bf16 a[96:99], a[216:219], a[200:203], a[96:99]\n\t"
"v_mfma_f32_16x16x32_bf16 a[96:99], v[236:239], a[204:207], a[96:99]\n\t"
"v_mfma_f32_16x16x32_bf16 a[104:107], a[220:223], a[168:171], a[104:107]\n\t"
"buffer_load_dwordx4 a[216:219], off, s[80:83], s20 offset:1024 sc1\n\t"
"v_mfma_f32_16x16x32_bf16 a[104:107], a[208:211], a[176:179], a[104:107]\n\t"
"ds_read_b128 a[168:171], v222 offset:1792\n\t"
"v_mfma_f32_16x16x32_bf16 a[104:107], a[224:227], a[172:175], a[104:107]\n\t"
"v_mfma_f32_16x16x32_bf16 a[112:115], a[220:223], a[172:175], a[112:115]\n\t"
"ds_read_b128 a[172:175], v223 offset:7936\n\t"
"v_mfma_f32_16x16x32_bf16 a[112:115], a[224:227], a[176:179], a[112:115]\n\t"
"v_mfma_f32_16x16x32_bf16 a[112:115], a[208:211], a[180:183], a[112:115]\n\t"
"v_mfma_f32_16x16x32_bf16 a[120:123], a[208:211], a[184:187], a[120:123]\n\t"
"v_mfma_f32_16x16x32_bf16 a[120:123], a[224:227], a[180:183], a[120:123]\n\t"
"v_mfma_f32_16x16x32_bf16 a[120:123], a[220:223], a[176:179], a[120:123]\n\t"
"ds_read_b128 a[176:179], v223 offset:14080\n\t"
"v_mfma_f32_16x16x32_bf16 a[128:131], a[220:223], a[180:183], a[128:131]\n\t"
"ds_read_b128 a[180:183], v222 offset:21760\n\t"
"v_mfma_f32_16x16x32_bf16 a[128:131], a[224:227], a[184:187], a[128:131]\n\t"
"v_mfma_f32_16x16x32_bf16 a[128:131], a[208:211], a[188:191], a[128:131]\n\t"
"v_mfma_f32_16x16x32_bf16 a[136:139], a[208:211], a[192:195], a[136:139]\n\t"
"v_mfma_f32_16x16x32_bf16 a[136:139], a[224:227], a[188:191], a[136:139]\n\t"
"v_mfma_f32_16x16x32_bf16 a[136:139], a[220:223], a[184:187], a[136:139]\n\t"
"ds_read_b128 a[184:187], v223 offset:27904\n\t"
"v_mfma_f32_16x16x32_bf16 a[144:147], a[220:223], a[188:191], a[144:147]\n\t"
"ds_read_b128 a[188:191], v223 offset:34048\n\t"
"v_mfma_f32_16x16x32_bf16 a[144:147], a[224:227], a[192:195], a[144:147]\n\t"
"v_mfma_f32_16x16x32_bf16 a[144:147], a[208:211], a[196:199], a[144:147]\n\t"
"v_mfma_f32_16x16x32_bf16 a[152:155], a[208:211], a[200:203], a[152:155]\n\t"
"v_mfma_f32_16x16x32_bf16 a[152:155], a[224:227], a[196:199], a[152:155]\n\t"
"v_mfma_f32_16x16x32_bf16 a[152:155], a[220:223], a[192:195], a[152:155]\n\t"
"ds_read_b128 a[192:195], v222 offset:41728\n\t"
"v_mfma_f32_16x16x32_bf16 a[160:163], a[220:223], a[196:199], a[160:163]\n\t"
"buffer_load_dwordx4 a[220:223], off, s[80:83], s20 offset:2048 sc1\n\t"
"v_mfma_f32_16x16x32_bf16 a[160:163], a[224:227], a[200:203], a[160:163]\n\t"
"s_waitcnt vmcnt(9) lgkmcnt(4)\n\t"
"ds_read_b128 a[196:199], v223 offset:47872\n\t"
"s_mov_b32 s20, 897024\n\t"
"v_mfma_f32_16x16x32_bf16 a[160:163], a[208:211], a[204:207], a[160:163]\n\t"
"ds_read_b128 a[200:203], v223 offset:54016\n\t"
"v_mfma_f32_16x16x32_bf16 v[200:203], a[252:255], a[168:171], v[200:203]\n\t"
"s_waitcnt lgkmcnt(5)\n\t"
"ds_read_b128 a[204:207], v222 offset:61696\n\t"
"v_mfma_f32_16x16x32_bf16 v[200:203], v[240:243], a[176:179], v[200:203]\n\t"
"buffer_load_dwordx4 a[208:211], off, s[80:83], s20 offset:0 sc1\n\t"
"v_mfma_f32_16x16x32_bf16 v[200:203], a[228:231], a[172:175], v[200:203]\n\t"
"v_mfma_f32_16x16x32_bf16 v[208:211], a[252:255], a[172:175], v[208:211]\n\t"
"buffer_load_dwordx4 a[224:227], off, s[80:83], s20 offset:1024 sc1\n\t"
"v_mfma_f32_16x16x32_bf16 v[208:211], a[228:231], a[176:179], v[208:211]\n\t"
"s_waitcnt lgkmcnt(5)\n\t"
"v_mfma_f32_16x16x32_bf16 v[208:211], v[240:243], a[180:183], v[208:211]\n\t"
"s_waitcnt lgkmcnt(0)\n\t"
"s_barrier\n\t"
"v_mfma_f32_16x16x32_bf16 v[216:219], v[240:243], a[184:187], v[216:219]\n\t"
"v_mfma_f32_16x16x32_bf16 v[216:219], a[228:231], a[180:183], v[216:219]\n\t"
"v_mfma_f32_16x16x32_bf16 v[216:219], a[252:255], a[176:179], v[216:219]\n\t"
"v_pk_fma_f32 v[196:197], v[200:201], s[56:57], v[196:197]\n\t"
"v_pk_fma_f32 v[200:201], v[196:197], s[58:59], v[200:201] neg_lo:[0,0,1] neg_hi:[0,0,1]\n\t"
"v_pk_fma_f32 v[228:229], v[0:1], s[60:61], v[196:197]\n\t"
"v_pk_fma_f32 v[0:1], v[196:197], s[60:61], v[0:1]\n\t"
"v_pk_fma_f32 v[196:197], v[100:101], s[62:63], v[200:201]\n\t"
"v_pk_fma_f32 v[100:101], v[200:201], s[62:63], v[100:101]\n\t"
"v_pk_fma_f32 v[198:199], v[202:203], s[56:57], v[198:199]\n\t"
"v_pk_fma_f32 v[202:203], v[198:199], s[58:59], v[202:203] neg_lo:[0,0,1] neg_hi:[0,0,1]\n\t"
"v_pk_fma_f32 v[234:235], v[2:3], s[60:61], v[198:199]\n\t"
"v_pk_fma_f32 v[2:3], v[198:199], s[60:61], v[2:3]\n\t"
"v_pk_fma_f32 v[198:199], v[102:103], s[62:63], v[202:203]\n\t"
"v_pk_fma_f32 v[102:103], v[202:203], s[62:63], v[102:103]\n\t"
"v_mfma_f32_16x16x32_bf16 a[4:7], a[252:255], a[180:183], a[4:7]\n\t"
"v_cvt_pk_bf16_f32 v196, v196, v197\n\t"
"v_cvt_pk_bf16_f32 v200, v228, v229\n\t"
"v_mfma_f32_16x16x32_bf16 a[4:7], a[228:231], a[184:187], a[4:7]\n\t"
"v_cvt_pk_bf16_f32 v197, v198, v199\n\t"
"v_cvt_pk_bf16_f32 v201, v234, v235\n\t"
"v_mfma_f32_16x16x32_bf16 a[4:7], v[240:243], a[188:191], a[4:7]\n\t"
"v_pk_fma_f32 v[204:205], v[208:209], s[56:57], v[204:205]\n\t"
"v_pk_fma_f32 v[208:209], v[204:205], s[58:59], v[208:209] neg_lo:[0,0,1] neg_hi:[0,0,1]\n\t"
"v_pk_fma_f32 v[98:99], v[4:5], s[60:61], v[204:205]\n\t"
"v_pk_fma_f32 v[4:5], v[204:205], s[60:61], v[4:5]\n\t"
"v_pk_fma_f32 v[204:205], v[104:105], s[62:63], v[208:209]\n\t"
"v_pk_fma_f32 v[104:105], v[208:209], s[62:63], v[104:105]\n\t"
"v_mfma_f32_16x16x32_bf16 a[12:15], v[240:243], a[192:195], a[12:15]\n\t"
"v_cvt_pk_bf16_f32 v204, v204, v205\n\t"
"v_cvt_pk_bf16_f32 v208, v98, v99\n\t"
"v_mfma_f32_16x16x32_bf16 a[12:15], a[228:231], a[188:191], a[12:15]\n\t"
"v_mfma_f32_16x16x32_bf16 a[12:15], a[252:255], a[184:187], a[12:15]\n\t"
"v_pk_fma_f32 v[206:207], v[210:211], s[56:57], v[206:207]\n\t"
"v_pk_fma_f32 v[210:211], v[206:207], s[58:59], v[210:211] neg_lo:[0,0,1] neg_hi:[0,0,1]\n\t"
"v_pk_fma_f32 v[228:229], v[6:7], s[60:61], v[206:207]\n\t"
"v_pk_fma_f32 v[6:7], v[206:207], s[60:61], v[6:7]\n\t"
"v_pk_fma_f32 v[206:207], v[106:107], s[62:63], v[210:211]\n\t"
"v_pk_fma_f32 v[106:107], v[210:211], s[62:63], v[106:107]\n\t"
"v_mfma_f32_16x16x32_bf16 a[20:23], a[252:255], a[188:191], a[20:23]\n\t"
"v_cvt_pk_bf16_f32 v205, v206, v207\n\t"
"v_cvt_pk_bf16_f32 v209, v228, v229\n\t"
"v_mfma_f32_16x16x32_bf16 a[20:23], a[228:231], a[192:195], a[20:23]\n\t"
"v_accvgpr_read_b32 v224, a0\n\t"
"v_accvgpr_read_b32 v226, a4\n\t"
"v_mfma_f32_16x16x32_bf16 a[20:23], v[240:243], a[196:199], a[20:23]\n\t"
"v_accvgpr_read_b32 v225, a1\n\t"
"v_accvgpr_read_b32 v227, a5\n\t"
"v_pk_fma_f32 v[212:213], v[216:217], s[56:57], v[212:213]\n\t"
"v_pk_fma_f32 v[216:217], v[212:213], s[58:59], v[216:217] neg_lo:[0,0,1] neg_hi:[0,0,1]\n\t"
"v_pk_fma_f32 v[234:235], v[8:9], s[60:61], v[212:213]\n\t"
"v_pk_fma_f32 v[8:9], v[212:213], s[60:61], v[8:9]\n\t"
"v_pk_fma_f32 v[212:213], v[108:109], s[62:63], v[216:217]\n\t"
"v_pk_fma_f32 v[108:109], v[216:217], s[62:63], v[108:109]\n\t"
"v_mfma_f32_16x16x32_bf16 a[28:31], v[240:243], a[200:203], a[28:31]\n\t"
"v_cvt_pk_bf16_f32 v212, v212, v213\n\t"
"v_cvt_pk_bf16_f32 v216, v234, v235\n\t"
"v_mfma_f32_16x16x32_bf16 a[28:31], a[228:231], a[196:199], a[28:31]\n\t"
"v_accvgpr_read_b32 v230, a2\n\t"
"v_accvgpr_read_b32 v232, a6\n\t"
"v_mfma_f32_16x16x32_bf16 a[28:31], a[252:255], a[192:195], a[28:31]\n\t"
"v_accvgpr_read_b32 v231, a3\n\t"
"v_accvgpr_read_b32 v233, a7\n\t"
"v_pk_fma_f32 v[214:215], v[218:219], s[56:57], v[214:215]\n\t"
"v_pk_fma_f32 v[218:219], v[214:215], s[58:59], v[218:219] neg_lo:[0,0,1] neg_hi:[0,0,1]\n\t"
"v_pk_fma_f32 v[98:99], v[10:11], s[60:61], v[214:215]\n\t"
"v_pk_fma_f32 v[10:11], v[214:215], s[60:61], v[10:11]\n\t"
"v_pk_fma_f32 v[214:215], v[110:111], s[62:63], v[218:219]\n\t"
"v_pk_fma_f32 v[110:111], v[218:219], s[62:63], v[110:111]\n\t"
"v_mfma_f32_16x16x32_bf16 a[36:39], a[252:255], a[196:199], a[36:39]\n\t"
"s_waitcnt vmcnt(8)\n\t"
"v_cvt_pk_bf16_f32 v213, v214, v215\n\t"
"v_cvt_pk_bf16_f32 v217, v98, v99\n\t"
"v_mfma_f32_16x16x32_bf16 a[36:39], a[228:231], a[200:203], a[36:39]\n\t"
"v_accvgpr_read_b32 v236, a8\n\t"
"v_accvgpr_read_b32 v238, a12\n\t"
"v_mfma_f32_16x16x32_bf16 a[36:39], v[240:243], a[204:207], a[36:39]\n\t"
"buffer_load_dwordx4 a[252:255], off, s[80:83], s20 offset:2048 sc1\n\t"
"v_accvgpr_read_b32 v237, a9\n\t"
"v_accvgpr_read_b32 v239, a13\n\t"
"s_mov_b32 s20, 909312\n\t"
"v_pk_fma_f32 v[224:225], v[226:227], s[56:57], v[224:225]\n\t"
"v_pk_fma_f32 v[226:227], v[224:225], s[58:59], v[226:227] neg_lo:[0,0,1] neg_hi:[0,0,1]\n\t"
"v_pk_fma_f32 v[228:229], v[12:13], s[60:61], v[224:225]\n\t"
"v_pk_fma_f32 v[12:13], v[224:225], s[60:61], v[12:13]\n\t"
"v_pk_fma_f32 v[224:225], v[112:113], s[62:63], v[226:227]\n\t"
"v_pk_fma_f32 v[112:113], v[226:227], s[62:63], v[112:113]\n\t"
"v_mfma_f32_16x16x32_bf16 a[44:47], v[248:251], a[168:171], a[44:47]\n\t"
"v_cvt_pk_bf16_f32 v224, v224, v225\n\t"
"v_cvt_pk_bf16_f32 v228, v228, v229\n\t"
"v_mfma_f32_16x16x32_bf16 a[44:47], a[236:239], a[176:179], a[44:47]\n\t"
"v_accvgpr_write_b32 a0, v224\n\t"
"v_accvgpr_write_b32 a4, v228\n\t"
"v_mfma_f32_16x16x32_bf16 a[44:47], a[232:235], a[172:175], a[44:47]\n\t"
"buffer_load_dwordx4 a[228:231], off, s[80:83], s20 offset:0 sc1\n\t"
"v_accvgpr_read_b32 v224, a10\n\t"
"v_accvgpr_read_b32 v226, a14\n\t"
"v_pk_fma_f32 v[230:231], v[232:233], s[56:57], v[230:231]\n\t"
"v_pk_fma_f32 v[232:233], v[230:231], s[58:59], v[232:233] neg_lo:[0,0,1] neg_hi:[0,0,1]\n\t"
"v_pk_fma_f32 v[234:235], v[14:15], s[60:61], v[230:231]\n\t"
"v_pk_fma_f32 v[14:15], v[230:231], s[60:61], v[14:15]\n\t"
"v_pk_fma_f32 v[230:231], v[114:115], s[62:63], v[232:233]\n\t"
"v_pk_fma_f32 v[114:115], v[232:233], s[62:63], v[114:115]\n\t"
"v_mfma_f32_16x16x32_bf16 a[52:55], v[248:251], a[172:175], a[52:55]\n\t"
"v_cvt_pk_bf16_f32 v230, v230, v231\n\t"
"v_cvt_pk_bf16_f32 v234, v234, v235\n\t"
"v_mfma_f32_16x16x32_bf16 a[52:55], a[232:235], a[176:179], a[52:55]\n\t"
"v_accvgpr_write_b32 a1, v230\n\t"
"v_accvgpr_write_b32 a5, v234\n\t"
"v_mfma_f32_16x16x32_bf16 a[52:55], a[236:239], a[180:183], a[52:55]\n\t"
"buffer_load_dwordx4 v[240:243], off, s[80:83], s20 offset:1024 sc1\n\t"
"v_accvgpr_read_b32 v225, a11\n\t"
"v_accvgpr_read_b32 v227, a15\n\t"
"v_pk_fma_f32 v[236:237], v[238:239], s[56:57], v[236:237]\n\t"
"v_pk_fma_f32 v[238:239], v[236:237], s[58:59], v[238:239] neg_lo:[0,0,1] neg_hi:[0,0,1]\n\t"
"v_pk_fma_f32 v[98:99], v[16:17], s[60:61], v[236:237]\n\t"
"v_pk_fma_f32 v[16:17], v[236:237], s[60:61], v[16:17]\n\t"
"v_pk_fma_f32 v[236:237], v[116:117], s[62:63], v[238:239]\n\t"
"v_pk_fma_f32 v[116:117], v[238:239], s[62:63], v[116:117]\n\t"
"v_mfma_f32_16x16x32_bf16 a[60:63], a[236:239], a[184:187], a[60:63]\n\t"
"v_accvgpr_read_b32 v230, a16\n\t"
"v_accvgpr_read_b32 v232, a20\n\t"
"v_mfma_f32_16x16x32_bf16 a[60:63], a[232:235], a[180:183], a[60:63]\n\t"
"v_accvgpr_read_b32 v231, a17\n\t"
"v_accvgpr_read_b32 v233, a21\n\t"
"v_mfma_f32_16x16x32_bf16 a[60:63], v[248:251], a[176:179], a[60:63]\n\t"
"v_cvt_pk_bf16_f32 v236, v236, v237\n\t"
"v_cvt_pk_bf16_f32 v98, v98, v99\n\t"
"v_accvgpr_write_b32 a8, v236\n\t"
"v_accvgpr_write_b32 a12, v98\n\t"
"v_accvgpr_read_b32 v236, a18\n\t"
"v_pk_fma_f32 v[224:225], v[226:227], s[56:57], v[224:225]\n\t"
"v_pk_fma_f32 v[226:227], v[224:225], s[58:59], v[226:227] neg_lo:[0,0,1] neg_hi:[0,0,1]\n\t"
"v_pk_fma_f32 v[228:229], v[18:19], s[60:61], v[224:225]\n\t"
"v_pk_fma_f32 v[18:19], v[224:225], s[60:61], v[18:19]\n\t"
"v_pk_fma_f32 v[224:225], v[118:119], s[62:63], v[226:227]\n\t"
"v_pk_fma_f32 v[118:119], v[226:227], s[62:63], v[118:119]\n\t"
"v_pk_fma_f32 v[230:231], v[232:233], s[56:57], v[230:231]\n\t"
"v_pk_fma_f32 v[232:233], v[230:231], s[58:59], v[232:233] neg_lo:[0,0,1] neg_hi:[0,0,1]\n\t"
"v_pk_fma_f32 v[234:235], v[20:21], s[60:61], v[230:231]\n\t"
"v_pk_fma_f32 v[20:21], v[230:231], s[60:61], v[20:21]\n\t"
"v_pk_fma_f32 v[230:231], v[120:121], s[62:63], v[232:233]\n\t"
"v_pk_fma_f32 v[120:121], v[232:233], s[62:63], v[120:121]\n\t"
"v_mfma_f32_16x16x32_bf16 a[68:71], v[248:251], a[180:183], a[68:71]\n\t"
"v_accvgpr_read_b32 v238, a22\n\t"
"v_accvgpr_read_b32 v237, a19\n\t"
"v_mfma_f32_16x16x32_bf16 a[68:71], a[232:235], a[184:187], a[68:71]\n\t"
"v_accvgpr_read_b32 v239, a23\n\t"
"v_cvt_pk_bf16_f32 v224, v224, v225\n\t"
"v_mfma_f32_16x16x32_bf16 a[68:71], a[236:239], a[188:191], a[68:71]\n\t"
"v_cvt_pk_bf16_f32 v228, v228, v229\n\t"
"v_cvt_pk_bf16_f32 v230, v230, v231\n\t"
"v_cvt_pk_bf16_f32 v234, v234, v235\n\t"
"v_accvgpr_write_b32 a9, v224\n\t"
"v_accvgpr_write_b32 a13, v228\n\t"
"v_pk_fma_f32 v[236:237], v[238:239], s[56:57], v[236:237]\n\t"
"v_pk_fma_f32 v[238:239], v[236:237], s[58:59], v[238:239] neg_lo:[0,0,1] neg_hi:[0,0,1]\n\t"
"v_pk_fma_f32 v[98:99], v[22:23], s[60:61], v[236:237]\n\t"
"v_pk_fma_f32 v[22:23], v[236:237], s[60:61], v[22:23]\n\t"
"v_pk_fma_f32 v[236:237], v[122:123], s[62:63], v[238:239]\n\t"
"v_pk_fma_f32 v[122:123], v[238:239], s[62:63], v[122:123]\n\t"
"v_mfma_f32_16x16x32_bf16 a[76:79], a[236:239], a[192:195], a[76:79]\n\t"
"v_accvgpr_write_b32 a16, v230\n\t"
"v_accvgpr_write_b32 a20, v234\n\t"
"v_mfma_f32_16x16x32_bf16 a[76:79], a[232:235], a[188:191], a[76:79]\n\t"
"v_accvgpr_read_b32 v224, a24\n\t"
"v_accvgpr_read_b32 v226, a28\n\t"
"v_mfma_f32_16x16x32_bf16 a[76:79], v[248:251], a[184:187], a[76:79]\n\t"
"v_accvgpr_read_b32 v225, a25\n\t"
"v_accvgpr_read_b32 v227, a29\n\t"
"v_cvt_pk_bf16_f32 v236, v236, v237\n\t"
"v_cvt_pk_bf16_f32 v98, v98, v99\n\t"
"v_accvgpr_write_b32 a17, v236\n\t"
"v_pk_fma_f32 v[224:225], v[226:227], s[56:57], v[224:225]\n\t"
"v_pk_fma_f32 v[226:227], v[224:225], s[58:59], v[226:227] neg_lo:[0,0,1] neg_hi:[0,0,1]\n\t"
"v_pk_fma_f32 v[228:229], v[24:25], s[60:61], v[224:225]\n\t"
"v_pk_fma_f32 v[24:25], v[224:225], s[60:61], v[24:25]\n\t"
"v_pk_fma_f32 v[224:225], v[124:125], s[62:63], v[226:227]\n\t"
"v_pk_fma_f32 v[124:125], v[226:227], s[62:63], v[124:125]\n\t"
"v_mfma_f32_16x16x32_bf16 a[84:87], v[248:251], a[188:191], a[84:87]\n\t"
"v_accvgpr_write_b32 a21, v98\n\t"
"v_accvgpr_read_b32 v230, a26\n\t"
"v_mfma_f32_16x16x32_bf16 a[84:87], a[232:235], a[192:195], a[84:87]\n\t"
"v_accvgpr_read_b32 v232, a30\n\t"
"v_accvgpr_read_b32 v231, a27\n\t"
"v_mfma_f32_16x16x32_bf16 a[84:87], a[236:239], a[196:199], a[84:87]\n\t"
"v_accvgpr_read_b32 v233, a31\n\t"
"v_cvt_pk_bf16_f32 v224, v224, v225\n\t"
"v_cvt_pk_bf16_f32 v228, v228, v229\n\t"
"v_accvgpr_write_b32 a24, v224\n\t"
"v_accvgpr_write_b32 a28, v228\n\t"
"v_pk_fma_f32 v[230:231], v[232:233], s[56:57], v[230:231]\n\t"
"v_pk_fma_f32 v[232:233], v[230:231], s[58:59], v[232:233] neg_lo:[0,0,1] neg_hi:[0,0,1]\n\t"
"v_pk_fma_f32 v[234:235], v[26:27], s[60:61], v[230:231]\n\t"
"v_pk_fma_f32 v[26:27], v[230:231], s[60:61], v[26:27]\n\t"
"v_pk_fma_f32 v[230:231], v[126:127], s[62:63], v[232:233]\n\t"
"v_pk_fma_f32 v[126:127], v[232:233], s[62:63], v[126:127]\n\t"
"v_mfma_f32_16x16x32_bf16 a[92:95], a[236:239], a[200:203], a[92:95]\n\t"
"v_accvgpr_read_b32 v236, a32\n\t"
"v_accvgpr_read_b32 v238, a36\n\t"
"v_mfma_f32_16x16x32_bf16 a[92:95], a[232:235], a[196:199], a[92:95]\n\t"
"v_accvgpr_read_b32 v237, a33\n\t"
"v_accvgpr_read_b32 v239, a37\n\t"
"v_mfma_f32_16x16x32_bf16 a[92:95], v[248:251], a[192:195], a[92:95]\n\t"
"v_cvt_pk_bf16_f32 v230, v230, v231\n\t"
"v_cvt_pk_bf16_f32 v234, v234, v235\n\t"
"v_accvgpr_write_b32 a25, v230\n\t"
"v_accvgpr_write_b32 a29, v234\n\t"
"v_accvgpr_read_b32 v224, a34\n\t"
"v_pk_fma_f32 v[236:237], v[238:239], s[56:57], v[236:237]\n\t"
"v_pk_fma_f32 v[238:239], v[236:237], s[58:59], v[238:239] neg_lo:[0,0,1] neg_hi:[0,0,1]\n\t"
"v_pk_fma_f32 v[98:99], v[28:29], s[60:61], v[236:237]\n\t"
"v_pk_fma_f32 v[28:29], v[236:237], s[60:61], v[28:29]\n\t"
"v_pk_fma_f32 v[236:237], v[128:129], s[62:63], v[238:239]\n\t"
"v_pk_fma_f32 v[128:129], v[238:239], s[62:63], v[128:129]\n\t"
"v_mfma_f32_16x16x32_bf16 a[100:103], v[248:251], a[196:199], a[100:103]\n\t"
"s_waitcnt vmcnt(8)\n\t"
"v_accvgpr_read_b32 v226, a38\n\t"
"v_accvgpr_read_b32 v225, a35\n\t"
"v_mfma_f32_16x16x32_bf16 a[100:103], a[232:235], a[200:203], a[100:103]\n\t"
"v_accvgpr_read_b32 v227, a39\n\t"
"v_cvt_pk_bf16_f32 v236, v236, v237\n\t"
"v_mfma_f32_16x16x32_bf16 a[100:103], a[236:239], a[204:207], a[100:103]\n\t"
"buffer_load_dwordx4 a[232:235], off, s[80:83], s20 offset:2048 sc1\n\t"
"v_cvt_pk_bf16_f32 v98, v98, v99\n\t"
"v_accvgpr_write_b32 a32, v236\n\t"
"v_pk_fma_f32 v[224:225], v[226:227], s[56:57], v[224:225]\n\t"
"v_pk_fma_f32 v[226:227], v[224:225], s[58:59], v[226:227] neg_lo:[0,0,1] neg_hi:[0,0,1]\n\t"
"v_pk_fma_f32 v[228:229], v[30:31], s[60:61], v[224:225]\n\t"
"v_pk_fma_f32 v[30:31], v[224:225], s[60:61], v[30:31]\n\t"
"v_pk_fma_f32 v[224:225], v[130:131], s[62:63], v[226:227]\n\t"
"v_pk_fma_f32 v[130:131], v[226:227], s[62:63], v[130:131]\n\t"
"v_mfma_f32_16x16x32_bf16 a[108:111], a[244:247], a[168:171], a[108:111]\n\t"
"s_mov_b32 s20, 1069056\n\t"
"ds_read_b128 a[168:171], v244 offset:0\n\t"
"v_accvgpr_write_b32 a36, v98\n\t"
"v_mfma_f32_16x16x32_bf16 a[108:111], a[240:243], a[176:179], a[108:111]\n\t"
"v_accvgpr_read_b32 v230, a40\n\t"
"v_accvgpr_read_b32 v232, a44\n\t"
"v_mfma_f32_16x16x32_bf16 a[108:111], a[212:215], a[172:175], a[108:111]\n\t"
"buffer_load_dwordx4 a[236:239], off, s[80:83], s20 offset:0 sc1\n\t"
"v_accvgpr_read_b32 v231, a41\n\t"
"v_accvgpr_read_b32 v233, a45\n\t"
"v_cvt_pk_bf16_f32 v224, v224, v225\n\t"
"v_cvt_pk_bf16_f32 v228, v228, v229\n\t"
"v_pk_fma_f32 v[230:231], v[232:233], s[56:57], v[230:231]\n\t"
"v_pk_fma_f32 v[232:233], v[230:231], s[58:59], v[232:233] neg_lo:[0,0,1] neg_hi:[0,0,1]\n\t"
"v_pk_fma_f32 v[234:235], v[32:33], s[60:61], v[230:231]\n\t"
"v_pk_fma_f32 v[32:33], v[230:231], s[60:61], v[32:33]\n\t"
"v_pk_fma_f32 v[230:231], v[132:133], s[62:63], v[232:233]\n\t"
"v_pk_fma_f32 v[132:133], v[232:233], s[62:63], v[132:133]\n\t"
"v_mfma_f32_16x16x32_bf16 a[116:119], a[244:247], a[172:175], a[116:119]\n\t"
"ds_read_b128 a[172:175], v244 offset:512\n\t"
"v_accvgpr_write_b32 a33, v224\n\t"
"v_mfma_f32_16x16x32_bf16 a[116:119], a[212:215], a[176:179], a[116:119]\n\t"
"v_accvgpr_write_b32 a37, v228\n\t"
"v_accvgpr_read_b32 v236, a42\n\t"
"v_mfma_f32_16x16x32_bf16 a[116:119], a[240:243], a[180:183], a[116:119]\n\t"
"global_load_dwordx4 v[248:251], v252, s[48:49] offset:0\n\t"
"v_accvgpr_read_b32 v238, a46\n\t"
"v_accvgpr_read_b32 v237, a43\n\t"
"v_accvgpr_read_b32 v239, a47\n\t"
"v_pk_fma_f32 v[236:237], v[238:239], s[56:57], v[236:237]\n\t"
"v_pk_fma_f32 v[238:239], v[236:237], s[58:59], v[238:239] neg_lo:[0,0,1] neg_hi:[0,0,1]\n\t"
"v_pk_fma_f32 v[98:99], v[34:35], s[60:61], v[236:237]\n\t"
"v_pk_fma_f32 v[34:35], v[236:237], s[60:61], v[34:35]\n\t"
"v_pk_fma_f32 v[236:237], v[134:135], s[62:63], v[238:239]\n\t"
"v_pk_fma_f32 v[134:135], v[238:239], s[62:63], v[134:135]\n\t"
"v_mfma_f32_16x16x32_bf16 a[124:127], a[240:243], a[184:187], a[124:127]\n\t"
"v_cvt_pk_bf16_f32 v198, v230, v231\n\t"
"v_cvt_pk_bf16_f32 v202, v234, v235\n\t"
"v_mfma_f32_16x16x32_bf16 a[124:127], a[212:215], a[180:183], a[124:127]\n\t"
"v_accvgpr_read_b32 v224, a48\n\t"
"v_accvgpr_read_b32 v226, a52\n\t"
"v_mfma_f32_16x16x32_bf16 a[124:127], a[244:247], a[176:179], a[124:127]\n\t"
"ds_read_b128 a[176:179], v244 offset:1024\n\t"
"v_accvgpr_read_b32 v225, a49\n\t"
"v_accvgpr_read_b32 v227, a53\n\t"
"v_accvgpr_read_b32 v230, a50\n\t"
"v_accvgpr_read_b32 v232, a54\n\t"
"v_accvgpr_read_b32 v231, a51\n\t"
"v_accvgpr_read_b32 v233, a55\n\t"
"v_pk_fma_f32 v[224:225], v[226:227], s[56:57], v[224:225]\n\t"
"v_pk_fma_f32 v[226:227], v[224:225], s[58:59], v[226:227] neg_lo:[0,0,1] neg_hi:[0,0,1]\n\t"
"v_pk_fma_f32 v[228:229], v[36:37], s[60:61], v[224:225]\n\t"
"v_pk_fma_f32 v[36:37], v[224:225], s[60:61], v[36:37]\n\t"
"v_pk_fma_f32 v[224:225], v[136:137], s[62:63], v[226:227]\n\t"
"v_pk_fma_f32 v[136:137], v[226:227], s[62:63], v[136:137]\n\t"
"v_pk_fma_f32 v[230:231], v[232:233], s[56:57], v[230:231]\n\t"
"v_pk_fma_f32 v[232:233], v[230:231], s[58:59], v[232:233] neg_lo:[0,0,1] neg_hi:[0,0,1]\n\t"
"v_pk_fma_f32 v[234:235], v[38:39], s[60:61], v[230:231]\n\t"
"v_pk_fma_f32 v[38:39], v[230:231], s[60:61], v[38:39]\n\t"
"v_pk_fma_f32 v[230:231], v[138:139], s[62:63], v[232:233]\n\t"
"v_pk_fma_f32 v[138:139], v[232:233], s[62:63], v[138:139]\n\t"
"v_mfma_f32_16x16x32_bf16 a[132:135], a[244:247], a[180:183], a[132:135]\n\t"
"ds_read_b128 a[180:183], v244 offset:1536\n\t"
"v_cvt_pk_bf16_f32 v199, v236, v237\n\t"
"v_mfma_f32_16x16x32_bf16 a[132:135], a[212:215], a[184:187], a[132:135]\n\t"
"v_cvt_pk_bf16_f32 v203, v98, v99\n\t"
"v_accvgpr_read_b32 v236, a56\n\t"
"v_mfma_f32_16x16x32_bf16 a[132:135], a[240:243], a[188:191], a[132:135]\n\t"
"v_accvgpr_read_b32 v238, a60\n\t"
"v_accvgpr_read_b32 v237, a57\n\t"
"v_accvgpr_read_b32 v239, a61\n\t"
"v_cvt_pk_bf16_f32 v206, v224, v225\n\t"
"v_cvt_pk_bf16_f32 v210, v228, v229\n\t"
"v_pk_fma_f32 v[236:237], v[238:239], s[56:57], v[236:237]\n\t"
"v_pk_fma_f32 v[238:239], v[236:237], s[58:59], v[238:239] neg_lo:[0,0,1] neg_hi:[0,0,1]\n\t"
"v_pk_fma_f32 v[98:99], v[40:41], s[60:61], v[236:237]\n\t"
"v_pk_fma_f32 v[40:41], v[236:237], s[60:61], v[40:41]\n\t"
"v_pk_fma_f32 v[236:237], v[140:141], s[62:63], v[238:239]\n\t"
"v_pk_fma_f32 v[140:141], v[238:239], s[62:63], v[140:141]\n\t"
"v_mfma_f32_16x16x32_bf16 a[140:143], a[240:243], a[192:195], a[140:143]\n\t"
"v_cvt_pk_bf16_f32 v207, v230, v231\n\t"
"v_cvt_pk_bf16_f32 v211, v234, v235\n\t"
"v_mfma_f32_16x16x32_bf16 a[140:143], a[212:215], a[188:191], a[140:143]\n\t"
"v_accvgpr_read_b32 v224, a58\n\t"
"v_accvgpr_read_b32 v226, a62\n\t"
"v_mfma_f32_16x16x32_bf16 a[140:143], a[244:247], a[184:187], a[140:143]\n\t"
"ds_read_b128 a[184:187], v244 offset:2048\n\t"
"v_accvgpr_read_b32 v225, a59\n\t"
"v_accvgpr_read_b32 v227, a63\n\t"
"v_cvt_pk_bf16_f32 v214, v236, v237\n\t"
"v_cvt_pk_bf16_f32 v218, v98, v99\n\t"
"v_pk_fma_f32 v[224:225], v[226:227], s[56:57], v[224:225]\n\t"
"v_pk_fma_f32 v[226:227], v[224:225], s[58:59], v[226:227] neg_lo:[0,0,1] neg_hi:[0,0,1]\n\t"
"v_pk_fma_f32 v[228:229], v[42:43], s[60:61], v[224:225]\n\t"
"v_pk_fma_f32 v[42:43], v[224:225], s[60:61], v[42:43]\n\t"
"v_pk_fma_f32 v[224:225], v[142:143], s[62:63], v[226:227]\n\t"
"v_pk_fma_f32 v[142:143], v[226:227], s[62:63], v[142:143]\n\t"
"v_mfma_f32_16x16x32_bf16 a[148:151], a[244:247], a[188:191], a[148:151]\n\t"
"ds_read_b128 a[188:191], v244 offset:2560\n\t"
"v_accvgpr_read_b32 v230, a64\n\t"
"v_mfma_f32_16x16x32_bf16 a[148:151], a[212:215], a[192:195], a[148:151]\n\t"
"v_accvgpr_read_b32 v232, a68\n\t"
"v_accvgpr_read_b32 v231, a65\n\t"
"v_mfma_f32_16x16x32_bf16 a[148:151], a[240:243], a[196:199], a[148:151]\n\t"
"buffer_load_dwordx4 a[56:59], off, s[80:83], s20 offset:1024 sc1\n\t"
"v_accvgpr_read_b32 v233, a69\n\t"
"v_cvt_pk_bf16_f32 v215, v224, v225\n\t"
"v_pk_fma_f32 v[230:231], v[232:233], s[56:57], v[230:231]\n\t"
"v_pk_fma_f32 v[232:233], v[230:231], s[58:59], v[232:233] neg_lo:[0,0,1] neg_hi:[0,0,1]\n\t"
"v_pk_fma_f32 v[234:235], v[44:45], s[60:61], v[230:231]\n\t"
"v_pk_fma_f32 v[44:45], v[230:231], s[60:61], v[44:45]\n\t"
"v_pk_fma_f32 v[230:231], v[144:145], s[62:63], v[232:233]\n\t"
"v_pk_fma_f32 v[144:145], v[232:233], s[62:63], v[144:145]\n\t"
"v_mfma_f32_16x16x32_bf16 a[156:159], a[240:243], a[200:203], a[156:159]\n\t"
"v_cvt_pk_bf16_f32 v219, v228, v229\n\t"
"v_accvgpr_read_b32 v236, a66\n\t"
"v_mfma_f32_16x16x32_bf16 a[156:159], a[212:215], a[196:199], a[156:159]\n\t"
"v_accvgpr_read_b32 v238, a70\n\t"
"v_accvgpr_read_b32 v237, a67\n\t"
"v_mfma_f32_16x16x32_bf16 a[156:159], a[244:247], a[192:195], a[156:159]\n\t"
"ds_read_b128 a[192:195], v244 offset:3072\n\t"
"buffer_load_dwordx4 a[60:63], off, s[80:83], s20 offset:2048 sc1\n\t"
"v_accvgpr_read_b32 v239, a71\n\t"
"v_cvt_pk_bf16_f32 v230, v230, v231\n\t"
"v_cvt_pk_bf16_f32 v234, v234, v235\n\t"
"v_pk_fma_f32 v[236:237], v[238:239], s[56:57], v[236:237]\n\t"
"v_pk_fma_f32 v[238:239], v[236:237], s[58:59], v[238:239] neg_lo:[0,0,1] neg_hi:[0,0,1]\n\t"
"v_pk_fma_f32 v[98:99], v[46:47], s[60:61], v[236:237]\n\t"
"v_pk_fma_f32 v[46:47], v[236:237], s[60:61], v[46:47]\n\t"
"v_pk_fma_f32 v[236:237], v[146:147], s[62:63], v[238:239]\n\t"
"v_pk_fma_f32 v[146:147], v[238:239], s[62:63], v[146:147]\n\t"
"v_mfma_f32_16x16x32_bf16 a[164:167], a[244:247], a[196:199], a[164:167]\n\t"
"s_waitcnt vmcnt(10) lgkmcnt(4)\n\t"
"ds_read_b128 a[196:199], v244 offset:3584\n\t"
"v_accvgpr_write_b32 a2, v230\n\t"
"v_mfma_f32_16x16x32_bf16 a[164:167], a[212:215], a[200:203], a[164:167]\n\t"
"s_mov_b32 s20, 1081344\n\t"
"ds_read_b128 a[200:203], v244 offset:4096\n\t"
"v_accvgpr_write_b32 a6, v234\n\t"
"v_mfma_f32_16x16x32_bf16 a[164:167], a[240:243], a[204:207], a[164:167]\n\t"
"buffer_load_dwordx4 a[212:215], off, s[80:83], s20 offset:0 sc1\n\t"
"ds_read_b128 a[204:207], v244 offset:4608\n\t"
"v_accvgpr_read_b32 v224, a72\n\t"
"v_accvgpr_read_b32 v226, a76\n\t"
"v_accvgpr_read_b32 v225, a73\n\t"
"v_accvgpr_read_b32 v227, a77\n\t"
"v_cvt_pk_bf16_f32 v236, v236, v237\n\t"
"v_cvt_pk_bf16_f32 v98, v98, v99\n\t"
"v_pk_fma_f32 v[224:225], v[226:227], s[56:57], v[224:225]\n\t"
"v_pk_fma_f32 v[226:227], v[224:225], s[58:59], v[226:227] neg_lo:[0,0,1] neg_hi:[0,0,1]\n\t"
"v_pk_fma_f32 v[228:229], v[48:49], s[60:61], v[224:225]\n\t"
"v_pk_fma_f32 v[48:49], v[224:225], s[60:61], v[48:49]\n\t"
"v_pk_fma_f32 v[224:225], v[148:149], s[62:63], v[226:227]\n\t"
"v_pk_fma_f32 v[148:149], v[226:227], s[62:63], v[148:149]\n\t"
"Lst32_%=:\n\t"
"v_mfma_f32_16x16x32_bf16 v[0:3], a[248:251], a[168:171], v[0:3]\n\t"
"s_waitcnt lgkmcnt(6)\n\t"
"v_accvgpr_write_b32 a3, v236\n\t"
"v_accvgpr_write_b32 a7, v98\n\t"
"v_mfma_f32_16x16x32_bf16 v[0:3], a[220:223], a[176:179], v[0:3]\n\t"
"v_accvgpr_read_b32 v230, a74\n\t"
"v_accvgpr_read_b32 v232, a78\n\t"
"v_mfma_f32_16x16x32_bf16 v[0:3], a[216:219], a[172:175], v[0:3]\n\t"
"buffer_load_dwordx4 a[240:243], off, s[80:83], s20 offset:1024 sc1\n\t"
"v_accvgpr_read_b32 v231, a75\n\t"
"v_accvgpr_read_b32 v233, a79\n\t"
"v_cvt_pk_bf16_f32 v224, v224, v225\n\t"
"v_cvt_pk_bf16_f32 v228, v228, v229\n\t"
"v_pk_fma_f32 v[230:231], v[232:233], s[56:57], v[230:231]\n\t"
"v_pk_fma_f32 v[232:233], v[230:231], s[58:59], v[232:233] neg_lo:[0,0,1] neg_hi:[0,0,1]\n\t"
"v_pk_fma_f32 v[234:235], v[50:51], s[60:61], v[230:231]\n\t"
"v_pk_fma_f32 v[50:51], v[230:231], s[60:61], v[50:51]\n\t"
"v_pk_fma_f32 v[230:231], v[150:151], s[62:63], v[232:233]\n\t"
"v_pk_fma_f32 v[150:151], v[232:233], s[62:63], v[150:151]\n\t"
"v_mfma_f32_16x16x32_bf16 v[4:7], a[248:251], a[172:175], v[4:7]\n\t"
"s_waitcnt lgkmcnt(5)\n\t"
"v_accvgpr_write_b32 a10, v224\n\t"
"v_accvgpr_write_b32 a14, v228\n\t"
"v_mfma_f32_16x16x32_bf16 v[4:7], a[216:219], a[176:179], v[4:7]\n\t"
"v_accvgpr_read_b32 v236, a80\n\t"
"v_accvgpr_read_b32 v238, a84\n\t"
"v_mfma_f32_16x16x32_bf16 v[4:7], a[220:223], a[180:183], v[4:7]\n\t"
"buffer_load_dwordx4 a[244:247], off, s[80:83], s20 offset:2048 sc1\n\t"
"v_accvgpr_read_b32 v237, a81\n\t"
"v_accvgpr_read_b32 v239, a85\n\t"
"v_cvt_pk_bf16_f32 v230, v230, v231\n\t"
"v_cvt_pk_bf16_f32 v234, v234, v235\n\t"
"v_pk_fma_f32 v[236:237], v[238:239], s[56:57], v[236:237]\n\t"
"v_pk_fma_f32 v[238:239], v[236:237], s[58:59], v[238:239] neg_lo:[0,0,1] neg_hi:[0,0,1]\n\t"
"v_pk_fma_f32 v[98:99], v[52:53], s[60:61], v[236:237]\n\t"
"v_pk_fma_f32 v[52:53], v[236:237], s[60:61], v[52:53]\n\t"
"v_pk_fma_f32 v[236:237], v[152:153], s[62:63], v[238:239]\n\t"
"v_pk_fma_f32 v[152:153], v[238:239], s[62:63], v[152:153]\n\t"
"v_mfma_f32_16x16x32_bf16 v[8:11], a[220:223], a[184:187], v[8:11]\n\t"
"s_waitcnt lgkmcnt(4)\n\t"
"s_mov_b32 s20, 1093632\n\t"
"v_accvgpr_write_b32 a11, v230\n\t"
"v_mfma_f32_16x16x32_bf16 v[8:11], a[216:219], a[180:183], v[8:11]\n\t"
"v_accvgpr_write_b32 a15, v234\n\t"
"v_accvgpr_read_b32 v224, a82\n\t"
"v_mfma_f32_16x16x32_bf16 v[8:11], a[248:251], a[176:179], v[8:11]\n\t"
"buffer_load_dwordx4 a[64:67], off, s[80:83], s20 offset:0 sc1\n\t"
"v_accvgpr_read_b32 v226, a86\n\t"
"v_accvgpr_read_b32 v225, a83\n\t"
"v_accvgpr_read_b32 v227, a87\n\t"
"v_cvt_pk_bf16_f32 v236, v236, v237\n\t"
"v_cvt_pk_bf16_f32 v98, v98, v99\n\t"
"v_pk_fma_f32 v[224:225], v[226:227], s[56:57], v[224:225]\n\t"
"v_pk_fma_f32 v[226:227], v[224:225], s[58:59], v[226:227] neg_lo:[0,0,1] neg_hi:[0,0,1]\n\t"
"v_pk_fma_f32 v[228:229], v[54:55], s[60:61], v[224:225]\n\t"
"v_pk_fma_f32 v[54:55], v[224:225], s[60:61], v[54:55]\n\t"
"v_pk_fma_f32 v[224:225], v[154:155], s[62:63], v[226:227]\n\t"
"v_pk_fma_f32 v[154:155], v[226:227], s[62:63], v[154:155]\n\t"
"v_mfma_f32_16x16x32_bf16 v[12:15], a[248:251], a[180:183], v[12:15]\n\t"
"s_waitcnt lgkmcnt(3)\n\t"
"v_accvgpr_write_b32 a18, v236\n\t"
"v_accvgpr_write_b32 a22, v98\n\t"
"v_mfma_f32_16x16x32_bf16 v[12:15], a[216:219], a[184:187], v[12:15]\n\t"
"v_accvgpr_read_b32 v230, a88\n\t"
"v_accvgpr_read_b32 v232, a92\n\t"
"v_mfma_f32_16x16x32_bf16 v[12:15], a[220:223], a[188:191], v[12:15]\n\t"
"buffer_load_dwordx4 a[68:71], off, s[80:83], s20 offset:1024 sc1\n\t"
"v_accvgpr_read_b32 v231, a89\n\t"
"v_accvgpr_read_b32 v233, a93\n\t"
"v_accvgpr_read_b32 v236, a90\n\t"
"v_accvgpr_read_b32 v238, a94\n\t"
"v_accvgpr_read_b32 v237, a91\n\t"
"v_accvgpr_read_b32 v239, a95\n\t"
"v_cvt_pk_bf16_f32 v224, v224, v225\n\t"
"v_cvt_pk_bf16_f32 v228, v228, v229\n\t"
"Lpg23_%=:\n\t"
"s_cbranch_vccnz Lvr3_%=\n\t"
"v_pk_fma_f32 v[230:231], v[232:233], s[56:57], v[230:231]\n\t"
"v_pk_fma_f32 v[232:233], v[230:231], s[58:59], v[232:233] neg_lo:[0,0,1] neg_hi:[0,0,1]\n\t"
"v_pk_fma_f32 v[234:235], v[56:57], s[60:61], v[230:231]\n\t"
"v_pk_fma_f32 v[56:57], v[230:231], s[60:61], v[56:57]\n\t"
"v_pk_fma_f32 v[230:231], v[156:157], s[62:63], v[232:233]\n\t"
"v_pk_fma_f32 v[156:157], v[232:233], s[62:63], v[156:157]\n\t"
"v_pk_fma_f32 v[236:237], v[238:239], s[56:57], v[236:237]\n\t"
"v_pk_fma_f32 v[238:239], v[236:237], s[58:59], v[238:239] neg_lo:[0,0,1] neg_hi:[0,0,1]\n\t"
"v_pk_fma_f32 v[98:99], v[58:59], s[60:61], v[236:237]\n\t"
"v_pk_fma_f32 v[58:59], v[236:237], s[60:61], v[58:59]\n\t"
"v_pk_fma_f32 v[236:237], v[158:159], s[62:63], v[238:239]\n\t"
"v_pk_fma_f32 v[158:159], v[238:239], s[62:63], v[158:159]\n\t"
"v_mfma_f32_16x16x32_bf16 v[16:19], a[220:223], a[192:195], v[16:19]\n\t"
"s_waitcnt lgkmcnt(2)\n\t"
"v_accvgpr_write_b32 a19, v224\n\t"
"v_accvgpr_write_b32 a23, v228\n\t"
"v_mfma_f32_16x16x32_bf16 v[16:19], a[216:219], a[188:191], v[16:19]\n\t"
"v_accvgpr_read_b32 v224, a96\n\t"
"v_accvgpr_read_b32 v226, a100\n\t"
"v_mfma_f32_16x16x32_bf16 v[16:19], a[248:251], a[184:187], v[16:19]\n\t"
"buffer_load_dwordx4 a[72:75], off, s[80:83], s20 offset:2048 sc1\n\t"
"v_accvgpr_read_b32 v225, a97\n\t"
"v_accvgpr_read_b32 v227, a101\n\t"
"v_cvt_pk_bf16_f32 v230, v230, v231\n\t"
"v_cvt_pk_bf16_f32 v234, v234, v235\n\t"
"v_pk_fma_f32 v[224:225], v[226:227], s[56:57], v[224:225]\n\t"
"v_pk_fma_f32 v[226:227], v[224:225], s[58:59], v[226:227] neg_lo:[0,0,1] neg_hi:[0,0,1]\n\t"
"v_pk_fma_f32 v[228:229], v[60:61], s[60:61], v[224:225]\n\t"
"v_pk_fma_f32 v[60:61], v[224:225], s[60:61], v[60:61]\n\t"
"v_pk_fma_f32 v[224:225], v[160:161], s[62:63], v[226:227]\n\t"
"v_pk_fma_f32 v[160:161], v[226:227], s[62:63], v[160:161]\n\t"
"v_mfma_f32_16x16x32_bf16 v[20:23], a[248:251], a[188:191], v[20:23]\n\t"
"s_waitcnt lgkmcnt(1)\n\t"
"s_mov_b32 s20, 589824\n\t"
"v_accvgpr_write_b32 a26, v230\n\t"
"v_mfma_f32_16x16x32_bf16 v[20:23], a[216:219], a[192:195], v[20:23]\n\t"
"v_cvt_pk_bf16_f32 v236, v236, v237\n\t"
"v_accvgpr_write_b32 a30, v234\n\t"
"v_mfma_f32_16x16x32_bf16 v[20:23], a[220:223], a[196:199], v[20:23]\n\t"
"buffer_load_dwordx4 a[76:79], off, s[80:83], s20 offset:0 sc1\n\t"
"v_accvgpr_read_b32 v230, a98\n\t"
"v_accvgpr_read_b32 v232, a102\n\t"
"v_accvgpr_read_b32 v231, a99\n\t"
"v_accvgpr_read_b32 v233, a103\n\t"
"v_cvt_pk_bf16_f32 v98, v98, v99\n\t"
"v_accvgpr_write_b32 a27, v236\n\t"
"v_accvgpr_write_b32 a31, v98\n\t"
"v_cvt_pk_bf16_f32 v224, v224, v225\n\t"
"v_cvt_pk_bf16_f32 v228, v228, v229\n\t"
"v_pk_fma_f32 v[230:231], v[232:233], s[56:57], v[230:231]\n\t"
"v_pk_fma_f32 v[232:233], v[230:231], s[58:59], v[232:233] neg_lo:[0,0,1] neg_hi:[0,0,1]\n\t"
"v_pk_fma_f32 v[234:235], v[62:63], s[60:61], v[230:231]\n\t"
"v_pk_fma_f32 v[62:63], v[230:231], s[60:61], v[62:63]\n\t"
"v_pk_fma_f32 v[230:231], v[162:163], s[62:63], v[232:233]\n\t"
"v_pk_fma_f32 v[162:163], v[232:233], s[62:63], v[162:163]\n\t"
"v_mfma_f32_16x16x32_bf16 v[24:27], a[220:223], a[200:203], v[24:27]\n\t"
"s_waitcnt lgkmcnt(0)\n\t"
"v_accvgpr_write_b32 a34, v224\n\t"
"v_accvgpr_write_b32 a38, v228\n\t"
"v_mfma_f32_16x16x32_bf16 v[24:27], a[216:219], a[196:199], v[24:27]\n\t"
"v_accvgpr_read_b32 v236, a104\n\t"
"v_accvgpr_read_b32 v238, a108\n\t"
"v_mfma_f32_16x16x32_bf16 v[24:27], a[248:251], a[192:195], v[24:27]\n\t"
"buffer_load_dwordx4 a[80:83], off, s[80:83], s20 offset:1024 sc1\n\t"
"v_accvgpr_read_b32 v237, a105\n\t"
"v_accvgpr_read_b32 v239, a109\n\t"
"v_cvt_pk_bf16_f32 v230, v230, v231\n\t"
"v_cvt_pk_bf16_f32 v234, v234, v235\n\t"
"v_pk_fma_f32 v[236:237], v[238:239], s[56:57], v[236:237]\n\t"
"v_pk_fma_f32 v[238:239], v[236:237], s[58:59], v[238:239] neg_lo:[0,0,1] neg_hi:[0,0,1]\n\t"
"v_pk_fma_f32 v[98:99], v[64:65], s[60:61], v[236:237]\n\t"
"v_pk_fma_f32 v[64:65], v[236:237], s[60:61], v[64:65]\n\t"
"v_pk_fma_f32 v[236:237], v[164:165], s[62:63], v[238:239]\n\t"
"v_pk_fma_f32 v[164:165], v[238:239], s[62:63], v[164:165]\n\t"
"v_mfma_f32_16x16x32_bf16 v[28:31], a[248:251], a[196:199], v[28:31]\n\t"
"s_waitcnt vmcnt(15)\n\t"
"s_waitcnt vmcnt(10)\n\t"
"v_lshrrev_b32 v245, 10, v249\n\t"
"v_mfma_f32_16x16x32_bf16 v[28:31], a[216:219], a[200:203], v[28:31]\n\t"
"v_lshl_add_u32 v245, v245, 9, v249\n\t"
"v_lshrrev_b32 v246, 10, v250\n\t"
"v_mfma_f32_16x16x32_bf16 v[28:31], a[220:223], a[204:207], v[28:31]\n\t"
"buffer_load_dwordx4 a[216:219], off, s[80:83], s20 offset:2048 sc1\n\t"
"v_accvgpr_read_b32 v224, a106\n\t"
"v_accvgpr_read_b32 v226, a110\n\t"
"v_accvgpr_read_b32 v225, a107\n\t"
"v_accvgpr_read_b32 v227, a111\n\t"
"v_lshl_add_u32 v246, v246, 9, v250\n\t"
"v_lshrrev_b32 v255, 10, v252\n\t"
"v_lshl_add_u32 v255, v255, 9, v252\n\t"
"v_accvgpr_write_b32 a35, v230\n\t"
"v_accvgpr_write_b32 a39, v234\n\t"
"v_cvt_pk_bf16_f32 v236, v236, v237\n\t"
"v_cvt_pk_bf16_f32 v98, v98, v99\n\t"
"v_add_u32 v245, 44032, v245\n\t"
"v_add_u32 v249, 55296, v245\n\t"
"v_add_u32 v246, 44032, v246\n\t"
"v_add_u32 v250, 55296, v246\n\t"
"v_add_u32 v255, 44032, v255\n\t"
"v_add_u32 v247, 55296, v255\n\t"
"v_pk_fma_f32 v[224:225], v[226:227], s[56:57], v[224:225]\n\t"
"v_pk_fma_f32 v[226:227], v[224:225], s[58:59], v[226:227] neg_lo:[0,0,1] neg_hi:[0,0,1]\n\t"
"v_pk_fma_f32 v[228:229], v[66:67], s[60:61], v[224:225]\n\t"
"v_pk_fma_f32 v[66:67], v[224:225], s[60:61], v[66:67]\n\t"
"v_pk_fma_f32 v[224:225], v[166:167], s[62:63], v[226:227]\n\t"
"v_pk_fma_f32 v[166:167], v[226:227], s[62:63], v[166:167]\n\t"
"v_mfma_f32_16x16x32_bf16 v[32:35], a[208:211], a[168:171], v[32:35]\n\t"
"s_mov_b32 s20, 602112\n\t"
"v_accvgpr_write_b32 a104, v236\n\t"
"v_accvgpr_write_b32 a106, v98\n\t"
"v_mfma_f32_16x16x32_bf16 v[32:35], a[252:255], a[176:179], v[32:35]\n\t"
"v_accvgpr_read_b32 v230, a112\n\t"
"v_accvgpr_read_b32 v232, a116\n\t"
"v_mfma_f32_16x16x32_bf16 v[32:35], a[224:227], a[172:175], v[32:35]\n\t"
"buffer_load_dwordx4 a[220:223], off, s[80:83], s20 offset:0 sc1\n\t"
"v_accvgpr_read_b32 v231, a113\n\t"
"v_accvgpr_read_b32 v233, a117\n\t"
"v_cvt_pk_bf16_f32 v224, v224, v225\n\t"
"v_cvt_pk_bf16_f32 v228, v228, v229\n\t"
"v_pk_fma_f32 v[230:231], v[232:233], s[56:57], v[230:231]\n\t"
"v_pk_fma_f32 v[232:233], v[230:231], s[58:59], v[232:233] neg_lo:[0,0,1] neg_hi:[0,0,1]\n\t"
"v_pk_fma_f32 v[234:235], v[68:69], s[60:61], v[230:231]\n\t"
"v_pk_fma_f32 v[68:69], v[230:231], s[60:61], v[68:69]\n\t"
"v_pk_fma_f32 v[230:231], v[168:169], s[62:63], v[232:233]\n\t"
"v_pk_fma_f32 v[168:169], v[232:233], s[62:63], v[168:169]\n\t"
"v_mfma_f32_16x16x32_bf16 v[36:39], a[208:211], a[172:175], v[36:39]\n\t"
"v_accvgpr_write_b32 a105, v224\n\t"
"v_accvgpr_write_b32 a107, v228\n\t"
"v_mfma_f32_16x16x32_bf16 v[36:39], a[224:227], a[176:179], v[36:39]\n\t"
"v_accvgpr_read_b32 v236, a114\n\t"
"v_accvgpr_read_b32 v238, a118\n\t"
"v_mfma_f32_16x16x32_bf16 v[36:39], a[252:255], a[180:183], v[36:39]\n\t"
"buffer_load_dwordx4 a[248:251], off, s[80:83], s20 offset:1024 sc1\n\t"
"v_accvgpr_read_b32 v237, a115\n\t"
"v_accvgpr_read_b32 v239, a119\n\t"
"v_cvt_pk_bf16_f32 v230, v230, v231\n\t"
"v_cvt_pk_bf16_f32 v234, v234, v235\n\t"
"v_pk_fma_f32 v[236:237], v[238:239], s[56:57], v[236:237]\n\t"
"v_pk_fma_f32 v[238:239], v[236:237], s[58:59], v[238:239] neg_lo:[0,0,1] neg_hi:[0,0,1]\n\t"
"v_pk_fma_f32 v[98:99], v[70:71], s[60:61], v[236:237]\n\t"
"v_pk_fma_f32 v[70:71], v[236:237], s[60:61], v[70:71]\n\t"
"v_pk_fma_f32 v[236:237], v[170:171], s[62:63], v[238:239]\n\t"
"v_pk_fma_f32 v[170:171], v[238:239], s[62:63], v[170:171]\n\t"
"v_mfma_f32_16x16x32_bf16 v[40:43], a[252:255], a[184:187], v[40:43]\n\t"
"v_accvgpr_write_b32 a112, v230\n\t"
"v_accvgpr_write_b32 a114, v234\n\t"
"v_mfma_f32_16x16x32_bf16 v[40:43], a[224:227], a[180:183], v[40:43]\n\t"
"v_accvgpr_read_b32 v224, a120\n\t"
"v_accvgpr_read_b32 v226, a124\n\t"
"v_mfma_f32_16x16x32_bf16 v[40:43], a[208:211], a[176:179], v[40:43]\n\t"
"buffer_load_dwordx4 a[84:87], off, s[80:83], s20 offset:2048 sc1\n\t"
"v_accvgpr_read_b32 v225, a121\n\t"
"v_accvgpr_read_b32 v227, a125\n\t"
"v_cvt_pk_bf16_f32 v236, v236, v237\n\t"
"v_cvt_pk_bf16_f32 v98, v98, v99\n\t"
"v_pk_fma_f32 v[224:225], v[226:227], s[56:57], v[224:225]\n\t"
"v_pk_fma_f32 v[226:227], v[224:225], s[58:59], v[226:227] neg_lo:[0,0,1] neg_hi:[0,0,1]\n\t"
"v_pk_fma_f32 v[228:229], v[72:73], s[60:61], v[224:225]\n\t"
"v_pk_fma_f32 v[72:73], v[224:225], s[60:61], v[72:73]\n\t"
"v_pk_fma_f32 v[224:225], v[172:173], s[62:63], v[226:227]\n\t"
"v_pk_fma_f32 v[172:173], v[226:227], s[62:63], v[172:173]\n\t"
"v_mfma_f32_16x16x32_bf16 v[44:47], a[208:211], a[180:183], v[44:47]\n\t"
"s_mov_b32 s20, 614400\n\t"
"v_accvgpr_write_b32 a113, v236\n\t"
"v_accvgpr_write_b32 a115, v98\n\t"
"v_mfma_f32_16x16x32_bf16 v[44:47], a[224:227], a[184:187], v[44:47]\n\t"
"v_accvgpr_read_b32 v230, a122\n\t"
"v_accvgpr_read_b32 v232, a126\n\t"
"v_mfma_f32_16x16x32_bf16 v[44:47], a[252:255], a[188:191], v[44:47]\n\t"
"buffer_load_dwordx4 a[88:91], off, s[80:83], s20 offset:0 sc1\n\t"
"v_accvgpr_read_b32 v231, a123\n\t"
"v_accvgpr_read_b32 v233, a127\n\t"
"v_accvgpr_read_b32 v236, a128\n\t"
"v_accvgpr_read_b32 v238, a132\n\t"
"v_accvgpr_read_b32 v237, a129\n\t"
"v_accvgpr_read_b32 v239, a133\n\t"
"v_cvt_pk_bf16_f32 v224, v224, v225\n\t"
"v_cvt_pk_bf16_f32 v228, v228, v229\n\t"
"v_pk_fma_f32 v[230:231], v[232:233], s[56:57], v[230:231]\n\t"
"v_pk_fma_f32 v[232:233], v[230:231], s[58:59], v[232:233] neg_lo:[0,0,1] neg_hi:[0,0,1]\n\t"
"v_pk_fma_f32 v[234:235], v[74:75], s[60:61], v[230:231]\n\t"
"v_pk_fma_f32 v[74:75], v[230:231], s[60:61], v[74:75]\n\t"
"v_pk_fma_f32 v[230:231], v[174:175], s[62:63], v[232:233]\n\t"
"v_pk_fma_f32 v[174:175], v[232:233], s[62:63], v[174:175]\n\t"
"v_pk_fma_f32 v[236:237], v[238:239], s[56:57], v[236:237]\n\t"
"v_pk_fma_f32 v[238:239], v[236:237], s[58:59], v[238:239] neg_lo:[0,0,1] neg_hi:[0,0,1]\n\t"
"v_pk_fma_f32 v[98:99], v[76:77], s[60:61], v[236:237]\n\t"
"v_pk_fma_f32 v[76:77], v[236:237], s[60:61], v[76:77]\n\t"
"v_pk_fma_f32 v[236:237], v[176:177], s[62:63], v[238:239]\n\t"
"v_pk_fma_f32 v[176:177], v[238:239], s[62:63], v[176:177]\n\t"
"v_mfma_f32_16x16x32_bf16 v[48:51], a[252:255], a[192:195], v[48:51]\n\t"
"v_accvgpr_write_b32 a120, v224\n\t"
"v_accvgpr_write_b32 a122, v228\n\t"
"v_mfma_f32_16x16x32_bf16 v[48:51], a[224:227], a[188:191], v[48:51]\n\t"
"v_accvgpr_read_b32 v224, a130\n\t"
"v_accvgpr_read_b32 v226, a134\n\t"
"v_mfma_f32_16x16x32_bf16 v[48:51], a[208:211], a[184:187], v[48:51]\n\t"
"buffer_load_dwordx4 a[92:95], off, s[80:83], s20 offset:1024 sc1\n\t"
"v_accvgpr_read_b32 v225, a131\n\t"
"v_accvgpr_read_b32 v227, a135\n\t"
"v_cvt_pk_bf16_f32 v230, v230, v231\n\t"
"v_cvt_pk_bf16_f32 v234, v234, v235\n\t"
"v_cvt_pk_bf16_f32 v236, v236, v237\n\t"
"v_cvt_pk_bf16_f32 v98, v98, v99\n\t"
"v_pk_fma_f32 v[224:225], v[226:227], s[56:57], v[224:225]\n\t"
"v_pk_fma_f32 v[226:227], v[224:225], s[58:59], v[226:227] neg_lo:[0,0,1] neg_hi:[0,0,1]\n\t"
"v_pk_fma_f32 v[228:229], v[78:79], s[60:61], v[224:225]\n\t"
"v_pk_fma_f32 v[78:79], v[224:225], s[60:61], v[78:79]\n\t"
"v_pk_fma_f32 v[224:225], v[178:179], s[62:63], v[226:227]\n\t"
"v_pk_fma_f32 v[178:179], v[226:227], s[62:63], v[178:179]\n\t"
"v_mfma_f32_16x16x32_bf16 v[52:55], a[208:211], a[188:191], v[52:55]\n\t"
"v_accvgpr_write_b32 a121, v230\n\t"
"v_accvgpr_write_b32 a123, v234\n\t"
"v_mfma_f32_16x16x32_bf16 v[52:55], a[224:227], a[192:195], v[52:55]\n\t"
"v_accvgpr_write_b32 a128, v236\n\t"
"v_accvgpr_write_b32 a130, v98\n\t"
"v_mfma_f32_16x16x32_bf16 v[52:55], a[252:255], a[196:199], v[52:55]\n\t"
"ds_write_b128 v245, v[196:199] offset:0\n\t"
"v_accvgpr_read_b32 v230, a136\n\t"
"v_accvgpr_read_b32 v232, a140\n\t"
"v_accvgpr_read_b32 v231, a137\n\t"
"v_accvgpr_read_b32 v233, a141\n\t"
"v_cvt_pk_bf16_f32 v224, v224, v225\n\t"
"v_cvt_pk_bf16_f32 v228, v228, v229\n\t"
"v_pk_fma_f32 v[230:231], v[232:233], s[56:57], v[230:231]\n\t"
"v_pk_fma_f32 v[232:233], v[230:231], s[58:59], v[232:233] neg_lo:[0,0,1] neg_hi:[0,0,1]\n\t"
"v_pk_fma_f32 v[234:235], v[80:81], s[60:61], v[230:231]\n\t"
"v_pk_fma_f32 v[80:81], v[230:231], s[60:61], v[80:81]\n\t"
"v_pk_fma_f32 v[230:231], v[180:181], s[62:63], v[232:233]\n\t"
"v_pk_fma_f32 v[180:181], v[232:233], s[62:63], v[180:181]\n\t"
"v_mfma_f32_16x16x32_bf16 v[56:59], a[252:255], a[200:203], v[56:59]\n\t"
"v_accvgpr_write_b32 a129, v224\n\t"
"v_accvgpr_write_b32 a131, v228\n\t"
"v_mfma_f32_16x16x32_bf16 v[56:59], a[224:227], a[196:199], v[56:59]\n\t"
"v_accvgpr_read_b32 v236, a138\n\t"
"v_accvgpr_read_b32 v238, a142\n\t"
"v_mfma_f32_16x16x32_bf16 v[56:59], a[208:211], a[192:195], v[56:59]\n\t"
"buffer_load_dwordx4 a[96:99], off, s[80:83], s20 offset:2048 sc1\n\t"
"v_accvgpr_read_b32 v237, a139\n\t"
"v_accvgpr_read_b32 v239, a143\n\t"
"v_cvt_pk_bf16_f32 v230, v230, v231\n\t"
"v_cvt_pk_bf16_f32 v234, v234, v235\n\t"
"s_mov_b32 s20, 774144\n\t"
"v_pk_fma_f32 v[236:237], v[238:239], s[56:57], v[236:237]\n\t"
"v_pk_fma_f32 v[238:239], v[236:237], s[58:59], v[238:239] neg_lo:[0,0,1] neg_hi:[0,0,1]\n\t"
"v_pk_fma_f32 v[98:99], v[82:83], s[60:61], v[236:237]\n\t"
"v_pk_fma_f32 v[82:83], v[236:237], s[60:61], v[82:83]\n\t"
"v_pk_fma_f32 v[236:237], v[182:183], s[62:63], v[238:239]\n\t"
"v_pk_fma_f32 v[182:183], v[238:239], s[62:63], v[182:183]\n\t"
"v_mfma_f32_16x16x32_bf16 v[60:63], a[208:211], a[196:199], v[60:63]\n\t"
"v_accvgpr_write_b32 a136, v230\n\t"
"v_accvgpr_write_b32 a138, v234\n\t"
"v_mfma_f32_16x16x32_bf16 v[60:63], a[224:227], a[200:203], v[60:63]\n\t"
"v_accvgpr_read_b32 v224, a144\n\t"
"v_accvgpr_read_b32 v226, a148\n\t"
"v_mfma_f32_16x16x32_bf16 v[60:63], a[252:255], a[204:207], v[60:63]\n\t"
"ds_write_b128 v245, v[204:207] offset:18432\n\t"
"v_accvgpr_read_b32 v225, a145\n\t"
"v_accvgpr_read_b32 v227, a149\n\t"
"v_cvt_pk_bf16_f32 v236, v236, v237\n\t"
"v_cvt_pk_bf16_f32 v98, v98, v99\n\t"
"v_pk_fma_f32 v[224:225], v[226:227], s[56:57], v[224:225]\n\t"
"v_pk_fma_f32 v[226:227], v[224:225], s[58:59], v[226:227] neg_lo:[0,0,1] neg_hi:[0,0,1]\n\t"
"v_pk_fma_f32 v[228:229], v[84:85], s[60:61], v[224:225]\n\t"
"v_pk_fma_f32 v[84:85], v[224:225], s[60:61], v[84:85]\n\t"
"v_pk_fma_f32 v[224:225], v[184:185], s[62:63], v[226:227]\n\t"
"v_pk_fma_f32 v[184:185], v[226:227], s[62:63], v[184:185]\n\t"
"v_mfma_f32_16x16x32_bf16 v[64:67], a[228:231], a[168:171], v[64:67]\n\t"
"ds_read_b128 a[168:171], v244 offset:9216\n\t"
"v_accvgpr_write_b32 a137, v236\n\t"
"v_mfma_f32_16x16x32_bf16 v[64:67], a[232:235], a[176:179], v[64:67]\n\t"
"v_accvgpr_write_b32 a139, v98\n\t"
"v_accvgpr_read_b32 v230, a146\n\t"
"v_mfma_f32_16x16x32_bf16 v[64:67], v[240:243], a[172:175], v[64:67]\n\t"
"buffer_load_dwordx4 a[100:103], off, s[80:83], s20 offset:0 sc1\n\t"
"v_accvgpr_read_b32 v232, a150\n\t"
"v_accvgpr_read_b32 v231, a147\n\t"
"v_accvgpr_read_b32 v233, a151\n\t"
"v_cvt_pk_bf16_f32 v224, v224, v225\n\t"
"v_cvt_pk_bf16_f32 v228, v228, v229\n\t"
"v_pk_fma_f32 v[230:231], v[232:233], s[56:57], v[230:231]\n\t"
"v_pk_fma_f32 v[232:233], v[230:231], s[58:59], v[232:233] neg_lo:[0,0,1] neg_hi:[0,0,1]\n\t"
"v_pk_fma_f32 v[234:235], v[86:87], s[60:61], v[230:231]\n\t"
"v_pk_fma_f32 v[86:87], v[230:231], s[60:61], v[86:87]\n\t"
"v_pk_fma_f32 v[230:231], v[186:187], s[62:63], v[232:233]\n\t"
"v_pk_fma_f32 v[186:187], v[232:233], s[62:63], v[186:187]\n\t"
"v_mfma_f32_16x16x32_bf16 v[68:71], a[228:231], a[172:175], v[68:71]\n\t"
"ds_read_b128 a[172:175], v244 offset:9728\n\t"
"v_accvgpr_write_b32 a144, v224\n\t"
"v_mfma_f32_16x16x32_bf16 v[68:71], v[240:243], a[176:179], v[68:71]\n\t"
"v_accvgpr_write_b32 a146, v228\n\t"
"v_accvgpr_read_b32 v236, a152\n\t"
"v_mfma_f32_16x16x32_bf16 v[68:71], a[232:235], a[180:183], v[68:71]\n\t"
"ds_write_b64 v246, a[104:105] offset:0\n\t"
"v_accvgpr_read_b32 v238, a156\n\t"
"v_accvgpr_read_b32 v237, a153\n\t"
"v_accvgpr_read_b32 v239, a157\n\t"
"v_cvt_pk_bf16_f32 v230, v230, v231\n\t"
"v_cvt_pk_bf16_f32 v234, v234, v235\n\t"
"v_pk_fma_f32 v[236:237], v[238:239], s[56:57], v[236:237]\n\t"
"v_pk_fma_f32 v[238:239], v[236:237], s[58:59], v[238:239] neg_lo:[0,0,1] neg_hi:[0,0,1]\n\t"
"v_pk_fma_f32 v[98:99], v[88:89], s[60:61], v[236:237]\n\t"
"v_pk_fma_f32 v[88:89], v[236:237], s[60:61], v[88:89]\n\t"
"v_pk_fma_f32 v[236:237], v[188:189], s[62:63], v[238:239]\n\t"
"v_pk_fma_f32 v[188:189], v[238:239], s[62:63], v[188:189]\n\t"
"v_mfma_f32_16x16x32_bf16 v[72:75], a[232:235], a[184:187], v[72:75]\n\t"
"v_accvgpr_write_b32 a145, v230\n\t"
"v_accvgpr_write_b32 a147, v234\n\t"
"v_mfma_f32_16x16x32_bf16 v[72:75], v[240:243], a[180:183], v[72:75]\n\t"
"v_accvgpr_read_b32 v224, a154\n\t"
"v_accvgpr_read_b32 v226, a158\n\t"
"v_mfma_f32_16x16x32_bf16 v[72:75], a[228:231], a[176:179], v[72:75]\n\t"
"buffer_load_dwordx4 a[208:211], off, s[80:83], s20 offset:1024 sc1\n\t"
"ds_read_b128 a[176:179], v244 offset:10240\n\t"
"v_accvgpr_read_b32 v225, a155\n\t"
"v_accvgpr_read_b32 v227, a159\n\t"
"v_cvt_pk_bf16_f32 v236, v236, v237\n\t"
"v_cvt_pk_bf16_f32 v98, v98, v99\n\t"
"v_pk_fma_f32 v[224:225], v[226:227], s[56:57], v[224:225]\n\t"
"v_pk_fma_f32 v[226:227], v[224:225], s[58:59], v[226:227] neg_lo:[0,0,1] neg_hi:[0,0,1]\n\t"
"v_pk_fma_f32 v[228:229], v[90:91], s[60:61], v[224:225]\n\t"
"v_pk_fma_f32 v[90:91], v[224:225], s[60:61], v[90:91]\n\t"
"v_pk_fma_f32 v[224:225], v[190:191], s[62:63], v[226:227]\n\t"
"v_pk_fma_f32 v[190:191], v[226:227], s[62:63], v[190:191]\n\t"
"v_mfma_f32_16x16x32_bf16 v[76:79], a[228:231], a[180:183], v[76:79]\n\t"
"ds_read_b128 a[180:183], v244 offset:10752\n\t"
"v_accvgpr_write_b32 a152, v236\n\t"
"v_mfma_f32_16x16x32_bf16 v[76:79], v[240:243], a[184:187], v[76:79]\n\t"
"v_accvgpr_write_b32 a154, v98\n\t"
"v_accvgpr_read_b32 v230, a160\n\t"
"v_mfma_f32_16x16x32_bf16 v[76:79], a[232:235], a[188:191], v[76:79]\n\t"
"ds_write_b64 v246, a[112:113] offset:18432\n\t"
"v_accvgpr_read_b32 v232, a164\n\t"
"v_accvgpr_read_b32 v231, a161\n\t"
"v_accvgpr_read_b32 v233, a165\n\t"
"v_cvt_pk_bf16_f32 v224, v224, v225\n\t"
"v_cvt_pk_bf16_f32 v228, v228, v229\n\t"
"v_pk_fma_f32 v[230:231], v[232:233], s[56:57], v[230:231]\n\t"
"v_pk_fma_f32 v[232:233], v[230:231], s[58:59], v[232:233] neg_lo:[0,0,1] neg_hi:[0,0,1]\n\t"
"v_pk_fma_f32 v[234:235], v[92:93], s[60:61], v[230:231]\n\t"
"v_pk_fma_f32 v[92:93], v[230:231], s[60:61], v[92:93]\n\t"
"v_pk_fma_f32 v[230:231], v[192:193], s[62:63], v[232:233]\n\t"
"v_pk_fma_f32 v[192:193], v[232:233], s[62:63], v[192:193]\n\t"
"v_mfma_f32_16x16x32_bf16 v[80:83], a[232:235], a[192:195], v[80:83]\n\t"
"v_accvgpr_write_b32 a153, v224\n\t"
"v_accvgpr_write_b32 a155, v228\n\t"
"v_mfma_f32_16x16x32_bf16 v[80:83], v[240:243], a[188:191], v[80:83]\n\t"
"v_accvgpr_read_b32 v236, a162\n\t"
"v_accvgpr_read_b32 v238, a166\n\t"
"v_mfma_f32_16x16x32_bf16 v[80:83], a[228:231], a[184:187], v[80:83]\n\t"
"buffer_load_dwordx4 a[224:227], off, s[80:83], s20 offset:2048 sc1\n\t"
"ds_read_b128 a[184:187], v244 offset:11264\n\t"
"v_accvgpr_read_b32 v237, a163\n\t"
"v_accvgpr_read_b32 v239, a167\n\t"
"v_cvt_pk_bf16_f32 v230, v230, v231\n\t"
"v_cvt_pk_bf16_f32 v234, v234, v235\n\t"
"s_mov_b32 s20, 786432\n\t"
"v_pk_fma_f32 v[236:237], v[238:239], s[56:57], v[236:237]\n\t"
"v_pk_fma_f32 v[238:239], v[236:237], s[58:59], v[238:239] neg_lo:[0,0,1] neg_hi:[0,0,1]\n\t"
"v_pk_fma_f32 v[98:99], v[94:95], s[60:61], v[236:237]\n\t"
"v_pk_fma_f32 v[94:95], v[236:237], s[60:61], v[94:95]\n\t"
"v_pk_fma_f32 v[236:237], v[194:195], s[62:63], v[238:239]\n\t"
"v_pk_fma_f32 v[194:195], v[238:239], s[62:63], v[194:195]\n\t"
"v_mfma_f32_16x16x32_bf16 v[84:87], a[228:231], a[188:191], v[84:87]\n\t"
"ds_read_b128 a[188:191], v244 offset:11776\n\t"
"v_accvgpr_write_b32 a160, v230\n\t"
"v_mfma_f32_16x16x32_bf16 v[84:87], v[240:243], a[192:195], v[84:87]\n\t"
"v_accvgpr_write_b32 a162, v234\n\t"
"v_cvt_pk_bf16_f32 v236, v236, v237\n\t"
"v_mfma_f32_16x16x32_bf16 v[84:87], a[232:235], a[196:199], v[84:87]\n\t"
"buffer_load_dwordx4 a[252:255], off, s[80:83], s20 offset:0 sc1\n\t"
"v_cvt_pk_bf16_f32 v98, v98, v99\n\t"
"v_mfma_f32_16x16x32_bf16 v[88:91], a[232:235], a[200:203], v[88:91]\n\t"
"v_accvgpr_write_b32 a161, v236\n\t"
"v_accvgpr_write_b32 a163, v98\n\t"
"v_mfma_f32_16x16x32_bf16 v[88:91], v[240:243], a[196:199], v[88:91]\n\t"
"v_mfma_f32_16x16x32_bf16 v[88:91], a[228:231], a[192:195], v[88:91]\n\t"
"buffer_load_dwordx4 a[108:111], off, s[80:83], s20 offset:1024 sc1\n\t"
"ds_write_b128 v245, v[200:203] offset:9216\n\t"
"ds_write_b128 v245, v[208:211] offset:27648\n\t"
"v_mfma_f32_16x16x32_bf16 v[92:95], a[228:231], a[196:199], v[92:95]\n\t"
"buffer_load_dwordx4 a[116:119], off, s[80:83], s20 offset:2048 sc1\n\t"
"v_mfma_f32_16x16x32_bf16 v[92:95], v[240:243], a[200:203], v[92:95]\n\t"
"s_waitcnt vmcnt(21) lgkmcnt(6)\n\t"
"ds_read_b128 a[192:195], v244 offset:12288\n\t"
"v_mfma_f32_16x16x32_bf16 v[92:95], a[232:235], a[204:207], v[92:95]\n\t"
"s_mov_b32 s20, 798720\n\t"
"ds_write_b64 v246, a[106:107] offset:9216\n\t"
"v_mfma_f32_16x16x32_bf16 v[100:103], a[236:239], a[168:171], v[100:103]\n\t"
"buffer_load_dwordx4 a[124:127], off, s[80:83], s20 offset:0 sc1\n\t"
"v_mfma_f32_16x16x32_bf16 v[100:103], a[60:63], a[176:179], v[100:103]\n\t"
"buffer_load_dwordx4 a[132:135], off, s[80:83], s20 offset:1024 sc1\n\t"
"v_mfma_f32_16x16x32_bf16 v[100:103], a[56:59], a[172:175], v[100:103]\n\t"
"s_waitcnt lgkmcnt(7)\n\t"
"ds_write_b64 v246, a[114:115] offset:27648\n\t"
"buffer_load_dwordx4 a[140:143], off, s[80:83], s20 offset:2048 sc1\n\t"
"s_mov_b32 s20, 1179648\n\t"
"v_mfma_f32_16x16x32_bf16 v[104:107], a[236:239], a[172:175], v[104:107]\n\t"
"s_waitcnt lgkmcnt(6)\n\t"
"ds_read_b128 a[196:199], v244 offset:12800\n\t"
"v_mfma_f32_16x16x32_bf16 v[104:107], a[56:59], a[176:179], v[104:107]\n\t"
"ds_read_b128 a[200:203], v244 offset:13312\n\t"
"v_mfma_f32_16x16x32_bf16 v[104:107], a[60:63], a[180:183], v[104:107]\n\t"
"ds_read_b128 a[204:207], v244 offset:13824\n\t"
"s_waitcnt lgkmcnt(0)\n\t"
"s_barrier\n\t"
"v_mfma_f32_16x16x32_bf16 v[108:111], a[60:63], a[184:187], v[108:111]\n\t"
"ds_read_b128 a[40:43], v255 offset:0\n\t"
"v_mfma_f32_16x16x32_bf16 v[108:111], a[56:59], a[180:183], v[108:111]\n\t"
"buffer_load_dwordx4 a[228:231], off, s[80:83], s20 offset:0 sc1\n\t"
"v_mfma_f32_16x16x32_bf16 v[108:111], a[236:239], a[176:179], v[108:111]\n\t"
"buffer_load_dwordx4 a[232:235], off, s[80:83], s20 offset:1024 sc1\n\t"
"v_mfma_f32_16x16x32_bf16 v[112:115], a[236:239], a[180:183], v[112:115]\n\t"
"buffer_load_dwordx4 a[148:151], off, s[80:83], s20 offset:2048 sc1\n\t"
"v_mfma_f32_16x16x32_bf16 v[112:115], a[56:59], a[184:187], v[112:115]\n\t"
"s_mov_b32 s20, 1474560\n\t"
"ds_read_b128 a[44:47], v255 offset:1536\n\t"
"s_add_u32 s29, s22, 384\n\t"
"v_mfma_f32_16x16x32_bf16 v[112:115], a[60:63], a[188:191], v[112:115]\n\t"
"buffer_load_dwordx4 a[104:107], off, s[80:83], s20 offset:0 sc1\n\t"
"v_mfma_f32_16x16x32_bf16 v[116:119], a[60:63], a[192:195], v[116:119]\n\t"
"s_waitcnt lgkmcnt(1)\n\t"
"ds_read_b128 a[48:51], v255 offset:3072\n\t"
"v_mfma_f32_16x16x32_bf16 v[116:119], a[56:59], a[188:191], v[116:119]\n\t"
"buffer_load_dwordx4 a[112:115], off, s[80:83], s20 offset:1024 sc1\n\t"
"v_mfma_f32_16x16x32_bf16 v[116:119], a[236:239], a[184:187], v[116:119]\n\t"
"ds_read_b128 a[52:55], v255 offset:18432\n\t"
"v_mfma_f32_16x16x32_bf16 v[120:123], a[236:239], a[188:191], v[120:123]\n\t"
"buffer_load_dwordx4 a[156:159], off, s[80:83], s20 offset:2048 sc1\n\t"
"v_mfma_f32_16x16x32_bf16 v[120:123], a[56:59], a[192:195], v[120:123]\n\t"
"s_mov_b32 s20, 1486848\n\t"
"ds_read_b128 v[196:199], v255 offset:19968\n\t"
"s_waitcnt lgkmcnt(3)\n\t"
"v_mfma_f32_16x16x32_bf16 v[120:123], a[60:63], a[196:199], v[120:123]\n\t"
"buffer_load_dwordx4 a[164:167], off, s[80:83], s20 offset:0 sc1\n\t"
"v_mfma_f32_16x16x32_bf16 v[124:127], a[60:63], a[200:203], v[124:127]\n\t"
"s_waitcnt lgkmcnt(2)\n\t"
"ds_read_b128 v[200:203], v255 offset:21504\n\t"
"v_mfma_f32_16x16x32_bf16 v[124:127], a[56:59], a[196:199], v[124:127]\n\t"
"buffer_load_dwordx4 v[240:243], off, s[80:83], s20 offset:1024 sc1\n\t"
"v_mfma_f32_16x16x32_bf16 v[124:127], a[236:239], a[192:195], v[124:127]\n\t"
"ds_write_b128 v245, v[212:215] offset:36864\n\t"
"v_mfma_f32_16x16x32_bf16 v[128:131], a[236:239], a[196:199], v[128:131]\n\t"
"s_waitcnt vmcnt(29)\n\t"
"s_waitcnt lgkmcnt(3)\n\t"
"v_mfma_f32_16x16x32_bf16 v[128:131], a[56:59], a[200:203], v[128:131]\n\t"
"buffer_load_dwordx4 a[236:239], off, s[80:83], s20 offset:2048 sc1\n\t"
"v_mfma_f32_16x16x32_bf16 v[128:131], a[60:63], a[204:207], v[128:131]\n\t"
"s_mov_b32 s20, 1191936\n\t"
"ds_write_b128 v249, a[0:3] offset:0\n\t"
"v_mfma_f32_16x16x32_bf16 v[132:135], a[212:215], a[168:171], v[132:135]\n\t"
"s_waitcnt lgkmcnt(3)\n\t"
"v_mfma_f32_16x16x32_bf16 v[132:135], a[244:247], a[176:179], v[132:135]\n\t"
"v_mfma_f32_16x16x32_bf16 v[132:135], a[240:243], a[172:175], v[132:135]\n\t"
"ds_write_b128 v249, a[8:11] offset:18432\n\t"
"ds_write_b128 v249, a[16:19] offset:36864\n\t"
"buffer_store_dwordx4 a[40:43], v248, s[16:19], s29 offen offset:0 nt\n\t"
"v_mfma_f32_16x16x32_bf16 v[136:139], a[212:215], a[172:175], v[136:139]\n\t"
"s_waitcnt lgkmcnt(4)\n\t"
"v_mfma_f32_16x16x32_bf16 v[136:139], a[240:243], a[176:179], v[136:139]\n\t"
"buffer_load_dwordx4 a[40:43], off, s[80:83], s20 offset:0 sc1\n\t"
"v_mfma_f32_16x16x32_bf16 v[136:139], a[244:247], a[180:183], v[136:139]\n\t"
"buffer_store_dwordx4 a[44:47], v248, s[16:19], s29 offen offset:128 nt\n\t"
"ds_write_b64 v246, a[120:121] offset:36864\n\t"
"v_mfma_f32_16x16x32_bf16 v[140:143], a[244:247], a[184:187], v[140:143]\n\t"
"v_mfma_f32_16x16x32_bf16 v[140:143], a[240:243], a[180:183], v[140:143]\n\t"
"v_mfma_f32_16x16x32_bf16 v[140:143], a[212:215], a[176:179], v[140:143]\n\t"
"buffer_store_dwordx4 a[48:51], v248, s[16:19], s29 offen offset:256 nt\n\t"
"ds_write_b64 v250, a[128:129] offset:0\n\t"
"s_add_u32 s29, s22, 196992\n\t"
"v_mfma_f32_16x16x32_bf16 v[144:147], a[212:215], a[180:183], v[144:147]\n\t"
"v_mfma_f32_16x16x32_bf16 v[144:147], a[240:243], a[184:187], v[144:147]\n\t"
"buffer_load_dwordx4 a[44:47], off, s[80:83], s20 offset:1024 sc1\n\t"
"v_mfma_f32_16x16x32_bf16 v[144:147], a[244:247], a[188:191], v[144:147]\n\t"
"ds_write_b64 v250, a[136:137] offset:18432\n\t"
"ds_write_b64 v250, a[144:145] offset:36864\n\t"
"buffer_store_dwordx4 a[52:55], v248, s[16:19], s29 offen offset:0 nt\n\t"
"ds_write_b64 v246, a[122:123] offset:46080\n\t"
"v_mfma_f32_16x16x32_bf16 v[148:151], a[244:247], a[192:195], v[148:151]\n\t"
"v_mfma_f32_16x16x32_bf16 v[148:151], a[240:243], a[188:191], v[148:151]\n\t"
"v_mfma_f32_16x16x32_bf16 v[148:151], a[212:215], a[184:187], v[148:151]\n\t"
"buffer_store_dwordx4 v[196:199], v248, s[16:19], s29 offen offset:128 nt\n\t"
"buffer_load_dwordx4 a[48:51], off, s[80:83], s20 offset:2048 sc1\n\t"
"s_mov_b32 s20, 1204224\n\t"
"v_mfma_f32_16x16x32_bf16 v[152:155], a[212:215], a[188:191], v[152:155]\n\t"
"v_mfma_f32_16x16x32_bf16 v[152:155], a[240:243], a[192:195], v[152:155]\n\t"
"v_mfma_f32_16x16x32_bf16 v[152:155], a[244:247], a[196:199], v[152:155]\n\t"
"buffer_store_dwordx4 v[200:203], v248, s[16:19], s29 offen offset:256 nt\n\t"
"ds_write_b64 v250, a[130:131] offset:9216\n\t"
"v_mfma_f32_16x16x32_bf16 v[156:159], a[244:247], a[200:203], v[156:159]\n\t"
"v_mfma_f32_16x16x32_bf16 v[156:159], a[240:243], a[196:199], v[156:159]\n\t"
"buffer_load_dwordx4 a[52:55], off, s[80:83], s20 offset:0 sc1\n\t"
"v_mfma_f32_16x16x32_bf16 v[156:159], a[212:215], a[192:195], v[156:159]\n\t"
"ds_write_b128 v245, v[216:219] offset:46080\n\t"
"v_mfma_f32_16x16x32_bf16 v[160:163], a[212:215], a[196:199], v[160:163]\n\t"
"s_waitcnt vmcnt(37)\n\t"
"v_mfma_f32_16x16x32_bf16 v[160:163], a[240:243], a[200:203], v[160:163]\n\t"
"buffer_load_dwordx4 a[212:215], off, s[80:83], s20 offset:1024 sc1\n\t"
"v_mfma_f32_16x16x32_bf16 v[160:163], a[244:247], a[204:207], v[160:163]\n\t"
"ds_write_b128 v249, a[4:7] offset:9216\n\t"
"ds_write_b128 v249, a[12:15] offset:27648\n\t"
"ds_write_b128 v249, a[20:23] offset:46080\n\t"
"v_mfma_f32_16x16x32_bf16 v[164:167], a[64:67], a[168:171], v[164:167]\n\t"
"v_mfma_f32_16x16x32_bf16 v[164:167], a[72:75], a[176:179], v[164:167]\n\t"
"ds_read_b128 a[168:171], v244 offset:24576\n\t"
"v_mfma_f32_16x16x32_bf16 v[164:167], a[68:71], a[172:175], v[164:167]\n\t"
"ds_write_b64 v250, a[138:139] offset:27648\n\t"
"v_mfma_f32_16x16x32_bf16 v[168:171], a[64:67], a[172:175], v[168:171]\n\t"
"buffer_load_dwordx4 a[240:243], off, s[80:83], s20 offset:2048 sc1\n\t"
"v_mfma_f32_16x16x32_bf16 v[168:171], a[68:71], a[176:179], v[168:171]\n\t"
"s_mov_b32 s20, 1499136\n\t"
"ds_read_b128 a[172:175], v244 offset:25088\n\t"
"v_mfma_f32_16x16x32_bf16 v[168:171], a[72:75], a[180:183], v[168:171]\n\t"
"ds_write_b64 v250, a[146:147] offset:46080\n\t"
"v_mfma_f32_16x16x32_bf16 v[172:175], a[72:75], a[184:187], v[172:175]\n\t"
"buffer_load_dwordx4 a[244:247], off, s[80:83], s20 offset:0 sc1\n\t"
"v_mfma_f32_16x16x32_bf16 v[172:175], a[68:71], a[180:183], v[172:175]\n\t"
"v_mfma_f32_16x16x32_bf16 v[172:175], a[64:67], a[176:179], v[172:175]\n\t"
"ds_read_b128 a[176:179], v244 offset:25600\n\t"
"v_mfma_f32_16x16x32_bf16 v[176:179], a[64:67], a[180:183], v[176:179]\n\t"
"Lpg24_%=:\n\t"
"s_cbranch_vccnz Lvr3_%=\n\t"
"ds_read_b128 a[180:183], v244 offset:26112\n\t"
"v_mfma_f32_16x16x32_bf16 v[176:179], a[68:71], a[184:187], v[176:179]\n\t"
"v_mfma_f32_16x16x32_bf16 v[176:179], a[72:75], a[188:191], v[176:179]\n\t"
"s_waitcnt lgkmcnt(2)\n\t"
"s_barrier\n\t"
"v_mfma_f32_16x16x32_bf16 v[180:183], a[72:75], a[192:195], v[180:183]\n\t"
"ds_read_b128 v[196:199], v255 offset:36864\n\t"
"v_mfma_f32_16x16x32_bf16 v[180:183], a[68:71], a[188:191], v[180:183]\n\t"
"buffer_load_dwordx4 a[56:59], off, s[80:83], s20 offset:1024 sc1\n\t"
"v_mfma_f32_16x16x32_bf16 v[180:183], a[64:67], a[184:187], v[180:183]\n\t"
"ds_read_b128 v[200:203], v255 offset:38400\n\t"
"v_mfma_f32_16x16x32_bf16 v[184:187], a[64:67], a[188:191], v[184:187]\n\t"
"s_add_u32 s29, s22, 393600\n\t"
"ds_read_b128 a[184:187], v244 offset:26624\n\t"
"v_mfma_f32_16x16x32_bf16 v[184:187], a[68:71], a[192:195], v[184:187]\n\t"
"buffer_load_dwordx4 a[20:23], off, s[80:83], s20 offset:2048 sc1\n\t"
"v_mfma_f32_16x16x32_bf16 v[184:187], a[72:75], a[196:199], v[184:187]\n\t"
"s_mov_b32 s20, 1363968\n\t"
"ds_read_b128 v[204:207], v255 offset:39936\n\t"
"v_mfma_f32_16x16x32_bf16 v[188:191], a[72:75], a[200:203], v[188:191]\n\t"
"s_waitcnt lgkmcnt(3)\n\t"
"ds_read_b128 a[188:191], v244 offset:27136\n\t"
"v_mfma_f32_16x16x32_bf16 v[188:191], a[68:71], a[196:199], v[188:191]\n\t"
"buffer_load_dwordx4 a[60:63], off, s[80:83], s20 offset:0 sc1\n\t"
"v_mfma_f32_16x16x32_bf16 v[188:191], a[64:67], a[192:195], v[188:191]\n\t"
"ds_read_b128 v[208:211], v247 offset:0\n\t"
"v_mfma_f32_16x16x32_bf16 v[192:195], a[64:67], a[196:199], v[192:195]\n\t"
"s_waitcnt vmcnt(40)\n\t"
"ds_read_b128 a[192:195], v244 offset:27648\n\t"
"s_waitcnt lgkmcnt(5)\n\t"
"v_mfma_f32_16x16x32_bf16 v[192:195], a[68:71], a[200:203], v[192:195]\n\t"
"buffer_load_dwordx4 a[120:123], off, s[80:83], s20 offset:1024 sc1\n\t"
"v_mfma_f32_16x16x32_bf16 v[192:195], a[72:75], a[204:207], v[192:195]\n\t"
"ds_write_b128 v245, a[24:27] offset:0\n\t"
"Lst33_%=:\n\t"
"v_mfma_f32_16x16x32_bf16 v[0:3], a[76:79], a[168:171], v[0:3]\n\t"
"s_waitcnt lgkmcnt(4)\n\t"
"v_mfma_f32_16x16x32_bf16 v[0:3], a[216:219], a[176:179], v[0:3]\n\t"
"ds_read_b128 v[212:215], v247 offset:1536\n\t"
"v_mfma_f32_16x16x32_bf16 v[0:3], a[80:83], a[172:175], v[0:3]\n\t"
"ds_write_b128 v245, a[32:35] offset:18432\n\t"
"v_mfma_f32_16x16x32_bf16 v[4:7], a[76:79], a[172:175], v[4:7]\n\t"
"s_waitcnt lgkmcnt(4)\n\t"
"v_mfma_f32_16x16x32_bf16 v[4:7], a[80:83], a[176:179], v[4:7]\n\t"
"ds_read_b128 a[196:199], v244 offset:28160\n\t"
"v_mfma_f32_16x16x32_bf16 v[4:7], a[216:219], a[180:183], v[4:7]\n\t"
"buffer_store_dwordx4 v[196:199], v248, s[16:19], s29 offen offset:0 nt\n\t"
"ds_read_b128 a[200:203], v244 offset:28672\n\t"
"ds_read_b128 a[204:207], v244 offset:29184\n\t"
"v_mfma_f32_16x16x32_bf16 v[8:11], a[216:219], a[184:187], v[8:11]\n\t"
"s_waitcnt lgkmcnt(4)\n\t"
"ds_read_b128 v[216:219], v247 offset:3072\n\t"
"v_mfma_f32_16x16x32_bf16 v[8:11], a[80:83], a[180:183], v[8:11]\n\t"
"v_mfma_f32_16x16x32_bf16 v[8:11], a[76:79], a[176:179], v[8:11]\n\t"
"buffer_store_dwordx4 v[200:203], v248, s[16:19], s29 offen offset:128 nt\n\t"
"ds_write_b64 v246, a[152:153] offset:0\n\t"
"v_mfma_f32_16x16x32_bf16 v[12:15], a[76:79], a[180:183], v[12:15]\n\t"
"v_mfma_f32_16x16x32_bf16 v[12:15], a[80:83], a[184:187], v[12:15]\n\t"
"v_mfma_f32_16x16x32_bf16 v[12:15], a[216:219], a[188:191], v[12:15]\n\t"
"ds_write_b64 v246, a[160:161] offset:18432\n\t"
"v_mfma_f32_16x16x32_bf16 v[16:19], a[216:219], a[192:195], v[16:19]\n\t"
"s_waitcnt lgkmcnt(5)\n\t"
"v_mfma_f32_16x16x32_bf16 v[16:19], a[80:83], a[188:191], v[16:19]\n\t"
"v_mfma_f32_16x16x32_bf16 v[16:19], a[76:79], a[184:187], v[16:19]\n\t"
"buffer_store_dwordx4 v[204:207], v248, s[16:19], s29 offen offset:256 nt\n\t"
"ds_read_b128 v[196:199], v247 offset:18432\n\t"
"ds_read_b128 a[0:3], v247 offset:36864\n\t"
"s_add_u32 s29, s22, 590208\n\t"
"v_mfma_f32_16x16x32_bf16 v[20:23], a[76:79], a[188:191], v[20:23]\n\t"
"s_waitcnt lgkmcnt(6)\n\t"
"v_mfma_f32_16x16x32_bf16 v[20:23], a[80:83], a[192:195], v[20:23]\n\t"
"ds_read_b128 v[200:203], v247 offset:19968\n\t"
"v_mfma_f32_16x16x32_bf16 v[20:23], a[216:219], a[196:199], v[20:23]\n\t"
"buffer_store_dwordx4 v[208:211], v248, s[16:19], s29 offen offset:0 nt\n\t"
"buffer_load_dwordx4 a[64:67], off, s[80:83], s20 offset:2048 sc1\n\t"
"s_mov_b32 s20, 1658880\n\t"
"v_mfma_f32_16x16x32_bf16 v[24:27], a[216:219], a[200:203], v[24:27]\n\t"
"s_waitcnt lgkmcnt(6)\n\t"
"ds_read_b128 v[208:211], v247 offset:38400\n\t"
"v_mfma_f32_16x16x32_bf16 v[24:27], a[80:83], a[196:199], v[24:27]\n\t"
"ds_read_b128 v[204:207], v247 offset:21504\n\t"
"v_mfma_f32_16x16x32_bf16 v[24:27], a[76:79], a[192:195], v[24:27]\n\t"
"buffer_store_dwordx4 v[212:215], v248, s[16:19], s29 offen offset:128 nt\n\t"
"ds_write_b64 v246, a[154:155] offset:9216\n\t"
"s_waitcnt lgkmcnt(8)\n\t"
"v_mfma_f32_16x16x32_bf16 v[28:31], a[76:79], a[196:199], v[28:31]\n\t"
"s_waitcnt vmcnt(44)\n\t"
"ds_read_b128 v[212:215], v247 offset:39936\n\t"
"v_mfma_f32_16x16x32_bf16 v[28:31], a[80:83], a[200:203], v[28:31]\n\t"
"buffer_load_dwordx4 a[68:71], off, s[80:83], s20 offset:0 sc1\n\t"
"v_mfma_f32_16x16x32_bf16 v[28:31], a[216:219], a[204:207], v[28:31]\n\t"
"ds_write_b128 v245, a[28:31] offset:9216\n\t"
"v_mfma_f32_16x16x32_bf16 v[32:35], a[220:223], a[168:171], v[32:35]\n\t"
"v_mfma_f32_16x16x32_bf16 v[32:35], a[84:87], a[176:179], v[32:35]\n\t"
"v_mfma_f32_16x16x32_bf16 v[32:35], a[248:251], a[172:175], v[32:35]\n\t"
"ds_write_b128 v245, a[36:39] offset:27648\n\t"
"v_mfma_f32_16x16x32_bf16 v[36:39], a[220:223], a[172:175], v[36:39]\n\t"
"v_mfma_f32_16x16x32_bf16 v[36:39], a[248:251], a[176:179], v[36:39]\n\t"
"v_mfma_f32_16x16x32_bf16 v[36:39], a[84:87], a[180:183], v[36:39]\n\t"
"ds_write_b64 v246, a[162:163] offset:27648\n\t"
"v_mfma_f32_16x16x32_bf16 v[40:43], a[84:87], a[184:187], v[40:43]\n\t"
"buffer_load_dwordx4 a[216:219], off, s[80:83], s20 offset:1024 sc1\n\t"
"v_mfma_f32_16x16x32_bf16 v[40:43], a[248:251], a[180:183], v[40:43]\n\t"
"v_mfma_f32_16x16x32_bf16 v[40:43], a[220:223], a[176:179], v[40:43]\n\t"
"buffer_store_dwordx4 v[216:219], v248, s[16:19], s29 offen offset:256 nt\n\t"
"v_mfma_f32_16x16x32_bf16 v[44:47], a[220:223], a[180:183], v[44:47]\n\t"
"s_add_u32 s29, s22, 786816\n\t"
"v_mfma_f32_16x16x32_bf16 v[44:47], a[248:251], a[184:187], v[44:47]\n\t"
"v_mfma_f32_16x16x32_bf16 v[44:47], a[84:87], a[188:191], v[44:47]\n\t"
"s_waitcnt lgkmcnt(0)\n\t"
"s_barrier\n\t"
"v_mfma_f32_16x16x32_bf16 v[48:51], a[84:87], a[192:195], v[48:51]\n\t"
"v_mfma_f32_16x16x32_bf16 v[48:51], a[248:251], a[188:191], v[48:51]\n\t"
"buffer_load_dwordx4 a[72:75], off, s[80:83], s20 offset:2048 sc1\n\t"
"v_mfma_f32_16x16x32_bf16 v[48:51], a[220:223], a[184:187], v[48:51]\n\t"
"s_mov_b32 s20, 1671168\n\t"
"v_mfma_f32_16x16x32_bf16 v[52:55], a[220:223], a[188:191], v[52:55]\n\t"
"buffer_load_dwordx4 a[76:79], off, s[80:83], s20 offset:0 sc1\n\t"
"v_mfma_f32_16x16x32_bf16 v[52:55], a[248:251], a[192:195], v[52:55]\n\t"
"v_mfma_f32_16x16x32_bf16 v[52:55], a[84:87], a[196:199], v[52:55]\n\t"
"buffer_store_dwordx4 v[196:199], v248, s[16:19], s29 offen offset:0 nt\n\t"
"v_mfma_f32_16x16x32_bf16 v[56:59], a[84:87], a[200:203], v[56:59]\n\t"
"v_mfma_f32_16x16x32_bf16 v[56:59], a[248:251], a[196:199], v[56:59]\n\t"
"buffer_load_dwordx4 a[24:27], off, s[80:83], s20 offset:1024 sc1\n\t"
"v_mfma_f32_16x16x32_bf16 v[56:59], a[220:223], a[192:195], v[56:59]\n\t"
"v_mfma_f32_16x16x32_bf16 v[60:63], a[220:223], a[196:199], v[60:63]\n\t"
"s_waitcnt vmcnt(48)\n\t"
"v_mfma_f32_16x16x32_bf16 v[60:63], a[248:251], a[200:203], v[60:63]\n\t"
"buffer_load_dwordx4 a[220:223], off, s[80:83], s20 offset:2048 sc1\n\t"
"v_mfma_f32_16x16x32_bf16 v[60:63], a[84:87], a[204:207], v[60:63]\n\t"
"s_mov_b32 s20, 1376256\n\t"
"v_mfma_f32_16x16x32_bf16 v[64:67], a[88:91], a[168:171], v[64:67]\n\t"
"ds_read_b128 a[168:171], v244 offset:33792\n\t"
"v_mfma_f32_16x16x32_bf16 v[64:67], a[96:99], a[176:179], v[64:67]\n\t"
"v_mfma_f32_16x16x32_bf16 v[64:67], a[92:95], a[172:175], v[64:67]\n\t"
"buffer_load_dwordx4 a[248:251], off, s[80:83], s20 offset:0 sc1\n\t"
"v_mfma_f32_16x16x32_bf16 v[68:71], a[88:91], a[172:175], v[68:71]\n\t"
"ds_read_b128 a[172:175], v244 offset:34304\n\t"
"v_mfma_f32_16x16x32_bf16 v[68:71], a[92:95], a[176:179], v[68:71]\n\t"
"v_mfma_f32_16x16x32_bf16 v[68:71], a[96:99], a[180:183], v[68:71]\n\t"
"v_mfma_f32_16x16x32_bf16 v[72:75], a[96:99], a[184:187], v[72:75]\n\t"
"v_mfma_f32_16x16x32_bf16 v[72:75], a[92:95], a[180:183], v[72:75]\n\t"
"v_mfma_f32_16x16x32_bf16 v[72:75], a[88:91], a[176:179], v[72:75]\n\t"
"ds_read_b128 a[176:179], v244 offset:34816\n\t"
"v_mfma_f32_16x16x32_bf16 v[76:79], a[88:91], a[180:183], v[76:79]\n\t"
"ds_read_b128 a[180:183], v244 offset:35328\n\t"
"v_mfma_f32_16x16x32_bf16 v[76:79], a[92:95], a[184:187], v[76:79]\n\t"
"buffer_load_dwordx4 a[28:31], off, s[80:83], s20 offset:1024 sc1\n\t"
"v_mfma_f32_16x16x32_bf16 v[76:79], a[96:99], a[188:191], v[76:79]\n\t"
"buffer_store_dwordx4 v[200:203], v248, s[16:19], s29 offen offset:128 nt\n\t"
"ds_read_b128 v[196:199], v255 offset:0\n\t"
"ds_read_b128 a[8:11], v255 offset:18432\n\t"
"v_mfma_f32_16x16x32_bf16 v[80:83], a[96:99], a[192:195], v[80:83]\n\t"
"ds_read_b128 v[216:219], v255 offset:1536\n\t"
"v_mfma_f32_16x16x32_bf16 v[80:83], a[92:95], a[188:191], v[80:83]\n\t"
"ds_read_b128 a[12:15], v255 offset:19968\n\t"
"v_mfma_f32_16x16x32_bf16 v[80:83], a[88:91], a[184:187], v[80:83]\n\t"
"ds_read_b128 a[184:187], v244 offset:35840\n\t"
"v_mfma_f32_16x16x32_bf16 v[84:87], a[88:91], a[188:191], v[84:87]\n\t"
"buffer_load_dwordx4 a[32:35], off, s[80:83], s20 offset:2048 sc1\n\t"
"v_mfma_f32_16x16x32_bf16 v[84:87], a[92:95], a[192:195], v[84:87]\n\t"
"s_mov_b32 s20, 1388544\n\t"
"ds_read_b128 a[188:191], v244 offset:36352\n\t"
"v_mfma_f32_16x16x32_bf16 v[84:87], a[96:99], a[196:199], v[84:87]\n\t"
"buffer_load_dwordx4 a[36:39], off, s[80:83], s20 offset:0 sc1\n\t"
"v_mfma_f32_16x16x32_bf16 v[88:91], a[96:99], a[200:203], v[88:91]\n\t"
"ds_read_b128 a[4:7], v255 offset:3072\n\t"
"v_mfma_f32_16x16x32_bf16 v[88:91], a[92:95], a[196:199], v[88:91]\n\t"
"ds_read_b128 a[16:19], v255 offset:21504\n\t"
"v_mfma_f32_16x16x32_bf16 v[88:91], a[88:91], a[192:195], v[88:91]\n\t"
"ds_read_b128 a[192:195], v244 offset:36864\n\t"
"v_mfma_f32_16x16x32_bf16 v[92:95], a[88:91], a[196:199], v[92:95]\n\t"
"buffer_load_dwordx4 a[80:83], off, s[80:83], s20 offset:1024 sc1\n\t"
"v_mfma_f32_16x16x32_bf16 v[92:95], a[92:95], a[200:203], v[92:95]\n\t"
"s_waitcnt vmcnt(52) lgkmcnt(10)\n\t"
"ds_read_b128 a[196:199], v244 offset:37376\n\t"
"v_mfma_f32_16x16x32_bf16 v[92:95], a[96:99], a[204:207], v[92:95]\n\t"
"ds_read_b128 a[200:203], v244 offset:37888\n\t"
"v_mfma_f32_16x16x32_bf16 v[100:103], a[100:103], a[168:171], v[100:103]\n\t"
"s_waitcnt lgkmcnt(11)\n\t"
"ds_read_b128 a[204:207], v244 offset:38400\n\t"
"v_mfma_f32_16x16x32_bf16 v[100:103], a[224:227], a[176:179], v[100:103]\n\t"
"v_mfma_f32_16x16x32_bf16 v[100:103], a[208:211], a[172:175], v[100:103]\n\t"
"buffer_store_dwordx4 v[204:207], v248, s[16:19], s29 offen offset:256 nt\n\t"
"v_mfma_f32_16x16x32_bf16 v[104:107], a[100:103], a[172:175], v[104:107]\n\t"
"s_waitcnt lgkmcnt(7)\n\t"
"v_mfma_f32_16x16x32_bf16 v[104:107], a[208:211], a[176:179], v[104:107]\n\t"
"v_mfma_f32_16x16x32_bf16 v[104:107], a[224:227], a[180:183], v[104:107]\n\t"
"s_waitcnt lgkmcnt(0)\n\t"
"s_barrier\n\t"
"v_mfma_f32_16x16x32_bf16 v[108:111], a[224:227], a[184:187], v[108:111]\n\t"
"v_mfma_f32_16x16x32_bf16 v[108:111], a[208:211], a[180:183], v[108:111]\n\t"
"buffer_load_dwordx4 a[84:87], off, s[80:83], s20 offset:2048 sc1\n\t"
"v_mfma_f32_16x16x32_bf16 v[108:111], a[100:103], a[176:179], v[108:111]\n\t"
"s_mov_b32 s20, 1683456\n\t"
"s_add_u32 s29, s22, 983424\n\t"
"v_mfma_f32_16x16x32_bf16 v[112:115], a[100:103], a[180:183], v[112:115]\n\t"
"buffer_load_dwordx4 a[88:91], off, s[80:83], s20 offset:0 sc1\n\t"
"v_mfma_f32_16x16x32_bf16 v[112:115], a[208:211], a[184:187], v[112:115]\n\t"
"v_mfma_f32_16x16x32_bf16 v[112:115], a[224:227], a[188:191], v[112:115]\n\t"
"buffer_load_dwordx4 a[92:95], off, s[80:83], s20 offset:1024 sc1\n\t"
"v_mfma_f32_16x16x32_bf16 v[116:119], a[224:227], a[192:195], v[116:119]\n\t"
"v_mfma_f32_16x16x32_bf16 v[116:119], a[208:211], a[188:191], v[116:119]\n\t"
"v_mfma_f32_16x16x32_bf16 v[116:119], a[100:103], a[184:187], v[116:119]\n\t"
"buffer_store_dwordx4 a[0:3], v248, s[16:19], s29 offen offset:0 nt\n\t"
"v_mfma_f32_16x16x32_bf16 v[120:123], a[100:103], a[188:191], v[120:123]\n\t"
"v_mfma_f32_16x16x32_bf16 v[120:123], a[208:211], a[192:195], v[120:123]\n\t"
"v_mfma_f32_16x16x32_bf16 v[120:123], a[224:227], a[196:199], v[120:123]\n\t"
"v_mfma_f32_16x16x32_bf16 v[124:127], a[224:227], a[200:203], v[124:127]\n\t"
"buffer_load_dwordx4 a[96:99], off, s[80:83], s20 offset:2048 sc1\n\t"
"v_mfma_f32_16x16x32_bf16 v[124:127], a[208:211], a[196:199], v[124:127]\n\t"
"v_mfma_f32_16x16x32_bf16 v[124:127], a[100:103], a[192:195], v[124:127]\n\t"
"v_mfma_f32_16x16x32_bf16 v[128:131], a[100:103], a[196:199], v[128:131]\n\t"
"s_waitcnt vmcnt(55)\n\t"
"v_mfma_f32_16x16x32_bf16 v[128:131], a[208:211], a[200:203], v[128:131]\n\t"
"v_mfma_f32_16x16x32_bf16 v[128:131], a[224:227], a[204:207], v[128:131]\n\t"
"v_mfma_f32_16x16x32_bf16 v[132:135], a[252:255], a[168:171], v[132:135]\n\t"
"v_mfma_f32_16x16x32_bf16 v[132:135], a[116:119], a[176:179], v[132:135]\n\t"
"v_mfma_f32_16x16x32_bf16 v[132:135], a[108:111], a[172:175], v[132:135]\n\t"
"v_mfma_f32_16x16x32_bf16 v[136:139], a[252:255], a[172:175], v[136:139]\n\t"
"v_mfma_f32_16x16x32_bf16 v[136:139], a[108:111], a[176:179], v[136:139]\n\t"
"v_mfma_f32_16x16x32_bf16 v[136:139], a[116:119], a[180:183], v[136:139]\n\t"
"v_mfma_f32_16x16x32_bf16 v[140:143], a[116:119], a[184:187], v[140:143]\n\t"
"v_mfma_f32_16x16x32_bf16 v[140:143], a[108:111], a[180:183], v[140:143]\n\t"
"v_mfma_f32_16x16x32_bf16 v[140:143], a[252:255], a[176:179], v[140:143]\n\t"
"buffer_store_dwordx4 v[208:211], v248, s[16:19], s29 offen offset:128 nt\n\t"
"v_mfma_f32_16x16x32_bf16 v[144:147], a[252:255], a[180:183], v[144:147]\n\t"
"v_mfma_f32_16x16x32_bf16 v[144:147], a[108:111], a[184:187], v[144:147]\n\t"
"v_mfma_f32_16x16x32_bf16 v[144:147], a[116:119], a[188:191], v[144:147]\n\t"
"v_mfma_f32_16x16x32_bf16 v[148:151], a[116:119], a[192:195], v[148:151]\n\t"
"v_mfma_f32_16x16x32_bf16 v[148:151], a[108:111], a[188:191], v[148:151]\n\t"
"v_mfma_f32_16x16x32_bf16 v[148:151], a[252:255], a[184:187], v[148:151]\n\t"
"v_mfma_f32_16x16x32_bf16 v[152:155], a[252:255], a[188:191], v[152:155]\n\t"
"v_mfma_f32_16x16x32_bf16 v[152:155], a[108:111], a[192:195], v[152:155]\n\t"
"v_mfma_f32_16x16x32_bf16 v[152:155], a[116:119], a[196:199], v[152:155]\n\t"
"v_mfma_f32_16x16x32_bf16 v[156:159], a[116:119], a[200:203], v[156:159]\n\t"
"v_mfma_f32_16x16x32_bf16 v[156:159], a[108:111], a[196:199], v[156:159]\n\t"
"v_mfma_f32_16x16x32_bf16 v[156:159], a[252:255], a[192:195], v[156:159]\n\t"
"v_mfma_f32_16x16x32_bf16 v[160:163], a[252:255], a[196:199], v[160:163]\n\t"
"s_waitcnt vmcnt(53)\n\t"
"v_mfma_f32_16x16x32_bf16 v[160:163], a[108:111], a[200:203], v[160:163]\n\t"
"v_mfma_f32_16x16x32_bf16 v[160:163], a[116:119], a[204:207], v[160:163]\n\t"
"buffer_store_dwordx4 v[212:215], v248, s[16:19], s29 offen offset:256 nt\n\t"
"v_mfma_f32_16x16x32_bf16 v[164:167], a[124:127], a[168:171], v[164:167]\n\t"
"ds_read_b128 a[168:171], v222 offset:2048\n\t"
"v_mfma_f32_16x16x32_bf16 v[164:167], a[140:143], a[176:179], v[164:167]\n\t"
"ds_read_b128 v[200:203], v223 offset:8192\n\t"
"v_mfma_f32_16x16x32_bf16 v[164:167], a[132:135], a[172:175], v[164:167]\n\t"
"ds_read_b128 v[208:211], v223 offset:14336\n\t"
"v_mfma_f32_16x16x32_bf16 v[168:171], a[124:127], a[172:175], v[168:171]\n\t"
"ds_read_b128 a[172:175], v222 offset:512\n\t"
"v_mfma_f32_16x16x32_bf16 v[168:171], a[132:135], a[176:179], v[168:171]\n\t"
"ds_read_b128 v[204:207], v223 offset:6656\n\t"
"v_mfma_f32_16x16x32_bf16 v[168:171], a[140:143], a[180:183], v[168:171]\n\t"
"ds_read_b128 a[0:3], v223 offset:12800\n\t"
"v_mfma_f32_16x16x32_bf16 v[172:175], a[140:143], a[184:187], v[172:175]\n\t"
"v_mfma_f32_16x16x32_bf16 v[172:175], a[132:135], a[180:183], v[172:175]\n\t"
"v_mfma_f32_16x16x32_bf16 v[172:175], a[124:127], a[176:179], v[172:175]\n\t"
"s_add_u32 s29, s22, 1180032\n\t"
"v_mfma_f32_16x16x32_bf16 v[176:179], a[124:127], a[180:183], v[176:179]\n\t"
"v_mfma_f32_16x16x32_bf16 v[176:179], a[132:135], a[184:187], v[176:179]\n\t"
"v_mfma_f32_16x16x32_bf16 v[176:179], a[140:143], a[188:191], v[176:179]\n\t"
"s_barrier\n\t"
"v_mfma_f32_16x16x32_bf16 v[180:183], a[140:143], a[192:195], v[180:183]\n\t"
"v_mfma_f32_16x16x32_bf16 v[180:183], a[132:135], a[188:191], v[180:183]\n\t"
"v_mfma_f32_16x16x32_bf16 v[180:183], a[124:127], a[184:187], v[180:183]\n\t"
"buffer_store_dwordx4 v[196:199], v248, s[16:19], s29 offen offset:0 nt\n\t"
"v_mfma_f32_16x16x32_bf16 v[184:187], a[124:127], a[188:191], v[184:187]\n\t"
"v_mfma_f32_16x16x32_bf16 v[184:187], a[132:135], a[192:195], v[184:187]\n\t"
"v_mfma_f32_16x16x32_bf16 v[184:187], a[140:143], a[196:199], v[184:187]\n\t"
"v_mfma_f32_16x16x32_bf16 v[188:191], a[140:143], a[200:203], v[188:191]\n\t"
"ds_read_b128 a[176:179], v222 offset:2304\n\t"
"v_mfma_f32_16x16x32_bf16 v[188:191], a[132:135], a[196:199], v[188:191]\n\t"
"ds_read_b128 a[180:183], v223 offset:8448\n\t"
"v_mfma_f32_16x16x32_bf16 v[188:191], a[124:127], a[192:195], v[188:191]\n\t"
"ds_read_b128 a[184:187], v223 offset:14592\n\t"
"v_mfma_f32_16x16x32_bf16 v[192:195], a[124:127], a[196:199], v[192:195]\n\t"
"s_waitcnt vmcnt(52) lgkmcnt(6)\n\t"
"ds_read_b128 a[188:191], v222 offset:768\n\t"
"v_mfma_f32_16x16x32_bf16 v[192:195], a[132:135], a[200:203], v[192:195]\n\t"
"ds_read_b128 a[192:195], v223 offset:6912\n\t"
"v_mfma_f32_16x16x32_bf16 v[192:195], a[140:143], a[204:207], v[192:195]\n\t"
"ds_read_b128 a[196:199], v223 offset:13056\n\t"
"Lst34_%=:\n\t"
"v_mfma_f32_16x16x32_bf16 v[0:3], a[228:231], a[168:171], v[0:3]\n\t"
"s_waitcnt vmcnt(49) lgkmcnt(6)\n\t"
"v_mfma_f32_16x16x32_bf16 v[0:3], a[232:235], v[200:203], v[0:3]\n\t"
"v_mfma_f32_16x16x32_bf16 v[0:3], a[148:151], v[208:211], v[0:3]\n\t"
"v_mfma_f32_16x16x32_bf16 v[0:3], a[104:107], a[172:175], v[0:3]\n\t"
"s_waitcnt vmcnt(46)\n\t"
"v_mfma_f32_16x16x32_bf16 v[0:3], a[112:115], v[204:207], v[0:3]\n\t"
"v_mfma_f32_16x16x32_bf16 v[0:3], a[156:159], a[0:3], v[0:3]\n\t"
"v_mfma_f32_16x16x32_bf16 v[32:35], v[240:243], v[204:207], v[32:35]\n\t"
"s_waitcnt vmcnt(38)\n\t"
"v_mfma_f32_16x16x32_bf16 v[32:35], a[236:239], a[0:3], v[32:35]\n\t"
"v_mfma_f32_16x16x32_bf16 v[32:35], a[164:167], a[172:175], v[32:35]\n\t"
"buffer_store_dwordx4 v[216:219], v248, s[16:19], s29 offen offset:128 nt\n\t"
"v_mfma_f32_16x16x32_bf16 v[32:35], a[40:43], a[168:171], v[32:35]\n\t"
"s_waitcnt vmcnt(35)\n\t"
"v_cvt_pk_bf16_f32 v0, v0, v1\n\t"
"v_cvt_pk_bf16_f32 v1, v2, v3\n\t"
"v_mfma_f32_16x16x32_bf16 v[32:35], a[44:47], v[200:203], v[32:35]\n\t"
"v_mfma_f32_16x16x32_bf16 v[32:35], a[48:51], v[208:211], v[32:35]\n\t"
"v_mfma_f32_16x16x32_bf16 v[64:67], a[212:215], v[200:203], v[64:67]\n\t"
"s_waitcnt vmcnt(32)\n\t"
"ds_read_b128 a[200:203], v222 offset:22016\n\t"
"v_mfma_f32_16x16x32_bf16 v[64:67], a[240:243], v[208:211], v[64:67]\n\t"
"v_mfma_f32_16x16x32_bf16 v[64:67], a[52:55], a[168:171], v[64:67]\n\t"
"s_barrier\n\t"
"s_cmp_lt_u32 s69, 216\n\t"
"s_cbranch_scc0 Lna2_%=\n\t"
"s_add_u32 s71, s71, 1\n\t"
"v_mov_b32 v228, s71\n\t"
"s_mov_b64 exec, 1\n\t"
"buffer_store_dword v228, off, s[8:11], s70 sc1\n\t"
"s_mov_b64 exec, -1\n\t"
"Lna2_%=:\n\t"
"v_mfma_f32_16x16x32_bf16 v[64:67], a[244:247], a[172:175], v[64:67]\n\t"
"s_waitcnt vmcnt(25) lgkmcnt(4)\n\t"
"ds_read_b128 a[168:171], v222 offset:20480\n\t"
"v_mfma_f32_16x16x32_bf16 v[64:67], a[56:59], v[204:207], v[64:67]\n\t"
"v_cvt_pk_bf16_f32 v2, v32, v33\n\t"
"v_cvt_pk_bf16_f32 v3, v34, v35\n\t"
"v_mfma_f32_16x16x32_bf16 v[64:67], a[20:23], a[0:3], v[64:67]\n\t"
"ds_write_b128 v245, v[0:3] offset:0\n\t"
"v_mfma_f32_16x16x32_bf16 v[100:103], a[60:63], a[176:179], v[100:103]\n\t"
"s_waitcnt vmcnt(20) lgkmcnt(3)\n\t"
"v_mfma_f32_16x16x32_bf16 v[100:103], a[120:123], a[180:183], v[100:103]\n\t"
"v_mfma_f32_16x16x32_bf16 v[100:103], a[64:67], a[184:187], v[100:103]\n\t"
"v_mfma_f32_16x16x32_bf16 v[100:103], a[68:71], a[188:191], v[100:103]\n\t"
"s_waitcnt vmcnt(16)\n\t"
"v_cvt_pk_bf16_f32 v64, v64, v65\n\t"
"v_cvt_pk_bf16_f32 v65, v66, v67\n\t"
"v_mfma_f32_16x16x32_bf16 v[100:103], a[216:219], a[192:195], v[100:103]\n\t"
"v_mfma_f32_16x16x32_bf16 v[100:103], a[72:75], a[196:199], v[100:103]\n\t"
"buffer_store_dwordx4 a[4:7], v248, s[16:19], s29 offen offset:256 nt\n\t"
"ds_write_b64 v246, v[64:65] offset:0\n\t"
"v_mfma_f32_16x16x32_bf16 v[132:135], a[24:27], a[192:195], v[132:135]\n\t"
"s_waitcnt vmcnt(13)\n\t"
"v_mfma_f32_16x16x32_bf16 v[132:135], a[220:223], a[196:199], v[132:135]\n\t"
"v_mfma_f32_16x16x32_bf16 v[132:135], a[76:79], a[188:191], v[132:135]\n\t"
"v_mfma_f32_16x16x32_bf16 v[132:135], a[248:251], a[176:179], v[132:135]\n\t"
"s_waitcnt vmcnt(9)\n\t"
"v_cvt_pk_bf16_f32 v100, v100, v101\n\t"
"v_cvt_pk_bf16_f32 v101, v102, v103\n\t"
"v_mfma_f32_16x16x32_bf16 v[132:135], a[28:31], a[180:183], v[132:135]\n\t"
"v_mfma_f32_16x16x32_bf16 v[132:135], a[32:35], a[184:187], v[132:135]\n\t"
"v_mfma_f32_16x16x32_bf16 v[164:167], a[80:83], a[180:183], v[164:167]\n\t"
"s_waitcnt vmcnt(5)\n\t"
"ds_read_b128 a[172:175], v222 offset:22272\n\t"
"v_mfma_f32_16x16x32_bf16 v[164:167], a[84:87], a[184:187], v[164:167]\n\t"
"v_mfma_f32_16x16x32_bf16 v[164:167], a[36:39], a[176:179], v[164:167]\n\t"
"s_add_u32 s29, s22, 1376640\n\t"
"v_mfma_f32_16x16x32_bf16 v[164:167], a[88:91], a[188:191], v[164:167]\n\t"
"s_waitcnt lgkmcnt(4)\n\t"
"ds_read_b128 a[176:179], v222 offset:20736\n\t"
"v_cvt_pk_bf16_f32 v102, v132, v133\n\t"
"v_mfma_f32_16x16x32_bf16 v[164:167], a[92:95], a[192:195], v[164:167]\n\t"
"v_cvt_pk_bf16_f32 v103, v134, v135\n\t"
"v_mfma_f32_16x16x32_bf16 v[164:167], a[96:99], a[196:199], v[164:167]\n\t"
"ds_write_b128 v245, v[100:103] offset:9216\n\t"
"v_mfma_f32_16x16x32_bf16 v[4:7], a[228:231], v[200:203], v[4:7]\n\t"
"s_waitcnt lgkmcnt(5)\n\t"
"v_mfma_f32_16x16x32_bf16 v[4:7], a[232:235], v[208:211], v[4:7]\n\t"
"v_mfma_f32_16x16x32_bf16 v[4:7], a[148:151], a[200:203], v[4:7]\n\t"
"buffer_store_dwordx4 a[8:11], v248, s[16:19], s29 offen offset:0 nt\n\t"
"v_mfma_f32_16x16x32_bf16 v[4:7], a[104:107], v[204:207], v[4:7]\n\t"
"v_cvt_pk_bf16_f32 v164, v164, v165\n\t"
"v_cvt_pk_bf16_f32 v165, v166, v167\n\t"
"v_mfma_f32_16x16x32_bf16 v[4:7], a[112:115], a[0:3], v[4:7]\n\t"
"v_mfma_f32_16x16x32_bf16 v[4:7], a[156:159], a[168:171], v[4:7]\n\t"
"ds_write_b64 v246, v[164:165] offset:9216\n\t"
"v_mfma_f32_16x16x32_bf16 v[36:39], v[240:243], a[0:3], v[36:39]\n\t"
"v_mfma_f32_16x16x32_bf16 v[36:39], a[236:239], a[168:171], v[36:39]\n\t"
"v_mfma_f32_16x16x32_bf16 v[36:39], a[164:167], v[204:207], v[36:39]\n\t"
"v_mfma_f32_16x16x32_bf16 v[36:39], a[40:43], v[200:203], v[36:39]\n\t"
"v_cvt_pk_bf16_f32 v4, v4, v5\n\t"
"v_cvt_pk_bf16_f32 v5, v6, v7\n\t"
"v_mfma_f32_16x16x32_bf16 v[36:39], a[44:47], v[208:211], v[36:39]\n\t"
"v_mfma_f32_16x16x32_bf16 v[36:39], a[48:51], a[200:203], v[36:39]\n\t"
"v_mfma_f32_16x16x32_bf16 v[68:71], a[212:215], v[208:211], v[68:71]\n\t"
"ds_read_b128 a[188:191], v223 offset:28160\n\t"
"v_mfma_f32_16x16x32_bf16 v[68:71], a[240:243], a[200:203], v[68:71]\n\t"
"v_mfma_f32_16x16x32_bf16 v[68:71], a[52:55], v[200:203], v[68:71]\n\t"
"s_waitcnt lgkmcnt(1)\n\t"
"s_barrier\n\t"
"v_mfma_f32_16x16x32_bf16 v[68:71], a[244:247], v[204:207], v[68:71]\n\t"
"ds_read_b128 a[204:207], v223 offset:26624\n\t"
"v_mfma_f32_16x16x32_bf16 v[68:71], a[56:59], a[0:3], v[68:71]\n\t"
"v_cvt_pk_bf16_f32 v6, v36, v37\n\t"
"v_cvt_pk_bf16_f32 v7, v38, v39\n\t"
"v_mfma_f32_16x16x32_bf16 v[68:71], a[20:23], a[168:171], v[68:71]\n\t"
"ds_write_b128 v245, v[4:7] offset:18432\n\t"
"v_mfma_f32_16x16x32_bf16 v[104:107], a[60:63], a[180:183], v[104:107]\n\t"
"v_mfma_f32_16x16x32_bf16 v[104:107], a[120:123], a[184:187], v[104:107]\n\t"
"ds_read_b128 v[196:199], v255 offset:0\n\t"
"v_mfma_f32_16x16x32_bf16 v[104:107], a[64:67], a[172:175], v[104:107]\n\t"
"buffer_store_dwordx4 a[12:15], v248, s[16:19], s29 offen offset:128 nt\n\t"
"v_mfma_f32_16x16x32_bf16 v[104:107], a[68:71], a[192:195], v[104:107]\n\t"
"v_cvt_pk_bf16_f32 v68, v68, v69\n\t"
"v_cvt_pk_bf16_f32 v69, v70, v71\n\t"
"v_mfma_f32_16x16x32_bf16 v[104:107], a[216:219], a[196:199], v[104:107]\n\t"
"ds_read_b128 v[200:203], v255 offset:1536\n\t"
"v_mfma_f32_16x16x32_bf16 v[104:107], a[72:75], a[176:179], v[104:107]\n\t"
"ds_write_b64 v246, v[68:69] offset:18432\n\t"
"v_mfma_f32_16x16x32_bf16 v[136:139], a[24:27], a[196:199], v[136:139]\n\t"
"v_mfma_f32_16x16x32_bf16 v[136:139], a[220:223], a[176:179], v[136:139]\n\t"
"ds_read_b128 v[204:207], v255 offset:3072\n\t"
"v_mfma_f32_16x16x32_bf16 v[136:139], a[76:79], a[192:195], v[136:139]\n\t"
"v_mfma_f32_16x16x32_bf16 v[136:139], a[248:251], a[180:183], v[136:139]\n\t"
"v_cvt_pk_bf16_f32 v104, v104, v105\n\t"
"v_cvt_pk_bf16_f32 v105, v106, v107\n\t"
"v_mfma_f32_16x16x32_bf16 v[136:139], a[28:31], a[184:187], v[136:139]\n\t"
"v_mfma_f32_16x16x32_bf16 v[136:139], a[32:35], a[172:175], v[136:139]\n\t"
"v_mfma_f32_16x16x32_bf16 v[168:171], a[80:83], a[184:187], v[168:171]\n\t"
"ds_read_b128 v[212:215], v223 offset:28416\n\t"
"v_mfma_f32_16x16x32_bf16 v[168:171], a[84:87], a[172:175], v[168:171]\n\t"
"v_mfma_f32_16x16x32_bf16 v[168:171], a[36:39], a[180:183], v[168:171]\n\t"
"v_mfma_f32_16x16x32_bf16 v[168:171], a[88:91], a[192:195], v[168:171]\n\t"
"s_waitcnt lgkmcnt(7)\n\t"
"ds_read_b128 a[180:183], v223 offset:26880\n\t"
"v_cvt_pk_bf16_f32 v106, v136, v137\n\t"
"v_mfma_f32_16x16x32_bf16 v[168:171], a[92:95], a[196:199], v[168:171]\n\t"
"s_waitcnt lgkmcnt(5)\n\t"
"v_cvt_pk_bf16_f32 v107, v138, v139\n\t"
"v_mfma_f32_16x16x32_bf16 v[168:171], a[96:99], a[176:179], v[168:171]\n\t"
"buffer_store_dwordx4 a[16:19], v248, s[16:19], s29 offen offset:256 nt\n\t"
"s_add_u32 s29, s23, 0\n\t"
"v_mfma_f32_16x16x32_bf16 v[8:11], a[228:231], v[208:211], v[8:11]\n\t"
"v_mfma_f32_16x16x32_bf16 v[8:11], a[232:235], a[200:203], v[8:11]\n\t"
"s_waitcnt lgkmcnt(4)\n\t"
"v_mfma_f32_16x16x32_bf16 v[8:11], a[148:151], a[188:191], v[8:11]\n\t"
"buffer_store_dwordx4 v[196:199], v248, s[16:19], s29 offen offset:0 sc1\n\t"
"v_mfma_f32_16x16x32_bf16 v[8:11], a[104:107], a[0:3], v[8:11]\n\t"
"v_cvt_pk_bf16_f32 v168, v168, v169\n\t"
"v_cvt_pk_bf16_f32 v169, v170, v171\n\t"
"v_mfma_f32_16x16x32_bf16 v[8:11], a[112:115], a[168:171], v[8:11]\n\t"
"s_waitcnt lgkmcnt(2)\n\t"
"v_mfma_f32_16x16x32_bf16 v[8:11], a[156:159], a[204:207], v[8:11]\n\t"
"ds_write_b128 v245, v[104:107] offset:27648\n\t"
"v_mfma_f32_16x16x32_bf16 v[40:43], v[240:243], a[168:171], v[40:43]\n\t"
"v_mfma_f32_16x16x32_bf16 v[40:43], a[236:239], a[204:207], v[40:43]\n\t"
"v_mfma_f32_16x16x32_bf16 v[40:43], a[164:167], a[0:3], v[40:43]\n\t"
"ds_write_b64 v246, v[168:169] offset:27648\n\t"
"v_mfma_f32_16x16x32_bf16 v[40:43], a[40:43], v[208:211], v[40:43]\n\t"
"v_cvt_pk_bf16_f32 v8, v8, v9\n\t"
"v_cvt_pk_bf16_f32 v9, v10, v11\n\t"
"v_mfma_f32_16x16x32_bf16 v[40:43], a[44:47], a[200:203], v[40:43]\n\t"
"v_mfma_f32_16x16x32_bf16 v[40:43], a[48:51], a[188:191], v[40:43]\n\t"
"buffer_store_dwordx4 v[200:203], v248, s[16:19], s29 offen offset:128 sc1\n\t"
"v_mfma_f32_16x16x32_bf16 v[72:75], a[212:215], a[200:203], v[72:75]\n\t"
"ds_read_b128 a[192:195], v223 offset:34304\n\t"
"v_mfma_f32_16x16x32_bf16 v[72:75], a[240:243], a[188:191], v[72:75]\n\t"
"v_mfma_f32_16x16x32_bf16 v[72:75], a[52:55], v[208:211], v[72:75]\n\t"
"s_waitcnt lgkmcnt(1)\n\t"
"s_barrier\n\t"
"v_mfma_f32_16x16x32_bf16 v[72:75], a[244:247], a[0:3], v[72:75]\n\t"
"ds_read_b128 v[196:199], v255 offset:18432\n\t"
"v_mfma_f32_16x16x32_bf16 v[72:75], a[56:59], a[168:171], v[72:75]\n\t"
"ds_read_b128 v[200:203], v255 offset:19968\n\t"
"v_cvt_pk_bf16_f32 v10, v40, v41\n\t"
"v_mfma_f32_16x16x32_bf16 v[72:75], a[20:23], a[204:207], v[72:75]\n\t"
"buffer_store_dwordx4 v[204:207], v248, s[16:19], s29 offen offset:256 sc1\n\t"
"ds_read_b128 v[208:211], v255 offset:21504\n\t"
"ds_read_b128 v[216:219], v223 offset:32768\n\t"
"v_cvt_pk_bf16_f32 v11, v42, v43\n\t"
"v_mfma_f32_16x16x32_bf16 v[108:111], a[60:63], a[184:187], v[108:111]\n\t"
"s_add_u32 s29, s23, 196608\n\t"
"v_mfma_f32_16x16x32_bf16 v[108:111], a[120:123], a[172:175], v[108:111]\n\t"
"v_mfma_f32_16x16x32_bf16 v[108:111], a[64:67], v[212:215], v[108:111]\n\t"
"ds_write_b128 v245, v[8:11] offset:0\n\t"
"v_mfma_f32_16x16x32_bf16 v[108:111], a[68:71], a[196:199], v[108:111]\n\t"
"s_waitcnt lgkmcnt(4)\n\t"
"v_cvt_pk_bf16_f32 v72, v72, v73\n\t"
"v_cvt_pk_bf16_f32 v73, v74, v75\n\t"
"v_mfma_f32_16x16x32_bf16 v[108:111], a[216:219], a[176:179], v[108:111]\n\t"
"v_mfma_f32_16x16x32_bf16 v[108:111], a[72:75], a[180:183], v[108:111]\n\t"
"ds_write_b64 v246, v[72:73] offset:0\n\t"
"v_mfma_f32_16x16x32_bf16 v[140:143], a[24:27], a[176:179], v[140:143]\n\t"
"s_waitcnt lgkmcnt(4)\n\t"
"v_mfma_f32_16x16x32_bf16 v[140:143], a[220:223], a[180:183], v[140:143]\n\t"
"v_mfma_f32_16x16x32_bf16 v[140:143], a[76:79], a[196:199], v[140:143]\n\t"
"buffer_store_dwordx4 v[196:199], v248, s[16:19], s29 offen offset:0 sc1\n\t"
"v_mfma_f32_16x16x32_bf16 v[140:143], a[248:251], a[184:187], v[140:143]\n\t"
"s_waitcnt lgkmcnt(3)\n\t"
"v_cvt_pk_bf16_f32 v108, v108, v109\n\t"
"v_cvt_pk_bf16_f32 v109, v110, v111\n\t"
"v_mfma_f32_16x16x32_bf16 v[140:143], a[28:31], a[172:175], v[140:143]\n\t"
"v_mfma_f32_16x16x32_bf16 v[140:143], a[32:35], v[212:215], v[140:143]\n\t"
"buffer_store_dwordx4 v[200:203], v248, s[16:19], s29 offen offset:128 sc1\n\t"
"v_mfma_f32_16x16x32_bf16 v[172:175], a[80:83], a[172:175], v[172:175]\n\t"
"ds_read_b128 a[0:3], v223 offset:34560\n\t"
"v_mfma_f32_16x16x32_bf16 v[172:175], a[84:87], v[212:215], v[172:175]\n\t"
"v_mfma_f32_16x16x32_bf16 v[172:175], a[36:39], a[184:187], v[172:175]\n\t"
"buffer_store_dwordx4 v[208:211], v248, s[16:19], s29 offen offset:256 sc1\n\t"
"v_mfma_f32_16x16x32_bf16 v[172:175], a[88:91], a[196:199], v[172:175]\n\t"
"ds_read_b128 a[184:187], v223 offset:33024\n\t"
"v_cvt_pk_bf16_f32 v110, v140, v141\n\t"
"v_mfma_f32_16x16x32_bf16 v[172:175], a[92:95], a[176:179], v[172:175]\n\t"
"v_cvt_pk_bf16_f32 v111, v142, v143\n\t"
"v_mfma_f32_16x16x32_bf16 v[172:175], a[96:99], a[180:183], v[172:175]\n\t"
"ds_write_b128 v245, v[108:111] offset:9216\n\t"
"v_mfma_f32_16x16x32_bf16 v[12:15], a[228:231], a[200:203], v[12:15]\n\t"
"s_waitcnt lgkmcnt(5)\n\t"
"v_mfma_f32_16x16x32_bf16 v[12:15], a[232:235], a[188:191], v[12:15]\n\t"
"v_mfma_f32_16x16x32_bf16 v[12:15], a[148:151], a[192:195], v[12:15]\n\t"
"v_mfma_f32_16x16x32_bf16 v[12:15], a[104:107], a[168:171], v[12:15]\n\t"
"v_cvt_pk_bf16_f32 v172, v172, v173\n\t"
"v_cvt_pk_bf16_f32 v173, v174, v175\n\t"
"v_mfma_f32_16x16x32_bf16 v[12:15], a[112:115], a[204:207], v[12:15]\n\t"
"v_mfma_f32_16x16x32_bf16 v[12:15], a[156:159], v[216:219], v[12:15]\n\t"
"ds_write_b64 v246, v[172:173] offset:9216\n\t"
"v_mfma_f32_16x16x32_bf16 v[44:47], v[240:243], a[204:207], v[44:47]\n\t"
"v_mfma_f32_16x16x32_bf16 v[44:47], a[236:239], v[216:219], v[44:47]\n\t"
"v_mfma_f32_16x16x32_bf16 v[44:47], a[164:167], a[168:171], v[44:47]\n\t"
"v_mfma_f32_16x16x32_bf16 v[44:47], a[40:43], a[200:203], v[44:47]\n\t"
"v_cvt_pk_bf16_f32 v12, v12, v13\n\t"
"v_cvt_pk_bf16_f32 v13, v14, v15\n\t"
"v_mfma_f32_16x16x32_bf16 v[44:47], a[44:47], a[188:191], v[44:47]\n\t"
"v_mfma_f32_16x16x32_bf16 v[44:47], a[48:51], a[192:195], v[44:47]\n\t"
"v_mfma_f32_16x16x32_bf16 v[76:79], a[212:215], a[188:191], v[76:79]\n\t"
"ds_read_b128 a[196:199], v222 offset:41984\n\t"
"v_mfma_f32_16x16x32_bf16 v[76:79], a[240:243], a[192:195], v[76:79]\n\t"
"v_mfma_f32_16x16x32_bf16 v[76:79], a[52:55], a[200:203], v[76:79]\n\t"
"s_waitcnt lgkmcnt(1)\n\t"
"s_barrier\n\t"
"v_mfma_f32_16x16x32_bf16 v[76:79], a[244:247], a[168:171], v[76:79]\n\t"
"ds_read_b128 v[196:199], v255 offset:0\n\t"
"v_mfma_f32_16x16x32_bf16 v[76:79], a[56:59], a[204:207], v[76:79]\n\t"
"ds_read_b128 v[200:203], v255 offset:1536\n\t"
"v_cvt_pk_bf16_f32 v14, v44, v45\n\t"
"v_mfma_f32_16x16x32_bf16 v[76:79], a[20:23], v[216:219], v[76:79]\n\t"
"ds_read_b128 v[204:207], v255 offset:3072\n\t"
"v_cvt_pk_bf16_f32 v15, v46, v47\n\t"
"v_mfma_f32_16x16x32_bf16 v[112:115], a[60:63], a[172:175], v[112:115]\n\t"
"s_add_u32 s29, s23, 393216\n\t"
"ds_read_b128 a[168:171], v222 offset:40448\n\t"
"v_mfma_f32_16x16x32_bf16 v[112:115], a[120:123], v[212:215], v[112:115]\n\t"
"v_mfma_f32_16x16x32_bf16 v[112:115], a[64:67], a[0:3], v[112:115]\n\t"
"ds_write_b128 v245, v[12:15] offset:18432\n\t"
"v_mfma_f32_16x16x32_bf16 v[112:115], a[68:71], a[176:179], v[112:115]\n\t"
"s_waitcnt lgkmcnt(4)\n\t"
"v_cvt_pk_bf16_f32 v76, v76, v77\n\t"
"v_cvt_pk_bf16_f32 v77, v78, v79\n\t"
"v_mfma_f32_16x16x32_bf16 v[112:115], a[216:219], a[180:183], v[112:115]\n\t"
"Lpg25_%=:\n\t"
"s_cbranch_vccnz Lvr4_%=\n\t"
"v_mfma_f32_16x16x32_bf16 v[112:115], a[72:75], a[184:187], v[112:115]\n\t"
"ds_write_b64 v246, v[76:77] offset:18432\n\t"
"v_mfma_f32_16x16x32_bf16 v[144:147], a[24:27], a[180:183], v[144:147]\n\t"
"s_waitcnt lgkmcnt(4)\n\t"
"v_mfma_f32_16x16x32_bf16 v[144:147], a[220:223], a[184:187], v[144:147]\n\t"
"v_mfma_f32_16x16x32_bf16 v[144:147], a[76:79], a[176:179], v[144:147]\n\t"
"buffer_store_dwordx4 v[196:199], v248, s[16:19], s29 offen offset:0 sc1\n\t"
"v_mfma_f32_16x16x32_bf16 v[144:147], a[248:251], a[172:175], v[144:147]\n\t"
"s_waitcnt lgkmcnt(3)\n\t"
"v_cvt_pk_bf16_f32 v112, v112, v113\n\t"
"v_cvt_pk_bf16_f32 v113, v114, v115\n\t"
"v_mfma_f32_16x16x32_bf16 v[144:147], a[28:31], v[212:215], v[144:147]\n\t"
"v_mfma_f32_16x16x32_bf16 v[144:147], a[32:35], a[0:3], v[144:147]\n\t"
"buffer_store_dwordx4 v[200:203], v248, s[16:19], s29 offen offset:128 sc1\n\t"
"v_mfma_f32_16x16x32_bf16 v[176:179], a[80:83], v[212:215], v[176:179]\n\t"
"ds_read_b128 a[200:203], v222 offset:42240\n\t"
"v_mfma_f32_16x16x32_bf16 v[176:179], a[84:87], a[0:3], v[176:179]\n\t"
"v_mfma_f32_16x16x32_bf16 v[176:179], a[36:39], a[172:175], v[176:179]\n\t"
"buffer_store_dwordx4 v[204:207], v248, s[16:19], s29 offen offset:256 sc1\n\t"
"v_mfma_f32_16x16x32_bf16 v[176:179], a[88:91], a[176:179], v[176:179]\n\t"
"ds_read_b128 a[172:175], v222 offset:40704\n\t"
"v_cvt_pk_bf16_f32 v114, v144, v145\n\t"
"v_mfma_f32_16x16x32_bf16 v[176:179], a[92:95], a[180:183], v[176:179]\n\t"
"v_cvt_pk_bf16_f32 v115, v146, v147\n\t"
"v_mfma_f32_16x16x32_bf16 v[176:179], a[96:99], a[184:187], v[176:179]\n\t"
"ds_write_b128 v245, v[112:115] offset:27648\n\t"
"Lst35_%=:\n\t"
"v_mfma_f32_16x16x32_bf16 v[16:19], a[228:231], a[188:191], v[16:19]\n\t"
"s_waitcnt lgkmcnt(5)\n\t"
"v_mfma_f32_16x16x32_bf16 v[16:19], a[232:235], a[192:195], v[16:19]\n\t"
"v_mfma_f32_16x16x32_bf16 v[16:19], a[148:151], a[196:199], v[16:19]\n\t"
"v_mfma_f32_16x16x32_bf16 v[16:19], a[104:107], a[204:207], v[16:19]\n\t"
"v_cvt_pk_bf16_f32 v176, v176, v177\n\t"
"v_cvt_pk_bf16_f32 v177, v178, v179\n\t"
"v_mfma_f32_16x16x32_bf16 v[16:19], a[112:115], v[216:219], v[16:19]\n\t"
"v_mfma_f32_16x16x32_bf16 v[16:19], a[156:159], a[168:171], v[16:19]\n\t"
"ds_write_b64 v246, v[176:177] offset:27648\n\t"
"v_mfma_f32_16x16x32_bf16 v[48:51], v[240:243], v[216:219], v[48:51]\n\t"
"v_mfma_f32_16x16x32_bf16 v[48:51], a[236:239], a[168:171], v[48:51]\n\t"
"v_mfma_f32_16x16x32_bf16 v[48:51], a[164:167], a[204:207], v[48:51]\n\t"
"v_mfma_f32_16x16x32_bf16 v[48:51], a[40:43], a[188:191], v[48:51]\n\t"
"v_cvt_pk_bf16_f32 v16, v16, v17\n\t"
"v_cvt_pk_bf16_f32 v17, v18, v19\n\t"
"v_mfma_f32_16x16x32_bf16 v[48:51], a[44:47], a[192:195], v[48:51]\n\t"
"v_mfma_f32_16x16x32_bf16 v[48:51], a[48:51], a[196:199], v[48:51]\n\t"
"v_mfma_f32_16x16x32_bf16 v[80:83], a[212:215], a[192:195], v[80:83]\n\t"
"ds_read_b128 a[176:179], v223 offset:48128\n\t"
"v_mfma_f32_16x16x32_bf16 v[80:83], a[240:243], a[196:199], v[80:83]\n\t"
"v_mfma_f32_16x16x32_bf16 v[80:83], a[52:55], a[188:191], v[80:83]\n\t"
"s_waitcnt lgkmcnt(1)\n\t"
"s_barrier\n\t"
"v_mfma_f32_16x16x32_bf16 v[80:83], a[244:247], a[204:207], v[80:83]\n\t"
"ds_read_b128 v[196:199], v255 offset:18432\n\t"
"v_mfma_f32_16x16x32_bf16 v[80:83], a[56:59], v[216:219], v[80:83]\n\t"
"ds_read_b128 v[200:203], v255 offset:19968\n\t"
"v_cvt_pk_bf16_f32 v18, v48, v49\n\t"
"v_mfma_f32_16x16x32_bf16 v[80:83], a[20:23], a[168:171], v[80:83]\n\t"
"ds_read_b128 v[204:207], v255 offset:21504\n\t"
"v_cvt_pk_bf16_f32 v19, v50, v51\n\t"
"v_mfma_f32_16x16x32_bf16 v[116:119], a[60:63], v[212:215], v[116:119]\n\t"
"s_add_u32 s29, s23, 589824\n\t"
"ds_read_b128 a[188:191], v223 offset:46592\n\t"
"v_mfma_f32_16x16x32_bf16 v[116:119], a[120:123], a[0:3], v[116:119]\n\t"
"v_mfma_f32_16x16x32_bf16 v[116:119], a[64:67], a[200:203], v[116:119]\n\t"
"ds_write_b128 v245, v[16:19] offset:0\n\t"
"v_mfma_f32_16x16x32_bf16 v[116:119], a[68:71], a[180:183], v[116:119]\n\t"
"s_waitcnt lgkmcnt(4)\n\t"
"v_cvt_pk_bf16_f32 v80, v80, v81\n\t"
"v_cvt_pk_bf16_f32 v81, v82, v83\n\t"
"v_mfma_f32_16x16x32_bf16 v[116:119], a[216:219], a[184:187], v[116:119]\n\t"
"v_mfma_f32_16x16x32_bf16 v[116:119], a[72:75], a[172:175], v[116:119]\n\t"
"ds_write_b64 v246, v[80:81] offset:0\n\t"
"v_mfma_f32_16x16x32_bf16 v[148:151], a[24:27], a[184:187], v[148:151]\n\t"
"s_waitcnt lgkmcnt(4)\n\t"
"v_mfma_f32_16x16x32_bf16 v[148:151], a[220:223], a[172:175], v[148:151]\n\t"
"v_mfma_f32_16x16x32_bf16 v[148:151], a[76:79], a[180:183], v[148:151]\n\t"
"buffer_store_dwordx4 v[196:199], v248, s[16:19], s29 offen offset:0 sc1\n\t"
"v_mfma_f32_16x16x32_bf16 v[148:151], a[248:251], v[212:215], v[148:151]\n\t"
"s_waitcnt lgkmcnt(3)\n\t"
"v_cvt_pk_bf16_f32 v116, v116, v117\n\t"
"v_cvt_pk_bf16_f32 v117, v118, v119\n\t"
"v_mfma_f32_16x16x32_bf16 v[148:151], a[28:31], a[0:3], v[148:151]\n\t"
"v_mfma_f32_16x16x32_bf16 v[148:151], a[32:35], a[200:203], v[148:151]\n\t"
"buffer_store_dwordx4 v[200:203], v248, s[16:19], s29 offen offset:128 sc1\n\t"
"v_mfma_f32_16x16x32_bf16 v[180:183], a[80:83], a[0:3], v[180:183]\n\t"
"ds_read_b128 a[204:207], v223 offset:48384\n\t"
"v_mfma_f32_16x16x32_bf16 v[180:183], a[84:87], a[200:203], v[180:183]\n\t"
"v_mfma_f32_16x16x32_bf16 v[180:183], a[36:39], v[212:215], v[180:183]\n\t"
"buffer_store_dwordx4 v[204:207], v248, s[16:19], s29 offen offset:256 sc1\n\t"
"v_mfma_f32_16x16x32_bf16 v[180:183], a[88:91], a[180:183], v[180:183]\n\t"
"ds_read_b128 a[180:183], v223 offset:46848\n\t"
"v_cvt_pk_bf16_f32 v118, v148, v149\n\t"
"v_mfma_f32_16x16x32_bf16 v[180:183], a[92:95], a[184:187], v[180:183]\n\t"
"v_cvt_pk_bf16_f32 v119, v150, v151\n\t"
"v_mfma_f32_16x16x32_bf16 v[180:183], a[96:99], a[172:175], v[180:183]\n\t"
"ds_write_b128 v245, v[116:119] offset:9216\n\t"
"v_mfma_f32_16x16x32_bf16 v[20:23], a[228:231], a[192:195], v[20:23]\n\t"
"s_waitcnt lgkmcnt(5)\n\t"
"v_mfma_f32_16x16x32_bf16 v[20:23], a[232:235], a[196:199], v[20:23]\n\t"
"v_mfma_f32_16x16x32_bf16 v[20:23], a[148:151], a[176:179], v[20:23]\n\t"
"v_mfma_f32_16x16x32_bf16 v[20:23], a[104:107], v[216:219], v[20:23]\n\t"
"v_cvt_pk_bf16_f32 v180, v180, v181\n\t"
"v_cvt_pk_bf16_f32 v181, v182, v183\n\t"
"v_mfma_f32_16x16x32_bf16 v[20:23], a[112:115], a[168:171], v[20:23]\n\t"
"v_mfma_f32_16x16x32_bf16 v[20:23], a[156:159], a[188:191], v[20:23]\n\t"
"ds_write_b64 v246, v[180:181] offset:9216\n\t"
"v_mfma_f32_16x16x32_bf16 v[52:55], v[240:243], a[168:171], v[52:55]\n\t"
"v_mfma_f32_16x16x32_bf16 v[52:55], a[236:239], a[188:191], v[52:55]\n\t"
"v_mfma_f32_16x16x32_bf16 v[52:55], a[164:167], v[216:219], v[52:55]\n\t"
"v_mfma_f32_16x16x32_bf16 v[52:55], a[40:43], a[192:195], v[52:55]\n\t"
"v_cvt_pk_bf16_f32 v20, v20, v21\n\t"
"v_cvt_pk_bf16_f32 v21, v22, v23\n\t"
"v_mfma_f32_16x16x32_bf16 v[52:55], a[44:47], a[196:199], v[52:55]\n\t"
"v_mfma_f32_16x16x32_bf16 v[52:55], a[48:51], a[176:179], v[52:55]\n\t"
"v_mfma_f32_16x16x32_bf16 v[84:87], a[212:215], a[196:199], v[84:87]\n\t"
"ds_read_b128 v[208:211], v223 offset:54272\n\t"
"v_mfma_f32_16x16x32_bf16 v[84:87], a[240:243], a[176:179], v[84:87]\n\t"
"v_mfma_f32_16x16x32_bf16 v[84:87], a[52:55], a[192:195], v[84:87]\n\t"
"s_waitcnt lgkmcnt(1)\n\t"
"s_barrier\n\t"
"v_mfma_f32_16x16x32_bf16 v[84:87], a[244:247], v[216:219], v[84:87]\n\t"
"ds_read_b128 v[196:199], v255 offset:0\n\t"
"v_mfma_f32_16x16x32_bf16 v[84:87], a[56:59], a[168:171], v[84:87]\n\t"
"ds_read_b128 v[200:203], v255 offset:1536\n\t"
"v_cvt_pk_bf16_f32 v22, v52, v53\n\t"
"v_mfma_f32_16x16x32_bf16 v[84:87], a[20:23], a[188:191], v[84:87]\n\t"
"ds_read_b128 v[204:207], v255 offset:3072\n\t"
"v_cvt_pk_bf16_f32 v23, v54, v55\n\t"
"v_mfma_f32_16x16x32_bf16 v[120:123], a[60:63], a[0:3], v[120:123]\n\t"
"s_add_u32 s29, s23, 786432\n\t"
"ds_read_b128 a[192:195], v223 offset:52736\n\t"
"v_mfma_f32_16x16x32_bf16 v[120:123], a[120:123], a[200:203], v[120:123]\n\t"
"v_mfma_f32_16x16x32_bf16 v[120:123], a[64:67], a[204:207], v[120:123]\n\t"
"ds_write_b128 v245, v[20:23] offset:18432\n\t"
"v_mfma_f32_16x16x32_bf16 v[120:123], a[68:71], a[184:187], v[120:123]\n\t"
"s_waitcnt lgkmcnt(4)\n\t"
"v_cvt_pk_bf16_f32 v84, v84, v85\n\t"
"v_cvt_pk_bf16_f32 v85, v86, v87\n\t"
"v_mfma_f32_16x16x32_bf16 v[120:123], a[216:219], a[172:175], v[120:123]\n\t"
"v_mfma_f32_16x16x32_bf16 v[120:123], a[72:75], a[180:183], v[120:123]\n\t"
"ds_write_b64 v246, v[84:85] offset:18432\n\t"
"v_mfma_f32_16x16x32_bf16 v[152:155], a[24:27], a[172:175], v[152:155]\n\t"
"s_waitcnt lgkmcnt(4)\n\t"
"v_mfma_f32_16x16x32_bf16 v[152:155], a[220:223], a[180:183], v[152:155]\n\t"
"v_mfma_f32_16x16x32_bf16 v[152:155], a[76:79], a[184:187], v[152:155]\n\t"
"buffer_store_dwordx4 v[196:199], v248, s[16:19], s29 offen offset:0 sc1\n\t"
"v_mfma_f32_16x16x32_bf16 v[152:155], a[248:251], a[0:3], v[152:155]\n\t"
"s_waitcnt lgkmcnt(3)\n\t"
"v_cvt_pk_bf16_f32 v120, v120, v121\n\t"
"v_cvt_pk_bf16_f32 v121, v122, v123\n\t"
"v_mfma_f32_16x16x32_bf16 v[152:155], a[28:31], a[200:203], v[152:155]\n\t"
"v_mfma_f32_16x16x32_bf16 v[152:155], a[32:35], a[204:207], v[152:155]\n\t"
"buffer_store_dwordx4 v[200:203], v248, s[16:19], s29 offen offset:128 sc1\n\t"
"v_mfma_f32_16x16x32_bf16 v[184:187], a[80:83], a[200:203], v[184:187]\n\t"
"ds_read_b128 v[212:215], v223 offset:54528\n\t"
"v_mfma_f32_16x16x32_bf16 v[184:187], a[84:87], a[204:207], v[184:187]\n\t"
"v_mfma_f32_16x16x32_bf16 v[184:187], a[36:39], a[0:3], v[184:187]\n\t"
"buffer_store_dwordx4 v[204:207], v248, s[16:19], s29 offen offset:256 sc1\n\t"
"v_mfma_f32_16x16x32_bf16 v[184:187], a[88:91], a[184:187], v[184:187]\n\t"
"ds_read_b128 a[184:187], v223 offset:52992\n\t"
"v_cvt_pk_bf16_f32 v122, v152, v153\n\t"
"v_mfma_f32_16x16x32_bf16 v[184:187], a[92:95], a[172:175], v[184:187]\n\t"
"v_cvt_pk_bf16_f32 v123, v154, v155\n\t"
"v_mfma_f32_16x16x32_bf16 v[184:187], a[96:99], a[180:183], v[184:187]\n\t"
"ds_write_b128 v245, v[120:123] offset:27648\n\t"
"v_mfma_f32_16x16x32_bf16 v[24:27], a[228:231], a[196:199], v[24:27]\n\t"
"s_waitcnt lgkmcnt(5)\n\t"
"v_mfma_f32_16x16x32_bf16 v[24:27], a[232:235], a[176:179], v[24:27]\n\t"
"v_mfma_f32_16x16x32_bf16 v[24:27], a[148:151], v[208:211], v[24:27]\n\t"
"v_mfma_f32_16x16x32_bf16 v[24:27], a[104:107], a[168:171], v[24:27]\n\t"
"v_cvt_pk_bf16_f32 v184, v184, v185\n\t"
"v_cvt_pk_bf16_f32 v185, v186, v187\n\t"
"v_mfma_f32_16x16x32_bf16 v[24:27], a[112:115], a[188:191], v[24:27]\n\t"
"v_mfma_f32_16x16x32_bf16 v[24:27], a[156:159], a[192:195], v[24:27]\n\t"
"ds_write_b64 v246, v[184:185] offset:27648\n\t"
"v_mfma_f32_16x16x32_bf16 v[56:59], v[240:243], a[188:191], v[56:59]\n\t"
"v_mfma_f32_16x16x32_bf16 v[56:59], a[236:239], a[192:195], v[56:59]\n\t"
"v_mfma_f32_16x16x32_bf16 v[56:59], a[164:167], a[168:171], v[56:59]\n\t"
"v_mfma_f32_16x16x32_bf16 v[56:59], a[40:43], a[196:199], v[56:59]\n\t"
"v_cvt_pk_bf16_f32 v24, v24, v25\n\t"
"v_cvt_pk_bf16_f32 v25, v26, v27\n\t"
"v_mfma_f32_16x16x32_bf16 v[56:59], a[44:47], a[176:179], v[56:59]\n\t"
"v_mfma_f32_16x16x32_bf16 v[56:59], a[48:51], v[208:211], v[56:59]\n\t"
"v_mfma_f32_16x16x32_bf16 v[88:91], a[212:215], a[176:179], v[88:91]\n\t"
"ds_read_b128 v[216:219], v222 offset:61952\n\t"
"v_mfma_f32_16x16x32_bf16 v[88:91], a[240:243], v[208:211], v[88:91]\n\t"
"v_mfma_f32_16x16x32_bf16 v[88:91], a[52:55], a[196:199], v[88:91]\n\t"
"s_waitcnt lgkmcnt(1)\n\t"
"s_barrier\n\t"
"v_mfma_f32_16x16x32_bf16 v[88:91], a[244:247], a[168:171], v[88:91]\n\t"
"ds_read_b128 v[196:199], v255 offset:18432\n\t"
"v_mfma_f32_16x16x32_bf16 v[88:91], a[56:59], a[188:191], v[88:91]\n\t"
"ds_read_b128 v[200:203], v255 offset:19968\n\t"
"v_cvt_pk_bf16_f32 v26, v56, v57\n\t"
"v_mfma_f32_16x16x32_bf16 v[88:91], a[20:23], a[192:195], v[88:91]\n\t"
"ds_read_b128 v[204:207], v255 offset:21504\n\t"
"v_cvt_pk_bf16_f32 v27, v58, v59\n\t"
"v_mfma_f32_16x16x32_bf16 v[124:127], a[60:63], a[200:203], v[124:127]\n\t"
"s_add_u32 s29, s23, 983040\n\t"
"ds_read_b128 a[168:171], v222 offset:60416\n\t"
"v_mfma_f32_16x16x32_bf16 v[124:127], a[120:123], a[204:207], v[124:127]\n\t"
"v_mfma_f32_16x16x32_bf16 v[124:127], a[64:67], v[212:215], v[124:127]\n\t"
"ds_write_b128 v245, v[24:27] offset:0\n\t"
"v_mfma_f32_16x16x32_bf16 v[124:127], a[68:71], a[172:175], v[124:127]\n\t"
"s_waitcnt lgkmcnt(4)\n\t"
"v_cvt_pk_bf16_f32 v88, v88, v89\n\t"
"v_cvt_pk_bf16_f32 v89, v90, v91\n\t"
"v_mfma_f32_16x16x32_bf16 v[124:127], a[216:219], a[180:183], v[124:127]\n\t"
"v_mfma_f32_16x16x32_bf16 v[124:127], a[72:75], a[184:187], v[124:127]\n\t"
"ds_write_b64 v246, v[88:89] offset:0\n\t"
"v_mfma_f32_16x16x32_bf16 v[156:159], a[24:27], a[180:183], v[156:159]\n\t"
"s_waitcnt lgkmcnt(4)\n\t"
"v_mfma_f32_16x16x32_bf16 v[156:159], a[220:223], a[184:187], v[156:159]\n\t"
"v_mfma_f32_16x16x32_bf16 v[156:159], a[76:79], a[172:175], v[156:159]\n\t"
"buffer_store_dwordx4 v[196:199], v248, s[16:19], s29 offen offset:0 sc1\n\t"
"v_mfma_f32_16x16x32_bf16 v[156:159], a[248:251], a[200:203], v[156:159]\n\t"
"s_waitcnt lgkmcnt(3)\n\t"
"v_cvt_pk_bf16_f32 v124, v124, v125\n\t"
"v_cvt_pk_bf16_f32 v125, v126, v127\n\t"
"v_mfma_f32_16x16x32_bf16 v[156:159], a[28:31], a[204:207], v[156:159]\n\t"
"v_mfma_f32_16x16x32_bf16 v[156:159], a[32:35], v[212:215], v[156:159]\n\t"
"buffer_store_dwordx4 v[200:203], v248, s[16:19], s29 offen offset:128 sc1\n\t"
"v_mfma_f32_16x16x32_bf16 v[188:191], a[80:83], a[204:207], v[188:191]\n\t"
"ds_read_b128 a[196:199], v222 offset:62208\n\t"
"v_mfma_f32_16x16x32_bf16 v[188:191], a[84:87], v[212:215], v[188:191]\n\t"
"v_mfma_f32_16x16x32_bf16 v[188:191], a[36:39], a[200:203], v[188:191]\n\t"
"buffer_store_dwordx4 v[204:207], v248, s[16:19], s29 offen offset:256 sc1\n\t"
"v_mfma_f32_16x16x32_bf16 v[188:191], a[88:91], a[172:175], v[188:191]\n\t"
"ds_read_b128 a[172:175], v222 offset:60672\n\t"
"v_cvt_pk_bf16_f32 v126, v156, v157\n\t"
"v_mfma_f32_16x16x32_bf16 v[188:191], a[92:95], a[180:183], v[188:191]\n\t"
"v_cvt_pk_bf16_f32 v127, v158, v159\n\t"
"v_mfma_f32_16x16x32_bf16 v[188:191], a[96:99], a[184:187], v[188:191]\n\t"
"ds_write_b128 v245, v[124:127] offset:9216\n\t"
"v_mfma_f32_16x16x32_bf16 v[28:31], a[228:231], a[176:179], v[28:31]\n\t"
"s_waitcnt lgkmcnt(5)\n\t"
"v_mfma_f32_16x16x32_bf16 v[28:31], a[232:235], v[208:211], v[28:31]\n\t"
"v_mfma_f32_16x16x32_bf16 v[28:31], a[148:151], v[216:219], v[28:31]\n\t"
"v_mfma_f32_16x16x32_bf16 v[28:31], a[104:107], a[188:191], v[28:31]\n\t"
"v_cvt_pk_bf16_f32 v188, v188, v189\n\t"
"v_cvt_pk_bf16_f32 v189, v190, v191\n\t"
"v_mfma_f32_16x16x32_bf16 v[28:31], a[112:115], a[192:195], v[28:31]\n\t"
"v_mfma_f32_16x16x32_bf16 v[28:31], a[156:159], a[168:171], v[28:31]\n\t"
"ds_write_b64 v246, v[188:189] offset:9216\n\t"
"v_mfma_f32_16x16x32_bf16 v[60:63], v[240:243], a[192:195], v[60:63]\n\t"
"v_mfma_f32_16x16x32_bf16 v[60:63], a[236:239], a[168:171], v[60:63]\n\t"
"v_mfma_f32_16x16x32_bf16 v[60:63], a[164:167], a[188:191], v[60:63]\n\t"
"v_mfma_f32_16x16x32_bf16 v[60:63], a[40:43], a[176:179], v[60:63]\n\t"
"v_cvt_pk_bf16_f32 v28, v28, v29\n\t"
"v_cvt_pk_bf16_f32 v29, v30, v31\n\t"
"v_mfma_f32_16x16x32_bf16 v[60:63], a[44:47], v[208:211], v[60:63]\n\t"
"v_mfma_f32_16x16x32_bf16 v[60:63], a[48:51], v[216:219], v[60:63]\n\t"
"v_mfma_f32_16x16x32_bf16 v[92:95], a[212:215], v[208:211], v[92:95]\n\t"
"v_mfma_f32_16x16x32_bf16 v[92:95], a[240:243], v[216:219], v[92:95]\n\t"
"v_mfma_f32_16x16x32_bf16 v[92:95], a[52:55], a[176:179], v[92:95]\n\t"
"s_waitcnt lgkmcnt(0)\n\t"
"s_barrier\n\t"
"v_mfma_f32_16x16x32_bf16 v[92:95], a[244:247], a[188:191], v[92:95]\n\t"
"ds_read_b128 v[196:199], v255 offset:0\n\t"
"v_mfma_f32_16x16x32_bf16 v[92:95], a[56:59], a[192:195], v[92:95]\n\t"
"ds_read_b128 v[200:203], v255 offset:1536\n\t"
"v_cvt_pk_bf16_f32 v30, v60, v61\n\t"
"v_mfma_f32_16x16x32_bf16 v[92:95], a[20:23], a[168:171], v[92:95]\n\t"
"ds_read_b128 v[204:207], v255 offset:3072\n\t"
"v_cvt_pk_bf16_f32 v31, v62, v63\n\t"
"v_mfma_f32_16x16x32_bf16 v[128:131], a[60:63], a[204:207], v[128:131]\n\t"
"s_add_u32 s29, s23, 1179648\n\t"
"v_mfma_f32_16x16x32_bf16 v[128:131], a[120:123], v[212:215], v[128:131]\n\t"
"v_mfma_f32_16x16x32_bf16 v[128:131], a[64:67], a[196:199], v[128:131]\n\t"
"ds_write_b128 v245, v[28:31] offset:18432\n\t"
"v_mfma_f32_16x16x32_bf16 v[128:131], a[68:71], a[180:183], v[128:131]\n\t"
"s_waitcnt lgkmcnt(3)\n\t"
"v_cvt_pk_bf16_f32 v92, v92, v93\n\t"
"v_cvt_pk_bf16_f32 v93, v94, v95\n\t"
"v_mfma_f32_16x16x32_bf16 v[128:131], a[216:219], a[184:187], v[128:131]\n\t"
"v_mfma_f32_16x16x32_bf16 v[128:131], a[72:75], a[172:175], v[128:131]\n\t"
"ds_write_b64 v246, v[92:93] offset:18432\n\t"
"v_mfma_f32_16x16x32_bf16 v[160:163], a[24:27], a[184:187], v[160:163]\n\t"
"s_waitcnt lgkmcnt(3)\n\t"
"v_mfma_f32_16x16x32_bf16 v[160:163], a[220:223], a[172:175], v[160:163]\n\t"
"v_mfma_f32_16x16x32_bf16 v[160:163], a[76:79], a[180:183], v[160:163]\n\t"
"buffer_store_dwordx4 v[196:199], v248, s[16:19], s29 offen offset:0 sc1\n\t"
"v_mfma_f32_16x16x32_bf16 v[160:163], a[248:251], a[204:207], v[160:163]\n\t"
"s_waitcnt lgkmcnt(2)\n\t"
"v_cvt_pk_bf16_f32 v128, v128, v129\n\t"
"v_cvt_pk_bf16_f32 v129, v130, v131\n\t"
"v_mfma_f32_16x16x32_bf16 v[160:163], a[28:31], v[212:215], v[160:163]\n\t"
"v_mfma_f32_16x16x32_bf16 v[160:163], a[32:35], a[196:199], v[160:163]\n\t"
"buffer_store_dwordx4 v[200:203], v248, s[16:19], s29 offen offset:128 sc1\n\t"
"v_mfma_f32_16x16x32_bf16 v[192:195], a[80:83], v[212:215], v[192:195]\n\t"
"v_mfma_f32_16x16x32_bf16 v[192:195], a[84:87], a[196:199], v[192:195]\n\t"
"v_mfma_f32_16x16x32_bf16 v[192:195], a[36:39], a[204:207], v[192:195]\n\t"
"buffer_store_dwordx4 v[204:207], v248, s[16:19], s29 offen offset:256 sc1\n\t"
"v_mfma_f32_16x16x32_bf16 v[192:195], a[88:91], a[180:183], v[192:195]\n\t"
"v_cvt_pk_bf16_f32 v130, v160, v161\n\t"
"v_cvt_pk_bf16_f32 v131, v162, v163\n\t"
"v_mfma_f32_16x16x32_bf16 v[192:195], a[92:95], a[184:187], v[192:195]\n\t"
"v_mfma_f32_16x16x32_bf16 v[192:195], a[96:99], a[172:175], v[192:195]\n\t"
"ds_write_b128 v245, v[128:131] offset:27648\n\t"
"s_nop 6\n\t"
"v_cvt_pk_bf16_f32 v192, v192, v193\n\t"
"v_cvt_pk_bf16_f32 v193, v194, v195\n\t"
"ds_write_b64 v246, v[192:193] offset:27648\n\t"
"s_waitcnt lgkmcnt(0)\n\t"
"s_barrier\n\t"
"ds_read_b128 v[196:199], v255 offset:18432\n\t"
"ds_read_b128 v[200:203], v255 offset:19968\n\t"
"ds_read_b128 v[208:211], v255 offset:21504\n\t"
"s_waitcnt lgkmcnt(2)\n\t"
"s_add_u32 s29, s23, 1376256\n\t"
"buffer_store_dwordx4 v[196:199], v248, s[16:19], s29 offen offset:0 sc1\n\t"
"s_waitcnt lgkmcnt(1)\n\t"
"buffer_store_dwordx4 v[200:203], v248, s[16:19], s29 offen offset:128 sc1\n\t"
"s_waitcnt lgkmcnt(0)\n\t"
"buffer_store_dwordx4 v[208:211], v248, s[16:19], s29 offen offset:256 sc1\n\t"
"s_waitcnt vmcnt(0)\n\t"
"Lwino_end:\n\t"
  : "+{v205}"(tid)
  : "s"((unsigned long long)__builtin_amdgcn_kernarg_segment_ptr()), "s"(blockIdx.x), "s"((unsigned long long)lane_tab), "s"((unsigned)(size_t)(__attribute__((address_space(3))) char*)smem), "s"((unsigned)(size_t)__builtin_amdgcn_dispatch_ptr())
  : "v0", "v1", "v2", "v3", "v4", "v5", "v6", "v7", "v8", "v9", "v10", "v11", "v12", "v13", "v14", "v15", "v16", "v17", "v18", "v19", "v20", "v21", "v22", "v23", "v24", "v25", "v26", "v27", "v28", "v29", "v30", "v31", "v32", "v33", "v34", "v35", "v36", "v37", "v38", "v39", "v40", "v41", "v42", "v43", "v44", "v45", "v46", "v47", "v48", "v49", "v50", "v51", "v52", "v53", "v54", "v55", "v56", "v57", "v58", "v59", "v60", "v61", "v62", "v63", "v64", "v65", "v66", "v67", "v68", "v69", "v70", "v71", "v72", "v73", "v74", "v75", "v76", "v77", "v78", "v79", "v80", "v81", "v82", "v83", "v84", "v85", "v86", "v87", "v88", "v89", "v90", "v91", "v92", "v93", "v94", "v95", "v96", "v97", "v98", "v99", "v100", "v101", "v102", "v103", "v104", "v105", "v106", "v107", "v108", "v109", "v110", "v111", "v112", "v113", "v114", "v115", "v116", "v117", "v118", "v119", "v120", "v121", "v122", "v123", "v124", "v125", "v126", "v127", "v128", "v129", "v130", "v131", "v132", "v133", "v134", "v135", "v136", "v137", "v138", "v139", "v140", "v141", "v142", "v143", "v144", "v145", "v146", "v147", "v148", "v149", "v150", "v151", "v152", "v153", "v154", "v155", "v156", "v157", "v158", "v159", "v160", "v161", "v162", "v163", "v164", "v165", "v166", "v167", "v168", "v169", "v170", "v171", "v172", "v173", "v174", "v175", "v176", "v177", "v178", "v179", "v180", "v181", "v182", "v183", "v184", "v185", "v186", "v187", "v188", "v189", "v190", "v191", "v192", "v193", "v194", "v195", "v196", "v197", "v198", "v199", "v200", "v201", "v202", "v203", "v204", "v206", "v207", "v208", "v209", "v210", "v211", "v212", "v213", "v214", "v215", "v216", "v217", "v218", "v219", "v220", "v221", "v222", "v223", "v224", "v225", "v226", "v227", "v228", "v229", "v230", "v231", "v232", "v233", "v234", "v235", "v236", "v237", "v238", "v239", "v240", "v241", "v242", "v243", "v244", "v245", "v246", "v247", "v248", "v249", "v250", "v251", "v252", "v253", "v254", "v255", "a0", "a1", "a2", "a3", "a4", "a5", "a6", "a7", "a8", "a9", "a10", "a11", "a12", "a13", "a14", "a15", "a16", "a17", "a18", "a19", "a20", "a21", "a22", "a23", "a24", "a25", "a26", "a27", "a28", "a29", "a30", "a31", "a32", "a33", "a34", "a35", "a36", "a37", "a38", "a39", "a40", "a41", "a42", "a43", "a44", "a45", "a46", "a47", "a48", "a49", "a50", "a51", "a52", "a53", "a54", "a55", "a56", "a57", "a58", "a59", "a60", "a61", "a62", "a63", "a64", "a65", "a66", "a67", "a68", "a69", "a70", "a71", "a72", "a73", "a74", "a75", "a76", "a77", "a78", "a79", "a80", "a81", "a82", "a83", "a84", "a85", "a86", "a87", "a88", "a89", "a90", "a91", "a92", "a93", "a94", "a95", "a96", "a97", "a98", "a99", "a100", "a101", "a102", "a103", "a104", "a105", "a106", "a107", "a108", "a109", "a110", "a111", "a112", "a113", "a114", "a115", "a116", "a117", "a118", "a119", "a120", "a121", "a122", "a123", "a124", "a125", "a126", "a127", "a128", "a129", "a130", "a131", "a132", "a133", "a134", "a135", "a136", "a137", "a138", "a139", "a140", "a141", "a142", "a143", "a144", "a145", "a146", "a147", "a148", "a149", "a150", "a151", "a152", "a153", "a154", "a155", "a156", "a157", "a158", "a159", "a160", "a161", "a162", "a163", "a164", "a165", "a166", "a167", "a168", "a169", "a170", "a171", "a172", "a173", "a174", "a175", "a176", "a177", "a178", "a179", "a180", "a181", "a182", "a183", "a184", "a185", "a186", "a187", "a188", "a189", "a190", "a191", "a192", "a193", "a194", "a195", "a196", "a197", "a198", "a199", "a200", "a201", "a202", "a203", "a204", "a205", "a206", "a207", "a208", "a209", "a210", "a211", "a212", "a213", "a214", "a215", "a216", "a217", "a218", "a219", "a220", "a221", "a222", "a223", "a224", "a225", "a226", "a227", "a228", "a229", "a230", "a231", "a232", "a233", "a234", "a235", "a236", "a237", "a238", "a239", "a240", "a241", "a242", "a243", "a244", "a245", "a246", "a247", "a248", "a249", "a250", "a251", "a252", "a253", "a254", "a255", "s8", "s9", "s10", "s11", "s12", "s13", "s14", "s15", "s16", "s17", "s18", "s19", "s20", "s21", "s22", "s23", "s24", "s25", "s26", "s27", "s28", "s29", "s30", "s31", "s32", "s33", "s34", "s35", "s36", "s37", "s38", "s39", "s40", "s41", "s42", "s43", "s44", "s45", "s46", "s47", "s48", "s49", "s50", "s51", "s52", "s53", "s54", "s55", "s56", "s57", "s58", "s59", "s60", "s61", "s62", "s63", "s64", "s65", "s66", "s67", "s68", "s69", "s70", "s71", "s72", "s73", "s74", "s75", "s76", "s77", "s78", "s79", "s80", "s81", "s82", "s83", "scc", "vcc", "memory");
}

extern "C" void kernel_launch(void* const* d_in, const int* in_sizes, int n_in, void* d_out, int out_size, void* d_ws,
                              size_t ws_size, hipStream_t stream) {
  wino_main_kernel<<<256, 256, 0, stream>>>((const char*)d_in[0], (char*)d_ws, (char*)d_out, (const char*)d_in[1],
                                            ws_size >= (36ull << 20) ? ~0ull : 0ull);
}
```
